```python
import torch

def _softmax(x, axis=-1):
    return torch.softmax(x, dim=axis)
def _gelu(x):
    return torch.nn.functional.gelu(x, approximate="tanh")
def _silu(x):
    return torch.nn.functional.silu(x)
def _sigmoid(x):
    return torch.sigmoid(x)
def _layernorm(x, axis=-1, eps=1e-5):
    m = x.mean(dim=axis, keepdim=True)
    v = x.var(dim=axis, keepdim=True, unbiased=False)
    return (x - m) / torch.sqrt(v + eps)
def _scatter_add(tgt, idx, src, axis=0):
    return tgt.clone().index_add_(axis, idx, src)
def _segment_sum(src, seg, n):
    out = torch.zeros((n,) + src.shape[1:], dtype=src.dtype)
    return out.index_add_(0, seg, src)
def _pad(x, pw):
    flat = tuple(v for pair in reversed(pw) for v in pair)
    return torch.nn.functional.pad(x, flat)

kernel_name = 'gated_mix__sliding_local_dilated__compressed_kv_cla__prompt'

def setup_inputs():

    _w = (lambda a: (lambda t: t.to(torch.bfloat16) if t.is_floating_point() else t)(torch.as_tensor(a)))
    def _A_setup():
        B, S, D = 1, 2048, 576
        return {
            "x": _w((lambda *s: torch.randn(*s, dtype=torch.bfloat16) * 0.05)(B, S, D)),
            "wqkv": _w((lambda *s: torch.randn(*s, dtype=torch.bfloat16) * 1.6)(D, 3 * D)),
            "wo": _w((lambda *s: torch.randn(*s, dtype=torch.bfloat16) * 0.05)(D, D)),
        }
    _A_in = _A_setup()

    def _B_setup():
        B, H, Sq, Sk, D, M = 1, 32, 2048, 2048, 128, 576
        Dc = 4 * D
        return {
            "x": _w((lambda *s: torch.randn(*s, dtype=torch.bfloat16) * 0.05)(B, Sq, M)),
            "c_kv": _w((lambda *s: torch.randn(*s, dtype=torch.bfloat16) * 1.6)(B, Sk, Dc)),
            "w_q": _w((lambda *s: torch.randn(*s, dtype=torch.bfloat16) * 6.4)(M, H, D) * (M ** -0.5)),
            "w_uk": _w((lambda *s: torch.randn(*s, dtype=torch.bfloat16) * 6.4)(Dc, H, D) * (Dc ** -0.5)),
            "w_uv": _w((lambda *s: torch.randn(*s, dtype=torch.bfloat16) * 1.6)(Dc, H, D) * (Dc ** -0.5)),
            "w_o": _w((lambda *s: torch.randn(*s, dtype=torch.bfloat16) * 1.6)(H, D, M) * ((H * D) ** -0.5)),
        }
    _B_in = _B_setup()

    return {
        "A_x": _A_in["x"],
        "A_wqkv": _A_in["wqkv"],
        "A_wo": _A_in["wo"],
        "B_x": _B_in["x"],
        "B_c_kv": _B_in["c_kv"],
        "B_w_q": _B_in["w_q"],
        "B_w_uk": _B_in["w_uk"],
        "B_w_uv": _B_in["w_uv"],
        "B_w_o": _B_in["w_o"],
        "gate_w": _w((lambda *s: torch.randn(*s, dtype=torch.bfloat16) * 0.05)(576) * 16.0),
    }

def reference(A_x, A_wqkv, A_wo, B_x, B_c_kv, B_w_q, B_w_uk, B_w_uv, B_w_o, gate_w):
    def _A_ref(x, wqkv, wo):
        B, S, D = x.shape
        H = 32; Dh = D // H
        W, dil = 8, 2
        qkv = (lambda x, *d: x.permute(*d))((x @ wqkv).reshape(B, S, 3, H, Dh), 2, 0, 3, 1, 4)
        q, k, v = qkv[0], qkv[1], qkv[2]
        scale = Dh ** -0.5
        pos = torch.arange(S)
        off = torch.arange(W) * dil
        idx = pos[:, None] - off[None, :]
        mask = idx >= 0
        idx_c = torch.where(mask, idx, 0).reshape(-1)
        kg = k[:, :, idx_c, :].reshape(B, H, S, W, Dh)
        vg = v[:, :, idx_c, :].reshape(B, H, S, W, Dh)
        logits = torch.einsum("bhsd,bhswd->bhsw", q, kg) * scale
        logits = torch.where(mask[None, None, :, :], logits, -1e9)
        attn = _softmax(logits, axis=-1)
        o = torch.einsum("bhsw,bhswd->bhsd", attn, vg)
        return (lambda x, *d: x.permute(*d))(o, 0, 2, 1, 3).reshape(B, S, D) @ wo

    def _B_ref(x, c_kv, w_q, w_uk, w_uv, w_o):
        B, Sq, M = x.shape[0], x.shape[1], x.shape[2]
        Sk = c_kv.shape[1]
        H, D = w_q.shape[1], w_q.shape[2]
        q = torch.einsum("bqm,mhd->bhqd", x, w_q)
        k = torch.einsum("bkc,chd->bhkd", c_kv, w_uk)
        v = torch.einsum("bkc,chd->bhkd", c_kv, w_uv)
        scores = torch.einsum("bhqd,bhkd->bhqk", q, k) * (D ** -0.5)
        off = Sk - Sq
        qi = torch.arange(Sq)[:, None]
        kj = torch.arange(Sk)[None, :]
        cmask = kj <= (qi + off)
        scores = torch.where(cmask[None, None], scores, -1e9)
        p = _softmax(scores, axis=-1)
        ctx = torch.einsum("bhqk,bhkd->bhqd", p, v)
        return torch.einsum("bhqd,hdm->bqm", ctx, w_o)

    x = A_x
    g = _sigmoid((x * gate_w).sum(-1, keepdims=True))
    a = _A_ref(x, A_wqkv, A_wo)
    b = _B_ref(x, B_c_kv, B_w_q, B_w_uk, B_w_uv, B_w_o)
    return g * a + (1 - g) * b
```

```cpp
#include <hip/hip_runtime.h>
#include <hip/hip_bf16.h>
#include <stdint.h>
#include <utility>

typedef short bf16x8 __attribute__((ext_vector_type(8)));
typedef short s16x4 __attribute__((ext_vector_type(4)));
typedef float f32x16 __attribute__((ext_vector_type(16)));
typedef float f32x4 __attribute__((ext_vector_type(4)));
typedef unsigned u32x4 __attribute__((ext_vector_type(4)));
typedef unsigned u32x2 __attribute__((ext_vector_type(2)));
typedef unsigned short u16;

constexpr int S = 2048, DM = 576, NH = 32, HD = 128, DC = 512, NQKV = 1728, DHA = 18;
constexpr int NBIG = NH * HD;
constexpr int KS_TOT = 146;

#define SBAR() __builtin_amdgcn_sched_barrier(0)
constexpr size_t WS_Q = 0, WS_K = WS_Q + (size_t)S * NBIG * 2, WS_V = WS_K + (size_t)S * NBIG * 2, WS_AF = WS_V + (size_t)S * NBIG * 2, WS_X = WS_AF + (size_t)128 * KS_TOT * 1024,
                 WS_P = WS_X + (size_t)64 * 16 * 9216, WS_F = WS_P + (size_t)128 * (4 * 32 * 256 + 1024) * 4;
constexpr size_t WS_L = (size_t)96 << 20;
static_assert(WS_F == 96206848, "workspace map is baked into the generated asm");
constexpr int F_EPOCH = 0, F_J = 1024, F_ST = 8192, F_KV = 40960, F_R = 65536;
constexpr unsigned TAG_STEP = 0x9E3779B1u;
__device__ __forceinline__ unsigned ld_flag(const unsigned* p) { return __hip_atomic_load(p, __ATOMIC_RELAXED, __HIP_MEMORY_SCOPE_AGENT); }
__device__ __forceinline__ void st_flag(unsigned* p, unsigned v) { __hip_atomic_store(p, v, __ATOMIC_RELAXED, __HIP_MEMORY_SCOPE_AGENT); }
__device__ __forceinline__ unsigned ld_flag_async(const unsigned* p) { unsigned v; asm volatile("global_load_dword %0, %1, off sc1" : "=v"(v) : "v"(p) : "memory"); return v; }
#define WAITV(v, N) asm volatile("s_waitcnt vmcnt(" #N ")" : "+v"(v) :: "memory")
__device__ __forceinline__ void wait_flags(const unsigned* p, unsigned v, unsigned tag) {
    if (__builtin_expect(__builtin_amdgcn_ballot_w64(v != tag) != 0ull, 0)) {
        unsigned spins = 0;
        do { __builtin_amdgcn_s_sleep(1); } while (__builtin_amdgcn_ballot_w64(ld_flag(p) != tag) != 0ull && ++spins < (1u << 22));
    }
    asm volatile("" ::: "memory");
}

__device__ __forceinline__ unsigned cvtpk(float lo, float hi) {
    unsigned r; asm volatile("v_cvt_pk_bf16_f32 %0, %1, %2" : "=v"(r) : "v"(lo), "v"(hi)); return r;
}
__device__ __forceinline__ float bf2f(u16 v) { return __uint_as_float(((unsigned)v) << 16); }
__device__ __forceinline__ int crow(int r, int hi) { return (r & 3) + 8 * (r >> 2) + 4 * hi; }

typedef __attribute__((address_space(3))) s16x4* lds_s16x4_p;
__device__ __forceinline__ s16x4 trrd(unsigned addr) { return __builtin_amdgcn_ds_read_tr16_b64_v4i16((lds_s16x4_p)addr); }

__device__ __forceinline__ unsigned lane_id() { unsigned l; asm volatile("v_mbcnt_lo_u32_b32 %0, -1, 0\n\tv_mbcnt_hi_u32_b32 %0, -1, %0" : "=v"(l)); return l; }
#define RTS(slot) do {} while (0)
#define CT_END 0x1cb80
#define ACLOB_LO "a0","a1","a2","a3","a4","a5","a6","a7","a8","a9","a10","a11","a12","a13","a14","a15","a16","a17","a18","a19","a20","a21","a22","a23","a24","a25","a26","a27","a28","a29","a30","a31","a32","a33","a34","a35","a36","a37","a38","a39","a40","a41","a42","a43","a44","a45","a46","a47","a48","a49","a50","a51","a52","a53","a54","a55","a56","a57","a58","a59","a60","a61","a62","a63","a64","a65","a66","a67","a68","a69","a70","a71","a72","a73","a74","a75","a76","a77","a78","a79","a80","a81","a82","a83","a84","a85","a86","a87","a88","a89","a90","a91","a92","a93","a94","a95","a96","a97","a98","a99","a100","a101","a102","a103","a104","a105","a106","a107","a108","a109","a110","a111","a112","a113","a114","a115","a116","a117","a118","a119","a120","a121","a122","a123","a124","a125","a126","a127"
#define ACLOB_HI "a128","a129","a130","a131","a132","a133","a134","a135","a136","a137","a138","a139","a140","a141","a142","a143","a144","a145","a146","a147","a148","a149","a150","a151","a152","a153","a154","a155","a156","a157","a158","a159","a160","a161","a162","a163","a164","a165","a166","a167","a168","a169","a170","a171","a172","a173","a174","a175","a176","a177","a178","a179","a180","a181","a182","a183","a184","a185","a186","a187","a188","a189","a190","a191","a192","a193","a194","a195","a196","a197","a198","a199","a200","a201","a202","a203","a204","a205","a206","a207","a208","a209","a210","a211","a212","a213","a214","a215","a216","a217","a218","a219","a220","a221","a222","a223","a224","a225","a226","a227","a228","a229","a230","a231","a232","a233","a234","a235","a236","a237","a238","a239","a240","a241","a242","a243","a244","a245","a246","a247","a248","a249","a250","a251","a252","a253","a254","a255"
#define PLO(p) ((unsigned)(size_t)(p))
#define PHI(p) ((unsigned)((size_t)(p) >> 32))
__device__ __forceinline__ unsigned vsel(bool c, unsigned a, unsigned b) { asm("" : "+v"(a), "+v"(b)); return c ? a : b; }
__device__ __forceinline__ unsigned ssel(unsigned c, unsigned a, unsigned b) { unsigned r; asm("s_cmp_lg_u32 %1, 0\n\ts_cselect_b32 %0, %2, %3" : "=s"(r) : "s"(c), "s"(a), "s"(b) : "scc"); return r; }
template <int N> __device__ __forceinline__ unsigned sslt(unsigned c, unsigned a, unsigned b) { unsigned r; asm("s_cmpk_lt_u32 %1, %4\n\ts_cselect_b32 %0, %2, %3" : "=s"(r) : "s"(c), "s"(a), "s"(b), "n"(N) : "scc"); return r; }
namespace att {
constexpr int LDS_BYTES = 163840;
constexpr unsigned LDS_G = 159744u, LDS_L = 161792u;
}
#define SD(J8, H, W) ((unsigned)WS_AF + (((J8) + 2u * ((W) & 2u) + ((W) & 1u)) * KS_TOT + 18u + 4u * (H)) * 1024u)
#define SRDA (u32x4){PLO(ws), PHI(ws) & 0xffffu, (unsigned)WS_L, 0x00020000u}
#define PROJ_ASM \
".p2align 3\n" \
"s_waitcnt lgkmcnt(0)\n" \
"s_mov_b32 s64, s10\n" \
"s_and_b32 s65, s11, 0xffff\n" \
"s_mov_b32 s66, 0x200000\n" \
"s_mov_b32 s67, 0x20000\n" \
"s_mov_b32 s68, s14\n" \
"s_and_b32 s69, s15, 0xffff\n" \
"s_mov_b32 s70, 0x400000\n" \
"s_mov_b32 s71, 0x20000\n" \
"s_mov_b32 s72, s16\n" \
"s_and_b32 s73, s17, 0xffff\n" \
"s_mov_b32 s74, 0x400000\n" \
"s_mov_b32 s75, 0x20000\n" \
"s_add_u32 m0, s30, 0x0\n" \
"s_mov_b32 s85, 0x0\n" \
"buffer_load_dwordx4 v22, s[64:67], s85 offen lds\n" \
"s_add_u32 m0, s30, 0x400\n" \
"s_mov_b32 s84, 0x2000\n" \
"buffer_load_dwordx4 v23, s[64:67], s84 offen lds\n" \
"s_add_u32 m0, s31, 0x0\n" \
"s_mov_b32 s85, 0x0\n" \
"buffer_load_dwordx4 v26, s[68:71], s85 offen lds\n" \
"s_add_u32 m0, s31, 0x4000\n" \
"s_mov_b32 s84, 0x0\n" \
"s_nop 0\n" \
"buffer_load_dwordx4 v26, s[72:75], s84 offen lds\n" \
"s_add_u32 m0, s30, 0x800\n" \
"s_mov_b32 s85, 0x4000\n" \
"buffer_load_dwordx4 v22, s[64:67], s85 offen lds\n" \
"s_add_u32 m0, s30, 0xc00\n" \
"s_mov_b32 s84, 0x6000\n" \
"buffer_load_dwordx4 v23, s[64:67], s84 offen lds\n" \
"s_add_u32 m0, s31, 0x400\n" \
"s_mov_b32 s85, 0x8000\n" \
"buffer_load_dwordx4 v26, s[68:71], s85 offen lds\n" \
"s_add_u32 m0, s31, 0x4400\n" \
"s_mov_b32 s84, 0x8000\n" \
"buffer_load_dwordx4 v26, s[72:75], s84 offen lds\n" \
"s_movk_i32 s7, 0x300\n" \
"s_lshr_b32 s4, s2, 1\n" \
"s_and_b32 s4, s4, 60\n" \
"s_or_b32 s4, s4, s3\n" \
"v_lshrrev_b32_e32 v212, 4, v20\n" \
"v_min_u32_e32 v212, 59, v212\n" \
"s_cmpk_lt_u32 s2, 0x80\n" \
"s_cselect_b32 s6, s14, s16\n" \
"v_subrev_u32_e32 v214, 48, v212\n" \
"v_mov_b32_e32 v212, s10\n" \
"v_mov_b32_e32 v215, s6\n" \
"s_cmpk_lt_u32 s2, 0x80\n" \
"s_cselect_b32 s5, s15, s17\n" \
"v_cmp_gt_u32_e32 vcc, s7, v20\n" \
"v_mov_b32_e64 v213, s11\n" \
"v_bfe_u32 v216, v20, 4, 3\n" \
"v_cndmask_b32_e32 v212, v212, v215, vcc\n" \
"v_mov_b32_e32 v215, s5\n" \
"s_lshl_b32 s5, s3, 4\n" \
"v_cndmask_b32_e32 v213, v213, v215, vcc\n" \
"v_lshlrev_b32_e64 v215, 5, v20\n" \
"v_and_b32_e32 v215, 0x7000, v215\n" \
"v_lshl_add_u32 v215, s4, 6, v215\n" \
"s_lshl_b32 s4, s2, 3\n" \
"s_and_b32 s4, s4, 56\n" \
"v_or3_b32 v215, v215, s4, v216\n" \
"s_lshl_b32 s4, s2, 6\n" \
"s_or_b32 s6, s5, s4\n" \
"v_cmp_gt_u32_e64 s[4:5], 6, v214\n" \
"v_add_u32_e32 v215, 0x2000, v215\n" \
"s_nop 0\n" \
"v_cndmask_b32_e64 v216, 4, 2, s[4:5]\n" \
"v_add3_u32 v214, s6, v214, v216\n" \
"s_lshl_b32 s4, s3, 9\n" \
"v_cndmask_b32_e32 v214, v214, v215, vcc\n" \
"v_lshlrev_b32_e32 v214, 7, v214\n" \
"v_mov_b32_e64 v215, 0\n" \
"v_lshl_add_u64 v[184:185], v[212:213], 0, v[214:215]\n" \
"s_or_b32 s32, s4, 0x25000\n" \
"s_add_u32 m0, s30, 0x1000\n" \
"s_mov_b32 s85, 0x8000\n" \
"buffer_load_dwordx4 v22, s[64:67], s85 offen lds\n" \
"s_and_b32 s33, s3, 1\n" \
"v_lshrrev_b32_e32 v212, 4, v20\n" \
"v_lshrrev_b32_e64 v214, 8, v20\n" \
"v_bfe_u32 v212, v212, 1, 3\n" \
"v_lshlrev_b32_e64 v215, 2, v20\n" \
"s_add_u32 m0, s30, 0x1400\n" \
"s_mov_b32 s84, 0xa000\n" \
"buffer_load_dwordx4 v23, s[64:67], s84 offen lds\n" \
"v_lshrrev_b32_e64 v216, 1, v20\n" \
"v_bfe_u32 v213, v20, 4, 4\n" \
"s_lshl_b32 s4, s3, 5\n" \
"v_xor_b32_e32 v212, v212, v214\n" \
"v_lshlrev_b32_e64 v214, 11, v214\n" \
"s_add_u32 m0, s31, 0x800\n" \
"s_mov_b32 s85, 0x10000\n" \
"buffer_load_dwordx4 v27, s[68:71], s85 offen lds\n" \
"v_and_b32_e32 v215, 0x300, v215\n" \
"v_and_b32_e64 v217, 24, v216\n" \
"v_and_or_b32 v213, s4, 64, v213\n" \
"v_or3_b32 v214, v215, v214, v217\n" \
"s_lshl_b32 s4, s33, 14\n" \
"s_add_u32 m0, s31, 0x4800\n" \
"s_mov_b32 s84, 0x10000\n" \
"buffer_load_dwordx4 v27, s[72:75], s84 offen lds\n" \
"v_and_b32_e32 v215, 0xe0, v216\n" \
"v_lshlrev_b32_e32 v213, 7, v213\n" \
"v_lshlrev_b32_e64 v212, 4, v212\n" \
"v_or3_b32 v214, v214, s4, v215\n" \
"v_or_b32_e64 v28, v212, v213\n" \
"s_add_u32 m0, s30, 0x1800\n" \
"s_mov_b32 s85, 0xc000\n" \
"buffer_load_dwordx4 v22, s[64:67], s85 offen lds\n" \
"v_add_u32_e32 v32, 0x10000, v214\n" \
"v_bitop3_b32 v29, v212, 64, v213 bitop3:0x36\n" \
"v_xor_b32_e32 v33, 0x20, v32\n" \
"v_xor_b32_e32 v34, 0x40, v32\n" \
"v_xor_b32_e32 v35, 0x60, v32\n" \
"s_add_u32 m0, s30, 0x1c00\n" \
"s_mov_b32 s84, 0xe000\n" \
"buffer_load_dwordx4 v23, s[64:67], s84 offen lds\n" \
"v_xor_b32_e32 v36, 0x80, v32\n" \
"v_xor_b32_e32 v37, 0xa0, v32\n" \
"v_xor_b32_e32 v38, 0xc0, v32\n" \
"v_xor_b32_e32 v39, 0xe0, v32\n" \
"s_mov_b32 s80, s26\n" \
"s_add_u32 m0, s31, 0xc00\n" \
"s_mov_b32 s85, 0x18000\n" \
"buffer_load_dwordx4 v27, s[68:71], s85 offen lds\n" \
"s_and_b32 s81, s27, 0xffff\n" \
"s_mov_b32 s82, 0x10000000\n" \
"s_mov_b32 s83, 0x20000\n" \
"s_mov_b32 s76, s18\n" \
"s_and_b32 s77, s19, 0xffff\n" \
"s_add_u32 m0, s31, 0x4c00\n" \
"s_mov_b32 s84, 0x18000\n" \
"buffer_load_dwordx4 v27, s[72:75], s84 offen lds\n" \
"s_mov_b32 s78, 0x1e6000\n" \
"s_mov_b32 s79, 0x20000\n" \
"s_add_u32 m0, s30, 0x8000\n" \
"s_mov_b32 s85, 0x80\n" \
"buffer_load_dwordx4 v22, s[64:67], s85 offen lds\n" \
"s_lshr_b32 s4, s2, 1\n" \
"s_and_b32 s4, s4, 0x7c\n" \
"v_bfe_u32 v214, v20, 4, 5\n" \
"s_or_b32 s7, s4, s3\n" \
"v_min_u32_e64 v214, 26, v214\n" \
"v_mad_u64_u32 v[214:215], s[4:5], s7, 27, v[214:215]\n" \
"s_add_u32 m0, s30, 0x8400\n" \
"s_mov_b32 s84, 0x2080\n" \
"buffer_load_dwordx4 v23, s[64:67], s84 offen lds\n" \
"v_mul_u32_u24_e32 v215, 0xaaab, v214\n" \
"v_lshrrev_b32_e64 v215, 18, v215\n" \
"s_mov_b32 s4, 0x1fffffa\n" \
"v_mad_u64_u32 v[216:217], s[4:5], v215, s4, v[214:215]\n" \
"s_and_b32 s6, s2, 7\n" \
"v_mul_u32_u24_e32 v215, 27, v215\n" \
"s_add_u32 m0, s31, 0x8000\n" \
"s_mov_b32 s85, 0x80000\n" \
"buffer_load_dwordx4 v26, s[68:71], s85 offen lds\n" \
"v_lshrrev_b32_e32 v216, 1, v216\n" \
"v_and_b32_e32 v214, 1, v214\n" \
"v_mov_b32_e32 v213, s13\n" \
"v_lshrrev_b32_e32 v212, 4, v20\n" \
"v_mul_u32_u24_e64 v216, 9, v216\n" \
"v_add3_u32 v214, v214, s6, v215\n" \
"s_add_u32 m0, s31, 0xc000\n" \
"s_mov_b32 s84, 0x80000\n" \
"buffer_load_dwordx4 v26, s[72:75], s84 offen lds\n" \
"v_add_lshl_u32 v214, v214, v216, 7\n" \
"v_mad_u64_u32 v[216:217], s[4:5], s7, 36, v[212:213]\n" \
"s_movk_i32 s4, 0x240\n" \
"v_mov_b32_e32 v219, s9\n" \
"v_mov_b32_e32 v215, 0\n" \
"v_cmp_gt_u32_e32 vcc, s4, v20\n" \
"s_add_u32 m0, s30, 0x8800\n" \
"s_mov_b32 s85, 0x4080\n" \
"buffer_load_dwordx4 v22, s[64:67], s85 offen lds\n" \
"s_lshl_b32 s4, s2, 2\n" \
"v_lshl_add_u64 v[186:187], s[18:19], 0, v[214:215]\n" \
"v_mov_b32_e32 v214, s12\n" \
"v_mov_b32_e32 v217, s8\n" \
"s_or_b32 s4, s4, s3\n" \
"v_cndmask_b32_e64 v219, v219, v213, vcc\n" \
"s_add_u32 m0, s30, 0x8c00\n" \
"s_mov_b32 s84, 0x6080\n" \
"buffer_load_dwordx4 v23, s[64:67], s84 offen lds\n" \
"v_and_b32_e32 v213, 0x3ff8, v216\n" \
"s_mul_i32 s4, s4, 18\n" \
"v_cndmask_b32_e32 v218, v217, v214, vcc\n" \
"v_or_b32_e32 v213, s6, v213\n" \
"v_and_b32_e32 v214, 7, v216\n" \
"v_min_u32_e64 v212, 53, v212\n" \
"s_add_u32 m0, s31, 0x8400\n" \
"s_mov_b32 s85, 0x88000\n" \
"buffer_load_dwordx4 v26, s[68:71], s85 offen lds\n" \
"s_sub_i32 s4, s4, 36\n" \
"v_lshl_or_b32 v213, v213, 3, v214\n" \
"v_add_u32_e32 v212, s4, v212\n" \
"v_cndmask_b32_e32 v212, v212, v213, vcc\n" \
"v_lshlrev_b32_e32 v214, 7, v212\n" \
"v_lshl_add_u64 v[188:189], v[218:219], 0, v[214:215]\n" \
"s_add_u32 m0, s31, 0xc400\n" \
"s_mov_b32 s84, 0x88000\n" \
"buffer_load_dwordx4 v26, s[72:75], s84 offen lds\n" \
"s_movk_i32 s5, 0xd80\n" \
"s_lshr_b32 s6, s2, 5\n" \
"v_lshrrev_b32_e64 v212, 4, v20\n" \
"v_lshl_or_b32 v215, s3, 6, v212\n" \
"v_mul_u32_u24_e32 v216, 0x2aab, v215\n" \
"v_lshrrev_b32_e64 v216, 16, v216\n" \
"s_add_u32 m0, s30, 0x9000\n" \
"s_mov_b32 s85, 0x8080\n" \
"buffer_load_dwordx4 v22, s[64:67], s85 offen lds\n" \
"v_mad_i32_i24 v217, v216, -6, v215\n" \
"v_add_u32_e32 v218, 0xfffffe50, v215\n" \
"s_mul_i32 s35, s6, 0x48\n" \
"v_lshrrev_b32_e32 v219, 1, v217\n" \
"v_add_u32_e32 v218, s35, v216\n" \
"v_mul_u32_u24_e32 v219, 0x480, v219\n" \
"s_add_u32 m0, s30, 0x9400\n" \
"s_mov_b32 s84, 0xa080\n" \
"buffer_load_dwordx4 v23, s[64:67], s84 offen lds\n" \
"v_mad_u32_u24 v219, v218, s5, v219\n" \
"v_lshlrev_b32_e32 v221, 7, v218\n" \
"v_lshrrev_b32_e32 v218, 1, v218\n" \
"v_mov_b32_e32 v222, 7\n" \
"v_and_b32_e32 v218, 4, v218\n" \
"v_and_b32_e64 v216, 2, v216\n" \
"s_add_u32 m0, s31, 0x8800\n" \
"s_mov_b32 s85, 0x90000\n" \
"buffer_load_dwordx4 v27, s[68:71], s85 offen lds\n" \
"s_mul_i32 s7, s6, 0x2400\n" \
"s_sub_i32 s34, 0x22000, s7\n" \
"v_bitop3_b32 v216, v217, v218, v216 bitop3:0x1e\n" \
"v_or_b32_e32 v213, 0x24400, v20\n" \
"v_lshlrev_b32_e64 v216, 4, v216\n" \
"v_add3_u32 v196, v221, s34, v216\n" \
"s_add_u32 m0, s31, 0xc800\n" \
"s_mov_b32 s84, 0x90000\n" \
"buffer_load_dwordx4 v27, s[72:75], s84 offen lds\n" \
"v_mov_b32_e32 v216, v213\n" \
"s_movk_i32 s36, 0x1b0\n" \
"v_or_b32_e32 v216, 0x100, v215\n" \
"v_mul_u32_u24_e32 v217, 0x2aab, v216\n" \
"v_add_u32_e32 v215, 0xffffff50, v215\n" \
"v_lshrrev_b32_e64 v217, 16, v217\n" \
"s_add_u32 m0, s30, 0x9800\n" \
"s_mov_b32 s85, 0xc080\n" \
"buffer_load_dwordx4 v22, s[64:67], s85 offen lds\n" \
"v_mov_b32_e64 v221, v215\n" \
"v_mad_i32_i24 v218, v217, -6, v216\n" \
"v_cmp_gt_u32_e32 vcc, s36, v216\n" \
"v_and_b32_e32 v214, 16, v20\n" \
"v_lshrrev_b32_e32 v222, 1, v218\n" \
"v_cndmask_b32_e32 v217, v221, v217, vcc\n" \
"s_add_u32 m0, s30, 0x9c00\n" \
"s_mov_b32 s84, 0xe080\n" \
"buffer_load_dwordx4 v23, s[64:67], s84 offen lds\n" \
"s_movk_i32 s4, 0x48\n" \
"v_or_b32_e32 v192, v219, v214\n" \
"v_mov_b32_e32 v220, 2.0\n" \
"v_add_u32_e32 v221, s35, v217\n" \
"v_mul_u32_u24_e32 v222, 0x480, v222\n" \
"v_mad_u32_u24 v222, v221, s5, v222\n" \
"s_add_u32 m0, s31, 0x8c00\n" \
"s_mov_b32 s85, 0x98000\n" \
"buffer_load_dwordx4 v27, s[68:71], s85 offen lds\n" \
"v_lshlrev_b32_e32 v223, 1, v221\n" \
"v_mov_b32_e32 v224, 2.0\n" \
"v_cmp_gt_u32_e64 s[4:5], s4, v215\n" \
"v_mov_b32_e32 v220, 7\n" \
"v_or_b32_e32 v214, v222, v214\n" \
"v_mov_b32_e64 v222, 2.0\n" \
"s_add_u32 m0, s31, 0xcc00\n" \
"s_mov_b32 s84, 0x98000\n" \
"buffer_load_dwordx4 v27, s[72:75], s84 offen lds\n" \
"v_and_b32_e64 v217, 2, v217\n" \
"v_cndmask_b32_e64 v195, v224, v223, s[4:5]\n" \
"s_movk_i32 s4, 0x1f8\n" \
"v_cndmask_b32_e32 v193, v222, v214, vcc\n" \
"v_cndmask_b32_e32 v215, v220, v218, vcc\n" \
"v_cmp_gt_u32_e32 vcc, s4, v216\n" \
"s_mov_b32 m0, s32\n" \
"s_mov_b32 exec_lo, 0xffff\n" \
"s_mov_b32 exec_hi, 0xfff0000\n" \
"global_load_lds_dword v[184:185], off\n" \
"s_mov_b64 exec, -1\n" \
"s_mov_b32 s84, 0x5bc0000\n" \
"buffer_load_dword v180, off, s[80:83], s84 sc1\n" \
"s_cmp_lg_u32 s33, 0\n" \
"s_cbranch_scc1 L_w1_%=\n" \
"s_waitcnt vmcnt(18)\n" \
"s_barrier\n" \
"ds_read_b64_tr_b16 v[84:85], v32 offset:0\n" \
"ds_read_b64_tr_b16 v[86:87], v32 offset:1024\n" \
"ds_read_b128 v[52:55], v28 offset:16384\n" \
"ds_read_b128 v[56:59], v28 offset:18432\n" \
"ds_read_b64_tr_b16 v[88:89], v33 offset:0\n" \
"ds_read_b64_tr_b16 v[90:91], v33 offset:1024\n" \
"ds_read_b64_tr_b16 v[92:93], v34 offset:0\n" \
"ds_read_b64_tr_b16 v[94:95], v34 offset:1024\n" \
"ds_read_b64_tr_b16 v[96:97], v35 offset:0\n" \
"ds_read_b64_tr_b16 v[98:99], v35 offset:1024\n" \
"ds_read_b64_tr_b16 v[100:101], v36 offset:0\n" \
"ds_read_b64_tr_b16 v[102:103], v36 offset:1024\n" \
"ds_read_b64_tr_b16 v[104:105], v37 offset:0\n" \
"ds_read_b64_tr_b16 v[106:107], v37 offset:1024\n" \
"ds_read_b64_tr_b16 v[108:109], v38 offset:0\n" \
"ds_read_b64_tr_b16 v[110:111], v38 offset:1024\n" \
"ds_read_b64_tr_b16 v[112:113], v39 offset:0\n" \
"ds_read_b64_tr_b16 v[114:115], v39 offset:1024\n" \
"ds_read_b128 v[60:63], v28 offset:20480\n" \
"ds_read_b128 v[64:67], v28 offset:22528\n" \
"ds_read_b128 v[68:71], v28 offset:0\n" \
"ds_read_b128 v[72:75], v28 offset:2048\n" \
"ds_read_b128 v[76:79], v28 offset:4096\n" \
"ds_read_b128 v[80:83], v28 offset:6144\n" \
"s_waitcnt lgkmcnt(15)\n" \
"v_mfma_f32_16x16x32_bf16 a[0:3], v[84:87], v[52:55], 0\n" \
"ds_read_b64_tr_b16 v[148:149], v32 offset:8192\n" \
"s_lshl_b32 s4, s2, 2\n" \
"v_mfma_f32_16x16x32_bf16 a[32:35], v[84:87], v[56:59], 0\n" \
"v_lshrrev_b32_e32 v218, 1, v221\n" \
"s_and_b32 s4, s4, 28\n" \
"v_mfma_f32_16x16x32_bf16 a[4:7], v[88:91], v[52:55], 0\n" \
"ds_read_b64_tr_b16 v[150:151], v32 offset:9216\n" \
"s_bfe_u32 s5, s2, 0x20003\n" \
"v_mfma_f32_16x16x32_bf16 a[36:39], v[88:91], v[56:59], 0\n" \
"v_and_b32_e32 v218, 4, v218\n" \
"s_or_b32 s4, s4, s5\n" \
"v_mfma_f32_16x16x32_bf16 a[8:11], v[92:95], v[52:55], 0\n" \
"ds_read_b128 v[116:119], v29 offset:16384\n" \
"v_bitop3_b32 v215, v215, v218, v217 bitop3:0x1e\n" \
"v_mfma_f32_16x16x32_bf16 a[40:43], v[92:95], v[56:59], 0\n" \
"s_lshl_b32 s5, s4, 8\n" \
"v_lshlrev_b32_e32 v214, 7, v221\n" \
"s_waitcnt lgkmcnt(15)\n" \
"v_mfma_f32_16x16x32_bf16 a[12:15], v[96:99], v[52:55], 0\n" \
"ds_read_b128 v[120:123], v29 offset:18432\n" \
"v_lshlrev_b32_e32 v215, 4, v215\n" \
"v_mfma_f32_16x16x32_bf16 a[44:47], v[96:99], v[56:59], 0\n" \
"s_or_b32 s38, s5, 0x5be8000\n" \
"s_mul_i32 s5, s3, 18\n" \
"v_mfma_f32_16x16x32_bf16 a[16:19], v[100:103], v[52:55], 0\n" \
"ds_read_b64_tr_b16 v[152:153], v33 offset:8192\n" \
"v_add3_u32 v214, v214, s34, v215\n" \
"v_mfma_f32_16x16x32_bf16 a[48:51], v[100:103], v[56:59], 0\n" \
"v_mov_b32_e32 v215, v213\n" \
"s_add_i32 s35, s35, s5\n" \
"s_waitcnt lgkmcnt(15)\n" \
"v_mfma_f32_16x16x32_bf16 a[20:23], v[104:107], v[52:55], 0\n" \
"ds_read_b64_tr_b16 v[154:155], v33 offset:9216\n" \
"v_add_u32_e64 v212, s35, v212\n" \
"v_mfma_f32_16x16x32_bf16 a[52:55], v[104:107], v[56:59], 0\n" \
"v_mov_b32_e32 v219, 2.0\n" \
"v_cndmask_b32_e32 v197, v215, v214, vcc\n" \
"s_waitcnt lgkmcnt(14)\n" \
"v_mfma_f32_16x16x32_bf16 a[24:27], v[108:111], v[52:55], 0\n" \
"s_movk_i32 s5, 0x120\n" \
"v_mul_u32_u24_e32 v214, 0xd80, v212\n" \
"v_mfma_f32_16x16x32_bf16 a[56:59], v[108:111], v[56:59], 0\n" \
"ds_read_b64_tr_b16 v[156:157], v34 offset:8192\n" \
"v_cmp_gt_u32_e32 vcc, s5, v20\n" \
"s_waitcnt lgkmcnt(13)\n" \
"v_mfma_f32_16x16x32_bf16 a[28:31], v[112:115], v[52:55], 0\n" \
"v_lshrrev_b32_e32 v215, 1, v212\n" \
"s_movk_i32 s5, 0x60\n" \
"v_mfma_f32_16x16x32_bf16 a[60:63], v[112:115], v[56:59], 0\n" \
"ds_read_b64_tr_b16 v[158:159], v34 offset:9216\n" \
"v_cndmask_b32_e32 v194, v219, v214, vcc\n" \
"s_waitcnt lgkmcnt(13)\n" \
"v_mfma_f32_16x16x32_bf16 a[92:95], v[112:115], v[60:63], 0\n" \
"s_mov_b32 m0, s32\n" \
"v_lshlrev_b32_e32 v214, 7, v212\n" \
"s_waitcnt lgkmcnt(12)\n" \
"v_mfma_f32_16x16x32_bf16 a[124:127], v[112:115], v[64:67], 0\n" \
"s_mov_b32 exec_lo, 0xffff0000\n" \
"s_mov_b32 exec_hi, 0x0\n" \
"global_load_lds_dword v[184:185], off\n" \
"s_mov_b64 exec, -1\n" \
"v_mfma_f32_16x16x32_bf16 a[88:91], v[108:111], v[60:63], 0\n" \
"ds_read_b64_tr_b16 v[160:161], v35 offset:8192\n" \
"v_lshrrev_b32_e32 v212, 2, v212\n" \
"v_mfma_f32_16x16x32_bf16 a[120:123], v[108:111], v[64:67], 0\n" \
"ds_read_b64_tr_b16 v[162:163], v35 offset:9216\n" \
"v_and_b32_e64 v212, 2, v212\n" \
"v_mfma_f32_16x16x32_bf16 a[84:87], v[104:107], v[60:63], 0\n" \
"v_and_or_b32 v212, v215, 1, v212\n" \
"v_lshlrev_b32_e64 v212, 5, v212\n" \
"v_mfma_f32_16x16x32_bf16 a[116:119], v[104:107], v[64:67], 0\n" \
"s_mov_b32 m0, s32\n" \
"ds_read_b64_tr_b16 v[164:165], v36 offset:8192\n" \
"v_mfma_f32_16x16x32_bf16 a[80:83], v[100:103], v[60:63], 0\n" \
"s_mov_b32 exec_lo, 0xffff\n" \
"s_mov_b32 exec_hi, 0x0\n" \
"global_load_lds_dword v[186:187], off\n" \
"s_mov_b64 exec, -1\n" \
"v_mfma_f32_16x16x32_bf16 a[112:115], v[100:103], v[64:67], 0\n" \
"ds_read_b64_tr_b16 v[166:167], v36 offset:9216\n" \
"v_bitop3_b32 v212, v212, s5, v214 bitop3:0x36\n" \
"v_mfma_f32_16x16x32_bf16 a[76:79], v[96:99], v[60:63], 0\n" \
"v_add_u32_e32 v212, s34, v212\n" \
"s_mul_i32 s34, s4, 0x12000\n" \
"v_mfma_f32_16x16x32_bf16 a[108:111], v[96:99], v[64:67], 0\n" \
"s_or_b32 s36, s34, 0x6000000\n" \
"s_mul_i32 s34, s4, 36\n" \
"v_mfma_f32_16x16x32_bf16 a[72:75], v[92:95], v[60:63], 0\n" \
"ds_read_b64_tr_b16 v[168:169], v37 offset:8192\n" \
"s_add_i32 s4, s7, s36\n" \
"v_mfma_f32_16x16x32_bf16 a[104:107], v[92:95], v[64:67], 0\n" \
"s_lshl_b32 s5, s3, 10\n" \
"s_addk_i32 s4, 0x2000\n" \
"v_mfma_f32_16x16x32_bf16 a[68:71], v[88:91], v[60:63], 0\n" \
"ds_read_b64_tr_b16 v[170:171], v37 offset:9216\n" \
"s_mov_b32 s37, 2.0\n" \
"v_mfma_f32_16x16x32_bf16 a[100:103], v[88:91], v[64:67], 0\n" \
"v_cndmask_b32_e32 v198, v213, v212, vcc\n" \
"v_or_b32_e64 v212, s5, v20\n" \
"v_mfma_f32_16x16x32_bf16 a[64:67], v[84:87], v[60:63], 0\n" \
"ds_read_b64_tr_b16 v[172:173], v38 offset:8192\n" \
"s_or_b32 s39, s36, s5\n" \
"v_mfma_f32_16x16x32_bf16 a[96:99], v[84:87], v[64:67], 0\n" \
"s_cmp_lg_u32 s3, 0\n" \
"s_cselect_b32 s36, s37, s4\n" \
"s_waitcnt lgkmcnt(15)\n" \
"v_mfma_f32_16x16x32_bf16 a[128:131], v[84:87], v[68:71], 0\n" \
"ds_read_b64_tr_b16 v[174:175], v38 offset:9216\n" \
"s_lshl_b32 s4, s6, 2\n" \
"v_mfma_f32_16x16x32_bf16 a[160:163], v[84:87], v[72:75], 0\n" \
"v_or_b32_e32 v199, 0x22000, v212\n" \
"s_add_i32 s35, s39, s7\n" \
"v_mfma_f32_16x16x32_bf16 a[132:135], v[88:91], v[68:71], 0\n" \
"ds_read_b64_tr_b16 v[176:177], v39 offset:8192\n" \
"s_or_b32 s4, s38, s4\n" \
"v_mfma_f32_16x16x32_bf16 a[164:167], v[88:91], v[72:75], 0\n" \
"s_cmpk_lt_u32 s3, 3\n" \
"s_cselect_b32 s37, s37, s4\n" \
"v_mfma_f32_16x16x32_bf16 a[136:139], v[92:95], v[68:71], 0\n" \
"ds_read_b64_tr_b16 v[178:179], v39 offset:9216\n" \
"v_mov_b32_e32 v220, 0xffff\n" \
"v_mfma_f32_16x16x32_bf16 a[168:171], v[92:95], v[72:75], 0\n" \
"s_and_b32 s4, s3, 1\n" \
"s_lshl_b32 s6, s4, 4\n" \
"v_mfma_f32_16x16x32_bf16 a[140:143], v[96:99], v[68:71], 0\n" \
"v_lshrrev_b32_e32 v212, 4, v20\n" \
"v_lshrrev_b32_e64 v213, 6, v20\n" \
"v_mfma_f32_16x16x32_bf16 a[172:175], v[96:99], v[72:75], 0\n" \
"ds_read_b128 v[124:127], v29 offset:20480\n" \
"v_and_b32_e64 v222, 12, v213\n" \
"v_mfma_f32_16x16x32_bf16 a[144:147], v[100:103], v[68:71], 0\n" \
"v_and_or_b32 v221, v212, 15, s6\n" \
"v_cmp_gt_u32_e64 vcc, v222, v221\n" \
"v_mfma_f32_16x16x32_bf16 a[176:179], v[100:103], v[72:75], 0\n" \
"ds_read_b128 v[128:131], v29 offset:22528\n" \
"v_mov_b32_e32 v223, 0xffff0000\n" \
"v_mfma_f32_16x16x32_bf16 a[148:151], v[104:107], v[68:71], 0\n" \
"v_or_b32_e64 v217, 2, v222\n" \
"v_cndmask_b32_e64 v214, v220, 0, vcc\n" \
"v_mfma_f32_16x16x32_bf16 a[180:183], v[104:107], v[72:75], 0\n" \
"ds_read_b128 v[132:135], v29 offset:0\n" \
"v_cmp_lt_u32_e64 vcc, v222, v221\n" \
"v_mfma_f32_16x16x32_bf16 a[152:155], v[108:111], v[68:71], 0\n" \
"v_or_b32_e32 v219, 16, v222\n" \
"v_or_b32_e32 v224, 18, v222\n" \
"v_mfma_f32_16x16x32_bf16 a[184:187], v[108:111], v[72:75], 0\n" \
"ds_read_b128 v[136:139], v29 offset:2048\n" \
"v_cndmask_b32_e64 v215, 0, v223, vcc\n" \
"v_mfma_f32_16x16x32_bf16 a[156:159], v[112:115], v[68:71], 0\n" \
"v_cmp_gt_u32_e32 vcc, v217, v221\n" \
"s_lshl_b32 s7, s2, 2\n" \
"v_mfma_f32_16x16x32_bf16 a[188:191], v[112:115], v[72:75], 0\n" \
"ds_read_b128 v[140:143], v29 offset:4096\n" \
"s_or_b32 s7, s7, s3\n" \
"v_mfma_f32_16x16x32_bf16 a[220:223], v[112:115], v[76:79], 0\n" \
"v_cndmask_b32_e64 v216, v220, 0, vcc\n" \
"v_cmp_lt_u32_e32 vcc, v217, v221\n" \
"v_mfma_f32_16x16x32_bf16 a[252:255], v[112:115], v[80:83], 0\n" \
"ds_read_b128 v[144:147], v29 offset:6144\n" \
"s_add_i32 s42, s7, 0xffffffaf\n" \
"v_mfma_f32_16x16x32_bf16 a[216:219], v[108:111], v[76:79], 0\n" \
"s_mov_b32 s6, 0\n" \
"v_cndmask_b32_e32 v217, 0, v223, vcc\n" \
"v_mfma_f32_16x16x32_bf16 a[248:251], v[108:111], v[80:83], 0\n" \
"v_cmp_gt_u32_e64 vcc, v219, v221\n" \
"v_mfma_f32_16x16x32_bf16 a[212:215], v[104:107], v[76:79], 0\n" \
"s_cmpk_lt_u32 s7, 0x51\n" \
"s_cselect_b32 s41, s23, s25\n" \
"v_mfma_f32_16x16x32_bf16 a[244:247], v[104:107], v[80:83], 0\n" \
"v_mov_b32_e64 v11, 0\n" \
"v_mfma_f32_16x16x32_bf16 a[208:211], v[100:103], v[76:79], 0\n" \
"s_cmpk_lt_u32 s7, 0x51\n" \
"s_cselect_b32 s40, s22, s24\n" \
"v_mfma_f32_16x16x32_bf16 a[240:243], v[100:103], v[80:83], 0\n" \
"s_lshl_b32 s44, s3, 1\n" \
"v_cndmask_b32_e64 v218, v220, 0, vcc\n" \
"v_mfma_f32_16x16x32_bf16 a[204:207], v[96:99], v[76:79], 0\n" \
"v_cmp_lt_u32_e32 vcc, v219, v221\n" \
"v_mfma_f32_16x16x32_bf16 a[236:239], v[96:99], v[80:83], 0\n" \
"s_cmpk_lt_u32 s7, 0x291\n" \
"s_cselect_b32 s42, s42, s6\n" \
"v_mfma_f32_16x16x32_bf16 a[200:203], v[92:95], v[76:79], 0\n" \
"s_lshr_b32 s5, s2, 5\n" \
"v_mfma_f32_16x16x32_bf16 a[232:235], v[92:95], v[80:83], 0\n" \
"s_cmpk_lt_u32 s7, 0x51\n" \
"s_cselect_b32 s7, s7, s42\n" \
"v_mfma_f32_16x16x32_bf16 a[196:199], v[88:91], v[76:79], 0\n" \
"s_movk_i32 s42, 0xf400\n" \
"v_cndmask_b32_e64 v219, 0, v223, vcc\n" \
"v_mfma_f32_16x16x32_bf16 a[228:231], v[88:91], v[80:83], 0\n" \
"v_cmp_gt_u32_e64 vcc, v224, v221\n" \
"s_bfe_u32 s43, s2, 0x20003\n" \
"v_mfma_f32_16x16x32_bf16 a[192:195], v[84:87], v[76:79], 0\n" \
"s_lshl_b32 s47, s3, 8\n" \
"v_cndmask_b32_e64 v220, v220, 0, vcc\n" \
"v_mfma_f32_16x16x32_bf16 a[224:227], v[84:87], v[80:83], 0\n" \
"s_waitcnt vmcnt(4) lgkmcnt(0)\n" \
"s_barrier\n" \
"v_mfma_f32_16x16x32_bf16 a[0:3], v[148:151], v[116:119], a[0:3]\n" \
"s_add_u32 m0, s30, 0x0\n" \
"s_mov_b32 s85, 0x100\n" \
"v_mfma_f32_16x16x32_bf16 a[32:35], v[148:151], v[120:123], a[32:35]\n" \
"buffer_load_dwordx4 v22, s[64:67], s85 offen lds\n" \
"v_mfma_f32_16x16x32_bf16 a[4:7], v[152:155], v[116:119], a[4:7]\n" \
"ds_read_b64_tr_b16 v[84:85], v32 offset:32768\n" \
"v_cmp_lt_u32_e64 vcc, v224, v221\n" \
"v_mfma_f32_16x16x32_bf16 a[36:39], v[152:155], v[120:123], a[36:39]\n" \
"ds_read_b64_tr_b16 v[86:87], v32 offset:33792\n" \
"s_min_u32 s49, s3, 2\n" \
"v_mfma_f32_16x16x32_bf16 a[8:11], v[156:159], v[116:119], a[8:11]\n" \
"s_add_u32 m0, s30, 0x400\n" \
"s_mov_b32 s84, 0x2100\n" \
"v_mfma_f32_16x16x32_bf16 a[40:43], v[156:159], v[120:123], a[40:43]\n" \
"buffer_load_dwordx4 v23, s[64:67], s84 offen lds\n" \
"v_mfma_f32_16x16x32_bf16 a[12:15], v[160:163], v[116:119], a[12:15]\n" \
"ds_read_b128 v[52:55], v28 offset:49152\n" \
"s_mul_i32 s50, s43, 0x120\n" \
"v_mfma_f32_16x16x32_bf16 a[44:47], v[160:163], v[120:123], a[44:47]\n" \
"ds_read_b128 v[56:59], v28 offset:51200\n" \
"v_cndmask_b32_e32 v221, 0, v223, vcc\n" \
"v_mfma_f32_16x16x32_bf16 a[16:19], v[164:167], v[116:119], a[16:19]\n" \
"s_add_u32 m0, s31, 0x0\n" \
"s_mov_b32 s85, 0x100000\n" \
"v_mfma_f32_16x16x32_bf16 a[48:51], v[164:167], v[120:123], a[48:51]\n" \
"buffer_load_dwordx4 v26, s[68:71], s85 offen lds\n" \
"v_mfma_f32_16x16x32_bf16 a[20:23], v[168:171], v[116:119], a[20:23]\n" \
"ds_read_b64_tr_b16 v[88:89], v33 offset:32768\n" \
"v_lshlrev_b32_e32 v223, 7, v212\n" \
"v_mfma_f32_16x16x32_bf16 a[52:55], v[168:171], v[120:123], a[52:55]\n" \
"ds_read_b64_tr_b16 v[90:91], v33 offset:33792\n" \
"v_lshl_or_b32 v10, s7, 13, v223\n" \
"v_mfma_f32_16x16x32_bf16 a[24:27], v[172:175], v[116:119], a[24:27]\n" \
"s_add_u32 m0, s31, 0x4000\n" \
"s_mov_b32 s84, 0x100000\n" \
"v_mfma_f32_16x16x32_bf16 a[56:59], v[172:175], v[120:123], a[56:59]\n" \
"buffer_load_dwordx4 v26, s[72:75], s84 offen lds\n" \
"v_mfma_f32_16x16x32_bf16 a[28:31], v[176:179], v[116:119], a[28:31]\n" \
"ds_read_b64_tr_b16 v[92:93], v34 offset:32768\n" \
"v_mul_u32_u24_e32 v223, 0xe39, v212\n" \
"v_mfma_f32_16x16x32_bf16 a[60:63], v[176:179], v[120:123], a[60:63]\n" \
"ds_read_b64_tr_b16 v[94:95], v34 offset:33792\n" \
"v_lshl_add_u64 v[16:17], s[40:41], 0, v[10:11]\n" \
"v_mfma_f32_16x16x32_bf16 a[92:95], v[176:179], v[124:127], a[92:95]\n" \
"s_add_u32 m0, s30, 0x800\n" \
"s_mov_b32 s85, 0x4100\n" \
"v_mfma_f32_16x16x32_bf16 a[124:127], v[176:179], v[128:131], a[124:127]\n" \
"buffer_load_dwordx4 v22, s[64:67], s85 offen lds\n" \
"v_mfma_f32_16x16x32_bf16 a[88:91], v[172:175], v[124:127], a[88:91]\n" \
"ds_read_b64_tr_b16 v[96:97], v35 offset:32768\n" \
"v_lshrrev_b32_e64 v223, 16, v223\n" \
"v_mfma_f32_16x16x32_bf16 a[120:123], v[172:175], v[128:131], a[120:123]\n" \
"ds_read_b64_tr_b16 v[98:99], v35 offset:33792\n" \
"s_movk_i32 s40, 0xffee\n" \
"v_mfma_f32_16x16x32_bf16 a[84:87], v[168:171], v[124:127], a[84:87]\n" \
"s_add_u32 m0, s30, 0xc00\n" \
"s_mov_b32 s84, 0x6100\n" \
"v_mfma_f32_16x16x32_bf16 a[116:119], v[168:171], v[128:131], a[116:119]\n" \
"buffer_load_dwordx4 v23, s[64:67], s84 offen lds\n" \
"v_mfma_f32_16x16x32_bf16 a[80:83], v[164:167], v[124:127], a[80:83]\n" \
"ds_read_b64_tr_b16 v[100:101], v36 offset:32768\n" \
"s_lshl_b32 s7, s2, 5\n" \
"v_mfma_f32_16x16x32_bf16 a[112:115], v[164:167], v[128:131], a[112:115]\n" \
"ds_read_b64_tr_b16 v[102:103], v36 offset:33792\n" \
"v_mad_i32_i24 v224, v223, s40, v212\n" \
"v_mfma_f32_16x16x32_bf16 a[76:79], v[160:163], v[124:127], a[76:79]\n" \
"s_add_u32 m0, s31, 0x400\n" \
"s_mov_b32 s85, 0x108000\n" \
"v_mfma_f32_16x16x32_bf16 a[108:111], v[160:163], v[128:131], a[108:111]\n" \
"buffer_load_dwordx4 v26, s[68:71], s85 offen lds\n" \
"v_mfma_f32_16x16x32_bf16 a[72:75], v[156:159], v[124:127], a[72:75]\n" \
"ds_read_b64_tr_b16 v[104:105], v37 offset:32768\n" \
"s_add_i32 s7, s7, 32\n" \
"v_mfma_f32_16x16x32_bf16 a[104:107], v[156:159], v[128:131], a[104:107]\n" \
"v_lshlrev_b32_e32 v225, 4, v224\n" \
"v_add_u32_e64 v226, s7, v225\n" \
"v_mfma_f32_16x16x32_bf16 a[68:71], v[152:155], v[124:127], a[68:71]\n" \
"s_add_u32 m0, s31, 0x4400\n" \
"s_mov_b32 s84, 0x108000\n" \
"v_mfma_f32_16x16x32_bf16 a[100:103], v[152:155], v[128:131], a[100:103]\n" \
"buffer_load_dwordx4 v26, s[72:75], s84 offen lds\n" \
"v_mfma_f32_16x16x32_bf16 a[64:67], v[148:151], v[124:127], a[64:67]\n" \
"ds_read_b64_tr_b16 v[106:107], v37 offset:33792\n" \
"v_and_b32_e32 v226, 0xf0, v226\n" \
"v_mfma_f32_16x16x32_bf16 a[96:99], v[148:151], v[128:131], a[96:99]\n" \
"ds_read_b64_tr_b16 v[108:109], v38 offset:32768\n" \
"v_cmp_gt_u32_e64 vcc, 16, v224\n" \
"v_mfma_f32_16x16x32_bf16 a[128:131], v[148:151], v[132:135], a[128:131]\n" \
"s_add_u32 m0, s30, 0x1000\n" \
"s_mov_b32 s85, 0x8100\n" \
"v_mfma_f32_16x16x32_bf16 a[160:163], v[148:151], v[136:139], a[160:163]\n" \
"buffer_load_dwordx4 v22, s[64:67], s85 offen lds\n" \
"v_mfma_f32_16x16x32_bf16 a[132:135], v[152:155], v[132:135], a[132:135]\n" \
"ds_read_b64_tr_b16 v[110:111], v38 offset:33792\n" \
"s_movk_i32 s41, 0xf800\n" \
"v_mfma_f32_16x16x32_bf16 a[164:167], v[152:155], v[136:139], a[164:167]\n" \
"ds_read_b64_tr_b16 v[112:113], v39 offset:32768\n" \
"v_or_b32_e32 v12, v214, v215\n" \
"v_mfma_f32_16x16x32_bf16 a[136:139], v[156:159], v[132:135], a[136:139]\n" \
"s_add_u32 m0, s30, 0x1400\n" \
"s_mov_b32 s84, 0xa100\n" \
"v_mfma_f32_16x16x32_bf16 a[168:171], v[156:159], v[136:139], a[168:171]\n" \
"buffer_load_dwordx4 v23, s[64:67], s84 offen lds\n" \
"v_mfma_f32_16x16x32_bf16 a[140:143], v[160:163], v[132:135], a[140:143]\n" \
"ds_read_b64_tr_b16 v[114:115], v39 offset:33792\n" \
"v_cndmask_b32_e64 v224, v225, v226, vcc\n" \
"v_mfma_f32_16x16x32_bf16 a[172:175], v[160:163], v[136:139], a[172:175]\n" \
"ds_read_b128 v[60:63], v28 offset:53248\n" \
"v_or_b32_e64 v225, 64, v212\n" \
"v_mfma_f32_16x16x32_bf16 a[144:147], v[164:167], v[132:135], a[144:147]\n" \
"s_add_u32 m0, s31, 0x800\n" \
"s_mov_b32 s85, 0x110000\n" \
"v_mfma_f32_16x16x32_bf16 a[176:179], v[164:167], v[136:139], a[176:179]\n" \
"buffer_load_dwordx4 v27, s[68:71], s85 offen lds\n" \
"v_mfma_f32_16x16x32_bf16 a[148:151], v[168:171], v[132:135], a[148:151]\n" \
"ds_read_b128 v[64:67], v28 offset:55296\n" \
"v_mul_u32_u24_e32 v226, 0xe39, v225\n" \
"v_mfma_f32_16x16x32_bf16 a[180:183], v[168:171], v[136:139], a[180:183]\n" \
"ds_read_b128 v[68:71], v28 offset:32768\n" \
"v_lshrrev_b32_e64 v226, 16, v226\n" \
"v_mfma_f32_16x16x32_bf16 a[152:155], v[172:175], v[132:135], a[152:155]\n" \
"s_add_u32 m0, s31, 0x4800\n" \
"s_mov_b32 s84, 0x110000\n" \
"v_mfma_f32_16x16x32_bf16 a[184:187], v[172:175], v[136:139], a[184:187]\n" \
"buffer_load_dwordx4 v27, s[72:75], s84 offen lds\n" \
"v_mfma_f32_16x16x32_bf16 a[156:159], v[176:179], v[132:135], a[156:159]\n" \
"ds_read_b128 v[72:75], v28 offset:34816\n" \
"v_mad_i32_i24 v225, v226, s40, v225\n" \
"v_mfma_f32_16x16x32_bf16 a[188:191], v[176:179], v[136:139], a[188:191]\n" \
"ds_read_b128 v[76:79], v28 offset:36864\n" \
"v_lshlrev_b32_e64 v227, 4, v225\n" \
"v_mfma_f32_16x16x32_bf16 a[220:223], v[176:179], v[140:143], a[220:223]\n" \
"v_add_u32_e64 v228, s7, v227\n" \
"v_and_b32_e32 v228, 0xf0, v228\n" \
"v_mfma_f32_16x16x32_bf16 a[252:255], v[176:179], v[144:147], a[252:255]\n" \
"ds_read_b128 v[80:83], v28 offset:38912\n" \
"v_cmp_gt_u32_e64 vcc, 16, v225\n" \
"v_mfma_f32_16x16x32_bf16 a[216:219], v[172:175], v[140:143], a[216:219]\n" \
"v_or_b32_e32 v13, v216, v217\n" \
"v_or_b32_e32 v14, v218, v219\n" \
"v_mfma_f32_16x16x32_bf16 a[248:251], v[172:175], v[144:147], a[248:251]\n" \
"v_cndmask_b32_e64 v225, v227, v228, vcc\n" \
"v_or_b32_e32 v227, 0x80, v212\n" \
"v_mfma_f32_16x16x32_bf16 a[212:215], v[168:171], v[140:143], a[212:215]\n" \
"v_mul_u32_u24_e32 v228, 0xe39, v227\n" \
"v_lshrrev_b32_e64 v228, 16, v228\n" \
"v_mfma_f32_16x16x32_bf16 a[244:247], v[168:171], v[144:147], a[244:247]\n" \
"v_mad_i32_i24 v227, v228, s40, v227\n" \
"v_lshlrev_b32_e64 v229, 4, v227\n" \
"v_mfma_f32_16x16x32_bf16 a[208:211], v[164:167], v[140:143], a[208:211]\n" \
"v_add_u32_e64 v230, s7, v229\n" \
"v_and_b32_e32 v230, 0xf0, v230\n" \
"v_mfma_f32_16x16x32_bf16 a[240:243], v[164:167], v[144:147], a[240:243]\n" \
"v_cmp_gt_u32_e32 vcc, 16, v227\n" \
"v_or_b32_e32 v15, v220, v221\n" \
"v_mfma_f32_16x16x32_bf16 a[204:207], v[160:163], v[140:143], a[204:207]\n" \
"v_cndmask_b32_e64 v227, v229, v230, vcc\n" \
"v_or_b32_e32 v229, 0xc0, v212\n" \
"v_mfma_f32_16x16x32_bf16 a[236:239], v[160:163], v[144:147], a[236:239]\n" \
"v_mul_u32_u24_e32 v230, 0xe39, v229\n" \
"v_lshrrev_b32_e64 v230, 16, v230\n" \
"v_mfma_f32_16x16x32_bf16 a[200:203], v[156:159], v[140:143], a[200:203]\n" \
"v_mad_i32_i24 v229, v230, s40, v229\n" \
"v_lshlrev_b32_e64 v231, 4, v229\n" \
"v_mfma_f32_16x16x32_bf16 a[232:235], v[156:159], v[144:147], a[232:235]\n" \
"v_add_u32_e64 v232, s7, v231\n" \
"v_and_b32_e32 v232, 0xf0, v232\n" \
"v_mfma_f32_16x16x32_bf16 a[196:199], v[152:155], v[140:143], a[196:199]\n" \
"v_cmp_gt_u32_e32 vcc, 16, v229\n" \
"s_nop 1\n" \
"v_cndmask_b32_e64 v229, v231, v232, vcc\n" \
"v_mfma_f32_16x16x32_bf16 a[228:231], v[152:155], v[144:147], a[228:231]\n" \
"v_or_b32_e32 v231, 0x100, v212\n" \
"v_mul_u32_u24_e32 v232, 0xe39, v231\n" \
"v_mfma_f32_16x16x32_bf16 a[192:195], v[148:151], v[140:143], a[192:195]\n" \
"v_lshrrev_b32_e64 v232, 16, v232\n" \
"v_mad_i32_i24 v231, v232, s40, v231\n" \
"v_mfma_f32_16x16x32_bf16 a[224:227], v[148:151], v[144:147], a[224:227]\n" \
"s_waitcnt lgkmcnt(15)\n" \
"v_mfma_f32_16x16x32_bf16 a[0:3], v[84:87], v[52:55], a[0:3]\n" \
"s_add_u32 m0, s30, 0x1800\n" \
"s_mov_b32 s85, 0xc100\n" \
"v_mfma_f32_16x16x32_bf16 a[32:35], v[84:87], v[56:59], a[32:35]\n" \
"buffer_load_dwordx4 v22, s[64:67], s85 offen lds\n" \
"v_mfma_f32_16x16x32_bf16 a[4:7], v[88:91], v[52:55], a[4:7]\n" \
"ds_read_b64_tr_b16 v[148:149], v32 offset:40960\n" \
"v_lshlrev_b32_e32 v233, 4, v231\n" \
"v_mfma_f32_16x16x32_bf16 a[36:39], v[88:91], v[56:59], a[36:39]\n" \
"s_add_u32 m0, s30, 0x1c00\n" \
"s_mov_b32 s84, 0xe100\n" \
"v_mfma_f32_16x16x32_bf16 a[8:11], v[92:95], v[52:55], a[8:11]\n" \
"buffer_load_dwordx4 v23, s[64:67], s84 offen lds\n" \
"v_mfma_f32_16x16x32_bf16 a[40:43], v[92:95], v[56:59], a[40:43]\n" \
"ds_read_b64_tr_b16 v[150:151], v32 offset:41984\n" \
"v_add_u32_e32 v234, s7, v233\n" \
"s_waitcnt lgkmcnt(15)\n" \
"v_mfma_f32_16x16x32_bf16 a[12:15], v[96:99], v[52:55], a[12:15]\n" \
"s_add_u32 m0, s31, 0xc00\n" \
"s_mov_b32 s85, 0x118000\n" \
"v_mfma_f32_16x16x32_bf16 a[44:47], v[96:99], v[56:59], a[44:47]\n" \
"buffer_load_dwordx4 v27, s[68:71], s85 offen lds\n" \
"s_waitcnt lgkmcnt(14)\n" \
"v_mfma_f32_16x16x32_bf16 a[16:19], v[100:103], v[52:55], a[16:19]\n" \
"ds_read_b128 v[116:119], v29 offset:49152\n" \
"v_and_b32_e32 v234, 0xf0, v234\n" \
"v_mfma_f32_16x16x32_bf16 a[48:51], v[100:103], v[56:59], a[48:51]\n" \
"s_add_u32 m0, s31, 0x4c00\n" \
"s_mov_b32 s84, 0x118000\n" \
"s_waitcnt lgkmcnt(13)\n" \
"v_mfma_f32_16x16x32_bf16 a[20:23], v[104:107], v[52:55], a[20:23]\n" \
"buffer_load_dwordx4 v27, s[72:75], s84 offen lds\n" \
"v_mfma_f32_16x16x32_bf16 a[52:55], v[104:107], v[56:59], a[52:55]\n" \
"ds_read_b128 v[120:123], v29 offset:51200\n" \
"v_cmp_gt_u32_e32 vcc, 16, v231\n" \
"s_waitcnt lgkmcnt(12)\n" \
"v_mfma_f32_16x16x32_bf16 a[24:27], v[108:111], v[52:55], a[24:27]\n" \
"ds_read_b64_tr_b16 v[152:153], v33 offset:40960\n" \
"v_cndmask_b32_e64 v231, v233, v234, vcc\n" \
"v_mfma_f32_16x16x32_bf16 a[56:59], v[108:111], v[56:59], a[56:59]\n" \
"ds_read_b64_tr_b16 v[154:155], v33 offset:41984\n" \
"v_or_b32_e32 v233, 0x140, v212\n" \
"s_waitcnt lgkmcnt(12)\n" \
"v_mfma_f32_16x16x32_bf16 a[28:31], v[112:115], v[52:55], a[28:31]\n" \
"ds_read_b64_tr_b16 v[156:157], v34 offset:40960\n" \
"v_mul_u32_u24_e32 v234, 0xe39, v233\n" \
"v_mfma_f32_16x16x32_bf16 a[60:63], v[112:115], v[56:59], a[60:63]\n" \
"ds_read_b64_tr_b16 v[158:159], v34 offset:41984\n" \
"v_lshrrev_b32_e32 v234, 16, v234\n" \
"s_waitcnt lgkmcnt(13)\n" \
"v_mfma_f32_16x16x32_bf16 a[92:95], v[112:115], v[60:63], a[92:95]\n" \
"s_mov_b32 m0, s32\n" \
"v_mad_i32_i24 v233, v234, s40, v233\n" \
"s_waitcnt lgkmcnt(12)\n" \
"v_mfma_f32_16x16x32_bf16 a[124:127], v[112:115], v[64:67], a[124:127]\n" \
"s_mov_b32 exec_lo, 0x0\n" \
"s_mov_b32 exec_hi, 0xff\n" \
"global_load_lds_dword v[184:185], off\n" \
"s_mov_b64 exec, -1\n" \
"v_mfma_f32_16x16x32_bf16 a[88:91], v[108:111], v[60:63], a[88:91]\n" \
"ds_read_b64_tr_b16 v[160:161], v35 offset:40960\n" \
"v_lshlrev_b32_e32 v235, 4, v233\n" \
"v_mfma_f32_16x16x32_bf16 a[120:123], v[108:111], v[64:67], a[120:123]\n" \
"ds_read_b64_tr_b16 v[162:163], v35 offset:41984\n" \
"v_add_u32_e64 v236, s7, v235\n" \
"v_mfma_f32_16x16x32_bf16 a[84:87], v[104:107], v[60:63], a[84:87]\n" \
"v_and_b32_e32 v236, 0xf0, v236\n" \
"v_cmp_gt_u32_e64 vcc, 16, v233\n" \
"v_mfma_f32_16x16x32_bf16 a[116:119], v[104:107], v[64:67], a[116:119]\n" \
"s_mov_b32 m0, s32\n" \
"ds_read_b64_tr_b16 v[164:165], v36 offset:40960\n" \
"v_mfma_f32_16x16x32_bf16 a[80:83], v[100:103], v[60:63], a[80:83]\n" \
"s_mov_b32 exec_lo, 0xffff0000\n" \
"s_mov_b32 exec_hi, 0x0\n" \
"global_load_lds_dword v[186:187], off\n" \
"s_mov_b64 exec, -1\n" \
"v_mfma_f32_16x16x32_bf16 a[112:115], v[100:103], v[64:67], a[112:115]\n" \
"ds_read_b64_tr_b16 v[166:167], v36 offset:41984\n" \
"v_cndmask_b32_e32 v233, v235, v236, vcc\n" \
"v_mfma_f32_16x16x32_bf16 a[76:79], v[96:99], v[60:63], a[76:79]\n" \
"v_or_b32_e32 v235, 0x180, v212\n" \
"v_mul_u32_u24_e32 v236, 0xe39, v235\n" \
"v_mfma_f32_16x16x32_bf16 a[108:111], v[96:99], v[64:67], a[108:111]\n" \
"v_lshrrev_b32_e64 v236, 16, v236\n" \
"v_mad_i32_i24 v235, v236, s40, v235\n" \
"v_mfma_f32_16x16x32_bf16 a[72:75], v[92:95], v[60:63], a[72:75]\n" \
"ds_read_b64_tr_b16 v[168:169], v37 offset:40960\n" \
"v_lshlrev_b32_e64 v237, 4, v235\n" \
"v_mfma_f32_16x16x32_bf16 a[104:107], v[92:95], v[64:67], a[104:107]\n" \
"v_add_u32_e64 v238, s7, v237\n" \
"v_and_b32_e32 v238, 0xf0, v238\n" \
"v_mfma_f32_16x16x32_bf16 a[68:71], v[88:91], v[60:63], a[68:71]\n" \
"ds_read_b64_tr_b16 v[170:171], v37 offset:41984\n" \
"v_cmp_gt_u32_e64 vcc, 16, v235\n" \
"v_mfma_f32_16x16x32_bf16 a[100:103], v[88:91], v[64:67], a[100:103]\n" \
"s_nop 0\n" \
"v_cndmask_b32_e32 v235, v237, v238, vcc\n" \
"v_or_b32_e32 v237, 0x1c0, v212\n" \
"v_mfma_f32_16x16x32_bf16 a[64:67], v[84:87], v[60:63], a[64:67]\n" \
"ds_read_b64_tr_b16 v[172:173], v38 offset:40960\n" \
"v_mul_u32_u24_e32 v238, 0xe39, v237\n" \
"v_mfma_f32_16x16x32_bf16 a[96:99], v[84:87], v[64:67], a[96:99]\n" \
"v_lshrrev_b32_e64 v238, 16, v238\n" \
"v_mad_i32_i24 v237, v238, s40, v237\n" \
"s_waitcnt lgkmcnt(15)\n" \
"v_mfma_f32_16x16x32_bf16 a[128:131], v[84:87], v[68:71], a[128:131]\n" \
"ds_read_b64_tr_b16 v[174:175], v38 offset:41984\n" \
"v_lshlrev_b32_e32 v239, 4, v237\n" \
"v_mfma_f32_16x16x32_bf16 a[160:163], v[84:87], v[72:75], a[160:163]\n" \
"v_add_u32_e64 v240, s7, v239\n" \
"v_and_b32_e32 v240, 0xf0, v240\n" \
"v_mfma_f32_16x16x32_bf16 a[132:135], v[88:91], v[68:71], a[132:135]\n" \
"ds_read_b64_tr_b16 v[176:177], v39 offset:40960\n" \
"v_cmp_gt_u32_e64 vcc, 16, v237\n" \
"v_mfma_f32_16x16x32_bf16 a[164:167], v[88:91], v[72:75], a[164:167]\n" \
"s_nop 0\n" \
"v_cndmask_b32_e32 v237, v239, v240, vcc\n" \
"v_or_b32_e32 v239, 0x200, v212\n" \
"v_mfma_f32_16x16x32_bf16 a[136:139], v[92:95], v[68:71], a[136:139]\n" \
"ds_read_b64_tr_b16 v[178:179], v39 offset:41984\n" \
"v_mul_u32_u24_e32 v240, 0xe39, v239\n" \
"v_mfma_f32_16x16x32_bf16 a[168:171], v[92:95], v[72:75], a[168:171]\n" \
"v_lshrrev_b32_e64 v240, 16, v240\n" \
"v_mad_i32_i24 v239, v240, s40, v239\n" \
"v_mfma_f32_16x16x32_bf16 a[140:143], v[96:99], v[68:71], a[140:143]\n" \
"v_lshlrev_b32_e32 v241, 4, v239\n" \
"v_add_u32_e32 v242, s7, v241\n" \
"v_mfma_f32_16x16x32_bf16 a[172:175], v[96:99], v[72:75], a[172:175]\n" \
"ds_read_b128 v[124:127], v29 offset:53248\n" \
"s_movk_i32 s7, 0x480\n" \
"v_mfma_f32_16x16x32_bf16 a[144:147], v[100:103], v[68:71], a[144:147]\n" \
"v_mad_u32_u24 v0, v223, s7, v224\n" \
"v_mul_u32_u24_e32 v223, 0x480, v226\n" \
"v_mfma_f32_16x16x32_bf16 a[176:179], v[100:103], v[72:75], a[176:179]\n" \
"ds_read_b128 v[128:131], v29 offset:55296\n" \
"s_movk_i32 s40, 0xfc00\n" \
"v_mfma_f32_16x16x32_bf16 a[148:151], v[104:107], v[68:71], a[148:151]\n" \
"v_add3_u32 v1, v223, v225, s40\n" \
"v_mul_u32_u24_e32 v223, 0x480, v228\n" \
"v_mfma_f32_16x16x32_bf16 a[180:183], v[104:107], v[72:75], a[180:183]\n" \
"ds_read_b128 v[132:135], v29 offset:32768\n" \
"v_add3_u32 v2, v223, v227, s41\n" \
"v_mfma_f32_16x16x32_bf16 a[152:155], v[108:111], v[68:71], a[152:155]\n" \
"v_mul_u32_u24_e32 v223, 0x480, v230\n" \
"v_add3_u32 v3, v223, v229, s42\n" \
"v_mfma_f32_16x16x32_bf16 a[184:187], v[108:111], v[72:75], a[184:187]\n" \
"ds_read_b128 v[136:139], v29 offset:34816\n" \
"v_mul_u32_u24_e32 v223, 0x480, v234\n" \
"v_mfma_f32_16x16x32_bf16 a[156:159], v[112:115], v[68:71], a[156:159]\n" \
"v_add3_u32 v5, v223, v233, s40\n" \
"v_mul_u32_u24_e32 v223, 0x480, v236\n" \
"v_mfma_f32_16x16x32_bf16 a[188:191], v[112:115], v[72:75], a[188:191]\n" \
"ds_read_b128 v[140:143], v29 offset:36864\n" \
"v_add3_u32 v6, v223, v235, s41\n" \
"v_mfma_f32_16x16x32_bf16 a[220:223], v[112:115], v[76:79], a[220:223]\n" \
"v_mul_u32_u24_e32 v223, 0x480, v238\n" \
"s_lshl_b32 s40, s3, 3\n" \
"v_mfma_f32_16x16x32_bf16 a[252:255], v[112:115], v[80:83], a[252:255]\n" \
"ds_read_b128 v[144:147], v29 offset:38912\n" \
"v_add3_u32 v7, v223, v237, s42\n" \
"v_mfma_f32_16x16x32_bf16 a[216:219], v[108:111], v[76:79], a[216:219]\n" \
"s_and_b32 s40, s40, 16\n" \
"s_and_b32 s42, s44, 2\n" \
"v_mfma_f32_16x16x32_bf16 a[248:251], v[108:111], v[80:83], a[248:251]\n" \
"s_and_b32 s41, s2, 7\n" \
"v_bfe_u32 v223, v20, 7, 1\n" \
"v_mfma_f32_16x16x32_bf16 a[212:215], v[104:107], v[76:79], a[212:215]\n" \
"s_or_b32 s40, s42, s40\n" \
"v_and_b32_e32 v242, 0xf0, v242\n" \
"v_mfma_f32_16x16x32_bf16 a[244:247], v[104:107], v[80:83], a[244:247]\n" \
"v_cmp_gt_u32_e32 vcc, 16, v239\n" \
"v_or3_b32 v222, s40, v223, v222\n" \
"v_mfma_f32_16x16x32_bf16 a[208:211], v[100:103], v[76:79], a[208:211]\n" \
"v_and_b32_e32 v223, 32, v20\n" \
"s_lshl_b32 s40, s41, 5\n" \
"v_mfma_f32_16x16x32_bf16 a[240:243], v[100:103], v[80:83], a[240:243]\n" \
"v_cndmask_b32_e32 v239, v241, v242, vcc\n" \
"v_mul_u32_u24_e32 v222, 18, v222\n" \
"v_mfma_f32_16x16x32_bf16 a[204:207], v[96:99], v[76:79], a[204:207]\n" \
"v_mov_b32_e32 v224, 0x100\n" \
"v_mov_b32_e64 v225, s40\n" \
"v_mfma_f32_16x16x32_bf16 a[236:239], v[96:99], v[80:83], a[236:239]\n" \
"v_cmp_eq_u32_e64 vcc, 0, v223\n" \
"v_and_or_b32 v222, v213, 1, v222\n" \
"v_mfma_f32_16x16x32_bf16 a[200:203], v[92:95], v[76:79], a[200:203]\n" \
"v_mad_u32_u24 v4, v232, s7, v231\n" \
"v_cndmask_b32_e64 v223, v224, v225, vcc\n" \
"v_mfma_f32_16x16x32_bf16 a[232:235], v[92:95], v[80:83], a[232:235]\n" \
"v_mad_u32_u24 v8, v240, s7, v239\n" \
"v_mad_u32_u24 v222, v222, s7, v223\n" \
"v_mfma_f32_16x16x32_bf16 a[196:199], v[88:91], v[76:79], a[196:199]\n" \
"s_lshl_b32 s7, s41, 2\n" \
"s_mul_i32 s42, s5, 0xa00\n" \
"v_mfma_f32_16x16x32_bf16 a[228:231], v[88:91], v[80:83], a[228:231]\n" \
"s_or_b32 s45, s7, s43\n" \
"s_add_i32 s42, s42, 0x5bc1000\n" \
"v_mfma_f32_16x16x32_bf16 a[192:195], v[84:87], v[76:79], a[192:195]\n" \
"s_lshl_b32 s46, s45, 2\n" \
"s_or_b32 s46, s46, s42\n" \
"v_mfma_f32_16x16x32_bf16 a[224:227], v[84:87], v[80:83], a[224:227]\n" \
"s_waitcnt vmcnt(20)\n" \
"v_readfirstlane_b32 s62, v180\n" \
"s_nop 3\n" \
"s_add_u32 s62, s62, 0x9E3779B1\n" \
"s_waitcnt vmcnt(2) lgkmcnt(0)\n" \
"s_barrier\n" \
"v_mfma_f32_16x16x32_bf16 a[0:3], v[148:151], v[116:119], a[0:3]\n" \
"s_add_u32 m0, s30, 0x8000\n" \
"s_mov_b32 s85, 0x180\n" \
"v_mfma_f32_16x16x32_bf16 a[32:35], v[148:151], v[120:123], a[32:35]\n" \
"buffer_load_dwordx4 v22, s[64:67], s85 offen lds\n" \
"v_mfma_f32_16x16x32_bf16 a[4:7], v[152:155], v[116:119], a[4:7]\n" \
"ds_read_b64_tr_b16 v[84:85], v32 offset:0\n" \
"v_writelane_b32 v11, s46, 0\n" \
"v_mfma_f32_16x16x32_bf16 a[36:39], v[152:155], v[120:123], a[36:39]\n" \
"ds_read_b64_tr_b16 v[86:87], v32 offset:1024\n" \
"s_lshl_b32 s46, s5, 3\n" \
"v_mfma_f32_16x16x32_bf16 a[8:11], v[156:159], v[116:119], a[8:11]\n" \
"s_add_u32 m0, s30, 0x8400\n" \
"s_mov_b32 s84, 0x2180\n" \
"v_mfma_f32_16x16x32_bf16 a[40:43], v[156:159], v[120:123], a[40:43]\n" \
"buffer_load_dwordx4 v23, s[64:67], s84 offen lds\n" \
"v_mfma_f32_16x16x32_bf16 a[12:15], v[160:163], v[116:119], a[12:15]\n" \
"ds_read_b128 v[52:55], v28 offset:16384\n" \
"v_writelane_b32 v11, s42, 1\n" \
"v_mfma_f32_16x16x32_bf16 a[44:47], v[160:163], v[120:123], a[44:47]\n" \
"ds_read_b128 v[56:59], v28 offset:18432\n" \
"s_or_b32 s42, s46, s41\n" \
"v_mfma_f32_16x16x32_bf16 a[16:19], v[164:167], v[116:119], a[16:19]\n" \
"s_add_u32 m0, s31, 0x8000\n" \
"s_mov_b32 s85, 0x180000\n" \
"v_mfma_f32_16x16x32_bf16 a[48:51], v[164:167], v[120:123], a[48:51]\n" \
"buffer_load_dwordx4 v26, s[68:71], s85 offen lds\n" \
"v_mfma_f32_16x16x32_bf16 a[20:23], v[168:171], v[116:119], a[20:23]\n" \
"ds_read_b64_tr_b16 v[88:89], v33 offset:0\n" \
"s_lshl_b32 s42, s42, 10\n" \
"v_mfma_f32_16x16x32_bf16 a[52:55], v[168:171], v[120:123], a[52:55]\n" \
"ds_read_b64_tr_b16 v[90:91], v33 offset:1024\n" \
"s_or_b32 s42, s42, s47\n" \
"s_nop 0\n" \
"v_mfma_f32_16x16x32_bf16 a[24:27], v[172:175], v[116:119], a[24:27]\n" \
"s_add_u32 m0, s31, 0xc000\n" \
"s_mov_b32 s84, 0x180000\n" \
"v_mfma_f32_16x16x32_bf16 a[56:59], v[172:175], v[120:123], a[56:59]\n" \
"buffer_load_dwordx4 v26, s[72:75], s84 offen lds\n" \
"v_mfma_f32_16x16x32_bf16 a[28:31], v[176:179], v[116:119], a[28:31]\n" \
"ds_read_b64_tr_b16 v[92:93], v34 offset:0\n" \
"s_or_b32 s47, s42, 0x5c00200\n" \
"v_mfma_f32_16x16x32_bf16 a[60:63], v[176:179], v[120:123], a[60:63]\n" \
"ds_read_b64_tr_b16 v[94:95], v34 offset:1024\n" \
"v_writelane_b32 v11, s47, 2\n" \
"v_mfma_f32_16x16x32_bf16 a[92:95], v[176:179], v[124:127], a[92:95]\n" \
"s_add_u32 m0, s30, 0x8800\n" \
"s_mov_b32 s85, 0x4180\n" \
"v_mfma_f32_16x16x32_bf16 a[124:127], v[176:179], v[128:131], a[124:127]\n" \
"buffer_load_dwordx4 v22, s[64:67], s85 offen lds\n" \
"v_mfma_f32_16x16x32_bf16 a[88:91], v[172:175], v[124:127], a[88:91]\n" \
"ds_read_b64_tr_b16 v[96:97], v35 offset:0\n" \
"s_lshl_b32 s47, s43, 2\n" \
"v_mfma_f32_16x16x32_bf16 a[120:123], v[172:175], v[128:131], a[120:123]\n" \
"ds_read_b64_tr_b16 v[98:99], v35 offset:1024\n" \
"s_or_b32 s48, s47, s42\n" \
"v_mfma_f32_16x16x32_bf16 a[84:87], v[168:171], v[124:127], a[84:87]\n" \
"s_add_u32 m0, s30, 0x8c00\n" \
"s_mov_b32 s84, 0x6180\n" \
"v_mfma_f32_16x16x32_bf16 a[116:119], v[168:171], v[128:131], a[116:119]\n" \
"buffer_load_dwordx4 v23, s[64:67], s84 offen lds\n" \
"v_mfma_f32_16x16x32_bf16 a[80:83], v[164:167], v[124:127], a[80:83]\n" \
"ds_read_b64_tr_b16 v[100:101], v36 offset:0\n" \
"s_or_b32 s48, s48, 0x5c00000\n" \
"v_mfma_f32_16x16x32_bf16 a[112:115], v[164:167], v[128:131], a[112:115]\n" \
"ds_read_b64_tr_b16 v[102:103], v36 offset:1024\n" \
"v_writelane_b32 v11, s48, 3\n" \
"v_mfma_f32_16x16x32_bf16 a[76:79], v[160:163], v[124:127], a[76:79]\n" \
"s_add_u32 m0, s31, 0x8400\n" \
"s_mov_b32 s85, 0x188000\n" \
"v_mfma_f32_16x16x32_bf16 a[108:111], v[160:163], v[128:131], a[108:111]\n" \
"buffer_load_dwordx4 v26, s[68:71], s85 offen lds\n" \
"v_mfma_f32_16x16x32_bf16 a[72:75], v[156:159], v[124:127], a[72:75]\n" \
"ds_read_b64_tr_b16 v[104:105], v37 offset:0\n" \
"s_or_b32 s42, s42, 0x5c00080\n" \
"v_mfma_f32_16x16x32_bf16 a[104:107], v[156:159], v[128:131], a[104:107]\n" \
"v_writelane_b32 v11, s42, 4\n" \
"s_or_b32 s42, s42, s47\n" \
"v_mfma_f32_16x16x32_bf16 a[68:71], v[152:155], v[124:127], a[68:71]\n" \
"s_add_u32 m0, s31, 0xc400\n" \
"s_mov_b32 s84, 0x188000\n" \
"v_mfma_f32_16x16x32_bf16 a[100:103], v[152:155], v[128:131], a[100:103]\n" \
"buffer_load_dwordx4 v26, s[72:75], s84 offen lds\n" \
"v_mfma_f32_16x16x32_bf16 a[64:67], v[148:151], v[124:127], a[64:67]\n" \
"ds_read_b64_tr_b16 v[106:107], v37 offset:1024\n" \
"s_add_i32 s47, s3, s2\n" \
"v_mfma_f32_16x16x32_bf16 a[96:99], v[148:151], v[128:131], a[96:99]\n" \
"ds_read_b64_tr_b16 v[108:109], v38 offset:0\n" \
"v_writelane_b32 v11, s42, 5\n" \
"v_mfma_f32_16x16x32_bf16 a[128:131], v[148:151], v[132:135], a[128:131]\n" \
"s_add_u32 m0, s30, 0x9000\n" \
"s_mov_b32 s85, 0x8180\n" \
"v_mfma_f32_16x16x32_bf16 a[160:163], v[148:151], v[136:139], a[160:163]\n" \
"buffer_load_dwordx4 v22, s[64:67], s85 offen lds\n" \
"v_mfma_f32_16x16x32_bf16 a[132:135], v[152:155], v[132:135], a[132:135]\n" \
"ds_read_b64_tr_b16 v[110:111], v38 offset:1024\n" \
"s_mov_b32 s42, 2.0\n" \
"s_nop 0\n" \
"v_mfma_f32_16x16x32_bf16 a[164:167], v[152:155], v[136:139], a[164:167]\n" \
"ds_read_b64_tr_b16 v[112:113], v39 offset:0\n" \
"s_mov_b32 s48, 0x5bc0000\n" \
"v_mfma_f32_16x16x32_bf16 a[136:139], v[156:159], v[132:135], a[136:139]\n" \
"s_add_u32 m0, s30, 0x9400\n" \
"s_mov_b32 s84, 0xa180\n" \
"v_mfma_f32_16x16x32_bf16 a[168:171], v[156:159], v[136:139], a[168:171]\n" \
"buffer_load_dwordx4 v23, s[64:67], s84 offen lds\n" \
"v_mfma_f32_16x16x32_bf16 a[140:143], v[160:163], v[132:135], a[140:143]\n" \
"ds_read_b64_tr_b16 v[114:115], v39 offset:1024\n" \
"v_mfma_f32_16x16x32_bf16 a[172:175], v[160:163], v[136:139], a[172:175]\n" \
"ds_read_b128 v[60:63], v28 offset:20480\n" \
"v_mfma_f32_16x16x32_bf16 a[144:147], v[164:167], v[132:135], a[144:147]\n" \
"s_add_u32 m0, s31, 0x8800\n" \
"s_mov_b32 s85, 0x190000\n" \
"v_mfma_f32_16x16x32_bf16 a[176:179], v[164:167], v[136:139], a[176:179]\n" \
"buffer_load_dwordx4 v27, s[68:71], s85 offen lds\n" \
"v_mfma_f32_16x16x32_bf16 a[148:151], v[168:171], v[132:135], a[148:151]\n" \
"ds_read_b128 v[64:67], v28 offset:22528\n" \
"v_mfma_f32_16x16x32_bf16 a[180:183], v[168:171], v[136:139], a[180:183]\n" \
"ds_read_b128 v[68:71], v28 offset:0\n" \
"v_mfma_f32_16x16x32_bf16 a[152:155], v[172:175], v[132:135], a[152:155]\n" \
"s_add_u32 m0, s31, 0xc800\n" \
"s_mov_b32 s84, 0x190000\n" \
"v_mfma_f32_16x16x32_bf16 a[184:187], v[172:175], v[136:139], a[184:187]\n" \
"buffer_load_dwordx4 v27, s[72:75], s84 offen lds\n" \
"v_mfma_f32_16x16x32_bf16 a[156:159], v[176:179], v[132:135], a[156:159]\n" \
"ds_read_b128 v[72:75], v28 offset:2048\n" \
"v_mfma_f32_16x16x32_bf16 a[188:191], v[176:179], v[136:139], a[188:191]\n" \
"ds_read_b128 v[76:79], v28 offset:4096\n" \
"v_mfma_f32_16x16x32_bf16 a[220:223], v[176:179], v[140:143], a[220:223]\n" \
"s_cmp_lg_u32 s47, 0\n" \
"s_cselect_b32 s47, s42, s48\n" \
"v_mfma_f32_16x16x32_bf16 a[252:255], v[176:179], v[144:147], a[252:255]\n" \
"ds_read_b128 v[80:83], v28 offset:6144\n" \
"s_and_b32 s44, s44, 4\n" \
"v_mfma_f32_16x16x32_bf16 a[216:219], v[172:175], v[140:143], a[216:219]\n" \
"v_writelane_b32 v11, s47, 52\n" \
"s_or_b32 s47, s44, s4\n" \
"v_mfma_f32_16x16x32_bf16 a[248:251], v[172:175], v[144:147], a[248:251]\n" \
"s_lshr_b32 s48, s2, 3\n" \
"v_mfma_f32_16x16x32_bf16 a[212:215], v[168:171], v[140:143], a[212:215]\n" \
"s_add_u32 s44, s22, s50\n" \
"s_addc_u32 s51, s23, 0\n" \
"v_mfma_f32_16x16x32_bf16 a[244:247], v[168:171], v[144:147], a[244:247]\n" \
"v_writelane_b32 v11, s44, 9\n" \
"v_mfma_f32_16x16x32_bf16 a[208:211], v[164:167], v[140:143], a[208:211]\n" \
"s_add_u32 s52, s24, s50\n" \
"s_addc_u32 s53, s25, 0\n" \
"v_mfma_f32_16x16x32_bf16 a[240:243], v[164:167], v[144:147], a[240:243]\n" \
"s_and_b32 s54, s51, 0xffff\n" \
"v_writelane_b32 v11, s54, 10\n" \
"v_mfma_f32_16x16x32_bf16 a[204:207], v[160:163], v[140:143], a[204:207]\n" \
"s_and_b32 s54, s53, 0xffff\n" \
"v_writelane_b32 v11, s52, 11\n" \
"v_mfma_f32_16x16x32_bf16 a[236:239], v[160:163], v[144:147], a[236:239]\n" \
"s_cmpk_lt_u32 s3, 3\n" \
"s_cselect_b32 s44, s44, s52\n" \
"v_mfma_f32_16x16x32_bf16 a[200:203], v[156:159], v[140:143], a[200:203]\n" \
"s_movk_i32 s52, 0xfff\n" \
"v_writelane_b32 v11, s54, 12\n" \
"v_mfma_f32_16x16x32_bf16 a[232:235], v[156:159], v[144:147], a[232:235]\n" \
"s_and_b32 s54, s29, 0xffff\n" \
"v_writelane_b32 v11, s28, 13\n" \
"v_mfma_f32_16x16x32_bf16 a[196:199], v[152:155], v[140:143], a[196:199]\n" \
"v_mul_u32_u24_e32 v213, 0x120, v213\n" \
"v_writelane_b32 v11, s54, 14\n" \
"v_mfma_f32_16x16x32_bf16 a[228:231], v[152:155], v[144:147], a[228:231]\n" \
"v_lshlrev_b32_e64 v212, 3, v212\n" \
"v_writelane_b32 v11, s44, 15\n" \
"v_mfma_f32_16x16x32_bf16 a[192:195], v[148:151], v[140:143], a[192:195]\n" \
"s_cmpk_lt_u32 s3, 3\n" \
"s_cselect_b32 s44, s51, s53\n" \
"v_mfma_f32_16x16x32_bf16 a[224:227], v[148:151], v[144:147], a[224:227]\n" \
"s_waitcnt lgkmcnt(15)\n" \
"s_nop 0\n" \
"v_mfma_f32_16x16x32_bf16 a[0:3], v[84:87], v[52:55], a[0:3]\n" \
"s_add_u32 m0, s30, 0x9800\n" \
"s_mov_b32 s85, 0xc180\n" \
"v_mfma_f32_16x16x32_bf16 a[32:35], v[84:87], v[56:59], a[32:35]\n" \
"buffer_load_dwordx4 v22, s[64:67], s85 offen lds\n" \
"v_mfma_f32_16x16x32_bf16 a[4:7], v[88:91], v[52:55], a[4:7]\n" \
"ds_read_b64_tr_b16 v[148:149], v32 offset:8192\n" \
"s_and_b32 s44, s44, 0xffff\n" \
"v_mfma_f32_16x16x32_bf16 a[36:39], v[88:91], v[56:59], a[36:39]\n" \
"s_add_u32 m0, s30, 0x9c00\n" \
"s_mov_b32 s84, 0xe180\n" \
"v_mfma_f32_16x16x32_bf16 a[8:11], v[92:95], v[52:55], a[8:11]\n" \
"buffer_load_dwordx4 v23, s[64:67], s84 offen lds\n" \
"v_mfma_f32_16x16x32_bf16 a[40:43], v[92:95], v[56:59], a[40:43]\n" \
"ds_read_b64_tr_b16 v[150:151], v32 offset:9216\n" \
"v_writelane_b32 v11, s44, 16\n" \
"s_waitcnt lgkmcnt(15)\n" \
"v_mfma_f32_16x16x32_bf16 a[12:15], v[96:99], v[52:55], a[12:15]\n" \
"s_add_u32 m0, s31, 0x8c00\n" \
"s_mov_b32 s85, 0x198000\n" \
"v_mfma_f32_16x16x32_bf16 a[44:47], v[96:99], v[56:59], a[44:47]\n" \
"buffer_load_dwordx4 v27, s[68:71], s85 offen lds\n" \
"s_waitcnt lgkmcnt(14)\n" \
"v_mfma_f32_16x16x32_bf16 a[16:19], v[100:103], v[52:55], a[16:19]\n" \
"ds_read_b128 v[116:119], v29 offset:16384\n" \
"s_mul_i32 s44, s41, 0x48\n" \
"v_mfma_f32_16x16x32_bf16 a[48:51], v[100:103], v[56:59], a[48:51]\n" \
"s_add_u32 m0, s31, 0xcc00\n" \
"s_mov_b32 s84, 0x198000\n" \
"s_waitcnt lgkmcnt(13)\n" \
"v_mfma_f32_16x16x32_bf16 a[20:23], v[104:107], v[52:55], a[20:23]\n" \
"buffer_load_dwordx4 v27, s[72:75], s84 offen lds\n" \
"v_mfma_f32_16x16x32_bf16 a[52:55], v[104:107], v[56:59], a[52:55]\n" \
"ds_read_b128 v[120:123], v29 offset:18432\n" \
"s_add_i32 s51, s44, 0x47\n" \
"s_waitcnt lgkmcnt(12)\n" \
"v_mfma_f32_16x16x32_bf16 a[24:27], v[108:111], v[52:55], a[24:27]\n" \
"ds_read_b64_tr_b16 v[152:153], v33 offset:8192\n" \
"v_mfma_f32_16x16x32_bf16 a[56:59], v[108:111], v[56:59], a[56:59]\n" \
"ds_read_b64_tr_b16 v[154:155], v33 offset:9216\n" \
"s_waitcnt lgkmcnt(12)\n" \
"v_mfma_f32_16x16x32_bf16 a[28:31], v[112:115], v[52:55], a[28:31]\n" \
"ds_read_b64_tr_b16 v[156:157], v34 offset:8192\n" \
"v_mfma_f32_16x16x32_bf16 a[60:63], v[112:115], v[56:59], a[60:63]\n" \
"ds_read_b64_tr_b16 v[158:159], v34 offset:9216\n" \
"s_waitcnt lgkmcnt(13)\n" \
"v_mfma_f32_16x16x32_bf16 a[92:95], v[112:115], v[60:63], a[92:95]\n" \
"s_cmpk_lt_u32 s3, 3\n" \
"s_cselect_b32 s51, s51, s52\n" \
"s_waitcnt lgkmcnt(12)\n" \
"v_mfma_f32_16x16x32_bf16 a[124:127], v[112:115], v[64:67], a[124:127]\n" \
"buffer_load_dwordx4 v[200:203], v192, s[76:79], s34 offen offset:0\n" \
"v_mfma_f32_16x16x32_bf16 a[88:91], v[108:111], v[60:63], a[88:91]\n" \
"s_mov_b32 m0, s32\n" \
"ds_read_b64_tr_b16 v[160:161], v35 offset:8192\n" \
"v_mfma_f32_16x16x32_bf16 a[120:123], v[108:111], v[64:67], a[120:123]\n" \
"s_mov_b32 exec_lo, 0x0\n" \
"s_mov_b32 exec_hi, 0xff00\n" \
"global_load_lds_dword v[184:185], off\n" \
"s_mov_b64 exec, -1\n" \
"v_mfma_f32_16x16x32_bf16 a[84:87], v[104:107], v[60:63], a[84:87]\n" \
"ds_read_b64_tr_b16 v[162:163], v35 offset:9216\n" \
"s_mulk_i32 s51, 0x480\n" \
"v_mfma_f32_16x16x32_bf16 a[116:119], v[104:107], v[64:67], a[116:119]\n" \
"buffer_load_dwordx4 v[204:207], v193, s[76:79], s34 offen offset:0\n" \
"v_mfma_f32_16x16x32_bf16 a[80:83], v[100:103], v[60:63], a[80:83]\n" \
"ds_read_b64_tr_b16 v[164:165], v36 offset:8192\n" \
"s_addk_i32 s51, 0x120\n" \
"v_mfma_f32_16x16x32_bf16 a[112:115], v[100:103], v[64:67], a[112:115]\n" \
"ds_read_b64_tr_b16 v[166:167], v36 offset:9216\n" \
"v_writelane_b32 v11, s51, 17\n" \
"v_mfma_f32_16x16x32_bf16 a[76:79], v[96:99], v[60:63], a[76:79]\n" \
"s_lshl_b32 s51, s3, 5\n" \
"s_add_i32 s44, s44, s51\n" \
"v_mfma_f32_16x16x32_bf16 a[108:111], v[96:99], v[64:67], a[108:111]\n" \
"s_mul_i32 s51, s44, 0x480\n" \
"s_mul_i32 s44, s45, 0x24000\n" \
"v_mfma_f32_16x16x32_bf16 a[72:75], v[92:95], v[60:63], a[72:75]\n" \
"buffer_load_dword v208, v194, s[76:79], s34 offen offset:32\n" \
"v_mfma_f32_16x16x32_bf16 a[104:107], v[92:95], v[64:67], a[104:107]\n" \
"ds_read_b64_tr_b16 v[168:169], v37 offset:8192\n" \
"s_add_i32 s52, s44, 0x1b000\n" \
"v_mfma_f32_16x16x32_bf16 a[68:71], v[88:91], v[60:63], a[68:71]\n" \
"ds_read_b64_tr_b16 v[170:171], v37 offset:9216\n" \
"v_mfma_f32_16x16x32_bf16 a[100:103], v[88:91], v[64:67], a[100:103]\n" \
"s_cmpk_lt_u32 s3, 3\n" \
"s_cselect_b32 s51, s51, s52\n" \
"v_mfma_f32_16x16x32_bf16 a[64:67], v[84:87], v[60:63], a[64:67]\n" \
"buffer_load_dword v209, v194, s[76:79], s34 offen offset:1184\n" \
"v_mfma_f32_16x16x32_bf16 a[96:99], v[84:87], v[64:67], a[96:99]\n" \
"ds_read_b64_tr_b16 v[172:173], v38 offset:8192\n" \
"s_mul_i32 s52, s3, 0x9000\n" \
"s_waitcnt lgkmcnt(15)\n" \
"v_mfma_f32_16x16x32_bf16 a[128:131], v[84:87], v[68:71], a[128:131]\n" \
"ds_read_b64_tr_b16 v[174:175], v38 offset:9216\n" \
"v_writelane_b32 v11, s51, 18\n" \
"v_mfma_f32_16x16x32_bf16 a[160:163], v[84:87], v[72:75], a[160:163]\n" \
"s_mul_i32 s51, s49, 0x2400\n" \
"s_bitset1_b32 s51, 17\n" \
"v_mfma_f32_16x16x32_bf16 a[132:135], v[88:91], v[68:71], a[132:135]\n" \
"buffer_load_dword v211, v194, s[76:79], s34 offen offset:2336\n" \
"v_mfma_f32_16x16x32_bf16 a[164:167], v[88:91], v[72:75], a[164:167]\n" \
"ds_read_b64_tr_b16 v[176:177], v39 offset:8192\n" \
"v_writelane_b32 v11, s51, 19\n" \
"v_mfma_f32_16x16x32_bf16 a[136:139], v[92:95], v[68:71], a[136:139]\n" \
"ds_read_b64_tr_b16 v[178:179], v39 offset:9216\n" \
"s_mul_i32 s49, s49, 0x9000\n" \
"v_mfma_f32_16x16x32_bf16 a[168:171], v[92:95], v[72:75], a[168:171]\n" \
"s_add_i32 s49, s44, s49\n" \
"v_writelane_b32 v11, s49, 24\n" \
"v_mfma_f32_16x16x32_bf16 a[140:143], v[96:99], v[68:71], a[140:143]\n" \
"s_mov_b32 s51, 0x12000\n" \
"s_mul_i32 s49, s3, 0x2400\n" \
"v_mfma_f32_16x16x32_bf16 a[172:175], v[96:99], v[72:75], a[172:175]\n" \
"s_mov_b32 s76, s20\n" \
"s_and_b32 s77, s21, 0xffff\n" \
"v_mfma_f32_16x16x32_bf16 a[144:147], v[100:103], v[68:71], a[144:147]\n" \
"s_mov_b32 s78, 0x480\n" \
"ds_read_b128 v[124:127], v29 offset:20480\n" \
"v_mfma_f32_16x16x32_bf16 a[176:179], v[100:103], v[72:75], a[176:179]\n" \
"buffer_load_ushort v210, v195, s[76:79], 0 offen\n" \
"v_mfma_f32_16x16x32_bf16 a[148:151], v[104:107], v[68:71], a[148:151]\n" \
"ds_read_b128 v[128:131], v29 offset:22528\n" \
"v_mfma_f32_16x16x32_bf16 a[180:183], v[104:107], v[72:75], a[180:183]\n" \
"s_mov_b32 s76, s8\n" \
"s_and_b32 s77, s9, 0xffff\n" \
"v_mfma_f32_16x16x32_bf16 a[152:155], v[108:111], v[68:71], a[152:155]\n" \
"s_mov_b32 s78, 0x240000\n" \
"ds_read_b128 v[132:135], v29 offset:0\n" \
"v_mfma_f32_16x16x32_bf16 a[184:187], v[108:111], v[72:75], a[184:187]\n" \
"ds_read_b128 v[136:139], v29 offset:2048\n" \
"v_mfma_f32_16x16x32_bf16 a[156:159], v[112:115], v[68:71], a[156:159]\n" \
"s_cmpk_lt_u32 s3, 3\n" \
"s_cselect_b32 s51, s49, s51\n" \
"v_mfma_f32_16x16x32_bf16 a[188:191], v[112:115], v[72:75], a[188:191]\n" \
"ds_read_b128 v[140:143], v29 offset:4096\n" \
"s_xor_b32 s45, s45, 2\n" \
"v_mfma_f32_16x16x32_bf16 a[220:223], v[112:115], v[76:79], a[220:223]\n" \
"v_writelane_b32 v11, s51, 20\n" \
"s_mov_b32 s51, 0xfc00\n" \
"v_mfma_f32_16x16x32_bf16 a[252:255], v[112:115], v[80:83], a[252:255]\n" \
"ds_read_b128 v[144:147], v29 offset:6144\n" \
"v_writelane_b32 v11, s49, 21\n" \
"v_mfma_f32_16x16x32_bf16 a[216:219], v[108:111], v[76:79], a[216:219]\n" \
"s_bitset1_b32 s49, 17\n" \
"v_mfma_f32_16x16x32_bf16 a[248:251], v[108:111], v[80:83], a[248:251]\n" \
"s_cmpk_lt_u32 s3, 3\n" \
"s_cselect_b32 s49, s49, s51\n" \
"v_mfma_f32_16x16x32_bf16 a[212:215], v[104:107], v[76:79], a[212:215]\n" \
"v_and_or_b32 v10, v212, 24, v213\n" \
"v_writelane_b32 v11, s49, 22\n" \
"v_mfma_f32_16x16x32_bf16 a[244:247], v[104:107], v[80:83], a[244:247]\n" \
"s_lshl_b32 s49, s3, 10\n" \
"s_add_i32 s49, s49, 0x1d400\n" \
"v_mfma_f32_16x16x32_bf16 a[208:211], v[100:103], v[76:79], a[208:211]\n" \
"v_writelane_b32 v11, s49, 23\n" \
"s_add_i32 s49, s48, 1\n" \
"v_mfma_f32_16x16x32_bf16 a[240:243], v[100:103], v[80:83], a[240:243]\n" \
"s_and_b32 s49, s49, 3\n" \
"s_or_b32 s49, s49, s7\n" \
"v_mfma_f32_16x16x32_bf16 a[204:207], v[96:99], v[76:79], a[204:207]\n" \
"s_mul_i32 s51, s49, 0x24000\n" \
"s_add_i32 s51, s51, s52\n" \
"v_mfma_f32_16x16x32_bf16 a[236:239], v[96:99], v[80:83], a[236:239]\n" \
"s_add_i32 s48, s48, -1\n" \
"v_writelane_b32 v11, s51, 25\n" \
"v_mfma_f32_16x16x32_bf16 a[200:203], v[92:95], v[76:79], a[200:203]\n" \
"s_mul_i32 s51, s45, 0x24000\n" \
"s_and_b32 s48, s48, 3\n" \
"v_mfma_f32_16x16x32_bf16 a[232:235], v[92:95], v[80:83], a[232:235]\n" \
"s_add_i32 s51, s51, s52\n" \
"s_or_b32 s48, s48, s7\n" \
"v_mfma_f32_16x16x32_bf16 a[196:199], v[88:91], v[76:79], a[196:199]\n" \
"v_writelane_b32 v11, s51, 26\n" \
"s_mul_i32 s51, s48, 0x24000\n" \
"v_mfma_f32_16x16x32_bf16 a[228:231], v[88:91], v[80:83], a[228:231]\n" \
"s_add_i32 s51, s51, s52\n" \
"v_writelane_b32 v11, s51, 27\n" \
"v_mfma_f32_16x16x32_bf16 a[192:195], v[84:87], v[76:79], a[192:195]\n" \
"s_or_b32 s51, s47, s46\n" \
"s_mul_i32 s51, s51, 0x24800\n" \
"v_mfma_f32_16x16x32_bf16 a[224:227], v[84:87], v[80:83], a[224:227]\n" \
"s_waitcnt vmcnt(7) lgkmcnt(0)\n" \
"s_barrier\n" \
"v_mfma_f32_16x16x32_bf16 a[0:3], v[148:151], v[116:119], a[0:3]\n" \
"s_add_u32 m0, s30, 0x0\n" \
"s_mov_b32 s85, 0x200\n" \
"v_mfma_f32_16x16x32_bf16 a[32:35], v[148:151], v[120:123], a[32:35]\n" \
"buffer_load_dwordx4 v22, s[64:67], s85 offen lds\n" \
"v_mfma_f32_16x16x32_bf16 a[4:7], v[152:155], v[116:119], a[4:7]\n" \
"ds_read_b64_tr_b16 v[84:85], v32 offset:32768\n" \
"s_lshl_b32 s49, s49, 12\n" \
"v_mfma_f32_16x16x32_bf16 a[36:39], v[152:155], v[120:123], a[36:39]\n" \
"ds_read_b64_tr_b16 v[86:87], v32 offset:33792\n" \
"s_add_i32 s52, s51, 0x3004800\n" \
"v_mfma_f32_16x16x32_bf16 a[8:11], v[156:159], v[116:119], a[8:11]\n" \
"s_add_u32 m0, s30, 0x400\n" \
"s_mov_b32 s84, 0x2200\n" \
"v_mfma_f32_16x16x32_bf16 a[40:43], v[156:159], v[120:123], a[40:43]\n" \
"buffer_load_dwordx4 v23, s[64:67], s84 offen lds\n" \
"v_mfma_f32_16x16x32_bf16 a[12:15], v[160:163], v[116:119], a[12:15]\n" \
"ds_read_b128 v[52:55], v28 offset:49152\n" \
"s_add_i32 s53, s52, s49\n" \
"v_mfma_f32_16x16x32_bf16 a[44:47], v[160:163], v[120:123], a[44:47]\n" \
"ds_read_b128 v[56:59], v28 offset:51200\n" \
"v_writelane_b32 v11, s53, 28\n" \
"v_mfma_f32_16x16x32_bf16 a[16:19], v[164:167], v[116:119], a[16:19]\n" \
"s_add_u32 m0, s31, 0x0\n" \
"s_mov_b32 s85, 0x200000\n" \
"v_mfma_f32_16x16x32_bf16 a[48:51], v[164:167], v[120:123], a[48:51]\n" \
"buffer_load_dwordx4 v26, s[68:71], s85 offen lds\n" \
"v_mfma_f32_16x16x32_bf16 a[20:23], v[168:171], v[116:119], a[20:23]\n" \
"ds_read_b64_tr_b16 v[88:89], v33 offset:32768\n" \
"s_lshl_b32 s45, s45, 12\n" \
"v_mfma_f32_16x16x32_bf16 a[52:55], v[168:171], v[120:123], a[52:55]\n" \
"ds_read_b64_tr_b16 v[90:91], v33 offset:33792\n" \
"s_lshl_b32 s48, s48, 12\n" \
"v_mfma_f32_16x16x32_bf16 a[24:27], v[172:175], v[116:119], a[24:27]\n" \
"s_add_u32 m0, s31, 0x4000\n" \
"s_mov_b32 s84, 0x200000\n" \
"v_mfma_f32_16x16x32_bf16 a[56:59], v[172:175], v[120:123], a[56:59]\n" \
"buffer_load_dwordx4 v26, s[72:75], s84 offen lds\n" \
"v_mfma_f32_16x16x32_bf16 a[28:31], v[176:179], v[116:119], a[28:31]\n" \
"ds_read_b64_tr_b16 v[92:93], v34 offset:32768\n" \
"s_sub_i32 s46, s47, s46\n" \
"v_mfma_f32_16x16x32_bf16 a[60:63], v[176:179], v[120:123], a[60:63]\n" \
"ds_read_b64_tr_b16 v[94:95], v34 offset:33792\n" \
"s_add_i32 s53, s52, s45\n" \
"v_mfma_f32_16x16x32_bf16 a[92:95], v[176:179], v[124:127], a[92:95]\n" \
"s_add_u32 m0, s30, 0x800\n" \
"s_mov_b32 s85, 0x4200\n" \
"v_mfma_f32_16x16x32_bf16 a[124:127], v[176:179], v[128:131], a[124:127]\n" \
"buffer_load_dwordx4 v22, s[64:67], s85 offen lds\n" \
"v_mfma_f32_16x16x32_bf16 a[88:91], v[172:175], v[124:127], a[88:91]\n" \
"ds_read_b64_tr_b16 v[96:97], v35 offset:32768\n" \
"v_writelane_b32 v11, s53, 29\n" \
"v_mfma_f32_16x16x32_bf16 a[120:123], v[172:175], v[128:131], a[120:123]\n" \
"ds_read_b64_tr_b16 v[98:99], v35 offset:33792\n" \
"s_add_i32 s52, s52, s48\n" \
"s_nop 0\n" \
"v_mfma_f32_16x16x32_bf16 a[84:87], v[168:171], v[124:127], a[84:87]\n" \
"s_add_u32 m0, s30, 0xc00\n" \
"s_mov_b32 s84, 0x6200\n" \
"v_mfma_f32_16x16x32_bf16 a[116:119], v[168:171], v[128:131], a[116:119]\n" \
"buffer_load_dwordx4 v23, s[64:67], s84 offen lds\n" \
"v_mfma_f32_16x16x32_bf16 a[80:83], v[164:167], v[124:127], a[80:83]\n" \
"ds_read_b64_tr_b16 v[100:101], v36 offset:32768\n" \
"s_mul_i32 s46, s46, 0x24800\n" \
"v_mfma_f32_16x16x32_bf16 a[112:115], v[164:167], v[128:131], a[112:115]\n" \
"ds_read_b64_tr_b16 v[102:103], v36 offset:33792\n" \
"v_writelane_b32 v11, s52, 30\n" \
"v_mfma_f32_16x16x32_bf16 a[76:79], v[160:163], v[124:127], a[76:79]\n" \
"s_add_u32 m0, s31, 0x400\n" \
"s_mov_b32 s85, 0x208000\n" \
"v_mfma_f32_16x16x32_bf16 a[108:111], v[160:163], v[128:131], a[108:111]\n" \
"buffer_load_dwordx4 v26, s[68:71], s85 offen lds\n" \
"v_mfma_f32_16x16x32_bf16 a[72:75], v[156:159], v[124:127], a[72:75]\n" \
"ds_read_b64_tr_b16 v[104:105], v37 offset:32768\n" \
"s_add_i32 s52, s46, 0x4120800\n" \
"v_mfma_f32_16x16x32_bf16 a[104:107], v[156:159], v[128:131], a[104:107]\n" \
"s_add_i32 s49, s52, s49\n" \
"v_writelane_b32 v11, s49, 31\n" \
"v_mfma_f32_16x16x32_bf16 a[68:71], v[152:155], v[124:127], a[68:71]\n" \
"s_add_u32 m0, s31, 0x4400\n" \
"s_mov_b32 s84, 0x208000\n" \
"v_mfma_f32_16x16x32_bf16 a[100:103], v[152:155], v[128:131], a[100:103]\n" \
"buffer_load_dwordx4 v26, s[72:75], s84 offen lds\n" \
"v_mfma_f32_16x16x32_bf16 a[64:67], v[148:151], v[124:127], a[64:67]\n" \
"ds_read_b64_tr_b16 v[106:107], v37 offset:33792\n" \
"s_add_i32 s45, s52, s45\n" \
"v_mfma_f32_16x16x32_bf16 a[96:99], v[148:151], v[128:131], a[96:99]\n" \
"ds_read_b64_tr_b16 v[108:109], v38 offset:32768\n" \
"v_writelane_b32 v11, s45, 32\n" \
"v_mfma_f32_16x16x32_bf16 a[128:131], v[148:151], v[132:135], a[128:131]\n" \
"s_add_u32 m0, s30, 0x1000\n" \
"s_mov_b32 s85, 0x8200\n" \
"v_mfma_f32_16x16x32_bf16 a[160:163], v[148:151], v[136:139], a[160:163]\n" \
"buffer_load_dwordx4 v22, s[64:67], s85 offen lds\n" \
"v_mfma_f32_16x16x32_bf16 a[132:135], v[152:155], v[132:135], a[132:135]\n" \
"ds_read_b64_tr_b16 v[110:111], v38 offset:33792\n" \
"s_add_i32 s52, s52, s48\n" \
"v_mfma_f32_16x16x32_bf16 a[164:167], v[152:155], v[136:139], a[164:167]\n" \
"ds_read_b64_tr_b16 v[112:113], v39 offset:32768\n" \
"s_or_b32 s45, s51, 0x3000000\n" \
"v_mfma_f32_16x16x32_bf16 a[136:139], v[156:159], v[132:135], a[136:139]\n" \
"s_add_u32 m0, s30, 0x1400\n" \
"s_mov_b32 s84, 0xa200\n" \
"v_mfma_f32_16x16x32_bf16 a[168:171], v[156:159], v[136:139], a[168:171]\n" \
"buffer_load_dwordx4 v23, s[64:67], s84 offen lds\n" \
"v_mfma_f32_16x16x32_bf16 a[140:143], v[160:163], v[132:135], a[140:143]\n" \
"ds_read_b64_tr_b16 v[114:115], v39 offset:33792\n" \
"s_lshl_b32 s48, s41, 11\n" \
"v_mfma_f32_16x16x32_bf16 a[172:175], v[160:163], v[136:139], a[172:175]\n" \
"ds_read_b128 v[60:63], v28 offset:53248\n" \
"v_writelane_b32 v11, s52, 33\n" \
"v_mfma_f32_16x16x32_bf16 a[144:147], v[164:167], v[132:135], a[144:147]\n" \
"s_add_u32 m0, s31, 0x800\n" \
"s_mov_b32 s85, 0x210000\n" \
"v_mfma_f32_16x16x32_bf16 a[176:179], v[164:167], v[136:139], a[176:179]\n" \
"buffer_load_dwordx4 v27, s[68:71], s85 offen lds\n" \
"v_mfma_f32_16x16x32_bf16 a[148:151], v[168:171], v[132:135], a[148:151]\n" \
"ds_read_b128 v[64:67], v28 offset:55296\n" \
"s_add_i32 s49, s45, s48\n" \
"s_nop 0\n" \
"v_mfma_f32_16x16x32_bf16 a[180:183], v[168:171], v[136:139], a[180:183]\n" \
"ds_read_b128 v[68:71], v28 offset:32768\n" \
"v_writelane_b32 v11, s49, 34\n" \
"v_mfma_f32_16x16x32_bf16 a[152:155], v[172:175], v[132:135], a[152:155]\n" \
"s_add_u32 m0, s31, 0x4800\n" \
"s_mov_b32 s84, 0x210000\n" \
"v_mfma_f32_16x16x32_bf16 a[184:187], v[172:175], v[136:139], a[184:187]\n" \
"buffer_load_dwordx4 v27, s[72:75], s84 offen lds\n" \
"v_mfma_f32_16x16x32_bf16 a[156:159], v[176:179], v[132:135], a[156:159]\n" \
"ds_read_b128 v[72:75], v28 offset:34816\n" \
"s_add_i32 s49, s46, 0x411c000\n" \
"v_mfma_f32_16x16x32_bf16 a[188:191], v[176:179], v[136:139], a[188:191]\n" \
"ds_read_b128 v[76:79], v28 offset:36864\n" \
"s_add_i32 s52, s49, s48\n" \
"v_mfma_f32_16x16x32_bf16 a[220:223], v[176:179], v[140:143], a[220:223]\n" \
"v_writelane_b32 v11, s52, 35\n" \
"s_add_i32 s51, s51, 0x3004000\n" \
"v_mfma_f32_16x16x32_bf16 a[252:255], v[176:179], v[144:147], a[252:255]\n" \
"ds_read_b128 v[80:83], v28 offset:38912\n" \
"v_writelane_b32 v11, s51, 36\n" \
"v_mfma_f32_16x16x32_bf16 a[216:219], v[172:175], v[140:143], a[216:219]\n" \
"s_add_i32 s46, s46, 0x4120000\n" \
"v_writelane_b32 v11, s46, 37\n" \
"v_mfma_f32_16x16x32_bf16 a[248:251], v[172:175], v[144:147], a[248:251]\n" \
"s_lshl_b32 s46, s5, 2\n" \
"s_or_b32 s43, s46, s43\n" \
"v_mfma_f32_16x16x32_bf16 a[212:215], v[168:171], v[140:143], a[212:215]\n" \
"s_mul_i32 s43, s43, 0x90000\n" \
"s_lshl_b32 s46, s3, 14\n" \
"v_mfma_f32_16x16x32_bf16 a[244:247], v[168:171], v[144:147], a[244:247]\n" \
"s_add_i32 s43, s43, s46\n" \
"s_lshl_b32 s46, s47, 4\n" \
"v_mfma_f32_16x16x32_bf16 a[208:211], v[164:167], v[140:143], a[208:211]\n" \
"s_lshl_b32 s47, s5, 7\n" \
"s_or_b32 s51, s46, s47\n" \
"v_mfma_f32_16x16x32_bf16 a[240:243], v[164:167], v[144:147], a[240:243]\n" \
"s_sub_i32 s46, s46, s47\n" \
"s_mulk_i32 s51, 0x480\n" \
"v_mfma_f32_16x16x32_bf16 a[204:207], v[160:163], v[140:143], a[204:207]\n" \
"s_mulk_i32 s46, 0x480\n" \
"s_or_b32 s47, s51, s50\n" \
"v_mfma_f32_16x16x32_bf16 a[236:239], v[160:163], v[144:147], a[236:239]\n" \
"s_or_b32 s46, s46, s50\n" \
"s_and_b32 s50, s2, 1\n" \
"v_mfma_f32_16x16x32_bf16 a[200:203], v[156:159], v[140:143], a[200:203]\n" \
"s_cmp_lg_u32 s50, 0\n" \
"s_cselect_b32 s45, s49, s45\n" \
"v_mfma_f32_16x16x32_bf16 a[232:235], v[156:159], v[144:147], a[232:235]\n" \
"s_bfe_u32 s49, s2, 0x20001\n" \
"s_addk_i32 s45, 0x4000\n" \
"v_mfma_f32_16x16x32_bf16 a[196:199], v[152:155], v[140:143], a[196:199]\n" \
"s_xor_b32 s49, s49, s3\n" \
"v_mfma_f32_16x16x32_bf16 a[228:231], v[152:155], v[144:147], a[228:231]\n" \
"s_cmp_lg_u32 s49, 0\n" \
"s_cselect_b32 s45, s42, s45\n" \
"v_mfma_f32_16x16x32_bf16 a[192:195], v[148:151], v[140:143], a[192:195]\n" \
"s_lshl_b32 s41, s41, 16\n" \
"v_writelane_b32 v11, s45, 38\n" \
"v_mfma_f32_16x16x32_bf16 a[224:227], v[148:151], v[144:147], a[224:227]\n" \
"s_waitcnt lgkmcnt(15)\n" \
"v_mfma_f32_16x16x32_bf16 a[0:3], v[84:87], v[52:55], a[0:3]\n" \
"s_add_u32 m0, s30, 0x1800\n" \
"s_mov_b32 s85, 0xc200\n" \
"v_mfma_f32_16x16x32_bf16 a[32:35], v[84:87], v[56:59], a[32:35]\n" \
"buffer_load_dwordx4 v22, s[64:67], s85 offen lds\n" \
"v_mfma_f32_16x16x32_bf16 a[4:7], v[88:91], v[52:55], a[4:7]\n" \
"ds_read_b64_tr_b16 v[148:149], v32 offset:40960\n" \
"s_add_i32 s45, s43, 0x4240000\n" \
"v_mfma_f32_16x16x32_bf16 a[36:39], v[88:91], v[56:59], a[36:39]\n" \
"s_add_u32 m0, s30, 0x1c00\n" \
"s_mov_b32 s84, 0xe200\n" \
"v_mfma_f32_16x16x32_bf16 a[8:11], v[92:95], v[52:55], a[8:11]\n" \
"buffer_load_dwordx4 v23, s[64:67], s84 offen lds\n" \
"v_mfma_f32_16x16x32_bf16 a[40:43], v[92:95], v[56:59], a[40:43]\n" \
"ds_read_b64_tr_b16 v[150:151], v32 offset:41984\n" \
"s_add_i32 s41, s45, s41\n" \
"s_waitcnt lgkmcnt(15)\n" \
"v_mfma_f32_16x16x32_bf16 a[12:15], v[96:99], v[52:55], a[12:15]\n" \
"s_add_u32 m0, s31, 0xc00\n" \
"s_mov_b32 s85, 0x218000\n" \
"v_mfma_f32_16x16x32_bf16 a[44:47], v[96:99], v[56:59], a[44:47]\n" \
"buffer_load_dwordx4 v27, s[68:71], s85 offen lds\n" \
"s_waitcnt lgkmcnt(14)\n" \
"v_mfma_f32_16x16x32_bf16 a[16:19], v[100:103], v[52:55], a[16:19]\n" \
"ds_read_b128 v[116:119], v29 offset:49152\n" \
"v_writelane_b32 v11, s41, 39\n" \
"v_mfma_f32_16x16x32_bf16 a[48:51], v[100:103], v[56:59], a[48:51]\n" \
"s_add_u32 m0, s31, 0x4c00\n" \
"s_mov_b32 s84, 0x218000\n" \
"s_waitcnt lgkmcnt(13)\n" \
"v_mfma_f32_16x16x32_bf16 a[20:23], v[104:107], v[52:55], a[20:23]\n" \
"buffer_load_dwordx4 v27, s[72:75], s84 offen lds\n" \
"v_mfma_f32_16x16x32_bf16 a[52:55], v[104:107], v[56:59], a[52:55]\n" \
"ds_read_b128 v[120:123], v29 offset:51200\n" \
"s_add_i32 s43, s43, 0x42c0000\n" \
"s_waitcnt lgkmcnt(12)\n" \
"v_mfma_f32_16x16x32_bf16 a[24:27], v[108:111], v[52:55], a[24:27]\n" \
"ds_read_b64_tr_b16 v[152:153], v33 offset:40960\n" \
"s_lshl_b32 s41, s50, 10\n" \
"v_mfma_f32_16x16x32_bf16 a[56:59], v[108:111], v[56:59], a[56:59]\n" \
"ds_read_b64_tr_b16 v[154:155], v33 offset:41984\n" \
"s_or_b32 s41, s43, s41\n" \
"s_waitcnt lgkmcnt(12)\n" \
"v_mfma_f32_16x16x32_bf16 a[28:31], v[112:115], v[52:55], a[28:31]\n" \
"ds_read_b64_tr_b16 v[156:157], v34 offset:40960\n" \
"v_mfma_f32_16x16x32_bf16 a[60:63], v[112:115], v[56:59], a[60:63]\n" \
"ds_read_b64_tr_b16 v[158:159], v34 offset:41984\n" \
"s_waitcnt lgkmcnt(13)\n" \
"v_mfma_f32_16x16x32_bf16 a[92:95], v[112:115], v[60:63], a[92:95]\n" \
"s_cmp_lg_u32 s49, 0\n" \
"s_cselect_b32 s41, s42, s41\n" \
"s_waitcnt lgkmcnt(12)\n" \
"v_mfma_f32_16x16x32_bf16 a[124:127], v[112:115], v[64:67], a[124:127]\n" \
"ds_read_b64_tr_b16 v[160:161], v35 offset:40960\n" \
"s_mulk_i32 s5, 0x4800\n" \
"v_mfma_f32_16x16x32_bf16 a[88:91], v[108:111], v[60:63], a[88:91]\n" \
"v_writelane_b32 v11, s41, 40\n" \
"s_add_i32 s41, s47, s40\n" \
"v_mfma_f32_16x16x32_bf16 a[120:123], v[108:111], v[64:67], a[120:123]\n" \
"ds_read_b64_tr_b16 v[162:163], v35 offset:41984\n" \
"v_writelane_b32 v11, s41, 41\n" \
"v_mfma_f32_16x16x32_bf16 a[84:87], v[104:107], v[60:63], a[84:87]\n" \
"s_mov_b32 m0, s32\n" \
"s_add_i32 s41, s46, 0x21c000\n" \
"v_mfma_f32_16x16x32_bf16 a[116:119], v[104:107], v[64:67], a[116:119]\n" \
"s_mov_b32 exec_lo, 0xffffffff\n" \
"s_mov_b32 exec_hi, 0x0\n" \
"global_load_lds_dword v[188:189], off\n" \
"s_mov_b64 exec, -1\n" \
"v_mfma_f32_16x16x32_bf16 a[80:83], v[100:103], v[60:63], a[80:83]\n" \
"ds_read_b64_tr_b16 v[164:165], v36 offset:40960\n" \
"v_mfma_f32_16x16x32_bf16 a[112:115], v[100:103], v[64:67], a[112:115]\n" \
"ds_read_b64_tr_b16 v[166:167], v36 offset:41984\n" \
"v_mfma_f32_16x16x32_bf16 a[76:79], v[96:99], v[60:63], a[76:79]\n" \
"s_waitcnt vmcnt(17)\n" \
"v_or_b32_e32 v204, v210, v204\n" \
"v_or_b32_e64 v206, v210, v206\n" \
"v_mfma_f32_16x16x32_bf16 a[108:111], v[96:99], v[64:67], a[108:111]\n" \
"v_mov_b32_e64 v210, 0\n" \
"s_add_u32 s88, s35, 0x1000\n" \
"v_mfma_f32_16x16x32_bf16 a[72:75], v[92:95], v[60:63], a[72:75]\n" \
"ds_read_b64_tr_b16 v[168:169], v37 offset:40960\n" \
"v_mfma_f32_16x16x32_bf16 a[104:107], v[92:95], v[64:67], a[104:107]\n" \
"s_cmp_lg_u32 s50, 0\n" \
"s_cselect_b32 s46, s41, s47\n" \
"v_mfma_f32_16x16x32_bf16 a[68:71], v[88:91], v[60:63], a[68:71]\n" \
"ds_read_b64_tr_b16 v[170:171], v37 offset:41984\n" \
"s_add_i32 s41, s41, s40\n" \
"v_mfma_f32_16x16x32_bf16 a[100:103], v[88:91], v[64:67], a[100:103]\n" \
"v_writelane_b32 v11, s41, 42\n" \
"s_addk_i32 s46, 0x100\n" \
"v_mfma_f32_16x16x32_bf16 a[64:67], v[84:87], v[60:63], a[64:67]\n" \
"ds_write_b128 v196, v[200:203]\n" \
"v_mfma_f32_16x16x32_bf16 a[96:99], v[84:87], v[64:67], a[96:99]\n" \
"ds_read_b64_tr_b16 v[172:173], v38 offset:40960\n" \
"s_waitcnt lgkmcnt(15)\n" \
"v_mfma_f32_16x16x32_bf16 a[128:131], v[84:87], v[68:71], a[128:131]\n" \
"ds_read_b64_tr_b16 v[174:175], v38 offset:41984\n" \
"v_mfma_f32_16x16x32_bf16 a[160:163], v[84:87], v[72:75], a[160:163]\n" \
"s_cmp_lg_u32 s49, 0\n" \
"s_cselect_b32 s40, s42, s46\n" \
"v_mfma_f32_16x16x32_bf16 a[132:135], v[88:91], v[68:71], a[132:135]\n" \
"ds_read_b64_tr_b16 v[176:177], v39 offset:40960\n" \
"s_lshl_b32 s4, s4, 6\n" \
"v_mfma_f32_16x16x32_bf16 a[164:167], v[88:91], v[72:75], a[164:167]\n" \
"v_writelane_b32 v11, s40, 43\n" \
"s_or_b32 s40, s43, s48\n" \
"v_mfma_f32_16x16x32_bf16 a[136:139], v[92:95], v[68:71], a[136:139]\n" \
"ds_write_b128 v197, v[204:207]\n" \
"v_mfma_f32_16x16x32_bf16 a[168:171], v[92:95], v[72:75], a[168:171]\n" \
"ds_read_b64_tr_b16 v[178:179], v39 offset:41984\n" \
"v_writelane_b32 v11, s40, 51\n" \
"v_mfma_f32_16x16x32_bf16 a[140:143], v[96:99], v[68:71], a[140:143]\n" \
"s_lshl_b32 s40, s3, 17\n" \
"s_add_i32 s40, s45, s40\n" \
"v_mfma_f32_16x16x32_bf16 a[172:175], v[96:99], v[72:75], a[172:175]\n" \
"ds_read_b128 v[124:127], v29 offset:53248\n" \
"s_or_b32 s7, s40, s7\n" \
"v_mfma_f32_16x16x32_bf16 a[144:147], v[100:103], v[68:71], a[144:147]\n" \
"s_or_b32 s7, s7, 0x3840\n" \
"v_writelane_b32 v11, s7, 6\n" \
"v_mfma_f32_16x16x32_bf16 a[176:179], v[100:103], v[72:75], a[176:179]\n" \
"ds_read_b128 v[128:131], v29 offset:55296\n" \
"s_mov_b32 s7, 0xff0000\n" \
"v_mfma_f32_16x16x32_bf16 a[148:151], v[104:107], v[68:71], a[148:151]\n" \
"ds_write_b128 v198, v[208:211]\n" \
"v_mfma_f32_16x16x32_bf16 a[180:183], v[104:107], v[72:75], a[180:183]\n" \
"ds_read_b128 v[132:135], v29 offset:32768\n" \
"v_mfma_f32_16x16x32_bf16 a[152:155], v[108:111], v[68:71], a[152:155]\n" \
"s_cmp_lg_u32 s49, 0\n" \
"s_cselect_b32 s6, s6, s7\n" \
"v_mfma_f32_16x16x32_bf16 a[184:187], v[108:111], v[72:75], a[184:187]\n" \
"ds_read_b128 v[136:139], v29 offset:34816\n" \
"v_lshrrev_b32_e64 v213, 2, v20\n" \
"v_mfma_f32_16x16x32_bf16 a[156:159], v[112:115], v[68:71], a[156:159]\n" \
"v_writelane_b32 v11, s6, 7\n" \
"s_mul_i32 s6, s3, 0x1200\n" \
"v_mfma_f32_16x16x32_bf16 a[188:191], v[112:115], v[72:75], a[188:191]\n" \
"ds_read_b128 v[140:143], v29 offset:36864\n" \
"s_add_i32 s5, s6, s5\n" \
"v_mfma_f32_16x16x32_bf16 a[220:223], v[112:115], v[76:79], a[220:223]\n" \
"s_add_i32 s5, s5, s44\n" \
"v_writelane_b32 v11, s5, 8\n" \
"v_mfma_f32_16x16x32_bf16 a[252:255], v[112:115], v[80:83], a[252:255]\n" \
"ds_read_b128 v[144:147], v29 offset:38912\n" \
"s_lshl_b32 s5, s2, 16\n" \
"v_mfma_f32_16x16x32_bf16 a[216:219], v[108:111], v[76:79], a[216:219]\n" \
"s_add_i32 s6, s5, 0x10000\n" \
"s_and_b32 s6, s6, 0x70000\n" \
"v_mfma_f32_16x16x32_bf16 a[248:251], v[108:111], v[80:83], a[248:251]\n" \
"s_add_i32 s6, s45, s6\n" \
"v_writelane_b32 v11, s6, 44\n" \
"v_mfma_f32_16x16x32_bf16 a[212:215], v[104:107], v[76:79], a[212:215]\n" \
"s_add_i32 s6, s5, 0x20000\n" \
"s_and_b32 s6, s6, 0x70000\n" \
"v_mfma_f32_16x16x32_bf16 a[244:247], v[104:107], v[80:83], a[244:247]\n" \
"s_add_i32 s6, s45, s6\n" \
"s_bitset1_b32 s6, 11\n" \
"v_mfma_f32_16x16x32_bf16 a[208:211], v[100:103], v[76:79], a[208:211]\n" \
"v_writelane_b32 v11, s6, 45\n" \
"s_add_i32 s6, s5, 0x30000\n" \
"v_mfma_f32_16x16x32_bf16 a[240:243], v[100:103], v[80:83], a[240:243]\n" \
"s_and_b32 s6, s6, 0x70000\n" \
"s_add_i32 s6, s45, s6\n" \
"v_mfma_f32_16x16x32_bf16 a[204:207], v[96:99], v[76:79], a[204:207]\n" \
"s_bitset1_b32 s6, 12\n" \
"v_writelane_b32 v11, s6, 46\n" \
"v_mfma_f32_16x16x32_bf16 a[236:239], v[96:99], v[80:83], a[236:239]\n" \
"s_and_b32 s6, s5, 0x70000\n" \
"s_xor_b32 s6, s6, 0x40000\n" \
"v_mfma_f32_16x16x32_bf16 a[200:203], v[92:95], v[76:79], a[200:203]\n" \
"s_add_i32 s6, s45, s6\n" \
"s_or_b32 s6, s6, 0x1800\n" \
"v_mfma_f32_16x16x32_bf16 a[232:235], v[92:95], v[80:83], a[232:235]\n" \
"v_writelane_b32 v11, s6, 47\n" \
"s_add_i32 s6, s5, 0x50000\n" \
"v_mfma_f32_16x16x32_bf16 a[196:199], v[88:91], v[76:79], a[196:199]\n" \
"s_and_b32 s6, s6, 0x70000\n" \
"s_add_i32 s6, s45, s6\n" \
"v_mfma_f32_16x16x32_bf16 a[228:231], v[88:91], v[80:83], a[228:231]\n" \
"s_bitset1_b32 s6, 13\n" \
"v_writelane_b32 v11, s6, 48\n" \
"v_mfma_f32_16x16x32_bf16 a[192:195], v[84:87], v[76:79], a[192:195]\n" \
"s_add_i32 s6, s5, 0x60000\n" \
"s_and_b32 s6, s6, 0x70000\n" \
"v_mfma_f32_16x16x32_bf16 a[224:227], v[84:87], v[80:83], a[224:227]\n" \
"s_waitcnt vmcnt(1) lgkmcnt(0)\n" \
"s_barrier\n" \
"s_nop 0\n" \
"v_mfma_f32_16x16x32_bf16 a[0:3], v[148:151], v[116:119], a[0:3]\n" \
"s_add_u32 m0, s30, 0x8000\n" \
"s_mov_b32 s85, 0x280\n" \
"v_mfma_f32_16x16x32_bf16 a[32:35], v[148:151], v[120:123], a[32:35]\n" \
"buffer_load_dwordx4 v22, s[64:67], s85 offen lds\n" \
"v_mfma_f32_16x16x32_bf16 a[4:7], v[152:155], v[116:119], a[4:7]\n" \
"ds_read_b64_tr_b16 v[84:85], v32 offset:0\n" \
"s_add_i32 s5, s5, 0x70000\n" \
"v_mfma_f32_16x16x32_bf16 a[36:39], v[152:155], v[120:123], a[36:39]\n" \
"s_add_u32 m0, s30, 0x8400\n" \
"s_mov_b32 s84, 0x2280\n" \
"v_mfma_f32_16x16x32_bf16 a[8:11], v[156:159], v[116:119], a[8:11]\n" \
"buffer_load_dwordx4 v23, s[64:67], s84 offen lds\n" \
"v_mfma_f32_16x16x32_bf16 a[40:43], v[156:159], v[120:123], a[40:43]\n" \
"ds_read_b64_tr_b16 v[86:87], v32 offset:1024\n" \
"s_add_i32 s6, s45, s6\n" \
"s_nop 0\n" \
"v_mfma_f32_16x16x32_bf16 a[12:15], v[160:163], v[116:119], a[12:15]\n" \
"s_add_u32 m0, s31, 0x8000\n" \
"s_mov_b32 s85, 0x280000\n" \
"v_mfma_f32_16x16x32_bf16 a[44:47], v[160:163], v[120:123], a[44:47]\n" \
"buffer_load_dwordx4 v26, s[68:71], s85 offen lds\n" \
"v_mfma_f32_16x16x32_bf16 a[16:19], v[164:167], v[116:119], a[16:19]\n" \
"ds_read_b128 v[52:55], v28 offset:16384\n" \
"s_and_b32 s5, s5, 0x70000\n" \
"v_mfma_f32_16x16x32_bf16 a[48:51], v[164:167], v[120:123], a[48:51]\n" \
"s_add_u32 m0, s31, 0xc000\n" \
"s_mov_b32 s84, 0x280000\n" \
"v_mfma_f32_16x16x32_bf16 a[20:23], v[168:171], v[116:119], a[20:23]\n" \
"buffer_load_dwordx4 v26, s[72:75], s84 offen lds\n" \
"v_mfma_f32_16x16x32_bf16 a[52:55], v[168:171], v[120:123], a[52:55]\n" \
"ds_read_b128 v[56:59], v28 offset:18432\n" \
"v_and_or_b32 v214, v20, 16, v222\n" \
"v_mfma_f32_16x16x32_bf16 a[24:27], v[172:175], v[116:119], a[24:27]\n" \
"s_add_u32 m0, s30, 0x8800\n" \
"s_mov_b32 s85, 0x4280\n" \
"v_mfma_f32_16x16x32_bf16 a[56:59], v[172:175], v[120:123], a[56:59]\n" \
"buffer_load_dwordx4 v22, s[64:67], s85 offen lds\n" \
"v_mfma_f32_16x16x32_bf16 a[28:31], v[176:179], v[116:119], a[28:31]\n" \
"ds_read_b64_tr_b16 v[88:89], v33 offset:0\n" \
"s_or_b32 s6, s6, 0x2800\n" \
"v_mfma_f32_16x16x32_bf16 a[60:63], v[176:179], v[120:123], a[60:63]\n" \
"ds_read_b64_tr_b16 v[90:91], v33 offset:1024\n" \
"v_writelane_b32 v11, s6, 49\n" \
"v_mfma_f32_16x16x32_bf16 a[92:95], v[176:179], v[124:127], a[92:95]\n" \
"s_add_u32 m0, s30, 0x8c00\n" \
"s_mov_b32 s84, 0x6280\n" \
"v_mfma_f32_16x16x32_bf16 a[124:127], v[176:179], v[128:131], a[124:127]\n" \
"buffer_load_dwordx4 v23, s[64:67], s84 offen lds\n" \
"v_mfma_f32_16x16x32_bf16 a[88:91], v[172:175], v[124:127], a[88:91]\n" \
"ds_read_b64_tr_b16 v[92:93], v34 offset:0\n" \
"s_add_i32 s45, s45, s5\n" \
"s_nop 0\n" \
"v_mfma_f32_16x16x32_bf16 a[120:123], v[172:175], v[128:131], a[120:123]\n" \
"s_add_u32 m0, s31, 0x8400\n" \
"s_mov_b32 s85, 0x288000\n" \
"v_mfma_f32_16x16x32_bf16 a[84:87], v[168:171], v[124:127], a[84:87]\n" \
"buffer_load_dwordx4 v26, s[68:71], s85 offen lds\n" \
"v_mfma_f32_16x16x32_bf16 a[116:119], v[168:171], v[128:131], a[116:119]\n" \
"ds_read_b64_tr_b16 v[94:95], v34 offset:1024\n" \
"v_and_or_b32 v213, v213, 60, s4\n" \
"v_mfma_f32_16x16x32_bf16 a[80:83], v[164:167], v[124:127], a[80:83]\n" \
"s_add_u32 m0, s31, 0xc400\n" \
"s_mov_b32 s84, 0x288000\n" \
"v_mfma_f32_16x16x32_bf16 a[112:115], v[164:167], v[128:131], a[112:115]\n" \
"buffer_load_dwordx4 v26, s[72:75], s84 offen lds\n" \
"v_mfma_f32_16x16x32_bf16 a[76:79], v[160:163], v[124:127], a[76:79]\n" \
"ds_read_b64_tr_b16 v[96:97], v35 offset:0\n" \
"v_lshl_or_b32 v212, s3, 9, v212\n" \
"v_mfma_f32_16x16x32_bf16 a[108:111], v[160:163], v[128:131], a[108:111]\n" \
"s_add_u32 m0, s30, 0x9000\n" \
"s_mov_b32 s85, 0x8280\n" \
"v_mfma_f32_16x16x32_bf16 a[72:75], v[156:159], v[124:127], a[72:75]\n" \
"buffer_load_dwordx4 v22, s[64:67], s85 offen lds\n" \
"v_mfma_f32_16x16x32_bf16 a[104:107], v[156:159], v[128:131], a[104:107]\n" \
"ds_read_b64_tr_b16 v[98:99], v35 offset:1024\n" \
"v_add_u32_e32 v9, 0x4800, v214\n" \
"v_mfma_f32_16x16x32_bf16 a[68:71], v[152:155], v[124:127], a[68:71]\n" \
"s_add_u32 m0, s30, 0x9400\n" \
"s_mov_b32 s84, 0xa280\n" \
"v_mfma_f32_16x16x32_bf16 a[100:103], v[152:155], v[128:131], a[100:103]\n" \
"buffer_load_dwordx4 v23, s[64:67], s84 offen lds\n" \
"v_mfma_f32_16x16x32_bf16 a[64:67], v[148:151], v[124:127], a[64:67]\n" \
"ds_read_b64_tr_b16 v[100:101], v36 offset:0\n" \
"s_or_b32 s5, s45, 0x3000\n" \
"v_mfma_f32_16x16x32_bf16 a[96:99], v[148:151], v[128:131], a[96:99]\n" \
"ds_read_b64_tr_b16 v[102:103], v36 offset:1024\n" \
"v_writelane_b32 v11, s5, 50\n" \
"v_mfma_f32_16x16x32_bf16 a[128:131], v[148:151], v[132:135], a[128:131]\n" \
"s_add_u32 m0, s31, 0x8800\n" \
"s_mov_b32 s85, 0x290000\n" \
"v_mfma_f32_16x16x32_bf16 a[160:163], v[148:151], v[136:139], a[160:163]\n" \
"buffer_load_dwordx4 v27, s[68:71], s85 offen lds\n" \
"v_mfma_f32_16x16x32_bf16 a[132:135], v[152:155], v[132:135], a[132:135]\n" \
"ds_read_b64_tr_b16 v[104:105], v37 offset:0\n" \
"v_or_b32_e32 v18, 0x27000, v213\n" \
"v_mfma_f32_16x16x32_bf16 a[164:167], v[152:155], v[136:139], a[164:167]\n" \
"s_add_u32 m0, s31, 0xc800\n" \
"s_mov_b32 s84, 0x290000\n" \
"v_mfma_f32_16x16x32_bf16 a[136:139], v[156:159], v[132:135], a[136:139]\n" \
"buffer_load_dwordx4 v27, s[72:75], s84 offen lds\n" \
"v_mfma_f32_16x16x32_bf16 a[168:171], v[156:159], v[136:139], a[168:171]\n" \
"ds_read_b64_tr_b16 v[106:107], v37 offset:1024\n" \
"v_or_b32_e32 v19, 0x27800, v212\n" \
"v_mfma_f32_16x16x32_bf16 a[140:143], v[160:163], v[132:135], a[140:143]\n" \
"ds_read_b128 v[200:203], v199 offset:0\n" \
"v_mov_b32_e32 v220, 0x1000c0c\n" \
"v_mfma_f32_16x16x32_bf16 a[172:175], v[160:163], v[136:139], a[172:175]\n" \
"ds_read_b64_tr_b16 v[108:109], v38 offset:0\n" \
"s_lshr_b32 s4, s2, 5\n" \
"v_mfma_f32_16x16x32_bf16 a[144:147], v[164:167], v[132:135], a[144:147]\n" \
"ds_read_b64_tr_b16 v[110:111], v38 offset:1024\n" \
"s_and_b32 s5, s3, 1\n" \
"v_mfma_f32_16x16x32_bf16 a[176:179], v[164:167], v[136:139], a[176:179]\n" \
"ds_read_b128 v[204:207], v199 offset:4096\n" \
"s_xor_b32 s6, s4, 15\n" \
"s_nop 0\n" \
"v_mfma_f32_16x16x32_bf16 a[148:151], v[168:171], v[132:135], a[148:151]\n" \
"ds_read_b64_tr_b16 v[112:113], v39 offset:0\n" \
"v_mfma_f32_16x16x32_bf16 a[180:183], v[168:171], v[136:139], a[180:183]\n" \
"ds_read_b64_tr_b16 v[114:115], v39 offset:1024\n" \
"v_mfma_f32_16x16x32_bf16 a[152:155], v[172:175], v[132:135], a[152:155]\n" \
"ds_read_b128 v[208:211], v199 offset:8192\n" \
"v_mfma_f32_16x16x32_bf16 a[184:187], v[172:175], v[136:139], a[184:187]\n" \
"ds_read_b128 v[60:63], v28 offset:20480\n" \
"v_mfma_f32_16x16x32_bf16 a[156:159], v[176:179], v[132:135], a[156:159]\n" \
"ds_read_b128 v[64:67], v28 offset:22528\n" \
"v_mfma_f32_16x16x32_bf16 a[188:191], v[176:179], v[136:139], a[188:191]\n" \
"ds_read_b128 v[68:71], v28 offset:0\n" \
"v_mfma_f32_16x16x32_bf16 a[220:223], v[176:179], v[140:143], a[220:223]\n" \
"ds_read_b128 v[72:75], v28 offset:2048\n" \
"v_mfma_f32_16x16x32_bf16 a[252:255], v[176:179], v[144:147], a[252:255]\n" \
"ds_read_b128 v[76:79], v28 offset:4096\n" \
"v_mfma_f32_16x16x32_bf16 a[216:219], v[172:175], v[140:143], a[216:219]\n" \
"ds_read_b128 v[80:83], v28 offset:6144\n" \
"v_mfma_f32_16x16x32_bf16 a[248:251], v[172:175], v[144:147], a[248:251]\n" \
"s_cmpk_lt_u32 s3, 2\n" \
"s_cselect_b32 s7, s6, s4\n" \
"v_mfma_f32_16x16x32_bf16 a[212:215], v[168:171], v[140:143], a[212:215]\n" \
"s_lshl_b32 s40, s7, 7\n" \
"s_lshl_b32 s41, s5, 6\n" \
"v_mfma_f32_16x16x32_bf16 a[244:247], v[168:171], v[144:147], a[244:247]\n" \
"v_bfe_u32 v219, v20, 4, 1\n" \
"v_lshrrev_b32_e64 v212, 7, v20\n" \
"v_mfma_f32_16x16x32_bf16 a[208:211], v[164:167], v[140:143], a[208:211]\n" \
"v_lshrrev_b32_e32 v213, 8, v20\n" \
"s_or_b32 s41, s41, s40\n" \
"v_mfma_f32_16x16x32_bf16 a[240:243], v[164:167], v[144:147], a[240:243]\n" \
"v_mov_b32_e32 v221, 0xc0c0100\n" \
"v_cmp_eq_u32_e64 vcc, 0, v219\n" \
"v_mfma_f32_16x16x32_bf16 a[204:207], v[160:163], v[140:143], a[204:207]\n" \
"v_or_b32_e32 v214, s41, v212\n" \
"s_lshl_b32 s41, s2, 2\n" \
"v_mfma_f32_16x16x32_bf16 a[236:239], v[160:163], v[144:147], a[236:239]\n" \
"v_bfe_u32 v217, v20, 4, 4\n" \
"v_cndmask_b32_e64 v220, v220, v221, vcc\n" \
"v_mfma_f32_16x16x32_bf16 a[200:203], v[156:159], v[140:143], a[200:203]\n" \
"v_mov_b32_e32 v221, 0x3020c0c\n" \
"v_mov_b32_e32 v222, 0xc0c0302\n" \
"v_mfma_f32_16x16x32_bf16 a[232:235], v[156:159], v[144:147], a[232:235]\n" \
"v_lshl_or_b32 v219, v213, 2, v219\n" \
"s_and_b32 s43, s41, 28\n" \
"v_mfma_f32_16x16x32_bf16 a[196:199], v[152:155], v[140:143], a[196:199]\n" \
"s_add_i32 s41, s40, -16\n" \
"v_cndmask_b32_e64 v221, v221, v222, vcc\n" \
"v_mfma_f32_16x16x32_bf16 a[228:231], v[152:155], v[144:147], a[228:231]\n" \
"v_mov_b32_e32 v222, 0xc0c0c0c\n" \
"v_cmp_gt_u32_e64 vcc, v219, v217\n" \
"v_mfma_f32_16x16x32_bf16 a[192:195], v[148:151], v[140:143], a[192:195]\n" \
"v_or_b32_e32 v219, 2, v219\n" \
"s_mov_b32 s42, 0\n" \
"v_mfma_f32_16x16x32_bf16 a[224:227], v[148:151], v[144:147], a[224:227]\n" \
"s_waitcnt lgkmcnt(15)\n" \
"s_nop 0\n" \
"v_mfma_f32_16x16x32_bf16 a[0:3], v[84:87], v[52:55], a[0:3]\n" \
"s_add_u32 m0, s30, 0x9800\n" \
"s_mov_b32 s85, 0xc280\n" \
"v_mfma_f32_16x16x32_bf16 a[32:35], v[84:87], v[56:59], a[32:35]\n" \
"buffer_load_dwordx4 v22, s[64:67], s85 offen lds\n" \
"v_mfma_f32_16x16x32_bf16 a[4:7], v[88:91], v[52:55], a[4:7]\n" \
"ds_read_b64_tr_b16 v[148:149], v32 offset:8192\n" \
"v_mfma_f32_16x16x32_bf16 a[36:39], v[88:91], v[56:59], a[36:39]\n" \
"s_add_u32 m0, s30, 0x9c00\n" \
"s_mov_b32 s84, 0xe280\n" \
"v_mfma_f32_16x16x32_bf16 a[8:11], v[92:95], v[52:55], a[8:11]\n" \
"buffer_load_dwordx4 v23, s[64:67], s84 offen lds\n" \
"v_mfma_f32_16x16x32_bf16 a[40:43], v[92:95], v[56:59], a[40:43]\n" \
"ds_read_b64_tr_b16 v[150:151], v32 offset:9216\n" \
"v_mfma_f32_16x16x32_bf16 a[12:15], v[96:99], v[52:55], a[12:15]\n" \
"s_add_u32 m0, s31, 0x8c00\n" \
"s_mov_b32 s85, 0x298000\n" \
"v_mfma_f32_16x16x32_bf16 a[44:47], v[96:99], v[56:59], a[44:47]\n" \
"buffer_load_dwordx4 v27, s[68:71], s85 offen lds\n" \
"v_mfma_f32_16x16x32_bf16 a[16:19], v[100:103], v[52:55], a[16:19]\n" \
"ds_read_b128 v[116:119], v29 offset:16384\n" \
"v_mfma_f32_16x16x32_bf16 a[48:51], v[100:103], v[56:59], a[48:51]\n" \
"s_add_u32 m0, s31, 0xcc00\n" \
"s_mov_b32 s84, 0x298000\n" \
"s_waitcnt lgkmcnt(15)\n" \
"v_mfma_f32_16x16x32_bf16 a[20:23], v[104:107], v[52:55], a[20:23]\n" \
"buffer_load_dwordx4 v27, s[72:75], s84 offen lds\n" \
"v_mfma_f32_16x16x32_bf16 a[52:55], v[104:107], v[56:59], a[52:55]\n" \
"ds_read_b128 v[120:123], v29 offset:18432\n" \
"s_waitcnt lgkmcnt(14)\n" \
"v_mfma_f32_16x16x32_bf16 a[24:27], v[108:111], v[52:55], a[24:27]\n" \
"ds_read_b64_tr_b16 v[152:153], v33 offset:8192\n" \
"v_mfma_f32_16x16x32_bf16 a[56:59], v[108:111], v[56:59], a[56:59]\n" \
"ds_read_b64_tr_b16 v[154:155], v33 offset:9216\n" \
"s_waitcnt lgkmcnt(13)\n" \
"v_mfma_f32_16x16x32_bf16 a[28:31], v[112:115], v[52:55], a[28:31]\n" \
"ds_read_b64_tr_b16 v[156:157], v34 offset:8192\n" \
"v_mfma_f32_16x16x32_bf16 a[60:63], v[112:115], v[56:59], a[60:63]\n" \
"ds_read_b64_tr_b16 v[158:159], v34 offset:9216\n" \
"s_waitcnt lgkmcnt(13)\n" \
"v_mfma_f32_16x16x32_bf16 a[92:95], v[112:115], v[60:63], a[92:95]\n" \
"s_cmp_lg_u32 s40, 0\n" \
"s_cselect_b32 s41, s41, s42\n" \
"s_waitcnt lgkmcnt(12)\n" \
"v_mfma_f32_16x16x32_bf16 a[124:127], v[112:115], v[64:67], a[124:127]\n" \
"ds_read_b64_tr_b16 v[160:161], v35 offset:8192\n" \
"v_lshl_or_b32 v218, s5, 3, v212\n" \
"v_mfma_f32_16x16x32_bf16 a[88:91], v[108:111], v[60:63], a[88:91]\n" \
"v_cndmask_b32_e32 v40, v222, v220, vcc\n" \
"v_cndmask_b32_e64 v42, v220, v222, vcc\n" \
"v_mfma_f32_16x16x32_bf16 a[120:123], v[108:111], v[64:67], a[120:123]\n" \
"ds_read_b64_tr_b16 v[162:163], v35 offset:9216\n" \
"v_cmp_gt_u32_e64 vcc, v219, v217\n" \
"v_mfma_f32_16x16x32_bf16 a[84:87], v[104:107], v[60:63], a[84:87]\n" \
"s_mov_b32 m0, s32\n" \
"v_and_b32_e32 v219, 16, v20\n" \
"v_mfma_f32_16x16x32_bf16 a[116:119], v[104:107], v[64:67], a[116:119]\n" \
"s_mov_b32 exec_lo, 0x0\n" \
"s_mov_b32 exec_hi, 0xffffffff\n" \
"global_load_lds_dword v[188:189], off\n" \
"s_mov_b64 exec, -1\n" \
"v_mfma_f32_16x16x32_bf16 a[80:83], v[100:103], v[60:63], a[80:83]\n" \
"ds_read_b64_tr_b16 v[164:165], v36 offset:8192\n" \
"s_movk_i32 s44, 0x3f80\n" \
"v_mfma_f32_16x16x32_bf16 a[112:115], v[100:103], v[64:67], a[112:115]\n" \
"ds_read_b64_tr_b16 v[166:167], v36 offset:9216\n" \
"v_add_u32_e64 v218, s41, v218\n" \
"v_mfma_f32_16x16x32_bf16 a[76:79], v[96:99], v[60:63], a[76:79]\n" \
"v_and_b32_e32 v217, 0x320, v20\n" \
"s_movk_i32 s41, 0x100\n" \
"v_mfma_f32_16x16x32_bf16 a[108:111], v[96:99], v[64:67], a[108:111]\n" \
"v_lshlrev_b32_e64 v219, v219, s44\n" \
"v_mov_b32_e32 v220, 0\n" \
"v_mfma_f32_16x16x32_bf16 a[72:75], v[92:95], v[60:63], a[72:75]\n" \
"ds_read_b64_tr_b16 v[168:169], v37 offset:8192\n" \
"v_lshrrev_b32_e64 v215, 6, v20\n" \
"v_mfma_f32_16x16x32_bf16 a[104:107], v[92:95], v[64:67], a[104:107]\n" \
"buffer_store_dwordx4 v[200:203], v20, s[80:83], s35 offen sc1\n" \
"v_mfma_f32_16x16x32_bf16 a[68:71], v[88:91], v[60:63], a[68:71]\n" \
"ds_read_b64_tr_b16 v[170:171], v37 offset:9216\n" \
"v_cndmask_b32_e64 v41, v222, v221, vcc\n" \
"v_mfma_f32_16x16x32_bf16 a[100:103], v[88:91], v[64:67], a[100:103]\n" \
"v_cndmask_b32_e32 v43, v221, v222, vcc\n" \
"v_cmp_eq_u32_e32 vcc, s41, v217\n" \
"v_mfma_f32_16x16x32_bf16 a[64:67], v[84:87], v[60:63], a[64:67]\n" \
"ds_read_b64_tr_b16 v[172:173], v38 offset:8192\n" \
"v_and_b32_e32 v217, 0x330, v20\n" \
"v_mfma_f32_16x16x32_bf16 a[96:99], v[84:87], v[64:67], a[96:99]\n" \
"v_bfe_u32 v216, v20, 6, 2\n" \
"v_cndmask_b32_e32 v46, v220, v219, vcc\n" \
"s_waitcnt lgkmcnt(15)\n" \
"v_mfma_f32_16x16x32_bf16 a[128:131], v[84:87], v[68:71], a[128:131]\n" \
"ds_read_b64_tr_b16 v[174:175], v38 offset:9216\n" \
"v_mov_b32_e32 v219, 0x3f80\n" \
"v_mfma_f32_16x16x32_bf16 a[160:163], v[84:87], v[72:75], a[160:163]\n" \
"v_cmp_eq_u32_e32 vcc, 32, v217\n" \
"v_lshrrev_b32_e32 v220, 1, v20\n" \
"v_mfma_f32_16x16x32_bf16 a[132:135], v[88:91], v[68:71], a[132:135]\n" \
"buffer_store_dwordx4 v[204:207], v20, s[80:83], s88 offen sc1\n" \
"v_mfma_f32_16x16x32_bf16 a[164:167], v[88:91], v[72:75], a[164:167]\n" \
"ds_read_b64_tr_b16 v[176:177], v39 offset:8192\n" \
"v_bfe_u32 v215, v215, 1, 1\n" \
"v_mfma_f32_16x16x32_bf16 a[136:139], v[92:95], v[68:71], a[136:139]\n" \
"ds_read_b64_tr_b16 v[178:179], v39 offset:9216\n" \
"v_cndmask_b32_e64 v47, 0, v219, vcc\n" \
"v_mfma_f32_16x16x32_bf16 a[168:171], v[92:95], v[72:75], a[168:171]\n" \
"s_lshl_b32 s41, s5, 2\n" \
"v_lshlrev_b32_e32 v219, 10, v213\n" \
"v_mfma_f32_16x16x32_bf16 a[140:143], v[96:99], v[68:71], a[140:143]\n" \
"v_lshlrev_b32_e32 v216, 7, v216\n" \
"v_and_b32_e32 v220, 24, v220\n" \
"v_mfma_f32_16x16x32_bf16 a[172:175], v[96:99], v[72:75], a[172:175]\n" \
"ds_read_b128 v[124:127], v29 offset:20480\n" \
"v_and_or_b32 v212, v212, 2, v215\n" \
"v_mfma_f32_16x16x32_bf16 a[144:147], v[100:103], v[68:71], a[144:147]\n" \
"s_lshl_b32 s7, s7, 3\n" \
"v_or_b32_e32 v217, s41, v213\n" \
"v_mfma_f32_16x16x32_bf16 a[176:179], v[100:103], v[72:75], a[176:179]\n" \
"buffer_store_dwordx4 v[208:211], v20, s[80:83], s36 offen sc1\n" \
"v_mfma_f32_16x16x32_bf16 a[148:151], v[104:107], v[68:71], a[148:151]\n" \
"ds_read_b128 v[128:131], v29 offset:22528\n" \
"v_or3_b32 v216, v216, v219, v220\n" \
"v_mfma_f32_16x16x32_bf16 a[180:183], v[104:107], v[72:75], a[180:183]\n" \
"ds_read_b128 v[132:135], v29 offset:0\n" \
"v_lshlrev_b32_e64 v212, 5, v212\n" \
"v_mfma_f32_16x16x32_bf16 a[152:155], v[108:111], v[68:71], a[152:155]\n" \
"s_mov_b32 s44, 0x14000\n" \
"s_or_b32 s7, s7, s41\n" \
"v_mfma_f32_16x16x32_bf16 a[184:187], v[108:111], v[72:75], a[184:187]\n" \
"ds_read_b128 v[136:139], v29 offset:2048\n" \
"s_mov_b32 s41, 0xff800000\n" \
"v_mfma_f32_16x16x32_bf16 a[156:159], v[112:115], v[68:71], a[156:159]\n" \
"s_cmp_lg_u32 s40, 0\n" \
"s_cselect_b32 s47, s42, s41\n" \
"v_mfma_f32_16x16x32_bf16 a[188:191], v[112:115], v[72:75], a[188:191]\n" \
"ds_read_b128 v[140:143], v29 offset:4096\n" \
"s_mov_b32 s40, 0xc000\n" \
"v_mfma_f32_16x16x32_bf16 a[220:223], v[112:115], v[76:79], a[220:223]\n" \
"s_bfe_u32 s45, s2, 0x20003\n" \
"v_or3_b32 v190, v216, v212, s44\n" \
"v_mfma_f32_16x16x32_bf16 a[252:255], v[112:115], v[80:83], a[252:255]\n" \
"ds_read_b128 v[144:147], v29 offset:6144\n" \
"s_mov_b32 s41, 0x8000\n" \
"v_mfma_f32_16x16x32_bf16 a[216:219], v[108:111], v[76:79], a[216:219]\n" \
"s_cmp_lg_u32 s4, 0\n" \
"s_cselect_b32 s51, s40, s41\n" \
"v_mfma_f32_16x16x32_bf16 a[248:251], v[108:111], v[80:83], a[248:251]\n" \
"s_movk_i32 s40, 0x4000\n" \
"s_lshr_b32 s44, s3, 1\n" \
"v_mfma_f32_16x16x32_bf16 a[212:215], v[104:107], v[76:79], a[212:215]\n" \
"s_or_b32 s46, s43, s45\n" \
"v_mfma_f32_16x16x32_bf16 a[244:247], v[104:107], v[80:83], a[244:247]\n" \
"s_cmp_lg_u32 s4, 0\n" \
"s_cselect_b32 s53, s41, s40\n" \
"v_mfma_f32_16x16x32_bf16 a[208:211], v[100:103], v[76:79], a[208:211]\n" \
"s_lshl_b32 s40, s44, 15\n" \
"s_lshl_b32 s41, s5, 14\n" \
"v_mfma_f32_16x16x32_bf16 a[240:243], v[100:103], v[80:83], a[240:243]\n" \
"s_lshl_b32 s48, s46, 20\n" \
"s_or_b32 s41, s40, s41\n" \
"v_mfma_f32_16x16x32_bf16 a[204:207], v[96:99], v[76:79], a[204:207]\n" \
"s_add_i32 s49, s48, 0x1000000\n" \
"s_or_b32 s42, s49, s41\n" \
"v_mfma_f32_16x16x32_bf16 a[236:239], v[96:99], v[80:83], a[236:239]\n" \
"s_lshl_b32 s4, s4, 16\n" \
"s_or_b32 s41, s42, s4\n" \
"v_mfma_f32_16x16x32_bf16 a[200:203], v[92:95], v[76:79], a[200:203]\n" \
"s_lshl_b32 s4, s6, 16\n" \
"s_or_b32 s42, s42, s4\n" \
"v_mfma_f32_16x16x32_bf16 a[232:235], v[92:95], v[80:83], a[232:235]\n" \
"s_lshl_b32 s4, s5, 11\n" \
"s_mul_i32 s5, s7, 0x24800\n" \
"v_mfma_f32_16x16x32_bf16 a[196:199], v[88:91], v[76:79], a[196:199]\n" \
"s_lshl_b32 s6, s46, 9\n" \
"s_lshl_b32 s7, s43, 6\n" \
"v_mfma_f32_16x16x32_bf16 a[228:231], v[88:91], v[80:83], a[228:231]\n" \
"s_add_i32 s6, s5, s6\n" \
"s_or_b32 s5, s5, s7\n" \
"v_mfma_f32_16x16x32_bf16 a[192:195], v[84:87], v[76:79], a[192:195]\n" \
"s_lshl_b32 s7, s45, 2\n" \
"s_or_b32 s5, s5, s7\n" \
"v_mfma_f32_16x16x32_bf16 a[224:227], v[84:87], v[80:83], a[224:227]\n" \
"s_waitcnt vmcnt(4) lgkmcnt(0)\n" \
"s_barrier\n" \
"v_mfma_f32_16x16x32_bf16 a[0:3], v[148:151], v[116:119], a[0:3]\n" \
"s_add_u32 m0, s30, 0x0\n" \
"s_mov_b32 s85, 0x300\n" \
"v_mfma_f32_16x16x32_bf16 a[32:35], v[148:151], v[120:123], a[32:35]\n" \
"buffer_load_dwordx4 v22, s[64:67], s85 offen lds\n" \
"v_mfma_f32_16x16x32_bf16 a[4:7], v[152:155], v[116:119], a[4:7]\n" \
"ds_read_b64_tr_b16 v[84:85], v32 offset:32768\n" \
"s_lshl_b32 s7, s3, 12\n" \
"v_mfma_f32_16x16x32_bf16 a[36:39], v[152:155], v[120:123], a[36:39]\n" \
"ds_read_b64_tr_b16 v[86:87], v32 offset:33792\n" \
"s_or_b32 s50, s49, s7\n" \
"v_mfma_f32_16x16x32_bf16 a[8:11], v[156:159], v[116:119], a[8:11]\n" \
"s_add_u32 m0, s30, 0x400\n" \
"s_mov_b32 s84, 0x2300\n" \
"v_mfma_f32_16x16x32_bf16 a[40:43], v[156:159], v[120:123], a[40:43]\n" \
"buffer_load_dwordx4 v23, s[64:67], s84 offen lds\n" \
"v_mfma_f32_16x16x32_bf16 a[12:15], v[160:163], v[116:119], a[12:15]\n" \
"ds_read_b128 v[52:55], v28 offset:49152\n" \
"s_lshl_b32 s7, s3, 11\n" \
"v_mfma_f32_16x16x32_bf16 a[44:47], v[160:163], v[120:123], a[44:47]\n" \
"ds_read_b128 v[56:59], v28 offset:51200\n" \
"v_lshrrev_b32_e64 v212, 4, v20\n" \
"v_mfma_f32_16x16x32_bf16 a[16:19], v[164:167], v[116:119], a[16:19]\n" \
"s_add_u32 m0, s31, 0x0\n" \
"s_mov_b32 s85, 0x300000\n" \
"v_mfma_f32_16x16x32_bf16 a[48:51], v[164:167], v[120:123], a[48:51]\n" \
"buffer_load_dwordx4 v26, s[68:71], s85 offen lds\n" \
"v_mfma_f32_16x16x32_bf16 a[20:23], v[168:171], v[116:119], a[20:23]\n" \
"ds_read_b64_tr_b16 v[88:89], v33 offset:32768\n" \
"v_bfe_u32 v215, v20, 4, 3\n" \
"v_mfma_f32_16x16x32_bf16 a[52:55], v[168:171], v[120:123], a[52:55]\n" \
"ds_read_b64_tr_b16 v[90:91], v33 offset:33792\n" \
"s_add_i32 s52, s48, s7\n" \
"v_mfma_f32_16x16x32_bf16 a[24:27], v[172:175], v[116:119], a[24:27]\n" \
"s_add_u32 m0, s31, 0x4000\n" \
"s_mov_b32 s84, 0x300000\n" \
"v_mfma_f32_16x16x32_bf16 a[56:59], v[172:175], v[120:123], a[56:59]\n" \
"buffer_load_dwordx4 v26, s[72:75], s84 offen lds\n" \
"v_mfma_f32_16x16x32_bf16 a[28:31], v[176:179], v[116:119], a[28:31]\n" \
"ds_read_b64_tr_b16 v[92:93], v34 offset:32768\n" \
"s_add_i32 s53, s53, s7\n" \
"s_nop 0\n" \
"v_mfma_f32_16x16x32_bf16 a[60:63], v[176:179], v[120:123], a[60:63]\n" \
"ds_read_b64_tr_b16 v[94:95], v34 offset:33792\n" \
"v_mul_u32_u24_e32 v214, 0x480, v214\n" \
"v_mfma_f32_16x16x32_bf16 a[92:95], v[176:179], v[124:127], a[92:95]\n" \
"s_add_u32 m0, s30, 0x800\n" \
"s_mov_b32 s85, 0x4300\n" \
"v_mfma_f32_16x16x32_bf16 a[124:127], v[176:179], v[128:131], a[124:127]\n" \
"buffer_load_dwordx4 v22, s[64:67], s85 offen lds\n" \
"v_mfma_f32_16x16x32_bf16 a[88:91], v[172:175], v[124:127], a[88:91]\n" \
"ds_read_b64_tr_b16 v[96:97], v35 offset:32768\n" \
"v_bitop3_b32 v216, v213, v212, 7 bitop3:0x78\n" \
"v_mfma_f32_16x16x32_bf16 a[120:123], v[172:175], v[128:131], a[120:123]\n" \
"ds_read_b64_tr_b16 v[98:99], v35 offset:33792\n" \
"v_bitop3_b32 v213, v213, v215, 4 bitop3:0x36\n" \
"v_mfma_f32_16x16x32_bf16 a[84:87], v[168:171], v[124:127], a[84:87]\n" \
"s_add_u32 m0, s30, 0xc00\n" \
"s_mov_b32 s84, 0x6300\n" \
"v_mfma_f32_16x16x32_bf16 a[116:119], v[168:171], v[128:131], a[116:119]\n" \
"buffer_load_dwordx4 v23, s[64:67], s84 offen lds\n" \
"v_mfma_f32_16x16x32_bf16 a[80:83], v[164:167], v[124:127], a[80:83]\n" \
"ds_read_b64_tr_b16 v[100:101], v36 offset:32768\n" \
"s_lshl_b32 s7, s3, 10\n" \
"v_mfma_f32_16x16x32_bf16 a[112:115], v[164:167], v[128:131], a[112:115]\n" \
"ds_read_b64_tr_b16 v[102:103], v36 offset:33792\n" \
"s_or_b32 s4, s40, s4\n" \
"v_mfma_f32_16x16x32_bf16 a[76:79], v[160:163], v[124:127], a[76:79]\n" \
"s_add_u32 m0, s31, 0x400\n" \
"s_mov_b32 s85, 0x308000\n" \
"v_mfma_f32_16x16x32_bf16 a[108:111], v[160:163], v[128:131], a[108:111]\n" \
"buffer_load_dwordx4 v26, s[68:71], s85 offen lds\n" \
"v_mfma_f32_16x16x32_bf16 a[72:75], v[156:159], v[124:127], a[72:75]\n" \
"ds_read_b64_tr_b16 v[104:105], v37 offset:32768\n" \
"v_lshl_or_b32 v25, v213, 4, v214\n" \
"v_mfma_f32_16x16x32_bf16 a[104:107], v[156:159], v[128:131], a[104:107]\n" \
"v_mul_u32_u24_e32 v213, 0x480, v218\n" \
"v_bitop3_b32 v212, v212, v217, 7 bitop3:0x6c\n" \
"v_mfma_f32_16x16x32_bf16 a[68:71], v[152:155], v[124:127], a[68:71]\n" \
"s_add_u32 m0, s31, 0x4400\n" \
"s_mov_b32 s84, 0x308000\n" \
"v_mfma_f32_16x16x32_bf16 a[100:103], v[152:155], v[128:131], a[100:103]\n" \
"buffer_load_dwordx4 v26, s[72:75], s84 offen lds\n" \
"v_mfma_f32_16x16x32_bf16 a[64:67], v[148:151], v[124:127], a[64:67]\n" \
"ds_read_b64_tr_b16 v[106:107], v37 offset:33792\n" \
"s_or_b32 s40, s7, 0x14000\n" \
"v_mfma_f32_16x16x32_bf16 a[96:99], v[148:151], v[128:131], a[96:99]\n" \
"ds_read_b64_tr_b16 v[108:109], v38 offset:32768\n" \
"s_lshl_b32 s43, s44, 13\n" \
"v_mfma_f32_16x16x32_bf16 a[128:131], v[148:151], v[132:135], a[128:131]\n" \
"s_add_u32 m0, s30, 0x1000\n" \
"s_mov_b32 s85, 0x8300\n" \
"v_mfma_f32_16x16x32_bf16 a[160:163], v[148:151], v[136:139], a[160:163]\n" \
"buffer_load_dwordx4 v22, s[64:67], s85 offen lds\n" \
"v_mfma_f32_16x16x32_bf16 a[132:135], v[152:155], v[132:135], a[132:135]\n" \
"ds_read_b64_tr_b16 v[110:111], v38 offset:33792\n" \
"s_mulk_i32 s44, 0xe800\n" \
"v_mfma_f32_16x16x32_bf16 a[164:167], v[152:155], v[136:139], a[164:167]\n" \
"ds_read_b64_tr_b16 v[112:113], v39 offset:32768\n" \
"s_lshl_b32 s7, s3, 8\n" \
"s_nop 0\n" \
"v_mfma_f32_16x16x32_bf16 a[136:139], v[156:159], v[132:135], a[136:139]\n" \
"s_add_u32 m0, s30, 0x1400\n" \
"s_mov_b32 s84, 0xa300\n" \
"v_mfma_f32_16x16x32_bf16 a[168:171], v[156:159], v[136:139], a[168:171]\n" \
"buffer_load_dwordx4 v23, s[64:67], s84 offen lds\n" \
"v_mfma_f32_16x16x32_bf16 a[140:143], v[160:163], v[132:135], a[140:143]\n" \
"ds_read_b64_tr_b16 v[114:115], v39 offset:33792\n" \
"v_lshl_or_b32 v24, v216, 4, v214\n" \
"v_mfma_f32_16x16x32_bf16 a[172:175], v[160:163], v[136:139], a[172:175]\n" \
"ds_read_b128 v[60:63], v28 offset:53248\n" \
"v_lshl_or_b32 v21, v212, 4, v213\n" \
"v_mfma_f32_16x16x32_bf16 a[144:147], v[164:167], v[132:135], a[144:147]\n" \
"s_add_u32 m0, s31, 0x800\n" \
"s_mov_b32 s85, 0x310000\n" \
"v_mfma_f32_16x16x32_bf16 a[176:179], v[164:167], v[136:139], a[176:179]\n" \
"buffer_load_dwordx4 v27, s[68:71], s85 offen lds\n" \
"v_mfma_f32_16x16x32_bf16 a[148:151], v[168:171], v[132:135], a[148:151]\n" \
"ds_read_b128 v[64:67], v28 offset:55296\n" \
"s_add_i32 s44, s44, 0x17000\n" \
"v_mfma_f32_16x16x32_bf16 a[180:183], v[168:171], v[136:139], a[180:183]\n" \
"ds_read_b128 v[68:71], v28 offset:32768\n" \
"s_or_b32 s45, s7, 0x27000\n" \
"v_mfma_f32_16x16x32_bf16 a[152:155], v[172:175], v[132:135], a[152:155]\n" \
"s_add_u32 m0, s31, 0x4800\n" \
"s_mov_b32 s84, 0x310000\n" \
"v_mfma_f32_16x16x32_bf16 a[184:187], v[172:175], v[136:139], a[184:187]\n" \
"buffer_load_dwordx4 v27, s[72:75], s84 offen lds\n" \
"v_mfma_f32_16x16x32_bf16 a[156:159], v[176:179], v[132:135], a[156:159]\n" \
"ds_read_b128 v[72:75], v28 offset:34816\n" \
"s_or_b32 s46, s4, 0x10000\n" \
"v_mfma_f32_16x16x32_bf16 a[188:191], v[176:179], v[136:139], a[188:191]\n" \
"ds_read_b128 v[76:79], v28 offset:36864\n" \
"s_add_i32 s48, s6, 0x3000000\n" \
"v_mfma_f32_16x16x32_bf16 a[220:223], v[176:179], v[140:143], a[220:223]\n" \
"s_add_i32 s49, s5, 0x3004000\n" \
"s_add_i32 s52, s52, 0x1010000\n" \
"v_mfma_f32_16x16x32_bf16 a[252:255], v[176:179], v[144:147], a[252:255]\n" \
"ds_read_b128 v[80:83], v28 offset:38912\n" \
"v_mfma_f32_16x16x32_bf16 a[216:219], v[172:175], v[140:143], a[216:219]\n" \
"v_mfma_f32_16x16x32_bf16 a[248:251], v[172:175], v[144:147], a[248:251]\n" \
"v_mfma_f32_16x16x32_bf16 a[212:215], v[168:171], v[140:143], a[212:215]\n" \
"v_mfma_f32_16x16x32_bf16 a[244:247], v[168:171], v[144:147], a[244:247]\n" \
"v_mfma_f32_16x16x32_bf16 a[208:211], v[164:167], v[140:143], a[208:211]\n" \
"v_mfma_f32_16x16x32_bf16 a[240:243], v[164:167], v[144:147], a[240:243]\n" \
"v_mfma_f32_16x16x32_bf16 a[204:207], v[160:163], v[140:143], a[204:207]\n" \
"v_mfma_f32_16x16x32_bf16 a[236:239], v[160:163], v[144:147], a[236:239]\n" \
"v_mfma_f32_16x16x32_bf16 a[200:203], v[156:159], v[140:143], a[200:203]\n" \
"v_mfma_f32_16x16x32_bf16 a[232:235], v[156:159], v[144:147], a[232:235]\n" \
"v_mfma_f32_16x16x32_bf16 a[196:199], v[152:155], v[140:143], a[196:199]\n" \
"v_mfma_f32_16x16x32_bf16 a[228:231], v[152:155], v[144:147], a[228:231]\n" \
"v_mfma_f32_16x16x32_bf16 a[192:195], v[148:151], v[140:143], a[192:195]\n" \
"v_mfma_f32_16x16x32_bf16 a[224:227], v[148:151], v[144:147], a[224:227]\n" \
"s_waitcnt lgkmcnt(15)\n" \
"s_nop 0\n" \
"v_mfma_f32_16x16x32_bf16 a[0:3], v[84:87], v[52:55], a[0:3]\n" \
"s_add_u32 m0, s30, 0x1800\n" \
"s_mov_b32 s85, 0xc300\n" \
"v_mfma_f32_16x16x32_bf16 a[32:35], v[84:87], v[56:59], a[32:35]\n" \
"buffer_load_dwordx4 v22, s[64:67], s85 offen lds\n" \
"v_mfma_f32_16x16x32_bf16 a[4:7], v[88:91], v[52:55], a[4:7]\n" \
"ds_read_b64_tr_b16 v[148:149], v32 offset:40960\n" \
"v_mfma_f32_16x16x32_bf16 a[36:39], v[88:91], v[56:59], a[36:39]\n" \
"s_add_u32 m0, s30, 0x1c00\n" \
"s_mov_b32 s84, 0xe300\n" \
"v_mfma_f32_16x16x32_bf16 a[8:11], v[92:95], v[52:55], a[8:11]\n" \
"buffer_load_dwordx4 v23, s[64:67], s84 offen lds\n" \
"v_mfma_f32_16x16x32_bf16 a[40:43], v[92:95], v[56:59], a[40:43]\n" \
"ds_read_b64_tr_b16 v[150:151], v32 offset:41984\n" \
"s_waitcnt lgkmcnt(15)\n" \
"v_mfma_f32_16x16x32_bf16 a[12:15], v[96:99], v[52:55], a[12:15]\n" \
"s_add_u32 m0, s31, 0xc00\n" \
"s_mov_b32 s85, 0x318000\n" \
"v_mfma_f32_16x16x32_bf16 a[44:47], v[96:99], v[56:59], a[44:47]\n" \
"buffer_load_dwordx4 v27, s[68:71], s85 offen lds\n" \
"s_waitcnt lgkmcnt(14)\n" \
"v_mfma_f32_16x16x32_bf16 a[16:19], v[100:103], v[52:55], a[16:19]\n" \
"ds_read_b128 v[116:119], v29 offset:49152\n" \
"v_mfma_f32_16x16x32_bf16 a[48:51], v[100:103], v[56:59], a[48:51]\n" \
"s_add_u32 m0, s31, 0x4c00\n" \
"s_mov_b32 s84, 0x318000\n" \
"s_waitcnt lgkmcnt(13)\n" \
"v_mfma_f32_16x16x32_bf16 a[20:23], v[104:107], v[52:55], a[20:23]\n" \
"buffer_load_dwordx4 v27, s[72:75], s84 offen lds\n" \
"v_mfma_f32_16x16x32_bf16 a[52:55], v[104:107], v[56:59], a[52:55]\n" \
"ds_read_b128 v[120:123], v29 offset:51200\n" \
"s_waitcnt lgkmcnt(12)\n" \
"v_mfma_f32_16x16x32_bf16 a[24:27], v[108:111], v[52:55], a[24:27]\n" \
"ds_read_b64_tr_b16 v[152:153], v33 offset:40960\n" \
"v_mfma_f32_16x16x32_bf16 a[56:59], v[108:111], v[56:59], a[56:59]\n" \
"ds_read_b64_tr_b16 v[154:155], v33 offset:41984\n" \
"s_waitcnt lgkmcnt(12)\n" \
"v_mfma_f32_16x16x32_bf16 a[28:31], v[112:115], v[52:55], a[28:31]\n" \
"ds_read_b64_tr_b16 v[156:157], v34 offset:40960\n" \
"v_mfma_f32_16x16x32_bf16 a[60:63], v[112:115], v[56:59], a[60:63]\n" \
"ds_read_b64_tr_b16 v[158:159], v34 offset:41984\n" \
"s_waitcnt lgkmcnt(13)\n" \
"v_mfma_f32_16x16x32_bf16 a[92:95], v[112:115], v[60:63], a[92:95]\n" \
"s_waitcnt lgkmcnt(12)\n" \
"v_mfma_f32_16x16x32_bf16 a[124:127], v[112:115], v[64:67], a[124:127]\n" \
"ds_read_b64_tr_b16 v[160:161], v35 offset:40960\n" \
"v_mfma_f32_16x16x32_bf16 a[88:91], v[108:111], v[60:63], a[88:91]\n" \
"v_lshlrev_b32_e32 v45, 3, v20\n" \
"v_add_u32_e64 v45, s31, v45\n" \
"v_mfma_f32_16x16x32_bf16 a[120:123], v[108:111], v[64:67], a[120:123]\n" \
"s_getpc_b64 s[86:87]\n" \
"s_sub_u32 s86, s86, 0xf800\n" \
"s_subb_u32 s87, s87, 0\n" \
"s_mov_b32 m0, s32\n" \
"s_nop 0\n" \
"v_mfma_f32_16x16x32_bf16 a[84:87], v[104:107], v[60:63], a[84:87]\n" \
"global_load_lds_dword v45, s[86:87]\n" \
"v_mfma_f32_16x16x32_bf16 a[116:119], v[104:107], v[64:67], a[116:119]\n" \
"ds_read_b64_tr_b16 v[162:163], v35 offset:41984\n" \
"v_mfma_f32_16x16x32_bf16 a[80:83], v[100:103], v[60:63], a[80:83]\n" \
"ds_read_b64_tr_b16 v[164:165], v36 offset:40960\n" \
"v_mfma_f32_16x16x32_bf16 a[112:115], v[100:103], v[64:67], a[112:115]\n" \
"ds_read_b64_tr_b16 v[166:167], v36 offset:41984\n" \
"v_mfma_f32_16x16x32_bf16 a[76:79], v[96:99], v[60:63], a[76:79]\n" \
"v_mfma_f32_16x16x32_bf16 a[108:111], v[96:99], v[64:67], a[108:111]\n" \
"v_mfma_f32_16x16x32_bf16 a[72:75], v[92:95], v[60:63], a[72:75]\n" \
"ds_read_b64_tr_b16 v[168:169], v37 offset:40960\n" \
"v_mfma_f32_16x16x32_bf16 a[104:107], v[92:95], v[64:67], a[104:107]\n" \
"v_mfma_f32_16x16x32_bf16 a[68:71], v[88:91], v[60:63], a[68:71]\n" \
"ds_read_b64_tr_b16 v[170:171], v37 offset:41984\n" \
"v_mfma_f32_16x16x32_bf16 a[100:103], v[88:91], v[64:67], a[100:103]\n" \
"v_mfma_f32_16x16x32_bf16 a[64:67], v[84:87], v[60:63], a[64:67]\n" \
"ds_read_b64_tr_b16 v[172:173], v38 offset:40960\n" \
"v_mfma_f32_16x16x32_bf16 a[96:99], v[84:87], v[64:67], a[96:99]\n" \
"s_waitcnt lgkmcnt(15)\n" \
"s_nop 0\n" \
"v_mfma_f32_16x16x32_bf16 a[128:131], v[84:87], v[68:71], a[128:131]\n" \
"ds_read_b64_tr_b16 v[174:175], v38 offset:41984\n" \
"v_mfma_f32_16x16x32_bf16 a[160:163], v[84:87], v[72:75], a[160:163]\n" \
"v_mfma_f32_16x16x32_bf16 a[132:135], v[88:91], v[68:71], a[132:135]\n" \
"ds_read_b64_tr_b16 v[176:177], v39 offset:40960\n" \
"v_mfma_f32_16x16x32_bf16 a[164:167], v[88:91], v[72:75], a[164:167]\n" \
"v_mfma_f32_16x16x32_bf16 a[136:139], v[92:95], v[68:71], a[136:139]\n" \
"ds_read_b64_tr_b16 v[178:179], v39 offset:41984\n" \
"v_mfma_f32_16x16x32_bf16 a[168:171], v[92:95], v[72:75], a[168:171]\n" \
"v_mfma_f32_16x16x32_bf16 a[140:143], v[96:99], v[68:71], a[140:143]\n" \
"v_mfma_f32_16x16x32_bf16 a[172:175], v[96:99], v[72:75], a[172:175]\n" \
"ds_read_b128 v[124:127], v29 offset:53248\n" \
"v_mfma_f32_16x16x32_bf16 a[144:147], v[100:103], v[68:71], a[144:147]\n" \
"v_mfma_f32_16x16x32_bf16 a[176:179], v[100:103], v[72:75], a[176:179]\n" \
"ds_read_b128 v[128:131], v29 offset:55296\n" \
"v_mfma_f32_16x16x32_bf16 a[148:151], v[104:107], v[68:71], a[148:151]\n" \
"v_mfma_f32_16x16x32_bf16 a[180:183], v[104:107], v[72:75], a[180:183]\n" \
"ds_read_b128 v[132:135], v29 offset:32768\n" \
"v_mfma_f32_16x16x32_bf16 a[152:155], v[108:111], v[68:71], a[152:155]\n" \
"v_mfma_f32_16x16x32_bf16 a[184:187], v[108:111], v[72:75], a[184:187]\n" \
"ds_read_b128 v[136:139], v29 offset:34816\n" \
"v_mfma_f32_16x16x32_bf16 a[156:159], v[112:115], v[68:71], a[156:159]\n" \
"v_mfma_f32_16x16x32_bf16 a[188:191], v[112:115], v[72:75], a[188:191]\n" \
"ds_read_b128 v[140:143], v29 offset:36864\n" \
"v_mfma_f32_16x16x32_bf16 a[220:223], v[112:115], v[76:79], a[220:223]\n" \
"v_mfma_f32_16x16x32_bf16 a[252:255], v[112:115], v[80:83], a[252:255]\n" \
"ds_read_b128 v[144:147], v29 offset:38912\n" \
"v_mfma_f32_16x16x32_bf16 a[216:219], v[108:111], v[76:79], a[216:219]\n" \
"v_mfma_f32_16x16x32_bf16 a[248:251], v[108:111], v[80:83], a[248:251]\n" \
"v_mfma_f32_16x16x32_bf16 a[212:215], v[104:107], v[76:79], a[212:215]\n" \
"v_mfma_f32_16x16x32_bf16 a[244:247], v[104:107], v[80:83], a[244:247]\n" \
"v_mfma_f32_16x16x32_bf16 a[208:211], v[100:103], v[76:79], a[208:211]\n" \
"v_mfma_f32_16x16x32_bf16 a[240:243], v[100:103], v[80:83], a[240:243]\n" \
"v_mfma_f32_16x16x32_bf16 a[204:207], v[96:99], v[76:79], a[204:207]\n" \
"v_mfma_f32_16x16x32_bf16 a[236:239], v[96:99], v[80:83], a[236:239]\n" \
"v_mfma_f32_16x16x32_bf16 a[200:203], v[92:95], v[76:79], a[200:203]\n" \
"v_mfma_f32_16x16x32_bf16 a[232:235], v[92:95], v[80:83], a[232:235]\n" \
"v_mfma_f32_16x16x32_bf16 a[196:199], v[88:91], v[76:79], a[196:199]\n" \
"v_mfma_f32_16x16x32_bf16 a[228:231], v[88:91], v[80:83], a[228:231]\n" \
"v_mfma_f32_16x16x32_bf16 a[192:195], v[84:87], v[76:79], a[192:195]\n" \
"v_mfma_f32_16x16x32_bf16 a[224:227], v[84:87], v[80:83], a[224:227]\n" \
"s_waitcnt vmcnt(1) lgkmcnt(0)\n" \
"s_barrier\n" \
"v_mov_b32_e32 v45, s62\n" \
"s_mov_b64 exec, 1\n" \
"buffer_store_dword v45, off, s[80:83], s37 offset:128 sc1\n" \
"s_mov_b64 exec, -1\n" \
"s_nop 0\n" \
"v_mfma_f32_16x16x32_bf16 a[0:3], v[148:151], v[116:119], a[0:3]\n" \
"s_add_u32 m0, s30, 0x8000\n" \
"s_mov_b32 s85, 0x380\n" \
"v_mfma_f32_16x16x32_bf16 a[32:35], v[148:151], v[120:123], a[32:35]\n" \
"buffer_load_dwordx4 v22, s[64:67], s85 offen lds\n" \
"v_mfma_f32_16x16x32_bf16 a[4:7], v[152:155], v[116:119], a[4:7]\n" \
"ds_read_b64_tr_b16 v[84:85], v32 offset:0\n" \
"v_mfma_f32_16x16x32_bf16 a[36:39], v[152:155], v[120:123], a[36:39]\n" \
"ds_read_b64_tr_b16 v[86:87], v32 offset:1024\n" \
"v_mfma_f32_16x16x32_bf16 a[8:11], v[156:159], v[116:119], a[8:11]\n" \
"s_add_u32 m0, s30, 0x8400\n" \
"s_mov_b32 s84, 0x2380\n" \
"v_mfma_f32_16x16x32_bf16 a[40:43], v[156:159], v[120:123], a[40:43]\n" \
"buffer_load_dwordx4 v23, s[64:67], s84 offen lds\n" \
"v_mfma_f32_16x16x32_bf16 a[12:15], v[160:163], v[116:119], a[12:15]\n" \
"ds_read_b128 v[52:55], v28 offset:16384\n" \
"v_mfma_f32_16x16x32_bf16 a[44:47], v[160:163], v[120:123], a[44:47]\n" \
"ds_read_b128 v[56:59], v28 offset:18432\n" \
"v_mfma_f32_16x16x32_bf16 a[16:19], v[164:167], v[116:119], a[16:19]\n" \
"s_add_u32 m0, s31, 0x8000\n" \
"s_mov_b32 s85, 0x380000\n" \
"v_mfma_f32_16x16x32_bf16 a[48:51], v[164:167], v[120:123], a[48:51]\n" \
"buffer_load_dwordx4 v26, s[68:71], s85 offen lds\n" \
"v_mfma_f32_16x16x32_bf16 a[20:23], v[168:171], v[116:119], a[20:23]\n" \
"ds_read_b64_tr_b16 v[88:89], v33 offset:0\n" \
"v_mfma_f32_16x16x32_bf16 a[52:55], v[168:171], v[120:123], a[52:55]\n" \
"ds_read_b64_tr_b16 v[90:91], v33 offset:1024\n" \
"v_mfma_f32_16x16x32_bf16 a[24:27], v[172:175], v[116:119], a[24:27]\n" \
"s_add_u32 m0, s31, 0xc000\n" \
"s_mov_b32 s84, 0x380000\n" \
"v_mfma_f32_16x16x32_bf16 a[56:59], v[172:175], v[120:123], a[56:59]\n" \
"buffer_load_dwordx4 v26, s[72:75], s84 offen lds\n" \
"v_mfma_f32_16x16x32_bf16 a[28:31], v[176:179], v[116:119], a[28:31]\n" \
"ds_read_b64_tr_b16 v[92:93], v34 offset:0\n" \
"v_mfma_f32_16x16x32_bf16 a[60:63], v[176:179], v[120:123], a[60:63]\n" \
"ds_read_b64_tr_b16 v[94:95], v34 offset:1024\n" \
"v_mfma_f32_16x16x32_bf16 a[92:95], v[176:179], v[124:127], a[92:95]\n" \
"s_add_u32 m0, s30, 0x8800\n" \
"s_mov_b32 s85, 0x4380\n" \
"v_mfma_f32_16x16x32_bf16 a[124:127], v[176:179], v[128:131], a[124:127]\n" \
"buffer_load_dwordx4 v22, s[64:67], s85 offen lds\n" \
"v_mfma_f32_16x16x32_bf16 a[88:91], v[172:175], v[124:127], a[88:91]\n" \
"ds_read_b64_tr_b16 v[96:97], v35 offset:0\n" \
"v_mfma_f32_16x16x32_bf16 a[120:123], v[172:175], v[128:131], a[120:123]\n" \
"ds_read_b64_tr_b16 v[98:99], v35 offset:1024\n" \
"v_mfma_f32_16x16x32_bf16 a[84:87], v[168:171], v[124:127], a[84:87]\n" \
"s_add_u32 m0, s30, 0x8c00\n" \
"s_mov_b32 s84, 0x6380\n" \
"v_mfma_f32_16x16x32_bf16 a[116:119], v[168:171], v[128:131], a[116:119]\n" \
"buffer_load_dwordx4 v23, s[64:67], s84 offen lds\n" \
"v_mfma_f32_16x16x32_bf16 a[80:83], v[164:167], v[124:127], a[80:83]\n" \
"ds_read_b64_tr_b16 v[100:101], v36 offset:0\n" \
"v_mfma_f32_16x16x32_bf16 a[112:115], v[164:167], v[128:131], a[112:115]\n" \
"ds_read_b64_tr_b16 v[102:103], v36 offset:1024\n" \
"v_mfma_f32_16x16x32_bf16 a[76:79], v[160:163], v[124:127], a[76:79]\n" \
"s_add_u32 m0, s31, 0x8400\n" \
"s_mov_b32 s85, 0x388000\n" \
"v_mfma_f32_16x16x32_bf16 a[108:111], v[160:163], v[128:131], a[108:111]\n" \
"buffer_load_dwordx4 v26, s[68:71], s85 offen lds\n" \
"v_mfma_f32_16x16x32_bf16 a[72:75], v[156:159], v[124:127], a[72:75]\n" \
"ds_read_b64_tr_b16 v[104:105], v37 offset:0\n" \
"v_mfma_f32_16x16x32_bf16 a[104:107], v[156:159], v[128:131], a[104:107]\n" \
"v_mfma_f32_16x16x32_bf16 a[68:71], v[152:155], v[124:127], a[68:71]\n" \
"s_add_u32 m0, s31, 0xc400\n" \
"s_mov_b32 s84, 0x388000\n" \
"v_mfma_f32_16x16x32_bf16 a[100:103], v[152:155], v[128:131], a[100:103]\n" \
"buffer_load_dwordx4 v26, s[72:75], s84 offen lds\n" \
"v_mfma_f32_16x16x32_bf16 a[64:67], v[148:151], v[124:127], a[64:67]\n" \
"ds_read_b64_tr_b16 v[106:107], v37 offset:1024\n" \
"v_mfma_f32_16x16x32_bf16 a[96:99], v[148:151], v[128:131], a[96:99]\n" \
"ds_read_b64_tr_b16 v[108:109], v38 offset:0\n" \
"v_mfma_f32_16x16x32_bf16 a[128:131], v[148:151], v[132:135], a[128:131]\n" \
"s_add_u32 m0, s30, 0x9000\n" \
"s_mov_b32 s85, 0x8380\n" \
"v_mfma_f32_16x16x32_bf16 a[160:163], v[148:151], v[136:139], a[160:163]\n" \
"buffer_load_dwordx4 v22, s[64:67], s85 offen lds\n" \
"v_mfma_f32_16x16x32_bf16 a[132:135], v[152:155], v[132:135], a[132:135]\n" \
"ds_read_b64_tr_b16 v[110:111], v38 offset:1024\n" \
"v_mfma_f32_16x16x32_bf16 a[164:167], v[152:155], v[136:139], a[164:167]\n" \
"ds_read_b64_tr_b16 v[112:113], v39 offset:0\n" \
"v_mfma_f32_16x16x32_bf16 a[136:139], v[156:159], v[132:135], a[136:139]\n" \
"s_add_u32 m0, s30, 0x9400\n" \
"s_mov_b32 s84, 0xa380\n" \
"v_mfma_f32_16x16x32_bf16 a[168:171], v[156:159], v[136:139], a[168:171]\n" \
"buffer_load_dwordx4 v23, s[64:67], s84 offen lds\n" \
"v_mfma_f32_16x16x32_bf16 a[140:143], v[160:163], v[132:135], a[140:143]\n" \
"ds_read_b64_tr_b16 v[114:115], v39 offset:1024\n" \
"v_mfma_f32_16x16x32_bf16 a[172:175], v[160:163], v[136:139], a[172:175]\n" \
"ds_read_b128 v[60:63], v28 offset:20480\n" \
"v_mfma_f32_16x16x32_bf16 a[144:147], v[164:167], v[132:135], a[144:147]\n" \
"s_add_u32 m0, s31, 0x8800\n" \
"s_mov_b32 s85, 0x390000\n" \
"v_mfma_f32_16x16x32_bf16 a[176:179], v[164:167], v[136:139], a[176:179]\n" \
"buffer_load_dwordx4 v27, s[68:71], s85 offen lds\n" \
"v_mfma_f32_16x16x32_bf16 a[148:151], v[168:171], v[132:135], a[148:151]\n" \
"ds_read_b128 v[64:67], v28 offset:22528\n" \
"v_mfma_f32_16x16x32_bf16 a[180:183], v[168:171], v[136:139], a[180:183]\n" \
"ds_read_b128 v[68:71], v28 offset:0\n" \
"v_mfma_f32_16x16x32_bf16 a[152:155], v[172:175], v[132:135], a[152:155]\n" \
"s_add_u32 m0, s31, 0xc800\n" \
"s_mov_b32 s84, 0x390000\n" \
"v_mfma_f32_16x16x32_bf16 a[184:187], v[172:175], v[136:139], a[184:187]\n" \
"buffer_load_dwordx4 v27, s[72:75], s84 offen lds\n" \
"v_mfma_f32_16x16x32_bf16 a[156:159], v[176:179], v[132:135], a[156:159]\n" \
"ds_read_b128 v[72:75], v28 offset:2048\n" \
"v_mfma_f32_16x16x32_bf16 a[188:191], v[176:179], v[136:139], a[188:191]\n" \
"ds_read_b128 v[76:79], v28 offset:4096\n" \
"v_mfma_f32_16x16x32_bf16 a[220:223], v[176:179], v[140:143], a[220:223]\n" \
"v_mfma_f32_16x16x32_bf16 a[252:255], v[176:179], v[144:147], a[252:255]\n" \
"ds_read_b128 v[80:83], v28 offset:6144\n" \
"v_mfma_f32_16x16x32_bf16 a[216:219], v[172:175], v[140:143], a[216:219]\n" \
"v_mfma_f32_16x16x32_bf16 a[248:251], v[172:175], v[144:147], a[248:251]\n" \
"v_mfma_f32_16x16x32_bf16 a[212:215], v[168:171], v[140:143], a[212:215]\n" \
"v_mfma_f32_16x16x32_bf16 a[244:247], v[168:171], v[144:147], a[244:247]\n" \
"v_mfma_f32_16x16x32_bf16 a[208:211], v[164:167], v[140:143], a[208:211]\n" \
"v_mfma_f32_16x16x32_bf16 a[240:243], v[164:167], v[144:147], a[240:243]\n" \
"v_mfma_f32_16x16x32_bf16 a[204:207], v[160:163], v[140:143], a[204:207]\n" \
"v_mfma_f32_16x16x32_bf16 a[236:239], v[160:163], v[144:147], a[236:239]\n" \
"v_mfma_f32_16x16x32_bf16 a[200:203], v[156:159], v[140:143], a[200:203]\n" \
"v_mfma_f32_16x16x32_bf16 a[232:235], v[156:159], v[144:147], a[232:235]\n" \
"v_mfma_f32_16x16x32_bf16 a[196:199], v[152:155], v[140:143], a[196:199]\n" \
"v_mfma_f32_16x16x32_bf16 a[228:231], v[152:155], v[144:147], a[228:231]\n" \
"v_mfma_f32_16x16x32_bf16 a[192:195], v[148:151], v[140:143], a[192:195]\n" \
"v_mfma_f32_16x16x32_bf16 a[224:227], v[148:151], v[144:147], a[224:227]\n" \
"s_waitcnt lgkmcnt(15)\n" \
"s_nop 0\n" \
"v_mfma_f32_16x16x32_bf16 a[0:3], v[84:87], v[52:55], a[0:3]\n" \
"s_add_u32 m0, s30, 0x9800\n" \
"s_mov_b32 s85, 0xc380\n" \
"v_mfma_f32_16x16x32_bf16 a[32:35], v[84:87], v[56:59], a[32:35]\n" \
"buffer_load_dwordx4 v22, s[64:67], s85 offen lds\n" \
"v_mfma_f32_16x16x32_bf16 a[4:7], v[88:91], v[52:55], a[4:7]\n" \
"ds_read_b64_tr_b16 v[148:149], v32 offset:8192\n" \
"v_mfma_f32_16x16x32_bf16 a[36:39], v[88:91], v[56:59], a[36:39]\n" \
"s_add_u32 m0, s30, 0x9c00\n" \
"s_mov_b32 s84, 0xe380\n" \
"v_mfma_f32_16x16x32_bf16 a[8:11], v[92:95], v[52:55], a[8:11]\n" \
"buffer_load_dwordx4 v23, s[64:67], s84 offen lds\n" \
"v_mfma_f32_16x16x32_bf16 a[40:43], v[92:95], v[56:59], a[40:43]\n" \
"ds_read_b64_tr_b16 v[150:151], v32 offset:9216\n" \
"s_waitcnt lgkmcnt(15)\n" \
"v_mfma_f32_16x16x32_bf16 a[12:15], v[96:99], v[52:55], a[12:15]\n" \
"s_add_u32 m0, s31, 0x8c00\n" \
"s_mov_b32 s85, 0x398000\n" \
"v_mfma_f32_16x16x32_bf16 a[44:47], v[96:99], v[56:59], a[44:47]\n" \
"buffer_load_dwordx4 v27, s[68:71], s85 offen lds\n" \
"s_waitcnt lgkmcnt(14)\n" \
"v_mfma_f32_16x16x32_bf16 a[16:19], v[100:103], v[52:55], a[16:19]\n" \
"ds_read_b128 v[116:119], v29 offset:16384\n" \
"v_mfma_f32_16x16x32_bf16 a[48:51], v[100:103], v[56:59], a[48:51]\n" \
"s_add_u32 m0, s31, 0xcc00\n" \
"s_mov_b32 s84, 0x398000\n" \
"s_waitcnt lgkmcnt(13)\n" \
"v_mfma_f32_16x16x32_bf16 a[20:23], v[104:107], v[52:55], a[20:23]\n" \
"buffer_load_dwordx4 v27, s[72:75], s84 offen lds\n" \
"v_mfma_f32_16x16x32_bf16 a[52:55], v[104:107], v[56:59], a[52:55]\n" \
"ds_read_b128 v[120:123], v29 offset:18432\n" \
"s_waitcnt lgkmcnt(12)\n" \
"v_mfma_f32_16x16x32_bf16 a[24:27], v[108:111], v[52:55], a[24:27]\n" \
"ds_read_b64_tr_b16 v[152:153], v33 offset:8192\n" \
"v_mfma_f32_16x16x32_bf16 a[56:59], v[108:111], v[56:59], a[56:59]\n" \
"ds_read_b64_tr_b16 v[154:155], v33 offset:9216\n" \
"s_waitcnt lgkmcnt(12)\n" \
"v_mfma_f32_16x16x32_bf16 a[28:31], v[112:115], v[52:55], a[28:31]\n" \
"ds_read_b64_tr_b16 v[156:157], v34 offset:8192\n" \
"v_mfma_f32_16x16x32_bf16 a[60:63], v[112:115], v[56:59], a[60:63]\n" \
"ds_read_b64_tr_b16 v[158:159], v34 offset:9216\n" \
"s_waitcnt lgkmcnt(13)\n" \
"v_mfma_f32_16x16x32_bf16 a[92:95], v[112:115], v[60:63], a[92:95]\n" \
"s_waitcnt lgkmcnt(12)\n" \
"s_nop 0\n" \
"v_mfma_f32_16x16x32_bf16 a[124:127], v[112:115], v[64:67], a[124:127]\n" \
"ds_read_b64_tr_b16 v[160:161], v35 offset:8192\n" \
"v_mfma_f32_16x16x32_bf16 a[88:91], v[108:111], v[60:63], a[88:91]\n" \
"v_mfma_f32_16x16x32_bf16 a[120:123], v[108:111], v[64:67], a[120:123]\n" \
"ds_read_b64_tr_b16 v[162:163], v35 offset:9216\n" \
"v_mfma_f32_16x16x32_bf16 a[84:87], v[104:107], v[60:63], a[84:87]\n" \
"v_mfma_f32_16x16x32_bf16 a[116:119], v[104:107], v[64:67], a[116:119]\n" \
"ds_read_b64_tr_b16 v[164:165], v36 offset:8192\n" \
"v_mfma_f32_16x16x32_bf16 a[80:83], v[100:103], v[60:63], a[80:83]\n" \
"v_mfma_f32_16x16x32_bf16 a[112:115], v[100:103], v[64:67], a[112:115]\n" \
"ds_read_b64_tr_b16 v[166:167], v36 offset:9216\n" \
"v_mfma_f32_16x16x32_bf16 a[76:79], v[96:99], v[60:63], a[76:79]\n" \
"v_mfma_f32_16x16x32_bf16 a[108:111], v[96:99], v[64:67], a[108:111]\n" \
"v_mfma_f32_16x16x32_bf16 a[72:75], v[92:95], v[60:63], a[72:75]\n" \
"ds_read_b64_tr_b16 v[168:169], v37 offset:8192\n" \
"v_mfma_f32_16x16x32_bf16 a[104:107], v[92:95], v[64:67], a[104:107]\n" \
"v_mfma_f32_16x16x32_bf16 a[68:71], v[88:91], v[60:63], a[68:71]\n" \
"ds_read_b64_tr_b16 v[170:171], v37 offset:9216\n" \
"v_mfma_f32_16x16x32_bf16 a[100:103], v[88:91], v[64:67], a[100:103]\n" \
"v_mfma_f32_16x16x32_bf16 a[64:67], v[84:87], v[60:63], a[64:67]\n" \
"ds_read_b64_tr_b16 v[172:173], v38 offset:8192\n" \
"v_mfma_f32_16x16x32_bf16 a[96:99], v[84:87], v[64:67], a[96:99]\n" \
"s_waitcnt lgkmcnt(15)\n" \
"s_nop 0\n" \
"v_mfma_f32_16x16x32_bf16 a[128:131], v[84:87], v[68:71], a[128:131]\n" \
"ds_read_b64_tr_b16 v[174:175], v38 offset:9216\n" \
"v_mfma_f32_16x16x32_bf16 a[160:163], v[84:87], v[72:75], a[160:163]\n" \
"v_mfma_f32_16x16x32_bf16 a[132:135], v[88:91], v[68:71], a[132:135]\n" \
"ds_read_b64_tr_b16 v[176:177], v39 offset:8192\n" \
"v_mfma_f32_16x16x32_bf16 a[164:167], v[88:91], v[72:75], a[164:167]\n" \
"v_mfma_f32_16x16x32_bf16 a[136:139], v[92:95], v[68:71], a[136:139]\n" \
"ds_read_b64_tr_b16 v[178:179], v39 offset:9216\n" \
"v_mfma_f32_16x16x32_bf16 a[168:171], v[92:95], v[72:75], a[168:171]\n" \
"v_mfma_f32_16x16x32_bf16 a[140:143], v[96:99], v[68:71], a[140:143]\n" \
"v_bfe_u32 v44, v20, 2, 5\n" \
"v_mfma_f32_16x16x32_bf16 a[172:175], v[96:99], v[72:75], a[172:175]\n" \
"buffer_load_dword v44, v44, s[80:83], s38 offen offset:128 sc1\n" \
"v_mfma_f32_16x16x32_bf16 a[144:147], v[100:103], v[68:71], a[144:147]\n" \
"ds_read_b128 v[124:127], v29 offset:20480\n" \
"v_mfma_f32_16x16x32_bf16 a[176:179], v[100:103], v[72:75], a[176:179]\n" \
"ds_read_b128 v[128:131], v29 offset:22528\n" \
"v_mfma_f32_16x16x32_bf16 a[148:151], v[104:107], v[68:71], a[148:151]\n" \
"v_mfma_f32_16x16x32_bf16 a[180:183], v[104:107], v[72:75], a[180:183]\n" \
"ds_read_b128 v[132:135], v29 offset:0\n" \
"v_mfma_f32_16x16x32_bf16 a[152:155], v[108:111], v[68:71], a[152:155]\n" \
"v_mfma_f32_16x16x32_bf16 a[184:187], v[108:111], v[72:75], a[184:187]\n" \
"ds_read_b128 v[136:139], v29 offset:2048\n" \
"v_mfma_f32_16x16x32_bf16 a[156:159], v[112:115], v[68:71], a[156:159]\n" \
"v_mfma_f32_16x16x32_bf16 a[188:191], v[112:115], v[72:75], a[188:191]\n" \
"ds_read_b128 v[140:143], v29 offset:4096\n" \
"v_mfma_f32_16x16x32_bf16 a[220:223], v[112:115], v[76:79], a[220:223]\n" \
"v_mfma_f32_16x16x32_bf16 a[252:255], v[112:115], v[80:83], a[252:255]\n" \
"ds_read_b128 v[144:147], v29 offset:6144\n" \
"v_mfma_f32_16x16x32_bf16 a[216:219], v[108:111], v[76:79], a[216:219]\n" \
"v_mfma_f32_16x16x32_bf16 a[248:251], v[108:111], v[80:83], a[248:251]\n" \
"v_mfma_f32_16x16x32_bf16 a[212:215], v[104:107], v[76:79], a[212:215]\n" \
"v_mfma_f32_16x16x32_bf16 a[244:247], v[104:107], v[80:83], a[244:247]\n" \
"v_mfma_f32_16x16x32_bf16 a[208:211], v[100:103], v[76:79], a[208:211]\n" \
"v_mfma_f32_16x16x32_bf16 a[240:243], v[100:103], v[80:83], a[240:243]\n" \
"v_mfma_f32_16x16x32_bf16 a[204:207], v[96:99], v[76:79], a[204:207]\n" \
"v_mfma_f32_16x16x32_bf16 a[236:239], v[96:99], v[80:83], a[236:239]\n" \
"v_mfma_f32_16x16x32_bf16 a[200:203], v[92:95], v[76:79], a[200:203]\n" \
"v_mfma_f32_16x16x32_bf16 a[232:235], v[92:95], v[80:83], a[232:235]\n" \
"v_mfma_f32_16x16x32_bf16 a[196:199], v[88:91], v[76:79], a[196:199]\n" \
"v_mfma_f32_16x16x32_bf16 a[228:231], v[88:91], v[80:83], a[228:231]\n" \
"v_mfma_f32_16x16x32_bf16 a[192:195], v[84:87], v[76:79], a[192:195]\n" \
"v_mfma_f32_16x16x32_bf16 a[224:227], v[84:87], v[80:83], a[224:227]\n" \
"s_waitcnt vmcnt(0) lgkmcnt(0)\n" \
"s_barrier\n" \
"v_cmp_ne_u32_e32 vcc, s62, v44\n" \
"s_nop 4\n" \
"s_cbranch_vccnz L_poll60_%=\n" \
"L_pollr60_%=:\n" \
"s_mov_b32 s68, s12\n" \
"s_and_b32 s69, s13, 0xffff\n" \
"s_mov_b32 s70, 0x480000\n" \
"s_mov_b32 s71, 0x20000\n" \
"v_mfma_f32_16x16x32_bf16 a[0:3], v[148:151], v[116:119], a[0:3]\n" \
"s_add_u32 m0, s40, 0x0\n" \
"s_add_u32 s85, s39, 0x0\n" \
"v_mfma_f32_16x16x32_bf16 a[32:35], v[148:151], v[120:123], a[32:35]\n" \
"buffer_load_dwordx4 v20, s[80:83], s85 offen sc1 lds\n" \
"v_mfma_f32_16x16x32_bf16 a[4:7], v[152:155], v[116:119], a[4:7]\n" \
"ds_read_b64_tr_b16 v[84:85], v32 offset:32768\n" \
"v_mfma_f32_16x16x32_bf16 a[36:39], v[152:155], v[120:123], a[36:39]\n" \
"ds_read_b64_tr_b16 v[86:87], v32 offset:33792\n" \
"v_mfma_f32_16x16x32_bf16 a[8:11], v[156:159], v[116:119], a[8:11]\n" \
"s_add_u32 m0, s40, 0x1000\n" \
"s_add_u32 s84, s39, 0x1000\n" \
"v_mfma_f32_16x16x32_bf16 a[40:43], v[156:159], v[120:123], a[40:43]\n" \
"buffer_load_dwordx4 v20, s[80:83], s84 offen sc1 lds\n" \
"v_mfma_f32_16x16x32_bf16 a[12:15], v[160:163], v[116:119], a[12:15]\n" \
"ds_read_b128 v[52:55], v28 offset:49152\n" \
"v_mfma_f32_16x16x32_bf16 a[44:47], v[160:163], v[120:123], a[44:47]\n" \
"ds_read_b128 v[56:59], v28 offset:51200\n" \
"v_mfma_f32_16x16x32_bf16 a[16:19], v[164:167], v[116:119], a[16:19]\n" \
"s_add_u32 m0, s40, 0x3000\n" \
"s_mov_b32 s85, 0x0\n" \
"v_mfma_f32_16x16x32_bf16 a[48:51], v[164:167], v[120:123], a[48:51]\n" \
"buffer_load_dwordx4 v21, s[76:79], s85 offen lds\n" \
"v_mfma_f32_16x16x32_bf16 a[20:23], v[168:171], v[116:119], a[20:23]\n" \
"ds_read_b64_tr_b16 v[88:89], v33 offset:32768\n" \
"v_mfma_f32_16x16x32_bf16 a[52:55], v[168:171], v[120:123], a[52:55]\n" \
"ds_read_b64_tr_b16 v[90:91], v33 offset:33792\n" \
"v_mfma_f32_16x16x32_bf16 a[24:27], v[172:175], v[116:119], a[24:27]\n" \
"s_add_u32 m0, s30, 0x0\n" \
"s_mov_b32 s84, 0x0\n" \
"s_nop 0\n" \
"v_mfma_f32_16x16x32_bf16 a[56:59], v[172:175], v[120:123], a[56:59]\n" \
"buffer_load_dwordx4 v24, s[76:79], s84 offen lds\n" \
"v_mfma_f32_16x16x32_bf16 a[28:31], v[176:179], v[116:119], a[28:31]\n" \
"ds_read_b64_tr_b16 v[92:93], v34 offset:32768\n" \
"v_mfma_f32_16x16x32_bf16 a[60:63], v[176:179], v[120:123], a[60:63]\n" \
"ds_read_b64_tr_b16 v[94:95], v34 offset:33792\n" \
"v_mfma_f32_16x16x32_bf16 a[92:95], v[176:179], v[124:127], a[92:95]\n" \
"s_add_u32 m0, s30, 0x400\n" \
"s_mov_b32 s85, 0x2400\n" \
"v_mfma_f32_16x16x32_bf16 a[124:127], v[176:179], v[128:131], a[124:127]\n" \
"buffer_load_dwordx4 v25, s[76:79], s85 offen lds\n" \
"v_mfma_f32_16x16x32_bf16 a[88:91], v[172:175], v[124:127], a[88:91]\n" \
"ds_read_b64_tr_b16 v[96:97], v35 offset:32768\n" \
"v_mfma_f32_16x16x32_bf16 a[120:123], v[172:175], v[128:131], a[120:123]\n" \
"ds_read_b64_tr_b16 v[98:99], v35 offset:33792\n" \
"v_mfma_f32_16x16x32_bf16 a[84:87], v[168:171], v[124:127], a[84:87]\n" \
"s_add_u32 m0, s31, 0x0\n" \
"s_mov_b32 s84, 0x0\n" \
"v_mfma_f32_16x16x32_bf16 a[116:119], v[168:171], v[128:131], a[116:119]\n" \
"buffer_load_dwordx4 v26, s[68:71], s84 offen lds\n" \
"v_mfma_f32_16x16x32_bf16 a[80:83], v[164:167], v[124:127], a[80:83]\n" \
"ds_read_b64_tr_b16 v[100:101], v36 offset:32768\n" \
"v_mfma_f32_16x16x32_bf16 a[112:115], v[164:167], v[128:131], a[112:115]\n" \
"ds_read_b64_tr_b16 v[102:103], v36 offset:33792\n" \
"v_mfma_f32_16x16x32_bf16 a[76:79], v[160:163], v[124:127], a[76:79]\n" \
"s_add_u32 m0, s30, 0x800\n" \
"s_mov_b32 s85, 0x4800\n" \
"v_mfma_f32_16x16x32_bf16 a[108:111], v[160:163], v[128:131], a[108:111]\n" \
"buffer_load_dwordx4 v24, s[76:79], s85 offen lds\n" \
"v_mfma_f32_16x16x32_bf16 a[72:75], v[156:159], v[124:127], a[72:75]\n" \
"ds_read_b64_tr_b16 v[104:105], v37 offset:32768\n" \
"v_mfma_f32_16x16x32_bf16 a[104:107], v[156:159], v[128:131], a[104:107]\n" \
"v_mfma_f32_16x16x32_bf16 a[68:71], v[152:155], v[124:127], a[68:71]\n" \
"s_add_u32 m0, s30, 0xc00\n" \
"s_mov_b32 s84, 0x6c00\n" \
"v_mfma_f32_16x16x32_bf16 a[100:103], v[152:155], v[128:131], a[100:103]\n" \
"buffer_load_dwordx4 v25, s[76:79], s84 offen lds\n" \
"v_mfma_f32_16x16x32_bf16 a[64:67], v[148:151], v[124:127], a[64:67]\n" \
"ds_read_b64_tr_b16 v[106:107], v37 offset:33792\n" \
"v_mfma_f32_16x16x32_bf16 a[96:99], v[148:151], v[128:131], a[96:99]\n" \
"ds_read_b64_tr_b16 v[108:109], v38 offset:32768\n" \
"v_mfma_f32_16x16x32_bf16 a[128:131], v[148:151], v[132:135], a[128:131]\n" \
"s_add_u32 m0, s31, 0x400\n" \
"s_mov_b32 s85, 0x8000\n" \
"v_mfma_f32_16x16x32_bf16 a[160:163], v[148:151], v[136:139], a[160:163]\n" \
"buffer_load_dwordx4 v26, s[68:71], s85 offen lds\n" \
"v_mfma_f32_16x16x32_bf16 a[132:135], v[152:155], v[132:135], a[132:135]\n" \
"ds_read_b64_tr_b16 v[110:111], v38 offset:33792\n" \
"v_mfma_f32_16x16x32_bf16 a[164:167], v[152:155], v[136:139], a[164:167]\n" \
"ds_read_b64_tr_b16 v[112:113], v39 offset:32768\n" \
"v_mfma_f32_16x16x32_bf16 a[136:139], v[156:159], v[132:135], a[136:139]\n" \
"s_add_u32 m0, s30, 0x1000\n" \
"s_mov_b32 s84, 0x9000\n" \
"v_mfma_f32_16x16x32_bf16 a[168:171], v[156:159], v[136:139], a[168:171]\n" \
"buffer_load_dwordx4 v24, s[76:79], s84 offen lds\n" \
"v_mfma_f32_16x16x32_bf16 a[140:143], v[160:163], v[132:135], a[140:143]\n" \
"ds_read_b64_tr_b16 v[114:115], v39 offset:33792\n" \
"v_mfma_f32_16x16x32_bf16 a[172:175], v[160:163], v[136:139], a[172:175]\n" \
"ds_read_b128 v[60:63], v28 offset:53248\n" \
"v_mfma_f32_16x16x32_bf16 a[144:147], v[164:167], v[132:135], a[144:147]\n" \
"s_add_u32 m0, s30, 0x1400\n" \
"s_mov_b32 s85, 0xb400\n" \
"v_mfma_f32_16x16x32_bf16 a[176:179], v[164:167], v[136:139], a[176:179]\n" \
"buffer_load_dwordx4 v25, s[76:79], s85 offen lds\n" \
"v_mfma_f32_16x16x32_bf16 a[148:151], v[168:171], v[132:135], a[148:151]\n" \
"ds_read_b128 v[64:67], v28 offset:55296\n" \
"v_mfma_f32_16x16x32_bf16 a[180:183], v[168:171], v[136:139], a[180:183]\n" \
"ds_read_b128 v[68:71], v28 offset:32768\n" \
"v_mfma_f32_16x16x32_bf16 a[152:155], v[172:175], v[132:135], a[152:155]\n" \
"s_add_u32 m0, s31, 0x800\n" \
"s_mov_b32 s84, 0x10000\n" \
"v_mfma_f32_16x16x32_bf16 a[184:187], v[172:175], v[136:139], a[184:187]\n" \
"buffer_load_dwordx4 v27, s[68:71], s84 offen lds\n" \
"v_mfma_f32_16x16x32_bf16 a[156:159], v[176:179], v[132:135], a[156:159]\n" \
"ds_read_b128 v[72:75], v28 offset:34816\n" \
"v_mfma_f32_16x16x32_bf16 a[188:191], v[176:179], v[136:139], a[188:191]\n" \
"ds_read_b128 v[76:79], v28 offset:36864\n" \
"v_mfma_f32_16x16x32_bf16 a[220:223], v[176:179], v[140:143], a[220:223]\n" \
"v_mfma_f32_16x16x32_bf16 a[252:255], v[176:179], v[144:147], a[252:255]\n" \
"ds_read_b128 v[80:83], v28 offset:38912\n" \
"v_mfma_f32_16x16x32_bf16 a[216:219], v[172:175], v[140:143], a[216:219]\n" \
"v_mfma_f32_16x16x32_bf16 a[248:251], v[172:175], v[144:147], a[248:251]\n" \
"v_mfma_f32_16x16x32_bf16 a[212:215], v[168:171], v[140:143], a[212:215]\n" \
"v_mfma_f32_16x16x32_bf16 a[244:247], v[168:171], v[144:147], a[244:247]\n" \
"v_mfma_f32_16x16x32_bf16 a[208:211], v[164:167], v[140:143], a[208:211]\n" \
"v_mfma_f32_16x16x32_bf16 a[240:243], v[164:167], v[144:147], a[240:243]\n" \
"v_mfma_f32_16x16x32_bf16 a[204:207], v[160:163], v[140:143], a[204:207]\n" \
"v_mfma_f32_16x16x32_bf16 a[236:239], v[160:163], v[144:147], a[236:239]\n" \
"v_mfma_f32_16x16x32_bf16 a[200:203], v[156:159], v[140:143], a[200:203]\n" \
"v_mfma_f32_16x16x32_bf16 a[232:235], v[156:159], v[144:147], a[232:235]\n" \
"v_mfma_f32_16x16x32_bf16 a[196:199], v[152:155], v[140:143], a[196:199]\n" \
"v_mfma_f32_16x16x32_bf16 a[228:231], v[152:155], v[144:147], a[228:231]\n" \
"v_mfma_f32_16x16x32_bf16 a[192:195], v[148:151], v[140:143], a[192:195]\n" \
"v_mfma_f32_16x16x32_bf16 a[224:227], v[148:151], v[144:147], a[224:227]\n" \
"s_waitcnt lgkmcnt(15)\n" \
"s_nop 0\n" \
"v_mfma_f32_16x16x32_bf16 a[0:3], v[84:87], v[52:55], a[0:3]\n" \
"s_add_u32 m0, s30, 0x1800\n" \
"s_mov_b32 s84, 0xd800\n" \
"v_mfma_f32_16x16x32_bf16 a[32:35], v[84:87], v[56:59], a[32:35]\n" \
"buffer_load_dwordx4 v24, s[76:79], s84 offen lds\n" \
"v_mfma_f32_16x16x32_bf16 a[4:7], v[88:91], v[52:55], a[4:7]\n" \
"ds_read_b64_tr_b16 v[148:149], v32 offset:40960\n" \
"v_mfma_f32_16x16x32_bf16 a[36:39], v[88:91], v[56:59], a[36:39]\n" \
"s_add_u32 m0, s30, 0x1c00\n" \
"s_mov_b32 s85, 0xfc00\n" \
"v_mfma_f32_16x16x32_bf16 a[8:11], v[92:95], v[52:55], a[8:11]\n" \
"buffer_load_dwordx4 v25, s[76:79], s85 offen lds\n" \
"v_mfma_f32_16x16x32_bf16 a[40:43], v[92:95], v[56:59], a[40:43]\n" \
"ds_read_b64_tr_b16 v[150:151], v32 offset:41984\n" \
"s_waitcnt lgkmcnt(15)\n" \
"v_mfma_f32_16x16x32_bf16 a[12:15], v[96:99], v[52:55], a[12:15]\n" \
"s_add_u32 m0, s31, 0xc00\n" \
"s_mov_b32 s84, 0x18000\n" \
"v_mfma_f32_16x16x32_bf16 a[44:47], v[96:99], v[56:59], a[44:47]\n" \
"buffer_load_dwordx4 v27, s[68:71], s84 offen lds\n" \
"s_waitcnt lgkmcnt(14)\n" \
"v_mfma_f32_16x16x32_bf16 a[16:19], v[100:103], v[52:55], a[16:19]\n" \
"ds_read_b128 v[116:119], v29 offset:49152\n" \
"v_mfma_f32_16x16x32_bf16 a[48:51], v[100:103], v[56:59], a[48:51]\n" \
"ds_read_b128 v[120:123], v29 offset:51200\n" \
"s_waitcnt lgkmcnt(14)\n" \
"v_mfma_f32_16x16x32_bf16 a[20:23], v[104:107], v[52:55], a[20:23]\n" \
"ds_read_b64_tr_b16 v[152:153], v33 offset:40960\n" \
"v_mfma_f32_16x16x32_bf16 a[52:55], v[104:107], v[56:59], a[52:55]\n" \
"ds_read_b64_tr_b16 v[154:155], v33 offset:41984\n" \
"s_waitcnt lgkmcnt(14)\n" \
"v_mfma_f32_16x16x32_bf16 a[24:27], v[108:111], v[52:55], a[24:27]\n" \
"v_mfma_f32_16x16x32_bf16 a[56:59], v[108:111], v[56:59], a[56:59]\n" \
"ds_read_b64_tr_b16 v[156:157], v34 offset:40960\n" \
"s_waitcnt lgkmcnt(13)\n" \
"v_mfma_f32_16x16x32_bf16 a[28:31], v[112:115], v[52:55], a[28:31]\n" \
"v_mfma_f32_16x16x32_bf16 a[60:63], v[112:115], v[56:59], a[60:63]\n" \
"ds_read_b64_tr_b16 v[158:159], v34 offset:41984\n" \
"s_waitcnt lgkmcnt(13)\n" \
"v_mfma_f32_16x16x32_bf16 a[92:95], v[112:115], v[60:63], a[92:95]\n" \
"s_waitcnt lgkmcnt(12)\n" \
"v_mfma_f32_16x16x32_bf16 a[124:127], v[112:115], v[64:67], a[124:127]\n" \
"ds_read_b64_tr_b16 v[160:161], v35 offset:40960\n" \
"v_mfma_f32_16x16x32_bf16 a[88:91], v[108:111], v[60:63], a[88:91]\n" \
"v_lshlrev_b32_e32 v45, 3, v20\n" \
"v_add_u32_e64 v45, s31, v45\n" \
"v_mfma_f32_16x16x32_bf16 a[120:123], v[108:111], v[64:67], a[120:123]\n" \
"s_getpc_b64 s[86:87]\n" \
"s_sub_u32 s86, s86, 0xf800\n" \
"s_subb_u32 s87, s87, 0\n" \
"s_mov_b32 m0, s32\n" \
"s_nop 0\n" \
"v_mfma_f32_16x16x32_bf16 a[84:87], v[104:107], v[60:63], a[84:87]\n" \
"global_load_lds_dword v45, s[86:87]\n" \
"v_mfma_f32_16x16x32_bf16 a[116:119], v[104:107], v[64:67], a[116:119]\n" \
"ds_read_b64_tr_b16 v[162:163], v35 offset:41984\n" \
"v_mfma_f32_16x16x32_bf16 a[80:83], v[100:103], v[60:63], a[80:83]\n" \
"ds_read_b64_tr_b16 v[164:165], v36 offset:40960\n" \
"v_mfma_f32_16x16x32_bf16 a[112:115], v[100:103], v[64:67], a[112:115]\n" \
"ds_read_b64_tr_b16 v[166:167], v36 offset:41984\n" \
"v_mfma_f32_16x16x32_bf16 a[76:79], v[96:99], v[60:63], a[76:79]\n" \
"v_mfma_f32_16x16x32_bf16 a[108:111], v[96:99], v[64:67], a[108:111]\n" \
"v_mfma_f32_16x16x32_bf16 a[72:75], v[92:95], v[60:63], a[72:75]\n" \
"ds_read_b64_tr_b16 v[168:169], v37 offset:40960\n" \
"v_mfma_f32_16x16x32_bf16 a[104:107], v[92:95], v[64:67], a[104:107]\n" \
"v_mfma_f32_16x16x32_bf16 a[68:71], v[88:91], v[60:63], a[68:71]\n" \
"ds_read_b64_tr_b16 v[170:171], v37 offset:41984\n" \
"v_mfma_f32_16x16x32_bf16 a[100:103], v[88:91], v[64:67], a[100:103]\n" \
"v_mfma_f32_16x16x32_bf16 a[64:67], v[84:87], v[60:63], a[64:67]\n" \
"ds_read_b64_tr_b16 v[172:173], v38 offset:40960\n" \
"v_mfma_f32_16x16x32_bf16 a[96:99], v[84:87], v[64:67], a[96:99]\n" \
"s_waitcnt lgkmcnt(15)\n" \
"s_nop 0\n" \
"v_mfma_f32_16x16x32_bf16 a[128:131], v[84:87], v[68:71], a[128:131]\n" \
"ds_read_b64_tr_b16 v[174:175], v38 offset:41984\n" \
"v_mfma_f32_16x16x32_bf16 a[160:163], v[84:87], v[72:75], a[160:163]\n" \
"v_mfma_f32_16x16x32_bf16 a[132:135], v[88:91], v[68:71], a[132:135]\n" \
"ds_read_b64_tr_b16 v[176:177], v39 offset:40960\n" \
"v_mfma_f32_16x16x32_bf16 a[164:167], v[88:91], v[72:75], a[164:167]\n" \
"v_mfma_f32_16x16x32_bf16 a[136:139], v[92:95], v[68:71], a[136:139]\n" \
"ds_read_b64_tr_b16 v[178:179], v39 offset:41984\n" \
"v_mfma_f32_16x16x32_bf16 a[168:171], v[92:95], v[72:75], a[168:171]\n" \
"v_mfma_f32_16x16x32_bf16 a[140:143], v[96:99], v[68:71], a[140:143]\n" \
"v_mfma_f32_16x16x32_bf16 a[172:175], v[96:99], v[72:75], a[172:175]\n" \
"ds_read_b128 v[124:127], v29 offset:53248\n" \
"v_mfma_f32_16x16x32_bf16 a[144:147], v[100:103], v[68:71], a[144:147]\n" \
"v_mfma_f32_16x16x32_bf16 a[176:179], v[100:103], v[72:75], a[176:179]\n" \
"ds_read_b128 v[128:131], v29 offset:55296\n" \
"v_mfma_f32_16x16x32_bf16 a[148:151], v[104:107], v[68:71], a[148:151]\n" \
"v_mfma_f32_16x16x32_bf16 a[180:183], v[104:107], v[72:75], a[180:183]\n" \
"ds_read_b128 v[132:135], v29 offset:32768\n" \
"v_mfma_f32_16x16x32_bf16 a[152:155], v[108:111], v[68:71], a[152:155]\n" \
"v_mfma_f32_16x16x32_bf16 a[184:187], v[108:111], v[72:75], a[184:187]\n" \
"ds_read_b128 v[136:139], v29 offset:34816\n" \
"v_mfma_f32_16x16x32_bf16 a[156:159], v[112:115], v[68:71], a[156:159]\n" \
"v_mfma_f32_16x16x32_bf16 a[188:191], v[112:115], v[72:75], a[188:191]\n" \
"ds_read_b128 v[140:143], v29 offset:36864\n" \
"v_mfma_f32_16x16x32_bf16 a[220:223], v[112:115], v[76:79], a[220:223]\n" \
"v_mfma_f32_16x16x32_bf16 a[252:255], v[112:115], v[80:83], a[252:255]\n" \
"ds_read_b128 v[144:147], v29 offset:38912\n" \
"v_mfma_f32_16x16x32_bf16 a[216:219], v[108:111], v[76:79], a[216:219]\n" \
"v_mfma_f32_16x16x32_bf16 a[248:251], v[108:111], v[80:83], a[248:251]\n" \
"v_mfma_f32_16x16x32_bf16 a[212:215], v[104:107], v[76:79], a[212:215]\n" \
"v_mfma_f32_16x16x32_bf16 a[244:247], v[104:107], v[80:83], a[244:247]\n" \
"v_mfma_f32_16x16x32_bf16 a[208:211], v[100:103], v[76:79], a[208:211]\n" \
"v_mfma_f32_16x16x32_bf16 a[240:243], v[100:103], v[80:83], a[240:243]\n" \
"v_mfma_f32_16x16x32_bf16 a[204:207], v[96:99], v[76:79], a[204:207]\n" \
"v_mfma_f32_16x16x32_bf16 a[236:239], v[96:99], v[80:83], a[236:239]\n" \
"v_mfma_f32_16x16x32_bf16 a[200:203], v[92:95], v[76:79], a[200:203]\n" \
"v_mfma_f32_16x16x32_bf16 a[232:235], v[92:95], v[80:83], a[232:235]\n" \
"v_mfma_f32_16x16x32_bf16 a[196:199], v[88:91], v[76:79], a[196:199]\n" \
"v_mfma_f32_16x16x32_bf16 a[228:231], v[88:91], v[80:83], a[228:231]\n" \
"v_mfma_f32_16x16x32_bf16 a[192:195], v[84:87], v[76:79], a[192:195]\n" \
"v_mfma_f32_16x16x32_bf16 a[224:227], v[84:87], v[80:83], a[224:227]\n" \
"s_waitcnt vmcnt(1) lgkmcnt(0)\n" \
"s_barrier\n" \
"v_add_u32_e32 v22, s44, v28\n" \
"v_xor_b32_e32 v36, 0x0, v190\n" \
"v_add_u32_e32 v23, s44, v29\n" \
"v_xor_b32_e32 v37, 0x20, v190\n" \
"v_add_u32_e32 v22, s44, v28\n" \
"v_xor_b32_e32 v38, 0x40, v190\n" \
"v_add_u32_e64 v23, s44, v29\n" \
"v_xor_b32_e32 v39, 0x60, v190\n" \
"v_add_u32_e32 v28, s43, v28\n" \
"v_add_u32_e32 v29, s43, v29\n" \
"v_mfma_f32_16x16x32_bf16 a[0:3], v[148:151], v[116:119], a[0:3]\n" \
"s_add_u32 m0, s40, 0x8000\n" \
"s_add_u32 s85, s39, 0x2000\n" \
"v_mfma_f32_16x16x32_bf16 a[32:35], v[148:151], v[120:123], a[32:35]\n" \
"buffer_load_dwordx4 v20, s[80:83], s85 offen sc1 lds\n" \
"v_mfma_f32_16x16x32_bf16 a[4:7], v[152:155], v[116:119], a[4:7]\n" \
"ds_read_b64_tr_b16 v[104:105], v37 offset:0\n" \
"v_mfma_f32_16x16x32_bf16 a[36:39], v[152:155], v[120:123], a[36:39]\n" \
"ds_read_b64_tr_b16 v[106:107], v37 offset:512\n" \
"v_mfma_f32_16x16x32_bf16 a[8:11], v[156:159], v[116:119], a[8:11]\n" \
"s_add_u32 m0, s40, 0x9000\n" \
"s_add_u32 s84, s39, 0x3000\n" \
"v_mfma_f32_16x16x32_bf16 a[40:43], v[156:159], v[120:123], a[40:43]\n" \
"buffer_load_dwordx4 v20, s[80:83], s84 offen sc1 lds\n" \
"v_accvgpr_read_b32 v184, a0\n" \
"v_accvgpr_read_b32 v185, a1\n" \
"v_mfma_f32_16x16x32_bf16 a[12:15], v[160:163], v[116:119], a[12:15]\n" \
"ds_read_b64_tr_b16 v[108:109], v38 offset:0\n" \
"v_accvgpr_read_b32 v186, a2\n" \
"v_accvgpr_read_b32 v187, a3\n" \
"v_accvgpr_read_b32 v180, a4\n" \
"v_mfma_f32_16x16x32_bf16 a[44:47], v[160:163], v[120:123], a[44:47]\n" \
"ds_read_b64_tr_b16 v[110:111], v38 offset:512\n" \
"v_accvgpr_read_b32 v181, a5\n" \
"v_accvgpr_read_b32 v182, a6\n" \
"v_accvgpr_read_b32 v183, a7\n" \
"v_mfma_f32_16x16x32_bf16 a[16:19], v[164:167], v[116:119], a[16:19]\n" \
"s_add_u32 m0, s40, 0xb000\n" \
"s_mov_b32 s85, 0x80\n" \
"v_cvt_pk_bf16_f32 v184, v184, v185\n" \
"v_cvt_pk_bf16_f32 v185, v186, v187\n" \
"v_mfma_f32_16x16x32_bf16 a[48:51], v[164:167], v[120:123], a[48:51]\n" \
"buffer_load_dwordx4 v21, s[76:79], s85 offen lds\n" \
"v_cvt_pk_bf16_f32 v186, v180, v181\n" \
"v_cvt_pk_bf16_f32 v187, v182, v183\n" \
"v_mfma_f32_16x16x32_bf16 a[20:23], v[168:171], v[116:119], a[20:23]\n" \
"ds_read_b64_tr_b16 v[112:113], v39 offset:0\n" \
"v_accvgpr_read_b32 v188, a8\n" \
"v_accvgpr_read_b32 v189, a9\n" \
"v_accvgpr_read_b32 v190, a10\n" \
"v_mfma_f32_16x16x32_bf16 a[52:55], v[168:171], v[120:123], a[52:55]\n" \
"ds_read_b64_tr_b16 v[114:115], v39 offset:512\n" \
"v_accvgpr_read_b32 v191, a11\n" \
"v_accvgpr_read_b32 v180, a12\n" \
"v_accvgpr_read_b32 v181, a13\n" \
"v_mfma_f32_16x16x32_bf16 a[24:27], v[172:175], v[116:119], a[24:27]\n" \
"s_add_u32 m0, s30, 0x8000\n" \
"s_mov_b32 s84, 0x80\n" \
"v_accvgpr_read_b32 v182, a14\n" \
"v_accvgpr_read_b32 v183, a15\n" \
"v_mfma_f32_16x16x32_bf16 a[56:59], v[172:175], v[120:123], a[56:59]\n" \
"buffer_load_dwordx4 v24, s[76:79], s84 offen lds\n" \
"v_cvt_pk_bf16_f32 v188, v188, v189\n" \
"v_cvt_pk_bf16_f32 v189, v190, v191\n" \
"v_mfma_f32_16x16x32_bf16 a[28:31], v[176:179], v[116:119], a[28:31]\n" \
"ds_read_b64_tr_b16 v[84:85], v32 offset:0\n" \
"v_cvt_pk_bf16_f32 v190, v180, v181\n" \
"v_cvt_pk_bf16_f32 v191, v182, v183\n" \
"v_accvgpr_read_b32 v192, a16\n" \
"v_mfma_f32_16x16x32_bf16 a[60:63], v[176:179], v[120:123], a[60:63]\n" \
"ds_read_b64_tr_b16 v[86:87], v32 offset:1024\n" \
"v_accvgpr_read_b32 v193, a17\n" \
"v_accvgpr_read_b32 v194, a18\n" \
"v_accvgpr_read_b32 v195, a19\n" \
"v_mfma_f32_16x16x32_bf16 a[92:95], v[176:179], v[124:127], a[92:95]\n" \
"s_add_u32 m0, s30, 0x8400\n" \
"s_mov_b32 s85, 0x2480\n" \
"v_accvgpr_read_b32 v180, a20\n" \
"v_accvgpr_read_b32 v181, a21\n" \
"v_mfma_f32_16x16x32_bf16 a[124:127], v[176:179], v[128:131], a[124:127]\n" \
"buffer_load_dwordx4 v25, s[76:79], s85 offen lds\n" \
"v_accvgpr_read_b32 v182, a22\n" \
"v_accvgpr_read_b32 v183, a23\n" \
"v_mfma_f32_16x16x32_bf16 a[88:91], v[172:175], v[124:127], a[88:91]\n" \
"ds_read_b128 v[248:251], v22 offset:0\n" \
"v_cvt_pk_bf16_f32 v192, v192, v193\n" \
"v_cvt_pk_bf16_f32 v193, v194, v195\n" \
"v_cvt_pk_bf16_f32 v194, v180, v181\n" \
"v_mfma_f32_16x16x32_bf16 a[120:123], v[172:175], v[128:131], a[120:123]\n" \
"ds_read_b64_tr_b16 v[88:89], v33 offset:0\n" \
"v_cvt_pk_bf16_f32 v195, v182, v183\n" \
"v_accvgpr_read_b32 v196, a24\n" \
"v_accvgpr_read_b32 v197, a25\n" \
"v_mfma_f32_16x16x32_bf16 a[84:87], v[168:171], v[124:127], a[84:87]\n" \
"s_add_u32 m0, s31, 0x8000\n" \
"s_mov_b32 s84, 0x80000\n" \
"v_accvgpr_read_b32 v198, a26\n" \
"v_accvgpr_read_b32 v199, a27\n" \
"v_mfma_f32_16x16x32_bf16 a[116:119], v[168:171], v[128:131], a[116:119]\n" \
"buffer_load_dwordx4 v26, s[68:71], s84 offen lds\n" \
"v_accvgpr_read_b32 v180, a28\n" \
"v_accvgpr_read_b32 v181, a29\n" \
"v_mfma_f32_16x16x32_bf16 a[80:83], v[164:167], v[124:127], a[80:83]\n" \
"ds_read_b64_tr_b16 v[90:91], v33 offset:1024\n" \
"v_accvgpr_read_b32 v182, a30\n" \
"v_accvgpr_read_b32 v183, a31\n" \
"v_cvt_pk_bf16_f32 v196, v196, v197\n" \
"v_mfma_f32_16x16x32_bf16 a[112:115], v[164:167], v[128:131], a[112:115]\n" \
"ds_read_b64_tr_b16 v[92:93], v34 offset:0\n" \
"v_cvt_pk_bf16_f32 v197, v198, v199\n" \
"v_cvt_pk_bf16_f32 v198, v180, v181\n" \
"v_cvt_pk_bf16_f32 v199, v182, v183\n" \
"v_mfma_f32_16x16x32_bf16 a[76:79], v[160:163], v[124:127], a[76:79]\n" \
"s_add_u32 m0, s30, 0x8800\n" \
"s_mov_b32 s85, 0x4880\n" \
"v_accvgpr_read_b32 v200, a32\n" \
"v_accvgpr_read_b32 v201, a33\n" \
"v_mfma_f32_16x16x32_bf16 a[108:111], v[160:163], v[128:131], a[108:111]\n" \
"buffer_load_dwordx4 v24, s[76:79], s85 offen lds\n" \
"v_accvgpr_read_b32 v202, a34\n" \
"v_accvgpr_read_b32 v203, a35\n" \
"v_mfma_f32_16x16x32_bf16 a[72:75], v[156:159], v[124:127], a[72:75]\n" \
"ds_read_b64_tr_b16 v[94:95], v34 offset:1024\n" \
"v_accvgpr_read_b32 v180, a36\n" \
"v_accvgpr_read_b32 v181, a37\n" \
"v_accvgpr_read_b32 v182, a38\n" \
"v_mfma_f32_16x16x32_bf16 a[104:107], v[156:159], v[128:131], a[104:107]\n" \
"ds_read_b64_tr_b16 v[96:97], v35 offset:0\n" \
"v_accvgpr_read_b32 v183, a39\n" \
"v_cvt_pk_bf16_f32 v200, v200, v201\n" \
"v_cvt_pk_bf16_f32 v201, v202, v203\n" \
"v_mfma_f32_16x16x32_bf16 a[68:71], v[152:155], v[124:127], a[68:71]\n" \
"s_add_u32 m0, s30, 0x8c00\n" \
"s_mov_b32 s84, 0x6c80\n" \
"v_cvt_pk_bf16_f32 v202, v180, v181\n" \
"v_cvt_pk_bf16_f32 v203, v182, v183\n" \
"v_mfma_f32_16x16x32_bf16 a[100:103], v[152:155], v[128:131], a[100:103]\n" \
"buffer_load_dwordx4 v25, s[76:79], s84 offen lds\n" \
"v_accvgpr_read_b32 v204, a40\n" \
"v_accvgpr_read_b32 v205, a41\n" \
"v_mfma_f32_16x16x32_bf16 a[64:67], v[148:151], v[124:127], a[64:67]\n" \
"ds_read_b64_tr_b16 v[98:99], v35 offset:1024\n" \
"v_accvgpr_read_b32 v206, a42\n" \
"v_accvgpr_read_b32 v207, a43\n" \
"v_accvgpr_read_b32 v180, a44\n" \
"v_mfma_f32_16x16x32_bf16 a[96:99], v[148:151], v[128:131], a[96:99]\n" \
"ds_read_b64_tr_b16 v[100:101], v36 offset:0\n" \
"v_accvgpr_read_b32 v181, a45\n" \
"v_accvgpr_read_b32 v182, a46\n" \
"v_accvgpr_read_b32 v183, a47\n" \
"v_mfma_f32_16x16x32_bf16 a[128:131], v[148:151], v[132:135], a[128:131]\n" \
"s_add_u32 m0, s31, 0x8400\n" \
"s_mov_b32 s85, 0x88000\n" \
"v_cvt_pk_bf16_f32 v204, v204, v205\n" \
"v_cvt_pk_bf16_f32 v205, v206, v207\n" \
"v_mfma_f32_16x16x32_bf16 a[160:163], v[148:151], v[136:139], a[160:163]\n" \
"buffer_load_dwordx4 v26, s[68:71], s85 offen lds\n" \
"v_cvt_pk_bf16_f32 v206, v180, v181\n" \
"v_cvt_pk_bf16_f32 v207, v182, v183\n" \
"v_mfma_f32_16x16x32_bf16 a[132:135], v[152:155], v[132:135], a[132:135]\n" \
"ds_read_b64_tr_b16 v[102:103], v36 offset:512\n" \
"v_accvgpr_read_b32 v208, a48\n" \
"v_accvgpr_read_b32 v209, a49\n" \
"v_accvgpr_read_b32 v210, a50\n" \
"v_mfma_f32_16x16x32_bf16 a[164:167], v[152:155], v[136:139], a[164:167]\n" \
"ds_read_b128 v[52:55], v28 offset:0\n" \
"v_accvgpr_read_b32 v211, a51\n" \
"v_accvgpr_read_b32 v180, a52\n" \
"v_accvgpr_read_b32 v181, a53\n" \
"v_mfma_f32_16x16x32_bf16 a[136:139], v[156:159], v[132:135], a[136:139]\n" \
"s_add_u32 m0, s30, 0x9000\n" \
"s_mov_b32 s84, 0x9080\n" \
"v_accvgpr_read_b32 v182, a54\n" \
"v_accvgpr_read_b32 v183, a55\n" \
"v_mfma_f32_16x16x32_bf16 a[168:171], v[156:159], v[136:139], a[168:171]\n" \
"buffer_load_dwordx4 v24, s[76:79], s84 offen lds\n" \
"v_cvt_pk_bf16_f32 v208, v208, v209\n" \
"v_cvt_pk_bf16_f32 v209, v210, v211\n" \
"v_mfma_f32_16x16x32_bf16 a[140:143], v[160:163], v[132:135], a[140:143]\n" \
"ds_read_b128 v[56:59], v28 offset:2048\n" \
"v_cvt_pk_bf16_f32 v210, v180, v181\n" \
"v_cvt_pk_bf16_f32 v211, v182, v183\n" \
"v_accvgpr_read_b32 v212, a56\n" \
"v_mfma_f32_16x16x32_bf16 a[172:175], v[160:163], v[136:139], a[172:175]\n" \
"ds_read_b128 v[60:63], v28 offset:4096\n" \
"v_accvgpr_read_b32 v213, a57\n" \
"v_accvgpr_read_b32 v214, a58\n" \
"v_accvgpr_read_b32 v215, a59\n" \
"v_mfma_f32_16x16x32_bf16 a[144:147], v[164:167], v[132:135], a[144:147]\n" \
"s_add_u32 m0, s30, 0x9400\n" \
"s_mov_b32 s85, 0xb480\n" \
"v_accvgpr_read_b32 v180, a60\n" \
"v_accvgpr_read_b32 v181, a61\n" \
"v_mfma_f32_16x16x32_bf16 a[176:179], v[164:167], v[136:139], a[176:179]\n" \
"buffer_load_dwordx4 v25, s[76:79], s85 offen lds\n" \
"v_accvgpr_read_b32 v182, a62\n" \
"v_accvgpr_read_b32 v183, a63\n" \
"v_mfma_f32_16x16x32_bf16 a[148:151], v[168:171], v[132:135], a[148:151]\n" \
"ds_read_b128 v[64:67], v28 offset:6144\n" \
"v_cvt_pk_bf16_f32 v212, v212, v213\n" \
"v_cvt_pk_bf16_f32 v213, v214, v215\n" \
"v_cvt_pk_bf16_f32 v214, v180, v181\n" \
"v_mfma_f32_16x16x32_bf16 a[180:183], v[168:171], v[136:139], a[180:183]\n" \
"ds_read_b128 v[68:71], v28 offset:8192\n" \
"v_cvt_pk_bf16_f32 v215, v182, v183\n" \
"v_accvgpr_read_b32 v216, a64\n" \
"v_accvgpr_read_b32 v217, a65\n" \
"v_mfma_f32_16x16x32_bf16 a[152:155], v[172:175], v[132:135], a[152:155]\n" \
"s_add_u32 m0, s31, 0x8800\n" \
"s_mov_b32 s84, 0x90000\n" \
"v_accvgpr_read_b32 v218, a66\n" \
"v_accvgpr_read_b32 v219, a67\n" \
"v_mfma_f32_16x16x32_bf16 a[184:187], v[172:175], v[136:139], a[184:187]\n" \
"buffer_load_dwordx4 v27, s[68:71], s84 offen lds\n" \
"v_accvgpr_read_b32 v180, a68\n" \
"v_accvgpr_read_b32 v181, a69\n" \
"v_mfma_f32_16x16x32_bf16 a[156:159], v[176:179], v[132:135], a[156:159]\n" \
"ds_read_b128 v[72:75], v28 offset:10240\n" \
"v_accvgpr_read_b32 v182, a70\n" \
"v_accvgpr_read_b32 v183, a71\n" \
"v_cvt_pk_bf16_f32 v216, v216, v217\n" \
"v_mfma_f32_16x16x32_bf16 a[188:191], v[176:179], v[136:139], a[188:191]\n" \
"ds_read_b128 v[76:79], v28 offset:12288\n" \
"v_cvt_pk_bf16_f32 v217, v218, v219\n" \
"v_cvt_pk_bf16_f32 v218, v180, v181\n" \
"v_cvt_pk_bf16_f32 v219, v182, v183\n" \
"v_mfma_f32_16x16x32_bf16 a[220:223], v[176:179], v[140:143], a[220:223]\n" \
"v_accvgpr_read_b32 v220, a72\n" \
"v_accvgpr_read_b32 v221, a73\n" \
"v_accvgpr_read_b32 v222, a74\n" \
"v_accvgpr_read_b32 v223, a75\n" \
"v_mfma_f32_16x16x32_bf16 a[252:255], v[176:179], v[144:147], a[252:255]\n" \
"ds_read_b128 v[80:83], v28 offset:14336\n" \
"v_accvgpr_read_b32 v180, a76\n" \
"v_accvgpr_read_b32 v181, a77\n" \
"v_accvgpr_read_b32 v182, a78\n" \
"v_mfma_f32_16x16x32_bf16 a[216:219], v[172:175], v[140:143], a[216:219]\n" \
"v_accvgpr_read_b32 v183, a79\n" \
"v_cvt_pk_bf16_f32 v220, v220, v221\n" \
"v_cvt_pk_bf16_f32 v221, v222, v223\n" \
"v_cvt_pk_bf16_f32 v222, v180, v181\n" \
"v_mfma_f32_16x16x32_bf16 a[248:251], v[172:175], v[144:147], a[248:251]\n" \
"v_cvt_pk_bf16_f32 v223, v182, v183\n" \
"v_accvgpr_read_b32 v224, a80\n" \
"v_accvgpr_read_b32 v225, a81\n" \
"v_accvgpr_read_b32 v226, a82\n" \
"v_mfma_f32_16x16x32_bf16 a[212:215], v[168:171], v[140:143], a[212:215]\n" \
"v_accvgpr_read_b32 v227, a83\n" \
"v_accvgpr_read_b32 v180, a84\n" \
"v_accvgpr_read_b32 v181, a85\n" \
"v_accvgpr_read_b32 v182, a86\n" \
"v_mfma_f32_16x16x32_bf16 a[244:247], v[168:171], v[144:147], a[244:247]\n" \
"v_accvgpr_read_b32 v183, a87\n" \
"v_cvt_pk_bf16_f32 v224, v224, v225\n" \
"v_cvt_pk_bf16_f32 v225, v226, v227\n" \
"v_cvt_pk_bf16_f32 v226, v180, v181\n" \
"v_mfma_f32_16x16x32_bf16 a[208:211], v[164:167], v[140:143], a[208:211]\n" \
"v_cvt_pk_bf16_f32 v227, v182, v183\n" \
"v_accvgpr_read_b32 v228, a88\n" \
"v_accvgpr_read_b32 v229, a89\n" \
"v_accvgpr_read_b32 v230, a90\n" \
"v_mfma_f32_16x16x32_bf16 a[240:243], v[164:167], v[144:147], a[240:243]\n" \
"v_accvgpr_read_b32 v231, a91\n" \
"v_accvgpr_read_b32 v180, a92\n" \
"v_accvgpr_read_b32 v181, a93\n" \
"v_accvgpr_read_b32 v182, a94\n" \
"v_mfma_f32_16x16x32_bf16 a[204:207], v[160:163], v[140:143], a[204:207]\n" \
"v_accvgpr_read_b32 v183, a95\n" \
"v_cvt_pk_bf16_f32 v228, v228, v229\n" \
"v_cvt_pk_bf16_f32 v229, v230, v231\n" \
"v_cvt_pk_bf16_f32 v230, v180, v181\n" \
"v_mfma_f32_16x16x32_bf16 a[236:239], v[160:163], v[144:147], a[236:239]\n" \
"v_cvt_pk_bf16_f32 v231, v182, v183\n" \
"v_accvgpr_read_b32 v232, a96\n" \
"v_accvgpr_read_b32 v233, a97\n" \
"v_accvgpr_read_b32 v234, a98\n" \
"v_mfma_f32_16x16x32_bf16 a[200:203], v[156:159], v[140:143], a[200:203]\n" \
"v_accvgpr_read_b32 v235, a99\n" \
"v_accvgpr_read_b32 v180, a100\n" \
"v_accvgpr_read_b32 v181, a101\n" \
"v_accvgpr_read_b32 v182, a102\n" \
"v_mfma_f32_16x16x32_bf16 a[232:235], v[156:159], v[144:147], a[232:235]\n" \
"v_accvgpr_read_b32 v183, a103\n" \
"v_cvt_pk_bf16_f32 v232, v232, v233\n" \
"v_cvt_pk_bf16_f32 v233, v234, v235\n" \
"v_cvt_pk_bf16_f32 v234, v180, v181\n" \
"v_mfma_f32_16x16x32_bf16 a[196:199], v[152:155], v[140:143], a[196:199]\n" \
"v_cvt_pk_bf16_f32 v235, v182, v183\n" \
"v_accvgpr_read_b32 v236, a104\n" \
"v_accvgpr_read_b32 v237, a105\n" \
"v_accvgpr_read_b32 v238, a106\n" \
"v_mfma_f32_16x16x32_bf16 a[228:231], v[152:155], v[144:147], a[228:231]\n" \
"v_accvgpr_read_b32 v239, a107\n" \
"v_accvgpr_read_b32 v180, a108\n" \
"v_accvgpr_read_b32 v181, a109\n" \
"v_accvgpr_read_b32 v182, a110\n" \
"v_mfma_f32_16x16x32_bf16 a[192:195], v[148:151], v[140:143], a[192:195]\n" \
"v_accvgpr_read_b32 v183, a111\n" \
"v_cvt_pk_bf16_f32 v236, v236, v237\n" \
"v_cvt_pk_bf16_f32 v237, v238, v239\n" \
"v_cvt_pk_bf16_f32 v238, v180, v181\n" \
"v_mfma_f32_16x16x32_bf16 a[224:227], v[148:151], v[144:147], a[224:227]\n" \
"v_cvt_pk_bf16_f32 v239, v182, v183\n" \
"v_accvgpr_read_b32 v240, a112\n" \
"v_accvgpr_read_b32 v241, a113\n" \
"v_accvgpr_read_b32 v242, a114\n" \
"s_waitcnt lgkmcnt(15)\n" \
"s_nop 0\n" \
"v_mfma_f32_16x16x32_bf16 a[24:27], v[104:107], v[248:251], 0\n" \
"s_add_u32 m0, s30, 0x9800\n" \
"s_mov_b32 s84, 0xd880\n" \
"v_accvgpr_read_b32 v243, a115\n" \
"v_accvgpr_read_b32 v180, a116\n" \
"v_accvgpr_read_b32 v181, a117\n" \
"v_mfma_f32_16x16x32_bf16 a[28:31], v[248:251], v[108:111], 0\n" \
"buffer_load_dwordx4 v24, s[76:79], s84 offen lds\n" \
"v_accvgpr_read_b32 v182, a118\n" \
"v_accvgpr_read_b32 v183, a119\n" \
"v_cvt_pk_bf16_f32 v240, v240, v241\n" \
"v_mfma_f32_16x16x32_bf16 a[56:59], v[112:115], v[248:251], 0\n" \
"ds_read_b64_tr_b16 v[168:169], v37 offset:4096\n" \
"v_cvt_pk_bf16_f32 v241, v242, v243\n" \
"v_cvt_pk_bf16_f32 v242, v180, v181\n" \
"v_cvt_pk_bf16_f32 v243, v182, v183\n" \
"v_accvgpr_read_b32 v244, a120\n" \
"s_waitcnt lgkmcnt(8)\n" \
"v_mfma_f32_16x16x32_bf16 a[0:3], v[84:87], v[52:55], 0\n" \
"s_add_u32 m0, s30, 0x9c00\n" \
"s_mov_b32 s85, 0xfc80\n" \
"v_accvgpr_read_b32 v245, a121\n" \
"v_accvgpr_read_b32 v246, a122\n" \
"v_accvgpr_read_b32 v247, a123\n" \
"s_waitcnt lgkmcnt(7)\n" \
"v_mfma_f32_16x16x32_bf16 a[32:35], v[84:87], v[56:59], 0\n" \
"buffer_load_dwordx4 v25, s[76:79], s85 offen lds\n" \
"v_accvgpr_read_b32 v180, a124\n" \
"v_accvgpr_read_b32 v181, a125\n" \
"v_accvgpr_read_b32 v182, a126\n" \
"v_mfma_f32_16x16x32_bf16 a[4:7], v[88:91], v[52:55], 0\n" \
"ds_read_b64_tr_b16 v[170:171], v37 offset:4608\n" \
"v_accvgpr_read_b32 v183, a127\n" \
"v_cvt_pk_bf16_f32 v244, v244, v245\n" \
"v_cvt_pk_bf16_f32 v245, v246, v247\n" \
"v_cvt_pk_bf16_f32 v246, v180, v181\n" \
"v_mfma_f32_16x16x32_bf16 a[36:39], v[88:91], v[56:59], 0\n" \
"s_add_u32 m0, s31, 0x8c00\n" \
"s_mov_b32 s84, 0x98000\n" \
"v_cvt_pk_bf16_f32 v247, v182, v183\n" \
"v_accvgpr_read_b32 v252, a152\n" \
"v_accvgpr_read_b32 v253, a153\n" \
"v_mfma_f32_16x16x32_bf16 a[8:11], v[92:95], v[52:55], 0\n" \
"buffer_load_dwordx4 v27, s[68:71], s84 offen lds\n" \
"v_accvgpr_read_b32 v254, a154\n" \
"v_accvgpr_read_b32 v255, a155\n" \
"v_accvgpr_read_b32 v180, a156\n" \
"v_mfma_f32_16x16x32_bf16 a[40:43], v[92:95], v[56:59], 0\n" \
"ds_read_b64_tr_b16 v[172:173], v38 offset:4096\n" \
"v_accvgpr_read_b32 v181, a157\n" \
"v_accvgpr_read_b32 v182, a158\n" \
"v_accvgpr_read_b32 v183, a159\n" \
"v_cvt_pk_bf16_f32 v252, v252, v253\n" \
"v_mfma_f32_16x16x32_bf16 a[12:15], v[96:99], v[52:55], 0\n" \
"s_add_u32 s89, s41, 0x0\n" \
"s_nop 0\n" \
"buffer_store_dwordx4 v[184:187], v20, s[80:83], s89 offen offset:0 sc1\n" \
"v_cvt_pk_bf16_f32 v253, v254, v255\n" \
"v_cvt_pk_bf16_f32 v254, v180, v181\n" \
"v_mfma_f32_16x16x32_bf16 a[44:47], v[96:99], v[56:59], 0\n" \
"ds_read_b64_tr_b16 v[174:175], v38 offset:4608\n" \
"v_cvt_pk_bf16_f32 v255, v182, v183\n" \
"v_accvgpr_write_b32 a60, v252\n" \
"v_accvgpr_write_b32 a61, v253\n" \
"v_accvgpr_write_b32 a62, v254\n" \
"v_mfma_f32_16x16x32_bf16 a[16:19], v[100:103], v[52:55], 0\n" \
"ds_read_b64_tr_b16 v[176:177], v39 offset:4096\n" \
"v_accvgpr_write_b32 a63, v255\n" \
"v_accvgpr_read_b32 v252, a128\n" \
"v_accvgpr_read_b32 v253, a129\n" \
"v_accvgpr_read_b32 v254, a130\n" \
"v_mfma_f32_16x16x32_bf16 a[20:23], v[104:107], v[52:55], 0\n" \
"s_add_u32 s88, s41, 0x0\n" \
"s_nop 0\n" \
"buffer_store_dwordx4 v[188:191], v20, s[80:83], s88 offen offset:1024 sc1\n" \
"v_accvgpr_read_b32 v255, a131\n" \
"v_accvgpr_read_b32 v180, a132\n" \
"v_mfma_f32_16x16x32_bf16 a[52:55], v[112:115], v[52:55], 0\n" \
"ds_read_b64_tr_b16 v[178:179], v39 offset:4608\n" \
"v_accvgpr_read_b32 v181, a133\n" \
"v_accvgpr_read_b32 v182, a134\n" \
"v_accvgpr_read_b32 v183, a135\n" \
"v_cvt_pk_bf16_f32 v252, v252, v253\n" \
"v_mfma_f32_16x16x32_bf16 a[48:51], v[52:55], v[108:111], 0\n" \
"ds_read_b64_tr_b16 v[148:149], v32 offset:8192\n" \
"v_cvt_pk_bf16_f32 v253, v254, v255\n" \
"v_cvt_pk_bf16_f32 v254, v180, v181\n" \
"v_cvt_pk_bf16_f32 v255, v182, v183\n" \
"v_accvgpr_write_b32 a88, v252\n" \
"s_waitcnt lgkmcnt(12)\n" \
"v_mfma_f32_16x16x32_bf16 a[64:67], v[84:87], v[60:63], 0\n" \
"s_add_u32 s89, s41, 0x0\n" \
"buffer_store_dwordx4 v[192:195], v20, s[80:83], s89 offen offset:2048 sc1\n" \
"v_accvgpr_write_b32 a89, v253\n" \
"v_accvgpr_write_b32 a90, v254\n" \
"s_waitcnt lgkmcnt(11)\n" \
"s_nop 0\n" \
"v_mfma_f32_16x16x32_bf16 a[96:99], v[84:87], v[64:67], 0\n" \
"ds_read_b64_tr_b16 v[150:151], v32 offset:9216\n" \
"v_accvgpr_write_b32 a91, v255\n" \
"v_accvgpr_read_b32 v252, a136\n" \
"v_accvgpr_read_b32 v253, a137\n" \
"v_accvgpr_read_b32 v254, a138\n" \
"v_mfma_f32_16x16x32_bf16 a[68:71], v[88:91], v[60:63], 0\n" \
"ds_read_b128 v[248:251], v23 offset:0\n" \
"v_accvgpr_read_b32 v255, a139\n" \
"v_accvgpr_read_b32 v180, a140\n" \
"v_accvgpr_read_b32 v181, a141\n" \
"v_accvgpr_read_b32 v182, a142\n" \
"v_mfma_f32_16x16x32_bf16 a[100:103], v[88:91], v[64:67], 0\n" \
"s_add_u32 s88, s41, 0x0\n" \
"s_nop 0\n" \
"buffer_store_dwordx4 v[196:199], v20, s[80:83], s88 offen offset:3072 sc1\n" \
"v_accvgpr_read_b32 v183, a143\n" \
"v_cvt_pk_bf16_f32 v252, v252, v253\n" \
"v_mfma_f32_16x16x32_bf16 a[72:75], v[92:95], v[60:63], 0\n" \
"ds_read_b64_tr_b16 v[152:153], v33 offset:8192\n" \
"v_cvt_pk_bf16_f32 v253, v254, v255\n" \
"v_cvt_pk_bf16_f32 v254, v180, v181\n" \
"v_cvt_pk_bf16_f32 v255, v182, v183\n" \
"v_accvgpr_write_b32 a92, v252\n" \
"v_mfma_f32_16x16x32_bf16 a[104:107], v[92:95], v[64:67], 0\n" \
"ds_read_b64_tr_b16 v[154:155], v33 offset:9216\n" \
"v_accvgpr_write_b32 a93, v253\n" \
"v_accvgpr_write_b32 a94, v254\n" \
"v_accvgpr_write_b32 a95, v255\n" \
"v_accvgpr_read_b32 v252, a144\n" \
"v_mfma_f32_16x16x32_bf16 a[76:79], v[96:99], v[60:63], 0\n" \
"ds_read_b64_tr_b16 v[156:157], v34 offset:8192\n" \
"v_accvgpr_read_b32 v253, a145\n" \
"v_accvgpr_read_b32 v254, a146\n" \
"v_accvgpr_read_b32 v255, a147\n" \
"v_accvgpr_read_b32 v180, a148\n" \
"v_mfma_f32_16x16x32_bf16 a[108:111], v[96:99], v[64:67], 0\n" \
"ds_read_b64_tr_b16 v[158:159], v34 offset:9216\n" \
"v_accvgpr_read_b32 v181, a149\n" \
"v_accvgpr_read_b32 v182, a150\n" \
"v_accvgpr_read_b32 v183, a151\n" \
"v_cvt_pk_bf16_f32 v252, v252, v253\n" \
"v_mfma_f32_16x16x32_bf16 a[80:83], v[100:103], v[56:59], 0\n" \
"ds_read_b64_tr_b16 v[160:161], v35 offset:8192\n" \
"v_cvt_pk_bf16_f32 v253, v254, v255\n" \
"v_cvt_pk_bf16_f32 v254, v180, v181\n" \
"v_cvt_pk_bf16_f32 v255, v182, v183\n" \
"v_accvgpr_write_b32 a120, v252\n" \
"v_mfma_f32_16x16x32_bf16 a[84:87], v[104:107], v[56:59], 0\n" \
"ds_read_b64_tr_b16 v[162:163], v35 offset:9216\n" \
"v_accvgpr_write_b32 a121, v253\n" \
"v_accvgpr_write_b32 a122, v254\n" \
"v_accvgpr_write_b32 a123, v255\n" \
"v_accvgpr_read_b32 v252, a184\n" \
"v_mfma_f32_16x16x32_bf16 a[116:119], v[112:115], v[56:59], 0\n" \
"ds_read_b64_tr_b16 v[164:165], v36 offset:4096\n" \
"v_accvgpr_read_b32 v253, a185\n" \
"v_accvgpr_read_b32 v254, a186\n" \
"v_accvgpr_read_b32 v255, a187\n" \
"v_accvgpr_read_b32 v180, a188\n" \
"v_mfma_f32_16x16x32_bf16 a[112:115], v[56:59], v[108:111], 0\n" \
"ds_read_b64_tr_b16 v[166:167], v36 offset:4608\n" \
"v_accvgpr_read_b32 v181, a189\n" \
"v_accvgpr_read_b32 v182, a190\n" \
"v_accvgpr_read_b32 v183, a191\n" \
"v_cvt_pk_bf16_f32 v252, v252, v253\n" \
"s_waitcnt lgkmcnt(15)\n" \
"s_nop 0\n" \
"v_mfma_f32_16x16x32_bf16 a[128:131], v[84:87], v[68:71], 0\n" \
"ds_read_b128 v[116:119], v29 offset:0\n" \
"v_cvt_pk_bf16_f32 v253, v254, v255\n" \
"v_cvt_pk_bf16_f32 v254, v180, v181\n" \
"v_cvt_pk_bf16_f32 v255, v182, v183\n" \
"v_accvgpr_write_b32 a124, v252\n" \
"v_accvgpr_write_b32 a125, v253\n" \
"v_accvgpr_write_b32 a126, v254\n" \
"v_accvgpr_write_b32 a127, v255\n" \
"v_accvgpr_read_b32 v252, a160\n" \
"v_accvgpr_read_b32 v253, a161\n" \
"v_accvgpr_read_b32 v254, a162\n" \
"v_accvgpr_read_b32 v255, a163\n" \
"v_accvgpr_read_b32 v180, a164\n" \
"v_accvgpr_read_b32 v181, a165\n" \
"v_accvgpr_read_b32 v182, a166\n" \
"v_accvgpr_read_b32 v183, a167\n" \
"v_cvt_pk_bf16_f32 v252, v252, v253\n" \
"v_cvt_pk_bf16_f32 v253, v254, v255\n" \
"v_cvt_pk_bf16_f32 v254, v180, v181\n" \
"v_cvt_pk_bf16_f32 v255, v182, v183\n" \
"v_accvgpr_write_b32 a152, v252\n" \
"v_accvgpr_write_b32 a153, v253\n" \
"v_accvgpr_write_b32 a154, v254\n" \
"v_accvgpr_write_b32 a155, v255\n" \
"v_mfma_f32_16x16x32_bf16 a[160:163], v[84:87], v[72:75], 0\n" \
"ds_read_b128 v[120:123], v29 offset:2048\n" \
"v_accvgpr_read_b32 v252, a168\n" \
"v_accvgpr_read_b32 v253, a169\n" \
"v_accvgpr_read_b32 v254, a170\n" \
"v_accvgpr_read_b32 v255, a171\n" \
"v_mfma_f32_16x16x32_bf16 a[132:135], v[88:91], v[68:71], 0\n" \
"ds_read_b128 v[124:127], v29 offset:4096\n" \
"v_accvgpr_read_b32 v180, a172\n" \
"v_accvgpr_read_b32 v181, a173\n" \
"v_accvgpr_read_b32 v182, a174\n" \
"v_accvgpr_read_b32 v183, a175\n" \
"v_mfma_f32_16x16x32_bf16 a[164:167], v[88:91], v[72:75], 0\n" \
"v_cvt_pk_bf16_f32 v252, v252, v253\n" \
"v_cvt_pk_bf16_f32 v253, v254, v255\n" \
"v_cvt_pk_bf16_f32 v254, v180, v181\n" \
"v_cvt_pk_bf16_f32 v255, v182, v183\n" \
"v_accvgpr_write_b32 a156, v252\n" \
"v_mfma_f32_16x16x32_bf16 a[136:139], v[92:95], v[68:71], 0\n" \
"ds_read_b128 v[128:131], v29 offset:6144\n" \
"v_accvgpr_write_b32 a157, v253\n" \
"v_accvgpr_write_b32 a158, v254\n" \
"v_accvgpr_write_b32 a159, v255\n" \
"v_accvgpr_read_b32 v252, a176\n" \
"v_mfma_f32_16x16x32_bf16 a[168:171], v[92:95], v[72:75], 0\n" \
"ds_read_b128 v[132:135], v29 offset:8192\n" \
"v_accvgpr_read_b32 v253, a177\n" \
"v_accvgpr_read_b32 v254, a178\n" \
"v_accvgpr_read_b32 v255, a179\n" \
"v_accvgpr_read_b32 v180, a180\n" \
"v_mfma_f32_16x16x32_bf16 a[140:143], v[96:99], v[68:71], 0\n" \
"v_accvgpr_read_b32 v181, a181\n" \
"v_accvgpr_read_b32 v182, a182\n" \
"v_accvgpr_read_b32 v183, a183\n" \
"v_cvt_pk_bf16_f32 v252, v252, v253\n" \
"v_cvt_pk_bf16_f32 v253, v254, v255\n" \
"v_mfma_f32_16x16x32_bf16 a[172:175], v[96:99], v[72:75], 0\n" \
"ds_read_b128 v[136:139], v29 offset:10240\n" \
"v_cvt_pk_bf16_f32 v254, v180, v181\n" \
"v_cvt_pk_bf16_f32 v255, v182, v183\n" \
"v_accvgpr_write_b32 a184, v252\n" \
"v_accvgpr_write_b32 a185, v253\n" \
"v_mfma_f32_16x16x32_bf16 a[144:147], v[100:103], v[60:63], 0\n" \
"ds_read_b128 v[140:143], v29 offset:12288\n" \
"v_accvgpr_write_b32 a186, v254\n" \
"v_accvgpr_write_b32 a187, v255\n" \
"v_accvgpr_read_b32 v252, a216\n" \
"v_accvgpr_read_b32 v253, a217\n" \
"v_mfma_f32_16x16x32_bf16 a[148:151], v[104:107], v[60:63], 0\n" \
"v_accvgpr_read_b32 v254, a218\n" \
"v_accvgpr_read_b32 v255, a219\n" \
"v_accvgpr_read_b32 v180, a220\n" \
"v_accvgpr_read_b32 v181, a221\n" \
"v_accvgpr_read_b32 v182, a222\n" \
"v_mfma_f32_16x16x32_bf16 a[180:183], v[112:115], v[60:63], 0\n" \
"ds_read_b128 v[144:147], v29 offset:14336\n" \
"v_accvgpr_read_b32 v183, a223\n" \
"v_cvt_pk_bf16_f32 v252, v252, v253\n" \
"v_cvt_pk_bf16_f32 v253, v254, v255\n" \
"v_cvt_pk_bf16_f32 v254, v180, v181\n" \
"v_mfma_f32_16x16x32_bf16 a[176:179], v[60:63], v[108:111], 0\n" \
"v_cvt_pk_bf16_f32 v255, v182, v183\n" \
"v_accvgpr_write_b32 a188, v252\n" \
"v_accvgpr_write_b32 a189, v253\n" \
"v_accvgpr_write_b32 a190, v254\n" \
"v_accvgpr_write_b32 a191, v255\n" \
"v_accvgpr_read_b32 v252, a192\n" \
"v_accvgpr_read_b32 v253, a193\n" \
"v_accvgpr_read_b32 v254, a194\n" \
"v_accvgpr_read_b32 v255, a195\n" \
"v_accvgpr_read_b32 v180, a196\n" \
"v_accvgpr_read_b32 v181, a197\n" \
"v_accvgpr_read_b32 v182, a198\n" \
"v_accvgpr_read_b32 v183, a199\n" \
"v_cvt_pk_bf16_f32 v252, v252, v253\n" \
"v_cvt_pk_bf16_f32 v253, v254, v255\n" \
"v_cvt_pk_bf16_f32 v254, v180, v181\n" \
"v_cvt_pk_bf16_f32 v255, v182, v183\n" \
"v_accvgpr_write_b32 a216, v252\n" \
"v_accvgpr_write_b32 a217, v253\n" \
"v_accvgpr_write_b32 a218, v254\n" \
"v_accvgpr_write_b32 a219, v255\n" \
"v_mfma_f32_16x16x32_bf16 a[192:195], v[84:87], v[76:79], 0\n" \
"v_accvgpr_read_b32 v252, a200\n" \
"v_accvgpr_read_b32 v253, a201\n" \
"v_accvgpr_read_b32 v254, a202\n" \
"v_accvgpr_read_b32 v255, a203\n" \
"v_accvgpr_read_b32 v180, a204\n" \
"v_accvgpr_read_b32 v181, a205\n" \
"v_accvgpr_read_b32 v182, a206\n" \
"v_accvgpr_read_b32 v183, a207\n" \
"v_cvt_pk_bf16_f32 v252, v252, v253\n" \
"v_cvt_pk_bf16_f32 v253, v254, v255\n" \
"v_cvt_pk_bf16_f32 v254, v180, v181\n" \
"v_cvt_pk_bf16_f32 v255, v182, v183\n" \
"v_accvgpr_write_b32 a220, v252\n" \
"v_accvgpr_write_b32 a221, v253\n" \
"v_accvgpr_write_b32 a222, v254\n" \
"v_accvgpr_write_b32 a223, v255\n" \
"v_accvgpr_read_b32 v184, a208\n" \
"v_accvgpr_read_b32 v185, a209\n" \
"v_accvgpr_read_b32 v186, a210\n" \
"v_accvgpr_read_b32 v187, a211\n" \
"v_accvgpr_read_b32 v180, a212\n" \
"v_accvgpr_read_b32 v181, a213\n" \
"v_accvgpr_read_b32 v182, a214\n" \
"v_accvgpr_read_b32 v183, a215\n" \
"v_cvt_pk_bf16_f32 v184, v184, v185\n" \
"v_cvt_pk_bf16_f32 v185, v186, v187\n" \
"v_cvt_pk_bf16_f32 v186, v180, v181\n" \
"v_cvt_pk_bf16_f32 v187, v182, v183\n" \
"v_accvgpr_read_b32 v252, a248\n" \
"v_accvgpr_read_b32 v253, a249\n" \
"v_accvgpr_read_b32 v254, a250\n" \
"v_accvgpr_read_b32 v255, a251\n" \
"v_accvgpr_read_b32 v180, a252\n" \
"v_accvgpr_read_b32 v181, a253\n" \
"v_accvgpr_read_b32 v182, a254\n" \
"v_accvgpr_read_b32 v183, a255\n" \
"v_cvt_pk_bf16_f32 v252, v252, v253\n" \
"v_cvt_pk_bf16_f32 v253, v254, v255\n" \
"v_cvt_pk_bf16_f32 v254, v180, v181\n" \
"v_cvt_pk_bf16_f32 v255, v182, v183\n" \
"v_accvgpr_write_b32 a248, v252\n" \
"v_accvgpr_write_b32 a249, v253\n" \
"v_accvgpr_write_b32 a250, v254\n" \
"v_accvgpr_write_b32 a251, v255\n" \
"v_accvgpr_read_b32 v252, a224\n" \
"v_accvgpr_read_b32 v253, a225\n" \
"v_accvgpr_read_b32 v254, a226\n" \
"v_accvgpr_read_b32 v255, a227\n" \
"v_accvgpr_read_b32 v180, a228\n" \
"v_accvgpr_read_b32 v181, a229\n" \
"v_accvgpr_read_b32 v182, a230\n" \
"v_accvgpr_read_b32 v183, a231\n" \
"v_cvt_pk_bf16_f32 v252, v252, v253\n" \
"v_cvt_pk_bf16_f32 v253, v254, v255\n" \
"v_cvt_pk_bf16_f32 v254, v180, v181\n" \
"v_cvt_pk_bf16_f32 v255, v182, v183\n" \
"v_accvgpr_write_b32 a252, v252\n" \
"v_accvgpr_write_b32 a253, v253\n" \
"v_accvgpr_write_b32 a254, v254\n" \
"v_accvgpr_write_b32 a255, v255\n" \
"v_mfma_f32_16x16x32_bf16 a[224:227], v[84:87], v[80:83], 0\n" \
"v_accvgpr_read_b32 v188, a232\n" \
"v_accvgpr_read_b32 v189, a233\n" \
"v_accvgpr_read_b32 v190, a234\n" \
"v_accvgpr_read_b32 v191, a235\n" \
"v_accvgpr_read_b32 v180, a236\n" \
"v_mfma_f32_16x16x32_bf16 a[196:199], v[88:91], v[76:79], 0\n" \
"v_accvgpr_read_b32 v181, a237\n" \
"v_accvgpr_read_b32 v182, a238\n" \
"v_accvgpr_read_b32 v183, a239\n" \
"v_cvt_pk_bf16_f32 v188, v188, v189\n" \
"v_cvt_pk_bf16_f32 v189, v190, v191\n" \
"v_mfma_f32_16x16x32_bf16 a[228:231], v[88:91], v[80:83], 0\n" \
"v_cvt_pk_bf16_f32 v190, v180, v181\n" \
"v_cvt_pk_bf16_f32 v191, v182, v183\n" \
"v_accvgpr_read_b32 v252, a240\n" \
"v_accvgpr_read_b32 v253, a241\n" \
"v_accvgpr_read_b32 v254, a242\n" \
"v_mfma_f32_16x16x32_bf16 a[200:203], v[92:95], v[76:79], 0\n" \
"v_accvgpr_read_b32 v255, a243\n" \
"v_accvgpr_read_b32 v180, a244\n" \
"v_accvgpr_read_b32 v181, a245\n" \
"v_accvgpr_read_b32 v182, a246\n" \
"v_accvgpr_read_b32 v183, a247\n" \
"v_mfma_f32_16x16x32_bf16 a[232:235], v[92:95], v[80:83], 0\n" \
"v_cvt_pk_bf16_f32 v252, v252, v253\n" \
"v_cvt_pk_bf16_f32 v253, v254, v255\n" \
"v_cvt_pk_bf16_f32 v254, v180, v181\n" \
"v_cvt_pk_bf16_f32 v255, v182, v183\n" \
"v_mfma_f32_16x16x32_bf16 a[204:207], v[96:99], v[76:79], 0\n" \
"v_mfma_f32_16x16x32_bf16 a[236:239], v[96:99], v[80:83], 0\n" \
"v_mfma_f32_16x16x32_bf16 a[208:211], v[100:103], v[64:67], 0\n" \
"v_mfma_f32_16x16x32_bf16 a[212:215], v[104:107], v[64:67], 0\n" \
"v_mfma_f32_16x16x32_bf16 a[244:247], v[112:115], v[64:67], 0\n" \
"v_mfma_f32_16x16x32_bf16 a[240:243], v[64:67], v[108:111], 0\n" \
"s_waitcnt vmcnt(4) lgkmcnt(0)\n" \
"s_barrier\n" \
"v_mfma_f32_16x16x32_bf16 a[24:27], v[168:171], v[248:251], a[24:27]\n" \
"s_add_u32 m0, s40, 0x0\n" \
"s_nop 0\n" \
"s_add_u32 s85, s39, 0x4000\n" \
"v_mfma_f32_16x16x32_bf16 a[28:31], v[248:251], v[172:175], a[28:31]\n" \
"buffer_load_dwordx4 v20, s[80:83], s85 offen sc1 lds\n" \
"v_mfma_f32_16x16x32_bf16 a[56:59], v[176:179], v[248:251], a[56:59]\n" \
"ds_read_b64_tr_b16 v[104:105], v37 offset:32768\n" \
"v_mfma_f32_16x16x32_bf16 a[0:3], v[148:151], v[116:119], a[0:3]\n" \
"ds_read_b64_tr_b16 v[106:107], v37 offset:33280\n" \
"v_mfma_f32_16x16x32_bf16 a[32:35], v[148:151], v[120:123], a[32:35]\n" \
"s_add_u32 m0, s40, 0x1000\n" \
"s_add_u32 s84, s39, 0x5000\n" \
"v_mfma_f32_16x16x32_bf16 a[4:7], v[152:155], v[116:119], a[4:7]\n" \
"buffer_load_dwordx4 v20, s[80:83], s84 offen sc1 lds\n" \
"v_mfma_f32_16x16x32_bf16 a[36:39], v[152:155], v[120:123], a[36:39]\n" \
"ds_read_b64_tr_b16 v[108:109], v38 offset:32768\n" \
"v_mfma_f32_16x16x32_bf16 a[8:11], v[156:159], v[116:119], a[8:11]\n" \
"ds_read_b64_tr_b16 v[110:111], v38 offset:33280\n" \
"v_mfma_f32_16x16x32_bf16 a[40:43], v[156:159], v[120:123], a[40:43]\n" \
"s_add_u32 m0, s40, 0x3000\n" \
"s_mov_b32 s85, 0x100\n" \
"v_mfma_f32_16x16x32_bf16 a[12:15], v[160:163], v[116:119], a[12:15]\n" \
"buffer_load_dwordx4 v21, s[76:79], s85 offen lds\n" \
"v_mfma_f32_16x16x32_bf16 a[44:47], v[160:163], v[120:123], a[44:47]\n" \
"ds_read_b64_tr_b16 v[112:113], v39 offset:32768\n" \
"v_mfma_f32_16x16x32_bf16 a[16:19], v[164:167], v[116:119], a[16:19]\n" \
"ds_read_b64_tr_b16 v[114:115], v39 offset:33280\n" \
"v_mfma_f32_16x16x32_bf16 a[20:23], v[168:171], v[116:119], a[20:23]\n" \
"s_add_u32 m0, s30, 0x0\n" \
"s_nop 0\n" \
"s_mov_b32 s84, 0x100\n" \
"v_mfma_f32_16x16x32_bf16 a[52:55], v[176:179], v[116:119], a[52:55]\n" \
"buffer_load_dwordx4 v24, s[76:79], s84 offen lds\n" \
"v_mfma_f32_16x16x32_bf16 a[48:51], v[116:119], v[172:175], a[48:51]\n" \
"ds_read_b64_tr_b16 v[84:85], v32 offset:32768\n" \
"v_mfma_f32_16x16x32_bf16 a[64:67], v[148:151], v[124:127], a[64:67]\n" \
"ds_read_b64_tr_b16 v[86:87], v32 offset:33792\n" \
"v_mfma_f32_16x16x32_bf16 a[96:99], v[148:151], v[128:131], a[96:99]\n" \
"ds_read_b128 v[248:251], v22 offset:32768\n" \
"v_mfma_f32_16x16x32_bf16 a[68:71], v[152:155], v[124:127], a[68:71]\n" \
"s_add_u32 m0, s30, 0x400\n" \
"s_mov_b32 s85, 0x2500\n" \
"v_mfma_f32_16x16x32_bf16 a[100:103], v[152:155], v[128:131], a[100:103]\n" \
"buffer_load_dwordx4 v25, s[76:79], s85 offen lds\n" \
"v_mfma_f32_16x16x32_bf16 a[72:75], v[156:159], v[124:127], a[72:75]\n" \
"ds_read_b64_tr_b16 v[88:89], v33 offset:32768\n" \
"v_mfma_f32_16x16x32_bf16 a[104:107], v[156:159], v[128:131], a[104:107]\n" \
"ds_read_b64_tr_b16 v[90:91], v33 offset:33792\n" \
"v_mfma_f32_16x16x32_bf16 a[76:79], v[160:163], v[124:127], a[76:79]\n" \
"s_add_u32 m0, s31, 0x0\n" \
"s_nop 0\n" \
"s_mov_b32 s84, 0x100000\n" \
"v_mfma_f32_16x16x32_bf16 a[108:111], v[160:163], v[128:131], a[108:111]\n" \
"buffer_load_dwordx4 v26, s[68:71], s84 offen lds\n" \
"v_mfma_f32_16x16x32_bf16 a[80:83], v[164:167], v[120:123], a[80:83]\n" \
"ds_read_b64_tr_b16 v[92:93], v34 offset:32768\n" \
"v_mfma_f32_16x16x32_bf16 a[84:87], v[168:171], v[120:123], a[84:87]\n" \
"ds_read_b64_tr_b16 v[94:95], v34 offset:33792\n" \
"v_mfma_f32_16x16x32_bf16 a[116:119], v[176:179], v[120:123], a[116:119]\n" \
"s_add_u32 m0, s30, 0x800\n" \
"s_mov_b32 s85, 0x4900\n" \
"v_mfma_f32_16x16x32_bf16 a[112:115], v[120:123], v[172:175], a[112:115]\n" \
"buffer_load_dwordx4 v24, s[76:79], s85 offen lds\n" \
"v_mfma_f32_16x16x32_bf16 a[128:131], v[148:151], v[132:135], a[128:131]\n" \
"ds_read_b64_tr_b16 v[96:97], v35 offset:32768\n" \
"v_mfma_f32_16x16x32_bf16 a[160:163], v[148:151], v[136:139], a[160:163]\n" \
"ds_read_b64_tr_b16 v[98:99], v35 offset:33792\n" \
"v_mfma_f32_16x16x32_bf16 a[132:135], v[152:155], v[132:135], a[132:135]\n" \
"s_add_u32 m0, s30, 0xc00\n" \
"s_mov_b32 s84, 0x6d00\n" \
"v_mfma_f32_16x16x32_bf16 a[164:167], v[152:155], v[136:139], a[164:167]\n" \
"buffer_load_dwordx4 v25, s[76:79], s84 offen lds\n" \
"v_mfma_f32_16x16x32_bf16 a[136:139], v[156:159], v[132:135], a[136:139]\n" \
"ds_read_b64_tr_b16 v[100:101], v36 offset:32768\n" \
"v_mfma_f32_16x16x32_bf16 a[168:171], v[156:159], v[136:139], a[168:171]\n" \
"ds_read_b64_tr_b16 v[102:103], v36 offset:33280\n" \
"v_mfma_f32_16x16x32_bf16 a[140:143], v[160:163], v[132:135], a[140:143]\n" \
"ds_read_b128 v[52:55], v28 offset:32768\n" \
"v_mfma_f32_16x16x32_bf16 a[172:175], v[160:163], v[136:139], a[172:175]\n" \
"s_add_u32 m0, s31, 0x400\n" \
"s_mov_b32 s85, 0x108000\n" \
"v_mfma_f32_16x16x32_bf16 a[144:147], v[164:167], v[124:127], a[144:147]\n" \
"buffer_load_dwordx4 v26, s[68:71], s85 offen lds\n" \
"v_mfma_f32_16x16x32_bf16 a[148:151], v[168:171], v[124:127], a[148:151]\n" \
"ds_read_b128 v[56:59], v28 offset:34816\n" \
"v_mfma_f32_16x16x32_bf16 a[180:183], v[176:179], v[124:127], a[180:183]\n" \
"ds_read_b128 v[60:63], v28 offset:36864\n" \
"v_mfma_f32_16x16x32_bf16 a[176:179], v[124:127], v[172:175], a[176:179]\n" \
"s_add_u32 m0, s30, 0x1000\n" \
"s_mov_b32 s84, 0x9100\n" \
"v_mfma_f32_16x16x32_bf16 a[192:195], v[148:151], v[140:143], a[192:195]\n" \
"buffer_load_dwordx4 v24, s[76:79], s84 offen lds\n" \
"v_mfma_f32_16x16x32_bf16 a[224:227], v[148:151], v[144:147], a[224:227]\n" \
"ds_read_b128 v[64:67], v28 offset:38912\n" \
"v_mfma_f32_16x16x32_bf16 a[196:199], v[152:155], v[140:143], a[196:199]\n" \
"ds_read_b128 v[68:71], v28 offset:40960\n" \
"v_mfma_f32_16x16x32_bf16 a[228:231], v[152:155], v[144:147], a[228:231]\n" \
"s_add_u32 m0, s30, 0x1400\n" \
"s_mov_b32 s85, 0xb500\n" \
"v_mfma_f32_16x16x32_bf16 a[200:203], v[156:159], v[140:143], a[200:203]\n" \
"buffer_load_dwordx4 v25, s[76:79], s85 offen lds\n" \
"v_mfma_f32_16x16x32_bf16 a[232:235], v[156:159], v[144:147], a[232:235]\n" \
"ds_read_b128 v[72:75], v28 offset:43008\n" \
"v_mfma_f32_16x16x32_bf16 a[204:207], v[160:163], v[140:143], a[204:207]\n" \
"ds_read_b128 v[76:79], v28 offset:45056\n" \
"v_mfma_f32_16x16x32_bf16 a[236:239], v[160:163], v[144:147], a[236:239]\n" \
"s_add_u32 m0, s31, 0x800\n" \
"s_mov_b32 s84, 0x110000\n" \
"v_mfma_f32_16x16x32_bf16 a[208:211], v[164:167], v[128:131], a[208:211]\n" \
"buffer_load_dwordx4 v27, s[68:71], s84 offen lds\n" \
"v_mfma_f32_16x16x32_bf16 a[212:215], v[168:171], v[128:131], a[212:215]\n" \
"ds_read_b128 v[80:83], v28 offset:47104\n" \
"v_mfma_f32_16x16x32_bf16 a[244:247], v[176:179], v[128:131], a[244:247]\n" \
"v_mfma_f32_16x16x32_bf16 a[240:243], v[128:131], v[172:175], a[240:243]\n" \
"s_waitcnt lgkmcnt(15)\n" \
"v_mfma_f32_16x16x32_bf16 a[24:27], v[104:107], v[248:251], a[24:27]\n" \
"s_add_u32 m0, s30, 0x1800\n" \
"s_mov_b32 s84, 0xd900\n" \
"v_mfma_f32_16x16x32_bf16 a[28:31], v[248:251], v[108:111], a[28:31]\n" \
"buffer_load_dwordx4 v24, s[76:79], s84 offen lds\n" \
"v_mfma_f32_16x16x32_bf16 a[56:59], v[112:115], v[248:251], a[56:59]\n" \
"ds_read_b64_tr_b16 v[168:169], v37 offset:36864\n" \
"s_movk_i32 s15, 0x4000\n" \
"s_waitcnt lgkmcnt(8)\n" \
"v_mfma_f32_16x16x32_bf16 a[0:3], v[84:87], v[52:55], a[0:3]\n" \
"s_add_u32 m0, s30, 0x1c00\n" \
"s_mov_b32 s85, 0xfd00\n" \
"s_waitcnt lgkmcnt(7)\n" \
"v_mfma_f32_16x16x32_bf16 a[32:35], v[84:87], v[56:59], a[32:35]\n" \
"buffer_load_dwordx4 v25, s[76:79], s85 offen lds\n" \
"v_mfma_f32_16x16x32_bf16 a[4:7], v[88:91], v[52:55], a[4:7]\n" \
"ds_read_b64_tr_b16 v[170:171], v37 offset:37376\n" \
"s_lshl_b32 s4, s2, 2\n" \
"v_mfma_f32_16x16x32_bf16 a[36:39], v[88:91], v[56:59], a[36:39]\n" \
"s_add_u32 m0, s31, 0xc00\n" \
"s_mov_b32 s84, 0x118000\n" \
"v_mfma_f32_16x16x32_bf16 a[8:11], v[92:95], v[52:55], a[8:11]\n" \
"buffer_load_dwordx4 v27, s[68:71], s84 offen lds\n" \
"v_mfma_f32_16x16x32_bf16 a[40:43], v[92:95], v[56:59], a[40:43]\n" \
"ds_read_b64_tr_b16 v[172:173], v38 offset:36864\n" \
"s_and_b32 s4, s4, 28\n" \
"v_mfma_f32_16x16x32_bf16 a[12:15], v[96:99], v[52:55], a[12:15]\n" \
"ds_read_b64_tr_b16 v[174:175], v38 offset:37376\n" \
"s_bfe_u32 s5, s2, 0x20003\n" \
"v_mfma_f32_16x16x32_bf16 a[44:47], v[96:99], v[56:59], a[44:47]\n" \
"ds_read_b64_tr_b16 v[176:177], v39 offset:36864\n" \
"s_or_b32 s4, s4, s5\n" \
"v_mfma_f32_16x16x32_bf16 a[16:19], v[100:103], v[52:55], a[16:19]\n" \
"ds_read_b64_tr_b16 v[178:179], v39 offset:37376\n" \
"s_lshr_b32 s5, s2, 5\n" \
"v_mfma_f32_16x16x32_bf16 a[20:23], v[104:107], v[52:55], a[20:23]\n" \
"ds_read_b64_tr_b16 v[148:149], v32 offset:40960\n" \
"s_lshr_b32 s19, s3, 1\n" \
"v_mfma_f32_16x16x32_bf16 a[52:55], v[112:115], v[52:55], a[52:55]\n" \
"ds_read_b64_tr_b16 v[150:151], v32 offset:41984\n" \
"s_lshl_b32 s18, s5, 1\n" \
"v_mfma_f32_16x16x32_bf16 a[48:51], v[52:55], v[108:111], a[48:51]\n" \
"ds_read_b128 v[248:251], v23 offset:32768\n" \
"s_lshl_b32 s6, s4, 20\n" \
"s_waitcnt lgkmcnt(14)\n" \
"v_mfma_f32_16x16x32_bf16 a[64:67], v[84:87], v[60:63], a[64:67]\n" \
"ds_read_b64_tr_b16 v[152:153], v33 offset:40960\n" \
"s_lshl_b32 s7, s4, 8\n" \
"s_waitcnt lgkmcnt(14)\n" \
"v_mfma_f32_16x16x32_bf16 a[96:99], v[84:87], v[64:67], a[96:99]\n" \
"ds_read_b64_tr_b16 v[154:155], v33 offset:41984\n" \
"s_and_b32 s13, s3, 1\n" \
"v_mfma_f32_16x16x32_bf16 a[68:71], v[88:91], v[60:63], a[68:71]\n" \
"ds_read_b64_tr_b16 v[156:157], v34 offset:40960\n" \
"s_add_i32 s14, s18, s19\n" \
"v_mfma_f32_16x16x32_bf16 a[100:103], v[88:91], v[64:67], a[100:103]\n" \
"ds_read_b64_tr_b16 v[158:159], v34 offset:41984\n" \
"v_mfma_f32_16x16x32_bf16 a[72:75], v[92:95], v[60:63], a[72:75]\n" \
"s_add_u32 s6, s26, s6\n" \
"s_addc_u32 s9, s27, 0\n" \
"v_mfma_f32_16x16x32_bf16 a[104:107], v[92:95], v[64:67], a[104:107]\n" \
"ds_read_b64_tr_b16 v[160:161], v35 offset:40960\n" \
"v_mfma_f32_16x16x32_bf16 a[76:79], v[96:99], v[60:63], a[76:79]\n" \
"s_add_u32 s89, s41, 0x1000\n" \
"buffer_store_dwordx4 v[200:203], v20, s[80:83], s89 offen offset:0 sc1\n" \
"v_mfma_f32_16x16x32_bf16 a[108:111], v[96:99], v[64:67], a[108:111]\n" \
"ds_read_b64_tr_b16 v[162:163], v35 offset:41984\n" \
"v_mfma_f32_16x16x32_bf16 a[80:83], v[100:103], v[56:59], a[80:83]\n" \
"ds_read_b64_tr_b16 v[164:165], v36 offset:36864\n" \
"v_mfma_f32_16x16x32_bf16 a[84:87], v[104:107], v[56:59], a[84:87]\n" \
"ds_read_b64_tr_b16 v[166:167], v36 offset:37376\n" \
"v_mfma_f32_16x16x32_bf16 a[116:119], v[112:115], v[56:59], a[116:119]\n" \
"s_add_u32 s8, s6, 0x1000000\n" \
"s_addc_u32 s6, s9, 0\n" \
"v_mfma_f32_16x16x32_bf16 a[112:115], v[56:59], v[108:111], a[112:115]\n" \
"ds_read_b128 v[116:119], v29 offset:32768\n" \
"s_and_b32 s9, s6, 0xffff\n" \
"s_waitcnt lgkmcnt(15)\n" \
"v_mfma_f32_16x16x32_bf16 a[128:131], v[84:87], v[68:71], a[128:131]\n" \
"s_lshl_b32 s6, s5, 16\n" \
"s_sub_i32 s10, 0x100000, s6\n" \
"v_mfma_f32_16x16x32_bf16 a[160:163], v[84:87], v[72:75], a[160:163]\n" \
"ds_read_b128 v[120:123], v29 offset:34816\n" \
"s_lshl_b32 s6, s3, 11\n" \
"v_mfma_f32_16x16x32_bf16 a[132:135], v[88:91], v[68:71], a[132:135]\n" \
"s_add_u32 s88, s41, 0x1000\n" \
"buffer_store_dwordx4 v[204:207], v20, s[80:83], s88 offen offset:1024 sc1\n" \
"v_mfma_f32_16x16x32_bf16 a[164:167], v[88:91], v[72:75], a[164:167]\n" \
"ds_read_b128 v[124:127], v29 offset:36864\n" \
"s_or_b32 s12, s6, 0xc000\n" \
"v_mfma_f32_16x16x32_bf16 a[136:139], v[92:95], v[68:71], a[136:139]\n" \
"ds_read_b128 v[128:131], v29 offset:38912\n" \
"s_mov_b32 s6, 0x8000\n" \
"v_mfma_f32_16x16x32_bf16 a[168:171], v[92:95], v[72:75], a[168:171]\n" \
"ds_read_b128 v[132:135], v29 offset:40960\n" \
"v_mfma_f32_16x16x32_bf16 a[140:143], v[96:99], v[68:71], a[140:143]\n" \
"s_cmp_lg_u32 s5, 0\n" \
"s_cselect_b32 s17, s15, s6\n" \
"v_mfma_f32_16x16x32_bf16 a[172:175], v[96:99], v[72:75], a[172:175]\n" \
"ds_read_b128 v[136:139], v29 offset:43008\n" \
"s_lshl_b32 s6, s3, 1\n" \
"v_mfma_f32_16x16x32_bf16 a[144:147], v[100:103], v[60:63], a[144:147]\n" \
"ds_read_b128 v[140:143], v29 offset:45056\n" \
"s_lshl_b32 s5, s5, 3\n" \
"v_mfma_f32_16x16x32_bf16 a[148:151], v[104:107], v[60:63], a[148:151]\n" \
"s_add_u32 s89, s41, 0x1000\n" \
"buffer_store_dwordx4 v[208:211], v20, s[80:83], s89 offen offset:2048 sc1\n" \
"v_mfma_f32_16x16x32_bf16 a[180:183], v[112:115], v[60:63], a[180:183]\n" \
"ds_read_b128 v[144:147], v29 offset:47104\n" \
"s_and_b32 s6, s6, 4\n" \
"v_mfma_f32_16x16x32_bf16 a[176:179], v[60:63], v[108:111], a[176:179]\n" \
"s_or_b32 s15, s5, s6\n" \
"s_sub_i32 s5, s13, s5\n" \
"v_mfma_f32_16x16x32_bf16 a[192:195], v[84:87], v[76:79], a[192:195]\n" \
"s_or_b32 s5, s5, s6\n" \
"s_or_b32 s6, s15, s13\n" \
"v_mfma_f32_16x16x32_bf16 a[224:227], v[84:87], v[80:83], a[224:227]\n" \
"s_lshl_b32 s4, s4, 2\n" \
"s_mulk_i32 s6, 0x92\n" \
"v_mfma_f32_16x16x32_bf16 a[196:199], v[88:91], v[76:79], a[196:199]\n" \
"s_add_i32 s4, s4, 18\n" \
"s_mulk_i32 s5, 0x92\n" \
"v_mfma_f32_16x16x32_bf16 a[228:231], v[88:91], v[80:83], a[228:231]\n" \
"s_lshl_b32 s16, s19, 14\n" \
"s_lshl_b32 s15, s13, 12\n" \
"v_mfma_f32_16x16x32_bf16 a[200:203], v[92:95], v[76:79], a[200:203]\n" \
"s_add_u32 s88, s41, 0x1000\n" \
"buffer_store_dwordx4 v[212:215], v20, s[80:83], s88 offen offset:3072 sc1\n" \
"v_mfma_f32_16x16x32_bf16 a[232:235], v[92:95], v[80:83], a[232:235]\n" \
"s_add_i32 s6, s4, s6\n" \
"s_add_i32 s5, s5, s4\n" \
"v_mfma_f32_16x16x32_bf16 a[204:207], v[96:99], v[76:79], a[204:207]\n" \
"s_mul_i32 s19, s19, 0xe000\n" \
"s_mov_b32 s11, 0x20000\n" \
"v_mfma_f32_16x16x32_bf16 a[236:239], v[96:99], v[80:83], a[236:239]\n" \
"s_lshl_b32 s4, s3, 8\n" \
"s_or_b32 s22, s15, s16\n" \
"v_mfma_f32_16x16x32_bf16 a[208:211], v[100:103], v[64:67], a[208:211]\n" \
"s_or_b32 s23, s19, s15\n" \
"s_lshl_b32 s60, s6, 10\n" \
"v_mfma_f32_16x16x32_bf16 a[212:215], v[104:107], v[64:67], a[212:215]\n" \
"s_lshl_b32 s61, s5, 10\n" \
"s_lshl_b32 s14, s14, 15\n" \
"v_mfma_f32_16x16x32_bf16 a[244:247], v[112:115], v[64:67], a[244:247]\n" \
"s_or_b32 s20, s4, 0x26c00\n" \
"s_or_b32 s13, s22, 0x8000\n" \
"v_mfma_f32_16x16x32_bf16 a[240:243], v[64:67], v[108:111], a[240:243]\n" \
"s_waitcnt vmcnt(4) lgkmcnt(0)\n" \
"s_barrier\n" \
"v_mfma_f32_16x16x32_bf16 a[24:27], v[168:171], v[248:251], a[24:27]\n" \
"s_add_u32 m0, s40, 0x8000\n" \
"s_add_u32 s85, s39, 0x6000\n" \
"v_mfma_f32_16x16x32_bf16 a[28:31], v[248:251], v[172:175], a[28:31]\n" \
"buffer_load_dwordx4 v20, s[80:83], s85 offen sc1 lds\n" \
"v_mfma_f32_16x16x32_bf16 a[56:59], v[176:179], v[248:251], a[56:59]\n" \
"ds_read_b64_tr_b16 v[104:105], v37 offset:0\n" \
"s_add_i32 s22, s22, 0x14000\n" \
"v_mfma_f32_16x16x32_bf16 a[0:3], v[148:151], v[116:119], a[0:3]\n" \
"ds_read_b64_tr_b16 v[106:107], v37 offset:512\n" \
"s_bitset1_b32 s23, 16\n" \
"v_mfma_f32_16x16x32_bf16 a[32:35], v[148:151], v[120:123], a[32:35]\n" \
"s_add_u32 m0, s40, 0x9000\n" \
"s_add_u32 s84, s39, 0x7000\n" \
"v_mfma_f32_16x16x32_bf16 a[4:7], v[152:155], v[116:119], a[4:7]\n" \
"buffer_load_dwordx4 v20, s[80:83], s84 offen sc1 lds\n" \
"v_mfma_f32_16x16x32_bf16 a[36:39], v[152:155], v[120:123], a[36:39]\n" \
"ds_read_b64_tr_b16 v[108:109], v38 offset:0\n" \
"s_or_b32 s24, s7, 0x5be8020\n" \
"v_mfma_f32_16x16x32_bf16 a[8:11], v[156:159], v[116:119], a[8:11]\n" \
"ds_read_b64_tr_b16 v[110:111], v38 offset:512\n" \
"s_and_b32 s57, s27, 0xffff\n" \
"v_mfma_f32_16x16x32_bf16 a[40:43], v[156:159], v[120:123], a[40:43]\n" \
"s_add_u32 m0, s40, 0xb000\n" \
"s_mov_b32 s85, 0x180\n" \
"v_mfma_f32_16x16x32_bf16 a[12:15], v[160:163], v[116:119], a[12:15]\n" \
"buffer_load_dwordx4 v21, s[76:79], s85 offen lds\n" \
"v_mfma_f32_16x16x32_bf16 a[44:47], v[160:163], v[120:123], a[44:47]\n" \
"ds_read_b64_tr_b16 v[112:113], v39 offset:0\n" \
"s_mov_b32 s58, 0x6000000\n" \
"v_mfma_f32_16x16x32_bf16 a[16:19], v[164:167], v[116:119], a[16:19]\n" \
"ds_read_b64_tr_b16 v[114:115], v39 offset:512\n" \
"s_mov_b32 s56, s26\n" \
"v_mfma_f32_16x16x32_bf16 a[20:23], v[168:171], v[116:119], a[20:23]\n" \
"s_add_u32 m0, s30, 0x8000\n" \
"s_mov_b32 s84, 0x180\n" \
"v_mfma_f32_16x16x32_bf16 a[52:55], v[176:179], v[116:119], a[52:55]\n" \
"buffer_load_dwordx4 v24, s[76:79], s84 offen lds\n" \
"v_mfma_f32_16x16x32_bf16 a[48:51], v[116:119], v[172:175], a[48:51]\n" \
"ds_read_b64_tr_b16 v[84:85], v32 offset:0\n" \
"s_mov_b32 s59, s11\n" \
"v_mfma_f32_16x16x32_bf16 a[64:67], v[148:151], v[124:127], a[64:67]\n" \
"ds_read_b64_tr_b16 v[86:87], v32 offset:1024\n" \
"s_add_i32 s60, s60, 0x3000000\n" \
"v_mfma_f32_16x16x32_bf16 a[96:99], v[148:151], v[128:131], a[96:99]\n" \
"ds_read_b128 v[248:251], v22 offset:0\n" \
"s_add_i32 s61, s61, 0x411c000\n" \
"v_mfma_f32_16x16x32_bf16 a[68:71], v[152:155], v[124:127], a[68:71]\n" \
"s_add_u32 m0, s30, 0x8400\n" \
"s_mov_b32 s85, 0x2580\n" \
"v_mfma_f32_16x16x32_bf16 a[100:103], v[152:155], v[128:131], a[100:103]\n" \
"buffer_load_dwordx4 v25, s[76:79], s85 offen lds\n" \
"v_mfma_f32_16x16x32_bf16 a[72:75], v[156:159], v[124:127], a[72:75]\n" \
"ds_read_b64_tr_b16 v[88:89], v33 offset:0\n" \
"s_sub_i32 s19, 14, s18\n" \
"s_nop 0\n" \
"v_mfma_f32_16x16x32_bf16 a[104:107], v[156:159], v[128:131], a[104:107]\n" \
"ds_read_b64_tr_b16 v[90:91], v33 offset:1024\n" \
"s_mov_b32 s21, 0x71800000\n" \
"v_mfma_f32_16x16x32_bf16 a[76:79], v[160:163], v[124:127], a[76:79]\n" \
"s_add_u32 m0, s31, 0x8000\n" \
"s_mov_b32 s84, 0x180000\n" \
"v_mfma_f32_16x16x32_bf16 a[108:111], v[160:163], v[128:131], a[108:111]\n" \
"buffer_load_dwordx4 v26, s[68:71], s84 offen lds\n" \
"v_mfma_f32_16x16x32_bf16 a[80:83], v[164:167], v[120:123], a[80:83]\n" \
"ds_read_b64_tr_b16 v[92:93], v34 offset:0\n" \
"v_mfma_f32_16x16x32_bf16 a[84:87], v[168:171], v[120:123], a[84:87]\n" \
"ds_read_b64_tr_b16 v[94:95], v34 offset:1024\n" \
"v_mfma_f32_16x16x32_bf16 a[116:119], v[176:179], v[120:123], a[116:119]\n" \
"s_add_u32 m0, s30, 0x8800\n" \
"s_mov_b32 s85, 0x4980\n" \
"v_mfma_f32_16x16x32_bf16 a[112:115], v[120:123], v[172:175], a[112:115]\n" \
"buffer_load_dwordx4 v24, s[76:79], s85 offen lds\n" \
"v_mfma_f32_16x16x32_bf16 a[128:131], v[148:151], v[132:135], a[128:131]\n" \
"ds_read_b64_tr_b16 v[96:97], v35 offset:0\n" \
"v_mfma_f32_16x16x32_bf16 a[160:163], v[148:151], v[136:139], a[160:163]\n" \
"ds_read_b64_tr_b16 v[98:99], v35 offset:1024\n" \
"v_mfma_f32_16x16x32_bf16 a[132:135], v[152:155], v[132:135], a[132:135]\n" \
"s_add_u32 m0, s30, 0x8c00\n" \
"s_mov_b32 s84, 0x6d80\n" \
"v_mfma_f32_16x16x32_bf16 a[164:167], v[152:155], v[136:139], a[164:167]\n" \
"buffer_load_dwordx4 v25, s[76:79], s84 offen lds\n" \
"v_mfma_f32_16x16x32_bf16 a[136:139], v[156:159], v[132:135], a[136:139]\n" \
"ds_read_b64_tr_b16 v[100:101], v36 offset:0\n" \
"v_mfma_f32_16x16x32_bf16 a[168:171], v[156:159], v[136:139], a[168:171]\n" \
"ds_read_b64_tr_b16 v[102:103], v36 offset:512\n" \
"v_mfma_f32_16x16x32_bf16 a[140:143], v[160:163], v[132:135], a[140:143]\n" \
"ds_read_b128 v[52:55], v28 offset:0\n" \
"v_mfma_f32_16x16x32_bf16 a[172:175], v[160:163], v[136:139], a[172:175]\n" \
"s_add_u32 m0, s31, 0x8400\n" \
"s_mov_b32 s85, 0x188000\n" \
"v_mfma_f32_16x16x32_bf16 a[144:147], v[164:167], v[124:127], a[144:147]\n" \
"buffer_load_dwordx4 v26, s[68:71], s85 offen lds\n" \
"v_mfma_f32_16x16x32_bf16 a[148:151], v[168:171], v[124:127], a[148:151]\n" \
"ds_read_b128 v[56:59], v28 offset:2048\n" \
"v_mfma_f32_16x16x32_bf16 a[180:183], v[176:179], v[124:127], a[180:183]\n" \
"ds_read_b128 v[60:63], v28 offset:4096\n" \
"v_mfma_f32_16x16x32_bf16 a[176:179], v[124:127], v[172:175], a[176:179]\n" \
"s_add_u32 m0, s30, 0x9000\n" \
"s_mov_b32 s84, 0x9180\n" \
"v_mfma_f32_16x16x32_bf16 a[192:195], v[148:151], v[140:143], a[192:195]\n" \
"buffer_load_dwordx4 v24, s[76:79], s84 offen lds\n" \
"v_mfma_f32_16x16x32_bf16 a[224:227], v[148:151], v[144:147], a[224:227]\n" \
"ds_read_b128 v[64:67], v28 offset:6144\n" \
"v_mfma_f32_16x16x32_bf16 a[196:199], v[152:155], v[140:143], a[196:199]\n" \
"ds_read_b128 v[68:71], v28 offset:8192\n" \
"v_mfma_f32_16x16x32_bf16 a[228:231], v[152:155], v[144:147], a[228:231]\n" \
"s_add_u32 m0, s30, 0x9400\n" \
"s_mov_b32 s85, 0xb580\n" \
"v_mfma_f32_16x16x32_bf16 a[200:203], v[156:159], v[140:143], a[200:203]\n" \
"buffer_load_dwordx4 v25, s[76:79], s85 offen lds\n" \
"v_mfma_f32_16x16x32_bf16 a[232:235], v[156:159], v[144:147], a[232:235]\n" \
"ds_read_b128 v[72:75], v28 offset:10240\n" \
"v_mfma_f32_16x16x32_bf16 a[204:207], v[160:163], v[140:143], a[204:207]\n" \
"ds_read_b128 v[76:79], v28 offset:12288\n" \
"v_mfma_f32_16x16x32_bf16 a[236:239], v[160:163], v[144:147], a[236:239]\n" \
"s_add_u32 m0, s31, 0x8800\n" \
"s_mov_b32 s84, 0x190000\n" \
"v_mfma_f32_16x16x32_bf16 a[208:211], v[164:167], v[128:131], a[208:211]\n" \
"buffer_load_dwordx4 v27, s[68:71], s84 offen lds\n" \
"v_mfma_f32_16x16x32_bf16 a[212:215], v[168:171], v[128:131], a[212:215]\n" \
"ds_read_b128 v[80:83], v28 offset:14336\n" \
"v_mfma_f32_16x16x32_bf16 a[244:247], v[176:179], v[128:131], a[244:247]\n" \
"v_mfma_f32_16x16x32_bf16 a[240:243], v[128:131], v[172:175], a[240:243]\n" \
"s_waitcnt lgkmcnt(15)\n" \
"v_mfma_f32_16x16x32_bf16 a[24:27], v[104:107], v[248:251], a[24:27]\n" \
"s_add_u32 m0, s30, 0x9800\n" \
"s_mov_b32 s84, 0xd980\n" \
"v_mfma_f32_16x16x32_bf16 a[28:31], v[248:251], v[108:111], a[28:31]\n" \
"buffer_load_dwordx4 v24, s[76:79], s84 offen lds\n" \
"v_mfma_f32_16x16x32_bf16 a[56:59], v[112:115], v[248:251], a[56:59]\n" \
"ds_read_b64_tr_b16 v[168:169], v37 offset:4096\n" \
"s_waitcnt lgkmcnt(8)\n" \
"v_mfma_f32_16x16x32_bf16 a[0:3], v[84:87], v[52:55], a[0:3]\n" \
"s_add_u32 m0, s30, 0x9c00\n" \
"s_mov_b32 s85, 0xfd80\n" \
"s_waitcnt lgkmcnt(7)\n" \
"s_nop 0\n" \
"v_mfma_f32_16x16x32_bf16 a[32:35], v[84:87], v[56:59], a[32:35]\n" \
"buffer_load_dwordx4 v25, s[76:79], s85 offen lds\n" \
"v_mfma_f32_16x16x32_bf16 a[4:7], v[88:91], v[52:55], a[4:7]\n" \
"ds_read_b64_tr_b16 v[170:171], v37 offset:4608\n" \
"v_mfma_f32_16x16x32_bf16 a[36:39], v[88:91], v[56:59], a[36:39]\n" \
"s_add_u32 m0, s31, 0x8c00\n" \
"s_mov_b32 s84, 0x198000\n" \
"v_mfma_f32_16x16x32_bf16 a[8:11], v[92:95], v[52:55], a[8:11]\n" \
"buffer_load_dwordx4 v27, s[68:71], s84 offen lds\n" \
"v_mfma_f32_16x16x32_bf16 a[40:43], v[92:95], v[56:59], a[40:43]\n" \
"ds_read_b64_tr_b16 v[172:173], v38 offset:4096\n" \
"v_mfma_f32_16x16x32_bf16 a[12:15], v[96:99], v[52:55], a[12:15]\n" \
"ds_read_b64_tr_b16 v[174:175], v38 offset:4608\n" \
"v_mfma_f32_16x16x32_bf16 a[44:47], v[96:99], v[56:59], a[44:47]\n" \
"ds_read_b64_tr_b16 v[176:177], v39 offset:4096\n" \
"v_mfma_f32_16x16x32_bf16 a[16:19], v[100:103], v[52:55], a[16:19]\n" \
"ds_read_b64_tr_b16 v[178:179], v39 offset:4608\n" \
"v_mfma_f32_16x16x32_bf16 a[20:23], v[104:107], v[52:55], a[20:23]\n" \
"ds_read_b64_tr_b16 v[148:149], v32 offset:8192\n" \
"v_mfma_f32_16x16x32_bf16 a[52:55], v[112:115], v[52:55], a[52:55]\n" \
"ds_read_b64_tr_b16 v[150:151], v32 offset:9216\n" \
"v_mfma_f32_16x16x32_bf16 a[48:51], v[52:55], v[108:111], a[48:51]\n" \
"ds_read_b128 v[248:251], v23 offset:0\n" \
"s_waitcnt lgkmcnt(14)\n" \
"v_mfma_f32_16x16x32_bf16 a[64:67], v[84:87], v[60:63], a[64:67]\n" \
"ds_read_b64_tr_b16 v[152:153], v33 offset:8192\n" \
"s_waitcnt lgkmcnt(14)\n" \
"v_mfma_f32_16x16x32_bf16 a[96:99], v[84:87], v[64:67], a[96:99]\n" \
"ds_read_b64_tr_b16 v[154:155], v33 offset:9216\n" \
"v_mfma_f32_16x16x32_bf16 a[68:71], v[88:91], v[60:63], a[68:71]\n" \
"ds_read_b64_tr_b16 v[156:157], v34 offset:8192\n" \
"v_mfma_f32_16x16x32_bf16 a[100:103], v[88:91], v[64:67], a[100:103]\n" \
"ds_read_b64_tr_b16 v[158:159], v34 offset:9216\n" \
"v_mfma_f32_16x16x32_bf16 a[72:75], v[92:95], v[60:63], a[72:75]\n" \
"v_mfma_f32_16x16x32_bf16 a[104:107], v[92:95], v[64:67], a[104:107]\n" \
"ds_read_b64_tr_b16 v[160:161], v35 offset:8192\n" \
"v_mfma_f32_16x16x32_bf16 a[76:79], v[96:99], v[60:63], a[76:79]\n" \
"s_add_u32 s89, s41, 0x2000\n" \
"buffer_store_dwordx4 v[216:219], v20, s[80:83], s89 offen offset:0 sc1\n" \
"v_mfma_f32_16x16x32_bf16 a[108:111], v[96:99], v[64:67], a[108:111]\n" \
"ds_read_b64_tr_b16 v[162:163], v35 offset:9216\n" \
"v_mfma_f32_16x16x32_bf16 a[80:83], v[100:103], v[56:59], a[80:83]\n" \
"ds_read_b64_tr_b16 v[164:165], v36 offset:4096\n" \
"v_mfma_f32_16x16x32_bf16 a[84:87], v[104:107], v[56:59], a[84:87]\n" \
"ds_read_b64_tr_b16 v[166:167], v36 offset:4608\n" \
"v_mfma_f32_16x16x32_bf16 a[116:119], v[112:115], v[56:59], a[116:119]\n" \
"v_mfma_f32_16x16x32_bf16 a[112:115], v[56:59], v[108:111], a[112:115]\n" \
"ds_read_b128 v[116:119], v29 offset:0\n" \
"s_waitcnt lgkmcnt(15)\n" \
"s_nop 0\n" \
"v_mfma_f32_16x16x32_bf16 a[128:131], v[84:87], v[68:71], a[128:131]\n" \
"v_mfma_f32_16x16x32_bf16 a[160:163], v[84:87], v[72:75], a[160:163]\n" \
"ds_read_b128 v[120:123], v29 offset:2048\n" \
"v_mfma_f32_16x16x32_bf16 a[132:135], v[88:91], v[68:71], a[132:135]\n" \
"s_add_u32 s88, s41, 0x2000\n" \
"buffer_store_dwordx4 v[220:223], v20, s[80:83], s88 offen offset:1024 sc1\n" \
"v_mfma_f32_16x16x32_bf16 a[164:167], v[88:91], v[72:75], a[164:167]\n" \
"ds_read_b128 v[124:127], v29 offset:4096\n" \
"v_mfma_f32_16x16x32_bf16 a[136:139], v[92:95], v[68:71], a[136:139]\n" \
"ds_read_b128 v[128:131], v29 offset:6144\n" \
"v_mfma_f32_16x16x32_bf16 a[168:171], v[92:95], v[72:75], a[168:171]\n" \
"ds_read_b128 v[132:135], v29 offset:8192\n" \
"v_mfma_f32_16x16x32_bf16 a[140:143], v[96:99], v[68:71], a[140:143]\n" \
"v_mfma_f32_16x16x32_bf16 a[172:175], v[96:99], v[72:75], a[172:175]\n" \
"ds_read_b128 v[136:139], v29 offset:10240\n" \
"v_mfma_f32_16x16x32_bf16 a[144:147], v[100:103], v[60:63], a[144:147]\n" \
"ds_read_b128 v[140:143], v29 offset:12288\n" \
"v_mfma_f32_16x16x32_bf16 a[148:151], v[104:107], v[60:63], a[148:151]\n" \
"s_add_u32 s89, s41, 0x2000\n" \
"buffer_store_dwordx4 v[224:227], v20, s[80:83], s89 offen offset:2048 sc1\n" \
"v_mfma_f32_16x16x32_bf16 a[180:183], v[112:115], v[60:63], a[180:183]\n" \
"ds_read_b128 v[144:147], v29 offset:14336\n" \
"v_mfma_f32_16x16x32_bf16 a[176:179], v[60:63], v[108:111], a[176:179]\n" \
"v_mfma_f32_16x16x32_bf16 a[192:195], v[84:87], v[76:79], a[192:195]\n" \
"v_mfma_f32_16x16x32_bf16 a[224:227], v[84:87], v[80:83], a[224:227]\n" \
"v_mfma_f32_16x16x32_bf16 a[196:199], v[88:91], v[76:79], a[196:199]\n" \
"v_mfma_f32_16x16x32_bf16 a[228:231], v[88:91], v[80:83], a[228:231]\n" \
"v_mfma_f32_16x16x32_bf16 a[200:203], v[92:95], v[76:79], a[200:203]\n" \
"s_add_u32 s88, s41, 0x2000\n" \
"buffer_store_dwordx4 v[228:231], v20, s[80:83], s88 offen offset:3072 sc1\n" \
"v_mfma_f32_16x16x32_bf16 a[232:235], v[92:95], v[80:83], a[232:235]\n" \
"v_mfma_f32_16x16x32_bf16 a[204:207], v[96:99], v[76:79], a[204:207]\n" \
"v_mfma_f32_16x16x32_bf16 a[236:239], v[96:99], v[80:83], a[236:239]\n" \
"v_mfma_f32_16x16x32_bf16 a[208:211], v[100:103], v[64:67], a[208:211]\n" \
"v_mfma_f32_16x16x32_bf16 a[212:215], v[104:107], v[64:67], a[212:215]\n" \
"v_mfma_f32_16x16x32_bf16 a[244:247], v[112:115], v[64:67], a[244:247]\n" \
"v_mfma_f32_16x16x32_bf16 a[240:243], v[64:67], v[108:111], a[240:243]\n" \
"s_waitcnt vmcnt(4) lgkmcnt(0)\n" \
"s_barrier\n" \
"v_mfma_f32_16x16x32_bf16 a[24:27], v[168:171], v[248:251], a[24:27]\n" \
"s_add_u32 m0, s40, 0x0\n" \
"s_nop 0\n" \
"s_add_u32 s85, s39, 0x8000\n" \
"v_mfma_f32_16x16x32_bf16 a[28:31], v[248:251], v[172:175], a[28:31]\n" \
"buffer_load_dwordx4 v20, s[80:83], s85 offen sc1 lds\n" \
"v_mfma_f32_16x16x32_bf16 a[56:59], v[176:179], v[248:251], a[56:59]\n" \
"ds_read_b64_tr_b16 v[104:105], v37 offset:32768\n" \
"v_mfma_f32_16x16x32_bf16 a[0:3], v[148:151], v[116:119], a[0:3]\n" \
"ds_read_b64_tr_b16 v[106:107], v37 offset:33280\n" \
"v_mfma_f32_16x16x32_bf16 a[32:35], v[148:151], v[120:123], a[32:35]\n" \
"s_add_u32 m0, s40, 0x1000\n" \
"s_add_u32 s84, s39, 0x9000\n" \
"v_mfma_f32_16x16x32_bf16 a[4:7], v[152:155], v[116:119], a[4:7]\n" \
"buffer_load_dwordx4 v20, s[80:83], s84 offen sc1 lds\n" \
"v_mfma_f32_16x16x32_bf16 a[36:39], v[152:155], v[120:123], a[36:39]\n" \
"ds_read_b64_tr_b16 v[108:109], v38 offset:32768\n" \
"v_mfma_f32_16x16x32_bf16 a[8:11], v[156:159], v[116:119], a[8:11]\n" \
"ds_read_b64_tr_b16 v[110:111], v38 offset:33280\n" \
"v_mfma_f32_16x16x32_bf16 a[40:43], v[156:159], v[120:123], a[40:43]\n" \
"s_add_u32 m0, s40, 0x3000\n" \
"s_mov_b32 s85, 0x200\n" \
"v_mfma_f32_16x16x32_bf16 a[12:15], v[160:163], v[116:119], a[12:15]\n" \
"buffer_load_dwordx4 v21, s[76:79], s85 offen lds\n" \
"v_mfma_f32_16x16x32_bf16 a[44:47], v[160:163], v[120:123], a[44:47]\n" \
"ds_read_b64_tr_b16 v[112:113], v39 offset:32768\n" \
"v_mfma_f32_16x16x32_bf16 a[16:19], v[164:167], v[116:119], a[16:19]\n" \
"ds_read_b64_tr_b16 v[114:115], v39 offset:33280\n" \
"v_mfma_f32_16x16x32_bf16 a[20:23], v[168:171], v[116:119], a[20:23]\n" \
"s_add_u32 m0, s30, 0x0\n" \
"s_nop 0\n" \
"s_mov_b32 s84, 0x200\n" \
"v_mfma_f32_16x16x32_bf16 a[52:55], v[176:179], v[116:119], a[52:55]\n" \
"buffer_load_dwordx4 v24, s[76:79], s84 offen lds\n" \
"v_mfma_f32_16x16x32_bf16 a[48:51], v[116:119], v[172:175], a[48:51]\n" \
"ds_read_b64_tr_b16 v[84:85], v32 offset:32768\n" \
"v_mfma_f32_16x16x32_bf16 a[64:67], v[148:151], v[124:127], a[64:67]\n" \
"ds_read_b64_tr_b16 v[86:87], v32 offset:33792\n" \
"v_mfma_f32_16x16x32_bf16 a[96:99], v[148:151], v[128:131], a[96:99]\n" \
"ds_read_b128 v[248:251], v22 offset:32768\n" \
"v_mfma_f32_16x16x32_bf16 a[68:71], v[152:155], v[124:127], a[68:71]\n" \
"s_add_u32 m0, s30, 0x400\n" \
"s_mov_b32 s85, 0x2600\n" \
"v_mfma_f32_16x16x32_bf16 a[100:103], v[152:155], v[128:131], a[100:103]\n" \
"buffer_load_dwordx4 v25, s[76:79], s85 offen lds\n" \
"v_mfma_f32_16x16x32_bf16 a[72:75], v[156:159], v[124:127], a[72:75]\n" \
"ds_read_b64_tr_b16 v[88:89], v33 offset:32768\n" \
"v_mfma_f32_16x16x32_bf16 a[104:107], v[156:159], v[128:131], a[104:107]\n" \
"ds_read_b64_tr_b16 v[90:91], v33 offset:33792\n" \
"v_mfma_f32_16x16x32_bf16 a[76:79], v[160:163], v[124:127], a[76:79]\n" \
"s_add_u32 m0, s31, 0x0\n" \
"s_nop 0\n" \
"s_mov_b32 s84, 0x200000\n" \
"v_mfma_f32_16x16x32_bf16 a[108:111], v[160:163], v[128:131], a[108:111]\n" \
"buffer_load_dwordx4 v26, s[68:71], s84 offen lds\n" \
"v_mfma_f32_16x16x32_bf16 a[80:83], v[164:167], v[120:123], a[80:83]\n" \
"ds_read_b64_tr_b16 v[92:93], v34 offset:32768\n" \
"v_mfma_f32_16x16x32_bf16 a[84:87], v[168:171], v[120:123], a[84:87]\n" \
"ds_read_b64_tr_b16 v[94:95], v34 offset:33792\n" \
"v_mfma_f32_16x16x32_bf16 a[116:119], v[176:179], v[120:123], a[116:119]\n" \
"s_add_u32 m0, s30, 0x800\n" \
"s_mov_b32 s85, 0x4a00\n" \
"v_mfma_f32_16x16x32_bf16 a[112:115], v[120:123], v[172:175], a[112:115]\n" \
"buffer_load_dwordx4 v24, s[76:79], s85 offen lds\n" \
"v_mfma_f32_16x16x32_bf16 a[128:131], v[148:151], v[132:135], a[128:131]\n" \
"ds_read_b64_tr_b16 v[96:97], v35 offset:32768\n" \
"v_mfma_f32_16x16x32_bf16 a[160:163], v[148:151], v[136:139], a[160:163]\n" \
"ds_read_b64_tr_b16 v[98:99], v35 offset:33792\n" \
"v_mfma_f32_16x16x32_bf16 a[132:135], v[152:155], v[132:135], a[132:135]\n" \
"s_add_u32 m0, s30, 0xc00\n" \
"s_mov_b32 s84, 0x6e00\n" \
"v_mfma_f32_16x16x32_bf16 a[164:167], v[152:155], v[136:139], a[164:167]\n" \
"buffer_load_dwordx4 v25, s[76:79], s84 offen lds\n" \
"v_mfma_f32_16x16x32_bf16 a[136:139], v[156:159], v[132:135], a[136:139]\n" \
"ds_read_b64_tr_b16 v[100:101], v36 offset:32768\n" \
"v_mfma_f32_16x16x32_bf16 a[168:171], v[156:159], v[136:139], a[168:171]\n" \
"ds_read_b64_tr_b16 v[102:103], v36 offset:33280\n" \
"v_mfma_f32_16x16x32_bf16 a[140:143], v[160:163], v[132:135], a[140:143]\n" \
"ds_read_b128 v[52:55], v28 offset:32768\n" \
"v_mfma_f32_16x16x32_bf16 a[172:175], v[160:163], v[136:139], a[172:175]\n" \
"s_add_u32 m0, s31, 0x400\n" \
"s_mov_b32 s85, 0x208000\n" \
"v_mfma_f32_16x16x32_bf16 a[144:147], v[164:167], v[124:127], a[144:147]\n" \
"buffer_load_dwordx4 v26, s[68:71], s85 offen lds\n" \
"v_mfma_f32_16x16x32_bf16 a[148:151], v[168:171], v[124:127], a[148:151]\n" \
"ds_read_b128 v[56:59], v28 offset:34816\n" \
"v_mfma_f32_16x16x32_bf16 a[180:183], v[176:179], v[124:127], a[180:183]\n" \
"ds_read_b128 v[60:63], v28 offset:36864\n" \
"v_mfma_f32_16x16x32_bf16 a[176:179], v[124:127], v[172:175], a[176:179]\n" \
"s_add_u32 m0, s30, 0x1000\n" \
"s_mov_b32 s84, 0x9200\n" \
"v_mfma_f32_16x16x32_bf16 a[192:195], v[148:151], v[140:143], a[192:195]\n" \
"buffer_load_dwordx4 v24, s[76:79], s84 offen lds\n" \
"v_mfma_f32_16x16x32_bf16 a[224:227], v[148:151], v[144:147], a[224:227]\n" \
"ds_read_b128 v[64:67], v28 offset:38912\n" \
"v_mfma_f32_16x16x32_bf16 a[196:199], v[152:155], v[140:143], a[196:199]\n" \
"ds_read_b128 v[68:71], v28 offset:40960\n" \
"v_mfma_f32_16x16x32_bf16 a[228:231], v[152:155], v[144:147], a[228:231]\n" \
"s_add_u32 m0, s30, 0x1400\n" \
"s_mov_b32 s85, 0xb600\n" \
"v_mfma_f32_16x16x32_bf16 a[200:203], v[156:159], v[140:143], a[200:203]\n" \
"buffer_load_dwordx4 v25, s[76:79], s85 offen lds\n" \
"v_mfma_f32_16x16x32_bf16 a[232:235], v[156:159], v[144:147], a[232:235]\n" \
"ds_read_b128 v[72:75], v28 offset:43008\n" \
"v_mfma_f32_16x16x32_bf16 a[204:207], v[160:163], v[140:143], a[204:207]\n" \
"ds_read_b128 v[76:79], v28 offset:45056\n" \
"v_mfma_f32_16x16x32_bf16 a[236:239], v[160:163], v[144:147], a[236:239]\n" \
"s_add_u32 m0, s31, 0x800\n" \
"s_mov_b32 s84, 0x210000\n" \
"v_mfma_f32_16x16x32_bf16 a[208:211], v[164:167], v[128:131], a[208:211]\n" \
"buffer_load_dwordx4 v27, s[68:71], s84 offen lds\n" \
"v_mfma_f32_16x16x32_bf16 a[212:215], v[168:171], v[128:131], a[212:215]\n" \
"ds_read_b128 v[80:83], v28 offset:47104\n" \
"v_mfma_f32_16x16x32_bf16 a[244:247], v[176:179], v[128:131], a[244:247]\n" \
"v_mfma_f32_16x16x32_bf16 a[240:243], v[128:131], v[172:175], a[240:243]\n" \
"s_waitcnt lgkmcnt(15)\n" \
"v_mfma_f32_16x16x32_bf16 a[24:27], v[104:107], v[248:251], a[24:27]\n" \
"s_add_u32 m0, s30, 0x1800\n" \
"s_mov_b32 s84, 0xda00\n" \
"v_mfma_f32_16x16x32_bf16 a[28:31], v[248:251], v[108:111], a[28:31]\n" \
"buffer_load_dwordx4 v24, s[76:79], s84 offen lds\n" \
"v_mfma_f32_16x16x32_bf16 a[56:59], v[112:115], v[248:251], a[56:59]\n" \
"ds_read_b64_tr_b16 v[168:169], v37 offset:36864\n" \
"s_waitcnt lgkmcnt(8)\n" \
"v_mfma_f32_16x16x32_bf16 a[0:3], v[84:87], v[52:55], a[0:3]\n" \
"s_add_u32 m0, s30, 0x1c00\n" \
"s_mov_b32 s85, 0xfe00\n" \
"s_waitcnt lgkmcnt(7)\n" \
"s_nop 0\n" \
"v_mfma_f32_16x16x32_bf16 a[32:35], v[84:87], v[56:59], a[32:35]\n" \
"buffer_load_dwordx4 v25, s[76:79], s85 offen lds\n" \
"v_mfma_f32_16x16x32_bf16 a[4:7], v[88:91], v[52:55], a[4:7]\n" \
"ds_read_b64_tr_b16 v[170:171], v37 offset:37376\n" \
"v_mfma_f32_16x16x32_bf16 a[36:39], v[88:91], v[56:59], a[36:39]\n" \
"s_add_u32 m0, s31, 0xc00\n" \
"s_mov_b32 s84, 0x218000\n" \
"v_mfma_f32_16x16x32_bf16 a[8:11], v[92:95], v[52:55], a[8:11]\n" \
"buffer_load_dwordx4 v27, s[68:71], s84 offen lds\n" \
"v_mfma_f32_16x16x32_bf16 a[40:43], v[92:95], v[56:59], a[40:43]\n" \
"ds_read_b64_tr_b16 v[172:173], v38 offset:36864\n" \
"v_mfma_f32_16x16x32_bf16 a[12:15], v[96:99], v[52:55], a[12:15]\n" \
"ds_read_b64_tr_b16 v[174:175], v38 offset:37376\n" \
"v_mfma_f32_16x16x32_bf16 a[44:47], v[96:99], v[56:59], a[44:47]\n" \
"ds_read_b64_tr_b16 v[176:177], v39 offset:36864\n" \
"v_mfma_f32_16x16x32_bf16 a[16:19], v[100:103], v[52:55], a[16:19]\n" \
"ds_read_b64_tr_b16 v[178:179], v39 offset:37376\n" \
"v_mfma_f32_16x16x32_bf16 a[20:23], v[104:107], v[52:55], a[20:23]\n" \
"ds_read_b64_tr_b16 v[148:149], v32 offset:40960\n" \
"v_mfma_f32_16x16x32_bf16 a[52:55], v[112:115], v[52:55], a[52:55]\n" \
"ds_read_b64_tr_b16 v[150:151], v32 offset:41984\n" \
"v_mfma_f32_16x16x32_bf16 a[48:51], v[52:55], v[108:111], a[48:51]\n" \
"ds_read_b128 v[248:251], v23 offset:32768\n" \
"s_waitcnt lgkmcnt(14)\n" \
"v_mfma_f32_16x16x32_bf16 a[64:67], v[84:87], v[60:63], a[64:67]\n" \
"ds_read_b64_tr_b16 v[152:153], v33 offset:40960\n" \
"s_waitcnt lgkmcnt(14)\n" \
"v_mfma_f32_16x16x32_bf16 a[96:99], v[84:87], v[64:67], a[96:99]\n" \
"ds_read_b64_tr_b16 v[154:155], v33 offset:41984\n" \
"v_mfma_f32_16x16x32_bf16 a[68:71], v[88:91], v[60:63], a[68:71]\n" \
"ds_read_b64_tr_b16 v[156:157], v34 offset:40960\n" \
"v_mfma_f32_16x16x32_bf16 a[100:103], v[88:91], v[64:67], a[100:103]\n" \
"ds_read_b64_tr_b16 v[158:159], v34 offset:41984\n" \
"v_mfma_f32_16x16x32_bf16 a[72:75], v[92:95], v[60:63], a[72:75]\n" \
"v_mfma_f32_16x16x32_bf16 a[104:107], v[92:95], v[64:67], a[104:107]\n" \
"ds_read_b64_tr_b16 v[160:161], v35 offset:40960\n" \
"v_mfma_f32_16x16x32_bf16 a[76:79], v[96:99], v[60:63], a[76:79]\n" \
"s_add_u32 s89, s41, 0x3000\n" \
"buffer_store_dwordx4 v[232:235], v20, s[80:83], s89 offen offset:0 sc1\n" \
"v_mfma_f32_16x16x32_bf16 a[108:111], v[96:99], v[64:67], a[108:111]\n" \
"ds_read_b64_tr_b16 v[162:163], v35 offset:41984\n" \
"v_mfma_f32_16x16x32_bf16 a[80:83], v[100:103], v[56:59], a[80:83]\n" \
"ds_read_b64_tr_b16 v[164:165], v36 offset:36864\n" \
"v_mfma_f32_16x16x32_bf16 a[84:87], v[104:107], v[56:59], a[84:87]\n" \
"ds_read_b64_tr_b16 v[166:167], v36 offset:37376\n" \
"v_mfma_f32_16x16x32_bf16 a[116:119], v[112:115], v[56:59], a[116:119]\n" \
"v_mfma_f32_16x16x32_bf16 a[112:115], v[56:59], v[108:111], a[112:115]\n" \
"ds_read_b128 v[116:119], v29 offset:32768\n" \
"s_waitcnt lgkmcnt(15)\n" \
"s_nop 0\n" \
"v_mfma_f32_16x16x32_bf16 a[128:131], v[84:87], v[68:71], a[128:131]\n" \
"v_mfma_f32_16x16x32_bf16 a[160:163], v[84:87], v[72:75], a[160:163]\n" \
"ds_read_b128 v[120:123], v29 offset:34816\n" \
"v_mfma_f32_16x16x32_bf16 a[132:135], v[88:91], v[68:71], a[132:135]\n" \
"s_add_u32 s88, s41, 0x3000\n" \
"buffer_store_dwordx4 v[236:239], v20, s[80:83], s88 offen offset:1024 sc1\n" \
"v_mfma_f32_16x16x32_bf16 a[164:167], v[88:91], v[72:75], a[164:167]\n" \
"ds_read_b128 v[124:127], v29 offset:36864\n" \
"v_mfma_f32_16x16x32_bf16 a[136:139], v[92:95], v[68:71], a[136:139]\n" \
"ds_read_b128 v[128:131], v29 offset:38912\n" \
"v_mfma_f32_16x16x32_bf16 a[168:171], v[92:95], v[72:75], a[168:171]\n" \
"ds_read_b128 v[132:135], v29 offset:40960\n" \
"v_mfma_f32_16x16x32_bf16 a[140:143], v[96:99], v[68:71], a[140:143]\n" \
"v_mfma_f32_16x16x32_bf16 a[172:175], v[96:99], v[72:75], a[172:175]\n" \
"ds_read_b128 v[136:139], v29 offset:43008\n" \
"v_mfma_f32_16x16x32_bf16 a[144:147], v[100:103], v[60:63], a[144:147]\n" \
"ds_read_b128 v[140:143], v29 offset:45056\n" \
"v_mfma_f32_16x16x32_bf16 a[148:151], v[104:107], v[60:63], a[148:151]\n" \
"s_add_u32 s89, s41, 0x3000\n" \
"buffer_store_dwordx4 v[240:243], v20, s[80:83], s89 offen offset:2048 sc1\n" \
"v_mfma_f32_16x16x32_bf16 a[180:183], v[112:115], v[60:63], a[180:183]\n" \
"ds_read_b128 v[144:147], v29 offset:47104\n" \
"v_mfma_f32_16x16x32_bf16 a[176:179], v[60:63], v[108:111], a[176:179]\n" \
"v_mfma_f32_16x16x32_bf16 a[192:195], v[84:87], v[76:79], a[192:195]\n" \
"v_mfma_f32_16x16x32_bf16 a[224:227], v[84:87], v[80:83], a[224:227]\n" \
"v_mfma_f32_16x16x32_bf16 a[196:199], v[88:91], v[76:79], a[196:199]\n" \
"v_mfma_f32_16x16x32_bf16 a[228:231], v[88:91], v[80:83], a[228:231]\n" \
"v_mfma_f32_16x16x32_bf16 a[200:203], v[92:95], v[76:79], a[200:203]\n" \
"s_add_u32 s88, s41, 0x3000\n" \
"buffer_store_dwordx4 v[244:247], v20, s[80:83], s88 offen offset:3072 sc1\n" \
"v_mfma_f32_16x16x32_bf16 a[232:235], v[92:95], v[80:83], a[232:235]\n" \
"v_mfma_f32_16x16x32_bf16 a[204:207], v[96:99], v[76:79], a[204:207]\n" \
"v_mfma_f32_16x16x32_bf16 a[236:239], v[96:99], v[80:83], a[236:239]\n" \
"v_mfma_f32_16x16x32_bf16 a[208:211], v[100:103], v[64:67], a[208:211]\n" \
"v_mfma_f32_16x16x32_bf16 a[212:215], v[104:107], v[64:67], a[212:215]\n" \
"v_mfma_f32_16x16x32_bf16 a[244:247], v[112:115], v[64:67], a[244:247]\n" \
"v_mfma_f32_16x16x32_bf16 a[240:243], v[64:67], v[108:111], a[240:243]\n" \
"s_waitcnt vmcnt(4) lgkmcnt(0)\n" \
"s_barrier\n" \
"v_mfma_f32_16x16x32_bf16 a[24:27], v[168:171], v[248:251], a[24:27]\n" \
"s_add_u32 m0, s40, 0x8000\n" \
"s_add_u32 s85, s39, 0xa000\n" \
"v_mfma_f32_16x16x32_bf16 a[28:31], v[248:251], v[172:175], a[28:31]\n" \
"buffer_load_dwordx4 v20, s[80:83], s85 offen sc1 lds\n" \
"v_mfma_f32_16x16x32_bf16 a[56:59], v[176:179], v[248:251], a[56:59]\n" \
"ds_read_b64_tr_b16 v[104:105], v37 offset:0\n" \
"v_mfma_f32_16x16x32_bf16 a[0:3], v[148:151], v[116:119], a[0:3]\n" \
"ds_read_b64_tr_b16 v[106:107], v37 offset:512\n" \
"v_mfma_f32_16x16x32_bf16 a[32:35], v[148:151], v[120:123], a[32:35]\n" \
"s_add_u32 m0, s40, 0x9000\n" \
"s_add_u32 s84, s39, 0xb000\n" \
"v_mfma_f32_16x16x32_bf16 a[4:7], v[152:155], v[116:119], a[4:7]\n" \
"buffer_load_dwordx4 v20, s[80:83], s84 offen sc1 lds\n" \
"v_mfma_f32_16x16x32_bf16 a[36:39], v[152:155], v[120:123], a[36:39]\n" \
"ds_read_b64_tr_b16 v[108:109], v38 offset:0\n" \
"v_mfma_f32_16x16x32_bf16 a[8:11], v[156:159], v[116:119], a[8:11]\n" \
"ds_read_b64_tr_b16 v[110:111], v38 offset:512\n" \
"v_mfma_f32_16x16x32_bf16 a[40:43], v[156:159], v[120:123], a[40:43]\n" \
"s_add_u32 m0, s40, 0xb000\n" \
"s_mov_b32 s85, 0x280\n" \
"v_mfma_f32_16x16x32_bf16 a[12:15], v[160:163], v[116:119], a[12:15]\n" \
"buffer_load_dwordx4 v21, s[76:79], s85 offen lds\n" \
"v_mfma_f32_16x16x32_bf16 a[44:47], v[160:163], v[120:123], a[44:47]\n" \
"ds_read_b64_tr_b16 v[112:113], v39 offset:0\n" \
"v_mfma_f32_16x16x32_bf16 a[16:19], v[164:167], v[116:119], a[16:19]\n" \
"ds_read_b64_tr_b16 v[114:115], v39 offset:512\n" \
"v_mfma_f32_16x16x32_bf16 a[20:23], v[168:171], v[116:119], a[20:23]\n" \
"s_add_u32 m0, s30, 0x8000\n" \
"s_mov_b32 s84, 0x280\n" \
"v_mfma_f32_16x16x32_bf16 a[52:55], v[176:179], v[116:119], a[52:55]\n" \
"buffer_load_dwordx4 v24, s[76:79], s84 offen lds\n" \
"v_mfma_f32_16x16x32_bf16 a[48:51], v[116:119], v[172:175], a[48:51]\n" \
"ds_read_b64_tr_b16 v[84:85], v32 offset:0\n" \
"v_mfma_f32_16x16x32_bf16 a[64:67], v[148:151], v[124:127], a[64:67]\n" \
"ds_read_b64_tr_b16 v[86:87], v32 offset:1024\n" \
"v_mfma_f32_16x16x32_bf16 a[96:99], v[148:151], v[128:131], a[96:99]\n" \
"ds_read_b128 v[248:251], v22 offset:0\n" \
"v_mfma_f32_16x16x32_bf16 a[68:71], v[152:155], v[124:127], a[68:71]\n" \
"s_add_u32 m0, s30, 0x8400\n" \
"s_mov_b32 s85, 0x2680\n" \
"v_mfma_f32_16x16x32_bf16 a[100:103], v[152:155], v[128:131], a[100:103]\n" \
"buffer_load_dwordx4 v25, s[76:79], s85 offen lds\n" \
"v_mfma_f32_16x16x32_bf16 a[72:75], v[156:159], v[124:127], a[72:75]\n" \
"ds_read_b64_tr_b16 v[88:89], v33 offset:0\n" \
"v_mfma_f32_16x16x32_bf16 a[104:107], v[156:159], v[128:131], a[104:107]\n" \
"ds_read_b64_tr_b16 v[90:91], v33 offset:1024\n" \
"v_mfma_f32_16x16x32_bf16 a[76:79], v[160:163], v[124:127], a[76:79]\n" \
"s_add_u32 m0, s31, 0x8000\n" \
"s_mov_b32 s84, 0x280000\n" \
"v_mfma_f32_16x16x32_bf16 a[108:111], v[160:163], v[128:131], a[108:111]\n" \
"buffer_load_dwordx4 v26, s[68:71], s84 offen lds\n" \
"v_mfma_f32_16x16x32_bf16 a[80:83], v[164:167], v[120:123], a[80:83]\n" \
"ds_read_b64_tr_b16 v[92:93], v34 offset:0\n" \
"v_mfma_f32_16x16x32_bf16 a[84:87], v[168:171], v[120:123], a[84:87]\n" \
"ds_read_b64_tr_b16 v[94:95], v34 offset:1024\n" \
"v_mfma_f32_16x16x32_bf16 a[116:119], v[176:179], v[120:123], a[116:119]\n" \
"s_add_u32 m0, s30, 0x8800\n" \
"s_mov_b32 s85, 0x4a80\n" \
"v_mfma_f32_16x16x32_bf16 a[112:115], v[120:123], v[172:175], a[112:115]\n" \
"buffer_load_dwordx4 v24, s[76:79], s85 offen lds\n" \
"v_mfma_f32_16x16x32_bf16 a[128:131], v[148:151], v[132:135], a[128:131]\n" \
"ds_read_b64_tr_b16 v[96:97], v35 offset:0\n" \
"v_mfma_f32_16x16x32_bf16 a[160:163], v[148:151], v[136:139], a[160:163]\n" \
"ds_read_b64_tr_b16 v[98:99], v35 offset:1024\n" \
"v_mfma_f32_16x16x32_bf16 a[132:135], v[152:155], v[132:135], a[132:135]\n" \
"s_add_u32 m0, s30, 0x8c00\n" \
"s_mov_b32 s84, 0x6e80\n" \
"v_mfma_f32_16x16x32_bf16 a[164:167], v[152:155], v[136:139], a[164:167]\n" \
"buffer_load_dwordx4 v25, s[76:79], s84 offen lds\n" \
"v_mfma_f32_16x16x32_bf16 a[136:139], v[156:159], v[132:135], a[136:139]\n" \
"ds_read_b64_tr_b16 v[100:101], v36 offset:0\n" \
"v_mfma_f32_16x16x32_bf16 a[168:171], v[156:159], v[136:139], a[168:171]\n" \
"ds_read_b64_tr_b16 v[102:103], v36 offset:512\n" \
"v_mfma_f32_16x16x32_bf16 a[140:143], v[160:163], v[132:135], a[140:143]\n" \
"ds_read_b128 v[52:55], v28 offset:0\n" \
"v_mfma_f32_16x16x32_bf16 a[172:175], v[160:163], v[136:139], a[172:175]\n" \
"s_add_u32 m0, s31, 0x8400\n" \
"s_mov_b32 s85, 0x288000\n" \
"v_mfma_f32_16x16x32_bf16 a[144:147], v[164:167], v[124:127], a[144:147]\n" \
"buffer_load_dwordx4 v26, s[68:71], s85 offen lds\n" \
"v_mfma_f32_16x16x32_bf16 a[148:151], v[168:171], v[124:127], a[148:151]\n" \
"ds_read_b128 v[56:59], v28 offset:2048\n" \
"v_mfma_f32_16x16x32_bf16 a[180:183], v[176:179], v[124:127], a[180:183]\n" \
"ds_read_b128 v[60:63], v28 offset:4096\n" \
"v_mfma_f32_16x16x32_bf16 a[176:179], v[124:127], v[172:175], a[176:179]\n" \
"s_add_u32 m0, s30, 0x9000\n" \
"s_mov_b32 s84, 0x9280\n" \
"v_mfma_f32_16x16x32_bf16 a[192:195], v[148:151], v[140:143], a[192:195]\n" \
"buffer_load_dwordx4 v24, s[76:79], s84 offen lds\n" \
"v_mfma_f32_16x16x32_bf16 a[224:227], v[148:151], v[144:147], a[224:227]\n" \
"ds_read_b128 v[64:67], v28 offset:6144\n" \
"v_mfma_f32_16x16x32_bf16 a[196:199], v[152:155], v[140:143], a[196:199]\n" \
"ds_read_b128 v[68:71], v28 offset:8192\n" \
"v_mfma_f32_16x16x32_bf16 a[228:231], v[152:155], v[144:147], a[228:231]\n" \
"s_add_u32 m0, s30, 0x9400\n" \
"s_mov_b32 s85, 0xb680\n" \
"v_mfma_f32_16x16x32_bf16 a[200:203], v[156:159], v[140:143], a[200:203]\n" \
"buffer_load_dwordx4 v25, s[76:79], s85 offen lds\n" \
"v_mfma_f32_16x16x32_bf16 a[232:235], v[156:159], v[144:147], a[232:235]\n" \
"ds_read_b128 v[72:75], v28 offset:10240\n" \
"v_mfma_f32_16x16x32_bf16 a[204:207], v[160:163], v[140:143], a[204:207]\n" \
"ds_read_b128 v[76:79], v28 offset:12288\n" \
"v_mfma_f32_16x16x32_bf16 a[236:239], v[160:163], v[144:147], a[236:239]\n" \
"s_add_u32 m0, s31, 0x8800\n" \
"s_mov_b32 s84, 0x290000\n" \
"v_mfma_f32_16x16x32_bf16 a[208:211], v[164:167], v[128:131], a[208:211]\n" \
"buffer_load_dwordx4 v27, s[68:71], s84 offen lds\n" \
"v_mfma_f32_16x16x32_bf16 a[212:215], v[168:171], v[128:131], a[212:215]\n" \
"ds_read_b128 v[80:83], v28 offset:14336\n" \
"v_mfma_f32_16x16x32_bf16 a[244:247], v[176:179], v[128:131], a[244:247]\n" \
"v_mfma_f32_16x16x32_bf16 a[240:243], v[128:131], v[172:175], a[240:243]\n" \
"s_waitcnt lgkmcnt(15)\n" \
"v_mfma_f32_16x16x32_bf16 a[24:27], v[104:107], v[248:251], a[24:27]\n" \
"s_add_u32 m0, s30, 0x9800\n" \
"s_mov_b32 s84, 0xda80\n" \
"v_mfma_f32_16x16x32_bf16 a[28:31], v[248:251], v[108:111], a[28:31]\n" \
"buffer_load_dwordx4 v24, s[76:79], s84 offen lds\n" \
"v_mfma_f32_16x16x32_bf16 a[56:59], v[112:115], v[248:251], a[56:59]\n" \
"ds_read_b64_tr_b16 v[168:169], v37 offset:4096\n" \
"s_waitcnt lgkmcnt(8)\n" \
"v_mfma_f32_16x16x32_bf16 a[0:3], v[84:87], v[52:55], a[0:3]\n" \
"s_add_u32 m0, s30, 0x9c00\n" \
"s_mov_b32 s85, 0xfe80\n" \
"s_waitcnt lgkmcnt(7)\n" \
"s_nop 0\n" \
"v_mfma_f32_16x16x32_bf16 a[32:35], v[84:87], v[56:59], a[32:35]\n" \
"buffer_load_dwordx4 v25, s[76:79], s85 offen lds\n" \
"v_mfma_f32_16x16x32_bf16 a[4:7], v[88:91], v[52:55], a[4:7]\n" \
"ds_read_b64_tr_b16 v[170:171], v37 offset:4608\n" \
"v_mfma_f32_16x16x32_bf16 a[36:39], v[88:91], v[56:59], a[36:39]\n" \
"s_add_u32 m0, s31, 0x8c00\n" \
"s_mov_b32 s84, 0x298000\n" \
"v_mfma_f32_16x16x32_bf16 a[8:11], v[92:95], v[52:55], a[8:11]\n" \
"buffer_load_dwordx4 v27, s[68:71], s84 offen lds\n" \
"v_mfma_f32_16x16x32_bf16 a[40:43], v[92:95], v[56:59], a[40:43]\n" \
"ds_read_b64_tr_b16 v[172:173], v38 offset:4096\n" \
"v_mfma_f32_16x16x32_bf16 a[12:15], v[96:99], v[52:55], a[12:15]\n" \
"ds_read_b64_tr_b16 v[174:175], v38 offset:4608\n" \
"v_mfma_f32_16x16x32_bf16 a[44:47], v[96:99], v[56:59], a[44:47]\n" \
"ds_read_b64_tr_b16 v[176:177], v39 offset:4096\n" \
"v_mfma_f32_16x16x32_bf16 a[16:19], v[100:103], v[52:55], a[16:19]\n" \
"ds_read_b64_tr_b16 v[178:179], v39 offset:4608\n" \
"v_mfma_f32_16x16x32_bf16 a[20:23], v[104:107], v[52:55], a[20:23]\n" \
"ds_read_b64_tr_b16 v[148:149], v32 offset:8192\n" \
"v_mfma_f32_16x16x32_bf16 a[52:55], v[112:115], v[52:55], a[52:55]\n" \
"ds_read_b64_tr_b16 v[150:151], v32 offset:9216\n" \
"v_mfma_f32_16x16x32_bf16 a[48:51], v[52:55], v[108:111], a[48:51]\n" \
"ds_read_b128 v[248:251], v23 offset:0\n" \
"s_waitcnt lgkmcnt(14)\n" \
"v_mfma_f32_16x16x32_bf16 a[64:67], v[84:87], v[60:63], a[64:67]\n" \
"ds_read_b64_tr_b16 v[152:153], v33 offset:8192\n" \
"s_waitcnt lgkmcnt(14)\n" \
"v_mfma_f32_16x16x32_bf16 a[96:99], v[84:87], v[64:67], a[96:99]\n" \
"ds_read_b64_tr_b16 v[154:155], v33 offset:9216\n" \
"v_mfma_f32_16x16x32_bf16 a[68:71], v[88:91], v[60:63], a[68:71]\n" \
"ds_read_b64_tr_b16 v[156:157], v34 offset:8192\n" \
"v_mfma_f32_16x16x32_bf16 a[100:103], v[88:91], v[64:67], a[100:103]\n" \
"ds_read_b64_tr_b16 v[158:159], v34 offset:9216\n" \
"v_mfma_f32_16x16x32_bf16 a[72:75], v[92:95], v[60:63], a[72:75]\n" \
"v_mfma_f32_16x16x32_bf16 a[104:107], v[92:95], v[64:67], a[104:107]\n" \
"ds_read_b64_tr_b16 v[160:161], v35 offset:8192\n" \
"v_mfma_f32_16x16x32_bf16 a[76:79], v[96:99], v[60:63], a[76:79]\n" \
"ds_read_b64_tr_b16 v[162:163], v35 offset:9216\n" \
"v_mfma_f32_16x16x32_bf16 a[108:111], v[96:99], v[64:67], a[108:111]\n" \
"v_mfma_f32_16x16x32_bf16 a[80:83], v[100:103], v[56:59], a[80:83]\n" \
"ds_read_b64_tr_b16 v[164:165], v36 offset:4096\n" \
"v_mfma_f32_16x16x32_bf16 a[84:87], v[104:107], v[56:59], a[84:87]\n" \
"ds_read_b64_tr_b16 v[166:167], v36 offset:4608\n" \
"v_mfma_f32_16x16x32_bf16 a[116:119], v[112:115], v[56:59], a[116:119]\n" \
"s_add_u32 s89, s42, 0x0\n" \
"buffer_store_dwordx4 a[60:63], v20, s[80:83], s89 offen offset:3072 sc1\n" \
"v_mfma_f32_16x16x32_bf16 a[112:115], v[56:59], v[108:111], a[112:115]\n" \
"ds_read_b128 v[116:119], v29 offset:0\n" \
"s_waitcnt lgkmcnt(15)\n" \
"v_mfma_f32_16x16x32_bf16 a[128:131], v[84:87], v[68:71], a[128:131]\n" \
"v_mfma_f32_16x16x32_bf16 a[160:163], v[84:87], v[72:75], a[160:163]\n" \
"ds_read_b128 v[120:123], v29 offset:2048\n" \
"v_mfma_f32_16x16x32_bf16 a[132:135], v[88:91], v[68:71], a[132:135]\n" \
"ds_read_b128 v[124:127], v29 offset:4096\n" \
"v_mfma_f32_16x16x32_bf16 a[164:167], v[88:91], v[72:75], a[164:167]\n" \
"v_mfma_f32_16x16x32_bf16 a[136:139], v[92:95], v[68:71], a[136:139]\n" \
"ds_read_b128 v[128:131], v29 offset:6144\n" \
"v_mfma_f32_16x16x32_bf16 a[168:171], v[92:95], v[72:75], a[168:171]\n" \
"ds_read_b128 v[132:135], v29 offset:8192\n" \
"v_mfma_f32_16x16x32_bf16 a[140:143], v[96:99], v[68:71], a[140:143]\n" \
"v_mfma_f32_16x16x32_bf16 a[172:175], v[96:99], v[72:75], a[172:175]\n" \
"s_add_u32 s88, s42, 0x0\n" \
"s_nop 0\n" \
"buffer_store_dwordx4 a[88:91], v20, s[80:83], s88 offen offset:0 sc1\n" \
"v_mfma_f32_16x16x32_bf16 a[144:147], v[100:103], v[60:63], a[144:147]\n" \
"ds_read_b128 v[136:139], v29 offset:10240\n" \
"v_mfma_f32_16x16x32_bf16 a[148:151], v[104:107], v[60:63], a[148:151]\n" \
"ds_read_b128 v[140:143], v29 offset:12288\n" \
"v_mfma_f32_16x16x32_bf16 a[180:183], v[112:115], v[60:63], a[180:183]\n" \
"ds_read_b128 v[144:147], v29 offset:14336\n" \
"v_mfma_f32_16x16x32_bf16 a[176:179], v[60:63], v[108:111], a[176:179]\n" \
"v_mfma_f32_16x16x32_bf16 a[192:195], v[84:87], v[76:79], a[192:195]\n" \
"v_mfma_f32_16x16x32_bf16 a[224:227], v[84:87], v[80:83], a[224:227]\n" \
"v_mfma_f32_16x16x32_bf16 a[196:199], v[88:91], v[76:79], a[196:199]\n" \
"v_mfma_f32_16x16x32_bf16 a[228:231], v[88:91], v[80:83], a[228:231]\n" \
"s_add_u32 s89, s42, 0x0\n" \
"buffer_store_dwordx4 a[92:95], v20, s[80:83], s89 offen offset:1024 sc1\n" \
"v_mfma_f32_16x16x32_bf16 a[200:203], v[92:95], v[76:79], a[200:203]\n" \
"v_mfma_f32_16x16x32_bf16 a[232:235], v[92:95], v[80:83], a[232:235]\n" \
"v_mfma_f32_16x16x32_bf16 a[204:207], v[96:99], v[76:79], a[204:207]\n" \
"v_mfma_f32_16x16x32_bf16 a[236:239], v[96:99], v[80:83], a[236:239]\n" \
"v_mfma_f32_16x16x32_bf16 a[208:211], v[100:103], v[64:67], a[208:211]\n" \
"v_mfma_f32_16x16x32_bf16 a[212:215], v[104:107], v[64:67], a[212:215]\n" \
"v_mfma_f32_16x16x32_bf16 a[244:247], v[112:115], v[64:67], a[244:247]\n" \
"v_mfma_f32_16x16x32_bf16 a[240:243], v[64:67], v[108:111], a[240:243]\n" \
"s_waitcnt vmcnt(3) lgkmcnt(0)\n" \
"s_barrier\n" \
"v_mov_b32_e32 v45, s62\n" \
"s_mov_b64 exec, 1\n" \
"buffer_store_dword v45, off, s[80:83], s37 offset:0 sc1\n" \
"s_mov_b64 exec, -1\n" \
"v_mfma_f32_16x16x32_bf16 a[24:27], v[168:171], v[248:251], a[24:27]\n" \
"s_add_u32 m0, s40, 0x0\n" \
"s_nop 0\n" \
"s_add_u32 s84, s39, 0xc000\n" \
"v_mfma_f32_16x16x32_bf16 a[28:31], v[248:251], v[172:175], a[28:31]\n" \
"buffer_load_dwordx4 v20, s[80:83], s84 offen sc1 lds\n" \
"v_mfma_f32_16x16x32_bf16 a[56:59], v[176:179], v[248:251], a[56:59]\n" \
"ds_read_b64_tr_b16 v[104:105], v37 offset:32768\n" \
"v_mfma_f32_16x16x32_bf16 a[0:3], v[148:151], v[116:119], a[0:3]\n" \
"ds_read_b64_tr_b16 v[106:107], v37 offset:33280\n" \
"v_mfma_f32_16x16x32_bf16 a[32:35], v[148:151], v[120:123], a[32:35]\n" \
"s_add_u32 m0, s40, 0x1000\n" \
"s_add_u32 s85, s39, 0xd000\n" \
"v_mfma_f32_16x16x32_bf16 a[4:7], v[152:155], v[116:119], a[4:7]\n" \
"buffer_load_dwordx4 v20, s[80:83], s85 offen sc1 lds\n" \
"v_mfma_f32_16x16x32_bf16 a[36:39], v[152:155], v[120:123], a[36:39]\n" \
"ds_read_b64_tr_b16 v[108:109], v38 offset:32768\n" \
"v_mfma_f32_16x16x32_bf16 a[8:11], v[156:159], v[116:119], a[8:11]\n" \
"ds_read_b64_tr_b16 v[110:111], v38 offset:33280\n" \
"v_mfma_f32_16x16x32_bf16 a[40:43], v[156:159], v[120:123], a[40:43]\n" \
"s_add_u32 m0, s40, 0x3000\n" \
"s_mov_b32 s84, 0x300\n" \
"v_mfma_f32_16x16x32_bf16 a[12:15], v[160:163], v[116:119], a[12:15]\n" \
"buffer_load_dwordx4 v21, s[76:79], s84 offen lds\n" \
"v_mfma_f32_16x16x32_bf16 a[44:47], v[160:163], v[120:123], a[44:47]\n" \
"ds_read_b64_tr_b16 v[112:113], v39 offset:32768\n" \
"v_mfma_f32_16x16x32_bf16 a[16:19], v[164:167], v[116:119], a[16:19]\n" \
"ds_read_b64_tr_b16 v[114:115], v39 offset:33280\n" \
"v_mfma_f32_16x16x32_bf16 a[20:23], v[168:171], v[116:119], a[20:23]\n" \
"s_add_u32 m0, s30, 0x0\n" \
"s_nop 0\n" \
"s_mov_b32 s85, 0x300\n" \
"v_mfma_f32_16x16x32_bf16 a[52:55], v[176:179], v[116:119], a[52:55]\n" \
"buffer_load_dwordx4 v24, s[76:79], s85 offen lds\n" \
"v_mfma_f32_16x16x32_bf16 a[48:51], v[116:119], v[172:175], a[48:51]\n" \
"ds_read_b64_tr_b16 v[84:85], v32 offset:32768\n" \
"v_mfma_f32_16x16x32_bf16 a[64:67], v[148:151], v[124:127], a[64:67]\n" \
"ds_read_b64_tr_b16 v[86:87], v32 offset:33792\n" \
"v_mfma_f32_16x16x32_bf16 a[96:99], v[148:151], v[128:131], a[96:99]\n" \
"ds_read_b128 v[248:251], v22 offset:32768\n" \
"v_mfma_f32_16x16x32_bf16 a[68:71], v[152:155], v[124:127], a[68:71]\n" \
"s_add_u32 m0, s30, 0x400\n" \
"s_mov_b32 s84, 0x2700\n" \
"v_mfma_f32_16x16x32_bf16 a[100:103], v[152:155], v[128:131], a[100:103]\n" \
"buffer_load_dwordx4 v25, s[76:79], s84 offen lds\n" \
"v_mfma_f32_16x16x32_bf16 a[72:75], v[156:159], v[124:127], a[72:75]\n" \
"ds_read_b64_tr_b16 v[88:89], v33 offset:32768\n" \
"v_mfma_f32_16x16x32_bf16 a[104:107], v[156:159], v[128:131], a[104:107]\n" \
"ds_read_b64_tr_b16 v[90:91], v33 offset:33792\n" \
"v_mfma_f32_16x16x32_bf16 a[76:79], v[160:163], v[124:127], a[76:79]\n" \
"s_add_u32 m0, s31, 0x0\n" \
"s_nop 0\n" \
"s_mov_b32 s85, 0x300000\n" \
"v_mfma_f32_16x16x32_bf16 a[108:111], v[160:163], v[128:131], a[108:111]\n" \
"buffer_load_dwordx4 v26, s[68:71], s85 offen lds\n" \
"v_mfma_f32_16x16x32_bf16 a[80:83], v[164:167], v[120:123], a[80:83]\n" \
"ds_read_b64_tr_b16 v[92:93], v34 offset:32768\n" \
"v_mfma_f32_16x16x32_bf16 a[84:87], v[168:171], v[120:123], a[84:87]\n" \
"ds_read_b64_tr_b16 v[94:95], v34 offset:33792\n" \
"v_mfma_f32_16x16x32_bf16 a[116:119], v[176:179], v[120:123], a[116:119]\n" \
"s_add_u32 m0, s30, 0x800\n" \
"s_mov_b32 s84, 0x4b00\n" \
"v_mfma_f32_16x16x32_bf16 a[112:115], v[120:123], v[172:175], a[112:115]\n" \
"buffer_load_dwordx4 v24, s[76:79], s84 offen lds\n" \
"v_mfma_f32_16x16x32_bf16 a[128:131], v[148:151], v[132:135], a[128:131]\n" \
"ds_read_b64_tr_b16 v[96:97], v35 offset:32768\n" \
"v_mfma_f32_16x16x32_bf16 a[160:163], v[148:151], v[136:139], a[160:163]\n" \
"ds_read_b64_tr_b16 v[98:99], v35 offset:33792\n" \
"v_mfma_f32_16x16x32_bf16 a[132:135], v[152:155], v[132:135], a[132:135]\n" \
"s_add_u32 m0, s30, 0xc00\n" \
"s_mov_b32 s85, 0x6f00\n" \
"v_mfma_f32_16x16x32_bf16 a[164:167], v[152:155], v[136:139], a[164:167]\n" \
"buffer_load_dwordx4 v25, s[76:79], s85 offen lds\n" \
"v_mfma_f32_16x16x32_bf16 a[136:139], v[156:159], v[132:135], a[136:139]\n" \
"ds_read_b64_tr_b16 v[100:101], v36 offset:32768\n" \
"v_mfma_f32_16x16x32_bf16 a[168:171], v[156:159], v[136:139], a[168:171]\n" \
"ds_read_b64_tr_b16 v[102:103], v36 offset:33280\n" \
"v_mfma_f32_16x16x32_bf16 a[140:143], v[160:163], v[132:135], a[140:143]\n" \
"ds_read_b128 v[52:55], v28 offset:32768\n" \
"v_mfma_f32_16x16x32_bf16 a[172:175], v[160:163], v[136:139], a[172:175]\n" \
"s_add_u32 m0, s31, 0x400\n" \
"s_mov_b32 s84, 0x308000\n" \
"v_mfma_f32_16x16x32_bf16 a[144:147], v[164:167], v[124:127], a[144:147]\n" \
"buffer_load_dwordx4 v26, s[68:71], s84 offen lds\n" \
"v_mfma_f32_16x16x32_bf16 a[148:151], v[168:171], v[124:127], a[148:151]\n" \
"ds_read_b128 v[56:59], v28 offset:34816\n" \
"v_mfma_f32_16x16x32_bf16 a[180:183], v[176:179], v[124:127], a[180:183]\n" \
"ds_read_b128 v[60:63], v28 offset:36864\n" \
"v_mfma_f32_16x16x32_bf16 a[176:179], v[124:127], v[172:175], a[176:179]\n" \
"s_add_u32 m0, s30, 0x1000\n" \
"s_mov_b32 s85, 0x9300\n" \
"v_mfma_f32_16x16x32_bf16 a[192:195], v[148:151], v[140:143], a[192:195]\n" \
"buffer_load_dwordx4 v24, s[76:79], s85 offen lds\n" \
"v_mfma_f32_16x16x32_bf16 a[224:227], v[148:151], v[144:147], a[224:227]\n" \
"ds_read_b128 v[64:67], v28 offset:38912\n" \
"v_mfma_f32_16x16x32_bf16 a[196:199], v[152:155], v[140:143], a[196:199]\n" \
"ds_read_b128 v[68:71], v28 offset:40960\n" \
"v_mfma_f32_16x16x32_bf16 a[228:231], v[152:155], v[144:147], a[228:231]\n" \
"s_add_u32 m0, s30, 0x1400\n" \
"s_mov_b32 s84, 0xb700\n" \
"v_mfma_f32_16x16x32_bf16 a[200:203], v[156:159], v[140:143], a[200:203]\n" \
"buffer_load_dwordx4 v25, s[76:79], s84 offen lds\n" \
"v_mfma_f32_16x16x32_bf16 a[232:235], v[156:159], v[144:147], a[232:235]\n" \
"ds_read_b128 v[72:75], v28 offset:43008\n" \
"v_mfma_f32_16x16x32_bf16 a[204:207], v[160:163], v[140:143], a[204:207]\n" \
"ds_read_b128 v[76:79], v28 offset:45056\n" \
"v_mfma_f32_16x16x32_bf16 a[236:239], v[160:163], v[144:147], a[236:239]\n" \
"s_add_u32 m0, s31, 0x800\n" \
"s_mov_b32 s85, 0x310000\n" \
"v_mfma_f32_16x16x32_bf16 a[208:211], v[164:167], v[128:131], a[208:211]\n" \
"buffer_load_dwordx4 v27, s[68:71], s85 offen lds\n" \
"v_mfma_f32_16x16x32_bf16 a[212:215], v[168:171], v[128:131], a[212:215]\n" \
"ds_read_b128 v[80:83], v28 offset:47104\n" \
"v_mfma_f32_16x16x32_bf16 a[244:247], v[176:179], v[128:131], a[244:247]\n" \
"v_mfma_f32_16x16x32_bf16 a[240:243], v[128:131], v[172:175], a[240:243]\n" \
"s_waitcnt lgkmcnt(15)\n" \
"v_mfma_f32_16x16x32_bf16 a[24:27], v[104:107], v[248:251], a[24:27]\n" \
"s_add_u32 m0, s30, 0x1800\n" \
"s_mov_b32 s85, 0xdb00\n" \
"v_mfma_f32_16x16x32_bf16 a[28:31], v[248:251], v[108:111], a[28:31]\n" \
"buffer_load_dwordx4 v24, s[76:79], s85 offen lds\n" \
"v_mfma_f32_16x16x32_bf16 a[56:59], v[112:115], v[248:251], a[56:59]\n" \
"ds_read_b64_tr_b16 v[168:169], v37 offset:36864\n" \
"s_waitcnt lgkmcnt(8)\n" \
"v_mfma_f32_16x16x32_bf16 a[0:3], v[84:87], v[52:55], a[0:3]\n" \
"s_add_u32 m0, s30, 0x1c00\n" \
"s_mov_b32 s84, 0xff00\n" \
"s_waitcnt lgkmcnt(7)\n" \
"s_nop 0\n" \
"v_mfma_f32_16x16x32_bf16 a[32:35], v[84:87], v[56:59], a[32:35]\n" \
"buffer_load_dwordx4 v25, s[76:79], s84 offen lds\n" \
"v_mfma_f32_16x16x32_bf16 a[4:7], v[88:91], v[52:55], a[4:7]\n" \
"ds_read_b64_tr_b16 v[170:171], v37 offset:37376\n" \
"v_mfma_f32_16x16x32_bf16 a[36:39], v[88:91], v[56:59], a[36:39]\n" \
"s_add_u32 m0, s31, 0xc00\n" \
"s_mov_b32 s85, 0x318000\n" \
"v_mfma_f32_16x16x32_bf16 a[8:11], v[92:95], v[52:55], a[8:11]\n" \
"buffer_load_dwordx4 v27, s[68:71], s85 offen lds\n" \
"v_mfma_f32_16x16x32_bf16 a[40:43], v[92:95], v[56:59], a[40:43]\n" \
"ds_read_b64_tr_b16 v[172:173], v38 offset:36864\n" \
"v_mfma_f32_16x16x32_bf16 a[12:15], v[96:99], v[52:55], a[12:15]\n" \
"ds_read_b64_tr_b16 v[174:175], v38 offset:37376\n" \
"v_mfma_f32_16x16x32_bf16 a[44:47], v[96:99], v[56:59], a[44:47]\n" \
"ds_read_b64_tr_b16 v[176:177], v39 offset:36864\n" \
"v_mfma_f32_16x16x32_bf16 a[16:19], v[100:103], v[52:55], a[16:19]\n" \
"ds_read_b64_tr_b16 v[178:179], v39 offset:37376\n" \
"v_mfma_f32_16x16x32_bf16 a[20:23], v[104:107], v[52:55], a[20:23]\n" \
"ds_read_b64_tr_b16 v[148:149], v32 offset:40960\n" \
"v_mfma_f32_16x16x32_bf16 a[52:55], v[112:115], v[52:55], a[52:55]\n" \
"ds_read_b64_tr_b16 v[150:151], v32 offset:41984\n" \
"v_mfma_f32_16x16x32_bf16 a[48:51], v[52:55], v[108:111], a[48:51]\n" \
"ds_read_b128 v[248:251], v23 offset:32768\n" \
"s_waitcnt lgkmcnt(14)\n" \
"v_mfma_f32_16x16x32_bf16 a[64:67], v[84:87], v[60:63], a[64:67]\n" \
"ds_read_b64_tr_b16 v[152:153], v33 offset:40960\n" \
"s_waitcnt lgkmcnt(14)\n" \
"v_mfma_f32_16x16x32_bf16 a[96:99], v[84:87], v[64:67], a[96:99]\n" \
"ds_read_b64_tr_b16 v[154:155], v33 offset:41984\n" \
"v_mfma_f32_16x16x32_bf16 a[68:71], v[88:91], v[60:63], a[68:71]\n" \
"ds_read_b64_tr_b16 v[156:157], v34 offset:40960\n" \
"v_mfma_f32_16x16x32_bf16 a[100:103], v[88:91], v[64:67], a[100:103]\n" \
"ds_read_b64_tr_b16 v[158:159], v34 offset:41984\n" \
"v_mfma_f32_16x16x32_bf16 a[72:75], v[92:95], v[60:63], a[72:75]\n" \
"v_mfma_f32_16x16x32_bf16 a[104:107], v[92:95], v[64:67], a[104:107]\n" \
"ds_read_b64_tr_b16 v[160:161], v35 offset:40960\n" \
"v_mfma_f32_16x16x32_bf16 a[76:79], v[96:99], v[60:63], a[76:79]\n" \
"ds_read_b64_tr_b16 v[162:163], v35 offset:41984\n" \
"v_mfma_f32_16x16x32_bf16 a[108:111], v[96:99], v[64:67], a[108:111]\n" \
"v_mfma_f32_16x16x32_bf16 a[80:83], v[100:103], v[56:59], a[80:83]\n" \
"ds_read_b64_tr_b16 v[164:165], v36 offset:36864\n" \
"v_mfma_f32_16x16x32_bf16 a[84:87], v[104:107], v[56:59], a[84:87]\n" \
"ds_read_b64_tr_b16 v[166:167], v36 offset:37376\n" \
"v_mfma_f32_16x16x32_bf16 a[116:119], v[112:115], v[56:59], a[116:119]\n" \
"s_add_u32 s88, s42, 0x0\n" \
"buffer_store_dwordx4 a[120:123], v20, s[80:83], s88 offen offset:2048 sc1\n" \
"v_mfma_f32_16x16x32_bf16 a[112:115], v[56:59], v[108:111], a[112:115]\n" \
"ds_read_b128 v[116:119], v29 offset:32768\n" \
"s_waitcnt lgkmcnt(15)\n" \
"v_mfma_f32_16x16x32_bf16 a[128:131], v[84:87], v[68:71], a[128:131]\n" \
"v_mfma_f32_16x16x32_bf16 a[160:163], v[84:87], v[72:75], a[160:163]\n" \
"ds_read_b128 v[120:123], v29 offset:34816\n" \
"v_mfma_f32_16x16x32_bf16 a[132:135], v[88:91], v[68:71], a[132:135]\n" \
"ds_read_b128 v[124:127], v29 offset:36864\n" \
"v_mfma_f32_16x16x32_bf16 a[164:167], v[88:91], v[72:75], a[164:167]\n" \
"v_mfma_f32_16x16x32_bf16 a[136:139], v[92:95], v[68:71], a[136:139]\n" \
"ds_read_b128 v[128:131], v29 offset:38912\n" \
"v_mfma_f32_16x16x32_bf16 a[168:171], v[92:95], v[72:75], a[168:171]\n" \
"ds_read_b128 v[132:135], v29 offset:40960\n" \
"v_mfma_f32_16x16x32_bf16 a[140:143], v[96:99], v[68:71], a[140:143]\n" \
"v_mfma_f32_16x16x32_bf16 a[172:175], v[96:99], v[72:75], a[172:175]\n" \
"s_add_u32 s89, s42, 0x1000\n" \
"buffer_store_dwordx4 a[124:127], v20, s[80:83], s89 offen offset:3072 sc1\n" \
"v_mfma_f32_16x16x32_bf16 a[144:147], v[100:103], v[60:63], a[144:147]\n" \
"ds_read_b128 v[136:139], v29 offset:43008\n" \
"v_mfma_f32_16x16x32_bf16 a[148:151], v[104:107], v[60:63], a[148:151]\n" \
"ds_read_b128 v[140:143], v29 offset:45056\n" \
"v_mfma_f32_16x16x32_bf16 a[180:183], v[112:115], v[60:63], a[180:183]\n" \
"ds_read_b128 v[144:147], v29 offset:47104\n" \
"v_mfma_f32_16x16x32_bf16 a[176:179], v[60:63], v[108:111], a[176:179]\n" \
"v_mfma_f32_16x16x32_bf16 a[192:195], v[84:87], v[76:79], a[192:195]\n" \
"v_mfma_f32_16x16x32_bf16 a[224:227], v[84:87], v[80:83], a[224:227]\n" \
"v_mfma_f32_16x16x32_bf16 a[196:199], v[88:91], v[76:79], a[196:199]\n" \
"v_mfma_f32_16x16x32_bf16 a[228:231], v[88:91], v[80:83], a[228:231]\n" \
"s_add_u32 s88, s42, 0x1000\n" \
"buffer_store_dwordx4 a[152:155], v20, s[80:83], s88 offen offset:0 sc1\n" \
"v_mfma_f32_16x16x32_bf16 a[200:203], v[92:95], v[76:79], a[200:203]\n" \
"v_mfma_f32_16x16x32_bf16 a[232:235], v[92:95], v[80:83], a[232:235]\n" \
"v_mfma_f32_16x16x32_bf16 a[204:207], v[96:99], v[76:79], a[204:207]\n" \
"v_mfma_f32_16x16x32_bf16 a[236:239], v[96:99], v[80:83], a[236:239]\n" \
"v_mfma_f32_16x16x32_bf16 a[208:211], v[100:103], v[64:67], a[208:211]\n" \
"v_mfma_f32_16x16x32_bf16 a[212:215], v[104:107], v[64:67], a[212:215]\n" \
"v_mfma_f32_16x16x32_bf16 a[244:247], v[112:115], v[64:67], a[244:247]\n" \
"v_mfma_f32_16x16x32_bf16 a[240:243], v[64:67], v[108:111], a[240:243]\n" \
"s_waitcnt vmcnt(3) lgkmcnt(0)\n" \
"s_barrier\n" \
"v_mfma_f32_16x16x32_bf16 a[24:27], v[168:171], v[248:251], a[24:27]\n" \
"s_add_u32 m0, s40, 0x8000\n" \
"s_add_u32 s85, s39, 0xe000\n" \
"v_mfma_f32_16x16x32_bf16 a[28:31], v[248:251], v[172:175], a[28:31]\n" \
"buffer_load_dwordx4 v20, s[80:83], s85 offen sc1 lds\n" \
"v_mfma_f32_16x16x32_bf16 a[56:59], v[176:179], v[248:251], a[56:59]\n" \
"ds_read_b64_tr_b16 v[104:105], v37 offset:0\n" \
"v_mfma_f32_16x16x32_bf16 a[0:3], v[148:151], v[116:119], a[0:3]\n" \
"ds_read_b64_tr_b16 v[106:107], v37 offset:512\n" \
"v_mfma_f32_16x16x32_bf16 a[32:35], v[148:151], v[120:123], a[32:35]\n" \
"s_add_u32 m0, s40, 0x9000\n" \
"s_add_u32 s84, s39, 0xf000\n" \
"v_mfma_f32_16x16x32_bf16 a[4:7], v[152:155], v[116:119], a[4:7]\n" \
"buffer_load_dwordx4 v20, s[80:83], s84 offen sc1 lds\n" \
"v_mfma_f32_16x16x32_bf16 a[36:39], v[152:155], v[120:123], a[36:39]\n" \
"ds_read_b64_tr_b16 v[108:109], v38 offset:0\n" \
"v_mfma_f32_16x16x32_bf16 a[8:11], v[156:159], v[116:119], a[8:11]\n" \
"ds_read_b64_tr_b16 v[110:111], v38 offset:512\n" \
"v_mfma_f32_16x16x32_bf16 a[40:43], v[156:159], v[120:123], a[40:43]\n" \
"s_add_u32 m0, s40, 0xb000\n" \
"s_mov_b32 s85, 0x380\n" \
"v_mfma_f32_16x16x32_bf16 a[12:15], v[160:163], v[116:119], a[12:15]\n" \
"buffer_load_dwordx4 v21, s[76:79], s85 offen lds\n" \
"v_mfma_f32_16x16x32_bf16 a[44:47], v[160:163], v[120:123], a[44:47]\n" \
"ds_read_b64_tr_b16 v[112:113], v39 offset:0\n" \
"v_mfma_f32_16x16x32_bf16 a[16:19], v[164:167], v[116:119], a[16:19]\n" \
"ds_read_b64_tr_b16 v[114:115], v39 offset:512\n" \
"v_mfma_f32_16x16x32_bf16 a[20:23], v[168:171], v[116:119], a[20:23]\n" \
"s_add_u32 m0, s30, 0x8000\n" \
"s_mov_b32 s84, 0x380\n" \
"v_mfma_f32_16x16x32_bf16 a[52:55], v[176:179], v[116:119], a[52:55]\n" \
"buffer_load_dwordx4 v24, s[76:79], s84 offen lds\n" \
"v_mfma_f32_16x16x32_bf16 a[48:51], v[116:119], v[172:175], a[48:51]\n" \
"ds_read_b64_tr_b16 v[84:85], v32 offset:0\n" \
"v_mfma_f32_16x16x32_bf16 a[64:67], v[148:151], v[124:127], a[64:67]\n" \
"ds_read_b64_tr_b16 v[86:87], v32 offset:1024\n" \
"v_mfma_f32_16x16x32_bf16 a[96:99], v[148:151], v[128:131], a[96:99]\n" \
"ds_read_b128 v[248:251], v22 offset:0\n" \
"v_mfma_f32_16x16x32_bf16 a[68:71], v[152:155], v[124:127], a[68:71]\n" \
"s_add_u32 m0, s30, 0x8400\n" \
"s_mov_b32 s85, 0x2780\n" \
"v_mfma_f32_16x16x32_bf16 a[100:103], v[152:155], v[128:131], a[100:103]\n" \
"buffer_load_dwordx4 v25, s[76:79], s85 offen lds\n" \
"v_mfma_f32_16x16x32_bf16 a[72:75], v[156:159], v[124:127], a[72:75]\n" \
"ds_read_b64_tr_b16 v[88:89], v33 offset:0\n" \
"v_mfma_f32_16x16x32_bf16 a[104:107], v[156:159], v[128:131], a[104:107]\n" \
"ds_read_b64_tr_b16 v[90:91], v33 offset:1024\n" \
"v_mfma_f32_16x16x32_bf16 a[76:79], v[160:163], v[124:127], a[76:79]\n" \
"s_add_u32 m0, s31, 0x8000\n" \
"s_mov_b32 s84, 0x380000\n" \
"v_mfma_f32_16x16x32_bf16 a[108:111], v[160:163], v[128:131], a[108:111]\n" \
"buffer_load_dwordx4 v26, s[68:71], s84 offen lds\n" \
"v_mfma_f32_16x16x32_bf16 a[80:83], v[164:167], v[120:123], a[80:83]\n" \
"ds_read_b64_tr_b16 v[92:93], v34 offset:0\n" \
"v_mfma_f32_16x16x32_bf16 a[84:87], v[168:171], v[120:123], a[84:87]\n" \
"ds_read_b64_tr_b16 v[94:95], v34 offset:1024\n" \
"v_mfma_f32_16x16x32_bf16 a[116:119], v[176:179], v[120:123], a[116:119]\n" \
"s_add_u32 m0, s30, 0x8800\n" \
"s_mov_b32 s85, 0x4b80\n" \
"v_mfma_f32_16x16x32_bf16 a[112:115], v[120:123], v[172:175], a[112:115]\n" \
"buffer_load_dwordx4 v24, s[76:79], s85 offen lds\n" \
"v_mfma_f32_16x16x32_bf16 a[128:131], v[148:151], v[132:135], a[128:131]\n" \
"ds_read_b64_tr_b16 v[96:97], v35 offset:0\n" \
"v_mfma_f32_16x16x32_bf16 a[160:163], v[148:151], v[136:139], a[160:163]\n" \
"ds_read_b64_tr_b16 v[98:99], v35 offset:1024\n" \
"v_mfma_f32_16x16x32_bf16 a[132:135], v[152:155], v[132:135], a[132:135]\n" \
"s_add_u32 m0, s30, 0x8c00\n" \
"s_mov_b32 s84, 0x6f80\n" \
"v_mfma_f32_16x16x32_bf16 a[164:167], v[152:155], v[136:139], a[164:167]\n" \
"buffer_load_dwordx4 v25, s[76:79], s84 offen lds\n" \
"v_mfma_f32_16x16x32_bf16 a[136:139], v[156:159], v[132:135], a[136:139]\n" \
"ds_read_b64_tr_b16 v[100:101], v36 offset:0\n" \
"v_mfma_f32_16x16x32_bf16 a[168:171], v[156:159], v[136:139], a[168:171]\n" \
"ds_read_b64_tr_b16 v[102:103], v36 offset:512\n" \
"v_mfma_f32_16x16x32_bf16 a[140:143], v[160:163], v[132:135], a[140:143]\n" \
"ds_read_b128 v[52:55], v28 offset:0\n" \
"v_mfma_f32_16x16x32_bf16 a[172:175], v[160:163], v[136:139], a[172:175]\n" \
"s_add_u32 m0, s31, 0x8400\n" \
"s_mov_b32 s85, 0x388000\n" \
"v_mfma_f32_16x16x32_bf16 a[144:147], v[164:167], v[124:127], a[144:147]\n" \
"buffer_load_dwordx4 v26, s[68:71], s85 offen lds\n" \
"v_mfma_f32_16x16x32_bf16 a[148:151], v[168:171], v[124:127], a[148:151]\n" \
"ds_read_b128 v[56:59], v28 offset:2048\n" \
"v_mfma_f32_16x16x32_bf16 a[180:183], v[176:179], v[124:127], a[180:183]\n" \
"ds_read_b128 v[60:63], v28 offset:4096\n" \
"v_mfma_f32_16x16x32_bf16 a[176:179], v[124:127], v[172:175], a[176:179]\n" \
"s_add_u32 m0, s30, 0x9000\n" \
"s_mov_b32 s84, 0x9380\n" \
"v_mfma_f32_16x16x32_bf16 a[192:195], v[148:151], v[140:143], a[192:195]\n" \
"buffer_load_dwordx4 v24, s[76:79], s84 offen lds\n" \
"v_mfma_f32_16x16x32_bf16 a[224:227], v[148:151], v[144:147], a[224:227]\n" \
"ds_read_b128 v[64:67], v28 offset:6144\n" \
"v_mfma_f32_16x16x32_bf16 a[196:199], v[152:155], v[140:143], a[196:199]\n" \
"ds_read_b128 v[68:71], v28 offset:8192\n" \
"v_mfma_f32_16x16x32_bf16 a[228:231], v[152:155], v[144:147], a[228:231]\n" \
"s_add_u32 m0, s30, 0x9400\n" \
"s_mov_b32 s85, 0xb780\n" \
"v_mfma_f32_16x16x32_bf16 a[200:203], v[156:159], v[140:143], a[200:203]\n" \
"buffer_load_dwordx4 v25, s[76:79], s85 offen lds\n" \
"v_mfma_f32_16x16x32_bf16 a[232:235], v[156:159], v[144:147], a[232:235]\n" \
"ds_read_b128 v[72:75], v28 offset:10240\n" \
"v_mfma_f32_16x16x32_bf16 a[204:207], v[160:163], v[140:143], a[204:207]\n" \
"ds_read_b128 v[76:79], v28 offset:12288\n" \
"v_mfma_f32_16x16x32_bf16 a[236:239], v[160:163], v[144:147], a[236:239]\n" \
"s_add_u32 m0, s31, 0x8800\n" \
"s_mov_b32 s84, 0x390000\n" \
"v_mfma_f32_16x16x32_bf16 a[208:211], v[164:167], v[128:131], a[208:211]\n" \
"buffer_load_dwordx4 v27, s[68:71], s84 offen lds\n" \
"v_mfma_f32_16x16x32_bf16 a[212:215], v[168:171], v[128:131], a[212:215]\n" \
"ds_read_b128 v[80:83], v28 offset:14336\n" \
"v_mfma_f32_16x16x32_bf16 a[244:247], v[176:179], v[128:131], a[244:247]\n" \
"v_mfma_f32_16x16x32_bf16 a[240:243], v[128:131], v[172:175], a[240:243]\n" \
"s_waitcnt lgkmcnt(15)\n" \
"v_mfma_f32_16x16x32_bf16 a[24:27], v[104:107], v[248:251], a[24:27]\n" \
"s_add_u32 m0, s30, 0x9800\n" \
"s_mov_b32 s84, 0xdb80\n" \
"v_mfma_f32_16x16x32_bf16 a[28:31], v[248:251], v[108:111], a[28:31]\n" \
"buffer_load_dwordx4 v24, s[76:79], s84 offen lds\n" \
"v_mfma_f32_16x16x32_bf16 a[56:59], v[112:115], v[248:251], a[56:59]\n" \
"ds_read_b64_tr_b16 v[168:169], v37 offset:4096\n" \
"s_waitcnt lgkmcnt(8)\n" \
"v_mfma_f32_16x16x32_bf16 a[0:3], v[84:87], v[52:55], a[0:3]\n" \
"s_add_u32 m0, s30, 0x9c00\n" \
"s_mov_b32 s85, 0xff80\n" \
"s_waitcnt lgkmcnt(7)\n" \
"s_nop 0\n" \
"v_mfma_f32_16x16x32_bf16 a[32:35], v[84:87], v[56:59], a[32:35]\n" \
"buffer_load_dwordx4 v25, s[76:79], s85 offen lds\n" \
"v_mfma_f32_16x16x32_bf16 a[4:7], v[88:91], v[52:55], a[4:7]\n" \
"ds_read_b64_tr_b16 v[170:171], v37 offset:4608\n" \
"v_mfma_f32_16x16x32_bf16 a[36:39], v[88:91], v[56:59], a[36:39]\n" \
"s_add_u32 m0, s31, 0x8c00\n" \
"s_mov_b32 s84, 0x398000\n" \
"v_mfma_f32_16x16x32_bf16 a[8:11], v[92:95], v[52:55], a[8:11]\n" \
"buffer_load_dwordx4 v27, s[68:71], s84 offen lds\n" \
"v_mfma_f32_16x16x32_bf16 a[40:43], v[92:95], v[56:59], a[40:43]\n" \
"ds_read_b64_tr_b16 v[172:173], v38 offset:4096\n" \
"v_mfma_f32_16x16x32_bf16 a[12:15], v[96:99], v[52:55], a[12:15]\n" \
"ds_read_b64_tr_b16 v[174:175], v38 offset:4608\n" \
"v_mfma_f32_16x16x32_bf16 a[44:47], v[96:99], v[56:59], a[44:47]\n" \
"ds_read_b64_tr_b16 v[176:177], v39 offset:4096\n" \
"v_mfma_f32_16x16x32_bf16 a[16:19], v[100:103], v[52:55], a[16:19]\n" \
"ds_read_b64_tr_b16 v[178:179], v39 offset:4608\n" \
"v_mfma_f32_16x16x32_bf16 a[20:23], v[104:107], v[52:55], a[20:23]\n" \
"ds_read_b64_tr_b16 v[148:149], v32 offset:8192\n" \
"v_mfma_f32_16x16x32_bf16 a[52:55], v[112:115], v[52:55], a[52:55]\n" \
"ds_read_b64_tr_b16 v[150:151], v32 offset:9216\n" \
"v_mfma_f32_16x16x32_bf16 a[48:51], v[52:55], v[108:111], a[48:51]\n" \
"ds_read_b128 v[248:251], v23 offset:0\n" \
"s_waitcnt lgkmcnt(14)\n" \
"v_mfma_f32_16x16x32_bf16 a[64:67], v[84:87], v[60:63], a[64:67]\n" \
"ds_read_b64_tr_b16 v[152:153], v33 offset:8192\n" \
"s_waitcnt lgkmcnt(14)\n" \
"v_mfma_f32_16x16x32_bf16 a[96:99], v[84:87], v[64:67], a[96:99]\n" \
"ds_read_b64_tr_b16 v[154:155], v33 offset:9216\n" \
"v_mfma_f32_16x16x32_bf16 a[68:71], v[88:91], v[60:63], a[68:71]\n" \
"ds_read_b64_tr_b16 v[156:157], v34 offset:8192\n" \
"v_mfma_f32_16x16x32_bf16 a[100:103], v[88:91], v[64:67], a[100:103]\n" \
"ds_read_b64_tr_b16 v[158:159], v34 offset:9216\n" \
"v_mfma_f32_16x16x32_bf16 a[72:75], v[92:95], v[60:63], a[72:75]\n" \
"v_mfma_f32_16x16x32_bf16 a[104:107], v[92:95], v[64:67], a[104:107]\n" \
"ds_read_b64_tr_b16 v[160:161], v35 offset:8192\n" \
"v_mfma_f32_16x16x32_bf16 a[76:79], v[96:99], v[60:63], a[76:79]\n" \
"ds_read_b64_tr_b16 v[162:163], v35 offset:9216\n" \
"v_mfma_f32_16x16x32_bf16 a[108:111], v[96:99], v[64:67], a[108:111]\n" \
"v_mfma_f32_16x16x32_bf16 a[80:83], v[100:103], v[56:59], a[80:83]\n" \
"ds_read_b64_tr_b16 v[164:165], v36 offset:4096\n" \
"v_mfma_f32_16x16x32_bf16 a[84:87], v[104:107], v[56:59], a[84:87]\n" \
"ds_read_b64_tr_b16 v[166:167], v36 offset:4608\n" \
"v_mfma_f32_16x16x32_bf16 a[116:119], v[112:115], v[56:59], a[116:119]\n" \
"s_add_u32 s89, s42, 0x1000\n" \
"buffer_store_dwordx4 a[156:159], v20, s[80:83], s89 offen offset:1024 sc1\n" \
"v_mfma_f32_16x16x32_bf16 a[112:115], v[56:59], v[108:111], a[112:115]\n" \
"ds_read_b128 v[116:119], v29 offset:0\n" \
"s_waitcnt lgkmcnt(15)\n" \
"s_nop 0\n" \
"v_mfma_f32_16x16x32_bf16 a[128:131], v[84:87], v[68:71], a[128:131]\n" \
"v_mfma_f32_16x16x32_bf16 a[160:163], v[84:87], v[72:75], a[160:163]\n" \
"ds_read_b128 v[120:123], v29 offset:2048\n" \
"v_mfma_f32_16x16x32_bf16 a[132:135], v[88:91], v[68:71], a[132:135]\n" \
"ds_read_b128 v[124:127], v29 offset:4096\n" \
"v_mfma_f32_16x16x32_bf16 a[164:167], v[88:91], v[72:75], a[164:167]\n" \
"v_mfma_f32_16x16x32_bf16 a[136:139], v[92:95], v[68:71], a[136:139]\n" \
"ds_read_b128 v[128:131], v29 offset:6144\n" \
"v_mfma_f32_16x16x32_bf16 a[168:171], v[92:95], v[72:75], a[168:171]\n" \
"ds_read_b128 v[132:135], v29 offset:8192\n" \
"v_mfma_f32_16x16x32_bf16 a[140:143], v[96:99], v[68:71], a[140:143]\n" \
"v_mfma_f32_16x16x32_bf16 a[172:175], v[96:99], v[72:75], a[172:175]\n" \
"s_add_u32 s88, s42, 0x1000\n" \
"buffer_store_dwordx4 a[184:187], v20, s[80:83], s88 offen offset:2048 sc1\n" \
"v_mfma_f32_16x16x32_bf16 a[144:147], v[100:103], v[60:63], a[144:147]\n" \
"ds_read_b128 v[136:139], v29 offset:10240\n" \
"v_mfma_f32_16x16x32_bf16 a[148:151], v[104:107], v[60:63], a[148:151]\n" \
"ds_read_b128 v[140:143], v29 offset:12288\n" \
"v_mfma_f32_16x16x32_bf16 a[180:183], v[112:115], v[60:63], a[180:183]\n" \
"ds_read_b128 v[144:147], v29 offset:14336\n" \
"v_mfma_f32_16x16x32_bf16 a[176:179], v[60:63], v[108:111], a[176:179]\n" \
"v_mfma_f32_16x16x32_bf16 a[192:195], v[84:87], v[76:79], a[192:195]\n" \
"v_mfma_f32_16x16x32_bf16 a[224:227], v[84:87], v[80:83], a[224:227]\n" \
"v_mfma_f32_16x16x32_bf16 a[196:199], v[88:91], v[76:79], a[196:199]\n" \
"v_mfma_f32_16x16x32_bf16 a[228:231], v[88:91], v[80:83], a[228:231]\n" \
"s_add_u32 s89, s42, 0x2000\n" \
"buffer_store_dwordx4 a[188:191], v20, s[80:83], s89 offen offset:3072 sc1\n" \
"v_mfma_f32_16x16x32_bf16 a[200:203], v[92:95], v[76:79], a[200:203]\n" \
"v_mfma_f32_16x16x32_bf16 a[232:235], v[92:95], v[80:83], a[232:235]\n" \
"v_mfma_f32_16x16x32_bf16 a[204:207], v[96:99], v[76:79], a[204:207]\n" \
"v_mfma_f32_16x16x32_bf16 a[236:239], v[96:99], v[80:83], a[236:239]\n" \
"v_mfma_f32_16x16x32_bf16 a[208:211], v[100:103], v[64:67], a[208:211]\n" \
"v_mfma_f32_16x16x32_bf16 a[212:215], v[104:107], v[64:67], a[212:215]\n" \
"v_mfma_f32_16x16x32_bf16 a[244:247], v[112:115], v[64:67], a[244:247]\n" \
"v_mfma_f32_16x16x32_bf16 a[240:243], v[64:67], v[108:111], a[240:243]\n" \
"s_waitcnt vmcnt(3) lgkmcnt(0)\n" \
"s_barrier\n" \
"v_mfma_f32_16x16x32_bf16 a[24:27], v[168:171], v[248:251], a[24:27]\n" \
"s_add_u32 m0, s40, 0x0\n" \
"s_nop 0\n" \
"s_add_u32 s84, s39, 0x10000\n" \
"v_mfma_f32_16x16x32_bf16 a[28:31], v[248:251], v[172:175], a[28:31]\n" \
"buffer_load_dwordx4 v20, s[80:83], s84 offen sc1 lds\n" \
"v_mfma_f32_16x16x32_bf16 a[56:59], v[176:179], v[248:251], a[56:59]\n" \
"ds_read_b64_tr_b16 v[104:105], v37 offset:32768\n" \
"v_mfma_f32_16x16x32_bf16 a[0:3], v[148:151], v[116:119], a[0:3]\n" \
"ds_read_b64_tr_b16 v[106:107], v37 offset:33280\n" \
"v_mfma_f32_16x16x32_bf16 a[32:35], v[148:151], v[120:123], a[32:35]\n" \
"s_add_u32 m0, s40, 0x1000\n" \
"s_add_u32 s85, s39, 0x11000\n" \
"v_mfma_f32_16x16x32_bf16 a[4:7], v[152:155], v[116:119], a[4:7]\n" \
"buffer_load_dwordx4 v20, s[80:83], s85 offen sc1 lds\n" \
"v_mfma_f32_16x16x32_bf16 a[36:39], v[152:155], v[120:123], a[36:39]\n" \
"ds_read_b64_tr_b16 v[108:109], v38 offset:32768\n" \
"v_mfma_f32_16x16x32_bf16 a[8:11], v[156:159], v[116:119], a[8:11]\n" \
"ds_read_b64_tr_b16 v[110:111], v38 offset:33280\n" \
"v_mfma_f32_16x16x32_bf16 a[40:43], v[156:159], v[120:123], a[40:43]\n" \
"s_add_u32 m0, s40, 0x3000\n" \
"s_mov_b32 s84, 0x400\n" \
"v_mfma_f32_16x16x32_bf16 a[12:15], v[160:163], v[116:119], a[12:15]\n" \
"buffer_load_dwordx4 v21, s[76:79], s84 offen lds\n" \
"v_mfma_f32_16x16x32_bf16 a[44:47], v[160:163], v[120:123], a[44:47]\n" \
"ds_read_b64_tr_b16 v[112:113], v39 offset:32768\n" \
"v_mfma_f32_16x16x32_bf16 a[16:19], v[164:167], v[116:119], a[16:19]\n" \
"ds_read_b64_tr_b16 v[114:115], v39 offset:33280\n" \
"v_mfma_f32_16x16x32_bf16 a[20:23], v[168:171], v[116:119], a[20:23]\n" \
"s_add_u32 m0, s30, 0x0\n" \
"s_nop 0\n" \
"s_mov_b32 s85, 0x400\n" \
"v_mfma_f32_16x16x32_bf16 a[52:55], v[176:179], v[116:119], a[52:55]\n" \
"buffer_load_dwordx4 v24, s[76:79], s85 offen lds\n" \
"v_mfma_f32_16x16x32_bf16 a[48:51], v[116:119], v[172:175], a[48:51]\n" \
"ds_read_b64_tr_b16 v[84:85], v32 offset:32768\n" \
"v_mfma_f32_16x16x32_bf16 a[64:67], v[148:151], v[124:127], a[64:67]\n" \
"ds_read_b64_tr_b16 v[86:87], v32 offset:33792\n" \
"v_mfma_f32_16x16x32_bf16 a[96:99], v[148:151], v[128:131], a[96:99]\n" \
"ds_read_b128 v[248:251], v22 offset:32768\n" \
"v_mfma_f32_16x16x32_bf16 a[68:71], v[152:155], v[124:127], a[68:71]\n" \
"s_add_u32 m0, s30, 0x400\n" \
"s_mov_b32 s84, 0x2800\n" \
"v_mfma_f32_16x16x32_bf16 a[100:103], v[152:155], v[128:131], a[100:103]\n" \
"buffer_load_dwordx4 v25, s[76:79], s84 offen lds\n" \
"v_mfma_f32_16x16x32_bf16 a[72:75], v[156:159], v[124:127], a[72:75]\n" \
"ds_read_b64_tr_b16 v[88:89], v33 offset:32768\n" \
"v_mfma_f32_16x16x32_bf16 a[104:107], v[156:159], v[128:131], a[104:107]\n" \
"ds_read_b64_tr_b16 v[90:91], v33 offset:33792\n" \
"v_mfma_f32_16x16x32_bf16 a[76:79], v[160:163], v[124:127], a[76:79]\n" \
"s_add_u32 m0, s31, 0x0\n" \
"s_nop 0\n" \
"s_mov_b32 s85, 0x400000\n" \
"v_mfma_f32_16x16x32_bf16 a[108:111], v[160:163], v[128:131], a[108:111]\n" \
"buffer_load_dwordx4 v26, s[68:71], s85 offen lds\n" \
"v_mfma_f32_16x16x32_bf16 a[80:83], v[164:167], v[120:123], a[80:83]\n" \
"ds_read_b64_tr_b16 v[92:93], v34 offset:32768\n" \
"v_mfma_f32_16x16x32_bf16 a[84:87], v[168:171], v[120:123], a[84:87]\n" \
"ds_read_b64_tr_b16 v[94:95], v34 offset:33792\n" \
"v_mfma_f32_16x16x32_bf16 a[116:119], v[176:179], v[120:123], a[116:119]\n" \
"s_add_u32 m0, s30, 0x800\n" \
"s_mov_b32 s84, 0x4c00\n" \
"v_mfma_f32_16x16x32_bf16 a[112:115], v[120:123], v[172:175], a[112:115]\n" \
"buffer_load_dwordx4 v24, s[76:79], s84 offen lds\n" \
"v_mfma_f32_16x16x32_bf16 a[128:131], v[148:151], v[132:135], a[128:131]\n" \
"ds_read_b64_tr_b16 v[96:97], v35 offset:32768\n" \
"v_mfma_f32_16x16x32_bf16 a[160:163], v[148:151], v[136:139], a[160:163]\n" \
"ds_read_b64_tr_b16 v[98:99], v35 offset:33792\n" \
"v_mfma_f32_16x16x32_bf16 a[132:135], v[152:155], v[132:135], a[132:135]\n" \
"s_add_u32 m0, s30, 0xc00\n" \
"s_mov_b32 s85, 0x7000\n" \
"v_mfma_f32_16x16x32_bf16 a[164:167], v[152:155], v[136:139], a[164:167]\n" \
"buffer_load_dwordx4 v25, s[76:79], s85 offen lds\n" \
"v_mfma_f32_16x16x32_bf16 a[136:139], v[156:159], v[132:135], a[136:139]\n" \
"ds_read_b64_tr_b16 v[100:101], v36 offset:32768\n" \
"v_mfma_f32_16x16x32_bf16 a[168:171], v[156:159], v[136:139], a[168:171]\n" \
"ds_read_b64_tr_b16 v[102:103], v36 offset:33280\n" \
"v_mfma_f32_16x16x32_bf16 a[140:143], v[160:163], v[132:135], a[140:143]\n" \
"ds_read_b128 v[52:55], v28 offset:32768\n" \
"v_mfma_f32_16x16x32_bf16 a[172:175], v[160:163], v[136:139], a[172:175]\n" \
"s_add_u32 m0, s31, 0x400\n" \
"s_mov_b32 s84, 0x408000\n" \
"v_mfma_f32_16x16x32_bf16 a[144:147], v[164:167], v[124:127], a[144:147]\n" \
"buffer_load_dwordx4 v26, s[68:71], s84 offen lds\n" \
"v_mfma_f32_16x16x32_bf16 a[148:151], v[168:171], v[124:127], a[148:151]\n" \
"ds_read_b128 v[56:59], v28 offset:34816\n" \
"v_mfma_f32_16x16x32_bf16 a[180:183], v[176:179], v[124:127], a[180:183]\n" \
"ds_read_b128 v[60:63], v28 offset:36864\n" \
"v_mfma_f32_16x16x32_bf16 a[176:179], v[124:127], v[172:175], a[176:179]\n" \
"s_add_u32 m0, s30, 0x1000\n" \
"s_mov_b32 s85, 0x9400\n" \
"v_mfma_f32_16x16x32_bf16 a[192:195], v[148:151], v[140:143], a[192:195]\n" \
"buffer_load_dwordx4 v24, s[76:79], s85 offen lds\n" \
"v_mfma_f32_16x16x32_bf16 a[224:227], v[148:151], v[144:147], a[224:227]\n" \
"ds_read_b128 v[64:67], v28 offset:38912\n" \
"v_mfma_f32_16x16x32_bf16 a[196:199], v[152:155], v[140:143], a[196:199]\n" \
"ds_read_b128 v[68:71], v28 offset:40960\n" \
"v_mfma_f32_16x16x32_bf16 a[228:231], v[152:155], v[144:147], a[228:231]\n" \
"s_add_u32 m0, s30, 0x1400\n" \
"s_mov_b32 s84, 0xb800\n" \
"v_mfma_f32_16x16x32_bf16 a[200:203], v[156:159], v[140:143], a[200:203]\n" \
"buffer_load_dwordx4 v25, s[76:79], s84 offen lds\n" \
"v_mfma_f32_16x16x32_bf16 a[232:235], v[156:159], v[144:147], a[232:235]\n" \
"ds_read_b128 v[72:75], v28 offset:43008\n" \
"v_mfma_f32_16x16x32_bf16 a[204:207], v[160:163], v[140:143], a[204:207]\n" \
"ds_read_b128 v[76:79], v28 offset:45056\n" \
"v_mfma_f32_16x16x32_bf16 a[236:239], v[160:163], v[144:147], a[236:239]\n" \
"s_add_u32 m0, s31, 0x800\n" \
"s_mov_b32 s85, 0x410000\n" \
"v_mfma_f32_16x16x32_bf16 a[208:211], v[164:167], v[128:131], a[208:211]\n" \
"buffer_load_dwordx4 v27, s[68:71], s85 offen lds\n" \
"v_mfma_f32_16x16x32_bf16 a[212:215], v[168:171], v[128:131], a[212:215]\n" \
"ds_read_b128 v[80:83], v28 offset:47104\n" \
"v_mfma_f32_16x16x32_bf16 a[244:247], v[176:179], v[128:131], a[244:247]\n" \
"v_mfma_f32_16x16x32_bf16 a[240:243], v[128:131], v[172:175], a[240:243]\n" \
"s_waitcnt lgkmcnt(15)\n" \
"v_mfma_f32_16x16x32_bf16 a[24:27], v[104:107], v[248:251], a[24:27]\n" \
"s_add_u32 m0, s30, 0x1800\n" \
"s_mov_b32 s85, 0xdc00\n" \
"v_mfma_f32_16x16x32_bf16 a[28:31], v[248:251], v[108:111], a[28:31]\n" \
"buffer_load_dwordx4 v24, s[76:79], s85 offen lds\n" \
"v_mfma_f32_16x16x32_bf16 a[56:59], v[112:115], v[248:251], a[56:59]\n" \
"ds_read_b64_tr_b16 v[168:169], v37 offset:36864\n" \
"s_waitcnt lgkmcnt(8)\n" \
"v_mfma_f32_16x16x32_bf16 a[0:3], v[84:87], v[52:55], a[0:3]\n" \
"s_add_u32 m0, s30, 0x1c00\n" \
"s_mov_b32 s84, 0x10000\n" \
"s_waitcnt lgkmcnt(7)\n" \
"s_nop 0\n" \
"v_mfma_f32_16x16x32_bf16 a[32:35], v[84:87], v[56:59], a[32:35]\n" \
"buffer_load_dwordx4 v25, s[76:79], s84 offen lds\n" \
"v_mfma_f32_16x16x32_bf16 a[4:7], v[88:91], v[52:55], a[4:7]\n" \
"ds_read_b64_tr_b16 v[170:171], v37 offset:37376\n" \
"v_mfma_f32_16x16x32_bf16 a[36:39], v[88:91], v[56:59], a[36:39]\n" \
"s_add_u32 m0, s31, 0xc00\n" \
"s_mov_b32 s85, 0x418000\n" \
"v_mfma_f32_16x16x32_bf16 a[8:11], v[92:95], v[52:55], a[8:11]\n" \
"buffer_load_dwordx4 v27, s[68:71], s85 offen lds\n" \
"v_mfma_f32_16x16x32_bf16 a[40:43], v[92:95], v[56:59], a[40:43]\n" \
"ds_read_b64_tr_b16 v[172:173], v38 offset:36864\n" \
"v_mfma_f32_16x16x32_bf16 a[12:15], v[96:99], v[52:55], a[12:15]\n" \
"ds_read_b64_tr_b16 v[174:175], v38 offset:37376\n" \
"v_mfma_f32_16x16x32_bf16 a[44:47], v[96:99], v[56:59], a[44:47]\n" \
"ds_read_b64_tr_b16 v[176:177], v39 offset:36864\n" \
"v_mfma_f32_16x16x32_bf16 a[16:19], v[100:103], v[52:55], a[16:19]\n" \
"ds_read_b64_tr_b16 v[178:179], v39 offset:37376\n" \
"v_mfma_f32_16x16x32_bf16 a[20:23], v[104:107], v[52:55], a[20:23]\n" \
"ds_read_b64_tr_b16 v[148:149], v32 offset:40960\n" \
"v_mfma_f32_16x16x32_bf16 a[52:55], v[112:115], v[52:55], a[52:55]\n" \
"ds_read_b64_tr_b16 v[150:151], v32 offset:41984\n" \
"v_mfma_f32_16x16x32_bf16 a[48:51], v[52:55], v[108:111], a[48:51]\n" \
"ds_read_b128 v[248:251], v23 offset:32768\n" \
"s_waitcnt lgkmcnt(14)\n" \
"v_mfma_f32_16x16x32_bf16 a[64:67], v[84:87], v[60:63], a[64:67]\n" \
"ds_read_b64_tr_b16 v[152:153], v33 offset:40960\n" \
"s_waitcnt lgkmcnt(14)\n" \
"v_mfma_f32_16x16x32_bf16 a[96:99], v[84:87], v[64:67], a[96:99]\n" \
"ds_read_b64_tr_b16 v[154:155], v33 offset:41984\n" \
"v_mfma_f32_16x16x32_bf16 a[68:71], v[88:91], v[60:63], a[68:71]\n" \
"ds_read_b64_tr_b16 v[156:157], v34 offset:40960\n" \
"v_mfma_f32_16x16x32_bf16 a[100:103], v[88:91], v[64:67], a[100:103]\n" \
"ds_read_b64_tr_b16 v[158:159], v34 offset:41984\n" \
"v_mfma_f32_16x16x32_bf16 a[72:75], v[92:95], v[60:63], a[72:75]\n" \
"v_mfma_f32_16x16x32_bf16 a[104:107], v[92:95], v[64:67], a[104:107]\n" \
"ds_read_b64_tr_b16 v[160:161], v35 offset:40960\n" \
"v_mfma_f32_16x16x32_bf16 a[76:79], v[96:99], v[60:63], a[76:79]\n" \
"ds_read_b64_tr_b16 v[162:163], v35 offset:41984\n" \
"v_mfma_f32_16x16x32_bf16 a[108:111], v[96:99], v[64:67], a[108:111]\n" \
"v_mfma_f32_16x16x32_bf16 a[80:83], v[100:103], v[56:59], a[80:83]\n" \
"ds_read_b64_tr_b16 v[164:165], v36 offset:36864\n" \
"v_mfma_f32_16x16x32_bf16 a[84:87], v[104:107], v[56:59], a[84:87]\n" \
"ds_read_b64_tr_b16 v[166:167], v36 offset:37376\n" \
"v_mfma_f32_16x16x32_bf16 a[116:119], v[112:115], v[56:59], a[116:119]\n" \
"s_add_u32 s88, s42, 0x2000\n" \
"buffer_store_dwordx4 a[216:219], v20, s[80:83], s88 offen offset:0 sc1\n" \
"v_mfma_f32_16x16x32_bf16 a[112:115], v[56:59], v[108:111], a[112:115]\n" \
"ds_read_b128 v[116:119], v29 offset:32768\n" \
"s_waitcnt lgkmcnt(15)\n" \
"s_nop 0\n" \
"v_mfma_f32_16x16x32_bf16 a[128:131], v[84:87], v[68:71], a[128:131]\n" \
"v_mfma_f32_16x16x32_bf16 a[160:163], v[84:87], v[72:75], a[160:163]\n" \
"ds_read_b128 v[120:123], v29 offset:34816\n" \
"v_mfma_f32_16x16x32_bf16 a[132:135], v[88:91], v[68:71], a[132:135]\n" \
"ds_read_b128 v[124:127], v29 offset:36864\n" \
"v_mfma_f32_16x16x32_bf16 a[164:167], v[88:91], v[72:75], a[164:167]\n" \
"v_mfma_f32_16x16x32_bf16 a[136:139], v[92:95], v[68:71], a[136:139]\n" \
"ds_read_b128 v[128:131], v29 offset:38912\n" \
"v_mfma_f32_16x16x32_bf16 a[168:171], v[92:95], v[72:75], a[168:171]\n" \
"ds_read_b128 v[132:135], v29 offset:40960\n" \
"v_mfma_f32_16x16x32_bf16 a[140:143], v[96:99], v[68:71], a[140:143]\n" \
"v_mfma_f32_16x16x32_bf16 a[172:175], v[96:99], v[72:75], a[172:175]\n" \
"s_add_u32 s89, s42, 0x2000\n" \
"buffer_store_dwordx4 a[220:223], v20, s[80:83], s89 offen offset:1024 sc1\n" \
"v_mfma_f32_16x16x32_bf16 a[144:147], v[100:103], v[60:63], a[144:147]\n" \
"ds_read_b128 v[136:139], v29 offset:43008\n" \
"v_mfma_f32_16x16x32_bf16 a[148:151], v[104:107], v[60:63], a[148:151]\n" \
"ds_read_b128 v[140:143], v29 offset:45056\n" \
"v_mfma_f32_16x16x32_bf16 a[180:183], v[112:115], v[60:63], a[180:183]\n" \
"ds_read_b128 v[144:147], v29 offset:47104\n" \
"v_mfma_f32_16x16x32_bf16 a[176:179], v[60:63], v[108:111], a[176:179]\n" \
"v_mfma_f32_16x16x32_bf16 a[192:195], v[84:87], v[76:79], a[192:195]\n" \
"v_mfma_f32_16x16x32_bf16 a[224:227], v[84:87], v[80:83], a[224:227]\n" \
"v_mfma_f32_16x16x32_bf16 a[196:199], v[88:91], v[76:79], a[196:199]\n" \
"v_mfma_f32_16x16x32_bf16 a[228:231], v[88:91], v[80:83], a[228:231]\n" \
"s_add_u32 s88, s42, 0x2000\n" \
"buffer_store_dwordx4 v[184:187], v20, s[80:83], s88 offen offset:2048 sc1\n" \
"v_mfma_f32_16x16x32_bf16 a[200:203], v[92:95], v[76:79], a[200:203]\n" \
"v_mfma_f32_16x16x32_bf16 a[232:235], v[92:95], v[80:83], a[232:235]\n" \
"v_mfma_f32_16x16x32_bf16 a[204:207], v[96:99], v[76:79], a[204:207]\n" \
"v_mfma_f32_16x16x32_bf16 a[236:239], v[96:99], v[80:83], a[236:239]\n" \
"v_mfma_f32_16x16x32_bf16 a[208:211], v[100:103], v[64:67], a[208:211]\n" \
"v_mfma_f32_16x16x32_bf16 a[212:215], v[104:107], v[64:67], a[212:215]\n" \
"v_mfma_f32_16x16x32_bf16 a[244:247], v[112:115], v[64:67], a[244:247]\n" \
"v_mfma_f32_16x16x32_bf16 a[240:243], v[64:67], v[108:111], a[240:243]\n" \
"s_waitcnt vmcnt(3) lgkmcnt(0)\n" \
"s_barrier\n" \
"v_mfma_f32_16x16x32_bf16 a[24:27], v[168:171], v[248:251], a[24:27]\n" \
"s_add_u32 s89, s42, 0x3000\n" \
"buffer_store_dwordx4 a[248:251], v20, s[80:83], s89 offen offset:3072 sc1\n" \
"v_mfma_f32_16x16x32_bf16 a[28:31], v[248:251], v[172:175], a[28:31]\n" \
"ds_read_b64_tr_b16 v[104:105], v37 offset:0\n" \
"v_mfma_f32_16x16x32_bf16 a[56:59], v[176:179], v[248:251], a[56:59]\n" \
"ds_read_b64_tr_b16 v[106:107], v37 offset:512\n" \
"v_mfma_f32_16x16x32_bf16 a[0:3], v[148:151], v[116:119], a[0:3]\n" \
"ds_read_b64_tr_b16 v[108:109], v38 offset:0\n" \
"v_mfma_f32_16x16x32_bf16 a[32:35], v[148:151], v[120:123], a[32:35]\n" \
"ds_read_b64_tr_b16 v[110:111], v38 offset:512\n" \
"v_mfma_f32_16x16x32_bf16 a[4:7], v[152:155], v[116:119], a[4:7]\n" \
"v_mfma_f32_16x16x32_bf16 a[36:39], v[152:155], v[120:123], a[36:39]\n" \
"ds_read_b64_tr_b16 v[112:113], v39 offset:0\n" \
"v_mfma_f32_16x16x32_bf16 a[8:11], v[156:159], v[116:119], a[8:11]\n" \
"ds_read_b64_tr_b16 v[114:115], v39 offset:512\n" \
"v_mfma_f32_16x16x32_bf16 a[40:43], v[156:159], v[120:123], a[40:43]\n" \
"v_mfma_f32_16x16x32_bf16 a[12:15], v[160:163], v[116:119], a[12:15]\n" \
"ds_read_b64_tr_b16 v[84:85], v32 offset:0\n" \
"v_mfma_f32_16x16x32_bf16 a[44:47], v[160:163], v[120:123], a[44:47]\n" \
"ds_read_b64_tr_b16 v[86:87], v32 offset:1024\n" \
"v_mfma_f32_16x16x32_bf16 a[16:19], v[164:167], v[116:119], a[16:19]\n" \
"v_mfma_f32_16x16x32_bf16 a[20:23], v[168:171], v[116:119], a[20:23]\n" \
"s_add_u32 s88, s42, 0x3000\n" \
"buffer_store_dwordx4 a[252:255], v20, s[80:83], s88 offen offset:0 sc1\n" \
"v_mfma_f32_16x16x32_bf16 a[52:55], v[176:179], v[116:119], a[52:55]\n" \
"ds_read_b128 v[248:251], v22 offset:0\n" \
"v_mfma_f32_16x16x32_bf16 a[48:51], v[116:119], v[172:175], a[48:51]\n" \
"ds_read_b64_tr_b16 v[88:89], v33 offset:0\n" \
"v_mfma_f32_16x16x32_bf16 a[64:67], v[148:151], v[124:127], a[64:67]\n" \
"ds_read_b64_tr_b16 v[90:91], v33 offset:1024\n" \
"v_mfma_f32_16x16x32_bf16 a[96:99], v[148:151], v[128:131], a[96:99]\n" \
"v_mfma_f32_16x16x32_bf16 a[68:71], v[152:155], v[124:127], a[68:71]\n" \
"ds_read_b64_tr_b16 v[92:93], v34 offset:0\n" \
"v_mfma_f32_16x16x32_bf16 a[100:103], v[152:155], v[128:131], a[100:103]\n" \
"ds_read_b64_tr_b16 v[94:95], v34 offset:1024\n" \
"v_mfma_f32_16x16x32_bf16 a[72:75], v[156:159], v[124:127], a[72:75]\n" \
"v_mfma_f32_16x16x32_bf16 a[104:107], v[156:159], v[128:131], a[104:107]\n" \
"ds_read_b64_tr_b16 v[96:97], v35 offset:0\n" \
"v_mfma_f32_16x16x32_bf16 a[76:79], v[160:163], v[124:127], a[76:79]\n" \
"ds_read_b64_tr_b16 v[98:99], v35 offset:1024\n" \
"v_mfma_f32_16x16x32_bf16 a[108:111], v[160:163], v[128:131], a[108:111]\n" \
"v_mfma_f32_16x16x32_bf16 a[80:83], v[164:167], v[120:123], a[80:83]\n" \
"ds_read_b64_tr_b16 v[100:101], v36 offset:0\n" \
"v_mfma_f32_16x16x32_bf16 a[84:87], v[168:171], v[120:123], a[84:87]\n" \
"ds_read_b64_tr_b16 v[102:103], v36 offset:512\n" \
"v_mfma_f32_16x16x32_bf16 a[116:119], v[176:179], v[120:123], a[116:119]\n" \
"s_add_u32 s89, s42, 0x3000\n" \
"buffer_store_dwordx4 v[188:191], v20, s[80:83], s89 offen offset:1024 sc1\n" \
"v_mfma_f32_16x16x32_bf16 a[112:115], v[120:123], v[172:175], a[112:115]\n" \
"ds_read_b128 v[52:55], v28 offset:0\n" \
"v_mfma_f32_16x16x32_bf16 a[128:131], v[148:151], v[132:135], a[128:131]\n" \
"v_mfma_f32_16x16x32_bf16 a[160:163], v[148:151], v[136:139], a[160:163]\n" \
"ds_read_b128 v[56:59], v28 offset:2048\n" \
"v_mfma_f32_16x16x32_bf16 a[132:135], v[152:155], v[132:135], a[132:135]\n" \
"ds_read_b128 v[60:63], v28 offset:4096\n" \
"v_mfma_f32_16x16x32_bf16 a[164:167], v[152:155], v[136:139], a[164:167]\n" \
"v_mfma_f32_16x16x32_bf16 a[136:139], v[156:159], v[132:135], a[136:139]\n" \
"ds_read_b128 v[64:67], v28 offset:6144\n" \
"v_mfma_f32_16x16x32_bf16 a[168:171], v[156:159], v[136:139], a[168:171]\n" \
"ds_read_b128 v[68:71], v28 offset:8192\n" \
"v_mfma_f32_16x16x32_bf16 a[140:143], v[160:163], v[132:135], a[140:143]\n" \
"v_mfma_f32_16x16x32_bf16 a[172:175], v[160:163], v[136:139], a[172:175]\n" \
"ds_read_b128 v[72:75], v28 offset:10240\n" \
"v_mfma_f32_16x16x32_bf16 a[144:147], v[164:167], v[124:127], a[144:147]\n" \
"ds_read_b128 v[76:79], v28 offset:12288\n" \
"v_mfma_f32_16x16x32_bf16 a[148:151], v[168:171], v[124:127], a[148:151]\n" \
"v_mfma_f32_16x16x32_bf16 a[180:183], v[176:179], v[124:127], a[180:183]\n" \
"ds_read_b128 v[80:83], v28 offset:14336\n" \
"v_mfma_f32_16x16x32_bf16 a[176:179], v[124:127], v[172:175], a[176:179]\n" \
"s_add_u32 s88, s42, 0x3000\n" \
"buffer_store_dwordx4 v[252:255], v20, s[80:83], s88 offen offset:2048 sc1\n" \
"v_mfma_f32_16x16x32_bf16 a[192:195], v[148:151], v[140:143], a[192:195]\n" \
"v_mfma_f32_16x16x32_bf16 a[224:227], v[148:151], v[144:147], a[224:227]\n" \
"v_mfma_f32_16x16x32_bf16 a[196:199], v[152:155], v[140:143], a[196:199]\n" \
"v_mfma_f32_16x16x32_bf16 a[228:231], v[152:155], v[144:147], a[228:231]\n" \
"v_mfma_f32_16x16x32_bf16 a[200:203], v[156:159], v[140:143], a[200:203]\n" \
"v_mfma_f32_16x16x32_bf16 a[232:235], v[156:159], v[144:147], a[232:235]\n" \
"v_mfma_f32_16x16x32_bf16 a[204:207], v[160:163], v[140:143], a[204:207]\n" \
"v_mfma_f32_16x16x32_bf16 a[236:239], v[160:163], v[144:147], a[236:239]\n" \
"v_mfma_f32_16x16x32_bf16 a[208:211], v[164:167], v[128:131], a[208:211]\n" \
"v_mfma_f32_16x16x32_bf16 a[212:215], v[168:171], v[128:131], a[212:215]\n" \
"v_mfma_f32_16x16x32_bf16 a[244:247], v[176:179], v[128:131], a[244:247]\n" \
"v_mfma_f32_16x16x32_bf16 a[240:243], v[128:131], v[172:175], a[240:243]\n" \
"s_waitcnt lgkmcnt(15)\n" \
"v_mfma_f32_16x16x32_bf16 a[24:27], v[104:107], v[248:251], a[24:27]\n" \
"v_bfe_u32 v44, v20, 2, 5\n" \
"ds_read_b64_tr_b16 v[168:169], v37 offset:4096\n" \
"v_lshrrev_b32_e32 v24, 9, v20\n" \
"v_mfma_f32_16x16x32_bf16 a[28:31], v[248:251], v[108:111], a[28:31]\n" \
"buffer_load_dword v44, v44, s[80:83], s38 offen offset:0 sc1\n" \
"v_and_b32_e32 v26, 0x1ff, v20\n" \
"v_mfma_f32_16x16x32_bf16 a[56:59], v[112:115], v[248:251], a[56:59]\n" \
"ds_read_b64_tr_b16 v[170:171], v37 offset:4608\n" \
"v_lshl_add_u32 v24, v24, 3, v26\n" \
"v_lshrrev_b32_e32 v25, 8, v20\n" \
"s_waitcnt lgkmcnt(9)\n" \
"v_mfma_f32_16x16x32_bf16 a[0:3], v[84:87], v[52:55], a[0:3]\n" \
"ds_read_b64_tr_b16 v[172:173], v38 offset:4096\n" \
"v_mul_u32_u24_e32 v25, 0x24700, v25\n" \
"v_add_u32_e32 v25, v25, v20\n" \
"s_waitcnt lgkmcnt(9)\n" \
"v_mfma_f32_16x16x32_bf16 a[32:35], v[84:87], v[56:59], a[32:35]\n" \
"ds_read_b64_tr_b16 v[174:175], v38 offset:4608\n" \
"v_lshrrev_b32_e32 v26, 2, v20\n" \
"v_add_u32_e32 v26, s45, v26\n" \
"v_mfma_f32_16x16x32_bf16 a[4:7], v[88:91], v[52:55], a[4:7]\n" \
"v_add_u32_e32 v27, s46, v20\n" \
"v_add_u32_e32 v21, s43, v27\n" \
"v_accvgpr_read_b32 v180, a24\n" \
"v_mfma_f32_16x16x32_bf16 a[36:39], v[88:91], v[56:59], a[36:39]\n" \
"ds_read_b64_tr_b16 v[176:177], v39 offset:4096\n" \
"v_accvgpr_read_b32 v181, a25\n" \
"v_accvgpr_read_b32 v182, a26\n" \
"v_mfma_f32_16x16x32_bf16 a[8:11], v[92:95], v[52:55], a[8:11]\n" \
"ds_read_b64_tr_b16 v[178:179], v39 offset:4608\n" \
"v_accvgpr_read_b32 v183, a27\n" \
"v_accvgpr_read_b32 v252, a28\n" \
"v_mfma_f32_16x16x32_bf16 a[40:43], v[92:95], v[56:59], a[40:43]\n" \
"v_accvgpr_read_b32 v253, a29\n" \
"v_accvgpr_read_b32 v254, a30\n" \
"v_accvgpr_read_b32 v255, a31\n" \
"v_mfma_f32_16x16x32_bf16 a[12:15], v[96:99], v[52:55], a[12:15]\n" \
"ds_read_b64_tr_b16 v[148:149], v32 offset:8192\n" \
"v_accvgpr_read_b32 v48, a56\n" \
"v_accvgpr_read_b32 v49, a57\n" \
"v_mfma_f32_16x16x32_bf16 a[44:47], v[96:99], v[56:59], a[44:47]\n" \
"ds_read_b64_tr_b16 v[150:151], v32 offset:9216\n" \
"v_accvgpr_read_b32 v50, a58\n" \
"v_accvgpr_read_b32 v51, a59\n" \
"v_mfma_f32_16x16x32_bf16 a[16:19], v[100:103], v[52:55], a[16:19]\n" \
"v_mfma_f32_16x16x32_bf16 a[20:23], v[104:107], v[52:55], a[20:23]\n" \
"ds_read_b128 v[248:251], v23 offset:0\n" \
"v_mfma_f32_16x16x32_bf16 a[52:55], v[112:115], v[52:55], a[52:55]\n" \
"v_mfma_f32_16x16x32_bf16 a[48:51], v[52:55], v[108:111], a[48:51]\n" \
"ds_read_b64_tr_b16 v[152:153], v33 offset:8192\n" \
"s_nop 2\n" \
"v_accvgpr_read_b32 v184, a16\n" \
"v_accvgpr_read_b32 v185, a17\n" \
"s_waitcnt lgkmcnt(15)\n" \
"v_mfma_f32_16x16x32_bf16 a[64:67], v[84:87], v[60:63], a[64:67]\n" \
"ds_read_b64_tr_b16 v[154:155], v33 offset:9216\n" \
"v_accvgpr_read_b32 v186, a18\n" \
"v_accvgpr_read_b32 v187, a19\n" \
"s_waitcnt lgkmcnt(15)\n" \
"v_mfma_f32_16x16x32_bf16 a[96:99], v[84:87], v[64:67], a[96:99]\n" \
"v_accvgpr_read_b32 v188, a20\n" \
"v_accvgpr_read_b32 v189, a21\n" \
"v_accvgpr_read_b32 v190, a22\n" \
"v_mfma_f32_16x16x32_bf16 a[68:71], v[88:91], v[60:63], a[68:71]\n" \
"ds_read_b64_tr_b16 v[156:157], v34 offset:8192\n" \
"v_accvgpr_read_b32 v191, a23\n" \
"v_accvgpr_read_b32 v196, a52\n" \
"v_mfma_f32_16x16x32_bf16 a[100:103], v[88:91], v[64:67], a[100:103]\n" \
"v_lshlrev_b32_e32 v45, 3, v20\n" \
"v_add_u32_e64 v45, s30, v45\n" \
"v_accvgpr_read_b32 v197, a53\n" \
"v_mfma_f32_16x16x32_bf16 a[72:75], v[92:95], v[60:63], a[72:75]\n" \
"s_getpc_b64 s[86:87]\n" \
"L_ct0_%=:\n" \
"s_add_u32 s86, s86, L_end_%=-L_ct0_%=\n" \
"s_addc_u32 s87, s87, 0\n" \
"s_mov_b32 m0, s32\n" \
"s_nop 0\n" \
"v_mfma_f32_16x16x32_bf16 a[104:107], v[92:95], v[64:67], a[104:107]\n" \
"global_load_lds_dword v45, s[86:87]\n" \
"v_accvgpr_read_b32 v198, a54\n" \
"v_mfma_f32_16x16x32_bf16 a[76:79], v[96:99], v[60:63], a[76:79]\n" \
"ds_read_b64_tr_b16 v[158:159], v34 offset:9216\n" \
"v_accvgpr_read_b32 v199, a55\n" \
"v_accvgpr_read_b32 v192, a48\n" \
"v_mfma_f32_16x16x32_bf16 a[108:111], v[96:99], v[64:67], a[108:111]\n" \
"ds_read_b64_tr_b16 v[160:161], v35 offset:8192\n" \
"v_accvgpr_read_b32 v193, a49\n" \
"v_accvgpr_read_b32 v194, a50\n" \
"v_mfma_f32_16x16x32_bf16 a[80:83], v[100:103], v[56:59], a[80:83]\n" \
"ds_read_b64_tr_b16 v[162:163], v35 offset:9216\n" \
"v_accvgpr_read_b32 v195, a51\n" \
"v_mfma_f32_16x16x32_bf16 a[84:87], v[104:107], v[56:59], a[84:87]\n" \
"ds_read_b64_tr_b16 v[164:165], v36 offset:4096\n" \
"v_mfma_f32_16x16x32_bf16 a[116:119], v[112:115], v[56:59], a[116:119]\n" \
"ds_read_b64_tr_b16 v[166:167], v36 offset:4608\n" \
"v_mfma_f32_16x16x32_bf16 a[112:115], v[56:59], v[108:111], a[112:115]\n" \
"ds_read_b128 v[116:119], v29 offset:0\n" \
"v_accvgpr_read_b32 v200, a80\n" \
"v_accvgpr_read_b32 v201, a81\n" \
"s_waitcnt lgkmcnt(15)\n" \
"s_nop 0\n" \
"v_mfma_f32_16x16x32_bf16 a[128:131], v[84:87], v[68:71], a[128:131]\n" \
"v_accvgpr_read_b32 v202, a82\n" \
"v_accvgpr_read_b32 v203, a83\n" \
"v_accvgpr_read_b32 v204, a84\n" \
"v_mfma_f32_16x16x32_bf16 a[160:163], v[84:87], v[72:75], a[160:163]\n" \
"ds_read_b128 v[120:123], v29 offset:2048\n" \
"v_accvgpr_read_b32 v205, a85\n" \
"v_accvgpr_read_b32 v206, a86\n" \
"v_mfma_f32_16x16x32_bf16 a[132:135], v[88:91], v[68:71], a[132:135]\n" \
"ds_read_b128 v[124:127], v29 offset:4096\n" \
"v_accvgpr_read_b32 v207, a87\n" \
"v_accvgpr_read_b32 v212, a116\n" \
"v_mfma_f32_16x16x32_bf16 a[164:167], v[88:91], v[72:75], a[164:167]\n" \
"v_accvgpr_read_b32 v213, a117\n" \
"v_accvgpr_read_b32 v214, a118\n" \
"v_accvgpr_read_b32 v215, a119\n" \
"v_mfma_f32_16x16x32_bf16 a[136:139], v[92:95], v[68:71], a[136:139]\n" \
"ds_read_b128 v[128:131], v29 offset:6144\n" \
"v_accvgpr_read_b32 v208, a112\n" \
"v_accvgpr_read_b32 v209, a113\n" \
"v_mfma_f32_16x16x32_bf16 a[168:171], v[92:95], v[72:75], a[168:171]\n" \
"ds_read_b128 v[132:135], v29 offset:8192\n" \
"v_accvgpr_read_b32 v210, a114\n" \
"v_accvgpr_read_b32 v211, a115\n" \
"v_mfma_f32_16x16x32_bf16 a[140:143], v[96:99], v[68:71], a[140:143]\n" \
"v_mfma_f32_16x16x32_bf16 a[172:175], v[96:99], v[72:75], a[172:175]\n" \
"ds_read_b128 v[136:139], v29 offset:10240\n" \
"v_mfma_f32_16x16x32_bf16 a[144:147], v[100:103], v[60:63], a[144:147]\n" \
"ds_read_b128 v[140:143], v29 offset:12288\n" \
"v_mfma_f32_16x16x32_bf16 a[148:151], v[104:107], v[60:63], a[148:151]\n" \
"v_mfma_f32_16x16x32_bf16 a[180:183], v[112:115], v[60:63], a[180:183]\n" \
"ds_read_b128 v[144:147], v29 offset:14336\n" \
"v_mfma_f32_16x16x32_bf16 a[176:179], v[60:63], v[108:111], a[176:179]\n" \
"s_nop 2\n" \
"s_nop 0\n" \
"v_accvgpr_read_b32 v216, a144\n" \
"v_accvgpr_read_b32 v217, a145\n" \
"v_accvgpr_read_b32 v218, a146\n" \
"v_mfma_f32_16x16x32_bf16 a[192:195], v[84:87], v[76:79], a[192:195]\n" \
"v_accvgpr_read_b32 v219, a147\n" \
"v_accvgpr_read_b32 v220, a148\n" \
"v_accvgpr_read_b32 v221, a149\n" \
"v_mfma_f32_16x16x32_bf16 a[224:227], v[84:87], v[80:83], a[224:227]\n" \
"v_accvgpr_read_b32 v222, a150\n" \
"v_accvgpr_read_b32 v223, a151\n" \
"v_accvgpr_read_b32 v228, a180\n" \
"v_mfma_f32_16x16x32_bf16 a[196:199], v[88:91], v[76:79], a[196:199]\n" \
"v_accvgpr_read_b32 v229, a181\n" \
"v_accvgpr_read_b32 v230, a182\n" \
"v_accvgpr_read_b32 v231, a183\n" \
"v_mfma_f32_16x16x32_bf16 a[228:231], v[88:91], v[80:83], a[228:231]\n" \
"v_accvgpr_read_b32 v224, a176\n" \
"v_accvgpr_read_b32 v225, a177\n" \
"v_accvgpr_read_b32 v226, a178\n" \
"v_mfma_f32_16x16x32_bf16 a[200:203], v[92:95], v[76:79], a[200:203]\n" \
"v_accvgpr_read_b32 v227, a179\n" \
"v_mfma_f32_16x16x32_bf16 a[232:235], v[92:95], v[80:83], a[232:235]\n" \
"v_mfma_f32_16x16x32_bf16 a[204:207], v[96:99], v[76:79], a[204:207]\n" \
"v_mfma_f32_16x16x32_bf16 a[236:239], v[96:99], v[80:83], a[236:239]\n" \
"v_mfma_f32_16x16x32_bf16 a[208:211], v[100:103], v[64:67], a[208:211]\n" \
"v_mfma_f32_16x16x32_bf16 a[212:215], v[104:107], v[64:67], a[212:215]\n" \
"v_mfma_f32_16x16x32_bf16 a[244:247], v[112:115], v[64:67], a[244:247]\n" \
"v_mfma_f32_16x16x32_bf16 a[240:243], v[64:67], v[108:111], a[240:243]\n" \
"s_nop 4\n" \
"s_nop 0\n" \
"v_accvgpr_read_b32 v232, a208\n" \
"v_accvgpr_read_b32 v233, a209\n" \
"v_accvgpr_read_b32 v234, a210\n" \
"v_accvgpr_read_b32 v235, a211\n" \
"v_accvgpr_read_b32 v236, a212\n" \
"v_accvgpr_read_b32 v237, a213\n" \
"v_accvgpr_read_b32 v238, a214\n" \
"v_accvgpr_read_b32 v239, a215\n" \
"v_accvgpr_read_b32 v244, a244\n" \
"v_accvgpr_read_b32 v245, a245\n" \
"v_accvgpr_read_b32 v246, a246\n" \
"v_accvgpr_read_b32 v247, a247\n" \
"v_accvgpr_read_b32 v240, a240\n" \
"v_accvgpr_read_b32 v241, a241\n" \
"v_accvgpr_read_b32 v242, a242\n" \
"v_accvgpr_read_b32 v243, a243\n" \
"s_waitcnt vmcnt(1) lgkmcnt(0)\n" \
"s_barrier\n" \
"v_mov_b32_e32 v45, s62\n" \
"s_mov_b64 exec, 1\n" \
"buffer_store_dword v45, off, s[80:83], s37 offset:32 sc1\n" \
"s_mov_b64 exec, -1\n" \
"v_cmp_ne_u32_e32 vcc, s62, v44\n" \
"s_nop 4\n" \
"s_cbranch_vccnz L_poll160_%=\n" \
"L_pollr160_%=:\n" \
"v_mfma_f32_16x16x32_bf16 v[48:51], v[176:179], v[248:251], v[48:51]\n" \
"s_mov_b32 s73, s50\n" \
"s_nop 0\n" \
"s_sub_u32 m0, s31, 0x10000\n" \
"v_mfma_f32_16x16x32_bf16 v[180:183], v[168:171], v[248:251], v[180:183]\n" \
"buffer_load_dwordx4 v20, s[80:83], s73 offen offset:0 sc1 lds\n" \
"v_mfma_f32_16x16x32_bf16 v[252:255], v[248:251], v[172:175], v[252:255]\n" \
"s_add_u32 s74, s50, 0x4000\n" \
"s_mov_b32 s70, 0x3e0293ee\n" \
"v_mfma_f32_16x16x32_bf16 v[196:199], v[176:179], v[116:119], v[196:199]\n" \
"s_add_u32 s75, s50, 0x8000\n" \
"s_mov_b32 s64, 0xaaaaaaaa\n" \
"v_mfma_f32_16x16x32_bf16 v[188:191], v[168:171], v[116:119], v[188:191]\n" \
"v_cvt_pk_bf16_f32 v180, v180, v181\n" \
"v_cvt_pk_bf16_f32 v181, v182, v183\n" \
"v_mfma_f32_16x16x32_bf16 v[184:187], v[164:167], v[116:119], v[184:187]\n" \
"buffer_load_dwordx4 v20, s[80:83], s73 offen offset:1024 sc1 lds\n" \
"v_mfma_f32_16x16x32_bf16 v[192:195], v[116:119], v[172:175], v[192:195]\n" \
"v_cvt_pk_bf16_f32 v182, v50, v51\n" \
"v_mov_b32_e32 v183, 0x3f80\n" \
"v_mfma_f32_16x16x32_bf16 v[212:215], v[176:179], v[120:123], v[212:215]\n" \
"v_cvt_pk_bf16_f32 v188, v188, v189\n" \
"v_cvt_pk_bf16_f32 v189, v190, v191\n" \
"v_mfma_f32_16x16x32_bf16 v[204:207], v[168:171], v[120:123], v[204:207]\n" \
"v_cvt_pk_bf16_f32 v184, v184, v185\n" \
"v_cvt_pk_bf16_f32 v185, v186, v187\n" \
"v_mfma_f32_16x16x32_bf16 v[200:203], v[164:167], v[120:123], v[200:203]\n" \
"buffer_load_dwordx4 v20, s[80:83], s73 offen offset:2048 sc1 lds\n" \
"v_mfma_f32_16x16x32_bf16 v[208:211], v[120:123], v[172:175], v[208:211]\n" \
"v_cvt_pk_bf16_f32 v186, v196, v197\n" \
"v_mov_b32_e64 v187, 0\n" \
"v_mfma_f32_16x16x32_bf16 v[228:231], v[176:179], v[124:127], v[228:231]\n" \
"v_cvt_pk_bf16_f32 v204, v204, v205\n" \
"v_cvt_pk_bf16_f32 v205, v206, v207\n" \
"v_mfma_f32_16x16x32_bf16 v[220:223], v[168:171], v[124:127], v[220:223]\n" \
"v_cvt_pk_bf16_f32 v200, v200, v201\n" \
"v_cvt_pk_bf16_f32 v201, v202, v203\n" \
"v_mfma_f32_16x16x32_bf16 v[216:219], v[164:167], v[124:127], v[216:219]\n" \
"buffer_load_dwordx4 v20, s[80:83], s73 offen offset:3072 sc1 lds\n" \
"v_mfma_f32_16x16x32_bf16 v[224:227], v[124:127], v[172:175], v[224:227]\n" \
"s_sub_u32 m0, s31, s51\n" \
"s_nop 0\n" \
"v_cvt_pk_bf16_f32 v190, v198, v199\n" \
"v_mfma_f32_16x16x32_bf16 v[244:247], v[176:179], v[128:131], v[244:247]\n" \
"v_cvt_pk_bf16_f32 v220, v220, v221\n" \
"v_cvt_pk_bf16_f32 v221, v222, v223\n" \
"v_mfma_f32_16x16x32_bf16 v[236:239], v[168:171], v[128:131], v[236:239]\n" \
"v_cvt_pk_bf16_f32 v216, v216, v217\n" \
"v_cvt_pk_bf16_f32 v217, v218, v219\n" \
"v_mfma_f32_16x16x32_bf16 v[232:235], v[164:167], v[128:131], v[232:235]\n" \
"buffer_load_dwordx4 v20, s[80:83], s74 offen offset:0 sc1 lds\n" \
"v_mfma_f32_16x16x32_bf16 v[240:243], v[128:131], v[172:175], v[240:243]\n" \
"v_mov_b32_e32 v191, 0x3f80\n" \
"v_cvt_pk_bf16_f32 v54, v192, v193\n" \
"v_mfma_f32_16x16x32_bf16 a[0:3], v[148:151], v[116:119], a[0:3]\n" \
"v_cvt_pk_bf16_f32 v236, v236, v237\n" \
"v_cvt_pk_bf16_f32 v237, v238, v239\n" \
"v_mfma_f32_16x16x32_bf16 a[32:35], v[148:151], v[120:123], a[32:35]\n" \
"v_cvt_pk_bf16_f32 v232, v232, v233\n" \
"v_cvt_pk_bf16_f32 v233, v234, v235\n" \
"v_mfma_f32_16x16x32_bf16 a[4:7], v[152:155], v[116:119], a[4:7]\n" \
"buffer_load_dwordx4 v20, s[80:83], s74 offen offset:1024 sc1 lds\n" \
"v_mfma_f32_16x16x32_bf16 a[36:39], v[152:155], v[120:123], a[36:39]\n" \
"v_accvgpr_read_b32 v72, a0\n" \
"v_accvgpr_read_b32 v73, a1\n" \
"v_mfma_f32_16x16x32_bf16 a[8:11], v[156:159], v[116:119], a[8:11]\n" \
"v_accvgpr_read_b32 v74, a2\n" \
"v_accvgpr_read_b32 v75, a3\n" \
"v_mfma_f32_16x16x32_bf16 a[40:43], v[156:159], v[120:123], a[40:43]\n" \
"v_mul_f32_e32 v72, s70, v72\n" \
"v_mul_f32_e32 v73, s70, v73\n" \
"v_mfma_f32_16x16x32_bf16 a[12:15], v[160:163], v[116:119], a[12:15]\n" \
"buffer_load_dwordx4 v20, s[80:83], s74 offen offset:2048 sc1 lds\n" \
"v_mfma_f32_16x16x32_bf16 a[44:47], v[160:163], v[120:123], a[44:47]\n" \
"v_accvgpr_read_b32 v76, a4\n" \
"v_accvgpr_read_b32 v77, a5\n" \
"v_mfma_f32_16x16x32_bf16 a[64:67], v[148:151], v[124:127], a[64:67]\n" \
"v_accvgpr_read_b32 v78, a6\n" \
"v_accvgpr_read_b32 v79, a7\n" \
"v_mfma_f32_16x16x32_bf16 a[96:99], v[148:151], v[128:131], a[96:99]\n" \
"v_mul_f32_e32 v74, s70, v74\n" \
"v_mul_f32_e32 v75, s70, v75\n" \
"v_mfma_f32_16x16x32_bf16 a[68:71], v[152:155], v[124:127], a[68:71]\n" \
"buffer_load_dwordx4 v20, s[80:83], s74 offen offset:3072 sc1 lds\n" \
"v_mfma_f32_16x16x32_bf16 a[100:103], v[152:155], v[128:131], a[100:103]\n" \
"v_cvt_pk_bf16_f32 v72, v72, v73\n" \
"s_sub_u32 m0, s31, 0x4000\n" \
"v_mfma_f32_16x16x32_bf16 a[72:75], v[156:159], v[124:127], a[72:75]\n" \
"v_mul_f32_e32 v76, s70, v76\n" \
"v_mul_f32_e32 v77, s70, v77\n" \
"v_mfma_f32_16x16x32_bf16 a[104:107], v[156:159], v[128:131], a[104:107]\n" \
"v_mul_f32_e32 v78, s70, v78\n" \
"v_mul_f32_e32 v79, s70, v79\n" \
"v_mfma_f32_16x16x32_bf16 a[76:79], v[160:163], v[124:127], a[76:79]\n" \
"v_cvt_pk_bf16_f32 v73, v74, v75\n" \
"v_cvt_pk_bf16_f32 v74, v76, v77\n" \
"v_mfma_f32_16x16x32_bf16 a[108:111], v[160:163], v[128:131], a[108:111]\n" \
"buffer_load_dwordx4 v20, s[80:83], s75 offen offset:0 sc1 lds\n" \
"v_cvt_pk_bf16_f32 v75, v78, v79\n" \
"ds_write_b128 v27, v[72:75] offset:0\n" \
"v_mfma_f32_16x16x32_bf16 a[128:131], v[148:151], v[132:135], a[128:131]\n" \
"v_accvgpr_read_b32 v72, a8\n" \
"v_accvgpr_read_b32 v73, a9\n" \
"v_accvgpr_read_b32 v74, a10\n" \
"v_accvgpr_read_b32 v75, a11\n" \
"v_mul_f32_e32 v72, s70, v72\n" \
"v_mul_f32_e32 v73, s70, v73\n" \
"v_accvgpr_read_b32 v76, a12\n" \
"v_accvgpr_read_b32 v77, a13\n" \
"v_accvgpr_read_b32 v78, a14\n" \
"v_accvgpr_read_b32 v79, a15\n" \
"v_mul_f32_e32 v74, s70, v74\n" \
"v_mul_f32_e32 v75, s70, v75\n" \
"v_cvt_pk_bf16_f32 v72, v72, v73\n" \
"v_mfma_f32_16x16x32_bf16 a[160:163], v[148:151], v[136:139], a[160:163]\n" \
"v_mul_f32_e32 v76, s70, v76\n" \
"v_mul_f32_e32 v77, s70, v77\n" \
"v_mul_f32_e32 v78, s70, v78\n" \
"v_mul_f32_e32 v79, s70, v79\n" \
"v_cvt_pk_bf16_f32 v73, v74, v75\n" \
"v_cvt_pk_bf16_f32 v74, v76, v77\n" \
"v_cvt_pk_bf16_f32 v75, v78, v79\n" \
"ds_write_b128 v27, v[72:75] offset:1024\n" \
"v_mfma_f32_16x16x32_bf16 a[132:135], v[152:155], v[132:135], a[132:135]\n" \
"v_accvgpr_read_b32 v72, a32\n" \
"v_accvgpr_read_b32 v73, a33\n" \
"v_accvgpr_read_b32 v74, a34\n" \
"v_accvgpr_read_b32 v75, a35\n" \
"v_mul_f32_e32 v72, s70, v72\n" \
"v_mul_f32_e32 v73, s70, v73\n" \
"v_accvgpr_read_b32 v76, a36\n" \
"v_accvgpr_read_b32 v77, a37\n" \
"v_accvgpr_read_b32 v78, a38\n" \
"v_accvgpr_read_b32 v79, a39\n" \
"v_mul_f32_e32 v74, s70, v74\n" \
"v_mul_f32_e32 v75, s70, v75\n" \
"v_cvt_pk_bf16_f32 v72, v72, v73\n" \
"v_mul_f32_e32 v76, s70, v76\n" \
"v_mul_f32_e32 v77, s70, v77\n" \
"v_mul_f32_e32 v78, s70, v78\n" \
"v_mul_f32_e32 v79, s70, v79\n" \
"v_cvt_pk_bf16_f32 v73, v74, v75\n" \
"v_mfma_f32_16x16x32_bf16 a[164:167], v[152:155], v[136:139], a[164:167]\n" \
"v_cvt_pk_bf16_f32 v74, v76, v77\n" \
"v_cvt_pk_bf16_f32 v75, v78, v79\n" \
"ds_write_b128 v27, v[72:75] offset:4096\n" \
"buffer_load_dwordx4 v20, s[80:83], s75 offen offset:1024 sc1 lds\n" \
"v_cvt_pk_bf16_f32 v55, v194, v195\n" \
"v_cvt_pk_bf16_f32 v206, v214, v215\n" \
"v_mov_b32_e32 v207, 0x3f80\n" \
"v_cvt_pk_bf16_f32 v202, v212, v213\n" \
"v_mov_b32_e64 v203, 0\n" \
"v_cvt_pk_bf16_f32 v56, v208, v209\n" \
"v_cvt_pk_bf16_f32 v57, v210, v211\n" \
"v_cvt_pk_bf16_f32 v222, v230, v231\n" \
"v_mov_b32_e32 v223, 0x3f80\n" \
"v_cvt_pk_bf16_f32 v218, v228, v229\n" \
"v_mov_b32_e64 v219, 0\n" \
"v_cvt_pk_bf16_f32 v58, v224, v225\n" \
"v_cvt_pk_bf16_f32 v59, v226, v227\n" \
"v_cvt_pk_bf16_f32 v238, v246, v247\n" \
"v_mov_b32_e32 v239, 0x3f80\n" \
"v_cvt_pk_bf16_f32 v234, v244, v245\n" \
"v_mov_b32_e64 v235, 0\n" \
"v_cvt_pk_bf16_f32 v60, v240, v241\n" \
"v_cvt_pk_bf16_f32 v61, v242, v243\n" \
"v_mfma_f32_16x16x32_bf16 v[164:167], v[180:183], v[184:187], 0\n" \
"v_accvgpr_read_b32 v72, a40\n" \
"v_accvgpr_read_b32 v73, a41\n" \
"v_mfma_f32_16x16x32_bf16 a[136:139], v[156:159], v[132:135], a[136:139]\n" \
"v_mfma_f32_16x16x32_bf16 v[192:195], v[188:191], v[184:187], 0\n" \
"v_mfma_f32_16x16x32_bf16 v[208:211], v[204:207], v[200:203], 0\n" \
"v_mfma_f32_16x16x32_bf16 v[168:171], v[188:191], v[200:203], 0\n" \
"v_mfma_f32_16x16x32_bf16 v[224:227], v[220:223], v[216:219], 0\n" \
"v_mfma_f32_16x16x32_bf16 v[172:175], v[204:207], v[216:219], 0\n" \
"v_mfma_f32_16x16x32_bf16 v[240:243], v[236:239], v[232:235], 0\n" \
"v_mfma_f32_16x16x32_bf16 v[176:179], v[220:223], v[232:235], 0\n" \
"v_accvgpr_read_b32 v74, a42\n" \
"v_accvgpr_read_b32 v75, a43\n" \
"v_mul_f32_e32 v72, s70, v72\n" \
"v_mul_f32_e32 v73, s70, v73\n" \
"v_accvgpr_read_b32 v76, a44\n" \
"v_accvgpr_read_b32 v77, a45\n" \
"v_accvgpr_read_b32 v78, a46\n" \
"v_accvgpr_read_b32 v79, a47\n" \
"v_mul_f32_e32 v74, s70, v74\n" \
"v_mul_f32_e32 v75, s70, v75\n" \
"v_cvt_pk_bf16_f32 v72, v72, v73\n" \
"v_mul_f32_e32 v76, s70, v76\n" \
"v_mul_f32_e32 v77, s70, v77\n" \
"v_mul_f32_e32 v78, s70, v78\n" \
"v_mul_f32_e32 v79, s70, v79\n" \
"v_cvt_pk_bf16_f32 v73, v74, v75\n" \
"v_cvt_pk_bf16_f32 v74, v76, v77\n" \
"v_cvt_pk_bf16_f32 v75, v78, v79\n" \
"v_mfma_f32_16x16x32_bf16 a[168:171], v[156:159], v[136:139], a[168:171]\n" \
"ds_write_b128 v27, v[72:75] offset:5120\n" \
"v_accvgpr_read_b32 v116, a64\n" \
"v_accvgpr_read_b32 v117, a65\n" \
"v_accvgpr_read_b32 v72, a72\n" \
"v_accvgpr_read_b32 v73, a73\n" \
"v_accvgpr_read_b32 v118, a66\n" \
"v_accvgpr_read_b32 v119, a67\n" \
"v_mul_f32_e32 v116, s70, v116\n" \
"v_mul_f32_e32 v117, s70, v117\n" \
"v_accvgpr_read_b32 v74, a74\n" \
"v_accvgpr_read_b32 v75, a75\n" \
"v_mul_f32_e32 v72, s70, v72\n" \
"v_mul_f32_e32 v73, s70, v73\n" \
"v_mfma_f32_16x16x32_bf16 a[140:143], v[160:163], v[132:135], a[140:143]\n" \
"s_mov_b32 s65, 0xaaaaaaaa\n" \
"v_accvgpr_read_b32 v120, a68\n" \
"v_accvgpr_read_b32 v121, a69\n" \
"v_accvgpr_read_b32 v122, a70\n" \
"v_accvgpr_read_b32 v123, a71\n" \
"v_mul_f32_e32 v118, s70, v118\n" \
"v_mul_f32_e32 v119, s70, v119\n" \
"v_cvt_pk_bf16_f32 v116, v116, v117\n" \
"v_accvgpr_read_b32 v76, a76\n" \
"v_accvgpr_read_b32 v77, a77\n" \
"v_accvgpr_read_b32 v78, a78\n" \
"v_accvgpr_read_b32 v79, a79\n" \
"v_mul_f32_e32 v74, s70, v74\n" \
"v_mul_f32_e32 v75, s70, v75\n" \
"v_cvt_pk_bf16_f32 v72, v72, v73\n" \
"v_cndmask_b32_e64 v164, v164, v165, s[64:65]\n" \
"v_cndmask_b32_e64 v166, v166, v167, s[64:65]\n" \
"v_mul_f32_e32 v120, s70, v120\n" \
"v_mul_f32_e32 v121, s70, v121\n" \
"v_mul_f32_e32 v122, s70, v122\n" \
"v_mul_f32_e32 v123, s70, v123\n" \
"v_cvt_pk_bf16_f32 v117, v118, v119\n" \
"v_mul_f32_e32 v76, s70, v76\n" \
"v_mul_f32_e32 v77, s70, v77\n" \
"v_mul_f32_e32 v78, s70, v78\n" \
"v_mul_f32_e32 v79, s70, v79\n" \
"v_cvt_pk_bf16_f32 v73, v74, v75\n" \
"s_mov_b32 s66, 0xfff0ffff\n" \
"s_mov_b32 s67, 0xf000ff00\n" \
"s_mov_b32 s68, 0xffc0fffc\n" \
"s_mov_b32 s69, 0xc000fc00\n" \
"v_cndmask_b32_e64 v192, v192, v193, s[64:65]\n" \
"v_add_f32_e64 v164, s47, v164\n" \
"v_cndmask_b32_e64 v194, v194, v195, s[64:65]\n" \
"v_add_f32_e64 v166, s47, v166\n" \
"v_cndmask_b32_e64 v208, v208, v209, s[64:65]\n" \
"v_cndmask_b32_e64 v168, v168, v169, s[64:65]\n" \
"v_cndmask_b32_e64 v210, v210, v211, s[64:65]\n" \
"v_cndmask_b32_e64 v170, v170, v171, s[64:65]\n" \
"v_cndmask_b32_e64 v224, v224, v225, s[64:65]\n" \
"v_cndmask_b32_e64 v172, v172, v173, s[64:65]\n" \
"v_cndmask_b32_e64 v226, v226, v227, s[64:65]\n" \
"v_cndmask_b32_e64 v174, v174, v175, s[64:65]\n" \
"v_cndmask_b32_e64 v240, v240, v241, s[64:65]\n" \
"v_cndmask_b32_e64 v176, v176, v177, s[64:65]\n" \
"v_cndmask_b32_e64 v242, v242, v243, s[64:65]\n" \
"v_cndmask_b32_e64 v178, v178, v179, s[64:65]\n" \
"v_cvt_pk_bf16_f32 v118, v120, v121\n" \
"v_cvt_pk_bf16_f32 v119, v122, v123\n" \
"v_cvt_pk_bf16_f32 v74, v76, v77\n" \
"v_cvt_pk_bf16_f32 v75, v78, v79\n" \
"v_cndmask_b32_e64 v192, v164, v192, s[66:67]\n" \
"v_cndmask_b32_e64 v194, v166, v194, s[68:69]\n" \
"v_cndmask_b32_e64 v208, v168, v208, s[66:67]\n" \
"v_cndmask_b32_e64 v210, v170, v210, s[68:69]\n" \
"v_cndmask_b32_e64 v224, v172, v224, s[66:67]\n" \
"v_cndmask_b32_e64 v226, v174, v226, s[68:69]\n" \
"v_cndmask_b32_e64 v240, v176, v240, s[66:67]\n" \
"v_cndmask_b32_e64 v242, v178, v242, s[68:69]\n" \
"ds_write_b128 v27, v[116:119] offset:8192\n" \
"ds_write_b128 v27, v[72:75] offset:9216\n" \
"v_mfma_f32_16x16x32_bf16 a[172:175], v[160:163], v[136:139], a[172:175]\n" \
"v_max_f32_e32 v193, v192, v194\n" \
"v_max_f32_e32 v209, v208, v210\n" \
"v_max_f32_e32 v225, v224, v226\n" \
"v_max_f32_e32 v241, v240, v242\n" \
"s_mov_b32 s71, 0x3e0293ee\n" \
"v_permlane32_swap_b32_e32 v193, v225\n" \
"v_permlane32_swap_b32_e32 v209, v241\n" \
"v_accvgpr_read_b32 v116, a96\n" \
"v_accvgpr_read_b32 v117, a97\n" \
"v_accvgpr_read_b32 v72, a104\n" \
"v_accvgpr_read_b32 v73, a105\n" \
"v_accvgpr_read_b32 v124, a128\n" \
"v_accvgpr_read_b32 v125, a129\n" \
"buffer_load_dwordx4 v20, s[80:83], s75 offen offset:2048 sc1 lds\n" \
"v_permlane32_swap_b32_e32 v196, v228\n" \
"v_permlane32_swap_b32_e32 v212, v244\n" \
"v_max_f32_e32 v193, v193, v225\n" \
"v_max_f32_e32 v209, v209, v241\n" \
"v_accvgpr_read_b32 v118, a98\n" \
"v_accvgpr_read_b32 v119, a99\n" \
"v_mul_f32_e32 v116, s70, v116\n" \
"v_mul_f32_e32 v117, s70, v117\n" \
"v_accvgpr_read_b32 v74, a106\n" \
"v_accvgpr_read_b32 v75, a107\n" \
"v_mul_f32_e32 v72, s70, v72\n" \
"v_mul_f32_e32 v73, s70, v73\n" \
"v_accvgpr_read_b32 v126, a130\n" \
"v_accvgpr_read_b32 v127, a131\n" \
"v_pk_mul_f32 v[124:125], v[124:125], s[70:71]\n" \
"v_accvgpr_read_b32 v84, a192\n" \
"v_accvgpr_read_b32 v85, a193\n" \
"v_accvgpr_read_b32 v86, a194\n" \
"v_accvgpr_read_b32 v87, a195\n" \
"v_permlane16_swap_b32_e32 v228, v244\n" \
"v_permlane16_swap_b32_e32 v193, v209\n" \
"v_accvgpr_read_b32 v120, a100\n" \
"v_accvgpr_read_b32 v121, a101\n" \
"v_accvgpr_read_b32 v122, a102\n" \
"v_accvgpr_read_b32 v123, a103\n" \
"v_mul_f32_e32 v118, s70, v118\n" \
"v_mul_f32_e32 v119, s70, v119\n" \
"v_cvt_pk_bf16_f32 v116, v116, v117\n" \
"v_accvgpr_read_b32 v76, a108\n" \
"v_accvgpr_read_b32 v77, a109\n" \
"v_accvgpr_read_b32 v78, a110\n" \
"v_accvgpr_read_b32 v79, a111\n" \
"v_mul_f32_e32 v74, s70, v74\n" \
"v_mul_f32_e32 v75, s70, v75\n" \
"v_cvt_pk_bf16_f32 v72, v72, v73\n" \
"v_accvgpr_read_b32 v128, a132\n" \
"v_accvgpr_read_b32 v129, a133\n" \
"v_accvgpr_read_b32 v130, a134\n" \
"v_accvgpr_read_b32 v131, a135\n" \
"v_pk_mul_f32 v[126:127], v[126:127], s[70:71]\n" \
"v_cvt_pk_bf16_f32 v124, v124, v125\n" \
"v_mfma_f32_16x16x32_bf16 v[84:87], v[148:151], v[140:143], v[84:87]\n" \
"s_mov_b32 s72, 0x3eae1a93\n" \
"v_mul_f32_e32 v45, 0xbfb8aa3b, v228\n" \
"v_mfma_f32_16x16x16_bf16 v[48:51], v[182:183], v[46:47], 0\n" \
"v_mfma_f32_16x16x16_bf16 v[196:199], v[190:191], v[46:47], 0\n" \
"v_mfma_f32_16x16x16_bf16 v[212:215], v[206:207], v[46:47], 0\n" \
"v_max_f32_e32 v193, v193, v209\n" \
"v_mul_f32_e32 v120, s70, v120\n" \
"v_mul_f32_e32 v121, s70, v121\n" \
"v_mul_f32_e32 v122, s70, v122\n" \
"v_mul_f32_e64 v123, s70, v123\n" \
"v_cvt_pk_bf16_f32 v117, v118, v119\n" \
"v_mul_f32_e32 v76, s70, v76\n" \
"v_mul_f32_e32 v77, s70, v77\n" \
"v_mul_f32_e32 v78, s70, v78\n" \
"v_mul_f32_e32 v79, s70, v79\n" \
"v_cvt_pk_bf16_f32 v73, v74, v75\n" \
"v_pk_mul_f32 v[128:129], v[128:129], s[70:71]\n" \
"v_pk_mul_f32 v[130:131], v[130:131], s[70:71]\n" \
"v_cvt_pk_bf16_f32 v125, v126, v127\n" \
"v_mfma_f32_16x16x16_bf16 v[228:231], v[222:223], v[46:47], 0\n" \
"v_mul_f32_e64 v193, s72, v193\n" \
"v_cvt_pk_bf16_f32 v118, v120, v121\n" \
"v_cvt_pk_bf16_f32 v119, v122, v123\n" \
"v_cvt_pk_bf16_f32 v74, v76, v77\n" \
"v_cvt_pk_bf16_f32 v75, v78, v79\n" \
"v_cvt_pk_bf16_f32 v126, v128, v129\n" \
"v_cvt_pk_bf16_f32 v127, v130, v131\n" \
"v_mfma_f32_16x16x16_bf16 v[244:247], v[238:239], v[46:47], 0\n" \
"v_mov_b32_e64 v209, v193\n" \
"ds_write_b128 v27, v[116:119] offset:12288\n" \
"ds_write_b128 v27, v[72:75] offset:13312\n" \
"ds_write_b128 v21, v[124:127] offset:16384\n" \
"v_accvgpr_read_b32 v100, a224\n" \
"v_accvgpr_read_b32 v101, a225\n" \
"v_accvgpr_read_b32 v102, a226\n" \
"v_accvgpr_read_b32 v103, a227\n" \
"v_permlane16_swap_b32_e32 v193, v209\n" \
"v_mov_b32_e32 v225, v193\n" \
"v_mfma_f32_16x16x32_bf16 v[100:103], v[148:151], v[144:147], v[100:103]\n" \
"v_mov_b32_e64 v241, v209\n" \
"v_accvgpr_read_b32 v116, a136\n" \
"v_accvgpr_read_b32 v117, a137\n" \
"v_accvgpr_read_b32 v72, a160\n" \
"v_accvgpr_read_b32 v73, a161\n" \
"v_accvgpr_read_b32 v124, a168\n" \
"v_accvgpr_read_b32 v125, a169\n" \
"v_permlane32_swap_b32_e32 v193, v225\n" \
"v_permlane32_swap_b32_e32 v209, v241\n" \
"v_accvgpr_read_b32 v118, a138\n" \
"v_accvgpr_read_b32 v119, a139\n" \
"v_pk_mul_f32 v[116:117], v[116:117], s[70:71]\n" \
"v_accvgpr_read_b32 v74, a162\n" \
"v_accvgpr_read_b32 v75, a163\n" \
"v_pk_mul_f32 v[72:73], v[72:73], s[70:71]\n" \
"v_accvgpr_read_b32 v126, a170\n" \
"v_accvgpr_read_b32 v127, a171\n" \
"v_pk_mul_f32 v[124:125], v[124:125], s[70:71]\n" \
"v_pk_mul_f32 v[84:85], v[84:85], s[70:71]\n" \
"v_pk_mul_f32 v[100:101], v[100:101], s[70:71]\n" \
"v_fma_f32 v192, v192, s72, -v193\n" \
"v_fma_f32 v194, v194, s72, -v193\n" \
"v_fma_f32 v208, v208, s72, -v209\n" \
"v_fma_f32 v210, v210, s72, -v209\n" \
"v_accvgpr_read_b32 v120, a140\n" \
"v_accvgpr_read_b32 v121, a141\n" \
"v_accvgpr_read_b32 v122, a142\n" \
"v_accvgpr_read_b32 v123, a143\n" \
"v_pk_mul_f32 v[118:119], v[118:119], s[70:71]\n" \
"v_cvt_pk_bf16_f32 v116, v116, v117\n" \
"v_accvgpr_read_b32 v76, a164\n" \
"v_accvgpr_read_b32 v77, a165\n" \
"v_accvgpr_read_b32 v78, a166\n" \
"v_accvgpr_read_b32 v79, a167\n" \
"v_pk_mul_f32 v[74:75], v[74:75], s[70:71]\n" \
"v_cvt_pk_bf16_f32 v72, v72, v73\n" \
"v_accvgpr_read_b32 v128, a172\n" \
"v_accvgpr_read_b32 v129, a173\n" \
"v_accvgpr_read_b32 v130, a174\n" \
"v_accvgpr_read_b32 v131, a175\n" \
"v_pk_mul_f32 v[126:127], v[126:127], s[70:71]\n" \
"v_cvt_pk_bf16_f32 v124, v124, v125\n" \
"v_pk_mul_f32 v[86:87], v[86:87], s[70:71]\n" \
"v_cvt_pk_bf16_f32 v84, v84, v85\n" \
"v_pk_mul_f32 v[102:103], v[102:103], s[70:71]\n" \
"v_cvt_pk_bf16_f32 v100, v100, v101\n" \
"v_accvgpr_read_b32 v88, a196\n" \
"v_accvgpr_read_b32 v89, a197\n" \
"v_accvgpr_read_b32 v90, a198\n" \
"v_accvgpr_read_b32 v91, a199\n" \
"v_exp_f32_e32 v192, v192\n" \
"v_exp_f32_e32 v194, v194\n" \
"v_exp_f32_e32 v208, v208\n" \
"v_exp_f32_e32 v210, v210\n" \
"v_fma_f32 v224, v224, s72, -v225\n" \
"v_fma_f32 v226, v226, s72, -v225\n" \
"v_pk_mul_f32 v[120:121], v[120:121], s[70:71]\n" \
"v_pk_mul_f32 v[122:123], v[122:123], s[70:71]\n" \
"v_cvt_pk_bf16_f32 v117, v118, v119\n" \
"v_pk_mul_f32 v[76:77], v[76:77], s[70:71]\n" \
"v_pk_mul_f32 v[78:79], v[78:79], s[70:71]\n" \
"v_cvt_pk_bf16_f32 v73, v74, v75\n" \
"v_pk_mul_f32 v[128:129], v[128:129], s[70:71]\n" \
"v_pk_mul_f32 v[130:131], v[130:131], s[70:71]\n" \
"v_cvt_pk_bf16_f32 v125, v126, v127\n" \
"v_cvt_pk_bf16_f32 v85, v86, v87\n" \
"v_cvt_pk_bf16_f32 v101, v102, v103\n" \
"v_mfma_f32_16x16x32_bf16 v[88:91], v[152:155], v[140:143], v[88:91]\n" \
"v_exp_f32_e32 v224, v224\n" \
"v_exp_f32_e32 v226, v226\n" \
"v_fma_f32 v240, v240, s72, -v241\n" \
"v_fma_f32 v242, v242, s72, -v241\n" \
"v_cvt_pk_bf16_f32 v118, v120, v121\n" \
"v_cvt_pk_bf16_f32 v119, v122, v123\n" \
"v_cvt_pk_bf16_f32 v74, v76, v77\n" \
"v_cvt_pk_bf16_f32 v75, v78, v79\n" \
"v_pk_mul_f32 v[88:89], v[88:89], s[70:71]\n" \
"v_pk_mul_f32 v[90:91], v[90:91], s[70:71]\n" \
"v_cvt_pk_bf16_f32 v126, v128, v129\n" \
"v_cvt_pk_bf16_f32 v127, v130, v131\n" \
"v_cvt_pk_bf16_f32 v86, v88, v89\n" \
"v_cvt_pk_bf16_f32 v87, v90, v91\n" \
"v_cvt_pk_bf16_f32 v192, v192, v194\n" \
"v_cvt_pk_bf16_f32 v208, v208, v210\n" \
"v_exp_f32_e32 v240, v240\n" \
"v_exp_f32_e32 v242, v242\n" \
"ds_write_b128 v21, v[116:119] offset:17408\n" \
"ds_write_b128 v21, v[72:75] offset:20480\n" \
"ds_write_b128 v21, v[124:127] offset:21504\n" \
"ds_write_b128 v21, v[84:87] offset:24576\n" \
"v_cvt_pk_bf16_f32 v62, v48, v49\n" \
"v_cvt_pk_bf16_f32 v63, v50, v51\n" \
"v_cvt_pk_bf16_f32 v64, v196, v197\n" \
"v_cvt_pk_bf16_f32 v65, v198, v199\n" \
"v_cvt_pk_bf16_f32 v66, v212, v213\n" \
"v_cvt_pk_bf16_f32 v67, v214, v215\n" \
"v_perm_b32 v184, v192, v192, v40\n" \
"v_perm_b32 v185, v192, v192, v41\n" \
"v_perm_b32 v186, v192, v192, v42\n" \
"v_perm_b32 v187, v192, v192, v43\n" \
"v_perm_b32 v200, v208, v208, v40\n" \
"v_perm_b32 v201, v208, v208, v41\n" \
"v_perm_b32 v202, v208, v208, v42\n" \
"v_perm_b32 v203, v208, v208, v43\n" \
"v_cvt_pk_bf16_f32 v224, v224, v226\n" \
"v_accvgpr_read_b32 v104, a228\n" \
"v_accvgpr_read_b32 v105, a229\n" \
"v_accvgpr_read_b32 v106, a230\n" \
"v_accvgpr_read_b32 v107, a231\n" \
"v_cvt_pk_bf16_f32 v68, v228, v229\n" \
"v_cvt_pk_bf16_f32 v69, v230, v231\n" \
"v_mfma_f32_16x16x32_bf16 v[164:167], v[62:65], v[184:187], 0\n" \
"v_mfma_f32_16x16x32_bf16 v[168:171], v[64:67], v[200:203], 0\n" \
"v_perm_b32 v216, v224, v224, v40\n" \
"v_perm_b32 v217, v224, v224, v41\n" \
"v_perm_b32 v218, v224, v224, v42\n" \
"v_perm_b32 v219, v224, v224, v43\n" \
"v_cvt_pk_bf16_f32 v240, v240, v242\n" \
"v_mfma_f32_16x16x32_bf16 v[104:107], v[152:155], v[144:147], v[104:107]\n" \
"v_cvt_pk_bf16_f32 v70, v244, v245\n" \
"v_cvt_pk_bf16_f32 v71, v246, v247\n" \
"v_mfma_f32_16x16x32_bf16 v[172:175], v[66:69], v[216:219], 0\n" \
"v_perm_b32 v232, v240, v240, v40\n" \
"v_perm_b32 v233, v240, v240, v41\n" \
"v_perm_b32 v234, v240, v240, v42\n" \
"v_perm_b32 v235, v240, v240, v43\n" \
"buffer_load_dwordx4 v20, s[80:83], s75 offen offset:3072 sc1 lds\n" \
"v_pk_mul_f32 v[104:105], v[104:105], s[70:71]\n" \
"v_pk_mul_f32 v[106:107], v[106:107], s[70:71]\n" \
"v_cvt_pk_bf16_f32 v102, v104, v105\n" \
"v_cvt_pk_bf16_f32 v103, v106, v107\n" \
"ds_write_b128 v21, v[100:103] offset:28672\n" \
"v_mfma_f32_16x16x32_bf16 v[176:179], v[68:71], v[232:235], 0\n" \
"v_accvgpr_read_b32 v92, a200\n" \
"v_accvgpr_read_b32 v93, a201\n" \
"v_accvgpr_read_b32 v94, a202\n" \
"v_accvgpr_read_b32 v95, a203\n" \
"v_accvgpr_read_b32 v108, a232\n" \
"v_accvgpr_read_b32 v109, a233\n" \
"v_mfma_f32_16x16x32_bf16 v[92:95], v[156:159], v[140:143], v[92:95]\n" \
"v_accvgpr_read_b32 v110, a234\n" \
"v_accvgpr_read_b32 v111, a235\n" \
"v_cvt_pk_bf16_f32 v52, v252, v253\n" \
"v_cvt_pk_bf16_f32 v53, v254, v255\n" \
"v_mfma_f32_16x16x32_bf16 v[108:111], v[156:159], v[144:147], v[108:111]\n" \
"v_exp_f32_e64 v45, v45\n" \
"v_mfma_f32_16x16x32_bf16 v[192:195], v[52:55], v[184:187], 0\n" \
"v_mfma_f32_16x16x32_bf16 v[208:211], v[54:57], v[200:203], 0\n" \
"v_pk_mul_f32 v[92:93], v[92:93], s[70:71]\n" \
"v_pk_mul_f32 v[94:95], v[94:95], s[70:71]\n" \
"v_cvt_pk_bf16_f32 v92, v92, v93\n" \
"v_cvt_pk_bf16_f32 v93, v94, v95\n" \
"v_mfma_f32_16x16x32_bf16 v[224:227], v[56:59], v[216:219], 0\n" \
"v_pk_mul_f32 v[108:109], v[108:109], s[70:71]\n" \
"v_pk_mul_f32 v[110:111], v[110:111], s[70:71]\n" \
"v_cvt_pk_bf16_f32 v108, v108, v109\n" \
"v_cvt_pk_bf16_f32 v109, v110, v111\n" \
"v_mfma_f32_16x16x32_bf16 v[240:243], v[58:61], v[232:235], 0\n" \
"v_mov_b32_dpp v166, v170 quad_perm:[0,1,2,3] row_mask:0x2 bank_mask:0xf\n" \
"v_accvgpr_read_b32 v96, a204\n" \
"v_accvgpr_read_b32 v97, a205\n" \
"v_mov_b32_dpp v166, v174 quad_perm:[0,1,2,3] row_mask:0x4 bank_mask:0xf\n" \
"v_accvgpr_read_b32 v98, a206\n" \
"v_accvgpr_read_b32 v99, a207\n" \
"v_add_f32_e64 v45, 1.0, v45\n" \
"v_mov_b32_dpp v166, v178 quad_perm:[0,1,2,3] row_mask:0x8 bank_mask:0xf\n" \
"v_mfma_f32_16x16x32_bf16 v[96:99], v[160:163], v[140:143], v[96:99]\n" \
"v_rcp_f32_e64 v45, v45\n" \
"v_mov_b32_dpp v164, v168 quad_perm:[0,1,2,3] row_mask:0x2 bank_mask:0xf\n" \
"v_mov_b32_dpp v165, v169 quad_perm:[0,1,2,3] row_mask:0x2 bank_mask:0xf\n" \
"v_rcp_f32_e64 v166, v166\n" \
"v_mov_b32_dpp v164, v172 quad_perm:[0,1,2,3] row_mask:0x4 bank_mask:0xf\n" \
"v_mov_b32_dpp v165, v173 quad_perm:[0,1,2,3] row_mask:0x4 bank_mask:0xf\n" \
"v_mul_f32_e64 v166, v45, v166\n" \
"v_mov_b32_dpp v164, v176 quad_perm:[0,1,2,3] row_mask:0x8 bank_mask:0xf\n" \
"v_pk_mul_f32 v[96:97], v[96:97], s[70:71]\n" \
"v_pk_mul_f32 v[98:99], v[98:99], s[70:71]\n" \
"v_cvt_pk_bf16_f32 v94, v96, v97\n" \
"v_cvt_pk_bf16_f32 v95, v98, v99\n" \
"ds_write_b128 v21, v[92:95] offset:25600\n" \
"v_mov_b32_dpp v165, v177 quad_perm:[0,1,2,3] row_mask:0x8 bank_mask:0xf\n" \
"v_accvgpr_read_b32 v112, a236\n" \
"v_accvgpr_read_b32 v113, a237\n" \
"v_accvgpr_read_b32 v114, a238\n" \
"v_accvgpr_read_b32 v115, a239\n" \
"v_mul_f32_e32 v164, v166, v164\n" \
"v_mul_f32_e32 v165, v166, v165\n" \
"v_mov_b32_e64 v170, v166\n" \
"v_mfma_f32_16x16x32_bf16 v[112:115], v[160:163], v[144:147], v[112:115]\n" \
"s_mov_b32 s76, s52\n" \
"v_permlane16_swap_b32_e32 v166, v170\n" \
"s_mov_b32 m0, s53\n" \
"v_mov_b32_e32 v174, v166\n" \
"v_mov_b32_e64 v178, v170\n" \
"buffer_load_dwordx4 v20, s[80:83], s76 offen offset:0 sc1 lds\n" \
"v_permlane32_swap_b32_e32 v166, v174\n" \
"v_permlane32_swap_b32_e32 v170, v178\n" \
"v_pk_mul_f32 v[112:113], v[112:113], s[70:71]\n" \
"v_pk_mul_f32 v[114:115], v[114:115], s[70:71]\n" \
"v_cvt_pk_bf16_f32 v110, v112, v113\n" \
"v_cvt_pk_bf16_f32 v111, v114, v115\n" \
"ds_write_b128 v21, v[108:111] offset:29696\n" \
"v_mul_f32_e32 v192, v166, v192\n" \
"v_mul_f32_e32 v193, v166, v193\n" \
"v_mul_f32_e32 v208, v170, v208\n" \
"v_mul_f32_e32 v209, v170, v209\n" \
"v_mul_f32_e64 v224, v174, v224\n" \
"buffer_load_dwordx4 v20, s[80:83], s76 offen offset:1024 sc1 lds\n" \
"v_mul_f32_e32 v225, v174, v225\n" \
"v_mul_f32_e32 v240, v178, v240\n" \
"v_mul_f32_e32 v241, v178, v241\n" \
"v_mul_f32_e32 v194, v166, v194\n" \
"v_mul_f32_e64 v195, v166, v195\n" \
"v_cvt_pk_bf16_f32 v192, v192, v193\n" \
"v_mul_f32_e32 v210, v170, v210\n" \
"v_mul_f32_e32 v211, v170, v211\n" \
"v_cvt_pk_bf16_f32 v208, v208, v209\n" \
"v_mul_f32_e32 v226, v174, v226\n" \
"v_mul_f32_e32 v227, v174, v227\n" \
"v_cvt_pk_bf16_f32 v224, v224, v225\n" \
"v_mul_f32_e32 v242, v178, v242\n" \
"v_mul_f32_e32 v243, v178, v243\n" \
"v_cvt_pk_bf16_f32 v240, v240, v241\n" \
"v_sub_f32_e32 v44, 1.0, v45\n" \
"v_cvt_pk_bf16_f32 v164, v164, v165\n" \
"v_cvt_pk_bf16_f32 v193, v194, v195\n" \
"s_add_u32 s84, s48, 0x0\n" \
"v_cvt_pk_bf16_f32 v209, v210, v211\n" \
"s_add_u32 s85, s48, 0x24800\n" \
"v_cvt_pk_bf16_f32 v225, v226, v227\n" \
"s_add_u32 s86, s48, 0x49000\n" \
"v_cvt_pk_bf16_f32 v241, v242, v243\n" \
"s_add_u32 s87, s48, 0x6d800\n" \
"ds_write_b32 v26, v44\n" \
"buffer_store_dword v164, v25, s[80:83], s49 offen sc1\n" \
"buffer_store_dwordx2 v[192:193], v24, s[80:83], s84 offen sc1\n" \
"buffer_store_dwordx2 v[208:209], v24, s[80:83], s85 offen sc1\n" \
"buffer_store_dwordx2 v[224:225], v24, s[80:83], s86 offen sc1\n" \
"buffer_store_dwordx2 v[240:241], v24, s[80:83], s87 offen sc1\n" \
"s_branch L_end_%=\n" \
"L_poll60_%=:\n" \
"s_mov_b32 s84, 0\n" \
"L_pl60_%=:\n" \
"s_sleep 1\n" \
"v_bfe_u32 v44, v20, 2, 5\n" \
"buffer_load_dword v44, v44, s[80:83], s38 offen offset:128 sc1\n" \
"s_waitcnt vmcnt(0)\n" \
"v_cmp_ne_u32_e32 vcc, s62, v44\n" \
"s_add_u32 s84, s84, 1\n" \
"s_nop 3\n" \
"s_cbranch_vccz L_pollr60_%=\n" \
"s_cmp_gt_u32 s84, 0x400000\n" \
"s_cbranch_scc0 L_pl60_%=\n" \
"s_branch L_pollr60_%=\n" \
"L_poll160_%=:\n" \
"s_mov_b32 s84, 0\n" \
"L_pl160_%=:\n" \
"s_sleep 1\n" \
"v_bfe_u32 v44, v20, 2, 5\n" \
"buffer_load_dword v44, v44, s[80:83], s38 offen offset:0 sc1\n" \
"s_waitcnt vmcnt(0)\n" \
"v_cmp_ne_u32_e32 vcc, s62, v44\n" \
"s_add_u32 s84, s84, 1\n" \
"s_nop 3\n" \
"s_cbranch_vccz L_pollr160_%=\n" \
"s_cmp_gt_u32 s84, 0x400000\n" \
"s_cbranch_scc0 L_pl160_%=\n" \
"s_branch L_pollr160_%=\n" \
"L_w1_%=:\n" \
"s_waitcnt vmcnt(18)\n" \
"s_barrier\n" \
"s_nop 0\n" \
"ds_read_b64_tr_b16 v[84:85], v32 offset:0\n" \
"ds_read_b64_tr_b16 v[86:87], v32 offset:1024\n" \
"ds_read_b128 v[52:55], v28 offset:16384\n" \
"ds_read_b128 v[56:59], v28 offset:18432\n" \
"ds_read_b64_tr_b16 v[88:89], v33 offset:0\n" \
"ds_read_b64_tr_b16 v[90:91], v33 offset:1024\n" \
"ds_read_b64_tr_b16 v[92:93], v34 offset:0\n" \
"ds_read_b64_tr_b16 v[94:95], v34 offset:1024\n" \
"ds_read_b64_tr_b16 v[96:97], v35 offset:0\n" \
"ds_read_b64_tr_b16 v[98:99], v35 offset:1024\n" \
"ds_read_b64_tr_b16 v[100:101], v36 offset:0\n" \
"ds_read_b64_tr_b16 v[102:103], v36 offset:1024\n" \
"ds_read_b64_tr_b16 v[104:105], v37 offset:0\n" \
"ds_read_b64_tr_b16 v[106:107], v37 offset:1024\n" \
"ds_read_b64_tr_b16 v[108:109], v38 offset:0\n" \
"ds_read_b64_tr_b16 v[110:111], v38 offset:1024\n" \
"ds_read_b64_tr_b16 v[112:113], v39 offset:0\n" \
"ds_read_b64_tr_b16 v[114:115], v39 offset:1024\n" \
"ds_read_b128 v[60:63], v28 offset:20480\n" \
"ds_read_b128 v[64:67], v28 offset:22528\n" \
"ds_read_b128 v[68:71], v28 offset:0\n" \
"ds_read_b128 v[72:75], v28 offset:2048\n" \
"ds_read_b128 v[76:79], v28 offset:4096\n" \
"ds_read_b128 v[80:83], v28 offset:6144\n" \
"s_waitcnt lgkmcnt(15)\n" \
"v_mfma_f32_16x16x32_bf16 a[0:3], v[52:55], v[84:87], 0\n" \
"ds_read_b64_tr_b16 v[148:149], v32 offset:8192\n" \
"s_lshl_b32 s4, s2, 2\n" \
"v_mfma_f32_16x16x32_bf16 a[32:35], v[56:59], v[84:87], 0\n" \
"v_lshrrev_b32_e32 v218, 1, v221\n" \
"s_and_b32 s4, s4, 28\n" \
"v_mfma_f32_16x16x32_bf16 a[4:7], v[52:55], v[88:91], 0\n" \
"ds_read_b64_tr_b16 v[150:151], v32 offset:9216\n" \
"s_bfe_u32 s5, s2, 0x20003\n" \
"v_mfma_f32_16x16x32_bf16 a[36:39], v[56:59], v[88:91], 0\n" \
"v_and_b32_e32 v218, 4, v218\n" \
"s_or_b32 s4, s4, s5\n" \
"v_mfma_f32_16x16x32_bf16 a[8:11], v[52:55], v[92:95], 0\n" \
"ds_read_b128 v[116:119], v29 offset:16384\n" \
"v_bitop3_b32 v215, v215, v218, v217 bitop3:0x1e\n" \
"v_mfma_f32_16x16x32_bf16 a[40:43], v[56:59], v[92:95], 0\n" \
"s_lshl_b32 s5, s4, 8\n" \
"v_lshlrev_b32_e32 v214, 7, v221\n" \
"s_waitcnt lgkmcnt(15)\n" \
"v_mfma_f32_16x16x32_bf16 a[12:15], v[52:55], v[96:99], 0\n" \
"ds_read_b128 v[120:123], v29 offset:18432\n" \
"v_lshlrev_b32_e32 v215, 4, v215\n" \
"v_mfma_f32_16x16x32_bf16 a[44:47], v[56:59], v[96:99], 0\n" \
"s_or_b32 s38, s5, 0x5be8000\n" \
"s_mul_i32 s5, s3, 18\n" \
"v_mfma_f32_16x16x32_bf16 a[16:19], v[52:55], v[100:103], 0\n" \
"ds_read_b64_tr_b16 v[152:153], v33 offset:8192\n" \
"v_add3_u32 v214, v214, s34, v215\n" \
"v_mfma_f32_16x16x32_bf16 a[48:51], v[56:59], v[100:103], 0\n" \
"v_mov_b32_e32 v215, v213\n" \
"s_add_i32 s35, s35, s5\n" \
"s_waitcnt lgkmcnt(15)\n" \
"v_mfma_f32_16x16x32_bf16 a[20:23], v[52:55], v[104:107], 0\n" \
"ds_read_b64_tr_b16 v[154:155], v33 offset:9216\n" \
"v_add_u32_e64 v212, s35, v212\n" \
"v_mfma_f32_16x16x32_bf16 a[52:55], v[56:59], v[104:107], 0\n" \
"v_mov_b32_e32 v219, 2.0\n" \
"v_cndmask_b32_e32 v197, v215, v214, vcc\n" \
"s_waitcnt lgkmcnt(14)\n" \
"v_mfma_f32_16x16x32_bf16 a[24:27], v[52:55], v[108:111], 0\n" \
"s_movk_i32 s5, 0x120\n" \
"v_mul_u32_u24_e32 v214, 0xd80, v212\n" \
"v_mfma_f32_16x16x32_bf16 a[56:59], v[56:59], v[108:111], 0\n" \
"ds_read_b64_tr_b16 v[156:157], v34 offset:8192\n" \
"v_cmp_gt_u32_e32 vcc, s5, v20\n" \
"s_waitcnt lgkmcnt(13)\n" \
"v_mfma_f32_16x16x32_bf16 a[28:31], v[52:55], v[112:115], 0\n" \
"v_lshrrev_b32_e32 v215, 1, v212\n" \
"s_movk_i32 s5, 0x60\n" \
"v_mfma_f32_16x16x32_bf16 a[60:63], v[56:59], v[112:115], 0\n" \
"ds_read_b64_tr_b16 v[158:159], v34 offset:9216\n" \
"v_cndmask_b32_e32 v194, v219, v214, vcc\n" \
"s_waitcnt lgkmcnt(13)\n" \
"v_mfma_f32_16x16x32_bf16 a[92:95], v[60:63], v[112:115], 0\n" \
"s_mov_b32 m0, s32\n" \
"v_lshlrev_b32_e32 v214, 7, v212\n" \
"s_waitcnt lgkmcnt(12)\n" \
"v_mfma_f32_16x16x32_bf16 a[124:127], v[64:67], v[112:115], 0\n" \
"s_mov_b32 exec_lo, 0xffff0000\n" \
"s_mov_b32 exec_hi, 0x0\n" \
"global_load_lds_dword v[184:185], off\n" \
"s_mov_b64 exec, -1\n" \
"v_mfma_f32_16x16x32_bf16 a[88:91], v[60:63], v[108:111], 0\n" \
"ds_read_b64_tr_b16 v[160:161], v35 offset:8192\n" \
"v_lshrrev_b32_e32 v212, 2, v212\n" \
"v_mfma_f32_16x16x32_bf16 a[120:123], v[64:67], v[108:111], 0\n" \
"ds_read_b64_tr_b16 v[162:163], v35 offset:9216\n" \
"v_and_b32_e64 v212, 2, v212\n" \
"v_mfma_f32_16x16x32_bf16 a[84:87], v[60:63], v[104:107], 0\n" \
"v_and_or_b32 v212, v215, 1, v212\n" \
"v_lshlrev_b32_e64 v212, 5, v212\n" \
"v_mfma_f32_16x16x32_bf16 a[116:119], v[64:67], v[104:107], 0\n" \
"s_mov_b32 m0, s32\n" \
"ds_read_b64_tr_b16 v[164:165], v36 offset:8192\n" \
"v_mfma_f32_16x16x32_bf16 a[80:83], v[60:63], v[100:103], 0\n" \
"s_mov_b32 exec_lo, 0xffff\n" \
"s_mov_b32 exec_hi, 0x0\n" \
"global_load_lds_dword v[186:187], off\n" \
"s_mov_b64 exec, -1\n" \
"v_mfma_f32_16x16x32_bf16 a[112:115], v[64:67], v[100:103], 0\n" \
"ds_read_b64_tr_b16 v[166:167], v36 offset:9216\n" \
"v_bitop3_b32 v212, v212, s5, v214 bitop3:0x36\n" \
"v_mfma_f32_16x16x32_bf16 a[76:79], v[60:63], v[96:99], 0\n" \
"v_add_u32_e32 v212, s34, v212\n" \
"s_mul_i32 s34, s4, 0x12000\n" \
"v_mfma_f32_16x16x32_bf16 a[108:111], v[64:67], v[96:99], 0\n" \
"s_or_b32 s36, s34, 0x6000000\n" \
"s_mul_i32 s34, s4, 36\n" \
"v_mfma_f32_16x16x32_bf16 a[72:75], v[60:63], v[92:95], 0\n" \
"ds_read_b64_tr_b16 v[168:169], v37 offset:8192\n" \
"s_add_i32 s4, s7, s36\n" \
"v_mfma_f32_16x16x32_bf16 a[104:107], v[64:67], v[92:95], 0\n" \
"s_lshl_b32 s5, s3, 10\n" \
"s_addk_i32 s4, 0x2000\n" \
"v_mfma_f32_16x16x32_bf16 a[68:71], v[60:63], v[88:91], 0\n" \
"ds_read_b64_tr_b16 v[170:171], v37 offset:9216\n" \
"s_mov_b32 s37, 2.0\n" \
"v_mfma_f32_16x16x32_bf16 a[100:103], v[64:67], v[88:91], 0\n" \
"v_cndmask_b32_e32 v198, v213, v212, vcc\n" \
"v_or_b32_e64 v212, s5, v20\n" \
"v_mfma_f32_16x16x32_bf16 a[64:67], v[60:63], v[84:87], 0\n" \
"ds_read_b64_tr_b16 v[172:173], v38 offset:8192\n" \
"s_or_b32 s39, s36, s5\n" \
"v_mfma_f32_16x16x32_bf16 a[96:99], v[64:67], v[84:87], 0\n" \
"s_cmp_lg_u32 s3, 0\n" \
"s_cselect_b32 s36, s37, s4\n" \
"s_waitcnt lgkmcnt(15)\n" \
"v_mfma_f32_16x16x32_bf16 a[128:131], v[68:71], v[84:87], 0\n" \
"ds_read_b64_tr_b16 v[174:175], v38 offset:9216\n" \
"s_lshl_b32 s4, s6, 2\n" \
"v_mfma_f32_16x16x32_bf16 a[160:163], v[72:75], v[84:87], 0\n" \
"v_or_b32_e32 v199, 0x22000, v212\n" \
"s_add_i32 s35, s39, s7\n" \
"v_mfma_f32_16x16x32_bf16 a[132:135], v[68:71], v[88:91], 0\n" \
"ds_read_b64_tr_b16 v[176:177], v39 offset:8192\n" \
"s_or_b32 s4, s38, s4\n" \
"v_mfma_f32_16x16x32_bf16 a[164:167], v[72:75], v[88:91], 0\n" \
"s_cmpk_lt_u32 s3, 3\n" \
"s_cselect_b32 s37, s37, s4\n" \
"v_mfma_f32_16x16x32_bf16 a[136:139], v[68:71], v[92:95], 0\n" \
"ds_read_b64_tr_b16 v[178:179], v39 offset:9216\n" \
"v_mov_b32_e32 v220, 0xffff\n" \
"v_mfma_f32_16x16x32_bf16 a[168:171], v[72:75], v[92:95], 0\n" \
"s_and_b32 s4, s3, 1\n" \
"s_lshl_b32 s6, s4, 4\n" \
"v_mfma_f32_16x16x32_bf16 a[140:143], v[68:71], v[96:99], 0\n" \
"v_lshrrev_b32_e32 v212, 4, v20\n" \
"v_lshrrev_b32_e64 v213, 6, v20\n" \
"v_mfma_f32_16x16x32_bf16 a[172:175], v[72:75], v[96:99], 0\n" \
"ds_read_b128 v[124:127], v29 offset:20480\n" \
"v_and_b32_e64 v222, 12, v213\n" \
"v_mfma_f32_16x16x32_bf16 a[144:147], v[68:71], v[100:103], 0\n" \
"v_and_or_b32 v221, v212, 15, s6\n" \
"v_cmp_gt_u32_e64 vcc, v222, v221\n" \
"v_mfma_f32_16x16x32_bf16 a[176:179], v[72:75], v[100:103], 0\n" \
"ds_read_b128 v[128:131], v29 offset:22528\n" \
"v_mov_b32_e32 v223, 0xffff0000\n" \
"v_mfma_f32_16x16x32_bf16 a[148:151], v[68:71], v[104:107], 0\n" \
"v_or_b32_e64 v217, 2, v222\n" \
"v_cndmask_b32_e64 v214, v220, 0, vcc\n" \
"v_mfma_f32_16x16x32_bf16 a[180:183], v[72:75], v[104:107], 0\n" \
"ds_read_b128 v[132:135], v29 offset:0\n" \
"v_cmp_lt_u32_e64 vcc, v222, v221\n" \
"v_mfma_f32_16x16x32_bf16 a[152:155], v[68:71], v[108:111], 0\n" \
"v_or_b32_e32 v219, 16, v222\n" \
"v_or_b32_e32 v224, 18, v222\n" \
"v_mfma_f32_16x16x32_bf16 a[184:187], v[72:75], v[108:111], 0\n" \
"ds_read_b128 v[136:139], v29 offset:2048\n" \
"v_cndmask_b32_e64 v215, 0, v223, vcc\n" \
"v_mfma_f32_16x16x32_bf16 a[156:159], v[68:71], v[112:115], 0\n" \
"v_cmp_gt_u32_e32 vcc, v217, v221\n" \
"s_lshl_b32 s7, s2, 2\n" \
"v_mfma_f32_16x16x32_bf16 a[188:191], v[72:75], v[112:115], 0\n" \
"ds_read_b128 v[140:143], v29 offset:4096\n" \
"s_or_b32 s7, s7, s3\n" \
"v_mfma_f32_16x16x32_bf16 a[220:223], v[76:79], v[112:115], 0\n" \
"v_cndmask_b32_e64 v216, v220, 0, vcc\n" \
"v_cmp_lt_u32_e32 vcc, v217, v221\n" \
"v_mfma_f32_16x16x32_bf16 a[252:255], v[80:83], v[112:115], 0\n" \
"ds_read_b128 v[144:147], v29 offset:6144\n" \
"s_add_i32 s42, s7, 0xffffffaf\n" \
"v_mfma_f32_16x16x32_bf16 a[216:219], v[76:79], v[108:111], 0\n" \
"s_mov_b32 s6, 0\n" \
"v_cndmask_b32_e32 v217, 0, v223, vcc\n" \
"v_mfma_f32_16x16x32_bf16 a[248:251], v[80:83], v[108:111], 0\n" \
"v_cmp_gt_u32_e64 vcc, v219, v221\n" \
"v_mfma_f32_16x16x32_bf16 a[212:215], v[76:79], v[104:107], 0\n" \
"s_cmpk_lt_u32 s7, 0x51\n" \
"s_cselect_b32 s41, s23, s25\n" \
"v_mfma_f32_16x16x32_bf16 a[244:247], v[80:83], v[104:107], 0\n" \
"v_mov_b32_e64 v11, 0\n" \
"v_mfma_f32_16x16x32_bf16 a[208:211], v[76:79], v[100:103], 0\n" \
"s_cmpk_lt_u32 s7, 0x51\n" \
"s_cselect_b32 s40, s22, s24\n" \
"v_mfma_f32_16x16x32_bf16 a[240:243], v[80:83], v[100:103], 0\n" \
"s_lshl_b32 s44, s3, 1\n" \
"v_cndmask_b32_e64 v218, v220, 0, vcc\n" \
"v_mfma_f32_16x16x32_bf16 a[204:207], v[76:79], v[96:99], 0\n" \
"v_cmp_lt_u32_e32 vcc, v219, v221\n" \
"v_mfma_f32_16x16x32_bf16 a[236:239], v[80:83], v[96:99], 0\n" \
"s_cmpk_lt_u32 s7, 0x291\n" \
"s_cselect_b32 s42, s42, s6\n" \
"v_mfma_f32_16x16x32_bf16 a[200:203], v[76:79], v[92:95], 0\n" \
"s_lshr_b32 s5, s2, 5\n" \
"v_mfma_f32_16x16x32_bf16 a[232:235], v[80:83], v[92:95], 0\n" \
"s_cmpk_lt_u32 s7, 0x51\n" \
"s_cselect_b32 s7, s7, s42\n" \
"v_mfma_f32_16x16x32_bf16 a[196:199], v[76:79], v[88:91], 0\n" \
"s_movk_i32 s42, 0xf400\n" \
"v_cndmask_b32_e64 v219, 0, v223, vcc\n" \
"v_mfma_f32_16x16x32_bf16 a[228:231], v[80:83], v[88:91], 0\n" \
"v_cmp_gt_u32_e64 vcc, v224, v221\n" \
"s_bfe_u32 s43, s2, 0x20003\n" \
"v_mfma_f32_16x16x32_bf16 a[192:195], v[76:79], v[84:87], 0\n" \
"s_lshl_b32 s47, s3, 8\n" \
"v_cndmask_b32_e64 v220, v220, 0, vcc\n" \
"v_mfma_f32_16x16x32_bf16 a[224:227], v[80:83], v[84:87], 0\n" \
"s_waitcnt vmcnt(4) lgkmcnt(0)\n" \
"s_barrier\n" \
"v_mfma_f32_16x16x32_bf16 a[0:3], v[116:119], v[148:151], a[0:3]\n" \
"s_add_u32 m0, s30, 0x0\n" \
"s_mov_b32 s85, 0x100\n" \
"v_mfma_f32_16x16x32_bf16 a[32:35], v[120:123], v[148:151], a[32:35]\n" \
"buffer_load_dwordx4 v22, s[64:67], s85 offen lds\n" \
"v_mfma_f32_16x16x32_bf16 a[4:7], v[116:119], v[152:155], a[4:7]\n" \
"ds_read_b64_tr_b16 v[84:85], v32 offset:32768\n" \
"v_cmp_lt_u32_e64 vcc, v224, v221\n" \
"v_mfma_f32_16x16x32_bf16 a[36:39], v[120:123], v[152:155], a[36:39]\n" \
"ds_read_b64_tr_b16 v[86:87], v32 offset:33792\n" \
"s_min_u32 s49, s3, 2\n" \
"v_mfma_f32_16x16x32_bf16 a[8:11], v[116:119], v[156:159], a[8:11]\n" \
"s_add_u32 m0, s30, 0x400\n" \
"s_mov_b32 s84, 0x2100\n" \
"v_mfma_f32_16x16x32_bf16 a[40:43], v[120:123], v[156:159], a[40:43]\n" \
"buffer_load_dwordx4 v23, s[64:67], s84 offen lds\n" \
"v_mfma_f32_16x16x32_bf16 a[12:15], v[116:119], v[160:163], a[12:15]\n" \
"ds_read_b128 v[52:55], v28 offset:49152\n" \
"s_mul_i32 s50, s43, 0x120\n" \
"v_mfma_f32_16x16x32_bf16 a[44:47], v[120:123], v[160:163], a[44:47]\n" \
"ds_read_b128 v[56:59], v28 offset:51200\n" \
"v_cndmask_b32_e32 v221, 0, v223, vcc\n" \
"v_mfma_f32_16x16x32_bf16 a[16:19], v[116:119], v[164:167], a[16:19]\n" \
"s_add_u32 m0, s31, 0x0\n" \
"s_mov_b32 s85, 0x100000\n" \
"v_mfma_f32_16x16x32_bf16 a[48:51], v[120:123], v[164:167], a[48:51]\n" \
"buffer_load_dwordx4 v26, s[68:71], s85 offen lds\n" \
"v_mfma_f32_16x16x32_bf16 a[20:23], v[116:119], v[168:171], a[20:23]\n" \
"ds_read_b64_tr_b16 v[88:89], v33 offset:32768\n" \
"v_lshlrev_b32_e32 v223, 7, v212\n" \
"v_mfma_f32_16x16x32_bf16 a[52:55], v[120:123], v[168:171], a[52:55]\n" \
"ds_read_b64_tr_b16 v[90:91], v33 offset:33792\n" \
"v_lshl_or_b32 v10, s7, 13, v223\n" \
"v_mfma_f32_16x16x32_bf16 a[24:27], v[116:119], v[172:175], a[24:27]\n" \
"s_add_u32 m0, s31, 0x4000\n" \
"s_mov_b32 s84, 0x100000\n" \
"v_mfma_f32_16x16x32_bf16 a[56:59], v[120:123], v[172:175], a[56:59]\n" \
"buffer_load_dwordx4 v26, s[72:75], s84 offen lds\n" \
"v_mfma_f32_16x16x32_bf16 a[28:31], v[116:119], v[176:179], a[28:31]\n" \
"ds_read_b64_tr_b16 v[92:93], v34 offset:32768\n" \
"v_mul_u32_u24_e32 v223, 0xe39, v212\n" \
"v_mfma_f32_16x16x32_bf16 a[60:63], v[120:123], v[176:179], a[60:63]\n" \
"ds_read_b64_tr_b16 v[94:95], v34 offset:33792\n" \
"v_lshl_add_u64 v[16:17], s[40:41], 0, v[10:11]\n" \
"v_mfma_f32_16x16x32_bf16 a[92:95], v[124:127], v[176:179], a[92:95]\n" \
"s_add_u32 m0, s30, 0x800\n" \
"s_mov_b32 s85, 0x4100\n" \
"v_mfma_f32_16x16x32_bf16 a[124:127], v[128:131], v[176:179], a[124:127]\n" \
"buffer_load_dwordx4 v22, s[64:67], s85 offen lds\n" \
"v_mfma_f32_16x16x32_bf16 a[88:91], v[124:127], v[172:175], a[88:91]\n" \
"ds_read_b64_tr_b16 v[96:97], v35 offset:32768\n" \
"v_lshrrev_b32_e64 v223, 16, v223\n" \
"v_mfma_f32_16x16x32_bf16 a[120:123], v[128:131], v[172:175], a[120:123]\n" \
"ds_read_b64_tr_b16 v[98:99], v35 offset:33792\n" \
"s_movk_i32 s40, 0xffee\n" \
"v_mfma_f32_16x16x32_bf16 a[84:87], v[124:127], v[168:171], a[84:87]\n" \
"s_add_u32 m0, s30, 0xc00\n" \
"s_mov_b32 s84, 0x6100\n" \
"v_mfma_f32_16x16x32_bf16 a[116:119], v[128:131], v[168:171], a[116:119]\n" \
"buffer_load_dwordx4 v23, s[64:67], s84 offen lds\n" \
"v_mfma_f32_16x16x32_bf16 a[80:83], v[124:127], v[164:167], a[80:83]\n" \
"ds_read_b64_tr_b16 v[100:101], v36 offset:32768\n" \
"s_lshl_b32 s7, s2, 5\n" \
"v_mfma_f32_16x16x32_bf16 a[112:115], v[128:131], v[164:167], a[112:115]\n" \
"ds_read_b64_tr_b16 v[102:103], v36 offset:33792\n" \
"v_mad_i32_i24 v224, v223, s40, v212\n" \
"v_mfma_f32_16x16x32_bf16 a[76:79], v[124:127], v[160:163], a[76:79]\n" \
"s_add_u32 m0, s31, 0x400\n" \
"s_mov_b32 s85, 0x108000\n" \
"v_mfma_f32_16x16x32_bf16 a[108:111], v[128:131], v[160:163], a[108:111]\n" \
"buffer_load_dwordx4 v26, s[68:71], s85 offen lds\n" \
"v_mfma_f32_16x16x32_bf16 a[72:75], v[124:127], v[156:159], a[72:75]\n" \
"ds_read_b64_tr_b16 v[104:105], v37 offset:32768\n" \
"s_add_i32 s7, s7, 32\n" \
"v_mfma_f32_16x16x32_bf16 a[104:107], v[128:131], v[156:159], a[104:107]\n" \
"v_lshlrev_b32_e32 v225, 4, v224\n" \
"v_add_u32_e64 v226, s7, v225\n" \
"v_mfma_f32_16x16x32_bf16 a[68:71], v[124:127], v[152:155], a[68:71]\n" \
"s_add_u32 m0, s31, 0x4400\n" \
"s_mov_b32 s84, 0x108000\n" \
"v_mfma_f32_16x16x32_bf16 a[100:103], v[128:131], v[152:155], a[100:103]\n" \
"buffer_load_dwordx4 v26, s[72:75], s84 offen lds\n" \
"v_mfma_f32_16x16x32_bf16 a[64:67], v[124:127], v[148:151], a[64:67]\n" \
"ds_read_b64_tr_b16 v[106:107], v37 offset:33792\n" \
"v_and_b32_e32 v226, 0xf0, v226\n" \
"v_mfma_f32_16x16x32_bf16 a[96:99], v[128:131], v[148:151], a[96:99]\n" \
"ds_read_b64_tr_b16 v[108:109], v38 offset:32768\n" \
"v_cmp_gt_u32_e64 vcc, 16, v224\n" \
"v_mfma_f32_16x16x32_bf16 a[128:131], v[132:135], v[148:151], a[128:131]\n" \
"s_add_u32 m0, s30, 0x1000\n" \
"s_mov_b32 s85, 0x8100\n" \
"v_mfma_f32_16x16x32_bf16 a[160:163], v[136:139], v[148:151], a[160:163]\n" \
"buffer_load_dwordx4 v22, s[64:67], s85 offen lds\n" \
"v_mfma_f32_16x16x32_bf16 a[132:135], v[132:135], v[152:155], a[132:135]\n" \
"ds_read_b64_tr_b16 v[110:111], v38 offset:33792\n" \
"s_movk_i32 s41, 0xf800\n" \
"v_mfma_f32_16x16x32_bf16 a[164:167], v[136:139], v[152:155], a[164:167]\n" \
"ds_read_b64_tr_b16 v[112:113], v39 offset:32768\n" \
"v_or_b32_e32 v12, v214, v215\n" \
"v_mfma_f32_16x16x32_bf16 a[136:139], v[132:135], v[156:159], a[136:139]\n" \
"s_add_u32 m0, s30, 0x1400\n" \
"s_mov_b32 s84, 0xa100\n" \
"v_mfma_f32_16x16x32_bf16 a[168:171], v[136:139], v[156:159], a[168:171]\n" \
"buffer_load_dwordx4 v23, s[64:67], s84 offen lds\n" \
"v_mfma_f32_16x16x32_bf16 a[140:143], v[132:135], v[160:163], a[140:143]\n" \
"ds_read_b64_tr_b16 v[114:115], v39 offset:33792\n" \
"v_cndmask_b32_e64 v224, v225, v226, vcc\n" \
"v_mfma_f32_16x16x32_bf16 a[172:175], v[136:139], v[160:163], a[172:175]\n" \
"ds_read_b128 v[60:63], v28 offset:53248\n" \
"v_or_b32_e64 v225, 64, v212\n" \
"v_mfma_f32_16x16x32_bf16 a[144:147], v[132:135], v[164:167], a[144:147]\n" \
"s_add_u32 m0, s31, 0x800\n" \
"s_mov_b32 s85, 0x110000\n" \
"v_mfma_f32_16x16x32_bf16 a[176:179], v[136:139], v[164:167], a[176:179]\n" \
"buffer_load_dwordx4 v27, s[68:71], s85 offen lds\n" \
"v_mfma_f32_16x16x32_bf16 a[148:151], v[132:135], v[168:171], a[148:151]\n" \
"ds_read_b128 v[64:67], v28 offset:55296\n" \
"v_mul_u32_u24_e32 v226, 0xe39, v225\n" \
"v_mfma_f32_16x16x32_bf16 a[180:183], v[136:139], v[168:171], a[180:183]\n" \
"ds_read_b128 v[68:71], v28 offset:32768\n" \
"v_lshrrev_b32_e64 v226, 16, v226\n" \
"v_mfma_f32_16x16x32_bf16 a[152:155], v[132:135], v[172:175], a[152:155]\n" \
"s_add_u32 m0, s31, 0x4800\n" \
"s_mov_b32 s84, 0x110000\n" \
"v_mfma_f32_16x16x32_bf16 a[184:187], v[136:139], v[172:175], a[184:187]\n" \
"buffer_load_dwordx4 v27, s[72:75], s84 offen lds\n" \
"v_mfma_f32_16x16x32_bf16 a[156:159], v[132:135], v[176:179], a[156:159]\n" \
"ds_read_b128 v[72:75], v28 offset:34816\n" \
"v_mad_i32_i24 v225, v226, s40, v225\n" \
"v_mfma_f32_16x16x32_bf16 a[188:191], v[136:139], v[176:179], a[188:191]\n" \
"ds_read_b128 v[76:79], v28 offset:36864\n" \
"v_lshlrev_b32_e64 v227, 4, v225\n" \
"v_mfma_f32_16x16x32_bf16 a[220:223], v[140:143], v[176:179], a[220:223]\n" \
"v_add_u32_e64 v228, s7, v227\n" \
"v_and_b32_e32 v228, 0xf0, v228\n" \
"v_mfma_f32_16x16x32_bf16 a[252:255], v[144:147], v[176:179], a[252:255]\n" \
"ds_read_b128 v[80:83], v28 offset:38912\n" \
"v_cmp_gt_u32_e64 vcc, 16, v225\n" \
"v_mfma_f32_16x16x32_bf16 a[216:219], v[140:143], v[172:175], a[216:219]\n" \
"v_or_b32_e32 v13, v216, v217\n" \
"v_or_b32_e32 v14, v218, v219\n" \
"v_mfma_f32_16x16x32_bf16 a[248:251], v[144:147], v[172:175], a[248:251]\n" \
"v_cndmask_b32_e64 v225, v227, v228, vcc\n" \
"v_or_b32_e32 v227, 0x80, v212\n" \
"v_mfma_f32_16x16x32_bf16 a[212:215], v[140:143], v[168:171], a[212:215]\n" \
"v_mul_u32_u24_e32 v228, 0xe39, v227\n" \
"v_lshrrev_b32_e64 v228, 16, v228\n" \
"v_mfma_f32_16x16x32_bf16 a[244:247], v[144:147], v[168:171], a[244:247]\n" \
"v_mad_i32_i24 v227, v228, s40, v227\n" \
"v_lshlrev_b32_e64 v229, 4, v227\n" \
"v_mfma_f32_16x16x32_bf16 a[208:211], v[140:143], v[164:167], a[208:211]\n" \
"v_add_u32_e64 v230, s7, v229\n" \
"v_and_b32_e32 v230, 0xf0, v230\n" \
"v_mfma_f32_16x16x32_bf16 a[240:243], v[144:147], v[164:167], a[240:243]\n" \
"v_cmp_gt_u32_e32 vcc, 16, v227\n" \
"v_or_b32_e32 v15, v220, v221\n" \
"v_mfma_f32_16x16x32_bf16 a[204:207], v[140:143], v[160:163], a[204:207]\n" \
"v_cndmask_b32_e64 v227, v229, v230, vcc\n" \
"v_or_b32_e32 v229, 0xc0, v212\n" \
"v_mfma_f32_16x16x32_bf16 a[236:239], v[144:147], v[160:163], a[236:239]\n" \
"v_mul_u32_u24_e32 v230, 0xe39, v229\n" \
"v_lshrrev_b32_e64 v230, 16, v230\n" \
"v_mfma_f32_16x16x32_bf16 a[200:203], v[140:143], v[156:159], a[200:203]\n" \
"v_mad_i32_i24 v229, v230, s40, v229\n" \
"v_lshlrev_b32_e64 v231, 4, v229\n" \
"v_mfma_f32_16x16x32_bf16 a[232:235], v[144:147], v[156:159], a[232:235]\n" \
"v_add_u32_e64 v232, s7, v231\n" \
"v_and_b32_e32 v232, 0xf0, v232\n" \
"v_mfma_f32_16x16x32_bf16 a[196:199], v[140:143], v[152:155], a[196:199]\n" \
"v_cmp_gt_u32_e32 vcc, 16, v229\n" \
"s_nop 1\n" \
"v_cndmask_b32_e64 v229, v231, v232, vcc\n" \
"v_mfma_f32_16x16x32_bf16 a[228:231], v[144:147], v[152:155], a[228:231]\n" \
"v_or_b32_e32 v231, 0x100, v212\n" \
"v_mul_u32_u24_e32 v232, 0xe39, v231\n" \
"v_mfma_f32_16x16x32_bf16 a[192:195], v[140:143], v[148:151], a[192:195]\n" \
"v_lshrrev_b32_e64 v232, 16, v232\n" \
"v_mad_i32_i24 v231, v232, s40, v231\n" \
"v_mfma_f32_16x16x32_bf16 a[224:227], v[144:147], v[148:151], a[224:227]\n" \
"s_waitcnt lgkmcnt(15)\n" \
"v_mfma_f32_16x16x32_bf16 a[0:3], v[52:55], v[84:87], a[0:3]\n" \
"s_add_u32 m0, s30, 0x1800\n" \
"s_mov_b32 s85, 0xc100\n" \
"v_mfma_f32_16x16x32_bf16 a[32:35], v[56:59], v[84:87], a[32:35]\n" \
"buffer_load_dwordx4 v22, s[64:67], s85 offen lds\n" \
"v_mfma_f32_16x16x32_bf16 a[4:7], v[52:55], v[88:91], a[4:7]\n" \
"ds_read_b64_tr_b16 v[148:149], v32 offset:40960\n" \
"v_lshlrev_b32_e32 v233, 4, v231\n" \
"v_mfma_f32_16x16x32_bf16 a[36:39], v[56:59], v[88:91], a[36:39]\n" \
"s_add_u32 m0, s30, 0x1c00\n" \
"s_mov_b32 s84, 0xe100\n" \
"v_mfma_f32_16x16x32_bf16 a[8:11], v[52:55], v[92:95], a[8:11]\n" \
"buffer_load_dwordx4 v23, s[64:67], s84 offen lds\n" \
"v_mfma_f32_16x16x32_bf16 a[40:43], v[56:59], v[92:95], a[40:43]\n" \
"ds_read_b64_tr_b16 v[150:151], v32 offset:41984\n" \
"v_add_u32_e32 v234, s7, v233\n" \
"s_waitcnt lgkmcnt(15)\n" \
"v_mfma_f32_16x16x32_bf16 a[12:15], v[52:55], v[96:99], a[12:15]\n" \
"s_add_u32 m0, s31, 0xc00\n" \
"s_mov_b32 s85, 0x118000\n" \
"v_mfma_f32_16x16x32_bf16 a[44:47], v[56:59], v[96:99], a[44:47]\n" \
"buffer_load_dwordx4 v27, s[68:71], s85 offen lds\n" \
"s_waitcnt lgkmcnt(14)\n" \
"v_mfma_f32_16x16x32_bf16 a[16:19], v[52:55], v[100:103], a[16:19]\n" \
"ds_read_b128 v[116:119], v29 offset:49152\n" \
"v_and_b32_e32 v234, 0xf0, v234\n" \
"v_mfma_f32_16x16x32_bf16 a[48:51], v[56:59], v[100:103], a[48:51]\n" \
"s_add_u32 m0, s31, 0x4c00\n" \
"s_mov_b32 s84, 0x118000\n" \
"s_waitcnt lgkmcnt(13)\n" \
"v_mfma_f32_16x16x32_bf16 a[20:23], v[52:55], v[104:107], a[20:23]\n" \
"buffer_load_dwordx4 v27, s[72:75], s84 offen lds\n" \
"v_mfma_f32_16x16x32_bf16 a[52:55], v[56:59], v[104:107], a[52:55]\n" \
"ds_read_b128 v[120:123], v29 offset:51200\n" \
"v_cmp_gt_u32_e32 vcc, 16, v231\n" \
"s_waitcnt lgkmcnt(12)\n" \
"v_mfma_f32_16x16x32_bf16 a[24:27], v[52:55], v[108:111], a[24:27]\n" \
"ds_read_b64_tr_b16 v[152:153], v33 offset:40960\n" \
"v_cndmask_b32_e64 v231, v233, v234, vcc\n" \
"v_mfma_f32_16x16x32_bf16 a[56:59], v[56:59], v[108:111], a[56:59]\n" \
"ds_read_b64_tr_b16 v[154:155], v33 offset:41984\n" \
"v_or_b32_e32 v233, 0x140, v212\n" \
"s_waitcnt lgkmcnt(12)\n" \
"v_mfma_f32_16x16x32_bf16 a[28:31], v[52:55], v[112:115], a[28:31]\n" \
"ds_read_b64_tr_b16 v[156:157], v34 offset:40960\n" \
"v_mul_u32_u24_e32 v234, 0xe39, v233\n" \
"v_mfma_f32_16x16x32_bf16 a[60:63], v[56:59], v[112:115], a[60:63]\n" \
"ds_read_b64_tr_b16 v[158:159], v34 offset:41984\n" \
"v_lshrrev_b32_e32 v234, 16, v234\n" \
"s_waitcnt lgkmcnt(13)\n" \
"v_mfma_f32_16x16x32_bf16 a[92:95], v[60:63], v[112:115], a[92:95]\n" \
"s_mov_b32 m0, s32\n" \
"v_mad_i32_i24 v233, v234, s40, v233\n" \
"s_waitcnt lgkmcnt(12)\n" \
"v_mfma_f32_16x16x32_bf16 a[124:127], v[64:67], v[112:115], a[124:127]\n" \
"s_mov_b32 exec_lo, 0x0\n" \
"s_mov_b32 exec_hi, 0xff\n" \
"global_load_lds_dword v[184:185], off\n" \
"s_mov_b64 exec, -1\n" \
"v_mfma_f32_16x16x32_bf16 a[88:91], v[60:63], v[108:111], a[88:91]\n" \
"ds_read_b64_tr_b16 v[160:161], v35 offset:40960\n" \
"v_lshlrev_b32_e32 v235, 4, v233\n" \
"v_mfma_f32_16x16x32_bf16 a[120:123], v[64:67], v[108:111], a[120:123]\n" \
"ds_read_b64_tr_b16 v[162:163], v35 offset:41984\n" \
"v_add_u32_e64 v236, s7, v235\n" \
"v_mfma_f32_16x16x32_bf16 a[84:87], v[60:63], v[104:107], a[84:87]\n" \
"v_and_b32_e32 v236, 0xf0, v236\n" \
"v_cmp_gt_u32_e64 vcc, 16, v233\n" \
"v_mfma_f32_16x16x32_bf16 a[116:119], v[64:67], v[104:107], a[116:119]\n" \
"s_mov_b32 m0, s32\n" \
"ds_read_b64_tr_b16 v[164:165], v36 offset:40960\n" \
"v_mfma_f32_16x16x32_bf16 a[80:83], v[60:63], v[100:103], a[80:83]\n" \
"s_mov_b32 exec_lo, 0xffff0000\n" \
"s_mov_b32 exec_hi, 0x0\n" \
"global_load_lds_dword v[186:187], off\n" \
"s_mov_b64 exec, -1\n" \
"v_mfma_f32_16x16x32_bf16 a[112:115], v[64:67], v[100:103], a[112:115]\n" \
"ds_read_b64_tr_b16 v[166:167], v36 offset:41984\n" \
"v_cndmask_b32_e32 v233, v235, v236, vcc\n" \
"v_mfma_f32_16x16x32_bf16 a[76:79], v[60:63], v[96:99], a[76:79]\n" \
"v_or_b32_e32 v235, 0x180, v212\n" \
"v_mul_u32_u24_e32 v236, 0xe39, v235\n" \
"v_mfma_f32_16x16x32_bf16 a[108:111], v[64:67], v[96:99], a[108:111]\n" \
"v_lshrrev_b32_e64 v236, 16, v236\n" \
"v_mad_i32_i24 v235, v236, s40, v235\n" \
"v_mfma_f32_16x16x32_bf16 a[72:75], v[60:63], v[92:95], a[72:75]\n" \
"ds_read_b64_tr_b16 v[168:169], v37 offset:40960\n" \
"v_lshlrev_b32_e64 v237, 4, v235\n" \
"v_mfma_f32_16x16x32_bf16 a[104:107], v[64:67], v[92:95], a[104:107]\n" \
"v_add_u32_e64 v238, s7, v237\n" \
"v_and_b32_e32 v238, 0xf0, v238\n" \
"v_mfma_f32_16x16x32_bf16 a[68:71], v[60:63], v[88:91], a[68:71]\n" \
"ds_read_b64_tr_b16 v[170:171], v37 offset:41984\n" \
"v_cmp_gt_u32_e64 vcc, 16, v235\n" \
"v_mfma_f32_16x16x32_bf16 a[100:103], v[64:67], v[88:91], a[100:103]\n" \
"s_nop 0\n" \
"v_cndmask_b32_e32 v235, v237, v238, vcc\n" \
"v_or_b32_e32 v237, 0x1c0, v212\n" \
"v_mfma_f32_16x16x32_bf16 a[64:67], v[60:63], v[84:87], a[64:67]\n" \
"ds_read_b64_tr_b16 v[172:173], v38 offset:40960\n" \
"v_mul_u32_u24_e32 v238, 0xe39, v237\n" \
"v_mfma_f32_16x16x32_bf16 a[96:99], v[64:67], v[84:87], a[96:99]\n" \
"v_lshrrev_b32_e64 v238, 16, v238\n" \
"v_mad_i32_i24 v237, v238, s40, v237\n" \
"s_waitcnt lgkmcnt(15)\n" \
"v_mfma_f32_16x16x32_bf16 a[128:131], v[68:71], v[84:87], a[128:131]\n" \
"ds_read_b64_tr_b16 v[174:175], v38 offset:41984\n" \
"v_lshlrev_b32_e32 v239, 4, v237\n" \
"v_mfma_f32_16x16x32_bf16 a[160:163], v[72:75], v[84:87], a[160:163]\n" \
"v_add_u32_e64 v240, s7, v239\n" \
"v_and_b32_e32 v240, 0xf0, v240\n" \
"v_mfma_f32_16x16x32_bf16 a[132:135], v[68:71], v[88:91], a[132:135]\n" \
"ds_read_b64_tr_b16 v[176:177], v39 offset:40960\n" \
"v_cmp_gt_u32_e64 vcc, 16, v237\n" \
"v_mfma_f32_16x16x32_bf16 a[164:167], v[72:75], v[88:91], a[164:167]\n" \
"s_nop 0\n" \
"v_cndmask_b32_e32 v237, v239, v240, vcc\n" \
"v_or_b32_e32 v239, 0x200, v212\n" \
"v_mfma_f32_16x16x32_bf16 a[136:139], v[68:71], v[92:95], a[136:139]\n" \
"ds_read_b64_tr_b16 v[178:179], v39 offset:41984\n" \
"v_mul_u32_u24_e32 v240, 0xe39, v239\n" \
"v_mfma_f32_16x16x32_bf16 a[168:171], v[72:75], v[92:95], a[168:171]\n" \
"v_lshrrev_b32_e64 v240, 16, v240\n" \
"v_mad_i32_i24 v239, v240, s40, v239\n" \
"v_mfma_f32_16x16x32_bf16 a[140:143], v[68:71], v[96:99], a[140:143]\n" \
"v_lshlrev_b32_e32 v241, 4, v239\n" \
"v_add_u32_e32 v242, s7, v241\n" \
"v_mfma_f32_16x16x32_bf16 a[172:175], v[72:75], v[96:99], a[172:175]\n" \
"ds_read_b128 v[124:127], v29 offset:53248\n" \
"s_movk_i32 s7, 0x480\n" \
"v_mfma_f32_16x16x32_bf16 a[144:147], v[68:71], v[100:103], a[144:147]\n" \
"v_mad_u32_u24 v0, v223, s7, v224\n" \
"v_mul_u32_u24_e32 v223, 0x480, v226\n" \
"v_mfma_f32_16x16x32_bf16 a[176:179], v[72:75], v[100:103], a[176:179]\n" \
"ds_read_b128 v[128:131], v29 offset:55296\n" \
"s_movk_i32 s40, 0xfc00\n" \
"v_mfma_f32_16x16x32_bf16 a[148:151], v[68:71], v[104:107], a[148:151]\n" \
"v_add3_u32 v1, v223, v225, s40\n" \
"v_mul_u32_u24_e32 v223, 0x480, v228\n" \
"v_mfma_f32_16x16x32_bf16 a[180:183], v[72:75], v[104:107], a[180:183]\n" \
"ds_read_b128 v[132:135], v29 offset:32768\n" \
"v_add3_u32 v2, v223, v227, s41\n" \
"v_mfma_f32_16x16x32_bf16 a[152:155], v[68:71], v[108:111], a[152:155]\n" \
"v_mul_u32_u24_e32 v223, 0x480, v230\n" \
"v_add3_u32 v3, v223, v229, s42\n" \
"v_mfma_f32_16x16x32_bf16 a[184:187], v[72:75], v[108:111], a[184:187]\n" \
"ds_read_b128 v[136:139], v29 offset:34816\n" \
"v_mul_u32_u24_e32 v223, 0x480, v234\n" \
"v_mfma_f32_16x16x32_bf16 a[156:159], v[68:71], v[112:115], a[156:159]\n" \
"v_add3_u32 v5, v223, v233, s40\n" \
"v_mul_u32_u24_e32 v223, 0x480, v236\n" \
"v_mfma_f32_16x16x32_bf16 a[188:191], v[72:75], v[112:115], a[188:191]\n" \
"ds_read_b128 v[140:143], v29 offset:36864\n" \
"v_add3_u32 v6, v223, v235, s41\n" \
"v_mfma_f32_16x16x32_bf16 a[220:223], v[76:79], v[112:115], a[220:223]\n" \
"v_mul_u32_u24_e32 v223, 0x480, v238\n" \
"s_lshl_b32 s40, s3, 3\n" \
"v_mfma_f32_16x16x32_bf16 a[252:255], v[80:83], v[112:115], a[252:255]\n" \
"ds_read_b128 v[144:147], v29 offset:38912\n" \
"v_add3_u32 v7, v223, v237, s42\n" \
"v_mfma_f32_16x16x32_bf16 a[216:219], v[76:79], v[108:111], a[216:219]\n" \
"s_and_b32 s40, s40, 16\n" \
"s_and_b32 s42, s44, 2\n" \
"v_mfma_f32_16x16x32_bf16 a[248:251], v[80:83], v[108:111], a[248:251]\n" \
"s_and_b32 s41, s2, 7\n" \
"v_bfe_u32 v223, v20, 7, 1\n" \
"v_mfma_f32_16x16x32_bf16 a[212:215], v[76:79], v[104:107], a[212:215]\n" \
"s_or_b32 s40, s42, s40\n" \
"v_and_b32_e32 v242, 0xf0, v242\n" \
"v_mfma_f32_16x16x32_bf16 a[244:247], v[80:83], v[104:107], a[244:247]\n" \
"v_cmp_gt_u32_e32 vcc, 16, v239\n" \
"v_or3_b32 v222, s40, v223, v222\n" \
"v_mfma_f32_16x16x32_bf16 a[208:211], v[76:79], v[100:103], a[208:211]\n" \
"v_and_b32_e32 v223, 32, v20\n" \
"s_lshl_b32 s40, s41, 5\n" \
"v_mfma_f32_16x16x32_bf16 a[240:243], v[80:83], v[100:103], a[240:243]\n" \
"v_cndmask_b32_e32 v239, v241, v242, vcc\n" \
"v_mul_u32_u24_e32 v222, 18, v222\n" \
"v_mfma_f32_16x16x32_bf16 a[204:207], v[76:79], v[96:99], a[204:207]\n" \
"v_mov_b32_e32 v224, 0x100\n" \
"v_mov_b32_e64 v225, s40\n" \
"v_mfma_f32_16x16x32_bf16 a[236:239], v[80:83], v[96:99], a[236:239]\n" \
"v_cmp_eq_u32_e64 vcc, 0, v223\n" \
"v_and_or_b32 v222, v213, 1, v222\n" \
"v_mfma_f32_16x16x32_bf16 a[200:203], v[76:79], v[92:95], a[200:203]\n" \
"v_mad_u32_u24 v4, v232, s7, v231\n" \
"v_cndmask_b32_e64 v223, v224, v225, vcc\n" \
"v_mfma_f32_16x16x32_bf16 a[232:235], v[80:83], v[92:95], a[232:235]\n" \
"v_mad_u32_u24 v8, v240, s7, v239\n" \
"v_mad_u32_u24 v222, v222, s7, v223\n" \
"v_mfma_f32_16x16x32_bf16 a[196:199], v[76:79], v[88:91], a[196:199]\n" \
"s_lshl_b32 s7, s41, 2\n" \
"s_mul_i32 s42, s5, 0xa00\n" \
"v_mfma_f32_16x16x32_bf16 a[228:231], v[80:83], v[88:91], a[228:231]\n" \
"s_or_b32 s45, s7, s43\n" \
"s_add_i32 s42, s42, 0x5bc1000\n" \
"v_mfma_f32_16x16x32_bf16 a[192:195], v[76:79], v[84:87], a[192:195]\n" \
"s_lshl_b32 s46, s45, 2\n" \
"s_or_b32 s46, s46, s42\n" \
"v_mfma_f32_16x16x32_bf16 a[224:227], v[80:83], v[84:87], a[224:227]\n" \
"s_waitcnt vmcnt(20)\n" \
"v_readfirstlane_b32 s62, v180\n" \
"s_nop 3\n" \
"s_add_u32 s62, s62, 0x9E3779B1\n" \
"s_waitcnt vmcnt(2) lgkmcnt(0)\n" \
"s_barrier\n" \
"v_mfma_f32_16x16x32_bf16 a[0:3], v[116:119], v[148:151], a[0:3]\n" \
"s_add_u32 m0, s30, 0x8000\n" \
"s_mov_b32 s85, 0x180\n" \
"v_mfma_f32_16x16x32_bf16 a[32:35], v[120:123], v[148:151], a[32:35]\n" \
"buffer_load_dwordx4 v22, s[64:67], s85 offen lds\n" \
"v_mfma_f32_16x16x32_bf16 a[4:7], v[116:119], v[152:155], a[4:7]\n" \
"ds_read_b64_tr_b16 v[84:85], v32 offset:0\n" \
"v_writelane_b32 v11, s46, 0\n" \
"v_mfma_f32_16x16x32_bf16 a[36:39], v[120:123], v[152:155], a[36:39]\n" \
"ds_read_b64_tr_b16 v[86:87], v32 offset:1024\n" \
"s_lshl_b32 s46, s5, 3\n" \
"v_mfma_f32_16x16x32_bf16 a[8:11], v[116:119], v[156:159], a[8:11]\n" \
"s_add_u32 m0, s30, 0x8400\n" \
"s_mov_b32 s84, 0x2180\n" \
"v_mfma_f32_16x16x32_bf16 a[40:43], v[120:123], v[156:159], a[40:43]\n" \
"buffer_load_dwordx4 v23, s[64:67], s84 offen lds\n" \
"v_mfma_f32_16x16x32_bf16 a[12:15], v[116:119], v[160:163], a[12:15]\n" \
"ds_read_b128 v[52:55], v28 offset:16384\n" \
"v_writelane_b32 v11, s42, 1\n" \
"v_mfma_f32_16x16x32_bf16 a[44:47], v[120:123], v[160:163], a[44:47]\n" \
"ds_read_b128 v[56:59], v28 offset:18432\n" \
"s_or_b32 s42, s46, s41\n" \
"v_mfma_f32_16x16x32_bf16 a[16:19], v[116:119], v[164:167], a[16:19]\n" \
"s_add_u32 m0, s31, 0x8000\n" \
"s_mov_b32 s85, 0x180000\n" \
"v_mfma_f32_16x16x32_bf16 a[48:51], v[120:123], v[164:167], a[48:51]\n" \
"buffer_load_dwordx4 v26, s[68:71], s85 offen lds\n" \
"v_mfma_f32_16x16x32_bf16 a[20:23], v[116:119], v[168:171], a[20:23]\n" \
"ds_read_b64_tr_b16 v[88:89], v33 offset:0\n" \
"s_lshl_b32 s42, s42, 10\n" \
"v_mfma_f32_16x16x32_bf16 a[52:55], v[120:123], v[168:171], a[52:55]\n" \
"ds_read_b64_tr_b16 v[90:91], v33 offset:1024\n" \
"s_or_b32 s42, s42, s47\n" \
"s_nop 0\n" \
"v_mfma_f32_16x16x32_bf16 a[24:27], v[116:119], v[172:175], a[24:27]\n" \
"s_add_u32 m0, s31, 0xc000\n" \
"s_mov_b32 s84, 0x180000\n" \
"v_mfma_f32_16x16x32_bf16 a[56:59], v[120:123], v[172:175], a[56:59]\n" \
"buffer_load_dwordx4 v26, s[72:75], s84 offen lds\n" \
"v_mfma_f32_16x16x32_bf16 a[28:31], v[116:119], v[176:179], a[28:31]\n" \
"ds_read_b64_tr_b16 v[92:93], v34 offset:0\n" \
"s_or_b32 s47, s42, 0x5c00200\n" \
"v_mfma_f32_16x16x32_bf16 a[60:63], v[120:123], v[176:179], a[60:63]\n" \
"ds_read_b64_tr_b16 v[94:95], v34 offset:1024\n" \
"v_writelane_b32 v11, s47, 2\n" \
"v_mfma_f32_16x16x32_bf16 a[92:95], v[124:127], v[176:179], a[92:95]\n" \
"s_add_u32 m0, s30, 0x8800\n" \
"s_mov_b32 s85, 0x4180\n" \
"v_mfma_f32_16x16x32_bf16 a[124:127], v[128:131], v[176:179], a[124:127]\n" \
"buffer_load_dwordx4 v22, s[64:67], s85 offen lds\n" \
"v_mfma_f32_16x16x32_bf16 a[88:91], v[124:127], v[172:175], a[88:91]\n" \
"ds_read_b64_tr_b16 v[96:97], v35 offset:0\n" \
"s_lshl_b32 s47, s43, 2\n" \
"v_mfma_f32_16x16x32_bf16 a[120:123], v[128:131], v[172:175], a[120:123]\n" \
"ds_read_b64_tr_b16 v[98:99], v35 offset:1024\n" \
"s_or_b32 s48, s47, s42\n" \
"v_mfma_f32_16x16x32_bf16 a[84:87], v[124:127], v[168:171], a[84:87]\n" \
"s_add_u32 m0, s30, 0x8c00\n" \
"s_mov_b32 s84, 0x6180\n" \
"v_mfma_f32_16x16x32_bf16 a[116:119], v[128:131], v[168:171], a[116:119]\n" \
"buffer_load_dwordx4 v23, s[64:67], s84 offen lds\n" \
"v_mfma_f32_16x16x32_bf16 a[80:83], v[124:127], v[164:167], a[80:83]\n" \
"ds_read_b64_tr_b16 v[100:101], v36 offset:0\n" \
"s_or_b32 s48, s48, 0x5c00000\n" \
"v_mfma_f32_16x16x32_bf16 a[112:115], v[128:131], v[164:167], a[112:115]\n" \
"ds_read_b64_tr_b16 v[102:103], v36 offset:1024\n" \
"v_writelane_b32 v11, s48, 3\n" \
"v_mfma_f32_16x16x32_bf16 a[76:79], v[124:127], v[160:163], a[76:79]\n" \
"s_add_u32 m0, s31, 0x8400\n" \
"s_mov_b32 s85, 0x188000\n" \
"v_mfma_f32_16x16x32_bf16 a[108:111], v[128:131], v[160:163], a[108:111]\n" \
"buffer_load_dwordx4 v26, s[68:71], s85 offen lds\n" \
"v_mfma_f32_16x16x32_bf16 a[72:75], v[124:127], v[156:159], a[72:75]\n" \
"ds_read_b64_tr_b16 v[104:105], v37 offset:0\n" \
"s_or_b32 s42, s42, 0x5c00080\n" \
"v_mfma_f32_16x16x32_bf16 a[104:107], v[128:131], v[156:159], a[104:107]\n" \
"v_writelane_b32 v11, s42, 4\n" \
"s_or_b32 s42, s42, s47\n" \
"v_mfma_f32_16x16x32_bf16 a[68:71], v[124:127], v[152:155], a[68:71]\n" \
"s_add_u32 m0, s31, 0xc400\n" \
"s_mov_b32 s84, 0x188000\n" \
"v_mfma_f32_16x16x32_bf16 a[100:103], v[128:131], v[152:155], a[100:103]\n" \
"buffer_load_dwordx4 v26, s[72:75], s84 offen lds\n" \
"v_mfma_f32_16x16x32_bf16 a[64:67], v[124:127], v[148:151], a[64:67]\n" \
"ds_read_b64_tr_b16 v[106:107], v37 offset:1024\n" \
"s_add_i32 s47, s3, s2\n" \
"v_mfma_f32_16x16x32_bf16 a[96:99], v[128:131], v[148:151], a[96:99]\n" \
"ds_read_b64_tr_b16 v[108:109], v38 offset:0\n" \
"v_writelane_b32 v11, s42, 5\n" \
"v_mfma_f32_16x16x32_bf16 a[128:131], v[132:135], v[148:151], a[128:131]\n" \
"s_add_u32 m0, s30, 0x9000\n" \
"s_mov_b32 s85, 0x8180\n" \
"v_mfma_f32_16x16x32_bf16 a[160:163], v[136:139], v[148:151], a[160:163]\n" \
"buffer_load_dwordx4 v22, s[64:67], s85 offen lds\n" \
"v_mfma_f32_16x16x32_bf16 a[132:135], v[132:135], v[152:155], a[132:135]\n" \
"ds_read_b64_tr_b16 v[110:111], v38 offset:1024\n" \
"s_mov_b32 s42, 2.0\n" \
"s_nop 0\n" \
"v_mfma_f32_16x16x32_bf16 a[164:167], v[136:139], v[152:155], a[164:167]\n" \
"ds_read_b64_tr_b16 v[112:113], v39 offset:0\n" \
"s_mov_b32 s48, 0x5bc0000\n" \
"v_mfma_f32_16x16x32_bf16 a[136:139], v[132:135], v[156:159], a[136:139]\n" \
"s_add_u32 m0, s30, 0x9400\n" \
"s_mov_b32 s84, 0xa180\n" \
"v_mfma_f32_16x16x32_bf16 a[168:171], v[136:139], v[156:159], a[168:171]\n" \
"buffer_load_dwordx4 v23, s[64:67], s84 offen lds\n" \
"v_mfma_f32_16x16x32_bf16 a[140:143], v[132:135], v[160:163], a[140:143]\n" \
"ds_read_b64_tr_b16 v[114:115], v39 offset:1024\n" \
"v_mfma_f32_16x16x32_bf16 a[172:175], v[136:139], v[160:163], a[172:175]\n" \
"ds_read_b128 v[60:63], v28 offset:20480\n" \
"v_mfma_f32_16x16x32_bf16 a[144:147], v[132:135], v[164:167], a[144:147]\n" \
"s_add_u32 m0, s31, 0x8800\n" \
"s_mov_b32 s85, 0x190000\n" \
"v_mfma_f32_16x16x32_bf16 a[176:179], v[136:139], v[164:167], a[176:179]\n" \
"buffer_load_dwordx4 v27, s[68:71], s85 offen lds\n" \
"v_mfma_f32_16x16x32_bf16 a[148:151], v[132:135], v[168:171], a[148:151]\n" \
"ds_read_b128 v[64:67], v28 offset:22528\n" \
"v_mfma_f32_16x16x32_bf16 a[180:183], v[136:139], v[168:171], a[180:183]\n" \
"ds_read_b128 v[68:71], v28 offset:0\n" \
"v_mfma_f32_16x16x32_bf16 a[152:155], v[132:135], v[172:175], a[152:155]\n" \
"s_add_u32 m0, s31, 0xc800\n" \
"s_mov_b32 s84, 0x190000\n" \
"v_mfma_f32_16x16x32_bf16 a[184:187], v[136:139], v[172:175], a[184:187]\n" \
"buffer_load_dwordx4 v27, s[72:75], s84 offen lds\n" \
"v_mfma_f32_16x16x32_bf16 a[156:159], v[132:135], v[176:179], a[156:159]\n" \
"ds_read_b128 v[72:75], v28 offset:2048\n" \
"v_mfma_f32_16x16x32_bf16 a[188:191], v[136:139], v[176:179], a[188:191]\n" \
"ds_read_b128 v[76:79], v28 offset:4096\n" \
"v_mfma_f32_16x16x32_bf16 a[220:223], v[140:143], v[176:179], a[220:223]\n" \
"s_cmp_lg_u32 s47, 0\n" \
"s_cselect_b32 s47, s42, s48\n" \
"v_mfma_f32_16x16x32_bf16 a[252:255], v[144:147], v[176:179], a[252:255]\n" \
"ds_read_b128 v[80:83], v28 offset:6144\n" \
"s_and_b32 s44, s44, 4\n" \
"v_mfma_f32_16x16x32_bf16 a[216:219], v[140:143], v[172:175], a[216:219]\n" \
"v_writelane_b32 v11, s47, 52\n" \
"s_or_b32 s47, s44, s4\n" \
"v_mfma_f32_16x16x32_bf16 a[248:251], v[144:147], v[172:175], a[248:251]\n" \
"s_lshr_b32 s48, s2, 3\n" \
"v_mfma_f32_16x16x32_bf16 a[212:215], v[140:143], v[168:171], a[212:215]\n" \
"s_add_u32 s44, s22, s50\n" \
"s_addc_u32 s51, s23, 0\n" \
"v_mfma_f32_16x16x32_bf16 a[244:247], v[144:147], v[168:171], a[244:247]\n" \
"v_writelane_b32 v11, s44, 9\n" \
"v_mfma_f32_16x16x32_bf16 a[208:211], v[140:143], v[164:167], a[208:211]\n" \
"s_add_u32 s52, s24, s50\n" \
"s_addc_u32 s53, s25, 0\n" \
"v_mfma_f32_16x16x32_bf16 a[240:243], v[144:147], v[164:167], a[240:243]\n" \
"s_and_b32 s54, s51, 0xffff\n" \
"v_writelane_b32 v11, s54, 10\n" \
"v_mfma_f32_16x16x32_bf16 a[204:207], v[140:143], v[160:163], a[204:207]\n" \
"s_and_b32 s54, s53, 0xffff\n" \
"v_writelane_b32 v11, s52, 11\n" \
"v_mfma_f32_16x16x32_bf16 a[236:239], v[144:147], v[160:163], a[236:239]\n" \
"s_cmpk_lt_u32 s3, 3\n" \
"s_cselect_b32 s44, s44, s52\n" \
"v_mfma_f32_16x16x32_bf16 a[200:203], v[140:143], v[156:159], a[200:203]\n" \
"s_movk_i32 s52, 0xfff\n" \
"v_writelane_b32 v11, s54, 12\n" \
"v_mfma_f32_16x16x32_bf16 a[232:235], v[144:147], v[156:159], a[232:235]\n" \
"s_and_b32 s54, s29, 0xffff\n" \
"v_writelane_b32 v11, s28, 13\n" \
"v_mfma_f32_16x16x32_bf16 a[196:199], v[140:143], v[152:155], a[196:199]\n" \
"v_mul_u32_u24_e32 v213, 0x120, v213\n" \
"v_writelane_b32 v11, s54, 14\n" \
"v_mfma_f32_16x16x32_bf16 a[228:231], v[144:147], v[152:155], a[228:231]\n" \
"v_lshlrev_b32_e64 v212, 3, v212\n" \
"v_writelane_b32 v11, s44, 15\n" \
"v_mfma_f32_16x16x32_bf16 a[192:195], v[140:143], v[148:151], a[192:195]\n" \
"s_cmpk_lt_u32 s3, 3\n" \
"s_cselect_b32 s44, s51, s53\n" \
"v_mfma_f32_16x16x32_bf16 a[224:227], v[144:147], v[148:151], a[224:227]\n" \
"s_waitcnt lgkmcnt(15)\n" \
"s_nop 0\n" \
"v_mfma_f32_16x16x32_bf16 a[0:3], v[52:55], v[84:87], a[0:3]\n" \
"s_add_u32 m0, s30, 0x9800\n" \
"s_mov_b32 s85, 0xc180\n" \
"v_mfma_f32_16x16x32_bf16 a[32:35], v[56:59], v[84:87], a[32:35]\n" \
"buffer_load_dwordx4 v22, s[64:67], s85 offen lds\n" \
"v_mfma_f32_16x16x32_bf16 a[4:7], v[52:55], v[88:91], a[4:7]\n" \
"ds_read_b64_tr_b16 v[148:149], v32 offset:8192\n" \
"s_and_b32 s44, s44, 0xffff\n" \
"v_mfma_f32_16x16x32_bf16 a[36:39], v[56:59], v[88:91], a[36:39]\n" \
"s_add_u32 m0, s30, 0x9c00\n" \
"s_mov_b32 s84, 0xe180\n" \
"v_mfma_f32_16x16x32_bf16 a[8:11], v[52:55], v[92:95], a[8:11]\n" \
"buffer_load_dwordx4 v23, s[64:67], s84 offen lds\n" \
"v_mfma_f32_16x16x32_bf16 a[40:43], v[56:59], v[92:95], a[40:43]\n" \
"ds_read_b64_tr_b16 v[150:151], v32 offset:9216\n" \
"v_writelane_b32 v11, s44, 16\n" \
"s_waitcnt lgkmcnt(15)\n" \
"v_mfma_f32_16x16x32_bf16 a[12:15], v[52:55], v[96:99], a[12:15]\n" \
"s_add_u32 m0, s31, 0x8c00\n" \
"s_mov_b32 s85, 0x198000\n" \
"v_mfma_f32_16x16x32_bf16 a[44:47], v[56:59], v[96:99], a[44:47]\n" \
"buffer_load_dwordx4 v27, s[68:71], s85 offen lds\n" \
"s_waitcnt lgkmcnt(14)\n" \
"v_mfma_f32_16x16x32_bf16 a[16:19], v[52:55], v[100:103], a[16:19]\n" \
"ds_read_b128 v[116:119], v29 offset:16384\n" \
"s_mul_i32 s44, s41, 0x48\n" \
"v_mfma_f32_16x16x32_bf16 a[48:51], v[56:59], v[100:103], a[48:51]\n" \
"s_add_u32 m0, s31, 0xcc00\n" \
"s_mov_b32 s84, 0x198000\n" \
"s_waitcnt lgkmcnt(13)\n" \
"v_mfma_f32_16x16x32_bf16 a[20:23], v[52:55], v[104:107], a[20:23]\n" \
"buffer_load_dwordx4 v27, s[72:75], s84 offen lds\n" \
"v_mfma_f32_16x16x32_bf16 a[52:55], v[56:59], v[104:107], a[52:55]\n" \
"ds_read_b128 v[120:123], v29 offset:18432\n" \
"s_add_i32 s51, s44, 0x47\n" \
"s_waitcnt lgkmcnt(12)\n" \
"v_mfma_f32_16x16x32_bf16 a[24:27], v[52:55], v[108:111], a[24:27]\n" \
"ds_read_b64_tr_b16 v[152:153], v33 offset:8192\n" \
"v_mfma_f32_16x16x32_bf16 a[56:59], v[56:59], v[108:111], a[56:59]\n" \
"ds_read_b64_tr_b16 v[154:155], v33 offset:9216\n" \
"s_waitcnt lgkmcnt(12)\n" \
"v_mfma_f32_16x16x32_bf16 a[28:31], v[52:55], v[112:115], a[28:31]\n" \
"ds_read_b64_tr_b16 v[156:157], v34 offset:8192\n" \
"v_mfma_f32_16x16x32_bf16 a[60:63], v[56:59], v[112:115], a[60:63]\n" \
"ds_read_b64_tr_b16 v[158:159], v34 offset:9216\n" \
"s_waitcnt lgkmcnt(13)\n" \
"v_mfma_f32_16x16x32_bf16 a[92:95], v[60:63], v[112:115], a[92:95]\n" \
"s_cmpk_lt_u32 s3, 3\n" \
"s_cselect_b32 s51, s51, s52\n" \
"s_waitcnt lgkmcnt(12)\n" \
"v_mfma_f32_16x16x32_bf16 a[124:127], v[64:67], v[112:115], a[124:127]\n" \
"buffer_load_dwordx4 v[200:203], v192, s[76:79], s34 offen offset:0\n" \
"v_mfma_f32_16x16x32_bf16 a[88:91], v[60:63], v[108:111], a[88:91]\n" \
"s_mov_b32 m0, s32\n" \
"ds_read_b64_tr_b16 v[160:161], v35 offset:8192\n" \
"v_mfma_f32_16x16x32_bf16 a[120:123], v[64:67], v[108:111], a[120:123]\n" \
"s_mov_b32 exec_lo, 0x0\n" \
"s_mov_b32 exec_hi, 0xff00\n" \
"global_load_lds_dword v[184:185], off\n" \
"s_mov_b64 exec, -1\n" \
"v_mfma_f32_16x16x32_bf16 a[84:87], v[60:63], v[104:107], a[84:87]\n" \
"ds_read_b64_tr_b16 v[162:163], v35 offset:9216\n" \
"s_mulk_i32 s51, 0x480\n" \
"v_mfma_f32_16x16x32_bf16 a[116:119], v[64:67], v[104:107], a[116:119]\n" \
"buffer_load_dwordx4 v[204:207], v193, s[76:79], s34 offen offset:0\n" \
"v_mfma_f32_16x16x32_bf16 a[80:83], v[60:63], v[100:103], a[80:83]\n" \
"ds_read_b64_tr_b16 v[164:165], v36 offset:8192\n" \
"s_addk_i32 s51, 0x120\n" \
"v_mfma_f32_16x16x32_bf16 a[112:115], v[64:67], v[100:103], a[112:115]\n" \
"ds_read_b64_tr_b16 v[166:167], v36 offset:9216\n" \
"v_writelane_b32 v11, s51, 17\n" \
"v_mfma_f32_16x16x32_bf16 a[76:79], v[60:63], v[96:99], a[76:79]\n" \
"s_lshl_b32 s51, s3, 5\n" \
"s_add_i32 s44, s44, s51\n" \
"v_mfma_f32_16x16x32_bf16 a[108:111], v[64:67], v[96:99], a[108:111]\n" \
"s_mul_i32 s51, s44, 0x480\n" \
"s_mul_i32 s44, s45, 0x24000\n" \
"v_mfma_f32_16x16x32_bf16 a[72:75], v[60:63], v[92:95], a[72:75]\n" \
"buffer_load_dword v208, v194, s[76:79], s34 offen offset:32\n" \
"v_mfma_f32_16x16x32_bf16 a[104:107], v[64:67], v[92:95], a[104:107]\n" \
"ds_read_b64_tr_b16 v[168:169], v37 offset:8192\n" \
"s_add_i32 s52, s44, 0x1b000\n" \
"v_mfma_f32_16x16x32_bf16 a[68:71], v[60:63], v[88:91], a[68:71]\n" \
"ds_read_b64_tr_b16 v[170:171], v37 offset:9216\n" \
"v_mfma_f32_16x16x32_bf16 a[100:103], v[64:67], v[88:91], a[100:103]\n" \
"s_cmpk_lt_u32 s3, 3\n" \
"s_cselect_b32 s51, s51, s52\n" \
"v_mfma_f32_16x16x32_bf16 a[64:67], v[60:63], v[84:87], a[64:67]\n" \
"buffer_load_dword v209, v194, s[76:79], s34 offen offset:1184\n" \
"v_mfma_f32_16x16x32_bf16 a[96:99], v[64:67], v[84:87], a[96:99]\n" \
"ds_read_b64_tr_b16 v[172:173], v38 offset:8192\n" \
"s_mul_i32 s52, s3, 0x9000\n" \
"s_waitcnt lgkmcnt(15)\n" \
"v_mfma_f32_16x16x32_bf16 a[128:131], v[68:71], v[84:87], a[128:131]\n" \
"ds_read_b64_tr_b16 v[174:175], v38 offset:9216\n" \
"v_writelane_b32 v11, s51, 18\n" \
"v_mfma_f32_16x16x32_bf16 a[160:163], v[72:75], v[84:87], a[160:163]\n" \
"s_mul_i32 s51, s49, 0x2400\n" \
"s_bitset1_b32 s51, 17\n" \
"v_mfma_f32_16x16x32_bf16 a[132:135], v[68:71], v[88:91], a[132:135]\n" \
"buffer_load_dword v211, v194, s[76:79], s34 offen offset:2336\n" \
"v_mfma_f32_16x16x32_bf16 a[164:167], v[72:75], v[88:91], a[164:167]\n" \
"ds_read_b64_tr_b16 v[176:177], v39 offset:8192\n" \
"v_writelane_b32 v11, s51, 19\n" \
"v_mfma_f32_16x16x32_bf16 a[136:139], v[68:71], v[92:95], a[136:139]\n" \
"ds_read_b64_tr_b16 v[178:179], v39 offset:9216\n" \
"s_mul_i32 s49, s49, 0x9000\n" \
"v_mfma_f32_16x16x32_bf16 a[168:171], v[72:75], v[92:95], a[168:171]\n" \
"s_add_i32 s49, s44, s49\n" \
"v_writelane_b32 v11, s49, 24\n" \
"v_mfma_f32_16x16x32_bf16 a[140:143], v[68:71], v[96:99], a[140:143]\n" \
"s_mov_b32 s51, 0x12000\n" \
"s_mul_i32 s49, s3, 0x2400\n" \
"v_mfma_f32_16x16x32_bf16 a[172:175], v[72:75], v[96:99], a[172:175]\n" \
"s_mov_b32 s76, s20\n" \
"s_and_b32 s77, s21, 0xffff\n" \
"v_mfma_f32_16x16x32_bf16 a[144:147], v[68:71], v[100:103], a[144:147]\n" \
"s_mov_b32 s78, 0x480\n" \
"ds_read_b128 v[124:127], v29 offset:20480\n" \
"v_mfma_f32_16x16x32_bf16 a[176:179], v[72:75], v[100:103], a[176:179]\n" \
"buffer_load_ushort v210, v195, s[76:79], 0 offen\n" \
"v_mfma_f32_16x16x32_bf16 a[148:151], v[68:71], v[104:107], a[148:151]\n" \
"ds_read_b128 v[128:131], v29 offset:22528\n" \
"v_mfma_f32_16x16x32_bf16 a[180:183], v[72:75], v[104:107], a[180:183]\n" \
"s_mov_b32 s76, s8\n" \
"s_and_b32 s77, s9, 0xffff\n" \
"v_mfma_f32_16x16x32_bf16 a[152:155], v[68:71], v[108:111], a[152:155]\n" \
"s_mov_b32 s78, 0x240000\n" \
"ds_read_b128 v[132:135], v29 offset:0\n" \
"v_mfma_f32_16x16x32_bf16 a[184:187], v[72:75], v[108:111], a[184:187]\n" \
"ds_read_b128 v[136:139], v29 offset:2048\n" \
"v_mfma_f32_16x16x32_bf16 a[156:159], v[68:71], v[112:115], a[156:159]\n" \
"s_cmpk_lt_u32 s3, 3\n" \
"s_cselect_b32 s51, s49, s51\n" \
"v_mfma_f32_16x16x32_bf16 a[188:191], v[72:75], v[112:115], a[188:191]\n" \
"ds_read_b128 v[140:143], v29 offset:4096\n" \
"s_xor_b32 s45, s45, 2\n" \
"v_mfma_f32_16x16x32_bf16 a[220:223], v[76:79], v[112:115], a[220:223]\n" \
"v_writelane_b32 v11, s51, 20\n" \
"s_mov_b32 s51, 0xfc00\n" \
"v_mfma_f32_16x16x32_bf16 a[252:255], v[80:83], v[112:115], a[252:255]\n" \
"ds_read_b128 v[144:147], v29 offset:6144\n" \
"v_writelane_b32 v11, s49, 21\n" \
"v_mfma_f32_16x16x32_bf16 a[216:219], v[76:79], v[108:111], a[216:219]\n" \
"s_bitset1_b32 s49, 17\n" \
"v_mfma_f32_16x16x32_bf16 a[248:251], v[80:83], v[108:111], a[248:251]\n" \
"s_cmpk_lt_u32 s3, 3\n" \
"s_cselect_b32 s49, s49, s51\n" \
"v_mfma_f32_16x16x32_bf16 a[212:215], v[76:79], v[104:107], a[212:215]\n" \
"v_and_or_b32 v10, v212, 24, v213\n" \
"v_writelane_b32 v11, s49, 22\n" \
"v_mfma_f32_16x16x32_bf16 a[244:247], v[80:83], v[104:107], a[244:247]\n" \
"s_lshl_b32 s49, s3, 10\n" \
"s_add_i32 s49, s49, 0x1d400\n" \
"v_mfma_f32_16x16x32_bf16 a[208:211], v[76:79], v[100:103], a[208:211]\n" \
"v_writelane_b32 v11, s49, 23\n" \
"s_add_i32 s49, s48, 1\n" \
"v_mfma_f32_16x16x32_bf16 a[240:243], v[80:83], v[100:103], a[240:243]\n" \
"s_and_b32 s49, s49, 3\n" \
"s_or_b32 s49, s49, s7\n" \
"v_mfma_f32_16x16x32_bf16 a[204:207], v[76:79], v[96:99], a[204:207]\n" \
"s_mul_i32 s51, s49, 0x24000\n" \
"s_add_i32 s51, s51, s52\n" \
"v_mfma_f32_16x16x32_bf16 a[236:239], v[80:83], v[96:99], a[236:239]\n" \
"s_add_i32 s48, s48, -1\n" \
"v_writelane_b32 v11, s51, 25\n" \
"v_mfma_f32_16x16x32_bf16 a[200:203], v[76:79], v[92:95], a[200:203]\n" \
"s_mul_i32 s51, s45, 0x24000\n" \
"s_and_b32 s48, s48, 3\n" \
"v_mfma_f32_16x16x32_bf16 a[232:235], v[80:83], v[92:95], a[232:235]\n" \
"s_add_i32 s51, s51, s52\n" \
"s_or_b32 s48, s48, s7\n" \
"v_mfma_f32_16x16x32_bf16 a[196:199], v[76:79], v[88:91], a[196:199]\n" \
"v_writelane_b32 v11, s51, 26\n" \
"s_mul_i32 s51, s48, 0x24000\n" \
"v_mfma_f32_16x16x32_bf16 a[228:231], v[80:83], v[88:91], a[228:231]\n" \
"s_add_i32 s51, s51, s52\n" \
"v_writelane_b32 v11, s51, 27\n" \
"v_mfma_f32_16x16x32_bf16 a[192:195], v[76:79], v[84:87], a[192:195]\n" \
"s_or_b32 s51, s47, s46\n" \
"s_mul_i32 s51, s51, 0x24800\n" \
"v_mfma_f32_16x16x32_bf16 a[224:227], v[80:83], v[84:87], a[224:227]\n" \
"s_waitcnt vmcnt(7) lgkmcnt(0)\n" \
"s_barrier\n" \
"v_mfma_f32_16x16x32_bf16 a[0:3], v[116:119], v[148:151], a[0:3]\n" \
"s_add_u32 m0, s30, 0x0\n" \
"s_mov_b32 s85, 0x200\n" \
"v_mfma_f32_16x16x32_bf16 a[32:35], v[120:123], v[148:151], a[32:35]\n" \
"buffer_load_dwordx4 v22, s[64:67], s85 offen lds\n" \
"v_mfma_f32_16x16x32_bf16 a[4:7], v[116:119], v[152:155], a[4:7]\n" \
"ds_read_b64_tr_b16 v[84:85], v32 offset:32768\n" \
"s_lshl_b32 s49, s49, 12\n" \
"v_mfma_f32_16x16x32_bf16 a[36:39], v[120:123], v[152:155], a[36:39]\n" \
"ds_read_b64_tr_b16 v[86:87], v32 offset:33792\n" \
"s_add_i32 s52, s51, 0x3004800\n" \
"v_mfma_f32_16x16x32_bf16 a[8:11], v[116:119], v[156:159], a[8:11]\n" \
"s_add_u32 m0, s30, 0x400\n" \
"s_mov_b32 s84, 0x2200\n" \
"v_mfma_f32_16x16x32_bf16 a[40:43], v[120:123], v[156:159], a[40:43]\n" \
"buffer_load_dwordx4 v23, s[64:67], s84 offen lds\n" \
"v_mfma_f32_16x16x32_bf16 a[12:15], v[116:119], v[160:163], a[12:15]\n" \
"ds_read_b128 v[52:55], v28 offset:49152\n" \
"s_add_i32 s53, s52, s49\n" \
"v_mfma_f32_16x16x32_bf16 a[44:47], v[120:123], v[160:163], a[44:47]\n" \
"ds_read_b128 v[56:59], v28 offset:51200\n" \
"v_writelane_b32 v11, s53, 28\n" \
"v_mfma_f32_16x16x32_bf16 a[16:19], v[116:119], v[164:167], a[16:19]\n" \
"s_add_u32 m0, s31, 0x0\n" \
"s_mov_b32 s85, 0x200000\n" \
"v_mfma_f32_16x16x32_bf16 a[48:51], v[120:123], v[164:167], a[48:51]\n" \
"buffer_load_dwordx4 v26, s[68:71], s85 offen lds\n" \
"v_mfma_f32_16x16x32_bf16 a[20:23], v[116:119], v[168:171], a[20:23]\n" \
"ds_read_b64_tr_b16 v[88:89], v33 offset:32768\n" \
"s_lshl_b32 s45, s45, 12\n" \
"v_mfma_f32_16x16x32_bf16 a[52:55], v[120:123], v[168:171], a[52:55]\n" \
"ds_read_b64_tr_b16 v[90:91], v33 offset:33792\n" \
"s_lshl_b32 s48, s48, 12\n" \
"v_mfma_f32_16x16x32_bf16 a[24:27], v[116:119], v[172:175], a[24:27]\n" \
"s_add_u32 m0, s31, 0x4000\n" \
"s_mov_b32 s84, 0x200000\n" \
"v_mfma_f32_16x16x32_bf16 a[56:59], v[120:123], v[172:175], a[56:59]\n" \
"buffer_load_dwordx4 v26, s[72:75], s84 offen lds\n" \
"v_mfma_f32_16x16x32_bf16 a[28:31], v[116:119], v[176:179], a[28:31]\n" \
"ds_read_b64_tr_b16 v[92:93], v34 offset:32768\n" \
"s_sub_i32 s46, s47, s46\n" \
"v_mfma_f32_16x16x32_bf16 a[60:63], v[120:123], v[176:179], a[60:63]\n" \
"ds_read_b64_tr_b16 v[94:95], v34 offset:33792\n" \
"s_add_i32 s53, s52, s45\n" \
"v_mfma_f32_16x16x32_bf16 a[92:95], v[124:127], v[176:179], a[92:95]\n" \
"s_add_u32 m0, s30, 0x800\n" \
"s_mov_b32 s85, 0x4200\n" \
"v_mfma_f32_16x16x32_bf16 a[124:127], v[128:131], v[176:179], a[124:127]\n" \
"buffer_load_dwordx4 v22, s[64:67], s85 offen lds\n" \
"v_mfma_f32_16x16x32_bf16 a[88:91], v[124:127], v[172:175], a[88:91]\n" \
"ds_read_b64_tr_b16 v[96:97], v35 offset:32768\n" \
"v_writelane_b32 v11, s53, 29\n" \
"v_mfma_f32_16x16x32_bf16 a[120:123], v[128:131], v[172:175], a[120:123]\n" \
"ds_read_b64_tr_b16 v[98:99], v35 offset:33792\n" \
"s_add_i32 s52, s52, s48\n" \
"s_nop 0\n" \
"v_mfma_f32_16x16x32_bf16 a[84:87], v[124:127], v[168:171], a[84:87]\n" \
"s_add_u32 m0, s30, 0xc00\n" \
"s_mov_b32 s84, 0x6200\n" \
"v_mfma_f32_16x16x32_bf16 a[116:119], v[128:131], v[168:171], a[116:119]\n" \
"buffer_load_dwordx4 v23, s[64:67], s84 offen lds\n" \
"v_mfma_f32_16x16x32_bf16 a[80:83], v[124:127], v[164:167], a[80:83]\n" \
"ds_read_b64_tr_b16 v[100:101], v36 offset:32768\n" \
"s_mul_i32 s46, s46, 0x24800\n" \
"v_mfma_f32_16x16x32_bf16 a[112:115], v[128:131], v[164:167], a[112:115]\n" \
"ds_read_b64_tr_b16 v[102:103], v36 offset:33792\n" \
"v_writelane_b32 v11, s52, 30\n" \
"v_mfma_f32_16x16x32_bf16 a[76:79], v[124:127], v[160:163], a[76:79]\n" \
"s_add_u32 m0, s31, 0x400\n" \
"s_mov_b32 s85, 0x208000\n" \
"v_mfma_f32_16x16x32_bf16 a[108:111], v[128:131], v[160:163], a[108:111]\n" \
"buffer_load_dwordx4 v26, s[68:71], s85 offen lds\n" \
"v_mfma_f32_16x16x32_bf16 a[72:75], v[124:127], v[156:159], a[72:75]\n" \
"ds_read_b64_tr_b16 v[104:105], v37 offset:32768\n" \
"s_add_i32 s52, s46, 0x4120800\n" \
"v_mfma_f32_16x16x32_bf16 a[104:107], v[128:131], v[156:159], a[104:107]\n" \
"s_add_i32 s49, s52, s49\n" \
"v_writelane_b32 v11, s49, 31\n" \
"v_mfma_f32_16x16x32_bf16 a[68:71], v[124:127], v[152:155], a[68:71]\n" \
"s_add_u32 m0, s31, 0x4400\n" \
"s_mov_b32 s84, 0x208000\n" \
"v_mfma_f32_16x16x32_bf16 a[100:103], v[128:131], v[152:155], a[100:103]\n" \
"buffer_load_dwordx4 v26, s[72:75], s84 offen lds\n" \
"v_mfma_f32_16x16x32_bf16 a[64:67], v[124:127], v[148:151], a[64:67]\n" \
"ds_read_b64_tr_b16 v[106:107], v37 offset:33792\n" \
"s_add_i32 s45, s52, s45\n" \
"v_mfma_f32_16x16x32_bf16 a[96:99], v[128:131], v[148:151], a[96:99]\n" \
"ds_read_b64_tr_b16 v[108:109], v38 offset:32768\n" \
"v_writelane_b32 v11, s45, 32\n" \
"v_mfma_f32_16x16x32_bf16 a[128:131], v[132:135], v[148:151], a[128:131]\n" \
"s_add_u32 m0, s30, 0x1000\n" \
"s_mov_b32 s85, 0x8200\n" \
"v_mfma_f32_16x16x32_bf16 a[160:163], v[136:139], v[148:151], a[160:163]\n" \
"buffer_load_dwordx4 v22, s[64:67], s85 offen lds\n" \
"v_mfma_f32_16x16x32_bf16 a[132:135], v[132:135], v[152:155], a[132:135]\n" \
"ds_read_b64_tr_b16 v[110:111], v38 offset:33792\n" \
"s_add_i32 s52, s52, s48\n" \
"v_mfma_f32_16x16x32_bf16 a[164:167], v[136:139], v[152:155], a[164:167]\n" \
"ds_read_b64_tr_b16 v[112:113], v39 offset:32768\n" \
"s_or_b32 s45, s51, 0x3000000\n" \
"v_mfma_f32_16x16x32_bf16 a[136:139], v[132:135], v[156:159], a[136:139]\n" \
"s_add_u32 m0, s30, 0x1400\n" \
"s_mov_b32 s84, 0xa200\n" \
"v_mfma_f32_16x16x32_bf16 a[168:171], v[136:139], v[156:159], a[168:171]\n" \
"buffer_load_dwordx4 v23, s[64:67], s84 offen lds\n" \
"v_mfma_f32_16x16x32_bf16 a[140:143], v[132:135], v[160:163], a[140:143]\n" \
"ds_read_b64_tr_b16 v[114:115], v39 offset:33792\n" \
"s_lshl_b32 s48, s41, 11\n" \
"v_mfma_f32_16x16x32_bf16 a[172:175], v[136:139], v[160:163], a[172:175]\n" \
"ds_read_b128 v[60:63], v28 offset:53248\n" \
"v_writelane_b32 v11, s52, 33\n" \
"v_mfma_f32_16x16x32_bf16 a[144:147], v[132:135], v[164:167], a[144:147]\n" \
"s_add_u32 m0, s31, 0x800\n" \
"s_mov_b32 s85, 0x210000\n" \
"v_mfma_f32_16x16x32_bf16 a[176:179], v[136:139], v[164:167], a[176:179]\n" \
"buffer_load_dwordx4 v27, s[68:71], s85 offen lds\n" \
"v_mfma_f32_16x16x32_bf16 a[148:151], v[132:135], v[168:171], a[148:151]\n" \
"ds_read_b128 v[64:67], v28 offset:55296\n" \
"s_add_i32 s49, s45, s48\n" \
"s_nop 0\n" \
"v_mfma_f32_16x16x32_bf16 a[180:183], v[136:139], v[168:171], a[180:183]\n" \
"ds_read_b128 v[68:71], v28 offset:32768\n" \
"v_writelane_b32 v11, s49, 34\n" \
"v_mfma_f32_16x16x32_bf16 a[152:155], v[132:135], v[172:175], a[152:155]\n" \
"s_add_u32 m0, s31, 0x4800\n" \
"s_mov_b32 s84, 0x210000\n" \
"v_mfma_f32_16x16x32_bf16 a[184:187], v[136:139], v[172:175], a[184:187]\n" \
"buffer_load_dwordx4 v27, s[72:75], s84 offen lds\n" \
"v_mfma_f32_16x16x32_bf16 a[156:159], v[132:135], v[176:179], a[156:159]\n" \
"ds_read_b128 v[72:75], v28 offset:34816\n" \
"s_add_i32 s49, s46, 0x411c000\n" \
"v_mfma_f32_16x16x32_bf16 a[188:191], v[136:139], v[176:179], a[188:191]\n" \
"ds_read_b128 v[76:79], v28 offset:36864\n" \
"s_add_i32 s52, s49, s48\n" \
"v_mfma_f32_16x16x32_bf16 a[220:223], v[140:143], v[176:179], a[220:223]\n" \
"v_writelane_b32 v11, s52, 35\n" \
"s_add_i32 s51, s51, 0x3004000\n" \
"v_mfma_f32_16x16x32_bf16 a[252:255], v[144:147], v[176:179], a[252:255]\n" \
"ds_read_b128 v[80:83], v28 offset:38912\n" \
"v_writelane_b32 v11, s51, 36\n" \
"v_mfma_f32_16x16x32_bf16 a[216:219], v[140:143], v[172:175], a[216:219]\n" \
"s_add_i32 s46, s46, 0x4120000\n" \
"v_writelane_b32 v11, s46, 37\n" \
"v_mfma_f32_16x16x32_bf16 a[248:251], v[144:147], v[172:175], a[248:251]\n" \
"s_lshl_b32 s46, s5, 2\n" \
"s_or_b32 s43, s46, s43\n" \
"v_mfma_f32_16x16x32_bf16 a[212:215], v[140:143], v[168:171], a[212:215]\n" \
"s_mul_i32 s43, s43, 0x90000\n" \
"s_lshl_b32 s46, s3, 14\n" \
"v_mfma_f32_16x16x32_bf16 a[244:247], v[144:147], v[168:171], a[244:247]\n" \
"s_add_i32 s43, s43, s46\n" \
"s_lshl_b32 s46, s47, 4\n" \
"v_mfma_f32_16x16x32_bf16 a[208:211], v[140:143], v[164:167], a[208:211]\n" \
"s_lshl_b32 s47, s5, 7\n" \
"s_or_b32 s51, s46, s47\n" \
"v_mfma_f32_16x16x32_bf16 a[240:243], v[144:147], v[164:167], a[240:243]\n" \
"s_sub_i32 s46, s46, s47\n" \
"s_mulk_i32 s51, 0x480\n" \
"v_mfma_f32_16x16x32_bf16 a[204:207], v[140:143], v[160:163], a[204:207]\n" \
"s_mulk_i32 s46, 0x480\n" \
"s_or_b32 s47, s51, s50\n" \
"v_mfma_f32_16x16x32_bf16 a[236:239], v[144:147], v[160:163], a[236:239]\n" \
"s_or_b32 s46, s46, s50\n" \
"s_and_b32 s50, s2, 1\n" \
"v_mfma_f32_16x16x32_bf16 a[200:203], v[140:143], v[156:159], a[200:203]\n" \
"s_cmp_lg_u32 s50, 0\n" \
"s_cselect_b32 s45, s49, s45\n" \
"v_mfma_f32_16x16x32_bf16 a[232:235], v[144:147], v[156:159], a[232:235]\n" \
"s_bfe_u32 s49, s2, 0x20001\n" \
"s_addk_i32 s45, 0x4000\n" \
"v_mfma_f32_16x16x32_bf16 a[196:199], v[140:143], v[152:155], a[196:199]\n" \
"s_xor_b32 s49, s49, s3\n" \
"v_mfma_f32_16x16x32_bf16 a[228:231], v[144:147], v[152:155], a[228:231]\n" \
"s_cmp_lg_u32 s49, 0\n" \
"s_cselect_b32 s45, s42, s45\n" \
"v_mfma_f32_16x16x32_bf16 a[192:195], v[140:143], v[148:151], a[192:195]\n" \
"s_lshl_b32 s41, s41, 16\n" \
"v_writelane_b32 v11, s45, 38\n" \
"v_mfma_f32_16x16x32_bf16 a[224:227], v[144:147], v[148:151], a[224:227]\n" \
"s_waitcnt lgkmcnt(15)\n" \
"v_mfma_f32_16x16x32_bf16 a[0:3], v[52:55], v[84:87], a[0:3]\n" \
"s_add_u32 m0, s30, 0x1800\n" \
"s_mov_b32 s85, 0xc200\n" \
"v_mfma_f32_16x16x32_bf16 a[32:35], v[56:59], v[84:87], a[32:35]\n" \
"buffer_load_dwordx4 v22, s[64:67], s85 offen lds\n" \
"v_mfma_f32_16x16x32_bf16 a[4:7], v[52:55], v[88:91], a[4:7]\n" \
"ds_read_b64_tr_b16 v[148:149], v32 offset:40960\n" \
"s_add_i32 s45, s43, 0x4240000\n" \
"v_mfma_f32_16x16x32_bf16 a[36:39], v[56:59], v[88:91], a[36:39]\n" \
"s_add_u32 m0, s30, 0x1c00\n" \
"s_mov_b32 s84, 0xe200\n" \
"v_mfma_f32_16x16x32_bf16 a[8:11], v[52:55], v[92:95], a[8:11]\n" \
"buffer_load_dwordx4 v23, s[64:67], s84 offen lds\n" \
"v_mfma_f32_16x16x32_bf16 a[40:43], v[56:59], v[92:95], a[40:43]\n" \
"ds_read_b64_tr_b16 v[150:151], v32 offset:41984\n" \
"s_add_i32 s41, s45, s41\n" \
"s_waitcnt lgkmcnt(15)\n" \
"v_mfma_f32_16x16x32_bf16 a[12:15], v[52:55], v[96:99], a[12:15]\n" \
"s_add_u32 m0, s31, 0xc00\n" \
"s_mov_b32 s85, 0x218000\n" \
"v_mfma_f32_16x16x32_bf16 a[44:47], v[56:59], v[96:99], a[44:47]\n" \
"buffer_load_dwordx4 v27, s[68:71], s85 offen lds\n" \
"s_waitcnt lgkmcnt(14)\n" \
"v_mfma_f32_16x16x32_bf16 a[16:19], v[52:55], v[100:103], a[16:19]\n" \
"ds_read_b128 v[116:119], v29 offset:49152\n" \
"v_writelane_b32 v11, s41, 39\n" \
"v_mfma_f32_16x16x32_bf16 a[48:51], v[56:59], v[100:103], a[48:51]\n" \
"s_add_u32 m0, s31, 0x4c00\n" \
"s_mov_b32 s84, 0x218000\n" \
"s_waitcnt lgkmcnt(13)\n" \
"v_mfma_f32_16x16x32_bf16 a[20:23], v[52:55], v[104:107], a[20:23]\n" \
"buffer_load_dwordx4 v27, s[72:75], s84 offen lds\n" \
"v_mfma_f32_16x16x32_bf16 a[52:55], v[56:59], v[104:107], a[52:55]\n" \
"ds_read_b128 v[120:123], v29 offset:51200\n" \
"s_add_i32 s43, s43, 0x42c0000\n" \
"s_waitcnt lgkmcnt(12)\n" \
"v_mfma_f32_16x16x32_bf16 a[24:27], v[52:55], v[108:111], a[24:27]\n" \
"ds_read_b64_tr_b16 v[152:153], v33 offset:40960\n" \
"s_lshl_b32 s41, s50, 10\n" \
"v_mfma_f32_16x16x32_bf16 a[56:59], v[56:59], v[108:111], a[56:59]\n" \
"ds_read_b64_tr_b16 v[154:155], v33 offset:41984\n" \
"s_or_b32 s41, s43, s41\n" \
"s_waitcnt lgkmcnt(12)\n" \
"v_mfma_f32_16x16x32_bf16 a[28:31], v[52:55], v[112:115], a[28:31]\n" \
"ds_read_b64_tr_b16 v[156:157], v34 offset:40960\n" \
"v_mfma_f32_16x16x32_bf16 a[60:63], v[56:59], v[112:115], a[60:63]\n" \
"ds_read_b64_tr_b16 v[158:159], v34 offset:41984\n" \
"s_waitcnt lgkmcnt(13)\n" \
"v_mfma_f32_16x16x32_bf16 a[92:95], v[60:63], v[112:115], a[92:95]\n" \
"s_cmp_lg_u32 s49, 0\n" \
"s_cselect_b32 s41, s42, s41\n" \
"s_waitcnt lgkmcnt(12)\n" \
"v_mfma_f32_16x16x32_bf16 a[124:127], v[64:67], v[112:115], a[124:127]\n" \
"ds_read_b64_tr_b16 v[160:161], v35 offset:40960\n" \
"s_mulk_i32 s5, 0x4800\n" \
"v_mfma_f32_16x16x32_bf16 a[88:91], v[60:63], v[108:111], a[88:91]\n" \
"v_writelane_b32 v11, s41, 40\n" \
"s_add_i32 s41, s47, s40\n" \
"v_mfma_f32_16x16x32_bf16 a[120:123], v[64:67], v[108:111], a[120:123]\n" \
"ds_read_b64_tr_b16 v[162:163], v35 offset:41984\n" \
"v_writelane_b32 v11, s41, 41\n" \
"v_mfma_f32_16x16x32_bf16 a[84:87], v[60:63], v[104:107], a[84:87]\n" \
"s_mov_b32 m0, s32\n" \
"s_add_i32 s41, s46, 0x21c000\n" \
"v_mfma_f32_16x16x32_bf16 a[116:119], v[64:67], v[104:107], a[116:119]\n" \
"s_mov_b32 exec_lo, 0xffffffff\n" \
"s_mov_b32 exec_hi, 0x0\n" \
"global_load_lds_dword v[188:189], off\n" \
"s_mov_b64 exec, -1\n" \
"v_mfma_f32_16x16x32_bf16 a[80:83], v[60:63], v[100:103], a[80:83]\n" \
"ds_read_b64_tr_b16 v[164:165], v36 offset:40960\n" \
"v_mfma_f32_16x16x32_bf16 a[112:115], v[64:67], v[100:103], a[112:115]\n" \
"ds_read_b64_tr_b16 v[166:167], v36 offset:41984\n" \
"v_mfma_f32_16x16x32_bf16 a[76:79], v[60:63], v[96:99], a[76:79]\n" \
"s_waitcnt vmcnt(17)\n" \
"v_or_b32_e32 v204, v210, v204\n" \
"v_or_b32_e64 v206, v210, v206\n" \
"v_mfma_f32_16x16x32_bf16 a[108:111], v[64:67], v[96:99], a[108:111]\n" \
"v_mov_b32_e64 v210, 0\n" \
"s_add_u32 s88, s35, 0x1000\n" \
"v_mfma_f32_16x16x32_bf16 a[72:75], v[60:63], v[92:95], a[72:75]\n" \
"ds_read_b64_tr_b16 v[168:169], v37 offset:40960\n" \
"v_mfma_f32_16x16x32_bf16 a[104:107], v[64:67], v[92:95], a[104:107]\n" \
"s_cmp_lg_u32 s50, 0\n" \
"s_cselect_b32 s46, s41, s47\n" \
"v_mfma_f32_16x16x32_bf16 a[68:71], v[60:63], v[88:91], a[68:71]\n" \
"ds_read_b64_tr_b16 v[170:171], v37 offset:41984\n" \
"s_add_i32 s41, s41, s40\n" \
"v_mfma_f32_16x16x32_bf16 a[100:103], v[64:67], v[88:91], a[100:103]\n" \
"v_writelane_b32 v11, s41, 42\n" \
"s_addk_i32 s46, 0x100\n" \
"v_mfma_f32_16x16x32_bf16 a[64:67], v[60:63], v[84:87], a[64:67]\n" \
"ds_write_b128 v196, v[200:203]\n" \
"v_mfma_f32_16x16x32_bf16 a[96:99], v[64:67], v[84:87], a[96:99]\n" \
"ds_read_b64_tr_b16 v[172:173], v38 offset:40960\n" \
"s_waitcnt lgkmcnt(15)\n" \
"v_mfma_f32_16x16x32_bf16 a[128:131], v[68:71], v[84:87], a[128:131]\n" \
"ds_read_b64_tr_b16 v[174:175], v38 offset:41984\n" \
"v_mfma_f32_16x16x32_bf16 a[160:163], v[72:75], v[84:87], a[160:163]\n" \
"s_cmp_lg_u32 s49, 0\n" \
"s_cselect_b32 s40, s42, s46\n" \
"v_mfma_f32_16x16x32_bf16 a[132:135], v[68:71], v[88:91], a[132:135]\n" \
"ds_read_b64_tr_b16 v[176:177], v39 offset:40960\n" \
"s_lshl_b32 s4, s4, 6\n" \
"v_mfma_f32_16x16x32_bf16 a[164:167], v[72:75], v[88:91], a[164:167]\n" \
"v_writelane_b32 v11, s40, 43\n" \
"s_or_b32 s40, s43, s48\n" \
"v_mfma_f32_16x16x32_bf16 a[136:139], v[68:71], v[92:95], a[136:139]\n" \
"ds_write_b128 v197, v[204:207]\n" \
"v_mfma_f32_16x16x32_bf16 a[168:171], v[72:75], v[92:95], a[168:171]\n" \
"ds_read_b64_tr_b16 v[178:179], v39 offset:41984\n" \
"v_writelane_b32 v11, s40, 51\n" \
"v_mfma_f32_16x16x32_bf16 a[140:143], v[68:71], v[96:99], a[140:143]\n" \
"s_lshl_b32 s40, s3, 17\n" \
"s_add_i32 s40, s45, s40\n" \
"v_mfma_f32_16x16x32_bf16 a[172:175], v[72:75], v[96:99], a[172:175]\n" \
"ds_read_b128 v[124:127], v29 offset:53248\n" \
"s_or_b32 s7, s40, s7\n" \
"v_mfma_f32_16x16x32_bf16 a[144:147], v[68:71], v[100:103], a[144:147]\n" \
"s_or_b32 s7, s7, 0x3840\n" \
"v_writelane_b32 v11, s7, 6\n" \
"v_mfma_f32_16x16x32_bf16 a[176:179], v[72:75], v[100:103], a[176:179]\n" \
"ds_read_b128 v[128:131], v29 offset:55296\n" \
"s_mov_b32 s7, 0xff0000\n" \
"v_mfma_f32_16x16x32_bf16 a[148:151], v[68:71], v[104:107], a[148:151]\n" \
"ds_write_b128 v198, v[208:211]\n" \
"v_mfma_f32_16x16x32_bf16 a[180:183], v[72:75], v[104:107], a[180:183]\n" \
"ds_read_b128 v[132:135], v29 offset:32768\n" \
"v_mfma_f32_16x16x32_bf16 a[152:155], v[68:71], v[108:111], a[152:155]\n" \
"s_cmp_lg_u32 s49, 0\n" \
"s_cselect_b32 s6, s6, s7\n" \
"v_mfma_f32_16x16x32_bf16 a[184:187], v[72:75], v[108:111], a[184:187]\n" \
"ds_read_b128 v[136:139], v29 offset:34816\n" \
"v_lshrrev_b32_e64 v213, 2, v20\n" \
"v_mfma_f32_16x16x32_bf16 a[156:159], v[68:71], v[112:115], a[156:159]\n" \
"v_writelane_b32 v11, s6, 7\n" \
"s_mul_i32 s6, s3, 0x1200\n" \
"v_mfma_f32_16x16x32_bf16 a[188:191], v[72:75], v[112:115], a[188:191]\n" \
"ds_read_b128 v[140:143], v29 offset:36864\n" \
"s_add_i32 s5, s6, s5\n" \
"v_mfma_f32_16x16x32_bf16 a[220:223], v[76:79], v[112:115], a[220:223]\n" \
"s_add_i32 s5, s5, s44\n" \
"v_writelane_b32 v11, s5, 8\n" \
"v_mfma_f32_16x16x32_bf16 a[252:255], v[80:83], v[112:115], a[252:255]\n" \
"ds_read_b128 v[144:147], v29 offset:38912\n" \
"s_lshl_b32 s5, s2, 16\n" \
"v_mfma_f32_16x16x32_bf16 a[216:219], v[76:79], v[108:111], a[216:219]\n" \
"s_add_i32 s6, s5, 0x10000\n" \
"s_and_b32 s6, s6, 0x70000\n" \
"v_mfma_f32_16x16x32_bf16 a[248:251], v[80:83], v[108:111], a[248:251]\n" \
"s_add_i32 s6, s45, s6\n" \
"v_writelane_b32 v11, s6, 44\n" \
"v_mfma_f32_16x16x32_bf16 a[212:215], v[76:79], v[104:107], a[212:215]\n" \
"s_add_i32 s6, s5, 0x20000\n" \
"s_and_b32 s6, s6, 0x70000\n" \
"v_mfma_f32_16x16x32_bf16 a[244:247], v[80:83], v[104:107], a[244:247]\n" \
"s_add_i32 s6, s45, s6\n" \
"s_bitset1_b32 s6, 11\n" \
"v_mfma_f32_16x16x32_bf16 a[208:211], v[76:79], v[100:103], a[208:211]\n" \
"v_writelane_b32 v11, s6, 45\n" \
"s_add_i32 s6, s5, 0x30000\n" \
"v_mfma_f32_16x16x32_bf16 a[240:243], v[80:83], v[100:103], a[240:243]\n" \
"s_and_b32 s6, s6, 0x70000\n" \
"s_add_i32 s6, s45, s6\n" \
"v_mfma_f32_16x16x32_bf16 a[204:207], v[76:79], v[96:99], a[204:207]\n" \
"s_bitset1_b32 s6, 12\n" \
"v_writelane_b32 v11, s6, 46\n" \
"v_mfma_f32_16x16x32_bf16 a[236:239], v[80:83], v[96:99], a[236:239]\n" \
"s_and_b32 s6, s5, 0x70000\n" \
"s_xor_b32 s6, s6, 0x40000\n" \
"v_mfma_f32_16x16x32_bf16 a[200:203], v[76:79], v[92:95], a[200:203]\n" \
"s_add_i32 s6, s45, s6\n" \
"s_or_b32 s6, s6, 0x1800\n" \
"v_mfma_f32_16x16x32_bf16 a[232:235], v[80:83], v[92:95], a[232:235]\n" \
"v_writelane_b32 v11, s6, 47\n" \
"s_add_i32 s6, s5, 0x50000\n" \
"v_mfma_f32_16x16x32_bf16 a[196:199], v[76:79], v[88:91], a[196:199]\n" \
"s_and_b32 s6, s6, 0x70000\n" \
"s_add_i32 s6, s45, s6\n" \
"v_mfma_f32_16x16x32_bf16 a[228:231], v[80:83], v[88:91], a[228:231]\n" \
"s_bitset1_b32 s6, 13\n" \
"v_writelane_b32 v11, s6, 48\n" \
"v_mfma_f32_16x16x32_bf16 a[192:195], v[76:79], v[84:87], a[192:195]\n" \
"s_add_i32 s6, s5, 0x60000\n" \
"s_and_b32 s6, s6, 0x70000\n" \
"v_mfma_f32_16x16x32_bf16 a[224:227], v[80:83], v[84:87], a[224:227]\n" \
"s_waitcnt vmcnt(1) lgkmcnt(0)\n" \
"s_barrier\n" \
"s_nop 0\n" \
"v_mfma_f32_16x16x32_bf16 a[0:3], v[116:119], v[148:151], a[0:3]\n" \
"s_add_u32 m0, s30, 0x8000\n" \
"s_mov_b32 s85, 0x280\n" \
"v_mfma_f32_16x16x32_bf16 a[32:35], v[120:123], v[148:151], a[32:35]\n" \
"buffer_load_dwordx4 v22, s[64:67], s85 offen lds\n" \
"v_mfma_f32_16x16x32_bf16 a[4:7], v[116:119], v[152:155], a[4:7]\n" \
"ds_read_b64_tr_b16 v[84:85], v32 offset:0\n" \
"s_add_i32 s5, s5, 0x70000\n" \
"v_mfma_f32_16x16x32_bf16 a[36:39], v[120:123], v[152:155], a[36:39]\n" \
"s_add_u32 m0, s30, 0x8400\n" \
"s_mov_b32 s84, 0x2280\n" \
"v_mfma_f32_16x16x32_bf16 a[8:11], v[116:119], v[156:159], a[8:11]\n" \
"buffer_load_dwordx4 v23, s[64:67], s84 offen lds\n" \
"v_mfma_f32_16x16x32_bf16 a[40:43], v[120:123], v[156:159], a[40:43]\n" \
"ds_read_b64_tr_b16 v[86:87], v32 offset:1024\n" \
"s_add_i32 s6, s45, s6\n" \
"s_nop 0\n" \
"v_mfma_f32_16x16x32_bf16 a[12:15], v[116:119], v[160:163], a[12:15]\n" \
"s_add_u32 m0, s31, 0x8000\n" \
"s_mov_b32 s85, 0x280000\n" \
"v_mfma_f32_16x16x32_bf16 a[44:47], v[120:123], v[160:163], a[44:47]\n" \
"buffer_load_dwordx4 v26, s[68:71], s85 offen lds\n" \
"v_mfma_f32_16x16x32_bf16 a[16:19], v[116:119], v[164:167], a[16:19]\n" \
"ds_read_b128 v[52:55], v28 offset:16384\n" \
"s_and_b32 s5, s5, 0x70000\n" \
"v_mfma_f32_16x16x32_bf16 a[48:51], v[120:123], v[164:167], a[48:51]\n" \
"s_add_u32 m0, s31, 0xc000\n" \
"s_mov_b32 s84, 0x280000\n" \
"v_mfma_f32_16x16x32_bf16 a[20:23], v[116:119], v[168:171], a[20:23]\n" \
"buffer_load_dwordx4 v26, s[72:75], s84 offen lds\n" \
"v_mfma_f32_16x16x32_bf16 a[52:55], v[120:123], v[168:171], a[52:55]\n" \
"ds_read_b128 v[56:59], v28 offset:18432\n" \
"v_and_or_b32 v214, v20, 16, v222\n" \
"v_mfma_f32_16x16x32_bf16 a[24:27], v[116:119], v[172:175], a[24:27]\n" \
"s_add_u32 m0, s30, 0x8800\n" \
"s_mov_b32 s85, 0x4280\n" \
"v_mfma_f32_16x16x32_bf16 a[56:59], v[120:123], v[172:175], a[56:59]\n" \
"buffer_load_dwordx4 v22, s[64:67], s85 offen lds\n" \
"v_mfma_f32_16x16x32_bf16 a[28:31], v[116:119], v[176:179], a[28:31]\n" \
"ds_read_b64_tr_b16 v[88:89], v33 offset:0\n" \
"s_or_b32 s6, s6, 0x2800\n" \
"v_mfma_f32_16x16x32_bf16 a[60:63], v[120:123], v[176:179], a[60:63]\n" \
"ds_read_b64_tr_b16 v[90:91], v33 offset:1024\n" \
"v_writelane_b32 v11, s6, 49\n" \
"v_mfma_f32_16x16x32_bf16 a[92:95], v[124:127], v[176:179], a[92:95]\n" \
"s_add_u32 m0, s30, 0x8c00\n" \
"s_mov_b32 s84, 0x6280\n" \
"v_mfma_f32_16x16x32_bf16 a[124:127], v[128:131], v[176:179], a[124:127]\n" \
"buffer_load_dwordx4 v23, s[64:67], s84 offen lds\n" \
"v_mfma_f32_16x16x32_bf16 a[88:91], v[124:127], v[172:175], a[88:91]\n" \
"ds_read_b64_tr_b16 v[92:93], v34 offset:0\n" \
"s_add_i32 s45, s45, s5\n" \
"s_nop 0\n" \
"v_mfma_f32_16x16x32_bf16 a[120:123], v[128:131], v[172:175], a[120:123]\n" \
"s_add_u32 m0, s31, 0x8400\n" \
"s_mov_b32 s85, 0x288000\n" \
"v_mfma_f32_16x16x32_bf16 a[84:87], v[124:127], v[168:171], a[84:87]\n" \
"buffer_load_dwordx4 v26, s[68:71], s85 offen lds\n" \
"v_mfma_f32_16x16x32_bf16 a[116:119], v[128:131], v[168:171], a[116:119]\n" \
"ds_read_b64_tr_b16 v[94:95], v34 offset:1024\n" \
"v_and_or_b32 v213, v213, 60, s4\n" \
"v_mfma_f32_16x16x32_bf16 a[80:83], v[124:127], v[164:167], a[80:83]\n" \
"s_add_u32 m0, s31, 0xc400\n" \
"s_mov_b32 s84, 0x288000\n" \
"v_mfma_f32_16x16x32_bf16 a[112:115], v[128:131], v[164:167], a[112:115]\n" \
"buffer_load_dwordx4 v26, s[72:75], s84 offen lds\n" \
"v_mfma_f32_16x16x32_bf16 a[76:79], v[124:127], v[160:163], a[76:79]\n" \
"ds_read_b64_tr_b16 v[96:97], v35 offset:0\n" \
"v_lshl_or_b32 v212, s3, 9, v212\n" \
"v_mfma_f32_16x16x32_bf16 a[108:111], v[128:131], v[160:163], a[108:111]\n" \
"s_add_u32 m0, s30, 0x9000\n" \
"s_mov_b32 s85, 0x8280\n" \
"v_mfma_f32_16x16x32_bf16 a[72:75], v[124:127], v[156:159], a[72:75]\n" \
"buffer_load_dwordx4 v22, s[64:67], s85 offen lds\n" \
"v_mfma_f32_16x16x32_bf16 a[104:107], v[128:131], v[156:159], a[104:107]\n" \
"ds_read_b64_tr_b16 v[98:99], v35 offset:1024\n" \
"v_add_u32_e32 v9, 0x4800, v214\n" \
"v_mfma_f32_16x16x32_bf16 a[68:71], v[124:127], v[152:155], a[68:71]\n" \
"s_add_u32 m0, s30, 0x9400\n" \
"s_mov_b32 s84, 0xa280\n" \
"v_mfma_f32_16x16x32_bf16 a[100:103], v[128:131], v[152:155], a[100:103]\n" \
"buffer_load_dwordx4 v23, s[64:67], s84 offen lds\n" \
"v_mfma_f32_16x16x32_bf16 a[64:67], v[124:127], v[148:151], a[64:67]\n" \
"ds_read_b64_tr_b16 v[100:101], v36 offset:0\n" \
"s_or_b32 s5, s45, 0x3000\n" \
"v_mfma_f32_16x16x32_bf16 a[96:99], v[128:131], v[148:151], a[96:99]\n" \
"ds_read_b64_tr_b16 v[102:103], v36 offset:1024\n" \
"v_writelane_b32 v11, s5, 50\n" \
"v_mfma_f32_16x16x32_bf16 a[128:131], v[132:135], v[148:151], a[128:131]\n" \
"s_add_u32 m0, s31, 0x8800\n" \
"s_mov_b32 s85, 0x290000\n" \
"v_mfma_f32_16x16x32_bf16 a[160:163], v[136:139], v[148:151], a[160:163]\n" \
"buffer_load_dwordx4 v27, s[68:71], s85 offen lds\n" \
"v_mfma_f32_16x16x32_bf16 a[132:135], v[132:135], v[152:155], a[132:135]\n" \
"ds_read_b64_tr_b16 v[104:105], v37 offset:0\n" \
"v_or_b32_e32 v18, 0x27000, v213\n" \
"v_mfma_f32_16x16x32_bf16 a[164:167], v[136:139], v[152:155], a[164:167]\n" \
"s_add_u32 m0, s31, 0xc800\n" \
"s_mov_b32 s84, 0x290000\n" \
"v_mfma_f32_16x16x32_bf16 a[136:139], v[132:135], v[156:159], a[136:139]\n" \
"buffer_load_dwordx4 v27, s[72:75], s84 offen lds\n" \
"v_mfma_f32_16x16x32_bf16 a[168:171], v[136:139], v[156:159], a[168:171]\n" \
"ds_read_b64_tr_b16 v[106:107], v37 offset:1024\n" \
"v_or_b32_e32 v19, 0x27800, v212\n" \
"v_mfma_f32_16x16x32_bf16 a[140:143], v[132:135], v[160:163], a[140:143]\n" \
"ds_read_b128 v[200:203], v199 offset:0\n" \
"v_mov_b32_e32 v220, 0x1000c0c\n" \
"v_mfma_f32_16x16x32_bf16 a[172:175], v[136:139], v[160:163], a[172:175]\n" \
"ds_read_b64_tr_b16 v[108:109], v38 offset:0\n" \
"s_lshr_b32 s4, s2, 5\n" \
"v_mfma_f32_16x16x32_bf16 a[144:147], v[132:135], v[164:167], a[144:147]\n" \
"ds_read_b64_tr_b16 v[110:111], v38 offset:1024\n" \
"s_and_b32 s5, s3, 1\n" \
"v_mfma_f32_16x16x32_bf16 a[176:179], v[136:139], v[164:167], a[176:179]\n" \
"ds_read_b128 v[204:207], v199 offset:4096\n" \
"s_xor_b32 s6, s4, 15\n" \
"s_nop 0\n" \
"v_mfma_f32_16x16x32_bf16 a[148:151], v[132:135], v[168:171], a[148:151]\n" \
"ds_read_b64_tr_b16 v[112:113], v39 offset:0\n" \
"v_mfma_f32_16x16x32_bf16 a[180:183], v[136:139], v[168:171], a[180:183]\n" \
"ds_read_b64_tr_b16 v[114:115], v39 offset:1024\n" \
"v_mfma_f32_16x16x32_bf16 a[152:155], v[132:135], v[172:175], a[152:155]\n" \
"ds_read_b128 v[208:211], v199 offset:8192\n" \
"v_mfma_f32_16x16x32_bf16 a[184:187], v[136:139], v[172:175], a[184:187]\n" \
"ds_read_b128 v[60:63], v28 offset:20480\n" \
"v_mfma_f32_16x16x32_bf16 a[156:159], v[132:135], v[176:179], a[156:159]\n" \
"ds_read_b128 v[64:67], v28 offset:22528\n" \
"v_mfma_f32_16x16x32_bf16 a[188:191], v[136:139], v[176:179], a[188:191]\n" \
"ds_read_b128 v[68:71], v28 offset:0\n" \
"v_mfma_f32_16x16x32_bf16 a[220:223], v[140:143], v[176:179], a[220:223]\n" \
"ds_read_b128 v[72:75], v28 offset:2048\n" \
"v_mfma_f32_16x16x32_bf16 a[252:255], v[144:147], v[176:179], a[252:255]\n" \
"ds_read_b128 v[76:79], v28 offset:4096\n" \
"v_mfma_f32_16x16x32_bf16 a[216:219], v[140:143], v[172:175], a[216:219]\n" \
"ds_read_b128 v[80:83], v28 offset:6144\n" \
"v_mfma_f32_16x16x32_bf16 a[248:251], v[144:147], v[172:175], a[248:251]\n" \
"s_cmpk_lt_u32 s3, 2\n" \
"s_cselect_b32 s7, s6, s4\n" \
"v_mfma_f32_16x16x32_bf16 a[212:215], v[140:143], v[168:171], a[212:215]\n" \
"s_lshl_b32 s40, s7, 7\n" \
"s_lshl_b32 s41, s5, 6\n" \
"v_mfma_f32_16x16x32_bf16 a[244:247], v[144:147], v[168:171], a[244:247]\n" \
"v_bfe_u32 v219, v20, 4, 1\n" \
"v_lshrrev_b32_e64 v212, 7, v20\n" \
"v_mfma_f32_16x16x32_bf16 a[208:211], v[140:143], v[164:167], a[208:211]\n" \
"v_lshrrev_b32_e32 v213, 8, v20\n" \
"s_or_b32 s41, s41, s40\n" \
"v_mfma_f32_16x16x32_bf16 a[240:243], v[144:147], v[164:167], a[240:243]\n" \
"v_mov_b32_e32 v221, 0xc0c0100\n" \
"v_cmp_eq_u32_e64 vcc, 0, v219\n" \
"v_mfma_f32_16x16x32_bf16 a[204:207], v[140:143], v[160:163], a[204:207]\n" \
"v_or_b32_e32 v214, s41, v212\n" \
"s_lshl_b32 s41, s2, 2\n" \
"v_mfma_f32_16x16x32_bf16 a[236:239], v[144:147], v[160:163], a[236:239]\n" \
"v_bfe_u32 v217, v20, 4, 4\n" \
"v_cndmask_b32_e64 v220, v220, v221, vcc\n" \
"v_mfma_f32_16x16x32_bf16 a[200:203], v[140:143], v[156:159], a[200:203]\n" \
"v_mov_b32_e32 v221, 0x3020c0c\n" \
"v_mov_b32_e32 v222, 0xc0c0302\n" \
"v_mfma_f32_16x16x32_bf16 a[232:235], v[144:147], v[156:159], a[232:235]\n" \
"v_lshl_or_b32 v219, v213, 2, v219\n" \
"s_and_b32 s43, s41, 28\n" \
"v_mfma_f32_16x16x32_bf16 a[196:199], v[140:143], v[152:155], a[196:199]\n" \
"s_add_i32 s41, s40, -16\n" \
"v_cndmask_b32_e64 v221, v221, v222, vcc\n" \
"v_mfma_f32_16x16x32_bf16 a[228:231], v[144:147], v[152:155], a[228:231]\n" \
"v_mov_b32_e32 v222, 0xc0c0c0c\n" \
"v_cmp_gt_u32_e64 vcc, v219, v217\n" \
"v_mfma_f32_16x16x32_bf16 a[192:195], v[140:143], v[148:151], a[192:195]\n" \
"v_or_b32_e32 v219, 2, v219\n" \
"s_mov_b32 s42, 0\n" \
"v_mfma_f32_16x16x32_bf16 a[224:227], v[144:147], v[148:151], a[224:227]\n" \
"s_waitcnt lgkmcnt(15)\n" \
"s_nop 0\n" \
"v_mfma_f32_16x16x32_bf16 a[0:3], v[52:55], v[84:87], a[0:3]\n" \
"s_add_u32 m0, s30, 0x9800\n" \
"s_mov_b32 s85, 0xc280\n" \
"v_mfma_f32_16x16x32_bf16 a[32:35], v[56:59], v[84:87], a[32:35]\n" \
"buffer_load_dwordx4 v22, s[64:67], s85 offen lds\n" \
"v_mfma_f32_16x16x32_bf16 a[4:7], v[52:55], v[88:91], a[4:7]\n" \
"ds_read_b64_tr_b16 v[148:149], v32 offset:8192\n" \
"v_mfma_f32_16x16x32_bf16 a[36:39], v[56:59], v[88:91], a[36:39]\n" \
"s_add_u32 m0, s30, 0x9c00\n" \
"s_mov_b32 s84, 0xe280\n" \
"v_mfma_f32_16x16x32_bf16 a[8:11], v[52:55], v[92:95], a[8:11]\n" \
"buffer_load_dwordx4 v23, s[64:67], s84 offen lds\n" \
"v_mfma_f32_16x16x32_bf16 a[40:43], v[56:59], v[92:95], a[40:43]\n" \
"ds_read_b64_tr_b16 v[150:151], v32 offset:9216\n" \
"v_mfma_f32_16x16x32_bf16 a[12:15], v[52:55], v[96:99], a[12:15]\n" \
"s_add_u32 m0, s31, 0x8c00\n" \
"s_mov_b32 s85, 0x298000\n" \
"v_mfma_f32_16x16x32_bf16 a[44:47], v[56:59], v[96:99], a[44:47]\n" \
"buffer_load_dwordx4 v27, s[68:71], s85 offen lds\n" \
"v_mfma_f32_16x16x32_bf16 a[16:19], v[52:55], v[100:103], a[16:19]\n" \
"ds_read_b128 v[116:119], v29 offset:16384\n" \
"v_mfma_f32_16x16x32_bf16 a[48:51], v[56:59], v[100:103], a[48:51]\n" \
"s_add_u32 m0, s31, 0xcc00\n" \
"s_mov_b32 s84, 0x298000\n" \
"s_waitcnt lgkmcnt(15)\n" \
"v_mfma_f32_16x16x32_bf16 a[20:23], v[52:55], v[104:107], a[20:23]\n" \
"buffer_load_dwordx4 v27, s[72:75], s84 offen lds\n" \
"v_mfma_f32_16x16x32_bf16 a[52:55], v[56:59], v[104:107], a[52:55]\n" \
"ds_read_b128 v[120:123], v29 offset:18432\n" \
"s_waitcnt lgkmcnt(14)\n" \
"v_mfma_f32_16x16x32_bf16 a[24:27], v[52:55], v[108:111], a[24:27]\n" \
"ds_read_b64_tr_b16 v[152:153], v33 offset:8192\n" \
"v_mfma_f32_16x16x32_bf16 a[56:59], v[56:59], v[108:111], a[56:59]\n" \
"ds_read_b64_tr_b16 v[154:155], v33 offset:9216\n" \
"s_waitcnt lgkmcnt(13)\n" \
"v_mfma_f32_16x16x32_bf16 a[28:31], v[52:55], v[112:115], a[28:31]\n" \
"ds_read_b64_tr_b16 v[156:157], v34 offset:8192\n" \
"v_mfma_f32_16x16x32_bf16 a[60:63], v[56:59], v[112:115], a[60:63]\n" \
"ds_read_b64_tr_b16 v[158:159], v34 offset:9216\n" \
"s_waitcnt lgkmcnt(13)\n" \
"v_mfma_f32_16x16x32_bf16 a[92:95], v[60:63], v[112:115], a[92:95]\n" \
"s_cmp_lg_u32 s40, 0\n" \
"s_cselect_b32 s41, s41, s42\n" \
"s_waitcnt lgkmcnt(12)\n" \
"v_mfma_f32_16x16x32_bf16 a[124:127], v[64:67], v[112:115], a[124:127]\n" \
"ds_read_b64_tr_b16 v[160:161], v35 offset:8192\n" \
"v_lshl_or_b32 v218, s5, 3, v212\n" \
"v_mfma_f32_16x16x32_bf16 a[88:91], v[60:63], v[108:111], a[88:91]\n" \
"v_cndmask_b32_e32 v40, v222, v220, vcc\n" \
"v_cndmask_b32_e64 v42, v220, v222, vcc\n" \
"v_mfma_f32_16x16x32_bf16 a[120:123], v[64:67], v[108:111], a[120:123]\n" \
"ds_read_b64_tr_b16 v[162:163], v35 offset:9216\n" \
"v_cmp_gt_u32_e64 vcc, v219, v217\n" \
"v_mfma_f32_16x16x32_bf16 a[84:87], v[60:63], v[104:107], a[84:87]\n" \
"s_mov_b32 m0, s32\n" \
"v_and_b32_e32 v219, 16, v20\n" \
"v_mfma_f32_16x16x32_bf16 a[116:119], v[64:67], v[104:107], a[116:119]\n" \
"s_mov_b32 exec_lo, 0x0\n" \
"s_mov_b32 exec_hi, 0xffffffff\n" \
"global_load_lds_dword v[188:189], off\n" \
"s_mov_b64 exec, -1\n" \
"v_mfma_f32_16x16x32_bf16 a[80:83], v[60:63], v[100:103], a[80:83]\n" \
"ds_read_b64_tr_b16 v[164:165], v36 offset:8192\n" \
"s_movk_i32 s44, 0x3f80\n" \
"v_mfma_f32_16x16x32_bf16 a[112:115], v[64:67], v[100:103], a[112:115]\n" \
"ds_read_b64_tr_b16 v[166:167], v36 offset:9216\n" \
"v_add_u32_e64 v218, s41, v218\n" \
"v_mfma_f32_16x16x32_bf16 a[76:79], v[60:63], v[96:99], a[76:79]\n" \
"v_and_b32_e32 v217, 0x320, v20\n" \
"s_movk_i32 s41, 0x100\n" \
"v_mfma_f32_16x16x32_bf16 a[108:111], v[64:67], v[96:99], a[108:111]\n" \
"v_lshlrev_b32_e64 v219, v219, s44\n" \
"v_mov_b32_e32 v220, 0\n" \
"v_mfma_f32_16x16x32_bf16 a[72:75], v[60:63], v[92:95], a[72:75]\n" \
"ds_read_b64_tr_b16 v[168:169], v37 offset:8192\n" \
"v_lshrrev_b32_e64 v215, 6, v20\n" \
"v_mfma_f32_16x16x32_bf16 a[104:107], v[64:67], v[92:95], a[104:107]\n" \
"buffer_store_dwordx4 v[200:203], v20, s[80:83], s35 offen sc1\n" \
"v_mfma_f32_16x16x32_bf16 a[68:71], v[60:63], v[88:91], a[68:71]\n" \
"ds_read_b64_tr_b16 v[170:171], v37 offset:9216\n" \
"v_cndmask_b32_e64 v41, v222, v221, vcc\n" \
"v_mfma_f32_16x16x32_bf16 a[100:103], v[64:67], v[88:91], a[100:103]\n" \
"v_cndmask_b32_e32 v43, v221, v222, vcc\n" \
"v_cmp_eq_u32_e32 vcc, s41, v217\n" \
"v_mfma_f32_16x16x32_bf16 a[64:67], v[60:63], v[84:87], a[64:67]\n" \
"ds_read_b64_tr_b16 v[172:173], v38 offset:8192\n" \
"v_and_b32_e32 v217, 0x330, v20\n" \
"v_mfma_f32_16x16x32_bf16 a[96:99], v[64:67], v[84:87], a[96:99]\n" \
"v_bfe_u32 v216, v20, 6, 2\n" \
"v_cndmask_b32_e32 v46, v220, v219, vcc\n" \
"s_waitcnt lgkmcnt(15)\n" \
"v_mfma_f32_16x16x32_bf16 a[128:131], v[68:71], v[84:87], a[128:131]\n" \
"ds_read_b64_tr_b16 v[174:175], v38 offset:9216\n" \
"v_mov_b32_e32 v219, 0x3f80\n" \
"v_mfma_f32_16x16x32_bf16 a[160:163], v[72:75], v[84:87], a[160:163]\n" \
"v_cmp_eq_u32_e32 vcc, 32, v217\n" \
"v_lshrrev_b32_e32 v220, 1, v20\n" \
"v_mfma_f32_16x16x32_bf16 a[132:135], v[68:71], v[88:91], a[132:135]\n" \
"buffer_store_dwordx4 v[204:207], v20, s[80:83], s88 offen sc1\n" \
"v_mfma_f32_16x16x32_bf16 a[164:167], v[72:75], v[88:91], a[164:167]\n" \
"ds_read_b64_tr_b16 v[176:177], v39 offset:8192\n" \
"v_bfe_u32 v215, v215, 1, 1\n" \
"v_mfma_f32_16x16x32_bf16 a[136:139], v[68:71], v[92:95], a[136:139]\n" \
"ds_read_b64_tr_b16 v[178:179], v39 offset:9216\n" \
"v_cndmask_b32_e64 v47, 0, v219, vcc\n" \
"v_mfma_f32_16x16x32_bf16 a[168:171], v[72:75], v[92:95], a[168:171]\n" \
"s_lshl_b32 s41, s5, 2\n" \
"v_lshlrev_b32_e32 v219, 10, v213\n" \
"v_mfma_f32_16x16x32_bf16 a[140:143], v[68:71], v[96:99], a[140:143]\n" \
"v_lshlrev_b32_e32 v216, 7, v216\n" \
"v_and_b32_e32 v220, 24, v220\n" \
"v_mfma_f32_16x16x32_bf16 a[172:175], v[72:75], v[96:99], a[172:175]\n" \
"ds_read_b128 v[124:127], v29 offset:20480\n" \
"v_and_or_b32 v212, v212, 2, v215\n" \
"v_mfma_f32_16x16x32_bf16 a[144:147], v[68:71], v[100:103], a[144:147]\n" \
"s_lshl_b32 s7, s7, 3\n" \
"v_or_b32_e32 v217, s41, v213\n" \
"v_mfma_f32_16x16x32_bf16 a[176:179], v[72:75], v[100:103], a[176:179]\n" \
"buffer_store_dwordx4 v[208:211], v20, s[80:83], s36 offen sc1\n" \
"v_mfma_f32_16x16x32_bf16 a[148:151], v[68:71], v[104:107], a[148:151]\n" \
"ds_read_b128 v[128:131], v29 offset:22528\n" \
"v_or3_b32 v216, v216, v219, v220\n" \
"v_mfma_f32_16x16x32_bf16 a[180:183], v[72:75], v[104:107], a[180:183]\n" \
"ds_read_b128 v[132:135], v29 offset:0\n" \
"v_lshlrev_b32_e64 v212, 5, v212\n" \
"v_mfma_f32_16x16x32_bf16 a[152:155], v[68:71], v[108:111], a[152:155]\n" \
"s_mov_b32 s44, 0x14000\n" \
"s_or_b32 s7, s7, s41\n" \
"v_mfma_f32_16x16x32_bf16 a[184:187], v[72:75], v[108:111], a[184:187]\n" \
"ds_read_b128 v[136:139], v29 offset:2048\n" \
"s_mov_b32 s41, 0xff800000\n" \
"v_mfma_f32_16x16x32_bf16 a[156:159], v[68:71], v[112:115], a[156:159]\n" \
"s_cmp_lg_u32 s40, 0\n" \
"s_cselect_b32 s47, s42, s41\n" \
"v_mfma_f32_16x16x32_bf16 a[188:191], v[72:75], v[112:115], a[188:191]\n" \
"ds_read_b128 v[140:143], v29 offset:4096\n" \
"s_mov_b32 s40, 0xc000\n" \
"v_mfma_f32_16x16x32_bf16 a[220:223], v[76:79], v[112:115], a[220:223]\n" \
"s_bfe_u32 s45, s2, 0x20003\n" \
"v_or3_b32 v190, v216, v212, s44\n" \
"v_mfma_f32_16x16x32_bf16 a[252:255], v[80:83], v[112:115], a[252:255]\n" \
"ds_read_b128 v[144:147], v29 offset:6144\n" \
"s_mov_b32 s41, 0x8000\n" \
"v_mfma_f32_16x16x32_bf16 a[216:219], v[76:79], v[108:111], a[216:219]\n" \
"s_cmp_lg_u32 s4, 0\n" \
"s_cselect_b32 s51, s40, s41\n" \
"v_mfma_f32_16x16x32_bf16 a[248:251], v[80:83], v[108:111], a[248:251]\n" \
"s_movk_i32 s40, 0x4000\n" \
"s_lshr_b32 s44, s3, 1\n" \
"v_mfma_f32_16x16x32_bf16 a[212:215], v[76:79], v[104:107], a[212:215]\n" \
"s_or_b32 s46, s43, s45\n" \
"v_mfma_f32_16x16x32_bf16 a[244:247], v[80:83], v[104:107], a[244:247]\n" \
"s_cmp_lg_u32 s4, 0\n" \
"s_cselect_b32 s53, s41, s40\n" \
"v_mfma_f32_16x16x32_bf16 a[208:211], v[76:79], v[100:103], a[208:211]\n" \
"s_lshl_b32 s40, s44, 15\n" \
"s_lshl_b32 s41, s5, 14\n" \
"v_mfma_f32_16x16x32_bf16 a[240:243], v[80:83], v[100:103], a[240:243]\n" \
"s_lshl_b32 s48, s46, 20\n" \
"s_or_b32 s41, s40, s41\n" \
"v_mfma_f32_16x16x32_bf16 a[204:207], v[76:79], v[96:99], a[204:207]\n" \
"s_add_i32 s49, s48, 0x1000000\n" \
"s_or_b32 s42, s49, s41\n" \
"v_mfma_f32_16x16x32_bf16 a[236:239], v[80:83], v[96:99], a[236:239]\n" \
"s_lshl_b32 s4, s4, 16\n" \
"s_or_b32 s41, s42, s4\n" \
"v_mfma_f32_16x16x32_bf16 a[200:203], v[76:79], v[92:95], a[200:203]\n" \
"s_lshl_b32 s4, s6, 16\n" \
"s_or_b32 s42, s42, s4\n" \
"v_mfma_f32_16x16x32_bf16 a[232:235], v[80:83], v[92:95], a[232:235]\n" \
"s_lshl_b32 s4, s5, 11\n" \
"s_mul_i32 s5, s7, 0x24800\n" \
"v_mfma_f32_16x16x32_bf16 a[196:199], v[76:79], v[88:91], a[196:199]\n" \
"s_lshl_b32 s6, s46, 9\n" \
"s_lshl_b32 s7, s43, 6\n" \
"v_mfma_f32_16x16x32_bf16 a[228:231], v[80:83], v[88:91], a[228:231]\n" \
"s_add_i32 s6, s5, s6\n" \
"s_or_b32 s5, s5, s7\n" \
"v_mfma_f32_16x16x32_bf16 a[192:195], v[76:79], v[84:87], a[192:195]\n" \
"s_lshl_b32 s7, s45, 2\n" \
"s_or_b32 s5, s5, s7\n" \
"v_mfma_f32_16x16x32_bf16 a[224:227], v[80:83], v[84:87], a[224:227]\n" \
"s_waitcnt vmcnt(4) lgkmcnt(0)\n" \
"s_barrier\n" \
"v_mfma_f32_16x16x32_bf16 a[0:3], v[116:119], v[148:151], a[0:3]\n" \
"s_add_u32 m0, s30, 0x0\n" \
"s_mov_b32 s85, 0x300\n" \
"v_mfma_f32_16x16x32_bf16 a[32:35], v[120:123], v[148:151], a[32:35]\n" \
"buffer_load_dwordx4 v22, s[64:67], s85 offen lds\n" \
"v_mfma_f32_16x16x32_bf16 a[4:7], v[116:119], v[152:155], a[4:7]\n" \
"ds_read_b64_tr_b16 v[84:85], v32 offset:32768\n" \
"s_lshl_b32 s7, s3, 12\n" \
"v_mfma_f32_16x16x32_bf16 a[36:39], v[120:123], v[152:155], a[36:39]\n" \
"ds_read_b64_tr_b16 v[86:87], v32 offset:33792\n" \
"s_or_b32 s50, s49, s7\n" \
"v_mfma_f32_16x16x32_bf16 a[8:11], v[116:119], v[156:159], a[8:11]\n" \
"s_add_u32 m0, s30, 0x400\n" \
"s_mov_b32 s84, 0x2300\n" \
"v_mfma_f32_16x16x32_bf16 a[40:43], v[120:123], v[156:159], a[40:43]\n" \
"buffer_load_dwordx4 v23, s[64:67], s84 offen lds\n" \
"v_mfma_f32_16x16x32_bf16 a[12:15], v[116:119], v[160:163], a[12:15]\n" \
"ds_read_b128 v[52:55], v28 offset:49152\n" \
"s_lshl_b32 s7, s3, 11\n" \
"v_mfma_f32_16x16x32_bf16 a[44:47], v[120:123], v[160:163], a[44:47]\n" \
"ds_read_b128 v[56:59], v28 offset:51200\n" \
"v_lshrrev_b32_e64 v212, 4, v20\n" \
"v_mfma_f32_16x16x32_bf16 a[16:19], v[116:119], v[164:167], a[16:19]\n" \
"s_add_u32 m0, s31, 0x0\n" \
"s_mov_b32 s85, 0x300000\n" \
"v_mfma_f32_16x16x32_bf16 a[48:51], v[120:123], v[164:167], a[48:51]\n" \
"buffer_load_dwordx4 v26, s[68:71], s85 offen lds\n" \
"v_mfma_f32_16x16x32_bf16 a[20:23], v[116:119], v[168:171], a[20:23]\n" \
"ds_read_b64_tr_b16 v[88:89], v33 offset:32768\n" \
"v_bfe_u32 v215, v20, 4, 3\n" \
"v_mfma_f32_16x16x32_bf16 a[52:55], v[120:123], v[168:171], a[52:55]\n" \
"ds_read_b64_tr_b16 v[90:91], v33 offset:33792\n" \
"s_add_i32 s52, s48, s7\n" \
"v_mfma_f32_16x16x32_bf16 a[24:27], v[116:119], v[172:175], a[24:27]\n" \
"s_add_u32 m0, s31, 0x4000\n" \
"s_mov_b32 s84, 0x300000\n" \
"v_mfma_f32_16x16x32_bf16 a[56:59], v[120:123], v[172:175], a[56:59]\n" \
"buffer_load_dwordx4 v26, s[72:75], s84 offen lds\n" \
"v_mfma_f32_16x16x32_bf16 a[28:31], v[116:119], v[176:179], a[28:31]\n" \
"ds_read_b64_tr_b16 v[92:93], v34 offset:32768\n" \
"s_add_i32 s53, s53, s7\n" \
"s_nop 0\n" \
"v_mfma_f32_16x16x32_bf16 a[60:63], v[120:123], v[176:179], a[60:63]\n" \
"ds_read_b64_tr_b16 v[94:95], v34 offset:33792\n" \
"v_mul_u32_u24_e32 v214, 0x480, v214\n" \
"v_mfma_f32_16x16x32_bf16 a[92:95], v[124:127], v[176:179], a[92:95]\n" \
"s_add_u32 m0, s30, 0x800\n" \
"s_mov_b32 s85, 0x4300\n" \
"v_mfma_f32_16x16x32_bf16 a[124:127], v[128:131], v[176:179], a[124:127]\n" \
"buffer_load_dwordx4 v22, s[64:67], s85 offen lds\n" \
"v_mfma_f32_16x16x32_bf16 a[88:91], v[124:127], v[172:175], a[88:91]\n" \
"ds_read_b64_tr_b16 v[96:97], v35 offset:32768\n" \
"v_bitop3_b32 v216, v213, v212, 7 bitop3:0x78\n" \
"v_mfma_f32_16x16x32_bf16 a[120:123], v[128:131], v[172:175], a[120:123]\n" \
"ds_read_b64_tr_b16 v[98:99], v35 offset:33792\n" \
"v_bitop3_b32 v213, v213, v215, 4 bitop3:0x36\n" \
"v_mfma_f32_16x16x32_bf16 a[84:87], v[124:127], v[168:171], a[84:87]\n" \
"s_add_u32 m0, s30, 0xc00\n" \
"s_mov_b32 s84, 0x6300\n" \
"v_mfma_f32_16x16x32_bf16 a[116:119], v[128:131], v[168:171], a[116:119]\n" \
"buffer_load_dwordx4 v23, s[64:67], s84 offen lds\n" \
"v_mfma_f32_16x16x32_bf16 a[80:83], v[124:127], v[164:167], a[80:83]\n" \
"ds_read_b64_tr_b16 v[100:101], v36 offset:32768\n" \
"s_lshl_b32 s7, s3, 10\n" \
"v_mfma_f32_16x16x32_bf16 a[112:115], v[128:131], v[164:167], a[112:115]\n" \
"ds_read_b64_tr_b16 v[102:103], v36 offset:33792\n" \
"s_or_b32 s4, s40, s4\n" \
"v_mfma_f32_16x16x32_bf16 a[76:79], v[124:127], v[160:163], a[76:79]\n" \
"s_add_u32 m0, s31, 0x400\n" \
"s_mov_b32 s85, 0x308000\n" \
"v_mfma_f32_16x16x32_bf16 a[108:111], v[128:131], v[160:163], a[108:111]\n" \
"buffer_load_dwordx4 v26, s[68:71], s85 offen lds\n" \
"v_mfma_f32_16x16x32_bf16 a[72:75], v[124:127], v[156:159], a[72:75]\n" \
"ds_read_b64_tr_b16 v[104:105], v37 offset:32768\n" \
"v_lshl_or_b32 v25, v213, 4, v214\n" \
"v_mfma_f32_16x16x32_bf16 a[104:107], v[128:131], v[156:159], a[104:107]\n" \
"v_mul_u32_u24_e32 v213, 0x480, v218\n" \
"v_bitop3_b32 v212, v212, v217, 7 bitop3:0x6c\n" \
"v_mfma_f32_16x16x32_bf16 a[68:71], v[124:127], v[152:155], a[68:71]\n" \
"s_add_u32 m0, s31, 0x4400\n" \
"s_mov_b32 s84, 0x308000\n" \
"v_mfma_f32_16x16x32_bf16 a[100:103], v[128:131], v[152:155], a[100:103]\n" \
"buffer_load_dwordx4 v26, s[72:75], s84 offen lds\n" \
"v_mfma_f32_16x16x32_bf16 a[64:67], v[124:127], v[148:151], a[64:67]\n" \
"ds_read_b64_tr_b16 v[106:107], v37 offset:33792\n" \
"s_or_b32 s40, s7, 0x14000\n" \
"v_mfma_f32_16x16x32_bf16 a[96:99], v[128:131], v[148:151], a[96:99]\n" \
"ds_read_b64_tr_b16 v[108:109], v38 offset:32768\n" \
"s_lshl_b32 s43, s44, 13\n" \
"v_mfma_f32_16x16x32_bf16 a[128:131], v[132:135], v[148:151], a[128:131]\n" \
"s_add_u32 m0, s30, 0x1000\n" \
"s_mov_b32 s85, 0x8300\n" \
"v_mfma_f32_16x16x32_bf16 a[160:163], v[136:139], v[148:151], a[160:163]\n" \
"buffer_load_dwordx4 v22, s[64:67], s85 offen lds\n" \
"v_mfma_f32_16x16x32_bf16 a[132:135], v[132:135], v[152:155], a[132:135]\n" \
"ds_read_b64_tr_b16 v[110:111], v38 offset:33792\n" \
"s_mulk_i32 s44, 0xe800\n" \
"v_mfma_f32_16x16x32_bf16 a[164:167], v[136:139], v[152:155], a[164:167]\n" \
"ds_read_b64_tr_b16 v[112:113], v39 offset:32768\n" \
"s_lshl_b32 s7, s3, 8\n" \
"s_nop 0\n" \
"v_mfma_f32_16x16x32_bf16 a[136:139], v[132:135], v[156:159], a[136:139]\n" \
"s_add_u32 m0, s30, 0x1400\n" \
"s_mov_b32 s84, 0xa300\n" \
"v_mfma_f32_16x16x32_bf16 a[168:171], v[136:139], v[156:159], a[168:171]\n" \
"buffer_load_dwordx4 v23, s[64:67], s84 offen lds\n" \
"v_mfma_f32_16x16x32_bf16 a[140:143], v[132:135], v[160:163], a[140:143]\n" \
"ds_read_b64_tr_b16 v[114:115], v39 offset:33792\n" \
"v_lshl_or_b32 v24, v216, 4, v214\n" \
"v_mfma_f32_16x16x32_bf16 a[172:175], v[136:139], v[160:163], a[172:175]\n" \
"ds_read_b128 v[60:63], v28 offset:53248\n" \
"v_lshl_or_b32 v21, v212, 4, v213\n" \
"v_mfma_f32_16x16x32_bf16 a[144:147], v[132:135], v[164:167], a[144:147]\n" \
"s_add_u32 m0, s31, 0x800\n" \
"s_mov_b32 s85, 0x310000\n" \
"v_mfma_f32_16x16x32_bf16 a[176:179], v[136:139], v[164:167], a[176:179]\n" \
"buffer_load_dwordx4 v27, s[68:71], s85 offen lds\n" \
"v_mfma_f32_16x16x32_bf16 a[148:151], v[132:135], v[168:171], a[148:151]\n" \
"ds_read_b128 v[64:67], v28 offset:55296\n" \
"s_add_i32 s44, s44, 0x17000\n" \
"v_mfma_f32_16x16x32_bf16 a[180:183], v[136:139], v[168:171], a[180:183]\n" \
"ds_read_b128 v[68:71], v28 offset:32768\n" \
"s_or_b32 s45, s7, 0x27000\n" \
"v_mfma_f32_16x16x32_bf16 a[152:155], v[132:135], v[172:175], a[152:155]\n" \
"s_add_u32 m0, s31, 0x4800\n" \
"s_mov_b32 s84, 0x310000\n" \
"v_mfma_f32_16x16x32_bf16 a[184:187], v[136:139], v[172:175], a[184:187]\n" \
"buffer_load_dwordx4 v27, s[72:75], s84 offen lds\n" \
"v_mfma_f32_16x16x32_bf16 a[156:159], v[132:135], v[176:179], a[156:159]\n" \
"ds_read_b128 v[72:75], v28 offset:34816\n" \
"s_or_b32 s46, s4, 0x10000\n" \
"v_mfma_f32_16x16x32_bf16 a[188:191], v[136:139], v[176:179], a[188:191]\n" \
"ds_read_b128 v[76:79], v28 offset:36864\n" \
"s_add_i32 s48, s6, 0x3000000\n" \
"v_mfma_f32_16x16x32_bf16 a[220:223], v[140:143], v[176:179], a[220:223]\n" \
"s_add_i32 s49, s5, 0x3004000\n" \
"s_add_i32 s52, s52, 0x1010000\n" \
"v_mfma_f32_16x16x32_bf16 a[252:255], v[144:147], v[176:179], a[252:255]\n" \
"ds_read_b128 v[80:83], v28 offset:38912\n" \
"v_mfma_f32_16x16x32_bf16 a[216:219], v[140:143], v[172:175], a[216:219]\n" \
"v_mfma_f32_16x16x32_bf16 a[248:251], v[144:147], v[172:175], a[248:251]\n" \
"v_mfma_f32_16x16x32_bf16 a[212:215], v[140:143], v[168:171], a[212:215]\n" \
"v_mfma_f32_16x16x32_bf16 a[244:247], v[144:147], v[168:171], a[244:247]\n" \
"v_mfma_f32_16x16x32_bf16 a[208:211], v[140:143], v[164:167], a[208:211]\n" \
"v_mfma_f32_16x16x32_bf16 a[240:243], v[144:147], v[164:167], a[240:243]\n" \
"v_mfma_f32_16x16x32_bf16 a[204:207], v[140:143], v[160:163], a[204:207]\n" \
"v_mfma_f32_16x16x32_bf16 a[236:239], v[144:147], v[160:163], a[236:239]\n" \
"v_mfma_f32_16x16x32_bf16 a[200:203], v[140:143], v[156:159], a[200:203]\n" \
"v_mfma_f32_16x16x32_bf16 a[232:235], v[144:147], v[156:159], a[232:235]\n" \
"v_mfma_f32_16x16x32_bf16 a[196:199], v[140:143], v[152:155], a[196:199]\n" \
"v_mfma_f32_16x16x32_bf16 a[228:231], v[144:147], v[152:155], a[228:231]\n" \
"v_mfma_f32_16x16x32_bf16 a[192:195], v[140:143], v[148:151], a[192:195]\n" \
"v_mfma_f32_16x16x32_bf16 a[224:227], v[144:147], v[148:151], a[224:227]\n" \
"s_waitcnt lgkmcnt(15)\n" \
"s_nop 0\n" \
"v_mfma_f32_16x16x32_bf16 a[0:3], v[52:55], v[84:87], a[0:3]\n" \
"s_add_u32 m0, s30, 0x1800\n" \
"s_mov_b32 s85, 0xc300\n" \
"v_mfma_f32_16x16x32_bf16 a[32:35], v[56:59], v[84:87], a[32:35]\n" \
"buffer_load_dwordx4 v22, s[64:67], s85 offen lds\n" \
"v_mfma_f32_16x16x32_bf16 a[4:7], v[52:55], v[88:91], a[4:7]\n" \
"ds_read_b64_tr_b16 v[148:149], v32 offset:40960\n" \
"v_mfma_f32_16x16x32_bf16 a[36:39], v[56:59], v[88:91], a[36:39]\n" \
"s_add_u32 m0, s30, 0x1c00\n" \
"s_mov_b32 s84, 0xe300\n" \
"v_mfma_f32_16x16x32_bf16 a[8:11], v[52:55], v[92:95], a[8:11]\n" \
"buffer_load_dwordx4 v23, s[64:67], s84 offen lds\n" \
"v_mfma_f32_16x16x32_bf16 a[40:43], v[56:59], v[92:95], a[40:43]\n" \
"ds_read_b64_tr_b16 v[150:151], v32 offset:41984\n" \
"s_waitcnt lgkmcnt(15)\n" \
"v_mfma_f32_16x16x32_bf16 a[12:15], v[52:55], v[96:99], a[12:15]\n" \
"s_add_u32 m0, s31, 0xc00\n" \
"s_mov_b32 s85, 0x318000\n" \
"v_mfma_f32_16x16x32_bf16 a[44:47], v[56:59], v[96:99], a[44:47]\n" \
"buffer_load_dwordx4 v27, s[68:71], s85 offen lds\n" \
"s_waitcnt lgkmcnt(14)\n" \
"v_mfma_f32_16x16x32_bf16 a[16:19], v[52:55], v[100:103], a[16:19]\n" \
"ds_read_b128 v[116:119], v29 offset:49152\n" \
"v_mfma_f32_16x16x32_bf16 a[48:51], v[56:59], v[100:103], a[48:51]\n" \
"s_add_u32 m0, s31, 0x4c00\n" \
"s_mov_b32 s84, 0x318000\n" \
"s_waitcnt lgkmcnt(13)\n" \
"v_mfma_f32_16x16x32_bf16 a[20:23], v[52:55], v[104:107], a[20:23]\n" \
"buffer_load_dwordx4 v27, s[72:75], s84 offen lds\n" \
"v_mfma_f32_16x16x32_bf16 a[52:55], v[56:59], v[104:107], a[52:55]\n" \
"ds_read_b128 v[120:123], v29 offset:51200\n" \
"s_waitcnt lgkmcnt(12)\n" \
"v_mfma_f32_16x16x32_bf16 a[24:27], v[52:55], v[108:111], a[24:27]\n" \
"ds_read_b64_tr_b16 v[152:153], v33 offset:40960\n" \
"v_mfma_f32_16x16x32_bf16 a[56:59], v[56:59], v[108:111], a[56:59]\n" \
"ds_read_b64_tr_b16 v[154:155], v33 offset:41984\n" \
"s_waitcnt lgkmcnt(12)\n" \
"v_mfma_f32_16x16x32_bf16 a[28:31], v[52:55], v[112:115], a[28:31]\n" \
"ds_read_b64_tr_b16 v[156:157], v34 offset:40960\n" \
"v_mfma_f32_16x16x32_bf16 a[60:63], v[56:59], v[112:115], a[60:63]\n" \
"ds_read_b64_tr_b16 v[158:159], v34 offset:41984\n" \
"s_waitcnt lgkmcnt(13)\n" \
"v_mfma_f32_16x16x32_bf16 a[92:95], v[60:63], v[112:115], a[92:95]\n" \
"s_waitcnt lgkmcnt(12)\n" \
"v_mfma_f32_16x16x32_bf16 a[124:127], v[64:67], v[112:115], a[124:127]\n" \
"ds_read_b64_tr_b16 v[160:161], v35 offset:40960\n" \
"v_mfma_f32_16x16x32_bf16 a[88:91], v[60:63], v[108:111], a[88:91]\n" \
"v_lshlrev_b32_e32 v45, 3, v20\n" \
"v_add_u32_e64 v45, s31, v45\n" \
"v_mfma_f32_16x16x32_bf16 a[120:123], v[64:67], v[108:111], a[120:123]\n" \
"s_getpc_b64 s[86:87]\n" \
"s_sub_u32 s86, s86, 0x10800\n" \
"s_subb_u32 s87, s87, 0\n" \
"s_mov_b32 m0, s32\n" \
"s_nop 0\n" \
"v_mfma_f32_16x16x32_bf16 a[84:87], v[60:63], v[104:107], a[84:87]\n" \
"global_load_lds_dword v45, s[86:87]\n" \
"v_mfma_f32_16x16x32_bf16 a[116:119], v[64:67], v[104:107], a[116:119]\n" \
"ds_read_b64_tr_b16 v[162:163], v35 offset:41984\n" \
"v_mfma_f32_16x16x32_bf16 a[80:83], v[60:63], v[100:103], a[80:83]\n" \
"ds_read_b64_tr_b16 v[164:165], v36 offset:40960\n" \
"v_mfma_f32_16x16x32_bf16 a[112:115], v[64:67], v[100:103], a[112:115]\n" \
"ds_read_b64_tr_b16 v[166:167], v36 offset:41984\n" \
"v_mfma_f32_16x16x32_bf16 a[76:79], v[60:63], v[96:99], a[76:79]\n" \
"v_mfma_f32_16x16x32_bf16 a[108:111], v[64:67], v[96:99], a[108:111]\n" \
"v_mfma_f32_16x16x32_bf16 a[72:75], v[60:63], v[92:95], a[72:75]\n" \
"ds_read_b64_tr_b16 v[168:169], v37 offset:40960\n" \
"v_mfma_f32_16x16x32_bf16 a[104:107], v[64:67], v[92:95], a[104:107]\n" \
"v_mfma_f32_16x16x32_bf16 a[68:71], v[60:63], v[88:91], a[68:71]\n" \
"ds_read_b64_tr_b16 v[170:171], v37 offset:41984\n" \
"v_mfma_f32_16x16x32_bf16 a[100:103], v[64:67], v[88:91], a[100:103]\n" \
"v_mfma_f32_16x16x32_bf16 a[64:67], v[60:63], v[84:87], a[64:67]\n" \
"ds_read_b64_tr_b16 v[172:173], v38 offset:40960\n" \
"v_mfma_f32_16x16x32_bf16 a[96:99], v[64:67], v[84:87], a[96:99]\n" \
"s_waitcnt lgkmcnt(15)\n" \
"s_nop 0\n" \
"v_mfma_f32_16x16x32_bf16 a[128:131], v[68:71], v[84:87], a[128:131]\n" \
"ds_read_b64_tr_b16 v[174:175], v38 offset:41984\n" \
"v_mfma_f32_16x16x32_bf16 a[160:163], v[72:75], v[84:87], a[160:163]\n" \
"v_mfma_f32_16x16x32_bf16 a[132:135], v[68:71], v[88:91], a[132:135]\n" \
"ds_read_b64_tr_b16 v[176:177], v39 offset:40960\n" \
"v_mfma_f32_16x16x32_bf16 a[164:167], v[72:75], v[88:91], a[164:167]\n" \
"v_mfma_f32_16x16x32_bf16 a[136:139], v[68:71], v[92:95], a[136:139]\n" \
"ds_read_b64_tr_b16 v[178:179], v39 offset:41984\n" \
"v_mfma_f32_16x16x32_bf16 a[168:171], v[72:75], v[92:95], a[168:171]\n" \
"v_mfma_f32_16x16x32_bf16 a[140:143], v[68:71], v[96:99], a[140:143]\n" \
"v_mfma_f32_16x16x32_bf16 a[172:175], v[72:75], v[96:99], a[172:175]\n" \
"ds_read_b128 v[124:127], v29 offset:53248\n" \
"v_mfma_f32_16x16x32_bf16 a[144:147], v[68:71], v[100:103], a[144:147]\n" \
"v_mfma_f32_16x16x32_bf16 a[176:179], v[72:75], v[100:103], a[176:179]\n" \
"ds_read_b128 v[128:131], v29 offset:55296\n" \
"v_mfma_f32_16x16x32_bf16 a[148:151], v[68:71], v[104:107], a[148:151]\n" \
"v_mfma_f32_16x16x32_bf16 a[180:183], v[72:75], v[104:107], a[180:183]\n" \
"ds_read_b128 v[132:135], v29 offset:32768\n" \
"v_mfma_f32_16x16x32_bf16 a[152:155], v[68:71], v[108:111], a[152:155]\n" \
"v_mfma_f32_16x16x32_bf16 a[184:187], v[72:75], v[108:111], a[184:187]\n" \
"ds_read_b128 v[136:139], v29 offset:34816\n" \
"v_mfma_f32_16x16x32_bf16 a[156:159], v[68:71], v[112:115], a[156:159]\n" \
"v_mfma_f32_16x16x32_bf16 a[188:191], v[72:75], v[112:115], a[188:191]\n" \
"ds_read_b128 v[140:143], v29 offset:36864\n" \
"v_mfma_f32_16x16x32_bf16 a[220:223], v[76:79], v[112:115], a[220:223]\n" \
"v_mfma_f32_16x16x32_bf16 a[252:255], v[80:83], v[112:115], a[252:255]\n" \
"ds_read_b128 v[144:147], v29 offset:38912\n" \
"v_mfma_f32_16x16x32_bf16 a[216:219], v[76:79], v[108:111], a[216:219]\n" \
"v_mfma_f32_16x16x32_bf16 a[248:251], v[80:83], v[108:111], a[248:251]\n" \
"v_mfma_f32_16x16x32_bf16 a[212:215], v[76:79], v[104:107], a[212:215]\n" \
"v_mfma_f32_16x16x32_bf16 a[244:247], v[80:83], v[104:107], a[244:247]\n" \
"v_mfma_f32_16x16x32_bf16 a[208:211], v[76:79], v[100:103], a[208:211]\n" \
"v_mfma_f32_16x16x32_bf16 a[240:243], v[80:83], v[100:103], a[240:243]\n" \
"v_mfma_f32_16x16x32_bf16 a[204:207], v[76:79], v[96:99], a[204:207]\n" \
"v_mfma_f32_16x16x32_bf16 a[236:239], v[80:83], v[96:99], a[236:239]\n" \
"v_mfma_f32_16x16x32_bf16 a[200:203], v[76:79], v[92:95], a[200:203]\n" \
"v_mfma_f32_16x16x32_bf16 a[232:235], v[80:83], v[92:95], a[232:235]\n" \
"v_mfma_f32_16x16x32_bf16 a[196:199], v[76:79], v[88:91], a[196:199]\n" \
"v_mfma_f32_16x16x32_bf16 a[228:231], v[80:83], v[88:91], a[228:231]\n" \
"v_mfma_f32_16x16x32_bf16 a[192:195], v[76:79], v[84:87], a[192:195]\n" \
"v_mfma_f32_16x16x32_bf16 a[224:227], v[80:83], v[84:87], a[224:227]\n" \
"s_waitcnt vmcnt(1) lgkmcnt(0)\n" \
"s_barrier\n" \
"v_mov_b32_e32 v45, s62\n" \
"s_mov_b64 exec, 1\n" \
"buffer_store_dword v45, off, s[80:83], s37 offset:128 sc1\n" \
"s_mov_b64 exec, -1\n" \
"s_nop 0\n" \
"v_mfma_f32_16x16x32_bf16 a[0:3], v[116:119], v[148:151], a[0:3]\n" \
"s_add_u32 m0, s30, 0x8000\n" \
"s_mov_b32 s85, 0x380\n" \
"v_mfma_f32_16x16x32_bf16 a[32:35], v[120:123], v[148:151], a[32:35]\n" \
"buffer_load_dwordx4 v22, s[64:67], s85 offen lds\n" \
"v_mfma_f32_16x16x32_bf16 a[4:7], v[116:119], v[152:155], a[4:7]\n" \
"ds_read_b64_tr_b16 v[84:85], v32 offset:0\n" \
"v_mfma_f32_16x16x32_bf16 a[36:39], v[120:123], v[152:155], a[36:39]\n" \
"ds_read_b64_tr_b16 v[86:87], v32 offset:1024\n" \
"v_mfma_f32_16x16x32_bf16 a[8:11], v[116:119], v[156:159], a[8:11]\n" \
"s_add_u32 m0, s30, 0x8400\n" \
"s_mov_b32 s84, 0x2380\n" \
"v_mfma_f32_16x16x32_bf16 a[40:43], v[120:123], v[156:159], a[40:43]\n" \
"buffer_load_dwordx4 v23, s[64:67], s84 offen lds\n" \
"v_mfma_f32_16x16x32_bf16 a[12:15], v[116:119], v[160:163], a[12:15]\n" \
"ds_read_b128 v[52:55], v28 offset:16384\n" \
"v_mfma_f32_16x16x32_bf16 a[44:47], v[120:123], v[160:163], a[44:47]\n" \
"ds_read_b128 v[56:59], v28 offset:18432\n" \
"v_mfma_f32_16x16x32_bf16 a[16:19], v[116:119], v[164:167], a[16:19]\n" \
"s_add_u32 m0, s31, 0x8000\n" \
"s_mov_b32 s85, 0x380000\n" \
"v_mfma_f32_16x16x32_bf16 a[48:51], v[120:123], v[164:167], a[48:51]\n" \
"buffer_load_dwordx4 v26, s[68:71], s85 offen lds\n" \
"v_mfma_f32_16x16x32_bf16 a[20:23], v[116:119], v[168:171], a[20:23]\n" \
"ds_read_b64_tr_b16 v[88:89], v33 offset:0\n" \
"v_mfma_f32_16x16x32_bf16 a[52:55], v[120:123], v[168:171], a[52:55]\n" \
"ds_read_b64_tr_b16 v[90:91], v33 offset:1024\n" \
"v_mfma_f32_16x16x32_bf16 a[24:27], v[116:119], v[172:175], a[24:27]\n" \
"s_add_u32 m0, s31, 0xc000\n" \
"s_mov_b32 s84, 0x380000\n" \
"v_mfma_f32_16x16x32_bf16 a[56:59], v[120:123], v[172:175], a[56:59]\n" \
"buffer_load_dwordx4 v26, s[72:75], s84 offen lds\n" \
"v_mfma_f32_16x16x32_bf16 a[28:31], v[116:119], v[176:179], a[28:31]\n" \
"ds_read_b64_tr_b16 v[92:93], v34 offset:0\n" \
"v_mfma_f32_16x16x32_bf16 a[60:63], v[120:123], v[176:179], a[60:63]\n" \
"ds_read_b64_tr_b16 v[94:95], v34 offset:1024\n" \
"v_mfma_f32_16x16x32_bf16 a[92:95], v[124:127], v[176:179], a[92:95]\n" \
"s_add_u32 m0, s30, 0x8800\n" \
"s_mov_b32 s85, 0x4380\n" \
"v_mfma_f32_16x16x32_bf16 a[124:127], v[128:131], v[176:179], a[124:127]\n" \
"buffer_load_dwordx4 v22, s[64:67], s85 offen lds\n" \
"v_mfma_f32_16x16x32_bf16 a[88:91], v[124:127], v[172:175], a[88:91]\n" \
"ds_read_b64_tr_b16 v[96:97], v35 offset:0\n" \
"v_mfma_f32_16x16x32_bf16 a[120:123], v[128:131], v[172:175], a[120:123]\n" \
"ds_read_b64_tr_b16 v[98:99], v35 offset:1024\n" \
"v_mfma_f32_16x16x32_bf16 a[84:87], v[124:127], v[168:171], a[84:87]\n" \
"s_add_u32 m0, s30, 0x8c00\n" \
"s_mov_b32 s84, 0x6380\n" \
"v_mfma_f32_16x16x32_bf16 a[116:119], v[128:131], v[168:171], a[116:119]\n" \
"buffer_load_dwordx4 v23, s[64:67], s84 offen lds\n" \
"v_mfma_f32_16x16x32_bf16 a[80:83], v[124:127], v[164:167], a[80:83]\n" \
"ds_read_b64_tr_b16 v[100:101], v36 offset:0\n" \
"v_mfma_f32_16x16x32_bf16 a[112:115], v[128:131], v[164:167], a[112:115]\n" \
"ds_read_b64_tr_b16 v[102:103], v36 offset:1024\n" \
"v_mfma_f32_16x16x32_bf16 a[76:79], v[124:127], v[160:163], a[76:79]\n" \
"s_add_u32 m0, s31, 0x8400\n" \
"s_mov_b32 s85, 0x388000\n" \
"v_mfma_f32_16x16x32_bf16 a[108:111], v[128:131], v[160:163], a[108:111]\n" \
"buffer_load_dwordx4 v26, s[68:71], s85 offen lds\n" \
"v_mfma_f32_16x16x32_bf16 a[72:75], v[124:127], v[156:159], a[72:75]\n" \
"ds_read_b64_tr_b16 v[104:105], v37 offset:0\n" \
"v_mfma_f32_16x16x32_bf16 a[104:107], v[128:131], v[156:159], a[104:107]\n" \
"v_mfma_f32_16x16x32_bf16 a[68:71], v[124:127], v[152:155], a[68:71]\n" \
"s_add_u32 m0, s31, 0xc400\n" \
"s_mov_b32 s84, 0x388000\n" \
"v_mfma_f32_16x16x32_bf16 a[100:103], v[128:131], v[152:155], a[100:103]\n" \
"buffer_load_dwordx4 v26, s[72:75], s84 offen lds\n" \
"v_mfma_f32_16x16x32_bf16 a[64:67], v[124:127], v[148:151], a[64:67]\n" \
"ds_read_b64_tr_b16 v[106:107], v37 offset:1024\n" \
"v_mfma_f32_16x16x32_bf16 a[96:99], v[128:131], v[148:151], a[96:99]\n" \
"ds_read_b64_tr_b16 v[108:109], v38 offset:0\n" \
"v_mfma_f32_16x16x32_bf16 a[128:131], v[132:135], v[148:151], a[128:131]\n" \
"s_add_u32 m0, s30, 0x9000\n" \
"s_mov_b32 s85, 0x8380\n" \
"v_mfma_f32_16x16x32_bf16 a[160:163], v[136:139], v[148:151], a[160:163]\n" \
"buffer_load_dwordx4 v22, s[64:67], s85 offen lds\n" \
"v_mfma_f32_16x16x32_bf16 a[132:135], v[132:135], v[152:155], a[132:135]\n" \
"ds_read_b64_tr_b16 v[110:111], v38 offset:1024\n" \
"v_mfma_f32_16x16x32_bf16 a[164:167], v[136:139], v[152:155], a[164:167]\n" \
"ds_read_b64_tr_b16 v[112:113], v39 offset:0\n" \
"v_mfma_f32_16x16x32_bf16 a[136:139], v[132:135], v[156:159], a[136:139]\n" \
"s_add_u32 m0, s30, 0x9400\n" \
"s_mov_b32 s84, 0xa380\n" \
"v_mfma_f32_16x16x32_bf16 a[168:171], v[136:139], v[156:159], a[168:171]\n" \
"buffer_load_dwordx4 v23, s[64:67], s84 offen lds\n" \
"v_mfma_f32_16x16x32_bf16 a[140:143], v[132:135], v[160:163], a[140:143]\n" \
"ds_read_b64_tr_b16 v[114:115], v39 offset:1024\n" \
"v_mfma_f32_16x16x32_bf16 a[172:175], v[136:139], v[160:163], a[172:175]\n" \
"ds_read_b128 v[60:63], v28 offset:20480\n" \
"v_mfma_f32_16x16x32_bf16 a[144:147], v[132:135], v[164:167], a[144:147]\n" \
"s_add_u32 m0, s31, 0x8800\n" \
"s_mov_b32 s85, 0x390000\n" \
"v_mfma_f32_16x16x32_bf16 a[176:179], v[136:139], v[164:167], a[176:179]\n" \
"buffer_load_dwordx4 v27, s[68:71], s85 offen lds\n" \
"v_mfma_f32_16x16x32_bf16 a[148:151], v[132:135], v[168:171], a[148:151]\n" \
"ds_read_b128 v[64:67], v28 offset:22528\n" \
"v_mfma_f32_16x16x32_bf16 a[180:183], v[136:139], v[168:171], a[180:183]\n" \
"ds_read_b128 v[68:71], v28 offset:0\n" \
"v_mfma_f32_16x16x32_bf16 a[152:155], v[132:135], v[172:175], a[152:155]\n" \
"s_add_u32 m0, s31, 0xc800\n" \
"s_mov_b32 s84, 0x390000\n" \
"v_mfma_f32_16x16x32_bf16 a[184:187], v[136:139], v[172:175], a[184:187]\n" \
"buffer_load_dwordx4 v27, s[72:75], s84 offen lds\n" \
"v_mfma_f32_16x16x32_bf16 a[156:159], v[132:135], v[176:179], a[156:159]\n" \
"ds_read_b128 v[72:75], v28 offset:2048\n" \
"v_mfma_f32_16x16x32_bf16 a[188:191], v[136:139], v[176:179], a[188:191]\n" \
"ds_read_b128 v[76:79], v28 offset:4096\n" \
"v_mfma_f32_16x16x32_bf16 a[220:223], v[140:143], v[176:179], a[220:223]\n" \
"v_mfma_f32_16x16x32_bf16 a[252:255], v[144:147], v[176:179], a[252:255]\n" \
"ds_read_b128 v[80:83], v28 offset:6144\n" \
"v_mfma_f32_16x16x32_bf16 a[216:219], v[140:143], v[172:175], a[216:219]\n" \
"v_mfma_f32_16x16x32_bf16 a[248:251], v[144:147], v[172:175], a[248:251]\n" \
"v_mfma_f32_16x16x32_bf16 a[212:215], v[140:143], v[168:171], a[212:215]\n" \
"v_mfma_f32_16x16x32_bf16 a[244:247], v[144:147], v[168:171], a[244:247]\n" \
"v_mfma_f32_16x16x32_bf16 a[208:211], v[140:143], v[164:167], a[208:211]\n" \
"v_mfma_f32_16x16x32_bf16 a[240:243], v[144:147], v[164:167], a[240:243]\n" \
"v_mfma_f32_16x16x32_bf16 a[204:207], v[140:143], v[160:163], a[204:207]\n" \
"v_mfma_f32_16x16x32_bf16 a[236:239], v[144:147], v[160:163], a[236:239]\n" \
"v_mfma_f32_16x16x32_bf16 a[200:203], v[140:143], v[156:159], a[200:203]\n" \
"v_mfma_f32_16x16x32_bf16 a[232:235], v[144:147], v[156:159], a[232:235]\n" \
"v_mfma_f32_16x16x32_bf16 a[196:199], v[140:143], v[152:155], a[196:199]\n" \
"v_mfma_f32_16x16x32_bf16 a[228:231], v[144:147], v[152:155], a[228:231]\n" \
"v_mfma_f32_16x16x32_bf16 a[192:195], v[140:143], v[148:151], a[192:195]\n" \
"v_mfma_f32_16x16x32_bf16 a[224:227], v[144:147], v[148:151], a[224:227]\n" \
"s_waitcnt lgkmcnt(15)\n" \
"s_nop 0\n" \
"v_mfma_f32_16x16x32_bf16 a[0:3], v[52:55], v[84:87], a[0:3]\n" \
"s_add_u32 m0, s30, 0x9800\n" \
"s_mov_b32 s85, 0xc380\n" \
"v_mfma_f32_16x16x32_bf16 a[32:35], v[56:59], v[84:87], a[32:35]\n" \
"buffer_load_dwordx4 v22, s[64:67], s85 offen lds\n" \
"v_mfma_f32_16x16x32_bf16 a[4:7], v[52:55], v[88:91], a[4:7]\n" \
"ds_read_b64_tr_b16 v[148:149], v32 offset:8192\n" \
"v_mfma_f32_16x16x32_bf16 a[36:39], v[56:59], v[88:91], a[36:39]\n" \
"s_add_u32 m0, s30, 0x9c00\n" \
"s_mov_b32 s84, 0xe380\n" \
"v_mfma_f32_16x16x32_bf16 a[8:11], v[52:55], v[92:95], a[8:11]\n" \
"buffer_load_dwordx4 v23, s[64:67], s84 offen lds\n" \
"v_mfma_f32_16x16x32_bf16 a[40:43], v[56:59], v[92:95], a[40:43]\n" \
"ds_read_b64_tr_b16 v[150:151], v32 offset:9216\n" \
"s_waitcnt lgkmcnt(15)\n" \
"v_mfma_f32_16x16x32_bf16 a[12:15], v[52:55], v[96:99], a[12:15]\n" \
"s_add_u32 m0, s31, 0x8c00\n" \
"s_mov_b32 s85, 0x398000\n" \
"v_mfma_f32_16x16x32_bf16 a[44:47], v[56:59], v[96:99], a[44:47]\n" \
"buffer_load_dwordx4 v27, s[68:71], s85 offen lds\n" \
"s_waitcnt lgkmcnt(14)\n" \
"v_mfma_f32_16x16x32_bf16 a[16:19], v[52:55], v[100:103], a[16:19]\n" \
"ds_read_b128 v[116:119], v29 offset:16384\n" \
"v_mfma_f32_16x16x32_bf16 a[48:51], v[56:59], v[100:103], a[48:51]\n" \
"s_add_u32 m0, s31, 0xcc00\n" \
"s_mov_b32 s84, 0x398000\n" \
"s_waitcnt lgkmcnt(13)\n" \
"v_mfma_f32_16x16x32_bf16 a[20:23], v[52:55], v[104:107], a[20:23]\n" \
"buffer_load_dwordx4 v27, s[72:75], s84 offen lds\n" \
"v_mfma_f32_16x16x32_bf16 a[52:55], v[56:59], v[104:107], a[52:55]\n" \
"ds_read_b128 v[120:123], v29 offset:18432\n" \
"s_waitcnt lgkmcnt(12)\n" \
"v_mfma_f32_16x16x32_bf16 a[24:27], v[52:55], v[108:111], a[24:27]\n" \
"ds_read_b64_tr_b16 v[152:153], v33 offset:8192\n" \
"v_mfma_f32_16x16x32_bf16 a[56:59], v[56:59], v[108:111], a[56:59]\n" \
"ds_read_b64_tr_b16 v[154:155], v33 offset:9216\n" \
"s_waitcnt lgkmcnt(12)\n" \
"v_mfma_f32_16x16x32_bf16 a[28:31], v[52:55], v[112:115], a[28:31]\n" \
"ds_read_b64_tr_b16 v[156:157], v34 offset:8192\n" \
"v_mfma_f32_16x16x32_bf16 a[60:63], v[56:59], v[112:115], a[60:63]\n" \
"ds_read_b64_tr_b16 v[158:159], v34 offset:9216\n" \
"s_waitcnt lgkmcnt(13)\n" \
"v_mfma_f32_16x16x32_bf16 a[92:95], v[60:63], v[112:115], a[92:95]\n" \
"s_waitcnt lgkmcnt(12)\n" \
"s_nop 0\n" \
"v_mfma_f32_16x16x32_bf16 a[124:127], v[64:67], v[112:115], a[124:127]\n" \
"ds_read_b64_tr_b16 v[160:161], v35 offset:8192\n" \
"v_mfma_f32_16x16x32_bf16 a[88:91], v[60:63], v[108:111], a[88:91]\n" \
"v_mfma_f32_16x16x32_bf16 a[120:123], v[64:67], v[108:111], a[120:123]\n" \
"ds_read_b64_tr_b16 v[162:163], v35 offset:9216\n" \
"v_mfma_f32_16x16x32_bf16 a[84:87], v[60:63], v[104:107], a[84:87]\n" \
"v_mfma_f32_16x16x32_bf16 a[116:119], v[64:67], v[104:107], a[116:119]\n" \
"ds_read_b64_tr_b16 v[164:165], v36 offset:8192\n" \
"v_mfma_f32_16x16x32_bf16 a[80:83], v[60:63], v[100:103], a[80:83]\n" \
"v_mfma_f32_16x16x32_bf16 a[112:115], v[64:67], v[100:103], a[112:115]\n" \
"ds_read_b64_tr_b16 v[166:167], v36 offset:9216\n" \
"v_mfma_f32_16x16x32_bf16 a[76:79], v[60:63], v[96:99], a[76:79]\n" \
"v_mfma_f32_16x16x32_bf16 a[108:111], v[64:67], v[96:99], a[108:111]\n" \
"v_mfma_f32_16x16x32_bf16 a[72:75], v[60:63], v[92:95], a[72:75]\n" \
"ds_read_b64_tr_b16 v[168:169], v37 offset:8192\n" \
"v_mfma_f32_16x16x32_bf16 a[104:107], v[64:67], v[92:95], a[104:107]\n" \
"v_mfma_f32_16x16x32_bf16 a[68:71], v[60:63], v[88:91], a[68:71]\n" \
"ds_read_b64_tr_b16 v[170:171], v37 offset:9216\n" \
"v_mfma_f32_16x16x32_bf16 a[100:103], v[64:67], v[88:91], a[100:103]\n" \
"v_mfma_f32_16x16x32_bf16 a[64:67], v[60:63], v[84:87], a[64:67]\n" \
"ds_read_b64_tr_b16 v[172:173], v38 offset:8192\n" \
"v_mfma_f32_16x16x32_bf16 a[96:99], v[64:67], v[84:87], a[96:99]\n" \
"s_waitcnt lgkmcnt(15)\n" \
"s_nop 0\n" \
"v_mfma_f32_16x16x32_bf16 a[128:131], v[68:71], v[84:87], a[128:131]\n" \
"ds_read_b64_tr_b16 v[174:175], v38 offset:9216\n" \
"v_mfma_f32_16x16x32_bf16 a[160:163], v[72:75], v[84:87], a[160:163]\n" \
"v_mfma_f32_16x16x32_bf16 a[132:135], v[68:71], v[88:91], a[132:135]\n" \
"ds_read_b64_tr_b16 v[176:177], v39 offset:8192\n" \
"v_mfma_f32_16x16x32_bf16 a[164:167], v[72:75], v[88:91], a[164:167]\n" \
"v_mfma_f32_16x16x32_bf16 a[136:139], v[68:71], v[92:95], a[136:139]\n" \
"ds_read_b64_tr_b16 v[178:179], v39 offset:9216\n" \
"v_mfma_f32_16x16x32_bf16 a[168:171], v[72:75], v[92:95], a[168:171]\n" \
"v_mfma_f32_16x16x32_bf16 a[140:143], v[68:71], v[96:99], a[140:143]\n" \
"v_bfe_u32 v44, v20, 2, 5\n" \
"v_mfma_f32_16x16x32_bf16 a[172:175], v[72:75], v[96:99], a[172:175]\n" \
"buffer_load_dword v44, v44, s[80:83], s38 offen offset:128 sc1\n" \
"v_mfma_f32_16x16x32_bf16 a[144:147], v[68:71], v[100:103], a[144:147]\n" \
"ds_read_b128 v[124:127], v29 offset:20480\n" \
"v_mfma_f32_16x16x32_bf16 a[176:179], v[72:75], v[100:103], a[176:179]\n" \
"ds_read_b128 v[128:131], v29 offset:22528\n" \
"v_mfma_f32_16x16x32_bf16 a[148:151], v[68:71], v[104:107], a[148:151]\n" \
"v_mfma_f32_16x16x32_bf16 a[180:183], v[72:75], v[104:107], a[180:183]\n" \
"ds_read_b128 v[132:135], v29 offset:0\n" \
"v_mfma_f32_16x16x32_bf16 a[152:155], v[68:71], v[108:111], a[152:155]\n" \
"v_mfma_f32_16x16x32_bf16 a[184:187], v[72:75], v[108:111], a[184:187]\n" \
"ds_read_b128 v[136:139], v29 offset:2048\n" \
"v_mfma_f32_16x16x32_bf16 a[156:159], v[68:71], v[112:115], a[156:159]\n" \
"v_mfma_f32_16x16x32_bf16 a[188:191], v[72:75], v[112:115], a[188:191]\n" \
"ds_read_b128 v[140:143], v29 offset:4096\n" \
"v_mfma_f32_16x16x32_bf16 a[220:223], v[76:79], v[112:115], a[220:223]\n" \
"v_mfma_f32_16x16x32_bf16 a[252:255], v[80:83], v[112:115], a[252:255]\n" \
"ds_read_b128 v[144:147], v29 offset:6144\n" \
"v_mfma_f32_16x16x32_bf16 a[216:219], v[76:79], v[108:111], a[216:219]\n" \
"v_mfma_f32_16x16x32_bf16 a[248:251], v[80:83], v[108:111], a[248:251]\n" \
"v_mfma_f32_16x16x32_bf16 a[212:215], v[76:79], v[104:107], a[212:215]\n" \
"v_mfma_f32_16x16x32_bf16 a[244:247], v[80:83], v[104:107], a[244:247]\n" \
"v_mfma_f32_16x16x32_bf16 a[208:211], v[76:79], v[100:103], a[208:211]\n" \
"v_mfma_f32_16x16x32_bf16 a[240:243], v[80:83], v[100:103], a[240:243]\n" \
"v_mfma_f32_16x16x32_bf16 a[204:207], v[76:79], v[96:99], a[204:207]\n" \
"v_mfma_f32_16x16x32_bf16 a[236:239], v[80:83], v[96:99], a[236:239]\n" \
"v_mfma_f32_16x16x32_bf16 a[200:203], v[76:79], v[92:95], a[200:203]\n" \
"v_mfma_f32_16x16x32_bf16 a[232:235], v[80:83], v[92:95], a[232:235]\n" \
"v_mfma_f32_16x16x32_bf16 a[196:199], v[76:79], v[88:91], a[196:199]\n" \
"v_mfma_f32_16x16x32_bf16 a[228:231], v[80:83], v[88:91], a[228:231]\n" \
"v_mfma_f32_16x16x32_bf16 a[192:195], v[76:79], v[84:87], a[192:195]\n" \
"v_mfma_f32_16x16x32_bf16 a[224:227], v[80:83], v[84:87], a[224:227]\n" \
"s_waitcnt vmcnt(0) lgkmcnt(0)\n" \
"s_barrier\n" \
"v_cmp_ne_u32_e32 vcc, s62, v44\n" \
"s_nop 4\n" \
"s_cbranch_vccnz L_poll61_%=\n" \
"L_pollr61_%=:\n" \
"s_mov_b32 s68, s12\n" \
"s_and_b32 s69, s13, 0xffff\n" \
"s_mov_b32 s70, 0x480000\n" \
"s_mov_b32 s71, 0x20000\n" \
"v_mfma_f32_16x16x32_bf16 a[0:3], v[116:119], v[148:151], a[0:3]\n" \
"s_add_u32 m0, s40, 0x0\n" \
"s_add_u32 s85, s39, 0x0\n" \
"v_mfma_f32_16x16x32_bf16 a[32:35], v[120:123], v[148:151], a[32:35]\n" \
"buffer_load_dwordx4 v20, s[80:83], s85 offen sc1 lds\n" \
"v_mfma_f32_16x16x32_bf16 a[4:7], v[116:119], v[152:155], a[4:7]\n" \
"ds_read_b64_tr_b16 v[84:85], v32 offset:32768\n" \
"v_mfma_f32_16x16x32_bf16 a[36:39], v[120:123], v[152:155], a[36:39]\n" \
"ds_read_b64_tr_b16 v[86:87], v32 offset:33792\n" \
"v_mfma_f32_16x16x32_bf16 a[8:11], v[116:119], v[156:159], a[8:11]\n" \
"s_add_u32 m0, s40, 0x1000\n" \
"s_add_u32 s84, s39, 0x1000\n" \
"v_mfma_f32_16x16x32_bf16 a[40:43], v[120:123], v[156:159], a[40:43]\n" \
"buffer_load_dwordx4 v20, s[80:83], s84 offen sc1 lds\n" \
"v_mfma_f32_16x16x32_bf16 a[12:15], v[116:119], v[160:163], a[12:15]\n" \
"ds_read_b128 v[52:55], v28 offset:49152\n" \
"v_mfma_f32_16x16x32_bf16 a[44:47], v[120:123], v[160:163], a[44:47]\n" \
"ds_read_b128 v[56:59], v28 offset:51200\n" \
"v_mfma_f32_16x16x32_bf16 a[16:19], v[116:119], v[164:167], a[16:19]\n" \
"s_add_u32 m0, s40, 0x3000\n" \
"s_mov_b32 s85, 0x0\n" \
"v_mfma_f32_16x16x32_bf16 a[48:51], v[120:123], v[164:167], a[48:51]\n" \
"buffer_load_dwordx4 v21, s[76:79], s85 offen lds\n" \
"v_mfma_f32_16x16x32_bf16 a[20:23], v[116:119], v[168:171], a[20:23]\n" \
"ds_read_b64_tr_b16 v[88:89], v33 offset:32768\n" \
"v_mfma_f32_16x16x32_bf16 a[52:55], v[120:123], v[168:171], a[52:55]\n" \
"ds_read_b64_tr_b16 v[90:91], v33 offset:33792\n" \
"v_mfma_f32_16x16x32_bf16 a[24:27], v[116:119], v[172:175], a[24:27]\n" \
"s_add_u32 m0, s30, 0x0\n" \
"s_mov_b32 s84, 0x0\n" \
"s_nop 0\n" \
"v_mfma_f32_16x16x32_bf16 a[56:59], v[120:123], v[172:175], a[56:59]\n" \
"buffer_load_dwordx4 v24, s[76:79], s84 offen lds\n" \
"v_mfma_f32_16x16x32_bf16 a[28:31], v[116:119], v[176:179], a[28:31]\n" \
"ds_read_b64_tr_b16 v[92:93], v34 offset:32768\n" \
"v_mfma_f32_16x16x32_bf16 a[60:63], v[120:123], v[176:179], a[60:63]\n" \
"ds_read_b64_tr_b16 v[94:95], v34 offset:33792\n" \
"v_mfma_f32_16x16x32_bf16 a[92:95], v[124:127], v[176:179], a[92:95]\n" \
"s_add_u32 m0, s30, 0x400\n" \
"s_mov_b32 s85, 0x2400\n" \
"v_mfma_f32_16x16x32_bf16 a[124:127], v[128:131], v[176:179], a[124:127]\n" \
"buffer_load_dwordx4 v25, s[76:79], s85 offen lds\n" \
"v_mfma_f32_16x16x32_bf16 a[88:91], v[124:127], v[172:175], a[88:91]\n" \
"ds_read_b64_tr_b16 v[96:97], v35 offset:32768\n" \
"v_mfma_f32_16x16x32_bf16 a[120:123], v[128:131], v[172:175], a[120:123]\n" \
"ds_read_b64_tr_b16 v[98:99], v35 offset:33792\n" \
"v_mfma_f32_16x16x32_bf16 a[84:87], v[124:127], v[168:171], a[84:87]\n" \
"s_add_u32 m0, s31, 0x0\n" \
"s_mov_b32 s84, 0x0\n" \
"v_mfma_f32_16x16x32_bf16 a[116:119], v[128:131], v[168:171], a[116:119]\n" \
"buffer_load_dwordx4 v26, s[68:71], s84 offen lds\n" \
"v_mfma_f32_16x16x32_bf16 a[80:83], v[124:127], v[164:167], a[80:83]\n" \
"ds_read_b64_tr_b16 v[100:101], v36 offset:32768\n" \
"v_mfma_f32_16x16x32_bf16 a[112:115], v[128:131], v[164:167], a[112:115]\n" \
"ds_read_b64_tr_b16 v[102:103], v36 offset:33792\n" \
"v_mfma_f32_16x16x32_bf16 a[76:79], v[124:127], v[160:163], a[76:79]\n" \
"s_add_u32 m0, s30, 0x800\n" \
"s_mov_b32 s85, 0x4800\n" \
"v_mfma_f32_16x16x32_bf16 a[108:111], v[128:131], v[160:163], a[108:111]\n" \
"buffer_load_dwordx4 v24, s[76:79], s85 offen lds\n" \
"v_mfma_f32_16x16x32_bf16 a[72:75], v[124:127], v[156:159], a[72:75]\n" \
"ds_read_b64_tr_b16 v[104:105], v37 offset:32768\n" \
"v_mfma_f32_16x16x32_bf16 a[104:107], v[128:131], v[156:159], a[104:107]\n" \
"v_mfma_f32_16x16x32_bf16 a[68:71], v[124:127], v[152:155], a[68:71]\n" \
"s_add_u32 m0, s30, 0xc00\n" \
"s_mov_b32 s84, 0x6c00\n" \
"v_mfma_f32_16x16x32_bf16 a[100:103], v[128:131], v[152:155], a[100:103]\n" \
"buffer_load_dwordx4 v25, s[76:79], s84 offen lds\n" \
"v_mfma_f32_16x16x32_bf16 a[64:67], v[124:127], v[148:151], a[64:67]\n" \
"ds_read_b64_tr_b16 v[106:107], v37 offset:33792\n" \
"v_mfma_f32_16x16x32_bf16 a[96:99], v[128:131], v[148:151], a[96:99]\n" \
"ds_read_b64_tr_b16 v[108:109], v38 offset:32768\n" \
"v_mfma_f32_16x16x32_bf16 a[128:131], v[132:135], v[148:151], a[128:131]\n" \
"s_add_u32 m0, s31, 0x400\n" \
"s_mov_b32 s85, 0x8000\n" \
"v_mfma_f32_16x16x32_bf16 a[160:163], v[136:139], v[148:151], a[160:163]\n" \
"buffer_load_dwordx4 v26, s[68:71], s85 offen lds\n" \
"v_mfma_f32_16x16x32_bf16 a[132:135], v[132:135], v[152:155], a[132:135]\n" \
"ds_read_b64_tr_b16 v[110:111], v38 offset:33792\n" \
"v_mfma_f32_16x16x32_bf16 a[164:167], v[136:139], v[152:155], a[164:167]\n" \
"ds_read_b64_tr_b16 v[112:113], v39 offset:32768\n" \
"v_mfma_f32_16x16x32_bf16 a[136:139], v[132:135], v[156:159], a[136:139]\n" \
"s_add_u32 m0, s30, 0x1000\n" \
"s_mov_b32 s84, 0x9000\n" \
"v_mfma_f32_16x16x32_bf16 a[168:171], v[136:139], v[156:159], a[168:171]\n" \
"buffer_load_dwordx4 v24, s[76:79], s84 offen lds\n" \
"v_mfma_f32_16x16x32_bf16 a[140:143], v[132:135], v[160:163], a[140:143]\n" \
"ds_read_b64_tr_b16 v[114:115], v39 offset:33792\n" \
"v_mfma_f32_16x16x32_bf16 a[172:175], v[136:139], v[160:163], a[172:175]\n" \
"ds_read_b128 v[60:63], v28 offset:53248\n" \
"v_mfma_f32_16x16x32_bf16 a[144:147], v[132:135], v[164:167], a[144:147]\n" \
"s_add_u32 m0, s30, 0x1400\n" \
"s_mov_b32 s85, 0xb400\n" \
"v_mfma_f32_16x16x32_bf16 a[176:179], v[136:139], v[164:167], a[176:179]\n" \
"buffer_load_dwordx4 v25, s[76:79], s85 offen lds\n" \
"v_mfma_f32_16x16x32_bf16 a[148:151], v[132:135], v[168:171], a[148:151]\n" \
"ds_read_b128 v[64:67], v28 offset:55296\n" \
"v_mfma_f32_16x16x32_bf16 a[180:183], v[136:139], v[168:171], a[180:183]\n" \
"ds_read_b128 v[68:71], v28 offset:32768\n" \
"v_mfma_f32_16x16x32_bf16 a[152:155], v[132:135], v[172:175], a[152:155]\n" \
"s_add_u32 m0, s31, 0x800\n" \
"s_mov_b32 s84, 0x10000\n" \
"v_mfma_f32_16x16x32_bf16 a[184:187], v[136:139], v[172:175], a[184:187]\n" \
"buffer_load_dwordx4 v27, s[68:71], s84 offen lds\n" \
"v_mfma_f32_16x16x32_bf16 a[156:159], v[132:135], v[176:179], a[156:159]\n" \
"ds_read_b128 v[72:75], v28 offset:34816\n" \
"v_mfma_f32_16x16x32_bf16 a[188:191], v[136:139], v[176:179], a[188:191]\n" \
"ds_read_b128 v[76:79], v28 offset:36864\n" \
"v_mfma_f32_16x16x32_bf16 a[220:223], v[140:143], v[176:179], a[220:223]\n" \
"v_mfma_f32_16x16x32_bf16 a[252:255], v[144:147], v[176:179], a[252:255]\n" \
"ds_read_b128 v[80:83], v28 offset:38912\n" \
"v_mfma_f32_16x16x32_bf16 a[216:219], v[140:143], v[172:175], a[216:219]\n" \
"v_mfma_f32_16x16x32_bf16 a[248:251], v[144:147], v[172:175], a[248:251]\n" \
"v_mfma_f32_16x16x32_bf16 a[212:215], v[140:143], v[168:171], a[212:215]\n" \
"v_mfma_f32_16x16x32_bf16 a[244:247], v[144:147], v[168:171], a[244:247]\n" \
"v_mfma_f32_16x16x32_bf16 a[208:211], v[140:143], v[164:167], a[208:211]\n" \
"v_mfma_f32_16x16x32_bf16 a[240:243], v[144:147], v[164:167], a[240:243]\n" \
"v_mfma_f32_16x16x32_bf16 a[204:207], v[140:143], v[160:163], a[204:207]\n" \
"v_mfma_f32_16x16x32_bf16 a[236:239], v[144:147], v[160:163], a[236:239]\n" \
"v_mfma_f32_16x16x32_bf16 a[200:203], v[140:143], v[156:159], a[200:203]\n" \
"v_mfma_f32_16x16x32_bf16 a[232:235], v[144:147], v[156:159], a[232:235]\n" \
"v_mfma_f32_16x16x32_bf16 a[196:199], v[140:143], v[152:155], a[196:199]\n" \
"v_mfma_f32_16x16x32_bf16 a[228:231], v[144:147], v[152:155], a[228:231]\n" \
"v_mfma_f32_16x16x32_bf16 a[192:195], v[140:143], v[148:151], a[192:195]\n" \
"v_mfma_f32_16x16x32_bf16 a[224:227], v[144:147], v[148:151], a[224:227]\n" \
"s_waitcnt lgkmcnt(15)\n" \
"s_nop 0\n" \
"v_mfma_f32_16x16x32_bf16 a[0:3], v[52:55], v[84:87], a[0:3]\n" \
"s_add_u32 m0, s30, 0x1800\n" \
"s_mov_b32 s84, 0xd800\n" \
"v_mfma_f32_16x16x32_bf16 a[32:35], v[56:59], v[84:87], a[32:35]\n" \
"buffer_load_dwordx4 v24, s[76:79], s84 offen lds\n" \
"v_mfma_f32_16x16x32_bf16 a[4:7], v[52:55], v[88:91], a[4:7]\n" \
"ds_read_b64_tr_b16 v[148:149], v32 offset:40960\n" \
"v_mfma_f32_16x16x32_bf16 a[36:39], v[56:59], v[88:91], a[36:39]\n" \
"s_add_u32 m0, s30, 0x1c00\n" \
"s_mov_b32 s85, 0xfc00\n" \
"v_mfma_f32_16x16x32_bf16 a[8:11], v[52:55], v[92:95], a[8:11]\n" \
"buffer_load_dwordx4 v25, s[76:79], s85 offen lds\n" \
"v_mfma_f32_16x16x32_bf16 a[40:43], v[56:59], v[92:95], a[40:43]\n" \
"ds_read_b64_tr_b16 v[150:151], v32 offset:41984\n" \
"s_waitcnt lgkmcnt(15)\n" \
"v_mfma_f32_16x16x32_bf16 a[12:15], v[52:55], v[96:99], a[12:15]\n" \
"s_add_u32 m0, s31, 0xc00\n" \
"s_mov_b32 s84, 0x18000\n" \
"v_mfma_f32_16x16x32_bf16 a[44:47], v[56:59], v[96:99], a[44:47]\n" \
"buffer_load_dwordx4 v27, s[68:71], s84 offen lds\n" \
"s_waitcnt lgkmcnt(14)\n" \
"v_mfma_f32_16x16x32_bf16 a[16:19], v[52:55], v[100:103], a[16:19]\n" \
"ds_read_b128 v[116:119], v29 offset:49152\n" \
"v_mfma_f32_16x16x32_bf16 a[48:51], v[56:59], v[100:103], a[48:51]\n" \
"ds_read_b128 v[120:123], v29 offset:51200\n" \
"s_waitcnt lgkmcnt(14)\n" \
"v_mfma_f32_16x16x32_bf16 a[20:23], v[52:55], v[104:107], a[20:23]\n" \
"ds_read_b64_tr_b16 v[152:153], v33 offset:40960\n" \
"v_mfma_f32_16x16x32_bf16 a[52:55], v[56:59], v[104:107], a[52:55]\n" \
"ds_read_b64_tr_b16 v[154:155], v33 offset:41984\n" \
"s_waitcnt lgkmcnt(14)\n" \
"v_mfma_f32_16x16x32_bf16 a[24:27], v[52:55], v[108:111], a[24:27]\n" \
"v_mfma_f32_16x16x32_bf16 a[56:59], v[56:59], v[108:111], a[56:59]\n" \
"ds_read_b64_tr_b16 v[156:157], v34 offset:40960\n" \
"s_waitcnt lgkmcnt(13)\n" \
"v_mfma_f32_16x16x32_bf16 a[28:31], v[52:55], v[112:115], a[28:31]\n" \
"v_mfma_f32_16x16x32_bf16 a[60:63], v[56:59], v[112:115], a[60:63]\n" \
"ds_read_b64_tr_b16 v[158:159], v34 offset:41984\n" \
"s_waitcnt lgkmcnt(13)\n" \
"v_mfma_f32_16x16x32_bf16 a[92:95], v[60:63], v[112:115], a[92:95]\n" \
"s_waitcnt lgkmcnt(12)\n" \
"v_mfma_f32_16x16x32_bf16 a[124:127], v[64:67], v[112:115], a[124:127]\n" \
"ds_read_b64_tr_b16 v[160:161], v35 offset:40960\n" \
"v_mfma_f32_16x16x32_bf16 a[88:91], v[60:63], v[108:111], a[88:91]\n" \
"v_lshlrev_b32_e32 v45, 3, v20\n" \
"v_add_u32_e64 v45, s31, v45\n" \
"v_mfma_f32_16x16x32_bf16 a[120:123], v[64:67], v[108:111], a[120:123]\n" \
"s_getpc_b64 s[86:87]\n" \
"s_sub_u32 s86, s86, 0x10800\n" \
"s_subb_u32 s87, s87, 0\n" \
"s_mov_b32 m0, s32\n" \
"s_nop 0\n" \
"v_mfma_f32_16x16x32_bf16 a[84:87], v[60:63], v[104:107], a[84:87]\n" \
"global_load_lds_dword v45, s[86:87]\n" \
"v_mfma_f32_16x16x32_bf16 a[116:119], v[64:67], v[104:107], a[116:119]\n" \
"ds_read_b64_tr_b16 v[162:163], v35 offset:41984\n" \
"v_mfma_f32_16x16x32_bf16 a[80:83], v[60:63], v[100:103], a[80:83]\n" \
"ds_read_b64_tr_b16 v[164:165], v36 offset:40960\n" \
"v_mfma_f32_16x16x32_bf16 a[112:115], v[64:67], v[100:103], a[112:115]\n" \
"ds_read_b64_tr_b16 v[166:167], v36 offset:41984\n" \
"v_mfma_f32_16x16x32_bf16 a[76:79], v[60:63], v[96:99], a[76:79]\n" \
"v_mfma_f32_16x16x32_bf16 a[108:111], v[64:67], v[96:99], a[108:111]\n" \
"v_mfma_f32_16x16x32_bf16 a[72:75], v[60:63], v[92:95], a[72:75]\n" \
"ds_read_b64_tr_b16 v[168:169], v37 offset:40960\n" \
"v_mfma_f32_16x16x32_bf16 a[104:107], v[64:67], v[92:95], a[104:107]\n" \
"v_mfma_f32_16x16x32_bf16 a[68:71], v[60:63], v[88:91], a[68:71]\n" \
"ds_read_b64_tr_b16 v[170:171], v37 offset:41984\n" \
"v_mfma_f32_16x16x32_bf16 a[100:103], v[64:67], v[88:91], a[100:103]\n" \
"v_mfma_f32_16x16x32_bf16 a[64:67], v[60:63], v[84:87], a[64:67]\n" \
"ds_read_b64_tr_b16 v[172:173], v38 offset:40960\n" \
"v_mfma_f32_16x16x32_bf16 a[96:99], v[64:67], v[84:87], a[96:99]\n" \
"s_waitcnt lgkmcnt(15)\n" \
"s_nop 0\n" \
"v_mfma_f32_16x16x32_bf16 a[128:131], v[68:71], v[84:87], a[128:131]\n" \
"ds_read_b64_tr_b16 v[174:175], v38 offset:41984\n" \
"v_mfma_f32_16x16x32_bf16 a[160:163], v[72:75], v[84:87], a[160:163]\n" \
"v_mfma_f32_16x16x32_bf16 a[132:135], v[68:71], v[88:91], a[132:135]\n" \
"ds_read_b64_tr_b16 v[176:177], v39 offset:40960\n" \
"v_mfma_f32_16x16x32_bf16 a[164:167], v[72:75], v[88:91], a[164:167]\n" \
"v_mfma_f32_16x16x32_bf16 a[136:139], v[68:71], v[92:95], a[136:139]\n" \
"ds_read_b64_tr_b16 v[178:179], v39 offset:41984\n" \
"v_mfma_f32_16x16x32_bf16 a[168:171], v[72:75], v[92:95], a[168:171]\n" \
"v_mfma_f32_16x16x32_bf16 a[140:143], v[68:71], v[96:99], a[140:143]\n" \
"v_mfma_f32_16x16x32_bf16 a[172:175], v[72:75], v[96:99], a[172:175]\n" \
"ds_read_b128 v[124:127], v29 offset:53248\n" \
"v_mfma_f32_16x16x32_bf16 a[144:147], v[68:71], v[100:103], a[144:147]\n" \
"v_mfma_f32_16x16x32_bf16 a[176:179], v[72:75], v[100:103], a[176:179]\n" \
"ds_read_b128 v[128:131], v29 offset:55296\n" \
"v_mfma_f32_16x16x32_bf16 a[148:151], v[68:71], v[104:107], a[148:151]\n" \
"v_mfma_f32_16x16x32_bf16 a[180:183], v[72:75], v[104:107], a[180:183]\n" \
"ds_read_b128 v[132:135], v29 offset:32768\n" \
"v_mfma_f32_16x16x32_bf16 a[152:155], v[68:71], v[108:111], a[152:155]\n" \
"v_mfma_f32_16x16x32_bf16 a[184:187], v[72:75], v[108:111], a[184:187]\n" \
"ds_read_b128 v[136:139], v29 offset:34816\n" \
"v_mfma_f32_16x16x32_bf16 a[156:159], v[68:71], v[112:115], a[156:159]\n" \
"v_mfma_f32_16x16x32_bf16 a[188:191], v[72:75], v[112:115], a[188:191]\n" \
"ds_read_b128 v[140:143], v29 offset:36864\n" \
"v_mfma_f32_16x16x32_bf16 a[220:223], v[76:79], v[112:115], a[220:223]\n" \
"v_mfma_f32_16x16x32_bf16 a[252:255], v[80:83], v[112:115], a[252:255]\n" \
"ds_read_b128 v[144:147], v29 offset:38912\n" \
"v_mfma_f32_16x16x32_bf16 a[216:219], v[76:79], v[108:111], a[216:219]\n" \
"v_mfma_f32_16x16x32_bf16 a[248:251], v[80:83], v[108:111], a[248:251]\n" \
"v_mfma_f32_16x16x32_bf16 a[212:215], v[76:79], v[104:107], a[212:215]\n" \
"v_mfma_f32_16x16x32_bf16 a[244:247], v[80:83], v[104:107], a[244:247]\n" \
"v_mfma_f32_16x16x32_bf16 a[208:211], v[76:79], v[100:103], a[208:211]\n" \
"v_mfma_f32_16x16x32_bf16 a[240:243], v[80:83], v[100:103], a[240:243]\n" \
"v_mfma_f32_16x16x32_bf16 a[204:207], v[76:79], v[96:99], a[204:207]\n" \
"v_mfma_f32_16x16x32_bf16 a[236:239], v[80:83], v[96:99], a[236:239]\n" \
"v_mfma_f32_16x16x32_bf16 a[200:203], v[76:79], v[92:95], a[200:203]\n" \
"v_mfma_f32_16x16x32_bf16 a[232:235], v[80:83], v[92:95], a[232:235]\n" \
"v_mfma_f32_16x16x32_bf16 a[196:199], v[76:79], v[88:91], a[196:199]\n" \
"v_mfma_f32_16x16x32_bf16 a[228:231], v[80:83], v[88:91], a[228:231]\n" \
"v_mfma_f32_16x16x32_bf16 a[192:195], v[76:79], v[84:87], a[192:195]\n" \
"v_mfma_f32_16x16x32_bf16 a[224:227], v[80:83], v[84:87], a[224:227]\n" \
"s_waitcnt vmcnt(1) lgkmcnt(0)\n" \
"s_barrier\n" \
"v_xor_b32_e32 v32, 0x4000, v36\n" \
"v_xor_b32_e64 v36, 0x0, v190\n" \
"v_xor_b32_e32 v33, 0x4000, v37\n" \
"v_xor_b32_e64 v37, 0x20, v190\n" \
"v_xor_b32_e32 v34, 0x4000, v38\n" \
"v_xor_b32_e64 v38, 0x40, v190\n" \
"v_xor_b32_e32 v35, 0x4000, v39\n" \
"v_xor_b32_e32 v39, 0x60, v190\n" \
"v_add_u32_e32 v28, s43, v28\n" \
"v_add_u32_e32 v29, s43, v29\n" \
"v_mfma_f32_16x16x32_bf16 a[0:3], v[116:119], v[148:151], a[0:3]\n" \
"s_add_u32 m0, s40, 0x8000\n" \
"s_add_u32 s85, s39, 0x2000\n" \
"v_mfma_f32_16x16x32_bf16 a[32:35], v[120:123], v[148:151], a[32:35]\n" \
"buffer_load_dwordx4 v20, s[80:83], s85 offen sc1 lds\n" \
"v_mfma_f32_16x16x32_bf16 a[4:7], v[116:119], v[152:155], a[4:7]\n" \
"ds_read_b64_tr_b16 v[104:105], v37 offset:0\n" \
"v_mfma_f32_16x16x32_bf16 a[36:39], v[120:123], v[152:155], a[36:39]\n" \
"ds_read_b64_tr_b16 v[106:107], v37 offset:512\n" \
"v_mfma_f32_16x16x32_bf16 a[8:11], v[116:119], v[156:159], a[8:11]\n" \
"s_add_u32 m0, s40, 0x9000\n" \
"s_add_u32 s84, s39, 0x3000\n" \
"v_accvgpr_read_b32 v184, a0\n" \
"v_accvgpr_read_b32 v185, a1\n" \
"v_mfma_f32_16x16x32_bf16 a[40:43], v[120:123], v[156:159], a[40:43]\n" \
"buffer_load_dwordx4 v20, s[80:83], s84 offen sc1 lds\n" \
"v_accvgpr_read_b32 v186, a2\n" \
"v_accvgpr_read_b32 v187, a3\n" \
"v_mfma_f32_16x16x32_bf16 a[12:15], v[116:119], v[160:163], a[12:15]\n" \
"ds_read_b64_tr_b16 v[108:109], v38 offset:0\n" \
"v_accvgpr_read_b32 v180, a32\n" \
"v_accvgpr_read_b32 v181, a33\n" \
"v_accvgpr_read_b32 v182, a34\n" \
"v_mfma_f32_16x16x32_bf16 a[44:47], v[120:123], v[160:163], a[44:47]\n" \
"ds_read_b64_tr_b16 v[110:111], v38 offset:512\n" \
"v_accvgpr_read_b32 v183, a35\n" \
"v_cvt_pk_bf16_f32 v184, v184, v185\n" \
"v_cvt_pk_bf16_f32 v185, v186, v187\n" \
"v_mfma_f32_16x16x32_bf16 a[16:19], v[116:119], v[164:167], a[16:19]\n" \
"s_add_u32 m0, s40, 0xb000\n" \
"s_mov_b32 s85, 0x80\n" \
"v_cvt_pk_bf16_f32 v186, v180, v181\n" \
"v_cvt_pk_bf16_f32 v187, v182, v183\n" \
"v_mfma_f32_16x16x32_bf16 a[48:51], v[120:123], v[164:167], a[48:51]\n" \
"buffer_load_dwordx4 v21, s[76:79], s85 offen lds\n" \
"v_accvgpr_read_b32 v188, a4\n" \
"v_accvgpr_read_b32 v189, a5\n" \
"v_mfma_f32_16x16x32_bf16 a[20:23], v[116:119], v[168:171], a[20:23]\n" \
"ds_read_b64_tr_b16 v[112:113], v39 offset:0\n" \
"v_accvgpr_read_b32 v190, a6\n" \
"v_accvgpr_read_b32 v191, a7\n" \
"v_accvgpr_read_b32 v180, a36\n" \
"v_mfma_f32_16x16x32_bf16 a[52:55], v[120:123], v[168:171], a[52:55]\n" \
"ds_read_b64_tr_b16 v[114:115], v39 offset:512\n" \
"v_accvgpr_read_b32 v181, a37\n" \
"v_accvgpr_read_b32 v182, a38\n" \
"v_accvgpr_read_b32 v183, a39\n" \
"v_mfma_f32_16x16x32_bf16 a[24:27], v[116:119], v[172:175], a[24:27]\n" \
"s_add_u32 m0, s30, 0x8000\n" \
"s_mov_b32 s84, 0x80\n" \
"v_cvt_pk_bf16_f32 v188, v188, v189\n" \
"v_cvt_pk_bf16_f32 v189, v190, v191\n" \
"v_mfma_f32_16x16x32_bf16 a[56:59], v[120:123], v[172:175], a[56:59]\n" \
"buffer_load_dwordx4 v24, s[76:79], s84 offen lds\n" \
"v_cvt_pk_bf16_f32 v190, v180, v181\n" \
"v_cvt_pk_bf16_f32 v191, v182, v183\n" \
"v_mfma_f32_16x16x32_bf16 a[28:31], v[116:119], v[176:179], a[28:31]\n" \
"ds_read_b64_tr_b16 v[84:85], v32 offset:0\n" \
"v_accvgpr_read_b32 v192, a8\n" \
"v_accvgpr_read_b32 v193, a9\n" \
"v_accvgpr_read_b32 v194, a10\n" \
"v_mfma_f32_16x16x32_bf16 a[60:63], v[120:123], v[176:179], a[60:63]\n" \
"ds_read_b64_tr_b16 v[86:87], v32 offset:1024\n" \
"v_accvgpr_read_b32 v195, a11\n" \
"v_accvgpr_read_b32 v180, a40\n" \
"v_accvgpr_read_b32 v181, a41\n" \
"v_mfma_f32_16x16x32_bf16 a[92:95], v[124:127], v[176:179], a[92:95]\n" \
"s_add_u32 m0, s30, 0x8400\n" \
"s_mov_b32 s85, 0x2480\n" \
"v_accvgpr_read_b32 v182, a42\n" \
"v_accvgpr_read_b32 v183, a43\n" \
"v_mfma_f32_16x16x32_bf16 a[124:127], v[128:131], v[176:179], a[124:127]\n" \
"buffer_load_dwordx4 v25, s[76:79], s85 offen lds\n" \
"v_cvt_pk_bf16_f32 v192, v192, v193\n" \
"v_cvt_pk_bf16_f32 v193, v194, v195\n" \
"v_mfma_f32_16x16x32_bf16 a[88:91], v[124:127], v[172:175], a[88:91]\n" \
"ds_read_b64_tr_b16 v[88:89], v33 offset:0\n" \
"v_cvt_pk_bf16_f32 v194, v180, v181\n" \
"v_cvt_pk_bf16_f32 v195, v182, v183\n" \
"v_accvgpr_read_b32 v196, a12\n" \
"v_mfma_f32_16x16x32_bf16 a[120:123], v[128:131], v[172:175], a[120:123]\n" \
"ds_read_b64_tr_b16 v[90:91], v33 offset:1024\n" \
"v_accvgpr_read_b32 v197, a13\n" \
"v_accvgpr_read_b32 v198, a14\n" \
"v_accvgpr_read_b32 v199, a15\n" \
"v_mfma_f32_16x16x32_bf16 a[84:87], v[124:127], v[168:171], a[84:87]\n" \
"s_add_u32 m0, s31, 0x8000\n" \
"s_mov_b32 s84, 0x80000\n" \
"v_accvgpr_read_b32 v180, a44\n" \
"v_accvgpr_read_b32 v181, a45\n" \
"v_mfma_f32_16x16x32_bf16 a[116:119], v[128:131], v[168:171], a[116:119]\n" \
"buffer_load_dwordx4 v26, s[68:71], s84 offen lds\n" \
"v_accvgpr_read_b32 v182, a46\n" \
"v_accvgpr_read_b32 v183, a47\n" \
"v_mfma_f32_16x16x32_bf16 a[80:83], v[124:127], v[164:167], a[80:83]\n" \
"ds_read_b64_tr_b16 v[92:93], v34 offset:0\n" \
"v_cvt_pk_bf16_f32 v196, v196, v197\n" \
"v_cvt_pk_bf16_f32 v197, v198, v199\n" \
"v_cvt_pk_bf16_f32 v198, v180, v181\n" \
"v_mfma_f32_16x16x32_bf16 a[112:115], v[128:131], v[164:167], a[112:115]\n" \
"ds_read_b64_tr_b16 v[94:95], v34 offset:1024\n" \
"v_cvt_pk_bf16_f32 v199, v182, v183\n" \
"v_accvgpr_read_b32 v200, a16\n" \
"v_accvgpr_read_b32 v201, a17\n" \
"v_mfma_f32_16x16x32_bf16 a[76:79], v[124:127], v[160:163], a[76:79]\n" \
"s_add_u32 m0, s30, 0x8800\n" \
"s_mov_b32 s85, 0x4880\n" \
"v_accvgpr_read_b32 v202, a18\n" \
"v_accvgpr_read_b32 v203, a19\n" \
"v_mfma_f32_16x16x32_bf16 a[108:111], v[128:131], v[160:163], a[108:111]\n" \
"buffer_load_dwordx4 v24, s[76:79], s85 offen lds\n" \
"v_accvgpr_read_b32 v180, a48\n" \
"v_accvgpr_read_b32 v181, a49\n" \
"v_mfma_f32_16x16x32_bf16 a[72:75], v[124:127], v[156:159], a[72:75]\n" \
"ds_read_b64_tr_b16 v[96:97], v35 offset:0\n" \
"v_accvgpr_read_b32 v182, a50\n" \
"v_accvgpr_read_b32 v183, a51\n" \
"v_cvt_pk_bf16_f32 v200, v200, v201\n" \
"v_mfma_f32_16x16x32_bf16 a[104:107], v[128:131], v[156:159], a[104:107]\n" \
"v_cvt_pk_bf16_f32 v201, v202, v203\n" \
"v_cvt_pk_bf16_f32 v202, v180, v181\n" \
"v_cvt_pk_bf16_f32 v203, v182, v183\n" \
"v_accvgpr_read_b32 v204, a20\n" \
"v_mfma_f32_16x16x32_bf16 a[68:71], v[124:127], v[152:155], a[68:71]\n" \
"s_add_u32 m0, s30, 0x8c00\n" \
"s_mov_b32 s84, 0x6c80\n" \
"v_accvgpr_read_b32 v205, a21\n" \
"v_accvgpr_read_b32 v206, a22\n" \
"v_mfma_f32_16x16x32_bf16 a[100:103], v[128:131], v[152:155], a[100:103]\n" \
"buffer_load_dwordx4 v25, s[76:79], s84 offen lds\n" \
"v_accvgpr_read_b32 v207, a23\n" \
"v_accvgpr_read_b32 v180, a52\n" \
"v_mfma_f32_16x16x32_bf16 a[64:67], v[124:127], v[148:151], a[64:67]\n" \
"ds_read_b64_tr_b16 v[98:99], v35 offset:1024\n" \
"v_accvgpr_read_b32 v181, a53\n" \
"v_accvgpr_read_b32 v182, a54\n" \
"v_accvgpr_read_b32 v183, a55\n" \
"v_mfma_f32_16x16x32_bf16 a[96:99], v[128:131], v[148:151], a[96:99]\n" \
"ds_read_b64_tr_b16 v[100:101], v36 offset:0\n" \
"v_cvt_pk_bf16_f32 v204, v204, v205\n" \
"v_cvt_pk_bf16_f32 v205, v206, v207\n" \
"v_cvt_pk_bf16_f32 v206, v180, v181\n" \
"v_mfma_f32_16x16x32_bf16 a[128:131], v[132:135], v[148:151], a[128:131]\n" \
"s_add_u32 m0, s31, 0x8400\n" \
"s_mov_b32 s85, 0x88000\n" \
"v_cvt_pk_bf16_f32 v207, v182, v183\n" \
"v_accvgpr_read_b32 v208, a24\n" \
"v_mfma_f32_16x16x32_bf16 a[160:163], v[136:139], v[148:151], a[160:163]\n" \
"buffer_load_dwordx4 v26, s[68:71], s85 offen lds\n" \
"v_accvgpr_read_b32 v209, a25\n" \
"v_accvgpr_read_b32 v210, a26\n" \
"v_mfma_f32_16x16x32_bf16 a[132:135], v[132:135], v[152:155], a[132:135]\n" \
"ds_read_b64_tr_b16 v[102:103], v36 offset:512\n" \
"v_accvgpr_read_b32 v211, a27\n" \
"v_accvgpr_read_b32 v180, a56\n" \
"v_accvgpr_read_b32 v181, a57\n" \
"v_mfma_f32_16x16x32_bf16 a[164:167], v[136:139], v[152:155], a[164:167]\n" \
"ds_read_b128 v[52:55], v28 offset:0\n" \
"v_accvgpr_read_b32 v182, a58\n" \
"v_accvgpr_read_b32 v183, a59\n" \
"v_cvt_pk_bf16_f32 v208, v208, v209\n" \
"v_mfma_f32_16x16x32_bf16 a[136:139], v[132:135], v[156:159], a[136:139]\n" \
"s_add_u32 m0, s30, 0x9000\n" \
"s_mov_b32 s84, 0x9080\n" \
"v_cvt_pk_bf16_f32 v209, v210, v211\n" \
"v_cvt_pk_bf16_f32 v210, v180, v181\n" \
"v_mfma_f32_16x16x32_bf16 a[168:171], v[136:139], v[156:159], a[168:171]\n" \
"buffer_load_dwordx4 v24, s[76:79], s84 offen lds\n" \
"v_cvt_pk_bf16_f32 v211, v182, v183\n" \
"v_accvgpr_read_b32 v212, a28\n" \
"v_mfma_f32_16x16x32_bf16 a[140:143], v[132:135], v[160:163], a[140:143]\n" \
"ds_read_b128 v[56:59], v28 offset:2048\n" \
"v_accvgpr_read_b32 v213, a29\n" \
"v_accvgpr_read_b32 v214, a30\n" \
"v_accvgpr_read_b32 v215, a31\n" \
"v_mfma_f32_16x16x32_bf16 a[172:175], v[136:139], v[160:163], a[172:175]\n" \
"ds_read_b128 v[60:63], v28 offset:4096\n" \
"v_accvgpr_read_b32 v180, a60\n" \
"v_accvgpr_read_b32 v181, a61\n" \
"v_accvgpr_read_b32 v182, a62\n" \
"v_mfma_f32_16x16x32_bf16 a[144:147], v[132:135], v[164:167], a[144:147]\n" \
"s_add_u32 m0, s30, 0x9400\n" \
"s_mov_b32 s85, 0xb480\n" \
"v_accvgpr_read_b32 v183, a63\n" \
"v_cvt_pk_bf16_f32 v212, v212, v213\n" \
"v_mfma_f32_16x16x32_bf16 a[176:179], v[136:139], v[164:167], a[176:179]\n" \
"buffer_load_dwordx4 v25, s[76:79], s85 offen lds\n" \
"v_cvt_pk_bf16_f32 v213, v214, v215\n" \
"v_cvt_pk_bf16_f32 v214, v180, v181\n" \
"v_mfma_f32_16x16x32_bf16 a[148:151], v[132:135], v[168:171], a[148:151]\n" \
"ds_read_b128 v[64:67], v28 offset:6144\n" \
"v_cvt_pk_bf16_f32 v215, v182, v183\n" \
"v_accvgpr_read_b32 v216, a64\n" \
"v_accvgpr_read_b32 v217, a65\n" \
"v_mfma_f32_16x16x32_bf16 a[180:183], v[136:139], v[168:171], a[180:183]\n" \
"ds_read_b128 v[68:71], v28 offset:8192\n" \
"v_accvgpr_read_b32 v218, a66\n" \
"v_accvgpr_read_b32 v219, a67\n" \
"v_accvgpr_read_b32 v180, a96\n" \
"v_mfma_f32_16x16x32_bf16 a[152:155], v[132:135], v[172:175], a[152:155]\n" \
"s_add_u32 m0, s31, 0x8800\n" \
"s_mov_b32 s84, 0x90000\n" \
"v_accvgpr_read_b32 v181, a97\n" \
"v_accvgpr_read_b32 v182, a98\n" \
"v_mfma_f32_16x16x32_bf16 a[184:187], v[136:139], v[172:175], a[184:187]\n" \
"buffer_load_dwordx4 v27, s[68:71], s84 offen lds\n" \
"v_accvgpr_read_b32 v183, a99\n" \
"v_cvt_pk_bf16_f32 v216, v216, v217\n" \
"v_mfma_f32_16x16x32_bf16 a[156:159], v[132:135], v[176:179], a[156:159]\n" \
"ds_read_b128 v[72:75], v28 offset:10240\n" \
"v_cvt_pk_bf16_f32 v217, v218, v219\n" \
"v_cvt_pk_bf16_f32 v218, v180, v181\n" \
"v_cvt_pk_bf16_f32 v219, v182, v183\n" \
"v_mfma_f32_16x16x32_bf16 a[188:191], v[136:139], v[176:179], a[188:191]\n" \
"ds_read_b128 v[76:79], v28 offset:12288\n" \
"v_accvgpr_read_b32 v220, a68\n" \
"v_accvgpr_read_b32 v221, a69\n" \
"v_accvgpr_read_b32 v222, a70\n" \
"v_mfma_f32_16x16x32_bf16 a[220:223], v[140:143], v[176:179], a[220:223]\n" \
"v_accvgpr_read_b32 v223, a71\n" \
"v_accvgpr_read_b32 v180, a100\n" \
"v_accvgpr_read_b32 v181, a101\n" \
"v_accvgpr_read_b32 v182, a102\n" \
"v_mfma_f32_16x16x32_bf16 a[252:255], v[144:147], v[176:179], a[252:255]\n" \
"ds_read_b128 v[80:83], v28 offset:14336\n" \
"v_accvgpr_read_b32 v183, a103\n" \
"v_cvt_pk_bf16_f32 v220, v220, v221\n" \
"v_cvt_pk_bf16_f32 v221, v222, v223\n" \
"v_mfma_f32_16x16x32_bf16 a[216:219], v[140:143], v[172:175], a[216:219]\n" \
"v_cvt_pk_bf16_f32 v222, v180, v181\n" \
"v_cvt_pk_bf16_f32 v223, v182, v183\n" \
"v_accvgpr_read_b32 v224, a72\n" \
"v_accvgpr_read_b32 v225, a73\n" \
"v_mfma_f32_16x16x32_bf16 a[248:251], v[144:147], v[172:175], a[248:251]\n" \
"v_accvgpr_read_b32 v226, a74\n" \
"v_accvgpr_read_b32 v227, a75\n" \
"v_accvgpr_read_b32 v180, a104\n" \
"v_accvgpr_read_b32 v181, a105\n" \
"v_mfma_f32_16x16x32_bf16 a[212:215], v[140:143], v[168:171], a[212:215]\n" \
"v_accvgpr_read_b32 v182, a106\n" \
"v_accvgpr_read_b32 v183, a107\n" \
"v_cvt_pk_bf16_f32 v224, v224, v225\n" \
"v_cvt_pk_bf16_f32 v225, v226, v227\n" \
"v_mfma_f32_16x16x32_bf16 a[244:247], v[144:147], v[168:171], a[244:247]\n" \
"v_cvt_pk_bf16_f32 v226, v180, v181\n" \
"v_cvt_pk_bf16_f32 v227, v182, v183\n" \
"v_accvgpr_read_b32 v228, a76\n" \
"v_accvgpr_read_b32 v229, a77\n" \
"v_mfma_f32_16x16x32_bf16 a[208:211], v[140:143], v[164:167], a[208:211]\n" \
"v_accvgpr_read_b32 v230, a78\n" \
"v_accvgpr_read_b32 v231, a79\n" \
"v_accvgpr_read_b32 v180, a108\n" \
"v_accvgpr_read_b32 v181, a109\n" \
"v_mfma_f32_16x16x32_bf16 a[240:243], v[144:147], v[164:167], a[240:243]\n" \
"v_accvgpr_read_b32 v182, a110\n" \
"v_accvgpr_read_b32 v183, a111\n" \
"v_cvt_pk_bf16_f32 v228, v228, v229\n" \
"v_cvt_pk_bf16_f32 v229, v230, v231\n" \
"v_mfma_f32_16x16x32_bf16 a[204:207], v[140:143], v[160:163], a[204:207]\n" \
"v_cvt_pk_bf16_f32 v230, v180, v181\n" \
"v_cvt_pk_bf16_f32 v231, v182, v183\n" \
"v_accvgpr_read_b32 v232, a80\n" \
"v_accvgpr_read_b32 v233, a81\n" \
"v_mfma_f32_16x16x32_bf16 a[236:239], v[144:147], v[160:163], a[236:239]\n" \
"v_accvgpr_read_b32 v234, a82\n" \
"v_accvgpr_read_b32 v235, a83\n" \
"v_accvgpr_read_b32 v180, a112\n" \
"v_accvgpr_read_b32 v181, a113\n" \
"v_mfma_f32_16x16x32_bf16 a[200:203], v[140:143], v[156:159], a[200:203]\n" \
"v_accvgpr_read_b32 v182, a114\n" \
"v_accvgpr_read_b32 v183, a115\n" \
"v_cvt_pk_bf16_f32 v232, v232, v233\n" \
"v_cvt_pk_bf16_f32 v233, v234, v235\n" \
"v_mfma_f32_16x16x32_bf16 a[232:235], v[144:147], v[156:159], a[232:235]\n" \
"v_cvt_pk_bf16_f32 v234, v180, v181\n" \
"v_cvt_pk_bf16_f32 v235, v182, v183\n" \
"v_accvgpr_read_b32 v236, a84\n" \
"v_accvgpr_read_b32 v237, a85\n" \
"v_mfma_f32_16x16x32_bf16 a[196:199], v[140:143], v[152:155], a[196:199]\n" \
"v_accvgpr_read_b32 v238, a86\n" \
"v_accvgpr_read_b32 v239, a87\n" \
"v_accvgpr_read_b32 v180, a116\n" \
"v_accvgpr_read_b32 v181, a117\n" \
"v_mfma_f32_16x16x32_bf16 a[228:231], v[144:147], v[152:155], a[228:231]\n" \
"v_accvgpr_read_b32 v182, a118\n" \
"v_accvgpr_read_b32 v183, a119\n" \
"v_cvt_pk_bf16_f32 v236, v236, v237\n" \
"v_cvt_pk_bf16_f32 v237, v238, v239\n" \
"v_mfma_f32_16x16x32_bf16 a[192:195], v[140:143], v[148:151], a[192:195]\n" \
"v_cvt_pk_bf16_f32 v238, v180, v181\n" \
"v_cvt_pk_bf16_f32 v239, v182, v183\n" \
"v_accvgpr_read_b32 v240, a88\n" \
"v_accvgpr_read_b32 v241, a89\n" \
"v_mfma_f32_16x16x32_bf16 a[224:227], v[144:147], v[148:151], a[224:227]\n" \
"v_accvgpr_read_b32 v242, a90\n" \
"v_accvgpr_read_b32 v243, a91\n" \
"v_accvgpr_read_b32 v180, a120\n" \
"v_accvgpr_read_b32 v181, a121\n" \
"s_waitcnt lgkmcnt(4)\n" \
"s_nop 0\n" \
"v_mfma_f32_16x16x32_bf16 a[24:27], v[104:107], v[64:67], 0\n" \
"s_add_u32 m0, s30, 0x9800\n" \
"s_mov_b32 s84, 0xd880\n" \
"v_accvgpr_read_b32 v182, a122\n" \
"v_accvgpr_read_b32 v183, a123\n" \
"v_cvt_pk_bf16_f32 v240, v240, v241\n" \
"v_mfma_f32_16x16x32_bf16 a[28:31], v[64:67], v[108:111], 0\n" \
"buffer_load_dwordx4 v24, s[76:79], s84 offen lds\n" \
"v_cvt_pk_bf16_f32 v241, v242, v243\n" \
"v_cvt_pk_bf16_f32 v242, v180, v181\n" \
"v_cvt_pk_bf16_f32 v243, v182, v183\n" \
"v_mfma_f32_16x16x32_bf16 a[56:59], v[112:115], v[64:67], 0\n" \
"ds_read_b64_tr_b16 v[168:169], v37 offset:4096\n" \
"v_accvgpr_read_b32 v244, a92\n" \
"v_accvgpr_read_b32 v245, a93\n" \
"v_accvgpr_read_b32 v246, a94\n" \
"v_accvgpr_read_b32 v247, a95\n" \
"v_mfma_f32_16x16x32_bf16 a[0:3], v[84:87], v[52:55], 0\n" \
"s_add_u32 m0, s30, 0x9c00\n" \
"s_mov_b32 s85, 0xfc80\n" \
"v_accvgpr_read_b32 v180, a124\n" \
"v_accvgpr_read_b32 v181, a125\n" \
"v_accvgpr_read_b32 v182, a126\n" \
"v_mfma_f32_16x16x32_bf16 a[32:35], v[84:87], v[56:59], 0\n" \
"buffer_load_dwordx4 v25, s[76:79], s85 offen lds\n" \
"v_accvgpr_read_b32 v183, a127\n" \
"v_cvt_pk_bf16_f32 v244, v244, v245\n" \
"v_cvt_pk_bf16_f32 v245, v246, v247\n" \
"v_mfma_f32_16x16x32_bf16 a[4:7], v[88:91], v[52:55], 0\n" \
"ds_read_b64_tr_b16 v[170:171], v37 offset:4608\n" \
"v_cvt_pk_bf16_f32 v246, v180, v181\n" \
"v_cvt_pk_bf16_f32 v247, v182, v183\n" \
"v_accvgpr_read_b32 v252, a152\n" \
"v_accvgpr_read_b32 v253, a153\n" \
"v_mfma_f32_16x16x32_bf16 a[36:39], v[88:91], v[56:59], 0\n" \
"s_add_u32 m0, s31, 0x8c00\n" \
"s_mov_b32 s84, 0x98000\n" \
"v_accvgpr_read_b32 v254, a154\n" \
"v_accvgpr_read_b32 v255, a155\n" \
"v_accvgpr_read_b32 v180, a184\n" \
"v_mfma_f32_16x16x32_bf16 a[8:11], v[92:95], v[52:55], 0\n" \
"buffer_load_dwordx4 v27, s[68:71], s84 offen lds\n" \
"v_accvgpr_read_b32 v181, a185\n" \
"v_accvgpr_read_b32 v182, a186\n" \
"v_accvgpr_read_b32 v183, a187\n" \
"v_mfma_f32_16x16x32_bf16 a[40:43], v[92:95], v[56:59], 0\n" \
"ds_read_b64_tr_b16 v[172:173], v38 offset:4096\n" \
"v_cvt_pk_bf16_f32 v252, v252, v253\n" \
"v_cvt_pk_bf16_f32 v253, v254, v255\n" \
"v_cvt_pk_bf16_f32 v254, v180, v181\n" \
"v_cvt_pk_bf16_f32 v255, v182, v183\n" \
"v_mfma_f32_16x16x32_bf16 a[12:15], v[96:99], v[52:55], 0\n" \
"s_add_u32 s89, s41, 0x0\n" \
"buffer_store_dwordx4 v[184:187], v20, s[80:83], s89 offen offset:0 sc1\n" \
"v_accvgpr_write_b32 a60, v252\n" \
"v_accvgpr_write_b32 a61, v253\n" \
"v_mfma_f32_16x16x32_bf16 a[44:47], v[96:99], v[56:59], 0\n" \
"ds_read_b64_tr_b16 v[174:175], v38 offset:4608\n" \
"v_accvgpr_write_b32 a62, v254\n" \
"v_accvgpr_write_b32 a63, v255\n" \
"v_accvgpr_read_b32 v252, a156\n" \
"v_accvgpr_read_b32 v253, a157\n" \
"s_waitcnt lgkmcnt(7)\n" \
"v_mfma_f32_16x16x32_bf16 a[16:19], v[100:103], v[68:71], 0\n" \
"ds_read_b64_tr_b16 v[176:177], v39 offset:4096\n" \
"v_accvgpr_read_b32 v254, a158\n" \
"v_accvgpr_read_b32 v255, a159\n" \
"v_accvgpr_read_b32 v180, a188\n" \
"v_accvgpr_read_b32 v181, a189\n" \
"v_mfma_f32_16x16x32_bf16 a[20:23], v[104:107], v[68:71], 0\n" \
"s_add_u32 s88, s41, 0x0\n" \
"s_nop 0\n" \
"buffer_store_dwordx4 v[188:191], v20, s[80:83], s88 offen offset:1024 sc1\n" \
"v_accvgpr_read_b32 v182, a190\n" \
"v_accvgpr_read_b32 v183, a191\n" \
"v_mfma_f32_16x16x32_bf16 a[52:55], v[112:115], v[68:71], 0\n" \
"ds_read_b64_tr_b16 v[178:179], v39 offset:4608\n" \
"v_cvt_pk_bf16_f32 v252, v252, v253\n" \
"v_cvt_pk_bf16_f32 v253, v254, v255\n" \
"v_cvt_pk_bf16_f32 v254, v180, v181\n" \
"v_cvt_pk_bf16_f32 v255, v182, v183\n" \
"v_mfma_f32_16x16x32_bf16 a[48:51], v[68:71], v[108:111], 0\n" \
"ds_read_b64_tr_b16 v[148:149], v32 offset:8192\n" \
"v_accvgpr_write_b32 a88, v252\n" \
"v_accvgpr_write_b32 a89, v253\n" \
"v_accvgpr_write_b32 a90, v254\n" \
"v_accvgpr_write_b32 a91, v255\n" \
"v_mfma_f32_16x16x32_bf16 a[64:67], v[84:87], v[60:63], 0\n" \
"s_add_u32 s89, s41, 0x0\n" \
"s_nop 0\n" \
"buffer_store_dwordx4 v[192:195], v20, s[80:83], s89 offen offset:2048 sc1\n" \
"v_accvgpr_read_b32 v252, a128\n" \
"v_accvgpr_read_b32 v253, a129\n" \
"v_mfma_f32_16x16x32_bf16 a[96:99], v[84:87], v[64:67], 0\n" \
"ds_read_b64_tr_b16 v[150:151], v32 offset:9216\n" \
"v_accvgpr_read_b32 v254, a130\n" \
"v_accvgpr_read_b32 v255, a131\n" \
"v_accvgpr_read_b32 v180, a160\n" \
"v_accvgpr_read_b32 v181, a161\n" \
"v_mfma_f32_16x16x32_bf16 a[68:71], v[88:91], v[60:63], 0\n" \
"ds_read_b64_tr_b16 v[152:153], v33 offset:8192\n" \
"v_accvgpr_read_b32 v182, a162\n" \
"v_accvgpr_read_b32 v183, a163\n" \
"v_cvt_pk_bf16_f32 v252, v252, v253\n" \
"v_cvt_pk_bf16_f32 v253, v254, v255\n" \
"v_mfma_f32_16x16x32_bf16 a[100:103], v[88:91], v[64:67], 0\n" \
"s_add_u32 s88, s41, 0x0\n" \
"s_nop 0\n" \
"buffer_store_dwordx4 v[196:199], v20, s[80:83], s88 offen offset:3072 sc1\n" \
"v_cvt_pk_bf16_f32 v254, v180, v181\n" \
"v_cvt_pk_bf16_f32 v255, v182, v183\n" \
"v_mfma_f32_16x16x32_bf16 a[72:75], v[92:95], v[60:63], 0\n" \
"ds_read_b64_tr_b16 v[154:155], v33 offset:9216\n" \
"v_accvgpr_write_b32 a120, v252\n" \
"v_accvgpr_write_b32 a121, v253\n" \
"v_accvgpr_write_b32 a122, v254\n" \
"v_accvgpr_write_b32 a123, v255\n" \
"v_mfma_f32_16x16x32_bf16 a[104:107], v[92:95], v[64:67], 0\n" \
"ds_read_b64_tr_b16 v[156:157], v34 offset:8192\n" \
"v_accvgpr_read_b32 v252, a132\n" \
"v_accvgpr_read_b32 v253, a133\n" \
"v_accvgpr_read_b32 v254, a134\n" \
"v_accvgpr_read_b32 v255, a135\n" \
"v_mfma_f32_16x16x32_bf16 a[76:79], v[96:99], v[60:63], 0\n" \
"ds_read_b64_tr_b16 v[158:159], v34 offset:9216\n" \
"v_accvgpr_read_b32 v180, a164\n" \
"v_accvgpr_read_b32 v181, a165\n" \
"v_accvgpr_read_b32 v182, a166\n" \
"v_accvgpr_read_b32 v183, a167\n" \
"v_mfma_f32_16x16x32_bf16 a[108:111], v[96:99], v[64:67], 0\n" \
"ds_read_b64_tr_b16 v[160:161], v35 offset:8192\n" \
"v_cvt_pk_bf16_f32 v252, v252, v253\n" \
"v_cvt_pk_bf16_f32 v253, v254, v255\n" \
"v_cvt_pk_bf16_f32 v254, v180, v181\n" \
"v_cvt_pk_bf16_f32 v255, v182, v183\n" \
"s_waitcnt lgkmcnt(15)\n" \
"s_nop 0\n" \
"v_mfma_f32_16x16x32_bf16 a[80:83], v[100:103], v[72:75], 0\n" \
"ds_read_b64_tr_b16 v[162:163], v35 offset:9216\n" \
"v_accvgpr_write_b32 a92, v252\n" \
"v_accvgpr_write_b32 a93, v253\n" \
"v_accvgpr_write_b32 a94, v254\n" \
"v_accvgpr_write_b32 a95, v255\n" \
"v_mfma_f32_16x16x32_bf16 a[84:87], v[104:107], v[72:75], 0\n" \
"ds_read_b64_tr_b16 v[164:165], v36 offset:4096\n" \
"v_accvgpr_read_b32 v252, a136\n" \
"v_accvgpr_read_b32 v253, a137\n" \
"v_accvgpr_read_b32 v254, a138\n" \
"v_accvgpr_read_b32 v255, a139\n" \
"v_mfma_f32_16x16x32_bf16 a[116:119], v[112:115], v[72:75], 0\n" \
"ds_read_b64_tr_b16 v[166:167], v36 offset:4608\n" \
"v_accvgpr_read_b32 v180, a168\n" \
"v_accvgpr_read_b32 v181, a169\n" \
"v_accvgpr_read_b32 v182, a170\n" \
"v_accvgpr_read_b32 v183, a171\n" \
"v_mfma_f32_16x16x32_bf16 a[112:115], v[72:75], v[108:111], 0\n" \
"ds_read_b128 v[116:119], v29 offset:0\n" \
"v_cvt_pk_bf16_f32 v252, v252, v253\n" \
"v_cvt_pk_bf16_f32 v253, v254, v255\n" \
"v_cvt_pk_bf16_f32 v254, v180, v181\n" \
"v_cvt_pk_bf16_f32 v255, v182, v183\n" \
"v_mfma_f32_16x16x32_bf16 a[128:131], v[84:87], v[68:71], 0\n" \
"ds_read_b128 v[120:123], v29 offset:2048\n" \
"v_accvgpr_write_b32 a124, v252\n" \
"v_accvgpr_write_b32 a125, v253\n" \
"v_accvgpr_write_b32 a126, v254\n" \
"v_accvgpr_write_b32 a127, v255\n" \
"v_mfma_f32_16x16x32_bf16 a[160:163], v[84:87], v[72:75], 0\n" \
"v_accvgpr_read_b32 v252, a140\n" \
"v_accvgpr_read_b32 v253, a141\n" \
"v_accvgpr_read_b32 v254, a142\n" \
"v_accvgpr_read_b32 v255, a143\n" \
"v_accvgpr_read_b32 v180, a172\n" \
"v_mfma_f32_16x16x32_bf16 a[132:135], v[88:91], v[68:71], 0\n" \
"ds_read_b128 v[124:127], v29 offset:4096\n" \
"v_accvgpr_read_b32 v181, a173\n" \
"v_accvgpr_read_b32 v182, a174\n" \
"v_accvgpr_read_b32 v183, a175\n" \
"v_cvt_pk_bf16_f32 v252, v252, v253\n" \
"v_mfma_f32_16x16x32_bf16 a[164:167], v[88:91], v[72:75], 0\n" \
"ds_read_b128 v[128:131], v29 offset:6144\n" \
"v_cvt_pk_bf16_f32 v253, v254, v255\n" \
"v_cvt_pk_bf16_f32 v254, v180, v181\n" \
"v_cvt_pk_bf16_f32 v255, v182, v183\n" \
"v_accvgpr_write_b32 a152, v252\n" \
"v_mfma_f32_16x16x32_bf16 a[136:139], v[92:95], v[68:71], 0\n" \
"v_accvgpr_write_b32 a153, v253\n" \
"v_accvgpr_write_b32 a154, v254\n" \
"v_accvgpr_write_b32 a155, v255\n" \
"v_accvgpr_read_b32 v252, a144\n" \
"v_accvgpr_read_b32 v253, a145\n" \
"v_mfma_f32_16x16x32_bf16 a[168:171], v[92:95], v[72:75], 0\n" \
"ds_read_b128 v[132:135], v29 offset:8192\n" \
"v_accvgpr_read_b32 v254, a146\n" \
"v_accvgpr_read_b32 v255, a147\n" \
"v_accvgpr_read_b32 v180, a176\n" \
"v_accvgpr_read_b32 v181, a177\n" \
"v_mfma_f32_16x16x32_bf16 a[140:143], v[96:99], v[68:71], 0\n" \
"v_accvgpr_read_b32 v182, a178\n" \
"v_accvgpr_read_b32 v183, a179\n" \
"v_cvt_pk_bf16_f32 v252, v252, v253\n" \
"v_cvt_pk_bf16_f32 v253, v254, v255\n" \
"v_cvt_pk_bf16_f32 v254, v180, v181\n" \
"v_mfma_f32_16x16x32_bf16 a[172:175], v[96:99], v[72:75], 0\n" \
"ds_read_b128 v[136:139], v29 offset:10240\n" \
"v_cvt_pk_bf16_f32 v255, v182, v183\n" \
"v_accvgpr_write_b32 a184, v252\n" \
"v_accvgpr_write_b32 a185, v253\n" \
"v_accvgpr_write_b32 a186, v254\n" \
"v_accvgpr_write_b32 a187, v255\n" \
"s_waitcnt lgkmcnt(15)\n" \
"s_nop 0\n" \
"v_mfma_f32_16x16x32_bf16 a[144:147], v[100:103], v[76:79], 0\n" \
"ds_read_b128 v[140:143], v29 offset:12288\n" \
"v_accvgpr_read_b32 v252, a148\n" \
"v_accvgpr_read_b32 v253, a149\n" \
"v_accvgpr_read_b32 v254, a150\n" \
"v_accvgpr_read_b32 v255, a151\n" \
"v_accvgpr_read_b32 v180, a180\n" \
"v_accvgpr_read_b32 v181, a181\n" \
"v_accvgpr_read_b32 v182, a182\n" \
"v_accvgpr_read_b32 v183, a183\n" \
"v_cvt_pk_bf16_f32 v252, v252, v253\n" \
"v_cvt_pk_bf16_f32 v253, v254, v255\n" \
"v_cvt_pk_bf16_f32 v254, v180, v181\n" \
"v_cvt_pk_bf16_f32 v255, v182, v183\n" \
"v_accvgpr_write_b32 a156, v252\n" \
"v_accvgpr_write_b32 a157, v253\n" \
"v_accvgpr_write_b32 a158, v254\n" \
"v_accvgpr_write_b32 a159, v255\n" \
"v_mfma_f32_16x16x32_bf16 a[148:151], v[104:107], v[76:79], 0\n" \
"v_accvgpr_read_b32 v252, a216\n" \
"v_accvgpr_read_b32 v253, a217\n" \
"v_accvgpr_read_b32 v254, a218\n" \
"v_accvgpr_read_b32 v255, a219\n" \
"v_accvgpr_read_b32 v180, a248\n" \
"v_mfma_f32_16x16x32_bf16 a[180:183], v[112:115], v[76:79], 0\n" \
"ds_read_b128 v[144:147], v29 offset:14336\n" \
"v_accvgpr_read_b32 v181, a249\n" \
"v_accvgpr_read_b32 v182, a250\n" \
"v_accvgpr_read_b32 v183, a251\n" \
"v_cvt_pk_bf16_f32 v252, v252, v253\n" \
"v_mfma_f32_16x16x32_bf16 a[176:179], v[76:79], v[108:111], 0\n" \
"v_cvt_pk_bf16_f32 v253, v254, v255\n" \
"v_cvt_pk_bf16_f32 v254, v180, v181\n" \
"v_cvt_pk_bf16_f32 v255, v182, v183\n" \
"v_accvgpr_write_b32 a188, v252\n" \
"v_accvgpr_write_b32 a189, v253\n" \
"v_accvgpr_write_b32 a190, v254\n" \
"v_accvgpr_write_b32 a191, v255\n" \
"v_accvgpr_read_b32 v252, a220\n" \
"v_accvgpr_read_b32 v253, a221\n" \
"v_accvgpr_read_b32 v254, a222\n" \
"v_accvgpr_read_b32 v255, a223\n" \
"v_accvgpr_read_b32 v180, a252\n" \
"v_accvgpr_read_b32 v181, a253\n" \
"v_accvgpr_read_b32 v182, a254\n" \
"v_accvgpr_read_b32 v183, a255\n" \
"v_cvt_pk_bf16_f32 v252, v252, v253\n" \
"v_cvt_pk_bf16_f32 v253, v254, v255\n" \
"v_cvt_pk_bf16_f32 v254, v180, v181\n" \
"v_cvt_pk_bf16_f32 v255, v182, v183\n" \
"v_accvgpr_write_b32 a216, v252\n" \
"v_accvgpr_write_b32 a217, v253\n" \
"v_accvgpr_write_b32 a218, v254\n" \
"v_accvgpr_write_b32 a219, v255\n" \
"v_accvgpr_read_b32 v252, a192\n" \
"v_accvgpr_read_b32 v253, a193\n" \
"v_accvgpr_read_b32 v254, a194\n" \
"v_accvgpr_read_b32 v255, a195\n" \
"v_accvgpr_read_b32 v180, a224\n" \
"v_accvgpr_read_b32 v181, a225\n" \
"v_accvgpr_read_b32 v182, a226\n" \
"v_accvgpr_read_b32 v183, a227\n" \
"v_cvt_pk_bf16_f32 v252, v252, v253\n" \
"v_cvt_pk_bf16_f32 v253, v254, v255\n" \
"v_cvt_pk_bf16_f32 v254, v180, v181\n" \
"v_cvt_pk_bf16_f32 v255, v182, v183\n" \
"v_accvgpr_write_b32 a248, v252\n" \
"v_accvgpr_write_b32 a249, v253\n" \
"v_accvgpr_write_b32 a250, v254\n" \
"v_accvgpr_write_b32 a251, v255\n" \
"v_mfma_f32_16x16x32_bf16 a[192:195], v[84:87], v[76:79], 0\n" \
"v_accvgpr_read_b32 v252, a196\n" \
"v_accvgpr_read_b32 v253, a197\n" \
"v_accvgpr_read_b32 v254, a198\n" \
"v_accvgpr_read_b32 v255, a199\n" \
"v_accvgpr_read_b32 v180, a228\n" \
"v_mfma_f32_16x16x32_bf16 a[224:227], v[84:87], v[80:83], 0\n" \
"v_accvgpr_read_b32 v181, a229\n" \
"v_accvgpr_read_b32 v182, a230\n" \
"v_accvgpr_read_b32 v183, a231\n" \
"v_cvt_pk_bf16_f32 v252, v252, v253\n" \
"v_cvt_pk_bf16_f32 v253, v254, v255\n" \
"v_cvt_pk_bf16_f32 v254, v180, v181\n" \
"v_cvt_pk_bf16_f32 v255, v182, v183\n" \
"v_accvgpr_write_b32 a220, v252\n" \
"v_accvgpr_write_b32 a221, v253\n" \
"v_accvgpr_write_b32 a222, v254\n" \
"v_accvgpr_write_b32 a223, v255\n" \
"v_mfma_f32_16x16x32_bf16 a[196:199], v[88:91], v[76:79], 0\n" \
"v_accvgpr_read_b32 v252, a200\n" \
"v_accvgpr_read_b32 v253, a201\n" \
"v_accvgpr_read_b32 v254, a202\n" \
"v_accvgpr_read_b32 v255, a203\n" \
"v_accvgpr_read_b32 v180, a232\n" \
"v_mfma_f32_16x16x32_bf16 a[228:231], v[88:91], v[80:83], 0\n" \
"v_accvgpr_read_b32 v181, a233\n" \
"v_accvgpr_read_b32 v182, a234\n" \
"v_accvgpr_read_b32 v183, a235\n" \
"v_cvt_pk_bf16_f32 v252, v252, v253\n" \
"v_cvt_pk_bf16_f32 v253, v254, v255\n" \
"v_cvt_pk_bf16_f32 v254, v180, v181\n" \
"v_cvt_pk_bf16_f32 v255, v182, v183\n" \
"v_accvgpr_write_b32 a252, v252\n" \
"v_accvgpr_write_b32 a253, v253\n" \
"v_accvgpr_write_b32 a254, v254\n" \
"v_accvgpr_write_b32 a255, v255\n" \
"v_mfma_f32_16x16x32_bf16 a[200:203], v[92:95], v[76:79], 0\n" \
"v_accvgpr_read_b32 v184, a204\n" \
"v_accvgpr_read_b32 v185, a205\n" \
"v_accvgpr_read_b32 v186, a206\n" \
"v_accvgpr_read_b32 v187, a207\n" \
"v_accvgpr_read_b32 v180, a236\n" \
"v_mfma_f32_16x16x32_bf16 a[232:235], v[92:95], v[80:83], 0\n" \
"v_accvgpr_read_b32 v181, a237\n" \
"v_accvgpr_read_b32 v182, a238\n" \
"v_accvgpr_read_b32 v183, a239\n" \
"v_cvt_pk_bf16_f32 v184, v184, v185\n" \
"v_cvt_pk_bf16_f32 v185, v186, v187\n" \
"v_cvt_pk_bf16_f32 v186, v180, v181\n" \
"v_cvt_pk_bf16_f32 v187, v182, v183\n" \
"v_mfma_f32_16x16x32_bf16 a[204:207], v[96:99], v[76:79], 0\n" \
"v_accvgpr_read_b32 v188, a208\n" \
"v_accvgpr_read_b32 v189, a209\n" \
"v_accvgpr_read_b32 v190, a210\n" \
"v_accvgpr_read_b32 v191, a211\n" \
"v_accvgpr_read_b32 v180, a240\n" \
"v_mfma_f32_16x16x32_bf16 a[236:239], v[96:99], v[80:83], 0\n" \
"v_accvgpr_read_b32 v181, a241\n" \
"v_accvgpr_read_b32 v182, a242\n" \
"v_accvgpr_read_b32 v183, a243\n" \
"v_cvt_pk_bf16_f32 v188, v188, v189\n" \
"v_cvt_pk_bf16_f32 v189, v190, v191\n" \
"v_cvt_pk_bf16_f32 v190, v180, v181\n" \
"v_cvt_pk_bf16_f32 v191, v182, v183\n" \
"v_mfma_f32_16x16x32_bf16 a[208:211], v[100:103], v[80:83], 0\n" \
"v_accvgpr_read_b32 v252, a212\n" \
"v_accvgpr_read_b32 v253, a213\n" \
"v_accvgpr_read_b32 v254, a214\n" \
"v_accvgpr_read_b32 v255, a215\n" \
"v_accvgpr_read_b32 v180, a244\n" \
"v_accvgpr_read_b32 v181, a245\n" \
"v_accvgpr_read_b32 v182, a246\n" \
"v_accvgpr_read_b32 v183, a247\n" \
"v_cvt_pk_bf16_f32 v252, v252, v253\n" \
"v_cvt_pk_bf16_f32 v253, v254, v255\n" \
"v_cvt_pk_bf16_f32 v254, v180, v181\n" \
"v_cvt_pk_bf16_f32 v255, v182, v183\n" \
"v_mfma_f32_16x16x32_bf16 a[212:215], v[104:107], v[80:83], 0\n" \
"v_mfma_f32_16x16x32_bf16 a[244:247], v[112:115], v[80:83], 0\n" \
"v_mfma_f32_16x16x32_bf16 a[240:243], v[80:83], v[108:111], 0\n" \
"s_waitcnt vmcnt(4) lgkmcnt(0)\n" \
"s_barrier\n" \
"v_mfma_f32_16x16x32_bf16 a[24:27], v[168:171], v[128:131], a[24:27]\n" \
"s_add_u32 m0, s40, 0x0\n" \
"s_nop 0\n" \
"s_add_u32 s85, s39, 0x4000\n" \
"v_mfma_f32_16x16x32_bf16 a[28:31], v[128:131], v[172:175], a[28:31]\n" \
"buffer_load_dwordx4 v20, s[80:83], s85 offen sc1 lds\n" \
"v_mfma_f32_16x16x32_bf16 a[56:59], v[176:179], v[128:131], a[56:59]\n" \
"ds_read_b64_tr_b16 v[104:105], v37 offset:32768\n" \
"v_mfma_f32_16x16x32_bf16 a[0:3], v[148:151], v[116:119], a[0:3]\n" \
"ds_read_b64_tr_b16 v[106:107], v37 offset:33280\n" \
"v_mfma_f32_16x16x32_bf16 a[32:35], v[148:151], v[120:123], a[32:35]\n" \
"s_add_u32 m0, s40, 0x1000\n" \
"s_add_u32 s84, s39, 0x5000\n" \
"v_mfma_f32_16x16x32_bf16 a[4:7], v[152:155], v[116:119], a[4:7]\n" \
"buffer_load_dwordx4 v20, s[80:83], s84 offen sc1 lds\n" \
"v_mfma_f32_16x16x32_bf16 a[36:39], v[152:155], v[120:123], a[36:39]\n" \
"ds_read_b64_tr_b16 v[108:109], v38 offset:32768\n" \
"v_mfma_f32_16x16x32_bf16 a[8:11], v[156:159], v[116:119], a[8:11]\n" \
"ds_read_b64_tr_b16 v[110:111], v38 offset:33280\n" \
"v_mfma_f32_16x16x32_bf16 a[40:43], v[156:159], v[120:123], a[40:43]\n" \
"s_add_u32 m0, s40, 0x3000\n" \
"s_mov_b32 s85, 0x100\n" \
"v_mfma_f32_16x16x32_bf16 a[12:15], v[160:163], v[116:119], a[12:15]\n" \
"buffer_load_dwordx4 v21, s[76:79], s85 offen lds\n" \
"v_mfma_f32_16x16x32_bf16 a[44:47], v[160:163], v[120:123], a[44:47]\n" \
"ds_read_b64_tr_b16 v[112:113], v39 offset:32768\n" \
"v_mfma_f32_16x16x32_bf16 a[16:19], v[164:167], v[132:135], a[16:19]\n" \
"ds_read_b64_tr_b16 v[114:115], v39 offset:33280\n" \
"v_mfma_f32_16x16x32_bf16 a[20:23], v[168:171], v[132:135], a[20:23]\n" \
"s_add_u32 m0, s30, 0x0\n" \
"s_nop 0\n" \
"s_mov_b32 s84, 0x100\n" \
"v_mfma_f32_16x16x32_bf16 a[52:55], v[176:179], v[132:135], a[52:55]\n" \
"buffer_load_dwordx4 v24, s[76:79], s84 offen lds\n" \
"v_mfma_f32_16x16x32_bf16 a[48:51], v[132:135], v[172:175], a[48:51]\n" \
"ds_read_b64_tr_b16 v[84:85], v32 offset:32768\n" \
"v_mfma_f32_16x16x32_bf16 a[64:67], v[148:151], v[124:127], a[64:67]\n" \
"ds_read_b64_tr_b16 v[86:87], v32 offset:33792\n" \
"v_mfma_f32_16x16x32_bf16 a[96:99], v[148:151], v[128:131], a[96:99]\n" \
"ds_read_b64_tr_b16 v[88:89], v33 offset:32768\n" \
"v_mfma_f32_16x16x32_bf16 a[68:71], v[152:155], v[124:127], a[68:71]\n" \
"s_add_u32 m0, s30, 0x400\n" \
"s_mov_b32 s85, 0x2500\n" \
"v_mfma_f32_16x16x32_bf16 a[100:103], v[152:155], v[128:131], a[100:103]\n" \
"buffer_load_dwordx4 v25, s[76:79], s85 offen lds\n" \
"v_mfma_f32_16x16x32_bf16 a[72:75], v[156:159], v[124:127], a[72:75]\n" \
"ds_read_b64_tr_b16 v[90:91], v33 offset:33792\n" \
"v_mfma_f32_16x16x32_bf16 a[104:107], v[156:159], v[128:131], a[104:107]\n" \
"ds_read_b64_tr_b16 v[92:93], v34 offset:32768\n" \
"v_mfma_f32_16x16x32_bf16 a[76:79], v[160:163], v[124:127], a[76:79]\n" \
"s_add_u32 m0, s31, 0x0\n" \
"s_nop 0\n" \
"s_mov_b32 s84, 0x100000\n" \
"v_mfma_f32_16x16x32_bf16 a[108:111], v[160:163], v[128:131], a[108:111]\n" \
"buffer_load_dwordx4 v26, s[68:71], s84 offen lds\n" \
"v_mfma_f32_16x16x32_bf16 a[80:83], v[164:167], v[136:139], a[80:83]\n" \
"ds_read_b64_tr_b16 v[94:95], v34 offset:33792\n" \
"v_mfma_f32_16x16x32_bf16 a[84:87], v[168:171], v[136:139], a[84:87]\n" \
"ds_read_b64_tr_b16 v[96:97], v35 offset:32768\n" \
"v_mfma_f32_16x16x32_bf16 a[116:119], v[176:179], v[136:139], a[116:119]\n" \
"s_add_u32 m0, s30, 0x800\n" \
"s_mov_b32 s85, 0x4900\n" \
"v_mfma_f32_16x16x32_bf16 a[112:115], v[136:139], v[172:175], a[112:115]\n" \
"buffer_load_dwordx4 v24, s[76:79], s85 offen lds\n" \
"v_mfma_f32_16x16x32_bf16 a[128:131], v[148:151], v[132:135], a[128:131]\n" \
"ds_read_b64_tr_b16 v[98:99], v35 offset:33792\n" \
"v_mfma_f32_16x16x32_bf16 a[160:163], v[148:151], v[136:139], a[160:163]\n" \
"ds_read_b64_tr_b16 v[100:101], v36 offset:32768\n" \
"v_mfma_f32_16x16x32_bf16 a[132:135], v[152:155], v[132:135], a[132:135]\n" \
"s_add_u32 m0, s30, 0xc00\n" \
"s_mov_b32 s84, 0x6d00\n" \
"v_mfma_f32_16x16x32_bf16 a[164:167], v[152:155], v[136:139], a[164:167]\n" \
"buffer_load_dwordx4 v25, s[76:79], s84 offen lds\n" \
"v_mfma_f32_16x16x32_bf16 a[136:139], v[156:159], v[132:135], a[136:139]\n" \
"ds_read_b64_tr_b16 v[102:103], v36 offset:33280\n" \
"v_mfma_f32_16x16x32_bf16 a[168:171], v[156:159], v[136:139], a[168:171]\n" \
"ds_read_b128 v[52:55], v28 offset:32768\n" \
"v_mfma_f32_16x16x32_bf16 a[140:143], v[160:163], v[132:135], a[140:143]\n" \
"ds_read_b128 v[56:59], v28 offset:34816\n" \
"v_mfma_f32_16x16x32_bf16 a[172:175], v[160:163], v[136:139], a[172:175]\n" \
"s_add_u32 m0, s31, 0x400\n" \
"s_mov_b32 s85, 0x108000\n" \
"v_mfma_f32_16x16x32_bf16 a[144:147], v[164:167], v[140:143], a[144:147]\n" \
"buffer_load_dwordx4 v26, s[68:71], s85 offen lds\n" \
"v_mfma_f32_16x16x32_bf16 a[148:151], v[168:171], v[140:143], a[148:151]\n" \
"ds_read_b128 v[60:63], v28 offset:36864\n" \
"v_mfma_f32_16x16x32_bf16 a[180:183], v[176:179], v[140:143], a[180:183]\n" \
"ds_read_b128 v[64:67], v28 offset:38912\n" \
"v_mfma_f32_16x16x32_bf16 a[176:179], v[140:143], v[172:175], a[176:179]\n" \
"s_add_u32 m0, s30, 0x1000\n" \
"s_mov_b32 s84, 0x9100\n" \
"v_mfma_f32_16x16x32_bf16 a[192:195], v[148:151], v[140:143], a[192:195]\n" \
"buffer_load_dwordx4 v24, s[76:79], s84 offen lds\n" \
"v_mfma_f32_16x16x32_bf16 a[224:227], v[148:151], v[144:147], a[224:227]\n" \
"ds_read_b128 v[68:71], v28 offset:40960\n" \
"v_mfma_f32_16x16x32_bf16 a[196:199], v[152:155], v[140:143], a[196:199]\n" \
"ds_read_b128 v[72:75], v28 offset:43008\n" \
"v_mfma_f32_16x16x32_bf16 a[228:231], v[152:155], v[144:147], a[228:231]\n" \
"s_add_u32 m0, s30, 0x1400\n" \
"s_mov_b32 s85, 0xb500\n" \
"v_mfma_f32_16x16x32_bf16 a[200:203], v[156:159], v[140:143], a[200:203]\n" \
"buffer_load_dwordx4 v25, s[76:79], s85 offen lds\n" \
"v_mfma_f32_16x16x32_bf16 a[232:235], v[156:159], v[144:147], a[232:235]\n" \
"ds_read_b128 v[76:79], v28 offset:45056\n" \
"v_mfma_f32_16x16x32_bf16 a[204:207], v[160:163], v[140:143], a[204:207]\n" \
"ds_read_b128 v[80:83], v28 offset:47104\n" \
"v_mfma_f32_16x16x32_bf16 a[236:239], v[160:163], v[144:147], a[236:239]\n" \
"s_add_u32 m0, s31, 0x800\n" \
"s_mov_b32 s84, 0x110000\n" \
"v_mfma_f32_16x16x32_bf16 a[208:211], v[164:167], v[144:147], a[208:211]\n" \
"buffer_load_dwordx4 v27, s[68:71], s84 offen lds\n" \
"v_mfma_f32_16x16x32_bf16 a[212:215], v[168:171], v[144:147], a[212:215]\n" \
"v_mfma_f32_16x16x32_bf16 a[244:247], v[176:179], v[144:147], a[244:247]\n" \
"v_mfma_f32_16x16x32_bf16 a[240:243], v[144:147], v[172:175], a[240:243]\n" \
"s_waitcnt lgkmcnt(4)\n" \
"v_mfma_f32_16x16x32_bf16 a[24:27], v[104:107], v[64:67], a[24:27]\n" \
"s_add_u32 m0, s30, 0x1800\n" \
"s_mov_b32 s84, 0xd900\n" \
"v_mfma_f32_16x16x32_bf16 a[28:31], v[64:67], v[108:111], a[28:31]\n" \
"buffer_load_dwordx4 v24, s[76:79], s84 offen lds\n" \
"v_mfma_f32_16x16x32_bf16 a[56:59], v[112:115], v[64:67], a[56:59]\n" \
"ds_read_b64_tr_b16 v[168:169], v37 offset:36864\n" \
"s_movk_i32 s15, 0x4000\n" \
"v_mfma_f32_16x16x32_bf16 a[0:3], v[84:87], v[52:55], a[0:3]\n" \
"s_add_u32 m0, s30, 0x1c00\n" \
"s_mov_b32 s85, 0xfd00\n" \
"v_mfma_f32_16x16x32_bf16 a[32:35], v[84:87], v[56:59], a[32:35]\n" \
"buffer_load_dwordx4 v25, s[76:79], s85 offen lds\n" \
"v_mfma_f32_16x16x32_bf16 a[4:7], v[88:91], v[52:55], a[4:7]\n" \
"ds_read_b64_tr_b16 v[170:171], v37 offset:37376\n" \
"s_lshl_b32 s4, s2, 2\n" \
"v_mfma_f32_16x16x32_bf16 a[36:39], v[88:91], v[56:59], a[36:39]\n" \
"s_add_u32 m0, s31, 0xc00\n" \
"s_mov_b32 s84, 0x118000\n" \
"v_mfma_f32_16x16x32_bf16 a[8:11], v[92:95], v[52:55], a[8:11]\n" \
"buffer_load_dwordx4 v27, s[68:71], s84 offen lds\n" \
"v_mfma_f32_16x16x32_bf16 a[40:43], v[92:95], v[56:59], a[40:43]\n" \
"ds_read_b64_tr_b16 v[172:173], v38 offset:36864\n" \
"s_and_b32 s4, s4, 28\n" \
"v_mfma_f32_16x16x32_bf16 a[12:15], v[96:99], v[52:55], a[12:15]\n" \
"ds_read_b64_tr_b16 v[174:175], v38 offset:37376\n" \
"s_bfe_u32 s5, s2, 0x20003\n" \
"v_mfma_f32_16x16x32_bf16 a[44:47], v[96:99], v[56:59], a[44:47]\n" \
"ds_read_b64_tr_b16 v[176:177], v39 offset:36864\n" \
"s_or_b32 s4, s4, s5\n" \
"s_waitcnt lgkmcnt(8)\n" \
"v_mfma_f32_16x16x32_bf16 a[16:19], v[100:103], v[68:71], a[16:19]\n" \
"ds_read_b64_tr_b16 v[178:179], v39 offset:37376\n" \
"s_lshr_b32 s5, s2, 5\n" \
"v_mfma_f32_16x16x32_bf16 a[20:23], v[104:107], v[68:71], a[20:23]\n" \
"ds_read_b64_tr_b16 v[148:149], v32 offset:40960\n" \
"s_lshr_b32 s19, s3, 1\n" \
"v_mfma_f32_16x16x32_bf16 a[52:55], v[112:115], v[68:71], a[52:55]\n" \
"ds_read_b64_tr_b16 v[150:151], v32 offset:41984\n" \
"s_lshl_b32 s18, s5, 1\n" \
"v_mfma_f32_16x16x32_bf16 a[48:51], v[68:71], v[108:111], a[48:51]\n" \
"ds_read_b64_tr_b16 v[152:153], v33 offset:40960\n" \
"s_lshl_b32 s6, s4, 20\n" \
"v_mfma_f32_16x16x32_bf16 a[64:67], v[84:87], v[60:63], a[64:67]\n" \
"ds_read_b64_tr_b16 v[154:155], v33 offset:41984\n" \
"s_lshl_b32 s7, s4, 8\n" \
"v_mfma_f32_16x16x32_bf16 a[96:99], v[84:87], v[64:67], a[96:99]\n" \
"ds_read_b64_tr_b16 v[156:157], v34 offset:40960\n" \
"s_and_b32 s13, s3, 1\n" \
"v_mfma_f32_16x16x32_bf16 a[68:71], v[88:91], v[60:63], a[68:71]\n" \
"ds_read_b64_tr_b16 v[158:159], v34 offset:41984\n" \
"s_add_i32 s14, s18, s19\n" \
"v_mfma_f32_16x16x32_bf16 a[100:103], v[88:91], v[64:67], a[100:103]\n" \
"s_add_u32 s6, s26, s6\n" \
"s_addc_u32 s9, s27, 0\n" \
"v_mfma_f32_16x16x32_bf16 a[72:75], v[92:95], v[60:63], a[72:75]\n" \
"ds_read_b64_tr_b16 v[160:161], v35 offset:40960\n" \
"v_mfma_f32_16x16x32_bf16 a[104:107], v[92:95], v[64:67], a[104:107]\n" \
"s_add_u32 s8, s6, 0x1000000\n" \
"s_addc_u32 s6, s9, 0\n" \
"v_mfma_f32_16x16x32_bf16 a[76:79], v[96:99], v[60:63], a[76:79]\n" \
"s_add_u32 s89, s41, 0x1000\n" \
"buffer_store_dwordx4 v[200:203], v20, s[80:83], s89 offen offset:0 sc1\n" \
"v_mfma_f32_16x16x32_bf16 a[108:111], v[96:99], v[64:67], a[108:111]\n" \
"ds_read_b64_tr_b16 v[162:163], v35 offset:41984\n" \
"s_and_b32 s9, s6, 0xffff\n" \
"s_waitcnt lgkmcnt(15)\n" \
"v_mfma_f32_16x16x32_bf16 a[80:83], v[100:103], v[72:75], a[80:83]\n" \
"ds_read_b64_tr_b16 v[164:165], v36 offset:36864\n" \
"s_lshl_b32 s6, s5, 16\n" \
"v_mfma_f32_16x16x32_bf16 a[84:87], v[104:107], v[72:75], a[84:87]\n" \
"ds_read_b64_tr_b16 v[166:167], v36 offset:37376\n" \
"s_sub_i32 s10, 0x100000, s6\n" \
"v_mfma_f32_16x16x32_bf16 a[116:119], v[112:115], v[72:75], a[116:119]\n" \
"s_lshl_b32 s6, s3, 11\n" \
"s_or_b32 s12, s6, 0xc000\n" \
"v_mfma_f32_16x16x32_bf16 a[112:115], v[72:75], v[108:111], a[112:115]\n" \
"ds_read_b128 v[116:119], v29 offset:32768\n" \
"s_mov_b32 s6, 0x8000\n" \
"v_mfma_f32_16x16x32_bf16 a[128:131], v[84:87], v[68:71], a[128:131]\n" \
"ds_read_b128 v[120:123], v29 offset:34816\n" \
"v_mfma_f32_16x16x32_bf16 a[160:163], v[84:87], v[72:75], a[160:163]\n" \
"s_cmp_lg_u32 s5, 0\n" \
"s_cselect_b32 s17, s15, s6\n" \
"v_mfma_f32_16x16x32_bf16 a[132:135], v[88:91], v[68:71], a[132:135]\n" \
"s_add_u32 s88, s41, 0x1000\n" \
"buffer_store_dwordx4 v[204:207], v20, s[80:83], s88 offen offset:1024 sc1\n" \
"v_mfma_f32_16x16x32_bf16 a[164:167], v[88:91], v[72:75], a[164:167]\n" \
"ds_read_b128 v[124:127], v29 offset:36864\n" \
"s_lshl_b32 s6, s3, 1\n" \
"v_mfma_f32_16x16x32_bf16 a[136:139], v[92:95], v[68:71], a[136:139]\n" \
"ds_read_b128 v[128:131], v29 offset:38912\n" \
"s_lshl_b32 s5, s5, 3\n" \
"v_mfma_f32_16x16x32_bf16 a[168:171], v[92:95], v[72:75], a[168:171]\n" \
"ds_read_b128 v[132:135], v29 offset:40960\n" \
"s_and_b32 s6, s6, 4\n" \
"v_mfma_f32_16x16x32_bf16 a[140:143], v[96:99], v[68:71], a[140:143]\n" \
"s_or_b32 s15, s5, s6\n" \
"s_sub_i32 s5, s13, s5\n" \
"v_mfma_f32_16x16x32_bf16 a[172:175], v[96:99], v[72:75], a[172:175]\n" \
"ds_read_b128 v[136:139], v29 offset:43008\n" \
"s_or_b32 s5, s5, s6\n" \
"v_mfma_f32_16x16x32_bf16 a[144:147], v[100:103], v[76:79], a[144:147]\n" \
"ds_read_b128 v[140:143], v29 offset:45056\n" \
"s_or_b32 s6, s15, s13\n" \
"v_mfma_f32_16x16x32_bf16 a[148:151], v[104:107], v[76:79], a[148:151]\n" \
"s_add_u32 s89, s41, 0x1000\n" \
"buffer_store_dwordx4 v[208:211], v20, s[80:83], s89 offen offset:2048 sc1\n" \
"v_mfma_f32_16x16x32_bf16 a[180:183], v[112:115], v[76:79], a[180:183]\n" \
"ds_read_b128 v[144:147], v29 offset:47104\n" \
"s_lshl_b32 s4, s4, 2\n" \
"v_mfma_f32_16x16x32_bf16 a[176:179], v[76:79], v[108:111], a[176:179]\n" \
"s_mulk_i32 s6, 0x92\n" \
"s_add_i32 s4, s4, 18\n" \
"v_mfma_f32_16x16x32_bf16 a[192:195], v[84:87], v[76:79], a[192:195]\n" \
"s_mulk_i32 s5, 0x92\n" \
"s_lshl_b32 s16, s19, 14\n" \
"s_waitcnt lgkmcnt(15)\n" \
"v_mfma_f32_16x16x32_bf16 a[224:227], v[84:87], v[80:83], a[224:227]\n" \
"s_lshl_b32 s15, s13, 12\n" \
"s_add_i32 s6, s4, s6\n" \
"v_mfma_f32_16x16x32_bf16 a[196:199], v[88:91], v[76:79], a[196:199]\n" \
"s_add_i32 s5, s5, s4\n" \
"s_mul_i32 s19, s19, 0xe000\n" \
"v_mfma_f32_16x16x32_bf16 a[228:231], v[88:91], v[80:83], a[228:231]\n" \
"s_mov_b32 s11, 0x20000\n" \
"s_lshl_b32 s4, s3, 8\n" \
"v_mfma_f32_16x16x32_bf16 a[200:203], v[92:95], v[76:79], a[200:203]\n" \
"s_add_u32 s88, s41, 0x1000\n" \
"buffer_store_dwordx4 v[212:215], v20, s[80:83], s88 offen offset:3072 sc1\n" \
"v_mfma_f32_16x16x32_bf16 a[232:235], v[92:95], v[80:83], a[232:235]\n" \
"s_or_b32 s22, s15, s16\n" \
"s_or_b32 s23, s19, s15\n" \
"v_mfma_f32_16x16x32_bf16 a[204:207], v[96:99], v[76:79], a[204:207]\n" \
"s_lshl_b32 s60, s6, 10\n" \
"s_lshl_b32 s61, s5, 10\n" \
"v_mfma_f32_16x16x32_bf16 a[236:239], v[96:99], v[80:83], a[236:239]\n" \
"s_lshl_b32 s14, s14, 15\n" \
"s_or_b32 s20, s4, 0x26c00\n" \
"v_mfma_f32_16x16x32_bf16 a[208:211], v[100:103], v[80:83], a[208:211]\n" \
"s_or_b32 s13, s22, 0x8000\n" \
"s_add_i32 s22, s22, 0x14000\n" \
"v_mfma_f32_16x16x32_bf16 a[212:215], v[104:107], v[80:83], a[212:215]\n" \
"s_bitset1_b32 s23, 16\n" \
"s_or_b32 s24, s7, 0x5be8020\n" \
"v_mfma_f32_16x16x32_bf16 a[244:247], v[112:115], v[80:83], a[244:247]\n" \
"s_and_b32 s57, s27, 0xffff\n" \
"s_mov_b32 s58, 0x6000000\n" \
"v_mfma_f32_16x16x32_bf16 a[240:243], v[80:83], v[108:111], a[240:243]\n" \
"s_waitcnt vmcnt(4) lgkmcnt(0)\n" \
"s_barrier\n" \
"v_mfma_f32_16x16x32_bf16 a[24:27], v[168:171], v[128:131], a[24:27]\n" \
"s_add_u32 m0, s40, 0x8000\n" \
"s_add_u32 s85, s39, 0x6000\n" \
"v_mfma_f32_16x16x32_bf16 a[28:31], v[128:131], v[172:175], a[28:31]\n" \
"buffer_load_dwordx4 v20, s[80:83], s85 offen sc1 lds\n" \
"v_mfma_f32_16x16x32_bf16 a[56:59], v[176:179], v[128:131], a[56:59]\n" \
"ds_read_b64_tr_b16 v[104:105], v37 offset:0\n" \
"s_mov_b32 s56, s26\n" \
"v_mfma_f32_16x16x32_bf16 a[0:3], v[148:151], v[116:119], a[0:3]\n" \
"ds_read_b64_tr_b16 v[106:107], v37 offset:512\n" \
"s_mov_b32 s59, s11\n" \
"v_mfma_f32_16x16x32_bf16 a[32:35], v[148:151], v[120:123], a[32:35]\n" \
"s_add_u32 m0, s40, 0x9000\n" \
"s_add_u32 s84, s39, 0x7000\n" \
"v_mfma_f32_16x16x32_bf16 a[4:7], v[152:155], v[116:119], a[4:7]\n" \
"buffer_load_dwordx4 v20, s[80:83], s84 offen sc1 lds\n" \
"v_mfma_f32_16x16x32_bf16 a[36:39], v[152:155], v[120:123], a[36:39]\n" \
"ds_read_b64_tr_b16 v[108:109], v38 offset:0\n" \
"s_add_i32 s60, s60, 0x3000000\n" \
"v_mfma_f32_16x16x32_bf16 a[8:11], v[156:159], v[116:119], a[8:11]\n" \
"ds_read_b64_tr_b16 v[110:111], v38 offset:512\n" \
"s_add_i32 s61, s61, 0x411c000\n" \
"v_mfma_f32_16x16x32_bf16 a[40:43], v[156:159], v[120:123], a[40:43]\n" \
"s_add_u32 m0, s40, 0xb000\n" \
"s_mov_b32 s85, 0x180\n" \
"v_mfma_f32_16x16x32_bf16 a[12:15], v[160:163], v[116:119], a[12:15]\n" \
"buffer_load_dwordx4 v21, s[76:79], s85 offen lds\n" \
"v_mfma_f32_16x16x32_bf16 a[44:47], v[160:163], v[120:123], a[44:47]\n" \
"ds_read_b64_tr_b16 v[112:113], v39 offset:0\n" \
"s_sub_i32 s19, 14, s18\n" \
"s_nop 0\n" \
"v_mfma_f32_16x16x32_bf16 a[16:19], v[164:167], v[132:135], a[16:19]\n" \
"ds_read_b64_tr_b16 v[114:115], v39 offset:512\n" \
"s_mov_b32 s21, 0x71800000\n" \
"v_mfma_f32_16x16x32_bf16 a[20:23], v[168:171], v[132:135], a[20:23]\n" \
"s_add_u32 m0, s30, 0x8000\n" \
"s_mov_b32 s84, 0x180\n" \
"v_mfma_f32_16x16x32_bf16 a[52:55], v[176:179], v[132:135], a[52:55]\n" \
"buffer_load_dwordx4 v24, s[76:79], s84 offen lds\n" \
"v_mfma_f32_16x16x32_bf16 a[48:51], v[132:135], v[172:175], a[48:51]\n" \
"ds_read_b64_tr_b16 v[84:85], v32 offset:0\n" \
"v_mfma_f32_16x16x32_bf16 a[64:67], v[148:151], v[124:127], a[64:67]\n" \
"ds_read_b64_tr_b16 v[86:87], v32 offset:1024\n" \
"v_mfma_f32_16x16x32_bf16 a[96:99], v[148:151], v[128:131], a[96:99]\n" \
"ds_read_b64_tr_b16 v[88:89], v33 offset:0\n" \
"v_mfma_f32_16x16x32_bf16 a[68:71], v[152:155], v[124:127], a[68:71]\n" \
"s_add_u32 m0, s30, 0x8400\n" \
"s_mov_b32 s85, 0x2580\n" \
"v_mfma_f32_16x16x32_bf16 a[100:103], v[152:155], v[128:131], a[100:103]\n" \
"buffer_load_dwordx4 v25, s[76:79], s85 offen lds\n" \
"v_mfma_f32_16x16x32_bf16 a[72:75], v[156:159], v[124:127], a[72:75]\n" \
"ds_read_b64_tr_b16 v[90:91], v33 offset:1024\n" \
"v_mfma_f32_16x16x32_bf16 a[104:107], v[156:159], v[128:131], a[104:107]\n" \
"ds_read_b64_tr_b16 v[92:93], v34 offset:0\n" \
"v_mfma_f32_16x16x32_bf16 a[76:79], v[160:163], v[124:127], a[76:79]\n" \
"s_add_u32 m0, s31, 0x8000\n" \
"s_mov_b32 s84, 0x180000\n" \
"v_mfma_f32_16x16x32_bf16 a[108:111], v[160:163], v[128:131], a[108:111]\n" \
"buffer_load_dwordx4 v26, s[68:71], s84 offen lds\n" \
"v_mfma_f32_16x16x32_bf16 a[80:83], v[164:167], v[136:139], a[80:83]\n" \
"ds_read_b64_tr_b16 v[94:95], v34 offset:1024\n" \
"v_mfma_f32_16x16x32_bf16 a[84:87], v[168:171], v[136:139], a[84:87]\n" \
"ds_read_b64_tr_b16 v[96:97], v35 offset:0\n" \
"v_mfma_f32_16x16x32_bf16 a[116:119], v[176:179], v[136:139], a[116:119]\n" \
"s_add_u32 m0, s30, 0x8800\n" \
"s_mov_b32 s85, 0x4980\n" \
"v_mfma_f32_16x16x32_bf16 a[112:115], v[136:139], v[172:175], a[112:115]\n" \
"buffer_load_dwordx4 v24, s[76:79], s85 offen lds\n" \
"v_mfma_f32_16x16x32_bf16 a[128:131], v[148:151], v[132:135], a[128:131]\n" \
"ds_read_b64_tr_b16 v[98:99], v35 offset:1024\n" \
"v_mfma_f32_16x16x32_bf16 a[160:163], v[148:151], v[136:139], a[160:163]\n" \
"ds_read_b64_tr_b16 v[100:101], v36 offset:0\n" \
"v_mfma_f32_16x16x32_bf16 a[132:135], v[152:155], v[132:135], a[132:135]\n" \
"s_add_u32 m0, s30, 0x8c00\n" \
"s_mov_b32 s84, 0x6d80\n" \
"v_mfma_f32_16x16x32_bf16 a[164:167], v[152:155], v[136:139], a[164:167]\n" \
"buffer_load_dwordx4 v25, s[76:79], s84 offen lds\n" \
"v_mfma_f32_16x16x32_bf16 a[136:139], v[156:159], v[132:135], a[136:139]\n" \
"ds_read_b64_tr_b16 v[102:103], v36 offset:512\n" \
"v_mfma_f32_16x16x32_bf16 a[168:171], v[156:159], v[136:139], a[168:171]\n" \
"ds_read_b128 v[52:55], v28 offset:0\n" \
"v_mfma_f32_16x16x32_bf16 a[140:143], v[160:163], v[132:135], a[140:143]\n" \
"ds_read_b128 v[56:59], v28 offset:2048\n" \
"v_mfma_f32_16x16x32_bf16 a[172:175], v[160:163], v[136:139], a[172:175]\n" \
"s_add_u32 m0, s31, 0x8400\n" \
"s_mov_b32 s85, 0x188000\n" \
"v_mfma_f32_16x16x32_bf16 a[144:147], v[164:167], v[140:143], a[144:147]\n" \
"buffer_load_dwordx4 v26, s[68:71], s85 offen lds\n" \
"v_mfma_f32_16x16x32_bf16 a[148:151], v[168:171], v[140:143], a[148:151]\n" \
"ds_read_b128 v[60:63], v28 offset:4096\n" \
"v_mfma_f32_16x16x32_bf16 a[180:183], v[176:179], v[140:143], a[180:183]\n" \
"ds_read_b128 v[64:67], v28 offset:6144\n" \
"v_mfma_f32_16x16x32_bf16 a[176:179], v[140:143], v[172:175], a[176:179]\n" \
"s_add_u32 m0, s30, 0x9000\n" \
"s_mov_b32 s84, 0x9180\n" \
"v_mfma_f32_16x16x32_bf16 a[192:195], v[148:151], v[140:143], a[192:195]\n" \
"buffer_load_dwordx4 v24, s[76:79], s84 offen lds\n" \
"v_mfma_f32_16x16x32_bf16 a[224:227], v[148:151], v[144:147], a[224:227]\n" \
"ds_read_b128 v[68:71], v28 offset:8192\n" \
"v_mfma_f32_16x16x32_bf16 a[196:199], v[152:155], v[140:143], a[196:199]\n" \
"ds_read_b128 v[72:75], v28 offset:10240\n" \
"v_mfma_f32_16x16x32_bf16 a[228:231], v[152:155], v[144:147], a[228:231]\n" \
"s_add_u32 m0, s30, 0x9400\n" \
"s_mov_b32 s85, 0xb580\n" \
"v_mfma_f32_16x16x32_bf16 a[200:203], v[156:159], v[140:143], a[200:203]\n" \
"buffer_load_dwordx4 v25, s[76:79], s85 offen lds\n" \
"v_mfma_f32_16x16x32_bf16 a[232:235], v[156:159], v[144:147], a[232:235]\n" \
"ds_read_b128 v[76:79], v28 offset:12288\n" \
"v_mfma_f32_16x16x32_bf16 a[204:207], v[160:163], v[140:143], a[204:207]\n" \
"ds_read_b128 v[80:83], v28 offset:14336\n" \
"v_mfma_f32_16x16x32_bf16 a[236:239], v[160:163], v[144:147], a[236:239]\n" \
"s_add_u32 m0, s31, 0x8800\n" \
"s_mov_b32 s84, 0x190000\n" \
"v_mfma_f32_16x16x32_bf16 a[208:211], v[164:167], v[144:147], a[208:211]\n" \
"buffer_load_dwordx4 v27, s[68:71], s84 offen lds\n" \
"v_mfma_f32_16x16x32_bf16 a[212:215], v[168:171], v[144:147], a[212:215]\n" \
"v_mfma_f32_16x16x32_bf16 a[244:247], v[176:179], v[144:147], a[244:247]\n" \
"v_mfma_f32_16x16x32_bf16 a[240:243], v[144:147], v[172:175], a[240:243]\n" \
"s_waitcnt lgkmcnt(4)\n" \
"s_nop 0\n" \
"v_mfma_f32_16x16x32_bf16 a[24:27], v[104:107], v[64:67], a[24:27]\n" \
"s_add_u32 m0, s30, 0x9800\n" \
"s_mov_b32 s84, 0xd980\n" \
"v_mfma_f32_16x16x32_bf16 a[28:31], v[64:67], v[108:111], a[28:31]\n" \
"buffer_load_dwordx4 v24, s[76:79], s84 offen lds\n" \
"v_mfma_f32_16x16x32_bf16 a[56:59], v[112:115], v[64:67], a[56:59]\n" \
"ds_read_b64_tr_b16 v[168:169], v37 offset:4096\n" \
"v_mfma_f32_16x16x32_bf16 a[0:3], v[84:87], v[52:55], a[0:3]\n" \
"s_add_u32 m0, s30, 0x9c00\n" \
"s_mov_b32 s85, 0xfd80\n" \
"v_mfma_f32_16x16x32_bf16 a[32:35], v[84:87], v[56:59], a[32:35]\n" \
"buffer_load_dwordx4 v25, s[76:79], s85 offen lds\n" \
"v_mfma_f32_16x16x32_bf16 a[4:7], v[88:91], v[52:55], a[4:7]\n" \
"ds_read_b64_tr_b16 v[170:171], v37 offset:4608\n" \
"v_mfma_f32_16x16x32_bf16 a[36:39], v[88:91], v[56:59], a[36:39]\n" \
"s_add_u32 m0, s31, 0x8c00\n" \
"s_mov_b32 s84, 0x198000\n" \
"v_mfma_f32_16x16x32_bf16 a[8:11], v[92:95], v[52:55], a[8:11]\n" \
"buffer_load_dwordx4 v27, s[68:71], s84 offen lds\n" \
"v_mfma_f32_16x16x32_bf16 a[40:43], v[92:95], v[56:59], a[40:43]\n" \
"ds_read_b64_tr_b16 v[172:173], v38 offset:4096\n" \
"v_mfma_f32_16x16x32_bf16 a[12:15], v[96:99], v[52:55], a[12:15]\n" \
"ds_read_b64_tr_b16 v[174:175], v38 offset:4608\n" \
"v_mfma_f32_16x16x32_bf16 a[44:47], v[96:99], v[56:59], a[44:47]\n" \
"ds_read_b64_tr_b16 v[176:177], v39 offset:4096\n" \
"s_waitcnt lgkmcnt(8)\n" \
"s_nop 0\n" \
"v_mfma_f32_16x16x32_bf16 a[16:19], v[100:103], v[68:71], a[16:19]\n" \
"ds_read_b64_tr_b16 v[178:179], v39 offset:4608\n" \
"v_mfma_f32_16x16x32_bf16 a[20:23], v[104:107], v[68:71], a[20:23]\n" \
"ds_read_b64_tr_b16 v[148:149], v32 offset:8192\n" \
"v_mfma_f32_16x16x32_bf16 a[52:55], v[112:115], v[68:71], a[52:55]\n" \
"ds_read_b64_tr_b16 v[150:151], v32 offset:9216\n" \
"v_mfma_f32_16x16x32_bf16 a[48:51], v[68:71], v[108:111], a[48:51]\n" \
"ds_read_b64_tr_b16 v[152:153], v33 offset:8192\n" \
"v_mfma_f32_16x16x32_bf16 a[64:67], v[84:87], v[60:63], a[64:67]\n" \
"ds_read_b64_tr_b16 v[154:155], v33 offset:9216\n" \
"v_mfma_f32_16x16x32_bf16 a[96:99], v[84:87], v[64:67], a[96:99]\n" \
"ds_read_b64_tr_b16 v[156:157], v34 offset:8192\n" \
"v_mfma_f32_16x16x32_bf16 a[68:71], v[88:91], v[60:63], a[68:71]\n" \
"ds_read_b64_tr_b16 v[158:159], v34 offset:9216\n" \
"v_mfma_f32_16x16x32_bf16 a[100:103], v[88:91], v[64:67], a[100:103]\n" \
"v_mfma_f32_16x16x32_bf16 a[72:75], v[92:95], v[60:63], a[72:75]\n" \
"ds_read_b64_tr_b16 v[160:161], v35 offset:8192\n" \
"v_mfma_f32_16x16x32_bf16 a[104:107], v[92:95], v[64:67], a[104:107]\n" \
"v_mfma_f32_16x16x32_bf16 a[76:79], v[96:99], v[60:63], a[76:79]\n" \
"s_add_u32 s89, s41, 0x2000\n" \
"buffer_store_dwordx4 v[216:219], v20, s[80:83], s89 offen offset:0 sc1\n" \
"v_mfma_f32_16x16x32_bf16 a[108:111], v[96:99], v[64:67], a[108:111]\n" \
"ds_read_b64_tr_b16 v[162:163], v35 offset:9216\n" \
"s_waitcnt lgkmcnt(15)\n" \
"s_nop 0\n" \
"v_mfma_f32_16x16x32_bf16 a[80:83], v[100:103], v[72:75], a[80:83]\n" \
"ds_read_b64_tr_b16 v[164:165], v36 offset:4096\n" \
"v_mfma_f32_16x16x32_bf16 a[84:87], v[104:107], v[72:75], a[84:87]\n" \
"ds_read_b64_tr_b16 v[166:167], v36 offset:4608\n" \
"v_mfma_f32_16x16x32_bf16 a[116:119], v[112:115], v[72:75], a[116:119]\n" \
"v_mfma_f32_16x16x32_bf16 a[112:115], v[72:75], v[108:111], a[112:115]\n" \
"ds_read_b128 v[116:119], v29 offset:0\n" \
"v_mfma_f32_16x16x32_bf16 a[128:131], v[84:87], v[68:71], a[128:131]\n" \
"ds_read_b128 v[120:123], v29 offset:2048\n" \
"v_mfma_f32_16x16x32_bf16 a[160:163], v[84:87], v[72:75], a[160:163]\n" \
"v_mfma_f32_16x16x32_bf16 a[132:135], v[88:91], v[68:71], a[132:135]\n" \
"s_add_u32 s88, s41, 0x2000\n" \
"buffer_store_dwordx4 v[220:223], v20, s[80:83], s88 offen offset:1024 sc1\n" \
"v_mfma_f32_16x16x32_bf16 a[164:167], v[88:91], v[72:75], a[164:167]\n" \
"ds_read_b128 v[124:127], v29 offset:4096\n" \
"v_mfma_f32_16x16x32_bf16 a[136:139], v[92:95], v[68:71], a[136:139]\n" \
"ds_read_b128 v[128:131], v29 offset:6144\n" \
"v_mfma_f32_16x16x32_bf16 a[168:171], v[92:95], v[72:75], a[168:171]\n" \
"ds_read_b128 v[132:135], v29 offset:8192\n" \
"v_mfma_f32_16x16x32_bf16 a[140:143], v[96:99], v[68:71], a[140:143]\n" \
"v_mfma_f32_16x16x32_bf16 a[172:175], v[96:99], v[72:75], a[172:175]\n" \
"ds_read_b128 v[136:139], v29 offset:10240\n" \
"v_mfma_f32_16x16x32_bf16 a[144:147], v[100:103], v[76:79], a[144:147]\n" \
"ds_read_b128 v[140:143], v29 offset:12288\n" \
"v_mfma_f32_16x16x32_bf16 a[148:151], v[104:107], v[76:79], a[148:151]\n" \
"s_add_u32 s89, s41, 0x2000\n" \
"buffer_store_dwordx4 v[224:227], v20, s[80:83], s89 offen offset:2048 sc1\n" \
"v_mfma_f32_16x16x32_bf16 a[180:183], v[112:115], v[76:79], a[180:183]\n" \
"ds_read_b128 v[144:147], v29 offset:14336\n" \
"v_mfma_f32_16x16x32_bf16 a[176:179], v[76:79], v[108:111], a[176:179]\n" \
"v_mfma_f32_16x16x32_bf16 a[192:195], v[84:87], v[76:79], a[192:195]\n" \
"s_waitcnt lgkmcnt(15)\n" \
"s_nop 0\n" \
"v_mfma_f32_16x16x32_bf16 a[224:227], v[84:87], v[80:83], a[224:227]\n" \
"v_mfma_f32_16x16x32_bf16 a[196:199], v[88:91], v[76:79], a[196:199]\n" \
"v_mfma_f32_16x16x32_bf16 a[228:231], v[88:91], v[80:83], a[228:231]\n" \
"v_mfma_f32_16x16x32_bf16 a[200:203], v[92:95], v[76:79], a[200:203]\n" \
"s_add_u32 s88, s41, 0x2000\n" \
"buffer_store_dwordx4 v[228:231], v20, s[80:83], s88 offen offset:3072 sc1\n" \
"v_mfma_f32_16x16x32_bf16 a[232:235], v[92:95], v[80:83], a[232:235]\n" \
"v_mfma_f32_16x16x32_bf16 a[204:207], v[96:99], v[76:79], a[204:207]\n" \
"v_mfma_f32_16x16x32_bf16 a[236:239], v[96:99], v[80:83], a[236:239]\n" \
"v_mfma_f32_16x16x32_bf16 a[208:211], v[100:103], v[80:83], a[208:211]\n" \
"v_mfma_f32_16x16x32_bf16 a[212:215], v[104:107], v[80:83], a[212:215]\n" \
"v_mfma_f32_16x16x32_bf16 a[244:247], v[112:115], v[80:83], a[244:247]\n" \
"v_mfma_f32_16x16x32_bf16 a[240:243], v[80:83], v[108:111], a[240:243]\n" \
"s_waitcnt vmcnt(4) lgkmcnt(0)\n" \
"s_barrier\n" \
"v_mfma_f32_16x16x32_bf16 a[24:27], v[168:171], v[128:131], a[24:27]\n" \
"s_add_u32 m0, s40, 0x0\n" \
"s_nop 0\n" \
"s_add_u32 s85, s39, 0x8000\n" \
"v_mfma_f32_16x16x32_bf16 a[28:31], v[128:131], v[172:175], a[28:31]\n" \
"buffer_load_dwordx4 v20, s[80:83], s85 offen sc1 lds\n" \
"v_mfma_f32_16x16x32_bf16 a[56:59], v[176:179], v[128:131], a[56:59]\n" \
"ds_read_b64_tr_b16 v[104:105], v37 offset:32768\n" \
"v_mfma_f32_16x16x32_bf16 a[0:3], v[148:151], v[116:119], a[0:3]\n" \
"ds_read_b64_tr_b16 v[106:107], v37 offset:33280\n" \
"v_mfma_f32_16x16x32_bf16 a[32:35], v[148:151], v[120:123], a[32:35]\n" \
"s_add_u32 m0, s40, 0x1000\n" \
"s_add_u32 s84, s39, 0x9000\n" \
"v_mfma_f32_16x16x32_bf16 a[4:7], v[152:155], v[116:119], a[4:7]\n" \
"buffer_load_dwordx4 v20, s[80:83], s84 offen sc1 lds\n" \
"v_mfma_f32_16x16x32_bf16 a[36:39], v[152:155], v[120:123], a[36:39]\n" \
"ds_read_b64_tr_b16 v[108:109], v38 offset:32768\n" \
"v_mfma_f32_16x16x32_bf16 a[8:11], v[156:159], v[116:119], a[8:11]\n" \
"ds_read_b64_tr_b16 v[110:111], v38 offset:33280\n" \
"v_mfma_f32_16x16x32_bf16 a[40:43], v[156:159], v[120:123], a[40:43]\n" \
"s_add_u32 m0, s40, 0x3000\n" \
"s_mov_b32 s85, 0x200\n" \
"v_mfma_f32_16x16x32_bf16 a[12:15], v[160:163], v[116:119], a[12:15]\n" \
"buffer_load_dwordx4 v21, s[76:79], s85 offen lds\n" \
"v_mfma_f32_16x16x32_bf16 a[44:47], v[160:163], v[120:123], a[44:47]\n" \
"ds_read_b64_tr_b16 v[112:113], v39 offset:32768\n" \
"v_mfma_f32_16x16x32_bf16 a[16:19], v[164:167], v[132:135], a[16:19]\n" \
"ds_read_b64_tr_b16 v[114:115], v39 offset:33280\n" \
"v_mfma_f32_16x16x32_bf16 a[20:23], v[168:171], v[132:135], a[20:23]\n" \
"s_add_u32 m0, s30, 0x0\n" \
"s_nop 0\n" \
"s_mov_b32 s84, 0x200\n" \
"v_mfma_f32_16x16x32_bf16 a[52:55], v[176:179], v[132:135], a[52:55]\n" \
"buffer_load_dwordx4 v24, s[76:79], s84 offen lds\n" \
"v_mfma_f32_16x16x32_bf16 a[48:51], v[132:135], v[172:175], a[48:51]\n" \
"ds_read_b64_tr_b16 v[84:85], v32 offset:32768\n" \
"v_mfma_f32_16x16x32_bf16 a[64:67], v[148:151], v[124:127], a[64:67]\n" \
"ds_read_b64_tr_b16 v[86:87], v32 offset:33792\n" \
"v_mfma_f32_16x16x32_bf16 a[96:99], v[148:151], v[128:131], a[96:99]\n" \
"ds_read_b64_tr_b16 v[88:89], v33 offset:32768\n" \
"v_mfma_f32_16x16x32_bf16 a[68:71], v[152:155], v[124:127], a[68:71]\n" \
"s_add_u32 m0, s30, 0x400\n" \
"s_mov_b32 s85, 0x2600\n" \
"v_mfma_f32_16x16x32_bf16 a[100:103], v[152:155], v[128:131], a[100:103]\n" \
"buffer_load_dwordx4 v25, s[76:79], s85 offen lds\n" \
"v_mfma_f32_16x16x32_bf16 a[72:75], v[156:159], v[124:127], a[72:75]\n" \
"ds_read_b64_tr_b16 v[90:91], v33 offset:33792\n" \
"v_mfma_f32_16x16x32_bf16 a[104:107], v[156:159], v[128:131], a[104:107]\n" \
"ds_read_b64_tr_b16 v[92:93], v34 offset:32768\n" \
"v_mfma_f32_16x16x32_bf16 a[76:79], v[160:163], v[124:127], a[76:79]\n" \
"s_add_u32 m0, s31, 0x0\n" \
"s_nop 0\n" \
"s_mov_b32 s84, 0x200000\n" \
"v_mfma_f32_16x16x32_bf16 a[108:111], v[160:163], v[128:131], a[108:111]\n" \
"buffer_load_dwordx4 v26, s[68:71], s84 offen lds\n" \
"v_mfma_f32_16x16x32_bf16 a[80:83], v[164:167], v[136:139], a[80:83]\n" \
"ds_read_b64_tr_b16 v[94:95], v34 offset:33792\n" \
"v_mfma_f32_16x16x32_bf16 a[84:87], v[168:171], v[136:139], a[84:87]\n" \
"ds_read_b64_tr_b16 v[96:97], v35 offset:32768\n" \
"v_mfma_f32_16x16x32_bf16 a[116:119], v[176:179], v[136:139], a[116:119]\n" \
"s_add_u32 m0, s30, 0x800\n" \
"s_mov_b32 s85, 0x4a00\n" \
"v_mfma_f32_16x16x32_bf16 a[112:115], v[136:139], v[172:175], a[112:115]\n" \
"buffer_load_dwordx4 v24, s[76:79], s85 offen lds\n" \
"v_mfma_f32_16x16x32_bf16 a[128:131], v[148:151], v[132:135], a[128:131]\n" \
"ds_read_b64_tr_b16 v[98:99], v35 offset:33792\n" \
"v_mfma_f32_16x16x32_bf16 a[160:163], v[148:151], v[136:139], a[160:163]\n" \
"ds_read_b64_tr_b16 v[100:101], v36 offset:32768\n" \
"v_mfma_f32_16x16x32_bf16 a[132:135], v[152:155], v[132:135], a[132:135]\n" \
"s_add_u32 m0, s30, 0xc00\n" \
"s_mov_b32 s84, 0x6e00\n" \
"v_mfma_f32_16x16x32_bf16 a[164:167], v[152:155], v[136:139], a[164:167]\n" \
"buffer_load_dwordx4 v25, s[76:79], s84 offen lds\n" \
"v_mfma_f32_16x16x32_bf16 a[136:139], v[156:159], v[132:135], a[136:139]\n" \
"ds_read_b64_tr_b16 v[102:103], v36 offset:33280\n" \
"v_mfma_f32_16x16x32_bf16 a[168:171], v[156:159], v[136:139], a[168:171]\n" \
"ds_read_b128 v[52:55], v28 offset:32768\n" \
"v_mfma_f32_16x16x32_bf16 a[140:143], v[160:163], v[132:135], a[140:143]\n" \
"ds_read_b128 v[56:59], v28 offset:34816\n" \
"v_mfma_f32_16x16x32_bf16 a[172:175], v[160:163], v[136:139], a[172:175]\n" \
"s_add_u32 m0, s31, 0x400\n" \
"s_mov_b32 s85, 0x208000\n" \
"v_mfma_f32_16x16x32_bf16 a[144:147], v[164:167], v[140:143], a[144:147]\n" \
"buffer_load_dwordx4 v26, s[68:71], s85 offen lds\n" \
"v_mfma_f32_16x16x32_bf16 a[148:151], v[168:171], v[140:143], a[148:151]\n" \
"ds_read_b128 v[60:63], v28 offset:36864\n" \
"v_mfma_f32_16x16x32_bf16 a[180:183], v[176:179], v[140:143], a[180:183]\n" \
"ds_read_b128 v[64:67], v28 offset:38912\n" \
"v_mfma_f32_16x16x32_bf16 a[176:179], v[140:143], v[172:175], a[176:179]\n" \
"s_add_u32 m0, s30, 0x1000\n" \
"s_mov_b32 s84, 0x9200\n" \
"v_mfma_f32_16x16x32_bf16 a[192:195], v[148:151], v[140:143], a[192:195]\n" \
"buffer_load_dwordx4 v24, s[76:79], s84 offen lds\n" \
"v_mfma_f32_16x16x32_bf16 a[224:227], v[148:151], v[144:147], a[224:227]\n" \
"ds_read_b128 v[68:71], v28 offset:40960\n" \
"v_mfma_f32_16x16x32_bf16 a[196:199], v[152:155], v[140:143], a[196:199]\n" \
"ds_read_b128 v[72:75], v28 offset:43008\n" \
"v_mfma_f32_16x16x32_bf16 a[228:231], v[152:155], v[144:147], a[228:231]\n" \
"s_add_u32 m0, s30, 0x1400\n" \
"s_mov_b32 s85, 0xb600\n" \
"v_mfma_f32_16x16x32_bf16 a[200:203], v[156:159], v[140:143], a[200:203]\n" \
"buffer_load_dwordx4 v25, s[76:79], s85 offen lds\n" \
"v_mfma_f32_16x16x32_bf16 a[232:235], v[156:159], v[144:147], a[232:235]\n" \
"ds_read_b128 v[76:79], v28 offset:45056\n" \
"v_mfma_f32_16x16x32_bf16 a[204:207], v[160:163], v[140:143], a[204:207]\n" \
"ds_read_b128 v[80:83], v28 offset:47104\n" \
"v_mfma_f32_16x16x32_bf16 a[236:239], v[160:163], v[144:147], a[236:239]\n" \
"s_add_u32 m0, s31, 0x800\n" \
"s_mov_b32 s84, 0x210000\n" \
"v_mfma_f32_16x16x32_bf16 a[208:211], v[164:167], v[144:147], a[208:211]\n" \
"buffer_load_dwordx4 v27, s[68:71], s84 offen lds\n" \
"v_mfma_f32_16x16x32_bf16 a[212:215], v[168:171], v[144:147], a[212:215]\n" \
"v_mfma_f32_16x16x32_bf16 a[244:247], v[176:179], v[144:147], a[244:247]\n" \
"v_mfma_f32_16x16x32_bf16 a[240:243], v[144:147], v[172:175], a[240:243]\n" \
"s_waitcnt lgkmcnt(4)\n" \
"s_nop 0\n" \
"v_mfma_f32_16x16x32_bf16 a[24:27], v[104:107], v[64:67], a[24:27]\n" \
"s_add_u32 m0, s30, 0x1800\n" \
"s_mov_b32 s84, 0xda00\n" \
"v_mfma_f32_16x16x32_bf16 a[28:31], v[64:67], v[108:111], a[28:31]\n" \
"buffer_load_dwordx4 v24, s[76:79], s84 offen lds\n" \
"v_mfma_f32_16x16x32_bf16 a[56:59], v[112:115], v[64:67], a[56:59]\n" \
"ds_read_b64_tr_b16 v[168:169], v37 offset:36864\n" \
"v_mfma_f32_16x16x32_bf16 a[0:3], v[84:87], v[52:55], a[0:3]\n" \
"s_add_u32 m0, s30, 0x1c00\n" \
"s_mov_b32 s85, 0xfe00\n" \
"v_mfma_f32_16x16x32_bf16 a[32:35], v[84:87], v[56:59], a[32:35]\n" \
"buffer_load_dwordx4 v25, s[76:79], s85 offen lds\n" \
"v_mfma_f32_16x16x32_bf16 a[4:7], v[88:91], v[52:55], a[4:7]\n" \
"ds_read_b64_tr_b16 v[170:171], v37 offset:37376\n" \
"v_mfma_f32_16x16x32_bf16 a[36:39], v[88:91], v[56:59], a[36:39]\n" \
"s_add_u32 m0, s31, 0xc00\n" \
"s_mov_b32 s84, 0x218000\n" \
"v_mfma_f32_16x16x32_bf16 a[8:11], v[92:95], v[52:55], a[8:11]\n" \
"buffer_load_dwordx4 v27, s[68:71], s84 offen lds\n" \
"v_mfma_f32_16x16x32_bf16 a[40:43], v[92:95], v[56:59], a[40:43]\n" \
"ds_read_b64_tr_b16 v[172:173], v38 offset:36864\n" \
"v_mfma_f32_16x16x32_bf16 a[12:15], v[96:99], v[52:55], a[12:15]\n" \
"ds_read_b64_tr_b16 v[174:175], v38 offset:37376\n" \
"v_mfma_f32_16x16x32_bf16 a[44:47], v[96:99], v[56:59], a[44:47]\n" \
"ds_read_b64_tr_b16 v[176:177], v39 offset:36864\n" \
"s_waitcnt lgkmcnt(8)\n" \
"s_nop 0\n" \
"v_mfma_f32_16x16x32_bf16 a[16:19], v[100:103], v[68:71], a[16:19]\n" \
"ds_read_b64_tr_b16 v[178:179], v39 offset:37376\n" \
"v_mfma_f32_16x16x32_bf16 a[20:23], v[104:107], v[68:71], a[20:23]\n" \
"ds_read_b64_tr_b16 v[148:149], v32 offset:40960\n" \
"v_mfma_f32_16x16x32_bf16 a[52:55], v[112:115], v[68:71], a[52:55]\n" \
"ds_read_b64_tr_b16 v[150:151], v32 offset:41984\n" \
"v_mfma_f32_16x16x32_bf16 a[48:51], v[68:71], v[108:111], a[48:51]\n" \
"ds_read_b64_tr_b16 v[152:153], v33 offset:40960\n" \
"v_mfma_f32_16x16x32_bf16 a[64:67], v[84:87], v[60:63], a[64:67]\n" \
"ds_read_b64_tr_b16 v[154:155], v33 offset:41984\n" \
"v_mfma_f32_16x16x32_bf16 a[96:99], v[84:87], v[64:67], a[96:99]\n" \
"ds_read_b64_tr_b16 v[156:157], v34 offset:40960\n" \
"v_mfma_f32_16x16x32_bf16 a[68:71], v[88:91], v[60:63], a[68:71]\n" \
"ds_read_b64_tr_b16 v[158:159], v34 offset:41984\n" \
"v_mfma_f32_16x16x32_bf16 a[100:103], v[88:91], v[64:67], a[100:103]\n" \
"v_mfma_f32_16x16x32_bf16 a[72:75], v[92:95], v[60:63], a[72:75]\n" \
"ds_read_b64_tr_b16 v[160:161], v35 offset:40960\n" \
"v_mfma_f32_16x16x32_bf16 a[104:107], v[92:95], v[64:67], a[104:107]\n" \
"v_mfma_f32_16x16x32_bf16 a[76:79], v[96:99], v[60:63], a[76:79]\n" \
"s_add_u32 s89, s41, 0x3000\n" \
"buffer_store_dwordx4 v[232:235], v20, s[80:83], s89 offen offset:0 sc1\n" \
"v_mfma_f32_16x16x32_bf16 a[108:111], v[96:99], v[64:67], a[108:111]\n" \
"ds_read_b64_tr_b16 v[162:163], v35 offset:41984\n" \
"s_waitcnt lgkmcnt(15)\n" \
"s_nop 0\n" \
"v_mfma_f32_16x16x32_bf16 a[80:83], v[100:103], v[72:75], a[80:83]\n" \
"ds_read_b64_tr_b16 v[164:165], v36 offset:36864\n" \
"v_mfma_f32_16x16x32_bf16 a[84:87], v[104:107], v[72:75], a[84:87]\n" \
"ds_read_b64_tr_b16 v[166:167], v36 offset:37376\n" \
"v_mfma_f32_16x16x32_bf16 a[116:119], v[112:115], v[72:75], a[116:119]\n" \
"v_mfma_f32_16x16x32_bf16 a[112:115], v[72:75], v[108:111], a[112:115]\n" \
"ds_read_b128 v[116:119], v29 offset:32768\n" \
"v_mfma_f32_16x16x32_bf16 a[128:131], v[84:87], v[68:71], a[128:131]\n" \
"ds_read_b128 v[120:123], v29 offset:34816\n" \
"v_mfma_f32_16x16x32_bf16 a[160:163], v[84:87], v[72:75], a[160:163]\n" \
"v_mfma_f32_16x16x32_bf16 a[132:135], v[88:91], v[68:71], a[132:135]\n" \
"s_add_u32 s88, s41, 0x3000\n" \
"buffer_store_dwordx4 v[236:239], v20, s[80:83], s88 offen offset:1024 sc1\n" \
"v_mfma_f32_16x16x32_bf16 a[164:167], v[88:91], v[72:75], a[164:167]\n" \
"ds_read_b128 v[124:127], v29 offset:36864\n" \
"v_mfma_f32_16x16x32_bf16 a[136:139], v[92:95], v[68:71], a[136:139]\n" \
"ds_read_b128 v[128:131], v29 offset:38912\n" \
"v_mfma_f32_16x16x32_bf16 a[168:171], v[92:95], v[72:75], a[168:171]\n" \
"ds_read_b128 v[132:135], v29 offset:40960\n" \
"v_mfma_f32_16x16x32_bf16 a[140:143], v[96:99], v[68:71], a[140:143]\n" \
"v_mfma_f32_16x16x32_bf16 a[172:175], v[96:99], v[72:75], a[172:175]\n" \
"ds_read_b128 v[136:139], v29 offset:43008\n" \
"v_mfma_f32_16x16x32_bf16 a[144:147], v[100:103], v[76:79], a[144:147]\n" \
"ds_read_b128 v[140:143], v29 offset:45056\n" \
"v_mfma_f32_16x16x32_bf16 a[148:151], v[104:107], v[76:79], a[148:151]\n" \
"s_add_u32 s89, s41, 0x3000\n" \
"buffer_store_dwordx4 v[240:243], v20, s[80:83], s89 offen offset:2048 sc1\n" \
"v_mfma_f32_16x16x32_bf16 a[180:183], v[112:115], v[76:79], a[180:183]\n" \
"ds_read_b128 v[144:147], v29 offset:47104\n" \
"v_mfma_f32_16x16x32_bf16 a[176:179], v[76:79], v[108:111], a[176:179]\n" \
"v_mfma_f32_16x16x32_bf16 a[192:195], v[84:87], v[76:79], a[192:195]\n" \
"s_waitcnt lgkmcnt(15)\n" \
"s_nop 0\n" \
"v_mfma_f32_16x16x32_bf16 a[224:227], v[84:87], v[80:83], a[224:227]\n" \
"v_mfma_f32_16x16x32_bf16 a[196:199], v[88:91], v[76:79], a[196:199]\n" \
"v_mfma_f32_16x16x32_bf16 a[228:231], v[88:91], v[80:83], a[228:231]\n" \
"v_mfma_f32_16x16x32_bf16 a[200:203], v[92:95], v[76:79], a[200:203]\n" \
"s_add_u32 s88, s41, 0x3000\n" \
"buffer_store_dwordx4 v[244:247], v20, s[80:83], s88 offen offset:3072 sc1\n" \
"v_mfma_f32_16x16x32_bf16 a[232:235], v[92:95], v[80:83], a[232:235]\n" \
"v_mfma_f32_16x16x32_bf16 a[204:207], v[96:99], v[76:79], a[204:207]\n" \
"v_mfma_f32_16x16x32_bf16 a[236:239], v[96:99], v[80:83], a[236:239]\n" \
"v_mfma_f32_16x16x32_bf16 a[208:211], v[100:103], v[80:83], a[208:211]\n" \
"v_mfma_f32_16x16x32_bf16 a[212:215], v[104:107], v[80:83], a[212:215]\n" \
"v_mfma_f32_16x16x32_bf16 a[244:247], v[112:115], v[80:83], a[244:247]\n" \
"v_mfma_f32_16x16x32_bf16 a[240:243], v[80:83], v[108:111], a[240:243]\n" \
"s_waitcnt vmcnt(4) lgkmcnt(0)\n" \
"s_barrier\n" \
"v_mfma_f32_16x16x32_bf16 a[24:27], v[168:171], v[128:131], a[24:27]\n" \
"s_add_u32 m0, s40, 0x8000\n" \
"s_add_u32 s85, s39, 0xa000\n" \
"v_mfma_f32_16x16x32_bf16 a[28:31], v[128:131], v[172:175], a[28:31]\n" \
"buffer_load_dwordx4 v20, s[80:83], s85 offen sc1 lds\n" \
"v_mfma_f32_16x16x32_bf16 a[56:59], v[176:179], v[128:131], a[56:59]\n" \
"ds_read_b64_tr_b16 v[104:105], v37 offset:0\n" \
"v_mfma_f32_16x16x32_bf16 a[0:3], v[148:151], v[116:119], a[0:3]\n" \
"ds_read_b64_tr_b16 v[106:107], v37 offset:512\n" \
"v_mfma_f32_16x16x32_bf16 a[32:35], v[148:151], v[120:123], a[32:35]\n" \
"s_add_u32 m0, s40, 0x9000\n" \
"s_add_u32 s84, s39, 0xb000\n" \
"v_mfma_f32_16x16x32_bf16 a[4:7], v[152:155], v[116:119], a[4:7]\n" \
"buffer_load_dwordx4 v20, s[80:83], s84 offen sc1 lds\n" \
"v_mfma_f32_16x16x32_bf16 a[36:39], v[152:155], v[120:123], a[36:39]\n" \
"ds_read_b64_tr_b16 v[108:109], v38 offset:0\n" \
"v_mfma_f32_16x16x32_bf16 a[8:11], v[156:159], v[116:119], a[8:11]\n" \
"ds_read_b64_tr_b16 v[110:111], v38 offset:512\n" \
"v_mfma_f32_16x16x32_bf16 a[40:43], v[156:159], v[120:123], a[40:43]\n" \
"s_add_u32 m0, s40, 0xb000\n" \
"s_mov_b32 s85, 0x280\n" \
"v_mfma_f32_16x16x32_bf16 a[12:15], v[160:163], v[116:119], a[12:15]\n" \
"buffer_load_dwordx4 v21, s[76:79], s85 offen lds\n" \
"v_mfma_f32_16x16x32_bf16 a[44:47], v[160:163], v[120:123], a[44:47]\n" \
"ds_read_b64_tr_b16 v[112:113], v39 offset:0\n" \
"v_mfma_f32_16x16x32_bf16 a[16:19], v[164:167], v[132:135], a[16:19]\n" \
"ds_read_b64_tr_b16 v[114:115], v39 offset:512\n" \
"v_mfma_f32_16x16x32_bf16 a[20:23], v[168:171], v[132:135], a[20:23]\n" \
"s_add_u32 m0, s30, 0x8000\n" \
"s_mov_b32 s84, 0x280\n" \
"v_mfma_f32_16x16x32_bf16 a[52:55], v[176:179], v[132:135], a[52:55]\n" \
"buffer_load_dwordx4 v24, s[76:79], s84 offen lds\n" \
"v_mfma_f32_16x16x32_bf16 a[48:51], v[132:135], v[172:175], a[48:51]\n" \
"ds_read_b64_tr_b16 v[84:85], v32 offset:0\n" \
"v_mfma_f32_16x16x32_bf16 a[64:67], v[148:151], v[124:127], a[64:67]\n" \
"ds_read_b64_tr_b16 v[86:87], v32 offset:1024\n" \
"v_mfma_f32_16x16x32_bf16 a[96:99], v[148:151], v[128:131], a[96:99]\n" \
"ds_read_b64_tr_b16 v[88:89], v33 offset:0\n" \
"v_mfma_f32_16x16x32_bf16 a[68:71], v[152:155], v[124:127], a[68:71]\n" \
"s_add_u32 m0, s30, 0x8400\n" \
"s_mov_b32 s85, 0x2680\n" \
"v_mfma_f32_16x16x32_bf16 a[100:103], v[152:155], v[128:131], a[100:103]\n" \
"buffer_load_dwordx4 v25, s[76:79], s85 offen lds\n" \
"v_mfma_f32_16x16x32_bf16 a[72:75], v[156:159], v[124:127], a[72:75]\n" \
"ds_read_b64_tr_b16 v[90:91], v33 offset:1024\n" \
"v_mfma_f32_16x16x32_bf16 a[104:107], v[156:159], v[128:131], a[104:107]\n" \
"ds_read_b64_tr_b16 v[92:93], v34 offset:0\n" \
"v_mfma_f32_16x16x32_bf16 a[76:79], v[160:163], v[124:127], a[76:79]\n" \
"s_add_u32 m0, s31, 0x8000\n" \
"s_mov_b32 s84, 0x280000\n" \
"v_mfma_f32_16x16x32_bf16 a[108:111], v[160:163], v[128:131], a[108:111]\n" \
"buffer_load_dwordx4 v26, s[68:71], s84 offen lds\n" \
"v_mfma_f32_16x16x32_bf16 a[80:83], v[164:167], v[136:139], a[80:83]\n" \
"ds_read_b64_tr_b16 v[94:95], v34 offset:1024\n" \
"v_mfma_f32_16x16x32_bf16 a[84:87], v[168:171], v[136:139], a[84:87]\n" \
"ds_read_b64_tr_b16 v[96:97], v35 offset:0\n" \
"v_mfma_f32_16x16x32_bf16 a[116:119], v[176:179], v[136:139], a[116:119]\n" \
"s_add_u32 m0, s30, 0x8800\n" \
"s_mov_b32 s85, 0x4a80\n" \
"v_mfma_f32_16x16x32_bf16 a[112:115], v[136:139], v[172:175], a[112:115]\n" \
"buffer_load_dwordx4 v24, s[76:79], s85 offen lds\n" \
"v_mfma_f32_16x16x32_bf16 a[128:131], v[148:151], v[132:135], a[128:131]\n" \
"ds_read_b64_tr_b16 v[98:99], v35 offset:1024\n" \
"v_mfma_f32_16x16x32_bf16 a[160:163], v[148:151], v[136:139], a[160:163]\n" \
"ds_read_b64_tr_b16 v[100:101], v36 offset:0\n" \
"v_mfma_f32_16x16x32_bf16 a[132:135], v[152:155], v[132:135], a[132:135]\n" \
"s_add_u32 m0, s30, 0x8c00\n" \
"s_mov_b32 s84, 0x6e80\n" \
"v_mfma_f32_16x16x32_bf16 a[164:167], v[152:155], v[136:139], a[164:167]\n" \
"buffer_load_dwordx4 v25, s[76:79], s84 offen lds\n" \
"v_mfma_f32_16x16x32_bf16 a[136:139], v[156:159], v[132:135], a[136:139]\n" \
"ds_read_b64_tr_b16 v[102:103], v36 offset:512\n" \
"v_mfma_f32_16x16x32_bf16 a[168:171], v[156:159], v[136:139], a[168:171]\n" \
"ds_read_b128 v[52:55], v28 offset:0\n" \
"v_mfma_f32_16x16x32_bf16 a[140:143], v[160:163], v[132:135], a[140:143]\n" \
"ds_read_b128 v[56:59], v28 offset:2048\n" \
"v_mfma_f32_16x16x32_bf16 a[172:175], v[160:163], v[136:139], a[172:175]\n" \
"s_add_u32 m0, s31, 0x8400\n" \
"s_mov_b32 s85, 0x288000\n" \
"v_mfma_f32_16x16x32_bf16 a[144:147], v[164:167], v[140:143], a[144:147]\n" \
"buffer_load_dwordx4 v26, s[68:71], s85 offen lds\n" \
"v_mfma_f32_16x16x32_bf16 a[148:151], v[168:171], v[140:143], a[148:151]\n" \
"ds_read_b128 v[60:63], v28 offset:4096\n" \
"v_mfma_f32_16x16x32_bf16 a[180:183], v[176:179], v[140:143], a[180:183]\n" \
"ds_read_b128 v[64:67], v28 offset:6144\n" \
"v_mfma_f32_16x16x32_bf16 a[176:179], v[140:143], v[172:175], a[176:179]\n" \
"s_add_u32 m0, s30, 0x9000\n" \
"s_mov_b32 s84, 0x9280\n" \
"v_mfma_f32_16x16x32_bf16 a[192:195], v[148:151], v[140:143], a[192:195]\n" \
"buffer_load_dwordx4 v24, s[76:79], s84 offen lds\n" \
"v_mfma_f32_16x16x32_bf16 a[224:227], v[148:151], v[144:147], a[224:227]\n" \
"ds_read_b128 v[68:71], v28 offset:8192\n" \
"v_mfma_f32_16x16x32_bf16 a[196:199], v[152:155], v[140:143], a[196:199]\n" \
"ds_read_b128 v[72:75], v28 offset:10240\n" \
"v_mfma_f32_16x16x32_bf16 a[228:231], v[152:155], v[144:147], a[228:231]\n" \
"s_add_u32 m0, s30, 0x9400\n" \
"s_mov_b32 s85, 0xb680\n" \
"v_mfma_f32_16x16x32_bf16 a[200:203], v[156:159], v[140:143], a[200:203]\n" \
"buffer_load_dwordx4 v25, s[76:79], s85 offen lds\n" \
"v_mfma_f32_16x16x32_bf16 a[232:235], v[156:159], v[144:147], a[232:235]\n" \
"ds_read_b128 v[76:79], v28 offset:12288\n" \
"v_mfma_f32_16x16x32_bf16 a[204:207], v[160:163], v[140:143], a[204:207]\n" \
"ds_read_b128 v[80:83], v28 offset:14336\n" \
"v_mfma_f32_16x16x32_bf16 a[236:239], v[160:163], v[144:147], a[236:239]\n" \
"s_add_u32 m0, s31, 0x8800\n" \
"s_mov_b32 s84, 0x290000\n" \
"v_mfma_f32_16x16x32_bf16 a[208:211], v[164:167], v[144:147], a[208:211]\n" \
"buffer_load_dwordx4 v27, s[68:71], s84 offen lds\n" \
"v_mfma_f32_16x16x32_bf16 a[212:215], v[168:171], v[144:147], a[212:215]\n" \
"v_mfma_f32_16x16x32_bf16 a[244:247], v[176:179], v[144:147], a[244:247]\n" \
"v_mfma_f32_16x16x32_bf16 a[240:243], v[144:147], v[172:175], a[240:243]\n" \
"s_waitcnt lgkmcnt(4)\n" \
"s_nop 0\n" \
"v_mfma_f32_16x16x32_bf16 a[24:27], v[104:107], v[64:67], a[24:27]\n" \
"s_add_u32 m0, s30, 0x9800\n" \
"s_mov_b32 s84, 0xda80\n" \
"v_mfma_f32_16x16x32_bf16 a[28:31], v[64:67], v[108:111], a[28:31]\n" \
"buffer_load_dwordx4 v24, s[76:79], s84 offen lds\n" \
"v_mfma_f32_16x16x32_bf16 a[56:59], v[112:115], v[64:67], a[56:59]\n" \
"ds_read_b64_tr_b16 v[168:169], v37 offset:4096\n" \
"v_mfma_f32_16x16x32_bf16 a[0:3], v[84:87], v[52:55], a[0:3]\n" \
"s_add_u32 m0, s30, 0x9c00\n" \
"s_mov_b32 s85, 0xfe80\n" \
"v_mfma_f32_16x16x32_bf16 a[32:35], v[84:87], v[56:59], a[32:35]\n" \
"buffer_load_dwordx4 v25, s[76:79], s85 offen lds\n" \
"v_mfma_f32_16x16x32_bf16 a[4:7], v[88:91], v[52:55], a[4:7]\n" \
"ds_read_b64_tr_b16 v[170:171], v37 offset:4608\n" \
"v_mfma_f32_16x16x32_bf16 a[36:39], v[88:91], v[56:59], a[36:39]\n" \
"s_add_u32 m0, s31, 0x8c00\n" \
"s_mov_b32 s84, 0x298000\n" \
"v_mfma_f32_16x16x32_bf16 a[8:11], v[92:95], v[52:55], a[8:11]\n" \
"buffer_load_dwordx4 v27, s[68:71], s84 offen lds\n" \
"v_mfma_f32_16x16x32_bf16 a[40:43], v[92:95], v[56:59], a[40:43]\n" \
"ds_read_b64_tr_b16 v[172:173], v38 offset:4096\n" \
"v_mfma_f32_16x16x32_bf16 a[12:15], v[96:99], v[52:55], a[12:15]\n" \
"ds_read_b64_tr_b16 v[174:175], v38 offset:4608\n" \
"v_mfma_f32_16x16x32_bf16 a[44:47], v[96:99], v[56:59], a[44:47]\n" \
"ds_read_b64_tr_b16 v[176:177], v39 offset:4096\n" \
"s_waitcnt lgkmcnt(8)\n" \
"s_nop 0\n" \
"v_mfma_f32_16x16x32_bf16 a[16:19], v[100:103], v[68:71], a[16:19]\n" \
"ds_read_b64_tr_b16 v[178:179], v39 offset:4608\n" \
"v_mfma_f32_16x16x32_bf16 a[20:23], v[104:107], v[68:71], a[20:23]\n" \
"ds_read_b64_tr_b16 v[148:149], v32 offset:8192\n" \
"v_mfma_f32_16x16x32_bf16 a[52:55], v[112:115], v[68:71], a[52:55]\n" \
"ds_read_b64_tr_b16 v[150:151], v32 offset:9216\n" \
"v_mfma_f32_16x16x32_bf16 a[48:51], v[68:71], v[108:111], a[48:51]\n" \
"ds_read_b64_tr_b16 v[152:153], v33 offset:8192\n" \
"v_mfma_f32_16x16x32_bf16 a[64:67], v[84:87], v[60:63], a[64:67]\n" \
"ds_read_b64_tr_b16 v[154:155], v33 offset:9216\n" \
"v_mfma_f32_16x16x32_bf16 a[96:99], v[84:87], v[64:67], a[96:99]\n" \
"ds_read_b64_tr_b16 v[156:157], v34 offset:8192\n" \
"v_mfma_f32_16x16x32_bf16 a[68:71], v[88:91], v[60:63], a[68:71]\n" \
"ds_read_b64_tr_b16 v[158:159], v34 offset:9216\n" \
"v_mfma_f32_16x16x32_bf16 a[100:103], v[88:91], v[64:67], a[100:103]\n" \
"v_mfma_f32_16x16x32_bf16 a[72:75], v[92:95], v[60:63], a[72:75]\n" \
"ds_read_b64_tr_b16 v[160:161], v35 offset:8192\n" \
"v_mfma_f32_16x16x32_bf16 a[104:107], v[92:95], v[64:67], a[104:107]\n" \
"v_mfma_f32_16x16x32_bf16 a[76:79], v[96:99], v[60:63], a[76:79]\n" \
"ds_read_b64_tr_b16 v[162:163], v35 offset:9216\n" \
"v_mfma_f32_16x16x32_bf16 a[108:111], v[96:99], v[64:67], a[108:111]\n" \
"ds_read_b64_tr_b16 v[164:165], v36 offset:4096\n" \
"s_waitcnt lgkmcnt(15)\n" \
"s_nop 0\n" \
"v_mfma_f32_16x16x32_bf16 a[80:83], v[100:103], v[72:75], a[80:83]\n" \
"v_mfma_f32_16x16x32_bf16 a[84:87], v[104:107], v[72:75], a[84:87]\n" \
"ds_read_b64_tr_b16 v[166:167], v36 offset:4608\n" \
"v_mfma_f32_16x16x32_bf16 a[116:119], v[112:115], v[72:75], a[116:119]\n" \
"s_add_u32 s89, s42, 0x1000\n" \
"buffer_store_dwordx4 a[60:63], v20, s[80:83], s89 offen offset:2048 sc1\n" \
"v_mfma_f32_16x16x32_bf16 a[112:115], v[72:75], v[108:111], a[112:115]\n" \
"ds_read_b128 v[116:119], v29 offset:0\n" \
"v_mfma_f32_16x16x32_bf16 a[128:131], v[84:87], v[68:71], a[128:131]\n" \
"ds_read_b128 v[120:123], v29 offset:2048\n" \
"v_mfma_f32_16x16x32_bf16 a[160:163], v[84:87], v[72:75], a[160:163]\n" \
"v_mfma_f32_16x16x32_bf16 a[132:135], v[88:91], v[68:71], a[132:135]\n" \
"ds_read_b128 v[124:127], v29 offset:4096\n" \
"v_mfma_f32_16x16x32_bf16 a[164:167], v[88:91], v[72:75], a[164:167]\n" \
"ds_read_b128 v[128:131], v29 offset:6144\n" \
"v_mfma_f32_16x16x32_bf16 a[136:139], v[92:95], v[68:71], a[136:139]\n" \
"v_mfma_f32_16x16x32_bf16 a[168:171], v[92:95], v[72:75], a[168:171]\n" \
"ds_read_b128 v[132:135], v29 offset:8192\n" \
"v_mfma_f32_16x16x32_bf16 a[140:143], v[96:99], v[68:71], a[140:143]\n" \
"v_mfma_f32_16x16x32_bf16 a[172:175], v[96:99], v[72:75], a[172:175]\n" \
"s_add_u32 s88, s42, 0x1000\n" \
"buffer_store_dwordx4 a[88:91], v20, s[80:83], s88 offen offset:3072 sc1\n" \
"v_mfma_f32_16x16x32_bf16 a[144:147], v[100:103], v[76:79], a[144:147]\n" \
"ds_read_b128 v[136:139], v29 offset:10240\n" \
"v_mfma_f32_16x16x32_bf16 a[148:151], v[104:107], v[76:79], a[148:151]\n" \
"ds_read_b128 v[140:143], v29 offset:12288\n" \
"v_mfma_f32_16x16x32_bf16 a[180:183], v[112:115], v[76:79], a[180:183]\n" \
"ds_read_b128 v[144:147], v29 offset:14336\n" \
"v_mfma_f32_16x16x32_bf16 a[176:179], v[76:79], v[108:111], a[176:179]\n" \
"v_mfma_f32_16x16x32_bf16 a[192:195], v[84:87], v[76:79], a[192:195]\n" \
"v_mfma_f32_16x16x32_bf16 a[224:227], v[84:87], v[80:83], a[224:227]\n" \
"v_mfma_f32_16x16x32_bf16 a[196:199], v[88:91], v[76:79], a[196:199]\n" \
"v_mfma_f32_16x16x32_bf16 a[228:231], v[88:91], v[80:83], a[228:231]\n" \
"s_add_u32 s89, s42, 0x0\n" \
"buffer_store_dwordx4 a[120:123], v20, s[80:83], s89 offen offset:0 sc1\n" \
"v_mfma_f32_16x16x32_bf16 a[200:203], v[92:95], v[76:79], a[200:203]\n" \
"v_mfma_f32_16x16x32_bf16 a[232:235], v[92:95], v[80:83], a[232:235]\n" \
"v_mfma_f32_16x16x32_bf16 a[204:207], v[96:99], v[76:79], a[204:207]\n" \
"v_mfma_f32_16x16x32_bf16 a[236:239], v[96:99], v[80:83], a[236:239]\n" \
"v_mfma_f32_16x16x32_bf16 a[208:211], v[100:103], v[80:83], a[208:211]\n" \
"v_mfma_f32_16x16x32_bf16 a[212:215], v[104:107], v[80:83], a[212:215]\n" \
"v_mfma_f32_16x16x32_bf16 a[244:247], v[112:115], v[80:83], a[244:247]\n" \
"v_mfma_f32_16x16x32_bf16 a[240:243], v[80:83], v[108:111], a[240:243]\n" \
"s_waitcnt vmcnt(3) lgkmcnt(0)\n" \
"s_barrier\n" \
"v_mov_b32_e32 v45, s62\n" \
"s_mov_b64 exec, 1\n" \
"buffer_store_dword v45, off, s[80:83], s37 offset:0 sc1\n" \
"s_mov_b64 exec, -1\n" \
"v_mfma_f32_16x16x32_bf16 a[24:27], v[168:171], v[128:131], a[24:27]\n" \
"s_add_u32 m0, s40, 0x0\n" \
"s_nop 0\n" \
"s_add_u32 s84, s39, 0xc000\n" \
"v_mfma_f32_16x16x32_bf16 a[28:31], v[128:131], v[172:175], a[28:31]\n" \
"buffer_load_dwordx4 v20, s[80:83], s84 offen sc1 lds\n" \
"v_mfma_f32_16x16x32_bf16 a[56:59], v[176:179], v[128:131], a[56:59]\n" \
"ds_read_b64_tr_b16 v[104:105], v37 offset:32768\n" \
"v_mfma_f32_16x16x32_bf16 a[0:3], v[148:151], v[116:119], a[0:3]\n" \
"ds_read_b64_tr_b16 v[106:107], v37 offset:33280\n" \
"v_mfma_f32_16x16x32_bf16 a[32:35], v[148:151], v[120:123], a[32:35]\n" \
"s_add_u32 m0, s40, 0x1000\n" \
"s_add_u32 s85, s39, 0xd000\n" \
"v_mfma_f32_16x16x32_bf16 a[4:7], v[152:155], v[116:119], a[4:7]\n" \
"buffer_load_dwordx4 v20, s[80:83], s85 offen sc1 lds\n" \
"v_mfma_f32_16x16x32_bf16 a[36:39], v[152:155], v[120:123], a[36:39]\n" \
"ds_read_b64_tr_b16 v[108:109], v38 offset:32768\n" \
"v_mfma_f32_16x16x32_bf16 a[8:11], v[156:159], v[116:119], a[8:11]\n" \
"ds_read_b64_tr_b16 v[110:111], v38 offset:33280\n" \
"v_mfma_f32_16x16x32_bf16 a[40:43], v[156:159], v[120:123], a[40:43]\n" \
"s_add_u32 m0, s40, 0x3000\n" \
"s_mov_b32 s84, 0x300\n" \
"v_mfma_f32_16x16x32_bf16 a[12:15], v[160:163], v[116:119], a[12:15]\n" \
"buffer_load_dwordx4 v21, s[76:79], s84 offen lds\n" \
"v_mfma_f32_16x16x32_bf16 a[44:47], v[160:163], v[120:123], a[44:47]\n" \
"ds_read_b64_tr_b16 v[112:113], v39 offset:32768\n" \
"v_mfma_f32_16x16x32_bf16 a[16:19], v[164:167], v[132:135], a[16:19]\n" \
"ds_read_b64_tr_b16 v[114:115], v39 offset:33280\n" \
"v_mfma_f32_16x16x32_bf16 a[20:23], v[168:171], v[132:135], a[20:23]\n" \
"s_add_u32 m0, s30, 0x0\n" \
"s_nop 0\n" \
"s_mov_b32 s85, 0x300\n" \
"v_mfma_f32_16x16x32_bf16 a[52:55], v[176:179], v[132:135], a[52:55]\n" \
"buffer_load_dwordx4 v24, s[76:79], s85 offen lds\n" \
"v_mfma_f32_16x16x32_bf16 a[48:51], v[132:135], v[172:175], a[48:51]\n" \
"ds_read_b64_tr_b16 v[84:85], v32 offset:32768\n" \
"v_mfma_f32_16x16x32_bf16 a[64:67], v[148:151], v[124:127], a[64:67]\n" \
"ds_read_b64_tr_b16 v[86:87], v32 offset:33792\n" \
"v_mfma_f32_16x16x32_bf16 a[96:99], v[148:151], v[128:131], a[96:99]\n" \
"ds_read_b64_tr_b16 v[88:89], v33 offset:32768\n" \
"v_mfma_f32_16x16x32_bf16 a[68:71], v[152:155], v[124:127], a[68:71]\n" \
"s_add_u32 m0, s30, 0x400\n" \
"s_mov_b32 s84, 0x2700\n" \
"v_mfma_f32_16x16x32_bf16 a[100:103], v[152:155], v[128:131], a[100:103]\n" \
"buffer_load_dwordx4 v25, s[76:79], s84 offen lds\n" \
"v_mfma_f32_16x16x32_bf16 a[72:75], v[156:159], v[124:127], a[72:75]\n" \
"ds_read_b64_tr_b16 v[90:91], v33 offset:33792\n" \
"v_mfma_f32_16x16x32_bf16 a[104:107], v[156:159], v[128:131], a[104:107]\n" \
"ds_read_b64_tr_b16 v[92:93], v34 offset:32768\n" \
"v_mfma_f32_16x16x32_bf16 a[76:79], v[160:163], v[124:127], a[76:79]\n" \
"s_add_u32 m0, s31, 0x0\n" \
"s_nop 0\n" \
"s_mov_b32 s85, 0x300000\n" \
"v_mfma_f32_16x16x32_bf16 a[108:111], v[160:163], v[128:131], a[108:111]\n" \
"buffer_load_dwordx4 v26, s[68:71], s85 offen lds\n" \
"v_mfma_f32_16x16x32_bf16 a[80:83], v[164:167], v[136:139], a[80:83]\n" \
"ds_read_b64_tr_b16 v[94:95], v34 offset:33792\n" \
"v_mfma_f32_16x16x32_bf16 a[84:87], v[168:171], v[136:139], a[84:87]\n" \
"ds_read_b64_tr_b16 v[96:97], v35 offset:32768\n" \
"v_mfma_f32_16x16x32_bf16 a[116:119], v[176:179], v[136:139], a[116:119]\n" \
"s_add_u32 m0, s30, 0x800\n" \
"s_mov_b32 s84, 0x4b00\n" \
"v_mfma_f32_16x16x32_bf16 a[112:115], v[136:139], v[172:175], a[112:115]\n" \
"buffer_load_dwordx4 v24, s[76:79], s84 offen lds\n" \
"v_mfma_f32_16x16x32_bf16 a[128:131], v[148:151], v[132:135], a[128:131]\n" \
"ds_read_b64_tr_b16 v[98:99], v35 offset:33792\n" \
"v_mfma_f32_16x16x32_bf16 a[160:163], v[148:151], v[136:139], a[160:163]\n" \
"ds_read_b64_tr_b16 v[100:101], v36 offset:32768\n" \
"v_mfma_f32_16x16x32_bf16 a[132:135], v[152:155], v[132:135], a[132:135]\n" \
"s_add_u32 m0, s30, 0xc00\n" \
"s_mov_b32 s85, 0x6f00\n" \
"v_mfma_f32_16x16x32_bf16 a[164:167], v[152:155], v[136:139], a[164:167]\n" \
"buffer_load_dwordx4 v25, s[76:79], s85 offen lds\n" \
"v_mfma_f32_16x16x32_bf16 a[136:139], v[156:159], v[132:135], a[136:139]\n" \
"ds_read_b64_tr_b16 v[102:103], v36 offset:33280\n" \
"v_mfma_f32_16x16x32_bf16 a[168:171], v[156:159], v[136:139], a[168:171]\n" \
"ds_read_b128 v[52:55], v28 offset:32768\n" \
"v_mfma_f32_16x16x32_bf16 a[140:143], v[160:163], v[132:135], a[140:143]\n" \
"ds_read_b128 v[56:59], v28 offset:34816\n" \
"v_mfma_f32_16x16x32_bf16 a[172:175], v[160:163], v[136:139], a[172:175]\n" \
"s_add_u32 m0, s31, 0x400\n" \
"s_mov_b32 s84, 0x308000\n" \
"v_mfma_f32_16x16x32_bf16 a[144:147], v[164:167], v[140:143], a[144:147]\n" \
"buffer_load_dwordx4 v26, s[68:71], s84 offen lds\n" \
"v_mfma_f32_16x16x32_bf16 a[148:151], v[168:171], v[140:143], a[148:151]\n" \
"ds_read_b128 v[60:63], v28 offset:36864\n" \
"v_mfma_f32_16x16x32_bf16 a[180:183], v[176:179], v[140:143], a[180:183]\n" \
"ds_read_b128 v[64:67], v28 offset:38912\n" \
"v_mfma_f32_16x16x32_bf16 a[176:179], v[140:143], v[172:175], a[176:179]\n" \
"s_add_u32 m0, s30, 0x1000\n" \
"s_mov_b32 s85, 0x9300\n" \
"v_mfma_f32_16x16x32_bf16 a[192:195], v[148:151], v[140:143], a[192:195]\n" \
"buffer_load_dwordx4 v24, s[76:79], s85 offen lds\n" \
"v_mfma_f32_16x16x32_bf16 a[224:227], v[148:151], v[144:147], a[224:227]\n" \
"ds_read_b128 v[68:71], v28 offset:40960\n" \
"v_mfma_f32_16x16x32_bf16 a[196:199], v[152:155], v[140:143], a[196:199]\n" \
"ds_read_b128 v[72:75], v28 offset:43008\n" \
"v_mfma_f32_16x16x32_bf16 a[228:231], v[152:155], v[144:147], a[228:231]\n" \
"s_add_u32 m0, s30, 0x1400\n" \
"s_mov_b32 s84, 0xb700\n" \
"v_mfma_f32_16x16x32_bf16 a[200:203], v[156:159], v[140:143], a[200:203]\n" \
"buffer_load_dwordx4 v25, s[76:79], s84 offen lds\n" \
"v_mfma_f32_16x16x32_bf16 a[232:235], v[156:159], v[144:147], a[232:235]\n" \
"ds_read_b128 v[76:79], v28 offset:45056\n" \
"v_mfma_f32_16x16x32_bf16 a[204:207], v[160:163], v[140:143], a[204:207]\n" \
"ds_read_b128 v[80:83], v28 offset:47104\n" \
"v_mfma_f32_16x16x32_bf16 a[236:239], v[160:163], v[144:147], a[236:239]\n" \
"s_add_u32 m0, s31, 0x800\n" \
"s_mov_b32 s85, 0x310000\n" \
"v_mfma_f32_16x16x32_bf16 a[208:211], v[164:167], v[144:147], a[208:211]\n" \
"buffer_load_dwordx4 v27, s[68:71], s85 offen lds\n" \
"v_mfma_f32_16x16x32_bf16 a[212:215], v[168:171], v[144:147], a[212:215]\n" \
"v_mfma_f32_16x16x32_bf16 a[244:247], v[176:179], v[144:147], a[244:247]\n" \
"v_mfma_f32_16x16x32_bf16 a[240:243], v[144:147], v[172:175], a[240:243]\n" \
"s_waitcnt lgkmcnt(4)\n" \
"s_nop 0\n" \
"v_mfma_f32_16x16x32_bf16 a[24:27], v[104:107], v[64:67], a[24:27]\n" \
"s_add_u32 m0, s30, 0x1800\n" \
"s_mov_b32 s85, 0xdb00\n" \
"v_mfma_f32_16x16x32_bf16 a[28:31], v[64:67], v[108:111], a[28:31]\n" \
"buffer_load_dwordx4 v24, s[76:79], s85 offen lds\n" \
"v_mfma_f32_16x16x32_bf16 a[56:59], v[112:115], v[64:67], a[56:59]\n" \
"ds_read_b64_tr_b16 v[168:169], v37 offset:36864\n" \
"v_mfma_f32_16x16x32_bf16 a[0:3], v[84:87], v[52:55], a[0:3]\n" \
"s_add_u32 m0, s30, 0x1c00\n" \
"s_mov_b32 s84, 0xff00\n" \
"v_mfma_f32_16x16x32_bf16 a[32:35], v[84:87], v[56:59], a[32:35]\n" \
"buffer_load_dwordx4 v25, s[76:79], s84 offen lds\n" \
"v_mfma_f32_16x16x32_bf16 a[4:7], v[88:91], v[52:55], a[4:7]\n" \
"ds_read_b64_tr_b16 v[170:171], v37 offset:37376\n" \
"v_mfma_f32_16x16x32_bf16 a[36:39], v[88:91], v[56:59], a[36:39]\n" \
"s_add_u32 m0, s31, 0xc00\n" \
"s_mov_b32 s85, 0x318000\n" \
"v_mfma_f32_16x16x32_bf16 a[8:11], v[92:95], v[52:55], a[8:11]\n" \
"buffer_load_dwordx4 v27, s[68:71], s85 offen lds\n" \
"v_mfma_f32_16x16x32_bf16 a[40:43], v[92:95], v[56:59], a[40:43]\n" \
"ds_read_b64_tr_b16 v[172:173], v38 offset:36864\n" \
"v_mfma_f32_16x16x32_bf16 a[12:15], v[96:99], v[52:55], a[12:15]\n" \
"ds_read_b64_tr_b16 v[174:175], v38 offset:37376\n" \
"v_mfma_f32_16x16x32_bf16 a[44:47], v[96:99], v[56:59], a[44:47]\n" \
"ds_read_b64_tr_b16 v[176:177], v39 offset:36864\n" \
"s_waitcnt lgkmcnt(8)\n" \
"s_nop 0\n" \
"v_mfma_f32_16x16x32_bf16 a[16:19], v[100:103], v[68:71], a[16:19]\n" \
"ds_read_b64_tr_b16 v[178:179], v39 offset:37376\n" \
"v_mfma_f32_16x16x32_bf16 a[20:23], v[104:107], v[68:71], a[20:23]\n" \
"ds_read_b64_tr_b16 v[148:149], v32 offset:40960\n" \
"v_mfma_f32_16x16x32_bf16 a[52:55], v[112:115], v[68:71], a[52:55]\n" \
"ds_read_b64_tr_b16 v[150:151], v32 offset:41984\n" \
"v_mfma_f32_16x16x32_bf16 a[48:51], v[68:71], v[108:111], a[48:51]\n" \
"ds_read_b64_tr_b16 v[152:153], v33 offset:40960\n" \
"v_mfma_f32_16x16x32_bf16 a[64:67], v[84:87], v[60:63], a[64:67]\n" \
"ds_read_b64_tr_b16 v[154:155], v33 offset:41984\n" \
"v_mfma_f32_16x16x32_bf16 a[96:99], v[84:87], v[64:67], a[96:99]\n" \
"ds_read_b64_tr_b16 v[156:157], v34 offset:40960\n" \
"v_mfma_f32_16x16x32_bf16 a[68:71], v[88:91], v[60:63], a[68:71]\n" \
"ds_read_b64_tr_b16 v[158:159], v34 offset:41984\n" \
"v_mfma_f32_16x16x32_bf16 a[100:103], v[88:91], v[64:67], a[100:103]\n" \
"v_mfma_f32_16x16x32_bf16 a[72:75], v[92:95], v[60:63], a[72:75]\n" \
"ds_read_b64_tr_b16 v[160:161], v35 offset:40960\n" \
"v_mfma_f32_16x16x32_bf16 a[104:107], v[92:95], v[64:67], a[104:107]\n" \
"v_mfma_f32_16x16x32_bf16 a[76:79], v[96:99], v[60:63], a[76:79]\n" \
"ds_read_b64_tr_b16 v[162:163], v35 offset:41984\n" \
"v_mfma_f32_16x16x32_bf16 a[108:111], v[96:99], v[64:67], a[108:111]\n" \
"ds_read_b64_tr_b16 v[164:165], v36 offset:36864\n" \
"s_waitcnt lgkmcnt(15)\n" \
"v_mfma_f32_16x16x32_bf16 a[80:83], v[100:103], v[72:75], a[80:83]\n" \
"v_mfma_f32_16x16x32_bf16 a[84:87], v[104:107], v[72:75], a[84:87]\n" \
"ds_read_b64_tr_b16 v[166:167], v36 offset:37376\n" \
"v_mfma_f32_16x16x32_bf16 a[116:119], v[112:115], v[72:75], a[116:119]\n" \
"s_add_u32 s88, s42, 0x0\n" \
"buffer_store_dwordx4 a[92:95], v20, s[80:83], s88 offen offset:1024 sc1\n" \
"v_mfma_f32_16x16x32_bf16 a[112:115], v[72:75], v[108:111], a[112:115]\n" \
"ds_read_b128 v[116:119], v29 offset:32768\n" \
"v_mfma_f32_16x16x32_bf16 a[128:131], v[84:87], v[68:71], a[128:131]\n" \
"ds_read_b128 v[120:123], v29 offset:34816\n" \
"v_mfma_f32_16x16x32_bf16 a[160:163], v[84:87], v[72:75], a[160:163]\n" \
"v_mfma_f32_16x16x32_bf16 a[132:135], v[88:91], v[68:71], a[132:135]\n" \
"ds_read_b128 v[124:127], v29 offset:36864\n" \
"v_mfma_f32_16x16x32_bf16 a[164:167], v[88:91], v[72:75], a[164:167]\n" \
"ds_read_b128 v[128:131], v29 offset:38912\n" \
"v_mfma_f32_16x16x32_bf16 a[136:139], v[92:95], v[68:71], a[136:139]\n" \
"v_mfma_f32_16x16x32_bf16 a[168:171], v[92:95], v[72:75], a[168:171]\n" \
"ds_read_b128 v[132:135], v29 offset:40960\n" \
"v_mfma_f32_16x16x32_bf16 a[140:143], v[96:99], v[68:71], a[140:143]\n" \
"v_mfma_f32_16x16x32_bf16 a[172:175], v[96:99], v[72:75], a[172:175]\n" \
"s_add_u32 s89, s42, 0x0\n" \
"s_nop 0\n" \
"buffer_store_dwordx4 a[124:127], v20, s[80:83], s89 offen offset:2048 sc1\n" \
"v_mfma_f32_16x16x32_bf16 a[144:147], v[100:103], v[76:79], a[144:147]\n" \
"ds_read_b128 v[136:139], v29 offset:43008\n" \
"v_mfma_f32_16x16x32_bf16 a[148:151], v[104:107], v[76:79], a[148:151]\n" \
"ds_read_b128 v[140:143], v29 offset:45056\n" \
"v_mfma_f32_16x16x32_bf16 a[180:183], v[112:115], v[76:79], a[180:183]\n" \
"ds_read_b128 v[144:147], v29 offset:47104\n" \
"v_mfma_f32_16x16x32_bf16 a[176:179], v[76:79], v[108:111], a[176:179]\n" \
"v_mfma_f32_16x16x32_bf16 a[192:195], v[84:87], v[76:79], a[192:195]\n" \
"v_mfma_f32_16x16x32_bf16 a[224:227], v[84:87], v[80:83], a[224:227]\n" \
"v_mfma_f32_16x16x32_bf16 a[196:199], v[88:91], v[76:79], a[196:199]\n" \
"v_mfma_f32_16x16x32_bf16 a[228:231], v[88:91], v[80:83], a[228:231]\n" \
"s_add_u32 s88, s42, 0x0\n" \
"buffer_store_dwordx4 a[152:155], v20, s[80:83], s88 offen offset:3072 sc1\n" \
"v_mfma_f32_16x16x32_bf16 a[200:203], v[92:95], v[76:79], a[200:203]\n" \
"v_mfma_f32_16x16x32_bf16 a[232:235], v[92:95], v[80:83], a[232:235]\n" \
"v_mfma_f32_16x16x32_bf16 a[204:207], v[96:99], v[76:79], a[204:207]\n" \
"v_mfma_f32_16x16x32_bf16 a[236:239], v[96:99], v[80:83], a[236:239]\n" \
"v_mfma_f32_16x16x32_bf16 a[208:211], v[100:103], v[80:83], a[208:211]\n" \
"v_mfma_f32_16x16x32_bf16 a[212:215], v[104:107], v[80:83], a[212:215]\n" \
"v_mfma_f32_16x16x32_bf16 a[244:247], v[112:115], v[80:83], a[244:247]\n" \
"v_mfma_f32_16x16x32_bf16 a[240:243], v[80:83], v[108:111], a[240:243]\n" \
"s_waitcnt vmcnt(3) lgkmcnt(0)\n" \
"s_barrier\n" \
"s_nop 0\n" \
"v_mfma_f32_16x16x32_bf16 a[24:27], v[168:171], v[128:131], a[24:27]\n" \
"s_add_u32 m0, s40, 0x8000\n" \
"s_add_u32 s85, s39, 0xe000\n" \
"v_mfma_f32_16x16x32_bf16 a[28:31], v[128:131], v[172:175], a[28:31]\n" \
"buffer_load_dwordx4 v20, s[80:83], s85 offen sc1 lds\n" \
"v_mfma_f32_16x16x32_bf16 a[56:59], v[176:179], v[128:131], a[56:59]\n" \
"ds_read_b64_tr_b16 v[104:105], v37 offset:0\n" \
"v_mfma_f32_16x16x32_bf16 a[0:3], v[148:151], v[116:119], a[0:3]\n" \
"ds_read_b64_tr_b16 v[106:107], v37 offset:512\n" \
"v_mfma_f32_16x16x32_bf16 a[32:35], v[148:151], v[120:123], a[32:35]\n" \
"s_add_u32 m0, s40, 0x9000\n" \
"s_add_u32 s84, s39, 0xf000\n" \
"v_mfma_f32_16x16x32_bf16 a[4:7], v[152:155], v[116:119], a[4:7]\n" \
"buffer_load_dwordx4 v20, s[80:83], s84 offen sc1 lds\n" \
"v_mfma_f32_16x16x32_bf16 a[36:39], v[152:155], v[120:123], a[36:39]\n" \
"ds_read_b64_tr_b16 v[108:109], v38 offset:0\n" \
"v_mfma_f32_16x16x32_bf16 a[8:11], v[156:159], v[116:119], a[8:11]\n" \
"ds_read_b64_tr_b16 v[110:111], v38 offset:512\n" \
"v_mfma_f32_16x16x32_bf16 a[40:43], v[156:159], v[120:123], a[40:43]\n" \
"s_add_u32 m0, s40, 0xb000\n" \
"s_mov_b32 s85, 0x380\n" \
"v_mfma_f32_16x16x32_bf16 a[12:15], v[160:163], v[116:119], a[12:15]\n" \
"buffer_load_dwordx4 v21, s[76:79], s85 offen lds\n" \
"v_mfma_f32_16x16x32_bf16 a[44:47], v[160:163], v[120:123], a[44:47]\n" \
"ds_read_b64_tr_b16 v[112:113], v39 offset:0\n" \
"v_mfma_f32_16x16x32_bf16 a[16:19], v[164:167], v[132:135], a[16:19]\n" \
"ds_read_b64_tr_b16 v[114:115], v39 offset:512\n" \
"v_mfma_f32_16x16x32_bf16 a[20:23], v[168:171], v[132:135], a[20:23]\n" \
"s_add_u32 m0, s30, 0x8000\n" \
"s_mov_b32 s84, 0x380\n" \
"v_mfma_f32_16x16x32_bf16 a[52:55], v[176:179], v[132:135], a[52:55]\n" \
"buffer_load_dwordx4 v24, s[76:79], s84 offen lds\n" \
"v_mfma_f32_16x16x32_bf16 a[48:51], v[132:135], v[172:175], a[48:51]\n" \
"ds_read_b64_tr_b16 v[84:85], v32 offset:0\n" \
"v_mfma_f32_16x16x32_bf16 a[64:67], v[148:151], v[124:127], a[64:67]\n" \
"ds_read_b64_tr_b16 v[86:87], v32 offset:1024\n" \
"v_mfma_f32_16x16x32_bf16 a[96:99], v[148:151], v[128:131], a[96:99]\n" \
"ds_read_b64_tr_b16 v[88:89], v33 offset:0\n" \
"v_mfma_f32_16x16x32_bf16 a[68:71], v[152:155], v[124:127], a[68:71]\n" \
"s_add_u32 m0, s30, 0x8400\n" \
"s_mov_b32 s85, 0x2780\n" \
"v_mfma_f32_16x16x32_bf16 a[100:103], v[152:155], v[128:131], a[100:103]\n" \
"buffer_load_dwordx4 v25, s[76:79], s85 offen lds\n" \
"v_mfma_f32_16x16x32_bf16 a[72:75], v[156:159], v[124:127], a[72:75]\n" \
"ds_read_b64_tr_b16 v[90:91], v33 offset:1024\n" \
"v_mfma_f32_16x16x32_bf16 a[104:107], v[156:159], v[128:131], a[104:107]\n" \
"ds_read_b64_tr_b16 v[92:93], v34 offset:0\n" \
"v_mfma_f32_16x16x32_bf16 a[76:79], v[160:163], v[124:127], a[76:79]\n" \
"s_add_u32 m0, s31, 0x8000\n" \
"s_mov_b32 s84, 0x380000\n" \
"v_mfma_f32_16x16x32_bf16 a[108:111], v[160:163], v[128:131], a[108:111]\n" \
"buffer_load_dwordx4 v26, s[68:71], s84 offen lds\n" \
"v_mfma_f32_16x16x32_bf16 a[80:83], v[164:167], v[136:139], a[80:83]\n" \
"ds_read_b64_tr_b16 v[94:95], v34 offset:1024\n" \
"v_mfma_f32_16x16x32_bf16 a[84:87], v[168:171], v[136:139], a[84:87]\n" \
"ds_read_b64_tr_b16 v[96:97], v35 offset:0\n" \
"v_mfma_f32_16x16x32_bf16 a[116:119], v[176:179], v[136:139], a[116:119]\n" \
"s_add_u32 m0, s30, 0x8800\n" \
"s_mov_b32 s85, 0x4b80\n" \
"v_mfma_f32_16x16x32_bf16 a[112:115], v[136:139], v[172:175], a[112:115]\n" \
"buffer_load_dwordx4 v24, s[76:79], s85 offen lds\n" \
"v_mfma_f32_16x16x32_bf16 a[128:131], v[148:151], v[132:135], a[128:131]\n" \
"ds_read_b64_tr_b16 v[98:99], v35 offset:1024\n" \
"v_mfma_f32_16x16x32_bf16 a[160:163], v[148:151], v[136:139], a[160:163]\n" \
"ds_read_b64_tr_b16 v[100:101], v36 offset:0\n" \
"v_mfma_f32_16x16x32_bf16 a[132:135], v[152:155], v[132:135], a[132:135]\n" \
"s_add_u32 m0, s30, 0x8c00\n" \
"s_mov_b32 s84, 0x6f80\n" \
"v_mfma_f32_16x16x32_bf16 a[164:167], v[152:155], v[136:139], a[164:167]\n" \
"buffer_load_dwordx4 v25, s[76:79], s84 offen lds\n" \
"v_mfma_f32_16x16x32_bf16 a[136:139], v[156:159], v[132:135], a[136:139]\n" \
"ds_read_b64_tr_b16 v[102:103], v36 offset:512\n" \
"v_mfma_f32_16x16x32_bf16 a[168:171], v[156:159], v[136:139], a[168:171]\n" \
"ds_read_b128 v[52:55], v28 offset:0\n" \
"v_mfma_f32_16x16x32_bf16 a[140:143], v[160:163], v[132:135], a[140:143]\n" \
"ds_read_b128 v[56:59], v28 offset:2048\n" \
"v_mfma_f32_16x16x32_bf16 a[172:175], v[160:163], v[136:139], a[172:175]\n" \
"s_add_u32 m0, s31, 0x8400\n" \
"s_mov_b32 s85, 0x388000\n" \
"v_mfma_f32_16x16x32_bf16 a[144:147], v[164:167], v[140:143], a[144:147]\n" \
"buffer_load_dwordx4 v26, s[68:71], s85 offen lds\n" \
"v_mfma_f32_16x16x32_bf16 a[148:151], v[168:171], v[140:143], a[148:151]\n" \
"ds_read_b128 v[60:63], v28 offset:4096\n" \
"v_mfma_f32_16x16x32_bf16 a[180:183], v[176:179], v[140:143], a[180:183]\n" \
"ds_read_b128 v[64:67], v28 offset:6144\n" \
"v_mfma_f32_16x16x32_bf16 a[176:179], v[140:143], v[172:175], a[176:179]\n" \
"s_add_u32 m0, s30, 0x9000\n" \
"s_mov_b32 s84, 0x9380\n" \
"v_mfma_f32_16x16x32_bf16 a[192:195], v[148:151], v[140:143], a[192:195]\n" \
"buffer_load_dwordx4 v24, s[76:79], s84 offen lds\n" \
"v_mfma_f32_16x16x32_bf16 a[224:227], v[148:151], v[144:147], a[224:227]\n" \
"ds_read_b128 v[68:71], v28 offset:8192\n" \
"v_mfma_f32_16x16x32_bf16 a[196:199], v[152:155], v[140:143], a[196:199]\n" \
"ds_read_b128 v[72:75], v28 offset:10240\n" \
"v_mfma_f32_16x16x32_bf16 a[228:231], v[152:155], v[144:147], a[228:231]\n" \
"s_add_u32 m0, s30, 0x9400\n" \
"s_mov_b32 s85, 0xb780\n" \
"v_mfma_f32_16x16x32_bf16 a[200:203], v[156:159], v[140:143], a[200:203]\n" \
"buffer_load_dwordx4 v25, s[76:79], s85 offen lds\n" \
"v_mfma_f32_16x16x32_bf16 a[232:235], v[156:159], v[144:147], a[232:235]\n" \
"ds_read_b128 v[76:79], v28 offset:12288\n" \
"v_mfma_f32_16x16x32_bf16 a[204:207], v[160:163], v[140:143], a[204:207]\n" \
"ds_read_b128 v[80:83], v28 offset:14336\n" \
"v_mfma_f32_16x16x32_bf16 a[236:239], v[160:163], v[144:147], a[236:239]\n" \
"s_add_u32 m0, s31, 0x8800\n" \
"s_mov_b32 s84, 0x390000\n" \
"v_mfma_f32_16x16x32_bf16 a[208:211], v[164:167], v[144:147], a[208:211]\n" \
"buffer_load_dwordx4 v27, s[68:71], s84 offen lds\n" \
"v_mfma_f32_16x16x32_bf16 a[212:215], v[168:171], v[144:147], a[212:215]\n" \
"v_mfma_f32_16x16x32_bf16 a[244:247], v[176:179], v[144:147], a[244:247]\n" \
"v_mfma_f32_16x16x32_bf16 a[240:243], v[144:147], v[172:175], a[240:243]\n" \
"s_waitcnt lgkmcnt(4)\n" \
"s_nop 0\n" \
"v_mfma_f32_16x16x32_bf16 a[24:27], v[104:107], v[64:67], a[24:27]\n" \
"s_add_u32 m0, s30, 0x9800\n" \
"s_mov_b32 s84, 0xdb80\n" \
"v_mfma_f32_16x16x32_bf16 a[28:31], v[64:67], v[108:111], a[28:31]\n" \
"buffer_load_dwordx4 v24, s[76:79], s84 offen lds\n" \
"v_mfma_f32_16x16x32_bf16 a[56:59], v[112:115], v[64:67], a[56:59]\n" \
"ds_read_b64_tr_b16 v[168:169], v37 offset:4096\n" \
"v_mfma_f32_16x16x32_bf16 a[0:3], v[84:87], v[52:55], a[0:3]\n" \
"s_add_u32 m0, s30, 0x9c00\n" \
"s_mov_b32 s85, 0xff80\n" \
"v_mfma_f32_16x16x32_bf16 a[32:35], v[84:87], v[56:59], a[32:35]\n" \
"buffer_load_dwordx4 v25, s[76:79], s85 offen lds\n" \
"v_mfma_f32_16x16x32_bf16 a[4:7], v[88:91], v[52:55], a[4:7]\n" \
"ds_read_b64_tr_b16 v[170:171], v37 offset:4608\n" \
"v_mfma_f32_16x16x32_bf16 a[36:39], v[88:91], v[56:59], a[36:39]\n" \
"s_add_u32 m0, s31, 0x8c00\n" \
"s_mov_b32 s84, 0x398000\n" \
"v_mfma_f32_16x16x32_bf16 a[8:11], v[92:95], v[52:55], a[8:11]\n" \
"buffer_load_dwordx4 v27, s[68:71], s84 offen lds\n" \
"v_mfma_f32_16x16x32_bf16 a[40:43], v[92:95], v[56:59], a[40:43]\n" \
"ds_read_b64_tr_b16 v[172:173], v38 offset:4096\n" \
"v_mfma_f32_16x16x32_bf16 a[12:15], v[96:99], v[52:55], a[12:15]\n" \
"ds_read_b64_tr_b16 v[174:175], v38 offset:4608\n" \
"v_mfma_f32_16x16x32_bf16 a[44:47], v[96:99], v[56:59], a[44:47]\n" \
"ds_read_b64_tr_b16 v[176:177], v39 offset:4096\n" \
"s_waitcnt lgkmcnt(8)\n" \
"s_nop 0\n" \
"v_mfma_f32_16x16x32_bf16 a[16:19], v[100:103], v[68:71], a[16:19]\n" \
"ds_read_b64_tr_b16 v[178:179], v39 offset:4608\n" \
"v_mfma_f32_16x16x32_bf16 a[20:23], v[104:107], v[68:71], a[20:23]\n" \
"ds_read_b64_tr_b16 v[148:149], v32 offset:8192\n" \
"v_mfma_f32_16x16x32_bf16 a[52:55], v[112:115], v[68:71], a[52:55]\n" \
"ds_read_b64_tr_b16 v[150:151], v32 offset:9216\n" \
"v_mfma_f32_16x16x32_bf16 a[48:51], v[68:71], v[108:111], a[48:51]\n" \
"ds_read_b64_tr_b16 v[152:153], v33 offset:8192\n" \
"v_mfma_f32_16x16x32_bf16 a[64:67], v[84:87], v[60:63], a[64:67]\n" \
"ds_read_b64_tr_b16 v[154:155], v33 offset:9216\n" \
"v_mfma_f32_16x16x32_bf16 a[96:99], v[84:87], v[64:67], a[96:99]\n" \
"ds_read_b64_tr_b16 v[156:157], v34 offset:8192\n" \
"v_mfma_f32_16x16x32_bf16 a[68:71], v[88:91], v[60:63], a[68:71]\n" \
"ds_read_b64_tr_b16 v[158:159], v34 offset:9216\n" \
"v_mfma_f32_16x16x32_bf16 a[100:103], v[88:91], v[64:67], a[100:103]\n" \
"v_mfma_f32_16x16x32_bf16 a[72:75], v[92:95], v[60:63], a[72:75]\n" \
"ds_read_b64_tr_b16 v[160:161], v35 offset:8192\n" \
"v_mfma_f32_16x16x32_bf16 a[104:107], v[92:95], v[64:67], a[104:107]\n" \
"v_mfma_f32_16x16x32_bf16 a[76:79], v[96:99], v[60:63], a[76:79]\n" \
"ds_read_b64_tr_b16 v[162:163], v35 offset:9216\n" \
"v_mfma_f32_16x16x32_bf16 a[108:111], v[96:99], v[64:67], a[108:111]\n" \
"ds_read_b64_tr_b16 v[164:165], v36 offset:4096\n" \
"s_waitcnt lgkmcnt(15)\n" \
"s_nop 0\n" \
"v_mfma_f32_16x16x32_bf16 a[80:83], v[100:103], v[72:75], a[80:83]\n" \
"v_mfma_f32_16x16x32_bf16 a[84:87], v[104:107], v[72:75], a[84:87]\n" \
"ds_read_b64_tr_b16 v[166:167], v36 offset:4608\n" \
"v_mfma_f32_16x16x32_bf16 a[116:119], v[112:115], v[72:75], a[116:119]\n" \
"s_add_u32 s89, s42, 0x1000\n" \
"buffer_store_dwordx4 a[184:187], v20, s[80:83], s89 offen offset:0 sc1\n" \
"v_mfma_f32_16x16x32_bf16 a[112:115], v[72:75], v[108:111], a[112:115]\n" \
"ds_read_b128 v[116:119], v29 offset:0\n" \
"v_mfma_f32_16x16x32_bf16 a[128:131], v[84:87], v[68:71], a[128:131]\n" \
"ds_read_b128 v[120:123], v29 offset:2048\n" \
"v_mfma_f32_16x16x32_bf16 a[160:163], v[84:87], v[72:75], a[160:163]\n" \
"v_mfma_f32_16x16x32_bf16 a[132:135], v[88:91], v[68:71], a[132:135]\n" \
"ds_read_b128 v[124:127], v29 offset:4096\n" \
"v_mfma_f32_16x16x32_bf16 a[164:167], v[88:91], v[72:75], a[164:167]\n" \
"ds_read_b128 v[128:131], v29 offset:6144\n" \
"v_mfma_f32_16x16x32_bf16 a[136:139], v[92:95], v[68:71], a[136:139]\n" \
"v_mfma_f32_16x16x32_bf16 a[168:171], v[92:95], v[72:75], a[168:171]\n" \
"ds_read_b128 v[132:135], v29 offset:8192\n" \
"v_mfma_f32_16x16x32_bf16 a[140:143], v[96:99], v[68:71], a[140:143]\n" \
"v_mfma_f32_16x16x32_bf16 a[172:175], v[96:99], v[72:75], a[172:175]\n" \
"s_add_u32 s88, s42, 0x1000\n" \
"buffer_store_dwordx4 a[156:159], v20, s[80:83], s88 offen offset:1024 sc1\n" \
"v_mfma_f32_16x16x32_bf16 a[144:147], v[100:103], v[76:79], a[144:147]\n" \
"ds_read_b128 v[136:139], v29 offset:10240\n" \
"v_mfma_f32_16x16x32_bf16 a[148:151], v[104:107], v[76:79], a[148:151]\n" \
"ds_read_b128 v[140:143], v29 offset:12288\n" \
"v_mfma_f32_16x16x32_bf16 a[180:183], v[112:115], v[76:79], a[180:183]\n" \
"ds_read_b128 v[144:147], v29 offset:14336\n" \
"v_mfma_f32_16x16x32_bf16 a[176:179], v[76:79], v[108:111], a[176:179]\n" \
"v_mfma_f32_16x16x32_bf16 a[192:195], v[84:87], v[76:79], a[192:195]\n" \
"v_mfma_f32_16x16x32_bf16 a[224:227], v[84:87], v[80:83], a[224:227]\n" \
"v_mfma_f32_16x16x32_bf16 a[196:199], v[88:91], v[76:79], a[196:199]\n" \
"v_mfma_f32_16x16x32_bf16 a[228:231], v[88:91], v[80:83], a[228:231]\n" \
"s_add_u32 s89, s42, 0x3000\n" \
"buffer_store_dwordx4 a[188:191], v20, s[80:83], s89 offen offset:2048 sc1\n" \
"v_mfma_f32_16x16x32_bf16 a[200:203], v[92:95], v[76:79], a[200:203]\n" \
"v_mfma_f32_16x16x32_bf16 a[232:235], v[92:95], v[80:83], a[232:235]\n" \
"v_mfma_f32_16x16x32_bf16 a[204:207], v[96:99], v[76:79], a[204:207]\n" \
"v_mfma_f32_16x16x32_bf16 a[236:239], v[96:99], v[80:83], a[236:239]\n" \
"v_mfma_f32_16x16x32_bf16 a[208:211], v[100:103], v[80:83], a[208:211]\n" \
"v_mfma_f32_16x16x32_bf16 a[212:215], v[104:107], v[80:83], a[212:215]\n" \
"v_mfma_f32_16x16x32_bf16 a[244:247], v[112:115], v[80:83], a[244:247]\n" \
"v_mfma_f32_16x16x32_bf16 a[240:243], v[80:83], v[108:111], a[240:243]\n" \
"s_waitcnt vmcnt(3) lgkmcnt(0)\n" \
"s_barrier\n" \
"v_mfma_f32_16x16x32_bf16 a[24:27], v[168:171], v[128:131], a[24:27]\n" \
"s_add_u32 m0, s40, 0x0\n" \
"s_nop 0\n" \
"s_add_u32 s84, s39, 0x10000\n" \
"v_mfma_f32_16x16x32_bf16 a[28:31], v[128:131], v[172:175], a[28:31]\n" \
"buffer_load_dwordx4 v20, s[80:83], s84 offen sc1 lds\n" \
"v_mfma_f32_16x16x32_bf16 a[56:59], v[176:179], v[128:131], a[56:59]\n" \
"ds_read_b64_tr_b16 v[104:105], v37 offset:32768\n" \
"v_mfma_f32_16x16x32_bf16 a[0:3], v[148:151], v[116:119], a[0:3]\n" \
"ds_read_b64_tr_b16 v[106:107], v37 offset:33280\n" \
"v_mfma_f32_16x16x32_bf16 a[32:35], v[148:151], v[120:123], a[32:35]\n" \
"s_add_u32 m0, s40, 0x1000\n" \
"s_add_u32 s85, s39, 0x11000\n" \
"v_mfma_f32_16x16x32_bf16 a[4:7], v[152:155], v[116:119], a[4:7]\n" \
"buffer_load_dwordx4 v20, s[80:83], s85 offen sc1 lds\n" \
"v_mfma_f32_16x16x32_bf16 a[36:39], v[152:155], v[120:123], a[36:39]\n" \
"ds_read_b64_tr_b16 v[108:109], v38 offset:32768\n" \
"v_mfma_f32_16x16x32_bf16 a[8:11], v[156:159], v[116:119], a[8:11]\n" \
"ds_read_b64_tr_b16 v[110:111], v38 offset:33280\n" \
"v_mfma_f32_16x16x32_bf16 a[40:43], v[156:159], v[120:123], a[40:43]\n" \
"s_add_u32 m0, s40, 0x3000\n" \
"s_mov_b32 s84, 0x400\n" \
"v_mfma_f32_16x16x32_bf16 a[12:15], v[160:163], v[116:119], a[12:15]\n" \
"buffer_load_dwordx4 v21, s[76:79], s84 offen lds\n" \
"v_mfma_f32_16x16x32_bf16 a[44:47], v[160:163], v[120:123], a[44:47]\n" \
"ds_read_b64_tr_b16 v[112:113], v39 offset:32768\n" \
"v_mfma_f32_16x16x32_bf16 a[16:19], v[164:167], v[132:135], a[16:19]\n" \
"ds_read_b64_tr_b16 v[114:115], v39 offset:33280\n" \
"v_mfma_f32_16x16x32_bf16 a[20:23], v[168:171], v[132:135], a[20:23]\n" \
"s_add_u32 m0, s30, 0x0\n" \
"s_nop 0\n" \
"s_mov_b32 s85, 0x400\n" \
"v_mfma_f32_16x16x32_bf16 a[52:55], v[176:179], v[132:135], a[52:55]\n" \
"buffer_load_dwordx4 v24, s[76:79], s85 offen lds\n" \
"v_mfma_f32_16x16x32_bf16 a[48:51], v[132:135], v[172:175], a[48:51]\n" \
"ds_read_b64_tr_b16 v[84:85], v32 offset:32768\n" \
"v_mfma_f32_16x16x32_bf16 a[64:67], v[148:151], v[124:127], a[64:67]\n" \
"ds_read_b64_tr_b16 v[86:87], v32 offset:33792\n" \
"v_mfma_f32_16x16x32_bf16 a[96:99], v[148:151], v[128:131], a[96:99]\n" \
"ds_read_b64_tr_b16 v[88:89], v33 offset:32768\n" \
"v_mfma_f32_16x16x32_bf16 a[68:71], v[152:155], v[124:127], a[68:71]\n" \
"s_add_u32 m0, s30, 0x400\n" \
"s_mov_b32 s84, 0x2800\n" \
"v_mfma_f32_16x16x32_bf16 a[100:103], v[152:155], v[128:131], a[100:103]\n" \
"buffer_load_dwordx4 v25, s[76:79], s84 offen lds\n" \
"v_mfma_f32_16x16x32_bf16 a[72:75], v[156:159], v[124:127], a[72:75]\n" \
"ds_read_b64_tr_b16 v[90:91], v33 offset:33792\n" \
"v_mfma_f32_16x16x32_bf16 a[104:107], v[156:159], v[128:131], a[104:107]\n" \
"ds_read_b64_tr_b16 v[92:93], v34 offset:32768\n" \
"v_mfma_f32_16x16x32_bf16 a[76:79], v[160:163], v[124:127], a[76:79]\n" \
"s_add_u32 m0, s31, 0x0\n" \
"s_nop 0\n" \
"s_mov_b32 s85, 0x400000\n" \
"v_mfma_f32_16x16x32_bf16 a[108:111], v[160:163], v[128:131], a[108:111]\n" \
"buffer_load_dwordx4 v26, s[68:71], s85 offen lds\n" \
"v_mfma_f32_16x16x32_bf16 a[80:83], v[164:167], v[136:139], a[80:83]\n" \
"ds_read_b64_tr_b16 v[94:95], v34 offset:33792\n" \
"v_mfma_f32_16x16x32_bf16 a[84:87], v[168:171], v[136:139], a[84:87]\n" \
"ds_read_b64_tr_b16 v[96:97], v35 offset:32768\n" \
"v_mfma_f32_16x16x32_bf16 a[116:119], v[176:179], v[136:139], a[116:119]\n" \
"s_add_u32 m0, s30, 0x800\n" \
"s_mov_b32 s84, 0x4c00\n" \
"v_mfma_f32_16x16x32_bf16 a[112:115], v[136:139], v[172:175], a[112:115]\n" \
"buffer_load_dwordx4 v24, s[76:79], s84 offen lds\n" \
"v_mfma_f32_16x16x32_bf16 a[128:131], v[148:151], v[132:135], a[128:131]\n" \
"ds_read_b64_tr_b16 v[98:99], v35 offset:33792\n" \
"v_mfma_f32_16x16x32_bf16 a[160:163], v[148:151], v[136:139], a[160:163]\n" \
"ds_read_b64_tr_b16 v[100:101], v36 offset:32768\n" \
"v_mfma_f32_16x16x32_bf16 a[132:135], v[152:155], v[132:135], a[132:135]\n" \
"s_add_u32 m0, s30, 0xc00\n" \
"s_mov_b32 s85, 0x7000\n" \
"v_mfma_f32_16x16x32_bf16 a[164:167], v[152:155], v[136:139], a[164:167]\n" \
"buffer_load_dwordx4 v25, s[76:79], s85 offen lds\n" \
"v_mfma_f32_16x16x32_bf16 a[136:139], v[156:159], v[132:135], a[136:139]\n" \
"ds_read_b64_tr_b16 v[102:103], v36 offset:33280\n" \
"v_mfma_f32_16x16x32_bf16 a[168:171], v[156:159], v[136:139], a[168:171]\n" \
"ds_read_b128 v[52:55], v28 offset:32768\n" \
"v_mfma_f32_16x16x32_bf16 a[140:143], v[160:163], v[132:135], a[140:143]\n" \
"ds_read_b128 v[56:59], v28 offset:34816\n" \
"v_mfma_f32_16x16x32_bf16 a[172:175], v[160:163], v[136:139], a[172:175]\n" \
"s_add_u32 m0, s31, 0x400\n" \
"s_mov_b32 s84, 0x408000\n" \
"v_mfma_f32_16x16x32_bf16 a[144:147], v[164:167], v[140:143], a[144:147]\n" \
"buffer_load_dwordx4 v26, s[68:71], s84 offen lds\n" \
"v_mfma_f32_16x16x32_bf16 a[148:151], v[168:171], v[140:143], a[148:151]\n" \
"ds_read_b128 v[60:63], v28 offset:36864\n" \
"v_mfma_f32_16x16x32_bf16 a[180:183], v[176:179], v[140:143], a[180:183]\n" \
"ds_read_b128 v[64:67], v28 offset:38912\n" \
"v_mfma_f32_16x16x32_bf16 a[176:179], v[140:143], v[172:175], a[176:179]\n" \
"s_add_u32 m0, s30, 0x1000\n" \
"s_mov_b32 s85, 0x9400\n" \
"v_mfma_f32_16x16x32_bf16 a[192:195], v[148:151], v[140:143], a[192:195]\n" \
"buffer_load_dwordx4 v24, s[76:79], s85 offen lds\n" \
"v_mfma_f32_16x16x32_bf16 a[224:227], v[148:151], v[144:147], a[224:227]\n" \
"ds_read_b128 v[68:71], v28 offset:40960\n" \
"v_mfma_f32_16x16x32_bf16 a[196:199], v[152:155], v[140:143], a[196:199]\n" \
"ds_read_b128 v[72:75], v28 offset:43008\n" \
"v_mfma_f32_16x16x32_bf16 a[228:231], v[152:155], v[144:147], a[228:231]\n" \
"s_add_u32 m0, s30, 0x1400\n" \
"s_mov_b32 s84, 0xb800\n" \
"v_mfma_f32_16x16x32_bf16 a[200:203], v[156:159], v[140:143], a[200:203]\n" \
"buffer_load_dwordx4 v25, s[76:79], s84 offen lds\n" \
"v_mfma_f32_16x16x32_bf16 a[232:235], v[156:159], v[144:147], a[232:235]\n" \
"ds_read_b128 v[76:79], v28 offset:45056\n" \
"v_mfma_f32_16x16x32_bf16 a[204:207], v[160:163], v[140:143], a[204:207]\n" \
"ds_read_b128 v[80:83], v28 offset:47104\n" \
"v_mfma_f32_16x16x32_bf16 a[236:239], v[160:163], v[144:147], a[236:239]\n" \
"s_add_u32 m0, s31, 0x800\n" \
"s_mov_b32 s85, 0x410000\n" \
"v_mfma_f32_16x16x32_bf16 a[208:211], v[164:167], v[144:147], a[208:211]\n" \
"buffer_load_dwordx4 v27, s[68:71], s85 offen lds\n" \
"v_mfma_f32_16x16x32_bf16 a[212:215], v[168:171], v[144:147], a[212:215]\n" \
"v_mfma_f32_16x16x32_bf16 a[244:247], v[176:179], v[144:147], a[244:247]\n" \
"v_mfma_f32_16x16x32_bf16 a[240:243], v[144:147], v[172:175], a[240:243]\n" \
"s_waitcnt lgkmcnt(4)\n" \
"s_nop 0\n" \
"v_mfma_f32_16x16x32_bf16 a[24:27], v[104:107], v[64:67], a[24:27]\n" \
"s_add_u32 m0, s30, 0x1800\n" \
"s_mov_b32 s85, 0xdc00\n" \
"v_mfma_f32_16x16x32_bf16 a[28:31], v[64:67], v[108:111], a[28:31]\n" \
"buffer_load_dwordx4 v24, s[76:79], s85 offen lds\n" \
"v_mfma_f32_16x16x32_bf16 a[56:59], v[112:115], v[64:67], a[56:59]\n" \
"ds_read_b64_tr_b16 v[168:169], v37 offset:36864\n" \
"v_mfma_f32_16x16x32_bf16 a[0:3], v[84:87], v[52:55], a[0:3]\n" \
"s_add_u32 m0, s30, 0x1c00\n" \
"s_mov_b32 s84, 0x10000\n" \
"v_mfma_f32_16x16x32_bf16 a[32:35], v[84:87], v[56:59], a[32:35]\n" \
"buffer_load_dwordx4 v25, s[76:79], s84 offen lds\n" \
"v_mfma_f32_16x16x32_bf16 a[4:7], v[88:91], v[52:55], a[4:7]\n" \
"ds_read_b64_tr_b16 v[170:171], v37 offset:37376\n" \
"v_mfma_f32_16x16x32_bf16 a[36:39], v[88:91], v[56:59], a[36:39]\n" \
"s_add_u32 m0, s31, 0xc00\n" \
"s_mov_b32 s85, 0x418000\n" \
"v_mfma_f32_16x16x32_bf16 a[8:11], v[92:95], v[52:55], a[8:11]\n" \
"buffer_load_dwordx4 v27, s[68:71], s85 offen lds\n" \
"v_mfma_f32_16x16x32_bf16 a[40:43], v[92:95], v[56:59], a[40:43]\n" \
"ds_read_b64_tr_b16 v[172:173], v38 offset:36864\n" \
"v_mfma_f32_16x16x32_bf16 a[12:15], v[96:99], v[52:55], a[12:15]\n" \
"ds_read_b64_tr_b16 v[174:175], v38 offset:37376\n" \
"v_mfma_f32_16x16x32_bf16 a[44:47], v[96:99], v[56:59], a[44:47]\n" \
"ds_read_b64_tr_b16 v[176:177], v39 offset:36864\n" \
"s_waitcnt lgkmcnt(8)\n" \
"s_nop 0\n" \
"v_mfma_f32_16x16x32_bf16 a[16:19], v[100:103], v[68:71], a[16:19]\n" \
"ds_read_b64_tr_b16 v[178:179], v39 offset:37376\n" \
"v_mfma_f32_16x16x32_bf16 a[20:23], v[104:107], v[68:71], a[20:23]\n" \
"ds_read_b64_tr_b16 v[148:149], v32 offset:40960\n" \
"v_mfma_f32_16x16x32_bf16 a[52:55], v[112:115], v[68:71], a[52:55]\n" \
"ds_read_b64_tr_b16 v[150:151], v32 offset:41984\n" \
"v_mfma_f32_16x16x32_bf16 a[48:51], v[68:71], v[108:111], a[48:51]\n" \
"ds_read_b64_tr_b16 v[152:153], v33 offset:40960\n" \
"v_mfma_f32_16x16x32_bf16 a[64:67], v[84:87], v[60:63], a[64:67]\n" \
"ds_read_b64_tr_b16 v[154:155], v33 offset:41984\n" \
"v_mfma_f32_16x16x32_bf16 a[96:99], v[84:87], v[64:67], a[96:99]\n" \
"ds_read_b64_tr_b16 v[156:157], v34 offset:40960\n" \
"v_mfma_f32_16x16x32_bf16 a[68:71], v[88:91], v[60:63], a[68:71]\n" \
"ds_read_b64_tr_b16 v[158:159], v34 offset:41984\n" \
"v_mfma_f32_16x16x32_bf16 a[100:103], v[88:91], v[64:67], a[100:103]\n" \
"v_mfma_f32_16x16x32_bf16 a[72:75], v[92:95], v[60:63], a[72:75]\n" \
"ds_read_b64_tr_b16 v[160:161], v35 offset:40960\n" \
"v_mfma_f32_16x16x32_bf16 a[104:107], v[92:95], v[64:67], a[104:107]\n" \
"v_mfma_f32_16x16x32_bf16 a[76:79], v[96:99], v[60:63], a[76:79]\n" \
"ds_read_b64_tr_b16 v[162:163], v35 offset:41984\n" \
"v_mfma_f32_16x16x32_bf16 a[108:111], v[96:99], v[64:67], a[108:111]\n" \
"ds_read_b64_tr_b16 v[164:165], v36 offset:36864\n" \
"s_waitcnt lgkmcnt(15)\n" \
"s_nop 0\n" \
"v_mfma_f32_16x16x32_bf16 a[80:83], v[100:103], v[72:75], a[80:83]\n" \
"v_mfma_f32_16x16x32_bf16 a[84:87], v[104:107], v[72:75], a[84:87]\n" \
"ds_read_b64_tr_b16 v[166:167], v36 offset:37376\n" \
"v_mfma_f32_16x16x32_bf16 a[116:119], v[112:115], v[72:75], a[116:119]\n" \
"s_add_u32 s88, s42, 0x3000\n" \
"buffer_store_dwordx4 a[216:219], v20, s[80:83], s88 offen offset:3072 sc1\n" \
"v_mfma_f32_16x16x32_bf16 a[112:115], v[72:75], v[108:111], a[112:115]\n" \
"ds_read_b128 v[116:119], v29 offset:32768\n" \
"v_mfma_f32_16x16x32_bf16 a[128:131], v[84:87], v[68:71], a[128:131]\n" \
"ds_read_b128 v[120:123], v29 offset:34816\n" \
"v_mfma_f32_16x16x32_bf16 a[160:163], v[84:87], v[72:75], a[160:163]\n" \
"v_mfma_f32_16x16x32_bf16 a[132:135], v[88:91], v[68:71], a[132:135]\n" \
"ds_read_b128 v[124:127], v29 offset:36864\n" \
"v_mfma_f32_16x16x32_bf16 a[164:167], v[88:91], v[72:75], a[164:167]\n" \
"ds_read_b128 v[128:131], v29 offset:38912\n" \
"v_mfma_f32_16x16x32_bf16 a[136:139], v[92:95], v[68:71], a[136:139]\n" \
"v_mfma_f32_16x16x32_bf16 a[168:171], v[92:95], v[72:75], a[168:171]\n" \
"ds_read_b128 v[132:135], v29 offset:40960\n" \
"v_mfma_f32_16x16x32_bf16 a[140:143], v[96:99], v[68:71], a[140:143]\n" \
"v_mfma_f32_16x16x32_bf16 a[172:175], v[96:99], v[72:75], a[172:175]\n" \
"s_add_u32 s89, s42, 0x2000\n" \
"buffer_store_dwordx4 a[248:251], v20, s[80:83], s89 offen offset:0 sc1\n" \
"v_mfma_f32_16x16x32_bf16 a[144:147], v[100:103], v[76:79], a[144:147]\n" \
"ds_read_b128 v[136:139], v29 offset:43008\n" \
"v_mfma_f32_16x16x32_bf16 a[148:151], v[104:107], v[76:79], a[148:151]\n" \
"ds_read_b128 v[140:143], v29 offset:45056\n" \
"v_mfma_f32_16x16x32_bf16 a[180:183], v[112:115], v[76:79], a[180:183]\n" \
"ds_read_b128 v[144:147], v29 offset:47104\n" \
"v_mfma_f32_16x16x32_bf16 a[176:179], v[76:79], v[108:111], a[176:179]\n" \
"v_mfma_f32_16x16x32_bf16 a[192:195], v[84:87], v[76:79], a[192:195]\n" \
"v_mfma_f32_16x16x32_bf16 a[224:227], v[84:87], v[80:83], a[224:227]\n" \
"v_mfma_f32_16x16x32_bf16 a[196:199], v[88:91], v[76:79], a[196:199]\n" \
"v_mfma_f32_16x16x32_bf16 a[228:231], v[88:91], v[80:83], a[228:231]\n" \
"s_add_u32 s88, s42, 0x2000\n" \
"buffer_store_dwordx4 a[220:223], v20, s[80:83], s88 offen offset:1024 sc1\n" \
"v_mfma_f32_16x16x32_bf16 a[200:203], v[92:95], v[76:79], a[200:203]\n" \
"v_mfma_f32_16x16x32_bf16 a[232:235], v[92:95], v[80:83], a[232:235]\n" \
"v_mfma_f32_16x16x32_bf16 a[204:207], v[96:99], v[76:79], a[204:207]\n" \
"v_mfma_f32_16x16x32_bf16 a[236:239], v[96:99], v[80:83], a[236:239]\n" \
"v_mfma_f32_16x16x32_bf16 a[208:211], v[100:103], v[80:83], a[208:211]\n" \
"v_mfma_f32_16x16x32_bf16 a[212:215], v[104:107], v[80:83], a[212:215]\n" \
"v_mfma_f32_16x16x32_bf16 a[244:247], v[112:115], v[80:83], a[244:247]\n" \
"v_mfma_f32_16x16x32_bf16 a[240:243], v[80:83], v[108:111], a[240:243]\n" \
"s_waitcnt vmcnt(3) lgkmcnt(0)\n" \
"s_barrier\n" \
"v_mfma_f32_16x16x32_bf16 a[24:27], v[168:171], v[128:131], a[24:27]\n" \
"s_add_u32 s89, s42, 0x2000\n" \
"buffer_store_dwordx4 a[252:255], v20, s[80:83], s89 offen offset:2048 sc1\n" \
"v_mfma_f32_16x16x32_bf16 a[28:31], v[128:131], v[172:175], a[28:31]\n" \
"ds_read_b64_tr_b16 v[104:105], v37 offset:0\n" \
"v_mfma_f32_16x16x32_bf16 a[56:59], v[176:179], v[128:131], a[56:59]\n" \
"ds_read_b64_tr_b16 v[106:107], v37 offset:512\n" \
"v_mfma_f32_16x16x32_bf16 a[0:3], v[148:151], v[116:119], a[0:3]\n" \
"ds_read_b64_tr_b16 v[108:109], v38 offset:0\n" \
"v_mfma_f32_16x16x32_bf16 a[32:35], v[148:151], v[120:123], a[32:35]\n" \
"ds_read_b64_tr_b16 v[110:111], v38 offset:512\n" \
"v_mfma_f32_16x16x32_bf16 a[4:7], v[152:155], v[116:119], a[4:7]\n" \
"v_mfma_f32_16x16x32_bf16 a[36:39], v[152:155], v[120:123], a[36:39]\n" \
"ds_read_b64_tr_b16 v[112:113], v39 offset:0\n" \
"v_mfma_f32_16x16x32_bf16 a[8:11], v[156:159], v[116:119], a[8:11]\n" \
"v_mfma_f32_16x16x32_bf16 a[40:43], v[156:159], v[120:123], a[40:43]\n" \
"ds_read_b64_tr_b16 v[114:115], v39 offset:512\n" \
"v_mfma_f32_16x16x32_bf16 a[12:15], v[160:163], v[116:119], a[12:15]\n" \
"ds_read_b64_tr_b16 v[84:85], v32 offset:0\n" \
"v_mfma_f32_16x16x32_bf16 a[44:47], v[160:163], v[120:123], a[44:47]\n" \
"v_mfma_f32_16x16x32_bf16 a[16:19], v[164:167], v[132:135], a[16:19]\n" \
"ds_read_b64_tr_b16 v[86:87], v32 offset:1024\n" \
"v_mfma_f32_16x16x32_bf16 a[20:23], v[168:171], v[132:135], a[20:23]\n" \
"s_add_u32 s88, s42, 0x2000\n" \
"buffer_store_dwordx4 v[184:187], v20, s[80:83], s88 offen offset:3072 sc1\n" \
"v_mfma_f32_16x16x32_bf16 a[52:55], v[176:179], v[132:135], a[52:55]\n" \
"ds_read_b64_tr_b16 v[88:89], v33 offset:0\n" \
"v_mfma_f32_16x16x32_bf16 a[48:51], v[132:135], v[172:175], a[48:51]\n" \
"ds_read_b64_tr_b16 v[90:91], v33 offset:1024\n" \
"v_mfma_f32_16x16x32_bf16 a[64:67], v[148:151], v[124:127], a[64:67]\n" \
"v_mfma_f32_16x16x32_bf16 a[96:99], v[148:151], v[128:131], a[96:99]\n" \
"ds_read_b64_tr_b16 v[92:93], v34 offset:0\n" \
"v_mfma_f32_16x16x32_bf16 a[68:71], v[152:155], v[124:127], a[68:71]\n" \
"ds_read_b64_tr_b16 v[94:95], v34 offset:1024\n" \
"v_mfma_f32_16x16x32_bf16 a[100:103], v[152:155], v[128:131], a[100:103]\n" \
"v_mfma_f32_16x16x32_bf16 a[72:75], v[156:159], v[124:127], a[72:75]\n" \
"ds_read_b64_tr_b16 v[96:97], v35 offset:0\n" \
"v_mfma_f32_16x16x32_bf16 a[104:107], v[156:159], v[128:131], a[104:107]\n" \
"v_mfma_f32_16x16x32_bf16 a[76:79], v[160:163], v[124:127], a[76:79]\n" \
"ds_read_b64_tr_b16 v[98:99], v35 offset:1024\n" \
"v_mfma_f32_16x16x32_bf16 a[108:111], v[160:163], v[128:131], a[108:111]\n" \
"ds_read_b64_tr_b16 v[100:101], v36 offset:0\n" \
"v_mfma_f32_16x16x32_bf16 a[80:83], v[164:167], v[136:139], a[80:83]\n" \
"v_mfma_f32_16x16x32_bf16 a[84:87], v[168:171], v[136:139], a[84:87]\n" \
"ds_read_b64_tr_b16 v[102:103], v36 offset:512\n" \
"v_mfma_f32_16x16x32_bf16 a[116:119], v[176:179], v[136:139], a[116:119]\n" \
"s_add_u32 s89, s42, 0x3000\n" \
"buffer_store_dwordx4 v[188:191], v20, s[80:83], s89 offen offset:0 sc1\n" \
"v_mfma_f32_16x16x32_bf16 a[112:115], v[136:139], v[172:175], a[112:115]\n" \
"ds_read_b128 v[52:55], v28 offset:0\n" \
"v_mfma_f32_16x16x32_bf16 a[128:131], v[148:151], v[132:135], a[128:131]\n" \
"ds_read_b128 v[56:59], v28 offset:2048\n" \
"v_mfma_f32_16x16x32_bf16 a[160:163], v[148:151], v[136:139], a[160:163]\n" \
"v_mfma_f32_16x16x32_bf16 a[132:135], v[152:155], v[132:135], a[132:135]\n" \
"ds_read_b128 v[60:63], v28 offset:4096\n" \
"v_mfma_f32_16x16x32_bf16 a[164:167], v[152:155], v[136:139], a[164:167]\n" \
"ds_read_b128 v[64:67], v28 offset:6144\n" \
"v_mfma_f32_16x16x32_bf16 a[136:139], v[156:159], v[132:135], a[136:139]\n" \
"v_mfma_f32_16x16x32_bf16 a[168:171], v[156:159], v[136:139], a[168:171]\n" \
"ds_read_b128 v[68:71], v28 offset:8192\n" \
"v_mfma_f32_16x16x32_bf16 a[140:143], v[160:163], v[132:135], a[140:143]\n" \
"v_mfma_f32_16x16x32_bf16 a[172:175], v[160:163], v[136:139], a[172:175]\n" \
"ds_read_b128 v[72:75], v28 offset:10240\n" \
"v_mfma_f32_16x16x32_bf16 a[144:147], v[164:167], v[140:143], a[144:147]\n" \
"ds_read_b128 v[76:79], v28 offset:12288\n" \
"v_mfma_f32_16x16x32_bf16 a[148:151], v[168:171], v[140:143], a[148:151]\n" \
"v_mfma_f32_16x16x32_bf16 a[180:183], v[176:179], v[140:143], a[180:183]\n" \
"ds_read_b128 v[80:83], v28 offset:14336\n" \
"v_mfma_f32_16x16x32_bf16 a[176:179], v[140:143], v[172:175], a[176:179]\n" \
"s_add_u32 s88, s42, 0x3000\n" \
"buffer_store_dwordx4 v[252:255], v20, s[80:83], s88 offen offset:1024 sc1\n" \
"v_mfma_f32_16x16x32_bf16 a[192:195], v[148:151], v[140:143], a[192:195]\n" \
"v_mfma_f32_16x16x32_bf16 a[224:227], v[148:151], v[144:147], a[224:227]\n" \
"v_mfma_f32_16x16x32_bf16 a[196:199], v[152:155], v[140:143], a[196:199]\n" \
"v_mfma_f32_16x16x32_bf16 a[228:231], v[152:155], v[144:147], a[228:231]\n" \
"v_mfma_f32_16x16x32_bf16 a[200:203], v[156:159], v[140:143], a[200:203]\n" \
"v_mfma_f32_16x16x32_bf16 a[232:235], v[156:159], v[144:147], a[232:235]\n" \
"v_mfma_f32_16x16x32_bf16 a[204:207], v[160:163], v[140:143], a[204:207]\n" \
"v_mfma_f32_16x16x32_bf16 a[236:239], v[160:163], v[144:147], a[236:239]\n" \
"v_mfma_f32_16x16x32_bf16 a[208:211], v[164:167], v[144:147], a[208:211]\n" \
"v_mfma_f32_16x16x32_bf16 a[212:215], v[168:171], v[144:147], a[212:215]\n" \
"v_mfma_f32_16x16x32_bf16 a[244:247], v[176:179], v[144:147], a[244:247]\n" \
"v_mfma_f32_16x16x32_bf16 a[240:243], v[144:147], v[172:175], a[240:243]\n" \
"s_waitcnt lgkmcnt(4)\n" \
"v_mfma_f32_16x16x32_bf16 a[24:27], v[104:107], v[64:67], a[24:27]\n" \
"v_bfe_u32 v44, v20, 2, 5\n" \
"ds_read_b64_tr_b16 v[168:169], v37 offset:4096\n" \
"v_lshrrev_b32_e32 v24, 9, v20\n" \
"v_mfma_f32_16x16x32_bf16 a[28:31], v[64:67], v[108:111], a[28:31]\n" \
"buffer_load_dword v44, v44, s[80:83], s38 offen offset:0 sc1\n" \
"v_and_b32_e32 v26, 0x1ff, v20\n" \
"v_mfma_f32_16x16x32_bf16 a[56:59], v[112:115], v[64:67], a[56:59]\n" \
"ds_read_b64_tr_b16 v[170:171], v37 offset:4608\n" \
"v_lshl_add_u32 v24, v24, 3, v26\n" \
"v_lshrrev_b32_e64 v25, 8, v20\n" \
"v_mfma_f32_16x16x32_bf16 a[0:3], v[84:87], v[52:55], a[0:3]\n" \
"ds_read_b64_tr_b16 v[172:173], v38 offset:4096\n" \
"v_mul_u32_u24_e32 v25, 0x24700, v25\n" \
"v_add_u32_e64 v25, v25, v20\n" \
"v_mfma_f32_16x16x32_bf16 a[32:35], v[84:87], v[56:59], a[32:35]\n" \
"ds_read_b64_tr_b16 v[174:175], v38 offset:4608\n" \
"v_lshrrev_b32_e32 v26, 2, v20\n" \
"v_add_u32_e32 v26, s45, v26\n" \
"v_mfma_f32_16x16x32_bf16 a[4:7], v[88:91], v[52:55], a[4:7]\n" \
"v_add_u32_e32 v27, s46, v20\n" \
"v_add_u32_e32 v21, s43, v27\n" \
"v_accvgpr_read_b32 v180, a24\n" \
"v_mfma_f32_16x16x32_bf16 a[36:39], v[88:91], v[56:59], a[36:39]\n" \
"ds_read_b64_tr_b16 v[176:177], v39 offset:4096\n" \
"v_accvgpr_read_b32 v181, a25\n" \
"v_accvgpr_read_b32 v182, a26\n" \
"v_mfma_f32_16x16x32_bf16 a[8:11], v[92:95], v[52:55], a[8:11]\n" \
"v_accvgpr_read_b32 v183, a27\n" \
"v_accvgpr_read_b32 v252, a28\n" \
"v_accvgpr_read_b32 v253, a29\n" \
"v_mfma_f32_16x16x32_bf16 a[40:43], v[92:95], v[56:59], a[40:43]\n" \
"ds_read_b64_tr_b16 v[178:179], v39 offset:4608\n" \
"v_accvgpr_read_b32 v254, a30\n" \
"v_accvgpr_read_b32 v255, a31\n" \
"v_mfma_f32_16x16x32_bf16 a[12:15], v[96:99], v[52:55], a[12:15]\n" \
"ds_read_b64_tr_b16 v[148:149], v32 offset:8192\n" \
"v_accvgpr_read_b32 v48, a56\n" \
"v_accvgpr_read_b32 v49, a57\n" \
"v_mfma_f32_16x16x32_bf16 a[44:47], v[96:99], v[56:59], a[44:47]\n" \
"v_accvgpr_read_b32 v50, a58\n" \
"v_accvgpr_read_b32 v51, a59\n" \
"s_waitcnt lgkmcnt(10)\n" \
"v_mfma_f32_16x16x32_bf16 a[16:19], v[100:103], v[68:71], a[16:19]\n" \
"ds_read_b64_tr_b16 v[150:151], v32 offset:9216\n" \
"v_mfma_f32_16x16x32_bf16 a[20:23], v[104:107], v[68:71], a[20:23]\n" \
"v_mfma_f32_16x16x32_bf16 a[52:55], v[112:115], v[68:71], a[52:55]\n" \
"ds_read_b64_tr_b16 v[152:153], v33 offset:8192\n" \
"v_mfma_f32_16x16x32_bf16 a[48:51], v[68:71], v[108:111], a[48:51]\n" \
"ds_read_b64_tr_b16 v[154:155], v33 offset:9216\n" \
"s_nop 1\n" \
"v_accvgpr_read_b32 v184, a16\n" \
"v_accvgpr_read_b32 v185, a17\n" \
"v_mfma_f32_16x16x32_bf16 a[64:67], v[84:87], v[60:63], a[64:67]\n" \
"v_accvgpr_read_b32 v186, a18\n" \
"v_accvgpr_read_b32 v187, a19\n" \
"v_accvgpr_read_b32 v188, a20\n" \
"v_mfma_f32_16x16x32_bf16 a[96:99], v[84:87], v[64:67], a[96:99]\n" \
"ds_read_b64_tr_b16 v[156:157], v34 offset:8192\n" \
"v_accvgpr_read_b32 v189, a21\n" \
"v_accvgpr_read_b32 v190, a22\n" \
"v_mfma_f32_16x16x32_bf16 a[68:71], v[88:91], v[60:63], a[68:71]\n" \
"ds_read_b64_tr_b16 v[158:159], v34 offset:9216\n" \
"v_accvgpr_read_b32 v191, a23\n" \
"v_accvgpr_read_b32 v196, a52\n" \
"v_mfma_f32_16x16x32_bf16 a[100:103], v[88:91], v[64:67], a[100:103]\n" \
"v_lshlrev_b32_e32 v45, 3, v20\n" \
"v_add_u32_e32 v45, s30, v45\n" \
"v_accvgpr_read_b32 v197, a53\n" \
"v_mfma_f32_16x16x32_bf16 a[72:75], v[92:95], v[60:63], a[72:75]\n" \
"s_getpc_b64 s[86:87]\n" \
"L_ct1_%=:\n" \
"s_add_u32 s86, s86, L_end_%=-L_ct1_%=\n" \
"s_addc_u32 s87, s87, 0\n" \
"s_mov_b32 m0, s32\n" \
"s_nop 0\n" \
"v_mfma_f32_16x16x32_bf16 a[104:107], v[92:95], v[64:67], a[104:107]\n" \
"global_load_lds_dword v45, s[86:87]\n" \
"v_accvgpr_read_b32 v198, a54\n" \
"v_mfma_f32_16x16x32_bf16 a[76:79], v[96:99], v[60:63], a[76:79]\n" \
"ds_read_b64_tr_b16 v[160:161], v35 offset:8192\n" \
"v_accvgpr_read_b32 v199, a55\n" \
"v_accvgpr_read_b32 v192, a48\n" \
"v_mfma_f32_16x16x32_bf16 a[108:111], v[96:99], v[64:67], a[108:111]\n" \
"ds_read_b64_tr_b16 v[162:163], v35 offset:9216\n" \
"v_accvgpr_read_b32 v193, a49\n" \
"v_accvgpr_read_b32 v194, a50\n" \
"s_waitcnt lgkmcnt(15)\n" \
"v_mfma_f32_16x16x32_bf16 a[80:83], v[100:103], v[72:75], a[80:83]\n" \
"ds_read_b64_tr_b16 v[164:165], v36 offset:4096\n" \
"v_accvgpr_read_b32 v195, a51\n" \
"v_mfma_f32_16x16x32_bf16 a[84:87], v[104:107], v[72:75], a[84:87]\n" \
"ds_read_b64_tr_b16 v[166:167], v36 offset:4608\n" \
"v_mfma_f32_16x16x32_bf16 a[116:119], v[112:115], v[72:75], a[116:119]\n" \
"v_mfma_f32_16x16x32_bf16 a[112:115], v[72:75], v[108:111], a[112:115]\n" \
"ds_read_b128 v[116:119], v29 offset:0\n" \
"s_nop 0\n" \
"v_accvgpr_read_b32 v200, a80\n" \
"v_accvgpr_read_b32 v201, a81\n" \
"v_mfma_f32_16x16x32_bf16 a[128:131], v[84:87], v[68:71], a[128:131]\n" \
"ds_read_b128 v[120:123], v29 offset:2048\n" \
"v_accvgpr_read_b32 v202, a82\n" \
"v_accvgpr_read_b32 v203, a83\n" \
"v_mfma_f32_16x16x32_bf16 a[160:163], v[84:87], v[72:75], a[160:163]\n" \
"v_accvgpr_read_b32 v204, a84\n" \
"v_accvgpr_read_b32 v205, a85\n" \
"v_accvgpr_read_b32 v206, a86\n" \
"v_mfma_f32_16x16x32_bf16 a[132:135], v[88:91], v[68:71], a[132:135]\n" \
"ds_read_b128 v[124:127], v29 offset:4096\n" \
"v_accvgpr_read_b32 v207, a87\n" \
"v_accvgpr_read_b32 v212, a116\n" \
"v_mfma_f32_16x16x32_bf16 a[164:167], v[88:91], v[72:75], a[164:167]\n" \
"ds_read_b128 v[128:131], v29 offset:6144\n" \
"v_accvgpr_read_b32 v213, a117\n" \
"v_accvgpr_read_b32 v214, a118\n" \
"v_mfma_f32_16x16x32_bf16 a[136:139], v[92:95], v[68:71], a[136:139]\n" \
"v_accvgpr_read_b32 v215, a119\n" \
"v_accvgpr_read_b32 v208, a112\n" \
"v_accvgpr_read_b32 v209, a113\n" \
"v_mfma_f32_16x16x32_bf16 a[168:171], v[92:95], v[72:75], a[168:171]\n" \
"ds_read_b128 v[132:135], v29 offset:8192\n" \
"v_accvgpr_read_b32 v210, a114\n" \
"v_accvgpr_read_b32 v211, a115\n" \
"v_mfma_f32_16x16x32_bf16 a[140:143], v[96:99], v[68:71], a[140:143]\n" \
"v_mfma_f32_16x16x32_bf16 a[172:175], v[96:99], v[72:75], a[172:175]\n" \
"ds_read_b128 v[136:139], v29 offset:10240\n" \
"v_mfma_f32_16x16x32_bf16 a[144:147], v[100:103], v[76:79], a[144:147]\n" \
"ds_read_b128 v[140:143], v29 offset:12288\n" \
"v_mfma_f32_16x16x32_bf16 a[148:151], v[104:107], v[76:79], a[148:151]\n" \
"v_mfma_f32_16x16x32_bf16 a[180:183], v[112:115], v[76:79], a[180:183]\n" \
"ds_read_b128 v[144:147], v29 offset:14336\n" \
"v_mfma_f32_16x16x32_bf16 a[176:179], v[76:79], v[108:111], a[176:179]\n" \
"s_nop 2\n" \
"v_accvgpr_read_b32 v216, a144\n" \
"v_accvgpr_read_b32 v217, a145\n" \
"v_accvgpr_read_b32 v218, a146\n" \
"v_mfma_f32_16x16x32_bf16 a[192:195], v[84:87], v[76:79], a[192:195]\n" \
"v_accvgpr_read_b32 v219, a147\n" \
"v_accvgpr_read_b32 v220, a148\n" \
"v_accvgpr_read_b32 v221, a149\n" \
"s_waitcnt lgkmcnt(15)\n" \
"v_mfma_f32_16x16x32_bf16 a[224:227], v[84:87], v[80:83], a[224:227]\n" \
"v_accvgpr_read_b32 v222, a150\n" \
"v_accvgpr_read_b32 v223, a151\n" \
"v_accvgpr_read_b32 v228, a180\n" \
"v_mfma_f32_16x16x32_bf16 a[196:199], v[88:91], v[76:79], a[196:199]\n" \
"v_accvgpr_read_b32 v229, a181\n" \
"v_accvgpr_read_b32 v230, a182\n" \
"v_accvgpr_read_b32 v231, a183\n" \
"v_mfma_f32_16x16x32_bf16 a[228:231], v[88:91], v[80:83], a[228:231]\n" \
"v_accvgpr_read_b32 v224, a176\n" \
"v_accvgpr_read_b32 v225, a177\n" \
"v_accvgpr_read_b32 v226, a178\n" \
"v_mfma_f32_16x16x32_bf16 a[200:203], v[92:95], v[76:79], a[200:203]\n" \
"v_accvgpr_read_b32 v227, a179\n" \
"v_mfma_f32_16x16x32_bf16 a[232:235], v[92:95], v[80:83], a[232:235]\n" \
"v_mfma_f32_16x16x32_bf16 a[204:207], v[96:99], v[76:79], a[204:207]\n" \
"v_mfma_f32_16x16x32_bf16 a[236:239], v[96:99], v[80:83], a[236:239]\n" \
"v_mfma_f32_16x16x32_bf16 a[208:211], v[100:103], v[80:83], a[208:211]\n" \
"v_mfma_f32_16x16x32_bf16 a[212:215], v[104:107], v[80:83], a[212:215]\n" \
"v_mfma_f32_16x16x32_bf16 a[244:247], v[112:115], v[80:83], a[244:247]\n" \
"v_mfma_f32_16x16x32_bf16 a[240:243], v[80:83], v[108:111], a[240:243]\n" \
"s_nop 4\n" \
"s_nop 0\n" \
"v_accvgpr_read_b32 v232, a208\n" \
"v_accvgpr_read_b32 v233, a209\n" \
"v_accvgpr_read_b32 v234, a210\n" \
"v_accvgpr_read_b32 v235, a211\n" \
"v_accvgpr_read_b32 v236, a212\n" \
"v_accvgpr_read_b32 v237, a213\n" \
"v_accvgpr_read_b32 v238, a214\n" \
"v_accvgpr_read_b32 v239, a215\n" \
"v_accvgpr_read_b32 v244, a244\n" \
"v_accvgpr_read_b32 v245, a245\n" \
"v_accvgpr_read_b32 v246, a246\n" \
"v_accvgpr_read_b32 v247, a247\n" \
"v_accvgpr_read_b32 v240, a240\n" \
"v_accvgpr_read_b32 v241, a241\n" \
"v_accvgpr_read_b32 v242, a242\n" \
"v_accvgpr_read_b32 v243, a243\n" \
"s_waitcnt vmcnt(1) lgkmcnt(0)\n" \
"s_barrier\n" \
"v_mov_b32_e32 v45, s62\n" \
"s_mov_b64 exec, 1\n" \
"buffer_store_dword v45, off, s[80:83], s37 offset:32 sc1\n" \
"s_mov_b64 exec, -1\n" \
"v_cmp_ne_u32_e32 vcc, s62, v44\n" \
"s_nop 4\n" \
"s_cbranch_vccnz L_poll161_%=\n" \
"L_pollr161_%=:\n" \
"v_mfma_f32_16x16x32_bf16 v[48:51], v[176:179], v[128:131], v[48:51]\n" \
"s_mov_b32 s73, s50\n" \
"s_nop 0\n" \
"s_sub_u32 m0, s31, 0x10000\n" \
"v_mfma_f32_16x16x32_bf16 v[180:183], v[168:171], v[128:131], v[180:183]\n" \
"buffer_load_dwordx4 v20, s[80:83], s73 offen offset:0 sc1 lds\n" \
"v_mfma_f32_16x16x32_bf16 v[252:255], v[128:131], v[172:175], v[252:255]\n" \
"s_add_u32 s74, s50, 0x4000\n" \
"s_mov_b32 s70, 0x3e0293ee\n" \
"v_mfma_f32_16x16x32_bf16 v[196:199], v[176:179], v[132:135], v[196:199]\n" \
"s_add_u32 s75, s50, 0x8000\n" \
"s_mov_b32 s64, 0xaaaaaaaa\n" \
"v_mfma_f32_16x16x32_bf16 v[188:191], v[168:171], v[132:135], v[188:191]\n" \
"v_cvt_pk_bf16_f32 v180, v180, v181\n" \
"v_cvt_pk_bf16_f32 v181, v182, v183\n" \
"v_mfma_f32_16x16x32_bf16 v[184:187], v[164:167], v[132:135], v[184:187]\n" \
"buffer_load_dwordx4 v20, s[80:83], s73 offen offset:1024 sc1 lds\n" \
"v_mfma_f32_16x16x32_bf16 v[192:195], v[132:135], v[172:175], v[192:195]\n" \
"v_cvt_pk_bf16_f32 v182, v50, v51\n" \
"v_mov_b32_e32 v183, 0x3f80\n" \
"v_mfma_f32_16x16x32_bf16 v[212:215], v[176:179], v[136:139], v[212:215]\n" \
"v_cvt_pk_bf16_f32 v188, v188, v189\n" \
"v_cvt_pk_bf16_f32 v189, v190, v191\n" \
"v_mfma_f32_16x16x32_bf16 v[204:207], v[168:171], v[136:139], v[204:207]\n" \
"v_cvt_pk_bf16_f32 v184, v184, v185\n" \
"v_cvt_pk_bf16_f32 v185, v186, v187\n" \
"v_mfma_f32_16x16x32_bf16 v[200:203], v[164:167], v[136:139], v[200:203]\n" \
"buffer_load_dwordx4 v20, s[80:83], s73 offen offset:2048 sc1 lds\n" \
"v_mfma_f32_16x16x32_bf16 v[208:211], v[136:139], v[172:175], v[208:211]\n" \
"v_cvt_pk_bf16_f32 v190, v198, v199\n" \
"v_mov_b32_e32 v191, 0x3f80\n" \
"v_mfma_f32_16x16x32_bf16 v[228:231], v[176:179], v[140:143], v[228:231]\n" \
"v_cvt_pk_bf16_f32 v204, v204, v205\n" \
"v_cvt_pk_bf16_f32 v205, v206, v207\n" \
"v_mfma_f32_16x16x32_bf16 v[220:223], v[168:171], v[140:143], v[220:223]\n" \
"v_cvt_pk_bf16_f32 v200, v200, v201\n" \
"v_cvt_pk_bf16_f32 v201, v202, v203\n" \
"v_mfma_f32_16x16x32_bf16 v[216:219], v[164:167], v[140:143], v[216:219]\n" \
"buffer_load_dwordx4 v20, s[80:83], s73 offen offset:3072 sc1 lds\n" \
"v_mfma_f32_16x16x32_bf16 v[224:227], v[140:143], v[172:175], v[224:227]\n" \
"s_sub_u32 m0, s31, s51\n" \
"v_cvt_pk_bf16_f32 v186, v196, v197\n" \
"v_mfma_f32_16x16x32_bf16 v[244:247], v[176:179], v[144:147], v[244:247]\n" \
"v_cvt_pk_bf16_f32 v220, v220, v221\n" \
"v_cvt_pk_bf16_f32 v221, v222, v223\n" \
"v_mfma_f32_16x16x32_bf16 v[236:239], v[168:171], v[144:147], v[236:239]\n" \
"v_cvt_pk_bf16_f32 v216, v216, v217\n" \
"v_cvt_pk_bf16_f32 v217, v218, v219\n" \
"v_mfma_f32_16x16x32_bf16 v[232:235], v[164:167], v[144:147], v[232:235]\n" \
"buffer_load_dwordx4 v20, s[80:83], s74 offen offset:0 sc1 lds\n" \
"v_mfma_f32_16x16x32_bf16 v[240:243], v[144:147], v[172:175], v[240:243]\n" \
"v_mov_b32_e32 v187, 0\n" \
"v_cvt_pk_bf16_f32 v54, v192, v193\n" \
"v_mfma_f32_16x16x32_bf16 a[0:3], v[148:151], v[116:119], a[0:3]\n" \
"v_cvt_pk_bf16_f32 v236, v236, v237\n" \
"v_cvt_pk_bf16_f32 v237, v238, v239\n" \
"v_mfma_f32_16x16x32_bf16 a[32:35], v[148:151], v[120:123], a[32:35]\n" \
"v_cvt_pk_bf16_f32 v232, v232, v233\n" \
"v_cvt_pk_bf16_f32 v233, v234, v235\n" \
"v_mfma_f32_16x16x32_bf16 a[4:7], v[152:155], v[116:119], a[4:7]\n" \
"buffer_load_dwordx4 v20, s[80:83], s74 offen offset:1024 sc1 lds\n" \
"v_mfma_f32_16x16x32_bf16 a[36:39], v[152:155], v[120:123], a[36:39]\n" \
"v_accvgpr_read_b32 v72, a0\n" \
"v_accvgpr_read_b32 v73, a1\n" \
"v_mfma_f32_16x16x32_bf16 a[8:11], v[156:159], v[116:119], a[8:11]\n" \
"v_accvgpr_read_b32 v74, a2\n" \
"v_accvgpr_read_b32 v75, a3\n" \
"v_mfma_f32_16x16x32_bf16 a[40:43], v[156:159], v[120:123], a[40:43]\n" \
"v_mul_f32_e32 v72, s70, v72\n" \
"v_mul_f32_e32 v73, s70, v73\n" \
"v_mfma_f32_16x16x32_bf16 a[12:15], v[160:163], v[116:119], a[12:15]\n" \
"buffer_load_dwordx4 v20, s[80:83], s74 offen offset:2048 sc1 lds\n" \
"v_mfma_f32_16x16x32_bf16 a[44:47], v[160:163], v[120:123], a[44:47]\n" \
"v_accvgpr_read_b32 v76, a4\n" \
"v_accvgpr_read_b32 v77, a5\n" \
"v_mfma_f32_16x16x32_bf16 a[64:67], v[148:151], v[124:127], a[64:67]\n" \
"v_accvgpr_read_b32 v78, a6\n" \
"v_accvgpr_read_b32 v79, a7\n" \
"v_mfma_f32_16x16x32_bf16 a[96:99], v[148:151], v[128:131], a[96:99]\n" \
"v_mul_f32_e32 v74, s70, v74\n" \
"v_mul_f32_e32 v75, s70, v75\n" \
"v_mfma_f32_16x16x32_bf16 a[68:71], v[152:155], v[124:127], a[68:71]\n" \
"buffer_load_dwordx4 v20, s[80:83], s74 offen offset:3072 sc1 lds\n" \
"v_mfma_f32_16x16x32_bf16 a[100:103], v[152:155], v[128:131], a[100:103]\n" \
"v_cvt_pk_bf16_f32 v72, v72, v73\n" \
"s_sub_u32 m0, s31, 0x4000\n" \
"v_mfma_f32_16x16x32_bf16 a[72:75], v[156:159], v[124:127], a[72:75]\n" \
"v_mul_f32_e32 v76, s70, v76\n" \
"v_mul_f32_e32 v77, s70, v77\n" \
"v_mfma_f32_16x16x32_bf16 a[104:107], v[156:159], v[128:131], a[104:107]\n" \
"v_mul_f32_e32 v78, s70, v78\n" \
"v_mul_f32_e32 v79, s70, v79\n" \
"v_mfma_f32_16x16x32_bf16 a[76:79], v[160:163], v[124:127], a[76:79]\n" \
"v_cvt_pk_bf16_f32 v73, v74, v75\n" \
"v_cvt_pk_bf16_f32 v74, v76, v77\n" \
"v_mfma_f32_16x16x32_bf16 a[108:111], v[160:163], v[128:131], a[108:111]\n" \
"buffer_load_dwordx4 v20, s[80:83], s75 offen offset:0 sc1 lds\n" \
"v_cvt_pk_bf16_f32 v75, v78, v79\n" \
"ds_write_b128 v27, v[72:75] offset:0\n" \
"v_mfma_f32_16x16x32_bf16 a[128:131], v[148:151], v[132:135], a[128:131]\n" \
"v_accvgpr_read_b32 v72, a8\n" \
"v_accvgpr_read_b32 v73, a9\n" \
"v_accvgpr_read_b32 v74, a10\n" \
"v_accvgpr_read_b32 v75, a11\n" \
"v_mul_f32_e32 v72, s70, v72\n" \
"v_mul_f32_e32 v73, s70, v73\n" \
"v_accvgpr_read_b32 v76, a12\n" \
"v_accvgpr_read_b32 v77, a13\n" \
"v_accvgpr_read_b32 v78, a14\n" \
"v_accvgpr_read_b32 v79, a15\n" \
"v_mul_f32_e32 v74, s70, v74\n" \
"v_mul_f32_e32 v75, s70, v75\n" \
"v_cvt_pk_bf16_f32 v72, v72, v73\n" \
"v_mfma_f32_16x16x32_bf16 a[160:163], v[148:151], v[136:139], a[160:163]\n" \
"v_mul_f32_e32 v76, s70, v76\n" \
"v_mul_f32_e32 v77, s70, v77\n" \
"v_mul_f32_e32 v78, s70, v78\n" \
"v_mul_f32_e32 v79, s70, v79\n" \
"v_cvt_pk_bf16_f32 v73, v74, v75\n" \
"v_cvt_pk_bf16_f32 v74, v76, v77\n" \
"v_cvt_pk_bf16_f32 v75, v78, v79\n" \
"ds_write_b128 v27, v[72:75] offset:1024\n" \
"v_mfma_f32_16x16x32_bf16 a[132:135], v[152:155], v[132:135], a[132:135]\n" \
"v_accvgpr_read_b32 v72, a32\n" \
"v_accvgpr_read_b32 v73, a33\n" \
"v_accvgpr_read_b32 v74, a34\n" \
"v_accvgpr_read_b32 v75, a35\n" \
"v_mul_f32_e32 v72, s70, v72\n" \
"v_mul_f32_e32 v73, s70, v73\n" \
"v_accvgpr_read_b32 v76, a36\n" \
"v_accvgpr_read_b32 v77, a37\n" \
"v_accvgpr_read_b32 v78, a38\n" \
"v_accvgpr_read_b32 v79, a39\n" \
"v_mul_f32_e32 v74, s70, v74\n" \
"v_mul_f32_e32 v75, s70, v75\n" \
"v_cvt_pk_bf16_f32 v72, v72, v73\n" \
"v_mul_f32_e32 v76, s70, v76\n" \
"v_mul_f32_e32 v77, s70, v77\n" \
"v_mul_f32_e32 v78, s70, v78\n" \
"v_mul_f32_e32 v79, s70, v79\n" \
"v_cvt_pk_bf16_f32 v73, v74, v75\n" \
"v_mfma_f32_16x16x32_bf16 a[164:167], v[152:155], v[136:139], a[164:167]\n" \
"v_cvt_pk_bf16_f32 v74, v76, v77\n" \
"v_cvt_pk_bf16_f32 v75, v78, v79\n" \
"ds_write_b128 v27, v[72:75] offset:4096\n" \
"buffer_load_dwordx4 v20, s[80:83], s75 offen offset:1024 sc1 lds\n" \
"v_cvt_pk_bf16_f32 v55, v194, v195\n" \
"v_cvt_pk_bf16_f32 v206, v214, v215\n" \
"v_mov_b32_e32 v207, 0x3f80\n" \
"v_cvt_pk_bf16_f32 v202, v212, v213\n" \
"v_mov_b32_e64 v203, 0\n" \
"v_cvt_pk_bf16_f32 v56, v208, v209\n" \
"v_cvt_pk_bf16_f32 v57, v210, v211\n" \
"v_cvt_pk_bf16_f32 v222, v230, v231\n" \
"v_mov_b32_e32 v223, 0x3f80\n" \
"v_cvt_pk_bf16_f32 v218, v228, v229\n" \
"v_mov_b32_e64 v219, 0\n" \
"v_cvt_pk_bf16_f32 v58, v224, v225\n" \
"v_cvt_pk_bf16_f32 v59, v226, v227\n" \
"v_cvt_pk_bf16_f32 v238, v246, v247\n" \
"v_mov_b32_e32 v239, 0x3f80\n" \
"v_cvt_pk_bf16_f32 v234, v244, v245\n" \
"v_mov_b32_e64 v235, 0\n" \
"v_cvt_pk_bf16_f32 v60, v240, v241\n" \
"v_cvt_pk_bf16_f32 v61, v242, v243\n" \
"v_accvgpr_read_b32 v72, a40\n" \
"v_accvgpr_read_b32 v73, a41\n" \
"v_mfma_f32_16x16x32_bf16 a[136:139], v[156:159], v[132:135], a[136:139]\n" \
"v_mfma_f32_16x16x32_bf16 v[192:195], v[188:191], v[184:187], 0\n" \
"v_mfma_f32_16x16x32_bf16 v[164:167], v[180:183], v[184:187], 0\n" \
"v_mfma_f32_16x16x32_bf16 v[208:211], v[204:207], v[200:203], 0\n" \
"v_mfma_f32_16x16x32_bf16 v[168:171], v[188:191], v[200:203], 0\n" \
"v_mfma_f32_16x16x32_bf16 v[224:227], v[220:223], v[216:219], 0\n" \
"v_mfma_f32_16x16x32_bf16 v[172:175], v[204:207], v[216:219], 0\n" \
"v_mfma_f32_16x16x32_bf16 v[240:243], v[236:239], v[232:235], 0\n" \
"v_mfma_f32_16x16x32_bf16 v[176:179], v[220:223], v[232:235], 0\n" \
"v_accvgpr_read_b32 v74, a42\n" \
"v_accvgpr_read_b32 v75, a43\n" \
"v_mul_f32_e32 v72, s70, v72\n" \
"v_mul_f32_e32 v73, s70, v73\n" \
"v_accvgpr_read_b32 v76, a44\n" \
"v_accvgpr_read_b32 v77, a45\n" \
"v_accvgpr_read_b32 v78, a46\n" \
"v_accvgpr_read_b32 v79, a47\n" \
"v_mul_f32_e32 v74, s70, v74\n" \
"v_mul_f32_e32 v75, s70, v75\n" \
"v_cvt_pk_bf16_f32 v72, v72, v73\n" \
"v_mul_f32_e32 v76, s70, v76\n" \
"v_mul_f32_e32 v77, s70, v77\n" \
"v_mul_f32_e32 v78, s70, v78\n" \
"v_mul_f32_e32 v79, s70, v79\n" \
"v_cvt_pk_bf16_f32 v73, v74, v75\n" \
"v_cvt_pk_bf16_f32 v74, v76, v77\n" \
"v_cvt_pk_bf16_f32 v75, v78, v79\n" \
"v_mfma_f32_16x16x32_bf16 a[168:171], v[156:159], v[136:139], a[168:171]\n" \
"ds_write_b128 v27, v[72:75] offset:5120\n" \
"v_accvgpr_read_b32 v116, a64\n" \
"v_accvgpr_read_b32 v117, a65\n" \
"v_accvgpr_read_b32 v72, a72\n" \
"v_accvgpr_read_b32 v73, a73\n" \
"v_accvgpr_read_b32 v118, a66\n" \
"v_accvgpr_read_b32 v119, a67\n" \
"v_mul_f32_e32 v116, s70, v116\n" \
"v_mul_f32_e32 v117, s70, v117\n" \
"v_accvgpr_read_b32 v74, a74\n" \
"v_accvgpr_read_b32 v75, a75\n" \
"v_mul_f32_e32 v72, s70, v72\n" \
"v_mul_f32_e32 v73, s70, v73\n" \
"v_mfma_f32_16x16x32_bf16 a[140:143], v[160:163], v[132:135], a[140:143]\n" \
"v_accvgpr_read_b32 v120, a68\n" \
"v_accvgpr_read_b32 v121, a69\n" \
"v_accvgpr_read_b32 v122, a70\n" \
"v_accvgpr_read_b32 v123, a71\n" \
"v_mul_f32_e32 v118, s70, v118\n" \
"v_mul_f32_e32 v119, s70, v119\n" \
"v_cvt_pk_bf16_f32 v116, v116, v117\n" \
"v_accvgpr_read_b32 v76, a76\n" \
"v_accvgpr_read_b32 v77, a77\n" \
"v_accvgpr_read_b32 v78, a78\n" \
"v_accvgpr_read_b32 v79, a79\n" \
"v_mul_f32_e32 v74, s70, v74\n" \
"v_mul_f32_e32 v75, s70, v75\n" \
"v_cvt_pk_bf16_f32 v72, v72, v73\n" \
"s_mov_b32 s65, 0xaaaaaaaa\n" \
"v_mul_f32_e32 v120, s70, v120\n" \
"v_mul_f32_e32 v121, s70, v121\n" \
"v_mul_f32_e32 v122, s70, v122\n" \
"v_mul_f32_e32 v123, s70, v123\n" \
"v_cvt_pk_bf16_f32 v117, v118, v119\n" \
"v_mul_f32_e32 v76, s70, v76\n" \
"v_mul_f32_e32 v77, s70, v77\n" \
"v_mul_f32_e32 v78, s70, v78\n" \
"v_mul_f32_e32 v79, s70, v79\n" \
"v_cvt_pk_bf16_f32 v73, v74, v75\n" \
"s_mov_b32 s66, 0xfff0ffff\n" \
"s_mov_b32 s67, 0xf000ff00\n" \
"s_mov_b32 s68, 0xffc0fffc\n" \
"s_mov_b32 s69, 0xc000fc00\n" \
"v_cndmask_b32_e64 v192, v192, v193, s[64:65]\n" \
"v_cndmask_b32_e64 v164, v164, v165, s[64:65]\n" \
"v_cndmask_b32_e64 v194, v194, v195, s[64:65]\n" \
"v_cndmask_b32_e64 v166, v166, v167, s[64:65]\n" \
"v_cndmask_b32_e64 v208, v208, v209, s[64:65]\n" \
"v_cndmask_b32_e64 v168, v168, v169, s[64:65]\n" \
"v_cndmask_b32_e64 v210, v210, v211, s[64:65]\n" \
"v_cndmask_b32_e64 v170, v170, v171, s[64:65]\n" \
"v_cndmask_b32_e64 v224, v224, v225, s[64:65]\n" \
"v_cndmask_b32_e64 v172, v172, v173, s[64:65]\n" \
"v_cndmask_b32_e64 v226, v226, v227, s[64:65]\n" \
"v_cndmask_b32_e64 v174, v174, v175, s[64:65]\n" \
"v_cndmask_b32_e64 v240, v240, v241, s[64:65]\n" \
"v_cndmask_b32_e64 v176, v176, v177, s[64:65]\n" \
"v_cndmask_b32_e64 v242, v242, v243, s[64:65]\n" \
"v_cndmask_b32_e64 v178, v178, v179, s[64:65]\n" \
"v_cvt_pk_bf16_f32 v118, v120, v121\n" \
"v_cvt_pk_bf16_f32 v119, v122, v123\n" \
"v_cvt_pk_bf16_f32 v74, v76, v77\n" \
"v_cvt_pk_bf16_f32 v75, v78, v79\n" \
"v_cndmask_b32_e64 v192, v164, v192, s[66:67]\n" \
"v_cndmask_b32_e64 v194, v166, v194, s[68:69]\n" \
"v_cndmask_b32_e64 v208, v168, v208, s[66:67]\n" \
"v_cndmask_b32_e64 v210, v170, v210, s[68:69]\n" \
"v_cndmask_b32_e64 v224, v172, v224, s[66:67]\n" \
"v_cndmask_b32_e64 v226, v174, v226, s[68:69]\n" \
"v_cndmask_b32_e64 v240, v176, v240, s[66:67]\n" \
"v_cndmask_b32_e64 v242, v178, v242, s[68:69]\n" \
"ds_write_b128 v27, v[116:119] offset:8192\n" \
"ds_write_b128 v27, v[72:75] offset:9216\n" \
"v_mfma_f32_16x16x32_bf16 a[172:175], v[160:163], v[136:139], a[172:175]\n" \
"v_max_f32_e32 v193, v192, v194\n" \
"v_max_f32_e32 v209, v208, v210\n" \
"v_max_f32_e32 v225, v224, v226\n" \
"v_max_f32_e32 v241, v240, v242\n" \
"s_mov_b32 s71, 0x3e0293ee\n" \
"v_permlane32_swap_b32_e32 v193, v225\n" \
"v_permlane32_swap_b32_e32 v209, v241\n" \
"v_accvgpr_read_b32 v116, a96\n" \
"v_accvgpr_read_b32 v117, a97\n" \
"v_accvgpr_read_b32 v72, a104\n" \
"v_accvgpr_read_b32 v73, a105\n" \
"v_accvgpr_read_b32 v124, a128\n" \
"v_accvgpr_read_b32 v125, a129\n" \
"buffer_load_dwordx4 v20, s[80:83], s75 offen offset:2048 sc1 lds\n" \
"v_permlane32_swap_b32_e32 v196, v228\n" \
"v_permlane32_swap_b32_e32 v212, v244\n" \
"v_max_f32_e32 v193, v193, v225\n" \
"v_max_f32_e32 v209, v209, v241\n" \
"v_accvgpr_read_b32 v118, a98\n" \
"v_accvgpr_read_b32 v119, a99\n" \
"v_mul_f32_e32 v116, s70, v116\n" \
"v_mul_f32_e32 v117, s70, v117\n" \
"v_accvgpr_read_b32 v74, a106\n" \
"v_accvgpr_read_b32 v75, a107\n" \
"v_mul_f32_e32 v72, s70, v72\n" \
"v_mul_f32_e32 v73, s70, v73\n" \
"v_accvgpr_read_b32 v126, a130\n" \
"v_accvgpr_read_b32 v127, a131\n" \
"v_pk_mul_f32 v[124:125], v[124:125], s[70:71]\n" \
"v_accvgpr_read_b32 v84, a192\n" \
"v_accvgpr_read_b32 v85, a193\n" \
"v_accvgpr_read_b32 v86, a194\n" \
"v_accvgpr_read_b32 v87, a195\n" \
"v_permlane16_swap_b32_e32 v228, v244\n" \
"v_permlane16_swap_b32_e32 v193, v209\n" \
"v_accvgpr_read_b32 v120, a100\n" \
"v_accvgpr_read_b32 v121, a101\n" \
"v_accvgpr_read_b32 v122, a102\n" \
"v_accvgpr_read_b32 v123, a103\n" \
"v_mul_f32_e32 v118, s70, v118\n" \
"v_mul_f32_e32 v119, s70, v119\n" \
"v_cvt_pk_bf16_f32 v116, v116, v117\n" \
"v_accvgpr_read_b32 v76, a108\n" \
"v_accvgpr_read_b32 v77, a109\n" \
"v_accvgpr_read_b32 v78, a110\n" \
"v_accvgpr_read_b32 v79, a111\n" \
"v_mul_f32_e32 v74, s70, v74\n" \
"v_mul_f32_e32 v75, s70, v75\n" \
"v_cvt_pk_bf16_f32 v72, v72, v73\n" \
"v_accvgpr_read_b32 v128, a132\n" \
"v_accvgpr_read_b32 v129, a133\n" \
"v_accvgpr_read_b32 v130, a134\n" \
"v_accvgpr_read_b32 v131, a135\n" \
"v_pk_mul_f32 v[126:127], v[126:127], s[70:71]\n" \
"v_cvt_pk_bf16_f32 v124, v124, v125\n" \
"v_mfma_f32_16x16x32_bf16 v[84:87], v[148:151], v[140:143], v[84:87]\n" \
"s_mov_b32 s72, 0x3eae1a93\n" \
"v_mul_f32_e32 v45, 0xbfb8aa3b, v228\n" \
"v_mfma_f32_16x16x16_bf16 v[48:51], v[182:183], v[46:47], 0\n" \
"v_mfma_f32_16x16x16_bf16 v[196:199], v[190:191], v[46:47], 0\n" \
"v_mfma_f32_16x16x16_bf16 v[212:215], v[206:207], v[46:47], 0\n" \
"v_max_f32_e32 v193, v193, v209\n" \
"v_mul_f32_e32 v120, s70, v120\n" \
"v_mul_f32_e32 v121, s70, v121\n" \
"v_mul_f32_e32 v122, s70, v122\n" \
"v_mul_f32_e64 v123, s70, v123\n" \
"v_cvt_pk_bf16_f32 v117, v118, v119\n" \
"v_mul_f32_e32 v76, s70, v76\n" \
"v_mul_f32_e32 v77, s70, v77\n" \
"v_mul_f32_e32 v78, s70, v78\n" \
"v_mul_f32_e32 v79, s70, v79\n" \
"v_cvt_pk_bf16_f32 v73, v74, v75\n" \
"v_pk_mul_f32 v[128:129], v[128:129], s[70:71]\n" \
"v_pk_mul_f32 v[130:131], v[130:131], s[70:71]\n" \
"v_cvt_pk_bf16_f32 v125, v126, v127\n" \
"v_mfma_f32_16x16x16_bf16 v[228:231], v[222:223], v[46:47], 0\n" \
"v_mul_f32_e64 v193, s72, v193\n" \
"v_cvt_pk_bf16_f32 v118, v120, v121\n" \
"v_cvt_pk_bf16_f32 v119, v122, v123\n" \
"v_cvt_pk_bf16_f32 v74, v76, v77\n" \
"v_cvt_pk_bf16_f32 v75, v78, v79\n" \
"v_cvt_pk_bf16_f32 v126, v128, v129\n" \
"v_cvt_pk_bf16_f32 v127, v130, v131\n" \
"v_mfma_f32_16x16x16_bf16 v[244:247], v[238:239], v[46:47], 0\n" \
"v_mov_b32_e64 v209, v193\n" \
"ds_write_b128 v27, v[116:119] offset:12288\n" \
"ds_write_b128 v27, v[72:75] offset:13312\n" \
"ds_write_b128 v21, v[124:127] offset:16384\n" \
"v_accvgpr_read_b32 v100, a224\n" \
"v_accvgpr_read_b32 v101, a225\n" \
"v_accvgpr_read_b32 v102, a226\n" \
"v_accvgpr_read_b32 v103, a227\n" \
"v_permlane16_swap_b32_e32 v193, v209\n" \
"v_mov_b32_e32 v225, v193\n" \
"v_mfma_f32_16x16x32_bf16 v[100:103], v[148:151], v[144:147], v[100:103]\n" \
"v_mov_b32_e64 v241, v209\n" \
"v_accvgpr_read_b32 v116, a136\n" \
"v_accvgpr_read_b32 v117, a137\n" \
"v_accvgpr_read_b32 v72, a160\n" \
"v_accvgpr_read_b32 v73, a161\n" \
"v_accvgpr_read_b32 v124, a168\n" \
"v_accvgpr_read_b32 v125, a169\n" \
"v_permlane32_swap_b32_e32 v193, v225\n" \
"v_permlane32_swap_b32_e32 v209, v241\n" \
"v_accvgpr_read_b32 v118, a138\n" \
"v_accvgpr_read_b32 v119, a139\n" \
"v_pk_mul_f32 v[116:117], v[116:117], s[70:71]\n" \
"v_accvgpr_read_b32 v74, a162\n" \
"v_accvgpr_read_b32 v75, a163\n" \
"v_pk_mul_f32 v[72:73], v[72:73], s[70:71]\n" \
"v_accvgpr_read_b32 v126, a170\n" \
"v_accvgpr_read_b32 v127, a171\n" \
"v_pk_mul_f32 v[124:125], v[124:125], s[70:71]\n" \
"v_pk_mul_f32 v[84:85], v[84:85], s[70:71]\n" \
"v_pk_mul_f32 v[100:101], v[100:101], s[70:71]\n" \
"v_fma_f32 v192, v192, s72, -v193\n" \
"v_fma_f32 v194, v194, s72, -v193\n" \
"v_fma_f32 v208, v208, s72, -v209\n" \
"v_fma_f32 v210, v210, s72, -v209\n" \
"v_accvgpr_read_b32 v120, a140\n" \
"v_accvgpr_read_b32 v121, a141\n" \
"v_accvgpr_read_b32 v122, a142\n" \
"v_accvgpr_read_b32 v123, a143\n" \
"v_pk_mul_f32 v[118:119], v[118:119], s[70:71]\n" \
"v_cvt_pk_bf16_f32 v116, v116, v117\n" \
"v_accvgpr_read_b32 v76, a164\n" \
"v_accvgpr_read_b32 v77, a165\n" \
"v_accvgpr_read_b32 v78, a166\n" \
"v_accvgpr_read_b32 v79, a167\n" \
"v_pk_mul_f32 v[74:75], v[74:75], s[70:71]\n" \
"v_cvt_pk_bf16_f32 v72, v72, v73\n" \
"v_accvgpr_read_b32 v128, a172\n" \
"v_accvgpr_read_b32 v129, a173\n" \
"v_accvgpr_read_b32 v130, a174\n" \
"v_accvgpr_read_b32 v131, a175\n" \
"v_pk_mul_f32 v[126:127], v[126:127], s[70:71]\n" \
"v_cvt_pk_bf16_f32 v124, v124, v125\n" \
"v_pk_mul_f32 v[86:87], v[86:87], s[70:71]\n" \
"v_cvt_pk_bf16_f32 v84, v84, v85\n" \
"v_pk_mul_f32 v[102:103], v[102:103], s[70:71]\n" \
"v_cvt_pk_bf16_f32 v100, v100, v101\n" \
"v_accvgpr_read_b32 v88, a196\n" \
"v_accvgpr_read_b32 v89, a197\n" \
"v_accvgpr_read_b32 v90, a198\n" \
"v_accvgpr_read_b32 v91, a199\n" \
"v_exp_f32_e32 v192, v192\n" \
"v_exp_f32_e32 v194, v194\n" \
"v_exp_f32_e32 v208, v208\n" \
"v_exp_f32_e32 v210, v210\n" \
"v_fma_f32 v224, v224, s72, -v225\n" \
"v_fma_f32 v226, v226, s72, -v225\n" \
"v_pk_mul_f32 v[120:121], v[120:121], s[70:71]\n" \
"v_pk_mul_f32 v[122:123], v[122:123], s[70:71]\n" \
"v_cvt_pk_bf16_f32 v117, v118, v119\n" \
"v_pk_mul_f32 v[76:77], v[76:77], s[70:71]\n" \
"v_pk_mul_f32 v[78:79], v[78:79], s[70:71]\n" \
"v_cvt_pk_bf16_f32 v73, v74, v75\n" \
"v_pk_mul_f32 v[128:129], v[128:129], s[70:71]\n" \
"v_pk_mul_f32 v[130:131], v[130:131], s[70:71]\n" \
"v_cvt_pk_bf16_f32 v125, v126, v127\n" \
"v_cvt_pk_bf16_f32 v85, v86, v87\n" \
"v_cvt_pk_bf16_f32 v101, v102, v103\n" \
"v_mfma_f32_16x16x32_bf16 v[88:91], v[152:155], v[140:143], v[88:91]\n" \
"v_exp_f32_e32 v224, v224\n" \
"v_exp_f32_e32 v226, v226\n" \
"v_fma_f32 v240, v240, s72, -v241\n" \
"v_fma_f32 v242, v242, s72, -v241\n" \
"v_cvt_pk_bf16_f32 v118, v120, v121\n" \
"v_cvt_pk_bf16_f32 v119, v122, v123\n" \
"v_cvt_pk_bf16_f32 v74, v76, v77\n" \
"v_cvt_pk_bf16_f32 v75, v78, v79\n" \
"v_pk_mul_f32 v[88:89], v[88:89], s[70:71]\n" \
"v_pk_mul_f32 v[90:91], v[90:91], s[70:71]\n" \
"v_cvt_pk_bf16_f32 v126, v128, v129\n" \
"v_cvt_pk_bf16_f32 v127, v130, v131\n" \
"v_cvt_pk_bf16_f32 v86, v88, v89\n" \
"v_cvt_pk_bf16_f32 v87, v90, v91\n" \
"v_cvt_pk_bf16_f32 v192, v192, v194\n" \
"v_cvt_pk_bf16_f32 v208, v208, v210\n" \
"v_exp_f32_e32 v240, v240\n" \
"v_exp_f32_e32 v242, v242\n" \
"ds_write_b128 v21, v[116:119] offset:17408\n" \
"ds_write_b128 v21, v[72:75] offset:20480\n" \
"ds_write_b128 v21, v[124:127] offset:21504\n" \
"ds_write_b128 v21, v[84:87] offset:24576\n" \
"v_cvt_pk_bf16_f32 v62, v48, v49\n" \
"v_cvt_pk_bf16_f32 v63, v50, v51\n" \
"v_cvt_pk_bf16_f32 v64, v196, v197\n" \
"v_cvt_pk_bf16_f32 v65, v198, v199\n" \
"v_cvt_pk_bf16_f32 v66, v212, v213\n" \
"v_cvt_pk_bf16_f32 v67, v214, v215\n" \
"v_perm_b32 v184, v192, v192, v40\n" \
"v_perm_b32 v185, v192, v192, v41\n" \
"v_perm_b32 v186, v192, v192, v42\n" \
"v_perm_b32 v187, v192, v192, v43\n" \
"v_perm_b32 v200, v208, v208, v40\n" \
"v_perm_b32 v201, v208, v208, v41\n" \
"v_perm_b32 v202, v208, v208, v42\n" \
"v_perm_b32 v203, v208, v208, v43\n" \
"v_cvt_pk_bf16_f32 v224, v224, v226\n" \
"v_accvgpr_read_b32 v104, a228\n" \
"v_accvgpr_read_b32 v105, a229\n" \
"v_accvgpr_read_b32 v106, a230\n" \
"v_accvgpr_read_b32 v107, a231\n" \
"v_cvt_pk_bf16_f32 v68, v228, v229\n" \
"v_cvt_pk_bf16_f32 v69, v230, v231\n" \
"v_mfma_f32_16x16x32_bf16 v[164:167], v[62:65], v[184:187], 0\n" \
"v_mfma_f32_16x16x32_bf16 v[168:171], v[64:67], v[200:203], 0\n" \
"v_perm_b32 v216, v224, v224, v40\n" \
"v_perm_b32 v217, v224, v224, v41\n" \
"v_perm_b32 v218, v224, v224, v42\n" \
"v_perm_b32 v219, v224, v224, v43\n" \
"v_cvt_pk_bf16_f32 v240, v240, v242\n" \
"v_mfma_f32_16x16x32_bf16 v[104:107], v[152:155], v[144:147], v[104:107]\n" \
"v_cvt_pk_bf16_f32 v70, v244, v245\n" \
"v_cvt_pk_bf16_f32 v71, v246, v247\n" \
"v_mfma_f32_16x16x32_bf16 v[172:175], v[66:69], v[216:219], 0\n" \
"v_perm_b32 v232, v240, v240, v40\n" \
"v_perm_b32 v233, v240, v240, v41\n" \
"v_perm_b32 v234, v240, v240, v42\n" \
"v_perm_b32 v235, v240, v240, v43\n" \
"buffer_load_dwordx4 v20, s[80:83], s75 offen offset:3072 sc1 lds\n" \
"v_pk_mul_f32 v[104:105], v[104:105], s[70:71]\n" \
"v_pk_mul_f32 v[106:107], v[106:107], s[70:71]\n" \
"v_cvt_pk_bf16_f32 v102, v104, v105\n" \
"v_cvt_pk_bf16_f32 v103, v106, v107\n" \
"ds_write_b128 v21, v[100:103] offset:28672\n" \
"v_mfma_f32_16x16x32_bf16 v[176:179], v[68:71], v[232:235], 0\n" \
"v_accvgpr_read_b32 v92, a200\n" \
"v_accvgpr_read_b32 v93, a201\n" \
"v_accvgpr_read_b32 v94, a202\n" \
"v_accvgpr_read_b32 v95, a203\n" \
"v_accvgpr_read_b32 v108, a232\n" \
"v_accvgpr_read_b32 v109, a233\n" \
"v_mfma_f32_16x16x32_bf16 v[92:95], v[156:159], v[140:143], v[92:95]\n" \
"v_accvgpr_read_b32 v110, a234\n" \
"v_accvgpr_read_b32 v111, a235\n" \
"v_cvt_pk_bf16_f32 v52, v252, v253\n" \
"v_cvt_pk_bf16_f32 v53, v254, v255\n" \
"v_mfma_f32_16x16x32_bf16 v[108:111], v[156:159], v[144:147], v[108:111]\n" \
"v_exp_f32_e64 v45, v45\n" \
"v_mfma_f32_16x16x32_bf16 v[192:195], v[52:55], v[184:187], 0\n" \
"v_mfma_f32_16x16x32_bf16 v[208:211], v[54:57], v[200:203], 0\n" \
"v_pk_mul_f32 v[92:93], v[92:93], s[70:71]\n" \
"v_pk_mul_f32 v[94:95], v[94:95], s[70:71]\n" \
"v_cvt_pk_bf16_f32 v92, v92, v93\n" \
"v_cvt_pk_bf16_f32 v93, v94, v95\n" \
"v_mfma_f32_16x16x32_bf16 v[224:227], v[56:59], v[216:219], 0\n" \
"v_pk_mul_f32 v[108:109], v[108:109], s[70:71]\n" \
"v_pk_mul_f32 v[110:111], v[110:111], s[70:71]\n" \
"v_cvt_pk_bf16_f32 v108, v108, v109\n" \
"v_cvt_pk_bf16_f32 v109, v110, v111\n" \
"v_mfma_f32_16x16x32_bf16 v[240:243], v[58:61], v[232:235], 0\n" \
"v_mov_b32_dpp v166, v170 quad_perm:[0,1,2,3] row_mask:0x2 bank_mask:0xf\n" \
"v_accvgpr_read_b32 v96, a204\n" \
"v_accvgpr_read_b32 v97, a205\n" \
"v_mov_b32_dpp v166, v174 quad_perm:[0,1,2,3] row_mask:0x4 bank_mask:0xf\n" \
"v_accvgpr_read_b32 v98, a206\n" \
"v_accvgpr_read_b32 v99, a207\n" \
"v_add_f32_e64 v45, 1.0, v45\n" \
"v_mov_b32_dpp v166, v178 quad_perm:[0,1,2,3] row_mask:0x8 bank_mask:0xf\n" \
"v_mfma_f32_16x16x32_bf16 v[96:99], v[160:163], v[140:143], v[96:99]\n" \
"v_rcp_f32_e64 v45, v45\n" \
"v_mov_b32_dpp v164, v168 quad_perm:[0,1,2,3] row_mask:0x2 bank_mask:0xf\n" \
"v_mov_b32_dpp v165, v169 quad_perm:[0,1,2,3] row_mask:0x2 bank_mask:0xf\n" \
"v_rcp_f32_e64 v166, v166\n" \
"v_mov_b32_dpp v164, v172 quad_perm:[0,1,2,3] row_mask:0x4 bank_mask:0xf\n" \
"v_mov_b32_dpp v165, v173 quad_perm:[0,1,2,3] row_mask:0x4 bank_mask:0xf\n" \
"v_mul_f32_e64 v166, v45, v166\n" \
"v_mov_b32_dpp v164, v176 quad_perm:[0,1,2,3] row_mask:0x8 bank_mask:0xf\n" \
"v_pk_mul_f32 v[96:97], v[96:97], s[70:71]\n" \
"v_pk_mul_f32 v[98:99], v[98:99], s[70:71]\n" \
"v_cvt_pk_bf16_f32 v94, v96, v97\n" \
"v_cvt_pk_bf16_f32 v95, v98, v99\n" \
"ds_write_b128 v21, v[92:95] offset:25600\n" \
"v_mov_b32_dpp v165, v177 quad_perm:[0,1,2,3] row_mask:0x8 bank_mask:0xf\n" \
"v_accvgpr_read_b32 v112, a236\n" \
"v_accvgpr_read_b32 v113, a237\n" \
"v_accvgpr_read_b32 v114, a238\n" \
"v_accvgpr_read_b32 v115, a239\n" \
"v_mul_f32_e32 v164, v166, v164\n" \
"v_mul_f32_e32 v165, v166, v165\n" \
"v_mov_b32_e64 v170, v166\n" \
"v_mfma_f32_16x16x32_bf16 v[112:115], v[160:163], v[144:147], v[112:115]\n" \
"s_mov_b32 s76, s52\n" \
"v_permlane16_swap_b32_e32 v166, v170\n" \
"s_mov_b32 m0, s53\n" \
"v_mov_b32_e32 v174, v166\n" \
"v_mov_b32_e64 v178, v170\n" \
"buffer_load_dwordx4 v20, s[80:83], s76 offen offset:0 sc1 lds\n" \
"v_permlane32_swap_b32_e32 v166, v174\n" \
"v_permlane32_swap_b32_e32 v170, v178\n" \
"v_pk_mul_f32 v[112:113], v[112:113], s[70:71]\n" \
"v_pk_mul_f32 v[114:115], v[114:115], s[70:71]\n" \
"v_cvt_pk_bf16_f32 v110, v112, v113\n" \
"v_cvt_pk_bf16_f32 v111, v114, v115\n" \
"ds_write_b128 v21, v[108:111] offset:29696\n" \
"v_mul_f32_e32 v192, v166, v192\n" \
"v_mul_f32_e32 v193, v166, v193\n" \
"v_mul_f32_e32 v208, v170, v208\n" \
"v_mul_f32_e32 v209, v170, v209\n" \
"v_mul_f32_e64 v224, v174, v224\n" \
"buffer_load_dwordx4 v20, s[80:83], s76 offen offset:1024 sc1 lds\n" \
"v_mul_f32_e32 v225, v174, v225\n" \
"v_mul_f32_e32 v240, v178, v240\n" \
"v_mul_f32_e32 v241, v178, v241\n" \
"v_mul_f32_e32 v194, v166, v194\n" \
"v_mul_f32_e64 v195, v166, v195\n" \
"v_cvt_pk_bf16_f32 v192, v192, v193\n" \
"v_mul_f32_e32 v210, v170, v210\n" \
"v_mul_f32_e32 v211, v170, v211\n" \
"v_cvt_pk_bf16_f32 v208, v208, v209\n" \
"v_mul_f32_e32 v226, v174, v226\n" \
"v_mul_f32_e32 v227, v174, v227\n" \
"v_cvt_pk_bf16_f32 v224, v224, v225\n" \
"v_mul_f32_e32 v242, v178, v242\n" \
"v_mul_f32_e32 v243, v178, v243\n" \
"v_cvt_pk_bf16_f32 v240, v240, v241\n" \
"v_sub_f32_e32 v44, 1.0, v45\n" \
"v_cvt_pk_bf16_f32 v164, v164, v165\n" \
"v_cvt_pk_bf16_f32 v193, v194, v195\n" \
"s_add_u32 s84, s48, 0x0\n" \
"v_cvt_pk_bf16_f32 v209, v210, v211\n" \
"s_add_u32 s85, s48, 0x24800\n" \
"v_cvt_pk_bf16_f32 v225, v226, v227\n" \
"s_add_u32 s86, s48, 0x49000\n" \
"v_cvt_pk_bf16_f32 v241, v242, v243\n" \
"s_add_u32 s87, s48, 0x6d800\n" \
"ds_write_b32 v26, v44\n" \
"buffer_store_dword v164, v25, s[80:83], s49 offen sc1\n" \
"buffer_store_dwordx2 v[192:193], v24, s[80:83], s84 offen sc1\n" \
"buffer_store_dwordx2 v[208:209], v24, s[80:83], s85 offen sc1\n" \
"buffer_store_dwordx2 v[224:225], v24, s[80:83], s86 offen sc1\n" \
"buffer_store_dwordx2 v[240:241], v24, s[80:83], s87 offen sc1\n" \
"s_branch L_end_%=\n" \
"L_poll61_%=:\n" \
"s_mov_b32 s84, 0\n" \
"L_pl61_%=:\n" \
"s_sleep 1\n" \
"v_bfe_u32 v44, v20, 2, 5\n" \
"buffer_load_dword v44, v44, s[80:83], s38 offen offset:128 sc1\n" \
"s_waitcnt vmcnt(0)\n" \
"v_cmp_ne_u32_e32 vcc, s62, v44\n" \
"s_add_u32 s84, s84, 1\n" \
"s_nop 3\n" \
"s_cbranch_vccz L_pollr61_%=\n" \
"s_cmp_gt_u32 s84, 0x400000\n" \
"s_cbranch_scc0 L_pl61_%=\n" \
"s_branch L_pollr61_%=\n" \
"L_poll161_%=:\n" \
"s_mov_b32 s84, 0\n" \
"L_pl161_%=:\n" \
"s_sleep 1\n" \
"v_bfe_u32 v44, v20, 2, 5\n" \
"buffer_load_dword v44, v44, s[80:83], s38 offen offset:0 sc1\n" \
"s_waitcnt vmcnt(0)\n" \
"v_cmp_ne_u32_e32 vcc, s62, v44\n" \
"s_add_u32 s84, s84, 1\n" \
"s_nop 3\n" \
"s_cbranch_vccz L_pollr161_%=\n" \
"s_cmp_gt_u32 s84, 0x400000\n" \
"s_cbranch_scc0 L_pl161_%=\n" \
"s_branch L_pollr161_%=\n" \
"L_end_%=:\n"
#define PROJ_VCLOB "v21","v24","v25","v28","v29","v32","v33","v34","v35","v36","v37","v38","v39","v40","v41","v42","v43","v44","v45","v46","v47","v48","v49","v50","v51","v52","v53","v54","v55","v56","v57","v58","v59","v60","v61","v62","v63","v64","v65","v66","v67","v68","v69","v70","v71","v72","v73","v74","v75","v76","v77","v78","v79","v80","v81","v82","v83","v84","v85","v86","v87","v88","v89","v90","v91","v92","v93","v94","v95","v96","v97","v98","v99","v100","v101","v102","v103","v104","v105","v106","v107","v108","v109","v110","v111","v112","v113","v114","v115","v116","v117","v118","v119","v120","v121","v122","v123","v124","v125","v126","v127","v128","v129","v130","v131","v132","v133","v134","v135","v136","v137","v138","v139","v140","v141","v142","v143","v144","v145","v146","v147","v148","v149","v150","v151","v152","v153","v154","v155","v156","v157","v158","v159","v160","v161","v162","v163","v164","v165","v166","v167","v168","v169","v170","v171","v172","v173","v174","v175","v176","v177","v178","v179","v180","v181","v182","v183","v184","v185","v186","v187","v188","v189","v190","v192","v193","v194","v195","v196","v197","v198","v199","v200","v201","v202","v203","v204","v205","v206","v207","v208","v209","v210","v211","v212","v213","v214","v215","v216","v217","v218","v219","v220","v221","v222","v223","v224","v225","v226","v227","v228","v229","v230","v231","v232","v233","v234","v235","v236","v237","v238","v239","v240","v241","v242","v243","v244","v245","v246","v247","v248","v249","v250","v251","v252","v253","v254","v255"
#define PROJ_SCLOB "s4","s5","s6","s7","s32","s33","s34","s35","s36","s37","s38","s39","s40","s41","s42","s43","s44","s45","s46","s47","s48","s49","s50","s51","s52","s53","s54","s55","s63","s64","s65","s66","s67","s68","s69","s70","s71","s72","s73","s74","s75","s76","s77","s78","s79","s80","s81","s82","s83","s84","s85","s86","s87","s88","s89","s90","s91","s92","s93","s94","s95","s96","s97","s98","s99","s100","s101"

typedef const u16* const __attribute__((address_space(4)))* kaptr;
__device__ __forceinline__ void proj_phase(kaptr& ka, char*& ws, const unsigned wid, u32x4 (&vp)[5], unsigned& vl, u32x4 (&sa)[5], u32x2 s24, u32x2 s28, unsigned tdum, u32x4& srdA, u32x2& sd, unsigned& tag
    ) {
    const unsigned b = blockIdx.x, h = (b & 7) * 4 + ((b >> 3) & 3), mt = b >> 5;
    const unsigned rb = (wid < 2u ? 15u - mt : mt) * 128u;
    const unsigned lane = threadIdx.x & 63u, wc = wid & 1u;
    u32x2 vAc, vB;
#pragma unroll
    for (int par = 0; par < 2; ++par) {
        const unsigned lr = lane >> 3, cc = (lane & 7u) ^ (4u * par + (lr >> 1)), r0 = rb + wc * 64u + lr;
        vAc[par] = r0 * (DC * 2) + cc * 16u;
        const unsigned br = lane >> 4, p16 = lane & 15u, sc = (((p16 >> 1) ^ (br | (par << 2))) << 1) | (p16 & 1u);
        vB[par] = (16u * wid + br) * (NBIG * 2) + h * (HD * 2) + sc * 16u;
    }
    vl = lane * 16u;
    u32x2 s30 = {wid * 8192u, 65536u + wid * 4096u};
    asm volatile(PROJ_ASM
                 : "={s62}"(tag), "={s[56:59]}"(srdA), "={s[60:61]}"(sd), "+{s[8:11]}"(sa[0]), "+{s[12:15]}"(sa[1]), "+{s[16:19]}"(sa[2]), "+{s[20:23]}"(sa[3]), "+{s[24:25]}"(s24), "+{s[28:29]}"(s28), "+{s[30:31]}"(s30),
                   "={v[0:3]}"(vp[0]), "={v[4:7]}"(vp[1]), "={v[8:11]}"(vp[2]), "={v[12:15]}"(vp[3]), "={v[16:19]}"(vp[4]), "+{v[22:23]}"(vAc), "+{v[26:27]}"(vB), "+{v191}"(tdum), "+{s[26:27]}"(ws), "+{s[0:1]}"(ka)
                 : "{s2}"(b), "{s3}"(wid), "{v20}"(vl)
                 : "memory", "m0", "vcc", "scc", PROJ_VCLOB, PROJ_SCLOB, ACLOB_LO, ACLOB_HI);
    sa[4] = (u32x4){s24[0], 0u, 0u, 0u};
}

namespace att {
#define DI __device__ __forceinline__
typedef __bf16 bfx8 __attribute__((ext_vector_type(8)));
DI unsigned rfl(unsigned x) { return __builtin_amdgcn_readfirstlane(x); }
DI f32x4 mfma(u32x4 a, u32x4 b, f32x4 c) { return __builtin_amdgcn_mfma_f32_16x16x32_bf16(__builtin_bit_cast(bfx8, a), __builtin_bit_cast(bfx8, b), c, 0, 0, 0); }
typedef __bf16 bfx2 __attribute__((ext_vector_type(2)));
typedef float f32x2 __attribute__((ext_vector_type(2)));
DI unsigned cvtpk2(float lo, float hi) { f32x2 v = {lo, hi}; bfx2 b = __builtin_convertvector(v, bfx2); return __builtin_bit_cast(unsigned, b); }
DI void att_exact(const u16* __restrict__ x, const u16* __restrict__ wq, char* __restrict__ ws, unsigned h, unsigned r0, float g1) {
    const unsigned lane = lane_id(), g = lane >> 4, c = lane & 15u;
    f32x4 o[8];
#pragma unroll
    for (int n = 0; n < 8; ++n) o[n] = (f32x4){0.f, 0.f, 0.f, 0.f};
#pragma unroll 1
    for (unsigned kk = 0; kk < (unsigned)DM; kk += 32u) {
        const unsigned k = kk + 8u * g;
        const u32x4 xf = *reinterpret_cast<const u32x4*>(x + (r0 + c) * DM + k);
#pragma unroll
        for (int n = 0; n < 8; ++n) {
            const u16* w = wq + k * NBIG + h * HD + n * 16 + c; u32x4 wf;
#pragma unroll
            for (int e = 0; e < 4; ++e) wf[e] = (unsigned)w[2 * e * NBIG] | ((unsigned)w[(2 * e + 1) * NBIG] << 16);
            o[n] = mfma(wf, xf, o[n]);
        }
    }
    u32x4 q[4];
#pragma unroll
    for (int p = 0; p < 4; ++p) {
        const f32x4 a = o[2 * p] * 0.12751743082f, b = o[2 * p + 1] * 0.12751743082f;
        q[p] = (u32x4){cvtpk2(a[0], a[1]), cvtpk2(a[2], a[3]), cvtpk2(b[0], b[1]), cvtpk2(b[2], b[3])};
    }
#pragma unroll
    for (int n = 0; n < 8; ++n) o[n] = (f32x4){0.f, 0.f, 0.f, 0.f};
    const __amdgpu_buffer_rsrc_t srd = __builtin_amdgcn_make_buffer_rsrc((void*)(ws + WS_K + (size_t)h * 1048576), (short)0, 1048576, 0x00020000);
    float m = -3.0e38f, l = 0.f;
#pragma unroll 1
    for (unsigned k0 = 0; k0 <= r0 + 15u; k0 += 32u) {
        const unsigned tb = (k0 >> 6) * 32768u + (k0 & 32u) * 256u;
        f32x4 s[2]; float mx = m;
#pragma unroll
        for (int kt = 0; kt < 2; ++kt) {
            s[kt] = (f32x4){0.f, 0.f, 0.f, 0.f};
#pragma unroll
            for (int p = 0; p < 4; ++p) s[kt] = mfma(__builtin_amdgcn_raw_buffer_load_b128(srd, lane * 16u + (kt * 4 + p) * 1024u, tb, 16), q[p], s[kt]);
#pragma unroll
            for (int e = 0; e < 4; ++e) { if (k0 + 16u * kt + 4u * g + e > r0 + c) s[kt][e] = -3.0e38f; mx = fmaxf(mx, s[kt][e]); }
        }
        mx = fmaxf(mx, __shfl_xor(mx, 16)); mx = fmaxf(mx, __shfl_xor(mx, 32));
        const float al = __builtin_amdgcn_exp2f(m - mx); m = mx;
        u32x4 pf;
#pragma unroll
        for (int e = 0; e < 4; ++e) pf[e] = cvtpk2(__builtin_amdgcn_exp2f(s[e >> 1][2 * (e & 1)] - m), __builtin_amdgcn_exp2f(s[e >> 1][2 * (e & 1) + 1] - m));
        l *= al;
#pragma unroll
        for (int e = 0; e < 4; ++e) l += __uint_as_float(pf[e] << 16) + __uint_as_float(pf[e] & 0xffff0000u);
#pragma unroll
        for (int n = 0; n < 8; ++n) o[n] = mfma(__builtin_amdgcn_raw_buffer_load_b128(srd, lane * 16u + 16384u + n * 1024u, tb, 16), pf, o[n] * al);
    }
    l += __shfl_xor(l, 16); l += __shfl_xor(l, 32);
    const float f = g1 / l;
    const __amdgpu_buffer_rsrc_t srdA = __builtin_amdgcn_make_buffer_rsrc((void*)(ws + WS_AF), (short)0, 128 * KS_TOT * 1024, 0x00020000);
#pragma unroll
    for (int p = 0; p < 4; ++p) {
        const f32x4 a = o[2 * p] * f, b = o[2 * p + 1] * f;
        __builtin_amdgcn_raw_buffer_store_b128((u32x4){cvtpk2(a[0], a[1]), cvtpk2(a[2], a[3]), cvtpk2(b[0], b[1]), cvtpk2(b[2], b[3])}, srdA, lane * 16u + p * 1024u, ((r0 >> 4) * KS_TOT + 18u + 4u * h) * 1024u, 16);
    }
}
}

#define ATT_ASM \
"s_mov_b32 s101, 0\n" \
"s_waitcnt vmcnt(15) lgkmcnt(0)\n" \
"s_barrier\n" \
"v_accvgpr_write_b32 a216, 0\n" \
"v_accvgpr_write_b32 a217, 0\n" \
"v_accvgpr_write_b32 a218, 0\n" \
"v_accvgpr_write_b32 a219, 0\n" \
"v_bfe_u32 v61, v20, 2, 5\n" \
"s_mov_b32 s96, s24\n" \
"v_mfma_f32_32x32x16_bf16 v[128:143], a[216:219], a[216:219], 0\n" \
"buffer_load_dword v57, v61, s[56:59], s96 offen sc1\n" \
"v_and_b32_e32 v63, 0x300, v20\n" \
"ds_read_b128 v[96:99], v63 offset:0\n" \
"v_add_u32_e32 v58, s22, v20\n" \
"ds_read_b128 a[0:3], v58 offset:0\n" \
"v_mfma_f32_32x32x16_bf16 v[144:159], a[216:219], a[216:219], 0\n" \
"ds_read_b128 a[16:19], v58 offset:8192\n" \
"v_add_u32_e32 v59, s23, v20\n" \
"ds_read_b128 a[32:35], v59 offset:0\n" \
"ds_read_b128 a[48:51], v59 offset:8192\n" \
"ds_read_b128 v[100:103], v63 offset:1024\n" \
"ds_read_b128 a[4:7], v58 offset:1024\n" \
"v_mfma_f32_32x32x16_bf16 v[160:175], a[216:219], a[216:219], 0\n" \
"ds_read_b128 a[20:23], v58 offset:9216\n" \
"ds_read_b128 a[36:39], v59 offset:1024\n" \
"ds_read_b128 a[52:55], v59 offset:9216\n" \
"s_sub_u32 s67, s12, 0xc000\n" \
"ds_read_b128 v[104:107], v63 offset:2048\n" \
"s_mov_b32 s65, s12\n" \
"v_mfma_f32_32x32x16_bf16 v[176:191], a[216:219], a[216:219], 0\n" \
"v_add_u32_e32 v55, s67, v20\n" \
"v_add_u32_e32 v52, 0xc000, v55\n" \
"s_xor_b32 m0, s65, 0x10000\n" \
"ds_read_b128 a[8:11], v58 offset:2048\n" \
"buffer_load_dwordx4 v52, s[8:11], 0 offen offset:0 sc1 lds\n" \
"v_mfma_f32_32x32x16_bf16 v[192:207], a[216:219], a[216:219], 0\n" \
"ds_read_b128 a[24:27], v58 offset:10240\n" \
"ds_read_b128 a[40:43], v59 offset:2048\n" \
"ds_read_b128 a[56:59], v59 offset:10240\n" \
"ds_read_b128 v[108:111], v63 offset:3072\n" \
"buffer_load_dwordx4 v52, s[8:11], 0 offen offset:1024 sc1 lds\n" \
"v_mfma_f32_32x32x16_bf16 v[208:223], a[216:219], a[216:219], 0\n" \
"ds_read_b128 a[12:15], v58 offset:3072\n" \
"ds_read_b128 a[28:31], v58 offset:11264\n" \
"ds_read_b128 a[44:47], v59 offset:3072\n" \
"ds_read_b128 a[60:63], v59 offset:11264\n" \
"s_waitcnt lgkmcnt(15)\n" \
"v_xor_b32_e32 v96, 0x80008000, v96\n" \
"v_mfma_f32_32x32x16_bf16 v[224:239], a[216:219], a[216:219], 0\n" \
"v_xor_b32_e32 v97, 0x80008000, v97\n" \
"v_xor_b32_e32 v98, 0x80008000, v98\n" \
"v_xor_b32_e32 v99, 0x80008000, v99\n" \
"ds_read_b128 a[160:163], v20 offset:0\n" \
"ds_read_b128 a[164:167], v20 offset:1024\n" \
"s_waitcnt lgkmcnt(15)\n" \
"v_xor_b32_e32 v100, 0x80008000, v100\n" \
"v_mfma_f32_32x32x16_bf16 v[240:255], a[216:219], a[216:219], 0\n" \
"v_xor_b32_e32 v101, 0x80008000, v101\n" \
"v_xor_b32_e32 v102, 0x80008000, v102\n" \
"v_xor_b32_e32 v103, 0x80008000, v103\n" \
"ds_read_b128 a[168:171], v20 offset:2048\n" \
"ds_read_b128 a[172:175], v20 offset:3072\n" \
"ds_read_b128 a[188:191], v20 offset:7168\n" \
"v_mfma_f32_16x16x32_bf16 a[192:195], a[216:219], a[216:219], 0\n" \
"ds_read_b128 a[184:187], v20 offset:6144\n" \
"s_waitcnt lgkmcnt(15)\n" \
"v_xor_b32_e32 v104, 0x80008000, v104\n" \
"v_mfma_f32_16x16x32_bf16 a[196:199], a[216:219], a[216:219], 0\n" \
"v_xor_b32_e32 v105, 0x80008000, v105\n" \
"v_xor_b32_e32 v106, 0x80008000, v106\n" \
"v_mfma_f32_16x16x32_bf16 v[32:35], v[96:99], a[0:3], 0\n" \
"v_xor_b32_e32 v107, 0x80008000, v107\n" \
"ds_read_b128 a[180:183], v20 offset:5120\n" \
"v_mfma_f32_16x16x32_bf16 v[36:39], v[96:99], a[16:19], 0\n" \
"ds_read_b128 a[176:179], v20 offset:4096\n" \
"s_waitcnt lgkmcnt(12)\n" \
"v_xor_b32_e32 v108, 0x80008000, v108\n" \
"v_mfma_f32_16x16x32_bf16 v[40:43], v[96:99], a[32:35], 0\n" \
"v_xor_b32_e32 v109, 0x80008000, v109\n" \
"v_xor_b32_e32 v110, 0x80008000, v110\n" \
"v_mfma_f32_16x16x32_bf16 v[44:47], v[96:99], a[48:51], 0\n" \
"v_xor_b32_e32 v111, 0x80008000, v111\n" \
"v_add_u32_e32 v60, s15, v20\n" \
"v_mfma_f32_16x16x32_bf16 v[32:35], v[100:103], a[4:7], v[32:35]\n" \
"v_add_u32_e32 v60, 0x10000, v60\n" \
"ds_read_b128 a[128:131], v20 offset:8192\n" \
"v_mfma_f32_16x16x32_bf16 v[36:39], v[100:103], a[20:23], v[36:39]\n" \
"ds_read_b128 a[144:147], v20 offset:12288\n" \
"ds_read_b128 a[132:135], v20 offset:9216\n" \
"v_mfma_f32_16x16x32_bf16 v[40:43], v[100:103], a[36:39], v[40:43]\n" \
"ds_read_b128 a[148:151], v20 offset:13312\n" \
"ds_read_b128 a[136:139], v20 offset:10240\n" \
"v_mfma_f32_16x16x32_bf16 v[44:47], v[100:103], a[52:55], v[44:47]\n" \
"ds_read_b128 a[152:155], v20 offset:14336\n" \
"ds_read_b128 a[140:143], v20 offset:11264\n" \
"v_mfma_f32_16x16x32_bf16 v[32:35], v[104:107], a[8:11], v[32:35]\n" \
"ds_read_b128 a[156:159], v20 offset:15360\n" \
"ds_read_b128 a[64:67], v60 offset:16384\n" \
"v_mfma_f32_16x16x32_bf16 v[36:39], v[104:107], a[24:27], v[36:39]\n" \
"ds_read_b128 a[68:71], v60 offset:17408\n" \
"ds_read_b128 a[72:75], v60 offset:18432\n" \
"v_mfma_f32_16x16x32_bf16 v[40:43], v[104:107], a[40:43], v[40:43]\n" \
"ds_read_b128 a[76:79], v60 offset:19456\n" \
"ds_read_b128 a[80:83], v60 offset:24576\n" \
"v_mfma_f32_16x16x32_bf16 v[44:47], v[104:107], a[56:59], v[44:47]\n" \
"ds_read_b128 a[84:87], v60 offset:25600\n" \
"s_waitcnt lgkmcnt(15)\n" \
"v_mfma_f32_16x16x32_bf16 v[32:35], v[108:111], a[12:15], v[32:35]\n" \
"ds_read_b128 a[88:91], v60 offset:26624\n" \
"ds_read_b128 a[92:95], v60 offset:27648\n" \
"v_mfma_f32_16x16x32_bf16 v[36:39], v[108:111], a[28:31], v[36:39]\n" \
"ds_read_b128 a[96:99], v60 offset:0\n" \
"ds_read_b128 a[100:103], v60 offset:1024\n" \
"v_mfma_f32_16x16x32_bf16 v[40:43], v[108:111], a[44:47], v[40:43]\n" \
"ds_read_b128 a[104:107], v60 offset:2048\n" \
"ds_read_b128 a[108:111], v60 offset:3072\n" \
"v_mfma_f32_16x16x32_bf16 v[44:47], v[108:111], a[60:63], v[44:47]\n" \
"ds_read_b128 a[112:115], v60 offset:8192\n" \
"ds_read_b128 a[116:119], v60 offset:9216\n" \
"v_mfma_f32_16x16x32_bf16 v[64:67], a[160:163], a[0:3], v[32:35]\n" \
"ds_read_b128 a[120:123], v60 offset:10240\n" \
"ds_read_b128 a[124:127], v60 offset:11264\n" \
"v_mfma_f32_16x16x32_bf16 v[64:67], a[164:167], a[4:7], v[64:67]\n" \
"s_mov_b32 m0, s20\n" \
"v_mov_b32_e32 v54, 0x3f803f80\n" \
"v_mfma_f32_16x16x32_bf16 v[64:67], a[168:171], a[8:11], v[64:67]\n" \
"global_load_lds_dword v[16:17], off\n" \
"v_mfma_f32_16x16x32_bf16 v[64:67], a[172:175], a[12:15], v[64:67]\n" \
"s_mov_b32 s84, 0x33333333\n" \
"s_mov_b32 s85, 0xcccccccc\n" \
"v_mfma_f32_16x16x32_bf16 v[68:71], a[188:191], a[12:15], v[32:35]\n" \
"s_mov_b32 s86, 0x11111111\n" \
"s_mov_b32 s87, 0x11111111\n" \
"v_mfma_f32_16x16x32_bf16 v[68:71], a[184:187], a[8:11], v[68:71]\n" \
"s_mov_b32 s88, 0x44444444\n" \
"s_mov_b32 s89, 0x44444444\n" \
"v_mfma_f32_16x16x32_bf16 v[68:71], a[180:183], a[4:7], v[68:71]\n" \
"v_bfe_i32 v21, v20, 4, 1\n" \
"s_waitcnt lgkmcnt(15)\n" \
"v_mfma_f32_16x16x32_bf16 v[68:71], a[176:179], a[0:3], v[68:71]\n" \
"v_exp_f32_e32 v66, v66\n" \
"v_mfma_f32_16x16x32_bf16 v[76:79], a[176:179], a[16:19], v[36:39]\n" \
"v_exp_f32_e32 v67, v67\n" \
"v_mfma_f32_16x16x32_bf16 v[76:79], a[180:183], a[20:23], v[76:79]\n" \
"v_exp_f32_e32 v64, v64\n" \
"v_mfma_f32_16x16x32_bf16 v[76:79], a[184:187], a[24:27], v[76:79]\n" \
"v_exp_f32_e32 v65, v65\n" \
"v_mfma_f32_16x16x32_bf16 v[76:79], a[188:191], a[28:31], v[76:79]\n" \
"v_exp_f32_e32 v70, v70\n" \
"v_mfma_f32_16x16x32_bf16 v[72:75], a[172:175], a[28:31], v[36:39]\n" \
"v_exp_f32_e32 v71, v71\n" \
"v_mfma_f32_16x16x32_bf16 v[72:75], a[168:171], a[24:27], v[72:75]\n" \
"v_exp_f32_e32 v68, v68\n" \
"v_mfma_f32_16x16x32_bf16 v[72:75], a[164:167], a[20:23], v[72:75]\n" \
"v_exp_f32_e32 v69, v69\n" \
"v_mfma_f32_16x16x32_bf16 v[72:75], a[160:163], a[16:19], v[72:75]\n" \
"v_cvt_pk_bf16_f32 v71, v70, v71\n" \
"v_cndmask_b32_e64 v22, 0, v54, s[84:85]\n" \
"v_mfma_f32_16x16x32_bf16 v[80:83], a[160:163], a[32:35], v[40:43]\n" \
"v_exp_f32_e32 v76, v76\n" \
"v_mfma_f32_16x16x32_bf16 v[80:83], a[164:167], a[36:39], v[80:83]\n" \
"v_exp_f32_e32 v77, v77\n" \
"v_mfma_f32_16x16x32_bf16 v[80:83], a[168:171], a[40:43], v[80:83]\n" \
"v_exp_f32_e32 v78, v78\n" \
"v_mfma_f32_16x16x32_bf16 v[80:83], a[172:175], a[44:47], v[80:83]\n" \
"v_exp_f32_e32 v72, v72\n" \
"v_mfma_f32_16x16x32_bf16 v[84:87], a[188:191], a[44:47], v[40:43]\n" \
"s_waitcnt vmcnt(3)\n" \
"v_cmp_ne_u32_e32 vcc, s62, v57\n" \
"v_mfma_f32_16x16x32_bf16 v[84:87], a[184:187], a[40:43], v[84:87]\n" \
"v_exp_f32_e32 v73, v73\n" \
"v_mfma_f32_16x16x32_bf16 v[84:87], a[180:183], a[36:39], v[84:87]\n" \
"v_exp_f32_e32 v74, v74\n" \
"v_mfma_f32_16x16x32_bf16 v[84:87], a[176:179], a[32:35], v[84:87]\n" \
"s_cbranch_vccnz L_skb_%=\n" \
"L_rkb_%=:\n" \
"v_cvt_pk_bf16_f32 v72, v72, v73\n" \
"v_mfma_f32_16x16x32_bf16 v[92:95], a[176:179], a[48:51], v[44:47]\n" \
"v_exp_f32_e32 v75, v75\n" \
"v_mfma_f32_16x16x32_bf16 v[92:95], a[180:183], a[52:55], v[92:95]\n" \
"v_exp_f32_e32 v79, v79\n" \
"v_mfma_f32_16x16x32_bf16 v[92:95], a[184:187], a[56:59], v[92:95]\n" \
"v_cndmask_b32_e64 v23, 0, v54, s[86:87]\n" \
"v_cndmask_b32_e64 v24, 0, v54, s[88:89]\n" \
"v_mfma_f32_16x16x32_bf16 v[92:95], a[188:191], a[60:63], v[92:95]\n" \
"v_and_b32_e32 v21, 0xaaaaaaaa, v21\n" \
"v_cvt_pk_bf16_f32 v70, v68, v69\n" \
"v_mfma_f32_16x16x32_bf16 v[88:91], a[172:175], a[60:63], v[44:47]\n" \
"v_cvt_pk_bf16_f32 v73, v74, v75\n" \
"v_mov_b32_e32 v62, 0\n" \
"v_mfma_f32_16x16x32_bf16 v[88:91], a[168:171], a[56:59], v[88:91]\n" \
"v_accvgpr_write_b32 a208, 0\n" \
"v_accvgpr_write_b32 a209, 0\n" \
"v_mfma_f32_16x16x32_bf16 v[88:91], a[164:167], a[52:55], v[88:91]\n" \
"v_accvgpr_write_b32 a210, 0\n" \
"v_accvgpr_write_b32 a211, 0\n" \
"v_mfma_f32_16x16x32_bf16 v[88:91], a[160:163], a[48:51], v[88:91]\n" \
"v_accvgpr_write_b32 a200, v22\n" \
"v_accvgpr_write_b32 a201, v22\n" \
"s_waitcnt vmcnt(4) lgkmcnt(0)\n" \
"s_barrier\n" \
"v_accvgpr_write_b32 a202, v22\n" \
"v_accvgpr_write_b32 a203, v22\n" \
"v_accvgpr_write_b32 a204, v23\n" \
"v_accvgpr_write_b32 a205, v23\n" \
"v_accvgpr_write_b32 a206, v23\n" \
"v_accvgpr_write_b32 a207, v23\n" \
"v_accvgpr_write_b32 a212, v24\n" \
"v_accvgpr_write_b32 a213, v24\n" \
"v_accvgpr_write_b32 a214, v24\n" \
"v_accvgpr_write_b32 a215, v24\n" \
"v_or_b32_e32 v21, 0x44444444, v21\n" \
"s_mov_b32 s64, 0x8000\n" \
"v_add_u32_e32 v50, 0x6000, v55\n" \
"v_add_u32_e32 v51, 0xa000, v55\n" \
"v_add_u32_e32 v53, 0x10000, v55\n" \
"v_add_u32_e32 v48, 0xc000, v20\n" \
"v_add_u32_e32 v49, s17, v20\n" \
"v_accvgpr_write_b32 a220, v96\n" \
"v_accvgpr_write_b32 a221, v97\n" \
"v_accvgpr_write_b32 a222, v98\n" \
"v_accvgpr_write_b32 a223, v99\n" \
"v_accvgpr_write_b32 a224, v100\n" \
"v_accvgpr_write_b32 a225, v101\n" \
"v_accvgpr_write_b32 a226, v102\n" \
"v_accvgpr_write_b32 a227, v103\n" \
"v_accvgpr_write_b32 a228, v104\n" \
"v_accvgpr_write_b32 a229, v105\n" \
"v_accvgpr_write_b32 a230, v106\n" \
"v_accvgpr_write_b32 a231, v107\n" \
"v_accvgpr_write_b32 a232, v108\n" \
"v_accvgpr_write_b32 a233, v109\n" \
"v_accvgpr_write_b32 a234, v110\n" \
"v_accvgpr_write_b32 a235, v111\n" \
"v_cvt_pk_bf16_f32 v69, v66, v67\n" \
"v_cvt_pk_bf16_f32 v68, v64, v65\n" \
"v_cvt_pk_bf16_f32 v74, v76, v77\n" \
"v_cvt_pk_bf16_f32 v75, v78, v79\n" \
"s_sub_u32 s66, s18, 1\n" \
"s_cbranch_scc1 L_tr_%=\n" \
"L_1_%=:\n" \
"v_mfma_f32_16x16x32_bf16 v[96:99], a[128:131], a[0:3], v[32:35]\n" \
"s_xor_b32 m0, s65, 0x12000\n" \
"ds_read_b128 a[160:163], v49 offset:0\n" \
"v_mfma_f32_16x16x32_bf16 v[96:99], a[132:135], a[4:7], v[96:99]\n" \
"buffer_load_dwordx4 v50, s[8:11], s64 offen offset:0 sc1 lds\n" \
"v_mfma_f32_16x16x32_bf16 v[96:99], a[136:139], a[8:11], v[96:99]\n" \
"ds_read_b128 a[164:167], v49 offset:1024\n" \
"ds_read_b128 a[168:171], v49 offset:2048\n" \
"v_mfma_f32_16x16x32_bf16 v[96:99], a[140:143], a[12:15], v[96:99]\n" \
"ds_read_b128 a[172:175], v49 offset:3072\n" \
"ds_read_b128 a[176:179], v49 offset:4096\n" \
"v_mfma_f32_16x16x32_bf16 v[100:103], a[156:159], a[12:15], v[32:35]\n" \
"ds_read_b128 a[180:183], v49 offset:5120\n" \
"ds_read_b128 a[184:187], v49 offset:6144\n" \
"v_mfma_f32_16x16x32_bf16 v[100:103], a[152:155], a[8:11], v[100:103]\n" \
"v_exp_f32_e32 v86, v86\n" \
"v_mfma_f32_16x16x32_bf16 v[100:103], a[148:151], a[4:7], v[100:103]\n" \
"v_exp_f32_e32 v87, v87\n" \
"v_mfma_f32_16x16x32_bf16 v[100:103], a[144:147], a[0:3], v[100:103]\n" \
"ds_read_b128 a[188:191], v49 offset:7168\n" \
"v_cvt_pk_bf16_f32 v87, v86, v87\n" \
"v_mfma_f32_16x16x32_bf16 v[108:111], a[144:147], a[16:19], v[36:39]\n" \
"v_exp_f32_e32 v84, v84\n" \
"v_mfma_f32_16x16x32_bf16 v[108:111], a[148:151], a[20:23], v[108:111]\n" \
"v_exp_f32_e32 v85, v85\n" \
"v_mfma_f32_16x16x32_bf16 v[108:111], a[152:155], a[24:27], v[108:111]\n" \
"v_exp_f32_e32 v82, v82\n" \
"v_mfma_f32_16x16x32_bf16 v[108:111], a[156:159], a[28:31], v[108:111]\n" \
"v_exp_f32_e32 v83, v83\n" \
"v_mfma_f32_16x16x32_bf16 v[104:107], a[140:143], a[28:31], v[36:39]\n" \
"v_exp_f32_e32 v80, v80\n" \
"v_mfma_f32_16x16x32_bf16 v[104:107], a[136:139], a[24:27], v[104:107]\n" \
"v_exp_f32_e32 v81, v81\n" \
"v_mfma_f32_16x16x32_bf16 v[104:107], a[132:135], a[20:23], v[104:107]\n" \
"v_exp_f32_e32 v88, v88\n" \
"v_mfma_f32_16x16x32_bf16 v[104:107], a[128:131], a[16:19], v[104:107]\n" \
"buffer_load_dwordx4 v50, s[8:11], s64 offen offset:1024 sc1 lds\n" \
"v_mfma_f32_16x16x32_bf16 v[112:115], a[128:131], a[32:35], v[40:43]\n" \
"s_xor_b32 m0, s65, 0x6000\n" \
"v_cvt_pk_bf16_f32 v86, v84, v85\n" \
"v_mfma_f32_16x16x32_bf16 v[112:115], a[132:135], a[36:39], v[112:115]\n" \
"v_cvt_pk_bf16_f32 v85, v82, v83\n" \
"v_cvt_pk_bf16_f32 v84, v80, v81\n" \
"v_mfma_f32_16x16x32_bf16 v[112:115], a[136:139], a[40:43], v[112:115]\n" \
"v_exp_f32_e32 v89, v89\n" \
"v_mfma_f32_16x16x32_bf16 v[112:115], a[140:143], a[44:47], v[112:115]\n" \
"v_exp_f32_e32 v90, v90\n" \
"v_mfma_f32_16x16x32_bf16 v[116:119], a[156:159], a[44:47], v[40:43]\n" \
"v_exp_f32_e32 v91, v91\n" \
"v_mfma_f32_16x16x32_bf16 v[116:119], a[152:155], a[40:43], v[116:119]\n" \
"v_exp_f32_e32 v92, v92\n" \
"v_mfma_f32_16x16x32_bf16 v[116:119], a[148:151], a[36:39], v[116:119]\n" \
"v_exp_f32_e32 v93, v93\n" \
"v_mfma_f32_16x16x32_bf16 v[116:119], a[144:147], a[32:35], v[116:119]\n" \
"v_exp_f32_e32 v94, v94\n" \
"v_mfma_f32_16x16x32_bf16 v[124:127], a[144:147], a[48:51], v[44:47]\n" \
"v_cvt_pk_bf16_f32 v88, v88, v89\n" \
"v_cvt_pk_bf16_f32 v89, v90, v91\n" \
"v_mfma_f32_16x16x32_bf16 v[124:127], a[148:151], a[52:55], v[124:127]\n" \
"v_exp_f32_e32 v95, v95\n" \
"v_mfma_f32_16x16x32_bf16 v[124:127], a[152:155], a[56:59], v[124:127]\n" \
"v_cvt_pk_bf16_f32 v90, v92, v93\n" \
"v_cvt_pk_bf16_f32 v91, v94, v95\n" \
"v_mfma_f32_16x16x32_bf16 v[124:127], a[156:159], a[60:63], v[124:127]\n" \
"ds_read_b128 a[144:147], v48 offset:4096\n" \
"ds_read_b128 a[148:151], v48 offset:5120\n" \
"v_mfma_f32_16x16x32_bf16 v[120:123], a[140:143], a[60:63], v[44:47]\n" \
"ds_read_b128 a[152:155], v48 offset:6144\n" \
"ds_read_b128 a[140:143], v48 offset:3072\n" \
"v_mfma_f32_16x16x32_bf16 v[120:123], a[136:139], a[56:59], v[120:123]\n" \
"ds_read_b128 a[136:139], v48 offset:2048\n" \
"ds_read_b128 a[156:159], v48 offset:7168\n" \
"v_mfma_f32_16x16x32_bf16 v[120:123], a[132:135], a[52:55], v[120:123]\n" \
"buffer_load_dwordx4 v51, s[8:11], s64 offen offset:0 sc1 lds\n" \
"v_mfma_f32_16x16x32_bf16 v[120:123], a[128:131], a[48:51], v[120:123]\n" \
"ds_read_b128 a[128:131], v48 offset:0\n" \
"s_waitcnt lgkmcnt(7)\n" \
"v_mfma_f32_16x16x32_bf16 v[128:131], a[160:163], v[68:71], v[128:131]\n" \
"v_exp_f32_e32 v102, v102\n" \
"v_mfma_f32_16x16x32_bf16 v[160:163], a[160:163], v[72:75], v[160:163]\n" \
"v_exp_f32_e32 v103, v103\n" \
"v_mfma_f32_16x16x32_bf16 v[132:135], a[164:167], v[68:71], v[132:135]\n" \
"ds_read_b128 a[132:135], v48 offset:1024\n" \
"v_cvt_pk_bf16_f32 v103, v102, v103\n" \
"v_mfma_f32_16x16x32_bf16 v[164:167], a[164:167], v[72:75], v[164:167]\n" \
"v_exp_f32_e32 v100, v100\n" \
"v_mfma_f32_16x16x32_bf16 v[136:139], a[168:171], v[68:71], v[136:139]\n" \
"v_exp_f32_e32 v101, v101\n" \
"v_mfma_f32_16x16x32_bf16 v[168:171], a[168:171], v[72:75], v[168:171]\n" \
"v_exp_f32_e32 v98, v98\n" \
"v_mfma_f32_16x16x32_bf16 v[140:143], a[172:175], v[68:71], v[140:143]\n" \
"v_exp_f32_e32 v99, v99\n" \
"v_mfma_f32_16x16x32_bf16 v[172:175], a[172:175], v[72:75], v[172:175]\n" \
"v_exp_f32_e32 v96, v96\n" \
"v_mfma_f32_16x16x32_bf16 v[144:147], a[176:179], v[68:71], v[144:147]\n" \
"v_exp_f32_e32 v97, v97\n" \
"v_mfma_f32_16x16x32_bf16 v[176:179], a[176:179], v[72:75], v[176:179]\n" \
"v_exp_f32_e32 v104, v104\n" \
"v_mfma_f32_16x16x32_bf16 v[148:151], a[180:183], v[68:71], v[148:151]\n" \
"v_exp_f32_e32 v105, v105\n" \
"v_mfma_f32_16x16x32_bf16 v[180:183], a[180:183], v[72:75], v[180:183]\n" \
"v_cvt_pk_bf16_f32 v102, v100, v101\n" \
"v_cvt_pk_bf16_f32 v101, v98, v99\n" \
"v_mfma_f32_16x16x32_bf16 v[152:155], a[184:187], v[68:71], v[152:155]\n" \
"v_cvt_pk_bf16_f32 v100, v96, v97\n" \
"v_cvt_pk_bf16_f32 v104, v104, v105\n" \
"v_mfma_f32_16x16x32_bf16 v[184:187], a[184:187], v[72:75], v[184:187]\n" \
"buffer_load_dwordx4 v51, s[8:11], s64 offen offset:1024 sc1 lds\n" \
"v_mfma_f32_16x16x32_bf16 v[156:159], a[188:191], v[68:71], v[156:159]\n" \
"v_exp_f32_e32 v106, v106\n" \
"v_mfma_f32_16x16x32_bf16 v[188:191], a[188:191], v[72:75], v[188:191]\n" \
"v_exp_f32_e32 v107, v107\n" \
"v_smfmac_f32_16x16x64_bf16 a[192:195], a[200:203], v[68:75], v21\n" \
"v_exp_f32_e32 v108, v108\n" \
"v_mfma_f32_16x16x32_bf16 v[220:223], a[188:191], v[84:87], v[220:223]\n" \
"v_exp_f32_e32 v109, v109\n" \
"v_mfma_f32_16x16x32_bf16 v[252:255], a[188:191], v[88:91], v[252:255]\n" \
"v_exp_f32_e32 v110, v110\n" \
"v_mfma_f32_16x16x32_bf16 v[216:219], a[184:187], v[84:87], v[216:219]\n" \
"v_exp_f32_e32 v111, v111\n" \
"v_mfma_f32_16x16x32_bf16 v[248:251], a[184:187], v[88:91], v[248:251]\n" \
"v_exp_f32_e32 v118, v118\n" \
"v_mfma_f32_16x16x32_bf16 v[212:215], a[180:183], v[84:87], v[212:215]\n" \
"v_exp_f32_e32 v119, v119\n" \
"v_mfma_f32_16x16x32_bf16 v[244:247], a[180:183], v[88:91], v[244:247]\n" \
"v_cvt_pk_bf16_f32 v105, v106, v107\n" \
"v_cvt_pk_bf16_f32 v106, v108, v109\n" \
"v_mfma_f32_16x16x32_bf16 v[208:211], a[176:179], v[84:87], v[208:211]\n" \
"v_cvt_pk_bf16_f32 v107, v110, v111\n" \
"v_cvt_pk_bf16_f32 v119, v118, v119\n" \
"v_mfma_f32_16x16x32_bf16 v[240:243], a[176:179], v[88:91], v[240:243]\n" \
"v_exp_f32_e32 v116, v116\n" \
"v_mfma_f32_16x16x32_bf16 v[204:207], a[172:175], v[84:87], v[204:207]\n" \
"v_exp_f32_e32 v117, v117\n" \
"v_mfma_f32_16x16x32_bf16 v[236:239], a[172:175], v[88:91], v[236:239]\n" \
"v_exp_f32_e32 v114, v114\n" \
"v_mfma_f32_16x16x32_bf16 v[200:203], a[168:171], v[84:87], v[200:203]\n" \
"v_exp_f32_e32 v115, v115\n" \
"v_mfma_f32_16x16x32_bf16 v[232:235], a[168:171], v[88:91], v[232:235]\n" \
"v_exp_f32_e32 v112, v112\n" \
"v_mfma_f32_16x16x32_bf16 v[196:199], a[164:167], v[84:87], v[196:199]\n" \
"v_exp_f32_e32 v113, v113\n" \
"v_mfma_f32_16x16x32_bf16 v[228:231], a[164:167], v[88:91], v[228:231]\n" \
"v_exp_f32_e32 v120, v120\n" \
"v_mfma_f32_16x16x32_bf16 v[192:195], a[160:163], v[84:87], v[192:195]\n" \
"v_exp_f32_e32 v121, v121\n" \
"v_mfma_f32_16x16x32_bf16 v[224:227], a[160:163], v[88:91], v[224:227]\n" \
"v_cvt_pk_bf16_f32 v118, v116, v117\n" \
"v_cvt_pk_bf16_f32 v117, v114, v115\n" \
"v_smfmac_f32_16x16x64_bf16 a[196:199], a[200:203], v[84:91], v21\n" \
"v_cvt_pk_bf16_f32 v116, v112, v113\n" \
"v_cvt_pk_bf16_f32 v120, v120, v121\n" \
"s_waitcnt vmcnt(4) lgkmcnt(0)\n" \
"s_barrier\n" \
"v_mfma_f32_16x16x32_bf16 v[64:67], a[128:131], a[0:3], v[32:35]\n" \
"s_xor_b32 m0, s65, 0x14000\n" \
"ds_read_b128 a[160:163], v49 offset:8192\n" \
"v_mfma_f32_16x16x32_bf16 v[64:67], a[132:135], a[4:7], v[64:67]\n" \
"buffer_load_dwordx4 v52, s[8:11], s64 offen offset:0 sc1 lds\n" \
"v_mfma_f32_16x16x32_bf16 v[64:67], a[136:139], a[8:11], v[64:67]\n" \
"ds_read_b128 a[164:167], v49 offset:9216\n" \
"ds_read_b128 a[168:171], v49 offset:10240\n" \
"v_mfma_f32_16x16x32_bf16 v[64:67], a[140:143], a[12:15], v[64:67]\n" \
"ds_read_b128 a[172:175], v49 offset:11264\n" \
"ds_read_b128 a[176:179], v49 offset:12288\n" \
"v_mfma_f32_16x16x32_bf16 v[68:71], a[156:159], a[12:15], v[32:35]\n" \
"ds_read_b128 a[180:183], v49 offset:13312\n" \
"ds_read_b128 a[184:187], v49 offset:14336\n" \
"v_mfma_f32_16x16x32_bf16 v[68:71], a[152:155], a[8:11], v[68:71]\n" \
"v_exp_f32_e32 v122, v122\n" \
"v_mfma_f32_16x16x32_bf16 v[68:71], a[148:151], a[4:7], v[68:71]\n" \
"v_exp_f32_e32 v123, v123\n" \
"v_mfma_f32_16x16x32_bf16 v[68:71], a[144:147], a[0:3], v[68:71]\n" \
"ds_read_b128 a[188:191], v49 offset:15360\n" \
"v_cvt_pk_bf16_f32 v121, v122, v123\n" \
"v_mfma_f32_16x16x32_bf16 v[76:79], a[144:147], a[16:19], v[36:39]\n" \
"v_exp_f32_e32 v124, v124\n" \
"v_mfma_f32_16x16x32_bf16 v[76:79], a[148:151], a[20:23], v[76:79]\n" \
"v_exp_f32_e32 v125, v125\n" \
"v_mfma_f32_16x16x32_bf16 v[76:79], a[152:155], a[24:27], v[76:79]\n" \
"v_exp_f32_e32 v126, v126\n" \
"v_mfma_f32_16x16x32_bf16 v[76:79], a[156:159], a[28:31], v[76:79]\n" \
"v_exp_f32_e32 v127, v127\n" \
"v_mfma_f32_16x16x32_bf16 v[72:75], a[140:143], a[28:31], v[36:39]\n" \
"v_cvt_pk_bf16_f32 v122, v124, v125\n" \
"v_cvt_pk_bf16_f32 v123, v126, v127\n" \
"v_mfma_f32_16x16x32_bf16 v[72:75], a[136:139], a[24:27], v[72:75]\n" \
"v_exp_f32_e32 v70, v70\n" \
"v_mfma_f32_16x16x32_bf16 v[72:75], a[132:135], a[20:23], v[72:75]\n" \
"v_exp_f32_e32 v71, v71\n" \
"v_mfma_f32_16x16x32_bf16 v[72:75], a[128:131], a[16:19], v[72:75]\n" \
"buffer_load_dwordx4 v52, s[8:11], s64 offen offset:1024 sc1 lds\n" \
"v_mfma_f32_16x16x32_bf16 v[80:83], a[128:131], a[32:35], v[40:43]\n" \
"s_mov_b32 m0, s65\n" \
"v_cvt_pk_bf16_f32 v71, v70, v71\n" \
"v_mfma_f32_16x16x32_bf16 v[80:83], a[132:135], a[36:39], v[80:83]\n" \
"v_exp_f32_e32 v68, v68\n" \
"v_mfma_f32_16x16x32_bf16 v[80:83], a[136:139], a[40:43], v[80:83]\n" \
"v_exp_f32_e32 v69, v69\n" \
"v_mfma_f32_16x16x32_bf16 v[80:83], a[140:143], a[44:47], v[80:83]\n" \
"v_exp_f32_e32 v72, v72\n" \
"v_mfma_f32_16x16x32_bf16 v[84:87], a[156:159], a[44:47], v[40:43]\n" \
"v_exp_f32_e32 v73, v73\n" \
"v_mfma_f32_16x16x32_bf16 v[84:87], a[152:155], a[40:43], v[84:87]\n" \
"v_exp_f32_e32 v74, v74\n" \
"v_mfma_f32_16x16x32_bf16 v[84:87], a[148:151], a[36:39], v[84:87]\n" \
"v_exp_f32_e32 v75, v75\n" \
"v_mfma_f32_16x16x32_bf16 v[84:87], a[144:147], a[32:35], v[84:87]\n" \
"v_exp_f32_e32 v66, v66\n" \
"v_mfma_f32_16x16x32_bf16 v[92:95], a[144:147], a[48:51], v[44:47]\n" \
"ds_read_b128 a[144:147], v48 offset:12288\n" \
"v_cvt_pk_bf16_f32 v72, v72, v73\n" \
"v_mfma_f32_16x16x32_bf16 v[92:95], a[148:151], a[52:55], v[92:95]\n" \
"ds_read_b128 a[148:151], v48 offset:13312\n" \
"v_cvt_pk_bf16_f32 v70, v68, v69\n" \
"v_mfma_f32_16x16x32_bf16 v[92:95], a[152:155], a[56:59], v[92:95]\n" \
"ds_read_b128 a[152:155], v48 offset:14336\n" \
"v_cvt_pk_bf16_f32 v73, v74, v75\n" \
"v_mfma_f32_16x16x32_bf16 v[92:95], a[156:159], a[60:63], v[92:95]\n" \
"ds_read_b128 a[156:159], v48 offset:15360\n" \
"v_add_u32_e32 v49, 0x10000, v48\n" \
"v_mfma_f32_16x16x32_bf16 v[88:91], a[140:143], a[60:63], v[44:47]\n" \
"ds_read_b128 a[140:143], v48 offset:11264\n" \
"s_xor_b32 s65, s65, 0x4000\n" \
"v_mfma_f32_16x16x32_bf16 v[88:91], a[136:139], a[56:59], v[88:91]\n" \
"v_exp_f32_e32 v67, v67\n" \
"v_mfma_f32_16x16x32_bf16 v[88:91], a[132:135], a[52:55], v[88:91]\n" \
"buffer_load_dwordx4 v53, s[8:11], s64 offen offset:0 sc1 lds\n" \
"v_mfma_f32_16x16x32_bf16 v[88:91], a[128:131], a[48:51], v[88:91]\n" \
"ds_read_b128 a[128:131], v48 offset:8192\n" \
"s_waitcnt lgkmcnt(6)\n" \
"v_mfma_f32_16x16x32_bf16 v[128:131], a[160:163], v[100:103], v[128:131]\n" \
"ds_read_b128 a[132:135], v48 offset:9216\n" \
"ds_read_b128 a[136:139], v48 offset:10240\n" \
"v_mfma_f32_16x16x32_bf16 v[160:163], a[160:163], v[104:107], v[160:163]\n" \
"v_exp_f32_e32 v64, v64\n" \
"v_mfma_f32_16x16x32_bf16 v[132:135], a[164:167], v[100:103], v[132:135]\n" \
"v_exp_f32_e32 v65, v65\n" \
"v_mfma_f32_16x16x32_bf16 v[164:167], a[164:167], v[104:107], v[164:167]\n" \
"v_exp_f32_e32 v76, v76\n" \
"v_mfma_f32_16x16x32_bf16 v[136:139], a[168:171], v[100:103], v[136:139]\n" \
"v_exp_f32_e32 v77, v77\n" \
"v_mfma_f32_16x16x32_bf16 v[168:171], a[168:171], v[104:107], v[168:171]\n" \
"v_exp_f32_e32 v78, v78\n" \
"v_mfma_f32_16x16x32_bf16 v[140:143], a[172:175], v[100:103], v[140:143]\n" \
"v_exp_f32_e32 v79, v79\n" \
"v_mfma_f32_16x16x32_bf16 v[172:175], a[172:175], v[104:107], v[172:175]\n" \
"v_cvt_pk_bf16_f32 v69, v66, v67\n" \
"v_cvt_pk_bf16_f32 v68, v64, v65\n" \
"v_mfma_f32_16x16x32_bf16 v[144:147], a[176:179], v[100:103], v[144:147]\n" \
"v_cvt_pk_bf16_f32 v74, v76, v77\n" \
"v_cvt_pk_bf16_f32 v75, v78, v79\n" \
"v_mfma_f32_16x16x32_bf16 v[176:179], a[176:179], v[104:107], v[176:179]\n" \
"v_xor_b32_e32 v48, 0x4000, v48\n" \
"v_mfma_f32_16x16x32_bf16 v[148:151], a[180:183], v[100:103], v[148:151]\n" \
"v_mfma_f32_16x16x32_bf16 v[180:183], a[180:183], v[104:107], v[180:183]\n" \
"v_mfma_f32_16x16x32_bf16 v[152:155], a[184:187], v[100:103], v[152:155]\n" \
"v_mfma_f32_16x16x32_bf16 v[184:187], a[184:187], v[104:107], v[184:187]\n" \
"buffer_load_dwordx4 v53, s[8:11], s64 offen offset:1024 sc1 lds\n" \
"v_mfma_f32_16x16x32_bf16 v[156:159], a[188:191], v[100:103], v[156:159]\n" \
"s_add_u32 s64, s64, 0x8000\n" \
"s_sub_u32 s66, s66, 1\n" \
"v_mfma_f32_16x16x32_bf16 v[188:191], a[188:191], v[104:107], v[188:191]\n" \
"v_smfmac_f32_16x16x64_bf16 a[192:195], a[200:203], v[100:107], v21\n" \
"v_mfma_f32_16x16x32_bf16 v[220:223], a[188:191], v[116:119], v[220:223]\n" \
"v_mfma_f32_16x16x32_bf16 v[252:255], a[188:191], v[120:123], v[252:255]\n" \
"v_mfma_f32_16x16x32_bf16 v[216:219], a[184:187], v[116:119], v[216:219]\n" \
"v_mfma_f32_16x16x32_bf16 v[248:251], a[184:187], v[120:123], v[248:251]\n" \
"v_mfma_f32_16x16x32_bf16 v[212:215], a[180:183], v[116:119], v[212:215]\n" \
"v_mfma_f32_16x16x32_bf16 v[244:247], a[180:183], v[120:123], v[244:247]\n" \
"v_mfma_f32_16x16x32_bf16 v[208:211], a[176:179], v[116:119], v[208:211]\n" \
"v_mfma_f32_16x16x32_bf16 v[240:243], a[176:179], v[120:123], v[240:243]\n" \
"v_mfma_f32_16x16x32_bf16 v[204:207], a[172:175], v[116:119], v[204:207]\n" \
"v_mfma_f32_16x16x32_bf16 v[236:239], a[172:175], v[120:123], v[236:239]\n" \
"v_mfma_f32_16x16x32_bf16 v[200:203], a[168:171], v[116:119], v[200:203]\n" \
"v_mfma_f32_16x16x32_bf16 v[232:235], a[168:171], v[120:123], v[232:235]\n" \
"v_mfma_f32_16x16x32_bf16 v[196:199], a[164:167], v[116:119], v[196:199]\n" \
"v_mfma_f32_16x16x32_bf16 v[228:231], a[164:167], v[120:123], v[228:231]\n" \
"v_mfma_f32_16x16x32_bf16 v[192:195], a[160:163], v[116:119], v[192:195]\n" \
"v_mfma_f32_16x16x32_bf16 v[224:227], a[160:163], v[120:123], v[224:227]\n" \
"v_smfmac_f32_16x16x64_bf16 a[196:199], a[200:203], v[116:123], v21\n" \
"s_waitcnt vmcnt(4) lgkmcnt(0)\n" \
"s_barrier\n" \
"s_cbranch_scc0 L_1_%=\n" \
"L_tr_%=:\n" \
"s_cmp_lg_u32 s16, 0\n" \
"s_cbranch_scc1 L_sw_%=\n" \
"v_mfma_f32_16x16x32_bf16 v[96:99], a[128:131], a[0:3], v[32:35]\n" \
"v_mov_b32_e32 v56, s62\n" \
"v_readlane_b32 s68, v11, 0\n" \
"v_mfma_f32_16x16x32_bf16 v[96:99], a[132:135], a[4:7], v[96:99]\n" \
"s_xor_b32 m0, s65, 0x12000\n" \
"s_mov_b32 s65, s13\n" \
"v_mfma_f32_16x16x32_bf16 v[96:99], a[136:139], a[8:11], v[96:99]\n" \
"buffer_load_dwordx4 v50, s[8:11], s64 offen offset:0 sc1 lds\n" \
"v_mfma_f32_16x16x32_bf16 v[96:99], a[140:143], a[12:15], v[96:99]\n" \
"v_add_u32_e32 v63, s15, v20\n" \
"s_add_u32 s67, s14, 40960\n" \
"v_mfma_f32_16x16x32_bf16 v[100:103], a[156:159], a[12:15], v[32:35]\n" \
"buffer_load_dwordx4 v50, s[8:11], s64 offen offset:1024 sc1 lds\n" \
"s_mov_b64 exec, 1\n" \
"buffer_store_dword v56, off, s[56:59], s68 offset:0 sc1\n" \
"s_mov_b64 exec, -1\n" \
"v_mfma_f32_16x16x32_bf16 v[100:103], a[152:155], a[8:11], v[100:103]\n" \
"v_add_u32_e32 v50, 0x6000, v63\n" \
"s_xor_b32 m0, s65, 0x8000\n" \
"v_mfma_f32_16x16x32_bf16 v[100:103], a[148:151], a[4:7], v[100:103]\n" \
"ds_read_b128 a[160:163], v49 offset:0\n" \
"ds_read_b128 a[164:167], v49 offset:1024\n" \
"v_mfma_f32_16x16x32_bf16 v[100:103], a[144:147], a[0:3], v[100:103]\n" \
"ds_read_b128 a[168:171], v49 offset:2048\n" \
"ds_read_b128 a[172:175], v49 offset:3072\n" \
"v_mfma_f32_16x16x32_bf16 v[108:111], a[144:147], a[16:19], v[36:39]\n" \
"buffer_load_dwordx4 v50, s[8:11], s67 offen offset:0 sc1 lds\n" \
"v_mfma_f32_16x16x32_bf16 v[108:111], a[148:151], a[20:23], v[108:111]\n" \
"ds_read_b128 a[176:179], v49 offset:4096\n" \
"ds_read_b128 a[180:183], v49 offset:5120\n" \
"v_mfma_f32_16x16x32_bf16 v[108:111], a[152:155], a[24:27], v[108:111]\n" \
"ds_read_b128 a[184:187], v49 offset:6144\n" \
"ds_read_b128 a[188:191], v49 offset:7168\n" \
"v_mfma_f32_16x16x32_bf16 v[108:111], a[156:159], a[28:31], v[108:111]\n" \
"s_mov_b32 s64, s14\n" \
"v_add_u32_e32 v51, 0x12000, v63\n" \
"v_mfma_f32_16x16x32_bf16 v[104:107], a[140:143], a[28:31], v[36:39]\n" \
"buffer_load_dwordx4 v50, s[8:11], s67 offen offset:1024 sc1 lds\n" \
"v_mfma_f32_16x16x32_bf16 v[104:107], a[136:139], a[24:27], v[104:107]\n" \
"v_exp_f32_e32 v86, v86\n" \
"v_mfma_f32_16x16x32_bf16 v[104:107], a[132:135], a[20:23], v[104:107]\n" \
"v_exp_f32_e32 v87, v87\n" \
"v_mfma_f32_16x16x32_bf16 v[104:107], a[128:131], a[16:19], v[104:107]\n" \
"v_exp_f32_e32 v84, v84\n" \
"v_mfma_f32_16x16x32_bf16 v[112:115], a[128:131], a[32:35], v[40:43]\n" \
"v_exp_f32_e32 v85, v85\n" \
"v_mfma_f32_16x16x32_bf16 v[112:115], a[132:135], a[36:39], v[112:115]\n" \
"buffer_load_dwordx4 v50, s[8:11], s67 offen offset:2048 sc1 lds\n" \
"v_mfma_f32_16x16x32_bf16 v[112:115], a[136:139], a[40:43], v[112:115]\n" \
"v_cvt_pk_bf16_f32 v87, v86, v87\n" \
"v_cvt_pk_bf16_f32 v86, v84, v85\n" \
"v_mfma_f32_16x16x32_bf16 v[112:115], a[140:143], a[44:47], v[112:115]\n" \
"v_exp_f32_e32 v82, v82\n" \
"v_mfma_f32_16x16x32_bf16 v[116:119], a[156:159], a[44:47], v[40:43]\n" \
"v_exp_f32_e32 v83, v83\n" \
"v_mfma_f32_16x16x32_bf16 v[116:119], a[152:155], a[40:43], v[116:119]\n" \
"buffer_load_dwordx4 v50, s[8:11], s67 offen offset:3072 sc1 lds\n" \
"v_mfma_f32_16x16x32_bf16 v[116:119], a[148:151], a[36:39], v[116:119]\n" \
"s_xor_b32 m0, s65, 0xa000\n" \
"v_cvt_pk_bf16_f32 v85, v82, v83\n" \
"v_mfma_f32_16x16x32_bf16 v[116:119], a[144:147], a[32:35], v[116:119]\n" \
"v_exp_f32_e32 v80, v80\n" \
"v_mfma_f32_16x16x32_bf16 v[124:127], a[144:147], a[48:51], v[44:47]\n" \
"v_exp_f32_e32 v81, v81\n" \
"v_mfma_f32_16x16x32_bf16 v[124:127], a[148:151], a[52:55], v[124:127]\n" \
"buffer_load_dwordx4 v51, s[8:11], s64 offen offset:0 sc1 lds\n" \
"v_mfma_f32_16x16x32_bf16 v[124:127], a[152:155], a[56:59], v[124:127]\n" \
"v_cvt_pk_bf16_f32 v84, v80, v81\n" \
"v_readlane_b32 s93, v11, 19\n" \
"v_mfma_f32_16x16x32_bf16 v[124:127], a[156:159], a[60:63], v[124:127]\n" \
"v_exp_f32_e32 v88, v88\n" \
"v_mfma_f32_16x16x32_bf16 v[120:123], a[140:143], a[60:63], v[44:47]\n" \
"v_exp_f32_e32 v89, v89\n" \
"v_mfma_f32_16x16x32_bf16 v[120:123], a[136:139], a[56:59], v[120:123]\n" \
"v_exp_f32_e32 v90, v90\n" \
"v_mfma_f32_16x16x32_bf16 v[120:123], a[132:135], a[52:55], v[120:123]\n" \
"buffer_load_dwordx4 v51, s[8:11], s64 offen offset:1024 sc1 lds\n" \
"v_mfma_f32_16x16x32_bf16 v[120:123], a[128:131], a[48:51], v[120:123]\n" \
"v_cvt_pk_bf16_f32 v88, v88, v89\n" \
"s_waitcnt lgkmcnt(0)\n" \
"v_mfma_f32_16x16x32_bf16 v[128:131], a[160:163], v[68:71], v[128:131]\n" \
"v_exp_f32_e32 v91, v91\n" \
"v_mfma_f32_16x16x32_bf16 v[160:163], a[160:163], v[72:75], v[160:163]\n" \
"v_exp_f32_e32 v92, v92\n" \
"v_mfma_f32_16x16x32_bf16 v[132:135], a[164:167], v[68:71], v[132:135]\n" \
"buffer_load_dwordx4 v51, s[8:11], s64 offen offset:2048 sc1 lds\n" \
"v_mfma_f32_16x16x32_bf16 v[164:167], a[164:167], v[72:75], v[164:167]\n" \
"v_exp_f32_e32 v93, v93\n" \
"v_mfma_f32_16x16x32_bf16 v[136:139], a[168:171], v[68:71], v[136:139]\n" \
"v_exp_f32_e32 v94, v94\n" \
"v_mfma_f32_16x16x32_bf16 v[168:171], a[168:171], v[72:75], v[168:171]\n" \
"v_exp_f32_e32 v95, v95\n" \
"v_mfma_f32_16x16x32_bf16 v[140:143], a[172:175], v[68:71], v[140:143]\n" \
"buffer_load_dwordx4 v51, s[8:11], s64 offen offset:3072 sc1 lds\n" \
"v_mfma_f32_16x16x32_bf16 v[172:175], a[172:175], v[72:75], v[172:175]\n" \
"v_cvt_pk_bf16_f32 v89, v90, v91\n" \
"v_cvt_pk_bf16_f32 v90, v92, v93\n" \
"v_mfma_f32_16x16x32_bf16 v[144:147], a[176:179], v[68:71], v[144:147]\n" \
"v_cvt_pk_bf16_f32 v91, v94, v95\n" \
"v_readlane_b32 s80, v11, 11\n" \
"v_mfma_f32_16x16x32_bf16 v[176:179], a[176:179], v[72:75], v[176:179]\n" \
"v_readlane_b32 s81, v11, 12\n" \
"s_mov_b32 s82, 0x47fca0\n" \
"v_mfma_f32_16x16x32_bf16 v[148:151], a[180:183], v[68:71], v[148:151]\n" \
"s_mov_b32 s83, 0x20000\n" \
"v_readlane_b32 s92, v11, 24\n" \
"v_mfma_f32_16x16x32_bf16 v[180:183], a[180:183], v[72:75], v[180:183]\n" \
"s_add_u32 m0, s93, 0x0\n" \
"ds_read_b128 a[128:131], v48 offset:0\n" \
"v_mfma_f32_16x16x32_bf16 v[152:155], a[184:187], v[68:71], v[152:155]\n" \
"ds_read_b128 a[144:147], v48 offset:4096\n" \
"ds_read_b128 a[132:135], v48 offset:1024\n" \
"v_mfma_f32_16x16x32_bf16 v[184:187], a[184:187], v[72:75], v[184:187]\n" \
"buffer_load_dwordx4 v0, s[80:83], s92 offen offset:0 lds\n" \
"v_mfma_f32_16x16x32_bf16 v[156:159], a[188:191], v[68:71], v[156:159]\n" \
"ds_read_b128 a[148:151], v48 offset:5120\n" \
"ds_read_b128 a[136:139], v48 offset:2048\n" \
"v_mfma_f32_16x16x32_bf16 v[188:191], a[188:191], v[72:75], v[188:191]\n" \
"ds_read_b128 a[152:155], v48 offset:6144\n" \
"ds_read_b128 a[140:143], v48 offset:3072\n" \
"v_smfmac_f32_16x16x64_bf16 a[192:195], a[200:203], v[68:75], v21\n" \
"ds_read_b128 a[156:159], v48 offset:7168\n" \
"v_add_u32_e32 v52, 0x14000, v63\n" \
"v_mfma_f32_16x16x32_bf16 v[220:223], a[188:191], v[84:87], v[220:223]\n" \
"buffer_load_dwordx4 v1, s[80:83], s92 offen offset:1024 lds\n" \
"v_mfma_f32_16x16x32_bf16 v[252:255], a[188:191], v[88:91], v[252:255]\n" \
"v_add_u32_e32 v53, 0x20000, v63\n" \
"s_mov_b32 s66, s19\n" \
"v_mfma_f32_16x16x32_bf16 v[216:219], a[184:187], v[84:87], v[216:219]\n" \
"buffer_load_dwordx4 v2, s[80:83], s92 offen offset:2048 lds\n" \
"v_mfma_f32_16x16x32_bf16 v[248:251], a[184:187], v[88:91], v[248:251]\n" \
"v_exp_f32_e32 v102, v102\n" \
"v_mfma_f32_16x16x32_bf16 v[212:215], a[180:183], v[84:87], v[212:215]\n" \
"v_exp_f32_e32 v103, v103\n" \
"v_mfma_f32_16x16x32_bf16 v[244:247], a[180:183], v[88:91], v[244:247]\n" \
"v_exp_f32_e32 v100, v100\n" \
"v_mfma_f32_16x16x32_bf16 v[208:211], a[176:179], v[84:87], v[208:211]\n" \
"buffer_load_dwordx4 v3, s[80:83], s92 offen offset:3072 lds\n" \
"v_mfma_f32_16x16x32_bf16 v[240:243], a[176:179], v[88:91], v[240:243]\n" \
"v_exp_f32_e32 v101, v101\n" \
"v_mfma_f32_16x16x32_bf16 v[204:207], a[172:175], v[84:87], v[204:207]\n" \
"v_exp_f32_e32 v98, v98\n" \
"v_mfma_f32_16x16x32_bf16 v[236:239], a[172:175], v[88:91], v[236:239]\n" \
"v_exp_f32_e32 v99, v99\n" \
"v_mfma_f32_16x16x32_bf16 v[200:203], a[168:171], v[84:87], v[200:203]\n" \
"v_exp_f32_e32 v96, v96\n" \
"v_mfma_f32_16x16x32_bf16 v[232:235], a[168:171], v[88:91], v[232:235]\n" \
"v_exp_f32_e32 v97, v97\n" \
"v_mfma_f32_16x16x32_bf16 v[196:199], a[164:167], v[84:87], v[196:199]\n" \
"v_exp_f32_e32 v104, v104\n" \
"v_mfma_f32_16x16x32_bf16 v[228:231], a[164:167], v[88:91], v[228:231]\n" \
"v_exp_f32_e32 v105, v105\n" \
"v_mfma_f32_16x16x32_bf16 v[192:195], a[160:163], v[84:87], v[192:195]\n" \
"v_cvt_pk_bf16_f32 v103, v102, v103\n" \
"v_cvt_pk_bf16_f32 v102, v100, v101\n" \
"v_mfma_f32_16x16x32_bf16 v[224:227], a[160:163], v[88:91], v[224:227]\n" \
"v_cvt_pk_bf16_f32 v101, v98, v99\n" \
"v_cvt_pk_bf16_f32 v100, v96, v97\n" \
"v_smfmac_f32_16x16x64_bf16 a[196:199], a[200:203], v[84:91], v21\n" \
"v_exp_f32_e32 v106, v106\n" \
"s_waitcnt vmcnt(15) lgkmcnt(0)\n" \
"s_barrier\n" \
"v_mfma_f32_16x16x32_bf16 v[64:67], a[128:131], a[0:3], v[32:35]\n" \
"s_xor_b32 m0, s65, 0x18000\n" \
"ds_read_b128 a[160:163], v49 offset:8192\n" \
"v_mfma_f32_16x16x32_bf16 v[64:67], a[132:135], a[4:7], v[64:67]\n" \
"buffer_load_dwordx4 v52, s[8:11], s64 offen offset:0 sc1 lds\n" \
"v_mfma_f32_16x16x32_bf16 v[64:67], a[136:139], a[8:11], v[64:67]\n" \
"ds_read_b128 a[164:167], v49 offset:9216\n" \
"ds_read_b128 a[168:171], v49 offset:10240\n" \
"v_mfma_f32_16x16x32_bf16 v[64:67], a[140:143], a[12:15], v[64:67]\n" \
"ds_read_b128 a[172:175], v49 offset:11264\n" \
"ds_read_b128 a[176:179], v49 offset:12288\n" \
"v_mfma_f32_16x16x32_bf16 v[68:71], a[156:159], a[12:15], v[32:35]\n" \
"buffer_load_dwordx4 v52, s[8:11], s64 offen offset:1024 sc1 lds\n" \
"v_mfma_f32_16x16x32_bf16 v[68:71], a[152:155], a[8:11], v[68:71]\n" \
"ds_read_b128 a[180:183], v49 offset:13312\n" \
"ds_read_b128 a[184:187], v49 offset:14336\n" \
"v_mfma_f32_16x16x32_bf16 v[68:71], a[148:151], a[4:7], v[68:71]\n" \
"v_exp_f32_e32 v107, v107\n" \
"v_mfma_f32_16x16x32_bf16 v[68:71], a[144:147], a[0:3], v[68:71]\n" \
"v_cvt_pk_bf16_f32 v104, v104, v105\n" \
"v_cvt_pk_bf16_f32 v105, v106, v107\n" \
"v_mfma_f32_16x16x32_bf16 v[76:79], a[144:147], a[16:19], v[36:39]\n" \
"v_exp_f32_e32 v108, v108\n" \
"v_mfma_f32_16x16x32_bf16 v[76:79], a[148:151], a[20:23], v[76:79]\n" \
"buffer_load_dwordx4 v52, s[8:11], s64 offen offset:2048 sc1 lds\n" \
"v_mfma_f32_16x16x32_bf16 v[76:79], a[152:155], a[24:27], v[76:79]\n" \
"v_exp_f32_e32 v109, v109\n" \
"v_mfma_f32_16x16x32_bf16 v[76:79], a[156:159], a[28:31], v[76:79]\n" \
"v_exp_f32_e32 v110, v110\n" \
"v_mfma_f32_16x16x32_bf16 v[72:75], a[140:143], a[28:31], v[36:39]\n" \
"v_exp_f32_e32 v111, v111\n" \
"v_mfma_f32_16x16x32_bf16 v[72:75], a[136:139], a[24:27], v[72:75]\n" \
"buffer_load_dwordx4 v52, s[8:11], s64 offen offset:3072 sc1 lds\n" \
"v_mfma_f32_16x16x32_bf16 v[72:75], a[132:135], a[20:23], v[72:75]\n" \
"s_mov_b32 m0, s65\n" \
"v_cvt_pk_bf16_f32 v106, v108, v109\n" \
"v_mfma_f32_16x16x32_bf16 v[72:75], a[128:131], a[16:19], v[72:75]\n" \
"v_cvt_pk_bf16_f32 v107, v110, v111\n" \
"ds_read_b128 a[188:191], v49 offset:15360\n" \
"v_mfma_f32_16x16x32_bf16 v[80:83], a[128:131], a[32:35], v[40:43]\n" \
"v_exp_f32_e32 v118, v118\n" \
"v_mfma_f32_16x16x32_bf16 v[80:83], a[132:135], a[36:39], v[80:83]\n" \
"v_exp_f32_e32 v119, v119\n" \
"v_mfma_f32_16x16x32_bf16 v[80:83], a[136:139], a[40:43], v[80:83]\n" \
"buffer_load_dwordx4 v53, s[8:11], s64 offen offset:0 sc1 lds\n" \
"v_mfma_f32_16x16x32_bf16 v[80:83], a[140:143], a[44:47], v[80:83]\n" \
"v_exp_f32_e32 v116, v116\n" \
"v_mfma_f32_16x16x32_bf16 v[84:87], a[156:159], a[44:47], v[40:43]\n" \
"v_exp_f32_e32 v117, v117\n" \
"v_mfma_f32_16x16x32_bf16 v[84:87], a[152:155], a[40:43], v[84:87]\n" \
"v_cvt_pk_bf16_f32 v119, v118, v119\n" \
"v_cvt_pk_bf16_f32 v118, v116, v117\n" \
"v_mfma_f32_16x16x32_bf16 v[84:87], a[148:151], a[36:39], v[84:87]\n" \
"v_exp_f32_e32 v114, v114\n" \
"v_mfma_f32_16x16x32_bf16 v[84:87], a[144:147], a[32:35], v[84:87]\n" \
"buffer_load_dwordx4 v53, s[8:11], s64 offen offset:1024 sc1 lds\n" \
"v_mfma_f32_16x16x32_bf16 v[92:95], a[144:147], a[48:51], v[44:47]\n" \
"v_exp_f32_e32 v115, v115\n" \
"v_mfma_f32_16x16x32_bf16 v[92:95], a[148:151], a[52:55], v[92:95]\n" \
"v_exp_f32_e32 v112, v112\n" \
"v_mfma_f32_16x16x32_bf16 v[92:95], a[152:155], a[56:59], v[92:95]\n" \
"v_exp_f32_e32 v113, v113\n" \
"v_mfma_f32_16x16x32_bf16 v[92:95], a[156:159], a[60:63], v[92:95]\n" \
"buffer_load_dwordx4 v53, s[8:11], s64 offen offset:2048 sc1 lds\n" \
"v_mfma_f32_16x16x32_bf16 v[88:91], a[140:143], a[60:63], v[44:47]\n" \
"v_cvt_pk_bf16_f32 v117, v114, v115\n" \
"v_cvt_pk_bf16_f32 v116, v112, v113\n" \
"v_mfma_f32_16x16x32_bf16 v[88:91], a[136:139], a[56:59], v[88:91]\n" \
"v_exp_f32_e32 v120, v120\n" \
"v_mfma_f32_16x16x32_bf16 v[88:91], a[132:135], a[52:55], v[88:91]\n" \
"v_exp_f32_e32 v121, v121\n" \
"v_mfma_f32_16x16x32_bf16 v[88:91], a[128:131], a[48:51], v[88:91]\n" \
"v_cvt_pk_bf16_f32 v120, v120, v121\n" \
"s_waitcnt lgkmcnt(0)\n" \
"v_mfma_f32_16x16x32_bf16 v[128:131], a[160:163], v[100:103], v[128:131]\n" \
"buffer_load_dwordx4 v53, s[8:11], s64 offen offset:3072 sc1 lds\n" \
"v_mfma_f32_16x16x32_bf16 v[160:163], a[160:163], v[104:107], v[160:163]\n" \
"s_add_u32 m0, s93, 0x1000\n" \
"ds_read_b128 a[128:131], v48 offset:8192\n" \
"v_mfma_f32_16x16x32_bf16 v[132:135], a[164:167], v[100:103], v[132:135]\n" \
"v_exp_f32_e32 v122, v122\n" \
"v_mfma_f32_16x16x32_bf16 v[164:167], a[164:167], v[104:107], v[164:167]\n" \
"v_exp_f32_e32 v123, v123\n" \
"v_mfma_f32_16x16x32_bf16 v[136:139], a[168:171], v[100:103], v[136:139]\n" \
"buffer_load_dwordx4 v4, s[80:83], s92 offen offset:0 lds\n" \
"v_mfma_f32_16x16x32_bf16 v[168:171], a[168:171], v[104:107], v[168:171]\n" \
"v_exp_f32_e32 v124, v124\n" \
"v_mfma_f32_16x16x32_bf16 v[140:143], a[172:175], v[100:103], v[140:143]\n" \
"v_exp_f32_e32 v125, v125\n" \
"v_mfma_f32_16x16x32_bf16 v[172:175], a[172:175], v[104:107], v[172:175]\n" \
"v_exp_f32_e32 v126, v126\n" \
"v_mfma_f32_16x16x32_bf16 v[144:147], a[176:179], v[100:103], v[144:147]\n" \
"v_exp_f32_e32 v127, v127\n" \
"v_mfma_f32_16x16x32_bf16 v[176:179], a[176:179], v[104:107], v[176:179]\n" \
"v_cvt_pk_bf16_f32 v121, v122, v123\n" \
"v_cvt_pk_bf16_f32 v122, v124, v125\n" \
"v_mfma_f32_16x16x32_bf16 v[148:151], a[180:183], v[100:103], v[148:151]\n" \
"buffer_load_dwordx4 v5, s[80:83], s92 offen offset:1024 lds\n" \
"v_mfma_f32_16x16x32_bf16 v[180:183], a[180:183], v[104:107], v[180:183]\n" \
"v_cvt_pk_bf16_f32 v123, v126, v127\n" \
"ds_read_b128 a[144:147], v48 offset:12288\n" \
"v_mfma_f32_16x16x32_bf16 v[152:155], a[184:187], v[100:103], v[152:155]\n" \
"ds_read_b128 a[132:135], v48 offset:9216\n" \
"ds_read_b128 a[148:151], v48 offset:13312\n" \
"v_mfma_f32_16x16x32_bf16 v[184:187], a[184:187], v[104:107], v[184:187]\n" \
"ds_read_b128 a[136:139], v48 offset:10240\n" \
"ds_read_b128 a[152:155], v48 offset:14336\n" \
"v_mfma_f32_16x16x32_bf16 v[156:159], a[188:191], v[100:103], v[156:159]\n" \
"ds_read_b128 a[140:143], v48 offset:11264\n" \
"ds_read_b128 a[156:159], v48 offset:15360\n" \
"v_mfma_f32_16x16x32_bf16 v[188:191], a[188:191], v[104:107], v[188:191]\n" \
"buffer_load_dwordx4 v6, s[80:83], s92 offen offset:2048 lds\n" \
"v_smfmac_f32_16x16x64_bf16 a[192:195], a[200:203], v[100:107], v21\n" \
"v_exp_f32_e32 v70, v70\n" \
"v_mfma_f32_16x16x32_bf16 v[220:223], a[188:191], v[116:119], v[220:223]\n" \
"v_exp_f32_e32 v71, v71\n" \
"v_mfma_f32_16x16x32_bf16 v[252:255], a[188:191], v[120:123], v[252:255]\n" \
"buffer_load_dwordx4 v7, s[80:83], s92 offen offset:3072 lds\n" \
"v_mfma_f32_16x16x32_bf16 v[216:219], a[184:187], v[116:119], v[216:219]\n" \
"s_add_u32 m0, s93, 0x2000\n" \
"v_cvt_pk_bf16_f32 v71, v70, v71\n" \
"v_mfma_f32_16x16x32_bf16 v[248:251], a[184:187], v[120:123], v[248:251]\n" \
"v_exp_f32_e32 v72, v72\n" \
"v_mfma_f32_16x16x32_bf16 v[212:215], a[180:183], v[116:119], v[212:215]\n" \
"v_exp_f32_e32 v73, v73\n" \
"v_mfma_f32_16x16x32_bf16 v[244:247], a[180:183], v[120:123], v[244:247]\n" \
"v_exp_f32_e32 v68, v68\n" \
"v_mfma_f32_16x16x32_bf16 v[208:211], a[176:179], v[116:119], v[208:211]\n" \
"buffer_load_dwordx4 v8, s[80:83], s92 offen offset:0 lds\n" \
"v_mfma_f32_16x16x32_bf16 v[240:243], a[176:179], v[120:123], v[240:243]\n" \
"v_exp_f32_e32 v69, v69\n" \
"v_mfma_f32_16x16x32_bf16 v[204:207], a[172:175], v[116:119], v[204:207]\n" \
"v_exp_f32_e32 v74, v74\n" \
"v_mfma_f32_16x16x32_bf16 v[236:239], a[172:175], v[120:123], v[236:239]\n" \
"v_exp_f32_e32 v75, v75\n" \
"v_mfma_f32_16x16x32_bf16 v[200:203], a[168:171], v[116:119], v[200:203]\n" \
"v_exp_f32_e32 v66, v66\n" \
"v_mfma_f32_16x16x32_bf16 v[232:235], a[168:171], v[120:123], v[232:235]\n" \
"v_exp_f32_e32 v67, v67\n" \
"v_mfma_f32_16x16x32_bf16 v[196:199], a[164:167], v[116:119], v[196:199]\n" \
"v_exp_f32_e32 v64, v64\n" \
"v_mfma_f32_16x16x32_bf16 v[228:231], a[164:167], v[120:123], v[228:231]\n" \
"v_exp_f32_e32 v65, v65\n" \
"v_mfma_f32_16x16x32_bf16 v[192:195], a[160:163], v[116:119], v[192:195]\n" \
"v_cvt_pk_bf16_f32 v72, v72, v73\n" \
"v_cvt_pk_bf16_f32 v70, v68, v69\n" \
"v_mfma_f32_16x16x32_bf16 v[224:227], a[160:163], v[120:123], v[224:227]\n" \
"v_exp_f32_e32 v76, v76\n" \
"v_smfmac_f32_16x16x64_bf16 a[196:199], a[200:203], v[116:123], v21\n" \
"v_exp_f32_e32 v77, v77\n" \
"s_waitcnt vmcnt(13) lgkmcnt(0)\n" \
"s_barrier\n" \
"v_exp_f32_e32 v78, v78\n" \
"v_exp_f32_e32 v79, v79\n" \
"v_cvt_pk_bf16_f32 v73, v74, v75\n" \
"v_add_u32_e32 v49, 0x10000, v48\n" \
"v_cvt_pk_bf16_f32 v69, v66, v67\n" \
"v_cvt_pk_bf16_f32 v68, v64, v65\n" \
"v_cvt_pk_bf16_f32 v74, v76, v77\n" \
"v_cvt_pk_bf16_f32 v75, v78, v79\n" \
"v_add_u32_e32 v48, s16, v20\n" \
"s_xor_b32 s65, s65, 0x8000\n" \
"s_add_u32 s64, s64, 0x10000\n" \
"L_2_%=:\n" \
"v_mfma_f32_16x16x32_bf16 v[96:99], a[128:131], a[64:67], v[32:35]\n" \
"s_xor_b32 m0, s65, 0x12000\n" \
"ds_read_b128 a[160:163], v49 offset:0\n" \
"v_mfma_f32_16x16x32_bf16 v[96:99], a[132:135], a[68:71], v[96:99]\n" \
"buffer_load_dwordx4 v50, s[8:11], s64 offen offset:0 sc1 lds\n" \
"v_mfma_f32_16x16x32_bf16 v[96:99], a[136:139], a[72:75], v[96:99]\n" \
"ds_read_b128 a[164:167], v49 offset:1024\n" \
"ds_read_b128 a[168:171], v49 offset:2048\n" \
"v_mfma_f32_16x16x32_bf16 v[96:99], a[140:143], a[76:79], v[96:99]\n" \
"ds_read_b128 a[172:175], v49 offset:3072\n" \
"ds_read_b128 a[176:179], v49 offset:4096\n" \
"v_mfma_f32_16x16x32_bf16 v[100:103], a[156:159], a[76:79], v[32:35]\n" \
"ds_read_b128 a[180:183], v49 offset:5120\n" \
"ds_read_b128 a[184:187], v49 offset:6144\n" \
"v_mfma_f32_16x16x32_bf16 v[100:103], a[152:155], a[72:75], v[100:103]\n" \
"v_exp_f32_e32 v86, v86\n" \
"v_mfma_f32_16x16x32_bf16 v[100:103], a[148:151], a[68:71], v[100:103]\n" \
"v_exp_f32_e32 v87, v87\n" \
"v_mfma_f32_16x16x32_bf16 v[100:103], a[144:147], a[64:67], v[100:103]\n" \
"buffer_load_dwordx4 v50, s[8:11], s64 offen offset:1024 sc1 lds\n" \
"v_mfma_f32_16x16x32_bf16 v[108:111], a[144:147], a[80:83], v[36:39]\n" \
"ds_read_b128 a[188:191], v49 offset:7168\n" \
"v_cvt_pk_bf16_f32 v87, v86, v87\n" \
"v_mfma_f32_16x16x32_bf16 v[108:111], a[148:151], a[84:87], v[108:111]\n" \
"v_exp_f32_e32 v84, v84\n" \
"v_mfma_f32_16x16x32_bf16 v[108:111], a[152:155], a[88:91], v[108:111]\n" \
"v_exp_f32_e32 v85, v85\n" \
"v_mfma_f32_16x16x32_bf16 v[108:111], a[156:159], a[92:95], v[108:111]\n" \
"v_exp_f32_e32 v82, v82\n" \
"v_mfma_f32_16x16x32_bf16 v[104:107], a[140:143], a[92:95], v[36:39]\n" \
"v_exp_f32_e32 v83, v83\n" \
"v_mfma_f32_16x16x32_bf16 v[104:107], a[136:139], a[88:91], v[104:107]\n" \
"v_exp_f32_e32 v80, v80\n" \
"v_mfma_f32_16x16x32_bf16 v[104:107], a[132:135], a[84:87], v[104:107]\n" \
"v_exp_f32_e32 v81, v81\n" \
"v_mfma_f32_16x16x32_bf16 v[104:107], a[128:131], a[80:83], v[104:107]\n" \
"buffer_load_dwordx4 v50, s[8:11], s64 offen offset:2048 sc1 lds\n" \
"v_mfma_f32_16x16x32_bf16 v[112:115], a[128:131], a[96:99], v[40:43]\n" \
"v_exp_f32_e32 v88, v88\n" \
"v_mfma_f32_16x16x32_bf16 v[112:115], a[132:135], a[100:103], v[112:115]\n" \
"v_exp_f32_e32 v89, v89\n" \
"v_mfma_f32_16x16x32_bf16 v[112:115], a[136:139], a[104:107], v[112:115]\n" \
"v_cvt_pk_bf16_f32 v86, v84, v85\n" \
"v_cvt_pk_bf16_f32 v85, v82, v83\n" \
"v_mfma_f32_16x16x32_bf16 v[112:115], a[140:143], a[108:111], v[112:115]\n" \
"v_cvt_pk_bf16_f32 v84, v80, v81\n" \
"v_cvt_pk_bf16_f32 v88, v88, v89\n" \
"v_mfma_f32_16x16x32_bf16 v[116:119], a[156:159], a[108:111], v[40:43]\n" \
"v_exp_f32_e32 v90, v90\n" \
"v_mfma_f32_16x16x32_bf16 v[116:119], a[152:155], a[104:107], v[116:119]\n" \
"v_exp_f32_e32 v91, v91\n" \
"v_mfma_f32_16x16x32_bf16 v[116:119], a[148:151], a[100:103], v[116:119]\n" \
"buffer_load_dwordx4 v50, s[8:11], s64 offen offset:3072 sc1 lds\n" \
"v_mfma_f32_16x16x32_bf16 v[116:119], a[144:147], a[96:99], v[116:119]\n" \
"s_xor_b32 m0, s65, 0xa000\n" \
"v_cvt_pk_bf16_f32 v89, v90, v91\n" \
"v_mfma_f32_16x16x32_bf16 v[124:127], a[144:147], a[112:115], v[44:47]\n" \
"v_exp_f32_e32 v92, v92\n" \
"v_mfma_f32_16x16x32_bf16 v[124:127], a[148:151], a[116:119], v[124:127]\n" \
"v_exp_f32_e32 v93, v93\n" \
"v_mfma_f32_16x16x32_bf16 v[124:127], a[152:155], a[120:123], v[124:127]\n" \
"v_exp_f32_e32 v94, v94\n" \
"v_mfma_f32_16x16x32_bf16 v[124:127], a[156:159], a[124:127], v[124:127]\n" \
"v_exp_f32_e32 v95, v95\n" \
"v_mfma_f32_16x16x32_bf16 v[120:123], a[140:143], a[124:127], v[44:47]\n" \
"v_cvt_pk_bf16_f32 v90, v92, v93\n" \
"v_cvt_pk_bf16_f32 v91, v94, v95\n" \
"v_mfma_f32_16x16x32_bf16 v[120:123], a[136:139], a[120:123], v[120:123]\n" \
"ds_read_b128 a[144:147], v48 offset:4096\n" \
"ds_read_b128 a[148:151], v48 offset:5120\n" \
"v_mfma_f32_16x16x32_bf16 v[120:123], a[132:135], a[116:119], v[120:123]\n" \
"buffer_load_dwordx4 v51, s[8:11], s64 offen offset:0 sc1 lds\n" \
"v_mfma_f32_16x16x32_bf16 v[120:123], a[128:131], a[112:115], v[120:123]\n" \
"ds_read_b128 a[128:131], v48 offset:0\n" \
"s_waitcnt lgkmcnt(3)\n" \
"v_mfma_f32_16x16x32_bf16 v[128:131], a[160:163], v[68:71], v[128:131]\n" \
"ds_read_b128 a[132:135], v48 offset:1024\n" \
"ds_read_b128 a[136:139], v48 offset:2048\n" \
"v_mfma_f32_16x16x32_bf16 v[160:163], a[160:163], v[72:75], v[160:163]\n" \
"ds_read_b128 a[152:155], v48 offset:6144\n" \
"ds_read_b128 a[140:143], v48 offset:3072\n" \
"v_mfma_f32_16x16x32_bf16 v[132:135], a[164:167], v[68:71], v[132:135]\n" \
"v_exp_f32_e32 v102, v102\n" \
"v_mfma_f32_16x16x32_bf16 v[164:167], a[164:167], v[72:75], v[164:167]\n" \
"v_exp_f32_e32 v103, v103\n" \
"v_mfma_f32_16x16x32_bf16 v[136:139], a[168:171], v[68:71], v[136:139]\n" \
"ds_read_b128 a[156:159], v48 offset:7168\n" \
"v_cvt_pk_bf16_f32 v103, v102, v103\n" \
"v_mfma_f32_16x16x32_bf16 v[168:171], a[168:171], v[72:75], v[168:171]\n" \
"buffer_load_dwordx4 v51, s[8:11], s64 offen offset:1024 sc1 lds\n" \
"v_mfma_f32_16x16x32_bf16 v[140:143], a[172:175], v[68:71], v[140:143]\n" \
"v_exp_f32_e32 v100, v100\n" \
"v_mfma_f32_16x16x32_bf16 v[172:175], a[172:175], v[72:75], v[172:175]\n" \
"v_exp_f32_e32 v101, v101\n" \
"v_mfma_f32_16x16x32_bf16 v[144:147], a[176:179], v[68:71], v[144:147]\n" \
"v_exp_f32_e32 v98, v98\n" \
"v_mfma_f32_16x16x32_bf16 v[176:179], a[176:179], v[72:75], v[176:179]\n" \
"v_exp_f32_e32 v99, v99\n" \
"v_mfma_f32_16x16x32_bf16 v[148:151], a[180:183], v[68:71], v[148:151]\n" \
"v_exp_f32_e32 v96, v96\n" \
"v_mfma_f32_16x16x32_bf16 v[180:183], a[180:183], v[72:75], v[180:183]\n" \
"v_exp_f32_e32 v97, v97\n" \
"v_mfma_f32_16x16x32_bf16 v[152:155], a[184:187], v[68:71], v[152:155]\n" \
"v_exp_f32_e32 v104, v104\n" \
"v_mfma_f32_16x16x32_bf16 v[184:187], a[184:187], v[72:75], v[184:187]\n" \
"buffer_load_dwordx4 v51, s[8:11], s64 offen offset:2048 sc1 lds\n" \
"v_mfma_f32_16x16x32_bf16 v[156:159], a[188:191], v[68:71], v[156:159]\n" \
"v_exp_f32_e32 v105, v105\n" \
"v_mfma_f32_16x16x32_bf16 v[188:191], a[188:191], v[72:75], v[188:191]\n" \
"v_cvt_pk_bf16_f32 v102, v100, v101\n" \
"v_cvt_pk_bf16_f32 v101, v98, v99\n" \
"v_smfmac_f32_16x16x64_bf16 a[192:195], a[200:203], v[68:75], v21\n" \
"v_cvt_pk_bf16_f32 v100, v96, v97\n" \
"v_cvt_pk_bf16_f32 v104, v104, v105\n" \
"v_mfma_f32_16x16x32_bf16 v[220:223], a[188:191], v[84:87], v[220:223]\n" \
"v_exp_f32_e32 v106, v106\n" \
"v_mfma_f32_16x16x32_bf16 v[252:255], a[188:191], v[88:91], v[252:255]\n" \
"v_exp_f32_e32 v107, v107\n" \
"v_mfma_f32_16x16x32_bf16 v[216:219], a[184:187], v[84:87], v[216:219]\n" \
"v_exp_f32_e32 v108, v108\n" \
"v_mfma_f32_16x16x32_bf16 v[248:251], a[184:187], v[88:91], v[248:251]\n" \
"buffer_load_dwordx4 v51, s[8:11], s64 offen offset:3072 sc1 lds\n" \
"v_mfma_f32_16x16x32_bf16 v[212:215], a[180:183], v[84:87], v[212:215]\n" \
"v_exp_f32_e32 v109, v109\n" \
"v_mfma_f32_16x16x32_bf16 v[244:247], a[180:183], v[88:91], v[244:247]\n" \
"v_exp_f32_e32 v110, v110\n" \
"v_mfma_f32_16x16x32_bf16 v[208:211], a[176:179], v[84:87], v[208:211]\n" \
"v_exp_f32_e32 v111, v111\n" \
"v_mfma_f32_16x16x32_bf16 v[240:243], a[176:179], v[88:91], v[240:243]\n" \
"v_exp_f32_e32 v118, v118\n" \
"v_mfma_f32_16x16x32_bf16 v[204:207], a[172:175], v[84:87], v[204:207]\n" \
"v_exp_f32_e32 v119, v119\n" \
"v_mfma_f32_16x16x32_bf16 v[236:239], a[172:175], v[88:91], v[236:239]\n" \
"v_cvt_pk_bf16_f32 v105, v106, v107\n" \
"v_cvt_pk_bf16_f32 v106, v108, v109\n" \
"v_mfma_f32_16x16x32_bf16 v[200:203], a[168:171], v[84:87], v[200:203]\n" \
"v_cvt_pk_bf16_f32 v107, v110, v111\n" \
"v_cvt_pk_bf16_f32 v119, v118, v119\n" \
"v_mfma_f32_16x16x32_bf16 v[232:235], a[168:171], v[88:91], v[232:235]\n" \
"v_exp_f32_e32 v116, v116\n" \
"v_mfma_f32_16x16x32_bf16 v[196:199], a[164:167], v[84:87], v[196:199]\n" \
"v_exp_f32_e32 v117, v117\n" \
"v_mfma_f32_16x16x32_bf16 v[228:231], a[164:167], v[88:91], v[228:231]\n" \
"v_exp_f32_e32 v114, v114\n" \
"v_mfma_f32_16x16x32_bf16 v[192:195], a[160:163], v[84:87], v[192:195]\n" \
"v_exp_f32_e32 v115, v115\n" \
"v_mfma_f32_16x16x32_bf16 v[224:227], a[160:163], v[88:91], v[224:227]\n" \
"v_exp_f32_e32 v112, v112\n" \
"v_smfmac_f32_16x16x64_bf16 a[196:199], a[200:203], v[84:91], v21\n" \
"v_exp_f32_e32 v113, v113\n" \
"s_waitcnt vmcnt(8) lgkmcnt(0)\n" \
"s_barrier\n" \
"v_mfma_f32_16x16x32_bf16 v[64:67], a[128:131], a[64:67], v[32:35]\n" \
"s_xor_b32 m0, s65, 0x18000\n" \
"ds_read_b128 a[160:163], v49 offset:8192\n" \
"v_mfma_f32_16x16x32_bf16 v[64:67], a[132:135], a[68:71], v[64:67]\n" \
"buffer_load_dwordx4 v52, s[8:11], s64 offen offset:0 sc1 lds\n" \
"v_mfma_f32_16x16x32_bf16 v[64:67], a[136:139], a[72:75], v[64:67]\n" \
"ds_read_b128 a[164:167], v49 offset:9216\n" \
"ds_read_b128 a[168:171], v49 offset:10240\n" \
"v_mfma_f32_16x16x32_bf16 v[64:67], a[140:143], a[76:79], v[64:67]\n" \
"ds_read_b128 a[172:175], v49 offset:11264\n" \
"ds_read_b128 a[176:179], v49 offset:12288\n" \
"v_mfma_f32_16x16x32_bf16 v[68:71], a[156:159], a[76:79], v[32:35]\n" \
"ds_read_b128 a[180:183], v49 offset:13312\n" \
"ds_read_b128 a[184:187], v49 offset:14336\n" \
"v_mfma_f32_16x16x32_bf16 v[68:71], a[152:155], a[72:75], v[68:71]\n" \
"ds_read_b128 a[188:191], v49 offset:15360\n" \
"v_cvt_pk_bf16_f32 v118, v116, v117\n" \
"v_mfma_f32_16x16x32_bf16 v[68:71], a[148:151], a[68:71], v[68:71]\n" \
"v_cvt_pk_bf16_f32 v117, v114, v115\n" \
"v_cvt_pk_bf16_f32 v116, v112, v113\n" \
"v_mfma_f32_16x16x32_bf16 v[68:71], a[144:147], a[64:67], v[68:71]\n" \
"buffer_load_dwordx4 v52, s[8:11], s64 offen offset:1024 sc1 lds\n" \
"v_mfma_f32_16x16x32_bf16 v[76:79], a[144:147], a[80:83], v[36:39]\n" \
"v_exp_f32_e32 v120, v120\n" \
"v_mfma_f32_16x16x32_bf16 v[76:79], a[148:151], a[84:87], v[76:79]\n" \
"v_exp_f32_e32 v121, v121\n" \
"v_mfma_f32_16x16x32_bf16 v[76:79], a[152:155], a[88:91], v[76:79]\n" \
"v_exp_f32_e32 v122, v122\n" \
"v_mfma_f32_16x16x32_bf16 v[76:79], a[156:159], a[92:95], v[76:79]\n" \
"v_exp_f32_e32 v123, v123\n" \
"v_mfma_f32_16x16x32_bf16 v[72:75], a[140:143], a[92:95], v[36:39]\n" \
"v_cvt_pk_bf16_f32 v120, v120, v121\n" \
"v_cvt_pk_bf16_f32 v121, v122, v123\n" \
"v_mfma_f32_16x16x32_bf16 v[72:75], a[136:139], a[88:91], v[72:75]\n" \
"v_exp_f32_e32 v124, v124\n" \
"v_mfma_f32_16x16x32_bf16 v[72:75], a[132:135], a[84:87], v[72:75]\n" \
"v_exp_f32_e32 v125, v125\n" \
"v_mfma_f32_16x16x32_bf16 v[72:75], a[128:131], a[80:83], v[72:75]\n" \
"buffer_load_dwordx4 v52, s[8:11], s64 offen offset:2048 sc1 lds\n" \
"v_mfma_f32_16x16x32_bf16 v[80:83], a[128:131], a[96:99], v[40:43]\n" \
"v_exp_f32_e32 v126, v126\n" \
"v_mfma_f32_16x16x32_bf16 v[80:83], a[132:135], a[100:103], v[80:83]\n" \
"v_exp_f32_e32 v127, v127\n" \
"v_mfma_f32_16x16x32_bf16 v[80:83], a[136:139], a[104:107], v[80:83]\n" \
"v_cvt_pk_bf16_f32 v122, v124, v125\n" \
"v_cvt_pk_bf16_f32 v123, v126, v127\n" \
"v_mfma_f32_16x16x32_bf16 v[80:83], a[140:143], a[108:111], v[80:83]\n" \
"v_exp_f32_e32 v70, v70\n" \
"v_mfma_f32_16x16x32_bf16 v[84:87], a[156:159], a[108:111], v[40:43]\n" \
"v_exp_f32_e32 v71, v71\n" \
"v_mfma_f32_16x16x32_bf16 v[84:87], a[152:155], a[104:107], v[84:87]\n" \
"v_exp_f32_e32 v72, v72\n" \
"v_mfma_f32_16x16x32_bf16 v[84:87], a[148:151], a[100:103], v[84:87]\n" \
"buffer_load_dwordx4 v52, s[8:11], s64 offen offset:3072 sc1 lds\n" \
"v_mfma_f32_16x16x32_bf16 v[84:87], a[144:147], a[96:99], v[84:87]\n" \
"s_mov_b32 m0, s65\n" \
"v_cvt_pk_bf16_f32 v71, v70, v71\n" \
"v_mfma_f32_16x16x32_bf16 v[92:95], a[144:147], a[112:115], v[44:47]\n" \
"ds_read_b128 a[144:147], v48 offset:12288\n" \
"v_add_u32_e32 v49, 0x10000, v48\n" \
"v_mfma_f32_16x16x32_bf16 v[92:95], a[148:151], a[116:119], v[92:95]\n" \
"ds_read_b128 a[148:151], v48 offset:13312\n" \
"s_xor_b32 s65, s65, 0x8000\n" \
"v_mfma_f32_16x16x32_bf16 v[92:95], a[152:155], a[120:123], v[92:95]\n" \
"v_exp_f32_e32 v73, v73\n" \
"v_mfma_f32_16x16x32_bf16 v[92:95], a[156:159], a[124:127], v[92:95]\n" \
"ds_read_b128 a[152:155], v48 offset:14336\n" \
"ds_read_b128 a[156:159], v48 offset:15360\n" \
"v_mfma_f32_16x16x32_bf16 v[88:91], a[140:143], a[124:127], v[44:47]\n" \
"ds_read_b128 a[140:143], v48 offset:11264\n" \
"v_cvt_pk_bf16_f32 v72, v72, v73\n" \
"v_mfma_f32_16x16x32_bf16 v[88:91], a[136:139], a[120:123], v[88:91]\n" \
"v_exp_f32_e32 v68, v68\n" \
"v_mfma_f32_16x16x32_bf16 v[88:91], a[132:135], a[116:119], v[88:91]\n" \
"buffer_load_dwordx4 v53, s[8:11], s64 offen offset:0 sc1 lds\n" \
"v_mfma_f32_16x16x32_bf16 v[88:91], a[128:131], a[112:115], v[88:91]\n" \
"ds_read_b128 a[128:131], v48 offset:8192\n" \
"s_waitcnt lgkmcnt(6)\n" \
"v_mfma_f32_16x16x32_bf16 v[128:131], a[160:163], v[100:103], v[128:131]\n" \
"ds_read_b128 a[132:135], v48 offset:9216\n" \
"ds_read_b128 a[136:139], v48 offset:10240\n" \
"v_mfma_f32_16x16x32_bf16 v[160:163], a[160:163], v[104:107], v[160:163]\n" \
"v_exp_f32_e32 v69, v69\n" \
"v_mfma_f32_16x16x32_bf16 v[132:135], a[164:167], v[100:103], v[132:135]\n" \
"v_exp_f32_e32 v74, v74\n" \
"v_mfma_f32_16x16x32_bf16 v[164:167], a[164:167], v[104:107], v[164:167]\n" \
"v_exp_f32_e32 v75, v75\n" \
"v_mfma_f32_16x16x32_bf16 v[136:139], a[168:171], v[100:103], v[136:139]\n" \
"v_exp_f32_e32 v66, v66\n" \
"v_mfma_f32_16x16x32_bf16 v[168:171], a[168:171], v[104:107], v[168:171]\n" \
"buffer_load_dwordx4 v53, s[8:11], s64 offen offset:1024 sc1 lds\n" \
"v_mfma_f32_16x16x32_bf16 v[140:143], a[172:175], v[100:103], v[140:143]\n" \
"v_exp_f32_e32 v67, v67\n" \
"v_mfma_f32_16x16x32_bf16 v[172:175], a[172:175], v[104:107], v[172:175]\n" \
"v_exp_f32_e32 v64, v64\n" \
"v_mfma_f32_16x16x32_bf16 v[144:147], a[176:179], v[100:103], v[144:147]\n" \
"v_exp_f32_e32 v65, v65\n" \
"v_mfma_f32_16x16x32_bf16 v[176:179], a[176:179], v[104:107], v[176:179]\n" \
"v_exp_f32_e32 v76, v76\n" \
"v_mfma_f32_16x16x32_bf16 v[148:151], a[180:183], v[100:103], v[148:151]\n" \
"v_exp_f32_e32 v77, v77\n" \
"v_mfma_f32_16x16x32_bf16 v[180:183], a[180:183], v[104:107], v[180:183]\n" \
"v_exp_f32_e32 v78, v78\n" \
"v_mfma_f32_16x16x32_bf16 v[152:155], a[184:187], v[100:103], v[152:155]\n" \
"v_exp_f32_e32 v79, v79\n" \
"v_mfma_f32_16x16x32_bf16 v[184:187], a[184:187], v[104:107], v[184:187]\n" \
"buffer_load_dwordx4 v53, s[8:11], s64 offen offset:2048 sc1 lds\n" \
"v_mfma_f32_16x16x32_bf16 v[156:159], a[188:191], v[100:103], v[156:159]\n" \
"v_cvt_pk_bf16_f32 v70, v68, v69\n" \
"v_cvt_pk_bf16_f32 v73, v74, v75\n" \
"v_mfma_f32_16x16x32_bf16 v[188:191], a[188:191], v[104:107], v[188:191]\n" \
"v_cvt_pk_bf16_f32 v69, v66, v67\n" \
"v_cvt_pk_bf16_f32 v68, v64, v65\n" \
"v_smfmac_f32_16x16x64_bf16 a[192:195], a[200:203], v[100:107], v21\n" \
"v_cvt_pk_bf16_f32 v74, v76, v77\n" \
"v_cvt_pk_bf16_f32 v75, v78, v79\n" \
"v_mfma_f32_16x16x32_bf16 v[220:223], a[188:191], v[116:119], v[220:223]\n" \
"v_xor_b32_e32 v48, 0x8000, v48\n" \
"v_mfma_f32_16x16x32_bf16 v[252:255], a[188:191], v[120:123], v[252:255]\n" \
"v_mfma_f32_16x16x32_bf16 v[216:219], a[184:187], v[116:119], v[216:219]\n" \
"v_mfma_f32_16x16x32_bf16 v[248:251], a[184:187], v[120:123], v[248:251]\n" \
"buffer_load_dwordx4 v53, s[8:11], s64 offen offset:3072 sc1 lds\n" \
"v_mfma_f32_16x16x32_bf16 v[212:215], a[180:183], v[116:119], v[212:215]\n" \
"s_add_u32 s64, s64, 0x10000\n" \
"s_sub_u32 s66, s66, 1\n" \
"v_mfma_f32_16x16x32_bf16 v[244:247], a[180:183], v[120:123], v[244:247]\n" \
"v_mfma_f32_16x16x32_bf16 v[208:211], a[176:179], v[116:119], v[208:211]\n" \
"v_mfma_f32_16x16x32_bf16 v[240:243], a[176:179], v[120:123], v[240:243]\n" \
"v_mfma_f32_16x16x32_bf16 v[204:207], a[172:175], v[116:119], v[204:207]\n" \
"v_mfma_f32_16x16x32_bf16 v[236:239], a[172:175], v[120:123], v[236:239]\n" \
"v_mfma_f32_16x16x32_bf16 v[200:203], a[168:171], v[116:119], v[200:203]\n" \
"v_mfma_f32_16x16x32_bf16 v[232:235], a[168:171], v[120:123], v[232:235]\n" \
"v_mfma_f32_16x16x32_bf16 v[196:199], a[164:167], v[116:119], v[196:199]\n" \
"v_mfma_f32_16x16x32_bf16 v[228:231], a[164:167], v[120:123], v[228:231]\n" \
"v_mfma_f32_16x16x32_bf16 v[192:195], a[160:163], v[116:119], v[192:195]\n" \
"v_mfma_f32_16x16x32_bf16 v[224:227], a[160:163], v[120:123], v[224:227]\n" \
"v_smfmac_f32_16x16x64_bf16 a[196:199], a[200:203], v[116:123], v21\n" \
"s_waitcnt vmcnt(8) lgkmcnt(0)\n" \
"s_barrier\n" \
"s_cbranch_scc0 L_2_%=\n" \
"s_cmp_lg_u32 s16, 0\n" \
"s_cbranch_scc1 L_f1_%=\n" \
"v_bfe_u32 v58, v20, 2, 7\n" \
"v_readlane_b32 s96, v11, 1\n" \
"ds_read_b128 a[160:163], v49 offset:0\n" \
"ds_read_b128 a[164:167], v49 offset:1024\n" \
"ds_read_b128 a[168:171], v49 offset:2048\n" \
"v_mfma_f32_16x16x32_bf16 v[120:123], a[128:131], a[112:115], v[44:47]\n" \
"s_add_u32 s70, s61, 0x0\n" \
"s_add_u32 s71, s61, 0x49000\n" \
"buffer_load_dword v55, v58, s[56:59], s96 offen sc1\n" \
"ds_read_b128 a[172:175], v49 offset:3072\n" \
"v_mfma_f32_16x16x32_bf16 v[120:123], a[132:135], a[116:119], v[120:123]\n" \
"ds_read_b128 a[176:179], v49 offset:4096\n" \
"ds_read_b128 a[180:183], v49 offset:5120\n" \
"v_mfma_f32_16x16x32_bf16 v[120:123], a[136:139], a[120:123], v[120:123]\n" \
"ds_read_b128 a[184:187], v49 offset:6144\n" \
"ds_read_b128 a[188:191], v49 offset:7168\n" \
"v_mfma_f32_16x16x32_bf16 v[120:123], a[140:143], a[124:127], v[120:123]\n" \
"v_add_u32_e32 v54, 0x49000, v20\n" \
"ds_read_b32 v60, v18 offset:0\n" \
"v_mfma_f32_16x16x32_bf16 v[124:127], a[156:159], a[124:127], v[44:47]\n" \
"buffer_load_dwordx4 a[220:223], v20, s[56:59], s60 offen offset:0 sc1\n" \
"v_mfma_f32_16x16x32_bf16 v[124:127], a[152:155], a[120:123], v[124:127]\n" \
"v_exp_f32_e32 v86, v86\n" \
"v_mfma_f32_16x16x32_bf16 v[124:127], a[148:151], a[116:119], v[124:127]\n" \
"v_exp_f32_e32 v87, v87\n" \
"v_mfma_f32_16x16x32_bf16 v[124:127], a[144:147], a[112:115], v[124:127]\n" \
"v_exp_f32_e32 v84, v84\n" \
"v_exp_f32_e32 v85, v85\n" \
"buffer_load_dwordx4 a[236:239], v54, s[56:59], s60 offen offset:0 sc1\n" \
"s_waitcnt lgkmcnt(8)\n" \
"v_mfma_f32_16x16x32_bf16 v[128:131], a[160:163], v[68:71], v[128:131]\n" \
"v_cvt_pk_bf16_f32 v87, v86, v87\n" \
"v_cvt_pk_bf16_f32 v86, v84, v85\n" \
"v_mfma_f32_16x16x32_bf16 v[160:163], a[160:163], v[72:75], v[160:163]\n" \
"v_and_b32_e32 v86, v14, v86\n" \
"s_waitcnt lgkmcnt(7)\n" \
"v_mfma_f32_16x16x32_bf16 v[132:135], a[164:167], v[68:71], v[132:135]\n" \
"v_exp_f32_e32 v82, v82\n" \
"v_mfma_f32_16x16x32_bf16 v[164:167], a[164:167], v[72:75], v[164:167]\n" \
"v_and_b32_e32 v87, v15, v87\n" \
"s_waitcnt lgkmcnt(6)\n" \
"v_mfma_f32_16x16x32_bf16 v[136:139], a[168:171], v[68:71], v[136:139]\n" \
"buffer_load_dwordx4 a[224:227], v20, s[56:59], s60 offen offset:1024 sc1\n" \
"v_mfma_f32_16x16x32_bf16 v[168:171], a[168:171], v[72:75], v[168:171]\n" \
"ds_read_b32 v61, v18 offset:128\n" \
"s_waitcnt lgkmcnt(6)\n" \
"v_mfma_f32_16x16x32_bf16 v[140:143], a[172:175], v[68:71], v[140:143]\n" \
"v_exp_f32_e32 v83, v83\n" \
"v_mfma_f32_16x16x32_bf16 v[172:175], a[172:175], v[72:75], v[172:175]\n" \
"v_cvt_pk_bf16_f32 v85, v82, v83\n" \
"s_waitcnt lgkmcnt(2)\n" \
"v_mfma_f32_16x16x32_bf16 v[144:147], a[176:179], v[68:71], v[144:147]\n" \
"buffer_load_dwordx4 a[240:243], v54, s[56:59], s60 offen offset:1024 sc1\n" \
"v_mfma_f32_16x16x32_bf16 v[176:179], a[176:179], v[72:75], v[176:179]\n" \
"v_exp_f32_e32 v80, v80\n" \
"v_mfma_f32_16x16x32_bf16 v[148:151], a[180:183], v[68:71], v[148:151]\n" \
"v_exp_f32_e32 v81, v81\n" \
"v_mfma_f32_16x16x32_bf16 v[180:183], a[180:183], v[72:75], v[180:183]\n" \
"v_cvt_pk_bf16_f32 v84, v80, v81\n" \
"v_and_b32_e32 v84, v12, v84\n" \
"v_mfma_f32_16x16x32_bf16 v[152:155], a[184:187], v[68:71], v[152:155]\n" \
"buffer_load_dwordx4 a[228:231], v20, s[56:59], s60 offen offset:2048 sc1\n" \
"v_mfma_f32_16x16x32_bf16 v[184:187], a[184:187], v[72:75], v[184:187]\n" \
"v_and_b32_e32 v85, v13, v85\n" \
"ds_read_b128 a[0:3], v49 offset:8192\n" \
"v_mfma_f32_16x16x32_bf16 v[156:159], a[188:191], v[68:71], v[156:159]\n" \
"ds_read_b128 a[4:7], v49 offset:9216\n" \
"ds_read_b128 a[8:11], v49 offset:10240\n" \
"v_mfma_f32_16x16x32_bf16 v[188:191], a[188:191], v[72:75], v[188:191]\n" \
"ds_read_b128 a[12:15], v49 offset:11264\n" \
"ds_read_b128 a[16:19], v49 offset:12288\n" \
"v_smfmac_f32_16x16x64_bf16 a[192:195], a[200:203], v[68:75], v21\n" \
"buffer_load_dwordx4 a[244:247], v54, s[56:59], s60 offen offset:2048 sc1\n" \
"v_mfma_f32_16x16x32_bf16 v[192:195], a[160:163], v[84:87], v[192:195]\n" \
"ds_read_b128 a[20:23], v49 offset:13312\n" \
"ds_read_b128 a[24:27], v49 offset:14336\n" \
"v_mfma_f32_16x16x32_bf16 v[196:199], a[164:167], v[84:87], v[196:199]\n" \
"ds_read_b128 a[28:31], v49 offset:15360\n" \
"v_add_u32_e32 v50, s15, v48\n" \
"v_mfma_f32_16x16x32_bf16 v[200:203], a[168:171], v[84:87], v[200:203]\n" \
"v_accvgpr_read_b32 v40, a192\n" \
"v_accvgpr_read_b32 v22, a193\n" \
"v_mfma_f32_16x16x32_bf16 v[204:207], a[172:175], v[84:87], v[204:207]\n" \
"buffer_load_dwordx4 a[232:235], v20, s[56:59], s60 offen offset:3072 sc1\n" \
"v_mfma_f32_16x16x32_bf16 v[208:211], a[176:179], v[84:87], v[208:211]\n" \
"v_accvgpr_read_b32 v41, a194\n" \
"v_accvgpr_read_b32 v23, a195\n" \
"v_mfma_f32_16x16x32_bf16 v[212:215], a[180:183], v[84:87], v[212:215]\n" \
"v_add_u32_e32 v51, 0x10000, v50\n" \
"v_add_f32_e32 v40, v40, v22\n" \
"v_mfma_f32_16x16x32_bf16 v[216:219], a[184:187], v[84:87], v[216:219]\n" \
"v_add_f32_e32 v41, v41, v23\n" \
"ds_write_b64 v19, v[40:41]\n" \
"v_mfma_f32_16x16x32_bf16 v[220:223], a[188:191], v[84:87], v[220:223]\n" \
"buffer_load_dwordx4 a[248:251], v54, s[56:59], s60 offen offset:3072 sc1\n" \
"v_mfma_f32_16x16x32_bf16 a[196:199], a[204:207], v[84:87], a[196:199]\n" \
"s_waitcnt vmcnt(9)\n" \
"ds_write_b128 v50, v[128:131] offset:0\n" \
"ds_write_b128 v50, v[132:135] offset:1024\n" \
"ds_write_b128 v50, v[136:139] offset:2048\n" \
"ds_write_b128 v50, v[140:143] offset:3072\n" \
"ds_write_b128 v50, v[144:147] offset:8192\n" \
"ds_write_b128 v50, v[148:151] offset:9216\n" \
"ds_write_b128 v50, v[152:155] offset:10240\n" \
"ds_write_b128 v50, v[156:159] offset:11264\n" \
"ds_write_b128 v51, v[160:163] offset:0\n" \
"ds_write_b128 v51, v[164:167] offset:1024\n" \
"ds_write_b128 v51, v[168:171] offset:2048\n" \
"ds_write_b128 v51, v[172:175] offset:3072\n" \
"ds_write_b128 v51, v[176:179] offset:8192\n" \
"ds_write_b128 v51, v[180:183] offset:9216\n" \
"ds_write_b128 v51, v[184:187] offset:10240\n" \
"ds_write_b128 v51, v[188:191] offset:11264\n" \
"v_xor_b32_e32 v52, 0x4000, v50\n" \
"v_xor_b32_e32 v53, 0x4000, v51\n" \
"v_exp_f32_e32 v88, v88\n" \
"v_exp_f32_e32 v89, v89\n" \
"v_exp_f32_e32 v90, v90\n" \
"v_exp_f32_e32 v91, v91\n" \
"v_cvt_pk_bf16_f32 v88, v88, v89\n" \
"v_exp_f32_e32 v92, v92\n" \
"v_exp_f32_e32 v93, v93\n" \
"v_exp_f32_e32 v94, v94\n" \
"v_exp_f32_e32 v95, v95\n" \
"v_xor_b32_e32 v63, 0x400, v19\n" \
"v_cvt_pk_bf16_f32 v89, v90, v91\n" \
"v_cvt_pk_bf16_f32 v90, v92, v93\n" \
"v_cvt_pk_bf16_f32 v91, v94, v95\n" \
"v_readlane_b32 s93, v11, 20\n" \
"v_readlane_b32 s84, v11, 15\n" \
"v_readlane_b32 s85, v11, 16\n" \
"v_readlane_b32 s86, v11, 17\n" \
"s_mov_b32 s87, 0x20000\n" \
"v_readlane_b32 s92, v11, 18\n" \
"s_add_u32 m0, s93, 0x0\n" \
"v_readlane_b32 s97, v11, 34\n" \
"v_readlane_b32 s98, v11, 35\n" \
"v_exp_f32_e32 v120, v120\n" \
"v_exp_f32_e32 v121, v121\n" \
"v_exp_f32_e32 v122, v122\n" \
"v_exp_f32_e32 v123, v123\n" \
"s_waitcnt lgkmcnt(0)\n" \
"s_barrier\n" \
"ds_read_b128 v[128:131], v52 offset:0\n" \
"ds_read_b128 v[132:135], v52 offset:1024\n" \
"ds_read_b128 v[136:139], v52 offset:2048\n" \
"ds_read_b128 v[140:143], v52 offset:3072\n" \
"ds_read_b128 v[144:147], v52 offset:8192\n" \
"ds_read_b128 v[148:151], v52 offset:9216\n" \
"ds_read_b128 v[152:155], v52 offset:10240\n" \
"ds_read_b128 v[156:159], v52 offset:11264\n" \
"ds_read_b128 v[160:163], v53 offset:0\n" \
"ds_read_b128 v[164:167], v53 offset:1024\n" \
"ds_read_b128 v[168:171], v53 offset:2048\n" \
"ds_read_b128 v[172:175], v53 offset:3072\n" \
"ds_read_b128 v[176:179], v53 offset:8192\n" \
"ds_read_b128 v[180:183], v53 offset:9216\n" \
"ds_read_b128 v[184:187], v53 offset:10240\n" \
"ds_read_b128 v[188:191], v53 offset:11264\n" \
"ds_read_b64 v[42:43], v63\n" \
"v_mfma_f32_16x16x32_bf16 v[224:227], a[160:163], v[88:91], v[224:227]\n" \
"buffer_load_dwordx4 v0, s[84:87], s92 offen offset:0 lds\n" \
"v_mfma_f32_16x16x32_bf16 v[228:231], a[164:167], v[88:91], v[228:231]\n" \
"s_waitcnt vmcnt(9)\n" \
"v_cmp_ne_u32_e32 vcc, s62, v55\n" \
"v_mfma_f32_16x16x32_bf16 v[232:235], a[168:171], v[88:91], v[232:235]\n" \
"v_exp_f32_e32 v124, v124\n" \
"v_mfma_f32_16x16x32_bf16 v[236:239], a[172:175], v[88:91], v[236:239]\n" \
"v_exp_f32_e32 v125, v125\n" \
"v_mfma_f32_16x16x32_bf16 v[240:243], a[176:179], v[88:91], v[240:243]\n" \
"buffer_load_dwordx4 v1, s[84:87], s92 offen offset:1024 lds\n" \
"s_cbranch_vccnz L_so0_%=\n" \
"L_ro0_%=:\n" \
"v_mfma_f32_16x16x32_bf16 v[244:247], a[180:183], v[88:91], v[244:247]\n" \
"v_cvt_pk_bf16_f32 v120, v120, v121\n" \
"v_cvt_pk_bf16_f32 v121, v122, v123\n" \
"v_mfma_f32_16x16x32_bf16 v[248:251], a[184:187], v[88:91], v[248:251]\n" \
"v_exp_f32_e32 v126, v126\n" \
"v_mfma_f32_16x16x32_bf16 v[252:255], a[188:191], v[88:91], v[252:255]\n" \
"v_exp_f32_e32 v127, v127\n" \
"buffer_load_dwordx4 v2, s[84:87], s92 offen offset:2048 lds\n" \
"v_mfma_f32_16x16x32_bf16 a[196:199], a[212:215], v[88:91], a[196:199]\n" \
"v_cvt_pk_bf16_f32 v122, v124, v125\n" \
"v_cvt_pk_bf16_f32 v123, v126, v127\n" \
"s_waitcnt lgkmcnt(1)\n" \
"s_barrier\n" \
"v_and_b32_e32 v120, v12, v120\n" \
"v_and_b32_e32 v121, v13, v121\n" \
"v_and_b32_e32 v122, v14, v122\n" \
"v_and_b32_e32 v123, v15, v123\n" \
"buffer_load_dwordx4 v3, s[84:87], s92 offen offset:3072 lds\n" \
"s_add_u32 m0, s93, 0x1000\n" \
"v_mfma_f32_16x16x32_bf16 v[224:227], a[0:3], v[120:123], v[224:227]\n" \
"v_add_f32_e32 v192, v192, v128\n" \
"v_add_f32_e32 v193, v193, v129\n" \
"v_mfma_f32_16x16x32_bf16 v[228:231], a[4:7], v[120:123], v[228:231]\n" \
"v_add_f32_e32 v194, v194, v130\n" \
"v_add_f32_e32 v195, v195, v131\n" \
"v_mfma_f32_16x16x32_bf16 v[232:235], a[8:11], v[120:123], v[232:235]\n" \
"v_add_f32_e32 v196, v196, v132\n" \
"v_add_f32_e32 v197, v197, v133\n" \
"buffer_load_dwordx4 v4, s[84:87], s92 offen offset:0 lds\n" \
"v_mfma_f32_16x16x32_bf16 v[236:239], a[12:15], v[120:123], v[236:239]\n" \
"v_add_f32_e32 v198, v198, v134\n" \
"v_add_f32_e32 v199, v199, v135\n" \
"v_mfma_f32_16x16x32_bf16 v[240:243], a[16:19], v[120:123], v[240:243]\n" \
"v_add_f32_e32 v200, v200, v136\n" \
"v_add_f32_e32 v201, v201, v137\n" \
"v_add_f32_e32 v202, v202, v138\n" \
"v_add_f32_e32 v203, v203, v139\n" \
"v_add_f32_e32 v204, v204, v140\n" \
"v_add_f32_e32 v205, v205, v141\n" \
"v_add_f32_e32 v206, v206, v142\n" \
"buffer_load_dwordx4 v5, s[84:87], s92 offen offset:1024 lds\n" \
"v_add_f32_e32 v207, v207, v143\n" \
"v_mfma_f32_16x16x32_bf16 v[244:247], a[20:23], v[120:123], v[244:247]\n" \
"v_add_f32_e32 v208, v208, v144\n" \
"v_add_f32_e32 v209, v209, v145\n" \
"v_add_f32_e32 v210, v210, v146\n" \
"v_add_f32_e32 v211, v211, v147\n" \
"v_add_f32_e32 v212, v212, v148\n" \
"v_add_f32_e32 v213, v213, v149\n" \
"v_add_f32_e32 v214, v214, v150\n" \
"v_add_f32_e32 v215, v215, v151\n" \
"v_add_f32_e32 v216, v216, v152\n" \
"v_add_f32_e32 v217, v217, v153\n" \
"buffer_load_dwordx4 v6, s[84:87], s92 offen offset:2048 lds\n" \
"v_add_f32_e32 v218, v218, v154\n" \
"v_add_f32_e32 v219, v219, v155\n" \
"v_add_f32_e32 v220, v220, v156\n" \
"v_add_f32_e32 v221, v221, v157\n" \
"v_add_f32_e32 v222, v222, v158\n" \
"v_add_f32_e32 v223, v223, v159\n" \
"v_mfma_f32_16x16x32_bf16 v[248:251], a[24:27], v[120:123], v[248:251]\n" \
"v_add_f32_e32 v224, v224, v160\n" \
"v_add_f32_e32 v225, v225, v161\n" \
"v_add_f32_e32 v226, v226, v162\n" \
"v_add_f32_e32 v227, v227, v163\n" \
"v_add_f32_e32 v228, v228, v164\n" \
"buffer_load_dwordx4 v7, s[84:87], s92 offen offset:3072 lds\n" \
"s_add_u32 m0, s93, 0x2000\n" \
"v_add_f32_e32 v229, v229, v165\n" \
"v_add_f32_e32 v230, v230, v166\n" \
"v_add_f32_e32 v231, v231, v167\n" \
"v_add_f32_e32 v232, v232, v168\n" \
"v_add_f32_e32 v233, v233, v169\n" \
"v_add_f32_e32 v234, v234, v170\n" \
"v_add_f32_e32 v235, v235, v171\n" \
"v_add_f32_e32 v236, v236, v172\n" \
"v_add_f32_e32 v237, v237, v173\n" \
"v_add_f32_e32 v238, v238, v174\n" \
"v_add_f32_e32 v239, v239, v175\n" \
"v_mfma_f32_16x16x32_bf16 v[252:255], a[28:31], v[120:123], v[252:255]\n" \
"buffer_load_dwordx4 v8, s[84:87], s92 offen offset:0 lds\n" \
"v_add_f32_e32 v240, v240, v176\n" \
"v_add_f32_e32 v241, v241, v177\n" \
"v_add_f32_e32 v242, v242, v178\n" \
"v_add_f32_e32 v243, v243, v179\n" \
"v_add_f32_e32 v244, v244, v180\n" \
"v_add_f32_e32 v245, v245, v181\n" \
"v_add_f32_e32 v246, v246, v182\n" \
"v_add_f32_e32 v247, v247, v183\n" \
"v_add_f32_e32 v248, v248, v184\n" \
"v_add_f32_e32 v249, v249, v185\n" \
"v_add_f32_e32 v250, v250, v186\n" \
"v_add_f32_e32 v251, v251, v187\n" \
"v_add_f32_e32 v252, v252, v188\n" \
"buffer_load_dwordx4 a[96:99], v20, s[56:59], s97 offen offset:0 sc1\n" \
"v_add_f32_e32 v253, v253, v189\n" \
"v_add_f32_e32 v254, v254, v190\n" \
"v_add_f32_e32 v255, v255, v191\n" \
"v_mfma_f32_16x16x32_bf16 a[196:199], a[212:215], v[120:123], a[196:199]\n" \
"buffer_load_dwordx4 a[104:107], v54, s[56:59], s97 offen offset:0 sc1\n" \
"buffer_load_dwordx4 a[112:115], v20, s[56:59], s98 offen offset:0 sc1\n" \
"buffer_load_dwordx4 a[120:123], v54, s[56:59], s98 offen offset:0 sc1\n" \
"buffer_load_dwordx4 a[100:103], v20, s[56:59], s97 offen offset:1024 sc1\n" \
"buffer_load_dwordx4 a[108:111], v54, s[56:59], s97 offen offset:1024 sc1\n" \
"buffer_load_dwordx4 a[116:119], v20, s[56:59], s98 offen offset:1024 sc1\n" \
"buffer_load_dwordx4 a[124:127], v54, s[56:59], s98 offen offset:1024 sc1\n" \
"s_nop 0\n" \
"v_accvgpr_read_b32 v56, a196\n" \
"v_accvgpr_read_b32 v24, a197\n" \
"v_add_f32_e32 v56, v56, v24\n" \
"s_waitcnt lgkmcnt(0)\n" \
"v_add_f32_e32 v56, v56, v42\n" \
"v_max_f32_e32 v62, v62, v56\n" \
"v_rcp_f32_e32 v56, v56\n" \
"v_accvgpr_read_b32 v57, a198\n" \
"v_mul_f32_e32 v56, v56, v60\n" \
"v_mul_f32_e32 v192, v56, v192\n" \
"v_mul_f32_e32 v193, v56, v193\n" \
"v_mul_f32_e32 v194, v56, v194\n" \
"v_mul_f32_e32 v195, v56, v195\n" \
"v_mul_f32_e32 v196, v56, v196\n" \
"v_mul_f32_e32 v197, v56, v197\n" \
"v_mul_f32_e32 v198, v56, v198\n" \
"v_mul_f32_e32 v199, v56, v199\n" \
"v_cvt_pk_bf16_f32 v64, v192, v193\n" \
"v_cvt_pk_bf16_f32 v65, v194, v195\n" \
"v_cvt_pk_bf16_f32 v66, v196, v197\n" \
"v_cvt_pk_bf16_f32 v67, v198, v199\n" \
"v_accvgpr_read_b32 v25, a199\n" \
"v_mul_f32_e32 v200, v56, v200\n" \
"v_mfma_f32_16x16x32_bf16 a[208:211], a[216:219], v[64:67], a[208:211]\n" \
"v_mul_f32_e32 v201, v56, v201\n" \
"v_mul_f32_e32 v202, v56, v202\n" \
"v_mul_f32_e32 v203, v56, v203\n" \
"v_mul_f32_e32 v204, v56, v204\n" \
"v_mul_f32_e32 v205, v56, v205\n" \
"v_mul_f32_e32 v206, v56, v206\n" \
"v_mul_f32_e32 v207, v56, v207\n" \
"v_add_f32_e32 v57, v57, v25\n" \
"v_cvt_pk_bf16_f32 v68, v200, v201\n" \
"v_cvt_pk_bf16_f32 v69, v202, v203\n" \
"v_cvt_pk_bf16_f32 v70, v204, v205\n" \
"v_cvt_pk_bf16_f32 v71, v206, v207\n" \
"v_add_f32_e32 v57, v57, v43\n" \
"v_mul_f32_e32 v208, v56, v208\n" \
"v_mfma_f32_16x16x32_bf16 a[208:211], a[216:219], v[68:71], a[208:211]\n" \
"v_mul_f32_e32 v209, v56, v209\n" \
"v_mul_f32_e32 v210, v56, v210\n" \
"v_mul_f32_e32 v211, v56, v211\n" \
"v_mul_f32_e32 v212, v56, v212\n" \
"v_mul_f32_e32 v213, v56, v213\n" \
"v_mul_f32_e32 v214, v56, v214\n" \
"v_mul_f32_e32 v215, v56, v215\n" \
"v_max_f32_e32 v62, v62, v57\n" \
"v_cvt_pk_bf16_f32 v72, v208, v209\n" \
"v_cvt_pk_bf16_f32 v73, v210, v211\n" \
"v_cvt_pk_bf16_f32 v74, v212, v213\n" \
"v_cvt_pk_bf16_f32 v75, v214, v215\n" \
"v_rcp_f32_e32 v57, v57\n" \
"v_mul_f32_e32 v216, v56, v216\n" \
"v_mfma_f32_16x16x32_bf16 a[208:211], a[216:219], v[72:75], a[208:211]\n" \
"v_mul_f32_e32 v217, v56, v217\n" \
"v_mul_f32_e32 v218, v56, v218\n" \
"v_mul_f32_e32 v219, v56, v219\n" \
"v_mul_f32_e32 v220, v56, v220\n" \
"v_mul_f32_e32 v221, v56, v221\n" \
"v_mul_f32_e32 v222, v56, v222\n" \
"v_mul_f32_e32 v223, v56, v223\n" \
"v_cvt_pk_bf16_f32 v76, v216, v217\n" \
"v_cvt_pk_bf16_f32 v77, v218, v219\n" \
"v_cvt_pk_bf16_f32 v78, v220, v221\n" \
"v_cvt_pk_bf16_f32 v79, v222, v223\n" \
"v_mul_f32_e32 v57, v57, v61\n" \
"v_mul_f32_e32 v224, v57, v224\n" \
"v_mfma_f32_16x16x32_bf16 a[208:211], a[216:219], v[76:79], a[208:211]\n" \
"v_mul_f32_e32 v225, v57, v225\n" \
"v_mul_f32_e32 v226, v57, v226\n" \
"v_mul_f32_e32 v227, v57, v227\n" \
"v_mul_f32_e32 v228, v57, v228\n" \
"v_mul_f32_e32 v229, v57, v229\n" \
"v_mul_f32_e32 v230, v57, v230\n" \
"v_mul_f32_e32 v231, v57, v231\n" \
"v_cvt_pk_bf16_f32 v80, v224, v225\n" \
"v_cvt_pk_bf16_f32 v81, v226, v227\n" \
"v_cvt_pk_bf16_f32 v82, v228, v229\n" \
"v_cvt_pk_bf16_f32 v83, v230, v231\n" \
"v_mul_f32_e32 v232, v57, v232\n" \
"v_mul_f32_e32 v233, v57, v233\n" \
"v_mfma_f32_16x16x32_bf16 a[208:211], a[216:219], v[80:83], a[208:211]\n" \
"v_mul_f32_e32 v234, v57, v234\n" \
"v_mul_f32_e32 v235, v57, v235\n" \
"v_mul_f32_e32 v236, v57, v236\n" \
"v_mul_f32_e32 v237, v57, v237\n" \
"v_mul_f32_e32 v238, v57, v238\n" \
"v_mul_f32_e32 v239, v57, v239\n" \
"v_cvt_pk_bf16_f32 v84, v232, v233\n" \
"v_cvt_pk_bf16_f32 v85, v234, v235\n" \
"v_cvt_pk_bf16_f32 v86, v236, v237\n" \
"buffer_store_dwordx4 v[64:67], v20, s[56:59], s70 offen offset:0 sc1\n" \
"v_cvt_pk_bf16_f32 v87, v238, v239\n" \
"v_mul_f32_e32 v240, v57, v240\n" \
"v_mul_f32_e32 v241, v57, v241\n" \
"v_mfma_f32_16x16x32_bf16 a[208:211], a[216:219], v[84:87], a[208:211]\n" \
"v_mul_f32_e32 v242, v57, v242\n" \
"v_mul_f32_e32 v243, v57, v243\n" \
"v_mul_f32_e32 v244, v57, v244\n" \
"v_mul_f32_e32 v245, v57, v245\n" \
"v_mul_f32_e32 v246, v57, v246\n" \
"v_mul_f32_e32 v247, v57, v247\n" \
"v_cvt_pk_bf16_f32 v96, v240, v241\n" \
"v_cvt_pk_bf16_f32 v97, v242, v243\n" \
"buffer_store_dwordx4 v[68:71], v20, s[56:59], s70 offen offset:1024 sc1\n" \
"v_cvt_pk_bf16_f32 v98, v244, v245\n" \
"v_cvt_pk_bf16_f32 v99, v246, v247\n" \
"v_mul_f32_e32 v248, v57, v248\n" \
"v_mul_f32_e32 v249, v57, v249\n" \
"v_mfma_f32_16x16x32_bf16 a[208:211], a[216:219], v[96:99], a[208:211]\n" \
"v_mul_f32_e32 v250, v57, v250\n" \
"v_mul_f32_e32 v251, v57, v251\n" \
"v_mul_f32_e32 v252, v57, v252\n" \
"v_mul_f32_e32 v253, v57, v253\n" \
"v_mul_f32_e32 v254, v57, v254\n" \
"v_mul_f32_e32 v255, v57, v255\n" \
"v_cvt_pk_bf16_f32 v100, v248, v249\n" \
"buffer_store_dwordx4 v[72:75], v20, s[56:59], s70 offen offset:2048 sc1\n" \
"v_cvt_pk_bf16_f32 v101, v250, v251\n" \
"v_cvt_pk_bf16_f32 v102, v252, v253\n" \
"v_cvt_pk_bf16_f32 v103, v254, v255\n" \
"v_cmp_lt_f32_e64 s[72:73], s21, v62\n" \
"buffer_store_dwordx4 v[76:79], v20, s[56:59], s70 offen offset:3072 sc1\n" \
"v_mfma_f32_16x16x32_bf16 a[208:211], a[216:219], v[100:103], a[208:211]\n" \
"buffer_store_dwordx4 v[80:83], v20, s[56:59], s71 offen offset:0 sc1\n" \
"buffer_store_dwordx4 v[84:87], v20, s[56:59], s71 offen offset:1024 sc1\n" \
"buffer_store_dwordx4 v[96:99], v20, s[56:59], s71 offen offset:2048 sc1\n" \
"buffer_store_dwordx4 v[100:103], v20, s[56:59], s71 offen offset:3072 sc1\n" \
"s_nop 3\n" \
"v_accvgpr_read_b32 v63, a208\n" \
"v_cmp_u_f32_e32 vcc, v63, v63\n" \
"s_nop 0\n" \
"s_or_b64 vcc, s[72:73], vcc\n" \
"s_cbranch_scc0 L_out_%=\n" \
"s_branch L_end_%=\n" \
"L_skb_%=:\n" \
"s_sleep 1\n" \
"buffer_load_dword v57, v61, s[56:59], s96 offen sc1\n" \
"s_add_u32 s101, s101, 1\n" \
"s_waitcnt vmcnt(0)\n" \
"v_cmp_ne_u32_e32 vcc, s62, v57\n" \
"s_cmp_gt_u32 s101, 0x100000\n" \
"s_cbranch_scc1 L_rkb_%=\n" \
"s_nop 2\n" \
"s_cbranch_vccnz L_skb_%=\n" \
"s_branch L_rkb_%=\n" \
"L_so0_%=:\n" \
"s_sleep 1\n" \
"buffer_load_dword v55, v58, s[56:59], s96 offen sc1\n" \
"s_add_u32 s101, s101, 1\n" \
"s_waitcnt vmcnt(0)\n" \
"v_cmp_ne_u32_e32 vcc, s62, v55\n" \
"s_cmp_gt_u32 s101, 0x100000\n" \
"s_cbranch_scc1 L_ro0_%=\n" \
"s_nop 2\n" \
"s_cbranch_vccnz L_so0_%=\n" \
"s_branch L_ro0_%=\n" \
"L_so1_%=:\n" \
"s_sleep 1\n" \
"buffer_load_dword v57, v60, s[56:59], s96 offen sc1\n" \
"s_add_u32 s101, s101, 1\n" \
"s_waitcnt vmcnt(0)\n" \
"v_cmp_ne_u32_e32 vcc, s62, v57\n" \
"s_cmp_gt_u32 s101, 0x100000\n" \
"s_cbranch_scc1 L_ro1_%=\n" \
"s_nop 2\n" \
"s_cbranch_vccnz L_so1_%=\n" \
"s_branch L_ro1_%=\n" \
"L_sa_%=:\n" \
"s_sleep 1\n" \
"buffer_load_dword v37, v38, s[56:59], s64 offen sc1\n" \
"s_add_u32 s101, s101, 1\n" \
"s_waitcnt vmcnt(0)\n" \
"v_cmp_ne_u32_e32 vcc, s62, v37\n" \
"s_cmp_gt_u32 s101, 0x100000\n" \
"s_cbranch_scc1 L_ra_%=\n" \
"s_nop 2\n" \
"s_cbranch_vccnz L_sa_%=\n" \
"s_branch L_ra_%=\n" \
"L_sb_%=:\n" \
"s_sleep 1\n" \
"buffer_load_dword v47, v38, s[56:59], s71 offen sc1\n" \
"s_add_u32 s101, s101, 1\n" \
"s_waitcnt vmcnt(0)\n" \
"v_cmp_ne_u32_e32 vcc, s62, v47\n" \
"s_cmp_gt_u32 s101, 0x100000\n" \
"s_cbranch_scc1 L_rb_%=\n" \
"s_nop 2\n" \
"s_cbranch_vccnz L_sb_%=\n" \
"s_branch L_rb_%=\n" \
"L_sx0_%=:\n" \
"s_sleep 1\n" \
"buffer_load_dword v37, v40, s[56:59], s74 offen sc1\n" \
"s_add_u32 s101, s101, 1\n" \
"s_waitcnt vmcnt(0)\n" \
"v_cmp_eq_u32_e64 s[98:99], s62, v37\n" \
"s_cmp_gt_u32 s101, 0x100000\n" \
"s_cbranch_scc1 L_rx0_%=\n" \
"s_andn2_b32 s75, 0x1, s98\n" \
"s_cbranch_scc1 L_sx0_%=\n" \
"s_branch L_rx0_%=\n" \
"L_sx1_%=:\n" \
"s_sleep 1\n" \
"buffer_load_dword v37, v40, s[56:59], s74 offen sc1\n" \
"s_add_u32 s101, s101, 1\n" \
"s_waitcnt vmcnt(0)\n" \
"v_cmp_eq_u32_e64 s[98:99], s62, v37\n" \
"s_cmp_gt_u32 s101, 0x100000\n" \
"s_cbranch_scc1 L_rx1_%=\n" \
"s_andn2_b32 s75, 0x2, s98\n" \
"s_cbranch_scc1 L_sx1_%=\n" \
"s_branch L_rx1_%=\n" \
"L_sx2_%=:\n" \
"s_sleep 1\n" \
"buffer_load_dword v37, v40, s[56:59], s74 offen sc1\n" \
"s_add_u32 s101, s101, 1\n" \
"s_waitcnt vmcnt(0)\n" \
"v_cmp_eq_u32_e64 s[98:99], s62, v37\n" \
"s_cmp_gt_u32 s101, 0x100000\n" \
"s_cbranch_scc1 L_rx2_%=\n" \
"s_andn2_b32 s75, 0x4, s98\n" \
"s_cbranch_scc1 L_sx2_%=\n" \
"s_branch L_rx2_%=\n" \
"L_sx3_%=:\n" \
"s_sleep 1\n" \
"buffer_load_dword v37, v40, s[56:59], s74 offen sc1\n" \
"s_add_u32 s101, s101, 1\n" \
"s_waitcnt vmcnt(0)\n" \
"v_cmp_eq_u32_e64 s[98:99], s62, v37\n" \
"s_cmp_gt_u32 s101, 0x100000\n" \
"s_cbranch_scc1 L_rx3_%=\n" \
"s_andn2_b32 s75, s100, s98\n" \
"s_cbranch_scc1 L_sx3_%=\n" \
"s_branch L_rx3_%=\n" \
"L_sx4_%=:\n" \
"s_sleep 1\n" \
"buffer_load_dword v37, v40, s[56:59], s74 offen sc1\n" \
"s_add_u32 s101, s101, 1\n" \
"s_waitcnt vmcnt(0)\n" \
"v_cmp_eq_u32_e64 s[98:99], s62, v37\n" \
"s_cmp_gt_u32 s101, 0x100000\n" \
"s_cbranch_scc1 L_rx4_%=\n" \
"s_andn2_b32 s75, 0x10, s98\n" \
"s_cbranch_scc1 L_sx4_%=\n" \
"s_branch L_rx4_%=\n" \
"L_sx5_%=:\n" \
"s_sleep 1\n" \
"buffer_load_dword v37, v40, s[56:59], s74 offen sc1\n" \
"s_add_u32 s101, s101, 1\n" \
"s_waitcnt vmcnt(0)\n" \
"v_cmp_eq_u32_e64 s[98:99], s62, v37\n" \
"s_cmp_gt_u32 s101, 0x100000\n" \
"s_cbranch_scc1 L_rx5_%=\n" \
"s_andn2_b32 s75, 0x20, s98\n" \
"s_cbranch_scc1 L_sx5_%=\n" \
"s_branch L_rx5_%=\n" \
"L_sx6_%=:\n" \
"s_sleep 1\n" \
"buffer_load_dword v37, v40, s[56:59], s74 offen sc1\n" \
"s_add_u32 s101, s101, 1\n" \
"s_waitcnt vmcnt(0)\n" \
"v_cmp_eq_u32_e64 s[98:99], s62, v37\n" \
"s_cmp_gt_u32 s101, 0x100000\n" \
"s_cbranch_scc1 L_rx6_%=\n" \
"s_andn2_b32 s75, 0x40, s98\n" \
"s_cbranch_scc1 L_sx6_%=\n" \
"s_branch L_rx6_%=\n" \
"L_sx7_%=:\n" \
"s_sleep 1\n" \
"buffer_load_dword v37, v40, s[56:59], s74 offen sc1\n" \
"s_add_u32 s101, s101, 1\n" \
"s_waitcnt vmcnt(0)\n" \
"v_cmp_eq_u32_e64 s[98:99], s62, v37\n" \
"s_cmp_gt_u32 s101, 0x100000\n" \
"s_cbranch_scc1 L_rx7_%=\n" \
"s_andn2_b32 s75, 0x80, s98\n" \
"s_cbranch_scc1 L_sx7_%=\n" \
"s_branch L_rx7_%=\n" \
"L_sx8_%=:\n" \
"s_sleep 1\n" \
"buffer_load_dword v37, v40, s[56:59], s74 offen sc1\n" \
"s_add_u32 s101, s101, 1\n" \
"s_waitcnt vmcnt(0)\n" \
"v_cmp_eq_u32_e64 s[98:99], s62, v37\n" \
"s_cmp_gt_u32 s101, 0x100000\n" \
"s_cbranch_scc1 L_rx8_%=\n" \
"s_andn2_b32 s75, 0x100, s98\n" \
"s_cbranch_scc1 L_sx8_%=\n" \
"s_branch L_rx8_%=\n" \
"L_sx9_%=:\n" \
"s_sleep 1\n" \
"buffer_load_dword v37, v40, s[56:59], s74 offen sc1\n" \
"s_add_u32 s101, s101, 1\n" \
"s_waitcnt vmcnt(0)\n" \
"v_cmp_eq_u32_e64 s[98:99], s62, v37\n" \
"s_cmp_gt_u32 s101, 0x100000\n" \
"s_cbranch_scc1 L_rx9_%=\n" \
"s_andn2_b32 s75, 0x200, s98\n" \
"s_cbranch_scc1 L_sx9_%=\n" \
"s_branch L_rx9_%=\n" \
"L_sx10_%=:\n" \
"s_sleep 1\n" \
"buffer_load_dword v37, v40, s[56:59], s74 offen sc1\n" \
"s_add_u32 s101, s101, 1\n" \
"s_waitcnt vmcnt(0)\n" \
"v_cmp_eq_u32_e64 s[98:99], s62, v37\n" \
"s_cmp_gt_u32 s101, 0x100000\n" \
"s_cbranch_scc1 L_rx10_%=\n" \
"s_andn2_b32 s75, 0x400, s98\n" \
"s_cbranch_scc1 L_sx10_%=\n" \
"s_branch L_rx10_%=\n" \
"L_att_stub:\n" \
"s_mov_b32 s101, 0\n" \
"v_add_u32_e32 v32, 0x49000, v20\n" \
"v_readlane_b32 s96, v11, 34\n" \
"v_readlane_b32 s97, v11, 35\n" \
"buffer_load_dwordx4 a[220:223], v20, s[56:59], s60 offen offset:0 sc1\n" \
"buffer_load_dwordx4 a[236:239], v32, s[56:59], s60 offen offset:0 sc1\n" \
"buffer_load_dwordx4 a[224:227], v20, s[56:59], s60 offen offset:1024 sc1\n" \
"buffer_load_dwordx4 a[240:243], v32, s[56:59], s60 offen offset:1024 sc1\n" \
"buffer_load_dwordx4 a[228:231], v20, s[56:59], s60 offen offset:2048 sc1\n" \
"buffer_load_dwordx4 a[244:247], v32, s[56:59], s60 offen offset:2048 sc1\n" \
"buffer_load_dwordx4 a[232:235], v20, s[56:59], s60 offen offset:3072 sc1\n" \
"buffer_load_dwordx4 a[248:251], v32, s[56:59], s60 offen offset:3072 sc1\n" \
"buffer_load_dwordx4 a[96:99], v20, s[56:59], s96 offen offset:0 sc1\n" \
"buffer_load_dwordx4 a[104:107], v32, s[56:59], s96 offen offset:0 sc1\n" \
"buffer_load_dwordx4 a[112:115], v20, s[56:59], s97 offen offset:0 sc1\n" \
"buffer_load_dwordx4 a[120:123], v32, s[56:59], s97 offen offset:0 sc1\n" \
"buffer_load_dwordx4 a[100:103], v20, s[56:59], s96 offen offset:1024 sc1\n" \
"buffer_load_dwordx4 a[108:111], v32, s[56:59], s96 offen offset:1024 sc1\n" \
"buffer_load_dwordx4 a[116:119], v20, s[56:59], s97 offen offset:1024 sc1\n" \
"buffer_load_dwordx4 a[124:127], v32, s[56:59], s97 offen offset:1024 sc1\n" \
"buffer_load_dwordx4 v[64:67], v20, s[56:59], s61 offen offset:0 sc1\n" \
"buffer_load_dwordx4 v[68:71], v20, s[56:59], s61 offen offset:1024 sc1\n" \
"buffer_load_dwordx4 v[72:75], v20, s[56:59], s61 offen offset:2048 sc1\n" \
"buffer_load_dwordx4 v[76:79], v20, s[56:59], s61 offen offset:3072 sc1\n" \
"buffer_load_dwordx4 v[80:83], v32, s[56:59], s61 offen offset:0 sc1\n" \
"buffer_load_dwordx4 v[84:87], v32, s[56:59], s61 offen offset:1024 sc1\n" \
"buffer_load_dwordx4 v[96:99], v32, s[56:59], s61 offen offset:2048 sc1\n" \
"buffer_load_dwordx4 v[100:103], v32, s[56:59], s61 offen offset:3072 sc1\n" \
"s_waitcnt vmcnt(0)\n" \
"s_branch L_out_%=\n" \
"L_sw_%=:\n" \
"v_mfma_f32_16x16x32_bf16 v[104:107], a[128:131], a[16:19], v[36:39]\n" \
"s_add_u32 s68, s60, 0xfff6e000\n" \
"s_add_u32 s69, s60, 0xfffb7000\n" \
"s_add_u32 s70, s60, 0x0\n" \
"s_add_u32 s71, s60, 0x49000\n" \
"v_mfma_f32_16x16x32_bf16 v[104:107], a[132:135], a[20:23], v[104:107]\n" \
"s_xor_b32 m0, s65, 0x12000\n" \
"ds_read_b128 a[160:163], v49 offset:0\n" \
"v_mfma_f32_16x16x32_bf16 v[104:107], a[136:139], a[24:27], v[104:107]\n" \
"buffer_load_dwordx4 v50, s[8:11], s64 offen offset:0 sc1 lds\n" \
"v_mfma_f32_16x16x32_bf16 v[104:107], a[140:143], a[28:31], v[104:107]\n" \
"ds_read_b128 a[164:167], v49 offset:1024\n" \
"ds_read_b128 a[168:171], v49 offset:2048\n" \
"v_mfma_f32_16x16x32_bf16 v[108:111], a[156:159], a[28:31], v[36:39]\n" \
"ds_read_b128 a[172:175], v49 offset:3072\n" \
"ds_read_b128 a[176:179], v49 offset:4096\n" \
"v_mfma_f32_16x16x32_bf16 v[108:111], a[152:155], a[24:27], v[108:111]\n" \
"buffer_load_dwordx4 v50, s[8:11], s64 offen offset:1024 sc1 lds\n" \
"v_mfma_f32_16x16x32_bf16 v[108:111], a[148:151], a[20:23], v[108:111]\n" \
"s_mov_b32 s65, s13\n" \
"v_add_u32_e32 v63, s15, v20\n" \
"v_mfma_f32_16x16x32_bf16 v[108:111], a[144:147], a[16:19], v[108:111]\n" \
"s_add_u32 s67, s14, 40960\n" \
"v_add_u32_e32 v50, 0x6000, v63\n" \
"v_mfma_f32_16x16x32_bf16 v[116:119], a[144:147], a[32:35], v[40:43]\n" \
"s_xor_b32 m0, s65, 0x8000\n" \
"ds_read_b128 a[180:183], v49 offset:5120\n" \
"v_mfma_f32_16x16x32_bf16 v[116:119], a[148:151], a[36:39], v[116:119]\n" \
"ds_read_b128 a[184:187], v49 offset:6144\n" \
"v_and_b32_e32 v68, v12, v68\n" \
"v_mfma_f32_16x16x32_bf16 v[116:119], a[152:155], a[40:43], v[116:119]\n" \
"v_and_b32_e32 v69, v13, v69\n" \
"v_and_b32_e32 v70, v14, v70\n" \
"v_mfma_f32_16x16x32_bf16 v[116:119], a[156:159], a[44:47], v[116:119]\n" \
"buffer_load_dwordx4 v50, s[8:11], s67 offen offset:0 sc1 lds\n" \
"v_mfma_f32_16x16x32_bf16 v[112:115], a[140:143], a[44:47], v[40:43]\n" \
"v_and_b32_e32 v71, v15, v71\n" \
"ds_read_b128 a[188:191], v49 offset:7168\n" \
"v_mfma_f32_16x16x32_bf16 v[112:115], a[136:139], a[40:43], v[112:115]\n" \
"v_exp_f32_e32 v86, v86\n" \
"v_mfma_f32_16x16x32_bf16 v[112:115], a[132:135], a[36:39], v[112:115]\n" \
"v_exp_f32_e32 v87, v87\n" \
"v_mfma_f32_16x16x32_bf16 v[112:115], a[128:131], a[32:35], v[112:115]\n" \
"v_exp_f32_e32 v84, v84\n" \
"v_mfma_f32_16x16x32_bf16 v[120:123], a[128:131], a[48:51], v[44:47]\n" \
"buffer_load_dwordx4 v50, s[8:11], s67 offen offset:1024 sc1 lds\n" \
"v_mfma_f32_16x16x32_bf16 v[120:123], a[132:135], a[52:55], v[120:123]\n" \
"v_exp_f32_e32 v85, v85\n" \
"v_mfma_f32_16x16x32_bf16 v[120:123], a[136:139], a[56:59], v[120:123]\n" \
"v_cvt_pk_bf16_f32 v87, v86, v87\n" \
"v_cvt_pk_bf16_f32 v86, v84, v85\n" \
"v_mfma_f32_16x16x32_bf16 v[120:123], a[140:143], a[60:63], v[120:123]\n" \
"v_exp_f32_e32 v82, v82\n" \
"v_mfma_f32_16x16x32_bf16 v[124:127], a[156:159], a[60:63], v[44:47]\n" \
"v_exp_f32_e32 v83, v83\n" \
"v_mfma_f32_16x16x32_bf16 v[124:127], a[152:155], a[56:59], v[124:127]\n" \
"v_exp_f32_e32 v80, v80\n" \
"v_mfma_f32_16x16x32_bf16 v[124:127], a[148:151], a[52:55], v[124:127]\n" \
"buffer_load_dwordx4 v50, s[8:11], s67 offen offset:2048 sc1 lds\n" \
"v_mfma_f32_16x16x32_bf16 v[124:127], a[144:147], a[48:51], v[124:127]\n" \
"v_cvt_pk_bf16_f32 v85, v82, v83\n" \
"s_waitcnt lgkmcnt(1)\n" \
"v_mfma_f32_16x16x32_bf16 v[128:131], a[160:163], v[68:71], v[128:131]\n" \
"v_exp_f32_e32 v81, v81\n" \
"v_mfma_f32_16x16x32_bf16 v[160:163], a[160:163], v[72:75], v[160:163]\n" \
"v_cvt_pk_bf16_f32 v84, v80, v81\n" \
"s_mov_b32 s64, s14\n" \
"v_mfma_f32_16x16x32_bf16 v[132:135], a[164:167], v[68:71], v[132:135]\n" \
"v_exp_f32_e32 v88, v88\n" \
"v_mfma_f32_16x16x32_bf16 v[164:167], a[164:167], v[72:75], v[164:167]\n" \
"v_exp_f32_e32 v89, v89\n" \
"v_mfma_f32_16x16x32_bf16 v[136:139], a[168:171], v[68:71], v[136:139]\n" \
"buffer_load_dwordx4 v50, s[8:11], s67 offen offset:3072 sc1 lds\n" \
"v_mfma_f32_16x16x32_bf16 v[168:171], a[168:171], v[72:75], v[168:171]\n" \
"v_exp_f32_e32 v90, v90\n" \
"v_mfma_f32_16x16x32_bf16 v[140:143], a[172:175], v[68:71], v[140:143]\n" \
"v_exp_f32_e32 v91, v91\n" \
"v_mfma_f32_16x16x32_bf16 v[172:175], a[172:175], v[72:75], v[172:175]\n" \
"v_cvt_pk_bf16_f32 v88, v88, v89\n" \
"v_cvt_pk_bf16_f32 v89, v90, v91\n" \
"v_mfma_f32_16x16x32_bf16 v[144:147], a[176:179], v[68:71], v[144:147]\n" \
"v_exp_f32_e32 v92, v92\n" \
"v_mfma_f32_16x16x32_bf16 v[176:179], a[176:179], v[72:75], v[176:179]\n" \
"v_exp_f32_e32 v93, v93\n" \
"v_mfma_f32_16x16x32_bf16 v[148:151], a[180:183], v[68:71], v[148:151]\n" \
"v_exp_f32_e32 v94, v94\n" \
"v_mfma_f32_16x16x32_bf16 v[180:183], a[180:183], v[72:75], v[180:183]\n" \
"v_exp_f32_e32 v95, v95\n" \
"v_mfma_f32_16x16x32_bf16 v[152:155], a[184:187], v[68:71], v[152:155]\n" \
"v_cvt_pk_bf16_f32 v90, v92, v93\n" \
"v_cvt_pk_bf16_f32 v91, v94, v95\n" \
"v_mfma_f32_16x16x32_bf16 v[184:187], a[184:187], v[72:75], v[184:187]\n" \
"v_add_u32_e32 v51, 0x12000, v63\n" \
"s_waitcnt lgkmcnt(0)\n" \
"v_mfma_f32_16x16x32_bf16 v[156:159], a[188:191], v[68:71], v[156:159]\n" \
"s_xor_b32 m0, s65, 0xa000\n" \
"ds_read_b32 v58, v18 offset:512\n" \
"v_mfma_f32_16x16x32_bf16 v[188:191], a[188:191], v[72:75], v[188:191]\n" \
"buffer_load_dwordx4 v51, s[8:11], s64 offen offset:0 sc1 lds\n" \
"v_smfmac_f32_16x16x64_bf16 a[192:195], a[200:203], v[68:75], v21\n" \
"buffer_load_dwordx4 v51, s[8:11], s64 offen offset:1024 sc1 lds\n" \
"v_mfma_f32_16x16x32_bf16 v[220:223], a[188:191], v[84:87], v[220:223]\n" \
"ds_read_b32 v59, v18 offset:640\n" \
"ds_read_b32 v60, v18 offset:768\n" \
"v_mfma_f32_16x16x32_bf16 v[252:255], a[188:191], v[88:91], v[252:255]\n" \
"ds_read_b32 v61, v18 offset:896\n" \
"ds_read_b128 a[128:131], v48 offset:0\n" \
"v_mfma_f32_16x16x32_bf16 v[216:219], a[184:187], v[84:87], v[216:219]\n" \
"ds_read_b128 a[144:147], v48 offset:4096\n" \
"ds_read_b128 a[132:135], v48 offset:1024\n" \
"v_mfma_f32_16x16x32_bf16 v[248:251], a[184:187], v[88:91], v[248:251]\n" \
"ds_read_b128 a[148:151], v48 offset:5120\n" \
"ds_read_b128 a[136:139], v48 offset:2048\n" \
"v_mfma_f32_16x16x32_bf16 v[212:215], a[180:183], v[84:87], v[212:215]\n" \
"ds_read_b128 a[152:155], v48 offset:6144\n" \
"ds_read_b128 a[140:143], v48 offset:3072\n" \
"v_mfma_f32_16x16x32_bf16 v[244:247], a[180:183], v[88:91], v[244:247]\n" \
"ds_read_b128 a[156:159], v48 offset:7168\n" \
"v_add_u32_e32 v52, 0x14000, v63\n" \
"v_mfma_f32_16x16x32_bf16 v[208:211], a[176:179], v[84:87], v[208:211]\n" \
"buffer_load_dwordx4 v51, s[8:11], s64 offen offset:2048 sc1 lds\n" \
"v_mfma_f32_16x16x32_bf16 v[240:243], a[176:179], v[88:91], v[240:243]\n" \
"v_exp_f32_e32 v104, v104\n" \
"v_mfma_f32_16x16x32_bf16 v[204:207], a[172:175], v[84:87], v[204:207]\n" \
"v_exp_f32_e32 v105, v105\n" \
"v_mfma_f32_16x16x32_bf16 v[236:239], a[172:175], v[88:91], v[236:239]\n" \
"buffer_load_dwordx4 v51, s[8:11], s64 offen offset:3072 sc1 lds\n" \
"v_mfma_f32_16x16x32_bf16 v[200:203], a[168:171], v[84:87], v[200:203]\n" \
"v_exp_f32_e32 v106, v106\n" \
"v_mfma_f32_16x16x32_bf16 v[232:235], a[168:171], v[88:91], v[232:235]\n" \
"v_exp_f32_e32 v107, v107\n" \
"v_mfma_f32_16x16x32_bf16 v[196:199], a[164:167], v[84:87], v[196:199]\n" \
"v_cvt_pk_bf16_f32 v104, v104, v105\n" \
"v_cvt_pk_bf16_f32 v105, v106, v107\n" \
"v_mfma_f32_16x16x32_bf16 v[228:231], a[164:167], v[88:91], v[228:231]\n" \
"v_exp_f32_e32 v108, v108\n" \
"v_mfma_f32_16x16x32_bf16 v[192:195], a[160:163], v[84:87], v[192:195]\n" \
"v_exp_f32_e32 v109, v109\n" \
"v_mfma_f32_16x16x32_bf16 v[224:227], a[160:163], v[88:91], v[224:227]\n" \
"v_exp_f32_e32 v110, v110\n" \
"v_smfmac_f32_16x16x64_bf16 a[196:199], a[200:203], v[84:91], v21\n" \
"v_exp_f32_e32 v111, v111\n" \
"v_cvt_pk_bf16_f32 v106, v108, v109\n" \
"v_cvt_pk_bf16_f32 v107, v110, v111\n" \
"v_and_b32_e32 v104, v12, v104\n" \
"v_and_b32_e32 v105, v13, v105\n" \
"v_and_b32_e32 v106, v14, v106\n" \
"v_and_b32_e32 v107, v15, v107\n" \
"v_add_u32_e32 v53, 0x20000, v63\n" \
"v_exp_f32_e32 v118, v118\n" \
"v_exp_f32_e32 v119, v119\n" \
"v_exp_f32_e32 v116, v116\n" \
"v_exp_f32_e32 v117, v117\n" \
"v_cvt_pk_bf16_f32 v119, v118, v119\n" \
"v_exp_f32_e32 v114, v114\n" \
"v_exp_f32_e32 v115, v115\n" \
"v_exp_f32_e32 v112, v112\n" \
"v_exp_f32_e32 v113, v113\n" \
"v_accvgpr_read_b32 v54, a192\n" \
"v_accvgpr_read_b32 v22, a193\n" \
"v_cvt_pk_bf16_f32 v118, v116, v117\n" \
"v_cvt_pk_bf16_f32 v117, v114, v115\n" \
"v_cvt_pk_bf16_f32 v116, v112, v113\n" \
"v_readlane_b32 s93, v11, 19\n" \
"s_waitcnt vmcnt(10) lgkmcnt(0)\n" \
"s_barrier\n" \
"v_mfma_f32_16x16x32_bf16 v[80:83], a[128:131], a[32:35], v[40:43]\n" \
"ds_read_b128 a[160:163], v49 offset:8192\n" \
"ds_read_b128 a[164:167], v49 offset:9216\n" \
"v_mfma_f32_16x16x32_bf16 v[80:83], a[132:135], a[36:39], v[80:83]\n" \
"ds_read_b128 a[168:171], v49 offset:10240\n" \
"ds_read_b128 a[172:175], v49 offset:11264\n" \
"v_mfma_f32_16x16x32_bf16 v[80:83], a[136:139], a[40:43], v[80:83]\n" \
"s_xor_b32 m0, s65, 0x18000\n" \
"ds_read_b128 a[176:179], v49 offset:12288\n" \
"v_mfma_f32_16x16x32_bf16 v[80:83], a[140:143], a[44:47], v[80:83]\n" \
"buffer_load_dwordx4 v52, s[8:11], s64 offen offset:0 sc1 lds\n" \
"v_mfma_f32_16x16x32_bf16 v[84:87], a[156:159], a[44:47], v[40:43]\n" \
"ds_read_b128 a[180:183], v49 offset:13312\n" \
"ds_read_b128 a[184:187], v49 offset:14336\n" \
"v_mfma_f32_16x16x32_bf16 v[84:87], a[152:155], a[40:43], v[84:87]\n" \
"buffer_load_dwordx4 v52, s[8:11], s64 offen offset:1024 sc1 lds\n" \
"v_mfma_f32_16x16x32_bf16 v[84:87], a[148:151], a[36:39], v[84:87]\n" \
"ds_read_b128 a[188:191], v49 offset:15360\n" \
"v_readlane_b32 s80, v11, 11\n" \
"v_mfma_f32_16x16x32_bf16 v[84:87], a[144:147], a[32:35], v[84:87]\n" \
"buffer_load_dwordx4 v52, s[8:11], s64 offen offset:2048 sc1 lds\n" \
"v_mfma_f32_16x16x32_bf16 v[92:95], a[144:147], a[48:51], v[44:47]\n" \
"v_exp_f32_e32 v120, v120\n" \
"v_mfma_f32_16x16x32_bf16 v[92:95], a[148:151], a[52:55], v[92:95]\n" \
"v_exp_f32_e32 v121, v121\n" \
"v_mfma_f32_16x16x32_bf16 v[92:95], a[152:155], a[56:59], v[92:95]\n" \
"buffer_load_dwordx4 v52, s[8:11], s64 offen offset:3072 sc1 lds\n" \
"v_mfma_f32_16x16x32_bf16 v[92:95], a[156:159], a[60:63], v[92:95]\n" \
"s_mov_b32 m0, s65\n" \
"v_cvt_pk_bf16_f32 v120, v120, v121\n" \
"v_mfma_f32_16x16x32_bf16 v[88:91], a[140:143], a[60:63], v[44:47]\n" \
"v_exp_f32_e32 v122, v122\n" \
"v_mfma_f32_16x16x32_bf16 v[88:91], a[136:139], a[56:59], v[88:91]\n" \
"buffer_load_dwordx4 v53, s[8:11], s64 offen offset:0 sc1 lds\n" \
"v_mfma_f32_16x16x32_bf16 v[88:91], a[132:135], a[52:55], v[88:91]\n" \
"v_exp_f32_e32 v123, v123\n" \
"v_mfma_f32_16x16x32_bf16 v[88:91], a[128:131], a[48:51], v[88:91]\n" \
"v_cvt_pk_bf16_f32 v121, v122, v123\n" \
"s_waitcnt lgkmcnt(4)\n" \
"v_mfma_f32_16x16x32_bf16 v[160:163], a[160:163], v[104:107], v[160:163]\n" \
"buffer_load_dwordx4 v53, s[8:11], s64 offen offset:1024 sc1 lds\n" \
"v_mfma_f32_16x16x32_bf16 v[164:167], a[164:167], v[104:107], v[164:167]\n" \
"v_exp_f32_e32 v124, v124\n" \
"v_mfma_f32_16x16x32_bf16 v[168:171], a[168:171], v[104:107], v[168:171]\n" \
"v_exp_f32_e32 v125, v125\n" \
"v_mfma_f32_16x16x32_bf16 v[172:175], a[172:175], v[104:107], v[172:175]\n" \
"v_cvt_pk_bf16_f32 v122, v124, v125\n" \
"s_waitcnt lgkmcnt(1)\n" \
"v_mfma_f32_16x16x32_bf16 v[176:179], a[176:179], v[104:107], v[176:179]\n" \
"v_exp_f32_e32 v126, v126\n" \
"v_mfma_f32_16x16x32_bf16 v[180:183], a[180:183], v[104:107], v[180:183]\n" \
"v_exp_f32_e32 v127, v127\n" \
"v_mfma_f32_16x16x32_bf16 v[184:187], a[184:187], v[104:107], v[184:187]\n" \
"v_cvt_pk_bf16_f32 v123, v126, v127\n" \
"s_waitcnt lgkmcnt(0)\n" \
"v_mfma_f32_16x16x32_bf16 v[188:191], a[188:191], v[104:107], v[188:191]\n" \
"buffer_load_dwordx4 v53, s[8:11], s64 offen offset:2048 sc1 lds\n" \
"v_mfma_f32_16x16x32_bf16 a[192:195], a[212:215], v[104:107], a[192:195]\n" \
"buffer_load_dwordx4 v53, s[8:11], s64 offen offset:3072 sc1 lds\n" \
"v_mfma_f32_16x16x32_bf16 v[220:223], a[188:191], v[116:119], v[220:223]\n" \
"v_readlane_b32 s81, v11, 12\n" \
"s_mov_b32 s82, 0x47fca0\n" \
"v_mfma_f32_16x16x32_bf16 v[252:255], a[188:191], v[120:123], v[252:255]\n" \
"s_mov_b32 s83, 0x20000\n" \
"v_readlane_b32 s92, v11, 24\n" \
"v_mfma_f32_16x16x32_bf16 v[216:219], a[184:187], v[116:119], v[216:219]\n" \
"s_add_u32 m0, s93, 0x0\n" \
"ds_read_b128 a[128:131], v48 offset:8192\n" \
"v_mfma_f32_16x16x32_bf16 v[248:251], a[184:187], v[120:123], v[248:251]\n" \
"ds_read_b128 a[144:147], v48 offset:12288\n" \
"ds_read_b128 a[132:135], v48 offset:9216\n" \
"v_mfma_f32_16x16x32_bf16 v[212:215], a[180:183], v[116:119], v[212:215]\n" \
"buffer_load_dwordx4 v0, s[80:83], s92 offen offset:0 lds\n" \
"v_mfma_f32_16x16x32_bf16 v[244:247], a[180:183], v[120:123], v[244:247]\n" \
"buffer_load_dwordx4 v1, s[80:83], s92 offen offset:1024 lds\n" \
"v_mfma_f32_16x16x32_bf16 v[208:211], a[176:179], v[116:119], v[208:211]\n" \
"ds_read_b128 a[148:151], v48 offset:13312\n" \
"ds_read_b128 a[136:139], v48 offset:10240\n" \
"v_mfma_f32_16x16x32_bf16 v[240:243], a[176:179], v[120:123], v[240:243]\n" \
"ds_read_b128 a[152:155], v48 offset:14336\n" \
"ds_read_b128 a[140:143], v48 offset:11264\n" \
"v_mfma_f32_16x16x32_bf16 v[204:207], a[172:175], v[116:119], v[204:207]\n" \
"buffer_load_dwordx4 v2, s[80:83], s92 offen offset:2048 lds\n" \
"v_mfma_f32_16x16x32_bf16 v[236:239], a[172:175], v[120:123], v[236:239]\n" \
"ds_read_b128 a[156:159], v48 offset:15360\n" \
"s_xor_b32 s65, s65, 0x8000\n" \
"v_mfma_f32_16x16x32_bf16 v[200:203], a[168:171], v[116:119], v[200:203]\n" \
"s_add_u32 s64, s64, 0x10000\n" \
"v_add_u32_e32 v49, 0x10000, v48\n" \
"v_mfma_f32_16x16x32_bf16 v[232:235], a[168:171], v[120:123], v[232:235]\n" \
"buffer_load_dwordx4 v3, s[80:83], s92 offen offset:3072 lds\n" \
"v_mfma_f32_16x16x32_bf16 v[196:199], a[164:167], v[116:119], v[196:199]\n" \
"v_exp_f32_e32 v86, v86\n" \
"v_mfma_f32_16x16x32_bf16 v[228:231], a[164:167], v[120:123], v[228:231]\n" \
"v_exp_f32_e32 v87, v87\n" \
"v_mfma_f32_16x16x32_bf16 v[192:195], a[160:163], v[116:119], v[192:195]\n" \
"v_exp_f32_e32 v84, v84\n" \
"v_mfma_f32_16x16x32_bf16 v[224:227], a[160:163], v[120:123], v[224:227]\n" \
"v_exp_f32_e32 v85, v85\n" \
"v_smfmac_f32_16x16x64_bf16 a[196:199], a[200:203], v[116:123], v21\n" \
"v_cvt_pk_bf16_f32 v87, v86, v87\n" \
"v_exp_f32_e32 v82, v82\n" \
"v_exp_f32_e32 v83, v83\n" \
"v_exp_f32_e32 v80, v80\n" \
"v_exp_f32_e32 v81, v81\n" \
"v_cvt_pk_bf16_f32 v86, v84, v85\n" \
"v_cvt_pk_bf16_f32 v85, v82, v83\n" \
"v_cvt_pk_bf16_f32 v84, v80, v81\n" \
"v_and_b32_e32 v84, v12, v84\n" \
"v_and_b32_e32 v85, v13, v85\n" \
"v_and_b32_e32 v86, v14, v86\n" \
"v_and_b32_e32 v87, v15, v87\n" \
"v_exp_f32_e32 v88, v88\n" \
"v_exp_f32_e32 v89, v89\n" \
"v_exp_f32_e32 v90, v90\n" \
"v_exp_f32_e32 v91, v91\n" \
"v_cvt_pk_bf16_f32 v88, v88, v89\n" \
"v_exp_f32_e32 v92, v92\n" \
"v_exp_f32_e32 v93, v93\n" \
"v_exp_f32_e32 v94, v94\n" \
"v_exp_f32_e32 v95, v95\n" \
"v_cvt_pk_bf16_f32 v89, v90, v91\n" \
"v_cvt_pk_bf16_f32 v90, v92, v93\n" \
"v_cvt_pk_bf16_f32 v91, v94, v95\n" \
"v_add_u32_e32 v48, s16, v20\n" \
"v_add_f32_e32 v54, v54, v22\n" \
"v_accvgpr_read_b32 v55, a194\n" \
"v_accvgpr_read_b32 v23, a195\n" \
"v_mfma_f32_16x16x32_bf16 a[192:195], a[216:219], a[216:219], 0\n" \
"v_max_f32_e32 v62, v62, v54\n" \
"v_add_f32_e32 v55, v55, v23\n" \
"v_max_f32_e32 v62, v62, v55\n" \
"v_rcp_f32_e32 v54, v54\n" \
"v_rcp_f32_e32 v55, v55\n" \
"v_mul_f32_e32 v54, v54, v58\n" \
"v_mul_f32_e32 v55, v55, v59\n" \
"v_mul_f32_e32 v128, v54, v128\n" \
"v_mul_f32_e32 v129, v54, v129\n" \
"v_mul_f32_e32 v136, v54, v136\n" \
"v_mul_f32_e32 v137, v54, v137\n" \
"v_mul_f32_e32 v144, v54, v144\n" \
"v_mul_f32_e32 v145, v54, v145\n" \
"v_mul_f32_e32 v152, v54, v152\n" \
"v_mul_f32_e32 v153, v54, v153\n" \
"v_mul_f32_e32 v160, v55, v160\n" \
"v_mul_f32_e32 v161, v55, v161\n" \
"v_mul_f32_e32 v168, v55, v168\n" \
"v_mul_f32_e32 v169, v55, v169\n" \
"v_mul_f32_e32 v176, v55, v176\n" \
"v_mul_f32_e32 v177, v55, v177\n" \
"v_mul_f32_e32 v184, v55, v184\n" \
"v_mul_f32_e32 v185, v55, v185\n" \
"v_cvt_pk_bf16_f32 v128, v128, v129\n" \
"v_mul_f32_e32 v130, v54, v130\n" \
"v_mul_f32_e32 v131, v54, v131\n" \
"v_cvt_pk_bf16_f32 v136, v136, v137\n" \
"v_mul_f32_e32 v138, v54, v138\n" \
"v_mul_f32_e32 v139, v54, v139\n" \
"v_cvt_pk_bf16_f32 v144, v144, v145\n" \
"v_mul_f32_e32 v146, v54, v146\n" \
"v_mul_f32_e32 v147, v54, v147\n" \
"v_cvt_pk_bf16_f32 v152, v152, v153\n" \
"v_mul_f32_e32 v154, v54, v154\n" \
"v_mul_f32_e32 v155, v54, v155\n" \
"v_cvt_pk_bf16_f32 v160, v160, v161\n" \
"v_mul_f32_e32 v162, v55, v162\n" \
"v_mul_f32_e32 v163, v55, v163\n" \
"v_cvt_pk_bf16_f32 v168, v168, v169\n" \
"v_mul_f32_e32 v170, v55, v170\n" \
"v_mul_f32_e32 v171, v55, v171\n" \
"v_cvt_pk_bf16_f32 v176, v176, v177\n" \
"v_mul_f32_e32 v178, v55, v178\n" \
"v_mul_f32_e32 v179, v55, v179\n" \
"v_cvt_pk_bf16_f32 v184, v184, v185\n" \
"v_mul_f32_e32 v186, v55, v186\n" \
"v_mul_f32_e32 v187, v55, v187\n" \
"v_cvt_pk_bf16_f32 v129, v130, v131\n" \
"v_mul_f32_e32 v132, v54, v132\n" \
"v_mul_f32_e32 v133, v54, v133\n" \
"v_mul_f32_e32 v134, v54, v134\n" \
"v_mul_f32_e32 v135, v54, v135\n" \
"v_cvt_pk_bf16_f32 v137, v138, v139\n" \
"v_mul_f32_e32 v140, v54, v140\n" \
"s_waitcnt vmcnt(12) lgkmcnt(0)\n" \
"s_barrier\n" \
"v_mfma_f32_16x16x32_bf16 v[120:123], a[128:131], a[48:51], v[44:47]\n" \
"s_xor_b32 m0, s65, 0x12000\n" \
"ds_read_b128 a[188:191], v49 offset:7168\n" \
"v_mfma_f32_16x16x32_bf16 v[120:123], a[132:135], a[52:55], v[120:123]\n" \
"buffer_load_dwordx4 v50, s[8:11], s64 offen offset:0 sc1 lds\n" \
"v_mfma_f32_16x16x32_bf16 v[120:123], a[136:139], a[56:59], v[120:123]\n" \
"ds_read_b128 a[184:187], v49 offset:6144\n" \
"ds_read_b128 a[180:183], v49 offset:5120\n" \
"v_mfma_f32_16x16x32_bf16 v[120:123], a[140:143], a[60:63], v[120:123]\n" \
"buffer_load_dwordx4 v50, s[8:11], s64 offen offset:1024 sc1 lds\n" \
"v_mfma_f32_16x16x32_bf16 v[124:127], a[156:159], a[60:63], v[44:47]\n" \
"ds_read_b128 a[176:179], v49 offset:4096\n" \
"ds_read_b128 a[172:175], v49 offset:3072\n" \
"v_mfma_f32_16x16x32_bf16 v[124:127], a[152:155], a[56:59], v[124:127]\n" \
"ds_read_b128 a[168:171], v49 offset:2048\n" \
"ds_read_b128 a[164:167], v49 offset:1024\n" \
"v_mfma_f32_16x16x32_bf16 v[124:127], a[148:151], a[52:55], v[124:127]\n" \
"ds_read_b128 a[160:163], v49 offset:0\n" \
"ds_read_b128 a[0:3], v49 offset:8192\n" \
"v_mfma_f32_16x16x32_bf16 v[124:127], a[144:147], a[48:51], v[124:127]\n" \
"buffer_load_dwordx4 v50, s[8:11], s64 offen offset:2048 sc1 lds\n" \
"v_mfma_f32_16x16x32_bf16 v[32:35], a[220:223], a[64:67], 0\n" \
"ds_read_b128 a[4:7], v49 offset:9216\n" \
"ds_read_b128 a[8:11], v49 offset:10240\n" \
"v_mfma_f32_16x16x32_bf16 v[36:39], a[220:223], a[80:83], 0\n" \
"ds_read_b128 a[12:15], v49 offset:11264\n" \
"ds_read_b128 a[16:19], v49 offset:12288\n" \
"v_mfma_f32_16x16x32_bf16 v[40:43], a[220:223], a[96:99], 0\n" \
"ds_read_b128 a[20:23], v49 offset:13312\n" \
"ds_read_b128 a[24:27], v49 offset:14336\n" \
"v_mfma_f32_16x16x32_bf16 v[44:47], a[220:223], a[112:115], 0\n" \
"buffer_load_dwordx4 v50, s[8:11], s64 offen offset:3072 sc1 lds\n" \
"v_mfma_f32_16x16x32_bf16 v[32:35], a[224:227], a[68:71], v[32:35]\n" \
"s_xor_b32 m0, s65, 0xa000\n" \
"ds_read_b128 a[28:31], v49 offset:15360\n" \
"v_mfma_f32_16x16x32_bf16 v[36:39], a[224:227], a[84:87], v[36:39]\n" \
"v_exp_f32_e32 v120, v120\n" \
"v_mfma_f32_16x16x32_bf16 v[40:43], a[224:227], a[100:103], v[40:43]\n" \
"buffer_load_dwordx4 v51, s[8:11], s64 offen offset:0 sc1 lds\n" \
"v_mfma_f32_16x16x32_bf16 v[44:47], a[224:227], a[116:119], v[44:47]\n" \
"v_exp_f32_e32 v121, v121\n" \
"v_mfma_f32_16x16x32_bf16 v[32:35], a[228:231], a[72:75], v[32:35]\n" \
"v_exp_f32_e32 v122, v122\n" \
"v_mfma_f32_16x16x32_bf16 v[36:39], a[228:231], a[88:91], v[36:39]\n" \
"v_exp_f32_e32 v123, v123\n" \
"v_mfma_f32_16x16x32_bf16 v[40:43], a[228:231], a[104:107], v[40:43]\n" \
"buffer_load_dwordx4 v51, s[8:11], s64 offen offset:1024 sc1 lds\n" \
"v_mfma_f32_16x16x32_bf16 v[44:47], a[228:231], a[120:123], v[44:47]\n" \
"v_cvt_pk_bf16_f32 v120, v120, v121\n" \
"v_cvt_pk_bf16_f32 v121, v122, v123\n" \
"v_mfma_f32_16x16x32_bf16 v[32:35], a[232:235], a[76:79], v[32:35]\n" \
"v_exp_f32_e32 v124, v124\n" \
"v_mfma_f32_16x16x32_bf16 v[36:39], a[232:235], a[92:95], v[36:39]\n" \
"v_exp_f32_e32 v125, v125\n" \
"v_mfma_f32_16x16x32_bf16 v[40:43], a[232:235], a[108:111], v[40:43]\n" \
"buffer_load_dwordx4 v51, s[8:11], s64 offen offset:2048 sc1 lds\n" \
"v_mfma_f32_16x16x32_bf16 v[44:47], a[232:235], a[124:127], v[44:47]\n" \
"v_cvt_pk_bf16_f32 v122, v124, v125\n" \
"s_waitcnt lgkmcnt(3)\n" \
"v_mfma_f32_16x16x32_bf16 v[220:223], a[188:191], v[84:87], v[220:223]\n" \
"v_exp_f32_e32 v126, v126\n" \
"v_mfma_f32_16x16x32_bf16 v[252:255], a[188:191], v[88:91], v[252:255]\n" \
"buffer_load_dwordx4 v51, s[8:11], s64 offen offset:3072 sc1 lds\n" \
"v_mfma_f32_16x16x32_bf16 v[216:219], a[184:187], v[84:87], v[216:219]\n" \
"s_add_u32 m0, s93, 0x1000\n" \
"v_and_b32_e32 v120, v12, v120\n" \
"v_mfma_f32_16x16x32_bf16 v[248:251], a[184:187], v[88:91], v[248:251]\n" \
"v_exp_f32_e32 v127, v127\n" \
"v_mfma_f32_16x16x32_bf16 v[212:215], a[180:183], v[84:87], v[212:215]\n" \
"v_cvt_pk_bf16_f32 v123, v126, v127\n" \
"v_and_b32_e32 v121, v13, v121\n" \
"v_mfma_f32_16x16x32_bf16 v[244:247], a[180:183], v[88:91], v[244:247]\n" \
"buffer_load_dwordx4 v4, s[80:83], s92 offen offset:0 lds\n" \
"v_mfma_f32_16x16x32_bf16 v[208:211], a[176:179], v[84:87], v[208:211]\n" \
"v_and_b32_e32 v122, v14, v122\n" \
"v_and_b32_e32 v123, v15, v123\n" \
"v_mfma_f32_16x16x32_bf16 v[240:243], a[176:179], v[88:91], v[240:243]\n" \
"ds_read_b128 a[128:131], v48 offset:0\n" \
"ds_read_b128 a[144:147], v48 offset:4096\n" \
"v_mfma_f32_16x16x32_bf16 v[204:207], a[172:175], v[84:87], v[204:207]\n" \
"ds_read_b128 a[132:135], v48 offset:1024\n" \
"ds_read_b128 a[148:151], v48 offset:5120\n" \
"v_mfma_f32_16x16x32_bf16 v[236:239], a[172:175], v[88:91], v[236:239]\n" \
"buffer_load_dwordx4 v5, s[80:83], s92 offen offset:1024 lds\n" \
"v_mfma_f32_16x16x32_bf16 v[200:203], a[168:171], v[84:87], v[200:203]\n" \
"ds_read_b128 a[136:139], v48 offset:2048\n" \
"ds_read_b128 a[152:155], v48 offset:6144\n" \
"v_mfma_f32_16x16x32_bf16 v[232:235], a[168:171], v[88:91], v[232:235]\n" \
"ds_read_b128 a[140:143], v48 offset:3072\n" \
"ds_read_b128 a[156:159], v48 offset:7168\n" \
"v_mfma_f32_16x16x32_bf16 v[196:199], a[164:167], v[84:87], v[196:199]\n" \
"buffer_load_dwordx4 v6, s[80:83], s92 offen offset:2048 lds\n" \
"v_mfma_f32_16x16x32_bf16 v[228:231], a[164:167], v[88:91], v[228:231]\n" \
"v_mul_f32_e32 v141, v54, v141\n" \
"v_mul_f32_e32 v142, v54, v142\n" \
"v_mfma_f32_16x16x32_bf16 v[192:195], a[160:163], v[84:87], v[192:195]\n" \
"v_mul_f32_e32 v143, v54, v143\n" \
"v_cvt_pk_bf16_f32 v145, v146, v147\n" \
"v_mfma_f32_16x16x32_bf16 v[224:227], a[160:163], v[88:91], v[224:227]\n" \
"v_mul_f32_e32 v148, v54, v148\n" \
"v_mul_f32_e32 v149, v54, v149\n" \
"v_smfmac_f32_16x16x64_bf16 a[196:199], a[200:203], v[84:91], v21\n" \
"buffer_load_dwordx4 v7, s[80:83], s92 offen offset:3072 lds\n" \
"v_mfma_f32_16x16x32_bf16 v[224:227], a[0:3], v[120:123], v[224:227]\n" \
"v_mul_f32_e32 v150, v54, v150\n" \
"v_mul_f32_e32 v151, v54, v151\n" \
"v_mfma_f32_16x16x32_bf16 v[228:231], a[4:7], v[120:123], v[228:231]\n" \
"v_cvt_pk_bf16_f32 v153, v154, v155\n" \
"v_mul_f32_e32 v156, v54, v156\n" \
"v_mfma_f32_16x16x32_bf16 v[232:235], a[8:11], v[120:123], v[232:235]\n" \
"v_mul_f32_e32 v157, v54, v157\n" \
"v_mul_f32_e32 v158, v54, v158\n" \
"v_mfma_f32_16x16x32_bf16 v[236:239], a[12:15], v[120:123], v[236:239]\n" \
"v_mul_f32_e32 v159, v54, v159\n" \
"v_cvt_pk_bf16_f32 v161, v162, v163\n" \
"v_mfma_f32_16x16x32_bf16 v[240:243], a[16:19], v[120:123], v[240:243]\n" \
"v_mul_f32_e32 v164, v55, v164\n" \
"s_waitcnt lgkmcnt(6)\n" \
"v_mfma_f32_16x16x32_bf16 v[244:247], a[20:23], v[120:123], v[244:247]\n" \
"v_mul_f32_e32 v165, v55, v165\n" \
"v_mul_f32_e32 v166, v55, v166\n" \
"v_mfma_f32_16x16x32_bf16 v[248:251], a[24:27], v[120:123], v[248:251]\n" \
"v_mul_f32_e32 v167, v55, v167\n" \
"v_cvt_pk_bf16_f32 v169, v170, v171\n" \
"v_mfma_f32_16x16x32_bf16 v[252:255], a[28:31], v[120:123], v[252:255]\n" \
"v_mul_f32_e32 v172, v55, v172\n" \
"v_mul_f32_e32 v173, v55, v173\n" \
"v_mfma_f32_16x16x32_bf16 a[196:199], a[212:215], v[120:123], a[196:199]\n" \
"v_mul_f32_e32 v174, v55, v174\n" \
"v_mul_f32_e32 v175, v55, v175\n" \
"v_cvt_pk_bf16_f32 v177, v178, v179\n" \
"v_mul_f32_e32 v180, v55, v180\n" \
"v_mul_f32_e32 v181, v55, v181\n" \
"v_mul_f32_e32 v182, v55, v182\n" \
"v_mul_f32_e32 v183, v55, v183\n" \
"v_cvt_pk_bf16_f32 v185, v186, v187\n" \
"v_mul_f32_e32 v188, v55, v188\n" \
"v_mul_f32_e32 v189, v55, v189\n" \
"v_mul_f32_e32 v190, v55, v190\n" \
"v_mul_f32_e32 v191, v55, v191\n" \
"v_cvt_pk_bf16_f32 v130, v132, v133\n" \
"v_cvt_pk_bf16_f32 v131, v134, v135\n" \
"buffer_store_dwordx4 v[128:131], v20, s[56:59], s68 offen offset:0 sc1\n" \
"v_cvt_pk_bf16_f32 v138, v140, v141\n" \
"v_cvt_pk_bf16_f32 v139, v142, v143\n" \
"buffer_store_dwordx4 v[136:139], v20, s[56:59], s68 offen offset:1024 sc1\n" \
"v_cvt_pk_bf16_f32 v146, v148, v149\n" \
"v_cvt_pk_bf16_f32 v147, v150, v151\n" \
"buffer_store_dwordx4 v[144:147], v20, s[56:59], s68 offen offset:2048 sc1\n" \
"v_cvt_pk_bf16_f32 v154, v156, v157\n" \
"v_cvt_pk_bf16_f32 v155, v158, v159\n" \
"buffer_store_dwordx4 v[152:155], v20, s[56:59], s68 offen offset:3072 sc1\n" \
"v_cvt_pk_bf16_f32 v162, v164, v165\n" \
"v_cvt_pk_bf16_f32 v163, v166, v167\n" \
"buffer_store_dwordx4 v[160:163], v20, s[56:59], s69 offen offset:0 sc1\n" \
"v_cvt_pk_bf16_f32 v170, v172, v173\n" \
"v_cvt_pk_bf16_f32 v171, v174, v175\n" \
"buffer_store_dwordx4 v[168:171], v20, s[56:59], s69 offen offset:1024 sc1\n" \
"v_cvt_pk_bf16_f32 v178, v180, v181\n" \
"v_cvt_pk_bf16_f32 v179, v182, v183\n" \
"buffer_store_dwordx4 v[176:179], v20, s[56:59], s69 offen offset:2048 sc1\n" \
"v_cvt_pk_bf16_f32 v186, v188, v189\n" \
"v_cvt_pk_bf16_f32 v187, v190, v191\n" \
"buffer_store_dwordx4 v[184:187], v20, s[56:59], s69 offen offset:3072 sc1\n" \
"s_add_u32 m0, s93, 0x2000\n" \
"v_mfma_f32_16x16x32_bf16 a[208:211], a[216:219], v[128:131], a[208:211]\n" \
"buffer_load_dwordx4 v8, s[80:83], s92 offen offset:0 lds\n" \
"v_mfma_f32_16x16x32_bf16 a[208:211], a[216:219], v[136:139], a[208:211]\n" \
"v_mfma_f32_32x32x16_bf16 v[128:143], a[216:219], a[216:219], 0\n" \
"v_mfma_f32_16x16x32_bf16 a[208:211], a[216:219], v[144:147], a[208:211]\n" \
"v_mfma_f32_16x16x32_bf16 a[208:211], a[216:219], v[152:155], a[208:211]\n" \
"v_mfma_f32_32x32x16_bf16 v[144:159], a[216:219], a[216:219], 0\n" \
"v_accvgpr_read_b32 v56, a196\n" \
"v_accvgpr_read_b32 v24, a197\n" \
"v_add_f32_e32 v56, v56, v24\n" \
"v_mfma_f32_16x16x32_bf16 a[208:211], a[216:219], v[160:163], a[208:211]\n" \
"v_max_f32_e32 v62, v62, v56\n" \
"v_accvgpr_read_b32 v57, a198\n" \
"s_waitcnt vmcnt(21) lgkmcnt(0)\n" \
"s_barrier\n" \
"v_mfma_f32_16x16x32_bf16 v[64:67], a[128:131], a[64:67], v[32:35]\n" \
"s_xor_b32 m0, s65, 0x18000\n" \
"v_accvgpr_read_b32 v25, a199\n" \
"v_mfma_f32_16x16x32_bf16 a[196:199], a[216:219], a[216:219], 0\n" \
"buffer_load_dwordx4 v52, s[8:11], s64 offen offset:0 sc1 lds\n" \
"v_mfma_f32_16x16x32_bf16 v[64:67], a[132:135], a[68:71], v[64:67]\n" \
"v_rcp_f32_e32 v56, v56\n" \
"v_mfma_f32_16x16x32_bf16 v[64:67], a[136:139], a[72:75], v[64:67]\n" \
"v_mfma_f32_16x16x32_bf16 a[208:211], a[216:219], v[168:171], a[208:211]\n" \
"v_mfma_f32_32x32x16_bf16 v[160:175], a[216:219], a[216:219], 0\n" \
"v_mul_f32_e32 v56, v56, v60\n" \
"v_mfma_f32_16x16x32_bf16 a[208:211], a[216:219], v[176:179], a[208:211]\n" \
"v_mul_f32_e32 v192, v56, v192\n" \
"v_mul_f32_e32 v193, v56, v193\n" \
"v_mfma_f32_16x16x32_bf16 v[64:67], a[140:143], a[76:79], v[64:67]\n" \
"buffer_load_dwordx4 v52, s[8:11], s64 offen offset:1024 sc1 lds\n" \
"v_mfma_f32_16x16x32_bf16 v[68:71], a[156:159], a[76:79], v[32:35]\n" \
"v_cvt_pk_bf16_f32 v192, v192, v193\n" \
"v_mul_f32_e32 v194, v56, v194\n" \
"v_mfma_f32_16x16x32_bf16 v[68:71], a[152:155], a[72:75], v[68:71]\n" \
"v_mul_f32_e32 v195, v56, v195\n" \
"v_cvt_pk_bf16_f32 v193, v194, v195\n" \
"v_mfma_f32_16x16x32_bf16 v[68:71], a[148:151], a[68:71], v[68:71]\n" \
"v_mfma_f32_16x16x32_bf16 a[208:211], a[216:219], v[184:187], a[208:211]\n" \
"v_mfma_f32_32x32x16_bf16 v[176:191], a[216:219], a[216:219], 0\n" \
"v_mul_f32_e32 v196, v56, v196\n" \
"v_mul_f32_e32 v197, v56, v197\n" \
"v_mul_f32_e32 v198, v56, v198\n" \
"v_mul_f32_e32 v199, v56, v199\n" \
"v_mul_f32_e32 v200, v56, v200\n" \
"v_mul_f32_e32 v201, v56, v201\n" \
"v_mfma_f32_16x16x32_bf16 v[68:71], a[144:147], a[64:67], v[68:71]\n" \
"buffer_load_dwordx4 v52, s[8:11], s64 offen offset:2048 sc1 lds\n" \
"v_mfma_f32_16x16x32_bf16 v[76:79], a[144:147], a[80:83], v[36:39]\n" \
"v_cvt_pk_bf16_f32 v194, v196, v197\n" \
"v_cvt_pk_bf16_f32 v195, v198, v199\n" \
"buffer_store_dwordx4 v[192:195], v20, s[56:59], s70 offen offset:0 sc1\n" \
"v_mfma_f32_16x16x32_bf16 v[76:79], a[148:151], a[84:87], v[76:79]\n" \
"v_cvt_pk_bf16_f32 v200, v200, v201\n" \
"v_mul_f32_e32 v202, v56, v202\n" \
"v_mfma_f32_16x16x32_bf16 v[76:79], a[152:155], a[88:91], v[76:79]\n" \
"v_mul_f32_e32 v203, v56, v203\n" \
"v_add_f32_e32 v57, v57, v25\n" \
"v_mfma_f32_16x16x32_bf16 v[76:79], a[156:159], a[92:95], v[76:79]\n" \
"buffer_load_dwordx4 v52, s[8:11], s64 offen offset:3072 sc1 lds\n" \
"v_mfma_f32_16x16x32_bf16 v[72:75], a[140:143], a[92:95], v[36:39]\n" \
"s_mov_b32 m0, s65\n" \
"v_cvt_pk_bf16_f32 v201, v202, v203\n" \
"v_mfma_f32_16x16x32_bf16 v[72:75], a[136:139], a[88:91], v[72:75]\n" \
"v_mfma_f32_16x16x32_bf16 a[208:211], a[216:219], v[192:195], a[208:211]\n" \
"v_mul_f32_e32 v204, v56, v204\n" \
"v_mul_f32_e32 v205, v56, v205\n" \
"v_mfma_f32_16x16x32_bf16 v[72:75], a[132:135], a[84:87], v[72:75]\n" \
"v_mul_f32_e32 v206, v56, v206\n" \
"v_mul_f32_e32 v207, v56, v207\n" \
"v_mfma_f32_16x16x32_bf16 v[72:75], a[128:131], a[80:83], v[72:75]\n" \
"buffer_load_dwordx4 v53, s[8:11], s64 offen offset:0 sc1 lds\n" \
"v_mfma_f32_16x16x32_bf16 v[80:83], a[128:131], a[96:99], v[40:43]\n" \
"v_mul_f32_e32 v208, v56, v208\n" \
"v_mul_f32_e32 v209, v56, v209\n" \
"v_mfma_f32_16x16x32_bf16 v[80:83], a[132:135], a[100:103], v[80:83]\n" \
"v_max_f32_e32 v62, v62, v57\n" \
"v_cvt_pk_bf16_f32 v202, v204, v205\n" \
"v_mfma_f32_16x16x32_bf16 v[80:83], a[136:139], a[104:107], v[80:83]\n" \
"buffer_load_dwordx4 v53, s[8:11], s64 offen offset:1024 sc1 lds\n" \
"v_mfma_f32_16x16x32_bf16 v[80:83], a[140:143], a[108:111], v[80:83]\n" \
"v_cvt_pk_bf16_f32 v203, v206, v207\n" \
"v_cvt_pk_bf16_f32 v208, v208, v209\n" \
"buffer_store_dwordx4 v[200:203], v20, s[56:59], s70 offen offset:1024 sc1\n" \
"v_mfma_f32_16x16x32_bf16 v[84:87], a[156:159], a[108:111], v[40:43]\n" \
"v_mul_f32_e32 v210, v56, v210\n" \
"v_mul_f32_e32 v211, v56, v211\n" \
"v_mfma_f32_16x16x32_bf16 v[84:87], a[152:155], a[104:107], v[84:87]\n" \
"v_rcp_f32_e32 v57, v57\n" \
"v_mfma_f32_16x16x32_bf16 v[84:87], a[148:151], a[100:103], v[84:87]\n" \
"buffer_load_dwordx4 v53, s[8:11], s64 offen offset:2048 sc1 lds\n" \
"v_mfma_f32_16x16x32_bf16 v[84:87], a[144:147], a[96:99], v[84:87]\n" \
"v_mfma_f32_16x16x32_bf16 a[208:211], a[216:219], v[200:203], a[208:211]\n" \
"v_mfma_f32_32x32x16_bf16 v[192:207], a[216:219], a[216:219], 0\n" \
"v_cvt_pk_bf16_f32 v209, v210, v211\n" \
"v_mul_f32_e32 v212, v56, v212\n" \
"v_mul_f32_e32 v213, v56, v213\n" \
"v_mul_f32_e32 v214, v56, v214\n" \
"v_mul_f32_e32 v215, v56, v215\n" \
"v_mul_f32_e32 v216, v56, v216\n" \
"v_mfma_f32_16x16x32_bf16 v[92:95], a[144:147], a[112:115], v[44:47]\n" \
"ds_read_b128 a[144:147], v48 offset:12288\n" \
"v_mul_f32_e32 v217, v56, v217\n" \
"v_mfma_f32_16x16x32_bf16 v[92:95], a[148:151], a[116:119], v[92:95]\n" \
"ds_read_b128 a[148:151], v48 offset:13312\n" \
"v_cvt_pk_bf16_f32 v210, v212, v213\n" \
"v_mfma_f32_16x16x32_bf16 v[92:95], a[152:155], a[120:123], v[92:95]\n" \
"ds_read_b128 a[152:155], v48 offset:14336\n" \
"v_cvt_pk_bf16_f32 v211, v214, v215\n" \
"buffer_store_dwordx4 v[208:211], v20, s[56:59], s70 offen offset:2048 sc1\n" \
"v_mfma_f32_16x16x32_bf16 v[92:95], a[156:159], a[124:127], v[92:95]\n" \
"ds_read_b128 a[156:159], v48 offset:15360\n" \
"v_cvt_pk_bf16_f32 v216, v216, v217\n" \
"v_mfma_f32_16x16x32_bf16 v[88:91], a[140:143], a[124:127], v[44:47]\n" \
"ds_read_b128 a[140:143], v48 offset:11264\n" \
"v_mul_f32_e32 v218, v56, v218\n" \
"v_mfma_f32_16x16x32_bf16 v[88:91], a[136:139], a[120:123], v[88:91]\n" \
"ds_read_b128 a[136:139], v48 offset:10240\n" \
"v_mul_f32_e32 v219, v56, v219\n" \
"v_mfma_f32_16x16x32_bf16 v[88:91], a[132:135], a[116:119], v[88:91]\n" \
"ds_read_b128 a[132:135], v48 offset:9216\n" \
"v_mul_f32_e32 v57, v57, v61\n" \
"v_mfma_f32_16x16x32_bf16 v[88:91], a[128:131], a[112:115], v[88:91]\n" \
"ds_read_b128 a[128:131], v48 offset:8192\n" \
"v_mfma_f32_16x16x32_bf16 a[208:211], a[216:219], v[208:211], a[208:211]\n" \
"v_cvt_pk_bf16_f32 v217, v218, v219\n" \
"v_mul_f32_e32 v220, v56, v220\n" \
"v_mul_f32_e32 v221, v56, v221\n" \
"v_mul_f32_e32 v222, v56, v222\n" \
"v_mul_f32_e32 v223, v56, v223\n" \
"v_mul_f32_e32 v224, v57, v224\n" \
"v_mul_f32_e32 v225, v57, v225\n" \
"v_cvt_pk_bf16_f32 v218, v220, v221\n" \
"v_cvt_pk_bf16_f32 v219, v222, v223\n" \
"buffer_store_dwordx4 v[216:219], v20, s[56:59], s70 offen offset:3072 sc1\n" \
"v_cvt_pk_bf16_f32 v224, v224, v225\n" \
"v_mul_f32_e32 v226, v57, v226\n" \
"v_mul_f32_e32 v227, v57, v227\n" \
"v_mfma_f32_16x16x32_bf16 a[208:211], a[216:219], v[216:219], a[208:211]\n" \
"v_mfma_f32_32x32x16_bf16 v[208:223], a[216:219], a[216:219], 0\n" \
"v_cvt_pk_bf16_f32 v225, v226, v227\n" \
"v_mul_f32_e32 v228, v57, v228\n" \
"v_mul_f32_e32 v229, v57, v229\n" \
"v_mul_f32_e32 v230, v57, v230\n" \
"v_mul_f32_e32 v231, v57, v231\n" \
"v_mul_f32_e32 v232, v57, v232\n" \
"v_mul_f32_e32 v233, v57, v233\n" \
"v_cvt_pk_bf16_f32 v226, v228, v229\n" \
"v_cvt_pk_bf16_f32 v227, v230, v231\n" \
"buffer_store_dwordx4 v[224:227], v20, s[56:59], s71 offen offset:0 sc1\n" \
"v_cvt_pk_bf16_f32 v232, v232, v233\n" \
"v_mul_f32_e32 v234, v57, v234\n" \
"v_mul_f32_e32 v235, v57, v235\n" \
"v_mfma_f32_16x16x32_bf16 a[208:211], a[216:219], v[224:227], a[208:211]\n" \
"v_cvt_pk_bf16_f32 v233, v234, v235\n" \
"v_mul_f32_e32 v236, v57, v236\n" \
"v_mul_f32_e32 v237, v57, v237\n" \
"v_mul_f32_e32 v238, v57, v238\n" \
"v_mul_f32_e32 v239, v57, v239\n" \
"v_mul_f32_e32 v240, v57, v240\n" \
"v_mul_f32_e32 v241, v57, v241\n" \
"v_cvt_pk_bf16_f32 v234, v236, v237\n" \
"v_cvt_pk_bf16_f32 v235, v238, v239\n" \
"buffer_store_dwordx4 v[232:235], v20, s[56:59], s71 offen offset:1024 sc1\n" \
"v_cvt_pk_bf16_f32 v240, v240, v241\n" \
"v_mul_f32_e32 v242, v57, v242\n" \
"v_mul_f32_e32 v243, v57, v243\n" \
"v_mfma_f32_16x16x32_bf16 a[208:211], a[216:219], v[232:235], a[208:211]\n" \
"v_mfma_f32_32x32x16_bf16 v[224:239], a[216:219], a[216:219], 0\n" \
"v_cvt_pk_bf16_f32 v241, v242, v243\n" \
"v_mul_f32_e32 v244, v57, v244\n" \
"v_mul_f32_e32 v245, v57, v245\n" \
"v_mul_f32_e32 v246, v57, v246\n" \
"v_mul_f32_e32 v247, v57, v247\n" \
"v_mul_f32_e32 v248, v57, v248\n" \
"v_mul_f32_e32 v249, v57, v249\n" \
"v_cvt_pk_bf16_f32 v242, v244, v245\n" \
"v_cvt_pk_bf16_f32 v243, v246, v247\n" \
"buffer_store_dwordx4 v[240:243], v20, s[56:59], s71 offen offset:2048 sc1\n" \
"v_cvt_pk_bf16_f32 v248, v248, v249\n" \
"v_mul_f32_e32 v250, v57, v250\n" \
"v_mul_f32_e32 v251, v57, v251\n" \
"v_mfma_f32_16x16x32_bf16 a[208:211], a[216:219], v[240:243], a[208:211]\n" \
"v_cvt_pk_bf16_f32 v249, v250, v251\n" \
"v_mul_f32_e32 v252, v57, v252\n" \
"v_mul_f32_e32 v253, v57, v253\n" \
"v_mul_f32_e32 v254, v57, v254\n" \
"v_mul_f32_e32 v255, v57, v255\n" \
"v_cvt_pk_bf16_f32 v250, v252, v253\n" \
"v_cvt_pk_bf16_f32 v251, v254, v255\n" \
"buffer_store_dwordx4 v[248:251], v20, s[56:59], s71 offen offset:3072 sc1\n" \
"buffer_load_dwordx4 v53, s[8:11], s64 offen offset:3072 sc1 lds\n" \
"s_waitcnt vmcnt(16) lgkmcnt(0)\n" \
"s_barrier\n" \
"v_mfma_f32_16x16x32_bf16 a[208:211], a[216:219], v[248:251], a[208:211]\n" \
"v_mfma_f32_32x32x16_bf16 v[240:255], a[216:219], a[216:219], 0\n" \
"v_exp_f32_e32 v70, v70\n" \
"v_exp_f32_e32 v71, v71\n" \
"v_exp_f32_e32 v72, v72\n" \
"v_exp_f32_e32 v73, v73\n" \
"s_add_u32 s64, s64, 0x10000\n" \
"v_cmp_lt_f32_e64 s[72:73], s21, v62\n" \
"v_readlane_b32 s95, v11, 3\n" \
"v_accvgpr_read_b32 v63, a208\n" \
"v_cmp_u_f32_e32 vcc, v63, v63\n" \
"v_exp_f32_e32 v68, v68\n" \
"v_exp_f32_e32 v69, v69\n" \
"v_exp_f32_e32 v74, v74\n" \
"v_exp_f32_e32 v75, v75\n" \
"s_or_b64 vcc, s[72:73], vcc\n" \
"s_cselect_b32 s95, 0x40000000, s95\n" \
"s_cselect_b32 s94, 0x7f800000, 0\n" \
"v_cvt_pk_bf16_f32 v71, v70, v71\n" \
"v_exp_f32_e32 v66, v66\n" \
"v_exp_f32_e32 v67, v67\n" \
"v_exp_f32_e32 v64, v64\n" \
"v_exp_f32_e32 v65, v65\n" \
"v_cvt_pk_bf16_f32 v72, v72, v73\n" \
"v_exp_f32_e32 v76, v76\n" \
"v_exp_f32_e32 v77, v77\n" \
"v_exp_f32_e32 v78, v78\n" \
"v_exp_f32_e32 v79, v79\n" \
"v_cvt_pk_bf16_f32 v70, v68, v69\n" \
"v_cvt_pk_bf16_f32 v73, v74, v75\n" \
"v_add_u32_e32 v49, 0x10000, v48\n" \
"s_xor_b32 s65, s65, 0x8000\n" \
"v_cvt_pk_bf16_f32 v69, v66, v67\n" \
"v_cvt_pk_bf16_f32 v68, v64, v65\n" \
"v_cvt_pk_bf16_f32 v74, v76, v77\n" \
"v_cvt_pk_bf16_f32 v75, v78, v79\n" \
"v_xor_b32_e32 v48, 0x8000, v48\n" \
"s_sub_u32 s66, s19, 1\n" \
"s_cbranch_scc0 L_2_%=\n" \
"L_f1_%=:\n" \
"v_bfe_u32 v60, v20, 2, 7\n" \
"v_readlane_b32 s96, v11, 1\n" \
"ds_read_b128 a[160:163], v49 offset:0\n" \
"ds_read_b128 a[164:167], v49 offset:1024\n" \
"ds_read_b128 a[168:171], v49 offset:2048\n" \
"v_mfma_f32_16x16x32_bf16 v[104:107], a[128:131], a[80:83], v[36:39]\n" \
"s_add_u32 s68, s61, 0x0\n" \
"s_add_u32 s69, s61, 0x49000\n" \
"buffer_load_dword v57, v60, s[56:59], s96 offen sc1\n" \
"ds_read_b128 a[172:175], v49 offset:3072\n" \
"v_mfma_f32_16x16x32_bf16 v[104:107], a[132:135], a[84:87], v[104:107]\n" \
"ds_read_b128 a[176:179], v49 offset:4096\n" \
"ds_read_b128 a[180:183], v49 offset:5120\n" \
"v_mfma_f32_16x16x32_bf16 v[104:107], a[136:139], a[88:91], v[104:107]\n" \
"ds_read_b128 a[184:187], v49 offset:6144\n" \
"v_and_b32_e32 v68, v12, v68\n" \
"v_mfma_f32_16x16x32_bf16 v[104:107], a[140:143], a[92:95], v[104:107]\n" \
"v_and_b32_e32 v69, v13, v69\n" \
"v_and_b32_e32 v70, v14, v70\n" \
"v_mfma_f32_16x16x32_bf16 v[108:111], a[156:159], a[92:95], v[36:39]\n" \
"v_and_b32_e32 v71, v15, v71\n" \
"ds_read_b128 a[188:191], v49 offset:7168\n" \
"v_mfma_f32_16x16x32_bf16 v[108:111], a[152:155], a[88:91], v[108:111]\n" \
"v_mov_b32_e32 v61, s62\n" \
"ds_read_b128 a[0:3], v20 offset:8192\n" \
"v_mfma_f32_16x16x32_bf16 v[108:111], a[148:151], a[84:87], v[108:111]\n" \
"v_add_u32_e32 v56, 0x49000, v20\n" \
"ds_read_b128 a[4:7], v20 offset:9216\n" \
"v_mfma_f32_16x16x32_bf16 v[108:111], a[144:147], a[80:83], v[108:111]\n" \
"ds_read_b128 a[8:11], v20 offset:10240\n" \
"ds_read_b128 a[12:15], v20 offset:11264\n" \
"ds_read_b128 a[28:31], v20 offset:15360\n" \
"ds_read_b128 a[24:27], v20 offset:14336\n" \
"ds_read_b128 a[20:23], v20 offset:13312\n" \
"s_waitcnt lgkmcnt(14)\n" \
"v_mfma_f32_16x16x32_bf16 v[128:131], a[160:163], v[68:71], v[128:131]\n" \
"ds_read_b128 a[16:19], v20 offset:12288\n" \
"v_add_u32_e32 v50, s15, v48\n" \
"v_mfma_f32_16x16x32_bf16 v[160:163], a[160:163], v[72:75], v[160:163]\n" \
"v_xor_b32_e32 v48, 0x4000, v49\n" \
"s_waitcnt lgkmcnt(14)\n" \
"v_mfma_f32_16x16x32_bf16 v[132:135], a[164:167], v[68:71], v[132:135]\n" \
"ds_read_b32 v58, v18 offset:256\n" \
"ds_read_b32 v59, v18 offset:384\n" \
"v_mfma_f32_16x16x32_bf16 v[164:167], a[164:167], v[72:75], v[164:167]\n" \
"ds_read_b128 a[32:35], v48 offset:8192\n" \
"s_waitcnt lgkmcnt(15)\n" \
"v_mfma_f32_16x16x32_bf16 v[136:139], a[168:171], v[68:71], v[136:139]\n" \
"ds_read_b128 a[36:39], v48 offset:9216\n" \
"ds_read_b128 a[40:43], v48 offset:10240\n" \
"v_mfma_f32_16x16x32_bf16 v[168:171], a[168:171], v[72:75], v[168:171]\n" \
"ds_read_b128 a[44:47], v48 offset:11264\n" \
"ds_read_b128 a[48:51], v48 offset:12288\n" \
"v_mfma_f32_16x16x32_bf16 v[140:143], a[172:175], v[68:71], v[140:143]\n" \
"ds_read_b128 a[52:55], v48 offset:13312\n" \
"ds_read_b128 a[56:59], v48 offset:14336\n" \
"v_mfma_f32_16x16x32_bf16 v[172:175], a[172:175], v[72:75], v[172:175]\n" \
"ds_read_b128 a[60:63], v48 offset:15360\n" \
"s_waitcnt vmcnt(1)\n" \
"s_mov_b64 exec, 1\n" \
"buffer_store_dword v61, off, s[56:59], s95 offset:0 sc1\n" \
"s_mov_b64 exec, -1\n" \
"s_waitcnt lgkmcnt(15)\n" \
"v_mfma_f32_16x16x32_bf16 v[144:147], a[176:179], v[68:71], v[144:147]\n" \
"ds_read_b128 a[128:131], v49 offset:8192\n" \
"ds_read_b128 a[132:135], v49 offset:9216\n" \
"v_mfma_f32_16x16x32_bf16 v[176:179], a[176:179], v[72:75], v[176:179]\n" \
"ds_read_b128 a[136:139], v49 offset:10240\n" \
"ds_read_b128 a[140:143], v49 offset:11264\n" \
"v_mfma_f32_16x16x32_bf16 v[148:151], a[180:183], v[68:71], v[148:151]\n" \
"ds_read_b128 a[144:147], v49 offset:12288\n" \
"ds_read_b128 a[148:151], v49 offset:13312\n" \
"buffer_load_dwordx4 a[220:223], v20, s[56:59], s60 offen offset:0 sc1\n" \
"v_mfma_f32_16x16x32_bf16 v[180:183], a[180:183], v[72:75], v[180:183]\n" \
"ds_read_b128 a[152:155], v49 offset:14336\n" \
"ds_read_b128 a[156:159], v49 offset:15360\n" \
"v_mfma_f32_16x16x32_bf16 v[152:155], a[184:187], v[68:71], v[152:155]\n" \
"v_accvgpr_read_b32 v40, a196\n" \
"v_accvgpr_read_b32 v24, a197\n" \
"v_mfma_f32_16x16x32_bf16 v[184:187], a[184:187], v[72:75], v[184:187]\n" \
"v_accvgpr_read_b32 v41, a198\n" \
"v_accvgpr_read_b32 v25, a199\n" \
"v_mfma_f32_16x16x32_bf16 v[156:159], a[188:191], v[68:71], v[156:159]\n" \
"buffer_load_dwordx4 a[236:239], v56, s[56:59], s60 offen offset:0 sc1\n" \
"v_mfma_f32_16x16x32_bf16 v[188:191], a[188:191], v[72:75], v[188:191]\n" \
"v_add_f32_e32 v40, v40, v24\n" \
"v_add_f32_e32 v41, v41, v25\n" \
"v_smfmac_f32_16x16x64_bf16 a[192:195], a[200:203], v[68:75], v21\n" \
"v_add_u32_e32 v51, 0x10000, v50\n" \
"v_add_f32_e32 v40, s94, v40\n" \
"v_mfma_f32_16x16x32_bf16 v[112:115], a[0:3], a[64:67], v[32:35]\n" \
"v_add_f32_e32 v41, s94, v41\n" \
"ds_write_b64 v19, v[40:41]\n" \
"v_mfma_f32_16x16x32_bf16 v[112:115], a[4:7], a[68:71], v[112:115]\n" \
"buffer_load_dwordx4 a[224:227], v20, s[56:59], s60 offen offset:1024 sc1\n" \
"v_mfma_f32_16x16x32_bf16 v[112:115], a[8:11], a[72:75], v[112:115]\n" \
"v_xor_b32_e32 v52, 0x4000, v50\n" \
"s_waitcnt lgkmcnt(9)\n" \
"v_mfma_f32_16x16x32_bf16 v[112:115], a[12:15], a[76:79], v[112:115]\n" \
"ds_write_b128 v50, v[192:195] offset:0\n" \
"v_mfma_f32_16x16x32_bf16 v[116:119], a[28:31], a[76:79], v[32:35]\n" \
"ds_write_b128 v50, v[196:199] offset:1024\n" \
"v_mfma_f32_16x16x32_bf16 v[116:119], a[24:27], a[72:75], v[116:119]\n" \
"buffer_load_dwordx4 a[240:243], v56, s[56:59], s60 offen offset:1024 sc1\n" \
"v_mfma_f32_16x16x32_bf16 v[116:119], a[20:23], a[68:71], v[116:119]\n" \
"ds_write_b128 v50, v[200:203] offset:2048\n" \
"v_mfma_f32_16x16x32_bf16 v[116:119], a[16:19], a[64:67], v[116:119]\n" \
"ds_write_b128 v50, v[204:207] offset:3072\n" \
"v_mfma_f32_16x16x32_bf16 v[124:127], a[16:19], a[80:83], v[36:39]\n" \
"ds_write_b128 v50, v[208:211] offset:8192\n" \
"v_mfma_f32_16x16x32_bf16 v[124:127], a[20:23], a[84:87], v[124:127]\n" \
"buffer_load_dwordx4 a[228:231], v20, s[56:59], s60 offen offset:2048 sc1\n" \
"v_mfma_f32_16x16x32_bf16 v[124:127], a[24:27], a[88:91], v[124:127]\n" \
"ds_write_b128 v50, v[212:215] offset:9216\n" \
"v_mfma_f32_16x16x32_bf16 v[124:127], a[28:31], a[92:95], v[124:127]\n" \
"ds_write_b128 v50, v[216:219] offset:10240\n" \
"v_mfma_f32_16x16x32_bf16 v[120:123], a[12:15], a[92:95], v[36:39]\n" \
"ds_write_b128 v50, v[220:223] offset:11264\n" \
"v_mfma_f32_16x16x32_bf16 v[120:123], a[8:11], a[88:91], v[120:123]\n" \
"buffer_load_dwordx4 a[244:247], v56, s[56:59], s60 offen offset:2048 sc1\n" \
"ds_write_b128 v51, v[224:227] offset:0\n" \
"ds_write_b128 v51, v[228:231] offset:1024\n" \
"ds_write_b128 v51, v[232:235] offset:2048\n" \
"ds_write_b128 v51, v[236:239] offset:3072\n" \
"ds_write_b128 v51, v[240:243] offset:8192\n" \
"ds_write_b128 v51, v[244:247] offset:9216\n" \
"ds_write_b128 v51, v[248:251] offset:10240\n" \
"buffer_load_dwordx4 a[232:235], v20, s[56:59], s60 offen offset:3072 sc1\n" \
"ds_write_b128 v51, v[252:255] offset:11264\n" \
"v_mfma_f32_16x16x32_bf16 v[120:123], a[4:7], a[84:87], v[120:123]\n" \
"v_xor_b32_e32 v53, 0x4000, v51\n" \
"v_readlane_b32 s93, v11, 20\n" \
"v_mfma_f32_16x16x32_bf16 v[120:123], a[0:3], a[80:83], v[120:123]\n" \
"v_exp_f32_e32 v104, v104\n" \
"v_exp_f32_e32 v105, v105\n" \
"v_exp_f32_e32 v106, v106\n" \
"buffer_load_dwordx4 a[248:251], v56, s[56:59], s60 offen offset:3072 sc1\n" \
"v_exp_f32_e32 v107, v107\n" \
"v_cvt_pk_bf16_f32 v104, v104, v105\n" \
"v_exp_f32_e32 v108, v108\n" \
"v_exp_f32_e32 v109, v109\n" \
"v_exp_f32_e32 v110, v110\n" \
"v_exp_f32_e32 v111, v111\n" \
"v_cvt_pk_bf16_f32 v105, v106, v107\n" \
"v_cvt_pk_bf16_f32 v106, v108, v109\n" \
"v_cvt_pk_bf16_f32 v107, v110, v111\n" \
"v_and_b32_e32 v104, v12, v104\n" \
"v_and_b32_e32 v105, v13, v105\n" \
"v_and_b32_e32 v106, v14, v106\n" \
"v_and_b32_e32 v107, v15, v107\n" \
"v_readlane_b32 s84, v11, 15\n" \
"v_readlane_b32 s85, v11, 16\n" \
"v_readlane_b32 s86, v11, 17\n" \
"s_mov_b32 s87, 0x20000\n" \
"v_readlane_b32 s92, v11, 18\n" \
"s_add_u32 m0, s93, 0x0\n" \
"v_readlane_b32 s97, v11, 34\n" \
"v_readlane_b32 s98, v11, 35\n" \
"s_waitcnt lgkmcnt(0)\n" \
"s_barrier\n" \
"ds_read_b128 v[192:195], v52 offset:0\n" \
"ds_read_b128 v[196:199], v52 offset:1024\n" \
"ds_read_b128 v[200:203], v52 offset:2048\n" \
"ds_read_b128 v[204:207], v52 offset:3072\n" \
"ds_read_b128 v[208:211], v52 offset:8192\n" \
"ds_read_b128 v[212:215], v52 offset:9216\n" \
"ds_read_b128 v[216:219], v52 offset:10240\n" \
"ds_read_b128 v[220:223], v52 offset:11264\n" \
"ds_read_b128 v[224:227], v53 offset:0\n" \
"ds_read_b128 v[228:231], v53 offset:1024\n" \
"ds_read_b128 v[232:235], v53 offset:2048\n" \
"ds_read_b128 v[236:239], v53 offset:3072\n" \
"ds_read_b128 v[240:243], v53 offset:8192\n" \
"ds_read_b128 v[244:247], v53 offset:9216\n" \
"ds_read_b128 v[248:251], v53 offset:10240\n" \
"ds_read_b128 v[252:255], v53 offset:11264\n" \
"v_mfma_f32_16x16x32_bf16 v[160:163], a[128:131], v[104:107], v[160:163]\n" \
"buffer_load_dwordx4 v0, s[84:87], s92 offen offset:0 lds\n" \
"v_mfma_f32_16x16x32_bf16 v[164:167], a[132:135], v[104:107], v[164:167]\n" \
"s_waitcnt vmcnt(10)\n" \
"v_cmp_ne_u32_e32 vcc, s62, v57\n" \
"v_mfma_f32_16x16x32_bf16 v[168:171], a[136:139], v[104:107], v[168:171]\n" \
"v_exp_f32_e32 v118, v118\n" \
"v_mfma_f32_16x16x32_bf16 v[172:175], a[140:143], v[104:107], v[172:175]\n" \
"v_exp_f32_e32 v119, v119\n" \
"v_mfma_f32_16x16x32_bf16 v[176:179], a[144:147], v[104:107], v[176:179]\n" \
"buffer_load_dwordx4 v1, s[84:87], s92 offen offset:1024 lds\n" \
"s_cbranch_vccnz L_so1_%=\n" \
"L_ro1_%=:\n" \
"v_mfma_f32_16x16x32_bf16 v[180:183], a[148:151], v[104:107], v[180:183]\n" \
"v_xor_b32_e32 v63, 0x400, v19\n" \
"ds_read_b64 v[42:43], v63\n" \
"v_mfma_f32_16x16x32_bf16 v[184:187], a[152:155], v[104:107], v[184:187]\n" \
"v_exp_f32_e32 v116, v116\n" \
"v_mfma_f32_16x16x32_bf16 v[188:191], a[156:159], v[104:107], v[188:191]\n" \
"v_exp_f32_e32 v117, v117\n" \
"buffer_load_dwordx4 v2, s[84:87], s92 offen offset:2048 lds\n" \
"v_mfma_f32_16x16x32_bf16 a[192:195], a[212:215], v[104:107], a[192:195]\n" \
"v_cvt_pk_bf16_f32 v119, v118, v119\n" \
"v_exp_f32_e32 v114, v114\n" \
"s_waitcnt lgkmcnt(1)\n" \
"s_barrier\n" \
"v_exp_f32_e32 v115, v115\n" \
"v_exp_f32_e32 v112, v112\n" \
"buffer_load_dwordx4 v3, s[84:87], s92 offen offset:3072 lds\n" \
"s_add_u32 m0, s93, 0x1000\n" \
"v_exp_f32_e32 v113, v113\n" \
"v_cvt_pk_bf16_f32 v118, v116, v117\n" \
"v_cvt_pk_bf16_f32 v117, v114, v115\n" \
"v_cvt_pk_bf16_f32 v116, v112, v113\n" \
"v_exp_f32_e32 v120, v120\n" \
"v_exp_f32_e32 v121, v121\n" \
"v_mfma_f32_16x16x32_bf16 v[128:131], a[32:35], v[116:119], v[128:131]\n" \
"v_exp_f32_e32 v122, v122\n" \
"buffer_load_dwordx4 v4, s[84:87], s92 offen offset:0 lds\n" \
"v_mfma_f32_16x16x32_bf16 v[132:135], a[36:39], v[116:119], v[132:135]\n" \
"v_exp_f32_e32 v123, v123\n" \
"v_mfma_f32_16x16x32_bf16 v[136:139], a[40:43], v[116:119], v[136:139]\n" \
"v_cvt_pk_bf16_f32 v120, v120, v121\n" \
"v_cvt_pk_bf16_f32 v121, v122, v123\n" \
"v_mfma_f32_16x16x32_bf16 v[140:143], a[44:47], v[116:119], v[140:143]\n" \
"v_exp_f32_e32 v124, v124\n" \
"v_mfma_f32_16x16x32_bf16 v[144:147], a[48:51], v[116:119], v[144:147]\n" \
"buffer_load_dwordx4 v5, s[84:87], s92 offen offset:1024 lds\n" \
"v_mfma_f32_16x16x32_bf16 v[148:151], a[52:55], v[116:119], v[148:151]\n" \
"v_exp_f32_e32 v125, v125\n" \
"v_mfma_f32_16x16x32_bf16 v[152:155], a[56:59], v[116:119], v[152:155]\n" \
"v_exp_f32_e32 v126, v126\n" \
"v_mfma_f32_16x16x32_bf16 v[156:159], a[60:63], v[116:119], v[156:159]\n" \
"v_exp_f32_e32 v127, v127\n" \
"v_mfma_f32_16x16x32_bf16 a[192:195], a[204:207], v[116:119], a[192:195]\n" \
"buffer_load_dwordx4 v6, s[84:87], s92 offen offset:2048 lds\n" \
"v_cvt_pk_bf16_f32 v122, v124, v125\n" \
"v_cvt_pk_bf16_f32 v123, v126, v127\n" \
"v_add_f32_e32 v160, v160, v224\n" \
"v_add_f32_e32 v161, v161, v225\n" \
"v_add_f32_e32 v162, v162, v226\n" \
"v_add_f32_e32 v163, v163, v227\n" \
"v_add_f32_e32 v164, v164, v228\n" \
"v_add_f32_e32 v165, v165, v229\n" \
"v_mfma_f32_16x16x32_bf16 v[160:163], a[32:35], v[120:123], v[160:163]\n" \
"v_add_f32_e32 v166, v166, v230\n" \
"v_add_f32_e32 v167, v167, v231\n" \
"v_add_f32_e32 v168, v168, v232\n" \
"buffer_load_dwordx4 v7, s[84:87], s92 offen offset:3072 lds\n" \
"s_add_u32 m0, s93, 0x2000\n" \
"v_mfma_f32_16x16x32_bf16 v[164:167], a[36:39], v[120:123], v[164:167]\n" \
"v_add_f32_e32 v169, v169, v233\n" \
"v_add_f32_e32 v170, v170, v234\n" \
"v_add_f32_e32 v171, v171, v235\n" \
"v_add_f32_e32 v172, v172, v236\n" \
"v_add_f32_e32 v173, v173, v237\n" \
"v_mfma_f32_16x16x32_bf16 v[168:171], a[40:43], v[120:123], v[168:171]\n" \
"v_add_f32_e32 v174, v174, v238\n" \
"v_add_f32_e32 v175, v175, v239\n" \
"v_add_f32_e32 v176, v176, v240\n" \
"buffer_load_dwordx4 v8, s[84:87], s92 offen offset:0 lds\n" \
"v_mfma_f32_16x16x32_bf16 v[172:175], a[44:47], v[120:123], v[172:175]\n" \
"v_add_f32_e32 v177, v177, v241\n" \
"v_add_f32_e32 v178, v178, v242\n" \
"v_add_f32_e32 v179, v179, v243\n" \
"v_add_f32_e32 v128, v128, v192\n" \
"v_add_f32_e32 v129, v129, v193\n" \
"v_mfma_f32_16x16x32_bf16 v[176:179], a[48:51], v[120:123], v[176:179]\n" \
"v_add_f32_e32 v130, v130, v194\n" \
"v_add_f32_e32 v131, v131, v195\n" \
"v_add_f32_e32 v132, v132, v196\n" \
"v_add_f32_e32 v133, v133, v197\n" \
"buffer_load_dwordx4 a[96:99], v20, s[56:59], s97 offen offset:0 sc1\n" \
"v_add_f32_e32 v134, v134, v198\n" \
"v_add_f32_e32 v135, v135, v199\n" \
"v_add_f32_e32 v180, v180, v244\n" \
"v_add_f32_e32 v181, v181, v245\n" \
"v_add_f32_e32 v182, v182, v246\n" \
"v_add_f32_e32 v183, v183, v247\n" \
"v_add_f32_e32 v136, v136, v200\n" \
"v_add_f32_e32 v137, v137, v201\n" \
"v_add_f32_e32 v138, v138, v202\n" \
"v_add_f32_e32 v139, v139, v203\n" \
"v_add_f32_e32 v140, v140, v204\n" \
"v_add_f32_e32 v141, v141, v205\n" \
"v_add_f32_e32 v142, v142, v206\n" \
"buffer_load_dwordx4 a[104:107], v56, s[56:59], s97 offen offset:0 sc1\n" \
"v_add_f32_e32 v143, v143, v207\n" \
"v_mfma_f32_16x16x32_bf16 v[180:183], a[52:55], v[120:123], v[180:183]\n" \
"v_add_f32_e32 v144, v144, v208\n" \
"v_add_f32_e32 v145, v145, v209\n" \
"v_add_f32_e32 v146, v146, v210\n" \
"v_add_f32_e32 v147, v147, v211\n" \
"v_add_f32_e32 v148, v148, v212\n" \
"v_add_f32_e32 v149, v149, v213\n" \
"v_add_f32_e32 v150, v150, v214\n" \
"v_add_f32_e32 v151, v151, v215\n" \
"v_add_f32_e32 v184, v184, v248\n" \
"v_add_f32_e32 v185, v185, v249\n" \
"buffer_load_dwordx4 a[112:115], v20, s[56:59], s98 offen offset:0 sc1\n" \
"v_add_f32_e32 v186, v186, v250\n" \
"v_add_f32_e32 v187, v187, v251\n" \
"v_add_f32_e32 v152, v152, v216\n" \
"v_add_f32_e32 v153, v153, v217\n" \
"v_add_f32_e32 v154, v154, v218\n" \
"v_add_f32_e32 v155, v155, v219\n" \
"v_add_f32_e32 v156, v156, v220\n" \
"v_add_f32_e32 v157, v157, v221\n" \
"v_add_f32_e32 v158, v158, v222\n" \
"v_add_f32_e32 v159, v159, v223\n" \
"v_mfma_f32_16x16x32_bf16 v[184:187], a[56:59], v[120:123], v[184:187]\n" \
"v_add_f32_e32 v188, v188, v252\n" \
"buffer_load_dwordx4 a[120:123], v56, s[56:59], s98 offen offset:0 sc1\n" \
"v_add_f32_e32 v189, v189, v253\n" \
"v_add_f32_e32 v190, v190, v254\n" \
"v_add_f32_e32 v191, v191, v255\n" \
"s_nop 1\n" \
"v_mfma_f32_16x16x32_bf16 v[188:191], a[60:63], v[120:123], v[188:191]\n" \
"v_mfma_f32_16x16x32_bf16 a[192:195], a[212:215], v[120:123], a[192:195]\n" \
"buffer_load_dwordx4 a[100:103], v20, s[56:59], s97 offen offset:1024 sc1\n" \
"buffer_load_dwordx4 a[108:111], v56, s[56:59], s97 offen offset:1024 sc1\n" \
"buffer_load_dwordx4 a[116:119], v20, s[56:59], s98 offen offset:1024 sc1\n" \
"buffer_load_dwordx4 a[124:127], v56, s[56:59], s98 offen offset:1024 sc1\n" \
"s_nop 3\n" \
"v_accvgpr_read_b32 v54, a192\n" \
"v_accvgpr_read_b32 v22, a193\n" \
"v_add_f32_e32 v54, v54, v22\n" \
"s_waitcnt lgkmcnt(0)\n" \
"v_add_f32_e32 v54, v54, v42\n" \
"v_max_f32_e32 v62, v62, v54\n" \
"v_rcp_f32_e32 v54, v54\n" \
"v_accvgpr_read_b32 v55, a194\n" \
"v_mul_f32_e32 v54, v54, v58\n" \
"v_mul_f32_e32 v128, v54, v128\n" \
"v_mul_f32_e32 v129, v54, v129\n" \
"v_mul_f32_e32 v130, v54, v130\n" \
"v_mul_f32_e32 v131, v54, v131\n" \
"v_mul_f32_e32 v132, v54, v132\n" \
"v_mul_f32_e32 v133, v54, v133\n" \
"v_mul_f32_e32 v134, v54, v134\n" \
"v_mul_f32_e32 v135, v54, v135\n" \
"v_cvt_pk_bf16_f32 v64, v128, v129\n" \
"v_cvt_pk_bf16_f32 v65, v130, v131\n" \
"v_cvt_pk_bf16_f32 v66, v132, v133\n" \
"v_cvt_pk_bf16_f32 v67, v134, v135\n" \
"v_accvgpr_read_b32 v23, a195\n" \
"v_mul_f32_e32 v136, v54, v136\n" \
"v_mfma_f32_16x16x32_bf16 a[208:211], a[216:219], v[64:67], a[208:211]\n" \
"v_mul_f32_e32 v137, v54, v137\n" \
"v_mul_f32_e32 v138, v54, v138\n" \
"v_mul_f32_e32 v139, v54, v139\n" \
"v_mul_f32_e32 v140, v54, v140\n" \
"v_mul_f32_e32 v141, v54, v141\n" \
"v_mul_f32_e32 v142, v54, v142\n" \
"v_mul_f32_e32 v143, v54, v143\n" \
"v_add_f32_e32 v55, v55, v23\n" \
"v_cvt_pk_bf16_f32 v68, v136, v137\n" \
"v_cvt_pk_bf16_f32 v69, v138, v139\n" \
"v_cvt_pk_bf16_f32 v70, v140, v141\n" \
"v_cvt_pk_bf16_f32 v71, v142, v143\n" \
"v_add_f32_e32 v55, v55, v43\n" \
"v_mul_f32_e32 v144, v54, v144\n" \
"v_mfma_f32_16x16x32_bf16 a[208:211], a[216:219], v[68:71], a[208:211]\n" \
"v_mul_f32_e32 v145, v54, v145\n" \
"v_mul_f32_e32 v146, v54, v146\n" \
"v_mul_f32_e32 v147, v54, v147\n" \
"v_mul_f32_e32 v148, v54, v148\n" \
"v_mul_f32_e32 v149, v54, v149\n" \
"v_mul_f32_e32 v150, v54, v150\n" \
"v_mul_f32_e32 v151, v54, v151\n" \
"v_max_f32_e32 v62, v62, v55\n" \
"buffer_store_dwordx4 v[64:67], v20, s[56:59], s68 offen offset:0 sc1\n" \
"v_cvt_pk_bf16_f32 v72, v144, v145\n" \
"v_cvt_pk_bf16_f32 v73, v146, v147\n" \
"v_cvt_pk_bf16_f32 v74, v148, v149\n" \
"v_cvt_pk_bf16_f32 v75, v150, v151\n" \
"v_rcp_f32_e32 v55, v55\n" \
"v_mul_f32_e32 v152, v54, v152\n" \
"v_mfma_f32_16x16x32_bf16 a[208:211], a[216:219], v[72:75], a[208:211]\n" \
"v_mul_f32_e32 v153, v54, v153\n" \
"v_mul_f32_e32 v154, v54, v154\n" \
"v_mul_f32_e32 v155, v54, v155\n" \
"v_mul_f32_e32 v156, v54, v156\n" \
"buffer_store_dwordx4 v[68:71], v20, s[56:59], s68 offen offset:1024 sc1\n" \
"v_mul_f32_e32 v157, v54, v157\n" \
"v_mul_f32_e32 v158, v54, v158\n" \
"v_mul_f32_e32 v159, v54, v159\n" \
"v_cvt_pk_bf16_f32 v76, v152, v153\n" \
"v_cvt_pk_bf16_f32 v77, v154, v155\n" \
"v_cvt_pk_bf16_f32 v78, v156, v157\n" \
"v_cvt_pk_bf16_f32 v79, v158, v159\n" \
"v_mul_f32_e32 v55, v55, v59\n" \
"v_mul_f32_e32 v160, v55, v160\n" \
"v_mfma_f32_16x16x32_bf16 a[208:211], a[216:219], v[76:79], a[208:211]\n" \
"v_mul_f32_e32 v161, v55, v161\n" \
"v_mul_f32_e32 v162, v55, v162\n" \
"buffer_store_dwordx4 v[72:75], v20, s[56:59], s68 offen offset:2048 sc1\n" \
"v_mul_f32_e32 v163, v55, v163\n" \
"v_mul_f32_e32 v164, v55, v164\n" \
"v_mul_f32_e32 v165, v55, v165\n" \
"v_mul_f32_e32 v166, v55, v166\n" \
"v_mul_f32_e32 v167, v55, v167\n" \
"v_cvt_pk_bf16_f32 v80, v160, v161\n" \
"v_cvt_pk_bf16_f32 v81, v162, v163\n" \
"v_cvt_pk_bf16_f32 v82, v164, v165\n" \
"v_cvt_pk_bf16_f32 v83, v166, v167\n" \
"v_mul_f32_e32 v168, v55, v168\n" \
"v_mul_f32_e32 v169, v55, v169\n" \
"v_mfma_f32_16x16x32_bf16 a[208:211], a[216:219], v[80:83], a[208:211]\n" \
"buffer_store_dwordx4 v[76:79], v20, s[56:59], s68 offen offset:3072 sc1\n" \
"v_mul_f32_e32 v170, v55, v170\n" \
"v_mul_f32_e32 v171, v55, v171\n" \
"v_mul_f32_e32 v172, v55, v172\n" \
"v_mul_f32_e32 v173, v55, v173\n" \
"v_mul_f32_e32 v174, v55, v174\n" \
"v_mul_f32_e32 v175, v55, v175\n" \
"v_cvt_pk_bf16_f32 v84, v168, v169\n" \
"v_cvt_pk_bf16_f32 v85, v170, v171\n" \
"v_cvt_pk_bf16_f32 v86, v172, v173\n" \
"v_cvt_pk_bf16_f32 v87, v174, v175\n" \
"v_mul_f32_e32 v176, v55, v176\n" \
"v_mul_f32_e32 v177, v55, v177\n" \
"v_mfma_f32_16x16x32_bf16 a[208:211], a[216:219], v[84:87], a[208:211]\n" \
"buffer_store_dwordx4 v[80:83], v20, s[56:59], s69 offen offset:0 sc1\n" \
"v_mul_f32_e32 v178, v55, v178\n" \
"v_mul_f32_e32 v179, v55, v179\n" \
"v_mul_f32_e32 v180, v55, v180\n" \
"v_mul_f32_e32 v181, v55, v181\n" \
"v_mul_f32_e32 v182, v55, v182\n" \
"v_mul_f32_e32 v183, v55, v183\n" \
"v_cvt_pk_bf16_f32 v96, v176, v177\n" \
"v_cvt_pk_bf16_f32 v97, v178, v179\n" \
"v_cvt_pk_bf16_f32 v98, v180, v181\n" \
"v_cvt_pk_bf16_f32 v99, v182, v183\n" \
"v_mul_f32_e32 v184, v55, v184\n" \
"v_mul_f32_e32 v185, v55, v185\n" \
"v_mfma_f32_16x16x32_bf16 a[208:211], a[216:219], v[96:99], a[208:211]\n" \
"buffer_store_dwordx4 v[84:87], v20, s[56:59], s69 offen offset:1024 sc1\n" \
"v_mul_f32_e32 v186, v55, v186\n" \
"v_mul_f32_e32 v187, v55, v187\n" \
"v_mul_f32_e32 v188, v55, v188\n" \
"v_mul_f32_e32 v189, v55, v189\n" \
"v_mul_f32_e32 v190, v55, v190\n" \
"v_mul_f32_e32 v191, v55, v191\n" \
"v_cvt_pk_bf16_f32 v100, v184, v185\n" \
"v_cvt_pk_bf16_f32 v101, v186, v187\n" \
"v_cvt_pk_bf16_f32 v102, v188, v189\n" \
"v_cvt_pk_bf16_f32 v103, v190, v191\n" \
"v_cmp_lt_f32_e64 s[72:73], s21, v62\n" \
"buffer_store_dwordx4 v[96:99], v20, s[56:59], s69 offen offset:2048 sc1\n" \
"v_mfma_f32_16x16x32_bf16 a[208:211], a[216:219], v[100:103], a[208:211]\n" \
"buffer_store_dwordx4 v[100:103], v20, s[56:59], s69 offen offset:3072 sc1\n" \
"s_nop 6\n" \
"v_accvgpr_read_b32 v63, a208\n" \
"v_cmp_u_f32_e32 vcc, v63, v63\n" \
"s_nop 0\n" \
"s_or_b64 vcc, s[72:73], vcc\n" \
"s_cbranch_scc1 L_end_%=\n" \
"L_out_%=:\n" \
"v_add_u32_e32 v34, 0x20000, v10\n" \
"ds_read_b64_tr_b16 a[0:1], v34 offset:0\n" \
"ds_read_b64_tr_b16 a[2:3], v34 offset:4608\n" \
"ds_read_b64_tr_b16 a[4:5], v34 offset:32\n" \
"ds_read_b64_tr_b16 a[6:7], v34 offset:4640\n" \
"ds_read_b64_tr_b16 a[8:9], v34 offset:64\n" \
"ds_read_b64_tr_b16 a[10:11], v34 offset:4672\n" \
"ds_read_b64_tr_b16 a[12:13], v34 offset:96\n" \
"ds_read_b64_tr_b16 a[14:15], v34 offset:4704\n" \
"v_mov_b32_e32 v39, s62\n" \
"v_readlane_b32 s70, v11, 5\n" \
"v_bfe_u32 v38, v20, 2, 4\n" \
"v_readlane_b32 s64, v11, 2\n" \
"v_readlane_b32 s71, v11, 4\n" \
"ds_read_b64_tr_b16 a[16:17], v34 offset:128\n" \
"ds_read_b64_tr_b16 a[18:19], v34 offset:4736\n" \
"ds_read_b64_tr_b16 a[20:21], v34 offset:160\n" \
"ds_read_b64_tr_b16 a[22:23], v34 offset:4768\n" \
"buffer_load_dword v37, v38, s[56:59], s64 offen sc1\n" \
"ds_read_b64_tr_b16 a[24:25], v34 offset:192\n" \
"ds_read_b64_tr_b16 a[26:27], v34 offset:4800\n" \
"ds_read_b64_tr_b16 a[28:29], v34 offset:224\n" \
"ds_read_b64_tr_b16 a[30:31], v34 offset:4832\n" \
"ds_read_b64_tr_b16 a[192:193], v34 offset:256\n" \
"ds_read_b64_tr_b16 a[194:195], v34 offset:4864\n" \
"ds_read_b64_tr_b16 a[196:197], v34 offset:9216\n" \
"ds_read_b64_tr_b16 a[198:199], v34 offset:13824\n" \
"ds_read_b64_tr_b16 a[200:201], v34 offset:9248\n" \
"ds_read_b64_tr_b16 a[202:203], v34 offset:13856\n" \
"ds_read_b64_tr_b16 a[204:205], v34 offset:9280\n" \
"ds_read_b64_tr_b16 a[206:207], v34 offset:13888\n" \
"ds_read_b64_tr_b16 a[208:209], v34 offset:9312\n" \
"ds_read_b64_tr_b16 a[210:211], v34 offset:13920\n" \
"ds_read_b64_tr_b16 a[212:213], v34 offset:9344\n" \
"ds_read_b64_tr_b16 a[214:215], v34 offset:13952\n" \
"ds_read_b64_tr_b16 a[216:217], v34 offset:9376\n" \
"ds_read_b64_tr_b16 a[218:219], v34 offset:13984\n" \
"v_add_u32_e32 v33, 0xfc00, v10\n" \
"v_readlane_b32 s99, v11, 21\n" \
"v_readlane_b32 s80, v11, 11\n" \
"v_readlane_b32 s81, v11, 12\n" \
"s_mov_b32 s82, 0x47fca0\n" \
"s_waitcnt vmcnt(26) lgkmcnt(15)\n" \
"v_mfma_f32_16x16x32_bf16 v[112:115], a[0:3], a[220:223], 0\n" \
"s_mov_b32 s83, 0x20000\n" \
"v_readlane_b32 s65, v11, 25\n" \
"v_mfma_f32_16x16x32_bf16 v[116:119], a[0:3], a[236:239], 0\n" \
"s_add_u32 m0, s99, 0x6c00\n" \
"v_readlane_b32 s66, v11, 28\n" \
"v_mfma_f32_16x16x32_bf16 v[184:187], a[0:3], v[64:67], 0\n" \
"v_add_u32_e32 v32, 0x49000, v20\n" \
"v_lshrrev_b32_e32 v44, 9, v20\n" \
"v_mfma_f32_16x16x32_bf16 v[188:191], a[0:3], v[80:83], 0\n" \
"ds_read_b64_tr_b16 a[0:1], v34 offset:9408\n" \
"ds_read_b64_tr_b16 a[2:3], v34 offset:14016\n" \
"v_mfma_f32_16x16x32_bf16 v[120:123], a[4:7], a[220:223], 0\n" \
"buffer_load_dwordx4 v0, s[80:83], s65 offen offset:0 lds\n" \
"v_mfma_f32_16x16x32_bf16 v[124:127], a[4:7], a[236:239], 0\n" \
"v_mul_u32_u24_e32 v44, 0xb40, v44\n" \
"v_add_u32_e32 v35, v44, v10\n" \
"v_mfma_f32_16x16x32_bf16 v[192:195], a[4:7], v[64:67], 0\n" \
"v_readlane_b32 s67, v11, 26\n" \
"v_readlane_b32 s92, v11, 22\n" \
"v_mfma_f32_16x16x32_bf16 v[196:199], a[4:7], v[80:83], 0\n" \
"ds_read_b64_tr_b16 a[4:5], v34 offset:9440\n" \
"ds_read_b64_tr_b16 a[6:7], v34 offset:14048\n" \
"v_mfma_f32_16x16x32_bf16 v[128:131], a[8:11], a[220:223], 0\n" \
"v_readlane_b32 s73, v11, 27\n" \
"v_readlane_b32 s94, v11, 23\n" \
"v_mfma_f32_16x16x32_bf16 v[132:135], a[8:11], a[236:239], 0\n" \
"buffer_load_dwordx4 v1, s[80:83], s65 offen offset:1024 lds\n" \
"v_mfma_f32_16x16x32_bf16 v[200:203], a[8:11], v[64:67], 0\n" \
"v_readlane_b32 s84, v11, 9\n" \
"v_readlane_b32 s85, v11, 10\n" \
"v_mfma_f32_16x16x32_bf16 v[204:207], a[8:11], v[80:83], 0\n" \
"ds_read_b64_tr_b16 a[8:9], v34 offset:9472\n" \
"ds_read_b64_tr_b16 a[10:11], v34 offset:14080\n" \
"v_mfma_f32_16x16x32_bf16 v[136:139], a[12:15], a[220:223], 0\n" \
"s_mov_b32 s86, 0xa1ca0\n" \
"s_mov_b32 s87, 0x20000\n" \
"v_mfma_f32_16x16x32_bf16 v[140:143], a[12:15], a[236:239], 0\n" \
"v_readlane_b32 s68, v11, 29\n" \
"v_readlane_b32 s69, v11, 30\n" \
"v_mfma_f32_16x16x32_bf16 v[208:211], a[12:15], v[64:67], 0\n" \
"buffer_load_dwordx4 v2, s[80:83], s65 offen offset:2048 lds\n" \
"v_mfma_f32_16x16x32_bf16 v[212:215], a[12:15], v[80:83], 0\n" \
"ds_read_b64_tr_b16 a[12:13], v34 offset:18432\n" \
"ds_read_b64_tr_b16 a[14:15], v34 offset:23040\n" \
"v_mfma_f32_16x16x32_bf16 v[144:147], a[16:19], a[220:223], 0\n" \
"v_readlane_b32 s72, v11, 32\n" \
"v_readlane_b32 s93, v11, 33\n" \
"v_mfma_f32_16x16x32_bf16 v[148:151], a[16:19], a[236:239], 0\n" \
"v_lshlrev_b32_e32 v41, 12, v20\n" \
"v_readlane_b32 s63, v11, 39\n" \
"v_mfma_f32_16x16x32_bf16 v[216:219], a[16:19], v[64:67], 0\n" \
"v_bfe_u32 v40, v20, 2, 7\n" \
"s_add_u32 s74, s63, 0x3800\n" \
"v_mfma_f32_16x16x32_bf16 v[220:223], a[16:19], v[80:83], 0\n" \
"ds_read_b64_tr_b16 a[16:17], v34 offset:18464\n" \
"ds_read_b64_tr_b16 a[18:19], v34 offset:23072\n" \
"v_mfma_f32_16x16x32_bf16 v[152:155], a[20:23], a[220:223], 0\n" \
"buffer_load_dwordx4 v3, s[80:83], s65 offen offset:3072 lds\n" \
"v_mfma_f32_16x16x32_bf16 v[156:159], a[20:23], a[236:239], 0\n" \
"s_add_u32 m0, s99, 0x7c00\n" \
"v_readlane_b32 s95, v11, 36\n" \
"v_mfma_f32_16x16x32_bf16 v[224:227], a[20:23], v[64:67], 0\n" \
"v_readlane_b32 s96, v11, 37\n" \
"v_readlane_b32 s97, v11, 38\n" \
"v_mfma_f32_16x16x32_bf16 v[228:231], a[20:23], v[80:83], 0\n" \
"ds_read_b64_tr_b16 a[20:21], v34 offset:18496\n" \
"ds_read_b64_tr_b16 a[22:23], v34 offset:23104\n" \
"v_mfma_f32_16x16x32_bf16 v[160:163], a[24:27], a[220:223], 0\n" \
"v_readlane_b32 s100, v11, 7\n" \
"v_bfe_u32 v44, v20, 4, 4\n" \
"v_mfma_f32_16x16x32_bf16 v[164:167], a[24:27], a[236:239], 0\n" \
"buffer_load_dwordx4 v4, s[80:83], s65 offen offset:0 lds\n" \
"v_mfma_f32_16x16x32_bf16 v[232:235], a[24:27], v[64:67], 0\n" \
"v_bfe_u32 v45, v20, 8, 2\n" \
"v_mul_u32_u24_e32 v42, 0x480, v44\n" \
"v_mfma_f32_16x16x32_bf16 v[236:239], a[24:27], v[80:83], 0\n" \
"ds_read_b64_tr_b16 a[24:25], v34 offset:18528\n" \
"s_waitcnt lgkmcnt(7)\n" \
"v_mfma_f32_16x16x32_bf16 v[168:171], a[28:31], a[220:223], 0\n" \
"ds_read_b64_tr_b16 a[26:27], v34 offset:23136\n" \
"v_lshl_add_u32 v42, v45, 3, v42\n" \
"v_mfma_f32_16x16x32_bf16 v[172:175], a[28:31], a[236:239], 0\n" \
"v_lshrrev_b32_e32 v44, 1, v44\n" \
"v_lshlrev_b32_e32 v45, 1, v45\n" \
"v_mfma_f32_16x16x32_bf16 v[240:243], a[28:31], v[64:67], 0\n" \
"buffer_load_dwordx4 v5, s[80:83], s65 offen offset:1024 lds\n" \
"v_mfma_f32_16x16x32_bf16 v[244:247], a[28:31], v[80:83], 0\n" \
"ds_read_b64_tr_b16 a[28:29], v34 offset:18560\n" \
"ds_read_b64_tr_b16 a[30:31], v34 offset:23168\n" \
"v_mfma_f32_16x16x32_bf16 v[176:179], a[192:195], a[220:223], 0\n" \
"v_sub_u32_e32 v44, v44, v45\n" \
"v_and_b32_e32 v45, 16, v20\n" \
"v_mfma_f32_16x16x32_bf16 v[180:183], a[192:195], a[236:239], 0\n" \
"v_mov_b32_e32 v46, 0x3f80\n" \
"v_lshlrev_b32_e32 v45, v45, v46\n" \
"v_mfma_f32_16x16x32_bf16 v[248:251], a[192:195], v[64:67], 0\n" \
"v_mov_b32_e32 v52, 0\n" \
"v_mov_b32_e32 v53, 0\n" \
"v_mfma_f32_16x16x32_bf16 v[252:255], a[192:195], v[80:83], 0\n" \
"ds_read_b64_tr_b16 a[192:193], v34 offset:18592\n" \
"ds_read_b64_tr_b16 a[194:195], v34 offset:23200\n" \
"v_mfma_f32_16x16x32_bf16 v[112:115], a[196:199], a[224:227], v[112:115]\n" \
"buffer_load_dwordx4 v6, s[80:83], s65 offen offset:2048 lds\n" \
"v_mfma_f32_16x16x32_bf16 v[116:119], a[196:199], a[240:243], v[116:119]\n" \
"v_mov_b32_e32 v48, 0\n" \
"v_mov_b32_e32 v49, 0\n" \
"v_mfma_f32_16x16x32_bf16 v[184:187], a[196:199], v[68:71], v[184:187]\n" \
"v_add_u32_e32 v43, 0x9000, v42\n" \
"v_readlane_b32 s88, v11, 13\n" \
"v_mfma_f32_16x16x32_bf16 v[188:191], a[196:199], v[84:87], v[188:191]\n" \
"ds_read_b64_tr_b16 a[196:197], v34 offset:18624\n" \
"ds_read_b64_tr_b16 a[198:199], v34 offset:23232\n" \
"v_mfma_f32_16x16x32_bf16 v[120:123], a[200:203], a[224:227], v[120:123]\n" \
"v_readlane_b32 s89, v11, 14\n" \
"s_mov_b32 s90, 0x240000\n" \
"v_mfma_f32_16x16x32_bf16 v[124:127], a[200:203], a[240:243], v[124:127]\n" \
"buffer_load_dwordx4 v7, s[80:83], s65 offen offset:3072 lds\n" \
"v_mfma_f32_16x16x32_bf16 v[192:195], a[200:203], v[68:71], v[192:195]\n" \
"s_add_u32 m0, s99, 0x8c00\n" \
"s_mov_b32 s91, 0x20000\n" \
"v_mfma_f32_16x16x32_bf16 v[196:199], a[200:203], v[84:87], v[196:199]\n" \
"ds_read_b64_tr_b16 a[200:201], v34 offset:18656\n" \
"ds_read_b64_tr_b16 a[202:203], v34 offset:23264\n" \
"v_mfma_f32_16x16x32_bf16 v[128:131], a[204:207], a[224:227], v[128:131]\n" \
"v_mov_b32_e32 v46, s62\n" \
"v_mfma_f32_16x16x32_bf16 v[132:135], a[204:207], a[240:243], v[132:135]\n" \
"s_waitcnt vmcnt(8)\n" \
"v_cmp_ne_u32_e32 vcc, s62, v37\n" \
"s_mov_b64 exec, 1\n" \
"buffer_store_dword v39, off, s[56:59], s70 offset:0 sc1\n" \
"s_mov_b64 exec, -1\n" \
"v_mfma_f32_16x16x32_bf16 v[200:203], a[204:207], v[68:71], v[200:203]\n" \
"v_readlane_b32 s70, v11, 47\n" \
"s_cbranch_vccnz L_sa_%=\n" \
"L_ra_%=:\n" \
"v_mfma_f32_16x16x32_bf16 v[204:207], a[204:207], v[84:87], v[204:207]\n" \
"ds_read_b64_tr_b16 a[204:205], v34 offset:18688\n" \
"ds_read_b64_tr_b16 a[206:207], v34 offset:23296\n" \
"v_mfma_f32_16x16x32_bf16 v[136:139], a[208:211], a[224:227], v[136:139]\n" \
"v_readlane_b32 s64, v11, 31\n" \
"v_mfma_f32_16x16x32_bf16 v[140:143], a[208:211], a[240:243], v[140:143]\n" \
"buffer_load_dwordx4 v8, s[80:83], s65 offen offset:0 lds\n" \
"v_mfma_f32_16x16x32_bf16 v[208:211], a[208:211], v[68:71], v[208:211]\n" \
"s_add_u32 m0, s99, 0x14400\n" \
"v_readlane_b32 s65, v11, 44\n" \
"v_mfma_f32_16x16x32_bf16 v[212:215], a[208:211], v[84:87], v[212:215]\n" \
"v_mfma_f32_16x16x32_bf16 v[144:147], a[212:215], a[224:227], v[144:147]\n" \
"v_mfma_f32_16x16x32_bf16 v[148:151], a[212:215], a[240:243], v[148:151]\n" \
"v_mfma_f32_16x16x32_bf16 v[216:219], a[212:215], v[68:71], v[216:219]\n" \
"buffer_load_dwordx4 a[64:67], v20, s[56:59], s66 offen offset:0 sc1\n" \
"v_mfma_f32_16x16x32_bf16 v[220:223], a[212:215], v[84:87], v[220:223]\n" \
"v_mfma_f32_16x16x32_bf16 v[152:155], a[216:219], a[224:227], v[152:155]\n" \
"v_mfma_f32_16x16x32_bf16 v[156:159], a[216:219], a[240:243], v[156:159]\n" \
"v_mfma_f32_16x16x32_bf16 v[224:227], a[216:219], v[68:71], v[224:227]\n" \
"v_mfma_f32_16x16x32_bf16 v[228:231], a[216:219], v[84:87], v[228:231]\n" \
"buffer_load_dwordx4 a[80:83], v32, s[56:59], s66 offen offset:0 sc1\n" \
"v_mfma_f32_16x16x32_bf16 v[160:163], a[0:3], a[224:227], v[160:163]\n" \
"v_mfma_f32_16x16x32_bf16 v[164:167], a[0:3], a[240:243], v[164:167]\n" \
"v_mfma_f32_16x16x32_bf16 v[232:235], a[0:3], v[68:71], v[232:235]\n" \
"v_mfma_f32_16x16x32_bf16 v[236:239], a[0:3], v[84:87], v[236:239]\n" \
"v_mfma_f32_16x16x32_bf16 v[168:171], a[4:7], a[224:227], v[168:171]\n" \
"buffer_load_dwordx4 a[68:71], v20, s[56:59], s66 offen offset:1024 sc1\n" \
"v_mfma_f32_16x16x32_bf16 v[172:175], a[4:7], a[240:243], v[172:175]\n" \
"v_mfma_f32_16x16x32_bf16 v[240:243], a[4:7], v[68:71], v[240:243]\n" \
"v_mfma_f32_16x16x32_bf16 v[244:247], a[4:7], v[84:87], v[244:247]\n" \
"v_mfma_f32_16x16x32_bf16 v[176:179], a[8:11], a[224:227], v[176:179]\n" \
"v_mfma_f32_16x16x32_bf16 v[180:183], a[8:11], a[240:243], v[180:183]\n" \
"buffer_load_dwordx4 a[84:87], v32, s[56:59], s66 offen offset:1024 sc1\n" \
"v_mfma_f32_16x16x32_bf16 v[248:251], a[8:11], v[68:71], v[248:251]\n" \
"v_mfma_f32_16x16x32_bf16 v[252:255], a[8:11], v[84:87], v[252:255]\n" \
"s_waitcnt lgkmcnt(0)\n" \
"v_mfma_f32_16x16x32_bf16 v[112:115], a[12:15], a[228:231], v[112:115]\n" \
"v_mfma_f32_16x16x32_bf16 v[116:119], a[12:15], a[244:247], v[116:119]\n" \
"v_mfma_f32_16x16x32_bf16 v[184:187], a[12:15], v[72:75], v[184:187]\n" \
"buffer_load_dwordx4 a[72:75], v20, s[56:59], s66 offen offset:2048 sc1\n" \
"v_mfma_f32_16x16x32_bf16 v[188:191], a[12:15], v[96:99], v[188:191]\n" \
"v_mfma_f32_16x16x32_bf16 v[120:123], a[16:19], a[228:231], v[120:123]\n" \
"v_mfma_f32_16x16x32_bf16 v[124:127], a[16:19], a[244:247], v[124:127]\n" \
"v_mfma_f32_16x16x32_bf16 v[192:195], a[16:19], v[72:75], v[192:195]\n" \
"v_mfma_f32_16x16x32_bf16 v[196:199], a[16:19], v[96:99], v[196:199]\n" \
"buffer_load_dwordx4 a[88:91], v32, s[56:59], s66 offen offset:2048 sc1\n" \
"v_mfma_f32_16x16x32_bf16 v[128:131], a[20:23], a[228:231], v[128:131]\n" \
"v_mfma_f32_16x16x32_bf16 v[132:135], a[20:23], a[244:247], v[132:135]\n" \
"v_mfma_f32_16x16x32_bf16 v[200:203], a[20:23], v[72:75], v[200:203]\n" \
"v_mfma_f32_16x16x32_bf16 v[204:207], a[20:23], v[96:99], v[204:207]\n" \
"s_barrier\n" \
"v_mfma_f32_16x16x32_bf16 v[136:139], a[24:27], a[228:231], v[136:139]\n" \
"ds_read_b64_tr_b16 a[208:209], v33 offset:9216\n" \
"ds_read_b64_tr_b16 a[210:211], v33 offset:13824\n" \
"v_mfma_f32_16x16x32_bf16 v[140:143], a[24:27], a[244:247], v[140:143]\n" \
"ds_read_b64_tr_b16 a[212:213], v33 offset:9248\n" \
"ds_read_b64_tr_b16 a[214:215], v33 offset:13856\n" \
"v_mfma_f32_16x16x32_bf16 v[208:211], a[24:27], v[72:75], v[208:211]\n" \
"ds_read_b64_tr_b16 a[216:217], v33 offset:9280\n" \
"ds_read_b64_tr_b16 a[218:219], v33 offset:13888\n" \
"v_mfma_f32_16x16x32_bf16 v[212:215], a[24:27], v[96:99], v[212:215]\n" \
"ds_read_b64_tr_b16 a[0:1], v33 offset:9312\n" \
"ds_read_b64_tr_b16 a[2:3], v33 offset:13920\n" \
"v_mfma_f32_16x16x32_bf16 v[144:147], a[28:31], a[228:231], v[144:147]\n" \
"ds_read_b64_tr_b16 a[4:5], v33 offset:9344\n" \
"ds_read_b64_tr_b16 a[6:7], v33 offset:13952\n" \
"v_mfma_f32_16x16x32_bf16 v[148:151], a[28:31], a[244:247], v[148:151]\n" \
"ds_read_b64_tr_b16 a[8:9], v33 offset:9376\n" \
"ds_read_b64_tr_b16 a[10:11], v33 offset:13984\n" \
"v_mfma_f32_16x16x32_bf16 v[216:219], a[28:31], v[72:75], v[216:219]\n" \
"ds_read_b64_tr_b16 a[12:13], v33 offset:9408\n" \
"ds_read_b64_tr_b16 a[14:15], v33 offset:14016\n" \
"v_mfma_f32_16x16x32_bf16 v[220:223], a[28:31], v[96:99], v[220:223]\n" \
"ds_read_b64_tr_b16 a[16:17], v33 offset:9440\n" \
"ds_read_b64_tr_b16 a[18:19], v33 offset:14048\n" \
"v_mfma_f32_16x16x32_bf16 v[152:155], a[192:195], a[228:231], v[152:155]\n" \
"ds_read_b64_tr_b16 a[20:21], v33 offset:9472\n" \
"ds_read_b64_tr_b16 a[22:23], v33 offset:14080\n" \
"v_mfma_f32_16x16x32_bf16 v[156:159], a[192:195], a[244:247], v[156:159]\n" \
"ds_read_b64_tr_b16 a[24:25], v35 offset:0\n" \
"ds_read_b64_tr_b16 a[26:27], v35 offset:2304\n" \
"v_mfma_f32_16x16x32_bf16 v[224:227], a[192:195], v[72:75], v[224:227]\n" \
"ds_read_b64_tr_b16 a[28:29], v35 offset:32\n" \
"ds_read_b64_tr_b16 a[30:31], v35 offset:2336\n" \
"v_mfma_f32_16x16x32_bf16 v[228:231], a[192:195], v[96:99], v[228:231]\n" \
"ds_read_b64_tr_b16 a[192:193], v35 offset:64\n" \
"ds_read_b64_tr_b16 a[194:195], v35 offset:2368\n" \
"v_mfma_f32_16x16x32_bf16 v[160:163], a[196:199], a[228:231], v[160:163]\n" \
"buffer_load_dwordx4 v0, s[80:83], s67 offen offset:0 lds\n" \
"v_mfma_f32_16x16x32_bf16 v[164:167], a[196:199], a[244:247], v[164:167]\n" \
"v_mfma_f32_16x16x32_bf16 v[232:235], a[196:199], v[72:75], v[232:235]\n" \
"v_mfma_f32_16x16x32_bf16 v[236:239], a[196:199], v[96:99], v[236:239]\n" \
"ds_read_b64_tr_b16 a[196:197], v35 offset:96\n" \
"ds_read_b64_tr_b16 a[198:199], v35 offset:2400\n" \
"v_mfma_f32_16x16x32_bf16 v[168:171], a[200:203], a[228:231], v[168:171]\n" \
"v_mfma_f32_16x16x32_bf16 v[172:175], a[200:203], a[244:247], v[172:175]\n" \
"buffer_load_dwordx4 v1, s[80:83], s67 offen offset:1024 lds\n" \
"v_mfma_f32_16x16x32_bf16 v[240:243], a[200:203], v[72:75], v[240:243]\n" \
"v_mfma_f32_16x16x32_bf16 v[244:247], a[200:203], v[96:99], v[244:247]\n" \
"ds_read_b64_tr_b16 a[200:201], v35 offset:128\n" \
"ds_read_b64_tr_b16 a[202:203], v35 offset:2432\n" \
"v_mfma_f32_16x16x32_bf16 v[176:179], a[204:207], a[228:231], v[176:179]\n" \
"v_mfma_f32_16x16x32_bf16 v[180:183], a[204:207], a[244:247], v[180:183]\n" \
"v_mfma_f32_16x16x32_bf16 v[248:251], a[204:207], v[72:75], v[248:251]\n" \
"buffer_load_dwordx4 v2, s[80:83], s67 offen offset:2048 lds\n" \
"v_mfma_f32_16x16x32_bf16 v[252:255], a[204:207], v[96:99], v[252:255]\n" \
"ds_read_b64_tr_b16 a[204:205], v35 offset:160\n" \
"s_waitcnt lgkmcnt(9)\n" \
"v_mfma_f32_16x16x32_bf16 v[112:115], a[208:211], a[232:235], v[112:115]\n" \
"ds_read_b64_tr_b16 a[206:207], v35 offset:2464\n" \
"v_mfma_f32_16x16x32_bf16 v[116:119], a[208:211], a[248:251], v[116:119]\n" \
"v_mfma_f32_16x16x32_bf16 v[184:187], a[208:211], v[76:79], v[184:187]\n" \
"v_mfma_f32_16x16x32_bf16 v[188:191], a[208:211], v[100:103], v[188:191]\n" \
"ds_read_b64_tr_b16 a[208:209], v35 offset:192\n" \
"ds_read_b64_tr_b16 a[210:211], v35 offset:2496\n" \
"v_mfma_f32_16x16x32_bf16 v[120:123], a[212:215], a[232:235], v[120:123]\n" \
"buffer_load_dwordx4 v3, s[80:83], s67 offen offset:3072 lds\n" \
"v_mfma_f32_16x16x32_bf16 v[124:127], a[212:215], a[248:251], v[124:127]\n" \
"s_add_u32 m0, s99, 0x15400\n" \
"v_mfma_f32_16x16x32_bf16 v[192:195], a[212:215], v[76:79], v[192:195]\n" \
"v_mfma_f32_16x16x32_bf16 v[196:199], a[212:215], v[100:103], v[196:199]\n" \
"ds_read_b64_tr_b16 a[212:213], v35 offset:224\n" \
"ds_read_b64_tr_b16 a[214:215], v35 offset:2528\n" \
"v_mfma_f32_16x16x32_bf16 v[128:131], a[216:219], a[232:235], v[128:131]\n" \
"v_mfma_f32_16x16x32_bf16 v[132:135], a[216:219], a[248:251], v[132:135]\n" \
"buffer_load_dwordx4 v4, s[80:83], s67 offen offset:0 lds\n" \
"v_mfma_f32_16x16x32_bf16 v[200:203], a[216:219], v[76:79], v[200:203]\n" \
"v_mfma_f32_16x16x32_bf16 v[204:207], a[216:219], v[100:103], v[204:207]\n" \
"ds_read_b64_tr_b16 a[216:217], v35 offset:256\n" \
"ds_read_b64_tr_b16 a[218:219], v35 offset:2560\n" \
"v_mfma_f32_16x16x32_bf16 v[136:139], a[0:3], a[232:235], v[136:139]\n" \
"v_mfma_f32_16x16x32_bf16 v[140:143], a[0:3], a[248:251], v[140:143]\n" \
"v_mfma_f32_16x16x32_bf16 v[208:211], a[0:3], v[76:79], v[208:211]\n" \
"buffer_load_dwordx4 v5, s[80:83], s67 offen offset:1024 lds\n" \
"v_mfma_f32_16x16x32_bf16 v[212:215], a[0:3], v[100:103], v[212:215]\n" \
"ds_read_b64_tr_b16 a[0:1], v35 offset:10368\n" \
"ds_read_b64_tr_b16 a[2:3], v35 offset:12672\n" \
"v_mfma_f32_16x16x32_bf16 v[144:147], a[4:7], a[232:235], v[144:147]\n" \
"v_mfma_f32_16x16x32_bf16 v[148:151], a[4:7], a[248:251], v[148:151]\n" \
"v_mfma_f32_16x16x32_bf16 v[216:219], a[4:7], v[76:79], v[216:219]\n" \
"v_mfma_f32_16x16x32_bf16 v[220:223], a[4:7], v[100:103], v[220:223]\n" \
"ds_read_b64_tr_b16 a[4:5], v35 offset:10400\n" \
"ds_read_b64_tr_b16 a[6:7], v35 offset:12704\n" \
"v_mfma_f32_16x16x32_bf16 v[152:155], a[8:11], a[232:235], v[152:155]\n" \
"buffer_load_dwordx4 v6, s[80:83], s67 offen offset:2048 lds\n" \
"v_mfma_f32_16x16x32_bf16 v[156:159], a[8:11], a[248:251], v[156:159]\n" \
"v_mfma_f32_16x16x32_bf16 v[224:227], a[8:11], v[76:79], v[224:227]\n" \
"v_mfma_f32_16x16x32_bf16 v[228:231], a[8:11], v[100:103], v[228:231]\n" \
"ds_read_b64_tr_b16 a[8:9], v35 offset:10432\n" \
"ds_read_b64_tr_b16 a[10:11], v35 offset:12736\n" \
"v_mfma_f32_16x16x32_bf16 v[160:163], a[12:15], a[232:235], v[160:163]\n" \
"v_mfma_f32_16x16x32_bf16 v[164:167], a[12:15], a[248:251], v[164:167]\n" \
"buffer_load_dwordx4 v7, s[80:83], s67 offen offset:3072 lds\n" \
"v_mfma_f32_16x16x32_bf16 v[232:235], a[12:15], v[76:79], v[232:235]\n" \
"s_add_u32 m0, s99, 0x16400\n" \
"v_mfma_f32_16x16x32_bf16 v[236:239], a[12:15], v[100:103], v[236:239]\n" \
"ds_read_b64_tr_b16 a[12:13], v35 offset:10464\n" \
"ds_read_b64_tr_b16 a[14:15], v35 offset:12768\n" \
"v_mfma_f32_16x16x32_bf16 v[168:171], a[16:19], a[232:235], v[168:171]\n" \
"v_mfma_f32_16x16x32_bf16 v[172:175], a[16:19], a[248:251], v[172:175]\n" \
"v_mfma_f32_16x16x32_bf16 v[240:243], a[16:19], v[76:79], v[240:243]\n" \
"buffer_load_dwordx4 v8, s[80:83], s67 offen offset:0 lds\n" \
"v_mfma_f32_16x16x32_bf16 v[244:247], a[16:19], v[100:103], v[244:247]\n" \
"s_add_u32 m0, s92, 0x0\n" \
"ds_read_b64_tr_b16 a[16:17], v35 offset:10496\n" \
"v_mfma_f32_16x16x32_bf16 v[176:179], a[20:23], a[232:235], v[176:179]\n" \
"ds_read_b64_tr_b16 a[18:19], v35 offset:12800\n" \
"v_readlane_b32 s67, v11, 46\n" \
"v_mfma_f32_16x16x32_bf16 v[180:183], a[20:23], a[248:251], v[180:183]\n" \
"v_mfma_f32_16x16x32_bf16 v[248:251], a[20:23], v[76:79], v[248:251]\n" \
"v_mfma_f32_16x16x32_bf16 v[252:255], a[20:23], v[100:103], v[252:255]\n" \
"buffer_load_dwordx4 v0, s[80:83], s73 offen offset:0 lds\n" \
"v_mfma_f32_16x16x32_bf16 v[112:115], a[24:27], a[96:99], v[112:115]\n" \
"ds_read_b64_tr_b16 a[20:21], v35 offset:10528\n" \
"ds_read_b64_tr_b16 a[22:23], v35 offset:12832\n" \
"v_mfma_f32_16x16x32_bf16 v[116:119], a[24:27], a[104:107], v[116:119]\n" \
"v_mfma_f32_16x16x32_bf16 v[184:187], a[24:27], a[112:115], v[184:187]\n" \
"v_mfma_f32_16x16x32_bf16 v[188:191], a[24:27], a[120:123], v[188:191]\n" \
"ds_read_b64_tr_b16 a[24:25], v35 offset:10560\n" \
"s_waitcnt lgkmcnt(7)\n" \
"v_mfma_f32_16x16x32_bf16 v[120:123], a[28:31], a[96:99], v[120:123]\n" \
"buffer_load_dword v47, v38, s[56:59], s71 offen sc1\n" \
"v_mfma_f32_16x16x32_bf16 v[124:127], a[28:31], a[104:107], v[124:127]\n" \
"ds_read_b64_tr_b16 a[26:27], v35 offset:12864\n" \
"v_mfma_f32_16x16x32_bf16 v[192:195], a[28:31], a[112:115], v[192:195]\n" \
"v_mfma_f32_16x16x32_bf16 v[196:199], a[28:31], a[120:123], v[196:199]\n" \
"ds_read_b64_tr_b16 a[28:29], v35 offset:10592\n" \
"ds_read_b64_tr_b16 a[30:31], v35 offset:12896\n" \
"v_mfma_f32_16x16x32_bf16 v[128:131], a[192:195], a[96:99], v[128:131]\n" \
"v_mfma_f32_16x16x32_bf16 v[132:135], a[192:195], a[104:107], v[132:135]\n" \
"buffer_load_dwordx4 v1, s[80:83], s73 offen offset:1024 lds\n" \
"v_mfma_f32_16x16x32_bf16 v[200:203], a[192:195], a[112:115], v[200:203]\n" \
"v_mfma_f32_16x16x32_bf16 v[204:207], a[192:195], a[120:123], v[204:207]\n" \
"ds_read_b64_tr_b16 a[192:193], v35 offset:10624\n" \
"ds_read_b64_tr_b16 a[194:195], v35 offset:12928\n" \
"v_mfma_f32_16x16x32_bf16 v[136:139], a[196:199], a[96:99], v[136:139]\n" \
"v_mfma_f32_16x16x32_bf16 v[140:143], a[196:199], a[104:107], v[140:143]\n" \
"v_mfma_f32_16x16x32_bf16 v[208:211], a[196:199], a[112:115], v[208:211]\n" \
"buffer_load_dwordx4 v2, s[80:83], s73 offen offset:2048 lds\n" \
"v_mfma_f32_16x16x32_bf16 v[212:215], a[196:199], a[120:123], v[212:215]\n" \
"v_mfma_f32_16x16x32_bf16 v[144:147], a[200:203], a[96:99], v[144:147]\n" \
"v_mfma_f32_16x16x32_bf16 v[148:151], a[200:203], a[104:107], v[148:151]\n" \
"v_mfma_f32_16x16x32_bf16 v[216:219], a[200:203], a[112:115], v[216:219]\n" \
"v_mfma_f32_16x16x32_bf16 v[220:223], a[200:203], a[120:123], v[220:223]\n" \
"buffer_load_dwordx4 v3, s[80:83], s73 offen offset:3072 lds\n" \
"v_mfma_f32_16x16x32_bf16 v[152:155], a[204:207], a[96:99], v[152:155]\n" \
"s_add_u32 m0, s92, 0x1000\n" \
"v_mfma_f32_16x16x32_bf16 v[156:159], a[204:207], a[104:107], v[156:159]\n" \
"v_mfma_f32_16x16x32_bf16 v[224:227], a[204:207], a[112:115], v[224:227]\n" \
"v_mfma_f32_16x16x32_bf16 v[228:231], a[204:207], a[120:123], v[228:231]\n" \
"v_mfma_f32_16x16x32_bf16 v[160:163], a[208:211], a[96:99], v[160:163]\n" \
"buffer_load_dwordx4 v4, s[80:83], s73 offen offset:0 lds\n" \
"v_mfma_f32_16x16x32_bf16 v[164:167], a[208:211], a[104:107], v[164:167]\n" \
"v_mfma_f32_16x16x32_bf16 v[232:235], a[208:211], a[112:115], v[232:235]\n" \
"v_mfma_f32_16x16x32_bf16 v[236:239], a[208:211], a[120:123], v[236:239]\n" \
"v_mfma_f32_16x16x32_bf16 v[168:171], a[212:215], a[96:99], v[168:171]\n" \
"v_mfma_f32_16x16x32_bf16 v[172:175], a[212:215], a[104:107], v[172:175]\n" \
"buffer_load_dwordx4 v5, s[80:83], s73 offen offset:1024 lds\n" \
"v_mfma_f32_16x16x32_bf16 v[240:243], a[212:215], a[112:115], v[240:243]\n" \
"v_mfma_f32_16x16x32_bf16 v[244:247], a[212:215], a[120:123], v[244:247]\n" \
"v_mfma_f32_16x16x32_bf16 v[176:179], a[216:219], a[96:99], v[176:179]\n" \
"v_mfma_f32_16x16x32_bf16 v[180:183], a[216:219], a[104:107], v[180:183]\n" \
"v_mfma_f32_16x16x32_bf16 v[248:251], a[216:219], a[112:115], v[248:251]\n" \
"buffer_load_dwordx4 v6, s[80:83], s73 offen offset:2048 lds\n" \
"v_mfma_f32_16x16x32_bf16 v[252:255], a[216:219], a[120:123], v[252:255]\n" \
"v_mfma_f32_16x16x32_bf16 v[112:115], a[0:3], a[100:103], v[112:115]\n" \
"v_mfma_f32_16x16x32_bf16 v[116:119], a[0:3], a[108:111], v[116:119]\n" \
"v_mfma_f32_16x16x32_bf16 v[184:187], a[0:3], a[116:119], v[184:187]\n" \
"v_mfma_f32_16x16x32_bf16 v[188:191], a[0:3], a[124:127], v[188:191]\n" \
"buffer_load_dwordx4 v7, s[80:83], s73 offen offset:3072 lds\n" \
"s_add_u32 m0, s92, 0x2000\n" \
"v_mfma_f32_16x16x32_bf16 v[120:123], a[4:7], a[100:103], v[120:123]\n" \
"v_readlane_b32 s92, v11, 44\n" \
"v_mfma_f32_16x16x32_bf16 v[124:127], a[4:7], a[108:111], v[124:127]\n" \
"v_mfma_f32_16x16x32_bf16 v[192:195], a[4:7], a[116:119], v[192:195]\n" \
"v_mfma_f32_16x16x32_bf16 v[196:199], a[4:7], a[124:127], v[196:199]\n" \
"buffer_load_dwordx4 v8, s[80:83], s73 offen offset:0 lds\n" \
"s_mov_b32 m0, s94\n" \
"v_mfma_f32_16x16x32_bf16 v[128:131], a[8:11], a[100:103], v[128:131]\n" \
"v_readlane_b32 s73, v11, 50\n" \
"v_readlane_b32 s94, v11, 46\n" \
"v_mfma_f32_16x16x32_bf16 v[132:135], a[8:11], a[108:111], v[132:135]\n" \
"v_mfma_f32_16x16x32_bf16 v[200:203], a[8:11], a[116:119], v[200:203]\n" \
"v_mfma_f32_16x16x32_bf16 v[204:207], a[8:11], a[124:127], v[204:207]\n" \
"buffer_load_dwordx4 v9, s[84:87], 0 offen lds\n" \
"s_waitcnt lgkmcnt(0)\n" \
"v_mfma_f32_16x16x32_bf16 v[136:139], a[12:15], a[100:103], v[136:139]\n" \
"v_mfma_f32_16x16x32_bf16 v[140:143], a[12:15], a[108:111], v[140:143]\n" \
"v_mfma_f32_16x16x32_bf16 v[208:211], a[12:15], a[116:119], v[208:211]\n" \
"v_mfma_f32_16x16x32_bf16 v[212:215], a[12:15], a[124:127], v[212:215]\n" \
"buffer_load_dwordx4 a[76:79], v20, s[56:59], s66 offen offset:3072 sc1\n" \
"s_waitcnt vmcnt(23)\n" \
"s_barrier\n" \
"v_mfma_f32_16x16x32_bf16 v[144:147], a[16:19], a[100:103], v[144:147]\n" \
"ds_read_b64_tr_b16 a[196:197], v10 offset:27648\n" \
"ds_read_b64_tr_b16 a[198:199], v10 offset:32256\n" \
"v_mfma_f32_16x16x32_bf16 v[148:151], a[16:19], a[108:111], v[148:151]\n" \
"ds_read_b64_tr_b16 a[200:201], v10 offset:27680\n" \
"ds_read_b64_tr_b16 a[202:203], v10 offset:32288\n" \
"v_mfma_f32_16x16x32_bf16 v[216:219], a[16:19], a[116:119], v[216:219]\n" \
"buffer_load_dwordx4 a[92:95], v32, s[56:59], s66 offen offset:3072 sc1\n" \
"v_mfma_f32_16x16x32_bf16 v[220:223], a[16:19], a[124:127], v[220:223]\n" \
"ds_read_b64_tr_b16 a[204:205], v10 offset:27712\n" \
"ds_read_b64_tr_b16 a[206:207], v10 offset:32320\n" \
"v_mfma_f32_16x16x32_bf16 v[152:155], a[20:23], a[100:103], v[152:155]\n" \
"ds_read_b64_tr_b16 a[208:209], v10 offset:27744\n" \
"ds_read_b64_tr_b16 a[210:211], v10 offset:32352\n" \
"v_mfma_f32_16x16x32_bf16 v[156:159], a[20:23], a[108:111], v[156:159]\n" \
"ds_read_b64_tr_b16 a[212:213], v10 offset:27776\n" \
"ds_read_b64_tr_b16 a[214:215], v10 offset:32384\n" \
"v_mfma_f32_16x16x32_bf16 v[224:227], a[20:23], a[116:119], v[224:227]\n" \
"ds_read_b64_tr_b16 a[216:217], v10 offset:27808\n" \
"ds_read_b64_tr_b16 a[218:219], v10 offset:32416\n" \
"buffer_load_dwordx4 a[128:131], v20, s[56:59], s68 offen offset:0 sc1\n" \
"v_mfma_f32_16x16x32_bf16 v[228:231], a[20:23], a[124:127], v[228:231]\n" \
"ds_read_b64_tr_b16 a[0:1], v10 offset:27840\n" \
"ds_read_b64_tr_b16 a[2:3], v10 offset:32448\n" \
"v_mfma_f32_16x16x32_bf16 v[160:163], a[24:27], a[100:103], v[160:163]\n" \
"ds_read_b64_tr_b16 a[4:5], v10 offset:27872\n" \
"ds_read_b64_tr_b16 a[6:7], v10 offset:32480\n" \
"v_mfma_f32_16x16x32_bf16 v[164:167], a[24:27], a[108:111], v[164:167]\n" \
"ds_read_b64_tr_b16 a[8:9], v10 offset:27904\n" \
"ds_read_b64_tr_b16 a[10:11], v10 offset:32512\n" \
"v_mfma_f32_16x16x32_bf16 v[232:235], a[24:27], a[116:119], v[232:235]\n" \
"ds_read_b64_tr_b16 a[12:13], v10 offset:36864\n" \
"ds_read_b64_tr_b16 a[14:15], v10 offset:41472\n" \
"buffer_load_dwordx4 a[144:147], v32, s[56:59], s68 offen offset:0 sc1\n" \
"v_mfma_f32_16x16x32_bf16 v[236:239], a[24:27], a[124:127], v[236:239]\n" \
"ds_read_b64_tr_b16 a[16:17], v10 offset:36896\n" \
"ds_read_b64_tr_b16 a[18:19], v10 offset:41504\n" \
"v_mfma_f32_16x16x32_bf16 v[168:171], a[28:31], a[100:103], v[168:171]\n" \
"ds_read_b64_tr_b16 a[20:21], v10 offset:36928\n" \
"ds_read_b64_tr_b16 a[22:23], v10 offset:41536\n" \
"v_mfma_f32_16x16x32_bf16 v[172:175], a[28:31], a[108:111], v[172:175]\n" \
"ds_read_b64_tr_b16 a[24:25], v10 offset:36960\n" \
"ds_read_b64_tr_b16 a[26:27], v10 offset:41568\n" \
"v_mfma_f32_16x16x32_bf16 v[240:243], a[28:31], a[116:119], v[240:243]\n" \
"v_readlane_b32 s66, v11, 45\n" \
"s_waitcnt vmcnt(13)\n" \
"v_cmp_ne_u32_e32 vcc, s62, v47\n" \
"buffer_load_dwordx4 a[132:135], v20, s[56:59], s68 offen offset:1024 sc1\n" \
"v_mfma_f32_16x16x32_bf16 v[244:247], a[28:31], a[124:127], v[244:247]\n" \
"ds_read_b64_tr_b16 a[28:29], v10 offset:36992\n" \
"ds_read_b64_tr_b16 a[30:31], v10 offset:41600\n" \
"v_mfma_f32_16x16x32_bf16 v[176:179], a[192:195], a[100:103], v[176:179]\n" \
"s_cbranch_vccnz L_sb_%=\n" \
"L_rb_%=:\n" \
"v_readlane_b32 s71, v11, 48\n" \
"v_mfma_f32_16x16x32_bf16 v[180:183], a[192:195], a[108:111], v[180:183]\n" \
"v_cmp_eq_u32_e32 vcc, 0, v44\n" \
"v_mfma_f32_16x16x32_bf16 v[248:251], a[192:195], a[116:119], v[248:251]\n" \
"v_mfma_f32_16x16x32_bf16 v[252:255], a[192:195], a[124:127], v[252:255]\n" \
"ds_read_b64_tr_b16 a[192:193], v10 offset:37024\n" \
"buffer_load_dwordx4 a[148:151], v32, s[56:59], s68 offen offset:1024 sc1\n" \
"s_waitcnt lgkmcnt(15)\n" \
"v_mfma_f32_16x16x32_bf16 v[112:115], a[196:199], a[64:67], v[112:115]\n" \
"ds_read_b64_tr_b16 a[194:195], v10 offset:41632\n" \
"v_cndmask_b32_e32 v50, 0, v45, vcc\n" \
"v_mfma_f32_16x16x32_bf16 v[116:119], a[196:199], a[80:83], v[116:119]\n" \
"ds_read_b64_tr_b16 a[196:197], v10 offset:37056\n" \
"ds_read_b64_tr_b16 a[198:199], v10 offset:41664\n" \
"v_mfma_f32_16x16x32_bf16 v[120:123], a[200:203], a[64:67], v[120:123]\n" \
"v_cmp_eq_u32_e32 vcc, 1, v44\n" \
"v_and_b32_e32 v44, 0x3c0, v20\n" \
"v_mfma_f32_16x16x32_bf16 v[124:127], a[200:203], a[80:83], v[124:127]\n" \
"buffer_load_dwordx4 a[136:139], v20, s[56:59], s68 offen offset:2048 sc1\n" \
"v_mfma_f32_16x16x32_bf16 v[128:131], a[204:207], a[64:67], v[128:131]\n" \
"ds_read_b64_tr_b16 a[200:201], v10 offset:37088\n" \
"ds_read_b64_tr_b16 a[202:203], v10 offset:41696\n" \
"v_mfma_f32_16x16x32_bf16 v[132:135], a[204:207], a[80:83], v[132:135]\n" \
"ds_read_b64_tr_b16 a[204:205], v10 offset:37120\n" \
"ds_read_b64_tr_b16 a[206:207], v10 offset:41728\n" \
"v_mfma_f32_16x16x32_bf16 v[136:139], a[208:211], a[64:67], v[136:139]\n" \
"v_cndmask_b32_e32 v51, 0, v45, vcc\n" \
"v_bfe_u32 v45, v20, 1, 5\n" \
"v_mfma_f32_16x16x32_bf16 v[140:143], a[208:211], a[80:83], v[140:143]\n" \
"ds_read_b64_tr_b16 a[208:209], v10 offset:46080\n" \
"ds_read_b64_tr_b16 a[210:211], v10 offset:50688\n" \
"buffer_load_dwordx4 a[152:155], v32, s[56:59], s68 offen offset:2048 sc1\n" \
"v_mfma_f32_16x16x32_bf16 v[144:147], a[212:215], a[64:67], v[144:147]\n" \
"v_and_b32_e32 v45, 24, v45\n" \
"v_add_u32_e32 v45, 0x1d400, v45\n" \
"v_mfma_f32_16x16x32_bf16 v[148:151], a[212:215], a[80:83], v[148:151]\n" \
"ds_read_b64_tr_b16 a[212:213], v10 offset:46112\n" \
"ds_read_b64_tr_b16 a[214:215], v10 offset:50720\n" \
"v_mfma_f32_16x16x32_bf16 v[152:155], a[216:219], a[64:67], v[152:155]\n" \
"v_add_u32_e32 v36, v44, v45\n" \
"v_mfma_f32_16x16x32_bf16 v[156:159], a[216:219], a[80:83], v[156:159]\n" \
"ds_read_b64_tr_b16 a[216:217], v10 offset:46144\n" \
"ds_read_b64_tr_b16 a[218:219], v10 offset:50752\n" \
"buffer_load_dwordx4 a[140:143], v20, s[56:59], s68 offen offset:3072 sc1\n" \
"v_mfma_f32_16x16x32_bf16 v[160:163], a[0:3], a[64:67], v[160:163]\n" \
"v_mfma_f32_16x16x32_bf16 v[164:167], a[0:3], a[80:83], v[164:167]\n" \
"ds_read_b64_tr_b16 a[0:1], v10 offset:46176\n" \
"s_waitcnt lgkmcnt(13)\n" \
"v_mfma_f32_16x16x32_bf16 v[168:171], a[4:7], a[64:67], v[168:171]\n" \
"ds_read_b64_tr_b16 a[2:3], v10 offset:50784\n" \
"v_mfma_f32_16x16x32_bf16 v[172:175], a[4:7], a[80:83], v[172:175]\n" \
"ds_read_b64_tr_b16 a[4:5], v10 offset:46208\n" \
"ds_read_b64_tr_b16 a[6:7], v10 offset:50816\n" \
"buffer_load_dwordx4 a[156:159], v32, s[56:59], s68 offen offset:3072 sc1\n" \
"v_mfma_f32_16x16x32_bf16 v[176:179], a[8:11], a[64:67], v[176:179]\n" \
"v_readlane_b32 s68, v11, 51\n" \
"v_mfma_f32_16x16x32_bf16 v[180:183], a[8:11], a[80:83], v[180:183]\n" \
"ds_read_b64_tr_b16 a[8:9], v10 offset:46240\n" \
"ds_read_b64_tr_b16 a[10:11], v10 offset:50848\n" \
"v_mfma_f32_16x16x32_bf16 v[112:115], a[12:15], a[68:71], v[112:115]\n" \
"v_mfma_f32_16x16x32_bf16 v[116:119], a[12:15], a[84:87], v[116:119]\n" \
"ds_read_b64_tr_b16 a[12:13], v10 offset:46272\n" \
"ds_read_b64_tr_b16 a[14:15], v10 offset:50880\n" \
"buffer_load_dwordx4 a[32:35], v20, s[56:59], s69 offen offset:0 sc1\n" \
"v_mfma_f32_16x16x32_bf16 v[120:123], a[16:19], a[68:71], v[120:123]\n" \
"v_mfma_f32_16x16x32_bf16 v[124:127], a[16:19], a[84:87], v[124:127]\n" \
"ds_read_b64_tr_b16 a[16:17], v10 offset:46304\n" \
"ds_read_b64_tr_b16 a[18:19], v10 offset:50912\n" \
"v_mfma_f32_16x16x32_bf16 v[128:131], a[20:23], a[68:71], v[128:131]\n" \
"v_mfma_f32_16x16x32_bf16 v[132:135], a[20:23], a[84:87], v[132:135]\n" \
"ds_read_b64_tr_b16 a[20:21], v10 offset:46336\n" \
"ds_read_b64_tr_b16 a[22:23], v10 offset:50944\n" \
"buffer_load_dwordx4 a[48:51], v32, s[56:59], s69 offen offset:0 sc1\n" \
"v_mfma_f32_16x16x32_bf16 v[136:139], a[24:27], a[68:71], v[136:139]\n" \
"v_mfma_f32_16x16x32_bf16 v[140:143], a[24:27], a[84:87], v[140:143]\n" \
"ds_read_b64_tr_b16 a[24:25], v10 offset:55296\n" \
"ds_read_b64_tr_b16 a[26:27], v10 offset:59904\n" \
"v_mfma_f32_16x16x32_bf16 v[144:147], a[28:31], a[68:71], v[144:147]\n" \
"v_mfma_f32_16x16x32_bf16 v[148:151], a[28:31], a[84:87], v[148:151]\n" \
"ds_read_b64_tr_b16 a[28:29], v10 offset:55328\n" \
"ds_read_b64_tr_b16 a[30:31], v10 offset:59936\n" \
"buffer_load_dwordx4 a[36:39], v20, s[56:59], s69 offen offset:1024 sc1\n" \
"v_mfma_f32_16x16x32_bf16 v[152:155], a[192:195], a[68:71], v[152:155]\n" \
"v_mfma_f32_16x16x32_bf16 v[156:159], a[192:195], a[84:87], v[156:159]\n" \
"ds_read_b64_tr_b16 a[192:193], v10 offset:55360\n" \
"s_waitcnt lgkmcnt(13)\n" \
"v_mfma_f32_16x16x32_bf16 v[160:163], a[196:199], a[68:71], v[160:163]\n" \
"ds_read_b64_tr_b16 a[194:195], v10 offset:59968\n" \
"v_mfma_f32_16x16x32_bf16 v[164:167], a[196:199], a[84:87], v[164:167]\n" \
"ds_read_b64_tr_b16 a[196:197], v10 offset:55392\n" \
"ds_read_b64_tr_b16 a[198:199], v10 offset:60000\n" \
"buffer_load_dwordx4 a[52:55], v32, s[56:59], s69 offen offset:1024 sc1\n" \
"v_mfma_f32_16x16x32_bf16 v[168:171], a[200:203], a[68:71], v[168:171]\n" \
"v_mfma_f32_16x16x32_bf16 v[172:175], a[200:203], a[84:87], v[172:175]\n" \
"ds_read_b64_tr_b16 a[200:201], v10 offset:55424\n" \
"ds_read_b64_tr_b16 a[202:203], v10 offset:60032\n" \
"v_mfma_f32_16x16x32_bf16 v[176:179], a[204:207], a[68:71], v[176:179]\n" \
"v_mfma_f32_16x16x32_bf16 v[180:183], a[204:207], a[84:87], v[180:183]\n" \
"ds_read_b64_tr_b16 a[204:205], v10 offset:55456\n" \
"ds_read_b64_tr_b16 a[206:207], v10 offset:60064\n" \
"buffer_load_dwordx4 a[40:43], v20, s[56:59], s69 offen offset:2048 sc1\n" \
"v_mfma_f32_16x16x32_bf16 v[112:115], a[208:211], a[72:75], v[112:115]\n" \
"v_mfma_f32_16x16x32_bf16 v[116:119], a[208:211], a[88:91], v[116:119]\n" \
"ds_read_b64_tr_b16 a[208:209], v10 offset:55488\n" \
"ds_read_b64_tr_b16 a[210:211], v10 offset:60096\n" \
"v_mfma_f32_16x16x32_bf16 v[120:123], a[212:215], a[72:75], v[120:123]\n" \
"v_mfma_f32_16x16x32_bf16 v[124:127], a[212:215], a[88:91], v[124:127]\n" \
"ds_read_b64_tr_b16 a[212:213], v10 offset:55520\n" \
"ds_read_b64_tr_b16 a[214:215], v10 offset:60128\n" \
"buffer_load_dwordx4 a[56:59], v32, s[56:59], s69 offen offset:2048 sc1\n" \
"v_mfma_f32_16x16x32_bf16 v[128:131], a[216:219], a[72:75], v[128:131]\n" \
"v_mfma_f32_16x16x32_bf16 v[132:135], a[216:219], a[88:91], v[132:135]\n" \
"ds_read_b64_tr_b16 a[216:217], v10 offset:55552\n" \
"ds_read_b64_tr_b16 a[218:219], v10 offset:60160\n" \
"v_mfma_f32_16x16x32_bf16 v[136:139], a[0:3], a[72:75], v[136:139]\n" \
"v_mfma_f32_16x16x32_bf16 v[140:143], a[0:3], a[88:91], v[140:143]\n" \
"v_mfma_f32_16x16x32_bf16 v[144:147], a[4:7], a[72:75], v[144:147]\n" \
"buffer_load_dwordx4 a[44:47], v20, s[56:59], s69 offen offset:3072 sc1\n" \
"v_mfma_f32_16x16x32_bf16 v[148:151], a[4:7], a[88:91], v[148:151]\n" \
"s_waitcnt lgkmcnt(10)\n" \
"v_mfma_f32_16x16x32_bf16 v[152:155], a[8:11], a[72:75], v[152:155]\n" \
"v_mfma_f32_16x16x32_bf16 v[156:159], a[8:11], a[88:91], v[156:159]\n" \
"v_mfma_f32_16x16x32_bf16 v[160:163], a[12:15], a[72:75], v[160:163]\n" \
"v_mfma_f32_16x16x32_bf16 v[164:167], a[12:15], a[88:91], v[164:167]\n" \
"buffer_load_dwordx4 a[60:63], v32, s[56:59], s69 offen offset:3072 sc1\n" \
"v_mfma_f32_16x16x32_bf16 v[168:171], a[16:19], a[72:75], v[168:171]\n" \
"v_readlane_b32 s69, v11, 6\n" \
"v_mfma_f32_16x16x32_bf16 v[172:175], a[16:19], a[88:91], v[172:175]\n" \
"v_mfma_f32_16x16x32_bf16 v[176:179], a[20:23], a[72:75], v[176:179]\n" \
"v_mfma_f32_16x16x32_bf16 v[180:183], a[20:23], a[88:91], v[180:183]\n" \
"s_waitcnt vmcnt(18)\n" \
"s_barrier\n" \
"buffer_load_dwordx4 a[96:99], v20, s[56:59], s64 offen offset:0 sc1\n" \
"ds_read_b64_tr_b16 a[0:1], v33 offset:18432\n" \
"ds_read_b64_tr_b16 a[2:3], v33 offset:23040\n" \
"ds_read_b64_tr_b16 a[4:5], v33 offset:27648\n" \
"ds_read_b64_tr_b16 a[6:7], v33 offset:32256\n" \
"ds_read_b64_tr_b16 a[8:9], v33 offset:36864\n" \
"ds_read_b64_tr_b16 a[10:11], v33 offset:41472\n" \
"ds_read_b64_tr_b16 a[12:13], v33 offset:46080\n" \
"ds_read_b64_tr_b16 a[14:15], v33 offset:50688\n" \
"ds_read_b64_tr_b16 a[16:17], v34 offset:0\n" \
"ds_read_b64_tr_b16 a[18:19], v34 offset:4608\n" \
"s_waitcnt vmcnt(18)\n" \
"v_mfma_f32_16x16x32_bf16 v[112:115], a[24:27], a[76:79], v[112:115]\n" \
"ds_read_b64_tr_b16 a[20:21], v34 offset:9216\n" \
"ds_read_b64_tr_b16 a[22:23], v34 offset:13824\n" \
"buffer_load_dwordx4 a[112:115], v32, s[56:59], s64 offen offset:0 sc1\n" \
"s_waitcnt vmcnt(18)\n" \
"v_mfma_f32_16x16x32_bf16 v[116:119], a[24:27], a[92:95], v[116:119]\n" \
"ds_read_b64_tr_b16 a[24:25], v34 offset:18432\n" \
"ds_read_b64_tr_b16 a[26:27], v34 offset:23040\n" \
"buffer_load_dwordx4 a[100:103], v20, s[56:59], s64 offen offset:1024 sc1\n" \
"v_mfma_f32_16x16x32_bf16 v[120:123], a[28:31], a[76:79], v[120:123]\n" \
"v_mfma_f32_16x16x32_bf16 v[124:127], a[28:31], a[92:95], v[124:127]\n" \
"ds_read_b64_tr_b16 a[28:29], v33 offset:0\n" \
"ds_read_b64_tr_b16 a[30:31], v33 offset:4608\n" \
"v_mfma_f32_16x16x32_bf16 v[128:131], a[192:195], a[76:79], v[128:131]\n" \
"v_mfma_f32_16x16x32_bf16 v[132:135], a[192:195], a[92:95], v[132:135]\n" \
"ds_read_b64_tr_b16 a[192:193], v33 offset:18464\n" \
"ds_read_b64_tr_b16 a[194:195], v33 offset:23072\n" \
"buffer_load_dwordx4 a[116:119], v32, s[56:59], s64 offen offset:1024 sc1\n" \
"v_mfma_f32_16x16x32_bf16 v[136:139], a[196:199], a[76:79], v[136:139]\n" \
"v_mfma_f32_16x16x32_bf16 v[140:143], a[196:199], a[92:95], v[140:143]\n" \
"ds_read_b64_tr_b16 a[196:197], v33 offset:27680\n" \
"s_waitcnt lgkmcnt(15)\n" \
"v_mfma_f32_16x16x32_bf16 v[144:147], a[200:203], a[76:79], v[144:147]\n" \
"ds_read_b64_tr_b16 a[198:199], v33 offset:32288\n" \
"v_mfma_f32_16x16x32_bf16 v[148:151], a[200:203], a[92:95], v[148:151]\n" \
"ds_read_b64_tr_b16 a[200:201], v33 offset:36896\n" \
"ds_read_b64_tr_b16 a[202:203], v33 offset:41504\n" \
"buffer_load_dwordx4 a[104:107], v20, s[56:59], s64 offen offset:2048 sc1\n" \
"v_mfma_f32_16x16x32_bf16 v[152:155], a[204:207], a[76:79], v[152:155]\n" \
"v_mfma_f32_16x16x32_bf16 v[156:159], a[204:207], a[92:95], v[156:159]\n" \
"ds_read_b64_tr_b16 a[204:205], v33 offset:46112\n" \
"ds_read_b64_tr_b16 a[206:207], v33 offset:50720\n" \
"v_mfma_f32_16x16x32_bf16 v[160:163], a[208:211], a[76:79], v[160:163]\n" \
"v_mfma_f32_16x16x32_bf16 v[164:167], a[208:211], a[92:95], v[164:167]\n" \
"ds_read_b64_tr_b16 a[208:209], v34 offset:32\n" \
"ds_read_b64_tr_b16 a[210:211], v34 offset:4640\n" \
"buffer_load_dwordx4 a[120:123], v32, s[56:59], s64 offen offset:2048 sc1\n" \
"v_mfma_f32_16x16x32_bf16 v[168:171], a[212:215], a[76:79], v[168:171]\n" \
"v_mfma_f32_16x16x32_bf16 v[172:175], a[212:215], a[92:95], v[172:175]\n" \
"ds_read_b64_tr_b16 a[212:213], v34 offset:9248\n" \
"ds_read_b64_tr_b16 a[214:215], v34 offset:13856\n" \
"v_mfma_f32_16x16x32_bf16 v[176:179], a[216:219], a[76:79], v[176:179]\n" \
"v_mfma_f32_16x16x32_bf16 v[180:183], a[216:219], a[92:95], v[180:183]\n" \
"ds_read_b64_tr_b16 a[216:217], v34 offset:18464\n" \
"buffer_load_dwordx4 a[108:111], v20, s[56:59], s64 offen offset:3072 sc1\n" \
"s_waitcnt vmcnt(22)\n" \
"v_mfma_f32_16x16x32_bf16 v[112:115], a[0:3], a[128:131], v[112:115]\n" \
"ds_read_b64_tr_b16 a[218:219], v34 offset:23072\n" \
"s_waitcnt vmcnt(21)\n" \
"v_mfma_f32_16x16x32_bf16 v[116:119], a[0:3], a[144:147], v[116:119]\n" \
"ds_read_b64_tr_b16 a[0:1], v33 offset:32\n" \
"s_waitcnt vmcnt(20)\n" \
"v_mfma_f32_16x16x32_bf16 v[112:115], a[4:7], a[132:135], v[112:115]\n" \
"ds_read_b64_tr_b16 a[2:3], v33 offset:4640\n" \
"s_waitcnt vmcnt(19)\n" \
"v_mfma_f32_16x16x32_bf16 v[116:119], a[4:7], a[148:151], v[116:119]\n" \
"ds_read_b64_tr_b16 a[4:5], v33 offset:18496\n" \
"buffer_load_dwordx4 a[124:127], v32, s[56:59], s64 offen offset:3072 sc1\n" \
"s_waitcnt vmcnt(19) lgkmcnt(11)\n" \
"v_mfma_f32_16x16x32_bf16 v[112:115], a[8:11], a[136:139], v[112:115]\n" \
"ds_read_b64_tr_b16 a[6:7], v33 offset:23104\n" \
"v_readlane_b32 s64, v11, 40\n" \
"s_waitcnt vmcnt(18)\n" \
"v_mfma_f32_16x16x32_bf16 v[116:119], a[8:11], a[152:155], v[116:119]\n" \
"buffer_load_dwordx4 a[160:163], v20, s[56:59], s72 offen offset:0 sc1\n" \
"ds_read_b64_tr_b16 a[8:9], v33 offset:27712\n" \
"ds_read_b64_tr_b16 a[10:11], v33 offset:32320\n" \
"s_waitcnt vmcnt(18)\n" \
"v_mfma_f32_16x16x32_bf16 v[112:115], a[12:15], a[140:143], v[112:115]\n" \
"buffer_load_dwordx4 a[176:179], v32, s[56:59], s72 offen offset:0 sc1\n" \
"s_waitcnt vmcnt(18)\n" \
"v_mfma_f32_16x16x32_bf16 v[116:119], a[12:15], a[156:159], v[116:119]\n" \
"buffer_load_dwordx4 a[164:167], v20, s[56:59], s72 offen offset:1024 sc1\n" \
"ds_read_b64_tr_b16 a[12:13], v33 offset:36928\n" \
"ds_read_b64_tr_b16 a[14:15], v33 offset:41536\n" \
"s_waitcnt vmcnt(18)\n" \
"v_mfma_f32_16x16x32_bf16 v[112:115], a[16:19], a[32:35], v[112:115]\n" \
"buffer_load_dwordx4 a[180:183], v32, s[56:59], s72 offen offset:1024 sc1\n" \
"s_waitcnt vmcnt(18)\n" \
"v_mfma_f32_16x16x32_bf16 v[116:119], a[16:19], a[48:51], v[116:119]\n" \
"buffer_load_dwordx4 a[168:171], v20, s[56:59], s72 offen offset:2048 sc1\n" \
"ds_read_b64_tr_b16 a[16:17], v33 offset:46144\n" \
"ds_read_b64_tr_b16 a[18:19], v33 offset:50752\n" \
"s_waitcnt vmcnt(18)\n" \
"v_mfma_f32_16x16x32_bf16 v[112:115], a[20:23], a[36:39], v[112:115]\n" \
"buffer_load_dwordx4 a[184:187], v32, s[56:59], s72 offen offset:2048 sc1\n" \
"s_waitcnt vmcnt(18)\n" \
"v_mfma_f32_16x16x32_bf16 v[116:119], a[20:23], a[52:55], v[116:119]\n" \
"buffer_load_dwordx4 a[172:175], v20, s[56:59], s72 offen offset:3072 sc1\n" \
"ds_read_b64_tr_b16 a[20:21], v34 offset:64\n" \
"ds_read_b64_tr_b16 a[22:23], v34 offset:4672\n" \
"s_waitcnt vmcnt(18)\n" \
"v_mfma_f32_16x16x32_bf16 v[112:115], a[24:27], a[40:43], v[112:115]\n" \
"buffer_load_dwordx4 a[188:191], v32, s[56:59], s72 offen offset:3072 sc1\n" \
"v_readlane_b32 s72, v11, 49\n" \
"s_waitcnt vmcnt(18)\n" \
"v_mfma_f32_16x16x32_bf16 v[116:119], a[24:27], a[56:59], v[116:119]\n" \
"buffer_load_dwordx4 a[220:223], v20, s[56:59], s93 offen offset:0 sc1\n" \
"ds_read_b64_tr_b16 a[24:25], v34 offset:9280\n" \
"ds_read_b64_tr_b16 a[26:27], v34 offset:13888\n" \
"s_waitcnt vmcnt(18)\n" \
"v_mfma_f32_16x16x32_bf16 v[112:115], a[28:31], a[44:47], v[112:115]\n" \
"buffer_load_dwordx4 a[236:239], v32, s[56:59], s93 offen offset:0 sc1\n" \
"s_waitcnt vmcnt(18)\n" \
"v_mfma_f32_16x16x32_bf16 v[116:119], a[28:31], a[60:63], v[116:119]\n" \
"buffer_load_dwordx4 a[224:227], v20, s[56:59], s93 offen offset:1024 sc1\n" \
"v_mfma_f32_16x16x32_bf16 v[120:123], a[192:195], a[128:131], v[120:123]\n" \
"ds_read_b64_tr_b16 a[28:29], v34 offset:18496\n" \
"ds_read_b64_tr_b16 a[30:31], v34 offset:23104\n" \
"v_mfma_f32_16x16x32_bf16 v[124:127], a[192:195], a[144:147], v[124:127]\n" \
"v_cvt_pk_bf16_f32 v112, v112, v113\n" \
"v_cvt_pk_bf16_f32 v113, v114, v115\n" \
"v_mfma_f32_16x16x32_bf16 v[120:123], a[196:199], a[132:135], v[120:123]\n" \
"v_cvt_pk_bf16_f32 v114, v116, v117\n" \
"v_cvt_pk_bf16_f32 v115, v118, v119\n" \
"buffer_store_dwordx4 v[112:115], v20, s[56:59], s65 offen offset:0 sc1\n" \
"s_add_u32 s65, s65, 0x3800\n" \
"v_mfma_f32_16x16x32_bf16 v[124:127], a[196:199], a[148:151], v[124:127]\n" \
"ds_read_b64_tr_b16 a[192:193], v33 offset:64\n" \
"ds_read_b64_tr_b16 a[194:195], v33 offset:4672\n" \
"v_mfma_f32_16x16x32_bf16 v[120:123], a[200:203], a[136:139], v[120:123]\n" \
"ds_read_b64_tr_b16 a[196:197], v10 offset:27904\n" \
"ds_read_b64_tr_b16 a[198:199], v10 offset:32512\n" \
"v_mfma_f32_16x16x32_bf16 v[124:127], a[200:203], a[152:155], v[124:127]\n" \
"ds_read_b64_tr_b16 a[200:201], v10 offset:37120\n" \
"s_waitcnt lgkmcnt(7)\n" \
"v_mfma_f32_16x16x32_bf16 v[120:123], a[204:207], a[140:143], v[120:123]\n" \
"buffer_load_dwordx4 a[240:243], v32, s[56:59], s93 offen offset:1024 sc1\n" \
"v_mfma_f32_16x16x32_bf16 v[124:127], a[204:207], a[156:159], v[124:127]\n" \
"ds_read_b64_tr_b16 a[202:203], v10 offset:41728\n" \
"ds_read_b64_tr_b16 a[204:205], v10 offset:46336\n" \
"v_mfma_f32_16x16x32_bf16 v[120:123], a[208:211], a[32:35], v[120:123]\n" \
"ds_read_b64_tr_b16 a[206:207], v10 offset:50944\n" \
"v_mfma_f32_16x16x32_bf16 v[124:127], a[208:211], a[48:51], v[124:127]\n" \
"ds_read_b64_tr_b16 a[208:209], v10 offset:55552\n" \
"ds_read_b64_tr_b16 a[210:211], v10 offset:60160\n" \
"v_mfma_f32_16x16x32_bf16 v[120:123], a[212:215], a[36:39], v[120:123]\n" \
"v_mfma_f32_16x16x32_bf16 v[124:127], a[212:215], a[52:55], v[124:127]\n" \
"buffer_load_dwordx4 a[228:231], v20, s[56:59], s93 offen offset:2048 sc1\n" \
"v_mfma_f32_16x16x32_bf16 v[120:123], a[216:219], a[40:43], v[120:123]\n" \
"ds_read_b64_tr_b16 a[212:213], v33 offset:18688\n" \
"ds_read_b64_tr_b16 a[214:215], v33 offset:23296\n" \
"v_mfma_f32_16x16x32_bf16 v[124:127], a[216:219], a[56:59], v[124:127]\n" \
"ds_read_b64_tr_b16 a[216:217], v33 offset:27904\n" \
"ds_read_b64_tr_b16 a[218:219], v33 offset:32512\n" \
"v_mfma_f32_16x16x32_bf16 v[120:123], a[0:3], a[44:47], v[120:123]\n" \
"v_mfma_f32_16x16x32_bf16 v[124:127], a[0:3], a[60:63], v[124:127]\n" \
"ds_read_b64_tr_b16 a[0:1], v33 offset:37120\n" \
"ds_read_b64_tr_b16 a[2:3], v33 offset:41728\n" \
"buffer_load_dwordx4 a[244:247], v32, s[56:59], s93 offen offset:2048 sc1\n" \
"v_mfma_f32_16x16x32_bf16 v[128:131], a[4:7], a[128:131], v[128:131]\n" \
"v_mfma_f32_16x16x32_bf16 v[132:135], a[4:7], a[144:147], v[132:135]\n" \
"ds_read_b64_tr_b16 a[4:5], v33 offset:46336\n" \
"ds_read_b64_tr_b16 a[6:7], v33 offset:50944\n" \
"v_cvt_pk_bf16_f32 v120, v120, v121\n" \
"v_cvt_pk_bf16_f32 v121, v122, v123\n" \
"v_cvt_pk_bf16_f32 v122, v124, v125\n" \
"v_cvt_pk_bf16_f32 v123, v126, v127\n" \
"buffer_store_dwordx4 v[120:123], v20, s[56:59], s66 offen offset:0 sc1\n" \
"s_add_u32 s66, s66, 0x3000\n" \
"v_mfma_f32_16x16x32_bf16 v[128:131], a[8:11], a[132:135], v[128:131]\n" \
"v_mfma_f32_16x16x32_bf16 v[132:135], a[8:11], a[148:151], v[132:135]\n" \
"ds_read_b64_tr_b16 a[8:9], v34 offset:256\n" \
"ds_read_b64_tr_b16 a[10:11], v34 offset:4864\n" \
"v_mfma_f32_16x16x32_bf16 v[128:131], a[12:15], a[136:139], v[128:131]\n" \
"v_mfma_f32_16x16x32_bf16 v[132:135], a[12:15], a[152:155], v[132:135]\n" \
"buffer_load_dwordx4 a[232:235], v20, s[56:59], s93 offen offset:3072 sc1\n" \
"v_mfma_f32_16x16x32_bf16 v[128:131], a[16:19], a[140:143], v[128:131]\n" \
"ds_read_b64_tr_b16 a[12:13], v34 offset:9472\n" \
"ds_read_b64_tr_b16 a[14:15], v34 offset:14080\n" \
"v_mfma_f32_16x16x32_bf16 v[132:135], a[16:19], a[156:159], v[132:135]\n" \
"ds_read_b64_tr_b16 a[16:17], v34 offset:18688\n" \
"ds_read_b64_tr_b16 a[18:19], v34 offset:23296\n" \
"v_mfma_f32_16x16x32_bf16 v[128:131], a[20:23], a[32:35], v[128:131]\n" \
"v_mfma_f32_16x16x32_bf16 v[132:135], a[20:23], a[48:51], v[132:135]\n" \
"ds_read_b64_tr_b16 a[20:21], v33 offset:256\n" \
"ds_read_b64_tr_b16 a[22:23], v33 offset:4864\n" \
"buffer_load_dwordx4 a[248:251], v32, s[56:59], s93 offen offset:3072 sc1\n" \
"v_mfma_f32_16x16x32_bf16 v[128:131], a[24:27], a[36:39], v[128:131]\n" \
"v_readlane_b32 s93, v11, 45\n" \
"v_mfma_f32_16x16x32_bf16 v[132:135], a[24:27], a[52:55], v[132:135]\n" \
"ds_read_b64_tr_b16 a[24:25], v10 offset:27744\n" \
"s_waitcnt lgkmcnt(11)\n" \
"v_mfma_f32_16x16x32_bf16 v[128:131], a[28:31], a[40:43], v[128:131]\n" \
"ds_read_b64_tr_b16 a[26:27], v10 offset:32352\n" \
"v_mfma_f32_16x16x32_bf16 v[132:135], a[28:31], a[56:59], v[132:135]\n" \
"ds_read_b64_tr_b16 a[28:29], v10 offset:36960\n" \
"ds_read_b64_tr_b16 a[30:31], v10 offset:41568\n" \
"v_mfma_f32_16x16x32_bf16 v[128:131], a[192:195], a[44:47], v[128:131]\n" \
"buffer_load_dwordx4 v[64:67], v20, s[56:59], s95 offen offset:0 sc1\n" \
"v_mfma_f32_16x16x32_bf16 v[132:135], a[192:195], a[60:63], v[132:135]\n" \
"ds_read_b64_tr_b16 a[192:193], v10 offset:46176\n" \
"s_waitcnt vmcnt(26)\n" \
"v_mfma_f32_16x16x32_bf16 v[248:251], a[196:199], a[96:99], v[248:251]\n" \
"ds_read_b64_tr_b16 a[194:195], v10 offset:50784\n" \
"s_waitcnt vmcnt(25)\n" \
"v_mfma_f32_16x16x32_bf16 v[252:255], a[196:199], a[112:115], v[252:255]\n" \
"v_cvt_pk_bf16_f32 v128, v128, v129\n" \
"v_cvt_pk_bf16_f32 v129, v130, v131\n" \
"v_cvt_pk_bf16_f32 v130, v132, v133\n" \
"v_cvt_pk_bf16_f32 v131, v134, v135\n" \
"buffer_store_dwordx4 v[128:131], v20, s[56:59], s67 offen offset:0 sc1\n" \
"s_add_u32 s67, s67, 0x2800\n" \
"s_waitcnt vmcnt(25)\n" \
"v_mfma_f32_16x16x32_bf16 v[248:251], a[200:203], a[100:103], v[248:251]\n" \
"ds_read_b64_tr_b16 a[196:197], v10 offset:55392\n" \
"s_waitcnt vmcnt(24)\n" \
"v_mfma_f32_16x16x32_bf16 v[252:255], a[200:203], a[116:119], v[252:255]\n" \
"ds_read_b64_tr_b16 a[198:199], v10 offset:60000\n" \
"s_waitcnt vmcnt(23)\n" \
"v_mfma_f32_16x16x32_bf16 v[248:251], a[204:207], a[104:107], v[248:251]\n" \
"ds_read_b64_tr_b16 a[200:201], v33 offset:18528\n" \
"s_waitcnt vmcnt(22)\n" \
"v_mfma_f32_16x16x32_bf16 v[252:255], a[204:207], a[120:123], v[252:255]\n" \
"ds_read_b64_tr_b16 a[202:203], v33 offset:23136\n" \
"s_waitcnt vmcnt(21)\n" \
"v_mfma_f32_16x16x32_bf16 v[248:251], a[208:211], a[108:111], v[248:251]\n" \
"ds_read_b64_tr_b16 a[204:205], v33 offset:27744\n" \
"s_waitcnt vmcnt(20)\n" \
"v_mfma_f32_16x16x32_bf16 v[252:255], a[208:211], a[124:127], v[252:255]\n" \
"ds_read_b64_tr_b16 a[206:207], v33 offset:32352\n" \
"ds_read_b64_tr_b16 a[208:209], v33 offset:36960\n" \
"v_mfma_f32_16x16x32_bf16 v[176:179], a[212:215], a[128:131], v[176:179]\n" \
"buffer_load_dwordx4 v[72:75], v32, s[56:59], s95 offen offset:0 sc1\n" \
"v_mfma_f32_16x16x32_bf16 v[180:183], a[212:215], a[144:147], v[180:183]\n" \
"ds_read_b64_tr_b16 a[210:211], v33 offset:41568\n" \
"s_waitcnt vmcnt(20)\n" \
"v_mfma_f32_16x16x32_bf16 v[248:251], a[212:215], a[160:163], v[248:251]\n" \
"s_waitcnt vmcnt(19)\n" \
"v_mfma_f32_16x16x32_bf16 v[252:255], a[212:215], a[176:179], v[252:255]\n" \
"ds_read_b64_tr_b16 a[212:213], v33 offset:46176\n" \
"ds_read_b64_tr_b16 a[214:215], v33 offset:50784\n" \
"v_mfma_f32_16x16x32_bf16 v[176:179], a[216:219], a[132:135], v[176:179]\n" \
"buffer_load_dwordx4 v[80:83], v20, s[56:59], s96 offen offset:0 sc1\n" \
"v_mfma_f32_16x16x32_bf16 v[180:183], a[216:219], a[148:151], v[180:183]\n" \
"s_waitcnt vmcnt(19)\n" \
"v_mfma_f32_16x16x32_bf16 v[248:251], a[216:219], a[164:167], v[248:251]\n" \
"s_waitcnt vmcnt(18)\n" \
"v_mfma_f32_16x16x32_bf16 v[252:255], a[216:219], a[180:183], v[252:255]\n" \
"ds_read_b64_tr_b16 a[216:217], v34 offset:96\n" \
"ds_read_b64_tr_b16 a[218:219], v34 offset:4704\n" \
"v_mfma_f32_16x16x32_bf16 v[176:179], a[0:3], a[136:139], v[176:179]\n" \
"buffer_load_dwordx4 v[88:91], v32, s[56:59], s96 offen offset:0 sc1\n" \
"v_mfma_f32_16x16x32_bf16 v[180:183], a[0:3], a[152:155], v[180:183]\n" \
"s_waitcnt vmcnt(18)\n" \
"v_mfma_f32_16x16x32_bf16 v[248:251], a[0:3], a[168:171], v[248:251]\n" \
"s_waitcnt vmcnt(17)\n" \
"v_mfma_f32_16x16x32_bf16 v[252:255], a[0:3], a[184:187], v[252:255]\n" \
"ds_read_b64_tr_b16 a[0:1], v34 offset:9312\n" \
"s_waitcnt lgkmcnt(3)\n" \
"v_mfma_f32_16x16x32_bf16 v[176:179], a[4:7], a[140:143], v[176:179]\n" \
"buffer_load_dwordx4 v[68:71], v20, s[56:59], s95 offen offset:1024 sc1\n" \
"v_mfma_f32_16x16x32_bf16 v[180:183], a[4:7], a[156:159], v[180:183]\n" \
"ds_read_b64_tr_b16 a[2:3], v34 offset:13920\n" \
"s_waitcnt vmcnt(17)\n" \
"v_mfma_f32_16x16x32_bf16 v[248:251], a[4:7], a[172:175], v[248:251]\n" \
"s_waitcnt vmcnt(16)\n" \
"v_mfma_f32_16x16x32_bf16 v[252:255], a[4:7], a[188:191], v[252:255]\n" \
"ds_read_b64_tr_b16 a[4:5], v34 offset:18528\n" \
"ds_read_b64_tr_b16 a[6:7], v34 offset:23136\n" \
"v_mfma_f32_16x16x32_bf16 v[176:179], a[8:11], a[32:35], v[176:179]\n" \
"buffer_load_dwordx4 v[76:79], v32, s[56:59], s95 offen offset:1024 sc1\n" \
"v_mfma_f32_16x16x32_bf16 v[180:183], a[8:11], a[48:51], v[180:183]\n" \
"s_waitcnt vmcnt(16)\n" \
"v_mfma_f32_16x16x32_bf16 v[248:251], a[8:11], a[220:223], v[248:251]\n" \
"s_waitcnt vmcnt(15)\n" \
"v_mfma_f32_16x16x32_bf16 v[252:255], a[8:11], a[236:239], v[252:255]\n" \
"ds_read_b64_tr_b16 a[8:9], v33 offset:96\n" \
"ds_read_b64_tr_b16 a[10:11], v33 offset:4704\n" \
"v_mfma_f32_16x16x32_bf16 v[176:179], a[12:15], a[36:39], v[176:179]\n" \
"buffer_load_dwordx4 v[84:87], v20, s[56:59], s96 offen offset:1024 sc1\n" \
"v_mfma_f32_16x16x32_bf16 v[180:183], a[12:15], a[52:55], v[180:183]\n" \
"s_waitcnt vmcnt(15)\n" \
"v_mfma_f32_16x16x32_bf16 v[248:251], a[12:15], a[224:227], v[248:251]\n" \
"s_waitcnt vmcnt(13)\n" \
"v_mfma_f32_16x16x32_bf16 v[252:255], a[12:15], a[240:243], v[252:255]\n" \
"ds_read_b64_tr_b16 a[12:13], v10 offset:27776\n" \
"ds_read_b64_tr_b16 a[14:15], v10 offset:32384\n" \
"s_mov_b64 exec, 1\n" \
"buffer_store_dword v39, off, s[56:59], s65 offset:0 sc1\n" \
"s_mov_b64 exec, -1\n" \
"v_mfma_f32_16x16x32_bf16 v[176:179], a[16:19], a[40:43], v[176:179]\n" \
"v_mfma_f32_16x16x32_bf16 v[180:183], a[16:19], a[56:59], v[180:183]\n" \
"s_waitcnt vmcnt(13)\n" \
"v_mfma_f32_16x16x32_bf16 v[248:251], a[16:19], a[228:231], v[248:251]\n" \
"buffer_load_dwordx4 v[92:95], v32, s[56:59], s96 offen offset:1024 sc1\n" \
"s_waitcnt vmcnt(13)\n" \
"v_mfma_f32_16x16x32_bf16 v[252:255], a[16:19], a[244:247], v[252:255]\n" \
"ds_read_b64_tr_b16 a[16:17], v10 offset:36992\n" \
"ds_read_b64_tr_b16 a[18:19], v10 offset:41600\n" \
"v_mfma_f32_16x16x32_bf16 v[176:179], a[20:23], a[44:47], v[176:179]\n" \
"buffer_load_dwordx4 v[96:99], v20, s[56:59], s97 offen offset:0 sc1\n" \
"v_mfma_f32_16x16x32_bf16 v[180:183], a[20:23], a[60:63], v[180:183]\n" \
"s_waitcnt vmcnt(12)\n" \
"v_mfma_f32_16x16x32_bf16 v[248:251], a[20:23], a[232:235], v[248:251]\n" \
"s_mov_b64 exec, 1\n" \
"buffer_store_dword v39, off, s[56:59], s66 offset:4 sc1\n" \
"s_mov_b64 exec, -1\n" \
"s_waitcnt vmcnt(12)\n" \
"v_mfma_f32_16x16x32_bf16 v[252:255], a[20:23], a[248:251], v[252:255]\n" \
"v_cvt_pk_bf16_f32 v176, v176, v177\n" \
"v_cvt_pk_bf16_f32 v177, v178, v179\n" \
"v_cvt_pk_bf16_f32 v178, v180, v181\n" \
"v_cvt_pk_bf16_f32 v248, v248, v249\n" \
"v_cvt_pk_bf16_f32 v249, v250, v251\n" \
"v_cvt_pk_bf16_f32 v179, v182, v183\n" \
"buffer_store_dwordx4 v[176:179], v20, s[56:59], s68 offen offset:0 sc1\n" \
"v_mfma_f32_16x16x32_bf16 v[208:211], a[24:27], a[96:99], v[208:211]\n" \
"v_cvt_pk_bf16_f32 v250, v252, v253\n" \
"v_cvt_pk_bf16_f32 v251, v254, v255\n" \
"v_mfma_f32_16x16x32_bf16 v[212:215], a[24:27], a[112:115], v[212:215]\n" \
"ds_read_b64_tr_b16 a[20:21], v10 offset:46208\n" \
"ds_read_b64_tr_b16 a[22:23], v10 offset:50816\n" \
"buffer_store_dwordx4 v[248:251], v20, s[56:59], s68 offen offset:1024 sc1\n" \
"v_mfma_f32_16x16x32_bf16 v[208:211], a[28:31], a[100:103], v[208:211]\n" \
"ds_read_b64_tr_b16 a[24:25], v10 offset:55424\n" \
"ds_read_b64_tr_b16 a[26:27], v10 offset:60032\n" \
"v_mfma_f32_16x16x32_bf16 v[212:215], a[28:31], a[116:119], v[212:215]\n" \
"ds_read_b64_tr_b16 a[28:29], v33 offset:18560\n" \
"ds_read_b64_tr_b16 a[30:31], v33 offset:23168\n" \
"v_mfma_f32_16x16x32_bf16 v[208:211], a[192:195], a[104:107], v[208:211]\n" \
"v_mfma_f32_16x16x32_bf16 v[212:215], a[192:195], a[120:123], v[212:215]\n" \
"ds_read_b64_tr_b16 a[192:193], v33 offset:27776\n" \
"ds_read_b64_tr_b16 a[194:195], v33 offset:32384\n" \
"s_waitcnt vmcnt(12)\n" \
"s_mov_b64 exec, 1\n" \
"buffer_store_dword v39, off, s[56:59], s67 offset:8 sc1\n" \
"s_mov_b64 exec, -1\n" \
"v_mfma_f32_16x16x32_bf16 v[208:211], a[196:199], a[108:111], v[208:211]\n" \
"v_mfma_f32_16x16x32_bf16 v[212:215], a[196:199], a[124:127], v[212:215]\n" \
"ds_read_b64_tr_b16 a[196:197], v33 offset:36992\n" \
"ds_read_b64_tr_b16 a[198:199], v33 offset:41600\n" \
"v_mfma_f32_16x16x32_bf16 v[136:139], a[200:203], a[128:131], v[136:139]\n" \
"v_mfma_f32_16x16x32_bf16 v[140:143], a[200:203], a[144:147], v[140:143]\n" \
"buffer_load_dwordx4 v[104:107], v32, s[56:59], s97 offen offset:0 sc1\n" \
"v_mfma_f32_16x16x32_bf16 v[208:211], a[200:203], a[160:163], v[208:211]\n" \
"v_mfma_f32_16x16x32_bf16 v[212:215], a[200:203], a[176:179], v[212:215]\n" \
"ds_read_b64_tr_b16 a[200:201], v33 offset:46208\n" \
"ds_read_b64_tr_b16 a[202:203], v33 offset:50816\n" \
"v_mfma_f32_16x16x32_bf16 v[136:139], a[204:207], a[132:135], v[136:139]\n" \
"v_mfma_f32_16x16x32_bf16 v[140:143], a[204:207], a[148:151], v[140:143]\n" \
"v_mfma_f32_16x16x32_bf16 v[208:211], a[204:207], a[164:167], v[208:211]\n" \
"buffer_load_dwordx4 v[100:103], v20, s[56:59], s97 offen offset:1024 sc1\n" \
"v_mfma_f32_16x16x32_bf16 v[212:215], a[204:207], a[180:183], v[212:215]\n" \
"ds_read_b64_tr_b16 a[204:205], v34 offset:128\n" \
"ds_read_b64_tr_b16 a[206:207], v34 offset:4736\n" \
"v_mfma_f32_16x16x32_bf16 v[136:139], a[208:211], a[136:139], v[136:139]\n" \
"v_mfma_f32_16x16x32_bf16 v[140:143], a[208:211], a[152:155], v[140:143]\n" \
"v_mfma_f32_16x16x32_bf16 v[208:211], a[208:211], a[168:171], v[208:211]\n" \
"v_mfma_f32_16x16x32_bf16 v[212:215], a[208:211], a[184:187], v[212:215]\n" \
"ds_read_b64_tr_b16 a[208:209], v34 offset:9344\n" \
"ds_read_b64_tr_b16 a[210:211], v34 offset:13952\n" \
"v_mfma_f32_16x16x32_bf16 v[136:139], a[212:215], a[140:143], v[136:139]\n" \
"buffer_load_dwordx4 v[108:111], v32, s[56:59], s97 offen offset:1024 sc1\n" \
"v_mfma_f32_16x16x32_bf16 v[140:143], a[212:215], a[156:159], v[140:143]\n" \
"v_mfma_f32_16x16x32_bf16 v[208:211], a[212:215], a[172:175], v[208:211]\n" \
"v_mfma_f32_16x16x32_bf16 v[212:215], a[212:215], a[188:191], v[212:215]\n" \
"ds_read_b64_tr_b16 a[212:213], v34 offset:18560\n" \
"s_waitcnt lgkmcnt(7)\n" \
"v_mfma_f32_16x16x32_bf16 v[136:139], a[216:219], a[32:35], v[136:139]\n" \
"ds_read_b64_tr_b16 a[214:215], v34 offset:23168\n" \
"v_mfma_f32_16x16x32_bf16 v[140:143], a[216:219], a[48:51], v[140:143]\n" \
"v_mfma_f32_16x16x32_bf16 v[208:211], a[216:219], a[220:223], v[208:211]\n" \
"v_mfma_f32_16x16x32_bf16 v[212:215], a[216:219], a[236:239], v[212:215]\n" \
"ds_read_b64_tr_b16 a[216:217], v33 offset:128\n" \
"ds_read_b64_tr_b16 a[218:219], v33 offset:4736\n" \
"v_mfma_f32_16x16x32_bf16 v[136:139], a[0:3], a[36:39], v[136:139]\n" \
"v_mfma_f32_16x16x32_bf16 v[140:143], a[0:3], a[52:55], v[140:143]\n" \
"v_mfma_f32_16x16x32_bf16 v[208:211], a[0:3], a[224:227], v[208:211]\n" \
"v_mfma_f32_16x16x32_bf16 v[212:215], a[0:3], a[240:243], v[212:215]\n" \
"ds_read_b64_tr_b16 a[0:1], v10 offset:27808\n" \
"ds_read_b64_tr_b16 a[2:3], v10 offset:32416\n" \
"v_mfma_f32_16x16x32_bf16 v[136:139], a[4:7], a[40:43], v[136:139]\n" \
"v_mfma_f32_16x16x32_bf16 v[140:143], a[4:7], a[56:59], v[140:143]\n" \
"v_mfma_f32_16x16x32_bf16 v[208:211], a[4:7], a[228:231], v[208:211]\n" \
"v_mfma_f32_16x16x32_bf16 v[212:215], a[4:7], a[244:247], v[212:215]\n" \
"ds_read_b64_tr_b16 a[4:5], v10 offset:37024\n" \
"ds_read_b64_tr_b16 a[6:7], v10 offset:41632\n" \
"v_mfma_f32_16x16x32_bf16 v[136:139], a[8:11], a[44:47], v[136:139]\n" \
"v_mfma_f32_16x16x32_bf16 v[140:143], a[8:11], a[60:63], v[140:143]\n" \
"v_mfma_f32_16x16x32_bf16 v[208:211], a[8:11], a[232:235], v[208:211]\n" \
"v_mfma_f32_16x16x32_bf16 v[212:215], a[8:11], a[248:251], v[212:215]\n" \
"ds_read_b64_tr_b16 a[8:9], v10 offset:46240\n" \
"ds_read_b64_tr_b16 a[10:11], v10 offset:50848\n" \
"v_mfma_f32_16x16x32_bf16 v[216:219], a[12:15], a[96:99], v[216:219]\n" \
"v_mfma_f32_16x16x32_bf16 v[220:223], a[12:15], a[112:115], v[220:223]\n" \
"ds_read_b64_tr_b16 a[12:13], v10 offset:55456\n" \
"v_cvt_pk_bf16_f32 v136, v136, v137\n" \
"v_cvt_pk_bf16_f32 v137, v138, v139\n" \
"v_cvt_pk_bf16_f32 v208, v208, v209\n" \
"v_cvt_pk_bf16_f32 v209, v210, v211\n" \
"v_cvt_pk_bf16_f32 v138, v140, v141\n" \
"v_cvt_pk_bf16_f32 v139, v142, v143\n" \
"v_cvt_pk_bf16_f32 v210, v212, v213\n" \
"v_cvt_pk_bf16_f32 v211, v214, v215\n" \
"buffer_store_dwordx4 v[136:139], v20, s[56:59], s70 offen offset:0 sc1\n" \
"v_mfma_f32_16x16x32_bf16 v[216:219], a[16:19], a[100:103], v[216:219]\n" \
"ds_read_b64_tr_b16 a[14:15], v10 offset:60064\n" \
"v_mfma_f32_16x16x32_bf16 v[220:223], a[16:19], a[116:119], v[220:223]\n" \
"ds_read_b64_tr_b16 a[16:17], v33 offset:18592\n" \
"ds_read_b64_tr_b16 a[18:19], v33 offset:23200\n" \
"buffer_store_dwordx4 v[208:211], v20, s[56:59], s70 offen offset:1024 sc1\n" \
"s_add_u32 s70, s70, 0x2000\n" \
"v_mfma_f32_16x16x32_bf16 v[216:219], a[20:23], a[104:107], v[216:219]\n" \
"v_mfma_f32_16x16x32_bf16 v[220:223], a[20:23], a[120:123], v[220:223]\n" \
"ds_read_b64_tr_b16 a[20:21], v33 offset:27808\n" \
"ds_read_b64_tr_b16 a[22:23], v33 offset:32416\n" \
"v_mfma_f32_16x16x32_bf16 v[216:219], a[24:27], a[108:111], v[216:219]\n" \
"v_mfma_f32_16x16x32_bf16 v[220:223], a[24:27], a[124:127], v[220:223]\n" \
"ds_read_b64_tr_b16 a[24:25], v33 offset:37024\n" \
"ds_read_b64_tr_b16 a[26:27], v33 offset:41632\n" \
"v_mfma_f32_16x16x32_bf16 v[144:147], a[28:31], a[128:131], v[144:147]\n" \
"v_mfma_f32_16x16x32_bf16 v[148:151], a[28:31], a[144:147], v[148:151]\n" \
"v_mfma_f32_16x16x32_bf16 v[216:219], a[28:31], a[160:163], v[216:219]\n" \
"v_mfma_f32_16x16x32_bf16 v[220:223], a[28:31], a[176:179], v[220:223]\n" \
"ds_read_b64_tr_b16 a[28:29], v33 offset:46240\n" \
"ds_read_b64_tr_b16 a[30:31], v33 offset:50848\n" \
"v_mfma_f32_16x16x32_bf16 v[144:147], a[192:195], a[132:135], v[144:147]\n" \
"v_mfma_f32_16x16x32_bf16 v[148:151], a[192:195], a[148:151], v[148:151]\n" \
"v_mfma_f32_16x16x32_bf16 v[216:219], a[192:195], a[164:167], v[216:219]\n" \
"v_mfma_f32_16x16x32_bf16 v[220:223], a[192:195], a[180:183], v[220:223]\n" \
"ds_read_b64_tr_b16 a[192:193], v34 offset:160\n" \
"ds_read_b64_tr_b16 a[194:195], v34 offset:4768\n" \
"v_mfma_f32_16x16x32_bf16 v[144:147], a[196:199], a[136:139], v[144:147]\n" \
"v_mfma_f32_16x16x32_bf16 v[148:151], a[196:199], a[152:155], v[148:151]\n" \
"v_mfma_f32_16x16x32_bf16 v[216:219], a[196:199], a[168:171], v[216:219]\n" \
"v_mfma_f32_16x16x32_bf16 v[220:223], a[196:199], a[184:187], v[220:223]\n" \
"ds_read_b64_tr_b16 a[196:197], v34 offset:9376\n" \
"s_waitcnt lgkmcnt(7)\n" \
"v_mfma_f32_16x16x32_bf16 v[144:147], a[200:203], a[140:143], v[144:147]\n" \
"ds_read_b64_tr_b16 a[198:199], v34 offset:13984\n" \
"v_mfma_f32_16x16x32_bf16 v[148:151], a[200:203], a[156:159], v[148:151]\n" \
"v_mfma_f32_16x16x32_bf16 v[216:219], a[200:203], a[172:175], v[216:219]\n" \
"v_mfma_f32_16x16x32_bf16 v[220:223], a[200:203], a[188:191], v[220:223]\n" \
"ds_read_b64_tr_b16 a[200:201], v34 offset:18592\n" \
"ds_read_b64_tr_b16 a[202:203], v34 offset:23200\n" \
"v_mfma_f32_16x16x32_bf16 v[144:147], a[204:207], a[32:35], v[144:147]\n" \
"v_mfma_f32_16x16x32_bf16 v[148:151], a[204:207], a[48:51], v[148:151]\n" \
"v_mfma_f32_16x16x32_bf16 v[216:219], a[204:207], a[220:223], v[216:219]\n" \
"v_mfma_f32_16x16x32_bf16 v[220:223], a[204:207], a[236:239], v[220:223]\n" \
"ds_read_b64_tr_b16 a[204:205], v33 offset:160\n" \
"ds_read_b64_tr_b16 a[206:207], v33 offset:4768\n" \
"v_mfma_f32_16x16x32_bf16 v[144:147], a[208:211], a[36:39], v[144:147]\n" \
"v_mfma_f32_16x16x32_bf16 v[148:151], a[208:211], a[52:55], v[148:151]\n" \
"v_mfma_f32_16x16x32_bf16 v[216:219], a[208:211], a[224:227], v[216:219]\n" \
"v_mfma_f32_16x16x32_bf16 v[220:223], a[208:211], a[240:243], v[220:223]\n" \
"ds_read_b64_tr_b16 a[208:209], v10 offset:27840\n" \
"ds_read_b64_tr_b16 a[210:211], v10 offset:32448\n" \
"v_mfma_f32_16x16x32_bf16 v[144:147], a[212:215], a[40:43], v[144:147]\n" \
"v_mfma_f32_16x16x32_bf16 v[148:151], a[212:215], a[56:59], v[148:151]\n" \
"v_mfma_f32_16x16x32_bf16 v[216:219], a[212:215], a[228:231], v[216:219]\n" \
"v_mfma_f32_16x16x32_bf16 v[220:223], a[212:215], a[244:247], v[220:223]\n" \
"ds_read_b64_tr_b16 a[212:213], v10 offset:37056\n" \
"ds_read_b64_tr_b16 a[214:215], v10 offset:41664\n" \
"v_mfma_f32_16x16x32_bf16 v[144:147], a[216:219], a[44:47], v[144:147]\n" \
"v_mfma_f32_16x16x32_bf16 v[148:151], a[216:219], a[60:63], v[148:151]\n" \
"v_mfma_f32_16x16x32_bf16 v[216:219], a[216:219], a[232:235], v[216:219]\n" \
"v_mfma_f32_16x16x32_bf16 v[220:223], a[216:219], a[248:251], v[220:223]\n" \
"ds_read_b64_tr_b16 a[216:217], v10 offset:46272\n" \
"ds_read_b64_tr_b16 a[218:219], v10 offset:50880\n" \
"v_mfma_f32_16x16x32_bf16 v[224:227], a[0:3], a[96:99], v[224:227]\n" \
"v_mfma_f32_16x16x32_bf16 v[228:231], a[0:3], a[112:115], v[228:231]\n" \
"ds_read_b64_tr_b16 a[0:1], v10 offset:55488\n" \
"v_cvt_pk_bf16_f32 v144, v144, v145\n" \
"v_cvt_pk_bf16_f32 v145, v146, v147\n" \
"v_cvt_pk_bf16_f32 v216, v216, v217\n" \
"v_cvt_pk_bf16_f32 v217, v218, v219\n" \
"v_cvt_pk_bf16_f32 v146, v148, v149\n" \
"v_cvt_pk_bf16_f32 v147, v150, v151\n" \
"v_cvt_pk_bf16_f32 v218, v220, v221\n" \
"v_cvt_pk_bf16_f32 v219, v222, v223\n" \
"buffer_store_dwordx4 v[144:147], v20, s[56:59], s71 offen offset:0 sc1\n" \
"v_mfma_f32_16x16x32_bf16 v[224:227], a[4:7], a[100:103], v[224:227]\n" \
"ds_read_b64_tr_b16 a[2:3], v10 offset:60096\n" \
"v_mfma_f32_16x16x32_bf16 v[228:231], a[4:7], a[116:119], v[228:231]\n" \
"ds_read_b64_tr_b16 a[4:5], v33 offset:18624\n" \
"ds_read_b64_tr_b16 a[6:7], v33 offset:23232\n" \
"buffer_store_dwordx4 v[216:219], v20, s[56:59], s71 offen offset:1024 sc1\n" \
"s_add_u32 s71, s71, 0x1800\n" \
"v_mfma_f32_16x16x32_bf16 v[224:227], a[8:11], a[104:107], v[224:227]\n" \
"v_mfma_f32_16x16x32_bf16 v[228:231], a[8:11], a[120:123], v[228:231]\n" \
"ds_read_b64_tr_b16 a[8:9], v33 offset:27840\n" \
"ds_read_b64_tr_b16 a[10:11], v33 offset:32448\n" \
"v_mfma_f32_16x16x32_bf16 v[224:227], a[12:15], a[108:111], v[224:227]\n" \
"v_mfma_f32_16x16x32_bf16 v[228:231], a[12:15], a[124:127], v[228:231]\n" \
"ds_read_b64_tr_b16 a[12:13], v33 offset:37056\n" \
"ds_read_b64_tr_b16 a[14:15], v33 offset:41664\n" \
"v_mfma_f32_16x16x32_bf16 v[152:155], a[16:19], a[128:131], v[152:155]\n" \
"s_waitcnt vmcnt(8)\n" \
"s_mov_b64 exec, 3\n" \
"buffer_store_dword v39, v41, s[56:59], s69 offen sc1\n" \
"s_mov_b64 exec, -1\n" \
"v_mfma_f32_16x16x32_bf16 v[156:159], a[16:19], a[144:147], v[156:159]\n" \
"v_mfma_f32_16x16x32_bf16 v[224:227], a[16:19], a[160:163], v[224:227]\n" \
"v_mfma_f32_16x16x32_bf16 v[228:231], a[16:19], a[176:179], v[228:231]\n" \
"ds_read_b64_tr_b16 a[16:17], v33 offset:46272\n" \
"ds_read_b64_tr_b16 a[18:19], v33 offset:50880\n" \
"v_mfma_f32_16x16x32_bf16 v[152:155], a[20:23], a[132:135], v[152:155]\n" \
"v_mfma_f32_16x16x32_bf16 v[156:159], a[20:23], a[148:151], v[156:159]\n" \
"v_mfma_f32_16x16x32_bf16 v[224:227], a[20:23], a[164:167], v[224:227]\n" \
"v_mfma_f32_16x16x32_bf16 v[228:231], a[20:23], a[180:183], v[228:231]\n" \
"ds_read_b64_tr_b16 a[20:21], v34 offset:192\n" \
"s_waitcnt lgkmcnt(9)\n" \
"v_mfma_f32_16x16x32_bf16 v[152:155], a[24:27], a[136:139], v[152:155]\n" \
"ds_read_b64_tr_b16 a[22:23], v34 offset:4800\n" \
"v_mfma_f32_16x16x32_bf16 v[156:159], a[24:27], a[152:155], v[156:159]\n" \
"v_mfma_f32_16x16x32_bf16 v[224:227], a[24:27], a[168:171], v[224:227]\n" \
"v_mfma_f32_16x16x32_bf16 v[228:231], a[24:27], a[184:187], v[228:231]\n" \
"ds_read_b64_tr_b16 a[24:25], v34 offset:9408\n" \
"ds_read_b64_tr_b16 a[26:27], v34 offset:14016\n" \
"v_mfma_f32_16x16x32_bf16 v[152:155], a[28:31], a[140:143], v[152:155]\n" \
"v_mfma_f32_16x16x32_bf16 v[156:159], a[28:31], a[156:159], v[156:159]\n" \
"v_mfma_f32_16x16x32_bf16 v[224:227], a[28:31], a[172:175], v[224:227]\n" \
"v_mfma_f32_16x16x32_bf16 v[228:231], a[28:31], a[188:191], v[228:231]\n" \
"ds_read_b64_tr_b16 a[28:29], v34 offset:18624\n" \
"ds_read_b64_tr_b16 a[30:31], v34 offset:23232\n" \
"v_mfma_f32_16x16x32_bf16 v[152:155], a[192:195], a[32:35], v[152:155]\n" \
"v_mfma_f32_16x16x32_bf16 v[156:159], a[192:195], a[48:51], v[156:159]\n" \
"v_mfma_f32_16x16x32_bf16 v[224:227], a[192:195], a[220:223], v[224:227]\n" \
"v_mfma_f32_16x16x32_bf16 v[228:231], a[192:195], a[236:239], v[228:231]\n" \
"ds_read_b64_tr_b16 a[192:193], v33 offset:192\n" \
"ds_read_b64_tr_b16 a[194:195], v33 offset:4800\n" \
"v_mfma_f32_16x16x32_bf16 v[152:155], a[196:199], a[36:39], v[152:155]\n" \
"v_mfma_f32_16x16x32_bf16 v[156:159], a[196:199], a[52:55], v[156:159]\n" \
"v_mfma_f32_16x16x32_bf16 v[224:227], a[196:199], a[224:227], v[224:227]\n" \
"v_mfma_f32_16x16x32_bf16 v[228:231], a[196:199], a[240:243], v[228:231]\n" \
"ds_read_b64_tr_b16 a[196:197], v10 offset:27648\n" \
"ds_read_b64_tr_b16 a[198:199], v10 offset:32256\n" \
"v_mfma_f32_16x16x32_bf16 v[152:155], a[200:203], a[40:43], v[152:155]\n" \
"v_mfma_f32_16x16x32_bf16 v[156:159], a[200:203], a[56:59], v[156:159]\n" \
"v_mfma_f32_16x16x32_bf16 v[224:227], a[200:203], a[228:231], v[224:227]\n" \
"v_mfma_f32_16x16x32_bf16 v[228:231], a[200:203], a[244:247], v[228:231]\n" \
"ds_read_b64_tr_b16 a[200:201], v10 offset:36864\n" \
"ds_read_b64_tr_b16 a[202:203], v10 offset:41472\n" \
"v_mfma_f32_16x16x32_bf16 v[152:155], a[204:207], a[44:47], v[152:155]\n" \
"v_mfma_f32_16x16x32_bf16 v[156:159], a[204:207], a[60:63], v[156:159]\n" \
"v_mfma_f32_16x16x32_bf16 v[224:227], a[204:207], a[232:235], v[224:227]\n" \
"v_mfma_f32_16x16x32_bf16 v[228:231], a[204:207], a[248:251], v[228:231]\n" \
"ds_read_b64_tr_b16 a[204:205], v10 offset:46080\n" \
"ds_read_b64_tr_b16 a[206:207], v10 offset:50688\n" \
"v_mfma_f32_16x16x32_bf16 v[232:235], a[208:211], a[96:99], v[232:235]\n" \
"v_mfma_f32_16x16x32_bf16 v[236:239], a[208:211], a[112:115], v[236:239]\n" \
"ds_read_b64_tr_b16 a[208:209], v10 offset:55296\n" \
"v_cvt_pk_bf16_f32 v152, v152, v153\n" \
"v_cvt_pk_bf16_f32 v153, v154, v155\n" \
"v_cvt_pk_bf16_f32 v224, v224, v225\n" \
"v_cvt_pk_bf16_f32 v225, v226, v227\n" \
"v_cvt_pk_bf16_f32 v154, v156, v157\n" \
"v_cvt_pk_bf16_f32 v155, v158, v159\n" \
"v_cvt_pk_bf16_f32 v226, v228, v229\n" \
"v_cvt_pk_bf16_f32 v227, v230, v231\n" \
"buffer_store_dwordx4 v[152:155], v20, s[56:59], s72 offen offset:0 sc1\n" \
"v_mfma_f32_16x16x32_bf16 v[232:235], a[212:215], a[100:103], v[232:235]\n" \
"ds_read_b64_tr_b16 a[210:211], v10 offset:59904\n" \
"v_mfma_f32_16x16x32_bf16 v[236:239], a[212:215], a[116:119], v[236:239]\n" \
"ds_read_b64_tr_b16 a[212:213], v33 offset:18432\n" \
"ds_read_b64_tr_b16 a[214:215], v33 offset:23040\n" \
"buffer_store_dwordx4 v[224:227], v20, s[56:59], s72 offen offset:1024 sc1\n" \
"s_add_u32 s72, s72, 0x1000\n" \
"v_mfma_f32_16x16x32_bf16 v[232:235], a[216:219], a[104:107], v[232:235]\n" \
"v_mfma_f32_16x16x32_bf16 v[236:239], a[216:219], a[120:123], v[236:239]\n" \
"ds_read_b64_tr_b16 a[216:217], v33 offset:27648\n" \
"ds_read_b64_tr_b16 a[218:219], v33 offset:32256\n" \
"v_mfma_f32_16x16x32_bf16 v[232:235], a[0:3], a[108:111], v[232:235]\n" \
"v_mfma_f32_16x16x32_bf16 v[236:239], a[0:3], a[124:127], v[236:239]\n" \
"ds_read_b64_tr_b16 a[0:1], v33 offset:36864\n" \
"s_waitcnt lgkmcnt(9)\n" \
"v_mfma_f32_16x16x32_bf16 v[160:163], a[4:7], a[128:131], v[160:163]\n" \
"ds_read_b64_tr_b16 a[2:3], v33 offset:41472\n" \
"v_mfma_f32_16x16x32_bf16 v[164:167], a[4:7], a[144:147], v[164:167]\n" \
"v_mfma_f32_16x16x32_bf16 v[232:235], a[4:7], a[160:163], v[232:235]\n" \
"v_mfma_f32_16x16x32_bf16 v[236:239], a[4:7], a[176:179], v[236:239]\n" \
"ds_read_b64_tr_b16 a[4:5], v33 offset:46080\n" \
"ds_read_b64_tr_b16 a[6:7], v33 offset:50688\n" \
"s_waitcnt vmcnt(5)\n" \
"s_mov_b64 exec, 1\n" \
"buffer_store_dword v39, off, s[56:59], s70 offset:16 sc1\n" \
"s_mov_b64 exec, -1\n" \
"v_mfma_f32_16x16x32_bf16 v[160:163], a[8:11], a[132:135], v[160:163]\n" \
"v_mfma_f32_16x16x32_bf16 v[164:167], a[8:11], a[148:151], v[164:167]\n" \
"v_mfma_f32_16x16x32_bf16 v[232:235], a[8:11], a[164:167], v[232:235]\n" \
"v_mfma_f32_16x16x32_bf16 v[236:239], a[8:11], a[180:183], v[236:239]\n" \
"ds_read_b64_tr_b16 a[8:9], v34 offset:0\n" \
"ds_read_b64_tr_b16 a[10:11], v34 offset:4608\n" \
"v_mfma_f32_16x16x32_bf16 v[160:163], a[12:15], a[136:139], v[160:163]\n" \
"v_mfma_f32_16x16x32_bf16 v[164:167], a[12:15], a[152:155], v[164:167]\n" \
"v_mfma_f32_16x16x32_bf16 v[232:235], a[12:15], a[168:171], v[232:235]\n" \
"v_mfma_f32_16x16x32_bf16 v[236:239], a[12:15], a[184:187], v[236:239]\n" \
"ds_read_b64_tr_b16 a[12:13], v34 offset:9216\n" \
"ds_read_b64_tr_b16 a[14:15], v34 offset:13824\n" \
"v_mfma_f32_16x16x32_bf16 v[160:163], a[16:19], a[140:143], v[160:163]\n" \
"v_mfma_f32_16x16x32_bf16 v[164:167], a[16:19], a[156:159], v[164:167]\n" \
"v_mfma_f32_16x16x32_bf16 v[232:235], a[16:19], a[172:175], v[232:235]\n" \
"v_mfma_f32_16x16x32_bf16 v[236:239], a[16:19], a[188:191], v[236:239]\n" \
"ds_read_b64_tr_b16 a[16:17], v34 offset:18432\n" \
"ds_read_b64_tr_b16 a[18:19], v34 offset:23040\n" \
"v_mfma_f32_16x16x32_bf16 v[160:163], a[20:23], a[32:35], v[160:163]\n" \
"v_mfma_f32_16x16x32_bf16 v[164:167], a[20:23], a[48:51], v[164:167]\n" \
"v_mfma_f32_16x16x32_bf16 v[232:235], a[20:23], a[220:223], v[232:235]\n" \
"v_mfma_f32_16x16x32_bf16 v[236:239], a[20:23], a[236:239], v[236:239]\n" \
"ds_read_b64_tr_b16 a[20:21], v33 offset:0\n" \
"ds_read_b64_tr_b16 a[22:23], v33 offset:4608\n" \
"v_mfma_f32_16x16x32_bf16 v[160:163], a[24:27], a[36:39], v[160:163]\n" \
"v_mfma_f32_16x16x32_bf16 v[164:167], a[24:27], a[52:55], v[164:167]\n" \
"v_mfma_f32_16x16x32_bf16 v[232:235], a[24:27], a[224:227], v[232:235]\n" \
"v_mfma_f32_16x16x32_bf16 v[236:239], a[24:27], a[240:243], v[236:239]\n" \
"ds_read_b64_tr_b16 a[24:25], v10 offset:27680\n" \
"ds_read_b64_tr_b16 a[26:27], v10 offset:32288\n" \
"v_mfma_f32_16x16x32_bf16 v[160:163], a[28:31], a[40:43], v[160:163]\n" \
"v_mfma_f32_16x16x32_bf16 v[164:167], a[28:31], a[56:59], v[164:167]\n" \
"v_mfma_f32_16x16x32_bf16 v[232:235], a[28:31], a[228:231], v[232:235]\n" \
"v_mfma_f32_16x16x32_bf16 v[236:239], a[28:31], a[244:247], v[236:239]\n" \
"ds_read_b64_tr_b16 a[28:29], v10 offset:36896\n" \
"ds_read_b64_tr_b16 a[30:31], v10 offset:41504\n" \
"v_mfma_f32_16x16x32_bf16 v[160:163], a[192:195], a[44:47], v[160:163]\n" \
"v_mfma_f32_16x16x32_bf16 v[164:167], a[192:195], a[60:63], v[164:167]\n" \
"v_mfma_f32_16x16x32_bf16 v[232:235], a[192:195], a[232:235], v[232:235]\n" \
"v_mfma_f32_16x16x32_bf16 v[236:239], a[192:195], a[248:251], v[236:239]\n" \
"ds_read_b64_tr_b16 a[192:193], v10 offset:46112\n" \
"ds_read_b64_tr_b16 a[194:195], v10 offset:50720\n" \
"v_mfma_f32_16x16x32_bf16 v[184:187], a[196:199], a[96:99], v[184:187]\n" \
"v_mfma_f32_16x16x32_bf16 v[188:191], a[196:199], a[112:115], v[188:191]\n" \
"ds_read_b64_tr_b16 a[196:197], v10 offset:55328\n" \
"v_cvt_pk_bf16_f32 v160, v160, v161\n" \
"v_cvt_pk_bf16_f32 v161, v162, v163\n" \
"v_cvt_pk_bf16_f32 v232, v232, v233\n" \
"v_cvt_pk_bf16_f32 v233, v234, v235\n" \
"v_cvt_pk_bf16_f32 v162, v164, v165\n" \
"v_cvt_pk_bf16_f32 v163, v166, v167\n" \
"v_cvt_pk_bf16_f32 v234, v236, v237\n" \
"v_cvt_pk_bf16_f32 v235, v238, v239\n" \
"buffer_store_dwordx4 v[160:163], v20, s[56:59], s73 offen offset:0 sc1\n" \
"v_mfma_f32_16x16x32_bf16 v[184:187], a[200:203], a[100:103], v[184:187]\n" \
"ds_read_b64_tr_b16 a[198:199], v10 offset:59936\n" \
"v_mfma_f32_16x16x32_bf16 v[188:191], a[200:203], a[116:119], v[188:191]\n" \
"ds_read_b64_tr_b16 a[200:201], v33 offset:18464\n" \
"s_waitcnt lgkmcnt(7)\n" \
"v_mfma_f32_16x16x32_bf16 v[184:187], a[204:207], a[104:107], v[184:187]\n" \
"buffer_store_dwordx4 v[232:235], v20, s[56:59], s73 offen offset:1024 sc1\n" \
"s_add_u32 s73, s73, 0x800\n" \
"v_mfma_f32_16x16x32_bf16 v[188:191], a[204:207], a[120:123], v[188:191]\n" \
"ds_read_b64_tr_b16 a[202:203], v33 offset:23072\n" \
"ds_read_b64_tr_b16 a[204:205], v33 offset:27680\n" \
"v_mfma_f32_16x16x32_bf16 v[184:187], a[208:211], a[108:111], v[184:187]\n" \
"ds_read_b64_tr_b16 a[206:207], v33 offset:32288\n" \
"v_mfma_f32_16x16x32_bf16 v[188:191], a[208:211], a[124:127], v[188:191]\n" \
"ds_read_b64_tr_b16 a[208:209], v33 offset:36896\n" \
"ds_read_b64_tr_b16 a[210:211], v33 offset:41504\n" \
"v_mfma_f32_16x16x32_bf16 v[184:187], a[212:215], a[160:163], v[184:187]\n" \
"v_mfma_f32_16x16x32_bf16 v[188:191], a[212:215], a[176:179], v[188:191]\n" \
"ds_read_b64_tr_b16 a[212:213], v33 offset:46112\n" \
"ds_read_b64_tr_b16 a[214:215], v33 offset:50720\n" \
"s_waitcnt vmcnt(6)\n" \
"s_mov_b64 exec, 1\n" \
"buffer_store_dword v39, off, s[56:59], s71 offset:20 sc1\n" \
"s_mov_b64 exec, -1\n" \
"v_mfma_f32_16x16x32_bf16 v[184:187], a[216:219], a[164:167], v[184:187]\n" \
"v_mfma_f32_16x16x32_bf16 v[188:191], a[216:219], a[180:183], v[188:191]\n" \
"ds_read_b64_tr_b16 a[216:217], v34 offset:32\n" \
"ds_read_b64_tr_b16 a[218:219], v34 offset:4640\n" \
"v_mfma_f32_16x16x32_bf16 v[184:187], a[0:3], a[168:171], v[184:187]\n" \
"v_mfma_f32_16x16x32_bf16 v[188:191], a[0:3], a[184:187], v[188:191]\n" \
"ds_read_b64_tr_b16 a[0:1], v34 offset:9248\n" \
"ds_read_b64_tr_b16 a[2:3], v34 offset:13856\n" \
"v_mfma_f32_16x16x32_bf16 v[184:187], a[4:7], a[172:175], v[184:187]\n" \
"v_mfma_f32_16x16x32_bf16 v[188:191], a[4:7], a[188:191], v[188:191]\n" \
"ds_read_b64_tr_b16 a[4:5], v34 offset:18464\n" \
"ds_read_b64_tr_b16 a[6:7], v34 offset:23072\n" \
"v_mfma_f32_16x16x32_bf16 v[184:187], a[8:11], a[220:223], v[184:187]\n" \
"v_mfma_f32_16x16x32_bf16 v[188:191], a[8:11], a[236:239], v[188:191]\n" \
"ds_read_b64_tr_b16 a[8:9], v33 offset:32\n" \
"ds_read_b64_tr_b16 a[10:11], v33 offset:4640\n" \
"v_mfma_f32_16x16x32_bf16 v[184:187], a[12:15], a[224:227], v[184:187]\n" \
"v_mfma_f32_16x16x32_bf16 v[188:191], a[12:15], a[240:243], v[188:191]\n" \
"ds_read_b64_tr_b16 a[12:13], v10 offset:27712\n" \
"ds_read_b64_tr_b16 a[14:15], v10 offset:32320\n" \
"v_mfma_f32_16x16x32_bf16 v[184:187], a[16:19], a[228:231], v[184:187]\n" \
"v_mfma_f32_16x16x32_bf16 v[188:191], a[16:19], a[244:247], v[188:191]\n" \
"ds_read_b64_tr_b16 a[16:17], v10 offset:36928\n" \
"ds_read_b64_tr_b16 a[18:19], v10 offset:41536\n" \
"v_mfma_f32_16x16x32_bf16 v[184:187], a[20:23], a[232:235], v[184:187]\n" \
"v_mfma_f32_16x16x32_bf16 v[188:191], a[20:23], a[248:251], v[188:191]\n" \
"ds_read_b64_tr_b16 a[20:21], v10 offset:46144\n" \
"ds_read_b64_tr_b16 a[22:23], v10 offset:50752\n" \
"v_mfma_f32_16x16x32_bf16 v[192:195], a[24:27], a[96:99], v[192:195]\n" \
"v_mfma_f32_16x16x32_bf16 v[196:199], a[24:27], a[112:115], v[196:199]\n" \
"ds_read_b64_tr_b16 a[24:25], v10 offset:55360\n" \
"s_waitcnt lgkmcnt(13)\n" \
"v_mfma_f32_16x16x32_bf16 v[192:195], a[28:31], a[100:103], v[192:195]\n" \
"v_cvt_pk_bf16_f32 v184, v184, v185\n" \
"v_cvt_pk_bf16_f32 v185, v186, v187\n" \
"v_cvt_pk_bf16_f32 v186, v188, v189\n" \
"v_cvt_pk_bf16_f32 v187, v190, v191\n" \
"buffer_store_dwordx4 v[184:187], v20, s[56:59], s92 offen offset:1024 sc1\n" \
"s_add_u32 s92, s92, 0x3800\n" \
"v_mfma_f32_16x16x32_bf16 v[196:199], a[28:31], a[116:119], v[196:199]\n" \
"ds_read_b64_tr_b16 a[26:27], v10 offset:59968\n" \
"ds_read_b64_tr_b16 a[28:29], v33 offset:18496\n" \
"v_mfma_f32_16x16x32_bf16 v[192:195], a[192:195], a[104:107], v[192:195]\n" \
"ds_read_b64_tr_b16 a[30:31], v33 offset:23104\n" \
"v_mfma_f32_16x16x32_bf16 v[196:199], a[192:195], a[120:123], v[196:199]\n" \
"ds_read_b64_tr_b16 a[192:193], v33 offset:27712\n" \
"ds_read_b64_tr_b16 a[194:195], v33 offset:32320\n" \
"v_mfma_f32_16x16x32_bf16 v[192:195], a[196:199], a[108:111], v[192:195]\n" \
"v_mfma_f32_16x16x32_bf16 v[196:199], a[196:199], a[124:127], v[196:199]\n" \
"ds_read_b64_tr_b16 a[196:197], v33 offset:36928\n" \
"ds_read_b64_tr_b16 a[198:199], v33 offset:41536\n" \
"v_mfma_f32_16x16x32_bf16 v[192:195], a[200:203], a[160:163], v[192:195]\n" \
"v_mfma_f32_16x16x32_bf16 v[196:199], a[200:203], a[176:179], v[196:199]\n" \
"ds_read_b64_tr_b16 a[200:201], v33 offset:46144\n" \
"ds_read_b64_tr_b16 a[202:203], v33 offset:50752\n" \
"v_mfma_f32_16x16x32_bf16 v[192:195], a[204:207], a[164:167], v[192:195]\n" \
"v_mfma_f32_16x16x32_bf16 v[196:199], a[204:207], a[180:183], v[196:199]\n" \
"ds_read_b64_tr_b16 a[204:205], v34 offset:64\n" \
"ds_read_b64_tr_b16 a[206:207], v34 offset:4672\n" \
"v_mfma_f32_16x16x32_bf16 v[192:195], a[208:211], a[168:171], v[192:195]\n" \
"v_mfma_f32_16x16x32_bf16 v[196:199], a[208:211], a[184:187], v[196:199]\n" \
"ds_read_b64_tr_b16 a[208:209], v34 offset:9280\n" \
"ds_read_b64_tr_b16 a[210:211], v34 offset:13888\n" \
"v_mfma_f32_16x16x32_bf16 v[192:195], a[212:215], a[172:175], v[192:195]\n" \
"v_mfma_f32_16x16x32_bf16 v[196:199], a[212:215], a[188:191], v[196:199]\n" \
"ds_read_b64_tr_b16 a[212:213], v34 offset:18496\n" \
"ds_read_b64_tr_b16 a[214:215], v34 offset:23104\n" \
"v_mfma_f32_16x16x32_bf16 v[192:195], a[216:219], a[220:223], v[192:195]\n" \
"v_mfma_f32_16x16x32_bf16 v[196:199], a[216:219], a[236:239], v[196:199]\n" \
"ds_read_b64_tr_b16 a[216:217], v33 offset:64\n" \
"s_waitcnt lgkmcnt(14)\n" \
"v_mfma_f32_16x16x32_bf16 v[192:195], a[0:3], a[224:227], v[192:195]\n" \
"ds_read_b64_tr_b16 a[218:219], v33 offset:4672\n" \
"v_mfma_f32_16x16x32_bf16 v[196:199], a[0:3], a[240:243], v[196:199]\n" \
"ds_read_b64_tr_b16 a[0:1], v10 offset:27872\n" \
"ds_read_b64_tr_b16 a[2:3], v10 offset:32480\n" \
"v_mfma_f32_16x16x32_bf16 v[192:195], a[4:7], a[228:231], v[192:195]\n" \
"v_mfma_f32_16x16x32_bf16 v[196:199], a[4:7], a[244:247], v[196:199]\n" \
"ds_read_b64_tr_b16 a[4:5], v10 offset:37088\n" \
"ds_read_b64_tr_b16 a[6:7], v10 offset:41696\n" \
"v_mfma_f32_16x16x32_bf16 v[192:195], a[8:11], a[232:235], v[192:195]\n" \
"v_mfma_f32_16x16x32_bf16 v[196:199], a[8:11], a[248:251], v[196:199]\n" \
"ds_read_b64_tr_b16 a[8:9], v10 offset:46304\n" \
"ds_read_b64_tr_b16 a[10:11], v10 offset:50912\n" \
"v_mfma_f32_16x16x32_bf16 v[200:203], a[12:15], a[96:99], v[200:203]\n" \
"v_mfma_f32_16x16x32_bf16 v[204:207], a[12:15], a[112:115], v[204:207]\n" \
"ds_read_b64_tr_b16 a[12:13], v10 offset:55520\n" \
"ds_read_b64_tr_b16 a[14:15], v10 offset:60128\n" \
"v_mfma_f32_16x16x32_bf16 v[200:203], a[16:19], a[100:103], v[200:203]\n" \
"v_cvt_pk_bf16_f32 v192, v192, v193\n" \
"v_cvt_pk_bf16_f32 v193, v194, v195\n" \
"v_cvt_pk_bf16_f32 v194, v196, v197\n" \
"v_cvt_pk_bf16_f32 v195, v198, v199\n" \
"buffer_store_dwordx4 v[192:195], v20, s[56:59], s93 offen offset:1024 sc1\n" \
"s_waitcnt vmcnt(6)\n" \
"s_mov_b64 exec, 1\n" \
"buffer_store_dword v39, off, s[56:59], s72 offset:24 sc1\n" \
"s_mov_b64 exec, -1\n" \
"s_add_u32 s93, s93, 0x3000\n" \
"v_mfma_f32_16x16x32_bf16 v[204:207], a[16:19], a[116:119], v[204:207]\n" \
"ds_read_b64_tr_b16 a[16:17], v33 offset:18656\n" \
"ds_read_b64_tr_b16 a[18:19], v33 offset:23264\n" \
"v_mfma_f32_16x16x32_bf16 v[200:203], a[20:23], a[104:107], v[200:203]\n" \
"v_readlane_b32 s72, v11, 52\n" \
"v_mfma_f32_16x16x32_bf16 v[204:207], a[20:23], a[120:123], v[204:207]\n" \
"ds_read_b64_tr_b16 a[20:21], v33 offset:27872\n" \
"ds_read_b64_tr_b16 a[22:23], v33 offset:32480\n" \
"v_mfma_f32_16x16x32_bf16 v[200:203], a[24:27], a[108:111], v[200:203]\n" \
"v_mfma_f32_16x16x32_bf16 v[204:207], a[24:27], a[124:127], v[204:207]\n" \
"ds_read_b64_tr_b16 a[24:25], v33 offset:37088\n" \
"s_waitcnt lgkmcnt(11)\n" \
"v_mfma_f32_16x16x32_bf16 v[200:203], a[28:31], a[160:163], v[200:203]\n" \
"ds_read_b64_tr_b16 a[26:27], v33 offset:41696\n" \
"v_mfma_f32_16x16x32_bf16 v[204:207], a[28:31], a[176:179], v[204:207]\n" \
"ds_read_b64_tr_b16 a[28:29], v33 offset:46304\n" \
"ds_read_b64_tr_b16 a[30:31], v33 offset:50912\n" \
"v_mfma_f32_16x16x32_bf16 v[200:203], a[192:195], a[164:167], v[200:203]\n" \
"v_mfma_f32_16x16x32_bf16 v[204:207], a[192:195], a[180:183], v[204:207]\n" \
"ds_read_b64_tr_b16 a[192:193], v34 offset:224\n" \
"ds_read_b64_tr_b16 a[194:195], v34 offset:4832\n" \
"v_mfma_f32_16x16x32_bf16 v[200:203], a[196:199], a[168:171], v[200:203]\n" \
"v_mfma_f32_16x16x32_bf16 v[204:207], a[196:199], a[184:187], v[204:207]\n" \
"ds_read_b64_tr_b16 a[196:197], v34 offset:9440\n" \
"ds_read_b64_tr_b16 a[198:199], v34 offset:14048\n" \
"v_mfma_f32_16x16x32_bf16 v[200:203], a[200:203], a[172:175], v[200:203]\n" \
"v_mfma_f32_16x16x32_bf16 v[204:207], a[200:203], a[188:191], v[204:207]\n" \
"ds_read_b64_tr_b16 a[200:201], v34 offset:18656\n" \
"ds_read_b64_tr_b16 a[202:203], v34 offset:23264\n" \
"v_mfma_f32_16x16x32_bf16 v[200:203], a[204:207], a[220:223], v[200:203]\n" \
"v_mfma_f32_16x16x32_bf16 v[204:207], a[204:207], a[236:239], v[204:207]\n" \
"ds_read_b64_tr_b16 a[204:205], v33 offset:224\n" \
"ds_read_b64_tr_b16 a[206:207], v33 offset:4832\n" \
"v_mfma_f32_16x16x32_bf16 v[200:203], a[208:211], a[224:227], v[200:203]\n" \
"v_mfma_f32_16x16x32_bf16 v[204:207], a[208:211], a[240:243], v[204:207]\n" \
"ds_read_b64_tr_b16 a[208:209], v36 offset:0\n" \
"ds_read_b64_tr_b16 a[210:211], v36 offset:1024\n" \
"v_mfma_f32_16x16x32_bf16 v[200:203], a[212:215], a[228:231], v[200:203]\n" \
"v_mfma_f32_16x16x32_bf16 v[204:207], a[212:215], a[244:247], v[204:207]\n" \
"ds_read_b64_tr_b16 a[212:213], v36 offset:2048\n" \
"ds_read_b64_tr_b16 a[214:215], v36 offset:3072\n" \
"v_mfma_f32_16x16x32_bf16 v[200:203], a[216:219], a[232:235], v[200:203]\n" \
"v_mfma_f32_16x16x32_bf16 v[204:207], a[216:219], a[248:251], v[204:207]\n" \
"ds_read_b64_tr_b16 a[216:217], v36 offset:32\n" \
"ds_read_b64_tr_b16 a[218:219], v36 offset:1056\n" \
"v_mfma_f32_16x16x32_bf16 v[240:243], a[0:3], a[96:99], v[240:243]\n" \
"v_mfma_f32_16x16x32_bf16 v[244:247], a[0:3], a[112:115], v[244:247]\n" \
"ds_read_b64_tr_b16 a[0:1], v36 offset:2080\n" \
"s_waitcnt lgkmcnt(13)\n" \
"v_mfma_f32_16x16x32_bf16 v[240:243], a[4:7], a[100:103], v[240:243]\n" \
"v_cvt_pk_bf16_f32 v200, v200, v201\n" \
"v_cvt_pk_bf16_f32 v201, v202, v203\n" \
"v_cvt_pk_bf16_f32 v202, v204, v205\n" \
"v_cvt_pk_bf16_f32 v203, v206, v207\n" \
"buffer_store_dwordx4 v[200:203], v20, s[56:59], s94 offen offset:1024 sc1\n" \
"s_add_u32 s94, s94, 0x2800\n" \
"v_mfma_f32_16x16x32_bf16 v[244:247], a[4:7], a[116:119], v[244:247]\n" \
"ds_read_b64_tr_b16 a[2:3], v36 offset:3104\n" \
"v_mfma_f32_16x16x32_bf16 v[240:243], a[8:11], a[104:107], v[240:243]\n" \
"v_mfma_f32_16x16x32_bf16 v[244:247], a[8:11], a[120:123], v[244:247]\n" \
"v_mfma_f32_16x16x32_bf16 v[240:243], a[12:15], a[108:111], v[240:243]\n" \
"v_mfma_f32_16x16x32_bf16 v[244:247], a[12:15], a[124:127], v[244:247]\n" \
"v_mfma_f32_16x16x32_bf16 v[168:171], a[16:19], a[128:131], v[168:171]\n" \
"v_mfma_f32_16x16x32_bf16 v[172:175], a[16:19], a[144:147], v[172:175]\n" \
"v_mfma_f32_16x16x32_bf16 v[240:243], a[16:19], a[160:163], v[240:243]\n" \
"v_mfma_f32_16x16x32_bf16 v[244:247], a[16:19], a[176:179], v[244:247]\n" \
"v_mfma_f32_16x16x32_bf16 v[168:171], a[20:23], a[132:135], v[168:171]\n" \
"v_mfma_f32_16x16x32_bf16 v[172:175], a[20:23], a[148:151], v[172:175]\n" \
"v_mfma_f32_16x16x32_bf16 v[240:243], a[20:23], a[164:167], v[240:243]\n" \
"v_mfma_f32_16x16x32_bf16 v[244:247], a[20:23], a[180:183], v[244:247]\n" \
"v_mfma_f32_16x16x32_bf16 v[168:171], a[24:27], a[136:139], v[168:171]\n" \
"v_mfma_f32_16x16x32_bf16 v[172:175], a[24:27], a[152:155], v[172:175]\n" \
"v_mfma_f32_16x16x32_bf16 v[240:243], a[24:27], a[168:171], v[240:243]\n" \
"v_mfma_f32_16x16x32_bf16 v[244:247], a[24:27], a[184:187], v[244:247]\n" \
"v_mfma_f32_16x16x32_bf16 v[168:171], a[28:31], a[140:143], v[168:171]\n" \
"s_waitcnt vmcnt(5)\n" \
"s_mov_b64 exec, 1\n" \
"buffer_store_dword v39, off, s[56:59], s73 offset:28 sc1\n" \
"s_mov_b64 exec, -1\n" \
"v_mfma_f32_16x16x32_bf16 v[172:175], a[28:31], a[156:159], v[172:175]\n" \
"v_readlane_b32 s73, v11, 43\n" \
"v_mfma_f32_16x16x32_bf16 v[240:243], a[28:31], a[172:175], v[240:243]\n" \
"v_mfma_f32_16x16x32_bf16 v[244:247], a[28:31], a[188:191], v[244:247]\n" \
"v_mfma_f32_16x16x32_bf16 v[168:171], a[192:195], a[32:35], v[168:171]\n" \
"v_mfma_f32_16x16x32_bf16 v[172:175], a[192:195], a[48:51], v[172:175]\n" \
"v_mfma_f32_16x16x32_bf16 v[240:243], a[192:195], a[220:223], v[240:243]\n" \
"v_mfma_f32_16x16x32_bf16 v[244:247], a[192:195], a[236:239], v[244:247]\n" \
"s_waitcnt lgkmcnt(0)\n" \
"v_mfma_f32_16x16x32_bf16 v[168:171], a[196:199], a[36:39], v[168:171]\n" \
"v_mfma_f32_16x16x32_bf16 v[172:175], a[196:199], a[52:55], v[172:175]\n" \
"v_mfma_f32_16x16x32_bf16 v[240:243], a[196:199], a[224:227], v[240:243]\n" \
"v_mfma_f32_16x16x32_bf16 v[244:247], a[196:199], a[240:243], v[244:247]\n" \
"v_mfma_f32_16x16x32_bf16 v[168:171], a[200:203], a[40:43], v[168:171]\n" \
"v_mfma_f32_16x16x32_bf16 v[172:175], a[200:203], a[56:59], v[172:175]\n" \
"v_mfma_f32_16x16x32_bf16 v[240:243], a[200:203], a[228:231], v[240:243]\n" \
"v_mfma_f32_16x16x32_bf16 v[244:247], a[200:203], a[244:247], v[244:247]\n" \
"v_mfma_f32_16x16x32_bf16 v[168:171], a[204:207], a[44:47], v[168:171]\n" \
"v_mfma_f32_16x16x32_bf16 v[172:175], a[204:207], a[60:63], v[172:175]\n" \
"v_mfma_f32_16x16x32_bf16 v[240:243], a[204:207], a[232:235], v[240:243]\n" \
"v_mfma_f32_16x16x32_bf16 v[244:247], a[204:207], a[248:251], v[244:247]\n" \
"v_mfma_f32_16x16x32_bf16 v[168:171], a[208:211], v[64:67], v[168:171]\n" \
"v_mfma_f32_16x16x32_bf16 v[172:175], a[208:211], v[72:75], v[172:175]\n" \
"v_mfma_f32_16x16x32_bf16 v[240:243], a[208:211], v[80:83], v[240:243]\n" \
"v_mfma_f32_16x16x32_bf16 v[244:247], a[208:211], v[88:91], v[244:247]\n" \
"v_mfma_f32_16x16x32_bf16 v[168:171], a[212:215], v[68:71], v[168:171]\n" \
"s_waitcnt vmcnt(4)\n" \
"s_mov_b64 exec, 1\n" \
"buffer_store_dword v39, off, s[56:59], s92 offset:32 sc1\n" \
"s_mov_b64 exec, -1\n" \
"v_mfma_f32_16x16x32_bf16 v[172:175], a[212:215], v[76:79], v[172:175]\n" \
"v_readlane_b32 s92, v11, 41\n" \
"v_mfma_f32_16x16x32_bf16 v[240:243], a[212:215], v[84:87], v[240:243]\n" \
"v_mfma_f32_16x16x32_bf16 v[244:247], a[212:215], v[92:95], v[244:247]\n" \
"v_mfma_f32_16x16x32_bf16 v[56:59], a[216:219], v[96:99], 0\n" \
"v_mfma_f32_16x16x32_bf16 v[60:63], a[216:219], v[104:107], 0\n" \
"v_mfma_f32_16x16x32_bf16 v[56:59], a[0:3], v[100:103], v[56:59]\n" \
"v_mfma_f32_16x16x32_bf16 v[60:63], a[0:3], v[108:111], v[60:63]\n" \
"s_waitcnt vmcnt(4)\n" \
"s_mov_b64 exec, 1\n" \
"buffer_store_dword v39, off, s[56:59], s93 offset:36 sc1\n" \
"s_mov_b64 exec, -1\n" \
"v_readlane_b32 s93, v11, 42\n" \
"s_waitcnt vmcnt(3)\n" \
"s_mov_b64 exec, 1\n" \
"buffer_store_dword v39, off, s[56:59], s94 offset:40 sc1\n" \
"s_mov_b64 exec, -1\n" \
"buffer_load_dword v37, v40, s[56:59], s74 offen sc1\n" \
"s_waitcnt vmcnt(0)\n" \
"v_cmp_eq_u32_e64 s[98:99], s62, v37\n" \
"s_nop 0\n" \
"s_andn2_b32 s75, 0x1, s98\n" \
"s_cbranch_scc1 L_sx0_%=\n" \
"L_rx0_%=:\n" \
"s_add_u32 s95, s63, 0x0\n" \
"buffer_load_dwordx4 v[116:119], v20, s[56:59], s95 offen offset:0 sc1\n" \
"s_andn2_b32 s75, 0x2, s98\n" \
"s_cbranch_scc1 L_sx1_%=\n" \
"L_rx1_%=:\n" \
"s_add_u32 s96, s63, 0x800\n" \
"buffer_load_dwordx4 v[124:127], v20, s[56:59], s96 offen offset:0 sc1\n" \
"s_andn2_b32 s75, 0x4, s98\n" \
"s_cbranch_scc1 L_sx2_%=\n" \
"L_rx2_%=:\n" \
"s_add_u32 s97, s63, 0x1000\n" \
"buffer_load_dwordx4 v[132:135], v20, s[56:59], s97 offen offset:0 sc1\n" \
"s_andn2_b32 s75, s100, s98\n" \
"s_cbranch_scc1 L_sx3_%=\n" \
"L_rx3_%=:\n" \
"buffer_load_dwordx4 v[136:139], v20, s[56:59], s64 offen offset:0 sc1\n" \
"buffer_load_dwordx4 v[208:211], v20, s[56:59], s64 offen offset:2048 sc1\n" \
"s_add_u32 s64, s64, 0x1000\n" \
"buffer_load_dwordx4 v[144:147], v20, s[56:59], s64 offen offset:0 sc1\n" \
"buffer_load_dwordx4 v[216:219], v20, s[56:59], s64 offen offset:2048 sc1\n" \
"s_add_u32 s64, s64, 0x1000\n" \
"buffer_load_dwordx4 v[152:155], v20, s[56:59], s64 offen offset:0 sc1\n" \
"buffer_load_dwordx4 v[224:227], v20, s[56:59], s64 offen offset:2048 sc1\n" \
"s_add_u32 s64, s64, 0x1000\n" \
"buffer_load_dwordx4 v[176:179], v20, s[56:59], s64 offen offset:0 sc1\n" \
"buffer_load_dwordx4 v[248:251], v20, s[56:59], s64 offen offset:2048 sc1\n" \
"s_andn2_b32 s75, 0x10, s98\n" \
"s_cbranch_scc1 L_sx4_%=\n" \
"L_rx4_%=:\n" \
"s_add_u32 s65, s63, 0x1800\n" \
"buffer_load_dwordx4 v[140:143], v20, s[56:59], s65 offen offset:0 sc1\n" \
"buffer_load_dwordx4 v[212:215], v20, s[56:59], s65 offen offset:1024 sc1\n" \
"s_andn2_b32 s75, 0x20, s98\n" \
"s_cbranch_scc1 L_sx5_%=\n" \
"L_rx5_%=:\n" \
"s_add_u32 s66, s63, 0x2000\n" \
"buffer_load_dwordx4 v[148:151], v20, s[56:59], s66 offen offset:0 sc1\n" \
"buffer_load_dwordx4 v[220:223], v20, s[56:59], s66 offen offset:1024 sc1\n" \
"s_andn2_b32 s75, 0x40, s98\n" \
"s_cbranch_scc1 L_sx6_%=\n" \
"L_rx6_%=:\n" \
"s_add_u32 s67, s63, 0x2800\n" \
"buffer_load_dwordx4 v[156:159], v20, s[56:59], s67 offen offset:0 sc1\n" \
"buffer_load_dwordx4 v[228:231], v20, s[56:59], s67 offen offset:1024 sc1\n" \
"s_andn2_b32 s75, 0x80, s98\n" \
"s_cbranch_scc1 L_sx7_%=\n" \
"L_rx7_%=:\n" \
"s_add_u32 s68, s63, 0x3000\n" \
"buffer_load_dwordx4 v[164:167], v20, s[56:59], s68 offen offset:0 sc1\n" \
"buffer_load_dwordx4 v[236:239], v20, s[56:59], s68 offen offset:1024 sc1\n" \
"s_andn2_b32 s75, 0x100, s98\n" \
"s_cbranch_scc1 L_sx8_%=\n" \
"L_rx8_%=:\n" \
"s_add_u32 s69, s63, 0x0\n" \
"buffer_load_dwordx4 v[188:191], v20, s[56:59], s69 offen offset:1024 sc1\n" \
"s_andn2_b32 s75, 0x200, s98\n" \
"s_cbranch_scc1 L_sx9_%=\n" \
"L_rx9_%=:\n" \
"s_add_u32 s70, s63, 0x800\n" \
"buffer_load_dwordx4 v[196:199], v20, s[56:59], s70 offen offset:1024 sc1\n" \
"s_andn2_b32 s75, 0x400, s98\n" \
"s_cbranch_scc1 L_sx10_%=\n" \
"L_rx10_%=:\n" \
"s_add_u32 s71, s63, 0x1000\n" \
"buffer_load_dwordx4 v[204:207], v20, s[56:59], s71 offen offset:1024 sc1\n" \
"s_waitcnt vmcnt(21)\n" \
"v_mfma_f32_16x16x32_bf16 v[168:171], v[50:53], v[116:119], v[168:171]\n" \
"v_mfma_f32_16x16x32_bf16 v[172:175], v[48:51], v[116:119], v[172:175]\n" \
"s_waitcnt vmcnt(20)\n" \
"v_mfma_f32_16x16x32_bf16 v[168:171], v[50:53], v[124:127], v[168:171]\n" \
"v_mfma_f32_16x16x32_bf16 v[172:175], v[48:51], v[124:127], v[172:175]\n" \
"s_waitcnt vmcnt(19)\n" \
"v_mfma_f32_16x16x32_bf16 v[168:171], v[50:53], v[132:135], v[168:171]\n" \
"v_mfma_f32_16x16x32_bf16 v[172:175], v[48:51], v[132:135], v[172:175]\n" \
"s_waitcnt vmcnt(18)\n" \
"v_mfma_f32_16x16x32_bf16 v[56:59], v[50:53], v[136:139], v[56:59]\n" \
"v_mfma_f32_16x16x32_bf16 v[60:63], v[48:51], v[136:139], v[60:63]\n" \
"s_waitcnt vmcnt(17)\n" \
"v_mfma_f32_16x16x32_bf16 v[56:59], v[50:53], v[208:211], v[56:59]\n" \
"v_mfma_f32_16x16x32_bf16 v[60:63], v[48:51], v[208:211], v[60:63]\n" \
"s_waitcnt vmcnt(16)\n" \
"v_mfma_f32_16x16x32_bf16 v[56:59], v[50:53], v[144:147], v[56:59]\n" \
"v_mfma_f32_16x16x32_bf16 v[60:63], v[48:51], v[144:147], v[60:63]\n" \
"s_waitcnt vmcnt(15)\n" \
"v_mfma_f32_16x16x32_bf16 v[56:59], v[50:53], v[216:219], v[56:59]\n" \
"v_mfma_f32_16x16x32_bf16 v[60:63], v[48:51], v[216:219], v[60:63]\n" \
"s_waitcnt vmcnt(14)\n" \
"v_mfma_f32_16x16x32_bf16 v[56:59], v[50:53], v[152:155], v[56:59]\n" \
"v_mfma_f32_16x16x32_bf16 v[60:63], v[48:51], v[152:155], v[60:63]\n" \
"s_waitcnt vmcnt(13)\n" \
"v_mfma_f32_16x16x32_bf16 v[56:59], v[50:53], v[224:227], v[56:59]\n" \
"v_mfma_f32_16x16x32_bf16 v[60:63], v[48:51], v[224:227], v[60:63]\n" \
"s_waitcnt vmcnt(12)\n" \
"v_mfma_f32_16x16x32_bf16 v[56:59], v[50:53], v[176:179], v[56:59]\n" \
"v_mfma_f32_16x16x32_bf16 v[60:63], v[48:51], v[176:179], v[60:63]\n" \
"s_waitcnt vmcnt(11)\n" \
"v_mfma_f32_16x16x32_bf16 v[56:59], v[50:53], v[248:251], v[56:59]\n" \
"v_mfma_f32_16x16x32_bf16 v[60:63], v[48:51], v[248:251], v[60:63]\n" \
"s_waitcnt vmcnt(10)\n" \
"v_mfma_f32_16x16x32_bf16 v[168:171], v[50:53], v[140:143], v[168:171]\n" \
"v_mfma_f32_16x16x32_bf16 v[172:175], v[48:51], v[140:143], v[172:175]\n" \
"s_waitcnt vmcnt(9)\n" \
"v_mfma_f32_16x16x32_bf16 v[240:243], v[50:53], v[212:215], v[240:243]\n" \
"v_mfma_f32_16x16x32_bf16 v[244:247], v[48:51], v[212:215], v[244:247]\n" \
"s_waitcnt vmcnt(8)\n" \
"v_mfma_f32_16x16x32_bf16 v[168:171], v[50:53], v[148:151], v[168:171]\n" \
"v_cvt_pk_bf16_f32 v56, v56, v57\n" \
"v_cvt_pk_bf16_f32 v60, v60, v61\n" \
"v_mfma_f32_16x16x32_bf16 v[172:175], v[48:51], v[148:151], v[172:175]\n" \
"v_cvt_pk_bf16_f32 v57, v58, v59\n" \
"s_waitcnt vmcnt(7)\n" \
"v_mfma_f32_16x16x32_bf16 v[240:243], v[50:53], v[220:223], v[240:243]\n" \
"buffer_store_dwordx2 v[56:57], v42, s[88:91], s73 offen\n" \
"v_mfma_f32_16x16x32_bf16 v[244:247], v[48:51], v[220:223], v[244:247]\n" \
"v_cvt_pk_bf16_f32 v61, v62, v63\n" \
"s_waitcnt vmcnt(7)\n" \
"v_mfma_f32_16x16x32_bf16 v[168:171], v[50:53], v[156:159], v[168:171]\n" \
"buffer_store_dwordx2 v[60:61], v43, s[88:91], s73 offen\n" \
"v_mfma_f32_16x16x32_bf16 v[172:175], v[48:51], v[156:159], v[172:175]\n" \
"s_waitcnt vmcnt(7)\n" \
"v_mfma_f32_16x16x32_bf16 v[240:243], v[50:53], v[228:231], v[240:243]\n" \
"v_mfma_f32_16x16x32_bf16 v[244:247], v[48:51], v[228:231], v[244:247]\n" \
"s_waitcnt vmcnt(6)\n" \
"v_mfma_f32_16x16x32_bf16 v[168:171], v[50:53], v[164:167], v[168:171]\n" \
"v_mfma_f32_16x16x32_bf16 v[172:175], v[48:51], v[164:167], v[172:175]\n" \
"s_waitcnt vmcnt(5)\n" \
"v_mfma_f32_16x16x32_bf16 v[240:243], v[50:53], v[236:239], v[240:243]\n" \
"v_mfma_f32_16x16x32_bf16 v[244:247], v[48:51], v[236:239], v[244:247]\n" \
"s_waitcnt vmcnt(4)\n" \
"v_mfma_f32_16x16x32_bf16 v[240:243], v[50:53], v[188:191], v[240:243]\n" \
"v_mfma_f32_16x16x32_bf16 v[244:247], v[48:51], v[188:191], v[244:247]\n" \
"s_waitcnt vmcnt(3)\n" \
"v_mfma_f32_16x16x32_bf16 v[240:243], v[50:53], v[196:199], v[240:243]\n" \
"v_cvt_pk_bf16_f32 v168, v168, v169\n" \
"v_cvt_pk_bf16_f32 v172, v172, v173\n" \
"v_mfma_f32_16x16x32_bf16 v[244:247], v[48:51], v[196:199], v[244:247]\n" \
"v_cvt_pk_bf16_f32 v169, v170, v171\n" \
"s_waitcnt vmcnt(2)\n" \
"v_mfma_f32_16x16x32_bf16 v[240:243], v[50:53], v[204:207], v[240:243]\n" \
"v_cvt_pk_bf16_f32 v173, v174, v175\n" \
"v_mfma_f32_16x16x32_bf16 v[244:247], v[48:51], v[204:207], v[244:247]\n" \
"s_mov_b64 exec, 1\n" \
"buffer_store_dword v46, off, s[56:59], s72 offset:0 sc1\n" \
"s_mov_b64 exec, -1\n" \
"buffer_store_dwordx2 v[168:169], v42, s[88:91], s92 offen\n" \
"buffer_store_dwordx2 v[172:173], v43, s[88:91], s92 offen\n" \
"s_nop 0\n" \
"v_cvt_pk_bf16_f32 v240, v240, v241\n" \
"v_cvt_pk_bf16_f32 v241, v242, v243\n" \
"v_cvt_pk_bf16_f32 v244, v244, v245\n" \
"v_cvt_pk_bf16_f32 v245, v246, v247\n" \
"buffer_store_dwordx2 v[240:241], v42, s[88:91], s93 offen\n" \
"buffer_store_dwordx2 v[244:245], v43, s[88:91], s93 offen\n"
#define ATT_VCLOB "v21","v22","v23","v24","v25","v32","v33","v34","v35","v36","v37","v38","v39","v40","v41","v42","v43","v44","v45","v46","v47","v48","v49","v50","v51","v52","v53","v54","v55","v56","v57","v58","v59","v60","v61","v62","v63","v64","v65","v66","v67","v68","v69","v70","v71","v72","v73","v74","v75","v76","v77","v78","v79","v80","v81","v82","v83","v84","v85","v86","v87","v88","v89","v90","v91","v92","v93","v94","v95","v96","v97","v98","v99","v100","v101","v102","v103","v104","v105","v106","v107","v108","v109","v110","v111","v112","v113","v114","v115","v116","v117","v118","v119","v120","v121","v122","v123","v124","v125","v126","v127","v128","v129","v130","v131","v132","v133","v134","v135","v136","v137","v138","v139","v140","v141","v142","v143","v144","v145","v146","v147","v148","v149","v150","v151","v152","v153","v154","v155","v156","v157","v158","v159","v160","v161","v162","v163","v164","v165","v166","v167","v168","v169","v170","v171","v172","v173","v174","v175","v176","v177","v178","v179","v180","v181","v182","v183","v184","v185","v186","v187","v188","v189","v190","v191","v192","v193","v194","v195","v196","v197","v198","v199","v200","v201","v202","v203","v204","v205","v206","v207","v208","v209","v210","v211","v212","v213","v214","v215","v216","v217","v218","v219","v220","v221","v222","v223","v224","v225","v226","v227","v228","v229","v230","v231","v232","v233","v234","v235","v236","v237","v238","v239","v240","v241","v242","v243","v244","v245","v246","v247","v248","v249","v250","v251","v252","v253","v254","v255"
#define ATT_SCLOB "s63","s64","s65","s66","s67","s68","s69","s70","s71","s72","s73","s74","s75","s76","s77","s78","s79","s80","s81","s82","s83","s84","s85","s86","s87","s88","s89","s90","s91","s92","s93","s94","s95","s96","s97","s98","s99","s100","s101"
#define ATT_WTCH 0
enum { T_fo, T_fj, T_fa, T_fas, T_fb, T_fbs, T_f8, T_mk8, T_wt, T_wol, T_woh, T_wbl, T_wbh, T_outl, T_outh, T_r1l, T_r1h, T_r1n, T_r1s, T_m0, T_m1, T_mw, T_m4, T_ml, T_w0, T_w1, T_w2, T_w3, T_A1, T_A2, T_A3, T_B1, T_B2, T_B3, T_Ao, T_Bo, T_Al, T_Bl, T_l8, T_xr, T_x8r, T_oA, T_oB, T_o8, T_x0, T_x1, T_x2, T_x3, T_x4, T_x5, T_x6, T_x8, T_ep };

__device__ __forceinline__ void out_consts(u32x4 (&vp)[3], const u16* wo, const u16* wob, const u16* out, const unsigned b, const unsigned w, const unsigned lane) {
    const unsigned sp = b & 7u, nt = (b >> 3) & 3u, j = b >> 5, head = 4u * sp + nt, OOR = 0x40000000u;
    unsigned vw[9];
#pragma unroll
    for (unsigned i = 0; i < 9u; ++i) {
        const unsigned q = 64u * i + lane, row = __umul24(q, 3641u) >> 16, c = q - __umul24(row, 18u);
        vw[i] = __umul24(row, DM * 2) + (c < 16u ? (c * 16u + sp * 32u + 32u) & 255u : c * 16u) - (i & 3u) * 1024u;
    }
    const unsigned row = 16u * w + (lane >> 2), r9 = __umul24(16u * (row >> 5) + (row & 12u) + 2u * ((row >> 4) & 1u) + ((row >> 1) & 1u), 18u) + 16u + (row & 1u);
    unsigned vwl = __umul24(r9, DM * 2) + ((lane & 2u) ? 256u : sp * 32u) + (lane & 1u) * 16u, vrd = __umul24(lane >> 2, 288u) + (lane & 3u) * 8u;
    unsigned tab = 0;
#define TB(n, v) asm("v_writelane_b32 %0, %1, %2" : "+v"(tab) : "s"((unsigned)(v)), "n"((int)T_##n))
    const unsigned FJ = (unsigned)WS_F + (F_J + 640u * j) * 4u, mt = 2u * (w & 2u) + (w & 1u), w2 = w < 2u ? w : 2u, hw = head * 147456u;
    const unsigned FR = (unsigned)WS_F + F_R * 4u + (8u * j + sp) * 1024u;
    TB(fo, FJ + 4u * head); TB(fj, FJ); TB(fa, FR + (w | 2u) * 256u); TB(fas, FR + w * 256u + nt * 4u);
    TB(fb, FR + 128u + w * 256u); TB(fbs, FR + 128u + w * 256u + nt * 4u);
    TB(ep, ssel(b + w, OOR, (unsigned)WS_F + F_EPOCH * 4u));
    const u16* pwo = wo + nt * 144u; const u16* pwb = wob + nt * 144u;
    TB(wol, PLO(pwo)); TB(woh, PHI(pwo) & 0xffffu); TB(wbl, PLO(pwb)); TB(wbh, PHI(pwb) & 0xffffu); TB(outl, PLO(out)); TB(outh, PHI(out) & 0xffffu); TB(r1l, sslt<3>(w, PLO(pwo), PLO(pwb))); TB(r1h, sslt<3>(w, PHI(pwo), PHI(pwb)) & 0xffffu);
    TB(r1n, sslt<3>(w, 72u * sp + 71u, 4095u) * 1152u + 288u); TB(r1s, sslt<3>(w, (72u * sp + 32u * w) * 1152u, hw + 110592u));
    TB(m0, 131072u + 9216u * w2); TB(w0, hw + 36864u * w2); TB(m1, sslt<3>(w, 9216u * w, 73728u)); TB(mw, 9216u * w); TB(m4, sslt<3>(w, 131072u + 9216u * w, 64512u)); TB(ml, 119808u + 1024u * w);
    const unsigned tA = (unsigned)WS_AF + (8u * j + mt) * (KS_TOT * 1024u), tB = (unsigned)WS_AF + (120u - 8u * j + mt) * (KS_TOT * 1024u);
#define HK(k) (4u * sp + ((nt + k) & 3u))
    TB(w1, HK(1u) * 147456u + w * 36864u); TB(w2, HK(2u) * 147456u + w * 36864u); TB(w3, HK(3u) * 147456u + w * 36864u);
    TB(A1, tA + 18432u + HK(1u) * 4096u); TB(A2, tA + 18432u + HK(2u) * 4096u); TB(A3, tA + 18432u + HK(3u) * 4096u);
    TB(B1, tB + 18432u + HK(1u) * 4096u); TB(B2, tB + 18432u + HK(2u) * 4096u); TB(B3, tB + 18432u + HK(3u) * 4096u);
    TB(Ao, tA + sp * 2048u); TB(Bo, tB + sp * 2048u); TB(Al, tA + 16384u); TB(Bl, tB + 16384u);
    const unsigned nred = w ^ (sp >> 1), blk = sp & 1u;
    const unsigned xg = (unsigned)WS_X + (4u * j + nt) * 589824u + w * 16384u, oA = (128u * j + 16u * mt) * 1152u + nt * 288u, oB = (1920u - 128u * j + 16u * mt) * 1152u + nt * 288u;
    TB(l8, ssel(nred, OOR, ssel(blk, tB, tA) + 16384u)); TB(xr, xg + sp * 65536u); TB(x8r, ssel(nred, OOR, xg + 524288u + (sp & 1u) * 1024u));
    TB(oA, oA + sp * 32u); TB(oB, oB + sp * 32u); TB(o8, ssel(nred, OOR, ssel(blk, oB, oA) + 256u)); TB(x8, xg + 524288u + sp * 2048u);
    TB(f8, xg + w * 131072u + 14400u + sp * 4u); TB(mk8, ssel(nred, 0u, 0xff0000u)); TB(wt, hw + j * 18432u + w * 4608u);
#define XI(i) TB(x##i, xg + ((sp + 1u + i) & 7u) * 65536u + i * 2048u)
    XI(0); XI(1); XI(2); XI(3); XI(4); XI(5); XI(6);
#undef XI
#undef HK
#undef TB
    vp[0] = (u32x4){vw[0], vw[1], vw[2], vw[3]}; vp[1] = (u32x4){vw[4], vw[5], vw[6], vw[7]}; vp[2] = (u32x4){vw[8], vwl, vrd, tab};
}
__global__ __launch_bounds__(256, 1) void fused_kernel(const u16* __restrict__ x, const u16* __restrict__ ckv, const u16* __restrict__ wq, const u16* __restrict__ wuk,
                                                       const u16* __restrict__ wuv, const u16* __restrict__ wqkv, const u16* __restrict__ gw,
                                                       const u16* __restrict__ wo, const u16* __restrict__ wob, char* __restrict__ ws_, u16* __restrict__ out) {
    using namespace att;
    extern __shared__ __attribute__((aligned(16))) char lds[];
    u32x4 sa[5]; u32x2 s24, s28; char* ws; unsigned tdum; kaptr ka = (kaptr)__builtin_amdgcn_kernarg_segment_ptr();
    asm volatile("s_load_dwordx8 s[8:15], %[ka], 0x0\n\ts_load_dwordx8 s[16:23], %[ka], 0x20\n\ts_getpc_b64 s[4:5]\n\tv_lshlrev_b32_e32 v191, 7, %[t]\n\ts_bfe_u32 s6, %[b], 0x20003\n\t"
                 "v_lshl_add_u32 v191, s6, 15, v191\n\tv_min_u32_e32 v191, %[e], v191\n\tglobal_load_dword v191, v191, s[4:5]\n\ts_load_dwordx4 s[24:27], %[ka], 0x40\n\ts_load_dwordx2 s[28:29], %[ka], 0x50"
                 : "={s[8:11]}"(sa[0]), "={s[12:15]}"(sa[1]), "={s[16:19]}"(sa[2]), "={s[20:23]}"(sa[3]), "={s[24:25]}"(s24), "={s[26:27]}"(ws), "={s[28:29]}"(s28), "={v191}"(tdum), [ka] "+{s[0:1]}"(ka)
                 : [t] "v"(threadIdx.x), [b] "s"(blockIdx.x), [e] "i"(CT_END) : "s4", "s5", "s6", "scc");
    SBAR();
    const unsigned wid = rfl(threadIdx.x >> 6);
    unsigned tag, vl;
    const int b = blockIdx.x;
    u32x4 vp[5], srdA; u32x2 sd;
    proj_phase(ka, ws, wid, vp, vl, sa, s24, s28, tdum, srdA, sd, tag
    );
    unsigned* const flags = (unsigned*)(ws + WS_F);
    RTS(2);
#define AST(L) do {} while (0)
#define ATT_TAIL "s_endpgm\n"
    asm volatile(ATT_ASM ATT_TAIL "L_end_%=:\n s_nop 1"
                 :
                 : "{v[0:3]}"(vp[0]), "{v[4:7]}"(vp[1]), "{v[8:11]}"(vp[2]), "{v[12:15]}"(vp[3]), "{v[16:19]}"(vp[4]), "{v20}"(vl), "{s[56:59]}"(srdA), "{s[60:61]}"(sd), "{s62}"(tag),
                   "{s[8:11]}"(sa[0]), "{s[12:15]}"(sa[1]), "{s[16:19]}"(sa[2]), "{s[20:23]}"(sa[3]), "{s24}"(sa[4][0])
                 : "memory", "m0", "vcc", "scc", ATT_VCLOB, ATT_SCLOB, ACLOB_LO, ACLOB_HI);
    {
        unsigned b2 = b, w2 = wid;
        asm volatile("s_waitcnt vmcnt(0)" : "+s"(ka), "+s"(b2), "+s"(w2), "+s"(ws) :: "memory");
        const unsigned j2 = b2 >> 5, p2 = w2 & 1u, h2 = (b2 & 7u) * 4u + ((b2 >> 3) & 3u);
#pragma unroll 1
        for (unsigned i = w2 & 2u; i < 2u + 3u * (w2 & 2u); ++i)
            att_exact(ka[0], ka[2], ws, h2, (i < 4u ? 1920u - 128u * j2 : 128u * j2 - 128u) + 32u * i + 16u * p2, *reinterpret_cast<const float*>(lds + LDS_G + (32u * i + 16u * p2 + (lane_id() & 15u)) * 4u));
        asm volatile("s_waitcnt vmcnt(0)" ::: "memory");
        unsigned* fa = (unsigned*)(ws + WS_F) + F_R + (8u * j2 + (b2 & 7u)) * 256u + (2u + p2) * 64u + ((b2 >> 3) & 3u);
        if (w2 & 2u) st_flag(fa, tag);
        wait_flags(fa, ld_flag(fa), tag);
        const unsigned l2 = lane_id(); u32x4 c[3]; out_consts(c, ka[7], ka[8], ka[10], b2, w2, l2);
        asm volatile("s_waitcnt vmcnt(0) lgkmcnt(0)\n\ts_nop 7\n\ts_branch L_att_stub" :: "{v[0:3]}"(c[0]), "{v[4:7]}"(c[1]), "{v[8:11]}"(c[2]), "{v20}"(l2 * 16u), "{s[56:59]}"(SRDA), "{s60}"(SD(8u * j2, h2, w2)),
                     "{s61}"(SD(120u - 8u * j2, h2, w2)), "{s62}"(tag) : "memory");
    }
    RTS(5);
}

extern "C" void kernel_launch(void* const* d_in, const int* in_sizes, int n_in, void* d_out, int out_size, void* d_ws, size_t ws_size,
                              hipStream_t stream) {
    (void)hipFuncSetAttribute((const void*)fused_kernel, hipFuncAttributeMaxDynamicSharedMemorySize, att::LDS_BYTES);
    fused_kernel<<<dim3(256), 256, att::LDS_BYTES, stream>>>((const u16*)d_in[0], (const u16*)d_in[4], (const u16*)d_in[5], (const u16*)d_in[6], (const u16*)d_in[7], (const u16*)d_in[1],
                                                             (const u16*)d_in[9], (const u16*)d_in[2], (const u16*)d_in[8], (char*)d_ws, (u16*)d_out);
}
```

```cpp
#include <hip/hip_runtime.h>
#include <hip/hip_bf16.h>
#include <stdint.h>
#include <utility>

typedef short bf16x8 __attribute__((ext_vector_type(8)));
typedef short s16x4 __attribute__((ext_vector_type(4)));
typedef float f32x16 __attribute__((ext_vector_type(16)));
typedef float f32x4 __attribute__((ext_vector_type(4)));
typedef unsigned u32x4 __attribute__((ext_vector_type(4)));
typedef unsigned u32x2 __attribute__((ext_vector_type(2)));
typedef unsigned short u16;

constexpr int S = 2048, DM = 576, NH = 32, HD = 128, DC = 512, NQKV = 1728, DHA = 18;
constexpr int NBIG = NH * HD;
constexpr int KS_TOT = 146;

#define SBAR() __builtin_amdgcn_sched_barrier(0)
constexpr size_t WS_Q = 0, WS_K = WS_Q + (size_t)S * NBIG * 2, WS_V = WS_K + (size_t)S * NBIG * 2, WS_AF = WS_V + (size_t)S * NBIG * 2, WS_X = WS_AF + (size_t)128 * KS_TOT * 1024,
                 WS_P = WS_X + (size_t)64 * 16 * 9216, WS_F = WS_P + (size_t)128 * (4 * 32 * 256 + 1024) * 4;
constexpr size_t WS_L = (size_t)96 << 20;
static_assert(WS_F == 96206848, "workspace map is baked into the generated asm");
constexpr int F_EPOCH = 0, F_J = 1024, F_ST = 8192, F_KV = 40960, F_R = 65536;
constexpr unsigned TAG_STEP = 0x9E3779B1u;
__device__ __forceinline__ unsigned ld_flag(const unsigned* p) { return __hip_atomic_load(p, __ATOMIC_RELAXED, __HIP_MEMORY_SCOPE_AGENT); }
__device__ __forceinline__ void st_flag(unsigned* p, unsigned v) { __hip_atomic_store(p, v, __ATOMIC_RELAXED, __HIP_MEMORY_SCOPE_AGENT); }
__device__ __forceinline__ unsigned ld_flag_async(const unsigned* p) { unsigned v; asm volatile("global_load_dword %0, %1, off sc1" : "=v"(v) : "v"(p) : "memory"); return v; }
#define WAITV(v, N) asm volatile("s_waitcnt vmcnt(" #N ")" : "+v"(v) :: "memory")
__device__ __forceinline__ void wait_flags(const unsigned* p, unsigned v, unsigned tag) {
    if (__builtin_expect(__builtin_amdgcn_ballot_w64(v != tag) != 0ull, 0)) {
        unsigned spins = 0;
        do { __builtin_amdgcn_s_sleep(1); } while (__builtin_amdgcn_ballot_w64(ld_flag(p) != tag) != 0ull && ++spins < (1u << 22));
    }
    asm volatile("" ::: "memory");
}

__device__ __forceinline__ unsigned cvtpk(float lo, float hi) {
    unsigned r; asm volatile("v_cvt_pk_bf16_f32 %0, %1, %2" : "=v"(r) : "v"(lo), "v"(hi)); return r;
}
__device__ __forceinline__ float bf2f(u16 v) { return __uint_as_float(((unsigned)v) << 16); }
__device__ __forceinline__ int crow(int r, int hi) { return (r & 3) + 8 * (r >> 2) + 4 * hi; }

typedef __attribute__((address_space(3))) s16x4* lds_s16x4_p;
__device__ __forceinline__ s16x4 trrd(unsigned addr) { return __builtin_amdgcn_ds_read_tr16_b64_v4i16((lds_s16x4_p)addr); }

__device__ __forceinline__ unsigned lane_id() { unsigned l; asm volatile("v_mbcnt_lo_u32_b32 %0, -1, 0\n\tv_mbcnt_hi_u32_b32 %0, -1, %0" : "=v"(l)); return l; }
#define RTS(slot) do {} while (0)
#define CT_END 0x1d000
#define ACLOB_LO "a0","a1","a2","a3","a4","a5","a6","a7","a8","a9","a10","a11","a12","a13","a14","a15","a16","a17","a18","a19","a20","a21","a22","a23","a24","a25","a26","a27","a28","a29","a30","a31","a32","a33","a34","a35","a36","a37","a38","a39","a40","a41","a42","a43","a44","a45","a46","a47","a48","a49","a50","a51","a52","a53","a54","a55","a56","a57","a58","a59","a60","a61","a62","a63","a64","a65","a66","a67","a68","a69","a70","a71","a72","a73","a74","a75","a76","a77","a78","a79","a80","a81","a82","a83","a84","a85","a86","a87","a88","a89","a90","a91","a92","a93","a94","a95","a96","a97","a98","a99","a100","a101","a102","a103","a104","a105","a106","a107","a108","a109","a110","a111","a112","a113","a114","a115","a116","a117","a118","a119","a120","a121","a122","a123","a124","a125","a126","a127"
#define ACLOB_HI "a128","a129","a130","a131","a132","a133","a134","a135","a136","a137","a138","a139","a140","a141","a142","a143","a144","a145","a146","a147","a148","a149","a150","a151","a152","a153","a154","a155","a156","a157","a158","a159","a160","a161","a162","a163","a164","a165","a166","a167","a168","a169","a170","a171","a172","a173","a174","a175","a176","a177","a178","a179","a180","a181","a182","a183","a184","a185","a186","a187","a188","a189","a190","a191","a192","a193","a194","a195","a196","a197","a198","a199","a200","a201","a202","a203","a204","a205","a206","a207","a208","a209","a210","a211","a212","a213","a214","a215","a216","a217","a218","a219","a220","a221","a222","a223","a224","a225","a226","a227","a228","a229","a230","a231","a232","a233","a234","a235","a236","a237","a238","a239","a240","a241","a242","a243","a244","a245","a246","a247","a248","a249","a250","a251","a252","a253","a254","a255"
#define PLO(p) ((unsigned)(size_t)(p))
#define PHI(p) ((unsigned)((size_t)(p) >> 32))
__device__ __forceinline__ unsigned vsel(bool c, unsigned a, unsigned b) { asm("" : "+v"(a), "+v"(b)); return c ? a : b; }
__device__ __forceinline__ unsigned ssel(unsigned c, unsigned a, unsigned b) { unsigned r; asm("s_cmp_lg_u32 %1, 0\n\ts_cselect_b32 %0, %2, %3" : "=s"(r) : "s"(c), "s"(a), "s"(b) : "scc"); return r; }
template <int N> __device__ __forceinline__ unsigned sslt(unsigned c, unsigned a, unsigned b) { unsigned r; asm("s_cmpk_lt_u32 %1, %4\n\ts_cselect_b32 %0, %2, %3" : "=s"(r) : "s"(c), "s"(a), "s"(b), "n"(N) : "scc"); return r; }
namespace att {
constexpr int LDS_BYTES = 163840;
constexpr unsigned LDS_G = 159744u, LDS_L = 161792u;
}
#define SD(J8, H, W) ((unsigned)WS_AF + (((J8) + 2u * ((W) & 2u) + ((W) & 1u)) * KS_TOT + 18u + 4u * (H)) * 1024u)
#define SRDA (u32x4){PLO(ws), PHI(ws) & 0xffffu, (unsigned)WS_L, 0x00020000u}
#define PROJ_ASM \
".p2align 3\n" \
"s_waitcnt lgkmcnt(0)\n" \
"s_mov_b32 s64, s10\n" \
"s_and_b32 s65, s11, 0xffff\n" \
"s_mov_b32 s66, 0x200000\n" \
"s_mov_b32 s67, 0x20000\n" \
"s_mov_b32 s68, s14\n" \
"s_and_b32 s69, s15, 0xffff\n" \
"s_mov_b32 s70, 0x400000\n" \
"s_mov_b32 s71, 0x20000\n" \
"s_mov_b32 s72, s16\n" \
"s_and_b32 s73, s17, 0xffff\n" \
"s_mov_b32 s74, 0x400000\n" \
"s_mov_b32 s75, 0x20000\n" \
"s_add_u32 m0, s30, 0x0\n" \
"s_mov_b32 s85, 0x0\n" \
"buffer_load_dwordx4 v22, s[64:67], s85 offen lds\n" \
"s_add_u32 m0, s30, 0x400\n" \
"s_mov_b32 s84, 0x2000\n" \
"buffer_load_dwordx4 v23, s[64:67], s84 offen lds\n" \
"s_add_u32 m0, s31, 0x0\n" \
"s_mov_b32 s85, 0x0\n" \
"buffer_load_dwordx4 v26, s[68:71], s85 offen lds\n" \
"s_add_u32 m0, s31, 0x4000\n" \
"s_mov_b32 s84, 0x0\n" \
"s_nop 0\n" \
"buffer_load_dwordx4 v26, s[72:75], s84 offen lds\n" \
"s_add_u32 m0, s30, 0x800\n" \
"s_mov_b32 s85, 0x4000\n" \
"buffer_load_dwordx4 v22, s[64:67], s85 offen lds\n" \
"s_add_u32 m0, s30, 0xc00\n" \
"s_mov_b32 s84, 0x6000\n" \
"buffer_load_dwordx4 v23, s[64:67], s84 offen lds\n" \
"s_add_u32 m0, s31, 0x400\n" \
"s_mov_b32 s85, 0x8000\n" \
"buffer_load_dwordx4 v26, s[68:71], s85 offen lds\n" \
"s_add_u32 m0, s31, 0x4400\n" \
"s_mov_b32 s84, 0x8000\n" \
"buffer_load_dwordx4 v26, s[72:75], s84 offen lds\n" \
"s_movk_i32 s7, 0x300\n" \
"s_lshr_b32 s4, s2, 1\n" \
"s_and_b32 s4, s4, 60\n" \
"s_or_b32 s4, s4, s3\n" \
"v_lshrrev_b32_e32 v212, 4, v20\n" \
"v_min_u32_e32 v212, 59, v212\n" \
"s_cmpk_lt_u32 s2, 0x80\n" \
"s_cselect_b32 s6, s14, s16\n" \
"v_subrev_u32_e32 v214, 48, v212\n" \
"v_mov_b32_e32 v212, s10\n" \
"v_mov_b32_e32 v215, s6\n" \
"s_cmpk_lt_u32 s2, 0x80\n" \
"s_cselect_b32 s5, s15, s17\n" \
"v_cmp_gt_u32_e32 vcc, s7, v20\n" \
"v_mov_b32_e64 v213, s11\n" \
"v_bfe_u32 v216, v20, 4, 3\n" \
"v_cndmask_b32_e32 v212, v212, v215, vcc\n" \
"v_mov_b32_e32 v215, s5\n" \
"s_lshl_b32 s5, s3, 4\n" \
"v_cndmask_b32_e32 v213, v213, v215, vcc\n" \
"v_lshlrev_b32_e64 v215, 5, v20\n" \
"v_and_b32_e32 v215, 0x7000, v215\n" \
"v_lshl_add_u32 v215, s4, 6, v215\n" \
"s_lshl_b32 s4, s2, 3\n" \
"s_and_b32 s4, s4, 56\n" \
"v_or3_b32 v215, v215, s4, v216\n" \
"s_lshl_b32 s4, s2, 6\n" \
"s_or_b32 s6, s5, s4\n" \
"v_cmp_gt_u32_e64 s[4:5], 6, v214\n" \
"v_add_u32_e32 v215, 0x2000, v215\n" \
"s_nop 0\n" \
"v_cndmask_b32_e64 v216, 4, 2, s[4:5]\n" \
"v_add3_u32 v214, s6, v214, v216\n" \
"s_lshl_b32 s4, s3, 9\n" \
"v_cndmask_b32_e32 v214, v214, v215, vcc\n" \
"v_lshlrev_b32_e32 v214, 7, v214\n" \
"v_mov_b32_e64 v215, 0\n" \
"v_lshl_add_u64 v[184:185], v[212:213], 0, v[214:215]\n" \
"s_or_b32 s32, s4, 0x25000\n" \
"s_add_u32 m0, s30, 0x1000\n" \
"s_mov_b32 s85, 0x8000\n" \
"buffer_load_dwordx4 v22, s[64:67], s85 offen lds\n" \
"s_and_b32 s33, s3, 1\n" \
"v_lshrrev_b32_e32 v212, 4, v20\n" \
"v_lshrrev_b32_e64 v214, 8, v20\n" \
"v_bfe_u32 v212, v212, 1, 3\n" \
"v_lshlrev_b32_e64 v215, 2, v20\n" \
"s_add_u32 m0, s30, 0x1400\n" \
"s_mov_b32 s84, 0xa000\n" \
"buffer_load_dwordx4 v23, s[64:67], s84 offen lds\n" \
"v_lshrrev_b32_e64 v216, 1, v20\n" \
"v_bfe_u32 v213, v20, 4, 4\n" \
"s_lshl_b32 s4, s3, 5\n" \
"v_xor_b32_e32 v212, v212, v214\n" \
"v_lshlrev_b32_e64 v214, 11, v214\n" \
"s_add_u32 m0, s31, 0x800\n" \
"s_mov_b32 s85, 0x10000\n" \
"buffer_load_dwordx4 v27, s[68:71], s85 offen lds\n" \
"v_and_b32_e32 v215, 0x300, v215\n" \
"v_and_b32_e64 v217, 24, v216\n" \
"v_and_or_b32 v213, s4, 64, v213\n" \
"v_or3_b32 v214, v215, v214, v217\n" \
"s_lshl_b32 s4, s33, 14\n" \
"s_add_u32 m0, s31, 0x4800\n" \
"s_mov_b32 s84, 0x10000\n" \
"buffer_load_dwordx4 v27, s[72:75], s84 offen lds\n" \
"v_and_b32_e32 v215, 0xe0, v216\n" \
"v_lshlrev_b32_e32 v213, 7, v213\n" \
"v_lshlrev_b32_e64 v212, 4, v212\n" \
"v_or3_b32 v214, v214, s4, v215\n" \
"v_or_b32_e64 v28, v212, v213\n" \
"s_add_u32 m0, s30, 0x1800\n" \
"s_mov_b32 s85, 0xc000\n" \
"buffer_load_dwordx4 v22, s[64:67], s85 offen lds\n" \
"v_add_u32_e32 v32, 0x10000, v214\n" \
"v_bitop3_b32 v29, v212, 64, v213 bitop3:0x36\n" \
"v_xor_b32_e32 v33, 0x20, v32\n" \
"v_xor_b32_e32 v34, 0x40, v32\n" \
"v_xor_b32_e32 v35, 0x60, v32\n" \
"s_add_u32 m0, s30, 0x1c00\n" \
"s_mov_b32 s84, 0xe000\n" \
"buffer_load_dwordx4 v23, s[64:67], s84 offen lds\n" \
"v_xor_b32_e32 v36, 0x80, v32\n" \
"v_xor_b32_e32 v37, 0xa0, v32\n" \
"v_xor_b32_e32 v38, 0xc0, v32\n" \
"v_xor_b32_e32 v39, 0xe0, v32\n" \
"s_mov_b32 s80, s26\n" \
"s_add_u32 m0, s31, 0xc00\n" \
"s_mov_b32 s85, 0x18000\n" \
"buffer_load_dwordx4 v27, s[68:71], s85 offen lds\n" \
"s_and_b32 s81, s27, 0xffff\n" \
"s_mov_b32 s82, 0x10000000\n" \
"s_mov_b32 s83, 0x20000\n" \
"s_mov_b32 s76, s18\n" \
"s_and_b32 s77, s19, 0xffff\n" \
"s_add_u32 m0, s31, 0x4c00\n" \
"s_mov_b32 s84, 0x18000\n" \
"buffer_load_dwordx4 v27, s[72:75], s84 offen lds\n" \
"s_mov_b32 s78, 0x1e6000\n" \
"s_mov_b32 s79, 0x20000\n" \
"s_add_u32 m0, s30, 0x8000\n" \
"s_mov_b32 s85, 0x80\n" \
"buffer_load_dwordx4 v22, s[64:67], s85 offen lds\n" \
"s_lshr_b32 s4, s2, 1\n" \
"s_and_b32 s4, s4, 0x7c\n" \
"v_bfe_u32 v214, v20, 4, 5\n" \
"s_or_b32 s7, s4, s3\n" \
"s_add_u32 m0, s30, 0x8400\n" \
"s_mov_b32 s84, 0x2080\n" \
"buffer_load_dwordx4 v23, s[64:67], s84 offen lds\n" \
"v_min_u32_e64 v214, 26, v214\n" \
"v_mad_u64_u32 v[214:215], s[4:5], s7, 27, v[214:215]\n" \
"v_mul_u32_u24_e32 v215, 0xaaab, v214\n" \
"v_lshrrev_b32_e64 v215, 18, v215\n" \
"s_add_u32 m0, s31, 0x8000\n" \
"s_mov_b32 s85, 0x80000\n" \
"buffer_load_dwordx4 v26, s[68:71], s85 offen lds\n" \
"s_mov_b32 s4, 0x1fffffa\n" \
"v_mad_u64_u32 v[216:217], s[4:5], v215, s4, v[214:215]\n" \
"s_and_b32 s6, s2, 7\n" \
"v_mul_u32_u24_e32 v215, 27, v215\n" \
"s_add_u32 m0, s31, 0xc000\n" \
"s_mov_b32 s84, 0x80000\n" \
"buffer_load_dwordx4 v26, s[72:75], s84 offen lds\n" \
"v_lshrrev_b32_e32 v216, 1, v216\n" \
"v_and_b32_e32 v214, 1, v214\n" \
"v_mov_b32_e32 v213, s13\n" \
"v_lshrrev_b32_e32 v212, 4, v20\n" \
"s_add_u32 m0, s30, 0x8800\n" \
"s_mov_b32 s85, 0x4080\n" \
"buffer_load_dwordx4 v22, s[64:67], s85 offen lds\n" \
"v_mul_u32_u24_e64 v216, 9, v216\n" \
"v_add3_u32 v214, v214, s6, v215\n" \
"v_add_lshl_u32 v214, v214, v216, 7\n" \
"v_mad_u64_u32 v[216:217], s[4:5], s7, 36, v[212:213]\n" \
"s_add_u32 m0, s30, 0x8c00\n" \
"s_mov_b32 s84, 0x6080\n" \
"buffer_load_dwordx4 v23, s[64:67], s84 offen lds\n" \
"s_movk_i32 s4, 0x240\n" \
"v_mov_b32_e32 v219, s9\n" \
"v_mov_b32_e32 v215, 0\n" \
"v_cmp_gt_u32_e32 vcc, s4, v20\n" \
"s_add_u32 m0, s31, 0x8400\n" \
"s_mov_b32 s85, 0x88000\n" \
"buffer_load_dwordx4 v26, s[68:71], s85 offen lds\n" \
"s_lshl_b32 s4, s2, 2\n" \
"v_lshl_add_u64 v[186:187], s[18:19], 0, v[214:215]\n" \
"v_mov_b32_e32 v214, s12\n" \
"v_mov_b32_e64 v217, s8\n" \
"s_add_u32 m0, s31, 0xc400\n" \
"s_mov_b32 s84, 0x88000\n" \
"buffer_load_dwordx4 v26, s[72:75], s84 offen lds\n" \
"s_or_b32 s4, s4, s3\n" \
"v_cndmask_b32_e32 v219, v219, v213, vcc\n" \
"v_and_b32_e32 v213, 0x3ff8, v216\n" \
"s_mul_i32 s4, s4, 18\n" \
"s_add_u32 m0, s30, 0x9000\n" \
"s_mov_b32 s85, 0x8080\n" \
"buffer_load_dwordx4 v22, s[64:67], s85 offen lds\n" \
"v_cndmask_b32_e32 v218, v217, v214, vcc\n" \
"v_or_b32_e32 v213, s6, v213\n" \
"v_and_b32_e32 v214, 7, v216\n" \
"v_min_u32_e64 v212, 53, v212\n" \
"s_add_u32 m0, s30, 0x9400\n" \
"s_mov_b32 s84, 0xa080\n" \
"buffer_load_dwordx4 v23, s[64:67], s84 offen lds\n" \
"s_sub_i32 s4, s4, 36\n" \
"v_lshl_or_b32 v213, v213, 3, v214\n" \
"v_add_u32_e32 v212, s4, v212\n" \
"v_cndmask_b32_e64 v212, v212, v213, vcc\n" \
"s_add_u32 m0, s31, 0x8800\n" \
"s_mov_b32 s85, 0x90000\n" \
"buffer_load_dwordx4 v27, s[68:71], s85 offen lds\n" \
"v_lshlrev_b32_e64 v214, 7, v212\n" \
"v_lshl_add_u64 v[188:189], v[218:219], 0, v[214:215]\n" \
"s_movk_i32 s5, 0xd80\n" \
"s_lshr_b32 s6, s2, 5\n" \
"s_add_u32 m0, s31, 0xc800\n" \
"s_mov_b32 s84, 0x90000\n" \
"buffer_load_dwordx4 v27, s[72:75], s84 offen lds\n" \
"v_lshrrev_b32_e64 v212, 4, v20\n" \
"v_lshl_or_b32 v215, s3, 6, v212\n" \
"v_mul_u32_u24_e32 v216, 0x2aab, v215\n" \
"v_lshrrev_b32_e32 v216, 16, v216\n" \
"s_mov_b32 m0, s32\n" \
"s_mov_b32 exec_lo, 0xffffff\n" \
"s_mov_b32 exec_hi, 0xfff0000\n" \
"global_load_lds_dword v[184:185], off\n" \
"s_mov_b64 exec, -1\n" \
"s_mov_b32 s84, 0x5bc0000\n" \
"buffer_load_dword v180, off, s[80:83], s84 sc1\n" \
"s_cmp_lg_u32 s33, 0\n" \
"s_cbranch_scc1 L_w1_%=\n" \
"s_waitcnt vmcnt(14)\n" \
"s_barrier\n" \
"s_nop 0\n" \
"ds_read_b64_tr_b16 v[84:85], v32 offset:0\n" \
"ds_read_b64_tr_b16 v[86:87], v32 offset:1024\n" \
"ds_read_b128 v[52:55], v28 offset:16384\n" \
"ds_read_b128 v[56:59], v28 offset:18432\n" \
"ds_read_b64_tr_b16 v[88:89], v33 offset:0\n" \
"ds_read_b64_tr_b16 v[90:91], v33 offset:1024\n" \
"ds_read_b64_tr_b16 v[92:93], v34 offset:0\n" \
"ds_read_b64_tr_b16 v[94:95], v34 offset:1024\n" \
"ds_read_b64_tr_b16 v[96:97], v35 offset:0\n" \
"ds_read_b64_tr_b16 v[98:99], v35 offset:1024\n" \
"ds_read_b64_tr_b16 v[100:101], v36 offset:0\n" \
"ds_read_b64_tr_b16 v[102:103], v36 offset:1024\n" \
"ds_read_b64_tr_b16 v[104:105], v37 offset:0\n" \
"ds_read_b64_tr_b16 v[106:107], v37 offset:1024\n" \
"ds_read_b64_tr_b16 v[108:109], v38 offset:0\n" \
"ds_read_b64_tr_b16 v[110:111], v38 offset:1024\n" \
"ds_read_b64_tr_b16 v[112:113], v39 offset:0\n" \
"ds_read_b64_tr_b16 v[114:115], v39 offset:1024\n" \
"ds_read_b128 v[60:63], v28 offset:20480\n" \
"ds_read_b128 v[64:67], v28 offset:22528\n" \
"ds_read_b128 v[68:71], v28 offset:0\n" \
"ds_read_b128 v[72:75], v28 offset:2048\n" \
"ds_read_b128 v[76:79], v28 offset:4096\n" \
"ds_read_b128 v[80:83], v28 offset:6144\n" \
"s_waitcnt lgkmcnt(15)\n" \
"s_nop 0\n" \
"v_mfma_f32_16x16x32_bf16 a[0:3], v[84:87], v[52:55], 0\n" \
"s_add_u32 m0, s30, 0x9800\n" \
"s_mov_b32 s85, 0xc080\n" \
"v_mfma_f32_16x16x32_bf16 a[32:35], v[84:87], v[56:59], 0\n" \
"buffer_load_dwordx4 v22, s[64:67], s85 offen lds\n" \
"v_mfma_f32_16x16x32_bf16 a[4:7], v[88:91], v[52:55], 0\n" \
"ds_read_b64_tr_b16 v[148:149], v32 offset:8192\n" \
"v_mad_i32_i24 v217, v216, -6, v215\n" \
"v_mfma_f32_16x16x32_bf16 a[36:39], v[88:91], v[56:59], 0\n" \
"s_add_u32 m0, s30, 0x9c00\n" \
"s_mov_b32 s84, 0xe080\n" \
"v_mfma_f32_16x16x32_bf16 a[8:11], v[92:95], v[52:55], 0\n" \
"buffer_load_dwordx4 v23, s[64:67], s84 offen lds\n" \
"v_mfma_f32_16x16x32_bf16 a[40:43], v[92:95], v[56:59], 0\n" \
"ds_read_b64_tr_b16 v[150:151], v32 offset:9216\n" \
"v_add_u32_e32 v218, 0xfffffe50, v215\n" \
"s_waitcnt lgkmcnt(15)\n" \
"v_mfma_f32_16x16x32_bf16 a[12:15], v[96:99], v[52:55], 0\n" \
"s_add_u32 m0, s31, 0x8c00\n" \
"s_mov_b32 s85, 0x98000\n" \
"v_mfma_f32_16x16x32_bf16 a[44:47], v[96:99], v[56:59], 0\n" \
"buffer_load_dwordx4 v27, s[68:71], s85 offen lds\n" \
"s_waitcnt lgkmcnt(14)\n" \
"v_mfma_f32_16x16x32_bf16 a[16:19], v[100:103], v[52:55], 0\n" \
"ds_read_b128 v[116:119], v29 offset:16384\n" \
"s_mul_i32 s35, s6, 0x48\n" \
"v_mfma_f32_16x16x32_bf16 a[48:51], v[100:103], v[56:59], 0\n" \
"s_add_u32 m0, s31, 0xcc00\n" \
"s_mov_b32 s84, 0x98000\n" \
"s_waitcnt lgkmcnt(13)\n" \
"v_mfma_f32_16x16x32_bf16 a[20:23], v[104:107], v[52:55], 0\n" \
"buffer_load_dwordx4 v27, s[72:75], s84 offen lds\n" \
"v_mfma_f32_16x16x32_bf16 a[52:55], v[104:107], v[56:59], 0\n" \
"ds_read_b128 v[120:123], v29 offset:18432\n" \
"v_lshrrev_b32_e32 v219, 1, v217\n" \
"s_waitcnt lgkmcnt(12)\n" \
"v_mfma_f32_16x16x32_bf16 a[24:27], v[108:111], v[52:55], 0\n" \
"ds_read_b64_tr_b16 v[152:153], v33 offset:8192\n" \
"v_add_u32_e32 v218, s35, v216\n" \
"v_mfma_f32_16x16x32_bf16 a[56:59], v[108:111], v[56:59], 0\n" \
"ds_read_b64_tr_b16 v[154:155], v33 offset:9216\n" \
"v_mul_u32_u24_e32 v219, 0x480, v219\n" \
"s_waitcnt lgkmcnt(12)\n" \
"v_mfma_f32_16x16x32_bf16 a[28:31], v[112:115], v[52:55], 0\n" \
"ds_read_b64_tr_b16 v[156:157], v34 offset:8192\n" \
"v_and_b32_e32 v214, 16, v20\n" \
"v_mfma_f32_16x16x32_bf16 a[60:63], v[112:115], v[56:59], 0\n" \
"ds_read_b64_tr_b16 v[158:159], v34 offset:9216\n" \
"v_mad_u32_u24 v219, v218, s5, v219\n" \
"s_waitcnt lgkmcnt(13)\n" \
"v_mfma_f32_16x16x32_bf16 a[92:95], v[112:115], v[60:63], 0\n" \
"s_mov_b32 m0, s32\n" \
"v_or_b32_e32 v192, v219, v214\n" \
"s_waitcnt lgkmcnt(12)\n" \
"v_mfma_f32_16x16x32_bf16 a[124:127], v[112:115], v[64:67], 0\n" \
"s_mov_b32 exec_lo, 0xff000000\n" \
"s_mov_b32 exec_hi, 0x0\n" \
"global_load_lds_dword v[184:185], off\n" \
"s_mov_b64 exec, -1\n" \
"v_mfma_f32_16x16x32_bf16 a[88:91], v[108:111], v[60:63], 0\n" \
"ds_read_b64_tr_b16 v[160:161], v35 offset:8192\n" \
"v_mov_b32_e64 v220, 2.0\n" \
"v_mfma_f32_16x16x32_bf16 a[120:123], v[108:111], v[64:67], 0\n" \
"ds_read_b64_tr_b16 v[162:163], v35 offset:9216\n" \
"v_mov_b32_e64 v222, 7\n" \
"v_mfma_f32_16x16x32_bf16 a[84:87], v[104:107], v[60:63], 0\n" \
"v_lshlrev_b32_e32 v220, 7, v218\n" \
"v_lshrrev_b32_e32 v218, 1, v218\n" \
"v_mfma_f32_16x16x32_bf16 a[116:119], v[104:107], v[64:67], 0\n" \
"s_mov_b32 m0, s32\n" \
"ds_read_b64_tr_b16 v[164:165], v36 offset:8192\n" \
"v_mfma_f32_16x16x32_bf16 a[80:83], v[100:103], v[60:63], 0\n" \
"s_mov_b32 exec_lo, 0xffff\n" \
"s_mov_b32 exec_hi, 0x0\n" \
"global_load_lds_dword v[186:187], off\n" \
"s_mov_b64 exec, -1\n" \
"v_mfma_f32_16x16x32_bf16 a[112:115], v[100:103], v[64:67], 0\n" \
"ds_read_b64_tr_b16 v[166:167], v36 offset:9216\n" \
"v_and_b32_e32 v218, 4, v218\n" \
"v_mfma_f32_16x16x32_bf16 a[76:79], v[96:99], v[60:63], 0\n" \
"v_and_b32_e64 v216, 2, v216\n" \
"s_mul_i32 s7, s6, 0x2400\n" \
"v_mfma_f32_16x16x32_bf16 a[108:111], v[96:99], v[64:67], 0\n" \
"s_sub_i32 s34, 0x22000, s7\n" \
"v_bitop3_b32 v216, v217, v218, v216 bitop3:0x1e\n" \
"v_mfma_f32_16x16x32_bf16 a[72:75], v[92:95], v[60:63], 0\n" \
"ds_read_b64_tr_b16 v[168:169], v37 offset:8192\n" \
"v_or_b32_e32 v213, 0x24400, v20\n" \
"v_mfma_f32_16x16x32_bf16 a[104:107], v[92:95], v[64:67], 0\n" \
"v_lshlrev_b32_e64 v216, 4, v216\n" \
"v_add3_u32 v196, v220, s34, v216\n" \
"v_mfma_f32_16x16x32_bf16 a[68:71], v[88:91], v[60:63], 0\n" \
"ds_read_b64_tr_b16 v[170:171], v37 offset:9216\n" \
"v_mov_b32_e64 v216, v213\n" \
"v_mfma_f32_16x16x32_bf16 a[100:103], v[88:91], v[64:67], 0\n" \
"s_movk_i32 s36, 0x1b0\n" \
"v_or_b32_e32 v216, 0x100, v215\n" \
"v_mfma_f32_16x16x32_bf16 a[64:67], v[84:87], v[60:63], 0\n" \
"ds_read_b64_tr_b16 v[172:173], v38 offset:8192\n" \
"v_mul_u32_u24_e32 v217, 0x2aab, v216\n" \
"v_mfma_f32_16x16x32_bf16 a[96:99], v[84:87], v[64:67], 0\n" \
"v_add_u32_e32 v215, 0xffffff50, v215\n" \
"v_lshrrev_b32_e32 v217, 16, v217\n" \
"s_waitcnt lgkmcnt(15)\n" \
"v_mfma_f32_16x16x32_bf16 a[128:131], v[84:87], v[68:71], 0\n" \
"ds_read_b64_tr_b16 v[174:175], v38 offset:9216\n" \
"v_mov_b32_e32 v220, v215\n" \
"v_mfma_f32_16x16x32_bf16 a[160:163], v[84:87], v[72:75], 0\n" \
"v_mad_i32_i24 v218, v217, -6, v216\n" \
"v_cmp_gt_u32_e64 vcc, s36, v216\n" \
"v_mfma_f32_16x16x32_bf16 a[132:135], v[88:91], v[68:71], 0\n" \
"ds_read_b64_tr_b16 v[176:177], v39 offset:8192\n" \
"v_mov_b32_e64 v221, 7\n" \
"v_mfma_f32_16x16x32_bf16 a[164:167], v[88:91], v[72:75], 0\n" \
"v_lshrrev_b32_e32 v222, 1, v218\n" \
"v_cndmask_b32_e32 v217, v220, v217, vcc\n" \
"v_mfma_f32_16x16x32_bf16 a[136:139], v[92:95], v[68:71], 0\n" \
"ds_read_b64_tr_b16 v[178:179], v39 offset:9216\n" \
"s_movk_i32 s4, 0x48\n" \
"v_mfma_f32_16x16x32_bf16 a[168:171], v[92:95], v[72:75], 0\n" \
"v_add_u32_e32 v220, s35, v217\n" \
"v_mul_u32_u24_e32 v222, 0x480, v222\n" \
"v_mfma_f32_16x16x32_bf16 a[140:143], v[96:99], v[68:71], 0\n" \
"v_mad_u32_u24 v222, v220, s5, v222\n" \
"v_cmp_gt_u32_e64 s[4:5], s4, v215\n" \
"v_mfma_f32_16x16x32_bf16 a[172:175], v[96:99], v[72:75], 0\n" \
"ds_read_b128 v[124:127], v29 offset:20480\n" \
"v_cndmask_b32_e64 v215, v221, v218, vcc\n" \
"v_mfma_f32_16x16x32_bf16 a[144:147], v[100:103], v[68:71], 0\n" \
"v_lshrrev_b32_e32 v218, 1, v220\n" \
"v_or_b32_e32 v214, v222, v214\n" \
"v_mfma_f32_16x16x32_bf16 a[176:179], v[100:103], v[72:75], 0\n" \
"ds_read_b128 v[128:131], v29 offset:22528\n" \
"v_mov_b32_e64 v222, 2.0\n" \
"v_mfma_f32_16x16x32_bf16 a[148:151], v[104:107], v[68:71], 0\n" \
"v_and_b32_e32 v218, 4, v218\n" \
"v_and_b32_e32 v217, 2, v217\n" \
"v_mfma_f32_16x16x32_bf16 a[180:183], v[104:107], v[72:75], 0\n" \
"ds_read_b128 v[132:135], v29 offset:0\n" \
"v_bitop3_b32 v215, v215, v218, v217 bitop3:0x1e\n" \
"v_mfma_f32_16x16x32_bf16 a[152:155], v[108:111], v[68:71], 0\n" \
"v_lshlrev_b32_e32 v223, 1, v220\n" \
"v_mov_b32_e32 v224, 2.0\n" \
"v_mfma_f32_16x16x32_bf16 a[184:187], v[108:111], v[72:75], 0\n" \
"ds_read_b128 v[136:139], v29 offset:2048\n" \
"v_cndmask_b32_e64 v193, v222, v214, vcc\n" \
"v_mfma_f32_16x16x32_bf16 a[156:159], v[112:115], v[68:71], 0\n" \
"v_lshlrev_b32_e32 v214, 7, v220\n" \
"v_lshlrev_b32_e32 v215, 4, v215\n" \
"v_mfma_f32_16x16x32_bf16 a[188:191], v[112:115], v[72:75], 0\n" \
"ds_read_b128 v[140:143], v29 offset:4096\n" \
"s_mul_i32 s39, s3, 18\n" \
"v_mfma_f32_16x16x32_bf16 a[220:223], v[112:115], v[76:79], 0\n" \
"v_add3_u32 v214, v214, s34, v215\n" \
"v_cndmask_b32_e64 v195, v224, v223, s[4:5]\n" \
"v_mfma_f32_16x16x32_bf16 a[252:255], v[112:115], v[80:83], 0\n" \
"ds_read_b128 v[144:147], v29 offset:6144\n" \
"s_movk_i32 s4, 0x1f8\n" \
"v_mfma_f32_16x16x32_bf16 a[216:219], v[108:111], v[76:79], 0\n" \
"v_mov_b32_e32 v215, v213\n" \
"s_add_i32 s35, s35, s39\n" \
"v_mfma_f32_16x16x32_bf16 a[248:251], v[108:111], v[80:83], 0\n" \
"v_cmp_gt_u32_e32 vcc, s4, v216\n" \
"v_add_u32_e32 v212, s35, v212\n" \
"v_mfma_f32_16x16x32_bf16 a[212:215], v[104:107], v[76:79], 0\n" \
"v_mov_b32_e32 v219, 2.0\n" \
"v_cndmask_b32_e32 v197, v215, v214, vcc\n" \
"v_mfma_f32_16x16x32_bf16 a[244:247], v[104:107], v[80:83], 0\n" \
"s_movk_i32 s35, 0x120\n" \
"v_mul_u32_u24_e32 v214, 0xd80, v212\n" \
"v_mfma_f32_16x16x32_bf16 a[208:211], v[100:103], v[76:79], 0\n" \
"v_cmp_gt_u32_e32 vcc, s35, v20\n" \
"v_lshrrev_b32_e64 v215, 1, v212\n" \
"v_mfma_f32_16x16x32_bf16 a[240:243], v[100:103], v[80:83], 0\n" \
"s_and_b32 s4, s2, 7\n" \
"v_cndmask_b32_e32 v194, v219, v214, vcc\n" \
"v_mfma_f32_16x16x32_bf16 a[204:207], v[96:99], v[76:79], 0\n" \
"v_lshlrev_b32_e32 v214, 7, v212\n" \
"v_lshrrev_b32_e32 v212, 2, v212\n" \
"v_mfma_f32_16x16x32_bf16 a[236:239], v[96:99], v[80:83], 0\n" \
"v_and_b32_e64 v212, 2, v212\n" \
"v_and_or_b32 v212, v215, 1, v212\n" \
"v_mfma_f32_16x16x32_bf16 a[200:203], v[92:95], v[76:79], 0\n" \
"s_lshl_b32 s5, s4, 2\n" \
"s_bfe_u32 s38, s2, 0x20003\n" \
"v_mfma_f32_16x16x32_bf16 a[232:235], v[92:95], v[80:83], 0\n" \
"v_lshlrev_b32_e32 v212, 5, v212\n" \
"s_movk_i32 s35, 0x60\n" \
"v_mfma_f32_16x16x32_bf16 a[196:199], v[88:91], v[76:79], 0\n" \
"s_or_b32 s5, s5, s38\n" \
"v_bitop3_b32 v212, v212, s35, v214 bitop3:0x36\n" \
"v_mfma_f32_16x16x32_bf16 a[228:231], v[88:91], v[80:83], 0\n" \
"s_lshr_b32 s36, s2, 3\n" \
"v_add_u32_e32 v212, s34, v212\n" \
"v_mfma_f32_16x16x32_bf16 a[192:195], v[84:87], v[76:79], 0\n" \
"s_mul_i32 s34, s5, 0x12000\n" \
"s_or_b32 s40, s34, 0x6000000\n" \
"v_mfma_f32_16x16x32_bf16 a[224:227], v[84:87], v[80:83], 0\n" \
"s_waitcnt vmcnt(2) lgkmcnt(0)\n" \
"s_barrier\n" \
"v_mfma_f32_16x16x32_bf16 a[0:3], v[148:151], v[116:119], a[0:3]\n" \
"s_add_u32 m0, s30, 0x0\n" \
"s_mov_b32 s85, 0x100\n" \
"v_mfma_f32_16x16x32_bf16 a[32:35], v[148:151], v[120:123], a[32:35]\n" \
"buffer_load_dwordx4 v22, s[64:67], s85 offen lds\n" \
"v_mfma_f32_16x16x32_bf16 a[4:7], v[152:155], v[116:119], a[4:7]\n" \
"ds_read_b64_tr_b16 v[84:85], v32 offset:32768\n" \
"s_mul_i32 s34, s36, 0x90000\n" \
"v_mfma_f32_16x16x32_bf16 a[36:39], v[152:155], v[120:123], a[36:39]\n" \
"ds_read_b64_tr_b16 v[86:87], v32 offset:33792\n" \
"s_lshl_b32 s36, s3, 14\n" \
"v_mfma_f32_16x16x32_bf16 a[8:11], v[156:159], v[116:119], a[8:11]\n" \
"s_add_u32 m0, s30, 0x400\n" \
"s_mov_b32 s84, 0x2100\n" \
"v_mfma_f32_16x16x32_bf16 a[40:43], v[156:159], v[120:123], a[40:43]\n" \
"buffer_load_dwordx4 v23, s[64:67], s84 offen lds\n" \
"v_mfma_f32_16x16x32_bf16 a[12:15], v[160:163], v[116:119], a[12:15]\n" \
"ds_read_b128 v[52:55], v28 offset:49152\n" \
"s_add_i32 s34, s34, s36\n" \
"v_mfma_f32_16x16x32_bf16 a[44:47], v[160:163], v[120:123], a[44:47]\n" \
"ds_read_b128 v[56:59], v28 offset:51200\n" \
"s_lshl_b32 s38, s5, 8\n" \
"v_mfma_f32_16x16x32_bf16 a[16:19], v[164:167], v[116:119], a[16:19]\n" \
"s_add_u32 m0, s31, 0x0\n" \
"s_mov_b32 s85, 0x100000\n" \
"v_mfma_f32_16x16x32_bf16 a[48:51], v[164:167], v[120:123], a[48:51]\n" \
"buffer_load_dwordx4 v26, s[68:71], s85 offen lds\n" \
"v_mfma_f32_16x16x32_bf16 a[20:23], v[168:171], v[116:119], a[20:23]\n" \
"ds_read_b64_tr_b16 v[88:89], v33 offset:32768\n" \
"s_add_i32 s41, s34, 0x4240000\n" \
"v_mfma_f32_16x16x32_bf16 a[52:55], v[168:171], v[120:123], a[52:55]\n" \
"ds_read_b64_tr_b16 v[90:91], v33 offset:33792\n" \
"s_mul_i32 s34, s5, 36\n" \
"v_mfma_f32_16x16x32_bf16 a[24:27], v[172:175], v[116:119], a[24:27]\n" \
"s_add_u32 m0, s31, 0x4000\n" \
"s_mov_b32 s84, 0x100000\n" \
"v_mfma_f32_16x16x32_bf16 a[56:59], v[172:175], v[120:123], a[56:59]\n" \
"buffer_load_dwordx4 v26, s[72:75], s84 offen lds\n" \
"v_mfma_f32_16x16x32_bf16 a[28:31], v[176:179], v[116:119], a[28:31]\n" \
"ds_read_b64_tr_b16 v[92:93], v34 offset:32768\n" \
"s_add_i32 s5, s7, s40\n" \
"v_mfma_f32_16x16x32_bf16 a[60:63], v[176:179], v[120:123], a[60:63]\n" \
"ds_read_b64_tr_b16 v[94:95], v34 offset:33792\n" \
"s_addk_i32 s5, 0x2000\n" \
"v_mfma_f32_16x16x32_bf16 a[92:95], v[176:179], v[124:127], a[92:95]\n" \
"s_add_u32 m0, s30, 0x800\n" \
"s_mov_b32 s85, 0x4100\n" \
"v_mfma_f32_16x16x32_bf16 a[124:127], v[176:179], v[128:131], a[124:127]\n" \
"buffer_load_dwordx4 v22, s[64:67], s85 offen lds\n" \
"v_mfma_f32_16x16x32_bf16 a[88:91], v[172:175], v[124:127], a[88:91]\n" \
"ds_read_b64_tr_b16 v[96:97], v35 offset:32768\n" \
"s_mov_b32 s37, 2.0\n" \
"s_nop 0\n" \
"v_mfma_f32_16x16x32_bf16 a[120:123], v[172:175], v[128:131], a[120:123]\n" \
"ds_read_b64_tr_b16 v[98:99], v35 offset:33792\n" \
"s_or_b32 s38, s38, 0x5be8000\n" \
"v_mfma_f32_16x16x32_bf16 a[84:87], v[168:171], v[124:127], a[84:87]\n" \
"s_add_u32 m0, s30, 0xc00\n" \
"s_mov_b32 s84, 0x6100\n" \
"v_mfma_f32_16x16x32_bf16 a[116:119], v[168:171], v[128:131], a[116:119]\n" \
"buffer_load_dwordx4 v23, s[64:67], s84 offen lds\n" \
"v_mfma_f32_16x16x32_bf16 a[80:83], v[164:167], v[124:127], a[80:83]\n" \
"ds_read_b64_tr_b16 v[100:101], v36 offset:32768\n" \
"v_mfma_f32_16x16x32_bf16 a[112:115], v[164:167], v[128:131], a[112:115]\n" \
"ds_read_b64_tr_b16 v[102:103], v36 offset:33792\n" \
"v_mfma_f32_16x16x32_bf16 a[76:79], v[160:163], v[124:127], a[76:79]\n" \
"s_add_u32 m0, s31, 0x400\n" \
"s_mov_b32 s85, 0x108000\n" \
"v_mfma_f32_16x16x32_bf16 a[108:111], v[160:163], v[128:131], a[108:111]\n" \
"buffer_load_dwordx4 v26, s[68:71], s85 offen lds\n" \
"v_mfma_f32_16x16x32_bf16 a[72:75], v[156:159], v[124:127], a[72:75]\n" \
"ds_read_b64_tr_b16 v[104:105], v37 offset:32768\n" \
"v_mfma_f32_16x16x32_bf16 a[104:107], v[156:159], v[128:131], a[104:107]\n" \
"s_cmp_lg_u32 s3, 0\n" \
"s_cselect_b32 s36, s37, s5\n" \
"v_mfma_f32_16x16x32_bf16 a[68:71], v[152:155], v[124:127], a[68:71]\n" \
"s_add_u32 m0, s31, 0x4400\n" \
"s_mov_b32 s84, 0x108000\n" \
"v_mfma_f32_16x16x32_bf16 a[100:103], v[152:155], v[128:131], a[100:103]\n" \
"buffer_load_dwordx4 v26, s[72:75], s84 offen lds\n" \
"v_mfma_f32_16x16x32_bf16 a[64:67], v[148:151], v[124:127], a[64:67]\n" \
"ds_read_b64_tr_b16 v[106:107], v37 offset:33792\n" \
"s_lshl_b32 s5, s6, 2\n" \
"v_mfma_f32_16x16x32_bf16 a[96:99], v[148:151], v[128:131], a[96:99]\n" \
"ds_read_b64_tr_b16 v[108:109], v38 offset:32768\n" \
"s_or_b32 s5, s38, s5\n" \
"v_mfma_f32_16x16x32_bf16 a[128:131], v[148:151], v[132:135], a[128:131]\n" \
"s_add_u32 m0, s30, 0x1000\n" \
"s_mov_b32 s85, 0x8100\n" \
"v_mfma_f32_16x16x32_bf16 a[160:163], v[148:151], v[136:139], a[160:163]\n" \
"buffer_load_dwordx4 v22, s[64:67], s85 offen lds\n" \
"v_mfma_f32_16x16x32_bf16 a[132:135], v[152:155], v[132:135], a[132:135]\n" \
"ds_read_b64_tr_b16 v[110:111], v38 offset:33792\n" \
"s_lshl_b32 s35, s3, 10\n" \
"s_nop 0\n" \
"v_mfma_f32_16x16x32_bf16 a[164:167], v[152:155], v[136:139], a[164:167]\n" \
"ds_read_b64_tr_b16 v[112:113], v39 offset:32768\n" \
"v_mfma_f32_16x16x32_bf16 a[136:139], v[156:159], v[132:135], a[136:139]\n" \
"s_add_u32 m0, s30, 0x1400\n" \
"s_mov_b32 s84, 0xa100\n" \
"v_mfma_f32_16x16x32_bf16 a[168:171], v[156:159], v[136:139], a[168:171]\n" \
"buffer_load_dwordx4 v23, s[64:67], s84 offen lds\n" \
"v_mfma_f32_16x16x32_bf16 a[140:143], v[160:163], v[132:135], a[140:143]\n" \
"ds_read_b64_tr_b16 v[114:115], v39 offset:33792\n" \
"v_mfma_f32_16x16x32_bf16 a[172:175], v[160:163], v[136:139], a[172:175]\n" \
"ds_read_b128 v[60:63], v28 offset:53248\n" \
"v_mfma_f32_16x16x32_bf16 a[144:147], v[164:167], v[132:135], a[144:147]\n" \
"s_add_u32 m0, s31, 0x800\n" \
"s_mov_b32 s85, 0x110000\n" \
"v_mfma_f32_16x16x32_bf16 a[176:179], v[164:167], v[136:139], a[176:179]\n" \
"buffer_load_dwordx4 v27, s[68:71], s85 offen lds\n" \
"v_mfma_f32_16x16x32_bf16 a[148:151], v[168:171], v[132:135], a[148:151]\n" \
"ds_read_b128 v[64:67], v28 offset:55296\n" \
"v_mfma_f32_16x16x32_bf16 a[180:183], v[168:171], v[136:139], a[180:183]\n" \
"ds_read_b128 v[68:71], v28 offset:32768\n" \
"v_mfma_f32_16x16x32_bf16 a[152:155], v[172:175], v[132:135], a[152:155]\n" \
"s_add_u32 m0, s31, 0x4800\n" \
"s_mov_b32 s84, 0x110000\n" \
"v_mfma_f32_16x16x32_bf16 a[184:187], v[172:175], v[136:139], a[184:187]\n" \
"buffer_load_dwordx4 v27, s[72:75], s84 offen lds\n" \
"v_mfma_f32_16x16x32_bf16 a[156:159], v[176:179], v[132:135], a[156:159]\n" \
"ds_read_b128 v[72:75], v28 offset:34816\n" \
"v_mfma_f32_16x16x32_bf16 a[188:191], v[176:179], v[136:139], a[188:191]\n" \
"ds_read_b128 v[76:79], v28 offset:36864\n" \
"v_mfma_f32_16x16x32_bf16 a[220:223], v[176:179], v[140:143], a[220:223]\n" \
"s_cmpk_lt_u32 s3, 3\n" \
"s_cselect_b32 s37, s37, s5\n" \
"v_mfma_f32_16x16x32_bf16 a[252:255], v[176:179], v[144:147], a[252:255]\n" \
"ds_read_b128 v[80:83], v28 offset:38912\n" \
"s_lshl_b32 s5, s4, 16\n" \
"v_mfma_f32_16x16x32_bf16 a[216:219], v[172:175], v[140:143], a[216:219]\n" \
"s_lshl_b32 s4, s4, 11\n" \
"v_cndmask_b32_e64 v198, v213, v212, vcc\n" \
"v_mfma_f32_16x16x32_bf16 a[248:251], v[172:175], v[144:147], a[248:251]\n" \
"v_or_b32_e32 v212, s35, v20\n" \
"s_or_b32 s39, s40, s35\n" \
"v_mfma_f32_16x16x32_bf16 a[212:215], v[168:171], v[140:143], a[212:215]\n" \
"s_or_b32 s55, s41, s4\n" \
"v_or_b32_e32 v199, 0x22000, v212\n" \
"v_mfma_f32_16x16x32_bf16 a[244:247], v[168:171], v[144:147], a[244:247]\n" \
"s_add_i32 s35, s39, s7\n" \
"s_add_i32 s54, s41, s5\n" \
"v_mfma_f32_16x16x32_bf16 a[208:211], v[164:167], v[140:143], a[208:211]\n" \
"s_add_i32 s55, s55, 0x80000\n" \
"v_mov_b32_e32 v220, 0xffff\n" \
"v_mfma_f32_16x16x32_bf16 a[240:243], v[164:167], v[144:147], a[240:243]\n" \
"s_and_b32 s4, s3, 1\n" \
"s_lshl_b32 s5, s4, 4\n" \
"v_mfma_f32_16x16x32_bf16 a[204:207], v[160:163], v[140:143], a[204:207]\n" \
"v_lshrrev_b32_e32 v212, 4, v20\n" \
"v_lshrrev_b32_e64 v213, 6, v20\n" \
"v_mfma_f32_16x16x32_bf16 a[236:239], v[160:163], v[144:147], a[236:239]\n" \
"v_and_b32_e64 v222, 12, v213\n" \
"v_and_or_b32 v221, v212, 15, s5\n" \
"v_mfma_f32_16x16x32_bf16 a[200:203], v[156:159], v[140:143], a[200:203]\n" \
"v_cmp_gt_u32_e64 vcc, v222, v221\n" \
"v_mov_b32_e32 v223, 0xffff0000\n" \
"v_mfma_f32_16x16x32_bf16 a[232:235], v[156:159], v[144:147], a[232:235]\n" \
"v_or_b32_e64 v217, 2, v222\n" \
"v_cndmask_b32_e64 v214, v220, 0, vcc\n" \
"v_mfma_f32_16x16x32_bf16 a[196:199], v[152:155], v[140:143], a[196:199]\n" \
"v_cmp_lt_u32_e32 vcc, v222, v221\n" \
"v_or_b32_e32 v219, 16, v222\n" \
"v_mfma_f32_16x16x32_bf16 a[228:231], v[152:155], v[144:147], a[228:231]\n" \
"v_or_b32_e32 v224, 18, v222\n" \
"v_cndmask_b32_e32 v215, 0, v223, vcc\n" \
"v_mfma_f32_16x16x32_bf16 a[192:195], v[148:151], v[140:143], a[192:195]\n" \
"v_cmp_gt_u32_e32 vcc, v217, v221\n" \
"s_lshl_b32 s6, s2, 2\n" \
"v_mfma_f32_16x16x32_bf16 a[224:227], v[148:151], v[144:147], a[224:227]\n" \
"s_waitcnt lgkmcnt(15)\n" \
"v_mfma_f32_16x16x32_bf16 a[0:3], v[84:87], v[52:55], a[0:3]\n" \
"s_add_u32 m0, s30, 0x1800\n" \
"s_mov_b32 s85, 0xc100\n" \
"v_mfma_f32_16x16x32_bf16 a[32:35], v[84:87], v[56:59], a[32:35]\n" \
"buffer_load_dwordx4 v22, s[64:67], s85 offen lds\n" \
"v_mfma_f32_16x16x32_bf16 a[4:7], v[88:91], v[52:55], a[4:7]\n" \
"ds_read_b64_tr_b16 v[148:149], v32 offset:40960\n" \
"s_or_b32 s40, s6, s3\n" \
"v_mfma_f32_16x16x32_bf16 a[36:39], v[88:91], v[56:59], a[36:39]\n" \
"s_add_u32 m0, s30, 0x1c00\n" \
"s_mov_b32 s84, 0xe100\n" \
"v_mfma_f32_16x16x32_bf16 a[8:11], v[92:95], v[52:55], a[8:11]\n" \
"buffer_load_dwordx4 v23, s[64:67], s84 offen lds\n" \
"v_mfma_f32_16x16x32_bf16 a[40:43], v[92:95], v[56:59], a[40:43]\n" \
"ds_read_b64_tr_b16 v[150:151], v32 offset:41984\n" \
"v_cndmask_b32_e64 v216, v220, 0, vcc\n" \
"s_waitcnt lgkmcnt(15)\n" \
"v_mfma_f32_16x16x32_bf16 a[12:15], v[96:99], v[52:55], a[12:15]\n" \
"s_add_u32 m0, s31, 0xc00\n" \
"s_mov_b32 s85, 0x118000\n" \
"v_mfma_f32_16x16x32_bf16 a[44:47], v[96:99], v[56:59], a[44:47]\n" \
"buffer_load_dwordx4 v27, s[68:71], s85 offen lds\n" \
"s_waitcnt lgkmcnt(14)\n" \
"v_mfma_f32_16x16x32_bf16 a[16:19], v[100:103], v[52:55], a[16:19]\n" \
"ds_read_b128 v[116:119], v29 offset:49152\n" \
"v_cmp_lt_u32_e64 vcc, v217, v221\n" \
"v_mfma_f32_16x16x32_bf16 a[48:51], v[100:103], v[56:59], a[48:51]\n" \
"s_add_u32 m0, s31, 0x4c00\n" \
"s_mov_b32 s84, 0x118000\n" \
"s_waitcnt lgkmcnt(13)\n" \
"v_mfma_f32_16x16x32_bf16 a[20:23], v[104:107], v[52:55], a[20:23]\n" \
"buffer_load_dwordx4 v27, s[72:75], s84 offen lds\n" \
"v_mfma_f32_16x16x32_bf16 a[52:55], v[104:107], v[56:59], a[52:55]\n" \
"ds_read_b128 v[120:123], v29 offset:51200\n" \
"s_add_i32 s41, s40, 0xffffffaf\n" \
"s_waitcnt lgkmcnt(12)\n" \
"v_mfma_f32_16x16x32_bf16 a[24:27], v[108:111], v[52:55], a[24:27]\n" \
"ds_read_b64_tr_b16 v[152:153], v33 offset:40960\n" \
"s_mov_b32 s5, 0\n" \
"v_mfma_f32_16x16x32_bf16 a[56:59], v[108:111], v[56:59], a[56:59]\n" \
"ds_read_b64_tr_b16 v[154:155], v33 offset:41984\n" \
"v_cndmask_b32_e32 v217, 0, v223, vcc\n" \
"s_waitcnt lgkmcnt(12)\n" \
"v_mfma_f32_16x16x32_bf16 a[28:31], v[112:115], v[52:55], a[28:31]\n" \
"ds_read_b64_tr_b16 v[156:157], v34 offset:40960\n" \
"v_cmp_gt_u32_e32 vcc, v219, v221\n" \
"v_mfma_f32_16x16x32_bf16 a[60:63], v[112:115], v[56:59], a[60:63]\n" \
"ds_read_b64_tr_b16 v[158:159], v34 offset:41984\n" \
"s_waitcnt lgkmcnt(13)\n" \
"v_mfma_f32_16x16x32_bf16 a[92:95], v[112:115], v[60:63], a[92:95]\n" \
"s_mov_b32 m0, s32\n" \
"s_waitcnt lgkmcnt(12)\n" \
"v_mfma_f32_16x16x32_bf16 a[124:127], v[112:115], v[64:67], a[124:127]\n" \
"s_mov_b32 exec_lo, 0x0\n" \
"s_mov_b32 exec_hi, 0xff\n" \
"global_load_lds_dword v[184:185], off\n" \
"s_mov_b64 exec, -1\n" \
"v_mfma_f32_16x16x32_bf16 a[88:91], v[108:111], v[60:63], a[88:91]\n" \
"ds_read_b64_tr_b16 v[160:161], v35 offset:40960\n" \
"v_mfma_f32_16x16x32_bf16 a[120:123], v[108:111], v[64:67], a[120:123]\n" \
"ds_read_b64_tr_b16 v[162:163], v35 offset:41984\n" \
"v_mfma_f32_16x16x32_bf16 a[84:87], v[104:107], v[60:63], a[84:87]\n" \
"s_cmpk_lt_u32 s40, 0x51\n" \
"s_cselect_b32 s7, s23, s25\n" \
"v_mfma_f32_16x16x32_bf16 a[116:119], v[104:107], v[64:67], a[116:119]\n" \
"s_mov_b32 m0, s32\n" \
"ds_read_b64_tr_b16 v[164:165], v36 offset:40960\n" \
"v_mfma_f32_16x16x32_bf16 a[80:83], v[100:103], v[60:63], a[80:83]\n" \
"s_mov_b32 exec_lo, 0xffff0000\n" \
"s_mov_b32 exec_hi, 0x0\n" \
"global_load_lds_dword v[186:187], off\n" \
"s_mov_b64 exec, -1\n" \
"v_mfma_f32_16x16x32_bf16 a[112:115], v[100:103], v[64:67], a[112:115]\n" \
"ds_read_b64_tr_b16 v[166:167], v36 offset:41984\n" \
"v_mov_b32_e64 v11, 0\n" \
"v_mfma_f32_16x16x32_bf16 a[76:79], v[96:99], v[60:63], a[76:79]\n" \
"s_cmpk_lt_u32 s40, 0x51\n" \
"s_cselect_b32 s6, s22, s24\n" \
"v_mfma_f32_16x16x32_bf16 a[108:111], v[96:99], v[64:67], a[108:111]\n" \
"s_lshl_b32 s44, s3, 1\n" \
"v_cndmask_b32_e64 v218, v220, 0, vcc\n" \
"v_mfma_f32_16x16x32_bf16 a[72:75], v[92:95], v[60:63], a[72:75]\n" \
"ds_read_b64_tr_b16 v[168:169], v37 offset:40960\n" \
"v_cmp_lt_u32_e32 vcc, v219, v221\n" \
"v_mfma_f32_16x16x32_bf16 a[104:107], v[92:95], v[64:67], a[104:107]\n" \
"s_cmpk_lt_u32 s40, 0x291\n" \
"s_cselect_b32 s41, s41, s5\n" \
"v_mfma_f32_16x16x32_bf16 a[68:71], v[88:91], v[60:63], a[68:71]\n" \
"ds_read_b64_tr_b16 v[170:171], v37 offset:41984\n" \
"s_bfe_u32 s43, s2, 0x20003\n" \
"v_mfma_f32_16x16x32_bf16 a[100:103], v[88:91], v[64:67], a[100:103]\n" \
"s_cmpk_lt_u32 s40, 0x51\n" \
"s_cselect_b32 s40, s40, s41\n" \
"v_mfma_f32_16x16x32_bf16 a[64:67], v[84:87], v[60:63], a[64:67]\n" \
"ds_read_b64_tr_b16 v[172:173], v38 offset:40960\n" \
"s_and_b32 s41, s44, 2\n" \
"v_mfma_f32_16x16x32_bf16 a[96:99], v[84:87], v[64:67], a[96:99]\n" \
"v_cndmask_b32_e32 v219, 0, v223, vcc\n" \
"v_cmp_gt_u32_e32 vcc, v224, v221\n" \
"s_waitcnt lgkmcnt(15)\n" \
"v_mfma_f32_16x16x32_bf16 a[128:131], v[84:87], v[68:71], a[128:131]\n" \
"ds_read_b64_tr_b16 v[174:175], v38 offset:41984\n" \
"s_and_b32 s44, s44, 4\n" \
"v_mfma_f32_16x16x32_bf16 a[160:163], v[84:87], v[72:75], a[160:163]\n" \
"s_lshr_b32 s49, s2, 3\n" \
"v_cndmask_b32_e64 v220, v220, 0, vcc\n" \
"v_mfma_f32_16x16x32_bf16 a[132:135], v[88:91], v[68:71], a[132:135]\n" \
"ds_read_b64_tr_b16 v[176:177], v39 offset:40960\n" \
"v_cmp_lt_u32_e64 vcc, v224, v221\n" \
"v_mfma_f32_16x16x32_bf16 a[164:167], v[88:91], v[72:75], a[164:167]\n" \
"s_min_u32 s50, s3, 2\n" \
"v_or_b32_e32 v12, v214, v215\n" \
"v_mfma_f32_16x16x32_bf16 a[136:139], v[92:95], v[68:71], a[136:139]\n" \
"ds_read_b64_tr_b16 v[178:179], v39 offset:41984\n" \
"v_cndmask_b32_e64 v221, 0, v223, vcc\n" \
"v_mfma_f32_16x16x32_bf16 a[168:171], v[92:95], v[72:75], a[168:171]\n" \
"v_lshlrev_b32_e64 v223, 7, v212\n" \
"v_lshl_or_b32 v10, s40, 13, v223\n" \
"v_mfma_f32_16x16x32_bf16 a[140:143], v[96:99], v[68:71], a[140:143]\n" \
"v_mul_u32_u24_e32 v223, 0xe39, v212\n" \
"v_lshl_add_u64 v[16:17], s[6:7], 0, v[10:11]\n" \
"v_mfma_f32_16x16x32_bf16 a[172:175], v[96:99], v[72:75], a[172:175]\n" \
"ds_read_b128 v[124:127], v29 offset:53248\n" \
"v_lshrrev_b32_e64 v223, 16, v223\n" \
"v_mfma_f32_16x16x32_bf16 a[144:147], v[100:103], v[68:71], a[144:147]\n" \
"s_movk_i32 s7, 0xffee\n" \
"s_lshl_b32 s6, s2, 5\n" \
"v_mfma_f32_16x16x32_bf16 a[176:179], v[100:103], v[72:75], a[176:179]\n" \
"ds_read_b128 v[128:131], v29 offset:55296\n" \
"v_mad_i32_i24 v224, v223, s7, v212\n" \
"v_mfma_f32_16x16x32_bf16 a[148:151], v[104:107], v[68:71], a[148:151]\n" \
"s_add_i32 s6, s6, 32\n" \
"v_lshlrev_b32_e32 v225, 4, v224\n" \
"v_mfma_f32_16x16x32_bf16 a[180:183], v[104:107], v[72:75], a[180:183]\n" \
"ds_read_b128 v[132:135], v29 offset:32768\n" \
"v_add_u32_e64 v226, s6, v225\n" \
"v_mfma_f32_16x16x32_bf16 a[152:155], v[108:111], v[68:71], a[152:155]\n" \
"v_and_b32_e32 v226, 0xf0, v226\n" \
"v_cmp_gt_u32_e64 vcc, 16, v224\n" \
"v_mfma_f32_16x16x32_bf16 a[184:187], v[108:111], v[72:75], a[184:187]\n" \
"ds_read_b128 v[136:139], v29 offset:34816\n" \
"s_movk_i32 s40, 0xf400\n" \
"v_mfma_f32_16x16x32_bf16 a[156:159], v[112:115], v[68:71], a[156:159]\n" \
"v_or_b32_e32 v13, v216, v217\n" \
"v_cndmask_b32_e64 v224, v225, v226, vcc\n" \
"v_mfma_f32_16x16x32_bf16 a[188:191], v[112:115], v[72:75], a[188:191]\n" \
"ds_read_b128 v[140:143], v29 offset:36864\n" \
"v_or_b32_e64 v225, 64, v212\n" \
"v_mfma_f32_16x16x32_bf16 a[220:223], v[112:115], v[76:79], a[220:223]\n" \
"v_mul_u32_u24_e32 v226, 0xe39, v225\n" \
"v_lshrrev_b32_e64 v226, 16, v226\n" \
"v_mfma_f32_16x16x32_bf16 a[252:255], v[112:115], v[80:83], a[252:255]\n" \
"ds_read_b128 v[144:147], v29 offset:38912\n" \
"v_mad_i32_i24 v225, v226, s7, v225\n" \
"v_mfma_f32_16x16x32_bf16 a[216:219], v[108:111], v[76:79], a[216:219]\n" \
"v_lshlrev_b32_e32 v227, 4, v225\n" \
"v_add_u32_e32 v228, s6, v227\n" \
"v_mfma_f32_16x16x32_bf16 a[248:251], v[108:111], v[80:83], a[248:251]\n" \
"v_and_b32_e32 v228, 0xf0, v228\n" \
"v_cmp_gt_u32_e64 vcc, 16, v225\n" \
"v_mfma_f32_16x16x32_bf16 a[212:215], v[104:107], v[76:79], a[212:215]\n" \
"v_mul_u32_u24_e32 v226, 0x480, v226\n" \
"v_or_b32_e64 v14, v218, v219\n" \
"v_mfma_f32_16x16x32_bf16 a[244:247], v[104:107], v[80:83], a[244:247]\n" \
"v_cndmask_b32_e64 v225, v227, v228, vcc\n" \
"v_or_b32_e32 v227, 0x80, v212\n" \
"v_mfma_f32_16x16x32_bf16 a[208:211], v[100:103], v[76:79], a[208:211]\n" \
"v_mul_u32_u24_e32 v228, 0xe39, v227\n" \
"v_lshrrev_b32_e64 v228, 16, v228\n" \
"v_mfma_f32_16x16x32_bf16 a[240:243], v[100:103], v[80:83], a[240:243]\n" \
"v_mad_i32_i24 v227, v228, s7, v227\n" \
"v_lshlrev_b32_e64 v229, 4, v227\n" \
"v_mfma_f32_16x16x32_bf16 a[204:207], v[96:99], v[76:79], a[204:207]\n" \
"v_add_u32_e64 v230, s6, v229\n" \
"v_and_b32_e32 v230, 0xf0, v230\n" \
"v_mfma_f32_16x16x32_bf16 a[236:239], v[96:99], v[80:83], a[236:239]\n" \
"v_cmp_gt_u32_e32 vcc, 16, v227\n" \
"v_or_b32_e32 v15, v220, v221\n" \
"v_mfma_f32_16x16x32_bf16 a[200:203], v[92:95], v[76:79], a[200:203]\n" \
"v_cndmask_b32_e64 v227, v229, v230, vcc\n" \
"v_or_b32_e32 v229, 0xc0, v212\n" \
"v_mfma_f32_16x16x32_bf16 a[232:235], v[92:95], v[80:83], a[232:235]\n" \
"v_mul_u32_u24_e32 v230, 0xe39, v229\n" \
"v_lshrrev_b32_e64 v230, 16, v230\n" \
"v_mfma_f32_16x16x32_bf16 a[196:199], v[88:91], v[76:79], a[196:199]\n" \
"v_mad_i32_i24 v229, v230, s7, v229\n" \
"v_lshlrev_b32_e64 v231, 4, v229\n" \
"v_mfma_f32_16x16x32_bf16 a[228:231], v[88:91], v[80:83], a[228:231]\n" \
"v_add_u32_e64 v232, s6, v231\n" \
"v_and_b32_e32 v232, 0xf0, v232\n" \
"v_mfma_f32_16x16x32_bf16 a[192:195], v[84:87], v[76:79], a[192:195]\n" \
"v_cmp_gt_u32_e32 vcc, 16, v229\n" \
"s_nop 1\n" \
"v_cndmask_b32_e64 v229, v231, v232, vcc\n" \
"v_mfma_f32_16x16x32_bf16 a[224:227], v[84:87], v[80:83], a[224:227]\n" \
"v_readfirstlane_b32 s62, v180\n" \
"s_nop 3\n" \
"s_add_u32 s62, s62, 0x9E3779B1\n" \
"s_waitcnt vmcnt(2) lgkmcnt(0)\n" \
"s_barrier\n" \
"v_mfma_f32_16x16x32_bf16 a[0:3], v[148:151], v[116:119], a[0:3]\n" \
"s_add_u32 m0, s30, 0x8000\n" \
"s_mov_b32 s85, 0x180\n" \
"v_mfma_f32_16x16x32_bf16 a[32:35], v[148:151], v[120:123], a[32:35]\n" \
"buffer_load_dwordx4 v22, s[64:67], s85 offen lds\n" \
"v_mfma_f32_16x16x32_bf16 a[4:7], v[152:155], v[116:119], a[4:7]\n" \
"ds_read_b64_tr_b16 v[84:85], v32 offset:0\n" \
"v_or_b32_e32 v231, 0x100, v212\n" \
"v_mfma_f32_16x16x32_bf16 a[36:39], v[152:155], v[120:123], a[36:39]\n" \
"ds_read_b64_tr_b16 v[86:87], v32 offset:1024\n" \
"v_mul_u32_u24_e32 v232, 0xe39, v231\n" \
"v_mfma_f32_16x16x32_bf16 a[8:11], v[156:159], v[116:119], a[8:11]\n" \
"s_add_u32 m0, s30, 0x8400\n" \
"s_mov_b32 s84, 0x2180\n" \
"v_mfma_f32_16x16x32_bf16 a[40:43], v[156:159], v[120:123], a[40:43]\n" \
"buffer_load_dwordx4 v23, s[64:67], s84 offen lds\n" \
"v_mfma_f32_16x16x32_bf16 a[12:15], v[160:163], v[116:119], a[12:15]\n" \
"ds_read_b128 v[52:55], v28 offset:16384\n" \
"v_lshrrev_b32_e64 v232, 16, v232\n" \
"v_mfma_f32_16x16x32_bf16 a[44:47], v[160:163], v[120:123], a[44:47]\n" \
"ds_read_b128 v[56:59], v28 offset:18432\n" \
"v_mad_i32_i24 v231, v232, s7, v231\n" \
"v_mfma_f32_16x16x32_bf16 a[16:19], v[164:167], v[116:119], a[16:19]\n" \
"s_add_u32 m0, s31, 0x8000\n" \
"s_mov_b32 s85, 0x180000\n" \
"v_mfma_f32_16x16x32_bf16 a[48:51], v[164:167], v[120:123], a[48:51]\n" \
"buffer_load_dwordx4 v26, s[68:71], s85 offen lds\n" \
"v_mfma_f32_16x16x32_bf16 a[20:23], v[168:171], v[116:119], a[20:23]\n" \
"ds_read_b64_tr_b16 v[88:89], v33 offset:0\n" \
"v_lshlrev_b32_e64 v233, 4, v231\n" \
"v_mfma_f32_16x16x32_bf16 a[52:55], v[168:171], v[120:123], a[52:55]\n" \
"ds_read_b64_tr_b16 v[90:91], v33 offset:1024\n" \
"v_add_u32_e64 v234, s6, v233\n" \
"v_mfma_f32_16x16x32_bf16 a[24:27], v[172:175], v[116:119], a[24:27]\n" \
"s_add_u32 m0, s31, 0xc000\n" \
"s_mov_b32 s84, 0x180000\n" \
"v_mfma_f32_16x16x32_bf16 a[56:59], v[172:175], v[120:123], a[56:59]\n" \
"buffer_load_dwordx4 v26, s[72:75], s84 offen lds\n" \
"v_mfma_f32_16x16x32_bf16 a[28:31], v[176:179], v[116:119], a[28:31]\n" \
"ds_read_b64_tr_b16 v[92:93], v34 offset:0\n" \
"v_and_b32_e32 v234, 0xf0, v234\n" \
"v_mfma_f32_16x16x32_bf16 a[60:63], v[176:179], v[120:123], a[60:63]\n" \
"ds_read_b64_tr_b16 v[94:95], v34 offset:1024\n" \
"v_cmp_gt_u32_e64 vcc, 16, v231\n" \
"v_mfma_f32_16x16x32_bf16 a[92:95], v[176:179], v[124:127], a[92:95]\n" \
"s_add_u32 m0, s30, 0x8800\n" \
"s_mov_b32 s85, 0x4180\n" \
"v_mfma_f32_16x16x32_bf16 a[124:127], v[176:179], v[128:131], a[124:127]\n" \
"buffer_load_dwordx4 v22, s[64:67], s85 offen lds\n" \
"v_mfma_f32_16x16x32_bf16 a[88:91], v[172:175], v[124:127], a[88:91]\n" \
"ds_read_b64_tr_b16 v[96:97], v35 offset:0\n" \
"v_cndmask_b32_e64 v231, v233, v234, vcc\n" \
"v_mfma_f32_16x16x32_bf16 a[120:123], v[172:175], v[128:131], a[120:123]\n" \
"ds_read_b64_tr_b16 v[98:99], v35 offset:1024\n" \
"v_or_b32_e32 v233, 0x140, v212\n" \
"v_mfma_f32_16x16x32_bf16 a[84:87], v[168:171], v[124:127], a[84:87]\n" \
"s_add_u32 m0, s30, 0x8c00\n" \
"s_mov_b32 s84, 0x6180\n" \
"v_mfma_f32_16x16x32_bf16 a[116:119], v[168:171], v[128:131], a[116:119]\n" \
"buffer_load_dwordx4 v23, s[64:67], s84 offen lds\n" \
"v_mfma_f32_16x16x32_bf16 a[80:83], v[164:167], v[124:127], a[80:83]\n" \
"ds_read_b64_tr_b16 v[100:101], v36 offset:0\n" \
"v_mul_u32_u24_e32 v234, 0xe39, v233\n" \
"v_mfma_f32_16x16x32_bf16 a[112:115], v[164:167], v[128:131], a[112:115]\n" \
"ds_read_b64_tr_b16 v[102:103], v36 offset:1024\n" \
"v_lshrrev_b32_e64 v234, 16, v234\n" \
"v_mfma_f32_16x16x32_bf16 a[76:79], v[160:163], v[124:127], a[76:79]\n" \
"s_add_u32 m0, s31, 0x8400\n" \
"s_mov_b32 s85, 0x188000\n" \
"v_mfma_f32_16x16x32_bf16 a[108:111], v[160:163], v[128:131], a[108:111]\n" \
"buffer_load_dwordx4 v26, s[68:71], s85 offen lds\n" \
"v_mfma_f32_16x16x32_bf16 a[72:75], v[156:159], v[124:127], a[72:75]\n" \
"ds_read_b64_tr_b16 v[104:105], v37 offset:0\n" \
"v_mad_i32_i24 v233, v234, s7, v233\n" \
"v_mfma_f32_16x16x32_bf16 a[104:107], v[156:159], v[128:131], a[104:107]\n" \
"v_lshlrev_b32_e32 v235, 4, v233\n" \
"v_add_u32_e32 v236, s6, v235\n" \
"v_mfma_f32_16x16x32_bf16 a[68:71], v[152:155], v[124:127], a[68:71]\n" \
"s_add_u32 m0, s31, 0xc400\n" \
"s_mov_b32 s84, 0x188000\n" \
"v_mfma_f32_16x16x32_bf16 a[100:103], v[152:155], v[128:131], a[100:103]\n" \
"buffer_load_dwordx4 v26, s[72:75], s84 offen lds\n" \
"v_mfma_f32_16x16x32_bf16 a[64:67], v[148:151], v[124:127], a[64:67]\n" \
"ds_read_b64_tr_b16 v[106:107], v37 offset:1024\n" \
"v_and_b32_e32 v236, 0xf0, v236\n" \
"v_mfma_f32_16x16x32_bf16 a[96:99], v[148:151], v[128:131], a[96:99]\n" \
"ds_read_b64_tr_b16 v[108:109], v38 offset:0\n" \
"v_cmp_gt_u32_e64 vcc, 16, v233\n" \
"v_mfma_f32_16x16x32_bf16 a[128:131], v[148:151], v[132:135], a[128:131]\n" \
"s_add_u32 m0, s30, 0x9000\n" \
"s_mov_b32 s85, 0x8180\n" \
"v_mfma_f32_16x16x32_bf16 a[160:163], v[148:151], v[136:139], a[160:163]\n" \
"buffer_load_dwordx4 v22, s[64:67], s85 offen lds\n" \
"v_mfma_f32_16x16x32_bf16 a[132:135], v[152:155], v[132:135], a[132:135]\n" \
"ds_read_b64_tr_b16 v[110:111], v38 offset:1024\n" \
"v_cndmask_b32_e64 v233, v235, v236, vcc\n" \
"v_mfma_f32_16x16x32_bf16 a[164:167], v[152:155], v[136:139], a[164:167]\n" \
"ds_read_b64_tr_b16 v[112:113], v39 offset:0\n" \
"v_or_b32_e32 v235, 0x180, v212\n" \
"v_mfma_f32_16x16x32_bf16 a[136:139], v[156:159], v[132:135], a[136:139]\n" \
"s_add_u32 m0, s30, 0x9400\n" \
"s_mov_b32 s84, 0xa180\n" \
"v_mfma_f32_16x16x32_bf16 a[168:171], v[156:159], v[136:139], a[168:171]\n" \
"buffer_load_dwordx4 v23, s[64:67], s84 offen lds\n" \
"v_mfma_f32_16x16x32_bf16 a[140:143], v[160:163], v[132:135], a[140:143]\n" \
"ds_read_b64_tr_b16 v[114:115], v39 offset:1024\n" \
"v_mul_u32_u24_e32 v236, 0xe39, v235\n" \
"v_mfma_f32_16x16x32_bf16 a[172:175], v[160:163], v[136:139], a[172:175]\n" \
"ds_read_b128 v[60:63], v28 offset:20480\n" \
"v_lshrrev_b32_e64 v236, 16, v236\n" \
"v_mfma_f32_16x16x32_bf16 a[144:147], v[164:167], v[132:135], a[144:147]\n" \
"s_add_u32 m0, s31, 0x8800\n" \
"s_mov_b32 s85, 0x190000\n" \
"v_mfma_f32_16x16x32_bf16 a[176:179], v[164:167], v[136:139], a[176:179]\n" \
"buffer_load_dwordx4 v27, s[68:71], s85 offen lds\n" \
"v_mfma_f32_16x16x32_bf16 a[148:151], v[168:171], v[132:135], a[148:151]\n" \
"ds_read_b128 v[64:67], v28 offset:22528\n" \
"v_mad_i32_i24 v235, v236, s7, v235\n" \
"v_mfma_f32_16x16x32_bf16 a[180:183], v[168:171], v[136:139], a[180:183]\n" \
"ds_read_b128 v[68:71], v28 offset:0\n" \
"v_lshlrev_b32_e64 v237, 4, v235\n" \
"v_mfma_f32_16x16x32_bf16 a[152:155], v[172:175], v[132:135], a[152:155]\n" \
"s_add_u32 m0, s31, 0xc800\n" \
"s_mov_b32 s84, 0x190000\n" \
"v_mfma_f32_16x16x32_bf16 a[184:187], v[172:175], v[136:139], a[184:187]\n" \
"buffer_load_dwordx4 v27, s[72:75], s84 offen lds\n" \
"v_mfma_f32_16x16x32_bf16 a[156:159], v[176:179], v[132:135], a[156:159]\n" \
"ds_read_b128 v[72:75], v28 offset:2048\n" \
"v_add_u32_e64 v238, s6, v237\n" \
"v_mfma_f32_16x16x32_bf16 a[188:191], v[176:179], v[136:139], a[188:191]\n" \
"ds_read_b128 v[76:79], v28 offset:4096\n" \
"v_and_b32_e32 v238, 0xf0, v238\n" \
"v_mfma_f32_16x16x32_bf16 a[220:223], v[176:179], v[140:143], a[220:223]\n" \
"v_cmp_gt_u32_e32 vcc, 16, v235\n" \
"s_nop 1\n" \
"v_cndmask_b32_e64 v235, v237, v238, vcc\n" \
"v_mfma_f32_16x16x32_bf16 a[252:255], v[176:179], v[144:147], a[252:255]\n" \
"ds_read_b128 v[80:83], v28 offset:6144\n" \
"v_or_b32_e32 v237, 0x1c0, v212\n" \
"v_mfma_f32_16x16x32_bf16 a[216:219], v[172:175], v[140:143], a[216:219]\n" \
"v_mul_u32_u24_e32 v238, 0xe39, v237\n" \
"v_lshrrev_b32_e64 v238, 16, v238\n" \
"v_mfma_f32_16x16x32_bf16 a[248:251], v[172:175], v[144:147], a[248:251]\n" \
"v_mad_i32_i24 v237, v238, s7, v237\n" \
"v_lshlrev_b32_e64 v239, 4, v237\n" \
"v_mfma_f32_16x16x32_bf16 a[212:215], v[168:171], v[140:143], a[212:215]\n" \
"v_add_u32_e64 v240, s6, v239\n" \
"v_and_b32_e32 v240, 0xf0, v240\n" \
"v_mfma_f32_16x16x32_bf16 a[244:247], v[168:171], v[144:147], a[244:247]\n" \
"v_cmp_gt_u32_e32 vcc, 16, v237\n" \
"s_nop 1\n" \
"v_cndmask_b32_e64 v237, v239, v240, vcc\n" \
"v_mfma_f32_16x16x32_bf16 a[208:211], v[164:167], v[140:143], a[208:211]\n" \
"v_or_b32_e32 v239, 0x200, v212\n" \
"v_mul_u32_u24_e32 v240, 0xe39, v239\n" \
"v_mfma_f32_16x16x32_bf16 a[240:243], v[164:167], v[144:147], a[240:243]\n" \
"v_lshrrev_b32_e64 v240, 16, v240\n" \
"v_mad_i32_i24 v239, v240, s7, v239\n" \
"v_mfma_f32_16x16x32_bf16 a[204:207], v[160:163], v[140:143], a[204:207]\n" \
"v_lshlrev_b32_e32 v241, 4, v239\n" \
"v_add_u32_e32 v242, s6, v241\n" \
"v_mfma_f32_16x16x32_bf16 a[236:239], v[160:163], v[144:147], a[236:239]\n" \
"s_movk_i32 s6, 0xfc00\n" \
"v_add3_u32 v1, v226, v225, s6\n" \
"v_mfma_f32_16x16x32_bf16 a[200:203], v[156:159], v[140:143], a[200:203]\n" \
"v_mul_u32_u24_e32 v225, 0x480, v228\n" \
"s_movk_i32 s7, 0xf800\n" \
"v_mfma_f32_16x16x32_bf16 a[232:235], v[156:159], v[144:147], a[232:235]\n" \
"v_add3_u32 v2, v225, v227, s7\n" \
"v_mul_u32_u24_e32 v225, 0x480, v230\n" \
"v_mfma_f32_16x16x32_bf16 a[196:199], v[152:155], v[140:143], a[196:199]\n" \
"v_add3_u32 v3, v225, v229, s40\n" \
"v_mul_u32_u24_e32 v225, 0x480, v234\n" \
"v_mfma_f32_16x16x32_bf16 a[228:231], v[152:155], v[144:147], a[228:231]\n" \
"v_add3_u32 v5, v225, v233, s6\n" \
"v_mul_u32_u24_e32 v225, 0x480, v236\n" \
"v_mfma_f32_16x16x32_bf16 a[192:195], v[148:151], v[140:143], a[192:195]\n" \
"v_add3_u32 v6, v225, v235, s7\n" \
"v_mul_u32_u24_e32 v225, 0x480, v238\n" \
"v_mfma_f32_16x16x32_bf16 a[224:227], v[148:151], v[144:147], a[224:227]\n" \
"s_waitcnt lgkmcnt(15)\n" \
"s_nop 0\n" \
"v_mfma_f32_16x16x32_bf16 a[0:3], v[84:87], v[52:55], a[0:3]\n" \
"s_add_u32 m0, s30, 0x9800\n" \
"s_mov_b32 s85, 0xc180\n" \
"v_mfma_f32_16x16x32_bf16 a[32:35], v[84:87], v[56:59], a[32:35]\n" \
"buffer_load_dwordx4 v22, s[64:67], s85 offen lds\n" \
"v_mfma_f32_16x16x32_bf16 a[4:7], v[88:91], v[52:55], a[4:7]\n" \
"ds_read_b64_tr_b16 v[148:149], v32 offset:8192\n" \
"v_add3_u32 v7, v225, v237, s40\n" \
"v_mfma_f32_16x16x32_bf16 a[36:39], v[88:91], v[56:59], a[36:39]\n" \
"s_add_u32 m0, s30, 0x9c00\n" \
"s_mov_b32 s84, 0xe180\n" \
"v_mfma_f32_16x16x32_bf16 a[8:11], v[92:95], v[52:55], a[8:11]\n" \
"buffer_load_dwordx4 v23, s[64:67], s84 offen lds\n" \
"v_mfma_f32_16x16x32_bf16 a[40:43], v[92:95], v[56:59], a[40:43]\n" \
"ds_read_b64_tr_b16 v[150:151], v32 offset:9216\n" \
"s_and_b32 s40, s2, 7\n" \
"s_waitcnt lgkmcnt(15)\n" \
"v_mfma_f32_16x16x32_bf16 a[12:15], v[96:99], v[52:55], a[12:15]\n" \
"s_add_u32 m0, s31, 0x8c00\n" \
"s_mov_b32 s85, 0x198000\n" \
"v_mfma_f32_16x16x32_bf16 a[44:47], v[96:99], v[56:59], a[44:47]\n" \
"buffer_load_dwordx4 v27, s[68:71], s85 offen lds\n" \
"s_waitcnt lgkmcnt(14)\n" \
"v_mfma_f32_16x16x32_bf16 a[16:19], v[100:103], v[52:55], a[16:19]\n" \
"ds_read_b128 v[116:119], v29 offset:16384\n" \
"v_and_b32_e32 v242, 0xf0, v242\n" \
"v_mfma_f32_16x16x32_bf16 a[48:51], v[100:103], v[56:59], a[48:51]\n" \
"s_add_u32 m0, s31, 0xcc00\n" \
"s_mov_b32 s84, 0x198000\n" \
"s_waitcnt lgkmcnt(13)\n" \
"v_mfma_f32_16x16x32_bf16 a[20:23], v[104:107], v[52:55], a[20:23]\n" \
"buffer_load_dwordx4 v27, s[72:75], s84 offen lds\n" \
"v_mfma_f32_16x16x32_bf16 a[52:55], v[104:107], v[56:59], a[52:55]\n" \
"ds_read_b128 v[120:123], v29 offset:18432\n" \
"v_cmp_gt_u32_e32 vcc, 16, v239\n" \
"s_waitcnt lgkmcnt(12)\n" \
"v_mfma_f32_16x16x32_bf16 a[24:27], v[108:111], v[52:55], a[24:27]\n" \
"ds_read_b64_tr_b16 v[152:153], v33 offset:8192\n" \
"s_lshl_b32 s6, s3, 3\n" \
"v_mfma_f32_16x16x32_bf16 a[56:59], v[108:111], v[56:59], a[56:59]\n" \
"ds_read_b64_tr_b16 v[154:155], v33 offset:9216\n" \
"v_and_b32_e32 v225, 32, v20\n" \
"s_waitcnt lgkmcnt(12)\n" \
"v_mfma_f32_16x16x32_bf16 a[28:31], v[112:115], v[52:55], a[28:31]\n" \
"ds_read_b64_tr_b16 v[156:157], v34 offset:8192\n" \
"s_lshl_b32 s7, s40, 5\n" \
"v_mfma_f32_16x16x32_bf16 a[60:63], v[112:115], v[56:59], a[60:63]\n" \
"ds_read_b64_tr_b16 v[158:159], v34 offset:9216\n" \
"v_cndmask_b32_e32 v239, v241, v242, vcc\n" \
"s_waitcnt lgkmcnt(13)\n" \
"v_mfma_f32_16x16x32_bf16 a[92:95], v[112:115], v[60:63], a[92:95]\n" \
"s_and_b32 s6, s6, 16\n" \
"v_mov_b32_e32 v226, 0x100\n" \
"s_waitcnt lgkmcnt(12)\n" \
"v_mfma_f32_16x16x32_bf16 a[124:127], v[112:115], v[64:67], a[124:127]\n" \
"buffer_load_dwordx4 v[200:203], v192, s[76:79], s34 offen offset:0\n" \
"v_mfma_f32_16x16x32_bf16 a[88:91], v[108:111], v[60:63], a[88:91]\n" \
"s_mov_b32 m0, s32\n" \
"ds_read_b64_tr_b16 v[160:161], v35 offset:8192\n" \
"v_mfma_f32_16x16x32_bf16 a[120:123], v[108:111], v[64:67], a[120:123]\n" \
"s_mov_b32 exec_lo, 0x0\n" \
"s_mov_b32 exec_hi, 0xff00\n" \
"global_load_lds_dword v[184:185], off\n" \
"s_mov_b64 exec, -1\n" \
"v_mfma_f32_16x16x32_bf16 a[84:87], v[104:107], v[60:63], a[84:87]\n" \
"ds_read_b64_tr_b16 v[162:163], v35 offset:9216\n" \
"v_mov_b32_e32 v227, s7\n" \
"v_mfma_f32_16x16x32_bf16 a[116:119], v[104:107], v[64:67], a[116:119]\n" \
"buffer_load_dwordx4 v[204:207], v193, s[76:79], s34 offen offset:0\n" \
"v_mfma_f32_16x16x32_bf16 a[80:83], v[100:103], v[60:63], a[80:83]\n" \
"ds_read_b64_tr_b16 v[164:165], v36 offset:8192\n" \
"v_cmp_eq_u32_e64 vcc, 0, v225\n" \
"v_mfma_f32_16x16x32_bf16 a[112:115], v[100:103], v[64:67], a[112:115]\n" \
"ds_read_b64_tr_b16 v[166:167], v36 offset:9216\n" \
"s_or_b32 s41, s41, s6\n" \
"v_mfma_f32_16x16x32_bf16 a[76:79], v[96:99], v[60:63], a[76:79]\n" \
"s_lshr_b32 s6, s2, 5\n" \
"v_cndmask_b32_e64 v225, v226, v227, vcc\n" \
"v_mfma_f32_16x16x32_bf16 a[108:111], v[96:99], v[64:67], a[108:111]\n" \
"v_bfe_u32 v226, v20, 7, 1\n" \
"v_or3_b32 v222, s41, v226, v222\n" \
"v_mfma_f32_16x16x32_bf16 a[72:75], v[92:95], v[60:63], a[72:75]\n" \
"buffer_load_dword v208, v194, s[76:79], s34 offen offset:32\n" \
"v_mfma_f32_16x16x32_bf16 a[104:107], v[92:95], v[64:67], a[104:107]\n" \
"ds_read_b64_tr_b16 v[168:169], v37 offset:8192\n" \
"v_mul_u32_u24_e64 v222, 18, v222\n" \
"v_mfma_f32_16x16x32_bf16 a[68:71], v[88:91], v[60:63], a[68:71]\n" \
"ds_read_b64_tr_b16 v[170:171], v37 offset:9216\n" \
"s_movk_i32 s41, 0x480\n" \
"v_mfma_f32_16x16x32_bf16 a[100:103], v[88:91], v[64:67], a[100:103]\n" \
"v_and_or_b32 v222, v213, 1, v222\n" \
"v_mad_u32_u24 v0, v223, s41, v224\n" \
"v_mfma_f32_16x16x32_bf16 a[64:67], v[84:87], v[60:63], a[64:67]\n" \
"buffer_load_dword v209, v194, s[76:79], s34 offen offset:1184\n" \
"v_mfma_f32_16x16x32_bf16 a[96:99], v[84:87], v[64:67], a[96:99]\n" \
"ds_read_b64_tr_b16 v[172:173], v38 offset:8192\n" \
"v_mad_u32_u24 v4, v232, s41, v231\n" \
"s_waitcnt lgkmcnt(15)\n" \
"v_mfma_f32_16x16x32_bf16 a[128:131], v[84:87], v[68:71], a[128:131]\n" \
"ds_read_b64_tr_b16 v[174:175], v38 offset:9216\n" \
"v_mad_u32_u24 v8, v240, s41, v239\n" \
"v_mfma_f32_16x16x32_bf16 a[160:163], v[84:87], v[72:75], a[160:163]\n" \
"v_mad_u32_u24 v222, v222, s41, v225\n" \
"s_mul_i32 s41, s6, 0xa00\n" \
"v_mfma_f32_16x16x32_bf16 a[132:135], v[88:91], v[68:71], a[132:135]\n" \
"buffer_load_dword v211, v194, s[76:79], s34 offen offset:2336\n" \
"v_mfma_f32_16x16x32_bf16 a[164:167], v[88:91], v[72:75], a[164:167]\n" \
"ds_read_b64_tr_b16 v[176:177], v39 offset:8192\n" \
"s_add_i32 s42, s41, 0x5bc1000\n" \
"v_mfma_f32_16x16x32_bf16 a[136:139], v[92:95], v[68:71], a[136:139]\n" \
"ds_read_b64_tr_b16 v[178:179], v39 offset:9216\n" \
"s_lshl_b32 s41, s40, 2\n" \
"v_mfma_f32_16x16x32_bf16 a[168:171], v[92:95], v[72:75], a[168:171]\n" \
"s_or_b32 s46, s41, s43\n" \
"s_lshl_b32 s45, s46, 2\n" \
"v_mfma_f32_16x16x32_bf16 a[140:143], v[96:99], v[68:71], a[140:143]\n" \
"s_or_b32 s45, s45, s42\n" \
"v_writelane_b32 v11, s45, 0\n" \
"v_mfma_f32_16x16x32_bf16 a[172:175], v[96:99], v[72:75], a[172:175]\n" \
"s_mov_b32 s76, s20\n" \
"s_and_b32 s77, s21, 0xffff\n" \
"v_mfma_f32_16x16x32_bf16 a[144:147], v[100:103], v[68:71], a[144:147]\n" \
"s_mov_b32 s78, 0x480\n" \
"ds_read_b128 v[124:127], v29 offset:20480\n" \
"v_mfma_f32_16x16x32_bf16 a[176:179], v[100:103], v[72:75], a[176:179]\n" \
"buffer_load_ushort v210, v195, s[76:79], 0 offen\n" \
"v_mfma_f32_16x16x32_bf16 a[148:151], v[104:107], v[68:71], a[148:151]\n" \
"ds_read_b128 v[128:131], v29 offset:22528\n" \
"s_lshl_b32 s47, s6, 3\n" \
"v_mfma_f32_16x16x32_bf16 a[180:183], v[104:107], v[72:75], a[180:183]\n" \
"s_mov_b32 s76, s8\n" \
"s_and_b32 s77, s9, 0xffff\n" \
"v_mfma_f32_16x16x32_bf16 a[152:155], v[108:111], v[68:71], a[152:155]\n" \
"s_mov_b32 s78, 0x240000\n" \
"ds_read_b128 v[132:135], v29 offset:0\n" \
"v_mfma_f32_16x16x32_bf16 a[184:187], v[108:111], v[72:75], a[184:187]\n" \
"ds_read_b128 v[136:139], v29 offset:2048\n" \
"v_writelane_b32 v11, s42, 1\n" \
"v_mfma_f32_16x16x32_bf16 a[156:159], v[112:115], v[68:71], a[156:159]\n" \
"s_or_b32 s42, s47, s40\n" \
"s_lshl_b32 s42, s42, 10\n" \
"v_mfma_f32_16x16x32_bf16 a[188:191], v[112:115], v[72:75], a[188:191]\n" \
"ds_read_b128 v[140:143], v29 offset:4096\n" \
"s_lshl_b32 s45, s3, 8\n" \
"v_mfma_f32_16x16x32_bf16 a[220:223], v[112:115], v[76:79], a[220:223]\n" \
"s_or_b32 s42, s42, s45\n" \
"s_or_b32 s45, s42, 0x5c00200\n" \
"v_mfma_f32_16x16x32_bf16 a[252:255], v[112:115], v[80:83], a[252:255]\n" \
"ds_read_b128 v[144:147], v29 offset:6144\n" \
"v_writelane_b32 v11, s45, 2\n" \
"v_mfma_f32_16x16x32_bf16 a[216:219], v[108:111], v[76:79], a[216:219]\n" \
"s_lshl_b32 s45, s43, 2\n" \
"s_or_b32 s48, s45, s42\n" \
"v_mfma_f32_16x16x32_bf16 a[248:251], v[108:111], v[80:83], a[248:251]\n" \
"s_or_b32 s48, s48, 0x5c00000\n" \
"v_writelane_b32 v11, s48, 3\n" \
"v_mfma_f32_16x16x32_bf16 a[212:215], v[104:107], v[76:79], a[212:215]\n" \
"s_or_b32 s42, s42, 0x5c00080\n" \
"v_writelane_b32 v11, s42, 4\n" \
"v_mfma_f32_16x16x32_bf16 a[244:247], v[104:107], v[80:83], a[244:247]\n" \
"s_or_b32 s42, s42, s45\n" \
"s_add_i32 s45, s3, s2\n" \
"v_mfma_f32_16x16x32_bf16 a[208:211], v[100:103], v[76:79], a[208:211]\n" \
"v_writelane_b32 v11, s42, 5\n" \
"s_mov_b32 s42, 2.0\n" \
"v_mfma_f32_16x16x32_bf16 a[240:243], v[100:103], v[80:83], a[240:243]\n" \
"s_mov_b32 s48, 0x5bc0000\n" \
"v_mfma_f32_16x16x32_bf16 a[204:207], v[96:99], v[76:79], a[204:207]\n" \
"s_cmp_lg_u32 s45, 0\n" \
"s_cselect_b32 s45, s42, s48\n" \
"v_mfma_f32_16x16x32_bf16 a[236:239], v[96:99], v[80:83], a[236:239]\n" \
"s_or_b32 s48, s44, s4\n" \
"v_writelane_b32 v11, s45, 53\n" \
"v_mfma_f32_16x16x32_bf16 a[200:203], v[92:95], v[76:79], a[200:203]\n" \
"s_mul_i32 s45, s43, 0x120\n" \
"v_mfma_f32_16x16x32_bf16 a[232:235], v[92:95], v[80:83], a[232:235]\n" \
"s_add_u32 s44, s22, s45\n" \
"s_addc_u32 s51, s23, 0\n" \
"v_mfma_f32_16x16x32_bf16 a[196:199], v[88:91], v[76:79], a[196:199]\n" \
"v_writelane_b32 v11, s44, 9\n" \
"v_mfma_f32_16x16x32_bf16 a[228:231], v[88:91], v[80:83], a[228:231]\n" \
"s_add_u32 s52, s24, s45\n" \
"s_addc_u32 s53, s25, 0\n" \
"v_mfma_f32_16x16x32_bf16 a[192:195], v[84:87], v[76:79], a[192:195]\n" \
"s_and_b32 s56, s51, 0xffff\n" \
"v_writelane_b32 v11, s56, 10\n" \
"v_mfma_f32_16x16x32_bf16 a[224:227], v[84:87], v[80:83], a[224:227]\n" \
"s_waitcnt vmcnt(7) lgkmcnt(0)\n" \
"s_barrier\n" \
"v_mfma_f32_16x16x32_bf16 a[0:3], v[148:151], v[116:119], a[0:3]\n" \
"s_add_u32 m0, s30, 0x0\n" \
"s_mov_b32 s85, 0x200\n" \
"v_mfma_f32_16x16x32_bf16 a[32:35], v[148:151], v[120:123], a[32:35]\n" \
"buffer_load_dwordx4 v22, s[64:67], s85 offen lds\n" \
"v_mfma_f32_16x16x32_bf16 a[4:7], v[152:155], v[116:119], a[4:7]\n" \
"ds_read_b64_tr_b16 v[84:85], v32 offset:32768\n" \
"s_and_b32 s56, s53, 0xffff\n" \
"v_mfma_f32_16x16x32_bf16 a[36:39], v[152:155], v[120:123], a[36:39]\n" \
"ds_read_b64_tr_b16 v[86:87], v32 offset:33792\n" \
"v_writelane_b32 v11, s52, 11\n" \
"v_mfma_f32_16x16x32_bf16 a[8:11], v[156:159], v[116:119], a[8:11]\n" \
"s_add_u32 m0, s30, 0x400\n" \
"s_mov_b32 s84, 0x2200\n" \
"v_mfma_f32_16x16x32_bf16 a[40:43], v[156:159], v[120:123], a[40:43]\n" \
"buffer_load_dwordx4 v23, s[64:67], s84 offen lds\n" \
"v_mfma_f32_16x16x32_bf16 a[12:15], v[160:163], v[116:119], a[12:15]\n" \
"ds_read_b128 v[52:55], v28 offset:49152\n" \
"v_mfma_f32_16x16x32_bf16 a[44:47], v[160:163], v[120:123], a[44:47]\n" \
"ds_read_b128 v[56:59], v28 offset:51200\n" \
"v_mfma_f32_16x16x32_bf16 a[16:19], v[164:167], v[116:119], a[16:19]\n" \
"s_add_u32 m0, s31, 0x0\n" \
"s_nop 0\n" \
"s_mov_b32 s85, 0x200000\n" \
"v_mfma_f32_16x16x32_bf16 a[48:51], v[164:167], v[120:123], a[48:51]\n" \
"buffer_load_dwordx4 v26, s[68:71], s85 offen lds\n" \
"v_mfma_f32_16x16x32_bf16 a[20:23], v[168:171], v[116:119], a[20:23]\n" \
"ds_read_b64_tr_b16 v[88:89], v33 offset:32768\n" \
"v_mfma_f32_16x16x32_bf16 a[52:55], v[168:171], v[120:123], a[52:55]\n" \
"ds_read_b64_tr_b16 v[90:91], v33 offset:33792\n" \
"v_mfma_f32_16x16x32_bf16 a[24:27], v[172:175], v[116:119], a[24:27]\n" \
"s_add_u32 m0, s31, 0x4000\n" \
"s_mov_b32 s84, 0x200000\n" \
"v_mfma_f32_16x16x32_bf16 a[56:59], v[172:175], v[120:123], a[56:59]\n" \
"buffer_load_dwordx4 v26, s[72:75], s84 offen lds\n" \
"v_mfma_f32_16x16x32_bf16 a[28:31], v[176:179], v[116:119], a[28:31]\n" \
"ds_read_b64_tr_b16 v[92:93], v34 offset:32768\n" \
"v_mfma_f32_16x16x32_bf16 a[60:63], v[176:179], v[120:123], a[60:63]\n" \
"ds_read_b64_tr_b16 v[94:95], v34 offset:33792\n" \
"v_mfma_f32_16x16x32_bf16 a[92:95], v[176:179], v[124:127], a[92:95]\n" \
"s_add_u32 m0, s30, 0x800\n" \
"s_mov_b32 s85, 0x4200\n" \
"v_mfma_f32_16x16x32_bf16 a[124:127], v[176:179], v[128:131], a[124:127]\n" \
"buffer_load_dwordx4 v22, s[64:67], s85 offen lds\n" \
"v_mfma_f32_16x16x32_bf16 a[88:91], v[172:175], v[124:127], a[88:91]\n" \
"ds_read_b64_tr_b16 v[96:97], v35 offset:32768\n" \
"v_mfma_f32_16x16x32_bf16 a[120:123], v[172:175], v[128:131], a[120:123]\n" \
"ds_read_b64_tr_b16 v[98:99], v35 offset:33792\n" \
"v_mfma_f32_16x16x32_bf16 a[84:87], v[168:171], v[124:127], a[84:87]\n" \
"s_add_u32 m0, s30, 0xc00\n" \
"s_mov_b32 s84, 0x6200\n" \
"v_mfma_f32_16x16x32_bf16 a[116:119], v[168:171], v[128:131], a[116:119]\n" \
"buffer_load_dwordx4 v23, s[64:67], s84 offen lds\n" \
"v_mfma_f32_16x16x32_bf16 a[80:83], v[164:167], v[124:127], a[80:83]\n" \
"ds_read_b64_tr_b16 v[100:101], v36 offset:32768\n" \
"v_mfma_f32_16x16x32_bf16 a[112:115], v[164:167], v[128:131], a[112:115]\n" \
"ds_read_b64_tr_b16 v[102:103], v36 offset:33792\n" \
"v_mfma_f32_16x16x32_bf16 a[76:79], v[160:163], v[124:127], a[76:79]\n" \
"s_add_u32 m0, s31, 0x400\n" \
"s_mov_b32 s85, 0x208000\n" \
"v_mfma_f32_16x16x32_bf16 a[108:111], v[160:163], v[128:131], a[108:111]\n" \
"buffer_load_dwordx4 v26, s[68:71], s85 offen lds\n" \
"v_mfma_f32_16x16x32_bf16 a[72:75], v[156:159], v[124:127], a[72:75]\n" \
"ds_read_b64_tr_b16 v[104:105], v37 offset:32768\n" \
"v_mfma_f32_16x16x32_bf16 a[104:107], v[156:159], v[128:131], a[104:107]\n" \
"s_cmpk_lt_u32 s3, 3\n" \
"s_cselect_b32 s44, s44, s52\n" \
"v_mfma_f32_16x16x32_bf16 a[68:71], v[152:155], v[124:127], a[68:71]\n" \
"s_add_u32 m0, s31, 0x4400\n" \
"s_mov_b32 s84, 0x208000\n" \
"v_mfma_f32_16x16x32_bf16 a[100:103], v[152:155], v[128:131], a[100:103]\n" \
"buffer_load_dwordx4 v26, s[72:75], s84 offen lds\n" \
"v_mfma_f32_16x16x32_bf16 a[64:67], v[148:151], v[124:127], a[64:67]\n" \
"ds_read_b64_tr_b16 v[106:107], v37 offset:33792\n" \
"s_movk_i32 s52, 0xfff\n" \
"s_nop 0\n" \
"v_mfma_f32_16x16x32_bf16 a[96:99], v[148:151], v[128:131], a[96:99]\n" \
"ds_read_b64_tr_b16 v[108:109], v38 offset:32768\n" \
"v_writelane_b32 v11, s56, 12\n" \
"v_mfma_f32_16x16x32_bf16 a[128:131], v[148:151], v[132:135], a[128:131]\n" \
"s_add_u32 m0, s30, 0x1000\n" \
"s_mov_b32 s85, 0x8200\n" \
"v_mfma_f32_16x16x32_bf16 a[160:163], v[148:151], v[136:139], a[160:163]\n" \
"buffer_load_dwordx4 v22, s[64:67], s85 offen lds\n" \
"v_mfma_f32_16x16x32_bf16 a[132:135], v[152:155], v[132:135], a[132:135]\n" \
"ds_read_b64_tr_b16 v[110:111], v38 offset:33792\n" \
"s_and_b32 s56, s29, 0xffff\n" \
"v_mfma_f32_16x16x32_bf16 a[164:167], v[152:155], v[136:139], a[164:167]\n" \
"ds_read_b64_tr_b16 v[112:113], v39 offset:32768\n" \
"v_writelane_b32 v11, s28, 13\n" \
"v_mfma_f32_16x16x32_bf16 a[136:139], v[156:159], v[132:135], a[136:139]\n" \
"s_add_u32 m0, s30, 0x1400\n" \
"s_mov_b32 s84, 0xa200\n" \
"v_mfma_f32_16x16x32_bf16 a[168:171], v[156:159], v[136:139], a[168:171]\n" \
"buffer_load_dwordx4 v23, s[64:67], s84 offen lds\n" \
"v_mfma_f32_16x16x32_bf16 a[140:143], v[160:163], v[132:135], a[140:143]\n" \
"ds_read_b64_tr_b16 v[114:115], v39 offset:33792\n" \
"v_mul_u32_u24_e32 v213, 0x120, v213\n" \
"v_mfma_f32_16x16x32_bf16 a[172:175], v[160:163], v[136:139], a[172:175]\n" \
"ds_read_b128 v[60:63], v28 offset:53248\n" \
"v_writelane_b32 v11, s56, 14\n" \
"v_mfma_f32_16x16x32_bf16 a[144:147], v[164:167], v[132:135], a[144:147]\n" \
"s_add_u32 m0, s31, 0x800\n" \
"s_mov_b32 s85, 0x210000\n" \
"v_mfma_f32_16x16x32_bf16 a[176:179], v[164:167], v[136:139], a[176:179]\n" \
"buffer_load_dwordx4 v27, s[68:71], s85 offen lds\n" \
"v_mfma_f32_16x16x32_bf16 a[148:151], v[168:171], v[132:135], a[148:151]\n" \
"ds_read_b128 v[64:67], v28 offset:55296\n" \
"v_lshlrev_b32_e64 v212, 3, v212\n" \
"v_mfma_f32_16x16x32_bf16 a[180:183], v[168:171], v[136:139], a[180:183]\n" \
"ds_read_b128 v[68:71], v28 offset:32768\n" \
"v_writelane_b32 v11, s44, 15\n" \
"v_mfma_f32_16x16x32_bf16 a[152:155], v[172:175], v[132:135], a[152:155]\n" \
"s_add_u32 m0, s31, 0x4800\n" \
"s_mov_b32 s84, 0x210000\n" \
"v_mfma_f32_16x16x32_bf16 a[184:187], v[172:175], v[136:139], a[184:187]\n" \
"buffer_load_dwordx4 v27, s[72:75], s84 offen lds\n" \
"v_mfma_f32_16x16x32_bf16 a[156:159], v[176:179], v[132:135], a[156:159]\n" \
"ds_read_b128 v[72:75], v28 offset:34816\n" \
"v_mfma_f32_16x16x32_bf16 a[188:191], v[176:179], v[136:139], a[188:191]\n" \
"ds_read_b128 v[76:79], v28 offset:36864\n" \
"v_mfma_f32_16x16x32_bf16 a[220:223], v[176:179], v[140:143], a[220:223]\n" \
"s_cmpk_lt_u32 s3, 3\n" \
"s_cselect_b32 s44, s51, s53\n" \
"v_mfma_f32_16x16x32_bf16 a[252:255], v[176:179], v[144:147], a[252:255]\n" \
"ds_read_b128 v[80:83], v28 offset:38912\n" \
"s_and_b32 s44, s44, 0xffff\n" \
"v_mfma_f32_16x16x32_bf16 a[216:219], v[172:175], v[140:143], a[216:219]\n" \
"v_writelane_b32 v11, s44, 16\n" \
"s_mul_i32 s44, s40, 0x48\n" \
"v_mfma_f32_16x16x32_bf16 a[248:251], v[172:175], v[144:147], a[248:251]\n" \
"s_add_i32 s51, s44, 0x47\n" \
"v_mfma_f32_16x16x32_bf16 a[212:215], v[168:171], v[140:143], a[212:215]\n" \
"s_cmpk_lt_u32 s3, 3\n" \
"s_cselect_b32 s51, s51, s52\n" \
"v_mfma_f32_16x16x32_bf16 a[244:247], v[168:171], v[144:147], a[244:247]\n" \
"s_mulk_i32 s51, 0x480\n" \
"s_addk_i32 s51, 0x120\n" \
"v_mfma_f32_16x16x32_bf16 a[208:211], v[164:167], v[140:143], a[208:211]\n" \
"v_writelane_b32 v11, s51, 17\n" \
"s_lshl_b32 s51, s3, 5\n" \
"v_mfma_f32_16x16x32_bf16 a[240:243], v[164:167], v[144:147], a[240:243]\n" \
"s_add_i32 s44, s44, s51\n" \
"s_mul_i32 s51, s44, 0x480\n" \
"v_mfma_f32_16x16x32_bf16 a[204:207], v[160:163], v[140:143], a[204:207]\n" \
"s_mul_i32 s44, s46, 0x24000\n" \
"s_add_i32 s52, s44, 0x1b000\n" \
"v_mfma_f32_16x16x32_bf16 a[236:239], v[160:163], v[144:147], a[236:239]\n" \
"s_cmpk_lt_u32 s3, 3\n" \
"s_cselect_b32 s51, s51, s52\n" \
"v_mfma_f32_16x16x32_bf16 a[200:203], v[156:159], v[140:143], a[200:203]\n" \
"s_mul_i32 s52, s3, 0x9000\n" \
"v_writelane_b32 v11, s51, 18\n" \
"v_mfma_f32_16x16x32_bf16 a[232:235], v[156:159], v[144:147], a[232:235]\n" \
"s_mul_i32 s51, s50, 0x2400\n" \
"s_mul_i32 s50, s50, 0x9000\n" \
"v_mfma_f32_16x16x32_bf16 a[196:199], v[152:155], v[140:143], a[196:199]\n" \
"s_bitset1_b32 s51, 17\n" \
"v_writelane_b32 v11, s51, 19\n" \
"v_mfma_f32_16x16x32_bf16 a[228:231], v[152:155], v[144:147], a[228:231]\n" \
"s_add_i32 s50, s44, s50\n" \
"v_writelane_b32 v11, s50, 25\n" \
"v_mfma_f32_16x16x32_bf16 a[192:195], v[148:151], v[140:143], a[192:195]\n" \
"s_mul_i32 s50, s3, 0x2400\n" \
"s_add_i32 s51, s50, 0x6000\n" \
"v_mfma_f32_16x16x32_bf16 a[224:227], v[148:151], v[144:147], a[224:227]\n" \
"s_waitcnt lgkmcnt(15)\n" \
"s_nop 0\n" \
"v_mfma_f32_16x16x32_bf16 a[0:3], v[84:87], v[52:55], a[0:3]\n" \
"s_add_u32 m0, s30, 0x1800\n" \
"s_mov_b32 s85, 0xc200\n" \
"v_mfma_f32_16x16x32_bf16 a[32:35], v[84:87], v[56:59], a[32:35]\n" \
"buffer_load_dwordx4 v22, s[64:67], s85 offen lds\n" \
"v_mfma_f32_16x16x32_bf16 a[4:7], v[88:91], v[52:55], a[4:7]\n" \
"ds_read_b64_tr_b16 v[148:149], v32 offset:40960\n" \
"v_writelane_b32 v11, s51, 20\n" \
"v_mfma_f32_16x16x32_bf16 a[36:39], v[88:91], v[56:59], a[36:39]\n" \
"s_add_u32 m0, s30, 0x1c00\n" \
"s_mov_b32 s84, 0xe200\n" \
"v_mfma_f32_16x16x32_bf16 a[8:11], v[92:95], v[52:55], a[8:11]\n" \
"buffer_load_dwordx4 v23, s[64:67], s84 offen lds\n" \
"v_mfma_f32_16x16x32_bf16 a[40:43], v[92:95], v[56:59], a[40:43]\n" \
"ds_read_b64_tr_b16 v[150:151], v32 offset:41984\n" \
"s_or_b32 s51, s50, 0x10000\n" \
"s_waitcnt lgkmcnt(15)\n" \
"v_mfma_f32_16x16x32_bf16 a[12:15], v[96:99], v[52:55], a[12:15]\n" \
"s_add_u32 m0, s31, 0xc00\n" \
"s_mov_b32 s85, 0x218000\n" \
"v_mfma_f32_16x16x32_bf16 a[44:47], v[96:99], v[56:59], a[44:47]\n" \
"buffer_load_dwordx4 v27, s[68:71], s85 offen lds\n" \
"s_waitcnt lgkmcnt(14)\n" \
"v_mfma_f32_16x16x32_bf16 a[16:19], v[100:103], v[52:55], a[16:19]\n" \
"ds_read_b128 v[116:119], v29 offset:49152\n" \
"v_writelane_b32 v11, s51, 21\n" \
"v_mfma_f32_16x16x32_bf16 a[48:51], v[100:103], v[56:59], a[48:51]\n" \
"s_add_u32 m0, s31, 0x4c00\n" \
"s_mov_b32 s84, 0x218000\n" \
"s_waitcnt lgkmcnt(13)\n" \
"v_mfma_f32_16x16x32_bf16 a[20:23], v[104:107], v[52:55], a[20:23]\n" \
"buffer_load_dwordx4 v27, s[72:75], s84 offen lds\n" \
"v_mfma_f32_16x16x32_bf16 a[52:55], v[104:107], v[56:59], a[52:55]\n" \
"ds_read_b128 v[120:123], v29 offset:51200\n" \
"s_add_i32 s51, s50, 0x19000\n" \
"s_waitcnt lgkmcnt(12)\n" \
"v_mfma_f32_16x16x32_bf16 a[24:27], v[108:111], v[52:55], a[24:27]\n" \
"ds_read_b64_tr_b16 v[152:153], v33 offset:40960\n" \
"v_mfma_f32_16x16x32_bf16 a[56:59], v[108:111], v[56:59], a[56:59]\n" \
"ds_read_b64_tr_b16 v[154:155], v33 offset:41984\n" \
"s_waitcnt lgkmcnt(12)\n" \
"v_mfma_f32_16x16x32_bf16 a[28:31], v[112:115], v[52:55], a[28:31]\n" \
"ds_read_b64_tr_b16 v[156:157], v34 offset:40960\n" \
"v_mfma_f32_16x16x32_bf16 a[60:63], v[112:115], v[56:59], a[60:63]\n" \
"ds_read_b64_tr_b16 v[158:159], v34 offset:41984\n" \
"s_waitcnt lgkmcnt(13)\n" \
"v_mfma_f32_16x16x32_bf16 a[92:95], v[112:115], v[60:63], a[92:95]\n" \
"s_cmpk_lt_u32 s3, 3\n" \
"s_cselect_b32 s51, s51, s5\n" \
"s_waitcnt lgkmcnt(12)\n" \
"v_mfma_f32_16x16x32_bf16 a[124:127], v[112:115], v[64:67], a[124:127]\n" \
"ds_read_b64_tr_b16 v[160:161], v35 offset:40960\n" \
"s_bitset1_b32 s50, 17\n" \
"v_mfma_f32_16x16x32_bf16 a[88:91], v[108:111], v[60:63], a[88:91]\n" \
"v_writelane_b32 v11, s51, 22\n" \
"s_movk_i32 s51, 0x2400\n" \
"v_mfma_f32_16x16x32_bf16 a[120:123], v[108:111], v[64:67], a[120:123]\n" \
"ds_read_b64_tr_b16 v[162:163], v35 offset:41984\n" \
"v_mfma_f32_16x16x32_bf16 a[84:87], v[104:107], v[60:63], a[84:87]\n" \
"s_mov_b32 m0, s32\n" \
"v_mfma_f32_16x16x32_bf16 a[116:119], v[104:107], v[64:67], a[116:119]\n" \
"s_mov_b32 exec_lo, 0xffffffff\n" \
"s_mov_b32 exec_hi, 0x0\n" \
"global_load_lds_dword v[188:189], off\n" \
"s_mov_b64 exec, -1\n" \
"v_mfma_f32_16x16x32_bf16 a[80:83], v[100:103], v[60:63], a[80:83]\n" \
"ds_read_b64_tr_b16 v[164:165], v36 offset:40960\n" \
"v_mfma_f32_16x16x32_bf16 a[112:115], v[100:103], v[64:67], a[112:115]\n" \
"ds_read_b64_tr_b16 v[166:167], v36 offset:41984\n" \
"v_mfma_f32_16x16x32_bf16 a[76:79], v[96:99], v[60:63], a[76:79]\n" \
"s_waitcnt vmcnt(17)\n" \
"v_or_b32_e32 v204, v210, v204\n" \
"v_or_b32_e32 v206, v210, v206\n" \
"v_mfma_f32_16x16x32_bf16 a[108:111], v[96:99], v[64:67], a[108:111]\n" \
"v_mov_b32_e64 v210, 0\n" \
"s_add_u32 s88, s35, 0x1000\n" \
"v_mfma_f32_16x16x32_bf16 a[72:75], v[92:95], v[60:63], a[72:75]\n" \
"ds_read_b64_tr_b16 v[168:169], v37 offset:40960\n" \
"v_mfma_f32_16x16x32_bf16 a[104:107], v[92:95], v[64:67], a[104:107]\n" \
"s_cmpk_lt_u32 s3, 3\n" \
"s_cselect_b32 s50, s50, s51\n" \
"v_mfma_f32_16x16x32_bf16 a[68:71], v[88:91], v[60:63], a[68:71]\n" \
"ds_read_b64_tr_b16 v[170:171], v37 offset:41984\n" \
"s_xor_b32 s46, s46, 2\n" \
"v_mfma_f32_16x16x32_bf16 a[100:103], v[88:91], v[64:67], a[100:103]\n" \
"v_writelane_b32 v11, s50, 23\n" \
"s_lshl_b32 s50, s3, 10\n" \
"v_mfma_f32_16x16x32_bf16 a[64:67], v[84:87], v[60:63], a[64:67]\n" \
"ds_write_b128 v196, v[200:203]\n" \
"v_mfma_f32_16x16x32_bf16 a[96:99], v[84:87], v[64:67], a[96:99]\n" \
"ds_read_b64_tr_b16 v[172:173], v38 offset:40960\n" \
"s_or_b32 s50, s50, 0xf000\n" \
"s_waitcnt lgkmcnt(15)\n" \
"v_mfma_f32_16x16x32_bf16 a[128:131], v[84:87], v[68:71], a[128:131]\n" \
"ds_read_b64_tr_b16 v[174:175], v38 offset:41984\n" \
"v_writelane_b32 v11, s50, 24\n" \
"v_mfma_f32_16x16x32_bf16 a[160:163], v[84:87], v[72:75], a[160:163]\n" \
"s_add_i32 s50, s49, 1\n" \
"s_and_b32 s50, s50, 3\n" \
"v_mfma_f32_16x16x32_bf16 a[132:135], v[88:91], v[68:71], a[132:135]\n" \
"ds_read_b64_tr_b16 v[176:177], v39 offset:40960\n" \
"s_or_b32 s50, s50, s41\n" \
"v_mfma_f32_16x16x32_bf16 a[164:167], v[88:91], v[72:75], a[164:167]\n" \
"s_mul_i32 s51, s50, 0x24000\n" \
"s_add_i32 s51, s51, s52\n" \
"v_mfma_f32_16x16x32_bf16 a[136:139], v[92:95], v[68:71], a[136:139]\n" \
"ds_write_b128 v197, v[204:207]\n" \
"v_mfma_f32_16x16x32_bf16 a[168:171], v[92:95], v[72:75], a[168:171]\n" \
"ds_read_b64_tr_b16 v[178:179], v39 offset:41984\n" \
"s_add_i32 s49, s49, -1\n" \
"v_mfma_f32_16x16x32_bf16 a[140:143], v[96:99], v[68:71], a[140:143]\n" \
"v_writelane_b32 v11, s51, 26\n" \
"s_mul_i32 s51, s46, 0x24000\n" \
"v_mfma_f32_16x16x32_bf16 a[172:175], v[96:99], v[72:75], a[172:175]\n" \
"ds_read_b128 v[124:127], v29 offset:53248\n" \
"s_and_b32 s49, s49, 3\n" \
"v_mfma_f32_16x16x32_bf16 a[144:147], v[100:103], v[68:71], a[144:147]\n" \
"s_add_i32 s51, s51, s52\n" \
"s_or_b32 s49, s49, s41\n" \
"v_mfma_f32_16x16x32_bf16 a[176:179], v[100:103], v[72:75], a[176:179]\n" \
"ds_read_b128 v[128:131], v29 offset:55296\n" \
"v_writelane_b32 v11, s51, 27\n" \
"v_mfma_f32_16x16x32_bf16 a[148:151], v[104:107], v[68:71], a[148:151]\n" \
"ds_write_b128 v198, v[208:211]\n" \
"v_mfma_f32_16x16x32_bf16 a[180:183], v[104:107], v[72:75], a[180:183]\n" \
"ds_read_b128 v[132:135], v29 offset:32768\n" \
"s_mul_i32 s51, s49, 0x24000\n" \
"v_mfma_f32_16x16x32_bf16 a[152:155], v[108:111], v[68:71], a[152:155]\n" \
"s_add_i32 s51, s51, s52\n" \
"v_writelane_b32 v11, s51, 28\n" \
"v_mfma_f32_16x16x32_bf16 a[184:187], v[108:111], v[72:75], a[184:187]\n" \
"ds_read_b128 v[136:139], v29 offset:34816\n" \
"s_or_b32 s51, s48, s47\n" \
"v_mfma_f32_16x16x32_bf16 a[156:159], v[112:115], v[68:71], a[156:159]\n" \
"s_mul_i32 s51, s51, 0x24800\n" \
"s_lshl_b32 s50, s50, 12\n" \
"v_mfma_f32_16x16x32_bf16 a[188:191], v[112:115], v[72:75], a[188:191]\n" \
"ds_read_b128 v[140:143], v29 offset:36864\n" \
"s_add_i32 s52, s51, 0x3004800\n" \
"v_mfma_f32_16x16x32_bf16 a[220:223], v[112:115], v[76:79], a[220:223]\n" \
"s_add_i32 s53, s52, s50\n" \
"v_writelane_b32 v11, s53, 29\n" \
"v_mfma_f32_16x16x32_bf16 a[252:255], v[112:115], v[80:83], a[252:255]\n" \
"ds_read_b128 v[144:147], v29 offset:38912\n" \
"s_lshl_b32 s46, s46, 12\n" \
"v_mfma_f32_16x16x32_bf16 a[216:219], v[108:111], v[76:79], a[216:219]\n" \
"s_lshl_b32 s49, s49, 12\n" \
"s_sub_i32 s47, s48, s47\n" \
"v_mfma_f32_16x16x32_bf16 a[248:251], v[108:111], v[80:83], a[248:251]\n" \
"s_add_i32 s53, s52, s46\n" \
"v_writelane_b32 v11, s53, 30\n" \
"v_mfma_f32_16x16x32_bf16 a[212:215], v[104:107], v[76:79], a[212:215]\n" \
"s_add_i32 s52, s52, s49\n" \
"s_mul_i32 s47, s47, 0x24800\n" \
"v_mfma_f32_16x16x32_bf16 a[244:247], v[104:107], v[80:83], a[244:247]\n" \
"v_writelane_b32 v11, s52, 31\n" \
"s_add_i32 s52, s47, 0x4120800\n" \
"v_mfma_f32_16x16x32_bf16 a[208:211], v[100:103], v[76:79], a[208:211]\n" \
"s_add_i32 s50, s52, s50\n" \
"v_writelane_b32 v11, s50, 32\n" \
"v_mfma_f32_16x16x32_bf16 a[240:243], v[100:103], v[80:83], a[240:243]\n" \
"s_add_i32 s46, s52, s46\n" \
"v_writelane_b32 v11, s46, 33\n" \
"v_mfma_f32_16x16x32_bf16 a[204:207], v[96:99], v[76:79], a[204:207]\n" \
"s_add_i32 s52, s52, s49\n" \
"s_or_b32 s46, s51, 0x3000000\n" \
"v_mfma_f32_16x16x32_bf16 a[236:239], v[96:99], v[80:83], a[236:239]\n" \
"s_lshl_b32 s49, s40, 11\n" \
"v_writelane_b32 v11, s52, 34\n" \
"v_mfma_f32_16x16x32_bf16 a[200:203], v[92:95], v[76:79], a[200:203]\n" \
"s_add_i32 s50, s46, s49\n" \
"v_writelane_b32 v11, s50, 35\n" \
"v_mfma_f32_16x16x32_bf16 a[232:235], v[92:95], v[80:83], a[232:235]\n" \
"s_add_i32 s50, s47, 0x411c000\n" \
"s_add_i32 s52, s50, s49\n" \
"v_mfma_f32_16x16x32_bf16 a[196:199], v[88:91], v[76:79], a[196:199]\n" \
"v_writelane_b32 v11, s52, 36\n" \
"s_add_i32 s51, s51, 0x3004000\n" \
"v_mfma_f32_16x16x32_bf16 a[228:231], v[88:91], v[80:83], a[228:231]\n" \
"v_writelane_b32 v11, s51, 37\n" \
"s_add_i32 s47, s47, 0x4120000\n" \
"v_mfma_f32_16x16x32_bf16 a[192:195], v[84:87], v[76:79], a[192:195]\n" \
"v_writelane_b32 v11, s47, 38\n" \
"s_lshl_b32 s47, s6, 2\n" \
"v_mfma_f32_16x16x32_bf16 a[224:227], v[84:87], v[80:83], a[224:227]\n" \
"s_waitcnt vmcnt(1) lgkmcnt(0)\n" \
"s_barrier\n" \
"v_mfma_f32_16x16x32_bf16 a[0:3], v[148:151], v[116:119], a[0:3]\n" \
"s_add_u32 m0, s30, 0x8000\n" \
"s_mov_b32 s85, 0x280\n" \
"v_mfma_f32_16x16x32_bf16 a[32:35], v[148:151], v[120:123], a[32:35]\n" \
"buffer_load_dwordx4 v22, s[64:67], s85 offen lds\n" \
"v_mfma_f32_16x16x32_bf16 a[4:7], v[152:155], v[116:119], a[4:7]\n" \
"ds_read_b64_tr_b16 v[84:85], v32 offset:0\n" \
"s_or_b32 s43, s47, s43\n" \
"v_mfma_f32_16x16x32_bf16 a[36:39], v[152:155], v[120:123], a[36:39]\n" \
"s_add_u32 m0, s30, 0x8400\n" \
"s_mov_b32 s84, 0x2280\n" \
"v_mfma_f32_16x16x32_bf16 a[8:11], v[156:159], v[116:119], a[8:11]\n" \
"buffer_load_dwordx4 v23, s[64:67], s84 offen lds\n" \
"v_mfma_f32_16x16x32_bf16 a[40:43], v[156:159], v[120:123], a[40:43]\n" \
"ds_read_b64_tr_b16 v[86:87], v32 offset:1024\n" \
"s_mul_i32 s43, s43, 0x90000\n" \
"v_mfma_f32_16x16x32_bf16 a[12:15], v[160:163], v[116:119], a[12:15]\n" \
"s_add_u32 m0, s31, 0x8000\n" \
"s_mov_b32 s85, 0x280000\n" \
"v_mfma_f32_16x16x32_bf16 a[44:47], v[160:163], v[120:123], a[44:47]\n" \
"buffer_load_dwordx4 v26, s[68:71], s85 offen lds\n" \
"v_mfma_f32_16x16x32_bf16 a[16:19], v[164:167], v[116:119], a[16:19]\n" \
"ds_read_b128 v[52:55], v28 offset:16384\n" \
"s_lshl_b32 s47, s3, 14\n" \
"v_mfma_f32_16x16x32_bf16 a[48:51], v[164:167], v[120:123], a[48:51]\n" \
"s_add_u32 m0, s31, 0xc000\n" \
"s_mov_b32 s84, 0x280000\n" \
"v_mfma_f32_16x16x32_bf16 a[20:23], v[168:171], v[116:119], a[20:23]\n" \
"buffer_load_dwordx4 v26, s[72:75], s84 offen lds\n" \
"v_mfma_f32_16x16x32_bf16 a[52:55], v[168:171], v[120:123], a[52:55]\n" \
"ds_read_b128 v[56:59], v28 offset:18432\n" \
"s_add_i32 s43, s43, s47\n" \
"v_mfma_f32_16x16x32_bf16 a[24:27], v[172:175], v[116:119], a[24:27]\n" \
"s_add_u32 m0, s30, 0x8800\n" \
"s_mov_b32 s85, 0x4280\n" \
"v_mfma_f32_16x16x32_bf16 a[56:59], v[172:175], v[120:123], a[56:59]\n" \
"buffer_load_dwordx4 v22, s[64:67], s85 offen lds\n" \
"v_mfma_f32_16x16x32_bf16 a[28:31], v[176:179], v[116:119], a[28:31]\n" \
"ds_read_b64_tr_b16 v[88:89], v33 offset:0\n" \
"s_lshl_b32 s47, s48, 4\n" \
"v_mfma_f32_16x16x32_bf16 a[60:63], v[176:179], v[120:123], a[60:63]\n" \
"ds_read_b64_tr_b16 v[90:91], v33 offset:1024\n" \
"s_lshl_b32 s48, s6, 7\n" \
"v_mfma_f32_16x16x32_bf16 a[92:95], v[176:179], v[124:127], a[92:95]\n" \
"s_add_u32 m0, s30, 0x8c00\n" \
"s_mov_b32 s84, 0x6280\n" \
"v_mfma_f32_16x16x32_bf16 a[124:127], v[176:179], v[128:131], a[124:127]\n" \
"buffer_load_dwordx4 v23, s[64:67], s84 offen lds\n" \
"v_mfma_f32_16x16x32_bf16 a[88:91], v[172:175], v[124:127], a[88:91]\n" \
"ds_read_b64_tr_b16 v[92:93], v34 offset:0\n" \
"s_or_b32 s51, s47, s48\n" \
"v_mfma_f32_16x16x32_bf16 a[120:123], v[172:175], v[128:131], a[120:123]\n" \
"s_add_u32 m0, s31, 0x8400\n" \
"s_mov_b32 s85, 0x288000\n" \
"v_mfma_f32_16x16x32_bf16 a[84:87], v[168:171], v[124:127], a[84:87]\n" \
"buffer_load_dwordx4 v26, s[68:71], s85 offen lds\n" \
"v_mfma_f32_16x16x32_bf16 a[116:119], v[168:171], v[128:131], a[116:119]\n" \
"ds_read_b64_tr_b16 v[94:95], v34 offset:1024\n" \
"s_sub_i32 s47, s47, s48\n" \
"v_mfma_f32_16x16x32_bf16 a[80:83], v[164:167], v[124:127], a[80:83]\n" \
"s_add_u32 m0, s31, 0xc400\n" \
"s_mov_b32 s84, 0x288000\n" \
"v_mfma_f32_16x16x32_bf16 a[112:115], v[164:167], v[128:131], a[112:115]\n" \
"buffer_load_dwordx4 v26, s[72:75], s84 offen lds\n" \
"v_mfma_f32_16x16x32_bf16 a[76:79], v[160:163], v[124:127], a[76:79]\n" \
"ds_read_b64_tr_b16 v[96:97], v35 offset:0\n" \
"s_mulk_i32 s51, 0x480\n" \
"v_mfma_f32_16x16x32_bf16 a[108:111], v[160:163], v[128:131], a[108:111]\n" \
"s_add_u32 m0, s30, 0x9000\n" \
"s_mov_b32 s85, 0x8280\n" \
"v_mfma_f32_16x16x32_bf16 a[72:75], v[156:159], v[124:127], a[72:75]\n" \
"buffer_load_dwordx4 v22, s[64:67], s85 offen lds\n" \
"v_mfma_f32_16x16x32_bf16 a[104:107], v[156:159], v[128:131], a[104:107]\n" \
"ds_read_b64_tr_b16 v[98:99], v35 offset:1024\n" \
"s_mulk_i32 s47, 0x480\n" \
"v_mfma_f32_16x16x32_bf16 a[68:71], v[152:155], v[124:127], a[68:71]\n" \
"s_add_u32 m0, s30, 0x9400\n" \
"s_mov_b32 s84, 0xa280\n" \
"v_mfma_f32_16x16x32_bf16 a[100:103], v[152:155], v[128:131], a[100:103]\n" \
"buffer_load_dwordx4 v23, s[64:67], s84 offen lds\n" \
"v_mfma_f32_16x16x32_bf16 a[64:67], v[148:151], v[124:127], a[64:67]\n" \
"ds_read_b64_tr_b16 v[100:101], v36 offset:0\n" \
"s_or_b32 s48, s51, s45\n" \
"v_mfma_f32_16x16x32_bf16 a[96:99], v[148:151], v[128:131], a[96:99]\n" \
"ds_read_b64_tr_b16 v[102:103], v36 offset:1024\n" \
"s_or_b32 s45, s47, s45\n" \
"v_mfma_f32_16x16x32_bf16 a[128:131], v[148:151], v[132:135], a[128:131]\n" \
"s_add_u32 m0, s31, 0x8800\n" \
"s_mov_b32 s85, 0x290000\n" \
"v_mfma_f32_16x16x32_bf16 a[160:163], v[148:151], v[136:139], a[160:163]\n" \
"buffer_load_dwordx4 v27, s[68:71], s85 offen lds\n" \
"v_mfma_f32_16x16x32_bf16 a[132:135], v[152:155], v[132:135], a[132:135]\n" \
"ds_read_b64_tr_b16 v[104:105], v37 offset:0\n" \
"s_and_b32 s47, s2, 1\n" \
"s_nop 0\n" \
"v_mfma_f32_16x16x32_bf16 a[164:167], v[152:155], v[136:139], a[164:167]\n" \
"s_add_u32 m0, s31, 0xc800\n" \
"s_mov_b32 s84, 0x290000\n" \
"v_mfma_f32_16x16x32_bf16 a[136:139], v[156:159], v[132:135], a[136:139]\n" \
"buffer_load_dwordx4 v27, s[72:75], s84 offen lds\n" \
"v_mfma_f32_16x16x32_bf16 a[168:171], v[156:159], v[136:139], a[168:171]\n" \
"ds_read_b64_tr_b16 v[106:107], v37 offset:1024\n" \
"v_mfma_f32_16x16x32_bf16 a[140:143], v[160:163], v[132:135], a[140:143]\n" \
"ds_read_b128 v[200:203], v199 offset:0\n" \
"v_mfma_f32_16x16x32_bf16 a[172:175], v[160:163], v[136:139], a[172:175]\n" \
"ds_read_b64_tr_b16 v[108:109], v38 offset:0\n" \
"v_mfma_f32_16x16x32_bf16 a[144:147], v[164:167], v[132:135], a[144:147]\n" \
"ds_read_b64_tr_b16 v[110:111], v38 offset:1024\n" \
"v_mfma_f32_16x16x32_bf16 a[176:179], v[164:167], v[136:139], a[176:179]\n" \
"ds_read_b128 v[204:207], v199 offset:4096\n" \
"v_mfma_f32_16x16x32_bf16 a[148:151], v[168:171], v[132:135], a[148:151]\n" \
"ds_read_b64_tr_b16 v[112:113], v39 offset:0\n" \
"v_mfma_f32_16x16x32_bf16 a[180:183], v[168:171], v[136:139], a[180:183]\n" \
"ds_read_b64_tr_b16 v[114:115], v39 offset:1024\n" \
"v_mfma_f32_16x16x32_bf16 a[152:155], v[172:175], v[132:135], a[152:155]\n" \
"ds_read_b128 v[208:211], v199 offset:8192\n" \
"v_mfma_f32_16x16x32_bf16 a[184:187], v[172:175], v[136:139], a[184:187]\n" \
"ds_read_b128 v[60:63], v28 offset:20480\n" \
"v_mfma_f32_16x16x32_bf16 a[156:159], v[176:179], v[132:135], a[156:159]\n" \
"ds_read_b128 v[64:67], v28 offset:22528\n" \
"v_mfma_f32_16x16x32_bf16 a[188:191], v[176:179], v[136:139], a[188:191]\n" \
"ds_read_b128 v[68:71], v28 offset:0\n" \
"v_mfma_f32_16x16x32_bf16 a[220:223], v[176:179], v[140:143], a[220:223]\n" \
"ds_read_b128 v[72:75], v28 offset:2048\n" \
"v_mfma_f32_16x16x32_bf16 a[252:255], v[176:179], v[144:147], a[252:255]\n" \
"ds_read_b128 v[76:79], v28 offset:4096\n" \
"v_mfma_f32_16x16x32_bf16 a[216:219], v[172:175], v[140:143], a[216:219]\n" \
"ds_read_b128 v[80:83], v28 offset:6144\n" \
"v_mfma_f32_16x16x32_bf16 a[248:251], v[172:175], v[144:147], a[248:251]\n" \
"s_cmp_lg_u32 s47, 0\n" \
"s_cselect_b32 s46, s50, s46\n" \
"v_mfma_f32_16x16x32_bf16 a[212:215], v[168:171], v[140:143], a[212:215]\n" \
"s_bfe_u32 s50, s2, 0x20001\n" \
"s_addk_i32 s46, 0x4000\n" \
"v_mfma_f32_16x16x32_bf16 a[244:247], v[168:171], v[144:147], a[244:247]\n" \
"s_xor_b32 s50, s50, s3\n" \
"v_mfma_f32_16x16x32_bf16 a[208:211], v[164:167], v[140:143], a[208:211]\n" \
"s_cmp_lg_u32 s50, 0\n" \
"s_cselect_b32 s46, s42, s46\n" \
"v_mfma_f32_16x16x32_bf16 a[240:243], v[164:167], v[144:147], a[240:243]\n" \
"s_lshl_b32 s40, s40, 16\n" \
"v_writelane_b32 v11, s46, 39\n" \
"v_mfma_f32_16x16x32_bf16 a[204:207], v[160:163], v[140:143], a[204:207]\n" \
"s_add_i32 s46, s43, 0x4240000\n" \
"s_add_i32 s40, s46, s40\n" \
"v_mfma_f32_16x16x32_bf16 a[236:239], v[160:163], v[144:147], a[236:239]\n" \
"v_writelane_b32 v11, s40, 40\n" \
"s_add_i32 s43, s43, 0x42c0000\n" \
"v_mfma_f32_16x16x32_bf16 a[200:203], v[156:159], v[140:143], a[200:203]\n" \
"s_lshl_b32 s40, s47, 10\n" \
"s_or_b32 s40, s43, s40\n" \
"v_mfma_f32_16x16x32_bf16 a[232:235], v[156:159], v[144:147], a[232:235]\n" \
"s_cmp_lg_u32 s50, 0\n" \
"s_cselect_b32 s40, s42, s40\n" \
"v_mfma_f32_16x16x32_bf16 a[196:199], v[152:155], v[140:143], a[196:199]\n" \
"s_mulk_i32 s6, 0x4800\n" \
"v_writelane_b32 v11, s40, 41\n" \
"v_mfma_f32_16x16x32_bf16 a[228:231], v[152:155], v[144:147], a[228:231]\n" \
"s_add_i32 s40, s48, s7\n" \
"v_writelane_b32 v11, s40, 42\n" \
"v_mfma_f32_16x16x32_bf16 a[192:195], v[148:151], v[140:143], a[192:195]\n" \
"s_add_i32 s40, s45, 0x21c000\n" \
"v_mfma_f32_16x16x32_bf16 a[224:227], v[148:151], v[144:147], a[224:227]\n" \
"s_waitcnt lgkmcnt(15)\n" \
"s_nop 0\n" \
"v_mfma_f32_16x16x32_bf16 a[0:3], v[84:87], v[52:55], a[0:3]\n" \
"s_add_u32 m0, s30, 0x9800\n" \
"s_mov_b32 s85, 0xc280\n" \
"v_mfma_f32_16x16x32_bf16 a[32:35], v[84:87], v[56:59], a[32:35]\n" \
"buffer_load_dwordx4 v22, s[64:67], s85 offen lds\n" \
"v_mfma_f32_16x16x32_bf16 a[4:7], v[88:91], v[52:55], a[4:7]\n" \
"ds_read_b64_tr_b16 v[148:149], v32 offset:8192\n" \
"v_mfma_f32_16x16x32_bf16 a[36:39], v[88:91], v[56:59], a[36:39]\n" \
"s_add_u32 m0, s30, 0x9c00\n" \
"s_mov_b32 s84, 0xe280\n" \
"v_mfma_f32_16x16x32_bf16 a[8:11], v[92:95], v[52:55], a[8:11]\n" \
"buffer_load_dwordx4 v23, s[64:67], s84 offen lds\n" \
"v_mfma_f32_16x16x32_bf16 a[40:43], v[92:95], v[56:59], a[40:43]\n" \
"ds_read_b64_tr_b16 v[150:151], v32 offset:9216\n" \
"v_mfma_f32_16x16x32_bf16 a[12:15], v[96:99], v[52:55], a[12:15]\n" \
"s_add_u32 m0, s31, 0x8c00\n" \
"s_mov_b32 s85, 0x298000\n" \
"v_mfma_f32_16x16x32_bf16 a[44:47], v[96:99], v[56:59], a[44:47]\n" \
"buffer_load_dwordx4 v27, s[68:71], s85 offen lds\n" \
"v_mfma_f32_16x16x32_bf16 a[16:19], v[100:103], v[52:55], a[16:19]\n" \
"ds_read_b128 v[116:119], v29 offset:16384\n" \
"v_mfma_f32_16x16x32_bf16 a[48:51], v[100:103], v[56:59], a[48:51]\n" \
"s_add_u32 m0, s31, 0xcc00\n" \
"s_mov_b32 s84, 0x298000\n" \
"s_waitcnt lgkmcnt(15)\n" \
"v_mfma_f32_16x16x32_bf16 a[20:23], v[104:107], v[52:55], a[20:23]\n" \
"buffer_load_dwordx4 v27, s[72:75], s84 offen lds\n" \
"v_mfma_f32_16x16x32_bf16 a[52:55], v[104:107], v[56:59], a[52:55]\n" \
"ds_read_b128 v[120:123], v29 offset:18432\n" \
"s_waitcnt lgkmcnt(14)\n" \
"v_mfma_f32_16x16x32_bf16 a[24:27], v[108:111], v[52:55], a[24:27]\n" \
"ds_read_b64_tr_b16 v[152:153], v33 offset:8192\n" \
"v_mfma_f32_16x16x32_bf16 a[56:59], v[108:111], v[56:59], a[56:59]\n" \
"ds_read_b64_tr_b16 v[154:155], v33 offset:9216\n" \
"s_waitcnt lgkmcnt(13)\n" \
"v_mfma_f32_16x16x32_bf16 a[28:31], v[112:115], v[52:55], a[28:31]\n" \
"ds_read_b64_tr_b16 v[156:157], v34 offset:8192\n" \
"v_mfma_f32_16x16x32_bf16 a[60:63], v[112:115], v[56:59], a[60:63]\n" \
"ds_read_b64_tr_b16 v[158:159], v34 offset:9216\n" \
"s_waitcnt lgkmcnt(13)\n" \
"v_mfma_f32_16x16x32_bf16 a[92:95], v[112:115], v[60:63], a[92:95]\n" \
"s_cmp_lg_u32 s47, 0\n" \
"s_cselect_b32 s45, s40, s48\n" \
"s_waitcnt lgkmcnt(12)\n" \
"v_mfma_f32_16x16x32_bf16 a[124:127], v[112:115], v[64:67], a[124:127]\n" \
"ds_read_b64_tr_b16 v[160:161], v35 offset:8192\n" \
"s_add_i32 s40, s40, s7\n" \
"v_mfma_f32_16x16x32_bf16 a[88:91], v[108:111], v[60:63], a[88:91]\n" \
"v_writelane_b32 v11, s40, 43\n" \
"s_addk_i32 s45, 0x100\n" \
"v_mfma_f32_16x16x32_bf16 a[120:123], v[108:111], v[64:67], a[120:123]\n" \
"ds_read_b64_tr_b16 v[162:163], v35 offset:9216\n" \
"v_mfma_f32_16x16x32_bf16 a[84:87], v[104:107], v[60:63], a[84:87]\n" \
"s_mov_b32 m0, s32\n" \
"v_mfma_f32_16x16x32_bf16 a[116:119], v[104:107], v[64:67], a[116:119]\n" \
"s_mov_b32 exec_lo, 0x0\n" \
"s_mov_b32 exec_hi, 0xffffffff\n" \
"global_load_lds_dword v[188:189], off\n" \
"s_mov_b64 exec, -1\n" \
"v_mfma_f32_16x16x32_bf16 a[80:83], v[100:103], v[60:63], a[80:83]\n" \
"ds_read_b64_tr_b16 v[164:165], v36 offset:8192\n" \
"v_mfma_f32_16x16x32_bf16 a[112:115], v[100:103], v[64:67], a[112:115]\n" \
"ds_read_b64_tr_b16 v[166:167], v36 offset:9216\n" \
"v_mfma_f32_16x16x32_bf16 a[76:79], v[96:99], v[60:63], a[76:79]\n" \
"s_cmp_lg_u32 s50, 0\n" \
"s_cselect_b32 s7, s42, s45\n" \
"v_mfma_f32_16x16x32_bf16 a[108:111], v[96:99], v[64:67], a[108:111]\n" \
"v_and_or_b32 v10, v212, 24, v213\n" \
"v_writelane_b32 v11, s7, 44\n" \
"v_mfma_f32_16x16x32_bf16 a[72:75], v[92:95], v[60:63], a[72:75]\n" \
"ds_read_b64_tr_b16 v[168:169], v37 offset:8192\n" \
"s_or_b32 s7, s43, s49\n" \
"v_mfma_f32_16x16x32_bf16 a[104:107], v[92:95], v[64:67], a[104:107]\n" \
"buffer_store_dwordx4 v[200:203], v20, s[80:83], s35 offen sc1\n" \
"v_mfma_f32_16x16x32_bf16 a[68:71], v[88:91], v[60:63], a[68:71]\n" \
"ds_read_b64_tr_b16 v[170:171], v37 offset:9216\n" \
"v_writelane_b32 v11, s7, 52\n" \
"v_mfma_f32_16x16x32_bf16 a[100:103], v[88:91], v[64:67], a[100:103]\n" \
"s_lshl_b32 s7, s3, 17\n" \
"s_add_i32 s7, s46, s7\n" \
"v_mfma_f32_16x16x32_bf16 a[64:67], v[84:87], v[60:63], a[64:67]\n" \
"ds_read_b64_tr_b16 v[172:173], v38 offset:8192\n" \
"s_or_b32 s7, s7, s41\n" \
"v_mfma_f32_16x16x32_bf16 a[96:99], v[84:87], v[64:67], a[96:99]\n" \
"s_or_b32 s7, s7, 0x3840\n" \
"v_writelane_b32 v11, s7, 6\n" \
"s_waitcnt lgkmcnt(15)\n" \
"v_mfma_f32_16x16x32_bf16 a[128:131], v[84:87], v[68:71], a[128:131]\n" \
"ds_read_b64_tr_b16 v[174:175], v38 offset:9216\n" \
"s_mov_b32 s7, 0xff0000\n" \
"v_mfma_f32_16x16x32_bf16 a[160:163], v[84:87], v[72:75], a[160:163]\n" \
"s_cmp_lg_u32 s50, 0\n" \
"s_cselect_b32 s5, s5, s7\n" \
"v_mfma_f32_16x16x32_bf16 a[132:135], v[88:91], v[68:71], a[132:135]\n" \
"buffer_store_dwordx4 v[204:207], v20, s[80:83], s88 offen sc1\n" \
"v_mfma_f32_16x16x32_bf16 a[164:167], v[88:91], v[72:75], a[164:167]\n" \
"ds_read_b64_tr_b16 v[176:177], v39 offset:8192\n" \
"s_lshl_b32 s4, s4, 6\n" \
"v_mfma_f32_16x16x32_bf16 a[136:139], v[92:95], v[68:71], a[136:139]\n" \
"ds_read_b64_tr_b16 v[178:179], v39 offset:9216\n" \
"v_writelane_b32 v11, s5, 7\n" \
"v_mfma_f32_16x16x32_bf16 a[168:171], v[92:95], v[72:75], a[168:171]\n" \
"s_mul_i32 s5, s3, 0x1200\n" \
"s_add_i32 s5, s5, s6\n" \
"v_mfma_f32_16x16x32_bf16 a[140:143], v[96:99], v[68:71], a[140:143]\n" \
"s_add_i32 s5, s5, s44\n" \
"v_writelane_b32 v11, s5, 8\n" \
"v_mfma_f32_16x16x32_bf16 a[172:175], v[96:99], v[72:75], a[172:175]\n" \
"ds_read_b128 v[124:127], v29 offset:20480\n" \
"s_lshl_b32 s5, s2, 16\n" \
"v_mfma_f32_16x16x32_bf16 a[144:147], v[100:103], v[68:71], a[144:147]\n" \
"s_add_i32 s6, s5, 0x10000\n" \
"s_and_b32 s6, s6, 0x70000\n" \
"v_mfma_f32_16x16x32_bf16 a[176:179], v[100:103], v[72:75], a[176:179]\n" \
"buffer_store_dwordx4 v[208:211], v20, s[80:83], s36 offen sc1\n" \
"v_mfma_f32_16x16x32_bf16 a[148:151], v[104:107], v[68:71], a[148:151]\n" \
"ds_read_b128 v[128:131], v29 offset:22528\n" \
"s_add_i32 s6, s46, s6\n" \
"v_mfma_f32_16x16x32_bf16 a[180:183], v[104:107], v[72:75], a[180:183]\n" \
"ds_read_b128 v[132:135], v29 offset:0\n" \
"v_writelane_b32 v11, s6, 45\n" \
"v_mfma_f32_16x16x32_bf16 a[152:155], v[108:111], v[68:71], a[152:155]\n" \
"s_add_i32 s6, s5, 0x20000\n" \
"s_and_b32 s6, s6, 0x70000\n" \
"v_mfma_f32_16x16x32_bf16 a[184:187], v[108:111], v[72:75], a[184:187]\n" \
"ds_read_b128 v[136:139], v29 offset:2048\n" \
"s_add_i32 s6, s46, s6\n" \
"v_mfma_f32_16x16x32_bf16 a[156:159], v[112:115], v[68:71], a[156:159]\n" \
"s_bitset1_b32 s6, 11\n" \
"v_writelane_b32 v11, s6, 46\n" \
"v_mfma_f32_16x16x32_bf16 a[188:191], v[112:115], v[72:75], a[188:191]\n" \
"ds_read_b128 v[140:143], v29 offset:4096\n" \
"s_add_i32 s6, s5, 0x30000\n" \
"v_mfma_f32_16x16x32_bf16 a[220:223], v[112:115], v[76:79], a[220:223]\n" \
"s_and_b32 s6, s6, 0x70000\n" \
"s_add_i32 s6, s46, s6\n" \
"v_mfma_f32_16x16x32_bf16 a[252:255], v[112:115], v[80:83], a[252:255]\n" \
"ds_read_b128 v[144:147], v29 offset:6144\n" \
"s_bitset1_b32 s6, 12\n" \
"v_mfma_f32_16x16x32_bf16 a[216:219], v[108:111], v[76:79], a[216:219]\n" \
"v_writelane_b32 v11, s6, 47\n" \
"s_and_b32 s6, s5, 0x70000\n" \
"v_mfma_f32_16x16x32_bf16 a[248:251], v[108:111], v[80:83], a[248:251]\n" \
"s_xor_b32 s6, s6, 0x40000\n" \
"s_add_i32 s6, s46, s6\n" \
"v_mfma_f32_16x16x32_bf16 a[212:215], v[104:107], v[76:79], a[212:215]\n" \
"s_or_b32 s6, s6, 0x1800\n" \
"v_writelane_b32 v11, s6, 48\n" \
"v_mfma_f32_16x16x32_bf16 a[244:247], v[104:107], v[80:83], a[244:247]\n" \
"s_add_i32 s6, s5, 0x50000\n" \
"s_and_b32 s6, s6, 0x70000\n" \
"v_mfma_f32_16x16x32_bf16 a[208:211], v[100:103], v[76:79], a[208:211]\n" \
"s_add_i32 s6, s46, s6\n" \
"s_bitset1_b32 s6, 13\n" \
"v_mfma_f32_16x16x32_bf16 a[240:243], v[100:103], v[80:83], a[240:243]\n" \
"v_writelane_b32 v11, s6, 49\n" \
"s_add_i32 s6, s5, 0x60000\n" \
"v_mfma_f32_16x16x32_bf16 a[204:207], v[96:99], v[76:79], a[204:207]\n" \
"s_and_b32 s6, s6, 0x70000\n" \
"s_add_i32 s5, s5, 0x70000\n" \
"v_mfma_f32_16x16x32_bf16 a[236:239], v[96:99], v[80:83], a[236:239]\n" \
"s_add_i32 s6, s46, s6\n" \
"s_and_b32 s5, s5, 0x70000\n" \
"v_mfma_f32_16x16x32_bf16 a[200:203], v[92:95], v[76:79], a[200:203]\n" \
"v_lshrrev_b32_e32 v213, 2, v20\n" \
"v_and_or_b32 v214, v20, 16, v222\n" \
"v_mfma_f32_16x16x32_bf16 a[232:235], v[92:95], v[80:83], a[232:235]\n" \
"s_or_b32 s6, s6, 0x2800\n" \
"v_writelane_b32 v11, s6, 50\n" \
"v_mfma_f32_16x16x32_bf16 a[196:199], v[88:91], v[76:79], a[196:199]\n" \
"s_add_i32 s46, s46, s5\n" \
"v_and_or_b32 v213, v213, 60, s4\n" \
"v_mfma_f32_16x16x32_bf16 a[228:231], v[88:91], v[80:83], a[228:231]\n" \
"v_lshl_or_b32 v212, s3, 9, v212\n" \
"v_add_u32_e32 v9, 0x4800, v214\n" \
"v_mfma_f32_16x16x32_bf16 a[192:195], v[84:87], v[76:79], a[192:195]\n" \
"s_or_b32 s5, s46, 0x3000\n" \
"v_writelane_b32 v11, s5, 51\n" \
"v_mfma_f32_16x16x32_bf16 a[224:227], v[84:87], v[80:83], a[224:227]\n" \
"s_waitcnt vmcnt(4) lgkmcnt(0)\n" \
"s_barrier\n" \
"v_mfma_f32_16x16x32_bf16 a[0:3], v[148:151], v[116:119], a[0:3]\n" \
"s_add_u32 m0, s30, 0x0\n" \
"s_mov_b32 s85, 0x300\n" \
"v_mfma_f32_16x16x32_bf16 a[32:35], v[148:151], v[120:123], a[32:35]\n" \
"buffer_load_dwordx4 v22, s[64:67], s85 offen lds\n" \
"v_mfma_f32_16x16x32_bf16 a[4:7], v[152:155], v[116:119], a[4:7]\n" \
"ds_read_b64_tr_b16 v[84:85], v32 offset:32768\n" \
"v_or_b32_e32 v18, 0x27000, v213\n" \
"v_mfma_f32_16x16x32_bf16 a[36:39], v[152:155], v[120:123], a[36:39]\n" \
"ds_read_b64_tr_b16 v[86:87], v32 offset:33792\n" \
"v_or_b32_e32 v19, 0x27800, v212\n" \
"v_mfma_f32_16x16x32_bf16 a[8:11], v[156:159], v[116:119], a[8:11]\n" \
"s_add_u32 m0, s30, 0x400\n" \
"s_mov_b32 s84, 0x2300\n" \
"v_mfma_f32_16x16x32_bf16 a[40:43], v[156:159], v[120:123], a[40:43]\n" \
"buffer_load_dwordx4 v23, s[64:67], s84 offen lds\n" \
"v_mfma_f32_16x16x32_bf16 a[12:15], v[160:163], v[116:119], a[12:15]\n" \
"ds_read_b128 v[52:55], v28 offset:49152\n" \
"v_mov_b32_e32 v219, 0x1000c0c\n" \
"v_mfma_f32_16x16x32_bf16 a[44:47], v[160:163], v[120:123], a[44:47]\n" \
"ds_read_b128 v[56:59], v28 offset:51200\n" \
"v_mov_b32_e32 v220, 0xc0c0100\n" \
"v_mfma_f32_16x16x32_bf16 a[16:19], v[164:167], v[116:119], a[16:19]\n" \
"s_add_u32 m0, s31, 0x0\n" \
"s_mov_b32 s85, 0x300000\n" \
"v_mfma_f32_16x16x32_bf16 a[48:51], v[164:167], v[120:123], a[48:51]\n" \
"buffer_load_dwordx4 v26, s[68:71], s85 offen lds\n" \
"v_mfma_f32_16x16x32_bf16 a[20:23], v[168:171], v[116:119], a[20:23]\n" \
"ds_read_b64_tr_b16 v[88:89], v33 offset:32768\n" \
"v_bfe_u32 v218, v20, 4, 1\n" \
"v_mfma_f32_16x16x32_bf16 a[52:55], v[168:171], v[120:123], a[52:55]\n" \
"ds_read_b64_tr_b16 v[90:91], v33 offset:33792\n" \
"v_lshrrev_b32_e32 v213, 8, v20\n" \
"v_mfma_f32_16x16x32_bf16 a[24:27], v[172:175], v[116:119], a[24:27]\n" \
"s_add_u32 m0, s31, 0x4000\n" \
"s_mov_b32 s84, 0x300000\n" \
"v_mfma_f32_16x16x32_bf16 a[56:59], v[172:175], v[120:123], a[56:59]\n" \
"buffer_load_dwordx4 v26, s[72:75], s84 offen lds\n" \
"v_mfma_f32_16x16x32_bf16 a[28:31], v[176:179], v[116:119], a[28:31]\n" \
"ds_read_b64_tr_b16 v[92:93], v34 offset:32768\n" \
"v_cmp_eq_u32_e64 vcc, 0, v218\n" \
"v_mfma_f32_16x16x32_bf16 a[60:63], v[176:179], v[120:123], a[60:63]\n" \
"ds_read_b64_tr_b16 v[94:95], v34 offset:33792\n" \
"s_lshr_b32 s4, s2, 5\n" \
"v_mfma_f32_16x16x32_bf16 a[92:95], v[176:179], v[124:127], a[92:95]\n" \
"s_add_u32 m0, s30, 0x800\n" \
"s_mov_b32 s85, 0x4300\n" \
"v_mfma_f32_16x16x32_bf16 a[124:127], v[176:179], v[128:131], a[124:127]\n" \
"buffer_load_dwordx4 v22, s[64:67], s85 offen lds\n" \
"v_mfma_f32_16x16x32_bf16 a[88:91], v[172:175], v[124:127], a[88:91]\n" \
"ds_read_b64_tr_b16 v[96:97], v35 offset:32768\n" \
"s_and_b32 s5, s3, 1\n" \
"v_mfma_f32_16x16x32_bf16 a[120:123], v[172:175], v[128:131], a[120:123]\n" \
"ds_read_b64_tr_b16 v[98:99], v35 offset:33792\n" \
"v_bfe_u32 v217, v20, 4, 4\n" \
"v_mfma_f32_16x16x32_bf16 a[84:87], v[168:171], v[124:127], a[84:87]\n" \
"s_add_u32 m0, s30, 0xc00\n" \
"s_mov_b32 s84, 0x6300\n" \
"v_mfma_f32_16x16x32_bf16 a[116:119], v[168:171], v[128:131], a[116:119]\n" \
"buffer_load_dwordx4 v23, s[64:67], s84 offen lds\n" \
"v_mfma_f32_16x16x32_bf16 a[80:83], v[164:167], v[124:127], a[80:83]\n" \
"ds_read_b64_tr_b16 v[100:101], v36 offset:32768\n" \
"v_cndmask_b32_e64 v219, v219, v220, vcc\n" \
"v_mfma_f32_16x16x32_bf16 a[112:115], v[164:167], v[128:131], a[112:115]\n" \
"ds_read_b64_tr_b16 v[102:103], v36 offset:33792\n" \
"v_mov_b32_e32 v220, 0x3020c0c\n" \
"v_mfma_f32_16x16x32_bf16 a[76:79], v[160:163], v[124:127], a[76:79]\n" \
"s_add_u32 m0, s31, 0x400\n" \
"s_mov_b32 s85, 0x308000\n" \
"v_mfma_f32_16x16x32_bf16 a[108:111], v[160:163], v[128:131], a[108:111]\n" \
"buffer_load_dwordx4 v26, s[68:71], s85 offen lds\n" \
"v_mfma_f32_16x16x32_bf16 a[72:75], v[156:159], v[124:127], a[72:75]\n" \
"ds_read_b64_tr_b16 v[104:105], v37 offset:32768\n" \
"v_mov_b32_e32 v221, 0xc0c0302\n" \
"v_mfma_f32_16x16x32_bf16 a[104:107], v[156:159], v[128:131], a[104:107]\n" \
"v_lshl_or_b32 v218, v213, 2, v218\n" \
"s_xor_b32 s6, s4, 15\n" \
"s_nop 0\n" \
"v_mfma_f32_16x16x32_bf16 a[68:71], v[152:155], v[124:127], a[68:71]\n" \
"s_add_u32 m0, s31, 0x4400\n" \
"s_mov_b32 s84, 0x308000\n" \
"v_mfma_f32_16x16x32_bf16 a[100:103], v[152:155], v[128:131], a[100:103]\n" \
"buffer_load_dwordx4 v26, s[72:75], s84 offen lds\n" \
"v_mfma_f32_16x16x32_bf16 a[64:67], v[148:151], v[124:127], a[64:67]\n" \
"ds_read_b64_tr_b16 v[106:107], v37 offset:33792\n" \
"v_mfma_f32_16x16x32_bf16 a[96:99], v[148:151], v[128:131], a[96:99]\n" \
"ds_read_b64_tr_b16 v[108:109], v38 offset:32768\n" \
"v_mfma_f32_16x16x32_bf16 a[128:131], v[148:151], v[132:135], a[128:131]\n" \
"s_add_u32 m0, s30, 0x1000\n" \
"s_mov_b32 s85, 0x8300\n" \
"v_mfma_f32_16x16x32_bf16 a[160:163], v[148:151], v[136:139], a[160:163]\n" \
"buffer_load_dwordx4 v22, s[64:67], s85 offen lds\n" \
"v_mfma_f32_16x16x32_bf16 a[132:135], v[152:155], v[132:135], a[132:135]\n" \
"ds_read_b64_tr_b16 v[110:111], v38 offset:33792\n" \
"v_mfma_f32_16x16x32_bf16 a[164:167], v[152:155], v[136:139], a[164:167]\n" \
"ds_read_b64_tr_b16 v[112:113], v39 offset:32768\n" \
"v_mfma_f32_16x16x32_bf16 a[136:139], v[156:159], v[132:135], a[136:139]\n" \
"s_add_u32 m0, s30, 0x1400\n" \
"s_mov_b32 s84, 0xa300\n" \
"v_mfma_f32_16x16x32_bf16 a[168:171], v[156:159], v[136:139], a[168:171]\n" \
"buffer_load_dwordx4 v23, s[64:67], s84 offen lds\n" \
"v_mfma_f32_16x16x32_bf16 a[140:143], v[160:163], v[132:135], a[140:143]\n" \
"ds_read_b64_tr_b16 v[114:115], v39 offset:33792\n" \
"v_mfma_f32_16x16x32_bf16 a[172:175], v[160:163], v[136:139], a[172:175]\n" \
"ds_read_b128 v[60:63], v28 offset:53248\n" \
"v_mfma_f32_16x16x32_bf16 a[144:147], v[164:167], v[132:135], a[144:147]\n" \
"s_add_u32 m0, s31, 0x800\n" \
"s_mov_b32 s85, 0x310000\n" \
"v_mfma_f32_16x16x32_bf16 a[176:179], v[164:167], v[136:139], a[176:179]\n" \
"buffer_load_dwordx4 v27, s[68:71], s85 offen lds\n" \
"v_mfma_f32_16x16x32_bf16 a[148:151], v[168:171], v[132:135], a[148:151]\n" \
"ds_read_b128 v[64:67], v28 offset:55296\n" \
"v_mfma_f32_16x16x32_bf16 a[180:183], v[168:171], v[136:139], a[180:183]\n" \
"ds_read_b128 v[68:71], v28 offset:32768\n" \
"v_mfma_f32_16x16x32_bf16 a[152:155], v[172:175], v[132:135], a[152:155]\n" \
"s_add_u32 m0, s31, 0x4800\n" \
"s_mov_b32 s84, 0x310000\n" \
"v_mfma_f32_16x16x32_bf16 a[184:187], v[172:175], v[136:139], a[184:187]\n" \
"buffer_load_dwordx4 v27, s[72:75], s84 offen lds\n" \
"v_mfma_f32_16x16x32_bf16 a[156:159], v[176:179], v[132:135], a[156:159]\n" \
"ds_read_b128 v[72:75], v28 offset:34816\n" \
"v_mfma_f32_16x16x32_bf16 a[188:191], v[176:179], v[136:139], a[188:191]\n" \
"ds_read_b128 v[76:79], v28 offset:36864\n" \
"v_mfma_f32_16x16x32_bf16 a[220:223], v[176:179], v[140:143], a[220:223]\n" \
"s_cmpk_lt_u32 s3, 2\n" \
"s_cselect_b32 s7, s6, s4\n" \
"v_mfma_f32_16x16x32_bf16 a[252:255], v[176:179], v[144:147], a[252:255]\n" \
"ds_read_b128 v[80:83], v28 offset:38912\n" \
"s_lshl_b32 s40, s7, 7\n" \
"v_mfma_f32_16x16x32_bf16 a[216:219], v[172:175], v[140:143], a[216:219]\n" \
"s_lshl_b32 s41, s5, 6\n" \
"v_cndmask_b32_e64 v220, v220, v221, vcc\n" \
"v_mfma_f32_16x16x32_bf16 a[248:251], v[172:175], v[144:147], a[248:251]\n" \
"v_mov_b32_e32 v221, 0xc0c0c0c\n" \
"v_cmp_gt_u32_e64 vcc, v218, v217\n" \
"v_mfma_f32_16x16x32_bf16 a[212:215], v[168:171], v[140:143], a[212:215]\n" \
"v_or_b32_e32 v218, 2, v218\n" \
"v_lshrrev_b32_e32 v212, 7, v20\n" \
"v_mfma_f32_16x16x32_bf16 a[244:247], v[168:171], v[144:147], a[244:247]\n" \
"s_or_b32 s41, s41, s40\n" \
"v_cndmask_b32_e32 v40, v221, v219, vcc\n" \
"v_mfma_f32_16x16x32_bf16 a[208:211], v[164:167], v[140:143], a[208:211]\n" \
"v_cndmask_b32_e32 v42, v219, v221, vcc\n" \
"v_cmp_gt_u32_e32 vcc, v218, v217\n" \
"v_mfma_f32_16x16x32_bf16 a[240:243], v[164:167], v[144:147], a[240:243]\n" \
"v_and_b32_e32 v218, 16, v20\n" \
"s_movk_i32 s42, 0x3f80\n" \
"v_mfma_f32_16x16x32_bf16 a[204:207], v[160:163], v[140:143], a[204:207]\n" \
"v_or_b32_e64 v214, s41, v212\n" \
"v_and_b32_e32 v217, 0x320, v20\n" \
"v_mfma_f32_16x16x32_bf16 a[236:239], v[160:163], v[144:147], a[236:239]\n" \
"s_movk_i32 s41, 0x100\n" \
"v_lshlrev_b32_e64 v218, v218, s42\n" \
"v_mfma_f32_16x16x32_bf16 a[200:203], v[156:159], v[140:143], a[200:203]\n" \
"v_mov_b32_e32 v219, 0\n" \
"v_lshrrev_b32_e64 v215, 6, v20\n" \
"v_mfma_f32_16x16x32_bf16 a[232:235], v[156:159], v[144:147], a[232:235]\n" \
"v_cndmask_b32_e32 v41, v221, v220, vcc\n" \
"v_cndmask_b32_e32 v43, v220, v221, vcc\n" \
"v_mfma_f32_16x16x32_bf16 a[196:199], v[152:155], v[140:143], a[196:199]\n" \
"v_cmp_eq_u32_e64 vcc, s41, v217\n" \
"v_and_b32_e32 v217, 0x330, v20\n" \
"v_mfma_f32_16x16x32_bf16 a[228:231], v[152:155], v[144:147], a[228:231]\n" \
"v_bfe_u32 v216, v20, 6, 2\n" \
"v_cndmask_b32_e64 v46, v219, v218, vcc\n" \
"v_mfma_f32_16x16x32_bf16 a[192:195], v[148:151], v[140:143], a[192:195]\n" \
"v_mov_b32_e32 v218, 0x3f80\n" \
"v_cmp_eq_u32_e64 vcc, 32, v217\n" \
"v_mfma_f32_16x16x32_bf16 a[224:227], v[148:151], v[144:147], a[224:227]\n" \
"s_waitcnt lgkmcnt(15)\n" \
"v_mfma_f32_16x16x32_bf16 a[0:3], v[84:87], v[52:55], a[0:3]\n" \
"s_add_u32 m0, s30, 0x1800\n" \
"s_mov_b32 s85, 0xc300\n" \
"v_mfma_f32_16x16x32_bf16 a[32:35], v[84:87], v[56:59], a[32:35]\n" \
"buffer_load_dwordx4 v22, s[64:67], s85 offen lds\n" \
"v_mfma_f32_16x16x32_bf16 a[4:7], v[88:91], v[52:55], a[4:7]\n" \
"ds_read_b64_tr_b16 v[148:149], v32 offset:40960\n" \
"s_add_i32 s41, s40, -16\n" \
"v_mfma_f32_16x16x32_bf16 a[36:39], v[88:91], v[56:59], a[36:39]\n" \
"s_add_u32 m0, s30, 0x1c00\n" \
"s_mov_b32 s84, 0xe300\n" \
"v_mfma_f32_16x16x32_bf16 a[8:11], v[92:95], v[52:55], a[8:11]\n" \
"buffer_load_dwordx4 v23, s[64:67], s84 offen lds\n" \
"v_mfma_f32_16x16x32_bf16 a[40:43], v[92:95], v[56:59], a[40:43]\n" \
"ds_read_b64_tr_b16 v[150:151], v32 offset:41984\n" \
"v_lshrrev_b32_e32 v219, 1, v20\n" \
"s_waitcnt lgkmcnt(15)\n" \
"v_mfma_f32_16x16x32_bf16 a[12:15], v[96:99], v[52:55], a[12:15]\n" \
"s_add_u32 m0, s31, 0xc00\n" \
"s_mov_b32 s85, 0x318000\n" \
"v_mfma_f32_16x16x32_bf16 a[44:47], v[96:99], v[56:59], a[44:47]\n" \
"buffer_load_dwordx4 v27, s[68:71], s85 offen lds\n" \
"s_waitcnt lgkmcnt(14)\n" \
"v_mfma_f32_16x16x32_bf16 a[16:19], v[100:103], v[52:55], a[16:19]\n" \
"ds_read_b128 v[116:119], v29 offset:49152\n" \
"v_bfe_u32 v215, v215, 1, 1\n" \
"v_mfma_f32_16x16x32_bf16 a[48:51], v[100:103], v[56:59], a[48:51]\n" \
"s_add_u32 m0, s31, 0x4c00\n" \
"s_mov_b32 s84, 0x318000\n" \
"s_waitcnt lgkmcnt(13)\n" \
"v_mfma_f32_16x16x32_bf16 a[20:23], v[104:107], v[52:55], a[20:23]\n" \
"buffer_load_dwordx4 v27, s[72:75], s84 offen lds\n" \
"v_mfma_f32_16x16x32_bf16 a[52:55], v[104:107], v[56:59], a[52:55]\n" \
"ds_read_b128 v[120:123], v29 offset:51200\n" \
"v_cndmask_b32_e32 v47, 0, v218, vcc\n" \
"s_waitcnt lgkmcnt(12)\n" \
"v_mfma_f32_16x16x32_bf16 a[24:27], v[108:111], v[52:55], a[24:27]\n" \
"ds_read_b64_tr_b16 v[152:153], v33 offset:40960\n" \
"s_mov_b32 s42, 0\n" \
"v_mfma_f32_16x16x32_bf16 a[56:59], v[108:111], v[56:59], a[56:59]\n" \
"ds_read_b64_tr_b16 v[154:155], v33 offset:41984\n" \
"s_waitcnt lgkmcnt(12)\n" \
"v_mfma_f32_16x16x32_bf16 a[28:31], v[112:115], v[52:55], a[28:31]\n" \
"ds_read_b64_tr_b16 v[156:157], v34 offset:40960\n" \
"v_mfma_f32_16x16x32_bf16 a[60:63], v[112:115], v[56:59], a[60:63]\n" \
"ds_read_b64_tr_b16 v[158:159], v34 offset:41984\n" \
"s_waitcnt lgkmcnt(13)\n" \
"v_mfma_f32_16x16x32_bf16 a[92:95], v[112:115], v[60:63], a[92:95]\n" \
"s_cmp_lg_u32 s40, 0\n" \
"s_cselect_b32 s41, s41, s42\n" \
"s_waitcnt lgkmcnt(12)\n" \
"v_mfma_f32_16x16x32_bf16 a[124:127], v[112:115], v[64:67], a[124:127]\n" \
"ds_read_b64_tr_b16 v[160:161], v35 offset:40960\n" \
"v_lshl_or_b32 v217, s5, 3, v212\n" \
"v_mfma_f32_16x16x32_bf16 a[88:91], v[108:111], v[60:63], a[88:91]\n" \
"v_lshlrev_b32_e32 v45, 3, v20\n" \
"v_add_u32_e32 v45, s31, v45\n" \
"v_mfma_f32_16x16x32_bf16 a[120:123], v[108:111], v[64:67], a[120:123]\n" \
"s_getpc_b64 s[86:87]\n" \
"s_sub_u32 s86, s86, 0xf800\n" \
"s_subb_u32 s87, s87, 0\n" \
"s_mov_b32 m0, s32\n" \
"v_mfma_f32_16x16x32_bf16 a[84:87], v[104:107], v[60:63], a[84:87]\n" \
"global_load_lds_dword v45, s[86:87]\n" \
"v_mfma_f32_16x16x32_bf16 a[116:119], v[104:107], v[64:67], a[116:119]\n" \
"ds_read_b64_tr_b16 v[162:163], v35 offset:41984\n" \
"v_lshlrev_b32_e32 v218, 10, v213\n" \
"v_mfma_f32_16x16x32_bf16 a[80:83], v[100:103], v[60:63], a[80:83]\n" \
"ds_read_b64_tr_b16 v[164:165], v36 offset:40960\n" \
"v_lshlrev_b32_e64 v216, 7, v216\n" \
"v_mfma_f32_16x16x32_bf16 a[112:115], v[100:103], v[64:67], a[112:115]\n" \
"ds_read_b64_tr_b16 v[166:167], v36 offset:41984\n" \
"v_and_b32_e64 v219, 24, v219\n" \
"v_mfma_f32_16x16x32_bf16 a[76:79], v[96:99], v[60:63], a[76:79]\n" \
"v_and_or_b32 v212, v212, 2, v215\n" \
"v_add_u32_e64 v217, s41, v217\n" \
"v_mfma_f32_16x16x32_bf16 a[108:111], v[96:99], v[64:67], a[108:111]\n" \
"v_or3_b32 v216, v216, v218, v219\n" \
"v_lshlrev_b32_e64 v212, 5, v212\n" \
"v_mfma_f32_16x16x32_bf16 a[72:75], v[92:95], v[60:63], a[72:75]\n" \
"ds_read_b64_tr_b16 v[168:169], v37 offset:40960\n" \
"s_mov_b32 s41, 0x14000\n" \
"v_mfma_f32_16x16x32_bf16 a[104:107], v[92:95], v[64:67], a[104:107]\n" \
"v_or3_b32 v190, v216, v212, s41\n" \
"s_lshl_b32 s41, s5, 2\n" \
"v_mfma_f32_16x16x32_bf16 a[68:71], v[88:91], v[60:63], a[68:71]\n" \
"ds_read_b64_tr_b16 v[170:171], v37 offset:41984\n" \
"s_lshl_b32 s7, s7, 3\n" \
"v_mfma_f32_16x16x32_bf16 a[100:103], v[88:91], v[64:67], a[100:103]\n" \
"v_or_b32_e32 v212, s41, v213\n" \
"s_or_b32 s7, s7, s41\n" \
"v_mfma_f32_16x16x32_bf16 a[64:67], v[84:87], v[60:63], a[64:67]\n" \
"ds_read_b64_tr_b16 v[172:173], v38 offset:40960\n" \
"s_mov_b32 s41, 0xff800000\n" \
"v_mfma_f32_16x16x32_bf16 a[96:99], v[84:87], v[64:67], a[96:99]\n" \
"s_cmp_lg_u32 s40, 0\n" \
"s_cselect_b32 s47, s42, s41\n" \
"s_waitcnt lgkmcnt(15)\n" \
"v_mfma_f32_16x16x32_bf16 a[128:131], v[84:87], v[68:71], a[128:131]\n" \
"ds_read_b64_tr_b16 v[174:175], v38 offset:41984\n" \
"s_mov_b32 s40, 0xc000\n" \
"v_mfma_f32_16x16x32_bf16 a[160:163], v[84:87], v[72:75], a[160:163]\n" \
"s_mov_b32 s41, 0x8000\n" \
"v_mfma_f32_16x16x32_bf16 a[132:135], v[88:91], v[68:71], a[132:135]\n" \
"ds_read_b64_tr_b16 v[176:177], v39 offset:40960\n" \
"v_mfma_f32_16x16x32_bf16 a[164:167], v[88:91], v[72:75], a[164:167]\n" \
"s_cmp_lg_u32 s4, 0\n" \
"s_cselect_b32 s51, s40, s41\n" \
"v_mfma_f32_16x16x32_bf16 a[136:139], v[92:95], v[68:71], a[136:139]\n" \
"ds_read_b64_tr_b16 v[178:179], v39 offset:41984\n" \
"s_movk_i32 s40, 0x4000\n" \
"v_mfma_f32_16x16x32_bf16 a[168:171], v[92:95], v[72:75], a[168:171]\n" \
"s_cmp_lg_u32 s4, 0\n" \
"s_cselect_b32 s53, s41, s40\n" \
"v_mfma_f32_16x16x32_bf16 a[140:143], v[96:99], v[68:71], a[140:143]\n" \
"s_lshl_b32 s40, s2, 2\n" \
"s_and_b32 s40, s40, 28\n" \
"v_mfma_f32_16x16x32_bf16 a[172:175], v[96:99], v[72:75], a[172:175]\n" \
"ds_read_b128 v[124:127], v29 offset:53248\n" \
"s_bfe_u32 s45, s2, 0x20003\n" \
"v_mfma_f32_16x16x32_bf16 a[144:147], v[100:103], v[68:71], a[144:147]\n" \
"s_lshr_b32 s44, s3, 1\n" \
"s_or_b32 s46, s40, s45\n" \
"v_mfma_f32_16x16x32_bf16 a[176:179], v[100:103], v[72:75], a[176:179]\n" \
"ds_read_b128 v[128:131], v29 offset:55296\n" \
"s_lshl_b32 s43, s44, 15\n" \
"v_mfma_f32_16x16x32_bf16 a[148:151], v[104:107], v[68:71], a[148:151]\n" \
"s_lshl_b32 s41, s5, 14\n" \
"s_lshl_b32 s48, s46, 20\n" \
"v_mfma_f32_16x16x32_bf16 a[180:183], v[104:107], v[72:75], a[180:183]\n" \
"ds_read_b128 v[132:135], v29 offset:32768\n" \
"s_or_b32 s41, s43, s41\n" \
"v_mfma_f32_16x16x32_bf16 a[152:155], v[108:111], v[68:71], a[152:155]\n" \
"s_add_i32 s49, s48, 0x1000000\n" \
"s_or_b32 s42, s49, s41\n" \
"v_mfma_f32_16x16x32_bf16 a[184:187], v[108:111], v[72:75], a[184:187]\n" \
"ds_read_b128 v[136:139], v29 offset:34816\n" \
"s_lshl_b32 s4, s4, 16\n" \
"v_mfma_f32_16x16x32_bf16 a[156:159], v[112:115], v[68:71], a[156:159]\n" \
"s_or_b32 s41, s42, s4\n" \
"s_lshl_b32 s4, s6, 16\n" \
"v_mfma_f32_16x16x32_bf16 a[188:191], v[112:115], v[72:75], a[188:191]\n" \
"ds_read_b128 v[140:143], v29 offset:36864\n" \
"s_or_b32 s42, s42, s4\n" \
"v_mfma_f32_16x16x32_bf16 a[220:223], v[112:115], v[76:79], a[220:223]\n" \
"s_lshl_b32 s4, s5, 11\n" \
"s_mul_i32 s5, s7, 0x24800\n" \
"v_mfma_f32_16x16x32_bf16 a[252:255], v[112:115], v[80:83], a[252:255]\n" \
"ds_read_b128 v[144:147], v29 offset:38912\n" \
"s_lshl_b32 s6, s46, 9\n" \
"v_mfma_f32_16x16x32_bf16 a[216:219], v[108:111], v[76:79], a[216:219]\n" \
"s_lshl_b32 s7, s40, 6\n" \
"s_add_i32 s6, s5, s6\n" \
"v_mfma_f32_16x16x32_bf16 a[248:251], v[108:111], v[80:83], a[248:251]\n" \
"s_or_b32 s5, s5, s7\n" \
"s_lshl_b32 s7, s45, 2\n" \
"v_mfma_f32_16x16x32_bf16 a[212:215], v[104:107], v[76:79], a[212:215]\n" \
"s_or_b32 s5, s5, s7\n" \
"s_lshl_b32 s7, s3, 12\n" \
"v_mfma_f32_16x16x32_bf16 a[244:247], v[104:107], v[80:83], a[244:247]\n" \
"s_or_b32 s50, s49, s7\n" \
"s_lshl_b32 s7, s3, 11\n" \
"v_mfma_f32_16x16x32_bf16 a[208:211], v[100:103], v[76:79], a[208:211]\n" \
"v_lshrrev_b32_e32 v215, 4, v20\n" \
"v_bfe_u32 v216, v20, 4, 3\n" \
"v_mfma_f32_16x16x32_bf16 a[240:243], v[100:103], v[80:83], a[240:243]\n" \
"s_add_i32 s52, s48, s7\n" \
"s_add_i32 s53, s53, s7\n" \
"v_mfma_f32_16x16x32_bf16 a[204:207], v[96:99], v[76:79], a[204:207]\n" \
"v_mul_u32_u24_e32 v214, 0x480, v214\n" \
"v_bitop3_b32 v218, v213, v215, 7 bitop3:0x78\n" \
"v_mfma_f32_16x16x32_bf16 a[236:239], v[96:99], v[80:83], a[236:239]\n" \
"v_bitop3_b32 v213, v213, v216, 4 bitop3:0x36\n" \
"s_lshl_b32 s7, s3, 10\n" \
"v_mfma_f32_16x16x32_bf16 a[200:203], v[92:95], v[76:79], a[200:203]\n" \
"s_or_b32 s4, s43, s4\n" \
"v_lshl_or_b32 v25, v213, 4, v214\n" \
"v_mfma_f32_16x16x32_bf16 a[232:235], v[92:95], v[80:83], a[232:235]\n" \
"v_mul_u32_u24_e32 v213, 0x480, v217\n" \
"v_bitop3_b32 v212, v215, v212, 7 bitop3:0x6c\n" \
"v_mfma_f32_16x16x32_bf16 a[196:199], v[88:91], v[76:79], a[196:199]\n" \
"s_or_b32 s40, s7, 0x14000\n" \
"s_lshl_b32 s43, s44, 13\n" \
"v_mfma_f32_16x16x32_bf16 a[228:231], v[88:91], v[80:83], a[228:231]\n" \
"s_mulk_i32 s44, 0xe800\n" \
"s_lshl_b32 s7, s3, 8\n" \
"v_mfma_f32_16x16x32_bf16 a[192:195], v[84:87], v[76:79], a[192:195]\n" \
"v_lshl_or_b32 v24, v218, 4, v214\n" \
"v_lshl_or_b32 v21, v212, 4, v213\n" \
"v_mfma_f32_16x16x32_bf16 a[224:227], v[84:87], v[80:83], a[224:227]\n" \
"s_waitcnt vmcnt(1) lgkmcnt(0)\n" \
"s_barrier\n" \
"v_mov_b32_e32 v45, s62\n" \
"s_mov_b64 exec, 1\n" \
"buffer_store_dword v45, off, s[80:83], s37 offset:128 sc1\n" \
"s_mov_b64 exec, -1\n" \
"v_mfma_f32_16x16x32_bf16 a[0:3], v[148:151], v[116:119], a[0:3]\n" \
"s_add_u32 m0, s30, 0x8000\n" \
"s_mov_b32 s85, 0x380\n" \
"v_mfma_f32_16x16x32_bf16 a[32:35], v[148:151], v[120:123], a[32:35]\n" \
"buffer_load_dwordx4 v22, s[64:67], s85 offen lds\n" \
"v_mfma_f32_16x16x32_bf16 a[4:7], v[152:155], v[116:119], a[4:7]\n" \
"ds_read_b64_tr_b16 v[84:85], v32 offset:0\n" \
"s_add_i32 s44, s44, 0x17000\n" \
"v_mfma_f32_16x16x32_bf16 a[36:39], v[152:155], v[120:123], a[36:39]\n" \
"ds_read_b64_tr_b16 v[86:87], v32 offset:1024\n" \
"s_or_b32 s45, s7, 0x27000\n" \
"v_mfma_f32_16x16x32_bf16 a[8:11], v[156:159], v[116:119], a[8:11]\n" \
"s_add_u32 m0, s30, 0x8400\n" \
"s_mov_b32 s84, 0x2380\n" \
"v_mfma_f32_16x16x32_bf16 a[40:43], v[156:159], v[120:123], a[40:43]\n" \
"buffer_load_dwordx4 v23, s[64:67], s84 offen lds\n" \
"v_mfma_f32_16x16x32_bf16 a[12:15], v[160:163], v[116:119], a[12:15]\n" \
"ds_read_b128 v[52:55], v28 offset:16384\n" \
"s_or_b32 s46, s4, 0x10000\n" \
"v_mfma_f32_16x16x32_bf16 a[44:47], v[160:163], v[120:123], a[44:47]\n" \
"ds_read_b128 v[56:59], v28 offset:18432\n" \
"s_add_i32 s48, s6, 0x3000000\n" \
"v_mfma_f32_16x16x32_bf16 a[16:19], v[164:167], v[116:119], a[16:19]\n" \
"s_add_u32 m0, s31, 0x8000\n" \
"s_mov_b32 s85, 0x380000\n" \
"v_mfma_f32_16x16x32_bf16 a[48:51], v[164:167], v[120:123], a[48:51]\n" \
"buffer_load_dwordx4 v26, s[68:71], s85 offen lds\n" \
"v_mfma_f32_16x16x32_bf16 a[20:23], v[168:171], v[116:119], a[20:23]\n" \
"ds_read_b64_tr_b16 v[88:89], v33 offset:0\n" \
"s_add_i32 s49, s5, 0x3004000\n" \
"v_mfma_f32_16x16x32_bf16 a[52:55], v[168:171], v[120:123], a[52:55]\n" \
"ds_read_b64_tr_b16 v[90:91], v33 offset:1024\n" \
"s_add_i32 s52, s52, 0x1010000\n" \
"v_mfma_f32_16x16x32_bf16 a[24:27], v[172:175], v[116:119], a[24:27]\n" \
"s_add_u32 m0, s31, 0xc000\n" \
"s_mov_b32 s84, 0x380000\n" \
"v_mfma_f32_16x16x32_bf16 a[56:59], v[172:175], v[120:123], a[56:59]\n" \
"buffer_load_dwordx4 v26, s[72:75], s84 offen lds\n" \
"v_mfma_f32_16x16x32_bf16 a[28:31], v[176:179], v[116:119], a[28:31]\n" \
"ds_read_b64_tr_b16 v[92:93], v34 offset:0\n" \
"v_mfma_f32_16x16x32_bf16 a[60:63], v[176:179], v[120:123], a[60:63]\n" \
"ds_read_b64_tr_b16 v[94:95], v34 offset:1024\n" \
"v_mfma_f32_16x16x32_bf16 a[92:95], v[176:179], v[124:127], a[92:95]\n" \
"s_add_u32 m0, s30, 0x8800\n" \
"s_mov_b32 s85, 0x4380\n" \
"v_mfma_f32_16x16x32_bf16 a[124:127], v[176:179], v[128:131], a[124:127]\n" \
"buffer_load_dwordx4 v22, s[64:67], s85 offen lds\n" \
"v_mfma_f32_16x16x32_bf16 a[88:91], v[172:175], v[124:127], a[88:91]\n" \
"ds_read_b64_tr_b16 v[96:97], v35 offset:0\n" \
"v_mfma_f32_16x16x32_bf16 a[120:123], v[172:175], v[128:131], a[120:123]\n" \
"ds_read_b64_tr_b16 v[98:99], v35 offset:1024\n" \
"v_mfma_f32_16x16x32_bf16 a[84:87], v[168:171], v[124:127], a[84:87]\n" \
"s_add_u32 m0, s30, 0x8c00\n" \
"s_mov_b32 s84, 0x6380\n" \
"v_mfma_f32_16x16x32_bf16 a[116:119], v[168:171], v[128:131], a[116:119]\n" \
"buffer_load_dwordx4 v23, s[64:67], s84 offen lds\n" \
"v_mfma_f32_16x16x32_bf16 a[80:83], v[164:167], v[124:127], a[80:83]\n" \
"ds_read_b64_tr_b16 v[100:101], v36 offset:0\n" \
"v_mfma_f32_16x16x32_bf16 a[112:115], v[164:167], v[128:131], a[112:115]\n" \
"ds_read_b64_tr_b16 v[102:103], v36 offset:1024\n" \
"v_mfma_f32_16x16x32_bf16 a[76:79], v[160:163], v[124:127], a[76:79]\n" \
"s_add_u32 m0, s31, 0x8400\n" \
"s_mov_b32 s85, 0x388000\n" \
"v_mfma_f32_16x16x32_bf16 a[108:111], v[160:163], v[128:131], a[108:111]\n" \
"buffer_load_dwordx4 v26, s[68:71], s85 offen lds\n" \
"v_mfma_f32_16x16x32_bf16 a[72:75], v[156:159], v[124:127], a[72:75]\n" \
"ds_read_b64_tr_b16 v[104:105], v37 offset:0\n" \
"v_mfma_f32_16x16x32_bf16 a[104:107], v[156:159], v[128:131], a[104:107]\n" \
"v_mfma_f32_16x16x32_bf16 a[68:71], v[152:155], v[124:127], a[68:71]\n" \
"s_add_u32 m0, s31, 0xc400\n" \
"s_mov_b32 s84, 0x388000\n" \
"v_mfma_f32_16x16x32_bf16 a[100:103], v[152:155], v[128:131], a[100:103]\n" \
"buffer_load_dwordx4 v26, s[72:75], s84 offen lds\n" \
"v_mfma_f32_16x16x32_bf16 a[64:67], v[148:151], v[124:127], a[64:67]\n" \
"ds_read_b64_tr_b16 v[106:107], v37 offset:1024\n" \
"v_mfma_f32_16x16x32_bf16 a[96:99], v[148:151], v[128:131], a[96:99]\n" \
"ds_read_b64_tr_b16 v[108:109], v38 offset:0\n" \
"v_mfma_f32_16x16x32_bf16 a[128:131], v[148:151], v[132:135], a[128:131]\n" \
"s_add_u32 m0, s30, 0x9000\n" \
"s_mov_b32 s85, 0x8380\n" \
"v_mfma_f32_16x16x32_bf16 a[160:163], v[148:151], v[136:139], a[160:163]\n" \
"buffer_load_dwordx4 v22, s[64:67], s85 offen lds\n" \
"v_mfma_f32_16x16x32_bf16 a[132:135], v[152:155], v[132:135], a[132:135]\n" \
"ds_read_b64_tr_b16 v[110:111], v38 offset:1024\n" \
"v_mfma_f32_16x16x32_bf16 a[164:167], v[152:155], v[136:139], a[164:167]\n" \
"ds_read_b64_tr_b16 v[112:113], v39 offset:0\n" \
"v_mfma_f32_16x16x32_bf16 a[136:139], v[156:159], v[132:135], a[136:139]\n" \
"s_add_u32 m0, s30, 0x9400\n" \
"s_mov_b32 s84, 0xa380\n" \
"v_mfma_f32_16x16x32_bf16 a[168:171], v[156:159], v[136:139], a[168:171]\n" \
"buffer_load_dwordx4 v23, s[64:67], s84 offen lds\n" \
"v_mfma_f32_16x16x32_bf16 a[140:143], v[160:163], v[132:135], a[140:143]\n" \
"ds_read_b64_tr_b16 v[114:115], v39 offset:1024\n" \
"v_mfma_f32_16x16x32_bf16 a[172:175], v[160:163], v[136:139], a[172:175]\n" \
"ds_read_b128 v[60:63], v28 offset:20480\n" \
"v_mfma_f32_16x16x32_bf16 a[144:147], v[164:167], v[132:135], a[144:147]\n" \
"s_add_u32 m0, s31, 0x8800\n" \
"s_mov_b32 s85, 0x390000\n" \
"v_mfma_f32_16x16x32_bf16 a[176:179], v[164:167], v[136:139], a[176:179]\n" \
"buffer_load_dwordx4 v27, s[68:71], s85 offen lds\n" \
"v_mfma_f32_16x16x32_bf16 a[148:151], v[168:171], v[132:135], a[148:151]\n" \
"ds_read_b128 v[64:67], v28 offset:22528\n" \
"v_mfma_f32_16x16x32_bf16 a[180:183], v[168:171], v[136:139], a[180:183]\n" \
"ds_read_b128 v[68:71], v28 offset:0\n" \
"v_mfma_f32_16x16x32_bf16 a[152:155], v[172:175], v[132:135], a[152:155]\n" \
"s_add_u32 m0, s31, 0xc800\n" \
"s_mov_b32 s84, 0x390000\n" \
"v_mfma_f32_16x16x32_bf16 a[184:187], v[172:175], v[136:139], a[184:187]\n" \
"buffer_load_dwordx4 v27, s[72:75], s84 offen lds\n" \
"v_mfma_f32_16x16x32_bf16 a[156:159], v[176:179], v[132:135], a[156:159]\n" \
"ds_read_b128 v[72:75], v28 offset:2048\n" \
"v_mfma_f32_16x16x32_bf16 a[188:191], v[176:179], v[136:139], a[188:191]\n" \
"ds_read_b128 v[76:79], v28 offset:4096\n" \
"v_mfma_f32_16x16x32_bf16 a[220:223], v[176:179], v[140:143], a[220:223]\n" \
"v_mfma_f32_16x16x32_bf16 a[252:255], v[176:179], v[144:147], a[252:255]\n" \
"ds_read_b128 v[80:83], v28 offset:6144\n" \
"v_mfma_f32_16x16x32_bf16 a[216:219], v[172:175], v[140:143], a[216:219]\n" \
"v_mfma_f32_16x16x32_bf16 a[248:251], v[172:175], v[144:147], a[248:251]\n" \
"v_mfma_f32_16x16x32_bf16 a[212:215], v[168:171], v[140:143], a[212:215]\n" \
"v_mfma_f32_16x16x32_bf16 a[244:247], v[168:171], v[144:147], a[244:247]\n" \
"v_mfma_f32_16x16x32_bf16 a[208:211], v[164:167], v[140:143], a[208:211]\n" \
"v_mfma_f32_16x16x32_bf16 a[240:243], v[164:167], v[144:147], a[240:243]\n" \
"v_mfma_f32_16x16x32_bf16 a[204:207], v[160:163], v[140:143], a[204:207]\n" \
"v_mfma_f32_16x16x32_bf16 a[236:239], v[160:163], v[144:147], a[236:239]\n" \
"v_mfma_f32_16x16x32_bf16 a[200:203], v[156:159], v[140:143], a[200:203]\n" \
"v_mfma_f32_16x16x32_bf16 a[232:235], v[156:159], v[144:147], a[232:235]\n" \
"v_mfma_f32_16x16x32_bf16 a[196:199], v[152:155], v[140:143], a[196:199]\n" \
"v_mfma_f32_16x16x32_bf16 a[228:231], v[152:155], v[144:147], a[228:231]\n" \
"v_mfma_f32_16x16x32_bf16 a[192:195], v[148:151], v[140:143], a[192:195]\n" \
"v_mfma_f32_16x16x32_bf16 a[224:227], v[148:151], v[144:147], a[224:227]\n" \
"s_waitcnt lgkmcnt(15)\n" \
"s_nop 0\n" \
"v_mfma_f32_16x16x32_bf16 a[0:3], v[84:87], v[52:55], a[0:3]\n" \
"s_add_u32 m0, s30, 0x9800\n" \
"s_mov_b32 s85, 0xc380\n" \
"v_mfma_f32_16x16x32_bf16 a[32:35], v[84:87], v[56:59], a[32:35]\n" \
"buffer_load_dwordx4 v22, s[64:67], s85 offen lds\n" \
"v_mfma_f32_16x16x32_bf16 a[4:7], v[88:91], v[52:55], a[4:7]\n" \
"ds_read_b64_tr_b16 v[148:149], v32 offset:8192\n" \
"v_mfma_f32_16x16x32_bf16 a[36:39], v[88:91], v[56:59], a[36:39]\n" \
"s_add_u32 m0, s30, 0x9c00\n" \
"s_mov_b32 s84, 0xe380\n" \
"v_mfma_f32_16x16x32_bf16 a[8:11], v[92:95], v[52:55], a[8:11]\n" \
"buffer_load_dwordx4 v23, s[64:67], s84 offen lds\n" \
"v_mfma_f32_16x16x32_bf16 a[40:43], v[92:95], v[56:59], a[40:43]\n" \
"ds_read_b64_tr_b16 v[150:151], v32 offset:9216\n" \
"s_waitcnt lgkmcnt(15)\n" \
"v_mfma_f32_16x16x32_bf16 a[12:15], v[96:99], v[52:55], a[12:15]\n" \
"s_add_u32 m0, s31, 0x8c00\n" \
"s_mov_b32 s85, 0x398000\n" \
"v_mfma_f32_16x16x32_bf16 a[44:47], v[96:99], v[56:59], a[44:47]\n" \
"buffer_load_dwordx4 v27, s[68:71], s85 offen lds\n" \
"s_waitcnt lgkmcnt(14)\n" \
"v_mfma_f32_16x16x32_bf16 a[16:19], v[100:103], v[52:55], a[16:19]\n" \
"ds_read_b128 v[116:119], v29 offset:16384\n" \
"v_mfma_f32_16x16x32_bf16 a[48:51], v[100:103], v[56:59], a[48:51]\n" \
"s_add_u32 m0, s31, 0xcc00\n" \
"s_mov_b32 s84, 0x398000\n" \
"s_waitcnt lgkmcnt(13)\n" \
"v_mfma_f32_16x16x32_bf16 a[20:23], v[104:107], v[52:55], a[20:23]\n" \
"buffer_load_dwordx4 v27, s[72:75], s84 offen lds\n" \
"v_mfma_f32_16x16x32_bf16 a[52:55], v[104:107], v[56:59], a[52:55]\n" \
"ds_read_b128 v[120:123], v29 offset:18432\n" \
"s_waitcnt lgkmcnt(12)\n" \
"v_mfma_f32_16x16x32_bf16 a[24:27], v[108:111], v[52:55], a[24:27]\n" \
"ds_read_b64_tr_b16 v[152:153], v33 offset:8192\n" \
"v_mfma_f32_16x16x32_bf16 a[56:59], v[108:111], v[56:59], a[56:59]\n" \
"ds_read_b64_tr_b16 v[154:155], v33 offset:9216\n" \
"s_waitcnt lgkmcnt(12)\n" \
"v_mfma_f32_16x16x32_bf16 a[28:31], v[112:115], v[52:55], a[28:31]\n" \
"ds_read_b64_tr_b16 v[156:157], v34 offset:8192\n" \
"v_mfma_f32_16x16x32_bf16 a[60:63], v[112:115], v[56:59], a[60:63]\n" \
"ds_read_b64_tr_b16 v[158:159], v34 offset:9216\n" \
"s_waitcnt lgkmcnt(13)\n" \
"v_mfma_f32_16x16x32_bf16 a[92:95], v[112:115], v[60:63], a[92:95]\n" \
"s_waitcnt lgkmcnt(12)\n" \
"s_nop 0\n" \
"v_mfma_f32_16x16x32_bf16 a[124:127], v[112:115], v[64:67], a[124:127]\n" \
"ds_read_b64_tr_b16 v[160:161], v35 offset:8192\n" \
"v_mfma_f32_16x16x32_bf16 a[88:91], v[108:111], v[60:63], a[88:91]\n" \
"v_mfma_f32_16x16x32_bf16 a[120:123], v[108:111], v[64:67], a[120:123]\n" \
"ds_read_b64_tr_b16 v[162:163], v35 offset:9216\n" \
"v_mfma_f32_16x16x32_bf16 a[84:87], v[104:107], v[60:63], a[84:87]\n" \
"v_mfma_f32_16x16x32_bf16 a[116:119], v[104:107], v[64:67], a[116:119]\n" \
"ds_read_b64_tr_b16 v[164:165], v36 offset:8192\n" \
"v_mfma_f32_16x16x32_bf16 a[80:83], v[100:103], v[60:63], a[80:83]\n" \
"v_mfma_f32_16x16x32_bf16 a[112:115], v[100:103], v[64:67], a[112:115]\n" \
"ds_read_b64_tr_b16 v[166:167], v36 offset:9216\n" \
"v_mfma_f32_16x16x32_bf16 a[76:79], v[96:99], v[60:63], a[76:79]\n" \
"v_mfma_f32_16x16x32_bf16 a[108:111], v[96:99], v[64:67], a[108:111]\n" \
"v_mfma_f32_16x16x32_bf16 a[72:75], v[92:95], v[60:63], a[72:75]\n" \
"ds_read_b64_tr_b16 v[168:169], v37 offset:8192\n" \
"v_mfma_f32_16x16x32_bf16 a[104:107], v[92:95], v[64:67], a[104:107]\n" \
"v_mfma_f32_16x16x32_bf16 a[68:71], v[88:91], v[60:63], a[68:71]\n" \
"ds_read_b64_tr_b16 v[170:171], v37 offset:9216\n" \
"v_mfma_f32_16x16x32_bf16 a[100:103], v[88:91], v[64:67], a[100:103]\n" \
"v_mfma_f32_16x16x32_bf16 a[64:67], v[84:87], v[60:63], a[64:67]\n" \
"ds_read_b64_tr_b16 v[172:173], v38 offset:8192\n" \
"v_mfma_f32_16x16x32_bf16 a[96:99], v[84:87], v[64:67], a[96:99]\n" \
"s_waitcnt lgkmcnt(15)\n" \
"s_nop 0\n" \
"v_mfma_f32_16x16x32_bf16 a[128:131], v[84:87], v[68:71], a[128:131]\n" \
"ds_read_b64_tr_b16 v[174:175], v38 offset:9216\n" \
"v_mfma_f32_16x16x32_bf16 a[160:163], v[84:87], v[72:75], a[160:163]\n" \
"v_mfma_f32_16x16x32_bf16 a[132:135], v[88:91], v[68:71], a[132:135]\n" \
"ds_read_b64_tr_b16 v[176:177], v39 offset:8192\n" \
"v_mfma_f32_16x16x32_bf16 a[164:167], v[88:91], v[72:75], a[164:167]\n" \
"v_mfma_f32_16x16x32_bf16 a[136:139], v[92:95], v[68:71], a[136:139]\n" \
"ds_read_b64_tr_b16 v[178:179], v39 offset:9216\n" \
"v_mfma_f32_16x16x32_bf16 a[168:171], v[92:95], v[72:75], a[168:171]\n" \
"v_mfma_f32_16x16x32_bf16 a[140:143], v[96:99], v[68:71], a[140:143]\n" \
"v_bfe_u32 v44, v20, 2, 5\n" \
"v_mfma_f32_16x16x32_bf16 a[172:175], v[96:99], v[72:75], a[172:175]\n" \
"buffer_load_dword v44, v44, s[80:83], s38 offen offset:128 sc1\n" \
"v_mfma_f32_16x16x32_bf16 a[144:147], v[100:103], v[68:71], a[144:147]\n" \
"ds_read_b128 v[124:127], v29 offset:20480\n" \
"v_mfma_f32_16x16x32_bf16 a[176:179], v[100:103], v[72:75], a[176:179]\n" \
"ds_read_b128 v[128:131], v29 offset:22528\n" \
"v_mfma_f32_16x16x32_bf16 a[148:151], v[104:107], v[68:71], a[148:151]\n" \
"v_mfma_f32_16x16x32_bf16 a[180:183], v[104:107], v[72:75], a[180:183]\n" \
"ds_read_b128 v[132:135], v29 offset:0\n" \
"v_mfma_f32_16x16x32_bf16 a[152:155], v[108:111], v[68:71], a[152:155]\n" \
"v_mfma_f32_16x16x32_bf16 a[184:187], v[108:111], v[72:75], a[184:187]\n" \
"ds_read_b128 v[136:139], v29 offset:2048\n" \
"v_mfma_f32_16x16x32_bf16 a[156:159], v[112:115], v[68:71], a[156:159]\n" \
"v_mfma_f32_16x16x32_bf16 a[188:191], v[112:115], v[72:75], a[188:191]\n" \
"ds_read_b128 v[140:143], v29 offset:4096\n" \
"v_mfma_f32_16x16x32_bf16 a[220:223], v[112:115], v[76:79], a[220:223]\n" \
"v_mfma_f32_16x16x32_bf16 a[252:255], v[112:115], v[80:83], a[252:255]\n" \
"ds_read_b128 v[144:147], v29 offset:6144\n" \
"v_mfma_f32_16x16x32_bf16 a[216:219], v[108:111], v[76:79], a[216:219]\n" \
"v_mfma_f32_16x16x32_bf16 a[248:251], v[108:111], v[80:83], a[248:251]\n" \
"v_mfma_f32_16x16x32_bf16 a[212:215], v[104:107], v[76:79], a[212:215]\n" \
"v_mfma_f32_16x16x32_bf16 a[244:247], v[104:107], v[80:83], a[244:247]\n" \
"v_mfma_f32_16x16x32_bf16 a[208:211], v[100:103], v[76:79], a[208:211]\n" \
"v_mfma_f32_16x16x32_bf16 a[240:243], v[100:103], v[80:83], a[240:243]\n" \
"v_mfma_f32_16x16x32_bf16 a[204:207], v[96:99], v[76:79], a[204:207]\n" \
"v_mfma_f32_16x16x32_bf16 a[236:239], v[96:99], v[80:83], a[236:239]\n" \
"v_mfma_f32_16x16x32_bf16 a[200:203], v[92:95], v[76:79], a[200:203]\n" \
"v_mfma_f32_16x16x32_bf16 a[232:235], v[92:95], v[80:83], a[232:235]\n" \
"v_mfma_f32_16x16x32_bf16 a[196:199], v[88:91], v[76:79], a[196:199]\n" \
"v_mfma_f32_16x16x32_bf16 a[228:231], v[88:91], v[80:83], a[228:231]\n" \
"v_mfma_f32_16x16x32_bf16 a[192:195], v[84:87], v[76:79], a[192:195]\n" \
"v_mfma_f32_16x16x32_bf16 a[224:227], v[84:87], v[80:83], a[224:227]\n" \
"s_waitcnt vmcnt(0) lgkmcnt(0)\n" \
"s_barrier\n" \
"v_cmp_ne_u32_e32 vcc, s62, v44\n" \
"s_nop 4\n" \
"s_cbranch_vccnz L_poll60_%=\n" \
"L_pollr60_%=:\n" \
"s_mov_b32 s68, s12\n" \
"s_and_b32 s69, s13, 0xffff\n" \
"s_mov_b32 s70, 0x480000\n" \
"s_mov_b32 s71, 0x20000\n" \
"v_mfma_f32_16x16x32_bf16 a[0:3], v[148:151], v[116:119], a[0:3]\n" \
"s_add_u32 m0, s40, 0x0\n" \
"s_add_u32 s85, s39, 0x0\n" \
"v_mfma_f32_16x16x32_bf16 a[32:35], v[148:151], v[120:123], a[32:35]\n" \
"buffer_load_dwordx4 v20, s[80:83], s85 offen sc1 lds\n" \
"v_mfma_f32_16x16x32_bf16 a[4:7], v[152:155], v[116:119], a[4:7]\n" \
"ds_read_b64_tr_b16 v[84:85], v32 offset:32768\n" \
"v_mfma_f32_16x16x32_bf16 a[36:39], v[152:155], v[120:123], a[36:39]\n" \
"ds_read_b64_tr_b16 v[86:87], v32 offset:33792\n" \
"v_mfma_f32_16x16x32_bf16 a[8:11], v[156:159], v[116:119], a[8:11]\n" \
"s_add_u32 m0, s40, 0x1000\n" \
"s_add_u32 s84, s39, 0x1000\n" \
"v_mfma_f32_16x16x32_bf16 a[40:43], v[156:159], v[120:123], a[40:43]\n" \
"buffer_load_dwordx4 v20, s[80:83], s84 offen sc1 lds\n" \
"v_mfma_f32_16x16x32_bf16 a[12:15], v[160:163], v[116:119], a[12:15]\n" \
"ds_read_b128 v[52:55], v28 offset:49152\n" \
"v_mfma_f32_16x16x32_bf16 a[44:47], v[160:163], v[120:123], a[44:47]\n" \
"ds_read_b128 v[56:59], v28 offset:51200\n" \
"v_mfma_f32_16x16x32_bf16 a[16:19], v[164:167], v[116:119], a[16:19]\n" \
"s_add_u32 m0, s40, 0x3000\n" \
"s_mov_b32 s85, 0x0\n" \
"v_mfma_f32_16x16x32_bf16 a[48:51], v[164:167], v[120:123], a[48:51]\n" \
"buffer_load_dwordx4 v21, s[76:79], s85 offen lds\n" \
"v_mfma_f32_16x16x32_bf16 a[20:23], v[168:171], v[116:119], a[20:23]\n" \
"ds_read_b64_tr_b16 v[88:89], v33 offset:32768\n" \
"v_mfma_f32_16x16x32_bf16 a[52:55], v[168:171], v[120:123], a[52:55]\n" \
"ds_read_b64_tr_b16 v[90:91], v33 offset:33792\n" \
"v_mfma_f32_16x16x32_bf16 a[24:27], v[172:175], v[116:119], a[24:27]\n" \
"s_add_u32 m0, s30, 0x0\n" \
"s_mov_b32 s84, 0x0\n" \
"s_nop 0\n" \
"v_mfma_f32_16x16x32_bf16 a[56:59], v[172:175], v[120:123], a[56:59]\n" \
"buffer_load_dwordx4 v24, s[76:79], s84 offen lds\n" \
"v_mfma_f32_16x16x32_bf16 a[28:31], v[176:179], v[116:119], a[28:31]\n" \
"ds_read_b64_tr_b16 v[92:93], v34 offset:32768\n" \
"v_mfma_f32_16x16x32_bf16 a[60:63], v[176:179], v[120:123], a[60:63]\n" \
"ds_read_b64_tr_b16 v[94:95], v34 offset:33792\n" \
"v_mfma_f32_16x16x32_bf16 a[92:95], v[176:179], v[124:127], a[92:95]\n" \
"s_add_u32 m0, s30, 0x400\n" \
"s_mov_b32 s85, 0x2400\n" \
"v_mfma_f32_16x16x32_bf16 a[124:127], v[176:179], v[128:131], a[124:127]\n" \
"buffer_load_dwordx4 v25, s[76:79], s85 offen lds\n" \
"v_mfma_f32_16x16x32_bf16 a[88:91], v[172:175], v[124:127], a[88:91]\n" \
"ds_read_b64_tr_b16 v[96:97], v35 offset:32768\n" \
"v_mfma_f32_16x16x32_bf16 a[120:123], v[172:175], v[128:131], a[120:123]\n" \
"ds_read_b64_tr_b16 v[98:99], v35 offset:33792\n" \
"v_mfma_f32_16x16x32_bf16 a[84:87], v[168:171], v[124:127], a[84:87]\n" \
"s_add_u32 m0, s31, 0x0\n" \
"s_mov_b32 s84, 0x0\n" \
"v_mfma_f32_16x16x32_bf16 a[116:119], v[168:171], v[128:131], a[116:119]\n" \
"buffer_load_dwordx4 v26, s[68:71], s84 offen lds\n" \
"v_mfma_f32_16x16x32_bf16 a[80:83], v[164:167], v[124:127], a[80:83]\n" \
"ds_read_b64_tr_b16 v[100:101], v36 offset:32768\n" \
"v_mfma_f32_16x16x32_bf16 a[112:115], v[164:167], v[128:131], a[112:115]\n" \
"ds_read_b64_tr_b16 v[102:103], v36 offset:33792\n" \
"v_mfma_f32_16x16x32_bf16 a[76:79], v[160:163], v[124:127], a[76:79]\n" \
"s_add_u32 m0, s30, 0x800\n" \
"s_mov_b32 s85, 0x4800\n" \
"v_mfma_f32_16x16x32_bf16 a[108:111], v[160:163], v[128:131], a[108:111]\n" \
"buffer_load_dwordx4 v24, s[76:79], s85 offen lds\n" \
"v_mfma_f32_16x16x32_bf16 a[72:75], v[156:159], v[124:127], a[72:75]\n" \
"ds_read_b64_tr_b16 v[104:105], v37 offset:32768\n" \
"v_mfma_f32_16x16x32_bf16 a[104:107], v[156:159], v[128:131], a[104:107]\n" \
"v_mfma_f32_16x16x32_bf16 a[68:71], v[152:155], v[124:127], a[68:71]\n" \
"s_add_u32 m0, s30, 0xc00\n" \
"s_mov_b32 s84, 0x6c00\n" \
"v_mfma_f32_16x16x32_bf16 a[100:103], v[152:155], v[128:131], a[100:103]\n" \
"buffer_load_dwordx4 v25, s[76:79], s84 offen lds\n" \
"v_mfma_f32_16x16x32_bf16 a[64:67], v[148:151], v[124:127], a[64:67]\n" \
"ds_read_b64_tr_b16 v[106:107], v37 offset:33792\n" \
"v_mfma_f32_16x16x32_bf16 a[96:99], v[148:151], v[128:131], a[96:99]\n" \
"ds_read_b64_tr_b16 v[108:109], v38 offset:32768\n" \
"v_mfma_f32_16x16x32_bf16 a[128:131], v[148:151], v[132:135], a[128:131]\n" \
"s_add_u32 m0, s31, 0x400\n" \
"s_mov_b32 s85, 0x8000\n" \
"v_mfma_f32_16x16x32_bf16 a[160:163], v[148:151], v[136:139], a[160:163]\n" \
"buffer_load_dwordx4 v26, s[68:71], s85 offen lds\n" \
"v_mfma_f32_16x16x32_bf16 a[132:135], v[152:155], v[132:135], a[132:135]\n" \
"ds_read_b64_tr_b16 v[110:111], v38 offset:33792\n" \
"v_mfma_f32_16x16x32_bf16 a[164:167], v[152:155], v[136:139], a[164:167]\n" \
"ds_read_b64_tr_b16 v[112:113], v39 offset:32768\n" \
"v_mfma_f32_16x16x32_bf16 a[136:139], v[156:159], v[132:135], a[136:139]\n" \
"s_add_u32 m0, s30, 0x1000\n" \
"s_mov_b32 s84, 0x9000\n" \
"v_mfma_f32_16x16x32_bf16 a[168:171], v[156:159], v[136:139], a[168:171]\n" \
"buffer_load_dwordx4 v24, s[76:79], s84 offen lds\n" \
"v_mfma_f32_16x16x32_bf16 a[140:143], v[160:163], v[132:135], a[140:143]\n" \
"ds_read_b64_tr_b16 v[114:115], v39 offset:33792\n" \
"v_mfma_f32_16x16x32_bf16 a[172:175], v[160:163], v[136:139], a[172:175]\n" \
"ds_read_b128 v[60:63], v28 offset:53248\n" \
"v_mfma_f32_16x16x32_bf16 a[144:147], v[164:167], v[132:135], a[144:147]\n" \
"s_add_u32 m0, s30, 0x1400\n" \
"s_mov_b32 s85, 0xb400\n" \
"v_mfma_f32_16x16x32_bf16 a[176:179], v[164:167], v[136:139], a[176:179]\n" \
"buffer_load_dwordx4 v25, s[76:79], s85 offen lds\n" \
"v_mfma_f32_16x16x32_bf16 a[148:151], v[168:171], v[132:135], a[148:151]\n" \
"ds_read_b128 v[64:67], v28 offset:55296\n" \
"v_mfma_f32_16x16x32_bf16 a[180:183], v[168:171], v[136:139], a[180:183]\n" \
"ds_read_b128 v[68:71], v28 offset:32768\n" \
"v_mfma_f32_16x16x32_bf16 a[152:155], v[172:175], v[132:135], a[152:155]\n" \
"s_add_u32 m0, s31, 0x800\n" \
"s_mov_b32 s84, 0x10000\n" \
"v_mfma_f32_16x16x32_bf16 a[184:187], v[172:175], v[136:139], a[184:187]\n" \
"buffer_load_dwordx4 v27, s[68:71], s84 offen lds\n" \
"v_mfma_f32_16x16x32_bf16 a[156:159], v[176:179], v[132:135], a[156:159]\n" \
"ds_read_b128 v[72:75], v28 offset:34816\n" \
"v_mfma_f32_16x16x32_bf16 a[188:191], v[176:179], v[136:139], a[188:191]\n" \
"ds_read_b128 v[76:79], v28 offset:36864\n" \
"v_mfma_f32_16x16x32_bf16 a[220:223], v[176:179], v[140:143], a[220:223]\n" \
"v_mfma_f32_16x16x32_bf16 a[252:255], v[176:179], v[144:147], a[252:255]\n" \
"ds_read_b128 v[80:83], v28 offset:38912\n" \
"v_mfma_f32_16x16x32_bf16 a[216:219], v[172:175], v[140:143], a[216:219]\n" \
"v_mfma_f32_16x16x32_bf16 a[248:251], v[172:175], v[144:147], a[248:251]\n" \
"v_mfma_f32_16x16x32_bf16 a[212:215], v[168:171], v[140:143], a[212:215]\n" \
"v_mfma_f32_16x16x32_bf16 a[244:247], v[168:171], v[144:147], a[244:247]\n" \
"v_mfma_f32_16x16x32_bf16 a[208:211], v[164:167], v[140:143], a[208:211]\n" \
"v_mfma_f32_16x16x32_bf16 a[240:243], v[164:167], v[144:147], a[240:243]\n" \
"v_mfma_f32_16x16x32_bf16 a[204:207], v[160:163], v[140:143], a[204:207]\n" \
"v_mfma_f32_16x16x32_bf16 a[236:239], v[160:163], v[144:147], a[236:239]\n" \
"v_mfma_f32_16x16x32_bf16 a[200:203], v[156:159], v[140:143], a[200:203]\n" \
"v_mfma_f32_16x16x32_bf16 a[232:235], v[156:159], v[144:147], a[232:235]\n" \
"v_mfma_f32_16x16x32_bf16 a[196:199], v[152:155], v[140:143], a[196:199]\n" \
"v_mfma_f32_16x16x32_bf16 a[228:231], v[152:155], v[144:147], a[228:231]\n" \
"v_mfma_f32_16x16x32_bf16 a[192:195], v[148:151], v[140:143], a[192:195]\n" \
"v_mfma_f32_16x16x32_bf16 a[224:227], v[148:151], v[144:147], a[224:227]\n" \
"s_waitcnt lgkmcnt(15)\n" \
"s_nop 0\n" \
"v_mfma_f32_16x16x32_bf16 a[0:3], v[84:87], v[52:55], a[0:3]\n" \
"s_add_u32 m0, s30, 0x1800\n" \
"s_mov_b32 s84, 0xd800\n" \
"v_mfma_f32_16x16x32_bf16 a[32:35], v[84:87], v[56:59], a[32:35]\n" \
"buffer_load_dwordx4 v24, s[76:79], s84 offen lds\n" \
"v_mfma_f32_16x16x32_bf16 a[4:7], v[88:91], v[52:55], a[4:7]\n" \
"ds_read_b64_tr_b16 v[148:149], v32 offset:40960\n" \
"v_mfma_f32_16x16x32_bf16 a[36:39], v[88:91], v[56:59], a[36:39]\n" \
"s_add_u32 m0, s30, 0x1c00\n" \
"s_mov_b32 s85, 0xfc00\n" \
"v_mfma_f32_16x16x32_bf16 a[8:11], v[92:95], v[52:55], a[8:11]\n" \
"buffer_load_dwordx4 v25, s[76:79], s85 offen lds\n" \
"v_mfma_f32_16x16x32_bf16 a[40:43], v[92:95], v[56:59], a[40:43]\n" \
"ds_read_b64_tr_b16 v[150:151], v32 offset:41984\n" \
"s_waitcnt lgkmcnt(15)\n" \
"v_mfma_f32_16x16x32_bf16 a[12:15], v[96:99], v[52:55], a[12:15]\n" \
"s_add_u32 m0, s31, 0xc00\n" \
"s_mov_b32 s84, 0x18000\n" \
"v_mfma_f32_16x16x32_bf16 a[44:47], v[96:99], v[56:59], a[44:47]\n" \
"buffer_load_dwordx4 v27, s[68:71], s84 offen lds\n" \
"s_waitcnt lgkmcnt(14)\n" \
"v_mfma_f32_16x16x32_bf16 a[16:19], v[100:103], v[52:55], a[16:19]\n" \
"ds_read_b128 v[116:119], v29 offset:49152\n" \
"v_mfma_f32_16x16x32_bf16 a[48:51], v[100:103], v[56:59], a[48:51]\n" \
"ds_read_b128 v[120:123], v29 offset:51200\n" \
"s_waitcnt lgkmcnt(14)\n" \
"v_mfma_f32_16x16x32_bf16 a[20:23], v[104:107], v[52:55], a[20:23]\n" \
"ds_read_b64_tr_b16 v[152:153], v33 offset:40960\n" \
"v_mfma_f32_16x16x32_bf16 a[52:55], v[104:107], v[56:59], a[52:55]\n" \
"ds_read_b64_tr_b16 v[154:155], v33 offset:41984\n" \
"s_waitcnt lgkmcnt(14)\n" \
"v_mfma_f32_16x16x32_bf16 a[24:27], v[108:111], v[52:55], a[24:27]\n" \
"v_mfma_f32_16x16x32_bf16 a[56:59], v[108:111], v[56:59], a[56:59]\n" \
"ds_read_b64_tr_b16 v[156:157], v34 offset:40960\n" \
"s_waitcnt lgkmcnt(13)\n" \
"v_mfma_f32_16x16x32_bf16 a[28:31], v[112:115], v[52:55], a[28:31]\n" \
"v_mfma_f32_16x16x32_bf16 a[60:63], v[112:115], v[56:59], a[60:63]\n" \
"ds_read_b64_tr_b16 v[158:159], v34 offset:41984\n" \
"s_waitcnt lgkmcnt(13)\n" \
"v_mfma_f32_16x16x32_bf16 a[92:95], v[112:115], v[60:63], a[92:95]\n" \
"s_waitcnt lgkmcnt(12)\n" \
"v_mfma_f32_16x16x32_bf16 a[124:127], v[112:115], v[64:67], a[124:127]\n" \
"ds_read_b64_tr_b16 v[160:161], v35 offset:40960\n" \
"v_mfma_f32_16x16x32_bf16 a[88:91], v[108:111], v[60:63], a[88:91]\n" \
"v_lshlrev_b32_e32 v45, 3, v20\n" \
"v_add_u32_e64 v45, s31, v45\n" \
"v_mfma_f32_16x16x32_bf16 a[120:123], v[108:111], v[64:67], a[120:123]\n" \
"s_getpc_b64 s[86:87]\n" \
"s_sub_u32 s86, s86, 0xf800\n" \
"s_subb_u32 s87, s87, 0\n" \
"s_mov_b32 m0, s32\n" \
"s_nop 0\n" \
"v_mfma_f32_16x16x32_bf16 a[84:87], v[104:107], v[60:63], a[84:87]\n" \
"global_load_lds_dword v45, s[86:87]\n" \
"v_mfma_f32_16x16x32_bf16 a[116:119], v[104:107], v[64:67], a[116:119]\n" \
"ds_read_b64_tr_b16 v[162:163], v35 offset:41984\n" \
"v_mfma_f32_16x16x32_bf16 a[80:83], v[100:103], v[60:63], a[80:83]\n" \
"ds_read_b64_tr_b16 v[164:165], v36 offset:40960\n" \
"v_mfma_f32_16x16x32_bf16 a[112:115], v[100:103], v[64:67], a[112:115]\n" \
"ds_read_b64_tr_b16 v[166:167], v36 offset:41984\n" \
"v_mfma_f32_16x16x32_bf16 a[76:79], v[96:99], v[60:63], a[76:79]\n" \
"v_mfma_f32_16x16x32_bf16 a[108:111], v[96:99], v[64:67], a[108:111]\n" \
"v_mfma_f32_16x16x32_bf16 a[72:75], v[92:95], v[60:63], a[72:75]\n" \
"ds_read_b64_tr_b16 v[168:169], v37 offset:40960\n" \
"v_mfma_f32_16x16x32_bf16 a[104:107], v[92:95], v[64:67], a[104:107]\n" \
"v_mfma_f32_16x16x32_bf16 a[68:71], v[88:91], v[60:63], a[68:71]\n" \
"ds_read_b64_tr_b16 v[170:171], v37 offset:41984\n" \
"v_mfma_f32_16x16x32_bf16 a[100:103], v[88:91], v[64:67], a[100:103]\n" \
"v_mfma_f32_16x16x32_bf16 a[64:67], v[84:87], v[60:63], a[64:67]\n" \
"ds_read_b64_tr_b16 v[172:173], v38 offset:40960\n" \
"v_mfma_f32_16x16x32_bf16 a[96:99], v[84:87], v[64:67], a[96:99]\n" \
"s_waitcnt lgkmcnt(15)\n" \
"s_nop 0\n" \
"v_mfma_f32_16x16x32_bf16 a[128:131], v[84:87], v[68:71], a[128:131]\n" \
"ds_read_b64_tr_b16 v[174:175], v38 offset:41984\n" \
"v_mfma_f32_16x16x32_bf16 a[160:163], v[84:87], v[72:75], a[160:163]\n" \
"v_mfma_f32_16x16x32_bf16 a[132:135], v[88:91], v[68:71], a[132:135]\n" \
"ds_read_b64_tr_b16 v[176:177], v39 offset:40960\n" \
"v_mfma_f32_16x16x32_bf16 a[164:167], v[88:91], v[72:75], a[164:167]\n" \
"v_mfma_f32_16x16x32_bf16 a[136:139], v[92:95], v[68:71], a[136:139]\n" \
"ds_read_b64_tr_b16 v[178:179], v39 offset:41984\n" \
"v_mfma_f32_16x16x32_bf16 a[168:171], v[92:95], v[72:75], a[168:171]\n" \
"v_mfma_f32_16x16x32_bf16 a[140:143], v[96:99], v[68:71], a[140:143]\n" \
"v_mfma_f32_16x16x32_bf16 a[172:175], v[96:99], v[72:75], a[172:175]\n" \
"ds_read_b128 v[124:127], v29 offset:53248\n" \
"v_mfma_f32_16x16x32_bf16 a[144:147], v[100:103], v[68:71], a[144:147]\n" \
"v_mfma_f32_16x16x32_bf16 a[176:179], v[100:103], v[72:75], a[176:179]\n" \
"ds_read_b128 v[128:131], v29 offset:55296\n" \
"v_mfma_f32_16x16x32_bf16 a[148:151], v[104:107], v[68:71], a[148:151]\n" \
"v_mfma_f32_16x16x32_bf16 a[180:183], v[104:107], v[72:75], a[180:183]\n" \
"ds_read_b128 v[132:135], v29 offset:32768\n" \
"v_mfma_f32_16x16x32_bf16 a[152:155], v[108:111], v[68:71], a[152:155]\n" \
"v_mfma_f32_16x16x32_bf16 a[184:187], v[108:111], v[72:75], a[184:187]\n" \
"ds_read_b128 v[136:139], v29 offset:34816\n" \
"v_mfma_f32_16x16x32_bf16 a[156:159], v[112:115], v[68:71], a[156:159]\n" \
"v_mfma_f32_16x16x32_bf16 a[188:191], v[112:115], v[72:75], a[188:191]\n" \
"ds_read_b128 v[140:143], v29 offset:36864\n" \
"v_mfma_f32_16x16x32_bf16 a[220:223], v[112:115], v[76:79], a[220:223]\n" \
"v_mfma_f32_16x16x32_bf16 a[252:255], v[112:115], v[80:83], a[252:255]\n" \
"ds_read_b128 v[144:147], v29 offset:38912\n" \
"v_mfma_f32_16x16x32_bf16 a[216:219], v[108:111], v[76:79], a[216:219]\n" \
"v_mfma_f32_16x16x32_bf16 a[248:251], v[108:111], v[80:83], a[248:251]\n" \
"v_mfma_f32_16x16x32_bf16 a[212:215], v[104:107], v[76:79], a[212:215]\n" \
"v_mfma_f32_16x16x32_bf16 a[244:247], v[104:107], v[80:83], a[244:247]\n" \
"v_mfma_f32_16x16x32_bf16 a[208:211], v[100:103], v[76:79], a[208:211]\n" \
"v_mfma_f32_16x16x32_bf16 a[240:243], v[100:103], v[80:83], a[240:243]\n" \
"v_mfma_f32_16x16x32_bf16 a[204:207], v[96:99], v[76:79], a[204:207]\n" \
"v_mfma_f32_16x16x32_bf16 a[236:239], v[96:99], v[80:83], a[236:239]\n" \
"v_mfma_f32_16x16x32_bf16 a[200:203], v[92:95], v[76:79], a[200:203]\n" \
"v_mfma_f32_16x16x32_bf16 a[232:235], v[92:95], v[80:83], a[232:235]\n" \
"v_mfma_f32_16x16x32_bf16 a[196:199], v[88:91], v[76:79], a[196:199]\n" \
"v_mfma_f32_16x16x32_bf16 a[228:231], v[88:91], v[80:83], a[228:231]\n" \
"v_mfma_f32_16x16x32_bf16 a[192:195], v[84:87], v[76:79], a[192:195]\n" \
"v_mfma_f32_16x16x32_bf16 a[224:227], v[84:87], v[80:83], a[224:227]\n" \
"s_waitcnt vmcnt(1) lgkmcnt(0)\n" \
"s_barrier\n" \
"v_add_u32_e32 v22, s44, v28\n" \
"v_xor_b32_e32 v36, 0x0, v190\n" \
"v_add_u32_e32 v23, s44, v29\n" \
"v_xor_b32_e32 v37, 0x20, v190\n" \
"v_add_u32_e32 v22, s44, v28\n" \
"v_xor_b32_e32 v38, 0x40, v190\n" \
"v_add_u32_e64 v23, s44, v29\n" \
"v_xor_b32_e32 v39, 0x60, v190\n" \
"v_add_u32_e32 v28, s43, v28\n" \
"v_add_u32_e32 v29, s43, v29\n" \
"v_mfma_f32_16x16x32_bf16 a[0:3], v[148:151], v[116:119], a[0:3]\n" \
"s_add_u32 m0, s40, 0x8000\n" \
"s_add_u32 s85, s39, 0x2000\n" \
"v_mfma_f32_16x16x32_bf16 a[32:35], v[148:151], v[120:123], a[32:35]\n" \
"buffer_load_dwordx4 v20, s[80:83], s85 offen sc1 lds\n" \
"v_mfma_f32_16x16x32_bf16 a[4:7], v[152:155], v[116:119], a[4:7]\n" \
"ds_read_b64_tr_b16 v[104:105], v37 offset:0\n" \
"v_mfma_f32_16x16x32_bf16 a[36:39], v[152:155], v[120:123], a[36:39]\n" \
"ds_read_b64_tr_b16 v[106:107], v37 offset:512\n" \
"v_mfma_f32_16x16x32_bf16 a[8:11], v[156:159], v[116:119], a[8:11]\n" \
"s_add_u32 m0, s40, 0x9000\n" \
"s_add_u32 s84, s39, 0x3000\n" \
"v_mfma_f32_16x16x32_bf16 a[40:43], v[156:159], v[120:123], a[40:43]\n" \
"buffer_load_dwordx4 v20, s[80:83], s84 offen sc1 lds\n" \
"v_accvgpr_read_b32 v184, a0\n" \
"v_accvgpr_read_b32 v185, a1\n" \
"v_mfma_f32_16x16x32_bf16 a[12:15], v[160:163], v[116:119], a[12:15]\n" \
"ds_read_b64_tr_b16 v[108:109], v38 offset:0\n" \
"v_accvgpr_read_b32 v186, a2\n" \
"v_accvgpr_read_b32 v187, a3\n" \
"v_accvgpr_read_b32 v180, a4\n" \
"v_mfma_f32_16x16x32_bf16 a[44:47], v[160:163], v[120:123], a[44:47]\n" \
"ds_read_b64_tr_b16 v[110:111], v38 offset:512\n" \
"v_accvgpr_read_b32 v181, a5\n" \
"v_accvgpr_read_b32 v182, a6\n" \
"v_accvgpr_read_b32 v183, a7\n" \
"v_mfma_f32_16x16x32_bf16 a[16:19], v[164:167], v[116:119], a[16:19]\n" \
"s_add_u32 m0, s40, 0xb000\n" \
"s_mov_b32 s85, 0x80\n" \
"v_cvt_pk_bf16_f32 v184, v184, v185\n" \
"v_cvt_pk_bf16_f32 v185, v186, v187\n" \
"v_mfma_f32_16x16x32_bf16 a[48:51], v[164:167], v[120:123], a[48:51]\n" \
"buffer_load_dwordx4 v21, s[76:79], s85 offen lds\n" \
"v_cvt_pk_bf16_f32 v186, v180, v181\n" \
"v_cvt_pk_bf16_f32 v187, v182, v183\n" \
"v_mfma_f32_16x16x32_bf16 a[20:23], v[168:171], v[116:119], a[20:23]\n" \
"ds_read_b64_tr_b16 v[112:113], v39 offset:0\n" \
"v_accvgpr_read_b32 v188, a8\n" \
"v_accvgpr_read_b32 v189, a9\n" \
"v_accvgpr_read_b32 v190, a10\n" \
"v_mfma_f32_16x16x32_bf16 a[52:55], v[168:171], v[120:123], a[52:55]\n" \
"ds_read_b64_tr_b16 v[114:115], v39 offset:512\n" \
"v_accvgpr_read_b32 v191, a11\n" \
"v_accvgpr_read_b32 v180, a12\n" \
"v_accvgpr_read_b32 v181, a13\n" \
"v_mfma_f32_16x16x32_bf16 a[24:27], v[172:175], v[116:119], a[24:27]\n" \
"s_add_u32 m0, s30, 0x8000\n" \
"s_mov_b32 s84, 0x80\n" \
"v_accvgpr_read_b32 v182, a14\n" \
"v_accvgpr_read_b32 v183, a15\n" \
"v_mfma_f32_16x16x32_bf16 a[56:59], v[172:175], v[120:123], a[56:59]\n" \
"buffer_load_dwordx4 v24, s[76:79], s84 offen lds\n" \
"v_cvt_pk_bf16_f32 v188, v188, v189\n" \
"v_cvt_pk_bf16_f32 v189, v190, v191\n" \
"v_mfma_f32_16x16x32_bf16 a[28:31], v[176:179], v[116:119], a[28:31]\n" \
"ds_read_b64_tr_b16 v[84:85], v32 offset:0\n" \
"v_cvt_pk_bf16_f32 v190, v180, v181\n" \
"v_cvt_pk_bf16_f32 v191, v182, v183\n" \
"v_accvgpr_read_b32 v192, a16\n" \
"v_mfma_f32_16x16x32_bf16 a[60:63], v[176:179], v[120:123], a[60:63]\n" \
"ds_read_b64_tr_b16 v[86:87], v32 offset:1024\n" \
"v_accvgpr_read_b32 v193, a17\n" \
"v_accvgpr_read_b32 v194, a18\n" \
"v_accvgpr_read_b32 v195, a19\n" \
"v_mfma_f32_16x16x32_bf16 a[92:95], v[176:179], v[124:127], a[92:95]\n" \
"s_add_u32 m0, s30, 0x8400\n" \
"s_mov_b32 s85, 0x2480\n" \
"v_accvgpr_read_b32 v180, a20\n" \
"v_accvgpr_read_b32 v181, a21\n" \
"v_mfma_f32_16x16x32_bf16 a[124:127], v[176:179], v[128:131], a[124:127]\n" \
"buffer_load_dwordx4 v25, s[76:79], s85 offen lds\n" \
"v_accvgpr_read_b32 v182, a22\n" \
"v_accvgpr_read_b32 v183, a23\n" \
"v_mfma_f32_16x16x32_bf16 a[88:91], v[172:175], v[124:127], a[88:91]\n" \
"ds_read_b128 v[248:251], v22 offset:0\n" \
"v_cvt_pk_bf16_f32 v192, v192, v193\n" \
"v_cvt_pk_bf16_f32 v193, v194, v195\n" \
"v_cvt_pk_bf16_f32 v194, v180, v181\n" \
"v_mfma_f32_16x16x32_bf16 a[120:123], v[172:175], v[128:131], a[120:123]\n" \
"ds_read_b64_tr_b16 v[88:89], v33 offset:0\n" \
"v_cvt_pk_bf16_f32 v195, v182, v183\n" \
"v_accvgpr_read_b32 v196, a24\n" \
"v_accvgpr_read_b32 v197, a25\n" \
"v_mfma_f32_16x16x32_bf16 a[84:87], v[168:171], v[124:127], a[84:87]\n" \
"s_add_u32 m0, s31, 0x8000\n" \
"s_mov_b32 s84, 0x80000\n" \
"v_accvgpr_read_b32 v198, a26\n" \
"v_accvgpr_read_b32 v199, a27\n" \
"v_mfma_f32_16x16x32_bf16 a[116:119], v[168:171], v[128:131], a[116:119]\n" \
"buffer_load_dwordx4 v26, s[68:71], s84 offen lds\n" \
"v_accvgpr_read_b32 v180, a28\n" \
"v_accvgpr_read_b32 v181, a29\n" \
"v_mfma_f32_16x16x32_bf16 a[80:83], v[164:167], v[124:127], a[80:83]\n" \
"ds_read_b64_tr_b16 v[90:91], v33 offset:1024\n" \
"v_accvgpr_read_b32 v182, a30\n" \
"v_accvgpr_read_b32 v183, a31\n" \
"v_cvt_pk_bf16_f32 v196, v196, v197\n" \
"v_mfma_f32_16x16x32_bf16 a[112:115], v[164:167], v[128:131], a[112:115]\n" \
"ds_read_b64_tr_b16 v[92:93], v34 offset:0\n" \
"v_cvt_pk_bf16_f32 v197, v198, v199\n" \
"v_cvt_pk_bf16_f32 v198, v180, v181\n" \
"v_cvt_pk_bf16_f32 v199, v182, v183\n" \
"v_mfma_f32_16x16x32_bf16 a[76:79], v[160:163], v[124:127], a[76:79]\n" \
"s_add_u32 m0, s30, 0x8800\n" \
"s_mov_b32 s85, 0x4880\n" \
"v_accvgpr_read_b32 v200, a32\n" \
"v_accvgpr_read_b32 v201, a33\n" \
"v_mfma_f32_16x16x32_bf16 a[108:111], v[160:163], v[128:131], a[108:111]\n" \
"buffer_load_dwordx4 v24, s[76:79], s85 offen lds\n" \
"v_accvgpr_read_b32 v202, a34\n" \
"v_accvgpr_read_b32 v203, a35\n" \
"v_mfma_f32_16x16x32_bf16 a[72:75], v[156:159], v[124:127], a[72:75]\n" \
"ds_read_b64_tr_b16 v[94:95], v34 offset:1024\n" \
"v_accvgpr_read_b32 v180, a36\n" \
"v_accvgpr_read_b32 v181, a37\n" \
"v_accvgpr_read_b32 v182, a38\n" \
"v_mfma_f32_16x16x32_bf16 a[104:107], v[156:159], v[128:131], a[104:107]\n" \
"ds_read_b64_tr_b16 v[96:97], v35 offset:0\n" \
"v_accvgpr_read_b32 v183, a39\n" \
"v_cvt_pk_bf16_f32 v200, v200, v201\n" \
"v_cvt_pk_bf16_f32 v201, v202, v203\n" \
"v_mfma_f32_16x16x32_bf16 a[68:71], v[152:155], v[124:127], a[68:71]\n" \
"s_add_u32 m0, s30, 0x8c00\n" \
"s_mov_b32 s84, 0x6c80\n" \
"v_cvt_pk_bf16_f32 v202, v180, v181\n" \
"v_cvt_pk_bf16_f32 v203, v182, v183\n" \
"v_mfma_f32_16x16x32_bf16 a[100:103], v[152:155], v[128:131], a[100:103]\n" \
"buffer_load_dwordx4 v25, s[76:79], s84 offen lds\n" \
"v_accvgpr_read_b32 v204, a40\n" \
"v_accvgpr_read_b32 v205, a41\n" \
"v_mfma_f32_16x16x32_bf16 a[64:67], v[148:151], v[124:127], a[64:67]\n" \
"ds_read_b64_tr_b16 v[98:99], v35 offset:1024\n" \
"v_accvgpr_read_b32 v206, a42\n" \
"v_accvgpr_read_b32 v207, a43\n" \
"v_accvgpr_read_b32 v180, a44\n" \
"v_mfma_f32_16x16x32_bf16 a[96:99], v[148:151], v[128:131], a[96:99]\n" \
"ds_read_b64_tr_b16 v[100:101], v36 offset:0\n" \
"v_accvgpr_read_b32 v181, a45\n" \
"v_accvgpr_read_b32 v182, a46\n" \
"v_accvgpr_read_b32 v183, a47\n" \
"v_mfma_f32_16x16x32_bf16 a[128:131], v[148:151], v[132:135], a[128:131]\n" \
"s_add_u32 m0, s31, 0x8400\n" \
"s_mov_b32 s85, 0x88000\n" \
"v_cvt_pk_bf16_f32 v204, v204, v205\n" \
"v_cvt_pk_bf16_f32 v205, v206, v207\n" \
"v_mfma_f32_16x16x32_bf16 a[160:163], v[148:151], v[136:139], a[160:163]\n" \
"buffer_load_dwordx4 v26, s[68:71], s85 offen lds\n" \
"v_cvt_pk_bf16_f32 v206, v180, v181\n" \
"v_cvt_pk_bf16_f32 v207, v182, v183\n" \
"v_mfma_f32_16x16x32_bf16 a[132:135], v[152:155], v[132:135], a[132:135]\n" \
"ds_read_b64_tr_b16 v[102:103], v36 offset:512\n" \
"v_accvgpr_read_b32 v208, a48\n" \
"v_accvgpr_read_b32 v209, a49\n" \
"v_accvgpr_read_b32 v210, a50\n" \
"v_mfma_f32_16x16x32_bf16 a[164:167], v[152:155], v[136:139], a[164:167]\n" \
"ds_read_b128 v[52:55], v28 offset:0\n" \
"v_accvgpr_read_b32 v211, a51\n" \
"v_accvgpr_read_b32 v180, a52\n" \
"v_accvgpr_read_b32 v181, a53\n" \
"v_mfma_f32_16x16x32_bf16 a[136:139], v[156:159], v[132:135], a[136:139]\n" \
"s_add_u32 m0, s30, 0x9000\n" \
"s_mov_b32 s84, 0x9080\n" \
"v_accvgpr_read_b32 v182, a54\n" \
"v_accvgpr_read_b32 v183, a55\n" \
"v_mfma_f32_16x16x32_bf16 a[168:171], v[156:159], v[136:139], a[168:171]\n" \
"buffer_load_dwordx4 v24, s[76:79], s84 offen lds\n" \
"v_cvt_pk_bf16_f32 v208, v208, v209\n" \
"v_cvt_pk_bf16_f32 v209, v210, v211\n" \
"v_mfma_f32_16x16x32_bf16 a[140:143], v[160:163], v[132:135], a[140:143]\n" \
"ds_read_b128 v[56:59], v28 offset:2048\n" \
"v_cvt_pk_bf16_f32 v210, v180, v181\n" \
"v_cvt_pk_bf16_f32 v211, v182, v183\n" \
"v_accvgpr_read_b32 v212, a56\n" \
"v_mfma_f32_16x16x32_bf16 a[172:175], v[160:163], v[136:139], a[172:175]\n" \
"ds_read_b128 v[60:63], v28 offset:4096\n" \
"v_accvgpr_read_b32 v213, a57\n" \
"v_accvgpr_read_b32 v214, a58\n" \
"v_accvgpr_read_b32 v215, a59\n" \
"v_mfma_f32_16x16x32_bf16 a[144:147], v[164:167], v[132:135], a[144:147]\n" \
"s_add_u32 m0, s30, 0x9400\n" \
"s_mov_b32 s85, 0xb480\n" \
"v_accvgpr_read_b32 v180, a60\n" \
"v_accvgpr_read_b32 v181, a61\n" \
"v_mfma_f32_16x16x32_bf16 a[176:179], v[164:167], v[136:139], a[176:179]\n" \
"buffer_load_dwordx4 v25, s[76:79], s85 offen lds\n" \
"v_accvgpr_read_b32 v182, a62\n" \
"v_accvgpr_read_b32 v183, a63\n" \
"v_mfma_f32_16x16x32_bf16 a[148:151], v[168:171], v[132:135], a[148:151]\n" \
"ds_read_b128 v[64:67], v28 offset:6144\n" \
"v_cvt_pk_bf16_f32 v212, v212, v213\n" \
"v_cvt_pk_bf16_f32 v213, v214, v215\n" \
"v_cvt_pk_bf16_f32 v214, v180, v181\n" \
"v_mfma_f32_16x16x32_bf16 a[180:183], v[168:171], v[136:139], a[180:183]\n" \
"ds_read_b128 v[68:71], v28 offset:8192\n" \
"v_cvt_pk_bf16_f32 v215, v182, v183\n" \
"v_accvgpr_read_b32 v216, a64\n" \
"v_accvgpr_read_b32 v217, a65\n" \
"v_mfma_f32_16x16x32_bf16 a[152:155], v[172:175], v[132:135], a[152:155]\n" \
"s_add_u32 m0, s31, 0x8800\n" \
"s_mov_b32 s84, 0x90000\n" \
"v_accvgpr_read_b32 v218, a66\n" \
"v_accvgpr_read_b32 v219, a67\n" \
"v_mfma_f32_16x16x32_bf16 a[184:187], v[172:175], v[136:139], a[184:187]\n" \
"buffer_load_dwordx4 v27, s[68:71], s84 offen lds\n" \
"v_accvgpr_read_b32 v180, a68\n" \
"v_accvgpr_read_b32 v181, a69\n" \
"v_mfma_f32_16x16x32_bf16 a[156:159], v[176:179], v[132:135], a[156:159]\n" \
"ds_read_b128 v[72:75], v28 offset:10240\n" \
"v_accvgpr_read_b32 v182, a70\n" \
"v_accvgpr_read_b32 v183, a71\n" \
"v_cvt_pk_bf16_f32 v216, v216, v217\n" \
"v_mfma_f32_16x16x32_bf16 a[188:191], v[176:179], v[136:139], a[188:191]\n" \
"ds_read_b128 v[76:79], v28 offset:12288\n" \
"v_cvt_pk_bf16_f32 v217, v218, v219\n" \
"v_cvt_pk_bf16_f32 v218, v180, v181\n" \
"v_cvt_pk_bf16_f32 v219, v182, v183\n" \
"v_mfma_f32_16x16x32_bf16 a[220:223], v[176:179], v[140:143], a[220:223]\n" \
"v_accvgpr_read_b32 v220, a72\n" \
"v_accvgpr_read_b32 v221, a73\n" \
"v_accvgpr_read_b32 v222, a74\n" \
"v_accvgpr_read_b32 v223, a75\n" \
"v_mfma_f32_16x16x32_bf16 a[252:255], v[176:179], v[144:147], a[252:255]\n" \
"ds_read_b128 v[80:83], v28 offset:14336\n" \
"v_accvgpr_read_b32 v180, a76\n" \
"v_accvgpr_read_b32 v181, a77\n" \
"v_accvgpr_read_b32 v182, a78\n" \
"v_mfma_f32_16x16x32_bf16 a[216:219], v[172:175], v[140:143], a[216:219]\n" \
"v_accvgpr_read_b32 v183, a79\n" \
"v_cvt_pk_bf16_f32 v220, v220, v221\n" \
"v_cvt_pk_bf16_f32 v221, v222, v223\n" \
"v_cvt_pk_bf16_f32 v222, v180, v181\n" \
"v_mfma_f32_16x16x32_bf16 a[248:251], v[172:175], v[144:147], a[248:251]\n" \
"v_cvt_pk_bf16_f32 v223, v182, v183\n" \
"v_accvgpr_read_b32 v224, a80\n" \
"v_accvgpr_read_b32 v225, a81\n" \
"v_accvgpr_read_b32 v226, a82\n" \
"v_mfma_f32_16x16x32_bf16 a[212:215], v[168:171], v[140:143], a[212:215]\n" \
"v_accvgpr_read_b32 v227, a83\n" \
"v_accvgpr_read_b32 v180, a84\n" \
"v_accvgpr_read_b32 v181, a85\n" \
"v_accvgpr_read_b32 v182, a86\n" \
"v_mfma_f32_16x16x32_bf16 a[244:247], v[168:171], v[144:147], a[244:247]\n" \
"v_accvgpr_read_b32 v183, a87\n" \
"v_cvt_pk_bf16_f32 v224, v224, v225\n" \
"v_cvt_pk_bf16_f32 v225, v226, v227\n" \
"v_cvt_pk_bf16_f32 v226, v180, v181\n" \
"v_mfma_f32_16x16x32_bf16 a[208:211], v[164:167], v[140:143], a[208:211]\n" \
"v_cvt_pk_bf16_f32 v227, v182, v183\n" \
"v_accvgpr_read_b32 v228, a88\n" \
"v_accvgpr_read_b32 v229, a89\n" \
"v_accvgpr_read_b32 v230, a90\n" \
"v_mfma_f32_16x16x32_bf16 a[240:243], v[164:167], v[144:147], a[240:243]\n" \
"v_accvgpr_read_b32 v231, a91\n" \
"v_accvgpr_read_b32 v180, a92\n" \
"v_accvgpr_read_b32 v181, a93\n" \
"v_accvgpr_read_b32 v182, a94\n" \
"v_mfma_f32_16x16x32_bf16 a[204:207], v[160:163], v[140:143], a[204:207]\n" \
"v_accvgpr_read_b32 v183, a95\n" \
"v_cvt_pk_bf16_f32 v228, v228, v229\n" \
"v_cvt_pk_bf16_f32 v229, v230, v231\n" \
"v_cvt_pk_bf16_f32 v230, v180, v181\n" \
"v_mfma_f32_16x16x32_bf16 a[236:239], v[160:163], v[144:147], a[236:239]\n" \
"v_cvt_pk_bf16_f32 v231, v182, v183\n" \
"v_accvgpr_read_b32 v232, a96\n" \
"v_accvgpr_read_b32 v233, a97\n" \
"v_accvgpr_read_b32 v234, a98\n" \
"v_mfma_f32_16x16x32_bf16 a[200:203], v[156:159], v[140:143], a[200:203]\n" \
"v_accvgpr_read_b32 v235, a99\n" \
"v_accvgpr_read_b32 v180, a100\n" \
"v_accvgpr_read_b32 v181, a101\n" \
"v_accvgpr_read_b32 v182, a102\n" \
"v_mfma_f32_16x16x32_bf16 a[232:235], v[156:159], v[144:147], a[232:235]\n" \
"v_accvgpr_read_b32 v183, a103\n" \
"v_cvt_pk_bf16_f32 v232, v232, v233\n" \
"v_cvt_pk_bf16_f32 v233, v234, v235\n" \
"v_cvt_pk_bf16_f32 v234, v180, v181\n" \
"v_mfma_f32_16x16x32_bf16 a[196:199], v[152:155], v[140:143], a[196:199]\n" \
"v_cvt_pk_bf16_f32 v235, v182, v183\n" \
"v_accvgpr_read_b32 v236, a104\n" \
"v_accvgpr_read_b32 v237, a105\n" \
"v_accvgpr_read_b32 v238, a106\n" \
"v_mfma_f32_16x16x32_bf16 a[228:231], v[152:155], v[144:147], a[228:231]\n" \
"v_accvgpr_read_b32 v239, a107\n" \
"v_accvgpr_read_b32 v180, a108\n" \
"v_accvgpr_read_b32 v181, a109\n" \
"v_accvgpr_read_b32 v182, a110\n" \
"v_mfma_f32_16x16x32_bf16 a[192:195], v[148:151], v[140:143], a[192:195]\n" \
"v_accvgpr_read_b32 v183, a111\n" \
"v_cvt_pk_bf16_f32 v236, v236, v237\n" \
"v_cvt_pk_bf16_f32 v237, v238, v239\n" \
"v_cvt_pk_bf16_f32 v238, v180, v181\n" \
"v_mfma_f32_16x16x32_bf16 a[224:227], v[148:151], v[144:147], a[224:227]\n" \
"v_cvt_pk_bf16_f32 v239, v182, v183\n" \
"v_accvgpr_read_b32 v240, a112\n" \
"v_accvgpr_read_b32 v241, a113\n" \
"v_accvgpr_read_b32 v242, a114\n" \
"s_waitcnt lgkmcnt(15)\n" \
"s_nop 0\n" \
"v_mfma_f32_16x16x32_bf16 a[24:27], v[104:107], v[248:251], 0\n" \
"s_add_u32 m0, s30, 0x9800\n" \
"s_mov_b32 s84, 0xd880\n" \
"v_accvgpr_read_b32 v243, a115\n" \
"v_accvgpr_read_b32 v180, a116\n" \
"v_accvgpr_read_b32 v181, a117\n" \
"v_mfma_f32_16x16x32_bf16 a[28:31], v[248:251], v[108:111], 0\n" \
"buffer_load_dwordx4 v24, s[76:79], s84 offen lds\n" \
"v_accvgpr_read_b32 v182, a118\n" \
"v_accvgpr_read_b32 v183, a119\n" \
"v_cvt_pk_bf16_f32 v240, v240, v241\n" \
"v_mfma_f32_16x16x32_bf16 a[56:59], v[112:115], v[248:251], 0\n" \
"ds_read_b64_tr_b16 v[168:169], v37 offset:4096\n" \
"v_cvt_pk_bf16_f32 v241, v242, v243\n" \
"v_cvt_pk_bf16_f32 v242, v180, v181\n" \
"v_cvt_pk_bf16_f32 v243, v182, v183\n" \
"v_accvgpr_read_b32 v244, a120\n" \
"s_waitcnt lgkmcnt(8)\n" \
"v_mfma_f32_16x16x32_bf16 a[0:3], v[84:87], v[52:55], 0\n" \
"s_add_u32 m0, s30, 0x9c00\n" \
"s_mov_b32 s85, 0xfc80\n" \
"v_accvgpr_read_b32 v245, a121\n" \
"v_accvgpr_read_b32 v246, a122\n" \
"v_accvgpr_read_b32 v247, a123\n" \
"s_waitcnt lgkmcnt(7)\n" \
"v_mfma_f32_16x16x32_bf16 a[32:35], v[84:87], v[56:59], 0\n" \
"buffer_load_dwordx4 v25, s[76:79], s85 offen lds\n" \
"v_accvgpr_read_b32 v180, a124\n" \
"v_accvgpr_read_b32 v181, a125\n" \
"v_accvgpr_read_b32 v182, a126\n" \
"v_mfma_f32_16x16x32_bf16 a[4:7], v[88:91], v[52:55], 0\n" \
"ds_read_b64_tr_b16 v[170:171], v37 offset:4608\n" \
"v_accvgpr_read_b32 v183, a127\n" \
"v_cvt_pk_bf16_f32 v244, v244, v245\n" \
"v_cvt_pk_bf16_f32 v245, v246, v247\n" \
"v_cvt_pk_bf16_f32 v246, v180, v181\n" \
"v_mfma_f32_16x16x32_bf16 a[36:39], v[88:91], v[56:59], 0\n" \
"s_add_u32 m0, s31, 0x8c00\n" \
"s_mov_b32 s84, 0x98000\n" \
"v_cvt_pk_bf16_f32 v247, v182, v183\n" \
"v_accvgpr_read_b32 v252, a152\n" \
"v_accvgpr_read_b32 v253, a153\n" \
"v_mfma_f32_16x16x32_bf16 a[8:11], v[92:95], v[52:55], 0\n" \
"buffer_load_dwordx4 v27, s[68:71], s84 offen lds\n" \
"v_accvgpr_read_b32 v254, a154\n" \
"v_accvgpr_read_b32 v255, a155\n" \
"v_accvgpr_read_b32 v180, a156\n" \
"v_mfma_f32_16x16x32_bf16 a[40:43], v[92:95], v[56:59], 0\n" \
"ds_read_b64_tr_b16 v[172:173], v38 offset:4096\n" \
"v_accvgpr_read_b32 v181, a157\n" \
"v_accvgpr_read_b32 v182, a158\n" \
"v_accvgpr_read_b32 v183, a159\n" \
"v_cvt_pk_bf16_f32 v252, v252, v253\n" \
"v_mfma_f32_16x16x32_bf16 a[12:15], v[96:99], v[52:55], 0\n" \
"s_add_u32 s89, s41, 0x0\n" \
"s_nop 0\n" \
"buffer_store_dwordx4 v[184:187], v20, s[80:83], s89 offen offset:0 sc1\n" \
"v_cvt_pk_bf16_f32 v253, v254, v255\n" \
"v_cvt_pk_bf16_f32 v254, v180, v181\n" \
"v_mfma_f32_16x16x32_bf16 a[44:47], v[96:99], v[56:59], 0\n" \
"ds_read_b64_tr_b16 v[174:175], v38 offset:4608\n" \
"v_cvt_pk_bf16_f32 v255, v182, v183\n" \
"v_accvgpr_write_b32 a60, v252\n" \
"v_accvgpr_write_b32 a61, v253\n" \
"v_accvgpr_write_b32 a62, v254\n" \
"v_mfma_f32_16x16x32_bf16 a[16:19], v[100:103], v[52:55], 0\n" \
"ds_read_b64_tr_b16 v[176:177], v39 offset:4096\n" \
"v_accvgpr_write_b32 a63, v255\n" \
"v_accvgpr_read_b32 v252, a128\n" \
"v_accvgpr_read_b32 v253, a129\n" \
"v_accvgpr_read_b32 v254, a130\n" \
"v_mfma_f32_16x16x32_bf16 a[20:23], v[104:107], v[52:55], 0\n" \
"s_add_u32 s88, s41, 0x0\n" \
"s_nop 0\n" \
"buffer_store_dwordx4 v[188:191], v20, s[80:83], s88 offen offset:1024 sc1\n" \
"v_accvgpr_read_b32 v255, a131\n" \
"v_accvgpr_read_b32 v180, a132\n" \
"v_mfma_f32_16x16x32_bf16 a[52:55], v[112:115], v[52:55], 0\n" \
"ds_read_b64_tr_b16 v[178:179], v39 offset:4608\n" \
"v_accvgpr_read_b32 v181, a133\n" \
"v_accvgpr_read_b32 v182, a134\n" \
"v_accvgpr_read_b32 v183, a135\n" \
"v_cvt_pk_bf16_f32 v252, v252, v253\n" \
"v_mfma_f32_16x16x32_bf16 a[48:51], v[52:55], v[108:111], 0\n" \
"ds_read_b64_tr_b16 v[148:149], v32 offset:8192\n" \
"v_cvt_pk_bf16_f32 v253, v254, v255\n" \
"v_cvt_pk_bf16_f32 v254, v180, v181\n" \
"v_cvt_pk_bf16_f32 v255, v182, v183\n" \
"v_accvgpr_write_b32 a88, v252\n" \
"s_waitcnt lgkmcnt(12)\n" \
"v_mfma_f32_16x16x32_bf16 a[64:67], v[84:87], v[60:63], 0\n" \
"s_add_u32 s89, s41, 0x0\n" \
"buffer_store_dwordx4 v[192:195], v20, s[80:83], s89 offen offset:2048 sc1\n" \
"v_accvgpr_write_b32 a89, v253\n" \
"v_accvgpr_write_b32 a90, v254\n" \
"s_waitcnt lgkmcnt(11)\n" \
"s_nop 0\n" \
"v_mfma_f32_16x16x32_bf16 a[96:99], v[84:87], v[64:67], 0\n" \
"ds_read_b64_tr_b16 v[150:151], v32 offset:9216\n" \
"v_accvgpr_write_b32 a91, v255\n" \
"v_accvgpr_read_b32 v252, a136\n" \
"v_accvgpr_read_b32 v253, a137\n" \
"v_accvgpr_read_b32 v254, a138\n" \
"v_mfma_f32_16x16x32_bf16 a[68:71], v[88:91], v[60:63], 0\n" \
"ds_read_b128 v[248:251], v23 offset:0\n" \
"v_accvgpr_read_b32 v255, a139\n" \
"v_accvgpr_read_b32 v180, a140\n" \
"v_accvgpr_read_b32 v181, a141\n" \
"v_accvgpr_read_b32 v182, a142\n" \
"v_mfma_f32_16x16x32_bf16 a[100:103], v[88:91], v[64:67], 0\n" \
"s_add_u32 s88, s41, 0x0\n" \
"s_nop 0\n" \
"buffer_store_dwordx4 v[196:199], v20, s[80:83], s88 offen offset:3072 sc1\n" \
"v_accvgpr_read_b32 v183, a143\n" \
"v_cvt_pk_bf16_f32 v252, v252, v253\n" \
"v_mfma_f32_16x16x32_bf16 a[72:75], v[92:95], v[60:63], 0\n" \
"ds_read_b64_tr_b16 v[152:153], v33 offset:8192\n" \
"v_cvt_pk_bf16_f32 v253, v254, v255\n" \
"v_cvt_pk_bf16_f32 v254, v180, v181\n" \
"v_cvt_pk_bf16_f32 v255, v182, v183\n" \
"v_accvgpr_write_b32 a92, v252\n" \
"v_mfma_f32_16x16x32_bf16 a[104:107], v[92:95], v[64:67], 0\n" \
"ds_read_b64_tr_b16 v[154:155], v33 offset:9216\n" \
"v_accvgpr_write_b32 a93, v253\n" \
"v_accvgpr_write_b32 a94, v254\n" \
"v_accvgpr_write_b32 a95, v255\n" \
"v_accvgpr_read_b32 v252, a144\n" \
"v_mfma_f32_16x16x32_bf16 a[76:79], v[96:99], v[60:63], 0\n" \
"ds_read_b64_tr_b16 v[156:157], v34 offset:8192\n" \
"v_accvgpr_read_b32 v253, a145\n" \
"v_accvgpr_read_b32 v254, a146\n" \
"v_accvgpr_read_b32 v255, a147\n" \
"v_accvgpr_read_b32 v180, a148\n" \
"v_mfma_f32_16x16x32_bf16 a[108:111], v[96:99], v[64:67], 0\n" \
"ds_read_b64_tr_b16 v[158:159], v34 offset:9216\n" \
"v_accvgpr_read_b32 v181, a149\n" \
"v_accvgpr_read_b32 v182, a150\n" \
"v_accvgpr_read_b32 v183, a151\n" \
"v_cvt_pk_bf16_f32 v252, v252, v253\n" \
"v_mfma_f32_16x16x32_bf16 a[80:83], v[100:103], v[56:59], 0\n" \
"ds_read_b64_tr_b16 v[160:161], v35 offset:8192\n" \
"v_cvt_pk_bf16_f32 v253, v254, v255\n" \
"v_cvt_pk_bf16_f32 v254, v180, v181\n" \
"v_cvt_pk_bf16_f32 v255, v182, v183\n" \
"v_accvgpr_write_b32 a120, v252\n" \
"v_mfma_f32_16x16x32_bf16 a[84:87], v[104:107], v[56:59], 0\n" \
"ds_read_b64_tr_b16 v[162:163], v35 offset:9216\n" \
"v_accvgpr_write_b32 a121, v253\n" \
"v_accvgpr_write_b32 a122, v254\n" \
"v_accvgpr_write_b32 a123, v255\n" \
"v_accvgpr_read_b32 v252, a184\n" \
"v_mfma_f32_16x16x32_bf16 a[116:119], v[112:115], v[56:59], 0\n" \
"ds_read_b64_tr_b16 v[164:165], v36 offset:4096\n" \
"v_accvgpr_read_b32 v253, a185\n" \
"v_accvgpr_read_b32 v254, a186\n" \
"v_accvgpr_read_b32 v255, a187\n" \
"v_accvgpr_read_b32 v180, a188\n" \
"v_mfma_f32_16x16x32_bf16 a[112:115], v[56:59], v[108:111], 0\n" \
"ds_read_b64_tr_b16 v[166:167], v36 offset:4608\n" \
"v_accvgpr_read_b32 v181, a189\n" \
"v_accvgpr_read_b32 v182, a190\n" \
"v_accvgpr_read_b32 v183, a191\n" \
"v_cvt_pk_bf16_f32 v252, v252, v253\n" \
"s_waitcnt lgkmcnt(15)\n" \
"s_nop 0\n" \
"v_mfma_f32_16x16x32_bf16 a[128:131], v[84:87], v[68:71], 0\n" \
"ds_read_b128 v[116:119], v29 offset:0\n" \
"v_cvt_pk_bf16_f32 v253, v254, v255\n" \
"v_cvt_pk_bf16_f32 v254, v180, v181\n" \
"v_cvt_pk_bf16_f32 v255, v182, v183\n" \
"v_accvgpr_write_b32 a124, v252\n" \
"v_accvgpr_write_b32 a125, v253\n" \
"v_accvgpr_write_b32 a126, v254\n" \
"v_accvgpr_write_b32 a127, v255\n" \
"v_accvgpr_read_b32 v252, a160\n" \
"v_accvgpr_read_b32 v253, a161\n" \
"v_accvgpr_read_b32 v254, a162\n" \
"v_accvgpr_read_b32 v255, a163\n" \
"v_accvgpr_read_b32 v180, a164\n" \
"v_accvgpr_read_b32 v181, a165\n" \
"v_accvgpr_read_b32 v182, a166\n" \
"v_accvgpr_read_b32 v183, a167\n" \
"v_cvt_pk_bf16_f32 v252, v252, v253\n" \
"v_cvt_pk_bf16_f32 v253, v254, v255\n" \
"v_cvt_pk_bf16_f32 v254, v180, v181\n" \
"v_cvt_pk_bf16_f32 v255, v182, v183\n" \
"v_accvgpr_write_b32 a152, v252\n" \
"v_accvgpr_write_b32 a153, v253\n" \
"v_accvgpr_write_b32 a154, v254\n" \
"v_accvgpr_write_b32 a155, v255\n" \
"v_mfma_f32_16x16x32_bf16 a[160:163], v[84:87], v[72:75], 0\n" \
"ds_read_b128 v[120:123], v29 offset:2048\n" \
"v_accvgpr_read_b32 v252, a168\n" \
"v_accvgpr_read_b32 v253, a169\n" \
"v_accvgpr_read_b32 v254, a170\n" \
"v_accvgpr_read_b32 v255, a171\n" \
"v_mfma_f32_16x16x32_bf16 a[132:135], v[88:91], v[68:71], 0\n" \
"ds_read_b128 v[124:127], v29 offset:4096\n" \
"v_accvgpr_read_b32 v180, a172\n" \
"v_accvgpr_read_b32 v181, a173\n" \
"v_accvgpr_read_b32 v182, a174\n" \
"v_accvgpr_read_b32 v183, a175\n" \
"v_mfma_f32_16x16x32_bf16 a[164:167], v[88:91], v[72:75], 0\n" \
"v_cvt_pk_bf16_f32 v252, v252, v253\n" \
"v_cvt_pk_bf16_f32 v253, v254, v255\n" \
"v_cvt_pk_bf16_f32 v254, v180, v181\n" \
"v_cvt_pk_bf16_f32 v255, v182, v183\n" \
"v_accvgpr_write_b32 a156, v252\n" \
"v_mfma_f32_16x16x32_bf16 a[136:139], v[92:95], v[68:71], 0\n" \
"ds_read_b128 v[128:131], v29 offset:6144\n" \
"v_accvgpr_write_b32 a157, v253\n" \
"v_accvgpr_write_b32 a158, v254\n" \
"v_accvgpr_write_b32 a159, v255\n" \
"v_accvgpr_read_b32 v252, a176\n" \
"v_mfma_f32_16x16x32_bf16 a[168:171], v[92:95], v[72:75], 0\n" \
"ds_read_b128 v[132:135], v29 offset:8192\n" \
"v_accvgpr_read_b32 v253, a177\n" \
"v_accvgpr_read_b32 v254, a178\n" \
"v_accvgpr_read_b32 v255, a179\n" \
"v_accvgpr_read_b32 v180, a180\n" \
"v_mfma_f32_16x16x32_bf16 a[140:143], v[96:99], v[68:71], 0\n" \
"v_accvgpr_read_b32 v181, a181\n" \
"v_accvgpr_read_b32 v182, a182\n" \
"v_accvgpr_read_b32 v183, a183\n" \
"v_cvt_pk_bf16_f32 v252, v252, v253\n" \
"v_cvt_pk_bf16_f32 v253, v254, v255\n" \
"v_mfma_f32_16x16x32_bf16 a[172:175], v[96:99], v[72:75], 0\n" \
"ds_read_b128 v[136:139], v29 offset:10240\n" \
"v_cvt_pk_bf16_f32 v254, v180, v181\n" \
"v_cvt_pk_bf16_f32 v255, v182, v183\n" \
"v_accvgpr_write_b32 a184, v252\n" \
"v_accvgpr_write_b32 a185, v253\n" \
"v_mfma_f32_16x16x32_bf16 a[144:147], v[100:103], v[60:63], 0\n" \
"ds_read_b128 v[140:143], v29 offset:12288\n" \
"v_accvgpr_write_b32 a186, v254\n" \
"v_accvgpr_write_b32 a187, v255\n" \
"v_accvgpr_read_b32 v252, a216\n" \
"v_accvgpr_read_b32 v253, a217\n" \
"v_mfma_f32_16x16x32_bf16 a[148:151], v[104:107], v[60:63], 0\n" \
"v_accvgpr_read_b32 v254, a218\n" \
"v_accvgpr_read_b32 v255, a219\n" \
"v_accvgpr_read_b32 v180, a220\n" \
"v_accvgpr_read_b32 v181, a221\n" \
"v_accvgpr_read_b32 v182, a222\n" \
"v_mfma_f32_16x16x32_bf16 a[180:183], v[112:115], v[60:63], 0\n" \
"ds_read_b128 v[144:147], v29 offset:14336\n" \
"v_accvgpr_read_b32 v183, a223\n" \
"v_cvt_pk_bf16_f32 v252, v252, v253\n" \
"v_cvt_pk_bf16_f32 v253, v254, v255\n" \
"v_cvt_pk_bf16_f32 v254, v180, v181\n" \
"v_mfma_f32_16x16x32_bf16 a[176:179], v[60:63], v[108:111], 0\n" \
"v_cvt_pk_bf16_f32 v255, v182, v183\n" \
"v_accvgpr_write_b32 a188, v252\n" \
"v_accvgpr_write_b32 a189, v253\n" \
"v_accvgpr_write_b32 a190, v254\n" \
"v_accvgpr_write_b32 a191, v255\n" \
"v_accvgpr_read_b32 v252, a192\n" \
"v_accvgpr_read_b32 v253, a193\n" \
"v_accvgpr_read_b32 v254, a194\n" \
"v_accvgpr_read_b32 v255, a195\n" \
"v_accvgpr_read_b32 v180, a196\n" \
"v_accvgpr_read_b32 v181, a197\n" \
"v_accvgpr_read_b32 v182, a198\n" \
"v_accvgpr_read_b32 v183, a199\n" \
"v_cvt_pk_bf16_f32 v252, v252, v253\n" \
"v_cvt_pk_bf16_f32 v253, v254, v255\n" \
"v_cvt_pk_bf16_f32 v254, v180, v181\n" \
"v_cvt_pk_bf16_f32 v255, v182, v183\n" \
"v_accvgpr_write_b32 a216, v252\n" \
"v_accvgpr_write_b32 a217, v253\n" \
"v_accvgpr_write_b32 a218, v254\n" \
"v_accvgpr_write_b32 a219, v255\n" \
"v_mfma_f32_16x16x32_bf16 a[192:195], v[84:87], v[76:79], 0\n" \
"v_accvgpr_read_b32 v252, a200\n" \
"v_accvgpr_read_b32 v253, a201\n" \
"v_accvgpr_read_b32 v254, a202\n" \
"v_accvgpr_read_b32 v255, a203\n" \
"v_accvgpr_read_b32 v180, a204\n" \
"v_accvgpr_read_b32 v181, a205\n" \
"v_accvgpr_read_b32 v182, a206\n" \
"v_accvgpr_read_b32 v183, a207\n" \
"v_cvt_pk_bf16_f32 v252, v252, v253\n" \
"v_cvt_pk_bf16_f32 v253, v254, v255\n" \
"v_cvt_pk_bf16_f32 v254, v180, v181\n" \
"v_cvt_pk_bf16_f32 v255, v182, v183\n" \
"v_accvgpr_write_b32 a220, v252\n" \
"v_accvgpr_write_b32 a221, v253\n" \
"v_accvgpr_write_b32 a222, v254\n" \
"v_accvgpr_write_b32 a223, v255\n" \
"v_accvgpr_read_b32 v184, a208\n" \
"v_accvgpr_read_b32 v185, a209\n" \
"v_accvgpr_read_b32 v186, a210\n" \
"v_accvgpr_read_b32 v187, a211\n" \
"v_accvgpr_read_b32 v180, a212\n" \
"v_accvgpr_read_b32 v181, a213\n" \
"v_accvgpr_read_b32 v182, a214\n" \
"v_accvgpr_read_b32 v183, a215\n" \
"v_cvt_pk_bf16_f32 v184, v184, v185\n" \
"v_cvt_pk_bf16_f32 v185, v186, v187\n" \
"v_cvt_pk_bf16_f32 v186, v180, v181\n" \
"v_cvt_pk_bf16_f32 v187, v182, v183\n" \
"v_accvgpr_read_b32 v252, a248\n" \
"v_accvgpr_read_b32 v253, a249\n" \
"v_accvgpr_read_b32 v254, a250\n" \
"v_accvgpr_read_b32 v255, a251\n" \
"v_accvgpr_read_b32 v180, a252\n" \
"v_accvgpr_read_b32 v181, a253\n" \
"v_accvgpr_read_b32 v182, a254\n" \
"v_accvgpr_read_b32 v183, a255\n" \
"v_cvt_pk_bf16_f32 v252, v252, v253\n" \
"v_cvt_pk_bf16_f32 v253, v254, v255\n" \
"v_cvt_pk_bf16_f32 v254, v180, v181\n" \
"v_cvt_pk_bf16_f32 v255, v182, v183\n" \
"v_accvgpr_write_b32 a248, v252\n" \
"v_accvgpr_write_b32 a249, v253\n" \
"v_accvgpr_write_b32 a250, v254\n" \
"v_accvgpr_write_b32 a251, v255\n" \
"v_accvgpr_read_b32 v252, a224\n" \
"v_accvgpr_read_b32 v253, a225\n" \
"v_accvgpr_read_b32 v254, a226\n" \
"v_accvgpr_read_b32 v255, a227\n" \
"v_accvgpr_read_b32 v180, a228\n" \
"v_accvgpr_read_b32 v181, a229\n" \
"v_accvgpr_read_b32 v182, a230\n" \
"v_accvgpr_read_b32 v183, a231\n" \
"v_cvt_pk_bf16_f32 v252, v252, v253\n" \
"v_cvt_pk_bf16_f32 v253, v254, v255\n" \
"v_cvt_pk_bf16_f32 v254, v180, v181\n" \
"v_cvt_pk_bf16_f32 v255, v182, v183\n" \
"v_accvgpr_write_b32 a252, v252\n" \
"v_accvgpr_write_b32 a253, v253\n" \
"v_accvgpr_write_b32 a254, v254\n" \
"v_accvgpr_write_b32 a255, v255\n" \
"v_mfma_f32_16x16x32_bf16 a[224:227], v[84:87], v[80:83], 0\n" \
"v_accvgpr_read_b32 v188, a232\n" \
"v_accvgpr_read_b32 v189, a233\n" \
"v_accvgpr_read_b32 v190, a234\n" \
"v_accvgpr_read_b32 v191, a235\n" \
"v_accvgpr_read_b32 v180, a236\n" \
"v_mfma_f32_16x16x32_bf16 a[196:199], v[88:91], v[76:79], 0\n" \
"v_accvgpr_read_b32 v181, a237\n" \
"v_accvgpr_read_b32 v182, a238\n" \
"v_accvgpr_read_b32 v183, a239\n" \
"v_cvt_pk_bf16_f32 v188, v188, v189\n" \
"v_cvt_pk_bf16_f32 v189, v190, v191\n" \
"v_mfma_f32_16x16x32_bf16 a[228:231], v[88:91], v[80:83], 0\n" \
"v_cvt_pk_bf16_f32 v190, v180, v181\n" \
"v_cvt_pk_bf16_f32 v191, v182, v183\n" \
"v_accvgpr_read_b32 v252, a240\n" \
"v_accvgpr_read_b32 v253, a241\n" \
"v_accvgpr_read_b32 v254, a242\n" \
"v_mfma_f32_16x16x32_bf16 a[200:203], v[92:95], v[76:79], 0\n" \
"v_accvgpr_read_b32 v255, a243\n" \
"v_accvgpr_read_b32 v180, a244\n" \
"v_accvgpr_read_b32 v181, a245\n" \
"v_accvgpr_read_b32 v182, a246\n" \
"v_accvgpr_read_b32 v183, a247\n" \
"v_mfma_f32_16x16x32_bf16 a[232:235], v[92:95], v[80:83], 0\n" \
"v_cvt_pk_bf16_f32 v252, v252, v253\n" \
"v_cvt_pk_bf16_f32 v253, v254, v255\n" \
"v_cvt_pk_bf16_f32 v254, v180, v181\n" \
"v_cvt_pk_bf16_f32 v255, v182, v183\n" \
"v_mfma_f32_16x16x32_bf16 a[204:207], v[96:99], v[76:79], 0\n" \
"v_mfma_f32_16x16x32_bf16 a[236:239], v[96:99], v[80:83], 0\n" \
"v_mfma_f32_16x16x32_bf16 a[208:211], v[100:103], v[64:67], 0\n" \
"v_mfma_f32_16x16x32_bf16 a[212:215], v[104:107], v[64:67], 0\n" \
"v_mfma_f32_16x16x32_bf16 a[244:247], v[112:115], v[64:67], 0\n" \
"v_mfma_f32_16x16x32_bf16 a[240:243], v[64:67], v[108:111], 0\n" \
"s_waitcnt vmcnt(4) lgkmcnt(0)\n" \
"s_barrier\n" \
"v_mfma_f32_16x16x32_bf16 a[24:27], v[168:171], v[248:251], a[24:27]\n" \
"s_add_u32 m0, s40, 0x0\n" \
"s_nop 0\n" \
"s_add_u32 s85, s39, 0x4000\n" \
"v_mfma_f32_16x16x32_bf16 a[28:31], v[248:251], v[172:175], a[28:31]\n" \
"buffer_load_dwordx4 v20, s[80:83], s85 offen sc1 lds\n" \
"v_mfma_f32_16x16x32_bf16 a[56:59], v[176:179], v[248:251], a[56:59]\n" \
"ds_read_b64_tr_b16 v[104:105], v37 offset:32768\n" \
"v_mfma_f32_16x16x32_bf16 a[0:3], v[148:151], v[116:119], a[0:3]\n" \
"ds_read_b64_tr_b16 v[106:107], v37 offset:33280\n" \
"v_mfma_f32_16x16x32_bf16 a[32:35], v[148:151], v[120:123], a[32:35]\n" \
"s_add_u32 m0, s40, 0x1000\n" \
"s_add_u32 s84, s39, 0x5000\n" \
"v_mfma_f32_16x16x32_bf16 a[4:7], v[152:155], v[116:119], a[4:7]\n" \
"buffer_load_dwordx4 v20, s[80:83], s84 offen sc1 lds\n" \
"v_mfma_f32_16x16x32_bf16 a[36:39], v[152:155], v[120:123], a[36:39]\n" \
"ds_read_b64_tr_b16 v[108:109], v38 offset:32768\n" \
"v_mfma_f32_16x16x32_bf16 a[8:11], v[156:159], v[116:119], a[8:11]\n" \
"ds_read_b64_tr_b16 v[110:111], v38 offset:33280\n" \
"v_mfma_f32_16x16x32_bf16 a[40:43], v[156:159], v[120:123], a[40:43]\n" \
"s_add_u32 m0, s40, 0x3000\n" \
"s_mov_b32 s85, 0x100\n" \
"v_mfma_f32_16x16x32_bf16 a[12:15], v[160:163], v[116:119], a[12:15]\n" \
"buffer_load_dwordx4 v21, s[76:79], s85 offen lds\n" \
"v_mfma_f32_16x16x32_bf16 a[44:47], v[160:163], v[120:123], a[44:47]\n" \
"ds_read_b64_tr_b16 v[112:113], v39 offset:32768\n" \
"v_mfma_f32_16x16x32_bf16 a[16:19], v[164:167], v[116:119], a[16:19]\n" \
"ds_read_b64_tr_b16 v[114:115], v39 offset:33280\n" \
"v_mfma_f32_16x16x32_bf16 a[20:23], v[168:171], v[116:119], a[20:23]\n" \
"s_add_u32 m0, s30, 0x0\n" \
"s_nop 0\n" \
"s_mov_b32 s84, 0x100\n" \
"v_mfma_f32_16x16x32_bf16 a[52:55], v[176:179], v[116:119], a[52:55]\n" \
"buffer_load_dwordx4 v24, s[76:79], s84 offen lds\n" \
"v_mfma_f32_16x16x32_bf16 a[48:51], v[116:119], v[172:175], a[48:51]\n" \
"ds_read_b64_tr_b16 v[84:85], v32 offset:32768\n" \
"v_mfma_f32_16x16x32_bf16 a[64:67], v[148:151], v[124:127], a[64:67]\n" \
"ds_read_b64_tr_b16 v[86:87], v32 offset:33792\n" \
"v_mfma_f32_16x16x32_bf16 a[96:99], v[148:151], v[128:131], a[96:99]\n" \
"ds_read_b128 v[248:251], v22 offset:32768\n" \
"v_mfma_f32_16x16x32_bf16 a[68:71], v[152:155], v[124:127], a[68:71]\n" \
"s_add_u32 m0, s30, 0x400\n" \
"s_mov_b32 s85, 0x2500\n" \
"v_mfma_f32_16x16x32_bf16 a[100:103], v[152:155], v[128:131], a[100:103]\n" \
"buffer_load_dwordx4 v25, s[76:79], s85 offen lds\n" \
"v_mfma_f32_16x16x32_bf16 a[72:75], v[156:159], v[124:127], a[72:75]\n" \
"ds_read_b64_tr_b16 v[88:89], v33 offset:32768\n" \
"v_mfma_f32_16x16x32_bf16 a[104:107], v[156:159], v[128:131], a[104:107]\n" \
"ds_read_b64_tr_b16 v[90:91], v33 offset:33792\n" \
"v_mfma_f32_16x16x32_bf16 a[76:79], v[160:163], v[124:127], a[76:79]\n" \
"s_add_u32 m0, s31, 0x0\n" \
"s_nop 0\n" \
"s_mov_b32 s84, 0x100000\n" \
"v_mfma_f32_16x16x32_bf16 a[108:111], v[160:163], v[128:131], a[108:111]\n" \
"buffer_load_dwordx4 v26, s[68:71], s84 offen lds\n" \
"v_mfma_f32_16x16x32_bf16 a[80:83], v[164:167], v[120:123], a[80:83]\n" \
"ds_read_b64_tr_b16 v[92:93], v34 offset:32768\n" \
"v_mfma_f32_16x16x32_bf16 a[84:87], v[168:171], v[120:123], a[84:87]\n" \
"ds_read_b64_tr_b16 v[94:95], v34 offset:33792\n" \
"v_mfma_f32_16x16x32_bf16 a[116:119], v[176:179], v[120:123], a[116:119]\n" \
"s_add_u32 m0, s30, 0x800\n" \
"s_mov_b32 s85, 0x4900\n" \
"v_mfma_f32_16x16x32_bf16 a[112:115], v[120:123], v[172:175], a[112:115]\n" \
"buffer_load_dwordx4 v24, s[76:79], s85 offen lds\n" \
"v_mfma_f32_16x16x32_bf16 a[128:131], v[148:151], v[132:135], a[128:131]\n" \
"ds_read_b64_tr_b16 v[96:97], v35 offset:32768\n" \
"v_mfma_f32_16x16x32_bf16 a[160:163], v[148:151], v[136:139], a[160:163]\n" \
"ds_read_b64_tr_b16 v[98:99], v35 offset:33792\n" \
"v_mfma_f32_16x16x32_bf16 a[132:135], v[152:155], v[132:135], a[132:135]\n" \
"s_add_u32 m0, s30, 0xc00\n" \
"s_mov_b32 s84, 0x6d00\n" \
"v_mfma_f32_16x16x32_bf16 a[164:167], v[152:155], v[136:139], a[164:167]\n" \
"buffer_load_dwordx4 v25, s[76:79], s84 offen lds\n" \
"v_mfma_f32_16x16x32_bf16 a[136:139], v[156:159], v[132:135], a[136:139]\n" \
"ds_read_b64_tr_b16 v[100:101], v36 offset:32768\n" \
"v_mfma_f32_16x16x32_bf16 a[168:171], v[156:159], v[136:139], a[168:171]\n" \
"ds_read_b64_tr_b16 v[102:103], v36 offset:33280\n" \
"v_mfma_f32_16x16x32_bf16 a[140:143], v[160:163], v[132:135], a[140:143]\n" \
"ds_read_b128 v[52:55], v28 offset:32768\n" \
"v_mfma_f32_16x16x32_bf16 a[172:175], v[160:163], v[136:139], a[172:175]\n" \
"s_add_u32 m0, s31, 0x400\n" \
"s_mov_b32 s85, 0x108000\n" \
"v_mfma_f32_16x16x32_bf16 a[144:147], v[164:167], v[124:127], a[144:147]\n" \
"buffer_load_dwordx4 v26, s[68:71], s85 offen lds\n" \
"v_mfma_f32_16x16x32_bf16 a[148:151], v[168:171], v[124:127], a[148:151]\n" \
"ds_read_b128 v[56:59], v28 offset:34816\n" \
"v_mfma_f32_16x16x32_bf16 a[180:183], v[176:179], v[124:127], a[180:183]\n" \
"ds_read_b128 v[60:63], v28 offset:36864\n" \
"v_mfma_f32_16x16x32_bf16 a[176:179], v[124:127], v[172:175], a[176:179]\n" \
"s_add_u32 m0, s30, 0x1000\n" \
"s_mov_b32 s84, 0x9100\n" \
"v_mfma_f32_16x16x32_bf16 a[192:195], v[148:151], v[140:143], a[192:195]\n" \
"buffer_load_dwordx4 v24, s[76:79], s84 offen lds\n" \
"v_mfma_f32_16x16x32_bf16 a[224:227], v[148:151], v[144:147], a[224:227]\n" \
"ds_read_b128 v[64:67], v28 offset:38912\n" \
"v_mfma_f32_16x16x32_bf16 a[196:199], v[152:155], v[140:143], a[196:199]\n" \
"ds_read_b128 v[68:71], v28 offset:40960\n" \
"v_mfma_f32_16x16x32_bf16 a[228:231], v[152:155], v[144:147], a[228:231]\n" \
"s_add_u32 m0, s30, 0x1400\n" \
"s_mov_b32 s85, 0xb500\n" \
"v_mfma_f32_16x16x32_bf16 a[200:203], v[156:159], v[140:143], a[200:203]\n" \
"buffer_load_dwordx4 v25, s[76:79], s85 offen lds\n" \
"v_mfma_f32_16x16x32_bf16 a[232:235], v[156:159], v[144:147], a[232:235]\n" \
"ds_read_b128 v[72:75], v28 offset:43008\n" \
"v_mfma_f32_16x16x32_bf16 a[204:207], v[160:163], v[140:143], a[204:207]\n" \
"ds_read_b128 v[76:79], v28 offset:45056\n" \
"v_mfma_f32_16x16x32_bf16 a[236:239], v[160:163], v[144:147], a[236:239]\n" \
"s_add_u32 m0, s31, 0x800\n" \
"s_mov_b32 s84, 0x110000\n" \
"v_mfma_f32_16x16x32_bf16 a[208:211], v[164:167], v[128:131], a[208:211]\n" \
"buffer_load_dwordx4 v27, s[68:71], s84 offen lds\n" \
"v_mfma_f32_16x16x32_bf16 a[212:215], v[168:171], v[128:131], a[212:215]\n" \
"ds_read_b128 v[80:83], v28 offset:47104\n" \
"v_mfma_f32_16x16x32_bf16 a[244:247], v[176:179], v[128:131], a[244:247]\n" \
"v_mfma_f32_16x16x32_bf16 a[240:243], v[128:131], v[172:175], a[240:243]\n" \
"s_waitcnt lgkmcnt(15)\n" \
"v_mfma_f32_16x16x32_bf16 a[24:27], v[104:107], v[248:251], a[24:27]\n" \
"s_add_u32 m0, s30, 0x1800\n" \
"s_mov_b32 s84, 0xd900\n" \
"v_mfma_f32_16x16x32_bf16 a[28:31], v[248:251], v[108:111], a[28:31]\n" \
"buffer_load_dwordx4 v24, s[76:79], s84 offen lds\n" \
"v_mfma_f32_16x16x32_bf16 a[56:59], v[112:115], v[248:251], a[56:59]\n" \
"ds_read_b64_tr_b16 v[168:169], v37 offset:36864\n" \
"s_movk_i32 s11, 0x4000\n" \
"s_waitcnt lgkmcnt(8)\n" \
"v_mfma_f32_16x16x32_bf16 a[0:3], v[84:87], v[52:55], a[0:3]\n" \
"s_add_u32 m0, s30, 0x1c00\n" \
"s_mov_b32 s85, 0xfd00\n" \
"s_waitcnt lgkmcnt(7)\n" \
"v_mfma_f32_16x16x32_bf16 a[32:35], v[84:87], v[56:59], a[32:35]\n" \
"buffer_load_dwordx4 v25, s[76:79], s85 offen lds\n" \
"v_mfma_f32_16x16x32_bf16 a[4:7], v[88:91], v[52:55], a[4:7]\n" \
"ds_read_b64_tr_b16 v[170:171], v37 offset:37376\n" \
"s_lshl_b32 s4, s2, 2\n" \
"v_mfma_f32_16x16x32_bf16 a[36:39], v[88:91], v[56:59], a[36:39]\n" \
"s_add_u32 m0, s31, 0xc00\n" \
"s_mov_b32 s84, 0x118000\n" \
"v_mfma_f32_16x16x32_bf16 a[8:11], v[92:95], v[52:55], a[8:11]\n" \
"buffer_load_dwordx4 v27, s[68:71], s84 offen lds\n" \
"v_mfma_f32_16x16x32_bf16 a[40:43], v[92:95], v[56:59], a[40:43]\n" \
"ds_read_b64_tr_b16 v[172:173], v38 offset:36864\n" \
"s_and_b32 s4, s4, 28\n" \
"v_mfma_f32_16x16x32_bf16 a[12:15], v[96:99], v[52:55], a[12:15]\n" \
"ds_read_b64_tr_b16 v[174:175], v38 offset:37376\n" \
"s_bfe_u32 s5, s2, 0x20003\n" \
"v_mfma_f32_16x16x32_bf16 a[44:47], v[96:99], v[56:59], a[44:47]\n" \
"ds_read_b64_tr_b16 v[176:177], v39 offset:36864\n" \
"s_or_b32 s4, s4, s5\n" \
"v_mfma_f32_16x16x32_bf16 a[16:19], v[100:103], v[52:55], a[16:19]\n" \
"ds_read_b64_tr_b16 v[178:179], v39 offset:37376\n" \
"s_lshr_b32 s5, s2, 5\n" \
"v_mfma_f32_16x16x32_bf16 a[20:23], v[104:107], v[52:55], a[20:23]\n" \
"ds_read_b64_tr_b16 v[148:149], v32 offset:40960\n" \
"s_lshr_b32 s19, s3, 1\n" \
"v_mfma_f32_16x16x32_bf16 a[52:55], v[112:115], v[52:55], a[52:55]\n" \
"ds_read_b64_tr_b16 v[150:151], v32 offset:41984\n" \
"s_lshl_b32 s18, s5, 1\n" \
"v_mfma_f32_16x16x32_bf16 a[48:51], v[52:55], v[108:111], a[48:51]\n" \
"ds_read_b128 v[248:251], v23 offset:32768\n" \
"s_lshl_b32 s6, s4, 20\n" \
"s_waitcnt lgkmcnt(14)\n" \
"v_mfma_f32_16x16x32_bf16 a[64:67], v[84:87], v[60:63], a[64:67]\n" \
"ds_read_b64_tr_b16 v[152:153], v33 offset:40960\n" \
"s_lshl_b32 s7, s4, 8\n" \
"s_waitcnt lgkmcnt(14)\n" \
"v_mfma_f32_16x16x32_bf16 a[96:99], v[84:87], v[64:67], a[96:99]\n" \
"ds_read_b64_tr_b16 v[154:155], v33 offset:41984\n" \
"s_and_b32 s13, s3, 1\n" \
"v_mfma_f32_16x16x32_bf16 a[68:71], v[88:91], v[60:63], a[68:71]\n" \
"ds_read_b64_tr_b16 v[156:157], v34 offset:40960\n" \
"s_add_i32 s14, s18, s19\n" \
"v_mfma_f32_16x16x32_bf16 a[100:103], v[88:91], v[64:67], a[100:103]\n" \
"ds_read_b64_tr_b16 v[158:159], v34 offset:41984\n" \
"v_mfma_f32_16x16x32_bf16 a[72:75], v[92:95], v[60:63], a[72:75]\n" \
"s_add_u32 s6, s26, s6\n" \
"s_addc_u32 s9, s27, 0\n" \
"v_mfma_f32_16x16x32_bf16 a[104:107], v[92:95], v[64:67], a[104:107]\n" \
"ds_read_b64_tr_b16 v[160:161], v35 offset:40960\n" \
"v_mfma_f32_16x16x32_bf16 a[76:79], v[96:99], v[60:63], a[76:79]\n" \
"s_add_u32 s89, s41, 0x1000\n" \
"buffer_store_dwordx4 v[200:203], v20, s[80:83], s89 offen offset:0 sc1\n" \
"v_mfma_f32_16x16x32_bf16 a[108:111], v[96:99], v[64:67], a[108:111]\n" \
"ds_read_b64_tr_b16 v[162:163], v35 offset:41984\n" \
"v_mfma_f32_16x16x32_bf16 a[80:83], v[100:103], v[56:59], a[80:83]\n" \
"ds_read_b64_tr_b16 v[164:165], v36 offset:36864\n" \
"v_mfma_f32_16x16x32_bf16 a[84:87], v[104:107], v[56:59], a[84:87]\n" \
"ds_read_b64_tr_b16 v[166:167], v36 offset:37376\n" \
"v_mfma_f32_16x16x32_bf16 a[116:119], v[112:115], v[56:59], a[116:119]\n" \
"s_add_u32 s8, s6, 0x1000000\n" \
"s_addc_u32 s6, s9, 0\n" \
"v_mfma_f32_16x16x32_bf16 a[112:115], v[56:59], v[108:111], a[112:115]\n" \
"ds_read_b128 v[116:119], v29 offset:32768\n" \
"s_and_b32 s9, s6, 0xffff\n" \
"s_waitcnt lgkmcnt(15)\n" \
"v_mfma_f32_16x16x32_bf16 a[128:131], v[84:87], v[68:71], a[128:131]\n" \
"s_lshl_b32 s6, s5, 16\n" \
"s_nop 0\n" \
"s_sub_i32 s10, 0x100000, s6\n" \
"v_mfma_f32_16x16x32_bf16 a[160:163], v[84:87], v[72:75], a[160:163]\n" \
"ds_read_b128 v[120:123], v29 offset:34816\n" \
"s_mov_b32 s6, 0x8000\n" \
"v_mfma_f32_16x16x32_bf16 a[132:135], v[88:91], v[68:71], a[132:135]\n" \
"s_add_u32 s88, s41, 0x1000\n" \
"buffer_store_dwordx4 v[204:207], v20, s[80:83], s88 offen offset:1024 sc1\n" \
"v_mfma_f32_16x16x32_bf16 a[164:167], v[88:91], v[72:75], a[164:167]\n" \
"ds_read_b128 v[124:127], v29 offset:36864\n" \
"v_mfma_f32_16x16x32_bf16 a[136:139], v[92:95], v[68:71], a[136:139]\n" \
"ds_read_b128 v[128:131], v29 offset:38912\n" \
"v_mfma_f32_16x16x32_bf16 a[168:171], v[92:95], v[72:75], a[168:171]\n" \
"ds_read_b128 v[132:135], v29 offset:40960\n" \
"v_mfma_f32_16x16x32_bf16 a[140:143], v[96:99], v[68:71], a[140:143]\n" \
"s_cmp_lg_u32 s5, 0\n" \
"s_cselect_b32 s17, s11, s6\n" \
"v_mfma_f32_16x16x32_bf16 a[172:175], v[96:99], v[72:75], a[172:175]\n" \
"ds_read_b128 v[136:139], v29 offset:43008\n" \
"s_lshl_b32 s6, s3, 1\n" \
"v_mfma_f32_16x16x32_bf16 a[144:147], v[100:103], v[60:63], a[144:147]\n" \
"ds_read_b128 v[140:143], v29 offset:45056\n" \
"s_lshl_b32 s5, s5, 3\n" \
"v_mfma_f32_16x16x32_bf16 a[148:151], v[104:107], v[60:63], a[148:151]\n" \
"s_add_u32 s89, s41, 0x1000\n" \
"buffer_store_dwordx4 v[208:211], v20, s[80:83], s89 offen offset:2048 sc1\n" \
"v_mfma_f32_16x16x32_bf16 a[180:183], v[112:115], v[60:63], a[180:183]\n" \
"ds_read_b128 v[144:147], v29 offset:47104\n" \
"s_and_b32 s6, s6, 4\n" \
"v_mfma_f32_16x16x32_bf16 a[176:179], v[60:63], v[108:111], a[176:179]\n" \
"s_or_b32 s12, s5, s6\n" \
"s_sub_i32 s5, s13, s5\n" \
"v_mfma_f32_16x16x32_bf16 a[192:195], v[84:87], v[76:79], a[192:195]\n" \
"s_or_b32 s5, s5, s6\n" \
"s_or_b32 s6, s12, s13\n" \
"v_mfma_f32_16x16x32_bf16 a[224:227], v[84:87], v[80:83], a[224:227]\n" \
"s_lshl_b32 s4, s4, 2\n" \
"s_mulk_i32 s6, 0x92\n" \
"v_mfma_f32_16x16x32_bf16 a[196:199], v[88:91], v[76:79], a[196:199]\n" \
"s_add_i32 s4, s4, 18\n" \
"s_mulk_i32 s5, 0x92\n" \
"v_mfma_f32_16x16x32_bf16 a[228:231], v[88:91], v[80:83], a[228:231]\n" \
"s_add_i32 s6, s4, s6\n" \
"s_add_i32 s5, s5, s4\n" \
"v_mfma_f32_16x16x32_bf16 a[200:203], v[92:95], v[76:79], a[200:203]\n" \
"s_add_u32 s88, s41, 0x1000\n" \
"buffer_store_dwordx4 v[212:215], v20, s[80:83], s88 offen offset:3072 sc1\n" \
"v_mfma_f32_16x16x32_bf16 a[232:235], v[92:95], v[80:83], a[232:235]\n" \
"s_lshl_b32 s4, s3, 11\n" \
"s_lshl_b32 s15, s13, 12\n" \
"v_mfma_f32_16x16x32_bf16 a[204:207], v[96:99], v[76:79], a[204:207]\n" \
"s_lshl_b32 s16, s19, 14\n" \
"s_mul_i32 s19, s19, 0xe000\n" \
"v_mfma_f32_16x16x32_bf16 a[236:239], v[96:99], v[80:83], a[236:239]\n" \
"s_mov_b32 s11, 0x20000\n" \
"s_or_b32 s12, s4, 0xc000\n" \
"v_mfma_f32_16x16x32_bf16 a[208:211], v[100:103], v[64:67], a[208:211]\n" \
"s_lshl_b32 s4, s3, 8\n" \
"s_or_b32 s22, s15, s16\n" \
"v_mfma_f32_16x16x32_bf16 a[212:215], v[104:107], v[64:67], a[212:215]\n" \
"s_or_b32 s23, s19, s15\n" \
"s_lshl_b32 s60, s6, 10\n" \
"v_mfma_f32_16x16x32_bf16 a[244:247], v[112:115], v[64:67], a[244:247]\n" \
"s_lshl_b32 s61, s5, 10\n" \
"s_lshl_b32 s14, s14, 15\n" \
"v_mfma_f32_16x16x32_bf16 a[240:243], v[64:67], v[108:111], a[240:243]\n" \
"s_waitcnt vmcnt(4) lgkmcnt(0)\n" \
"s_barrier\n" \
"v_mfma_f32_16x16x32_bf16 a[24:27], v[168:171], v[248:251], a[24:27]\n" \
"s_add_u32 m0, s40, 0x8000\n" \
"s_add_u32 s85, s39, 0x6000\n" \
"v_mfma_f32_16x16x32_bf16 a[28:31], v[248:251], v[172:175], a[28:31]\n" \
"buffer_load_dwordx4 v20, s[80:83], s85 offen sc1 lds\n" \
"v_mfma_f32_16x16x32_bf16 a[56:59], v[176:179], v[248:251], a[56:59]\n" \
"ds_read_b64_tr_b16 v[104:105], v37 offset:0\n" \
"s_or_b32 s20, s4, 0x26c00\n" \
"v_mfma_f32_16x16x32_bf16 a[0:3], v[148:151], v[116:119], a[0:3]\n" \
"ds_read_b64_tr_b16 v[106:107], v37 offset:512\n" \
"s_or_b32 s13, s22, 0x8000\n" \
"v_mfma_f32_16x16x32_bf16 a[32:35], v[148:151], v[120:123], a[32:35]\n" \
"s_add_u32 m0, s40, 0x9000\n" \
"s_add_u32 s84, s39, 0x7000\n" \
"v_mfma_f32_16x16x32_bf16 a[4:7], v[152:155], v[116:119], a[4:7]\n" \
"buffer_load_dwordx4 v20, s[80:83], s84 offen sc1 lds\n" \
"v_mfma_f32_16x16x32_bf16 a[36:39], v[152:155], v[120:123], a[36:39]\n" \
"ds_read_b64_tr_b16 v[108:109], v38 offset:0\n" \
"s_add_i32 s22, s22, 0x14000\n" \
"v_mfma_f32_16x16x32_bf16 a[8:11], v[156:159], v[116:119], a[8:11]\n" \
"ds_read_b64_tr_b16 v[110:111], v38 offset:512\n" \
"s_bitset1_b32 s23, 16\n" \
"s_nop 0\n" \
"v_mfma_f32_16x16x32_bf16 a[40:43], v[156:159], v[120:123], a[40:43]\n" \
"s_add_u32 m0, s40, 0xb000\n" \
"s_mov_b32 s85, 0x180\n" \
"v_mfma_f32_16x16x32_bf16 a[12:15], v[160:163], v[116:119], a[12:15]\n" \
"buffer_load_dwordx4 v21, s[76:79], s85 offen lds\n" \
"v_mfma_f32_16x16x32_bf16 a[44:47], v[160:163], v[120:123], a[44:47]\n" \
"ds_read_b64_tr_b16 v[112:113], v39 offset:0\n" \
"s_or_b32 s24, s7, 0x5be8020\n" \
"v_mfma_f32_16x16x32_bf16 a[16:19], v[164:167], v[116:119], a[16:19]\n" \
"ds_read_b64_tr_b16 v[114:115], v39 offset:512\n" \
"s_and_b32 s57, s27, 0xffff\n" \
"v_mfma_f32_16x16x32_bf16 a[20:23], v[168:171], v[116:119], a[20:23]\n" \
"s_add_u32 m0, s30, 0x8000\n" \
"s_mov_b32 s84, 0x180\n" \
"v_mfma_f32_16x16x32_bf16 a[52:55], v[176:179], v[116:119], a[52:55]\n" \
"buffer_load_dwordx4 v24, s[76:79], s84 offen lds\n" \
"v_mfma_f32_16x16x32_bf16 a[48:51], v[116:119], v[172:175], a[48:51]\n" \
"ds_read_b64_tr_b16 v[84:85], v32 offset:0\n" \
"s_mov_b32 s58, 0x6000000\n" \
"v_mfma_f32_16x16x32_bf16 a[64:67], v[148:151], v[124:127], a[64:67]\n" \
"ds_read_b64_tr_b16 v[86:87], v32 offset:1024\n" \
"s_mov_b32 s56, s26\n" \
"v_mfma_f32_16x16x32_bf16 a[96:99], v[148:151], v[128:131], a[96:99]\n" \
"ds_read_b128 v[248:251], v22 offset:0\n" \
"s_mov_b32 s59, s11\n" \
"v_mfma_f32_16x16x32_bf16 a[68:71], v[152:155], v[124:127], a[68:71]\n" \
"s_add_u32 m0, s30, 0x8400\n" \
"s_mov_b32 s85, 0x2580\n" \
"v_mfma_f32_16x16x32_bf16 a[100:103], v[152:155], v[128:131], a[100:103]\n" \
"buffer_load_dwordx4 v25, s[76:79], s85 offen lds\n" \
"v_mfma_f32_16x16x32_bf16 a[72:75], v[156:159], v[124:127], a[72:75]\n" \
"ds_read_b64_tr_b16 v[88:89], v33 offset:0\n" \
"s_add_i32 s60, s60, 0x3000000\n" \
"v_mfma_f32_16x16x32_bf16 a[104:107], v[156:159], v[128:131], a[104:107]\n" \
"ds_read_b64_tr_b16 v[90:91], v33 offset:1024\n" \
"s_add_i32 s61, s61, 0x411c000\n" \
"v_mfma_f32_16x16x32_bf16 a[76:79], v[160:163], v[124:127], a[76:79]\n" \
"s_add_u32 m0, s31, 0x8000\n" \
"s_mov_b32 s84, 0x180000\n" \
"v_mfma_f32_16x16x32_bf16 a[108:111], v[160:163], v[128:131], a[108:111]\n" \
"buffer_load_dwordx4 v26, s[68:71], s84 offen lds\n" \
"v_mfma_f32_16x16x32_bf16 a[80:83], v[164:167], v[120:123], a[80:83]\n" \
"ds_read_b64_tr_b16 v[92:93], v34 offset:0\n" \
"s_sub_i32 s19, 14, s18\n" \
"s_nop 0\n" \
"v_mfma_f32_16x16x32_bf16 a[84:87], v[168:171], v[120:123], a[84:87]\n" \
"ds_read_b64_tr_b16 v[94:95], v34 offset:1024\n" \
"s_mov_b32 s21, 0x71800000\n" \
"v_mfma_f32_16x16x32_bf16 a[116:119], v[176:179], v[120:123], a[116:119]\n" \
"s_add_u32 m0, s30, 0x8800\n" \
"s_mov_b32 s85, 0x4980\n" \
"v_mfma_f32_16x16x32_bf16 a[112:115], v[120:123], v[172:175], a[112:115]\n" \
"buffer_load_dwordx4 v24, s[76:79], s85 offen lds\n" \
"v_mfma_f32_16x16x32_bf16 a[128:131], v[148:151], v[132:135], a[128:131]\n" \
"ds_read_b64_tr_b16 v[96:97], v35 offset:0\n" \
"v_mfma_f32_16x16x32_bf16 a[160:163], v[148:151], v[136:139], a[160:163]\n" \
"ds_read_b64_tr_b16 v[98:99], v35 offset:1024\n" \
"v_mfma_f32_16x16x32_bf16 a[132:135], v[152:155], v[132:135], a[132:135]\n" \
"s_add_u32 m0, s30, 0x8c00\n" \
"s_mov_b32 s84, 0x6d80\n" \
"v_mfma_f32_16x16x32_bf16 a[164:167], v[152:155], v[136:139], a[164:167]\n" \
"buffer_load_dwordx4 v25, s[76:79], s84 offen lds\n" \
"v_mfma_f32_16x16x32_bf16 a[136:139], v[156:159], v[132:135], a[136:139]\n" \
"ds_read_b64_tr_b16 v[100:101], v36 offset:0\n" \
"v_mfma_f32_16x16x32_bf16 a[168:171], v[156:159], v[136:139], a[168:171]\n" \
"ds_read_b64_tr_b16 v[102:103], v36 offset:512\n" \
"v_mfma_f32_16x16x32_bf16 a[140:143], v[160:163], v[132:135], a[140:143]\n" \
"ds_read_b128 v[52:55], v28 offset:0\n" \
"v_mfma_f32_16x16x32_bf16 a[172:175], v[160:163], v[136:139], a[172:175]\n" \
"s_add_u32 m0, s31, 0x8400\n" \
"s_mov_b32 s85, 0x188000\n" \
"v_mfma_f32_16x16x32_bf16 a[144:147], v[164:167], v[124:127], a[144:147]\n" \
"buffer_load_dwordx4 v26, s[68:71], s85 offen lds\n" \
"v_mfma_f32_16x16x32_bf16 a[148:151], v[168:171], v[124:127], a[148:151]\n" \
"ds_read_b128 v[56:59], v28 offset:2048\n" \
"v_mfma_f32_16x16x32_bf16 a[180:183], v[176:179], v[124:127], a[180:183]\n" \
"ds_read_b128 v[60:63], v28 offset:4096\n" \
"v_mfma_f32_16x16x32_bf16 a[176:179], v[124:127], v[172:175], a[176:179]\n" \
"s_add_u32 m0, s30, 0x9000\n" \
"s_mov_b32 s84, 0x9180\n" \
"v_mfma_f32_16x16x32_bf16 a[192:195], v[148:151], v[140:143], a[192:195]\n" \
"buffer_load_dwordx4 v24, s[76:79], s84 offen lds\n" \
"v_mfma_f32_16x16x32_bf16 a[224:227], v[148:151], v[144:147], a[224:227]\n" \
"ds_read_b128 v[64:67], v28 offset:6144\n" \
"v_mfma_f32_16x16x32_bf16 a[196:199], v[152:155], v[140:143], a[196:199]\n" \
"ds_read_b128 v[68:71], v28 offset:8192\n" \
"v_mfma_f32_16x16x32_bf16 a[228:231], v[152:155], v[144:147], a[228:231]\n" \
"s_add_u32 m0, s30, 0x9400\n" \
"s_mov_b32 s85, 0xb580\n" \
"v_mfma_f32_16x16x32_bf16 a[200:203], v[156:159], v[140:143], a[200:203]\n" \
"buffer_load_dwordx4 v25, s[76:79], s85 offen lds\n" \
"v_mfma_f32_16x16x32_bf16 a[232:235], v[156:159], v[144:147], a[232:235]\n" \
"ds_read_b128 v[72:75], v28 offset:10240\n" \
"v_mfma_f32_16x16x32_bf16 a[204:207], v[160:163], v[140:143], a[204:207]\n" \
"ds_read_b128 v[76:79], v28 offset:12288\n" \
"v_mfma_f32_16x16x32_bf16 a[236:239], v[160:163], v[144:147], a[236:239]\n" \
"s_add_u32 m0, s31, 0x8800\n" \
"s_mov_b32 s84, 0x190000\n" \
"v_mfma_f32_16x16x32_bf16 a[208:211], v[164:167], v[128:131], a[208:211]\n" \
"buffer_load_dwordx4 v27, s[68:71], s84 offen lds\n" \
"v_mfma_f32_16x16x32_bf16 a[212:215], v[168:171], v[128:131], a[212:215]\n" \
"ds_read_b128 v[80:83], v28 offset:14336\n" \
"v_mfma_f32_16x16x32_bf16 a[244:247], v[176:179], v[128:131], a[244:247]\n" \
"v_mfma_f32_16x16x32_bf16 a[240:243], v[128:131], v[172:175], a[240:243]\n" \
"s_waitcnt lgkmcnt(15)\n" \
"v_mfma_f32_16x16x32_bf16 a[24:27], v[104:107], v[248:251], a[24:27]\n" \
"s_add_u32 m0, s30, 0x9800\n" \
"s_mov_b32 s84, 0xd980\n" \
"v_mfma_f32_16x16x32_bf16 a[28:31], v[248:251], v[108:111], a[28:31]\n" \
"buffer_load_dwordx4 v24, s[76:79], s84 offen lds\n" \
"v_mfma_f32_16x16x32_bf16 a[56:59], v[112:115], v[248:251], a[56:59]\n" \
"ds_read_b64_tr_b16 v[168:169], v37 offset:4096\n" \
"s_waitcnt lgkmcnt(8)\n" \
"v_mfma_f32_16x16x32_bf16 a[0:3], v[84:87], v[52:55], a[0:3]\n" \
"s_add_u32 m0, s30, 0x9c00\n" \
"s_mov_b32 s85, 0xfd80\n" \
"s_waitcnt lgkmcnt(7)\n" \
"s_nop 0\n" \
"v_mfma_f32_16x16x32_bf16 a[32:35], v[84:87], v[56:59], a[32:35]\n" \
"buffer_load_dwordx4 v25, s[76:79], s85 offen lds\n" \
"v_mfma_f32_16x16x32_bf16 a[4:7], v[88:91], v[52:55], a[4:7]\n" \
"ds_read_b64_tr_b16 v[170:171], v37 offset:4608\n" \
"v_mfma_f32_16x16x32_bf16 a[36:39], v[88:91], v[56:59], a[36:39]\n" \
"s_add_u32 m0, s31, 0x8c00\n" \
"s_mov_b32 s84, 0x198000\n" \
"v_mfma_f32_16x16x32_bf16 a[8:11], v[92:95], v[52:55], a[8:11]\n" \
"buffer_load_dwordx4 v27, s[68:71], s84 offen lds\n" \
"v_mfma_f32_16x16x32_bf16 a[40:43], v[92:95], v[56:59], a[40:43]\n" \
"ds_read_b64_tr_b16 v[172:173], v38 offset:4096\n" \
"v_mfma_f32_16x16x32_bf16 a[12:15], v[96:99], v[52:55], a[12:15]\n" \
"ds_read_b64_tr_b16 v[174:175], v38 offset:4608\n" \
"v_mfma_f32_16x16x32_bf16 a[44:47], v[96:99], v[56:59], a[44:47]\n" \
"ds_read_b64_tr_b16 v[176:177], v39 offset:4096\n" \
"v_mfma_f32_16x16x32_bf16 a[16:19], v[100:103], v[52:55], a[16:19]\n" \
"ds_read_b64_tr_b16 v[178:179], v39 offset:4608\n" \
"v_mfma_f32_16x16x32_bf16 a[20:23], v[104:107], v[52:55], a[20:23]\n" \
"ds_read_b64_tr_b16 v[148:149], v32 offset:8192\n" \
"v_mfma_f32_16x16x32_bf16 a[52:55], v[112:115], v[52:55], a[52:55]\n" \
"ds_read_b64_tr_b16 v[150:151], v32 offset:9216\n" \
"v_mfma_f32_16x16x32_bf16 a[48:51], v[52:55], v[108:111], a[48:51]\n" \
"ds_read_b128 v[248:251], v23 offset:0\n" \
"s_waitcnt lgkmcnt(14)\n" \
"v_mfma_f32_16x16x32_bf16 a[64:67], v[84:87], v[60:63], a[64:67]\n" \
"ds_read_b64_tr_b16 v[152:153], v33 offset:8192\n" \
"s_waitcnt lgkmcnt(14)\n" \
"v_mfma_f32_16x16x32_bf16 a[96:99], v[84:87], v[64:67], a[96:99]\n" \
"ds_read_b64_tr_b16 v[154:155], v33 offset:9216\n" \
"v_mfma_f32_16x16x32_bf16 a[68:71], v[88:91], v[60:63], a[68:71]\n" \
"ds_read_b64_tr_b16 v[156:157], v34 offset:8192\n" \
"v_mfma_f32_16x16x32_bf16 a[100:103], v[88:91], v[64:67], a[100:103]\n" \
"ds_read_b64_tr_b16 v[158:159], v34 offset:9216\n" \
"v_mfma_f32_16x16x32_bf16 a[72:75], v[92:95], v[60:63], a[72:75]\n" \
"v_mfma_f32_16x16x32_bf16 a[104:107], v[92:95], v[64:67], a[104:107]\n" \
"ds_read_b64_tr_b16 v[160:161], v35 offset:8192\n" \
"v_mfma_f32_16x16x32_bf16 a[76:79], v[96:99], v[60:63], a[76:79]\n" \
"s_add_u32 s89, s41, 0x2000\n" \
"buffer_store_dwordx4 v[216:219], v20, s[80:83], s89 offen offset:0 sc1\n" \
"v_mfma_f32_16x16x32_bf16 a[108:111], v[96:99], v[64:67], a[108:111]\n" \
"ds_read_b64_tr_b16 v[162:163], v35 offset:9216\n" \
"v_mfma_f32_16x16x32_bf16 a[80:83], v[100:103], v[56:59], a[80:83]\n" \
"ds_read_b64_tr_b16 v[164:165], v36 offset:4096\n" \
"v_mfma_f32_16x16x32_bf16 a[84:87], v[104:107], v[56:59], a[84:87]\n" \
"ds_read_b64_tr_b16 v[166:167], v36 offset:4608\n" \
"v_mfma_f32_16x16x32_bf16 a[116:119], v[112:115], v[56:59], a[116:119]\n" \
"v_mfma_f32_16x16x32_bf16 a[112:115], v[56:59], v[108:111], a[112:115]\n" \
"ds_read_b128 v[116:119], v29 offset:0\n" \
"s_waitcnt lgkmcnt(15)\n" \
"s_nop 0\n" \
"v_mfma_f32_16x16x32_bf16 a[128:131], v[84:87], v[68:71], a[128:131]\n" \
"v_mfma_f32_16x16x32_bf16 a[160:163], v[84:87], v[72:75], a[160:163]\n" \
"ds_read_b128 v[120:123], v29 offset:2048\n" \
"v_mfma_f32_16x16x32_bf16 a[132:135], v[88:91], v[68:71], a[132:135]\n" \
"s_add_u32 s88, s41, 0x2000\n" \
"buffer_store_dwordx4 v[220:223], v20, s[80:83], s88 offen offset:1024 sc1\n" \
"v_mfma_f32_16x16x32_bf16 a[164:167], v[88:91], v[72:75], a[164:167]\n" \
"ds_read_b128 v[124:127], v29 offset:4096\n" \
"v_mfma_f32_16x16x32_bf16 a[136:139], v[92:95], v[68:71], a[136:139]\n" \
"ds_read_b128 v[128:131], v29 offset:6144\n" \
"v_mfma_f32_16x16x32_bf16 a[168:171], v[92:95], v[72:75], a[168:171]\n" \
"ds_read_b128 v[132:135], v29 offset:8192\n" \
"v_mfma_f32_16x16x32_bf16 a[140:143], v[96:99], v[68:71], a[140:143]\n" \
"v_mfma_f32_16x16x32_bf16 a[172:175], v[96:99], v[72:75], a[172:175]\n" \
"ds_read_b128 v[136:139], v29 offset:10240\n" \
"v_mfma_f32_16x16x32_bf16 a[144:147], v[100:103], v[60:63], a[144:147]\n" \
"ds_read_b128 v[140:143], v29 offset:12288\n" \
"v_mfma_f32_16x16x32_bf16 a[148:151], v[104:107], v[60:63], a[148:151]\n" \
"s_add_u32 s89, s41, 0x2000\n" \
"buffer_store_dwordx4 v[224:227], v20, s[80:83], s89 offen offset:2048 sc1\n" \
"v_mfma_f32_16x16x32_bf16 a[180:183], v[112:115], v[60:63], a[180:183]\n" \
"ds_read_b128 v[144:147], v29 offset:14336\n" \
"v_mfma_f32_16x16x32_bf16 a[176:179], v[60:63], v[108:111], a[176:179]\n" \
"v_mfma_f32_16x16x32_bf16 a[192:195], v[84:87], v[76:79], a[192:195]\n" \
"v_mfma_f32_16x16x32_bf16 a[224:227], v[84:87], v[80:83], a[224:227]\n" \
"v_mfma_f32_16x16x32_bf16 a[196:199], v[88:91], v[76:79], a[196:199]\n" \
"v_mfma_f32_16x16x32_bf16 a[228:231], v[88:91], v[80:83], a[228:231]\n" \
"v_mfma_f32_16x16x32_bf16 a[200:203], v[92:95], v[76:79], a[200:203]\n" \
"s_add_u32 s88, s41, 0x2000\n" \
"buffer_store_dwordx4 v[228:231], v20, s[80:83], s88 offen offset:3072 sc1\n" \
"v_mfma_f32_16x16x32_bf16 a[232:235], v[92:95], v[80:83], a[232:235]\n" \
"v_mfma_f32_16x16x32_bf16 a[204:207], v[96:99], v[76:79], a[204:207]\n" \
"v_mfma_f32_16x16x32_bf16 a[236:239], v[96:99], v[80:83], a[236:239]\n" \
"v_mfma_f32_16x16x32_bf16 a[208:211], v[100:103], v[64:67], a[208:211]\n" \
"v_mfma_f32_16x16x32_bf16 a[212:215], v[104:107], v[64:67], a[212:215]\n" \
"v_mfma_f32_16x16x32_bf16 a[244:247], v[112:115], v[64:67], a[244:247]\n" \
"v_mfma_f32_16x16x32_bf16 a[240:243], v[64:67], v[108:111], a[240:243]\n" \
"s_waitcnt vmcnt(4) lgkmcnt(0)\n" \
"s_barrier\n" \
"v_mfma_f32_16x16x32_bf16 a[24:27], v[168:171], v[248:251], a[24:27]\n" \
"s_add_u32 m0, s40, 0x0\n" \
"s_nop 0\n" \
"s_add_u32 s85, s39, 0x8000\n" \
"v_mfma_f32_16x16x32_bf16 a[28:31], v[248:251], v[172:175], a[28:31]\n" \
"buffer_load_dwordx4 v20, s[80:83], s85 offen sc1 lds\n" \
"v_mfma_f32_16x16x32_bf16 a[56:59], v[176:179], v[248:251], a[56:59]\n" \
"ds_read_b64_tr_b16 v[104:105], v37 offset:32768\n" \
"v_mfma_f32_16x16x32_bf16 a[0:3], v[148:151], v[116:119], a[0:3]\n" \
"ds_read_b64_tr_b16 v[106:107], v37 offset:33280\n" \
"v_mfma_f32_16x16x32_bf16 a[32:35], v[148:151], v[120:123], a[32:35]\n" \
"s_add_u32 m0, s40, 0x1000\n" \
"s_add_u32 s84, s39, 0x9000\n" \
"v_mfma_f32_16x16x32_bf16 a[4:7], v[152:155], v[116:119], a[4:7]\n" \
"buffer_load_dwordx4 v20, s[80:83], s84 offen sc1 lds\n" \
"v_mfma_f32_16x16x32_bf16 a[36:39], v[152:155], v[120:123], a[36:39]\n" \
"ds_read_b64_tr_b16 v[108:109], v38 offset:32768\n" \
"v_mfma_f32_16x16x32_bf16 a[8:11], v[156:159], v[116:119], a[8:11]\n" \
"ds_read_b64_tr_b16 v[110:111], v38 offset:33280\n" \
"v_mfma_f32_16x16x32_bf16 a[40:43], v[156:159], v[120:123], a[40:43]\n" \
"s_add_u32 m0, s40, 0x3000\n" \
"s_mov_b32 s85, 0x200\n" \
"v_mfma_f32_16x16x32_bf16 a[12:15], v[160:163], v[116:119], a[12:15]\n" \
"buffer_load_dwordx4 v21, s[76:79], s85 offen lds\n" \
"v_mfma_f32_16x16x32_bf16 a[44:47], v[160:163], v[120:123], a[44:47]\n" \
"ds_read_b64_tr_b16 v[112:113], v39 offset:32768\n" \
"v_mfma_f32_16x16x32_bf16 a[16:19], v[164:167], v[116:119], a[16:19]\n" \
"ds_read_b64_tr_b16 v[114:115], v39 offset:33280\n" \
"v_mfma_f32_16x16x32_bf16 a[20:23], v[168:171], v[116:119], a[20:23]\n" \
"s_add_u32 m0, s30, 0x0\n" \
"s_nop 0\n" \
"s_mov_b32 s84, 0x200\n" \
"v_mfma_f32_16x16x32_bf16 a[52:55], v[176:179], v[116:119], a[52:55]\n" \
"buffer_load_dwordx4 v24, s[76:79], s84 offen lds\n" \
"v_mfma_f32_16x16x32_bf16 a[48:51], v[116:119], v[172:175], a[48:51]\n" \
"ds_read_b64_tr_b16 v[84:85], v32 offset:32768\n" \
"v_mfma_f32_16x16x32_bf16 a[64:67], v[148:151], v[124:127], a[64:67]\n" \
"ds_read_b64_tr_b16 v[86:87], v32 offset:33792\n" \
"v_mfma_f32_16x16x32_bf16 a[96:99], v[148:151], v[128:131], a[96:99]\n" \
"ds_read_b128 v[248:251], v22 offset:32768\n" \
"v_mfma_f32_16x16x32_bf16 a[68:71], v[152:155], v[124:127], a[68:71]\n" \
"s_add_u32 m0, s30, 0x400\n" \
"s_mov_b32 s85, 0x2600\n" \
"v_mfma_f32_16x16x32_bf16 a[100:103], v[152:155], v[128:131], a[100:103]\n" \
"buffer_load_dwordx4 v25, s[76:79], s85 offen lds\n" \
"v_mfma_f32_16x16x32_bf16 a[72:75], v[156:159], v[124:127], a[72:75]\n" \
"ds_read_b64_tr_b16 v[88:89], v33 offset:32768\n" \
"v_mfma_f32_16x16x32_bf16 a[104:107], v[156:159], v[128:131], a[104:107]\n" \
"ds_read_b64_tr_b16 v[90:91], v33 offset:33792\n" \
"v_mfma_f32_16x16x32_bf16 a[76:79], v[160:163], v[124:127], a[76:79]\n" \
"s_add_u32 m0, s31, 0x0\n" \
"s_nop 0\n" \
"s_mov_b32 s84, 0x200000\n" \
"v_mfma_f32_16x16x32_bf16 a[108:111], v[160:163], v[128:131], a[108:111]\n" \
"buffer_load_dwordx4 v26, s[68:71], s84 offen lds\n" \
"v_mfma_f32_16x16x32_bf16 a[80:83], v[164:167], v[120:123], a[80:83]\n" \
"ds_read_b64_tr_b16 v[92:93], v34 offset:32768\n" \
"v_mfma_f32_16x16x32_bf16 a[84:87], v[168:171], v[120:123], a[84:87]\n" \
"ds_read_b64_tr_b16 v[94:95], v34 offset:33792\n" \
"v_mfma_f32_16x16x32_bf16 a[116:119], v[176:179], v[120:123], a[116:119]\n" \
"s_add_u32 m0, s30, 0x800\n" \
"s_mov_b32 s85, 0x4a00\n" \
"v_mfma_f32_16x16x32_bf16 a[112:115], v[120:123], v[172:175], a[112:115]\n" \
"buffer_load_dwordx4 v24, s[76:79], s85 offen lds\n" \
"v_mfma_f32_16x16x32_bf16 a[128:131], v[148:151], v[132:135], a[128:131]\n" \
"ds_read_b64_tr_b16 v[96:97], v35 offset:32768\n" \
"v_mfma_f32_16x16x32_bf16 a[160:163], v[148:151], v[136:139], a[160:163]\n" \
"ds_read_b64_tr_b16 v[98:99], v35 offset:33792\n" \
"v_mfma_f32_16x16x32_bf16 a[132:135], v[152:155], v[132:135], a[132:135]\n" \
"s_add_u32 m0, s30, 0xc00\n" \
"s_mov_b32 s84, 0x6e00\n" \
"v_mfma_f32_16x16x32_bf16 a[164:167], v[152:155], v[136:139], a[164:167]\n" \
"buffer_load_dwordx4 v25, s[76:79], s84 offen lds\n" \
"v_mfma_f32_16x16x32_bf16 a[136:139], v[156:159], v[132:135], a[136:139]\n" \
"ds_read_b64_tr_b16 v[100:101], v36 offset:32768\n" \
"v_mfma_f32_16x16x32_bf16 a[168:171], v[156:159], v[136:139], a[168:171]\n" \
"ds_read_b64_tr_b16 v[102:103], v36 offset:33280\n" \
"v_mfma_f32_16x16x32_bf16 a[140:143], v[160:163], v[132:135], a[140:143]\n" \
"ds_read_b128 v[52:55], v28 offset:32768\n" \
"v_mfma_f32_16x16x32_bf16 a[172:175], v[160:163], v[136:139], a[172:175]\n" \
"s_add_u32 m0, s31, 0x400\n" \
"s_mov_b32 s85, 0x208000\n" \
"v_mfma_f32_16x16x32_bf16 a[144:147], v[164:167], v[124:127], a[144:147]\n" \
"buffer_load_dwordx4 v26, s[68:71], s85 offen lds\n" \
"v_mfma_f32_16x16x32_bf16 a[148:151], v[168:171], v[124:127], a[148:151]\n" \
"ds_read_b128 v[56:59], v28 offset:34816\n" \
"v_mfma_f32_16x16x32_bf16 a[180:183], v[176:179], v[124:127], a[180:183]\n" \
"ds_read_b128 v[60:63], v28 offset:36864\n" \
"v_mfma_f32_16x16x32_bf16 a[176:179], v[124:127], v[172:175], a[176:179]\n" \
"s_add_u32 m0, s30, 0x1000\n" \
"s_mov_b32 s84, 0x9200\n" \
"v_mfma_f32_16x16x32_bf16 a[192:195], v[148:151], v[140:143], a[192:195]\n" \
"buffer_load_dwordx4 v24, s[76:79], s84 offen lds\n" \
"v_mfma_f32_16x16x32_bf16 a[224:227], v[148:151], v[144:147], a[224:227]\n" \
"ds_read_b128 v[64:67], v28 offset:38912\n" \
"v_mfma_f32_16x16x32_bf16 a[196:199], v[152:155], v[140:143], a[196:199]\n" \
"ds_read_b128 v[68:71], v28 offset:40960\n" \
"v_mfma_f32_16x16x32_bf16 a[228:231], v[152:155], v[144:147], a[228:231]\n" \
"s_add_u32 m0, s30, 0x1400\n" \
"s_mov_b32 s85, 0xb600\n" \
"v_mfma_f32_16x16x32_bf16 a[200:203], v[156:159], v[140:143], a[200:203]\n" \
"buffer_load_dwordx4 v25, s[76:79], s85 offen lds\n" \
"v_mfma_f32_16x16x32_bf16 a[232:235], v[156:159], v[144:147], a[232:235]\n" \
"ds_read_b128 v[72:75], v28 offset:43008\n" \
"v_mfma_f32_16x16x32_bf16 a[204:207], v[160:163], v[140:143], a[204:207]\n" \
"ds_read_b128 v[76:79], v28 offset:45056\n" \
"v_mfma_f32_16x16x32_bf16 a[236:239], v[160:163], v[144:147], a[236:239]\n" \
"s_add_u32 m0, s31, 0x800\n" \
"s_mov_b32 s84, 0x210000\n" \
"v_mfma_f32_16x16x32_bf16 a[208:211], v[164:167], v[128:131], a[208:211]\n" \
"buffer_load_dwordx4 v27, s[68:71], s84 offen lds\n" \
"v_mfma_f32_16x16x32_bf16 a[212:215], v[168:171], v[128:131], a[212:215]\n" \
"ds_read_b128 v[80:83], v28 offset:47104\n" \
"v_mfma_f32_16x16x32_bf16 a[244:247], v[176:179], v[128:131], a[244:247]\n" \
"v_mfma_f32_16x16x32_bf16 a[240:243], v[128:131], v[172:175], a[240:243]\n" \
"s_waitcnt lgkmcnt(15)\n" \
"v_mfma_f32_16x16x32_bf16 a[24:27], v[104:107], v[248:251], a[24:27]\n" \
"s_add_u32 m0, s30, 0x1800\n" \
"s_mov_b32 s84, 0xda00\n" \
"v_mfma_f32_16x16x32_bf16 a[28:31], v[248:251], v[108:111], a[28:31]\n" \
"buffer_load_dwordx4 v24, s[76:79], s84 offen lds\n" \
"v_mfma_f32_16x16x32_bf16 a[56:59], v[112:115], v[248:251], a[56:59]\n" \
"ds_read_b64_tr_b16 v[168:169], v37 offset:36864\n" \
"s_waitcnt lgkmcnt(8)\n" \
"v_mfma_f32_16x16x32_bf16 a[0:3], v[84:87], v[52:55], a[0:3]\n" \
"s_add_u32 m0, s30, 0x1c00\n" \
"s_mov_b32 s85, 0xfe00\n" \
"s_waitcnt lgkmcnt(7)\n" \
"s_nop 0\n" \
"v_mfma_f32_16x16x32_bf16 a[32:35], v[84:87], v[56:59], a[32:35]\n" \
"buffer_load_dwordx4 v25, s[76:79], s85 offen lds\n" \
"v_mfma_f32_16x16x32_bf16 a[4:7], v[88:91], v[52:55], a[4:7]\n" \
"ds_read_b64_tr_b16 v[170:171], v37 offset:37376\n" \
"v_mfma_f32_16x16x32_bf16 a[36:39], v[88:91], v[56:59], a[36:39]\n" \
"s_add_u32 m0, s31, 0xc00\n" \
"s_mov_b32 s84, 0x218000\n" \
"v_mfma_f32_16x16x32_bf16 a[8:11], v[92:95], v[52:55], a[8:11]\n" \
"buffer_load_dwordx4 v27, s[68:71], s84 offen lds\n" \
"v_mfma_f32_16x16x32_bf16 a[40:43], v[92:95], v[56:59], a[40:43]\n" \
"ds_read_b64_tr_b16 v[172:173], v38 offset:36864\n" \
"v_mfma_f32_16x16x32_bf16 a[12:15], v[96:99], v[52:55], a[12:15]\n" \
"ds_read_b64_tr_b16 v[174:175], v38 offset:37376\n" \
"v_mfma_f32_16x16x32_bf16 a[44:47], v[96:99], v[56:59], a[44:47]\n" \
"ds_read_b64_tr_b16 v[176:177], v39 offset:36864\n" \
"v_mfma_f32_16x16x32_bf16 a[16:19], v[100:103], v[52:55], a[16:19]\n" \
"ds_read_b64_tr_b16 v[178:179], v39 offset:37376\n" \
"v_mfma_f32_16x16x32_bf16 a[20:23], v[104:107], v[52:55], a[20:23]\n" \
"ds_read_b64_tr_b16 v[148:149], v32 offset:40960\n" \
"v_mfma_f32_16x16x32_bf16 a[52:55], v[112:115], v[52:55], a[52:55]\n" \
"ds_read_b64_tr_b16 v[150:151], v32 offset:41984\n" \
"v_mfma_f32_16x16x32_bf16 a[48:51], v[52:55], v[108:111], a[48:51]\n" \
"ds_read_b128 v[248:251], v23 offset:32768\n" \
"s_waitcnt lgkmcnt(14)\n" \
"v_mfma_f32_16x16x32_bf16 a[64:67], v[84:87], v[60:63], a[64:67]\n" \
"ds_read_b64_tr_b16 v[152:153], v33 offset:40960\n" \
"s_waitcnt lgkmcnt(14)\n" \
"v_mfma_f32_16x16x32_bf16 a[96:99], v[84:87], v[64:67], a[96:99]\n" \
"ds_read_b64_tr_b16 v[154:155], v33 offset:41984\n" \
"v_mfma_f32_16x16x32_bf16 a[68:71], v[88:91], v[60:63], a[68:71]\n" \
"ds_read_b64_tr_b16 v[156:157], v34 offset:40960\n" \
"v_mfma_f32_16x16x32_bf16 a[100:103], v[88:91], v[64:67], a[100:103]\n" \
"ds_read_b64_tr_b16 v[158:159], v34 offset:41984\n" \
"v_mfma_f32_16x16x32_bf16 a[72:75], v[92:95], v[60:63], a[72:75]\n" \
"v_mfma_f32_16x16x32_bf16 a[104:107], v[92:95], v[64:67], a[104:107]\n" \
"ds_read_b64_tr_b16 v[160:161], v35 offset:40960\n" \
"v_mfma_f32_16x16x32_bf16 a[76:79], v[96:99], v[60:63], a[76:79]\n" \
"s_add_u32 s89, s41, 0x3000\n" \
"buffer_store_dwordx4 v[232:235], v20, s[80:83], s89 offen offset:0 sc1\n" \
"v_mfma_f32_16x16x32_bf16 a[108:111], v[96:99], v[64:67], a[108:111]\n" \
"ds_read_b64_tr_b16 v[162:163], v35 offset:41984\n" \
"v_mfma_f32_16x16x32_bf16 a[80:83], v[100:103], v[56:59], a[80:83]\n" \
"ds_read_b64_tr_b16 v[164:165], v36 offset:36864\n" \
"v_mfma_f32_16x16x32_bf16 a[84:87], v[104:107], v[56:59], a[84:87]\n" \
"ds_read_b64_tr_b16 v[166:167], v36 offset:37376\n" \
"v_mfma_f32_16x16x32_bf16 a[116:119], v[112:115], v[56:59], a[116:119]\n" \
"v_mfma_f32_16x16x32_bf16 a[112:115], v[56:59], v[108:111], a[112:115]\n" \
"ds_read_b128 v[116:119], v29 offset:32768\n" \
"s_waitcnt lgkmcnt(15)\n" \
"s_nop 0\n" \
"v_mfma_f32_16x16x32_bf16 a[128:131], v[84:87], v[68:71], a[128:131]\n" \
"v_mfma_f32_16x16x32_bf16 a[160:163], v[84:87], v[72:75], a[160:163]\n" \
"ds_read_b128 v[120:123], v29 offset:34816\n" \
"v_mfma_f32_16x16x32_bf16 a[132:135], v[88:91], v[68:71], a[132:135]\n" \
"s_add_u32 s88, s41, 0x3000\n" \
"buffer_store_dwordx4 v[236:239], v20, s[80:83], s88 offen offset:1024 sc1\n" \
"v_mfma_f32_16x16x32_bf16 a[164:167], v[88:91], v[72:75], a[164:167]\n" \
"ds_read_b128 v[124:127], v29 offset:36864\n" \
"v_mfma_f32_16x16x32_bf16 a[136:139], v[92:95], v[68:71], a[136:139]\n" \
"ds_read_b128 v[128:131], v29 offset:38912\n" \
"v_mfma_f32_16x16x32_bf16 a[168:171], v[92:95], v[72:75], a[168:171]\n" \
"ds_read_b128 v[132:135], v29 offset:40960\n" \
"v_mfma_f32_16x16x32_bf16 a[140:143], v[96:99], v[68:71], a[140:143]\n" \
"v_mfma_f32_16x16x32_bf16 a[172:175], v[96:99], v[72:75], a[172:175]\n" \
"ds_read_b128 v[136:139], v29 offset:43008\n" \
"v_mfma_f32_16x16x32_bf16 a[144:147], v[100:103], v[60:63], a[144:147]\n" \
"ds_read_b128 v[140:143], v29 offset:45056\n" \
"v_mfma_f32_16x16x32_bf16 a[148:151], v[104:107], v[60:63], a[148:151]\n" \
"s_add_u32 s89, s41, 0x3000\n" \
"buffer_store_dwordx4 v[240:243], v20, s[80:83], s89 offen offset:2048 sc1\n" \
"v_mfma_f32_16x16x32_bf16 a[180:183], v[112:115], v[60:63], a[180:183]\n" \
"ds_read_b128 v[144:147], v29 offset:47104\n" \
"v_mfma_f32_16x16x32_bf16 a[176:179], v[60:63], v[108:111], a[176:179]\n" \
"v_mfma_f32_16x16x32_bf16 a[192:195], v[84:87], v[76:79], a[192:195]\n" \
"v_mfma_f32_16x16x32_bf16 a[224:227], v[84:87], v[80:83], a[224:227]\n" \
"v_mfma_f32_16x16x32_bf16 a[196:199], v[88:91], v[76:79], a[196:199]\n" \
"v_mfma_f32_16x16x32_bf16 a[228:231], v[88:91], v[80:83], a[228:231]\n" \
"v_mfma_f32_16x16x32_bf16 a[200:203], v[92:95], v[76:79], a[200:203]\n" \
"s_add_u32 s88, s41, 0x3000\n" \
"buffer_store_dwordx4 v[244:247], v20, s[80:83], s88 offen offset:3072 sc1\n" \
"v_mfma_f32_16x16x32_bf16 a[232:235], v[92:95], v[80:83], a[232:235]\n" \
"v_mfma_f32_16x16x32_bf16 a[204:207], v[96:99], v[76:79], a[204:207]\n" \
"v_mfma_f32_16x16x32_bf16 a[236:239], v[96:99], v[80:83], a[236:239]\n" \
"v_mfma_f32_16x16x32_bf16 a[208:211], v[100:103], v[64:67], a[208:211]\n" \
"v_mfma_f32_16x16x32_bf16 a[212:215], v[104:107], v[64:67], a[212:215]\n" \
"v_mfma_f32_16x16x32_bf16 a[244:247], v[112:115], v[64:67], a[244:247]\n" \
"v_mfma_f32_16x16x32_bf16 a[240:243], v[64:67], v[108:111], a[240:243]\n" \
"s_waitcnt vmcnt(4) lgkmcnt(0)\n" \
"s_barrier\n" \
"v_mfma_f32_16x16x32_bf16 a[24:27], v[168:171], v[248:251], a[24:27]\n" \
"s_add_u32 m0, s40, 0x8000\n" \
"s_add_u32 s85, s39, 0xa000\n" \
"v_mfma_f32_16x16x32_bf16 a[28:31], v[248:251], v[172:175], a[28:31]\n" \
"buffer_load_dwordx4 v20, s[80:83], s85 offen sc1 lds\n" \
"v_mfma_f32_16x16x32_bf16 a[56:59], v[176:179], v[248:251], a[56:59]\n" \
"ds_read_b64_tr_b16 v[104:105], v37 offset:0\n" \
"v_mfma_f32_16x16x32_bf16 a[0:3], v[148:151], v[116:119], a[0:3]\n" \
"ds_read_b64_tr_b16 v[106:107], v37 offset:512\n" \
"v_mfma_f32_16x16x32_bf16 a[32:35], v[148:151], v[120:123], a[32:35]\n" \
"s_add_u32 m0, s40, 0x9000\n" \
"s_add_u32 s84, s39, 0xb000\n" \
"v_mfma_f32_16x16x32_bf16 a[4:7], v[152:155], v[116:119], a[4:7]\n" \
"buffer_load_dwordx4 v20, s[80:83], s84 offen sc1 lds\n" \
"v_mfma_f32_16x16x32_bf16 a[36:39], v[152:155], v[120:123], a[36:39]\n" \
"ds_read_b64_tr_b16 v[108:109], v38 offset:0\n" \
"v_mfma_f32_16x16x32_bf16 a[8:11], v[156:159], v[116:119], a[8:11]\n" \
"ds_read_b64_tr_b16 v[110:111], v38 offset:512\n" \
"v_mfma_f32_16x16x32_bf16 a[40:43], v[156:159], v[120:123], a[40:43]\n" \
"s_add_u32 m0, s40, 0xb000\n" \
"s_mov_b32 s85, 0x280\n" \
"v_mfma_f32_16x16x32_bf16 a[12:15], v[160:163], v[116:119], a[12:15]\n" \
"buffer_load_dwordx4 v21, s[76:79], s85 offen lds\n" \
"v_mfma_f32_16x16x32_bf16 a[44:47], v[160:163], v[120:123], a[44:47]\n" \
"ds_read_b64_tr_b16 v[112:113], v39 offset:0\n" \
"v_mfma_f32_16x16x32_bf16 a[16:19], v[164:167], v[116:119], a[16:19]\n" \
"ds_read_b64_tr_b16 v[114:115], v39 offset:512\n" \
"v_mfma_f32_16x16x32_bf16 a[20:23], v[168:171], v[116:119], a[20:23]\n" \
"s_add_u32 m0, s30, 0x8000\n" \
"s_mov_b32 s84, 0x280\n" \
"v_mfma_f32_16x16x32_bf16 a[52:55], v[176:179], v[116:119], a[52:55]\n" \
"buffer_load_dwordx4 v24, s[76:79], s84 offen lds\n" \
"v_mfma_f32_16x16x32_bf16 a[48:51], v[116:119], v[172:175], a[48:51]\n" \
"ds_read_b64_tr_b16 v[84:85], v32 offset:0\n" \
"v_mfma_f32_16x16x32_bf16 a[64:67], v[148:151], v[124:127], a[64:67]\n" \
"ds_read_b64_tr_b16 v[86:87], v32 offset:1024\n" \
"v_mfma_f32_16x16x32_bf16 a[96:99], v[148:151], v[128:131], a[96:99]\n" \
"ds_read_b128 v[248:251], v22 offset:0\n" \
"v_mfma_f32_16x16x32_bf16 a[68:71], v[152:155], v[124:127], a[68:71]\n" \
"s_add_u32 m0, s30, 0x8400\n" \
"s_mov_b32 s85, 0x2680\n" \
"v_mfma_f32_16x16x32_bf16 a[100:103], v[152:155], v[128:131], a[100:103]\n" \
"buffer_load_dwordx4 v25, s[76:79], s85 offen lds\n" \
"v_mfma_f32_16x16x32_bf16 a[72:75], v[156:159], v[124:127], a[72:75]\n" \
"ds_read_b64_tr_b16 v[88:89], v33 offset:0\n" \
"v_mfma_f32_16x16x32_bf16 a[104:107], v[156:159], v[128:131], a[104:107]\n" \
"ds_read_b64_tr_b16 v[90:91], v33 offset:1024\n" \
"v_mfma_f32_16x16x32_bf16 a[76:79], v[160:163], v[124:127], a[76:79]\n" \
"s_add_u32 m0, s31, 0x8000\n" \
"s_mov_b32 s84, 0x280000\n" \
"v_mfma_f32_16x16x32_bf16 a[108:111], v[160:163], v[128:131], a[108:111]\n" \
"buffer_load_dwordx4 v26, s[68:71], s84 offen lds\n" \
"v_mfma_f32_16x16x32_bf16 a[80:83], v[164:167], v[120:123], a[80:83]\n" \
"ds_read_b64_tr_b16 v[92:93], v34 offset:0\n" \
"v_mfma_f32_16x16x32_bf16 a[84:87], v[168:171], v[120:123], a[84:87]\n" \
"ds_read_b64_tr_b16 v[94:95], v34 offset:1024\n" \
"v_mfma_f32_16x16x32_bf16 a[116:119], v[176:179], v[120:123], a[116:119]\n" \
"s_add_u32 m0, s30, 0x8800\n" \
"s_mov_b32 s85, 0x4a80\n" \
"v_mfma_f32_16x16x32_bf16 a[112:115], v[120:123], v[172:175], a[112:115]\n" \
"buffer_load_dwordx4 v24, s[76:79], s85 offen lds\n" \
"v_mfma_f32_16x16x32_bf16 a[128:131], v[148:151], v[132:135], a[128:131]\n" \
"ds_read_b64_tr_b16 v[96:97], v35 offset:0\n" \
"v_mfma_f32_16x16x32_bf16 a[160:163], v[148:151], v[136:139], a[160:163]\n" \
"ds_read_b64_tr_b16 v[98:99], v35 offset:1024\n" \
"v_mfma_f32_16x16x32_bf16 a[132:135], v[152:155], v[132:135], a[132:135]\n" \
"s_add_u32 m0, s30, 0x8c00\n" \
"s_mov_b32 s84, 0x6e80\n" \
"v_mfma_f32_16x16x32_bf16 a[164:167], v[152:155], v[136:139], a[164:167]\n" \
"buffer_load_dwordx4 v25, s[76:79], s84 offen lds\n" \
"v_mfma_f32_16x16x32_bf16 a[136:139], v[156:159], v[132:135], a[136:139]\n" \
"ds_read_b64_tr_b16 v[100:101], v36 offset:0\n" \
"v_mfma_f32_16x16x32_bf16 a[168:171], v[156:159], v[136:139], a[168:171]\n" \
"ds_read_b64_tr_b16 v[102:103], v36 offset:512\n" \
"v_mfma_f32_16x16x32_bf16 a[140:143], v[160:163], v[132:135], a[140:143]\n" \
"ds_read_b128 v[52:55], v28 offset:0\n" \
"v_mfma_f32_16x16x32_bf16 a[172:175], v[160:163], v[136:139], a[172:175]\n" \
"s_add_u32 m0, s31, 0x8400\n" \
"s_mov_b32 s85, 0x288000\n" \
"v_mfma_f32_16x16x32_bf16 a[144:147], v[164:167], v[124:127], a[144:147]\n" \
"buffer_load_dwordx4 v26, s[68:71], s85 offen lds\n" \
"v_mfma_f32_16x16x32_bf16 a[148:151], v[168:171], v[124:127], a[148:151]\n" \
"ds_read_b128 v[56:59], v28 offset:2048\n" \
"v_mfma_f32_16x16x32_bf16 a[180:183], v[176:179], v[124:127], a[180:183]\n" \
"ds_read_b128 v[60:63], v28 offset:4096\n" \
"v_mfma_f32_16x16x32_bf16 a[176:179], v[124:127], v[172:175], a[176:179]\n" \
"s_add_u32 m0, s30, 0x9000\n" \
"s_mov_b32 s84, 0x9280\n" \
"v_mfma_f32_16x16x32_bf16 a[192:195], v[148:151], v[140:143], a[192:195]\n" \
"buffer_load_dwordx4 v24, s[76:79], s84 offen lds\n" \
"v_mfma_f32_16x16x32_bf16 a[224:227], v[148:151], v[144:147], a[224:227]\n" \
"ds_read_b128 v[64:67], v28 offset:6144\n" \
"v_mfma_f32_16x16x32_bf16 a[196:199], v[152:155], v[140:143], a[196:199]\n" \
"ds_read_b128 v[68:71], v28 offset:8192\n" \
"v_mfma_f32_16x16x32_bf16 a[228:231], v[152:155], v[144:147], a[228:231]\n" \
"s_add_u32 m0, s30, 0x9400\n" \
"s_mov_b32 s85, 0xb680\n" \
"v_mfma_f32_16x16x32_bf16 a[200:203], v[156:159], v[140:143], a[200:203]\n" \
"buffer_load_dwordx4 v25, s[76:79], s85 offen lds\n" \
"v_mfma_f32_16x16x32_bf16 a[232:235], v[156:159], v[144:147], a[232:235]\n" \
"ds_read_b128 v[72:75], v28 offset:10240\n" \
"v_mfma_f32_16x16x32_bf16 a[204:207], v[160:163], v[140:143], a[204:207]\n" \
"ds_read_b128 v[76:79], v28 offset:12288\n" \
"v_mfma_f32_16x16x32_bf16 a[236:239], v[160:163], v[144:147], a[236:239]\n" \
"s_add_u32 m0, s31, 0x8800\n" \
"s_mov_b32 s84, 0x290000\n" \
"v_mfma_f32_16x16x32_bf16 a[208:211], v[164:167], v[128:131], a[208:211]\n" \
"buffer_load_dwordx4 v27, s[68:71], s84 offen lds\n" \
"v_mfma_f32_16x16x32_bf16 a[212:215], v[168:171], v[128:131], a[212:215]\n" \
"ds_read_b128 v[80:83], v28 offset:14336\n" \
"v_mfma_f32_16x16x32_bf16 a[244:247], v[176:179], v[128:131], a[244:247]\n" \
"v_mfma_f32_16x16x32_bf16 a[240:243], v[128:131], v[172:175], a[240:243]\n" \
"s_waitcnt lgkmcnt(15)\n" \
"v_mfma_f32_16x16x32_bf16 a[24:27], v[104:107], v[248:251], a[24:27]\n" \
"s_add_u32 m0, s30, 0x9800\n" \
"s_mov_b32 s84, 0xda80\n" \
"v_mfma_f32_16x16x32_bf16 a[28:31], v[248:251], v[108:111], a[28:31]\n" \
"buffer_load_dwordx4 v24, s[76:79], s84 offen lds\n" \
"v_mfma_f32_16x16x32_bf16 a[56:59], v[112:115], v[248:251], a[56:59]\n" \
"ds_read_b64_tr_b16 v[168:169], v37 offset:4096\n" \
"s_waitcnt lgkmcnt(8)\n" \
"v_mfma_f32_16x16x32_bf16 a[0:3], v[84:87], v[52:55], a[0:3]\n" \
"s_add_u32 m0, s30, 0x9c00\n" \
"s_mov_b32 s85, 0xfe80\n" \
"s_waitcnt lgkmcnt(7)\n" \
"s_nop 0\n" \
"v_mfma_f32_16x16x32_bf16 a[32:35], v[84:87], v[56:59], a[32:35]\n" \
"buffer_load_dwordx4 v25, s[76:79], s85 offen lds\n" \
"v_mfma_f32_16x16x32_bf16 a[4:7], v[88:91], v[52:55], a[4:7]\n" \
"ds_read_b64_tr_b16 v[170:171], v37 offset:4608\n" \
"v_mfma_f32_16x16x32_bf16 a[36:39], v[88:91], v[56:59], a[36:39]\n" \
"s_add_u32 m0, s31, 0x8c00\n" \
"s_mov_b32 s84, 0x298000\n" \
"v_mfma_f32_16x16x32_bf16 a[8:11], v[92:95], v[52:55], a[8:11]\n" \
"buffer_load_dwordx4 v27, s[68:71], s84 offen lds\n" \
"v_mfma_f32_16x16x32_bf16 a[40:43], v[92:95], v[56:59], a[40:43]\n" \
"ds_read_b64_tr_b16 v[172:173], v38 offset:4096\n" \
"v_mfma_f32_16x16x32_bf16 a[12:15], v[96:99], v[52:55], a[12:15]\n" \
"ds_read_b64_tr_b16 v[174:175], v38 offset:4608\n" \
"v_mfma_f32_16x16x32_bf16 a[44:47], v[96:99], v[56:59], a[44:47]\n" \
"ds_read_b64_tr_b16 v[176:177], v39 offset:4096\n" \
"v_mfma_f32_16x16x32_bf16 a[16:19], v[100:103], v[52:55], a[16:19]\n" \
"ds_read_b64_tr_b16 v[178:179], v39 offset:4608\n" \
"v_mfma_f32_16x16x32_bf16 a[20:23], v[104:107], v[52:55], a[20:23]\n" \
"ds_read_b64_tr_b16 v[148:149], v32 offset:8192\n" \
"v_mfma_f32_16x16x32_bf16 a[52:55], v[112:115], v[52:55], a[52:55]\n" \
"ds_read_b64_tr_b16 v[150:151], v32 offset:9216\n" \
"v_mfma_f32_16x16x32_bf16 a[48:51], v[52:55], v[108:111], a[48:51]\n" \
"ds_read_b128 v[248:251], v23 offset:0\n" \
"s_waitcnt lgkmcnt(14)\n" \
"v_mfma_f32_16x16x32_bf16 a[64:67], v[84:87], v[60:63], a[64:67]\n" \
"ds_read_b64_tr_b16 v[152:153], v33 offset:8192\n" \
"s_waitcnt lgkmcnt(14)\n" \
"v_mfma_f32_16x16x32_bf16 a[96:99], v[84:87], v[64:67], a[96:99]\n" \
"ds_read_b64_tr_b16 v[154:155], v33 offset:9216\n" \
"v_mfma_f32_16x16x32_bf16 a[68:71], v[88:91], v[60:63], a[68:71]\n" \
"ds_read_b64_tr_b16 v[156:157], v34 offset:8192\n" \
"v_mfma_f32_16x16x32_bf16 a[100:103], v[88:91], v[64:67], a[100:103]\n" \
"ds_read_b64_tr_b16 v[158:159], v34 offset:9216\n" \
"v_mfma_f32_16x16x32_bf16 a[72:75], v[92:95], v[60:63], a[72:75]\n" \
"v_mfma_f32_16x16x32_bf16 a[104:107], v[92:95], v[64:67], a[104:107]\n" \
"ds_read_b64_tr_b16 v[160:161], v35 offset:8192\n" \
"v_mfma_f32_16x16x32_bf16 a[76:79], v[96:99], v[60:63], a[76:79]\n" \
"ds_read_b64_tr_b16 v[162:163], v35 offset:9216\n" \
"v_mfma_f32_16x16x32_bf16 a[108:111], v[96:99], v[64:67], a[108:111]\n" \
"v_mfma_f32_16x16x32_bf16 a[80:83], v[100:103], v[56:59], a[80:83]\n" \
"ds_read_b64_tr_b16 v[164:165], v36 offset:4096\n" \
"v_mfma_f32_16x16x32_bf16 a[84:87], v[104:107], v[56:59], a[84:87]\n" \
"ds_read_b64_tr_b16 v[166:167], v36 offset:4608\n" \
"v_mfma_f32_16x16x32_bf16 a[116:119], v[112:115], v[56:59], a[116:119]\n" \
"s_add_u32 s89, s42, 0x0\n" \
"buffer_store_dwordx4 a[60:63], v20, s[80:83], s89 offen offset:3072 sc1\n" \
"v_mfma_f32_16x16x32_bf16 a[112:115], v[56:59], v[108:111], a[112:115]\n" \
"ds_read_b128 v[116:119], v29 offset:0\n" \
"s_waitcnt lgkmcnt(15)\n" \
"v_mfma_f32_16x16x32_bf16 a[128:131], v[84:87], v[68:71], a[128:131]\n" \
"v_mfma_f32_16x16x32_bf16 a[160:163], v[84:87], v[72:75], a[160:163]\n" \
"ds_read_b128 v[120:123], v29 offset:2048\n" \
"v_mfma_f32_16x16x32_bf16 a[132:135], v[88:91], v[68:71], a[132:135]\n" \
"ds_read_b128 v[124:127], v29 offset:4096\n" \
"v_mfma_f32_16x16x32_bf16 a[164:167], v[88:91], v[72:75], a[164:167]\n" \
"v_mfma_f32_16x16x32_bf16 a[136:139], v[92:95], v[68:71], a[136:139]\n" \
"ds_read_b128 v[128:131], v29 offset:6144\n" \
"v_mfma_f32_16x16x32_bf16 a[168:171], v[92:95], v[72:75], a[168:171]\n" \
"ds_read_b128 v[132:135], v29 offset:8192\n" \
"v_mfma_f32_16x16x32_bf16 a[140:143], v[96:99], v[68:71], a[140:143]\n" \
"v_mfma_f32_16x16x32_bf16 a[172:175], v[96:99], v[72:75], a[172:175]\n" \
"s_add_u32 s88, s42, 0x0\n" \
"s_nop 0\n" \
"buffer_store_dwordx4 a[88:91], v20, s[80:83], s88 offen offset:0 sc1\n" \
"v_mfma_f32_16x16x32_bf16 a[144:147], v[100:103], v[60:63], a[144:147]\n" \
"ds_read_b128 v[136:139], v29 offset:10240\n" \
"v_mfma_f32_16x16x32_bf16 a[148:151], v[104:107], v[60:63], a[148:151]\n" \
"ds_read_b128 v[140:143], v29 offset:12288\n" \
"v_mfma_f32_16x16x32_bf16 a[180:183], v[112:115], v[60:63], a[180:183]\n" \
"ds_read_b128 v[144:147], v29 offset:14336\n" \
"v_mfma_f32_16x16x32_bf16 a[176:179], v[60:63], v[108:111], a[176:179]\n" \
"v_mfma_f32_16x16x32_bf16 a[192:195], v[84:87], v[76:79], a[192:195]\n" \
"v_mfma_f32_16x16x32_bf16 a[224:227], v[84:87], v[80:83], a[224:227]\n" \
"v_mfma_f32_16x16x32_bf16 a[196:199], v[88:91], v[76:79], a[196:199]\n" \
"v_mfma_f32_16x16x32_bf16 a[228:231], v[88:91], v[80:83], a[228:231]\n" \
"s_add_u32 s89, s42, 0x0\n" \
"buffer_store_dwordx4 a[92:95], v20, s[80:83], s89 offen offset:1024 sc1\n" \
"v_mfma_f32_16x16x32_bf16 a[200:203], v[92:95], v[76:79], a[200:203]\n" \
"v_mfma_f32_16x16x32_bf16 a[232:235], v[92:95], v[80:83], a[232:235]\n" \
"v_mfma_f32_16x16x32_bf16 a[204:207], v[96:99], v[76:79], a[204:207]\n" \
"v_mfma_f32_16x16x32_bf16 a[236:239], v[96:99], v[80:83], a[236:239]\n" \
"v_mfma_f32_16x16x32_bf16 a[208:211], v[100:103], v[64:67], a[208:211]\n" \
"v_mfma_f32_16x16x32_bf16 a[212:215], v[104:107], v[64:67], a[212:215]\n" \
"v_mfma_f32_16x16x32_bf16 a[244:247], v[112:115], v[64:67], a[244:247]\n" \
"v_mfma_f32_16x16x32_bf16 a[240:243], v[64:67], v[108:111], a[240:243]\n" \
"s_waitcnt vmcnt(3) lgkmcnt(0)\n" \
"s_barrier\n" \
"v_mov_b32_e32 v45, s62\n" \
"s_mov_b64 exec, 1\n" \
"buffer_store_dword v45, off, s[80:83], s37 offset:0 sc1\n" \
"s_mov_b64 exec, -1\n" \
"v_mfma_f32_16x16x32_bf16 a[24:27], v[168:171], v[248:251], a[24:27]\n" \
"s_add_u32 m0, s40, 0x0\n" \
"s_nop 0\n" \
"s_add_u32 s84, s39, 0xc000\n" \
"v_mfma_f32_16x16x32_bf16 a[28:31], v[248:251], v[172:175], a[28:31]\n" \
"buffer_load_dwordx4 v20, s[80:83], s84 offen sc1 lds\n" \
"v_mfma_f32_16x16x32_bf16 a[56:59], v[176:179], v[248:251], a[56:59]\n" \
"ds_read_b64_tr_b16 v[104:105], v37 offset:32768\n" \
"v_mfma_f32_16x16x32_bf16 a[0:3], v[148:151], v[116:119], a[0:3]\n" \
"ds_read_b64_tr_b16 v[106:107], v37 offset:33280\n" \
"v_mfma_f32_16x16x32_bf16 a[32:35], v[148:151], v[120:123], a[32:35]\n" \
"s_add_u32 m0, s40, 0x1000\n" \
"s_add_u32 s85, s39, 0xd000\n" \
"v_mfma_f32_16x16x32_bf16 a[4:7], v[152:155], v[116:119], a[4:7]\n" \
"buffer_load_dwordx4 v20, s[80:83], s85 offen sc1 lds\n" \
"v_mfma_f32_16x16x32_bf16 a[36:39], v[152:155], v[120:123], a[36:39]\n" \
"ds_read_b64_tr_b16 v[108:109], v38 offset:32768\n" \
"v_mfma_f32_16x16x32_bf16 a[8:11], v[156:159], v[116:119], a[8:11]\n" \
"ds_read_b64_tr_b16 v[110:111], v38 offset:33280\n" \
"v_mfma_f32_16x16x32_bf16 a[40:43], v[156:159], v[120:123], a[40:43]\n" \
"s_add_u32 m0, s40, 0x3000\n" \
"s_mov_b32 s84, 0x300\n" \
"v_mfma_f32_16x16x32_bf16 a[12:15], v[160:163], v[116:119], a[12:15]\n" \
"buffer_load_dwordx4 v21, s[76:79], s84 offen lds\n" \
"v_mfma_f32_16x16x32_bf16 a[44:47], v[160:163], v[120:123], a[44:47]\n" \
"ds_read_b64_tr_b16 v[112:113], v39 offset:32768\n" \
"v_mfma_f32_16x16x32_bf16 a[16:19], v[164:167], v[116:119], a[16:19]\n" \
"ds_read_b64_tr_b16 v[114:115], v39 offset:33280\n" \
"v_mfma_f32_16x16x32_bf16 a[20:23], v[168:171], v[116:119], a[20:23]\n" \
"s_add_u32 m0, s30, 0x0\n" \
"s_nop 0\n" \
"s_mov_b32 s85, 0x300\n" \
"v_mfma_f32_16x16x32_bf16 a[52:55], v[176:179], v[116:119], a[52:55]\n" \
"buffer_load_dwordx4 v24, s[76:79], s85 offen lds\n" \
"v_mfma_f32_16x16x32_bf16 a[48:51], v[116:119], v[172:175], a[48:51]\n" \
"ds_read_b64_tr_b16 v[84:85], v32 offset:32768\n" \
"v_mfma_f32_16x16x32_bf16 a[64:67], v[148:151], v[124:127], a[64:67]\n" \
"ds_read_b64_tr_b16 v[86:87], v32 offset:33792\n" \
"v_mfma_f32_16x16x32_bf16 a[96:99], v[148:151], v[128:131], a[96:99]\n" \
"ds_read_b128 v[248:251], v22 offset:32768\n" \
"v_mfma_f32_16x16x32_bf16 a[68:71], v[152:155], v[124:127], a[68:71]\n" \
"s_add_u32 m0, s30, 0x400\n" \
"s_mov_b32 s84, 0x2700\n" \
"v_mfma_f32_16x16x32_bf16 a[100:103], v[152:155], v[128:131], a[100:103]\n" \
"buffer_load_dwordx4 v25, s[76:79], s84 offen lds\n" \
"v_mfma_f32_16x16x32_bf16 a[72:75], v[156:159], v[124:127], a[72:75]\n" \
"ds_read_b64_tr_b16 v[88:89], v33 offset:32768\n" \
"v_mfma_f32_16x16x32_bf16 a[104:107], v[156:159], v[128:131], a[104:107]\n" \
"ds_read_b64_tr_b16 v[90:91], v33 offset:33792\n" \
"v_mfma_f32_16x16x32_bf16 a[76:79], v[160:163], v[124:127], a[76:79]\n" \
"s_add_u32 m0, s31, 0x0\n" \
"s_nop 0\n" \
"s_mov_b32 s85, 0x300000\n" \
"v_mfma_f32_16x16x32_bf16 a[108:111], v[160:163], v[128:131], a[108:111]\n" \
"buffer_load_dwordx4 v26, s[68:71], s85 offen lds\n" \
"v_mfma_f32_16x16x32_bf16 a[80:83], v[164:167], v[120:123], a[80:83]\n" \
"ds_read_b64_tr_b16 v[92:93], v34 offset:32768\n" \
"v_mfma_f32_16x16x32_bf16 a[84:87], v[168:171], v[120:123], a[84:87]\n" \
"ds_read_b64_tr_b16 v[94:95], v34 offset:33792\n" \
"v_mfma_f32_16x16x32_bf16 a[116:119], v[176:179], v[120:123], a[116:119]\n" \
"s_add_u32 m0, s30, 0x800\n" \
"s_mov_b32 s84, 0x4b00\n" \
"v_mfma_f32_16x16x32_bf16 a[112:115], v[120:123], v[172:175], a[112:115]\n" \
"buffer_load_dwordx4 v24, s[76:79], s84 offen lds\n" \
"v_mfma_f32_16x16x32_bf16 a[128:131], v[148:151], v[132:135], a[128:131]\n" \
"ds_read_b64_tr_b16 v[96:97], v35 offset:32768\n" \
"v_mfma_f32_16x16x32_bf16 a[160:163], v[148:151], v[136:139], a[160:163]\n" \
"ds_read_b64_tr_b16 v[98:99], v35 offset:33792\n" \
"v_mfma_f32_16x16x32_bf16 a[132:135], v[152:155], v[132:135], a[132:135]\n" \
"s_add_u32 m0, s30, 0xc00\n" \
"s_mov_b32 s85, 0x6f00\n" \
"v_mfma_f32_16x16x32_bf16 a[164:167], v[152:155], v[136:139], a[164:167]\n" \
"buffer_load_dwordx4 v25, s[76:79], s85 offen lds\n" \
"v_mfma_f32_16x16x32_bf16 a[136:139], v[156:159], v[132:135], a[136:139]\n" \
"ds_read_b64_tr_b16 v[100:101], v36 offset:32768\n" \
"v_mfma_f32_16x16x32_bf16 a[168:171], v[156:159], v[136:139], a[168:171]\n" \
"ds_read_b64_tr_b16 v[102:103], v36 offset:33280\n" \
"v_mfma_f32_16x16x32_bf16 a[140:143], v[160:163], v[132:135], a[140:143]\n" \
"ds_read_b128 v[52:55], v28 offset:32768\n" \
"v_mfma_f32_16x16x32_bf16 a[172:175], v[160:163], v[136:139], a[172:175]\n" \
"s_add_u32 m0, s31, 0x400\n" \
"s_mov_b32 s84, 0x308000\n" \
"v_mfma_f32_16x16x32_bf16 a[144:147], v[164:167], v[124:127], a[144:147]\n" \
"buffer_load_dwordx4 v26, s[68:71], s84 offen lds\n" \
"v_mfma_f32_16x16x32_bf16 a[148:151], v[168:171], v[124:127], a[148:151]\n" \
"ds_read_b128 v[56:59], v28 offset:34816\n" \
"v_mfma_f32_16x16x32_bf16 a[180:183], v[176:179], v[124:127], a[180:183]\n" \
"ds_read_b128 v[60:63], v28 offset:36864\n" \
"v_mfma_f32_16x16x32_bf16 a[176:179], v[124:127], v[172:175], a[176:179]\n" \
"s_add_u32 m0, s30, 0x1000\n" \
"s_mov_b32 s85, 0x9300\n" \
"v_mfma_f32_16x16x32_bf16 a[192:195], v[148:151], v[140:143], a[192:195]\n" \
"buffer_load_dwordx4 v24, s[76:79], s85 offen lds\n" \
"v_mfma_f32_16x16x32_bf16 a[224:227], v[148:151], v[144:147], a[224:227]\n" \
"ds_read_b128 v[64:67], v28 offset:38912\n" \
"v_mfma_f32_16x16x32_bf16 a[196:199], v[152:155], v[140:143], a[196:199]\n" \
"ds_read_b128 v[68:71], v28 offset:40960\n" \
"v_mfma_f32_16x16x32_bf16 a[228:231], v[152:155], v[144:147], a[228:231]\n" \
"s_add_u32 m0, s30, 0x1400\n" \
"s_mov_b32 s84, 0xb700\n" \
"v_mfma_f32_16x16x32_bf16 a[200:203], v[156:159], v[140:143], a[200:203]\n" \
"buffer_load_dwordx4 v25, s[76:79], s84 offen lds\n" \
"v_mfma_f32_16x16x32_bf16 a[232:235], v[156:159], v[144:147], a[232:235]\n" \
"ds_read_b128 v[72:75], v28 offset:43008\n" \
"v_mfma_f32_16x16x32_bf16 a[204:207], v[160:163], v[140:143], a[204:207]\n" \
"ds_read_b128 v[76:79], v28 offset:45056\n" \
"v_mfma_f32_16x16x32_bf16 a[236:239], v[160:163], v[144:147], a[236:239]\n" \
"s_add_u32 m0, s31, 0x800\n" \
"s_mov_b32 s85, 0x310000\n" \
"v_mfma_f32_16x16x32_bf16 a[208:211], v[164:167], v[128:131], a[208:211]\n" \
"buffer_load_dwordx4 v27, s[68:71], s85 offen lds\n" \
"v_mfma_f32_16x16x32_bf16 a[212:215], v[168:171], v[128:131], a[212:215]\n" \
"ds_read_b128 v[80:83], v28 offset:47104\n" \
"v_mfma_f32_16x16x32_bf16 a[244:247], v[176:179], v[128:131], a[244:247]\n" \
"v_mfma_f32_16x16x32_bf16 a[240:243], v[128:131], v[172:175], a[240:243]\n" \
"s_waitcnt lgkmcnt(15)\n" \
"v_mfma_f32_16x16x32_bf16 a[24:27], v[104:107], v[248:251], a[24:27]\n" \
"s_add_u32 m0, s30, 0x1800\n" \
"s_mov_b32 s85, 0xdb00\n" \
"v_mfma_f32_16x16x32_bf16 a[28:31], v[248:251], v[108:111], a[28:31]\n" \
"buffer_load_dwordx4 v24, s[76:79], s85 offen lds\n" \
"v_mfma_f32_16x16x32_bf16 a[56:59], v[112:115], v[248:251], a[56:59]\n" \
"ds_read_b64_tr_b16 v[168:169], v37 offset:36864\n" \
"s_waitcnt lgkmcnt(8)\n" \
"v_mfma_f32_16x16x32_bf16 a[0:3], v[84:87], v[52:55], a[0:3]\n" \
"s_add_u32 m0, s30, 0x1c00\n" \
"s_mov_b32 s84, 0xff00\n" \
"s_waitcnt lgkmcnt(7)\n" \
"s_nop 0\n" \
"v_mfma_f32_16x16x32_bf16 a[32:35], v[84:87], v[56:59], a[32:35]\n" \
"buffer_load_dwordx4 v25, s[76:79], s84 offen lds\n" \
"v_mfma_f32_16x16x32_bf16 a[4:7], v[88:91], v[52:55], a[4:7]\n" \
"ds_read_b64_tr_b16 v[170:171], v37 offset:37376\n" \
"v_mfma_f32_16x16x32_bf16 a[36:39], v[88:91], v[56:59], a[36:39]\n" \
"s_add_u32 m0, s31, 0xc00\n" \
"s_mov_b32 s85, 0x318000\n" \
"v_mfma_f32_16x16x32_bf16 a[8:11], v[92:95], v[52:55], a[8:11]\n" \
"buffer_load_dwordx4 v27, s[68:71], s85 offen lds\n" \
"v_mfma_f32_16x16x32_bf16 a[40:43], v[92:95], v[56:59], a[40:43]\n" \
"ds_read_b64_tr_b16 v[172:173], v38 offset:36864\n" \
"v_mfma_f32_16x16x32_bf16 a[12:15], v[96:99], v[52:55], a[12:15]\n" \
"ds_read_b64_tr_b16 v[174:175], v38 offset:37376\n" \
"v_mfma_f32_16x16x32_bf16 a[44:47], v[96:99], v[56:59], a[44:47]\n" \
"ds_read_b64_tr_b16 v[176:177], v39 offset:36864\n" \
"v_mfma_f32_16x16x32_bf16 a[16:19], v[100:103], v[52:55], a[16:19]\n" \
"ds_read_b64_tr_b16 v[178:179], v39 offset:37376\n" \
"v_mfma_f32_16x16x32_bf16 a[20:23], v[104:107], v[52:55], a[20:23]\n" \
"ds_read_b64_tr_b16 v[148:149], v32 offset:40960\n" \
"v_mfma_f32_16x16x32_bf16 a[52:55], v[112:115], v[52:55], a[52:55]\n" \
"ds_read_b64_tr_b16 v[150:151], v32 offset:41984\n" \
"v_mfma_f32_16x16x32_bf16 a[48:51], v[52:55], v[108:111], a[48:51]\n" \
"ds_read_b128 v[248:251], v23 offset:32768\n" \
"s_waitcnt lgkmcnt(14)\n" \
"v_mfma_f32_16x16x32_bf16 a[64:67], v[84:87], v[60:63], a[64:67]\n" \
"ds_read_b64_tr_b16 v[152:153], v33 offset:40960\n" \
"s_waitcnt lgkmcnt(14)\n" \
"v_mfma_f32_16x16x32_bf16 a[96:99], v[84:87], v[64:67], a[96:99]\n" \
"ds_read_b64_tr_b16 v[154:155], v33 offset:41984\n" \
"v_mfma_f32_16x16x32_bf16 a[68:71], v[88:91], v[60:63], a[68:71]\n" \
"ds_read_b64_tr_b16 v[156:157], v34 offset:40960\n" \
"v_mfma_f32_16x16x32_bf16 a[100:103], v[88:91], v[64:67], a[100:103]\n" \
"ds_read_b64_tr_b16 v[158:159], v34 offset:41984\n" \
"v_mfma_f32_16x16x32_bf16 a[72:75], v[92:95], v[60:63], a[72:75]\n" \
"v_mfma_f32_16x16x32_bf16 a[104:107], v[92:95], v[64:67], a[104:107]\n" \
"ds_read_b64_tr_b16 v[160:161], v35 offset:40960\n" \
"v_mfma_f32_16x16x32_bf16 a[76:79], v[96:99], v[60:63], a[76:79]\n" \
"ds_read_b64_tr_b16 v[162:163], v35 offset:41984\n" \
"v_mfma_f32_16x16x32_bf16 a[108:111], v[96:99], v[64:67], a[108:111]\n" \
"v_mfma_f32_16x16x32_bf16 a[80:83], v[100:103], v[56:59], a[80:83]\n" \
"ds_read_b64_tr_b16 v[164:165], v36 offset:36864\n" \
"v_mfma_f32_16x16x32_bf16 a[84:87], v[104:107], v[56:59], a[84:87]\n" \
"ds_read_b64_tr_b16 v[166:167], v36 offset:37376\n" \
"v_mfma_f32_16x16x32_bf16 a[116:119], v[112:115], v[56:59], a[116:119]\n" \
"s_add_u32 s88, s42, 0x0\n" \
"buffer_store_dwordx4 a[120:123], v20, s[80:83], s88 offen offset:2048 sc1\n" \
"v_mfma_f32_16x16x32_bf16 a[112:115], v[56:59], v[108:111], a[112:115]\n" \
"ds_read_b128 v[116:119], v29 offset:32768\n" \
"s_waitcnt lgkmcnt(15)\n" \
"v_mfma_f32_16x16x32_bf16 a[128:131], v[84:87], v[68:71], a[128:131]\n" \
"v_mfma_f32_16x16x32_bf16 a[160:163], v[84:87], v[72:75], a[160:163]\n" \
"ds_read_b128 v[120:123], v29 offset:34816\n" \
"v_mfma_f32_16x16x32_bf16 a[132:135], v[88:91], v[68:71], a[132:135]\n" \
"ds_read_b128 v[124:127], v29 offset:36864\n" \
"v_mfma_f32_16x16x32_bf16 a[164:167], v[88:91], v[72:75], a[164:167]\n" \
"v_mfma_f32_16x16x32_bf16 a[136:139], v[92:95], v[68:71], a[136:139]\n" \
"ds_read_b128 v[128:131], v29 offset:38912\n" \
"v_mfma_f32_16x16x32_bf16 a[168:171], v[92:95], v[72:75], a[168:171]\n" \
"ds_read_b128 v[132:135], v29 offset:40960\n" \
"v_mfma_f32_16x16x32_bf16 a[140:143], v[96:99], v[68:71], a[140:143]\n" \
"v_mfma_f32_16x16x32_bf16 a[172:175], v[96:99], v[72:75], a[172:175]\n" \
"s_add_u32 s89, s42, 0x1000\n" \
"buffer_store_dwordx4 a[124:127], v20, s[80:83], s89 offen offset:3072 sc1\n" \
"v_mfma_f32_16x16x32_bf16 a[144:147], v[100:103], v[60:63], a[144:147]\n" \
"ds_read_b128 v[136:139], v29 offset:43008\n" \
"v_mfma_f32_16x16x32_bf16 a[148:151], v[104:107], v[60:63], a[148:151]\n" \
"ds_read_b128 v[140:143], v29 offset:45056\n" \
"v_mfma_f32_16x16x32_bf16 a[180:183], v[112:115], v[60:63], a[180:183]\n" \
"ds_read_b128 v[144:147], v29 offset:47104\n" \
"v_mfma_f32_16x16x32_bf16 a[176:179], v[60:63], v[108:111], a[176:179]\n" \
"v_mfma_f32_16x16x32_bf16 a[192:195], v[84:87], v[76:79], a[192:195]\n" \
"v_mfma_f32_16x16x32_bf16 a[224:227], v[84:87], v[80:83], a[224:227]\n" \
"v_mfma_f32_16x16x32_bf16 a[196:199], v[88:91], v[76:79], a[196:199]\n" \
"v_mfma_f32_16x16x32_bf16 a[228:231], v[88:91], v[80:83], a[228:231]\n" \
"s_add_u32 s88, s42, 0x1000\n" \
"buffer_store_dwordx4 a[152:155], v20, s[80:83], s88 offen offset:0 sc1\n" \
"v_mfma_f32_16x16x32_bf16 a[200:203], v[92:95], v[76:79], a[200:203]\n" \
"v_mfma_f32_16x16x32_bf16 a[232:235], v[92:95], v[80:83], a[232:235]\n" \
"v_mfma_f32_16x16x32_bf16 a[204:207], v[96:99], v[76:79], a[204:207]\n" \
"v_mfma_f32_16x16x32_bf16 a[236:239], v[96:99], v[80:83], a[236:239]\n" \
"v_mfma_f32_16x16x32_bf16 a[208:211], v[100:103], v[64:67], a[208:211]\n" \
"v_mfma_f32_16x16x32_bf16 a[212:215], v[104:107], v[64:67], a[212:215]\n" \
"v_mfma_f32_16x16x32_bf16 a[244:247], v[112:115], v[64:67], a[244:247]\n" \
"v_mfma_f32_16x16x32_bf16 a[240:243], v[64:67], v[108:111], a[240:243]\n" \
"s_waitcnt vmcnt(3) lgkmcnt(0)\n" \
"s_barrier\n" \
"v_mfma_f32_16x16x32_bf16 a[24:27], v[168:171], v[248:251], a[24:27]\n" \
"s_add_u32 m0, s40, 0x8000\n" \
"s_add_u32 s85, s39, 0xe000\n" \
"v_mfma_f32_16x16x32_bf16 a[28:31], v[248:251], v[172:175], a[28:31]\n" \
"buffer_load_dwordx4 v20, s[80:83], s85 offen sc1 lds\n" \
"v_mfma_f32_16x16x32_bf16 a[56:59], v[176:179], v[248:251], a[56:59]\n" \
"ds_read_b64_tr_b16 v[104:105], v37 offset:0\n" \
"v_mfma_f32_16x16x32_bf16 a[0:3], v[148:151], v[116:119], a[0:3]\n" \
"ds_read_b64_tr_b16 v[106:107], v37 offset:512\n" \
"v_mfma_f32_16x16x32_bf16 a[32:35], v[148:151], v[120:123], a[32:35]\n" \
"s_add_u32 m0, s40, 0x9000\n" \
"s_add_u32 s84, s39, 0xf000\n" \
"v_mfma_f32_16x16x32_bf16 a[4:7], v[152:155], v[116:119], a[4:7]\n" \
"buffer_load_dwordx4 v20, s[80:83], s84 offen sc1 lds\n" \
"v_mfma_f32_16x16x32_bf16 a[36:39], v[152:155], v[120:123], a[36:39]\n" \
"ds_read_b64_tr_b16 v[108:109], v38 offset:0\n" \
"v_mfma_f32_16x16x32_bf16 a[8:11], v[156:159], v[116:119], a[8:11]\n" \
"ds_read_b64_tr_b16 v[110:111], v38 offset:512\n" \
"v_mfma_f32_16x16x32_bf16 a[40:43], v[156:159], v[120:123], a[40:43]\n" \
"s_add_u32 m0, s40, 0xb000\n" \
"s_mov_b32 s85, 0x380\n" \
"v_mfma_f32_16x16x32_bf16 a[12:15], v[160:163], v[116:119], a[12:15]\n" \
"buffer_load_dwordx4 v21, s[76:79], s85 offen lds\n" \
"v_mfma_f32_16x16x32_bf16 a[44:47], v[160:163], v[120:123], a[44:47]\n" \
"ds_read_b64_tr_b16 v[112:113], v39 offset:0\n" \
"v_mfma_f32_16x16x32_bf16 a[16:19], v[164:167], v[116:119], a[16:19]\n" \
"ds_read_b64_tr_b16 v[114:115], v39 offset:512\n" \
"v_mfma_f32_16x16x32_bf16 a[20:23], v[168:171], v[116:119], a[20:23]\n" \
"s_add_u32 m0, s30, 0x8000\n" \
"s_mov_b32 s84, 0x380\n" \
"v_mfma_f32_16x16x32_bf16 a[52:55], v[176:179], v[116:119], a[52:55]\n" \
"buffer_load_dwordx4 v24, s[76:79], s84 offen lds\n" \
"v_mfma_f32_16x16x32_bf16 a[48:51], v[116:119], v[172:175], a[48:51]\n" \
"ds_read_b64_tr_b16 v[84:85], v32 offset:0\n" \
"v_mfma_f32_16x16x32_bf16 a[64:67], v[148:151], v[124:127], a[64:67]\n" \
"ds_read_b64_tr_b16 v[86:87], v32 offset:1024\n" \
"v_mfma_f32_16x16x32_bf16 a[96:99], v[148:151], v[128:131], a[96:99]\n" \
"ds_read_b128 v[248:251], v22 offset:0\n" \
"v_mfma_f32_16x16x32_bf16 a[68:71], v[152:155], v[124:127], a[68:71]\n" \
"s_add_u32 m0, s30, 0x8400\n" \
"s_mov_b32 s85, 0x2780\n" \
"v_mfma_f32_16x16x32_bf16 a[100:103], v[152:155], v[128:131], a[100:103]\n" \
"buffer_load_dwordx4 v25, s[76:79], s85 offen lds\n" \
"v_mfma_f32_16x16x32_bf16 a[72:75], v[156:159], v[124:127], a[72:75]\n" \
"ds_read_b64_tr_b16 v[88:89], v33 offset:0\n" \
"v_mfma_f32_16x16x32_bf16 a[104:107], v[156:159], v[128:131], a[104:107]\n" \
"ds_read_b64_tr_b16 v[90:91], v33 offset:1024\n" \
"v_mfma_f32_16x16x32_bf16 a[76:79], v[160:163], v[124:127], a[76:79]\n" \
"s_add_u32 m0, s31, 0x8000\n" \
"s_mov_b32 s84, 0x380000\n" \
"v_mfma_f32_16x16x32_bf16 a[108:111], v[160:163], v[128:131], a[108:111]\n" \
"buffer_load_dwordx4 v26, s[68:71], s84 offen lds\n" \
"v_mfma_f32_16x16x32_bf16 a[80:83], v[164:167], v[120:123], a[80:83]\n" \
"ds_read_b64_tr_b16 v[92:93], v34 offset:0\n" \
"v_mfma_f32_16x16x32_bf16 a[84:87], v[168:171], v[120:123], a[84:87]\n" \
"ds_read_b64_tr_b16 v[94:95], v34 offset:1024\n" \
"v_mfma_f32_16x16x32_bf16 a[116:119], v[176:179], v[120:123], a[116:119]\n" \
"s_add_u32 m0, s30, 0x8800\n" \
"s_mov_b32 s85, 0x4b80\n" \
"v_mfma_f32_16x16x32_bf16 a[112:115], v[120:123], v[172:175], a[112:115]\n" \
"buffer_load_dwordx4 v24, s[76:79], s85 offen lds\n" \
"v_mfma_f32_16x16x32_bf16 a[128:131], v[148:151], v[132:135], a[128:131]\n" \
"ds_read_b64_tr_b16 v[96:97], v35 offset:0\n" \
"v_mfma_f32_16x16x32_bf16 a[160:163], v[148:151], v[136:139], a[160:163]\n" \
"ds_read_b64_tr_b16 v[98:99], v35 offset:1024\n" \
"v_mfma_f32_16x16x32_bf16 a[132:135], v[152:155], v[132:135], a[132:135]\n" \
"s_add_u32 m0, s30, 0x8c00\n" \
"s_mov_b32 s84, 0x6f80\n" \
"v_mfma_f32_16x16x32_bf16 a[164:167], v[152:155], v[136:139], a[164:167]\n" \
"buffer_load_dwordx4 v25, s[76:79], s84 offen lds\n" \
"v_mfma_f32_16x16x32_bf16 a[136:139], v[156:159], v[132:135], a[136:139]\n" \
"ds_read_b64_tr_b16 v[100:101], v36 offset:0\n" \
"v_mfma_f32_16x16x32_bf16 a[168:171], v[156:159], v[136:139], a[168:171]\n" \
"ds_read_b64_tr_b16 v[102:103], v36 offset:512\n" \
"v_mfma_f32_16x16x32_bf16 a[140:143], v[160:163], v[132:135], a[140:143]\n" \
"ds_read_b128 v[52:55], v28 offset:0\n" \
"v_mfma_f32_16x16x32_bf16 a[172:175], v[160:163], v[136:139], a[172:175]\n" \
"s_add_u32 m0, s31, 0x8400\n" \
"s_mov_b32 s85, 0x388000\n" \
"v_mfma_f32_16x16x32_bf16 a[144:147], v[164:167], v[124:127], a[144:147]\n" \
"buffer_load_dwordx4 v26, s[68:71], s85 offen lds\n" \
"v_mfma_f32_16x16x32_bf16 a[148:151], v[168:171], v[124:127], a[148:151]\n" \
"ds_read_b128 v[56:59], v28 offset:2048\n" \
"v_mfma_f32_16x16x32_bf16 a[180:183], v[176:179], v[124:127], a[180:183]\n" \
"ds_read_b128 v[60:63], v28 offset:4096\n" \
"v_mfma_f32_16x16x32_bf16 a[176:179], v[124:127], v[172:175], a[176:179]\n" \
"s_add_u32 m0, s30, 0x9000\n" \
"s_mov_b32 s84, 0x9380\n" \
"v_mfma_f32_16x16x32_bf16 a[192:195], v[148:151], v[140:143], a[192:195]\n" \
"buffer_load_dwordx4 v24, s[76:79], s84 offen lds\n" \
"v_mfma_f32_16x16x32_bf16 a[224:227], v[148:151], v[144:147], a[224:227]\n" \
"ds_read_b128 v[64:67], v28 offset:6144\n" \
"v_mfma_f32_16x16x32_bf16 a[196:199], v[152:155], v[140:143], a[196:199]\n" \
"ds_read_b128 v[68:71], v28 offset:8192\n" \
"v_mfma_f32_16x16x32_bf16 a[228:231], v[152:155], v[144:147], a[228:231]\n" \
"s_add_u32 m0, s30, 0x9400\n" \
"s_mov_b32 s85, 0xb780\n" \
"v_mfma_f32_16x16x32_bf16 a[200:203], v[156:159], v[140:143], a[200:203]\n" \
"buffer_load_dwordx4 v25, s[76:79], s85 offen lds\n" \
"v_mfma_f32_16x16x32_bf16 a[232:235], v[156:159], v[144:147], a[232:235]\n" \
"ds_read_b128 v[72:75], v28 offset:10240\n" \
"v_mfma_f32_16x16x32_bf16 a[204:207], v[160:163], v[140:143], a[204:207]\n" \
"ds_read_b128 v[76:79], v28 offset:12288\n" \
"v_mfma_f32_16x16x32_bf16 a[236:239], v[160:163], v[144:147], a[236:239]\n" \
"s_add_u32 m0, s31, 0x8800\n" \
"s_mov_b32 s84, 0x390000\n" \
"v_mfma_f32_16x16x32_bf16 a[208:211], v[164:167], v[128:131], a[208:211]\n" \
"buffer_load_dwordx4 v27, s[68:71], s84 offen lds\n" \
"v_mfma_f32_16x16x32_bf16 a[212:215], v[168:171], v[128:131], a[212:215]\n" \
"ds_read_b128 v[80:83], v28 offset:14336\n" \
"v_mfma_f32_16x16x32_bf16 a[244:247], v[176:179], v[128:131], a[244:247]\n" \
"v_mfma_f32_16x16x32_bf16 a[240:243], v[128:131], v[172:175], a[240:243]\n" \
"s_waitcnt lgkmcnt(15)\n" \
"v_mfma_f32_16x16x32_bf16 a[24:27], v[104:107], v[248:251], a[24:27]\n" \
"s_add_u32 m0, s30, 0x9800\n" \
"s_mov_b32 s84, 0xdb80\n" \
"v_mfma_f32_16x16x32_bf16 a[28:31], v[248:251], v[108:111], a[28:31]\n" \
"buffer_load_dwordx4 v24, s[76:79], s84 offen lds\n" \
"v_mfma_f32_16x16x32_bf16 a[56:59], v[112:115], v[248:251], a[56:59]\n" \
"ds_read_b64_tr_b16 v[168:169], v37 offset:4096\n" \
"s_waitcnt lgkmcnt(8)\n" \
"v_mfma_f32_16x16x32_bf16 a[0:3], v[84:87], v[52:55], a[0:3]\n" \
"s_add_u32 m0, s30, 0x9c00\n" \
"s_mov_b32 s85, 0xff80\n" \
"s_waitcnt lgkmcnt(7)\n" \
"s_nop 0\n" \
"v_mfma_f32_16x16x32_bf16 a[32:35], v[84:87], v[56:59], a[32:35]\n" \
"buffer_load_dwordx4 v25, s[76:79], s85 offen lds\n" \
"v_mfma_f32_16x16x32_bf16 a[4:7], v[88:91], v[52:55], a[4:7]\n" \
"ds_read_b64_tr_b16 v[170:171], v37 offset:4608\n" \
"v_mfma_f32_16x16x32_bf16 a[36:39], v[88:91], v[56:59], a[36:39]\n" \
"s_add_u32 m0, s31, 0x8c00\n" \
"s_mov_b32 s84, 0x398000\n" \
"v_mfma_f32_16x16x32_bf16 a[8:11], v[92:95], v[52:55], a[8:11]\n" \
"buffer_load_dwordx4 v27, s[68:71], s84 offen lds\n" \
"v_mfma_f32_16x16x32_bf16 a[40:43], v[92:95], v[56:59], a[40:43]\n" \
"ds_read_b64_tr_b16 v[172:173], v38 offset:4096\n" \
"v_mfma_f32_16x16x32_bf16 a[12:15], v[96:99], v[52:55], a[12:15]\n" \
"ds_read_b64_tr_b16 v[174:175], v38 offset:4608\n" \
"v_mfma_f32_16x16x32_bf16 a[44:47], v[96:99], v[56:59], a[44:47]\n" \
"ds_read_b64_tr_b16 v[176:177], v39 offset:4096\n" \
"v_mfma_f32_16x16x32_bf16 a[16:19], v[100:103], v[52:55], a[16:19]\n" \
"ds_read_b64_tr_b16 v[178:179], v39 offset:4608\n" \
"v_mfma_f32_16x16x32_bf16 a[20:23], v[104:107], v[52:55], a[20:23]\n" \
"ds_read_b64_tr_b16 v[148:149], v32 offset:8192\n" \
"v_mfma_f32_16x16x32_bf16 a[52:55], v[112:115], v[52:55], a[52:55]\n" \
"ds_read_b64_tr_b16 v[150:151], v32 offset:9216\n" \
"v_mfma_f32_16x16x32_bf16 a[48:51], v[52:55], v[108:111], a[48:51]\n" \
"ds_read_b128 v[248:251], v23 offset:0\n" \
"s_waitcnt lgkmcnt(14)\n" \
"v_mfma_f32_16x16x32_bf16 a[64:67], v[84:87], v[60:63], a[64:67]\n" \
"ds_read_b64_tr_b16 v[152:153], v33 offset:8192\n" \
"s_waitcnt lgkmcnt(14)\n" \
"v_mfma_f32_16x16x32_bf16 a[96:99], v[84:87], v[64:67], a[96:99]\n" \
"ds_read_b64_tr_b16 v[154:155], v33 offset:9216\n" \
"v_mfma_f32_16x16x32_bf16 a[68:71], v[88:91], v[60:63], a[68:71]\n" \
"ds_read_b64_tr_b16 v[156:157], v34 offset:8192\n" \
"v_mfma_f32_16x16x32_bf16 a[100:103], v[88:91], v[64:67], a[100:103]\n" \
"ds_read_b64_tr_b16 v[158:159], v34 offset:9216\n" \
"v_mfma_f32_16x16x32_bf16 a[72:75], v[92:95], v[60:63], a[72:75]\n" \
"v_mfma_f32_16x16x32_bf16 a[104:107], v[92:95], v[64:67], a[104:107]\n" \
"ds_read_b64_tr_b16 v[160:161], v35 offset:8192\n" \
"v_mfma_f32_16x16x32_bf16 a[76:79], v[96:99], v[60:63], a[76:79]\n" \
"ds_read_b64_tr_b16 v[162:163], v35 offset:9216\n" \
"v_mfma_f32_16x16x32_bf16 a[108:111], v[96:99], v[64:67], a[108:111]\n" \
"v_mfma_f32_16x16x32_bf16 a[80:83], v[100:103], v[56:59], a[80:83]\n" \
"ds_read_b64_tr_b16 v[164:165], v36 offset:4096\n" \
"v_mfma_f32_16x16x32_bf16 a[84:87], v[104:107], v[56:59], a[84:87]\n" \
"ds_read_b64_tr_b16 v[166:167], v36 offset:4608\n" \
"v_mfma_f32_16x16x32_bf16 a[116:119], v[112:115], v[56:59], a[116:119]\n" \
"s_add_u32 s89, s42, 0x1000\n" \
"buffer_store_dwordx4 a[156:159], v20, s[80:83], s89 offen offset:1024 sc1\n" \
"v_mfma_f32_16x16x32_bf16 a[112:115], v[56:59], v[108:111], a[112:115]\n" \
"ds_read_b128 v[116:119], v29 offset:0\n" \
"s_waitcnt lgkmcnt(15)\n" \
"s_nop 0\n" \
"v_mfma_f32_16x16x32_bf16 a[128:131], v[84:87], v[68:71], a[128:131]\n" \
"v_mfma_f32_16x16x32_bf16 a[160:163], v[84:87], v[72:75], a[160:163]\n" \
"ds_read_b128 v[120:123], v29 offset:2048\n" \
"v_mfma_f32_16x16x32_bf16 a[132:135], v[88:91], v[68:71], a[132:135]\n" \
"ds_read_b128 v[124:127], v29 offset:4096\n" \
"v_mfma_f32_16x16x32_bf16 a[164:167], v[88:91], v[72:75], a[164:167]\n" \
"v_mfma_f32_16x16x32_bf16 a[136:139], v[92:95], v[68:71], a[136:139]\n" \
"ds_read_b128 v[128:131], v29 offset:6144\n" \
"v_mfma_f32_16x16x32_bf16 a[168:171], v[92:95], v[72:75], a[168:171]\n" \
"ds_read_b128 v[132:135], v29 offset:8192\n" \
"v_mfma_f32_16x16x32_bf16 a[140:143], v[96:99], v[68:71], a[140:143]\n" \
"v_mfma_f32_16x16x32_bf16 a[172:175], v[96:99], v[72:75], a[172:175]\n" \
"s_add_u32 s88, s42, 0x1000\n" \
"buffer_store_dwordx4 a[184:187], v20, s[80:83], s88 offen offset:2048 sc1\n" \
"v_mfma_f32_16x16x32_bf16 a[144:147], v[100:103], v[60:63], a[144:147]\n" \
"ds_read_b128 v[136:139], v29 offset:10240\n" \
"v_mfma_f32_16x16x32_bf16 a[148:151], v[104:107], v[60:63], a[148:151]\n" \
"ds_read_b128 v[140:143], v29 offset:12288\n" \
"v_mfma_f32_16x16x32_bf16 a[180:183], v[112:115], v[60:63], a[180:183]\n" \
"ds_read_b128 v[144:147], v29 offset:14336\n" \
"v_mfma_f32_16x16x32_bf16 a[176:179], v[60:63], v[108:111], a[176:179]\n" \
"v_mfma_f32_16x16x32_bf16 a[192:195], v[84:87], v[76:79], a[192:195]\n" \
"v_mfma_f32_16x16x32_bf16 a[224:227], v[84:87], v[80:83], a[224:227]\n" \
"v_mfma_f32_16x16x32_bf16 a[196:199], v[88:91], v[76:79], a[196:199]\n" \
"v_mfma_f32_16x16x32_bf16 a[228:231], v[88:91], v[80:83], a[228:231]\n" \
"s_add_u32 s89, s42, 0x2000\n" \
"buffer_store_dwordx4 a[188:191], v20, s[80:83], s89 offen offset:3072 sc1\n" \
"v_mfma_f32_16x16x32_bf16 a[200:203], v[92:95], v[76:79], a[200:203]\n" \
"v_mfma_f32_16x16x32_bf16 a[232:235], v[92:95], v[80:83], a[232:235]\n" \
"v_mfma_f32_16x16x32_bf16 a[204:207], v[96:99], v[76:79], a[204:207]\n" \
"v_mfma_f32_16x16x32_bf16 a[236:239], v[96:99], v[80:83], a[236:239]\n" \
"v_mfma_f32_16x16x32_bf16 a[208:211], v[100:103], v[64:67], a[208:211]\n" \
"v_mfma_f32_16x16x32_bf16 a[212:215], v[104:107], v[64:67], a[212:215]\n" \
"v_mfma_f32_16x16x32_bf16 a[244:247], v[112:115], v[64:67], a[244:247]\n" \
"v_mfma_f32_16x16x32_bf16 a[240:243], v[64:67], v[108:111], a[240:243]\n" \
"s_waitcnt vmcnt(3) lgkmcnt(0)\n" \
"s_barrier\n" \
"v_mfma_f32_16x16x32_bf16 a[24:27], v[168:171], v[248:251], a[24:27]\n" \
"s_add_u32 m0, s40, 0x0\n" \
"s_nop 0\n" \
"s_add_u32 s84, s39, 0x10000\n" \
"v_mfma_f32_16x16x32_bf16 a[28:31], v[248:251], v[172:175], a[28:31]\n" \
"buffer_load_dwordx4 v20, s[80:83], s84 offen sc1 lds\n" \
"v_mfma_f32_16x16x32_bf16 a[56:59], v[176:179], v[248:251], a[56:59]\n" \
"ds_read_b64_tr_b16 v[104:105], v37 offset:32768\n" \
"v_mfma_f32_16x16x32_bf16 a[0:3], v[148:151], v[116:119], a[0:3]\n" \
"ds_read_b64_tr_b16 v[106:107], v37 offset:33280\n" \
"v_mfma_f32_16x16x32_bf16 a[32:35], v[148:151], v[120:123], a[32:35]\n" \
"s_add_u32 m0, s40, 0x1000\n" \
"s_add_u32 s85, s39, 0x11000\n" \
"v_mfma_f32_16x16x32_bf16 a[4:7], v[152:155], v[116:119], a[4:7]\n" \
"buffer_load_dwordx4 v20, s[80:83], s85 offen sc1 lds\n" \
"v_mfma_f32_16x16x32_bf16 a[36:39], v[152:155], v[120:123], a[36:39]\n" \
"ds_read_b64_tr_b16 v[108:109], v38 offset:32768\n" \
"v_mfma_f32_16x16x32_bf16 a[8:11], v[156:159], v[116:119], a[8:11]\n" \
"ds_read_b64_tr_b16 v[110:111], v38 offset:33280\n" \
"v_mfma_f32_16x16x32_bf16 a[40:43], v[156:159], v[120:123], a[40:43]\n" \
"s_add_u32 m0, s40, 0x3000\n" \
"s_mov_b32 s84, 0x400\n" \
"v_mfma_f32_16x16x32_bf16 a[12:15], v[160:163], v[116:119], a[12:15]\n" \
"buffer_load_dwordx4 v21, s[76:79], s84 offen lds\n" \
"v_mfma_f32_16x16x32_bf16 a[44:47], v[160:163], v[120:123], a[44:47]\n" \
"ds_read_b64_tr_b16 v[112:113], v39 offset:32768\n" \
"v_mfma_f32_16x16x32_bf16 a[16:19], v[164:167], v[116:119], a[16:19]\n" \
"ds_read_b64_tr_b16 v[114:115], v39 offset:33280\n" \
"v_mfma_f32_16x16x32_bf16 a[20:23], v[168:171], v[116:119], a[20:23]\n" \
"s_add_u32 m0, s30, 0x0\n" \
"s_nop 0\n" \
"s_mov_b32 s85, 0x400\n" \
"v_mfma_f32_16x16x32_bf16 a[52:55], v[176:179], v[116:119], a[52:55]\n" \
"buffer_load_dwordx4 v24, s[76:79], s85 offen lds\n" \
"v_mfma_f32_16x16x32_bf16 a[48:51], v[116:119], v[172:175], a[48:51]\n" \
"ds_read_b64_tr_b16 v[84:85], v32 offset:32768\n" \
"v_mfma_f32_16x16x32_bf16 a[64:67], v[148:151], v[124:127], a[64:67]\n" \
"ds_read_b64_tr_b16 v[86:87], v32 offset:33792\n" \
"v_mfma_f32_16x16x32_bf16 a[96:99], v[148:151], v[128:131], a[96:99]\n" \
"ds_read_b128 v[248:251], v22 offset:32768\n" \
"v_mfma_f32_16x16x32_bf16 a[68:71], v[152:155], v[124:127], a[68:71]\n" \
"s_add_u32 m0, s30, 0x400\n" \
"s_mov_b32 s84, 0x2800\n" \
"v_mfma_f32_16x16x32_bf16 a[100:103], v[152:155], v[128:131], a[100:103]\n" \
"buffer_load_dwordx4 v25, s[76:79], s84 offen lds\n" \
"v_mfma_f32_16x16x32_bf16 a[72:75], v[156:159], v[124:127], a[72:75]\n" \
"ds_read_b64_tr_b16 v[88:89], v33 offset:32768\n" \
"v_mfma_f32_16x16x32_bf16 a[104:107], v[156:159], v[128:131], a[104:107]\n" \
"ds_read_b64_tr_b16 v[90:91], v33 offset:33792\n" \
"v_mfma_f32_16x16x32_bf16 a[76:79], v[160:163], v[124:127], a[76:79]\n" \
"s_add_u32 m0, s31, 0x0\n" \
"s_nop 0\n" \
"s_mov_b32 s85, 0x400000\n" \
"v_mfma_f32_16x16x32_bf16 a[108:111], v[160:163], v[128:131], a[108:111]\n" \
"buffer_load_dwordx4 v26, s[68:71], s85 offen lds\n" \
"v_mfma_f32_16x16x32_bf16 a[80:83], v[164:167], v[120:123], a[80:83]\n" \
"ds_read_b64_tr_b16 v[92:93], v34 offset:32768\n" \
"v_mfma_f32_16x16x32_bf16 a[84:87], v[168:171], v[120:123], a[84:87]\n" \
"ds_read_b64_tr_b16 v[94:95], v34 offset:33792\n" \
"v_mfma_f32_16x16x32_bf16 a[116:119], v[176:179], v[120:123], a[116:119]\n" \
"s_add_u32 m0, s30, 0x800\n" \
"s_mov_b32 s84, 0x4c00\n" \
"v_mfma_f32_16x16x32_bf16 a[112:115], v[120:123], v[172:175], a[112:115]\n" \
"buffer_load_dwordx4 v24, s[76:79], s84 offen lds\n" \
"v_mfma_f32_16x16x32_bf16 a[128:131], v[148:151], v[132:135], a[128:131]\n" \
"ds_read_b64_tr_b16 v[96:97], v35 offset:32768\n" \
"v_mfma_f32_16x16x32_bf16 a[160:163], v[148:151], v[136:139], a[160:163]\n" \
"ds_read_b64_tr_b16 v[98:99], v35 offset:33792\n" \
"v_mfma_f32_16x16x32_bf16 a[132:135], v[152:155], v[132:135], a[132:135]\n" \
"s_add_u32 m0, s30, 0xc00\n" \
"s_mov_b32 s85, 0x7000\n" \
"v_mfma_f32_16x16x32_bf16 a[164:167], v[152:155], v[136:139], a[164:167]\n" \
"buffer_load_dwordx4 v25, s[76:79], s85 offen lds\n" \
"v_mfma_f32_16x16x32_bf16 a[136:139], v[156:159], v[132:135], a[136:139]\n" \
"ds_read_b64_tr_b16 v[100:101], v36 offset:32768\n" \
"v_mfma_f32_16x16x32_bf16 a[168:171], v[156:159], v[136:139], a[168:171]\n" \
"ds_read_b64_tr_b16 v[102:103], v36 offset:33280\n" \
"v_mfma_f32_16x16x32_bf16 a[140:143], v[160:163], v[132:135], a[140:143]\n" \
"ds_read_b128 v[52:55], v28 offset:32768\n" \
"v_mfma_f32_16x16x32_bf16 a[172:175], v[160:163], v[136:139], a[172:175]\n" \
"s_add_u32 m0, s31, 0x400\n" \
"s_mov_b32 s84, 0x408000\n" \
"v_mfma_f32_16x16x32_bf16 a[144:147], v[164:167], v[124:127], a[144:147]\n" \
"buffer_load_dwordx4 v26, s[68:71], s84 offen lds\n" \
"v_mfma_f32_16x16x32_bf16 a[148:151], v[168:171], v[124:127], a[148:151]\n" \
"ds_read_b128 v[56:59], v28 offset:34816\n" \
"v_mfma_f32_16x16x32_bf16 a[180:183], v[176:179], v[124:127], a[180:183]\n" \
"ds_read_b128 v[60:63], v28 offset:36864\n" \
"v_mfma_f32_16x16x32_bf16 a[176:179], v[124:127], v[172:175], a[176:179]\n" \
"s_add_u32 m0, s30, 0x1000\n" \
"s_mov_b32 s85, 0x9400\n" \
"v_mfma_f32_16x16x32_bf16 a[192:195], v[148:151], v[140:143], a[192:195]\n" \
"buffer_load_dwordx4 v24, s[76:79], s85 offen lds\n" \
"v_mfma_f32_16x16x32_bf16 a[224:227], v[148:151], v[144:147], a[224:227]\n" \
"ds_read_b128 v[64:67], v28 offset:38912\n" \
"v_mfma_f32_16x16x32_bf16 a[196:199], v[152:155], v[140:143], a[196:199]\n" \
"ds_read_b128 v[68:71], v28 offset:40960\n" \
"v_mfma_f32_16x16x32_bf16 a[228:231], v[152:155], v[144:147], a[228:231]\n" \
"s_add_u32 m0, s30, 0x1400\n" \
"s_mov_b32 s84, 0xb800\n" \
"v_mfma_f32_16x16x32_bf16 a[200:203], v[156:159], v[140:143], a[200:203]\n" \
"buffer_load_dwordx4 v25, s[76:79], s84 offen lds\n" \
"v_mfma_f32_16x16x32_bf16 a[232:235], v[156:159], v[144:147], a[232:235]\n" \
"ds_read_b128 v[72:75], v28 offset:43008\n" \
"v_mfma_f32_16x16x32_bf16 a[204:207], v[160:163], v[140:143], a[204:207]\n" \
"ds_read_b128 v[76:79], v28 offset:45056\n" \
"v_mfma_f32_16x16x32_bf16 a[236:239], v[160:163], v[144:147], a[236:239]\n" \
"s_add_u32 m0, s31, 0x800\n" \
"s_mov_b32 s85, 0x410000\n" \
"v_mfma_f32_16x16x32_bf16 a[208:211], v[164:167], v[128:131], a[208:211]\n" \
"buffer_load_dwordx4 v27, s[68:71], s85 offen lds\n" \
"v_mfma_f32_16x16x32_bf16 a[212:215], v[168:171], v[128:131], a[212:215]\n" \
"ds_read_b128 v[80:83], v28 offset:47104\n" \
"v_mfma_f32_16x16x32_bf16 a[244:247], v[176:179], v[128:131], a[244:247]\n" \
"v_mfma_f32_16x16x32_bf16 a[240:243], v[128:131], v[172:175], a[240:243]\n" \
"s_waitcnt lgkmcnt(15)\n" \
"v_mfma_f32_16x16x32_bf16 a[24:27], v[104:107], v[248:251], a[24:27]\n" \
"s_add_u32 m0, s30, 0x1800\n" \
"s_mov_b32 s85, 0xdc00\n" \
"v_mfma_f32_16x16x32_bf16 a[28:31], v[248:251], v[108:111], a[28:31]\n" \
"buffer_load_dwordx4 v24, s[76:79], s85 offen lds\n" \
"v_mfma_f32_16x16x32_bf16 a[56:59], v[112:115], v[248:251], a[56:59]\n" \
"ds_read_b64_tr_b16 v[168:169], v37 offset:36864\n" \
"s_waitcnt lgkmcnt(8)\n" \
"v_mfma_f32_16x16x32_bf16 a[0:3], v[84:87], v[52:55], a[0:3]\n" \
"s_add_u32 m0, s30, 0x1c00\n" \
"s_mov_b32 s84, 0x10000\n" \
"s_waitcnt lgkmcnt(7)\n" \
"s_nop 0\n" \
"v_mfma_f32_16x16x32_bf16 a[32:35], v[84:87], v[56:59], a[32:35]\n" \
"buffer_load_dwordx4 v25, s[76:79], s84 offen lds\n" \
"v_mfma_f32_16x16x32_bf16 a[4:7], v[88:91], v[52:55], a[4:7]\n" \
"ds_read_b64_tr_b16 v[170:171], v37 offset:37376\n" \
"v_mfma_f32_16x16x32_bf16 a[36:39], v[88:91], v[56:59], a[36:39]\n" \
"s_add_u32 m0, s31, 0xc00\n" \
"s_mov_b32 s85, 0x418000\n" \
"v_mfma_f32_16x16x32_bf16 a[8:11], v[92:95], v[52:55], a[8:11]\n" \
"buffer_load_dwordx4 v27, s[68:71], s85 offen lds\n" \
"v_mfma_f32_16x16x32_bf16 a[40:43], v[92:95], v[56:59], a[40:43]\n" \
"ds_read_b64_tr_b16 v[172:173], v38 offset:36864\n" \
"v_mfma_f32_16x16x32_bf16 a[12:15], v[96:99], v[52:55], a[12:15]\n" \
"ds_read_b64_tr_b16 v[174:175], v38 offset:37376\n" \
"v_mfma_f32_16x16x32_bf16 a[44:47], v[96:99], v[56:59], a[44:47]\n" \
"ds_read_b64_tr_b16 v[176:177], v39 offset:36864\n" \
"v_mfma_f32_16x16x32_bf16 a[16:19], v[100:103], v[52:55], a[16:19]\n" \
"ds_read_b64_tr_b16 v[178:179], v39 offset:37376\n" \
"v_mfma_f32_16x16x32_bf16 a[20:23], v[104:107], v[52:55], a[20:23]\n" \
"ds_read_b64_tr_b16 v[148:149], v32 offset:40960\n" \
"v_mfma_f32_16x16x32_bf16 a[52:55], v[112:115], v[52:55], a[52:55]\n" \
"ds_read_b64_tr_b16 v[150:151], v32 offset:41984\n" \
"v_mfma_f32_16x16x32_bf16 a[48:51], v[52:55], v[108:111], a[48:51]\n" \
"ds_read_b128 v[248:251], v23 offset:32768\n" \
"s_waitcnt lgkmcnt(14)\n" \
"v_mfma_f32_16x16x32_bf16 a[64:67], v[84:87], v[60:63], a[64:67]\n" \
"ds_read_b64_tr_b16 v[152:153], v33 offset:40960\n" \
"s_waitcnt lgkmcnt(14)\n" \
"v_mfma_f32_16x16x32_bf16 a[96:99], v[84:87], v[64:67], a[96:99]\n" \
"ds_read_b64_tr_b16 v[154:155], v33 offset:41984\n" \
"v_mfma_f32_16x16x32_bf16 a[68:71], v[88:91], v[60:63], a[68:71]\n" \
"ds_read_b64_tr_b16 v[156:157], v34 offset:40960\n" \
"v_mfma_f32_16x16x32_bf16 a[100:103], v[88:91], v[64:67], a[100:103]\n" \
"ds_read_b64_tr_b16 v[158:159], v34 offset:41984\n" \
"v_mfma_f32_16x16x32_bf16 a[72:75], v[92:95], v[60:63], a[72:75]\n" \
"v_mfma_f32_16x16x32_bf16 a[104:107], v[92:95], v[64:67], a[104:107]\n" \
"ds_read_b64_tr_b16 v[160:161], v35 offset:40960\n" \
"v_mfma_f32_16x16x32_bf16 a[76:79], v[96:99], v[60:63], a[76:79]\n" \
"ds_read_b64_tr_b16 v[162:163], v35 offset:41984\n" \
"v_mfma_f32_16x16x32_bf16 a[108:111], v[96:99], v[64:67], a[108:111]\n" \
"v_mfma_f32_16x16x32_bf16 a[80:83], v[100:103], v[56:59], a[80:83]\n" \
"ds_read_b64_tr_b16 v[164:165], v36 offset:36864\n" \
"v_mfma_f32_16x16x32_bf16 a[84:87], v[104:107], v[56:59], a[84:87]\n" \
"ds_read_b64_tr_b16 v[166:167], v36 offset:37376\n" \
"v_mfma_f32_16x16x32_bf16 a[116:119], v[112:115], v[56:59], a[116:119]\n" \
"s_add_u32 s88, s42, 0x2000\n" \
"buffer_store_dwordx4 a[216:219], v20, s[80:83], s88 offen offset:0 sc1\n" \
"v_mfma_f32_16x16x32_bf16 a[112:115], v[56:59], v[108:111], a[112:115]\n" \
"ds_read_b128 v[116:119], v29 offset:32768\n" \
"s_waitcnt lgkmcnt(15)\n" \
"s_nop 0\n" \
"v_mfma_f32_16x16x32_bf16 a[128:131], v[84:87], v[68:71], a[128:131]\n" \
"v_mfma_f32_16x16x32_bf16 a[160:163], v[84:87], v[72:75], a[160:163]\n" \
"ds_read_b128 v[120:123], v29 offset:34816\n" \
"v_mfma_f32_16x16x32_bf16 a[132:135], v[88:91], v[68:71], a[132:135]\n" \
"ds_read_b128 v[124:127], v29 offset:36864\n" \
"v_mfma_f32_16x16x32_bf16 a[164:167], v[88:91], v[72:75], a[164:167]\n" \
"v_mfma_f32_16x16x32_bf16 a[136:139], v[92:95], v[68:71], a[136:139]\n" \
"ds_read_b128 v[128:131], v29 offset:38912\n" \
"v_mfma_f32_16x16x32_bf16 a[168:171], v[92:95], v[72:75], a[168:171]\n" \
"ds_read_b128 v[132:135], v29 offset:40960\n" \
"v_mfma_f32_16x16x32_bf16 a[140:143], v[96:99], v[68:71], a[140:143]\n" \
"v_mfma_f32_16x16x32_bf16 a[172:175], v[96:99], v[72:75], a[172:175]\n" \
"s_add_u32 s89, s42, 0x2000\n" \
"buffer_store_dwordx4 a[220:223], v20, s[80:83], s89 offen offset:1024 sc1\n" \
"v_mfma_f32_16x16x32_bf16 a[144:147], v[100:103], v[60:63], a[144:147]\n" \
"ds_read_b128 v[136:139], v29 offset:43008\n" \
"v_mfma_f32_16x16x32_bf16 a[148:151], v[104:107], v[60:63], a[148:151]\n" \
"ds_read_b128 v[140:143], v29 offset:45056\n" \
"v_mfma_f32_16x16x32_bf16 a[180:183], v[112:115], v[60:63], a[180:183]\n" \
"ds_read_b128 v[144:147], v29 offset:47104\n" \
"v_mfma_f32_16x16x32_bf16 a[176:179], v[60:63], v[108:111], a[176:179]\n" \
"v_mfma_f32_16x16x32_bf16 a[192:195], v[84:87], v[76:79], a[192:195]\n" \
"v_mfma_f32_16x16x32_bf16 a[224:227], v[84:87], v[80:83], a[224:227]\n" \
"v_mfma_f32_16x16x32_bf16 a[196:199], v[88:91], v[76:79], a[196:199]\n" \
"v_mfma_f32_16x16x32_bf16 a[228:231], v[88:91], v[80:83], a[228:231]\n" \
"s_add_u32 s88, s42, 0x2000\n" \
"buffer_store_dwordx4 v[184:187], v20, s[80:83], s88 offen offset:2048 sc1\n" \
"v_mfma_f32_16x16x32_bf16 a[200:203], v[92:95], v[76:79], a[200:203]\n" \
"v_mfma_f32_16x16x32_bf16 a[232:235], v[92:95], v[80:83], a[232:235]\n" \
"v_mfma_f32_16x16x32_bf16 a[204:207], v[96:99], v[76:79], a[204:207]\n" \
"v_mfma_f32_16x16x32_bf16 a[236:239], v[96:99], v[80:83], a[236:239]\n" \
"v_mfma_f32_16x16x32_bf16 a[208:211], v[100:103], v[64:67], a[208:211]\n" \
"v_mfma_f32_16x16x32_bf16 a[212:215], v[104:107], v[64:67], a[212:215]\n" \
"v_mfma_f32_16x16x32_bf16 a[244:247], v[112:115], v[64:67], a[244:247]\n" \
"v_mfma_f32_16x16x32_bf16 a[240:243], v[64:67], v[108:111], a[240:243]\n" \
"s_waitcnt vmcnt(3) lgkmcnt(0)\n" \
"s_barrier\n" \
"v_mfma_f32_16x16x32_bf16 a[24:27], v[168:171], v[248:251], a[24:27]\n" \
"s_add_u32 s89, s42, 0x3000\n" \
"buffer_store_dwordx4 a[248:251], v20, s[80:83], s89 offen offset:3072 sc1\n" \
"v_mfma_f32_16x16x32_bf16 a[28:31], v[248:251], v[172:175], a[28:31]\n" \
"ds_read_b64_tr_b16 v[104:105], v37 offset:0\n" \
"v_mfma_f32_16x16x32_bf16 a[56:59], v[176:179], v[248:251], a[56:59]\n" \
"ds_read_b64_tr_b16 v[106:107], v37 offset:512\n" \
"v_mfma_f32_16x16x32_bf16 a[0:3], v[148:151], v[116:119], a[0:3]\n" \
"ds_read_b64_tr_b16 v[108:109], v38 offset:0\n" \
"v_mfma_f32_16x16x32_bf16 a[32:35], v[148:151], v[120:123], a[32:35]\n" \
"ds_read_b64_tr_b16 v[110:111], v38 offset:512\n" \
"v_mfma_f32_16x16x32_bf16 a[4:7], v[152:155], v[116:119], a[4:7]\n" \
"v_mfma_f32_16x16x32_bf16 a[36:39], v[152:155], v[120:123], a[36:39]\n" \
"ds_read_b64_tr_b16 v[112:113], v39 offset:0\n" \
"v_mfma_f32_16x16x32_bf16 a[8:11], v[156:159], v[116:119], a[8:11]\n" \
"ds_read_b64_tr_b16 v[114:115], v39 offset:512\n" \
"v_mfma_f32_16x16x32_bf16 a[40:43], v[156:159], v[120:123], a[40:43]\n" \
"v_mfma_f32_16x16x32_bf16 a[12:15], v[160:163], v[116:119], a[12:15]\n" \
"ds_read_b64_tr_b16 v[84:85], v32 offset:0\n" \
"v_mfma_f32_16x16x32_bf16 a[44:47], v[160:163], v[120:123], a[44:47]\n" \
"ds_read_b64_tr_b16 v[86:87], v32 offset:1024\n" \
"v_mfma_f32_16x16x32_bf16 a[16:19], v[164:167], v[116:119], a[16:19]\n" \
"v_mfma_f32_16x16x32_bf16 a[20:23], v[168:171], v[116:119], a[20:23]\n" \
"s_add_u32 s88, s42, 0x3000\n" \
"buffer_store_dwordx4 a[252:255], v20, s[80:83], s88 offen offset:0 sc1\n" \
"v_mfma_f32_16x16x32_bf16 a[52:55], v[176:179], v[116:119], a[52:55]\n" \
"ds_read_b128 v[248:251], v22 offset:0\n" \
"v_mfma_f32_16x16x32_bf16 a[48:51], v[116:119], v[172:175], a[48:51]\n" \
"ds_read_b64_tr_b16 v[88:89], v33 offset:0\n" \
"v_mfma_f32_16x16x32_bf16 a[64:67], v[148:151], v[124:127], a[64:67]\n" \
"ds_read_b64_tr_b16 v[90:91], v33 offset:1024\n" \
"v_mfma_f32_16x16x32_bf16 a[96:99], v[148:151], v[128:131], a[96:99]\n" \
"v_mfma_f32_16x16x32_bf16 a[68:71], v[152:155], v[124:127], a[68:71]\n" \
"ds_read_b64_tr_b16 v[92:93], v34 offset:0\n" \
"v_mfma_f32_16x16x32_bf16 a[100:103], v[152:155], v[128:131], a[100:103]\n" \
"ds_read_b64_tr_b16 v[94:95], v34 offset:1024\n" \
"v_mfma_f32_16x16x32_bf16 a[72:75], v[156:159], v[124:127], a[72:75]\n" \
"v_mfma_f32_16x16x32_bf16 a[104:107], v[156:159], v[128:131], a[104:107]\n" \
"ds_read_b64_tr_b16 v[96:97], v35 offset:0\n" \
"v_mfma_f32_16x16x32_bf16 a[76:79], v[160:163], v[124:127], a[76:79]\n" \
"ds_read_b64_tr_b16 v[98:99], v35 offset:1024\n" \
"v_mfma_f32_16x16x32_bf16 a[108:111], v[160:163], v[128:131], a[108:111]\n" \
"v_mfma_f32_16x16x32_bf16 a[80:83], v[164:167], v[120:123], a[80:83]\n" \
"ds_read_b64_tr_b16 v[100:101], v36 offset:0\n" \
"v_mfma_f32_16x16x32_bf16 a[84:87], v[168:171], v[120:123], a[84:87]\n" \
"ds_read_b64_tr_b16 v[102:103], v36 offset:512\n" \
"v_mfma_f32_16x16x32_bf16 a[116:119], v[176:179], v[120:123], a[116:119]\n" \
"s_add_u32 s89, s42, 0x3000\n" \
"buffer_store_dwordx4 v[188:191], v20, s[80:83], s89 offen offset:1024 sc1\n" \
"v_mfma_f32_16x16x32_bf16 a[112:115], v[120:123], v[172:175], a[112:115]\n" \
"ds_read_b128 v[52:55], v28 offset:0\n" \
"v_mfma_f32_16x16x32_bf16 a[128:131], v[148:151], v[132:135], a[128:131]\n" \
"v_mfma_f32_16x16x32_bf16 a[160:163], v[148:151], v[136:139], a[160:163]\n" \
"ds_read_b128 v[56:59], v28 offset:2048\n" \
"v_mfma_f32_16x16x32_bf16 a[132:135], v[152:155], v[132:135], a[132:135]\n" \
"ds_read_b128 v[60:63], v28 offset:4096\n" \
"v_mfma_f32_16x16x32_bf16 a[164:167], v[152:155], v[136:139], a[164:167]\n" \
"v_mfma_f32_16x16x32_bf16 a[136:139], v[156:159], v[132:135], a[136:139]\n" \
"ds_read_b128 v[64:67], v28 offset:6144\n" \
"v_mfma_f32_16x16x32_bf16 a[168:171], v[156:159], v[136:139], a[168:171]\n" \
"ds_read_b128 v[68:71], v28 offset:8192\n" \
"v_mfma_f32_16x16x32_bf16 a[140:143], v[160:163], v[132:135], a[140:143]\n" \
"v_mfma_f32_16x16x32_bf16 a[172:175], v[160:163], v[136:139], a[172:175]\n" \
"ds_read_b128 v[72:75], v28 offset:10240\n" \
"v_mfma_f32_16x16x32_bf16 a[144:147], v[164:167], v[124:127], a[144:147]\n" \
"ds_read_b128 v[76:79], v28 offset:12288\n" \
"v_mfma_f32_16x16x32_bf16 a[148:151], v[168:171], v[124:127], a[148:151]\n" \
"v_mfma_f32_16x16x32_bf16 a[180:183], v[176:179], v[124:127], a[180:183]\n" \
"ds_read_b128 v[80:83], v28 offset:14336\n" \
"v_mfma_f32_16x16x32_bf16 a[176:179], v[124:127], v[172:175], a[176:179]\n" \
"s_add_u32 s88, s42, 0x3000\n" \
"buffer_store_dwordx4 v[252:255], v20, s[80:83], s88 offen offset:2048 sc1\n" \
"v_mfma_f32_16x16x32_bf16 a[192:195], v[148:151], v[140:143], a[192:195]\n" \
"v_mfma_f32_16x16x32_bf16 a[224:227], v[148:151], v[144:147], a[224:227]\n" \
"v_mfma_f32_16x16x32_bf16 a[196:199], v[152:155], v[140:143], a[196:199]\n" \
"v_mfma_f32_16x16x32_bf16 a[228:231], v[152:155], v[144:147], a[228:231]\n" \
"v_mfma_f32_16x16x32_bf16 a[200:203], v[156:159], v[140:143], a[200:203]\n" \
"v_mfma_f32_16x16x32_bf16 a[232:235], v[156:159], v[144:147], a[232:235]\n" \
"v_mfma_f32_16x16x32_bf16 a[204:207], v[160:163], v[140:143], a[204:207]\n" \
"v_mfma_f32_16x16x32_bf16 a[236:239], v[160:163], v[144:147], a[236:239]\n" \
"v_mfma_f32_16x16x32_bf16 a[208:211], v[164:167], v[128:131], a[208:211]\n" \
"v_mfma_f32_16x16x32_bf16 a[212:215], v[168:171], v[128:131], a[212:215]\n" \
"v_mfma_f32_16x16x32_bf16 a[244:247], v[176:179], v[128:131], a[244:247]\n" \
"v_mfma_f32_16x16x32_bf16 a[240:243], v[128:131], v[172:175], a[240:243]\n" \
"s_waitcnt lgkmcnt(15)\n" \
"v_mfma_f32_16x16x32_bf16 a[24:27], v[104:107], v[248:251], a[24:27]\n" \
"v_bfe_u32 v44, v20, 2, 5\n" \
"ds_read_b64_tr_b16 v[168:169], v37 offset:4096\n" \
"v_lshrrev_b32_e32 v24, 9, v20\n" \
"v_mfma_f32_16x16x32_bf16 a[28:31], v[248:251], v[108:111], a[28:31]\n" \
"buffer_load_dword v44, v44, s[80:83], s38 offen offset:0 sc1\n" \
"v_and_b32_e32 v26, 0x1ff, v20\n" \
"v_mfma_f32_16x16x32_bf16 a[56:59], v[112:115], v[248:251], a[56:59]\n" \
"ds_read_b64_tr_b16 v[170:171], v37 offset:4608\n" \
"v_lshl_add_u32 v24, v24, 3, v26\n" \
"v_lshrrev_b32_e32 v25, 8, v20\n" \
"s_waitcnt lgkmcnt(9)\n" \
"v_mfma_f32_16x16x32_bf16 a[0:3], v[84:87], v[52:55], a[0:3]\n" \
"ds_read_b64_tr_b16 v[172:173], v38 offset:4096\n" \
"v_mul_u32_u24_e32 v25, 0x24700, v25\n" \
"v_add_u32_e32 v25, v25, v20\n" \
"s_waitcnt lgkmcnt(9)\n" \
"v_mfma_f32_16x16x32_bf16 a[32:35], v[84:87], v[56:59], a[32:35]\n" \
"ds_read_b64_tr_b16 v[174:175], v38 offset:4608\n" \
"v_lshrrev_b32_e32 v26, 2, v20\n" \
"v_add_u32_e32 v26, s45, v26\n" \
"v_mfma_f32_16x16x32_bf16 a[4:7], v[88:91], v[52:55], a[4:7]\n" \
"v_add_u32_e32 v27, s46, v20\n" \
"v_add_u32_e32 v21, s43, v27\n" \
"v_accvgpr_read_b32 v180, a24\n" \
"v_mfma_f32_16x16x32_bf16 a[36:39], v[88:91], v[56:59], a[36:39]\n" \
"ds_read_b64_tr_b16 v[176:177], v39 offset:4096\n" \
"v_accvgpr_read_b32 v181, a25\n" \
"v_accvgpr_read_b32 v182, a26\n" \
"v_mfma_f32_16x16x32_bf16 a[8:11], v[92:95], v[52:55], a[8:11]\n" \
"ds_read_b64_tr_b16 v[178:179], v39 offset:4608\n" \
"v_accvgpr_read_b32 v183, a27\n" \
"v_accvgpr_read_b32 v252, a28\n" \
"v_mfma_f32_16x16x32_bf16 a[40:43], v[92:95], v[56:59], a[40:43]\n" \
"v_accvgpr_read_b32 v253, a29\n" \
"v_accvgpr_read_b32 v254, a30\n" \
"v_accvgpr_read_b32 v255, a31\n" \
"v_mfma_f32_16x16x32_bf16 a[12:15], v[96:99], v[52:55], a[12:15]\n" \
"ds_read_b64_tr_b16 v[148:149], v32 offset:8192\n" \
"v_accvgpr_read_b32 v48, a56\n" \
"v_accvgpr_read_b32 v49, a57\n" \
"v_mfma_f32_16x16x32_bf16 a[44:47], v[96:99], v[56:59], a[44:47]\n" \
"ds_read_b64_tr_b16 v[150:151], v32 offset:9216\n" \
"v_accvgpr_read_b32 v50, a58\n" \
"v_accvgpr_read_b32 v51, a59\n" \
"v_mfma_f32_16x16x32_bf16 a[16:19], v[100:103], v[52:55], a[16:19]\n" \
"v_mfma_f32_16x16x32_bf16 a[20:23], v[104:107], v[52:55], a[20:23]\n" \
"ds_read_b128 v[248:251], v23 offset:0\n" \
"v_mfma_f32_16x16x32_bf16 a[52:55], v[112:115], v[52:55], a[52:55]\n" \
"v_mfma_f32_16x16x32_bf16 a[48:51], v[52:55], v[108:111], a[48:51]\n" \
"ds_read_b64_tr_b16 v[152:153], v33 offset:8192\n" \
"s_nop 2\n" \
"v_accvgpr_read_b32 v184, a16\n" \
"v_accvgpr_read_b32 v185, a17\n" \
"s_waitcnt lgkmcnt(15)\n" \
"v_mfma_f32_16x16x32_bf16 a[64:67], v[84:87], v[60:63], a[64:67]\n" \
"ds_read_b64_tr_b16 v[154:155], v33 offset:9216\n" \
"v_accvgpr_read_b32 v186, a18\n" \
"v_accvgpr_read_b32 v187, a19\n" \
"s_waitcnt lgkmcnt(15)\n" \
"v_mfma_f32_16x16x32_bf16 a[96:99], v[84:87], v[64:67], a[96:99]\n" \
"v_accvgpr_read_b32 v188, a20\n" \
"v_accvgpr_read_b32 v189, a21\n" \
"v_accvgpr_read_b32 v190, a22\n" \
"v_mfma_f32_16x16x32_bf16 a[68:71], v[88:91], v[60:63], a[68:71]\n" \
"ds_read_b64_tr_b16 v[156:157], v34 offset:8192\n" \
"v_accvgpr_read_b32 v191, a23\n" \
"v_accvgpr_read_b32 v196, a52\n" \
"v_mfma_f32_16x16x32_bf16 a[100:103], v[88:91], v[64:67], a[100:103]\n" \
"v_lshlrev_b32_e32 v45, 3, v20\n" \
"v_add_u32_e64 v45, s30, v45\n" \
"v_accvgpr_read_b32 v197, a53\n" \
"v_mfma_f32_16x16x32_bf16 a[72:75], v[92:95], v[60:63], a[72:75]\n" \
"s_getpc_b64 s[86:87]\n" \
"L_ct0_%=:\n" \
"s_add_u32 s86, s86, L_end_%=-L_ct0_%=\n" \
"s_addc_u32 s87, s87, 0\n" \
"s_mov_b32 m0, s32\n" \
"s_nop 0\n" \
"v_mfma_f32_16x16x32_bf16 a[104:107], v[92:95], v[64:67], a[104:107]\n" \
"global_load_lds_dword v45, s[86:87]\n" \
"v_accvgpr_read_b32 v198, a54\n" \
"v_mfma_f32_16x16x32_bf16 a[76:79], v[96:99], v[60:63], a[76:79]\n" \
"ds_read_b64_tr_b16 v[158:159], v34 offset:9216\n" \
"v_accvgpr_read_b32 v199, a55\n" \
"v_accvgpr_read_b32 v192, a48\n" \
"v_mfma_f32_16x16x32_bf16 a[108:111], v[96:99], v[64:67], a[108:111]\n" \
"ds_read_b64_tr_b16 v[160:161], v35 offset:8192\n" \
"v_accvgpr_read_b32 v193, a49\n" \
"v_accvgpr_read_b32 v194, a50\n" \
"v_mfma_f32_16x16x32_bf16 a[80:83], v[100:103], v[56:59], a[80:83]\n" \
"ds_read_b64_tr_b16 v[162:163], v35 offset:9216\n" \
"v_accvgpr_read_b32 v195, a51\n" \
"v_mfma_f32_16x16x32_bf16 a[84:87], v[104:107], v[56:59], a[84:87]\n" \
"ds_read_b64_tr_b16 v[164:165], v36 offset:4096\n" \
"v_mfma_f32_16x16x32_bf16 a[116:119], v[112:115], v[56:59], a[116:119]\n" \
"ds_read_b64_tr_b16 v[166:167], v36 offset:4608\n" \
"v_mfma_f32_16x16x32_bf16 a[112:115], v[56:59], v[108:111], a[112:115]\n" \
"ds_read_b128 v[116:119], v29 offset:0\n" \
"v_accvgpr_read_b32 v200, a80\n" \
"v_accvgpr_read_b32 v201, a81\n" \
"s_waitcnt lgkmcnt(15)\n" \
"s_nop 0\n" \
"v_mfma_f32_16x16x32_bf16 a[128:131], v[84:87], v[68:71], a[128:131]\n" \
"v_accvgpr_read_b32 v202, a82\n" \
"v_accvgpr_read_b32 v203, a83\n" \
"v_accvgpr_read_b32 v204, a84\n" \
"v_mfma_f32_16x16x32_bf16 a[160:163], v[84:87], v[72:75], a[160:163]\n" \
"ds_read_b128 v[120:123], v29 offset:2048\n" \
"v_accvgpr_read_b32 v205, a85\n" \
"v_accvgpr_read_b32 v206, a86\n" \
"v_mfma_f32_16x16x32_bf16 a[132:135], v[88:91], v[68:71], a[132:135]\n" \
"ds_read_b128 v[124:127], v29 offset:4096\n" \
"v_accvgpr_read_b32 v207, a87\n" \
"v_accvgpr_read_b32 v212, a116\n" \
"v_mfma_f32_16x16x32_bf16 a[164:167], v[88:91], v[72:75], a[164:167]\n" \
"v_accvgpr_read_b32 v213, a117\n" \
"v_accvgpr_read_b32 v214, a118\n" \
"v_accvgpr_read_b32 v215, a119\n" \
"v_mfma_f32_16x16x32_bf16 a[136:139], v[92:95], v[68:71], a[136:139]\n" \
"ds_read_b128 v[128:131], v29 offset:6144\n" \
"v_accvgpr_read_b32 v208, a112\n" \
"v_accvgpr_read_b32 v209, a113\n" \
"v_mfma_f32_16x16x32_bf16 a[168:171], v[92:95], v[72:75], a[168:171]\n" \
"ds_read_b128 v[132:135], v29 offset:8192\n" \
"v_accvgpr_read_b32 v210, a114\n" \
"v_accvgpr_read_b32 v211, a115\n" \
"v_mfma_f32_16x16x32_bf16 a[140:143], v[96:99], v[68:71], a[140:143]\n" \
"v_mfma_f32_16x16x32_bf16 a[172:175], v[96:99], v[72:75], a[172:175]\n" \
"ds_read_b128 v[136:139], v29 offset:10240\n" \
"v_mfma_f32_16x16x32_bf16 a[144:147], v[100:103], v[60:63], a[144:147]\n" \
"ds_read_b128 v[140:143], v29 offset:12288\n" \
"v_mfma_f32_16x16x32_bf16 a[148:151], v[104:107], v[60:63], a[148:151]\n" \
"v_mfma_f32_16x16x32_bf16 a[180:183], v[112:115], v[60:63], a[180:183]\n" \
"ds_read_b128 v[144:147], v29 offset:14336\n" \
"v_mfma_f32_16x16x32_bf16 a[176:179], v[60:63], v[108:111], a[176:179]\n" \
"s_nop 2\n" \
"s_nop 0\n" \
"v_accvgpr_read_b32 v216, a144\n" \
"v_accvgpr_read_b32 v217, a145\n" \
"v_accvgpr_read_b32 v218, a146\n" \
"v_mfma_f32_16x16x32_bf16 a[192:195], v[84:87], v[76:79], a[192:195]\n" \
"v_accvgpr_read_b32 v219, a147\n" \
"v_accvgpr_read_b32 v220, a148\n" \
"v_accvgpr_read_b32 v221, a149\n" \
"v_mfma_f32_16x16x32_bf16 a[224:227], v[84:87], v[80:83], a[224:227]\n" \
"v_accvgpr_read_b32 v222, a150\n" \
"v_accvgpr_read_b32 v223, a151\n" \
"v_accvgpr_read_b32 v228, a180\n" \
"v_mfma_f32_16x16x32_bf16 a[196:199], v[88:91], v[76:79], a[196:199]\n" \
"v_accvgpr_read_b32 v229, a181\n" \
"v_accvgpr_read_b32 v230, a182\n" \
"v_accvgpr_read_b32 v231, a183\n" \
"v_mfma_f32_16x16x32_bf16 a[228:231], v[88:91], v[80:83], a[228:231]\n" \
"v_accvgpr_read_b32 v224, a176\n" \
"v_accvgpr_read_b32 v225, a177\n" \
"v_accvgpr_read_b32 v226, a178\n" \
"v_mfma_f32_16x16x32_bf16 a[200:203], v[92:95], v[76:79], a[200:203]\n" \
"v_accvgpr_read_b32 v227, a179\n" \
"v_mfma_f32_16x16x32_bf16 a[232:235], v[92:95], v[80:83], a[232:235]\n" \
"v_mfma_f32_16x16x32_bf16 a[204:207], v[96:99], v[76:79], a[204:207]\n" \
"v_mfma_f32_16x16x32_bf16 a[236:239], v[96:99], v[80:83], a[236:239]\n" \
"v_mfma_f32_16x16x32_bf16 a[208:211], v[100:103], v[64:67], a[208:211]\n" \
"v_mfma_f32_16x16x32_bf16 a[212:215], v[104:107], v[64:67], a[212:215]\n" \
"v_mfma_f32_16x16x32_bf16 a[244:247], v[112:115], v[64:67], a[244:247]\n" \
"v_mfma_f32_16x16x32_bf16 a[240:243], v[64:67], v[108:111], a[240:243]\n" \
"s_nop 4\n" \
"s_nop 0\n" \
"v_accvgpr_read_b32 v232, a208\n" \
"v_accvgpr_read_b32 v233, a209\n" \
"v_accvgpr_read_b32 v234, a210\n" \
"v_accvgpr_read_b32 v235, a211\n" \
"v_accvgpr_read_b32 v236, a212\n" \
"v_accvgpr_read_b32 v237, a213\n" \
"v_accvgpr_read_b32 v238, a214\n" \
"v_accvgpr_read_b32 v239, a215\n" \
"v_accvgpr_read_b32 v244, a244\n" \
"v_accvgpr_read_b32 v245, a245\n" \
"v_accvgpr_read_b32 v246, a246\n" \
"v_accvgpr_read_b32 v247, a247\n" \
"v_accvgpr_read_b32 v240, a240\n" \
"v_accvgpr_read_b32 v241, a241\n" \
"v_accvgpr_read_b32 v242, a242\n" \
"v_accvgpr_read_b32 v243, a243\n" \
"s_waitcnt vmcnt(1) lgkmcnt(0)\n" \
"s_barrier\n" \
"v_mov_b32_e32 v45, s62\n" \
"s_mov_b64 exec, 1\n" \
"buffer_store_dword v45, off, s[80:83], s37 offset:32 sc1\n" \
"s_mov_b64 exec, -1\n" \
"v_cmp_ne_u32_e32 vcc, s62, v44\n" \
"s_nop 4\n" \
"s_cbranch_vccnz L_poll160_%=\n" \
"L_pollr160_%=:\n" \
"v_mfma_f32_16x16x32_bf16 v[48:51], v[176:179], v[248:251], v[48:51]\n" \
"s_mov_b32 s73, s50\n" \
"s_nop 0\n" \
"s_sub_u32 m0, s31, 0x10000\n" \
"v_mfma_f32_16x16x32_bf16 v[180:183], v[168:171], v[248:251], v[180:183]\n" \
"buffer_load_dwordx4 v20, s[80:83], s73 offen offset:0 sc1 lds\n" \
"v_mfma_f32_16x16x32_bf16 v[252:255], v[248:251], v[172:175], v[252:255]\n" \
"s_add_u32 s74, s50, 0x4000\n" \
"s_mov_b32 s70, 0x3e0293ee\n" \
"v_mfma_f32_16x16x32_bf16 v[196:199], v[176:179], v[116:119], v[196:199]\n" \
"s_add_u32 s75, s50, 0x8000\n" \
"s_mov_b32 s64, 0xaaaaaaaa\n" \
"v_mfma_f32_16x16x32_bf16 v[188:191], v[168:171], v[116:119], v[188:191]\n" \
"v_cvt_pk_bf16_f32 v180, v180, v181\n" \
"v_cvt_pk_bf16_f32 v181, v182, v183\n" \
"v_mfma_f32_16x16x32_bf16 v[184:187], v[164:167], v[116:119], v[184:187]\n" \
"buffer_load_dwordx4 v20, s[80:83], s73 offen offset:1024 sc1 lds\n" \
"v_mfma_f32_16x16x32_bf16 v[192:195], v[116:119], v[172:175], v[192:195]\n" \
"v_cvt_pk_bf16_f32 v182, v50, v51\n" \
"v_mov_b32_e32 v183, 0x3f80\n" \
"v_mfma_f32_16x16x32_bf16 v[212:215], v[176:179], v[120:123], v[212:215]\n" \
"v_cvt_pk_bf16_f32 v188, v188, v189\n" \
"v_cvt_pk_bf16_f32 v189, v190, v191\n" \
"v_mfma_f32_16x16x32_bf16 v[204:207], v[168:171], v[120:123], v[204:207]\n" \
"v_cvt_pk_bf16_f32 v184, v184, v185\n" \
"v_cvt_pk_bf16_f32 v185, v186, v187\n" \
"v_mfma_f32_16x16x32_bf16 v[200:203], v[164:167], v[120:123], v[200:203]\n" \
"buffer_load_dwordx4 v20, s[80:83], s73 offen offset:2048 sc1 lds\n" \
"v_mfma_f32_16x16x32_bf16 v[208:211], v[120:123], v[172:175], v[208:211]\n" \
"v_cvt_pk_bf16_f32 v186, v196, v197\n" \
"v_mov_b32_e64 v187, 0\n" \
"v_mfma_f32_16x16x32_bf16 v[228:231], v[176:179], v[124:127], v[228:231]\n" \
"v_cvt_pk_bf16_f32 v204, v204, v205\n" \
"v_cvt_pk_bf16_f32 v205, v206, v207\n" \
"v_mfma_f32_16x16x32_bf16 v[220:223], v[168:171], v[124:127], v[220:223]\n" \
"v_cvt_pk_bf16_f32 v200, v200, v201\n" \
"v_cvt_pk_bf16_f32 v201, v202, v203\n" \
"v_mfma_f32_16x16x32_bf16 v[216:219], v[164:167], v[124:127], v[216:219]\n" \
"buffer_load_dwordx4 v20, s[80:83], s73 offen offset:3072 sc1 lds\n" \
"v_mfma_f32_16x16x32_bf16 v[224:227], v[124:127], v[172:175], v[224:227]\n" \
"s_sub_u32 m0, s31, s51\n" \
"s_nop 0\n" \
"v_cvt_pk_bf16_f32 v190, v198, v199\n" \
"v_mfma_f32_16x16x32_bf16 v[244:247], v[176:179], v[128:131], v[244:247]\n" \
"v_cvt_pk_bf16_f32 v220, v220, v221\n" \
"v_cvt_pk_bf16_f32 v221, v222, v223\n" \
"v_mfma_f32_16x16x32_bf16 v[236:239], v[168:171], v[128:131], v[236:239]\n" \
"v_cvt_pk_bf16_f32 v216, v216, v217\n" \
"v_cvt_pk_bf16_f32 v217, v218, v219\n" \
"v_mfma_f32_16x16x32_bf16 v[232:235], v[164:167], v[128:131], v[232:235]\n" \
"buffer_load_dwordx4 v20, s[80:83], s74 offen offset:0 sc1 lds\n" \
"v_mfma_f32_16x16x32_bf16 v[240:243], v[128:131], v[172:175], v[240:243]\n" \
"v_mov_b32_e32 v191, 0x3f80\n" \
"v_cvt_pk_bf16_f32 v54, v192, v193\n" \
"v_mfma_f32_16x16x32_bf16 a[0:3], v[148:151], v[116:119], a[0:3]\n" \
"v_cvt_pk_bf16_f32 v236, v236, v237\n" \
"v_cvt_pk_bf16_f32 v237, v238, v239\n" \
"v_mfma_f32_16x16x32_bf16 a[32:35], v[148:151], v[120:123], a[32:35]\n" \
"v_cvt_pk_bf16_f32 v232, v232, v233\n" \
"v_cvt_pk_bf16_f32 v233, v234, v235\n" \
"v_mfma_f32_16x16x32_bf16 a[4:7], v[152:155], v[116:119], a[4:7]\n" \
"buffer_load_dwordx4 v20, s[80:83], s74 offen offset:1024 sc1 lds\n" \
"v_mfma_f32_16x16x32_bf16 a[36:39], v[152:155], v[120:123], a[36:39]\n" \
"v_accvgpr_read_b32 v72, a0\n" \
"v_accvgpr_read_b32 v73, a1\n" \
"v_mfma_f32_16x16x32_bf16 a[8:11], v[156:159], v[116:119], a[8:11]\n" \
"v_accvgpr_read_b32 v74, a2\n" \
"v_accvgpr_read_b32 v75, a3\n" \
"v_mfma_f32_16x16x32_bf16 a[40:43], v[156:159], v[120:123], a[40:43]\n" \
"v_mul_f32_e32 v72, s70, v72\n" \
"v_mul_f32_e32 v73, s70, v73\n" \
"v_mfma_f32_16x16x32_bf16 a[12:15], v[160:163], v[116:119], a[12:15]\n" \
"buffer_load_dwordx4 v20, s[80:83], s74 offen offset:2048 sc1 lds\n" \
"v_mfma_f32_16x16x32_bf16 a[44:47], v[160:163], v[120:123], a[44:47]\n" \
"v_accvgpr_read_b32 v76, a4\n" \
"v_accvgpr_read_b32 v77, a5\n" \
"v_mfma_f32_16x16x32_bf16 a[64:67], v[148:151], v[124:127], a[64:67]\n" \
"v_accvgpr_read_b32 v78, a6\n" \
"v_accvgpr_read_b32 v79, a7\n" \
"v_mfma_f32_16x16x32_bf16 a[96:99], v[148:151], v[128:131], a[96:99]\n" \
"v_mul_f32_e32 v74, s70, v74\n" \
"v_mul_f32_e32 v75, s70, v75\n" \
"v_mfma_f32_16x16x32_bf16 a[68:71], v[152:155], v[124:127], a[68:71]\n" \
"buffer_load_dwordx4 v20, s[80:83], s74 offen offset:3072 sc1 lds\n" \
"v_mfma_f32_16x16x32_bf16 a[100:103], v[152:155], v[128:131], a[100:103]\n" \
"v_cvt_pk_bf16_f32 v72, v72, v73\n" \
"s_sub_u32 m0, s31, 0x4000\n" \
"v_mfma_f32_16x16x32_bf16 a[72:75], v[156:159], v[124:127], a[72:75]\n" \
"v_mul_f32_e32 v76, s70, v76\n" \
"v_mul_f32_e32 v77, s70, v77\n" \
"v_mfma_f32_16x16x32_bf16 a[104:107], v[156:159], v[128:131], a[104:107]\n" \
"v_mul_f32_e32 v78, s70, v78\n" \
"v_mul_f32_e32 v79, s70, v79\n" \
"v_mfma_f32_16x16x32_bf16 a[76:79], v[160:163], v[124:127], a[76:79]\n" \
"v_cvt_pk_bf16_f32 v73, v74, v75\n" \
"v_cvt_pk_bf16_f32 v74, v76, v77\n" \
"v_mfma_f32_16x16x32_bf16 a[108:111], v[160:163], v[128:131], a[108:111]\n" \
"buffer_load_dwordx4 v20, s[80:83], s75 offen offset:0 sc1 lds\n" \
"v_cvt_pk_bf16_f32 v75, v78, v79\n" \
"ds_write_b128 v27, v[72:75] offset:0\n" \
"v_mfma_f32_16x16x32_bf16 a[128:131], v[148:151], v[132:135], a[128:131]\n" \
"v_accvgpr_read_b32 v72, a8\n" \
"v_accvgpr_read_b32 v73, a9\n" \
"v_accvgpr_read_b32 v74, a10\n" \
"v_accvgpr_read_b32 v75, a11\n" \
"v_mul_f32_e32 v72, s70, v72\n" \
"v_mul_f32_e32 v73, s70, v73\n" \
"v_accvgpr_read_b32 v76, a12\n" \
"v_accvgpr_read_b32 v77, a13\n" \
"v_accvgpr_read_b32 v78, a14\n" \
"v_accvgpr_read_b32 v79, a15\n" \
"v_mul_f32_e32 v74, s70, v74\n" \
"v_mul_f32_e32 v75, s70, v75\n" \
"v_cvt_pk_bf16_f32 v72, v72, v73\n" \
"v_mfma_f32_16x16x32_bf16 a[160:163], v[148:151], v[136:139], a[160:163]\n" \
"v_mul_f32_e32 v76, s70, v76\n" \
"v_mul_f32_e32 v77, s70, v77\n" \
"v_mul_f32_e32 v78, s70, v78\n" \
"v_mul_f32_e32 v79, s70, v79\n" \
"v_cvt_pk_bf16_f32 v73, v74, v75\n" \
"v_cvt_pk_bf16_f32 v74, v76, v77\n" \
"v_cvt_pk_bf16_f32 v75, v78, v79\n" \
"ds_write_b128 v27, v[72:75] offset:1024\n" \
"v_mfma_f32_16x16x32_bf16 a[132:135], v[152:155], v[132:135], a[132:135]\n" \
"v_accvgpr_read_b32 v72, a32\n" \
"v_accvgpr_read_b32 v73, a33\n" \
"v_accvgpr_read_b32 v74, a34\n" \
"v_accvgpr_read_b32 v75, a35\n" \
"v_mul_f32_e32 v72, s70, v72\n" \
"v_mul_f32_e32 v73, s70, v73\n" \
"v_accvgpr_read_b32 v76, a36\n" \
"v_accvgpr_read_b32 v77, a37\n" \
"v_accvgpr_read_b32 v78, a38\n" \
"v_accvgpr_read_b32 v79, a39\n" \
"v_mul_f32_e32 v74, s70, v74\n" \
"v_mul_f32_e32 v75, s70, v75\n" \
"v_cvt_pk_bf16_f32 v72, v72, v73\n" \
"v_mul_f32_e32 v76, s70, v76\n" \
"v_mul_f32_e32 v77, s70, v77\n" \
"v_mul_f32_e32 v78, s70, v78\n" \
"v_mul_f32_e32 v79, s70, v79\n" \
"v_cvt_pk_bf16_f32 v73, v74, v75\n" \
"v_mfma_f32_16x16x32_bf16 a[164:167], v[152:155], v[136:139], a[164:167]\n" \
"v_cvt_pk_bf16_f32 v74, v76, v77\n" \
"v_cvt_pk_bf16_f32 v75, v78, v79\n" \
"ds_write_b128 v27, v[72:75] offset:4096\n" \
"buffer_load_dwordx4 v20, s[80:83], s75 offen offset:1024 sc1 lds\n" \
"v_cvt_pk_bf16_f32 v55, v194, v195\n" \
"v_cvt_pk_bf16_f32 v206, v214, v215\n" \
"v_mov_b32_e32 v207, 0x3f80\n" \
"v_cvt_pk_bf16_f32 v202, v212, v213\n" \
"v_mov_b32_e64 v203, 0\n" \
"v_cvt_pk_bf16_f32 v56, v208, v209\n" \
"v_cvt_pk_bf16_f32 v57, v210, v211\n" \
"v_cvt_pk_bf16_f32 v222, v230, v231\n" \
"v_mov_b32_e32 v223, 0x3f80\n" \
"v_cvt_pk_bf16_f32 v218, v228, v229\n" \
"v_mov_b32_e64 v219, 0\n" \
"v_cvt_pk_bf16_f32 v58, v224, v225\n" \
"v_cvt_pk_bf16_f32 v59, v226, v227\n" \
"v_cvt_pk_bf16_f32 v238, v246, v247\n" \
"v_mov_b32_e32 v239, 0x3f80\n" \
"v_cvt_pk_bf16_f32 v234, v244, v245\n" \
"v_mov_b32_e64 v235, 0\n" \
"v_cvt_pk_bf16_f32 v60, v240, v241\n" \
"v_cvt_pk_bf16_f32 v61, v242, v243\n" \
"v_mfma_f32_16x16x32_bf16 v[164:167], v[180:183], v[184:187], 0\n" \
"v_accvgpr_read_b32 v72, a40\n" \
"v_accvgpr_read_b32 v73, a41\n" \
"v_mfma_f32_16x16x32_bf16 a[136:139], v[156:159], v[132:135], a[136:139]\n" \
"v_mfma_f32_16x16x32_bf16 v[192:195], v[188:191], v[184:187], 0\n" \
"v_mfma_f32_16x16x32_bf16 v[208:211], v[204:207], v[200:203], 0\n" \
"v_mfma_f32_16x16x32_bf16 v[168:171], v[188:191], v[200:203], 0\n" \
"v_mfma_f32_16x16x32_bf16 v[224:227], v[220:223], v[216:219], 0\n" \
"v_mfma_f32_16x16x32_bf16 v[172:175], v[204:207], v[216:219], 0\n" \
"v_mfma_f32_16x16x32_bf16 v[240:243], v[236:239], v[232:235], 0\n" \
"v_mfma_f32_16x16x32_bf16 v[176:179], v[220:223], v[232:235], 0\n" \
"v_accvgpr_read_b32 v74, a42\n" \
"v_accvgpr_read_b32 v75, a43\n" \
"v_mul_f32_e32 v72, s70, v72\n" \
"v_mul_f32_e32 v73, s70, v73\n" \
"v_accvgpr_read_b32 v76, a44\n" \
"v_accvgpr_read_b32 v77, a45\n" \
"v_accvgpr_read_b32 v78, a46\n" \
"v_accvgpr_read_b32 v79, a47\n" \
"v_mul_f32_e32 v74, s70, v74\n" \
"v_mul_f32_e32 v75, s70, v75\n" \
"v_cvt_pk_bf16_f32 v72, v72, v73\n" \
"v_mul_f32_e32 v76, s70, v76\n" \
"v_mul_f32_e32 v77, s70, v77\n" \
"v_mul_f32_e32 v78, s70, v78\n" \
"v_mul_f32_e32 v79, s70, v79\n" \
"v_cvt_pk_bf16_f32 v73, v74, v75\n" \
"v_cvt_pk_bf16_f32 v74, v76, v77\n" \
"v_cvt_pk_bf16_f32 v75, v78, v79\n" \
"v_mfma_f32_16x16x32_bf16 a[168:171], v[156:159], v[136:139], a[168:171]\n" \
"ds_write_b128 v27, v[72:75] offset:5120\n" \
"v_accvgpr_read_b32 v116, a64\n" \
"v_accvgpr_read_b32 v117, a65\n" \
"v_accvgpr_read_b32 v72, a72\n" \
"v_accvgpr_read_b32 v73, a73\n" \
"v_accvgpr_read_b32 v118, a66\n" \
"v_accvgpr_read_b32 v119, a67\n" \
"v_mul_f32_e32 v116, s70, v116\n" \
"v_mul_f32_e32 v117, s70, v117\n" \
"v_accvgpr_read_b32 v74, a74\n" \
"v_accvgpr_read_b32 v75, a75\n" \
"v_mul_f32_e32 v72, s70, v72\n" \
"v_mul_f32_e32 v73, s70, v73\n" \
"v_mfma_f32_16x16x32_bf16 a[140:143], v[160:163], v[132:135], a[140:143]\n" \
"s_mov_b32 s65, 0xaaaaaaaa\n" \
"v_accvgpr_read_b32 v120, a68\n" \
"v_accvgpr_read_b32 v121, a69\n" \
"v_accvgpr_read_b32 v122, a70\n" \
"v_accvgpr_read_b32 v123, a71\n" \
"v_mul_f32_e32 v118, s70, v118\n" \
"v_mul_f32_e32 v119, s70, v119\n" \
"v_cvt_pk_bf16_f32 v116, v116, v117\n" \
"v_accvgpr_read_b32 v76, a76\n" \
"v_accvgpr_read_b32 v77, a77\n" \
"v_accvgpr_read_b32 v78, a78\n" \
"v_accvgpr_read_b32 v79, a79\n" \
"v_mul_f32_e32 v74, s70, v74\n" \
"v_mul_f32_e32 v75, s70, v75\n" \
"v_cvt_pk_bf16_f32 v72, v72, v73\n" \
"v_cndmask_b32_e64 v164, v164, v165, s[64:65]\n" \
"v_cndmask_b32_e64 v166, v166, v167, s[64:65]\n" \
"v_mul_f32_e32 v120, s70, v120\n" \
"v_mul_f32_e32 v121, s70, v121\n" \
"v_mul_f32_e32 v122, s70, v122\n" \
"v_mul_f32_e32 v123, s70, v123\n" \
"v_cvt_pk_bf16_f32 v117, v118, v119\n" \
"v_mul_f32_e32 v76, s70, v76\n" \
"v_mul_f32_e32 v77, s70, v77\n" \
"v_mul_f32_e32 v78, s70, v78\n" \
"v_mul_f32_e32 v79, s70, v79\n" \
"v_cvt_pk_bf16_f32 v73, v74, v75\n" \
"s_mov_b32 s66, 0xfff0ffff\n" \
"s_mov_b32 s67, 0xf000ff00\n" \
"s_mov_b32 s68, 0xffc0fffc\n" \
"s_mov_b32 s69, 0xc000fc00\n" \
"v_cndmask_b32_e64 v192, v192, v193, s[64:65]\n" \
"v_add_f32_e64 v164, s47, v164\n" \
"v_cndmask_b32_e64 v194, v194, v195, s[64:65]\n" \
"v_add_f32_e64 v166, s47, v166\n" \
"v_cndmask_b32_e64 v208, v208, v209, s[64:65]\n" \
"v_cndmask_b32_e64 v168, v168, v169, s[64:65]\n" \
"v_cndmask_b32_e64 v210, v210, v211, s[64:65]\n" \
"v_cndmask_b32_e64 v170, v170, v171, s[64:65]\n" \
"v_cndmask_b32_e64 v224, v224, v225, s[64:65]\n" \
"v_cndmask_b32_e64 v172, v172, v173, s[64:65]\n" \
"v_cndmask_b32_e64 v226, v226, v227, s[64:65]\n" \
"v_cndmask_b32_e64 v174, v174, v175, s[64:65]\n" \
"v_cndmask_b32_e64 v240, v240, v241, s[64:65]\n" \
"v_cndmask_b32_e64 v176, v176, v177, s[64:65]\n" \
"v_cndmask_b32_e64 v242, v242, v243, s[64:65]\n" \
"v_cndmask_b32_e64 v178, v178, v179, s[64:65]\n" \
"v_cvt_pk_bf16_f32 v118, v120, v121\n" \
"v_cvt_pk_bf16_f32 v119, v122, v123\n" \
"v_cvt_pk_bf16_f32 v74, v76, v77\n" \
"v_cvt_pk_bf16_f32 v75, v78, v79\n" \
"v_cndmask_b32_e64 v192, v164, v192, s[66:67]\n" \
"v_cndmask_b32_e64 v194, v166, v194, s[68:69]\n" \
"v_cndmask_b32_e64 v208, v168, v208, s[66:67]\n" \
"v_cndmask_b32_e64 v210, v170, v210, s[68:69]\n" \
"v_cndmask_b32_e64 v224, v172, v224, s[66:67]\n" \
"v_cndmask_b32_e64 v226, v174, v226, s[68:69]\n" \
"v_cndmask_b32_e64 v240, v176, v240, s[66:67]\n" \
"v_cndmask_b32_e64 v242, v178, v242, s[68:69]\n" \
"ds_write_b128 v27, v[116:119] offset:8192\n" \
"ds_write_b128 v27, v[72:75] offset:9216\n" \
"v_mfma_f32_16x16x32_bf16 a[172:175], v[160:163], v[136:139], a[172:175]\n" \
"v_max_f32_e32 v193, v192, v194\n" \
"v_max_f32_e32 v209, v208, v210\n" \
"v_max_f32_e32 v225, v224, v226\n" \
"v_max_f32_e32 v241, v240, v242\n" \
"s_mov_b32 s71, 0x3e0293ee\n" \
"v_permlane32_swap_b32_e32 v193, v225\n" \
"v_permlane32_swap_b32_e32 v209, v241\n" \
"v_accvgpr_read_b32 v116, a96\n" \
"v_accvgpr_read_b32 v117, a97\n" \
"v_accvgpr_read_b32 v72, a104\n" \
"v_accvgpr_read_b32 v73, a105\n" \
"v_accvgpr_read_b32 v124, a128\n" \
"v_accvgpr_read_b32 v125, a129\n" \
"buffer_load_dwordx4 v20, s[80:83], s75 offen offset:2048 sc1 lds\n" \
"v_permlane32_swap_b32_e32 v196, v228\n" \
"v_permlane32_swap_b32_e32 v212, v244\n" \
"v_max_f32_e32 v193, v193, v225\n" \
"v_max_f32_e32 v209, v209, v241\n" \
"v_accvgpr_read_b32 v118, a98\n" \
"v_accvgpr_read_b32 v119, a99\n" \
"v_mul_f32_e32 v116, s70, v116\n" \
"v_mul_f32_e32 v117, s70, v117\n" \
"v_accvgpr_read_b32 v74, a106\n" \
"v_accvgpr_read_b32 v75, a107\n" \
"v_mul_f32_e32 v72, s70, v72\n" \
"v_mul_f32_e32 v73, s70, v73\n" \
"v_accvgpr_read_b32 v126, a130\n" \
"v_accvgpr_read_b32 v127, a131\n" \
"v_pk_mul_f32 v[124:125], v[124:125], s[70:71]\n" \
"v_accvgpr_read_b32 v84, a192\n" \
"v_accvgpr_read_b32 v85, a193\n" \
"v_accvgpr_read_b32 v86, a194\n" \
"v_accvgpr_read_b32 v87, a195\n" \
"v_permlane16_swap_b32_e32 v228, v244\n" \
"v_permlane16_swap_b32_e32 v193, v209\n" \
"v_accvgpr_read_b32 v120, a100\n" \
"v_accvgpr_read_b32 v121, a101\n" \
"v_accvgpr_read_b32 v122, a102\n" \
"v_accvgpr_read_b32 v123, a103\n" \
"v_mul_f32_e32 v118, s70, v118\n" \
"v_mul_f32_e32 v119, s70, v119\n" \
"v_cvt_pk_bf16_f32 v116, v116, v117\n" \
"v_accvgpr_read_b32 v76, a108\n" \
"v_accvgpr_read_b32 v77, a109\n" \
"v_accvgpr_read_b32 v78, a110\n" \
"v_accvgpr_read_b32 v79, a111\n" \
"v_mul_f32_e32 v74, s70, v74\n" \
"v_mul_f32_e32 v75, s70, v75\n" \
"v_cvt_pk_bf16_f32 v72, v72, v73\n" \
"v_accvgpr_read_b32 v128, a132\n" \
"v_accvgpr_read_b32 v129, a133\n" \
"v_accvgpr_read_b32 v130, a134\n" \
"v_accvgpr_read_b32 v131, a135\n" \
"v_pk_mul_f32 v[126:127], v[126:127], s[70:71]\n" \
"v_cvt_pk_bf16_f32 v124, v124, v125\n" \
"v_mfma_f32_16x16x32_bf16 v[84:87], v[148:151], v[140:143], v[84:87]\n" \
"s_mov_b32 s72, 0x3eae1a93\n" \
"v_mul_f32_e32 v45, 0xbfb8aa3b, v228\n" \
"v_mfma_f32_16x16x16_bf16 v[48:51], v[182:183], v[46:47], 0\n" \
"v_mfma_f32_16x16x16_bf16 v[196:199], v[190:191], v[46:47], 0\n" \
"v_mfma_f32_16x16x16_bf16 v[212:215], v[206:207], v[46:47], 0\n" \
"v_max_f32_e32 v193, v193, v209\n" \
"v_mul_f32_e32 v120, s70, v120\n" \
"v_mul_f32_e32 v121, s70, v121\n" \
"v_mul_f32_e32 v122, s70, v122\n" \
"v_mul_f32_e64 v123, s70, v123\n" \
"v_cvt_pk_bf16_f32 v117, v118, v119\n" \
"v_mul_f32_e32 v76, s70, v76\n" \
"v_mul_f32_e32 v77, s70, v77\n" \
"v_mul_f32_e32 v78, s70, v78\n" \
"v_mul_f32_e32 v79, s70, v79\n" \
"v_cvt_pk_bf16_f32 v73, v74, v75\n" \
"v_pk_mul_f32 v[128:129], v[128:129], s[70:71]\n" \
"v_pk_mul_f32 v[130:131], v[130:131], s[70:71]\n" \
"v_cvt_pk_bf16_f32 v125, v126, v127\n" \
"v_mfma_f32_16x16x16_bf16 v[228:231], v[222:223], v[46:47], 0\n" \
"v_mul_f32_e64 v193, s72, v193\n" \
"v_cvt_pk_bf16_f32 v118, v120, v121\n" \
"v_cvt_pk_bf16_f32 v119, v122, v123\n" \
"v_cvt_pk_bf16_f32 v74, v76, v77\n" \
"v_cvt_pk_bf16_f32 v75, v78, v79\n" \
"v_cvt_pk_bf16_f32 v126, v128, v129\n" \
"v_cvt_pk_bf16_f32 v127, v130, v131\n" \
"v_mfma_f32_16x16x16_bf16 v[244:247], v[238:239], v[46:47], 0\n" \
"v_mov_b32_e64 v209, v193\n" \
"ds_write_b128 v27, v[116:119] offset:12288\n" \
"ds_write_b128 v27, v[72:75] offset:13312\n" \
"ds_write_b128 v21, v[124:127] offset:16384\n" \
"v_accvgpr_read_b32 v100, a224\n" \
"v_accvgpr_read_b32 v101, a225\n" \
"v_accvgpr_read_b32 v102, a226\n" \
"v_accvgpr_read_b32 v103, a227\n" \
"v_permlane16_swap_b32_e32 v193, v209\n" \
"v_mov_b32_e32 v225, v193\n" \
"v_mfma_f32_16x16x32_bf16 v[100:103], v[148:151], v[144:147], v[100:103]\n" \
"v_mov_b32_e64 v241, v209\n" \
"v_accvgpr_read_b32 v116, a136\n" \
"v_accvgpr_read_b32 v117, a137\n" \
"v_accvgpr_read_b32 v72, a160\n" \
"v_accvgpr_read_b32 v73, a161\n" \
"v_accvgpr_read_b32 v124, a168\n" \
"v_accvgpr_read_b32 v125, a169\n" \
"v_permlane32_swap_b32_e32 v193, v225\n" \
"v_permlane32_swap_b32_e32 v209, v241\n" \
"v_accvgpr_read_b32 v118, a138\n" \
"v_accvgpr_read_b32 v119, a139\n" \
"v_pk_mul_f32 v[116:117], v[116:117], s[70:71]\n" \
"v_accvgpr_read_b32 v74, a162\n" \
"v_accvgpr_read_b32 v75, a163\n" \
"v_pk_mul_f32 v[72:73], v[72:73], s[70:71]\n" \
"v_accvgpr_read_b32 v126, a170\n" \
"v_accvgpr_read_b32 v127, a171\n" \
"v_pk_mul_f32 v[124:125], v[124:125], s[70:71]\n" \
"v_pk_mul_f32 v[84:85], v[84:85], s[70:71]\n" \
"v_pk_mul_f32 v[100:101], v[100:101], s[70:71]\n" \
"v_fma_f32 v192, v192, s72, -v193\n" \
"v_fma_f32 v194, v194, s72, -v193\n" \
"v_fma_f32 v208, v208, s72, -v209\n" \
"v_fma_f32 v210, v210, s72, -v209\n" \
"v_accvgpr_read_b32 v120, a140\n" \
"v_accvgpr_read_b32 v121, a141\n" \
"v_accvgpr_read_b32 v122, a142\n" \
"v_accvgpr_read_b32 v123, a143\n" \
"v_pk_mul_f32 v[118:119], v[118:119], s[70:71]\n" \
"v_cvt_pk_bf16_f32 v116, v116, v117\n" \
"v_accvgpr_read_b32 v76, a164\n" \
"v_accvgpr_read_b32 v77, a165\n" \
"v_accvgpr_read_b32 v78, a166\n" \
"v_accvgpr_read_b32 v79, a167\n" \
"v_pk_mul_f32 v[74:75], v[74:75], s[70:71]\n" \
"v_cvt_pk_bf16_f32 v72, v72, v73\n" \
"v_accvgpr_read_b32 v128, a172\n" \
"v_accvgpr_read_b32 v129, a173\n" \
"v_accvgpr_read_b32 v130, a174\n" \
"v_accvgpr_read_b32 v131, a175\n" \
"v_pk_mul_f32 v[126:127], v[126:127], s[70:71]\n" \
"v_cvt_pk_bf16_f32 v124, v124, v125\n" \
"v_pk_mul_f32 v[86:87], v[86:87], s[70:71]\n" \
"v_cvt_pk_bf16_f32 v84, v84, v85\n" \
"v_pk_mul_f32 v[102:103], v[102:103], s[70:71]\n" \
"v_cvt_pk_bf16_f32 v100, v100, v101\n" \
"v_accvgpr_read_b32 v88, a196\n" \
"v_accvgpr_read_b32 v89, a197\n" \
"v_accvgpr_read_b32 v90, a198\n" \
"v_accvgpr_read_b32 v91, a199\n" \
"v_exp_f32_e32 v192, v192\n" \
"v_exp_f32_e32 v194, v194\n" \
"v_exp_f32_e32 v208, v208\n" \
"v_exp_f32_e32 v210, v210\n" \
"v_fma_f32 v224, v224, s72, -v225\n" \
"v_fma_f32 v226, v226, s72, -v225\n" \
"v_pk_mul_f32 v[120:121], v[120:121], s[70:71]\n" \
"v_pk_mul_f32 v[122:123], v[122:123], s[70:71]\n" \
"v_cvt_pk_bf16_f32 v117, v118, v119\n" \
"v_pk_mul_f32 v[76:77], v[76:77], s[70:71]\n" \
"v_pk_mul_f32 v[78:79], v[78:79], s[70:71]\n" \
"v_cvt_pk_bf16_f32 v73, v74, v75\n" \
"v_pk_mul_f32 v[128:129], v[128:129], s[70:71]\n" \
"v_pk_mul_f32 v[130:131], v[130:131], s[70:71]\n" \
"v_cvt_pk_bf16_f32 v125, v126, v127\n" \
"v_cvt_pk_bf16_f32 v85, v86, v87\n" \
"v_cvt_pk_bf16_f32 v101, v102, v103\n" \
"v_mfma_f32_16x16x32_bf16 v[88:91], v[152:155], v[140:143], v[88:91]\n" \
"v_exp_f32_e32 v224, v224\n" \
"v_exp_f32_e32 v226, v226\n" \
"v_fma_f32 v240, v240, s72, -v241\n" \
"v_fma_f32 v242, v242, s72, -v241\n" \
"v_cvt_pk_bf16_f32 v118, v120, v121\n" \
"v_cvt_pk_bf16_f32 v119, v122, v123\n" \
"v_cvt_pk_bf16_f32 v74, v76, v77\n" \
"v_cvt_pk_bf16_f32 v75, v78, v79\n" \
"v_pk_mul_f32 v[88:89], v[88:89], s[70:71]\n" \
"v_pk_mul_f32 v[90:91], v[90:91], s[70:71]\n" \
"v_cvt_pk_bf16_f32 v126, v128, v129\n" \
"v_cvt_pk_bf16_f32 v127, v130, v131\n" \
"v_cvt_pk_bf16_f32 v86, v88, v89\n" \
"v_cvt_pk_bf16_f32 v87, v90, v91\n" \
"v_cvt_pk_bf16_f32 v192, v192, v194\n" \
"v_cvt_pk_bf16_f32 v208, v208, v210\n" \
"v_exp_f32_e32 v240, v240\n" \
"v_exp_f32_e32 v242, v242\n" \
"ds_write_b128 v21, v[116:119] offset:17408\n" \
"ds_write_b128 v21, v[72:75] offset:20480\n" \
"ds_write_b128 v21, v[124:127] offset:21504\n" \
"ds_write_b128 v21, v[84:87] offset:24576\n" \
"v_cvt_pk_bf16_f32 v62, v48, v49\n" \
"v_cvt_pk_bf16_f32 v63, v50, v51\n" \
"v_cvt_pk_bf16_f32 v64, v196, v197\n" \
"v_cvt_pk_bf16_f32 v65, v198, v199\n" \
"v_cvt_pk_bf16_f32 v66, v212, v213\n" \
"v_cvt_pk_bf16_f32 v67, v214, v215\n" \
"v_perm_b32 v184, v192, v192, v40\n" \
"v_perm_b32 v185, v192, v192, v41\n" \
"v_perm_b32 v186, v192, v192, v42\n" \
"v_perm_b32 v187, v192, v192, v43\n" \
"v_perm_b32 v200, v208, v208, v40\n" \
"v_perm_b32 v201, v208, v208, v41\n" \
"v_perm_b32 v202, v208, v208, v42\n" \
"v_perm_b32 v203, v208, v208, v43\n" \
"v_cvt_pk_bf16_f32 v224, v224, v226\n" \
"v_accvgpr_read_b32 v104, a228\n" \
"v_accvgpr_read_b32 v105, a229\n" \
"v_accvgpr_read_b32 v106, a230\n" \
"v_accvgpr_read_b32 v107, a231\n" \
"v_cvt_pk_bf16_f32 v68, v228, v229\n" \
"v_cvt_pk_bf16_f32 v69, v230, v231\n" \
"v_mfma_f32_16x16x32_bf16 v[164:167], v[62:65], v[184:187], 0\n" \
"v_mfma_f32_16x16x32_bf16 v[168:171], v[64:67], v[200:203], 0\n" \
"v_perm_b32 v216, v224, v224, v40\n" \
"v_perm_b32 v217, v224, v224, v41\n" \
"v_perm_b32 v218, v224, v224, v42\n" \
"v_perm_b32 v219, v224, v224, v43\n" \
"v_cvt_pk_bf16_f32 v240, v240, v242\n" \
"v_mfma_f32_16x16x32_bf16 v[104:107], v[152:155], v[144:147], v[104:107]\n" \
"v_cvt_pk_bf16_f32 v70, v244, v245\n" \
"v_cvt_pk_bf16_f32 v71, v246, v247\n" \
"v_mfma_f32_16x16x32_bf16 v[172:175], v[66:69], v[216:219], 0\n" \
"v_perm_b32 v232, v240, v240, v40\n" \
"v_perm_b32 v233, v240, v240, v41\n" \
"v_perm_b32 v234, v240, v240, v42\n" \
"v_perm_b32 v235, v240, v240, v43\n" \
"buffer_load_dwordx4 v20, s[80:83], s75 offen offset:3072 sc1 lds\n" \
"v_pk_mul_f32 v[104:105], v[104:105], s[70:71]\n" \
"v_pk_mul_f32 v[106:107], v[106:107], s[70:71]\n" \
"v_cvt_pk_bf16_f32 v102, v104, v105\n" \
"v_cvt_pk_bf16_f32 v103, v106, v107\n" \
"ds_write_b128 v21, v[100:103] offset:28672\n" \
"v_mfma_f32_16x16x32_bf16 v[176:179], v[68:71], v[232:235], 0\n" \
"v_accvgpr_read_b32 v92, a200\n" \
"v_accvgpr_read_b32 v93, a201\n" \
"v_accvgpr_read_b32 v94, a202\n" \
"v_accvgpr_read_b32 v95, a203\n" \
"v_accvgpr_read_b32 v108, a232\n" \
"v_accvgpr_read_b32 v109, a233\n" \
"v_mfma_f32_16x16x32_bf16 v[92:95], v[156:159], v[140:143], v[92:95]\n" \
"v_accvgpr_read_b32 v110, a234\n" \
"v_accvgpr_read_b32 v111, a235\n" \
"v_cvt_pk_bf16_f32 v52, v252, v253\n" \
"v_cvt_pk_bf16_f32 v53, v254, v255\n" \
"v_mfma_f32_16x16x32_bf16 v[108:111], v[156:159], v[144:147], v[108:111]\n" \
"v_exp_f32_e64 v45, v45\n" \
"v_mfma_f32_16x16x32_bf16 v[192:195], v[52:55], v[184:187], 0\n" \
"v_mfma_f32_16x16x32_bf16 v[208:211], v[54:57], v[200:203], 0\n" \
"v_pk_mul_f32 v[92:93], v[92:93], s[70:71]\n" \
"v_pk_mul_f32 v[94:95], v[94:95], s[70:71]\n" \
"v_cvt_pk_bf16_f32 v92, v92, v93\n" \
"v_cvt_pk_bf16_f32 v93, v94, v95\n" \
"v_mfma_f32_16x16x32_bf16 v[224:227], v[56:59], v[216:219], 0\n" \
"v_pk_mul_f32 v[108:109], v[108:109], s[70:71]\n" \
"v_pk_mul_f32 v[110:111], v[110:111], s[70:71]\n" \
"v_cvt_pk_bf16_f32 v108, v108, v109\n" \
"v_cvt_pk_bf16_f32 v109, v110, v111\n" \
"v_mfma_f32_16x16x32_bf16 v[240:243], v[58:61], v[232:235], 0\n" \
"v_mov_b32_dpp v166, v170 quad_perm:[0,1,2,3] row_mask:0x2 bank_mask:0xf\n" \
"v_accvgpr_read_b32 v96, a204\n" \
"v_accvgpr_read_b32 v97, a205\n" \
"v_mov_b32_dpp v166, v174 quad_perm:[0,1,2,3] row_mask:0x4 bank_mask:0xf\n" \
"v_accvgpr_read_b32 v98, a206\n" \
"v_accvgpr_read_b32 v99, a207\n" \
"v_add_f32_e64 v45, 1.0, v45\n" \
"v_mov_b32_dpp v166, v178 quad_perm:[0,1,2,3] row_mask:0x8 bank_mask:0xf\n" \
"v_mfma_f32_16x16x32_bf16 v[96:99], v[160:163], v[140:143], v[96:99]\n" \
"v_rcp_f32_e64 v45, v45\n" \
"v_mov_b32_dpp v164, v168 quad_perm:[0,1,2,3] row_mask:0x2 bank_mask:0xf\n" \
"v_mov_b32_dpp v165, v169 quad_perm:[0,1,2,3] row_mask:0x2 bank_mask:0xf\n" \
"v_rcp_f32_e64 v166, v166\n" \
"v_mov_b32_dpp v164, v172 quad_perm:[0,1,2,3] row_mask:0x4 bank_mask:0xf\n" \
"v_mov_b32_dpp v165, v173 quad_perm:[0,1,2,3] row_mask:0x4 bank_mask:0xf\n" \
"v_mul_f32_e64 v166, v45, v166\n" \
"v_mov_b32_dpp v164, v176 quad_perm:[0,1,2,3] row_mask:0x8 bank_mask:0xf\n" \
"v_pk_mul_f32 v[96:97], v[96:97], s[70:71]\n" \
"v_pk_mul_f32 v[98:99], v[98:99], s[70:71]\n" \
"v_cvt_pk_bf16_f32 v94, v96, v97\n" \
"v_cvt_pk_bf16_f32 v95, v98, v99\n" \
"ds_write_b128 v21, v[92:95] offset:25600\n" \
"v_mov_b32_dpp v165, v177 quad_perm:[0,1,2,3] row_mask:0x8 bank_mask:0xf\n" \
"v_accvgpr_read_b32 v112, a236\n" \
"v_accvgpr_read_b32 v113, a237\n" \
"v_accvgpr_read_b32 v114, a238\n" \
"v_accvgpr_read_b32 v115, a239\n" \
"v_mul_f32_e32 v164, v166, v164\n" \
"v_mul_f32_e32 v165, v166, v165\n" \
"v_mov_b32_e64 v170, v166\n" \
"v_mfma_f32_16x16x32_bf16 v[112:115], v[160:163], v[144:147], v[112:115]\n" \
"s_mov_b32 s76, s52\n" \
"v_permlane16_swap_b32_e32 v166, v170\n" \
"s_mov_b32 m0, s53\n" \
"v_mov_b32_e32 v174, v166\n" \
"v_mov_b32_e64 v178, v170\n" \
"buffer_load_dwordx4 v20, s[80:83], s76 offen offset:0 sc1 lds\n" \
"v_permlane32_swap_b32_e32 v166, v174\n" \
"v_permlane32_swap_b32_e32 v170, v178\n" \
"v_pk_mul_f32 v[112:113], v[112:113], s[70:71]\n" \
"v_pk_mul_f32 v[114:115], v[114:115], s[70:71]\n" \
"v_cvt_pk_bf16_f32 v110, v112, v113\n" \
"v_cvt_pk_bf16_f32 v111, v114, v115\n" \
"ds_write_b128 v21, v[108:111] offset:29696\n" \
"v_mul_f32_e32 v192, v166, v192\n" \
"v_mul_f32_e32 v193, v166, v193\n" \
"v_mul_f32_e32 v208, v170, v208\n" \
"v_mul_f32_e32 v209, v170, v209\n" \
"v_mul_f32_e64 v224, v174, v224\n" \
"buffer_load_dwordx4 v20, s[80:83], s76 offen offset:1024 sc1 lds\n" \
"v_mul_f32_e32 v225, v174, v225\n" \
"v_mul_f32_e32 v240, v178, v240\n" \
"v_mul_f32_e32 v241, v178, v241\n" \
"v_mul_f32_e32 v194, v166, v194\n" \
"v_mul_f32_e64 v195, v166, v195\n" \
"v_cvt_pk_bf16_f32 v192, v192, v193\n" \
"v_mul_f32_e32 v210, v170, v210\n" \
"v_mul_f32_e32 v211, v170, v211\n" \
"v_cvt_pk_bf16_f32 v208, v208, v209\n" \
"v_mul_f32_e32 v226, v174, v226\n" \
"v_mul_f32_e32 v227, v174, v227\n" \
"v_cvt_pk_bf16_f32 v224, v224, v225\n" \
"v_mul_f32_e32 v242, v178, v242\n" \
"v_mul_f32_e32 v243, v178, v243\n" \
"v_cvt_pk_bf16_f32 v240, v240, v241\n" \
"v_sub_f32_e32 v44, 1.0, v45\n" \
"v_cvt_pk_bf16_f32 v164, v164, v165\n" \
"v_cvt_pk_bf16_f32 v193, v194, v195\n" \
"s_add_u32 s84, s48, 0x0\n" \
"v_cvt_pk_bf16_f32 v209, v210, v211\n" \
"s_add_u32 s85, s48, 0x24800\n" \
"v_cvt_pk_bf16_f32 v225, v226, v227\n" \
"s_add_u32 s86, s48, 0x49000\n" \
"v_cvt_pk_bf16_f32 v241, v242, v243\n" \
"s_add_u32 s87, s48, 0x6d800\n" \
"ds_write_b32 v26, v44\n" \
"buffer_store_dword v164, v25, s[80:83], s49 offen sc1\n" \
"buffer_store_dwordx2 v[192:193], v24, s[80:83], s84 offen sc1\n" \
"buffer_store_dwordx2 v[208:209], v24, s[80:83], s85 offen sc1\n" \
"buffer_store_dwordx2 v[224:225], v24, s[80:83], s86 offen sc1\n" \
"buffer_store_dwordx2 v[240:241], v24, s[80:83], s87 offen sc1\n" \
"s_branch L_end_%=\n" \
"L_poll60_%=:\n" \
"s_mov_b32 s84, 0\n" \
"L_pl60_%=:\n" \
"s_sleep 1\n" \
"v_bfe_u32 v44, v20, 2, 5\n" \
"buffer_load_dword v44, v44, s[80:83], s38 offen offset:128 sc1\n" \
"s_waitcnt vmcnt(0)\n" \
"v_cmp_ne_u32_e32 vcc, s62, v44\n" \
"s_add_u32 s84, s84, 1\n" \
"s_nop 3\n" \
"s_cbranch_vccz L_pollr60_%=\n" \
"s_cmp_gt_u32 s84, 0x400000\n" \
"s_cbranch_scc0 L_pl60_%=\n" \
"s_branch L_pollr60_%=\n" \
"L_poll160_%=:\n" \
"s_mov_b32 s84, 0\n" \
"L_pl160_%=:\n" \
"s_sleep 1\n" \
"v_bfe_u32 v44, v20, 2, 5\n" \
"buffer_load_dword v44, v44, s[80:83], s38 offen offset:0 sc1\n" \
"s_waitcnt vmcnt(0)\n" \
"v_cmp_ne_u32_e32 vcc, s62, v44\n" \
"s_add_u32 s84, s84, 1\n" \
"s_nop 3\n" \
"s_cbranch_vccz L_pollr160_%=\n" \
"s_cmp_gt_u32 s84, 0x400000\n" \
"s_cbranch_scc0 L_pl160_%=\n" \
"s_branch L_pollr160_%=\n" \
"L_w1_%=:\n" \
"s_waitcnt vmcnt(14)\n" \
"s_barrier\n" \
"s_nop 0\n" \
"ds_read_b64_tr_b16 v[84:85], v32 offset:0\n" \
"ds_read_b64_tr_b16 v[86:87], v32 offset:1024\n" \
"ds_read_b128 v[52:55], v28 offset:16384\n" \
"ds_read_b128 v[56:59], v28 offset:18432\n" \
"ds_read_b64_tr_b16 v[88:89], v33 offset:0\n" \
"ds_read_b64_tr_b16 v[90:91], v33 offset:1024\n" \
"ds_read_b64_tr_b16 v[92:93], v34 offset:0\n" \
"ds_read_b64_tr_b16 v[94:95], v34 offset:1024\n" \
"ds_read_b64_tr_b16 v[96:97], v35 offset:0\n" \
"ds_read_b64_tr_b16 v[98:99], v35 offset:1024\n" \
"ds_read_b64_tr_b16 v[100:101], v36 offset:0\n" \
"ds_read_b64_tr_b16 v[102:103], v36 offset:1024\n" \
"ds_read_b64_tr_b16 v[104:105], v37 offset:0\n" \
"ds_read_b64_tr_b16 v[106:107], v37 offset:1024\n" \
"ds_read_b64_tr_b16 v[108:109], v38 offset:0\n" \
"ds_read_b64_tr_b16 v[110:111], v38 offset:1024\n" \
"ds_read_b64_tr_b16 v[112:113], v39 offset:0\n" \
"ds_read_b64_tr_b16 v[114:115], v39 offset:1024\n" \
"ds_read_b128 v[60:63], v28 offset:20480\n" \
"ds_read_b128 v[64:67], v28 offset:22528\n" \
"ds_read_b128 v[68:71], v28 offset:0\n" \
"ds_read_b128 v[72:75], v28 offset:2048\n" \
"ds_read_b128 v[76:79], v28 offset:4096\n" \
"ds_read_b128 v[80:83], v28 offset:6144\n" \
"s_waitcnt lgkmcnt(15)\n" \
"s_nop 0\n" \
"v_mfma_f32_16x16x32_bf16 a[0:3], v[52:55], v[84:87], 0\n" \
"s_add_u32 m0, s30, 0x9800\n" \
"s_mov_b32 s85, 0xc080\n" \
"v_mfma_f32_16x16x32_bf16 a[32:35], v[56:59], v[84:87], 0\n" \
"buffer_load_dwordx4 v22, s[64:67], s85 offen lds\n" \
"v_mfma_f32_16x16x32_bf16 a[4:7], v[52:55], v[88:91], 0\n" \
"ds_read_b64_tr_b16 v[148:149], v32 offset:8192\n" \
"v_mad_i32_i24 v217, v216, -6, v215\n" \
"v_mfma_f32_16x16x32_bf16 a[36:39], v[56:59], v[88:91], 0\n" \
"s_add_u32 m0, s30, 0x9c00\n" \
"s_mov_b32 s84, 0xe080\n" \
"v_mfma_f32_16x16x32_bf16 a[8:11], v[52:55], v[92:95], 0\n" \
"buffer_load_dwordx4 v23, s[64:67], s84 offen lds\n" \
"v_mfma_f32_16x16x32_bf16 a[40:43], v[56:59], v[92:95], 0\n" \
"ds_read_b64_tr_b16 v[150:151], v32 offset:9216\n" \
"v_add_u32_e32 v218, 0xfffffe50, v215\n" \
"s_waitcnt lgkmcnt(15)\n" \
"v_mfma_f32_16x16x32_bf16 a[12:15], v[52:55], v[96:99], 0\n" \
"s_add_u32 m0, s31, 0x8c00\n" \
"s_mov_b32 s85, 0x98000\n" \
"v_mfma_f32_16x16x32_bf16 a[44:47], v[56:59], v[96:99], 0\n" \
"buffer_load_dwordx4 v27, s[68:71], s85 offen lds\n" \
"s_waitcnt lgkmcnt(14)\n" \
"v_mfma_f32_16x16x32_bf16 a[16:19], v[52:55], v[100:103], 0\n" \
"ds_read_b128 v[116:119], v29 offset:16384\n" \
"s_mul_i32 s35, s6, 0x48\n" \
"v_mfma_f32_16x16x32_bf16 a[48:51], v[56:59], v[100:103], 0\n" \
"s_add_u32 m0, s31, 0xcc00\n" \
"s_mov_b32 s84, 0x98000\n" \
"s_waitcnt lgkmcnt(13)\n" \
"v_mfma_f32_16x16x32_bf16 a[20:23], v[52:55], v[104:107], 0\n" \
"buffer_load_dwordx4 v27, s[72:75], s84 offen lds\n" \
"v_mfma_f32_16x16x32_bf16 a[52:55], v[56:59], v[104:107], 0\n" \
"ds_read_b128 v[120:123], v29 offset:18432\n" \
"v_lshrrev_b32_e32 v219, 1, v217\n" \
"s_waitcnt lgkmcnt(12)\n" \
"v_mfma_f32_16x16x32_bf16 a[24:27], v[52:55], v[108:111], 0\n" \
"ds_read_b64_tr_b16 v[152:153], v33 offset:8192\n" \
"v_add_u32_e32 v218, s35, v216\n" \
"v_mfma_f32_16x16x32_bf16 a[56:59], v[56:59], v[108:111], 0\n" \
"ds_read_b64_tr_b16 v[154:155], v33 offset:9216\n" \
"v_mul_u32_u24_e32 v219, 0x480, v219\n" \
"s_waitcnt lgkmcnt(12)\n" \
"v_mfma_f32_16x16x32_bf16 a[28:31], v[52:55], v[112:115], 0\n" \
"ds_read_b64_tr_b16 v[156:157], v34 offset:8192\n" \
"v_and_b32_e32 v214, 16, v20\n" \
"v_mfma_f32_16x16x32_bf16 a[60:63], v[56:59], v[112:115], 0\n" \
"ds_read_b64_tr_b16 v[158:159], v34 offset:9216\n" \
"v_mad_u32_u24 v219, v218, s5, v219\n" \
"s_waitcnt lgkmcnt(13)\n" \
"v_mfma_f32_16x16x32_bf16 a[92:95], v[60:63], v[112:115], 0\n" \
"s_mov_b32 m0, s32\n" \
"v_or_b32_e32 v192, v219, v214\n" \
"s_waitcnt lgkmcnt(12)\n" \
"v_mfma_f32_16x16x32_bf16 a[124:127], v[64:67], v[112:115], 0\n" \
"s_mov_b32 exec_lo, 0xff000000\n" \
"s_mov_b32 exec_hi, 0x0\n" \
"global_load_lds_dword v[184:185], off\n" \
"s_mov_b64 exec, -1\n" \
"v_mfma_f32_16x16x32_bf16 a[88:91], v[60:63], v[108:111], 0\n" \
"ds_read_b64_tr_b16 v[160:161], v35 offset:8192\n" \
"v_mov_b32_e64 v220, 2.0\n" \
"v_mfma_f32_16x16x32_bf16 a[120:123], v[64:67], v[108:111], 0\n" \
"ds_read_b64_tr_b16 v[162:163], v35 offset:9216\n" \
"v_mov_b32_e64 v222, 7\n" \
"v_mfma_f32_16x16x32_bf16 a[84:87], v[60:63], v[104:107], 0\n" \
"v_lshlrev_b32_e32 v220, 7, v218\n" \
"v_lshrrev_b32_e32 v218, 1, v218\n" \
"v_mfma_f32_16x16x32_bf16 a[116:119], v[64:67], v[104:107], 0\n" \
"s_mov_b32 m0, s32\n" \
"ds_read_b64_tr_b16 v[164:165], v36 offset:8192\n" \
"v_mfma_f32_16x16x32_bf16 a[80:83], v[60:63], v[100:103], 0\n" \
"s_mov_b32 exec_lo, 0xffff\n" \
"s_mov_b32 exec_hi, 0x0\n" \
"global_load_lds_dword v[186:187], off\n" \
"s_mov_b64 exec, -1\n" \
"v_mfma_f32_16x16x32_bf16 a[112:115], v[64:67], v[100:103], 0\n" \
"ds_read_b64_tr_b16 v[166:167], v36 offset:9216\n" \
"v_and_b32_e32 v218, 4, v218\n" \
"v_mfma_f32_16x16x32_bf16 a[76:79], v[60:63], v[96:99], 0\n" \
"v_and_b32_e64 v216, 2, v216\n" \
"s_mul_i32 s7, s6, 0x2400\n" \
"v_mfma_f32_16x16x32_bf16 a[108:111], v[64:67], v[96:99], 0\n" \
"s_sub_i32 s34, 0x22000, s7\n" \
"v_bitop3_b32 v216, v217, v218, v216 bitop3:0x1e\n" \
"v_mfma_f32_16x16x32_bf16 a[72:75], v[60:63], v[92:95], 0\n" \
"ds_read_b64_tr_b16 v[168:169], v37 offset:8192\n" \
"v_or_b32_e32 v213, 0x24400, v20\n" \
"v_mfma_f32_16x16x32_bf16 a[104:107], v[64:67], v[92:95], 0\n" \
"v_lshlrev_b32_e64 v216, 4, v216\n" \
"v_add3_u32 v196, v220, s34, v216\n" \
"v_mfma_f32_16x16x32_bf16 a[68:71], v[60:63], v[88:91], 0\n" \
"ds_read_b64_tr_b16 v[170:171], v37 offset:9216\n" \
"v_mov_b32_e64 v216, v213\n" \
"v_mfma_f32_16x16x32_bf16 a[100:103], v[64:67], v[88:91], 0\n" \
"s_movk_i32 s36, 0x1b0\n" \
"v_or_b32_e32 v216, 0x100, v215\n" \
"v_mfma_f32_16x16x32_bf16 a[64:67], v[60:63], v[84:87], 0\n" \
"ds_read_b64_tr_b16 v[172:173], v38 offset:8192\n" \
"v_mul_u32_u24_e32 v217, 0x2aab, v216\n" \
"v_mfma_f32_16x16x32_bf16 a[96:99], v[64:67], v[84:87], 0\n" \
"v_add_u32_e32 v215, 0xffffff50, v215\n" \
"v_lshrrev_b32_e32 v217, 16, v217\n" \
"s_waitcnt lgkmcnt(15)\n" \
"v_mfma_f32_16x16x32_bf16 a[128:131], v[68:71], v[84:87], 0\n" \
"ds_read_b64_tr_b16 v[174:175], v38 offset:9216\n" \
"v_mov_b32_e32 v220, v215\n" \
"v_mfma_f32_16x16x32_bf16 a[160:163], v[72:75], v[84:87], 0\n" \
"v_mad_i32_i24 v218, v217, -6, v216\n" \
"v_cmp_gt_u32_e64 vcc, s36, v216\n" \
"v_mfma_f32_16x16x32_bf16 a[132:135], v[68:71], v[88:91], 0\n" \
"ds_read_b64_tr_b16 v[176:177], v39 offset:8192\n" \
"v_mov_b32_e64 v221, 7\n" \
"v_mfma_f32_16x16x32_bf16 a[164:167], v[72:75], v[88:91], 0\n" \
"v_lshrrev_b32_e32 v222, 1, v218\n" \
"v_cndmask_b32_e32 v217, v220, v217, vcc\n" \
"v_mfma_f32_16x16x32_bf16 a[136:139], v[68:71], v[92:95], 0\n" \
"ds_read_b64_tr_b16 v[178:179], v39 offset:9216\n" \
"s_movk_i32 s4, 0x48\n" \
"v_mfma_f32_16x16x32_bf16 a[168:171], v[72:75], v[92:95], 0\n" \
"v_add_u32_e32 v220, s35, v217\n" \
"v_mul_u32_u24_e32 v222, 0x480, v222\n" \
"v_mfma_f32_16x16x32_bf16 a[140:143], v[68:71], v[96:99], 0\n" \
"v_mad_u32_u24 v222, v220, s5, v222\n" \
"v_cmp_gt_u32_e64 s[4:5], s4, v215\n" \
"v_mfma_f32_16x16x32_bf16 a[172:175], v[72:75], v[96:99], 0\n" \
"ds_read_b128 v[124:127], v29 offset:20480\n" \
"v_cndmask_b32_e64 v215, v221, v218, vcc\n" \
"v_mfma_f32_16x16x32_bf16 a[144:147], v[68:71], v[100:103], 0\n" \
"v_lshrrev_b32_e32 v218, 1, v220\n" \
"v_or_b32_e32 v214, v222, v214\n" \
"v_mfma_f32_16x16x32_bf16 a[176:179], v[72:75], v[100:103], 0\n" \
"ds_read_b128 v[128:131], v29 offset:22528\n" \
"v_mov_b32_e64 v222, 2.0\n" \
"v_mfma_f32_16x16x32_bf16 a[148:151], v[68:71], v[104:107], 0\n" \
"v_and_b32_e32 v218, 4, v218\n" \
"v_and_b32_e32 v217, 2, v217\n" \
"v_mfma_f32_16x16x32_bf16 a[180:183], v[72:75], v[104:107], 0\n" \
"ds_read_b128 v[132:135], v29 offset:0\n" \
"v_bitop3_b32 v215, v215, v218, v217 bitop3:0x1e\n" \
"v_mfma_f32_16x16x32_bf16 a[152:155], v[68:71], v[108:111], 0\n" \
"v_lshlrev_b32_e32 v223, 1, v220\n" \
"v_mov_b32_e32 v224, 2.0\n" \
"v_mfma_f32_16x16x32_bf16 a[184:187], v[72:75], v[108:111], 0\n" \
"ds_read_b128 v[136:139], v29 offset:2048\n" \
"v_cndmask_b32_e64 v193, v222, v214, vcc\n" \
"v_mfma_f32_16x16x32_bf16 a[156:159], v[68:71], v[112:115], 0\n" \
"v_lshlrev_b32_e32 v214, 7, v220\n" \
"v_lshlrev_b32_e32 v215, 4, v215\n" \
"v_mfma_f32_16x16x32_bf16 a[188:191], v[72:75], v[112:115], 0\n" \
"ds_read_b128 v[140:143], v29 offset:4096\n" \
"s_mul_i32 s39, s3, 18\n" \
"v_mfma_f32_16x16x32_bf16 a[220:223], v[76:79], v[112:115], 0\n" \
"v_add3_u32 v214, v214, s34, v215\n" \
"v_cndmask_b32_e64 v195, v224, v223, s[4:5]\n" \
"v_mfma_f32_16x16x32_bf16 a[252:255], v[80:83], v[112:115], 0\n" \
"ds_read_b128 v[144:147], v29 offset:6144\n" \
"s_movk_i32 s4, 0x1f8\n" \
"v_mfma_f32_16x16x32_bf16 a[216:219], v[76:79], v[108:111], 0\n" \
"v_mov_b32_e32 v215, v213\n" \
"s_add_i32 s35, s35, s39\n" \
"v_mfma_f32_16x16x32_bf16 a[248:251], v[80:83], v[108:111], 0\n" \
"v_cmp_gt_u32_e32 vcc, s4, v216\n" \
"v_add_u32_e32 v212, s35, v212\n" \
"v_mfma_f32_16x16x32_bf16 a[212:215], v[76:79], v[104:107], 0\n" \
"v_mov_b32_e32 v219, 2.0\n" \
"v_cndmask_b32_e32 v197, v215, v214, vcc\n" \
"v_mfma_f32_16x16x32_bf16 a[244:247], v[80:83], v[104:107], 0\n" \
"s_movk_i32 s35, 0x120\n" \
"v_mul_u32_u24_e32 v214, 0xd80, v212\n" \
"v_mfma_f32_16x16x32_bf16 a[208:211], v[76:79], v[100:103], 0\n" \
"v_cmp_gt_u32_e32 vcc, s35, v20\n" \
"v_lshrrev_b32_e64 v215, 1, v212\n" \
"v_mfma_f32_16x16x32_bf16 a[240:243], v[80:83], v[100:103], 0\n" \
"s_and_b32 s4, s2, 7\n" \
"v_cndmask_b32_e32 v194, v219, v214, vcc\n" \
"v_mfma_f32_16x16x32_bf16 a[204:207], v[76:79], v[96:99], 0\n" \
"v_lshlrev_b32_e32 v214, 7, v212\n" \
"v_lshrrev_b32_e32 v212, 2, v212\n" \
"v_mfma_f32_16x16x32_bf16 a[236:239], v[80:83], v[96:99], 0\n" \
"v_and_b32_e64 v212, 2, v212\n" \
"v_and_or_b32 v212, v215, 1, v212\n" \
"v_mfma_f32_16x16x32_bf16 a[200:203], v[76:79], v[92:95], 0\n" \
"s_lshl_b32 s5, s4, 2\n" \
"s_bfe_u32 s38, s2, 0x20003\n" \
"v_mfma_f32_16x16x32_bf16 a[232:235], v[80:83], v[92:95], 0\n" \
"v_lshlrev_b32_e32 v212, 5, v212\n" \
"s_movk_i32 s35, 0x60\n" \
"v_mfma_f32_16x16x32_bf16 a[196:199], v[76:79], v[88:91], 0\n" \
"s_or_b32 s5, s5, s38\n" \
"v_bitop3_b32 v212, v212, s35, v214 bitop3:0x36\n" \
"v_mfma_f32_16x16x32_bf16 a[228:231], v[80:83], v[88:91], 0\n" \
"s_lshr_b32 s36, s2, 3\n" \
"v_add_u32_e32 v212, s34, v212\n" \
"v_mfma_f32_16x16x32_bf16 a[192:195], v[76:79], v[84:87], 0\n" \
"s_mul_i32 s34, s5, 0x12000\n" \
"s_or_b32 s40, s34, 0x6000000\n" \
"v_mfma_f32_16x16x32_bf16 a[224:227], v[80:83], v[84:87], 0\n" \
"s_waitcnt vmcnt(2) lgkmcnt(0)\n" \
"s_barrier\n" \
"v_mfma_f32_16x16x32_bf16 a[0:3], v[116:119], v[148:151], a[0:3]\n" \
"s_add_u32 m0, s30, 0x0\n" \
"s_mov_b32 s85, 0x100\n" \
"v_mfma_f32_16x16x32_bf16 a[32:35], v[120:123], v[148:151], a[32:35]\n" \
"buffer_load_dwordx4 v22, s[64:67], s85 offen lds\n" \
"v_mfma_f32_16x16x32_bf16 a[4:7], v[116:119], v[152:155], a[4:7]\n" \
"ds_read_b64_tr_b16 v[84:85], v32 offset:32768\n" \
"s_mul_i32 s34, s36, 0x90000\n" \
"v_mfma_f32_16x16x32_bf16 a[36:39], v[120:123], v[152:155], a[36:39]\n" \
"ds_read_b64_tr_b16 v[86:87], v32 offset:33792\n" \
"s_lshl_b32 s36, s3, 14\n" \
"v_mfma_f32_16x16x32_bf16 a[8:11], v[116:119], v[156:159], a[8:11]\n" \
"s_add_u32 m0, s30, 0x400\n" \
"s_mov_b32 s84, 0x2100\n" \
"v_mfma_f32_16x16x32_bf16 a[40:43], v[120:123], v[156:159], a[40:43]\n" \
"buffer_load_dwordx4 v23, s[64:67], s84 offen lds\n" \
"v_mfma_f32_16x16x32_bf16 a[12:15], v[116:119], v[160:163], a[12:15]\n" \
"ds_read_b128 v[52:55], v28 offset:49152\n" \
"s_add_i32 s34, s34, s36\n" \
"v_mfma_f32_16x16x32_bf16 a[44:47], v[120:123], v[160:163], a[44:47]\n" \
"ds_read_b128 v[56:59], v28 offset:51200\n" \
"s_lshl_b32 s38, s5, 8\n" \
"v_mfma_f32_16x16x32_bf16 a[16:19], v[116:119], v[164:167], a[16:19]\n" \
"s_add_u32 m0, s31, 0x0\n" \
"s_mov_b32 s85, 0x100000\n" \
"v_mfma_f32_16x16x32_bf16 a[48:51], v[120:123], v[164:167], a[48:51]\n" \
"buffer_load_dwordx4 v26, s[68:71], s85 offen lds\n" \
"v_mfma_f32_16x16x32_bf16 a[20:23], v[116:119], v[168:171], a[20:23]\n" \
"ds_read_b64_tr_b16 v[88:89], v33 offset:32768\n" \
"s_add_i32 s41, s34, 0x4240000\n" \
"v_mfma_f32_16x16x32_bf16 a[52:55], v[120:123], v[168:171], a[52:55]\n" \
"ds_read_b64_tr_b16 v[90:91], v33 offset:33792\n" \
"s_mul_i32 s34, s5, 36\n" \
"v_mfma_f32_16x16x32_bf16 a[24:27], v[116:119], v[172:175], a[24:27]\n" \
"s_add_u32 m0, s31, 0x4000\n" \
"s_mov_b32 s84, 0x100000\n" \
"v_mfma_f32_16x16x32_bf16 a[56:59], v[120:123], v[172:175], a[56:59]\n" \
"buffer_load_dwordx4 v26, s[72:75], s84 offen lds\n" \
"v_mfma_f32_16x16x32_bf16 a[28:31], v[116:119], v[176:179], a[28:31]\n" \
"ds_read_b64_tr_b16 v[92:93], v34 offset:32768\n" \
"s_add_i32 s5, s7, s40\n" \
"v_mfma_f32_16x16x32_bf16 a[60:63], v[120:123], v[176:179], a[60:63]\n" \
"ds_read_b64_tr_b16 v[94:95], v34 offset:33792\n" \
"s_addk_i32 s5, 0x2000\n" \
"v_mfma_f32_16x16x32_bf16 a[92:95], v[124:127], v[176:179], a[92:95]\n" \
"s_add_u32 m0, s30, 0x800\n" \
"s_mov_b32 s85, 0x4100\n" \
"v_mfma_f32_16x16x32_bf16 a[124:127], v[128:131], v[176:179], a[124:127]\n" \
"buffer_load_dwordx4 v22, s[64:67], s85 offen lds\n" \
"v_mfma_f32_16x16x32_bf16 a[88:91], v[124:127], v[172:175], a[88:91]\n" \
"ds_read_b64_tr_b16 v[96:97], v35 offset:32768\n" \
"s_mov_b32 s37, 2.0\n" \
"s_nop 0\n" \
"v_mfma_f32_16x16x32_bf16 a[120:123], v[128:131], v[172:175], a[120:123]\n" \
"ds_read_b64_tr_b16 v[98:99], v35 offset:33792\n" \
"s_or_b32 s38, s38, 0x5be8000\n" \
"v_mfma_f32_16x16x32_bf16 a[84:87], v[124:127], v[168:171], a[84:87]\n" \
"s_add_u32 m0, s30, 0xc00\n" \
"s_mov_b32 s84, 0x6100\n" \
"v_mfma_f32_16x16x32_bf16 a[116:119], v[128:131], v[168:171], a[116:119]\n" \
"buffer_load_dwordx4 v23, s[64:67], s84 offen lds\n" \
"v_mfma_f32_16x16x32_bf16 a[80:83], v[124:127], v[164:167], a[80:83]\n" \
"ds_read_b64_tr_b16 v[100:101], v36 offset:32768\n" \
"v_mfma_f32_16x16x32_bf16 a[112:115], v[128:131], v[164:167], a[112:115]\n" \
"ds_read_b64_tr_b16 v[102:103], v36 offset:33792\n" \
"v_mfma_f32_16x16x32_bf16 a[76:79], v[124:127], v[160:163], a[76:79]\n" \
"s_add_u32 m0, s31, 0x400\n" \
"s_mov_b32 s85, 0x108000\n" \
"v_mfma_f32_16x16x32_bf16 a[108:111], v[128:131], v[160:163], a[108:111]\n" \
"buffer_load_dwordx4 v26, s[68:71], s85 offen lds\n" \
"v_mfma_f32_16x16x32_bf16 a[72:75], v[124:127], v[156:159], a[72:75]\n" \
"ds_read_b64_tr_b16 v[104:105], v37 offset:32768\n" \
"v_mfma_f32_16x16x32_bf16 a[104:107], v[128:131], v[156:159], a[104:107]\n" \
"s_cmp_lg_u32 s3, 0\n" \
"s_cselect_b32 s36, s37, s5\n" \
"v_mfma_f32_16x16x32_bf16 a[68:71], v[124:127], v[152:155], a[68:71]\n" \
"s_add_u32 m0, s31, 0x4400\n" \
"s_mov_b32 s84, 0x108000\n" \
"v_mfma_f32_16x16x32_bf16 a[100:103], v[128:131], v[152:155], a[100:103]\n" \
"buffer_load_dwordx4 v26, s[72:75], s84 offen lds\n" \
"v_mfma_f32_16x16x32_bf16 a[64:67], v[124:127], v[148:151], a[64:67]\n" \
"ds_read_b64_tr_b16 v[106:107], v37 offset:33792\n" \
"s_lshl_b32 s5, s6, 2\n" \
"v_mfma_f32_16x16x32_bf16 a[96:99], v[128:131], v[148:151], a[96:99]\n" \
"ds_read_b64_tr_b16 v[108:109], v38 offset:32768\n" \
"s_or_b32 s5, s38, s5\n" \
"v_mfma_f32_16x16x32_bf16 a[128:131], v[132:135], v[148:151], a[128:131]\n" \
"s_add_u32 m0, s30, 0x1000\n" \
"s_mov_b32 s85, 0x8100\n" \
"v_mfma_f32_16x16x32_bf16 a[160:163], v[136:139], v[148:151], a[160:163]\n" \
"buffer_load_dwordx4 v22, s[64:67], s85 offen lds\n" \
"v_mfma_f32_16x16x32_bf16 a[132:135], v[132:135], v[152:155], a[132:135]\n" \
"ds_read_b64_tr_b16 v[110:111], v38 offset:33792\n" \
"s_lshl_b32 s35, s3, 10\n" \
"s_nop 0\n" \
"v_mfma_f32_16x16x32_bf16 a[164:167], v[136:139], v[152:155], a[164:167]\n" \
"ds_read_b64_tr_b16 v[112:113], v39 offset:32768\n" \
"v_mfma_f32_16x16x32_bf16 a[136:139], v[132:135], v[156:159], a[136:139]\n" \
"s_add_u32 m0, s30, 0x1400\n" \
"s_mov_b32 s84, 0xa100\n" \
"v_mfma_f32_16x16x32_bf16 a[168:171], v[136:139], v[156:159], a[168:171]\n" \
"buffer_load_dwordx4 v23, s[64:67], s84 offen lds\n" \
"v_mfma_f32_16x16x32_bf16 a[140:143], v[132:135], v[160:163], a[140:143]\n" \
"ds_read_b64_tr_b16 v[114:115], v39 offset:33792\n" \
"v_mfma_f32_16x16x32_bf16 a[172:175], v[136:139], v[160:163], a[172:175]\n" \
"ds_read_b128 v[60:63], v28 offset:53248\n" \
"v_mfma_f32_16x16x32_bf16 a[144:147], v[132:135], v[164:167], a[144:147]\n" \
"s_add_u32 m0, s31, 0x800\n" \
"s_mov_b32 s85, 0x110000\n" \
"v_mfma_f32_16x16x32_bf16 a[176:179], v[136:139], v[164:167], a[176:179]\n" \
"buffer_load_dwordx4 v27, s[68:71], s85 offen lds\n" \
"v_mfma_f32_16x16x32_bf16 a[148:151], v[132:135], v[168:171], a[148:151]\n" \
"ds_read_b128 v[64:67], v28 offset:55296\n" \
"v_mfma_f32_16x16x32_bf16 a[180:183], v[136:139], v[168:171], a[180:183]\n" \
"ds_read_b128 v[68:71], v28 offset:32768\n" \
"v_mfma_f32_16x16x32_bf16 a[152:155], v[132:135], v[172:175], a[152:155]\n" \
"s_add_u32 m0, s31, 0x4800\n" \
"s_mov_b32 s84, 0x110000\n" \
"v_mfma_f32_16x16x32_bf16 a[184:187], v[136:139], v[172:175], a[184:187]\n" \
"buffer_load_dwordx4 v27, s[72:75], s84 offen lds\n" \
"v_mfma_f32_16x16x32_bf16 a[156:159], v[132:135], v[176:179], a[156:159]\n" \
"ds_read_b128 v[72:75], v28 offset:34816\n" \
"v_mfma_f32_16x16x32_bf16 a[188:191], v[136:139], v[176:179], a[188:191]\n" \
"ds_read_b128 v[76:79], v28 offset:36864\n" \
"v_mfma_f32_16x16x32_bf16 a[220:223], v[140:143], v[176:179], a[220:223]\n" \
"s_cmpk_lt_u32 s3, 3\n" \
"s_cselect_b32 s37, s37, s5\n" \
"v_mfma_f32_16x16x32_bf16 a[252:255], v[144:147], v[176:179], a[252:255]\n" \
"ds_read_b128 v[80:83], v28 offset:38912\n" \
"s_lshl_b32 s5, s4, 16\n" \
"v_mfma_f32_16x16x32_bf16 a[216:219], v[140:143], v[172:175], a[216:219]\n" \
"s_lshl_b32 s4, s4, 11\n" \
"v_cndmask_b32_e64 v198, v213, v212, vcc\n" \
"v_mfma_f32_16x16x32_bf16 a[248:251], v[144:147], v[172:175], a[248:251]\n" \
"v_or_b32_e32 v212, s35, v20\n" \
"s_or_b32 s39, s40, s35\n" \
"v_mfma_f32_16x16x32_bf16 a[212:215], v[140:143], v[168:171], a[212:215]\n" \
"s_or_b32 s55, s41, s4\n" \
"v_or_b32_e32 v199, 0x22000, v212\n" \
"v_mfma_f32_16x16x32_bf16 a[244:247], v[144:147], v[168:171], a[244:247]\n" \
"s_add_i32 s35, s39, s7\n" \
"s_add_i32 s54, s41, s5\n" \
"v_mfma_f32_16x16x32_bf16 a[208:211], v[140:143], v[164:167], a[208:211]\n" \
"s_add_i32 s55, s55, 0x80000\n" \
"v_mov_b32_e32 v220, 0xffff\n" \
"v_mfma_f32_16x16x32_bf16 a[240:243], v[144:147], v[164:167], a[240:243]\n" \
"s_and_b32 s4, s3, 1\n" \
"s_lshl_b32 s5, s4, 4\n" \
"v_mfma_f32_16x16x32_bf16 a[204:207], v[140:143], v[160:163], a[204:207]\n" \
"v_lshrrev_b32_e32 v212, 4, v20\n" \
"v_lshrrev_b32_e64 v213, 6, v20\n" \
"v_mfma_f32_16x16x32_bf16 a[236:239], v[144:147], v[160:163], a[236:239]\n" \
"v_and_b32_e64 v222, 12, v213\n" \
"v_and_or_b32 v221, v212, 15, s5\n" \
"v_mfma_f32_16x16x32_bf16 a[200:203], v[140:143], v[156:159], a[200:203]\n" \
"v_cmp_gt_u32_e64 vcc, v222, v221\n" \
"v_mov_b32_e32 v223, 0xffff0000\n" \
"v_mfma_f32_16x16x32_bf16 a[232:235], v[144:147], v[156:159], a[232:235]\n" \
"v_or_b32_e64 v217, 2, v222\n" \
"v_cndmask_b32_e64 v214, v220, 0, vcc\n" \
"v_mfma_f32_16x16x32_bf16 a[196:199], v[140:143], v[152:155], a[196:199]\n" \
"v_cmp_lt_u32_e32 vcc, v222, v221\n" \
"v_or_b32_e32 v219, 16, v222\n" \
"v_mfma_f32_16x16x32_bf16 a[228:231], v[144:147], v[152:155], a[228:231]\n" \
"v_or_b32_e32 v224, 18, v222\n" \
"v_cndmask_b32_e32 v215, 0, v223, vcc\n" \
"v_mfma_f32_16x16x32_bf16 a[192:195], v[140:143], v[148:151], a[192:195]\n" \
"v_cmp_gt_u32_e32 vcc, v217, v221\n" \
"s_lshl_b32 s6, s2, 2\n" \
"v_mfma_f32_16x16x32_bf16 a[224:227], v[144:147], v[148:151], a[224:227]\n" \
"s_waitcnt lgkmcnt(15)\n" \
"v_mfma_f32_16x16x32_bf16 a[0:3], v[52:55], v[84:87], a[0:3]\n" \
"s_add_u32 m0, s30, 0x1800\n" \
"s_mov_b32 s85, 0xc100\n" \
"v_mfma_f32_16x16x32_bf16 a[32:35], v[56:59], v[84:87], a[32:35]\n" \
"buffer_load_dwordx4 v22, s[64:67], s85 offen lds\n" \
"v_mfma_f32_16x16x32_bf16 a[4:7], v[52:55], v[88:91], a[4:7]\n" \
"ds_read_b64_tr_b16 v[148:149], v32 offset:40960\n" \
"s_or_b32 s40, s6, s3\n" \
"v_mfma_f32_16x16x32_bf16 a[36:39], v[56:59], v[88:91], a[36:39]\n" \
"s_add_u32 m0, s30, 0x1c00\n" \
"s_mov_b32 s84, 0xe100\n" \
"v_mfma_f32_16x16x32_bf16 a[8:11], v[52:55], v[92:95], a[8:11]\n" \
"buffer_load_dwordx4 v23, s[64:67], s84 offen lds\n" \
"v_mfma_f32_16x16x32_bf16 a[40:43], v[56:59], v[92:95], a[40:43]\n" \
"ds_read_b64_tr_b16 v[150:151], v32 offset:41984\n" \
"v_cndmask_b32_e64 v216, v220, 0, vcc\n" \
"s_waitcnt lgkmcnt(15)\n" \
"v_mfma_f32_16x16x32_bf16 a[12:15], v[52:55], v[96:99], a[12:15]\n" \
"s_add_u32 m0, s31, 0xc00\n" \
"s_mov_b32 s85, 0x118000\n" \
"v_mfma_f32_16x16x32_bf16 a[44:47], v[56:59], v[96:99], a[44:47]\n" \
"buffer_load_dwordx4 v27, s[68:71], s85 offen lds\n" \
"s_waitcnt lgkmcnt(14)\n" \
"v_mfma_f32_16x16x32_bf16 a[16:19], v[52:55], v[100:103], a[16:19]\n" \
"ds_read_b128 v[116:119], v29 offset:49152\n" \
"v_cmp_lt_u32_e64 vcc, v217, v221\n" \
"v_mfma_f32_16x16x32_bf16 a[48:51], v[56:59], v[100:103], a[48:51]\n" \
"s_add_u32 m0, s31, 0x4c00\n" \
"s_mov_b32 s84, 0x118000\n" \
"s_waitcnt lgkmcnt(13)\n" \
"v_mfma_f32_16x16x32_bf16 a[20:23], v[52:55], v[104:107], a[20:23]\n" \
"buffer_load_dwordx4 v27, s[72:75], s84 offen lds\n" \
"v_mfma_f32_16x16x32_bf16 a[52:55], v[56:59], v[104:107], a[52:55]\n" \
"ds_read_b128 v[120:123], v29 offset:51200\n" \
"s_add_i32 s41, s40, 0xffffffaf\n" \
"s_waitcnt lgkmcnt(12)\n" \
"v_mfma_f32_16x16x32_bf16 a[24:27], v[52:55], v[108:111], a[24:27]\n" \
"ds_read_b64_tr_b16 v[152:153], v33 offset:40960\n" \
"s_mov_b32 s5, 0\n" \
"v_mfma_f32_16x16x32_bf16 a[56:59], v[56:59], v[108:111], a[56:59]\n" \
"ds_read_b64_tr_b16 v[154:155], v33 offset:41984\n" \
"v_cndmask_b32_e32 v217, 0, v223, vcc\n" \
"s_waitcnt lgkmcnt(12)\n" \
"v_mfma_f32_16x16x32_bf16 a[28:31], v[52:55], v[112:115], a[28:31]\n" \
"ds_read_b64_tr_b16 v[156:157], v34 offset:40960\n" \
"v_cmp_gt_u32_e32 vcc, v219, v221\n" \
"v_mfma_f32_16x16x32_bf16 a[60:63], v[56:59], v[112:115], a[60:63]\n" \
"ds_read_b64_tr_b16 v[158:159], v34 offset:41984\n" \
"s_waitcnt lgkmcnt(13)\n" \
"v_mfma_f32_16x16x32_bf16 a[92:95], v[60:63], v[112:115], a[92:95]\n" \
"s_mov_b32 m0, s32\n" \
"s_waitcnt lgkmcnt(12)\n" \
"v_mfma_f32_16x16x32_bf16 a[124:127], v[64:67], v[112:115], a[124:127]\n" \
"s_mov_b32 exec_lo, 0x0\n" \
"s_mov_b32 exec_hi, 0xff\n" \
"global_load_lds_dword v[184:185], off\n" \
"s_mov_b64 exec, -1\n" \
"v_mfma_f32_16x16x32_bf16 a[88:91], v[60:63], v[108:111], a[88:91]\n" \
"ds_read_b64_tr_b16 v[160:161], v35 offset:40960\n" \
"v_mfma_f32_16x16x32_bf16 a[120:123], v[64:67], v[108:111], a[120:123]\n" \
"ds_read_b64_tr_b16 v[162:163], v35 offset:41984\n" \
"v_mfma_f32_16x16x32_bf16 a[84:87], v[60:63], v[104:107], a[84:87]\n" \
"s_cmpk_lt_u32 s40, 0x51\n" \
"s_cselect_b32 s7, s23, s25\n" \
"v_mfma_f32_16x16x32_bf16 a[116:119], v[64:67], v[104:107], a[116:119]\n" \
"s_mov_b32 m0, s32\n" \
"ds_read_b64_tr_b16 v[164:165], v36 offset:40960\n" \
"v_mfma_f32_16x16x32_bf16 a[80:83], v[60:63], v[100:103], a[80:83]\n" \
"s_mov_b32 exec_lo, 0xffff0000\n" \
"s_mov_b32 exec_hi, 0x0\n" \
"global_load_lds_dword v[186:187], off\n" \
"s_mov_b64 exec, -1\n" \
"v_mfma_f32_16x16x32_bf16 a[112:115], v[64:67], v[100:103], a[112:115]\n" \
"ds_read_b64_tr_b16 v[166:167], v36 offset:41984\n" \
"v_mov_b32_e64 v11, 0\n" \
"v_mfma_f32_16x16x32_bf16 a[76:79], v[60:63], v[96:99], a[76:79]\n" \
"s_cmpk_lt_u32 s40, 0x51\n" \
"s_cselect_b32 s6, s22, s24\n" \
"v_mfma_f32_16x16x32_bf16 a[108:111], v[64:67], v[96:99], a[108:111]\n" \
"s_lshl_b32 s44, s3, 1\n" \
"v_cndmask_b32_e64 v218, v220, 0, vcc\n" \
"v_mfma_f32_16x16x32_bf16 a[72:75], v[60:63], v[92:95], a[72:75]\n" \
"ds_read_b64_tr_b16 v[168:169], v37 offset:40960\n" \
"v_cmp_lt_u32_e32 vcc, v219, v221\n" \
"v_mfma_f32_16x16x32_bf16 a[104:107], v[64:67], v[92:95], a[104:107]\n" \
"s_cmpk_lt_u32 s40, 0x291\n" \
"s_cselect_b32 s41, s41, s5\n" \
"v_mfma_f32_16x16x32_bf16 a[68:71], v[60:63], v[88:91], a[68:71]\n" \
"ds_read_b64_tr_b16 v[170:171], v37 offset:41984\n" \
"s_bfe_u32 s43, s2, 0x20003\n" \
"v_mfma_f32_16x16x32_bf16 a[100:103], v[64:67], v[88:91], a[100:103]\n" \
"s_cmpk_lt_u32 s40, 0x51\n" \
"s_cselect_b32 s40, s40, s41\n" \
"v_mfma_f32_16x16x32_bf16 a[64:67], v[60:63], v[84:87], a[64:67]\n" \
"ds_read_b64_tr_b16 v[172:173], v38 offset:40960\n" \
"s_and_b32 s41, s44, 2\n" \
"v_mfma_f32_16x16x32_bf16 a[96:99], v[64:67], v[84:87], a[96:99]\n" \
"v_cndmask_b32_e32 v219, 0, v223, vcc\n" \
"v_cmp_gt_u32_e32 vcc, v224, v221\n" \
"s_waitcnt lgkmcnt(15)\n" \
"v_mfma_f32_16x16x32_bf16 a[128:131], v[68:71], v[84:87], a[128:131]\n" \
"ds_read_b64_tr_b16 v[174:175], v38 offset:41984\n" \
"s_and_b32 s44, s44, 4\n" \
"v_mfma_f32_16x16x32_bf16 a[160:163], v[72:75], v[84:87], a[160:163]\n" \
"s_lshr_b32 s49, s2, 3\n" \
"v_cndmask_b32_e64 v220, v220, 0, vcc\n" \
"v_mfma_f32_16x16x32_bf16 a[132:135], v[68:71], v[88:91], a[132:135]\n" \
"ds_read_b64_tr_b16 v[176:177], v39 offset:40960\n" \
"v_cmp_lt_u32_e64 vcc, v224, v221\n" \
"v_mfma_f32_16x16x32_bf16 a[164:167], v[72:75], v[88:91], a[164:167]\n" \
"s_min_u32 s50, s3, 2\n" \
"v_or_b32_e32 v12, v214, v215\n" \
"v_mfma_f32_16x16x32_bf16 a[136:139], v[68:71], v[92:95], a[136:139]\n" \
"ds_read_b64_tr_b16 v[178:179], v39 offset:41984\n" \
"v_cndmask_b32_e64 v221, 0, v223, vcc\n" \
"v_mfma_f32_16x16x32_bf16 a[168:171], v[72:75], v[92:95], a[168:171]\n" \
"v_lshlrev_b32_e64 v223, 7, v212\n" \
"v_lshl_or_b32 v10, s40, 13, v223\n" \
"v_mfma_f32_16x16x32_bf16 a[140:143], v[68:71], v[96:99], a[140:143]\n" \
"v_mul_u32_u24_e32 v223, 0xe39, v212\n" \
"v_lshl_add_u64 v[16:17], s[6:7], 0, v[10:11]\n" \
"v_mfma_f32_16x16x32_bf16 a[172:175], v[72:75], v[96:99], a[172:175]\n" \
"ds_read_b128 v[124:127], v29 offset:53248\n" \
"v_lshrrev_b32_e64 v223, 16, v223\n" \
"v_mfma_f32_16x16x32_bf16 a[144:147], v[68:71], v[100:103], a[144:147]\n" \
"s_movk_i32 s7, 0xffee\n" \
"s_lshl_b32 s6, s2, 5\n" \
"v_mfma_f32_16x16x32_bf16 a[176:179], v[72:75], v[100:103], a[176:179]\n" \
"ds_read_b128 v[128:131], v29 offset:55296\n" \
"v_mad_i32_i24 v224, v223, s7, v212\n" \
"v_mfma_f32_16x16x32_bf16 a[148:151], v[68:71], v[104:107], a[148:151]\n" \
"s_add_i32 s6, s6, 32\n" \
"v_lshlrev_b32_e32 v225, 4, v224\n" \
"v_mfma_f32_16x16x32_bf16 a[180:183], v[72:75], v[104:107], a[180:183]\n" \
"ds_read_b128 v[132:135], v29 offset:32768\n" \
"v_add_u32_e64 v226, s6, v225\n" \
"v_mfma_f32_16x16x32_bf16 a[152:155], v[68:71], v[108:111], a[152:155]\n" \
"v_and_b32_e32 v226, 0xf0, v226\n" \
"v_cmp_gt_u32_e64 vcc, 16, v224\n" \
"v_mfma_f32_16x16x32_bf16 a[184:187], v[72:75], v[108:111], a[184:187]\n" \
"ds_read_b128 v[136:139], v29 offset:34816\n" \
"s_movk_i32 s40, 0xf400\n" \
"v_mfma_f32_16x16x32_bf16 a[156:159], v[68:71], v[112:115], a[156:159]\n" \
"v_or_b32_e32 v13, v216, v217\n" \
"v_cndmask_b32_e64 v224, v225, v226, vcc\n" \
"v_mfma_f32_16x16x32_bf16 a[188:191], v[72:75], v[112:115], a[188:191]\n" \
"ds_read_b128 v[140:143], v29 offset:36864\n" \
"v_or_b32_e64 v225, 64, v212\n" \
"v_mfma_f32_16x16x32_bf16 a[220:223], v[76:79], v[112:115], a[220:223]\n" \
"v_mul_u32_u24_e32 v226, 0xe39, v225\n" \
"v_lshrrev_b32_e64 v226, 16, v226\n" \
"v_mfma_f32_16x16x32_bf16 a[252:255], v[80:83], v[112:115], a[252:255]\n" \
"ds_read_b128 v[144:147], v29 offset:38912\n" \
"v_mad_i32_i24 v225, v226, s7, v225\n" \
"v_mfma_f32_16x16x32_bf16 a[216:219], v[76:79], v[108:111], a[216:219]\n" \
"v_lshlrev_b32_e32 v227, 4, v225\n" \
"v_add_u32_e32 v228, s6, v227\n" \
"v_mfma_f32_16x16x32_bf16 a[248:251], v[80:83], v[108:111], a[248:251]\n" \
"v_and_b32_e32 v228, 0xf0, v228\n" \
"v_cmp_gt_u32_e64 vcc, 16, v225\n" \
"v_mfma_f32_16x16x32_bf16 a[212:215], v[76:79], v[104:107], a[212:215]\n" \
"v_mul_u32_u24_e32 v226, 0x480, v226\n" \
"v_or_b32_e64 v14, v218, v219\n" \
"v_mfma_f32_16x16x32_bf16 a[244:247], v[80:83], v[104:107], a[244:247]\n" \
"v_cndmask_b32_e64 v225, v227, v228, vcc\n" \
"v_or_b32_e32 v227, 0x80, v212\n" \
"v_mfma_f32_16x16x32_bf16 a[208:211], v[76:79], v[100:103], a[208:211]\n" \
"v_mul_u32_u24_e32 v228, 0xe39, v227\n" \
"v_lshrrev_b32_e64 v228, 16, v228\n" \
"v_mfma_f32_16x16x32_bf16 a[240:243], v[80:83], v[100:103], a[240:243]\n" \
"v_mad_i32_i24 v227, v228, s7, v227\n" \
"v_lshlrev_b32_e64 v229, 4, v227\n" \
"v_mfma_f32_16x16x32_bf16 a[204:207], v[76:79], v[96:99], a[204:207]\n" \
"v_add_u32_e64 v230, s6, v229\n" \
"v_and_b32_e32 v230, 0xf0, v230\n" \
"v_mfma_f32_16x16x32_bf16 a[236:239], v[80:83], v[96:99], a[236:239]\n" \
"v_cmp_gt_u32_e32 vcc, 16, v227\n" \
"v_or_b32_e32 v15, v220, v221\n" \
"v_mfma_f32_16x16x32_bf16 a[200:203], v[76:79], v[92:95], a[200:203]\n" \
"v_cndmask_b32_e64 v227, v229, v230, vcc\n" \
"v_or_b32_e32 v229, 0xc0, v212\n" \
"v_mfma_f32_16x16x32_bf16 a[232:235], v[80:83], v[92:95], a[232:235]\n" \
"v_mul_u32_u24_e32 v230, 0xe39, v229\n" \
"v_lshrrev_b32_e64 v230, 16, v230\n" \
"v_mfma_f32_16x16x32_bf16 a[196:199], v[76:79], v[88:91], a[196:199]\n" \
"v_mad_i32_i24 v229, v230, s7, v229\n" \
"v_lshlrev_b32_e64 v231, 4, v229\n" \
"v_mfma_f32_16x16x32_bf16 a[228:231], v[80:83], v[88:91], a[228:231]\n" \
"v_add_u32_e64 v232, s6, v231\n" \
"v_and_b32_e32 v232, 0xf0, v232\n" \
"v_mfma_f32_16x16x32_bf16 a[192:195], v[76:79], v[84:87], a[192:195]\n" \
"v_cmp_gt_u32_e32 vcc, 16, v229\n" \
"s_nop 1\n" \
"v_cndmask_b32_e64 v229, v231, v232, vcc\n" \
"v_mfma_f32_16x16x32_bf16 a[224:227], v[80:83], v[84:87], a[224:227]\n" \
"v_readfirstlane_b32 s62, v180\n" \
"s_nop 3\n" \
"s_add_u32 s62, s62, 0x9E3779B1\n" \
"s_waitcnt vmcnt(2) lgkmcnt(0)\n" \
"s_barrier\n" \
"v_mfma_f32_16x16x32_bf16 a[0:3], v[116:119], v[148:151], a[0:3]\n" \
"s_add_u32 m0, s30, 0x8000\n" \
"s_mov_b32 s85, 0x180\n" \
"v_mfma_f32_16x16x32_bf16 a[32:35], v[120:123], v[148:151], a[32:35]\n" \
"buffer_load_dwordx4 v22, s[64:67], s85 offen lds\n" \
"v_mfma_f32_16x16x32_bf16 a[4:7], v[116:119], v[152:155], a[4:7]\n" \
"ds_read_b64_tr_b16 v[84:85], v32 offset:0\n" \
"v_or_b32_e32 v231, 0x100, v212\n" \
"v_mfma_f32_16x16x32_bf16 a[36:39], v[120:123], v[152:155], a[36:39]\n" \
"ds_read_b64_tr_b16 v[86:87], v32 offset:1024\n" \
"v_mul_u32_u24_e32 v232, 0xe39, v231\n" \
"v_mfma_f32_16x16x32_bf16 a[8:11], v[116:119], v[156:159], a[8:11]\n" \
"s_add_u32 m0, s30, 0x8400\n" \
"s_mov_b32 s84, 0x2180\n" \
"v_mfma_f32_16x16x32_bf16 a[40:43], v[120:123], v[156:159], a[40:43]\n" \
"buffer_load_dwordx4 v23, s[64:67], s84 offen lds\n" \
"v_mfma_f32_16x16x32_bf16 a[12:15], v[116:119], v[160:163], a[12:15]\n" \
"ds_read_b128 v[52:55], v28 offset:16384\n" \
"v_lshrrev_b32_e64 v232, 16, v232\n" \
"v_mfma_f32_16x16x32_bf16 a[44:47], v[120:123], v[160:163], a[44:47]\n" \
"ds_read_b128 v[56:59], v28 offset:18432\n" \
"v_mad_i32_i24 v231, v232, s7, v231\n" \
"v_mfma_f32_16x16x32_bf16 a[16:19], v[116:119], v[164:167], a[16:19]\n" \
"s_add_u32 m0, s31, 0x8000\n" \
"s_mov_b32 s85, 0x180000\n" \
"v_mfma_f32_16x16x32_bf16 a[48:51], v[120:123], v[164:167], a[48:51]\n" \
"buffer_load_dwordx4 v26, s[68:71], s85 offen lds\n" \
"v_mfma_f32_16x16x32_bf16 a[20:23], v[116:119], v[168:171], a[20:23]\n" \
"ds_read_b64_tr_b16 v[88:89], v33 offset:0\n" \
"v_lshlrev_b32_e64 v233, 4, v231\n" \
"v_mfma_f32_16x16x32_bf16 a[52:55], v[120:123], v[168:171], a[52:55]\n" \
"ds_read_b64_tr_b16 v[90:91], v33 offset:1024\n" \
"v_add_u32_e64 v234, s6, v233\n" \
"v_mfma_f32_16x16x32_bf16 a[24:27], v[116:119], v[172:175], a[24:27]\n" \
"s_add_u32 m0, s31, 0xc000\n" \
"s_mov_b32 s84, 0x180000\n" \
"v_mfma_f32_16x16x32_bf16 a[56:59], v[120:123], v[172:175], a[56:59]\n" \
"buffer_load_dwordx4 v26, s[72:75], s84 offen lds\n" \
"v_mfma_f32_16x16x32_bf16 a[28:31], v[116:119], v[176:179], a[28:31]\n" \
"ds_read_b64_tr_b16 v[92:93], v34 offset:0\n" \
"v_and_b32_e32 v234, 0xf0, v234\n" \
"v_mfma_f32_16x16x32_bf16 a[60:63], v[120:123], v[176:179], a[60:63]\n" \
"ds_read_b64_tr_b16 v[94:95], v34 offset:1024\n" \
"v_cmp_gt_u32_e64 vcc, 16, v231\n" \
"v_mfma_f32_16x16x32_bf16 a[92:95], v[124:127], v[176:179], a[92:95]\n" \
"s_add_u32 m0, s30, 0x8800\n" \
"s_mov_b32 s85, 0x4180\n" \
"v_mfma_f32_16x16x32_bf16 a[124:127], v[128:131], v[176:179], a[124:127]\n" \
"buffer_load_dwordx4 v22, s[64:67], s85 offen lds\n" \
"v_mfma_f32_16x16x32_bf16 a[88:91], v[124:127], v[172:175], a[88:91]\n" \
"ds_read_b64_tr_b16 v[96:97], v35 offset:0\n" \
"v_cndmask_b32_e64 v231, v233, v234, vcc\n" \
"v_mfma_f32_16x16x32_bf16 a[120:123], v[128:131], v[172:175], a[120:123]\n" \
"ds_read_b64_tr_b16 v[98:99], v35 offset:1024\n" \
"v_or_b32_e32 v233, 0x140, v212\n" \
"v_mfma_f32_16x16x32_bf16 a[84:87], v[124:127], v[168:171], a[84:87]\n" \
"s_add_u32 m0, s30, 0x8c00\n" \
"s_mov_b32 s84, 0x6180\n" \
"v_mfma_f32_16x16x32_bf16 a[116:119], v[128:131], v[168:171], a[116:119]\n" \
"buffer_load_dwordx4 v23, s[64:67], s84 offen lds\n" \
"v_mfma_f32_16x16x32_bf16 a[80:83], v[124:127], v[164:167], a[80:83]\n" \
"ds_read_b64_tr_b16 v[100:101], v36 offset:0\n" \
"v_mul_u32_u24_e32 v234, 0xe39, v233\n" \
"v_mfma_f32_16x16x32_bf16 a[112:115], v[128:131], v[164:167], a[112:115]\n" \
"ds_read_b64_tr_b16 v[102:103], v36 offset:1024\n" \
"v_lshrrev_b32_e64 v234, 16, v234\n" \
"v_mfma_f32_16x16x32_bf16 a[76:79], v[124:127], v[160:163], a[76:79]\n" \
"s_add_u32 m0, s31, 0x8400\n" \
"s_mov_b32 s85, 0x188000\n" \
"v_mfma_f32_16x16x32_bf16 a[108:111], v[128:131], v[160:163], a[108:111]\n" \
"buffer_load_dwordx4 v26, s[68:71], s85 offen lds\n" \
"v_mfma_f32_16x16x32_bf16 a[72:75], v[124:127], v[156:159], a[72:75]\n" \
"ds_read_b64_tr_b16 v[104:105], v37 offset:0\n" \
"v_mad_i32_i24 v233, v234, s7, v233\n" \
"v_mfma_f32_16x16x32_bf16 a[104:107], v[128:131], v[156:159], a[104:107]\n" \
"v_lshlrev_b32_e32 v235, 4, v233\n" \
"v_add_u32_e32 v236, s6, v235\n" \
"v_mfma_f32_16x16x32_bf16 a[68:71], v[124:127], v[152:155], a[68:71]\n" \
"s_add_u32 m0, s31, 0xc400\n" \
"s_mov_b32 s84, 0x188000\n" \
"v_mfma_f32_16x16x32_bf16 a[100:103], v[128:131], v[152:155], a[100:103]\n" \
"buffer_load_dwordx4 v26, s[72:75], s84 offen lds\n" \
"v_mfma_f32_16x16x32_bf16 a[64:67], v[124:127], v[148:151], a[64:67]\n" \
"ds_read_b64_tr_b16 v[106:107], v37 offset:1024\n" \
"v_and_b32_e32 v236, 0xf0, v236\n" \
"v_mfma_f32_16x16x32_bf16 a[96:99], v[128:131], v[148:151], a[96:99]\n" \
"ds_read_b64_tr_b16 v[108:109], v38 offset:0\n" \
"v_cmp_gt_u32_e64 vcc, 16, v233\n" \
"v_mfma_f32_16x16x32_bf16 a[128:131], v[132:135], v[148:151], a[128:131]\n" \
"s_add_u32 m0, s30, 0x9000\n" \
"s_mov_b32 s85, 0x8180\n" \
"v_mfma_f32_16x16x32_bf16 a[160:163], v[136:139], v[148:151], a[160:163]\n" \
"buffer_load_dwordx4 v22, s[64:67], s85 offen lds\n" \
"v_mfma_f32_16x16x32_bf16 a[132:135], v[132:135], v[152:155], a[132:135]\n" \
"ds_read_b64_tr_b16 v[110:111], v38 offset:1024\n" \
"v_cndmask_b32_e64 v233, v235, v236, vcc\n" \
"v_mfma_f32_16x16x32_bf16 a[164:167], v[136:139], v[152:155], a[164:167]\n" \
"ds_read_b64_tr_b16 v[112:113], v39 offset:0\n" \
"v_or_b32_e32 v235, 0x180, v212\n" \
"v_mfma_f32_16x16x32_bf16 a[136:139], v[132:135], v[156:159], a[136:139]\n" \
"s_add_u32 m0, s30, 0x9400\n" \
"s_mov_b32 s84, 0xa180\n" \
"v_mfma_f32_16x16x32_bf16 a[168:171], v[136:139], v[156:159], a[168:171]\n" \
"buffer_load_dwordx4 v23, s[64:67], s84 offen lds\n" \
"v_mfma_f32_16x16x32_bf16 a[140:143], v[132:135], v[160:163], a[140:143]\n" \
"ds_read_b64_tr_b16 v[114:115], v39 offset:1024\n" \
"v_mul_u32_u24_e32 v236, 0xe39, v235\n" \
"v_mfma_f32_16x16x32_bf16 a[172:175], v[136:139], v[160:163], a[172:175]\n" \
"ds_read_b128 v[60:63], v28 offset:20480\n" \
"v_lshrrev_b32_e64 v236, 16, v236\n" \
"v_mfma_f32_16x16x32_bf16 a[144:147], v[132:135], v[164:167], a[144:147]\n" \
"s_add_u32 m0, s31, 0x8800\n" \
"s_mov_b32 s85, 0x190000\n" \
"v_mfma_f32_16x16x32_bf16 a[176:179], v[136:139], v[164:167], a[176:179]\n" \
"buffer_load_dwordx4 v27, s[68:71], s85 offen lds\n" \
"v_mfma_f32_16x16x32_bf16 a[148:151], v[132:135], v[168:171], a[148:151]\n" \
"ds_read_b128 v[64:67], v28 offset:22528\n" \
"v_mad_i32_i24 v235, v236, s7, v235\n" \
"v_mfma_f32_16x16x32_bf16 a[180:183], v[136:139], v[168:171], a[180:183]\n" \
"ds_read_b128 v[68:71], v28 offset:0\n" \
"v_lshlrev_b32_e64 v237, 4, v235\n" \
"v_mfma_f32_16x16x32_bf16 a[152:155], v[132:135], v[172:175], a[152:155]\n" \
"s_add_u32 m0, s31, 0xc800\n" \
"s_mov_b32 s84, 0x190000\n" \
"v_mfma_f32_16x16x32_bf16 a[184:187], v[136:139], v[172:175], a[184:187]\n" \
"buffer_load_dwordx4 v27, s[72:75], s84 offen lds\n" \
"v_mfma_f32_16x16x32_bf16 a[156:159], v[132:135], v[176:179], a[156:159]\n" \
"ds_read_b128 v[72:75], v28 offset:2048\n" \
"v_add_u32_e64 v238, s6, v237\n" \
"v_mfma_f32_16x16x32_bf16 a[188:191], v[136:139], v[176:179], a[188:191]\n" \
"ds_read_b128 v[76:79], v28 offset:4096\n" \
"v_and_b32_e32 v238, 0xf0, v238\n" \
"v_mfma_f32_16x16x32_bf16 a[220:223], v[140:143], v[176:179], a[220:223]\n" \
"v_cmp_gt_u32_e32 vcc, 16, v235\n" \
"s_nop 1\n" \
"v_cndmask_b32_e64 v235, v237, v238, vcc\n" \
"v_mfma_f32_16x16x32_bf16 a[252:255], v[144:147], v[176:179], a[252:255]\n" \
"ds_read_b128 v[80:83], v28 offset:6144\n" \
"v_or_b32_e32 v237, 0x1c0, v212\n" \
"v_mfma_f32_16x16x32_bf16 a[216:219], v[140:143], v[172:175], a[216:219]\n" \
"v_mul_u32_u24_e32 v238, 0xe39, v237\n" \
"v_lshrrev_b32_e64 v238, 16, v238\n" \
"v_mfma_f32_16x16x32_bf16 a[248:251], v[144:147], v[172:175], a[248:251]\n" \
"v_mad_i32_i24 v237, v238, s7, v237\n" \
"v_lshlrev_b32_e64 v239, 4, v237\n" \
"v_mfma_f32_16x16x32_bf16 a[212:215], v[140:143], v[168:171], a[212:215]\n" \
"v_add_u32_e64 v240, s6, v239\n" \
"v_and_b32_e32 v240, 0xf0, v240\n" \
"v_mfma_f32_16x16x32_bf16 a[244:247], v[144:147], v[168:171], a[244:247]\n" \
"v_cmp_gt_u32_e32 vcc, 16, v237\n" \
"s_nop 1\n" \
"v_cndmask_b32_e64 v237, v239, v240, vcc\n" \
"v_mfma_f32_16x16x32_bf16 a[208:211], v[140:143], v[164:167], a[208:211]\n" \
"v_or_b32_e32 v239, 0x200, v212\n" \
"v_mul_u32_u24_e32 v240, 0xe39, v239\n" \
"v_mfma_f32_16x16x32_bf16 a[240:243], v[144:147], v[164:167], a[240:243]\n" \
"v_lshrrev_b32_e64 v240, 16, v240\n" \
"v_mad_i32_i24 v239, v240, s7, v239\n" \
"v_mfma_f32_16x16x32_bf16 a[204:207], v[140:143], v[160:163], a[204:207]\n" \
"v_lshlrev_b32_e32 v241, 4, v239\n" \
"v_add_u32_e32 v242, s6, v241\n" \
"v_mfma_f32_16x16x32_bf16 a[236:239], v[144:147], v[160:163], a[236:239]\n" \
"s_movk_i32 s6, 0xfc00\n" \
"v_add3_u32 v1, v226, v225, s6\n" \
"v_mfma_f32_16x16x32_bf16 a[200:203], v[140:143], v[156:159], a[200:203]\n" \
"v_mul_u32_u24_e32 v225, 0x480, v228\n" \
"s_movk_i32 s7, 0xf800\n" \
"v_mfma_f32_16x16x32_bf16 a[232:235], v[144:147], v[156:159], a[232:235]\n" \
"v_add3_u32 v2, v225, v227, s7\n" \
"v_mul_u32_u24_e32 v225, 0x480, v230\n" \
"v_mfma_f32_16x16x32_bf16 a[196:199], v[140:143], v[152:155], a[196:199]\n" \
"v_add3_u32 v3, v225, v229, s40\n" \
"v_mul_u32_u24_e32 v225, 0x480, v234\n" \
"v_mfma_f32_16x16x32_bf16 a[228:231], v[144:147], v[152:155], a[228:231]\n" \
"v_add3_u32 v5, v225, v233, s6\n" \
"v_mul_u32_u24_e32 v225, 0x480, v236\n" \
"v_mfma_f32_16x16x32_bf16 a[192:195], v[140:143], v[148:151], a[192:195]\n" \
"v_add3_u32 v6, v225, v235, s7\n" \
"v_mul_u32_u24_e32 v225, 0x480, v238\n" \
"v_mfma_f32_16x16x32_bf16 a[224:227], v[144:147], v[148:151], a[224:227]\n" \
"s_waitcnt lgkmcnt(15)\n" \
"s_nop 0\n" \
"v_mfma_f32_16x16x32_bf16 a[0:3], v[52:55], v[84:87], a[0:3]\n" \
"s_add_u32 m0, s30, 0x9800\n" \
"s_mov_b32 s85, 0xc180\n" \
"v_mfma_f32_16x16x32_bf16 a[32:35], v[56:59], v[84:87], a[32:35]\n" \
"buffer_load_dwordx4 v22, s[64:67], s85 offen lds\n" \
"v_mfma_f32_16x16x32_bf16 a[4:7], v[52:55], v[88:91], a[4:7]\n" \
"ds_read_b64_tr_b16 v[148:149], v32 offset:8192\n" \
"v_add3_u32 v7, v225, v237, s40\n" \
"v_mfma_f32_16x16x32_bf16 a[36:39], v[56:59], v[88:91], a[36:39]\n" \
"s_add_u32 m0, s30, 0x9c00\n" \
"s_mov_b32 s84, 0xe180\n" \
"v_mfma_f32_16x16x32_bf16 a[8:11], v[52:55], v[92:95], a[8:11]\n" \
"buffer_load_dwordx4 v23, s[64:67], s84 offen lds\n" \
"v_mfma_f32_16x16x32_bf16 a[40:43], v[56:59], v[92:95], a[40:43]\n" \
"ds_read_b64_tr_b16 v[150:151], v32 offset:9216\n" \
"s_and_b32 s40, s2, 7\n" \
"s_waitcnt lgkmcnt(15)\n" \
"v_mfma_f32_16x16x32_bf16 a[12:15], v[52:55], v[96:99], a[12:15]\n" \
"s_add_u32 m0, s31, 0x8c00\n" \
"s_mov_b32 s85, 0x198000\n" \
"v_mfma_f32_16x16x32_bf16 a[44:47], v[56:59], v[96:99], a[44:47]\n" \
"buffer_load_dwordx4 v27, s[68:71], s85 offen lds\n" \
"s_waitcnt lgkmcnt(14)\n" \
"v_mfma_f32_16x16x32_bf16 a[16:19], v[52:55], v[100:103], a[16:19]\n" \
"ds_read_b128 v[116:119], v29 offset:16384\n" \
"v_and_b32_e32 v242, 0xf0, v242\n" \
"v_mfma_f32_16x16x32_bf16 a[48:51], v[56:59], v[100:103], a[48:51]\n" \
"s_add_u32 m0, s31, 0xcc00\n" \
"s_mov_b32 s84, 0x198000\n" \
"s_waitcnt lgkmcnt(13)\n" \
"v_mfma_f32_16x16x32_bf16 a[20:23], v[52:55], v[104:107], a[20:23]\n" \
"buffer_load_dwordx4 v27, s[72:75], s84 offen lds\n" \
"v_mfma_f32_16x16x32_bf16 a[52:55], v[56:59], v[104:107], a[52:55]\n" \
"ds_read_b128 v[120:123], v29 offset:18432\n" \
"v_cmp_gt_u32_e32 vcc, 16, v239\n" \
"s_waitcnt lgkmcnt(12)\n" \
"v_mfma_f32_16x16x32_bf16 a[24:27], v[52:55], v[108:111], a[24:27]\n" \
"ds_read_b64_tr_b16 v[152:153], v33 offset:8192\n" \
"s_lshl_b32 s6, s3, 3\n" \
"v_mfma_f32_16x16x32_bf16 a[56:59], v[56:59], v[108:111], a[56:59]\n" \
"ds_read_b64_tr_b16 v[154:155], v33 offset:9216\n" \
"v_and_b32_e32 v225, 32, v20\n" \
"s_waitcnt lgkmcnt(12)\n" \
"v_mfma_f32_16x16x32_bf16 a[28:31], v[52:55], v[112:115], a[28:31]\n" \
"ds_read_b64_tr_b16 v[156:157], v34 offset:8192\n" \
"s_lshl_b32 s7, s40, 5\n" \
"v_mfma_f32_16x16x32_bf16 a[60:63], v[56:59], v[112:115], a[60:63]\n" \
"ds_read_b64_tr_b16 v[158:159], v34 offset:9216\n" \
"v_cndmask_b32_e32 v239, v241, v242, vcc\n" \
"s_waitcnt lgkmcnt(13)\n" \
"v_mfma_f32_16x16x32_bf16 a[92:95], v[60:63], v[112:115], a[92:95]\n" \
"s_and_b32 s6, s6, 16\n" \
"v_mov_b32_e32 v226, 0x100\n" \
"s_waitcnt lgkmcnt(12)\n" \
"v_mfma_f32_16x16x32_bf16 a[124:127], v[64:67], v[112:115], a[124:127]\n" \
"buffer_load_dwordx4 v[200:203], v192, s[76:79], s34 offen offset:0\n" \
"v_mfma_f32_16x16x32_bf16 a[88:91], v[60:63], v[108:111], a[88:91]\n" \
"s_mov_b32 m0, s32\n" \
"ds_read_b64_tr_b16 v[160:161], v35 offset:8192\n" \
"v_mfma_f32_16x16x32_bf16 a[120:123], v[64:67], v[108:111], a[120:123]\n" \
"s_mov_b32 exec_lo, 0x0\n" \
"s_mov_b32 exec_hi, 0xff00\n" \
"global_load_lds_dword v[184:185], off\n" \
"s_mov_b64 exec, -1\n" \
"v_mfma_f32_16x16x32_bf16 a[84:87], v[60:63], v[104:107], a[84:87]\n" \
"ds_read_b64_tr_b16 v[162:163], v35 offset:9216\n" \
"v_mov_b32_e32 v227, s7\n" \
"v_mfma_f32_16x16x32_bf16 a[116:119], v[64:67], v[104:107], a[116:119]\n" \
"buffer_load_dwordx4 v[204:207], v193, s[76:79], s34 offen offset:0\n" \
"v_mfma_f32_16x16x32_bf16 a[80:83], v[60:63], v[100:103], a[80:83]\n" \
"ds_read_b64_tr_b16 v[164:165], v36 offset:8192\n" \
"v_cmp_eq_u32_e64 vcc, 0, v225\n" \
"v_mfma_f32_16x16x32_bf16 a[112:115], v[64:67], v[100:103], a[112:115]\n" \
"ds_read_b64_tr_b16 v[166:167], v36 offset:9216\n" \
"s_or_b32 s41, s41, s6\n" \
"v_mfma_f32_16x16x32_bf16 a[76:79], v[60:63], v[96:99], a[76:79]\n" \
"s_lshr_b32 s6, s2, 5\n" \
"v_cndmask_b32_e64 v225, v226, v227, vcc\n" \
"v_mfma_f32_16x16x32_bf16 a[108:111], v[64:67], v[96:99], a[108:111]\n" \
"v_bfe_u32 v226, v20, 7, 1\n" \
"v_or3_b32 v222, s41, v226, v222\n" \
"v_mfma_f32_16x16x32_bf16 a[72:75], v[60:63], v[92:95], a[72:75]\n" \
"buffer_load_dword v208, v194, s[76:79], s34 offen offset:32\n" \
"v_mfma_f32_16x16x32_bf16 a[104:107], v[64:67], v[92:95], a[104:107]\n" \
"ds_read_b64_tr_b16 v[168:169], v37 offset:8192\n" \
"v_mul_u32_u24_e64 v222, 18, v222\n" \
"v_mfma_f32_16x16x32_bf16 a[68:71], v[60:63], v[88:91], a[68:71]\n" \
"ds_read_b64_tr_b16 v[170:171], v37 offset:9216\n" \
"s_movk_i32 s41, 0x480\n" \
"v_mfma_f32_16x16x32_bf16 a[100:103], v[64:67], v[88:91], a[100:103]\n" \
"v_and_or_b32 v222, v213, 1, v222\n" \
"v_mad_u32_u24 v0, v223, s41, v224\n" \
"v_mfma_f32_16x16x32_bf16 a[64:67], v[60:63], v[84:87], a[64:67]\n" \
"buffer_load_dword v209, v194, s[76:79], s34 offen offset:1184\n" \
"v_mfma_f32_16x16x32_bf16 a[96:99], v[64:67], v[84:87], a[96:99]\n" \
"ds_read_b64_tr_b16 v[172:173], v38 offset:8192\n" \
"v_mad_u32_u24 v4, v232, s41, v231\n" \
"s_waitcnt lgkmcnt(15)\n" \
"v_mfma_f32_16x16x32_bf16 a[128:131], v[68:71], v[84:87], a[128:131]\n" \
"ds_read_b64_tr_b16 v[174:175], v38 offset:9216\n" \
"v_mad_u32_u24 v8, v240, s41, v239\n" \
"v_mfma_f32_16x16x32_bf16 a[160:163], v[72:75], v[84:87], a[160:163]\n" \
"v_mad_u32_u24 v222, v222, s41, v225\n" \
"s_mul_i32 s41, s6, 0xa00\n" \
"v_mfma_f32_16x16x32_bf16 a[132:135], v[68:71], v[88:91], a[132:135]\n" \
"buffer_load_dword v211, v194, s[76:79], s34 offen offset:2336\n" \
"v_mfma_f32_16x16x32_bf16 a[164:167], v[72:75], v[88:91], a[164:167]\n" \
"ds_read_b64_tr_b16 v[176:177], v39 offset:8192\n" \
"s_add_i32 s42, s41, 0x5bc1000\n" \
"v_mfma_f32_16x16x32_bf16 a[136:139], v[68:71], v[92:95], a[136:139]\n" \
"ds_read_b64_tr_b16 v[178:179], v39 offset:9216\n" \
"s_lshl_b32 s41, s40, 2\n" \
"v_mfma_f32_16x16x32_bf16 a[168:171], v[72:75], v[92:95], a[168:171]\n" \
"s_or_b32 s46, s41, s43\n" \
"s_lshl_b32 s45, s46, 2\n" \
"v_mfma_f32_16x16x32_bf16 a[140:143], v[68:71], v[96:99], a[140:143]\n" \
"s_or_b32 s45, s45, s42\n" \
"v_writelane_b32 v11, s45, 0\n" \
"v_mfma_f32_16x16x32_bf16 a[172:175], v[72:75], v[96:99], a[172:175]\n" \
"s_mov_b32 s76, s20\n" \
"s_and_b32 s77, s21, 0xffff\n" \
"v_mfma_f32_16x16x32_bf16 a[144:147], v[68:71], v[100:103], a[144:147]\n" \
"s_mov_b32 s78, 0x480\n" \
"ds_read_b128 v[124:127], v29 offset:20480\n" \
"v_mfma_f32_16x16x32_bf16 a[176:179], v[72:75], v[100:103], a[176:179]\n" \
"buffer_load_ushort v210, v195, s[76:79], 0 offen\n" \
"v_mfma_f32_16x16x32_bf16 a[148:151], v[68:71], v[104:107], a[148:151]\n" \
"ds_read_b128 v[128:131], v29 offset:22528\n" \
"s_lshl_b32 s47, s6, 3\n" \
"v_mfma_f32_16x16x32_bf16 a[180:183], v[72:75], v[104:107], a[180:183]\n" \
"s_mov_b32 s76, s8\n" \
"s_and_b32 s77, s9, 0xffff\n" \
"v_mfma_f32_16x16x32_bf16 a[152:155], v[68:71], v[108:111], a[152:155]\n" \
"s_mov_b32 s78, 0x240000\n" \
"ds_read_b128 v[132:135], v29 offset:0\n" \
"v_mfma_f32_16x16x32_bf16 a[184:187], v[72:75], v[108:111], a[184:187]\n" \
"ds_read_b128 v[136:139], v29 offset:2048\n" \
"v_writelane_b32 v11, s42, 1\n" \
"v_mfma_f32_16x16x32_bf16 a[156:159], v[68:71], v[112:115], a[156:159]\n" \
"s_or_b32 s42, s47, s40\n" \
"s_lshl_b32 s42, s42, 10\n" \
"v_mfma_f32_16x16x32_bf16 a[188:191], v[72:75], v[112:115], a[188:191]\n" \
"ds_read_b128 v[140:143], v29 offset:4096\n" \
"s_lshl_b32 s45, s3, 8\n" \
"v_mfma_f32_16x16x32_bf16 a[220:223], v[76:79], v[112:115], a[220:223]\n" \
"s_or_b32 s42, s42, s45\n" \
"s_or_b32 s45, s42, 0x5c00200\n" \
"v_mfma_f32_16x16x32_bf16 a[252:255], v[80:83], v[112:115], a[252:255]\n" \
"ds_read_b128 v[144:147], v29 offset:6144\n" \
"v_writelane_b32 v11, s45, 2\n" \
"v_mfma_f32_16x16x32_bf16 a[216:219], v[76:79], v[108:111], a[216:219]\n" \
"s_lshl_b32 s45, s43, 2\n" \
"s_or_b32 s48, s45, s42\n" \
"v_mfma_f32_16x16x32_bf16 a[248:251], v[80:83], v[108:111], a[248:251]\n" \
"s_or_b32 s48, s48, 0x5c00000\n" \
"v_writelane_b32 v11, s48, 3\n" \
"v_mfma_f32_16x16x32_bf16 a[212:215], v[76:79], v[104:107], a[212:215]\n" \
"s_or_b32 s42, s42, 0x5c00080\n" \
"v_writelane_b32 v11, s42, 4\n" \
"v_mfma_f32_16x16x32_bf16 a[244:247], v[80:83], v[104:107], a[244:247]\n" \
"s_or_b32 s42, s42, s45\n" \
"s_add_i32 s45, s3, s2\n" \
"v_mfma_f32_16x16x32_bf16 a[208:211], v[76:79], v[100:103], a[208:211]\n" \
"v_writelane_b32 v11, s42, 5\n" \
"s_mov_b32 s42, 2.0\n" \
"v_mfma_f32_16x16x32_bf16 a[240:243], v[80:83], v[100:103], a[240:243]\n" \
"s_mov_b32 s48, 0x5bc0000\n" \
"v_mfma_f32_16x16x32_bf16 a[204:207], v[76:79], v[96:99], a[204:207]\n" \
"s_cmp_lg_u32 s45, 0\n" \
"s_cselect_b32 s45, s42, s48\n" \
"v_mfma_f32_16x16x32_bf16 a[236:239], v[80:83], v[96:99], a[236:239]\n" \
"s_or_b32 s48, s44, s4\n" \
"v_writelane_b32 v11, s45, 53\n" \
"v_mfma_f32_16x16x32_bf16 a[200:203], v[76:79], v[92:95], a[200:203]\n" \
"s_mul_i32 s45, s43, 0x120\n" \
"v_mfma_f32_16x16x32_bf16 a[232:235], v[80:83], v[92:95], a[232:235]\n" \
"s_add_u32 s44, s22, s45\n" \
"s_addc_u32 s51, s23, 0\n" \
"v_mfma_f32_16x16x32_bf16 a[196:199], v[76:79], v[88:91], a[196:199]\n" \
"v_writelane_b32 v11, s44, 9\n" \
"v_mfma_f32_16x16x32_bf16 a[228:231], v[80:83], v[88:91], a[228:231]\n" \
"s_add_u32 s52, s24, s45\n" \
"s_addc_u32 s53, s25, 0\n" \
"v_mfma_f32_16x16x32_bf16 a[192:195], v[76:79], v[84:87], a[192:195]\n" \
"s_and_b32 s56, s51, 0xffff\n" \
"v_writelane_b32 v11, s56, 10\n" \
"v_mfma_f32_16x16x32_bf16 a[224:227], v[80:83], v[84:87], a[224:227]\n" \
"s_waitcnt vmcnt(7) lgkmcnt(0)\n" \
"s_barrier\n" \
"v_mfma_f32_16x16x32_bf16 a[0:3], v[116:119], v[148:151], a[0:3]\n" \
"s_add_u32 m0, s30, 0x0\n" \
"s_mov_b32 s85, 0x200\n" \
"v_mfma_f32_16x16x32_bf16 a[32:35], v[120:123], v[148:151], a[32:35]\n" \
"buffer_load_dwordx4 v22, s[64:67], s85 offen lds\n" \
"v_mfma_f32_16x16x32_bf16 a[4:7], v[116:119], v[152:155], a[4:7]\n" \
"ds_read_b64_tr_b16 v[84:85], v32 offset:32768\n" \
"s_and_b32 s56, s53, 0xffff\n" \
"v_mfma_f32_16x16x32_bf16 a[36:39], v[120:123], v[152:155], a[36:39]\n" \
"ds_read_b64_tr_b16 v[86:87], v32 offset:33792\n" \
"v_writelane_b32 v11, s52, 11\n" \
"v_mfma_f32_16x16x32_bf16 a[8:11], v[116:119], v[156:159], a[8:11]\n" \
"s_add_u32 m0, s30, 0x400\n" \
"s_mov_b32 s84, 0x2200\n" \
"v_mfma_f32_16x16x32_bf16 a[40:43], v[120:123], v[156:159], a[40:43]\n" \
"buffer_load_dwordx4 v23, s[64:67], s84 offen lds\n" \
"v_mfma_f32_16x16x32_bf16 a[12:15], v[116:119], v[160:163], a[12:15]\n" \
"ds_read_b128 v[52:55], v28 offset:49152\n" \
"v_mfma_f32_16x16x32_bf16 a[44:47], v[120:123], v[160:163], a[44:47]\n" \
"ds_read_b128 v[56:59], v28 offset:51200\n" \
"v_mfma_f32_16x16x32_bf16 a[16:19], v[116:119], v[164:167], a[16:19]\n" \
"s_add_u32 m0, s31, 0x0\n" \
"s_nop 0\n" \
"s_mov_b32 s85, 0x200000\n" \
"v_mfma_f32_16x16x32_bf16 a[48:51], v[120:123], v[164:167], a[48:51]\n" \
"buffer_load_dwordx4 v26, s[68:71], s85 offen lds\n" \
"v_mfma_f32_16x16x32_bf16 a[20:23], v[116:119], v[168:171], a[20:23]\n" \
"ds_read_b64_tr_b16 v[88:89], v33 offset:32768\n" \
"v_mfma_f32_16x16x32_bf16 a[52:55], v[120:123], v[168:171], a[52:55]\n" \
"ds_read_b64_tr_b16 v[90:91], v33 offset:33792\n" \
"v_mfma_f32_16x16x32_bf16 a[24:27], v[116:119], v[172:175], a[24:27]\n" \
"s_add_u32 m0, s31, 0x4000\n" \
"s_mov_b32 s84, 0x200000\n" \
"v_mfma_f32_16x16x32_bf16 a[56:59], v[120:123], v[172:175], a[56:59]\n" \
"buffer_load_dwordx4 v26, s[72:75], s84 offen lds\n" \
"v_mfma_f32_16x16x32_bf16 a[28:31], v[116:119], v[176:179], a[28:31]\n" \
"ds_read_b64_tr_b16 v[92:93], v34 offset:32768\n" \
"v_mfma_f32_16x16x32_bf16 a[60:63], v[120:123], v[176:179], a[60:63]\n" \
"ds_read_b64_tr_b16 v[94:95], v34 offset:33792\n" \
"v_mfma_f32_16x16x32_bf16 a[92:95], v[124:127], v[176:179], a[92:95]\n" \
"s_add_u32 m0, s30, 0x800\n" \
"s_mov_b32 s85, 0x4200\n" \
"v_mfma_f32_16x16x32_bf16 a[124:127], v[128:131], v[176:179], a[124:127]\n" \
"buffer_load_dwordx4 v22, s[64:67], s85 offen lds\n" \
"v_mfma_f32_16x16x32_bf16 a[88:91], v[124:127], v[172:175], a[88:91]\n" \
"ds_read_b64_tr_b16 v[96:97], v35 offset:32768\n" \
"v_mfma_f32_16x16x32_bf16 a[120:123], v[128:131], v[172:175], a[120:123]\n" \
"ds_read_b64_tr_b16 v[98:99], v35 offset:33792\n" \
"v_mfma_f32_16x16x32_bf16 a[84:87], v[124:127], v[168:171], a[84:87]\n" \
"s_add_u32 m0, s30, 0xc00\n" \
"s_mov_b32 s84, 0x6200\n" \
"v_mfma_f32_16x16x32_bf16 a[116:119], v[128:131], v[168:171], a[116:119]\n" \
"buffer_load_dwordx4 v23, s[64:67], s84 offen lds\n" \
"v_mfma_f32_16x16x32_bf16 a[80:83], v[124:127], v[164:167], a[80:83]\n" \
"ds_read_b64_tr_b16 v[100:101], v36 offset:32768\n" \
"v_mfma_f32_16x16x32_bf16 a[112:115], v[128:131], v[164:167], a[112:115]\n" \
"ds_read_b64_tr_b16 v[102:103], v36 offset:33792\n" \
"v_mfma_f32_16x16x32_bf16 a[76:79], v[124:127], v[160:163], a[76:79]\n" \
"s_add_u32 m0, s31, 0x400\n" \
"s_mov_b32 s85, 0x208000\n" \
"v_mfma_f32_16x16x32_bf16 a[108:111], v[128:131], v[160:163], a[108:111]\n" \
"buffer_load_dwordx4 v26, s[68:71], s85 offen lds\n" \
"v_mfma_f32_16x16x32_bf16 a[72:75], v[124:127], v[156:159], a[72:75]\n" \
"ds_read_b64_tr_b16 v[104:105], v37 offset:32768\n" \
"v_mfma_f32_16x16x32_bf16 a[104:107], v[128:131], v[156:159], a[104:107]\n" \
"s_cmpk_lt_u32 s3, 3\n" \
"s_cselect_b32 s44, s44, s52\n" \
"v_mfma_f32_16x16x32_bf16 a[68:71], v[124:127], v[152:155], a[68:71]\n" \
"s_add_u32 m0, s31, 0x4400\n" \
"s_mov_b32 s84, 0x208000\n" \
"v_mfma_f32_16x16x32_bf16 a[100:103], v[128:131], v[152:155], a[100:103]\n" \
"buffer_load_dwordx4 v26, s[72:75], s84 offen lds\n" \
"v_mfma_f32_16x16x32_bf16 a[64:67], v[124:127], v[148:151], a[64:67]\n" \
"ds_read_b64_tr_b16 v[106:107], v37 offset:33792\n" \
"s_movk_i32 s52, 0xfff\n" \
"s_nop 0\n" \
"v_mfma_f32_16x16x32_bf16 a[96:99], v[128:131], v[148:151], a[96:99]\n" \
"ds_read_b64_tr_b16 v[108:109], v38 offset:32768\n" \
"v_writelane_b32 v11, s56, 12\n" \
"v_mfma_f32_16x16x32_bf16 a[128:131], v[132:135], v[148:151], a[128:131]\n" \
"s_add_u32 m0, s30, 0x1000\n" \
"s_mov_b32 s85, 0x8200\n" \
"v_mfma_f32_16x16x32_bf16 a[160:163], v[136:139], v[148:151], a[160:163]\n" \
"buffer_load_dwordx4 v22, s[64:67], s85 offen lds\n" \
"v_mfma_f32_16x16x32_bf16 a[132:135], v[132:135], v[152:155], a[132:135]\n" \
"ds_read_b64_tr_b16 v[110:111], v38 offset:33792\n" \
"s_and_b32 s56, s29, 0xffff\n" \
"v_mfma_f32_16x16x32_bf16 a[164:167], v[136:139], v[152:155], a[164:167]\n" \
"ds_read_b64_tr_b16 v[112:113], v39 offset:32768\n" \
"v_writelane_b32 v11, s28, 13\n" \
"v_mfma_f32_16x16x32_bf16 a[136:139], v[132:135], v[156:159], a[136:139]\n" \
"s_add_u32 m0, s30, 0x1400\n" \
"s_mov_b32 s84, 0xa200\n" \
"v_mfma_f32_16x16x32_bf16 a[168:171], v[136:139], v[156:159], a[168:171]\n" \
"buffer_load_dwordx4 v23, s[64:67], s84 offen lds\n" \
"v_mfma_f32_16x16x32_bf16 a[140:143], v[132:135], v[160:163], a[140:143]\n" \
"ds_read_b64_tr_b16 v[114:115], v39 offset:33792\n" \
"v_mul_u32_u24_e32 v213, 0x120, v213\n" \
"v_mfma_f32_16x16x32_bf16 a[172:175], v[136:139], v[160:163], a[172:175]\n" \
"ds_read_b128 v[60:63], v28 offset:53248\n" \
"v_writelane_b32 v11, s56, 14\n" \
"v_mfma_f32_16x16x32_bf16 a[144:147], v[132:135], v[164:167], a[144:147]\n" \
"s_add_u32 m0, s31, 0x800\n" \
"s_mov_b32 s85, 0x210000\n" \
"v_mfma_f32_16x16x32_bf16 a[176:179], v[136:139], v[164:167], a[176:179]\n" \
"buffer_load_dwordx4 v27, s[68:71], s85 offen lds\n" \
"v_mfma_f32_16x16x32_bf16 a[148:151], v[132:135], v[168:171], a[148:151]\n" \
"ds_read_b128 v[64:67], v28 offset:55296\n" \
"v_lshlrev_b32_e64 v212, 3, v212\n" \
"v_mfma_f32_16x16x32_bf16 a[180:183], v[136:139], v[168:171], a[180:183]\n" \
"ds_read_b128 v[68:71], v28 offset:32768\n" \
"v_writelane_b32 v11, s44, 15\n" \
"v_mfma_f32_16x16x32_bf16 a[152:155], v[132:135], v[172:175], a[152:155]\n" \
"s_add_u32 m0, s31, 0x4800\n" \
"s_mov_b32 s84, 0x210000\n" \
"v_mfma_f32_16x16x32_bf16 a[184:187], v[136:139], v[172:175], a[184:187]\n" \
"buffer_load_dwordx4 v27, s[72:75], s84 offen lds\n" \
"v_mfma_f32_16x16x32_bf16 a[156:159], v[132:135], v[176:179], a[156:159]\n" \
"ds_read_b128 v[72:75], v28 offset:34816\n" \
"v_mfma_f32_16x16x32_bf16 a[188:191], v[136:139], v[176:179], a[188:191]\n" \
"ds_read_b128 v[76:79], v28 offset:36864\n" \
"v_mfma_f32_16x16x32_bf16 a[220:223], v[140:143], v[176:179], a[220:223]\n" \
"s_cmpk_lt_u32 s3, 3\n" \
"s_cselect_b32 s44, s51, s53\n" \
"v_mfma_f32_16x16x32_bf16 a[252:255], v[144:147], v[176:179], a[252:255]\n" \
"ds_read_b128 v[80:83], v28 offset:38912\n" \
"s_and_b32 s44, s44, 0xffff\n" \
"v_mfma_f32_16x16x32_bf16 a[216:219], v[140:143], v[172:175], a[216:219]\n" \
"v_writelane_b32 v11, s44, 16\n" \
"s_mul_i32 s44, s40, 0x48\n" \
"v_mfma_f32_16x16x32_bf16 a[248:251], v[144:147], v[172:175], a[248:251]\n" \
"s_add_i32 s51, s44, 0x47\n" \
"v_mfma_f32_16x16x32_bf16 a[212:215], v[140:143], v[168:171], a[212:215]\n" \
"s_cmpk_lt_u32 s3, 3\n" \
"s_cselect_b32 s51, s51, s52\n" \
"v_mfma_f32_16x16x32_bf16 a[244:247], v[144:147], v[168:171], a[244:247]\n" \
"s_mulk_i32 s51, 0x480\n" \
"s_addk_i32 s51, 0x120\n" \
"v_mfma_f32_16x16x32_bf16 a[208:211], v[140:143], v[164:167], a[208:211]\n" \
"v_writelane_b32 v11, s51, 17\n" \
"s_lshl_b32 s51, s3, 5\n" \
"v_mfma_f32_16x16x32_bf16 a[240:243], v[144:147], v[164:167], a[240:243]\n" \
"s_add_i32 s44, s44, s51\n" \
"s_mul_i32 s51, s44, 0x480\n" \
"v_mfma_f32_16x16x32_bf16 a[204:207], v[140:143], v[160:163], a[204:207]\n" \
"s_mul_i32 s44, s46, 0x24000\n" \
"s_add_i32 s52, s44, 0x1b000\n" \
"v_mfma_f32_16x16x32_bf16 a[236:239], v[144:147], v[160:163], a[236:239]\n" \
"s_cmpk_lt_u32 s3, 3\n" \
"s_cselect_b32 s51, s51, s52\n" \
"v_mfma_f32_16x16x32_bf16 a[200:203], v[140:143], v[156:159], a[200:203]\n" \
"s_mul_i32 s52, s3, 0x9000\n" \
"v_writelane_b32 v11, s51, 18\n" \
"v_mfma_f32_16x16x32_bf16 a[232:235], v[144:147], v[156:159], a[232:235]\n" \
"s_mul_i32 s51, s50, 0x2400\n" \
"s_mul_i32 s50, s50, 0x9000\n" \
"v_mfma_f32_16x16x32_bf16 a[196:199], v[140:143], v[152:155], a[196:199]\n" \
"s_bitset1_b32 s51, 17\n" \
"v_writelane_b32 v11, s51, 19\n" \
"v_mfma_f32_16x16x32_bf16 a[228:231], v[144:147], v[152:155], a[228:231]\n" \
"s_add_i32 s50, s44, s50\n" \
"v_writelane_b32 v11, s50, 25\n" \
"v_mfma_f32_16x16x32_bf16 a[192:195], v[140:143], v[148:151], a[192:195]\n" \
"s_mul_i32 s50, s3, 0x2400\n" \
"s_add_i32 s51, s50, 0x6000\n" \
"v_mfma_f32_16x16x32_bf16 a[224:227], v[144:147], v[148:151], a[224:227]\n" \
"s_waitcnt lgkmcnt(15)\n" \
"s_nop 0\n" \
"v_mfma_f32_16x16x32_bf16 a[0:3], v[52:55], v[84:87], a[0:3]\n" \
"s_add_u32 m0, s30, 0x1800\n" \
"s_mov_b32 s85, 0xc200\n" \
"v_mfma_f32_16x16x32_bf16 a[32:35], v[56:59], v[84:87], a[32:35]\n" \
"buffer_load_dwordx4 v22, s[64:67], s85 offen lds\n" \
"v_mfma_f32_16x16x32_bf16 a[4:7], v[52:55], v[88:91], a[4:7]\n" \
"ds_read_b64_tr_b16 v[148:149], v32 offset:40960\n" \
"v_writelane_b32 v11, s51, 20\n" \
"v_mfma_f32_16x16x32_bf16 a[36:39], v[56:59], v[88:91], a[36:39]\n" \
"s_add_u32 m0, s30, 0x1c00\n" \
"s_mov_b32 s84, 0xe200\n" \
"v_mfma_f32_16x16x32_bf16 a[8:11], v[52:55], v[92:95], a[8:11]\n" \
"buffer_load_dwordx4 v23, s[64:67], s84 offen lds\n" \
"v_mfma_f32_16x16x32_bf16 a[40:43], v[56:59], v[92:95], a[40:43]\n" \
"ds_read_b64_tr_b16 v[150:151], v32 offset:41984\n" \
"s_or_b32 s51, s50, 0x10000\n" \
"s_waitcnt lgkmcnt(15)\n" \
"v_mfma_f32_16x16x32_bf16 a[12:15], v[52:55], v[96:99], a[12:15]\n" \
"s_add_u32 m0, s31, 0xc00\n" \
"s_mov_b32 s85, 0x218000\n" \
"v_mfma_f32_16x16x32_bf16 a[44:47], v[56:59], v[96:99], a[44:47]\n" \
"buffer_load_dwordx4 v27, s[68:71], s85 offen lds\n" \
"s_waitcnt lgkmcnt(14)\n" \
"v_mfma_f32_16x16x32_bf16 a[16:19], v[52:55], v[100:103], a[16:19]\n" \
"ds_read_b128 v[116:119], v29 offset:49152\n" \
"v_writelane_b32 v11, s51, 21\n" \
"v_mfma_f32_16x16x32_bf16 a[48:51], v[56:59], v[100:103], a[48:51]\n" \
"s_add_u32 m0, s31, 0x4c00\n" \
"s_mov_b32 s84, 0x218000\n" \
"s_waitcnt lgkmcnt(13)\n" \
"v_mfma_f32_16x16x32_bf16 a[20:23], v[52:55], v[104:107], a[20:23]\n" \
"buffer_load_dwordx4 v27, s[72:75], s84 offen lds\n" \
"v_mfma_f32_16x16x32_bf16 a[52:55], v[56:59], v[104:107], a[52:55]\n" \
"ds_read_b128 v[120:123], v29 offset:51200\n" \
"s_add_i32 s51, s50, 0x19000\n" \
"s_waitcnt lgkmcnt(12)\n" \
"v_mfma_f32_16x16x32_bf16 a[24:27], v[52:55], v[108:111], a[24:27]\n" \
"ds_read_b64_tr_b16 v[152:153], v33 offset:40960\n" \
"v_mfma_f32_16x16x32_bf16 a[56:59], v[56:59], v[108:111], a[56:59]\n" \
"ds_read_b64_tr_b16 v[154:155], v33 offset:41984\n" \
"s_waitcnt lgkmcnt(12)\n" \
"v_mfma_f32_16x16x32_bf16 a[28:31], v[52:55], v[112:115], a[28:31]\n" \
"ds_read_b64_tr_b16 v[156:157], v34 offset:40960\n" \
"v_mfma_f32_16x16x32_bf16 a[60:63], v[56:59], v[112:115], a[60:63]\n" \
"ds_read_b64_tr_b16 v[158:159], v34 offset:41984\n" \
"s_waitcnt lgkmcnt(13)\n" \
"v_mfma_f32_16x16x32_bf16 a[92:95], v[60:63], v[112:115], a[92:95]\n" \
"s_cmpk_lt_u32 s3, 3\n" \
"s_cselect_b32 s51, s51, s5\n" \
"s_waitcnt lgkmcnt(12)\n" \
"v_mfma_f32_16x16x32_bf16 a[124:127], v[64:67], v[112:115], a[124:127]\n" \
"ds_read_b64_tr_b16 v[160:161], v35 offset:40960\n" \
"s_bitset1_b32 s50, 17\n" \
"v_mfma_f32_16x16x32_bf16 a[88:91], v[60:63], v[108:111], a[88:91]\n" \
"v_writelane_b32 v11, s51, 22\n" \
"s_movk_i32 s51, 0x2400\n" \
"v_mfma_f32_16x16x32_bf16 a[120:123], v[64:67], v[108:111], a[120:123]\n" \
"ds_read_b64_tr_b16 v[162:163], v35 offset:41984\n" \
"v_mfma_f32_16x16x32_bf16 a[84:87], v[60:63], v[104:107], a[84:87]\n" \
"s_mov_b32 m0, s32\n" \
"v_mfma_f32_16x16x32_bf16 a[116:119], v[64:67], v[104:107], a[116:119]\n" \
"s_mov_b32 exec_lo, 0xffffffff\n" \
"s_mov_b32 exec_hi, 0x0\n" \
"global_load_lds_dword v[188:189], off\n" \
"s_mov_b64 exec, -1\n" \
"v_mfma_f32_16x16x32_bf16 a[80:83], v[60:63], v[100:103], a[80:83]\n" \
"ds_read_b64_tr_b16 v[164:165], v36 offset:40960\n" \
"v_mfma_f32_16x16x32_bf16 a[112:115], v[64:67], v[100:103], a[112:115]\n" \
"ds_read_b64_tr_b16 v[166:167], v36 offset:41984\n" \
"v_mfma_f32_16x16x32_bf16 a[76:79], v[60:63], v[96:99], a[76:79]\n" \
"s_waitcnt vmcnt(17)\n" \
"v_or_b32_e32 v204, v210, v204\n" \
"v_or_b32_e32 v206, v210, v206\n" \
"v_mfma_f32_16x16x32_bf16 a[108:111], v[64:67], v[96:99], a[108:111]\n" \
"v_mov_b32_e64 v210, 0\n" \
"s_add_u32 s88, s35, 0x1000\n" \
"v_mfma_f32_16x16x32_bf16 a[72:75], v[60:63], v[92:95], a[72:75]\n" \
"ds_read_b64_tr_b16 v[168:169], v37 offset:40960\n" \
"v_mfma_f32_16x16x32_bf16 a[104:107], v[64:67], v[92:95], a[104:107]\n" \
"s_cmpk_lt_u32 s3, 3\n" \
"s_cselect_b32 s50, s50, s51\n" \
"v_mfma_f32_16x16x32_bf16 a[68:71], v[60:63], v[88:91], a[68:71]\n" \
"ds_read_b64_tr_b16 v[170:171], v37 offset:41984\n" \
"s_xor_b32 s46, s46, 2\n" \
"v_mfma_f32_16x16x32_bf16 a[100:103], v[64:67], v[88:91], a[100:103]\n" \
"v_writelane_b32 v11, s50, 23\n" \
"s_lshl_b32 s50, s3, 10\n" \
"v_mfma_f32_16x16x32_bf16 a[64:67], v[60:63], v[84:87], a[64:67]\n" \
"ds_write_b128 v196, v[200:203]\n" \
"v_mfma_f32_16x16x32_bf16 a[96:99], v[64:67], v[84:87], a[96:99]\n" \
"ds_read_b64_tr_b16 v[172:173], v38 offset:40960\n" \
"s_or_b32 s50, s50, 0xf000\n" \
"s_waitcnt lgkmcnt(15)\n" \
"v_mfma_f32_16x16x32_bf16 a[128:131], v[68:71], v[84:87], a[128:131]\n" \
"ds_read_b64_tr_b16 v[174:175], v38 offset:41984\n" \
"v_writelane_b32 v11, s50, 24\n" \
"v_mfma_f32_16x16x32_bf16 a[160:163], v[72:75], v[84:87], a[160:163]\n" \
"s_add_i32 s50, s49, 1\n" \
"s_and_b32 s50, s50, 3\n" \
"v_mfma_f32_16x16x32_bf16 a[132:135], v[68:71], v[88:91], a[132:135]\n" \
"ds_read_b64_tr_b16 v[176:177], v39 offset:40960\n" \
"s_or_b32 s50, s50, s41\n" \
"v_mfma_f32_16x16x32_bf16 a[164:167], v[72:75], v[88:91], a[164:167]\n" \
"s_mul_i32 s51, s50, 0x24000\n" \
"s_add_i32 s51, s51, s52\n" \
"v_mfma_f32_16x16x32_bf16 a[136:139], v[68:71], v[92:95], a[136:139]\n" \
"ds_write_b128 v197, v[204:207]\n" \
"v_mfma_f32_16x16x32_bf16 a[168:171], v[72:75], v[92:95], a[168:171]\n" \
"ds_read_b64_tr_b16 v[178:179], v39 offset:41984\n" \
"s_add_i32 s49, s49, -1\n" \
"v_mfma_f32_16x16x32_bf16 a[140:143], v[68:71], v[96:99], a[140:143]\n" \
"v_writelane_b32 v11, s51, 26\n" \
"s_mul_i32 s51, s46, 0x24000\n" \
"v_mfma_f32_16x16x32_bf16 a[172:175], v[72:75], v[96:99], a[172:175]\n" \
"ds_read_b128 v[124:127], v29 offset:53248\n" \
"s_and_b32 s49, s49, 3\n" \
"v_mfma_f32_16x16x32_bf16 a[144:147], v[68:71], v[100:103], a[144:147]\n" \
"s_add_i32 s51, s51, s52\n" \
"s_or_b32 s49, s49, s41\n" \
"v_mfma_f32_16x16x32_bf16 a[176:179], v[72:75], v[100:103], a[176:179]\n" \
"ds_read_b128 v[128:131], v29 offset:55296\n" \
"v_writelane_b32 v11, s51, 27\n" \
"v_mfma_f32_16x16x32_bf16 a[148:151], v[68:71], v[104:107], a[148:151]\n" \
"ds_write_b128 v198, v[208:211]\n" \
"v_mfma_f32_16x16x32_bf16 a[180:183], v[72:75], v[104:107], a[180:183]\n" \
"ds_read_b128 v[132:135], v29 offset:32768\n" \
"s_mul_i32 s51, s49, 0x24000\n" \
"v_mfma_f32_16x16x32_bf16 a[152:155], v[68:71], v[108:111], a[152:155]\n" \
"s_add_i32 s51, s51, s52\n" \
"v_writelane_b32 v11, s51, 28\n" \
"v_mfma_f32_16x16x32_bf16 a[184:187], v[72:75], v[108:111], a[184:187]\n" \
"ds_read_b128 v[136:139], v29 offset:34816\n" \
"s_or_b32 s51, s48, s47\n" \
"v_mfma_f32_16x16x32_bf16 a[156:159], v[68:71], v[112:115], a[156:159]\n" \
"s_mul_i32 s51, s51, 0x24800\n" \
"s_lshl_b32 s50, s50, 12\n" \
"v_mfma_f32_16x16x32_bf16 a[188:191], v[72:75], v[112:115], a[188:191]\n" \
"ds_read_b128 v[140:143], v29 offset:36864\n" \
"s_add_i32 s52, s51, 0x3004800\n" \
"v_mfma_f32_16x16x32_bf16 a[220:223], v[76:79], v[112:115], a[220:223]\n" \
"s_add_i32 s53, s52, s50\n" \
"v_writelane_b32 v11, s53, 29\n" \
"v_mfma_f32_16x16x32_bf16 a[252:255], v[80:83], v[112:115], a[252:255]\n" \
"ds_read_b128 v[144:147], v29 offset:38912\n" \
"s_lshl_b32 s46, s46, 12\n" \
"v_mfma_f32_16x16x32_bf16 a[216:219], v[76:79], v[108:111], a[216:219]\n" \
"s_lshl_b32 s49, s49, 12\n" \
"s_sub_i32 s47, s48, s47\n" \
"v_mfma_f32_16x16x32_bf16 a[248:251], v[80:83], v[108:111], a[248:251]\n" \
"s_add_i32 s53, s52, s46\n" \
"v_writelane_b32 v11, s53, 30\n" \
"v_mfma_f32_16x16x32_bf16 a[212:215], v[76:79], v[104:107], a[212:215]\n" \
"s_add_i32 s52, s52, s49\n" \
"s_mul_i32 s47, s47, 0x24800\n" \
"v_mfma_f32_16x16x32_bf16 a[244:247], v[80:83], v[104:107], a[244:247]\n" \
"v_writelane_b32 v11, s52, 31\n" \
"s_add_i32 s52, s47, 0x4120800\n" \
"v_mfma_f32_16x16x32_bf16 a[208:211], v[76:79], v[100:103], a[208:211]\n" \
"s_add_i32 s50, s52, s50\n" \
"v_writelane_b32 v11, s50, 32\n" \
"v_mfma_f32_16x16x32_bf16 a[240:243], v[80:83], v[100:103], a[240:243]\n" \
"s_add_i32 s46, s52, s46\n" \
"v_writelane_b32 v11, s46, 33\n" \
"v_mfma_f32_16x16x32_bf16 a[204:207], v[76:79], v[96:99], a[204:207]\n" \
"s_add_i32 s52, s52, s49\n" \
"s_or_b32 s46, s51, 0x3000000\n" \
"v_mfma_f32_16x16x32_bf16 a[236:239], v[80:83], v[96:99], a[236:239]\n" \
"s_lshl_b32 s49, s40, 11\n" \
"v_writelane_b32 v11, s52, 34\n" \
"v_mfma_f32_16x16x32_bf16 a[200:203], v[76:79], v[92:95], a[200:203]\n" \
"s_add_i32 s50, s46, s49\n" \
"v_writelane_b32 v11, s50, 35\n" \
"v_mfma_f32_16x16x32_bf16 a[232:235], v[80:83], v[92:95], a[232:235]\n" \
"s_add_i32 s50, s47, 0x411c000\n" \
"s_add_i32 s52, s50, s49\n" \
"v_mfma_f32_16x16x32_bf16 a[196:199], v[76:79], v[88:91], a[196:199]\n" \
"v_writelane_b32 v11, s52, 36\n" \
"s_add_i32 s51, s51, 0x3004000\n" \
"v_mfma_f32_16x16x32_bf16 a[228:231], v[80:83], v[88:91], a[228:231]\n" \
"v_writelane_b32 v11, s51, 37\n" \
"s_add_i32 s47, s47, 0x4120000\n" \
"v_mfma_f32_16x16x32_bf16 a[192:195], v[76:79], v[84:87], a[192:195]\n" \
"v_writelane_b32 v11, s47, 38\n" \
"s_lshl_b32 s47, s6, 2\n" \
"v_mfma_f32_16x16x32_bf16 a[224:227], v[80:83], v[84:87], a[224:227]\n" \
"s_waitcnt vmcnt(1) lgkmcnt(0)\n" \
"s_barrier\n" \
"v_mfma_f32_16x16x32_bf16 a[0:3], v[116:119], v[148:151], a[0:3]\n" \
"s_add_u32 m0, s30, 0x8000\n" \
"s_mov_b32 s85, 0x280\n" \
"v_mfma_f32_16x16x32_bf16 a[32:35], v[120:123], v[148:151], a[32:35]\n" \
"buffer_load_dwordx4 v22, s[64:67], s85 offen lds\n" \
"v_mfma_f32_16x16x32_bf16 a[4:7], v[116:119], v[152:155], a[4:7]\n" \
"ds_read_b64_tr_b16 v[84:85], v32 offset:0\n" \
"s_or_b32 s43, s47, s43\n" \
"v_mfma_f32_16x16x32_bf16 a[36:39], v[120:123], v[152:155], a[36:39]\n" \
"s_add_u32 m0, s30, 0x8400\n" \
"s_mov_b32 s84, 0x2280\n" \
"v_mfma_f32_16x16x32_bf16 a[8:11], v[116:119], v[156:159], a[8:11]\n" \
"buffer_load_dwordx4 v23, s[64:67], s84 offen lds\n" \
"v_mfma_f32_16x16x32_bf16 a[40:43], v[120:123], v[156:159], a[40:43]\n" \
"ds_read_b64_tr_b16 v[86:87], v32 offset:1024\n" \
"s_mul_i32 s43, s43, 0x90000\n" \
"v_mfma_f32_16x16x32_bf16 a[12:15], v[116:119], v[160:163], a[12:15]\n" \
"s_add_u32 m0, s31, 0x8000\n" \
"s_mov_b32 s85, 0x280000\n" \
"v_mfma_f32_16x16x32_bf16 a[44:47], v[120:123], v[160:163], a[44:47]\n" \
"buffer_load_dwordx4 v26, s[68:71], s85 offen lds\n" \
"v_mfma_f32_16x16x32_bf16 a[16:19], v[116:119], v[164:167], a[16:19]\n" \
"ds_read_b128 v[52:55], v28 offset:16384\n" \
"s_lshl_b32 s47, s3, 14\n" \
"v_mfma_f32_16x16x32_bf16 a[48:51], v[120:123], v[164:167], a[48:51]\n" \
"s_add_u32 m0, s31, 0xc000\n" \
"s_mov_b32 s84, 0x280000\n" \
"v_mfma_f32_16x16x32_bf16 a[20:23], v[116:119], v[168:171], a[20:23]\n" \
"buffer_load_dwordx4 v26, s[72:75], s84 offen lds\n" \
"v_mfma_f32_16x16x32_bf16 a[52:55], v[120:123], v[168:171], a[52:55]\n" \
"ds_read_b128 v[56:59], v28 offset:18432\n" \
"s_add_i32 s43, s43, s47\n" \
"v_mfma_f32_16x16x32_bf16 a[24:27], v[116:119], v[172:175], a[24:27]\n" \
"s_add_u32 m0, s30, 0x8800\n" \
"s_mov_b32 s85, 0x4280\n" \
"v_mfma_f32_16x16x32_bf16 a[56:59], v[120:123], v[172:175], a[56:59]\n" \
"buffer_load_dwordx4 v22, s[64:67], s85 offen lds\n" \
"v_mfma_f32_16x16x32_bf16 a[28:31], v[116:119], v[176:179], a[28:31]\n" \
"ds_read_b64_tr_b16 v[88:89], v33 offset:0\n" \
"s_lshl_b32 s47, s48, 4\n" \
"v_mfma_f32_16x16x32_bf16 a[60:63], v[120:123], v[176:179], a[60:63]\n" \
"ds_read_b64_tr_b16 v[90:91], v33 offset:1024\n" \
"s_lshl_b32 s48, s6, 7\n" \
"v_mfma_f32_16x16x32_bf16 a[92:95], v[124:127], v[176:179], a[92:95]\n" \
"s_add_u32 m0, s30, 0x8c00\n" \
"s_mov_b32 s84, 0x6280\n" \
"v_mfma_f32_16x16x32_bf16 a[124:127], v[128:131], v[176:179], a[124:127]\n" \
"buffer_load_dwordx4 v23, s[64:67], s84 offen lds\n" \
"v_mfma_f32_16x16x32_bf16 a[88:91], v[124:127], v[172:175], a[88:91]\n" \
"ds_read_b64_tr_b16 v[92:93], v34 offset:0\n" \
"s_or_b32 s51, s47, s48\n" \
"v_mfma_f32_16x16x32_bf16 a[120:123], v[128:131], v[172:175], a[120:123]\n" \
"s_add_u32 m0, s31, 0x8400\n" \
"s_mov_b32 s85, 0x288000\n" \
"v_mfma_f32_16x16x32_bf16 a[84:87], v[124:127], v[168:171], a[84:87]\n" \
"buffer_load_dwordx4 v26, s[68:71], s85 offen lds\n" \
"v_mfma_f32_16x16x32_bf16 a[116:119], v[128:131], v[168:171], a[116:119]\n" \
"ds_read_b64_tr_b16 v[94:95], v34 offset:1024\n" \
"s_sub_i32 s47, s47, s48\n" \
"v_mfma_f32_16x16x32_bf16 a[80:83], v[124:127], v[164:167], a[80:83]\n" \
"s_add_u32 m0, s31, 0xc400\n" \
"s_mov_b32 s84, 0x288000\n" \
"v_mfma_f32_16x16x32_bf16 a[112:115], v[128:131], v[164:167], a[112:115]\n" \
"buffer_load_dwordx4 v26, s[72:75], s84 offen lds\n" \
"v_mfma_f32_16x16x32_bf16 a[76:79], v[124:127], v[160:163], a[76:79]\n" \
"ds_read_b64_tr_b16 v[96:97], v35 offset:0\n" \
"s_mulk_i32 s51, 0x480\n" \
"v_mfma_f32_16x16x32_bf16 a[108:111], v[128:131], v[160:163], a[108:111]\n" \
"s_add_u32 m0, s30, 0x9000\n" \
"s_mov_b32 s85, 0x8280\n" \
"v_mfma_f32_16x16x32_bf16 a[72:75], v[124:127], v[156:159], a[72:75]\n" \
"buffer_load_dwordx4 v22, s[64:67], s85 offen lds\n" \
"v_mfma_f32_16x16x32_bf16 a[104:107], v[128:131], v[156:159], a[104:107]\n" \
"ds_read_b64_tr_b16 v[98:99], v35 offset:1024\n" \
"s_mulk_i32 s47, 0x480\n" \
"v_mfma_f32_16x16x32_bf16 a[68:71], v[124:127], v[152:155], a[68:71]\n" \
"s_add_u32 m0, s30, 0x9400\n" \
"s_mov_b32 s84, 0xa280\n" \
"v_mfma_f32_16x16x32_bf16 a[100:103], v[128:131], v[152:155], a[100:103]\n" \
"buffer_load_dwordx4 v23, s[64:67], s84 offen lds\n" \
"v_mfma_f32_16x16x32_bf16 a[64:67], v[124:127], v[148:151], a[64:67]\n" \
"ds_read_b64_tr_b16 v[100:101], v36 offset:0\n" \
"s_or_b32 s48, s51, s45\n" \
"v_mfma_f32_16x16x32_bf16 a[96:99], v[128:131], v[148:151], a[96:99]\n" \
"ds_read_b64_tr_b16 v[102:103], v36 offset:1024\n" \
"s_or_b32 s45, s47, s45\n" \
"v_mfma_f32_16x16x32_bf16 a[128:131], v[132:135], v[148:151], a[128:131]\n" \
"s_add_u32 m0, s31, 0x8800\n" \
"s_mov_b32 s85, 0x290000\n" \
"v_mfma_f32_16x16x32_bf16 a[160:163], v[136:139], v[148:151], a[160:163]\n" \
"buffer_load_dwordx4 v27, s[68:71], s85 offen lds\n" \
"v_mfma_f32_16x16x32_bf16 a[132:135], v[132:135], v[152:155], a[132:135]\n" \
"ds_read_b64_tr_b16 v[104:105], v37 offset:0\n" \
"s_and_b32 s47, s2, 1\n" \
"s_nop 0\n" \
"v_mfma_f32_16x16x32_bf16 a[164:167], v[136:139], v[152:155], a[164:167]\n" \
"s_add_u32 m0, s31, 0xc800\n" \
"s_mov_b32 s84, 0x290000\n" \
"v_mfma_f32_16x16x32_bf16 a[136:139], v[132:135], v[156:159], a[136:139]\n" \
"buffer_load_dwordx4 v27, s[72:75], s84 offen lds\n" \
"v_mfma_f32_16x16x32_bf16 a[168:171], v[136:139], v[156:159], a[168:171]\n" \
"ds_read_b64_tr_b16 v[106:107], v37 offset:1024\n" \
"v_mfma_f32_16x16x32_bf16 a[140:143], v[132:135], v[160:163], a[140:143]\n" \
"ds_read_b128 v[200:203], v199 offset:0\n" \
"v_mfma_f32_16x16x32_bf16 a[172:175], v[136:139], v[160:163], a[172:175]\n" \
"ds_read_b64_tr_b16 v[108:109], v38 offset:0\n" \
"v_mfma_f32_16x16x32_bf16 a[144:147], v[132:135], v[164:167], a[144:147]\n" \
"ds_read_b64_tr_b16 v[110:111], v38 offset:1024\n" \
"v_mfma_f32_16x16x32_bf16 a[176:179], v[136:139], v[164:167], a[176:179]\n" \
"ds_read_b128 v[204:207], v199 offset:4096\n" \
"v_mfma_f32_16x16x32_bf16 a[148:151], v[132:135], v[168:171], a[148:151]\n" \
"ds_read_b64_tr_b16 v[112:113], v39 offset:0\n" \
"v_mfma_f32_16x16x32_bf16 a[180:183], v[136:139], v[168:171], a[180:183]\n" \
"ds_read_b64_tr_b16 v[114:115], v39 offset:1024\n" \
"v_mfma_f32_16x16x32_bf16 a[152:155], v[132:135], v[172:175], a[152:155]\n" \
"ds_read_b128 v[208:211], v199 offset:8192\n" \
"v_mfma_f32_16x16x32_bf16 a[184:187], v[136:139], v[172:175], a[184:187]\n" \
"ds_read_b128 v[60:63], v28 offset:20480\n" \
"v_mfma_f32_16x16x32_bf16 a[156:159], v[132:135], v[176:179], a[156:159]\n" \
"ds_read_b128 v[64:67], v28 offset:22528\n" \
"v_mfma_f32_16x16x32_bf16 a[188:191], v[136:139], v[176:179], a[188:191]\n" \
"ds_read_b128 v[68:71], v28 offset:0\n" \
"v_mfma_f32_16x16x32_bf16 a[220:223], v[140:143], v[176:179], a[220:223]\n" \
"ds_read_b128 v[72:75], v28 offset:2048\n" \
"v_mfma_f32_16x16x32_bf16 a[252:255], v[144:147], v[176:179], a[252:255]\n" \
"ds_read_b128 v[76:79], v28 offset:4096\n" \
"v_mfma_f32_16x16x32_bf16 a[216:219], v[140:143], v[172:175], a[216:219]\n" \
"ds_read_b128 v[80:83], v28 offset:6144\n" \
"v_mfma_f32_16x16x32_bf16 a[248:251], v[144:147], v[172:175], a[248:251]\n" \
"s_cmp_lg_u32 s47, 0\n" \
"s_cselect_b32 s46, s50, s46\n" \
"v_mfma_f32_16x16x32_bf16 a[212:215], v[140:143], v[168:171], a[212:215]\n" \
"s_bfe_u32 s50, s2, 0x20001\n" \
"s_addk_i32 s46, 0x4000\n" \
"v_mfma_f32_16x16x32_bf16 a[244:247], v[144:147], v[168:171], a[244:247]\n" \
"s_xor_b32 s50, s50, s3\n" \
"v_mfma_f32_16x16x32_bf16 a[208:211], v[140:143], v[164:167], a[208:211]\n" \
"s_cmp_lg_u32 s50, 0\n" \
"s_cselect_b32 s46, s42, s46\n" \
"v_mfma_f32_16x16x32_bf16 a[240:243], v[144:147], v[164:167], a[240:243]\n" \
"s_lshl_b32 s40, s40, 16\n" \
"v_writelane_b32 v11, s46, 39\n" \
"v_mfma_f32_16x16x32_bf16 a[204:207], v[140:143], v[160:163], a[204:207]\n" \
"s_add_i32 s46, s43, 0x4240000\n" \
"s_add_i32 s40, s46, s40\n" \
"v_mfma_f32_16x16x32_bf16 a[236:239], v[144:147], v[160:163], a[236:239]\n" \
"v_writelane_b32 v11, s40, 40\n" \
"s_add_i32 s43, s43, 0x42c0000\n" \
"v_mfma_f32_16x16x32_bf16 a[200:203], v[140:143], v[156:159], a[200:203]\n" \
"s_lshl_b32 s40, s47, 10\n" \
"s_or_b32 s40, s43, s40\n" \
"v_mfma_f32_16x16x32_bf16 a[232:235], v[144:147], v[156:159], a[232:235]\n" \
"s_cmp_lg_u32 s50, 0\n" \
"s_cselect_b32 s40, s42, s40\n" \
"v_mfma_f32_16x16x32_bf16 a[196:199], v[140:143], v[152:155], a[196:199]\n" \
"s_mulk_i32 s6, 0x4800\n" \
"v_writelane_b32 v11, s40, 41\n" \
"v_mfma_f32_16x16x32_bf16 a[228:231], v[144:147], v[152:155], a[228:231]\n" \
"s_add_i32 s40, s48, s7\n" \
"v_writelane_b32 v11, s40, 42\n" \
"v_mfma_f32_16x16x32_bf16 a[192:195], v[140:143], v[148:151], a[192:195]\n" \
"s_add_i32 s40, s45, 0x21c000\n" \
"v_mfma_f32_16x16x32_bf16 a[224:227], v[144:147], v[148:151], a[224:227]\n" \
"s_waitcnt lgkmcnt(15)\n" \
"s_nop 0\n" \
"v_mfma_f32_16x16x32_bf16 a[0:3], v[52:55], v[84:87], a[0:3]\n" \
"s_add_u32 m0, s30, 0x9800\n" \
"s_mov_b32 s85, 0xc280\n" \
"v_mfma_f32_16x16x32_bf16 a[32:35], v[56:59], v[84:87], a[32:35]\n" \
"buffer_load_dwordx4 v22, s[64:67], s85 offen lds\n" \
"v_mfma_f32_16x16x32_bf16 a[4:7], v[52:55], v[88:91], a[4:7]\n" \
"ds_read_b64_tr_b16 v[148:149], v32 offset:8192\n" \
"v_mfma_f32_16x16x32_bf16 a[36:39], v[56:59], v[88:91], a[36:39]\n" \
"s_add_u32 m0, s30, 0x9c00\n" \
"s_mov_b32 s84, 0xe280\n" \
"v_mfma_f32_16x16x32_bf16 a[8:11], v[52:55], v[92:95], a[8:11]\n" \
"buffer_load_dwordx4 v23, s[64:67], s84 offen lds\n" \
"v_mfma_f32_16x16x32_bf16 a[40:43], v[56:59], v[92:95], a[40:43]\n" \
"ds_read_b64_tr_b16 v[150:151], v32 offset:9216\n" \
"v_mfma_f32_16x16x32_bf16 a[12:15], v[52:55], v[96:99], a[12:15]\n" \
"s_add_u32 m0, s31, 0x8c00\n" \
"s_mov_b32 s85, 0x298000\n" \
"v_mfma_f32_16x16x32_bf16 a[44:47], v[56:59], v[96:99], a[44:47]\n" \
"buffer_load_dwordx4 v27, s[68:71], s85 offen lds\n" \
"v_mfma_f32_16x16x32_bf16 a[16:19], v[52:55], v[100:103], a[16:19]\n" \
"ds_read_b128 v[116:119], v29 offset:16384\n" \
"v_mfma_f32_16x16x32_bf16 a[48:51], v[56:59], v[100:103], a[48:51]\n" \
"s_add_u32 m0, s31, 0xcc00\n" \
"s_mov_b32 s84, 0x298000\n" \
"s_waitcnt lgkmcnt(15)\n" \
"v_mfma_f32_16x16x32_bf16 a[20:23], v[52:55], v[104:107], a[20:23]\n" \
"buffer_load_dwordx4 v27, s[72:75], s84 offen lds\n" \
"v_mfma_f32_16x16x32_bf16 a[52:55], v[56:59], v[104:107], a[52:55]\n" \
"ds_read_b128 v[120:123], v29 offset:18432\n" \
"s_waitcnt lgkmcnt(14)\n" \
"v_mfma_f32_16x16x32_bf16 a[24:27], v[52:55], v[108:111], a[24:27]\n" \
"ds_read_b64_tr_b16 v[152:153], v33 offset:8192\n" \
"v_mfma_f32_16x16x32_bf16 a[56:59], v[56:59], v[108:111], a[56:59]\n" \
"ds_read_b64_tr_b16 v[154:155], v33 offset:9216\n" \
"s_waitcnt lgkmcnt(13)\n" \
"v_mfma_f32_16x16x32_bf16 a[28:31], v[52:55], v[112:115], a[28:31]\n" \
"ds_read_b64_tr_b16 v[156:157], v34 offset:8192\n" \
"v_mfma_f32_16x16x32_bf16 a[60:63], v[56:59], v[112:115], a[60:63]\n" \
"ds_read_b64_tr_b16 v[158:159], v34 offset:9216\n" \
"s_waitcnt lgkmcnt(13)\n" \
"v_mfma_f32_16x16x32_bf16 a[92:95], v[60:63], v[112:115], a[92:95]\n" \
"s_cmp_lg_u32 s47, 0\n" \
"s_cselect_b32 s45, s40, s48\n" \
"s_waitcnt lgkmcnt(12)\n" \
"v_mfma_f32_16x16x32_bf16 a[124:127], v[64:67], v[112:115], a[124:127]\n" \
"ds_read_b64_tr_b16 v[160:161], v35 offset:8192\n" \
"s_add_i32 s40, s40, s7\n" \
"v_mfma_f32_16x16x32_bf16 a[88:91], v[60:63], v[108:111], a[88:91]\n" \
"v_writelane_b32 v11, s40, 43\n" \
"s_addk_i32 s45, 0x100\n" \
"v_mfma_f32_16x16x32_bf16 a[120:123], v[64:67], v[108:111], a[120:123]\n" \
"ds_read_b64_tr_b16 v[162:163], v35 offset:9216\n" \
"v_mfma_f32_16x16x32_bf16 a[84:87], v[60:63], v[104:107], a[84:87]\n" \
"s_mov_b32 m0, s32\n" \
"v_mfma_f32_16x16x32_bf16 a[116:119], v[64:67], v[104:107], a[116:119]\n" \
"s_mov_b32 exec_lo, 0x0\n" \
"s_mov_b32 exec_hi, 0xffffffff\n" \
"global_load_lds_dword v[188:189], off\n" \
"s_mov_b64 exec, -1\n" \
"v_mfma_f32_16x16x32_bf16 a[80:83], v[60:63], v[100:103], a[80:83]\n" \
"ds_read_b64_tr_b16 v[164:165], v36 offset:8192\n" \
"v_mfma_f32_16x16x32_bf16 a[112:115], v[64:67], v[100:103], a[112:115]\n" \
"ds_read_b64_tr_b16 v[166:167], v36 offset:9216\n" \
"v_mfma_f32_16x16x32_bf16 a[76:79], v[60:63], v[96:99], a[76:79]\n" \
"s_cmp_lg_u32 s50, 0\n" \
"s_cselect_b32 s7, s42, s45\n" \
"v_mfma_f32_16x16x32_bf16 a[108:111], v[64:67], v[96:99], a[108:111]\n" \
"v_and_or_b32 v10, v212, 24, v213\n" \
"v_writelane_b32 v11, s7, 44\n" \
"v_mfma_f32_16x16x32_bf16 a[72:75], v[60:63], v[92:95], a[72:75]\n" \
"ds_read_b64_tr_b16 v[168:169], v37 offset:8192\n" \
"s_or_b32 s7, s43, s49\n" \
"v_mfma_f32_16x16x32_bf16 a[104:107], v[64:67], v[92:95], a[104:107]\n" \
"buffer_store_dwordx4 v[200:203], v20, s[80:83], s35 offen sc1\n" \
"v_mfma_f32_16x16x32_bf16 a[68:71], v[60:63], v[88:91], a[68:71]\n" \
"ds_read_b64_tr_b16 v[170:171], v37 offset:9216\n" \
"v_writelane_b32 v11, s7, 52\n" \
"v_mfma_f32_16x16x32_bf16 a[100:103], v[64:67], v[88:91], a[100:103]\n" \
"s_lshl_b32 s7, s3, 17\n" \
"s_add_i32 s7, s46, s7\n" \
"v_mfma_f32_16x16x32_bf16 a[64:67], v[60:63], v[84:87], a[64:67]\n" \
"ds_read_b64_tr_b16 v[172:173], v38 offset:8192\n" \
"s_or_b32 s7, s7, s41\n" \
"v_mfma_f32_16x16x32_bf16 a[96:99], v[64:67], v[84:87], a[96:99]\n" \
"s_or_b32 s7, s7, 0x3840\n" \
"v_writelane_b32 v11, s7, 6\n" \
"s_waitcnt lgkmcnt(15)\n" \
"v_mfma_f32_16x16x32_bf16 a[128:131], v[68:71], v[84:87], a[128:131]\n" \
"ds_read_b64_tr_b16 v[174:175], v38 offset:9216\n" \
"s_mov_b32 s7, 0xff0000\n" \
"v_mfma_f32_16x16x32_bf16 a[160:163], v[72:75], v[84:87], a[160:163]\n" \
"s_cmp_lg_u32 s50, 0\n" \
"s_cselect_b32 s5, s5, s7\n" \
"v_mfma_f32_16x16x32_bf16 a[132:135], v[68:71], v[88:91], a[132:135]\n" \
"buffer_store_dwordx4 v[204:207], v20, s[80:83], s88 offen sc1\n" \
"v_mfma_f32_16x16x32_bf16 a[164:167], v[72:75], v[88:91], a[164:167]\n" \
"ds_read_b64_tr_b16 v[176:177], v39 offset:8192\n" \
"s_lshl_b32 s4, s4, 6\n" \
"v_mfma_f32_16x16x32_bf16 a[136:139], v[68:71], v[92:95], a[136:139]\n" \
"ds_read_b64_tr_b16 v[178:179], v39 offset:9216\n" \
"v_writelane_b32 v11, s5, 7\n" \
"v_mfma_f32_16x16x32_bf16 a[168:171], v[72:75], v[92:95], a[168:171]\n" \
"s_mul_i32 s5, s3, 0x1200\n" \
"s_add_i32 s5, s5, s6\n" \
"v_mfma_f32_16x16x32_bf16 a[140:143], v[68:71], v[96:99], a[140:143]\n" \
"s_add_i32 s5, s5, s44\n" \
"v_writelane_b32 v11, s5, 8\n" \
"v_mfma_f32_16x16x32_bf16 a[172:175], v[72:75], v[96:99], a[172:175]\n" \
"ds_read_b128 v[124:127], v29 offset:20480\n" \
"s_lshl_b32 s5, s2, 16\n" \
"v_mfma_f32_16x16x32_bf16 a[144:147], v[68:71], v[100:103], a[144:147]\n" \
"s_add_i32 s6, s5, 0x10000\n" \
"s_and_b32 s6, s6, 0x70000\n" \
"v_mfma_f32_16x16x32_bf16 a[176:179], v[72:75], v[100:103], a[176:179]\n" \
"buffer_store_dwordx4 v[208:211], v20, s[80:83], s36 offen sc1\n" \
"v_mfma_f32_16x16x32_bf16 a[148:151], v[68:71], v[104:107], a[148:151]\n" \
"ds_read_b128 v[128:131], v29 offset:22528\n" \
"s_add_i32 s6, s46, s6\n" \
"v_mfma_f32_16x16x32_bf16 a[180:183], v[72:75], v[104:107], a[180:183]\n" \
"ds_read_b128 v[132:135], v29 offset:0\n" \
"v_writelane_b32 v11, s6, 45\n" \
"v_mfma_f32_16x16x32_bf16 a[152:155], v[68:71], v[108:111], a[152:155]\n" \
"s_add_i32 s6, s5, 0x20000\n" \
"s_and_b32 s6, s6, 0x70000\n" \
"v_mfma_f32_16x16x32_bf16 a[184:187], v[72:75], v[108:111], a[184:187]\n" \
"ds_read_b128 v[136:139], v29 offset:2048\n" \
"s_add_i32 s6, s46, s6\n" \
"v_mfma_f32_16x16x32_bf16 a[156:159], v[68:71], v[112:115], a[156:159]\n" \
"s_bitset1_b32 s6, 11\n" \
"v_writelane_b32 v11, s6, 46\n" \
"v_mfma_f32_16x16x32_bf16 a[188:191], v[72:75], v[112:115], a[188:191]\n" \
"ds_read_b128 v[140:143], v29 offset:4096\n" \
"s_add_i32 s6, s5, 0x30000\n" \
"v_mfma_f32_16x16x32_bf16 a[220:223], v[76:79], v[112:115], a[220:223]\n" \
"s_and_b32 s6, s6, 0x70000\n" \
"s_add_i32 s6, s46, s6\n" \
"v_mfma_f32_16x16x32_bf16 a[252:255], v[80:83], v[112:115], a[252:255]\n" \
"ds_read_b128 v[144:147], v29 offset:6144\n" \
"s_bitset1_b32 s6, 12\n" \
"v_mfma_f32_16x16x32_bf16 a[216:219], v[76:79], v[108:111], a[216:219]\n" \
"v_writelane_b32 v11, s6, 47\n" \
"s_and_b32 s6, s5, 0x70000\n" \
"v_mfma_f32_16x16x32_bf16 a[248:251], v[80:83], v[108:111], a[248:251]\n" \
"s_xor_b32 s6, s6, 0x40000\n" \
"s_add_i32 s6, s46, s6\n" \
"v_mfma_f32_16x16x32_bf16 a[212:215], v[76:79], v[104:107], a[212:215]\n" \
"s_or_b32 s6, s6, 0x1800\n" \
"v_writelane_b32 v11, s6, 48\n" \
"v_mfma_f32_16x16x32_bf16 a[244:247], v[80:83], v[104:107], a[244:247]\n" \
"s_add_i32 s6, s5, 0x50000\n" \
"s_and_b32 s6, s6, 0x70000\n" \
"v_mfma_f32_16x16x32_bf16 a[208:211], v[76:79], v[100:103], a[208:211]\n" \
"s_add_i32 s6, s46, s6\n" \
"s_bitset1_b32 s6, 13\n" \
"v_mfma_f32_16x16x32_bf16 a[240:243], v[80:83], v[100:103], a[240:243]\n" \
"v_writelane_b32 v11, s6, 49\n" \
"s_add_i32 s6, s5, 0x60000\n" \
"v_mfma_f32_16x16x32_bf16 a[204:207], v[76:79], v[96:99], a[204:207]\n" \
"s_and_b32 s6, s6, 0x70000\n" \
"s_add_i32 s5, s5, 0x70000\n" \
"v_mfma_f32_16x16x32_bf16 a[236:239], v[80:83], v[96:99], a[236:239]\n" \
"s_add_i32 s6, s46, s6\n" \
"s_and_b32 s5, s5, 0x70000\n" \
"v_mfma_f32_16x16x32_bf16 a[200:203], v[76:79], v[92:95], a[200:203]\n" \
"v_lshrrev_b32_e32 v213, 2, v20\n" \
"v_and_or_b32 v214, v20, 16, v222\n" \
"v_mfma_f32_16x16x32_bf16 a[232:235], v[80:83], v[92:95], a[232:235]\n" \
"s_or_b32 s6, s6, 0x2800\n" \
"v_writelane_b32 v11, s6, 50\n" \
"v_mfma_f32_16x16x32_bf16 a[196:199], v[76:79], v[88:91], a[196:199]\n" \
"s_add_i32 s46, s46, s5\n" \
"v_and_or_b32 v213, v213, 60, s4\n" \
"v_mfma_f32_16x16x32_bf16 a[228:231], v[80:83], v[88:91], a[228:231]\n" \
"v_lshl_or_b32 v212, s3, 9, v212\n" \
"v_add_u32_e32 v9, 0x4800, v214\n" \
"v_mfma_f32_16x16x32_bf16 a[192:195], v[76:79], v[84:87], a[192:195]\n" \
"s_or_b32 s5, s46, 0x3000\n" \
"v_writelane_b32 v11, s5, 51\n" \
"v_mfma_f32_16x16x32_bf16 a[224:227], v[80:83], v[84:87], a[224:227]\n" \
"s_waitcnt vmcnt(4) lgkmcnt(0)\n" \
"s_barrier\n" \
"v_mfma_f32_16x16x32_bf16 a[0:3], v[116:119], v[148:151], a[0:3]\n" \
"s_add_u32 m0, s30, 0x0\n" \
"s_mov_b32 s85, 0x300\n" \
"v_mfma_f32_16x16x32_bf16 a[32:35], v[120:123], v[148:151], a[32:35]\n" \
"buffer_load_dwordx4 v22, s[64:67], s85 offen lds\n" \
"v_mfma_f32_16x16x32_bf16 a[4:7], v[116:119], v[152:155], a[4:7]\n" \
"ds_read_b64_tr_b16 v[84:85], v32 offset:32768\n" \
"v_or_b32_e32 v18, 0x27000, v213\n" \
"v_mfma_f32_16x16x32_bf16 a[36:39], v[120:123], v[152:155], a[36:39]\n" \
"ds_read_b64_tr_b16 v[86:87], v32 offset:33792\n" \
"v_or_b32_e32 v19, 0x27800, v212\n" \
"v_mfma_f32_16x16x32_bf16 a[8:11], v[116:119], v[156:159], a[8:11]\n" \
"s_add_u32 m0, s30, 0x400\n" \
"s_mov_b32 s84, 0x2300\n" \
"v_mfma_f32_16x16x32_bf16 a[40:43], v[120:123], v[156:159], a[40:43]\n" \
"buffer_load_dwordx4 v23, s[64:67], s84 offen lds\n" \
"v_mfma_f32_16x16x32_bf16 a[12:15], v[116:119], v[160:163], a[12:15]\n" \
"ds_read_b128 v[52:55], v28 offset:49152\n" \
"v_mov_b32_e32 v219, 0x1000c0c\n" \
"v_mfma_f32_16x16x32_bf16 a[44:47], v[120:123], v[160:163], a[44:47]\n" \
"ds_read_b128 v[56:59], v28 offset:51200\n" \
"v_mov_b32_e32 v220, 0xc0c0100\n" \
"v_mfma_f32_16x16x32_bf16 a[16:19], v[116:119], v[164:167], a[16:19]\n" \
"s_add_u32 m0, s31, 0x0\n" \
"s_mov_b32 s85, 0x300000\n" \
"v_mfma_f32_16x16x32_bf16 a[48:51], v[120:123], v[164:167], a[48:51]\n" \
"buffer_load_dwordx4 v26, s[68:71], s85 offen lds\n" \
"v_mfma_f32_16x16x32_bf16 a[20:23], v[116:119], v[168:171], a[20:23]\n" \
"ds_read_b64_tr_b16 v[88:89], v33 offset:32768\n" \
"v_bfe_u32 v218, v20, 4, 1\n" \
"v_mfma_f32_16x16x32_bf16 a[52:55], v[120:123], v[168:171], a[52:55]\n" \
"ds_read_b64_tr_b16 v[90:91], v33 offset:33792\n" \
"v_lshrrev_b32_e32 v213, 8, v20\n" \
"v_mfma_f32_16x16x32_bf16 a[24:27], v[116:119], v[172:175], a[24:27]\n" \
"s_add_u32 m0, s31, 0x4000\n" \
"s_mov_b32 s84, 0x300000\n" \
"v_mfma_f32_16x16x32_bf16 a[56:59], v[120:123], v[172:175], a[56:59]\n" \
"buffer_load_dwordx4 v26, s[72:75], s84 offen lds\n" \
"v_mfma_f32_16x16x32_bf16 a[28:31], v[116:119], v[176:179], a[28:31]\n" \
"ds_read_b64_tr_b16 v[92:93], v34 offset:32768\n" \
"v_cmp_eq_u32_e64 vcc, 0, v218\n" \
"v_mfma_f32_16x16x32_bf16 a[60:63], v[120:123], v[176:179], a[60:63]\n" \
"ds_read_b64_tr_b16 v[94:95], v34 offset:33792\n" \
"s_lshr_b32 s4, s2, 5\n" \
"v_mfma_f32_16x16x32_bf16 a[92:95], v[124:127], v[176:179], a[92:95]\n" \
"s_add_u32 m0, s30, 0x800\n" \
"s_mov_b32 s85, 0x4300\n" \
"v_mfma_f32_16x16x32_bf16 a[124:127], v[128:131], v[176:179], a[124:127]\n" \
"buffer_load_dwordx4 v22, s[64:67], s85 offen lds\n" \
"v_mfma_f32_16x16x32_bf16 a[88:91], v[124:127], v[172:175], a[88:91]\n" \
"ds_read_b64_tr_b16 v[96:97], v35 offset:32768\n" \
"s_and_b32 s5, s3, 1\n" \
"v_mfma_f32_16x16x32_bf16 a[120:123], v[128:131], v[172:175], a[120:123]\n" \
"ds_read_b64_tr_b16 v[98:99], v35 offset:33792\n" \
"v_bfe_u32 v217, v20, 4, 4\n" \
"v_mfma_f32_16x16x32_bf16 a[84:87], v[124:127], v[168:171], a[84:87]\n" \
"s_add_u32 m0, s30, 0xc00\n" \
"s_mov_b32 s84, 0x6300\n" \
"v_mfma_f32_16x16x32_bf16 a[116:119], v[128:131], v[168:171], a[116:119]\n" \
"buffer_load_dwordx4 v23, s[64:67], s84 offen lds\n" \
"v_mfma_f32_16x16x32_bf16 a[80:83], v[124:127], v[164:167], a[80:83]\n" \
"ds_read_b64_tr_b16 v[100:101], v36 offset:32768\n" \
"v_cndmask_b32_e64 v219, v219, v220, vcc\n" \
"v_mfma_f32_16x16x32_bf16 a[112:115], v[128:131], v[164:167], a[112:115]\n" \
"ds_read_b64_tr_b16 v[102:103], v36 offset:33792\n" \
"v_mov_b32_e32 v220, 0x3020c0c\n" \
"v_mfma_f32_16x16x32_bf16 a[76:79], v[124:127], v[160:163], a[76:79]\n" \
"s_add_u32 m0, s31, 0x400\n" \
"s_mov_b32 s85, 0x308000\n" \
"v_mfma_f32_16x16x32_bf16 a[108:111], v[128:131], v[160:163], a[108:111]\n" \
"buffer_load_dwordx4 v26, s[68:71], s85 offen lds\n" \
"v_mfma_f32_16x16x32_bf16 a[72:75], v[124:127], v[156:159], a[72:75]\n" \
"ds_read_b64_tr_b16 v[104:105], v37 offset:32768\n" \
"v_mov_b32_e32 v221, 0xc0c0302\n" \
"v_mfma_f32_16x16x32_bf16 a[104:107], v[128:131], v[156:159], a[104:107]\n" \
"v_lshl_or_b32 v218, v213, 2, v218\n" \
"s_xor_b32 s6, s4, 15\n" \
"s_nop 0\n" \
"v_mfma_f32_16x16x32_bf16 a[68:71], v[124:127], v[152:155], a[68:71]\n" \
"s_add_u32 m0, s31, 0x4400\n" \
"s_mov_b32 s84, 0x308000\n" \
"v_mfma_f32_16x16x32_bf16 a[100:103], v[128:131], v[152:155], a[100:103]\n" \
"buffer_load_dwordx4 v26, s[72:75], s84 offen lds\n" \
"v_mfma_f32_16x16x32_bf16 a[64:67], v[124:127], v[148:151], a[64:67]\n" \
"ds_read_b64_tr_b16 v[106:107], v37 offset:33792\n" \
"v_mfma_f32_16x16x32_bf16 a[96:99], v[128:131], v[148:151], a[96:99]\n" \
"ds_read_b64_tr_b16 v[108:109], v38 offset:32768\n" \
"v_mfma_f32_16x16x32_bf16 a[128:131], v[132:135], v[148:151], a[128:131]\n" \
"s_add_u32 m0, s30, 0x1000\n" \
"s_mov_b32 s85, 0x8300\n" \
"v_mfma_f32_16x16x32_bf16 a[160:163], v[136:139], v[148:151], a[160:163]\n" \
"buffer_load_dwordx4 v22, s[64:67], s85 offen lds\n" \
"v_mfma_f32_16x16x32_bf16 a[132:135], v[132:135], v[152:155], a[132:135]\n" \
"ds_read_b64_tr_b16 v[110:111], v38 offset:33792\n" \
"v_mfma_f32_16x16x32_bf16 a[164:167], v[136:139], v[152:155], a[164:167]\n" \
"ds_read_b64_tr_b16 v[112:113], v39 offset:32768\n" \
"v_mfma_f32_16x16x32_bf16 a[136:139], v[132:135], v[156:159], a[136:139]\n" \
"s_add_u32 m0, s30, 0x1400\n" \
"s_mov_b32 s84, 0xa300\n" \
"v_mfma_f32_16x16x32_bf16 a[168:171], v[136:139], v[156:159], a[168:171]\n" \
"buffer_load_dwordx4 v23, s[64:67], s84 offen lds\n" \
"v_mfma_f32_16x16x32_bf16 a[140:143], v[132:135], v[160:163], a[140:143]\n" \
"ds_read_b64_tr_b16 v[114:115], v39 offset:33792\n" \
"v_mfma_f32_16x16x32_bf16 a[172:175], v[136:139], v[160:163], a[172:175]\n" \
"ds_read_b128 v[60:63], v28 offset:53248\n" \
"v_mfma_f32_16x16x32_bf16 a[144:147], v[132:135], v[164:167], a[144:147]\n" \
"s_add_u32 m0, s31, 0x800\n" \
"s_mov_b32 s85, 0x310000\n" \
"v_mfma_f32_16x16x32_bf16 a[176:179], v[136:139], v[164:167], a[176:179]\n" \
"buffer_load_dwordx4 v27, s[68:71], s85 offen lds\n" \
"v_mfma_f32_16x16x32_bf16 a[148:151], v[132:135], v[168:171], a[148:151]\n" \
"ds_read_b128 v[64:67], v28 offset:55296\n" \
"v_mfma_f32_16x16x32_bf16 a[180:183], v[136:139], v[168:171], a[180:183]\n" \
"ds_read_b128 v[68:71], v28 offset:32768\n" \
"v_mfma_f32_16x16x32_bf16 a[152:155], v[132:135], v[172:175], a[152:155]\n" \
"s_add_u32 m0, s31, 0x4800\n" \
"s_mov_b32 s84, 0x310000\n" \
"v_mfma_f32_16x16x32_bf16 a[184:187], v[136:139], v[172:175], a[184:187]\n" \
"buffer_load_dwordx4 v27, s[72:75], s84 offen lds\n" \
"v_mfma_f32_16x16x32_bf16 a[156:159], v[132:135], v[176:179], a[156:159]\n" \
"ds_read_b128 v[72:75], v28 offset:34816\n" \
"v_mfma_f32_16x16x32_bf16 a[188:191], v[136:139], v[176:179], a[188:191]\n" \
"ds_read_b128 v[76:79], v28 offset:36864\n" \
"v_mfma_f32_16x16x32_bf16 a[220:223], v[140:143], v[176:179], a[220:223]\n" \
"s_cmpk_lt_u32 s3, 2\n" \
"s_cselect_b32 s7, s6, s4\n" \
"v_mfma_f32_16x16x32_bf16 a[252:255], v[144:147], v[176:179], a[252:255]\n" \
"ds_read_b128 v[80:83], v28 offset:38912\n" \
"s_lshl_b32 s40, s7, 7\n" \
"v_mfma_f32_16x16x32_bf16 a[216:219], v[140:143], v[172:175], a[216:219]\n" \
"s_lshl_b32 s41, s5, 6\n" \
"v_cndmask_b32_e64 v220, v220, v221, vcc\n" \
"v_mfma_f32_16x16x32_bf16 a[248:251], v[144:147], v[172:175], a[248:251]\n" \
"v_mov_b32_e32 v221, 0xc0c0c0c\n" \
"v_cmp_gt_u32_e64 vcc, v218, v217\n" \
"v_mfma_f32_16x16x32_bf16 a[212:215], v[140:143], v[168:171], a[212:215]\n" \
"v_or_b32_e32 v218, 2, v218\n" \
"v_lshrrev_b32_e32 v212, 7, v20\n" \
"v_mfma_f32_16x16x32_bf16 a[244:247], v[144:147], v[168:171], a[244:247]\n" \
"s_or_b32 s41, s41, s40\n" \
"v_cndmask_b32_e32 v40, v221, v219, vcc\n" \
"v_mfma_f32_16x16x32_bf16 a[208:211], v[140:143], v[164:167], a[208:211]\n" \
"v_cndmask_b32_e32 v42, v219, v221, vcc\n" \
"v_cmp_gt_u32_e32 vcc, v218, v217\n" \
"v_mfma_f32_16x16x32_bf16 a[240:243], v[144:147], v[164:167], a[240:243]\n" \
"v_and_b32_e32 v218, 16, v20\n" \
"s_movk_i32 s42, 0x3f80\n" \
"v_mfma_f32_16x16x32_bf16 a[204:207], v[140:143], v[160:163], a[204:207]\n" \
"v_or_b32_e64 v214, s41, v212\n" \
"v_and_b32_e32 v217, 0x320, v20\n" \
"v_mfma_f32_16x16x32_bf16 a[236:239], v[144:147], v[160:163], a[236:239]\n" \
"s_movk_i32 s41, 0x100\n" \
"v_lshlrev_b32_e64 v218, v218, s42\n" \
"v_mfma_f32_16x16x32_bf16 a[200:203], v[140:143], v[156:159], a[200:203]\n" \
"v_mov_b32_e32 v219, 0\n" \
"v_lshrrev_b32_e64 v215, 6, v20\n" \
"v_mfma_f32_16x16x32_bf16 a[232:235], v[144:147], v[156:159], a[232:235]\n" \
"v_cndmask_b32_e32 v41, v221, v220, vcc\n" \
"v_cndmask_b32_e32 v43, v220, v221, vcc\n" \
"v_mfma_f32_16x16x32_bf16 a[196:199], v[140:143], v[152:155], a[196:199]\n" \
"v_cmp_eq_u32_e64 vcc, s41, v217\n" \
"v_and_b32_e32 v217, 0x330, v20\n" \
"v_mfma_f32_16x16x32_bf16 a[228:231], v[144:147], v[152:155], a[228:231]\n" \
"v_bfe_u32 v216, v20, 6, 2\n" \
"v_cndmask_b32_e64 v46, v219, v218, vcc\n" \
"v_mfma_f32_16x16x32_bf16 a[192:195], v[140:143], v[148:151], a[192:195]\n" \
"v_mov_b32_e32 v218, 0x3f80\n" \
"v_cmp_eq_u32_e64 vcc, 32, v217\n" \
"v_mfma_f32_16x16x32_bf16 a[224:227], v[144:147], v[148:151], a[224:227]\n" \
"s_waitcnt lgkmcnt(15)\n" \
"v_mfma_f32_16x16x32_bf16 a[0:3], v[52:55], v[84:87], a[0:3]\n" \
"s_add_u32 m0, s30, 0x1800\n" \
"s_mov_b32 s85, 0xc300\n" \
"v_mfma_f32_16x16x32_bf16 a[32:35], v[56:59], v[84:87], a[32:35]\n" \
"buffer_load_dwordx4 v22, s[64:67], s85 offen lds\n" \
"v_mfma_f32_16x16x32_bf16 a[4:7], v[52:55], v[88:91], a[4:7]\n" \
"ds_read_b64_tr_b16 v[148:149], v32 offset:40960\n" \
"s_add_i32 s41, s40, -16\n" \
"v_mfma_f32_16x16x32_bf16 a[36:39], v[56:59], v[88:91], a[36:39]\n" \
"s_add_u32 m0, s30, 0x1c00\n" \
"s_mov_b32 s84, 0xe300\n" \
"v_mfma_f32_16x16x32_bf16 a[8:11], v[52:55], v[92:95], a[8:11]\n" \
"buffer_load_dwordx4 v23, s[64:67], s84 offen lds\n" \
"v_mfma_f32_16x16x32_bf16 a[40:43], v[56:59], v[92:95], a[40:43]\n" \
"ds_read_b64_tr_b16 v[150:151], v32 offset:41984\n" \
"v_lshrrev_b32_e32 v219, 1, v20\n" \
"s_waitcnt lgkmcnt(15)\n" \
"v_mfma_f32_16x16x32_bf16 a[12:15], v[52:55], v[96:99], a[12:15]\n" \
"s_add_u32 m0, s31, 0xc00\n" \
"s_mov_b32 s85, 0x318000\n" \
"v_mfma_f32_16x16x32_bf16 a[44:47], v[56:59], v[96:99], a[44:47]\n" \
"buffer_load_dwordx4 v27, s[68:71], s85 offen lds\n" \
"s_waitcnt lgkmcnt(14)\n" \
"v_mfma_f32_16x16x32_bf16 a[16:19], v[52:55], v[100:103], a[16:19]\n" \
"ds_read_b128 v[116:119], v29 offset:49152\n" \
"v_bfe_u32 v215, v215, 1, 1\n" \
"v_mfma_f32_16x16x32_bf16 a[48:51], v[56:59], v[100:103], a[48:51]\n" \
"s_add_u32 m0, s31, 0x4c00\n" \
"s_mov_b32 s84, 0x318000\n" \
"s_waitcnt lgkmcnt(13)\n" \
"v_mfma_f32_16x16x32_bf16 a[20:23], v[52:55], v[104:107], a[20:23]\n" \
"buffer_load_dwordx4 v27, s[72:75], s84 offen lds\n" \
"v_mfma_f32_16x16x32_bf16 a[52:55], v[56:59], v[104:107], a[52:55]\n" \
"ds_read_b128 v[120:123], v29 offset:51200\n" \
"v_cndmask_b32_e32 v47, 0, v218, vcc\n" \
"s_waitcnt lgkmcnt(12)\n" \
"v_mfma_f32_16x16x32_bf16 a[24:27], v[52:55], v[108:111], a[24:27]\n" \
"ds_read_b64_tr_b16 v[152:153], v33 offset:40960\n" \
"s_mov_b32 s42, 0\n" \
"v_mfma_f32_16x16x32_bf16 a[56:59], v[56:59], v[108:111], a[56:59]\n" \
"ds_read_b64_tr_b16 v[154:155], v33 offset:41984\n" \
"s_waitcnt lgkmcnt(12)\n" \
"v_mfma_f32_16x16x32_bf16 a[28:31], v[52:55], v[112:115], a[28:31]\n" \
"ds_read_b64_tr_b16 v[156:157], v34 offset:40960\n" \
"v_mfma_f32_16x16x32_bf16 a[60:63], v[56:59], v[112:115], a[60:63]\n" \
"ds_read_b64_tr_b16 v[158:159], v34 offset:41984\n" \
"s_waitcnt lgkmcnt(13)\n" \
"v_mfma_f32_16x16x32_bf16 a[92:95], v[60:63], v[112:115], a[92:95]\n" \
"s_cmp_lg_u32 s40, 0\n" \
"s_cselect_b32 s41, s41, s42\n" \
"s_waitcnt lgkmcnt(12)\n" \
"v_mfma_f32_16x16x32_bf16 a[124:127], v[64:67], v[112:115], a[124:127]\n" \
"ds_read_b64_tr_b16 v[160:161], v35 offset:40960\n" \
"v_lshl_or_b32 v217, s5, 3, v212\n" \
"v_mfma_f32_16x16x32_bf16 a[88:91], v[60:63], v[108:111], a[88:91]\n" \
"v_lshlrev_b32_e32 v45, 3, v20\n" \
"v_add_u32_e32 v45, s31, v45\n" \
"v_mfma_f32_16x16x32_bf16 a[120:123], v[64:67], v[108:111], a[120:123]\n" \
"s_getpc_b64 s[86:87]\n" \
"s_sub_u32 s86, s86, 0x10800\n" \
"s_subb_u32 s87, s87, 0\n" \
"s_mov_b32 m0, s32\n" \
"v_mfma_f32_16x16x32_bf16 a[84:87], v[60:63], v[104:107], a[84:87]\n" \
"global_load_lds_dword v45, s[86:87]\n" \
"v_mfma_f32_16x16x32_bf16 a[116:119], v[64:67], v[104:107], a[116:119]\n" \
"ds_read_b64_tr_b16 v[162:163], v35 offset:41984\n" \
"v_lshlrev_b32_e32 v218, 10, v213\n" \
"v_mfma_f32_16x16x32_bf16 a[80:83], v[60:63], v[100:103], a[80:83]\n" \
"ds_read_b64_tr_b16 v[164:165], v36 offset:40960\n" \
"v_lshlrev_b32_e64 v216, 7, v216\n" \
"v_mfma_f32_16x16x32_bf16 a[112:115], v[64:67], v[100:103], a[112:115]\n" \
"ds_read_b64_tr_b16 v[166:167], v36 offset:41984\n" \
"v_and_b32_e64 v219, 24, v219\n" \
"v_mfma_f32_16x16x32_bf16 a[76:79], v[60:63], v[96:99], a[76:79]\n" \
"v_and_or_b32 v212, v212, 2, v215\n" \
"v_add_u32_e64 v217, s41, v217\n" \
"v_mfma_f32_16x16x32_bf16 a[108:111], v[64:67], v[96:99], a[108:111]\n" \
"v_or3_b32 v216, v216, v218, v219\n" \
"v_lshlrev_b32_e64 v212, 5, v212\n" \
"v_mfma_f32_16x16x32_bf16 a[72:75], v[60:63], v[92:95], a[72:75]\n" \
"ds_read_b64_tr_b16 v[168:169], v37 offset:40960\n" \
"s_mov_b32 s41, 0x14000\n" \
"v_mfma_f32_16x16x32_bf16 a[104:107], v[64:67], v[92:95], a[104:107]\n" \
"v_or3_b32 v190, v216, v212, s41\n" \
"s_lshl_b32 s41, s5, 2\n" \
"v_mfma_f32_16x16x32_bf16 a[68:71], v[60:63], v[88:91], a[68:71]\n" \
"ds_read_b64_tr_b16 v[170:171], v37 offset:41984\n" \
"s_lshl_b32 s7, s7, 3\n" \
"v_mfma_f32_16x16x32_bf16 a[100:103], v[64:67], v[88:91], a[100:103]\n" \
"v_or_b32_e32 v212, s41, v213\n" \
"s_or_b32 s7, s7, s41\n" \
"v_mfma_f32_16x16x32_bf16 a[64:67], v[60:63], v[84:87], a[64:67]\n" \
"ds_read_b64_tr_b16 v[172:173], v38 offset:40960\n" \
"s_mov_b32 s41, 0xff800000\n" \
"v_mfma_f32_16x16x32_bf16 a[96:99], v[64:67], v[84:87], a[96:99]\n" \
"s_cmp_lg_u32 s40, 0\n" \
"s_cselect_b32 s47, s42, s41\n" \
"s_waitcnt lgkmcnt(15)\n" \
"v_mfma_f32_16x16x32_bf16 a[128:131], v[68:71], v[84:87], a[128:131]\n" \
"ds_read_b64_tr_b16 v[174:175], v38 offset:41984\n" \
"s_mov_b32 s40, 0xc000\n" \
"v_mfma_f32_16x16x32_bf16 a[160:163], v[72:75], v[84:87], a[160:163]\n" \
"s_mov_b32 s41, 0x8000\n" \
"v_mfma_f32_16x16x32_bf16 a[132:135], v[68:71], v[88:91], a[132:135]\n" \
"ds_read_b64_tr_b16 v[176:177], v39 offset:40960\n" \
"v_mfma_f32_16x16x32_bf16 a[164:167], v[72:75], v[88:91], a[164:167]\n" \
"s_cmp_lg_u32 s4, 0\n" \
"s_cselect_b32 s51, s40, s41\n" \
"v_mfma_f32_16x16x32_bf16 a[136:139], v[68:71], v[92:95], a[136:139]\n" \
"ds_read_b64_tr_b16 v[178:179], v39 offset:41984\n" \
"s_movk_i32 s40, 0x4000\n" \
"v_mfma_f32_16x16x32_bf16 a[168:171], v[72:75], v[92:95], a[168:171]\n" \
"s_cmp_lg_u32 s4, 0\n" \
"s_cselect_b32 s53, s41, s40\n" \
"v_mfma_f32_16x16x32_bf16 a[140:143], v[68:71], v[96:99], a[140:143]\n" \
"s_lshl_b32 s40, s2, 2\n" \
"s_and_b32 s40, s40, 28\n" \
"v_mfma_f32_16x16x32_bf16 a[172:175], v[72:75], v[96:99], a[172:175]\n" \
"ds_read_b128 v[124:127], v29 offset:53248\n" \
"s_bfe_u32 s45, s2, 0x20003\n" \
"v_mfma_f32_16x16x32_bf16 a[144:147], v[68:71], v[100:103], a[144:147]\n" \
"s_lshr_b32 s44, s3, 1\n" \
"s_or_b32 s46, s40, s45\n" \
"v_mfma_f32_16x16x32_bf16 a[176:179], v[72:75], v[100:103], a[176:179]\n" \
"ds_read_b128 v[128:131], v29 offset:55296\n" \
"s_lshl_b32 s43, s44, 15\n" \
"v_mfma_f32_16x16x32_bf16 a[148:151], v[68:71], v[104:107], a[148:151]\n" \
"s_lshl_b32 s41, s5, 14\n" \
"s_lshl_b32 s48, s46, 20\n" \
"v_mfma_f32_16x16x32_bf16 a[180:183], v[72:75], v[104:107], a[180:183]\n" \
"ds_read_b128 v[132:135], v29 offset:32768\n" \
"s_or_b32 s41, s43, s41\n" \
"v_mfma_f32_16x16x32_bf16 a[152:155], v[68:71], v[108:111], a[152:155]\n" \
"s_add_i32 s49, s48, 0x1000000\n" \
"s_or_b32 s42, s49, s41\n" \
"v_mfma_f32_16x16x32_bf16 a[184:187], v[72:75], v[108:111], a[184:187]\n" \
"ds_read_b128 v[136:139], v29 offset:34816\n" \
"s_lshl_b32 s4, s4, 16\n" \
"v_mfma_f32_16x16x32_bf16 a[156:159], v[68:71], v[112:115], a[156:159]\n" \
"s_or_b32 s41, s42, s4\n" \
"s_lshl_b32 s4, s6, 16\n" \
"v_mfma_f32_16x16x32_bf16 a[188:191], v[72:75], v[112:115], a[188:191]\n" \
"ds_read_b128 v[140:143], v29 offset:36864\n" \
"s_or_b32 s42, s42, s4\n" \
"v_mfma_f32_16x16x32_bf16 a[220:223], v[76:79], v[112:115], a[220:223]\n" \
"s_lshl_b32 s4, s5, 11\n" \
"s_mul_i32 s5, s7, 0x24800\n" \
"v_mfma_f32_16x16x32_bf16 a[252:255], v[80:83], v[112:115], a[252:255]\n" \
"ds_read_b128 v[144:147], v29 offset:38912\n" \
"s_lshl_b32 s6, s46, 9\n" \
"v_mfma_f32_16x16x32_bf16 a[216:219], v[76:79], v[108:111], a[216:219]\n" \
"s_lshl_b32 s7, s40, 6\n" \
"s_add_i32 s6, s5, s6\n" \
"v_mfma_f32_16x16x32_bf16 a[248:251], v[80:83], v[108:111], a[248:251]\n" \
"s_or_b32 s5, s5, s7\n" \
"s_lshl_b32 s7, s45, 2\n" \
"v_mfma_f32_16x16x32_bf16 a[212:215], v[76:79], v[104:107], a[212:215]\n" \
"s_or_b32 s5, s5, s7\n" \
"s_lshl_b32 s7, s3, 12\n" \
"v_mfma_f32_16x16x32_bf16 a[244:247], v[80:83], v[104:107], a[244:247]\n" \
"s_or_b32 s50, s49, s7\n" \
"s_lshl_b32 s7, s3, 11\n" \
"v_mfma_f32_16x16x32_bf16 a[208:211], v[76:79], v[100:103], a[208:211]\n" \
"v_lshrrev_b32_e32 v215, 4, v20\n" \
"v_bfe_u32 v216, v20, 4, 3\n" \
"v_mfma_f32_16x16x32_bf16 a[240:243], v[80:83], v[100:103], a[240:243]\n" \
"s_add_i32 s52, s48, s7\n" \
"s_add_i32 s53, s53, s7\n" \
"v_mfma_f32_16x16x32_bf16 a[204:207], v[76:79], v[96:99], a[204:207]\n" \
"v_mul_u32_u24_e32 v214, 0x480, v214\n" \
"v_bitop3_b32 v218, v213, v215, 7 bitop3:0x78\n" \
"v_mfma_f32_16x16x32_bf16 a[236:239], v[80:83], v[96:99], a[236:239]\n" \
"v_bitop3_b32 v213, v213, v216, 4 bitop3:0x36\n" \
"s_lshl_b32 s7, s3, 10\n" \
"v_mfma_f32_16x16x32_bf16 a[200:203], v[76:79], v[92:95], a[200:203]\n" \
"s_or_b32 s4, s43, s4\n" \
"v_lshl_or_b32 v25, v213, 4, v214\n" \
"v_mfma_f32_16x16x32_bf16 a[232:235], v[80:83], v[92:95], a[232:235]\n" \
"v_mul_u32_u24_e32 v213, 0x480, v217\n" \
"v_bitop3_b32 v212, v215, v212, 7 bitop3:0x6c\n" \
"v_mfma_f32_16x16x32_bf16 a[196:199], v[76:79], v[88:91], a[196:199]\n" \
"s_or_b32 s40, s7, 0x14000\n" \
"s_lshl_b32 s43, s44, 13\n" \
"v_mfma_f32_16x16x32_bf16 a[228:231], v[80:83], v[88:91], a[228:231]\n" \
"s_mulk_i32 s44, 0xe800\n" \
"s_lshl_b32 s7, s3, 8\n" \
"v_mfma_f32_16x16x32_bf16 a[192:195], v[76:79], v[84:87], a[192:195]\n" \
"v_lshl_or_b32 v24, v218, 4, v214\n" \
"v_lshl_or_b32 v21, v212, 4, v213\n" \
"v_mfma_f32_16x16x32_bf16 a[224:227], v[80:83], v[84:87], a[224:227]\n" \
"s_waitcnt vmcnt(1) lgkmcnt(0)\n" \
"s_barrier\n" \
"v_mov_b32_e32 v45, s62\n" \
"s_mov_b64 exec, 1\n" \
"buffer_store_dword v45, off, s[80:83], s37 offset:128 sc1\n" \
"s_mov_b64 exec, -1\n" \
"v_mfma_f32_16x16x32_bf16 a[0:3], v[116:119], v[148:151], a[0:3]\n" \
"s_add_u32 m0, s30, 0x8000\n" \
"s_mov_b32 s85, 0x380\n" \
"v_mfma_f32_16x16x32_bf16 a[32:35], v[120:123], v[148:151], a[32:35]\n" \
"buffer_load_dwordx4 v22, s[64:67], s85 offen lds\n" \
"v_mfma_f32_16x16x32_bf16 a[4:7], v[116:119], v[152:155], a[4:7]\n" \
"ds_read_b64_tr_b16 v[84:85], v32 offset:0\n" \
"s_add_i32 s44, s44, 0x17000\n" \
"v_mfma_f32_16x16x32_bf16 a[36:39], v[120:123], v[152:155], a[36:39]\n" \
"ds_read_b64_tr_b16 v[86:87], v32 offset:1024\n" \
"s_or_b32 s45, s7, 0x27000\n" \
"v_mfma_f32_16x16x32_bf16 a[8:11], v[116:119], v[156:159], a[8:11]\n" \
"s_add_u32 m0, s30, 0x8400\n" \
"s_mov_b32 s84, 0x2380\n" \
"v_mfma_f32_16x16x32_bf16 a[40:43], v[120:123], v[156:159], a[40:43]\n" \
"buffer_load_dwordx4 v23, s[64:67], s84 offen lds\n" \
"v_mfma_f32_16x16x32_bf16 a[12:15], v[116:119], v[160:163], a[12:15]\n" \
"ds_read_b128 v[52:55], v28 offset:16384\n" \
"s_or_b32 s46, s4, 0x10000\n" \
"v_mfma_f32_16x16x32_bf16 a[44:47], v[120:123], v[160:163], a[44:47]\n" \
"ds_read_b128 v[56:59], v28 offset:18432\n" \
"s_add_i32 s48, s6, 0x3000000\n" \
"v_mfma_f32_16x16x32_bf16 a[16:19], v[116:119], v[164:167], a[16:19]\n" \
"s_add_u32 m0, s31, 0x8000\n" \
"s_mov_b32 s85, 0x380000\n" \
"v_mfma_f32_16x16x32_bf16 a[48:51], v[120:123], v[164:167], a[48:51]\n" \
"buffer_load_dwordx4 v26, s[68:71], s85 offen lds\n" \
"v_mfma_f32_16x16x32_bf16 a[20:23], v[116:119], v[168:171], a[20:23]\n" \
"ds_read_b64_tr_b16 v[88:89], v33 offset:0\n" \
"s_add_i32 s49, s5, 0x3004000\n" \
"v_mfma_f32_16x16x32_bf16 a[52:55], v[120:123], v[168:171], a[52:55]\n" \
"ds_read_b64_tr_b16 v[90:91], v33 offset:1024\n" \
"s_add_i32 s52, s52, 0x1010000\n" \
"v_mfma_f32_16x16x32_bf16 a[24:27], v[116:119], v[172:175], a[24:27]\n" \
"s_add_u32 m0, s31, 0xc000\n" \
"s_mov_b32 s84, 0x380000\n" \
"v_mfma_f32_16x16x32_bf16 a[56:59], v[120:123], v[172:175], a[56:59]\n" \
"buffer_load_dwordx4 v26, s[72:75], s84 offen lds\n" \
"v_mfma_f32_16x16x32_bf16 a[28:31], v[116:119], v[176:179], a[28:31]\n" \
"ds_read_b64_tr_b16 v[92:93], v34 offset:0\n" \
"v_mfma_f32_16x16x32_bf16 a[60:63], v[120:123], v[176:179], a[60:63]\n" \
"ds_read_b64_tr_b16 v[94:95], v34 offset:1024\n" \
"v_mfma_f32_16x16x32_bf16 a[92:95], v[124:127], v[176:179], a[92:95]\n" \
"s_add_u32 m0, s30, 0x8800\n" \
"s_mov_b32 s85, 0x4380\n" \
"v_mfma_f32_16x16x32_bf16 a[124:127], v[128:131], v[176:179], a[124:127]\n" \
"buffer_load_dwordx4 v22, s[64:67], s85 offen lds\n" \
"v_mfma_f32_16x16x32_bf16 a[88:91], v[124:127], v[172:175], a[88:91]\n" \
"ds_read_b64_tr_b16 v[96:97], v35 offset:0\n" \
"v_mfma_f32_16x16x32_bf16 a[120:123], v[128:131], v[172:175], a[120:123]\n" \
"ds_read_b64_tr_b16 v[98:99], v35 offset:1024\n" \
"v_mfma_f32_16x16x32_bf16 a[84:87], v[124:127], v[168:171], a[84:87]\n" \
"s_add_u32 m0, s30, 0x8c00\n" \
"s_mov_b32 s84, 0x6380\n" \
"v_mfma_f32_16x16x32_bf16 a[116:119], v[128:131], v[168:171], a[116:119]\n" \
"buffer_load_dwordx4 v23, s[64:67], s84 offen lds\n" \
"v_mfma_f32_16x16x32_bf16 a[80:83], v[124:127], v[164:167], a[80:83]\n" \
"ds_read_b64_tr_b16 v[100:101], v36 offset:0\n" \
"v_mfma_f32_16x16x32_bf16 a[112:115], v[128:131], v[164:167], a[112:115]\n" \
"ds_read_b64_tr_b16 v[102:103], v36 offset:1024\n" \
"v_mfma_f32_16x16x32_bf16 a[76:79], v[124:127], v[160:163], a[76:79]\n" \
"s_add_u32 m0, s31, 0x8400\n" \
"s_mov_b32 s85, 0x388000\n" \
"v_mfma_f32_16x16x32_bf16 a[108:111], v[128:131], v[160:163], a[108:111]\n" \
"buffer_load_dwordx4 v26, s[68:71], s85 offen lds\n" \
"v_mfma_f32_16x16x32_bf16 a[72:75], v[124:127], v[156:159], a[72:75]\n" \
"ds_read_b64_tr_b16 v[104:105], v37 offset:0\n" \
"v_mfma_f32_16x16x32_bf16 a[104:107], v[128:131], v[156:159], a[104:107]\n" \
"v_mfma_f32_16x16x32_bf16 a[68:71], v[124:127], v[152:155], a[68:71]\n" \
"s_add_u32 m0, s31, 0xc400\n" \
"s_mov_b32 s84, 0x388000\n" \
"v_mfma_f32_16x16x32_bf16 a[100:103], v[128:131], v[152:155], a[100:103]\n" \
"buffer_load_dwordx4 v26, s[72:75], s84 offen lds\n" \
"v_mfma_f32_16x16x32_bf16 a[64:67], v[124:127], v[148:151], a[64:67]\n" \
"ds_read_b64_tr_b16 v[106:107], v37 offset:1024\n" \
"v_mfma_f32_16x16x32_bf16 a[96:99], v[128:131], v[148:151], a[96:99]\n" \
"ds_read_b64_tr_b16 v[108:109], v38 offset:0\n" \
"v_mfma_f32_16x16x32_bf16 a[128:131], v[132:135], v[148:151], a[128:131]\n" \
"s_add_u32 m0, s30, 0x9000\n" \
"s_mov_b32 s85, 0x8380\n" \
"v_mfma_f32_16x16x32_bf16 a[160:163], v[136:139], v[148:151], a[160:163]\n" \
"buffer_load_dwordx4 v22, s[64:67], s85 offen lds\n" \
"v_mfma_f32_16x16x32_bf16 a[132:135], v[132:135], v[152:155], a[132:135]\n" \
"ds_read_b64_tr_b16 v[110:111], v38 offset:1024\n" \
"v_mfma_f32_16x16x32_bf16 a[164:167], v[136:139], v[152:155], a[164:167]\n" \
"ds_read_b64_tr_b16 v[112:113], v39 offset:0\n" \
"v_mfma_f32_16x16x32_bf16 a[136:139], v[132:135], v[156:159], a[136:139]\n" \
"s_add_u32 m0, s30, 0x9400\n" \
"s_mov_b32 s84, 0xa380\n" \
"v_mfma_f32_16x16x32_bf16 a[168:171], v[136:139], v[156:159], a[168:171]\n" \
"buffer_load_dwordx4 v23, s[64:67], s84 offen lds\n" \
"v_mfma_f32_16x16x32_bf16 a[140:143], v[132:135], v[160:163], a[140:143]\n" \
"ds_read_b64_tr_b16 v[114:115], v39 offset:1024\n" \
"v_mfma_f32_16x16x32_bf16 a[172:175], v[136:139], v[160:163], a[172:175]\n" \
"ds_read_b128 v[60:63], v28 offset:20480\n" \
"v_mfma_f32_16x16x32_bf16 a[144:147], v[132:135], v[164:167], a[144:147]\n" \
"s_add_u32 m0, s31, 0x8800\n" \
"s_mov_b32 s85, 0x390000\n" \
"v_mfma_f32_16x16x32_bf16 a[176:179], v[136:139], v[164:167], a[176:179]\n" \
"buffer_load_dwordx4 v27, s[68:71], s85 offen lds\n" \
"v_mfma_f32_16x16x32_bf16 a[148:151], v[132:135], v[168:171], a[148:151]\n" \
"ds_read_b128 v[64:67], v28 offset:22528\n" \
"v_mfma_f32_16x16x32_bf16 a[180:183], v[136:139], v[168:171], a[180:183]\n" \
"ds_read_b128 v[68:71], v28 offset:0\n" \
"v_mfma_f32_16x16x32_bf16 a[152:155], v[132:135], v[172:175], a[152:155]\n" \
"s_add_u32 m0, s31, 0xc800\n" \
"s_mov_b32 s84, 0x390000\n" \
"v_mfma_f32_16x16x32_bf16 a[184:187], v[136:139], v[172:175], a[184:187]\n" \
"buffer_load_dwordx4 v27, s[72:75], s84 offen lds\n" \
"v_mfma_f32_16x16x32_bf16 a[156:159], v[132:135], v[176:179], a[156:159]\n" \
"ds_read_b128 v[72:75], v28 offset:2048\n" \
"v_mfma_f32_16x16x32_bf16 a[188:191], v[136:139], v[176:179], a[188:191]\n" \
"ds_read_b128 v[76:79], v28 offset:4096\n" \
"v_mfma_f32_16x16x32_bf16 a[220:223], v[140:143], v[176:179], a[220:223]\n" \
"v_mfma_f32_16x16x32_bf16 a[252:255], v[144:147], v[176:179], a[252:255]\n" \
"ds_read_b128 v[80:83], v28 offset:6144\n" \
"v_mfma_f32_16x16x32_bf16 a[216:219], v[140:143], v[172:175], a[216:219]\n" \
"v_mfma_f32_16x16x32_bf16 a[248:251], v[144:147], v[172:175], a[248:251]\n" \
"v_mfma_f32_16x16x32_bf16 a[212:215], v[140:143], v[168:171], a[212:215]\n" \
"v_mfma_f32_16x16x32_bf16 a[244:247], v[144:147], v[168:171], a[244:247]\n" \
"v_mfma_f32_16x16x32_bf16 a[208:211], v[140:143], v[164:167], a[208:211]\n" \
"v_mfma_f32_16x16x32_bf16 a[240:243], v[144:147], v[164:167], a[240:243]\n" \
"v_mfma_f32_16x16x32_bf16 a[204:207], v[140:143], v[160:163], a[204:207]\n" \
"v_mfma_f32_16x16x32_bf16 a[236:239], v[144:147], v[160:163], a[236:239]\n" \
"v_mfma_f32_16x16x32_bf16 a[200:203], v[140:143], v[156:159], a[200:203]\n" \
"v_mfma_f32_16x16x32_bf16 a[232:235], v[144:147], v[156:159], a[232:235]\n" \
"v_mfma_f32_16x16x32_bf16 a[196:199], v[140:143], v[152:155], a[196:199]\n" \
"v_mfma_f32_16x16x32_bf16 a[228:231], v[144:147], v[152:155], a[228:231]\n" \
"v_mfma_f32_16x16x32_bf16 a[192:195], v[140:143], v[148:151], a[192:195]\n" \
"v_mfma_f32_16x16x32_bf16 a[224:227], v[144:147], v[148:151], a[224:227]\n" \
"s_waitcnt lgkmcnt(15)\n" \
"s_nop 0\n" \
"v_mfma_f32_16x16x32_bf16 a[0:3], v[52:55], v[84:87], a[0:3]\n" \
"s_add_u32 m0, s30, 0x9800\n" \
"s_mov_b32 s85, 0xc380\n" \
"v_mfma_f32_16x16x32_bf16 a[32:35], v[56:59], v[84:87], a[32:35]\n" \
"buffer_load_dwordx4 v22, s[64:67], s85 offen lds\n" \
"v_mfma_f32_16x16x32_bf16 a[4:7], v[52:55], v[88:91], a[4:7]\n" \
"ds_read_b64_tr_b16 v[148:149], v32 offset:8192\n" \
"v_mfma_f32_16x16x32_bf16 a[36:39], v[56:59], v[88:91], a[36:39]\n" \
"s_add_u32 m0, s30, 0x9c00\n" \
"s_mov_b32 s84, 0xe380\n" \
"v_mfma_f32_16x16x32_bf16 a[8:11], v[52:55], v[92:95], a[8:11]\n" \
"buffer_load_dwordx4 v23, s[64:67], s84 offen lds\n" \
"v_mfma_f32_16x16x32_bf16 a[40:43], v[56:59], v[92:95], a[40:43]\n" \
"ds_read_b64_tr_b16 v[150:151], v32 offset:9216\n" \
"s_waitcnt lgkmcnt(15)\n" \
"v_mfma_f32_16x16x32_bf16 a[12:15], v[52:55], v[96:99], a[12:15]\n" \
"s_add_u32 m0, s31, 0x8c00\n" \
"s_mov_b32 s85, 0x398000\n" \
"v_mfma_f32_16x16x32_bf16 a[44:47], v[56:59], v[96:99], a[44:47]\n" \
"buffer_load_dwordx4 v27, s[68:71], s85 offen lds\n" \
"s_waitcnt lgkmcnt(14)\n" \
"v_mfma_f32_16x16x32_bf16 a[16:19], v[52:55], v[100:103], a[16:19]\n" \
"ds_read_b128 v[116:119], v29 offset:16384\n" \
"v_mfma_f32_16x16x32_bf16 a[48:51], v[56:59], v[100:103], a[48:51]\n" \
"s_add_u32 m0, s31, 0xcc00\n" \
"s_mov_b32 s84, 0x398000\n" \
"s_waitcnt lgkmcnt(13)\n" \
"v_mfma_f32_16x16x32_bf16 a[20:23], v[52:55], v[104:107], a[20:23]\n" \
"buffer_load_dwordx4 v27, s[72:75], s84 offen lds\n" \
"v_mfma_f32_16x16x32_bf16 a[52:55], v[56:59], v[104:107], a[52:55]\n" \
"ds_read_b128 v[120:123], v29 offset:18432\n" \
"s_waitcnt lgkmcnt(12)\n" \
"v_mfma_f32_16x16x32_bf16 a[24:27], v[52:55], v[108:111], a[24:27]\n" \
"ds_read_b64_tr_b16 v[152:153], v33 offset:8192\n" \
"v_mfma_f32_16x16x32_bf16 a[56:59], v[56:59], v[108:111], a[56:59]\n" \
"ds_read_b64_tr_b16 v[154:155], v33 offset:9216\n" \
"s_waitcnt lgkmcnt(12)\n" \
"v_mfma_f32_16x16x32_bf16 a[28:31], v[52:55], v[112:115], a[28:31]\n" \
"ds_read_b64_tr_b16 v[156:157], v34 offset:8192\n" \
"v_mfma_f32_16x16x32_bf16 a[60:63], v[56:59], v[112:115], a[60:63]\n" \
"ds_read_b64_tr_b16 v[158:159], v34 offset:9216\n" \
"s_waitcnt lgkmcnt(13)\n" \
"v_mfma_f32_16x16x32_bf16 a[92:95], v[60:63], v[112:115], a[92:95]\n" \
"s_waitcnt lgkmcnt(12)\n" \
"s_nop 0\n" \
"v_mfma_f32_16x16x32_bf16 a[124:127], v[64:67], v[112:115], a[124:127]\n" \
"ds_read_b64_tr_b16 v[160:161], v35 offset:8192\n" \
"v_mfma_f32_16x16x32_bf16 a[88:91], v[60:63], v[108:111], a[88:91]\n" \
"v_mfma_f32_16x16x32_bf16 a[120:123], v[64:67], v[108:111], a[120:123]\n" \
"ds_read_b64_tr_b16 v[162:163], v35 offset:9216\n" \
"v_mfma_f32_16x16x32_bf16 a[84:87], v[60:63], v[104:107], a[84:87]\n" \
"v_mfma_f32_16x16x32_bf16 a[116:119], v[64:67], v[104:107], a[116:119]\n" \
"ds_read_b64_tr_b16 v[164:165], v36 offset:8192\n" \
"v_mfma_f32_16x16x32_bf16 a[80:83], v[60:63], v[100:103], a[80:83]\n" \
"v_mfma_f32_16x16x32_bf16 a[112:115], v[64:67], v[100:103], a[112:115]\n" \
"ds_read_b64_tr_b16 v[166:167], v36 offset:9216\n" \
"v_mfma_f32_16x16x32_bf16 a[76:79], v[60:63], v[96:99], a[76:79]\n" \
"v_mfma_f32_16x16x32_bf16 a[108:111], v[64:67], v[96:99], a[108:111]\n" \
"v_mfma_f32_16x16x32_bf16 a[72:75], v[60:63], v[92:95], a[72:75]\n" \
"ds_read_b64_tr_b16 v[168:169], v37 offset:8192\n" \
"v_mfma_f32_16x16x32_bf16 a[104:107], v[64:67], v[92:95], a[104:107]\n" \
"v_mfma_f32_16x16x32_bf16 a[68:71], v[60:63], v[88:91], a[68:71]\n" \
"ds_read_b64_tr_b16 v[170:171], v37 offset:9216\n" \
"v_mfma_f32_16x16x32_bf16 a[100:103], v[64:67], v[88:91], a[100:103]\n" \
"v_mfma_f32_16x16x32_bf16 a[64:67], v[60:63], v[84:87], a[64:67]\n" \
"ds_read_b64_tr_b16 v[172:173], v38 offset:8192\n" \
"v_mfma_f32_16x16x32_bf16 a[96:99], v[64:67], v[84:87], a[96:99]\n" \
"s_waitcnt lgkmcnt(15)\n" \
"s_nop 0\n" \
"v_mfma_f32_16x16x32_bf16 a[128:131], v[68:71], v[84:87], a[128:131]\n" \
"ds_read_b64_tr_b16 v[174:175], v38 offset:9216\n" \
"v_mfma_f32_16x16x32_bf16 a[160:163], v[72:75], v[84:87], a[160:163]\n" \
"v_mfma_f32_16x16x32_bf16 a[132:135], v[68:71], v[88:91], a[132:135]\n" \
"ds_read_b64_tr_b16 v[176:177], v39 offset:8192\n" \
"v_mfma_f32_16x16x32_bf16 a[164:167], v[72:75], v[88:91], a[164:167]\n" \
"v_mfma_f32_16x16x32_bf16 a[136:139], v[68:71], v[92:95], a[136:139]\n" \
"ds_read_b64_tr_b16 v[178:179], v39 offset:9216\n" \
"v_mfma_f32_16x16x32_bf16 a[168:171], v[72:75], v[92:95], a[168:171]\n" \
"v_mfma_f32_16x16x32_bf16 a[140:143], v[68:71], v[96:99], a[140:143]\n" \
"v_bfe_u32 v44, v20, 2, 5\n" \
"v_mfma_f32_16x16x32_bf16 a[172:175], v[72:75], v[96:99], a[172:175]\n" \
"buffer_load_dword v44, v44, s[80:83], s38 offen offset:128 sc1\n" \
"v_mfma_f32_16x16x32_bf16 a[144:147], v[68:71], v[100:103], a[144:147]\n" \
"ds_read_b128 v[124:127], v29 offset:20480\n" \
"v_mfma_f32_16x16x32_bf16 a[176:179], v[72:75], v[100:103], a[176:179]\n" \
"ds_read_b128 v[128:131], v29 offset:22528\n" \
"v_mfma_f32_16x16x32_bf16 a[148:151], v[68:71], v[104:107], a[148:151]\n" \
"v_mfma_f32_16x16x32_bf16 a[180:183], v[72:75], v[104:107], a[180:183]\n" \
"ds_read_b128 v[132:135], v29 offset:0\n" \
"v_mfma_f32_16x16x32_bf16 a[152:155], v[68:71], v[108:111], a[152:155]\n" \
"v_mfma_f32_16x16x32_bf16 a[184:187], v[72:75], v[108:111], a[184:187]\n" \
"ds_read_b128 v[136:139], v29 offset:2048\n" \
"v_mfma_f32_16x16x32_bf16 a[156:159], v[68:71], v[112:115], a[156:159]\n" \
"v_mfma_f32_16x16x32_bf16 a[188:191], v[72:75], v[112:115], a[188:191]\n" \
"ds_read_b128 v[140:143], v29 offset:4096\n" \
"v_mfma_f32_16x16x32_bf16 a[220:223], v[76:79], v[112:115], a[220:223]\n" \
"v_mfma_f32_16x16x32_bf16 a[252:255], v[80:83], v[112:115], a[252:255]\n" \
"ds_read_b128 v[144:147], v29 offset:6144\n" \
"v_mfma_f32_16x16x32_bf16 a[216:219], v[76:79], v[108:111], a[216:219]\n" \
"v_mfma_f32_16x16x32_bf16 a[248:251], v[80:83], v[108:111], a[248:251]\n" \
"v_mfma_f32_16x16x32_bf16 a[212:215], v[76:79], v[104:107], a[212:215]\n" \
"v_mfma_f32_16x16x32_bf16 a[244:247], v[80:83], v[104:107], a[244:247]\n" \
"v_mfma_f32_16x16x32_bf16 a[208:211], v[76:79], v[100:103], a[208:211]\n" \
"v_mfma_f32_16x16x32_bf16 a[240:243], v[80:83], v[100:103], a[240:243]\n" \
"v_mfma_f32_16x16x32_bf16 a[204:207], v[76:79], v[96:99], a[204:207]\n" \
"v_mfma_f32_16x16x32_bf16 a[236:239], v[80:83], v[96:99], a[236:239]\n" \
"v_mfma_f32_16x16x32_bf16 a[200:203], v[76:79], v[92:95], a[200:203]\n" \
"v_mfma_f32_16x16x32_bf16 a[232:235], v[80:83], v[92:95], a[232:235]\n" \
"v_mfma_f32_16x16x32_bf16 a[196:199], v[76:79], v[88:91], a[196:199]\n" \
"v_mfma_f32_16x16x32_bf16 a[228:231], v[80:83], v[88:91], a[228:231]\n" \
"v_mfma_f32_16x16x32_bf16 a[192:195], v[76:79], v[84:87], a[192:195]\n" \
"v_mfma_f32_16x16x32_bf16 a[224:227], v[80:83], v[84:87], a[224:227]\n" \
"s_waitcnt vmcnt(0) lgkmcnt(0)\n" \
"s_barrier\n" \
"v_cmp_ne_u32_e32 vcc, s62, v44\n" \
"s_nop 4\n" \
"s_cbranch_vccnz L_poll61_%=\n" \
"L_pollr61_%=:\n" \
"s_mov_b32 s68, s12\n" \
"s_and_b32 s69, s13, 0xffff\n" \
"s_mov_b32 s70, 0x480000\n" \
"s_mov_b32 s71, 0x20000\n" \
"v_mfma_f32_16x16x32_bf16 a[0:3], v[116:119], v[148:151], a[0:3]\n" \
"s_add_u32 m0, s40, 0x0\n" \
"s_add_u32 s85, s39, 0x0\n" \
"v_mfma_f32_16x16x32_bf16 a[32:35], v[120:123], v[148:151], a[32:35]\n" \
"buffer_load_dwordx4 v20, s[80:83], s85 offen sc1 lds\n" \
"v_mfma_f32_16x16x32_bf16 a[4:7], v[116:119], v[152:155], a[4:7]\n" \
"ds_read_b64_tr_b16 v[84:85], v32 offset:32768\n" \
"v_mfma_f32_16x16x32_bf16 a[36:39], v[120:123], v[152:155], a[36:39]\n" \
"ds_read_b64_tr_b16 v[86:87], v32 offset:33792\n" \
"v_mfma_f32_16x16x32_bf16 a[8:11], v[116:119], v[156:159], a[8:11]\n" \
"s_add_u32 m0, s40, 0x1000\n" \
"s_add_u32 s84, s39, 0x1000\n" \
"v_mfma_f32_16x16x32_bf16 a[40:43], v[120:123], v[156:159], a[40:43]\n" \
"buffer_load_dwordx4 v20, s[80:83], s84 offen sc1 lds\n" \
"v_mfma_f32_16x16x32_bf16 a[12:15], v[116:119], v[160:163], a[12:15]\n" \
"ds_read_b128 v[52:55], v28 offset:49152\n" \
"v_mfma_f32_16x16x32_bf16 a[44:47], v[120:123], v[160:163], a[44:47]\n" \
"ds_read_b128 v[56:59], v28 offset:51200\n" \
"v_mfma_f32_16x16x32_bf16 a[16:19], v[116:119], v[164:167], a[16:19]\n" \
"s_add_u32 m0, s40, 0x3000\n" \
"s_mov_b32 s85, 0x0\n" \
"v_mfma_f32_16x16x32_bf16 a[48:51], v[120:123], v[164:167], a[48:51]\n" \
"buffer_load_dwordx4 v21, s[76:79], s85 offen lds\n" \
"v_mfma_f32_16x16x32_bf16 a[20:23], v[116:119], v[168:171], a[20:23]\n" \
"ds_read_b64_tr_b16 v[88:89], v33 offset:32768\n" \
"v_mfma_f32_16x16x32_bf16 a[52:55], v[120:123], v[168:171], a[52:55]\n" \
"ds_read_b64_tr_b16 v[90:91], v33 offset:33792\n" \
"v_mfma_f32_16x16x32_bf16 a[24:27], v[116:119], v[172:175], a[24:27]\n" \
"s_add_u32 m0, s30, 0x0\n" \
"s_mov_b32 s84, 0x0\n" \
"s_nop 0\n" \
"v_mfma_f32_16x16x32_bf16 a[56:59], v[120:123], v[172:175], a[56:59]\n" \
"buffer_load_dwordx4 v24, s[76:79], s84 offen lds\n" \
"v_mfma_f32_16x16x32_bf16 a[28:31], v[116:119], v[176:179], a[28:31]\n" \
"ds_read_b64_tr_b16 v[92:93], v34 offset:32768\n" \
"v_mfma_f32_16x16x32_bf16 a[60:63], v[120:123], v[176:179], a[60:63]\n" \
"ds_read_b64_tr_b16 v[94:95], v34 offset:33792\n" \
"v_mfma_f32_16x16x32_bf16 a[92:95], v[124:127], v[176:179], a[92:95]\n" \
"s_add_u32 m0, s30, 0x400\n" \
"s_mov_b32 s85, 0x2400\n" \
"v_mfma_f32_16x16x32_bf16 a[124:127], v[128:131], v[176:179], a[124:127]\n" \
"buffer_load_dwordx4 v25, s[76:79], s85 offen lds\n" \
"v_mfma_f32_16x16x32_bf16 a[88:91], v[124:127], v[172:175], a[88:91]\n" \
"ds_read_b64_tr_b16 v[96:97], v35 offset:32768\n" \
"v_mfma_f32_16x16x32_bf16 a[120:123], v[128:131], v[172:175], a[120:123]\n" \
"ds_read_b64_tr_b16 v[98:99], v35 offset:33792\n" \
"v_mfma_f32_16x16x32_bf16 a[84:87], v[124:127], v[168:171], a[84:87]\n" \
"s_add_u32 m0, s31, 0x0\n" \
"s_mov_b32 s84, 0x0\n" \
"v_mfma_f32_16x16x32_bf16 a[116:119], v[128:131], v[168:171], a[116:119]\n" \
"buffer_load_dwordx4 v26, s[68:71], s84 offen lds\n" \
"v_mfma_f32_16x16x32_bf16 a[80:83], v[124:127], v[164:167], a[80:83]\n" \
"ds_read_b64_tr_b16 v[100:101], v36 offset:32768\n" \
"v_mfma_f32_16x16x32_bf16 a[112:115], v[128:131], v[164:167], a[112:115]\n" \
"ds_read_b64_tr_b16 v[102:103], v36 offset:33792\n" \
"v_mfma_f32_16x16x32_bf16 a[76:79], v[124:127], v[160:163], a[76:79]\n" \
"s_add_u32 m0, s30, 0x800\n" \
"s_mov_b32 s85, 0x4800\n" \
"v_mfma_f32_16x16x32_bf16 a[108:111], v[128:131], v[160:163], a[108:111]\n" \
"buffer_load_dwordx4 v24, s[76:79], s85 offen lds\n" \
"v_mfma_f32_16x16x32_bf16 a[72:75], v[124:127], v[156:159], a[72:75]\n" \
"ds_read_b64_tr_b16 v[104:105], v37 offset:32768\n" \
"v_mfma_f32_16x16x32_bf16 a[104:107], v[128:131], v[156:159], a[104:107]\n" \
"v_mfma_f32_16x16x32_bf16 a[68:71], v[124:127], v[152:155], a[68:71]\n" \
"s_add_u32 m0, s30, 0xc00\n" \
"s_mov_b32 s84, 0x6c00\n" \
"v_mfma_f32_16x16x32_bf16 a[100:103], v[128:131], v[152:155], a[100:103]\n" \
"buffer_load_dwordx4 v25, s[76:79], s84 offen lds\n" \
"v_mfma_f32_16x16x32_bf16 a[64:67], v[124:127], v[148:151], a[64:67]\n" \
"ds_read_b64_tr_b16 v[106:107], v37 offset:33792\n" \
"v_mfma_f32_16x16x32_bf16 a[96:99], v[128:131], v[148:151], a[96:99]\n" \
"ds_read_b64_tr_b16 v[108:109], v38 offset:32768\n" \
"v_mfma_f32_16x16x32_bf16 a[128:131], v[132:135], v[148:151], a[128:131]\n" \
"s_add_u32 m0, s31, 0x400\n" \
"s_mov_b32 s85, 0x8000\n" \
"v_mfma_f32_16x16x32_bf16 a[160:163], v[136:139], v[148:151], a[160:163]\n" \
"buffer_load_dwordx4 v26, s[68:71], s85 offen lds\n" \
"v_mfma_f32_16x16x32_bf16 a[132:135], v[132:135], v[152:155], a[132:135]\n" \
"ds_read_b64_tr_b16 v[110:111], v38 offset:33792\n" \
"v_mfma_f32_16x16x32_bf16 a[164:167], v[136:139], v[152:155], a[164:167]\n" \
"ds_read_b64_tr_b16 v[112:113], v39 offset:32768\n" \
"v_mfma_f32_16x16x32_bf16 a[136:139], v[132:135], v[156:159], a[136:139]\n" \
"s_add_u32 m0, s30, 0x1000\n" \
"s_mov_b32 s84, 0x9000\n" \
"v_mfma_f32_16x16x32_bf16 a[168:171], v[136:139], v[156:159], a[168:171]\n" \
"buffer_load_dwordx4 v24, s[76:79], s84 offen lds\n" \
"v_mfma_f32_16x16x32_bf16 a[140:143], v[132:135], v[160:163], a[140:143]\n" \
"ds_read_b64_tr_b16 v[114:115], v39 offset:33792\n" \
"v_mfma_f32_16x16x32_bf16 a[172:175], v[136:139], v[160:163], a[172:175]\n" \
"ds_read_b128 v[60:63], v28 offset:53248\n" \
"v_mfma_f32_16x16x32_bf16 a[144:147], v[132:135], v[164:167], a[144:147]\n" \
"s_add_u32 m0, s30, 0x1400\n" \
"s_mov_b32 s85, 0xb400\n" \
"v_mfma_f32_16x16x32_bf16 a[176:179], v[136:139], v[164:167], a[176:179]\n" \
"buffer_load_dwordx4 v25, s[76:79], s85 offen lds\n" \
"v_mfma_f32_16x16x32_bf16 a[148:151], v[132:135], v[168:171], a[148:151]\n" \
"ds_read_b128 v[64:67], v28 offset:55296\n" \
"v_mfma_f32_16x16x32_bf16 a[180:183], v[136:139], v[168:171], a[180:183]\n" \
"ds_read_b128 v[68:71], v28 offset:32768\n" \
"v_mfma_f32_16x16x32_bf16 a[152:155], v[132:135], v[172:175], a[152:155]\n" \
"s_add_u32 m0, s31, 0x800\n" \
"s_mov_b32 s84, 0x10000\n" \
"v_mfma_f32_16x16x32_bf16 a[184:187], v[136:139], v[172:175], a[184:187]\n" \
"buffer_load_dwordx4 v27, s[68:71], s84 offen lds\n" \
"v_mfma_f32_16x16x32_bf16 a[156:159], v[132:135], v[176:179], a[156:159]\n" \
"ds_read_b128 v[72:75], v28 offset:34816\n" \
"v_mfma_f32_16x16x32_bf16 a[188:191], v[136:139], v[176:179], a[188:191]\n" \
"ds_read_b128 v[76:79], v28 offset:36864\n" \
"v_mfma_f32_16x16x32_bf16 a[220:223], v[140:143], v[176:179], a[220:223]\n" \
"v_mfma_f32_16x16x32_bf16 a[252:255], v[144:147], v[176:179], a[252:255]\n" \
"ds_read_b128 v[80:83], v28 offset:38912\n" \
"v_mfma_f32_16x16x32_bf16 a[216:219], v[140:143], v[172:175], a[216:219]\n" \
"v_mfma_f32_16x16x32_bf16 a[248:251], v[144:147], v[172:175], a[248:251]\n" \
"v_mfma_f32_16x16x32_bf16 a[212:215], v[140:143], v[168:171], a[212:215]\n" \
"v_mfma_f32_16x16x32_bf16 a[244:247], v[144:147], v[168:171], a[244:247]\n" \
"v_mfma_f32_16x16x32_bf16 a[208:211], v[140:143], v[164:167], a[208:211]\n" \
"v_mfma_f32_16x16x32_bf16 a[240:243], v[144:147], v[164:167], a[240:243]\n" \
"v_mfma_f32_16x16x32_bf16 a[204:207], v[140:143], v[160:163], a[204:207]\n" \
"v_mfma_f32_16x16x32_bf16 a[236:239], v[144:147], v[160:163], a[236:239]\n" \
"v_mfma_f32_16x16x32_bf16 a[200:203], v[140:143], v[156:159], a[200:203]\n" \
"v_mfma_f32_16x16x32_bf16 a[232:235], v[144:147], v[156:159], a[232:235]\n" \
"v_mfma_f32_16x16x32_bf16 a[196:199], v[140:143], v[152:155], a[196:199]\n" \
"v_mfma_f32_16x16x32_bf16 a[228:231], v[144:147], v[152:155], a[228:231]\n" \
"v_mfma_f32_16x16x32_bf16 a[192:195], v[140:143], v[148:151], a[192:195]\n" \
"v_mfma_f32_16x16x32_bf16 a[224:227], v[144:147], v[148:151], a[224:227]\n" \
"s_waitcnt lgkmcnt(15)\n" \
"s_nop 0\n" \
"v_mfma_f32_16x16x32_bf16 a[0:3], v[52:55], v[84:87], a[0:3]\n" \
"s_add_u32 m0, s30, 0x1800\n" \
"s_mov_b32 s84, 0xd800\n" \
"v_mfma_f32_16x16x32_bf16 a[32:35], v[56:59], v[84:87], a[32:35]\n" \
"buffer_load_dwordx4 v24, s[76:79], s84 offen lds\n" \
"v_mfma_f32_16x16x32_bf16 a[4:7], v[52:55], v[88:91], a[4:7]\n" \
"ds_read_b64_tr_b16 v[148:149], v32 offset:40960\n" \
"v_mfma_f32_16x16x32_bf16 a[36:39], v[56:59], v[88:91], a[36:39]\n" \
"s_add_u32 m0, s30, 0x1c00\n" \
"s_mov_b32 s85, 0xfc00\n" \
"v_mfma_f32_16x16x32_bf16 a[8:11], v[52:55], v[92:95], a[8:11]\n" \
"buffer_load_dwordx4 v25, s[76:79], s85 offen lds\n" \
"v_mfma_f32_16x16x32_bf16 a[40:43], v[56:59], v[92:95], a[40:43]\n" \
"ds_read_b64_tr_b16 v[150:151], v32 offset:41984\n" \
"s_waitcnt lgkmcnt(15)\n" \
"v_mfma_f32_16x16x32_bf16 a[12:15], v[52:55], v[96:99], a[12:15]\n" \
"s_add_u32 m0, s31, 0xc00\n" \
"s_mov_b32 s84, 0x18000\n" \
"v_mfma_f32_16x16x32_bf16 a[44:47], v[56:59], v[96:99], a[44:47]\n" \
"buffer_load_dwordx4 v27, s[68:71], s84 offen lds\n" \
"s_waitcnt lgkmcnt(14)\n" \
"v_mfma_f32_16x16x32_bf16 a[16:19], v[52:55], v[100:103], a[16:19]\n" \
"ds_read_b128 v[116:119], v29 offset:49152\n" \
"v_mfma_f32_16x16x32_bf16 a[48:51], v[56:59], v[100:103], a[48:51]\n" \
"ds_read_b128 v[120:123], v29 offset:51200\n" \
"s_waitcnt lgkmcnt(14)\n" \
"v_mfma_f32_16x16x32_bf16 a[20:23], v[52:55], v[104:107], a[20:23]\n" \
"ds_read_b64_tr_b16 v[152:153], v33 offset:40960\n" \
"v_mfma_f32_16x16x32_bf16 a[52:55], v[56:59], v[104:107], a[52:55]\n" \
"ds_read_b64_tr_b16 v[154:155], v33 offset:41984\n" \
"s_waitcnt lgkmcnt(14)\n" \
"v_mfma_f32_16x16x32_bf16 a[24:27], v[52:55], v[108:111], a[24:27]\n" \
"v_mfma_f32_16x16x32_bf16 a[56:59], v[56:59], v[108:111], a[56:59]\n" \
"ds_read_b64_tr_b16 v[156:157], v34 offset:40960\n" \
"s_waitcnt lgkmcnt(13)\n" \
"v_mfma_f32_16x16x32_bf16 a[28:31], v[52:55], v[112:115], a[28:31]\n" \
"v_mfma_f32_16x16x32_bf16 a[60:63], v[56:59], v[112:115], a[60:63]\n" \
"ds_read_b64_tr_b16 v[158:159], v34 offset:41984\n" \
"s_waitcnt lgkmcnt(13)\n" \
"v_mfma_f32_16x16x32_bf16 a[92:95], v[60:63], v[112:115], a[92:95]\n" \
"s_waitcnt lgkmcnt(12)\n" \
"v_mfma_f32_16x16x32_bf16 a[124:127], v[64:67], v[112:115], a[124:127]\n" \
"ds_read_b64_tr_b16 v[160:161], v35 offset:40960\n" \
"v_mfma_f32_16x16x32_bf16 a[88:91], v[60:63], v[108:111], a[88:91]\n" \
"v_lshlrev_b32_e32 v45, 3, v20\n" \
"v_add_u32_e64 v45, s31, v45\n" \
"v_mfma_f32_16x16x32_bf16 a[120:123], v[64:67], v[108:111], a[120:123]\n" \
"s_getpc_b64 s[86:87]\n" \
"s_sub_u32 s86, s86, 0x10800\n" \
"s_subb_u32 s87, s87, 0\n" \
"s_mov_b32 m0, s32\n" \
"s_nop 0\n" \
"v_mfma_f32_16x16x32_bf16 a[84:87], v[60:63], v[104:107], a[84:87]\n" \
"global_load_lds_dword v45, s[86:87]\n" \
"v_mfma_f32_16x16x32_bf16 a[116:119], v[64:67], v[104:107], a[116:119]\n" \
"ds_read_b64_tr_b16 v[162:163], v35 offset:41984\n" \
"v_mfma_f32_16x16x32_bf16 a[80:83], v[60:63], v[100:103], a[80:83]\n" \
"ds_read_b64_tr_b16 v[164:165], v36 offset:40960\n" \
"v_mfma_f32_16x16x32_bf16 a[112:115], v[64:67], v[100:103], a[112:115]\n" \
"ds_read_b64_tr_b16 v[166:167], v36 offset:41984\n" \
"v_mfma_f32_16x16x32_bf16 a[76:79], v[60:63], v[96:99], a[76:79]\n" \
"v_mfma_f32_16x16x32_bf16 a[108:111], v[64:67], v[96:99], a[108:111]\n" \
"v_mfma_f32_16x16x32_bf16 a[72:75], v[60:63], v[92:95], a[72:75]\n" \
"ds_read_b64_tr_b16 v[168:169], v37 offset:40960\n" \
"v_mfma_f32_16x16x32_bf16 a[104:107], v[64:67], v[92:95], a[104:107]\n" \
"v_mfma_f32_16x16x32_bf16 a[68:71], v[60:63], v[88:91], a[68:71]\n" \
"ds_read_b64_tr_b16 v[170:171], v37 offset:41984\n" \
"v_mfma_f32_16x16x32_bf16 a[100:103], v[64:67], v[88:91], a[100:103]\n" \
"v_mfma_f32_16x16x32_bf16 a[64:67], v[60:63], v[84:87], a[64:67]\n" \
"ds_read_b64_tr_b16 v[172:173], v38 offset:40960\n" \
"v_mfma_f32_16x16x32_bf16 a[96:99], v[64:67], v[84:87], a[96:99]\n" \
"s_waitcnt lgkmcnt(15)\n" \
"s_nop 0\n" \
"v_mfma_f32_16x16x32_bf16 a[128:131], v[68:71], v[84:87], a[128:131]\n" \
"ds_read_b64_tr_b16 v[174:175], v38 offset:41984\n" \
"v_mfma_f32_16x16x32_bf16 a[160:163], v[72:75], v[84:87], a[160:163]\n" \
"v_mfma_f32_16x16x32_bf16 a[132:135], v[68:71], v[88:91], a[132:135]\n" \
"ds_read_b64_tr_b16 v[176:177], v39 offset:40960\n" \
"v_mfma_f32_16x16x32_bf16 a[164:167], v[72:75], v[88:91], a[164:167]\n" \
"v_mfma_f32_16x16x32_bf16 a[136:139], v[68:71], v[92:95], a[136:139]\n" \
"ds_read_b64_tr_b16 v[178:179], v39 offset:41984\n" \
"v_mfma_f32_16x16x32_bf16 a[168:171], v[72:75], v[92:95], a[168:171]\n" \
"v_mfma_f32_16x16x32_bf16 a[140:143], v[68:71], v[96:99], a[140:143]\n" \
"v_mfma_f32_16x16x32_bf16 a[172:175], v[72:75], v[96:99], a[172:175]\n" \
"ds_read_b128 v[124:127], v29 offset:53248\n" \
"v_mfma_f32_16x16x32_bf16 a[144:147], v[68:71], v[100:103], a[144:147]\n" \
"v_mfma_f32_16x16x32_bf16 a[176:179], v[72:75], v[100:103], a[176:179]\n" \
"ds_read_b128 v[128:131], v29 offset:55296\n" \
"v_mfma_f32_16x16x32_bf16 a[148:151], v[68:71], v[104:107], a[148:151]\n" \
"v_mfma_f32_16x16x32_bf16 a[180:183], v[72:75], v[104:107], a[180:183]\n" \
"ds_read_b128 v[132:135], v29 offset:32768\n" \
"v_mfma_f32_16x16x32_bf16 a[152:155], v[68:71], v[108:111], a[152:155]\n" \
"v_mfma_f32_16x16x32_bf16 a[184:187], v[72:75], v[108:111], a[184:187]\n" \
"ds_read_b128 v[136:139], v29 offset:34816\n" \
"v_mfma_f32_16x16x32_bf16 a[156:159], v[68:71], v[112:115], a[156:159]\n" \
"v_mfma_f32_16x16x32_bf16 a[188:191], v[72:75], v[112:115], a[188:191]\n" \
"ds_read_b128 v[140:143], v29 offset:36864\n" \
"v_mfma_f32_16x16x32_bf16 a[220:223], v[76:79], v[112:115], a[220:223]\n" \
"v_mfma_f32_16x16x32_bf16 a[252:255], v[80:83], v[112:115], a[252:255]\n" \
"ds_read_b128 v[144:147], v29 offset:38912\n" \
"v_mfma_f32_16x16x32_bf16 a[216:219], v[76:79], v[108:111], a[216:219]\n" \
"v_mfma_f32_16x16x32_bf16 a[248:251], v[80:83], v[108:111], a[248:251]\n" \
"v_mfma_f32_16x16x32_bf16 a[212:215], v[76:79], v[104:107], a[212:215]\n" \
"v_mfma_f32_16x16x32_bf16 a[244:247], v[80:83], v[104:107], a[244:247]\n" \
"v_mfma_f32_16x16x32_bf16 a[208:211], v[76:79], v[100:103], a[208:211]\n" \
"v_mfma_f32_16x16x32_bf16 a[240:243], v[80:83], v[100:103], a[240:243]\n" \
"v_mfma_f32_16x16x32_bf16 a[204:207], v[76:79], v[96:99], a[204:207]\n" \
"v_mfma_f32_16x16x32_bf16 a[236:239], v[80:83], v[96:99], a[236:239]\n" \
"v_mfma_f32_16x16x32_bf16 a[200:203], v[76:79], v[92:95], a[200:203]\n" \
"v_mfma_f32_16x16x32_bf16 a[232:235], v[80:83], v[92:95], a[232:235]\n" \
"v_mfma_f32_16x16x32_bf16 a[196:199], v[76:79], v[88:91], a[196:199]\n" \
"v_mfma_f32_16x16x32_bf16 a[228:231], v[80:83], v[88:91], a[228:231]\n" \
"v_mfma_f32_16x16x32_bf16 a[192:195], v[76:79], v[84:87], a[192:195]\n" \
"v_mfma_f32_16x16x32_bf16 a[224:227], v[80:83], v[84:87], a[224:227]\n" \
"s_waitcnt vmcnt(1) lgkmcnt(0)\n" \
"s_barrier\n" \
"v_xor_b32_e32 v32, 0x4000, v36\n" \
"v_xor_b32_e64 v36, 0x0, v190\n" \
"v_xor_b32_e32 v33, 0x4000, v37\n" \
"v_xor_b32_e64 v37, 0x20, v190\n" \
"v_xor_b32_e32 v34, 0x4000, v38\n" \
"v_xor_b32_e64 v38, 0x40, v190\n" \
"v_xor_b32_e32 v35, 0x4000, v39\n" \
"v_xor_b32_e32 v39, 0x60, v190\n" \
"v_add_u32_e32 v28, s43, v28\n" \
"v_add_u32_e32 v29, s43, v29\n" \
"v_mfma_f32_16x16x32_bf16 a[0:3], v[116:119], v[148:151], a[0:3]\n" \
"s_add_u32 m0, s40, 0x8000\n" \
"s_add_u32 s85, s39, 0x2000\n" \
"v_mfma_f32_16x16x32_bf16 a[32:35], v[120:123], v[148:151], a[32:35]\n" \
"buffer_load_dwordx4 v20, s[80:83], s85 offen sc1 lds\n" \
"v_mfma_f32_16x16x32_bf16 a[4:7], v[116:119], v[152:155], a[4:7]\n" \
"ds_read_b64_tr_b16 v[104:105], v37 offset:0\n" \
"v_mfma_f32_16x16x32_bf16 a[36:39], v[120:123], v[152:155], a[36:39]\n" \
"ds_read_b64_tr_b16 v[106:107], v37 offset:512\n" \
"v_mfma_f32_16x16x32_bf16 a[8:11], v[116:119], v[156:159], a[8:11]\n" \
"s_add_u32 m0, s40, 0x9000\n" \
"s_add_u32 s84, s39, 0x3000\n" \
"v_accvgpr_read_b32 v184, a0\n" \
"v_accvgpr_read_b32 v185, a1\n" \
"v_mfma_f32_16x16x32_bf16 a[40:43], v[120:123], v[156:159], a[40:43]\n" \
"buffer_load_dwordx4 v20, s[80:83], s84 offen sc1 lds\n" \
"v_accvgpr_read_b32 v186, a2\n" \
"v_accvgpr_read_b32 v187, a3\n" \
"v_mfma_f32_16x16x32_bf16 a[12:15], v[116:119], v[160:163], a[12:15]\n" \
"ds_read_b64_tr_b16 v[108:109], v38 offset:0\n" \
"v_accvgpr_read_b32 v180, a32\n" \
"v_accvgpr_read_b32 v181, a33\n" \
"v_accvgpr_read_b32 v182, a34\n" \
"v_mfma_f32_16x16x32_bf16 a[44:47], v[120:123], v[160:163], a[44:47]\n" \
"ds_read_b64_tr_b16 v[110:111], v38 offset:512\n" \
"v_accvgpr_read_b32 v183, a35\n" \
"v_cvt_pk_bf16_f32 v184, v184, v185\n" \
"v_cvt_pk_bf16_f32 v185, v186, v187\n" \
"v_mfma_f32_16x16x32_bf16 a[16:19], v[116:119], v[164:167], a[16:19]\n" \
"s_add_u32 m0, s40, 0xb000\n" \
"s_mov_b32 s85, 0x80\n" \
"v_cvt_pk_bf16_f32 v186, v180, v181\n" \
"v_cvt_pk_bf16_f32 v187, v182, v183\n" \
"v_mfma_f32_16x16x32_bf16 a[48:51], v[120:123], v[164:167], a[48:51]\n" \
"buffer_load_dwordx4 v21, s[76:79], s85 offen lds\n" \
"v_accvgpr_read_b32 v188, a4\n" \
"v_accvgpr_read_b32 v189, a5\n" \
"v_mfma_f32_16x16x32_bf16 a[20:23], v[116:119], v[168:171], a[20:23]\n" \
"ds_read_b64_tr_b16 v[112:113], v39 offset:0\n" \
"v_accvgpr_read_b32 v190, a6\n" \
"v_accvgpr_read_b32 v191, a7\n" \
"v_accvgpr_read_b32 v180, a36\n" \
"v_mfma_f32_16x16x32_bf16 a[52:55], v[120:123], v[168:171], a[52:55]\n" \
"ds_read_b64_tr_b16 v[114:115], v39 offset:512\n" \
"v_accvgpr_read_b32 v181, a37\n" \
"v_accvgpr_read_b32 v182, a38\n" \
"v_accvgpr_read_b32 v183, a39\n" \
"v_mfma_f32_16x16x32_bf16 a[24:27], v[116:119], v[172:175], a[24:27]\n" \
"s_add_u32 m0, s30, 0x8000\n" \
"s_mov_b32 s84, 0x80\n" \
"v_cvt_pk_bf16_f32 v188, v188, v189\n" \
"v_cvt_pk_bf16_f32 v189, v190, v191\n" \
"v_mfma_f32_16x16x32_bf16 a[56:59], v[120:123], v[172:175], a[56:59]\n" \
"buffer_load_dwordx4 v24, s[76:79], s84 offen lds\n" \
"v_cvt_pk_bf16_f32 v190, v180, v181\n" \
"v_cvt_pk_bf16_f32 v191, v182, v183\n" \
"v_mfma_f32_16x16x32_bf16 a[28:31], v[116:119], v[176:179], a[28:31]\n" \
"ds_read_b64_tr_b16 v[84:85], v32 offset:0\n" \
"v_accvgpr_read_b32 v192, a8\n" \
"v_accvgpr_read_b32 v193, a9\n" \
"v_accvgpr_read_b32 v194, a10\n" \
"v_mfma_f32_16x16x32_bf16 a[60:63], v[120:123], v[176:179], a[60:63]\n" \
"ds_read_b64_tr_b16 v[86:87], v32 offset:1024\n" \
"v_accvgpr_read_b32 v195, a11\n" \
"v_accvgpr_read_b32 v180, a40\n" \
"v_accvgpr_read_b32 v181, a41\n" \
"v_mfma_f32_16x16x32_bf16 a[92:95], v[124:127], v[176:179], a[92:95]\n" \
"s_add_u32 m0, s30, 0x8400\n" \
"s_mov_b32 s85, 0x2480\n" \
"v_accvgpr_read_b32 v182, a42\n" \
"v_accvgpr_read_b32 v183, a43\n" \
"v_mfma_f32_16x16x32_bf16 a[124:127], v[128:131], v[176:179], a[124:127]\n" \
"buffer_load_dwordx4 v25, s[76:79], s85 offen lds\n" \
"v_cvt_pk_bf16_f32 v192, v192, v193\n" \
"v_cvt_pk_bf16_f32 v193, v194, v195\n" \
"v_mfma_f32_16x16x32_bf16 a[88:91], v[124:127], v[172:175], a[88:91]\n" \
"ds_read_b64_tr_b16 v[88:89], v33 offset:0\n" \
"v_cvt_pk_bf16_f32 v194, v180, v181\n" \
"v_cvt_pk_bf16_f32 v195, v182, v183\n" \
"v_accvgpr_read_b32 v196, a12\n" \
"v_mfma_f32_16x16x32_bf16 a[120:123], v[128:131], v[172:175], a[120:123]\n" \
"ds_read_b64_tr_b16 v[90:91], v33 offset:1024\n" \
"v_accvgpr_read_b32 v197, a13\n" \
"v_accvgpr_read_b32 v198, a14\n" \
"v_accvgpr_read_b32 v199, a15\n" \
"v_mfma_f32_16x16x32_bf16 a[84:87], v[124:127], v[168:171], a[84:87]\n" \
"s_add_u32 m0, s31, 0x8000\n" \
"s_mov_b32 s84, 0x80000\n" \
"v_accvgpr_read_b32 v180, a44\n" \
"v_accvgpr_read_b32 v181, a45\n" \
"v_mfma_f32_16x16x32_bf16 a[116:119], v[128:131], v[168:171], a[116:119]\n" \
"buffer_load_dwordx4 v26, s[68:71], s84 offen lds\n" \
"v_accvgpr_read_b32 v182, a46\n" \
"v_accvgpr_read_b32 v183, a47\n" \
"v_mfma_f32_16x16x32_bf16 a[80:83], v[124:127], v[164:167], a[80:83]\n" \
"ds_read_b64_tr_b16 v[92:93], v34 offset:0\n" \
"v_cvt_pk_bf16_f32 v196, v196, v197\n" \
"v_cvt_pk_bf16_f32 v197, v198, v199\n" \
"v_cvt_pk_bf16_f32 v198, v180, v181\n" \
"v_mfma_f32_16x16x32_bf16 a[112:115], v[128:131], v[164:167], a[112:115]\n" \
"ds_read_b64_tr_b16 v[94:95], v34 offset:1024\n" \
"v_cvt_pk_bf16_f32 v199, v182, v183\n" \
"v_accvgpr_read_b32 v200, a16\n" \
"v_accvgpr_read_b32 v201, a17\n" \
"v_mfma_f32_16x16x32_bf16 a[76:79], v[124:127], v[160:163], a[76:79]\n" \
"s_add_u32 m0, s30, 0x8800\n" \
"s_mov_b32 s85, 0x4880\n" \
"v_accvgpr_read_b32 v202, a18\n" \
"v_accvgpr_read_b32 v203, a19\n" \
"v_mfma_f32_16x16x32_bf16 a[108:111], v[128:131], v[160:163], a[108:111]\n" \
"buffer_load_dwordx4 v24, s[76:79], s85 offen lds\n" \
"v_accvgpr_read_b32 v180, a48\n" \
"v_accvgpr_read_b32 v181, a49\n" \
"v_mfma_f32_16x16x32_bf16 a[72:75], v[124:127], v[156:159], a[72:75]\n" \
"ds_read_b64_tr_b16 v[96:97], v35 offset:0\n" \
"v_accvgpr_read_b32 v182, a50\n" \
"v_accvgpr_read_b32 v183, a51\n" \
"v_cvt_pk_bf16_f32 v200, v200, v201\n" \
"v_mfma_f32_16x16x32_bf16 a[104:107], v[128:131], v[156:159], a[104:107]\n" \
"v_cvt_pk_bf16_f32 v201, v202, v203\n" \
"v_cvt_pk_bf16_f32 v202, v180, v181\n" \
"v_cvt_pk_bf16_f32 v203, v182, v183\n" \
"v_accvgpr_read_b32 v204, a20\n" \
"v_mfma_f32_16x16x32_bf16 a[68:71], v[124:127], v[152:155], a[68:71]\n" \
"s_add_u32 m0, s30, 0x8c00\n" \
"s_mov_b32 s84, 0x6c80\n" \
"v_accvgpr_read_b32 v205, a21\n" \
"v_accvgpr_read_b32 v206, a22\n" \
"v_mfma_f32_16x16x32_bf16 a[100:103], v[128:131], v[152:155], a[100:103]\n" \
"buffer_load_dwordx4 v25, s[76:79], s84 offen lds\n" \
"v_accvgpr_read_b32 v207, a23\n" \
"v_accvgpr_read_b32 v180, a52\n" \
"v_mfma_f32_16x16x32_bf16 a[64:67], v[124:127], v[148:151], a[64:67]\n" \
"ds_read_b64_tr_b16 v[98:99], v35 offset:1024\n" \
"v_accvgpr_read_b32 v181, a53\n" \
"v_accvgpr_read_b32 v182, a54\n" \
"v_accvgpr_read_b32 v183, a55\n" \
"v_mfma_f32_16x16x32_bf16 a[96:99], v[128:131], v[148:151], a[96:99]\n" \
"ds_read_b64_tr_b16 v[100:101], v36 offset:0\n" \
"v_cvt_pk_bf16_f32 v204, v204, v205\n" \
"v_cvt_pk_bf16_f32 v205, v206, v207\n" \
"v_cvt_pk_bf16_f32 v206, v180, v181\n" \
"v_mfma_f32_16x16x32_bf16 a[128:131], v[132:135], v[148:151], a[128:131]\n" \
"s_add_u32 m0, s31, 0x8400\n" \
"s_mov_b32 s85, 0x88000\n" \
"v_cvt_pk_bf16_f32 v207, v182, v183\n" \
"v_accvgpr_read_b32 v208, a24\n" \
"v_mfma_f32_16x16x32_bf16 a[160:163], v[136:139], v[148:151], a[160:163]\n" \
"buffer_load_dwordx4 v26, s[68:71], s85 offen lds\n" \
"v_accvgpr_read_b32 v209, a25\n" \
"v_accvgpr_read_b32 v210, a26\n" \
"v_mfma_f32_16x16x32_bf16 a[132:135], v[132:135], v[152:155], a[132:135]\n" \
"ds_read_b64_tr_b16 v[102:103], v36 offset:512\n" \
"v_accvgpr_read_b32 v211, a27\n" \
"v_accvgpr_read_b32 v180, a56\n" \
"v_accvgpr_read_b32 v181, a57\n" \
"v_mfma_f32_16x16x32_bf16 a[164:167], v[136:139], v[152:155], a[164:167]\n" \
"ds_read_b128 v[52:55], v28 offset:0\n" \
"v_accvgpr_read_b32 v182, a58\n" \
"v_accvgpr_read_b32 v183, a59\n" \
"v_cvt_pk_bf16_f32 v208, v208, v209\n" \
"v_mfma_f32_16x16x32_bf16 a[136:139], v[132:135], v[156:159], a[136:139]\n" \
"s_add_u32 m0, s30, 0x9000\n" \
"s_mov_b32 s84, 0x9080\n" \
"v_cvt_pk_bf16_f32 v209, v210, v211\n" \
"v_cvt_pk_bf16_f32 v210, v180, v181\n" \
"v_mfma_f32_16x16x32_bf16 a[168:171], v[136:139], v[156:159], a[168:171]\n" \
"buffer_load_dwordx4 v24, s[76:79], s84 offen lds\n" \
"v_cvt_pk_bf16_f32 v211, v182, v183\n" \
"v_accvgpr_read_b32 v212, a28\n" \
"v_mfma_f32_16x16x32_bf16 a[140:143], v[132:135], v[160:163], a[140:143]\n" \
"ds_read_b128 v[56:59], v28 offset:2048\n" \
"v_accvgpr_read_b32 v213, a29\n" \
"v_accvgpr_read_b32 v214, a30\n" \
"v_accvgpr_read_b32 v215, a31\n" \
"v_mfma_f32_16x16x32_bf16 a[172:175], v[136:139], v[160:163], a[172:175]\n" \
"ds_read_b128 v[60:63], v28 offset:4096\n" \
"v_accvgpr_read_b32 v180, a60\n" \
"v_accvgpr_read_b32 v181, a61\n" \
"v_accvgpr_read_b32 v182, a62\n" \
"v_mfma_f32_16x16x32_bf16 a[144:147], v[132:135], v[164:167], a[144:147]\n" \
"s_add_u32 m0, s30, 0x9400\n" \
"s_mov_b32 s85, 0xb480\n" \
"v_accvgpr_read_b32 v183, a63\n" \
"v_cvt_pk_bf16_f32 v212, v212, v213\n" \
"v_mfma_f32_16x16x32_bf16 a[176:179], v[136:139], v[164:167], a[176:179]\n" \
"buffer_load_dwordx4 v25, s[76:79], s85 offen lds\n" \
"v_cvt_pk_bf16_f32 v213, v214, v215\n" \
"v_cvt_pk_bf16_f32 v214, v180, v181\n" \
"v_mfma_f32_16x16x32_bf16 a[148:151], v[132:135], v[168:171], a[148:151]\n" \
"ds_read_b128 v[64:67], v28 offset:6144\n" \
"v_cvt_pk_bf16_f32 v215, v182, v183\n" \
"v_accvgpr_read_b32 v216, a64\n" \
"v_accvgpr_read_b32 v217, a65\n" \
"v_mfma_f32_16x16x32_bf16 a[180:183], v[136:139], v[168:171], a[180:183]\n" \
"ds_read_b128 v[68:71], v28 offset:8192\n" \
"v_accvgpr_read_b32 v218, a66\n" \
"v_accvgpr_read_b32 v219, a67\n" \
"v_accvgpr_read_b32 v180, a96\n" \
"v_mfma_f32_16x16x32_bf16 a[152:155], v[132:135], v[172:175], a[152:155]\n" \
"s_add_u32 m0, s31, 0x8800\n" \
"s_mov_b32 s84, 0x90000\n" \
"v_accvgpr_read_b32 v181, a97\n" \
"v_accvgpr_read_b32 v182, a98\n" \
"v_mfma_f32_16x16x32_bf16 a[184:187], v[136:139], v[172:175], a[184:187]\n" \
"buffer_load_dwordx4 v27, s[68:71], s84 offen lds\n" \
"v_accvgpr_read_b32 v183, a99\n" \
"v_cvt_pk_bf16_f32 v216, v216, v217\n" \
"v_mfma_f32_16x16x32_bf16 a[156:159], v[132:135], v[176:179], a[156:159]\n" \
"ds_read_b128 v[72:75], v28 offset:10240\n" \
"v_cvt_pk_bf16_f32 v217, v218, v219\n" \
"v_cvt_pk_bf16_f32 v218, v180, v181\n" \
"v_cvt_pk_bf16_f32 v219, v182, v183\n" \
"v_mfma_f32_16x16x32_bf16 a[188:191], v[136:139], v[176:179], a[188:191]\n" \
"ds_read_b128 v[76:79], v28 offset:12288\n" \
"v_accvgpr_read_b32 v220, a68\n" \
"v_accvgpr_read_b32 v221, a69\n" \
"v_accvgpr_read_b32 v222, a70\n" \
"v_mfma_f32_16x16x32_bf16 a[220:223], v[140:143], v[176:179], a[220:223]\n" \
"v_accvgpr_read_b32 v223, a71\n" \
"v_accvgpr_read_b32 v180, a100\n" \
"v_accvgpr_read_b32 v181, a101\n" \
"v_accvgpr_read_b32 v182, a102\n" \
"v_mfma_f32_16x16x32_bf16 a[252:255], v[144:147], v[176:179], a[252:255]\n" \
"ds_read_b128 v[80:83], v28 offset:14336\n" \
"v_accvgpr_read_b32 v183, a103\n" \
"v_cvt_pk_bf16_f32 v220, v220, v221\n" \
"v_cvt_pk_bf16_f32 v221, v222, v223\n" \
"v_mfma_f32_16x16x32_bf16 a[216:219], v[140:143], v[172:175], a[216:219]\n" \
"v_cvt_pk_bf16_f32 v222, v180, v181\n" \
"v_cvt_pk_bf16_f32 v223, v182, v183\n" \
"v_accvgpr_read_b32 v224, a72\n" \
"v_accvgpr_read_b32 v225, a73\n" \
"v_mfma_f32_16x16x32_bf16 a[248:251], v[144:147], v[172:175], a[248:251]\n" \
"v_accvgpr_read_b32 v226, a74\n" \
"v_accvgpr_read_b32 v227, a75\n" \
"v_accvgpr_read_b32 v180, a104\n" \
"v_accvgpr_read_b32 v181, a105\n" \
"v_mfma_f32_16x16x32_bf16 a[212:215], v[140:143], v[168:171], a[212:215]\n" \
"v_accvgpr_read_b32 v182, a106\n" \
"v_accvgpr_read_b32 v183, a107\n" \
"v_cvt_pk_bf16_f32 v224, v224, v225\n" \
"v_cvt_pk_bf16_f32 v225, v226, v227\n" \
"v_mfma_f32_16x16x32_bf16 a[244:247], v[144:147], v[168:171], a[244:247]\n" \
"v_cvt_pk_bf16_f32 v226, v180, v181\n" \
"v_cvt_pk_bf16_f32 v227, v182, v183\n" \
"v_accvgpr_read_b32 v228, a76\n" \
"v_accvgpr_read_b32 v229, a77\n" \
"v_mfma_f32_16x16x32_bf16 a[208:211], v[140:143], v[164:167], a[208:211]\n" \
"v_accvgpr_read_b32 v230, a78\n" \
"v_accvgpr_read_b32 v231, a79\n" \
"v_accvgpr_read_b32 v180, a108\n" \
"v_accvgpr_read_b32 v181, a109\n" \
"v_mfma_f32_16x16x32_bf16 a[240:243], v[144:147], v[164:167], a[240:243]\n" \
"v_accvgpr_read_b32 v182, a110\n" \
"v_accvgpr_read_b32 v183, a111\n" \
"v_cvt_pk_bf16_f32 v228, v228, v229\n" \
"v_cvt_pk_bf16_f32 v229, v230, v231\n" \
"v_mfma_f32_16x16x32_bf16 a[204:207], v[140:143], v[160:163], a[204:207]\n" \
"v_cvt_pk_bf16_f32 v230, v180, v181\n" \
"v_cvt_pk_bf16_f32 v231, v182, v183\n" \
"v_accvgpr_read_b32 v232, a80\n" \
"v_accvgpr_read_b32 v233, a81\n" \
"v_mfma_f32_16x16x32_bf16 a[236:239], v[144:147], v[160:163], a[236:239]\n" \
"v_accvgpr_read_b32 v234, a82\n" \
"v_accvgpr_read_b32 v235, a83\n" \
"v_accvgpr_read_b32 v180, a112\n" \
"v_accvgpr_read_b32 v181, a113\n" \
"v_mfma_f32_16x16x32_bf16 a[200:203], v[140:143], v[156:159], a[200:203]\n" \
"v_accvgpr_read_b32 v182, a114\n" \
"v_accvgpr_read_b32 v183, a115\n" \
"v_cvt_pk_bf16_f32 v232, v232, v233\n" \
"v_cvt_pk_bf16_f32 v233, v234, v235\n" \
"v_mfma_f32_16x16x32_bf16 a[232:235], v[144:147], v[156:159], a[232:235]\n" \
"v_cvt_pk_bf16_f32 v234, v180, v181\n" \
"v_cvt_pk_bf16_f32 v235, v182, v183\n" \
"v_accvgpr_read_b32 v236, a84\n" \
"v_accvgpr_read_b32 v237, a85\n" \
"v_mfma_f32_16x16x32_bf16 a[196:199], v[140:143], v[152:155], a[196:199]\n" \
"v_accvgpr_read_b32 v238, a86\n" \
"v_accvgpr_read_b32 v239, a87\n" \
"v_accvgpr_read_b32 v180, a116\n" \
"v_accvgpr_read_b32 v181, a117\n" \
"v_mfma_f32_16x16x32_bf16 a[228:231], v[144:147], v[152:155], a[228:231]\n" \
"v_accvgpr_read_b32 v182, a118\n" \
"v_accvgpr_read_b32 v183, a119\n" \
"v_cvt_pk_bf16_f32 v236, v236, v237\n" \
"v_cvt_pk_bf16_f32 v237, v238, v239\n" \
"v_mfma_f32_16x16x32_bf16 a[192:195], v[140:143], v[148:151], a[192:195]\n" \
"v_cvt_pk_bf16_f32 v238, v180, v181\n" \
"v_cvt_pk_bf16_f32 v239, v182, v183\n" \
"v_accvgpr_read_b32 v240, a88\n" \
"v_accvgpr_read_b32 v241, a89\n" \
"v_mfma_f32_16x16x32_bf16 a[224:227], v[144:147], v[148:151], a[224:227]\n" \
"v_accvgpr_read_b32 v242, a90\n" \
"v_accvgpr_read_b32 v243, a91\n" \
"v_accvgpr_read_b32 v180, a120\n" \
"v_accvgpr_read_b32 v181, a121\n" \
"s_waitcnt lgkmcnt(4)\n" \
"s_nop 0\n" \
"v_mfma_f32_16x16x32_bf16 a[24:27], v[104:107], v[64:67], 0\n" \
"s_add_u32 m0, s30, 0x9800\n" \
"s_mov_b32 s84, 0xd880\n" \
"v_accvgpr_read_b32 v182, a122\n" \
"v_accvgpr_read_b32 v183, a123\n" \
"v_cvt_pk_bf16_f32 v240, v240, v241\n" \
"v_mfma_f32_16x16x32_bf16 a[28:31], v[64:67], v[108:111], 0\n" \
"buffer_load_dwordx4 v24, s[76:79], s84 offen lds\n" \
"v_cvt_pk_bf16_f32 v241, v242, v243\n" \
"v_cvt_pk_bf16_f32 v242, v180, v181\n" \
"v_cvt_pk_bf16_f32 v243, v182, v183\n" \
"v_mfma_f32_16x16x32_bf16 a[56:59], v[112:115], v[64:67], 0\n" \
"ds_read_b64_tr_b16 v[168:169], v37 offset:4096\n" \
"v_accvgpr_read_b32 v244, a92\n" \
"v_accvgpr_read_b32 v245, a93\n" \
"v_accvgpr_read_b32 v246, a94\n" \
"v_accvgpr_read_b32 v247, a95\n" \
"v_mfma_f32_16x16x32_bf16 a[0:3], v[84:87], v[52:55], 0\n" \
"s_add_u32 m0, s30, 0x9c00\n" \
"s_mov_b32 s85, 0xfc80\n" \
"v_accvgpr_read_b32 v180, a124\n" \
"v_accvgpr_read_b32 v181, a125\n" \
"v_accvgpr_read_b32 v182, a126\n" \
"v_mfma_f32_16x16x32_bf16 a[32:35], v[84:87], v[56:59], 0\n" \
"buffer_load_dwordx4 v25, s[76:79], s85 offen lds\n" \
"v_accvgpr_read_b32 v183, a127\n" \
"v_cvt_pk_bf16_f32 v244, v244, v245\n" \
"v_cvt_pk_bf16_f32 v245, v246, v247\n" \
"v_mfma_f32_16x16x32_bf16 a[4:7], v[88:91], v[52:55], 0\n" \
"ds_read_b64_tr_b16 v[170:171], v37 offset:4608\n" \
"v_cvt_pk_bf16_f32 v246, v180, v181\n" \
"v_cvt_pk_bf16_f32 v247, v182, v183\n" \
"v_accvgpr_read_b32 v252, a152\n" \
"v_accvgpr_read_b32 v253, a153\n" \
"v_mfma_f32_16x16x32_bf16 a[36:39], v[88:91], v[56:59], 0\n" \
"s_add_u32 m0, s31, 0x8c00\n" \
"s_mov_b32 s84, 0x98000\n" \
"v_accvgpr_read_b32 v254, a154\n" \
"v_accvgpr_read_b32 v255, a155\n" \
"v_accvgpr_read_b32 v180, a184\n" \
"v_mfma_f32_16x16x32_bf16 a[8:11], v[92:95], v[52:55], 0\n" \
"buffer_load_dwordx4 v27, s[68:71], s84 offen lds\n" \
"v_accvgpr_read_b32 v181, a185\n" \
"v_accvgpr_read_b32 v182, a186\n" \
"v_accvgpr_read_b32 v183, a187\n" \
"v_mfma_f32_16x16x32_bf16 a[40:43], v[92:95], v[56:59], 0\n" \
"ds_read_b64_tr_b16 v[172:173], v38 offset:4096\n" \
"v_cvt_pk_bf16_f32 v252, v252, v253\n" \
"v_cvt_pk_bf16_f32 v253, v254, v255\n" \
"v_cvt_pk_bf16_f32 v254, v180, v181\n" \
"v_cvt_pk_bf16_f32 v255, v182, v183\n" \
"v_mfma_f32_16x16x32_bf16 a[12:15], v[96:99], v[52:55], 0\n" \
"s_add_u32 s89, s41, 0x0\n" \
"buffer_store_dwordx4 v[184:187], v20, s[80:83], s89 offen offset:0 sc1\n" \
"v_accvgpr_write_b32 a60, v252\n" \
"v_accvgpr_write_b32 a61, v253\n" \
"v_mfma_f32_16x16x32_bf16 a[44:47], v[96:99], v[56:59], 0\n" \
"ds_read_b64_tr_b16 v[174:175], v38 offset:4608\n" \
"v_accvgpr_write_b32 a62, v254\n" \
"v_accvgpr_write_b32 a63, v255\n" \
"v_accvgpr_read_b32 v252, a156\n" \
"v_accvgpr_read_b32 v253, a157\n" \
"s_waitcnt lgkmcnt(7)\n" \
"v_mfma_f32_16x16x32_bf16 a[16:19], v[100:103], v[68:71], 0\n" \
"ds_read_b64_tr_b16 v[176:177], v39 offset:4096\n" \
"v_accvgpr_read_b32 v254, a158\n" \
"v_accvgpr_read_b32 v255, a159\n" \
"v_accvgpr_read_b32 v180, a188\n" \
"v_accvgpr_read_b32 v181, a189\n" \
"v_mfma_f32_16x16x32_bf16 a[20:23], v[104:107], v[68:71], 0\n" \
"s_add_u32 s88, s41, 0x0\n" \
"s_nop 0\n" \
"buffer_store_dwordx4 v[188:191], v20, s[80:83], s88 offen offset:1024 sc1\n" \
"v_accvgpr_read_b32 v182, a190\n" \
"v_accvgpr_read_b32 v183, a191\n" \
"v_mfma_f32_16x16x32_bf16 a[52:55], v[112:115], v[68:71], 0\n" \
"ds_read_b64_tr_b16 v[178:179], v39 offset:4608\n" \
"v_cvt_pk_bf16_f32 v252, v252, v253\n" \
"v_cvt_pk_bf16_f32 v253, v254, v255\n" \
"v_cvt_pk_bf16_f32 v254, v180, v181\n" \
"v_cvt_pk_bf16_f32 v255, v182, v183\n" \
"v_mfma_f32_16x16x32_bf16 a[48:51], v[68:71], v[108:111], 0\n" \
"ds_read_b64_tr_b16 v[148:149], v32 offset:8192\n" \
"v_accvgpr_write_b32 a88, v252\n" \
"v_accvgpr_write_b32 a89, v253\n" \
"v_accvgpr_write_b32 a90, v254\n" \
"v_accvgpr_write_b32 a91, v255\n" \
"v_mfma_f32_16x16x32_bf16 a[64:67], v[84:87], v[60:63], 0\n" \
"s_add_u32 s89, s41, 0x0\n" \
"s_nop 0\n" \
"buffer_store_dwordx4 v[192:195], v20, s[80:83], s89 offen offset:2048 sc1\n" \
"v_accvgpr_read_b32 v252, a128\n" \
"v_accvgpr_read_b32 v253, a129\n" \
"v_mfma_f32_16x16x32_bf16 a[96:99], v[84:87], v[64:67], 0\n" \
"ds_read_b64_tr_b16 v[150:151], v32 offset:9216\n" \
"v_accvgpr_read_b32 v254, a130\n" \
"v_accvgpr_read_b32 v255, a131\n" \
"v_accvgpr_read_b32 v180, a160\n" \
"v_accvgpr_read_b32 v181, a161\n" \
"v_mfma_f32_16x16x32_bf16 a[68:71], v[88:91], v[60:63], 0\n" \
"ds_read_b64_tr_b16 v[152:153], v33 offset:8192\n" \
"v_accvgpr_read_b32 v182, a162\n" \
"v_accvgpr_read_b32 v183, a163\n" \
"v_cvt_pk_bf16_f32 v252, v252, v253\n" \
"v_cvt_pk_bf16_f32 v253, v254, v255\n" \
"v_mfma_f32_16x16x32_bf16 a[100:103], v[88:91], v[64:67], 0\n" \
"s_add_u32 s88, s41, 0x0\n" \
"s_nop 0\n" \
"buffer_store_dwordx4 v[196:199], v20, s[80:83], s88 offen offset:3072 sc1\n" \
"v_cvt_pk_bf16_f32 v254, v180, v181\n" \
"v_cvt_pk_bf16_f32 v255, v182, v183\n" \
"v_mfma_f32_16x16x32_bf16 a[72:75], v[92:95], v[60:63], 0\n" \
"ds_read_b64_tr_b16 v[154:155], v33 offset:9216\n" \
"v_accvgpr_write_b32 a120, v252\n" \
"v_accvgpr_write_b32 a121, v253\n" \
"v_accvgpr_write_b32 a122, v254\n" \
"v_accvgpr_write_b32 a123, v255\n" \
"v_mfma_f32_16x16x32_bf16 a[104:107], v[92:95], v[64:67], 0\n" \
"ds_read_b64_tr_b16 v[156:157], v34 offset:8192\n" \
"v_accvgpr_read_b32 v252, a132\n" \
"v_accvgpr_read_b32 v253, a133\n" \
"v_accvgpr_read_b32 v254, a134\n" \
"v_accvgpr_read_b32 v255, a135\n" \
"v_mfma_f32_16x16x32_bf16 a[76:79], v[96:99], v[60:63], 0\n" \
"ds_read_b64_tr_b16 v[158:159], v34 offset:9216\n" \
"v_accvgpr_read_b32 v180, a164\n" \
"v_accvgpr_read_b32 v181, a165\n" \
"v_accvgpr_read_b32 v182, a166\n" \
"v_accvgpr_read_b32 v183, a167\n" \
"v_mfma_f32_16x16x32_bf16 a[108:111], v[96:99], v[64:67], 0\n" \
"ds_read_b64_tr_b16 v[160:161], v35 offset:8192\n" \
"v_cvt_pk_bf16_f32 v252, v252, v253\n" \
"v_cvt_pk_bf16_f32 v253, v254, v255\n" \
"v_cvt_pk_bf16_f32 v254, v180, v181\n" \
"v_cvt_pk_bf16_f32 v255, v182, v183\n" \
"s_waitcnt lgkmcnt(15)\n" \
"s_nop 0\n" \
"v_mfma_f32_16x16x32_bf16 a[80:83], v[100:103], v[72:75], 0\n" \
"ds_read_b64_tr_b16 v[162:163], v35 offset:9216\n" \
"v_accvgpr_write_b32 a92, v252\n" \
"v_accvgpr_write_b32 a93, v253\n" \
"v_accvgpr_write_b32 a94, v254\n" \
"v_accvgpr_write_b32 a95, v255\n" \
"v_mfma_f32_16x16x32_bf16 a[84:87], v[104:107], v[72:75], 0\n" \
"ds_read_b64_tr_b16 v[164:165], v36 offset:4096\n" \
"v_accvgpr_read_b32 v252, a136\n" \
"v_accvgpr_read_b32 v253, a137\n" \
"v_accvgpr_read_b32 v254, a138\n" \
"v_accvgpr_read_b32 v255, a139\n" \
"v_mfma_f32_16x16x32_bf16 a[116:119], v[112:115], v[72:75], 0\n" \
"ds_read_b64_tr_b16 v[166:167], v36 offset:4608\n" \
"v_accvgpr_read_b32 v180, a168\n" \
"v_accvgpr_read_b32 v181, a169\n" \
"v_accvgpr_read_b32 v182, a170\n" \
"v_accvgpr_read_b32 v183, a171\n" \
"v_mfma_f32_16x16x32_bf16 a[112:115], v[72:75], v[108:111], 0\n" \
"ds_read_b128 v[116:119], v29 offset:0\n" \
"v_cvt_pk_bf16_f32 v252, v252, v253\n" \
"v_cvt_pk_bf16_f32 v253, v254, v255\n" \
"v_cvt_pk_bf16_f32 v254, v180, v181\n" \
"v_cvt_pk_bf16_f32 v255, v182, v183\n" \
"v_mfma_f32_16x16x32_bf16 a[128:131], v[84:87], v[68:71], 0\n" \
"ds_read_b128 v[120:123], v29 offset:2048\n" \
"v_accvgpr_write_b32 a124, v252\n" \
"v_accvgpr_write_b32 a125, v253\n" \
"v_accvgpr_write_b32 a126, v254\n" \
"v_accvgpr_write_b32 a127, v255\n" \
"v_mfma_f32_16x16x32_bf16 a[160:163], v[84:87], v[72:75], 0\n" \
"v_accvgpr_read_b32 v252, a140\n" \
"v_accvgpr_read_b32 v253, a141\n" \
"v_accvgpr_read_b32 v254, a142\n" \
"v_accvgpr_read_b32 v255, a143\n" \
"v_accvgpr_read_b32 v180, a172\n" \
"v_mfma_f32_16x16x32_bf16 a[132:135], v[88:91], v[68:71], 0\n" \
"ds_read_b128 v[124:127], v29 offset:4096\n" \
"v_accvgpr_read_b32 v181, a173\n" \
"v_accvgpr_read_b32 v182, a174\n" \
"v_accvgpr_read_b32 v183, a175\n" \
"v_cvt_pk_bf16_f32 v252, v252, v253\n" \
"v_mfma_f32_16x16x32_bf16 a[164:167], v[88:91], v[72:75], 0\n" \
"ds_read_b128 v[128:131], v29 offset:6144\n" \
"v_cvt_pk_bf16_f32 v253, v254, v255\n" \
"v_cvt_pk_bf16_f32 v254, v180, v181\n" \
"v_cvt_pk_bf16_f32 v255, v182, v183\n" \
"v_accvgpr_write_b32 a152, v252\n" \
"v_mfma_f32_16x16x32_bf16 a[136:139], v[92:95], v[68:71], 0\n" \
"v_accvgpr_write_b32 a153, v253\n" \
"v_accvgpr_write_b32 a154, v254\n" \
"v_accvgpr_write_b32 a155, v255\n" \
"v_accvgpr_read_b32 v252, a144\n" \
"v_accvgpr_read_b32 v253, a145\n" \
"v_mfma_f32_16x16x32_bf16 a[168:171], v[92:95], v[72:75], 0\n" \
"ds_read_b128 v[132:135], v29 offset:8192\n" \
"v_accvgpr_read_b32 v254, a146\n" \
"v_accvgpr_read_b32 v255, a147\n" \
"v_accvgpr_read_b32 v180, a176\n" \
"v_accvgpr_read_b32 v181, a177\n" \
"v_mfma_f32_16x16x32_bf16 a[140:143], v[96:99], v[68:71], 0\n" \
"v_accvgpr_read_b32 v182, a178\n" \
"v_accvgpr_read_b32 v183, a179\n" \
"v_cvt_pk_bf16_f32 v252, v252, v253\n" \
"v_cvt_pk_bf16_f32 v253, v254, v255\n" \
"v_cvt_pk_bf16_f32 v254, v180, v181\n" \
"v_mfma_f32_16x16x32_bf16 a[172:175], v[96:99], v[72:75], 0\n" \
"ds_read_b128 v[136:139], v29 offset:10240\n" \
"v_cvt_pk_bf16_f32 v255, v182, v183\n" \
"v_accvgpr_write_b32 a184, v252\n" \
"v_accvgpr_write_b32 a185, v253\n" \
"v_accvgpr_write_b32 a186, v254\n" \
"v_accvgpr_write_b32 a187, v255\n" \
"s_waitcnt lgkmcnt(15)\n" \
"s_nop 0\n" \
"v_mfma_f32_16x16x32_bf16 a[144:147], v[100:103], v[76:79], 0\n" \
"ds_read_b128 v[140:143], v29 offset:12288\n" \
"v_accvgpr_read_b32 v252, a148\n" \
"v_accvgpr_read_b32 v253, a149\n" \
"v_accvgpr_read_b32 v254, a150\n" \
"v_accvgpr_read_b32 v255, a151\n" \
"v_accvgpr_read_b32 v180, a180\n" \
"v_accvgpr_read_b32 v181, a181\n" \
"v_accvgpr_read_b32 v182, a182\n" \
"v_accvgpr_read_b32 v183, a183\n" \
"v_cvt_pk_bf16_f32 v252, v252, v253\n" \
"v_cvt_pk_bf16_f32 v253, v254, v255\n" \
"v_cvt_pk_bf16_f32 v254, v180, v181\n" \
"v_cvt_pk_bf16_f32 v255, v182, v183\n" \
"v_accvgpr_write_b32 a156, v252\n" \
"v_accvgpr_write_b32 a157, v253\n" \
"v_accvgpr_write_b32 a158, v254\n" \
"v_accvgpr_write_b32 a159, v255\n" \
"v_mfma_f32_16x16x32_bf16 a[148:151], v[104:107], v[76:79], 0\n" \
"v_accvgpr_read_b32 v252, a216\n" \
"v_accvgpr_read_b32 v253, a217\n" \
"v_accvgpr_read_b32 v254, a218\n" \
"v_accvgpr_read_b32 v255, a219\n" \
"v_accvgpr_read_b32 v180, a248\n" \
"v_mfma_f32_16x16x32_bf16 a[180:183], v[112:115], v[76:79], 0\n" \
"ds_read_b128 v[144:147], v29 offset:14336\n" \
"v_accvgpr_read_b32 v181, a249\n" \
"v_accvgpr_read_b32 v182, a250\n" \
"v_accvgpr_read_b32 v183, a251\n" \
"v_cvt_pk_bf16_f32 v252, v252, v253\n" \
"v_mfma_f32_16x16x32_bf16 a[176:179], v[76:79], v[108:111], 0\n" \
"v_cvt_pk_bf16_f32 v253, v254, v255\n" \
"v_cvt_pk_bf16_f32 v254, v180, v181\n" \
"v_cvt_pk_bf16_f32 v255, v182, v183\n" \
"v_accvgpr_write_b32 a188, v252\n" \
"v_accvgpr_write_b32 a189, v253\n" \
"v_accvgpr_write_b32 a190, v254\n" \
"v_accvgpr_write_b32 a191, v255\n" \
"v_accvgpr_read_b32 v252, a220\n" \
"v_accvgpr_read_b32 v253, a221\n" \
"v_accvgpr_read_b32 v254, a222\n" \
"v_accvgpr_read_b32 v255, a223\n" \
"v_accvgpr_read_b32 v180, a252\n" \
"v_accvgpr_read_b32 v181, a253\n" \
"v_accvgpr_read_b32 v182, a254\n" \
"v_accvgpr_read_b32 v183, a255\n" \
"v_cvt_pk_bf16_f32 v252, v252, v253\n" \
"v_cvt_pk_bf16_f32 v253, v254, v255\n" \
"v_cvt_pk_bf16_f32 v254, v180, v181\n" \
"v_cvt_pk_bf16_f32 v255, v182, v183\n" \
"v_accvgpr_write_b32 a216, v252\n" \
"v_accvgpr_write_b32 a217, v253\n" \
"v_accvgpr_write_b32 a218, v254\n" \
"v_accvgpr_write_b32 a219, v255\n" \
"v_accvgpr_read_b32 v252, a192\n" \
"v_accvgpr_read_b32 v253, a193\n" \
"v_accvgpr_read_b32 v254, a194\n" \
"v_accvgpr_read_b32 v255, a195\n" \
"v_accvgpr_read_b32 v180, a224\n" \
"v_accvgpr_read_b32 v181, a225\n" \
"v_accvgpr_read_b32 v182, a226\n" \
"v_accvgpr_read_b32 v183, a227\n" \
"v_cvt_pk_bf16_f32 v252, v252, v253\n" \
"v_cvt_pk_bf16_f32 v253, v254, v255\n" \
"v_cvt_pk_bf16_f32 v254, v180, v181\n" \
"v_cvt_pk_bf16_f32 v255, v182, v183\n" \
"v_accvgpr_write_b32 a248, v252\n" \
"v_accvgpr_write_b32 a249, v253\n" \
"v_accvgpr_write_b32 a250, v254\n" \
"v_accvgpr_write_b32 a251, v255\n" \
"v_mfma_f32_16x16x32_bf16 a[192:195], v[84:87], v[76:79], 0\n" \
"v_accvgpr_read_b32 v252, a196\n" \
"v_accvgpr_read_b32 v253, a197\n" \
"v_accvgpr_read_b32 v254, a198\n" \
"v_accvgpr_read_b32 v255, a199\n" \
"v_accvgpr_read_b32 v180, a228\n" \
"v_mfma_f32_16x16x32_bf16 a[224:227], v[84:87], v[80:83], 0\n" \
"v_accvgpr_read_b32 v181, a229\n" \
"v_accvgpr_read_b32 v182, a230\n" \
"v_accvgpr_read_b32 v183, a231\n" \
"v_cvt_pk_bf16_f32 v252, v252, v253\n" \
"v_cvt_pk_bf16_f32 v253, v254, v255\n" \
"v_cvt_pk_bf16_f32 v254, v180, v181\n" \
"v_cvt_pk_bf16_f32 v255, v182, v183\n" \
"v_accvgpr_write_b32 a220, v252\n" \
"v_accvgpr_write_b32 a221, v253\n" \
"v_accvgpr_write_b32 a222, v254\n" \
"v_accvgpr_write_b32 a223, v255\n" \
"v_mfma_f32_16x16x32_bf16 a[196:199], v[88:91], v[76:79], 0\n" \
"v_accvgpr_read_b32 v252, a200\n" \
"v_accvgpr_read_b32 v253, a201\n" \
"v_accvgpr_read_b32 v254, a202\n" \
"v_accvgpr_read_b32 v255, a203\n" \
"v_accvgpr_read_b32 v180, a232\n" \
"v_mfma_f32_16x16x32_bf16 a[228:231], v[88:91], v[80:83], 0\n" \
"v_accvgpr_read_b32 v181, a233\n" \
"v_accvgpr_read_b32 v182, a234\n" \
"v_accvgpr_read_b32 v183, a235\n" \
"v_cvt_pk_bf16_f32 v252, v252, v253\n" \
"v_cvt_pk_bf16_f32 v253, v254, v255\n" \
"v_cvt_pk_bf16_f32 v254, v180, v181\n" \
"v_cvt_pk_bf16_f32 v255, v182, v183\n" \
"v_accvgpr_write_b32 a252, v252\n" \
"v_accvgpr_write_b32 a253, v253\n" \
"v_accvgpr_write_b32 a254, v254\n" \
"v_accvgpr_write_b32 a255, v255\n" \
"v_mfma_f32_16x16x32_bf16 a[200:203], v[92:95], v[76:79], 0\n" \
"v_accvgpr_read_b32 v184, a204\n" \
"v_accvgpr_read_b32 v185, a205\n" \
"v_accvgpr_read_b32 v186, a206\n" \
"v_accvgpr_read_b32 v187, a207\n" \
"v_accvgpr_read_b32 v180, a236\n" \
"v_mfma_f32_16x16x32_bf16 a[232:235], v[92:95], v[80:83], 0\n" \
"v_accvgpr_read_b32 v181, a237\n" \
"v_accvgpr_read_b32 v182, a238\n" \
"v_accvgpr_read_b32 v183, a239\n" \
"v_cvt_pk_bf16_f32 v184, v184, v185\n" \
"v_cvt_pk_bf16_f32 v185, v186, v187\n" \
"v_cvt_pk_bf16_f32 v186, v180, v181\n" \
"v_cvt_pk_bf16_f32 v187, v182, v183\n" \
"v_mfma_f32_16x16x32_bf16 a[204:207], v[96:99], v[76:79], 0\n" \
"v_accvgpr_read_b32 v188, a208\n" \
"v_accvgpr_read_b32 v189, a209\n" \
"v_accvgpr_read_b32 v190, a210\n" \
"v_accvgpr_read_b32 v191, a211\n" \
"v_accvgpr_read_b32 v180, a240\n" \
"v_mfma_f32_16x16x32_bf16 a[236:239], v[96:99], v[80:83], 0\n" \
"v_accvgpr_read_b32 v181, a241\n" \
"v_accvgpr_read_b32 v182, a242\n" \
"v_accvgpr_read_b32 v183, a243\n" \
"v_cvt_pk_bf16_f32 v188, v188, v189\n" \
"v_cvt_pk_bf16_f32 v189, v190, v191\n" \
"v_cvt_pk_bf16_f32 v190, v180, v181\n" \
"v_cvt_pk_bf16_f32 v191, v182, v183\n" \
"v_mfma_f32_16x16x32_bf16 a[208:211], v[100:103], v[80:83], 0\n" \
"v_accvgpr_read_b32 v252, a212\n" \
"v_accvgpr_read_b32 v253, a213\n" \
"v_accvgpr_read_b32 v254, a214\n" \
"v_accvgpr_read_b32 v255, a215\n" \
"v_accvgpr_read_b32 v180, a244\n" \
"v_accvgpr_read_b32 v181, a245\n" \
"v_accvgpr_read_b32 v182, a246\n" \
"v_accvgpr_read_b32 v183, a247\n" \
"v_cvt_pk_bf16_f32 v252, v252, v253\n" \
"v_cvt_pk_bf16_f32 v253, v254, v255\n" \
"v_cvt_pk_bf16_f32 v254, v180, v181\n" \
"v_cvt_pk_bf16_f32 v255, v182, v183\n" \
"v_mfma_f32_16x16x32_bf16 a[212:215], v[104:107], v[80:83], 0\n" \
"v_mfma_f32_16x16x32_bf16 a[244:247], v[112:115], v[80:83], 0\n" \
"v_mfma_f32_16x16x32_bf16 a[240:243], v[80:83], v[108:111], 0\n" \
"s_waitcnt vmcnt(4) lgkmcnt(0)\n" \
"s_barrier\n" \
"v_mfma_f32_16x16x32_bf16 a[24:27], v[168:171], v[128:131], a[24:27]\n" \
"s_add_u32 m0, s40, 0x0\n" \
"s_nop 0\n" \
"s_add_u32 s85, s39, 0x4000\n" \
"v_mfma_f32_16x16x32_bf16 a[28:31], v[128:131], v[172:175], a[28:31]\n" \
"buffer_load_dwordx4 v20, s[80:83], s85 offen sc1 lds\n" \
"v_mfma_f32_16x16x32_bf16 a[56:59], v[176:179], v[128:131], a[56:59]\n" \
"ds_read_b64_tr_b16 v[104:105], v37 offset:32768\n" \
"v_mfma_f32_16x16x32_bf16 a[0:3], v[148:151], v[116:119], a[0:3]\n" \
"ds_read_b64_tr_b16 v[106:107], v37 offset:33280\n" \
"v_mfma_f32_16x16x32_bf16 a[32:35], v[148:151], v[120:123], a[32:35]\n" \
"s_add_u32 m0, s40, 0x1000\n" \
"s_add_u32 s84, s39, 0x5000\n" \
"v_mfma_f32_16x16x32_bf16 a[4:7], v[152:155], v[116:119], a[4:7]\n" \
"buffer_load_dwordx4 v20, s[80:83], s84 offen sc1 lds\n" \
"v_mfma_f32_16x16x32_bf16 a[36:39], v[152:155], v[120:123], a[36:39]\n" \
"ds_read_b64_tr_b16 v[108:109], v38 offset:32768\n" \
"v_mfma_f32_16x16x32_bf16 a[8:11], v[156:159], v[116:119], a[8:11]\n" \
"ds_read_b64_tr_b16 v[110:111], v38 offset:33280\n" \
"v_mfma_f32_16x16x32_bf16 a[40:43], v[156:159], v[120:123], a[40:43]\n" \
"s_add_u32 m0, s40, 0x3000\n" \
"s_mov_b32 s85, 0x100\n" \
"v_mfma_f32_16x16x32_bf16 a[12:15], v[160:163], v[116:119], a[12:15]\n" \
"buffer_load_dwordx4 v21, s[76:79], s85 offen lds\n" \
"v_mfma_f32_16x16x32_bf16 a[44:47], v[160:163], v[120:123], a[44:47]\n" \
"ds_read_b64_tr_b16 v[112:113], v39 offset:32768\n" \
"v_mfma_f32_16x16x32_bf16 a[16:19], v[164:167], v[132:135], a[16:19]\n" \
"ds_read_b64_tr_b16 v[114:115], v39 offset:33280\n" \
"v_mfma_f32_16x16x32_bf16 a[20:23], v[168:171], v[132:135], a[20:23]\n" \
"s_add_u32 m0, s30, 0x0\n" \
"s_nop 0\n" \
"s_mov_b32 s84, 0x100\n" \
"v_mfma_f32_16x16x32_bf16 a[52:55], v[176:179], v[132:135], a[52:55]\n" \
"buffer_load_dwordx4 v24, s[76:79], s84 offen lds\n" \
"v_mfma_f32_16x16x32_bf16 a[48:51], v[132:135], v[172:175], a[48:51]\n" \
"ds_read_b64_tr_b16 v[84:85], v32 offset:32768\n" \
"v_mfma_f32_16x16x32_bf16 a[64:67], v[148:151], v[124:127], a[64:67]\n" \
"ds_read_b64_tr_b16 v[86:87], v32 offset:33792\n" \
"v_mfma_f32_16x16x32_bf16 a[96:99], v[148:151], v[128:131], a[96:99]\n" \
"ds_read_b64_tr_b16 v[88:89], v33 offset:32768\n" \
"v_mfma_f32_16x16x32_bf16 a[68:71], v[152:155], v[124:127], a[68:71]\n" \
"s_add_u32 m0, s30, 0x400\n" \
"s_mov_b32 s85, 0x2500\n" \
"v_mfma_f32_16x16x32_bf16 a[100:103], v[152:155], v[128:131], a[100:103]\n" \
"buffer_load_dwordx4 v25, s[76:79], s85 offen lds\n" \
"v_mfma_f32_16x16x32_bf16 a[72:75], v[156:159], v[124:127], a[72:75]\n" \
"ds_read_b64_tr_b16 v[90:91], v33 offset:33792\n" \
"v_mfma_f32_16x16x32_bf16 a[104:107], v[156:159], v[128:131], a[104:107]\n" \
"ds_read_b64_tr_b16 v[92:93], v34 offset:32768\n" \
"v_mfma_f32_16x16x32_bf16 a[76:79], v[160:163], v[124:127], a[76:79]\n" \
"s_add_u32 m0, s31, 0x0\n" \
"s_nop 0\n" \
"s_mov_b32 s84, 0x100000\n" \
"v_mfma_f32_16x16x32_bf16 a[108:111], v[160:163], v[128:131], a[108:111]\n" \
"buffer_load_dwordx4 v26, s[68:71], s84 offen lds\n" \
"v_mfma_f32_16x16x32_bf16 a[80:83], v[164:167], v[136:139], a[80:83]\n" \
"ds_read_b64_tr_b16 v[94:95], v34 offset:33792\n" \
"v_mfma_f32_16x16x32_bf16 a[84:87], v[168:171], v[136:139], a[84:87]\n" \
"ds_read_b64_tr_b16 v[96:97], v35 offset:32768\n" \
"v_mfma_f32_16x16x32_bf16 a[116:119], v[176:179], v[136:139], a[116:119]\n" \
"s_add_u32 m0, s30, 0x800\n" \
"s_mov_b32 s85, 0x4900\n" \
"v_mfma_f32_16x16x32_bf16 a[112:115], v[136:139], v[172:175], a[112:115]\n" \
"buffer_load_dwordx4 v24, s[76:79], s85 offen lds\n" \
"v_mfma_f32_16x16x32_bf16 a[128:131], v[148:151], v[132:135], a[128:131]\n" \
"ds_read_b64_tr_b16 v[98:99], v35 offset:33792\n" \
"v_mfma_f32_16x16x32_bf16 a[160:163], v[148:151], v[136:139], a[160:163]\n" \
"ds_read_b64_tr_b16 v[100:101], v36 offset:32768\n" \
"v_mfma_f32_16x16x32_bf16 a[132:135], v[152:155], v[132:135], a[132:135]\n" \
"s_add_u32 m0, s30, 0xc00\n" \
"s_mov_b32 s84, 0x6d00\n" \
"v_mfma_f32_16x16x32_bf16 a[164:167], v[152:155], v[136:139], a[164:167]\n" \
"buffer_load_dwordx4 v25, s[76:79], s84 offen lds\n" \
"v_mfma_f32_16x16x32_bf16 a[136:139], v[156:159], v[132:135], a[136:139]\n" \
"ds_read_b64_tr_b16 v[102:103], v36 offset:33280\n" \
"v_mfma_f32_16x16x32_bf16 a[168:171], v[156:159], v[136:139], a[168:171]\n" \
"ds_read_b128 v[52:55], v28 offset:32768\n" \
"v_mfma_f32_16x16x32_bf16 a[140:143], v[160:163], v[132:135], a[140:143]\n" \
"ds_read_b128 v[56:59], v28 offset:34816\n" \
"v_mfma_f32_16x16x32_bf16 a[172:175], v[160:163], v[136:139], a[172:175]\n" \
"s_add_u32 m0, s31, 0x400\n" \
"s_mov_b32 s85, 0x108000\n" \
"v_mfma_f32_16x16x32_bf16 a[144:147], v[164:167], v[140:143], a[144:147]\n" \
"buffer_load_dwordx4 v26, s[68:71], s85 offen lds\n" \
"v_mfma_f32_16x16x32_bf16 a[148:151], v[168:171], v[140:143], a[148:151]\n" \
"ds_read_b128 v[60:63], v28 offset:36864\n" \
"v_mfma_f32_16x16x32_bf16 a[180:183], v[176:179], v[140:143], a[180:183]\n" \
"ds_read_b128 v[64:67], v28 offset:38912\n" \
"v_mfma_f32_16x16x32_bf16 a[176:179], v[140:143], v[172:175], a[176:179]\n" \
"s_add_u32 m0, s30, 0x1000\n" \
"s_mov_b32 s84, 0x9100\n" \
"v_mfma_f32_16x16x32_bf16 a[192:195], v[148:151], v[140:143], a[192:195]\n" \
"buffer_load_dwordx4 v24, s[76:79], s84 offen lds\n" \
"v_mfma_f32_16x16x32_bf16 a[224:227], v[148:151], v[144:147], a[224:227]\n" \
"ds_read_b128 v[68:71], v28 offset:40960\n" \
"v_mfma_f32_16x16x32_bf16 a[196:199], v[152:155], v[140:143], a[196:199]\n" \
"ds_read_b128 v[72:75], v28 offset:43008\n" \
"v_mfma_f32_16x16x32_bf16 a[228:231], v[152:155], v[144:147], a[228:231]\n" \
"s_add_u32 m0, s30, 0x1400\n" \
"s_mov_b32 s85, 0xb500\n" \
"v_mfma_f32_16x16x32_bf16 a[200:203], v[156:159], v[140:143], a[200:203]\n" \
"buffer_load_dwordx4 v25, s[76:79], s85 offen lds\n" \
"v_mfma_f32_16x16x32_bf16 a[232:235], v[156:159], v[144:147], a[232:235]\n" \
"ds_read_b128 v[76:79], v28 offset:45056\n" \
"v_mfma_f32_16x16x32_bf16 a[204:207], v[160:163], v[140:143], a[204:207]\n" \
"ds_read_b128 v[80:83], v28 offset:47104\n" \
"v_mfma_f32_16x16x32_bf16 a[236:239], v[160:163], v[144:147], a[236:239]\n" \
"s_add_u32 m0, s31, 0x800\n" \
"s_mov_b32 s84, 0x110000\n" \
"v_mfma_f32_16x16x32_bf16 a[208:211], v[164:167], v[144:147], a[208:211]\n" \
"buffer_load_dwordx4 v27, s[68:71], s84 offen lds\n" \
"v_mfma_f32_16x16x32_bf16 a[212:215], v[168:171], v[144:147], a[212:215]\n" \
"v_mfma_f32_16x16x32_bf16 a[244:247], v[176:179], v[144:147], a[244:247]\n" \
"v_mfma_f32_16x16x32_bf16 a[240:243], v[144:147], v[172:175], a[240:243]\n" \
"s_waitcnt lgkmcnt(4)\n" \
"v_mfma_f32_16x16x32_bf16 a[24:27], v[104:107], v[64:67], a[24:27]\n" \
"s_add_u32 m0, s30, 0x1800\n" \
"s_mov_b32 s84, 0xd900\n" \
"v_mfma_f32_16x16x32_bf16 a[28:31], v[64:67], v[108:111], a[28:31]\n" \
"buffer_load_dwordx4 v24, s[76:79], s84 offen lds\n" \
"v_mfma_f32_16x16x32_bf16 a[56:59], v[112:115], v[64:67], a[56:59]\n" \
"ds_read_b64_tr_b16 v[168:169], v37 offset:36864\n" \
"s_movk_i32 s11, 0x4000\n" \
"v_mfma_f32_16x16x32_bf16 a[0:3], v[84:87], v[52:55], a[0:3]\n" \
"s_add_u32 m0, s30, 0x1c00\n" \
"s_mov_b32 s85, 0xfd00\n" \
"v_mfma_f32_16x16x32_bf16 a[32:35], v[84:87], v[56:59], a[32:35]\n" \
"buffer_load_dwordx4 v25, s[76:79], s85 offen lds\n" \
"v_mfma_f32_16x16x32_bf16 a[4:7], v[88:91], v[52:55], a[4:7]\n" \
"ds_read_b64_tr_b16 v[170:171], v37 offset:37376\n" \
"s_lshl_b32 s4, s2, 2\n" \
"v_mfma_f32_16x16x32_bf16 a[36:39], v[88:91], v[56:59], a[36:39]\n" \
"s_add_u32 m0, s31, 0xc00\n" \
"s_mov_b32 s84, 0x118000\n" \
"v_mfma_f32_16x16x32_bf16 a[8:11], v[92:95], v[52:55], a[8:11]\n" \
"buffer_load_dwordx4 v27, s[68:71], s84 offen lds\n" \
"v_mfma_f32_16x16x32_bf16 a[40:43], v[92:95], v[56:59], a[40:43]\n" \
"ds_read_b64_tr_b16 v[172:173], v38 offset:36864\n" \
"s_and_b32 s4, s4, 28\n" \
"v_mfma_f32_16x16x32_bf16 a[12:15], v[96:99], v[52:55], a[12:15]\n" \
"ds_read_b64_tr_b16 v[174:175], v38 offset:37376\n" \
"s_bfe_u32 s5, s2, 0x20003\n" \
"v_mfma_f32_16x16x32_bf16 a[44:47], v[96:99], v[56:59], a[44:47]\n" \
"ds_read_b64_tr_b16 v[176:177], v39 offset:36864\n" \
"s_or_b32 s4, s4, s5\n" \
"s_waitcnt lgkmcnt(8)\n" \
"v_mfma_f32_16x16x32_bf16 a[16:19], v[100:103], v[68:71], a[16:19]\n" \
"ds_read_b64_tr_b16 v[178:179], v39 offset:37376\n" \
"s_lshr_b32 s5, s2, 5\n" \
"v_mfma_f32_16x16x32_bf16 a[20:23], v[104:107], v[68:71], a[20:23]\n" \
"ds_read_b64_tr_b16 v[148:149], v32 offset:40960\n" \
"s_lshr_b32 s19, s3, 1\n" \
"v_mfma_f32_16x16x32_bf16 a[52:55], v[112:115], v[68:71], a[52:55]\n" \
"ds_read_b64_tr_b16 v[150:151], v32 offset:41984\n" \
"s_lshl_b32 s18, s5, 1\n" \
"v_mfma_f32_16x16x32_bf16 a[48:51], v[68:71], v[108:111], a[48:51]\n" \
"ds_read_b64_tr_b16 v[152:153], v33 offset:40960\n" \
"s_lshl_b32 s6, s4, 20\n" \
"v_mfma_f32_16x16x32_bf16 a[64:67], v[84:87], v[60:63], a[64:67]\n" \
"ds_read_b64_tr_b16 v[154:155], v33 offset:41984\n" \
"s_lshl_b32 s7, s4, 8\n" \
"v_mfma_f32_16x16x32_bf16 a[96:99], v[84:87], v[64:67], a[96:99]\n" \
"ds_read_b64_tr_b16 v[156:157], v34 offset:40960\n" \
"s_and_b32 s13, s3, 1\n" \
"v_mfma_f32_16x16x32_bf16 a[68:71], v[88:91], v[60:63], a[68:71]\n" \
"ds_read_b64_tr_b16 v[158:159], v34 offset:41984\n" \
"s_add_i32 s14, s18, s19\n" \
"v_mfma_f32_16x16x32_bf16 a[100:103], v[88:91], v[64:67], a[100:103]\n" \
"s_add_u32 s6, s26, s6\n" \
"s_addc_u32 s9, s27, 0\n" \
"v_mfma_f32_16x16x32_bf16 a[72:75], v[92:95], v[60:63], a[72:75]\n" \
"ds_read_b64_tr_b16 v[160:161], v35 offset:40960\n" \
"v_mfma_f32_16x16x32_bf16 a[104:107], v[92:95], v[64:67], a[104:107]\n" \
"s_add_u32 s8, s6, 0x1000000\n" \
"s_addc_u32 s6, s9, 0\n" \
"v_mfma_f32_16x16x32_bf16 a[76:79], v[96:99], v[60:63], a[76:79]\n" \
"s_add_u32 s89, s41, 0x1000\n" \
"buffer_store_dwordx4 v[200:203], v20, s[80:83], s89 offen offset:0 sc1\n" \
"v_mfma_f32_16x16x32_bf16 a[108:111], v[96:99], v[64:67], a[108:111]\n" \
"ds_read_b64_tr_b16 v[162:163], v35 offset:41984\n" \
"s_and_b32 s9, s6, 0xffff\n" \
"s_waitcnt lgkmcnt(15)\n" \
"v_mfma_f32_16x16x32_bf16 a[80:83], v[100:103], v[72:75], a[80:83]\n" \
"ds_read_b64_tr_b16 v[164:165], v36 offset:36864\n" \
"s_lshl_b32 s6, s5, 16\n" \
"v_mfma_f32_16x16x32_bf16 a[84:87], v[104:107], v[72:75], a[84:87]\n" \
"ds_read_b64_tr_b16 v[166:167], v36 offset:37376\n" \
"s_sub_i32 s10, 0x100000, s6\n" \
"v_mfma_f32_16x16x32_bf16 a[116:119], v[112:115], v[72:75], a[116:119]\n" \
"s_mov_b32 s6, 0x8000\n" \
"v_mfma_f32_16x16x32_bf16 a[112:115], v[72:75], v[108:111], a[112:115]\n" \
"ds_read_b128 v[116:119], v29 offset:32768\n" \
"v_mfma_f32_16x16x32_bf16 a[128:131], v[84:87], v[68:71], a[128:131]\n" \
"ds_read_b128 v[120:123], v29 offset:34816\n" \
"v_mfma_f32_16x16x32_bf16 a[160:163], v[84:87], v[72:75], a[160:163]\n" \
"s_cmp_lg_u32 s5, 0\n" \
"s_cselect_b32 s17, s11, s6\n" \
"v_mfma_f32_16x16x32_bf16 a[132:135], v[88:91], v[68:71], a[132:135]\n" \
"s_add_u32 s88, s41, 0x1000\n" \
"buffer_store_dwordx4 v[204:207], v20, s[80:83], s88 offen offset:1024 sc1\n" \
"v_mfma_f32_16x16x32_bf16 a[164:167], v[88:91], v[72:75], a[164:167]\n" \
"ds_read_b128 v[124:127], v29 offset:36864\n" \
"s_lshl_b32 s6, s3, 1\n" \
"v_mfma_f32_16x16x32_bf16 a[136:139], v[92:95], v[68:71], a[136:139]\n" \
"ds_read_b128 v[128:131], v29 offset:38912\n" \
"s_lshl_b32 s5, s5, 3\n" \
"v_mfma_f32_16x16x32_bf16 a[168:171], v[92:95], v[72:75], a[168:171]\n" \
"ds_read_b128 v[132:135], v29 offset:40960\n" \
"s_and_b32 s6, s6, 4\n" \
"v_mfma_f32_16x16x32_bf16 a[140:143], v[96:99], v[68:71], a[140:143]\n" \
"s_or_b32 s12, s5, s6\n" \
"s_sub_i32 s5, s13, s5\n" \
"v_mfma_f32_16x16x32_bf16 a[172:175], v[96:99], v[72:75], a[172:175]\n" \
"ds_read_b128 v[136:139], v29 offset:43008\n" \
"s_or_b32 s5, s5, s6\n" \
"v_mfma_f32_16x16x32_bf16 a[144:147], v[100:103], v[76:79], a[144:147]\n" \
"ds_read_b128 v[140:143], v29 offset:45056\n" \
"s_or_b32 s6, s12, s13\n" \
"v_mfma_f32_16x16x32_bf16 a[148:151], v[104:107], v[76:79], a[148:151]\n" \
"s_add_u32 s89, s41, 0x1000\n" \
"buffer_store_dwordx4 v[208:211], v20, s[80:83], s89 offen offset:2048 sc1\n" \
"v_mfma_f32_16x16x32_bf16 a[180:183], v[112:115], v[76:79], a[180:183]\n" \
"ds_read_b128 v[144:147], v29 offset:47104\n" \
"s_lshl_b32 s4, s4, 2\n" \
"v_mfma_f32_16x16x32_bf16 a[176:179], v[76:79], v[108:111], a[176:179]\n" \
"s_mulk_i32 s6, 0x92\n" \
"s_add_i32 s4, s4, 18\n" \
"v_mfma_f32_16x16x32_bf16 a[192:195], v[84:87], v[76:79], a[192:195]\n" \
"s_mulk_i32 s5, 0x92\n" \
"s_add_i32 s6, s4, s6\n" \
"s_waitcnt lgkmcnt(15)\n" \
"v_mfma_f32_16x16x32_bf16 a[224:227], v[84:87], v[80:83], a[224:227]\n" \
"s_add_i32 s5, s5, s4\n" \
"s_lshl_b32 s4, s3, 11\n" \
"v_mfma_f32_16x16x32_bf16 a[196:199], v[88:91], v[76:79], a[196:199]\n" \
"s_lshl_b32 s15, s13, 12\n" \
"s_lshl_b32 s16, s19, 14\n" \
"v_mfma_f32_16x16x32_bf16 a[228:231], v[88:91], v[80:83], a[228:231]\n" \
"s_mul_i32 s19, s19, 0xe000\n" \
"s_mov_b32 s11, 0x20000\n" \
"v_mfma_f32_16x16x32_bf16 a[200:203], v[92:95], v[76:79], a[200:203]\n" \
"s_add_u32 s88, s41, 0x1000\n" \
"buffer_store_dwordx4 v[212:215], v20, s[80:83], s88 offen offset:3072 sc1\n" \
"v_mfma_f32_16x16x32_bf16 a[232:235], v[92:95], v[80:83], a[232:235]\n" \
"s_or_b32 s12, s4, 0xc000\n" \
"s_lshl_b32 s4, s3, 8\n" \
"v_mfma_f32_16x16x32_bf16 a[204:207], v[96:99], v[76:79], a[204:207]\n" \
"s_or_b32 s22, s15, s16\n" \
"s_or_b32 s23, s19, s15\n" \
"v_mfma_f32_16x16x32_bf16 a[236:239], v[96:99], v[80:83], a[236:239]\n" \
"s_lshl_b32 s60, s6, 10\n" \
"s_lshl_b32 s61, s5, 10\n" \
"v_mfma_f32_16x16x32_bf16 a[208:211], v[100:103], v[80:83], a[208:211]\n" \
"s_lshl_b32 s14, s14, 15\n" \
"s_or_b32 s20, s4, 0x26c00\n" \
"v_mfma_f32_16x16x32_bf16 a[212:215], v[104:107], v[80:83], a[212:215]\n" \
"s_or_b32 s13, s22, 0x8000\n" \
"s_add_i32 s22, s22, 0x14000\n" \
"v_mfma_f32_16x16x32_bf16 a[244:247], v[112:115], v[80:83], a[244:247]\n" \
"s_bitset1_b32 s23, 16\n" \
"s_or_b32 s24, s7, 0x5be8020\n" \
"v_mfma_f32_16x16x32_bf16 a[240:243], v[80:83], v[108:111], a[240:243]\n" \
"s_waitcnt vmcnt(4) lgkmcnt(0)\n" \
"s_barrier\n" \
"v_mfma_f32_16x16x32_bf16 a[24:27], v[168:171], v[128:131], a[24:27]\n" \
"s_add_u32 m0, s40, 0x8000\n" \
"s_add_u32 s85, s39, 0x6000\n" \
"v_mfma_f32_16x16x32_bf16 a[28:31], v[128:131], v[172:175], a[28:31]\n" \
"buffer_load_dwordx4 v20, s[80:83], s85 offen sc1 lds\n" \
"v_mfma_f32_16x16x32_bf16 a[56:59], v[176:179], v[128:131], a[56:59]\n" \
"ds_read_b64_tr_b16 v[104:105], v37 offset:0\n" \
"s_and_b32 s57, s27, 0xffff\n" \
"v_mfma_f32_16x16x32_bf16 a[0:3], v[148:151], v[116:119], a[0:3]\n" \
"ds_read_b64_tr_b16 v[106:107], v37 offset:512\n" \
"s_mov_b32 s58, 0x6000000\n" \
"v_mfma_f32_16x16x32_bf16 a[32:35], v[148:151], v[120:123], a[32:35]\n" \
"s_add_u32 m0, s40, 0x9000\n" \
"s_add_u32 s84, s39, 0x7000\n" \
"v_mfma_f32_16x16x32_bf16 a[4:7], v[152:155], v[116:119], a[4:7]\n" \
"buffer_load_dwordx4 v20, s[80:83], s84 offen sc1 lds\n" \
"v_mfma_f32_16x16x32_bf16 a[36:39], v[152:155], v[120:123], a[36:39]\n" \
"ds_read_b64_tr_b16 v[108:109], v38 offset:0\n" \
"s_mov_b32 s56, s26\n" \
"v_mfma_f32_16x16x32_bf16 a[8:11], v[156:159], v[116:119], a[8:11]\n" \
"ds_read_b64_tr_b16 v[110:111], v38 offset:512\n" \
"s_mov_b32 s59, s11\n" \
"v_mfma_f32_16x16x32_bf16 a[40:43], v[156:159], v[120:123], a[40:43]\n" \
"s_add_u32 m0, s40, 0xb000\n" \
"s_mov_b32 s85, 0x180\n" \
"v_mfma_f32_16x16x32_bf16 a[12:15], v[160:163], v[116:119], a[12:15]\n" \
"buffer_load_dwordx4 v21, s[76:79], s85 offen lds\n" \
"v_mfma_f32_16x16x32_bf16 a[44:47], v[160:163], v[120:123], a[44:47]\n" \
"ds_read_b64_tr_b16 v[112:113], v39 offset:0\n" \
"s_add_i32 s60, s60, 0x3000000\n" \
"v_mfma_f32_16x16x32_bf16 a[16:19], v[164:167], v[132:135], a[16:19]\n" \
"ds_read_b64_tr_b16 v[114:115], v39 offset:512\n" \
"s_add_i32 s61, s61, 0x411c000\n" \
"v_mfma_f32_16x16x32_bf16 a[20:23], v[168:171], v[132:135], a[20:23]\n" \
"s_add_u32 m0, s30, 0x8000\n" \
"s_mov_b32 s84, 0x180\n" \
"v_mfma_f32_16x16x32_bf16 a[52:55], v[176:179], v[132:135], a[52:55]\n" \
"buffer_load_dwordx4 v24, s[76:79], s84 offen lds\n" \
"v_mfma_f32_16x16x32_bf16 a[48:51], v[132:135], v[172:175], a[48:51]\n" \
"ds_read_b64_tr_b16 v[84:85], v32 offset:0\n" \
"s_sub_i32 s19, 14, s18\n" \
"s_nop 0\n" \
"v_mfma_f32_16x16x32_bf16 a[64:67], v[148:151], v[124:127], a[64:67]\n" \
"ds_read_b64_tr_b16 v[86:87], v32 offset:1024\n" \
"s_mov_b32 s21, 0x71800000\n" \
"v_mfma_f32_16x16x32_bf16 a[96:99], v[148:151], v[128:131], a[96:99]\n" \
"ds_read_b64_tr_b16 v[88:89], v33 offset:0\n" \
"v_mfma_f32_16x16x32_bf16 a[68:71], v[152:155], v[124:127], a[68:71]\n" \
"s_add_u32 m0, s30, 0x8400\n" \
"s_mov_b32 s85, 0x2580\n" \
"v_mfma_f32_16x16x32_bf16 a[100:103], v[152:155], v[128:131], a[100:103]\n" \
"buffer_load_dwordx4 v25, s[76:79], s85 offen lds\n" \
"v_mfma_f32_16x16x32_bf16 a[72:75], v[156:159], v[124:127], a[72:75]\n" \
"ds_read_b64_tr_b16 v[90:91], v33 offset:1024\n" \
"v_mfma_f32_16x16x32_bf16 a[104:107], v[156:159], v[128:131], a[104:107]\n" \
"ds_read_b64_tr_b16 v[92:93], v34 offset:0\n" \
"v_mfma_f32_16x16x32_bf16 a[76:79], v[160:163], v[124:127], a[76:79]\n" \
"s_add_u32 m0, s31, 0x8000\n" \
"s_mov_b32 s84, 0x180000\n" \
"v_mfma_f32_16x16x32_bf16 a[108:111], v[160:163], v[128:131], a[108:111]\n" \
"buffer_load_dwordx4 v26, s[68:71], s84 offen lds\n" \
"v_mfma_f32_16x16x32_bf16 a[80:83], v[164:167], v[136:139], a[80:83]\n" \
"ds_read_b64_tr_b16 v[94:95], v34 offset:1024\n" \
"v_mfma_f32_16x16x32_bf16 a[84:87], v[168:171], v[136:139], a[84:87]\n" \
"ds_read_b64_tr_b16 v[96:97], v35 offset:0\n" \
"v_mfma_f32_16x16x32_bf16 a[116:119], v[176:179], v[136:139], a[116:119]\n" \
"s_add_u32 m0, s30, 0x8800\n" \
"s_mov_b32 s85, 0x4980\n" \
"v_mfma_f32_16x16x32_bf16 a[112:115], v[136:139], v[172:175], a[112:115]\n" \
"buffer_load_dwordx4 v24, s[76:79], s85 offen lds\n" \
"v_mfma_f32_16x16x32_bf16 a[128:131], v[148:151], v[132:135], a[128:131]\n" \
"ds_read_b64_tr_b16 v[98:99], v35 offset:1024\n" \
"v_mfma_f32_16x16x32_bf16 a[160:163], v[148:151], v[136:139], a[160:163]\n" \
"ds_read_b64_tr_b16 v[100:101], v36 offset:0\n" \
"v_mfma_f32_16x16x32_bf16 a[132:135], v[152:155], v[132:135], a[132:135]\n" \
"s_add_u32 m0, s30, 0x8c00\n" \
"s_mov_b32 s84, 0x6d80\n" \
"v_mfma_f32_16x16x32_bf16 a[164:167], v[152:155], v[136:139], a[164:167]\n" \
"buffer_load_dwordx4 v25, s[76:79], s84 offen lds\n" \
"v_mfma_f32_16x16x32_bf16 a[136:139], v[156:159], v[132:135], a[136:139]\n" \
"ds_read_b64_tr_b16 v[102:103], v36 offset:512\n" \
"v_mfma_f32_16x16x32_bf16 a[168:171], v[156:159], v[136:139], a[168:171]\n" \
"ds_read_b128 v[52:55], v28 offset:0\n" \
"v_mfma_f32_16x16x32_bf16 a[140:143], v[160:163], v[132:135], a[140:143]\n" \
"ds_read_b128 v[56:59], v28 offset:2048\n" \
"v_mfma_f32_16x16x32_bf16 a[172:175], v[160:163], v[136:139], a[172:175]\n" \
"s_add_u32 m0, s31, 0x8400\n" \
"s_mov_b32 s85, 0x188000\n" \
"v_mfma_f32_16x16x32_bf16 a[144:147], v[164:167], v[140:143], a[144:147]\n" \
"buffer_load_dwordx4 v26, s[68:71], s85 offen lds\n" \
"v_mfma_f32_16x16x32_bf16 a[148:151], v[168:171], v[140:143], a[148:151]\n" \
"ds_read_b128 v[60:63], v28 offset:4096\n" \
"v_mfma_f32_16x16x32_bf16 a[180:183], v[176:179], v[140:143], a[180:183]\n" \
"ds_read_b128 v[64:67], v28 offset:6144\n" \
"v_mfma_f32_16x16x32_bf16 a[176:179], v[140:143], v[172:175], a[176:179]\n" \
"s_add_u32 m0, s30, 0x9000\n" \
"s_mov_b32 s84, 0x9180\n" \
"v_mfma_f32_16x16x32_bf16 a[192:195], v[148:151], v[140:143], a[192:195]\n" \
"buffer_load_dwordx4 v24, s[76:79], s84 offen lds\n" \
"v_mfma_f32_16x16x32_bf16 a[224:227], v[148:151], v[144:147], a[224:227]\n" \
"ds_read_b128 v[68:71], v28 offset:8192\n" \
"v_mfma_f32_16x16x32_bf16 a[196:199], v[152:155], v[140:143], a[196:199]\n" \
"ds_read_b128 v[72:75], v28 offset:10240\n" \
"v_mfma_f32_16x16x32_bf16 a[228:231], v[152:155], v[144:147], a[228:231]\n" \
"s_add_u32 m0, s30, 0x9400\n" \
"s_mov_b32 s85, 0xb580\n" \
"v_mfma_f32_16x16x32_bf16 a[200:203], v[156:159], v[140:143], a[200:203]\n" \
"buffer_load_dwordx4 v25, s[76:79], s85 offen lds\n" \
"v_mfma_f32_16x16x32_bf16 a[232:235], v[156:159], v[144:147], a[232:235]\n" \
"ds_read_b128 v[76:79], v28 offset:12288\n" \
"v_mfma_f32_16x16x32_bf16 a[204:207], v[160:163], v[140:143], a[204:207]\n" \
"ds_read_b128 v[80:83], v28 offset:14336\n" \
"v_mfma_f32_16x16x32_bf16 a[236:239], v[160:163], v[144:147], a[236:239]\n" \
"s_add_u32 m0, s31, 0x8800\n" \
"s_mov_b32 s84, 0x190000\n" \
"v_mfma_f32_16x16x32_bf16 a[208:211], v[164:167], v[144:147], a[208:211]\n" \
"buffer_load_dwordx4 v27, s[68:71], s84 offen lds\n" \
"v_mfma_f32_16x16x32_bf16 a[212:215], v[168:171], v[144:147], a[212:215]\n" \
"v_mfma_f32_16x16x32_bf16 a[244:247], v[176:179], v[144:147], a[244:247]\n" \
"v_mfma_f32_16x16x32_bf16 a[240:243], v[144:147], v[172:175], a[240:243]\n" \
"s_waitcnt lgkmcnt(4)\n" \
"s_nop 0\n" \
"v_mfma_f32_16x16x32_bf16 a[24:27], v[104:107], v[64:67], a[24:27]\n" \
"s_add_u32 m0, s30, 0x9800\n" \
"s_mov_b32 s84, 0xd980\n" \
"v_mfma_f32_16x16x32_bf16 a[28:31], v[64:67], v[108:111], a[28:31]\n" \
"buffer_load_dwordx4 v24, s[76:79], s84 offen lds\n" \
"v_mfma_f32_16x16x32_bf16 a[56:59], v[112:115], v[64:67], a[56:59]\n" \
"ds_read_b64_tr_b16 v[168:169], v37 offset:4096\n" \
"v_mfma_f32_16x16x32_bf16 a[0:3], v[84:87], v[52:55], a[0:3]\n" \
"s_add_u32 m0, s30, 0x9c00\n" \
"s_mov_b32 s85, 0xfd80\n" \
"v_mfma_f32_16x16x32_bf16 a[32:35], v[84:87], v[56:59], a[32:35]\n" \
"buffer_load_dwordx4 v25, s[76:79], s85 offen lds\n" \
"v_mfma_f32_16x16x32_bf16 a[4:7], v[88:91], v[52:55], a[4:7]\n" \
"ds_read_b64_tr_b16 v[170:171], v37 offset:4608\n" \
"v_mfma_f32_16x16x32_bf16 a[36:39], v[88:91], v[56:59], a[36:39]\n" \
"s_add_u32 m0, s31, 0x8c00\n" \
"s_mov_b32 s84, 0x198000\n" \
"v_mfma_f32_16x16x32_bf16 a[8:11], v[92:95], v[52:55], a[8:11]\n" \
"buffer_load_dwordx4 v27, s[68:71], s84 offen lds\n" \
"v_mfma_f32_16x16x32_bf16 a[40:43], v[92:95], v[56:59], a[40:43]\n" \
"ds_read_b64_tr_b16 v[172:173], v38 offset:4096\n" \
"v_mfma_f32_16x16x32_bf16 a[12:15], v[96:99], v[52:55], a[12:15]\n" \
"ds_read_b64_tr_b16 v[174:175], v38 offset:4608\n" \
"v_mfma_f32_16x16x32_bf16 a[44:47], v[96:99], v[56:59], a[44:47]\n" \
"ds_read_b64_tr_b16 v[176:177], v39 offset:4096\n" \
"s_waitcnt lgkmcnt(8)\n" \
"s_nop 0\n" \
"v_mfma_f32_16x16x32_bf16 a[16:19], v[100:103], v[68:71], a[16:19]\n" \
"ds_read_b64_tr_b16 v[178:179], v39 offset:4608\n" \
"v_mfma_f32_16x16x32_bf16 a[20:23], v[104:107], v[68:71], a[20:23]\n" \
"ds_read_b64_tr_b16 v[148:149], v32 offset:8192\n" \
"v_mfma_f32_16x16x32_bf16 a[52:55], v[112:115], v[68:71], a[52:55]\n" \
"ds_read_b64_tr_b16 v[150:151], v32 offset:9216\n" \
"v_mfma_f32_16x16x32_bf16 a[48:51], v[68:71], v[108:111], a[48:51]\n" \
"ds_read_b64_tr_b16 v[152:153], v33 offset:8192\n" \
"v_mfma_f32_16x16x32_bf16 a[64:67], v[84:87], v[60:63], a[64:67]\n" \
"ds_read_b64_tr_b16 v[154:155], v33 offset:9216\n" \
"v_mfma_f32_16x16x32_bf16 a[96:99], v[84:87], v[64:67], a[96:99]\n" \
"ds_read_b64_tr_b16 v[156:157], v34 offset:8192\n" \
"v_mfma_f32_16x16x32_bf16 a[68:71], v[88:91], v[60:63], a[68:71]\n" \
"ds_read_b64_tr_b16 v[158:159], v34 offset:9216\n" \
"v_mfma_f32_16x16x32_bf16 a[100:103], v[88:91], v[64:67], a[100:103]\n" \
"v_mfma_f32_16x16x32_bf16 a[72:75], v[92:95], v[60:63], a[72:75]\n" \
"ds_read_b64_tr_b16 v[160:161], v35 offset:8192\n" \
"v_mfma_f32_16x16x32_bf16 a[104:107], v[92:95], v[64:67], a[104:107]\n" \
"v_mfma_f32_16x16x32_bf16 a[76:79], v[96:99], v[60:63], a[76:79]\n" \
"s_add_u32 s89, s41, 0x2000\n" \
"buffer_store_dwordx4 v[216:219], v20, s[80:83], s89 offen offset:0 sc1\n" \
"v_mfma_f32_16x16x32_bf16 a[108:111], v[96:99], v[64:67], a[108:111]\n" \
"ds_read_b64_tr_b16 v[162:163], v35 offset:9216\n" \
"s_waitcnt lgkmcnt(15)\n" \
"s_nop 0\n" \
"v_mfma_f32_16x16x32_bf16 a[80:83], v[100:103], v[72:75], a[80:83]\n" \
"ds_read_b64_tr_b16 v[164:165], v36 offset:4096\n" \
"v_mfma_f32_16x16x32_bf16 a[84:87], v[104:107], v[72:75], a[84:87]\n" \
"ds_read_b64_tr_b16 v[166:167], v36 offset:4608\n" \
"v_mfma_f32_16x16x32_bf16 a[116:119], v[112:115], v[72:75], a[116:119]\n" \
"v_mfma_f32_16x16x32_bf16 a[112:115], v[72:75], v[108:111], a[112:115]\n" \
"ds_read_b128 v[116:119], v29 offset:0\n" \
"v_mfma_f32_16x16x32_bf16 a[128:131], v[84:87], v[68:71], a[128:131]\n" \
"ds_read_b128 v[120:123], v29 offset:2048\n" \
"v_mfma_f32_16x16x32_bf16 a[160:163], v[84:87], v[72:75], a[160:163]\n" \
"v_mfma_f32_16x16x32_bf16 a[132:135], v[88:91], v[68:71], a[132:135]\n" \
"s_add_u32 s88, s41, 0x2000\n" \
"buffer_store_dwordx4 v[220:223], v20, s[80:83], s88 offen offset:1024 sc1\n" \
"v_mfma_f32_16x16x32_bf16 a[164:167], v[88:91], v[72:75], a[164:167]\n" \
"ds_read_b128 v[124:127], v29 offset:4096\n" \
"v_mfma_f32_16x16x32_bf16 a[136:139], v[92:95], v[68:71], a[136:139]\n" \
"ds_read_b128 v[128:131], v29 offset:6144\n" \
"v_mfma_f32_16x16x32_bf16 a[168:171], v[92:95], v[72:75], a[168:171]\n" \
"ds_read_b128 v[132:135], v29 offset:8192\n" \
"v_mfma_f32_16x16x32_bf16 a[140:143], v[96:99], v[68:71], a[140:143]\n" \
"v_mfma_f32_16x16x32_bf16 a[172:175], v[96:99], v[72:75], a[172:175]\n" \
"ds_read_b128 v[136:139], v29 offset:10240\n" \
"v_mfma_f32_16x16x32_bf16 a[144:147], v[100:103], v[76:79], a[144:147]\n" \
"ds_read_b128 v[140:143], v29 offset:12288\n" \
"v_mfma_f32_16x16x32_bf16 a[148:151], v[104:107], v[76:79], a[148:151]\n" \
"s_add_u32 s89, s41, 0x2000\n" \
"buffer_store_dwordx4 v[224:227], v20, s[80:83], s89 offen offset:2048 sc1\n" \
"v_mfma_f32_16x16x32_bf16 a[180:183], v[112:115], v[76:79], a[180:183]\n" \
"ds_read_b128 v[144:147], v29 offset:14336\n" \
"v_mfma_f32_16x16x32_bf16 a[176:179], v[76:79], v[108:111], a[176:179]\n" \
"v_mfma_f32_16x16x32_bf16 a[192:195], v[84:87], v[76:79], a[192:195]\n" \
"s_waitcnt lgkmcnt(15)\n" \
"s_nop 0\n" \
"v_mfma_f32_16x16x32_bf16 a[224:227], v[84:87], v[80:83], a[224:227]\n" \
"v_mfma_f32_16x16x32_bf16 a[196:199], v[88:91], v[76:79], a[196:199]\n" \
"v_mfma_f32_16x16x32_bf16 a[228:231], v[88:91], v[80:83], a[228:231]\n" \
"v_mfma_f32_16x16x32_bf16 a[200:203], v[92:95], v[76:79], a[200:203]\n" \
"s_add_u32 s88, s41, 0x2000\n" \
"buffer_store_dwordx4 v[228:231], v20, s[80:83], s88 offen offset:3072 sc1\n" \
"v_mfma_f32_16x16x32_bf16 a[232:235], v[92:95], v[80:83], a[232:235]\n" \
"v_mfma_f32_16x16x32_bf16 a[204:207], v[96:99], v[76:79], a[204:207]\n" \
"v_mfma_f32_16x16x32_bf16 a[236:239], v[96:99], v[80:83], a[236:239]\n" \
"v_mfma_f32_16x16x32_bf16 a[208:211], v[100:103], v[80:83], a[208:211]\n" \
"v_mfma_f32_16x16x32_bf16 a[212:215], v[104:107], v[80:83], a[212:215]\n" \
"v_mfma_f32_16x16x32_bf16 a[244:247], v[112:115], v[80:83], a[244:247]\n" \
"v_mfma_f32_16x16x32_bf16 a[240:243], v[80:83], v[108:111], a[240:243]\n" \
"s_waitcnt vmcnt(4) lgkmcnt(0)\n" \
"s_barrier\n" \
"v_mfma_f32_16x16x32_bf16 a[24:27], v[168:171], v[128:131], a[24:27]\n" \
"s_add_u32 m0, s40, 0x0\n" \
"s_nop 0\n" \
"s_add_u32 s85, s39, 0x8000\n" \
"v_mfma_f32_16x16x32_bf16 a[28:31], v[128:131], v[172:175], a[28:31]\n" \
"buffer_load_dwordx4 v20, s[80:83], s85 offen sc1 lds\n" \
"v_mfma_f32_16x16x32_bf16 a[56:59], v[176:179], v[128:131], a[56:59]\n" \
"ds_read_b64_tr_b16 v[104:105], v37 offset:32768\n" \
"v_mfma_f32_16x16x32_bf16 a[0:3], v[148:151], v[116:119], a[0:3]\n" \
"ds_read_b64_tr_b16 v[106:107], v37 offset:33280\n" \
"v_mfma_f32_16x16x32_bf16 a[32:35], v[148:151], v[120:123], a[32:35]\n" \
"s_add_u32 m0, s40, 0x1000\n" \
"s_add_u32 s84, s39, 0x9000\n" \
"v_mfma_f32_16x16x32_bf16 a[4:7], v[152:155], v[116:119], a[4:7]\n" \
"buffer_load_dwordx4 v20, s[80:83], s84 offen sc1 lds\n" \
"v_mfma_f32_16x16x32_bf16 a[36:39], v[152:155], v[120:123], a[36:39]\n" \
"ds_read_b64_tr_b16 v[108:109], v38 offset:32768\n" \
"v_mfma_f32_16x16x32_bf16 a[8:11], v[156:159], v[116:119], a[8:11]\n" \
"ds_read_b64_tr_b16 v[110:111], v38 offset:33280\n" \
"v_mfma_f32_16x16x32_bf16 a[40:43], v[156:159], v[120:123], a[40:43]\n" \
"s_add_u32 m0, s40, 0x3000\n" \
"s_mov_b32 s85, 0x200\n" \
"v_mfma_f32_16x16x32_bf16 a[12:15], v[160:163], v[116:119], a[12:15]\n" \
"buffer_load_dwordx4 v21, s[76:79], s85 offen lds\n" \
"v_mfma_f32_16x16x32_bf16 a[44:47], v[160:163], v[120:123], a[44:47]\n" \
"ds_read_b64_tr_b16 v[112:113], v39 offset:32768\n" \
"v_mfma_f32_16x16x32_bf16 a[16:19], v[164:167], v[132:135], a[16:19]\n" \
"ds_read_b64_tr_b16 v[114:115], v39 offset:33280\n" \
"v_mfma_f32_16x16x32_bf16 a[20:23], v[168:171], v[132:135], a[20:23]\n" \
"s_add_u32 m0, s30, 0x0\n" \
"s_nop 0\n" \
"s_mov_b32 s84, 0x200\n" \
"v_mfma_f32_16x16x32_bf16 a[52:55], v[176:179], v[132:135], a[52:55]\n" \
"buffer_load_dwordx4 v24, s[76:79], s84 offen lds\n" \
"v_mfma_f32_16x16x32_bf16 a[48:51], v[132:135], v[172:175], a[48:51]\n" \
"ds_read_b64_tr_b16 v[84:85], v32 offset:32768\n" \
"v_mfma_f32_16x16x32_bf16 a[64:67], v[148:151], v[124:127], a[64:67]\n" \
"ds_read_b64_tr_b16 v[86:87], v32 offset:33792\n" \
"v_mfma_f32_16x16x32_bf16 a[96:99], v[148:151], v[128:131], a[96:99]\n" \
"ds_read_b64_tr_b16 v[88:89], v33 offset:32768\n" \
"v_mfma_f32_16x16x32_bf16 a[68:71], v[152:155], v[124:127], a[68:71]\n" \
"s_add_u32 m0, s30, 0x400\n" \
"s_mov_b32 s85, 0x2600\n" \
"v_mfma_f32_16x16x32_bf16 a[100:103], v[152:155], v[128:131], a[100:103]\n" \
"buffer_load_dwordx4 v25, s[76:79], s85 offen lds\n" \
"v_mfma_f32_16x16x32_bf16 a[72:75], v[156:159], v[124:127], a[72:75]\n" \
"ds_read_b64_tr_b16 v[90:91], v33 offset:33792\n" \
"v_mfma_f32_16x16x32_bf16 a[104:107], v[156:159], v[128:131], a[104:107]\n" \
"ds_read_b64_tr_b16 v[92:93], v34 offset:32768\n" \
"v_mfma_f32_16x16x32_bf16 a[76:79], v[160:163], v[124:127], a[76:79]\n" \
"s_add_u32 m0, s31, 0x0\n" \
"s_nop 0\n" \
"s_mov_b32 s84, 0x200000\n" \
"v_mfma_f32_16x16x32_bf16 a[108:111], v[160:163], v[128:131], a[108:111]\n" \
"buffer_load_dwordx4 v26, s[68:71], s84 offen lds\n" \
"v_mfma_f32_16x16x32_bf16 a[80:83], v[164:167], v[136:139], a[80:83]\n" \
"ds_read_b64_tr_b16 v[94:95], v34 offset:33792\n" \
"v_mfma_f32_16x16x32_bf16 a[84:87], v[168:171], v[136:139], a[84:87]\n" \
"ds_read_b64_tr_b16 v[96:97], v35 offset:32768\n" \
"v_mfma_f32_16x16x32_bf16 a[116:119], v[176:179], v[136:139], a[116:119]\n" \
"s_add_u32 m0, s30, 0x800\n" \
"s_mov_b32 s85, 0x4a00\n" \
"v_mfma_f32_16x16x32_bf16 a[112:115], v[136:139], v[172:175], a[112:115]\n" \
"buffer_load_dwordx4 v24, s[76:79], s85 offen lds\n" \
"v_mfma_f32_16x16x32_bf16 a[128:131], v[148:151], v[132:135], a[128:131]\n" \
"ds_read_b64_tr_b16 v[98:99], v35 offset:33792\n" \
"v_mfma_f32_16x16x32_bf16 a[160:163], v[148:151], v[136:139], a[160:163]\n" \
"ds_read_b64_tr_b16 v[100:101], v36 offset:32768\n" \
"v_mfma_f32_16x16x32_bf16 a[132:135], v[152:155], v[132:135], a[132:135]\n" \
"s_add_u32 m0, s30, 0xc00\n" \
"s_mov_b32 s84, 0x6e00\n" \
"v_mfma_f32_16x16x32_bf16 a[164:167], v[152:155], v[136:139], a[164:167]\n" \
"buffer_load_dwordx4 v25, s[76:79], s84 offen lds\n" \
"v_mfma_f32_16x16x32_bf16 a[136:139], v[156:159], v[132:135], a[136:139]\n" \
"ds_read_b64_tr_b16 v[102:103], v36 offset:33280\n" \
"v_mfma_f32_16x16x32_bf16 a[168:171], v[156:159], v[136:139], a[168:171]\n" \
"ds_read_b128 v[52:55], v28 offset:32768\n" \
"v_mfma_f32_16x16x32_bf16 a[140:143], v[160:163], v[132:135], a[140:143]\n" \
"ds_read_b128 v[56:59], v28 offset:34816\n" \
"v_mfma_f32_16x16x32_bf16 a[172:175], v[160:163], v[136:139], a[172:175]\n" \
"s_add_u32 m0, s31, 0x400\n" \
"s_mov_b32 s85, 0x208000\n" \
"v_mfma_f32_16x16x32_bf16 a[144:147], v[164:167], v[140:143], a[144:147]\n" \
"buffer_load_dwordx4 v26, s[68:71], s85 offen lds\n" \
"v_mfma_f32_16x16x32_bf16 a[148:151], v[168:171], v[140:143], a[148:151]\n" \
"ds_read_b128 v[60:63], v28 offset:36864\n" \
"v_mfma_f32_16x16x32_bf16 a[180:183], v[176:179], v[140:143], a[180:183]\n" \
"ds_read_b128 v[64:67], v28 offset:38912\n" \
"v_mfma_f32_16x16x32_bf16 a[176:179], v[140:143], v[172:175], a[176:179]\n" \
"s_add_u32 m0, s30, 0x1000\n" \
"s_mov_b32 s84, 0x9200\n" \
"v_mfma_f32_16x16x32_bf16 a[192:195], v[148:151], v[140:143], a[192:195]\n" \
"buffer_load_dwordx4 v24, s[76:79], s84 offen lds\n" \
"v_mfma_f32_16x16x32_bf16 a[224:227], v[148:151], v[144:147], a[224:227]\n" \
"ds_read_b128 v[68:71], v28 offset:40960\n" \
"v_mfma_f32_16x16x32_bf16 a[196:199], v[152:155], v[140:143], a[196:199]\n" \
"ds_read_b128 v[72:75], v28 offset:43008\n" \
"v_mfma_f32_16x16x32_bf16 a[228:231], v[152:155], v[144:147], a[228:231]\n" \
"s_add_u32 m0, s30, 0x1400\n" \
"s_mov_b32 s85, 0xb600\n" \
"v_mfma_f32_16x16x32_bf16 a[200:203], v[156:159], v[140:143], a[200:203]\n" \
"buffer_load_dwordx4 v25, s[76:79], s85 offen lds\n" \
"v_mfma_f32_16x16x32_bf16 a[232:235], v[156:159], v[144:147], a[232:235]\n" \
"ds_read_b128 v[76:79], v28 offset:45056\n" \
"v_mfma_f32_16x16x32_bf16 a[204:207], v[160:163], v[140:143], a[204:207]\n" \
"ds_read_b128 v[80:83], v28 offset:47104\n" \
"v_mfma_f32_16x16x32_bf16 a[236:239], v[160:163], v[144:147], a[236:239]\n" \
"s_add_u32 m0, s31, 0x800\n" \
"s_mov_b32 s84, 0x210000\n" \
"v_mfma_f32_16x16x32_bf16 a[208:211], v[164:167], v[144:147], a[208:211]\n" \
"buffer_load_dwordx4 v27, s[68:71], s84 offen lds\n" \
"v_mfma_f32_16x16x32_bf16 a[212:215], v[168:171], v[144:147], a[212:215]\n" \
"v_mfma_f32_16x16x32_bf16 a[244:247], v[176:179], v[144:147], a[244:247]\n" \
"v_mfma_f32_16x16x32_bf16 a[240:243], v[144:147], v[172:175], a[240:243]\n" \
"s_waitcnt lgkmcnt(4)\n" \
"s_nop 0\n" \
"v_mfma_f32_16x16x32_bf16 a[24:27], v[104:107], v[64:67], a[24:27]\n" \
"s_add_u32 m0, s30, 0x1800\n" \
"s_mov_b32 s84, 0xda00\n" \
"v_mfma_f32_16x16x32_bf16 a[28:31], v[64:67], v[108:111], a[28:31]\n" \
"buffer_load_dwordx4 v24, s[76:79], s84 offen lds\n" \
"v_mfma_f32_16x16x32_bf16 a[56:59], v[112:115], v[64:67], a[56:59]\n" \
"ds_read_b64_tr_b16 v[168:169], v37 offset:36864\n" \
"v_mfma_f32_16x16x32_bf16 a[0:3], v[84:87], v[52:55], a[0:3]\n" \
"s_add_u32 m0, s30, 0x1c00\n" \
"s_mov_b32 s85, 0xfe00\n" \
"v_mfma_f32_16x16x32_bf16 a[32:35], v[84:87], v[56:59], a[32:35]\n" \
"buffer_load_dwordx4 v25, s[76:79], s85 offen lds\n" \
"v_mfma_f32_16x16x32_bf16 a[4:7], v[88:91], v[52:55], a[4:7]\n" \
"ds_read_b64_tr_b16 v[170:171], v37 offset:37376\n" \
"v_mfma_f32_16x16x32_bf16 a[36:39], v[88:91], v[56:59], a[36:39]\n" \
"s_add_u32 m0, s31, 0xc00\n" \
"s_mov_b32 s84, 0x218000\n" \
"v_mfma_f32_16x16x32_bf16 a[8:11], v[92:95], v[52:55], a[8:11]\n" \
"buffer_load_dwordx4 v27, s[68:71], s84 offen lds\n" \
"v_mfma_f32_16x16x32_bf16 a[40:43], v[92:95], v[56:59], a[40:43]\n" \
"ds_read_b64_tr_b16 v[172:173], v38 offset:36864\n" \
"v_mfma_f32_16x16x32_bf16 a[12:15], v[96:99], v[52:55], a[12:15]\n" \
"ds_read_b64_tr_b16 v[174:175], v38 offset:37376\n" \
"v_mfma_f32_16x16x32_bf16 a[44:47], v[96:99], v[56:59], a[44:47]\n" \
"ds_read_b64_tr_b16 v[176:177], v39 offset:36864\n" \
"s_waitcnt lgkmcnt(8)\n" \
"s_nop 0\n" \
"v_mfma_f32_16x16x32_bf16 a[16:19], v[100:103], v[68:71], a[16:19]\n" \
"ds_read_b64_tr_b16 v[178:179], v39 offset:37376\n" \
"v_mfma_f32_16x16x32_bf16 a[20:23], v[104:107], v[68:71], a[20:23]\n" \
"ds_read_b64_tr_b16 v[148:149], v32 offset:40960\n" \
"v_mfma_f32_16x16x32_bf16 a[52:55], v[112:115], v[68:71], a[52:55]\n" \
"ds_read_b64_tr_b16 v[150:151], v32 offset:41984\n" \
"v_mfma_f32_16x16x32_bf16 a[48:51], v[68:71], v[108:111], a[48:51]\n" \
"ds_read_b64_tr_b16 v[152:153], v33 offset:40960\n" \
"v_mfma_f32_16x16x32_bf16 a[64:67], v[84:87], v[60:63], a[64:67]\n" \
"ds_read_b64_tr_b16 v[154:155], v33 offset:41984\n" \
"v_mfma_f32_16x16x32_bf16 a[96:99], v[84:87], v[64:67], a[96:99]\n" \
"ds_read_b64_tr_b16 v[156:157], v34 offset:40960\n" \
"v_mfma_f32_16x16x32_bf16 a[68:71], v[88:91], v[60:63], a[68:71]\n" \
"ds_read_b64_tr_b16 v[158:159], v34 offset:41984\n" \
"v_mfma_f32_16x16x32_bf16 a[100:103], v[88:91], v[64:67], a[100:103]\n" \
"v_mfma_f32_16x16x32_bf16 a[72:75], v[92:95], v[60:63], a[72:75]\n" \
"ds_read_b64_tr_b16 v[160:161], v35 offset:40960\n" \
"v_mfma_f32_16x16x32_bf16 a[104:107], v[92:95], v[64:67], a[104:107]\n" \
"v_mfma_f32_16x16x32_bf16 a[76:79], v[96:99], v[60:63], a[76:79]\n" \
"s_add_u32 s89, s41, 0x3000\n" \
"buffer_store_dwordx4 v[232:235], v20, s[80:83], s89 offen offset:0 sc1\n" \
"v_mfma_f32_16x16x32_bf16 a[108:111], v[96:99], v[64:67], a[108:111]\n" \
"ds_read_b64_tr_b16 v[162:163], v35 offset:41984\n" \
"s_waitcnt lgkmcnt(15)\n" \
"s_nop 0\n" \
"v_mfma_f32_16x16x32_bf16 a[80:83], v[100:103], v[72:75], a[80:83]\n" \
"ds_read_b64_tr_b16 v[164:165], v36 offset:36864\n" \
"v_mfma_f32_16x16x32_bf16 a[84:87], v[104:107], v[72:75], a[84:87]\n" \
"ds_read_b64_tr_b16 v[166:167], v36 offset:37376\n" \
"v_mfma_f32_16x16x32_bf16 a[116:119], v[112:115], v[72:75], a[116:119]\n" \
"v_mfma_f32_16x16x32_bf16 a[112:115], v[72:75], v[108:111], a[112:115]\n" \
"ds_read_b128 v[116:119], v29 offset:32768\n" \
"v_mfma_f32_16x16x32_bf16 a[128:131], v[84:87], v[68:71], a[128:131]\n" \
"ds_read_b128 v[120:123], v29 offset:34816\n" \
"v_mfma_f32_16x16x32_bf16 a[160:163], v[84:87], v[72:75], a[160:163]\n" \
"v_mfma_f32_16x16x32_bf16 a[132:135], v[88:91], v[68:71], a[132:135]\n" \
"s_add_u32 s88, s41, 0x3000\n" \
"buffer_store_dwordx4 v[236:239], v20, s[80:83], s88 offen offset:1024 sc1\n" \
"v_mfma_f32_16x16x32_bf16 a[164:167], v[88:91], v[72:75], a[164:167]\n" \
"ds_read_b128 v[124:127], v29 offset:36864\n" \
"v_mfma_f32_16x16x32_bf16 a[136:139], v[92:95], v[68:71], a[136:139]\n" \
"ds_read_b128 v[128:131], v29 offset:38912\n" \
"v_mfma_f32_16x16x32_bf16 a[168:171], v[92:95], v[72:75], a[168:171]\n" \
"ds_read_b128 v[132:135], v29 offset:40960\n" \
"v_mfma_f32_16x16x32_bf16 a[140:143], v[96:99], v[68:71], a[140:143]\n" \
"v_mfma_f32_16x16x32_bf16 a[172:175], v[96:99], v[72:75], a[172:175]\n" \
"ds_read_b128 v[136:139], v29 offset:43008\n" \
"v_mfma_f32_16x16x32_bf16 a[144:147], v[100:103], v[76:79], a[144:147]\n" \
"ds_read_b128 v[140:143], v29 offset:45056\n" \
"v_mfma_f32_16x16x32_bf16 a[148:151], v[104:107], v[76:79], a[148:151]\n" \
"s_add_u32 s89, s41, 0x3000\n" \
"buffer_store_dwordx4 v[240:243], v20, s[80:83], s89 offen offset:2048 sc1\n" \
"v_mfma_f32_16x16x32_bf16 a[180:183], v[112:115], v[76:79], a[180:183]\n" \
"ds_read_b128 v[144:147], v29 offset:47104\n" \
"v_mfma_f32_16x16x32_bf16 a[176:179], v[76:79], v[108:111], a[176:179]\n" \
"v_mfma_f32_16x16x32_bf16 a[192:195], v[84:87], v[76:79], a[192:195]\n" \
"s_waitcnt lgkmcnt(15)\n" \
"s_nop 0\n" \
"v_mfma_f32_16x16x32_bf16 a[224:227], v[84:87], v[80:83], a[224:227]\n" \
"v_mfma_f32_16x16x32_bf16 a[196:199], v[88:91], v[76:79], a[196:199]\n" \
"v_mfma_f32_16x16x32_bf16 a[228:231], v[88:91], v[80:83], a[228:231]\n" \
"v_mfma_f32_16x16x32_bf16 a[200:203], v[92:95], v[76:79], a[200:203]\n" \
"s_add_u32 s88, s41, 0x3000\n" \
"buffer_store_dwordx4 v[244:247], v20, s[80:83], s88 offen offset:3072 sc1\n" \
"v_mfma_f32_16x16x32_bf16 a[232:235], v[92:95], v[80:83], a[232:235]\n" \
"v_mfma_f32_16x16x32_bf16 a[204:207], v[96:99], v[76:79], a[204:207]\n" \
"v_mfma_f32_16x16x32_bf16 a[236:239], v[96:99], v[80:83], a[236:239]\n" \
"v_mfma_f32_16x16x32_bf16 a[208:211], v[100:103], v[80:83], a[208:211]\n" \
"v_mfma_f32_16x16x32_bf16 a[212:215], v[104:107], v[80:83], a[212:215]\n" \
"v_mfma_f32_16x16x32_bf16 a[244:247], v[112:115], v[80:83], a[244:247]\n" \
"v_mfma_f32_16x16x32_bf16 a[240:243], v[80:83], v[108:111], a[240:243]\n" \
"s_waitcnt vmcnt(4) lgkmcnt(0)\n" \
"s_barrier\n" \
"v_mfma_f32_16x16x32_bf16 a[24:27], v[168:171], v[128:131], a[24:27]\n" \
"s_add_u32 m0, s40, 0x8000\n" \
"s_add_u32 s85, s39, 0xa000\n" \
"v_mfma_f32_16x16x32_bf16 a[28:31], v[128:131], v[172:175], a[28:31]\n" \
"buffer_load_dwordx4 v20, s[80:83], s85 offen sc1 lds\n" \
"v_mfma_f32_16x16x32_bf16 a[56:59], v[176:179], v[128:131], a[56:59]\n" \
"ds_read_b64_tr_b16 v[104:105], v37 offset:0\n" \
"v_mfma_f32_16x16x32_bf16 a[0:3], v[148:151], v[116:119], a[0:3]\n" \
"ds_read_b64_tr_b16 v[106:107], v37 offset:512\n" \
"v_mfma_f32_16x16x32_bf16 a[32:35], v[148:151], v[120:123], a[32:35]\n" \
"s_add_u32 m0, s40, 0x9000\n" \
"s_add_u32 s84, s39, 0xb000\n" \
"v_mfma_f32_16x16x32_bf16 a[4:7], v[152:155], v[116:119], a[4:7]\n" \
"buffer_load_dwordx4 v20, s[80:83], s84 offen sc1 lds\n" \
"v_mfma_f32_16x16x32_bf16 a[36:39], v[152:155], v[120:123], a[36:39]\n" \
"ds_read_b64_tr_b16 v[108:109], v38 offset:0\n" \
"v_mfma_f32_16x16x32_bf16 a[8:11], v[156:159], v[116:119], a[8:11]\n" \
"ds_read_b64_tr_b16 v[110:111], v38 offset:512\n" \
"v_mfma_f32_16x16x32_bf16 a[40:43], v[156:159], v[120:123], a[40:43]\n" \
"s_add_u32 m0, s40, 0xb000\n" \
"s_mov_b32 s85, 0x280\n" \
"v_mfma_f32_16x16x32_bf16 a[12:15], v[160:163], v[116:119], a[12:15]\n" \
"buffer_load_dwordx4 v21, s[76:79], s85 offen lds\n" \
"v_mfma_f32_16x16x32_bf16 a[44:47], v[160:163], v[120:123], a[44:47]\n" \
"ds_read_b64_tr_b16 v[112:113], v39 offset:0\n" \
"v_mfma_f32_16x16x32_bf16 a[16:19], v[164:167], v[132:135], a[16:19]\n" \
"ds_read_b64_tr_b16 v[114:115], v39 offset:512\n" \
"v_mfma_f32_16x16x32_bf16 a[20:23], v[168:171], v[132:135], a[20:23]\n" \
"s_add_u32 m0, s30, 0x8000\n" \
"s_mov_b32 s84, 0x280\n" \
"v_mfma_f32_16x16x32_bf16 a[52:55], v[176:179], v[132:135], a[52:55]\n" \
"buffer_load_dwordx4 v24, s[76:79], s84 offen lds\n" \
"v_mfma_f32_16x16x32_bf16 a[48:51], v[132:135], v[172:175], a[48:51]\n" \
"ds_read_b64_tr_b16 v[84:85], v32 offset:0\n" \
"v_mfma_f32_16x16x32_bf16 a[64:67], v[148:151], v[124:127], a[64:67]\n" \
"ds_read_b64_tr_b16 v[86:87], v32 offset:1024\n" \
"v_mfma_f32_16x16x32_bf16 a[96:99], v[148:151], v[128:131], a[96:99]\n" \
"ds_read_b64_tr_b16 v[88:89], v33 offset:0\n" \
"v_mfma_f32_16x16x32_bf16 a[68:71], v[152:155], v[124:127], a[68:71]\n" \
"s_add_u32 m0, s30, 0x8400\n" \
"s_mov_b32 s85, 0x2680\n" \
"v_mfma_f32_16x16x32_bf16 a[100:103], v[152:155], v[128:131], a[100:103]\n" \
"buffer_load_dwordx4 v25, s[76:79], s85 offen lds\n" \
"v_mfma_f32_16x16x32_bf16 a[72:75], v[156:159], v[124:127], a[72:75]\n" \
"ds_read_b64_tr_b16 v[90:91], v33 offset:1024\n" \
"v_mfma_f32_16x16x32_bf16 a[104:107], v[156:159], v[128:131], a[104:107]\n" \
"ds_read_b64_tr_b16 v[92:93], v34 offset:0\n" \
"v_mfma_f32_16x16x32_bf16 a[76:79], v[160:163], v[124:127], a[76:79]\n" \
"s_add_u32 m0, s31, 0x8000\n" \
"s_mov_b32 s84, 0x280000\n" \
"v_mfma_f32_16x16x32_bf16 a[108:111], v[160:163], v[128:131], a[108:111]\n" \
"buffer_load_dwordx4 v26, s[68:71], s84 offen lds\n" \
"v_mfma_f32_16x16x32_bf16 a[80:83], v[164:167], v[136:139], a[80:83]\n" \
"ds_read_b64_tr_b16 v[94:95], v34 offset:1024\n" \
"v_mfma_f32_16x16x32_bf16 a[84:87], v[168:171], v[136:139], a[84:87]\n" \
"ds_read_b64_tr_b16 v[96:97], v35 offset:0\n" \
"v_mfma_f32_16x16x32_bf16 a[116:119], v[176:179], v[136:139], a[116:119]\n" \
"s_add_u32 m0, s30, 0x8800\n" \
"s_mov_b32 s85, 0x4a80\n" \
"v_mfma_f32_16x16x32_bf16 a[112:115], v[136:139], v[172:175], a[112:115]\n" \
"buffer_load_dwordx4 v24, s[76:79], s85 offen lds\n" \
"v_mfma_f32_16x16x32_bf16 a[128:131], v[148:151], v[132:135], a[128:131]\n" \
"ds_read_b64_tr_b16 v[98:99], v35 offset:1024\n" \
"v_mfma_f32_16x16x32_bf16 a[160:163], v[148:151], v[136:139], a[160:163]\n" \
"ds_read_b64_tr_b16 v[100:101], v36 offset:0\n" \
"v_mfma_f32_16x16x32_bf16 a[132:135], v[152:155], v[132:135], a[132:135]\n" \
"s_add_u32 m0, s30, 0x8c00\n" \
"s_mov_b32 s84, 0x6e80\n" \
"v_mfma_f32_16x16x32_bf16 a[164:167], v[152:155], v[136:139], a[164:167]\n" \
"buffer_load_dwordx4 v25, s[76:79], s84 offen lds\n" \
"v_mfma_f32_16x16x32_bf16 a[136:139], v[156:159], v[132:135], a[136:139]\n" \
"ds_read_b64_tr_b16 v[102:103], v36 offset:512\n" \
"v_mfma_f32_16x16x32_bf16 a[168:171], v[156:159], v[136:139], a[168:171]\n" \
"ds_read_b128 v[52:55], v28 offset:0\n" \
"v_mfma_f32_16x16x32_bf16 a[140:143], v[160:163], v[132:135], a[140:143]\n" \
"ds_read_b128 v[56:59], v28 offset:2048\n" \
"v_mfma_f32_16x16x32_bf16 a[172:175], v[160:163], v[136:139], a[172:175]\n" \
"s_add_u32 m0, s31, 0x8400\n" \
"s_mov_b32 s85, 0x288000\n" \
"v_mfma_f32_16x16x32_bf16 a[144:147], v[164:167], v[140:143], a[144:147]\n" \
"buffer_load_dwordx4 v26, s[68:71], s85 offen lds\n" \
"v_mfma_f32_16x16x32_bf16 a[148:151], v[168:171], v[140:143], a[148:151]\n" \
"ds_read_b128 v[60:63], v28 offset:4096\n" \
"v_mfma_f32_16x16x32_bf16 a[180:183], v[176:179], v[140:143], a[180:183]\n" \
"ds_read_b128 v[64:67], v28 offset:6144\n" \
"v_mfma_f32_16x16x32_bf16 a[176:179], v[140:143], v[172:175], a[176:179]\n" \
"s_add_u32 m0, s30, 0x9000\n" \
"s_mov_b32 s84, 0x9280\n" \
"v_mfma_f32_16x16x32_bf16 a[192:195], v[148:151], v[140:143], a[192:195]\n" \
"buffer_load_dwordx4 v24, s[76:79], s84 offen lds\n" \
"v_mfma_f32_16x16x32_bf16 a[224:227], v[148:151], v[144:147], a[224:227]\n" \
"ds_read_b128 v[68:71], v28 offset:8192\n" \
"v_mfma_f32_16x16x32_bf16 a[196:199], v[152:155], v[140:143], a[196:199]\n" \
"ds_read_b128 v[72:75], v28 offset:10240\n" \
"v_mfma_f32_16x16x32_bf16 a[228:231], v[152:155], v[144:147], a[228:231]\n" \
"s_add_u32 m0, s30, 0x9400\n" \
"s_mov_b32 s85, 0xb680\n" \
"v_mfma_f32_16x16x32_bf16 a[200:203], v[156:159], v[140:143], a[200:203]\n" \
"buffer_load_dwordx4 v25, s[76:79], s85 offen lds\n" \
"v_mfma_f32_16x16x32_bf16 a[232:235], v[156:159], v[144:147], a[232:235]\n" \
"ds_read_b128 v[76:79], v28 offset:12288\n" \
"v_mfma_f32_16x16x32_bf16 a[204:207], v[160:163], v[140:143], a[204:207]\n" \
"ds_read_b128 v[80:83], v28 offset:14336\n" \
"v_mfma_f32_16x16x32_bf16 a[236:239], v[160:163], v[144:147], a[236:239]\n" \
"s_add_u32 m0, s31, 0x8800\n" \
"s_mov_b32 s84, 0x290000\n" \
"v_mfma_f32_16x16x32_bf16 a[208:211], v[164:167], v[144:147], a[208:211]\n" \
"buffer_load_dwordx4 v27, s[68:71], s84 offen lds\n" \
"v_mfma_f32_16x16x32_bf16 a[212:215], v[168:171], v[144:147], a[212:215]\n" \
"v_mfma_f32_16x16x32_bf16 a[244:247], v[176:179], v[144:147], a[244:247]\n" \
"v_mfma_f32_16x16x32_bf16 a[240:243], v[144:147], v[172:175], a[240:243]\n" \
"s_waitcnt lgkmcnt(4)\n" \
"s_nop 0\n" \
"v_mfma_f32_16x16x32_bf16 a[24:27], v[104:107], v[64:67], a[24:27]\n" \
"s_add_u32 m0, s30, 0x9800\n" \
"s_mov_b32 s84, 0xda80\n" \
"v_mfma_f32_16x16x32_bf16 a[28:31], v[64:67], v[108:111], a[28:31]\n" \
"buffer_load_dwordx4 v24, s[76:79], s84 offen lds\n" \
"v_mfma_f32_16x16x32_bf16 a[56:59], v[112:115], v[64:67], a[56:59]\n" \
"ds_read_b64_tr_b16 v[168:169], v37 offset:4096\n" \
"v_mfma_f32_16x16x32_bf16 a[0:3], v[84:87], v[52:55], a[0:3]\n" \
"s_add_u32 m0, s30, 0x9c00\n" \
"s_mov_b32 s85, 0xfe80\n" \
"v_mfma_f32_16x16x32_bf16 a[32:35], v[84:87], v[56:59], a[32:35]\n" \
"buffer_load_dwordx4 v25, s[76:79], s85 offen lds\n" \
"v_mfma_f32_16x16x32_bf16 a[4:7], v[88:91], v[52:55], a[4:7]\n" \
"ds_read_b64_tr_b16 v[170:171], v37 offset:4608\n" \
"v_mfma_f32_16x16x32_bf16 a[36:39], v[88:91], v[56:59], a[36:39]\n" \
"s_add_u32 m0, s31, 0x8c00\n" \
"s_mov_b32 s84, 0x298000\n" \
"v_mfma_f32_16x16x32_bf16 a[8:11], v[92:95], v[52:55], a[8:11]\n" \
"buffer_load_dwordx4 v27, s[68:71], s84 offen lds\n" \
"v_mfma_f32_16x16x32_bf16 a[40:43], v[92:95], v[56:59], a[40:43]\n" \
"ds_read_b64_tr_b16 v[172:173], v38 offset:4096\n" \
"v_mfma_f32_16x16x32_bf16 a[12:15], v[96:99], v[52:55], a[12:15]\n" \
"ds_read_b64_tr_b16 v[174:175], v38 offset:4608\n" \
"v_mfma_f32_16x16x32_bf16 a[44:47], v[96:99], v[56:59], a[44:47]\n" \
"ds_read_b64_tr_b16 v[176:177], v39 offset:4096\n" \
"s_waitcnt lgkmcnt(8)\n" \
"s_nop 0\n" \
"v_mfma_f32_16x16x32_bf16 a[16:19], v[100:103], v[68:71], a[16:19]\n" \
"ds_read_b64_tr_b16 v[178:179], v39 offset:4608\n" \
"v_mfma_f32_16x16x32_bf16 a[20:23], v[104:107], v[68:71], a[20:23]\n" \
"ds_read_b64_tr_b16 v[148:149], v32 offset:8192\n" \
"v_mfma_f32_16x16x32_bf16 a[52:55], v[112:115], v[68:71], a[52:55]\n" \
"ds_read_b64_tr_b16 v[150:151], v32 offset:9216\n" \
"v_mfma_f32_16x16x32_bf16 a[48:51], v[68:71], v[108:111], a[48:51]\n" \
"ds_read_b64_tr_b16 v[152:153], v33 offset:8192\n" \
"v_mfma_f32_16x16x32_bf16 a[64:67], v[84:87], v[60:63], a[64:67]\n" \
"ds_read_b64_tr_b16 v[154:155], v33 offset:9216\n" \
"v_mfma_f32_16x16x32_bf16 a[96:99], v[84:87], v[64:67], a[96:99]\n" \
"ds_read_b64_tr_b16 v[156:157], v34 offset:8192\n" \
"v_mfma_f32_16x16x32_bf16 a[68:71], v[88:91], v[60:63], a[68:71]\n" \
"ds_read_b64_tr_b16 v[158:159], v34 offset:9216\n" \
"v_mfma_f32_16x16x32_bf16 a[100:103], v[88:91], v[64:67], a[100:103]\n" \
"v_mfma_f32_16x16x32_bf16 a[72:75], v[92:95], v[60:63], a[72:75]\n" \
"ds_read_b64_tr_b16 v[160:161], v35 offset:8192\n" \
"v_mfma_f32_16x16x32_bf16 a[104:107], v[92:95], v[64:67], a[104:107]\n" \
"v_mfma_f32_16x16x32_bf16 a[76:79], v[96:99], v[60:63], a[76:79]\n" \
"ds_read_b64_tr_b16 v[162:163], v35 offset:9216\n" \
"v_mfma_f32_16x16x32_bf16 a[108:111], v[96:99], v[64:67], a[108:111]\n" \
"ds_read_b64_tr_b16 v[164:165], v36 offset:4096\n" \
"s_waitcnt lgkmcnt(15)\n" \
"s_nop 0\n" \
"v_mfma_f32_16x16x32_bf16 a[80:83], v[100:103], v[72:75], a[80:83]\n" \
"v_mfma_f32_16x16x32_bf16 a[84:87], v[104:107], v[72:75], a[84:87]\n" \
"ds_read_b64_tr_b16 v[166:167], v36 offset:4608\n" \
"v_mfma_f32_16x16x32_bf16 a[116:119], v[112:115], v[72:75], a[116:119]\n" \
"s_add_u32 s89, s42, 0x1000\n" \
"buffer_store_dwordx4 a[60:63], v20, s[80:83], s89 offen offset:2048 sc1\n" \
"v_mfma_f32_16x16x32_bf16 a[112:115], v[72:75], v[108:111], a[112:115]\n" \
"ds_read_b128 v[116:119], v29 offset:0\n" \
"v_mfma_f32_16x16x32_bf16 a[128:131], v[84:87], v[68:71], a[128:131]\n" \
"ds_read_b128 v[120:123], v29 offset:2048\n" \
"v_mfma_f32_16x16x32_bf16 a[160:163], v[84:87], v[72:75], a[160:163]\n" \
"v_mfma_f32_16x16x32_bf16 a[132:135], v[88:91], v[68:71], a[132:135]\n" \
"ds_read_b128 v[124:127], v29 offset:4096\n" \
"v_mfma_f32_16x16x32_bf16 a[164:167], v[88:91], v[72:75], a[164:167]\n" \
"ds_read_b128 v[128:131], v29 offset:6144\n" \
"v_mfma_f32_16x16x32_bf16 a[136:139], v[92:95], v[68:71], a[136:139]\n" \
"v_mfma_f32_16x16x32_bf16 a[168:171], v[92:95], v[72:75], a[168:171]\n" \
"ds_read_b128 v[132:135], v29 offset:8192\n" \
"v_mfma_f32_16x16x32_bf16 a[140:143], v[96:99], v[68:71], a[140:143]\n" \
"v_mfma_f32_16x16x32_bf16 a[172:175], v[96:99], v[72:75], a[172:175]\n" \
"s_add_u32 s88, s42, 0x1000\n" \
"buffer_store_dwordx4 a[88:91], v20, s[80:83], s88 offen offset:3072 sc1\n" \
"v_mfma_f32_16x16x32_bf16 a[144:147], v[100:103], v[76:79], a[144:147]\n" \
"ds_read_b128 v[136:139], v29 offset:10240\n" \
"v_mfma_f32_16x16x32_bf16 a[148:151], v[104:107], v[76:79], a[148:151]\n" \
"ds_read_b128 v[140:143], v29 offset:12288\n" \
"v_mfma_f32_16x16x32_bf16 a[180:183], v[112:115], v[76:79], a[180:183]\n" \
"ds_read_b128 v[144:147], v29 offset:14336\n" \
"v_mfma_f32_16x16x32_bf16 a[176:179], v[76:79], v[108:111], a[176:179]\n" \
"v_mfma_f32_16x16x32_bf16 a[192:195], v[84:87], v[76:79], a[192:195]\n" \
"v_mfma_f32_16x16x32_bf16 a[224:227], v[84:87], v[80:83], a[224:227]\n" \
"v_mfma_f32_16x16x32_bf16 a[196:199], v[88:91], v[76:79], a[196:199]\n" \
"v_mfma_f32_16x16x32_bf16 a[228:231], v[88:91], v[80:83], a[228:231]\n" \
"s_add_u32 s89, s42, 0x0\n" \
"buffer_store_dwordx4 a[120:123], v20, s[80:83], s89 offen offset:0 sc1\n" \
"v_mfma_f32_16x16x32_bf16 a[200:203], v[92:95], v[76:79], a[200:203]\n" \
"v_mfma_f32_16x16x32_bf16 a[232:235], v[92:95], v[80:83], a[232:235]\n" \
"v_mfma_f32_16x16x32_bf16 a[204:207], v[96:99], v[76:79], a[204:207]\n" \
"v_mfma_f32_16x16x32_bf16 a[236:239], v[96:99], v[80:83], a[236:239]\n" \
"v_mfma_f32_16x16x32_bf16 a[208:211], v[100:103], v[80:83], a[208:211]\n" \
"v_mfma_f32_16x16x32_bf16 a[212:215], v[104:107], v[80:83], a[212:215]\n" \
"v_mfma_f32_16x16x32_bf16 a[244:247], v[112:115], v[80:83], a[244:247]\n" \
"v_mfma_f32_16x16x32_bf16 a[240:243], v[80:83], v[108:111], a[240:243]\n" \
"s_waitcnt vmcnt(3) lgkmcnt(0)\n" \
"s_barrier\n" \
"v_mov_b32_e32 v45, s62\n" \
"s_mov_b64 exec, 1\n" \
"buffer_store_dword v45, off, s[80:83], s37 offset:0 sc1\n" \
"s_mov_b64 exec, -1\n" \
"v_mfma_f32_16x16x32_bf16 a[24:27], v[168:171], v[128:131], a[24:27]\n" \
"s_add_u32 m0, s40, 0x0\n" \
"s_nop 0\n" \
"s_add_u32 s84, s39, 0xc000\n" \
"v_mfma_f32_16x16x32_bf16 a[28:31], v[128:131], v[172:175], a[28:31]\n" \
"buffer_load_dwordx4 v20, s[80:83], s84 offen sc1 lds\n" \
"v_mfma_f32_16x16x32_bf16 a[56:59], v[176:179], v[128:131], a[56:59]\n" \
"ds_read_b64_tr_b16 v[104:105], v37 offset:32768\n" \
"v_mfma_f32_16x16x32_bf16 a[0:3], v[148:151], v[116:119], a[0:3]\n" \
"ds_read_b64_tr_b16 v[106:107], v37 offset:33280\n" \
"v_mfma_f32_16x16x32_bf16 a[32:35], v[148:151], v[120:123], a[32:35]\n" \
"s_add_u32 m0, s40, 0x1000\n" \
"s_add_u32 s85, s39, 0xd000\n" \
"v_mfma_f32_16x16x32_bf16 a[4:7], v[152:155], v[116:119], a[4:7]\n" \
"buffer_load_dwordx4 v20, s[80:83], s85 offen sc1 lds\n" \
"v_mfma_f32_16x16x32_bf16 a[36:39], v[152:155], v[120:123], a[36:39]\n" \
"ds_read_b64_tr_b16 v[108:109], v38 offset:32768\n" \
"v_mfma_f32_16x16x32_bf16 a[8:11], v[156:159], v[116:119], a[8:11]\n" \
"ds_read_b64_tr_b16 v[110:111], v38 offset:33280\n" \
"v_mfma_f32_16x16x32_bf16 a[40:43], v[156:159], v[120:123], a[40:43]\n" \
"s_add_u32 m0, s40, 0x3000\n" \
"s_mov_b32 s84, 0x300\n" \
"v_mfma_f32_16x16x32_bf16 a[12:15], v[160:163], v[116:119], a[12:15]\n" \
"buffer_load_dwordx4 v21, s[76:79], s84 offen lds\n" \
"v_mfma_f32_16x16x32_bf16 a[44:47], v[160:163], v[120:123], a[44:47]\n" \
"ds_read_b64_tr_b16 v[112:113], v39 offset:32768\n" \
"v_mfma_f32_16x16x32_bf16 a[16:19], v[164:167], v[132:135], a[16:19]\n" \
"ds_read_b64_tr_b16 v[114:115], v39 offset:33280\n" \
"v_mfma_f32_16x16x32_bf16 a[20:23], v[168:171], v[132:135], a[20:23]\n" \
"s_add_u32 m0, s30, 0x0\n" \
"s_nop 0\n" \
"s_mov_b32 s85, 0x300\n" \
"v_mfma_f32_16x16x32_bf16 a[52:55], v[176:179], v[132:135], a[52:55]\n" \
"buffer_load_dwordx4 v24, s[76:79], s85 offen lds\n" \
"v_mfma_f32_16x16x32_bf16 a[48:51], v[132:135], v[172:175], a[48:51]\n" \
"ds_read_b64_tr_b16 v[84:85], v32 offset:32768\n" \
"v_mfma_f32_16x16x32_bf16 a[64:67], v[148:151], v[124:127], a[64:67]\n" \
"ds_read_b64_tr_b16 v[86:87], v32 offset:33792\n" \
"v_mfma_f32_16x16x32_bf16 a[96:99], v[148:151], v[128:131], a[96:99]\n" \
"ds_read_b64_tr_b16 v[88:89], v33 offset:32768\n" \
"v_mfma_f32_16x16x32_bf16 a[68:71], v[152:155], v[124:127], a[68:71]\n" \
"s_add_u32 m0, s30, 0x400\n" \
"s_mov_b32 s84, 0x2700\n" \
"v_mfma_f32_16x16x32_bf16 a[100:103], v[152:155], v[128:131], a[100:103]\n" \
"buffer_load_dwordx4 v25, s[76:79], s84 offen lds\n" \
"v_mfma_f32_16x16x32_bf16 a[72:75], v[156:159], v[124:127], a[72:75]\n" \
"ds_read_b64_tr_b16 v[90:91], v33 offset:33792\n" \
"v_mfma_f32_16x16x32_bf16 a[104:107], v[156:159], v[128:131], a[104:107]\n" \
"ds_read_b64_tr_b16 v[92:93], v34 offset:32768\n" \
"v_mfma_f32_16x16x32_bf16 a[76:79], v[160:163], v[124:127], a[76:79]\n" \
"s_add_u32 m0, s31, 0x0\n" \
"s_nop 0\n" \
"s_mov_b32 s85, 0x300000\n" \
"v_mfma_f32_16x16x32_bf16 a[108:111], v[160:163], v[128:131], a[108:111]\n" \
"buffer_load_dwordx4 v26, s[68:71], s85 offen lds\n" \
"v_mfma_f32_16x16x32_bf16 a[80:83], v[164:167], v[136:139], a[80:83]\n" \
"ds_read_b64_tr_b16 v[94:95], v34 offset:33792\n" \
"v_mfma_f32_16x16x32_bf16 a[84:87], v[168:171], v[136:139], a[84:87]\n" \
"ds_read_b64_tr_b16 v[96:97], v35 offset:32768\n" \
"v_mfma_f32_16x16x32_bf16 a[116:119], v[176:179], v[136:139], a[116:119]\n" \
"s_add_u32 m0, s30, 0x800\n" \
"s_mov_b32 s84, 0x4b00\n" \
"v_mfma_f32_16x16x32_bf16 a[112:115], v[136:139], v[172:175], a[112:115]\n" \
"buffer_load_dwordx4 v24, s[76:79], s84 offen lds\n" \
"v_mfma_f32_16x16x32_bf16 a[128:131], v[148:151], v[132:135], a[128:131]\n" \
"ds_read_b64_tr_b16 v[98:99], v35 offset:33792\n" \
"v_mfma_f32_16x16x32_bf16 a[160:163], v[148:151], v[136:139], a[160:163]\n" \
"ds_read_b64_tr_b16 v[100:101], v36 offset:32768\n" \
"v_mfma_f32_16x16x32_bf16 a[132:135], v[152:155], v[132:135], a[132:135]\n" \
"s_add_u32 m0, s30, 0xc00\n" \
"s_mov_b32 s85, 0x6f00\n" \
"v_mfma_f32_16x16x32_bf16 a[164:167], v[152:155], v[136:139], a[164:167]\n" \
"buffer_load_dwordx4 v25, s[76:79], s85 offen lds\n" \
"v_mfma_f32_16x16x32_bf16 a[136:139], v[156:159], v[132:135], a[136:139]\n" \
"ds_read_b64_tr_b16 v[102:103], v36 offset:33280\n" \
"v_mfma_f32_16x16x32_bf16 a[168:171], v[156:159], v[136:139], a[168:171]\n" \
"ds_read_b128 v[52:55], v28 offset:32768\n" \
"v_mfma_f32_16x16x32_bf16 a[140:143], v[160:163], v[132:135], a[140:143]\n" \
"ds_read_b128 v[56:59], v28 offset:34816\n" \
"v_mfma_f32_16x16x32_bf16 a[172:175], v[160:163], v[136:139], a[172:175]\n" \
"s_add_u32 m0, s31, 0x400\n" \
"s_mov_b32 s84, 0x308000\n" \
"v_mfma_f32_16x16x32_bf16 a[144:147], v[164:167], v[140:143], a[144:147]\n" \
"buffer_load_dwordx4 v26, s[68:71], s84 offen lds\n" \
"v_mfma_f32_16x16x32_bf16 a[148:151], v[168:171], v[140:143], a[148:151]\n" \
"ds_read_b128 v[60:63], v28 offset:36864\n" \
"v_mfma_f32_16x16x32_bf16 a[180:183], v[176:179], v[140:143], a[180:183]\n" \
"ds_read_b128 v[64:67], v28 offset:38912\n" \
"v_mfma_f32_16x16x32_bf16 a[176:179], v[140:143], v[172:175], a[176:179]\n" \
"s_add_u32 m0, s30, 0x1000\n" \
"s_mov_b32 s85, 0x9300\n" \
"v_mfma_f32_16x16x32_bf16 a[192:195], v[148:151], v[140:143], a[192:195]\n" \
"buffer_load_dwordx4 v24, s[76:79], s85 offen lds\n" \
"v_mfma_f32_16x16x32_bf16 a[224:227], v[148:151], v[144:147], a[224:227]\n" \
"ds_read_b128 v[68:71], v28 offset:40960\n" \
"v_mfma_f32_16x16x32_bf16 a[196:199], v[152:155], v[140:143], a[196:199]\n" \
"ds_read_b128 v[72:75], v28 offset:43008\n" \
"v_mfma_f32_16x16x32_bf16 a[228:231], v[152:155], v[144:147], a[228:231]\n" \
"s_add_u32 m0, s30, 0x1400\n" \
"s_mov_b32 s84, 0xb700\n" \
"v_mfma_f32_16x16x32_bf16 a[200:203], v[156:159], v[140:143], a[200:203]\n" \
"buffer_load_dwordx4 v25, s[76:79], s84 offen lds\n" \
"v_mfma_f32_16x16x32_bf16 a[232:235], v[156:159], v[144:147], a[232:235]\n" \
"ds_read_b128 v[76:79], v28 offset:45056\n" \
"v_mfma_f32_16x16x32_bf16 a[204:207], v[160:163], v[140:143], a[204:207]\n" \
"ds_read_b128 v[80:83], v28 offset:47104\n" \
"v_mfma_f32_16x16x32_bf16 a[236:239], v[160:163], v[144:147], a[236:239]\n" \
"s_add_u32 m0, s31, 0x800\n" \
"s_mov_b32 s85, 0x310000\n" \
"v_mfma_f32_16x16x32_bf16 a[208:211], v[164:167], v[144:147], a[208:211]\n" \
"buffer_load_dwordx4 v27, s[68:71], s85 offen lds\n" \
"v_mfma_f32_16x16x32_bf16 a[212:215], v[168:171], v[144:147], a[212:215]\n" \
"v_mfma_f32_16x16x32_bf16 a[244:247], v[176:179], v[144:147], a[244:247]\n" \
"v_mfma_f32_16x16x32_bf16 a[240:243], v[144:147], v[172:175], a[240:243]\n" \
"s_waitcnt lgkmcnt(4)\n" \
"s_nop 0\n" \
"v_mfma_f32_16x16x32_bf16 a[24:27], v[104:107], v[64:67], a[24:27]\n" \
"s_add_u32 m0, s30, 0x1800\n" \
"s_mov_b32 s85, 0xdb00\n" \
"v_mfma_f32_16x16x32_bf16 a[28:31], v[64:67], v[108:111], a[28:31]\n" \
"buffer_load_dwordx4 v24, s[76:79], s85 offen lds\n" \
"v_mfma_f32_16x16x32_bf16 a[56:59], v[112:115], v[64:67], a[56:59]\n" \
"ds_read_b64_tr_b16 v[168:169], v37 offset:36864\n" \
"v_mfma_f32_16x16x32_bf16 a[0:3], v[84:87], v[52:55], a[0:3]\n" \
"s_add_u32 m0, s30, 0x1c00\n" \
"s_mov_b32 s84, 0xff00\n" \
"v_mfma_f32_16x16x32_bf16 a[32:35], v[84:87], v[56:59], a[32:35]\n" \
"buffer_load_dwordx4 v25, s[76:79], s84 offen lds\n" \
"v_mfma_f32_16x16x32_bf16 a[4:7], v[88:91], v[52:55], a[4:7]\n" \
"ds_read_b64_tr_b16 v[170:171], v37 offset:37376\n" \
"v_mfma_f32_16x16x32_bf16 a[36:39], v[88:91], v[56:59], a[36:39]\n" \
"s_add_u32 m0, s31, 0xc00\n" \
"s_mov_b32 s85, 0x318000\n" \
"v_mfma_f32_16x16x32_bf16 a[8:11], v[92:95], v[52:55], a[8:11]\n" \
"buffer_load_dwordx4 v27, s[68:71], s85 offen lds\n" \
"v_mfma_f32_16x16x32_bf16 a[40:43], v[92:95], v[56:59], a[40:43]\n" \
"ds_read_b64_tr_b16 v[172:173], v38 offset:36864\n" \
"v_mfma_f32_16x16x32_bf16 a[12:15], v[96:99], v[52:55], a[12:15]\n" \
"ds_read_b64_tr_b16 v[174:175], v38 offset:37376\n" \
"v_mfma_f32_16x16x32_bf16 a[44:47], v[96:99], v[56:59], a[44:47]\n" \
"ds_read_b64_tr_b16 v[176:177], v39 offset:36864\n" \
"s_waitcnt lgkmcnt(8)\n" \
"s_nop 0\n" \
"v_mfma_f32_16x16x32_bf16 a[16:19], v[100:103], v[68:71], a[16:19]\n" \
"ds_read_b64_tr_b16 v[178:179], v39 offset:37376\n" \
"v_mfma_f32_16x16x32_bf16 a[20:23], v[104:107], v[68:71], a[20:23]\n" \
"ds_read_b64_tr_b16 v[148:149], v32 offset:40960\n" \
"v_mfma_f32_16x16x32_bf16 a[52:55], v[112:115], v[68:71], a[52:55]\n" \
"ds_read_b64_tr_b16 v[150:151], v32 offset:41984\n" \
"v_mfma_f32_16x16x32_bf16 a[48:51], v[68:71], v[108:111], a[48:51]\n" \
"ds_read_b64_tr_b16 v[152:153], v33 offset:40960\n" \
"v_mfma_f32_16x16x32_bf16 a[64:67], v[84:87], v[60:63], a[64:67]\n" \
"ds_read_b64_tr_b16 v[154:155], v33 offset:41984\n" \
"v_mfma_f32_16x16x32_bf16 a[96:99], v[84:87], v[64:67], a[96:99]\n" \
"ds_read_b64_tr_b16 v[156:157], v34 offset:40960\n" \
"v_mfma_f32_16x16x32_bf16 a[68:71], v[88:91], v[60:63], a[68:71]\n" \
"ds_read_b64_tr_b16 v[158:159], v34 offset:41984\n" \
"v_mfma_f32_16x16x32_bf16 a[100:103], v[88:91], v[64:67], a[100:103]\n" \
"v_mfma_f32_16x16x32_bf16 a[72:75], v[92:95], v[60:63], a[72:75]\n" \
"ds_read_b64_tr_b16 v[160:161], v35 offset:40960\n" \
"v_mfma_f32_16x16x32_bf16 a[104:107], v[92:95], v[64:67], a[104:107]\n" \
"v_mfma_f32_16x16x32_bf16 a[76:79], v[96:99], v[60:63], a[76:79]\n" \
"ds_read_b64_tr_b16 v[162:163], v35 offset:41984\n" \
"v_mfma_f32_16x16x32_bf16 a[108:111], v[96:99], v[64:67], a[108:111]\n" \
"ds_read_b64_tr_b16 v[164:165], v36 offset:36864\n" \
"s_waitcnt lgkmcnt(15)\n" \
"v_mfma_f32_16x16x32_bf16 a[80:83], v[100:103], v[72:75], a[80:83]\n" \
"v_mfma_f32_16x16x32_bf16 a[84:87], v[104:107], v[72:75], a[84:87]\n" \
"ds_read_b64_tr_b16 v[166:167], v36 offset:37376\n" \
"v_mfma_f32_16x16x32_bf16 a[116:119], v[112:115], v[72:75], a[116:119]\n" \
"s_add_u32 s88, s42, 0x0\n" \
"buffer_store_dwordx4 a[92:95], v20, s[80:83], s88 offen offset:1024 sc1\n" \
"v_mfma_f32_16x16x32_bf16 a[112:115], v[72:75], v[108:111], a[112:115]\n" \
"ds_read_b128 v[116:119], v29 offset:32768\n" \
"v_mfma_f32_16x16x32_bf16 a[128:131], v[84:87], v[68:71], a[128:131]\n" \
"ds_read_b128 v[120:123], v29 offset:34816\n" \
"v_mfma_f32_16x16x32_bf16 a[160:163], v[84:87], v[72:75], a[160:163]\n" \
"v_mfma_f32_16x16x32_bf16 a[132:135], v[88:91], v[68:71], a[132:135]\n" \
"ds_read_b128 v[124:127], v29 offset:36864\n" \
"v_mfma_f32_16x16x32_bf16 a[164:167], v[88:91], v[72:75], a[164:167]\n" \
"ds_read_b128 v[128:131], v29 offset:38912\n" \
"v_mfma_f32_16x16x32_bf16 a[136:139], v[92:95], v[68:71], a[136:139]\n" \
"v_mfma_f32_16x16x32_bf16 a[168:171], v[92:95], v[72:75], a[168:171]\n" \
"ds_read_b128 v[132:135], v29 offset:40960\n" \
"v_mfma_f32_16x16x32_bf16 a[140:143], v[96:99], v[68:71], a[140:143]\n" \
"v_mfma_f32_16x16x32_bf16 a[172:175], v[96:99], v[72:75], a[172:175]\n" \
"s_add_u32 s89, s42, 0x0\n" \
"s_nop 0\n" \
"buffer_store_dwordx4 a[124:127], v20, s[80:83], s89 offen offset:2048 sc1\n" \
"v_mfma_f32_16x16x32_bf16 a[144:147], v[100:103], v[76:79], a[144:147]\n" \
"ds_read_b128 v[136:139], v29 offset:43008\n" \
"v_mfma_f32_16x16x32_bf16 a[148:151], v[104:107], v[76:79], a[148:151]\n" \
"ds_read_b128 v[140:143], v29 offset:45056\n" \
"v_mfma_f32_16x16x32_bf16 a[180:183], v[112:115], v[76:79], a[180:183]\n" \
"ds_read_b128 v[144:147], v29 offset:47104\n" \
"v_mfma_f32_16x16x32_bf16 a[176:179], v[76:79], v[108:111], a[176:179]\n" \
"v_mfma_f32_16x16x32_bf16 a[192:195], v[84:87], v[76:79], a[192:195]\n" \
"v_mfma_f32_16x16x32_bf16 a[224:227], v[84:87], v[80:83], a[224:227]\n" \
"v_mfma_f32_16x16x32_bf16 a[196:199], v[88:91], v[76:79], a[196:199]\n" \
"v_mfma_f32_16x16x32_bf16 a[228:231], v[88:91], v[80:83], a[228:231]\n" \
"s_add_u32 s88, s42, 0x0\n" \
"buffer_store_dwordx4 a[152:155], v20, s[80:83], s88 offen offset:3072 sc1\n" \
"v_mfma_f32_16x16x32_bf16 a[200:203], v[92:95], v[76:79], a[200:203]\n" \
"v_mfma_f32_16x16x32_bf16 a[232:235], v[92:95], v[80:83], a[232:235]\n" \
"v_mfma_f32_16x16x32_bf16 a[204:207], v[96:99], v[76:79], a[204:207]\n" \
"v_mfma_f32_16x16x32_bf16 a[236:239], v[96:99], v[80:83], a[236:239]\n" \
"v_mfma_f32_16x16x32_bf16 a[208:211], v[100:103], v[80:83], a[208:211]\n" \
"v_mfma_f32_16x16x32_bf16 a[212:215], v[104:107], v[80:83], a[212:215]\n" \
"v_mfma_f32_16x16x32_bf16 a[244:247], v[112:115], v[80:83], a[244:247]\n" \
"v_mfma_f32_16x16x32_bf16 a[240:243], v[80:83], v[108:111], a[240:243]\n" \
"s_waitcnt vmcnt(3) lgkmcnt(0)\n" \
"s_barrier\n" \
"s_nop 0\n" \
"v_mfma_f32_16x16x32_bf16 a[24:27], v[168:171], v[128:131], a[24:27]\n" \
"s_add_u32 m0, s40, 0x8000\n" \
"s_add_u32 s85, s39, 0xe000\n" \
"v_mfma_f32_16x16x32_bf16 a[28:31], v[128:131], v[172:175], a[28:31]\n" \
"buffer_load_dwordx4 v20, s[80:83], s85 offen sc1 lds\n" \
"v_mfma_f32_16x16x32_bf16 a[56:59], v[176:179], v[128:131], a[56:59]\n" \
"ds_read_b64_tr_b16 v[104:105], v37 offset:0\n" \
"v_mfma_f32_16x16x32_bf16 a[0:3], v[148:151], v[116:119], a[0:3]\n" \
"ds_read_b64_tr_b16 v[106:107], v37 offset:512\n" \
"v_mfma_f32_16x16x32_bf16 a[32:35], v[148:151], v[120:123], a[32:35]\n" \
"s_add_u32 m0, s40, 0x9000\n" \
"s_add_u32 s84, s39, 0xf000\n" \
"v_mfma_f32_16x16x32_bf16 a[4:7], v[152:155], v[116:119], a[4:7]\n" \
"buffer_load_dwordx4 v20, s[80:83], s84 offen sc1 lds\n" \
"v_mfma_f32_16x16x32_bf16 a[36:39], v[152:155], v[120:123], a[36:39]\n" \
"ds_read_b64_tr_b16 v[108:109], v38 offset:0\n" \
"v_mfma_f32_16x16x32_bf16 a[8:11], v[156:159], v[116:119], a[8:11]\n" \
"ds_read_b64_tr_b16 v[110:111], v38 offset:512\n" \
"v_mfma_f32_16x16x32_bf16 a[40:43], v[156:159], v[120:123], a[40:43]\n" \
"s_add_u32 m0, s40, 0xb000\n" \
"s_mov_b32 s85, 0x380\n" \
"v_mfma_f32_16x16x32_bf16 a[12:15], v[160:163], v[116:119], a[12:15]\n" \
"buffer_load_dwordx4 v21, s[76:79], s85 offen lds\n" \
"v_mfma_f32_16x16x32_bf16 a[44:47], v[160:163], v[120:123], a[44:47]\n" \
"ds_read_b64_tr_b16 v[112:113], v39 offset:0\n" \
"v_mfma_f32_16x16x32_bf16 a[16:19], v[164:167], v[132:135], a[16:19]\n" \
"ds_read_b64_tr_b16 v[114:115], v39 offset:512\n" \
"v_mfma_f32_16x16x32_bf16 a[20:23], v[168:171], v[132:135], a[20:23]\n" \
"s_add_u32 m0, s30, 0x8000\n" \
"s_mov_b32 s84, 0x380\n" \
"v_mfma_f32_16x16x32_bf16 a[52:55], v[176:179], v[132:135], a[52:55]\n" \
"buffer_load_dwordx4 v24, s[76:79], s84 offen lds\n" \
"v_mfma_f32_16x16x32_bf16 a[48:51], v[132:135], v[172:175], a[48:51]\n" \
"ds_read_b64_tr_b16 v[84:85], v32 offset:0\n" \
"v_mfma_f32_16x16x32_bf16 a[64:67], v[148:151], v[124:127], a[64:67]\n" \
"ds_read_b64_tr_b16 v[86:87], v32 offset:1024\n" \
"v_mfma_f32_16x16x32_bf16 a[96:99], v[148:151], v[128:131], a[96:99]\n" \
"ds_read_b64_tr_b16 v[88:89], v33 offset:0\n" \
"v_mfma_f32_16x16x32_bf16 a[68:71], v[152:155], v[124:127], a[68:71]\n" \
"s_add_u32 m0, s30, 0x8400\n" \
"s_mov_b32 s85, 0x2780\n" \
"v_mfma_f32_16x16x32_bf16 a[100:103], v[152:155], v[128:131], a[100:103]\n" \
"buffer_load_dwordx4 v25, s[76:79], s85 offen lds\n" \
"v_mfma_f32_16x16x32_bf16 a[72:75], v[156:159], v[124:127], a[72:75]\n" \
"ds_read_b64_tr_b16 v[90:91], v33 offset:1024\n" \
"v_mfma_f32_16x16x32_bf16 a[104:107], v[156:159], v[128:131], a[104:107]\n" \
"ds_read_b64_tr_b16 v[92:93], v34 offset:0\n" \
"v_mfma_f32_16x16x32_bf16 a[76:79], v[160:163], v[124:127], a[76:79]\n" \
"s_add_u32 m0, s31, 0x8000\n" \
"s_mov_b32 s84, 0x380000\n" \
"v_mfma_f32_16x16x32_bf16 a[108:111], v[160:163], v[128:131], a[108:111]\n" \
"buffer_load_dwordx4 v26, s[68:71], s84 offen lds\n" \
"v_mfma_f32_16x16x32_bf16 a[80:83], v[164:167], v[136:139], a[80:83]\n" \
"ds_read_b64_tr_b16 v[94:95], v34 offset:1024\n" \
"v_mfma_f32_16x16x32_bf16 a[84:87], v[168:171], v[136:139], a[84:87]\n" \
"ds_read_b64_tr_b16 v[96:97], v35 offset:0\n" \
"v_mfma_f32_16x16x32_bf16 a[116:119], v[176:179], v[136:139], a[116:119]\n" \
"s_add_u32 m0, s30, 0x8800\n" \
"s_mov_b32 s85, 0x4b80\n" \
"v_mfma_f32_16x16x32_bf16 a[112:115], v[136:139], v[172:175], a[112:115]\n" \
"buffer_load_dwordx4 v24, s[76:79], s85 offen lds\n" \
"v_mfma_f32_16x16x32_bf16 a[128:131], v[148:151], v[132:135], a[128:131]\n" \
"ds_read_b64_tr_b16 v[98:99], v35 offset:1024\n" \
"v_mfma_f32_16x16x32_bf16 a[160:163], v[148:151], v[136:139], a[160:163]\n" \
"ds_read_b64_tr_b16 v[100:101], v36 offset:0\n" \
"v_mfma_f32_16x16x32_bf16 a[132:135], v[152:155], v[132:135], a[132:135]\n" \
"s_add_u32 m0, s30, 0x8c00\n" \
"s_mov_b32 s84, 0x6f80\n" \
"v_mfma_f32_16x16x32_bf16 a[164:167], v[152:155], v[136:139], a[164:167]\n" \
"buffer_load_dwordx4 v25, s[76:79], s84 offen lds\n" \
"v_mfma_f32_16x16x32_bf16 a[136:139], v[156:159], v[132:135], a[136:139]\n" \
"ds_read_b64_tr_b16 v[102:103], v36 offset:512\n" \
"v_mfma_f32_16x16x32_bf16 a[168:171], v[156:159], v[136:139], a[168:171]\n" \
"ds_read_b128 v[52:55], v28 offset:0\n" \
"v_mfma_f32_16x16x32_bf16 a[140:143], v[160:163], v[132:135], a[140:143]\n" \
"ds_read_b128 v[56:59], v28 offset:2048\n" \
"v_mfma_f32_16x16x32_bf16 a[172:175], v[160:163], v[136:139], a[172:175]\n" \
"s_add_u32 m0, s31, 0x8400\n" \
"s_mov_b32 s85, 0x388000\n" \
"v_mfma_f32_16x16x32_bf16 a[144:147], v[164:167], v[140:143], a[144:147]\n" \
"buffer_load_dwordx4 v26, s[68:71], s85 offen lds\n" \
"v_mfma_f32_16x16x32_bf16 a[148:151], v[168:171], v[140:143], a[148:151]\n" \
"ds_read_b128 v[60:63], v28 offset:4096\n" \
"v_mfma_f32_16x16x32_bf16 a[180:183], v[176:179], v[140:143], a[180:183]\n" \
"ds_read_b128 v[64:67], v28 offset:6144\n" \
"v_mfma_f32_16x16x32_bf16 a[176:179], v[140:143], v[172:175], a[176:179]\n" \
"s_add_u32 m0, s30, 0x9000\n" \
"s_mov_b32 s84, 0x9380\n" \
"v_mfma_f32_16x16x32_bf16 a[192:195], v[148:151], v[140:143], a[192:195]\n" \
"buffer_load_dwordx4 v24, s[76:79], s84 offen lds\n" \
"v_mfma_f32_16x16x32_bf16 a[224:227], v[148:151], v[144:147], a[224:227]\n" \
"ds_read_b128 v[68:71], v28 offset:8192\n" \
"v_mfma_f32_16x16x32_bf16 a[196:199], v[152:155], v[140:143], a[196:199]\n" \
"ds_read_b128 v[72:75], v28 offset:10240\n" \
"v_mfma_f32_16x16x32_bf16 a[228:231], v[152:155], v[144:147], a[228:231]\n" \
"s_add_u32 m0, s30, 0x9400\n" \
"s_mov_b32 s85, 0xb780\n" \
"v_mfma_f32_16x16x32_bf16 a[200:203], v[156:159], v[140:143], a[200:203]\n" \
"buffer_load_dwordx4 v25, s[76:79], s85 offen lds\n" \
"v_mfma_f32_16x16x32_bf16 a[232:235], v[156:159], v[144:147], a[232:235]\n" \
"ds_read_b128 v[76:79], v28 offset:12288\n" \
"v_mfma_f32_16x16x32_bf16 a[204:207], v[160:163], v[140:143], a[204:207]\n" \
"ds_read_b128 v[80:83], v28 offset:14336\n" \
"v_mfma_f32_16x16x32_bf16 a[236:239], v[160:163], v[144:147], a[236:239]\n" \
"s_add_u32 m0, s31, 0x8800\n" \
"s_mov_b32 s84, 0x390000\n" \
"v_mfma_f32_16x16x32_bf16 a[208:211], v[164:167], v[144:147], a[208:211]\n" \
"buffer_load_dwordx4 v27, s[68:71], s84 offen lds\n" \
"v_mfma_f32_16x16x32_bf16 a[212:215], v[168:171], v[144:147], a[212:215]\n" \
"v_mfma_f32_16x16x32_bf16 a[244:247], v[176:179], v[144:147], a[244:247]\n" \
"v_mfma_f32_16x16x32_bf16 a[240:243], v[144:147], v[172:175], a[240:243]\n" \
"s_waitcnt lgkmcnt(4)\n" \
"s_nop 0\n" \
"v_mfma_f32_16x16x32_bf16 a[24:27], v[104:107], v[64:67], a[24:27]\n" \
"s_add_u32 m0, s30, 0x9800\n" \
"s_mov_b32 s84, 0xdb80\n" \
"v_mfma_f32_16x16x32_bf16 a[28:31], v[64:67], v[108:111], a[28:31]\n" \
"buffer_load_dwordx4 v24, s[76:79], s84 offen lds\n" \
"v_mfma_f32_16x16x32_bf16 a[56:59], v[112:115], v[64:67], a[56:59]\n" \
"ds_read_b64_tr_b16 v[168:169], v37 offset:4096\n" \
"v_mfma_f32_16x16x32_bf16 a[0:3], v[84:87], v[52:55], a[0:3]\n" \
"s_add_u32 m0, s30, 0x9c00\n" \
"s_mov_b32 s85, 0xff80\n" \
"v_mfma_f32_16x16x32_bf16 a[32:35], v[84:87], v[56:59], a[32:35]\n" \
"buffer_load_dwordx4 v25, s[76:79], s85 offen lds\n" \
"v_mfma_f32_16x16x32_bf16 a[4:7], v[88:91], v[52:55], a[4:7]\n" \
"ds_read_b64_tr_b16 v[170:171], v37 offset:4608\n" \
"v_mfma_f32_16x16x32_bf16 a[36:39], v[88:91], v[56:59], a[36:39]\n" \
"s_add_u32 m0, s31, 0x8c00\n" \
"s_mov_b32 s84, 0x398000\n" \
"v_mfma_f32_16x16x32_bf16 a[8:11], v[92:95], v[52:55], a[8:11]\n" \
"buffer_load_dwordx4 v27, s[68:71], s84 offen lds\n" \
"v_mfma_f32_16x16x32_bf16 a[40:43], v[92:95], v[56:59], a[40:43]\n" \
"ds_read_b64_tr_b16 v[172:173], v38 offset:4096\n" \
"v_mfma_f32_16x16x32_bf16 a[12:15], v[96:99], v[52:55], a[12:15]\n" \
"ds_read_b64_tr_b16 v[174:175], v38 offset:4608\n" \
"v_mfma_f32_16x16x32_bf16 a[44:47], v[96:99], v[56:59], a[44:47]\n" \
"ds_read_b64_tr_b16 v[176:177], v39 offset:4096\n" \
"s_waitcnt lgkmcnt(8)\n" \
"s_nop 0\n" \
"v_mfma_f32_16x16x32_bf16 a[16:19], v[100:103], v[68:71], a[16:19]\n" \
"ds_read_b64_tr_b16 v[178:179], v39 offset:4608\n" \
"v_mfma_f32_16x16x32_bf16 a[20:23], v[104:107], v[68:71], a[20:23]\n" \
"ds_read_b64_tr_b16 v[148:149], v32 offset:8192\n" \
"v_mfma_f32_16x16x32_bf16 a[52:55], v[112:115], v[68:71], a[52:55]\n" \
"ds_read_b64_tr_b16 v[150:151], v32 offset:9216\n" \
"v_mfma_f32_16x16x32_bf16 a[48:51], v[68:71], v[108:111], a[48:51]\n" \
"ds_read_b64_tr_b16 v[152:153], v33 offset:8192\n" \
"v_mfma_f32_16x16x32_bf16 a[64:67], v[84:87], v[60:63], a[64:67]\n" \
"ds_read_b64_tr_b16 v[154:155], v33 offset:9216\n" \
"v_mfma_f32_16x16x32_bf16 a[96:99], v[84:87], v[64:67], a[96:99]\n" \
"ds_read_b64_tr_b16 v[156:157], v34 offset:8192\n" \
"v_mfma_f32_16x16x32_bf16 a[68:71], v[88:91], v[60:63], a[68:71]\n" \
"ds_read_b64_tr_b16 v[158:159], v34 offset:9216\n" \
"v_mfma_f32_16x16x32_bf16 a[100:103], v[88:91], v[64:67], a[100:103]\n" \
"v_mfma_f32_16x16x32_bf16 a[72:75], v[92:95], v[60:63], a[72:75]\n" \
"ds_read_b64_tr_b16 v[160:161], v35 offset:8192\n" \
"v_mfma_f32_16x16x32_bf16 a[104:107], v[92:95], v[64:67], a[104:107]\n" \
"v_mfma_f32_16x16x32_bf16 a[76:79], v[96:99], v[60:63], a[76:79]\n" \
"ds_read_b64_tr_b16 v[162:163], v35 offset:9216\n" \
"v_mfma_f32_16x16x32_bf16 a[108:111], v[96:99], v[64:67], a[108:111]\n" \
"ds_read_b64_tr_b16 v[164:165], v36 offset:4096\n" \
"s_waitcnt lgkmcnt(15)\n" \
"s_nop 0\n" \
"v_mfma_f32_16x16x32_bf16 a[80:83], v[100:103], v[72:75], a[80:83]\n" \
"v_mfma_f32_16x16x32_bf16 a[84:87], v[104:107], v[72:75], a[84:87]\n" \
"ds_read_b64_tr_b16 v[166:167], v36 offset:4608\n" \
"v_mfma_f32_16x16x32_bf16 a[116:119], v[112:115], v[72:75], a[116:119]\n" \
"s_add_u32 s89, s42, 0x1000\n" \
"buffer_store_dwordx4 a[184:187], v20, s[80:83], s89 offen offset:0 sc1\n" \
"v_mfma_f32_16x16x32_bf16 a[112:115], v[72:75], v[108:111], a[112:115]\n" \
"ds_read_b128 v[116:119], v29 offset:0\n" \
"v_mfma_f32_16x16x32_bf16 a[128:131], v[84:87], v[68:71], a[128:131]\n" \
"ds_read_b128 v[120:123], v29 offset:2048\n" \
"v_mfma_f32_16x16x32_bf16 a[160:163], v[84:87], v[72:75], a[160:163]\n" \
"v_mfma_f32_16x16x32_bf16 a[132:135], v[88:91], v[68:71], a[132:135]\n" \
"ds_read_b128 v[124:127], v29 offset:4096\n" \
"v_mfma_f32_16x16x32_bf16 a[164:167], v[88:91], v[72:75], a[164:167]\n" \
"ds_read_b128 v[128:131], v29 offset:6144\n" \
"v_mfma_f32_16x16x32_bf16 a[136:139], v[92:95], v[68:71], a[136:139]\n" \
"v_mfma_f32_16x16x32_bf16 a[168:171], v[92:95], v[72:75], a[168:171]\n" \
"ds_read_b128 v[132:135], v29 offset:8192\n" \
"v_mfma_f32_16x16x32_bf16 a[140:143], v[96:99], v[68:71], a[140:143]\n" \
"v_mfma_f32_16x16x32_bf16 a[172:175], v[96:99], v[72:75], a[172:175]\n" \
"s_add_u32 s88, s42, 0x1000\n" \
"buffer_store_dwordx4 a[156:159], v20, s[80:83], s88 offen offset:1024 sc1\n" \
"v_mfma_f32_16x16x32_bf16 a[144:147], v[100:103], v[76:79], a[144:147]\n" \
"ds_read_b128 v[136:139], v29 offset:10240\n" \
"v_mfma_f32_16x16x32_bf16 a[148:151], v[104:107], v[76:79], a[148:151]\n" \
"ds_read_b128 v[140:143], v29 offset:12288\n" \
"v_mfma_f32_16x16x32_bf16 a[180:183], v[112:115], v[76:79], a[180:183]\n" \
"ds_read_b128 v[144:147], v29 offset:14336\n" \
"v_mfma_f32_16x16x32_bf16 a[176:179], v[76:79], v[108:111], a[176:179]\n" \
"v_mfma_f32_16x16x32_bf16 a[192:195], v[84:87], v[76:79], a[192:195]\n" \
"v_mfma_f32_16x16x32_bf16 a[224:227], v[84:87], v[80:83], a[224:227]\n" \
"v_mfma_f32_16x16x32_bf16 a[196:199], v[88:91], v[76:79], a[196:199]\n" \
"v_mfma_f32_16x16x32_bf16 a[228:231], v[88:91], v[80:83], a[228:231]\n" \
"s_add_u32 s89, s42, 0x3000\n" \
"buffer_store_dwordx4 a[188:191], v20, s[80:83], s89 offen offset:2048 sc1\n" \
"v_mfma_f32_16x16x32_bf16 a[200:203], v[92:95], v[76:79], a[200:203]\n" \
"v_mfma_f32_16x16x32_bf16 a[232:235], v[92:95], v[80:83], a[232:235]\n" \
"v_mfma_f32_16x16x32_bf16 a[204:207], v[96:99], v[76:79], a[204:207]\n" \
"v_mfma_f32_16x16x32_bf16 a[236:239], v[96:99], v[80:83], a[236:239]\n" \
"v_mfma_f32_16x16x32_bf16 a[208:211], v[100:103], v[80:83], a[208:211]\n" \
"v_mfma_f32_16x16x32_bf16 a[212:215], v[104:107], v[80:83], a[212:215]\n" \
"v_mfma_f32_16x16x32_bf16 a[244:247], v[112:115], v[80:83], a[244:247]\n" \
"v_mfma_f32_16x16x32_bf16 a[240:243], v[80:83], v[108:111], a[240:243]\n" \
"s_waitcnt vmcnt(3) lgkmcnt(0)\n" \
"s_barrier\n" \
"v_mfma_f32_16x16x32_bf16 a[24:27], v[168:171], v[128:131], a[24:27]\n" \
"s_add_u32 m0, s40, 0x0\n" \
"s_nop 0\n" \
"s_add_u32 s84, s39, 0x10000\n" \
"v_mfma_f32_16x16x32_bf16 a[28:31], v[128:131], v[172:175], a[28:31]\n" \
"buffer_load_dwordx4 v20, s[80:83], s84 offen sc1 lds\n" \
"v_mfma_f32_16x16x32_bf16 a[56:59], v[176:179], v[128:131], a[56:59]\n" \
"ds_read_b64_tr_b16 v[104:105], v37 offset:32768\n" \
"v_mfma_f32_16x16x32_bf16 a[0:3], v[148:151], v[116:119], a[0:3]\n" \
"ds_read_b64_tr_b16 v[106:107], v37 offset:33280\n" \
"v_mfma_f32_16x16x32_bf16 a[32:35], v[148:151], v[120:123], a[32:35]\n" \
"s_add_u32 m0, s40, 0x1000\n" \
"s_add_u32 s85, s39, 0x11000\n" \
"v_mfma_f32_16x16x32_bf16 a[4:7], v[152:155], v[116:119], a[4:7]\n" \
"buffer_load_dwordx4 v20, s[80:83], s85 offen sc1 lds\n" \
"v_mfma_f32_16x16x32_bf16 a[36:39], v[152:155], v[120:123], a[36:39]\n" \
"ds_read_b64_tr_b16 v[108:109], v38 offset:32768\n" \
"v_mfma_f32_16x16x32_bf16 a[8:11], v[156:159], v[116:119], a[8:11]\n" \
"ds_read_b64_tr_b16 v[110:111], v38 offset:33280\n" \
"v_mfma_f32_16x16x32_bf16 a[40:43], v[156:159], v[120:123], a[40:43]\n" \
"s_add_u32 m0, s40, 0x3000\n" \
"s_mov_b32 s84, 0x400\n" \
"v_mfma_f32_16x16x32_bf16 a[12:15], v[160:163], v[116:119], a[12:15]\n" \
"buffer_load_dwordx4 v21, s[76:79], s84 offen lds\n" \
"v_mfma_f32_16x16x32_bf16 a[44:47], v[160:163], v[120:123], a[44:47]\n" \
"ds_read_b64_tr_b16 v[112:113], v39 offset:32768\n" \
"v_mfma_f32_16x16x32_bf16 a[16:19], v[164:167], v[132:135], a[16:19]\n" \
"ds_read_b64_tr_b16 v[114:115], v39 offset:33280\n" \
"v_mfma_f32_16x16x32_bf16 a[20:23], v[168:171], v[132:135], a[20:23]\n" \
"s_add_u32 m0, s30, 0x0\n" \
"s_nop 0\n" \
"s_mov_b32 s85, 0x400\n" \
"v_mfma_f32_16x16x32_bf16 a[52:55], v[176:179], v[132:135], a[52:55]\n" \
"buffer_load_dwordx4 v24, s[76:79], s85 offen lds\n" \
"v_mfma_f32_16x16x32_bf16 a[48:51], v[132:135], v[172:175], a[48:51]\n" \
"ds_read_b64_tr_b16 v[84:85], v32 offset:32768\n" \
"v_mfma_f32_16x16x32_bf16 a[64:67], v[148:151], v[124:127], a[64:67]\n" \
"ds_read_b64_tr_b16 v[86:87], v32 offset:33792\n" \
"v_mfma_f32_16x16x32_bf16 a[96:99], v[148:151], v[128:131], a[96:99]\n" \
"ds_read_b64_tr_b16 v[88:89], v33 offset:32768\n" \
"v_mfma_f32_16x16x32_bf16 a[68:71], v[152:155], v[124:127], a[68:71]\n" \
"s_add_u32 m0, s30, 0x400\n" \
"s_mov_b32 s84, 0x2800\n" \
"v_mfma_f32_16x16x32_bf16 a[100:103], v[152:155], v[128:131], a[100:103]\n" \
"buffer_load_dwordx4 v25, s[76:79], s84 offen lds\n" \
"v_mfma_f32_16x16x32_bf16 a[72:75], v[156:159], v[124:127], a[72:75]\n" \
"ds_read_b64_tr_b16 v[90:91], v33 offset:33792\n" \
"v_mfma_f32_16x16x32_bf16 a[104:107], v[156:159], v[128:131], a[104:107]\n" \
"ds_read_b64_tr_b16 v[92:93], v34 offset:32768\n" \
"v_mfma_f32_16x16x32_bf16 a[76:79], v[160:163], v[124:127], a[76:79]\n" \
"s_add_u32 m0, s31, 0x0\n" \
"s_nop 0\n" \
"s_mov_b32 s85, 0x400000\n" \
"v_mfma_f32_16x16x32_bf16 a[108:111], v[160:163], v[128:131], a[108:111]\n" \
"buffer_load_dwordx4 v26, s[68:71], s85 offen lds\n" \
"v_mfma_f32_16x16x32_bf16 a[80:83], v[164:167], v[136:139], a[80:83]\n" \
"ds_read_b64_tr_b16 v[94:95], v34 offset:33792\n" \
"v_mfma_f32_16x16x32_bf16 a[84:87], v[168:171], v[136:139], a[84:87]\n" \
"ds_read_b64_tr_b16 v[96:97], v35 offset:32768\n" \
"v_mfma_f32_16x16x32_bf16 a[116:119], v[176:179], v[136:139], a[116:119]\n" \
"s_add_u32 m0, s30, 0x800\n" \
"s_mov_b32 s84, 0x4c00\n" \
"v_mfma_f32_16x16x32_bf16 a[112:115], v[136:139], v[172:175], a[112:115]\n" \
"buffer_load_dwordx4 v24, s[76:79], s84 offen lds\n" \
"v_mfma_f32_16x16x32_bf16 a[128:131], v[148:151], v[132:135], a[128:131]\n" \
"ds_read_b64_tr_b16 v[98:99], v35 offset:33792\n" \
"v_mfma_f32_16x16x32_bf16 a[160:163], v[148:151], v[136:139], a[160:163]\n" \
"ds_read_b64_tr_b16 v[100:101], v36 offset:32768\n" \
"v_mfma_f32_16x16x32_bf16 a[132:135], v[152:155], v[132:135], a[132:135]\n" \
"s_add_u32 m0, s30, 0xc00\n" \
"s_mov_b32 s85, 0x7000\n" \
"v_mfma_f32_16x16x32_bf16 a[164:167], v[152:155], v[136:139], a[164:167]\n" \
"buffer_load_dwordx4 v25, s[76:79], s85 offen lds\n" \
"v_mfma_f32_16x16x32_bf16 a[136:139], v[156:159], v[132:135], a[136:139]\n" \
"ds_read_b64_tr_b16 v[102:103], v36 offset:33280\n" \
"v_mfma_f32_16x16x32_bf16 a[168:171], v[156:159], v[136:139], a[168:171]\n" \
"ds_read_b128 v[52:55], v28 offset:32768\n" \
"v_mfma_f32_16x16x32_bf16 a[140:143], v[160:163], v[132:135], a[140:143]\n" \
"ds_read_b128 v[56:59], v28 offset:34816\n" \
"v_mfma_f32_16x16x32_bf16 a[172:175], v[160:163], v[136:139], a[172:175]\n" \
"s_add_u32 m0, s31, 0x400\n" \
"s_mov_b32 s84, 0x408000\n" \
"v_mfma_f32_16x16x32_bf16 a[144:147], v[164:167], v[140:143], a[144:147]\n" \
"buffer_load_dwordx4 v26, s[68:71], s84 offen lds\n" \
"v_mfma_f32_16x16x32_bf16 a[148:151], v[168:171], v[140:143], a[148:151]\n" \
"ds_read_b128 v[60:63], v28 offset:36864\n" \
"v_mfma_f32_16x16x32_bf16 a[180:183], v[176:179], v[140:143], a[180:183]\n" \
"ds_read_b128 v[64:67], v28 offset:38912\n" \
"v_mfma_f32_16x16x32_bf16 a[176:179], v[140:143], v[172:175], a[176:179]\n" \
"s_add_u32 m0, s30, 0x1000\n" \
"s_mov_b32 s85, 0x9400\n" \
"v_mfma_f32_16x16x32_bf16 a[192:195], v[148:151], v[140:143], a[192:195]\n" \
"buffer_load_dwordx4 v24, s[76:79], s85 offen lds\n" \
"v_mfma_f32_16x16x32_bf16 a[224:227], v[148:151], v[144:147], a[224:227]\n" \
"ds_read_b128 v[68:71], v28 offset:40960\n" \
"v_mfma_f32_16x16x32_bf16 a[196:199], v[152:155], v[140:143], a[196:199]\n" \
"ds_read_b128 v[72:75], v28 offset:43008\n" \
"v_mfma_f32_16x16x32_bf16 a[228:231], v[152:155], v[144:147], a[228:231]\n" \
"s_add_u32 m0, s30, 0x1400\n" \
"s_mov_b32 s84, 0xb800\n" \
"v_mfma_f32_16x16x32_bf16 a[200:203], v[156:159], v[140:143], a[200:203]\n" \
"buffer_load_dwordx4 v25, s[76:79], s84 offen lds\n" \
"v_mfma_f32_16x16x32_bf16 a[232:235], v[156:159], v[144:147], a[232:235]\n" \
"ds_read_b128 v[76:79], v28 offset:45056\n" \
"v_mfma_f32_16x16x32_bf16 a[204:207], v[160:163], v[140:143], a[204:207]\n" \
"ds_read_b128 v[80:83], v28 offset:47104\n" \
"v_mfma_f32_16x16x32_bf16 a[236:239], v[160:163], v[144:147], a[236:239]\n" \
"s_add_u32 m0, s31, 0x800\n" \
"s_mov_b32 s85, 0x410000\n" \
"v_mfma_f32_16x16x32_bf16 a[208:211], v[164:167], v[144:147], a[208:211]\n" \
"buffer_load_dwordx4 v27, s[68:71], s85 offen lds\n" \
"v_mfma_f32_16x16x32_bf16 a[212:215], v[168:171], v[144:147], a[212:215]\n" \
"v_mfma_f32_16x16x32_bf16 a[244:247], v[176:179], v[144:147], a[244:247]\n" \
"v_mfma_f32_16x16x32_bf16 a[240:243], v[144:147], v[172:175], a[240:243]\n" \
"s_waitcnt lgkmcnt(4)\n" \
"s_nop 0\n" \
"v_mfma_f32_16x16x32_bf16 a[24:27], v[104:107], v[64:67], a[24:27]\n" \
"s_add_u32 m0, s30, 0x1800\n" \
"s_mov_b32 s85, 0xdc00\n" \
"v_mfma_f32_16x16x32_bf16 a[28:31], v[64:67], v[108:111], a[28:31]\n" \
"buffer_load_dwordx4 v24, s[76:79], s85 offen lds\n" \
"v_mfma_f32_16x16x32_bf16 a[56:59], v[112:115], v[64:67], a[56:59]\n" \
"ds_read_b64_tr_b16 v[168:169], v37 offset:36864\n" \
"v_mfma_f32_16x16x32_bf16 a[0:3], v[84:87], v[52:55], a[0:3]\n" \
"s_add_u32 m0, s30, 0x1c00\n" \
"s_mov_b32 s84, 0x10000\n" \
"v_mfma_f32_16x16x32_bf16 a[32:35], v[84:87], v[56:59], a[32:35]\n" \
"buffer_load_dwordx4 v25, s[76:79], s84 offen lds\n" \
"v_mfma_f32_16x16x32_bf16 a[4:7], v[88:91], v[52:55], a[4:7]\n" \
"ds_read_b64_tr_b16 v[170:171], v37 offset:37376\n" \
"v_mfma_f32_16x16x32_bf16 a[36:39], v[88:91], v[56:59], a[36:39]\n" \
"s_add_u32 m0, s31, 0xc00\n" \
"s_mov_b32 s85, 0x418000\n" \
"v_mfma_f32_16x16x32_bf16 a[8:11], v[92:95], v[52:55], a[8:11]\n" \
"buffer_load_dwordx4 v27, s[68:71], s85 offen lds\n" \
"v_mfma_f32_16x16x32_bf16 a[40:43], v[92:95], v[56:59], a[40:43]\n" \
"ds_read_b64_tr_b16 v[172:173], v38 offset:36864\n" \
"v_mfma_f32_16x16x32_bf16 a[12:15], v[96:99], v[52:55], a[12:15]\n" \
"ds_read_b64_tr_b16 v[174:175], v38 offset:37376\n" \
"v_mfma_f32_16x16x32_bf16 a[44:47], v[96:99], v[56:59], a[44:47]\n" \
"ds_read_b64_tr_b16 v[176:177], v39 offset:36864\n" \
"s_waitcnt lgkmcnt(8)\n" \
"s_nop 0\n" \
"v_mfma_f32_16x16x32_bf16 a[16:19], v[100:103], v[68:71], a[16:19]\n" \
"ds_read_b64_tr_b16 v[178:179], v39 offset:37376\n" \
"v_mfma_f32_16x16x32_bf16 a[20:23], v[104:107], v[68:71], a[20:23]\n" \
"ds_read_b64_tr_b16 v[148:149], v32 offset:40960\n" \
"v_mfma_f32_16x16x32_bf16 a[52:55], v[112:115], v[68:71], a[52:55]\n" \
"ds_read_b64_tr_b16 v[150:151], v32 offset:41984\n" \
"v_mfma_f32_16x16x32_bf16 a[48:51], v[68:71], v[108:111], a[48:51]\n" \
"ds_read_b64_tr_b16 v[152:153], v33 offset:40960\n" \
"v_mfma_f32_16x16x32_bf16 a[64:67], v[84:87], v[60:63], a[64:67]\n" \
"ds_read_b64_tr_b16 v[154:155], v33 offset:41984\n" \
"v_mfma_f32_16x16x32_bf16 a[96:99], v[84:87], v[64:67], a[96:99]\n" \
"ds_read_b64_tr_b16 v[156:157], v34 offset:40960\n" \
"v_mfma_f32_16x16x32_bf16 a[68:71], v[88:91], v[60:63], a[68:71]\n" \
"ds_read_b64_tr_b16 v[158:159], v34 offset:41984\n" \
"v_mfma_f32_16x16x32_bf16 a[100:103], v[88:91], v[64:67], a[100:103]\n" \
"v_mfma_f32_16x16x32_bf16 a[72:75], v[92:95], v[60:63], a[72:75]\n" \
"ds_read_b64_tr_b16 v[160:161], v35 offset:40960\n" \
"v_mfma_f32_16x16x32_bf16 a[104:107], v[92:95], v[64:67], a[104:107]\n" \
"v_mfma_f32_16x16x32_bf16 a[76:79], v[96:99], v[60:63], a[76:79]\n" \
"ds_read_b64_tr_b16 v[162:163], v35 offset:41984\n" \
"v_mfma_f32_16x16x32_bf16 a[108:111], v[96:99], v[64:67], a[108:111]\n" \
"ds_read_b64_tr_b16 v[164:165], v36 offset:36864\n" \
"s_waitcnt lgkmcnt(15)\n" \
"s_nop 0\n" \
"v_mfma_f32_16x16x32_bf16 a[80:83], v[100:103], v[72:75], a[80:83]\n" \
"v_mfma_f32_16x16x32_bf16 a[84:87], v[104:107], v[72:75], a[84:87]\n" \
"ds_read_b64_tr_b16 v[166:167], v36 offset:37376\n" \
"v_mfma_f32_16x16x32_bf16 a[116:119], v[112:115], v[72:75], a[116:119]\n" \
"s_add_u32 s88, s42, 0x3000\n" \
"buffer_store_dwordx4 a[216:219], v20, s[80:83], s88 offen offset:3072 sc1\n" \
"v_mfma_f32_16x16x32_bf16 a[112:115], v[72:75], v[108:111], a[112:115]\n" \
"ds_read_b128 v[116:119], v29 offset:32768\n" \
"v_mfma_f32_16x16x32_bf16 a[128:131], v[84:87], v[68:71], a[128:131]\n" \
"ds_read_b128 v[120:123], v29 offset:34816\n" \
"v_mfma_f32_16x16x32_bf16 a[160:163], v[84:87], v[72:75], a[160:163]\n" \
"v_mfma_f32_16x16x32_bf16 a[132:135], v[88:91], v[68:71], a[132:135]\n" \
"ds_read_b128 v[124:127], v29 offset:36864\n" \
"v_mfma_f32_16x16x32_bf16 a[164:167], v[88:91], v[72:75], a[164:167]\n" \
"ds_read_b128 v[128:131], v29 offset:38912\n" \
"v_mfma_f32_16x16x32_bf16 a[136:139], v[92:95], v[68:71], a[136:139]\n" \
"v_mfma_f32_16x16x32_bf16 a[168:171], v[92:95], v[72:75], a[168:171]\n" \
"ds_read_b128 v[132:135], v29 offset:40960\n" \
"v_mfma_f32_16x16x32_bf16 a[140:143], v[96:99], v[68:71], a[140:143]\n" \
"v_mfma_f32_16x16x32_bf16 a[172:175], v[96:99], v[72:75], a[172:175]\n" \
"s_add_u32 s89, s42, 0x2000\n" \
"buffer_store_dwordx4 a[248:251], v20, s[80:83], s89 offen offset:0 sc1\n" \
"v_mfma_f32_16x16x32_bf16 a[144:147], v[100:103], v[76:79], a[144:147]\n" \
"ds_read_b128 v[136:139], v29 offset:43008\n" \
"v_mfma_f32_16x16x32_bf16 a[148:151], v[104:107], v[76:79], a[148:151]\n" \
"ds_read_b128 v[140:143], v29 offset:45056\n" \
"v_mfma_f32_16x16x32_bf16 a[180:183], v[112:115], v[76:79], a[180:183]\n" \
"ds_read_b128 v[144:147], v29 offset:47104\n" \
"v_mfma_f32_16x16x32_bf16 a[176:179], v[76:79], v[108:111], a[176:179]\n" \
"v_mfma_f32_16x16x32_bf16 a[192:195], v[84:87], v[76:79], a[192:195]\n" \
"v_mfma_f32_16x16x32_bf16 a[224:227], v[84:87], v[80:83], a[224:227]\n" \
"v_mfma_f32_16x16x32_bf16 a[196:199], v[88:91], v[76:79], a[196:199]\n" \
"v_mfma_f32_16x16x32_bf16 a[228:231], v[88:91], v[80:83], a[228:231]\n" \
"s_add_u32 s88, s42, 0x2000\n" \
"buffer_store_dwordx4 a[220:223], v20, s[80:83], s88 offen offset:1024 sc1\n" \
"v_mfma_f32_16x16x32_bf16 a[200:203], v[92:95], v[76:79], a[200:203]\n" \
"v_mfma_f32_16x16x32_bf16 a[232:235], v[92:95], v[80:83], a[232:235]\n" \
"v_mfma_f32_16x16x32_bf16 a[204:207], v[96:99], v[76:79], a[204:207]\n" \
"v_mfma_f32_16x16x32_bf16 a[236:239], v[96:99], v[80:83], a[236:239]\n" \
"v_mfma_f32_16x16x32_bf16 a[208:211], v[100:103], v[80:83], a[208:211]\n" \
"v_mfma_f32_16x16x32_bf16 a[212:215], v[104:107], v[80:83], a[212:215]\n" \
"v_mfma_f32_16x16x32_bf16 a[244:247], v[112:115], v[80:83], a[244:247]\n" \
"v_mfma_f32_16x16x32_bf16 a[240:243], v[80:83], v[108:111], a[240:243]\n" \
"s_waitcnt vmcnt(3) lgkmcnt(0)\n" \
"s_barrier\n" \
"v_mfma_f32_16x16x32_bf16 a[24:27], v[168:171], v[128:131], a[24:27]\n" \
"s_add_u32 s89, s42, 0x2000\n" \
"buffer_store_dwordx4 a[252:255], v20, s[80:83], s89 offen offset:2048 sc1\n" \
"v_mfma_f32_16x16x32_bf16 a[28:31], v[128:131], v[172:175], a[28:31]\n" \
"ds_read_b64_tr_b16 v[104:105], v37 offset:0\n" \
"v_mfma_f32_16x16x32_bf16 a[56:59], v[176:179], v[128:131], a[56:59]\n" \
"ds_read_b64_tr_b16 v[106:107], v37 offset:512\n" \
"v_mfma_f32_16x16x32_bf16 a[0:3], v[148:151], v[116:119], a[0:3]\n" \
"ds_read_b64_tr_b16 v[108:109], v38 offset:0\n" \
"v_mfma_f32_16x16x32_bf16 a[32:35], v[148:151], v[120:123], a[32:35]\n" \
"ds_read_b64_tr_b16 v[110:111], v38 offset:512\n" \
"v_mfma_f32_16x16x32_bf16 a[4:7], v[152:155], v[116:119], a[4:7]\n" \
"v_mfma_f32_16x16x32_bf16 a[36:39], v[152:155], v[120:123], a[36:39]\n" \
"ds_read_b64_tr_b16 v[112:113], v39 offset:0\n" \
"v_mfma_f32_16x16x32_bf16 a[8:11], v[156:159], v[116:119], a[8:11]\n" \
"v_mfma_f32_16x16x32_bf16 a[40:43], v[156:159], v[120:123], a[40:43]\n" \
"ds_read_b64_tr_b16 v[114:115], v39 offset:512\n" \
"v_mfma_f32_16x16x32_bf16 a[12:15], v[160:163], v[116:119], a[12:15]\n" \
"ds_read_b64_tr_b16 v[84:85], v32 offset:0\n" \
"v_mfma_f32_16x16x32_bf16 a[44:47], v[160:163], v[120:123], a[44:47]\n" \
"v_mfma_f32_16x16x32_bf16 a[16:19], v[164:167], v[132:135], a[16:19]\n" \
"ds_read_b64_tr_b16 v[86:87], v32 offset:1024\n" \
"v_mfma_f32_16x16x32_bf16 a[20:23], v[168:171], v[132:135], a[20:23]\n" \
"s_add_u32 s88, s42, 0x2000\n" \
"buffer_store_dwordx4 v[184:187], v20, s[80:83], s88 offen offset:3072 sc1\n" \
"v_mfma_f32_16x16x32_bf16 a[52:55], v[176:179], v[132:135], a[52:55]\n" \
"ds_read_b64_tr_b16 v[88:89], v33 offset:0\n" \
"v_mfma_f32_16x16x32_bf16 a[48:51], v[132:135], v[172:175], a[48:51]\n" \
"ds_read_b64_tr_b16 v[90:91], v33 offset:1024\n" \
"v_mfma_f32_16x16x32_bf16 a[64:67], v[148:151], v[124:127], a[64:67]\n" \
"v_mfma_f32_16x16x32_bf16 a[96:99], v[148:151], v[128:131], a[96:99]\n" \
"ds_read_b64_tr_b16 v[92:93], v34 offset:0\n" \
"v_mfma_f32_16x16x32_bf16 a[68:71], v[152:155], v[124:127], a[68:71]\n" \
"ds_read_b64_tr_b16 v[94:95], v34 offset:1024\n" \
"v_mfma_f32_16x16x32_bf16 a[100:103], v[152:155], v[128:131], a[100:103]\n" \
"v_mfma_f32_16x16x32_bf16 a[72:75], v[156:159], v[124:127], a[72:75]\n" \
"ds_read_b64_tr_b16 v[96:97], v35 offset:0\n" \
"v_mfma_f32_16x16x32_bf16 a[104:107], v[156:159], v[128:131], a[104:107]\n" \
"v_mfma_f32_16x16x32_bf16 a[76:79], v[160:163], v[124:127], a[76:79]\n" \
"ds_read_b64_tr_b16 v[98:99], v35 offset:1024\n" \
"v_mfma_f32_16x16x32_bf16 a[108:111], v[160:163], v[128:131], a[108:111]\n" \
"ds_read_b64_tr_b16 v[100:101], v36 offset:0\n" \
"v_mfma_f32_16x16x32_bf16 a[80:83], v[164:167], v[136:139], a[80:83]\n" \
"v_mfma_f32_16x16x32_bf16 a[84:87], v[168:171], v[136:139], a[84:87]\n" \
"ds_read_b64_tr_b16 v[102:103], v36 offset:512\n" \
"v_mfma_f32_16x16x32_bf16 a[116:119], v[176:179], v[136:139], a[116:119]\n" \
"s_add_u32 s89, s42, 0x3000\n" \
"buffer_store_dwordx4 v[188:191], v20, s[80:83], s89 offen offset:0 sc1\n" \
"v_mfma_f32_16x16x32_bf16 a[112:115], v[136:139], v[172:175], a[112:115]\n" \
"ds_read_b128 v[52:55], v28 offset:0\n" \
"v_mfma_f32_16x16x32_bf16 a[128:131], v[148:151], v[132:135], a[128:131]\n" \
"ds_read_b128 v[56:59], v28 offset:2048\n" \
"v_mfma_f32_16x16x32_bf16 a[160:163], v[148:151], v[136:139], a[160:163]\n" \
"v_mfma_f32_16x16x32_bf16 a[132:135], v[152:155], v[132:135], a[132:135]\n" \
"ds_read_b128 v[60:63], v28 offset:4096\n" \
"v_mfma_f32_16x16x32_bf16 a[164:167], v[152:155], v[136:139], a[164:167]\n" \
"ds_read_b128 v[64:67], v28 offset:6144\n" \
"v_mfma_f32_16x16x32_bf16 a[136:139], v[156:159], v[132:135], a[136:139]\n" \
"v_mfma_f32_16x16x32_bf16 a[168:171], v[156:159], v[136:139], a[168:171]\n" \
"ds_read_b128 v[68:71], v28 offset:8192\n" \
"v_mfma_f32_16x16x32_bf16 a[140:143], v[160:163], v[132:135], a[140:143]\n" \
"v_mfma_f32_16x16x32_bf16 a[172:175], v[160:163], v[136:139], a[172:175]\n" \
"ds_read_b128 v[72:75], v28 offset:10240\n" \
"v_mfma_f32_16x16x32_bf16 a[144:147], v[164:167], v[140:143], a[144:147]\n" \
"ds_read_b128 v[76:79], v28 offset:12288\n" \
"v_mfma_f32_16x16x32_bf16 a[148:151], v[168:171], v[140:143], a[148:151]\n" \
"v_mfma_f32_16x16x32_bf16 a[180:183], v[176:179], v[140:143], a[180:183]\n" \
"ds_read_b128 v[80:83], v28 offset:14336\n" \
"v_mfma_f32_16x16x32_bf16 a[176:179], v[140:143], v[172:175], a[176:179]\n" \
"s_add_u32 s88, s42, 0x3000\n" \
"buffer_store_dwordx4 v[252:255], v20, s[80:83], s88 offen offset:1024 sc1\n" \
"v_mfma_f32_16x16x32_bf16 a[192:195], v[148:151], v[140:143], a[192:195]\n" \
"v_mfma_f32_16x16x32_bf16 a[224:227], v[148:151], v[144:147], a[224:227]\n" \
"v_mfma_f32_16x16x32_bf16 a[196:199], v[152:155], v[140:143], a[196:199]\n" \
"v_mfma_f32_16x16x32_bf16 a[228:231], v[152:155], v[144:147], a[228:231]\n" \
"v_mfma_f32_16x16x32_bf16 a[200:203], v[156:159], v[140:143], a[200:203]\n" \
"v_mfma_f32_16x16x32_bf16 a[232:235], v[156:159], v[144:147], a[232:235]\n" \
"v_mfma_f32_16x16x32_bf16 a[204:207], v[160:163], v[140:143], a[204:207]\n" \
"v_mfma_f32_16x16x32_bf16 a[236:239], v[160:163], v[144:147], a[236:239]\n" \
"v_mfma_f32_16x16x32_bf16 a[208:211], v[164:167], v[144:147], a[208:211]\n" \
"v_mfma_f32_16x16x32_bf16 a[212:215], v[168:171], v[144:147], a[212:215]\n" \
"v_mfma_f32_16x16x32_bf16 a[244:247], v[176:179], v[144:147], a[244:247]\n" \
"v_mfma_f32_16x16x32_bf16 a[240:243], v[144:147], v[172:175], a[240:243]\n" \
"s_waitcnt lgkmcnt(4)\n" \
"v_mfma_f32_16x16x32_bf16 a[24:27], v[104:107], v[64:67], a[24:27]\n" \
"v_bfe_u32 v44, v20, 2, 5\n" \
"ds_read_b64_tr_b16 v[168:169], v37 offset:4096\n" \
"v_lshrrev_b32_e32 v24, 9, v20\n" \
"v_mfma_f32_16x16x32_bf16 a[28:31], v[64:67], v[108:111], a[28:31]\n" \
"buffer_load_dword v44, v44, s[80:83], s38 offen offset:0 sc1\n" \
"v_and_b32_e32 v26, 0x1ff, v20\n" \
"v_mfma_f32_16x16x32_bf16 a[56:59], v[112:115], v[64:67], a[56:59]\n" \
"ds_read_b64_tr_b16 v[170:171], v37 offset:4608\n" \
"v_lshl_add_u32 v24, v24, 3, v26\n" \
"v_lshrrev_b32_e64 v25, 8, v20\n" \
"v_mfma_f32_16x16x32_bf16 a[0:3], v[84:87], v[52:55], a[0:3]\n" \
"ds_read_b64_tr_b16 v[172:173], v38 offset:4096\n" \
"v_mul_u32_u24_e32 v25, 0x24700, v25\n" \
"v_add_u32_e64 v25, v25, v20\n" \
"v_mfma_f32_16x16x32_bf16 a[32:35], v[84:87], v[56:59], a[32:35]\n" \
"ds_read_b64_tr_b16 v[174:175], v38 offset:4608\n" \
"v_lshrrev_b32_e32 v26, 2, v20\n" \
"v_add_u32_e32 v26, s45, v26\n" \
"v_mfma_f32_16x16x32_bf16 a[4:7], v[88:91], v[52:55], a[4:7]\n" \
"v_add_u32_e32 v27, s46, v20\n" \
"v_add_u32_e32 v21, s43, v27\n" \
"v_accvgpr_read_b32 v180, a24\n" \
"v_mfma_f32_16x16x32_bf16 a[36:39], v[88:91], v[56:59], a[36:39]\n" \
"ds_read_b64_tr_b16 v[176:177], v39 offset:4096\n" \
"v_accvgpr_read_b32 v181, a25\n" \
"v_accvgpr_read_b32 v182, a26\n" \
"v_mfma_f32_16x16x32_bf16 a[8:11], v[92:95], v[52:55], a[8:11]\n" \
"v_accvgpr_read_b32 v183, a27\n" \
"v_accvgpr_read_b32 v252, a28\n" \
"v_accvgpr_read_b32 v253, a29\n" \
"v_mfma_f32_16x16x32_bf16 a[40:43], v[92:95], v[56:59], a[40:43]\n" \
"ds_read_b64_tr_b16 v[178:179], v39 offset:4608\n" \
"v_accvgpr_read_b32 v254, a30\n" \
"v_accvgpr_read_b32 v255, a31\n" \
"v_mfma_f32_16x16x32_bf16 a[12:15], v[96:99], v[52:55], a[12:15]\n" \
"ds_read_b64_tr_b16 v[148:149], v32 offset:8192\n" \
"v_accvgpr_read_b32 v48, a56\n" \
"v_accvgpr_read_b32 v49, a57\n" \
"v_mfma_f32_16x16x32_bf16 a[44:47], v[96:99], v[56:59], a[44:47]\n" \
"v_accvgpr_read_b32 v50, a58\n" \
"v_accvgpr_read_b32 v51, a59\n" \
"s_waitcnt lgkmcnt(10)\n" \
"v_mfma_f32_16x16x32_bf16 a[16:19], v[100:103], v[68:71], a[16:19]\n" \
"ds_read_b64_tr_b16 v[150:151], v32 offset:9216\n" \
"v_mfma_f32_16x16x32_bf16 a[20:23], v[104:107], v[68:71], a[20:23]\n" \
"v_mfma_f32_16x16x32_bf16 a[52:55], v[112:115], v[68:71], a[52:55]\n" \
"ds_read_b64_tr_b16 v[152:153], v33 offset:8192\n" \
"v_mfma_f32_16x16x32_bf16 a[48:51], v[68:71], v[108:111], a[48:51]\n" \
"ds_read_b64_tr_b16 v[154:155], v33 offset:9216\n" \
"s_nop 1\n" \
"v_accvgpr_read_b32 v184, a16\n" \
"v_accvgpr_read_b32 v185, a17\n" \
"v_mfma_f32_16x16x32_bf16 a[64:67], v[84:87], v[60:63], a[64:67]\n" \
"v_accvgpr_read_b32 v186, a18\n" \
"v_accvgpr_read_b32 v187, a19\n" \
"v_accvgpr_read_b32 v188, a20\n" \
"v_mfma_f32_16x16x32_bf16 a[96:99], v[84:87], v[64:67], a[96:99]\n" \
"ds_read_b64_tr_b16 v[156:157], v34 offset:8192\n" \
"v_accvgpr_read_b32 v189, a21\n" \
"v_accvgpr_read_b32 v190, a22\n" \
"v_mfma_f32_16x16x32_bf16 a[68:71], v[88:91], v[60:63], a[68:71]\n" \
"ds_read_b64_tr_b16 v[158:159], v34 offset:9216\n" \
"v_accvgpr_read_b32 v191, a23\n" \
"v_accvgpr_read_b32 v196, a52\n" \
"v_mfma_f32_16x16x32_bf16 a[100:103], v[88:91], v[64:67], a[100:103]\n" \
"v_lshlrev_b32_e32 v45, 3, v20\n" \
"v_add_u32_e32 v45, s30, v45\n" \
"v_accvgpr_read_b32 v197, a53\n" \
"v_mfma_f32_16x16x32_bf16 a[72:75], v[92:95], v[60:63], a[72:75]\n" \
"s_getpc_b64 s[86:87]\n" \
"L_ct1_%=:\n" \
"s_add_u32 s86, s86, L_end_%=-L_ct1_%=\n" \
"s_addc_u32 s87, s87, 0\n" \
"s_mov_b32 m0, s32\n" \
"s_nop 0\n" \
"v_mfma_f32_16x16x32_bf16 a[104:107], v[92:95], v[64:67], a[104:107]\n" \
"global_load_lds_dword v45, s[86:87]\n" \
"v_accvgpr_read_b32 v198, a54\n" \
"v_mfma_f32_16x16x32_bf16 a[76:79], v[96:99], v[60:63], a[76:79]\n" \
"ds_read_b64_tr_b16 v[160:161], v35 offset:8192\n" \
"v_accvgpr_read_b32 v199, a55\n" \
"v_accvgpr_read_b32 v192, a48\n" \
"v_mfma_f32_16x16x32_bf16 a[108:111], v[96:99], v[64:67], a[108:111]\n" \
"ds_read_b64_tr_b16 v[162:163], v35 offset:9216\n" \
"v_accvgpr_read_b32 v193, a49\n" \
"v_accvgpr_read_b32 v194, a50\n" \
"s_waitcnt lgkmcnt(15)\n" \
"v_mfma_f32_16x16x32_bf16 a[80:83], v[100:103], v[72:75], a[80:83]\n" \
"ds_read_b64_tr_b16 v[164:165], v36 offset:4096\n" \
"v_accvgpr_read_b32 v195, a51\n" \
"v_mfma_f32_16x16x32_bf16 a[84:87], v[104:107], v[72:75], a[84:87]\n" \
"ds_read_b64_tr_b16 v[166:167], v36 offset:4608\n" \
"v_mfma_f32_16x16x32_bf16 a[116:119], v[112:115], v[72:75], a[116:119]\n" \
"v_mfma_f32_16x16x32_bf16 a[112:115], v[72:75], v[108:111], a[112:115]\n" \
"ds_read_b128 v[116:119], v29 offset:0\n" \
"s_nop 0\n" \
"v_accvgpr_read_b32 v200, a80\n" \
"v_accvgpr_read_b32 v201, a81\n" \
"v_mfma_f32_16x16x32_bf16 a[128:131], v[84:87], v[68:71], a[128:131]\n" \
"ds_read_b128 v[120:123], v29 offset:2048\n" \
"v_accvgpr_read_b32 v202, a82\n" \
"v_accvgpr_read_b32 v203, a83\n" \
"v_mfma_f32_16x16x32_bf16 a[160:163], v[84:87], v[72:75], a[160:163]\n" \
"v_accvgpr_read_b32 v204, a84\n" \
"v_accvgpr_read_b32 v205, a85\n" \
"v_accvgpr_read_b32 v206, a86\n" \
"v_mfma_f32_16x16x32_bf16 a[132:135], v[88:91], v[68:71], a[132:135]\n" \
"ds_read_b128 v[124:127], v29 offset:4096\n" \
"v_accvgpr_read_b32 v207, a87\n" \
"v_accvgpr_read_b32 v212, a116\n" \
"v_mfma_f32_16x16x32_bf16 a[164:167], v[88:91], v[72:75], a[164:167]\n" \
"ds_read_b128 v[128:131], v29 offset:6144\n" \
"v_accvgpr_read_b32 v213, a117\n" \
"v_accvgpr_read_b32 v214, a118\n" \
"v_mfma_f32_16x16x32_bf16 a[136:139], v[92:95], v[68:71], a[136:139]\n" \
"v_accvgpr_read_b32 v215, a119\n" \
"v_accvgpr_read_b32 v208, a112\n" \
"v_accvgpr_read_b32 v209, a113\n" \
"v_mfma_f32_16x16x32_bf16 a[168:171], v[92:95], v[72:75], a[168:171]\n" \
"ds_read_b128 v[132:135], v29 offset:8192\n" \
"v_accvgpr_read_b32 v210, a114\n" \
"v_accvgpr_read_b32 v211, a115\n" \
"v_mfma_f32_16x16x32_bf16 a[140:143], v[96:99], v[68:71], a[140:143]\n" \
"v_mfma_f32_16x16x32_bf16 a[172:175], v[96:99], v[72:75], a[172:175]\n" \
"ds_read_b128 v[136:139], v29 offset:10240\n" \
"v_mfma_f32_16x16x32_bf16 a[144:147], v[100:103], v[76:79], a[144:147]\n" \
"ds_read_b128 v[140:143], v29 offset:12288\n" \
"v_mfma_f32_16x16x32_bf16 a[148:151], v[104:107], v[76:79], a[148:151]\n" \
"v_mfma_f32_16x16x32_bf16 a[180:183], v[112:115], v[76:79], a[180:183]\n" \
"ds_read_b128 v[144:147], v29 offset:14336\n" \
"v_mfma_f32_16x16x32_bf16 a[176:179], v[76:79], v[108:111], a[176:179]\n" \
"s_nop 2\n" \
"v_accvgpr_read_b32 v216, a144\n" \
"v_accvgpr_read_b32 v217, a145\n" \
"v_accvgpr_read_b32 v218, a146\n" \
"v_mfma_f32_16x16x32_bf16 a[192:195], v[84:87], v[76:79], a[192:195]\n" \
"v_accvgpr_read_b32 v219, a147\n" \
"v_accvgpr_read_b32 v220, a148\n" \
"v_accvgpr_read_b32 v221, a149\n" \
"s_waitcnt lgkmcnt(15)\n" \
"v_mfma_f32_16x16x32_bf16 a[224:227], v[84:87], v[80:83], a[224:227]\n" \
"v_accvgpr_read_b32 v222, a150\n" \
"v_accvgpr_read_b32 v223, a151\n" \
"v_accvgpr_read_b32 v228, a180\n" \
"v_mfma_f32_16x16x32_bf16 a[196:199], v[88:91], v[76:79], a[196:199]\n" \
"v_accvgpr_read_b32 v229, a181\n" \
"v_accvgpr_read_b32 v230, a182\n" \
"v_accvgpr_read_b32 v231, a183\n" \
"v_mfma_f32_16x16x32_bf16 a[228:231], v[88:91], v[80:83], a[228:231]\n" \
"v_accvgpr_read_b32 v224, a176\n" \
"v_accvgpr_read_b32 v225, a177\n" \
"v_accvgpr_read_b32 v226, a178\n" \
"v_mfma_f32_16x16x32_bf16 a[200:203], v[92:95], v[76:79], a[200:203]\n" \
"v_accvgpr_read_b32 v227, a179\n" \
"v_mfma_f32_16x16x32_bf16 a[232:235], v[92:95], v[80:83], a[232:235]\n" \
"v_mfma_f32_16x16x32_bf16 a[204:207], v[96:99], v[76:79], a[204:207]\n" \
"v_mfma_f32_16x16x32_bf16 a[236:239], v[96:99], v[80:83], a[236:239]\n" \
"v_mfma_f32_16x16x32_bf16 a[208:211], v[100:103], v[80:83], a[208:211]\n" \
"v_mfma_f32_16x16x32_bf16 a[212:215], v[104:107], v[80:83], a[212:215]\n" \
"v_mfma_f32_16x16x32_bf16 a[244:247], v[112:115], v[80:83], a[244:247]\n" \
"v_mfma_f32_16x16x32_bf16 a[240:243], v[80:83], v[108:111], a[240:243]\n" \
"s_nop 4\n" \
"s_nop 0\n" \
"v_accvgpr_read_b32 v232, a208\n" \
"v_accvgpr_read_b32 v233, a209\n" \
"v_accvgpr_read_b32 v234, a210\n" \
"v_accvgpr_read_b32 v235, a211\n" \
"v_accvgpr_read_b32 v236, a212\n" \
"v_accvgpr_read_b32 v237, a213\n" \
"v_accvgpr_read_b32 v238, a214\n" \
"v_accvgpr_read_b32 v239, a215\n" \
"v_accvgpr_read_b32 v244, a244\n" \
"v_accvgpr_read_b32 v245, a245\n" \
"v_accvgpr_read_b32 v246, a246\n" \
"v_accvgpr_read_b32 v247, a247\n" \
"v_accvgpr_read_b32 v240, a240\n" \
"v_accvgpr_read_b32 v241, a241\n" \
"v_accvgpr_read_b32 v242, a242\n" \
"v_accvgpr_read_b32 v243, a243\n" \
"s_waitcnt vmcnt(1) lgkmcnt(0)\n" \
"s_barrier\n" \
"v_mov_b32_e32 v45, s62\n" \
"s_mov_b64 exec, 1\n" \
"buffer_store_dword v45, off, s[80:83], s37 offset:32 sc1\n" \
"s_mov_b64 exec, -1\n" \
"v_cmp_ne_u32_e32 vcc, s62, v44\n" \
"s_nop 4\n" \
"s_cbranch_vccnz L_poll161_%=\n" \
"L_pollr161_%=:\n" \
"v_mfma_f32_16x16x32_bf16 v[48:51], v[176:179], v[128:131], v[48:51]\n" \
"s_mov_b32 s73, s50\n" \
"s_nop 0\n" \
"s_sub_u32 m0, s31, 0x10000\n" \
"v_mfma_f32_16x16x32_bf16 v[180:183], v[168:171], v[128:131], v[180:183]\n" \
"buffer_load_dwordx4 v20, s[80:83], s73 offen offset:0 sc1 lds\n" \
"v_mfma_f32_16x16x32_bf16 v[252:255], v[128:131], v[172:175], v[252:255]\n" \
"s_add_u32 s74, s50, 0x4000\n" \
"s_mov_b32 s70, 0x3e0293ee\n" \
"v_mfma_f32_16x16x32_bf16 v[196:199], v[176:179], v[132:135], v[196:199]\n" \
"s_add_u32 s75, s50, 0x8000\n" \
"s_mov_b32 s64, 0xaaaaaaaa\n" \
"v_mfma_f32_16x16x32_bf16 v[188:191], v[168:171], v[132:135], v[188:191]\n" \
"v_cvt_pk_bf16_f32 v180, v180, v181\n" \
"v_cvt_pk_bf16_f32 v181, v182, v183\n" \
"v_mfma_f32_16x16x32_bf16 v[184:187], v[164:167], v[132:135], v[184:187]\n" \
"buffer_load_dwordx4 v20, s[80:83], s73 offen offset:1024 sc1 lds\n" \
"v_mfma_f32_16x16x32_bf16 v[192:195], v[132:135], v[172:175], v[192:195]\n" \
"v_cvt_pk_bf16_f32 v182, v50, v51\n" \
"v_mov_b32_e32 v183, 0x3f80\n" \
"v_mfma_f32_16x16x32_bf16 v[212:215], v[176:179], v[136:139], v[212:215]\n" \
"v_cvt_pk_bf16_f32 v188, v188, v189\n" \
"v_cvt_pk_bf16_f32 v189, v190, v191\n" \
"v_mfma_f32_16x16x32_bf16 v[204:207], v[168:171], v[136:139], v[204:207]\n" \
"v_cvt_pk_bf16_f32 v184, v184, v185\n" \
"v_cvt_pk_bf16_f32 v185, v186, v187\n" \
"v_mfma_f32_16x16x32_bf16 v[200:203], v[164:167], v[136:139], v[200:203]\n" \
"buffer_load_dwordx4 v20, s[80:83], s73 offen offset:2048 sc1 lds\n" \
"v_mfma_f32_16x16x32_bf16 v[208:211], v[136:139], v[172:175], v[208:211]\n" \
"v_cvt_pk_bf16_f32 v190, v198, v199\n" \
"v_mov_b32_e32 v191, 0x3f80\n" \
"v_mfma_f32_16x16x32_bf16 v[228:231], v[176:179], v[140:143], v[228:231]\n" \
"v_cvt_pk_bf16_f32 v204, v204, v205\n" \
"v_cvt_pk_bf16_f32 v205, v206, v207\n" \
"v_mfma_f32_16x16x32_bf16 v[220:223], v[168:171], v[140:143], v[220:223]\n" \
"v_cvt_pk_bf16_f32 v200, v200, v201\n" \
"v_cvt_pk_bf16_f32 v201, v202, v203\n" \
"v_mfma_f32_16x16x32_bf16 v[216:219], v[164:167], v[140:143], v[216:219]\n" \
"buffer_load_dwordx4 v20, s[80:83], s73 offen offset:3072 sc1 lds\n" \
"v_mfma_f32_16x16x32_bf16 v[224:227], v[140:143], v[172:175], v[224:227]\n" \
"s_sub_u32 m0, s31, s51\n" \
"v_cvt_pk_bf16_f32 v186, v196, v197\n" \
"v_mfma_f32_16x16x32_bf16 v[244:247], v[176:179], v[144:147], v[244:247]\n" \
"v_cvt_pk_bf16_f32 v220, v220, v221\n" \
"v_cvt_pk_bf16_f32 v221, v222, v223\n" \
"v_mfma_f32_16x16x32_bf16 v[236:239], v[168:171], v[144:147], v[236:239]\n" \
"v_cvt_pk_bf16_f32 v216, v216, v217\n" \
"v_cvt_pk_bf16_f32 v217, v218, v219\n" \
"v_mfma_f32_16x16x32_bf16 v[232:235], v[164:167], v[144:147], v[232:235]\n" \
"buffer_load_dwordx4 v20, s[80:83], s74 offen offset:0 sc1 lds\n" \
"v_mfma_f32_16x16x32_bf16 v[240:243], v[144:147], v[172:175], v[240:243]\n" \
"v_mov_b32_e32 v187, 0\n" \
"v_cvt_pk_bf16_f32 v54, v192, v193\n" \
"v_mfma_f32_16x16x32_bf16 a[0:3], v[148:151], v[116:119], a[0:3]\n" \
"v_cvt_pk_bf16_f32 v236, v236, v237\n" \
"v_cvt_pk_bf16_f32 v237, v238, v239\n" \
"v_mfma_f32_16x16x32_bf16 a[32:35], v[148:151], v[120:123], a[32:35]\n" \
"v_cvt_pk_bf16_f32 v232, v232, v233\n" \
"v_cvt_pk_bf16_f32 v233, v234, v235\n" \
"v_mfma_f32_16x16x32_bf16 a[4:7], v[152:155], v[116:119], a[4:7]\n" \
"buffer_load_dwordx4 v20, s[80:83], s74 offen offset:1024 sc1 lds\n" \
"v_mfma_f32_16x16x32_bf16 a[36:39], v[152:155], v[120:123], a[36:39]\n" \
"v_accvgpr_read_b32 v72, a0\n" \
"v_accvgpr_read_b32 v73, a1\n" \
"v_mfma_f32_16x16x32_bf16 a[8:11], v[156:159], v[116:119], a[8:11]\n" \
"v_accvgpr_read_b32 v74, a2\n" \
"v_accvgpr_read_b32 v75, a3\n" \
"v_mfma_f32_16x16x32_bf16 a[40:43], v[156:159], v[120:123], a[40:43]\n" \
"v_mul_f32_e32 v72, s70, v72\n" \
"v_mul_f32_e32 v73, s70, v73\n" \
"v_mfma_f32_16x16x32_bf16 a[12:15], v[160:163], v[116:119], a[12:15]\n" \
"buffer_load_dwordx4 v20, s[80:83], s74 offen offset:2048 sc1 lds\n" \
"v_mfma_f32_16x16x32_bf16 a[44:47], v[160:163], v[120:123], a[44:47]\n" \
"v_accvgpr_read_b32 v76, a4\n" \
"v_accvgpr_read_b32 v77, a5\n" \
"v_mfma_f32_16x16x32_bf16 a[64:67], v[148:151], v[124:127], a[64:67]\n" \
"v_accvgpr_read_b32 v78, a6\n" \
"v_accvgpr_read_b32 v79, a7\n" \
"v_mfma_f32_16x16x32_bf16 a[96:99], v[148:151], v[128:131], a[96:99]\n" \
"v_mul_f32_e32 v74, s70, v74\n" \
"v_mul_f32_e32 v75, s70, v75\n" \
"v_mfma_f32_16x16x32_bf16 a[68:71], v[152:155], v[124:127], a[68:71]\n" \
"buffer_load_dwordx4 v20, s[80:83], s74 offen offset:3072 sc1 lds\n" \
"v_mfma_f32_16x16x32_bf16 a[100:103], v[152:155], v[128:131], a[100:103]\n" \
"v_cvt_pk_bf16_f32 v72, v72, v73\n" \
"s_sub_u32 m0, s31, 0x4000\n" \
"v_mfma_f32_16x16x32_bf16 a[72:75], v[156:159], v[124:127], a[72:75]\n" \
"v_mul_f32_e32 v76, s70, v76\n" \
"v_mul_f32_e32 v77, s70, v77\n" \
"v_mfma_f32_16x16x32_bf16 a[104:107], v[156:159], v[128:131], a[104:107]\n" \
"v_mul_f32_e32 v78, s70, v78\n" \
"v_mul_f32_e32 v79, s70, v79\n" \
"v_mfma_f32_16x16x32_bf16 a[76:79], v[160:163], v[124:127], a[76:79]\n" \
"v_cvt_pk_bf16_f32 v73, v74, v75\n" \
"v_cvt_pk_bf16_f32 v74, v76, v77\n" \
"v_mfma_f32_16x16x32_bf16 a[108:111], v[160:163], v[128:131], a[108:111]\n" \
"buffer_load_dwordx4 v20, s[80:83], s75 offen offset:0 sc1 lds\n" \
"v_cvt_pk_bf16_f32 v75, v78, v79\n" \
"ds_write_b128 v27, v[72:75] offset:0\n" \
"v_mfma_f32_16x16x32_bf16 a[128:131], v[148:151], v[132:135], a[128:131]\n" \
"v_accvgpr_read_b32 v72, a8\n" \
"v_accvgpr_read_b32 v73, a9\n" \
"v_accvgpr_read_b32 v74, a10\n" \
"v_accvgpr_read_b32 v75, a11\n" \
"v_mul_f32_e32 v72, s70, v72\n" \
"v_mul_f32_e32 v73, s70, v73\n" \
"v_accvgpr_read_b32 v76, a12\n" \
"v_accvgpr_read_b32 v77, a13\n" \
"v_accvgpr_read_b32 v78, a14\n" \
"v_accvgpr_read_b32 v79, a15\n" \
"v_mul_f32_e32 v74, s70, v74\n" \
"v_mul_f32_e32 v75, s70, v75\n" \
"v_cvt_pk_bf16_f32 v72, v72, v73\n" \
"v_mfma_f32_16x16x32_bf16 a[160:163], v[148:151], v[136:139], a[160:163]\n" \
"v_mul_f32_e32 v76, s70, v76\n" \
"v_mul_f32_e32 v77, s70, v77\n" \
"v_mul_f32_e32 v78, s70, v78\n" \
"v_mul_f32_e32 v79, s70, v79\n" \
"v_cvt_pk_bf16_f32 v73, v74, v75\n" \
"v_cvt_pk_bf16_f32 v74, v76, v77\n" \
"v_cvt_pk_bf16_f32 v75, v78, v79\n" \
"ds_write_b128 v27, v[72:75] offset:1024\n" \
"v_mfma_f32_16x16x32_bf16 a[132:135], v[152:155], v[132:135], a[132:135]\n" \
"v_accvgpr_read_b32 v72, a32\n" \
"v_accvgpr_read_b32 v73, a33\n" \
"v_accvgpr_read_b32 v74, a34\n" \
"v_accvgpr_read_b32 v75, a35\n" \
"v_mul_f32_e32 v72, s70, v72\n" \
"v_mul_f32_e32 v73, s70, v73\n" \
"v_accvgpr_read_b32 v76, a36\n" \
"v_accvgpr_read_b32 v77, a37\n" \
"v_accvgpr_read_b32 v78, a38\n" \
"v_accvgpr_read_b32 v79, a39\n" \
"v_mul_f32_e32 v74, s70, v74\n" \
"v_mul_f32_e32 v75, s70, v75\n" \
"v_cvt_pk_bf16_f32 v72, v72, v73\n" \
"v_mul_f32_e32 v76, s70, v76\n" \
"v_mul_f32_e32 v77, s70, v77\n" \
"v_mul_f32_e32 v78, s70, v78\n" \
"v_mul_f32_e32 v79, s70, v79\n" \
"v_cvt_pk_bf16_f32 v73, v74, v75\n" \
"v_mfma_f32_16x16x32_bf16 a[164:167], v[152:155], v[136:139], a[164:167]\n" \
"v_cvt_pk_bf16_f32 v74, v76, v77\n" \
"v_cvt_pk_bf16_f32 v75, v78, v79\n" \
"ds_write_b128 v27, v[72:75] offset:4096\n" \
"buffer_load_dwordx4 v20, s[80:83], s75 offen offset:1024 sc1 lds\n" \
"v_cvt_pk_bf16_f32 v55, v194, v195\n" \
"v_cvt_pk_bf16_f32 v206, v214, v215\n" \
"v_mov_b32_e32 v207, 0x3f80\n" \
"v_cvt_pk_bf16_f32 v202, v212, v213\n" \
"v_mov_b32_e64 v203, 0\n" \
"v_cvt_pk_bf16_f32 v56, v208, v209\n" \
"v_cvt_pk_bf16_f32 v57, v210, v211\n" \
"v_cvt_pk_bf16_f32 v222, v230, v231\n" \
"v_mov_b32_e32 v223, 0x3f80\n" \
"v_cvt_pk_bf16_f32 v218, v228, v229\n" \
"v_mov_b32_e64 v219, 0\n" \
"v_cvt_pk_bf16_f32 v58, v224, v225\n" \
"v_cvt_pk_bf16_f32 v59, v226, v227\n" \
"v_cvt_pk_bf16_f32 v238, v246, v247\n" \
"v_mov_b32_e32 v239, 0x3f80\n" \
"v_cvt_pk_bf16_f32 v234, v244, v245\n" \
"v_mov_b32_e64 v235, 0\n" \
"v_cvt_pk_bf16_f32 v60, v240, v241\n" \
"v_cvt_pk_bf16_f32 v61, v242, v243\n" \
"v_accvgpr_read_b32 v72, a40\n" \
"v_accvgpr_read_b32 v73, a41\n" \
"v_mfma_f32_16x16x32_bf16 a[136:139], v[156:159], v[132:135], a[136:139]\n" \
"v_mfma_f32_16x16x32_bf16 v[192:195], v[188:191], v[184:187], 0\n" \
"v_mfma_f32_16x16x32_bf16 v[164:167], v[180:183], v[184:187], 0\n" \
"v_mfma_f32_16x16x32_bf16 v[208:211], v[204:207], v[200:203], 0\n" \
"v_mfma_f32_16x16x32_bf16 v[168:171], v[188:191], v[200:203], 0\n" \
"v_mfma_f32_16x16x32_bf16 v[224:227], v[220:223], v[216:219], 0\n" \
"v_mfma_f32_16x16x32_bf16 v[172:175], v[204:207], v[216:219], 0\n" \
"v_mfma_f32_16x16x32_bf16 v[240:243], v[236:239], v[232:235], 0\n" \
"v_mfma_f32_16x16x32_bf16 v[176:179], v[220:223], v[232:235], 0\n" \
"v_accvgpr_read_b32 v74, a42\n" \
"v_accvgpr_read_b32 v75, a43\n" \
"v_mul_f32_e32 v72, s70, v72\n" \
"v_mul_f32_e32 v73, s70, v73\n" \
"v_accvgpr_read_b32 v76, a44\n" \
"v_accvgpr_read_b32 v77, a45\n" \
"v_accvgpr_read_b32 v78, a46\n" \
"v_accvgpr_read_b32 v79, a47\n" \
"v_mul_f32_e32 v74, s70, v74\n" \
"v_mul_f32_e32 v75, s70, v75\n" \
"v_cvt_pk_bf16_f32 v72, v72, v73\n" \
"v_mul_f32_e32 v76, s70, v76\n" \
"v_mul_f32_e32 v77, s70, v77\n" \
"v_mul_f32_e32 v78, s70, v78\n" \
"v_mul_f32_e32 v79, s70, v79\n" \
"v_cvt_pk_bf16_f32 v73, v74, v75\n" \
"v_cvt_pk_bf16_f32 v74, v76, v77\n" \
"v_cvt_pk_bf16_f32 v75, v78, v79\n" \
"v_mfma_f32_16x16x32_bf16 a[168:171], v[156:159], v[136:139], a[168:171]\n" \
"ds_write_b128 v27, v[72:75] offset:5120\n" \
"v_accvgpr_read_b32 v116, a64\n" \
"v_accvgpr_read_b32 v117, a65\n" \
"v_accvgpr_read_b32 v72, a72\n" \
"v_accvgpr_read_b32 v73, a73\n" \
"v_accvgpr_read_b32 v118, a66\n" \
"v_accvgpr_read_b32 v119, a67\n" \
"v_mul_f32_e32 v116, s70, v116\n" \
"v_mul_f32_e32 v117, s70, v117\n" \
"v_accvgpr_read_b32 v74, a74\n" \
"v_accvgpr_read_b32 v75, a75\n" \
"v_mul_f32_e32 v72, s70, v72\n" \
"v_mul_f32_e32 v73, s70, v73\n" \
"v_mfma_f32_16x16x32_bf16 a[140:143], v[160:163], v[132:135], a[140:143]\n" \
"v_accvgpr_read_b32 v120, a68\n" \
"v_accvgpr_read_b32 v121, a69\n" \
"v_accvgpr_read_b32 v122, a70\n" \
"v_accvgpr_read_b32 v123, a71\n" \
"v_mul_f32_e32 v118, s70, v118\n" \
"v_mul_f32_e32 v119, s70, v119\n" \
"v_cvt_pk_bf16_f32 v116, v116, v117\n" \
"v_accvgpr_read_b32 v76, a76\n" \
"v_accvgpr_read_b32 v77, a77\n" \
"v_accvgpr_read_b32 v78, a78\n" \
"v_accvgpr_read_b32 v79, a79\n" \
"v_mul_f32_e32 v74, s70, v74\n" \
"v_mul_f32_e32 v75, s70, v75\n" \
"v_cvt_pk_bf16_f32 v72, v72, v73\n" \
"s_mov_b32 s65, 0xaaaaaaaa\n" \
"v_mul_f32_e32 v120, s70, v120\n" \
"v_mul_f32_e32 v121, s70, v121\n" \
"v_mul_f32_e32 v122, s70, v122\n" \
"v_mul_f32_e32 v123, s70, v123\n" \
"v_cvt_pk_bf16_f32 v117, v118, v119\n" \
"v_mul_f32_e32 v76, s70, v76\n" \
"v_mul_f32_e32 v77, s70, v77\n" \
"v_mul_f32_e32 v78, s70, v78\n" \
"v_mul_f32_e32 v79, s70, v79\n" \
"v_cvt_pk_bf16_f32 v73, v74, v75\n" \
"s_mov_b32 s66, 0xfff0ffff\n" \
"s_mov_b32 s67, 0xf000ff00\n" \
"s_mov_b32 s68, 0xffc0fffc\n" \
"s_mov_b32 s69, 0xc000fc00\n" \
"v_cndmask_b32_e64 v192, v192, v193, s[64:65]\n" \
"v_cndmask_b32_e64 v164, v164, v165, s[64:65]\n" \
"v_cndmask_b32_e64 v194, v194, v195, s[64:65]\n" \
"v_cndmask_b32_e64 v166, v166, v167, s[64:65]\n" \
"v_cndmask_b32_e64 v208, v208, v209, s[64:65]\n" \
"v_cndmask_b32_e64 v168, v168, v169, s[64:65]\n" \
"v_cndmask_b32_e64 v210, v210, v211, s[64:65]\n" \
"v_cndmask_b32_e64 v170, v170, v171, s[64:65]\n" \
"v_cndmask_b32_e64 v224, v224, v225, s[64:65]\n" \
"v_cndmask_b32_e64 v172, v172, v173, s[64:65]\n" \
"v_cndmask_b32_e64 v226, v226, v227, s[64:65]\n" \
"v_cndmask_b32_e64 v174, v174, v175, s[64:65]\n" \
"v_cndmask_b32_e64 v240, v240, v241, s[64:65]\n" \
"v_cndmask_b32_e64 v176, v176, v177, s[64:65]\n" \
"v_cndmask_b32_e64 v242, v242, v243, s[64:65]\n" \
"v_cndmask_b32_e64 v178, v178, v179, s[64:65]\n" \
"v_cvt_pk_bf16_f32 v118, v120, v121\n" \
"v_cvt_pk_bf16_f32 v119, v122, v123\n" \
"v_cvt_pk_bf16_f32 v74, v76, v77\n" \
"v_cvt_pk_bf16_f32 v75, v78, v79\n" \
"v_cndmask_b32_e64 v192, v164, v192, s[66:67]\n" \
"v_cndmask_b32_e64 v194, v166, v194, s[68:69]\n" \
"v_cndmask_b32_e64 v208, v168, v208, s[66:67]\n" \
"v_cndmask_b32_e64 v210, v170, v210, s[68:69]\n" \
"v_cndmask_b32_e64 v224, v172, v224, s[66:67]\n" \
"v_cndmask_b32_e64 v226, v174, v226, s[68:69]\n" \
"v_cndmask_b32_e64 v240, v176, v240, s[66:67]\n" \
"v_cndmask_b32_e64 v242, v178, v242, s[68:69]\n" \
"ds_write_b128 v27, v[116:119] offset:8192\n" \
"ds_write_b128 v27, v[72:75] offset:9216\n" \
"v_mfma_f32_16x16x32_bf16 a[172:175], v[160:163], v[136:139], a[172:175]\n" \
"v_max_f32_e32 v193, v192, v194\n" \
"v_max_f32_e32 v209, v208, v210\n" \
"v_max_f32_e32 v225, v224, v226\n" \
"v_max_f32_e32 v241, v240, v242\n" \
"s_mov_b32 s71, 0x3e0293ee\n" \
"v_permlane32_swap_b32_e32 v193, v225\n" \
"v_permlane32_swap_b32_e32 v209, v241\n" \
"v_accvgpr_read_b32 v116, a96\n" \
"v_accvgpr_read_b32 v117, a97\n" \
"v_accvgpr_read_b32 v72, a104\n" \
"v_accvgpr_read_b32 v73, a105\n" \
"v_accvgpr_read_b32 v124, a128\n" \
"v_accvgpr_read_b32 v125, a129\n" \
"buffer_load_dwordx4 v20, s[80:83], s75 offen offset:2048 sc1 lds\n" \
"v_permlane32_swap_b32_e32 v196, v228\n" \
"v_permlane32_swap_b32_e32 v212, v244\n" \
"v_max_f32_e32 v193, v193, v225\n" \
"v_max_f32_e32 v209, v209, v241\n" \
"v_accvgpr_read_b32 v118, a98\n" \
"v_accvgpr_read_b32 v119, a99\n" \
"v_mul_f32_e32 v116, s70, v116\n" \
"v_mul_f32_e32 v117, s70, v117\n" \
"v_accvgpr_read_b32 v74, a106\n" \
"v_accvgpr_read_b32 v75, a107\n" \
"v_mul_f32_e32 v72, s70, v72\n" \
"v_mul_f32_e32 v73, s70, v73\n" \
"v_accvgpr_read_b32 v126, a130\n" \
"v_accvgpr_read_b32 v127, a131\n" \
"v_pk_mul_f32 v[124:125], v[124:125], s[70:71]\n" \
"v_accvgpr_read_b32 v84, a192\n" \
"v_accvgpr_read_b32 v85, a193\n" \
"v_accvgpr_read_b32 v86, a194\n" \
"v_accvgpr_read_b32 v87, a195\n" \
"v_permlane16_swap_b32_e32 v228, v244\n" \
"v_permlane16_swap_b32_e32 v193, v209\n" \
"v_accvgpr_read_b32 v120, a100\n" \
"v_accvgpr_read_b32 v121, a101\n" \
"v_accvgpr_read_b32 v122, a102\n" \
"v_accvgpr_read_b32 v123, a103\n" \
"v_mul_f32_e32 v118, s70, v118\n" \
"v_mul_f32_e32 v119, s70, v119\n" \
"v_cvt_pk_bf16_f32 v116, v116, v117\n" \
"v_accvgpr_read_b32 v76, a108\n" \
"v_accvgpr_read_b32 v77, a109\n" \
"v_accvgpr_read_b32 v78, a110\n" \
"v_accvgpr_read_b32 v79, a111\n" \
"v_mul_f32_e32 v74, s70, v74\n" \
"v_mul_f32_e32 v75, s70, v75\n" \
"v_cvt_pk_bf16_f32 v72, v72, v73\n" \
"v_accvgpr_read_b32 v128, a132\n" \
"v_accvgpr_read_b32 v129, a133\n" \
"v_accvgpr_read_b32 v130, a134\n" \
"v_accvgpr_read_b32 v131, a135\n" \
"v_pk_mul_f32 v[126:127], v[126:127], s[70:71]\n" \
"v_cvt_pk_bf16_f32 v124, v124, v125\n" \
"v_mfma_f32_16x16x32_bf16 v[84:87], v[148:151], v[140:143], v[84:87]\n" \
"s_mov_b32 s72, 0x3eae1a93\n" \
"v_mul_f32_e32 v45, 0xbfb8aa3b, v228\n" \
"v_mfma_f32_16x16x16_bf16 v[48:51], v[182:183], v[46:47], 0\n" \
"v_mfma_f32_16x16x16_bf16 v[196:199], v[190:191], v[46:47], 0\n" \
"v_mfma_f32_16x16x16_bf16 v[212:215], v[206:207], v[46:47], 0\n" \
"v_max_f32_e32 v193, v193, v209\n" \
"v_mul_f32_e32 v120, s70, v120\n" \
"v_mul_f32_e32 v121, s70, v121\n" \
"v_mul_f32_e32 v122, s70, v122\n" \
"v_mul_f32_e64 v123, s70, v123\n" \
"v_cvt_pk_bf16_f32 v117, v118, v119\n" \
"v_mul_f32_e32 v76, s70, v76\n" \
"v_mul_f32_e32 v77, s70, v77\n" \
"v_mul_f32_e32 v78, s70, v78\n" \
"v_mul_f32_e32 v79, s70, v79\n" \
"v_cvt_pk_bf16_f32 v73, v74, v75\n" \
"v_pk_mul_f32 v[128:129], v[128:129], s[70:71]\n" \
"v_pk_mul_f32 v[130:131], v[130:131], s[70:71]\n" \
"v_cvt_pk_bf16_f32 v125, v126, v127\n" \
"v_mfma_f32_16x16x16_bf16 v[228:231], v[222:223], v[46:47], 0\n" \
"v_mul_f32_e64 v193, s72, v193\n" \
"v_cvt_pk_bf16_f32 v118, v120, v121\n" \
"v_cvt_pk_bf16_f32 v119, v122, v123\n" \
"v_cvt_pk_bf16_f32 v74, v76, v77\n" \
"v_cvt_pk_bf16_f32 v75, v78, v79\n" \
"v_cvt_pk_bf16_f32 v126, v128, v129\n" \
"v_cvt_pk_bf16_f32 v127, v130, v131\n" \
"v_mfma_f32_16x16x16_bf16 v[244:247], v[238:239], v[46:47], 0\n" \
"v_mov_b32_e64 v209, v193\n" \
"ds_write_b128 v27, v[116:119] offset:12288\n" \
"ds_write_b128 v27, v[72:75] offset:13312\n" \
"ds_write_b128 v21, v[124:127] offset:16384\n" \
"v_accvgpr_read_b32 v100, a224\n" \
"v_accvgpr_read_b32 v101, a225\n" \
"v_accvgpr_read_b32 v102, a226\n" \
"v_accvgpr_read_b32 v103, a227\n" \
"v_permlane16_swap_b32_e32 v193, v209\n" \
"v_mov_b32_e32 v225, v193\n" \
"v_mfma_f32_16x16x32_bf16 v[100:103], v[148:151], v[144:147], v[100:103]\n" \
"v_mov_b32_e64 v241, v209\n" \
"v_accvgpr_read_b32 v116, a136\n" \
"v_accvgpr_read_b32 v117, a137\n" \
"v_accvgpr_read_b32 v72, a160\n" \
"v_accvgpr_read_b32 v73, a161\n" \
"v_accvgpr_read_b32 v124, a168\n" \
"v_accvgpr_read_b32 v125, a169\n" \
"v_permlane32_swap_b32_e32 v193, v225\n" \
"v_permlane32_swap_b32_e32 v209, v241\n" \
"v_accvgpr_read_b32 v118, a138\n" \
"v_accvgpr_read_b32 v119, a139\n" \
"v_pk_mul_f32 v[116:117], v[116:117], s[70:71]\n" \
"v_accvgpr_read_b32 v74, a162\n" \
"v_accvgpr_read_b32 v75, a163\n" \
"v_pk_mul_f32 v[72:73], v[72:73], s[70:71]\n" \
"v_accvgpr_read_b32 v126, a170\n" \
"v_accvgpr_read_b32 v127, a171\n" \
"v_pk_mul_f32 v[124:125], v[124:125], s[70:71]\n" \
"v_pk_mul_f32 v[84:85], v[84:85], s[70:71]\n" \
"v_pk_mul_f32 v[100:101], v[100:101], s[70:71]\n" \
"v_fma_f32 v192, v192, s72, -v193\n" \
"v_fma_f32 v194, v194, s72, -v193\n" \
"v_fma_f32 v208, v208, s72, -v209\n" \
"v_fma_f32 v210, v210, s72, -v209\n" \
"v_accvgpr_read_b32 v120, a140\n" \
"v_accvgpr_read_b32 v121, a141\n" \
"v_accvgpr_read_b32 v122, a142\n" \
"v_accvgpr_read_b32 v123, a143\n" \
"v_pk_mul_f32 v[118:119], v[118:119], s[70:71]\n" \
"v_cvt_pk_bf16_f32 v116, v116, v117\n" \
"v_accvgpr_read_b32 v76, a164\n" \
"v_accvgpr_read_b32 v77, a165\n" \
"v_accvgpr_read_b32 v78, a166\n" \
"v_accvgpr_read_b32 v79, a167\n" \
"v_pk_mul_f32 v[74:75], v[74:75], s[70:71]\n" \
"v_cvt_pk_bf16_f32 v72, v72, v73\n" \
"v_accvgpr_read_b32 v128, a172\n" \
"v_accvgpr_read_b32 v129, a173\n" \
"v_accvgpr_read_b32 v130, a174\n" \
"v_accvgpr_read_b32 v131, a175\n" \
"v_pk_mul_f32 v[126:127], v[126:127], s[70:71]\n" \
"v_cvt_pk_bf16_f32 v124, v124, v125\n" \
"v_pk_mul_f32 v[86:87], v[86:87], s[70:71]\n" \
"v_cvt_pk_bf16_f32 v84, v84, v85\n" \
"v_pk_mul_f32 v[102:103], v[102:103], s[70:71]\n" \
"v_cvt_pk_bf16_f32 v100, v100, v101\n" \
"v_accvgpr_read_b32 v88, a196\n" \
"v_accvgpr_read_b32 v89, a197\n" \
"v_accvgpr_read_b32 v90, a198\n" \
"v_accvgpr_read_b32 v91, a199\n" \
"v_exp_f32_e32 v192, v192\n" \
"v_exp_f32_e32 v194, v194\n" \
"v_exp_f32_e32 v208, v208\n" \
"v_exp_f32_e32 v210, v210\n" \
"v_fma_f32 v224, v224, s72, -v225\n" \
"v_fma_f32 v226, v226, s72, -v225\n" \
"v_pk_mul_f32 v[120:121], v[120:121], s[70:71]\n" \
"v_pk_mul_f32 v[122:123], v[122:123], s[70:71]\n" \
"v_cvt_pk_bf16_f32 v117, v118, v119\n" \
"v_pk_mul_f32 v[76:77], v[76:77], s[70:71]\n" \
"v_pk_mul_f32 v[78:79], v[78:79], s[70:71]\n" \
"v_cvt_pk_bf16_f32 v73, v74, v75\n" \
"v_pk_mul_f32 v[128:129], v[128:129], s[70:71]\n" \
"v_pk_mul_f32 v[130:131], v[130:131], s[70:71]\n" \
"v_cvt_pk_bf16_f32 v125, v126, v127\n" \
"v_cvt_pk_bf16_f32 v85, v86, v87\n" \
"v_cvt_pk_bf16_f32 v101, v102, v103\n" \
"v_mfma_f32_16x16x32_bf16 v[88:91], v[152:155], v[140:143], v[88:91]\n" \
"v_exp_f32_e32 v224, v224\n" \
"v_exp_f32_e32 v226, v226\n" \
"v_fma_f32 v240, v240, s72, -v241\n" \
"v_fma_f32 v242, v242, s72, -v241\n" \
"v_cvt_pk_bf16_f32 v118, v120, v121\n" \
"v_cvt_pk_bf16_f32 v119, v122, v123\n" \
"v_cvt_pk_bf16_f32 v74, v76, v77\n" \
"v_cvt_pk_bf16_f32 v75, v78, v79\n" \
"v_pk_mul_f32 v[88:89], v[88:89], s[70:71]\n" \
"v_pk_mul_f32 v[90:91], v[90:91], s[70:71]\n" \
"v_cvt_pk_bf16_f32 v126, v128, v129\n" \
"v_cvt_pk_bf16_f32 v127, v130, v131\n" \
"v_cvt_pk_bf16_f32 v86, v88, v89\n" \
"v_cvt_pk_bf16_f32 v87, v90, v91\n" \
"v_cvt_pk_bf16_f32 v192, v192, v194\n" \
"v_cvt_pk_bf16_f32 v208, v208, v210\n" \
"v_exp_f32_e32 v240, v240\n" \
"v_exp_f32_e32 v242, v242\n" \
"ds_write_b128 v21, v[116:119] offset:17408\n" \
"ds_write_b128 v21, v[72:75] offset:20480\n" \
"ds_write_b128 v21, v[124:127] offset:21504\n" \
"ds_write_b128 v21, v[84:87] offset:24576\n" \
"v_cvt_pk_bf16_f32 v62, v48, v49\n" \
"v_cvt_pk_bf16_f32 v63, v50, v51\n" \
"v_cvt_pk_bf16_f32 v64, v196, v197\n" \
"v_cvt_pk_bf16_f32 v65, v198, v199\n" \
"v_cvt_pk_bf16_f32 v66, v212, v213\n" \
"v_cvt_pk_bf16_f32 v67, v214, v215\n" \
"v_perm_b32 v184, v192, v192, v40\n" \
"v_perm_b32 v185, v192, v192, v41\n" \
"v_perm_b32 v186, v192, v192, v42\n" \
"v_perm_b32 v187, v192, v192, v43\n" \
"v_perm_b32 v200, v208, v208, v40\n" \
"v_perm_b32 v201, v208, v208, v41\n" \
"v_perm_b32 v202, v208, v208, v42\n" \
"v_perm_b32 v203, v208, v208, v43\n" \
"v_cvt_pk_bf16_f32 v224, v224, v226\n" \
"v_accvgpr_read_b32 v104, a228\n" \
"v_accvgpr_read_b32 v105, a229\n" \
"v_accvgpr_read_b32 v106, a230\n" \
"v_accvgpr_read_b32 v107, a231\n" \
"v_cvt_pk_bf16_f32 v68, v228, v229\n" \
"v_cvt_pk_bf16_f32 v69, v230, v231\n" \
"v_mfma_f32_16x16x32_bf16 v[164:167], v[62:65], v[184:187], 0\n" \
"v_mfma_f32_16x16x32_bf16 v[168:171], v[64:67], v[200:203], 0\n" \
"v_perm_b32 v216, v224, v224, v40\n" \
"v_perm_b32 v217, v224, v224, v41\n" \
"v_perm_b32 v218, v224, v224, v42\n" \
"v_perm_b32 v219, v224, v224, v43\n" \
"v_cvt_pk_bf16_f32 v240, v240, v242\n" \
"v_mfma_f32_16x16x32_bf16 v[104:107], v[152:155], v[144:147], v[104:107]\n" \
"v_cvt_pk_bf16_f32 v70, v244, v245\n" \
"v_cvt_pk_bf16_f32 v71, v246, v247\n" \
"v_mfma_f32_16x16x32_bf16 v[172:175], v[66:69], v[216:219], 0\n" \
"v_perm_b32 v232, v240, v240, v40\n" \
"v_perm_b32 v233, v240, v240, v41\n" \
"v_perm_b32 v234, v240, v240, v42\n" \
"v_perm_b32 v235, v240, v240, v43\n" \
"buffer_load_dwordx4 v20, s[80:83], s75 offen offset:3072 sc1 lds\n" \
"v_pk_mul_f32 v[104:105], v[104:105], s[70:71]\n" \
"v_pk_mul_f32 v[106:107], v[106:107], s[70:71]\n" \
"v_cvt_pk_bf16_f32 v102, v104, v105\n" \
"v_cvt_pk_bf16_f32 v103, v106, v107\n" \
"ds_write_b128 v21, v[100:103] offset:28672\n" \
"v_mfma_f32_16x16x32_bf16 v[176:179], v[68:71], v[232:235], 0\n" \
"v_accvgpr_read_b32 v92, a200\n" \
"v_accvgpr_read_b32 v93, a201\n" \
"v_accvgpr_read_b32 v94, a202\n" \
"v_accvgpr_read_b32 v95, a203\n" \
"v_accvgpr_read_b32 v108, a232\n" \
"v_accvgpr_read_b32 v109, a233\n" \
"v_mfma_f32_16x16x32_bf16 v[92:95], v[156:159], v[140:143], v[92:95]\n" \
"v_accvgpr_read_b32 v110, a234\n" \
"v_accvgpr_read_b32 v111, a235\n" \
"v_cvt_pk_bf16_f32 v52, v252, v253\n" \
"v_cvt_pk_bf16_f32 v53, v254, v255\n" \
"v_mfma_f32_16x16x32_bf16 v[108:111], v[156:159], v[144:147], v[108:111]\n" \
"v_exp_f32_e64 v45, v45\n" \
"v_mfma_f32_16x16x32_bf16 v[192:195], v[52:55], v[184:187], 0\n" \
"v_mfma_f32_16x16x32_bf16 v[208:211], v[54:57], v[200:203], 0\n" \
"v_pk_mul_f32 v[92:93], v[92:93], s[70:71]\n" \
"v_pk_mul_f32 v[94:95], v[94:95], s[70:71]\n" \
"v_cvt_pk_bf16_f32 v92, v92, v93\n" \
"v_cvt_pk_bf16_f32 v93, v94, v95\n" \
"v_mfma_f32_16x16x32_bf16 v[224:227], v[56:59], v[216:219], 0\n" \
"v_pk_mul_f32 v[108:109], v[108:109], s[70:71]\n" \
"v_pk_mul_f32 v[110:111], v[110:111], s[70:71]\n" \
"v_cvt_pk_bf16_f32 v108, v108, v109\n" \
"v_cvt_pk_bf16_f32 v109, v110, v111\n" \
"v_mfma_f32_16x16x32_bf16 v[240:243], v[58:61], v[232:235], 0\n" \
"v_mov_b32_dpp v166, v170 quad_perm:[0,1,2,3] row_mask:0x2 bank_mask:0xf\n" \
"v_accvgpr_read_b32 v96, a204\n" \
"v_accvgpr_read_b32 v97, a205\n" \
"v_mov_b32_dpp v166, v174 quad_perm:[0,1,2,3] row_mask:0x4 bank_mask:0xf\n" \
"v_accvgpr_read_b32 v98, a206\n" \
"v_accvgpr_read_b32 v99, a207\n" \
"v_add_f32_e64 v45, 1.0, v45\n" \
"v_mov_b32_dpp v166, v178 quad_perm:[0,1,2,3] row_mask:0x8 bank_mask:0xf\n" \
"v_mfma_f32_16x16x32_bf16 v[96:99], v[160:163], v[140:143], v[96:99]\n" \
"v_rcp_f32_e64 v45, v45\n" \
"v_mov_b32_dpp v164, v168 quad_perm:[0,1,2,3] row_mask:0x2 bank_mask:0xf\n" \
"v_mov_b32_dpp v165, v169 quad_perm:[0,1,2,3] row_mask:0x2 bank_mask:0xf\n" \
"v_rcp_f32_e64 v166, v166\n" \
"v_mov_b32_dpp v164, v172 quad_perm:[0,1,2,3] row_mask:0x4 bank_mask:0xf\n" \
"v_mov_b32_dpp v165, v173 quad_perm:[0,1,2,3] row_mask:0x4 bank_mask:0xf\n" \
"v_mul_f32_e64 v166, v45, v166\n" \
"v_mov_b32_dpp v164, v176 quad_perm:[0,1,2,3] row_mask:0x8 bank_mask:0xf\n" \
"v_pk_mul_f32 v[96:97], v[96:97], s[70:71]\n" \
"v_pk_mul_f32 v[98:99], v[98:99], s[70:71]\n" \
"v_cvt_pk_bf16_f32 v94, v96, v97\n" \
"v_cvt_pk_bf16_f32 v95, v98, v99\n" \
"ds_write_b128 v21, v[92:95] offset:25600\n" \
"v_mov_b32_dpp v165, v177 quad_perm:[0,1,2,3] row_mask:0x8 bank_mask:0xf\n" \
"v_accvgpr_read_b32 v112, a236\n" \
"v_accvgpr_read_b32 v113, a237\n" \
"v_accvgpr_read_b32 v114, a238\n" \
"v_accvgpr_read_b32 v115, a239\n" \
"v_mul_f32_e32 v164, v166, v164\n" \
"v_mul_f32_e32 v165, v166, v165\n" \
"v_mov_b32_e64 v170, v166\n" \
"v_mfma_f32_16x16x32_bf16 v[112:115], v[160:163], v[144:147], v[112:115]\n" \
"s_mov_b32 s76, s52\n" \
"v_permlane16_swap_b32_e32 v166, v170\n" \
"s_mov_b32 m0, s53\n" \
"v_mov_b32_e32 v174, v166\n" \
"v_mov_b32_e64 v178, v170\n" \
"buffer_load_dwordx4 v20, s[80:83], s76 offen offset:0 sc1 lds\n" \
"v_permlane32_swap_b32_e32 v166, v174\n" \
"v_permlane32_swap_b32_e32 v170, v178\n" \
"v_pk_mul_f32 v[112:113], v[112:113], s[70:71]\n" \
"v_pk_mul_f32 v[114:115], v[114:115], s[70:71]\n" \
"v_cvt_pk_bf16_f32 v110, v112, v113\n" \
"v_cvt_pk_bf16_f32 v111, v114, v115\n" \
"ds_write_b128 v21, v[108:111] offset:29696\n" \
"v_mul_f32_e32 v192, v166, v192\n" \
"v_mul_f32_e32 v193, v166, v193\n" \
"v_mul_f32_e32 v208, v170, v208\n" \
"v_mul_f32_e32 v209, v170, v209\n" \
"v_mul_f32_e64 v224, v174, v224\n" \
"buffer_load_dwordx4 v20, s[80:83], s76 offen offset:1024 sc1 lds\n" \
"v_mul_f32_e32 v225, v174, v225\n" \
"v_mul_f32_e32 v240, v178, v240\n" \
"v_mul_f32_e32 v241, v178, v241\n" \
"v_mul_f32_e32 v194, v166, v194\n" \
"v_mul_f32_e64 v195, v166, v195\n" \
"v_cvt_pk_bf16_f32 v192, v192, v193\n" \
"v_mul_f32_e32 v210, v170, v210\n" \
"v_mul_f32_e32 v211, v170, v211\n" \
"v_cvt_pk_bf16_f32 v208, v208, v209\n" \
"v_mul_f32_e32 v226, v174, v226\n" \
"v_mul_f32_e32 v227, v174, v227\n" \
"v_cvt_pk_bf16_f32 v224, v224, v225\n" \
"v_mul_f32_e32 v242, v178, v242\n" \
"v_mul_f32_e32 v243, v178, v243\n" \
"v_cvt_pk_bf16_f32 v240, v240, v241\n" \
"v_sub_f32_e32 v44, 1.0, v45\n" \
"v_cvt_pk_bf16_f32 v164, v164, v165\n" \
"v_cvt_pk_bf16_f32 v193, v194, v195\n" \
"s_add_u32 s84, s48, 0x0\n" \
"v_cvt_pk_bf16_f32 v209, v210, v211\n" \
"s_add_u32 s85, s48, 0x24800\n" \
"v_cvt_pk_bf16_f32 v225, v226, v227\n" \
"s_add_u32 s86, s48, 0x49000\n" \
"v_cvt_pk_bf16_f32 v241, v242, v243\n" \
"s_add_u32 s87, s48, 0x6d800\n" \
"ds_write_b32 v26, v44\n" \
"buffer_store_dword v164, v25, s[80:83], s49 offen sc1\n" \
"buffer_store_dwordx2 v[192:193], v24, s[80:83], s84 offen sc1\n" \
"buffer_store_dwordx2 v[208:209], v24, s[80:83], s85 offen sc1\n" \
"buffer_store_dwordx2 v[224:225], v24, s[80:83], s86 offen sc1\n" \
"buffer_store_dwordx2 v[240:241], v24, s[80:83], s87 offen sc1\n" \
"s_branch L_end_%=\n" \
"L_poll61_%=:\n" \
"s_mov_b32 s84, 0\n" \
"L_pl61_%=:\n" \
"s_sleep 1\n" \
"v_bfe_u32 v44, v20, 2, 5\n" \
"buffer_load_dword v44, v44, s[80:83], s38 offen offset:128 sc1\n" \
"s_waitcnt vmcnt(0)\n" \
"v_cmp_ne_u32_e32 vcc, s62, v44\n" \
"s_add_u32 s84, s84, 1\n" \
"s_nop 3\n" \
"s_cbranch_vccz L_pollr61_%=\n" \
"s_cmp_gt_u32 s84, 0x400000\n" \
"s_cbranch_scc0 L_pl61_%=\n" \
"s_branch L_pollr61_%=\n" \
"L_poll161_%=:\n" \
"s_mov_b32 s84, 0\n" \
"L_pl161_%=:\n" \
"s_sleep 1\n" \
"v_bfe_u32 v44, v20, 2, 5\n" \
"buffer_load_dword v44, v44, s[80:83], s38 offen offset:0 sc1\n" \
"s_waitcnt vmcnt(0)\n" \
"v_cmp_ne_u32_e32 vcc, s62, v44\n" \
"s_add_u32 s84, s84, 1\n" \
"s_nop 3\n" \
"s_cbranch_vccz L_pollr161_%=\n" \
"s_cmp_gt_u32 s84, 0x400000\n" \
"s_cbranch_scc0 L_pl161_%=\n" \
"s_branch L_pollr161_%=\n" \
"L_end_%=:\n"
#define PROJ_VCLOB "v21","v24","v25","v28","v29","v32","v33","v34","v35","v36","v37","v38","v39","v40","v41","v42","v43","v44","v45","v46","v47","v48","v49","v50","v51","v52","v53","v54","v55","v56","v57","v58","v59","v60","v61","v62","v63","v64","v65","v66","v67","v68","v69","v70","v71","v72","v73","v74","v75","v76","v77","v78","v79","v80","v81","v82","v83","v84","v85","v86","v87","v88","v89","v90","v91","v92","v93","v94","v95","v96","v97","v98","v99","v100","v101","v102","v103","v104","v105","v106","v107","v108","v109","v110","v111","v112","v113","v114","v115","v116","v117","v118","v119","v120","v121","v122","v123","v124","v125","v126","v127","v128","v129","v130","v131","v132","v133","v134","v135","v136","v137","v138","v139","v140","v141","v142","v143","v144","v145","v146","v147","v148","v149","v150","v151","v152","v153","v154","v155","v156","v157","v158","v159","v160","v161","v162","v163","v164","v165","v166","v167","v168","v169","v170","v171","v172","v173","v174","v175","v176","v177","v178","v179","v180","v181","v182","v183","v184","v185","v186","v187","v188","v189","v190","v192","v193","v194","v195","v196","v197","v198","v199","v200","v201","v202","v203","v204","v205","v206","v207","v208","v209","v210","v211","v212","v213","v214","v215","v216","v217","v218","v219","v220","v221","v222","v223","v224","v225","v226","v227","v228","v229","v230","v231","v232","v233","v234","v235","v236","v237","v238","v239","v240","v241","v242","v243","v244","v245","v246","v247","v248","v249","v250","v251","v252","v253","v254","v255"
#define PROJ_SCLOB "s4","s5","s6","s7","s32","s33","s34","s35","s36","s37","s38","s39","s40","s41","s42","s43","s44","s45","s46","s47","s48","s49","s50","s51","s52","s53","s54","s55","s63","s64","s65","s66","s67","s68","s69","s70","s71","s72","s73","s74","s75","s76","s77","s78","s79","s80","s81","s82","s83","s84","s85","s86","s87","s88","s89","s90","s91","s92","s93","s94","s95","s96","s97","s98","s99","s100","s101"

typedef const u16* const __attribute__((address_space(4)))* kaptr;
__device__ __forceinline__ void proj_phase(kaptr& ka, char*& ws, const unsigned wid, u32x4 (&vp)[5], unsigned& vl, u32x4 (&sa)[5], u32x2 s24, u32x2 s28, unsigned tdum, u32x4& srdA, u32x2& sd, unsigned& tag
    ) {
    const unsigned b = blockIdx.x, h = (b & 7) * 4 + ((b >> 3) & 3), mt = b >> 5;
    const unsigned rb = (wid < 2u ? 15u - mt : mt) * 128u;
    const unsigned lane = threadIdx.x & 63u, wc = wid & 1u;
    u32x2 vAc, vB;
#pragma unroll
    for (int par = 0; par < 2; ++par) {
        const unsigned lr = lane >> 3, cc = (lane & 7u) ^ (4u * par + (lr >> 1)), r0 = rb + wc * 64u + lr;
        vAc[par] = r0 * (DC * 2) + cc * 16u;
        const unsigned br = lane >> 4, p16 = lane & 15u, sc = (((p16 >> 1) ^ (br | (par << 2))) << 1) | (p16 & 1u);
        vB[par] = (16u * wid + br) * (NBIG * 2) + h * (HD * 2) + sc * 16u;
    }
    vl = lane * 16u;
    u32x2 s30 = {wid * 8192u, 65536u + wid * 4096u};
    asm volatile(PROJ_ASM
                 : "={s62}"(tag), "={s[56:59]}"(srdA), "={s[60:61]}"(sd), "+{s[8:11]}"(sa[0]), "+{s[12:15]}"(sa[1]), "+{s[16:19]}"(sa[2]), "+{s[20:23]}"(sa[3]), "+{s[24:25]}"(s24), "+{s[28:29]}"(s28), "+{s[30:31]}"(s30),
                   "={v[0:3]}"(vp[0]), "={v[4:7]}"(vp[1]), "={v[8:11]}"(vp[2]), "={v[12:15]}"(vp[3]), "={v[16:19]}"(vp[4]), "+{v[22:23]}"(vAc), "+{v[26:27]}"(vB), "+{v191}"(tdum), "+{s[26:27]}"(ws), "+{s[0:1]}"(ka)
                 : "{s2}"(b), "{s3}"(wid), "{v20}"(vl)
                 : "memory", "m0", "vcc", "scc", PROJ_VCLOB, PROJ_SCLOB, ACLOB_LO, ACLOB_HI);
    sa[4] = (u32x4){s24[0], 0u, 0u, 0u};
}

namespace att {
#define DI __device__ __forceinline__
typedef __bf16 bfx8 __attribute__((ext_vector_type(8)));
DI unsigned rfl(unsigned x) { return __builtin_amdgcn_readfirstlane(x); }
DI f32x4 mfma(u32x4 a, u32x4 b, f32x4 c) { return __builtin_amdgcn_mfma_f32_16x16x32_bf16(__builtin_bit_cast(bfx8, a), __builtin_bit_cast(bfx8, b), c, 0, 0, 0); }
typedef __bf16 bfx2 __attribute__((ext_vector_type(2)));
typedef float f32x2 __attribute__((ext_vector_type(2)));
DI unsigned cvtpk2(float lo, float hi) { f32x2 v = {lo, hi}; bfx2 b = __builtin_convertvector(v, bfx2); return __builtin_bit_cast(unsigned, b); }
DI void att_exact(const u16* __restrict__ x, const u16* __restrict__ wq, char* __restrict__ ws, unsigned h, unsigned r0, float g1) {
    const unsigned lane = lane_id(), g = lane >> 4, c = lane & 15u;
    f32x4 o[8];
#pragma unroll
    for (int n = 0; n < 8; ++n) o[n] = (f32x4){0.f, 0.f, 0.f, 0.f};
#pragma unroll 1
    for (unsigned kk = 0; kk < (unsigned)DM; kk += 32u) {
        const unsigned k = kk + 8u * g;
        const u32x4 xf = *reinterpret_cast<const u32x4*>(x + (r0 + c) * DM + k);
#pragma unroll
        for (int n = 0; n < 8; ++n) {
            const u16* w = wq + k * NBIG + h * HD + n * 16 + c; u32x4 wf;
#pragma unroll
            for (int e = 0; e < 4; ++e) wf[e] = (unsigned)w[2 * e * NBIG] | ((unsigned)w[(2 * e + 1) * NBIG] << 16);
            o[n] = mfma(wf, xf, o[n]);
        }
    }
    u32x4 q[4];
#pragma unroll
    for (int p = 0; p < 4; ++p) {
        const f32x4 a = o[2 * p] * 0.12751743082f, b = o[2 * p + 1] * 0.12751743082f;
        q[p] = (u32x4){cvtpk2(a[0], a[1]), cvtpk2(a[2], a[3]), cvtpk2(b[0], b[1]), cvtpk2(b[2], b[3])};
    }
#pragma unroll
    for (int n = 0; n < 8; ++n) o[n] = (f32x4){0.f, 0.f, 0.f, 0.f};
    const __amdgpu_buffer_rsrc_t srd = __builtin_amdgcn_make_buffer_rsrc((void*)(ws + WS_K + (size_t)h * 1048576), (short)0, 1048576, 0x00020000);
    float m = -3.0e38f, l = 0.f;
#pragma unroll 1
    for (unsigned k0 = 0; k0 <= r0 + 15u; k0 += 32u) {
        const unsigned tb = (k0 >> 6) * 32768u + (k0 & 32u) * 256u;
        f32x4 s[2]; float mx = m;
#pragma unroll
        for (int kt = 0; kt < 2; ++kt) {
            s[kt] = (f32x4){0.f, 0.f, 0.f, 0.f};
#pragma unroll
            for (int p = 0; p < 4; ++p) s[kt] = mfma(__builtin_amdgcn_raw_buffer_load_b128(srd, lane * 16u + (kt * 4 + p) * 1024u, tb, 16), q[p], s[kt]);
#pragma unroll
            for (int e = 0; e < 4; ++e) { if (k0 + 16u * kt + 4u * g + e > r0 + c) s[kt][e] = -3.0e38f; mx = fmaxf(mx, s[kt][e]); }
        }
        mx = fmaxf(mx, __shfl_xor(mx, 16)); mx = fmaxf(mx, __shfl_xor(mx, 32));
        const float al = __builtin_amdgcn_exp2f(m - mx); m = mx;
        u32x4 pf;
#pragma unroll
        for (int e = 0; e < 4; ++e) pf[e] = cvtpk2(__builtin_amdgcn_exp2f(s[e >> 1][2 * (e & 1)] - m), __builtin_amdgcn_exp2f(s[e >> 1][2 * (e & 1) + 1] - m));
        l *= al;
#pragma unroll
        for (int e = 0; e < 4; ++e) l += __uint_as_float(pf[e] << 16) + __uint_as_float(pf[e] & 0xffff0000u);
#pragma unroll
        for (int n = 0; n < 8; ++n) o[n] = mfma(__builtin_amdgcn_raw_buffer_load_b128(srd, lane * 16u + 16384u + n * 1024u, tb, 16), pf, o[n] * al);
    }
    l += __shfl_xor(l, 16); l += __shfl_xor(l, 32);
    const float f = g1 / l;
    const __amdgpu_buffer_rsrc_t srdA = __builtin_amdgcn_make_buffer_rsrc((void*)(ws + WS_AF), (short)0, 128 * KS_TOT * 1024, 0x00020000);
#pragma unroll
    for (int p = 0; p < 4; ++p) {
        const f32x4 a = o[2 * p] * f, b = o[2 * p + 1] * f;
        __builtin_amdgcn_raw_buffer_store_b128((u32x4){cvtpk2(a[0], a[1]), cvtpk2(a[2], a[3]), cvtpk2(b[0], b[1]), cvtpk2(b[2], b[3])}, srdA, lane * 16u + p * 1024u, ((r0 >> 4) * KS_TOT + 18u + 4u * h) * 1024u, 16);
    }
}
}

#define ATT_ASM \
"s_mov_b32 s101, 0\n" \
"s_waitcnt vmcnt(15) lgkmcnt(0)\n" \
"s_barrier\n" \
"v_accvgpr_write_b32 a216, 0\n" \
"v_accvgpr_write_b32 a217, 0\n" \
"v_accvgpr_write_b32 a218, 0\n" \
"v_accvgpr_write_b32 a219, 0\n" \
"v_bfe_u32 v61, v20, 2, 5\n" \
"s_mov_b32 s96, s24\n" \
"v_mfma_f32_32x32x16_bf16 v[128:143], a[216:219], a[216:219], 0\n" \
"buffer_load_dword v57, v61, s[56:59], s96 offen sc1\n" \
"v_and_b32_e32 v63, 0x300, v20\n" \
"ds_read_b128 v[96:99], v63 offset:0\n" \
"v_add_u32_e32 v58, s22, v20\n" \
"ds_read_b128 a[0:3], v58 offset:0\n" \
"v_mfma_f32_32x32x16_bf16 v[144:159], a[216:219], a[216:219], 0\n" \
"ds_read_b128 a[16:19], v58 offset:8192\n" \
"v_add_u32_e32 v59, s23, v20\n" \
"ds_read_b128 a[32:35], v59 offset:0\n" \
"ds_read_b128 a[48:51], v59 offset:8192\n" \
"ds_read_b128 v[100:103], v63 offset:1024\n" \
"ds_read_b128 a[4:7], v58 offset:1024\n" \
"v_mfma_f32_32x32x16_bf16 v[160:175], a[216:219], a[216:219], 0\n" \
"ds_read_b128 a[20:23], v58 offset:9216\n" \
"ds_read_b128 a[36:39], v59 offset:1024\n" \
"ds_read_b128 a[52:55], v59 offset:9216\n" \
"s_sub_u32 s67, s12, 0xc000\n" \
"ds_read_b128 v[104:107], v63 offset:2048\n" \
"s_mov_b32 s65, s12\n" \
"v_mfma_f32_32x32x16_bf16 v[176:191], a[216:219], a[216:219], 0\n" \
"v_add_u32_e32 v55, s67, v20\n" \
"v_add_u32_e32 v52, 0xc000, v55\n" \
"s_xor_b32 m0, s65, 0x10000\n" \
"ds_read_b128 a[8:11], v58 offset:2048\n" \
"buffer_load_dwordx4 v52, s[8:11], 0 offen offset:0 sc1 lds\n" \
"v_mfma_f32_32x32x16_bf16 v[192:207], a[216:219], a[216:219], 0\n" \
"ds_read_b128 a[24:27], v58 offset:10240\n" \
"ds_read_b128 a[40:43], v59 offset:2048\n" \
"ds_read_b128 a[56:59], v59 offset:10240\n" \
"ds_read_b128 v[108:111], v63 offset:3072\n" \
"buffer_load_dwordx4 v52, s[8:11], 0 offen offset:1024 sc1 lds\n" \
"v_mfma_f32_32x32x16_bf16 v[208:223], a[216:219], a[216:219], 0\n" \
"ds_read_b128 a[12:15], v58 offset:3072\n" \
"ds_read_b128 a[28:31], v58 offset:11264\n" \
"ds_read_b128 a[44:47], v59 offset:3072\n" \
"ds_read_b128 a[60:63], v59 offset:11264\n" \
"s_waitcnt lgkmcnt(15)\n" \
"v_xor_b32_e32 v96, 0x80008000, v96\n" \
"v_mfma_f32_32x32x16_bf16 v[224:239], a[216:219], a[216:219], 0\n" \
"v_xor_b32_e32 v97, 0x80008000, v97\n" \
"v_xor_b32_e32 v98, 0x80008000, v98\n" \
"v_xor_b32_e32 v99, 0x80008000, v99\n" \
"ds_read_b128 a[160:163], v20 offset:0\n" \
"ds_read_b128 a[164:167], v20 offset:1024\n" \
"s_waitcnt lgkmcnt(15)\n" \
"v_xor_b32_e32 v100, 0x80008000, v100\n" \
"v_mfma_f32_32x32x16_bf16 v[240:255], a[216:219], a[216:219], 0\n" \
"v_xor_b32_e32 v101, 0x80008000, v101\n" \
"v_xor_b32_e32 v102, 0x80008000, v102\n" \
"v_xor_b32_e32 v103, 0x80008000, v103\n" \
"ds_read_b128 a[168:171], v20 offset:2048\n" \
"ds_read_b128 a[172:175], v20 offset:3072\n" \
"ds_read_b128 a[188:191], v20 offset:7168\n" \
"v_mfma_f32_16x16x32_bf16 a[192:195], a[216:219], a[216:219], 0\n" \
"ds_read_b128 a[184:187], v20 offset:6144\n" \
"s_waitcnt lgkmcnt(15)\n" \
"v_xor_b32_e32 v104, 0x80008000, v104\n" \
"v_mfma_f32_16x16x32_bf16 a[196:199], a[216:219], a[216:219], 0\n" \
"v_xor_b32_e32 v105, 0x80008000, v105\n" \
"v_xor_b32_e32 v106, 0x80008000, v106\n" \
"v_mfma_f32_16x16x32_bf16 v[32:35], v[96:99], a[0:3], 0\n" \
"v_xor_b32_e32 v107, 0x80008000, v107\n" \
"ds_read_b128 a[180:183], v20 offset:5120\n" \
"v_mfma_f32_16x16x32_bf16 v[36:39], v[96:99], a[16:19], 0\n" \
"ds_read_b128 a[176:179], v20 offset:4096\n" \
"s_waitcnt lgkmcnt(12)\n" \
"v_xor_b32_e32 v108, 0x80008000, v108\n" \
"v_mfma_f32_16x16x32_bf16 v[40:43], v[96:99], a[32:35], 0\n" \
"v_xor_b32_e32 v109, 0x80008000, v109\n" \
"v_xor_b32_e32 v110, 0x80008000, v110\n" \
"v_mfma_f32_16x16x32_bf16 v[44:47], v[96:99], a[48:51], 0\n" \
"v_xor_b32_e32 v111, 0x80008000, v111\n" \
"v_add_u32_e32 v60, s15, v20\n" \
"v_mfma_f32_16x16x32_bf16 v[32:35], v[100:103], a[4:7], v[32:35]\n" \
"v_add_u32_e32 v60, 0x10000, v60\n" \
"ds_read_b128 a[128:131], v20 offset:8192\n" \
"v_mfma_f32_16x16x32_bf16 v[36:39], v[100:103], a[20:23], v[36:39]\n" \
"ds_read_b128 a[144:147], v20 offset:12288\n" \
"ds_read_b128 a[132:135], v20 offset:9216\n" \
"v_mfma_f32_16x16x32_bf16 v[40:43], v[100:103], a[36:39], v[40:43]\n" \
"ds_read_b128 a[148:151], v20 offset:13312\n" \
"ds_read_b128 a[136:139], v20 offset:10240\n" \
"v_mfma_f32_16x16x32_bf16 v[44:47], v[100:103], a[52:55], v[44:47]\n" \
"ds_read_b128 a[152:155], v20 offset:14336\n" \
"ds_read_b128 a[140:143], v20 offset:11264\n" \
"v_mfma_f32_16x16x32_bf16 v[32:35], v[104:107], a[8:11], v[32:35]\n" \
"ds_read_b128 a[156:159], v20 offset:15360\n" \
"ds_read_b128 a[64:67], v60 offset:16384\n" \
"v_mfma_f32_16x16x32_bf16 v[36:39], v[104:107], a[24:27], v[36:39]\n" \
"ds_read_b128 a[68:71], v60 offset:17408\n" \
"ds_read_b128 a[72:75], v60 offset:18432\n" \
"v_mfma_f32_16x16x32_bf16 v[40:43], v[104:107], a[40:43], v[40:43]\n" \
"ds_read_b128 a[76:79], v60 offset:19456\n" \
"ds_read_b128 a[80:83], v60 offset:24576\n" \
"v_mfma_f32_16x16x32_bf16 v[44:47], v[104:107], a[56:59], v[44:47]\n" \
"ds_read_b128 a[84:87], v60 offset:25600\n" \
"s_waitcnt lgkmcnt(15)\n" \
"v_mfma_f32_16x16x32_bf16 v[32:35], v[108:111], a[12:15], v[32:35]\n" \
"ds_read_b128 a[88:91], v60 offset:26624\n" \
"ds_read_b128 a[92:95], v60 offset:27648\n" \
"v_mfma_f32_16x16x32_bf16 v[36:39], v[108:111], a[28:31], v[36:39]\n" \
"ds_read_b128 a[96:99], v60 offset:0\n" \
"ds_read_b128 a[100:103], v60 offset:1024\n" \
"v_mfma_f32_16x16x32_bf16 v[40:43], v[108:111], a[44:47], v[40:43]\n" \
"ds_read_b128 a[104:107], v60 offset:2048\n" \
"ds_read_b128 a[108:111], v60 offset:3072\n" \
"v_mfma_f32_16x16x32_bf16 v[44:47], v[108:111], a[60:63], v[44:47]\n" \
"ds_read_b128 a[112:115], v60 offset:8192\n" \
"ds_read_b128 a[116:119], v60 offset:9216\n" \
"v_mfma_f32_16x16x32_bf16 v[64:67], a[160:163], a[0:3], v[32:35]\n" \
"ds_read_b128 a[120:123], v60 offset:10240\n" \
"ds_read_b128 a[124:127], v60 offset:11264\n" \
"v_mfma_f32_16x16x32_bf16 v[64:67], a[164:167], a[4:7], v[64:67]\n" \
"v_bfe_u32 v26, v20, 8, 2\n" \
"s_mov_b32 m0, s20\n" \
"v_mfma_f32_16x16x32_bf16 v[64:67], a[168:171], a[8:11], v[64:67]\n" \
"v_bfe_u32 v25, v20, 5, 3\n" \
"v_lshlrev_b32_e32 v26, 1, v26\n" \
"v_mfma_f32_16x16x32_bf16 v[64:67], a[172:175], a[12:15], v[64:67]\n" \
"global_load_lds_dword v[16:17], off\n" \
"v_mfma_f32_16x16x32_bf16 v[68:71], a[188:191], a[12:15], v[32:35]\n" \
"v_sub_u32_e32 v25, v25, v26\n" \
"v_and_b32_e32 v26, 16, v20\n" \
"v_mfma_f32_16x16x32_bf16 v[68:71], a[184:187], a[8:11], v[68:71]\n" \
"v_mov_b32_e32 v27, 0x3f80\n" \
"v_lshlrev_b32_e32 v26, v26, v27\n" \
"v_mfma_f32_16x16x32_bf16 v[68:71], a[180:183], a[4:7], v[68:71]\n" \
"v_mov_b32_e32 v54, 0x3f803f80\n" \
"s_waitcnt lgkmcnt(15)\n" \
"v_mfma_f32_16x16x32_bf16 v[68:71], a[176:179], a[0:3], v[68:71]\n" \
"s_mov_b32 s84, 0x33333333\n" \
"s_mov_b32 s85, 0xcccccccc\n" \
"v_mfma_f32_16x16x32_bf16 v[76:79], a[176:179], a[16:19], v[36:39]\n" \
"s_mov_b32 s86, 0x11111111\n" \
"s_mov_b32 s87, 0x11111111\n" \
"v_mfma_f32_16x16x32_bf16 v[76:79], a[180:183], a[20:23], v[76:79]\n" \
"s_mov_b32 s88, 0x44444444\n" \
"s_mov_b32 s89, 0x44444444\n" \
"v_mfma_f32_16x16x32_bf16 v[76:79], a[184:187], a[24:27], v[76:79]\n" \
"v_exp_f32_e32 v70, v70\n" \
"v_mfma_f32_16x16x32_bf16 v[76:79], a[188:191], a[28:31], v[76:79]\n" \
"v_exp_f32_e32 v71, v71\n" \
"v_mfma_f32_16x16x32_bf16 v[72:75], a[172:175], a[28:31], v[36:39]\n" \
"v_exp_f32_e32 v68, v68\n" \
"v_mfma_f32_16x16x32_bf16 v[72:75], a[168:171], a[24:27], v[72:75]\n" \
"v_exp_f32_e32 v69, v69\n" \
"v_mfma_f32_16x16x32_bf16 v[72:75], a[164:167], a[20:23], v[72:75]\n" \
"v_bfe_i32 v21, v20, 4, 1\n" \
"v_cvt_pk_bf16_f32 v71, v70, v71\n" \
"v_mfma_f32_16x16x32_bf16 v[72:75], a[160:163], a[16:19], v[72:75]\n" \
"v_exp_f32_e32 v66, v66\n" \
"v_mfma_f32_16x16x32_bf16 v[80:83], a[160:163], a[32:35], v[40:43]\n" \
"v_exp_f32_e32 v67, v67\n" \
"v_mfma_f32_16x16x32_bf16 v[80:83], a[164:167], a[36:39], v[80:83]\n" \
"v_exp_f32_e32 v64, v64\n" \
"v_mfma_f32_16x16x32_bf16 v[80:83], a[168:171], a[40:43], v[80:83]\n" \
"v_exp_f32_e32 v65, v65\n" \
"v_mfma_f32_16x16x32_bf16 v[80:83], a[172:175], a[44:47], v[80:83]\n" \
"v_exp_f32_e32 v72, v72\n" \
"v_mfma_f32_16x16x32_bf16 v[84:87], a[188:191], a[44:47], v[40:43]\n" \
"s_waitcnt vmcnt(3)\n" \
"v_cmp_ne_u32_e32 vcc, s62, v57\n" \
"v_mfma_f32_16x16x32_bf16 v[84:87], a[184:187], a[40:43], v[84:87]\n" \
"v_exp_f32_e32 v73, v73\n" \
"v_mfma_f32_16x16x32_bf16 v[84:87], a[180:183], a[36:39], v[84:87]\n" \
"v_exp_f32_e32 v74, v74\n" \
"v_mfma_f32_16x16x32_bf16 v[84:87], a[176:179], a[32:35], v[84:87]\n" \
"s_cbranch_vccnz L_skb_%=\n" \
"L_rkb_%=:\n" \
"v_cmp_eq_u32_e32 vcc, 0, v25\n" \
"v_mfma_f32_16x16x32_bf16 v[92:95], a[176:179], a[48:51], v[44:47]\n" \
"v_exp_f32_e32 v75, v75\n" \
"v_mfma_f32_16x16x32_bf16 v[92:95], a[180:183], a[52:55], v[92:95]\n" \
"v_cndmask_b32_e32 v28, 0, v26, vcc\n" \
"v_cvt_pk_bf16_f32 v72, v72, v73\n" \
"v_mfma_f32_16x16x32_bf16 v[92:95], a[184:187], a[56:59], v[92:95]\n" \
"v_exp_f32_e32 v76, v76\n" \
"v_mfma_f32_16x16x32_bf16 v[92:95], a[188:191], a[60:63], v[92:95]\n" \
"v_exp_f32_e32 v77, v77\n" \
"v_mfma_f32_16x16x32_bf16 v[88:91], a[172:175], a[60:63], v[44:47]\n" \
"v_exp_f32_e32 v78, v78\n" \
"v_mfma_f32_16x16x32_bf16 v[88:91], a[168:171], a[56:59], v[88:91]\n" \
"v_exp_f32_e32 v79, v79\n" \
"v_mfma_f32_16x16x32_bf16 v[88:91], a[164:167], a[52:55], v[88:91]\n" \
"v_cndmask_b32_e64 v22, 0, v54, s[84:85]\n" \
"v_cndmask_b32_e64 v23, 0, v54, s[86:87]\n" \
"v_mfma_f32_16x16x32_bf16 v[88:91], a[160:163], a[48:51], v[88:91]\n" \
"v_cndmask_b32_e64 v24, 0, v54, s[88:89]\n" \
"v_and_b32_e32 v21, 0xaaaaaaaa, v21\n" \
"s_waitcnt vmcnt(4) lgkmcnt(0)\n" \
"s_barrier\n" \
"v_cmp_eq_u32_e32 vcc, 1, v25\n" \
"v_cvt_pk_bf16_f32 v70, v68, v69\n" \
"v_cvt_pk_bf16_f32 v73, v74, v75\n" \
"v_mov_b32_e32 v62, 0\n" \
"v_accvgpr_write_b32 a208, 0\n" \
"v_accvgpr_write_b32 a209, 0\n" \
"v_accvgpr_write_b32 a210, 0\n" \
"v_accvgpr_write_b32 a211, 0\n" \
"v_accvgpr_write_b32 a200, v22\n" \
"v_accvgpr_write_b32 a201, v22\n" \
"v_accvgpr_write_b32 a202, v22\n" \
"v_accvgpr_write_b32 a203, v22\n" \
"v_accvgpr_write_b32 a204, v23\n" \
"v_accvgpr_write_b32 a205, v23\n" \
"v_accvgpr_write_b32 a206, v23\n" \
"v_accvgpr_write_b32 a207, v23\n" \
"v_accvgpr_write_b32 a212, v24\n" \
"v_accvgpr_write_b32 a213, v24\n" \
"v_accvgpr_write_b32 a214, v24\n" \
"v_accvgpr_write_b32 a215, v24\n" \
"v_or_b32_e32 v21, 0x44444444, v21\n" \
"s_mov_b32 s64, 0x8000\n" \
"v_add_u32_e32 v50, 0x6000, v55\n" \
"v_add_u32_e32 v51, 0xa000, v55\n" \
"v_add_u32_e32 v53, 0x10000, v55\n" \
"v_add_u32_e32 v48, 0xc000, v20\n" \
"v_add_u32_e32 v49, s17, v20\n" \
"v_cndmask_b32_e32 v29, 0, v26, vcc\n" \
"v_accvgpr_write_b32 a220, v96\n" \
"v_accvgpr_write_b32 a221, v97\n" \
"v_accvgpr_write_b32 a222, v98\n" \
"v_accvgpr_write_b32 a223, v99\n" \
"v_accvgpr_write_b32 a224, v100\n" \
"v_accvgpr_write_b32 a225, v101\n" \
"v_accvgpr_write_b32 a226, v102\n" \
"v_accvgpr_write_b32 a227, v103\n" \
"v_accvgpr_write_b32 a228, v104\n" \
"v_accvgpr_write_b32 a229, v105\n" \
"v_accvgpr_write_b32 a230, v106\n" \
"v_accvgpr_write_b32 a231, v107\n" \
"v_accvgpr_write_b32 a232, v108\n" \
"v_accvgpr_write_b32 a233, v109\n" \
"v_accvgpr_write_b32 a234, v110\n" \
"v_accvgpr_write_b32 a235, v111\n" \
"v_cvt_pk_bf16_f32 v69, v66, v67\n" \
"v_cvt_pk_bf16_f32 v68, v64, v65\n" \
"v_cvt_pk_bf16_f32 v74, v76, v77\n" \
"v_cvt_pk_bf16_f32 v75, v78, v79\n" \
"s_sub_u32 s66, s18, 1\n" \
"s_cbranch_scc1 L_tr_%=\n" \
".p2align 6\n" \
"L_1_%=:\n" \
"v_mfma_f32_16x16x32_bf16 v[96:99], a[128:131], a[0:3], v[32:35]\n" \
"s_xor_b32 m0, s65, 0x12000\n" \
"ds_read_b128 a[160:163], v49 offset:0\n" \
"v_mfma_f32_16x16x32_bf16 v[96:99], a[132:135], a[4:7], v[96:99]\n" \
"buffer_load_dwordx4 v50, s[8:11], s64 offen offset:0 sc1 lds\n" \
"v_mfma_f32_16x16x32_bf16 v[96:99], a[136:139], a[8:11], v[96:99]\n" \
"ds_read_b128 a[164:167], v49 offset:1024\n" \
"ds_read_b128 a[168:171], v49 offset:2048\n" \
"v_mfma_f32_16x16x32_bf16 v[96:99], a[140:143], a[12:15], v[96:99]\n" \
"ds_read_b128 a[172:175], v49 offset:3072\n" \
"ds_read_b128 a[176:179], v49 offset:4096\n" \
"v_mfma_f32_16x16x32_bf16 v[100:103], a[156:159], a[12:15], v[32:35]\n" \
"ds_read_b128 a[180:183], v49 offset:5120\n" \
"ds_read_b128 a[184:187], v49 offset:6144\n" \
"v_mfma_f32_16x16x32_bf16 v[100:103], a[152:155], a[8:11], v[100:103]\n" \
"v_exp_f32_e64 v86, v86\n" \
"v_mfma_f32_16x16x32_bf16 v[100:103], a[148:151], a[4:7], v[100:103]\n" \
"v_exp_f32_e64 v87, v87\n" \
"v_mfma_f32_16x16x32_bf16 v[100:103], a[144:147], a[0:3], v[100:103]\n" \
"ds_read_b128 a[188:191], v49 offset:7168\n" \
"v_cvt_pk_bf16_f32 v87, v86, v87\n" \
"v_mfma_f32_16x16x32_bf16 v[108:111], a[144:147], a[16:19], v[36:39]\n" \
"v_exp_f32_e64 v84, v84\n" \
"v_mfma_f32_16x16x32_bf16 v[108:111], a[148:151], a[20:23], v[108:111]\n" \
"v_exp_f32_e64 v85, v85\n" \
"v_mfma_f32_16x16x32_bf16 v[108:111], a[152:155], a[24:27], v[108:111]\n" \
"v_exp_f32_e64 v82, v82\n" \
"v_mfma_f32_16x16x32_bf16 v[108:111], a[156:159], a[28:31], v[108:111]\n" \
"v_exp_f32_e64 v83, v83\n" \
"v_mfma_f32_16x16x32_bf16 v[104:107], a[140:143], a[28:31], v[36:39]\n" \
"v_exp_f32_e64 v80, v80\n" \
"v_mfma_f32_16x16x32_bf16 v[104:107], a[136:139], a[24:27], v[104:107]\n" \
"v_exp_f32_e64 v81, v81\n" \
"v_mfma_f32_16x16x32_bf16 v[104:107], a[132:135], a[20:23], v[104:107]\n" \
"v_exp_f32_e64 v88, v88\n" \
"v_mfma_f32_16x16x32_bf16 v[104:107], a[128:131], a[16:19], v[104:107]\n" \
"buffer_load_dwordx4 v50, s[8:11], s64 offen offset:1024 sc1 lds\n" \
"v_mfma_f32_16x16x32_bf16 v[112:115], a[128:131], a[32:35], v[40:43]\n" \
"s_xor_b32 m0, s65, 0x6000\n" \
"v_cvt_pk_bf16_f32 v86, v84, v85\n" \
"v_mfma_f32_16x16x32_bf16 v[112:115], a[132:135], a[36:39], v[112:115]\n" \
"v_cvt_pk_bf16_f32 v85, v82, v83\n" \
"v_cvt_pk_bf16_f32 v84, v80, v81\n" \
"v_mfma_f32_16x16x32_bf16 v[112:115], a[136:139], a[40:43], v[112:115]\n" \
"v_exp_f32_e64 v89, v89\n" \
"v_mfma_f32_16x16x32_bf16 v[112:115], a[140:143], a[44:47], v[112:115]\n" \
"v_exp_f32_e64 v90, v90\n" \
"v_mfma_f32_16x16x32_bf16 v[116:119], a[156:159], a[44:47], v[40:43]\n" \
"v_exp_f32_e64 v91, v91\n" \
"v_mfma_f32_16x16x32_bf16 v[116:119], a[152:155], a[40:43], v[116:119]\n" \
"v_exp_f32_e64 v92, v92\n" \
"v_mfma_f32_16x16x32_bf16 v[116:119], a[148:151], a[36:39], v[116:119]\n" \
"v_exp_f32_e64 v93, v93\n" \
"v_mfma_f32_16x16x32_bf16 v[116:119], a[144:147], a[32:35], v[116:119]\n" \
"v_exp_f32_e64 v94, v94\n" \
"v_mfma_f32_16x16x32_bf16 v[124:127], a[144:147], a[48:51], v[44:47]\n" \
"v_cvt_pk_bf16_f32 v88, v88, v89\n" \
"v_cvt_pk_bf16_f32 v89, v90, v91\n" \
"v_mfma_f32_16x16x32_bf16 v[124:127], a[148:151], a[52:55], v[124:127]\n" \
"v_exp_f32_e64 v95, v95\n" \
"v_mfma_f32_16x16x32_bf16 v[124:127], a[152:155], a[56:59], v[124:127]\n" \
"v_cvt_pk_bf16_f32 v90, v92, v93\n" \
"v_cvt_pk_bf16_f32 v91, v94, v95\n" \
"v_mfma_f32_16x16x32_bf16 v[124:127], a[156:159], a[60:63], v[124:127]\n" \
"ds_read_b128 a[144:147], v48 offset:4096\n" \
"ds_read_b128 a[148:151], v48 offset:5120\n" \
"v_mfma_f32_16x16x32_bf16 v[120:123], a[140:143], a[60:63], v[44:47]\n" \
"ds_read_b128 a[152:155], v48 offset:6144\n" \
"ds_read_b128 a[140:143], v48 offset:3072\n" \
"v_mfma_f32_16x16x32_bf16 v[120:123], a[136:139], a[56:59], v[120:123]\n" \
"ds_read_b128 a[136:139], v48 offset:2048\n" \
"ds_read_b128 a[156:159], v48 offset:7168\n" \
"v_mfma_f32_16x16x32_bf16 v[120:123], a[132:135], a[52:55], v[120:123]\n" \
"buffer_load_dwordx4 v51, s[8:11], s64 offen offset:0 sc1 lds\n" \
"v_mfma_f32_16x16x32_bf16 v[120:123], a[128:131], a[48:51], v[120:123]\n" \
"ds_read_b128 a[128:131], v48 offset:0\n" \
"s_waitcnt lgkmcnt(7)\n" \
"v_mfma_f32_16x16x32_bf16 v[128:131], a[160:163], v[68:71], v[128:131]\n" \
"v_exp_f32_e32 v102, v102\n" \
"v_mfma_f32_16x16x32_bf16 v[160:163], a[160:163], v[72:75], v[160:163]\n" \
"v_exp_f32_e64 v103, v103\n" \
"v_mfma_f32_16x16x32_bf16 v[132:135], a[164:167], v[68:71], v[132:135]\n" \
"ds_read_b128 a[132:135], v48 offset:1024\n" \
"v_cvt_pk_bf16_f32 v103, v102, v103\n" \
"v_mfma_f32_16x16x32_bf16 v[164:167], a[164:167], v[72:75], v[164:167]\n" \
"v_exp_f32_e64 v100, v100\n" \
"v_mfma_f32_16x16x32_bf16 v[136:139], a[168:171], v[68:71], v[136:139]\n" \
"v_exp_f32_e64 v101, v101\n" \
"v_mfma_f32_16x16x32_bf16 v[168:171], a[168:171], v[72:75], v[168:171]\n" \
"v_exp_f32_e64 v98, v98\n" \
"v_mfma_f32_16x16x32_bf16 v[140:143], a[172:175], v[68:71], v[140:143]\n" \
"v_exp_f32_e64 v99, v99\n" \
"v_mfma_f32_16x16x32_bf16 v[172:175], a[172:175], v[72:75], v[172:175]\n" \
"v_exp_f32_e64 v96, v96\n" \
"v_mfma_f32_16x16x32_bf16 v[144:147], a[176:179], v[68:71], v[144:147]\n" \
"v_exp_f32_e64 v97, v97\n" \
"v_mfma_f32_16x16x32_bf16 v[176:179], a[176:179], v[72:75], v[176:179]\n" \
"v_exp_f32_e64 v104, v104\n" \
"v_mfma_f32_16x16x32_bf16 v[148:151], a[180:183], v[68:71], v[148:151]\n" \
"v_exp_f32_e64 v105, v105\n" \
"v_mfma_f32_16x16x32_bf16 v[180:183], a[180:183], v[72:75], v[180:183]\n" \
"v_cvt_pk_bf16_f32 v102, v100, v101\n" \
"v_cvt_pk_bf16_f32 v101, v98, v99\n" \
"v_mfma_f32_16x16x32_bf16 v[152:155], a[184:187], v[68:71], v[152:155]\n" \
"v_cvt_pk_bf16_f32 v100, v96, v97\n" \
"v_cvt_pk_bf16_f32 v104, v104, v105\n" \
"v_mfma_f32_16x16x32_bf16 v[184:187], a[184:187], v[72:75], v[184:187]\n" \
"buffer_load_dwordx4 v51, s[8:11], s64 offen offset:1024 sc1 lds\n" \
"v_mfma_f32_16x16x32_bf16 v[156:159], a[188:191], v[68:71], v[156:159]\n" \
"v_exp_f32_e64 v106, v106\n" \
"v_mfma_f32_16x16x32_bf16 v[188:191], a[188:191], v[72:75], v[188:191]\n" \
"v_exp_f32_e64 v107, v107\n" \
"v_smfmac_f32_16x16x64_bf16 a[192:195], a[200:203], v[68:75], v21\n" \
"v_exp_f32_e64 v108, v108\n" \
"v_mfma_f32_16x16x32_bf16 v[220:223], a[188:191], v[84:87], v[220:223]\n" \
"v_exp_f32_e64 v109, v109\n" \
"v_mfma_f32_16x16x32_bf16 v[252:255], a[188:191], v[88:91], v[252:255]\n" \
"v_exp_f32_e64 v110, v110\n" \
"v_mfma_f32_16x16x32_bf16 v[216:219], a[184:187], v[84:87], v[216:219]\n" \
"v_exp_f32_e64 v111, v111\n" \
"v_mfma_f32_16x16x32_bf16 v[248:251], a[184:187], v[88:91], v[248:251]\n" \
"v_exp_f32_e64 v118, v118\n" \
"v_mfma_f32_16x16x32_bf16 v[212:215], a[180:183], v[84:87], v[212:215]\n" \
"v_exp_f32_e64 v119, v119\n" \
"v_mfma_f32_16x16x32_bf16 v[244:247], a[180:183], v[88:91], v[244:247]\n" \
"v_cvt_pk_bf16_f32 v105, v106, v107\n" \
"v_cvt_pk_bf16_f32 v106, v108, v109\n" \
"v_mfma_f32_16x16x32_bf16 v[208:211], a[176:179], v[84:87], v[208:211]\n" \
"v_cvt_pk_bf16_f32 v107, v110, v111\n" \
"v_cvt_pk_bf16_f32 v119, v118, v119\n" \
"v_mfma_f32_16x16x32_bf16 v[240:243], a[176:179], v[88:91], v[240:243]\n" \
"v_exp_f32_e64 v116, v116\n" \
"v_mfma_f32_16x16x32_bf16 v[204:207], a[172:175], v[84:87], v[204:207]\n" \
"v_exp_f32_e64 v117, v117\n" \
"v_mfma_f32_16x16x32_bf16 v[236:239], a[172:175], v[88:91], v[236:239]\n" \
"v_exp_f32_e64 v114, v114\n" \
"v_mfma_f32_16x16x32_bf16 v[200:203], a[168:171], v[84:87], v[200:203]\n" \
"v_exp_f32_e64 v115, v115\n" \
"v_mfma_f32_16x16x32_bf16 v[232:235], a[168:171], v[88:91], v[232:235]\n" \
"v_exp_f32_e64 v112, v112\n" \
"v_mfma_f32_16x16x32_bf16 v[196:199], a[164:167], v[84:87], v[196:199]\n" \
"v_exp_f32_e64 v113, v113\n" \
"v_mfma_f32_16x16x32_bf16 v[228:231], a[164:167], v[88:91], v[228:231]\n" \
"v_exp_f32_e64 v120, v120\n" \
"v_mfma_f32_16x16x32_bf16 v[192:195], a[160:163], v[84:87], v[192:195]\n" \
"v_exp_f32_e64 v121, v121\n" \
"v_mfma_f32_16x16x32_bf16 v[224:227], a[160:163], v[88:91], v[224:227]\n" \
"v_cvt_pk_bf16_f32 v118, v116, v117\n" \
"v_cvt_pk_bf16_f32 v117, v114, v115\n" \
"v_smfmac_f32_16x16x64_bf16 a[196:199], a[200:203], v[84:91], v21\n" \
"v_cvt_pk_bf16_f32 v116, v112, v113\n" \
"v_cvt_pk_bf16_f32 v120, v120, v121\n" \
"s_waitcnt vmcnt(4) lgkmcnt(0)\n" \
"s_barrier\n" \
"v_mfma_f32_16x16x32_bf16 v[64:67], a[128:131], a[0:3], v[32:35]\n" \
"s_xor_b32 m0, s65, 0x14000\n" \
"ds_read_b128 a[160:163], v49 offset:8192\n" \
"v_mfma_f32_16x16x32_bf16 v[64:67], a[132:135], a[4:7], v[64:67]\n" \
"buffer_load_dwordx4 v52, s[8:11], s64 offen offset:0 sc1 lds\n" \
"v_mfma_f32_16x16x32_bf16 v[64:67], a[136:139], a[8:11], v[64:67]\n" \
"ds_read_b128 a[164:167], v49 offset:9216\n" \
"ds_read_b128 a[168:171], v49 offset:10240\n" \
"v_mfma_f32_16x16x32_bf16 v[64:67], a[140:143], a[12:15], v[64:67]\n" \
"ds_read_b128 a[172:175], v49 offset:11264\n" \
"ds_read_b128 a[176:179], v49 offset:12288\n" \
"v_mfma_f32_16x16x32_bf16 v[68:71], a[156:159], a[12:15], v[32:35]\n" \
"ds_read_b128 a[180:183], v49 offset:13312\n" \
"ds_read_b128 a[184:187], v49 offset:14336\n" \
"v_mfma_f32_16x16x32_bf16 v[68:71], a[152:155], a[8:11], v[68:71]\n" \
"v_exp_f32_e64 v122, v122\n" \
"v_mfma_f32_16x16x32_bf16 v[68:71], a[148:151], a[4:7], v[68:71]\n" \
"v_exp_f32_e64 v123, v123\n" \
"v_mfma_f32_16x16x32_bf16 v[68:71], a[144:147], a[0:3], v[68:71]\n" \
"ds_read_b128 a[188:191], v49 offset:15360\n" \
"v_cvt_pk_bf16_f32 v121, v122, v123\n" \
"v_mfma_f32_16x16x32_bf16 v[76:79], a[144:147], a[16:19], v[36:39]\n" \
"v_exp_f32_e64 v124, v124\n" \
"v_mfma_f32_16x16x32_bf16 v[76:79], a[148:151], a[20:23], v[76:79]\n" \
"v_exp_f32_e64 v125, v125\n" \
"v_mfma_f32_16x16x32_bf16 v[76:79], a[152:155], a[24:27], v[76:79]\n" \
"v_exp_f32_e64 v126, v126\n" \
"v_mfma_f32_16x16x32_bf16 v[76:79], a[156:159], a[28:31], v[76:79]\n" \
"v_exp_f32_e64 v127, v127\n" \
"v_mfma_f32_16x16x32_bf16 v[72:75], a[140:143], a[28:31], v[36:39]\n" \
"v_cvt_pk_bf16_f32 v122, v124, v125\n" \
"v_cvt_pk_bf16_f32 v123, v126, v127\n" \
"v_mfma_f32_16x16x32_bf16 v[72:75], a[136:139], a[24:27], v[72:75]\n" \
"v_exp_f32_e64 v70, v70\n" \
"v_mfma_f32_16x16x32_bf16 v[72:75], a[132:135], a[20:23], v[72:75]\n" \
"v_exp_f32_e64 v71, v71\n" \
"v_mfma_f32_16x16x32_bf16 v[72:75], a[128:131], a[16:19], v[72:75]\n" \
"buffer_load_dwordx4 v52, s[8:11], s64 offen offset:1024 sc1 lds\n" \
"v_mfma_f32_16x16x32_bf16 v[80:83], a[128:131], a[32:35], v[40:43]\n" \
"s_mov_b32 m0, s65\n" \
"v_cvt_pk_bf16_f32 v71, v70, v71\n" \
"v_mfma_f32_16x16x32_bf16 v[80:83], a[132:135], a[36:39], v[80:83]\n" \
"v_exp_f32_e32 v68, v68\n" \
"v_mfma_f32_16x16x32_bf16 v[80:83], a[136:139], a[40:43], v[80:83]\n" \
"v_exp_f32_e64 v69, v69\n" \
"v_mfma_f32_16x16x32_bf16 v[80:83], a[140:143], a[44:47], v[80:83]\n" \
"v_exp_f32_e64 v72, v72\n" \
"v_mfma_f32_16x16x32_bf16 v[84:87], a[156:159], a[44:47], v[40:43]\n" \
"v_exp_f32_e64 v73, v73\n" \
"v_mfma_f32_16x16x32_bf16 v[84:87], a[152:155], a[40:43], v[84:87]\n" \
"v_exp_f32_e64 v74, v74\n" \
"v_mfma_f32_16x16x32_bf16 v[84:87], a[148:151], a[36:39], v[84:87]\n" \
"v_exp_f32_e64 v75, v75\n" \
"v_mfma_f32_16x16x32_bf16 v[84:87], a[144:147], a[32:35], v[84:87]\n" \
"v_exp_f32_e64 v66, v66\n" \
"v_mfma_f32_16x16x32_bf16 v[92:95], a[144:147], a[48:51], v[44:47]\n" \
"ds_read_b128 a[144:147], v48 offset:12288\n" \
"v_cvt_pk_bf16_f32 v72, v72, v73\n" \
"v_mfma_f32_16x16x32_bf16 v[92:95], a[148:151], a[52:55], v[92:95]\n" \
"ds_read_b128 a[148:151], v48 offset:13312\n" \
"v_cvt_pk_bf16_f32 v70, v68, v69\n" \
"v_mfma_f32_16x16x32_bf16 v[92:95], a[152:155], a[56:59], v[92:95]\n" \
"ds_read_b128 a[152:155], v48 offset:14336\n" \
"v_cvt_pk_bf16_f32 v73, v74, v75\n" \
"v_mfma_f32_16x16x32_bf16 v[92:95], a[156:159], a[60:63], v[92:95]\n" \
"ds_read_b128 a[156:159], v48 offset:15360\n" \
"v_add_u32_e32 v49, 0x10000, v48\n" \
"v_mfma_f32_16x16x32_bf16 v[88:91], a[140:143], a[60:63], v[44:47]\n" \
"ds_read_b128 a[140:143], v48 offset:11264\n" \
"s_xor_b32 s65, s65, 0x4000\n" \
"v_mfma_f32_16x16x32_bf16 v[88:91], a[136:139], a[56:59], v[88:91]\n" \
"v_exp_f32_e64 v67, v67\n" \
"v_mfma_f32_16x16x32_bf16 v[88:91], a[132:135], a[52:55], v[88:91]\n" \
"buffer_load_dwordx4 v53, s[8:11], s64 offen offset:0 sc1 lds\n" \
"v_mfma_f32_16x16x32_bf16 v[88:91], a[128:131], a[48:51], v[88:91]\n" \
"ds_read_b128 a[128:131], v48 offset:8192\n" \
"s_waitcnt lgkmcnt(6)\n" \
"v_mfma_f32_16x16x32_bf16 v[128:131], a[160:163], v[100:103], v[128:131]\n" \
"ds_read_b128 a[132:135], v48 offset:9216\n" \
"ds_read_b128 a[136:139], v48 offset:10240\n" \
"v_mfma_f32_16x16x32_bf16 v[160:163], a[160:163], v[104:107], v[160:163]\n" \
"v_exp_f32_e32 v64, v64\n" \
"v_mfma_f32_16x16x32_bf16 v[132:135], a[164:167], v[100:103], v[132:135]\n" \
"v_exp_f32_e64 v65, v65\n" \
"v_mfma_f32_16x16x32_bf16 v[164:167], a[164:167], v[104:107], v[164:167]\n" \
"v_exp_f32_e64 v76, v76\n" \
"v_mfma_f32_16x16x32_bf16 v[136:139], a[168:171], v[100:103], v[136:139]\n" \
"v_exp_f32_e64 v77, v77\n" \
"v_mfma_f32_16x16x32_bf16 v[168:171], a[168:171], v[104:107], v[168:171]\n" \
"v_exp_f32_e64 v78, v78\n" \
"v_mfma_f32_16x16x32_bf16 v[140:143], a[172:175], v[100:103], v[140:143]\n" \
"v_exp_f32_e64 v79, v79\n" \
"v_mfma_f32_16x16x32_bf16 v[172:175], a[172:175], v[104:107], v[172:175]\n" \
"v_cvt_pk_bf16_f32 v69, v66, v67\n" \
"v_cvt_pk_bf16_f32 v68, v64, v65\n" \
"v_mfma_f32_16x16x32_bf16 v[144:147], a[176:179], v[100:103], v[144:147]\n" \
"v_cvt_pk_bf16_f32 v74, v76, v77\n" \
"v_cvt_pk_bf16_f32 v75, v78, v79\n" \
"v_mfma_f32_16x16x32_bf16 v[176:179], a[176:179], v[104:107], v[176:179]\n" \
"v_xor_b32_e32 v48, 0x4000, v48\n" \
"v_mfma_f32_16x16x32_bf16 v[148:151], a[180:183], v[100:103], v[148:151]\n" \
"v_mfma_f32_16x16x32_bf16 v[180:183], a[180:183], v[104:107], v[180:183]\n" \
"v_mfma_f32_16x16x32_bf16 v[152:155], a[184:187], v[100:103], v[152:155]\n" \
"v_mfma_f32_16x16x32_bf16 v[184:187], a[184:187], v[104:107], v[184:187]\n" \
"buffer_load_dwordx4 v53, s[8:11], s64 offen offset:1024 sc1 lds\n" \
"v_mfma_f32_16x16x32_bf16 v[156:159], a[188:191], v[100:103], v[156:159]\n" \
"s_add_u32 s64, s64, 0x8000\n" \
"s_sub_u32 s66, s66, 1\n" \
"s_nop 0\n" \
"v_mfma_f32_16x16x32_bf16 v[188:191], a[188:191], v[104:107], v[188:191]\n" \
"v_smfmac_f32_16x16x64_bf16 a[192:195], a[200:203], v[100:107], v21\n" \
"v_mfma_f32_16x16x32_bf16 v[220:223], a[188:191], v[116:119], v[220:223]\n" \
"v_mfma_f32_16x16x32_bf16 v[252:255], a[188:191], v[120:123], v[252:255]\n" \
"v_mfma_f32_16x16x32_bf16 v[216:219], a[184:187], v[116:119], v[216:219]\n" \
"v_mfma_f32_16x16x32_bf16 v[248:251], a[184:187], v[120:123], v[248:251]\n" \
"v_mfma_f32_16x16x32_bf16 v[212:215], a[180:183], v[116:119], v[212:215]\n" \
"v_mfma_f32_16x16x32_bf16 v[244:247], a[180:183], v[120:123], v[244:247]\n" \
"v_mfma_f32_16x16x32_bf16 v[208:211], a[176:179], v[116:119], v[208:211]\n" \
"v_mfma_f32_16x16x32_bf16 v[240:243], a[176:179], v[120:123], v[240:243]\n" \
"v_mfma_f32_16x16x32_bf16 v[204:207], a[172:175], v[116:119], v[204:207]\n" \
"v_mfma_f32_16x16x32_bf16 v[236:239], a[172:175], v[120:123], v[236:239]\n" \
"v_mfma_f32_16x16x32_bf16 v[200:203], a[168:171], v[116:119], v[200:203]\n" \
"v_mfma_f32_16x16x32_bf16 v[232:235], a[168:171], v[120:123], v[232:235]\n" \
"v_mfma_f32_16x16x32_bf16 v[196:199], a[164:167], v[116:119], v[196:199]\n" \
"v_mfma_f32_16x16x32_bf16 v[228:231], a[164:167], v[120:123], v[228:231]\n" \
"v_mfma_f32_16x16x32_bf16 v[192:195], a[160:163], v[116:119], v[192:195]\n" \
"v_mfma_f32_16x16x32_bf16 v[224:227], a[160:163], v[120:123], v[224:227]\n" \
"v_smfmac_f32_16x16x64_bf16 a[196:199], a[200:203], v[116:123], v21\n" \
"s_waitcnt vmcnt(4) lgkmcnt(0)\n" \
"s_barrier\n" \
"s_cbranch_scc0 L_1_%=\n" \
"L_tr_%=:\n" \
"s_cmp_lg_u32 s16, 0\n" \
"s_cbranch_scc1 L_sw_%=\n" \
"v_mfma_f32_16x16x32_bf16 v[96:99], a[128:131], a[0:3], v[32:35]\n" \
"v_mov_b32_e32 v56, s62\n" \
"v_readlane_b32 s68, v11, 0\n" \
"v_mfma_f32_16x16x32_bf16 v[96:99], a[132:135], a[4:7], v[96:99]\n" \
"s_xor_b32 m0, s65, 0x12000\n" \
"s_mov_b32 s65, s13\n" \
"v_mfma_f32_16x16x32_bf16 v[96:99], a[136:139], a[8:11], v[96:99]\n" \
"buffer_load_dwordx4 v50, s[8:11], s64 offen offset:0 sc1 lds\n" \
"v_mfma_f32_16x16x32_bf16 v[96:99], a[140:143], a[12:15], v[96:99]\n" \
"v_add_u32_e32 v63, s15, v20\n" \
"s_add_u32 s67, s14, 40960\n" \
"v_mfma_f32_16x16x32_bf16 v[100:103], a[156:159], a[12:15], v[32:35]\n" \
"buffer_load_dwordx4 v50, s[8:11], s64 offen offset:1024 sc1 lds\n" \
"s_mov_b64 exec, 1\n" \
"buffer_store_dword v56, off, s[56:59], s68 offset:0 sc1\n" \
"s_mov_b64 exec, -1\n" \
"v_mfma_f32_16x16x32_bf16 v[100:103], a[152:155], a[8:11], v[100:103]\n" \
"v_add_u32_e32 v50, 0x6000, v63\n" \
"s_xor_b32 m0, s65, 0x8000\n" \
"v_mfma_f32_16x16x32_bf16 v[100:103], a[148:151], a[4:7], v[100:103]\n" \
"ds_read_b128 a[160:163], v49 offset:0\n" \
"ds_read_b128 a[164:167], v49 offset:1024\n" \
"v_mfma_f32_16x16x32_bf16 v[100:103], a[144:147], a[0:3], v[100:103]\n" \
"ds_read_b128 a[168:171], v49 offset:2048\n" \
"ds_read_b128 a[172:175], v49 offset:3072\n" \
"v_mfma_f32_16x16x32_bf16 v[108:111], a[144:147], a[16:19], v[36:39]\n" \
"buffer_load_dwordx4 v50, s[8:11], s67 offen offset:0 sc1 lds\n" \
"v_mfma_f32_16x16x32_bf16 v[108:111], a[148:151], a[20:23], v[108:111]\n" \
"ds_read_b128 a[176:179], v49 offset:4096\n" \
"ds_read_b128 a[180:183], v49 offset:5120\n" \
"v_mfma_f32_16x16x32_bf16 v[108:111], a[152:155], a[24:27], v[108:111]\n" \
"ds_read_b128 a[184:187], v49 offset:6144\n" \
"ds_read_b128 a[188:191], v49 offset:7168\n" \
"v_mfma_f32_16x16x32_bf16 v[108:111], a[156:159], a[28:31], v[108:111]\n" \
"s_mov_b32 s64, s14\n" \
"v_add_u32_e32 v51, 0x12000, v63\n" \
"v_mfma_f32_16x16x32_bf16 v[104:107], a[140:143], a[28:31], v[36:39]\n" \
"buffer_load_dwordx4 v50, s[8:11], s67 offen offset:1024 sc1 lds\n" \
"v_mfma_f32_16x16x32_bf16 v[104:107], a[136:139], a[24:27], v[104:107]\n" \
"v_exp_f32_e32 v86, v86\n" \
"v_mfma_f32_16x16x32_bf16 v[104:107], a[132:135], a[20:23], v[104:107]\n" \
"v_exp_f32_e32 v87, v87\n" \
"v_mfma_f32_16x16x32_bf16 v[104:107], a[128:131], a[16:19], v[104:107]\n" \
"v_exp_f32_e32 v84, v84\n" \
"v_mfma_f32_16x16x32_bf16 v[112:115], a[128:131], a[32:35], v[40:43]\n" \
"v_exp_f32_e32 v85, v85\n" \
"v_mfma_f32_16x16x32_bf16 v[112:115], a[132:135], a[36:39], v[112:115]\n" \
"buffer_load_dwordx4 v50, s[8:11], s67 offen offset:2048 sc1 lds\n" \
"v_mfma_f32_16x16x32_bf16 v[112:115], a[136:139], a[40:43], v[112:115]\n" \
"v_cvt_pk_bf16_f32 v87, v86, v87\n" \
"v_cvt_pk_bf16_f32 v86, v84, v85\n" \
"v_mfma_f32_16x16x32_bf16 v[112:115], a[140:143], a[44:47], v[112:115]\n" \
"v_exp_f32_e32 v82, v82\n" \
"v_mfma_f32_16x16x32_bf16 v[116:119], a[156:159], a[44:47], v[40:43]\n" \
"v_exp_f32_e32 v83, v83\n" \
"v_mfma_f32_16x16x32_bf16 v[116:119], a[152:155], a[40:43], v[116:119]\n" \
"buffer_load_dwordx4 v50, s[8:11], s67 offen offset:3072 sc1 lds\n" \
"v_mfma_f32_16x16x32_bf16 v[116:119], a[148:151], a[36:39], v[116:119]\n" \
"s_xor_b32 m0, s65, 0xa000\n" \
"v_cvt_pk_bf16_f32 v85, v82, v83\n" \
"v_mfma_f32_16x16x32_bf16 v[116:119], a[144:147], a[32:35], v[116:119]\n" \
"v_exp_f32_e32 v80, v80\n" \
"v_mfma_f32_16x16x32_bf16 v[124:127], a[144:147], a[48:51], v[44:47]\n" \
"v_exp_f32_e32 v81, v81\n" \
"v_mfma_f32_16x16x32_bf16 v[124:127], a[148:151], a[52:55], v[124:127]\n" \
"buffer_load_dwordx4 v51, s[8:11], s64 offen offset:0 sc1 lds\n" \
"v_mfma_f32_16x16x32_bf16 v[124:127], a[152:155], a[56:59], v[124:127]\n" \
"v_cvt_pk_bf16_f32 v84, v80, v81\n" \
"v_readlane_b32 s93, v11, 19\n" \
"v_mfma_f32_16x16x32_bf16 v[124:127], a[156:159], a[60:63], v[124:127]\n" \
"v_exp_f32_e32 v88, v88\n" \
"v_mfma_f32_16x16x32_bf16 v[120:123], a[140:143], a[60:63], v[44:47]\n" \
"v_exp_f32_e32 v89, v89\n" \
"v_mfma_f32_16x16x32_bf16 v[120:123], a[136:139], a[56:59], v[120:123]\n" \
"v_exp_f32_e32 v90, v90\n" \
"v_mfma_f32_16x16x32_bf16 v[120:123], a[132:135], a[52:55], v[120:123]\n" \
"buffer_load_dwordx4 v51, s[8:11], s64 offen offset:1024 sc1 lds\n" \
"v_mfma_f32_16x16x32_bf16 v[120:123], a[128:131], a[48:51], v[120:123]\n" \
"v_cvt_pk_bf16_f32 v88, v88, v89\n" \
"s_waitcnt lgkmcnt(0)\n" \
"v_mfma_f32_16x16x32_bf16 v[128:131], a[160:163], v[68:71], v[128:131]\n" \
"v_exp_f32_e32 v91, v91\n" \
"v_mfma_f32_16x16x32_bf16 v[160:163], a[160:163], v[72:75], v[160:163]\n" \
"v_exp_f32_e32 v92, v92\n" \
"v_mfma_f32_16x16x32_bf16 v[132:135], a[164:167], v[68:71], v[132:135]\n" \
"buffer_load_dwordx4 v51, s[8:11], s64 offen offset:2048 sc1 lds\n" \
"v_mfma_f32_16x16x32_bf16 v[164:167], a[164:167], v[72:75], v[164:167]\n" \
"v_exp_f32_e32 v93, v93\n" \
"v_mfma_f32_16x16x32_bf16 v[136:139], a[168:171], v[68:71], v[136:139]\n" \
"v_exp_f32_e32 v94, v94\n" \
"v_mfma_f32_16x16x32_bf16 v[168:171], a[168:171], v[72:75], v[168:171]\n" \
"v_exp_f32_e32 v95, v95\n" \
"v_mfma_f32_16x16x32_bf16 v[140:143], a[172:175], v[68:71], v[140:143]\n" \
"buffer_load_dwordx4 v51, s[8:11], s64 offen offset:3072 sc1 lds\n" \
"v_mfma_f32_16x16x32_bf16 v[172:175], a[172:175], v[72:75], v[172:175]\n" \
"v_cvt_pk_bf16_f32 v89, v90, v91\n" \
"v_cvt_pk_bf16_f32 v90, v92, v93\n" \
"v_mfma_f32_16x16x32_bf16 v[144:147], a[176:179], v[68:71], v[144:147]\n" \
"v_cvt_pk_bf16_f32 v91, v94, v95\n" \
"v_readlane_b32 s80, v11, 11\n" \
"v_mfma_f32_16x16x32_bf16 v[176:179], a[176:179], v[72:75], v[176:179]\n" \
"v_readlane_b32 s81, v11, 12\n" \
"s_mov_b32 s82, 0x47fca0\n" \
"v_mfma_f32_16x16x32_bf16 v[148:151], a[180:183], v[68:71], v[148:151]\n" \
"s_mov_b32 s83, 0x20000\n" \
"v_readlane_b32 s92, v11, 25\n" \
"v_mfma_f32_16x16x32_bf16 v[180:183], a[180:183], v[72:75], v[180:183]\n" \
"s_add_u32 m0, s93, 0x0\n" \
"ds_read_b128 a[128:131], v48 offset:0\n" \
"v_mfma_f32_16x16x32_bf16 v[152:155], a[184:187], v[68:71], v[152:155]\n" \
"ds_read_b128 a[144:147], v48 offset:4096\n" \
"ds_read_b128 a[132:135], v48 offset:1024\n" \
"v_mfma_f32_16x16x32_bf16 v[184:187], a[184:187], v[72:75], v[184:187]\n" \
"buffer_load_dwordx4 v0, s[80:83], s92 offen offset:0 lds\n" \
"v_mfma_f32_16x16x32_bf16 v[156:159], a[188:191], v[68:71], v[156:159]\n" \
"ds_read_b128 a[148:151], v48 offset:5120\n" \
"ds_read_b128 a[136:139], v48 offset:2048\n" \
"v_mfma_f32_16x16x32_bf16 v[188:191], a[188:191], v[72:75], v[188:191]\n" \
"ds_read_b128 a[152:155], v48 offset:6144\n" \
"ds_read_b128 a[140:143], v48 offset:3072\n" \
"v_smfmac_f32_16x16x64_bf16 a[192:195], a[200:203], v[68:75], v21\n" \
"ds_read_b128 a[156:159], v48 offset:7168\n" \
"v_add_u32_e32 v52, 0x14000, v63\n" \
"v_mfma_f32_16x16x32_bf16 v[220:223], a[188:191], v[84:87], v[220:223]\n" \
"buffer_load_dwordx4 v1, s[80:83], s92 offen offset:1024 lds\n" \
"v_mfma_f32_16x16x32_bf16 v[252:255], a[188:191], v[88:91], v[252:255]\n" \
"v_add_u32_e32 v53, 0x20000, v63\n" \
"s_mov_b32 s66, s19\n" \
"v_mfma_f32_16x16x32_bf16 v[216:219], a[184:187], v[84:87], v[216:219]\n" \
"buffer_load_dwordx4 v2, s[80:83], s92 offen offset:2048 lds\n" \
"v_mfma_f32_16x16x32_bf16 v[248:251], a[184:187], v[88:91], v[248:251]\n" \
"v_exp_f32_e32 v102, v102\n" \
"v_mfma_f32_16x16x32_bf16 v[212:215], a[180:183], v[84:87], v[212:215]\n" \
"v_exp_f32_e32 v103, v103\n" \
"v_mfma_f32_16x16x32_bf16 v[244:247], a[180:183], v[88:91], v[244:247]\n" \
"v_exp_f32_e32 v100, v100\n" \
"v_mfma_f32_16x16x32_bf16 v[208:211], a[176:179], v[84:87], v[208:211]\n" \
"buffer_load_dwordx4 v3, s[80:83], s92 offen offset:3072 lds\n" \
"v_mfma_f32_16x16x32_bf16 v[240:243], a[176:179], v[88:91], v[240:243]\n" \
"v_exp_f32_e32 v101, v101\n" \
"v_mfma_f32_16x16x32_bf16 v[204:207], a[172:175], v[84:87], v[204:207]\n" \
"v_exp_f32_e32 v98, v98\n" \
"v_mfma_f32_16x16x32_bf16 v[236:239], a[172:175], v[88:91], v[236:239]\n" \
"v_exp_f32_e32 v99, v99\n" \
"v_mfma_f32_16x16x32_bf16 v[200:203], a[168:171], v[84:87], v[200:203]\n" \
"v_exp_f32_e32 v96, v96\n" \
"v_mfma_f32_16x16x32_bf16 v[232:235], a[168:171], v[88:91], v[232:235]\n" \
"v_exp_f32_e32 v97, v97\n" \
"v_mfma_f32_16x16x32_bf16 v[196:199], a[164:167], v[84:87], v[196:199]\n" \
"v_exp_f32_e32 v104, v104\n" \
"v_mfma_f32_16x16x32_bf16 v[228:231], a[164:167], v[88:91], v[228:231]\n" \
"v_exp_f32_e32 v105, v105\n" \
"v_mfma_f32_16x16x32_bf16 v[192:195], a[160:163], v[84:87], v[192:195]\n" \
"v_cvt_pk_bf16_f32 v103, v102, v103\n" \
"v_cvt_pk_bf16_f32 v102, v100, v101\n" \
"v_mfma_f32_16x16x32_bf16 v[224:227], a[160:163], v[88:91], v[224:227]\n" \
"v_cvt_pk_bf16_f32 v101, v98, v99\n" \
"v_cvt_pk_bf16_f32 v100, v96, v97\n" \
"v_smfmac_f32_16x16x64_bf16 a[196:199], a[200:203], v[84:91], v21\n" \
"v_exp_f32_e32 v106, v106\n" \
"s_waitcnt vmcnt(15) lgkmcnt(0)\n" \
"s_barrier\n" \
"v_mfma_f32_16x16x32_bf16 v[64:67], a[128:131], a[0:3], v[32:35]\n" \
"s_xor_b32 m0, s65, 0x18000\n" \
"ds_read_b128 a[160:163], v49 offset:8192\n" \
"v_mfma_f32_16x16x32_bf16 v[64:67], a[132:135], a[4:7], v[64:67]\n" \
"buffer_load_dwordx4 v52, s[8:11], s64 offen offset:0 sc1 lds\n" \
"v_mfma_f32_16x16x32_bf16 v[64:67], a[136:139], a[8:11], v[64:67]\n" \
"ds_read_b128 a[164:167], v49 offset:9216\n" \
"ds_read_b128 a[168:171], v49 offset:10240\n" \
"v_mfma_f32_16x16x32_bf16 v[64:67], a[140:143], a[12:15], v[64:67]\n" \
"ds_read_b128 a[172:175], v49 offset:11264\n" \
"ds_read_b128 a[176:179], v49 offset:12288\n" \
"v_mfma_f32_16x16x32_bf16 v[68:71], a[156:159], a[12:15], v[32:35]\n" \
"buffer_load_dwordx4 v52, s[8:11], s64 offen offset:1024 sc1 lds\n" \
"v_mfma_f32_16x16x32_bf16 v[68:71], a[152:155], a[8:11], v[68:71]\n" \
"ds_read_b128 a[180:183], v49 offset:13312\n" \
"ds_read_b128 a[184:187], v49 offset:14336\n" \
"v_mfma_f32_16x16x32_bf16 v[68:71], a[148:151], a[4:7], v[68:71]\n" \
"v_exp_f32_e32 v107, v107\n" \
"v_mfma_f32_16x16x32_bf16 v[68:71], a[144:147], a[0:3], v[68:71]\n" \
"v_cvt_pk_bf16_f32 v104, v104, v105\n" \
"v_cvt_pk_bf16_f32 v105, v106, v107\n" \
"v_mfma_f32_16x16x32_bf16 v[76:79], a[144:147], a[16:19], v[36:39]\n" \
"v_exp_f32_e32 v108, v108\n" \
"v_mfma_f32_16x16x32_bf16 v[76:79], a[148:151], a[20:23], v[76:79]\n" \
"buffer_load_dwordx4 v52, s[8:11], s64 offen offset:2048 sc1 lds\n" \
"v_mfma_f32_16x16x32_bf16 v[76:79], a[152:155], a[24:27], v[76:79]\n" \
"v_exp_f32_e32 v109, v109\n" \
"v_mfma_f32_16x16x32_bf16 v[76:79], a[156:159], a[28:31], v[76:79]\n" \
"v_exp_f32_e32 v110, v110\n" \
"v_mfma_f32_16x16x32_bf16 v[72:75], a[140:143], a[28:31], v[36:39]\n" \
"v_exp_f32_e32 v111, v111\n" \
"v_mfma_f32_16x16x32_bf16 v[72:75], a[136:139], a[24:27], v[72:75]\n" \
"buffer_load_dwordx4 v52, s[8:11], s64 offen offset:3072 sc1 lds\n" \
"v_mfma_f32_16x16x32_bf16 v[72:75], a[132:135], a[20:23], v[72:75]\n" \
"s_mov_b32 m0, s65\n" \
"v_cvt_pk_bf16_f32 v106, v108, v109\n" \
"v_mfma_f32_16x16x32_bf16 v[72:75], a[128:131], a[16:19], v[72:75]\n" \
"v_cvt_pk_bf16_f32 v107, v110, v111\n" \
"ds_read_b128 a[188:191], v49 offset:15360\n" \
"v_mfma_f32_16x16x32_bf16 v[80:83], a[128:131], a[32:35], v[40:43]\n" \
"v_exp_f32_e32 v118, v118\n" \
"v_mfma_f32_16x16x32_bf16 v[80:83], a[132:135], a[36:39], v[80:83]\n" \
"v_exp_f32_e32 v119, v119\n" \
"v_mfma_f32_16x16x32_bf16 v[80:83], a[136:139], a[40:43], v[80:83]\n" \
"buffer_load_dwordx4 v53, s[8:11], s64 offen offset:0 sc1 lds\n" \
"v_mfma_f32_16x16x32_bf16 v[80:83], a[140:143], a[44:47], v[80:83]\n" \
"v_exp_f32_e32 v116, v116\n" \
"v_mfma_f32_16x16x32_bf16 v[84:87], a[156:159], a[44:47], v[40:43]\n" \
"v_exp_f32_e32 v117, v117\n" \
"v_mfma_f32_16x16x32_bf16 v[84:87], a[152:155], a[40:43], v[84:87]\n" \
"v_cvt_pk_bf16_f32 v119, v118, v119\n" \
"v_cvt_pk_bf16_f32 v118, v116, v117\n" \
"v_mfma_f32_16x16x32_bf16 v[84:87], a[148:151], a[36:39], v[84:87]\n" \
"v_exp_f32_e32 v114, v114\n" \
"v_mfma_f32_16x16x32_bf16 v[84:87], a[144:147], a[32:35], v[84:87]\n" \
"buffer_load_dwordx4 v53, s[8:11], s64 offen offset:1024 sc1 lds\n" \
"v_mfma_f32_16x16x32_bf16 v[92:95], a[144:147], a[48:51], v[44:47]\n" \
"v_exp_f32_e32 v115, v115\n" \
"v_mfma_f32_16x16x32_bf16 v[92:95], a[148:151], a[52:55], v[92:95]\n" \
"v_exp_f32_e32 v112, v112\n" \
"v_mfma_f32_16x16x32_bf16 v[92:95], a[152:155], a[56:59], v[92:95]\n" \
"v_exp_f32_e32 v113, v113\n" \
"v_mfma_f32_16x16x32_bf16 v[92:95], a[156:159], a[60:63], v[92:95]\n" \
"buffer_load_dwordx4 v53, s[8:11], s64 offen offset:2048 sc1 lds\n" \
"v_mfma_f32_16x16x32_bf16 v[88:91], a[140:143], a[60:63], v[44:47]\n" \
"v_cvt_pk_bf16_f32 v117, v114, v115\n" \
"v_cvt_pk_bf16_f32 v116, v112, v113\n" \
"v_mfma_f32_16x16x32_bf16 v[88:91], a[136:139], a[56:59], v[88:91]\n" \
"v_exp_f32_e32 v120, v120\n" \
"v_mfma_f32_16x16x32_bf16 v[88:91], a[132:135], a[52:55], v[88:91]\n" \
"v_exp_f32_e32 v121, v121\n" \
"v_mfma_f32_16x16x32_bf16 v[88:91], a[128:131], a[48:51], v[88:91]\n" \
"v_cvt_pk_bf16_f32 v120, v120, v121\n" \
"s_waitcnt lgkmcnt(0)\n" \
"v_mfma_f32_16x16x32_bf16 v[128:131], a[160:163], v[100:103], v[128:131]\n" \
"buffer_load_dwordx4 v53, s[8:11], s64 offen offset:3072 sc1 lds\n" \
"v_mfma_f32_16x16x32_bf16 v[160:163], a[160:163], v[104:107], v[160:163]\n" \
"s_add_u32 m0, s93, 0x1000\n" \
"ds_read_b128 a[128:131], v48 offset:8192\n" \
"v_mfma_f32_16x16x32_bf16 v[132:135], a[164:167], v[100:103], v[132:135]\n" \
"v_exp_f32_e32 v122, v122\n" \
"v_mfma_f32_16x16x32_bf16 v[164:167], a[164:167], v[104:107], v[164:167]\n" \
"v_exp_f32_e32 v123, v123\n" \
"v_mfma_f32_16x16x32_bf16 v[136:139], a[168:171], v[100:103], v[136:139]\n" \
"buffer_load_dwordx4 v4, s[80:83], s92 offen offset:0 lds\n" \
"v_mfma_f32_16x16x32_bf16 v[168:171], a[168:171], v[104:107], v[168:171]\n" \
"v_exp_f32_e32 v124, v124\n" \
"v_mfma_f32_16x16x32_bf16 v[140:143], a[172:175], v[100:103], v[140:143]\n" \
"v_exp_f32_e32 v125, v125\n" \
"v_mfma_f32_16x16x32_bf16 v[172:175], a[172:175], v[104:107], v[172:175]\n" \
"v_exp_f32_e32 v126, v126\n" \
"v_mfma_f32_16x16x32_bf16 v[144:147], a[176:179], v[100:103], v[144:147]\n" \
"v_exp_f32_e32 v127, v127\n" \
"v_mfma_f32_16x16x32_bf16 v[176:179], a[176:179], v[104:107], v[176:179]\n" \
"v_cvt_pk_bf16_f32 v121, v122, v123\n" \
"v_cvt_pk_bf16_f32 v122, v124, v125\n" \
"v_mfma_f32_16x16x32_bf16 v[148:151], a[180:183], v[100:103], v[148:151]\n" \
"buffer_load_dwordx4 v5, s[80:83], s92 offen offset:1024 lds\n" \
"v_mfma_f32_16x16x32_bf16 v[180:183], a[180:183], v[104:107], v[180:183]\n" \
"v_cvt_pk_bf16_f32 v123, v126, v127\n" \
"ds_read_b128 a[144:147], v48 offset:12288\n" \
"v_mfma_f32_16x16x32_bf16 v[152:155], a[184:187], v[100:103], v[152:155]\n" \
"ds_read_b128 a[132:135], v48 offset:9216\n" \
"ds_read_b128 a[148:151], v48 offset:13312\n" \
"v_mfma_f32_16x16x32_bf16 v[184:187], a[184:187], v[104:107], v[184:187]\n" \
"ds_read_b128 a[136:139], v48 offset:10240\n" \
"ds_read_b128 a[152:155], v48 offset:14336\n" \
"v_mfma_f32_16x16x32_bf16 v[156:159], a[188:191], v[100:103], v[156:159]\n" \
"ds_read_b128 a[140:143], v48 offset:11264\n" \
"ds_read_b128 a[156:159], v48 offset:15360\n" \
"v_mfma_f32_16x16x32_bf16 v[188:191], a[188:191], v[104:107], v[188:191]\n" \
"buffer_load_dwordx4 v6, s[80:83], s92 offen offset:2048 lds\n" \
"v_smfmac_f32_16x16x64_bf16 a[192:195], a[200:203], v[100:107], v21\n" \
"v_exp_f32_e32 v70, v70\n" \
"v_mfma_f32_16x16x32_bf16 v[220:223], a[188:191], v[116:119], v[220:223]\n" \
"v_exp_f32_e32 v71, v71\n" \
"v_mfma_f32_16x16x32_bf16 v[252:255], a[188:191], v[120:123], v[252:255]\n" \
"buffer_load_dwordx4 v7, s[80:83], s92 offen offset:3072 lds\n" \
"v_mfma_f32_16x16x32_bf16 v[216:219], a[184:187], v[116:119], v[216:219]\n" \
"s_add_u32 m0, s93, 0x2000\n" \
"v_cvt_pk_bf16_f32 v71, v70, v71\n" \
"v_mfma_f32_16x16x32_bf16 v[248:251], a[184:187], v[120:123], v[248:251]\n" \
"v_exp_f32_e32 v72, v72\n" \
"v_mfma_f32_16x16x32_bf16 v[212:215], a[180:183], v[116:119], v[212:215]\n" \
"v_exp_f32_e32 v73, v73\n" \
"v_mfma_f32_16x16x32_bf16 v[244:247], a[180:183], v[120:123], v[244:247]\n" \
"v_exp_f32_e32 v68, v68\n" \
"v_mfma_f32_16x16x32_bf16 v[208:211], a[176:179], v[116:119], v[208:211]\n" \
"buffer_load_dwordx4 v8, s[80:83], s92 offen offset:0 lds\n" \
"v_mfma_f32_16x16x32_bf16 v[240:243], a[176:179], v[120:123], v[240:243]\n" \
"v_exp_f32_e32 v69, v69\n" \
"v_mfma_f32_16x16x32_bf16 v[204:207], a[172:175], v[116:119], v[204:207]\n" \
"v_exp_f32_e32 v74, v74\n" \
"v_mfma_f32_16x16x32_bf16 v[236:239], a[172:175], v[120:123], v[236:239]\n" \
"v_exp_f32_e32 v75, v75\n" \
"v_mfma_f32_16x16x32_bf16 v[200:203], a[168:171], v[116:119], v[200:203]\n" \
"v_exp_f32_e32 v66, v66\n" \
"v_mfma_f32_16x16x32_bf16 v[232:235], a[168:171], v[120:123], v[232:235]\n" \
"v_exp_f32_e32 v67, v67\n" \
"v_mfma_f32_16x16x32_bf16 v[196:199], a[164:167], v[116:119], v[196:199]\n" \
"v_exp_f32_e32 v64, v64\n" \
"v_mfma_f32_16x16x32_bf16 v[228:231], a[164:167], v[120:123], v[228:231]\n" \
"v_exp_f32_e32 v65, v65\n" \
"v_mfma_f32_16x16x32_bf16 v[192:195], a[160:163], v[116:119], v[192:195]\n" \
"v_cvt_pk_bf16_f32 v72, v72, v73\n" \
"v_cvt_pk_bf16_f32 v70, v68, v69\n" \
"v_mfma_f32_16x16x32_bf16 v[224:227], a[160:163], v[120:123], v[224:227]\n" \
"v_exp_f32_e32 v76, v76\n" \
"v_smfmac_f32_16x16x64_bf16 a[196:199], a[200:203], v[116:123], v21\n" \
"v_exp_f32_e32 v77, v77\n" \
"s_waitcnt vmcnt(13) lgkmcnt(0)\n" \
"s_barrier\n" \
"v_exp_f32_e32 v78, v78\n" \
"v_exp_f32_e32 v79, v79\n" \
"v_cvt_pk_bf16_f32 v73, v74, v75\n" \
"v_add_u32_e32 v49, 0x10000, v48\n" \
"v_cvt_pk_bf16_f32 v69, v66, v67\n" \
"v_cvt_pk_bf16_f32 v68, v64, v65\n" \
"v_cvt_pk_bf16_f32 v74, v76, v77\n" \
"v_cvt_pk_bf16_f32 v75, v78, v79\n" \
"v_add_u32_e32 v48, s16, v20\n" \
"s_xor_b32 s65, s65, 0x8000\n" \
"s_add_u32 s64, s64, 0x10000\n" \
".p2align 6\n" \
"L_2_%=:\n" \
"v_mfma_f32_16x16x32_bf16 v[96:99], a[128:131], a[64:67], v[32:35]\n" \
"s_xor_b32 m0, s65, 0x12000\n" \
"ds_read_b128 a[160:163], v49 offset:0\n" \
"v_mfma_f32_16x16x32_bf16 v[96:99], a[132:135], a[68:71], v[96:99]\n" \
"buffer_load_dwordx4 v50, s[8:11], s64 offen offset:0 sc1 lds\n" \
"v_mfma_f32_16x16x32_bf16 v[96:99], a[136:139], a[72:75], v[96:99]\n" \
"ds_read_b128 a[164:167], v49 offset:1024\n" \
"ds_read_b128 a[168:171], v49 offset:2048\n" \
"v_mfma_f32_16x16x32_bf16 v[96:99], a[140:143], a[76:79], v[96:99]\n" \
"ds_read_b128 a[172:175], v49 offset:3072\n" \
"ds_read_b128 a[176:179], v49 offset:4096\n" \
"v_mfma_f32_16x16x32_bf16 v[100:103], a[156:159], a[76:79], v[32:35]\n" \
"ds_read_b128 a[180:183], v49 offset:5120\n" \
"ds_read_b128 a[184:187], v49 offset:6144\n" \
"v_mfma_f32_16x16x32_bf16 v[100:103], a[152:155], a[72:75], v[100:103]\n" \
"v_exp_f32_e64 v86, v86\n" \
"v_mfma_f32_16x16x32_bf16 v[100:103], a[148:151], a[68:71], v[100:103]\n" \
"v_exp_f32_e64 v87, v87\n" \
"v_mfma_f32_16x16x32_bf16 v[100:103], a[144:147], a[64:67], v[100:103]\n" \
"buffer_load_dwordx4 v50, s[8:11], s64 offen offset:1024 sc1 lds\n" \
"v_mfma_f32_16x16x32_bf16 v[108:111], a[144:147], a[80:83], v[36:39]\n" \
"ds_read_b128 a[188:191], v49 offset:7168\n" \
"v_cvt_pk_bf16_f32 v87, v86, v87\n" \
"v_mfma_f32_16x16x32_bf16 v[108:111], a[148:151], a[84:87], v[108:111]\n" \
"v_exp_f32_e64 v84, v84\n" \
"v_mfma_f32_16x16x32_bf16 v[108:111], a[152:155], a[88:91], v[108:111]\n" \
"v_exp_f32_e64 v85, v85\n" \
"v_mfma_f32_16x16x32_bf16 v[108:111], a[156:159], a[92:95], v[108:111]\n" \
"v_exp_f32_e64 v82, v82\n" \
"v_mfma_f32_16x16x32_bf16 v[104:107], a[140:143], a[92:95], v[36:39]\n" \
"v_exp_f32_e64 v83, v83\n" \
"v_mfma_f32_16x16x32_bf16 v[104:107], a[136:139], a[88:91], v[104:107]\n" \
"v_exp_f32_e64 v80, v80\n" \
"v_mfma_f32_16x16x32_bf16 v[104:107], a[132:135], a[84:87], v[104:107]\n" \
"v_exp_f32_e64 v81, v81\n" \
"v_mfma_f32_16x16x32_bf16 v[104:107], a[128:131], a[80:83], v[104:107]\n" \
"buffer_load_dwordx4 v50, s[8:11], s64 offen offset:2048 sc1 lds\n" \
"v_mfma_f32_16x16x32_bf16 v[112:115], a[128:131], a[96:99], v[40:43]\n" \
"v_exp_f32_e64 v88, v88\n" \
"v_mfma_f32_16x16x32_bf16 v[112:115], a[132:135], a[100:103], v[112:115]\n" \
"v_exp_f32_e64 v89, v89\n" \
"v_mfma_f32_16x16x32_bf16 v[112:115], a[136:139], a[104:107], v[112:115]\n" \
"v_cvt_pk_bf16_f32 v86, v84, v85\n" \
"v_cvt_pk_bf16_f32 v85, v82, v83\n" \
"v_mfma_f32_16x16x32_bf16 v[112:115], a[140:143], a[108:111], v[112:115]\n" \
"v_cvt_pk_bf16_f32 v84, v80, v81\n" \
"v_cvt_pk_bf16_f32 v88, v88, v89\n" \
"v_mfma_f32_16x16x32_bf16 v[116:119], a[156:159], a[108:111], v[40:43]\n" \
"v_exp_f32_e64 v90, v90\n" \
"v_mfma_f32_16x16x32_bf16 v[116:119], a[152:155], a[104:107], v[116:119]\n" \
"v_exp_f32_e64 v91, v91\n" \
"v_mfma_f32_16x16x32_bf16 v[116:119], a[148:151], a[100:103], v[116:119]\n" \
"buffer_load_dwordx4 v50, s[8:11], s64 offen offset:3072 sc1 lds\n" \
"v_mfma_f32_16x16x32_bf16 v[116:119], a[144:147], a[96:99], v[116:119]\n" \
"s_xor_b32 m0, s65, 0xa000\n" \
"v_cvt_pk_bf16_f32 v89, v90, v91\n" \
"v_mfma_f32_16x16x32_bf16 v[124:127], a[144:147], a[112:115], v[44:47]\n" \
"v_exp_f32_e64 v92, v92\n" \
"v_mfma_f32_16x16x32_bf16 v[124:127], a[148:151], a[116:119], v[124:127]\n" \
"v_exp_f32_e64 v93, v93\n" \
"v_mfma_f32_16x16x32_bf16 v[124:127], a[152:155], a[120:123], v[124:127]\n" \
"v_exp_f32_e64 v94, v94\n" \
"v_mfma_f32_16x16x32_bf16 v[124:127], a[156:159], a[124:127], v[124:127]\n" \
"v_exp_f32_e64 v95, v95\n" \
"v_mfma_f32_16x16x32_bf16 v[120:123], a[140:143], a[124:127], v[44:47]\n" \
"v_cvt_pk_bf16_f32 v90, v92, v93\n" \
"v_cvt_pk_bf16_f32 v91, v94, v95\n" \
"v_mfma_f32_16x16x32_bf16 v[120:123], a[136:139], a[120:123], v[120:123]\n" \
"ds_read_b128 a[144:147], v48 offset:4096\n" \
"ds_read_b128 a[148:151], v48 offset:5120\n" \
"v_mfma_f32_16x16x32_bf16 v[120:123], a[132:135], a[116:119], v[120:123]\n" \
"buffer_load_dwordx4 v51, s[8:11], s64 offen offset:0 sc1 lds\n" \
"v_mfma_f32_16x16x32_bf16 v[120:123], a[128:131], a[112:115], v[120:123]\n" \
"ds_read_b128 a[128:131], v48 offset:0\n" \
"s_waitcnt lgkmcnt(3)\n" \
"v_mfma_f32_16x16x32_bf16 v[128:131], a[160:163], v[68:71], v[128:131]\n" \
"ds_read_b128 a[132:135], v48 offset:1024\n" \
"ds_read_b128 a[136:139], v48 offset:2048\n" \
"v_mfma_f32_16x16x32_bf16 v[160:163], a[160:163], v[72:75], v[160:163]\n" \
"ds_read_b128 a[152:155], v48 offset:6144\n" \
"ds_read_b128 a[140:143], v48 offset:3072\n" \
"v_mfma_f32_16x16x32_bf16 v[132:135], a[164:167], v[68:71], v[132:135]\n" \
"v_exp_f32_e32 v102, v102\n" \
"v_mfma_f32_16x16x32_bf16 v[164:167], a[164:167], v[72:75], v[164:167]\n" \
"v_exp_f32_e64 v103, v103\n" \
"v_mfma_f32_16x16x32_bf16 v[136:139], a[168:171], v[68:71], v[136:139]\n" \
"ds_read_b128 a[156:159], v48 offset:7168\n" \
"v_cvt_pk_bf16_f32 v103, v102, v103\n" \
"v_mfma_f32_16x16x32_bf16 v[168:171], a[168:171], v[72:75], v[168:171]\n" \
"buffer_load_dwordx4 v51, s[8:11], s64 offen offset:1024 sc1 lds\n" \
"v_mfma_f32_16x16x32_bf16 v[140:143], a[172:175], v[68:71], v[140:143]\n" \
"v_exp_f32_e64 v100, v100\n" \
"v_mfma_f32_16x16x32_bf16 v[172:175], a[172:175], v[72:75], v[172:175]\n" \
"v_exp_f32_e64 v101, v101\n" \
"v_mfma_f32_16x16x32_bf16 v[144:147], a[176:179], v[68:71], v[144:147]\n" \
"v_exp_f32_e64 v98, v98\n" \
"v_mfma_f32_16x16x32_bf16 v[176:179], a[176:179], v[72:75], v[176:179]\n" \
"v_exp_f32_e64 v99, v99\n" \
"v_mfma_f32_16x16x32_bf16 v[148:151], a[180:183], v[68:71], v[148:151]\n" \
"v_exp_f32_e64 v96, v96\n" \
"v_mfma_f32_16x16x32_bf16 v[180:183], a[180:183], v[72:75], v[180:183]\n" \
"v_exp_f32_e64 v97, v97\n" \
"v_mfma_f32_16x16x32_bf16 v[152:155], a[184:187], v[68:71], v[152:155]\n" \
"v_exp_f32_e64 v104, v104\n" \
"v_mfma_f32_16x16x32_bf16 v[184:187], a[184:187], v[72:75], v[184:187]\n" \
"buffer_load_dwordx4 v51, s[8:11], s64 offen offset:2048 sc1 lds\n" \
"v_mfma_f32_16x16x32_bf16 v[156:159], a[188:191], v[68:71], v[156:159]\n" \
"v_exp_f32_e64 v105, v105\n" \
"v_mfma_f32_16x16x32_bf16 v[188:191], a[188:191], v[72:75], v[188:191]\n" \
"v_cvt_pk_bf16_f32 v102, v100, v101\n" \
"v_cvt_pk_bf16_f32 v101, v98, v99\n" \
"v_smfmac_f32_16x16x64_bf16 a[192:195], a[200:203], v[68:75], v21\n" \
"v_cvt_pk_bf16_f32 v100, v96, v97\n" \
"v_cvt_pk_bf16_f32 v104, v104, v105\n" \
"v_mfma_f32_16x16x32_bf16 v[220:223], a[188:191], v[84:87], v[220:223]\n" \
"v_exp_f32_e64 v106, v106\n" \
"v_mfma_f32_16x16x32_bf16 v[252:255], a[188:191], v[88:91], v[252:255]\n" \
"v_exp_f32_e64 v107, v107\n" \
"v_mfma_f32_16x16x32_bf16 v[216:219], a[184:187], v[84:87], v[216:219]\n" \
"v_exp_f32_e64 v108, v108\n" \
"v_mfma_f32_16x16x32_bf16 v[248:251], a[184:187], v[88:91], v[248:251]\n" \
"buffer_load_dwordx4 v51, s[8:11], s64 offen offset:3072 sc1 lds\n" \
"v_mfma_f32_16x16x32_bf16 v[212:215], a[180:183], v[84:87], v[212:215]\n" \
"v_exp_f32_e64 v109, v109\n" \
"v_mfma_f32_16x16x32_bf16 v[244:247], a[180:183], v[88:91], v[244:247]\n" \
"v_exp_f32_e64 v110, v110\n" \
"v_mfma_f32_16x16x32_bf16 v[208:211], a[176:179], v[84:87], v[208:211]\n" \
"v_exp_f32_e64 v111, v111\n" \
"v_mfma_f32_16x16x32_bf16 v[240:243], a[176:179], v[88:91], v[240:243]\n" \
"v_exp_f32_e64 v118, v118\n" \
"v_mfma_f32_16x16x32_bf16 v[204:207], a[172:175], v[84:87], v[204:207]\n" \
"v_exp_f32_e64 v119, v119\n" \
"v_mfma_f32_16x16x32_bf16 v[236:239], a[172:175], v[88:91], v[236:239]\n" \
"v_cvt_pk_bf16_f32 v105, v106, v107\n" \
"v_cvt_pk_bf16_f32 v106, v108, v109\n" \
"v_mfma_f32_16x16x32_bf16 v[200:203], a[168:171], v[84:87], v[200:203]\n" \
"v_cvt_pk_bf16_f32 v107, v110, v111\n" \
"v_cvt_pk_bf16_f32 v119, v118, v119\n" \
"v_mfma_f32_16x16x32_bf16 v[232:235], a[168:171], v[88:91], v[232:235]\n" \
"v_exp_f32_e64 v116, v116\n" \
"v_mfma_f32_16x16x32_bf16 v[196:199], a[164:167], v[84:87], v[196:199]\n" \
"v_exp_f32_e64 v117, v117\n" \
"v_mfma_f32_16x16x32_bf16 v[228:231], a[164:167], v[88:91], v[228:231]\n" \
"v_exp_f32_e64 v114, v114\n" \
"v_mfma_f32_16x16x32_bf16 v[192:195], a[160:163], v[84:87], v[192:195]\n" \
"v_exp_f32_e64 v115, v115\n" \
"v_mfma_f32_16x16x32_bf16 v[224:227], a[160:163], v[88:91], v[224:227]\n" \
"v_exp_f32_e64 v112, v112\n" \
"v_smfmac_f32_16x16x64_bf16 a[196:199], a[200:203], v[84:91], v21\n" \
"v_exp_f32_e32 v113, v113\n" \
"s_waitcnt vmcnt(8) lgkmcnt(0)\n" \
"s_barrier\n" \
"s_nop 0\n" \
"v_mfma_f32_16x16x32_bf16 v[64:67], a[128:131], a[64:67], v[32:35]\n" \
"s_xor_b32 m0, s65, 0x18000\n" \
"ds_read_b128 a[160:163], v49 offset:8192\n" \
"v_mfma_f32_16x16x32_bf16 v[64:67], a[132:135], a[68:71], v[64:67]\n" \
"buffer_load_dwordx4 v52, s[8:11], s64 offen offset:0 sc1 lds\n" \
"v_mfma_f32_16x16x32_bf16 v[64:67], a[136:139], a[72:75], v[64:67]\n" \
"ds_read_b128 a[164:167], v49 offset:9216\n" \
"ds_read_b128 a[168:171], v49 offset:10240\n" \
"v_mfma_f32_16x16x32_bf16 v[64:67], a[140:143], a[76:79], v[64:67]\n" \
"ds_read_b128 a[172:175], v49 offset:11264\n" \
"ds_read_b128 a[176:179], v49 offset:12288\n" \
"v_mfma_f32_16x16x32_bf16 v[68:71], a[156:159], a[76:79], v[32:35]\n" \
"ds_read_b128 a[180:183], v49 offset:13312\n" \
"ds_read_b128 a[184:187], v49 offset:14336\n" \
"v_mfma_f32_16x16x32_bf16 v[68:71], a[152:155], a[72:75], v[68:71]\n" \
"ds_read_b128 a[188:191], v49 offset:15360\n" \
"v_cvt_pk_bf16_f32 v118, v116, v117\n" \
"v_mfma_f32_16x16x32_bf16 v[68:71], a[148:151], a[68:71], v[68:71]\n" \
"v_cvt_pk_bf16_f32 v117, v114, v115\n" \
"v_cvt_pk_bf16_f32 v116, v112, v113\n" \
"v_mfma_f32_16x16x32_bf16 v[68:71], a[144:147], a[64:67], v[68:71]\n" \
"buffer_load_dwordx4 v52, s[8:11], s64 offen offset:1024 sc1 lds\n" \
"v_mfma_f32_16x16x32_bf16 v[76:79], a[144:147], a[80:83], v[36:39]\n" \
"v_exp_f32_e64 v120, v120\n" \
"v_mfma_f32_16x16x32_bf16 v[76:79], a[148:151], a[84:87], v[76:79]\n" \
"v_exp_f32_e64 v121, v121\n" \
"v_mfma_f32_16x16x32_bf16 v[76:79], a[152:155], a[88:91], v[76:79]\n" \
"v_exp_f32_e64 v122, v122\n" \
"v_mfma_f32_16x16x32_bf16 v[76:79], a[156:159], a[92:95], v[76:79]\n" \
"v_exp_f32_e64 v123, v123\n" \
"v_mfma_f32_16x16x32_bf16 v[72:75], a[140:143], a[92:95], v[36:39]\n" \
"v_cvt_pk_bf16_f32 v120, v120, v121\n" \
"v_cvt_pk_bf16_f32 v121, v122, v123\n" \
"v_mfma_f32_16x16x32_bf16 v[72:75], a[136:139], a[88:91], v[72:75]\n" \
"v_exp_f32_e64 v124, v124\n" \
"v_mfma_f32_16x16x32_bf16 v[72:75], a[132:135], a[84:87], v[72:75]\n" \
"v_exp_f32_e64 v125, v125\n" \
"v_mfma_f32_16x16x32_bf16 v[72:75], a[128:131], a[80:83], v[72:75]\n" \
"buffer_load_dwordx4 v52, s[8:11], s64 offen offset:2048 sc1 lds\n" \
"v_mfma_f32_16x16x32_bf16 v[80:83], a[128:131], a[96:99], v[40:43]\n" \
"v_exp_f32_e64 v126, v126\n" \
"v_mfma_f32_16x16x32_bf16 v[80:83], a[132:135], a[100:103], v[80:83]\n" \
"v_exp_f32_e64 v127, v127\n" \
"v_mfma_f32_16x16x32_bf16 v[80:83], a[136:139], a[104:107], v[80:83]\n" \
"v_cvt_pk_bf16_f32 v122, v124, v125\n" \
"v_cvt_pk_bf16_f32 v123, v126, v127\n" \
"v_mfma_f32_16x16x32_bf16 v[80:83], a[140:143], a[108:111], v[80:83]\n" \
"v_exp_f32_e64 v70, v70\n" \
"v_mfma_f32_16x16x32_bf16 v[84:87], a[156:159], a[108:111], v[40:43]\n" \
"v_exp_f32_e64 v71, v71\n" \
"v_mfma_f32_16x16x32_bf16 v[84:87], a[152:155], a[104:107], v[84:87]\n" \
"v_exp_f32_e64 v72, v72\n" \
"v_mfma_f32_16x16x32_bf16 v[84:87], a[148:151], a[100:103], v[84:87]\n" \
"buffer_load_dwordx4 v52, s[8:11], s64 offen offset:3072 sc1 lds\n" \
"v_mfma_f32_16x16x32_bf16 v[84:87], a[144:147], a[96:99], v[84:87]\n" \
"s_mov_b32 m0, s65\n" \
"v_cvt_pk_bf16_f32 v71, v70, v71\n" \
"v_mfma_f32_16x16x32_bf16 v[92:95], a[144:147], a[112:115], v[44:47]\n" \
"ds_read_b128 a[144:147], v48 offset:12288\n" \
"v_add_u32_e32 v49, 0x10000, v48\n" \
"v_mfma_f32_16x16x32_bf16 v[92:95], a[148:151], a[116:119], v[92:95]\n" \
"ds_read_b128 a[148:151], v48 offset:13312\n" \
"s_xor_b32 s65, s65, 0x8000\n" \
"v_mfma_f32_16x16x32_bf16 v[92:95], a[152:155], a[120:123], v[92:95]\n" \
"v_exp_f32_e32 v73, v73\n" \
"v_mfma_f32_16x16x32_bf16 v[92:95], a[156:159], a[124:127], v[92:95]\n" \
"ds_read_b128 a[152:155], v48 offset:14336\n" \
"ds_read_b128 a[156:159], v48 offset:15360\n" \
"v_mfma_f32_16x16x32_bf16 v[88:91], a[140:143], a[124:127], v[44:47]\n" \
"ds_read_b128 a[140:143], v48 offset:11264\n" \
"v_cvt_pk_bf16_f32 v72, v72, v73\n" \
"v_mfma_f32_16x16x32_bf16 v[88:91], a[136:139], a[120:123], v[88:91]\n" \
"v_exp_f32_e64 v68, v68\n" \
"v_mfma_f32_16x16x32_bf16 v[88:91], a[132:135], a[116:119], v[88:91]\n" \
"buffer_load_dwordx4 v53, s[8:11], s64 offen offset:0 sc1 lds\n" \
"v_mfma_f32_16x16x32_bf16 v[88:91], a[128:131], a[112:115], v[88:91]\n" \
"ds_read_b128 a[128:131], v48 offset:8192\n" \
"s_waitcnt lgkmcnt(6)\n" \
"v_mfma_f32_16x16x32_bf16 v[128:131], a[160:163], v[100:103], v[128:131]\n" \
"ds_read_b128 a[132:135], v48 offset:9216\n" \
"ds_read_b128 a[136:139], v48 offset:10240\n" \
"v_mfma_f32_16x16x32_bf16 v[160:163], a[160:163], v[104:107], v[160:163]\n" \
"v_exp_f32_e32 v69, v69\n" \
"v_mfma_f32_16x16x32_bf16 v[132:135], a[164:167], v[100:103], v[132:135]\n" \
"v_exp_f32_e64 v74, v74\n" \
"v_mfma_f32_16x16x32_bf16 v[164:167], a[164:167], v[104:107], v[164:167]\n" \
"v_exp_f32_e64 v75, v75\n" \
"v_mfma_f32_16x16x32_bf16 v[136:139], a[168:171], v[100:103], v[136:139]\n" \
"v_exp_f32_e64 v66, v66\n" \
"v_mfma_f32_16x16x32_bf16 v[168:171], a[168:171], v[104:107], v[168:171]\n" \
"buffer_load_dwordx4 v53, s[8:11], s64 offen offset:1024 sc1 lds\n" \
"v_mfma_f32_16x16x32_bf16 v[140:143], a[172:175], v[100:103], v[140:143]\n" \
"v_exp_f32_e64 v67, v67\n" \
"v_mfma_f32_16x16x32_bf16 v[172:175], a[172:175], v[104:107], v[172:175]\n" \
"v_exp_f32_e64 v64, v64\n" \
"v_mfma_f32_16x16x32_bf16 v[144:147], a[176:179], v[100:103], v[144:147]\n" \
"v_exp_f32_e64 v65, v65\n" \
"v_mfma_f32_16x16x32_bf16 v[176:179], a[176:179], v[104:107], v[176:179]\n" \
"v_exp_f32_e64 v76, v76\n" \
"v_mfma_f32_16x16x32_bf16 v[148:151], a[180:183], v[100:103], v[148:151]\n" \
"v_exp_f32_e64 v77, v77\n" \
"v_mfma_f32_16x16x32_bf16 v[180:183], a[180:183], v[104:107], v[180:183]\n" \
"v_exp_f32_e64 v78, v78\n" \
"v_mfma_f32_16x16x32_bf16 v[152:155], a[184:187], v[100:103], v[152:155]\n" \
"v_exp_f32_e64 v79, v79\n" \
"v_mfma_f32_16x16x32_bf16 v[184:187], a[184:187], v[104:107], v[184:187]\n" \
"buffer_load_dwordx4 v53, s[8:11], s64 offen offset:2048 sc1 lds\n" \
"v_mfma_f32_16x16x32_bf16 v[156:159], a[188:191], v[100:103], v[156:159]\n" \
"v_cvt_pk_bf16_f32 v70, v68, v69\n" \
"v_cvt_pk_bf16_f32 v73, v74, v75\n" \
"v_mfma_f32_16x16x32_bf16 v[188:191], a[188:191], v[104:107], v[188:191]\n" \
"v_cvt_pk_bf16_f32 v69, v66, v67\n" \
"v_cvt_pk_bf16_f32 v68, v64, v65\n" \
"v_smfmac_f32_16x16x64_bf16 a[192:195], a[200:203], v[100:107], v21\n" \
"v_cvt_pk_bf16_f32 v74, v76, v77\n" \
"v_cvt_pk_bf16_f32 v75, v78, v79\n" \
"v_mfma_f32_16x16x32_bf16 v[220:223], a[188:191], v[116:119], v[220:223]\n" \
"v_xor_b32_e32 v48, 0x8000, v48\n" \
"v_mfma_f32_16x16x32_bf16 v[252:255], a[188:191], v[120:123], v[252:255]\n" \
"v_mfma_f32_16x16x32_bf16 v[216:219], a[184:187], v[116:119], v[216:219]\n" \
"v_mfma_f32_16x16x32_bf16 v[248:251], a[184:187], v[120:123], v[248:251]\n" \
"buffer_load_dwordx4 v53, s[8:11], s64 offen offset:3072 sc1 lds\n" \
"v_mfma_f32_16x16x32_bf16 v[212:215], a[180:183], v[116:119], v[212:215]\n" \
"s_add_u32 s64, s64, 0x10000\n" \
"s_sub_u32 s66, s66, 1\n" \
"s_nop 0\n" \
"v_mfma_f32_16x16x32_bf16 v[244:247], a[180:183], v[120:123], v[244:247]\n" \
"v_mfma_f32_16x16x32_bf16 v[208:211], a[176:179], v[116:119], v[208:211]\n" \
"v_mfma_f32_16x16x32_bf16 v[240:243], a[176:179], v[120:123], v[240:243]\n" \
"v_mfma_f32_16x16x32_bf16 v[204:207], a[172:175], v[116:119], v[204:207]\n" \
"v_mfma_f32_16x16x32_bf16 v[236:239], a[172:175], v[120:123], v[236:239]\n" \
"v_mfma_f32_16x16x32_bf16 v[200:203], a[168:171], v[116:119], v[200:203]\n" \
"v_mfma_f32_16x16x32_bf16 v[232:235], a[168:171], v[120:123], v[232:235]\n" \
"v_mfma_f32_16x16x32_bf16 v[196:199], a[164:167], v[116:119], v[196:199]\n" \
"v_mfma_f32_16x16x32_bf16 v[228:231], a[164:167], v[120:123], v[228:231]\n" \
"v_mfma_f32_16x16x32_bf16 v[192:195], a[160:163], v[116:119], v[192:195]\n" \
"v_mfma_f32_16x16x32_bf16 v[224:227], a[160:163], v[120:123], v[224:227]\n" \
"v_smfmac_f32_16x16x64_bf16 a[196:199], a[200:203], v[116:123], v21\n" \
"s_waitcnt vmcnt(8) lgkmcnt(0)\n" \
"s_barrier\n" \
"s_cbranch_scc0 L_2_%=\n" \
"s_cmp_lg_u32 s16, 0\n" \
"s_cbranch_scc1 L_f1_%=\n" \
"v_bfe_u32 v58, v20, 2, 7\n" \
"v_readlane_b32 s96, v11, 1\n" \
"ds_read_b128 a[160:163], v49 offset:0\n" \
"ds_read_b128 a[164:167], v49 offset:1024\n" \
"ds_read_b128 a[168:171], v49 offset:2048\n" \
"v_mfma_f32_16x16x32_bf16 v[120:123], a[128:131], a[112:115], v[44:47]\n" \
"v_readlane_b32 s93, v11, 20\n" \
"ds_read_b128 a[172:175], v49 offset:3072\n" \
"buffer_load_dword v55, v58, s[56:59], s96 offen sc1\n" \
"v_readlane_b32 s84, v11, 15\n" \
"v_readlane_b32 s85, v11, 16\n" \
"v_readlane_b32 s86, v11, 17\n" \
"s_mov_b32 s87, 0x20000\n" \
"v_readlane_b32 s92, v11, 18\n" \
"s_add_u32 m0, s93, 0x0\n" \
"v_mfma_f32_16x16x32_bf16 v[120:123], a[132:135], a[116:119], v[120:123]\n" \
"ds_read_b128 a[176:179], v49 offset:4096\n" \
"v_add_u32_e32 v54, 0x49000, v20\n" \
"v_mfma_f32_16x16x32_bf16 v[120:123], a[136:139], a[120:123], v[120:123]\n" \
"v_readlane_b32 s97, v11, 35\n" \
"v_readlane_b32 s98, v11, 36\n" \
"v_mfma_f32_16x16x32_bf16 v[120:123], a[140:143], a[124:127], v[120:123]\n" \
"ds_read_b128 a[180:183], v49 offset:5120\n" \
"ds_read_b128 a[184:187], v49 offset:6144\n" \
"v_mfma_f32_16x16x32_bf16 v[124:127], a[156:159], a[124:127], v[44:47]\n" \
"ds_read_b128 a[188:191], v49 offset:7168\n" \
"v_add_u32_e32 v50, s15, v20\n" \
"buffer_load_dwordx4 v0, s[84:87], s92 offen offset:0 lds\n" \
"v_mfma_f32_16x16x32_bf16 v[124:127], a[152:155], a[120:123], v[124:127]\n" \
"v_exp_f32_e32 v86, v86\n" \
"v_mfma_f32_16x16x32_bf16 v[124:127], a[148:151], a[116:119], v[124:127]\n" \
"v_exp_f32_e32 v87, v87\n" \
"v_mfma_f32_16x16x32_bf16 v[124:127], a[144:147], a[112:115], v[124:127]\n" \
"v_cvt_pk_bf16_f32 v87, v86, v87\n" \
"s_waitcnt lgkmcnt(7)\n" \
"v_mfma_f32_16x16x32_bf16 v[128:131], a[160:163], v[68:71], v[128:131]\n" \
"v_exp_f32_e32 v84, v84\n" \
"v_mfma_f32_16x16x32_bf16 v[160:163], a[160:163], v[72:75], v[160:163]\n" \
"v_and_b32_e32 v87, v15, v87\n" \
"s_waitcnt lgkmcnt(6)\n" \
"v_mfma_f32_16x16x32_bf16 v[132:135], a[164:167], v[68:71], v[132:135]\n" \
"buffer_load_dwordx4 v1, s[84:87], s92 offen offset:1024 lds\n" \
"v_mfma_f32_16x16x32_bf16 v[164:167], a[164:167], v[72:75], v[164:167]\n" \
"v_add_u32_e32 v51, 0x10000, v50\n" \
"s_waitcnt lgkmcnt(5)\n" \
"v_mfma_f32_16x16x32_bf16 v[136:139], a[168:171], v[68:71], v[136:139]\n" \
"v_exp_f32_e32 v85, v85\n" \
"v_mfma_f32_16x16x32_bf16 v[168:171], a[168:171], v[72:75], v[168:171]\n" \
"v_cvt_pk_bf16_f32 v86, v84, v85\n" \
"s_waitcnt lgkmcnt(4)\n" \
"v_mfma_f32_16x16x32_bf16 v[140:143], a[172:175], v[68:71], v[140:143]\n" \
"v_exp_f32_e32 v82, v82\n" \
"v_mfma_f32_16x16x32_bf16 v[172:175], a[172:175], v[72:75], v[172:175]\n" \
"v_and_b32_e32 v86, v14, v86\n" \
"s_waitcnt lgkmcnt(3)\n" \
"v_mfma_f32_16x16x32_bf16 v[144:147], a[176:179], v[68:71], v[144:147]\n" \
"buffer_load_dwordx4 v2, s[84:87], s92 offen offset:2048 lds\n" \
"v_mfma_f32_16x16x32_bf16 v[176:179], a[176:179], v[72:75], v[176:179]\n" \
"v_cvt_pk_bf16_f32 v128, v128, v129\n" \
"s_waitcnt lgkmcnt(1)\n" \
"v_mfma_f32_16x16x32_bf16 v[148:151], a[180:183], v[68:71], v[148:151]\n" \
"v_exp_f32_e32 v83, v83\n" \
"v_mfma_f32_16x16x32_bf16 v[180:183], a[180:183], v[72:75], v[180:183]\n" \
"v_exp_f32_e32 v80, v80\n" \
"v_mfma_f32_16x16x32_bf16 v[152:155], a[184:187], v[68:71], v[152:155]\n" \
"v_exp_f32_e32 v81, v81\n" \
"v_mfma_f32_16x16x32_bf16 v[184:187], a[184:187], v[72:75], v[184:187]\n" \
"v_cvt_pk_bf16_f32 v85, v82, v83\n" \
"s_waitcnt lgkmcnt(0)\n" \
"v_mfma_f32_16x16x32_bf16 v[156:159], a[188:191], v[68:71], v[156:159]\n" \
"buffer_load_dwordx4 v3, s[84:87], s92 offen offset:3072 lds\n" \
"s_add_u32 m0, s93, 0x1000\n" \
"v_mfma_f32_16x16x32_bf16 v[188:191], a[188:191], v[72:75], v[188:191]\n" \
"v_cvt_pk_bf16_f32 v84, v80, v81\n" \
"v_and_b32_e32 v84, v12, v84\n" \
"v_smfmac_f32_16x16x64_bf16 a[192:195], a[200:203], v[68:75], v21\n" \
"v_and_b32_e32 v85, v13, v85\n" \
"v_exp_f32_e32 v88, v88\n" \
"v_exp_f32_e32 v89, v89\n" \
"v_mfma_f32_16x16x32_bf16 v[220:223], a[188:191], v[84:87], v[220:223]\n" \
"v_exp_f32_e32 v90, v90\n" \
"v_exp_f32_e32 v91, v91\n" \
"v_cvt_pk_bf16_f32 v88, v88, v89\n" \
"v_exp_f32_e32 v92, v92\n" \
"buffer_load_dwordx4 v4, s[84:87], s92 offen offset:0 lds\n" \
"v_exp_f32_e32 v93, v93\n" \
"v_exp_f32_e32 v94, v94\n" \
"v_exp_f32_e32 v95, v95\n" \
"v_cvt_pk_bf16_f32 v160, v160, v161\n" \
"v_cvt_pk_bf16_f32 v89, v90, v91\n" \
"v_cvt_pk_bf16_f32 v129, v130, v131\n" \
"v_cvt_pk_bf16_f32 v161, v162, v163\n" \
"v_cvt_pk_bf16_f32 v90, v92, v93\n" \
"v_cvt_pk_bf16_f32 v91, v94, v95\n" \
"v_cvt_pk_bf16_f32 v130, v132, v133\n" \
"v_cvt_pk_bf16_f32 v131, v134, v135\n" \
"v_cvt_pk_bf16_f32 v162, v164, v165\n" \
"v_cvt_pk_bf16_f32 v163, v166, v167\n" \
"v_mfma_f32_16x16x32_bf16 v[252:255], a[188:191], v[88:91], v[252:255]\n" \
"v_cvt_pk_bf16_f32 v132, v136, v137\n" \
"v_cvt_pk_bf16_f32 v133, v138, v139\n" \
"v_cvt_pk_bf16_f32 v134, v140, v141\n" \
"buffer_load_dwordx4 v5, s[84:87], s92 offen offset:1024 lds\n" \
"v_cvt_pk_bf16_f32 v135, v142, v143\n" \
"v_cvt_pk_bf16_f32 v164, v168, v169\n" \
"v_cvt_pk_bf16_f32 v165, v170, v171\n" \
"v_cvt_pk_bf16_f32 v166, v172, v173\n" \
"v_cvt_pk_bf16_f32 v167, v174, v175\n" \
"v_cvt_pk_bf16_f32 v136, v144, v145\n" \
"v_cvt_pk_bf16_f32 v137, v146, v147\n" \
"v_cvt_pk_bf16_f32 v138, v148, v149\n" \
"v_cvt_pk_bf16_f32 v139, v150, v151\n" \
"v_cvt_pk_bf16_f32 v140, v152, v153\n" \
"v_cvt_pk_bf16_f32 v141, v154, v155\n" \
"v_cvt_pk_bf16_f32 v142, v156, v157\n" \
"v_cvt_pk_bf16_f32 v143, v158, v159\n" \
"v_cvt_pk_bf16_f32 v168, v176, v177\n" \
"v_cvt_pk_bf16_f32 v169, v178, v179\n" \
"v_cvt_pk_bf16_f32 v170, v180, v181\n" \
"v_cvt_pk_bf16_f32 v171, v182, v183\n" \
"v_cvt_pk_bf16_f32 v172, v184, v185\n" \
"v_cvt_pk_bf16_f32 v173, v186, v187\n" \
"v_cvt_pk_bf16_f32 v174, v188, v189\n" \
"v_cvt_pk_bf16_f32 v175, v190, v191\n" \
"buffer_load_dwordx4 v6, s[84:87], s92 offen offset:2048 lds\n" \
"s_waitcnt vmcnt(8)\n" \
"ds_write_b128 v50, v[128:131] offset:0\n" \
"v_mfma_f32_16x16x32_bf16 v[216:219], a[184:187], v[84:87], v[216:219]\n" \
"ds_read_b32 v60, v18 offset:0\n" \
"ds_read_b32 v61, v18 offset:128\n" \
"v_mfma_f32_16x16x32_bf16 v[248:251], a[184:187], v[88:91], v[248:251]\n" \
"ds_write_b128 v50, v[132:135] offset:1024\n" \
"s_waitcnt vmcnt(7)\n" \
"v_cmp_ne_u32_e32 vcc, s62, v55\n" \
"v_mfma_f32_16x16x32_bf16 v[212:215], a[180:183], v[84:87], v[212:215]\n" \
"ds_read_b128 a[0:3], v49 offset:8192\n" \
"ds_read_b128 a[4:7], v49 offset:9216\n" \
"ds_write_b128 v50, v[136:139] offset:2048\n" \
"v_mfma_f32_16x16x32_bf16 v[244:247], a[180:183], v[88:91], v[244:247]\n" \
"buffer_load_dwordx4 v7, s[84:87], s92 offen offset:3072 lds\n" \
"s_add_u32 m0, s93, 0x2000\n" \
"s_cbranch_vccnz L_so0_%=\n" \
"L_ro0_%=:\n" \
"ds_write_b128 v50, v[140:143] offset:3072\n" \
"ds_read_b128 a[8:11], v49 offset:10240\n" \
"ds_read_b128 a[12:15], v49 offset:11264\n" \
"ds_read_b128 a[16:19], v49 offset:12288\n" \
"ds_read_b128 a[20:23], v49 offset:13312\n" \
"ds_read_b128 a[24:27], v49 offset:14336\n" \
"ds_read_b128 a[28:31], v49 offset:15360\n" \
"ds_write_b128 v51, v[160:163] offset:32768\n" \
"v_mfma_f32_16x16x32_bf16 v[208:211], a[176:179], v[84:87], v[208:211]\n" \
"v_accvgpr_read_b32 v40, a192\n" \
"v_accvgpr_read_b32 v22, a193\n" \
"v_mfma_f32_16x16x32_bf16 v[240:243], a[176:179], v[88:91], v[240:243]\n" \
"ds_write_b128 v51, v[164:167] offset:33792\n" \
"v_mfma_f32_16x16x32_bf16 v[204:207], a[172:175], v[84:87], v[204:207]\n" \
"buffer_load_dwordx4 v8, s[84:87], s92 offen offset:0 lds\n" \
"v_mfma_f32_16x16x32_bf16 v[236:239], a[172:175], v[88:91], v[236:239]\n" \
"ds_write_b128 v51, v[168:171] offset:34816\n" \
"v_mfma_f32_16x16x32_bf16 v[200:203], a[168:171], v[84:87], v[200:203]\n" \
"v_accvgpr_read_b32 v41, a194\n" \
"v_accvgpr_read_b32 v23, a195\n" \
"v_mfma_f32_16x16x32_bf16 v[232:235], a[168:171], v[88:91], v[232:235]\n" \
"ds_write_b128 v51, v[172:175] offset:35840\n" \
"v_mfma_f32_16x16x32_bf16 v[196:199], a[164:167], v[84:87], v[196:199]\n" \
"v_add_f32_e32 v40, v40, v22\n" \
"v_add_f32_e32 v41, v41, v23\n" \
"v_mfma_f32_16x16x32_bf16 v[228:231], a[164:167], v[88:91], v[228:231]\n" \
"ds_write_b64 v19, v[40:41]\n" \
"v_lshl_add_u32 v52, s15, 2, v20\n" \
"v_mfma_f32_16x16x32_bf16 v[192:195], a[160:163], v[84:87], v[192:195]\n" \
"buffer_load_dwordx4 a[220:223], v20, s[56:59], s60 offen offset:0 sc1\n" \
"v_mfma_f32_16x16x32_bf16 v[224:227], a[160:163], v[88:91], v[224:227]\n" \
"v_add_u32_e32 v52, 0x1a000, v52\n" \
"v_xor_b32_e32 v63, 0x400, v19\n" \
"v_smfmac_f32_16x16x64_bf16 a[196:199], a[200:203], v[84:91], v21\n" \
"v_exp_f32_e32 v120, v120\n" \
"v_exp_f32_e32 v121, v121\n" \
"v_exp_f32_e32 v122, v122\n" \
"v_exp_f32_e32 v123, v123\n" \
"v_cvt_pk_bf16_f32 v120, v120, v121\n" \
"v_exp_f32_e32 v124, v124\n" \
"v_exp_f32_e32 v125, v125\n" \
"v_exp_f32_e32 v126, v126\n" \
"buffer_load_dwordx4 a[236:239], v54, s[56:59], s60 offen offset:0 sc1\n" \
"v_exp_f32_e32 v127, v127\n" \
"v_cvt_pk_bf16_f32 v121, v122, v123\n" \
"v_cvt_pk_bf16_f32 v122, v124, v125\n" \
"v_cvt_pk_bf16_f32 v123, v126, v127\n" \
"v_and_b32_e32 v120, v12, v120\n" \
"v_and_b32_e32 v121, v13, v121\n" \
"v_and_b32_e32 v122, v14, v122\n" \
"v_and_b32_e32 v123, v15, v123\n" \
"s_waitcnt lgkmcnt(0)\n" \
"s_barrier\n" \
"ds_read_b128 v[144:147], v52 offset:0\n" \
"ds_read_b128 v[148:151], v52 offset:1024\n" \
"ds_read_b128 v[152:155], v52 offset:2048\n" \
"ds_read_b128 v[156:159], v52 offset:3072\n" \
"buffer_load_dwordx4 a[224:227], v20, s[56:59], s60 offen offset:1024 sc1\n" \
"ds_read_b128 v[176:179], v52 offset:4096\n" \
"ds_read_b128 v[180:183], v52 offset:5120\n" \
"ds_read_b128 v[184:187], v52 offset:6144\n" \
"ds_read_b128 v[188:191], v52 offset:7168\n" \
"v_mfma_f32_16x16x32_bf16 a[196:199], a[212:215], v[120:123], a[196:199]\n" \
"ds_read_b64 v[42:43], v63\n" \
"v_mov_b32_e32 v130, v28\n" \
"v_mfma_f32_16x16x32_bf16 v[224:227], a[0:3], v[120:123], v[224:227]\n" \
"v_mov_b32_e32 v131, v29\n" \
"v_mov_b32_e32 v132, 0\n" \
"v_mfma_f32_16x16x32_bf16 v[228:231], a[4:7], v[120:123], v[228:231]\n" \
"v_mov_b32_e32 v133, 0\n" \
"v_mov_b32_e32 v128, 0\n" \
"v_mfma_f32_16x16x32_bf16 v[232:235], a[8:11], v[120:123], v[232:235]\n" \
"v_mov_b32_e32 v129, 0\n" \
"v_accvgpr_read_b32 v56, a196\n" \
"v_mfma_f32_16x16x32_bf16 v[236:239], a[12:15], v[120:123], v[236:239]\n" \
"buffer_load_dwordx4 a[240:243], v54, s[56:59], s60 offen offset:1024 sc1\n" \
"v_mfma_f32_16x16x32_bf16 v[240:243], a[16:19], v[120:123], v[240:243]\n" \
"v_accvgpr_read_b32 v24, a197\n" \
"v_add_f32_e32 v56, v56, v24\n" \
"v_mfma_f32_16x16x32_bf16 v[244:247], a[20:23], v[120:123], v[244:247]\n" \
"v_accvgpr_read_b32 v57, a198\n" \
"v_accvgpr_read_b32 v25, a199\n" \
"v_mfma_f32_16x16x32_bf16 v[248:251], a[24:27], v[120:123], v[248:251]\n" \
"v_add_f32_e32 v57, v57, v25\n" \
"v_mfma_f32_16x16x32_bf16 v[252:255], a[28:31], v[120:123], v[252:255]\n" \
"s_waitcnt lgkmcnt(1)\n" \
"s_barrier\n" \
"buffer_load_dwordx4 a[228:231], v20, s[56:59], s60 offen offset:2048 sc1\n" \
"v_mfma_f32_16x16x32_bf16 v[192:195], v[130:133], v[144:147], v[192:195]\n" \
"s_waitcnt lgkmcnt(0)\n" \
"v_add_f32_e32 v56, v56, v42\n" \
"v_mfma_f32_16x16x32_bf16 v[196:199], v[128:131], v[144:147], v[196:199]\n" \
"v_max_f32_e32 v62, v62, v56\n" \
"v_add_f32_e32 v57, v57, v43\n" \
"v_mfma_f32_16x16x32_bf16 v[200:203], v[130:133], v[148:151], v[200:203]\n" \
"v_rcp_f32_e32 v56, v56\n" \
"v_mfma_f32_16x16x32_bf16 v[204:207], v[128:131], v[148:151], v[204:207]\n" \
"v_mul_f32_e32 v56, v56, v60\n" \
"v_mul_f32_e32 v192, v56, v192\n" \
"v_mfma_f32_16x16x32_bf16 v[208:211], v[130:133], v[152:155], v[208:211]\n" \
"v_mul_f32_e32 v193, v56, v193\n" \
"v_mul_f32_e32 v194, v56, v194\n" \
"v_mfma_f32_16x16x32_bf16 v[212:215], v[128:131], v[152:155], v[212:215]\n" \
"buffer_load_dwordx4 a[244:247], v54, s[56:59], s60 offen offset:2048 sc1\n" \
"v_mfma_f32_16x16x32_bf16 v[216:219], v[130:133], v[156:159], v[216:219]\n" \
"v_mul_f32_e32 v195, v56, v195\n" \
"v_mul_f32_e32 v196, v56, v196\n" \
"v_mfma_f32_16x16x32_bf16 v[220:223], v[128:131], v[156:159], v[220:223]\n" \
"v_mul_f32_e32 v197, v56, v197\n" \
"v_mul_f32_e32 v198, v56, v198\n" \
"v_mfma_f32_16x16x32_bf16 v[224:227], v[130:133], v[176:179], v[224:227]\n" \
"v_mul_f32_e32 v199, v56, v199\n" \
"v_cvt_pk_bf16_f32 v64, v192, v193\n" \
"v_mfma_f32_16x16x32_bf16 v[228:231], v[128:131], v[176:179], v[228:231]\n" \
"v_cvt_pk_bf16_f32 v65, v194, v195\n" \
"v_cvt_pk_bf16_f32 v66, v196, v197\n" \
"v_mfma_f32_16x16x32_bf16 v[232:235], v[130:133], v[180:183], v[232:235]\n" \
"v_cvt_pk_bf16_f32 v67, v198, v199\n" \
"v_mul_f32_e32 v200, v56, v200\n" \
"v_mfma_f32_16x16x32_bf16 v[236:239], v[128:131], v[180:183], v[236:239]\n" \
"buffer_load_dwordx4 a[232:235], v20, s[56:59], s60 offen offset:3072 sc1\n" \
"v_mfma_f32_16x16x32_bf16 v[240:243], v[130:133], v[184:187], v[240:243]\n" \
"v_mfma_f32_16x16x32_bf16 a[208:211], a[216:219], v[64:67], a[208:211]\n" \
"v_mul_f32_e32 v201, v56, v201\n" \
"v_mul_f32_e32 v202, v56, v202\n" \
"v_mul_f32_e32 v203, v56, v203\n" \
"v_mul_f32_e32 v204, v56, v204\n" \
"v_mul_f32_e32 v205, v56, v205\n" \
"v_mul_f32_e32 v206, v56, v206\n" \
"v_mul_f32_e32 v207, v56, v207\n" \
"v_cvt_pk_bf16_f32 v68, v200, v201\n" \
"v_cvt_pk_bf16_f32 v69, v202, v203\n" \
"v_cvt_pk_bf16_f32 v70, v204, v205\n" \
"v_cvt_pk_bf16_f32 v71, v206, v207\n" \
"v_mul_f32_e32 v208, v56, v208\n" \
"v_mul_f32_e32 v209, v56, v209\n" \
"v_mfma_f32_16x16x32_bf16 a[208:211], a[216:219], v[68:71], a[208:211]\n" \
"buffer_load_dwordx4 a[248:251], v54, s[56:59], s60 offen offset:3072 sc1\n" \
"v_mul_f32_e32 v210, v56, v210\n" \
"v_mul_f32_e32 v211, v56, v211\n" \
"v_mul_f32_e32 v212, v56, v212\n" \
"v_mul_f32_e32 v213, v56, v213\n" \
"v_mul_f32_e32 v214, v56, v214\n" \
"v_mul_f32_e32 v215, v56, v215\n" \
"v_max_f32_e32 v62, v62, v57\n" \
"v_cvt_pk_bf16_f32 v72, v208, v209\n" \
"v_cvt_pk_bf16_f32 v73, v210, v211\n" \
"v_cvt_pk_bf16_f32 v74, v212, v213\n" \
"v_cvt_pk_bf16_f32 v75, v214, v215\n" \
"v_rcp_f32_e32 v57, v57\n" \
"v_mfma_f32_16x16x32_bf16 v[244:247], v[128:131], v[184:187], v[244:247]\n" \
"v_mfma_f32_16x16x32_bf16 a[208:211], a[216:219], v[72:75], a[208:211]\n" \
"v_mul_f32_e32 v216, v56, v216\n" \
"v_mul_f32_e32 v217, v56, v217\n" \
"buffer_load_dwordx4 a[96:99], v20, s[56:59], s97 offen offset:0 sc1\n" \
"v_mul_f32_e32 v218, v56, v218\n" \
"v_mul_f32_e32 v219, v56, v219\n" \
"v_mul_f32_e32 v220, v56, v220\n" \
"v_mul_f32_e32 v221, v56, v221\n" \
"v_mul_f32_e32 v222, v56, v222\n" \
"v_mul_f32_e32 v223, v56, v223\n" \
"v_cvt_pk_bf16_f32 v76, v216, v217\n" \
"v_cvt_pk_bf16_f32 v77, v218, v219\n" \
"v_cvt_pk_bf16_f32 v78, v220, v221\n" \
"v_cvt_pk_bf16_f32 v79, v222, v223\n" \
"v_mul_f32_e32 v57, v57, v61\n" \
"v_mul_f32_e32 v224, v57, v224\n" \
"v_mfma_f32_16x16x32_bf16 a[208:211], a[216:219], v[76:79], a[208:211]\n" \
"v_mul_f32_e32 v225, v57, v225\n" \
"v_mul_f32_e32 v226, v57, v226\n" \
"v_mul_f32_e32 v227, v57, v227\n" \
"v_mul_f32_e32 v228, v57, v228\n" \
"v_mul_f32_e32 v229, v57, v229\n" \
"v_mul_f32_e32 v230, v57, v230\n" \
"v_mul_f32_e32 v231, v57, v231\n" \
"buffer_load_dwordx4 a[104:107], v54, s[56:59], s97 offen offset:0 sc1\n" \
"v_cvt_pk_bf16_f32 v80, v224, v225\n" \
"v_cvt_pk_bf16_f32 v81, v226, v227\n" \
"v_cvt_pk_bf16_f32 v82, v228, v229\n" \
"v_cvt_pk_bf16_f32 v83, v230, v231\n" \
"v_mfma_f32_16x16x32_bf16 v[248:251], v[130:133], v[188:191], v[248:251]\n" \
"v_mul_f32_e32 v232, v57, v232\n" \
"v_mul_f32_e32 v233, v57, v233\n" \
"v_mfma_f32_16x16x32_bf16 a[208:211], a[216:219], v[80:83], a[208:211]\n" \
"v_mul_f32_e32 v234, v57, v234\n" \
"v_mul_f32_e32 v235, v57, v235\n" \
"v_mul_f32_e32 v236, v57, v236\n" \
"v_mul_f32_e32 v237, v57, v237\n" \
"v_mul_f32_e32 v238, v57, v238\n" \
"v_mul_f32_e32 v239, v57, v239\n" \
"v_cvt_pk_bf16_f32 v84, v232, v233\n" \
"v_cvt_pk_bf16_f32 v85, v234, v235\n" \
"v_cvt_pk_bf16_f32 v86, v236, v237\n" \
"v_cvt_pk_bf16_f32 v87, v238, v239\n" \
"v_mul_f32_e32 v240, v57, v240\n" \
"buffer_load_dwordx4 a[112:115], v20, s[56:59], s98 offen offset:0 sc1\n" \
"v_mfma_f32_16x16x32_bf16 a[208:211], a[216:219], v[84:87], a[208:211]\n" \
"v_mul_f32_e32 v241, v57, v241\n" \
"v_mul_f32_e32 v242, v57, v242\n" \
"v_mul_f32_e32 v243, v57, v243\n" \
"v_mul_f32_e32 v244, v57, v244\n" \
"v_mul_f32_e32 v245, v57, v245\n" \
"v_mul_f32_e32 v246, v57, v246\n" \
"v_mul_f32_e32 v247, v57, v247\n" \
"v_cvt_pk_bf16_f32 v96, v240, v241\n" \
"v_cvt_pk_bf16_f32 v97, v242, v243\n" \
"v_cvt_pk_bf16_f32 v98, v244, v245\n" \
"v_cvt_pk_bf16_f32 v99, v246, v247\n" \
"v_mfma_f32_16x16x32_bf16 v[252:255], v[128:131], v[188:191], v[252:255]\n" \
"v_mul_f32_e32 v248, v57, v248\n" \
"v_mfma_f32_16x16x32_bf16 a[208:211], a[216:219], v[96:99], a[208:211]\n" \
"v_mul_f32_e32 v249, v57, v249\n" \
"v_mul_f32_e32 v250, v57, v250\n" \
"buffer_load_dwordx4 a[120:123], v54, s[56:59], s98 offen offset:0 sc1\n" \
"v_mul_f32_e32 v251, v57, v251\n" \
"v_cvt_pk_bf16_f32 v100, v248, v249\n" \
"v_cvt_pk_bf16_f32 v101, v250, v251\n" \
"v_mul_f32_e32 v252, v57, v252\n" \
"v_mul_f32_e32 v253, v57, v253\n" \
"v_mul_f32_e32 v254, v57, v254\n" \
"v_mul_f32_e32 v255, v57, v255\n" \
"v_cvt_pk_bf16_f32 v102, v252, v253\n" \
"v_cvt_pk_bf16_f32 v103, v254, v255\n" \
"v_cmp_lt_f32_e64 s[72:73], s21, v62\n" \
"buffer_load_dwordx4 a[100:103], v20, s[56:59], s97 offen offset:1024 sc1\n" \
"v_mfma_f32_16x16x32_bf16 a[208:211], a[216:219], v[100:103], a[208:211]\n" \
"buffer_load_dwordx4 a[108:111], v54, s[56:59], s97 offen offset:1024 sc1\n" \
"buffer_load_dwordx4 a[116:119], v20, s[56:59], s98 offen offset:1024 sc1\n" \
"buffer_load_dwordx4 a[124:127], v54, s[56:59], s98 offen offset:1024 sc1\n" \
"s_nop 4\n" \
"v_accvgpr_read_b32 v63, a208\n" \
"v_cmp_u_f32_e32 vcc, v63, v63\n" \
"s_nop 0\n" \
"s_or_b64 vcc, s[72:73], vcc\n" \
"s_cbranch_scc0 L_out_%=\n" \
"s_branch L_end_%=\n" \
"L_skb_%=:\n" \
"s_sleep 1\n" \
"buffer_load_dword v57, v61, s[56:59], s96 offen sc1\n" \
"s_add_u32 s101, s101, 1\n" \
"s_waitcnt vmcnt(0)\n" \
"v_cmp_ne_u32_e32 vcc, s62, v57\n" \
"s_cmp_gt_u32 s101, 0x100000\n" \
"s_cbranch_scc1 L_rkb_%=\n" \
"s_nop 2\n" \
"s_cbranch_vccnz L_skb_%=\n" \
"s_branch L_rkb_%=\n" \
"L_so0_%=:\n" \
"s_sleep 1\n" \
"buffer_load_dword v55, v58, s[56:59], s96 offen sc1\n" \
"s_add_u32 s101, s101, 1\n" \
"s_waitcnt vmcnt(0)\n" \
"v_cmp_ne_u32_e32 vcc, s62, v55\n" \
"s_cmp_gt_u32 s101, 0x100000\n" \
"s_cbranch_scc1 L_ro0_%=\n" \
"s_nop 2\n" \
"s_cbranch_vccnz L_so0_%=\n" \
"s_branch L_ro0_%=\n" \
"L_so1_%=:\n" \
"s_sleep 1\n" \
"buffer_load_dword v57, v60, s[56:59], s96 offen sc1\n" \
"s_add_u32 s101, s101, 1\n" \
"s_waitcnt vmcnt(0)\n" \
"v_cmp_ne_u32_e32 vcc, s62, v57\n" \
"s_cmp_gt_u32 s101, 0x100000\n" \
"s_cbranch_scc1 L_ro1_%=\n" \
"s_nop 2\n" \
"s_cbranch_vccnz L_so1_%=\n" \
"s_branch L_ro1_%=\n" \
"L_sa_%=:\n" \
"s_sleep 1\n" \
"buffer_load_dword v26, v38, s[56:59], s64 offen sc1\n" \
"s_add_u32 s101, s101, 1\n" \
"s_waitcnt vmcnt(0)\n" \
"v_cmp_ne_u32_e32 vcc, s62, v26\n" \
"s_cmp_gt_u32 s101, 0x100000\n" \
"s_cbranch_scc1 L_ra_%=\n" \
"s_nop 2\n" \
"s_cbranch_vccnz L_sa_%=\n" \
"s_branch L_ra_%=\n" \
"L_sb_%=:\n" \
"s_sleep 1\n" \
"buffer_load_dword v47, v38, s[56:59], s73 offen sc1\n" \
"s_add_u32 s101, s101, 1\n" \
"s_waitcnt vmcnt(0)\n" \
"v_cmp_ne_u32_e32 vcc, s62, v47\n" \
"s_cmp_gt_u32 s101, 0x100000\n" \
"s_cbranch_scc1 L_rb_%=\n" \
"s_nop 2\n" \
"s_cbranch_vccnz L_sb_%=\n" \
"s_branch L_rb_%=\n" \
"L_sx0_%=:\n" \
"s_sleep 1\n" \
"buffer_load_dword v37, v40, s[56:59], s74 offen sc1\n" \
"s_add_u32 s101, s101, 1\n" \
"s_waitcnt vmcnt(0)\n" \
"v_cmp_eq_u32_e64 s[98:99], s62, v37\n" \
"s_cmp_gt_u32 s101, 0x100000\n" \
"s_cbranch_scc1 L_rx0_%=\n" \
"s_or_b32 s75, 0x7f, s100\n" \
"s_andn2_b32 s75, s75, s98\n" \
"s_cbranch_scc1 L_sx0_%=\n" \
"s_branch L_rx0_%=\n" \
"L_sx1_%=:\n" \
"s_sleep 1\n" \
"buffer_load_dword v37, v40, s[56:59], s74 offen sc1\n" \
"s_add_u32 s101, s101, 1\n" \
"s_waitcnt vmcnt(0)\n" \
"v_cmp_eq_u32_e64 s[98:99], s62, v37\n" \
"s_cmp_gt_u32 s101, 0x100000\n" \
"s_cbranch_scc1 L_rx1_%=\n" \
"s_andn2_b32 s75, 0x100, s98\n" \
"s_cbranch_scc1 L_sx1_%=\n" \
"s_branch L_rx1_%=\n" \
"L_sx2_%=:\n" \
"s_sleep 1\n" \
"buffer_load_dword v37, v40, s[56:59], s74 offen sc1\n" \
"s_add_u32 s101, s101, 1\n" \
"s_waitcnt vmcnt(0)\n" \
"v_cmp_eq_u32_e64 s[98:99], s62, v37\n" \
"s_cmp_gt_u32 s101, 0x100000\n" \
"s_cbranch_scc1 L_rx2_%=\n" \
"s_andn2_b32 s75, 0x200, s98\n" \
"s_cbranch_scc1 L_sx2_%=\n" \
"s_branch L_rx2_%=\n" \
"L_sx3_%=:\n" \
"s_sleep 1\n" \
"buffer_load_dword v37, v40, s[56:59], s74 offen sc1\n" \
"s_add_u32 s101, s101, 1\n" \
"s_waitcnt vmcnt(0)\n" \
"v_cmp_eq_u32_e64 s[98:99], s62, v37\n" \
"s_cmp_gt_u32 s101, 0x100000\n" \
"s_cbranch_scc1 L_rx3_%=\n" \
"s_andn2_b32 s75, 0x400, s98\n" \
"s_cbranch_scc1 L_sx3_%=\n" \
"s_branch L_rx3_%=\n" \
"L_sx4_%=:\n" \
"s_sleep 1\n" \
"buffer_load_dword v37, v40, s[56:59], s74 offen sc1\n" \
"s_add_u32 s101, s101, 1\n" \
"s_waitcnt vmcnt(0)\n" \
"v_cmp_eq_u32_e64 s[98:99], s62, v37\n" \
"s_cmp_gt_u32 s101, 0x100000\n" \
"s_cbranch_scc1 L_rx4_%=\n" \
"s_andn2_b32 s75, 0x800, s98\n" \
"s_cbranch_scc1 L_sx4_%=\n" \
"s_branch L_rx4_%=\n" \
"L_sx5_%=:\n" \
"s_sleep 1\n" \
"buffer_load_dword v37, v40, s[56:59], s74 offen sc1\n" \
"s_add_u32 s101, s101, 1\n" \
"s_waitcnt vmcnt(0)\n" \
"v_cmp_eq_u32_e64 s[98:99], s62, v37\n" \
"s_cmp_gt_u32 s101, 0x100000\n" \
"s_cbranch_scc1 L_rx5_%=\n" \
"s_andn2_b32 s75, 0x1000, s98\n" \
"s_cbranch_scc1 L_sx5_%=\n" \
"s_branch L_rx5_%=\n" \
"L_sx6_%=:\n" \
"s_sleep 1\n" \
"buffer_load_dword v37, v40, s[56:59], s74 offen sc1\n" \
"s_add_u32 s101, s101, 1\n" \
"s_waitcnt vmcnt(0)\n" \
"v_cmp_eq_u32_e64 s[98:99], s62, v37\n" \
"s_cmp_gt_u32 s101, 0x100000\n" \
"s_cbranch_scc1 L_rx6_%=\n" \
"s_andn2_b32 s75, 0x2000, s98\n" \
"s_cbranch_scc1 L_sx6_%=\n" \
"s_branch L_rx6_%=\n" \
"L_sx7_%=:\n" \
"s_sleep 1\n" \
"buffer_load_dword v37, v40, s[56:59], s74 offen sc1\n" \
"s_add_u32 s101, s101, 1\n" \
"s_waitcnt vmcnt(0)\n" \
"v_cmp_eq_u32_e64 s[98:99], s62, v37\n" \
"s_cmp_gt_u32 s101, 0x100000\n" \
"s_cbranch_scc1 L_rx7_%=\n" \
"s_andn2_b32 s75, 0x4000, s98\n" \
"s_cbranch_scc1 L_sx7_%=\n" \
"s_branch L_rx7_%=\n" \
"L_att_stub:\n" \
"s_mov_b32 s101, 0\n" \
"v_add_u32_e32 v32, 0x49000, v20\n" \
"v_readlane_b32 s96, v11, 35\n" \
"v_readlane_b32 s97, v11, 36\n" \
"buffer_load_dwordx4 a[220:223], v20, s[56:59], s60 offen offset:0 sc1\n" \
"buffer_load_dwordx4 a[236:239], v32, s[56:59], s60 offen offset:0 sc1\n" \
"buffer_load_dwordx4 a[224:227], v20, s[56:59], s60 offen offset:1024 sc1\n" \
"buffer_load_dwordx4 a[240:243], v32, s[56:59], s60 offen offset:1024 sc1\n" \
"buffer_load_dwordx4 a[228:231], v20, s[56:59], s60 offen offset:2048 sc1\n" \
"buffer_load_dwordx4 a[244:247], v32, s[56:59], s60 offen offset:2048 sc1\n" \
"buffer_load_dwordx4 a[232:235], v20, s[56:59], s60 offen offset:3072 sc1\n" \
"buffer_load_dwordx4 a[248:251], v32, s[56:59], s60 offen offset:3072 sc1\n" \
"buffer_load_dwordx4 a[96:99], v20, s[56:59], s96 offen offset:0 sc1\n" \
"buffer_load_dwordx4 a[104:107], v32, s[56:59], s96 offen offset:0 sc1\n" \
"buffer_load_dwordx4 a[112:115], v20, s[56:59], s97 offen offset:0 sc1\n" \
"buffer_load_dwordx4 a[120:123], v32, s[56:59], s97 offen offset:0 sc1\n" \
"buffer_load_dwordx4 a[100:103], v20, s[56:59], s96 offen offset:1024 sc1\n" \
"buffer_load_dwordx4 a[108:111], v32, s[56:59], s96 offen offset:1024 sc1\n" \
"buffer_load_dwordx4 a[116:119], v20, s[56:59], s97 offen offset:1024 sc1\n" \
"buffer_load_dwordx4 a[124:127], v32, s[56:59], s97 offen offset:1024 sc1\n" \
"buffer_load_dwordx4 v[64:67], v20, s[56:59], s61 offen offset:0 sc1\n" \
"buffer_load_dwordx4 v[68:71], v20, s[56:59], s61 offen offset:1024 sc1\n" \
"buffer_load_dwordx4 v[72:75], v20, s[56:59], s61 offen offset:2048 sc1\n" \
"buffer_load_dwordx4 v[76:79], v20, s[56:59], s61 offen offset:3072 sc1\n" \
"buffer_load_dwordx4 v[80:83], v32, s[56:59], s61 offen offset:0 sc1\n" \
"buffer_load_dwordx4 v[84:87], v32, s[56:59], s61 offen offset:1024 sc1\n" \
"buffer_load_dwordx4 v[96:99], v32, s[56:59], s61 offen offset:2048 sc1\n" \
"buffer_load_dwordx4 v[100:103], v32, s[56:59], s61 offen offset:3072 sc1\n" \
"v_not_b32_e32 v26, s62\n" \
"s_waitcnt vmcnt(0)\n" \
"s_branch L_out_%=\n" \
"L_sw_%=:\n" \
"v_mfma_f32_16x16x32_bf16 v[104:107], a[128:131], a[16:19], v[36:39]\n" \
"s_add_u32 s68, s60, 0xfff6e000\n" \
"s_add_u32 s69, s60, 0xfffb7000\n" \
"s_add_u32 s70, s60, 0x0\n" \
"s_add_u32 s71, s60, 0x49000\n" \
"v_mfma_f32_16x16x32_bf16 v[104:107], a[132:135], a[20:23], v[104:107]\n" \
"s_xor_b32 m0, s65, 0x12000\n" \
"ds_read_b128 a[160:163], v49 offset:0\n" \
"v_mfma_f32_16x16x32_bf16 v[104:107], a[136:139], a[24:27], v[104:107]\n" \
"buffer_load_dwordx4 v50, s[8:11], s64 offen offset:0 sc1 lds\n" \
"v_mfma_f32_16x16x32_bf16 v[104:107], a[140:143], a[28:31], v[104:107]\n" \
"ds_read_b128 a[164:167], v49 offset:1024\n" \
"ds_read_b128 a[168:171], v49 offset:2048\n" \
"v_mfma_f32_16x16x32_bf16 v[108:111], a[156:159], a[28:31], v[36:39]\n" \
"ds_read_b128 a[172:175], v49 offset:3072\n" \
"ds_read_b128 a[176:179], v49 offset:4096\n" \
"v_mfma_f32_16x16x32_bf16 v[108:111], a[152:155], a[24:27], v[108:111]\n" \
"buffer_load_dwordx4 v50, s[8:11], s64 offen offset:1024 sc1 lds\n" \
"v_mfma_f32_16x16x32_bf16 v[108:111], a[148:151], a[20:23], v[108:111]\n" \
"s_mov_b32 s65, s13\n" \
"v_add_u32_e32 v63, s15, v20\n" \
"v_mfma_f32_16x16x32_bf16 v[108:111], a[144:147], a[16:19], v[108:111]\n" \
"s_add_u32 s67, s14, 40960\n" \
"v_add_u32_e32 v50, 0x6000, v63\n" \
"v_mfma_f32_16x16x32_bf16 v[116:119], a[144:147], a[32:35], v[40:43]\n" \
"s_xor_b32 m0, s65, 0x8000\n" \
"ds_read_b128 a[180:183], v49 offset:5120\n" \
"v_mfma_f32_16x16x32_bf16 v[116:119], a[148:151], a[36:39], v[116:119]\n" \
"ds_read_b128 a[184:187], v49 offset:6144\n" \
"v_and_b32_e32 v68, v12, v68\n" \
"v_mfma_f32_16x16x32_bf16 v[116:119], a[152:155], a[40:43], v[116:119]\n" \
"v_and_b32_e32 v69, v13, v69\n" \
"v_and_b32_e32 v70, v14, v70\n" \
"v_mfma_f32_16x16x32_bf16 v[116:119], a[156:159], a[44:47], v[116:119]\n" \
"buffer_load_dwordx4 v50, s[8:11], s67 offen offset:0 sc1 lds\n" \
"v_mfma_f32_16x16x32_bf16 v[112:115], a[140:143], a[44:47], v[40:43]\n" \
"v_and_b32_e32 v71, v15, v71\n" \
"ds_read_b128 a[188:191], v49 offset:7168\n" \
"v_mfma_f32_16x16x32_bf16 v[112:115], a[136:139], a[40:43], v[112:115]\n" \
"v_exp_f32_e32 v86, v86\n" \
"v_mfma_f32_16x16x32_bf16 v[112:115], a[132:135], a[36:39], v[112:115]\n" \
"v_exp_f32_e32 v87, v87\n" \
"v_mfma_f32_16x16x32_bf16 v[112:115], a[128:131], a[32:35], v[112:115]\n" \
"v_exp_f32_e32 v84, v84\n" \
"v_mfma_f32_16x16x32_bf16 v[120:123], a[128:131], a[48:51], v[44:47]\n" \
"buffer_load_dwordx4 v50, s[8:11], s67 offen offset:1024 sc1 lds\n" \
"v_mfma_f32_16x16x32_bf16 v[120:123], a[132:135], a[52:55], v[120:123]\n" \
"v_exp_f32_e32 v85, v85\n" \
"v_mfma_f32_16x16x32_bf16 v[120:123], a[136:139], a[56:59], v[120:123]\n" \
"v_cvt_pk_bf16_f32 v87, v86, v87\n" \
"v_cvt_pk_bf16_f32 v86, v84, v85\n" \
"v_mfma_f32_16x16x32_bf16 v[120:123], a[140:143], a[60:63], v[120:123]\n" \
"v_exp_f32_e32 v82, v82\n" \
"v_mfma_f32_16x16x32_bf16 v[124:127], a[156:159], a[60:63], v[44:47]\n" \
"v_exp_f32_e32 v83, v83\n" \
"v_mfma_f32_16x16x32_bf16 v[124:127], a[152:155], a[56:59], v[124:127]\n" \
"v_exp_f32_e32 v80, v80\n" \
"v_mfma_f32_16x16x32_bf16 v[124:127], a[148:151], a[52:55], v[124:127]\n" \
"buffer_load_dwordx4 v50, s[8:11], s67 offen offset:2048 sc1 lds\n" \
"v_mfma_f32_16x16x32_bf16 v[124:127], a[144:147], a[48:51], v[124:127]\n" \
"v_cvt_pk_bf16_f32 v85, v82, v83\n" \
"s_waitcnt lgkmcnt(1)\n" \
"v_mfma_f32_16x16x32_bf16 v[128:131], a[160:163], v[68:71], v[128:131]\n" \
"v_exp_f32_e32 v81, v81\n" \
"v_mfma_f32_16x16x32_bf16 v[160:163], a[160:163], v[72:75], v[160:163]\n" \
"v_cvt_pk_bf16_f32 v84, v80, v81\n" \
"s_mov_b32 s64, s14\n" \
"v_mfma_f32_16x16x32_bf16 v[132:135], a[164:167], v[68:71], v[132:135]\n" \
"v_exp_f32_e32 v88, v88\n" \
"v_mfma_f32_16x16x32_bf16 v[164:167], a[164:167], v[72:75], v[164:167]\n" \
"v_exp_f32_e32 v89, v89\n" \
"v_mfma_f32_16x16x32_bf16 v[136:139], a[168:171], v[68:71], v[136:139]\n" \
"buffer_load_dwordx4 v50, s[8:11], s67 offen offset:3072 sc1 lds\n" \
"v_mfma_f32_16x16x32_bf16 v[168:171], a[168:171], v[72:75], v[168:171]\n" \
"v_exp_f32_e32 v90, v90\n" \
"v_mfma_f32_16x16x32_bf16 v[140:143], a[172:175], v[68:71], v[140:143]\n" \
"v_exp_f32_e32 v91, v91\n" \
"v_mfma_f32_16x16x32_bf16 v[172:175], a[172:175], v[72:75], v[172:175]\n" \
"v_cvt_pk_bf16_f32 v88, v88, v89\n" \
"v_cvt_pk_bf16_f32 v89, v90, v91\n" \
"v_mfma_f32_16x16x32_bf16 v[144:147], a[176:179], v[68:71], v[144:147]\n" \
"v_exp_f32_e32 v92, v92\n" \
"v_mfma_f32_16x16x32_bf16 v[176:179], a[176:179], v[72:75], v[176:179]\n" \
"v_exp_f32_e32 v93, v93\n" \
"v_mfma_f32_16x16x32_bf16 v[148:151], a[180:183], v[68:71], v[148:151]\n" \
"v_exp_f32_e32 v94, v94\n" \
"v_mfma_f32_16x16x32_bf16 v[180:183], a[180:183], v[72:75], v[180:183]\n" \
"v_exp_f32_e32 v95, v95\n" \
"v_mfma_f32_16x16x32_bf16 v[152:155], a[184:187], v[68:71], v[152:155]\n" \
"v_cvt_pk_bf16_f32 v90, v92, v93\n" \
"v_cvt_pk_bf16_f32 v91, v94, v95\n" \
"v_mfma_f32_16x16x32_bf16 v[184:187], a[184:187], v[72:75], v[184:187]\n" \
"v_add_u32_e32 v51, 0x12000, v63\n" \
"s_waitcnt lgkmcnt(0)\n" \
"v_mfma_f32_16x16x32_bf16 v[156:159], a[188:191], v[68:71], v[156:159]\n" \
"s_xor_b32 m0, s65, 0xa000\n" \
"ds_read_b32 v58, v18 offset:512\n" \
"v_mfma_f32_16x16x32_bf16 v[188:191], a[188:191], v[72:75], v[188:191]\n" \
"buffer_load_dwordx4 v51, s[8:11], s64 offen offset:0 sc1 lds\n" \
"v_smfmac_f32_16x16x64_bf16 a[192:195], a[200:203], v[68:75], v21\n" \
"buffer_load_dwordx4 v51, s[8:11], s64 offen offset:1024 sc1 lds\n" \
"v_mfma_f32_16x16x32_bf16 v[220:223], a[188:191], v[84:87], v[220:223]\n" \
"ds_read_b32 v59, v18 offset:640\n" \
"ds_read_b32 v60, v18 offset:768\n" \
"v_mfma_f32_16x16x32_bf16 v[252:255], a[188:191], v[88:91], v[252:255]\n" \
"ds_read_b32 v61, v18 offset:896\n" \
"ds_read_b128 a[128:131], v48 offset:0\n" \
"v_mfma_f32_16x16x32_bf16 v[216:219], a[184:187], v[84:87], v[216:219]\n" \
"ds_read_b128 a[144:147], v48 offset:4096\n" \
"ds_read_b128 a[132:135], v48 offset:1024\n" \
"v_mfma_f32_16x16x32_bf16 v[248:251], a[184:187], v[88:91], v[248:251]\n" \
"ds_read_b128 a[148:151], v48 offset:5120\n" \
"ds_read_b128 a[136:139], v48 offset:2048\n" \
"v_mfma_f32_16x16x32_bf16 v[212:215], a[180:183], v[84:87], v[212:215]\n" \
"ds_read_b128 a[152:155], v48 offset:6144\n" \
"ds_read_b128 a[140:143], v48 offset:3072\n" \
"v_mfma_f32_16x16x32_bf16 v[244:247], a[180:183], v[88:91], v[244:247]\n" \
"ds_read_b128 a[156:159], v48 offset:7168\n" \
"v_add_u32_e32 v52, 0x14000, v63\n" \
"v_mfma_f32_16x16x32_bf16 v[208:211], a[176:179], v[84:87], v[208:211]\n" \
"buffer_load_dwordx4 v51, s[8:11], s64 offen offset:2048 sc1 lds\n" \
"v_mfma_f32_16x16x32_bf16 v[240:243], a[176:179], v[88:91], v[240:243]\n" \
"v_exp_f32_e32 v104, v104\n" \
"v_mfma_f32_16x16x32_bf16 v[204:207], a[172:175], v[84:87], v[204:207]\n" \
"v_exp_f32_e32 v105, v105\n" \
"v_mfma_f32_16x16x32_bf16 v[236:239], a[172:175], v[88:91], v[236:239]\n" \
"buffer_load_dwordx4 v51, s[8:11], s64 offen offset:3072 sc1 lds\n" \
"v_mfma_f32_16x16x32_bf16 v[200:203], a[168:171], v[84:87], v[200:203]\n" \
"v_exp_f32_e32 v106, v106\n" \
"v_mfma_f32_16x16x32_bf16 v[232:235], a[168:171], v[88:91], v[232:235]\n" \
"v_exp_f32_e32 v107, v107\n" \
"v_mfma_f32_16x16x32_bf16 v[196:199], a[164:167], v[84:87], v[196:199]\n" \
"v_cvt_pk_bf16_f32 v104, v104, v105\n" \
"v_cvt_pk_bf16_f32 v105, v106, v107\n" \
"v_mfma_f32_16x16x32_bf16 v[228:231], a[164:167], v[88:91], v[228:231]\n" \
"v_exp_f32_e32 v108, v108\n" \
"v_mfma_f32_16x16x32_bf16 v[192:195], a[160:163], v[84:87], v[192:195]\n" \
"v_exp_f32_e32 v109, v109\n" \
"v_mfma_f32_16x16x32_bf16 v[224:227], a[160:163], v[88:91], v[224:227]\n" \
"v_exp_f32_e32 v110, v110\n" \
"v_smfmac_f32_16x16x64_bf16 a[196:199], a[200:203], v[84:91], v21\n" \
"v_exp_f32_e32 v111, v111\n" \
"v_cvt_pk_bf16_f32 v106, v108, v109\n" \
"v_cvt_pk_bf16_f32 v107, v110, v111\n" \
"v_and_b32_e32 v104, v12, v104\n" \
"v_and_b32_e32 v105, v13, v105\n" \
"v_and_b32_e32 v106, v14, v106\n" \
"v_and_b32_e32 v107, v15, v107\n" \
"v_accvgpr_read_b32 v54, a192\n" \
"v_accvgpr_read_b32 v22, a193\n" \
"v_add_u32_e32 v53, 0x20000, v63\n" \
"v_exp_f32_e32 v118, v118\n" \
"v_exp_f32_e32 v119, v119\n" \
"v_exp_f32_e32 v116, v116\n" \
"v_exp_f32_e32 v117, v117\n" \
"v_cvt_pk_bf16_f32 v119, v118, v119\n" \
"v_exp_f32_e32 v114, v114\n" \
"v_exp_f32_e32 v115, v115\n" \
"v_exp_f32_e32 v112, v112\n" \
"v_exp_f32_e32 v113, v113\n" \
"v_cvt_pk_bf16_f32 v118, v116, v117\n" \
"v_cvt_pk_bf16_f32 v117, v114, v115\n" \
"v_cvt_pk_bf16_f32 v116, v112, v113\n" \
"v_readlane_b32 s93, v11, 19\n" \
"s_waitcnt vmcnt(10) lgkmcnt(0)\n" \
"s_barrier\n" \
"v_mfma_f32_16x16x32_bf16 v[80:83], a[128:131], a[32:35], v[40:43]\n" \
"ds_read_b128 a[160:163], v49 offset:8192\n" \
"ds_read_b128 a[164:167], v49 offset:9216\n" \
"v_mfma_f32_16x16x32_bf16 v[80:83], a[132:135], a[36:39], v[80:83]\n" \
"ds_read_b128 a[168:171], v49 offset:10240\n" \
"s_xor_b32 m0, s65, 0x18000\n" \
"v_mfma_f32_16x16x32_bf16 v[80:83], a[136:139], a[40:43], v[80:83]\n" \
"buffer_load_dwordx4 v52, s[8:11], s64 offen offset:0 sc1 lds\n" \
"v_mfma_f32_16x16x32_bf16 v[80:83], a[140:143], a[44:47], v[80:83]\n" \
"ds_read_b128 a[172:175], v49 offset:11264\n" \
"ds_read_b128 a[176:179], v49 offset:12288\n" \
"v_mfma_f32_16x16x32_bf16 v[84:87], a[156:159], a[44:47], v[40:43]\n" \
"ds_read_b128 a[180:183], v49 offset:13312\n" \
"ds_read_b128 a[184:187], v49 offset:14336\n" \
"v_mfma_f32_16x16x32_bf16 v[84:87], a[152:155], a[40:43], v[84:87]\n" \
"buffer_load_dwordx4 v52, s[8:11], s64 offen offset:1024 sc1 lds\n" \
"v_mfma_f32_16x16x32_bf16 v[84:87], a[148:151], a[36:39], v[84:87]\n" \
"ds_read_b128 a[188:191], v49 offset:15360\n" \
"v_readlane_b32 s80, v11, 11\n" \
"v_mfma_f32_16x16x32_bf16 v[84:87], a[144:147], a[32:35], v[84:87]\n" \
"buffer_load_dwordx4 v52, s[8:11], s64 offen offset:2048 sc1 lds\n" \
"v_mfma_f32_16x16x32_bf16 v[92:95], a[144:147], a[48:51], v[44:47]\n" \
"v_exp_f32_e32 v120, v120\n" \
"v_mfma_f32_16x16x32_bf16 v[92:95], a[148:151], a[52:55], v[92:95]\n" \
"v_exp_f32_e32 v121, v121\n" \
"v_mfma_f32_16x16x32_bf16 v[92:95], a[152:155], a[56:59], v[92:95]\n" \
"buffer_load_dwordx4 v52, s[8:11], s64 offen offset:3072 sc1 lds\n" \
"v_mfma_f32_16x16x32_bf16 v[92:95], a[156:159], a[60:63], v[92:95]\n" \
"s_mov_b32 m0, s65\n" \
"v_cvt_pk_bf16_f32 v120, v120, v121\n" \
"v_mfma_f32_16x16x32_bf16 v[88:91], a[140:143], a[60:63], v[44:47]\n" \
"v_exp_f32_e32 v122, v122\n" \
"v_mfma_f32_16x16x32_bf16 v[88:91], a[136:139], a[56:59], v[88:91]\n" \
"buffer_load_dwordx4 v53, s[8:11], s64 offen offset:0 sc1 lds\n" \
"v_mfma_f32_16x16x32_bf16 v[88:91], a[132:135], a[52:55], v[88:91]\n" \
"v_exp_f32_e32 v123, v123\n" \
"v_mfma_f32_16x16x32_bf16 v[88:91], a[128:131], a[48:51], v[88:91]\n" \
"v_exp_f32_e32 v124, v124\n" \
"v_mfma_f32_16x16x32_bf16 a[192:195], a[212:215], v[104:107], a[192:195]\n" \
"v_cvt_pk_bf16_f32 v121, v122, v123\n" \
"s_waitcnt lgkmcnt(5)\n" \
"v_mfma_f32_16x16x32_bf16 v[160:163], a[160:163], v[104:107], v[160:163]\n" \
"buffer_load_dwordx4 v53, s[8:11], s64 offen offset:1024 sc1 lds\n" \
"v_mfma_f32_16x16x32_bf16 v[164:167], a[164:167], v[104:107], v[164:167]\n" \
"v_exp_f32_e32 v125, v125\n" \
"v_mfma_f32_16x16x32_bf16 v[168:171], a[168:171], v[104:107], v[168:171]\n" \
"v_cvt_pk_bf16_f32 v122, v124, v125\n" \
"s_waitcnt lgkmcnt(1)\n" \
"v_mfma_f32_16x16x32_bf16 v[172:175], a[172:175], v[104:107], v[172:175]\n" \
"v_exp_f32_e32 v126, v126\n" \
"v_mfma_f32_16x16x32_bf16 v[176:179], a[176:179], v[104:107], v[176:179]\n" \
"v_exp_f32_e32 v127, v127\n" \
"v_mfma_f32_16x16x32_bf16 v[180:183], a[180:183], v[104:107], v[180:183]\n" \
"v_cvt_pk_bf16_f32 v123, v126, v127\n" \
"v_readlane_b32 s81, v11, 12\n" \
"v_mfma_f32_16x16x32_bf16 v[184:187], a[184:187], v[104:107], v[184:187]\n" \
"s_mov_b32 s82, 0x47fca0\n" \
"s_waitcnt lgkmcnt(0)\n" \
"v_mfma_f32_16x16x32_bf16 v[188:191], a[188:191], v[104:107], v[188:191]\n" \
"buffer_load_dwordx4 v53, s[8:11], s64 offen offset:2048 sc1 lds\n" \
"v_mfma_f32_16x16x32_bf16 v[220:223], a[188:191], v[116:119], v[220:223]\n" \
"buffer_load_dwordx4 v53, s[8:11], s64 offen offset:3072 sc1 lds\n" \
"v_mfma_f32_16x16x32_bf16 v[252:255], a[188:191], v[120:123], v[252:255]\n" \
"s_mov_b32 s83, 0x20000\n" \
"v_readlane_b32 s92, v11, 25\n" \
"v_mfma_f32_16x16x32_bf16 v[216:219], a[184:187], v[116:119], v[216:219]\n" \
"s_add_u32 m0, s93, 0x0\n" \
"ds_read_b128 a[128:131], v48 offset:8192\n" \
"v_mfma_f32_16x16x32_bf16 v[248:251], a[184:187], v[120:123], v[248:251]\n" \
"ds_read_b128 a[144:147], v48 offset:12288\n" \
"ds_read_b128 a[132:135], v48 offset:9216\n" \
"v_mfma_f32_16x16x32_bf16 v[212:215], a[180:183], v[116:119], v[212:215]\n" \
"buffer_load_dwordx4 v0, s[80:83], s92 offen offset:0 lds\n" \
"v_mfma_f32_16x16x32_bf16 v[244:247], a[180:183], v[120:123], v[244:247]\n" \
"buffer_load_dwordx4 v1, s[80:83], s92 offen offset:1024 lds\n" \
"v_mfma_f32_16x16x32_bf16 v[208:211], a[176:179], v[116:119], v[208:211]\n" \
"ds_read_b128 a[148:151], v48 offset:13312\n" \
"ds_read_b128 a[136:139], v48 offset:10240\n" \
"v_mfma_f32_16x16x32_bf16 v[240:243], a[176:179], v[120:123], v[240:243]\n" \
"ds_read_b128 a[152:155], v48 offset:14336\n" \
"ds_read_b128 a[140:143], v48 offset:11264\n" \
"v_mfma_f32_16x16x32_bf16 v[204:207], a[172:175], v[116:119], v[204:207]\n" \
"buffer_load_dwordx4 v2, s[80:83], s92 offen offset:2048 lds\n" \
"v_mfma_f32_16x16x32_bf16 v[236:239], a[172:175], v[120:123], v[236:239]\n" \
"ds_read_b128 a[156:159], v48 offset:15360\n" \
"s_xor_b32 s65, s65, 0x8000\n" \
"v_mfma_f32_16x16x32_bf16 v[200:203], a[168:171], v[116:119], v[200:203]\n" \
"s_add_u32 s64, s64, 0x10000\n" \
"v_add_u32_e32 v49, 0x10000, v48\n" \
"v_mfma_f32_16x16x32_bf16 v[232:235], a[168:171], v[120:123], v[232:235]\n" \
"buffer_load_dwordx4 v3, s[80:83], s92 offen offset:3072 lds\n" \
"v_mfma_f32_16x16x32_bf16 v[196:199], a[164:167], v[116:119], v[196:199]\n" \
"v_exp_f32_e32 v86, v86\n" \
"v_mfma_f32_16x16x32_bf16 v[228:231], a[164:167], v[120:123], v[228:231]\n" \
"v_exp_f32_e32 v87, v87\n" \
"v_mfma_f32_16x16x32_bf16 v[192:195], a[160:163], v[116:119], v[192:195]\n" \
"v_exp_f32_e32 v84, v84\n" \
"v_mfma_f32_16x16x32_bf16 v[224:227], a[160:163], v[120:123], v[224:227]\n" \
"v_exp_f32_e32 v85, v85\n" \
"v_smfmac_f32_16x16x64_bf16 a[196:199], a[200:203], v[116:123], v21\n" \
"v_cvt_pk_bf16_f32 v87, v86, v87\n" \
"v_exp_f32_e32 v82, v82\n" \
"v_exp_f32_e32 v83, v83\n" \
"v_exp_f32_e32 v80, v80\n" \
"v_exp_f32_e32 v81, v81\n" \
"v_cvt_pk_bf16_f32 v86, v84, v85\n" \
"v_cvt_pk_bf16_f32 v85, v82, v83\n" \
"v_cvt_pk_bf16_f32 v84, v80, v81\n" \
"v_and_b32_e32 v84, v12, v84\n" \
"v_and_b32_e32 v85, v13, v85\n" \
"v_and_b32_e32 v86, v14, v86\n" \
"v_and_b32_e32 v87, v15, v87\n" \
"v_exp_f32_e32 v88, v88\n" \
"v_exp_f32_e32 v89, v89\n" \
"v_exp_f32_e32 v90, v90\n" \
"v_exp_f32_e32 v91, v91\n" \
"v_cvt_pk_bf16_f32 v88, v88, v89\n" \
"v_exp_f32_e32 v92, v92\n" \
"v_exp_f32_e32 v93, v93\n" \
"v_exp_f32_e32 v94, v94\n" \
"v_exp_f32_e32 v95, v95\n" \
"v_cvt_pk_bf16_f32 v89, v90, v91\n" \
"v_cvt_pk_bf16_f32 v90, v92, v93\n" \
"v_cvt_pk_bf16_f32 v91, v94, v95\n" \
"v_add_u32_e32 v48, s16, v20\n" \
"v_add_f32_e32 v54, v54, v22\n" \
"v_accvgpr_read_b32 v55, a194\n" \
"v_accvgpr_read_b32 v23, a195\n" \
"v_mfma_f32_16x16x32_bf16 a[192:195], a[216:219], a[216:219], 0\n" \
"v_max_f32_e32 v62, v62, v54\n" \
"v_add_f32_e32 v55, v55, v23\n" \
"v_max_f32_e32 v62, v62, v55\n" \
"v_rcp_f32_e32 v54, v54\n" \
"v_rcp_f32_e32 v55, v55\n" \
"v_mul_f32_e32 v54, v54, v58\n" \
"v_mul_f32_e32 v55, v55, v59\n" \
"v_mul_f32_e32 v128, v54, v128\n" \
"v_mul_f32_e32 v129, v54, v129\n" \
"v_mul_f32_e32 v136, v54, v136\n" \
"v_mul_f32_e32 v137, v54, v137\n" \
"v_mul_f32_e32 v144, v54, v144\n" \
"v_mul_f32_e32 v145, v54, v145\n" \
"v_mul_f32_e32 v152, v54, v152\n" \
"v_mul_f32_e32 v153, v54, v153\n" \
"v_mul_f32_e32 v160, v55, v160\n" \
"v_mul_f32_e32 v161, v55, v161\n" \
"v_mul_f32_e32 v168, v55, v168\n" \
"v_mul_f32_e32 v169, v55, v169\n" \
"v_mul_f32_e32 v176, v55, v176\n" \
"v_mul_f32_e32 v177, v55, v177\n" \
"v_mul_f32_e32 v184, v55, v184\n" \
"v_mul_f32_e32 v185, v55, v185\n" \
"v_cvt_pk_bf16_f32 v128, v128, v129\n" \
"v_mul_f32_e32 v130, v54, v130\n" \
"v_mul_f32_e32 v131, v54, v131\n" \
"v_cvt_pk_bf16_f32 v136, v136, v137\n" \
"v_mul_f32_e32 v138, v54, v138\n" \
"v_mul_f32_e32 v139, v54, v139\n" \
"v_cvt_pk_bf16_f32 v144, v144, v145\n" \
"v_mul_f32_e32 v146, v54, v146\n" \
"v_mul_f32_e32 v147, v54, v147\n" \
"v_cvt_pk_bf16_f32 v152, v152, v153\n" \
"v_mul_f32_e32 v154, v54, v154\n" \
"v_mul_f32_e32 v155, v54, v155\n" \
"v_cvt_pk_bf16_f32 v160, v160, v161\n" \
"v_mul_f32_e32 v162, v55, v162\n" \
"v_mul_f32_e32 v163, v55, v163\n" \
"v_cvt_pk_bf16_f32 v168, v168, v169\n" \
"v_mul_f32_e32 v170, v55, v170\n" \
"v_mul_f32_e32 v171, v55, v171\n" \
"v_cvt_pk_bf16_f32 v176, v176, v177\n" \
"v_mul_f32_e32 v178, v55, v178\n" \
"v_mul_f32_e32 v179, v55, v179\n" \
"v_cvt_pk_bf16_f32 v184, v184, v185\n" \
"v_mul_f32_e32 v186, v55, v186\n" \
"v_mul_f32_e32 v187, v55, v187\n" \
"v_cvt_pk_bf16_f32 v129, v130, v131\n" \
"v_mul_f32_e32 v132, v54, v132\n" \
"v_mul_f32_e32 v133, v54, v133\n" \
"v_mul_f32_e32 v134, v54, v134\n" \
"v_mul_f32_e32 v135, v54, v135\n" \
"v_cvt_pk_bf16_f32 v137, v138, v139\n" \
"v_mul_f32_e32 v140, v54, v140\n" \
"s_waitcnt vmcnt(12) lgkmcnt(0)\n" \
"s_barrier\n" \
"v_mfma_f32_16x16x32_bf16 v[120:123], a[128:131], a[48:51], v[44:47]\n" \
"s_xor_b32 m0, s65, 0x12000\n" \
"ds_read_b128 a[188:191], v49 offset:7168\n" \
"v_mfma_f32_16x16x32_bf16 v[120:123], a[132:135], a[52:55], v[120:123]\n" \
"buffer_load_dwordx4 v50, s[8:11], s64 offen offset:0 sc1 lds\n" \
"v_mfma_f32_16x16x32_bf16 v[120:123], a[136:139], a[56:59], v[120:123]\n" \
"ds_read_b128 a[184:187], v49 offset:6144\n" \
"ds_read_b128 a[180:183], v49 offset:5120\n" \
"v_mfma_f32_16x16x32_bf16 v[120:123], a[140:143], a[60:63], v[120:123]\n" \
"buffer_load_dwordx4 v50, s[8:11], s64 offen offset:1024 sc1 lds\n" \
"v_mfma_f32_16x16x32_bf16 v[124:127], a[156:159], a[60:63], v[44:47]\n" \
"ds_read_b128 a[176:179], v49 offset:4096\n" \
"ds_read_b128 a[172:175], v49 offset:3072\n" \
"v_mfma_f32_16x16x32_bf16 v[124:127], a[152:155], a[56:59], v[124:127]\n" \
"ds_read_b128 a[168:171], v49 offset:2048\n" \
"ds_read_b128 a[164:167], v49 offset:1024\n" \
"v_mfma_f32_16x16x32_bf16 v[124:127], a[148:151], a[52:55], v[124:127]\n" \
"ds_read_b128 a[160:163], v49 offset:0\n" \
"ds_read_b128 a[0:3], v49 offset:8192\n" \
"v_mfma_f32_16x16x32_bf16 v[124:127], a[144:147], a[48:51], v[124:127]\n" \
"buffer_load_dwordx4 v50, s[8:11], s64 offen offset:2048 sc1 lds\n" \
"v_mfma_f32_16x16x32_bf16 v[32:35], a[220:223], a[64:67], 0\n" \
"ds_read_b128 a[4:7], v49 offset:9216\n" \
"ds_read_b128 a[8:11], v49 offset:10240\n" \
"v_mfma_f32_16x16x32_bf16 v[36:39], a[220:223], a[80:83], 0\n" \
"ds_read_b128 a[12:15], v49 offset:11264\n" \
"ds_read_b128 a[16:19], v49 offset:12288\n" \
"v_mfma_f32_16x16x32_bf16 v[40:43], a[220:223], a[96:99], 0\n" \
"ds_read_b128 a[20:23], v49 offset:13312\n" \
"ds_read_b128 a[24:27], v49 offset:14336\n" \
"v_mfma_f32_16x16x32_bf16 v[44:47], a[220:223], a[112:115], 0\n" \
"buffer_load_dwordx4 v50, s[8:11], s64 offen offset:3072 sc1 lds\n" \
"v_mfma_f32_16x16x32_bf16 v[32:35], a[224:227], a[68:71], v[32:35]\n" \
"s_xor_b32 m0, s65, 0xa000\n" \
"ds_read_b128 a[28:31], v49 offset:15360\n" \
"v_mfma_f32_16x16x32_bf16 v[36:39], a[224:227], a[84:87], v[36:39]\n" \
"v_exp_f32_e32 v120, v120\n" \
"v_mfma_f32_16x16x32_bf16 v[40:43], a[224:227], a[100:103], v[40:43]\n" \
"buffer_load_dwordx4 v51, s[8:11], s64 offen offset:0 sc1 lds\n" \
"v_mfma_f32_16x16x32_bf16 v[44:47], a[224:227], a[116:119], v[44:47]\n" \
"v_exp_f32_e32 v121, v121\n" \
"v_mfma_f32_16x16x32_bf16 v[32:35], a[228:231], a[72:75], v[32:35]\n" \
"v_exp_f32_e32 v122, v122\n" \
"v_mfma_f32_16x16x32_bf16 v[36:39], a[228:231], a[88:91], v[36:39]\n" \
"v_exp_f32_e32 v123, v123\n" \
"v_mfma_f32_16x16x32_bf16 v[40:43], a[228:231], a[104:107], v[40:43]\n" \
"buffer_load_dwordx4 v51, s[8:11], s64 offen offset:1024 sc1 lds\n" \
"v_mfma_f32_16x16x32_bf16 v[44:47], a[228:231], a[120:123], v[44:47]\n" \
"v_cvt_pk_bf16_f32 v120, v120, v121\n" \
"v_cvt_pk_bf16_f32 v121, v122, v123\n" \
"v_mfma_f32_16x16x32_bf16 v[32:35], a[232:235], a[76:79], v[32:35]\n" \
"v_exp_f32_e32 v124, v124\n" \
"v_mfma_f32_16x16x32_bf16 v[36:39], a[232:235], a[92:95], v[36:39]\n" \
"v_exp_f32_e32 v125, v125\n" \
"v_mfma_f32_16x16x32_bf16 v[40:43], a[232:235], a[108:111], v[40:43]\n" \
"buffer_load_dwordx4 v51, s[8:11], s64 offen offset:2048 sc1 lds\n" \
"v_mfma_f32_16x16x32_bf16 v[44:47], a[232:235], a[124:127], v[44:47]\n" \
"v_cvt_pk_bf16_f32 v122, v124, v125\n" \
"s_waitcnt lgkmcnt(3)\n" \
"v_mfma_f32_16x16x32_bf16 v[220:223], a[188:191], v[84:87], v[220:223]\n" \
"v_exp_f32_e32 v126, v126\n" \
"v_mfma_f32_16x16x32_bf16 v[252:255], a[188:191], v[88:91], v[252:255]\n" \
"buffer_load_dwordx4 v51, s[8:11], s64 offen offset:3072 sc1 lds\n" \
"v_mfma_f32_16x16x32_bf16 v[216:219], a[184:187], v[84:87], v[216:219]\n" \
"s_add_u32 m0, s93, 0x1000\n" \
"v_and_b32_e32 v120, v12, v120\n" \
"v_mfma_f32_16x16x32_bf16 v[248:251], a[184:187], v[88:91], v[248:251]\n" \
"v_exp_f32_e32 v127, v127\n" \
"v_mfma_f32_16x16x32_bf16 v[212:215], a[180:183], v[84:87], v[212:215]\n" \
"v_cvt_pk_bf16_f32 v123, v126, v127\n" \
"v_and_b32_e32 v121, v13, v121\n" \
"v_mfma_f32_16x16x32_bf16 v[244:247], a[180:183], v[88:91], v[244:247]\n" \
"buffer_load_dwordx4 v4, s[80:83], s92 offen offset:0 lds\n" \
"v_mfma_f32_16x16x32_bf16 v[208:211], a[176:179], v[84:87], v[208:211]\n" \
"v_and_b32_e32 v122, v14, v122\n" \
"v_and_b32_e32 v123, v15, v123\n" \
"v_mfma_f32_16x16x32_bf16 v[240:243], a[176:179], v[88:91], v[240:243]\n" \
"ds_read_b128 a[128:131], v48 offset:0\n" \
"ds_read_b128 a[144:147], v48 offset:4096\n" \
"v_mfma_f32_16x16x32_bf16 v[204:207], a[172:175], v[84:87], v[204:207]\n" \
"ds_read_b128 a[132:135], v48 offset:1024\n" \
"ds_read_b128 a[148:151], v48 offset:5120\n" \
"v_mfma_f32_16x16x32_bf16 v[236:239], a[172:175], v[88:91], v[236:239]\n" \
"buffer_load_dwordx4 v5, s[80:83], s92 offen offset:1024 lds\n" \
"v_mfma_f32_16x16x32_bf16 v[200:203], a[168:171], v[84:87], v[200:203]\n" \
"ds_read_b128 a[136:139], v48 offset:2048\n" \
"ds_read_b128 a[152:155], v48 offset:6144\n" \
"v_mfma_f32_16x16x32_bf16 v[232:235], a[168:171], v[88:91], v[232:235]\n" \
"ds_read_b128 a[140:143], v48 offset:3072\n" \
"ds_read_b128 a[156:159], v48 offset:7168\n" \
"v_mfma_f32_16x16x32_bf16 v[196:199], a[164:167], v[84:87], v[196:199]\n" \
"buffer_load_dwordx4 v6, s[80:83], s92 offen offset:2048 lds\n" \
"v_mfma_f32_16x16x32_bf16 v[228:231], a[164:167], v[88:91], v[228:231]\n" \
"v_mul_f32_e32 v141, v54, v141\n" \
"v_mul_f32_e32 v142, v54, v142\n" \
"v_mfma_f32_16x16x32_bf16 v[192:195], a[160:163], v[84:87], v[192:195]\n" \
"v_mul_f32_e32 v143, v54, v143\n" \
"v_cvt_pk_bf16_f32 v145, v146, v147\n" \
"v_mfma_f32_16x16x32_bf16 v[224:227], a[160:163], v[88:91], v[224:227]\n" \
"v_mul_f32_e32 v148, v54, v148\n" \
"v_mul_f32_e32 v149, v54, v149\n" \
"v_smfmac_f32_16x16x64_bf16 a[196:199], a[200:203], v[84:91], v21\n" \
"buffer_load_dwordx4 v7, s[80:83], s92 offen offset:3072 lds\n" \
"v_mul_f32_e32 v150, v54, v150\n" \
"v_mul_f32_e32 v151, v54, v151\n" \
"v_cvt_pk_bf16_f32 v153, v154, v155\n" \
"v_mul_f32_e32 v156, v54, v156\n" \
"v_mul_f32_e32 v157, v54, v157\n" \
"v_mfma_f32_16x16x32_bf16 a[196:199], a[212:215], v[120:123], a[196:199]\n" \
"v_mul_f32_e32 v158, v54, v158\n" \
"v_mul_f32_e32 v159, v54, v159\n" \
"v_mfma_f32_16x16x32_bf16 v[224:227], a[0:3], v[120:123], v[224:227]\n" \
"v_cvt_pk_bf16_f32 v161, v162, v163\n" \
"v_mul_f32_e32 v164, v55, v164\n" \
"v_mfma_f32_16x16x32_bf16 v[228:231], a[4:7], v[120:123], v[228:231]\n" \
"v_mul_f32_e32 v165, v55, v165\n" \
"v_mul_f32_e32 v166, v55, v166\n" \
"v_mfma_f32_16x16x32_bf16 v[232:235], a[8:11], v[120:123], v[232:235]\n" \
"v_mul_f32_e32 v167, v55, v167\n" \
"v_cvt_pk_bf16_f32 v169, v170, v171\n" \
"v_mfma_f32_16x16x32_bf16 v[236:239], a[12:15], v[120:123], v[236:239]\n" \
"v_mul_f32_e32 v172, v55, v172\n" \
"v_mul_f32_e32 v173, v55, v173\n" \
"v_mfma_f32_16x16x32_bf16 v[240:243], a[16:19], v[120:123], v[240:243]\n" \
"v_mul_f32_e32 v174, v55, v174\n" \
"s_waitcnt lgkmcnt(4)\n" \
"v_mfma_f32_16x16x32_bf16 v[244:247], a[20:23], v[120:123], v[244:247]\n" \
"v_mul_f32_e32 v175, v55, v175\n" \
"v_cvt_pk_bf16_f32 v177, v178, v179\n" \
"v_mfma_f32_16x16x32_bf16 v[248:251], a[24:27], v[120:123], v[248:251]\n" \
"v_mul_f32_e32 v180, v55, v180\n" \
"v_mul_f32_e32 v181, v55, v181\n" \
"v_mfma_f32_16x16x32_bf16 v[252:255], a[28:31], v[120:123], v[252:255]\n" \
"v_mul_f32_e32 v182, v55, v182\n" \
"v_mul_f32_e32 v183, v55, v183\n" \
"v_cvt_pk_bf16_f32 v185, v186, v187\n" \
"v_mul_f32_e32 v188, v55, v188\n" \
"v_mul_f32_e32 v189, v55, v189\n" \
"v_mul_f32_e32 v190, v55, v190\n" \
"v_mul_f32_e32 v191, v55, v191\n" \
"v_cvt_pk_bf16_f32 v130, v132, v133\n" \
"v_cvt_pk_bf16_f32 v131, v134, v135\n" \
"buffer_store_dwordx4 v[128:131], v20, s[56:59], s68 offen offset:0 sc1\n" \
"v_cvt_pk_bf16_f32 v138, v140, v141\n" \
"v_cvt_pk_bf16_f32 v139, v142, v143\n" \
"buffer_store_dwordx4 v[136:139], v20, s[56:59], s68 offen offset:1024 sc1\n" \
"v_cvt_pk_bf16_f32 v146, v148, v149\n" \
"v_cvt_pk_bf16_f32 v147, v150, v151\n" \
"buffer_store_dwordx4 v[144:147], v20, s[56:59], s68 offen offset:2048 sc1\n" \
"v_cvt_pk_bf16_f32 v154, v156, v157\n" \
"v_cvt_pk_bf16_f32 v155, v158, v159\n" \
"buffer_store_dwordx4 v[152:155], v20, s[56:59], s68 offen offset:3072 sc1\n" \
"v_cvt_pk_bf16_f32 v162, v164, v165\n" \
"v_cvt_pk_bf16_f32 v163, v166, v167\n" \
"buffer_store_dwordx4 v[160:163], v20, s[56:59], s69 offen offset:0 sc1\n" \
"v_cvt_pk_bf16_f32 v170, v172, v173\n" \
"v_cvt_pk_bf16_f32 v171, v174, v175\n" \
"buffer_store_dwordx4 v[168:171], v20, s[56:59], s69 offen offset:1024 sc1\n" \
"v_cvt_pk_bf16_f32 v178, v180, v181\n" \
"v_cvt_pk_bf16_f32 v179, v182, v183\n" \
"buffer_store_dwordx4 v[176:179], v20, s[56:59], s69 offen offset:2048 sc1\n" \
"v_cvt_pk_bf16_f32 v186, v188, v189\n" \
"v_cvt_pk_bf16_f32 v187, v190, v191\n" \
"buffer_store_dwordx4 v[184:187], v20, s[56:59], s69 offen offset:3072 sc1\n" \
"s_add_u32 m0, s93, 0x2000\n" \
"v_mfma_f32_16x16x32_bf16 a[208:211], a[216:219], v[128:131], a[208:211]\n" \
"buffer_load_dwordx4 v8, s[80:83], s92 offen offset:0 lds\n" \
"v_mfma_f32_16x16x32_bf16 a[208:211], a[216:219], v[136:139], a[208:211]\n" \
"v_mfma_f32_32x32x16_bf16 v[128:143], a[216:219], a[216:219], 0\n" \
"v_mfma_f32_16x16x32_bf16 a[208:211], a[216:219], v[144:147], a[208:211]\n" \
"v_mfma_f32_16x16x32_bf16 a[208:211], a[216:219], v[152:155], a[208:211]\n" \
"v_mfma_f32_32x32x16_bf16 v[144:159], a[216:219], a[216:219], 0\n" \
"v_accvgpr_read_b32 v56, a196\n" \
"v_accvgpr_read_b32 v24, a197\n" \
"v_add_f32_e32 v56, v56, v24\n" \
"v_mfma_f32_16x16x32_bf16 a[208:211], a[216:219], v[160:163], a[208:211]\n" \
"v_max_f32_e32 v62, v62, v56\n" \
"v_accvgpr_read_b32 v57, a198\n" \
"s_waitcnt vmcnt(21) lgkmcnt(0)\n" \
"s_barrier\n" \
"v_mfma_f32_16x16x32_bf16 v[64:67], a[128:131], a[64:67], v[32:35]\n" \
"s_xor_b32 m0, s65, 0x18000\n" \
"v_accvgpr_read_b32 v25, a199\n" \
"v_mfma_f32_16x16x32_bf16 a[196:199], a[216:219], a[216:219], 0\n" \
"buffer_load_dwordx4 v52, s[8:11], s64 offen offset:0 sc1 lds\n" \
"v_mfma_f32_16x16x32_bf16 v[64:67], a[132:135], a[68:71], v[64:67]\n" \
"v_rcp_f32_e32 v56, v56\n" \
"v_mfma_f32_16x16x32_bf16 v[64:67], a[136:139], a[72:75], v[64:67]\n" \
"v_mfma_f32_16x16x32_bf16 a[208:211], a[216:219], v[168:171], a[208:211]\n" \
"v_mfma_f32_32x32x16_bf16 v[160:175], a[216:219], a[216:219], 0\n" \
"v_mul_f32_e32 v56, v56, v60\n" \
"v_mfma_f32_16x16x32_bf16 a[208:211], a[216:219], v[176:179], a[208:211]\n" \
"v_mul_f32_e32 v192, v56, v192\n" \
"v_mul_f32_e32 v193, v56, v193\n" \
"v_mfma_f32_16x16x32_bf16 v[64:67], a[140:143], a[76:79], v[64:67]\n" \
"buffer_load_dwordx4 v52, s[8:11], s64 offen offset:1024 sc1 lds\n" \
"v_mfma_f32_16x16x32_bf16 v[68:71], a[156:159], a[76:79], v[32:35]\n" \
"v_cvt_pk_bf16_f32 v192, v192, v193\n" \
"v_mul_f32_e32 v194, v56, v194\n" \
"v_mfma_f32_16x16x32_bf16 v[68:71], a[152:155], a[72:75], v[68:71]\n" \
"v_mul_f32_e32 v195, v56, v195\n" \
"v_cvt_pk_bf16_f32 v193, v194, v195\n" \
"v_mfma_f32_16x16x32_bf16 v[68:71], a[148:151], a[68:71], v[68:71]\n" \
"v_mfma_f32_16x16x32_bf16 a[208:211], a[216:219], v[184:187], a[208:211]\n" \
"v_mfma_f32_32x32x16_bf16 v[176:191], a[216:219], a[216:219], 0\n" \
"v_mul_f32_e32 v196, v56, v196\n" \
"v_mul_f32_e32 v197, v56, v197\n" \
"v_mul_f32_e32 v198, v56, v198\n" \
"v_mul_f32_e32 v199, v56, v199\n" \
"v_mul_f32_e32 v200, v56, v200\n" \
"v_mul_f32_e32 v201, v56, v201\n" \
"v_mfma_f32_16x16x32_bf16 v[68:71], a[144:147], a[64:67], v[68:71]\n" \
"buffer_load_dwordx4 v52, s[8:11], s64 offen offset:2048 sc1 lds\n" \
"v_mfma_f32_16x16x32_bf16 v[76:79], a[144:147], a[80:83], v[36:39]\n" \
"v_cvt_pk_bf16_f32 v194, v196, v197\n" \
"v_cvt_pk_bf16_f32 v195, v198, v199\n" \
"buffer_store_dwordx4 v[192:195], v20, s[56:59], s70 offen offset:0 sc1\n" \
"v_mfma_f32_16x16x32_bf16 v[76:79], a[148:151], a[84:87], v[76:79]\n" \
"v_cvt_pk_bf16_f32 v200, v200, v201\n" \
"v_mul_f32_e32 v202, v56, v202\n" \
"v_mfma_f32_16x16x32_bf16 v[76:79], a[152:155], a[88:91], v[76:79]\n" \
"v_mul_f32_e32 v203, v56, v203\n" \
"v_add_f32_e32 v57, v57, v25\n" \
"v_mfma_f32_16x16x32_bf16 v[76:79], a[156:159], a[92:95], v[76:79]\n" \
"buffer_load_dwordx4 v52, s[8:11], s64 offen offset:3072 sc1 lds\n" \
"v_mfma_f32_16x16x32_bf16 v[72:75], a[140:143], a[92:95], v[36:39]\n" \
"s_mov_b32 m0, s65\n" \
"v_cvt_pk_bf16_f32 v201, v202, v203\n" \
"v_mfma_f32_16x16x32_bf16 v[72:75], a[136:139], a[88:91], v[72:75]\n" \
"v_mfma_f32_16x16x32_bf16 a[208:211], a[216:219], v[192:195], a[208:211]\n" \
"v_mul_f32_e32 v204, v56, v204\n" \
"v_mul_f32_e32 v205, v56, v205\n" \
"v_mfma_f32_16x16x32_bf16 v[72:75], a[132:135], a[84:87], v[72:75]\n" \
"v_mul_f32_e32 v206, v56, v206\n" \
"v_mul_f32_e32 v207, v56, v207\n" \
"v_mfma_f32_16x16x32_bf16 v[72:75], a[128:131], a[80:83], v[72:75]\n" \
"buffer_load_dwordx4 v53, s[8:11], s64 offen offset:0 sc1 lds\n" \
"v_mfma_f32_16x16x32_bf16 v[80:83], a[128:131], a[96:99], v[40:43]\n" \
"v_mul_f32_e32 v208, v56, v208\n" \
"v_mul_f32_e32 v209, v56, v209\n" \
"v_mfma_f32_16x16x32_bf16 v[80:83], a[132:135], a[100:103], v[80:83]\n" \
"v_max_f32_e32 v62, v62, v57\n" \
"v_cvt_pk_bf16_f32 v202, v204, v205\n" \
"v_mfma_f32_16x16x32_bf16 v[80:83], a[136:139], a[104:107], v[80:83]\n" \
"buffer_load_dwordx4 v53, s[8:11], s64 offen offset:1024 sc1 lds\n" \
"v_mfma_f32_16x16x32_bf16 v[80:83], a[140:143], a[108:111], v[80:83]\n" \
"v_cvt_pk_bf16_f32 v203, v206, v207\n" \
"v_cvt_pk_bf16_f32 v208, v208, v209\n" \
"buffer_store_dwordx4 v[200:203], v20, s[56:59], s70 offen offset:1024 sc1\n" \
"v_mfma_f32_16x16x32_bf16 v[84:87], a[156:159], a[108:111], v[40:43]\n" \
"v_mul_f32_e32 v210, v56, v210\n" \
"v_mul_f32_e32 v211, v56, v211\n" \
"v_mfma_f32_16x16x32_bf16 v[84:87], a[152:155], a[104:107], v[84:87]\n" \
"v_rcp_f32_e32 v57, v57\n" \
"v_mfma_f32_16x16x32_bf16 v[84:87], a[148:151], a[100:103], v[84:87]\n" \
"buffer_load_dwordx4 v53, s[8:11], s64 offen offset:2048 sc1 lds\n" \
"v_mfma_f32_16x16x32_bf16 v[84:87], a[144:147], a[96:99], v[84:87]\n" \
"v_mfma_f32_16x16x32_bf16 a[208:211], a[216:219], v[200:203], a[208:211]\n" \
"v_mfma_f32_32x32x16_bf16 v[192:207], a[216:219], a[216:219], 0\n" \
"v_cvt_pk_bf16_f32 v209, v210, v211\n" \
"v_mul_f32_e32 v212, v56, v212\n" \
"v_mul_f32_e32 v213, v56, v213\n" \
"v_mul_f32_e32 v214, v56, v214\n" \
"v_mul_f32_e32 v215, v56, v215\n" \
"v_mul_f32_e32 v216, v56, v216\n" \
"v_mfma_f32_16x16x32_bf16 v[92:95], a[144:147], a[112:115], v[44:47]\n" \
"ds_read_b128 a[144:147], v48 offset:12288\n" \
"v_mul_f32_e32 v217, v56, v217\n" \
"v_mfma_f32_16x16x32_bf16 v[92:95], a[148:151], a[116:119], v[92:95]\n" \
"ds_read_b128 a[148:151], v48 offset:13312\n" \
"v_cvt_pk_bf16_f32 v210, v212, v213\n" \
"v_mfma_f32_16x16x32_bf16 v[92:95], a[152:155], a[120:123], v[92:95]\n" \
"ds_read_b128 a[152:155], v48 offset:14336\n" \
"v_cvt_pk_bf16_f32 v211, v214, v215\n" \
"buffer_store_dwordx4 v[208:211], v20, s[56:59], s70 offen offset:2048 sc1\n" \
"v_mfma_f32_16x16x32_bf16 v[92:95], a[156:159], a[124:127], v[92:95]\n" \
"ds_read_b128 a[156:159], v48 offset:15360\n" \
"v_cvt_pk_bf16_f32 v216, v216, v217\n" \
"v_mfma_f32_16x16x32_bf16 v[88:91], a[140:143], a[124:127], v[44:47]\n" \
"ds_read_b128 a[140:143], v48 offset:11264\n" \
"v_mul_f32_e32 v218, v56, v218\n" \
"v_mfma_f32_16x16x32_bf16 v[88:91], a[136:139], a[120:123], v[88:91]\n" \
"ds_read_b128 a[136:139], v48 offset:10240\n" \
"v_mul_f32_e32 v219, v56, v219\n" \
"v_mfma_f32_16x16x32_bf16 v[88:91], a[132:135], a[116:119], v[88:91]\n" \
"ds_read_b128 a[132:135], v48 offset:9216\n" \
"v_mul_f32_e32 v57, v57, v61\n" \
"v_mfma_f32_16x16x32_bf16 v[88:91], a[128:131], a[112:115], v[88:91]\n" \
"ds_read_b128 a[128:131], v48 offset:8192\n" \
"v_mfma_f32_16x16x32_bf16 a[208:211], a[216:219], v[208:211], a[208:211]\n" \
"v_cvt_pk_bf16_f32 v217, v218, v219\n" \
"v_mul_f32_e32 v220, v56, v220\n" \
"v_mul_f32_e32 v221, v56, v221\n" \
"v_mul_f32_e32 v222, v56, v222\n" \
"v_mul_f32_e32 v223, v56, v223\n" \
"v_mul_f32_e32 v224, v57, v224\n" \
"v_mul_f32_e32 v225, v57, v225\n" \
"v_cvt_pk_bf16_f32 v218, v220, v221\n" \
"v_cvt_pk_bf16_f32 v219, v222, v223\n" \
"buffer_store_dwordx4 v[216:219], v20, s[56:59], s70 offen offset:3072 sc1\n" \
"v_cvt_pk_bf16_f32 v224, v224, v225\n" \
"v_mul_f32_e32 v226, v57, v226\n" \
"v_mul_f32_e32 v227, v57, v227\n" \
"v_mfma_f32_16x16x32_bf16 a[208:211], a[216:219], v[216:219], a[208:211]\n" \
"v_mfma_f32_32x32x16_bf16 v[208:223], a[216:219], a[216:219], 0\n" \
"v_cvt_pk_bf16_f32 v225, v226, v227\n" \
"v_mul_f32_e32 v228, v57, v228\n" \
"v_mul_f32_e32 v229, v57, v229\n" \
"v_mul_f32_e32 v230, v57, v230\n" \
"v_mul_f32_e32 v231, v57, v231\n" \
"v_mul_f32_e32 v232, v57, v232\n" \
"v_mul_f32_e32 v233, v57, v233\n" \
"v_cvt_pk_bf16_f32 v226, v228, v229\n" \
"v_cvt_pk_bf16_f32 v227, v230, v231\n" \
"buffer_store_dwordx4 v[224:227], v20, s[56:59], s71 offen offset:0 sc1\n" \
"v_cvt_pk_bf16_f32 v232, v232, v233\n" \
"v_mul_f32_e32 v234, v57, v234\n" \
"v_mul_f32_e32 v235, v57, v235\n" \
"v_mfma_f32_16x16x32_bf16 a[208:211], a[216:219], v[224:227], a[208:211]\n" \
"v_cvt_pk_bf16_f32 v233, v234, v235\n" \
"v_mul_f32_e32 v236, v57, v236\n" \
"v_mul_f32_e32 v237, v57, v237\n" \
"v_mul_f32_e32 v238, v57, v238\n" \
"v_mul_f32_e32 v239, v57, v239\n" \
"v_mul_f32_e32 v240, v57, v240\n" \
"v_mul_f32_e32 v241, v57, v241\n" \
"v_cvt_pk_bf16_f32 v234, v236, v237\n" \
"v_cvt_pk_bf16_f32 v235, v238, v239\n" \
"buffer_store_dwordx4 v[232:235], v20, s[56:59], s71 offen offset:1024 sc1\n" \
"v_cvt_pk_bf16_f32 v240, v240, v241\n" \
"v_mul_f32_e32 v242, v57, v242\n" \
"v_mul_f32_e32 v243, v57, v243\n" \
"v_mfma_f32_16x16x32_bf16 a[208:211], a[216:219], v[232:235], a[208:211]\n" \
"v_mfma_f32_32x32x16_bf16 v[224:239], a[216:219], a[216:219], 0\n" \
"v_cvt_pk_bf16_f32 v241, v242, v243\n" \
"v_mul_f32_e32 v244, v57, v244\n" \
"v_mul_f32_e32 v245, v57, v245\n" \
"v_mul_f32_e32 v246, v57, v246\n" \
"v_mul_f32_e32 v247, v57, v247\n" \
"v_mul_f32_e32 v248, v57, v248\n" \
"v_mul_f32_e32 v249, v57, v249\n" \
"v_cvt_pk_bf16_f32 v242, v244, v245\n" \
"v_cvt_pk_bf16_f32 v243, v246, v247\n" \
"buffer_store_dwordx4 v[240:243], v20, s[56:59], s71 offen offset:2048 sc1\n" \
"v_cvt_pk_bf16_f32 v248, v248, v249\n" \
"v_mul_f32_e32 v250, v57, v250\n" \
"v_mul_f32_e32 v251, v57, v251\n" \
"v_mfma_f32_16x16x32_bf16 a[208:211], a[216:219], v[240:243], a[208:211]\n" \
"v_cvt_pk_bf16_f32 v249, v250, v251\n" \
"v_mul_f32_e32 v252, v57, v252\n" \
"v_mul_f32_e32 v253, v57, v253\n" \
"v_mul_f32_e32 v254, v57, v254\n" \
"v_mul_f32_e32 v255, v57, v255\n" \
"v_cvt_pk_bf16_f32 v250, v252, v253\n" \
"v_cvt_pk_bf16_f32 v251, v254, v255\n" \
"buffer_store_dwordx4 v[248:251], v20, s[56:59], s71 offen offset:3072 sc1\n" \
"buffer_load_dwordx4 v53, s[8:11], s64 offen offset:3072 sc1 lds\n" \
"s_waitcnt vmcnt(16) lgkmcnt(0)\n" \
"s_barrier\n" \
"v_mfma_f32_16x16x32_bf16 a[208:211], a[216:219], v[248:251], a[208:211]\n" \
"v_mfma_f32_32x32x16_bf16 v[240:255], a[216:219], a[216:219], 0\n" \
"v_exp_f32_e32 v70, v70\n" \
"v_exp_f32_e32 v71, v71\n" \
"v_exp_f32_e32 v72, v72\n" \
"v_exp_f32_e32 v73, v73\n" \
"s_add_u32 s64, s64, 0x10000\n" \
"v_cmp_lt_f32_e64 s[72:73], s21, v62\n" \
"v_readlane_b32 s95, v11, 3\n" \
"v_accvgpr_read_b32 v63, a208\n" \
"v_cmp_u_f32_e32 vcc, v63, v63\n" \
"v_exp_f32_e32 v68, v68\n" \
"v_exp_f32_e32 v69, v69\n" \
"v_exp_f32_e32 v74, v74\n" \
"v_exp_f32_e32 v75, v75\n" \
"s_or_b64 vcc, s[72:73], vcc\n" \
"s_cselect_b32 s95, 0x40000000, s95\n" \
"s_cselect_b32 s94, 0x7f800000, 0\n" \
"v_cvt_pk_bf16_f32 v71, v70, v71\n" \
"v_exp_f32_e32 v66, v66\n" \
"v_exp_f32_e32 v67, v67\n" \
"v_exp_f32_e32 v64, v64\n" \
"v_exp_f32_e32 v65, v65\n" \
"v_cvt_pk_bf16_f32 v72, v72, v73\n" \
"v_exp_f32_e32 v76, v76\n" \
"v_exp_f32_e32 v77, v77\n" \
"v_exp_f32_e32 v78, v78\n" \
"v_exp_f32_e32 v79, v79\n" \
"v_cvt_pk_bf16_f32 v70, v68, v69\n" \
"v_cvt_pk_bf16_f32 v73, v74, v75\n" \
"v_add_u32_e32 v49, 0x10000, v48\n" \
"s_xor_b32 s65, s65, 0x8000\n" \
"v_cvt_pk_bf16_f32 v69, v66, v67\n" \
"v_cvt_pk_bf16_f32 v68, v64, v65\n" \
"v_cvt_pk_bf16_f32 v74, v76, v77\n" \
"v_cvt_pk_bf16_f32 v75, v78, v79\n" \
"v_xor_b32_e32 v48, 0x8000, v48\n" \
"s_sub_u32 s66, s19, 1\n" \
"s_cbranch_scc0 L_2_%=\n" \
"L_f1_%=:\n" \
"v_bfe_u32 v60, v20, 2, 7\n" \
"v_readlane_b32 s96, v11, 1\n" \
"ds_read_b128 a[160:163], v49 offset:0\n" \
"ds_read_b128 a[164:167], v49 offset:1024\n" \
"ds_read_b128 a[168:171], v49 offset:2048\n" \
"v_mfma_f32_16x16x32_bf16 v[104:107], a[128:131], a[80:83], v[36:39]\n" \
"v_cvt_pk_bf16_f32 v192, v192, v193\n" \
"v_cvt_pk_bf16_f32 v224, v224, v225\n" \
"buffer_load_dword v57, v60, s[56:59], s96 offen sc1\n" \
"v_readlane_b32 s93, v11, 20\n" \
"v_cvt_pk_bf16_f32 v193, v194, v195\n" \
"v_cvt_pk_bf16_f32 v225, v226, v227\n" \
"ds_read_b128 a[172:175], v49 offset:3072\n" \
"v_readlane_b32 s84, v11, 15\n" \
"v_readlane_b32 s85, v11, 16\n" \
"v_readlane_b32 s86, v11, 17\n" \
"s_mov_b32 s87, 0x20000\n" \
"v_readlane_b32 s92, v11, 18\n" \
"s_add_u32 m0, s93, 0x0\n" \
"v_cvt_pk_bf16_f32 v194, v196, v197\n" \
"v_cvt_pk_bf16_f32 v195, v198, v199\n" \
"v_cvt_pk_bf16_f32 v226, v228, v229\n" \
"v_cvt_pk_bf16_f32 v227, v230, v231\n" \
"v_mfma_f32_16x16x32_bf16 v[104:107], a[132:135], a[84:87], v[104:107]\n" \
"v_lshl_add_u32 v50, s15, 2, v20\n" \
"v_cvt_pk_bf16_f32 v196, v200, v201\n" \
"v_cvt_pk_bf16_f32 v197, v202, v203\n" \
"v_cvt_pk_bf16_f32 v198, v204, v205\n" \
"v_cvt_pk_bf16_f32 v199, v206, v207\n" \
"buffer_load_dwordx4 v0, s[84:87], s92 offen offset:0 lds\n" \
"v_cvt_pk_bf16_f32 v228, v232, v233\n" \
"v_cvt_pk_bf16_f32 v229, v234, v235\n" \
"v_cvt_pk_bf16_f32 v230, v236, v237\n" \
"v_cvt_pk_bf16_f32 v231, v238, v239\n" \
"v_add_u32_e32 v50, 0x1a000, v50\n" \
"v_mov_b32_e32 v61, s62\n" \
"v_cvt_pk_bf16_f32 v200, v208, v209\n" \
"v_cvt_pk_bf16_f32 v201, v210, v211\n" \
"v_cvt_pk_bf16_f32 v202, v212, v213\n" \
"v_cvt_pk_bf16_f32 v203, v214, v215\n" \
"v_cvt_pk_bf16_f32 v204, v216, v217\n" \
"v_cvt_pk_bf16_f32 v205, v218, v219\n" \
"v_cvt_pk_bf16_f32 v206, v220, v221\n" \
"v_cvt_pk_bf16_f32 v207, v222, v223\n" \
"v_cvt_pk_bf16_f32 v232, v240, v241\n" \
"v_cvt_pk_bf16_f32 v233, v242, v243\n" \
"v_cvt_pk_bf16_f32 v234, v244, v245\n" \
"v_cvt_pk_bf16_f32 v235, v246, v247\n" \
"v_cvt_pk_bf16_f32 v236, v248, v249\n" \
"v_cvt_pk_bf16_f32 v237, v250, v251\n" \
"v_cvt_pk_bf16_f32 v238, v252, v253\n" \
"buffer_load_dwordx4 v1, s[84:87], s92 offen offset:1024 lds\n" \
"v_cvt_pk_bf16_f32 v239, v254, v255\n" \
"ds_write_b128 v50, v[192:195] offset:0\n" \
"v_mfma_f32_16x16x32_bf16 v[104:107], a[136:139], a[88:91], v[104:107]\n" \
"ds_read_b128 a[176:179], v49 offset:4096\n" \
"v_add_u32_e32 v56, 0x49000, v20\n" \
"v_mfma_f32_16x16x32_bf16 v[104:107], a[140:143], a[92:95], v[104:107]\n" \
"ds_write_b128 v50, v[196:199] offset:1024\n" \
"v_mfma_f32_16x16x32_bf16 v[108:111], a[156:159], a[92:95], v[36:39]\n" \
"v_readlane_b32 s97, v11, 35\n" \
"v_readlane_b32 s98, v11, 36\n" \
"ds_read_b128 a[180:183], v49 offset:5120\n" \
"v_mfma_f32_16x16x32_bf16 v[108:111], a[152:155], a[88:91], v[108:111]\n" \
"ds_write_b128 v50, v[200:203] offset:2048\n" \
"v_mfma_f32_16x16x32_bf16 v[108:111], a[148:151], a[84:87], v[108:111]\n" \
"buffer_load_dwordx4 v2, s[84:87], s92 offen offset:2048 lds\n" \
"ds_read_b128 a[184:187], v49 offset:6144\n" \
"v_mfma_f32_16x16x32_bf16 v[108:111], a[144:147], a[80:83], v[108:111]\n" \
"ds_write_b128 v50, v[204:207] offset:3072\n" \
"v_and_b32_e32 v68, v12, v68\n" \
"v_and_b32_e32 v69, v13, v69\n" \
"v_and_b32_e32 v70, v14, v70\n" \
"v_and_b32_e32 v71, v15, v71\n" \
"ds_read_b128 a[188:191], v49 offset:7168\n" \
"ds_read_b128 a[0:3], v20 offset:8192\n" \
"ds_write_b128 v50, v[224:227] offset:4096\n" \
"s_waitcnt lgkmcnt(10)\n" \
"v_mfma_f32_16x16x32_bf16 v[128:131], a[160:163], v[68:71], v[128:131]\n" \
"ds_read_b128 a[4:7], v20 offset:9216\n" \
"ds_read_b128 a[8:11], v20 offset:10240\n" \
"v_mfma_f32_16x16x32_bf16 v[160:163], a[160:163], v[72:75], v[160:163]\n" \
"ds_write_b128 v50, v[228:231] offset:5120\n" \
"buffer_load_dwordx4 v3, s[84:87], s92 offen offset:3072 lds\n" \
"s_add_u32 m0, s93, 0x1000\n" \
"v_mfma_f32_16x16x32_bf16 v[132:135], a[164:167], v[68:71], v[132:135]\n" \
"ds_read_b128 a[12:15], v20 offset:11264\n" \
"ds_read_b128 a[28:31], v20 offset:15360\n" \
"ds_write_b128 v50, v[232:235] offset:6144\n" \
"v_mfma_f32_16x16x32_bf16 v[164:167], a[164:167], v[72:75], v[164:167]\n" \
"ds_read_b128 a[24:27], v20 offset:14336\n" \
"ds_read_b128 a[20:23], v20 offset:13312\n" \
"v_mfma_f32_16x16x32_bf16 v[136:139], a[168:171], v[68:71], v[136:139]\n" \
"ds_write_b128 v50, v[236:239] offset:7168\n" \
"v_mfma_f32_16x16x32_bf16 v[168:171], a[168:171], v[72:75], v[168:171]\n" \
"ds_read_b128 a[16:19], v20 offset:12288\n" \
"v_xor_b32_e32 v48, 0x4000, v49\n" \
"v_mfma_f32_16x16x32_bf16 v[140:143], a[172:175], v[68:71], v[140:143]\n" \
"buffer_load_dwordx4 v4, s[84:87], s92 offen offset:0 lds\n" \
"v_mfma_f32_16x16x32_bf16 v[172:175], a[172:175], v[72:75], v[172:175]\n" \
"ds_read_b32 v58, v18 offset:256\n" \
"s_waitcnt lgkmcnt(15)\n" \
"v_mfma_f32_16x16x32_bf16 v[144:147], a[176:179], v[68:71], v[144:147]\n" \
"ds_read_b32 v59, v18 offset:384\n" \
"ds_read_b128 a[32:35], v48 offset:8192\n" \
"v_mfma_f32_16x16x32_bf16 v[176:179], a[176:179], v[72:75], v[176:179]\n" \
"ds_read_b128 a[36:39], v48 offset:9216\n" \
"ds_read_b128 a[40:43], v48 offset:10240\n" \
"v_mfma_f32_16x16x32_bf16 v[148:151], a[180:183], v[68:71], v[148:151]\n" \
"ds_read_b128 a[44:47], v48 offset:11264\n" \
"ds_read_b128 a[48:51], v48 offset:12288\n" \
"v_mfma_f32_16x16x32_bf16 v[180:183], a[180:183], v[72:75], v[180:183]\n" \
"ds_read_b128 a[52:55], v48 offset:13312\n" \
"ds_read_b128 a[56:59], v48 offset:14336\n" \
"v_mfma_f32_16x16x32_bf16 v[152:155], a[184:187], v[68:71], v[152:155]\n" \
"buffer_load_dwordx4 v5, s[84:87], s92 offen offset:1024 lds\n" \
"v_mfma_f32_16x16x32_bf16 v[184:187], a[184:187], v[72:75], v[184:187]\n" \
"ds_read_b128 a[60:63], v48 offset:15360\n" \
"s_waitcnt lgkmcnt(15)\n" \
"v_mfma_f32_16x16x32_bf16 v[156:159], a[188:191], v[68:71], v[156:159]\n" \
"ds_read_b128 a[128:131], v49 offset:8192\n" \
"ds_read_b128 a[132:135], v49 offset:9216\n" \
"v_mfma_f32_16x16x32_bf16 v[188:191], a[188:191], v[72:75], v[188:191]\n" \
"ds_read_b128 a[136:139], v49 offset:10240\n" \
"ds_read_b128 a[140:143], v49 offset:11264\n" \
"v_smfmac_f32_16x16x64_bf16 a[192:195], a[200:203], v[68:75], v21\n" \
"ds_read_b128 a[144:147], v49 offset:12288\n" \
"ds_read_b128 a[148:151], v49 offset:13312\n" \
"v_mfma_f32_16x16x32_bf16 v[112:115], a[0:3], a[64:67], v[32:35]\n" \
"ds_read_b128 a[152:155], v49 offset:14336\n" \
"ds_read_b128 a[156:159], v49 offset:15360\n" \
"v_mfma_f32_16x16x32_bf16 v[112:115], a[4:7], a[68:71], v[112:115]\n" \
"buffer_load_dwordx4 v6, s[84:87], s92 offen offset:2048 lds\n" \
"v_mfma_f32_16x16x32_bf16 v[112:115], a[8:11], a[72:75], v[112:115]\n" \
"v_accvgpr_read_b32 v40, a196\n" \
"v_accvgpr_read_b32 v24, a197\n" \
"v_mfma_f32_16x16x32_bf16 v[112:115], a[12:15], a[76:79], v[112:115]\n" \
"v_accvgpr_read_b32 v41, a198\n" \
"v_accvgpr_read_b32 v25, a199\n" \
"v_mfma_f32_16x16x32_bf16 v[116:119], a[28:31], a[76:79], v[32:35]\n" \
"s_waitcnt vmcnt(7)\n" \
"v_cmp_ne_u32_e32 vcc, s62, v57\n" \
"s_mov_b64 exec, 1\n" \
"buffer_store_dword v61, off, s[56:59], s95 offset:0 sc1\n" \
"s_mov_b64 exec, -1\n" \
"s_waitcnt lgkmcnt(15)\n" \
"v_mfma_f32_16x16x32_bf16 v[116:119], a[24:27], a[72:75], v[116:119]\n" \
"s_cbranch_vccnz L_so1_%=\n" \
"L_ro1_%=:\n" \
"v_add_f32_e32 v40, v40, v24\n" \
"v_mfma_f32_16x16x32_bf16 v[116:119], a[20:23], a[68:71], v[116:119]\n" \
"v_add_f32_e32 v41, v41, v25\n" \
"v_add_f32_e32 v40, s94, v40\n" \
"v_mfma_f32_16x16x32_bf16 v[116:119], a[16:19], a[64:67], v[116:119]\n" \
"v_add_f32_e32 v41, s94, v41\n" \
"ds_write_b64 v19, v[40:41]\n" \
"v_mfma_f32_16x16x32_bf16 v[124:127], a[16:19], a[80:83], v[36:39]\n" \
"v_add_u32_e32 v52, s15, v20\n" \
"v_add_u32_e32 v51, 0x10000, v52\n" \
"v_mfma_f32_16x16x32_bf16 v[124:127], a[20:23], a[84:87], v[124:127]\n" \
"v_exp_f32_e32 v104, v104\n" \
"buffer_load_dwordx4 v7, s[84:87], s92 offen offset:3072 lds\n" \
"s_add_u32 m0, s93, 0x2000\n" \
"v_mfma_f32_16x16x32_bf16 v[124:127], a[24:27], a[88:91], v[124:127]\n" \
"v_exp_f32_e32 v105, v105\n" \
"v_mfma_f32_16x16x32_bf16 v[124:127], a[28:31], a[92:95], v[124:127]\n" \
"v_exp_f32_e32 v106, v106\n" \
"v_mfma_f32_16x16x32_bf16 v[120:123], a[12:15], a[92:95], v[36:39]\n" \
"v_exp_f32_e32 v107, v107\n" \
"v_mfma_f32_16x16x32_bf16 v[120:123], a[8:11], a[88:91], v[120:123]\n" \
"v_cvt_pk_bf16_f32 v104, v104, v105\n" \
"v_cvt_pk_bf16_f32 v105, v106, v107\n" \
"v_mfma_f32_16x16x32_bf16 v[120:123], a[4:7], a[84:87], v[120:123]\n" \
"v_exp_f32_e32 v108, v108\n" \
"buffer_load_dwordx4 v8, s[84:87], s92 offen offset:0 lds\n" \
"v_mfma_f32_16x16x32_bf16 v[120:123], a[0:3], a[80:83], v[120:123]\n" \
"v_exp_f32_e32 v109, v109\n" \
"v_exp_f32_e32 v110, v110\n" \
"v_exp_f32_e32 v111, v111\n" \
"s_waitcnt lgkmcnt(0)\n" \
"s_barrier\n" \
"ds_read_b128 v[208:211], v52 offset:0\n" \
"ds_read_b128 v[212:215], v52 offset:1024\n" \
"ds_read_b128 v[216:219], v52 offset:2048\n" \
"ds_read_b128 v[220:223], v52 offset:3072\n" \
"ds_read_b128 v[240:243], v51 offset:32768\n" \
"ds_read_b128 v[244:247], v51 offset:33792\n" \
"ds_read_b128 v[248:251], v51 offset:34816\n" \
"ds_read_b128 v[252:255], v51 offset:35840\n" \
"buffer_load_dwordx4 a[220:223], v20, s[56:59], s60 offen offset:0 sc1\n" \
"v_cvt_pk_bf16_f32 v106, v108, v109\n" \
"v_cvt_pk_bf16_f32 v107, v110, v111\n" \
"v_and_b32_e32 v104, v12, v104\n" \
"v_and_b32_e32 v105, v13, v105\n" \
"v_and_b32_e32 v106, v14, v106\n" \
"v_and_b32_e32 v107, v15, v107\n" \
"v_xor_b32_e32 v63, 0x400, v19\n" \
"ds_read_b64 v[42:43], v63\n" \
"v_mfma_f32_16x16x32_bf16 a[192:195], a[212:215], v[104:107], a[192:195]\n" \
"v_mov_b32_e32 v194, v28\n" \
"v_mov_b32_e32 v195, v29\n" \
"v_mfma_f32_16x16x32_bf16 v[160:163], a[128:131], v[104:107], v[160:163]\n" \
"v_mov_b32_e32 v196, 0\n" \
"v_mov_b32_e32 v197, 0\n" \
"v_mfma_f32_16x16x32_bf16 v[164:167], a[132:135], v[104:107], v[164:167]\n" \
"v_mov_b32_e32 v192, 0\n" \
"v_mov_b32_e32 v193, 0\n" \
"v_mfma_f32_16x16x32_bf16 v[168:171], a[136:139], v[104:107], v[168:171]\n" \
"buffer_load_dwordx4 a[236:239], v56, s[56:59], s60 offen offset:0 sc1\n" \
"v_mfma_f32_16x16x32_bf16 v[172:175], a[140:143], v[104:107], v[172:175]\n" \
"v_exp_f32_e32 v118, v118\n" \
"v_mfma_f32_16x16x32_bf16 v[176:179], a[144:147], v[104:107], v[176:179]\n" \
"v_exp_f32_e32 v119, v119\n" \
"v_mfma_f32_16x16x32_bf16 v[180:183], a[148:151], v[104:107], v[180:183]\n" \
"v_exp_f32_e32 v120, v120\n" \
"v_mfma_f32_16x16x32_bf16 v[184:187], a[152:155], v[104:107], v[184:187]\n" \
"v_exp_f32_e32 v121, v121\n" \
"v_mfma_f32_16x16x32_bf16 v[188:191], a[156:159], v[104:107], v[188:191]\n" \
"v_exp_f32_e32 v116, v116\n" \
"s_waitcnt lgkmcnt(1)\n" \
"s_barrier\n" \
"buffer_load_dwordx4 a[224:227], v20, s[56:59], s60 offen offset:1024 sc1\n" \
"v_mfma_f32_16x16x32_bf16 v[128:131], v[194:197], v[208:211], v[128:131]\n" \
"v_exp_f32_e32 v117, v117\n" \
"v_mfma_f32_16x16x32_bf16 v[132:135], v[192:195], v[208:211], v[132:135]\n" \
"v_exp_f32_e32 v122, v122\n" \
"v_mfma_f32_16x16x32_bf16 v[136:139], v[194:197], v[212:215], v[136:139]\n" \
"v_exp_f32_e32 v123, v123\n" \
"v_mfma_f32_16x16x32_bf16 v[140:143], v[192:195], v[212:215], v[140:143]\n" \
"v_cvt_pk_bf16_f32 v119, v118, v119\n" \
"v_cvt_pk_bf16_f32 v120, v120, v121\n" \
"v_mfma_f32_16x16x32_bf16 v[144:147], v[194:197], v[216:219], v[144:147]\n" \
"v_exp_f32_e32 v114, v114\n" \
"v_mfma_f32_16x16x32_bf16 v[148:151], v[192:195], v[216:219], v[148:151]\n" \
"buffer_load_dwordx4 a[240:243], v56, s[56:59], s60 offen offset:1024 sc1\n" \
"v_mfma_f32_16x16x32_bf16 v[152:155], v[194:197], v[220:223], v[152:155]\n" \
"v_exp_f32_e32 v115, v115\n" \
"v_mfma_f32_16x16x32_bf16 v[156:159], v[192:195], v[220:223], v[156:159]\n" \
"v_exp_f32_e32 v112, v112\n" \
"v_mfma_f32_16x16x32_bf16 v[160:163], v[194:197], v[240:243], v[160:163]\n" \
"v_exp_f32_e32 v113, v113\n" \
"v_mfma_f32_16x16x32_bf16 v[164:167], v[192:195], v[240:243], v[164:167]\n" \
"v_exp_f32_e32 v124, v124\n" \
"v_mfma_f32_16x16x32_bf16 v[168:171], v[194:197], v[244:247], v[168:171]\n" \
"v_exp_f32_e32 v125, v125\n" \
"v_mfma_f32_16x16x32_bf16 v[172:175], v[192:195], v[244:247], v[172:175]\n" \
"buffer_load_dwordx4 a[228:231], v20, s[56:59], s60 offen offset:2048 sc1\n" \
"v_mfma_f32_16x16x32_bf16 v[176:179], v[194:197], v[248:251], v[176:179]\n" \
"v_exp_f32_e32 v126, v126\n" \
"v_mfma_f32_16x16x32_bf16 v[180:183], v[192:195], v[248:251], v[180:183]\n" \
"v_exp_f32_e32 v127, v127\n" \
"v_mfma_f32_16x16x32_bf16 v[184:187], v[194:197], v[252:255], v[184:187]\n" \
"v_cvt_pk_bf16_f32 v118, v116, v117\n" \
"v_cvt_pk_bf16_f32 v121, v122, v123\n" \
"v_mfma_f32_16x16x32_bf16 v[188:191], v[192:195], v[252:255], v[188:191]\n" \
"v_cvt_pk_bf16_f32 v117, v114, v115\n" \
"v_cvt_pk_bf16_f32 v116, v112, v113\n" \
"v_cvt_pk_bf16_f32 v122, v124, v125\n" \
"v_cvt_pk_bf16_f32 v123, v126, v127\n" \
"s_nop 1\n" \
"v_smfmac_f32_16x16x64_bf16 a[192:195], a[200:203], v[116:123], v21\n" \
"buffer_load_dwordx4 a[244:247], v56, s[56:59], s60 offen offset:2048 sc1\n" \
"v_mfma_f32_16x16x32_bf16 v[128:131], a[32:35], v[116:119], v[128:131]\n" \
"v_mfma_f32_16x16x32_bf16 v[160:163], a[32:35], v[120:123], v[160:163]\n" \
"v_mfma_f32_16x16x32_bf16 v[132:135], a[36:39], v[116:119], v[132:135]\n" \
"v_mfma_f32_16x16x32_bf16 v[164:167], a[36:39], v[120:123], v[164:167]\n" \
"v_mfma_f32_16x16x32_bf16 v[136:139], a[40:43], v[116:119], v[136:139]\n" \
"v_mfma_f32_16x16x32_bf16 v[168:171], a[40:43], v[120:123], v[168:171]\n" \
"buffer_load_dwordx4 a[232:235], v20, s[56:59], s60 offen offset:3072 sc1\n" \
"v_mfma_f32_16x16x32_bf16 v[140:143], a[44:47], v[116:119], v[140:143]\n" \
"v_accvgpr_read_b32 v54, a192\n" \
"v_accvgpr_read_b32 v22, a193\n" \
"v_mfma_f32_16x16x32_bf16 v[172:175], a[44:47], v[120:123], v[172:175]\n" \
"v_add_f32_e32 v54, v54, v22\n" \
"s_waitcnt lgkmcnt(0)\n" \
"v_add_f32_e32 v54, v54, v42\n" \
"v_mfma_f32_16x16x32_bf16 v[144:147], a[48:51], v[116:119], v[144:147]\n" \
"v_max_f32_e32 v62, v62, v54\n" \
"v_accvgpr_read_b32 v55, a194\n" \
"v_mfma_f32_16x16x32_bf16 v[176:179], a[48:51], v[120:123], v[176:179]\n" \
"v_rcp_f32_e32 v54, v54\n" \
"v_mfma_f32_16x16x32_bf16 v[148:151], a[52:55], v[116:119], v[148:151]\n" \
"v_mul_f32_e32 v54, v54, v58\n" \
"v_mul_f32_e32 v128, v54, v128\n" \
"buffer_load_dwordx4 a[248:251], v56, s[56:59], s60 offen offset:3072 sc1\n" \
"v_mfma_f32_16x16x32_bf16 v[180:183], a[52:55], v[120:123], v[180:183]\n" \
"v_mul_f32_e32 v129, v54, v129\n" \
"v_mul_f32_e32 v130, v54, v130\n" \
"v_mul_f32_e32 v131, v54, v131\n" \
"v_mul_f32_e32 v132, v54, v132\n" \
"v_mul_f32_e32 v133, v54, v133\n" \
"v_mul_f32_e32 v134, v54, v134\n" \
"v_mul_f32_e32 v135, v54, v135\n" \
"v_cvt_pk_bf16_f32 v64, v128, v129\n" \
"v_cvt_pk_bf16_f32 v65, v130, v131\n" \
"v_cvt_pk_bf16_f32 v66, v132, v133\n" \
"v_cvt_pk_bf16_f32 v67, v134, v135\n" \
"v_accvgpr_read_b32 v23, a195\n" \
"v_mfma_f32_16x16x32_bf16 v[152:155], a[56:59], v[116:119], v[152:155]\n" \
"v_mfma_f32_16x16x32_bf16 a[208:211], a[216:219], v[64:67], a[208:211]\n" \
"v_mul_f32_e32 v136, v54, v136\n" \
"v_mul_f32_e32 v137, v54, v137\n" \
"buffer_load_dwordx4 a[96:99], v20, s[56:59], s97 offen offset:0 sc1\n" \
"v_mul_f32_e32 v138, v54, v138\n" \
"v_mul_f32_e32 v139, v54, v139\n" \
"v_mul_f32_e32 v140, v54, v140\n" \
"v_mul_f32_e32 v141, v54, v141\n" \
"v_mul_f32_e32 v142, v54, v142\n" \
"v_mul_f32_e32 v143, v54, v143\n" \
"v_add_f32_e32 v55, v55, v23\n" \
"v_cvt_pk_bf16_f32 v68, v136, v137\n" \
"v_cvt_pk_bf16_f32 v69, v138, v139\n" \
"v_cvt_pk_bf16_f32 v70, v140, v141\n" \
"v_cvt_pk_bf16_f32 v71, v142, v143\n" \
"v_add_f32_e32 v55, v55, v43\n" \
"v_mul_f32_e32 v144, v54, v144\n" \
"v_mfma_f32_16x16x32_bf16 a[208:211], a[216:219], v[68:71], a[208:211]\n" \
"v_mul_f32_e32 v145, v54, v145\n" \
"v_mul_f32_e32 v146, v54, v146\n" \
"v_mul_f32_e32 v147, v54, v147\n" \
"v_mul_f32_e32 v148, v54, v148\n" \
"v_mul_f32_e32 v149, v54, v149\n" \
"v_mul_f32_e32 v150, v54, v150\n" \
"buffer_load_dwordx4 a[104:107], v56, s[56:59], s97 offen offset:0 sc1\n" \
"v_mul_f32_e32 v151, v54, v151\n" \
"v_max_f32_e32 v62, v62, v55\n" \
"v_cvt_pk_bf16_f32 v72, v144, v145\n" \
"v_cvt_pk_bf16_f32 v73, v146, v147\n" \
"v_cvt_pk_bf16_f32 v74, v148, v149\n" \
"v_cvt_pk_bf16_f32 v75, v150, v151\n" \
"v_rcp_f32_e32 v55, v55\n" \
"v_mfma_f32_16x16x32_bf16 v[184:187], a[56:59], v[120:123], v[184:187]\n" \
"v_mfma_f32_16x16x32_bf16 a[208:211], a[216:219], v[72:75], a[208:211]\n" \
"v_mul_f32_e32 v152, v54, v152\n" \
"v_mul_f32_e32 v153, v54, v153\n" \
"v_mul_f32_e32 v154, v54, v154\n" \
"v_mul_f32_e32 v155, v54, v155\n" \
"v_cvt_pk_bf16_f32 v76, v152, v153\n" \
"v_cvt_pk_bf16_f32 v77, v154, v155\n" \
"v_mul_f32_e32 v55, v55, v59\n" \
"buffer_load_dwordx4 a[112:115], v20, s[56:59], s98 offen offset:0 sc1\n" \
"v_mul_f32_e32 v160, v55, v160\n" \
"v_mul_f32_e32 v161, v55, v161\n" \
"v_mul_f32_e32 v162, v55, v162\n" \
"v_mul_f32_e32 v163, v55, v163\n" \
"v_mul_f32_e32 v164, v55, v164\n" \
"v_mul_f32_e32 v165, v55, v165\n" \
"v_mul_f32_e32 v166, v55, v166\n" \
"v_mul_f32_e32 v167, v55, v167\n" \
"v_cvt_pk_bf16_f32 v80, v160, v161\n" \
"v_cvt_pk_bf16_f32 v81, v162, v163\n" \
"v_cvt_pk_bf16_f32 v82, v164, v165\n" \
"v_cvt_pk_bf16_f32 v83, v166, v167\n" \
"v_mfma_f32_16x16x32_bf16 v[156:159], a[60:63], v[116:119], v[156:159]\n" \
"v_mul_f32_e32 v168, v55, v168\n" \
"v_mul_f32_e32 v169, v55, v169\n" \
"v_mul_f32_e32 v170, v55, v170\n" \
"v_mul_f32_e32 v171, v55, v171\n" \
"v_mul_f32_e32 v172, v55, v172\n" \
"v_mul_f32_e32 v173, v55, v173\n" \
"v_mul_f32_e32 v174, v55, v174\n" \
"buffer_load_dwordx4 a[120:123], v56, s[56:59], s98 offen offset:0 sc1\n" \
"v_mul_f32_e32 v156, v54, v156\n" \
"v_mul_f32_e32 v157, v54, v157\n" \
"v_mul_f32_e32 v158, v54, v158\n" \
"v_mul_f32_e32 v159, v54, v159\n" \
"v_cvt_pk_bf16_f32 v78, v156, v157\n" \
"v_cvt_pk_bf16_f32 v79, v158, v159\n" \
"v_mul_f32_e32 v175, v55, v175\n" \
"v_cvt_pk_bf16_f32 v84, v168, v169\n" \
"v_mfma_f32_16x16x32_bf16 a[208:211], a[216:219], v[76:79], a[208:211]\n" \
"v_mfma_f32_16x16x32_bf16 a[208:211], a[216:219], v[80:83], a[208:211]\n" \
"v_cvt_pk_bf16_f32 v85, v170, v171\n" \
"v_cvt_pk_bf16_f32 v86, v172, v173\n" \
"v_cvt_pk_bf16_f32 v87, v174, v175\n" \
"v_mul_f32_e32 v176, v55, v176\n" \
"v_mul_f32_e32 v177, v55, v177\n" \
"v_mfma_f32_16x16x32_bf16 a[208:211], a[216:219], v[84:87], a[208:211]\n" \
"buffer_load_dwordx4 a[100:103], v20, s[56:59], s97 offen offset:1024 sc1\n" \
"v_mul_f32_e32 v178, v55, v178\n" \
"v_mul_f32_e32 v179, v55, v179\n" \
"v_mul_f32_e32 v180, v55, v180\n" \
"v_mul_f32_e32 v181, v55, v181\n" \
"v_mul_f32_e32 v182, v55, v182\n" \
"v_mul_f32_e32 v183, v55, v183\n" \
"v_cvt_pk_bf16_f32 v96, v176, v177\n" \
"v_cvt_pk_bf16_f32 v97, v178, v179\n" \
"v_cvt_pk_bf16_f32 v98, v180, v181\n" \
"v_cvt_pk_bf16_f32 v99, v182, v183\n" \
"v_mfma_f32_16x16x32_bf16 v[188:191], a[60:63], v[120:123], v[188:191]\n" \
"v_mul_f32_e32 v184, v55, v184\n" \
"v_mfma_f32_16x16x32_bf16 a[208:211], a[216:219], v[96:99], a[208:211]\n" \
"v_mul_f32_e32 v185, v55, v185\n" \
"v_mul_f32_e32 v186, v55, v186\n" \
"v_mul_f32_e32 v187, v55, v187\n" \
"v_cvt_pk_bf16_f32 v100, v184, v185\n" \
"v_cvt_pk_bf16_f32 v101, v186, v187\n" \
"buffer_load_dwordx4 a[108:111], v56, s[56:59], s97 offen offset:1024 sc1\n" \
"v_mul_f32_e32 v188, v55, v188\n" \
"v_mul_f32_e32 v189, v55, v189\n" \
"v_mul_f32_e32 v190, v55, v190\n" \
"v_mul_f32_e32 v191, v55, v191\n" \
"v_cvt_pk_bf16_f32 v102, v188, v189\n" \
"v_cvt_pk_bf16_f32 v103, v190, v191\n" \
"v_cmp_lt_f32_e64 s[72:73], s21, v62\n" \
"buffer_load_dwordx4 a[116:119], v20, s[56:59], s98 offen offset:1024 sc1\n" \
"v_mfma_f32_16x16x32_bf16 a[208:211], a[216:219], v[100:103], a[208:211]\n" \
"buffer_load_dwordx4 a[124:127], v56, s[56:59], s98 offen offset:1024 sc1\n" \
"s_nop 6\n" \
"v_accvgpr_read_b32 v63, a208\n" \
"v_cmp_u_f32_e32 vcc, v63, v63\n" \
"s_nop 0\n" \
"s_or_b64 vcc, s[72:73], vcc\n" \
"s_cbranch_scc1 L_end_%=\n" \
"L_out_%=:\n" \
"v_bfe_u32 v38, v20, 2, 4\n" \
"v_readlane_b32 s64, v11, 2\n" \
"v_add_u32_e32 v32, 0x49000, v20\n" \
"v_add_u32_e32 v34, 0x20000, v10\n" \
"ds_read_b64_tr_b16 a[0:1], v34 offset:0\n" \
"ds_read_b64_tr_b16 a[2:3], v34 offset:4608\n" \
"ds_read_b64_tr_b16 a[4:5], v34 offset:32\n" \
"buffer_load_dword v26, v38, s[56:59], s64 offen sc1\n" \
"ds_read_b64_tr_b16 a[6:7], v34 offset:4640\n" \
"ds_read_b64_tr_b16 a[8:9], v34 offset:64\n" \
"ds_read_b64_tr_b16 a[10:11], v34 offset:4672\n" \
"ds_read_b64_tr_b16 a[12:13], v34 offset:96\n" \
"buffer_store_dwordx4 v[64:67], v20, s[56:59], s61 offen offset:0 sc1\n" \
"ds_read_b64_tr_b16 a[14:15], v34 offset:4704\n" \
"v_mov_b32_e32 v39, s62\n" \
"v_readlane_b32 s72, v11, 5\n" \
"v_readlane_b32 s73, v11, 4\n" \
"buffer_store_dwordx4 v[68:71], v20, s[56:59], s61 offen offset:1024 sc1\n" \
"ds_read_b64_tr_b16 a[16:17], v34 offset:128\n" \
"ds_read_b64_tr_b16 a[18:19], v34 offset:4736\n" \
"ds_read_b64_tr_b16 a[20:21], v34 offset:160\n" \
"ds_read_b64_tr_b16 a[22:23], v34 offset:4768\n" \
"buffer_store_dwordx4 v[72:75], v20, s[56:59], s61 offen offset:2048 sc1\n" \
"ds_read_b64_tr_b16 a[24:25], v34 offset:192\n" \
"ds_read_b64_tr_b16 a[26:27], v34 offset:4800\n" \
"ds_read_b64_tr_b16 a[28:29], v34 offset:224\n" \
"ds_read_b64_tr_b16 a[30:31], v34 offset:4832\n" \
"buffer_store_dwordx4 v[76:79], v20, s[56:59], s61 offen offset:3072 sc1\n" \
"ds_read_b64_tr_b16 a[192:193], v34 offset:256\n" \
"ds_read_b64_tr_b16 a[194:195], v34 offset:4864\n" \
"ds_read_b64_tr_b16 a[196:197], v34 offset:9216\n" \
"ds_read_b64_tr_b16 a[198:199], v34 offset:13824\n" \
"buffer_store_dwordx4 v[80:83], v32, s[56:59], s61 offen offset:0 sc1\n" \
"ds_read_b64_tr_b16 a[200:201], v34 offset:9248\n" \
"ds_read_b64_tr_b16 a[202:203], v34 offset:13856\n" \
"ds_read_b64_tr_b16 a[204:205], v34 offset:9280\n" \
"ds_read_b64_tr_b16 a[206:207], v34 offset:13888\n" \
"buffer_store_dwordx4 v[84:87], v32, s[56:59], s61 offen offset:1024 sc1\n" \
"ds_read_b64_tr_b16 a[208:209], v34 offset:9312\n" \
"s_waitcnt vmcnt(15) lgkmcnt(15)\n" \
"v_mfma_f32_16x16x32_bf16 v[112:115], a[0:3], a[220:223], 0\n" \
"buffer_store_dwordx4 v[96:99], v32, s[56:59], s61 offen offset:2048 sc1\n" \
"v_mfma_f32_16x16x32_bf16 v[116:119], a[0:3], a[236:239], 0\n" \
"ds_read_b64_tr_b16 a[210:211], v34 offset:13920\n" \
"ds_read_b64_tr_b16 a[212:213], v34 offset:9344\n" \
"buffer_store_dwordx4 v[100:103], v32, s[56:59], s61 offen offset:3072 sc1\n" \
"v_mfma_f32_16x16x32_bf16 v[184:187], a[0:3], v[64:67], 0\n" \
"ds_read_b64_tr_b16 a[214:215], v34 offset:13952\n" \
"ds_read_b64_tr_b16 a[216:217], v34 offset:9376\n" \
"v_mfma_f32_16x16x32_bf16 v[188:191], a[0:3], v[80:83], 0\n" \
"ds_read_b64_tr_b16 a[218:219], v34 offset:13984\n" \
"ds_read_b64_tr_b16 a[0:1], v34 offset:9408\n" \
"v_mfma_f32_16x16x32_bf16 v[120:123], a[4:7], a[220:223], 0\n" \
"ds_read_b64_tr_b16 a[2:3], v34 offset:14016\n" \
"v_readlane_b32 s66, v11, 21\n" \
"v_mfma_f32_16x16x32_bf16 v[124:127], a[4:7], a[236:239], 0\n" \
"v_readlane_b32 s80, v11, 11\n" \
"v_readlane_b32 s81, v11, 12\n" \
"v_mfma_f32_16x16x32_bf16 v[192:195], a[4:7], v[64:67], 0\n" \
"s_mov_b32 s82, 0x47fca0\n" \
"s_mov_b32 s83, 0x20000\n" \
"v_mfma_f32_16x16x32_bf16 v[196:199], a[4:7], v[80:83], 0\n" \
"ds_read_b64_tr_b16 a[4:5], v34 offset:9440\n" \
"ds_read_b64_tr_b16 a[6:7], v34 offset:14048\n" \
"v_mfma_f32_16x16x32_bf16 v[128:131], a[8:11], a[220:223], 0\n" \
"v_readlane_b32 s65, v11, 26\n" \
"s_add_u32 m0, s66, 0x0\n" \
"v_mfma_f32_16x16x32_bf16 v[132:135], a[8:11], a[236:239], 0\n" \
"v_readlane_b32 s67, v11, 29\n" \
"v_lshrrev_b32_e32 v44, 9, v20\n" \
"v_mfma_f32_16x16x32_bf16 v[200:203], a[8:11], v[64:67], 0\n" \
"buffer_load_dwordx4 v0, s[80:83], s65 offen offset:0 lds\n" \
"v_mfma_f32_16x16x32_bf16 v[204:207], a[8:11], v[80:83], 0\n" \
"ds_read_b64_tr_b16 a[8:9], v34 offset:9472\n" \
"ds_read_b64_tr_b16 a[10:11], v34 offset:14080\n" \
"v_mfma_f32_16x16x32_bf16 v[136:139], a[12:15], a[220:223], 0\n" \
"v_mul_u32_u24_e32 v44, 0xb40, v44\n" \
"v_add_u32_e32 v35, v44, v10\n" \
"v_mfma_f32_16x16x32_bf16 v[140:143], a[12:15], a[236:239], 0\n" \
"v_readlane_b32 s69, v11, 22\n" \
"v_readlane_b32 s68, v11, 27\n" \
"v_mfma_f32_16x16x32_bf16 v[208:211], a[12:15], v[64:67], 0\n" \
"v_readlane_b32 s94, v11, 23\n" \
"v_readlane_b32 s93, v11, 28\n" \
"v_mfma_f32_16x16x32_bf16 v[212:215], a[12:15], v[80:83], 0\n" \
"ds_read_b64_tr_b16 a[12:13], v34 offset:18432\n" \
"ds_read_b64_tr_b16 a[14:15], v34 offset:23040\n" \
"v_mfma_f32_16x16x32_bf16 v[144:147], a[16:19], a[220:223], 0\n" \
"buffer_load_dwordx4 v1, s[80:83], s65 offen offset:1024 lds\n" \
"v_mfma_f32_16x16x32_bf16 v[148:151], a[16:19], a[236:239], 0\n" \
"v_readlane_b32 s96, v11, 24\n" \
"v_readlane_b32 s84, v11, 9\n" \
"v_mfma_f32_16x16x32_bf16 v[216:219], a[16:19], v[64:67], 0\n" \
"v_readlane_b32 s85, v11, 10\n" \
"s_mov_b32 s86, 0xa1ca0\n" \
"v_mfma_f32_16x16x32_bf16 v[220:223], a[16:19], v[80:83], 0\n" \
"ds_read_b64_tr_b16 a[16:17], v34 offset:18464\n" \
"s_waitcnt lgkmcnt(7)\n" \
"v_mfma_f32_16x16x32_bf16 v[152:155], a[20:23], a[220:223], 0\n" \
"ds_read_b64_tr_b16 a[18:19], v34 offset:23072\n" \
"s_mov_b32 s87, 0x20000\n" \
"v_mfma_f32_16x16x32_bf16 v[156:159], a[20:23], a[236:239], 0\n" \
"buffer_load_dwordx4 v2, s[80:83], s65 offen offset:2048 lds\n" \
"v_mfma_f32_16x16x32_bf16 v[224:227], a[20:23], v[64:67], 0\n" \
"v_readlane_b32 s70, v11, 30\n" \
"v_add_u32_e32 v33, 0x10000, v10\n" \
"v_mfma_f32_16x16x32_bf16 v[228:231], a[20:23], v[80:83], 0\n" \
"ds_read_b64_tr_b16 a[20:21], v34 offset:18496\n" \
"ds_read_b64_tr_b16 a[22:23], v34 offset:23104\n" \
"v_mfma_f32_16x16x32_bf16 v[160:163], a[24:27], a[220:223], 0\n" \
"v_readlane_b32 s71, v11, 31\n" \
"v_readlane_b32 s92, v11, 33\n" \
"v_mfma_f32_16x16x32_bf16 v[164:167], a[24:27], a[236:239], 0\n" \
"v_readlane_b32 s95, v11, 34\n" \
"v_lshlrev_b32_e32 v41, 12, v20\n" \
"v_mfma_f32_16x16x32_bf16 v[232:235], a[24:27], v[64:67], 0\n" \
"buffer_load_dwordx4 v3, s[80:83], s65 offen offset:3072 lds\n" \
"v_mfma_f32_16x16x32_bf16 v[236:239], a[24:27], v[80:83], 0\n" \
"ds_read_b64_tr_b16 a[24:25], v34 offset:18528\n" \
"ds_read_b64_tr_b16 a[26:27], v34 offset:23136\n" \
"v_mfma_f32_16x16x32_bf16 v[168:171], a[28:31], a[220:223], 0\n" \
"s_add_u32 m0, s66, 0x1000\n" \
"v_readlane_b32 s97, v11, 37\n" \
"v_mfma_f32_16x16x32_bf16 v[172:175], a[28:31], a[236:239], 0\n" \
"v_readlane_b32 s63, v11, 40\n" \
"v_bfe_u32 v40, v20, 2, 7\n" \
"v_mfma_f32_16x16x32_bf16 v[240:243], a[28:31], v[64:67], 0\n" \
"s_add_u32 s74, s63, 0x3800\n" \
"v_readlane_b32 s100, v11, 7\n" \
"v_mfma_f32_16x16x32_bf16 v[244:247], a[28:31], v[80:83], 0\n" \
"ds_read_b64_tr_b16 a[28:29], v34 offset:18560\n" \
"ds_read_b64_tr_b16 a[30:31], v34 offset:23168\n" \
"v_mfma_f32_16x16x32_bf16 v[176:179], a[192:195], a[220:223], 0\n" \
"buffer_load_dwordx4 v4, s[80:83], s65 offen offset:0 lds\n" \
"v_mfma_f32_16x16x32_bf16 v[180:183], a[192:195], a[236:239], 0\n" \
"v_bfe_u32 v44, v20, 4, 4\n" \
"v_bfe_u32 v45, v20, 8, 2\n" \
"v_mfma_f32_16x16x32_bf16 v[248:251], a[192:195], v[64:67], 0\n" \
"v_mul_u32_u24_e32 v42, 0x480, v44\n" \
"v_lshl_add_u32 v42, v45, 3, v42\n" \
"v_mfma_f32_16x16x32_bf16 v[252:255], a[192:195], v[80:83], 0\n" \
"ds_read_b64_tr_b16 a[192:193], v34 offset:18592\n" \
"ds_read_b64_tr_b16 a[194:195], v34 offset:23200\n" \
"v_mfma_f32_16x16x32_bf16 v[112:115], a[196:199], a[224:227], v[112:115]\n" \
"v_lshrrev_b32_e32 v44, 1, v44\n" \
"v_lshlrev_b32_e32 v45, 1, v45\n" \
"v_mfma_f32_16x16x32_bf16 v[116:119], a[196:199], a[240:243], v[116:119]\n" \
"buffer_load_dwordx4 v5, s[80:83], s65 offen offset:1024 lds\n" \
"v_mfma_f32_16x16x32_bf16 v[184:187], a[196:199], v[68:71], v[184:187]\n" \
"v_sub_u32_e32 v44, v44, v45\n" \
"v_and_b32_e32 v45, 16, v20\n" \
"v_mfma_f32_16x16x32_bf16 v[188:191], a[196:199], v[84:87], v[188:191]\n" \
"ds_read_b64_tr_b16 a[196:197], v34 offset:18624\n" \
"ds_read_b64_tr_b16 a[198:199], v34 offset:23232\n" \
"v_mfma_f32_16x16x32_bf16 v[120:123], a[200:203], a[224:227], v[120:123]\n" \
"v_mov_b32_e32 v46, 0x3f80\n" \
"v_lshlrev_b32_e32 v45, v45, v46\n" \
"v_mfma_f32_16x16x32_bf16 v[124:127], a[200:203], a[240:243], v[124:127]\n" \
"v_mov_b32_e32 v52, 0\n" \
"v_mov_b32_e32 v53, 0\n" \
"v_mfma_f32_16x16x32_bf16 v[192:195], a[200:203], v[68:71], v[192:195]\n" \
"buffer_load_dwordx4 v6, s[80:83], s65 offen offset:2048 lds\n" \
"v_mfma_f32_16x16x32_bf16 v[196:199], a[200:203], v[84:87], v[196:199]\n" \
"ds_read_b64_tr_b16 a[200:201], v34 offset:18656\n" \
"ds_read_b64_tr_b16 a[202:203], v34 offset:23264\n" \
"v_mfma_f32_16x16x32_bf16 v[128:131], a[204:207], a[224:227], v[128:131]\n" \
"s_waitcnt vmcnt(15)\n" \
"v_cmp_ne_u32_e32 vcc, s62, v26\n" \
"v_mfma_f32_16x16x32_bf16 v[132:135], a[204:207], a[240:243], v[132:135]\n" \
"v_mov_b32_e32 v48, 0\n" \
"v_mov_b32_e32 v49, 0\n" \
"v_mfma_f32_16x16x32_bf16 v[200:203], a[204:207], v[68:71], v[200:203]\n" \
"v_add_u32_e32 v43, 0x9000, v42\n" \
"s_cbranch_vccnz L_sa_%=\n" \
"L_ra_%=:\n" \
"v_mfma_f32_16x16x32_bf16 v[204:207], a[204:207], v[84:87], v[204:207]\n" \
"ds_read_b64_tr_b16 a[204:205], v34 offset:18688\n" \
"ds_read_b64_tr_b16 a[206:207], v34 offset:23296\n" \
"v_mfma_f32_16x16x32_bf16 v[136:139], a[208:211], a[224:227], v[136:139]\n" \
"buffer_load_dwordx4 v7, s[80:83], s65 offen offset:3072 lds\n" \
"v_mfma_f32_16x16x32_bf16 v[140:143], a[208:211], a[240:243], v[140:143]\n" \
"s_add_u32 m0, s66, 0x2000\n" \
"v_readlane_b32 s66, v11, 32\n" \
"v_mfma_f32_16x16x32_bf16 v[208:211], a[208:211], v[68:71], v[208:211]\n" \
"v_readlane_b32 s64, v11, 38\n" \
"v_readlane_b32 s88, v11, 13\n" \
"v_mfma_f32_16x16x32_bf16 v[212:215], a[208:211], v[84:87], v[212:215]\n" \
"v_readlane_b32 s89, v11, 14\n" \
"s_mov_b32 s90, 0x240000\n" \
"v_mfma_f32_16x16x32_bf16 v[144:147], a[212:215], a[224:227], v[144:147]\n" \
"s_mov_b32 s91, 0x20000\n" \
"v_mov_b32_e32 v46, s62\n" \
"v_mfma_f32_16x16x32_bf16 v[148:151], a[212:215], a[240:243], v[148:151]\n" \
"buffer_load_dwordx4 v8, s[80:83], s65 offen offset:0 lds\n" \
"v_mfma_f32_16x16x32_bf16 v[216:219], a[212:215], v[68:71], v[216:219]\n" \
"s_add_u32 m0, s69, 0x0\n" \
"v_readlane_b32 s65, v11, 39\n" \
"v_mfma_f32_16x16x32_bf16 v[220:223], a[212:215], v[84:87], v[220:223]\n" \
"v_mfma_f32_16x16x32_bf16 v[152:155], a[216:219], a[224:227], v[152:155]\n" \
"v_mfma_f32_16x16x32_bf16 v[156:159], a[216:219], a[240:243], v[156:159]\n" \
"v_mfma_f32_16x16x32_bf16 v[224:227], a[216:219], v[68:71], v[224:227]\n" \
"buffer_load_dwordx4 a[64:67], v20, s[56:59], s67 offen offset:0 sc1\n" \
"v_mfma_f32_16x16x32_bf16 v[228:231], a[216:219], v[84:87], v[228:231]\n" \
"v_mfma_f32_16x16x32_bf16 v[160:163], a[0:3], a[224:227], v[160:163]\n" \
"v_mfma_f32_16x16x32_bf16 v[164:167], a[0:3], a[240:243], v[164:167]\n" \
"v_mfma_f32_16x16x32_bf16 v[232:235], a[0:3], v[68:71], v[232:235]\n" \
"v_mfma_f32_16x16x32_bf16 v[236:239], a[0:3], v[84:87], v[236:239]\n" \
"s_waitcnt lgkmcnt(0)\n" \
"v_mfma_f32_16x16x32_bf16 v[168:171], a[4:7], a[224:227], v[168:171]\n" \
"buffer_load_dwordx4 a[80:83], v32, s[56:59], s67 offen offset:0 sc1\n" \
"v_mfma_f32_16x16x32_bf16 v[172:175], a[4:7], a[240:243], v[172:175]\n" \
"v_mfma_f32_16x16x32_bf16 v[240:243], a[4:7], v[68:71], v[240:243]\n" \
"v_mfma_f32_16x16x32_bf16 v[244:247], a[4:7], v[84:87], v[244:247]\n" \
"v_mfma_f32_16x16x32_bf16 v[176:179], a[8:11], a[224:227], v[176:179]\n" \
"v_mfma_f32_16x16x32_bf16 v[180:183], a[8:11], a[240:243], v[180:183]\n" \
"buffer_load_dwordx4 a[68:71], v20, s[56:59], s67 offen offset:1024 sc1\n" \
"v_mfma_f32_16x16x32_bf16 v[248:251], a[8:11], v[68:71], v[248:251]\n" \
"v_mfma_f32_16x16x32_bf16 v[252:255], a[8:11], v[84:87], v[252:255]\n" \
"v_mfma_f32_16x16x32_bf16 v[112:115], a[12:15], a[228:231], v[112:115]\n" \
"v_mfma_f32_16x16x32_bf16 v[116:119], a[12:15], a[244:247], v[116:119]\n" \
"v_mfma_f32_16x16x32_bf16 v[184:187], a[12:15], v[72:75], v[184:187]\n" \
"buffer_load_dwordx4 a[84:87], v32, s[56:59], s67 offen offset:1024 sc1\n" \
"v_mfma_f32_16x16x32_bf16 v[188:191], a[12:15], v[96:99], v[188:191]\n" \
"v_mfma_f32_16x16x32_bf16 v[120:123], a[16:19], a[228:231], v[120:123]\n" \
"v_mfma_f32_16x16x32_bf16 v[124:127], a[16:19], a[244:247], v[124:127]\n" \
"v_mfma_f32_16x16x32_bf16 v[192:195], a[16:19], v[72:75], v[192:195]\n" \
"v_mfma_f32_16x16x32_bf16 v[196:199], a[16:19], v[96:99], v[196:199]\n" \
"buffer_load_dwordx4 a[72:75], v20, s[56:59], s67 offen offset:2048 sc1\n" \
"v_mfma_f32_16x16x32_bf16 v[128:131], a[20:23], a[228:231], v[128:131]\n" \
"v_mfma_f32_16x16x32_bf16 v[132:135], a[20:23], a[244:247], v[132:135]\n" \
"v_mfma_f32_16x16x32_bf16 v[200:203], a[20:23], v[72:75], v[200:203]\n" \
"v_mfma_f32_16x16x32_bf16 v[204:207], a[20:23], v[96:99], v[204:207]\n" \
"s_barrier\n" \
"v_mfma_f32_16x16x32_bf16 v[136:139], a[24:27], a[228:231], v[136:139]\n" \
"ds_read_b64_tr_b16 a[208:209], v10 offset:52224\n" \
"ds_read_b64_tr_b16 a[210:211], v10 offset:56832\n" \
"v_mfma_f32_16x16x32_bf16 v[140:143], a[24:27], a[244:247], v[140:143]\n" \
"ds_read_b64_tr_b16 a[212:213], v10 offset:52256\n" \
"ds_read_b64_tr_b16 a[214:215], v10 offset:56864\n" \
"v_mfma_f32_16x16x32_bf16 v[208:211], a[24:27], v[72:75], v[208:211]\n" \
"ds_read_b64_tr_b16 a[216:217], v10 offset:52288\n" \
"ds_read_b64_tr_b16 a[218:219], v10 offset:56896\n" \
"v_mfma_f32_16x16x32_bf16 v[212:215], a[24:27], v[96:99], v[212:215]\n" \
"ds_read_b64_tr_b16 a[0:1], v10 offset:52320\n" \
"ds_read_b64_tr_b16 a[2:3], v10 offset:56928\n" \
"v_mfma_f32_16x16x32_bf16 v[144:147], a[28:31], a[228:231], v[144:147]\n" \
"ds_read_b64_tr_b16 a[4:5], v10 offset:52352\n" \
"ds_read_b64_tr_b16 a[6:7], v10 offset:56960\n" \
"v_mfma_f32_16x16x32_bf16 v[148:151], a[28:31], a[244:247], v[148:151]\n" \
"ds_read_b64_tr_b16 a[8:9], v10 offset:52384\n" \
"ds_read_b64_tr_b16 a[10:11], v10 offset:56992\n" \
"v_mfma_f32_16x16x32_bf16 v[216:219], a[28:31], v[72:75], v[216:219]\n" \
"ds_read_b64_tr_b16 a[12:13], v10 offset:52416\n" \
"ds_read_b64_tr_b16 a[14:15], v10 offset:57024\n" \
"v_mfma_f32_16x16x32_bf16 v[220:223], a[28:31], v[96:99], v[220:223]\n" \
"ds_read_b64_tr_b16 a[16:17], v10 offset:52448\n" \
"ds_read_b64_tr_b16 a[18:19], v10 offset:57056\n" \
"v_mfma_f32_16x16x32_bf16 v[152:155], a[192:195], a[228:231], v[152:155]\n" \
"ds_read_b64_tr_b16 a[20:21], v10 offset:52480\n" \
"ds_read_b64_tr_b16 a[22:23], v10 offset:57088\n" \
"v_mfma_f32_16x16x32_bf16 v[156:159], a[192:195], a[244:247], v[156:159]\n" \
"ds_read_b64_tr_b16 a[24:25], v35 offset:24576\n" \
"ds_read_b64_tr_b16 a[26:27], v35 offset:26880\n" \
"v_mfma_f32_16x16x32_bf16 v[224:227], a[192:195], v[72:75], v[224:227]\n" \
"ds_read_b64_tr_b16 a[28:29], v35 offset:24608\n" \
"ds_read_b64_tr_b16 a[30:31], v35 offset:26912\n" \
"v_mfma_f32_16x16x32_bf16 v[228:231], a[192:195], v[96:99], v[228:231]\n" \
"ds_read_b64_tr_b16 a[192:193], v35 offset:24640\n" \
"ds_read_b64_tr_b16 a[194:195], v35 offset:26944\n" \
"v_mfma_f32_16x16x32_bf16 v[160:163], a[196:199], a[228:231], v[160:163]\n" \
"buffer_load_dwordx4 a[88:91], v32, s[56:59], s67 offen offset:2048 sc1\n" \
"v_mfma_f32_16x16x32_bf16 v[164:167], a[196:199], a[244:247], v[164:167]\n" \
"v_mfma_f32_16x16x32_bf16 v[232:235], a[196:199], v[72:75], v[232:235]\n" \
"v_mfma_f32_16x16x32_bf16 v[236:239], a[196:199], v[96:99], v[236:239]\n" \
"ds_read_b64_tr_b16 a[196:197], v35 offset:24672\n" \
"ds_read_b64_tr_b16 a[198:199], v35 offset:26976\n" \
"v_mfma_f32_16x16x32_bf16 v[168:171], a[200:203], a[228:231], v[168:171]\n" \
"v_mfma_f32_16x16x32_bf16 v[172:175], a[200:203], a[244:247], v[172:175]\n" \
"buffer_load_dwordx4 v0, s[80:83], s68 offen offset:0 lds\n" \
"v_mfma_f32_16x16x32_bf16 v[240:243], a[200:203], v[72:75], v[240:243]\n" \
"v_mfma_f32_16x16x32_bf16 v[244:247], a[200:203], v[96:99], v[244:247]\n" \
"ds_read_b64_tr_b16 a[200:201], v35 offset:24704\n" \
"ds_read_b64_tr_b16 a[202:203], v35 offset:27008\n" \
"v_mfma_f32_16x16x32_bf16 v[176:179], a[204:207], a[228:231], v[176:179]\n" \
"v_mfma_f32_16x16x32_bf16 v[180:183], a[204:207], a[244:247], v[180:183]\n" \
"v_mfma_f32_16x16x32_bf16 v[248:251], a[204:207], v[72:75], v[248:251]\n" \
"buffer_load_dwordx4 v1, s[80:83], s68 offen offset:1024 lds\n" \
"v_mfma_f32_16x16x32_bf16 v[252:255], a[204:207], v[96:99], v[252:255]\n" \
"ds_read_b64_tr_b16 a[204:205], v35 offset:24736\n" \
"s_waitcnt lgkmcnt(9)\n" \
"v_mfma_f32_16x16x32_bf16 v[112:115], a[208:211], a[232:235], v[112:115]\n" \
"ds_read_b64_tr_b16 a[206:207], v35 offset:27040\n" \
"v_mfma_f32_16x16x32_bf16 v[116:119], a[208:211], a[248:251], v[116:119]\n" \
"v_mfma_f32_16x16x32_bf16 v[184:187], a[208:211], v[76:79], v[184:187]\n" \
"v_mfma_f32_16x16x32_bf16 v[188:191], a[208:211], v[100:103], v[188:191]\n" \
"ds_read_b64_tr_b16 a[208:209], v35 offset:24768\n" \
"ds_read_b64_tr_b16 a[210:211], v35 offset:27072\n" \
"v_mfma_f32_16x16x32_bf16 v[120:123], a[212:215], a[232:235], v[120:123]\n" \
"buffer_load_dwordx4 v2, s[80:83], s68 offen offset:2048 lds\n" \
"v_mfma_f32_16x16x32_bf16 v[124:127], a[212:215], a[248:251], v[124:127]\n" \
"v_mfma_f32_16x16x32_bf16 v[192:195], a[212:215], v[76:79], v[192:195]\n" \
"s_waitcnt vmcnt(18)\n" \
"s_mov_b64 exec, 1\n" \
"buffer_store_dword v39, off, s[56:59], s72 offset:0 sc1\n" \
"s_mov_b64 exec, -1\n" \
"v_mfma_f32_16x16x32_bf16 v[196:199], a[212:215], v[100:103], v[196:199]\n" \
"ds_read_b64_tr_b16 a[212:213], v35 offset:24800\n" \
"ds_read_b64_tr_b16 a[214:215], v35 offset:27104\n" \
"v_mfma_f32_16x16x32_bf16 v[128:131], a[216:219], a[232:235], v[128:131]\n" \
"v_readlane_b32 s72, v11, 50\n" \
"v_mfma_f32_16x16x32_bf16 v[132:135], a[216:219], a[248:251], v[132:135]\n" \
"v_mfma_f32_16x16x32_bf16 v[200:203], a[216:219], v[76:79], v[200:203]\n" \
"buffer_load_dwordx4 v3, s[80:83], s68 offen offset:3072 lds\n" \
"v_mfma_f32_16x16x32_bf16 v[204:207], a[216:219], v[100:103], v[204:207]\n" \
"ds_read_b64_tr_b16 a[216:217], v35 offset:24832\n" \
"ds_read_b64_tr_b16 a[218:219], v35 offset:27136\n" \
"v_mfma_f32_16x16x32_bf16 v[136:139], a[0:3], a[232:235], v[136:139]\n" \
"s_add_u32 m0, s69, 0x1000\n" \
"v_mfma_f32_16x16x32_bf16 v[140:143], a[0:3], a[248:251], v[140:143]\n" \
"v_mfma_f32_16x16x32_bf16 v[208:211], a[0:3], v[76:79], v[208:211]\n" \
"v_mfma_f32_16x16x32_bf16 v[212:215], a[0:3], v[100:103], v[212:215]\n" \
"ds_read_b64_tr_b16 a[0:1], v35 offset:34944\n" \
"ds_read_b64_tr_b16 a[2:3], v35 offset:37248\n" \
"v_mfma_f32_16x16x32_bf16 v[144:147], a[4:7], a[232:235], v[144:147]\n" \
"buffer_load_dwordx4 v4, s[80:83], s68 offen offset:0 lds\n" \
"v_mfma_f32_16x16x32_bf16 v[148:151], a[4:7], a[248:251], v[148:151]\n" \
"v_mfma_f32_16x16x32_bf16 v[216:219], a[4:7], v[76:79], v[216:219]\n" \
"v_mfma_f32_16x16x32_bf16 v[220:223], a[4:7], v[100:103], v[220:223]\n" \
"ds_read_b64_tr_b16 a[4:5], v35 offset:34976\n" \
"ds_read_b64_tr_b16 a[6:7], v35 offset:37280\n" \
"v_mfma_f32_16x16x32_bf16 v[152:155], a[8:11], a[232:235], v[152:155]\n" \
"v_mfma_f32_16x16x32_bf16 v[156:159], a[8:11], a[248:251], v[156:159]\n" \
"buffer_load_dwordx4 v5, s[80:83], s68 offen offset:1024 lds\n" \
"v_mfma_f32_16x16x32_bf16 v[224:227], a[8:11], v[76:79], v[224:227]\n" \
"v_mfma_f32_16x16x32_bf16 v[228:231], a[8:11], v[100:103], v[228:231]\n" \
"ds_read_b64_tr_b16 a[8:9], v35 offset:35008\n" \
"ds_read_b64_tr_b16 a[10:11], v35 offset:37312\n" \
"v_mfma_f32_16x16x32_bf16 v[160:163], a[12:15], a[232:235], v[160:163]\n" \
"v_mfma_f32_16x16x32_bf16 v[164:167], a[12:15], a[248:251], v[164:167]\n" \
"v_mfma_f32_16x16x32_bf16 v[232:235], a[12:15], v[76:79], v[232:235]\n" \
"buffer_load_dwordx4 v6, s[80:83], s68 offen offset:2048 lds\n" \
"v_mfma_f32_16x16x32_bf16 v[236:239], a[12:15], v[100:103], v[236:239]\n" \
"ds_read_b64_tr_b16 a[12:13], v35 offset:35040\n" \
"ds_read_b64_tr_b16 a[14:15], v35 offset:37344\n" \
"v_mfma_f32_16x16x32_bf16 v[168:171], a[16:19], a[232:235], v[168:171]\n" \
"v_mfma_f32_16x16x32_bf16 v[172:175], a[16:19], a[248:251], v[172:175]\n" \
"v_mfma_f32_16x16x32_bf16 v[240:243], a[16:19], v[76:79], v[240:243]\n" \
"v_mfma_f32_16x16x32_bf16 v[244:247], a[16:19], v[100:103], v[244:247]\n" \
"buffer_load_dwordx4 v7, s[80:83], s68 offen offset:3072 lds\n" \
"v_mfma_f32_16x16x32_bf16 v[176:179], a[20:23], a[232:235], v[176:179]\n" \
"s_add_u32 m0, s69, 0x2000\n" \
"ds_read_b64_tr_b16 a[16:17], v35 offset:35072\n" \
"v_mfma_f32_16x16x32_bf16 v[180:183], a[20:23], a[248:251], v[180:183]\n" \
"ds_read_b64_tr_b16 a[18:19], v35 offset:37376\n" \
"v_readlane_b32 s69, v11, 47\n" \
"v_mfma_f32_16x16x32_bf16 v[248:251], a[20:23], v[76:79], v[248:251]\n" \
"v_mfma_f32_16x16x32_bf16 v[252:255], a[20:23], v[100:103], v[252:255]\n" \
"ds_read_b64_tr_b16 a[20:21], v35 offset:35104\n" \
"ds_read_b64_tr_b16 a[22:23], v35 offset:37408\n" \
"v_mfma_f32_16x16x32_bf16 v[112:115], a[24:27], a[96:99], v[112:115]\n" \
"buffer_load_dwordx4 v8, s[80:83], s68 offen offset:0 lds\n" \
"v_mfma_f32_16x16x32_bf16 v[116:119], a[24:27], a[104:107], v[116:119]\n" \
"s_add_u32 m0, s94, 0x0\n" \
"v_readlane_b32 s68, v11, 46\n" \
"v_mfma_f32_16x16x32_bf16 v[184:187], a[24:27], a[112:115], v[184:187]\n" \
"v_mfma_f32_16x16x32_bf16 v[188:191], a[24:27], a[120:123], v[188:191]\n" \
"ds_read_b64_tr_b16 a[24:25], v35 offset:35136\n" \
"s_waitcnt lgkmcnt(7)\n" \
"v_mfma_f32_16x16x32_bf16 v[120:123], a[28:31], a[96:99], v[120:123]\n" \
"ds_read_b64_tr_b16 a[26:27], v35 offset:37440\n" \
"buffer_load_dword v47, v38, s[56:59], s73 offen sc1\n" \
"v_mfma_f32_16x16x32_bf16 v[124:127], a[28:31], a[104:107], v[124:127]\n" \
"v_mfma_f32_16x16x32_bf16 v[192:195], a[28:31], a[112:115], v[192:195]\n" \
"v_mfma_f32_16x16x32_bf16 v[196:199], a[28:31], a[120:123], v[196:199]\n" \
"ds_read_b64_tr_b16 a[28:29], v35 offset:35168\n" \
"ds_read_b64_tr_b16 a[30:31], v35 offset:37472\n" \
"v_mfma_f32_16x16x32_bf16 v[128:131], a[192:195], a[96:99], v[128:131]\n" \
"v_mfma_f32_16x16x32_bf16 v[132:135], a[192:195], a[104:107], v[132:135]\n" \
"buffer_load_dwordx4 v0, s[80:83], s93 offen offset:0 lds\n" \
"v_mfma_f32_16x16x32_bf16 v[200:203], a[192:195], a[112:115], v[200:203]\n" \
"v_mfma_f32_16x16x32_bf16 v[204:207], a[192:195], a[120:123], v[204:207]\n" \
"ds_read_b64_tr_b16 a[192:193], v35 offset:35200\n" \
"ds_read_b64_tr_b16 a[194:195], v35 offset:37504\n" \
"v_mfma_f32_16x16x32_bf16 v[136:139], a[196:199], a[96:99], v[136:139]\n" \
"v_mfma_f32_16x16x32_bf16 v[140:143], a[196:199], a[104:107], v[140:143]\n" \
"v_mfma_f32_16x16x32_bf16 v[208:211], a[196:199], a[112:115], v[208:211]\n" \
"buffer_load_dwordx4 v1, s[80:83], s93 offen offset:1024 lds\n" \
"v_mfma_f32_16x16x32_bf16 v[212:215], a[196:199], a[120:123], v[212:215]\n" \
"v_mfma_f32_16x16x32_bf16 v[144:147], a[200:203], a[96:99], v[144:147]\n" \
"v_mfma_f32_16x16x32_bf16 v[148:151], a[200:203], a[104:107], v[148:151]\n" \
"v_mfma_f32_16x16x32_bf16 v[216:219], a[200:203], a[112:115], v[216:219]\n" \
"v_mfma_f32_16x16x32_bf16 v[220:223], a[200:203], a[120:123], v[220:223]\n" \
"buffer_load_dwordx4 v2, s[80:83], s93 offen offset:2048 lds\n" \
"v_mfma_f32_16x16x32_bf16 v[152:155], a[204:207], a[96:99], v[152:155]\n" \
"v_mfma_f32_16x16x32_bf16 v[156:159], a[204:207], a[104:107], v[156:159]\n" \
"v_mfma_f32_16x16x32_bf16 v[224:227], a[204:207], a[112:115], v[224:227]\n" \
"v_mfma_f32_16x16x32_bf16 v[228:231], a[204:207], a[120:123], v[228:231]\n" \
"v_mfma_f32_16x16x32_bf16 v[160:163], a[208:211], a[96:99], v[160:163]\n" \
"buffer_load_dwordx4 v3, s[80:83], s93 offen offset:3072 lds\n" \
"v_mfma_f32_16x16x32_bf16 v[164:167], a[208:211], a[104:107], v[164:167]\n" \
"s_add_u32 m0, s94, 0x1000\n" \
"v_mfma_f32_16x16x32_bf16 v[232:235], a[208:211], a[112:115], v[232:235]\n" \
"v_mfma_f32_16x16x32_bf16 v[236:239], a[208:211], a[120:123], v[236:239]\n" \
"v_mfma_f32_16x16x32_bf16 v[168:171], a[212:215], a[96:99], v[168:171]\n" \
"v_mfma_f32_16x16x32_bf16 v[172:175], a[212:215], a[104:107], v[172:175]\n" \
"buffer_load_dwordx4 v4, s[80:83], s93 offen offset:0 lds\n" \
"v_mfma_f32_16x16x32_bf16 v[240:243], a[212:215], a[112:115], v[240:243]\n" \
"v_mfma_f32_16x16x32_bf16 v[244:247], a[212:215], a[120:123], v[244:247]\n" \
"v_mfma_f32_16x16x32_bf16 v[176:179], a[216:219], a[96:99], v[176:179]\n" \
"v_mfma_f32_16x16x32_bf16 v[180:183], a[216:219], a[104:107], v[180:183]\n" \
"v_mfma_f32_16x16x32_bf16 v[248:251], a[216:219], a[112:115], v[248:251]\n" \
"buffer_load_dwordx4 v5, s[80:83], s93 offen offset:1024 lds\n" \
"v_mfma_f32_16x16x32_bf16 v[252:255], a[216:219], a[120:123], v[252:255]\n" \
"v_mfma_f32_16x16x32_bf16 v[112:115], a[0:3], a[100:103], v[112:115]\n" \
"v_mfma_f32_16x16x32_bf16 v[116:119], a[0:3], a[108:111], v[116:119]\n" \
"v_mfma_f32_16x16x32_bf16 v[184:187], a[0:3], a[116:119], v[184:187]\n" \
"v_mfma_f32_16x16x32_bf16 v[188:191], a[0:3], a[124:127], v[188:191]\n" \
"buffer_load_dwordx4 v6, s[80:83], s93 offen offset:2048 lds\n" \
"v_mfma_f32_16x16x32_bf16 v[120:123], a[4:7], a[100:103], v[120:123]\n" \
"v_mfma_f32_16x16x32_bf16 v[124:127], a[4:7], a[108:111], v[124:127]\n" \
"v_mfma_f32_16x16x32_bf16 v[192:195], a[4:7], a[116:119], v[192:195]\n" \
"v_mfma_f32_16x16x32_bf16 v[196:199], a[4:7], a[124:127], v[196:199]\n" \
"v_mfma_f32_16x16x32_bf16 v[128:131], a[8:11], a[100:103], v[128:131]\n" \
"buffer_load_dwordx4 v7, s[80:83], s93 offen offset:3072 lds\n" \
"s_add_u32 m0, s94, 0x2000\n" \
"v_mfma_f32_16x16x32_bf16 v[132:135], a[8:11], a[108:111], v[132:135]\n" \
"v_readlane_b32 s94, v11, 45\n" \
"v_mfma_f32_16x16x32_bf16 v[200:203], a[8:11], a[116:119], v[200:203]\n" \
"v_mfma_f32_16x16x32_bf16 v[204:207], a[8:11], a[124:127], v[204:207]\n" \
"s_waitcnt lgkmcnt(0)\n" \
"v_mfma_f32_16x16x32_bf16 v[136:139], a[12:15], a[100:103], v[136:139]\n" \
"buffer_load_dwordx4 v8, s[80:83], s93 offen offset:0 lds\n" \
"s_mov_b32 m0, s96\n" \
"v_mfma_f32_16x16x32_bf16 v[140:143], a[12:15], a[108:111], v[140:143]\n" \
"v_readlane_b32 s96, v11, 47\n" \
"v_readlane_b32 s93, v11, 6\n" \
"v_mfma_f32_16x16x32_bf16 v[208:211], a[12:15], a[116:119], v[208:211]\n" \
"v_mfma_f32_16x16x32_bf16 v[212:215], a[12:15], a[124:127], v[212:215]\n" \
"s_waitcnt vmcnt(23)\n" \
"s_barrier\n" \
"buffer_load_dwordx4 v9, s[84:87], 0 offen lds\n" \
"v_mfma_f32_16x16x32_bf16 v[144:147], a[16:19], a[100:103], v[144:147]\n" \
"ds_read_b64_tr_b16 a[196:197], v33 offset:0\n" \
"ds_read_b64_tr_b16 a[198:199], v33 offset:4608\n" \
"v_mfma_f32_16x16x32_bf16 v[148:151], a[16:19], a[108:111], v[148:151]\n" \
"ds_read_b64_tr_b16 a[200:201], v33 offset:32\n" \
"ds_read_b64_tr_b16 a[202:203], v33 offset:4640\n" \
"v_mfma_f32_16x16x32_bf16 v[216:219], a[16:19], a[116:119], v[216:219]\n" \
"ds_read_b64_tr_b16 a[204:205], v33 offset:64\n" \
"ds_read_b64_tr_b16 a[206:207], v33 offset:4672\n" \
"v_mfma_f32_16x16x32_bf16 v[220:223], a[16:19], a[124:127], v[220:223]\n" \
"ds_read_b64_tr_b16 a[208:209], v33 offset:96\n" \
"ds_read_b64_tr_b16 a[210:211], v33 offset:4704\n" \
"buffer_load_dwordx4 a[76:79], v20, s[56:59], s67 offen offset:3072 sc1\n" \
"v_mfma_f32_16x16x32_bf16 v[152:155], a[20:23], a[100:103], v[152:155]\n" \
"ds_read_b64_tr_b16 a[212:213], v33 offset:128\n" \
"ds_read_b64_tr_b16 a[214:215], v33 offset:4736\n" \
"v_mfma_f32_16x16x32_bf16 v[156:159], a[20:23], a[108:111], v[156:159]\n" \
"ds_read_b64_tr_b16 a[216:217], v33 offset:160\n" \
"ds_read_b64_tr_b16 a[218:219], v33 offset:4768\n" \
"v_mfma_f32_16x16x32_bf16 v[224:227], a[20:23], a[116:119], v[224:227]\n" \
"ds_read_b64_tr_b16 a[0:1], v33 offset:192\n" \
"ds_read_b64_tr_b16 a[2:3], v33 offset:4800\n" \
"v_mfma_f32_16x16x32_bf16 v[228:231], a[20:23], a[124:127], v[228:231]\n" \
"ds_read_b64_tr_b16 a[4:5], v33 offset:224\n" \
"ds_read_b64_tr_b16 a[6:7], v33 offset:4832\n" \
"buffer_load_dwordx4 a[92:95], v32, s[56:59], s67 offen offset:3072 sc1\n" \
"v_mfma_f32_16x16x32_bf16 v[160:163], a[24:27], a[100:103], v[160:163]\n" \
"v_readlane_b32 s67, v11, 45\n" \
"ds_read_b64_tr_b16 a[8:9], v33 offset:256\n" \
"v_mfma_f32_16x16x32_bf16 v[164:167], a[24:27], a[108:111], v[164:167]\n" \
"ds_read_b64_tr_b16 a[10:11], v33 offset:4864\n" \
"ds_read_b64_tr_b16 a[12:13], v33 offset:9216\n" \
"v_mfma_f32_16x16x32_bf16 v[232:235], a[24:27], a[116:119], v[232:235]\n" \
"ds_read_b64_tr_b16 a[14:15], v33 offset:13824\n" \
"ds_read_b64_tr_b16 a[16:17], v33 offset:9248\n" \
"v_mfma_f32_16x16x32_bf16 v[236:239], a[24:27], a[124:127], v[236:239]\n" \
"ds_read_b64_tr_b16 a[18:19], v33 offset:13856\n" \
"ds_read_b64_tr_b16 a[20:21], v33 offset:9280\n" \
"buffer_load_dwordx4 a[128:131], v20, s[56:59], s70 offen offset:0 sc1\n" \
"v_mfma_f32_16x16x32_bf16 v[168:171], a[28:31], a[100:103], v[168:171]\n" \
"ds_read_b64_tr_b16 a[22:23], v33 offset:13888\n" \
"ds_read_b64_tr_b16 a[24:25], v33 offset:9312\n" \
"v_mfma_f32_16x16x32_bf16 v[172:175], a[28:31], a[108:111], v[172:175]\n" \
"ds_read_b64_tr_b16 a[26:27], v33 offset:13920\n" \
"s_waitcnt vmcnt(13)\n" \
"v_cmp_ne_u32_e32 vcc, s62, v47\n" \
"v_mfma_f32_16x16x32_bf16 v[240:243], a[28:31], a[116:119], v[240:243]\n" \
"v_mfma_f32_16x16x32_bf16 v[244:247], a[28:31], a[124:127], v[244:247]\n" \
"buffer_load_dwordx4 a[144:147], v32, s[56:59], s70 offen offset:0 sc1\n" \
"v_mfma_f32_16x16x32_bf16 v[176:179], a[192:195], a[100:103], v[176:179]\n" \
"ds_read_b64_tr_b16 a[28:29], v33 offset:9344\n" \
"ds_read_b64_tr_b16 a[30:31], v33 offset:13952\n" \
"v_mfma_f32_16x16x32_bf16 v[180:183], a[192:195], a[108:111], v[180:183]\n" \
"s_cbranch_vccnz L_sb_%=\n" \
"L_rb_%=:\n" \
"v_readlane_b32 s73, v11, 51\n" \
"v_mfma_f32_16x16x32_bf16 v[248:251], a[192:195], a[116:119], v[248:251]\n" \
"v_cmp_eq_u32_e32 vcc, 0, v44\n" \
"v_mfma_f32_16x16x32_bf16 v[252:255], a[192:195], a[124:127], v[252:255]\n" \
"ds_read_b64_tr_b16 a[192:193], v33 offset:9376\n" \
"buffer_load_dwordx4 a[132:135], v20, s[56:59], s70 offen offset:1024 sc1\n" \
"s_waitcnt lgkmcnt(14)\n" \
"v_mfma_f32_16x16x32_bf16 v[112:115], a[196:199], a[64:67], v[112:115]\n" \
"ds_read_b64_tr_b16 a[194:195], v33 offset:13984\n" \
"v_cndmask_b32_e32 v50, 0, v45, vcc\n" \
"v_mfma_f32_16x16x32_bf16 v[116:119], a[196:199], a[80:83], v[116:119]\n" \
"ds_read_b64_tr_b16 a[196:197], v33 offset:9408\n" \
"ds_read_b64_tr_b16 a[198:199], v33 offset:14016\n" \
"v_mfma_f32_16x16x32_bf16 v[120:123], a[200:203], a[64:67], v[120:123]\n" \
"v_cmp_eq_u32_e32 vcc, 1, v44\n" \
"v_and_b32_e32 v44, 0x3c0, v20\n" \
"v_mfma_f32_16x16x32_bf16 v[124:127], a[200:203], a[80:83], v[124:127]\n" \
"buffer_load_dwordx4 a[148:151], v32, s[56:59], s70 offen offset:1024 sc1\n" \
"v_mfma_f32_16x16x32_bf16 v[128:131], a[204:207], a[64:67], v[128:131]\n" \
"ds_read_b64_tr_b16 a[200:201], v33 offset:9440\n" \
"ds_read_b64_tr_b16 a[202:203], v33 offset:14048\n" \
"v_mfma_f32_16x16x32_bf16 v[132:135], a[204:207], a[80:83], v[132:135]\n" \
"ds_read_b64_tr_b16 a[204:205], v33 offset:9472\n" \
"ds_read_b64_tr_b16 a[206:207], v33 offset:14080\n" \
"v_mfma_f32_16x16x32_bf16 v[136:139], a[208:211], a[64:67], v[136:139]\n" \
"v_cndmask_b32_e32 v51, 0, v45, vcc\n" \
"v_bfe_u32 v45, v20, 1, 5\n" \
"v_mfma_f32_16x16x32_bf16 v[140:143], a[208:211], a[80:83], v[140:143]\n" \
"ds_read_b64_tr_b16 a[208:209], v33 offset:18432\n" \
"ds_read_b64_tr_b16 a[210:211], v33 offset:23040\n" \
"buffer_load_dwordx4 a[136:139], v20, s[56:59], s70 offen offset:2048 sc1\n" \
"v_mfma_f32_16x16x32_bf16 v[144:147], a[212:215], a[64:67], v[144:147]\n" \
"v_and_b32_e32 v45, 24, v45\n" \
"v_add_u32_e32 v45, 0xf000, v45\n" \
"v_mfma_f32_16x16x32_bf16 v[148:151], a[212:215], a[80:83], v[148:151]\n" \
"ds_read_b64_tr_b16 a[212:213], v33 offset:18464\n" \
"ds_read_b64_tr_b16 a[214:215], v33 offset:23072\n" \
"v_mfma_f32_16x16x32_bf16 v[152:155], a[216:219], a[64:67], v[152:155]\n" \
"v_add_u32_e32 v36, v44, v45\n" \
"v_mfma_f32_16x16x32_bf16 v[156:159], a[216:219], a[80:83], v[156:159]\n" \
"ds_read_b64_tr_b16 a[216:217], v33 offset:18496\n" \
"ds_read_b64_tr_b16 a[218:219], v33 offset:23104\n" \
"buffer_load_dwordx4 a[152:155], v32, s[56:59], s70 offen offset:2048 sc1\n" \
"v_mfma_f32_16x16x32_bf16 v[160:163], a[0:3], a[64:67], v[160:163]\n" \
"v_mfma_f32_16x16x32_bf16 v[164:167], a[0:3], a[80:83], v[164:167]\n" \
"ds_read_b64_tr_b16 a[0:1], v33 offset:18528\n" \
"s_waitcnt lgkmcnt(13)\n" \
"v_mfma_f32_16x16x32_bf16 v[168:171], a[4:7], a[64:67], v[168:171]\n" \
"ds_read_b64_tr_b16 a[2:3], v33 offset:23136\n" \
"v_mfma_f32_16x16x32_bf16 v[172:175], a[4:7], a[80:83], v[172:175]\n" \
"ds_read_b64_tr_b16 a[4:5], v33 offset:18560\n" \
"ds_read_b64_tr_b16 a[6:7], v33 offset:23168\n" \
"buffer_load_dwordx4 a[140:143], v20, s[56:59], s70 offen offset:3072 sc1\n" \
"v_mfma_f32_16x16x32_bf16 v[176:179], a[8:11], a[64:67], v[176:179]\n" \
"v_mfma_f32_16x16x32_bf16 v[180:183], a[8:11], a[80:83], v[180:183]\n" \
"ds_read_b64_tr_b16 a[8:9], v33 offset:18592\n" \
"ds_read_b64_tr_b16 a[10:11], v33 offset:23200\n" \
"v_mfma_f32_16x16x32_bf16 v[112:115], a[12:15], a[68:71], v[112:115]\n" \
"v_mfma_f32_16x16x32_bf16 v[116:119], a[12:15], a[84:87], v[116:119]\n" \
"ds_read_b64_tr_b16 a[12:13], v33 offset:18624\n" \
"ds_read_b64_tr_b16 a[14:15], v33 offset:23232\n" \
"buffer_load_dwordx4 a[156:159], v32, s[56:59], s70 offen offset:3072 sc1\n" \
"v_mfma_f32_16x16x32_bf16 v[120:123], a[16:19], a[68:71], v[120:123]\n" \
"v_readlane_b32 s70, v11, 48\n" \
"v_mfma_f32_16x16x32_bf16 v[124:127], a[16:19], a[84:87], v[124:127]\n" \
"ds_read_b64_tr_b16 a[16:17], v33 offset:18656\n" \
"ds_read_b64_tr_b16 a[18:19], v33 offset:23264\n" \
"v_mfma_f32_16x16x32_bf16 v[128:131], a[20:23], a[68:71], v[128:131]\n" \
"v_mfma_f32_16x16x32_bf16 v[132:135], a[20:23], a[84:87], v[132:135]\n" \
"ds_read_b64_tr_b16 a[20:21], v33 offset:18688\n" \
"ds_read_b64_tr_b16 a[22:23], v33 offset:23296\n" \
"buffer_load_dwordx4 a[32:35], v20, s[56:59], s71 offen offset:0 sc1\n" \
"v_mfma_f32_16x16x32_bf16 v[136:139], a[24:27], a[68:71], v[136:139]\n" \
"v_mfma_f32_16x16x32_bf16 v[140:143], a[24:27], a[84:87], v[140:143]\n" \
"ds_read_b64_tr_b16 a[24:25], v33 offset:27648\n" \
"ds_read_b64_tr_b16 a[26:27], v33 offset:32256\n" \
"v_mfma_f32_16x16x32_bf16 v[144:147], a[28:31], a[68:71], v[144:147]\n" \
"v_mfma_f32_16x16x32_bf16 v[148:151], a[28:31], a[84:87], v[148:151]\n" \
"ds_read_b64_tr_b16 a[28:29], v33 offset:27680\n" \
"ds_read_b64_tr_b16 a[30:31], v33 offset:32288\n" \
"buffer_load_dwordx4 a[48:51], v32, s[56:59], s71 offen offset:0 sc1\n" \
"v_mfma_f32_16x16x32_bf16 v[152:155], a[192:195], a[68:71], v[152:155]\n" \
"v_mfma_f32_16x16x32_bf16 v[156:159], a[192:195], a[84:87], v[156:159]\n" \
"ds_read_b64_tr_b16 a[192:193], v33 offset:27712\n" \
"s_waitcnt lgkmcnt(13)\n" \
"v_mfma_f32_16x16x32_bf16 v[160:163], a[196:199], a[68:71], v[160:163]\n" \
"ds_read_b64_tr_b16 a[194:195], v33 offset:32320\n" \
"v_mfma_f32_16x16x32_bf16 v[164:167], a[196:199], a[84:87], v[164:167]\n" \
"ds_read_b64_tr_b16 a[196:197], v33 offset:27744\n" \
"ds_read_b64_tr_b16 a[198:199], v33 offset:32352\n" \
"buffer_load_dwordx4 a[36:39], v20, s[56:59], s71 offen offset:1024 sc1\n" \
"v_mfma_f32_16x16x32_bf16 v[168:171], a[200:203], a[68:71], v[168:171]\n" \
"v_mfma_f32_16x16x32_bf16 v[172:175], a[200:203], a[84:87], v[172:175]\n" \
"ds_read_b64_tr_b16 a[200:201], v33 offset:27776\n" \
"ds_read_b64_tr_b16 a[202:203], v33 offset:32384\n" \
"v_mfma_f32_16x16x32_bf16 v[176:179], a[204:207], a[68:71], v[176:179]\n" \
"v_mfma_f32_16x16x32_bf16 v[180:183], a[204:207], a[84:87], v[180:183]\n" \
"ds_read_b64_tr_b16 a[204:205], v33 offset:27808\n" \
"ds_read_b64_tr_b16 a[206:207], v33 offset:32416\n" \
"buffer_load_dwordx4 a[52:55], v32, s[56:59], s71 offen offset:1024 sc1\n" \
"v_mfma_f32_16x16x32_bf16 v[112:115], a[208:211], a[72:75], v[112:115]\n" \
"v_mfma_f32_16x16x32_bf16 v[116:119], a[208:211], a[88:91], v[116:119]\n" \
"ds_read_b64_tr_b16 a[208:209], v33 offset:27840\n" \
"ds_read_b64_tr_b16 a[210:211], v33 offset:32448\n" \
"v_mfma_f32_16x16x32_bf16 v[120:123], a[212:215], a[72:75], v[120:123]\n" \
"v_mfma_f32_16x16x32_bf16 v[124:127], a[212:215], a[88:91], v[124:127]\n" \
"ds_read_b64_tr_b16 a[212:213], v33 offset:27872\n" \
"ds_read_b64_tr_b16 a[214:215], v33 offset:32480\n" \
"buffer_load_dwordx4 a[40:43], v20, s[56:59], s71 offen offset:2048 sc1\n" \
"v_mfma_f32_16x16x32_bf16 v[128:131], a[216:219], a[72:75], v[128:131]\n" \
"v_mfma_f32_16x16x32_bf16 v[132:135], a[216:219], a[88:91], v[132:135]\n" \
"ds_read_b64_tr_b16 a[216:217], v33 offset:27904\n" \
"ds_read_b64_tr_b16 a[218:219], v33 offset:32512\n" \
"v_mfma_f32_16x16x32_bf16 v[136:139], a[0:3], a[72:75], v[136:139]\n" \
"v_mfma_f32_16x16x32_bf16 v[140:143], a[0:3], a[88:91], v[140:143]\n" \
"v_mfma_f32_16x16x32_bf16 v[144:147], a[4:7], a[72:75], v[144:147]\n" \
"buffer_load_dwordx4 a[56:59], v32, s[56:59], s71 offen offset:2048 sc1\n" \
"v_mfma_f32_16x16x32_bf16 v[148:151], a[4:7], a[88:91], v[148:151]\n" \
"s_waitcnt lgkmcnt(10)\n" \
"v_mfma_f32_16x16x32_bf16 v[152:155], a[8:11], a[72:75], v[152:155]\n" \
"v_mfma_f32_16x16x32_bf16 v[156:159], a[8:11], a[88:91], v[156:159]\n" \
"v_mfma_f32_16x16x32_bf16 v[160:163], a[12:15], a[72:75], v[160:163]\n" \
"v_mfma_f32_16x16x32_bf16 v[164:167], a[12:15], a[88:91], v[164:167]\n" \
"buffer_load_dwordx4 a[44:47], v20, s[56:59], s71 offen offset:3072 sc1\n" \
"v_mfma_f32_16x16x32_bf16 v[168:171], a[16:19], a[72:75], v[168:171]\n" \
"v_mfma_f32_16x16x32_bf16 v[172:175], a[16:19], a[88:91], v[172:175]\n" \
"v_mfma_f32_16x16x32_bf16 v[176:179], a[20:23], a[72:75], v[176:179]\n" \
"v_mfma_f32_16x16x32_bf16 v[180:183], a[20:23], a[88:91], v[180:183]\n" \
"buffer_load_dwordx4 a[60:63], v32, s[56:59], s71 offen offset:3072 sc1\n" \
"v_readlane_b32 s71, v11, 49\n" \
"buffer_load_dwordx4 a[96:99], v20, s[56:59], s66 offen offset:0 sc1\n" \
"buffer_load_dwordx4 a[112:115], v32, s[56:59], s66 offen offset:0 sc1\n" \
"s_waitcnt vmcnt(20)\n" \
"s_barrier\n" \
"ds_read_b64_tr_b16 a[0:1], v33 offset:36864\n" \
"ds_read_b64_tr_b16 a[2:3], v33 offset:41472\n" \
"ds_read_b64_tr_b16 a[4:5], v33 offset:46080\n" \
"ds_read_b64_tr_b16 a[6:7], v33 offset:50688\n" \
"ds_read_b64_tr_b16 a[8:9], v33 offset:55296\n" \
"ds_read_b64_tr_b16 a[10:11], v33 offset:59904\n" \
"ds_read_b64_tr_b16 a[12:13], v10 offset:0\n" \
"ds_read_b64_tr_b16 a[14:15], v10 offset:4608\n" \
"ds_read_b64_tr_b16 a[16:17], v34 offset:0\n" \
"ds_read_b64_tr_b16 a[18:19], v34 offset:4608\n" \
"ds_read_b64_tr_b16 a[20:21], v34 offset:9216\n" \
"ds_read_b64_tr_b16 a[22:23], v34 offset:13824\n" \
"buffer_load_dwordx4 a[100:103], v20, s[56:59], s66 offen offset:1024 sc1\n" \
"s_waitcnt vmcnt(20)\n" \
"v_mfma_f32_16x16x32_bf16 v[112:115], a[24:27], a[76:79], v[112:115]\n" \
"s_waitcnt vmcnt(19)\n" \
"v_mfma_f32_16x16x32_bf16 v[116:119], a[24:27], a[92:95], v[116:119]\n" \
"ds_read_b64_tr_b16 a[24:25], v34 offset:18432\n" \
"ds_read_b64_tr_b16 a[26:27], v34 offset:23040\n" \
"v_mfma_f32_16x16x32_bf16 v[120:123], a[28:31], a[76:79], v[120:123]\n" \
"buffer_load_dwordx4 a[116:119], v32, s[56:59], s66 offen offset:1024 sc1\n" \
"v_mfma_f32_16x16x32_bf16 v[124:127], a[28:31], a[92:95], v[124:127]\n" \
"ds_read_b64_tr_b16 a[28:29], v10 offset:9216\n" \
"ds_read_b64_tr_b16 a[30:31], v10 offset:13824\n" \
"v_mfma_f32_16x16x32_bf16 v[128:131], a[192:195], a[76:79], v[128:131]\n" \
"v_mfma_f32_16x16x32_bf16 v[132:135], a[192:195], a[92:95], v[132:135]\n" \
"ds_read_b64_tr_b16 a[192:193], v33 offset:36896\n" \
"ds_read_b64_tr_b16 a[194:195], v33 offset:41504\n" \
"v_mfma_f32_16x16x32_bf16 v[136:139], a[196:199], a[76:79], v[136:139]\n" \
"v_mfma_f32_16x16x32_bf16 v[140:143], a[196:199], a[92:95], v[140:143]\n" \
"ds_read_b64_tr_b16 a[196:197], v33 offset:46112\n" \
"s_waitcnt lgkmcnt(15)\n" \
"v_mfma_f32_16x16x32_bf16 v[144:147], a[200:203], a[76:79], v[144:147]\n" \
"buffer_load_dwordx4 a[104:107], v20, s[56:59], s66 offen offset:2048 sc1\n" \
"v_mfma_f32_16x16x32_bf16 v[148:151], a[200:203], a[92:95], v[148:151]\n" \
"ds_read_b64_tr_b16 a[198:199], v33 offset:50720\n" \
"ds_read_b64_tr_b16 a[200:201], v33 offset:55328\n" \
"v_mfma_f32_16x16x32_bf16 v[152:155], a[204:207], a[76:79], v[152:155]\n" \
"ds_read_b64_tr_b16 a[202:203], v33 offset:59936\n" \
"v_mfma_f32_16x16x32_bf16 v[156:159], a[204:207], a[92:95], v[156:159]\n" \
"ds_read_b64_tr_b16 a[204:205], v10 offset:32\n" \
"ds_read_b64_tr_b16 a[206:207], v10 offset:4640\n" \
"v_mfma_f32_16x16x32_bf16 v[160:163], a[208:211], a[76:79], v[160:163]\n" \
"v_mfma_f32_16x16x32_bf16 v[164:167], a[208:211], a[92:95], v[164:167]\n" \
"ds_read_b64_tr_b16 a[208:209], v34 offset:32\n" \
"ds_read_b64_tr_b16 a[210:211], v34 offset:4640\n" \
"v_mfma_f32_16x16x32_bf16 v[168:171], a[212:215], a[76:79], v[168:171]\n" \
"buffer_load_dwordx4 a[120:123], v32, s[56:59], s66 offen offset:2048 sc1\n" \
"v_mfma_f32_16x16x32_bf16 v[172:175], a[212:215], a[92:95], v[172:175]\n" \
"ds_read_b64_tr_b16 a[212:213], v34 offset:9248\n" \
"ds_read_b64_tr_b16 a[214:215], v34 offset:13856\n" \
"v_mfma_f32_16x16x32_bf16 v[176:179], a[216:219], a[76:79], v[176:179]\n" \
"v_mfma_f32_16x16x32_bf16 v[180:183], a[216:219], a[92:95], v[180:183]\n" \
"ds_read_b64_tr_b16 a[216:217], v34 offset:18464\n" \
"s_waitcnt vmcnt(21)\n" \
"v_mfma_f32_16x16x32_bf16 v[112:115], a[0:3], a[128:131], v[112:115]\n" \
"ds_read_b64_tr_b16 a[218:219], v34 offset:23072\n" \
"s_waitcnt vmcnt(20)\n" \
"v_mfma_f32_16x16x32_bf16 v[116:119], a[0:3], a[144:147], v[116:119]\n" \
"ds_read_b64_tr_b16 a[0:1], v10 offset:9248\n" \
"s_waitcnt vmcnt(19)\n" \
"v_mfma_f32_16x16x32_bf16 v[112:115], a[4:7], a[132:135], v[112:115]\n" \
"ds_read_b64_tr_b16 a[2:3], v10 offset:13856\n" \
"s_waitcnt vmcnt(18)\n" \
"v_mfma_f32_16x16x32_bf16 v[116:119], a[4:7], a[148:151], v[116:119]\n" \
"ds_read_b64_tr_b16 a[4:5], v33 offset:36928\n" \
"ds_read_b64_tr_b16 a[6:7], v33 offset:41536\n" \
"buffer_load_dwordx4 a[108:111], v20, s[56:59], s66 offen offset:3072 sc1\n" \
"s_waitcnt vmcnt(18) lgkmcnt(15)\n" \
"v_mfma_f32_16x16x32_bf16 v[112:115], a[8:11], a[136:139], v[112:115]\n" \
"buffer_load_dwordx4 a[124:127], v32, s[56:59], s66 offen offset:3072 sc1\n" \
"s_waitcnt vmcnt(18)\n" \
"v_mfma_f32_16x16x32_bf16 v[116:119], a[8:11], a[152:155], v[116:119]\n" \
"ds_read_b64_tr_b16 a[8:9], v33 offset:46144\n" \
"ds_read_b64_tr_b16 a[10:11], v33 offset:50752\n" \
"buffer_load_dwordx4 a[160:163], v20, s[56:59], s92 offen offset:0 sc1\n" \
"s_waitcnt vmcnt(18)\n" \
"v_mfma_f32_16x16x32_bf16 v[112:115], a[12:15], a[140:143], v[112:115]\n" \
"buffer_load_dwordx4 a[176:179], v32, s[56:59], s92 offen offset:0 sc1\n" \
"s_waitcnt vmcnt(18)\n" \
"v_mfma_f32_16x16x32_bf16 v[116:119], a[12:15], a[156:159], v[116:119]\n" \
"ds_read_b64_tr_b16 a[12:13], v33 offset:55360\n" \
"ds_read_b64_tr_b16 a[14:15], v33 offset:59968\n" \
"buffer_load_dwordx4 a[164:167], v20, s[56:59], s92 offen offset:1024 sc1\n" \
"s_waitcnt vmcnt(18)\n" \
"v_mfma_f32_16x16x32_bf16 v[112:115], a[16:19], a[32:35], v[112:115]\n" \
"buffer_load_dwordx4 a[180:183], v32, s[56:59], s92 offen offset:1024 sc1\n" \
"s_waitcnt vmcnt(18)\n" \
"v_mfma_f32_16x16x32_bf16 v[116:119], a[16:19], a[48:51], v[116:119]\n" \
"ds_read_b64_tr_b16 a[16:17], v10 offset:64\n" \
"ds_read_b64_tr_b16 a[18:19], v10 offset:4672\n" \
"buffer_load_dwordx4 a[168:171], v20, s[56:59], s92 offen offset:2048 sc1\n" \
"s_waitcnt vmcnt(18)\n" \
"v_mfma_f32_16x16x32_bf16 v[112:115], a[20:23], a[36:39], v[112:115]\n" \
"buffer_load_dwordx4 a[184:187], v32, s[56:59], s92 offen offset:2048 sc1\n" \
"s_waitcnt vmcnt(18)\n" \
"v_mfma_f32_16x16x32_bf16 v[116:119], a[20:23], a[52:55], v[116:119]\n" \
"ds_read_b64_tr_b16 a[20:21], v34 offset:64\n" \
"ds_read_b64_tr_b16 a[22:23], v34 offset:4672\n" \
"buffer_load_dwordx4 a[172:175], v20, s[56:59], s92 offen offset:3072 sc1\n" \
"s_waitcnt vmcnt(18)\n" \
"v_mfma_f32_16x16x32_bf16 v[112:115], a[24:27], a[40:43], v[112:115]\n" \
"buffer_load_dwordx4 a[188:191], v32, s[56:59], s92 offen offset:3072 sc1\n" \
"v_readlane_b32 s92, v11, 52\n" \
"s_waitcnt vmcnt(18)\n" \
"v_mfma_f32_16x16x32_bf16 v[116:119], a[24:27], a[56:59], v[116:119]\n" \
"ds_read_b64_tr_b16 a[24:25], v34 offset:9280\n" \
"ds_read_b64_tr_b16 a[26:27], v34 offset:13888\n" \
"buffer_load_dwordx4 a[220:223], v20, s[56:59], s95 offen offset:0 sc1\n" \
"s_waitcnt vmcnt(18)\n" \
"v_mfma_f32_16x16x32_bf16 v[112:115], a[28:31], a[44:47], v[112:115]\n" \
"buffer_load_dwordx4 a[236:239], v32, s[56:59], s95 offen offset:0 sc1\n" \
"s_waitcnt vmcnt(18)\n" \
"v_mfma_f32_16x16x32_bf16 v[116:119], a[28:31], a[60:63], v[116:119]\n" \
"ds_read_b64_tr_b16 a[28:29], v34 offset:18496\n" \
"ds_read_b64_tr_b16 a[30:31], v34 offset:23104\n" \
"v_mfma_f32_16x16x32_bf16 v[120:123], a[192:195], a[128:131], v[120:123]\n" \
"buffer_load_dwordx4 a[224:227], v20, s[56:59], s95 offen offset:1024 sc1\n" \
"v_mfma_f32_16x16x32_bf16 v[124:127], a[192:195], a[144:147], v[124:127]\n" \
"v_cvt_pk_bf16_f32 v112, v112, v113\n" \
"v_cvt_pk_bf16_f32 v113, v114, v115\n" \
"s_waitcnt lgkmcnt(4)\n" \
"v_mfma_f32_16x16x32_bf16 v[120:123], a[196:199], a[132:135], v[120:123]\n" \
"v_cvt_pk_bf16_f32 v114, v116, v117\n" \
"v_cvt_pk_bf16_f32 v115, v118, v119\n" \
"buffer_store_dwordx4 v[112:115], v20, s[56:59], s67 offen offset:0 sc1\n" \
"s_add_u32 s67, s67, 0x3800\n" \
"v_mfma_f32_16x16x32_bf16 v[124:127], a[196:199], a[148:151], v[124:127]\n" \
"ds_read_b64_tr_b16 a[192:193], v10 offset:9280\n" \
"ds_read_b64_tr_b16 a[194:195], v10 offset:13888\n" \
"v_mfma_f32_16x16x32_bf16 v[120:123], a[200:203], a[136:139], v[120:123]\n" \
"ds_read_b64_tr_b16 a[196:197], v33 offset:36960\n" \
"ds_read_b64_tr_b16 a[198:199], v33 offset:41568\n" \
"v_mfma_f32_16x16x32_bf16 v[124:127], a[200:203], a[152:155], v[124:127]\n" \
"ds_read_b64_tr_b16 a[200:201], v33 offset:46176\n" \
"ds_read_b64_tr_b16 a[202:203], v33 offset:50784\n" \
"v_mfma_f32_16x16x32_bf16 v[120:123], a[204:207], a[140:143], v[120:123]\n" \
"buffer_load_dwordx4 a[240:243], v32, s[56:59], s95 offen offset:1024 sc1\n" \
"v_mfma_f32_16x16x32_bf16 v[124:127], a[204:207], a[156:159], v[124:127]\n" \
"ds_read_b64_tr_b16 a[204:205], v33 offset:55392\n" \
"ds_read_b64_tr_b16 a[206:207], v33 offset:60000\n" \
"v_mfma_f32_16x16x32_bf16 v[120:123], a[208:211], a[32:35], v[120:123]\n" \
"v_mfma_f32_16x16x32_bf16 v[124:127], a[208:211], a[48:51], v[124:127]\n" \
"ds_read_b64_tr_b16 a[208:209], v10 offset:96\n" \
"ds_read_b64_tr_b16 a[210:211], v10 offset:4704\n" \
"v_mfma_f32_16x16x32_bf16 v[120:123], a[212:215], a[36:39], v[120:123]\n" \
"v_mfma_f32_16x16x32_bf16 v[124:127], a[212:215], a[52:55], v[124:127]\n" \
"ds_read_b64_tr_b16 a[212:213], v34 offset:96\n" \
"ds_read_b64_tr_b16 a[214:215], v34 offset:4704\n" \
"v_mfma_f32_16x16x32_bf16 v[120:123], a[216:219], a[40:43], v[120:123]\n" \
"buffer_load_dwordx4 a[228:231], v20, s[56:59], s95 offen offset:2048 sc1\n" \
"v_mfma_f32_16x16x32_bf16 v[124:127], a[216:219], a[56:59], v[124:127]\n" \
"ds_read_b64_tr_b16 a[216:217], v34 offset:9312\n" \
"ds_read_b64_tr_b16 a[218:219], v34 offset:13920\n" \
"v_mfma_f32_16x16x32_bf16 v[120:123], a[0:3], a[44:47], v[120:123]\n" \
"v_mfma_f32_16x16x32_bf16 v[124:127], a[0:3], a[60:63], v[124:127]\n" \
"ds_read_b64_tr_b16 a[0:1], v34 offset:18528\n" \
"ds_read_b64_tr_b16 a[2:3], v34 offset:23136\n" \
"v_mfma_f32_16x16x32_bf16 v[128:131], a[4:7], a[128:131], v[128:131]\n" \
"v_mfma_f32_16x16x32_bf16 v[132:135], a[4:7], a[144:147], v[132:135]\n" \
"ds_read_b64_tr_b16 a[4:5], v10 offset:9312\n" \
"ds_read_b64_tr_b16 a[6:7], v10 offset:13920\n" \
"v_mfma_f32_16x16x32_bf16 v[128:131], a[8:11], a[132:135], v[128:131]\n" \
"v_cvt_pk_bf16_f32 v120, v120, v121\n" \
"v_cvt_pk_bf16_f32 v121, v122, v123\n" \
"v_cvt_pk_bf16_f32 v122, v124, v125\n" \
"v_cvt_pk_bf16_f32 v123, v126, v127\n" \
"buffer_store_dwordx4 v[120:123], v20, s[56:59], s68 offen offset:0 sc1\n" \
"s_add_u32 s68, s68, 0x3000\n" \
"v_mfma_f32_16x16x32_bf16 v[132:135], a[8:11], a[148:151], v[132:135]\n" \
"ds_read_b64_tr_b16 a[8:9], v33 offset:36992\n" \
"ds_read_b64_tr_b16 a[10:11], v33 offset:41600\n" \
"v_mfma_f32_16x16x32_bf16 v[128:131], a[12:15], a[136:139], v[128:131]\n" \
"v_mfma_f32_16x16x32_bf16 v[132:135], a[12:15], a[152:155], v[132:135]\n" \
"ds_read_b64_tr_b16 a[12:13], v33 offset:46208\n" \
"ds_read_b64_tr_b16 a[14:15], v33 offset:50816\n" \
"v_mfma_f32_16x16x32_bf16 v[128:131], a[16:19], a[140:143], v[128:131]\n" \
"buffer_load_dwordx4 a[244:247], v32, s[56:59], s95 offen offset:2048 sc1\n" \
"v_mfma_f32_16x16x32_bf16 v[132:135], a[16:19], a[156:159], v[132:135]\n" \
"ds_read_b64_tr_b16 a[16:17], v33 offset:55424\n" \
"ds_read_b64_tr_b16 a[18:19], v33 offset:60032\n" \
"v_mfma_f32_16x16x32_bf16 v[128:131], a[20:23], a[32:35], v[128:131]\n" \
"v_mfma_f32_16x16x32_bf16 v[132:135], a[20:23], a[48:51], v[132:135]\n" \
"ds_read_b64_tr_b16 a[20:21], v10 offset:128\n" \
"s_waitcnt lgkmcnt(13)\n" \
"v_mfma_f32_16x16x32_bf16 v[128:131], a[24:27], a[36:39], v[128:131]\n" \
"ds_read_b64_tr_b16 a[22:23], v10 offset:4736\n" \
"v_mfma_f32_16x16x32_bf16 v[132:135], a[24:27], a[52:55], v[132:135]\n" \
"buffer_load_dwordx4 a[232:235], v20, s[56:59], s95 offen offset:3072 sc1\n" \
"v_mfma_f32_16x16x32_bf16 v[128:131], a[28:31], a[40:43], v[128:131]\n" \
"ds_read_b64_tr_b16 a[24:25], v34 offset:128\n" \
"ds_read_b64_tr_b16 a[26:27], v34 offset:4736\n" \
"v_mfma_f32_16x16x32_bf16 v[132:135], a[28:31], a[56:59], v[132:135]\n" \
"ds_read_b64_tr_b16 a[28:29], v34 offset:9344\n" \
"ds_read_b64_tr_b16 a[30:31], v34 offset:13952\n" \
"v_mfma_f32_16x16x32_bf16 v[128:131], a[192:195], a[44:47], v[128:131]\n" \
"v_mfma_f32_16x16x32_bf16 v[132:135], a[192:195], a[60:63], v[132:135]\n" \
"ds_read_b64_tr_b16 a[192:193], v34 offset:18560\n" \
"ds_read_b64_tr_b16 a[194:195], v34 offset:23168\n" \
"v_mfma_f32_16x16x32_bf16 v[136:139], a[196:199], a[128:131], v[136:139]\n" \
"buffer_load_dwordx4 a[248:251], v32, s[56:59], s95 offen offset:3072 sc1\n" \
"v_mfma_f32_16x16x32_bf16 v[140:143], a[196:199], a[144:147], v[140:143]\n" \
"ds_read_b64_tr_b16 a[196:197], v10 offset:9344\n" \
"ds_read_b64_tr_b16 a[198:199], v10 offset:13952\n" \
"v_cvt_pk_bf16_f32 v128, v128, v129\n" \
"v_cvt_pk_bf16_f32 v129, v130, v131\n" \
"v_cvt_pk_bf16_f32 v130, v132, v133\n" \
"v_cvt_pk_bf16_f32 v131, v134, v135\n" \
"buffer_store_dwordx4 v[128:131], v20, s[56:59], s69 offen offset:0 sc1\n" \
"s_add_u32 s69, s69, 0x2800\n" \
"v_mfma_f32_16x16x32_bf16 v[136:139], a[200:203], a[132:135], v[136:139]\n" \
"v_readlane_b32 s95, v11, 46\n" \
"v_mfma_f32_16x16x32_bf16 v[140:143], a[200:203], a[148:151], v[140:143]\n" \
"ds_read_b64_tr_b16 a[200:201], v33 offset:37024\n" \
"ds_read_b64_tr_b16 a[202:203], v33 offset:41632\n" \
"v_mfma_f32_16x16x32_bf16 v[136:139], a[204:207], a[136:139], v[136:139]\n" \
"v_mfma_f32_16x16x32_bf16 v[140:143], a[204:207], a[152:155], v[140:143]\n" \
"ds_read_b64_tr_b16 a[204:205], v33 offset:46240\n" \
"ds_read_b64_tr_b16 a[206:207], v33 offset:50848\n" \
"v_mfma_f32_16x16x32_bf16 v[136:139], a[208:211], a[140:143], v[136:139]\n" \
"buffer_load_dwordx4 v[64:67], v20, s[56:59], s97 offen offset:0 sc1\n" \
"v_mfma_f32_16x16x32_bf16 v[140:143], a[208:211], a[156:159], v[140:143]\n" \
"ds_read_b64_tr_b16 a[208:209], v33 offset:55456\n" \
"ds_read_b64_tr_b16 a[210:211], v33 offset:60064\n" \
"v_mfma_f32_16x16x32_bf16 v[136:139], a[212:215], a[32:35], v[136:139]\n" \
"v_mfma_f32_16x16x32_bf16 v[140:143], a[212:215], a[48:51], v[140:143]\n" \
"ds_read_b64_tr_b16 a[212:213], v10 offset:160\n" \
"s_waitcnt lgkmcnt(12)\n" \
"v_mfma_f32_16x16x32_bf16 v[136:139], a[216:219], a[36:39], v[136:139]\n" \
"ds_read_b64_tr_b16 a[214:215], v10 offset:4768\n" \
"v_mfma_f32_16x16x32_bf16 v[140:143], a[216:219], a[52:55], v[140:143]\n" \
"ds_read_b64_tr_b16 a[216:217], v34 offset:160\n" \
"ds_read_b64_tr_b16 a[218:219], v34 offset:4768\n" \
"v_mfma_f32_16x16x32_bf16 v[136:139], a[0:3], a[40:43], v[136:139]\n" \
"buffer_load_dwordx4 v[68:71], v20, s[56:59], s97 offen offset:1024 sc1\n" \
"v_mfma_f32_16x16x32_bf16 v[140:143], a[0:3], a[56:59], v[140:143]\n" \
"ds_read_b64_tr_b16 a[0:1], v34 offset:9376\n" \
"ds_read_b64_tr_b16 a[2:3], v34 offset:13984\n" \
"v_mfma_f32_16x16x32_bf16 v[136:139], a[4:7], a[44:47], v[136:139]\n" \
"v_mfma_f32_16x16x32_bf16 v[140:143], a[4:7], a[60:63], v[140:143]\n" \
"ds_read_b64_tr_b16 a[4:5], v34 offset:18592\n" \
"ds_read_b64_tr_b16 a[6:7], v34 offset:23200\n" \
"v_mfma_f32_16x16x32_bf16 v[144:147], a[8:11], a[128:131], v[144:147]\n" \
"v_mfma_f32_16x16x32_bf16 v[148:151], a[8:11], a[144:147], v[148:151]\n" \
"ds_read_b64_tr_b16 a[8:9], v10 offset:9376\n" \
"ds_read_b64_tr_b16 a[10:11], v10 offset:13984\n" \
"v_mfma_f32_16x16x32_bf16 v[144:147], a[12:15], a[132:135], v[144:147]\n" \
"v_cvt_pk_bf16_f32 v136, v136, v137\n" \
"v_cvt_pk_bf16_f32 v137, v138, v139\n" \
"v_cvt_pk_bf16_f32 v138, v140, v141\n" \
"v_cvt_pk_bf16_f32 v139, v142, v143\n" \
"buffer_store_dwordx4 v[136:139], v20, s[56:59], s70 offen offset:0 sc1\n" \
"s_add_u32 s70, s70, 0x2000\n" \
"v_mfma_f32_16x16x32_bf16 v[148:151], a[12:15], a[148:151], v[148:151]\n" \
"ds_read_b64_tr_b16 a[12:13], v33 offset:37056\n" \
"ds_read_b64_tr_b16 a[14:15], v33 offset:41664\n" \
"v_mfma_f32_16x16x32_bf16 v[144:147], a[16:19], a[136:139], v[144:147]\n" \
"v_mfma_f32_16x16x32_bf16 v[148:151], a[16:19], a[152:155], v[148:151]\n" \
"ds_read_b64_tr_b16 a[16:17], v33 offset:46272\n" \
"ds_read_b64_tr_b16 a[18:19], v33 offset:50880\n" \
"v_mfma_f32_16x16x32_bf16 v[144:147], a[20:23], a[140:143], v[144:147]\n" \
"buffer_load_dwordx4 v[72:75], v32, s[56:59], s97 offen offset:0 sc1\n" \
"v_mfma_f32_16x16x32_bf16 v[148:151], a[20:23], a[156:159], v[148:151]\n" \
"ds_read_b64_tr_b16 a[20:21], v33 offset:55488\n" \
"ds_read_b64_tr_b16 a[22:23], v33 offset:60096\n" \
"v_mfma_f32_16x16x32_bf16 v[144:147], a[24:27], a[32:35], v[144:147]\n" \
"v_mfma_f32_16x16x32_bf16 v[148:151], a[24:27], a[48:51], v[148:151]\n" \
"ds_read_b64_tr_b16 a[24:25], v10 offset:192\n" \
"s_waitcnt lgkmcnt(13)\n" \
"v_mfma_f32_16x16x32_bf16 v[144:147], a[28:31], a[36:39], v[144:147]\n" \
"ds_read_b64_tr_b16 a[26:27], v10 offset:4800\n" \
"v_mfma_f32_16x16x32_bf16 v[148:151], a[28:31], a[52:55], v[148:151]\n" \
"ds_read_b64_tr_b16 a[28:29], v34 offset:192\n" \
"ds_read_b64_tr_b16 a[30:31], v34 offset:4800\n" \
"v_mfma_f32_16x16x32_bf16 v[144:147], a[192:195], a[40:43], v[144:147]\n" \
"buffer_load_dwordx4 v[76:79], v32, s[56:59], s97 offen offset:1024 sc1\n" \
"v_mfma_f32_16x16x32_bf16 v[148:151], a[192:195], a[56:59], v[148:151]\n" \
"ds_read_b64_tr_b16 a[192:193], v34 offset:9408\n" \
"ds_read_b64_tr_b16 a[194:195], v34 offset:14016\n" \
"v_mfma_f32_16x16x32_bf16 v[144:147], a[196:199], a[44:47], v[144:147]\n" \
"v_readlane_b32 s97, v11, 48\n" \
"v_mfma_f32_16x16x32_bf16 v[148:151], a[196:199], a[60:63], v[148:151]\n" \
"ds_read_b64_tr_b16 a[196:197], v34 offset:18624\n" \
"ds_read_b64_tr_b16 a[198:199], v34 offset:23232\n" \
"v_mfma_f32_16x16x32_bf16 v[152:155], a[200:203], a[128:131], v[152:155]\n" \
"v_mfma_f32_16x16x32_bf16 v[156:159], a[200:203], a[144:147], v[156:159]\n" \
"ds_read_b64_tr_b16 a[200:201], v10 offset:9408\n" \
"ds_read_b64_tr_b16 a[202:203], v10 offset:14016\n" \
"v_cvt_pk_bf16_f32 v144, v144, v145\n" \
"v_cvt_pk_bf16_f32 v145, v146, v147\n" \
"v_cvt_pk_bf16_f32 v146, v148, v149\n" \
"v_cvt_pk_bf16_f32 v147, v150, v151\n" \
"buffer_store_dwordx4 v[144:147], v20, s[56:59], s71 offen offset:0 sc1\n" \
"s_add_u32 s71, s71, 0x1800\n" \
"v_mfma_f32_16x16x32_bf16 v[152:155], a[204:207], a[132:135], v[152:155]\n" \
"v_mfma_f32_16x16x32_bf16 v[156:159], a[204:207], a[148:151], v[156:159]\n" \
"ds_read_b64_tr_b16 a[204:205], v33 offset:256\n" \
"ds_read_b64_tr_b16 a[206:207], v33 offset:4864\n" \
"v_mfma_f32_16x16x32_bf16 v[152:155], a[208:211], a[136:139], v[152:155]\n" \
"v_mfma_f32_16x16x32_bf16 v[156:159], a[208:211], a[152:155], v[156:159]\n" \
"ds_read_b64_tr_b16 a[208:209], v33 offset:9472\n" \
"ds_read_b64_tr_b16 a[210:211], v33 offset:14080\n" \
"v_mfma_f32_16x16x32_bf16 v[152:155], a[212:215], a[140:143], v[152:155]\n" \
"buffer_load_dwordx4 v[80:83], v20, s[56:59], s64 offen offset:0 sc1\n" \
"v_mfma_f32_16x16x32_bf16 v[156:159], a[212:215], a[156:159], v[156:159]\n" \
"ds_read_b64_tr_b16 a[212:213], v33 offset:18688\n" \
"ds_read_b64_tr_b16 a[214:215], v33 offset:23296\n" \
"v_mfma_f32_16x16x32_bf16 v[152:155], a[216:219], a[32:35], v[152:155]\n" \
"v_mfma_f32_16x16x32_bf16 v[156:159], a[216:219], a[48:51], v[156:159]\n" \
"ds_read_b64_tr_b16 a[216:217], v33 offset:27904\n" \
"s_waitcnt lgkmcnt(12)\n" \
"v_mfma_f32_16x16x32_bf16 v[152:155], a[0:3], a[36:39], v[152:155]\n" \
"ds_read_b64_tr_b16 a[218:219], v33 offset:32512\n" \
"v_mfma_f32_16x16x32_bf16 v[156:159], a[0:3], a[52:55], v[156:159]\n" \
"ds_read_b64_tr_b16 a[0:1], v33 offset:37120\n" \
"ds_read_b64_tr_b16 a[2:3], v33 offset:41728\n" \
"v_mfma_f32_16x16x32_bf16 v[152:155], a[4:7], a[40:43], v[152:155]\n" \
"buffer_load_dwordx4 v[84:87], v20, s[56:59], s64 offen offset:1024 sc1\n" \
"v_mfma_f32_16x16x32_bf16 v[156:159], a[4:7], a[56:59], v[156:159]\n" \
"ds_read_b64_tr_b16 a[4:5], v33 offset:46336\n" \
"ds_read_b64_tr_b16 a[6:7], v33 offset:50944\n" \
"v_mfma_f32_16x16x32_bf16 v[152:155], a[8:11], a[44:47], v[152:155]\n" \
"v_mfma_f32_16x16x32_bf16 v[156:159], a[8:11], a[60:63], v[156:159]\n" \
"ds_read_b64_tr_b16 a[8:9], v33 offset:55552\n" \
"ds_read_b64_tr_b16 a[10:11], v33 offset:60160\n" \
"v_mfma_f32_16x16x32_bf16 v[160:163], a[12:15], a[128:131], v[160:163]\n" \
"v_mfma_f32_16x16x32_bf16 v[164:167], a[12:15], a[144:147], v[164:167]\n" \
"ds_read_b64_tr_b16 a[12:13], v10 offset:256\n" \
"ds_read_b64_tr_b16 a[14:15], v10 offset:4864\n" \
"v_mfma_f32_16x16x32_bf16 v[160:163], a[16:19], a[132:135], v[160:163]\n" \
"v_cvt_pk_bf16_f32 v152, v152, v153\n" \
"v_cvt_pk_bf16_f32 v153, v154, v155\n" \
"v_cvt_pk_bf16_f32 v154, v156, v157\n" \
"v_cvt_pk_bf16_f32 v155, v158, v159\n" \
"buffer_store_dwordx4 v[152:155], v20, s[56:59], s72 offen offset:0 sc1\n" \
"s_add_u32 s72, s72, 0x1000\n" \
"v_mfma_f32_16x16x32_bf16 v[164:167], a[16:19], a[148:151], v[164:167]\n" \
"ds_read_b64_tr_b16 a[16:17], v34 offset:256\n" \
"ds_read_b64_tr_b16 a[18:19], v34 offset:4864\n" \
"v_mfma_f32_16x16x32_bf16 v[160:163], a[20:23], a[136:139], v[160:163]\n" \
"v_mfma_f32_16x16x32_bf16 v[164:167], a[20:23], a[152:155], v[164:167]\n" \
"ds_read_b64_tr_b16 a[20:21], v34 offset:9472\n" \
"ds_read_b64_tr_b16 a[22:23], v34 offset:14080\n" \
"v_mfma_f32_16x16x32_bf16 v[160:163], a[24:27], a[140:143], v[160:163]\n" \
"buffer_load_dwordx4 v[88:91], v32, s[56:59], s64 offen offset:0 sc1\n" \
"v_mfma_f32_16x16x32_bf16 v[164:167], a[24:27], a[156:159], v[164:167]\n" \
"ds_read_b64_tr_b16 a[24:25], v34 offset:18688\n" \
"ds_read_b64_tr_b16 a[26:27], v34 offset:23296\n" \
"v_mfma_f32_16x16x32_bf16 v[160:163], a[28:31], a[32:35], v[160:163]\n" \
"v_mfma_f32_16x16x32_bf16 v[164:167], a[28:31], a[48:51], v[164:167]\n" \
"ds_read_b64_tr_b16 a[28:29], v10 offset:9472\n" \
"s_waitcnt vmcnt(17)\n" \
"s_mov_b64 exec, 1\n" \
"buffer_store_dword v39, off, s[56:59], s67 offset:0 sc1\n" \
"s_mov_b64 exec, -1\n" \
"s_waitcnt lgkmcnt(11)\n" \
"v_mfma_f32_16x16x32_bf16 v[160:163], a[192:195], a[36:39], v[160:163]\n" \
"ds_read_b64_tr_b16 a[30:31], v10 offset:14080\n" \
"v_mfma_f32_16x16x32_bf16 v[164:167], a[192:195], a[52:55], v[164:167]\n" \
"ds_read_b64_tr_b16 a[192:193], v33 offset:0\n" \
"ds_read_b64_tr_b16 a[194:195], v33 offset:4608\n" \
"v_mfma_f32_16x16x32_bf16 v[160:163], a[196:199], a[40:43], v[160:163]\n" \
"v_mfma_f32_16x16x32_bf16 v[164:167], a[196:199], a[56:59], v[164:167]\n" \
"ds_read_b64_tr_b16 a[196:197], v33 offset:9216\n" \
"ds_read_b64_tr_b16 a[198:199], v33 offset:13824\n" \
"v_mfma_f32_16x16x32_bf16 v[160:163], a[200:203], a[44:47], v[160:163]\n" \
"buffer_load_dwordx4 v[92:95], v32, s[56:59], s64 offen offset:1024 sc1\n" \
"v_mfma_f32_16x16x32_bf16 v[164:167], a[200:203], a[60:63], v[164:167]\n" \
"ds_read_b64_tr_b16 a[200:201], v33 offset:18432\n" \
"ds_read_b64_tr_b16 a[202:203], v33 offset:23040\n" \
"v_mfma_f32_16x16x32_bf16 v[248:251], a[204:207], a[96:99], v[248:251]\n" \
"v_readlane_b32 s64, v11, 49\n" \
"v_mfma_f32_16x16x32_bf16 v[252:255], a[204:207], a[112:115], v[252:255]\n" \
"ds_read_b64_tr_b16 a[204:205], v33 offset:27648\n" \
"v_cvt_pk_bf16_f32 v160, v160, v161\n" \
"v_cvt_pk_bf16_f32 v161, v162, v163\n" \
"v_cvt_pk_bf16_f32 v162, v164, v165\n" \
"v_cvt_pk_bf16_f32 v163, v166, v167\n" \
"buffer_store_dwordx4 v[160:163], v20, s[56:59], s73 offen offset:0 sc1\n" \
"s_add_u32 s73, s73, 0x800\n" \
"v_mfma_f32_16x16x32_bf16 v[248:251], a[208:211], a[100:103], v[248:251]\n" \
"ds_read_b64_tr_b16 a[206:207], v33 offset:32256\n" \
"v_mfma_f32_16x16x32_bf16 v[252:255], a[208:211], a[116:119], v[252:255]\n" \
"ds_read_b64_tr_b16 a[208:209], v33 offset:36864\n" \
"ds_read_b64_tr_b16 a[210:211], v33 offset:41472\n" \
"v_mfma_f32_16x16x32_bf16 v[248:251], a[212:215], a[104:107], v[248:251]\n" \
"v_mfma_f32_16x16x32_bf16 v[252:255], a[212:215], a[120:123], v[252:255]\n" \
"ds_read_b64_tr_b16 a[212:213], v33 offset:46080\n" \
"ds_read_b64_tr_b16 a[214:215], v33 offset:50688\n" \
"v_mfma_f32_16x16x32_bf16 v[248:251], a[216:219], a[108:111], v[248:251]\n" \
"buffer_load_dwordx4 v[96:99], v20, s[56:59], s65 offen offset:0 sc1\n" \
"v_mfma_f32_16x16x32_bf16 v[252:255], a[216:219], a[124:127], v[252:255]\n" \
"ds_read_b64_tr_b16 a[216:217], v33 offset:55296\n" \
"ds_read_b64_tr_b16 a[218:219], v33 offset:59904\n" \
"s_waitcnt vmcnt(18)\n" \
"s_mov_b64 exec, 1\n" \
"buffer_store_dword v39, off, s[56:59], s68 offset:4 sc1\n" \
"s_mov_b64 exec, -1\n" \
"v_mfma_f32_16x16x32_bf16 v[176:179], a[0:3], a[128:131], v[176:179]\n" \
"v_mfma_f32_16x16x32_bf16 v[180:183], a[0:3], a[144:147], v[180:183]\n" \
"v_mfma_f32_16x16x32_bf16 v[248:251], a[0:3], a[160:163], v[248:251]\n" \
"v_mfma_f32_16x16x32_bf16 v[252:255], a[0:3], a[176:179], v[252:255]\n" \
"ds_read_b64_tr_b16 a[0:1], v10 offset:0\n" \
"ds_read_b64_tr_b16 a[2:3], v10 offset:4608\n" \
"v_mfma_f32_16x16x32_bf16 v[176:179], a[4:7], a[132:135], v[176:179]\n" \
"buffer_load_dwordx4 v[100:103], v20, s[56:59], s65 offen offset:1024 sc1\n" \
"v_mfma_f32_16x16x32_bf16 v[180:183], a[4:7], a[148:151], v[180:183]\n" \
"v_mfma_f32_16x16x32_bf16 v[248:251], a[4:7], a[164:167], v[248:251]\n" \
"v_mfma_f32_16x16x32_bf16 v[252:255], a[4:7], a[180:183], v[252:255]\n" \
"ds_read_b64_tr_b16 a[4:5], v34 offset:0\n" \
"s_waitcnt lgkmcnt(9)\n" \
"v_mfma_f32_16x16x32_bf16 v[176:179], a[8:11], a[136:139], v[176:179]\n" \
"ds_read_b64_tr_b16 a[6:7], v34 offset:4608\n" \
"v_mfma_f32_16x16x32_bf16 v[180:183], a[8:11], a[152:155], v[180:183]\n" \
"buffer_load_dwordx4 v[104:107], v32, s[56:59], s65 offen offset:0 sc1\n" \
"v_mfma_f32_16x16x32_bf16 v[248:251], a[8:11], a[168:171], v[248:251]\n" \
"v_mfma_f32_16x16x32_bf16 v[252:255], a[8:11], a[184:187], v[252:255]\n" \
"ds_read_b64_tr_b16 a[8:9], v34 offset:9216\n" \
"ds_read_b64_tr_b16 a[10:11], v34 offset:13824\n" \
"v_mfma_f32_16x16x32_bf16 v[176:179], a[12:15], a[140:143], v[176:179]\n" \
"v_mfma_f32_16x16x32_bf16 v[180:183], a[12:15], a[156:159], v[180:183]\n" \
"v_mfma_f32_16x16x32_bf16 v[248:251], a[12:15], a[172:175], v[248:251]\n" \
"buffer_load_dwordx4 v[108:111], v32, s[56:59], s65 offen offset:1024 sc1\n" \
"v_mfma_f32_16x16x32_bf16 v[252:255], a[12:15], a[188:191], v[252:255]\n" \
"ds_read_b64_tr_b16 a[12:13], v34 offset:18432\n" \
"ds_read_b64_tr_b16 a[14:15], v34 offset:23040\n" \
"s_waitcnt vmcnt(18)\n" \
"s_mov_b64 exec, 1\n" \
"buffer_store_dword v39, off, s[56:59], s69 offset:8 sc1\n" \
"s_mov_b64 exec, -1\n" \
"v_mfma_f32_16x16x32_bf16 v[176:179], a[16:19], a[32:35], v[176:179]\n" \
"v_mfma_f32_16x16x32_bf16 v[180:183], a[16:19], a[48:51], v[180:183]\n" \
"v_mfma_f32_16x16x32_bf16 v[248:251], a[16:19], a[220:223], v[248:251]\n" \
"v_mfma_f32_16x16x32_bf16 v[252:255], a[16:19], a[236:239], v[252:255]\n" \
"ds_read_b64_tr_b16 a[16:17], v10 offset:9216\n" \
"ds_read_b64_tr_b16 a[18:19], v10 offset:13824\n" \
"v_mfma_f32_16x16x32_bf16 v[176:179], a[20:23], a[36:39], v[176:179]\n" \
"v_mfma_f32_16x16x32_bf16 v[180:183], a[20:23], a[52:55], v[180:183]\n" \
"v_mfma_f32_16x16x32_bf16 v[248:251], a[20:23], a[224:227], v[248:251]\n" \
"v_mfma_f32_16x16x32_bf16 v[252:255], a[20:23], a[240:243], v[252:255]\n" \
"ds_read_b64_tr_b16 a[20:21], v33 offset:32\n" \
"ds_read_b64_tr_b16 a[22:23], v33 offset:4640\n" \
"v_mfma_f32_16x16x32_bf16 v[176:179], a[24:27], a[40:43], v[176:179]\n" \
"v_mfma_f32_16x16x32_bf16 v[180:183], a[24:27], a[56:59], v[180:183]\n" \
"v_mfma_f32_16x16x32_bf16 v[248:251], a[24:27], a[228:231], v[248:251]\n" \
"v_mfma_f32_16x16x32_bf16 v[252:255], a[24:27], a[244:247], v[252:255]\n" \
"ds_read_b64_tr_b16 a[24:25], v33 offset:9248\n" \
"ds_read_b64_tr_b16 a[26:27], v33 offset:13856\n" \
"v_mfma_f32_16x16x32_bf16 v[176:179], a[28:31], a[44:47], v[176:179]\n" \
"v_mfma_f32_16x16x32_bf16 v[180:183], a[28:31], a[60:63], v[180:183]\n" \
"v_mfma_f32_16x16x32_bf16 v[248:251], a[28:31], a[232:235], v[248:251]\n" \
"v_mfma_f32_16x16x32_bf16 v[252:255], a[28:31], a[248:251], v[252:255]\n" \
"ds_read_b64_tr_b16 a[28:29], v33 offset:18464\n" \
"ds_read_b64_tr_b16 a[30:31], v33 offset:23072\n" \
"v_mfma_f32_16x16x32_bf16 v[184:187], a[192:195], a[96:99], v[184:187]\n" \
"s_waitcnt vmcnt(16)\n" \
"s_mov_b64 exec, 1\n" \
"buffer_store_dword v39, off, s[56:59], s70 offset:12 sc1\n" \
"s_mov_b64 exec, -1\n" \
"v_cvt_pk_bf16_f32 v176, v176, v177\n" \
"v_cvt_pk_bf16_f32 v248, v248, v249\n" \
"v_cvt_pk_bf16_f32 v177, v178, v179\n" \
"v_cvt_pk_bf16_f32 v249, v250, v251\n" \
"v_cvt_pk_bf16_f32 v178, v180, v181\n" \
"v_cvt_pk_bf16_f32 v179, v182, v183\n" \
"v_cvt_pk_bf16_f32 v250, v252, v253\n" \
"v_cvt_pk_bf16_f32 v251, v254, v255\n" \
"buffer_store_dwordx4 v[176:179], v20, s[56:59], s92 offen offset:0 sc1\n" \
"v_mfma_f32_16x16x32_bf16 v[188:191], a[192:195], a[112:115], v[188:191]\n" \
"ds_read_b64_tr_b16 a[192:193], v33 offset:27680\n" \
"ds_read_b64_tr_b16 a[194:195], v33 offset:32288\n" \
"v_mfma_f32_16x16x32_bf16 v[184:187], a[196:199], a[100:103], v[184:187]\n" \
"v_mfma_f32_16x16x32_bf16 v[188:191], a[196:199], a[116:119], v[188:191]\n" \
"buffer_store_dwordx4 v[248:251], v20, s[56:59], s92 offen offset:1024 sc1\n" \
"v_mfma_f32_16x16x32_bf16 v[184:187], a[200:203], a[104:107], v[184:187]\n" \
"v_readlane_b32 s92, v11, 51\n" \
"ds_read_b64_tr_b16 a[196:197], v33 offset:36896\n" \
"v_mfma_f32_16x16x32_bf16 v[188:191], a[200:203], a[120:123], v[188:191]\n" \
"ds_read_b64_tr_b16 a[198:199], v33 offset:41504\n" \
"ds_read_b64_tr_b16 a[200:201], v33 offset:46112\n" \
"v_mfma_f32_16x16x32_bf16 v[184:187], a[204:207], a[108:111], v[184:187]\n" \
"ds_read_b64_tr_b16 a[202:203], v33 offset:50720\n" \
"v_mfma_f32_16x16x32_bf16 v[188:191], a[204:207], a[124:127], v[188:191]\n" \
"ds_read_b64_tr_b16 a[204:205], v33 offset:55328\n" \
"s_waitcnt lgkmcnt(9)\n" \
"v_mfma_f32_16x16x32_bf16 v[184:187], a[208:211], a[160:163], v[184:187]\n" \
"ds_read_b64_tr_b16 a[206:207], v33 offset:59936\n" \
"v_mfma_f32_16x16x32_bf16 v[188:191], a[208:211], a[176:179], v[188:191]\n" \
"ds_read_b64_tr_b16 a[208:209], v10 offset:32\n" \
"ds_read_b64_tr_b16 a[210:211], v10 offset:4640\n" \
"v_mfma_f32_16x16x32_bf16 v[184:187], a[212:215], a[164:167], v[184:187]\n" \
"v_mfma_f32_16x16x32_bf16 v[188:191], a[212:215], a[180:183], v[188:191]\n" \
"ds_read_b64_tr_b16 a[212:213], v34 offset:32\n" \
"ds_read_b64_tr_b16 a[214:215], v34 offset:4640\n" \
"v_mfma_f32_16x16x32_bf16 v[184:187], a[216:219], a[168:171], v[184:187]\n" \
"v_mfma_f32_16x16x32_bf16 v[188:191], a[216:219], a[184:187], v[188:191]\n" \
"ds_read_b64_tr_b16 a[216:217], v34 offset:9248\n" \
"ds_read_b64_tr_b16 a[218:219], v34 offset:13856\n" \
"s_waitcnt vmcnt(16)\n" \
"s_mov_b64 exec, 1\n" \
"buffer_store_dword v39, off, s[56:59], s71 offset:16 sc1\n" \
"s_mov_b64 exec, -1\n" \
"v_mfma_f32_16x16x32_bf16 v[184:187], a[0:3], a[172:175], v[184:187]\n" \
"v_mfma_f32_16x16x32_bf16 v[188:191], a[0:3], a[188:191], v[188:191]\n" \
"ds_read_b64_tr_b16 a[0:1], v34 offset:18464\n" \
"ds_read_b64_tr_b16 a[2:3], v34 offset:23072\n" \
"v_mfma_f32_16x16x32_bf16 v[184:187], a[4:7], a[220:223], v[184:187]\n" \
"v_mfma_f32_16x16x32_bf16 v[188:191], a[4:7], a[236:239], v[188:191]\n" \
"ds_read_b64_tr_b16 a[4:5], v10 offset:9248\n" \
"ds_read_b64_tr_b16 a[6:7], v10 offset:13856\n" \
"v_mfma_f32_16x16x32_bf16 v[184:187], a[8:11], a[224:227], v[184:187]\n" \
"v_mfma_f32_16x16x32_bf16 v[188:191], a[8:11], a[240:243], v[188:191]\n" \
"ds_read_b64_tr_b16 a[8:9], v33 offset:64\n" \
"ds_read_b64_tr_b16 a[10:11], v33 offset:4672\n" \
"v_mfma_f32_16x16x32_bf16 v[184:187], a[12:15], a[228:231], v[184:187]\n" \
"v_mfma_f32_16x16x32_bf16 v[188:191], a[12:15], a[244:247], v[188:191]\n" \
"ds_read_b64_tr_b16 a[12:13], v33 offset:9280\n" \
"ds_read_b64_tr_b16 a[14:15], v33 offset:13888\n" \
"v_mfma_f32_16x16x32_bf16 v[184:187], a[16:19], a[232:235], v[184:187]\n" \
"v_mfma_f32_16x16x32_bf16 v[188:191], a[16:19], a[248:251], v[188:191]\n" \
"ds_read_b64_tr_b16 a[16:17], v33 offset:18496\n" \
"ds_read_b64_tr_b16 a[18:19], v33 offset:23104\n" \
"v_mfma_f32_16x16x32_bf16 v[192:195], a[20:23], a[96:99], v[192:195]\n" \
"v_mfma_f32_16x16x32_bf16 v[196:199], a[20:23], a[112:115], v[196:199]\n" \
"ds_read_b64_tr_b16 a[20:21], v33 offset:27712\n" \
"ds_read_b64_tr_b16 a[22:23], v33 offset:32320\n" \
"v_mfma_f32_16x16x32_bf16 v[192:195], a[24:27], a[100:103], v[192:195]\n" \
"v_cvt_pk_bf16_f32 v184, v184, v185\n" \
"v_cvt_pk_bf16_f32 v185, v186, v187\n" \
"v_cvt_pk_bf16_f32 v186, v188, v189\n" \
"v_cvt_pk_bf16_f32 v187, v190, v191\n" \
"buffer_store_dwordx4 v[184:187], v20, s[56:59], s94 offen offset:1024 sc1\n" \
"s_add_u32 s94, s94, 0x3800\n" \
"v_mfma_f32_16x16x32_bf16 v[196:199], a[24:27], a[116:119], v[196:199]\n" \
"ds_read_b64_tr_b16 a[24:25], v33 offset:36928\n" \
"s_waitcnt lgkmcnt(13)\n" \
"v_mfma_f32_16x16x32_bf16 v[192:195], a[28:31], a[104:107], v[192:195]\n" \
"ds_read_b64_tr_b16 a[26:27], v33 offset:41536\n" \
"v_mfma_f32_16x16x32_bf16 v[196:199], a[28:31], a[120:123], v[196:199]\n" \
"ds_read_b64_tr_b16 a[28:29], v33 offset:46144\n" \
"ds_read_b64_tr_b16 a[30:31], v33 offset:50752\n" \
"s_waitcnt vmcnt(15)\n" \
"s_mov_b64 exec, 1\n" \
"buffer_store_dword v39, off, s[56:59], s72 offset:20 sc1\n" \
"s_mov_b64 exec, -1\n" \
"v_mfma_f32_16x16x32_bf16 v[192:195], a[192:195], a[108:111], v[192:195]\n" \
"v_readlane_b32 s72, v11, 41\n" \
"v_mfma_f32_16x16x32_bf16 v[196:199], a[192:195], a[124:127], v[196:199]\n" \
"ds_read_b64_tr_b16 a[192:193], v33 offset:55360\n" \
"ds_read_b64_tr_b16 a[194:195], v33 offset:59968\n" \
"v_mfma_f32_16x16x32_bf16 v[192:195], a[196:199], a[160:163], v[192:195]\n" \
"v_mfma_f32_16x16x32_bf16 v[196:199], a[196:199], a[176:179], v[196:199]\n" \
"ds_read_b64_tr_b16 a[196:197], v10 offset:64\n" \
"ds_read_b64_tr_b16 a[198:199], v10 offset:4672\n" \
"v_mfma_f32_16x16x32_bf16 v[192:195], a[200:203], a[164:167], v[192:195]\n" \
"v_mfma_f32_16x16x32_bf16 v[196:199], a[200:203], a[180:183], v[196:199]\n" \
"ds_read_b64_tr_b16 a[200:201], v34 offset:64\n" \
"ds_read_b64_tr_b16 a[202:203], v34 offset:4672\n" \
"v_mfma_f32_16x16x32_bf16 v[192:195], a[204:207], a[168:171], v[192:195]\n" \
"v_mfma_f32_16x16x32_bf16 v[196:199], a[204:207], a[184:187], v[196:199]\n" \
"ds_read_b64_tr_b16 a[204:205], v34 offset:9280\n" \
"ds_read_b64_tr_b16 a[206:207], v34 offset:13888\n" \
"v_mfma_f32_16x16x32_bf16 v[192:195], a[208:211], a[172:175], v[192:195]\n" \
"v_mfma_f32_16x16x32_bf16 v[196:199], a[208:211], a[188:191], v[196:199]\n" \
"ds_read_b64_tr_b16 a[208:209], v34 offset:18496\n" \
"ds_read_b64_tr_b16 a[210:211], v34 offset:23104\n" \
"v_mfma_f32_16x16x32_bf16 v[192:195], a[212:215], a[220:223], v[192:195]\n" \
"v_mfma_f32_16x16x32_bf16 v[196:199], a[212:215], a[236:239], v[196:199]\n" \
"ds_read_b64_tr_b16 a[212:213], v10 offset:9280\n" \
"ds_read_b64_tr_b16 a[214:215], v10 offset:13888\n" \
"v_mfma_f32_16x16x32_bf16 v[192:195], a[216:219], a[224:227], v[192:195]\n" \
"v_mfma_f32_16x16x32_bf16 v[196:199], a[216:219], a[240:243], v[196:199]\n" \
"ds_read_b64_tr_b16 a[216:217], v33 offset:96\n" \
"s_waitcnt lgkmcnt(13)\n" \
"v_mfma_f32_16x16x32_bf16 v[192:195], a[0:3], a[228:231], v[192:195]\n" \
"ds_read_b64_tr_b16 a[218:219], v33 offset:4704\n" \
"v_mfma_f32_16x16x32_bf16 v[196:199], a[0:3], a[244:247], v[196:199]\n" \
"ds_read_b64_tr_b16 a[0:1], v33 offset:9312\n" \
"ds_read_b64_tr_b16 a[2:3], v33 offset:13920\n" \
"s_waitcnt vmcnt(12)\n" \
"s_mov_b64 exec, 1\n" \
"buffer_store_dword v39, off, s[56:59], s73 offset:24 sc1\n" \
"s_mov_b64 exec, -1\n" \
"v_readlane_b32 s73, v11, 50\n" \
"v_mfma_f32_16x16x32_bf16 v[192:195], a[4:7], a[232:235], v[192:195]\n" \
"v_mfma_f32_16x16x32_bf16 v[196:199], a[4:7], a[248:251], v[196:199]\n" \
"ds_read_b64_tr_b16 a[4:5], v33 offset:18528\n" \
"ds_read_b64_tr_b16 a[6:7], v33 offset:23136\n" \
"v_mfma_f32_16x16x32_bf16 v[200:203], a[8:11], a[96:99], v[200:203]\n" \
"v_mfma_f32_16x16x32_bf16 v[204:207], a[8:11], a[112:115], v[204:207]\n" \
"ds_read_b64_tr_b16 a[8:9], v33 offset:27744\n" \
"ds_read_b64_tr_b16 a[10:11], v33 offset:32352\n" \
"v_mfma_f32_16x16x32_bf16 v[200:203], a[12:15], a[100:103], v[200:203]\n" \
"v_cvt_pk_bf16_f32 v192, v192, v193\n" \
"v_cvt_pk_bf16_f32 v193, v194, v195\n" \
"v_cvt_pk_bf16_f32 v194, v196, v197\n" \
"v_cvt_pk_bf16_f32 v195, v198, v199\n" \
"buffer_store_dwordx4 v[192:195], v20, s[56:59], s95 offen offset:1024 sc1\n" \
"s_add_u32 s95, s95, 0x3000\n" \
"v_mfma_f32_16x16x32_bf16 v[204:207], a[12:15], a[116:119], v[204:207]\n" \
"ds_read_b64_tr_b16 a[12:13], v33 offset:36960\n" \
"ds_read_b64_tr_b16 a[14:15], v33 offset:41568\n" \
"v_mfma_f32_16x16x32_bf16 v[200:203], a[16:19], a[104:107], v[200:203]\n" \
"v_mfma_f32_16x16x32_bf16 v[204:207], a[16:19], a[120:123], v[204:207]\n" \
"ds_read_b64_tr_b16 a[16:17], v33 offset:46176\n" \
"ds_read_b64_tr_b16 a[18:19], v33 offset:50784\n" \
"v_mfma_f32_16x16x32_bf16 v[200:203], a[20:23], a[108:111], v[200:203]\n" \
"v_mfma_f32_16x16x32_bf16 v[204:207], a[20:23], a[124:127], v[204:207]\n" \
"ds_read_b64_tr_b16 a[20:21], v33 offset:55392\n" \
"ds_read_b64_tr_b16 a[22:23], v33 offset:60000\n" \
"v_mfma_f32_16x16x32_bf16 v[200:203], a[24:27], a[160:163], v[200:203]\n" \
"v_mfma_f32_16x16x32_bf16 v[204:207], a[24:27], a[176:179], v[204:207]\n" \
"ds_read_b64_tr_b16 a[24:25], v10 offset:96\n" \
"ds_read_b64_tr_b16 a[26:27], v10 offset:4704\n" \
"v_mfma_f32_16x16x32_bf16 v[200:203], a[28:31], a[164:167], v[200:203]\n" \
"v_mfma_f32_16x16x32_bf16 v[204:207], a[28:31], a[180:183], v[204:207]\n" \
"ds_read_b64_tr_b16 a[28:29], v34 offset:96\n" \
"s_waitcnt lgkmcnt(13)\n" \
"v_mfma_f32_16x16x32_bf16 v[200:203], a[192:195], a[168:171], v[200:203]\n" \
"ds_read_b64_tr_b16 a[30:31], v34 offset:4704\n" \
"v_mfma_f32_16x16x32_bf16 v[204:207], a[192:195], a[184:187], v[204:207]\n" \
"ds_read_b64_tr_b16 a[192:193], v34 offset:9312\n" \
"ds_read_b64_tr_b16 a[194:195], v34 offset:13920\n" \
"v_mfma_f32_16x16x32_bf16 v[200:203], a[196:199], a[172:175], v[200:203]\n" \
"v_mfma_f32_16x16x32_bf16 v[204:207], a[196:199], a[188:191], v[204:207]\n" \
"ds_read_b64_tr_b16 a[196:197], v34 offset:18528\n" \
"ds_read_b64_tr_b16 a[198:199], v34 offset:23136\n" \
"v_mfma_f32_16x16x32_bf16 v[200:203], a[200:203], a[220:223], v[200:203]\n" \
"v_mfma_f32_16x16x32_bf16 v[204:207], a[200:203], a[236:239], v[204:207]\n" \
"ds_read_b64_tr_b16 a[200:201], v10 offset:9312\n" \
"ds_read_b64_tr_b16 a[202:203], v10 offset:13920\n" \
"v_mfma_f32_16x16x32_bf16 v[200:203], a[204:207], a[224:227], v[200:203]\n" \
"v_mfma_f32_16x16x32_bf16 v[204:207], a[204:207], a[240:243], v[204:207]\n" \
"ds_read_b64_tr_b16 a[204:205], v33 offset:128\n" \
"ds_read_b64_tr_b16 a[206:207], v33 offset:4736\n" \
"v_mfma_f32_16x16x32_bf16 v[200:203], a[208:211], a[228:231], v[200:203]\n" \
"v_mfma_f32_16x16x32_bf16 v[204:207], a[208:211], a[244:247], v[204:207]\n" \
"ds_read_b64_tr_b16 a[208:209], v33 offset:9344\n" \
"ds_read_b64_tr_b16 a[210:211], v33 offset:13952\n" \
"v_mfma_f32_16x16x32_bf16 v[200:203], a[212:215], a[232:235], v[200:203]\n" \
"v_mfma_f32_16x16x32_bf16 v[204:207], a[212:215], a[248:251], v[204:207]\n" \
"ds_read_b64_tr_b16 a[212:213], v33 offset:18560\n" \
"ds_read_b64_tr_b16 a[214:215], v33 offset:23168\n" \
"v_mfma_f32_16x16x32_bf16 v[208:211], a[216:219], a[96:99], v[208:211]\n" \
"v_mfma_f32_16x16x32_bf16 v[212:215], a[216:219], a[112:115], v[212:215]\n" \
"ds_read_b64_tr_b16 a[216:217], v33 offset:27776\n" \
"ds_read_b64_tr_b16 a[218:219], v33 offset:32384\n" \
"v_mfma_f32_16x16x32_bf16 v[208:211], a[0:3], a[100:103], v[208:211]\n" \
"v_cvt_pk_bf16_f32 v200, v200, v201\n" \
"v_cvt_pk_bf16_f32 v201, v202, v203\n" \
"v_cvt_pk_bf16_f32 v202, v204, v205\n" \
"v_cvt_pk_bf16_f32 v203, v206, v207\n" \
"buffer_store_dwordx4 v[200:203], v20, s[56:59], s96 offen offset:1024 sc1\n" \
"s_add_u32 s96, s96, 0x2800\n" \
"v_mfma_f32_16x16x32_bf16 v[212:215], a[0:3], a[116:119], v[212:215]\n" \
"ds_read_b64_tr_b16 a[0:1], v33 offset:36992\n" \
"s_waitcnt lgkmcnt(13)\n" \
"v_mfma_f32_16x16x32_bf16 v[208:211], a[4:7], a[104:107], v[208:211]\n" \
"ds_read_b64_tr_b16 a[2:3], v33 offset:41600\n" \
"v_mfma_f32_16x16x32_bf16 v[212:215], a[4:7], a[120:123], v[212:215]\n" \
"ds_read_b64_tr_b16 a[4:5], v33 offset:46208\n" \
"ds_read_b64_tr_b16 a[6:7], v33 offset:50816\n" \
"v_mfma_f32_16x16x32_bf16 v[208:211], a[8:11], a[108:111], v[208:211]\n" \
"v_mfma_f32_16x16x32_bf16 v[212:215], a[8:11], a[124:127], v[212:215]\n" \
"ds_read_b64_tr_b16 a[8:9], v33 offset:55424\n" \
"ds_read_b64_tr_b16 a[10:11], v33 offset:60032\n" \
"v_mfma_f32_16x16x32_bf16 v[208:211], a[12:15], a[160:163], v[208:211]\n" \
"v_mfma_f32_16x16x32_bf16 v[212:215], a[12:15], a[176:179], v[212:215]\n" \
"ds_read_b64_tr_b16 a[12:13], v10 offset:128\n" \
"ds_read_b64_tr_b16 a[14:15], v10 offset:4736\n" \
"v_mfma_f32_16x16x32_bf16 v[208:211], a[16:19], a[164:167], v[208:211]\n" \
"v_mfma_f32_16x16x32_bf16 v[212:215], a[16:19], a[180:183], v[212:215]\n" \
"ds_read_b64_tr_b16 a[16:17], v34 offset:128\n" \
"ds_read_b64_tr_b16 a[18:19], v34 offset:4736\n" \
"v_mfma_f32_16x16x32_bf16 v[208:211], a[20:23], a[168:171], v[208:211]\n" \
"v_mfma_f32_16x16x32_bf16 v[212:215], a[20:23], a[184:187], v[212:215]\n" \
"ds_read_b64_tr_b16 a[20:21], v34 offset:9344\n" \
"ds_read_b64_tr_b16 a[22:23], v34 offset:13952\n" \
"v_mfma_f32_16x16x32_bf16 v[208:211], a[24:27], a[172:175], v[208:211]\n" \
"v_mfma_f32_16x16x32_bf16 v[212:215], a[24:27], a[188:191], v[212:215]\n" \
"ds_read_b64_tr_b16 a[24:25], v34 offset:18560\n" \
"ds_read_b64_tr_b16 a[26:27], v34 offset:23168\n" \
"v_mfma_f32_16x16x32_bf16 v[208:211], a[28:31], a[220:223], v[208:211]\n" \
"v_mfma_f32_16x16x32_bf16 v[212:215], a[28:31], a[236:239], v[212:215]\n" \
"ds_read_b64_tr_b16 a[28:29], v10 offset:9344\n" \
"ds_read_b64_tr_b16 a[30:31], v10 offset:13952\n" \
"s_waitcnt vmcnt(6)\n" \
"s_mov_b64 exec, 3\n" \
"buffer_store_dword v39, v41, s[56:59], s93 offen sc1\n" \
"s_mov_b64 exec, -1\n" \
"v_mfma_f32_16x16x32_bf16 v[208:211], a[192:195], a[224:227], v[208:211]\n" \
"v_mfma_f32_16x16x32_bf16 v[212:215], a[192:195], a[240:243], v[212:215]\n" \
"ds_read_b64_tr_b16 a[192:193], v33 offset:160\n" \
"s_waitcnt lgkmcnt(13)\n" \
"v_mfma_f32_16x16x32_bf16 v[208:211], a[196:199], a[228:231], v[208:211]\n" \
"ds_read_b64_tr_b16 a[194:195], v33 offset:4768\n" \
"v_mfma_f32_16x16x32_bf16 v[212:215], a[196:199], a[244:247], v[212:215]\n" \
"ds_read_b64_tr_b16 a[196:197], v33 offset:9376\n" \
"ds_read_b64_tr_b16 a[198:199], v33 offset:13984\n" \
"v_mfma_f32_16x16x32_bf16 v[208:211], a[200:203], a[232:235], v[208:211]\n" \
"v_mfma_f32_16x16x32_bf16 v[212:215], a[200:203], a[248:251], v[212:215]\n" \
"ds_read_b64_tr_b16 a[200:201], v33 offset:18592\n" \
"ds_read_b64_tr_b16 a[202:203], v33 offset:23200\n" \
"v_mfma_f32_16x16x32_bf16 v[216:219], a[204:207], a[96:99], v[216:219]\n" \
"v_mfma_f32_16x16x32_bf16 v[220:223], a[204:207], a[112:115], v[220:223]\n" \
"ds_read_b64_tr_b16 a[204:205], v33 offset:27808\n" \
"ds_read_b64_tr_b16 a[206:207], v33 offset:32416\n" \
"v_mfma_f32_16x16x32_bf16 v[216:219], a[208:211], a[100:103], v[216:219]\n" \
"v_cvt_pk_bf16_f32 v208, v208, v209\n" \
"v_cvt_pk_bf16_f32 v209, v210, v211\n" \
"v_cvt_pk_bf16_f32 v210, v212, v213\n" \
"v_cvt_pk_bf16_f32 v211, v214, v215\n" \
"buffer_store_dwordx4 v[208:211], v20, s[56:59], s97 offen offset:1024 sc1\n" \
"s_add_u32 s97, s97, 0x2000\n" \
"v_mfma_f32_16x16x32_bf16 v[220:223], a[208:211], a[116:119], v[220:223]\n" \
"ds_read_b64_tr_b16 a[208:209], v33 offset:37024\n" \
"ds_read_b64_tr_b16 a[210:211], v33 offset:41632\n" \
"v_mfma_f32_16x16x32_bf16 v[216:219], a[212:215], a[104:107], v[216:219]\n" \
"v_mfma_f32_16x16x32_bf16 v[220:223], a[212:215], a[120:123], v[220:223]\n" \
"ds_read_b64_tr_b16 a[212:213], v33 offset:46240\n" \
"ds_read_b64_tr_b16 a[214:215], v33 offset:50848\n" \
"v_mfma_f32_16x16x32_bf16 v[216:219], a[216:219], a[108:111], v[216:219]\n" \
"v_mfma_f32_16x16x32_bf16 v[220:223], a[216:219], a[124:127], v[220:223]\n" \
"ds_read_b64_tr_b16 a[216:217], v33 offset:55456\n" \
"ds_read_b64_tr_b16 a[218:219], v33 offset:60064\n" \
"v_mfma_f32_16x16x32_bf16 v[216:219], a[0:3], a[160:163], v[216:219]\n" \
"v_mfma_f32_16x16x32_bf16 v[220:223], a[0:3], a[176:179], v[220:223]\n" \
"ds_read_b64_tr_b16 a[0:1], v10 offset:160\n" \
"ds_read_b64_tr_b16 a[2:3], v10 offset:4768\n" \
"v_mfma_f32_16x16x32_bf16 v[216:219], a[4:7], a[164:167], v[216:219]\n" \
"v_mfma_f32_16x16x32_bf16 v[220:223], a[4:7], a[180:183], v[220:223]\n" \
"ds_read_b64_tr_b16 a[4:5], v34 offset:160\n" \
"s_waitcnt lgkmcnt(13)\n" \
"v_mfma_f32_16x16x32_bf16 v[216:219], a[8:11], a[168:171], v[216:219]\n" \
"ds_read_b64_tr_b16 a[6:7], v34 offset:4768\n" \
"v_mfma_f32_16x16x32_bf16 v[220:223], a[8:11], a[184:187], v[220:223]\n" \
"ds_read_b64_tr_b16 a[8:9], v34 offset:9376\n" \
"ds_read_b64_tr_b16 a[10:11], v34 offset:13984\n" \
"v_mfma_f32_16x16x32_bf16 v[216:219], a[12:15], a[172:175], v[216:219]\n" \
"v_mfma_f32_16x16x32_bf16 v[220:223], a[12:15], a[188:191], v[220:223]\n" \
"ds_read_b64_tr_b16 a[12:13], v34 offset:18592\n" \
"ds_read_b64_tr_b16 a[14:15], v34 offset:23200\n" \
"v_mfma_f32_16x16x32_bf16 v[216:219], a[16:19], a[220:223], v[216:219]\n" \
"v_mfma_f32_16x16x32_bf16 v[220:223], a[16:19], a[236:239], v[220:223]\n" \
"ds_read_b64_tr_b16 a[16:17], v10 offset:9376\n" \
"ds_read_b64_tr_b16 a[18:19], v10 offset:13984\n" \
"s_waitcnt vmcnt(6)\n" \
"s_mov_b64 exec, 1\n" \
"buffer_store_dword v39, off, s[56:59], s94 offset:32 sc1\n" \
"s_mov_b64 exec, -1\n" \
"v_mfma_f32_16x16x32_bf16 v[216:219], a[20:23], a[224:227], v[216:219]\n" \
"v_mfma_f32_16x16x32_bf16 v[220:223], a[20:23], a[240:243], v[220:223]\n" \
"ds_read_b64_tr_b16 a[20:21], v33 offset:192\n" \
"ds_read_b64_tr_b16 a[22:23], v33 offset:4800\n" \
"v_mfma_f32_16x16x32_bf16 v[216:219], a[24:27], a[228:231], v[216:219]\n" \
"v_mfma_f32_16x16x32_bf16 v[220:223], a[24:27], a[244:247], v[220:223]\n" \
"ds_read_b64_tr_b16 a[24:25], v33 offset:9408\n" \
"ds_read_b64_tr_b16 a[26:27], v33 offset:14016\n" \
"v_mfma_f32_16x16x32_bf16 v[216:219], a[28:31], a[232:235], v[216:219]\n" \
"v_mfma_f32_16x16x32_bf16 v[220:223], a[28:31], a[248:251], v[220:223]\n" \
"buffer_load_dword v37, v40, s[56:59], s74 offen sc1\n" \
"v_mfma_f32_16x16x32_bf16 v[224:227], a[192:195], a[96:99], v[224:227]\n" \
"ds_read_b64_tr_b16 a[28:29], v33 offset:18624\n" \
"ds_read_b64_tr_b16 a[30:31], v33 offset:23232\n" \
"v_mfma_f32_16x16x32_bf16 v[228:231], a[192:195], a[112:115], v[228:231]\n" \
"ds_read_b64_tr_b16 a[192:193], v33 offset:27840\n" \
"ds_read_b64_tr_b16 a[194:195], v33 offset:32448\n" \
"v_cvt_pk_bf16_f32 v216, v216, v217\n" \
"v_cvt_pk_bf16_f32 v217, v218, v219\n" \
"v_cvt_pk_bf16_f32 v218, v220, v221\n" \
"v_cvt_pk_bf16_f32 v219, v222, v223\n" \
"buffer_store_dwordx4 v[216:219], v20, s[56:59], s64 offen offset:1024 sc1\n" \
"s_add_u32 s64, s64, 0x1800\n" \
"v_mfma_f32_16x16x32_bf16 v[224:227], a[196:199], a[100:103], v[224:227]\n" \
"v_mfma_f32_16x16x32_bf16 v[228:231], a[196:199], a[116:119], v[228:231]\n" \
"ds_read_b64_tr_b16 a[196:197], v33 offset:37056\n" \
"s_waitcnt lgkmcnt(11)\n" \
"v_mfma_f32_16x16x32_bf16 v[224:227], a[200:203], a[104:107], v[224:227]\n" \
"ds_read_b64_tr_b16 a[198:199], v33 offset:41664\n" \
"v_mfma_f32_16x16x32_bf16 v[228:231], a[200:203], a[120:123], v[228:231]\n" \
"ds_read_b64_tr_b16 a[200:201], v33 offset:46272\n" \
"ds_read_b64_tr_b16 a[202:203], v33 offset:50880\n" \
"v_mfma_f32_16x16x32_bf16 v[224:227], a[204:207], a[108:111], v[224:227]\n" \
"v_mfma_f32_16x16x32_bf16 v[228:231], a[204:207], a[124:127], v[228:231]\n" \
"ds_read_b64_tr_b16 a[204:205], v33 offset:55488\n" \
"ds_read_b64_tr_b16 a[206:207], v33 offset:60096\n" \
"v_mfma_f32_16x16x32_bf16 v[224:227], a[208:211], a[160:163], v[224:227]\n" \
"v_mfma_f32_16x16x32_bf16 v[228:231], a[208:211], a[176:179], v[228:231]\n" \
"ds_read_b64_tr_b16 a[208:209], v10 offset:192\n" \
"ds_read_b64_tr_b16 a[210:211], v10 offset:4800\n" \
"v_mfma_f32_16x16x32_bf16 v[224:227], a[212:215], a[164:167], v[224:227]\n" \
"v_mfma_f32_16x16x32_bf16 v[228:231], a[212:215], a[180:183], v[228:231]\n" \
"ds_read_b64_tr_b16 a[212:213], v34 offset:192\n" \
"ds_read_b64_tr_b16 a[214:215], v34 offset:4800\n" \
"v_mfma_f32_16x16x32_bf16 v[224:227], a[216:219], a[168:171], v[224:227]\n" \
"v_mfma_f32_16x16x32_bf16 v[228:231], a[216:219], a[184:187], v[228:231]\n" \
"ds_read_b64_tr_b16 a[216:217], v34 offset:9408\n" \
"ds_read_b64_tr_b16 a[218:219], v34 offset:14016\n" \
"v_mfma_f32_16x16x32_bf16 v[224:227], a[0:3], a[172:175], v[224:227]\n" \
"v_mfma_f32_16x16x32_bf16 v[228:231], a[0:3], a[188:191], v[228:231]\n" \
"ds_read_b64_tr_b16 a[0:1], v34 offset:18624\n" \
"ds_read_b64_tr_b16 a[2:3], v34 offset:23232\n" \
"v_mfma_f32_16x16x32_bf16 v[224:227], a[4:7], a[220:223], v[224:227]\n" \
"v_mfma_f32_16x16x32_bf16 v[228:231], a[4:7], a[236:239], v[228:231]\n" \
"ds_read_b64_tr_b16 a[4:5], v10 offset:9408\n" \
"ds_read_b64_tr_b16 a[6:7], v10 offset:14016\n" \
"s_waitcnt vmcnt(6)\n" \
"s_mov_b64 exec, 1\n" \
"buffer_store_dword v39, off, s[56:59], s95 offset:36 sc1\n" \
"s_mov_b64 exec, -1\n" \
"v_mfma_f32_16x16x32_bf16 v[224:227], a[8:11], a[224:227], v[224:227]\n" \
"v_mfma_f32_16x16x32_bf16 v[228:231], a[8:11], a[240:243], v[228:231]\n" \
"ds_read_b64_tr_b16 a[8:9], v33 offset:224\n" \
"ds_read_b64_tr_b16 a[10:11], v33 offset:4832\n" \
"v_mfma_f32_16x16x32_bf16 v[224:227], a[12:15], a[228:231], v[224:227]\n" \
"v_mfma_f32_16x16x32_bf16 v[228:231], a[12:15], a[244:247], v[228:231]\n" \
"ds_read_b64_tr_b16 a[12:13], v33 offset:9440\n" \
"s_waitcnt lgkmcnt(13)\n" \
"v_mfma_f32_16x16x32_bf16 v[224:227], a[16:19], a[232:235], v[224:227]\n" \
"ds_read_b64_tr_b16 a[14:15], v33 offset:14048\n" \
"v_mfma_f32_16x16x32_bf16 v[228:231], a[16:19], a[248:251], v[228:231]\n" \
"ds_read_b64_tr_b16 a[16:17], v33 offset:18656\n" \
"ds_read_b64_tr_b16 a[18:19], v33 offset:23264\n" \
"v_mfma_f32_16x16x32_bf16 v[232:235], a[20:23], a[96:99], v[232:235]\n" \
"v_mfma_f32_16x16x32_bf16 v[236:239], a[20:23], a[112:115], v[236:239]\n" \
"ds_read_b64_tr_b16 a[20:21], v33 offset:27872\n" \
"ds_read_b64_tr_b16 a[22:23], v33 offset:32480\n" \
"v_cvt_pk_bf16_f32 v224, v224, v225\n" \
"v_cvt_pk_bf16_f32 v225, v226, v227\n" \
"v_cvt_pk_bf16_f32 v226, v228, v229\n" \
"v_cvt_pk_bf16_f32 v227, v230, v231\n" \
"buffer_store_dwordx4 v[224:227], v20, s[56:59], s73 offen offset:1024 sc1\n" \
"v_mfma_f32_16x16x32_bf16 v[232:235], a[24:27], a[100:103], v[232:235]\n" \
"v_mfma_f32_16x16x32_bf16 v[236:239], a[24:27], a[116:119], v[236:239]\n" \
"ds_read_b64_tr_b16 a[24:25], v33 offset:37088\n" \
"ds_read_b64_tr_b16 a[26:27], v33 offset:41696\n" \
"v_mfma_f32_16x16x32_bf16 v[232:235], a[28:31], a[104:107], v[232:235]\n" \
"v_mfma_f32_16x16x32_bf16 v[236:239], a[28:31], a[120:123], v[236:239]\n" \
"ds_read_b64_tr_b16 a[28:29], v33 offset:46304\n" \
"ds_read_b64_tr_b16 a[30:31], v33 offset:50912\n" \
"v_mfma_f32_16x16x32_bf16 v[232:235], a[192:195], a[108:111], v[232:235]\n" \
"v_mfma_f32_16x16x32_bf16 v[236:239], a[192:195], a[124:127], v[236:239]\n" \
"ds_read_b64_tr_b16 a[192:193], v33 offset:55520\n" \
"ds_read_b64_tr_b16 a[194:195], v33 offset:60128\n" \
"v_mfma_f32_16x16x32_bf16 v[232:235], a[196:199], a[160:163], v[232:235]\n" \
"v_mfma_f32_16x16x32_bf16 v[236:239], a[196:199], a[176:179], v[236:239]\n" \
"ds_read_b64_tr_b16 a[196:197], v10 offset:224\n" \
"ds_read_b64_tr_b16 a[198:199], v10 offset:4832\n" \
"v_mfma_f32_16x16x32_bf16 v[232:235], a[200:203], a[164:167], v[232:235]\n" \
"v_mfma_f32_16x16x32_bf16 v[236:239], a[200:203], a[180:183], v[236:239]\n" \
"ds_read_b64_tr_b16 a[200:201], v34 offset:224\n" \
"ds_read_b64_tr_b16 a[202:203], v34 offset:4832\n" \
"v_mfma_f32_16x16x32_bf16 v[232:235], a[204:207], a[168:171], v[232:235]\n" \
"v_mfma_f32_16x16x32_bf16 v[236:239], a[204:207], a[184:187], v[236:239]\n" \
"ds_read_b64_tr_b16 a[204:205], v34 offset:9440\n" \
"s_waitcnt lgkmcnt(13)\n" \
"v_mfma_f32_16x16x32_bf16 v[232:235], a[208:211], a[172:175], v[232:235]\n" \
"ds_read_b64_tr_b16 a[206:207], v34 offset:14048\n" \
"v_mfma_f32_16x16x32_bf16 v[236:239], a[208:211], a[188:191], v[236:239]\n" \
"ds_read_b64_tr_b16 a[208:209], v34 offset:18656\n" \
"ds_read_b64_tr_b16 a[210:211], v34 offset:23264\n" \
"v_mfma_f32_16x16x32_bf16 v[232:235], a[212:215], a[220:223], v[232:235]\n" \
"s_waitcnt vmcnt(7)\n" \
"s_mov_b64 exec, 1\n" \
"buffer_store_dword v39, off, s[56:59], s96 offset:40 sc1\n" \
"s_mov_b64 exec, -1\n" \
"v_mfma_f32_16x16x32_bf16 v[236:239], a[212:215], a[236:239], v[236:239]\n" \
"ds_read_b64_tr_b16 a[212:213], v10 offset:9440\n" \
"ds_read_b64_tr_b16 a[214:215], v10 offset:14048\n" \
"v_mfma_f32_16x16x32_bf16 v[232:235], a[216:219], a[224:227], v[232:235]\n" \
"v_mfma_f32_16x16x32_bf16 v[236:239], a[216:219], a[240:243], v[236:239]\n" \
"ds_read_b64_tr_b16 a[216:217], v36 offset:0\n" \
"ds_read_b64_tr_b16 a[218:219], v36 offset:1024\n" \
"v_mfma_f32_16x16x32_bf16 v[232:235], a[0:3], a[228:231], v[232:235]\n" \
"v_mfma_f32_16x16x32_bf16 v[236:239], a[0:3], a[244:247], v[236:239]\n" \
"ds_read_b64_tr_b16 a[0:1], v36 offset:2048\n" \
"ds_read_b64_tr_b16 a[2:3], v36 offset:3072\n" \
"v_mfma_f32_16x16x32_bf16 v[232:235], a[4:7], a[232:235], v[232:235]\n" \
"v_mfma_f32_16x16x32_bf16 v[236:239], a[4:7], a[248:251], v[236:239]\n" \
"ds_read_b64_tr_b16 a[4:5], v36 offset:32\n" \
"ds_read_b64_tr_b16 a[6:7], v36 offset:1056\n" \
"v_mfma_f32_16x16x32_bf16 v[240:243], a[8:11], a[96:99], v[240:243]\n" \
"v_mfma_f32_16x16x32_bf16 v[244:247], a[8:11], a[112:115], v[244:247]\n" \
"ds_read_b64_tr_b16 a[8:9], v36 offset:2080\n" \
"ds_read_b64_tr_b16 a[10:11], v36 offset:3104\n" \
"v_mfma_f32_16x16x32_bf16 v[240:243], a[12:15], a[100:103], v[240:243]\n" \
"v_cvt_pk_bf16_f32 v232, v232, v233\n" \
"v_cvt_pk_bf16_f32 v233, v234, v235\n" \
"v_cvt_pk_bf16_f32 v234, v236, v237\n" \
"v_cvt_pk_bf16_f32 v235, v238, v239\n" \
"buffer_store_dwordx4 v[232:235], v20, s[56:59], s92 offen offset:1024 sc1\n" \
"s_waitcnt vmcnt(5)\n" \
"v_cmp_eq_u32_e64 s[98:99], s62, v37\n" \
"s_or_b32 s75, 0x7f, s100\n" \
"s_andn2_b32 s75, s75, s98\n" \
"s_cbranch_scc1 L_sx0_%=\n" \
"L_rx0_%=:\n" \
"s_mov_b64 exec, 1\n" \
"buffer_store_dword v39, off, s[56:59], s97 offset:44 sc1\n" \
"s_mov_b64 exec, -1\n" \
"s_add_u32 s73, s73, 0x1000\n" \
"s_add_u32 s92, s92, 0x800\n" \
"s_add_u32 s65, s63, 0x0\n" \
"s_add_u32 s66, s63, 0x800\n" \
"s_add_u32 s67, s63, 0x1000\n" \
"s_add_u32 s68, s63, 0x1800\n" \
"s_add_u32 s69, s63, 0x2000\n" \
"s_add_u32 s70, s63, 0x2800\n" \
"s_add_u32 s71, s63, 0x3000\n" \
"v_mfma_f32_16x16x32_bf16 v[244:247], a[12:15], a[116:119], v[244:247]\n" \
"v_mfma_f32_16x16x32_bf16 v[240:243], a[16:19], a[104:107], v[240:243]\n" \
"buffer_load_dwordx4 v[116:119], v20, s[56:59], s65 offen offset:0 sc1\n" \
"v_mfma_f32_16x16x32_bf16 v[244:247], a[16:19], a[120:123], v[244:247]\n" \
"s_waitcnt lgkmcnt(6)\n" \
"v_mfma_f32_16x16x32_bf16 v[240:243], a[20:23], a[108:111], v[240:243]\n" \
"v_mfma_f32_16x16x32_bf16 v[244:247], a[20:23], a[124:127], v[244:247]\n" \
"v_mfma_f32_16x16x32_bf16 v[168:171], a[24:27], a[128:131], v[168:171]\n" \
"buffer_load_dwordx4 v[124:127], v20, s[56:59], s66 offen offset:0 sc1\n" \
"v_mfma_f32_16x16x32_bf16 v[172:175], a[24:27], a[144:147], v[172:175]\n" \
"v_readlane_b32 s66, v11, 53\n" \
"v_mfma_f32_16x16x32_bf16 v[240:243], a[24:27], a[160:163], v[240:243]\n" \
"v_mfma_f32_16x16x32_bf16 v[244:247], a[24:27], a[176:179], v[244:247]\n" \
"v_mfma_f32_16x16x32_bf16 v[168:171], a[28:31], a[132:135], v[168:171]\n" \
"buffer_load_dwordx4 v[132:135], v20, s[56:59], s67 offen offset:0 sc1\n" \
"v_mfma_f32_16x16x32_bf16 v[172:175], a[28:31], a[148:151], v[172:175]\n" \
"v_readlane_b32 s67, v11, 44\n" \
"v_mfma_f32_16x16x32_bf16 v[240:243], a[28:31], a[164:167], v[240:243]\n" \
"v_mfma_f32_16x16x32_bf16 v[244:247], a[28:31], a[180:183], v[244:247]\n" \
"v_mfma_f32_16x16x32_bf16 v[168:171], a[192:195], a[136:139], v[168:171]\n" \
"buffer_load_dwordx4 v[140:143], v20, s[56:59], s68 offen offset:0 sc1\n" \
"v_mfma_f32_16x16x32_bf16 v[172:175], a[192:195], a[152:155], v[172:175]\n" \
"v_readlane_b32 s68, v11, 42\n" \
"v_mfma_f32_16x16x32_bf16 v[240:243], a[192:195], a[168:171], v[240:243]\n" \
"v_mfma_f32_16x16x32_bf16 v[244:247], a[192:195], a[184:187], v[244:247]\n" \
"v_mfma_f32_16x16x32_bf16 v[168:171], a[196:199], a[140:143], v[168:171]\n" \
"buffer_load_dwordx4 v[148:151], v20, s[56:59], s69 offen offset:0 sc1\n" \
"v_mfma_f32_16x16x32_bf16 v[172:175], a[196:199], a[156:159], v[172:175]\n" \
"v_readlane_b32 s69, v11, 43\n" \
"v_mfma_f32_16x16x32_bf16 v[240:243], a[196:199], a[172:175], v[240:243]\n" \
"v_mfma_f32_16x16x32_bf16 v[244:247], a[196:199], a[188:191], v[244:247]\n" \
"v_mfma_f32_16x16x32_bf16 v[168:171], a[200:203], a[32:35], v[168:171]\n" \
"buffer_load_dwordx4 v[156:159], v20, s[56:59], s70 offen offset:0 sc1\n" \
"v_mfma_f32_16x16x32_bf16 v[172:175], a[200:203], a[48:51], v[172:175]\n" \
"v_mfma_f32_16x16x32_bf16 v[240:243], a[200:203], a[220:223], v[240:243]\n" \
"v_mfma_f32_16x16x32_bf16 v[244:247], a[200:203], a[236:239], v[244:247]\n" \
"v_mfma_f32_16x16x32_bf16 v[168:171], a[204:207], a[36:39], v[168:171]\n" \
"buffer_load_dwordx4 v[164:167], v20, s[56:59], s71 offen offset:0 sc1\n" \
"v_mfma_f32_16x16x32_bf16 v[172:175], a[204:207], a[52:55], v[172:175]\n" \
"v_mfma_f32_16x16x32_bf16 v[240:243], a[204:207], a[224:227], v[240:243]\n" \
"v_mfma_f32_16x16x32_bf16 v[244:247], a[204:207], a[240:243], v[244:247]\n" \
"v_mfma_f32_16x16x32_bf16 v[168:171], a[208:211], a[40:43], v[168:171]\n" \
"buffer_load_dwordx4 v[112:115], v20, s[56:59], s72 offen offset:0 sc1\n" \
"v_mfma_f32_16x16x32_bf16 v[172:175], a[208:211], a[56:59], v[172:175]\n" \
"v_mfma_f32_16x16x32_bf16 v[240:243], a[208:211], a[228:231], v[240:243]\n" \
"v_mfma_f32_16x16x32_bf16 v[244:247], a[208:211], a[244:247], v[244:247]\n" \
"v_mfma_f32_16x16x32_bf16 v[168:171], a[212:215], a[44:47], v[168:171]\n" \
"buffer_load_dwordx4 v[184:187], v20, s[56:59], s72 offen offset:2048 sc1\n" \
"s_add_u32 s72, s72, 0x1000\n" \
"v_mfma_f32_16x16x32_bf16 v[172:175], a[212:215], a[60:63], v[172:175]\n" \
"v_mfma_f32_16x16x32_bf16 v[240:243], a[212:215], a[232:235], v[240:243]\n" \
"s_waitcnt vmcnt(14)\n" \
"s_mov_b64 exec, 1\n" \
"buffer_store_dword v39, off, s[56:59], s64 offset:48 sc1\n" \
"s_mov_b64 exec, -1\n" \
"v_mfma_f32_16x16x32_bf16 v[244:247], a[212:215], a[248:251], v[244:247]\n" \
"v_mfma_f32_16x16x32_bf16 v[168:171], a[216:219], v[64:67], v[168:171]\n" \
"v_mfma_f32_16x16x32_bf16 v[172:175], a[216:219], v[72:75], v[172:175]\n" \
"v_mfma_f32_16x16x32_bf16 v[240:243], a[216:219], v[80:83], v[240:243]\n" \
"buffer_load_dwordx4 v[120:123], v20, s[56:59], s72 offen offset:0 sc1\n" \
"v_mfma_f32_16x16x32_bf16 v[244:247], a[216:219], v[88:91], v[244:247]\n" \
"s_waitcnt lgkmcnt(0)\n" \
"v_mfma_f32_16x16x32_bf16 v[168:171], a[0:3], v[68:71], v[168:171]\n" \
"v_mfma_f32_16x16x32_bf16 v[172:175], a[0:3], v[76:79], v[172:175]\n" \
"v_mfma_f32_16x16x32_bf16 v[240:243], a[0:3], v[84:87], v[240:243]\n" \
"buffer_load_dwordx4 v[192:195], v20, s[56:59], s72 offen offset:2048 sc1\n" \
"s_add_u32 s72, s72, 0x1000\n" \
"v_mfma_f32_16x16x32_bf16 v[244:247], a[0:3], v[92:95], v[244:247]\n" \
"v_mfma_f32_16x16x32_bf16 v[56:59], a[4:7], v[96:99], 0\n" \
"v_mfma_f32_16x16x32_bf16 v[60:63], a[4:7], v[104:107], 0\n" \
"v_mfma_f32_16x16x32_bf16 v[56:59], a[8:11], v[100:103], v[56:59]\n" \
"buffer_load_dwordx4 v[128:131], v20, s[56:59], s72 offen offset:0 sc1\n" \
"v_mfma_f32_16x16x32_bf16 v[60:63], a[8:11], v[108:111], v[60:63]\n" \
"buffer_load_dwordx4 v[200:203], v20, s[56:59], s72 offen offset:2048 sc1\n" \
"s_add_u32 s72, s72, 0x1000\n" \
"buffer_load_dwordx4 v[176:179], v20, s[56:59], s72 offen offset:0 sc1\n" \
"buffer_load_dwordx4 v[248:251], v20, s[56:59], s72 offen offset:2048 sc1\n" \
"s_waitcnt vmcnt(15)\n" \
"v_mfma_f32_16x16x32_bf16 v[168:171], v[50:53], v[116:119], v[168:171]\n" \
"s_mov_b64 exec, 1\n" \
"buffer_store_dword v39, off, s[56:59], s73 offset:52 sc1\n" \
"s_mov_b64 exec, -1\n" \
"s_mov_b64 exec, 1\n" \
"buffer_store_dword v39, off, s[56:59], s92 offset:56 sc1\n" \
"s_mov_b64 exec, -1\n" \
"buffer_load_dword v37, v40, s[56:59], s74 offen sc1\n" \
"v_mfma_f32_16x16x32_bf16 v[172:175], v[48:51], v[116:119], v[172:175]\n" \
"s_waitcnt vmcnt(17)\n" \
"v_mfma_f32_16x16x32_bf16 v[168:171], v[50:53], v[124:127], v[168:171]\n" \
"v_mfma_f32_16x16x32_bf16 v[172:175], v[48:51], v[124:127], v[172:175]\n" \
"s_waitcnt vmcnt(16)\n" \
"v_mfma_f32_16x16x32_bf16 v[168:171], v[50:53], v[132:135], v[168:171]\n" \
"v_mfma_f32_16x16x32_bf16 v[172:175], v[48:51], v[132:135], v[172:175]\n" \
"s_waitcnt vmcnt(15)\n" \
"v_mfma_f32_16x16x32_bf16 v[168:171], v[50:53], v[140:143], v[168:171]\n" \
"v_mfma_f32_16x16x32_bf16 v[172:175], v[48:51], v[140:143], v[172:175]\n" \
"s_waitcnt vmcnt(14)\n" \
"v_mfma_f32_16x16x32_bf16 v[168:171], v[50:53], v[148:151], v[168:171]\n" \
"v_mfma_f32_16x16x32_bf16 v[172:175], v[48:51], v[148:151], v[172:175]\n" \
"s_waitcnt vmcnt(13)\n" \
"v_mfma_f32_16x16x32_bf16 v[168:171], v[50:53], v[156:159], v[168:171]\n" \
"v_mfma_f32_16x16x32_bf16 v[172:175], v[48:51], v[156:159], v[172:175]\n" \
"s_waitcnt vmcnt(12)\n" \
"v_mfma_f32_16x16x32_bf16 v[168:171], v[50:53], v[164:167], v[168:171]\n" \
"v_mfma_f32_16x16x32_bf16 v[172:175], v[48:51], v[164:167], v[172:175]\n" \
"s_waitcnt vmcnt(11)\n" \
"v_mfma_f32_16x16x32_bf16 v[56:59], v[50:53], v[112:115], v[56:59]\n" \
"v_mfma_f32_16x16x32_bf16 v[60:63], v[48:51], v[112:115], v[60:63]\n" \
"s_waitcnt vmcnt(10)\n" \
"v_mfma_f32_16x16x32_bf16 v[56:59], v[50:53], v[184:187], v[56:59]\n" \
"v_mfma_f32_16x16x32_bf16 v[60:63], v[48:51], v[184:187], v[60:63]\n" \
"s_waitcnt vmcnt(8)\n" \
"v_mfma_f32_16x16x32_bf16 v[56:59], v[50:53], v[120:123], v[56:59]\n" \
"v_cvt_pk_bf16_f32 v168, v168, v169\n" \
"v_cvt_pk_bf16_f32 v172, v172, v173\n" \
"v_mfma_f32_16x16x32_bf16 v[60:63], v[48:51], v[120:123], v[60:63]\n" \
"v_cvt_pk_bf16_f32 v169, v170, v171\n" \
"v_cvt_pk_bf16_f32 v173, v174, v175\n" \
"buffer_store_dwordx2 v[168:169], v42, s[88:91], s68 offen sc1\n" \
"buffer_store_dwordx2 v[172:173], v43, s[88:91], s68 offen sc1\n" \
"s_waitcnt vmcnt(9)\n" \
"v_mfma_f32_16x16x32_bf16 v[56:59], v[50:53], v[192:195], v[56:59]\n" \
"v_mfma_f32_16x16x32_bf16 v[60:63], v[48:51], v[192:195], v[60:63]\n" \
"s_waitcnt vmcnt(8)\n" \
"v_mfma_f32_16x16x32_bf16 v[56:59], v[50:53], v[128:131], v[56:59]\n" \
"v_mfma_f32_16x16x32_bf16 v[60:63], v[48:51], v[128:131], v[60:63]\n" \
"s_waitcnt vmcnt(7)\n" \
"v_mfma_f32_16x16x32_bf16 v[56:59], v[50:53], v[200:203], v[56:59]\n" \
"v_mfma_f32_16x16x32_bf16 v[60:63], v[48:51], v[200:203], v[60:63]\n" \
"s_waitcnt vmcnt(6)\n" \
"v_mfma_f32_16x16x32_bf16 v[56:59], v[50:53], v[176:179], v[56:59]\n" \
"v_mfma_f32_16x16x32_bf16 v[60:63], v[48:51], v[176:179], v[60:63]\n" \
"s_waitcnt vmcnt(5)\n" \
"v_mfma_f32_16x16x32_bf16 v[56:59], v[50:53], v[248:251], v[56:59]\n" \
"v_mfma_f32_16x16x32_bf16 v[60:63], v[48:51], v[248:251], v[60:63]\n" \
"s_waitcnt vmcnt(2)\n" \
"v_cmp_eq_u32_e64 s[98:99], s62, v37\n" \
"s_nop 0\n" \
"s_andn2_b32 s75, 0x100, s98\n" \
"s_cbranch_scc1 L_sx1_%=\n" \
"L_rx1_%=:\n" \
"s_add_u32 s93, s63, 0x0\n" \
"buffer_load_dwordx4 v[188:191], v20, s[56:59], s93 offen offset:1024 sc1\n" \
"s_andn2_b32 s75, 0x200, s98\n" \
"s_cbranch_scc1 L_sx2_%=\n" \
"L_rx2_%=:\n" \
"s_add_u32 s94, s63, 0x800\n" \
"v_cvt_pk_bf16_f32 v56, v56, v57\n" \
"v_cvt_pk_bf16_f32 v60, v60, v61\n" \
"v_cvt_pk_bf16_f32 v57, v58, v59\n" \
"v_cvt_pk_bf16_f32 v61, v62, v63\n" \
"buffer_store_dwordx2 v[56:57], v42, s[88:91], s67 offen sc1\n" \
"buffer_store_dwordx2 v[60:61], v43, s[88:91], s67 offen sc1\n" \
"buffer_load_dwordx4 v[196:199], v20, s[56:59], s94 offen offset:1024 sc1\n" \
"s_andn2_b32 s75, 0x400, s98\n" \
"s_cbranch_scc1 L_sx3_%=\n" \
"L_rx3_%=:\n" \
"s_add_u32 s95, s63, 0x1000\n" \
"buffer_load_dwordx4 v[204:207], v20, s[56:59], s95 offen offset:1024 sc1\n" \
"s_andn2_b32 s75, 0x800, s98\n" \
"s_cbranch_scc1 L_sx4_%=\n" \
"L_rx4_%=:\n" \
"s_add_u32 s96, s63, 0x1800\n" \
"buffer_load_dwordx4 v[212:215], v20, s[56:59], s96 offen offset:1024 sc1\n" \
"s_andn2_b32 s75, 0x1000, s98\n" \
"s_cbranch_scc1 L_sx5_%=\n" \
"L_rx5_%=:\n" \
"s_add_u32 s97, s63, 0x2000\n" \
"buffer_load_dwordx4 v[220:223], v20, s[56:59], s97 offen offset:1024 sc1\n" \
"s_andn2_b32 s75, 0x2000, s98\n" \
"s_cbranch_scc1 L_sx6_%=\n" \
"L_rx6_%=:\n" \
"s_add_u32 s64, s63, 0x2800\n" \
"buffer_load_dwordx4 v[228:231], v20, s[56:59], s64 offen offset:1024 sc1\n" \
"s_andn2_b32 s75, 0x4000, s98\n" \
"s_cbranch_scc1 L_sx7_%=\n" \
"L_rx7_%=:\n" \
"s_add_u32 s65, s63, 0x3000\n" \
"buffer_load_dwordx4 v[236:239], v20, s[56:59], s65 offen offset:1024 sc1\n" \
"s_mov_b64 exec, 1\n" \
"buffer_store_dword v46, off, s[56:59], s66 offset:0 sc1\n" \
"s_mov_b64 exec, -1\n" \
"s_waitcnt vmcnt(9)\n" \
"v_mfma_f32_16x16x32_bf16 v[240:243], v[50:53], v[188:191], v[240:243]\n" \
"v_mfma_f32_16x16x32_bf16 v[244:247], v[48:51], v[188:191], v[244:247]\n" \
"s_waitcnt vmcnt(6)\n" \
"v_mfma_f32_16x16x32_bf16 v[240:243], v[50:53], v[196:199], v[240:243]\n" \
"v_mfma_f32_16x16x32_bf16 v[244:247], v[48:51], v[196:199], v[244:247]\n" \
"s_waitcnt vmcnt(5)\n" \
"v_mfma_f32_16x16x32_bf16 v[240:243], v[50:53], v[204:207], v[240:243]\n" \
"v_mfma_f32_16x16x32_bf16 v[244:247], v[48:51], v[204:207], v[244:247]\n" \
"s_waitcnt vmcnt(4)\n" \
"v_mfma_f32_16x16x32_bf16 v[240:243], v[50:53], v[212:215], v[240:243]\n" \
"v_mfma_f32_16x16x32_bf16 v[244:247], v[48:51], v[212:215], v[244:247]\n" \
"s_waitcnt vmcnt(3)\n" \
"v_mfma_f32_16x16x32_bf16 v[240:243], v[50:53], v[220:223], v[240:243]\n" \
"v_mfma_f32_16x16x32_bf16 v[244:247], v[48:51], v[220:223], v[244:247]\n" \
"s_waitcnt vmcnt(2)\n" \
"v_mfma_f32_16x16x32_bf16 v[240:243], v[50:53], v[228:231], v[240:243]\n" \
"v_mfma_f32_16x16x32_bf16 v[244:247], v[48:51], v[228:231], v[244:247]\n" \
"s_waitcnt vmcnt(1)\n" \
"v_mfma_f32_16x16x32_bf16 v[240:243], v[50:53], v[236:239], v[240:243]\n" \
"v_mfma_f32_16x16x32_bf16 v[244:247], v[48:51], v[236:239], v[244:247]\n" \
"s_nop 6\n" \
"v_cvt_pk_bf16_f32 v240, v240, v241\n" \
"v_cvt_pk_bf16_f32 v244, v244, v245\n" \
"v_cvt_pk_bf16_f32 v241, v242, v243\n" \
"v_cvt_pk_bf16_f32 v245, v246, v247\n" \
"buffer_store_dwordx2 v[240:241], v42, s[88:91], s69 offen sc1\n" \
"buffer_store_dwordx2 v[244:245], v43, s[88:91], s69 offen sc1\n"
#define ATT_VCLOB "v21","v22","v23","v24","v25","v26","v27","v28","v29","v32","v33","v34","v35","v36","v37","v38","v39","v40","v41","v42","v43","v44","v45","v46","v47","v48","v49","v50","v51","v52","v53","v54","v55","v56","v57","v58","v59","v60","v61","v62","v63","v64","v65","v66","v67","v68","v69","v70","v71","v72","v73","v74","v75","v76","v77","v78","v79","v80","v81","v82","v83","v84","v85","v86","v87","v88","v89","v90","v91","v92","v93","v94","v95","v96","v97","v98","v99","v100","v101","v102","v103","v104","v105","v106","v107","v108","v109","v110","v111","v112","v113","v114","v115","v116","v117","v118","v119","v120","v121","v122","v123","v124","v125","v126","v127","v128","v129","v130","v131","v132","v133","v134","v135","v136","v137","v138","v139","v140","v141","v142","v143","v144","v145","v146","v147","v148","v149","v150","v151","v152","v153","v154","v155","v156","v157","v158","v159","v160","v161","v162","v163","v164","v165","v166","v167","v168","v169","v170","v171","v172","v173","v174","v175","v176","v177","v178","v179","v180","v181","v182","v183","v184","v185","v186","v187","v188","v189","v190","v191","v192","v193","v194","v195","v196","v197","v198","v199","v200","v201","v202","v203","v204","v205","v206","v207","v208","v209","v210","v211","v212","v213","v214","v215","v216","v217","v218","v219","v220","v221","v222","v223","v224","v225","v226","v227","v228","v229","v230","v231","v232","v233","v234","v235","v236","v237","v238","v239","v240","v241","v242","v243","v244","v245","v246","v247","v248","v249","v250","v251","v252","v253","v254","v255"
#define ATT_SCLOB "s63","s64","s65","s66","s67","s68","s69","s70","s71","s72","s73","s74","s75","s76","s77","s78","s79","s80","s81","s82","s83","s84","s85","s86","s87","s88","s89","s90","s91","s92","s93","s94","s95","s96","s97","s98","s99","s100","s101"
#define ATT_WTCH 0
enum { T_fo, T_fj, T_fa, T_fas, T_fb, T_fbs, T_f8, T_mk8, T_wt, T_wol, T_woh, T_wbl, T_wbh, T_outl, T_outh, T_r1l, T_r1h, T_r1n, T_r1s, T_m0, T_m1, T_mw, T_m3, T_m4, T_ml, T_w0, T_w1, T_w2, T_w3, T_A1, T_A2, T_A3, T_B1, T_B2, T_B3, T_Ao, T_Bo, T_Al, T_Bl, T_l8, T_xr, T_x8r, T_oA, T_oB, T_o8, T_x0, T_x1, T_x2, T_x3, T_x4, T_x5, T_x6, T_x8, T_ep };

__device__ __forceinline__ void out_consts(u32x4 (&vp)[3], const u16* wo, const u16* wob, const u16* out, const unsigned b, const unsigned w, const unsigned lane) {
    const unsigned sp = b & 7u, nt = (b >> 3) & 3u, j = b >> 5, head = 4u * sp + nt, OOR = 0x40000000u;
    unsigned vw[9];
#pragma unroll
    for (unsigned i = 0; i < 9u; ++i) {
        const unsigned q = 64u * i + lane, row = __umul24(q, 3641u) >> 16, c = q - __umul24(row, 18u);
        vw[i] = __umul24(row, DM * 2) + (c < 16u ? (c * 16u + sp * 32u + 32u) & 255u : c * 16u) - (i & 3u) * 1024u;
    }
    const unsigned row = 16u * w + (lane >> 2), r9 = __umul24(16u * (row >> 5) + (row & 12u) + 2u * ((row >> 4) & 1u) + ((row >> 1) & 1u), 18u) + 16u + (row & 1u);
    unsigned vwl = __umul24(r9, DM * 2) + ((lane & 2u) ? 256u : sp * 32u) + (lane & 1u) * 16u, vrd = __umul24(lane >> 2, 288u) + (lane & 3u) * 8u;
    unsigned tab = 0;
#define TB(n, v) asm("v_writelane_b32 %0, %1, %2" : "+v"(tab) : "s"((unsigned)(v)), "n"((int)T_##n))
    const unsigned FJ = (unsigned)WS_F + (F_J + 640u * j) * 4u, mt = 2u * (w & 2u) + (w & 1u), w2 = w < 2u ? w : 2u, hw = head * 147456u;
    const unsigned FR = (unsigned)WS_F + F_R * 4u + (8u * j + sp) * 1024u;
    TB(fo, FJ + 4u * head); TB(fj, FJ); TB(fa, FR + (w | 2u) * 256u); TB(fas, FR + w * 256u + nt * 4u);
    TB(fb, FR + 128u + w * 256u); TB(fbs, FR + 128u + w * 256u + nt * 4u);
    TB(ep, ssel(b + w, OOR, (unsigned)WS_F + F_EPOCH * 4u));
    const u16* pwo = wo + nt * 144u; const u16* pwb = wob + nt * 144u;
    TB(wol, PLO(pwo)); TB(woh, PHI(pwo) & 0xffffu); TB(wbl, PLO(pwb)); TB(wbh, PHI(pwb) & 0xffffu); TB(outl, PLO(out)); TB(outh, PHI(out) & 0xffffu); TB(r1l, sslt<3>(w, PLO(pwo), PLO(pwb))); TB(r1h, sslt<3>(w, PHI(pwo), PHI(pwb)) & 0xffffu);
    TB(r1n, sslt<3>(w, 72u * sp + 71u, 4095u) * 1152u + 288u); TB(r1s, sslt<3>(w, (72u * sp + 32u * w) * 1152u, hw + 110592u));
    TB(m0, 131072u + 9216u * w2); TB(w0, hw + 36864u * w2); TB(m1, 24576u + 9216u * w); TB(mw, 65536u + 9216u * w); TB(m3, sslt<3>(w, 102400u + 9216u * w, 0u)); TB(m4, sslt<3>(w, 131072u + 9216u * w, 9216u)); TB(ml, 61440u + 1024u * w);
    const unsigned tA = (unsigned)WS_AF + (8u * j + mt) * (KS_TOT * 1024u), tB = (unsigned)WS_AF + (120u - 8u * j + mt) * (KS_TOT * 1024u);
#define HK(k) (4u * sp + ((nt + k) & 3u))
    TB(w1, HK(1u) * 147456u + w * 36864u); TB(w2, HK(2u) * 147456u + w * 36864u); TB(w3, HK(3u) * 147456u + w * 36864u);
    TB(A1, tA + 18432u + HK(1u) * 4096u); TB(A2, tA + 18432u + HK(2u) * 4096u); TB(A3, tA + 18432u + HK(3u) * 4096u);
    TB(B1, tB + 18432u + HK(1u) * 4096u); TB(B2, tB + 18432u + HK(2u) * 4096u); TB(B3, tB + 18432u + HK(3u) * 4096u);
    TB(Ao, tA + sp * 2048u); TB(Bo, tB + sp * 2048u); TB(Al, tA + 16384u); TB(Bl, tB + 16384u);
    const unsigned nred = w ^ (sp >> 1), blk = sp & 1u;
    const unsigned xg = (unsigned)WS_X + (4u * j + nt) * 589824u + w * 16384u, oA = (128u * j + 16u * mt) * 1152u + nt * 288u, oB = (1920u - 128u * j + 16u * mt) * 1152u + nt * 288u;
    TB(l8, ssel(nred, OOR, ssel(blk, tB, tA) + 16384u)); TB(xr, xg + sp * 65536u); TB(x8r, ssel(nred, OOR, xg + 524288u + (sp & 1u) * 1024u));
    TB(oA, oA + sp * 32u); TB(oB, oB + sp * 32u); TB(o8, ssel(nred, OOR, ssel(blk, oB, oA) + 256u)); TB(x8, xg + 524288u + sp * 2048u);
    TB(f8, xg + w * 131072u + 14400u + sp * 4u); TB(mk8, ssel(nred, 0u, 0xff0000u)); TB(wt, hw + j * 18432u + w * 4608u);
#define XI(i) TB(x##i, xg + ((sp + 1u + i) & 7u) * 65536u + i * 2048u)
    XI(0); XI(1); XI(2); XI(3); XI(4); XI(5); XI(6);
#undef XI
#undef HK
#undef TB
    vp[0] = (u32x4){vw[0], vw[1], vw[2], vw[3]}; vp[1] = (u32x4){vw[4], vw[5], vw[6], vw[7]}; vp[2] = (u32x4){vw[8], vwl, vrd, tab};
}
__global__ __launch_bounds__(256, 1) void fused_kernel(const u16* __restrict__ x, const u16* __restrict__ ckv, const u16* __restrict__ wq, const u16* __restrict__ wuk,
                                                       const u16* __restrict__ wuv, const u16* __restrict__ wqkv, const u16* __restrict__ gw,
                                                       const u16* __restrict__ wo, const u16* __restrict__ wob, char* __restrict__ ws_, u16* __restrict__ out) {
    using namespace att;
    extern __shared__ __attribute__((aligned(16))) char lds[];
    u32x4 sa[5]; u32x2 s24, s28; char* ws; unsigned tdum; kaptr ka = (kaptr)__builtin_amdgcn_kernarg_segment_ptr();
    asm volatile("s_load_dwordx8 s[8:15], %[ka], 0x0\n\ts_load_dwordx8 s[16:23], %[ka], 0x20\n\ts_getpc_b64 s[4:5]\n\tv_lshlrev_b32_e32 v191, 7, %[t]\n\ts_bfe_u32 s6, %[b], 0x20003\n\t"
                 "v_lshl_add_u32 v191, s6, 15, v191\n\tv_min_u32_e32 v191, %[e], v191\n\tglobal_load_dword v191, v191, s[4:5]\n\ts_load_dwordx4 s[24:27], %[ka], 0x40\n\ts_load_dwordx2 s[28:29], %[ka], 0x50"
                 : "={s[8:11]}"(sa[0]), "={s[12:15]}"(sa[1]), "={s[16:19]}"(sa[2]), "={s[20:23]}"(sa[3]), "={s[24:25]}"(s24), "={s[26:27]}"(ws), "={s[28:29]}"(s28), "={v191}"(tdum), [ka] "+{s[0:1]}"(ka)
                 : [t] "v"(threadIdx.x), [b] "s"(blockIdx.x), [e] "i"(CT_END) : "s4", "s5", "s6", "scc");
    SBAR();
    const unsigned wid = rfl(threadIdx.x >> 6);
    unsigned tag, vl;
    const int b = blockIdx.x;
    u32x4 vp[5], srdA; u32x2 sd;
    proj_phase(ka, ws, wid, vp, vl, sa, s24, s28, tdum, srdA, sd, tag
    );
    unsigned* const flags = (unsigned*)(ws + WS_F);
    RTS(2);
#define AST(L) do {} while (0)
#define ATT_TAIL "s_endpgm\n"
    asm volatile(ATT_ASM ATT_TAIL "L_end_%=:\n s_nop 1"
                 :
                 : "{v[0:3]}"(vp[0]), "{v[4:7]}"(vp[1]), "{v[8:11]}"(vp[2]), "{v[12:15]}"(vp[3]), "{v[16:19]}"(vp[4]), "{v20}"(vl), "{s[56:59]}"(srdA), "{s[60:61]}"(sd), "{s62}"(tag),
                   "{s[8:11]}"(sa[0]), "{s[12:15]}"(sa[1]), "{s[16:19]}"(sa[2]), "{s[20:23]}"(sa[3]), "{s24}"(sa[4][0])
                 : "memory", "m0", "vcc", "scc", ATT_VCLOB, ATT_SCLOB, ACLOB_LO, ACLOB_HI);
    {
        unsigned b2 = b, w2 = wid;
        asm volatile("s_waitcnt vmcnt(0)" : "+s"(ka), "+s"(b2), "+s"(w2), "+s"(ws) :: "memory");
        const unsigned j2 = b2 >> 5, p2 = w2 & 1u, h2 = (b2 & 7u) * 4u + ((b2 >> 3) & 3u);
#pragma unroll 1
        for (unsigned i = w2 & 2u; i < 2u + 3u * (w2 & 2u); ++i)
            att_exact(ka[0], ka[2], ws, h2, (i < 4u ? 1920u - 128u * j2 : 128u * j2 - 128u) + 32u * i + 16u * p2, *reinterpret_cast<const float*>(lds + LDS_G + (32u * i + 16u * p2 + (lane_id() & 15u)) * 4u));
        asm volatile("s_waitcnt vmcnt(0)" ::: "memory");
        unsigned* fa = (unsigned*)(ws + WS_F) + F_R + (8u * j2 + (b2 & 7u)) * 256u + (2u + p2) * 64u + ((b2 >> 3) & 3u);
        if (w2 & 2u) st_flag(fa, tag);
        wait_flags(fa, ld_flag(fa), tag);
        const unsigned l2 = lane_id(); u32x4 c[3]; out_consts(c, ka[7], ka[8], ka[10], b2, w2, l2);
        asm volatile("s_waitcnt vmcnt(0) lgkmcnt(0)\n\ts_nop 7\n\ts_branch L_att_stub" :: "{v[0:3]}"(c[0]), "{v[4:7]}"(c[1]), "{v[8:11]}"(c[2]), "{v20}"(l2 * 16u), "{s[56:59]}"(SRDA), "{s60}"(SD(8u * j2, h2, w2)),
                     "{s61}"(SD(120u - 8u * j2, h2, w2)), "{s62}"(tag) : "memory");
    }
    RTS(5);
}

extern "C" void kernel_launch(void* const* d_in, const int* in_sizes, int n_in, void* d_out, int out_size, void* d_ws, size_t ws_size,
                              hipStream_t stream) {
    (void)hipFuncSetAttribute((const void*)fused_kernel, hipFuncAttributeMaxDynamicSharedMemorySize, att::LDS_BYTES);
    fused_kernel<<<dim3(256), 256, att::LDS_BYTES, stream>>>((const u16*)d_in[0], (const u16*)d_in[4], (const u16*)d_in[5], (const u16*)d_in[6], (const u16*)d_in[7], (const u16*)d_in[1],
                                                             (const u16*)d_in[9], (const u16*)d_in[2], (const u16*)d_in[8], (char*)d_ws, (u16*)d_out);
}
```

```cpp
#include <hip/hip_runtime.h>
#include <hip/hip_bf16.h>
#include <stdint.h>
#include <utility>

typedef short bf16x8 __attribute__((ext_vector_type(8)));
typedef short s16x4 __attribute__((ext_vector_type(4)));
typedef float f32x16 __attribute__((ext_vector_type(16)));
typedef float f32x4 __attribute__((ext_vector_type(4)));
typedef unsigned u32x4 __attribute__((ext_vector_type(4)));
typedef unsigned u32x2 __attribute__((ext_vector_type(2)));
typedef unsigned short u16;

constexpr int S = 2048, DM = 576, NH = 32, HD = 128, DC = 512, NQKV = 1728, DHA = 18;
constexpr int NBIG = NH * HD;
constexpr int KS_TOT = 146;

#define SBAR() __builtin_amdgcn_sched_barrier(0)
constexpr size_t WS_Q = 0, WS_K = WS_Q + (size_t)S * NBIG * 2, WS_V = WS_K + (size_t)S * NBIG * 2, WS_AF = WS_V + (size_t)S * NBIG * 2, WS_X = WS_AF + (size_t)128 * KS_TOT * 1024,
                 WS_P = WS_X + (size_t)64 * 16 * 9216, WS_F = WS_P + (size_t)128 * (4 * 32 * 256 + 1024) * 4;
constexpr size_t WS_L = (size_t)96 << 20;
static_assert(WS_F == 96206848, "workspace map is baked into the generated asm");
constexpr int F_EPOCH = 0, F_J = 1024, F_ST = 8192, F_KV = 40960, F_R = 65536;
constexpr unsigned TAG_STEP = 0x9E3779B1u;
__device__ __forceinline__ unsigned ld_flag(const unsigned* p) { return __hip_atomic_load(p, __ATOMIC_RELAXED, __HIP_MEMORY_SCOPE_AGENT); }
__device__ __forceinline__ void st_flag(unsigned* p, unsigned v) { __hip_atomic_store(p, v, __ATOMIC_RELAXED, __HIP_MEMORY_SCOPE_AGENT); }
__device__ __forceinline__ unsigned ld_flag_async(const unsigned* p) { unsigned v; asm volatile("global_load_dword %0, %1, off sc1" : "=v"(v) : "v"(p) : "memory"); return v; }
#define WAITV(v, N) asm volatile("s_waitcnt vmcnt(" #N ")" : "+v"(v) :: "memory")
__device__ __forceinline__ void wait_flags(const unsigned* p, unsigned v, unsigned tag) {
    if (__builtin_expect(__builtin_amdgcn_ballot_w64(v != tag) != 0ull, 0)) {
        unsigned spins = 0;
        do { __builtin_amdgcn_s_sleep(1); } while (__builtin_amdgcn_ballot_w64(ld_flag(p) != tag) != 0ull && ++spins < (1u << 22));
    }
    asm volatile("" ::: "memory");
}

__device__ __forceinline__ unsigned cvtpk(float lo, float hi) {
    unsigned r; asm volatile("v_cvt_pk_bf16_f32 %0, %1, %2" : "=v"(r) : "v"(lo), "v"(hi)); return r;
}
__device__ __forceinline__ float bf2f(u16 v) { return __uint_as_float(((unsigned)v) << 16); }
__device__ __forceinline__ int crow(int r, int hi) { return (r & 3) + 8 * (r >> 2) + 4 * hi; }

typedef __attribute__((address_space(3))) s16x4* lds_s16x4_p;
__device__ __forceinline__ s16x4 trrd(unsigned addr) { return __builtin_amdgcn_ds_read_tr16_b64_v4i16((lds_s16x4_p)addr); }

__device__ __forceinline__ unsigned lane_id() { unsigned l; asm volatile("v_mbcnt_lo_u32_b32 %0, -1, 0\n\tv_mbcnt_hi_u32_b32 %0, -1, %0" : "=v"(l)); return l; }
#define RTS(slot) do {} while (0)
#define CT_END 0x1da80
#define ACLOB_LO "a0","a1","a2","a3","a4","a5","a6","a7","a8","a9","a10","a11","a12","a13","a14","a15","a16","a17","a18","a19","a20","a21","a22","a23","a24","a25","a26","a27","a28","a29","a30","a31","a32","a33","a34","a35","a36","a37","a38","a39","a40","a41","a42","a43","a44","a45","a46","a47","a48","a49","a50","a51","a52","a53","a54","a55","a56","a57","a58","a59","a60","a61","a62","a63","a64","a65","a66","a67","a68","a69","a70","a71","a72","a73","a74","a75","a76","a77","a78","a79","a80","a81","a82","a83","a84","a85","a86","a87","a88","a89","a90","a91","a92","a93","a94","a95","a96","a97","a98","a99","a100","a101","a102","a103","a104","a105","a106","a107","a108","a109","a110","a111","a112","a113","a114","a115","a116","a117","a118","a119","a120","a121","a122","a123","a124","a125","a126","a127"
#define ACLOB_HI "a128","a129","a130","a131","a132","a133","a134","a135","a136","a137","a138","a139","a140","a141","a142","a143","a144","a145","a146","a147","a148","a149","a150","a151","a152","a153","a154","a155","a156","a157","a158","a159","a160","a161","a162","a163","a164","a165","a166","a167","a168","a169","a170","a171","a172","a173","a174","a175","a176","a177","a178","a179","a180","a181","a182","a183","a184","a185","a186","a187","a188","a189","a190","a191","a192","a193","a194","a195","a196","a197","a198","a199","a200","a201","a202","a203","a204","a205","a206","a207","a208","a209","a210","a211","a212","a213","a214","a215","a216","a217","a218","a219","a220","a221","a222","a223","a224","a225","a226","a227","a228","a229","a230","a231","a232","a233","a234","a235","a236","a237","a238","a239","a240","a241","a242","a243","a244","a245","a246","a247","a248","a249","a250","a251","a252","a253","a254","a255"
#define PLO(p) ((unsigned)(size_t)(p))
#define PHI(p) ((unsigned)((size_t)(p) >> 32))
__device__ __forceinline__ unsigned vsel(bool c, unsigned a, unsigned b) { asm("" : "+v"(a), "+v"(b)); return c ? a : b; }
__device__ __forceinline__ unsigned ssel(unsigned c, unsigned a, unsigned b) { unsigned r; asm("s_cmp_lg_u32 %1, 0\n\ts_cselect_b32 %0, %2, %3" : "=s"(r) : "s"(c), "s"(a), "s"(b) : "scc"); return r; }
template <int N> __device__ __forceinline__ unsigned sslt(unsigned c, unsigned a, unsigned b) { unsigned r; asm("s_cmpk_lt_u32 %1, %4\n\ts_cselect_b32 %0, %2, %3" : "=s"(r) : "s"(c), "s"(a), "s"(b), "n"(N) : "scc"); return r; }
namespace att {
constexpr int LDS_BYTES = 163840;
constexpr unsigned LDS_G = 159744u, LDS_L = 161792u;
}
#define SD(J8, H, W) ((unsigned)WS_AF + (((J8) + 2u * ((W) & 2u) + ((W) & 1u)) * KS_TOT + 18u + 4u * (H)) * 1024u)
#define SRDA (u32x4){PLO(ws), PHI(ws) & 0xffffu, (unsigned)WS_L, 0x00020000u}
#define PROJ_ASM \
".p2align 3\n" \
"s_waitcnt lgkmcnt(0)\n" \
"s_mov_b32 s64, s10\n" \
"s_and_b32 s65, s11, 0xffff\n" \
"s_mov_b32 s66, 0x200000\n" \
"s_mov_b32 s67, 0x20000\n" \
"s_mov_b32 s68, s14\n" \
"s_and_b32 s69, s15, 0xffff\n" \
"s_mov_b32 s70, 0x400000\n" \
"s_mov_b32 s71, 0x20000\n" \
"s_mov_b32 s72, s16\n" \
"s_and_b32 s73, s17, 0xffff\n" \
"s_mov_b32 s74, 0x400000\n" \
"s_mov_b32 s75, 0x20000\n" \
"s_add_u32 m0, s30, 0x0\n" \
"s_mov_b32 s85, 0x0\n" \
"buffer_load_dwordx4 v22, s[64:67], s85 offen lds\n" \
"s_add_u32 m0, s30, 0x400\n" \
"s_mov_b32 s84, 0x2000\n" \
"buffer_load_dwordx4 v23, s[64:67], s84 offen lds\n" \
"s_add_u32 m0, s31, 0x0\n" \
"s_mov_b32 s85, 0x0\n" \
"buffer_load_dwordx4 v26, s[68:71], s85 offen lds\n" \
"s_add_u32 m0, s31, 0x4000\n" \
"s_mov_b32 s84, 0x0\n" \
"s_nop 0\n" \
"buffer_load_dwordx4 v26, s[72:75], s84 offen lds\n" \
"s_add_u32 m0, s30, 0x800\n" \
"s_mov_b32 s85, 0x4000\n" \
"buffer_load_dwordx4 v22, s[64:67], s85 offen lds\n" \
"s_add_u32 m0, s30, 0xc00\n" \
"s_mov_b32 s84, 0x6000\n" \
"buffer_load_dwordx4 v23, s[64:67], s84 offen lds\n" \
"s_add_u32 m0, s31, 0x400\n" \
"s_mov_b32 s85, 0x8000\n" \
"buffer_load_dwordx4 v26, s[68:71], s85 offen lds\n" \
"s_add_u32 m0, s31, 0x4400\n" \
"s_mov_b32 s84, 0x8000\n" \
"buffer_load_dwordx4 v26, s[72:75], s84 offen lds\n" \
"s_movk_i32 s7, 0x300\n" \
"s_lshr_b32 s4, s2, 1\n" \
"s_and_b32 s4, s4, 60\n" \
"s_or_b32 s4, s4, s3\n" \
"v_lshrrev_b32_e32 v212, 4, v20\n" \
"v_min_u32_e32 v212, 59, v212\n" \
"s_cmpk_lt_u32 s2, 0x80\n" \
"s_cselect_b32 s6, s14, s16\n" \
"v_subrev_u32_e32 v214, 48, v212\n" \
"v_mov_b32_e32 v212, s10\n" \
"v_mov_b32_e32 v215, s6\n" \
"s_cmpk_lt_u32 s2, 0x80\n" \
"s_cselect_b32 s5, s15, s17\n" \
"v_cmp_gt_u32_e32 vcc, s7, v20\n" \
"v_mov_b32_e64 v213, s11\n" \
"v_bfe_u32 v216, v20, 4, 3\n" \
"v_cndmask_b32_e32 v212, v212, v215, vcc\n" \
"v_mov_b32_e32 v215, s5\n" \
"s_lshl_b32 s5, s3, 4\n" \
"v_cndmask_b32_e32 v213, v213, v215, vcc\n" \
"v_lshlrev_b32_e64 v215, 5, v20\n" \
"v_and_b32_e32 v215, 0x7000, v215\n" \
"v_lshl_add_u32 v215, s4, 6, v215\n" \
"s_lshl_b32 s4, s2, 3\n" \
"s_and_b32 s4, s4, 56\n" \
"v_or3_b32 v215, v215, s4, v216\n" \
"s_lshl_b32 s4, s2, 6\n" \
"s_or_b32 s6, s5, s4\n" \
"v_cmp_gt_u32_e64 s[4:5], 6, v214\n" \
"v_add_u32_e32 v215, 0x2000, v215\n" \
"s_nop 0\n" \
"v_cndmask_b32_e64 v216, 4, 2, s[4:5]\n" \
"v_add3_u32 v214, s6, v214, v216\n" \
"s_lshl_b32 s4, s3, 9\n" \
"v_cndmask_b32_e32 v214, v214, v215, vcc\n" \
"v_lshlrev_b32_e32 v214, 7, v214\n" \
"v_mov_b32_e64 v215, 0\n" \
"v_lshl_add_u64 v[184:185], v[212:213], 0, v[214:215]\n" \
"s_or_b32 s32, s4, 0x25000\n" \
"s_add_u32 m0, s30, 0x1000\n" \
"s_mov_b32 s85, 0x8000\n" \
"buffer_load_dwordx4 v22, s[64:67], s85 offen lds\n" \
"s_and_b32 s33, s3, 1\n" \
"v_lshrrev_b32_e32 v212, 4, v20\n" \
"v_lshrrev_b32_e64 v214, 8, v20\n" \
"v_bfe_u32 v212, v212, 1, 3\n" \
"v_lshlrev_b32_e64 v215, 2, v20\n" \
"s_add_u32 m0, s30, 0x1400\n" \
"s_mov_b32 s84, 0xa000\n" \
"buffer_load_dwordx4 v23, s[64:67], s84 offen lds\n" \
"v_lshrrev_b32_e64 v216, 1, v20\n" \
"v_bfe_u32 v213, v20, 4, 4\n" \
"s_lshl_b32 s4, s3, 5\n" \
"v_xor_b32_e32 v212, v212, v214\n" \
"v_lshlrev_b32_e64 v214, 11, v214\n" \
"s_add_u32 m0, s31, 0x800\n" \
"s_mov_b32 s85, 0x10000\n" \
"buffer_load_dwordx4 v27, s[68:71], s85 offen lds\n" \
"v_and_b32_e32 v215, 0x300, v215\n" \
"v_and_b32_e64 v217, 24, v216\n" \
"v_and_or_b32 v213, s4, 64, v213\n" \
"v_or3_b32 v214, v215, v214, v217\n" \
"s_lshl_b32 s4, s33, 14\n" \
"s_add_u32 m0, s31, 0x4800\n" \
"s_mov_b32 s84, 0x10000\n" \
"buffer_load_dwordx4 v27, s[72:75], s84 offen lds\n" \
"v_and_b32_e32 v215, 0xe0, v216\n" \
"v_lshlrev_b32_e32 v213, 7, v213\n" \
"v_lshlrev_b32_e64 v212, 4, v212\n" \
"v_or3_b32 v214, v214, s4, v215\n" \
"v_or_b32_e64 v28, v212, v213\n" \
"s_add_u32 m0, s30, 0x1800\n" \
"s_mov_b32 s85, 0xc000\n" \
"buffer_load_dwordx4 v22, s[64:67], s85 offen lds\n" \
"v_add_u32_e32 v32, 0x10000, v214\n" \
"v_bitop3_b32 v29, v212, 64, v213 bitop3:0x36\n" \
"v_xor_b32_e32 v33, 0x20, v32\n" \
"v_xor_b32_e32 v34, 0x40, v32\n" \
"v_xor_b32_e32 v35, 0x60, v32\n" \
"s_add_u32 m0, s30, 0x1c00\n" \
"s_mov_b32 s84, 0xe000\n" \
"buffer_load_dwordx4 v23, s[64:67], s84 offen lds\n" \
"v_xor_b32_e32 v36, 0x80, v32\n" \
"v_xor_b32_e32 v37, 0xa0, v32\n" \
"v_xor_b32_e32 v38, 0xc0, v32\n" \
"v_xor_b32_e32 v39, 0xe0, v32\n" \
"s_mov_b32 s80, s26\n" \
"s_add_u32 m0, s31, 0xc00\n" \
"s_mov_b32 s85, 0x18000\n" \
"buffer_load_dwordx4 v27, s[68:71], s85 offen lds\n" \
"s_and_b32 s81, s27, 0xffff\n" \
"s_mov_b32 s82, 0x10000000\n" \
"s_mov_b32 s83, 0x20000\n" \
"s_mov_b32 s76, s18\n" \
"s_and_b32 s77, s19, 0xffff\n" \
"s_add_u32 m0, s31, 0x4c00\n" \
"s_mov_b32 s84, 0x18000\n" \
"buffer_load_dwordx4 v27, s[72:75], s84 offen lds\n" \
"s_mov_b32 s78, 0x1e6000\n" \
"s_mov_b32 s79, 0x20000\n" \
"s_add_u32 m0, s30, 0x8000\n" \
"s_mov_b32 s85, 0x80\n" \
"buffer_load_dwordx4 v22, s[64:67], s85 offen lds\n" \
"s_lshr_b32 s4, s2, 1\n" \
"s_and_b32 s4, s4, 0x7c\n" \
"v_bfe_u32 v214, v20, 4, 5\n" \
"s_add_u32 m0, s30, 0x8400\n" \
"s_mov_b32 s84, 0x2080\n" \
"buffer_load_dwordx4 v23, s[64:67], s84 offen lds\n" \
"s_or_b32 s7, s4, s3\n" \
"v_min_u32_e64 v214, 26, v214\n" \
"v_mad_u64_u32 v[214:215], s[4:5], s7, 27, v[214:215]\n" \
"s_add_u32 m0, s31, 0x8000\n" \
"s_mov_b32 s85, 0x80000\n" \
"buffer_load_dwordx4 v26, s[68:71], s85 offen lds\n" \
"v_mul_u32_u24_e32 v215, 0xaaab, v214\n" \
"v_lshrrev_b32_e64 v215, 18, v215\n" \
"s_mov_b32 s4, 0x1fffffa\n" \
"s_add_u32 m0, s31, 0xc000\n" \
"s_mov_b32 s84, 0x80000\n" \
"buffer_load_dwordx4 v26, s[72:75], s84 offen lds\n" \
"v_mad_u64_u32 v[216:217], s[4:5], v215, s4, v[214:215]\n" \
"s_and_b32 s6, s2, 7\n" \
"v_mul_u32_u24_e32 v215, 27, v215\n" \
"s_add_u32 m0, s30, 0x8800\n" \
"s_mov_b32 s85, 0x4080\n" \
"buffer_load_dwordx4 v22, s[64:67], s85 offen lds\n" \
"v_lshrrev_b32_e32 v216, 1, v216\n" \
"v_and_b32_e32 v214, 1, v214\n" \
"v_mov_b32_e64 v213, s13\n" \
"s_add_u32 m0, s30, 0x8c00\n" \
"s_mov_b32 s84, 0x6080\n" \
"buffer_load_dwordx4 v23, s[64:67], s84 offen lds\n" \
"v_lshrrev_b32_e32 v212, 4, v20\n" \
"v_mul_u32_u24_e32 v216, 9, v216\n" \
"v_add3_u32 v214, v214, s6, v215\n" \
"s_add_u32 m0, s31, 0x8400\n" \
"s_mov_b32 s85, 0x88000\n" \
"buffer_load_dwordx4 v26, s[68:71], s85 offen lds\n" \
"v_add_lshl_u32 v214, v214, v216, 7\n" \
"v_mad_u64_u32 v[216:217], s[4:5], s7, 36, v[212:213]\n" \
"s_movk_i32 s4, 0x240\n" \
"s_add_u32 m0, s31, 0xc400\n" \
"s_mov_b32 s84, 0x88000\n" \
"buffer_load_dwordx4 v26, s[72:75], s84 offen lds\n" \
"v_mov_b32_e32 v219, s9\n" \
"v_mov_b32_e32 v215, 0\n" \
"v_cmp_gt_u32_e32 vcc, s4, v20\n" \
"s_add_u32 m0, s30, 0x9000\n" \
"s_mov_b32 s85, 0x8080\n" \
"buffer_load_dwordx4 v22, s[64:67], s85 offen lds\n" \
"s_lshl_b32 s4, s2, 2\n" \
"v_lshl_add_u64 v[186:187], s[18:19], 0, v[214:215]\n" \
"v_mov_b32_e32 v214, s12\n" \
"s_add_u32 m0, s30, 0x9400\n" \
"s_mov_b32 s84, 0xa080\n" \
"buffer_load_dwordx4 v23, s[64:67], s84 offen lds\n" \
"v_mov_b32_e32 v217, s8\n" \
"s_or_b32 s4, s4, s3\n" \
"v_cndmask_b32_e64 v219, v219, v213, vcc\n" \
"s_add_u32 m0, s31, 0x8800\n" \
"s_mov_b32 s85, 0x90000\n" \
"buffer_load_dwordx4 v27, s[68:71], s85 offen lds\n" \
"v_and_b32_e32 v213, 0x3ff8, v216\n" \
"s_mul_i32 s4, s4, 18\n" \
"v_cndmask_b32_e32 v218, v217, v214, vcc\n" \
"s_add_u32 m0, s31, 0xc800\n" \
"s_mov_b32 s84, 0x90000\n" \
"buffer_load_dwordx4 v27, s[72:75], s84 offen lds\n" \
"v_or_b32_e32 v213, s6, v213\n" \
"v_and_b32_e32 v214, 7, v216\n" \
"v_min_u32_e64 v212, 53, v212\n" \
"s_add_u32 m0, s30, 0x9800\n" \
"s_mov_b32 s85, 0xc080\n" \
"buffer_load_dwordx4 v22, s[64:67], s85 offen lds\n" \
"s_sub_i32 s4, s4, 36\n" \
"v_lshl_or_b32 v213, v213, 3, v214\n" \
"v_add_u32_e32 v212, s4, v212\n" \
"s_add_u32 m0, s30, 0x9c00\n" \
"s_mov_b32 s84, 0xe080\n" \
"buffer_load_dwordx4 v23, s[64:67], s84 offen lds\n" \
"v_cndmask_b32_e32 v212, v212, v213, vcc\n" \
"v_lshlrev_b32_e32 v214, 7, v212\n" \
"v_lshl_add_u64 v[188:189], v[218:219], 0, v[214:215]\n" \
"s_add_u32 m0, s31, 0x8c00\n" \
"s_mov_b32 s85, 0x98000\n" \
"buffer_load_dwordx4 v27, s[68:71], s85 offen lds\n" \
"s_movk_i32 s5, 0xd80\n" \
"s_lshr_b32 s6, s2, 5\n" \
"v_lshrrev_b32_e64 v212, 4, v20\n" \
"s_add_u32 m0, s31, 0xcc00\n" \
"s_mov_b32 s84, 0x98000\n" \
"buffer_load_dwordx4 v27, s[72:75], s84 offen lds\n" \
"v_lshl_or_b32 v215, s3, 6, v212\n" \
"v_mul_u32_u24_e32 v216, 0x2aab, v215\n" \
"v_lshrrev_b32_e32 v216, 16, v216\n" \
"s_mov_b32 m0, s32\n" \
"s_mov_b32 exec_lo, 0xffffff\n" \
"s_mov_b32 exec_hi, 0xfff0000\n" \
"global_load_lds_dword v[184:185], off\n" \
"s_mov_b64 exec, -1\n" \
"s_mov_b32 s84, 0x5bc0000\n" \
"buffer_load_dword v180, off, s[80:83], s84 sc1\n" \
"s_cmp_lg_u32 s33, 0\n" \
"s_cbranch_scc1 L_w1_%=\n" \
"L_w0_%=:\n" \
"s_waitcnt vmcnt(18)\n" \
"s_barrier\n" \
"s_nop 0\n" \
"ds_read_b64_tr_b16 v[84:85], v32 offset:0\n" \
"ds_read_b64_tr_b16 v[86:87], v32 offset:1024\n" \
"ds_read_b64_tr_b16 v[148:149], v32 offset:8192\n" \
"ds_read_b64_tr_b16 v[150:151], v32 offset:9216\n" \
"ds_read_b128 v[52:55], v28 offset:16384\n" \
"ds_read_b128 v[116:119], v29 offset:16384\n" \
"ds_read_b64_tr_b16 v[88:89], v33 offset:0\n" \
"ds_read_b64_tr_b16 v[90:91], v33 offset:1024\n" \
"ds_read_b64_tr_b16 v[152:153], v33 offset:8192\n" \
"ds_read_b64_tr_b16 v[154:155], v33 offset:9216\n" \
"ds_read_b64_tr_b16 v[92:93], v34 offset:0\n" \
"ds_read_b64_tr_b16 v[94:95], v34 offset:1024\n" \
"ds_read_b64_tr_b16 v[156:157], v34 offset:8192\n" \
"ds_read_b64_tr_b16 v[158:159], v34 offset:9216\n" \
"ds_read_b64_tr_b16 v[96:97], v35 offset:0\n" \
"ds_read_b64_tr_b16 v[98:99], v35 offset:1024\n" \
"ds_read_b64_tr_b16 v[160:161], v35 offset:8192\n" \
"ds_read_b64_tr_b16 v[162:163], v35 offset:9216\n" \
"ds_read_b128 v[56:59], v28 offset:18432\n" \
"ds_read_b128 v[120:123], v29 offset:18432\n" \
"ds_read_b128 v[60:63], v28 offset:20480\n" \
"ds_read_b128 v[124:127], v29 offset:20480\n" \
"ds_read_b128 v[64:67], v28 offset:22528\n" \
"ds_read_b128 v[128:131], v29 offset:22528\n" \
"ds_read_b128 v[68:71], v28 offset:0\n" \
"ds_read_b128 v[132:135], v29 offset:0\n" \
"ds_read_b128 v[72:75], v28 offset:2048\n" \
"ds_read_b128 v[136:139], v29 offset:2048\n" \
"ds_read_b128 v[76:79], v28 offset:4096\n" \
"ds_read_b128 v[140:143], v29 offset:4096\n" \
"ds_read_b128 v[80:83], v28 offset:6144\n" \
"ds_read_b128 v[144:147], v29 offset:6144\n" \
"s_waitcnt lgkmcnt(15)\n" \
"v_mfma_f32_16x16x32_bf16 a[0:3], v[84:87], v[52:55], 0\n" \
"ds_read_b64_tr_b16 v[100:101], v36 offset:0\n" \
"v_mad_i32_i24 v217, v216, -6, v215\n" \
"v_mfma_f32_16x16x32_bf16 a[0:3], v[148:151], v[116:119], a[0:3]\n" \
"v_add_u32_e32 v218, 0xfffffe50, v215\n" \
"s_mul_i32 s35, s6, 0x48\n" \
"v_mfma_f32_16x16x32_bf16 a[4:7], v[88:91], v[52:55], 0\n" \
"v_lshrrev_b32_e32 v219, 1, v217\n" \
"v_add_u32_e64 v218, s35, v216\n" \
"v_mfma_f32_16x16x32_bf16 a[4:7], v[152:155], v[116:119], a[4:7]\n" \
"ds_read_b64_tr_b16 v[102:103], v36 offset:1024\n" \
"v_mul_u32_u24_e32 v219, 0x480, v219\n" \
"v_mfma_f32_16x16x32_bf16 a[8:11], v[92:95], v[52:55], 0\n" \
"v_and_b32_e64 v214, 16, v20\n" \
"v_mad_u32_u24 v219, v218, s5, v219\n" \
"v_mfma_f32_16x16x32_bf16 a[8:11], v[156:159], v[116:119], a[8:11]\n" \
"v_or_b32_e32 v192, v219, v214\n" \
"v_mov_b32_e32 v220, 2.0\n" \
"v_mfma_f32_16x16x32_bf16 a[12:15], v[96:99], v[52:55], 0\n" \
"ds_read_b64_tr_b16 v[164:165], v36 offset:8192\n" \
"v_mov_b32_e32 v222, 7\n" \
"s_waitcnt lgkmcnt(15)\n" \
"v_mfma_f32_16x16x32_bf16 a[12:15], v[160:163], v[116:119], a[12:15]\n" \
"v_lshlrev_b32_e32 v220, 7, v218\n" \
"v_lshrrev_b32_e32 v218, 1, v218\n" \
"v_mfma_f32_16x16x32_bf16 a[44:47], v[96:99], v[56:59], 0\n" \
"v_and_b32_e32 v218, 4, v218\n" \
"v_and_b32_e32 v216, 2, v216\n" \
"v_mfma_f32_16x16x32_bf16 a[44:47], v[160:163], v[120:123], a[44:47]\n" \
"ds_read_b64_tr_b16 v[166:167], v36 offset:9216\n" \
"s_mul_i32 s7, s6, 0x2400\n" \
"v_mfma_f32_16x16x32_bf16 a[40:43], v[92:95], v[56:59], 0\n" \
"s_sub_i32 s34, 0x22000, s7\n" \
"v_bitop3_b32 v216, v217, v218, v216 bitop3:0x1e\n" \
"v_mfma_f32_16x16x32_bf16 a[40:43], v[156:159], v[120:123], a[40:43]\n" \
"v_or_b32_e32 v213, 0x24400, v20\n" \
"v_lshlrev_b32_e64 v216, 4, v216\n" \
"v_mfma_f32_16x16x32_bf16 a[36:39], v[88:91], v[56:59], 0\n" \
"v_add3_u32 v196, v220, s34, v216\n" \
"v_mov_b32_e64 v216, v213\n" \
"v_mfma_f32_16x16x32_bf16 a[36:39], v[152:155], v[120:123], a[36:39]\n" \
"ds_read_b64_tr_b16 v[104:105], v37 offset:0\n" \
"s_movk_i32 s36, 0x1b0\n" \
"v_mfma_f32_16x16x32_bf16 a[32:35], v[84:87], v[56:59], 0\n" \
"v_or_b32_e32 v216, 0x100, v215\n" \
"v_mul_u32_u24_e32 v217, 0x2aab, v216\n" \
"v_mfma_f32_16x16x32_bf16 a[32:35], v[148:151], v[120:123], a[32:35]\n" \
"v_add_u32_e32 v215, 0xffffff50, v215\n" \
"v_lshrrev_b32_e32 v217, 16, v217\n" \
"s_waitcnt lgkmcnt(15)\n" \
"v_mfma_f32_16x16x32_bf16 a[64:67], v[84:87], v[60:63], 0\n" \
"s_mov_b32 m0, s32\n" \
"ds_read_b64_tr_b16 v[106:107], v37 offset:1024\n" \
"v_mfma_f32_16x16x32_bf16 a[64:67], v[148:151], v[124:127], a[64:67]\n" \
"s_mov_b32 exec_lo, 0xff000000\n" \
"s_mov_b32 exec_hi, 0x0\n" \
"global_load_lds_dword v[184:185], off\n" \
"s_mov_b64 exec, -1\n" \
"v_mfma_f32_16x16x32_bf16 a[68:71], v[88:91], v[60:63], 0\n" \
"v_mov_b32_e64 v220, v215\n" \
"v_mad_i32_i24 v218, v217, -6, v216\n" \
"v_mfma_f32_16x16x32_bf16 a[68:71], v[152:155], v[124:127], a[68:71]\n" \
"ds_read_b64_tr_b16 v[168:169], v37 offset:8192\n" \
"v_cmp_gt_u32_e64 vcc, s36, v216\n" \
"v_mfma_f32_16x16x32_bf16 a[72:75], v[92:95], v[60:63], 0\n" \
"v_mov_b32_e32 v221, 7\n" \
"v_lshrrev_b32_e32 v222, 1, v218\n" \
"v_mfma_f32_16x16x32_bf16 a[72:75], v[156:159], v[124:127], a[72:75]\n" \
"s_mov_b32 m0, s32\n" \
"v_cndmask_b32_e32 v217, v220, v217, vcc\n" \
"v_mfma_f32_16x16x32_bf16 a[76:79], v[96:99], v[60:63], 0\n" \
"s_mov_b32 exec_lo, 0xffff\n" \
"s_mov_b32 exec_hi, 0x0\n" \
"global_load_lds_dword v[186:187], off\n" \
"s_mov_b64 exec, -1\n" \
"v_mfma_f32_16x16x32_bf16 a[76:79], v[160:163], v[124:127], a[76:79]\n" \
"ds_read_b64_tr_b16 v[170:171], v37 offset:9216\n" \
"s_movk_i32 s4, 0x48\n" \
"s_waitcnt lgkmcnt(15)\n" \
"v_mfma_f32_16x16x32_bf16 a[108:111], v[96:99], v[64:67], 0\n" \
"v_add_u32_e64 v220, s35, v217\n" \
"v_mul_u32_u24_e32 v222, 0x480, v222\n" \
"v_mfma_f32_16x16x32_bf16 a[108:111], v[160:163], v[128:131], a[108:111]\n" \
"v_mad_u32_u24 v222, v220, s5, v222\n" \
"v_cmp_gt_u32_e64 s[4:5], s4, v215\n" \
"v_mfma_f32_16x16x32_bf16 a[104:107], v[92:95], v[64:67], 0\n" \
"ds_read_b64_tr_b16 v[108:109], v38 offset:0\n" \
"v_cndmask_b32_e64 v215, v221, v218, vcc\n" \
"v_mfma_f32_16x16x32_bf16 a[104:107], v[156:159], v[128:131], a[104:107]\n" \
"v_lshrrev_b32_e32 v218, 1, v220\n" \
"v_or_b32_e32 v214, v222, v214\n" \
"v_mfma_f32_16x16x32_bf16 a[100:103], v[88:91], v[64:67], 0\n" \
"v_mov_b32_e32 v222, 2.0\n" \
"v_and_b32_e32 v218, 4, v218\n" \
"v_mfma_f32_16x16x32_bf16 a[100:103], v[152:155], v[128:131], a[100:103]\n" \
"ds_read_b64_tr_b16 v[110:111], v38 offset:1024\n" \
"v_and_b32_e64 v217, 2, v217\n" \
"v_mfma_f32_16x16x32_bf16 a[96:99], v[84:87], v[64:67], 0\n" \
"v_bitop3_b32 v215, v215, v218, v217 bitop3:0x1e\n" \
"v_lshlrev_b32_e64 v223, 1, v220\n" \
"v_mfma_f32_16x16x32_bf16 a[96:99], v[148:151], v[128:131], a[96:99]\n" \
"v_mov_b32_e32 v224, 2.0\n" \
"v_cndmask_b32_e32 v193, v222, v214, vcc\n" \
"v_mfma_f32_16x16x32_bf16 a[128:131], v[84:87], v[68:71], 0\n" \
"ds_read_b64_tr_b16 v[172:173], v38 offset:8192\n" \
"v_lshlrev_b32_e32 v214, 7, v220\n" \
"s_waitcnt lgkmcnt(15)\n" \
"v_mfma_f32_16x16x32_bf16 a[128:131], v[148:151], v[132:135], a[128:131]\n" \
"v_lshlrev_b32_e32 v215, 4, v215\n" \
"s_mul_i32 s39, s3, 18\n" \
"v_mfma_f32_16x16x32_bf16 a[132:135], v[88:91], v[68:71], 0\n" \
"v_add3_u32 v214, v214, s34, v215\n" \
"v_cndmask_b32_e64 v195, v224, v223, s[4:5]\n" \
"v_mfma_f32_16x16x32_bf16 a[132:135], v[152:155], v[132:135], a[132:135]\n" \
"ds_read_b64_tr_b16 v[174:175], v38 offset:9216\n" \
"s_movk_i32 s4, 0x1f8\n" \
"v_mfma_f32_16x16x32_bf16 a[136:139], v[92:95], v[68:71], 0\n" \
"v_mov_b32_e32 v215, v213\n" \
"s_add_i32 s35, s35, s39\n" \
"v_mfma_f32_16x16x32_bf16 a[136:139], v[156:159], v[132:135], a[136:139]\n" \
"v_cmp_gt_u32_e32 vcc, s4, v216\n" \
"v_add_u32_e64 v212, s35, v212\n" \
"v_mfma_f32_16x16x32_bf16 a[140:143], v[96:99], v[68:71], 0\n" \
"v_mov_b32_e32 v219, 2.0\n" \
"v_cndmask_b32_e32 v197, v215, v214, vcc\n" \
"v_mfma_f32_16x16x32_bf16 a[140:143], v[160:163], v[132:135], a[140:143]\n" \
"ds_read_b64_tr_b16 v[112:113], v39 offset:0\n" \
"s_movk_i32 s35, 0x120\n" \
"v_mfma_f32_16x16x32_bf16 a[172:175], v[96:99], v[72:75], 0\n" \
"v_mul_u32_u24_e32 v214, 0xd80, v212\n" \
"v_cmp_gt_u32_e32 vcc, s35, v20\n" \
"v_mfma_f32_16x16x32_bf16 a[172:175], v[160:163], v[136:139], a[172:175]\n" \
"v_lshrrev_b32_e32 v215, 1, v212\n" \
"s_and_b32 s4, s2, 7\n" \
"v_mfma_f32_16x16x32_bf16 a[168:171], v[92:95], v[72:75], 0\n" \
"ds_read_b64_tr_b16 v[114:115], v39 offset:1024\n" \
"v_cndmask_b32_e64 v194, v219, v214, vcc\n" \
"v_mfma_f32_16x16x32_bf16 a[168:171], v[156:159], v[136:139], a[168:171]\n" \
"v_lshlrev_b32_e32 v214, 7, v212\n" \
"v_lshrrev_b32_e32 v212, 2, v212\n" \
"v_mfma_f32_16x16x32_bf16 a[164:167], v[88:91], v[72:75], 0\n" \
"v_and_b32_e64 v212, 2, v212\n" \
"v_and_or_b32 v212, v215, 1, v212\n" \
"v_mfma_f32_16x16x32_bf16 a[164:167], v[152:155], v[136:139], a[164:167]\n" \
"ds_read_b64_tr_b16 v[176:177], v39 offset:8192\n" \
"s_lshl_b32 s5, s4, 2\n" \
"v_mfma_f32_16x16x32_bf16 a[160:163], v[84:87], v[72:75], 0\n" \
"s_bfe_u32 s38, s2, 0x20003\n" \
"v_lshlrev_b32_e32 v212, 5, v212\n" \
"v_mfma_f32_16x16x32_bf16 a[160:163], v[148:151], v[136:139], a[160:163]\n" \
"s_movk_i32 s35, 0x60\n" \
"s_or_b32 s5, s5, s38\n" \
"s_waitcnt lgkmcnt(15)\n" \
"v_mfma_f32_16x16x32_bf16 a[192:195], v[84:87], v[76:79], 0\n" \
"ds_read_b64_tr_b16 v[178:179], v39 offset:9216\n" \
"v_bitop3_b32 v212, v212, s35, v214 bitop3:0x36\n" \
"v_mfma_f32_16x16x32_bf16 a[192:195], v[148:151], v[140:143], a[192:195]\n" \
"s_lshr_b32 s36, s2, 3\n" \
"v_add_u32_e64 v212, s34, v212\n" \
"v_mfma_f32_16x16x32_bf16 a[196:199], v[88:91], v[76:79], 0\n" \
"s_mul_i32 s34, s5, 0x12000\n" \
"s_or_b32 s40, s34, 0x6000000\n" \
"v_mfma_f32_16x16x32_bf16 a[196:199], v[152:155], v[140:143], a[196:199]\n" \
"s_mul_i32 s34, s36, 0x90000\n" \
"s_lshl_b32 s36, s3, 14\n" \
"v_mfma_f32_16x16x32_bf16 a[200:203], v[92:95], v[76:79], 0\n" \
"s_add_i32 s34, s34, s36\n" \
"s_lshl_b32 s38, s5, 8\n" \
"v_mfma_f32_16x16x32_bf16 a[200:203], v[156:159], v[140:143], a[200:203]\n" \
"s_add_i32 s41, s34, 0x4240000\n" \
"s_mul_i32 s34, s5, 36\n" \
"v_mfma_f32_16x16x32_bf16 a[204:207], v[96:99], v[76:79], 0\n" \
"s_add_i32 s5, s7, s40\n" \
"s_addk_i32 s5, 0x2000\n" \
"v_mfma_f32_16x16x32_bf16 a[204:207], v[160:163], v[140:143], a[204:207]\n" \
"s_mov_b32 s37, 2.0\n" \
"s_or_b32 s38, s38, 0x5be8000\n" \
"v_mfma_f32_16x16x32_bf16 a[236:239], v[96:99], v[80:83], 0\n" \
"s_cmp_lg_u32 s3, 0\n" \
"s_cselect_b32 s36, s37, s5\n" \
"v_mfma_f32_16x16x32_bf16 a[236:239], v[160:163], v[144:147], a[236:239]\n" \
"s_lshl_b32 s5, s6, 2\n" \
"s_or_b32 s5, s38, s5\n" \
"v_mfma_f32_16x16x32_bf16 a[232:235], v[92:95], v[80:83], 0\n" \
"s_lshl_b32 s35, s3, 10\n" \
"v_mfma_f32_16x16x32_bf16 a[232:235], v[156:159], v[144:147], a[232:235]\n" \
"s_cmpk_lt_u32 s3, 3\n" \
"s_cselect_b32 s37, s37, s5\n" \
"v_mfma_f32_16x16x32_bf16 a[228:231], v[88:91], v[80:83], 0\n" \
"s_lshl_b32 s5, s4, 16\n" \
"s_lshl_b32 s4, s4, 11\n" \
"v_mfma_f32_16x16x32_bf16 a[228:231], v[152:155], v[144:147], a[228:231]\n" \
"v_cndmask_b32_e32 v198, v213, v212, vcc\n" \
"v_or_b32_e32 v212, s35, v20\n" \
"v_mfma_f32_16x16x32_bf16 a[224:227], v[84:87], v[80:83], 0\n" \
"s_or_b32 s39, s40, s35\n" \
"s_or_b32 s55, s41, s4\n" \
"v_mfma_f32_16x16x32_bf16 a[224:227], v[148:151], v[144:147], a[224:227]\n" \
"s_waitcnt vmcnt(4) lgkmcnt(0)\n" \
"s_barrier\n" \
"v_mfma_f32_16x16x32_bf16 a[16:19], v[100:103], v[52:55], 0\n" \
"s_add_u32 m0, s30, 0x0\n" \
"s_nop 0\n" \
"s_mov_b32 s85, 0x100\n" \
"v_mfma_f32_16x16x32_bf16 a[16:19], v[164:167], v[116:119], a[16:19]\n" \
"buffer_load_dwordx4 v22, s[64:67], s85 offen lds\n" \
"v_mfma_f32_16x16x32_bf16 a[20:23], v[104:107], v[52:55], 0\n" \
"ds_read_b64_tr_b16 v[84:85], v32 offset:32768\n" \
"v_or_b32_e32 v199, 0x22000, v212\n" \
"v_mfma_f32_16x16x32_bf16 a[20:23], v[168:171], v[116:119], a[20:23]\n" \
"s_add_u32 m0, s30, 0x400\n" \
"s_mov_b32 s84, 0x2100\n" \
"v_mfma_f32_16x16x32_bf16 a[24:27], v[108:111], v[52:55], 0\n" \
"buffer_load_dwordx4 v23, s[64:67], s84 offen lds\n" \
"v_mfma_f32_16x16x32_bf16 a[24:27], v[172:175], v[116:119], a[24:27]\n" \
"ds_read_b64_tr_b16 v[86:87], v32 offset:33792\n" \
"s_add_i32 s35, s39, s7\n" \
"v_mfma_f32_16x16x32_bf16 a[28:31], v[112:115], v[52:55], 0\n" \
"ds_read_b64_tr_b16 v[148:149], v32 offset:40960\n" \
"s_add_i32 s54, s41, s5\n" \
"v_mfma_f32_16x16x32_bf16 a[28:31], v[176:179], v[116:119], a[28:31]\n" \
"ds_read_b128 v[52:55], v28 offset:49152\n" \
"s_add_i32 s55, s55, 0x80000\n" \
"v_mfma_f32_16x16x32_bf16 a[60:63], v[112:115], v[56:59], 0\n" \
"ds_read_b128 v[116:119], v29 offset:49152\n" \
"v_mov_b32_e32 v213, 0xffff\n" \
"v_mfma_f32_16x16x32_bf16 a[60:63], v[176:179], v[120:123], a[60:63]\n" \
"s_add_u32 m0, s31, 0x0\n" \
"s_nop 0\n" \
"s_mov_b32 s85, 0x100000\n" \
"v_mfma_f32_16x16x32_bf16 a[56:59], v[108:111], v[56:59], 0\n" \
"buffer_load_dwordx4 v26, s[68:71], s85 offen lds\n" \
"v_mfma_f32_16x16x32_bf16 a[56:59], v[172:175], v[120:123], a[56:59]\n" \
"s_add_u32 m0, s31, 0x4000\n" \
"s_mov_b32 s84, 0x100000\n" \
"v_mfma_f32_16x16x32_bf16 a[52:55], v[104:107], v[56:59], 0\n" \
"buffer_load_dwordx4 v26, s[72:75], s84 offen lds\n" \
"v_mfma_f32_16x16x32_bf16 a[52:55], v[168:171], v[120:123], a[52:55]\n" \
"s_add_u32 m0, s30, 0x800\n" \
"s_mov_b32 s85, 0x4100\n" \
"v_mfma_f32_16x16x32_bf16 a[48:51], v[100:103], v[56:59], 0\n" \
"buffer_load_dwordx4 v22, s[64:67], s85 offen lds\n" \
"v_mfma_f32_16x16x32_bf16 a[48:51], v[164:167], v[120:123], a[48:51]\n" \
"ds_read_b128 v[56:59], v28 offset:51200\n" \
"s_and_b32 s6, s3, 1\n" \
"v_mfma_f32_16x16x32_bf16 a[80:83], v[100:103], v[60:63], 0\n" \
"ds_read_b128 v[120:123], v29 offset:51200\n" \
"s_lshl_b32 s4, s6, 4\n" \
"v_mfma_f32_16x16x32_bf16 a[80:83], v[164:167], v[124:127], a[80:83]\n" \
"s_add_u32 m0, s30, 0xc00\n" \
"s_mov_b32 s84, 0x6100\n" \
"v_mfma_f32_16x16x32_bf16 a[84:87], v[104:107], v[60:63], 0\n" \
"buffer_load_dwordx4 v23, s[64:67], s84 offen lds\n" \
"v_mfma_f32_16x16x32_bf16 a[84:87], v[168:171], v[124:127], a[84:87]\n" \
"ds_read_b64_tr_b16 v[150:151], v32 offset:41984\n" \
"v_lshrrev_b32_e64 v214, 4, v20\n" \
"v_mfma_f32_16x16x32_bf16 a[88:91], v[108:111], v[60:63], 0\n" \
"s_add_u32 m0, s31, 0x400\n" \
"s_mov_b32 s85, 0x108000\n" \
"v_mfma_f32_16x16x32_bf16 a[88:91], v[172:175], v[124:127], a[88:91]\n" \
"buffer_load_dwordx4 v26, s[68:71], s85 offen lds\n" \
"v_mfma_f32_16x16x32_bf16 a[92:95], v[112:115], v[60:63], 0\n" \
"ds_read_b64_tr_b16 v[88:89], v33 offset:32768\n" \
"v_lshrrev_b32_e64 v215, 6, v20\n" \
"v_mfma_f32_16x16x32_bf16 a[92:95], v[176:179], v[124:127], a[92:95]\n" \
"ds_read_b128 v[60:63], v28 offset:53248\n" \
"v_and_b32_e64 v216, 12, v215\n" \
"v_mfma_f32_16x16x32_bf16 a[124:127], v[112:115], v[64:67], 0\n" \
"ds_read_b128 v[124:127], v29 offset:53248\n" \
"v_and_or_b32 v212, v214, 15, s4\n" \
"v_mfma_f32_16x16x32_bf16 a[124:127], v[176:179], v[128:131], a[124:127]\n" \
"s_add_u32 m0, s31, 0x4400\n" \
"s_mov_b32 s84, 0x108000\n" \
"v_mfma_f32_16x16x32_bf16 a[120:123], v[108:111], v[64:67], 0\n" \
"buffer_load_dwordx4 v26, s[72:75], s84 offen lds\n" \
"v_mfma_f32_16x16x32_bf16 a[120:123], v[172:175], v[128:131], a[120:123]\n" \
"s_add_u32 m0, s30, 0x1000\n" \
"s_mov_b32 s85, 0x8100\n" \
"v_mfma_f32_16x16x32_bf16 a[116:119], v[104:107], v[64:67], 0\n" \
"buffer_load_dwordx4 v22, s[64:67], s85 offen lds\n" \
"v_mfma_f32_16x16x32_bf16 a[116:119], v[168:171], v[128:131], a[116:119]\n" \
"ds_read_b64_tr_b16 v[90:91], v33 offset:33792\n" \
"v_cmp_gt_u32_e64 vcc, v216, v212\n" \
"v_mfma_f32_16x16x32_bf16 a[112:115], v[100:103], v[64:67], 0\n" \
"ds_read_b64_tr_b16 v[152:153], v33 offset:40960\n" \
"v_mov_b32_e32 v218, 0xffff0000\n" \
"v_mfma_f32_16x16x32_bf16 a[112:115], v[164:167], v[128:131], a[112:115]\n" \
"ds_read_b128 v[64:67], v28 offset:55296\n" \
"s_lshl_b32 s4, s2, 2\n" \
"s_nop 0\n" \
"v_mfma_f32_16x16x32_bf16 a[144:147], v[100:103], v[68:71], 0\n" \
"ds_read_b128 v[128:131], v29 offset:55296\n" \
"v_cndmask_b32_e64 v217, v213, 0, vcc\n" \
"v_mfma_f32_16x16x32_bf16 a[144:147], v[164:167], v[132:135], a[144:147]\n" \
"s_add_u32 m0, s30, 0x1400\n" \
"s_mov_b32 s84, 0xa100\n" \
"v_mfma_f32_16x16x32_bf16 a[148:151], v[104:107], v[68:71], 0\n" \
"buffer_load_dwordx4 v23, s[64:67], s84 offen lds\n" \
"v_mfma_f32_16x16x32_bf16 a[148:151], v[168:171], v[132:135], a[148:151]\n" \
"s_add_u32 m0, s31, 0x800\n" \
"s_mov_b32 s85, 0x110000\n" \
"v_mfma_f32_16x16x32_bf16 a[152:155], v[108:111], v[68:71], 0\n" \
"buffer_load_dwordx4 v27, s[68:71], s85 offen lds\n" \
"v_mfma_f32_16x16x32_bf16 a[152:155], v[172:175], v[132:135], a[152:155]\n" \
"s_add_u32 m0, s31, 0x4800\n" \
"s_mov_b32 s84, 0x110000\n" \
"v_mfma_f32_16x16x32_bf16 a[156:159], v[112:115], v[68:71], 0\n" \
"buffer_load_dwordx4 v27, s[72:75], s84 offen lds\n" \
"v_mfma_f32_16x16x32_bf16 a[156:159], v[176:179], v[132:135], a[156:159]\n" \
"ds_read_b128 v[68:71], v28 offset:32768\n" \
"v_cmp_lt_u32_e64 vcc, v216, v212\n" \
"v_mfma_f32_16x16x32_bf16 a[188:191], v[112:115], v[72:75], 0\n" \
"ds_read_b128 v[132:135], v29 offset:32768\n" \
"s_mov_b32 s7, 0\n" \
"v_mfma_f32_16x16x32_bf16 a[188:191], v[176:179], v[136:139], a[188:191]\n" \
"s_add_u32 m0, s30, 0x1800\n" \
"s_mov_b32 s85, 0xc100\n" \
"v_mfma_f32_16x16x32_bf16 a[184:187], v[108:111], v[72:75], 0\n" \
"buffer_load_dwordx4 v22, s[64:67], s85 offen lds\n" \
"v_mfma_f32_16x16x32_bf16 a[184:187], v[172:175], v[136:139], a[184:187]\n" \
"ds_read_b64_tr_b16 v[154:155], v33 offset:41984\n" \
"s_or_b32 s40, s4, s3\n" \
"v_mfma_f32_16x16x32_bf16 a[180:183], v[104:107], v[72:75], 0\n" \
"s_add_u32 m0, s30, 0x1c00\n" \
"s_mov_b32 s84, 0xe100\n" \
"v_mfma_f32_16x16x32_bf16 a[180:183], v[168:171], v[136:139], a[180:183]\n" \
"buffer_load_dwordx4 v23, s[64:67], s84 offen lds\n" \
"v_mfma_f32_16x16x32_bf16 a[176:179], v[100:103], v[72:75], 0\n" \
"ds_read_b64_tr_b16 v[92:93], v34 offset:32768\n" \
"v_cndmask_b32_e64 v219, 0, v218, vcc\n" \
"v_mfma_f32_16x16x32_bf16 a[176:179], v[164:167], v[136:139], a[176:179]\n" \
"ds_read_b128 v[72:75], v28 offset:34816\n" \
"v_or_b32_e64 v12, v217, v219\n" \
"v_mfma_f32_16x16x32_bf16 a[208:211], v[100:103], v[76:79], 0\n" \
"ds_read_b128 v[136:139], v29 offset:34816\n" \
"v_or_b32_e64 v217, 2, v216\n" \
"v_mfma_f32_16x16x32_bf16 a[208:211], v[164:167], v[140:143], a[208:211]\n" \
"ds_read_b64_tr_b16 v[94:95], v34 offset:33792\n" \
"v_cmp_gt_u32_e64 vcc, v217, v212\n" \
"v_mfma_f32_16x16x32_bf16 a[212:215], v[104:107], v[76:79], 0\n" \
"ds_read_b64_tr_b16 v[156:157], v34 offset:40960\n" \
"s_add_i32 s41, s40, 0xffffffaf\n" \
"v_mfma_f32_16x16x32_bf16 a[212:215], v[168:171], v[140:143], a[212:215]\n" \
"ds_read_b64_tr_b16 v[158:159], v34 offset:41984\n" \
"v_mfma_f32_16x16x32_bf16 a[216:219], v[108:111], v[76:79], 0\n" \
"ds_read_b64_tr_b16 v[96:97], v35 offset:32768\n" \
"v_mfma_f32_16x16x32_bf16 a[216:219], v[172:175], v[140:143], a[216:219]\n" \
"ds_read_b64_tr_b16 v[98:99], v35 offset:33792\n" \
"v_mfma_f32_16x16x32_bf16 a[220:223], v[112:115], v[76:79], 0\n" \
"ds_read_b64_tr_b16 v[160:161], v35 offset:40960\n" \
"v_mfma_f32_16x16x32_bf16 a[220:223], v[176:179], v[140:143], a[220:223]\n" \
"ds_read_b128 v[76:79], v28 offset:36864\n" \
"v_mfma_f32_16x16x32_bf16 a[252:255], v[112:115], v[80:83], 0\n" \
"ds_read_b128 v[140:143], v29 offset:36864\n" \
"v_mfma_f32_16x16x32_bf16 a[252:255], v[176:179], v[144:147], a[252:255]\n" \
"ds_read_b64_tr_b16 v[162:163], v35 offset:41984\n" \
"v_mfma_f32_16x16x32_bf16 a[248:251], v[108:111], v[80:83], 0\n" \
"s_cmpk_lt_u32 s40, 0x291\n" \
"s_cselect_b32 s7, s41, s7\n" \
"v_mfma_f32_16x16x32_bf16 a[248:251], v[172:175], v[144:147], a[248:251]\n" \
"v_cndmask_b32_e64 v219, v213, 0, vcc\n" \
"v_cmp_lt_u32_e64 vcc, v217, v212\n" \
"v_mfma_f32_16x16x32_bf16 a[244:247], v[104:107], v[80:83], 0\n" \
"s_cmpk_lt_u32 s40, 0x51\n" \
"s_cselect_b32 s7, s40, s7\n" \
"v_mfma_f32_16x16x32_bf16 a[244:247], v[168:171], v[144:147], a[244:247]\n" \
"s_cmpk_lt_u32 s40, 0x51\n" \
"s_cselect_b32 s5, s23, s25\n" \
"v_mfma_f32_16x16x32_bf16 a[240:243], v[100:103], v[80:83], 0\n" \
"v_cndmask_b32_e32 v217, 0, v218, vcc\n" \
"v_or_b32_e32 v13, v219, v217\n" \
"v_mfma_f32_16x16x32_bf16 a[240:243], v[164:167], v[144:147], a[240:243]\n" \
"ds_read_b128 v[80:83], v28 offset:38912\n" \
"ds_read_b128 v[144:147], v29 offset:38912\n" \
"s_waitcnt lgkmcnt(15)\n" \
"v_mfma_f32_16x16x32_bf16 a[0:3], v[84:87], v[52:55], a[0:3]\n" \
"s_add_u32 m0, s31, 0xc00\n" \
"s_mov_b32 s85, 0x118000\n" \
"v_mfma_f32_16x16x32_bf16 a[0:3], v[148:151], v[116:119], a[0:3]\n" \
"buffer_load_dwordx4 v27, s[68:71], s85 offen lds\n" \
"v_mfma_f32_16x16x32_bf16 a[4:7], v[88:91], v[52:55], a[4:7]\n" \
"ds_read_b64_tr_b16 v[100:101], v36 offset:32768\n" \
"v_or_b32_e32 v217, 16, v216\n" \
"s_waitcnt lgkmcnt(15)\n" \
"v_mfma_f32_16x16x32_bf16 a[4:7], v[152:155], v[116:119], a[4:7]\n" \
"s_add_u32 m0, s31, 0x4c00\n" \
"s_mov_b32 s84, 0x118000\n" \
"s_waitcnt lgkmcnt(11)\n" \
"v_mfma_f32_16x16x32_bf16 a[8:11], v[92:95], v[52:55], a[8:11]\n" \
"buffer_load_dwordx4 v27, s[72:75], s84 offen lds\n" \
"s_waitcnt lgkmcnt(9)\n" \
"v_mfma_f32_16x16x32_bf16 a[8:11], v[156:159], v[116:119], a[8:11]\n" \
"ds_read_b64_tr_b16 v[102:103], v36 offset:33792\n" \
"v_cmp_gt_u32_e32 vcc, v217, v212\n" \
"s_waitcnt lgkmcnt(8)\n" \
"v_mfma_f32_16x16x32_bf16 a[12:15], v[96:99], v[52:55], a[12:15]\n" \
"ds_read_b64_tr_b16 v[164:165], v36 offset:40960\n" \
"s_waitcnt lgkmcnt(5)\n" \
"v_mfma_f32_16x16x32_bf16 a[12:15], v[160:163], v[116:119], a[12:15]\n" \
"s_cmpk_lt_u32 s40, 0x51\n" \
"s_cselect_b32 s4, s22, s24\n" \
"v_mfma_f32_16x16x32_bf16 a[44:47], v[96:99], v[56:59], a[44:47]\n" \
"s_movk_i32 s40, 0xf400\n" \
"v_cndmask_b32_e64 v219, v213, 0, vcc\n" \
"v_mfma_f32_16x16x32_bf16 a[44:47], v[160:163], v[120:123], a[44:47]\n" \
"ds_read_b64_tr_b16 v[166:167], v36 offset:41984\n" \
"v_cmp_lt_u32_e32 vcc, v217, v212\n" \
"v_mfma_f32_16x16x32_bf16 a[40:43], v[92:95], v[56:59], a[40:43]\n" \
"s_nop 0\n" \
"v_cndmask_b32_e32 v217, 0, v218, vcc\n" \
"v_or_b32_e64 v14, v219, v217\n" \
"v_mfma_f32_16x16x32_bf16 a[40:43], v[156:159], v[120:123], a[40:43]\n" \
"v_or_b32_e32 v217, 18, v216\n" \
"v_cmp_gt_u32_e32 vcc, v217, v212\n" \
"v_mfma_f32_16x16x32_bf16 a[36:39], v[88:91], v[56:59], a[36:39]\n" \
"s_nop 0\n" \
"v_cndmask_b32_e64 v213, v213, 0, vcc\n" \
"v_cmp_lt_u32_e32 vcc, v217, v212\n" \
"v_mfma_f32_16x16x32_bf16 a[36:39], v[152:155], v[120:123], a[36:39]\n" \
"ds_read_b64_tr_b16 v[104:105], v37 offset:32768\n" \
"v_cndmask_b32_e64 v212, 0, v218, vcc\n" \
"v_mfma_f32_16x16x32_bf16 a[32:35], v[84:87], v[56:59], a[32:35]\n" \
"v_or_b32_e32 v15, v213, v212\n" \
"v_lshlrev_b32_e32 v212, 7, v214\n" \
"v_mfma_f32_16x16x32_bf16 a[32:35], v[148:151], v[120:123], a[32:35]\n" \
"v_lshl_or_b32 v212, s7, 13, v212\n" \
"v_mov_b32_e64 v213, 0\n" \
"v_mfma_f32_16x16x32_bf16 a[64:67], v[84:87], v[60:63], a[64:67]\n" \
"s_mov_b32 m0, s32\n" \
"ds_read_b64_tr_b16 v[106:107], v37 offset:33792\n" \
"v_mfma_f32_16x16x32_bf16 a[64:67], v[148:151], v[124:127], a[64:67]\n" \
"s_mov_b32 exec_lo, 0x0\n" \
"s_mov_b32 exec_hi, 0xff\n" \
"global_load_lds_dword v[184:185], off\n" \
"s_mov_b64 exec, -1\n" \
"v_mfma_f32_16x16x32_bf16 a[68:71], v[88:91], v[60:63], a[68:71]\n" \
"v_lshl_add_u64 v[16:17], s[4:5], 0, v[212:213]\n" \
"v_mul_u32_u24_e32 v212, 0xe39, v214\n" \
"v_mfma_f32_16x16x32_bf16 a[68:71], v[152:155], v[124:127], a[68:71]\n" \
"ds_read_b64_tr_b16 v[168:169], v37 offset:40960\n" \
"v_lshrrev_b32_e32 v212, 16, v212\n" \
"v_mfma_f32_16x16x32_bf16 a[72:75], v[92:95], v[60:63], a[72:75]\n" \
"s_movk_i32 s5, 0xffee\n" \
"s_lshl_b32 s4, s2, 5\n" \
"v_mfma_f32_16x16x32_bf16 a[72:75], v[156:159], v[124:127], a[72:75]\n" \
"s_mov_b32 m0, s32\n" \
"v_mad_i32_i24 v213, v212, s5, v214\n" \
"v_mfma_f32_16x16x32_bf16 a[76:79], v[96:99], v[60:63], a[76:79]\n" \
"s_mov_b32 exec_lo, 0xffff0000\n" \
"s_mov_b32 exec_hi, 0x0\n" \
"global_load_lds_dword v[186:187], off\n" \
"s_mov_b64 exec, -1\n" \
"v_mfma_f32_16x16x32_bf16 a[76:79], v[160:163], v[124:127], a[76:79]\n" \
"ds_read_b64_tr_b16 v[170:171], v37 offset:41984\n" \
"s_add_i32 s4, s4, 32\n" \
"v_mfma_f32_16x16x32_bf16 a[108:111], v[96:99], v[64:67], a[108:111]\n" \
"v_lshlrev_b32_e32 v217, 4, v213\n" \
"v_add_u32_e32 v218, s4, v217\n" \
"v_mfma_f32_16x16x32_bf16 a[108:111], v[160:163], v[128:131], a[108:111]\n" \
"v_and_b32_e32 v218, 0xf0, v218\n" \
"v_cmp_gt_u32_e64 vcc, 16, v213\n" \
"v_mfma_f32_16x16x32_bf16 a[104:107], v[92:95], v[64:67], a[104:107]\n" \
"ds_read_b64_tr_b16 v[108:109], v38 offset:32768\n" \
"s_movk_i32 s7, 0xf800\n" \
"v_mfma_f32_16x16x32_bf16 a[104:107], v[156:159], v[128:131], a[104:107]\n" \
"v_cndmask_b32_e32 v213, v217, v218, vcc\n" \
"v_or_b32_e64 v217, 64, v214\n" \
"v_mfma_f32_16x16x32_bf16 a[100:103], v[88:91], v[64:67], a[100:103]\n" \
"v_mul_u32_u24_e32 v218, 0xe39, v217\n" \
"v_lshrrev_b32_e64 v218, 16, v218\n" \
"v_mfma_f32_16x16x32_bf16 a[100:103], v[152:155], v[128:131], a[100:103]\n" \
"ds_read_b64_tr_b16 v[110:111], v38 offset:33792\n" \
"v_mad_i32_i24 v217, v218, s5, v217\n" \
"v_mfma_f32_16x16x32_bf16 a[96:99], v[84:87], v[64:67], a[96:99]\n" \
"v_lshlrev_b32_e32 v219, 4, v217\n" \
"v_add_u32_e32 v220, s4, v219\n" \
"v_mfma_f32_16x16x32_bf16 a[96:99], v[148:151], v[128:131], a[96:99]\n" \
"v_and_b32_e32 v220, 0xf0, v220\n" \
"v_cmp_gt_u32_e64 vcc, 16, v217\n" \
"v_mfma_f32_16x16x32_bf16 a[128:131], v[84:87], v[68:71], a[128:131]\n" \
"ds_read_b64_tr_b16 v[172:173], v38 offset:40960\n" \
"v_cndmask_b32_e64 v217, v219, v220, vcc\n" \
"v_mfma_f32_16x16x32_bf16 a[128:131], v[148:151], v[132:135], a[128:131]\n" \
"v_or_b32_e32 v219, 0x80, v214\n" \
"v_mul_u32_u24_e32 v220, 0xe39, v219\n" \
"v_mfma_f32_16x16x32_bf16 a[132:135], v[88:91], v[68:71], a[132:135]\n" \
"v_lshrrev_b32_e64 v220, 16, v220\n" \
"v_mad_i32_i24 v219, v220, s5, v219\n" \
"v_mfma_f32_16x16x32_bf16 a[132:135], v[152:155], v[132:135], a[132:135]\n" \
"ds_read_b64_tr_b16 v[174:175], v38 offset:41984\n" \
"v_lshlrev_b32_e64 v221, 4, v219\n" \
"v_mfma_f32_16x16x32_bf16 a[136:139], v[92:95], v[68:71], a[136:139]\n" \
"v_add_u32_e64 v222, s4, v221\n" \
"v_and_b32_e32 v222, 0xf0, v222\n" \
"v_mfma_f32_16x16x32_bf16 a[136:139], v[156:159], v[132:135], a[136:139]\n" \
"v_cmp_gt_u32_e32 vcc, 16, v219\n" \
"s_nop 1\n" \
"v_cndmask_b32_e64 v219, v221, v222, vcc\n" \
"v_mfma_f32_16x16x32_bf16 a[140:143], v[96:99], v[68:71], a[140:143]\n" \
"v_or_b32_e32 v221, 0xc0, v214\n" \
"v_mul_u32_u24_e32 v222, 0xe39, v221\n" \
"v_mfma_f32_16x16x32_bf16 a[140:143], v[160:163], v[132:135], a[140:143]\n" \
"ds_read_b64_tr_b16 v[112:113], v39 offset:32768\n" \
"v_lshrrev_b32_e64 v222, 16, v222\n" \
"v_mfma_f32_16x16x32_bf16 a[172:175], v[96:99], v[72:75], a[172:175]\n" \
"v_mad_i32_i24 v221, v222, s5, v221\n" \
"v_lshlrev_b32_e64 v223, 4, v221\n" \
"v_mfma_f32_16x16x32_bf16 a[172:175], v[160:163], v[136:139], a[172:175]\n" \
"v_add_u32_e64 v224, s4, v223\n" \
"v_and_b32_e32 v224, 0xf0, v224\n" \
"v_mfma_f32_16x16x32_bf16 a[168:171], v[92:95], v[72:75], a[168:171]\n" \
"ds_read_b64_tr_b16 v[114:115], v39 offset:33792\n" \
"v_cmp_gt_u32_e64 vcc, 16, v221\n" \
"v_mfma_f32_16x16x32_bf16 a[168:171], v[156:159], v[136:139], a[168:171]\n" \
"s_nop 0\n" \
"v_cndmask_b32_e32 v221, v223, v224, vcc\n" \
"v_or_b32_e32 v223, 0x100, v214\n" \
"v_mfma_f32_16x16x32_bf16 a[164:167], v[88:91], v[72:75], a[164:167]\n" \
"v_mul_u32_u24_e32 v224, 0xe39, v223\n" \
"v_lshrrev_b32_e64 v224, 16, v224\n" \
"v_mfma_f32_16x16x32_bf16 a[164:167], v[152:155], v[136:139], a[164:167]\n" \
"ds_read_b64_tr_b16 v[176:177], v39 offset:40960\n" \
"v_mad_i32_i24 v223, v224, s5, v223\n" \
"v_mfma_f32_16x16x32_bf16 a[160:163], v[84:87], v[72:75], a[160:163]\n" \
"v_lshlrev_b32_e32 v225, 4, v223\n" \
"v_add_u32_e32 v226, s4, v225\n" \
"v_mfma_f32_16x16x32_bf16 a[160:163], v[148:151], v[136:139], a[160:163]\n" \
"v_and_b32_e32 v226, 0xf0, v226\n" \
"v_cmp_gt_u32_e64 vcc, 16, v223\n" \
"v_mfma_f32_16x16x32_bf16 a[192:195], v[84:87], v[76:79], a[192:195]\n" \
"ds_read_b64_tr_b16 v[178:179], v39 offset:41984\n" \
"v_cndmask_b32_e64 v223, v225, v226, vcc\n" \
"v_mfma_f32_16x16x32_bf16 a[192:195], v[148:151], v[140:143], a[192:195]\n" \
"v_or_b32_e32 v225, 0x140, v214\n" \
"v_mul_u32_u24_e32 v226, 0xe39, v225\n" \
"v_mfma_f32_16x16x32_bf16 a[196:199], v[88:91], v[76:79], a[196:199]\n" \
"v_lshrrev_b32_e64 v226, 16, v226\n" \
"v_mad_i32_i24 v225, v226, s5, v225\n" \
"v_mfma_f32_16x16x32_bf16 a[196:199], v[152:155], v[140:143], a[196:199]\n" \
"v_lshlrev_b32_e32 v227, 4, v225\n" \
"v_add_u32_e32 v228, s4, v227\n" \
"v_mfma_f32_16x16x32_bf16 a[200:203], v[92:95], v[76:79], a[200:203]\n" \
"v_and_b32_e32 v228, 0xf0, v228\n" \
"v_cmp_gt_u32_e64 vcc, 16, v225\n" \
"v_mfma_f32_16x16x32_bf16 a[200:203], v[156:159], v[140:143], a[200:203]\n" \
"s_nop 0\n" \
"v_cndmask_b32_e32 v225, v227, v228, vcc\n" \
"v_or_b32_e32 v227, 0x180, v214\n" \
"v_mfma_f32_16x16x32_bf16 a[204:207], v[96:99], v[76:79], a[204:207]\n" \
"v_mul_u32_u24_e32 v228, 0xe39, v227\n" \
"v_lshrrev_b32_e64 v228, 16, v228\n" \
"v_mfma_f32_16x16x32_bf16 a[204:207], v[160:163], v[140:143], a[204:207]\n" \
"v_mad_i32_i24 v227, v228, s5, v227\n" \
"v_lshlrev_b32_e32 v229, 4, v227\n" \
"s_waitcnt lgkmcnt(15)\n" \
"v_mfma_f32_16x16x32_bf16 a[236:239], v[96:99], v[80:83], a[236:239]\n" \
"v_add_u32_e64 v230, s4, v229\n" \
"v_and_b32_e32 v230, 0xf0, v230\n" \
"v_mfma_f32_16x16x32_bf16 a[236:239], v[160:163], v[144:147], a[236:239]\n" \
"v_cmp_gt_u32_e32 vcc, 16, v227\n" \
"s_nop 1\n" \
"v_cndmask_b32_e64 v227, v229, v230, vcc\n" \
"v_mfma_f32_16x16x32_bf16 a[232:235], v[92:95], v[80:83], a[232:235]\n" \
"v_or_b32_e32 v229, 0x1c0, v214\n" \
"v_mul_u32_u24_e32 v230, 0xe39, v229\n" \
"v_mfma_f32_16x16x32_bf16 a[232:235], v[156:159], v[144:147], a[232:235]\n" \
"v_lshrrev_b32_e64 v230, 16, v230\n" \
"v_mad_i32_i24 v229, v230, s5, v229\n" \
"v_mfma_f32_16x16x32_bf16 a[228:231], v[88:91], v[80:83], a[228:231]\n" \
"v_lshlrev_b32_e32 v231, 4, v229\n" \
"v_add_u32_e32 v232, s4, v231\n" \
"v_mfma_f32_16x16x32_bf16 a[228:231], v[152:155], v[144:147], a[228:231]\n" \
"v_and_b32_e32 v232, 0xf0, v232\n" \
"v_cmp_gt_u32_e64 vcc, 16, v229\n" \
"v_mfma_f32_16x16x32_bf16 a[224:227], v[84:87], v[80:83], a[224:227]\n" \
"s_nop 0\n" \
"v_cndmask_b32_e32 v229, v231, v232, vcc\n" \
"v_or_b32_e32 v231, 0x200, v214\n" \
"v_mfma_f32_16x16x32_bf16 a[224:227], v[148:151], v[144:147], a[224:227]\n" \
"s_waitcnt vmcnt(20)\n" \
"v_readfirstlane_b32 s62, v180\n" \
"s_nop 3\n" \
"s_add_u32 s62, s62, 0x9E3779B1\n" \
"s_waitcnt vmcnt(2) lgkmcnt(0)\n" \
"s_barrier\n" \
"s_nop 0\n" \
"v_mfma_f32_16x16x32_bf16 a[16:19], v[100:103], v[52:55], a[16:19]\n" \
"s_add_u32 m0, s30, 0x8000\n" \
"s_mov_b32 s85, 0x180\n" \
"v_mfma_f32_16x16x32_bf16 a[16:19], v[164:167], v[116:119], a[16:19]\n" \
"buffer_load_dwordx4 v22, s[64:67], s85 offen lds\n" \
"v_mfma_f32_16x16x32_bf16 a[20:23], v[104:107], v[52:55], a[20:23]\n" \
"ds_read_b64_tr_b16 v[84:85], v32 offset:0\n" \
"v_mul_u32_u24_e32 v232, 0xe39, v231\n" \
"v_mfma_f32_16x16x32_bf16 a[20:23], v[168:171], v[116:119], a[20:23]\n" \
"s_add_u32 m0, s30, 0x8400\n" \
"s_mov_b32 s84, 0x2180\n" \
"v_mfma_f32_16x16x32_bf16 a[24:27], v[108:111], v[52:55], a[24:27]\n" \
"buffer_load_dwordx4 v23, s[64:67], s84 offen lds\n" \
"v_mfma_f32_16x16x32_bf16 a[24:27], v[172:175], v[116:119], a[24:27]\n" \
"ds_read_b64_tr_b16 v[86:87], v32 offset:1024\n" \
"v_lshrrev_b32_e64 v232, 16, v232\n" \
"v_mfma_f32_16x16x32_bf16 a[28:31], v[112:115], v[52:55], a[28:31]\n" \
"ds_read_b64_tr_b16 v[148:149], v32 offset:8192\n" \
"v_mad_i32_i24 v231, v232, s5, v231\n" \
"v_mfma_f32_16x16x32_bf16 a[28:31], v[176:179], v[116:119], a[28:31]\n" \
"ds_read_b128 v[52:55], v28 offset:16384\n" \
"v_lshlrev_b32_e64 v233, 4, v231\n" \
"v_mfma_f32_16x16x32_bf16 a[60:63], v[112:115], v[56:59], a[60:63]\n" \
"ds_read_b128 v[116:119], v29 offset:16384\n" \
"v_add_u32_e64 v234, s4, v233\n" \
"v_mfma_f32_16x16x32_bf16 a[60:63], v[176:179], v[120:123], a[60:63]\n" \
"s_add_u32 m0, s31, 0x8000\n" \
"s_mov_b32 s85, 0x180000\n" \
"v_mfma_f32_16x16x32_bf16 a[56:59], v[108:111], v[56:59], a[56:59]\n" \
"buffer_load_dwordx4 v26, s[68:71], s85 offen lds\n" \
"v_mfma_f32_16x16x32_bf16 a[56:59], v[172:175], v[120:123], a[56:59]\n" \
"s_add_u32 m0, s31, 0xc000\n" \
"s_mov_b32 s84, 0x180000\n" \
"v_mfma_f32_16x16x32_bf16 a[52:55], v[104:107], v[56:59], a[52:55]\n" \
"buffer_load_dwordx4 v26, s[72:75], s84 offen lds\n" \
"v_mfma_f32_16x16x32_bf16 a[52:55], v[168:171], v[120:123], a[52:55]\n" \
"s_add_u32 m0, s30, 0x8800\n" \
"s_mov_b32 s85, 0x4180\n" \
"v_mfma_f32_16x16x32_bf16 a[48:51], v[100:103], v[56:59], a[48:51]\n" \
"buffer_load_dwordx4 v22, s[64:67], s85 offen lds\n" \
"v_mfma_f32_16x16x32_bf16 a[48:51], v[164:167], v[120:123], a[48:51]\n" \
"ds_read_b128 v[56:59], v28 offset:18432\n" \
"s_movk_i32 s4, 0x480\n" \
"s_nop 0\n" \
"v_mfma_f32_16x16x32_bf16 a[80:83], v[100:103], v[60:63], a[80:83]\n" \
"ds_read_b128 v[120:123], v29 offset:18432\n" \
"v_mad_u32_u24 v0, v212, s4, v213\n" \
"v_mfma_f32_16x16x32_bf16 a[80:83], v[164:167], v[124:127], a[80:83]\n" \
"s_add_u32 m0, s30, 0x8c00\n" \
"s_mov_b32 s84, 0x6180\n" \
"v_mfma_f32_16x16x32_bf16 a[84:87], v[104:107], v[60:63], a[84:87]\n" \
"buffer_load_dwordx4 v23, s[64:67], s84 offen lds\n" \
"v_mfma_f32_16x16x32_bf16 a[84:87], v[168:171], v[124:127], a[84:87]\n" \
"ds_read_b64_tr_b16 v[150:151], v32 offset:9216\n" \
"v_mul_u32_u24_e32 v212, 0x480, v218\n" \
"v_mfma_f32_16x16x32_bf16 a[88:91], v[108:111], v[60:63], a[88:91]\n" \
"s_add_u32 m0, s31, 0x8400\n" \
"s_mov_b32 s85, 0x188000\n" \
"v_mfma_f32_16x16x32_bf16 a[88:91], v[172:175], v[124:127], a[88:91]\n" \
"buffer_load_dwordx4 v26, s[68:71], s85 offen lds\n" \
"v_mfma_f32_16x16x32_bf16 a[92:95], v[112:115], v[60:63], a[92:95]\n" \
"ds_read_b64_tr_b16 v[88:89], v33 offset:0\n" \
"s_movk_i32 s5, 0xfc00\n" \
"s_nop 0\n" \
"v_mfma_f32_16x16x32_bf16 a[92:95], v[176:179], v[124:127], a[92:95]\n" \
"ds_read_b128 v[60:63], v28 offset:20480\n" \
"v_add3_u32 v1, v212, v217, s5\n" \
"v_mfma_f32_16x16x32_bf16 a[124:127], v[112:115], v[64:67], a[124:127]\n" \
"ds_read_b128 v[124:127], v29 offset:20480\n" \
"v_mul_u32_u24_e32 v212, 0x480, v220\n" \
"v_mfma_f32_16x16x32_bf16 a[124:127], v[176:179], v[128:131], a[124:127]\n" \
"s_add_u32 m0, s31, 0xc400\n" \
"s_mov_b32 s84, 0x188000\n" \
"v_mfma_f32_16x16x32_bf16 a[120:123], v[108:111], v[64:67], a[120:123]\n" \
"buffer_load_dwordx4 v26, s[72:75], s84 offen lds\n" \
"v_mfma_f32_16x16x32_bf16 a[120:123], v[172:175], v[128:131], a[120:123]\n" \
"s_add_u32 m0, s30, 0x9000\n" \
"s_mov_b32 s85, 0x8180\n" \
"v_mfma_f32_16x16x32_bf16 a[116:119], v[104:107], v[64:67], a[116:119]\n" \
"buffer_load_dwordx4 v22, s[64:67], s85 offen lds\n" \
"v_mfma_f32_16x16x32_bf16 a[116:119], v[168:171], v[128:131], a[116:119]\n" \
"ds_read_b64_tr_b16 v[90:91], v33 offset:1024\n" \
"v_add3_u32 v2, v212, v219, s7\n" \
"v_mfma_f32_16x16x32_bf16 a[112:115], v[100:103], v[64:67], a[112:115]\n" \
"ds_read_b64_tr_b16 v[152:153], v33 offset:8192\n" \
"v_mul_u32_u24_e32 v212, 0x480, v222\n" \
"v_mfma_f32_16x16x32_bf16 a[112:115], v[164:167], v[128:131], a[112:115]\n" \
"ds_read_b128 v[64:67], v28 offset:22528\n" \
"v_add3_u32 v3, v212, v221, s40\n" \
"v_mfma_f32_16x16x32_bf16 a[144:147], v[100:103], v[68:71], a[144:147]\n" \
"ds_read_b128 v[128:131], v29 offset:22528\n" \
"v_mul_u32_u24_e32 v212, 0x480, v226\n" \
"v_mfma_f32_16x16x32_bf16 a[144:147], v[164:167], v[132:135], a[144:147]\n" \
"s_add_u32 m0, s30, 0x9400\n" \
"s_mov_b32 s84, 0xa180\n" \
"v_mfma_f32_16x16x32_bf16 a[148:151], v[104:107], v[68:71], a[148:151]\n" \
"buffer_load_dwordx4 v23, s[64:67], s84 offen lds\n" \
"v_mfma_f32_16x16x32_bf16 a[148:151], v[168:171], v[132:135], a[148:151]\n" \
"s_add_u32 m0, s31, 0x8800\n" \
"s_mov_b32 s85, 0x190000\n" \
"v_mfma_f32_16x16x32_bf16 a[152:155], v[108:111], v[68:71], a[152:155]\n" \
"buffer_load_dwordx4 v27, s[68:71], s85 offen lds\n" \
"v_mfma_f32_16x16x32_bf16 a[152:155], v[172:175], v[132:135], a[152:155]\n" \
"s_add_u32 m0, s31, 0xc800\n" \
"s_mov_b32 s84, 0x190000\n" \
"v_mfma_f32_16x16x32_bf16 a[156:159], v[112:115], v[68:71], a[156:159]\n" \
"buffer_load_dwordx4 v27, s[72:75], s84 offen lds\n" \
"v_mfma_f32_16x16x32_bf16 a[156:159], v[176:179], v[132:135], a[156:159]\n" \
"ds_read_b128 v[68:71], v28 offset:0\n" \
"v_add3_u32 v5, v212, v225, s5\n" \
"v_mfma_f32_16x16x32_bf16 a[188:191], v[112:115], v[72:75], a[188:191]\n" \
"ds_read_b128 v[132:135], v29 offset:0\n" \
"v_mul_u32_u24_e32 v212, 0x480, v228\n" \
"v_mfma_f32_16x16x32_bf16 a[188:191], v[176:179], v[136:139], a[188:191]\n" \
"s_add_u32 m0, s30, 0x9800\n" \
"s_mov_b32 s85, 0xc180\n" \
"v_mfma_f32_16x16x32_bf16 a[184:187], v[108:111], v[72:75], a[184:187]\n" \
"buffer_load_dwordx4 v22, s[64:67], s85 offen lds\n" \
"v_mfma_f32_16x16x32_bf16 a[184:187], v[172:175], v[136:139], a[184:187]\n" \
"ds_read_b64_tr_b16 v[154:155], v33 offset:9216\n" \
"v_add3_u32 v6, v212, v227, s7\n" \
"v_mfma_f32_16x16x32_bf16 a[180:183], v[104:107], v[72:75], a[180:183]\n" \
"s_add_u32 m0, s30, 0x9c00\n" \
"s_mov_b32 s84, 0xe180\n" \
"v_mfma_f32_16x16x32_bf16 a[180:183], v[168:171], v[136:139], a[180:183]\n" \
"buffer_load_dwordx4 v23, s[64:67], s84 offen lds\n" \
"v_mfma_f32_16x16x32_bf16 a[176:179], v[100:103], v[72:75], a[176:179]\n" \
"ds_read_b64_tr_b16 v[92:93], v34 offset:0\n" \
"v_mul_u32_u24_e32 v212, 0x480, v230\n" \
"v_mfma_f32_16x16x32_bf16 a[176:179], v[164:167], v[136:139], a[176:179]\n" \
"ds_read_b128 v[72:75], v28 offset:2048\n" \
"v_add3_u32 v7, v212, v229, s40\n" \
"v_mfma_f32_16x16x32_bf16 a[208:211], v[100:103], v[76:79], a[208:211]\n" \
"ds_read_b128 v[136:139], v29 offset:2048\n" \
"s_lshl_b32 s7, s3, 3\n" \
"v_mfma_f32_16x16x32_bf16 a[208:211], v[164:167], v[140:143], a[208:211]\n" \
"ds_read_b64_tr_b16 v[94:95], v34 offset:1024\n" \
"s_lshl_b32 s40, s3, 1\n" \
"v_mfma_f32_16x16x32_bf16 a[212:215], v[104:107], v[76:79], a[212:215]\n" \
"ds_read_b64_tr_b16 v[156:157], v34 offset:8192\n" \
"s_and_b32 s7, s7, 16\n" \
"v_mfma_f32_16x16x32_bf16 a[212:215], v[168:171], v[140:143], a[212:215]\n" \
"ds_read_b64_tr_b16 v[158:159], v34 offset:9216\n" \
"s_and_b32 s40, s40, 2\n" \
"v_mfma_f32_16x16x32_bf16 a[216:219], v[108:111], v[76:79], a[216:219]\n" \
"ds_read_b64_tr_b16 v[96:97], v35 offset:0\n" \
"s_and_b32 s5, s2, 7\n" \
"v_mfma_f32_16x16x32_bf16 a[216:219], v[172:175], v[140:143], a[216:219]\n" \
"ds_read_b64_tr_b16 v[98:99], v35 offset:1024\n" \
"v_bfe_u32 v212, v20, 7, 1\n" \
"v_mfma_f32_16x16x32_bf16 a[220:223], v[112:115], v[76:79], a[220:223]\n" \
"ds_read_b64_tr_b16 v[160:161], v35 offset:8192\n" \
"s_or_b32 s7, s40, s7\n" \
"v_mfma_f32_16x16x32_bf16 a[220:223], v[176:179], v[140:143], a[220:223]\n" \
"ds_read_b128 v[76:79], v28 offset:4096\n" \
"v_and_b32_e32 v234, 0xf0, v234\n" \
"v_mfma_f32_16x16x32_bf16 a[252:255], v[112:115], v[80:83], a[252:255]\n" \
"ds_read_b128 v[140:143], v29 offset:4096\n" \
"v_cmp_gt_u32_e64 vcc, 16, v231\n" \
"v_mfma_f32_16x16x32_bf16 a[252:255], v[176:179], v[144:147], a[252:255]\n" \
"ds_read_b64_tr_b16 v[162:163], v35 offset:9216\n" \
"v_or3_b32 v212, s7, v212, v216\n" \
"v_mfma_f32_16x16x32_bf16 a[248:251], v[108:111], v[80:83], a[248:251]\n" \
"v_and_b32_e32 v213, 32, v20\n" \
"s_lshl_b32 s5, s5, 5\n" \
"v_mfma_f32_16x16x32_bf16 a[248:251], v[172:175], v[144:147], a[248:251]\n" \
"v_cndmask_b32_e32 v231, v233, v234, vcc\n" \
"v_mul_u32_u24_e32 v212, 18, v212\n" \
"v_mfma_f32_16x16x32_bf16 a[244:247], v[104:107], v[80:83], a[244:247]\n" \
"v_mov_b32_e32 v216, 0x100\n" \
"v_mov_b32_e64 v217, s5\n" \
"v_mfma_f32_16x16x32_bf16 a[244:247], v[168:171], v[144:147], a[244:247]\n" \
"v_cmp_eq_u32_e64 vcc, 0, v213\n" \
"v_and_or_b32 v212, v215, 1, v212\n" \
"v_mfma_f32_16x16x32_bf16 a[240:243], v[100:103], v[80:83], a[240:243]\n" \
"v_mad_u32_u24 v4, v224, s4, v223\n" \
"v_cndmask_b32_e64 v213, v216, v217, vcc\n" \
"v_mfma_f32_16x16x32_bf16 a[240:243], v[164:167], v[144:147], a[240:243]\n" \
"ds_read_b128 v[80:83], v28 offset:6144\n" \
"ds_read_b128 v[144:147], v29 offset:6144\n" \
"s_waitcnt lgkmcnt(15)\n" \
"v_mfma_f32_16x16x32_bf16 a[0:3], v[84:87], v[52:55], a[0:3]\n" \
"s_add_u32 m0, s31, 0x8c00\n" \
"s_mov_b32 s85, 0x198000\n" \
"v_mfma_f32_16x16x32_bf16 a[0:3], v[148:151], v[116:119], a[0:3]\n" \
"buffer_load_dwordx4 v27, s[68:71], s85 offen lds\n" \
"v_mfma_f32_16x16x32_bf16 a[4:7], v[88:91], v[52:55], a[4:7]\n" \
"ds_read_b64_tr_b16 v[100:101], v36 offset:0\n" \
"v_mad_u32_u24 v212, v212, s4, v213\n" \
"s_waitcnt lgkmcnt(15)\n" \
"v_mfma_f32_16x16x32_bf16 a[4:7], v[152:155], v[116:119], a[4:7]\n" \
"s_add_u32 m0, s31, 0xcc00\n" \
"s_mov_b32 s84, 0x198000\n" \
"s_waitcnt lgkmcnt(11)\n" \
"v_mfma_f32_16x16x32_bf16 a[8:11], v[92:95], v[52:55], a[8:11]\n" \
"buffer_load_dwordx4 v27, s[72:75], s84 offen lds\n" \
"s_waitcnt lgkmcnt(9)\n" \
"v_mfma_f32_16x16x32_bf16 a[8:11], v[156:159], v[116:119], a[8:11]\n" \
"ds_read_b64_tr_b16 v[102:103], v36 offset:1024\n" \
"v_and_or_b32 v212, v20, 16, v212\n" \
"s_waitcnt lgkmcnt(8)\n" \
"v_mfma_f32_16x16x32_bf16 a[12:15], v[96:99], v[52:55], a[12:15]\n" \
"ds_read_b64_tr_b16 v[164:165], v36 offset:8192\n" \
"v_add_u32_e32 v9, 0x4800, v212\n" \
"s_waitcnt lgkmcnt(5)\n" \
"v_mfma_f32_16x16x32_bf16 a[12:15], v[160:163], v[116:119], a[12:15]\n" \
"v_mul_u32_u24_e32 v212, 0x120, v215\n" \
"v_lshlrev_b32_e64 v213, 3, v214\n" \
"v_mfma_f32_16x16x32_bf16 a[44:47], v[96:99], v[56:59], a[44:47]\n" \
"v_mad_u32_u24 v8, v232, s4, v231\n" \
"v_and_or_b32 v10, v213, 24, v212\n" \
"v_mfma_f32_16x16x32_bf16 a[44:47], v[160:163], v[120:123], a[44:47]\n" \
"ds_read_b64_tr_b16 v[166:167], v36 offset:9216\n" \
"s_lshl_b32 s4, s6, 6\n" \
"v_mfma_f32_16x16x32_bf16 a[40:43], v[92:95], v[56:59], a[40:43]\n" \
"buffer_load_dwordx4 v[200:203], v192, s[76:79], s34 offen offset:0\n" \
"v_mfma_f32_16x16x32_bf16 a[40:43], v[156:159], v[120:123], a[40:43]\n" \
"v_lshrrev_b32_e32 v212, 2, v20\n" \
"v_and_or_b32 v212, v212, 60, s4\n" \
"v_mfma_f32_16x16x32_bf16 a[36:39], v[88:91], v[56:59], a[36:39]\n" \
"v_or_b32_e32 v18, 0x27000, v212\n" \
"v_lshl_or_b32 v212, s3, 9, v213\n" \
"v_mfma_f32_16x16x32_bf16 a[36:39], v[152:155], v[120:123], a[36:39]\n" \
"ds_read_b64_tr_b16 v[104:105], v37 offset:0\n" \
"v_or_b32_e32 v19, 0x27800, v212\n" \
"v_mfma_f32_16x16x32_bf16 a[32:35], v[84:87], v[56:59], a[32:35]\n" \
"v_mov_b32_e32 v219, 0x1000c0c\n" \
"v_mov_b32_e32 v220, 0xc0c0100\n" \
"v_mfma_f32_16x16x32_bf16 a[32:35], v[148:151], v[120:123], a[32:35]\n" \
"v_bfe_u32 v218, v20, 4, 1\n" \
"v_lshrrev_b32_e64 v213, 8, v20\n" \
"v_mfma_f32_16x16x32_bf16 a[64:67], v[84:87], v[60:63], a[64:67]\n" \
"buffer_load_dwordx4 v[204:207], v193, s[76:79], s34 offen offset:0\n" \
"v_mfma_f32_16x16x32_bf16 a[64:67], v[148:151], v[124:127], a[64:67]\n" \
"s_mov_b32 m0, s32\n" \
"ds_read_b64_tr_b16 v[106:107], v37 offset:1024\n" \
"v_mfma_f32_16x16x32_bf16 a[68:71], v[88:91], v[60:63], a[68:71]\n" \
"s_mov_b32 exec_lo, 0x0\n" \
"s_mov_b32 exec_hi, 0xff00\n" \
"global_load_lds_dword v[184:185], off\n" \
"s_mov_b64 exec, -1\n" \
"v_mfma_f32_16x16x32_bf16 a[68:71], v[152:155], v[124:127], a[68:71]\n" \
"ds_read_b64_tr_b16 v[168:169], v37 offset:8192\n" \
"v_cmp_eq_u32_e32 vcc, 0, v218\n" \
"v_mfma_f32_16x16x32_bf16 a[72:75], v[92:95], v[60:63], a[72:75]\n" \
"s_lshr_b32 s4, s2, 5\n" \
"s_and_b32 s5, s3, 1\n" \
"v_mfma_f32_16x16x32_bf16 a[72:75], v[156:159], v[124:127], a[72:75]\n" \
"buffer_load_dword v208, v194, s[76:79], s34 offen offset:32\n" \
"v_mfma_f32_16x16x32_bf16 a[76:79], v[96:99], v[60:63], a[76:79]\n" \
"ds_read_b64_tr_b16 v[170:171], v37 offset:9216\n" \
"v_bfe_u32 v217, v20, 4, 4\n" \
"v_mfma_f32_16x16x32_bf16 a[76:79], v[160:163], v[124:127], a[76:79]\n" \
"v_cndmask_b32_e64 v219, v219, v220, vcc\n" \
"v_mov_b32_e32 v220, 0x3020c0c\n" \
"v_mfma_f32_16x16x32_bf16 a[108:111], v[96:99], v[64:67], a[108:111]\n" \
"v_mov_b32_e32 v221, 0xc0c0302\n" \
"v_lshl_or_b32 v218, v213, 2, v218\n" \
"v_mfma_f32_16x16x32_bf16 a[108:111], v[160:163], v[128:131], a[108:111]\n" \
"s_xor_b32 s6, s4, 15\n" \
"v_mfma_f32_16x16x32_bf16 a[104:107], v[92:95], v[64:67], a[104:107]\n" \
"buffer_load_dword v209, v194, s[76:79], s34 offen offset:1184\n" \
"v_mfma_f32_16x16x32_bf16 a[104:107], v[156:159], v[128:131], a[104:107]\n" \
"ds_read_b64_tr_b16 v[108:109], v38 offset:0\n" \
"v_mfma_f32_16x16x32_bf16 a[100:103], v[88:91], v[64:67], a[100:103]\n" \
"s_cmpk_lt_u32 s3, 2\n" \
"s_cselect_b32 s7, s6, s4\n" \
"v_mfma_f32_16x16x32_bf16 a[100:103], v[152:155], v[128:131], a[100:103]\n" \
"ds_read_b64_tr_b16 v[110:111], v38 offset:1024\n" \
"s_lshl_b32 s40, s7, 7\n" \
"v_mfma_f32_16x16x32_bf16 a[96:99], v[84:87], v[64:67], a[96:99]\n" \
"s_lshl_b32 s41, s5, 6\n" \
"v_cndmask_b32_e32 v220, v220, v221, vcc\n" \
"v_mfma_f32_16x16x32_bf16 a[96:99], v[148:151], v[128:131], a[96:99]\n" \
"v_mov_b32_e32 v221, 0xc0c0c0c\n" \
"v_cmp_gt_u32_e64 vcc, v218, v217\n" \
"v_mfma_f32_16x16x32_bf16 a[128:131], v[84:87], v[68:71], a[128:131]\n" \
"buffer_load_dword v211, v194, s[76:79], s34 offen offset:2336\n" \
"v_mfma_f32_16x16x32_bf16 a[128:131], v[148:151], v[132:135], a[128:131]\n" \
"ds_read_b64_tr_b16 v[172:173], v38 offset:8192\n" \
"v_or_b32_e64 v218, 2, v218\n" \
"v_mfma_f32_16x16x32_bf16 a[132:135], v[88:91], v[68:71], a[132:135]\n" \
"v_lshrrev_b32_e32 v212, 7, v20\n" \
"s_or_b32 s41, s41, s40\n" \
"v_mfma_f32_16x16x32_bf16 a[132:135], v[152:155], v[132:135], a[132:135]\n" \
"ds_read_b64_tr_b16 v[174:175], v38 offset:9216\n" \
"v_cndmask_b32_e64 v40, v221, v219, vcc\n" \
"v_mfma_f32_16x16x32_bf16 a[136:139], v[92:95], v[68:71], a[136:139]\n" \
"v_cndmask_b32_e32 v42, v219, v221, vcc\n" \
"v_cmp_gt_u32_e32 vcc, v218, v217\n" \
"v_mfma_f32_16x16x32_bf16 a[136:139], v[156:159], v[132:135], a[136:139]\n" \
"s_mov_b32 s76, s20\n" \
"s_and_b32 s77, s21, 0xffff\n" \
"v_mfma_f32_16x16x32_bf16 a[140:143], v[96:99], v[68:71], a[140:143]\n" \
"s_mov_b32 s78, 0x480\n" \
"v_and_b32_e32 v218, 16, v20\n" \
"v_mfma_f32_16x16x32_bf16 a[140:143], v[160:163], v[132:135], a[140:143]\n" \
"buffer_load_ushort v210, v195, s[76:79], 0 offen\n" \
"v_mfma_f32_16x16x32_bf16 a[172:175], v[96:99], v[72:75], a[172:175]\n" \
"ds_read_b64_tr_b16 v[112:113], v39 offset:0\n" \
"s_movk_i32 s42, 0x3f80\n" \
"v_mfma_f32_16x16x32_bf16 a[172:175], v[160:163], v[136:139], a[172:175]\n" \
"v_or_b32_e32 v214, s41, v212\n" \
"v_and_b32_e32 v217, 0x320, v20\n" \
"v_mfma_f32_16x16x32_bf16 a[168:171], v[92:95], v[72:75], a[168:171]\n" \
"s_mov_b32 s76, s8\n" \
"s_and_b32 s77, s9, 0xffff\n" \
"v_mfma_f32_16x16x32_bf16 a[168:171], v[156:159], v[136:139], a[168:171]\n" \
"s_mov_b32 s78, 0x240000\n" \
"ds_read_b64_tr_b16 v[114:115], v39 offset:1024\n" \
"v_mfma_f32_16x16x32_bf16 a[164:167], v[88:91], v[72:75], a[164:167]\n" \
"s_movk_i32 s41, 0x100\n" \
"v_lshlrev_b32_e64 v218, v218, s42\n" \
"v_mfma_f32_16x16x32_bf16 a[164:167], v[152:155], v[136:139], a[164:167]\n" \
"ds_read_b64_tr_b16 v[176:177], v39 offset:8192\n" \
"v_mov_b32_e64 v219, 0\n" \
"v_mfma_f32_16x16x32_bf16 a[160:163], v[84:87], v[72:75], a[160:163]\n" \
"v_lshrrev_b32_e32 v215, 6, v20\n" \
"v_cndmask_b32_e32 v41, v221, v220, vcc\n" \
"v_mfma_f32_16x16x32_bf16 a[160:163], v[148:151], v[136:139], a[160:163]\n" \
"v_cndmask_b32_e32 v43, v220, v221, vcc\n" \
"v_cmp_eq_u32_e32 vcc, s41, v217\n" \
"v_mfma_f32_16x16x32_bf16 a[192:195], v[84:87], v[76:79], a[192:195]\n" \
"ds_read_b64_tr_b16 v[178:179], v39 offset:9216\n" \
"v_and_b32_e32 v217, 0x330, v20\n" \
"v_mfma_f32_16x16x32_bf16 a[192:195], v[148:151], v[140:143], a[192:195]\n" \
"v_bfe_u32 v216, v20, 6, 2\n" \
"v_cndmask_b32_e64 v46, v219, v218, vcc\n" \
"v_mfma_f32_16x16x32_bf16 a[196:199], v[88:91], v[76:79], a[196:199]\n" \
"v_mov_b32_e32 v218, 0x3f80\n" \
"v_cmp_eq_u32_e64 vcc, 32, v217\n" \
"v_mfma_f32_16x16x32_bf16 a[196:199], v[152:155], v[140:143], a[196:199]\n" \
"s_add_i32 s41, s40, -16\n" \
"v_lshrrev_b32_e32 v219, 1, v20\n" \
"v_mfma_f32_16x16x32_bf16 a[200:203], v[92:95], v[76:79], a[200:203]\n" \
"v_bfe_u32 v215, v215, 1, 1\n" \
"v_cndmask_b32_e64 v47, 0, v218, vcc\n" \
"v_mfma_f32_16x16x32_bf16 a[200:203], v[156:159], v[140:143], a[200:203]\n" \
"s_mov_b32 s42, 0\n" \
"v_mfma_f32_16x16x32_bf16 a[204:207], v[96:99], v[76:79], a[204:207]\n" \
"s_cmp_lg_u32 s40, 0\n" \
"s_cselect_b32 s41, s41, s42\n" \
"v_mfma_f32_16x16x32_bf16 a[204:207], v[160:163], v[140:143], a[204:207]\n" \
"v_lshl_or_b32 v217, s5, 3, v212\n" \
"v_lshlrev_b32_e32 v218, 10, v213\n" \
"s_waitcnt lgkmcnt(15)\n" \
"v_mfma_f32_16x16x32_bf16 a[236:239], v[96:99], v[80:83], a[236:239]\n" \
"v_lshlrev_b32_e32 v216, 7, v216\n" \
"v_and_b32_e64 v219, 24, v219\n" \
"v_mfma_f32_16x16x32_bf16 a[236:239], v[160:163], v[144:147], a[236:239]\n" \
"v_and_or_b32 v212, v212, 2, v215\n" \
"v_add_u32_e64 v217, s41, v217\n" \
"v_mfma_f32_16x16x32_bf16 a[232:235], v[92:95], v[80:83], a[232:235]\n" \
"v_or3_b32 v216, v216, v218, v219\n" \
"v_lshlrev_b32_e64 v212, 5, v212\n" \
"v_mfma_f32_16x16x32_bf16 a[232:235], v[156:159], v[144:147], a[232:235]\n" \
"s_mov_b32 s41, 0x14000\n" \
"v_or3_b32 v48, v216, v212, s41\n" \
"v_mfma_f32_16x16x32_bf16 a[228:231], v[88:91], v[80:83], a[228:231]\n" \
"s_lshl_b32 s41, s5, 2\n" \
"s_lshl_b32 s7, s7, 3\n" \
"v_mfma_f32_16x16x32_bf16 a[228:231], v[152:155], v[144:147], a[228:231]\n" \
"v_or_b32_e32 v212, s41, v213\n" \
"s_or_b32 s7, s7, s41\n" \
"v_mfma_f32_16x16x32_bf16 a[224:227], v[84:87], v[80:83], a[224:227]\n" \
"s_mov_b32 s41, 0xff800000\n" \
"v_mfma_f32_16x16x32_bf16 a[224:227], v[148:151], v[144:147], a[224:227]\n" \
"s_waitcnt vmcnt(7) lgkmcnt(0)\n" \
"s_barrier\n" \
"v_mfma_f32_16x16x32_bf16 a[16:19], v[100:103], v[52:55], a[16:19]\n" \
"s_add_u32 m0, s30, 0x0\n" \
"s_nop 0\n" \
"s_mov_b32 s85, 0x200\n" \
"v_mfma_f32_16x16x32_bf16 a[16:19], v[164:167], v[116:119], a[16:19]\n" \
"buffer_load_dwordx4 v22, s[64:67], s85 offen lds\n" \
"v_mfma_f32_16x16x32_bf16 a[20:23], v[104:107], v[52:55], a[20:23]\n" \
"ds_read_b64_tr_b16 v[84:85], v32 offset:32768\n" \
"v_mfma_f32_16x16x32_bf16 a[20:23], v[168:171], v[116:119], a[20:23]\n" \
"s_add_u32 m0, s30, 0x400\n" \
"s_mov_b32 s84, 0x2200\n" \
"v_mfma_f32_16x16x32_bf16 a[24:27], v[108:111], v[52:55], a[24:27]\n" \
"buffer_load_dwordx4 v23, s[64:67], s84 offen lds\n" \
"v_mfma_f32_16x16x32_bf16 a[24:27], v[172:175], v[116:119], a[24:27]\n" \
"ds_read_b64_tr_b16 v[86:87], v32 offset:33792\n" \
"v_mfma_f32_16x16x32_bf16 a[28:31], v[112:115], v[52:55], a[28:31]\n" \
"ds_read_b64_tr_b16 v[148:149], v32 offset:40960\n" \
"v_mfma_f32_16x16x32_bf16 a[28:31], v[176:179], v[116:119], a[28:31]\n" \
"ds_read_b128 v[52:55], v28 offset:49152\n" \
"v_mfma_f32_16x16x32_bf16 a[60:63], v[112:115], v[56:59], a[60:63]\n" \
"ds_read_b128 v[116:119], v29 offset:49152\n" \
"v_mfma_f32_16x16x32_bf16 a[60:63], v[176:179], v[120:123], a[60:63]\n" \
"s_add_u32 m0, s31, 0x0\n" \
"s_nop 0\n" \
"s_mov_b32 s85, 0x200000\n" \
"v_mfma_f32_16x16x32_bf16 a[56:59], v[108:111], v[56:59], a[56:59]\n" \
"buffer_load_dwordx4 v26, s[68:71], s85 offen lds\n" \
"v_mfma_f32_16x16x32_bf16 a[56:59], v[172:175], v[120:123], a[56:59]\n" \
"s_add_u32 m0, s31, 0x4000\n" \
"s_mov_b32 s84, 0x200000\n" \
"v_mfma_f32_16x16x32_bf16 a[52:55], v[104:107], v[56:59], a[52:55]\n" \
"buffer_load_dwordx4 v26, s[72:75], s84 offen lds\n" \
"v_mfma_f32_16x16x32_bf16 a[52:55], v[168:171], v[120:123], a[52:55]\n" \
"s_add_u32 m0, s30, 0x800\n" \
"s_mov_b32 s85, 0x4200\n" \
"v_mfma_f32_16x16x32_bf16 a[48:51], v[100:103], v[56:59], a[48:51]\n" \
"buffer_load_dwordx4 v22, s[64:67], s85 offen lds\n" \
"v_mfma_f32_16x16x32_bf16 a[48:51], v[164:167], v[120:123], a[48:51]\n" \
"ds_read_b128 v[56:59], v28 offset:51200\n" \
"v_mfma_f32_16x16x32_bf16 a[80:83], v[100:103], v[60:63], a[80:83]\n" \
"ds_read_b128 v[120:123], v29 offset:51200\n" \
"v_mfma_f32_16x16x32_bf16 a[80:83], v[164:167], v[124:127], a[80:83]\n" \
"s_add_u32 m0, s30, 0xc00\n" \
"s_mov_b32 s84, 0x6200\n" \
"v_mfma_f32_16x16x32_bf16 a[84:87], v[104:107], v[60:63], a[84:87]\n" \
"buffer_load_dwordx4 v23, s[64:67], s84 offen lds\n" \
"v_mfma_f32_16x16x32_bf16 a[84:87], v[168:171], v[124:127], a[84:87]\n" \
"ds_read_b64_tr_b16 v[150:151], v32 offset:41984\n" \
"v_mfma_f32_16x16x32_bf16 a[88:91], v[108:111], v[60:63], a[88:91]\n" \
"s_add_u32 m0, s31, 0x400\n" \
"s_mov_b32 s85, 0x208000\n" \
"v_mfma_f32_16x16x32_bf16 a[88:91], v[172:175], v[124:127], a[88:91]\n" \
"buffer_load_dwordx4 v26, s[68:71], s85 offen lds\n" \
"v_mfma_f32_16x16x32_bf16 a[92:95], v[112:115], v[60:63], a[92:95]\n" \
"ds_read_b64_tr_b16 v[88:89], v33 offset:32768\n" \
"v_mfma_f32_16x16x32_bf16 a[92:95], v[176:179], v[124:127], a[92:95]\n" \
"ds_read_b128 v[60:63], v28 offset:53248\n" \
"v_mfma_f32_16x16x32_bf16 a[124:127], v[112:115], v[64:67], a[124:127]\n" \
"ds_read_b128 v[124:127], v29 offset:53248\n" \
"v_mfma_f32_16x16x32_bf16 a[124:127], v[176:179], v[128:131], a[124:127]\n" \
"s_add_u32 m0, s31, 0x4400\n" \
"s_mov_b32 s84, 0x208000\n" \
"v_mfma_f32_16x16x32_bf16 a[120:123], v[108:111], v[64:67], a[120:123]\n" \
"buffer_load_dwordx4 v26, s[72:75], s84 offen lds\n" \
"v_mfma_f32_16x16x32_bf16 a[120:123], v[172:175], v[128:131], a[120:123]\n" \
"s_add_u32 m0, s30, 0x1000\n" \
"s_mov_b32 s85, 0x8200\n" \
"v_mfma_f32_16x16x32_bf16 a[116:119], v[104:107], v[64:67], a[116:119]\n" \
"buffer_load_dwordx4 v22, s[64:67], s85 offen lds\n" \
"v_mfma_f32_16x16x32_bf16 a[116:119], v[168:171], v[128:131], a[116:119]\n" \
"ds_read_b64_tr_b16 v[90:91], v33 offset:33792\n" \
"v_mfma_f32_16x16x32_bf16 a[112:115], v[100:103], v[64:67], a[112:115]\n" \
"ds_read_b64_tr_b16 v[152:153], v33 offset:40960\n" \
"v_mfma_f32_16x16x32_bf16 a[112:115], v[164:167], v[128:131], a[112:115]\n" \
"ds_read_b128 v[64:67], v28 offset:55296\n" \
"v_mfma_f32_16x16x32_bf16 a[144:147], v[100:103], v[68:71], a[144:147]\n" \
"ds_read_b128 v[128:131], v29 offset:55296\n" \
"v_mfma_f32_16x16x32_bf16 a[144:147], v[164:167], v[132:135], a[144:147]\n" \
"s_add_u32 m0, s30, 0x1400\n" \
"s_mov_b32 s84, 0xa200\n" \
"v_mfma_f32_16x16x32_bf16 a[148:151], v[104:107], v[68:71], a[148:151]\n" \
"buffer_load_dwordx4 v23, s[64:67], s84 offen lds\n" \
"v_mfma_f32_16x16x32_bf16 a[148:151], v[168:171], v[132:135], a[148:151]\n" \
"s_add_u32 m0, s31, 0x800\n" \
"s_mov_b32 s85, 0x210000\n" \
"v_mfma_f32_16x16x32_bf16 a[152:155], v[108:111], v[68:71], a[152:155]\n" \
"buffer_load_dwordx4 v27, s[68:71], s85 offen lds\n" \
"v_mfma_f32_16x16x32_bf16 a[152:155], v[172:175], v[132:135], a[152:155]\n" \
"s_add_u32 m0, s31, 0x4800\n" \
"s_mov_b32 s84, 0x210000\n" \
"v_mfma_f32_16x16x32_bf16 a[156:159], v[112:115], v[68:71], a[156:159]\n" \
"buffer_load_dwordx4 v27, s[72:75], s84 offen lds\n" \
"v_mfma_f32_16x16x32_bf16 a[156:159], v[176:179], v[132:135], a[156:159]\n" \
"ds_read_b128 v[68:71], v28 offset:32768\n" \
"v_mfma_f32_16x16x32_bf16 a[188:191], v[112:115], v[72:75], a[188:191]\n" \
"ds_read_b128 v[132:135], v29 offset:32768\n" \
"v_mfma_f32_16x16x32_bf16 a[188:191], v[176:179], v[136:139], a[188:191]\n" \
"s_add_u32 m0, s30, 0x1800\n" \
"s_mov_b32 s85, 0xc200\n" \
"v_mfma_f32_16x16x32_bf16 a[184:187], v[108:111], v[72:75], a[184:187]\n" \
"buffer_load_dwordx4 v22, s[64:67], s85 offen lds\n" \
"v_mfma_f32_16x16x32_bf16 a[184:187], v[172:175], v[136:139], a[184:187]\n" \
"ds_read_b64_tr_b16 v[154:155], v33 offset:41984\n" \
"v_mfma_f32_16x16x32_bf16 a[180:183], v[104:107], v[72:75], a[180:183]\n" \
"s_add_u32 m0, s30, 0x1c00\n" \
"s_mov_b32 s84, 0xe200\n" \
"v_mfma_f32_16x16x32_bf16 a[180:183], v[168:171], v[136:139], a[180:183]\n" \
"buffer_load_dwordx4 v23, s[64:67], s84 offen lds\n" \
"v_mfma_f32_16x16x32_bf16 a[176:179], v[100:103], v[72:75], a[176:179]\n" \
"ds_read_b64_tr_b16 v[92:93], v34 offset:32768\n" \
"v_mfma_f32_16x16x32_bf16 a[176:179], v[164:167], v[136:139], a[176:179]\n" \
"ds_read_b128 v[72:75], v28 offset:34816\n" \
"v_mfma_f32_16x16x32_bf16 a[208:211], v[100:103], v[76:79], a[208:211]\n" \
"ds_read_b128 v[136:139], v29 offset:34816\n" \
"v_mfma_f32_16x16x32_bf16 a[208:211], v[164:167], v[140:143], a[208:211]\n" \
"ds_read_b64_tr_b16 v[94:95], v34 offset:33792\n" \
"v_mfma_f32_16x16x32_bf16 a[212:215], v[104:107], v[76:79], a[212:215]\n" \
"ds_read_b64_tr_b16 v[156:157], v34 offset:40960\n" \
"v_mfma_f32_16x16x32_bf16 a[212:215], v[168:171], v[140:143], a[212:215]\n" \
"ds_read_b64_tr_b16 v[158:159], v34 offset:41984\n" \
"v_mfma_f32_16x16x32_bf16 a[216:219], v[108:111], v[76:79], a[216:219]\n" \
"ds_read_b64_tr_b16 v[96:97], v35 offset:32768\n" \
"v_mfma_f32_16x16x32_bf16 a[216:219], v[172:175], v[140:143], a[216:219]\n" \
"ds_read_b64_tr_b16 v[98:99], v35 offset:33792\n" \
"v_mfma_f32_16x16x32_bf16 a[220:223], v[112:115], v[76:79], a[220:223]\n" \
"ds_read_b64_tr_b16 v[160:161], v35 offset:40960\n" \
"v_mfma_f32_16x16x32_bf16 a[220:223], v[176:179], v[140:143], a[220:223]\n" \
"ds_read_b128 v[76:79], v28 offset:36864\n" \
"v_mfma_f32_16x16x32_bf16 a[252:255], v[112:115], v[80:83], a[252:255]\n" \
"ds_read_b128 v[140:143], v29 offset:36864\n" \
"v_mfma_f32_16x16x32_bf16 a[252:255], v[176:179], v[144:147], a[252:255]\n" \
"ds_read_b64_tr_b16 v[162:163], v35 offset:41984\n" \
"v_mfma_f32_16x16x32_bf16 a[248:251], v[108:111], v[80:83], a[248:251]\n" \
"s_cmp_lg_u32 s40, 0\n" \
"s_cselect_b32 s47, s42, s41\n" \
"v_mfma_f32_16x16x32_bf16 a[248:251], v[172:175], v[144:147], a[248:251]\n" \
"s_mov_b32 s40, 0xc000\n" \
"s_mov_b32 s41, 0x8000\n" \
"v_mfma_f32_16x16x32_bf16 a[244:247], v[104:107], v[80:83], a[244:247]\n" \
"s_cmp_lg_u32 s4, 0\n" \
"s_cselect_b32 s51, s40, s41\n" \
"v_mfma_f32_16x16x32_bf16 a[244:247], v[168:171], v[144:147], a[244:247]\n" \
"s_movk_i32 s40, 0x4000\n" \
"v_mfma_f32_16x16x32_bf16 a[240:243], v[100:103], v[80:83], a[240:243]\n" \
"s_cmp_lg_u32 s4, 0\n" \
"s_cselect_b32 s53, s41, s40\n" \
"v_mfma_f32_16x16x32_bf16 a[240:243], v[164:167], v[144:147], a[240:243]\n" \
"ds_read_b128 v[80:83], v28 offset:38912\n" \
"ds_read_b128 v[144:147], v29 offset:38912\n" \
"s_waitcnt lgkmcnt(15)\n" \
"v_mfma_f32_16x16x32_bf16 a[0:3], v[84:87], v[52:55], a[0:3]\n" \
"s_add_u32 m0, s31, 0xc00\n" \
"s_mov_b32 s85, 0x218000\n" \
"v_mfma_f32_16x16x32_bf16 a[0:3], v[148:151], v[116:119], a[0:3]\n" \
"buffer_load_dwordx4 v27, s[68:71], s85 offen lds\n" \
"v_mfma_f32_16x16x32_bf16 a[4:7], v[88:91], v[52:55], a[4:7]\n" \
"ds_read_b64_tr_b16 v[100:101], v36 offset:32768\n" \
"s_lshl_b32 s40, s2, 2\n" \
"s_waitcnt lgkmcnt(15)\n" \
"v_mfma_f32_16x16x32_bf16 a[4:7], v[152:155], v[116:119], a[4:7]\n" \
"s_add_u32 m0, s31, 0x4c00\n" \
"s_mov_b32 s84, 0x218000\n" \
"s_waitcnt lgkmcnt(11)\n" \
"v_mfma_f32_16x16x32_bf16 a[8:11], v[92:95], v[52:55], a[8:11]\n" \
"buffer_load_dwordx4 v27, s[72:75], s84 offen lds\n" \
"s_waitcnt lgkmcnt(9)\n" \
"v_mfma_f32_16x16x32_bf16 a[8:11], v[156:159], v[116:119], a[8:11]\n" \
"ds_read_b64_tr_b16 v[102:103], v36 offset:33792\n" \
"s_and_b32 s40, s40, 28\n" \
"s_waitcnt lgkmcnt(8)\n" \
"v_mfma_f32_16x16x32_bf16 a[12:15], v[96:99], v[52:55], a[12:15]\n" \
"ds_read_b64_tr_b16 v[164:165], v36 offset:40960\n" \
"s_bfe_u32 s45, s2, 0x20003\n" \
"s_waitcnt lgkmcnt(5)\n" \
"v_mfma_f32_16x16x32_bf16 a[12:15], v[160:163], v[116:119], a[12:15]\n" \
"s_lshr_b32 s44, s3, 1\n" \
"s_or_b32 s46, s40, s45\n" \
"v_mfma_f32_16x16x32_bf16 a[44:47], v[96:99], v[56:59], a[44:47]\n" \
"s_lshl_b32 s43, s44, 15\n" \
"s_lshl_b32 s41, s5, 14\n" \
"v_mfma_f32_16x16x32_bf16 a[44:47], v[160:163], v[120:123], a[44:47]\n" \
"ds_read_b64_tr_b16 v[166:167], v36 offset:41984\n" \
"s_lshl_b32 s48, s46, 20\n" \
"v_mfma_f32_16x16x32_bf16 a[40:43], v[92:95], v[56:59], a[40:43]\n" \
"s_or_b32 s41, s43, s41\n" \
"s_add_i32 s49, s48, 0x1000000\n" \
"v_mfma_f32_16x16x32_bf16 a[40:43], v[156:159], v[120:123], a[40:43]\n" \
"s_or_b32 s42, s49, s41\n" \
"s_lshl_b32 s4, s4, 16\n" \
"v_mfma_f32_16x16x32_bf16 a[36:39], v[88:91], v[56:59], a[36:39]\n" \
"s_or_b32 s41, s42, s4\n" \
"s_lshl_b32 s4, s6, 16\n" \
"v_mfma_f32_16x16x32_bf16 a[36:39], v[152:155], v[120:123], a[36:39]\n" \
"ds_read_b64_tr_b16 v[104:105], v37 offset:32768\n" \
"s_or_b32 s42, s42, s4\n" \
"v_mfma_f32_16x16x32_bf16 a[32:35], v[84:87], v[56:59], a[32:35]\n" \
"s_lshl_b32 s4, s5, 11\n" \
"s_mul_i32 s5, s7, 0x24800\n" \
"v_mfma_f32_16x16x32_bf16 a[32:35], v[148:151], v[120:123], a[32:35]\n" \
"s_lshl_b32 s6, s46, 9\n" \
"s_lshl_b32 s7, s40, 6\n" \
"v_mfma_f32_16x16x32_bf16 a[64:67], v[84:87], v[60:63], a[64:67]\n" \
"s_waitcnt vmcnt(16)\n" \
"v_or_b32_e32 v204, v210, v204\n" \
"v_or_b32_e32 v206, v210, v206\n" \
"v_mfma_f32_16x16x32_bf16 a[64:67], v[148:151], v[124:127], a[64:67]\n" \
"v_mov_b32_e64 v210, 0\n" \
"s_add_u32 s88, s35, 0x1000\n" \
"v_mfma_f32_16x16x32_bf16 a[68:71], v[88:91], v[60:63], a[68:71]\n" \
"ds_read_b64_tr_b16 v[106:107], v37 offset:33792\n" \
"s_add_i32 s6, s5, s6\n" \
"v_mfma_f32_16x16x32_bf16 a[68:71], v[152:155], v[124:127], a[68:71]\n" \
"ds_read_b64_tr_b16 v[168:169], v37 offset:40960\n" \
"s_or_b32 s5, s5, s7\n" \
"v_mfma_f32_16x16x32_bf16 a[72:75], v[92:95], v[60:63], a[72:75]\n" \
"s_mov_b32 m0, s32\n" \
"s_lshl_b32 s7, s45, 2\n" \
"v_mfma_f32_16x16x32_bf16 a[72:75], v[156:159], v[124:127], a[72:75]\n" \
"s_mov_b32 exec_lo, 0xffffffff\n" \
"s_mov_b32 exec_hi, 0x0\n" \
"global_load_lds_dword v[188:189], off\n" \
"s_mov_b64 exec, -1\n" \
"v_mfma_f32_16x16x32_bf16 a[76:79], v[96:99], v[60:63], a[76:79]\n" \
"ds_read_b64_tr_b16 v[170:171], v37 offset:41984\n" \
"s_or_b32 s5, s5, s7\n" \
"v_mfma_f32_16x16x32_bf16 a[76:79], v[160:163], v[124:127], a[76:79]\n" \
"s_lshl_b32 s7, s3, 12\n" \
"s_or_b32 s50, s49, s7\n" \
"v_mfma_f32_16x16x32_bf16 a[108:111], v[96:99], v[64:67], a[108:111]\n" \
"ds_write_b128 v196, v[200:203]\n" \
"v_mfma_f32_16x16x32_bf16 a[108:111], v[160:163], v[128:131], a[108:111]\n" \
"s_lshl_b32 s7, s3, 11\n" \
"v_lshrrev_b32_e32 v215, 4, v20\n" \
"v_mfma_f32_16x16x32_bf16 a[104:107], v[92:95], v[64:67], a[104:107]\n" \
"ds_read_b64_tr_b16 v[108:109], v38 offset:32768\n" \
"v_bfe_u32 v216, v20, 4, 3\n" \
"v_mfma_f32_16x16x32_bf16 a[104:107], v[156:159], v[128:131], a[104:107]\n" \
"s_add_i32 s52, s48, s7\n" \
"s_add_i32 s53, s53, s7\n" \
"v_mfma_f32_16x16x32_bf16 a[100:103], v[88:91], v[64:67], a[100:103]\n" \
"v_mul_u32_u24_e32 v214, 0x480, v214\n" \
"v_bitop3_b32 v218, v213, v215, 7 bitop3:0x78\n" \
"v_mfma_f32_16x16x32_bf16 a[100:103], v[152:155], v[128:131], a[100:103]\n" \
"ds_read_b64_tr_b16 v[110:111], v38 offset:33792\n" \
"v_bitop3_b32 v213, v213, v216, 4 bitop3:0x36\n" \
"v_mfma_f32_16x16x32_bf16 a[96:99], v[84:87], v[64:67], a[96:99]\n" \
"s_lshl_b32 s7, s3, 10\n" \
"s_or_b32 s4, s43, s4\n" \
"v_mfma_f32_16x16x32_bf16 a[96:99], v[148:151], v[128:131], a[96:99]\n" \
"v_lshl_or_b32 v25, v213, 4, v214\n" \
"v_mul_u32_u24_e32 v213, 0x480, v217\n" \
"v_mfma_f32_16x16x32_bf16 a[128:131], v[84:87], v[68:71], a[128:131]\n" \
"ds_write_b128 v197, v[204:207]\n" \
"v_mfma_f32_16x16x32_bf16 a[128:131], v[148:151], v[132:135], a[128:131]\n" \
"ds_read_b64_tr_b16 v[172:173], v38 offset:40960\n" \
"v_bitop3_b32 v212, v215, v212, 7 bitop3:0x6c\n" \
"v_mfma_f32_16x16x32_bf16 a[132:135], v[88:91], v[68:71], a[132:135]\n" \
"s_or_b32 s40, s7, 0x14000\n" \
"s_lshl_b32 s43, s44, 13\n" \
"v_mfma_f32_16x16x32_bf16 a[132:135], v[152:155], v[132:135], a[132:135]\n" \
"ds_read_b64_tr_b16 v[174:175], v38 offset:41984\n" \
"s_mulk_i32 s44, 0xe800\n" \
"v_mfma_f32_16x16x32_bf16 a[136:139], v[92:95], v[68:71], a[136:139]\n" \
"s_lshl_b32 s7, s3, 8\n" \
"s_nop 0\n" \
"v_lshl_or_b32 v24, v218, 4, v214\n" \
"v_mfma_f32_16x16x32_bf16 a[136:139], v[156:159], v[132:135], a[136:139]\n" \
"v_lshl_or_b32 v21, v212, 4, v213\n" \
"s_add_i32 s44, s44, 0x17000\n" \
"v_mfma_f32_16x16x32_bf16 a[140:143], v[96:99], v[68:71], a[140:143]\n" \
"s_or_b32 s45, s7, 0x27000\n" \
"s_or_b32 s46, s4, 0x10000\n" \
"v_mfma_f32_16x16x32_bf16 a[140:143], v[160:163], v[132:135], a[140:143]\n" \
"ds_read_b64_tr_b16 v[112:113], v39 offset:32768\n" \
"s_add_i32 s48, s6, 0x3000000\n" \
"v_mfma_f32_16x16x32_bf16 a[172:175], v[96:99], v[72:75], a[172:175]\n" \
"ds_write_b128 v198, v[208:211]\n" \
"v_mfma_f32_16x16x32_bf16 a[172:175], v[160:163], v[136:139], a[172:175]\n" \
"s_add_i32 s49, s5, 0x3004000\n" \
"s_add_i32 s52, s52, 0x1010000\n" \
"v_mfma_f32_16x16x32_bf16 a[168:171], v[92:95], v[72:75], a[168:171]\n" \
"ds_read_b64_tr_b16 v[114:115], v39 offset:33792\n" \
"v_mfma_f32_16x16x32_bf16 a[168:171], v[156:159], v[136:139], a[168:171]\n" \
"v_mfma_f32_16x16x32_bf16 a[164:167], v[88:91], v[72:75], a[164:167]\n" \
"v_mfma_f32_16x16x32_bf16 a[164:167], v[152:155], v[136:139], a[164:167]\n" \
"ds_read_b64_tr_b16 v[176:177], v39 offset:40960\n" \
"v_mfma_f32_16x16x32_bf16 a[160:163], v[84:87], v[72:75], a[160:163]\n" \
"v_mfma_f32_16x16x32_bf16 a[160:163], v[148:151], v[136:139], a[160:163]\n" \
"v_mfma_f32_16x16x32_bf16 a[192:195], v[84:87], v[76:79], a[192:195]\n" \
"ds_read_b64_tr_b16 v[178:179], v39 offset:41984\n" \
"v_mfma_f32_16x16x32_bf16 a[192:195], v[148:151], v[140:143], a[192:195]\n" \
"v_mfma_f32_16x16x32_bf16 a[196:199], v[88:91], v[76:79], a[196:199]\n" \
"v_mfma_f32_16x16x32_bf16 a[196:199], v[152:155], v[140:143], a[196:199]\n" \
"v_mfma_f32_16x16x32_bf16 a[200:203], v[92:95], v[76:79], a[200:203]\n" \
"v_mfma_f32_16x16x32_bf16 a[200:203], v[156:159], v[140:143], a[200:203]\n" \
"v_mfma_f32_16x16x32_bf16 a[204:207], v[96:99], v[76:79], a[204:207]\n" \
"v_mfma_f32_16x16x32_bf16 a[204:207], v[160:163], v[140:143], a[204:207]\n" \
"s_waitcnt lgkmcnt(15)\n" \
"v_mfma_f32_16x16x32_bf16 a[236:239], v[96:99], v[80:83], a[236:239]\n" \
"v_mfma_f32_16x16x32_bf16 a[236:239], v[160:163], v[144:147], a[236:239]\n" \
"v_mfma_f32_16x16x32_bf16 a[232:235], v[92:95], v[80:83], a[232:235]\n" \
"v_mfma_f32_16x16x32_bf16 a[232:235], v[156:159], v[144:147], a[232:235]\n" \
"v_mfma_f32_16x16x32_bf16 a[228:231], v[88:91], v[80:83], a[228:231]\n" \
"v_mfma_f32_16x16x32_bf16 a[228:231], v[152:155], v[144:147], a[228:231]\n" \
"v_mfma_f32_16x16x32_bf16 a[224:227], v[84:87], v[80:83], a[224:227]\n" \
"v_mfma_f32_16x16x32_bf16 a[224:227], v[148:151], v[144:147], a[224:227]\n" \
"s_waitcnt vmcnt(1) lgkmcnt(0)\n" \
"s_barrier\n" \
"s_nop 0\n" \
"v_mfma_f32_16x16x32_bf16 a[16:19], v[100:103], v[52:55], a[16:19]\n" \
"s_add_u32 m0, s30, 0x8000\n" \
"s_mov_b32 s85, 0x280\n" \
"v_mfma_f32_16x16x32_bf16 a[16:19], v[164:167], v[116:119], a[16:19]\n" \
"buffer_load_dwordx4 v22, s[64:67], s85 offen lds\n" \
"v_mfma_f32_16x16x32_bf16 a[20:23], v[104:107], v[52:55], a[20:23]\n" \
"s_add_u32 m0, s30, 0x8400\n" \
"s_mov_b32 s84, 0x2280\n" \
"v_mfma_f32_16x16x32_bf16 a[20:23], v[168:171], v[116:119], a[20:23]\n" \
"buffer_load_dwordx4 v23, s[64:67], s84 offen lds\n" \
"v_mfma_f32_16x16x32_bf16 a[24:27], v[108:111], v[52:55], a[24:27]\n" \
"ds_read_b64_tr_b16 v[84:85], v32 offset:0\n" \
"v_mfma_f32_16x16x32_bf16 a[24:27], v[172:175], v[116:119], a[24:27]\n" \
"s_add_u32 m0, s31, 0x8000\n" \
"s_mov_b32 s85, 0x280000\n" \
"v_mfma_f32_16x16x32_bf16 a[28:31], v[112:115], v[52:55], a[28:31]\n" \
"buffer_load_dwordx4 v26, s[68:71], s85 offen lds\n" \
"v_mfma_f32_16x16x32_bf16 a[28:31], v[176:179], v[116:119], a[28:31]\n" \
"ds_read_b128 v[52:55], v28 offset:16384\n" \
"v_mfma_f32_16x16x32_bf16 a[60:63], v[112:115], v[56:59], a[60:63]\n" \
"ds_read_b128 v[116:119], v29 offset:16384\n" \
"v_mfma_f32_16x16x32_bf16 a[60:63], v[176:179], v[120:123], a[60:63]\n" \
"s_add_u32 m0, s31, 0xc000\n" \
"s_mov_b32 s84, 0x280000\n" \
"v_mfma_f32_16x16x32_bf16 a[56:59], v[108:111], v[56:59], a[56:59]\n" \
"buffer_load_dwordx4 v26, s[72:75], s84 offen lds\n" \
"v_mfma_f32_16x16x32_bf16 a[56:59], v[172:175], v[120:123], a[56:59]\n" \
"s_add_u32 m0, s30, 0x8800\n" \
"s_mov_b32 s85, 0x4280\n" \
"v_mfma_f32_16x16x32_bf16 a[52:55], v[104:107], v[56:59], a[52:55]\n" \
"buffer_load_dwordx4 v22, s[64:67], s85 offen lds\n" \
"v_mfma_f32_16x16x32_bf16 a[52:55], v[168:171], v[120:123], a[52:55]\n" \
"ds_read_b64_tr_b16 v[86:87], v32 offset:1024\n" \
"v_mfma_f32_16x16x32_bf16 a[48:51], v[100:103], v[56:59], a[48:51]\n" \
"ds_read_b64_tr_b16 v[148:149], v32 offset:8192\n" \
"v_mfma_f32_16x16x32_bf16 a[48:51], v[164:167], v[120:123], a[48:51]\n" \
"ds_read_b128 v[56:59], v28 offset:18432\n" \
"v_mfma_f32_16x16x32_bf16 a[80:83], v[100:103], v[60:63], a[80:83]\n" \
"ds_read_b128 v[120:123], v29 offset:18432\n" \
"v_mfma_f32_16x16x32_bf16 a[80:83], v[164:167], v[124:127], a[80:83]\n" \
"s_add_u32 m0, s30, 0x8c00\n" \
"s_mov_b32 s84, 0x6280\n" \
"v_mfma_f32_16x16x32_bf16 a[84:87], v[104:107], v[60:63], a[84:87]\n" \
"buffer_load_dwordx4 v23, s[64:67], s84 offen lds\n" \
"v_mfma_f32_16x16x32_bf16 a[84:87], v[168:171], v[124:127], a[84:87]\n" \
"s_add_u32 m0, s31, 0x8400\n" \
"s_mov_b32 s85, 0x288000\n" \
"v_mfma_f32_16x16x32_bf16 a[88:91], v[108:111], v[60:63], a[88:91]\n" \
"buffer_load_dwordx4 v26, s[68:71], s85 offen lds\n" \
"v_mfma_f32_16x16x32_bf16 a[88:91], v[172:175], v[124:127], a[88:91]\n" \
"s_add_u32 m0, s31, 0xc400\n" \
"s_mov_b32 s84, 0x288000\n" \
"v_mfma_f32_16x16x32_bf16 a[92:95], v[112:115], v[60:63], a[92:95]\n" \
"buffer_load_dwordx4 v26, s[72:75], s84 offen lds\n" \
"v_mfma_f32_16x16x32_bf16 a[92:95], v[176:179], v[124:127], a[92:95]\n" \
"ds_read_b128 v[60:63], v28 offset:20480\n" \
"v_mfma_f32_16x16x32_bf16 a[124:127], v[112:115], v[64:67], a[124:127]\n" \
"ds_read_b128 v[124:127], v29 offset:20480\n" \
"v_mfma_f32_16x16x32_bf16 a[124:127], v[176:179], v[128:131], a[124:127]\n" \
"s_add_u32 m0, s30, 0x9000\n" \
"s_mov_b32 s85, 0x8280\n" \
"v_mfma_f32_16x16x32_bf16 a[120:123], v[108:111], v[64:67], a[120:123]\n" \
"buffer_load_dwordx4 v22, s[64:67], s85 offen lds\n" \
"v_mfma_f32_16x16x32_bf16 a[120:123], v[172:175], v[128:131], a[120:123]\n" \
"s_add_u32 m0, s30, 0x9400\n" \
"s_mov_b32 s84, 0xa280\n" \
"v_mfma_f32_16x16x32_bf16 a[116:119], v[104:107], v[64:67], a[116:119]\n" \
"buffer_load_dwordx4 v23, s[64:67], s84 offen lds\n" \
"v_mfma_f32_16x16x32_bf16 a[116:119], v[168:171], v[128:131], a[116:119]\n" \
"s_add_u32 m0, s31, 0x8800\n" \
"s_mov_b32 s85, 0x290000\n" \
"v_mfma_f32_16x16x32_bf16 a[112:115], v[100:103], v[64:67], a[112:115]\n" \
"buffer_load_dwordx4 v27, s[68:71], s85 offen lds\n" \
"v_mfma_f32_16x16x32_bf16 a[112:115], v[164:167], v[128:131], a[112:115]\n" \
"ds_read_b128 v[64:67], v28 offset:22528\n" \
"v_mfma_f32_16x16x32_bf16 a[144:147], v[100:103], v[68:71], a[144:147]\n" \
"ds_read_b128 v[128:131], v29 offset:22528\n" \
"v_mfma_f32_16x16x32_bf16 a[144:147], v[164:167], v[132:135], a[144:147]\n" \
"s_add_u32 m0, s31, 0xc800\n" \
"s_mov_b32 s84, 0x290000\n" \
"v_mfma_f32_16x16x32_bf16 a[148:151], v[104:107], v[68:71], a[148:151]\n" \
"buffer_load_dwordx4 v27, s[72:75], s84 offen lds\n" \
"v_mfma_f32_16x16x32_bf16 a[148:151], v[168:171], v[132:135], a[148:151]\n" \
"s_add_u32 m0, s30, 0x9800\n" \
"s_mov_b32 s85, 0xc280\n" \
"v_mfma_f32_16x16x32_bf16 a[152:155], v[108:111], v[68:71], a[152:155]\n" \
"buffer_load_dwordx4 v22, s[64:67], s85 offen lds\n" \
"v_mfma_f32_16x16x32_bf16 a[152:155], v[172:175], v[132:135], a[152:155]\n" \
"s_add_u32 m0, s30, 0x9c00\n" \
"s_mov_b32 s84, 0xe280\n" \
"v_mfma_f32_16x16x32_bf16 a[156:159], v[112:115], v[68:71], a[156:159]\n" \
"buffer_load_dwordx4 v23, s[64:67], s84 offen lds\n" \
"v_mfma_f32_16x16x32_bf16 a[156:159], v[176:179], v[132:135], a[156:159]\n" \
"ds_read_b128 v[68:71], v28 offset:0\n" \
"v_mfma_f32_16x16x32_bf16 a[188:191], v[112:115], v[72:75], a[188:191]\n" \
"ds_read_b128 v[132:135], v29 offset:0\n" \
"v_mfma_f32_16x16x32_bf16 a[188:191], v[176:179], v[136:139], a[188:191]\n" \
"ds_read_b128 v[200:203], v199 offset:0\n" \
"v_mfma_f32_16x16x32_bf16 a[184:187], v[108:111], v[72:75], a[184:187]\n" \
"ds_read_b128 v[204:207], v199 offset:4096\n" \
"v_mfma_f32_16x16x32_bf16 a[184:187], v[172:175], v[136:139], a[184:187]\n" \
"ds_read_b64_tr_b16 v[150:151], v32 offset:9216\n" \
"v_mfma_f32_16x16x32_bf16 a[180:183], v[104:107], v[72:75], a[180:183]\n" \
"ds_read_b64_tr_b16 v[88:89], v33 offset:0\n" \
"v_mfma_f32_16x16x32_bf16 a[180:183], v[168:171], v[136:139], a[180:183]\n" \
"ds_read_b128 v[208:211], v199 offset:8192\n" \
"v_mfma_f32_16x16x32_bf16 a[176:179], v[100:103], v[72:75], a[176:179]\n" \
"ds_read_b64_tr_b16 v[90:91], v33 offset:1024\n" \
"v_mfma_f32_16x16x32_bf16 a[176:179], v[164:167], v[136:139], a[176:179]\n" \
"ds_read_b128 v[72:75], v28 offset:2048\n" \
"v_mfma_f32_16x16x32_bf16 a[208:211], v[100:103], v[76:79], a[208:211]\n" \
"ds_read_b128 v[136:139], v29 offset:2048\n" \
"v_mfma_f32_16x16x32_bf16 a[208:211], v[164:167], v[140:143], a[208:211]\n" \
"ds_read_b64_tr_b16 v[152:153], v33 offset:8192\n" \
"v_mfma_f32_16x16x32_bf16 a[212:215], v[104:107], v[76:79], a[212:215]\n" \
"ds_read_b64_tr_b16 v[154:155], v33 offset:9216\n" \
"v_mfma_f32_16x16x32_bf16 a[212:215], v[168:171], v[140:143], a[212:215]\n" \
"ds_read_b64_tr_b16 v[92:93], v34 offset:0\n" \
"v_mfma_f32_16x16x32_bf16 a[216:219], v[108:111], v[76:79], a[216:219]\n" \
"ds_read_b64_tr_b16 v[94:95], v34 offset:1024\n" \
"v_mfma_f32_16x16x32_bf16 a[216:219], v[172:175], v[140:143], a[216:219]\n" \
"ds_read_b64_tr_b16 v[156:157], v34 offset:8192\n" \
"v_mfma_f32_16x16x32_bf16 a[220:223], v[112:115], v[76:79], a[220:223]\n" \
"ds_read_b64_tr_b16 v[158:159], v34 offset:9216\n" \
"v_mfma_f32_16x16x32_bf16 a[220:223], v[176:179], v[140:143], a[220:223]\n" \
"ds_read_b128 v[76:79], v28 offset:4096\n" \
"v_mfma_f32_16x16x32_bf16 a[252:255], v[112:115], v[80:83], a[252:255]\n" \
"ds_read_b128 v[140:143], v29 offset:4096\n" \
"v_mfma_f32_16x16x32_bf16 a[252:255], v[176:179], v[144:147], a[252:255]\n" \
"ds_read_b64_tr_b16 v[96:97], v35 offset:0\n" \
"v_mfma_f32_16x16x32_bf16 a[248:251], v[108:111], v[80:83], a[248:251]\n" \
"ds_read_b64_tr_b16 v[98:99], v35 offset:1024\n" \
"v_mfma_f32_16x16x32_bf16 a[248:251], v[172:175], v[144:147], a[248:251]\n" \
"ds_read_b64_tr_b16 v[160:161], v35 offset:8192\n" \
"v_mfma_f32_16x16x32_bf16 a[244:247], v[104:107], v[80:83], a[244:247]\n" \
"ds_read_b64_tr_b16 v[162:163], v35 offset:9216\n" \
"v_mfma_f32_16x16x32_bf16 a[244:247], v[168:171], v[144:147], a[244:247]\n" \
"v_mfma_f32_16x16x32_bf16 a[240:243], v[100:103], v[80:83], a[240:243]\n" \
"v_mfma_f32_16x16x32_bf16 a[240:243], v[164:167], v[144:147], a[240:243]\n" \
"ds_read_b128 v[80:83], v28 offset:6144\n" \
"ds_read_b128 v[144:147], v29 offset:6144\n" \
"s_waitcnt lgkmcnt(15)\n" \
"v_mfma_f32_16x16x32_bf16 a[0:3], v[84:87], v[52:55], a[0:3]\n" \
"s_add_u32 m0, s31, 0x8c00\n" \
"s_mov_b32 s85, 0x298000\n" \
"v_mfma_f32_16x16x32_bf16 a[0:3], v[148:151], v[116:119], a[0:3]\n" \
"buffer_load_dwordx4 v27, s[68:71], s85 offen lds\n" \
"v_mfma_f32_16x16x32_bf16 a[4:7], v[88:91], v[52:55], a[4:7]\n" \
"ds_read_b64_tr_b16 v[100:101], v36 offset:0\n" \
"s_waitcnt lgkmcnt(13)\n" \
"v_mfma_f32_16x16x32_bf16 a[4:7], v[152:155], v[116:119], a[4:7]\n" \
"s_add_u32 m0, s31, 0xcc00\n" \
"s_mov_b32 s84, 0x298000\n" \
"s_waitcnt lgkmcnt(11)\n" \
"v_mfma_f32_16x16x32_bf16 a[8:11], v[92:95], v[52:55], a[8:11]\n" \
"buffer_load_dwordx4 v27, s[72:75], s84 offen lds\n" \
"s_waitcnt lgkmcnt(9)\n" \
"v_mfma_f32_16x16x32_bf16 a[8:11], v[156:159], v[116:119], a[8:11]\n" \
"ds_read_b64_tr_b16 v[102:103], v36 offset:1024\n" \
"s_waitcnt lgkmcnt(6)\n" \
"v_mfma_f32_16x16x32_bf16 a[12:15], v[96:99], v[52:55], a[12:15]\n" \
"ds_read_b64_tr_b16 v[164:165], v36 offset:8192\n" \
"s_waitcnt lgkmcnt(5)\n" \
"v_mfma_f32_16x16x32_bf16 a[12:15], v[160:163], v[116:119], a[12:15]\n" \
"v_mfma_f32_16x16x32_bf16 a[44:47], v[96:99], v[56:59], a[44:47]\n" \
"v_mfma_f32_16x16x32_bf16 a[44:47], v[160:163], v[120:123], a[44:47]\n" \
"ds_read_b64_tr_b16 v[166:167], v36 offset:9216\n" \
"v_mfma_f32_16x16x32_bf16 a[40:43], v[92:95], v[56:59], a[40:43]\n" \
"v_mfma_f32_16x16x32_bf16 a[40:43], v[156:159], v[120:123], a[40:43]\n" \
"v_mfma_f32_16x16x32_bf16 a[36:39], v[88:91], v[56:59], a[36:39]\n" \
"v_mfma_f32_16x16x32_bf16 a[36:39], v[152:155], v[120:123], a[36:39]\n" \
"ds_read_b64_tr_b16 v[104:105], v37 offset:0\n" \
"v_mfma_f32_16x16x32_bf16 a[32:35], v[84:87], v[56:59], a[32:35]\n" \
"v_mfma_f32_16x16x32_bf16 a[32:35], v[148:151], v[120:123], a[32:35]\n" \
"v_mfma_f32_16x16x32_bf16 a[64:67], v[84:87], v[60:63], a[64:67]\n" \
"ds_read_b64_tr_b16 v[106:107], v37 offset:1024\n" \
"v_mfma_f32_16x16x32_bf16 a[64:67], v[148:151], v[124:127], a[64:67]\n" \
"v_mfma_f32_16x16x32_bf16 a[68:71], v[88:91], v[60:63], a[68:71]\n" \
"v_mfma_f32_16x16x32_bf16 a[68:71], v[152:155], v[124:127], a[68:71]\n" \
"buffer_store_dwordx4 v[200:203], v20, s[80:83], s35 offen sc1\n" \
"v_mfma_f32_16x16x32_bf16 a[72:75], v[92:95], v[60:63], a[72:75]\n" \
"s_mov_b32 m0, s32\n" \
"ds_read_b64_tr_b16 v[168:169], v37 offset:8192\n" \
"v_mfma_f32_16x16x32_bf16 a[72:75], v[156:159], v[124:127], a[72:75]\n" \
"s_mov_b32 exec_lo, 0x0\n" \
"s_mov_b32 exec_hi, 0xffffffff\n" \
"global_load_lds_dword v[188:189], off\n" \
"s_mov_b64 exec, -1\n" \
"v_mfma_f32_16x16x32_bf16 a[76:79], v[96:99], v[60:63], a[76:79]\n" \
"ds_read_b64_tr_b16 v[170:171], v37 offset:9216\n" \
"v_mfma_f32_16x16x32_bf16 a[76:79], v[160:163], v[124:127], a[76:79]\n" \
"v_mfma_f32_16x16x32_bf16 a[108:111], v[96:99], v[64:67], a[108:111]\n" \
"v_mfma_f32_16x16x32_bf16 a[108:111], v[160:163], v[128:131], a[108:111]\n" \
"v_mfma_f32_16x16x32_bf16 a[104:107], v[92:95], v[64:67], a[104:107]\n" \
"ds_read_b64_tr_b16 v[108:109], v38 offset:0\n" \
"v_mfma_f32_16x16x32_bf16 a[104:107], v[156:159], v[128:131], a[104:107]\n" \
"v_mfma_f32_16x16x32_bf16 a[100:103], v[88:91], v[64:67], a[100:103]\n" \
"buffer_store_dwordx4 v[204:207], v20, s[80:83], s88 offen sc1\n" \
"v_mfma_f32_16x16x32_bf16 a[100:103], v[152:155], v[128:131], a[100:103]\n" \
"ds_read_b64_tr_b16 v[110:111], v38 offset:1024\n" \
"v_mfma_f32_16x16x32_bf16 a[96:99], v[84:87], v[64:67], a[96:99]\n" \
"v_mfma_f32_16x16x32_bf16 a[96:99], v[148:151], v[128:131], a[96:99]\n" \
"v_mfma_f32_16x16x32_bf16 a[128:131], v[84:87], v[68:71], a[128:131]\n" \
"ds_read_b64_tr_b16 v[172:173], v38 offset:8192\n" \
"v_mfma_f32_16x16x32_bf16 a[128:131], v[148:151], v[132:135], a[128:131]\n" \
"v_mfma_f32_16x16x32_bf16 a[132:135], v[88:91], v[68:71], a[132:135]\n" \
"v_mfma_f32_16x16x32_bf16 a[132:135], v[152:155], v[132:135], a[132:135]\n" \
"ds_read_b64_tr_b16 v[174:175], v38 offset:9216\n" \
"v_mfma_f32_16x16x32_bf16 a[136:139], v[92:95], v[68:71], a[136:139]\n" \
"v_mfma_f32_16x16x32_bf16 a[136:139], v[156:159], v[132:135], a[136:139]\n" \
"v_mfma_f32_16x16x32_bf16 a[140:143], v[96:99], v[68:71], a[140:143]\n" \
"buffer_store_dwordx4 v[208:211], v20, s[80:83], s36 offen sc1\n" \
"v_mfma_f32_16x16x32_bf16 a[140:143], v[160:163], v[132:135], a[140:143]\n" \
"ds_read_b64_tr_b16 v[112:113], v39 offset:0\n" \
"v_mfma_f32_16x16x32_bf16 a[172:175], v[96:99], v[72:75], a[172:175]\n" \
"v_mfma_f32_16x16x32_bf16 a[172:175], v[160:163], v[136:139], a[172:175]\n" \
"v_mfma_f32_16x16x32_bf16 a[168:171], v[92:95], v[72:75], a[168:171]\n" \
"ds_read_b64_tr_b16 v[114:115], v39 offset:1024\n" \
"v_mfma_f32_16x16x32_bf16 a[168:171], v[156:159], v[136:139], a[168:171]\n" \
"v_mfma_f32_16x16x32_bf16 a[164:167], v[88:91], v[72:75], a[164:167]\n" \
"v_mfma_f32_16x16x32_bf16 a[164:167], v[152:155], v[136:139], a[164:167]\n" \
"ds_read_b64_tr_b16 v[176:177], v39 offset:8192\n" \
"v_mfma_f32_16x16x32_bf16 a[160:163], v[84:87], v[72:75], a[160:163]\n" \
"v_mfma_f32_16x16x32_bf16 a[160:163], v[148:151], v[136:139], a[160:163]\n" \
"v_mfma_f32_16x16x32_bf16 a[192:195], v[84:87], v[76:79], a[192:195]\n" \
"ds_read_b64_tr_b16 v[178:179], v39 offset:9216\n" \
"v_mfma_f32_16x16x32_bf16 a[192:195], v[148:151], v[140:143], a[192:195]\n" \
"v_mfma_f32_16x16x32_bf16 a[196:199], v[88:91], v[76:79], a[196:199]\n" \
"v_mfma_f32_16x16x32_bf16 a[196:199], v[152:155], v[140:143], a[196:199]\n" \
"v_mfma_f32_16x16x32_bf16 a[200:203], v[92:95], v[76:79], a[200:203]\n" \
"v_mfma_f32_16x16x32_bf16 a[200:203], v[156:159], v[140:143], a[200:203]\n" \
"v_mfma_f32_16x16x32_bf16 a[204:207], v[96:99], v[76:79], a[204:207]\n" \
"v_mfma_f32_16x16x32_bf16 a[204:207], v[160:163], v[140:143], a[204:207]\n" \
"s_waitcnt lgkmcnt(15)\n" \
"v_mfma_f32_16x16x32_bf16 a[236:239], v[96:99], v[80:83], a[236:239]\n" \
"v_mfma_f32_16x16x32_bf16 a[236:239], v[160:163], v[144:147], a[236:239]\n" \
"v_mfma_f32_16x16x32_bf16 a[232:235], v[92:95], v[80:83], a[232:235]\n" \
"v_mfma_f32_16x16x32_bf16 a[232:235], v[156:159], v[144:147], a[232:235]\n" \
"v_mfma_f32_16x16x32_bf16 a[228:231], v[88:91], v[80:83], a[228:231]\n" \
"v_mfma_f32_16x16x32_bf16 a[228:231], v[152:155], v[144:147], a[228:231]\n" \
"v_mfma_f32_16x16x32_bf16 a[224:227], v[84:87], v[80:83], a[224:227]\n" \
"v_mfma_f32_16x16x32_bf16 a[224:227], v[148:151], v[144:147], a[224:227]\n" \
"s_waitcnt vmcnt(4) lgkmcnt(0)\n" \
"s_barrier\n" \
"v_mfma_f32_16x16x32_bf16 a[16:19], v[100:103], v[52:55], a[16:19]\n" \
"s_add_u32 m0, s30, 0x0\n" \
"s_mov_b32 s85, 0x300\n" \
"v_mfma_f32_16x16x32_bf16 a[16:19], v[164:167], v[116:119], a[16:19]\n" \
"buffer_load_dwordx4 v22, s[64:67], s85 offen lds\n" \
"v_mfma_f32_16x16x32_bf16 a[20:23], v[104:107], v[52:55], a[20:23]\n" \
"ds_read_b64_tr_b16 v[84:85], v32 offset:32768\n" \
"v_mfma_f32_16x16x32_bf16 a[20:23], v[168:171], v[116:119], a[20:23]\n" \
"s_add_u32 m0, s30, 0x400\n" \
"s_mov_b32 s84, 0x2300\n" \
"v_mfma_f32_16x16x32_bf16 a[24:27], v[108:111], v[52:55], a[24:27]\n" \
"buffer_load_dwordx4 v23, s[64:67], s84 offen lds\n" \
"v_mfma_f32_16x16x32_bf16 a[24:27], v[172:175], v[116:119], a[24:27]\n" \
"ds_read_b64_tr_b16 v[86:87], v32 offset:33792\n" \
"v_mfma_f32_16x16x32_bf16 a[28:31], v[112:115], v[52:55], a[28:31]\n" \
"ds_read_b64_tr_b16 v[148:149], v32 offset:40960\n" \
"v_mfma_f32_16x16x32_bf16 a[28:31], v[176:179], v[116:119], a[28:31]\n" \
"ds_read_b128 v[52:55], v28 offset:49152\n" \
"v_mfma_f32_16x16x32_bf16 a[60:63], v[112:115], v[56:59], a[60:63]\n" \
"ds_read_b128 v[116:119], v29 offset:49152\n" \
"v_mfma_f32_16x16x32_bf16 a[60:63], v[176:179], v[120:123], a[60:63]\n" \
"s_add_u32 m0, s31, 0x0\n" \
"s_nop 0\n" \
"s_mov_b32 s85, 0x300000\n" \
"v_mfma_f32_16x16x32_bf16 a[56:59], v[108:111], v[56:59], a[56:59]\n" \
"buffer_load_dwordx4 v26, s[68:71], s85 offen lds\n" \
"v_mfma_f32_16x16x32_bf16 a[56:59], v[172:175], v[120:123], a[56:59]\n" \
"s_add_u32 m0, s31, 0x4000\n" \
"s_mov_b32 s84, 0x300000\n" \
"v_mfma_f32_16x16x32_bf16 a[52:55], v[104:107], v[56:59], a[52:55]\n" \
"buffer_load_dwordx4 v26, s[72:75], s84 offen lds\n" \
"v_mfma_f32_16x16x32_bf16 a[52:55], v[168:171], v[120:123], a[52:55]\n" \
"s_add_u32 m0, s30, 0x800\n" \
"s_mov_b32 s85, 0x4300\n" \
"v_mfma_f32_16x16x32_bf16 a[48:51], v[100:103], v[56:59], a[48:51]\n" \
"buffer_load_dwordx4 v22, s[64:67], s85 offen lds\n" \
"v_mfma_f32_16x16x32_bf16 a[48:51], v[164:167], v[120:123], a[48:51]\n" \
"ds_read_b128 v[56:59], v28 offset:51200\n" \
"v_mfma_f32_16x16x32_bf16 a[80:83], v[100:103], v[60:63], a[80:83]\n" \
"ds_read_b128 v[120:123], v29 offset:51200\n" \
"v_mfma_f32_16x16x32_bf16 a[80:83], v[164:167], v[124:127], a[80:83]\n" \
"s_add_u32 m0, s30, 0xc00\n" \
"s_mov_b32 s84, 0x6300\n" \
"v_mfma_f32_16x16x32_bf16 a[84:87], v[104:107], v[60:63], a[84:87]\n" \
"buffer_load_dwordx4 v23, s[64:67], s84 offen lds\n" \
"v_mfma_f32_16x16x32_bf16 a[84:87], v[168:171], v[124:127], a[84:87]\n" \
"ds_read_b64_tr_b16 v[150:151], v32 offset:41984\n" \
"v_mfma_f32_16x16x32_bf16 a[88:91], v[108:111], v[60:63], a[88:91]\n" \
"s_add_u32 m0, s31, 0x400\n" \
"s_mov_b32 s85, 0x308000\n" \
"v_mfma_f32_16x16x32_bf16 a[88:91], v[172:175], v[124:127], a[88:91]\n" \
"buffer_load_dwordx4 v26, s[68:71], s85 offen lds\n" \
"v_mfma_f32_16x16x32_bf16 a[92:95], v[112:115], v[60:63], a[92:95]\n" \
"ds_read_b64_tr_b16 v[88:89], v33 offset:32768\n" \
"v_mfma_f32_16x16x32_bf16 a[92:95], v[176:179], v[124:127], a[92:95]\n" \
"ds_read_b128 v[60:63], v28 offset:53248\n" \
"v_mfma_f32_16x16x32_bf16 a[124:127], v[112:115], v[64:67], a[124:127]\n" \
"ds_read_b128 v[124:127], v29 offset:53248\n" \
"v_mfma_f32_16x16x32_bf16 a[124:127], v[176:179], v[128:131], a[124:127]\n" \
"s_add_u32 m0, s31, 0x4400\n" \
"s_mov_b32 s84, 0x308000\n" \
"v_mfma_f32_16x16x32_bf16 a[120:123], v[108:111], v[64:67], a[120:123]\n" \
"buffer_load_dwordx4 v26, s[72:75], s84 offen lds\n" \
"v_mfma_f32_16x16x32_bf16 a[120:123], v[172:175], v[128:131], a[120:123]\n" \
"s_add_u32 m0, s30, 0x1000\n" \
"s_mov_b32 s85, 0x8300\n" \
"v_mfma_f32_16x16x32_bf16 a[116:119], v[104:107], v[64:67], a[116:119]\n" \
"buffer_load_dwordx4 v22, s[64:67], s85 offen lds\n" \
"v_mfma_f32_16x16x32_bf16 a[116:119], v[168:171], v[128:131], a[116:119]\n" \
"ds_read_b64_tr_b16 v[90:91], v33 offset:33792\n" \
"v_mfma_f32_16x16x32_bf16 a[112:115], v[100:103], v[64:67], a[112:115]\n" \
"ds_read_b64_tr_b16 v[152:153], v33 offset:40960\n" \
"v_mfma_f32_16x16x32_bf16 a[112:115], v[164:167], v[128:131], a[112:115]\n" \
"ds_read_b128 v[64:67], v28 offset:55296\n" \
"v_mfma_f32_16x16x32_bf16 a[144:147], v[100:103], v[68:71], a[144:147]\n" \
"ds_read_b128 v[128:131], v29 offset:55296\n" \
"v_mfma_f32_16x16x32_bf16 a[144:147], v[164:167], v[132:135], a[144:147]\n" \
"s_add_u32 m0, s30, 0x1400\n" \
"s_mov_b32 s84, 0xa300\n" \
"v_mfma_f32_16x16x32_bf16 a[148:151], v[104:107], v[68:71], a[148:151]\n" \
"buffer_load_dwordx4 v23, s[64:67], s84 offen lds\n" \
"v_mfma_f32_16x16x32_bf16 a[148:151], v[168:171], v[132:135], a[148:151]\n" \
"s_add_u32 m0, s31, 0x800\n" \
"s_mov_b32 s85, 0x310000\n" \
"v_mfma_f32_16x16x32_bf16 a[152:155], v[108:111], v[68:71], a[152:155]\n" \
"buffer_load_dwordx4 v27, s[68:71], s85 offen lds\n" \
"v_mfma_f32_16x16x32_bf16 a[152:155], v[172:175], v[132:135], a[152:155]\n" \
"s_add_u32 m0, s31, 0x4800\n" \
"s_mov_b32 s84, 0x310000\n" \
"v_mfma_f32_16x16x32_bf16 a[156:159], v[112:115], v[68:71], a[156:159]\n" \
"buffer_load_dwordx4 v27, s[72:75], s84 offen lds\n" \
"v_mfma_f32_16x16x32_bf16 a[156:159], v[176:179], v[132:135], a[156:159]\n" \
"ds_read_b128 v[68:71], v28 offset:32768\n" \
"v_mfma_f32_16x16x32_bf16 a[188:191], v[112:115], v[72:75], a[188:191]\n" \
"ds_read_b128 v[132:135], v29 offset:32768\n" \
"v_mfma_f32_16x16x32_bf16 a[188:191], v[176:179], v[136:139], a[188:191]\n" \
"s_add_u32 m0, s30, 0x1800\n" \
"s_mov_b32 s85, 0xc300\n" \
"v_mfma_f32_16x16x32_bf16 a[184:187], v[108:111], v[72:75], a[184:187]\n" \
"buffer_load_dwordx4 v22, s[64:67], s85 offen lds\n" \
"v_mfma_f32_16x16x32_bf16 a[184:187], v[172:175], v[136:139], a[184:187]\n" \
"ds_read_b64_tr_b16 v[154:155], v33 offset:41984\n" \
"v_mfma_f32_16x16x32_bf16 a[180:183], v[104:107], v[72:75], a[180:183]\n" \
"s_add_u32 m0, s30, 0x1c00\n" \
"s_mov_b32 s84, 0xe300\n" \
"v_mfma_f32_16x16x32_bf16 a[180:183], v[168:171], v[136:139], a[180:183]\n" \
"buffer_load_dwordx4 v23, s[64:67], s84 offen lds\n" \
"v_mfma_f32_16x16x32_bf16 a[176:179], v[100:103], v[72:75], a[176:179]\n" \
"ds_read_b64_tr_b16 v[92:93], v34 offset:32768\n" \
"v_mfma_f32_16x16x32_bf16 a[176:179], v[164:167], v[136:139], a[176:179]\n" \
"ds_read_b128 v[72:75], v28 offset:34816\n" \
"v_mfma_f32_16x16x32_bf16 a[208:211], v[100:103], v[76:79], a[208:211]\n" \
"ds_read_b128 v[136:139], v29 offset:34816\n" \
"v_mfma_f32_16x16x32_bf16 a[208:211], v[164:167], v[140:143], a[208:211]\n" \
"ds_read_b64_tr_b16 v[94:95], v34 offset:33792\n" \
"v_mfma_f32_16x16x32_bf16 a[212:215], v[104:107], v[76:79], a[212:215]\n" \
"ds_read_b64_tr_b16 v[156:157], v34 offset:40960\n" \
"v_mfma_f32_16x16x32_bf16 a[212:215], v[168:171], v[140:143], a[212:215]\n" \
"ds_read_b64_tr_b16 v[158:159], v34 offset:41984\n" \
"v_mfma_f32_16x16x32_bf16 a[216:219], v[108:111], v[76:79], a[216:219]\n" \
"ds_read_b64_tr_b16 v[96:97], v35 offset:32768\n" \
"v_mfma_f32_16x16x32_bf16 a[216:219], v[172:175], v[140:143], a[216:219]\n" \
"ds_read_b64_tr_b16 v[98:99], v35 offset:33792\n" \
"v_mfma_f32_16x16x32_bf16 a[220:223], v[112:115], v[76:79], a[220:223]\n" \
"ds_read_b64_tr_b16 v[160:161], v35 offset:40960\n" \
"v_mfma_f32_16x16x32_bf16 a[220:223], v[176:179], v[140:143], a[220:223]\n" \
"ds_read_b128 v[76:79], v28 offset:36864\n" \
"v_mfma_f32_16x16x32_bf16 a[252:255], v[112:115], v[80:83], a[252:255]\n" \
"ds_read_b128 v[140:143], v29 offset:36864\n" \
"v_mfma_f32_16x16x32_bf16 a[252:255], v[176:179], v[144:147], a[252:255]\n" \
"ds_read_b64_tr_b16 v[162:163], v35 offset:41984\n" \
"v_mfma_f32_16x16x32_bf16 a[248:251], v[108:111], v[80:83], a[248:251]\n" \
"v_mfma_f32_16x16x32_bf16 a[248:251], v[172:175], v[144:147], a[248:251]\n" \
"v_mfma_f32_16x16x32_bf16 a[244:247], v[104:107], v[80:83], a[244:247]\n" \
"v_mfma_f32_16x16x32_bf16 a[244:247], v[168:171], v[144:147], a[244:247]\n" \
"v_mfma_f32_16x16x32_bf16 a[240:243], v[100:103], v[80:83], a[240:243]\n" \
"v_mfma_f32_16x16x32_bf16 a[240:243], v[164:167], v[144:147], a[240:243]\n" \
"ds_read_b128 v[80:83], v28 offset:38912\n" \
"ds_read_b128 v[144:147], v29 offset:38912\n" \
"s_waitcnt lgkmcnt(15)\n" \
"v_mfma_f32_16x16x32_bf16 a[0:3], v[84:87], v[52:55], a[0:3]\n" \
"s_add_u32 m0, s31, 0xc00\n" \
"s_mov_b32 s85, 0x318000\n" \
"v_mfma_f32_16x16x32_bf16 a[0:3], v[148:151], v[116:119], a[0:3]\n" \
"buffer_load_dwordx4 v27, s[68:71], s85 offen lds\n" \
"v_mfma_f32_16x16x32_bf16 a[4:7], v[88:91], v[52:55], a[4:7]\n" \
"ds_read_b64_tr_b16 v[100:101], v36 offset:32768\n" \
"s_waitcnt lgkmcnt(15)\n" \
"v_mfma_f32_16x16x32_bf16 a[4:7], v[152:155], v[116:119], a[4:7]\n" \
"s_add_u32 m0, s31, 0x4c00\n" \
"s_mov_b32 s84, 0x318000\n" \
"s_waitcnt lgkmcnt(11)\n" \
"v_mfma_f32_16x16x32_bf16 a[8:11], v[92:95], v[52:55], a[8:11]\n" \
"buffer_load_dwordx4 v27, s[72:75], s84 offen lds\n" \
"s_waitcnt lgkmcnt(9)\n" \
"v_mfma_f32_16x16x32_bf16 a[8:11], v[156:159], v[116:119], a[8:11]\n" \
"ds_read_b64_tr_b16 v[102:103], v36 offset:33792\n" \
"s_waitcnt lgkmcnt(8)\n" \
"v_mfma_f32_16x16x32_bf16 a[12:15], v[96:99], v[52:55], a[12:15]\n" \
"ds_read_b64_tr_b16 v[164:165], v36 offset:40960\n" \
"s_waitcnt lgkmcnt(5)\n" \
"v_mfma_f32_16x16x32_bf16 a[12:15], v[160:163], v[116:119], a[12:15]\n" \
"v_mfma_f32_16x16x32_bf16 a[44:47], v[96:99], v[56:59], a[44:47]\n" \
"v_mfma_f32_16x16x32_bf16 a[44:47], v[160:163], v[120:123], a[44:47]\n" \
"ds_read_b64_tr_b16 v[166:167], v36 offset:41984\n" \
"v_mfma_f32_16x16x32_bf16 a[40:43], v[92:95], v[56:59], a[40:43]\n" \
"v_mfma_f32_16x16x32_bf16 a[40:43], v[156:159], v[120:123], a[40:43]\n" \
"v_mfma_f32_16x16x32_bf16 a[36:39], v[88:91], v[56:59], a[36:39]\n" \
"v_mfma_f32_16x16x32_bf16 a[36:39], v[152:155], v[120:123], a[36:39]\n" \
"ds_read_b64_tr_b16 v[104:105], v37 offset:32768\n" \
"v_mfma_f32_16x16x32_bf16 a[32:35], v[84:87], v[56:59], a[32:35]\n" \
"v_mfma_f32_16x16x32_bf16 a[32:35], v[148:151], v[120:123], a[32:35]\n" \
"v_mfma_f32_16x16x32_bf16 a[64:67], v[84:87], v[60:63], a[64:67]\n" \
"ds_read_b64_tr_b16 v[106:107], v37 offset:33792\n" \
"v_mfma_f32_16x16x32_bf16 a[64:67], v[148:151], v[124:127], a[64:67]\n" \
"v_mfma_f32_16x16x32_bf16 a[68:71], v[88:91], v[60:63], a[68:71]\n" \
"v_lshlrev_b32_e32 v45, 3, v20\n" \
"v_add_u32_e32 v45, s31, v45\n" \
"v_mfma_f32_16x16x32_bf16 a[68:71], v[152:155], v[124:127], a[68:71]\n" \
"s_getpc_b64 s[86:87]\n" \
"s_sub_u32 s86, s86, 0xf800\n" \
"s_subb_u32 s87, s87, 0\n" \
"s_mov_b32 m0, s32\n" \
"s_nop 0\n" \
"v_mfma_f32_16x16x32_bf16 a[72:75], v[92:95], v[60:63], a[72:75]\n" \
"global_load_lds_dword v45, s[86:87]\n" \
"v_mfma_f32_16x16x32_bf16 a[72:75], v[156:159], v[124:127], a[72:75]\n" \
"ds_read_b64_tr_b16 v[168:169], v37 offset:40960\n" \
"v_mfma_f32_16x16x32_bf16 a[76:79], v[96:99], v[60:63], a[76:79]\n" \
"ds_read_b64_tr_b16 v[170:171], v37 offset:41984\n" \
"v_mfma_f32_16x16x32_bf16 a[76:79], v[160:163], v[124:127], a[76:79]\n" \
"v_mfma_f32_16x16x32_bf16 a[108:111], v[96:99], v[64:67], a[108:111]\n" \
"v_mfma_f32_16x16x32_bf16 a[108:111], v[160:163], v[128:131], a[108:111]\n" \
"v_mfma_f32_16x16x32_bf16 a[104:107], v[92:95], v[64:67], a[104:107]\n" \
"ds_read_b64_tr_b16 v[108:109], v38 offset:32768\n" \
"v_mfma_f32_16x16x32_bf16 a[104:107], v[156:159], v[128:131], a[104:107]\n" \
"v_mfma_f32_16x16x32_bf16 a[100:103], v[88:91], v[64:67], a[100:103]\n" \
"v_mfma_f32_16x16x32_bf16 a[100:103], v[152:155], v[128:131], a[100:103]\n" \
"ds_read_b64_tr_b16 v[110:111], v38 offset:33792\n" \
"v_mfma_f32_16x16x32_bf16 a[96:99], v[84:87], v[64:67], a[96:99]\n" \
"v_mfma_f32_16x16x32_bf16 a[96:99], v[148:151], v[128:131], a[96:99]\n" \
"v_mfma_f32_16x16x32_bf16 a[128:131], v[84:87], v[68:71], a[128:131]\n" \
"ds_read_b64_tr_b16 v[172:173], v38 offset:40960\n" \
"v_mfma_f32_16x16x32_bf16 a[128:131], v[148:151], v[132:135], a[128:131]\n" \
"v_mfma_f32_16x16x32_bf16 a[132:135], v[88:91], v[68:71], a[132:135]\n" \
"v_mfma_f32_16x16x32_bf16 a[132:135], v[152:155], v[132:135], a[132:135]\n" \
"ds_read_b64_tr_b16 v[174:175], v38 offset:41984\n" \
"v_mfma_f32_16x16x32_bf16 a[136:139], v[92:95], v[68:71], a[136:139]\n" \
"v_mfma_f32_16x16x32_bf16 a[136:139], v[156:159], v[132:135], a[136:139]\n" \
"v_mfma_f32_16x16x32_bf16 a[140:143], v[96:99], v[68:71], a[140:143]\n" \
"v_mfma_f32_16x16x32_bf16 a[140:143], v[160:163], v[132:135], a[140:143]\n" \
"ds_read_b64_tr_b16 v[112:113], v39 offset:32768\n" \
"v_mfma_f32_16x16x32_bf16 a[172:175], v[96:99], v[72:75], a[172:175]\n" \
"v_mfma_f32_16x16x32_bf16 a[172:175], v[160:163], v[136:139], a[172:175]\n" \
"v_mfma_f32_16x16x32_bf16 a[168:171], v[92:95], v[72:75], a[168:171]\n" \
"ds_read_b64_tr_b16 v[114:115], v39 offset:33792\n" \
"v_mfma_f32_16x16x32_bf16 a[168:171], v[156:159], v[136:139], a[168:171]\n" \
"v_mfma_f32_16x16x32_bf16 a[164:167], v[88:91], v[72:75], a[164:167]\n" \
"v_mfma_f32_16x16x32_bf16 a[164:167], v[152:155], v[136:139], a[164:167]\n" \
"ds_read_b64_tr_b16 v[176:177], v39 offset:40960\n" \
"v_mfma_f32_16x16x32_bf16 a[160:163], v[84:87], v[72:75], a[160:163]\n" \
"v_mfma_f32_16x16x32_bf16 a[160:163], v[148:151], v[136:139], a[160:163]\n" \
"v_mfma_f32_16x16x32_bf16 a[192:195], v[84:87], v[76:79], a[192:195]\n" \
"ds_read_b64_tr_b16 v[178:179], v39 offset:41984\n" \
"v_mfma_f32_16x16x32_bf16 a[192:195], v[148:151], v[140:143], a[192:195]\n" \
"v_mfma_f32_16x16x32_bf16 a[196:199], v[88:91], v[76:79], a[196:199]\n" \
"v_mfma_f32_16x16x32_bf16 a[196:199], v[152:155], v[140:143], a[196:199]\n" \
"v_mfma_f32_16x16x32_bf16 a[200:203], v[92:95], v[76:79], a[200:203]\n" \
"v_mfma_f32_16x16x32_bf16 a[200:203], v[156:159], v[140:143], a[200:203]\n" \
"v_mfma_f32_16x16x32_bf16 a[204:207], v[96:99], v[76:79], a[204:207]\n" \
"v_mfma_f32_16x16x32_bf16 a[204:207], v[160:163], v[140:143], a[204:207]\n" \
"s_waitcnt lgkmcnt(15)\n" \
"v_mfma_f32_16x16x32_bf16 a[236:239], v[96:99], v[80:83], a[236:239]\n" \
"v_mfma_f32_16x16x32_bf16 a[236:239], v[160:163], v[144:147], a[236:239]\n" \
"v_mfma_f32_16x16x32_bf16 a[232:235], v[92:95], v[80:83], a[232:235]\n" \
"v_mfma_f32_16x16x32_bf16 a[232:235], v[156:159], v[144:147], a[232:235]\n" \
"v_mfma_f32_16x16x32_bf16 a[228:231], v[88:91], v[80:83], a[228:231]\n" \
"v_mfma_f32_16x16x32_bf16 a[228:231], v[152:155], v[144:147], a[228:231]\n" \
"v_mfma_f32_16x16x32_bf16 a[224:227], v[84:87], v[80:83], a[224:227]\n" \
"v_mfma_f32_16x16x32_bf16 a[224:227], v[148:151], v[144:147], a[224:227]\n" \
"s_waitcnt vmcnt(1) lgkmcnt(0)\n" \
"s_barrier\n" \
"v_mov_b32_e32 v45, s62\n" \
"s_mov_b64 exec, 1\n" \
"buffer_store_dword v45, off, s[80:83], s37 offset:128 sc1\n" \
"s_mov_b64 exec, -1\n" \
"v_mfma_f32_16x16x32_bf16 a[16:19], v[100:103], v[52:55], a[16:19]\n" \
"s_add_u32 m0, s30, 0x8000\n" \
"s_mov_b32 s85, 0x380\n" \
"v_mfma_f32_16x16x32_bf16 a[16:19], v[164:167], v[116:119], a[16:19]\n" \
"buffer_load_dwordx4 v22, s[64:67], s85 offen lds\n" \
"v_mfma_f32_16x16x32_bf16 a[20:23], v[104:107], v[52:55], a[20:23]\n" \
"ds_read_b64_tr_b16 v[84:85], v32 offset:0\n" \
"v_mfma_f32_16x16x32_bf16 a[20:23], v[168:171], v[116:119], a[20:23]\n" \
"s_add_u32 m0, s30, 0x8400\n" \
"s_mov_b32 s84, 0x2380\n" \
"v_mfma_f32_16x16x32_bf16 a[24:27], v[108:111], v[52:55], a[24:27]\n" \
"buffer_load_dwordx4 v23, s[64:67], s84 offen lds\n" \
"v_mfma_f32_16x16x32_bf16 a[24:27], v[172:175], v[116:119], a[24:27]\n" \
"ds_read_b64_tr_b16 v[86:87], v32 offset:1024\n" \
"v_mfma_f32_16x16x32_bf16 a[28:31], v[112:115], v[52:55], a[28:31]\n" \
"ds_read_b64_tr_b16 v[148:149], v32 offset:8192\n" \
"v_mfma_f32_16x16x32_bf16 a[28:31], v[176:179], v[116:119], a[28:31]\n" \
"ds_read_b128 v[52:55], v28 offset:16384\n" \
"v_mfma_f32_16x16x32_bf16 a[60:63], v[112:115], v[56:59], a[60:63]\n" \
"ds_read_b128 v[116:119], v29 offset:16384\n" \
"v_mfma_f32_16x16x32_bf16 a[60:63], v[176:179], v[120:123], a[60:63]\n" \
"s_add_u32 m0, s31, 0x8000\n" \
"s_mov_b32 s85, 0x380000\n" \
"v_mfma_f32_16x16x32_bf16 a[56:59], v[108:111], v[56:59], a[56:59]\n" \
"buffer_load_dwordx4 v26, s[68:71], s85 offen lds\n" \
"v_mfma_f32_16x16x32_bf16 a[56:59], v[172:175], v[120:123], a[56:59]\n" \
"s_add_u32 m0, s31, 0xc000\n" \
"s_mov_b32 s84, 0x380000\n" \
"v_mfma_f32_16x16x32_bf16 a[52:55], v[104:107], v[56:59], a[52:55]\n" \
"buffer_load_dwordx4 v26, s[72:75], s84 offen lds\n" \
"v_mfma_f32_16x16x32_bf16 a[52:55], v[168:171], v[120:123], a[52:55]\n" \
"s_add_u32 m0, s30, 0x8800\n" \
"s_mov_b32 s85, 0x4380\n" \
"v_mfma_f32_16x16x32_bf16 a[48:51], v[100:103], v[56:59], a[48:51]\n" \
"buffer_load_dwordx4 v22, s[64:67], s85 offen lds\n" \
"v_mfma_f32_16x16x32_bf16 a[48:51], v[164:167], v[120:123], a[48:51]\n" \
"ds_read_b128 v[56:59], v28 offset:18432\n" \
"v_mfma_f32_16x16x32_bf16 a[80:83], v[100:103], v[60:63], a[80:83]\n" \
"ds_read_b128 v[120:123], v29 offset:18432\n" \
"v_mfma_f32_16x16x32_bf16 a[80:83], v[164:167], v[124:127], a[80:83]\n" \
"s_add_u32 m0, s30, 0x8c00\n" \
"s_mov_b32 s84, 0x6380\n" \
"v_mfma_f32_16x16x32_bf16 a[84:87], v[104:107], v[60:63], a[84:87]\n" \
"buffer_load_dwordx4 v23, s[64:67], s84 offen lds\n" \
"v_mfma_f32_16x16x32_bf16 a[84:87], v[168:171], v[124:127], a[84:87]\n" \
"ds_read_b64_tr_b16 v[150:151], v32 offset:9216\n" \
"v_mfma_f32_16x16x32_bf16 a[88:91], v[108:111], v[60:63], a[88:91]\n" \
"s_add_u32 m0, s31, 0x8400\n" \
"s_mov_b32 s85, 0x388000\n" \
"v_mfma_f32_16x16x32_bf16 a[88:91], v[172:175], v[124:127], a[88:91]\n" \
"buffer_load_dwordx4 v26, s[68:71], s85 offen lds\n" \
"v_mfma_f32_16x16x32_bf16 a[92:95], v[112:115], v[60:63], a[92:95]\n" \
"ds_read_b64_tr_b16 v[88:89], v33 offset:0\n" \
"v_mfma_f32_16x16x32_bf16 a[92:95], v[176:179], v[124:127], a[92:95]\n" \
"ds_read_b128 v[60:63], v28 offset:20480\n" \
"v_mfma_f32_16x16x32_bf16 a[124:127], v[112:115], v[64:67], a[124:127]\n" \
"ds_read_b128 v[124:127], v29 offset:20480\n" \
"v_mfma_f32_16x16x32_bf16 a[124:127], v[176:179], v[128:131], a[124:127]\n" \
"s_add_u32 m0, s31, 0xc400\n" \
"s_mov_b32 s84, 0x388000\n" \
"v_mfma_f32_16x16x32_bf16 a[120:123], v[108:111], v[64:67], a[120:123]\n" \
"buffer_load_dwordx4 v26, s[72:75], s84 offen lds\n" \
"v_mfma_f32_16x16x32_bf16 a[120:123], v[172:175], v[128:131], a[120:123]\n" \
"s_add_u32 m0, s30, 0x9000\n" \
"s_mov_b32 s85, 0x8380\n" \
"v_mfma_f32_16x16x32_bf16 a[116:119], v[104:107], v[64:67], a[116:119]\n" \
"buffer_load_dwordx4 v22, s[64:67], s85 offen lds\n" \
"v_mfma_f32_16x16x32_bf16 a[116:119], v[168:171], v[128:131], a[116:119]\n" \
"ds_read_b64_tr_b16 v[90:91], v33 offset:1024\n" \
"v_mfma_f32_16x16x32_bf16 a[112:115], v[100:103], v[64:67], a[112:115]\n" \
"ds_read_b64_tr_b16 v[152:153], v33 offset:8192\n" \
"v_mfma_f32_16x16x32_bf16 a[112:115], v[164:167], v[128:131], a[112:115]\n" \
"ds_read_b128 v[64:67], v28 offset:22528\n" \
"v_mfma_f32_16x16x32_bf16 a[144:147], v[100:103], v[68:71], a[144:147]\n" \
"ds_read_b128 v[128:131], v29 offset:22528\n" \
"v_mfma_f32_16x16x32_bf16 a[144:147], v[164:167], v[132:135], a[144:147]\n" \
"s_add_u32 m0, s30, 0x9400\n" \
"s_mov_b32 s84, 0xa380\n" \
"v_mfma_f32_16x16x32_bf16 a[148:151], v[104:107], v[68:71], a[148:151]\n" \
"buffer_load_dwordx4 v23, s[64:67], s84 offen lds\n" \
"v_mfma_f32_16x16x32_bf16 a[148:151], v[168:171], v[132:135], a[148:151]\n" \
"s_add_u32 m0, s31, 0x8800\n" \
"s_mov_b32 s85, 0x390000\n" \
"v_mfma_f32_16x16x32_bf16 a[152:155], v[108:111], v[68:71], a[152:155]\n" \
"buffer_load_dwordx4 v27, s[68:71], s85 offen lds\n" \
"v_mfma_f32_16x16x32_bf16 a[152:155], v[172:175], v[132:135], a[152:155]\n" \
"s_add_u32 m0, s31, 0xc800\n" \
"s_mov_b32 s84, 0x390000\n" \
"v_mfma_f32_16x16x32_bf16 a[156:159], v[112:115], v[68:71], a[156:159]\n" \
"buffer_load_dwordx4 v27, s[72:75], s84 offen lds\n" \
"v_mfma_f32_16x16x32_bf16 a[156:159], v[176:179], v[132:135], a[156:159]\n" \
"ds_read_b128 v[68:71], v28 offset:0\n" \
"v_mfma_f32_16x16x32_bf16 a[188:191], v[112:115], v[72:75], a[188:191]\n" \
"ds_read_b128 v[132:135], v29 offset:0\n" \
"v_mfma_f32_16x16x32_bf16 a[188:191], v[176:179], v[136:139], a[188:191]\n" \
"s_add_u32 m0, s30, 0x9800\n" \
"s_mov_b32 s85, 0xc380\n" \
"v_mfma_f32_16x16x32_bf16 a[184:187], v[108:111], v[72:75], a[184:187]\n" \
"buffer_load_dwordx4 v22, s[64:67], s85 offen lds\n" \
"v_mfma_f32_16x16x32_bf16 a[184:187], v[172:175], v[136:139], a[184:187]\n" \
"ds_read_b64_tr_b16 v[154:155], v33 offset:9216\n" \
"v_mfma_f32_16x16x32_bf16 a[180:183], v[104:107], v[72:75], a[180:183]\n" \
"s_add_u32 m0, s30, 0x9c00\n" \
"s_mov_b32 s84, 0xe380\n" \
"v_mfma_f32_16x16x32_bf16 a[180:183], v[168:171], v[136:139], a[180:183]\n" \
"buffer_load_dwordx4 v23, s[64:67], s84 offen lds\n" \
"v_mfma_f32_16x16x32_bf16 a[176:179], v[100:103], v[72:75], a[176:179]\n" \
"ds_read_b64_tr_b16 v[92:93], v34 offset:0\n" \
"v_mfma_f32_16x16x32_bf16 a[176:179], v[164:167], v[136:139], a[176:179]\n" \
"ds_read_b128 v[72:75], v28 offset:2048\n" \
"v_mfma_f32_16x16x32_bf16 a[208:211], v[100:103], v[76:79], a[208:211]\n" \
"ds_read_b128 v[136:139], v29 offset:2048\n" \
"v_mfma_f32_16x16x32_bf16 a[208:211], v[164:167], v[140:143], a[208:211]\n" \
"ds_read_b64_tr_b16 v[94:95], v34 offset:1024\n" \
"v_mfma_f32_16x16x32_bf16 a[212:215], v[104:107], v[76:79], a[212:215]\n" \
"ds_read_b64_tr_b16 v[156:157], v34 offset:8192\n" \
"v_mfma_f32_16x16x32_bf16 a[212:215], v[168:171], v[140:143], a[212:215]\n" \
"ds_read_b64_tr_b16 v[158:159], v34 offset:9216\n" \
"v_mfma_f32_16x16x32_bf16 a[216:219], v[108:111], v[76:79], a[216:219]\n" \
"ds_read_b64_tr_b16 v[96:97], v35 offset:0\n" \
"v_mfma_f32_16x16x32_bf16 a[216:219], v[172:175], v[140:143], a[216:219]\n" \
"ds_read_b64_tr_b16 v[98:99], v35 offset:1024\n" \
"v_mfma_f32_16x16x32_bf16 a[220:223], v[112:115], v[76:79], a[220:223]\n" \
"ds_read_b64_tr_b16 v[160:161], v35 offset:8192\n" \
"v_mfma_f32_16x16x32_bf16 a[220:223], v[176:179], v[140:143], a[220:223]\n" \
"ds_read_b128 v[76:79], v28 offset:4096\n" \
"v_mfma_f32_16x16x32_bf16 a[252:255], v[112:115], v[80:83], a[252:255]\n" \
"ds_read_b128 v[140:143], v29 offset:4096\n" \
"v_mfma_f32_16x16x32_bf16 a[252:255], v[176:179], v[144:147], a[252:255]\n" \
"ds_read_b64_tr_b16 v[162:163], v35 offset:9216\n" \
"v_mfma_f32_16x16x32_bf16 a[248:251], v[108:111], v[80:83], a[248:251]\n" \
"v_mfma_f32_16x16x32_bf16 a[248:251], v[172:175], v[144:147], a[248:251]\n" \
"v_mfma_f32_16x16x32_bf16 a[244:247], v[104:107], v[80:83], a[244:247]\n" \
"v_mfma_f32_16x16x32_bf16 a[244:247], v[168:171], v[144:147], a[244:247]\n" \
"v_mfma_f32_16x16x32_bf16 a[240:243], v[100:103], v[80:83], a[240:243]\n" \
"v_mfma_f32_16x16x32_bf16 a[240:243], v[164:167], v[144:147], a[240:243]\n" \
"ds_read_b128 v[80:83], v28 offset:6144\n" \
"ds_read_b128 v[144:147], v29 offset:6144\n" \
"s_waitcnt lgkmcnt(15)\n" \
"v_mfma_f32_16x16x32_bf16 a[0:3], v[84:87], v[52:55], a[0:3]\n" \
"s_add_u32 m0, s31, 0x8c00\n" \
"s_mov_b32 s85, 0x398000\n" \
"v_mfma_f32_16x16x32_bf16 a[0:3], v[148:151], v[116:119], a[0:3]\n" \
"buffer_load_dwordx4 v27, s[68:71], s85 offen lds\n" \
"v_mfma_f32_16x16x32_bf16 a[4:7], v[88:91], v[52:55], a[4:7]\n" \
"ds_read_b64_tr_b16 v[100:101], v36 offset:0\n" \
"s_waitcnt lgkmcnt(15)\n" \
"v_mfma_f32_16x16x32_bf16 a[4:7], v[152:155], v[116:119], a[4:7]\n" \
"s_add_u32 m0, s31, 0xcc00\n" \
"s_mov_b32 s84, 0x398000\n" \
"s_waitcnt lgkmcnt(11)\n" \
"v_mfma_f32_16x16x32_bf16 a[8:11], v[92:95], v[52:55], a[8:11]\n" \
"buffer_load_dwordx4 v27, s[72:75], s84 offen lds\n" \
"s_waitcnt lgkmcnt(9)\n" \
"v_mfma_f32_16x16x32_bf16 a[8:11], v[156:159], v[116:119], a[8:11]\n" \
"ds_read_b64_tr_b16 v[102:103], v36 offset:1024\n" \
"s_waitcnt lgkmcnt(8)\n" \
"v_mfma_f32_16x16x32_bf16 a[12:15], v[96:99], v[52:55], a[12:15]\n" \
"ds_read_b64_tr_b16 v[164:165], v36 offset:8192\n" \
"s_waitcnt lgkmcnt(5)\n" \
"v_mfma_f32_16x16x32_bf16 a[12:15], v[160:163], v[116:119], a[12:15]\n" \
"v_mfma_f32_16x16x32_bf16 a[44:47], v[96:99], v[56:59], a[44:47]\n" \
"v_mfma_f32_16x16x32_bf16 a[44:47], v[160:163], v[120:123], a[44:47]\n" \
"ds_read_b64_tr_b16 v[166:167], v36 offset:9216\n" \
"v_mfma_f32_16x16x32_bf16 a[40:43], v[92:95], v[56:59], a[40:43]\n" \
"v_mfma_f32_16x16x32_bf16 a[40:43], v[156:159], v[120:123], a[40:43]\n" \
"v_mfma_f32_16x16x32_bf16 a[36:39], v[88:91], v[56:59], a[36:39]\n" \
"v_mfma_f32_16x16x32_bf16 a[36:39], v[152:155], v[120:123], a[36:39]\n" \
"ds_read_b64_tr_b16 v[104:105], v37 offset:0\n" \
"v_mfma_f32_16x16x32_bf16 a[32:35], v[84:87], v[56:59], a[32:35]\n" \
"v_mfma_f32_16x16x32_bf16 a[32:35], v[148:151], v[120:123], a[32:35]\n" \
"v_mfma_f32_16x16x32_bf16 a[64:67], v[84:87], v[60:63], a[64:67]\n" \
"ds_read_b64_tr_b16 v[106:107], v37 offset:1024\n" \
"v_mfma_f32_16x16x32_bf16 a[64:67], v[148:151], v[124:127], a[64:67]\n" \
"v_mfma_f32_16x16x32_bf16 a[68:71], v[88:91], v[60:63], a[68:71]\n" \
"v_mfma_f32_16x16x32_bf16 a[68:71], v[152:155], v[124:127], a[68:71]\n" \
"ds_read_b64_tr_b16 v[168:169], v37 offset:8192\n" \
"v_mfma_f32_16x16x32_bf16 a[72:75], v[92:95], v[60:63], a[72:75]\n" \
"v_mfma_f32_16x16x32_bf16 a[72:75], v[156:159], v[124:127], a[72:75]\n" \
"v_mfma_f32_16x16x32_bf16 a[76:79], v[96:99], v[60:63], a[76:79]\n" \
"ds_read_b64_tr_b16 v[170:171], v37 offset:9216\n" \
"v_mfma_f32_16x16x32_bf16 a[76:79], v[160:163], v[124:127], a[76:79]\n" \
"v_mfma_f32_16x16x32_bf16 a[108:111], v[96:99], v[64:67], a[108:111]\n" \
"v_mfma_f32_16x16x32_bf16 a[108:111], v[160:163], v[128:131], a[108:111]\n" \
"v_mfma_f32_16x16x32_bf16 a[104:107], v[92:95], v[64:67], a[104:107]\n" \
"ds_read_b64_tr_b16 v[108:109], v38 offset:0\n" \
"v_mfma_f32_16x16x32_bf16 a[104:107], v[156:159], v[128:131], a[104:107]\n" \
"v_mfma_f32_16x16x32_bf16 a[100:103], v[88:91], v[64:67], a[100:103]\n" \
"v_mfma_f32_16x16x32_bf16 a[100:103], v[152:155], v[128:131], a[100:103]\n" \
"ds_read_b64_tr_b16 v[110:111], v38 offset:1024\n" \
"v_mfma_f32_16x16x32_bf16 a[96:99], v[84:87], v[64:67], a[96:99]\n" \
"v_mfma_f32_16x16x32_bf16 a[96:99], v[148:151], v[128:131], a[96:99]\n" \
"v_mfma_f32_16x16x32_bf16 a[128:131], v[84:87], v[68:71], a[128:131]\n" \
"v_bfe_u32 v44, v20, 2, 5\n" \
"ds_read_b64_tr_b16 v[172:173], v38 offset:8192\n" \
"v_mfma_f32_16x16x32_bf16 a[128:131], v[148:151], v[132:135], a[128:131]\n" \
"buffer_load_dword v44, v44, s[80:83], s38 offen offset:128 sc1\n" \
"v_mfma_f32_16x16x32_bf16 a[132:135], v[88:91], v[68:71], a[132:135]\n" \
"v_mfma_f32_16x16x32_bf16 a[132:135], v[152:155], v[132:135], a[132:135]\n" \
"ds_read_b64_tr_b16 v[174:175], v38 offset:9216\n" \
"v_mfma_f32_16x16x32_bf16 a[136:139], v[92:95], v[68:71], a[136:139]\n" \
"v_mfma_f32_16x16x32_bf16 a[136:139], v[156:159], v[132:135], a[136:139]\n" \
"v_mfma_f32_16x16x32_bf16 a[140:143], v[96:99], v[68:71], a[140:143]\n" \
"v_mfma_f32_16x16x32_bf16 a[140:143], v[160:163], v[132:135], a[140:143]\n" \
"ds_read_b64_tr_b16 v[112:113], v39 offset:0\n" \
"v_mfma_f32_16x16x32_bf16 a[172:175], v[96:99], v[72:75], a[172:175]\n" \
"v_mfma_f32_16x16x32_bf16 a[172:175], v[160:163], v[136:139], a[172:175]\n" \
"v_mfma_f32_16x16x32_bf16 a[168:171], v[92:95], v[72:75], a[168:171]\n" \
"ds_read_b64_tr_b16 v[114:115], v39 offset:1024\n" \
"v_mfma_f32_16x16x32_bf16 a[168:171], v[156:159], v[136:139], a[168:171]\n" \
"v_mfma_f32_16x16x32_bf16 a[164:167], v[88:91], v[72:75], a[164:167]\n" \
"v_mfma_f32_16x16x32_bf16 a[164:167], v[152:155], v[136:139], a[164:167]\n" \
"ds_read_b64_tr_b16 v[176:177], v39 offset:8192\n" \
"v_mfma_f32_16x16x32_bf16 a[160:163], v[84:87], v[72:75], a[160:163]\n" \
"v_mfma_f32_16x16x32_bf16 a[160:163], v[148:151], v[136:139], a[160:163]\n" \
"v_mfma_f32_16x16x32_bf16 a[192:195], v[84:87], v[76:79], a[192:195]\n" \
"ds_read_b64_tr_b16 v[178:179], v39 offset:9216\n" \
"v_mfma_f32_16x16x32_bf16 a[192:195], v[148:151], v[140:143], a[192:195]\n" \
"v_mfma_f32_16x16x32_bf16 a[196:199], v[88:91], v[76:79], a[196:199]\n" \
"v_mfma_f32_16x16x32_bf16 a[196:199], v[152:155], v[140:143], a[196:199]\n" \
"v_mfma_f32_16x16x32_bf16 a[200:203], v[92:95], v[76:79], a[200:203]\n" \
"v_mfma_f32_16x16x32_bf16 a[200:203], v[156:159], v[140:143], a[200:203]\n" \
"v_mfma_f32_16x16x32_bf16 a[204:207], v[96:99], v[76:79], a[204:207]\n" \
"v_mfma_f32_16x16x32_bf16 a[204:207], v[160:163], v[140:143], a[204:207]\n" \
"s_waitcnt lgkmcnt(15)\n" \
"v_mfma_f32_16x16x32_bf16 a[236:239], v[96:99], v[80:83], a[236:239]\n" \
"v_mfma_f32_16x16x32_bf16 a[236:239], v[160:163], v[144:147], a[236:239]\n" \
"v_mfma_f32_16x16x32_bf16 a[232:235], v[92:95], v[80:83], a[232:235]\n" \
"v_mfma_f32_16x16x32_bf16 a[232:235], v[156:159], v[144:147], a[232:235]\n" \
"v_mfma_f32_16x16x32_bf16 a[228:231], v[88:91], v[80:83], a[228:231]\n" \
"v_mfma_f32_16x16x32_bf16 a[228:231], v[152:155], v[144:147], a[228:231]\n" \
"v_mfma_f32_16x16x32_bf16 a[224:227], v[84:87], v[80:83], a[224:227]\n" \
"v_mfma_f32_16x16x32_bf16 a[224:227], v[148:151], v[144:147], a[224:227]\n" \
"s_waitcnt vmcnt(0) lgkmcnt(0)\n" \
"s_barrier\n" \
"v_cmp_ne_u32_e32 vcc, s62, v44\n" \
"s_nop 4\n" \
"s_cbranch_vccnz L_poll60_%=\n" \
"L_pollr60_%=:\n" \
"s_mov_b32 s68, s12\n" \
"s_and_b32 s69, s13, 0xffff\n" \
"s_mov_b32 s70, 0x480000\n" \
"s_mov_b32 s71, 0x20000\n" \
"v_mfma_f32_16x16x32_bf16 a[16:19], v[100:103], v[52:55], a[16:19]\n" \
"s_add_u32 m0, s40, 0x0\n" \
"s_add_u32 s85, s39, 0x0\n" \
"s_nop 0\n" \
"v_mfma_f32_16x16x32_bf16 a[16:19], v[164:167], v[116:119], a[16:19]\n" \
"buffer_load_dwordx4 v20, s[80:83], s85 offen sc1 lds\n" \
"v_mfma_f32_16x16x32_bf16 a[20:23], v[104:107], v[52:55], a[20:23]\n" \
"ds_read_b64_tr_b16 v[84:85], v32 offset:32768\n" \
"v_mfma_f32_16x16x32_bf16 a[20:23], v[168:171], v[116:119], a[20:23]\n" \
"s_add_u32 m0, s40, 0x1000\n" \
"s_add_u32 s84, s39, 0x1000\n" \
"v_mfma_f32_16x16x32_bf16 a[24:27], v[108:111], v[52:55], a[24:27]\n" \
"buffer_load_dwordx4 v20, s[80:83], s84 offen sc1 lds\n" \
"v_mfma_f32_16x16x32_bf16 a[24:27], v[172:175], v[116:119], a[24:27]\n" \
"ds_read_b64_tr_b16 v[86:87], v32 offset:33792\n" \
"v_mfma_f32_16x16x32_bf16 a[28:31], v[112:115], v[52:55], a[28:31]\n" \
"ds_read_b64_tr_b16 v[148:149], v32 offset:40960\n" \
"v_mfma_f32_16x16x32_bf16 a[28:31], v[176:179], v[116:119], a[28:31]\n" \
"ds_read_b128 v[52:55], v28 offset:49152\n" \
"v_mfma_f32_16x16x32_bf16 a[60:63], v[112:115], v[56:59], a[60:63]\n" \
"ds_read_b128 v[116:119], v29 offset:49152\n" \
"v_mfma_f32_16x16x32_bf16 a[60:63], v[176:179], v[120:123], a[60:63]\n" \
"s_add_u32 m0, s40, 0x3000\n" \
"s_mov_b32 s85, 0x0\n" \
"v_mfma_f32_16x16x32_bf16 a[56:59], v[108:111], v[56:59], a[56:59]\n" \
"buffer_load_dwordx4 v21, s[76:79], s85 offen lds\n" \
"v_mfma_f32_16x16x32_bf16 a[56:59], v[172:175], v[120:123], a[56:59]\n" \
"s_add_u32 m0, s30, 0x0\n" \
"s_mov_b32 s84, 0x0\n" \
"s_nop 0\n" \
"v_mfma_f32_16x16x32_bf16 a[52:55], v[104:107], v[56:59], a[52:55]\n" \
"buffer_load_dwordx4 v24, s[76:79], s84 offen lds\n" \
"v_mfma_f32_16x16x32_bf16 a[52:55], v[168:171], v[120:123], a[52:55]\n" \
"ds_read_b64_tr_b16 v[150:151], v32 offset:41984\n" \
"v_mfma_f32_16x16x32_bf16 a[48:51], v[100:103], v[56:59], a[48:51]\n" \
"ds_read_b64_tr_b16 v[88:89], v33 offset:32768\n" \
"v_mfma_f32_16x16x32_bf16 a[48:51], v[164:167], v[120:123], a[48:51]\n" \
"ds_read_b128 v[56:59], v28 offset:51200\n" \
"v_mfma_f32_16x16x32_bf16 a[80:83], v[100:103], v[60:63], a[80:83]\n" \
"ds_read_b128 v[120:123], v29 offset:51200\n" \
"v_mfma_f32_16x16x32_bf16 a[80:83], v[164:167], v[124:127], a[80:83]\n" \
"s_add_u32 m0, s30, 0x400\n" \
"s_mov_b32 s85, 0x2400\n" \
"v_mfma_f32_16x16x32_bf16 a[84:87], v[104:107], v[60:63], a[84:87]\n" \
"buffer_load_dwordx4 v25, s[76:79], s85 offen lds\n" \
"v_mfma_f32_16x16x32_bf16 a[84:87], v[168:171], v[124:127], a[84:87]\n" \
"s_add_u32 m0, s31, 0x0\n" \
"s_mov_b32 s84, 0x0\n" \
"v_mfma_f32_16x16x32_bf16 a[88:91], v[108:111], v[60:63], a[88:91]\n" \
"buffer_load_dwordx4 v26, s[68:71], s84 offen lds\n" \
"v_mfma_f32_16x16x32_bf16 a[88:91], v[172:175], v[124:127], a[88:91]\n" \
"ds_read_b64_tr_b16 v[90:91], v33 offset:33792\n" \
"v_mfma_f32_16x16x32_bf16 a[92:95], v[112:115], v[60:63], a[92:95]\n" \
"ds_read_b64_tr_b16 v[152:153], v33 offset:40960\n" \
"v_mfma_f32_16x16x32_bf16 a[92:95], v[176:179], v[124:127], a[92:95]\n" \
"ds_read_b128 v[60:63], v28 offset:53248\n" \
"v_mfma_f32_16x16x32_bf16 a[124:127], v[112:115], v[64:67], a[124:127]\n" \
"ds_read_b128 v[124:127], v29 offset:53248\n" \
"v_mfma_f32_16x16x32_bf16 a[124:127], v[176:179], v[128:131], a[124:127]\n" \
"s_add_u32 m0, s30, 0x800\n" \
"s_mov_b32 s85, 0x4800\n" \
"v_mfma_f32_16x16x32_bf16 a[120:123], v[108:111], v[64:67], a[120:123]\n" \
"buffer_load_dwordx4 v24, s[76:79], s85 offen lds\n" \
"v_mfma_f32_16x16x32_bf16 a[120:123], v[172:175], v[128:131], a[120:123]\n" \
"s_add_u32 m0, s30, 0xc00\n" \
"s_mov_b32 s84, 0x6c00\n" \
"v_mfma_f32_16x16x32_bf16 a[116:119], v[104:107], v[64:67], a[116:119]\n" \
"buffer_load_dwordx4 v25, s[76:79], s84 offen lds\n" \
"v_mfma_f32_16x16x32_bf16 a[116:119], v[168:171], v[128:131], a[116:119]\n" \
"s_add_u32 m0, s31, 0x400\n" \
"s_mov_b32 s85, 0x8000\n" \
"v_mfma_f32_16x16x32_bf16 a[112:115], v[100:103], v[64:67], a[112:115]\n" \
"buffer_load_dwordx4 v26, s[68:71], s85 offen lds\n" \
"v_mfma_f32_16x16x32_bf16 a[112:115], v[164:167], v[128:131], a[112:115]\n" \
"ds_read_b128 v[64:67], v28 offset:55296\n" \
"v_mfma_f32_16x16x32_bf16 a[144:147], v[100:103], v[68:71], a[144:147]\n" \
"ds_read_b128 v[128:131], v29 offset:55296\n" \
"v_mfma_f32_16x16x32_bf16 a[144:147], v[164:167], v[132:135], a[144:147]\n" \
"s_add_u32 m0, s30, 0x1000\n" \
"s_mov_b32 s84, 0x9000\n" \
"v_mfma_f32_16x16x32_bf16 a[148:151], v[104:107], v[68:71], a[148:151]\n" \
"buffer_load_dwordx4 v24, s[76:79], s84 offen lds\n" \
"v_mfma_f32_16x16x32_bf16 a[148:151], v[168:171], v[132:135], a[148:151]\n" \
"ds_read_b64_tr_b16 v[154:155], v33 offset:41984\n" \
"v_mfma_f32_16x16x32_bf16 a[152:155], v[108:111], v[68:71], a[152:155]\n" \
"s_add_u32 m0, s30, 0x1400\n" \
"s_mov_b32 s85, 0xb400\n" \
"v_mfma_f32_16x16x32_bf16 a[152:155], v[172:175], v[132:135], a[152:155]\n" \
"buffer_load_dwordx4 v25, s[76:79], s85 offen lds\n" \
"v_mfma_f32_16x16x32_bf16 a[156:159], v[112:115], v[68:71], a[156:159]\n" \
"ds_read_b64_tr_b16 v[92:93], v34 offset:32768\n" \
"v_mfma_f32_16x16x32_bf16 a[156:159], v[176:179], v[132:135], a[156:159]\n" \
"ds_read_b128 v[68:71], v28 offset:32768\n" \
"v_mfma_f32_16x16x32_bf16 a[188:191], v[112:115], v[72:75], a[188:191]\n" \
"ds_read_b128 v[132:135], v29 offset:32768\n" \
"v_mfma_f32_16x16x32_bf16 a[188:191], v[176:179], v[136:139], a[188:191]\n" \
"s_add_u32 m0, s31, 0x800\n" \
"s_mov_b32 s84, 0x10000\n" \
"v_mfma_f32_16x16x32_bf16 a[184:187], v[108:111], v[72:75], a[184:187]\n" \
"buffer_load_dwordx4 v27, s[68:71], s84 offen lds\n" \
"v_mfma_f32_16x16x32_bf16 a[184:187], v[172:175], v[136:139], a[184:187]\n" \
"ds_read_b64_tr_b16 v[94:95], v34 offset:33792\n" \
"v_mfma_f32_16x16x32_bf16 a[180:183], v[104:107], v[72:75], a[180:183]\n" \
"s_add_u32 m0, s30, 0x1800\n" \
"s_mov_b32 s85, 0xd800\n" \
"v_mfma_f32_16x16x32_bf16 a[180:183], v[168:171], v[136:139], a[180:183]\n" \
"buffer_load_dwordx4 v24, s[76:79], s85 offen lds\n" \
"v_mfma_f32_16x16x32_bf16 a[176:179], v[100:103], v[72:75], a[176:179]\n" \
"ds_read_b64_tr_b16 v[156:157], v34 offset:40960\n" \
"v_mfma_f32_16x16x32_bf16 a[176:179], v[164:167], v[136:139], a[176:179]\n" \
"ds_read_b128 v[72:75], v28 offset:34816\n" \
"v_mfma_f32_16x16x32_bf16 a[208:211], v[100:103], v[76:79], a[208:211]\n" \
"ds_read_b128 v[136:139], v29 offset:34816\n" \
"v_mfma_f32_16x16x32_bf16 a[208:211], v[164:167], v[140:143], a[208:211]\n" \
"ds_read_b64_tr_b16 v[158:159], v34 offset:41984\n" \
"v_mfma_f32_16x16x32_bf16 a[212:215], v[104:107], v[76:79], a[212:215]\n" \
"ds_read_b64_tr_b16 v[96:97], v35 offset:32768\n" \
"v_mfma_f32_16x16x32_bf16 a[212:215], v[168:171], v[140:143], a[212:215]\n" \
"ds_read_b64_tr_b16 v[98:99], v35 offset:33792\n" \
"v_mfma_f32_16x16x32_bf16 a[216:219], v[108:111], v[76:79], a[216:219]\n" \
"ds_read_b64_tr_b16 v[160:161], v35 offset:40960\n" \
"v_mfma_f32_16x16x32_bf16 a[216:219], v[172:175], v[140:143], a[216:219]\n" \
"ds_read_b64_tr_b16 v[162:163], v35 offset:41984\n" \
"v_mfma_f32_16x16x32_bf16 a[220:223], v[112:115], v[76:79], a[220:223]\n" \
"v_mfma_f32_16x16x32_bf16 a[220:223], v[176:179], v[140:143], a[220:223]\n" \
"ds_read_b128 v[76:79], v28 offset:36864\n" \
"v_mfma_f32_16x16x32_bf16 a[252:255], v[112:115], v[80:83], a[252:255]\n" \
"ds_read_b128 v[140:143], v29 offset:36864\n" \
"v_mfma_f32_16x16x32_bf16 a[252:255], v[176:179], v[144:147], a[252:255]\n" \
"v_mfma_f32_16x16x32_bf16 a[248:251], v[108:111], v[80:83], a[248:251]\n" \
"v_mfma_f32_16x16x32_bf16 a[248:251], v[172:175], v[144:147], a[248:251]\n" \
"v_mfma_f32_16x16x32_bf16 a[244:247], v[104:107], v[80:83], a[244:247]\n" \
"v_mfma_f32_16x16x32_bf16 a[244:247], v[168:171], v[144:147], a[244:247]\n" \
"v_mfma_f32_16x16x32_bf16 a[240:243], v[100:103], v[80:83], a[240:243]\n" \
"v_mfma_f32_16x16x32_bf16 a[240:243], v[164:167], v[144:147], a[240:243]\n" \
"ds_read_b128 v[80:83], v28 offset:38912\n" \
"ds_read_b128 v[144:147], v29 offset:38912\n" \
"s_waitcnt lgkmcnt(15)\n" \
"v_mfma_f32_16x16x32_bf16 a[0:3], v[84:87], v[52:55], a[0:3]\n" \
"s_add_u32 m0, s30, 0x1c00\n" \
"s_mov_b32 s85, 0xfc00\n" \
"v_mfma_f32_16x16x32_bf16 a[0:3], v[148:151], v[116:119], a[0:3]\n" \
"buffer_load_dwordx4 v25, s[76:79], s85 offen lds\n" \
"v_mfma_f32_16x16x32_bf16 a[4:7], v[88:91], v[52:55], a[4:7]\n" \
"ds_read_b64_tr_b16 v[100:101], v36 offset:32768\n" \
"v_mfma_f32_16x16x32_bf16 a[4:7], v[152:155], v[116:119], a[4:7]\n" \
"s_add_u32 m0, s31, 0xc00\n" \
"s_mov_b32 s84, 0x18000\n" \
"s_waitcnt lgkmcnt(13)\n" \
"v_mfma_f32_16x16x32_bf16 a[8:11], v[92:95], v[52:55], a[8:11]\n" \
"buffer_load_dwordx4 v27, s[68:71], s84 offen lds\n" \
"s_waitcnt lgkmcnt(9)\n" \
"v_mfma_f32_16x16x32_bf16 a[8:11], v[156:159], v[116:119], a[8:11]\n" \
"ds_read_b64_tr_b16 v[102:103], v36 offset:33792\n" \
"s_waitcnt lgkmcnt(8)\n" \
"v_mfma_f32_16x16x32_bf16 a[12:15], v[96:99], v[52:55], a[12:15]\n" \
"ds_read_b64_tr_b16 v[164:165], v36 offset:40960\n" \
"v_accvgpr_read_b32 v184, a0\n" \
"s_waitcnt lgkmcnt(7)\n" \
"s_nop 0\n" \
"v_mfma_f32_16x16x32_bf16 a[12:15], v[160:163], v[116:119], a[12:15]\n" \
"v_accvgpr_read_b32 v185, a1\n" \
"v_accvgpr_read_b32 v186, a2\n" \
"v_mfma_f32_16x16x32_bf16 a[44:47], v[96:99], v[56:59], a[44:47]\n" \
"v_accvgpr_read_b32 v187, a3\n" \
"v_accvgpr_read_b32 v180, a4\n" \
"v_mfma_f32_16x16x32_bf16 a[44:47], v[160:163], v[120:123], a[44:47]\n" \
"ds_read_b64_tr_b16 v[166:167], v36 offset:41984\n" \
"v_accvgpr_read_b32 v181, a5\n" \
"v_mfma_f32_16x16x32_bf16 a[40:43], v[92:95], v[56:59], a[40:43]\n" \
"v_accvgpr_read_b32 v182, a6\n" \
"v_accvgpr_read_b32 v183, a7\n" \
"v_mfma_f32_16x16x32_bf16 a[40:43], v[156:159], v[120:123], a[40:43]\n" \
"v_cvt_pk_bf16_f32 v184, v184, v185\n" \
"v_cvt_pk_bf16_f32 v185, v186, v187\n" \
"v_mfma_f32_16x16x32_bf16 a[36:39], v[88:91], v[56:59], a[36:39]\n" \
"v_cvt_pk_bf16_f32 v186, v180, v181\n" \
"v_cvt_pk_bf16_f32 v187, v182, v183\n" \
"v_mfma_f32_16x16x32_bf16 a[36:39], v[152:155], v[120:123], a[36:39]\n" \
"ds_read_b64_tr_b16 v[104:105], v37 offset:32768\n" \
"v_accvgpr_read_b32 v188, a8\n" \
"v_mfma_f32_16x16x32_bf16 a[32:35], v[84:87], v[56:59], a[32:35]\n" \
"v_accvgpr_read_b32 v189, a9\n" \
"v_accvgpr_read_b32 v190, a10\n" \
"v_mfma_f32_16x16x32_bf16 a[32:35], v[148:151], v[120:123], a[32:35]\n" \
"v_accvgpr_read_b32 v191, a11\n" \
"v_accvgpr_read_b32 v180, a12\n" \
"v_mfma_f32_16x16x32_bf16 a[64:67], v[84:87], v[60:63], a[64:67]\n" \
"ds_read_b64_tr_b16 v[106:107], v37 offset:33792\n" \
"v_accvgpr_read_b32 v181, a13\n" \
"v_mfma_f32_16x16x32_bf16 a[64:67], v[148:151], v[124:127], a[64:67]\n" \
"v_accvgpr_read_b32 v182, a14\n" \
"v_accvgpr_read_b32 v183, a15\n" \
"v_mfma_f32_16x16x32_bf16 a[68:71], v[88:91], v[60:63], a[68:71]\n" \
"v_lshlrev_b32_e32 v45, 3, v20\n" \
"v_add_u32_e32 v45, s31, v45\n" \
"v_mfma_f32_16x16x32_bf16 a[68:71], v[152:155], v[124:127], a[68:71]\n" \
"s_getpc_b64 s[86:87]\n" \
"s_sub_u32 s86, s86, 0xf800\n" \
"s_subb_u32 s87, s87, 0\n" \
"s_mov_b32 m0, s32\n" \
"s_nop 0\n" \
"v_mfma_f32_16x16x32_bf16 a[72:75], v[92:95], v[60:63], a[72:75]\n" \
"global_load_lds_dword v45, s[86:87]\n" \
"v_mfma_f32_16x16x32_bf16 a[72:75], v[156:159], v[124:127], a[72:75]\n" \
"ds_read_b64_tr_b16 v[168:169], v37 offset:40960\n" \
"v_cvt_pk_bf16_f32 v188, v188, v189\n" \
"v_mfma_f32_16x16x32_bf16 a[76:79], v[96:99], v[60:63], a[76:79]\n" \
"ds_read_b64_tr_b16 v[170:171], v37 offset:41984\n" \
"v_cvt_pk_bf16_f32 v189, v190, v191\n" \
"v_mfma_f32_16x16x32_bf16 a[76:79], v[160:163], v[124:127], a[76:79]\n" \
"v_cvt_pk_bf16_f32 v190, v180, v181\n" \
"v_cvt_pk_bf16_f32 v191, v182, v183\n" \
"v_mfma_f32_16x16x32_bf16 a[108:111], v[96:99], v[64:67], a[108:111]\n" \
"v_accvgpr_read_b32 v192, a40\n" \
"v_accvgpr_read_b32 v193, a41\n" \
"v_mfma_f32_16x16x32_bf16 a[108:111], v[160:163], v[128:131], a[108:111]\n" \
"v_accvgpr_read_b32 v194, a42\n" \
"v_accvgpr_read_b32 v195, a43\n" \
"v_mfma_f32_16x16x32_bf16 a[104:107], v[92:95], v[64:67], a[104:107]\n" \
"ds_read_b64_tr_b16 v[108:109], v38 offset:32768\n" \
"v_accvgpr_read_b32 v180, a44\n" \
"v_mfma_f32_16x16x32_bf16 a[104:107], v[156:159], v[128:131], a[104:107]\n" \
"v_accvgpr_read_b32 v181, a45\n" \
"v_accvgpr_read_b32 v182, a46\n" \
"v_mfma_f32_16x16x32_bf16 a[100:103], v[88:91], v[64:67], a[100:103]\n" \
"v_accvgpr_read_b32 v183, a47\n" \
"v_cvt_pk_bf16_f32 v192, v192, v193\n" \
"v_mfma_f32_16x16x32_bf16 a[100:103], v[152:155], v[128:131], a[100:103]\n" \
"ds_read_b64_tr_b16 v[110:111], v38 offset:33792\n" \
"v_cvt_pk_bf16_f32 v193, v194, v195\n" \
"v_mfma_f32_16x16x32_bf16 a[96:99], v[84:87], v[64:67], a[96:99]\n" \
"v_cvt_pk_bf16_f32 v194, v180, v181\n" \
"v_cvt_pk_bf16_f32 v195, v182, v183\n" \
"v_mfma_f32_16x16x32_bf16 a[96:99], v[148:151], v[128:131], a[96:99]\n" \
"v_accvgpr_read_b32 v196, a32\n" \
"v_accvgpr_read_b32 v197, a33\n" \
"v_mfma_f32_16x16x32_bf16 a[128:131], v[84:87], v[68:71], a[128:131]\n" \
"ds_read_b64_tr_b16 v[172:173], v38 offset:40960\n" \
"v_accvgpr_read_b32 v198, a34\n" \
"v_mfma_f32_16x16x32_bf16 a[128:131], v[148:151], v[132:135], a[128:131]\n" \
"v_accvgpr_read_b32 v199, a35\n" \
"v_accvgpr_read_b32 v180, a36\n" \
"v_mfma_f32_16x16x32_bf16 a[132:135], v[88:91], v[68:71], a[132:135]\n" \
"v_accvgpr_read_b32 v181, a37\n" \
"v_accvgpr_read_b32 v182, a38\n" \
"v_mfma_f32_16x16x32_bf16 a[132:135], v[152:155], v[132:135], a[132:135]\n" \
"ds_read_b64_tr_b16 v[174:175], v38 offset:41984\n" \
"v_accvgpr_read_b32 v183, a39\n" \
"v_mfma_f32_16x16x32_bf16 a[136:139], v[92:95], v[68:71], a[136:139]\n" \
"v_cvt_pk_bf16_f32 v196, v196, v197\n" \
"v_cvt_pk_bf16_f32 v197, v198, v199\n" \
"v_mfma_f32_16x16x32_bf16 a[136:139], v[156:159], v[132:135], a[136:139]\n" \
"v_cvt_pk_bf16_f32 v198, v180, v181\n" \
"v_cvt_pk_bf16_f32 v199, v182, v183\n" \
"v_mfma_f32_16x16x32_bf16 a[140:143], v[96:99], v[68:71], a[140:143]\n" \
"v_accvgpr_read_b32 v200, a64\n" \
"v_accvgpr_read_b32 v201, a65\n" \
"v_mfma_f32_16x16x32_bf16 a[140:143], v[160:163], v[132:135], a[140:143]\n" \
"ds_read_b64_tr_b16 v[112:113], v39 offset:32768\n" \
"v_accvgpr_read_b32 v202, a66\n" \
"v_mfma_f32_16x16x32_bf16 a[172:175], v[96:99], v[72:75], a[172:175]\n" \
"s_add_u32 s89, s41, 0x0\n" \
"s_nop 0\n" \
"buffer_store_dwordx4 v[184:187], v20, s[80:83], s89 offen offset:0 sc1\n" \
"v_mfma_f32_16x16x32_bf16 a[172:175], v[160:163], v[136:139], a[172:175]\n" \
"v_accvgpr_read_b32 v203, a67\n" \
"v_accvgpr_read_b32 v180, a68\n" \
"v_mfma_f32_16x16x32_bf16 a[168:171], v[92:95], v[72:75], a[168:171]\n" \
"ds_read_b64_tr_b16 v[114:115], v39 offset:33792\n" \
"v_accvgpr_read_b32 v181, a69\n" \
"v_mfma_f32_16x16x32_bf16 a[168:171], v[156:159], v[136:139], a[168:171]\n" \
"v_accvgpr_read_b32 v182, a70\n" \
"v_accvgpr_read_b32 v183, a71\n" \
"v_mfma_f32_16x16x32_bf16 a[164:167], v[88:91], v[72:75], a[164:167]\n" \
"v_cvt_pk_bf16_f32 v200, v200, v201\n" \
"v_cvt_pk_bf16_f32 v201, v202, v203\n" \
"v_mfma_f32_16x16x32_bf16 a[164:167], v[152:155], v[136:139], a[164:167]\n" \
"ds_read_b64_tr_b16 v[176:177], v39 offset:40960\n" \
"v_cvt_pk_bf16_f32 v202, v180, v181\n" \
"v_mfma_f32_16x16x32_bf16 a[160:163], v[84:87], v[72:75], a[160:163]\n" \
"s_add_u32 s88, s41, 0x0\n" \
"buffer_store_dwordx4 v[188:191], v20, s[80:83], s88 offen offset:1024 sc1\n" \
"v_mfma_f32_16x16x32_bf16 a[160:163], v[148:151], v[136:139], a[160:163]\n" \
"v_cvt_pk_bf16_f32 v203, v182, v183\n" \
"v_accvgpr_read_b32 v204, a72\n" \
"s_waitcnt lgkmcnt(15)\n" \
"v_mfma_f32_16x16x32_bf16 a[192:195], v[84:87], v[76:79], a[192:195]\n" \
"ds_read_b64_tr_b16 v[178:179], v39 offset:41984\n" \
"v_accvgpr_read_b32 v205, a73\n" \
"v_mfma_f32_16x16x32_bf16 a[192:195], v[148:151], v[140:143], a[192:195]\n" \
"v_accvgpr_read_b32 v206, a74\n" \
"v_accvgpr_read_b32 v207, a75\n" \
"v_mfma_f32_16x16x32_bf16 a[196:199], v[88:91], v[76:79], a[196:199]\n" \
"v_accvgpr_read_b32 v180, a76\n" \
"v_accvgpr_read_b32 v181, a77\n" \
"v_mfma_f32_16x16x32_bf16 a[196:199], v[152:155], v[140:143], a[196:199]\n" \
"v_accvgpr_read_b32 v182, a78\n" \
"v_accvgpr_read_b32 v183, a79\n" \
"v_mfma_f32_16x16x32_bf16 a[200:203], v[92:95], v[76:79], a[200:203]\n" \
"s_add_u32 s89, s41, 0x1000\n" \
"buffer_store_dwordx4 v[192:195], v20, s[80:83], s89 offen offset:1024 sc1\n" \
"v_mfma_f32_16x16x32_bf16 a[200:203], v[156:159], v[140:143], a[200:203]\n" \
"v_cvt_pk_bf16_f32 v204, v204, v205\n" \
"v_cvt_pk_bf16_f32 v205, v206, v207\n" \
"v_mfma_f32_16x16x32_bf16 a[204:207], v[96:99], v[76:79], a[204:207]\n" \
"v_cvt_pk_bf16_f32 v206, v180, v181\n" \
"v_cvt_pk_bf16_f32 v207, v182, v183\n" \
"v_mfma_f32_16x16x32_bf16 a[204:207], v[160:163], v[140:143], a[204:207]\n" \
"v_accvgpr_read_b32 v208, a104\n" \
"v_accvgpr_read_b32 v209, a105\n" \
"v_mfma_f32_16x16x32_bf16 a[236:239], v[96:99], v[80:83], a[236:239]\n" \
"v_accvgpr_read_b32 v210, a106\n" \
"v_accvgpr_read_b32 v211, a107\n" \
"v_mfma_f32_16x16x32_bf16 a[236:239], v[160:163], v[144:147], a[236:239]\n" \
"v_accvgpr_read_b32 v180, a108\n" \
"v_accvgpr_read_b32 v181, a109\n" \
"v_mfma_f32_16x16x32_bf16 a[232:235], v[92:95], v[80:83], a[232:235]\n" \
"s_add_u32 s88, s41, 0x1000\n" \
"buffer_store_dwordx4 v[196:199], v20, s[80:83], s88 offen offset:0 sc1\n" \
"v_mfma_f32_16x16x32_bf16 a[232:235], v[156:159], v[144:147], a[232:235]\n" \
"v_accvgpr_read_b32 v182, a110\n" \
"v_accvgpr_read_b32 v183, a111\n" \
"v_mfma_f32_16x16x32_bf16 a[228:231], v[88:91], v[80:83], a[228:231]\n" \
"v_cvt_pk_bf16_f32 v208, v208, v209\n" \
"v_cvt_pk_bf16_f32 v209, v210, v211\n" \
"v_mfma_f32_16x16x32_bf16 a[228:231], v[152:155], v[144:147], a[228:231]\n" \
"v_cvt_pk_bf16_f32 v210, v180, v181\n" \
"v_cvt_pk_bf16_f32 v211, v182, v183\n" \
"v_mfma_f32_16x16x32_bf16 a[224:227], v[84:87], v[80:83], a[224:227]\n" \
"v_accvgpr_read_b32 v212, a96\n" \
"v_accvgpr_read_b32 v213, a97\n" \
"v_mfma_f32_16x16x32_bf16 a[224:227], v[148:151], v[144:147], a[224:227]\n" \
"v_accvgpr_read_b32 v214, a98\n" \
"v_accvgpr_read_b32 v215, a99\n" \
"s_waitcnt vmcnt(5) lgkmcnt(0)\n" \
"s_barrier\n" \
"v_mov_b32_e32 v36, v32\n" \
"v_xor_b32_e32 v32, 0x0, v48\n" \
"v_mov_b32_e32 v37, v33\n" \
"v_xor_b32_e32 v33, 0x20, v48\n" \
"v_mov_b32_e32 v38, v34\n" \
"v_xor_b32_e32 v34, 0x40, v48\n" \
"v_mov_b32_e64 v39, v35\n" \
"v_xor_b32_e32 v35, 0x60, v48\n" \
"v_add_u32_e32 v22, s44, v28\n" \
"v_add_u32_e32 v28, s43, v28\n" \
"v_add_u32_e32 v23, s44, v29\n" \
"v_add_u32_e32 v29, s43, v29\n" \
"v_mfma_f32_16x16x32_bf16 a[16:19], v[100:103], v[52:55], a[16:19]\n" \
"s_add_u32 m0, s40, 0x8000\n" \
"s_add_u32 s85, s39, 0x2000\n" \
"v_mfma_f32_16x16x32_bf16 a[16:19], v[164:167], v[116:119], a[16:19]\n" \
"buffer_load_dwordx4 v20, s[80:83], s85 offen sc1 lds\n" \
"v_mfma_f32_16x16x32_bf16 a[20:23], v[104:107], v[52:55], a[20:23]\n" \
"ds_read_b64_tr_b16 v[84:85], v32 offset:0\n" \
"v_accvgpr_read_b32 v180, a100\n" \
"v_mfma_f32_16x16x32_bf16 a[20:23], v[168:171], v[116:119], a[20:23]\n" \
"s_add_u32 m0, s40, 0x9000\n" \
"s_add_u32 s84, s39, 0x3000\n" \
"v_mfma_f32_16x16x32_bf16 a[24:27], v[108:111], v[52:55], a[24:27]\n" \
"buffer_load_dwordx4 v20, s[80:83], s84 offen sc1 lds\n" \
"v_mfma_f32_16x16x32_bf16 a[24:27], v[172:175], v[116:119], a[24:27]\n" \
"ds_read_b64_tr_b16 v[86:87], v32 offset:512\n" \
"v_accvgpr_read_b32 v181, a101\n" \
"v_mfma_f32_16x16x32_bf16 a[28:31], v[112:115], v[52:55], a[28:31]\n" \
"ds_read_b64_tr_b16 v[148:149], v32 offset:4096\n" \
"v_accvgpr_read_b32 v182, a102\n" \
"v_mfma_f32_16x16x32_bf16 a[28:31], v[176:179], v[116:119], a[28:31]\n" \
"ds_read_b128 v[52:55], v28 offset:0\n" \
"v_accvgpr_read_b32 v183, a103\n" \
"v_mfma_f32_16x16x32_bf16 a[60:63], v[112:115], v[56:59], a[60:63]\n" \
"ds_read_b128 v[116:119], v29 offset:0\n" \
"v_cvt_pk_bf16_f32 v212, v212, v213\n" \
"v_mfma_f32_16x16x32_bf16 a[60:63], v[176:179], v[120:123], a[60:63]\n" \
"s_add_u32 m0, s40, 0xb000\n" \
"s_mov_b32 s85, 0x80\n" \
"v_mfma_f32_16x16x32_bf16 a[56:59], v[108:111], v[56:59], a[56:59]\n" \
"buffer_load_dwordx4 v21, s[76:79], s85 offen lds\n" \
"v_mfma_f32_16x16x32_bf16 a[56:59], v[172:175], v[120:123], a[56:59]\n" \
"s_add_u32 m0, s30, 0x8000\n" \
"s_mov_b32 s84, 0x80\n" \
"v_mfma_f32_16x16x32_bf16 a[52:55], v[104:107], v[56:59], a[52:55]\n" \
"buffer_load_dwordx4 v24, s[76:79], s84 offen lds\n" \
"v_mfma_f32_16x16x32_bf16 a[52:55], v[168:171], v[120:123], a[52:55]\n" \
"ds_read_b64_tr_b16 v[150:151], v32 offset:4608\n" \
"v_cvt_pk_bf16_f32 v213, v214, v215\n" \
"v_mfma_f32_16x16x32_bf16 a[48:51], v[100:103], v[56:59], a[48:51]\n" \
"ds_read_b64_tr_b16 v[88:89], v33 offset:0\n" \
"v_cvt_pk_bf16_f32 v214, v180, v181\n" \
"v_mfma_f32_16x16x32_bf16 a[48:51], v[164:167], v[120:123], a[48:51]\n" \
"ds_read_b128 v[56:59], v28 offset:2048\n" \
"v_cvt_pk_bf16_f32 v215, v182, v183\n" \
"v_mfma_f32_16x16x32_bf16 a[80:83], v[100:103], v[60:63], a[80:83]\n" \
"ds_read_b128 v[120:123], v29 offset:2048\n" \
"v_accvgpr_read_b32 v216, a128\n" \
"v_mfma_f32_16x16x32_bf16 a[80:83], v[164:167], v[124:127], a[80:83]\n" \
"s_add_u32 m0, s30, 0x8400\n" \
"s_mov_b32 s85, 0x2480\n" \
"v_mfma_f32_16x16x32_bf16 a[84:87], v[104:107], v[60:63], a[84:87]\n" \
"buffer_load_dwordx4 v25, s[76:79], s85 offen lds\n" \
"v_mfma_f32_16x16x32_bf16 a[84:87], v[168:171], v[124:127], a[84:87]\n" \
"s_add_u32 m0, s31, 0x8000\n" \
"s_mov_b32 s84, 0x80000\n" \
"v_mfma_f32_16x16x32_bf16 a[88:91], v[108:111], v[60:63], a[88:91]\n" \
"buffer_load_dwordx4 v26, s[68:71], s84 offen lds\n" \
"v_mfma_f32_16x16x32_bf16 a[88:91], v[172:175], v[124:127], a[88:91]\n" \
"ds_read_b64_tr_b16 v[90:91], v33 offset:512\n" \
"v_accvgpr_read_b32 v217, a129\n" \
"v_mfma_f32_16x16x32_bf16 a[92:95], v[112:115], v[60:63], a[92:95]\n" \
"ds_read_b64_tr_b16 v[152:153], v33 offset:4096\n" \
"v_accvgpr_read_b32 v218, a130\n" \
"v_mfma_f32_16x16x32_bf16 a[92:95], v[176:179], v[124:127], a[92:95]\n" \
"ds_read_b128 v[60:63], v28 offset:4096\n" \
"v_accvgpr_read_b32 v219, a131\n" \
"v_mfma_f32_16x16x32_bf16 a[124:127], v[112:115], v[64:67], a[124:127]\n" \
"ds_read_b128 v[124:127], v29 offset:4096\n" \
"v_accvgpr_read_b32 v180, a132\n" \
"v_mfma_f32_16x16x32_bf16 a[124:127], v[176:179], v[128:131], a[124:127]\n" \
"s_add_u32 m0, s30, 0x8800\n" \
"s_mov_b32 s85, 0x4880\n" \
"v_mfma_f32_16x16x32_bf16 a[120:123], v[108:111], v[64:67], a[120:123]\n" \
"buffer_load_dwordx4 v24, s[76:79], s85 offen lds\n" \
"v_mfma_f32_16x16x32_bf16 a[120:123], v[172:175], v[128:131], a[120:123]\n" \
"s_add_u32 m0, s30, 0x8c00\n" \
"s_mov_b32 s84, 0x6c80\n" \
"v_mfma_f32_16x16x32_bf16 a[116:119], v[104:107], v[64:67], a[116:119]\n" \
"buffer_load_dwordx4 v25, s[76:79], s84 offen lds\n" \
"v_mfma_f32_16x16x32_bf16 a[116:119], v[168:171], v[128:131], a[116:119]\n" \
"s_add_u32 m0, s31, 0x8400\n" \
"s_mov_b32 s85, 0x88000\n" \
"v_mfma_f32_16x16x32_bf16 a[112:115], v[100:103], v[64:67], a[112:115]\n" \
"buffer_load_dwordx4 v26, s[68:71], s85 offen lds\n" \
"v_mfma_f32_16x16x32_bf16 a[112:115], v[164:167], v[128:131], a[112:115]\n" \
"ds_read_b128 v[64:67], v28 offset:6144\n" \
"v_accvgpr_read_b32 v181, a133\n" \
"v_mfma_f32_16x16x32_bf16 a[144:147], v[100:103], v[68:71], a[144:147]\n" \
"ds_read_b128 v[128:131], v29 offset:6144\n" \
"v_accvgpr_read_b32 v182, a134\n" \
"v_mfma_f32_16x16x32_bf16 a[144:147], v[164:167], v[132:135], a[144:147]\n" \
"s_add_u32 m0, s30, 0x9000\n" \
"s_mov_b32 s84, 0x9080\n" \
"v_mfma_f32_16x16x32_bf16 a[148:151], v[104:107], v[68:71], a[148:151]\n" \
"buffer_load_dwordx4 v24, s[76:79], s84 offen lds\n" \
"v_mfma_f32_16x16x32_bf16 a[148:151], v[168:171], v[132:135], a[148:151]\n" \
"ds_read_b64_tr_b16 v[154:155], v33 offset:4608\n" \
"v_accvgpr_read_b32 v183, a135\n" \
"v_mfma_f32_16x16x32_bf16 a[152:155], v[108:111], v[68:71], a[152:155]\n" \
"s_add_u32 m0, s30, 0x9400\n" \
"s_mov_b32 s85, 0xb480\n" \
"v_mfma_f32_16x16x32_bf16 a[152:155], v[172:175], v[132:135], a[152:155]\n" \
"buffer_load_dwordx4 v25, s[76:79], s85 offen lds\n" \
"v_mfma_f32_16x16x32_bf16 a[156:159], v[112:115], v[68:71], a[156:159]\n" \
"ds_read_b64_tr_b16 v[96:97], v35 offset:0\n" \
"v_cvt_pk_bf16_f32 v216, v216, v217\n" \
"v_mfma_f32_16x16x32_bf16 a[156:159], v[176:179], v[132:135], a[156:159]\n" \
"ds_read_b128 v[68:71], v28 offset:8192\n" \
"v_cvt_pk_bf16_f32 v217, v218, v219\n" \
"v_mfma_f32_16x16x32_bf16 a[188:191], v[112:115], v[72:75], a[188:191]\n" \
"ds_read_b128 v[132:135], v29 offset:8192\n" \
"v_cvt_pk_bf16_f32 v218, v180, v181\n" \
"v_mfma_f32_16x16x32_bf16 a[188:191], v[176:179], v[136:139], a[188:191]\n" \
"s_add_u32 m0, s31, 0x8800\n" \
"s_mov_b32 s84, 0x90000\n" \
"v_mfma_f32_16x16x32_bf16 a[184:187], v[108:111], v[72:75], a[184:187]\n" \
"buffer_load_dwordx4 v27, s[68:71], s84 offen lds\n" \
"v_mfma_f32_16x16x32_bf16 a[184:187], v[172:175], v[136:139], a[184:187]\n" \
"ds_read_b64_tr_b16 v[98:99], v35 offset:512\n" \
"v_cvt_pk_bf16_f32 v219, v182, v183\n" \
"v_mfma_f32_16x16x32_bf16 a[180:183], v[104:107], v[72:75], a[180:183]\n" \
"s_add_u32 m0, s30, 0x9800\n" \
"s_mov_b32 s85, 0xd880\n" \
"v_mfma_f32_16x16x32_bf16 a[180:183], v[168:171], v[136:139], a[180:183]\n" \
"buffer_load_dwordx4 v24, s[76:79], s85 offen lds\n" \
"v_mfma_f32_16x16x32_bf16 a[176:179], v[100:103], v[72:75], a[176:179]\n" \
"ds_read_b64_tr_b16 v[160:161], v35 offset:4096\n" \
"v_accvgpr_read_b32 v220, a136\n" \
"v_mfma_f32_16x16x32_bf16 a[176:179], v[164:167], v[136:139], a[176:179]\n" \
"ds_read_b128 v[72:75], v28 offset:10240\n" \
"v_accvgpr_read_b32 v221, a137\n" \
"v_mfma_f32_16x16x32_bf16 a[208:211], v[100:103], v[76:79], a[208:211]\n" \
"ds_read_b128 v[136:139], v29 offset:10240\n" \
"v_accvgpr_read_b32 v222, a138\n" \
"v_mfma_f32_16x16x32_bf16 a[208:211], v[164:167], v[140:143], a[208:211]\n" \
"ds_read_b64_tr_b16 v[162:163], v35 offset:4608\n" \
"v_accvgpr_read_b32 v223, a139\n" \
"v_mfma_f32_16x16x32_bf16 a[212:215], v[104:107], v[76:79], a[212:215]\n" \
"ds_read_b64_tr_b16 v[92:93], v34 offset:0\n" \
"v_accvgpr_read_b32 v180, a140\n" \
"v_mfma_f32_16x16x32_bf16 a[212:215], v[168:171], v[140:143], a[212:215]\n" \
"ds_read_b64_tr_b16 v[94:95], v34 offset:512\n" \
"v_accvgpr_read_b32 v181, a141\n" \
"v_mfma_f32_16x16x32_bf16 a[216:219], v[108:111], v[76:79], a[216:219]\n" \
"ds_read_b64_tr_b16 v[156:157], v34 offset:4096\n" \
"v_accvgpr_read_b32 v182, a142\n" \
"v_mfma_f32_16x16x32_bf16 a[216:219], v[172:175], v[140:143], a[216:219]\n" \
"ds_read_b64_tr_b16 v[158:159], v34 offset:4608\n" \
"v_accvgpr_read_b32 v183, a143\n" \
"v_mfma_f32_16x16x32_bf16 a[220:223], v[112:115], v[76:79], a[220:223]\n" \
"ds_read_b128 v[248:251], v22 offset:0\n" \
"v_cvt_pk_bf16_f32 v220, v220, v221\n" \
"v_mfma_f32_16x16x32_bf16 a[220:223], v[176:179], v[140:143], a[220:223]\n" \
"ds_read_b128 v[76:79], v28 offset:12288\n" \
"v_cvt_pk_bf16_f32 v221, v222, v223\n" \
"v_mfma_f32_16x16x32_bf16 a[252:255], v[112:115], v[80:83], a[252:255]\n" \
"ds_read_b128 v[140:143], v29 offset:12288\n" \
"v_cvt_pk_bf16_f32 v222, v180, v181\n" \
"v_mfma_f32_16x16x32_bf16 a[252:255], v[176:179], v[144:147], a[252:255]\n" \
"ds_read_b128 v[48:51], v23 offset:0\n" \
"v_cvt_pk_bf16_f32 v223, v182, v183\n" \
"v_mfma_f32_16x16x32_bf16 a[248:251], v[108:111], v[80:83], a[248:251]\n" \
"v_accvgpr_read_b32 v224, a168\n" \
"v_accvgpr_read_b32 v225, a169\n" \
"v_mfma_f32_16x16x32_bf16 a[248:251], v[172:175], v[144:147], a[248:251]\n" \
"v_accvgpr_read_b32 v226, a170\n" \
"v_accvgpr_read_b32 v227, a171\n" \
"v_mfma_f32_16x16x32_bf16 a[244:247], v[104:107], v[80:83], a[244:247]\n" \
"v_accvgpr_read_b32 v180, a172\n" \
"v_accvgpr_read_b32 v181, a173\n" \
"v_mfma_f32_16x16x32_bf16 a[244:247], v[168:171], v[144:147], a[244:247]\n" \
"v_accvgpr_read_b32 v182, a174\n" \
"v_accvgpr_read_b32 v183, a175\n" \
"v_mfma_f32_16x16x32_bf16 a[240:243], v[100:103], v[80:83], a[240:243]\n" \
"v_cvt_pk_bf16_f32 v224, v224, v225\n" \
"v_cvt_pk_bf16_f32 v225, v226, v227\n" \
"v_mfma_f32_16x16x32_bf16 a[240:243], v[164:167], v[144:147], a[240:243]\n" \
"ds_read_b128 v[80:83], v28 offset:14336\n" \
"ds_read_b128 v[144:147], v29 offset:14336\n" \
"s_waitcnt lgkmcnt(15)\n" \
"s_nop 0\n" \
"v_mfma_f32_16x16x32_bf16 a[0:3], v[84:87], v[52:55], 0\n" \
"s_add_u32 m0, s30, 0x9c00\n" \
"s_mov_b32 s85, 0xfc80\n" \
"v_cvt_pk_bf16_f32 v226, v180, v181\n" \
"v_mfma_f32_16x16x32_bf16 a[0:3], v[148:151], v[116:119], a[0:3]\n" \
"buffer_load_dwordx4 v25, s[76:79], s85 offen lds\n" \
"v_cvt_pk_bf16_f32 v227, v182, v183\n" \
"v_mfma_f32_16x16x32_bf16 a[4:7], v[88:91], v[52:55], 0\n" \
"ds_read_b64_tr_b16 v[100:101], v36 offset:0\n" \
"v_accvgpr_read_b32 v228, a160\n" \
"v_mfma_f32_16x16x32_bf16 a[4:7], v[152:155], v[116:119], a[4:7]\n" \
"s_add_u32 m0, s31, 0x8c00\n" \
"s_mov_b32 s84, 0x98000\n" \
"v_accvgpr_read_b32 v229, a161\n" \
"s_waitcnt lgkmcnt(15)\n" \
"v_mfma_f32_16x16x32_bf16 a[36:39], v[96:99], v[52:55], 0\n" \
"buffer_load_dwordx4 v27, s[68:71], s84 offen lds\n" \
"v_accvgpr_read_b32 v230, a162\n" \
"s_waitcnt lgkmcnt(11)\n" \
"v_mfma_f32_16x16x32_bf16 a[36:39], v[160:163], v[116:119], a[36:39]\n" \
"ds_read_b64_tr_b16 v[102:103], v36 offset:1024\n" \
"v_accvgpr_read_b32 v231, a163\n" \
"s_waitcnt lgkmcnt(10)\n" \
"v_mfma_f32_16x16x32_bf16 a[32:35], v[52:55], v[92:95], 0\n" \
"ds_read_b64_tr_b16 v[164:165], v36 offset:8192\n" \
"v_accvgpr_read_b32 v180, a164\n" \
"s_waitcnt lgkmcnt(9)\n" \
"v_mfma_f32_16x16x32_bf16 a[32:35], v[116:119], v[156:159], a[32:35]\n" \
"ds_read_b64_tr_b16 v[166:167], v36 offset:9216\n" \
"v_accvgpr_read_b32 v181, a165\n" \
"v_mfma_f32_16x16x32_bf16 a[96:99], v[56:59], v[92:95], 0\n" \
"ds_read_b64_tr_b16 v[104:105], v37 offset:0\n" \
"v_accvgpr_read_b32 v182, a166\n" \
"v_mfma_f32_16x16x32_bf16 a[96:99], v[120:123], v[156:159], a[96:99]\n" \
"ds_read_b64_tr_b16 v[106:107], v37 offset:1024\n" \
"v_accvgpr_read_b32 v183, a167\n" \
"v_mfma_f32_16x16x32_bf16 a[100:103], v[96:99], v[56:59], 0\n" \
"ds_read_b64_tr_b16 v[168:169], v37 offset:8192\n" \
"v_cvt_pk_bf16_f32 v228, v228, v229\n" \
"v_mfma_f32_16x16x32_bf16 a[100:103], v[160:163], v[120:123], a[100:103]\n" \
"s_add_u32 s89, s41, 0x2000\n" \
"buffer_store_dwordx4 v[200:203], v20, s[80:83], s89 offen offset:0 sc1\n" \
"v_mfma_f32_16x16x32_bf16 a[68:71], v[88:91], v[56:59], 0\n" \
"ds_read_b64_tr_b16 v[170:171], v37 offset:9216\n" \
"v_cvt_pk_bf16_f32 v229, v230, v231\n" \
"v_mfma_f32_16x16x32_bf16 a[68:71], v[152:155], v[120:123], a[68:71]\n" \
"ds_read_b64_tr_b16 v[108:109], v38 offset:0\n" \
"v_cvt_pk_bf16_f32 v230, v180, v181\n" \
"v_mfma_f32_16x16x32_bf16 a[64:67], v[84:87], v[56:59], 0\n" \
"ds_read_b64_tr_b16 v[110:111], v38 offset:1024\n" \
"v_cvt_pk_bf16_f32 v231, v182, v183\n" \
"v_mfma_f32_16x16x32_bf16 a[64:67], v[148:151], v[120:123], a[64:67]\n" \
"v_accvgpr_read_b32 v232, a192\n" \
"v_accvgpr_read_b32 v233, a193\n" \
"v_accvgpr_read_b32 v234, a194\n" \
"v_mfma_f32_16x16x32_bf16 a[128:131], v[84:87], v[60:63], 0\n" \
"s_add_u32 s88, s41, 0x2000\n" \
"buffer_store_dwordx4 v[204:207], v20, s[80:83], s88 offen offset:1024 sc1\n" \
"v_mfma_f32_16x16x32_bf16 a[128:131], v[148:151], v[124:127], a[128:131]\n" \
"ds_read_b64_tr_b16 v[172:173], v38 offset:8192\n" \
"v_accvgpr_read_b32 v235, a195\n" \
"v_mfma_f32_16x16x32_bf16 a[132:135], v[88:91], v[60:63], 0\n" \
"ds_read_b64_tr_b16 v[174:175], v38 offset:9216\n" \
"v_accvgpr_read_b32 v180, a196\n" \
"v_mfma_f32_16x16x32_bf16 a[132:135], v[152:155], v[124:127], a[132:135]\n" \
"ds_read_b64_tr_b16 v[112:113], v39 offset:0\n" \
"v_accvgpr_read_b32 v181, a197\n" \
"v_mfma_f32_16x16x32_bf16 a[164:167], v[96:99], v[60:63], 0\n" \
"v_accvgpr_read_b32 v182, a198\n" \
"v_accvgpr_read_b32 v183, a199\n" \
"v_cvt_pk_bf16_f32 v232, v232, v233\n" \
"v_mfma_f32_16x16x32_bf16 a[164:167], v[160:163], v[124:127], a[164:167]\n" \
"ds_read_b64_tr_b16 v[114:115], v39 offset:1024\n" \
"v_cvt_pk_bf16_f32 v233, v234, v235\n" \
"v_mfma_f32_16x16x32_bf16 a[160:163], v[60:63], v[92:95], 0\n" \
"s_add_u32 s89, s41, 0x3000\n" \
"buffer_store_dwordx4 v[208:211], v20, s[80:83], s89 offen offset:1024 sc1\n" \
"v_mfma_f32_16x16x32_bf16 a[160:163], v[124:127], v[156:159], a[160:163]\n" \
"ds_read_b64_tr_b16 v[176:177], v39 offset:8192\n" \
"v_cvt_pk_bf16_f32 v234, v180, v181\n" \
"v_cvt_pk_bf16_f32 v235, v182, v183\n" \
"v_accvgpr_read_b32 v236, a200\n" \
"v_accvgpr_read_b32 v237, a201\n" \
"v_accvgpr_read_b32 v238, a202\n" \
"v_accvgpr_read_b32 v239, a203\n" \
"v_accvgpr_read_b32 v180, a204\n" \
"v_accvgpr_read_b32 v181, a205\n" \
"v_accvgpr_read_b32 v182, a206\n" \
"v_accvgpr_read_b32 v183, a207\n" \
"v_cvt_pk_bf16_f32 v236, v236, v237\n" \
"v_cvt_pk_bf16_f32 v237, v238, v239\n" \
"v_cvt_pk_bf16_f32 v238, v180, v181\n" \
"v_cvt_pk_bf16_f32 v239, v182, v183\n" \
"v_accvgpr_read_b32 v240, a232\n" \
"v_accvgpr_read_b32 v241, a233\n" \
"v_accvgpr_read_b32 v242, a234\n" \
"v_accvgpr_read_b32 v243, a235\n" \
"v_accvgpr_read_b32 v180, a236\n" \
"v_accvgpr_read_b32 v181, a237\n" \
"v_accvgpr_read_b32 v182, a238\n" \
"v_accvgpr_read_b32 v183, a239\n" \
"v_cvt_pk_bf16_f32 v240, v240, v241\n" \
"v_cvt_pk_bf16_f32 v241, v242, v243\n" \
"v_cvt_pk_bf16_f32 v242, v180, v181\n" \
"v_cvt_pk_bf16_f32 v243, v182, v183\n" \
"v_accvgpr_read_b32 v244, a224\n" \
"v_accvgpr_read_b32 v245, a225\n" \
"v_accvgpr_read_b32 v246, a226\n" \
"v_accvgpr_read_b32 v247, a227\n" \
"v_accvgpr_read_b32 v180, a228\n" \
"v_accvgpr_read_b32 v181, a229\n" \
"v_accvgpr_read_b32 v182, a230\n" \
"v_accvgpr_read_b32 v183, a231\n" \
"v_cvt_pk_bf16_f32 v244, v244, v245\n" \
"v_cvt_pk_bf16_f32 v245, v246, v247\n" \
"v_cvt_pk_bf16_f32 v246, v180, v181\n" \
"v_cvt_pk_bf16_f32 v247, v182, v183\n" \
"v_mfma_f32_16x16x32_bf16 a[224:227], v[64:67], v[92:95], 0\n" \
"ds_read_b64_tr_b16 v[178:179], v39 offset:9216\n" \
"v_accvgpr_read_b32 v184, a24\n" \
"v_mfma_f32_16x16x32_bf16 a[224:227], v[128:131], v[156:159], a[224:227]\n" \
"v_accvgpr_read_b32 v185, a25\n" \
"v_accvgpr_read_b32 v186, a26\n" \
"v_accvgpr_read_b32 v187, a27\n" \
"v_mfma_f32_16x16x32_bf16 a[228:231], v[96:99], v[64:67], 0\n" \
"v_accvgpr_read_b32 v180, a28\n" \
"v_accvgpr_read_b32 v181, a29\n" \
"v_accvgpr_read_b32 v182, a30\n" \
"v_mfma_f32_16x16x32_bf16 a[228:231], v[160:163], v[128:131], a[228:231]\n" \
"s_add_u32 s88, s41, 0x3000\n" \
"buffer_store_dwordx4 v[212:215], v20, s[80:83], s88 offen offset:0 sc1\n" \
"v_mfma_f32_16x16x32_bf16 a[196:199], v[88:91], v[64:67], 0\n" \
"v_accvgpr_read_b32 v183, a31\n" \
"v_cvt_pk_bf16_f32 v184, v184, v185\n" \
"v_cvt_pk_bf16_f32 v185, v186, v187\n" \
"v_mfma_f32_16x16x32_bf16 a[196:199], v[152:155], v[128:131], a[196:199]\n" \
"v_cvt_pk_bf16_f32 v186, v180, v181\n" \
"v_cvt_pk_bf16_f32 v187, v182, v183\n" \
"v_accvgpr_read_b32 v188, a16\n" \
"v_mfma_f32_16x16x32_bf16 a[192:195], v[84:87], v[64:67], 0\n" \
"v_accvgpr_read_b32 v189, a17\n" \
"v_accvgpr_read_b32 v190, a18\n" \
"v_accvgpr_read_b32 v191, a19\n" \
"v_mfma_f32_16x16x32_bf16 a[192:195], v[148:151], v[128:131], a[192:195]\n" \
"v_accvgpr_read_b32 v180, a20\n" \
"v_accvgpr_read_b32 v181, a21\n" \
"v_accvgpr_read_b32 v182, a22\n" \
"s_waitcnt lgkmcnt(15)\n" \
"s_nop 0\n" \
"v_mfma_f32_16x16x32_bf16 a[8:11], v[88:91], v[248:251], 0\n" \
"v_accvgpr_read_b32 v183, a23\n" \
"v_cvt_pk_bf16_f32 v188, v188, v189\n" \
"v_cvt_pk_bf16_f32 v189, v190, v191\n" \
"v_mfma_f32_16x16x32_bf16 a[8:11], v[152:155], v[48:51], a[8:11]\n" \
"v_cvt_pk_bf16_f32 v190, v180, v181\n" \
"v_cvt_pk_bf16_f32 v191, v182, v183\n" \
"v_accvgpr_read_b32 v192, a48\n" \
"v_mfma_f32_16x16x32_bf16 a[40:43], v[96:99], v[248:251], 0\n" \
"v_accvgpr_read_b32 v193, a49\n" \
"v_accvgpr_read_b32 v194, a50\n" \
"v_accvgpr_read_b32 v195, a51\n" \
"v_mfma_f32_16x16x32_bf16 a[40:43], v[160:163], v[48:51], a[40:43]\n" \
"v_accvgpr_read_b32 v180, a52\n" \
"v_accvgpr_read_b32 v181, a53\n" \
"v_accvgpr_read_b32 v182, a54\n" \
"v_mfma_f32_16x16x32_bf16 a[12:15], v[248:251], v[92:95], 0\n" \
"v_accvgpr_read_b32 v183, a55\n" \
"v_cvt_pk_bf16_f32 v192, v192, v193\n" \
"v_cvt_pk_bf16_f32 v193, v194, v195\n" \
"v_mfma_f32_16x16x32_bf16 a[12:15], v[48:51], v[156:159], a[12:15]\n" \
"v_cvt_pk_bf16_f32 v194, v180, v181\n" \
"v_cvt_pk_bf16_f32 v195, v182, v183\n" \
"v_accvgpr_read_b32 v196, a56\n" \
"s_waitcnt vmcnt(4) lgkmcnt(0)\n" \
"s_barrier\n" \
"v_mfma_f32_16x16x32_bf16 a[72:75], v[100:103], v[72:75], 0\n" \
"s_add_u32 m0, s40, 0x0\n" \
"s_nop 0\n" \
"s_add_u32 s85, s39, 0x4000\n" \
"v_accvgpr_read_b32 v197, a57\n" \
"v_mfma_f32_16x16x32_bf16 a[72:75], v[164:167], v[136:139], a[72:75]\n" \
"buffer_load_dwordx4 v20, s[80:83], s85 offen sc1 lds\n" \
"v_accvgpr_read_b32 v198, a58\n" \
"v_mfma_f32_16x16x32_bf16 a[76:79], v[104:107], v[72:75], 0\n" \
"ds_read_b64_tr_b16 v[84:85], v32 offset:32768\n" \
"v_accvgpr_read_b32 v199, a59\n" \
"v_mfma_f32_16x16x32_bf16 a[76:79], v[168:171], v[136:139], a[76:79]\n" \
"ds_read_b64_tr_b16 v[86:87], v32 offset:33280\n" \
"v_accvgpr_read_b32 v180, a60\n" \
"v_mfma_f32_16x16x32_bf16 a[104:107], v[108:111], v[72:75], 0\n" \
"s_add_u32 m0, s40, 0x1000\n" \
"s_add_u32 s84, s39, 0x5000\n" \
"v_accvgpr_read_b32 v181, a61\n" \
"v_mfma_f32_16x16x32_bf16 a[104:107], v[172:175], v[136:139], a[104:107]\n" \
"buffer_load_dwordx4 v20, s[80:83], s84 offen sc1 lds\n" \
"v_accvgpr_read_b32 v182, a62\n" \
"v_mfma_f32_16x16x32_bf16 a[108:111], v[112:115], v[72:75], 0\n" \
"ds_read_b64_tr_b16 v[148:149], v32 offset:36864\n" \
"v_accvgpr_read_b32 v183, a63\n" \
"v_mfma_f32_16x16x32_bf16 a[108:111], v[176:179], v[136:139], a[108:111]\n" \
"ds_read_b128 v[72:75], v28 offset:43008\n" \
"v_cvt_pk_bf16_f32 v196, v196, v197\n" \
"v_mfma_f32_16x16x32_bf16 a[172:175], v[112:115], v[76:79], 0\n" \
"ds_read_b128 v[136:139], v29 offset:43008\n" \
"v_cvt_pk_bf16_f32 v197, v198, v199\n" \
"v_mfma_f32_16x16x32_bf16 a[172:175], v[176:179], v[140:143], a[172:175]\n" \
"s_add_u32 m0, s40, 0x3000\n" \
"s_mov_b32 s85, 0x100\n" \
"v_cvt_pk_bf16_f32 v198, v180, v181\n" \
"v_mfma_f32_16x16x32_bf16 a[168:171], v[108:111], v[76:79], 0\n" \
"buffer_load_dwordx4 v21, s[76:79], s85 offen lds\n" \
"v_cvt_pk_bf16_f32 v199, v182, v183\n" \
"v_mfma_f32_16x16x32_bf16 a[168:171], v[172:175], v[140:143], a[168:171]\n" \
"ds_read_b64_tr_b16 v[150:151], v32 offset:37376\n" \
"v_accvgpr_read_b32 v252, a88\n" \
"v_mfma_f32_16x16x32_bf16 a[140:143], v[104:107], v[76:79], 0\n" \
"s_add_u32 m0, s30, 0x0\n" \
"s_nop 0\n" \
"s_mov_b32 s84, 0x100\n" \
"v_accvgpr_read_b32 v253, a89\n" \
"v_mfma_f32_16x16x32_bf16 a[140:143], v[168:171], v[140:143], a[140:143]\n" \
"buffer_load_dwordx4 v24, s[76:79], s84 offen lds\n" \
"v_accvgpr_read_b32 v254, a90\n" \
"v_mfma_f32_16x16x32_bf16 a[136:139], v[100:103], v[76:79], 0\n" \
"ds_read_b64_tr_b16 v[88:89], v33 offset:32768\n" \
"v_accvgpr_read_b32 v255, a91\n" \
"v_mfma_f32_16x16x32_bf16 a[136:139], v[164:167], v[140:143], a[136:139]\n" \
"ds_read_b128 v[76:79], v28 offset:45056\n" \
"v_accvgpr_read_b32 v180, a92\n" \
"v_mfma_f32_16x16x32_bf16 a[200:203], v[100:103], v[80:83], 0\n" \
"ds_read_b128 v[140:143], v29 offset:45056\n" \
"v_accvgpr_read_b32 v181, a93\n" \
"v_mfma_f32_16x16x32_bf16 a[200:203], v[164:167], v[144:147], a[200:203]\n" \
"s_add_u32 m0, s30, 0x400\n" \
"s_mov_b32 s85, 0x2500\n" \
"v_accvgpr_read_b32 v182, a94\n" \
"v_mfma_f32_16x16x32_bf16 a[204:207], v[104:107], v[80:83], 0\n" \
"buffer_load_dwordx4 v25, s[76:79], s85 offen lds\n" \
"v_accvgpr_read_b32 v183, a95\n" \
"v_mfma_f32_16x16x32_bf16 a[204:207], v[168:171], v[144:147], a[204:207]\n" \
"ds_read_b64_tr_b16 v[90:91], v33 offset:33280\n" \
"v_cvt_pk_bf16_f32 v252, v252, v253\n" \
"v_mfma_f32_16x16x32_bf16 a[232:235], v[108:111], v[80:83], 0\n" \
"ds_read_b64_tr_b16 v[152:153], v33 offset:36864\n" \
"v_cvt_pk_bf16_f32 v253, v254, v255\n" \
"v_mfma_f32_16x16x32_bf16 a[232:235], v[172:175], v[144:147], a[232:235]\n" \
"s_add_u32 m0, s31, 0x0\n" \
"s_nop 0\n" \
"s_mov_b32 s84, 0x100000\n" \
"v_cvt_pk_bf16_f32 v254, v180, v181\n" \
"v_mfma_f32_16x16x32_bf16 a[236:239], v[112:115], v[80:83], 0\n" \
"buffer_load_dwordx4 v26, s[68:71], s84 offen lds\n" \
"v_cvt_pk_bf16_f32 v255, v182, v183\n" \
"v_mfma_f32_16x16x32_bf16 a[236:239], v[176:179], v[144:147], a[236:239]\n" \
"ds_read_b128 v[80:83], v28 offset:47104\n" \
"v_accvgpr_write_b32 a44, v252\n" \
"v_mfma_f32_16x16x32_bf16 a[28:31], v[112:115], v[52:55], 0\n" \
"ds_read_b128 v[144:147], v29 offset:47104\n" \
"v_accvgpr_write_b32 a45, v253\n" \
"v_mfma_f32_16x16x32_bf16 a[28:31], v[176:179], v[116:119], a[28:31]\n" \
"s_add_u32 m0, s30, 0x800\n" \
"s_mov_b32 s85, 0x4900\n" \
"v_accvgpr_write_b32 a46, v254\n" \
"v_mfma_f32_16x16x32_bf16 a[24:27], v[108:111], v[52:55], 0\n" \
"buffer_load_dwordx4 v24, s[76:79], s85 offen lds\n" \
"v_accvgpr_write_b32 a47, v255\n" \
"v_mfma_f32_16x16x32_bf16 a[24:27], v[172:175], v[116:119], a[24:27]\n" \
"ds_read_b64_tr_b16 v[154:155], v33 offset:37376\n" \
"v_accvgpr_read_b32 v252, a80\n" \
"v_mfma_f32_16x16x32_bf16 a[20:23], v[104:107], v[52:55], 0\n" \
"ds_read_b64_tr_b16 v[96:97], v35 offset:32768\n" \
"v_accvgpr_read_b32 v253, a81\n" \
"v_mfma_f32_16x16x32_bf16 a[20:23], v[168:171], v[116:119], a[20:23]\n" \
"ds_read_b64_tr_b16 v[98:99], v35 offset:33280\n" \
"v_accvgpr_read_b32 v254, a82\n" \
"v_mfma_f32_16x16x32_bf16 a[16:19], v[100:103], v[52:55], 0\n" \
"ds_read_b64_tr_b16 v[160:161], v35 offset:36864\n" \
"v_accvgpr_read_b32 v255, a83\n" \
"v_mfma_f32_16x16x32_bf16 a[16:19], v[164:167], v[116:119], a[16:19]\n" \
"ds_read_b128 v[52:55], v28 offset:32768\n" \
"v_accvgpr_read_b32 v180, a84\n" \
"v_mfma_f32_16x16x32_bf16 a[48:51], v[100:103], v[56:59], 0\n" \
"ds_read_b128 v[116:119], v29 offset:32768\n" \
"v_accvgpr_read_b32 v181, a85\n" \
"v_mfma_f32_16x16x32_bf16 a[48:51], v[164:167], v[120:123], a[48:51]\n" \
"s_add_u32 m0, s30, 0xc00\n" \
"s_mov_b32 s84, 0x6d00\n" \
"v_accvgpr_read_b32 v182, a86\n" \
"v_mfma_f32_16x16x32_bf16 a[52:55], v[104:107], v[56:59], 0\n" \
"buffer_load_dwordx4 v25, s[76:79], s84 offen lds\n" \
"v_accvgpr_read_b32 v183, a87\n" \
"v_mfma_f32_16x16x32_bf16 a[52:55], v[168:171], v[120:123], a[52:55]\n" \
"s_add_u32 m0, s31, 0x400\n" \
"s_mov_b32 s85, 0x108000\n" \
"v_cvt_pk_bf16_f32 v252, v252, v253\n" \
"v_mfma_f32_16x16x32_bf16 a[56:59], v[108:111], v[56:59], 0\n" \
"buffer_load_dwordx4 v26, s[68:71], s85 offen lds\n" \
"v_cvt_pk_bf16_f32 v253, v254, v255\n" \
"v_mfma_f32_16x16x32_bf16 a[56:59], v[172:175], v[120:123], a[56:59]\n" \
"ds_read_b64_tr_b16 v[162:163], v35 offset:37376\n" \
"v_cvt_pk_bf16_f32 v254, v180, v181\n" \
"v_mfma_f32_16x16x32_bf16 a[60:63], v[112:115], v[56:59], 0\n" \
"ds_read_b64_tr_b16 v[92:93], v34 offset:32768\n" \
"v_cvt_pk_bf16_f32 v255, v182, v183\n" \
"v_mfma_f32_16x16x32_bf16 a[60:63], v[176:179], v[120:123], a[60:63]\n" \
"ds_read_b128 v[56:59], v28 offset:34816\n" \
"v_accvgpr_read_b32 v200, a112\n" \
"v_mfma_f32_16x16x32_bf16 a[92:95], v[112:115], v[60:63], 0\n" \
"ds_read_b128 v[120:123], v29 offset:34816\n" \
"v_accvgpr_read_b32 v201, a113\n" \
"v_mfma_f32_16x16x32_bf16 a[92:95], v[176:179], v[124:127], a[92:95]\n" \
"s_add_u32 m0, s30, 0x1000\n" \
"s_mov_b32 s84, 0x9100\n" \
"v_accvgpr_read_b32 v202, a114\n" \
"v_mfma_f32_16x16x32_bf16 a[88:91], v[108:111], v[60:63], 0\n" \
"buffer_load_dwordx4 v24, s[76:79], s84 offen lds\n" \
"v_accvgpr_read_b32 v203, a115\n" \
"v_mfma_f32_16x16x32_bf16 a[88:91], v[172:175], v[124:127], a[88:91]\n" \
"s_add_u32 m0, s30, 0x1400\n" \
"s_mov_b32 s85, 0xb500\n" \
"v_accvgpr_read_b32 v180, a116\n" \
"v_mfma_f32_16x16x32_bf16 a[84:87], v[104:107], v[60:63], 0\n" \
"buffer_load_dwordx4 v25, s[76:79], s85 offen lds\n" \
"v_accvgpr_read_b32 v181, a117\n" \
"v_mfma_f32_16x16x32_bf16 a[84:87], v[168:171], v[124:127], a[84:87]\n" \
"ds_read_b64_tr_b16 v[94:95], v34 offset:33280\n" \
"v_accvgpr_read_b32 v182, a118\n" \
"v_mfma_f32_16x16x32_bf16 a[80:83], v[100:103], v[60:63], 0\n" \
"ds_read_b64_tr_b16 v[156:157], v34 offset:36864\n" \
"v_accvgpr_read_b32 v183, a119\n" \
"v_mfma_f32_16x16x32_bf16 a[80:83], v[164:167], v[124:127], a[80:83]\n" \
"ds_read_b128 v[60:63], v28 offset:36864\n" \
"v_cvt_pk_bf16_f32 v200, v200, v201\n" \
"v_cvt_pk_bf16_f32 v201, v202, v203\n" \
"v_cvt_pk_bf16_f32 v202, v180, v181\n" \
"v_cvt_pk_bf16_f32 v203, v182, v183\n" \
"v_mfma_f32_16x16x32_bf16 a[112:115], v[100:103], v[64:67], 0\n" \
"ds_read_b128 v[124:127], v29 offset:36864\n" \
"v_accvgpr_read_b32 v204, a120\n" \
"v_mfma_f32_16x16x32_bf16 a[112:115], v[164:167], v[128:131], a[112:115]\n" \
"s_add_u32 m0, s31, 0x800\n" \
"s_mov_b32 s84, 0x110000\n" \
"v_accvgpr_read_b32 v205, a121\n" \
"v_mfma_f32_16x16x32_bf16 a[116:119], v[104:107], v[64:67], 0\n" \
"buffer_load_dwordx4 v27, s[68:71], s84 offen lds\n" \
"v_accvgpr_read_b32 v206, a122\n" \
"v_mfma_f32_16x16x32_bf16 a[116:119], v[168:171], v[128:131], a[116:119]\n" \
"s_add_u32 m0, s30, 0x1800\n" \
"s_mov_b32 s85, 0xd900\n" \
"v_accvgpr_read_b32 v207, a123\n" \
"v_accvgpr_read_b32 v180, a124\n" \
"v_accvgpr_read_b32 v181, a125\n" \
"v_accvgpr_read_b32 v182, a126\n" \
"v_accvgpr_read_b32 v183, a127\n" \
"v_cvt_pk_bf16_f32 v204, v204, v205\n" \
"v_cvt_pk_bf16_f32 v205, v206, v207\n" \
"v_cvt_pk_bf16_f32 v206, v180, v181\n" \
"v_cvt_pk_bf16_f32 v207, v182, v183\n" \
"v_mfma_f32_16x16x32_bf16 a[120:123], v[108:111], v[64:67], 0\n" \
"buffer_load_dwordx4 v24, s[76:79], s85 offen lds\n" \
"v_accvgpr_read_b32 v208, a152\n" \
"v_mfma_f32_16x16x32_bf16 a[120:123], v[172:175], v[128:131], a[120:123]\n" \
"ds_read_b64_tr_b16 v[158:159], v34 offset:37376\n" \
"v_accvgpr_read_b32 v209, a153\n" \
"v_mfma_f32_16x16x32_bf16 a[124:127], v[112:115], v[64:67], 0\n" \
"ds_read_b128 v[248:251], v22 offset:32768\n" \
"v_accvgpr_read_b32 v210, a154\n" \
"v_mfma_f32_16x16x32_bf16 a[124:127], v[176:179], v[128:131], a[124:127]\n" \
"ds_read_b128 v[64:67], v28 offset:38912\n" \
"v_accvgpr_read_b32 v211, a155\n" \
"v_accvgpr_read_b32 v180, a156\n" \
"v_accvgpr_read_b32 v181, a157\n" \
"v_accvgpr_read_b32 v182, a158\n" \
"v_accvgpr_read_b32 v183, a159\n" \
"v_cvt_pk_bf16_f32 v208, v208, v209\n" \
"v_cvt_pk_bf16_f32 v209, v210, v211\n" \
"v_cvt_pk_bf16_f32 v210, v180, v181\n" \
"v_cvt_pk_bf16_f32 v211, v182, v183\n" \
"v_mfma_f32_16x16x32_bf16 a[156:159], v[112:115], v[68:71], 0\n" \
"ds_read_b128 v[128:131], v29 offset:38912\n" \
"v_accvgpr_read_b32 v212, a144\n" \
"v_mfma_f32_16x16x32_bf16 a[156:159], v[176:179], v[132:135], a[156:159]\n" \
"ds_read_b128 v[48:51], v23 offset:32768\n" \
"v_accvgpr_read_b32 v213, a145\n" \
"v_mfma_f32_16x16x32_bf16 a[152:155], v[108:111], v[68:71], 0\n" \
"v_accvgpr_read_b32 v214, a146\n" \
"v_accvgpr_read_b32 v215, a147\n" \
"v_accvgpr_read_b32 v180, a148\n" \
"v_mfma_f32_16x16x32_bf16 a[152:155], v[172:175], v[132:135], a[152:155]\n" \
"v_accvgpr_read_b32 v181, a149\n" \
"v_accvgpr_read_b32 v182, a150\n" \
"v_accvgpr_read_b32 v183, a151\n" \
"v_cvt_pk_bf16_f32 v212, v212, v213\n" \
"v_cvt_pk_bf16_f32 v213, v214, v215\n" \
"v_cvt_pk_bf16_f32 v214, v180, v181\n" \
"v_cvt_pk_bf16_f32 v215, v182, v183\n" \
"v_mfma_f32_16x16x32_bf16 a[148:151], v[104:107], v[68:71], 0\n" \
"v_mfma_f32_16x16x32_bf16 a[148:151], v[168:171], v[132:135], a[148:151]\n" \
"v_mfma_f32_16x16x32_bf16 a[144:147], v[100:103], v[68:71], 0\n" \
"v_mfma_f32_16x16x32_bf16 a[144:147], v[164:167], v[132:135], a[144:147]\n" \
"ds_read_b128 v[68:71], v28 offset:40960\n" \
"ds_read_b128 v[132:135], v29 offset:40960\n" \
"s_waitcnt lgkmcnt(15)\n" \
"v_mfma_f32_16x16x32_bf16 a[0:3], v[84:87], v[52:55], a[0:3]\n" \
"s_add_u32 m0, s30, 0x1c00\n" \
"s_mov_b32 s85, 0xfd00\n" \
"v_mfma_f32_16x16x32_bf16 a[0:3], v[148:151], v[116:119], a[0:3]\n" \
"buffer_load_dwordx4 v25, s[76:79], s85 offen lds\n" \
"v_mfma_f32_16x16x32_bf16 a[4:7], v[88:91], v[52:55], a[4:7]\n" \
"ds_read_b64_tr_b16 v[100:101], v36 offset:32768\n" \
"v_mov_b32_e32 v11, 0\n" \
"v_mfma_f32_16x16x32_bf16 a[4:7], v[152:155], v[116:119], a[4:7]\n" \
"s_add_u32 m0, s31, 0xc00\n" \
"s_mov_b32 s84, 0x118000\n" \
"v_mfma_f32_16x16x32_bf16 a[36:39], v[96:99], v[52:55], a[36:39]\n" \
"buffer_load_dwordx4 v27, s[68:71], s84 offen lds\n" \
"s_waitcnt lgkmcnt(15)\n" \
"v_mfma_f32_16x16x32_bf16 a[36:39], v[160:163], v[116:119], a[36:39]\n" \
"ds_read_b64_tr_b16 v[102:103], v36 offset:33792\n" \
"s_and_b32 s6, s2, 7\n" \
"s_waitcnt lgkmcnt(12)\n" \
"v_mfma_f32_16x16x32_bf16 a[32:35], v[52:55], v[92:95], a[32:35]\n" \
"ds_read_b64_tr_b16 v[164:165], v36 offset:40960\n" \
"s_lshl_b32 s7, s3, 1\n" \
"s_waitcnt lgkmcnt(9)\n" \
"v_mfma_f32_16x16x32_bf16 a[32:35], v[116:119], v[156:159], a[32:35]\n" \
"ds_read_b64_tr_b16 v[166:167], v36 offset:41984\n" \
"s_lshr_b32 s4, s2, 5\n" \
"v_mfma_f32_16x16x32_bf16 a[96:99], v[56:59], v[92:95], a[96:99]\n" \
"ds_read_b64_tr_b16 v[104:105], v37 offset:32768\n" \
"s_lshl_b32 s5, s6, 2\n" \
"v_mfma_f32_16x16x32_bf16 a[96:99], v[120:123], v[156:159], a[96:99]\n" \
"ds_read_b64_tr_b16 v[106:107], v37 offset:33792\n" \
"s_bfe_u32 s8, s2, 0x20003\n" \
"v_mfma_f32_16x16x32_bf16 a[100:103], v[96:99], v[56:59], a[100:103]\n" \
"ds_read_b64_tr_b16 v[168:169], v37 offset:40960\n" \
"s_and_b32 s7, s7, 4\n" \
"v_mfma_f32_16x16x32_bf16 a[100:103], v[160:163], v[120:123], a[100:103]\n" \
"s_add_u32 s89, s41, 0x0\n" \
"buffer_store_dwordx4 v[184:187], v20, s[80:83], s89 offen offset:3072 sc1\n" \
"v_mfma_f32_16x16x32_bf16 a[68:71], v[88:91], v[56:59], a[68:71]\n" \
"ds_read_b64_tr_b16 v[170:171], v37 offset:41984\n" \
"s_and_b32 s9, s3, 1\n" \
"v_mfma_f32_16x16x32_bf16 a[68:71], v[152:155], v[120:123], a[68:71]\n" \
"ds_read_b64_tr_b16 v[108:109], v38 offset:32768\n" \
"s_or_b32 s13, s5, s8\n" \
"v_mfma_f32_16x16x32_bf16 a[64:67], v[84:87], v[56:59], a[64:67]\n" \
"ds_read_b64_tr_b16 v[110:111], v38 offset:33792\n" \
"s_or_b32 s9, s7, s9\n" \
"v_mfma_f32_16x16x32_bf16 a[64:67], v[148:151], v[120:123], a[64:67]\n" \
"s_mul_i32 s7, s4, 0xa00\n" \
"s_add_i32 s7, s7, 0x5bc1000\n" \
"v_mfma_f32_16x16x32_bf16 a[128:131], v[84:87], v[60:63], a[128:131]\n" \
"s_add_u32 s88, s41, 0x0\n" \
"buffer_store_dwordx4 v[188:191], v20, s[80:83], s88 offen offset:2048 sc1\n" \
"v_mfma_f32_16x16x32_bf16 a[128:131], v[148:151], v[124:127], a[128:131]\n" \
"ds_read_b64_tr_b16 v[172:173], v38 offset:40960\n" \
"s_lshl_b32 s10, s13, 2\n" \
"v_mfma_f32_16x16x32_bf16 a[132:135], v[88:91], v[60:63], a[132:135]\n" \
"ds_read_b64_tr_b16 v[174:175], v38 offset:41984\n" \
"s_or_b32 s10, s10, s7\n" \
"v_mfma_f32_16x16x32_bf16 a[132:135], v[152:155], v[124:127], a[132:135]\n" \
"ds_read_b64_tr_b16 v[112:113], v39 offset:32768\n" \
"v_writelane_b32 v11, s10, 0\n" \
"v_mfma_f32_16x16x32_bf16 a[164:167], v[96:99], v[60:63], a[164:167]\n" \
"s_lshl_b32 s14, s4, 3\n" \
"v_writelane_b32 v11, s7, 1\n" \
"v_mfma_f32_16x16x32_bf16 a[164:167], v[160:163], v[124:127], a[164:167]\n" \
"ds_read_b64_tr_b16 v[114:115], v39 offset:33792\n" \
"s_or_b32 s7, s14, s6\n" \
"v_mfma_f32_16x16x32_bf16 a[160:163], v[60:63], v[92:95], a[160:163]\n" \
"s_add_u32 s89, s41, 0x1000\n" \
"buffer_store_dwordx4 v[192:195], v20, s[80:83], s89 offen offset:2048 sc1\n" \
"v_mfma_f32_16x16x32_bf16 a[160:163], v[124:127], v[156:159], a[160:163]\n" \
"ds_read_b64_tr_b16 v[176:177], v39 offset:40960\n" \
"s_lshl_b32 s7, s7, 10\n" \
"s_waitcnt lgkmcnt(15)\n" \
"v_mfma_f32_16x16x32_bf16 a[224:227], v[64:67], v[92:95], a[224:227]\n" \
"ds_read_b64_tr_b16 v[178:179], v39 offset:41984\n" \
"s_lshl_b32 s10, s3, 8\n" \
"v_mfma_f32_16x16x32_bf16 a[224:227], v[128:131], v[156:159], a[224:227]\n" \
"s_or_b32 s7, s7, s10\n" \
"s_or_b32 s10, s7, 0x5c00200\n" \
"v_mfma_f32_16x16x32_bf16 a[228:231], v[96:99], v[64:67], a[228:231]\n" \
"v_writelane_b32 v11, s10, 2\n" \
"s_lshl_b32 s10, s8, 2\n" \
"v_mfma_f32_16x16x32_bf16 a[228:231], v[160:163], v[128:131], a[228:231]\n" \
"s_add_u32 s88, s41, 0x1000\n" \
"buffer_store_dwordx4 v[196:199], v20, s[80:83], s88 offen offset:3072 sc1\n" \
"v_mfma_f32_16x16x32_bf16 a[196:199], v[88:91], v[64:67], a[196:199]\n" \
"s_or_b32 s11, s10, s7\n" \
"s_or_b32 s11, s11, 0x5c00000\n" \
"v_mfma_f32_16x16x32_bf16 a[196:199], v[152:155], v[128:131], a[196:199]\n" \
"v_writelane_b32 v11, s11, 3\n" \
"s_or_b32 s7, s7, 0x5c00080\n" \
"v_mfma_f32_16x16x32_bf16 a[192:195], v[84:87], v[64:67], a[192:195]\n" \
"v_writelane_b32 v11, s7, 4\n" \
"s_or_b32 s7, s7, s10\n" \
"v_mfma_f32_16x16x32_bf16 a[192:195], v[148:151], v[128:131], a[192:195]\n" \
"s_add_i32 s10, s3, s2\n" \
"v_writelane_b32 v11, s7, 5\n" \
"v_mfma_f32_16x16x32_bf16 a[8:11], v[88:91], v[248:251], a[8:11]\n" \
"s_mov_b32 s7, 2.0\n" \
"s_mov_b32 s11, 0x5bc0000\n" \
"v_mfma_f32_16x16x32_bf16 a[8:11], v[152:155], v[48:51], a[8:11]\n" \
"s_cmp_lg_u32 s10, 0\n" \
"s_cselect_b32 s10, s7, s11\n" \
"v_mfma_f32_16x16x32_bf16 a[40:43], v[96:99], v[248:251], a[40:43]\n" \
"s_lshr_b32 s15, s2, 3\n" \
"v_writelane_b32 v11, s10, 53\n" \
"v_mfma_f32_16x16x32_bf16 a[40:43], v[160:163], v[48:51], a[40:43]\n" \
"s_lshl_b32 s10, s6, 5\n" \
"s_min_u32 s16, s3, 2\n" \
"v_mfma_f32_16x16x32_bf16 a[12:15], v[248:251], v[92:95], a[12:15]\n" \
"s_mul_i32 s12, s8, 0x120\n" \
"v_mfma_f32_16x16x32_bf16 a[12:15], v[48:51], v[156:159], a[12:15]\n" \
"s_waitcnt vmcnt(4) lgkmcnt(0)\n" \
"s_barrier\n" \
"s_nop 0\n" \
"v_mfma_f32_16x16x32_bf16 a[16:19], v[100:103], v[52:55], a[16:19]\n" \
"s_add_u32 m0, s40, 0x8000\n" \
"s_add_u32 s85, s39, 0x6000\n" \
"v_mfma_f32_16x16x32_bf16 a[16:19], v[164:167], v[116:119], a[16:19]\n" \
"buffer_load_dwordx4 v20, s[80:83], s85 offen sc1 lds\n" \
"v_mfma_f32_16x16x32_bf16 a[20:23], v[104:107], v[52:55], a[20:23]\n" \
"ds_read_b64_tr_b16 v[84:85], v32 offset:0\n" \
"v_mfma_f32_16x16x32_bf16 a[20:23], v[168:171], v[116:119], a[20:23]\n" \
"ds_read_b64_tr_b16 v[86:87], v32 offset:512\n" \
"v_mfma_f32_16x16x32_bf16 a[24:27], v[108:111], v[52:55], a[24:27]\n" \
"s_add_u32 m0, s40, 0x9000\n" \
"s_add_u32 s84, s39, 0x7000\n" \
"v_mfma_f32_16x16x32_bf16 a[24:27], v[172:175], v[116:119], a[24:27]\n" \
"buffer_load_dwordx4 v20, s[80:83], s84 offen sc1 lds\n" \
"v_mfma_f32_16x16x32_bf16 a[28:31], v[112:115], v[52:55], a[28:31]\n" \
"ds_read_b64_tr_b16 v[148:149], v32 offset:4096\n" \
"v_mfma_f32_16x16x32_bf16 a[28:31], v[176:179], v[116:119], a[28:31]\n" \
"ds_read_b128 v[52:55], v28 offset:0\n" \
"v_mfma_f32_16x16x32_bf16 a[60:63], v[112:115], v[56:59], a[60:63]\n" \
"ds_read_b128 v[116:119], v29 offset:0\n" \
"v_mfma_f32_16x16x32_bf16 a[60:63], v[176:179], v[120:123], a[60:63]\n" \
"s_add_u32 m0, s40, 0xb000\n" \
"s_mov_b32 s85, 0x180\n" \
"v_mfma_f32_16x16x32_bf16 a[56:59], v[108:111], v[56:59], a[56:59]\n" \
"buffer_load_dwordx4 v21, s[76:79], s85 offen lds\n" \
"v_mfma_f32_16x16x32_bf16 a[56:59], v[172:175], v[120:123], a[56:59]\n" \
"ds_read_b64_tr_b16 v[150:151], v32 offset:4608\n" \
"v_mfma_f32_16x16x32_bf16 a[52:55], v[104:107], v[56:59], a[52:55]\n" \
"s_add_u32 m0, s30, 0x8000\n" \
"s_mov_b32 s84, 0x180\n" \
"v_mfma_f32_16x16x32_bf16 a[52:55], v[168:171], v[120:123], a[52:55]\n" \
"buffer_load_dwordx4 v24, s[76:79], s84 offen lds\n" \
"v_mfma_f32_16x16x32_bf16 a[48:51], v[100:103], v[56:59], a[48:51]\n" \
"ds_read_b64_tr_b16 v[88:89], v33 offset:0\n" \
"v_mfma_f32_16x16x32_bf16 a[48:51], v[164:167], v[120:123], a[48:51]\n" \
"ds_read_b128 v[56:59], v28 offset:2048\n" \
"v_mfma_f32_16x16x32_bf16 a[80:83], v[100:103], v[60:63], a[80:83]\n" \
"ds_read_b128 v[120:123], v29 offset:2048\n" \
"v_mfma_f32_16x16x32_bf16 a[80:83], v[164:167], v[124:127], a[80:83]\n" \
"s_add_u32 m0, s30, 0x8400\n" \
"s_mov_b32 s85, 0x2580\n" \
"v_mfma_f32_16x16x32_bf16 a[84:87], v[104:107], v[60:63], a[84:87]\n" \
"buffer_load_dwordx4 v25, s[76:79], s85 offen lds\n" \
"v_mfma_f32_16x16x32_bf16 a[84:87], v[168:171], v[124:127], a[84:87]\n" \
"ds_read_b64_tr_b16 v[90:91], v33 offset:512\n" \
"v_mfma_f32_16x16x32_bf16 a[88:91], v[108:111], v[60:63], a[88:91]\n" \
"ds_read_b64_tr_b16 v[152:153], v33 offset:4096\n" \
"v_mfma_f32_16x16x32_bf16 a[88:91], v[172:175], v[124:127], a[88:91]\n" \
"s_add_u32 m0, s31, 0x8000\n" \
"s_mov_b32 s84, 0x180000\n" \
"v_mfma_f32_16x16x32_bf16 a[92:95], v[112:115], v[60:63], a[92:95]\n" \
"buffer_load_dwordx4 v26, s[68:71], s84 offen lds\n" \
"v_mfma_f32_16x16x32_bf16 a[92:95], v[176:179], v[124:127], a[92:95]\n" \
"ds_read_b128 v[60:63], v28 offset:4096\n" \
"v_mfma_f32_16x16x32_bf16 a[124:127], v[112:115], v[64:67], a[124:127]\n" \
"ds_read_b128 v[124:127], v29 offset:4096\n" \
"v_mfma_f32_16x16x32_bf16 a[124:127], v[176:179], v[128:131], a[124:127]\n" \
"s_add_u32 m0, s30, 0x8800\n" \
"s_mov_b32 s85, 0x4980\n" \
"v_mfma_f32_16x16x32_bf16 a[120:123], v[108:111], v[64:67], a[120:123]\n" \
"buffer_load_dwordx4 v24, s[76:79], s85 offen lds\n" \
"v_mfma_f32_16x16x32_bf16 a[120:123], v[172:175], v[128:131], a[120:123]\n" \
"ds_read_b64_tr_b16 v[154:155], v33 offset:4608\n" \
"v_mfma_f32_16x16x32_bf16 a[116:119], v[104:107], v[64:67], a[116:119]\n" \
"ds_read_b64_tr_b16 v[96:97], v35 offset:0\n" \
"v_mfma_f32_16x16x32_bf16 a[116:119], v[168:171], v[128:131], a[116:119]\n" \
"ds_read_b64_tr_b16 v[98:99], v35 offset:512\n" \
"v_mfma_f32_16x16x32_bf16 a[112:115], v[100:103], v[64:67], a[112:115]\n" \
"ds_read_b64_tr_b16 v[160:161], v35 offset:4096\n" \
"v_mfma_f32_16x16x32_bf16 a[112:115], v[164:167], v[128:131], a[112:115]\n" \
"ds_read_b128 v[64:67], v28 offset:6144\n" \
"v_mfma_f32_16x16x32_bf16 a[144:147], v[100:103], v[68:71], a[144:147]\n" \
"ds_read_b128 v[128:131], v29 offset:6144\n" \
"v_mfma_f32_16x16x32_bf16 a[144:147], v[164:167], v[132:135], a[144:147]\n" \
"s_add_u32 m0, s30, 0x8c00\n" \
"s_mov_b32 s84, 0x6d80\n" \
"v_mfma_f32_16x16x32_bf16 a[148:151], v[104:107], v[68:71], a[148:151]\n" \
"buffer_load_dwordx4 v25, s[76:79], s84 offen lds\n" \
"v_mfma_f32_16x16x32_bf16 a[148:151], v[168:171], v[132:135], a[148:151]\n" \
"s_add_u32 m0, s31, 0x8400\n" \
"s_mov_b32 s85, 0x188000\n" \
"v_mfma_f32_16x16x32_bf16 a[152:155], v[108:111], v[68:71], a[152:155]\n" \
"buffer_load_dwordx4 v26, s[68:71], s85 offen lds\n" \
"v_mfma_f32_16x16x32_bf16 a[152:155], v[172:175], v[132:135], a[152:155]\n" \
"ds_read_b64_tr_b16 v[162:163], v35 offset:4608\n" \
"v_mfma_f32_16x16x32_bf16 a[156:159], v[112:115], v[68:71], a[156:159]\n" \
"ds_read_b64_tr_b16 v[92:93], v34 offset:0\n" \
"v_mfma_f32_16x16x32_bf16 a[156:159], v[176:179], v[132:135], a[156:159]\n" \
"ds_read_b128 v[68:71], v28 offset:8192\n" \
"v_mfma_f32_16x16x32_bf16 a[108:111], v[112:115], v[72:75], a[108:111]\n" \
"ds_read_b128 v[132:135], v29 offset:8192\n" \
"v_mfma_f32_16x16x32_bf16 a[108:111], v[176:179], v[136:139], a[108:111]\n" \
"s_add_u32 m0, s30, 0x9000\n" \
"s_mov_b32 s84, 0x9180\n" \
"v_mfma_f32_16x16x32_bf16 a[104:107], v[108:111], v[72:75], a[104:107]\n" \
"buffer_load_dwordx4 v24, s[76:79], s84 offen lds\n" \
"v_mfma_f32_16x16x32_bf16 a[104:107], v[172:175], v[136:139], a[104:107]\n" \
"s_add_u32 m0, s30, 0x9400\n" \
"s_mov_b32 s85, 0xb580\n" \
"v_mfma_f32_16x16x32_bf16 a[76:79], v[104:107], v[72:75], a[76:79]\n" \
"buffer_load_dwordx4 v25, s[76:79], s85 offen lds\n" \
"v_mfma_f32_16x16x32_bf16 a[76:79], v[168:171], v[136:139], a[76:79]\n" \
"ds_read_b64_tr_b16 v[94:95], v34 offset:512\n" \
"v_mfma_f32_16x16x32_bf16 a[72:75], v[100:103], v[72:75], a[72:75]\n" \
"ds_read_b64_tr_b16 v[156:157], v34 offset:4096\n" \
"v_mfma_f32_16x16x32_bf16 a[72:75], v[164:167], v[136:139], a[72:75]\n" \
"ds_read_b128 v[72:75], v28 offset:10240\n" \
"v_mfma_f32_16x16x32_bf16 a[136:139], v[100:103], v[76:79], a[136:139]\n" \
"ds_read_b128 v[136:139], v29 offset:10240\n" \
"v_mfma_f32_16x16x32_bf16 a[136:139], v[164:167], v[140:143], a[136:139]\n" \
"s_add_u32 m0, s31, 0x8800\n" \
"s_mov_b32 s84, 0x190000\n" \
"v_mfma_f32_16x16x32_bf16 a[140:143], v[104:107], v[76:79], a[140:143]\n" \
"buffer_load_dwordx4 v27, s[68:71], s84 offen lds\n" \
"v_mfma_f32_16x16x32_bf16 a[140:143], v[168:171], v[140:143], a[140:143]\n" \
"s_add_u32 m0, s30, 0x9800\n" \
"s_mov_b32 s85, 0xd980\n" \
"v_mfma_f32_16x16x32_bf16 a[168:171], v[108:111], v[76:79], a[168:171]\n" \
"buffer_load_dwordx4 v24, s[76:79], s85 offen lds\n" \
"v_mfma_f32_16x16x32_bf16 a[168:171], v[172:175], v[140:143], a[168:171]\n" \
"ds_read_b64_tr_b16 v[158:159], v34 offset:4608\n" \
"v_mfma_f32_16x16x32_bf16 a[172:175], v[112:115], v[76:79], a[172:175]\n" \
"ds_read_b128 v[248:251], v22 offset:0\n" \
"v_mfma_f32_16x16x32_bf16 a[172:175], v[176:179], v[140:143], a[172:175]\n" \
"ds_read_b128 v[76:79], v28 offset:12288\n" \
"v_mfma_f32_16x16x32_bf16 a[236:239], v[112:115], v[80:83], a[236:239]\n" \
"ds_read_b128 v[140:143], v29 offset:12288\n" \
"v_mfma_f32_16x16x32_bf16 a[236:239], v[176:179], v[144:147], a[236:239]\n" \
"ds_read_b128 v[48:51], v23 offset:0\n" \
"v_mfma_f32_16x16x32_bf16 a[232:235], v[108:111], v[80:83], a[232:235]\n" \
"s_add_u32 s11, s22, s12\n" \
"s_addc_u32 s17, s23, 0\n" \
"v_mfma_f32_16x16x32_bf16 a[232:235], v[172:175], v[144:147], a[232:235]\n" \
"s_add_u32 s18, s24, s12\n" \
"v_writelane_b32 v11, s11, 9\n" \
"s_addc_u32 s19, s25, 0\n" \
"v_mfma_f32_16x16x32_bf16 a[204:207], v[104:107], v[80:83], a[204:207]\n" \
"s_and_b32 s20, s17, 0xffff\n" \
"v_writelane_b32 v11, s20, 10\n" \
"v_mfma_f32_16x16x32_bf16 a[204:207], v[168:171], v[144:147], a[204:207]\n" \
"s_and_b32 s20, s19, 0xffff\n" \
"v_writelane_b32 v11, s18, 11\n" \
"v_mfma_f32_16x16x32_bf16 a[200:203], v[100:103], v[80:83], a[200:203]\n" \
"s_cmpk_lt_u32 s3, 3\n" \
"s_cselect_b32 s11, s11, s18\n" \
"v_mfma_f32_16x16x32_bf16 a[200:203], v[164:167], v[144:147], a[200:203]\n" \
"ds_read_b128 v[80:83], v28 offset:14336\n" \
"ds_read_b128 v[144:147], v29 offset:14336\n" \
"s_waitcnt lgkmcnt(15)\n" \
"v_mfma_f32_16x16x32_bf16 a[0:3], v[84:87], v[52:55], a[0:3]\n" \
"s_add_u32 m0, s30, 0x9c00\n" \
"s_mov_b32 s85, 0xfd80\n" \
"v_mfma_f32_16x16x32_bf16 a[0:3], v[148:151], v[116:119], a[0:3]\n" \
"buffer_load_dwordx4 v25, s[76:79], s85 offen lds\n" \
"v_mfma_f32_16x16x32_bf16 a[4:7], v[88:91], v[52:55], a[4:7]\n" \
"ds_read_b64_tr_b16 v[100:101], v36 offset:0\n" \
"s_movk_i32 s18, 0xfff\n" \
"v_mfma_f32_16x16x32_bf16 a[4:7], v[152:155], v[116:119], a[4:7]\n" \
"s_add_u32 m0, s31, 0x8c00\n" \
"s_mov_b32 s84, 0x198000\n" \
"v_mfma_f32_16x16x32_bf16 a[36:39], v[96:99], v[52:55], a[36:39]\n" \
"buffer_load_dwordx4 v27, s[68:71], s84 offen lds\n" \
"s_waitcnt lgkmcnt(15)\n" \
"v_mfma_f32_16x16x32_bf16 a[36:39], v[160:163], v[116:119], a[36:39]\n" \
"ds_read_b64_tr_b16 v[102:103], v36 offset:1024\n" \
"v_writelane_b32 v11, s20, 12\n" \
"s_waitcnt lgkmcnt(12)\n" \
"v_mfma_f32_16x16x32_bf16 a[32:35], v[52:55], v[92:95], a[32:35]\n" \
"ds_read_b64_tr_b16 v[164:165], v36 offset:8192\n" \
"s_and_b32 s20, s29, 0xffff\n" \
"s_waitcnt lgkmcnt(9)\n" \
"s_nop 0\n" \
"v_mfma_f32_16x16x32_bf16 a[32:35], v[116:119], v[156:159], a[32:35]\n" \
"ds_read_b64_tr_b16 v[166:167], v36 offset:9216\n" \
"v_writelane_b32 v11, s28, 13\n" \
"v_mfma_f32_16x16x32_bf16 a[96:99], v[56:59], v[92:95], a[96:99]\n" \
"ds_read_b64_tr_b16 v[104:105], v37 offset:0\n" \
"v_writelane_b32 v11, s20, 14\n" \
"v_mfma_f32_16x16x32_bf16 a[96:99], v[120:123], v[156:159], a[96:99]\n" \
"ds_read_b64_tr_b16 v[106:107], v37 offset:1024\n" \
"v_writelane_b32 v11, s11, 15\n" \
"v_mfma_f32_16x16x32_bf16 a[100:103], v[96:99], v[56:59], a[100:103]\n" \
"ds_read_b64_tr_b16 v[168:169], v37 offset:8192\n" \
"v_mfma_f32_16x16x32_bf16 a[100:103], v[160:163], v[120:123], a[100:103]\n" \
"s_add_u32 s89, s41, 0x2000\n" \
"buffer_store_dwordx4 a[44:47], v20, s[80:83], s89 offen offset:3072 sc1\n" \
"v_mfma_f32_16x16x32_bf16 a[68:71], v[88:91], v[56:59], a[68:71]\n" \
"ds_read_b64_tr_b16 v[170:171], v37 offset:9216\n" \
"v_mfma_f32_16x16x32_bf16 a[68:71], v[152:155], v[120:123], a[68:71]\n" \
"ds_read_b64_tr_b16 v[108:109], v38 offset:0\n" \
"v_mfma_f32_16x16x32_bf16 a[64:67], v[84:87], v[56:59], a[64:67]\n" \
"ds_read_b64_tr_b16 v[110:111], v38 offset:1024\n" \
"v_mfma_f32_16x16x32_bf16 a[64:67], v[148:151], v[120:123], a[64:67]\n" \
"s_cmpk_lt_u32 s3, 3\n" \
"s_cselect_b32 s11, s17, s19\n" \
"v_mfma_f32_16x16x32_bf16 a[128:131], v[84:87], v[60:63], a[128:131]\n" \
"s_add_u32 s88, s41, 0x2000\n" \
"buffer_store_dwordx4 v[252:255], v20, s[80:83], s88 offen offset:2048 sc1\n" \
"v_mfma_f32_16x16x32_bf16 a[128:131], v[148:151], v[124:127], a[128:131]\n" \
"ds_read_b64_tr_b16 v[172:173], v38 offset:8192\n" \
"s_and_b32 s11, s11, 0xffff\n" \
"v_mfma_f32_16x16x32_bf16 a[132:135], v[88:91], v[60:63], a[132:135]\n" \
"ds_read_b64_tr_b16 v[174:175], v38 offset:9216\n" \
"v_writelane_b32 v11, s11, 16\n" \
"v_mfma_f32_16x16x32_bf16 a[132:135], v[152:155], v[124:127], a[132:135]\n" \
"ds_read_b64_tr_b16 v[112:113], v39 offset:0\n" \
"s_mul_i32 s11, s6, 0x48\n" \
"v_mfma_f32_16x16x32_bf16 a[164:167], v[96:99], v[60:63], a[164:167]\n" \
"s_add_i32 s17, s11, 0x47\n" \
"v_mfma_f32_16x16x32_bf16 a[164:167], v[160:163], v[124:127], a[164:167]\n" \
"ds_read_b64_tr_b16 v[114:115], v39 offset:1024\n" \
"v_mfma_f32_16x16x32_bf16 a[160:163], v[60:63], v[92:95], a[160:163]\n" \
"s_add_u32 s89, s41, 0x3000\n" \
"buffer_store_dwordx4 v[200:203], v20, s[80:83], s89 offen offset:2048 sc1\n" \
"v_mfma_f32_16x16x32_bf16 a[160:163], v[124:127], v[156:159], a[160:163]\n" \
"ds_read_b64_tr_b16 v[176:177], v39 offset:8192\n" \
"v_mfma_f32_16x16x32_bf16 a[224:227], v[64:67], v[92:95], a[224:227]\n" \
"ds_read_b64_tr_b16 v[178:179], v39 offset:9216\n" \
"v_mfma_f32_16x16x32_bf16 a[224:227], v[128:131], v[156:159], a[224:227]\n" \
"s_cmpk_lt_u32 s3, 3\n" \
"s_cselect_b32 s17, s17, s18\n" \
"v_mfma_f32_16x16x32_bf16 a[228:231], v[96:99], v[64:67], a[228:231]\n" \
"s_mulk_i32 s17, 0x480\n" \
"s_addk_i32 s17, 0x120\n" \
"v_mfma_f32_16x16x32_bf16 a[228:231], v[160:163], v[128:131], a[228:231]\n" \
"s_add_u32 s88, s41, 0x3000\n" \
"buffer_store_dwordx4 v[204:207], v20, s[80:83], s88 offen offset:3072 sc1\n" \
"v_mfma_f32_16x16x32_bf16 a[196:199], v[88:91], v[64:67], a[196:199]\n" \
"v_writelane_b32 v11, s17, 17\n" \
"s_lshl_b32 s17, s3, 5\n" \
"v_mfma_f32_16x16x32_bf16 a[196:199], v[152:155], v[128:131], a[196:199]\n" \
"s_add_i32 s11, s11, s17\n" \
"s_mul_i32 s17, s11, 0x480\n" \
"v_mfma_f32_16x16x32_bf16 a[192:195], v[84:87], v[64:67], a[192:195]\n" \
"s_mul_i32 s11, s13, 0x24000\n" \
"s_add_i32 s18, s11, 0x1b000\n" \
"v_mfma_f32_16x16x32_bf16 a[192:195], v[148:151], v[128:131], a[192:195]\n" \
"s_cmpk_lt_u32 s3, 3\n" \
"s_cselect_b32 s17, s17, s18\n" \
"s_waitcnt lgkmcnt(15)\n" \
"v_mfma_f32_16x16x32_bf16 a[8:11], v[88:91], v[248:251], a[8:11]\n" \
"s_mul_i32 s19, s3, 0x9000\n" \
"v_writelane_b32 v11, s17, 18\n" \
"v_mfma_f32_16x16x32_bf16 a[8:11], v[152:155], v[48:51], a[8:11]\n" \
"s_mul_i32 s17, s16, 0x2400\n" \
"s_mul_i32 s16, s16, 0x9000\n" \
"v_mfma_f32_16x16x32_bf16 a[40:43], v[96:99], v[248:251], a[40:43]\n" \
"s_bitset1_b32 s17, 17\n" \
"v_writelane_b32 v11, s17, 19\n" \
"v_mfma_f32_16x16x32_bf16 a[40:43], v[160:163], v[48:51], a[40:43]\n" \
"s_add_i32 s16, s11, s16\n" \
"v_writelane_b32 v11, s16, 25\n" \
"v_mfma_f32_16x16x32_bf16 a[12:15], v[248:251], v[92:95], a[12:15]\n" \
"s_mul_i32 s16, s3, 0x2400\n" \
"s_add_i32 s17, s16, 0x6000\n" \
"v_mfma_f32_16x16x32_bf16 a[12:15], v[48:51], v[156:159], a[12:15]\n" \
"s_waitcnt vmcnt(4) lgkmcnt(0)\n" \
"s_barrier\n" \
"v_mfma_f32_16x16x32_bf16 a[16:19], v[100:103], v[52:55], a[16:19]\n" \
"s_add_u32 m0, s40, 0x0\n" \
"s_add_u32 s85, s39, 0x8000\n" \
"v_mfma_f32_16x16x32_bf16 a[16:19], v[164:167], v[116:119], a[16:19]\n" \
"buffer_load_dwordx4 v20, s[80:83], s85 offen sc1 lds\n" \
"v_mfma_f32_16x16x32_bf16 a[20:23], v[104:107], v[52:55], a[20:23]\n" \
"ds_read_b64_tr_b16 v[84:85], v32 offset:32768\n" \
"v_writelane_b32 v11, s17, 20\n" \
"v_mfma_f32_16x16x32_bf16 a[20:23], v[168:171], v[116:119], a[20:23]\n" \
"ds_read_b64_tr_b16 v[86:87], v32 offset:33280\n" \
"s_or_b32 s17, s16, 0x10000\n" \
"v_mfma_f32_16x16x32_bf16 a[24:27], v[108:111], v[52:55], a[24:27]\n" \
"s_add_u32 m0, s40, 0x1000\n" \
"s_add_u32 s84, s39, 0x9000\n" \
"v_mfma_f32_16x16x32_bf16 a[24:27], v[172:175], v[116:119], a[24:27]\n" \
"buffer_load_dwordx4 v20, s[80:83], s84 offen sc1 lds\n" \
"v_mfma_f32_16x16x32_bf16 a[28:31], v[112:115], v[52:55], a[28:31]\n" \
"ds_read_b64_tr_b16 v[148:149], v32 offset:36864\n" \
"s_add_i32 s18, s16, 0x19000\n" \
"v_mfma_f32_16x16x32_bf16 a[28:31], v[176:179], v[116:119], a[28:31]\n" \
"ds_read_b128 v[52:55], v28 offset:32768\n" \
"v_writelane_b32 v11, s17, 21\n" \
"v_mfma_f32_16x16x32_bf16 a[60:63], v[112:115], v[56:59], a[60:63]\n" \
"ds_read_b128 v[116:119], v29 offset:32768\n" \
"s_mov_b32 s17, 0\n" \
"v_mfma_f32_16x16x32_bf16 a[60:63], v[176:179], v[120:123], a[60:63]\n" \
"s_add_u32 m0, s40, 0x3000\n" \
"s_mov_b32 s85, 0x200\n" \
"v_mfma_f32_16x16x32_bf16 a[56:59], v[108:111], v[56:59], a[56:59]\n" \
"buffer_load_dwordx4 v21, s[76:79], s85 offen lds\n" \
"v_mfma_f32_16x16x32_bf16 a[56:59], v[172:175], v[120:123], a[56:59]\n" \
"ds_read_b64_tr_b16 v[150:151], v32 offset:37376\n" \
"v_mfma_f32_16x16x32_bf16 a[52:55], v[104:107], v[56:59], a[52:55]\n" \
"s_add_u32 m0, s30, 0x0\n" \
"s_mov_b32 s84, 0x200\n" \
"v_mfma_f32_16x16x32_bf16 a[52:55], v[168:171], v[120:123], a[52:55]\n" \
"buffer_load_dwordx4 v24, s[76:79], s84 offen lds\n" \
"v_mfma_f32_16x16x32_bf16 a[48:51], v[100:103], v[56:59], a[48:51]\n" \
"ds_read_b64_tr_b16 v[88:89], v33 offset:32768\n" \
"v_mfma_f32_16x16x32_bf16 a[48:51], v[164:167], v[120:123], a[48:51]\n" \
"ds_read_b128 v[56:59], v28 offset:34816\n" \
"v_mfma_f32_16x16x32_bf16 a[80:83], v[100:103], v[60:63], a[80:83]\n" \
"ds_read_b128 v[120:123], v29 offset:34816\n" \
"v_mfma_f32_16x16x32_bf16 a[80:83], v[164:167], v[124:127], a[80:83]\n" \
"s_add_u32 m0, s30, 0x400\n" \
"s_mov_b32 s85, 0x2600\n" \
"v_mfma_f32_16x16x32_bf16 a[84:87], v[104:107], v[60:63], a[84:87]\n" \
"buffer_load_dwordx4 v25, s[76:79], s85 offen lds\n" \
"v_mfma_f32_16x16x32_bf16 a[84:87], v[168:171], v[124:127], a[84:87]\n" \
"ds_read_b64_tr_b16 v[90:91], v33 offset:33280\n" \
"v_mfma_f32_16x16x32_bf16 a[88:91], v[108:111], v[60:63], a[88:91]\n" \
"ds_read_b64_tr_b16 v[152:153], v33 offset:36864\n" \
"v_mfma_f32_16x16x32_bf16 a[88:91], v[172:175], v[124:127], a[88:91]\n" \
"s_add_u32 m0, s31, 0x0\n" \
"s_nop 0\n" \
"s_mov_b32 s84, 0x200000\n" \
"v_mfma_f32_16x16x32_bf16 a[92:95], v[112:115], v[60:63], a[92:95]\n" \
"buffer_load_dwordx4 v26, s[68:71], s84 offen lds\n" \
"v_mfma_f32_16x16x32_bf16 a[92:95], v[176:179], v[124:127], a[92:95]\n" \
"ds_read_b128 v[60:63], v28 offset:36864\n" \
"v_mfma_f32_16x16x32_bf16 a[124:127], v[112:115], v[64:67], a[124:127]\n" \
"ds_read_b128 v[124:127], v29 offset:36864\n" \
"v_mfma_f32_16x16x32_bf16 a[124:127], v[176:179], v[128:131], a[124:127]\n" \
"s_add_u32 m0, s30, 0x800\n" \
"s_mov_b32 s85, 0x4a00\n" \
"v_mfma_f32_16x16x32_bf16 a[120:123], v[108:111], v[64:67], a[120:123]\n" \
"buffer_load_dwordx4 v24, s[76:79], s85 offen lds\n" \
"v_mfma_f32_16x16x32_bf16 a[120:123], v[172:175], v[128:131], a[120:123]\n" \
"ds_read_b64_tr_b16 v[154:155], v33 offset:37376\n" \
"v_mfma_f32_16x16x32_bf16 a[116:119], v[104:107], v[64:67], a[116:119]\n" \
"ds_read_b64_tr_b16 v[96:97], v35 offset:32768\n" \
"v_mfma_f32_16x16x32_bf16 a[116:119], v[168:171], v[128:131], a[116:119]\n" \
"ds_read_b64_tr_b16 v[98:99], v35 offset:33280\n" \
"v_mfma_f32_16x16x32_bf16 a[112:115], v[100:103], v[64:67], a[112:115]\n" \
"ds_read_b64_tr_b16 v[160:161], v35 offset:36864\n" \
"v_mfma_f32_16x16x32_bf16 a[112:115], v[164:167], v[128:131], a[112:115]\n" \
"ds_read_b128 v[64:67], v28 offset:38912\n" \
"v_mfma_f32_16x16x32_bf16 a[144:147], v[100:103], v[68:71], a[144:147]\n" \
"ds_read_b128 v[128:131], v29 offset:38912\n" \
"v_mfma_f32_16x16x32_bf16 a[144:147], v[164:167], v[132:135], a[144:147]\n" \
"s_add_u32 m0, s30, 0xc00\n" \
"s_mov_b32 s84, 0x6e00\n" \
"v_mfma_f32_16x16x32_bf16 a[148:151], v[104:107], v[68:71], a[148:151]\n" \
"buffer_load_dwordx4 v25, s[76:79], s84 offen lds\n" \
"v_mfma_f32_16x16x32_bf16 a[148:151], v[168:171], v[132:135], a[148:151]\n" \
"s_add_u32 m0, s31, 0x400\n" \
"s_mov_b32 s85, 0x208000\n" \
"v_mfma_f32_16x16x32_bf16 a[152:155], v[108:111], v[68:71], a[152:155]\n" \
"buffer_load_dwordx4 v26, s[68:71], s85 offen lds\n" \
"v_mfma_f32_16x16x32_bf16 a[152:155], v[172:175], v[132:135], a[152:155]\n" \
"ds_read_b64_tr_b16 v[162:163], v35 offset:37376\n" \
"v_mfma_f32_16x16x32_bf16 a[156:159], v[112:115], v[68:71], a[156:159]\n" \
"ds_read_b64_tr_b16 v[92:93], v34 offset:32768\n" \
"v_mfma_f32_16x16x32_bf16 a[156:159], v[176:179], v[132:135], a[156:159]\n" \
"ds_read_b128 v[68:71], v28 offset:40960\n" \
"v_mfma_f32_16x16x32_bf16 a[108:111], v[112:115], v[72:75], a[108:111]\n" \
"ds_read_b128 v[132:135], v29 offset:40960\n" \
"v_mfma_f32_16x16x32_bf16 a[108:111], v[176:179], v[136:139], a[108:111]\n" \
"s_add_u32 m0, s30, 0x1000\n" \
"s_mov_b32 s84, 0x9200\n" \
"v_mfma_f32_16x16x32_bf16 a[104:107], v[108:111], v[72:75], a[104:107]\n" \
"buffer_load_dwordx4 v24, s[76:79], s84 offen lds\n" \
"v_mfma_f32_16x16x32_bf16 a[104:107], v[172:175], v[136:139], a[104:107]\n" \
"s_add_u32 m0, s30, 0x1400\n" \
"s_mov_b32 s85, 0xb600\n" \
"v_mfma_f32_16x16x32_bf16 a[76:79], v[104:107], v[72:75], a[76:79]\n" \
"buffer_load_dwordx4 v25, s[76:79], s85 offen lds\n" \
"v_mfma_f32_16x16x32_bf16 a[76:79], v[168:171], v[136:139], a[76:79]\n" \
"ds_read_b64_tr_b16 v[94:95], v34 offset:33280\n" \
"v_mfma_f32_16x16x32_bf16 a[72:75], v[100:103], v[72:75], a[72:75]\n" \
"ds_read_b64_tr_b16 v[156:157], v34 offset:36864\n" \
"v_mfma_f32_16x16x32_bf16 a[72:75], v[164:167], v[136:139], a[72:75]\n" \
"ds_read_b128 v[72:75], v28 offset:43008\n" \
"v_mfma_f32_16x16x32_bf16 a[136:139], v[100:103], v[76:79], a[136:139]\n" \
"ds_read_b128 v[136:139], v29 offset:43008\n" \
"v_mfma_f32_16x16x32_bf16 a[136:139], v[164:167], v[140:143], a[136:139]\n" \
"s_add_u32 m0, s31, 0x800\n" \
"s_mov_b32 s84, 0x210000\n" \
"v_mfma_f32_16x16x32_bf16 a[140:143], v[104:107], v[76:79], a[140:143]\n" \
"buffer_load_dwordx4 v27, s[68:71], s84 offen lds\n" \
"v_mfma_f32_16x16x32_bf16 a[140:143], v[168:171], v[140:143], a[140:143]\n" \
"s_add_u32 m0, s30, 0x1800\n" \
"s_mov_b32 s85, 0xda00\n" \
"v_mfma_f32_16x16x32_bf16 a[168:171], v[108:111], v[76:79], a[168:171]\n" \
"buffer_load_dwordx4 v24, s[76:79], s85 offen lds\n" \
"v_mfma_f32_16x16x32_bf16 a[168:171], v[172:175], v[140:143], a[168:171]\n" \
"ds_read_b64_tr_b16 v[158:159], v34 offset:37376\n" \
"v_mfma_f32_16x16x32_bf16 a[172:175], v[112:115], v[76:79], a[172:175]\n" \
"ds_read_b128 v[248:251], v22 offset:32768\n" \
"v_mfma_f32_16x16x32_bf16 a[172:175], v[176:179], v[140:143], a[172:175]\n" \
"ds_read_b128 v[76:79], v28 offset:45056\n" \
"v_mfma_f32_16x16x32_bf16 a[236:239], v[112:115], v[80:83], a[236:239]\n" \
"ds_read_b128 v[140:143], v29 offset:45056\n" \
"v_mfma_f32_16x16x32_bf16 a[236:239], v[176:179], v[144:147], a[236:239]\n" \
"ds_read_b128 v[48:51], v23 offset:32768\n" \
"v_mfma_f32_16x16x32_bf16 a[232:235], v[108:111], v[80:83], a[232:235]\n" \
"s_cmpk_lt_u32 s3, 3\n" \
"s_cselect_b32 s18, s18, s17\n" \
"v_mfma_f32_16x16x32_bf16 a[232:235], v[172:175], v[144:147], a[232:235]\n" \
"s_bitset1_b32 s16, 17\n" \
"v_writelane_b32 v11, s18, 22\n" \
"v_mfma_f32_16x16x32_bf16 a[204:207], v[104:107], v[80:83], a[204:207]\n" \
"s_movk_i32 s18, 0x2400\n" \
"v_mfma_f32_16x16x32_bf16 a[204:207], v[168:171], v[144:147], a[204:207]\n" \
"s_cmpk_lt_u32 s3, 3\n" \
"s_cselect_b32 s16, s16, s18\n" \
"v_mfma_f32_16x16x32_bf16 a[200:203], v[100:103], v[80:83], a[200:203]\n" \
"s_xor_b32 s13, s13, 2\n" \
"v_writelane_b32 v11, s16, 23\n" \
"v_mfma_f32_16x16x32_bf16 a[200:203], v[164:167], v[144:147], a[200:203]\n" \
"ds_read_b128 v[80:83], v28 offset:47104\n" \
"ds_read_b128 v[144:147], v29 offset:47104\n" \
"s_waitcnt lgkmcnt(15)\n" \
"v_mfma_f32_16x16x32_bf16 a[0:3], v[84:87], v[52:55], a[0:3]\n" \
"s_add_u32 m0, s30, 0x1c00\n" \
"s_mov_b32 s85, 0xfe00\n" \
"v_mfma_f32_16x16x32_bf16 a[0:3], v[148:151], v[116:119], a[0:3]\n" \
"buffer_load_dwordx4 v25, s[76:79], s85 offen lds\n" \
"v_mfma_f32_16x16x32_bf16 a[4:7], v[88:91], v[52:55], a[4:7]\n" \
"ds_read_b64_tr_b16 v[100:101], v36 offset:32768\n" \
"s_lshl_b32 s16, s3, 10\n" \
"v_mfma_f32_16x16x32_bf16 a[4:7], v[152:155], v[116:119], a[4:7]\n" \
"s_add_u32 m0, s31, 0xc00\n" \
"s_mov_b32 s84, 0x218000\n" \
"v_mfma_f32_16x16x32_bf16 a[36:39], v[96:99], v[52:55], a[36:39]\n" \
"buffer_load_dwordx4 v27, s[68:71], s84 offen lds\n" \
"s_waitcnt lgkmcnt(15)\n" \
"v_mfma_f32_16x16x32_bf16 a[36:39], v[160:163], v[116:119], a[36:39]\n" \
"ds_read_b64_tr_b16 v[102:103], v36 offset:33792\n" \
"s_or_b32 s16, s16, 0xf000\n" \
"s_waitcnt lgkmcnt(12)\n" \
"v_mfma_f32_16x16x32_bf16 a[32:35], v[52:55], v[92:95], a[32:35]\n" \
"ds_read_b64_tr_b16 v[164:165], v36 offset:40960\n" \
"v_writelane_b32 v11, s16, 24\n" \
"s_waitcnt lgkmcnt(9)\n" \
"v_mfma_f32_16x16x32_bf16 a[32:35], v[116:119], v[156:159], a[32:35]\n" \
"ds_read_b64_tr_b16 v[166:167], v36 offset:41984\n" \
"s_add_i32 s16, s15, 1\n" \
"v_mfma_f32_16x16x32_bf16 a[96:99], v[56:59], v[92:95], a[96:99]\n" \
"ds_read_b64_tr_b16 v[104:105], v37 offset:32768\n" \
"s_and_b32 s16, s16, 3\n" \
"v_mfma_f32_16x16x32_bf16 a[96:99], v[120:123], v[156:159], a[96:99]\n" \
"ds_read_b64_tr_b16 v[106:107], v37 offset:33792\n" \
"s_or_b32 s16, s16, s5\n" \
"v_mfma_f32_16x16x32_bf16 a[100:103], v[96:99], v[56:59], a[100:103]\n" \
"ds_read_b64_tr_b16 v[168:169], v37 offset:40960\n" \
"s_mul_i32 s18, s16, 0x24000\n" \
"v_mfma_f32_16x16x32_bf16 a[100:103], v[160:163], v[120:123], a[100:103]\n" \
"s_add_u32 s89, s42, 0x0\n" \
"buffer_store_dwordx4 v[216:219], v20, s[80:83], s89 offen offset:0 sc1\n" \
"v_mfma_f32_16x16x32_bf16 a[68:71], v[88:91], v[56:59], a[68:71]\n" \
"ds_read_b64_tr_b16 v[170:171], v37 offset:41984\n" \
"s_add_i32 s18, s18, s19\n" \
"v_mfma_f32_16x16x32_bf16 a[68:71], v[152:155], v[120:123], a[68:71]\n" \
"ds_read_b64_tr_b16 v[108:109], v38 offset:32768\n" \
"s_add_i32 s15, s15, -1\n" \
"v_mfma_f32_16x16x32_bf16 a[64:67], v[84:87], v[56:59], a[64:67]\n" \
"ds_read_b64_tr_b16 v[110:111], v38 offset:33792\n" \
"v_writelane_b32 v11, s18, 26\n" \
"v_mfma_f32_16x16x32_bf16 a[64:67], v[148:151], v[120:123], a[64:67]\n" \
"s_mul_i32 s18, s13, 0x24000\n" \
"s_and_b32 s15, s15, 3\n" \
"v_mfma_f32_16x16x32_bf16 a[128:131], v[84:87], v[60:63], a[128:131]\n" \
"s_add_u32 s88, s42, 0x0\n" \
"buffer_store_dwordx4 v[220:223], v20, s[80:83], s88 offen offset:1024 sc1\n" \
"v_mfma_f32_16x16x32_bf16 a[128:131], v[148:151], v[124:127], a[128:131]\n" \
"ds_read_b64_tr_b16 v[172:173], v38 offset:40960\n" \
"s_add_i32 s18, s18, s19\n" \
"v_mfma_f32_16x16x32_bf16 a[132:135], v[88:91], v[60:63], a[132:135]\n" \
"ds_read_b64_tr_b16 v[174:175], v38 offset:41984\n" \
"s_or_b32 s15, s15, s5\n" \
"v_mfma_f32_16x16x32_bf16 a[132:135], v[152:155], v[124:127], a[132:135]\n" \
"ds_read_b64_tr_b16 v[112:113], v39 offset:32768\n" \
"v_writelane_b32 v11, s18, 27\n" \
"v_mfma_f32_16x16x32_bf16 a[164:167], v[96:99], v[60:63], a[164:167]\n" \
"s_mul_i32 s18, s15, 0x24000\n" \
"s_add_i32 s18, s18, s19\n" \
"v_mfma_f32_16x16x32_bf16 a[164:167], v[160:163], v[124:127], a[164:167]\n" \
"ds_read_b64_tr_b16 v[114:115], v39 offset:33792\n" \
"v_writelane_b32 v11, s18, 28\n" \
"v_mfma_f32_16x16x32_bf16 a[160:163], v[60:63], v[92:95], a[160:163]\n" \
"s_add_u32 s89, s42, 0x1000\n" \
"buffer_store_dwordx4 v[224:227], v20, s[80:83], s89 offen offset:1024 sc1\n" \
"v_mfma_f32_16x16x32_bf16 a[160:163], v[124:127], v[156:159], a[160:163]\n" \
"ds_read_b64_tr_b16 v[176:177], v39 offset:40960\n" \
"s_or_b32 s18, s9, s14\n" \
"v_mfma_f32_16x16x32_bf16 a[224:227], v[64:67], v[92:95], a[224:227]\n" \
"ds_read_b64_tr_b16 v[178:179], v39 offset:41984\n" \
"s_mul_i32 s18, s18, 0x24800\n" \
"v_mfma_f32_16x16x32_bf16 a[224:227], v[128:131], v[156:159], a[224:227]\n" \
"s_lshl_b32 s16, s16, 12\n" \
"s_add_i32 s19, s18, 0x3004800\n" \
"v_mfma_f32_16x16x32_bf16 a[228:231], v[96:99], v[64:67], a[228:231]\n" \
"s_add_i32 s20, s19, s16\n" \
"v_writelane_b32 v11, s20, 29\n" \
"v_mfma_f32_16x16x32_bf16 a[228:231], v[160:163], v[128:131], a[228:231]\n" \
"s_add_u32 s88, s42, 0x1000\n" \
"buffer_store_dwordx4 v[228:231], v20, s[80:83], s88 offen offset:0 sc1\n" \
"v_mfma_f32_16x16x32_bf16 a[196:199], v[88:91], v[64:67], a[196:199]\n" \
"s_lshl_b32 s13, s13, 12\n" \
"s_lshl_b32 s15, s15, 12\n" \
"v_mfma_f32_16x16x32_bf16 a[196:199], v[152:155], v[128:131], a[196:199]\n" \
"s_sub_i32 s14, s9, s14\n" \
"s_add_i32 s20, s19, s13\n" \
"v_mfma_f32_16x16x32_bf16 a[192:195], v[84:87], v[64:67], a[192:195]\n" \
"v_writelane_b32 v11, s20, 30\n" \
"s_add_i32 s19, s19, s15\n" \
"v_mfma_f32_16x16x32_bf16 a[192:195], v[148:151], v[128:131], a[192:195]\n" \
"s_mul_i32 s14, s14, 0x24800\n" \
"v_writelane_b32 v11, s19, 31\n" \
"s_waitcnt lgkmcnt(15)\n" \
"v_mfma_f32_16x16x32_bf16 a[8:11], v[88:91], v[248:251], a[8:11]\n" \
"s_add_i32 s19, s14, 0x4120800\n" \
"s_add_i32 s16, s19, s16\n" \
"v_mfma_f32_16x16x32_bf16 a[8:11], v[152:155], v[48:51], a[8:11]\n" \
"v_writelane_b32 v11, s16, 32\n" \
"s_add_i32 s13, s19, s13\n" \
"v_mfma_f32_16x16x32_bf16 a[40:43], v[96:99], v[248:251], a[40:43]\n" \
"v_writelane_b32 v11, s13, 33\n" \
"s_add_i32 s19, s19, s15\n" \
"v_mfma_f32_16x16x32_bf16 a[40:43], v[160:163], v[48:51], a[40:43]\n" \
"s_or_b32 s13, s18, 0x3000000\n" \
"s_lshl_b32 s15, s6, 11\n" \
"v_mfma_f32_16x16x32_bf16 a[12:15], v[248:251], v[92:95], a[12:15]\n" \
"v_writelane_b32 v11, s19, 34\n" \
"s_add_i32 s16, s13, s15\n" \
"v_mfma_f32_16x16x32_bf16 a[12:15], v[48:51], v[156:159], a[12:15]\n" \
"s_waitcnt vmcnt(4) lgkmcnt(0)\n" \
"s_barrier\n" \
"v_mov_b32_e32 v45, s62\n" \
"s_mov_b64 exec, 1\n" \
"buffer_store_dword v45, off, s[80:83], s37 offset:0 sc1\n" \
"s_mov_b64 exec, -1\n" \
"v_mfma_f32_16x16x32_bf16 a[16:19], v[100:103], v[52:55], a[16:19]\n" \
"s_add_u32 m0, s40, 0x8000\n" \
"s_add_u32 s85, s39, 0xa000\n" \
"v_mfma_f32_16x16x32_bf16 a[16:19], v[164:167], v[116:119], a[16:19]\n" \
"buffer_load_dwordx4 v20, s[80:83], s85 offen sc1 lds\n" \
"v_mfma_f32_16x16x32_bf16 a[20:23], v[104:107], v[52:55], a[20:23]\n" \
"ds_read_b64_tr_b16 v[84:85], v32 offset:0\n" \
"v_accvgpr_read_b32 v204, a248\n" \
"v_mfma_f32_16x16x32_bf16 a[20:23], v[168:171], v[116:119], a[20:23]\n" \
"ds_read_b64_tr_b16 v[86:87], v32 offset:512\n" \
"v_accvgpr_read_b32 v205, a249\n" \
"v_mfma_f32_16x16x32_bf16 a[24:27], v[108:111], v[52:55], a[24:27]\n" \
"s_add_u32 m0, s40, 0x9000\n" \
"s_add_u32 s84, s39, 0xb000\n" \
"v_mfma_f32_16x16x32_bf16 a[24:27], v[172:175], v[116:119], a[24:27]\n" \
"buffer_load_dwordx4 v20, s[80:83], s84 offen sc1 lds\n" \
"v_mfma_f32_16x16x32_bf16 a[28:31], v[112:115], v[52:55], a[28:31]\n" \
"ds_read_b64_tr_b16 v[148:149], v32 offset:4096\n" \
"v_accvgpr_read_b32 v206, a250\n" \
"v_mfma_f32_16x16x32_bf16 a[28:31], v[176:179], v[116:119], a[28:31]\n" \
"ds_read_b128 v[52:55], v28 offset:0\n" \
"v_accvgpr_read_b32 v207, a251\n" \
"v_mfma_f32_16x16x32_bf16 a[60:63], v[112:115], v[56:59], a[60:63]\n" \
"ds_read_b128 v[116:119], v29 offset:0\n" \
"v_accvgpr_read_b32 v180, a252\n" \
"v_mfma_f32_16x16x32_bf16 a[60:63], v[176:179], v[120:123], a[60:63]\n" \
"s_add_u32 m0, s40, 0xb000\n" \
"s_mov_b32 s85, 0x280\n" \
"v_mfma_f32_16x16x32_bf16 a[56:59], v[108:111], v[56:59], a[56:59]\n" \
"buffer_load_dwordx4 v21, s[76:79], s85 offen lds\n" \
"v_mfma_f32_16x16x32_bf16 a[56:59], v[172:175], v[120:123], a[56:59]\n" \
"ds_read_b64_tr_b16 v[150:151], v32 offset:4608\n" \
"v_accvgpr_read_b32 v181, a253\n" \
"v_mfma_f32_16x16x32_bf16 a[52:55], v[104:107], v[56:59], a[52:55]\n" \
"s_add_u32 m0, s30, 0x8000\n" \
"s_mov_b32 s84, 0x280\n" \
"v_mfma_f32_16x16x32_bf16 a[52:55], v[168:171], v[120:123], a[52:55]\n" \
"buffer_load_dwordx4 v24, s[76:79], s84 offen lds\n" \
"v_mfma_f32_16x16x32_bf16 a[48:51], v[100:103], v[56:59], a[48:51]\n" \
"ds_read_b64_tr_b16 v[88:89], v33 offset:0\n" \
"v_accvgpr_read_b32 v182, a254\n" \
"v_mfma_f32_16x16x32_bf16 a[48:51], v[164:167], v[120:123], a[48:51]\n" \
"ds_read_b128 v[56:59], v28 offset:2048\n" \
"v_accvgpr_read_b32 v183, a255\n" \
"v_mfma_f32_16x16x32_bf16 a[80:83], v[100:103], v[60:63], a[80:83]\n" \
"ds_read_b128 v[120:123], v29 offset:2048\n" \
"v_cvt_pk_bf16_f32 v204, v204, v205\n" \
"v_mfma_f32_16x16x32_bf16 a[80:83], v[164:167], v[124:127], a[80:83]\n" \
"s_add_u32 m0, s30, 0x8400\n" \
"s_mov_b32 s85, 0x2680\n" \
"v_mfma_f32_16x16x32_bf16 a[84:87], v[104:107], v[60:63], a[84:87]\n" \
"buffer_load_dwordx4 v25, s[76:79], s85 offen lds\n" \
"v_mfma_f32_16x16x32_bf16 a[84:87], v[168:171], v[124:127], a[84:87]\n" \
"ds_read_b64_tr_b16 v[90:91], v33 offset:512\n" \
"v_cvt_pk_bf16_f32 v205, v206, v207\n" \
"v_mfma_f32_16x16x32_bf16 a[88:91], v[108:111], v[60:63], a[88:91]\n" \
"ds_read_b64_tr_b16 v[152:153], v33 offset:4096\n" \
"v_cvt_pk_bf16_f32 v206, v180, v181\n" \
"v_mfma_f32_16x16x32_bf16 a[88:91], v[172:175], v[124:127], a[88:91]\n" \
"s_add_u32 m0, s31, 0x8000\n" \
"s_mov_b32 s84, 0x280000\n" \
"v_mfma_f32_16x16x32_bf16 a[92:95], v[112:115], v[60:63], a[92:95]\n" \
"buffer_load_dwordx4 v26, s[68:71], s84 offen lds\n" \
"v_mfma_f32_16x16x32_bf16 a[92:95], v[176:179], v[124:127], a[92:95]\n" \
"ds_read_b128 v[60:63], v28 offset:4096\n" \
"v_cvt_pk_bf16_f32 v207, v182, v183\n" \
"v_mfma_f32_16x16x32_bf16 a[124:127], v[112:115], v[64:67], a[124:127]\n" \
"ds_read_b128 v[124:127], v29 offset:4096\n" \
"v_accvgpr_read_b32 v200, a240\n" \
"v_mfma_f32_16x16x32_bf16 a[124:127], v[176:179], v[128:131], a[124:127]\n" \
"s_add_u32 m0, s30, 0x8800\n" \
"s_mov_b32 s85, 0x4a80\n" \
"v_mfma_f32_16x16x32_bf16 a[120:123], v[108:111], v[64:67], a[120:123]\n" \
"buffer_load_dwordx4 v24, s[76:79], s85 offen lds\n" \
"v_mfma_f32_16x16x32_bf16 a[120:123], v[172:175], v[128:131], a[120:123]\n" \
"ds_read_b64_tr_b16 v[154:155], v33 offset:4608\n" \
"v_accvgpr_read_b32 v201, a241\n" \
"v_mfma_f32_16x16x32_bf16 a[116:119], v[104:107], v[64:67], a[116:119]\n" \
"ds_read_b64_tr_b16 v[96:97], v35 offset:0\n" \
"v_accvgpr_read_b32 v202, a242\n" \
"v_mfma_f32_16x16x32_bf16 a[116:119], v[168:171], v[128:131], a[116:119]\n" \
"ds_read_b64_tr_b16 v[98:99], v35 offset:512\n" \
"v_accvgpr_read_b32 v203, a243\n" \
"v_mfma_f32_16x16x32_bf16 a[112:115], v[100:103], v[64:67], a[112:115]\n" \
"ds_read_b64_tr_b16 v[160:161], v35 offset:4096\n" \
"v_accvgpr_read_b32 v180, a244\n" \
"v_mfma_f32_16x16x32_bf16 a[112:115], v[164:167], v[128:131], a[112:115]\n" \
"ds_read_b128 v[64:67], v28 offset:6144\n" \
"v_accvgpr_read_b32 v181, a245\n" \
"v_mfma_f32_16x16x32_bf16 a[144:147], v[100:103], v[68:71], a[144:147]\n" \
"ds_read_b128 v[128:131], v29 offset:6144\n" \
"v_accvgpr_read_b32 v182, a246\n" \
"v_mfma_f32_16x16x32_bf16 a[144:147], v[164:167], v[132:135], a[144:147]\n" \
"s_add_u32 m0, s30, 0x8c00\n" \
"s_mov_b32 s84, 0x6e80\n" \
"v_mfma_f32_16x16x32_bf16 a[148:151], v[104:107], v[68:71], a[148:151]\n" \
"buffer_load_dwordx4 v25, s[76:79], s84 offen lds\n" \
"v_mfma_f32_16x16x32_bf16 a[148:151], v[168:171], v[132:135], a[148:151]\n" \
"s_add_u32 m0, s31, 0x8400\n" \
"s_mov_b32 s85, 0x288000\n" \
"v_mfma_f32_16x16x32_bf16 a[152:155], v[108:111], v[68:71], a[152:155]\n" \
"buffer_load_dwordx4 v26, s[68:71], s85 offen lds\n" \
"v_mfma_f32_16x16x32_bf16 a[152:155], v[172:175], v[132:135], a[152:155]\n" \
"ds_read_b64_tr_b16 v[162:163], v35 offset:4608\n" \
"v_accvgpr_read_b32 v183, a247\n" \
"v_mfma_f32_16x16x32_bf16 a[156:159], v[112:115], v[68:71], a[156:159]\n" \
"ds_read_b64_tr_b16 v[92:93], v34 offset:0\n" \
"v_cvt_pk_bf16_f32 v200, v200, v201\n" \
"v_mfma_f32_16x16x32_bf16 a[156:159], v[176:179], v[132:135], a[156:159]\n" \
"ds_read_b128 v[68:71], v28 offset:8192\n" \
"v_cvt_pk_bf16_f32 v201, v202, v203\n" \
"v_mfma_f32_16x16x32_bf16 a[108:111], v[112:115], v[72:75], a[108:111]\n" \
"ds_read_b128 v[132:135], v29 offset:8192\n" \
"v_cvt_pk_bf16_f32 v202, v180, v181\n" \
"v_mfma_f32_16x16x32_bf16 a[108:111], v[176:179], v[136:139], a[108:111]\n" \
"s_add_u32 m0, s30, 0x9000\n" \
"s_mov_b32 s84, 0x9280\n" \
"v_mfma_f32_16x16x32_bf16 a[104:107], v[108:111], v[72:75], a[104:107]\n" \
"buffer_load_dwordx4 v24, s[76:79], s84 offen lds\n" \
"v_mfma_f32_16x16x32_bf16 a[104:107], v[172:175], v[136:139], a[104:107]\n" \
"s_add_u32 m0, s30, 0x9400\n" \
"s_mov_b32 s85, 0xb680\n" \
"v_mfma_f32_16x16x32_bf16 a[76:79], v[104:107], v[72:75], a[76:79]\n" \
"buffer_load_dwordx4 v25, s[76:79], s85 offen lds\n" \
"v_mfma_f32_16x16x32_bf16 a[76:79], v[168:171], v[136:139], a[76:79]\n" \
"ds_read_b64_tr_b16 v[94:95], v34 offset:512\n" \
"v_cvt_pk_bf16_f32 v203, v182, v183\n" \
"v_mfma_f32_16x16x32_bf16 a[72:75], v[100:103], v[72:75], a[72:75]\n" \
"ds_read_b64_tr_b16 v[156:157], v34 offset:4096\n" \
"v_accvgpr_read_b32 v196, a216\n" \
"v_mfma_f32_16x16x32_bf16 a[72:75], v[164:167], v[136:139], a[72:75]\n" \
"ds_read_b128 v[72:75], v28 offset:10240\n" \
"v_accvgpr_read_b32 v197, a217\n" \
"v_mfma_f32_16x16x32_bf16 a[136:139], v[100:103], v[76:79], a[136:139]\n" \
"ds_read_b128 v[136:139], v29 offset:10240\n" \
"v_accvgpr_read_b32 v198, a218\n" \
"v_mfma_f32_16x16x32_bf16 a[136:139], v[164:167], v[140:143], a[136:139]\n" \
"s_add_u32 m0, s31, 0x8800\n" \
"s_mov_b32 s84, 0x290000\n" \
"v_mfma_f32_16x16x32_bf16 a[140:143], v[104:107], v[76:79], a[140:143]\n" \
"buffer_load_dwordx4 v27, s[68:71], s84 offen lds\n" \
"v_mfma_f32_16x16x32_bf16 a[140:143], v[168:171], v[140:143], a[140:143]\n" \
"s_add_u32 m0, s30, 0x9800\n" \
"s_mov_b32 s85, 0xda80\n" \
"v_mfma_f32_16x16x32_bf16 a[168:171], v[108:111], v[76:79], a[168:171]\n" \
"buffer_load_dwordx4 v24, s[76:79], s85 offen lds\n" \
"v_mfma_f32_16x16x32_bf16 a[168:171], v[172:175], v[140:143], a[168:171]\n" \
"ds_read_b64_tr_b16 v[158:159], v34 offset:4608\n" \
"v_accvgpr_read_b32 v199, a219\n" \
"v_mfma_f32_16x16x32_bf16 a[172:175], v[112:115], v[76:79], a[172:175]\n" \
"ds_read_b128 v[248:251], v22 offset:0\n" \
"v_accvgpr_read_b32 v180, a220\n" \
"v_mfma_f32_16x16x32_bf16 a[172:175], v[176:179], v[140:143], a[172:175]\n" \
"ds_read_b128 v[76:79], v28 offset:12288\n" \
"v_accvgpr_read_b32 v181, a221\n" \
"v_mfma_f32_16x16x32_bf16 a[236:239], v[112:115], v[80:83], a[236:239]\n" \
"ds_read_b128 v[140:143], v29 offset:12288\n" \
"v_accvgpr_read_b32 v182, a222\n" \
"v_mfma_f32_16x16x32_bf16 a[236:239], v[176:179], v[144:147], a[236:239]\n" \
"ds_read_b128 v[48:51], v23 offset:0\n" \
"v_accvgpr_read_b32 v183, a223\n" \
"v_mfma_f32_16x16x32_bf16 a[232:235], v[108:111], v[80:83], a[232:235]\n" \
"v_cvt_pk_bf16_f32 v196, v196, v197\n" \
"v_cvt_pk_bf16_f32 v197, v198, v199\n" \
"v_mfma_f32_16x16x32_bf16 a[232:235], v[172:175], v[144:147], a[232:235]\n" \
"v_cvt_pk_bf16_f32 v198, v180, v181\n" \
"v_cvt_pk_bf16_f32 v199, v182, v183\n" \
"v_mfma_f32_16x16x32_bf16 a[204:207], v[104:107], v[80:83], a[204:207]\n" \
"v_accvgpr_read_b32 v192, a208\n" \
"v_accvgpr_read_b32 v193, a209\n" \
"v_mfma_f32_16x16x32_bf16 a[204:207], v[168:171], v[144:147], a[204:207]\n" \
"v_accvgpr_read_b32 v194, a210\n" \
"v_accvgpr_read_b32 v195, a211\n" \
"v_mfma_f32_16x16x32_bf16 a[200:203], v[100:103], v[80:83], a[200:203]\n" \
"v_accvgpr_read_b32 v180, a212\n" \
"v_accvgpr_read_b32 v181, a213\n" \
"v_mfma_f32_16x16x32_bf16 a[200:203], v[164:167], v[144:147], a[200:203]\n" \
"ds_read_b128 v[80:83], v28 offset:14336\n" \
"ds_read_b128 v[144:147], v29 offset:14336\n" \
"s_waitcnt lgkmcnt(15)\n" \
"s_nop 0\n" \
"v_mfma_f32_16x16x32_bf16 a[0:3], v[84:87], v[52:55], a[0:3]\n" \
"s_add_u32 m0, s30, 0x9c00\n" \
"s_mov_b32 s85, 0xfe80\n" \
"v_mfma_f32_16x16x32_bf16 a[0:3], v[148:151], v[116:119], a[0:3]\n" \
"buffer_load_dwordx4 v25, s[76:79], s85 offen lds\n" \
"v_mfma_f32_16x16x32_bf16 a[4:7], v[88:91], v[52:55], a[4:7]\n" \
"ds_read_b64_tr_b16 v[100:101], v36 offset:0\n" \
"v_accvgpr_read_b32 v182, a214\n" \
"v_mfma_f32_16x16x32_bf16 a[4:7], v[152:155], v[116:119], a[4:7]\n" \
"s_add_u32 m0, s31, 0x8c00\n" \
"s_mov_b32 s84, 0x298000\n" \
"v_mfma_f32_16x16x32_bf16 a[36:39], v[96:99], v[52:55], a[36:39]\n" \
"buffer_load_dwordx4 v27, s[68:71], s84 offen lds\n" \
"s_waitcnt lgkmcnt(15)\n" \
"v_mfma_f32_16x16x32_bf16 a[36:39], v[160:163], v[116:119], a[36:39]\n" \
"ds_read_b64_tr_b16 v[102:103], v36 offset:1024\n" \
"v_accvgpr_read_b32 v183, a215\n" \
"s_waitcnt lgkmcnt(12)\n" \
"v_mfma_f32_16x16x32_bf16 a[32:35], v[52:55], v[92:95], a[32:35]\n" \
"ds_read_b64_tr_b16 v[164:165], v36 offset:8192\n" \
"v_cvt_pk_bf16_f32 v192, v192, v193\n" \
"s_waitcnt lgkmcnt(9)\n" \
"s_nop 0\n" \
"v_mfma_f32_16x16x32_bf16 a[32:35], v[116:119], v[156:159], a[32:35]\n" \
"ds_read_b64_tr_b16 v[166:167], v36 offset:9216\n" \
"v_cvt_pk_bf16_f32 v193, v194, v195\n" \
"v_mfma_f32_16x16x32_bf16 a[96:99], v[56:59], v[92:95], a[96:99]\n" \
"ds_read_b64_tr_b16 v[104:105], v37 offset:0\n" \
"v_cvt_pk_bf16_f32 v194, v180, v181\n" \
"v_mfma_f32_16x16x32_bf16 a[96:99], v[120:123], v[156:159], a[96:99]\n" \
"ds_read_b64_tr_b16 v[106:107], v37 offset:1024\n" \
"v_cvt_pk_bf16_f32 v195, v182, v183\n" \
"v_mfma_f32_16x16x32_bf16 a[100:103], v[96:99], v[56:59], a[100:103]\n" \
"ds_read_b64_tr_b16 v[168:169], v37 offset:8192\n" \
"v_accvgpr_read_b32 v188, a184\n" \
"v_mfma_f32_16x16x32_bf16 a[100:103], v[160:163], v[120:123], a[100:103]\n" \
"ds_read_b64_tr_b16 v[170:171], v37 offset:9216\n" \
"v_accvgpr_read_b32 v189, a185\n" \
"v_mfma_f32_16x16x32_bf16 a[68:71], v[88:91], v[56:59], a[68:71]\n" \
"v_accvgpr_read_b32 v190, a186\n" \
"v_accvgpr_read_b32 v191, a187\n" \
"v_mfma_f32_16x16x32_bf16 a[68:71], v[152:155], v[120:123], a[68:71]\n" \
"s_add_u32 s89, s42, 0x2000\n" \
"buffer_store_dwordx4 v[232:235], v20, s[80:83], s89 offen offset:0 sc1\n" \
"v_mfma_f32_16x16x32_bf16 a[64:67], v[84:87], v[56:59], a[64:67]\n" \
"ds_read_b64_tr_b16 v[108:109], v38 offset:0\n" \
"v_accvgpr_read_b32 v180, a188\n" \
"v_mfma_f32_16x16x32_bf16 a[64:67], v[148:151], v[120:123], a[64:67]\n" \
"ds_read_b64_tr_b16 v[110:111], v38 offset:1024\n" \
"v_accvgpr_read_b32 v181, a189\n" \
"v_mfma_f32_16x16x32_bf16 a[128:131], v[84:87], v[60:63], a[128:131]\n" \
"ds_read_b64_tr_b16 v[172:173], v38 offset:8192\n" \
"v_accvgpr_read_b32 v182, a190\n" \
"v_mfma_f32_16x16x32_bf16 a[128:131], v[148:151], v[124:127], a[128:131]\n" \
"ds_read_b64_tr_b16 v[174:175], v38 offset:9216\n" \
"v_accvgpr_read_b32 v183, a191\n" \
"v_mfma_f32_16x16x32_bf16 a[132:135], v[88:91], v[60:63], a[132:135]\n" \
"v_cvt_pk_bf16_f32 v188, v188, v189\n" \
"v_cvt_pk_bf16_f32 v189, v190, v191\n" \
"v_mfma_f32_16x16x32_bf16 a[132:135], v[152:155], v[124:127], a[132:135]\n" \
"s_add_u32 s88, s42, 0x2000\n" \
"buffer_store_dwordx4 v[236:239], v20, s[80:83], s88 offen offset:1024 sc1\n" \
"v_mfma_f32_16x16x32_bf16 a[164:167], v[96:99], v[60:63], a[164:167]\n" \
"ds_read_b64_tr_b16 v[112:113], v39 offset:0\n" \
"v_cvt_pk_bf16_f32 v190, v180, v181\n" \
"v_mfma_f32_16x16x32_bf16 a[164:167], v[160:163], v[124:127], a[164:167]\n" \
"ds_read_b64_tr_b16 v[114:115], v39 offset:1024\n" \
"v_cvt_pk_bf16_f32 v191, v182, v183\n" \
"v_mfma_f32_16x16x32_bf16 a[160:163], v[60:63], v[92:95], a[160:163]\n" \
"ds_read_b64_tr_b16 v[176:177], v39 offset:8192\n" \
"v_accvgpr_read_b32 v184, a176\n" \
"v_mfma_f32_16x16x32_bf16 a[160:163], v[124:127], v[156:159], a[160:163]\n" \
"v_accvgpr_read_b32 v185, a177\n" \
"v_accvgpr_read_b32 v186, a178\n" \
"v_mfma_f32_16x16x32_bf16 a[224:227], v[64:67], v[92:95], a[224:227]\n" \
"ds_read_b64_tr_b16 v[178:179], v39 offset:9216\n" \
"v_accvgpr_read_b32 v187, a179\n" \
"v_mfma_f32_16x16x32_bf16 a[224:227], v[128:131], v[156:159], a[224:227]\n" \
"v_accvgpr_read_b32 v180, a180\n" \
"v_accvgpr_read_b32 v181, a181\n" \
"v_mfma_f32_16x16x32_bf16 a[228:231], v[96:99], v[64:67], a[228:231]\n" \
"s_add_u32 s89, s42, 0x3000\n" \
"buffer_store_dwordx4 v[240:243], v20, s[80:83], s89 offen offset:1024 sc1\n" \
"v_mfma_f32_16x16x32_bf16 a[228:231], v[160:163], v[128:131], a[228:231]\n" \
"v_accvgpr_read_b32 v182, a182\n" \
"v_accvgpr_read_b32 v183, a183\n" \
"v_mfma_f32_16x16x32_bf16 a[196:199], v[88:91], v[64:67], a[196:199]\n" \
"v_cvt_pk_bf16_f32 v184, v184, v185\n" \
"v_cvt_pk_bf16_f32 v185, v186, v187\n" \
"v_mfma_f32_16x16x32_bf16 a[196:199], v[152:155], v[128:131], a[196:199]\n" \
"v_cvt_pk_bf16_f32 v186, v180, v181\n" \
"v_cvt_pk_bf16_f32 v187, v182, v183\n" \
"v_mfma_f32_16x16x32_bf16 a[192:195], v[84:87], v[64:67], a[192:195]\n" \
"v_writelane_b32 v11, s16, 35\n" \
"s_add_i32 s16, s14, 0x411c000\n" \
"v_mfma_f32_16x16x32_bf16 a[192:195], v[148:151], v[128:131], a[192:195]\n" \
"s_add_i32 s19, s16, s15\n" \
"v_writelane_b32 v11, s19, 36\n" \
"s_waitcnt lgkmcnt(15)\n" \
"v_mfma_f32_16x16x32_bf16 a[8:11], v[88:91], v[248:251], a[8:11]\n" \
"s_add_i32 s18, s18, 0x3004000\n" \
"v_writelane_b32 v11, s18, 37\n" \
"v_mfma_f32_16x16x32_bf16 a[8:11], v[152:155], v[48:51], a[8:11]\n" \
"s_add_i32 s14, s14, 0x4120000\n" \
"v_writelane_b32 v11, s14, 38\n" \
"v_mfma_f32_16x16x32_bf16 a[40:43], v[96:99], v[248:251], a[40:43]\n" \
"s_lshl_b32 s14, s4, 2\n" \
"s_or_b32 s8, s14, s8\n" \
"v_mfma_f32_16x16x32_bf16 a[40:43], v[160:163], v[48:51], a[40:43]\n" \
"s_mul_i32 s8, s8, 0x90000\n" \
"s_lshl_b32 s14, s3, 14\n" \
"v_mfma_f32_16x16x32_bf16 a[12:15], v[248:251], v[92:95], a[12:15]\n" \
"s_add_i32 s8, s8, s14\n" \
"s_lshl_b32 s9, s9, 4\n" \
"v_mfma_f32_16x16x32_bf16 a[12:15], v[48:51], v[156:159], a[12:15]\n" \
"s_waitcnt vmcnt(3) lgkmcnt(0)\n" \
"s_barrier\n" \
"v_mfma_f32_16x16x32_bf16 a[16:19], v[100:103], v[52:55], a[16:19]\n" \
"s_add_u32 m0, s40, 0x0\n" \
"s_add_u32 s84, s39, 0xc000\n" \
"v_mfma_f32_16x16x32_bf16 a[16:19], v[164:167], v[116:119], a[16:19]\n" \
"buffer_load_dwordx4 v20, s[80:83], s84 offen sc1 lds\n" \
"v_mfma_f32_16x16x32_bf16 a[20:23], v[104:107], v[52:55], a[20:23]\n" \
"ds_read_b64_tr_b16 v[84:85], v32 offset:32768\n" \
"s_lshl_b32 s14, s4, 7\n" \
"v_mfma_f32_16x16x32_bf16 a[20:23], v[168:171], v[116:119], a[20:23]\n" \
"ds_read_b64_tr_b16 v[86:87], v32 offset:33280\n" \
"s_or_b32 s18, s9, s14\n" \
"v_mfma_f32_16x16x32_bf16 a[24:27], v[108:111], v[52:55], a[24:27]\n" \
"s_add_u32 m0, s40, 0x1000\n" \
"s_add_u32 s85, s39, 0xd000\n" \
"v_mfma_f32_16x16x32_bf16 a[24:27], v[172:175], v[116:119], a[24:27]\n" \
"buffer_load_dwordx4 v20, s[80:83], s85 offen sc1 lds\n" \
"v_mfma_f32_16x16x32_bf16 a[28:31], v[112:115], v[52:55], a[28:31]\n" \
"ds_read_b64_tr_b16 v[148:149], v32 offset:36864\n" \
"s_sub_i32 s9, s9, s14\n" \
"v_mfma_f32_16x16x32_bf16 a[28:31], v[176:179], v[116:119], a[28:31]\n" \
"ds_read_b128 v[52:55], v28 offset:32768\n" \
"s_mulk_i32 s18, 0x480\n" \
"v_mfma_f32_16x16x32_bf16 a[60:63], v[112:115], v[56:59], a[60:63]\n" \
"ds_read_b128 v[116:119], v29 offset:32768\n" \
"s_mulk_i32 s9, 0x480\n" \
"v_mfma_f32_16x16x32_bf16 a[60:63], v[176:179], v[120:123], a[60:63]\n" \
"s_add_u32 m0, s40, 0x3000\n" \
"s_mov_b32 s84, 0x300\n" \
"v_mfma_f32_16x16x32_bf16 a[56:59], v[108:111], v[56:59], a[56:59]\n" \
"buffer_load_dwordx4 v21, s[76:79], s84 offen lds\n" \
"v_mfma_f32_16x16x32_bf16 a[56:59], v[172:175], v[120:123], a[56:59]\n" \
"ds_read_b64_tr_b16 v[150:151], v32 offset:37376\n" \
"s_or_b32 s14, s18, s12\n" \
"v_mfma_f32_16x16x32_bf16 a[52:55], v[104:107], v[56:59], a[52:55]\n" \
"s_add_u32 m0, s30, 0x0\n" \
"s_mov_b32 s85, 0x300\n" \
"v_mfma_f32_16x16x32_bf16 a[52:55], v[168:171], v[120:123], a[52:55]\n" \
"buffer_load_dwordx4 v24, s[76:79], s85 offen lds\n" \
"v_mfma_f32_16x16x32_bf16 a[48:51], v[100:103], v[56:59], a[48:51]\n" \
"ds_read_b64_tr_b16 v[88:89], v33 offset:32768\n" \
"s_or_b32 s9, s9, s12\n" \
"v_mfma_f32_16x16x32_bf16 a[48:51], v[164:167], v[120:123], a[48:51]\n" \
"ds_read_b128 v[56:59], v28 offset:34816\n" \
"s_and_b32 s12, s2, 1\n" \
"s_nop 0\n" \
"v_mfma_f32_16x16x32_bf16 a[80:83], v[100:103], v[60:63], a[80:83]\n" \
"ds_read_b128 v[120:123], v29 offset:34816\n" \
"v_mfma_f32_16x16x32_bf16 a[80:83], v[164:167], v[124:127], a[80:83]\n" \
"s_add_u32 m0, s30, 0x400\n" \
"s_mov_b32 s84, 0x2700\n" \
"v_mfma_f32_16x16x32_bf16 a[84:87], v[104:107], v[60:63], a[84:87]\n" \
"buffer_load_dwordx4 v25, s[76:79], s84 offen lds\n" \
"v_mfma_f32_16x16x32_bf16 a[84:87], v[168:171], v[124:127], a[84:87]\n" \
"ds_read_b64_tr_b16 v[90:91], v33 offset:33280\n" \
"v_mfma_f32_16x16x32_bf16 a[88:91], v[108:111], v[60:63], a[88:91]\n" \
"ds_read_b64_tr_b16 v[152:153], v33 offset:36864\n" \
"v_mfma_f32_16x16x32_bf16 a[88:91], v[172:175], v[124:127], a[88:91]\n" \
"s_add_u32 m0, s31, 0x0\n" \
"s_nop 0\n" \
"s_mov_b32 s85, 0x300000\n" \
"v_mfma_f32_16x16x32_bf16 a[92:95], v[112:115], v[60:63], a[92:95]\n" \
"buffer_load_dwordx4 v26, s[68:71], s85 offen lds\n" \
"v_mfma_f32_16x16x32_bf16 a[92:95], v[176:179], v[124:127], a[92:95]\n" \
"ds_read_b128 v[60:63], v28 offset:36864\n" \
"v_mfma_f32_16x16x32_bf16 a[124:127], v[112:115], v[64:67], a[124:127]\n" \
"ds_read_b128 v[124:127], v29 offset:36864\n" \
"v_mfma_f32_16x16x32_bf16 a[124:127], v[176:179], v[128:131], a[124:127]\n" \
"s_add_u32 m0, s30, 0x800\n" \
"s_mov_b32 s84, 0x4b00\n" \
"v_mfma_f32_16x16x32_bf16 a[120:123], v[108:111], v[64:67], a[120:123]\n" \
"buffer_load_dwordx4 v24, s[76:79], s84 offen lds\n" \
"v_mfma_f32_16x16x32_bf16 a[120:123], v[172:175], v[128:131], a[120:123]\n" \
"ds_read_b64_tr_b16 v[154:155], v33 offset:37376\n" \
"v_mfma_f32_16x16x32_bf16 a[116:119], v[104:107], v[64:67], a[116:119]\n" \
"ds_read_b64_tr_b16 v[96:97], v35 offset:32768\n" \
"v_mfma_f32_16x16x32_bf16 a[116:119], v[168:171], v[128:131], a[116:119]\n" \
"ds_read_b64_tr_b16 v[98:99], v35 offset:33280\n" \
"v_mfma_f32_16x16x32_bf16 a[112:115], v[100:103], v[64:67], a[112:115]\n" \
"ds_read_b64_tr_b16 v[160:161], v35 offset:36864\n" \
"v_mfma_f32_16x16x32_bf16 a[112:115], v[164:167], v[128:131], a[112:115]\n" \
"ds_read_b128 v[64:67], v28 offset:38912\n" \
"v_mfma_f32_16x16x32_bf16 a[144:147], v[100:103], v[68:71], a[144:147]\n" \
"ds_read_b128 v[128:131], v29 offset:38912\n" \
"v_mfma_f32_16x16x32_bf16 a[144:147], v[164:167], v[132:135], a[144:147]\n" \
"s_add_u32 m0, s30, 0xc00\n" \
"s_mov_b32 s85, 0x6f00\n" \
"v_mfma_f32_16x16x32_bf16 a[148:151], v[104:107], v[68:71], a[148:151]\n" \
"buffer_load_dwordx4 v25, s[76:79], s85 offen lds\n" \
"v_mfma_f32_16x16x32_bf16 a[148:151], v[168:171], v[132:135], a[148:151]\n" \
"s_add_u32 m0, s31, 0x400\n" \
"s_mov_b32 s84, 0x308000\n" \
"v_mfma_f32_16x16x32_bf16 a[152:155], v[108:111], v[68:71], a[152:155]\n" \
"buffer_load_dwordx4 v26, s[68:71], s84 offen lds\n" \
"v_mfma_f32_16x16x32_bf16 a[152:155], v[172:175], v[132:135], a[152:155]\n" \
"ds_read_b64_tr_b16 v[162:163], v35 offset:37376\n" \
"v_mfma_f32_16x16x32_bf16 a[156:159], v[112:115], v[68:71], a[156:159]\n" \
"ds_read_b64_tr_b16 v[92:93], v34 offset:32768\n" \
"v_mfma_f32_16x16x32_bf16 a[156:159], v[176:179], v[132:135], a[156:159]\n" \
"ds_read_b128 v[68:71], v28 offset:40960\n" \
"v_mfma_f32_16x16x32_bf16 a[108:111], v[112:115], v[72:75], a[108:111]\n" \
"ds_read_b128 v[132:135], v29 offset:40960\n" \
"v_mfma_f32_16x16x32_bf16 a[108:111], v[176:179], v[136:139], a[108:111]\n" \
"s_add_u32 m0, s30, 0x1000\n" \
"s_mov_b32 s85, 0x9300\n" \
"v_mfma_f32_16x16x32_bf16 a[104:107], v[108:111], v[72:75], a[104:107]\n" \
"buffer_load_dwordx4 v24, s[76:79], s85 offen lds\n" \
"v_mfma_f32_16x16x32_bf16 a[104:107], v[172:175], v[136:139], a[104:107]\n" \
"s_add_u32 m0, s30, 0x1400\n" \
"s_mov_b32 s84, 0xb700\n" \
"v_mfma_f32_16x16x32_bf16 a[76:79], v[104:107], v[72:75], a[76:79]\n" \
"buffer_load_dwordx4 v25, s[76:79], s84 offen lds\n" \
"v_mfma_f32_16x16x32_bf16 a[76:79], v[168:171], v[136:139], a[76:79]\n" \
"ds_read_b64_tr_b16 v[94:95], v34 offset:33280\n" \
"v_mfma_f32_16x16x32_bf16 a[72:75], v[100:103], v[72:75], a[72:75]\n" \
"ds_read_b64_tr_b16 v[156:157], v34 offset:36864\n" \
"v_mfma_f32_16x16x32_bf16 a[72:75], v[164:167], v[136:139], a[72:75]\n" \
"ds_read_b128 v[72:75], v28 offset:43008\n" \
"v_mfma_f32_16x16x32_bf16 a[136:139], v[100:103], v[76:79], a[136:139]\n" \
"ds_read_b128 v[136:139], v29 offset:43008\n" \
"v_mfma_f32_16x16x32_bf16 a[136:139], v[164:167], v[140:143], a[136:139]\n" \
"s_add_u32 m0, s31, 0x800\n" \
"s_mov_b32 s85, 0x310000\n" \
"v_mfma_f32_16x16x32_bf16 a[140:143], v[104:107], v[76:79], a[140:143]\n" \
"buffer_load_dwordx4 v27, s[68:71], s85 offen lds\n" \
"v_mfma_f32_16x16x32_bf16 a[140:143], v[168:171], v[140:143], a[140:143]\n" \
"s_add_u32 m0, s30, 0x1800\n" \
"s_mov_b32 s84, 0xdb00\n" \
"v_mfma_f32_16x16x32_bf16 a[168:171], v[108:111], v[76:79], a[168:171]\n" \
"buffer_load_dwordx4 v24, s[76:79], s84 offen lds\n" \
"v_mfma_f32_16x16x32_bf16 a[168:171], v[172:175], v[140:143], a[168:171]\n" \
"ds_read_b64_tr_b16 v[158:159], v34 offset:37376\n" \
"v_mfma_f32_16x16x32_bf16 a[172:175], v[112:115], v[76:79], a[172:175]\n" \
"ds_read_b128 v[248:251], v22 offset:32768\n" \
"v_mfma_f32_16x16x32_bf16 a[172:175], v[176:179], v[140:143], a[172:175]\n" \
"ds_read_b128 v[76:79], v28 offset:45056\n" \
"v_mfma_f32_16x16x32_bf16 a[236:239], v[112:115], v[80:83], a[236:239]\n" \
"ds_read_b128 v[140:143], v29 offset:45056\n" \
"v_mfma_f32_16x16x32_bf16 a[236:239], v[176:179], v[144:147], a[236:239]\n" \
"ds_read_b128 v[48:51], v23 offset:32768\n" \
"v_mfma_f32_16x16x32_bf16 a[232:235], v[108:111], v[80:83], a[232:235]\n" \
"s_cmp_lg_u32 s12, 0\n" \
"s_cselect_b32 s13, s16, s13\n" \
"v_mfma_f32_16x16x32_bf16 a[232:235], v[172:175], v[144:147], a[232:235]\n" \
"s_bfe_u32 s16, s2, 0x20001\n" \
"s_addk_i32 s13, 0x4000\n" \
"v_mfma_f32_16x16x32_bf16 a[204:207], v[104:107], v[80:83], a[204:207]\n" \
"s_xor_b32 s16, s16, s3\n" \
"v_mfma_f32_16x16x32_bf16 a[204:207], v[168:171], v[144:147], a[204:207]\n" \
"s_cmp_lg_u32 s16, 0\n" \
"s_cselect_b32 s13, s7, s13\n" \
"v_mfma_f32_16x16x32_bf16 a[200:203], v[100:103], v[80:83], a[200:203]\n" \
"s_lshl_b32 s6, s6, 16\n" \
"v_writelane_b32 v11, s13, 39\n" \
"v_mfma_f32_16x16x32_bf16 a[200:203], v[164:167], v[144:147], a[200:203]\n" \
"ds_read_b128 v[80:83], v28 offset:47104\n" \
"ds_read_b128 v[144:147], v29 offset:47104\n" \
"s_waitcnt lgkmcnt(15)\n" \
"v_mfma_f32_16x16x32_bf16 a[0:3], v[84:87], v[52:55], a[0:3]\n" \
"s_add_u32 m0, s30, 0x1c00\n" \
"s_mov_b32 s84, 0xff00\n" \
"v_mfma_f32_16x16x32_bf16 a[0:3], v[148:151], v[116:119], a[0:3]\n" \
"buffer_load_dwordx4 v25, s[76:79], s84 offen lds\n" \
"v_mfma_f32_16x16x32_bf16 a[4:7], v[88:91], v[52:55], a[4:7]\n" \
"ds_read_b64_tr_b16 v[100:101], v36 offset:32768\n" \
"s_add_i32 s13, s8, 0x4240000\n" \
"v_mfma_f32_16x16x32_bf16 a[4:7], v[152:155], v[116:119], a[4:7]\n" \
"s_add_u32 m0, s31, 0xc00\n" \
"s_mov_b32 s85, 0x318000\n" \
"v_mfma_f32_16x16x32_bf16 a[36:39], v[96:99], v[52:55], a[36:39]\n" \
"buffer_load_dwordx4 v27, s[68:71], s85 offen lds\n" \
"s_waitcnt lgkmcnt(15)\n" \
"v_mfma_f32_16x16x32_bf16 a[36:39], v[160:163], v[116:119], a[36:39]\n" \
"ds_read_b64_tr_b16 v[102:103], v36 offset:33792\n" \
"s_add_i32 s6, s13, s6\n" \
"s_waitcnt lgkmcnt(12)\n" \
"v_mfma_f32_16x16x32_bf16 a[32:35], v[52:55], v[92:95], a[32:35]\n" \
"ds_read_b64_tr_b16 v[164:165], v36 offset:40960\n" \
"v_writelane_b32 v11, s6, 40\n" \
"s_waitcnt lgkmcnt(9)\n" \
"v_mfma_f32_16x16x32_bf16 a[32:35], v[116:119], v[156:159], a[32:35]\n" \
"ds_read_b64_tr_b16 v[166:167], v36 offset:41984\n" \
"s_add_i32 s8, s8, 0x42c0000\n" \
"v_mfma_f32_16x16x32_bf16 a[96:99], v[56:59], v[92:95], a[96:99]\n" \
"ds_read_b64_tr_b16 v[104:105], v37 offset:32768\n" \
"s_lshl_b32 s6, s12, 10\n" \
"v_mfma_f32_16x16x32_bf16 a[96:99], v[120:123], v[156:159], a[96:99]\n" \
"ds_read_b64_tr_b16 v[106:107], v37 offset:33792\n" \
"s_or_b32 s6, s8, s6\n" \
"v_mfma_f32_16x16x32_bf16 a[100:103], v[96:99], v[56:59], a[100:103]\n" \
"ds_read_b64_tr_b16 v[168:169], v37 offset:40960\n" \
"v_mfma_f32_16x16x32_bf16 a[100:103], v[160:163], v[120:123], a[100:103]\n" \
"ds_read_b64_tr_b16 v[170:171], v37 offset:41984\n" \
"v_mfma_f32_16x16x32_bf16 a[68:71], v[88:91], v[56:59], a[68:71]\n" \
"s_cmp_lg_u32 s16, 0\n" \
"s_cselect_b32 s6, s7, s6\n" \
"v_mfma_f32_16x16x32_bf16 a[68:71], v[152:155], v[120:123], a[68:71]\n" \
"s_add_u32 s88, s42, 0x3000\n" \
"buffer_store_dwordx4 v[244:247], v20, s[80:83], s88 offen offset:0 sc1\n" \
"v_mfma_f32_16x16x32_bf16 a[64:67], v[84:87], v[56:59], a[64:67]\n" \
"ds_read_b64_tr_b16 v[108:109], v38 offset:32768\n" \
"s_mulk_i32 s4, 0x4800\n" \
"v_mfma_f32_16x16x32_bf16 a[64:67], v[148:151], v[120:123], a[64:67]\n" \
"ds_read_b64_tr_b16 v[110:111], v38 offset:33792\n" \
"v_writelane_b32 v11, s6, 41\n" \
"v_mfma_f32_16x16x32_bf16 a[128:131], v[84:87], v[60:63], a[128:131]\n" \
"ds_read_b64_tr_b16 v[172:173], v38 offset:40960\n" \
"s_add_i32 s6, s14, s10\n" \
"v_mfma_f32_16x16x32_bf16 a[128:131], v[148:151], v[124:127], a[128:131]\n" \
"ds_read_b64_tr_b16 v[174:175], v38 offset:41984\n" \
"v_writelane_b32 v11, s6, 42\n" \
"v_mfma_f32_16x16x32_bf16 a[132:135], v[88:91], v[60:63], a[132:135]\n" \
"s_add_i32 s6, s9, 0x21c000\n" \
"s_add_i32 s9, s6, s10\n" \
"v_mfma_f32_16x16x32_bf16 a[132:135], v[152:155], v[124:127], a[132:135]\n" \
"s_add_u32 s89, s42, 0x0\n" \
"buffer_store_dwordx4 v[208:211], v20, s[80:83], s89 offen offset:3072 sc1\n" \
"v_mfma_f32_16x16x32_bf16 a[164:167], v[96:99], v[60:63], a[164:167]\n" \
"ds_read_b64_tr_b16 v[112:113], v39 offset:32768\n" \
"v_mfma_f32_16x16x32_bf16 a[164:167], v[160:163], v[124:127], a[164:167]\n" \
"ds_read_b64_tr_b16 v[114:115], v39 offset:33792\n" \
"v_mfma_f32_16x16x32_bf16 a[160:163], v[60:63], v[92:95], a[160:163]\n" \
"ds_read_b64_tr_b16 v[176:177], v39 offset:40960\n" \
"v_mfma_f32_16x16x32_bf16 a[160:163], v[124:127], v[156:159], a[160:163]\n" \
"s_cmp_lg_u32 s12, 0\n" \
"s_cselect_b32 s6, s6, s14\n" \
"v_mfma_f32_16x16x32_bf16 a[224:227], v[64:67], v[92:95], a[224:227]\n" \
"ds_read_b64_tr_b16 v[178:179], v39 offset:41984\n" \
"s_addk_i32 s6, 0x100\n" \
"v_mfma_f32_16x16x32_bf16 a[224:227], v[128:131], v[156:159], a[224:227]\n" \
"v_writelane_b32 v11, s9, 43\n" \
"v_mfma_f32_16x16x32_bf16 a[228:231], v[96:99], v[64:67], a[228:231]\n" \
"s_add_u32 s88, s42, 0x0\n" \
"buffer_store_dwordx4 v[212:215], v20, s[80:83], s88 offen offset:2048 sc1\n" \
"v_mfma_f32_16x16x32_bf16 a[228:231], v[160:163], v[128:131], a[228:231]\n" \
"s_cmp_lg_u32 s16, 0\n" \
"s_cselect_b32 s6, s7, s6\n" \
"v_mfma_f32_16x16x32_bf16 a[196:199], v[88:91], v[64:67], a[196:199]\n" \
"v_writelane_b32 v11, s6, 44\n" \
"s_or_b32 s6, s8, s15\n" \
"v_mfma_f32_16x16x32_bf16 a[196:199], v[152:155], v[128:131], a[196:199]\n" \
"v_writelane_b32 v11, s6, 52\n" \
"s_lshl_b32 s6, s3, 17\n" \
"v_mfma_f32_16x16x32_bf16 a[192:195], v[84:87], v[64:67], a[192:195]\n" \
"s_add_i32 s6, s13, s6\n" \
"s_or_b32 s5, s6, s5\n" \
"v_mfma_f32_16x16x32_bf16 a[192:195], v[148:151], v[128:131], a[192:195]\n" \
"s_or_b32 s5, s5, 0x3840\n" \
"v_writelane_b32 v11, s5, 6\n" \
"s_waitcnt lgkmcnt(15)\n" \
"v_mfma_f32_16x16x32_bf16 a[8:11], v[88:91], v[248:251], a[8:11]\n" \
"s_mov_b32 s5, 0xff0000\n" \
"v_mfma_f32_16x16x32_bf16 a[8:11], v[152:155], v[48:51], a[8:11]\n" \
"s_cmp_lg_u32 s16, 0\n" \
"s_cselect_b32 s5, s17, s5\n" \
"v_mfma_f32_16x16x32_bf16 a[40:43], v[96:99], v[248:251], a[40:43]\n" \
"v_writelane_b32 v11, s5, 7\n" \
"s_mul_i32 s5, s3, 0x1200\n" \
"v_mfma_f32_16x16x32_bf16 a[40:43], v[160:163], v[48:51], a[40:43]\n" \
"s_add_i32 s4, s5, s4\n" \
"s_add_i32 s4, s4, s11\n" \
"v_mfma_f32_16x16x32_bf16 a[12:15], v[248:251], v[92:95], a[12:15]\n" \
"v_writelane_b32 v11, s4, 8\n" \
"s_lshl_b32 s4, s2, 16\n" \
"v_mfma_f32_16x16x32_bf16 a[12:15], v[48:51], v[156:159], a[12:15]\n" \
"s_waitcnt vmcnt(3) lgkmcnt(0)\n" \
"s_barrier\n" \
"v_mfma_f32_16x16x32_bf16 a[16:19], v[100:103], v[52:55], a[16:19]\n" \
"s_add_u32 m0, s40, 0x8000\n" \
"s_add_u32 s85, s39, 0xe000\n" \
"v_mfma_f32_16x16x32_bf16 a[16:19], v[164:167], v[116:119], a[16:19]\n" \
"buffer_load_dwordx4 v20, s[80:83], s85 offen sc1 lds\n" \
"v_mfma_f32_16x16x32_bf16 a[20:23], v[104:107], v[52:55], a[20:23]\n" \
"ds_read_b64_tr_b16 v[84:85], v32 offset:0\n" \
"s_add_i32 s5, s4, 0x10000\n" \
"v_mfma_f32_16x16x32_bf16 a[20:23], v[168:171], v[116:119], a[20:23]\n" \
"ds_read_b64_tr_b16 v[86:87], v32 offset:512\n" \
"s_and_b32 s5, s5, 0x70000\n" \
"v_mfma_f32_16x16x32_bf16 a[24:27], v[108:111], v[52:55], a[24:27]\n" \
"s_add_u32 m0, s40, 0x9000\n" \
"s_add_u32 s84, s39, 0xf000\n" \
"v_mfma_f32_16x16x32_bf16 a[24:27], v[172:175], v[116:119], a[24:27]\n" \
"buffer_load_dwordx4 v20, s[80:83], s84 offen sc1 lds\n" \
"v_mfma_f32_16x16x32_bf16 a[28:31], v[112:115], v[52:55], a[28:31]\n" \
"ds_read_b64_tr_b16 v[148:149], v32 offset:4096\n" \
"s_add_i32 s5, s13, s5\n" \
"s_nop 0\n" \
"v_mfma_f32_16x16x32_bf16 a[28:31], v[176:179], v[116:119], a[28:31]\n" \
"ds_read_b128 v[52:55], v28 offset:0\n" \
"v_writelane_b32 v11, s5, 45\n" \
"v_mfma_f32_16x16x32_bf16 a[60:63], v[112:115], v[56:59], a[60:63]\n" \
"ds_read_b128 v[116:119], v29 offset:0\n" \
"s_add_i32 s5, s4, 0x20000\n" \
"v_mfma_f32_16x16x32_bf16 a[60:63], v[176:179], v[120:123], a[60:63]\n" \
"s_add_u32 m0, s40, 0xb000\n" \
"s_mov_b32 s85, 0x380\n" \
"v_mfma_f32_16x16x32_bf16 a[56:59], v[108:111], v[56:59], a[56:59]\n" \
"buffer_load_dwordx4 v21, s[76:79], s85 offen lds\n" \
"v_mfma_f32_16x16x32_bf16 a[56:59], v[172:175], v[120:123], a[56:59]\n" \
"ds_read_b64_tr_b16 v[150:151], v32 offset:4608\n" \
"s_and_b32 s5, s5, 0x70000\n" \
"v_mfma_f32_16x16x32_bf16 a[52:55], v[104:107], v[56:59], a[52:55]\n" \
"s_add_u32 m0, s30, 0x8000\n" \
"s_mov_b32 s84, 0x380\n" \
"v_mfma_f32_16x16x32_bf16 a[52:55], v[168:171], v[120:123], a[52:55]\n" \
"buffer_load_dwordx4 v24, s[76:79], s84 offen lds\n" \
"v_mfma_f32_16x16x32_bf16 a[48:51], v[100:103], v[56:59], a[48:51]\n" \
"ds_read_b64_tr_b16 v[88:89], v33 offset:0\n" \
"s_add_i32 s5, s13, s5\n" \
"v_mfma_f32_16x16x32_bf16 a[48:51], v[164:167], v[120:123], a[48:51]\n" \
"ds_read_b128 v[56:59], v28 offset:2048\n" \
"s_bitset1_b32 s5, 11\n" \
"v_mfma_f32_16x16x32_bf16 a[80:83], v[100:103], v[60:63], a[80:83]\n" \
"ds_read_b128 v[120:123], v29 offset:2048\n" \
"v_writelane_b32 v11, s5, 46\n" \
"v_mfma_f32_16x16x32_bf16 a[80:83], v[164:167], v[124:127], a[80:83]\n" \
"s_add_u32 m0, s30, 0x8400\n" \
"s_mov_b32 s85, 0x2780\n" \
"v_mfma_f32_16x16x32_bf16 a[84:87], v[104:107], v[60:63], a[84:87]\n" \
"buffer_load_dwordx4 v25, s[76:79], s85 offen lds\n" \
"v_mfma_f32_16x16x32_bf16 a[84:87], v[168:171], v[124:127], a[84:87]\n" \
"ds_read_b64_tr_b16 v[90:91], v33 offset:512\n" \
"s_add_i32 s5, s4, 0x30000\n" \
"v_mfma_f32_16x16x32_bf16 a[88:91], v[108:111], v[60:63], a[88:91]\n" \
"ds_read_b64_tr_b16 v[152:153], v33 offset:4096\n" \
"s_and_b32 s5, s5, 0x70000\n" \
"v_mfma_f32_16x16x32_bf16 a[88:91], v[172:175], v[124:127], a[88:91]\n" \
"s_add_u32 m0, s31, 0x8000\n" \
"s_mov_b32 s84, 0x380000\n" \
"v_mfma_f32_16x16x32_bf16 a[92:95], v[112:115], v[60:63], a[92:95]\n" \
"buffer_load_dwordx4 v26, s[68:71], s84 offen lds\n" \
"v_mfma_f32_16x16x32_bf16 a[92:95], v[176:179], v[124:127], a[92:95]\n" \
"ds_read_b128 v[60:63], v28 offset:4096\n" \
"s_add_i32 s5, s13, s5\n" \
"v_mfma_f32_16x16x32_bf16 a[124:127], v[112:115], v[64:67], a[124:127]\n" \
"ds_read_b128 v[124:127], v29 offset:4096\n" \
"s_bitset1_b32 s5, 12\n" \
"v_mfma_f32_16x16x32_bf16 a[124:127], v[176:179], v[128:131], a[124:127]\n" \
"s_add_u32 m0, s30, 0x8800\n" \
"s_mov_b32 s85, 0x4b80\n" \
"v_mfma_f32_16x16x32_bf16 a[120:123], v[108:111], v[64:67], a[120:123]\n" \
"buffer_load_dwordx4 v24, s[76:79], s85 offen lds\n" \
"v_mfma_f32_16x16x32_bf16 a[120:123], v[172:175], v[128:131], a[120:123]\n" \
"ds_read_b64_tr_b16 v[154:155], v33 offset:4608\n" \
"v_writelane_b32 v11, s5, 47\n" \
"v_mfma_f32_16x16x32_bf16 a[116:119], v[104:107], v[64:67], a[116:119]\n" \
"ds_read_b64_tr_b16 v[96:97], v35 offset:0\n" \
"s_and_b32 s5, s4, 0x70000\n" \
"v_mfma_f32_16x16x32_bf16 a[116:119], v[168:171], v[128:131], a[116:119]\n" \
"ds_read_b64_tr_b16 v[98:99], v35 offset:512\n" \
"s_xor_b32 s5, s5, 0x40000\n" \
"v_mfma_f32_16x16x32_bf16 a[112:115], v[100:103], v[64:67], a[112:115]\n" \
"ds_read_b64_tr_b16 v[160:161], v35 offset:4096\n" \
"s_add_i32 s5, s13, s5\n" \
"s_nop 0\n" \
"v_mfma_f32_16x16x32_bf16 a[112:115], v[164:167], v[128:131], a[112:115]\n" \
"ds_read_b128 v[64:67], v28 offset:6144\n" \
"s_or_b32 s5, s5, 0x1800\n" \
"v_mfma_f32_16x16x32_bf16 a[144:147], v[100:103], v[68:71], a[144:147]\n" \
"ds_read_b128 v[128:131], v29 offset:6144\n" \
"v_writelane_b32 v11, s5, 48\n" \
"v_mfma_f32_16x16x32_bf16 a[144:147], v[164:167], v[132:135], a[144:147]\n" \
"s_add_u32 m0, s30, 0x8c00\n" \
"s_mov_b32 s84, 0x6f80\n" \
"v_mfma_f32_16x16x32_bf16 a[148:151], v[104:107], v[68:71], a[148:151]\n" \
"buffer_load_dwordx4 v25, s[76:79], s84 offen lds\n" \
"v_mfma_f32_16x16x32_bf16 a[148:151], v[168:171], v[132:135], a[148:151]\n" \
"s_add_u32 m0, s31, 0x8400\n" \
"s_mov_b32 s85, 0x388000\n" \
"v_mfma_f32_16x16x32_bf16 a[152:155], v[108:111], v[68:71], a[152:155]\n" \
"buffer_load_dwordx4 v26, s[68:71], s85 offen lds\n" \
"v_mfma_f32_16x16x32_bf16 a[152:155], v[172:175], v[132:135], a[152:155]\n" \
"ds_read_b64_tr_b16 v[162:163], v35 offset:4608\n" \
"s_add_i32 s5, s4, 0x50000\n" \
"v_mfma_f32_16x16x32_bf16 a[156:159], v[112:115], v[68:71], a[156:159]\n" \
"ds_read_b64_tr_b16 v[92:93], v34 offset:0\n" \
"s_and_b32 s5, s5, 0x70000\n" \
"v_mfma_f32_16x16x32_bf16 a[156:159], v[176:179], v[132:135], a[156:159]\n" \
"ds_read_b128 v[68:71], v28 offset:8192\n" \
"s_add_i32 s5, s13, s5\n" \
"v_mfma_f32_16x16x32_bf16 a[108:111], v[112:115], v[72:75], a[108:111]\n" \
"ds_read_b128 v[132:135], v29 offset:8192\n" \
"s_bitset1_b32 s5, 13\n" \
"v_mfma_f32_16x16x32_bf16 a[108:111], v[176:179], v[136:139], a[108:111]\n" \
"s_add_u32 m0, s30, 0x9000\n" \
"s_mov_b32 s84, 0x9380\n" \
"v_mfma_f32_16x16x32_bf16 a[104:107], v[108:111], v[72:75], a[104:107]\n" \
"buffer_load_dwordx4 v24, s[76:79], s84 offen lds\n" \
"v_mfma_f32_16x16x32_bf16 a[104:107], v[172:175], v[136:139], a[104:107]\n" \
"s_add_u32 m0, s30, 0x9400\n" \
"s_mov_b32 s85, 0xb780\n" \
"v_mfma_f32_16x16x32_bf16 a[76:79], v[104:107], v[72:75], a[76:79]\n" \
"buffer_load_dwordx4 v25, s[76:79], s85 offen lds\n" \
"v_mfma_f32_16x16x32_bf16 a[76:79], v[168:171], v[136:139], a[76:79]\n" \
"ds_read_b64_tr_b16 v[94:95], v34 offset:512\n" \
"v_writelane_b32 v11, s5, 49\n" \
"v_mfma_f32_16x16x32_bf16 a[72:75], v[100:103], v[72:75], a[72:75]\n" \
"ds_read_b64_tr_b16 v[156:157], v34 offset:4096\n" \
"s_add_i32 s5, s4, 0x60000\n" \
"v_mfma_f32_16x16x32_bf16 a[72:75], v[164:167], v[136:139], a[72:75]\n" \
"ds_read_b128 v[72:75], v28 offset:10240\n" \
"s_and_b32 s5, s5, 0x70000\n" \
"v_mfma_f32_16x16x32_bf16 a[136:139], v[100:103], v[76:79], a[136:139]\n" \
"ds_read_b128 v[136:139], v29 offset:10240\n" \
"s_add_i32 s4, s4, 0x70000\n" \
"v_mfma_f32_16x16x32_bf16 a[136:139], v[164:167], v[140:143], a[136:139]\n" \
"s_add_u32 m0, s31, 0x8800\n" \
"s_mov_b32 s84, 0x390000\n" \
"v_mfma_f32_16x16x32_bf16 a[140:143], v[104:107], v[76:79], a[140:143]\n" \
"buffer_load_dwordx4 v27, s[68:71], s84 offen lds\n" \
"v_mfma_f32_16x16x32_bf16 a[140:143], v[168:171], v[140:143], a[140:143]\n" \
"s_add_u32 m0, s30, 0x9800\n" \
"s_mov_b32 s85, 0xdb80\n" \
"v_mfma_f32_16x16x32_bf16 a[168:171], v[108:111], v[76:79], a[168:171]\n" \
"buffer_load_dwordx4 v24, s[76:79], s85 offen lds\n" \
"v_mfma_f32_16x16x32_bf16 a[168:171], v[172:175], v[140:143], a[168:171]\n" \
"ds_read_b64_tr_b16 v[158:159], v34 offset:4608\n" \
"s_add_i32 s5, s13, s5\n" \
"s_nop 0\n" \
"v_mfma_f32_16x16x32_bf16 a[172:175], v[112:115], v[76:79], a[172:175]\n" \
"ds_read_b128 v[248:251], v22 offset:0\n" \
"s_and_b32 s4, s4, 0x70000\n" \
"v_mfma_f32_16x16x32_bf16 a[172:175], v[176:179], v[140:143], a[172:175]\n" \
"ds_read_b128 v[76:79], v28 offset:12288\n" \
"s_or_b32 s5, s5, 0x2800\n" \
"v_mfma_f32_16x16x32_bf16 a[236:239], v[112:115], v[80:83], a[236:239]\n" \
"ds_read_b128 v[140:143], v29 offset:12288\n" \
"v_writelane_b32 v11, s5, 50\n" \
"v_mfma_f32_16x16x32_bf16 a[236:239], v[176:179], v[144:147], a[236:239]\n" \
"ds_read_b128 v[48:51], v23 offset:0\n" \
"s_add_i32 s13, s13, s4\n" \
"v_mfma_f32_16x16x32_bf16 a[232:235], v[108:111], v[80:83], a[232:235]\n" \
"s_or_b32 s4, s13, 0x3000\n" \
"v_writelane_b32 v11, s4, 51\n" \
"v_mfma_f32_16x16x32_bf16 a[232:235], v[172:175], v[144:147], a[232:235]\n" \
"s_movk_i32 s11, 0x4000\n" \
"s_lshl_b32 s4, s2, 2\n" \
"v_mfma_f32_16x16x32_bf16 a[204:207], v[104:107], v[80:83], a[204:207]\n" \
"s_and_b32 s4, s4, 28\n" \
"s_bfe_u32 s5, s2, 0x20003\n" \
"v_mfma_f32_16x16x32_bf16 a[204:207], v[168:171], v[144:147], a[204:207]\n" \
"s_or_b32 s4, s4, s5\n" \
"s_lshr_b32 s5, s2, 5\n" \
"v_mfma_f32_16x16x32_bf16 a[200:203], v[100:103], v[80:83], a[200:203]\n" \
"s_lshr_b32 s19, s3, 1\n" \
"s_lshl_b32 s18, s5, 1\n" \
"v_mfma_f32_16x16x32_bf16 a[200:203], v[164:167], v[144:147], a[200:203]\n" \
"ds_read_b128 v[80:83], v28 offset:14336\n" \
"ds_read_b128 v[144:147], v29 offset:14336\n" \
"s_waitcnt lgkmcnt(15)\n" \
"v_mfma_f32_16x16x32_bf16 a[0:3], v[84:87], v[52:55], a[0:3]\n" \
"s_add_u32 m0, s30, 0x9c00\n" \
"s_mov_b32 s85, 0xff80\n" \
"v_mfma_f32_16x16x32_bf16 a[0:3], v[148:151], v[116:119], a[0:3]\n" \
"buffer_load_dwordx4 v25, s[76:79], s85 offen lds\n" \
"v_mfma_f32_16x16x32_bf16 a[4:7], v[88:91], v[52:55], a[4:7]\n" \
"ds_read_b64_tr_b16 v[100:101], v36 offset:0\n" \
"s_lshl_b32 s6, s4, 20\n" \
"v_mfma_f32_16x16x32_bf16 a[4:7], v[152:155], v[116:119], a[4:7]\n" \
"s_add_u32 m0, s31, 0x8c00\n" \
"s_mov_b32 s84, 0x398000\n" \
"v_mfma_f32_16x16x32_bf16 a[36:39], v[96:99], v[52:55], a[36:39]\n" \
"buffer_load_dwordx4 v27, s[68:71], s84 offen lds\n" \
"s_waitcnt lgkmcnt(15)\n" \
"v_mfma_f32_16x16x32_bf16 a[36:39], v[160:163], v[116:119], a[36:39]\n" \
"ds_read_b64_tr_b16 v[102:103], v36 offset:1024\n" \
"s_lshl_b32 s7, s4, 8\n" \
"s_waitcnt lgkmcnt(12)\n" \
"v_mfma_f32_16x16x32_bf16 a[32:35], v[52:55], v[92:95], a[32:35]\n" \
"ds_read_b64_tr_b16 v[164:165], v36 offset:8192\n" \
"s_and_b32 s13, s3, 1\n" \
"s_waitcnt lgkmcnt(9)\n" \
"v_mfma_f32_16x16x32_bf16 a[32:35], v[116:119], v[156:159], a[32:35]\n" \
"ds_read_b64_tr_b16 v[166:167], v36 offset:9216\n" \
"s_add_i32 s14, s18, s19\n" \
"v_mfma_f32_16x16x32_bf16 a[96:99], v[56:59], v[92:95], a[96:99]\n" \
"ds_read_b64_tr_b16 v[104:105], v37 offset:0\n" \
"v_mfma_f32_16x16x32_bf16 a[96:99], v[120:123], v[156:159], a[96:99]\n" \
"ds_read_b64_tr_b16 v[106:107], v37 offset:1024\n" \
"v_mfma_f32_16x16x32_bf16 a[100:103], v[96:99], v[56:59], a[100:103]\n" \
"ds_read_b64_tr_b16 v[168:169], v37 offset:8192\n" \
"v_mfma_f32_16x16x32_bf16 a[100:103], v[160:163], v[120:123], a[100:103]\n" \
"ds_read_b64_tr_b16 v[170:171], v37 offset:9216\n" \
"v_mfma_f32_16x16x32_bf16 a[68:71], v[88:91], v[56:59], a[68:71]\n" \
"s_add_u32 s6, s26, s6\n" \
"s_addc_u32 s9, s27, 0\n" \
"v_mfma_f32_16x16x32_bf16 a[68:71], v[152:155], v[120:123], a[68:71]\n" \
"s_add_u32 s89, s42, 0x1000\n" \
"buffer_store_dwordx4 v[184:187], v20, s[80:83], s89 offen offset:2048 sc1\n" \
"v_mfma_f32_16x16x32_bf16 a[64:67], v[84:87], v[56:59], a[64:67]\n" \
"ds_read_b64_tr_b16 v[108:109], v38 offset:0\n" \
"v_mfma_f32_16x16x32_bf16 a[64:67], v[148:151], v[120:123], a[64:67]\n" \
"ds_read_b64_tr_b16 v[110:111], v38 offset:1024\n" \
"v_mfma_f32_16x16x32_bf16 a[128:131], v[84:87], v[60:63], a[128:131]\n" \
"ds_read_b64_tr_b16 v[172:173], v38 offset:8192\n" \
"v_mfma_f32_16x16x32_bf16 a[128:131], v[148:151], v[124:127], a[128:131]\n" \
"ds_read_b64_tr_b16 v[174:175], v38 offset:9216\n" \
"v_mfma_f32_16x16x32_bf16 a[132:135], v[88:91], v[60:63], a[132:135]\n" \
"s_add_u32 s8, s6, 0x1000000\n" \
"s_addc_u32 s6, s9, 0\n" \
"v_mfma_f32_16x16x32_bf16 a[132:135], v[152:155], v[124:127], a[132:135]\n" \
"s_add_u32 s88, s42, 0x1000\n" \
"buffer_store_dwordx4 v[188:191], v20, s[80:83], s88 offen offset:3072 sc1\n" \
"v_mfma_f32_16x16x32_bf16 a[164:167], v[96:99], v[60:63], a[164:167]\n" \
"ds_read_b64_tr_b16 v[112:113], v39 offset:0\n" \
"s_and_b32 s9, s6, 0xffff\n" \
"v_mfma_f32_16x16x32_bf16 a[164:167], v[160:163], v[124:127], a[164:167]\n" \
"ds_read_b64_tr_b16 v[114:115], v39 offset:1024\n" \
"s_lshl_b32 s6, s5, 16\n" \
"v_mfma_f32_16x16x32_bf16 a[160:163], v[60:63], v[92:95], a[160:163]\n" \
"ds_read_b64_tr_b16 v[176:177], v39 offset:8192\n" \
"s_sub_i32 s10, 0x100000, s6\n" \
"v_mfma_f32_16x16x32_bf16 a[160:163], v[124:127], v[156:159], a[160:163]\n" \
"s_mov_b32 s6, 0x8000\n" \
"v_mfma_f32_16x16x32_bf16 a[224:227], v[64:67], v[92:95], a[224:227]\n" \
"ds_read_b64_tr_b16 v[178:179], v39 offset:9216\n" \
"v_mfma_f32_16x16x32_bf16 a[224:227], v[128:131], v[156:159], a[224:227]\n" \
"s_cmp_lg_u32 s5, 0\n" \
"s_cselect_b32 s17, s11, s6\n" \
"v_mfma_f32_16x16x32_bf16 a[228:231], v[96:99], v[64:67], a[228:231]\n" \
"s_add_u32 s89, s42, 0x2000\n" \
"buffer_store_dwordx4 v[192:195], v20, s[80:83], s89 offen offset:2048 sc1\n" \
"v_mfma_f32_16x16x32_bf16 a[228:231], v[160:163], v[128:131], a[228:231]\n" \
"s_lshl_b32 s6, s3, 1\n" \
"s_lshl_b32 s5, s5, 3\n" \
"v_mfma_f32_16x16x32_bf16 a[196:199], v[88:91], v[64:67], a[196:199]\n" \
"s_and_b32 s6, s6, 4\n" \
"s_or_b32 s12, s5, s6\n" \
"v_mfma_f32_16x16x32_bf16 a[196:199], v[152:155], v[128:131], a[196:199]\n" \
"s_sub_i32 s5, s13, s5\n" \
"s_or_b32 s5, s5, s6\n" \
"v_mfma_f32_16x16x32_bf16 a[192:195], v[84:87], v[64:67], a[192:195]\n" \
"s_or_b32 s6, s12, s13\n" \
"s_lshl_b32 s4, s4, 2\n" \
"v_mfma_f32_16x16x32_bf16 a[192:195], v[148:151], v[128:131], a[192:195]\n" \
"s_mulk_i32 s6, 0x92\n" \
"s_add_i32 s4, s4, 18\n" \
"s_waitcnt lgkmcnt(15)\n" \
"v_mfma_f32_16x16x32_bf16 a[8:11], v[88:91], v[248:251], a[8:11]\n" \
"s_mulk_i32 s5, 0x92\n" \
"s_add_i32 s6, s4, s6\n" \
"v_mfma_f32_16x16x32_bf16 a[8:11], v[152:155], v[48:51], a[8:11]\n" \
"s_add_i32 s5, s5, s4\n" \
"s_lshl_b32 s4, s3, 11\n" \
"v_mfma_f32_16x16x32_bf16 a[40:43], v[96:99], v[248:251], a[40:43]\n" \
"s_lshl_b32 s15, s13, 12\n" \
"s_lshl_b32 s16, s19, 14\n" \
"v_mfma_f32_16x16x32_bf16 a[40:43], v[160:163], v[48:51], a[40:43]\n" \
"s_mul_i32 s19, s19, 0xe000\n" \
"s_mov_b32 s11, 0x20000\n" \
"v_mfma_f32_16x16x32_bf16 a[12:15], v[248:251], v[92:95], a[12:15]\n" \
"s_or_b32 s12, s4, 0xc000\n" \
"s_lshl_b32 s4, s3, 8\n" \
"v_mfma_f32_16x16x32_bf16 a[12:15], v[48:51], v[156:159], a[12:15]\n" \
"s_waitcnt vmcnt(3) lgkmcnt(0)\n" \
"s_barrier\n" \
"v_mfma_f32_16x16x32_bf16 a[16:19], v[100:103], v[52:55], a[16:19]\n" \
"s_add_u32 m0, s40, 0x0\n" \
"s_add_u32 s84, s39, 0x10000\n" \
"v_mfma_f32_16x16x32_bf16 a[16:19], v[164:167], v[116:119], a[16:19]\n" \
"buffer_load_dwordx4 v20, s[80:83], s84 offen sc1 lds\n" \
"v_mfma_f32_16x16x32_bf16 a[20:23], v[104:107], v[52:55], a[20:23]\n" \
"ds_read_b64_tr_b16 v[84:85], v32 offset:32768\n" \
"s_or_b32 s22, s15, s16\n" \
"v_mfma_f32_16x16x32_bf16 a[20:23], v[168:171], v[116:119], a[20:23]\n" \
"ds_read_b64_tr_b16 v[86:87], v32 offset:33280\n" \
"s_or_b32 s23, s19, s15\n" \
"v_mfma_f32_16x16x32_bf16 a[24:27], v[108:111], v[52:55], a[24:27]\n" \
"s_add_u32 m0, s40, 0x1000\n" \
"s_add_u32 s85, s39, 0x11000\n" \
"v_mfma_f32_16x16x32_bf16 a[24:27], v[172:175], v[116:119], a[24:27]\n" \
"buffer_load_dwordx4 v20, s[80:83], s85 offen sc1 lds\n" \
"v_mfma_f32_16x16x32_bf16 a[28:31], v[112:115], v[52:55], a[28:31]\n" \
"ds_read_b64_tr_b16 v[148:149], v32 offset:36864\n" \
"s_lshl_b32 s60, s6, 10\n" \
"v_mfma_f32_16x16x32_bf16 a[28:31], v[176:179], v[116:119], a[28:31]\n" \
"ds_read_b128 v[52:55], v28 offset:32768\n" \
"s_lshl_b32 s61, s5, 10\n" \
"v_mfma_f32_16x16x32_bf16 a[60:63], v[112:115], v[56:59], a[60:63]\n" \
"ds_read_b128 v[116:119], v29 offset:32768\n" \
"s_lshl_b32 s14, s14, 15\n" \
"v_mfma_f32_16x16x32_bf16 a[60:63], v[176:179], v[120:123], a[60:63]\n" \
"s_add_u32 m0, s40, 0x3000\n" \
"s_mov_b32 s84, 0x400\n" \
"v_mfma_f32_16x16x32_bf16 a[56:59], v[108:111], v[56:59], a[56:59]\n" \
"buffer_load_dwordx4 v21, s[76:79], s84 offen lds\n" \
"v_mfma_f32_16x16x32_bf16 a[56:59], v[172:175], v[120:123], a[56:59]\n" \
"ds_read_b64_tr_b16 v[150:151], v32 offset:37376\n" \
"s_or_b32 s20, s4, 0x26c00\n" \
"v_mfma_f32_16x16x32_bf16 a[52:55], v[104:107], v[56:59], a[52:55]\n" \
"s_add_u32 m0, s30, 0x0\n" \
"s_nop 0\n" \
"s_mov_b32 s85, 0x400\n" \
"v_mfma_f32_16x16x32_bf16 a[52:55], v[168:171], v[120:123], a[52:55]\n" \
"buffer_load_dwordx4 v24, s[76:79], s85 offen lds\n" \
"v_mfma_f32_16x16x32_bf16 a[48:51], v[100:103], v[56:59], a[48:51]\n" \
"ds_read_b64_tr_b16 v[88:89], v33 offset:32768\n" \
"s_or_b32 s13, s22, 0x8000\n" \
"v_mfma_f32_16x16x32_bf16 a[48:51], v[164:167], v[120:123], a[48:51]\n" \
"ds_read_b128 v[56:59], v28 offset:34816\n" \
"s_add_i32 s22, s22, 0x14000\n" \
"v_mfma_f32_16x16x32_bf16 a[80:83], v[100:103], v[60:63], a[80:83]\n" \
"ds_read_b128 v[120:123], v29 offset:34816\n" \
"s_bitset1_b32 s23, 16\n" \
"s_nop 0\n" \
"v_mfma_f32_16x16x32_bf16 a[80:83], v[164:167], v[124:127], a[80:83]\n" \
"s_add_u32 m0, s30, 0x400\n" \
"s_mov_b32 s84, 0x2800\n" \
"v_mfma_f32_16x16x32_bf16 a[84:87], v[104:107], v[60:63], a[84:87]\n" \
"buffer_load_dwordx4 v25, s[76:79], s84 offen lds\n" \
"v_mfma_f32_16x16x32_bf16 a[84:87], v[168:171], v[124:127], a[84:87]\n" \
"ds_read_b64_tr_b16 v[90:91], v33 offset:33280\n" \
"s_or_b32 s24, s7, 0x5be8020\n" \
"v_mfma_f32_16x16x32_bf16 a[88:91], v[108:111], v[60:63], a[88:91]\n" \
"ds_read_b64_tr_b16 v[152:153], v33 offset:36864\n" \
"s_and_b32 s57, s27, 0xffff\n" \
"v_mfma_f32_16x16x32_bf16 a[88:91], v[172:175], v[124:127], a[88:91]\n" \
"s_add_u32 m0, s31, 0x0\n" \
"s_mov_b32 s85, 0x400000\n" \
"v_mfma_f32_16x16x32_bf16 a[92:95], v[112:115], v[60:63], a[92:95]\n" \
"buffer_load_dwordx4 v26, s[68:71], s85 offen lds\n" \
"v_mfma_f32_16x16x32_bf16 a[92:95], v[176:179], v[124:127], a[92:95]\n" \
"ds_read_b128 v[60:63], v28 offset:36864\n" \
"s_mov_b32 s58, 0x6000000\n" \
"v_mfma_f32_16x16x32_bf16 a[124:127], v[112:115], v[64:67], a[124:127]\n" \
"ds_read_b128 v[124:127], v29 offset:36864\n" \
"s_mov_b32 s56, s26\n" \
"v_mfma_f32_16x16x32_bf16 a[124:127], v[176:179], v[128:131], a[124:127]\n" \
"s_add_u32 m0, s30, 0x800\n" \
"s_mov_b32 s84, 0x4c00\n" \
"v_mfma_f32_16x16x32_bf16 a[120:123], v[108:111], v[64:67], a[120:123]\n" \
"buffer_load_dwordx4 v24, s[76:79], s84 offen lds\n" \
"v_mfma_f32_16x16x32_bf16 a[120:123], v[172:175], v[128:131], a[120:123]\n" \
"ds_read_b64_tr_b16 v[154:155], v33 offset:37376\n" \
"s_mov_b32 s59, s11\n" \
"v_mfma_f32_16x16x32_bf16 a[116:119], v[104:107], v[64:67], a[116:119]\n" \
"ds_read_b64_tr_b16 v[96:97], v35 offset:32768\n" \
"s_add_i32 s60, s60, 0x3000000\n" \
"v_mfma_f32_16x16x32_bf16 a[116:119], v[168:171], v[128:131], a[116:119]\n" \
"ds_read_b64_tr_b16 v[98:99], v35 offset:33280\n" \
"s_add_i32 s61, s61, 0x411c000\n" \
"v_mfma_f32_16x16x32_bf16 a[112:115], v[100:103], v[64:67], a[112:115]\n" \
"ds_read_b64_tr_b16 v[160:161], v35 offset:36864\n" \
"s_sub_i32 s19, 14, s18\n" \
"v_mfma_f32_16x16x32_bf16 a[112:115], v[164:167], v[128:131], a[112:115]\n" \
"ds_read_b128 v[64:67], v28 offset:38912\n" \
"s_mov_b32 s21, 0x71800000\n" \
"v_mfma_f32_16x16x32_bf16 a[144:147], v[100:103], v[68:71], a[144:147]\n" \
"ds_read_b128 v[128:131], v29 offset:38912\n" \
"v_mfma_f32_16x16x32_bf16 a[144:147], v[164:167], v[132:135], a[144:147]\n" \
"s_add_u32 m0, s30, 0xc00\n" \
"s_mov_b32 s85, 0x7000\n" \
"v_mfma_f32_16x16x32_bf16 a[148:151], v[104:107], v[68:71], a[148:151]\n" \
"buffer_load_dwordx4 v25, s[76:79], s85 offen lds\n" \
"v_mfma_f32_16x16x32_bf16 a[148:151], v[168:171], v[132:135], a[148:151]\n" \
"s_add_u32 m0, s31, 0x400\n" \
"s_mov_b32 s84, 0x408000\n" \
"v_mfma_f32_16x16x32_bf16 a[152:155], v[108:111], v[68:71], a[152:155]\n" \
"buffer_load_dwordx4 v26, s[68:71], s84 offen lds\n" \
"v_mfma_f32_16x16x32_bf16 a[152:155], v[172:175], v[132:135], a[152:155]\n" \
"ds_read_b64_tr_b16 v[162:163], v35 offset:37376\n" \
"v_mfma_f32_16x16x32_bf16 a[156:159], v[112:115], v[68:71], a[156:159]\n" \
"ds_read_b64_tr_b16 v[92:93], v34 offset:32768\n" \
"v_mfma_f32_16x16x32_bf16 a[156:159], v[176:179], v[132:135], a[156:159]\n" \
"ds_read_b128 v[68:71], v28 offset:40960\n" \
"v_mfma_f32_16x16x32_bf16 a[108:111], v[112:115], v[72:75], a[108:111]\n" \
"ds_read_b128 v[132:135], v29 offset:40960\n" \
"v_mfma_f32_16x16x32_bf16 a[108:111], v[176:179], v[136:139], a[108:111]\n" \
"s_add_u32 m0, s30, 0x1000\n" \
"s_mov_b32 s85, 0x9400\n" \
"v_mfma_f32_16x16x32_bf16 a[104:107], v[108:111], v[72:75], a[104:107]\n" \
"buffer_load_dwordx4 v24, s[76:79], s85 offen lds\n" \
"v_mfma_f32_16x16x32_bf16 a[104:107], v[172:175], v[136:139], a[104:107]\n" \
"s_add_u32 m0, s30, 0x1400\n" \
"s_mov_b32 s84, 0xb800\n" \
"v_mfma_f32_16x16x32_bf16 a[76:79], v[104:107], v[72:75], a[76:79]\n" \
"buffer_load_dwordx4 v25, s[76:79], s84 offen lds\n" \
"v_mfma_f32_16x16x32_bf16 a[76:79], v[168:171], v[136:139], a[76:79]\n" \
"ds_read_b64_tr_b16 v[94:95], v34 offset:33280\n" \
"v_mfma_f32_16x16x32_bf16 a[72:75], v[100:103], v[72:75], a[72:75]\n" \
"ds_read_b64_tr_b16 v[156:157], v34 offset:36864\n" \
"v_mfma_f32_16x16x32_bf16 a[72:75], v[164:167], v[136:139], a[72:75]\n" \
"ds_read_b128 v[72:75], v28 offset:43008\n" \
"v_mfma_f32_16x16x32_bf16 a[136:139], v[100:103], v[76:79], a[136:139]\n" \
"ds_read_b128 v[136:139], v29 offset:43008\n" \
"v_mfma_f32_16x16x32_bf16 a[136:139], v[164:167], v[140:143], a[136:139]\n" \
"s_add_u32 m0, s31, 0x800\n" \
"s_mov_b32 s85, 0x410000\n" \
"v_mfma_f32_16x16x32_bf16 a[140:143], v[104:107], v[76:79], a[140:143]\n" \
"buffer_load_dwordx4 v27, s[68:71], s85 offen lds\n" \
"v_mfma_f32_16x16x32_bf16 a[140:143], v[168:171], v[140:143], a[140:143]\n" \
"s_add_u32 m0, s30, 0x1800\n" \
"s_mov_b32 s84, 0xdc00\n" \
"v_mfma_f32_16x16x32_bf16 a[168:171], v[108:111], v[76:79], a[168:171]\n" \
"buffer_load_dwordx4 v24, s[76:79], s84 offen lds\n" \
"v_mfma_f32_16x16x32_bf16 a[168:171], v[172:175], v[140:143], a[168:171]\n" \
"ds_read_b64_tr_b16 v[158:159], v34 offset:37376\n" \
"v_mfma_f32_16x16x32_bf16 a[172:175], v[112:115], v[76:79], a[172:175]\n" \
"ds_read_b128 v[248:251], v22 offset:32768\n" \
"v_mfma_f32_16x16x32_bf16 a[172:175], v[176:179], v[140:143], a[172:175]\n" \
"ds_read_b128 v[76:79], v28 offset:45056\n" \
"v_mfma_f32_16x16x32_bf16 a[236:239], v[112:115], v[80:83], a[236:239]\n" \
"ds_read_b128 v[140:143], v29 offset:45056\n" \
"v_mfma_f32_16x16x32_bf16 a[236:239], v[176:179], v[144:147], a[236:239]\n" \
"ds_read_b128 v[48:51], v23 offset:32768\n" \
"v_mfma_f32_16x16x32_bf16 a[232:235], v[108:111], v[80:83], a[232:235]\n" \
"v_mfma_f32_16x16x32_bf16 a[232:235], v[172:175], v[144:147], a[232:235]\n" \
"v_mfma_f32_16x16x32_bf16 a[204:207], v[104:107], v[80:83], a[204:207]\n" \
"v_mfma_f32_16x16x32_bf16 a[204:207], v[168:171], v[144:147], a[204:207]\n" \
"v_mfma_f32_16x16x32_bf16 a[200:203], v[100:103], v[80:83], a[200:203]\n" \
"v_mfma_f32_16x16x32_bf16 a[200:203], v[164:167], v[144:147], a[200:203]\n" \
"ds_read_b128 v[80:83], v28 offset:47104\n" \
"ds_read_b128 v[144:147], v29 offset:47104\n" \
"s_waitcnt lgkmcnt(15)\n" \
"s_nop 0\n" \
"v_mfma_f32_16x16x32_bf16 a[0:3], v[84:87], v[52:55], a[0:3]\n" \
"s_add_u32 m0, s30, 0x1c00\n" \
"s_mov_b32 s84, 0x10000\n" \
"v_mfma_f32_16x16x32_bf16 a[0:3], v[148:151], v[116:119], a[0:3]\n" \
"buffer_load_dwordx4 v25, s[76:79], s84 offen lds\n" \
"v_mfma_f32_16x16x32_bf16 a[4:7], v[88:91], v[52:55], a[4:7]\n" \
"ds_read_b64_tr_b16 v[100:101], v36 offset:32768\n" \
"v_mfma_f32_16x16x32_bf16 a[4:7], v[152:155], v[116:119], a[4:7]\n" \
"s_add_u32 m0, s31, 0xc00\n" \
"s_mov_b32 s85, 0x418000\n" \
"v_mfma_f32_16x16x32_bf16 a[36:39], v[96:99], v[52:55], a[36:39]\n" \
"buffer_load_dwordx4 v27, s[68:71], s85 offen lds\n" \
"s_waitcnt lgkmcnt(15)\n" \
"v_mfma_f32_16x16x32_bf16 a[36:39], v[160:163], v[116:119], a[36:39]\n" \
"ds_read_b64_tr_b16 v[102:103], v36 offset:33792\n" \
"s_waitcnt lgkmcnt(12)\n" \
"v_mfma_f32_16x16x32_bf16 a[32:35], v[52:55], v[92:95], a[32:35]\n" \
"ds_read_b64_tr_b16 v[164:165], v36 offset:40960\n" \
"s_waitcnt lgkmcnt(9)\n" \
"s_nop 0\n" \
"v_mfma_f32_16x16x32_bf16 a[32:35], v[116:119], v[156:159], a[32:35]\n" \
"ds_read_b64_tr_b16 v[166:167], v36 offset:41984\n" \
"v_mfma_f32_16x16x32_bf16 a[96:99], v[56:59], v[92:95], a[96:99]\n" \
"ds_read_b64_tr_b16 v[104:105], v37 offset:32768\n" \
"v_mfma_f32_16x16x32_bf16 a[96:99], v[120:123], v[156:159], a[96:99]\n" \
"ds_read_b64_tr_b16 v[106:107], v37 offset:33792\n" \
"v_mfma_f32_16x16x32_bf16 a[100:103], v[96:99], v[56:59], a[100:103]\n" \
"ds_read_b64_tr_b16 v[168:169], v37 offset:40960\n" \
"v_mfma_f32_16x16x32_bf16 a[100:103], v[160:163], v[120:123], a[100:103]\n" \
"ds_read_b64_tr_b16 v[170:171], v37 offset:41984\n" \
"v_mfma_f32_16x16x32_bf16 a[68:71], v[88:91], v[56:59], a[68:71]\n" \
"v_mfma_f32_16x16x32_bf16 a[68:71], v[152:155], v[120:123], a[68:71]\n" \
"s_add_u32 s88, s42, 0x2000\n" \
"buffer_store_dwordx4 v[196:199], v20, s[80:83], s88 offen offset:3072 sc1\n" \
"v_mfma_f32_16x16x32_bf16 a[64:67], v[84:87], v[56:59], a[64:67]\n" \
"ds_read_b64_tr_b16 v[108:109], v38 offset:32768\n" \
"v_mfma_f32_16x16x32_bf16 a[64:67], v[148:151], v[120:123], a[64:67]\n" \
"ds_read_b64_tr_b16 v[110:111], v38 offset:33792\n" \
"v_mfma_f32_16x16x32_bf16 a[128:131], v[84:87], v[60:63], a[128:131]\n" \
"ds_read_b64_tr_b16 v[172:173], v38 offset:40960\n" \
"v_mfma_f32_16x16x32_bf16 a[128:131], v[148:151], v[124:127], a[128:131]\n" \
"ds_read_b64_tr_b16 v[174:175], v38 offset:41984\n" \
"v_mfma_f32_16x16x32_bf16 a[132:135], v[88:91], v[60:63], a[132:135]\n" \
"v_mfma_f32_16x16x32_bf16 a[132:135], v[152:155], v[124:127], a[132:135]\n" \
"s_add_u32 s89, s42, 0x3000\n" \
"buffer_store_dwordx4 v[200:203], v20, s[80:83], s89 offen offset:2048 sc1\n" \
"v_mfma_f32_16x16x32_bf16 a[164:167], v[96:99], v[60:63], a[164:167]\n" \
"ds_read_b64_tr_b16 v[112:113], v39 offset:32768\n" \
"v_mfma_f32_16x16x32_bf16 a[164:167], v[160:163], v[124:127], a[164:167]\n" \
"ds_read_b64_tr_b16 v[114:115], v39 offset:33792\n" \
"v_mfma_f32_16x16x32_bf16 a[160:163], v[60:63], v[92:95], a[160:163]\n" \
"ds_read_b64_tr_b16 v[176:177], v39 offset:40960\n" \
"v_mfma_f32_16x16x32_bf16 a[160:163], v[124:127], v[156:159], a[160:163]\n" \
"v_mfma_f32_16x16x32_bf16 a[224:227], v[64:67], v[92:95], a[224:227]\n" \
"ds_read_b64_tr_b16 v[178:179], v39 offset:41984\n" \
"v_mfma_f32_16x16x32_bf16 a[224:227], v[128:131], v[156:159], a[224:227]\n" \
"v_mfma_f32_16x16x32_bf16 a[228:231], v[96:99], v[64:67], a[228:231]\n" \
"s_add_u32 s88, s42, 0x3000\n" \
"buffer_store_dwordx4 v[204:207], v20, s[80:83], s88 offen offset:3072 sc1\n" \
"v_mfma_f32_16x16x32_bf16 a[228:231], v[160:163], v[128:131], a[228:231]\n" \
"v_mfma_f32_16x16x32_bf16 a[196:199], v[88:91], v[64:67], a[196:199]\n" \
"v_mfma_f32_16x16x32_bf16 a[196:199], v[152:155], v[128:131], a[196:199]\n" \
"v_mfma_f32_16x16x32_bf16 a[192:195], v[84:87], v[64:67], a[192:195]\n" \
"v_mfma_f32_16x16x32_bf16 a[192:195], v[148:151], v[128:131], a[192:195]\n" \
"s_waitcnt lgkmcnt(15)\n" \
"v_mfma_f32_16x16x32_bf16 a[8:11], v[88:91], v[248:251], a[8:11]\n" \
"v_mfma_f32_16x16x32_bf16 a[8:11], v[152:155], v[48:51], a[8:11]\n" \
"v_mfma_f32_16x16x32_bf16 a[40:43], v[96:99], v[248:251], a[40:43]\n" \
"v_mfma_f32_16x16x32_bf16 a[40:43], v[160:163], v[48:51], a[40:43]\n" \
"v_mfma_f32_16x16x32_bf16 a[12:15], v[248:251], v[92:95], a[12:15]\n" \
"v_mfma_f32_16x16x32_bf16 a[12:15], v[48:51], v[156:159], a[12:15]\n" \
"s_waitcnt vmcnt(3) lgkmcnt(0)\n" \
"s_barrier\n" \
"v_mfma_f32_16x16x32_bf16 a[16:19], v[100:103], v[52:55], a[16:19]\n" \
"ds_read_b64_tr_b16 v[84:85], v32 offset:0\n" \
"v_lshrrev_b32_e32 v24, 9, v20\n" \
"v_mfma_f32_16x16x32_bf16 a[16:19], v[164:167], v[116:119], a[16:19]\n" \
"v_and_b32_e32 v26, 0x1ff, v20\n" \
"v_lshl_add_u32 v24, v24, 3, v26\n" \
"v_mfma_f32_16x16x32_bf16 a[20:23], v[104:107], v[52:55], a[20:23]\n" \
"ds_read_b64_tr_b16 v[86:87], v32 offset:512\n" \
"v_lshrrev_b32_e64 v25, 8, v20\n" \
"v_mfma_f32_16x16x32_bf16 a[20:23], v[168:171], v[116:119], a[20:23]\n" \
"v_mul_u32_u24_e32 v25, 0x24700, v25\n" \
"v_add_u32_e64 v25, v25, v20\n" \
"v_mfma_f32_16x16x32_bf16 a[24:27], v[108:111], v[52:55], a[24:27]\n" \
"v_lshrrev_b32_e32 v26, 2, v20\n" \
"v_add_u32_e32 v26, s45, v26\n" \
"v_mfma_f32_16x16x32_bf16 a[24:27], v[172:175], v[116:119], a[24:27]\n" \
"ds_read_b64_tr_b16 v[148:149], v32 offset:4096\n" \
"v_add_u32_e64 v27, s46, v20\n" \
"v_mfma_f32_16x16x32_bf16 a[28:31], v[112:115], v[52:55], a[28:31]\n" \
"v_add_u32_e64 v21, s43, v27\n" \
"v_mfma_f32_16x16x32_bf16 a[28:31], v[176:179], v[116:119], a[28:31]\n" \
"ds_read_b128 v[52:55], v28 offset:0\n" \
"v_mfma_f32_16x16x32_bf16 a[60:63], v[112:115], v[56:59], a[60:63]\n" \
"ds_read_b128 v[116:119], v29 offset:0\n" \
"v_mfma_f32_16x16x32_bf16 a[60:63], v[176:179], v[120:123], a[60:63]\n" \
"ds_read_b64_tr_b16 v[150:151], v32 offset:4608\n" \
"v_mfma_f32_16x16x32_bf16 a[56:59], v[108:111], v[56:59], a[56:59]\n" \
"v_mfma_f32_16x16x32_bf16 a[56:59], v[172:175], v[120:123], a[56:59]\n" \
"ds_read_b64_tr_b16 v[88:89], v33 offset:0\n" \
"v_mfma_f32_16x16x32_bf16 a[52:55], v[104:107], v[56:59], a[52:55]\n" \
"v_mfma_f32_16x16x32_bf16 a[52:55], v[168:171], v[120:123], a[52:55]\n" \
"v_mfma_f32_16x16x32_bf16 a[48:51], v[100:103], v[56:59], a[48:51]\n" \
"ds_read_b64_tr_b16 v[90:91], v33 offset:512\n" \
"v_mfma_f32_16x16x32_bf16 a[48:51], v[164:167], v[120:123], a[48:51]\n" \
"ds_read_b128 v[56:59], v28 offset:2048\n" \
"v_mfma_f32_16x16x32_bf16 a[80:83], v[100:103], v[60:63], a[80:83]\n" \
"ds_read_b128 v[120:123], v29 offset:2048\n" \
"v_mfma_f32_16x16x32_bf16 a[80:83], v[164:167], v[124:127], a[80:83]\n" \
"ds_read_b64_tr_b16 v[152:153], v33 offset:4096\n" \
"v_mfma_f32_16x16x32_bf16 a[84:87], v[104:107], v[60:63], a[84:87]\n" \
"v_mfma_f32_16x16x32_bf16 a[84:87], v[168:171], v[124:127], a[84:87]\n" \
"v_mfma_f32_16x16x32_bf16 a[88:91], v[108:111], v[60:63], a[88:91]\n" \
"ds_read_b64_tr_b16 v[154:155], v33 offset:4608\n" \
"v_mfma_f32_16x16x32_bf16 a[88:91], v[172:175], v[124:127], a[88:91]\n" \
"v_mfma_f32_16x16x32_bf16 a[92:95], v[112:115], v[60:63], a[92:95]\n" \
"v_mfma_f32_16x16x32_bf16 a[92:95], v[176:179], v[124:127], a[92:95]\n" \
"ds_read_b128 v[60:63], v28 offset:4096\n" \
"v_mfma_f32_16x16x32_bf16 a[124:127], v[112:115], v[64:67], a[124:127]\n" \
"ds_read_b128 v[124:127], v29 offset:4096\n" \
"v_mfma_f32_16x16x32_bf16 a[124:127], v[176:179], v[128:131], a[124:127]\n" \
"ds_read_b64_tr_b16 v[96:97], v35 offset:0\n" \
"v_mfma_f32_16x16x32_bf16 a[120:123], v[108:111], v[64:67], a[120:123]\n" \
"ds_read_b64_tr_b16 v[98:99], v35 offset:512\n" \
"v_mfma_f32_16x16x32_bf16 a[120:123], v[172:175], v[128:131], a[120:123]\n" \
"v_mfma_f32_16x16x32_bf16 a[116:119], v[104:107], v[64:67], a[116:119]\n" \
"ds_read_b64_tr_b16 v[160:161], v35 offset:4096\n" \
"v_mfma_f32_16x16x32_bf16 a[116:119], v[168:171], v[128:131], a[116:119]\n" \
"v_mfma_f32_16x16x32_bf16 a[112:115], v[100:103], v[64:67], a[112:115]\n" \
"v_mfma_f32_16x16x32_bf16 a[112:115], v[164:167], v[128:131], a[112:115]\n" \
"ds_read_b128 v[64:67], v28 offset:6144\n" \
"v_mfma_f32_16x16x32_bf16 a[144:147], v[100:103], v[68:71], a[144:147]\n" \
"ds_read_b128 v[128:131], v29 offset:6144\n" \
"v_mfma_f32_16x16x32_bf16 a[144:147], v[164:167], v[132:135], a[144:147]\n" \
"ds_read_b64_tr_b16 v[162:163], v35 offset:4608\n" \
"v_mfma_f32_16x16x32_bf16 a[148:151], v[104:107], v[68:71], a[148:151]\n" \
"ds_read_b64_tr_b16 v[92:93], v34 offset:0\n" \
"v_mfma_f32_16x16x32_bf16 a[148:151], v[168:171], v[132:135], a[148:151]\n" \
"v_mfma_f32_16x16x32_bf16 a[152:155], v[108:111], v[68:71], a[152:155]\n" \
"v_mfma_f32_16x16x32_bf16 a[152:155], v[172:175], v[132:135], a[152:155]\n" \
"ds_read_b64_tr_b16 v[94:95], v34 offset:512\n" \
"v_mfma_f32_16x16x32_bf16 a[156:159], v[112:115], v[68:71], a[156:159]\n" \
"v_mfma_f32_16x16x32_bf16 a[156:159], v[176:179], v[132:135], a[156:159]\n" \
"ds_read_b128 v[68:71], v28 offset:8192\n" \
"v_mfma_f32_16x16x32_bf16 a[108:111], v[112:115], v[72:75], a[108:111]\n" \
"ds_read_b128 v[132:135], v29 offset:8192\n" \
"v_mfma_f32_16x16x32_bf16 a[108:111], v[176:179], v[136:139], a[108:111]\n" \
"ds_read_b64_tr_b16 v[156:157], v34 offset:4096\n" \
"v_mfma_f32_16x16x32_bf16 a[104:107], v[108:111], v[72:75], a[104:107]\n" \
"v_mfma_f32_16x16x32_bf16 a[104:107], v[172:175], v[136:139], a[104:107]\n" \
"ds_read_b64_tr_b16 v[158:159], v34 offset:4608\n" \
"v_mfma_f32_16x16x32_bf16 a[76:79], v[104:107], v[72:75], a[76:79]\n" \
"v_mfma_f32_16x16x32_bf16 a[76:79], v[168:171], v[136:139], a[76:79]\n" \
"v_mfma_f32_16x16x32_bf16 a[72:75], v[100:103], v[72:75], a[72:75]\n" \
"ds_read_b128 v[248:251], v22 offset:0\n" \
"v_mfma_f32_16x16x32_bf16 a[72:75], v[164:167], v[136:139], a[72:75]\n" \
"ds_read_b128 v[72:75], v28 offset:10240\n" \
"v_mfma_f32_16x16x32_bf16 a[136:139], v[100:103], v[76:79], a[136:139]\n" \
"ds_read_b128 v[136:139], v29 offset:10240\n" \
"v_mfma_f32_16x16x32_bf16 a[136:139], v[164:167], v[140:143], a[136:139]\n" \
"ds_read_b128 v[48:51], v23 offset:0\n" \
"v_mfma_f32_16x16x32_bf16 a[140:143], v[104:107], v[76:79], a[140:143]\n" \
"v_mfma_f32_16x16x32_bf16 a[140:143], v[168:171], v[140:143], a[140:143]\n" \
"v_mfma_f32_16x16x32_bf16 a[168:171], v[108:111], v[76:79], a[168:171]\n" \
"v_mfma_f32_16x16x32_bf16 a[168:171], v[172:175], v[140:143], a[168:171]\n" \
"v_mfma_f32_16x16x32_bf16 a[172:175], v[112:115], v[76:79], a[172:175]\n" \
"v_mfma_f32_16x16x32_bf16 a[172:175], v[176:179], v[140:143], a[172:175]\n" \
"ds_read_b128 v[76:79], v28 offset:12288\n" \
"v_mfma_f32_16x16x32_bf16 a[236:239], v[112:115], v[80:83], a[236:239]\n" \
"ds_read_b128 v[140:143], v29 offset:12288\n" \
"v_mfma_f32_16x16x32_bf16 a[236:239], v[176:179], v[144:147], a[236:239]\n" \
"v_mfma_f32_16x16x32_bf16 a[232:235], v[108:111], v[80:83], a[232:235]\n" \
"v_mfma_f32_16x16x32_bf16 a[232:235], v[172:175], v[144:147], a[232:235]\n" \
"v_mfma_f32_16x16x32_bf16 a[204:207], v[104:107], v[80:83], a[204:207]\n" \
"v_mfma_f32_16x16x32_bf16 a[204:207], v[168:171], v[144:147], a[204:207]\n" \
"v_mfma_f32_16x16x32_bf16 a[200:203], v[100:103], v[80:83], a[200:203]\n" \
"v_mfma_f32_16x16x32_bf16 a[200:203], v[164:167], v[144:147], a[200:203]\n" \
"ds_read_b128 v[80:83], v28 offset:14336\n" \
"ds_read_b128 v[144:147], v29 offset:14336\n" \
"s_waitcnt lgkmcnt(15)\n" \
"s_nop 0\n" \
"v_mfma_f32_16x16x32_bf16 a[0:3], v[84:87], v[52:55], a[0:3]\n" \
"v_bfe_u32 v44, v20, 2, 5\n" \
"ds_read_b64_tr_b16 v[100:101], v36 offset:0\n" \
"v_mfma_f32_16x16x32_bf16 a[0:3], v[148:151], v[116:119], a[0:3]\n" \
"buffer_load_dword v44, v44, s[80:83], s38 offen offset:0 sc1\n" \
"v_mfma_f32_16x16x32_bf16 a[4:7], v[88:91], v[52:55], a[4:7]\n" \
"ds_read_b64_tr_b16 v[102:103], v36 offset:1024\n" \
"v_mfma_f32_16x16x32_bf16 a[4:7], v[152:155], v[116:119], a[4:7]\n" \
"ds_read_b64_tr_b16 v[164:165], v36 offset:8192\n" \
"v_mfma_f32_16x16x32_bf16 a[36:39], v[96:99], v[52:55], a[36:39]\n" \
"ds_read_b64_tr_b16 v[166:167], v36 offset:9216\n" \
"s_nop 0\n" \
"v_accvgpr_read_b32 v184, a0\n" \
"s_waitcnt lgkmcnt(15)\n" \
"v_mfma_f32_16x16x32_bf16 a[36:39], v[160:163], v[116:119], a[36:39]\n" \
"v_accvgpr_read_b32 v185, a1\n" \
"v_accvgpr_read_b32 v186, a2\n" \
"v_mfma_f32_16x16x32_bf16 a[32:35], v[52:55], v[92:95], a[32:35]\n" \
"ds_read_b64_tr_b16 v[104:105], v37 offset:0\n" \
"v_accvgpr_read_b32 v187, a3\n" \
"s_waitcnt lgkmcnt(13)\n" \
"s_nop 0\n" \
"v_mfma_f32_16x16x32_bf16 a[32:35], v[116:119], v[156:159], a[32:35]\n" \
"v_accvgpr_read_b32 v188, a4\n" \
"v_accvgpr_read_b32 v189, a5\n" \
"v_mfma_f32_16x16x32_bf16 a[96:99], v[56:59], v[92:95], a[96:99]\n" \
"ds_read_b64_tr_b16 v[106:107], v37 offset:1024\n" \
"v_accvgpr_read_b32 v190, a6\n" \
"v_mfma_f32_16x16x32_bf16 a[96:99], v[120:123], v[156:159], a[96:99]\n" \
"ds_read_b64_tr_b16 v[168:169], v37 offset:8192\n" \
"v_accvgpr_read_b32 v191, a7\n" \
"v_mfma_f32_16x16x32_bf16 a[100:103], v[96:99], v[56:59], a[100:103]\n" \
"v_accvgpr_read_b32 v196, a36\n" \
"v_accvgpr_read_b32 v197, a37\n" \
"v_mfma_f32_16x16x32_bf16 a[100:103], v[160:163], v[120:123], a[100:103]\n" \
"ds_read_b64_tr_b16 v[170:171], v37 offset:9216\n" \
"v_accvgpr_read_b32 v198, a38\n" \
"v_mfma_f32_16x16x32_bf16 a[68:71], v[88:91], v[56:59], a[68:71]\n" \
"v_accvgpr_read_b32 v199, a39\n" \
"v_accvgpr_read_b32 v192, a32\n" \
"v_mfma_f32_16x16x32_bf16 a[68:71], v[152:155], v[120:123], a[68:71]\n" \
"ds_read_b64_tr_b16 v[108:109], v38 offset:0\n" \
"v_accvgpr_read_b32 v193, a33\n" \
"v_mfma_f32_16x16x32_bf16 a[64:67], v[84:87], v[56:59], a[64:67]\n" \
"ds_read_b64_tr_b16 v[110:111], v38 offset:1024\n" \
"v_accvgpr_read_b32 v194, a34\n" \
"v_mfma_f32_16x16x32_bf16 a[64:67], v[148:151], v[120:123], a[64:67]\n" \
"v_accvgpr_read_b32 v195, a35\n" \
"v_accvgpr_read_b32 v208, a96\n" \
"v_mfma_f32_16x16x32_bf16 a[128:131], v[84:87], v[60:63], a[128:131]\n" \
"ds_read_b64_tr_b16 v[172:173], v38 offset:8192\n" \
"v_accvgpr_read_b32 v209, a97\n" \
"v_mfma_f32_16x16x32_bf16 a[128:131], v[148:151], v[124:127], a[128:131]\n" \
"ds_read_b64_tr_b16 v[174:175], v38 offset:9216\n" \
"v_accvgpr_read_b32 v210, a98\n" \
"v_mfma_f32_16x16x32_bf16 a[132:135], v[88:91], v[60:63], a[132:135]\n" \
"v_lshlrev_b32_e32 v45, 3, v20\n" \
"v_add_u32_e32 v45, s30, v45\n" \
"v_mfma_f32_16x16x32_bf16 a[132:135], v[152:155], v[124:127], a[132:135]\n" \
"s_getpc_b64 s[86:87]\n" \
"L_ct0_%=:\n" \
"s_add_u32 s86, s86, L_end_%=-L_ct0_%=\n" \
"s_addc_u32 s87, s87, 0\n" \
"s_mov_b32 m0, s32\n" \
"s_nop 0\n" \
"v_mfma_f32_16x16x32_bf16 a[164:167], v[96:99], v[60:63], a[164:167]\n" \
"global_load_lds_dword v45, s[86:87]\n" \
"v_mfma_f32_16x16x32_bf16 a[164:167], v[160:163], v[124:127], a[164:167]\n" \
"ds_read_b64_tr_b16 v[112:113], v39 offset:0\n" \
"v_accvgpr_read_b32 v211, a99\n" \
"v_mfma_f32_16x16x32_bf16 a[160:163], v[60:63], v[92:95], a[160:163]\n" \
"ds_read_b64_tr_b16 v[114:115], v39 offset:1024\n" \
"v_accvgpr_read_b32 v212, a100\n" \
"v_mfma_f32_16x16x32_bf16 a[160:163], v[124:127], v[156:159], a[160:163]\n" \
"ds_read_b64_tr_b16 v[176:177], v39 offset:8192\n" \
"v_accvgpr_read_b32 v213, a101\n" \
"v_mfma_f32_16x16x32_bf16 a[224:227], v[64:67], v[92:95], a[224:227]\n" \
"ds_read_b64_tr_b16 v[178:179], v39 offset:9216\n" \
"v_accvgpr_read_b32 v214, a102\n" \
"v_mfma_f32_16x16x32_bf16 a[224:227], v[128:131], v[156:159], a[224:227]\n" \
"v_accvgpr_read_b32 v215, a103\n" \
"v_accvgpr_read_b32 v204, a68\n" \
"v_mfma_f32_16x16x32_bf16 a[228:231], v[96:99], v[64:67], a[228:231]\n" \
"v_accvgpr_read_b32 v205, a69\n" \
"v_accvgpr_read_b32 v206, a70\n" \
"v_mfma_f32_16x16x32_bf16 a[228:231], v[160:163], v[128:131], a[228:231]\n" \
"v_accvgpr_read_b32 v207, a71\n" \
"v_accvgpr_read_b32 v200, a64\n" \
"v_mfma_f32_16x16x32_bf16 a[196:199], v[88:91], v[64:67], a[196:199]\n" \
"v_accvgpr_read_b32 v201, a65\n" \
"v_accvgpr_read_b32 v202, a66\n" \
"v_mfma_f32_16x16x32_bf16 a[196:199], v[152:155], v[128:131], a[196:199]\n" \
"v_accvgpr_read_b32 v203, a67\n" \
"v_accvgpr_read_b32 v216, a128\n" \
"v_mfma_f32_16x16x32_bf16 a[192:195], v[84:87], v[64:67], a[192:195]\n" \
"v_accvgpr_read_b32 v217, a129\n" \
"v_accvgpr_read_b32 v218, a130\n" \
"v_mfma_f32_16x16x32_bf16 a[192:195], v[148:151], v[128:131], a[192:195]\n" \
"v_accvgpr_read_b32 v219, a131\n" \
"v_accvgpr_read_b32 v220, a132\n" \
"s_waitcnt lgkmcnt(15)\n" \
"s_nop 0\n" \
"v_mfma_f32_16x16x32_bf16 a[8:11], v[88:91], v[248:251], a[8:11]\n" \
"v_accvgpr_read_b32 v221, a133\n" \
"v_accvgpr_read_b32 v222, a134\n" \
"v_mfma_f32_16x16x32_bf16 a[8:11], v[152:155], v[48:51], a[8:11]\n" \
"v_accvgpr_read_b32 v223, a135\n" \
"v_accvgpr_read_b32 v228, a164\n" \
"v_mfma_f32_16x16x32_bf16 a[40:43], v[96:99], v[248:251], a[40:43]\n" \
"v_accvgpr_read_b32 v229, a165\n" \
"v_accvgpr_read_b32 v230, a166\n" \
"v_mfma_f32_16x16x32_bf16 a[40:43], v[160:163], v[48:51], a[40:43]\n" \
"v_accvgpr_read_b32 v231, a167\n" \
"v_accvgpr_read_b32 v224, a160\n" \
"v_mfma_f32_16x16x32_bf16 a[12:15], v[248:251], v[92:95], a[12:15]\n" \
"v_accvgpr_read_b32 v225, a161\n" \
"v_accvgpr_read_b32 v226, a162\n" \
"v_mfma_f32_16x16x32_bf16 a[12:15], v[48:51], v[156:159], a[12:15]\n" \
"v_accvgpr_read_b32 v227, a163\n" \
"v_accvgpr_read_b32 v240, a224\n" \
"s_waitcnt vmcnt(1) lgkmcnt(0)\n" \
"s_barrier\n" \
"v_mov_b32_e32 v45, s62\n" \
"s_mov_b64 exec, 1\n" \
"buffer_store_dword v45, off, s[80:83], s37 offset:32 sc1\n" \
"s_mov_b64 exec, -1\n" \
"v_cmp_ne_u32_e32 vcc, s62, v44\n" \
"s_nop 4\n" \
"s_cbranch_vccnz L_poll160_%=\n" \
"L_pollr160_%=:\n" \
"v_mfma_f32_16x16x32_bf16 a[16:19], v[100:103], v[52:55], a[16:19]\n" \
"s_mov_b32 s73, s50\n" \
"s_sub_u32 m0, s31, 0x10000\n" \
"v_mfma_f32_16x16x32_bf16 a[16:19], v[164:167], v[116:119], a[16:19]\n" \
"buffer_load_dwordx4 v20, s[80:83], s73 offen offset:0 sc1 lds\n" \
"v_mfma_f32_16x16x32_bf16 a[20:23], v[104:107], v[52:55], a[20:23]\n" \
"s_add_u32 s74, s50, 0x4000\n" \
"s_add_u32 s75, s50, 0x8000\n" \
"v_mfma_f32_16x16x32_bf16 a[20:23], v[168:171], v[116:119], a[20:23]\n" \
"s_mov_b32 s70, 0x3e0293ee\n" \
"s_mov_b32 s76, s52\n" \
"v_mfma_f32_16x16x32_bf16 a[24:27], v[108:111], v[52:55], a[24:27]\n" \
"v_accvgpr_read_b32 v32, a16\n" \
"v_accvgpr_read_b32 v33, a17\n" \
"v_mfma_f32_16x16x32_bf16 a[24:27], v[172:175], v[116:119], a[24:27]\n" \
"buffer_load_dwordx4 v20, s[80:83], s73 offen offset:1024 sc1 lds\n" \
"v_mfma_f32_16x16x32_bf16 a[28:31], v[112:115], v[52:55], a[28:31]\n" \
"v_accvgpr_read_b32 v34, a18\n" \
"v_accvgpr_read_b32 v35, a19\n" \
"v_mfma_f32_16x16x32_bf16 a[28:31], v[176:179], v[116:119], a[28:31]\n" \
"v_mul_f32_e32 v32, s70, v32\n" \
"v_mul_f32_e32 v33, s70, v33\n" \
"v_mfma_f32_16x16x32_bf16 a[60:63], v[112:115], v[56:59], a[60:63]\n" \
"v_accvgpr_read_b32 v36, a20\n" \
"v_accvgpr_read_b32 v37, a21\n" \
"v_mfma_f32_16x16x32_bf16 a[60:63], v[176:179], v[120:123], a[60:63]\n" \
"buffer_load_dwordx4 v20, s[80:83], s73 offen offset:2048 sc1 lds\n" \
"v_mfma_f32_16x16x32_bf16 a[56:59], v[108:111], v[56:59], a[56:59]\n" \
"v_accvgpr_read_b32 v38, a22\n" \
"v_accvgpr_read_b32 v39, a23\n" \
"v_mfma_f32_16x16x32_bf16 a[56:59], v[172:175], v[120:123], a[56:59]\n" \
"v_mul_f32_e32 v34, s70, v34\n" \
"v_mul_f32_e32 v35, s70, v35\n" \
"v_mfma_f32_16x16x32_bf16 a[52:55], v[104:107], v[56:59], a[52:55]\n" \
"v_cvt_pk_bf16_f32 v32, v32, v33\n" \
"v_mul_f32_e64 v36, s70, v36\n" \
"v_mfma_f32_16x16x32_bf16 a[52:55], v[168:171], v[120:123], a[52:55]\n" \
"buffer_load_dwordx4 v20, s[80:83], s73 offen offset:3072 sc1 lds\n" \
"v_mfma_f32_16x16x32_bf16 a[48:51], v[100:103], v[56:59], a[48:51]\n" \
"s_sub_u32 m0, s31, s51\n" \
"v_mul_f32_e32 v37, s70, v37\n" \
"v_mfma_f32_16x16x32_bf16 a[48:51], v[164:167], v[120:123], a[48:51]\n" \
"v_mul_f32_e32 v38, s70, v38\n" \
"v_mul_f32_e32 v39, s70, v39\n" \
"v_mfma_f32_16x16x32_bf16 a[80:83], v[100:103], v[60:63], a[80:83]\n" \
"v_cvt_pk_bf16_f32 v33, v34, v35\n" \
"v_cvt_pk_bf16_f32 v34, v36, v37\n" \
"v_mfma_f32_16x16x32_bf16 a[80:83], v[164:167], v[124:127], a[80:83]\n" \
"buffer_load_dwordx4 v20, s[80:83], s74 offen offset:0 sc1 lds\n" \
"v_mfma_f32_16x16x32_bf16 a[84:87], v[104:107], v[60:63], a[84:87]\n" \
"v_cvt_pk_bf16_f32 v35, v38, v39\n" \
"v_accvgpr_read_b32 v36, a28\n" \
"v_mfma_f32_16x16x32_bf16 a[84:87], v[168:171], v[124:127], a[84:87]\n" \
"ds_write_b128 v27, v[32:35] offset:0\n" \
"v_mfma_f32_16x16x32_bf16 a[88:91], v[108:111], v[60:63], a[88:91]\n" \
"v_accvgpr_read_b32 v32, a24\n" \
"v_accvgpr_read_b32 v33, a25\n" \
"v_mfma_f32_16x16x32_bf16 a[88:91], v[172:175], v[124:127], a[88:91]\n" \
"buffer_load_dwordx4 v20, s[80:83], s74 offen offset:1024 sc1 lds\n" \
"v_mfma_f32_16x16x32_bf16 a[92:95], v[112:115], v[60:63], a[92:95]\n" \
"v_accvgpr_read_b32 v34, a26\n" \
"v_accvgpr_read_b32 v35, a27\n" \
"v_mfma_f32_16x16x32_bf16 a[92:95], v[176:179], v[124:127], a[92:95]\n" \
"v_mul_f32_e32 v32, s70, v32\n" \
"v_mul_f32_e32 v33, s70, v33\n" \
"v_mfma_f32_16x16x32_bf16 a[124:127], v[112:115], v[64:67], a[124:127]\n" \
"v_accvgpr_read_b32 v37, a29\n" \
"v_accvgpr_read_b32 v38, a30\n" \
"v_mfma_f32_16x16x32_bf16 a[124:127], v[176:179], v[128:131], a[124:127]\n" \
"buffer_load_dwordx4 v20, s[80:83], s74 offen offset:2048 sc1 lds\n" \
"v_mfma_f32_16x16x32_bf16 a[120:123], v[108:111], v[64:67], a[120:123]\n" \
"v_accvgpr_read_b32 v39, a31\n" \
"v_mul_f32_e64 v34, s70, v34\n" \
"v_mfma_f32_16x16x32_bf16 a[120:123], v[172:175], v[128:131], a[120:123]\n" \
"v_mul_f32_e64 v35, s70, v35\n" \
"v_cvt_pk_bf16_f32 v32, v32, v33\n" \
"v_mfma_f32_16x16x32_bf16 a[116:119], v[104:107], v[64:67], a[116:119]\n" \
"v_accvgpr_read_b32 v180, a8\n" \
"v_accvgpr_read_b32 v181, a9\n" \
"v_mfma_f32_16x16x32_bf16 a[116:119], v[168:171], v[128:131], a[116:119]\n" \
"buffer_load_dwordx4 v20, s[80:83], s74 offen offset:3072 sc1 lds\n" \
"v_mfma_f32_16x16x32_bf16 a[112:115], v[100:103], v[64:67], a[112:115]\n" \
"s_sub_u32 m0, s31, 0x4000\n" \
"v_mul_f32_e64 v36, s70, v36\n" \
"v_mfma_f32_16x16x32_bf16 a[112:115], v[164:167], v[128:131], a[112:115]\n" \
"v_mul_f32_e32 v37, s70, v37\n" \
"v_mul_f32_e32 v38, s70, v38\n" \
"v_mfma_f32_16x16x32_bf16 a[144:147], v[100:103], v[68:71], a[144:147]\n" \
"v_mul_f32_e64 v39, s70, v39\n" \
"v_cvt_pk_bf16_f32 v33, v34, v35\n" \
"v_mfma_f32_16x16x32_bf16 a[144:147], v[164:167], v[132:135], a[144:147]\n" \
"buffer_load_dwordx4 v20, s[80:83], s75 offen offset:0 sc1 lds\n" \
"v_mfma_f32_16x16x32_bf16 a[148:151], v[104:107], v[68:71], a[148:151]\n" \
"v_accvgpr_read_b32 v236, a196\n" \
"v_accvgpr_read_b32 v237, a197\n" \
"v_mfma_f32_16x16x32_bf16 a[148:151], v[168:171], v[132:135], a[148:151]\n" \
"v_accvgpr_read_b32 v232, a192\n" \
"v_accvgpr_read_b32 v233, a193\n" \
"v_mfma_f32_16x16x32_bf16 a[152:155], v[108:111], v[68:71], a[152:155]\n" \
"v_accvgpr_read_b32 v182, a10\n" \
"v_accvgpr_read_b32 v183, a11\n" \
"v_mfma_f32_16x16x32_bf16 a[152:155], v[172:175], v[132:135], a[152:155]\n" \
"buffer_load_dwordx4 v20, s[80:83], s75 offen offset:1024 sc1 lds\n" \
"v_mfma_f32_16x16x32_bf16 a[156:159], v[112:115], v[68:71], a[156:159]\n" \
"v_cvt_pk_bf16_f32 v180, v180, v181\n" \
"v_cvt_pk_bf16_f32 v184, v184, v185\n" \
"v_mfma_f32_16x16x32_bf16 a[156:159], v[176:179], v[132:135], a[156:159]\n" \
"v_cvt_pk_bf16_f32 v34, v36, v37\n" \
"v_cvt_pk_bf16_f32 v35, v38, v39\n" \
"v_mfma_f32_16x16x32_bf16 a[108:111], v[112:115], v[72:75], a[108:111]\n" \
"v_accvgpr_read_b32 v238, a198\n" \
"v_accvgpr_read_b32 v239, a199\n" \
"v_mfma_f32_16x16x32_bf16 a[108:111], v[176:179], v[136:139], a[108:111]\n" \
"buffer_load_dwordx4 v20, s[80:83], s75 offen offset:2048 sc1 lds\n" \
"v_mfma_f32_16x16x32_bf16 a[104:107], v[108:111], v[72:75], a[104:107]\n" \
"v_accvgpr_read_b32 v234, a194\n" \
"v_accvgpr_read_b32 v235, a195\n" \
"v_mfma_f32_16x16x32_bf16 a[104:107], v[172:175], v[136:139], a[104:107]\n" \
"v_accvgpr_read_b32 v50, a42\n" \
"v_accvgpr_read_b32 v51, a43\n" \
"v_mfma_f32_16x16x32_bf16 a[76:79], v[104:107], v[72:75], a[76:79]\n" \
"v_cvt_pk_bf16_f32 v181, v182, v183\n" \
"v_cvt_pk_bf16_f32 v188, v188, v189\n" \
"v_mfma_f32_16x16x32_bf16 a[76:79], v[168:171], v[136:139], a[76:79]\n" \
"buffer_load_dwordx4 v20, s[80:83], s75 offen offset:3072 sc1 lds\n" \
"v_mfma_f32_16x16x32_bf16 a[72:75], v[100:103], v[72:75], a[72:75]\n" \
"s_mov_b32 m0, s53\n" \
"s_nop 0\n" \
"v_cvt_pk_bf16_f32 v185, v186, v187\n" \
"v_mfma_f32_16x16x32_bf16 a[72:75], v[164:167], v[136:139], a[72:75]\n" \
"v_cvt_pk_bf16_f32 v204, v204, v205\n" \
"v_cvt_pk_bf16_f32 v200, v200, v201\n" \
"v_mfma_f32_16x16x32_bf16 a[136:139], v[100:103], v[76:79], a[136:139]\n" \
"v_cvt_pk_bf16_f32 v220, v220, v221\n" \
"v_cvt_pk_bf16_f32 v216, v216, v217\n" \
"v_mfma_f32_16x16x32_bf16 a[136:139], v[164:167], v[140:143], a[136:139]\n" \
"buffer_load_dwordx4 v20, s[80:83], s76 offen offset:0 sc1 lds\n" \
"v_mfma_f32_16x16x32_bf16 a[140:143], v[104:107], v[76:79], a[140:143]\n" \
"v_cvt_pk_bf16_f32 v236, v236, v237\n" \
"v_cvt_pk_bf16_f32 v232, v232, v233\n" \
"v_mfma_f32_16x16x32_bf16 a[140:143], v[168:171], v[140:143], a[140:143]\n" \
"ds_write_b128 v27, v[32:35] offset:1024\n" \
"v_accvgpr_read_b32 v241, a225\n" \
"v_accvgpr_read_b32 v242, a226\n" \
"v_accvgpr_read_b32 v243, a227\n" \
"v_accvgpr_read_b32 v244, a228\n" \
"v_accvgpr_read_b32 v245, a229\n" \
"buffer_load_dwordx4 v20, s[80:83], s76 offen offset:1024 sc1 lds\n" \
"v_accvgpr_read_b32 v246, a230\n" \
"v_accvgpr_read_b32 v247, a231\n" \
"v_cvt_pk_bf16_f32 v182, v50, v51\n" \
"v_mov_b32_e32 v183, 0x3f80\n" \
"v_cvt_pk_bf16_f32 v189, v190, v191\n" \
"v_cvt_pk_bf16_f32 v186, v196, v197\n" \
"v_mov_b32_e64 v187, 0\n" \
"v_cvt_pk_bf16_f32 v205, v206, v207\n" \
"v_cvt_pk_bf16_f32 v201, v202, v203\n" \
"v_cvt_pk_bf16_f32 v221, v222, v223\n" \
"v_cvt_pk_bf16_f32 v217, v218, v219\n" \
"v_cvt_pk_bf16_f32 v237, v238, v239\n" \
"v_cvt_pk_bf16_f32 v233, v234, v235\n" \
"v_cvt_pk_bf16_f32 v190, v198, v199\n" \
"v_mov_b32_e32 v191, 0x3f80\n" \
"v_cvt_pk_bf16_f32 v86, v192, v193\n" \
"v_cvt_pk_bf16_f32 v87, v194, v195\n" \
"v_cvt_pk_bf16_f32 v206, v214, v215\n" \
"v_mov_b32_e32 v207, 0x3f80\n" \
"v_cvt_pk_bf16_f32 v202, v212, v213\n" \
"v_mov_b32_e64 v203, 0\n" \
"v_cvt_pk_bf16_f32 v88, v208, v209\n" \
"v_cvt_pk_bf16_f32 v89, v210, v211\n" \
"v_cvt_pk_bf16_f32 v222, v230, v231\n" \
"v_mov_b32_e32 v223, 0x3f80\n" \
"v_cvt_pk_bf16_f32 v218, v228, v229\n" \
"v_mov_b32_e64 v219, 0\n" \
"v_cvt_pk_bf16_f32 v90, v224, v225\n" \
"v_cvt_pk_bf16_f32 v91, v226, v227\n" \
"v_cvt_pk_bf16_f32 v238, v246, v247\n" \
"v_mov_b32_e32 v239, 0x3f80\n" \
"v_cvt_pk_bf16_f32 v234, v244, v245\n" \
"v_mov_b32_e64 v235, 0\n" \
"v_cvt_pk_bf16_f32 v92, v240, v241\n" \
"v_cvt_pk_bf16_f32 v93, v242, v243\n" \
"v_mfma_f32_16x16x32_bf16 v[148:151], v[180:183], v[184:187], 0\n" \
"v_accvgpr_read_b32 v116, a48\n" \
"v_accvgpr_read_b32 v117, a49\n" \
"v_accvgpr_read_b32 v32, a56\n" \
"v_accvgpr_read_b32 v33, a57\n" \
"v_mfma_f32_16x16x32_bf16 a[168:171], v[108:111], v[76:79], a[168:171]\n" \
"v_mfma_f32_16x16x32_bf16 v[192:195], v[188:191], v[184:187], 0\n" \
"v_mfma_f32_16x16x32_bf16 v[208:211], v[204:207], v[200:203], 0\n" \
"v_mfma_f32_16x16x32_bf16 v[152:155], v[188:191], v[200:203], 0\n" \
"v_mfma_f32_16x16x32_bf16 v[224:227], v[220:223], v[216:219], 0\n" \
"v_mfma_f32_16x16x32_bf16 v[156:159], v[204:207], v[216:219], 0\n" \
"v_mfma_f32_16x16x32_bf16 v[240:243], v[236:239], v[232:235], 0\n" \
"v_mfma_f32_16x16x32_bf16 v[160:163], v[220:223], v[232:235], 0\n" \
"v_accvgpr_read_b32 v118, a50\n" \
"v_accvgpr_read_b32 v119, a51\n" \
"v_mul_f32_e32 v116, s70, v116\n" \
"v_mul_f32_e32 v117, s70, v117\n" \
"v_accvgpr_read_b32 v34, a58\n" \
"v_accvgpr_read_b32 v35, a59\n" \
"v_mul_f32_e32 v32, s70, v32\n" \
"v_mul_f32_e32 v33, s70, v33\n" \
"v_accvgpr_read_b32 v120, a52\n" \
"v_accvgpr_read_b32 v121, a53\n" \
"v_accvgpr_read_b32 v122, a54\n" \
"v_accvgpr_read_b32 v123, a55\n" \
"v_mul_f32_e32 v118, s70, v118\n" \
"v_mul_f32_e32 v119, s70, v119\n" \
"v_cvt_pk_bf16_f32 v116, v116, v117\n" \
"v_accvgpr_read_b32 v36, a60\n" \
"v_accvgpr_read_b32 v37, a61\n" \
"v_accvgpr_read_b32 v38, a62\n" \
"v_accvgpr_read_b32 v39, a63\n" \
"v_mul_f32_e32 v34, s70, v34\n" \
"v_mul_f32_e32 v35, s70, v35\n" \
"v_cvt_pk_bf16_f32 v32, v32, v33\n" \
"v_mul_f32_e32 v120, s70, v120\n" \
"v_mul_f32_e32 v121, s70, v121\n" \
"v_mul_f32_e32 v122, s70, v122\n" \
"v_mul_f32_e32 v123, s70, v123\n" \
"v_cvt_pk_bf16_f32 v117, v118, v119\n" \
"v_mul_f32_e32 v36, s70, v36\n" \
"v_mul_f32_e32 v37, s70, v37\n" \
"v_mul_f32_e32 v38, s70, v38\n" \
"v_mul_f32_e32 v39, s70, v39\n" \
"v_cvt_pk_bf16_f32 v33, v34, v35\n" \
"v_cvt_pk_bf16_f32 v118, v120, v121\n" \
"v_cvt_pk_bf16_f32 v119, v122, v123\n" \
"v_cvt_pk_bf16_f32 v34, v36, v37\n" \
"v_cvt_pk_bf16_f32 v35, v38, v39\n" \
"v_mfma_f32_16x16x32_bf16 a[168:171], v[172:175], v[140:143], a[168:171]\n" \
"ds_write_b128 v27, v[116:119] offset:4096\n" \
"ds_write_b128 v27, v[32:35] offset:5120\n" \
"v_accvgpr_read_b32 v116, a80\n" \
"v_accvgpr_read_b32 v117, a81\n" \
"v_accvgpr_read_b32 v32, a88\n" \
"v_accvgpr_read_b32 v33, a89\n" \
"v_accvgpr_read_b32 v124, a112\n" \
"v_accvgpr_read_b32 v125, a113\n" \
"v_accvgpr_read_b32 v118, a82\n" \
"v_accvgpr_read_b32 v119, a83\n" \
"v_mul_f32_e32 v116, s70, v116\n" \
"v_mul_f32_e32 v117, s70, v117\n" \
"v_accvgpr_read_b32 v34, a90\n" \
"v_accvgpr_read_b32 v35, a91\n" \
"v_mul_f32_e32 v32, s70, v32\n" \
"v_mul_f32_e32 v33, s70, v33\n" \
"v_accvgpr_read_b32 v126, a114\n" \
"v_accvgpr_read_b32 v127, a115\n" \
"v_mul_f32_e32 v124, s70, v124\n" \
"v_mul_f32_e32 v125, s70, v125\n" \
"v_mfma_f32_16x16x32_bf16 a[172:175], v[112:115], v[76:79], a[172:175]\n" \
"s_mov_b32 s64, 0xaaaaaaaa\n" \
"s_mov_b32 s65, 0xaaaaaaaa\n" \
"v_accvgpr_read_b32 v120, a84\n" \
"v_accvgpr_read_b32 v121, a85\n" \
"v_accvgpr_read_b32 v122, a86\n" \
"v_accvgpr_read_b32 v123, a87\n" \
"v_mul_f32_e32 v118, s70, v118\n" \
"v_mul_f32_e32 v119, s70, v119\n" \
"v_cvt_pk_bf16_f32 v116, v116, v117\n" \
"v_accvgpr_read_b32 v36, a92\n" \
"v_accvgpr_read_b32 v37, a93\n" \
"v_accvgpr_read_b32 v38, a94\n" \
"v_accvgpr_read_b32 v39, a95\n" \
"v_mul_f32_e32 v34, s70, v34\n" \
"v_mul_f32_e32 v35, s70, v35\n" \
"v_cvt_pk_bf16_f32 v32, v32, v33\n" \
"v_accvgpr_read_b32 v128, a116\n" \
"v_accvgpr_read_b32 v129, a117\n" \
"v_accvgpr_read_b32 v130, a118\n" \
"v_accvgpr_read_b32 v131, a119\n" \
"v_mul_f32_e32 v126, s70, v126\n" \
"v_mul_f32_e32 v127, s70, v127\n" \
"v_cvt_pk_bf16_f32 v124, v124, v125\n" \
"v_cndmask_b32_e64 v148, v148, v149, s[64:65]\n" \
"v_cndmask_b32_e64 v150, v150, v151, s[64:65]\n" \
"v_mul_f32_e32 v120, s70, v120\n" \
"v_mul_f32_e32 v121, s70, v121\n" \
"v_mul_f32_e32 v122, s70, v122\n" \
"v_mul_f32_e32 v123, s70, v123\n" \
"v_cvt_pk_bf16_f32 v117, v118, v119\n" \
"v_mul_f32_e32 v36, s70, v36\n" \
"v_mul_f32_e32 v37, s70, v37\n" \
"v_mul_f32_e32 v38, s70, v38\n" \
"v_mul_f32_e32 v39, s70, v39\n" \
"v_cvt_pk_bf16_f32 v33, v34, v35\n" \
"v_mul_f32_e32 v128, s70, v128\n" \
"v_mul_f32_e32 v129, s70, v129\n" \
"v_mul_f32_e32 v130, s70, v130\n" \
"v_mul_f32_e32 v131, s70, v131\n" \
"v_cvt_pk_bf16_f32 v125, v126, v127\n" \
"s_mov_b32 s66, 0xfff0ffff\n" \
"s_mov_b32 s67, 0xf000ff00\n" \
"s_mov_b32 s68, 0xffc0fffc\n" \
"s_mov_b32 s69, 0xc000fc00\n" \
"v_cndmask_b32_e64 v192, v192, v193, s[64:65]\n" \
"v_add_f32_e64 v148, s47, v148\n" \
"v_cndmask_b32_e64 v194, v194, v195, s[64:65]\n" \
"v_add_f32_e64 v150, s47, v150\n" \
"v_cndmask_b32_e64 v208, v208, v209, s[64:65]\n" \
"v_cndmask_b32_e64 v152, v152, v153, s[64:65]\n" \
"v_cndmask_b32_e64 v210, v210, v211, s[64:65]\n" \
"v_cndmask_b32_e64 v154, v154, v155, s[64:65]\n" \
"v_cndmask_b32_e64 v224, v224, v225, s[64:65]\n" \
"v_cndmask_b32_e64 v156, v156, v157, s[64:65]\n" \
"v_cndmask_b32_e64 v226, v226, v227, s[64:65]\n" \
"v_cndmask_b32_e64 v158, v158, v159, s[64:65]\n" \
"v_cndmask_b32_e64 v240, v240, v241, s[64:65]\n" \
"v_cndmask_b32_e64 v160, v160, v161, s[64:65]\n" \
"v_cndmask_b32_e64 v242, v242, v243, s[64:65]\n" \
"v_cndmask_b32_e64 v162, v162, v163, s[64:65]\n" \
"v_cvt_pk_bf16_f32 v118, v120, v121\n" \
"v_cvt_pk_bf16_f32 v119, v122, v123\n" \
"v_cvt_pk_bf16_f32 v34, v36, v37\n" \
"v_cvt_pk_bf16_f32 v35, v38, v39\n" \
"v_cvt_pk_bf16_f32 v126, v128, v129\n" \
"v_cvt_pk_bf16_f32 v127, v130, v131\n" \
"v_cndmask_b32_e64 v192, v148, v192, s[66:67]\n" \
"v_cndmask_b32_e64 v194, v150, v194, s[68:69]\n" \
"v_cndmask_b32_e64 v208, v152, v208, s[66:67]\n" \
"v_cndmask_b32_e64 v210, v154, v210, s[68:69]\n" \
"v_cndmask_b32_e64 v224, v156, v224, s[66:67]\n" \
"v_cndmask_b32_e64 v226, v158, v226, s[68:69]\n" \
"v_cndmask_b32_e64 v240, v160, v240, s[66:67]\n" \
"v_cndmask_b32_e64 v242, v162, v242, s[68:69]\n" \
"ds_write_b128 v27, v[116:119] offset:8192\n" \
"ds_write_b128 v27, v[32:35] offset:9216\n" \
"ds_write_b128 v27, v[124:127] offset:12288\n" \
"v_mfma_f32_16x16x32_bf16 a[172:175], v[176:179], v[140:143], a[172:175]\n" \
"v_max_f32_e32 v193, v192, v194\n" \
"v_max_f32_e32 v209, v208, v210\n" \
"v_max_f32_e32 v225, v224, v226\n" \
"v_max_f32_e32 v241, v240, v242\n" \
"s_mov_b32 s71, 0x3e0293ee\n" \
"v_permlane32_swap_b32_e32 v193, v225\n" \
"v_permlane32_swap_b32_e32 v209, v241\n" \
"v_accvgpr_read_b32 v116, a120\n" \
"v_accvgpr_read_b32 v117, a121\n" \
"v_accvgpr_read_b32 v32, a144\n" \
"v_accvgpr_read_b32 v33, a145\n" \
"v_accvgpr_read_b32 v124, a152\n" \
"v_accvgpr_read_b32 v125, a153\n" \
"v_accvgpr_read_b32 v132, a72\n" \
"v_accvgpr_read_b32 v133, a73\n" \
"v_permlane32_swap_b32_e32 v196, v228\n" \
"v_permlane32_swap_b32_e32 v212, v244\n" \
"v_max_f32_e32 v193, v193, v225\n" \
"v_max_f32_e32 v209, v209, v241\n" \
"v_accvgpr_read_b32 v118, a122\n" \
"v_accvgpr_read_b32 v119, a123\n" \
"v_mul_f32_e32 v116, s70, v116\n" \
"v_mul_f32_e32 v117, s70, v117\n" \
"v_accvgpr_read_b32 v34, a146\n" \
"v_accvgpr_read_b32 v35, a147\n" \
"v_pk_mul_f32 v[32:33], v[32:33], s[70:71]\n" \
"v_accvgpr_read_b32 v126, a154\n" \
"v_accvgpr_read_b32 v127, a155\n" \
"v_pk_mul_f32 v[124:125], v[124:125], s[70:71]\n" \
"v_accvgpr_read_b32 v134, a74\n" \
"v_accvgpr_read_b32 v135, a75\n" \
"v_pk_mul_f32 v[132:133], v[132:133], s[70:71]\n" \
"v_accvgpr_read_b32 v48, a40\n" \
"v_accvgpr_read_b32 v49, a41\n" \
"v_permlane16_swap_b32_e32 v228, v244\n" \
"v_permlane16_swap_b32_e32 v193, v209\n" \
"v_accvgpr_read_b32 v120, a124\n" \
"v_accvgpr_read_b32 v121, a125\n" \
"v_accvgpr_read_b32 v122, a126\n" \
"v_accvgpr_read_b32 v123, a127\n" \
"v_mul_f32_e32 v118, s70, v118\n" \
"v_mul_f32_e32 v119, s70, v119\n" \
"v_cvt_pk_bf16_f32 v116, v116, v117\n" \
"v_accvgpr_read_b32 v36, a148\n" \
"v_accvgpr_read_b32 v37, a149\n" \
"v_accvgpr_read_b32 v38, a150\n" \
"v_accvgpr_read_b32 v39, a151\n" \
"v_pk_mul_f32 v[34:35], v[34:35], s[70:71]\n" \
"v_cvt_pk_bf16_f32 v32, v32, v33\n" \
"v_accvgpr_read_b32 v128, a156\n" \
"v_accvgpr_read_b32 v129, a157\n" \
"v_accvgpr_read_b32 v130, a158\n" \
"v_accvgpr_read_b32 v131, a159\n" \
"v_pk_mul_f32 v[126:127], v[126:127], s[70:71]\n" \
"v_cvt_pk_bf16_f32 v124, v124, v125\n" \
"v_accvgpr_read_b32 v136, a76\n" \
"v_accvgpr_read_b32 v137, a77\n" \
"v_accvgpr_read_b32 v138, a78\n" \
"v_accvgpr_read_b32 v139, a79\n" \
"v_pk_mul_f32 v[134:135], v[134:135], s[70:71]\n" \
"v_cvt_pk_bf16_f32 v132, v132, v133\n" \
"v_mfma_f32_16x16x32_bf16 a[236:239], v[112:115], v[80:83], a[236:239]\n" \
"s_mov_b32 s72, 0x3eae1a93\n" \
"v_mul_f32_e32 v45, 0xbfb8aa3b, v228\n" \
"v_mfma_f32_16x16x16_bf16 v[48:51], v[182:183], v[46:47], 0\n" \
"v_mfma_f32_16x16x16_bf16 v[196:199], v[190:191], v[46:47], 0\n" \
"v_mfma_f32_16x16x16_bf16 v[212:215], v[206:207], v[46:47], 0\n" \
"v_max_f32_e32 v193, v193, v209\n" \
"v_mul_f32_e32 v120, s70, v120\n" \
"v_mul_f32_e32 v121, s70, v121\n" \
"v_mul_f32_e32 v122, s70, v122\n" \
"v_mul_f32_e64 v123, s70, v123\n" \
"v_cvt_pk_bf16_f32 v117, v118, v119\n" \
"v_pk_mul_f32 v[36:37], v[36:37], s[70:71]\n" \
"v_pk_mul_f32 v[38:39], v[38:39], s[70:71]\n" \
"v_cvt_pk_bf16_f32 v33, v34, v35\n" \
"v_pk_mul_f32 v[128:129], v[128:129], s[70:71]\n" \
"v_pk_mul_f32 v[130:131], v[130:131], s[70:71]\n" \
"v_cvt_pk_bf16_f32 v125, v126, v127\n" \
"v_pk_mul_f32 v[136:137], v[136:137], s[70:71]\n" \
"v_pk_mul_f32 v[138:139], v[138:139], s[70:71]\n" \
"v_cvt_pk_bf16_f32 v133, v134, v135\n" \
"v_mfma_f32_16x16x16_bf16 v[228:231], v[222:223], v[46:47], 0\n" \
"v_mul_f32_e64 v193, s72, v193\n" \
"v_cvt_pk_bf16_f32 v118, v120, v121\n" \
"v_cvt_pk_bf16_f32 v119, v122, v123\n" \
"v_cvt_pk_bf16_f32 v34, v36, v37\n" \
"v_cvt_pk_bf16_f32 v35, v38, v39\n" \
"v_cvt_pk_bf16_f32 v126, v128, v129\n" \
"v_cvt_pk_bf16_f32 v127, v130, v131\n" \
"v_cvt_pk_bf16_f32 v134, v136, v137\n" \
"v_cvt_pk_bf16_f32 v135, v138, v139\n" \
"v_mfma_f32_16x16x16_bf16 v[244:247], v[238:239], v[46:47], 0\n" \
"v_mov_b32_e64 v209, v193\n" \
"v_accvgpr_read_b32 v68, a104\n" \
"ds_write_b128 v27, v[116:119] offset:13312\n" \
"ds_write_b128 v21, v[32:35] offset:16384\n" \
"ds_write_b128 v21, v[124:127] offset:17408\n" \
"ds_write_b128 v21, v[132:135] offset:20480\n" \
"v_permlane16_swap_b32_e32 v193, v209\n" \
"v_mfma_f32_16x16x32_bf16 a[236:239], v[176:179], v[144:147], a[236:239]\n" \
"v_mov_b32_e32 v225, v193\n" \
"v_mov_b32_e64 v241, v209\n" \
"v_accvgpr_read_b32 v69, a105\n" \
"v_accvgpr_read_b32 v116, a136\n" \
"v_accvgpr_read_b32 v117, a137\n" \
"v_accvgpr_read_b32 v32, a168\n" \
"v_accvgpr_read_b32 v33, a169\n" \
"v_permlane32_swap_b32_e32 v193, v225\n" \
"v_permlane32_swap_b32_e32 v209, v241\n" \
"v_accvgpr_read_b32 v70, a106\n" \
"v_accvgpr_read_b32 v71, a107\n" \
"v_pk_mul_f32 v[68:69], v[68:69], s[70:71]\n" \
"v_accvgpr_read_b32 v118, a138\n" \
"v_accvgpr_read_b32 v119, a139\n" \
"v_pk_mul_f32 v[116:117], v[116:117], s[70:71]\n" \
"v_accvgpr_read_b32 v34, a170\n" \
"v_accvgpr_read_b32 v35, a171\n" \
"v_pk_mul_f32 v[32:33], v[32:33], s[70:71]\n" \
"v_fma_f32 v192, v192, s72, -v193\n" \
"v_fma_f32 v194, v194, s72, -v193\n" \
"v_fma_f32 v208, v208, s72, -v209\n" \
"v_fma_f32 v210, v210, s72, -v209\n" \
"v_accvgpr_read_b32 v72, a108\n" \
"v_accvgpr_read_b32 v73, a109\n" \
"v_accvgpr_read_b32 v74, a110\n" \
"v_accvgpr_read_b32 v75, a111\n" \
"v_pk_mul_f32 v[70:71], v[70:71], s[70:71]\n" \
"v_cvt_pk_bf16_f32 v68, v68, v69\n" \
"v_accvgpr_read_b32 v120, a140\n" \
"v_accvgpr_read_b32 v121, a141\n" \
"v_accvgpr_read_b32 v122, a142\n" \
"v_accvgpr_read_b32 v123, a143\n" \
"v_pk_mul_f32 v[118:119], v[118:119], s[70:71]\n" \
"v_cvt_pk_bf16_f32 v116, v116, v117\n" \
"v_accvgpr_read_b32 v36, a172\n" \
"v_accvgpr_read_b32 v37, a173\n" \
"v_accvgpr_read_b32 v38, a174\n" \
"v_accvgpr_read_b32 v39, a175\n" \
"v_pk_mul_f32 v[34:35], v[34:35], s[70:71]\n" \
"v_cvt_pk_bf16_f32 v32, v32, v33\n" \
"v_accvgpr_read_b32 v136, a236\n" \
"v_accvgpr_read_b32 v137, a237\n" \
"v_accvgpr_read_b32 v138, a238\n" \
"v_accvgpr_read_b32 v139, a239\n" \
"v_exp_f32_e32 v192, v192\n" \
"v_exp_f32_e32 v194, v194\n" \
"v_exp_f32_e32 v208, v208\n" \
"v_exp_f32_e32 v210, v210\n" \
"v_fma_f32 v224, v224, s72, -v225\n" \
"v_fma_f32 v226, v226, s72, -v225\n" \
"v_pk_mul_f32 v[72:73], v[72:73], s[70:71]\n" \
"v_pk_mul_f32 v[74:75], v[74:75], s[70:71]\n" \
"v_cvt_pk_bf16_f32 v69, v70, v71\n" \
"v_pk_mul_f32 v[120:121], v[120:121], s[70:71]\n" \
"v_pk_mul_f32 v[122:123], v[122:123], s[70:71]\n" \
"v_cvt_pk_bf16_f32 v117, v118, v119\n" \
"v_pk_mul_f32 v[36:37], v[36:37], s[70:71]\n" \
"v_pk_mul_f32 v[38:39], v[38:39], s[70:71]\n" \
"v_cvt_pk_bf16_f32 v33, v34, v35\n" \
"v_pk_mul_f32 v[136:137], v[136:137], s[70:71]\n" \
"v_pk_mul_f32 v[138:139], v[138:139], s[70:71]\n" \
"v_mfma_f32_16x16x32_bf16 a[232:235], v[108:111], v[80:83], a[232:235]\n" \
"v_exp_f32_e32 v224, v224\n" \
"v_exp_f32_e32 v226, v226\n" \
"v_fma_f32 v240, v240, s72, -v241\n" \
"v_fma_f32 v242, v242, s72, -v241\n" \
"v_cvt_pk_bf16_f32 v70, v72, v73\n" \
"v_cvt_pk_bf16_f32 v71, v74, v75\n" \
"v_cvt_pk_bf16_f32 v118, v120, v121\n" \
"v_cvt_pk_bf16_f32 v119, v122, v123\n" \
"v_cvt_pk_bf16_f32 v34, v36, v37\n" \
"v_cvt_pk_bf16_f32 v35, v38, v39\n" \
"v_cvt_pk_bf16_f32 v192, v192, v194\n" \
"v_cvt_pk_bf16_f32 v208, v208, v210\n" \
"v_exp_f32_e32 v240, v240\n" \
"v_exp_f32_e32 v242, v242\n" \
"ds_write_b128 v21, v[68:71] offset:21504\n" \
"ds_write_b128 v21, v[116:119] offset:24576\n" \
"ds_write_b128 v21, v[32:35] offset:25600\n" \
"v_cvt_pk_bf16_f32 v52, v48, v49\n" \
"v_cvt_pk_bf16_f32 v53, v50, v51\n" \
"v_cvt_pk_bf16_f32 v54, v196, v197\n" \
"v_cvt_pk_bf16_f32 v55, v198, v199\n" \
"v_cvt_pk_bf16_f32 v56, v212, v213\n" \
"v_cvt_pk_bf16_f32 v57, v214, v215\n" \
"v_perm_b32 v184, v192, v192, v40\n" \
"v_perm_b32 v185, v192, v192, v41\n" \
"v_perm_b32 v186, v192, v192, v42\n" \
"v_perm_b32 v187, v192, v192, v43\n" \
"v_perm_b32 v200, v208, v208, v40\n" \
"v_perm_b32 v201, v208, v208, v41\n" \
"v_perm_b32 v202, v208, v208, v42\n" \
"v_perm_b32 v203, v208, v208, v43\n" \
"v_cvt_pk_bf16_f32 v224, v224, v226\n" \
"v_cvt_pk_bf16_f32 v58, v228, v229\n" \
"v_cvt_pk_bf16_f32 v59, v230, v231\n" \
"v_mfma_f32_16x16x32_bf16 v[148:151], v[52:55], v[184:187], 0\n" \
"v_mfma_f32_16x16x32_bf16 v[152:155], v[54:57], v[200:203], 0\n" \
"v_perm_b32 v216, v224, v224, v40\n" \
"v_perm_b32 v217, v224, v224, v41\n" \
"v_perm_b32 v218, v224, v224, v42\n" \
"v_perm_b32 v219, v224, v224, v43\n" \
"v_cvt_pk_bf16_f32 v240, v240, v242\n" \
"v_mfma_f32_16x16x32_bf16 a[232:235], v[172:175], v[144:147], a[232:235]\n" \
"v_cvt_pk_bf16_f32 v60, v244, v245\n" \
"v_cvt_pk_bf16_f32 v61, v246, v247\n" \
"v_mfma_f32_16x16x32_bf16 v[156:159], v[56:59], v[216:219], 0\n" \
"v_perm_b32 v232, v240, v240, v40\n" \
"v_perm_b32 v233, v240, v240, v41\n" \
"v_perm_b32 v234, v240, v240, v42\n" \
"v_perm_b32 v235, v240, v240, v43\n" \
"v_mfma_f32_16x16x32_bf16 a[204:207], v[104:107], v[80:83], a[204:207]\n" \
"v_accvgpr_read_b32 v132, a232\n" \
"v_accvgpr_read_b32 v133, a233\n" \
"v_accvgpr_read_b32 v134, a234\n" \
"v_accvgpr_read_b32 v135, a235\n" \
"v_pk_mul_f32 v[132:133], v[132:133], s[70:71]\n" \
"v_pk_mul_f32 v[134:135], v[134:135], s[70:71]\n" \
"v_cvt_pk_bf16_f32 v132, v132, v133\n" \
"v_cvt_pk_bf16_f32 v133, v134, v135\n" \
"v_cvt_pk_bf16_f32 v134, v136, v137\n" \
"v_cvt_pk_bf16_f32 v135, v138, v139\n" \
"ds_write_b128 v21, v[132:135] offset:29696\n" \
"v_mfma_f32_16x16x32_bf16 v[160:163], v[58:61], v[232:235], 0\n" \
"v_accvgpr_read_b32 v252, a12\n" \
"v_accvgpr_read_b32 v253, a13\n" \
"v_accvgpr_read_b32 v254, a14\n" \
"v_accvgpr_read_b32 v255, a15\n" \
"v_mfma_f32_16x16x32_bf16 a[204:207], v[168:171], v[144:147], a[204:207]\n" \
"v_cvt_pk_bf16_f32 v84, v252, v253\n" \
"v_cvt_pk_bf16_f32 v85, v254, v255\n" \
"v_exp_f32_e64 v45, v45\n" \
"v_mfma_f32_16x16x32_bf16 v[208:211], v[86:89], v[200:203], 0\n" \
"v_mfma_f32_16x16x32_bf16 v[192:195], v[84:87], v[184:187], 0\n" \
"v_mfma_f32_16x16x32_bf16 v[224:227], v[88:91], v[216:219], 0\n" \
"v_mfma_f32_16x16x32_bf16 v[240:243], v[90:93], v[232:235], 0\n" \
"v_mov_b32_dpp v150, v154 quad_perm:[0,1,2,3] row_mask:0x2 bank_mask:0xf\n" \
"v_accvgpr_read_b32 v128, a204\n" \
"v_accvgpr_read_b32 v129, a205\n" \
"v_accvgpr_read_b32 v130, a206\n" \
"v_accvgpr_read_b32 v131, a207\n" \
"v_pk_mul_f32 v[128:129], v[128:129], s[70:71]\n" \
"v_pk_mul_f32 v[130:131], v[130:131], s[70:71]\n" \
"v_mov_b32_dpp v150, v158 quad_perm:[0,1,2,3] row_mask:0x4 bank_mask:0xf\n" \
"v_add_f32_e64 v45, 1.0, v45\n" \
"v_mfma_f32_16x16x32_bf16 a[200:203], v[100:103], v[80:83], a[200:203]\n" \
"v_mov_b32_dpp v150, v162 quad_perm:[0,1,2,3] row_mask:0x8 bank_mask:0xf\n" \
"v_mov_b32_dpp v148, v152 quad_perm:[0,1,2,3] row_mask:0x2 bank_mask:0xf\n" \
"v_rcp_f32_e64 v45, v45\n" \
"v_mov_b32_dpp v149, v153 quad_perm:[0,1,2,3] row_mask:0x2 bank_mask:0xf\n" \
"v_rcp_f32_e64 v150, v150\n" \
"v_mov_b32_dpp v148, v156 quad_perm:[0,1,2,3] row_mask:0x4 bank_mask:0xf\n" \
"v_mov_b32_dpp v149, v157 quad_perm:[0,1,2,3] row_mask:0x4 bank_mask:0xf\n" \
"v_mul_f32_e64 v150, v45, v150\n" \
"v_mov_b32_dpp v148, v160 quad_perm:[0,1,2,3] row_mask:0x8 bank_mask:0xf\n" \
"v_mov_b32_dpp v149, v161 quad_perm:[0,1,2,3] row_mask:0x8 bank_mask:0xf\n" \
"v_mul_f32_e32 v148, v150, v148\n" \
"v_mul_f32_e32 v149, v150, v149\n" \
"v_mov_b32_e64 v154, v150\n" \
"v_mfma_f32_16x16x32_bf16 a[200:203], v[164:167], v[144:147], a[200:203]\n" \
"v_sub_f32_e32 v44, 1.0, v45\n" \
"v_permlane16_swap_b32_e32 v150, v154\n" \
"v_mov_b32_e32 v158, v150\n" \
"v_mov_b32_e32 v162, v154\n" \
"v_cvt_pk_bf16_f32 v148, v148, v149\n" \
"v_permlane32_swap_b32_e32 v150, v158\n" \
"v_permlane32_swap_b32_e32 v154, v162\n" \
"v_mul_f32_e64 v192, v150, v192\n" \
"v_accvgpr_read_b32 v124, a200\n" \
"v_accvgpr_read_b32 v125, a201\n" \
"v_accvgpr_read_b32 v126, a202\n" \
"v_accvgpr_read_b32 v127, a203\n" \
"v_pk_mul_f32 v[124:125], v[124:125], s[70:71]\n" \
"v_pk_mul_f32 v[126:127], v[126:127], s[70:71]\n" \
"v_cvt_pk_bf16_f32 v124, v124, v125\n" \
"v_cvt_pk_bf16_f32 v125, v126, v127\n" \
"v_cvt_pk_bf16_f32 v126, v128, v129\n" \
"v_cvt_pk_bf16_f32 v127, v130, v131\n" \
"ds_write_b128 v21, v[124:127] offset:28672\n" \
"v_mul_f32_e32 v193, v150, v193\n" \
"v_mul_f32_e32 v208, v154, v208\n" \
"v_mul_f32_e32 v209, v154, v209\n" \
"v_mul_f32_e32 v224, v158, v224\n" \
"v_mul_f32_e32 v225, v158, v225\n" \
"v_mul_f32_e32 v240, v162, v240\n" \
"v_mul_f32_e32 v241, v162, v241\n" \
"v_mul_f32_e32 v194, v150, v194\n" \
"v_mul_f32_e64 v195, v150, v195\n" \
"v_cvt_pk_bf16_f32 v192, v192, v193\n" \
"v_mul_f32_e32 v210, v154, v210\n" \
"v_mul_f32_e32 v211, v154, v211\n" \
"v_cvt_pk_bf16_f32 v208, v208, v209\n" \
"v_mul_f32_e32 v226, v158, v226\n" \
"v_mul_f32_e32 v227, v158, v227\n" \
"v_cvt_pk_bf16_f32 v224, v224, v225\n" \
"v_mul_f32_e32 v242, v162, v242\n" \
"v_mul_f32_e32 v243, v162, v243\n" \
"v_cvt_pk_bf16_f32 v240, v240, v241\n" \
"v_cvt_pk_bf16_f32 v193, v194, v195\n" \
"s_add_u32 s84, s48, 0x0\n" \
"s_nop 0\n" \
"v_cvt_pk_bf16_f32 v209, v210, v211\n" \
"s_add_u32 s85, s48, 0x24800\n" \
"v_cvt_pk_bf16_f32 v225, v226, v227\n" \
"s_add_u32 s86, s48, 0x49000\n" \
"v_cvt_pk_bf16_f32 v241, v242, v243\n" \
"s_add_u32 s87, s48, 0x6d800\n" \
"ds_write_b32 v26, v44\n" \
"buffer_store_dword v148, v25, s[80:83], s49 offen sc1\n" \
"buffer_store_dwordx2 v[192:193], v24, s[80:83], s84 offen sc1\n" \
"buffer_store_dwordx2 v[208:209], v24, s[80:83], s85 offen sc1\n" \
"buffer_store_dwordx2 v[224:225], v24, s[80:83], s86 offen sc1\n" \
"buffer_store_dwordx2 v[240:241], v24, s[80:83], s87 offen sc1\n" \
"s_branch L_end_%=\n" \
"L_poll60_%=:\n" \
"s_mov_b32 s84, 0\n" \
"L_pl60_%=:\n" \
"s_sleep 1\n" \
"v_bfe_u32 v44, v20, 2, 5\n" \
"buffer_load_dword v44, v44, s[80:83], s38 offen offset:128 sc1\n" \
"s_waitcnt vmcnt(0)\n" \
"v_cmp_ne_u32_e32 vcc, s62, v44\n" \
"s_add_u32 s84, s84, 1\n" \
"s_nop 3\n" \
"s_cbranch_vccz L_pollr60_%=\n" \
"s_cmp_gt_u32 s84, 0x400000\n" \
"s_cbranch_scc0 L_pl60_%=\n" \
"s_branch L_pollr60_%=\n" \
"L_poll160_%=:\n" \
"s_mov_b32 s84, 0\n" \
"L_pl160_%=:\n" \
"s_sleep 1\n" \
"v_bfe_u32 v44, v20, 2, 5\n" \
"buffer_load_dword v44, v44, s[80:83], s38 offen offset:0 sc1\n" \
"s_waitcnt vmcnt(0)\n" \
"v_cmp_ne_u32_e32 vcc, s62, v44\n" \
"s_add_u32 s84, s84, 1\n" \
"s_nop 3\n" \
"s_cbranch_vccz L_pollr160_%=\n" \
"s_cmp_gt_u32 s84, 0x400000\n" \
"s_cbranch_scc0 L_pl160_%=\n" \
"s_branch L_pollr160_%=\n" \
".fill ((4096 - ((. - L_w0_%=) & 4095)) & 4095) / 4, 4, 0xBF800000\n" \
"L_w1_%=:\n" \
"s_waitcnt vmcnt(18)\n" \
"s_barrier\n" \
"s_nop 0\n" \
"ds_read_b64_tr_b16 v[84:85], v32 offset:0\n" \
"ds_read_b64_tr_b16 v[86:87], v32 offset:1024\n" \
"ds_read_b64_tr_b16 v[148:149], v32 offset:8192\n" \
"ds_read_b64_tr_b16 v[150:151], v32 offset:9216\n" \
"ds_read_b128 v[52:55], v28 offset:16384\n" \
"ds_read_b128 v[116:119], v29 offset:16384\n" \
"ds_read_b64_tr_b16 v[88:89], v33 offset:0\n" \
"ds_read_b64_tr_b16 v[90:91], v33 offset:1024\n" \
"ds_read_b64_tr_b16 v[152:153], v33 offset:8192\n" \
"ds_read_b64_tr_b16 v[154:155], v33 offset:9216\n" \
"ds_read_b64_tr_b16 v[92:93], v34 offset:0\n" \
"ds_read_b64_tr_b16 v[94:95], v34 offset:1024\n" \
"ds_read_b64_tr_b16 v[156:157], v34 offset:8192\n" \
"ds_read_b64_tr_b16 v[158:159], v34 offset:9216\n" \
"ds_read_b64_tr_b16 v[96:97], v35 offset:0\n" \
"ds_read_b64_tr_b16 v[98:99], v35 offset:1024\n" \
"ds_read_b64_tr_b16 v[160:161], v35 offset:8192\n" \
"ds_read_b64_tr_b16 v[162:163], v35 offset:9216\n" \
"ds_read_b128 v[56:59], v28 offset:18432\n" \
"ds_read_b128 v[120:123], v29 offset:18432\n" \
"ds_read_b128 v[60:63], v28 offset:20480\n" \
"ds_read_b128 v[124:127], v29 offset:20480\n" \
"ds_read_b128 v[64:67], v28 offset:22528\n" \
"ds_read_b128 v[128:131], v29 offset:22528\n" \
"ds_read_b128 v[68:71], v28 offset:0\n" \
"ds_read_b128 v[132:135], v29 offset:0\n" \
"ds_read_b128 v[72:75], v28 offset:2048\n" \
"ds_read_b128 v[136:139], v29 offset:2048\n" \
"ds_read_b128 v[76:79], v28 offset:4096\n" \
"ds_read_b128 v[140:143], v29 offset:4096\n" \
"ds_read_b128 v[80:83], v28 offset:6144\n" \
"ds_read_b128 v[144:147], v29 offset:6144\n" \
"s_waitcnt lgkmcnt(15)\n" \
"v_mfma_f32_16x16x32_bf16 a[0:3], v[52:55], v[84:87], 0\n" \
"ds_read_b64_tr_b16 v[100:101], v36 offset:0\n" \
"v_mad_i32_i24 v217, v216, -6, v215\n" \
"v_mfma_f32_16x16x32_bf16 a[0:3], v[116:119], v[148:151], a[0:3]\n" \
"v_add_u32_e32 v218, 0xfffffe50, v215\n" \
"s_mul_i32 s35, s6, 0x48\n" \
"v_mfma_f32_16x16x32_bf16 a[4:7], v[52:55], v[88:91], 0\n" \
"v_lshrrev_b32_e32 v219, 1, v217\n" \
"v_add_u32_e64 v218, s35, v216\n" \
"v_mfma_f32_16x16x32_bf16 a[4:7], v[116:119], v[152:155], a[4:7]\n" \
"ds_read_b64_tr_b16 v[102:103], v36 offset:1024\n" \
"v_mul_u32_u24_e32 v219, 0x480, v219\n" \
"v_mfma_f32_16x16x32_bf16 a[8:11], v[52:55], v[92:95], 0\n" \
"v_and_b32_e64 v214, 16, v20\n" \
"v_mad_u32_u24 v219, v218, s5, v219\n" \
"v_mfma_f32_16x16x32_bf16 a[8:11], v[116:119], v[156:159], a[8:11]\n" \
"v_or_b32_e32 v192, v219, v214\n" \
"v_mov_b32_e32 v220, 2.0\n" \
"v_mfma_f32_16x16x32_bf16 a[12:15], v[52:55], v[96:99], 0\n" \
"ds_read_b64_tr_b16 v[164:165], v36 offset:8192\n" \
"v_mov_b32_e32 v222, 7\n" \
"s_waitcnt lgkmcnt(15)\n" \
"v_mfma_f32_16x16x32_bf16 a[12:15], v[116:119], v[160:163], a[12:15]\n" \
"v_lshlrev_b32_e32 v220, 7, v218\n" \
"v_lshrrev_b32_e32 v218, 1, v218\n" \
"v_mfma_f32_16x16x32_bf16 a[44:47], v[56:59], v[96:99], 0\n" \
"v_and_b32_e32 v218, 4, v218\n" \
"v_and_b32_e32 v216, 2, v216\n" \
"v_mfma_f32_16x16x32_bf16 a[44:47], v[120:123], v[160:163], a[44:47]\n" \
"ds_read_b64_tr_b16 v[166:167], v36 offset:9216\n" \
"s_mul_i32 s7, s6, 0x2400\n" \
"v_mfma_f32_16x16x32_bf16 a[40:43], v[56:59], v[92:95], 0\n" \
"s_sub_i32 s34, 0x22000, s7\n" \
"v_bitop3_b32 v216, v217, v218, v216 bitop3:0x1e\n" \
"v_mfma_f32_16x16x32_bf16 a[40:43], v[120:123], v[156:159], a[40:43]\n" \
"v_or_b32_e32 v213, 0x24400, v20\n" \
"v_lshlrev_b32_e64 v216, 4, v216\n" \
"v_mfma_f32_16x16x32_bf16 a[36:39], v[56:59], v[88:91], 0\n" \
"v_add3_u32 v196, v220, s34, v216\n" \
"v_mov_b32_e64 v216, v213\n" \
"v_mfma_f32_16x16x32_bf16 a[36:39], v[120:123], v[152:155], a[36:39]\n" \
"ds_read_b64_tr_b16 v[104:105], v37 offset:0\n" \
"s_movk_i32 s36, 0x1b0\n" \
"v_mfma_f32_16x16x32_bf16 a[32:35], v[56:59], v[84:87], 0\n" \
"v_or_b32_e32 v216, 0x100, v215\n" \
"v_mul_u32_u24_e32 v217, 0x2aab, v216\n" \
"v_mfma_f32_16x16x32_bf16 a[32:35], v[120:123], v[148:151], a[32:35]\n" \
"v_add_u32_e32 v215, 0xffffff50, v215\n" \
"v_lshrrev_b32_e32 v217, 16, v217\n" \
"s_waitcnt lgkmcnt(15)\n" \
"v_mfma_f32_16x16x32_bf16 a[64:67], v[60:63], v[84:87], 0\n" \
"s_mov_b32 m0, s32\n" \
"ds_read_b64_tr_b16 v[106:107], v37 offset:1024\n" \
"v_mfma_f32_16x16x32_bf16 a[64:67], v[124:127], v[148:151], a[64:67]\n" \
"s_mov_b32 exec_lo, 0xff000000\n" \
"s_mov_b32 exec_hi, 0x0\n" \
"global_load_lds_dword v[184:185], off\n" \
"s_mov_b64 exec, -1\n" \
"v_mfma_f32_16x16x32_bf16 a[68:71], v[60:63], v[88:91], 0\n" \
"v_mov_b32_e64 v220, v215\n" \
"v_mad_i32_i24 v218, v217, -6, v216\n" \
"v_mfma_f32_16x16x32_bf16 a[68:71], v[124:127], v[152:155], a[68:71]\n" \
"ds_read_b64_tr_b16 v[168:169], v37 offset:8192\n" \
"v_cmp_gt_u32_e64 vcc, s36, v216\n" \
"v_mfma_f32_16x16x32_bf16 a[72:75], v[60:63], v[92:95], 0\n" \
"v_mov_b32_e32 v221, 7\n" \
"v_lshrrev_b32_e32 v222, 1, v218\n" \
"v_mfma_f32_16x16x32_bf16 a[72:75], v[124:127], v[156:159], a[72:75]\n" \
"s_mov_b32 m0, s32\n" \
"v_cndmask_b32_e32 v217, v220, v217, vcc\n" \
"v_mfma_f32_16x16x32_bf16 a[76:79], v[60:63], v[96:99], 0\n" \
"s_mov_b32 exec_lo, 0xffff\n" \
"s_mov_b32 exec_hi, 0x0\n" \
"global_load_lds_dword v[186:187], off\n" \
"s_mov_b64 exec, -1\n" \
"v_mfma_f32_16x16x32_bf16 a[76:79], v[124:127], v[160:163], a[76:79]\n" \
"ds_read_b64_tr_b16 v[170:171], v37 offset:9216\n" \
"s_movk_i32 s4, 0x48\n" \
"s_waitcnt lgkmcnt(15)\n" \
"v_mfma_f32_16x16x32_bf16 a[108:111], v[64:67], v[96:99], 0\n" \
"v_add_u32_e64 v220, s35, v217\n" \
"v_mul_u32_u24_e32 v222, 0x480, v222\n" \
"v_mfma_f32_16x16x32_bf16 a[108:111], v[128:131], v[160:163], a[108:111]\n" \
"v_mad_u32_u24 v222, v220, s5, v222\n" \
"v_cmp_gt_u32_e64 s[4:5], s4, v215\n" \
"v_mfma_f32_16x16x32_bf16 a[104:107], v[64:67], v[92:95], 0\n" \
"ds_read_b64_tr_b16 v[108:109], v38 offset:0\n" \
"v_cndmask_b32_e64 v215, v221, v218, vcc\n" \
"v_mfma_f32_16x16x32_bf16 a[104:107], v[128:131], v[156:159], a[104:107]\n" \
"v_lshrrev_b32_e32 v218, 1, v220\n" \
"v_or_b32_e32 v214, v222, v214\n" \
"v_mfma_f32_16x16x32_bf16 a[100:103], v[64:67], v[88:91], 0\n" \
"v_mov_b32_e32 v222, 2.0\n" \
"v_and_b32_e32 v218, 4, v218\n" \
"v_mfma_f32_16x16x32_bf16 a[100:103], v[128:131], v[152:155], a[100:103]\n" \
"ds_read_b64_tr_b16 v[110:111], v38 offset:1024\n" \
"v_and_b32_e64 v217, 2, v217\n" \
"v_mfma_f32_16x16x32_bf16 a[96:99], v[64:67], v[84:87], 0\n" \
"v_bitop3_b32 v215, v215, v218, v217 bitop3:0x1e\n" \
"v_lshlrev_b32_e64 v223, 1, v220\n" \
"v_mfma_f32_16x16x32_bf16 a[96:99], v[128:131], v[148:151], a[96:99]\n" \
"v_mov_b32_e32 v224, 2.0\n" \
"v_cndmask_b32_e32 v193, v222, v214, vcc\n" \
"v_mfma_f32_16x16x32_bf16 a[128:131], v[68:71], v[84:87], 0\n" \
"ds_read_b64_tr_b16 v[172:173], v38 offset:8192\n" \
"v_lshlrev_b32_e32 v214, 7, v220\n" \
"s_waitcnt lgkmcnt(15)\n" \
"v_mfma_f32_16x16x32_bf16 a[128:131], v[132:135], v[148:151], a[128:131]\n" \
"v_lshlrev_b32_e32 v215, 4, v215\n" \
"s_mul_i32 s39, s3, 18\n" \
"v_mfma_f32_16x16x32_bf16 a[132:135], v[68:71], v[88:91], 0\n" \
"v_add3_u32 v214, v214, s34, v215\n" \
"v_cndmask_b32_e64 v195, v224, v223, s[4:5]\n" \
"v_mfma_f32_16x16x32_bf16 a[132:135], v[132:135], v[152:155], a[132:135]\n" \
"ds_read_b64_tr_b16 v[174:175], v38 offset:9216\n" \
"s_movk_i32 s4, 0x1f8\n" \
"v_mfma_f32_16x16x32_bf16 a[136:139], v[68:71], v[92:95], 0\n" \
"v_mov_b32_e32 v215, v213\n" \
"s_add_i32 s35, s35, s39\n" \
"v_mfma_f32_16x16x32_bf16 a[136:139], v[132:135], v[156:159], a[136:139]\n" \
"v_cmp_gt_u32_e32 vcc, s4, v216\n" \
"v_add_u32_e64 v212, s35, v212\n" \
"v_mfma_f32_16x16x32_bf16 a[140:143], v[68:71], v[96:99], 0\n" \
"v_mov_b32_e32 v219, 2.0\n" \
"v_cndmask_b32_e32 v197, v215, v214, vcc\n" \
"v_mfma_f32_16x16x32_bf16 a[140:143], v[132:135], v[160:163], a[140:143]\n" \
"ds_read_b64_tr_b16 v[112:113], v39 offset:0\n" \
"s_movk_i32 s35, 0x120\n" \
"v_mfma_f32_16x16x32_bf16 a[172:175], v[72:75], v[96:99], 0\n" \
"v_mul_u32_u24_e32 v214, 0xd80, v212\n" \
"v_cmp_gt_u32_e32 vcc, s35, v20\n" \
"v_mfma_f32_16x16x32_bf16 a[172:175], v[136:139], v[160:163], a[172:175]\n" \
"v_lshrrev_b32_e32 v215, 1, v212\n" \
"s_and_b32 s4, s2, 7\n" \
"v_mfma_f32_16x16x32_bf16 a[168:171], v[72:75], v[92:95], 0\n" \
"ds_read_b64_tr_b16 v[114:115], v39 offset:1024\n" \
"v_cndmask_b32_e64 v194, v219, v214, vcc\n" \
"v_mfma_f32_16x16x32_bf16 a[168:171], v[136:139], v[156:159], a[168:171]\n" \
"v_lshlrev_b32_e32 v214, 7, v212\n" \
"v_lshrrev_b32_e32 v212, 2, v212\n" \
"v_mfma_f32_16x16x32_bf16 a[164:167], v[72:75], v[88:91], 0\n" \
"v_and_b32_e64 v212, 2, v212\n" \
"v_and_or_b32 v212, v215, 1, v212\n" \
"v_mfma_f32_16x16x32_bf16 a[164:167], v[136:139], v[152:155], a[164:167]\n" \
"ds_read_b64_tr_b16 v[176:177], v39 offset:8192\n" \
"s_lshl_b32 s5, s4, 2\n" \
"v_mfma_f32_16x16x32_bf16 a[160:163], v[72:75], v[84:87], 0\n" \
"s_bfe_u32 s38, s2, 0x20003\n" \
"v_lshlrev_b32_e32 v212, 5, v212\n" \
"v_mfma_f32_16x16x32_bf16 a[160:163], v[136:139], v[148:151], a[160:163]\n" \
"s_movk_i32 s35, 0x60\n" \
"s_or_b32 s5, s5, s38\n" \
"s_waitcnt lgkmcnt(15)\n" \
"v_mfma_f32_16x16x32_bf16 a[192:195], v[76:79], v[84:87], 0\n" \
"ds_read_b64_tr_b16 v[178:179], v39 offset:9216\n" \
"v_bitop3_b32 v212, v212, s35, v214 bitop3:0x36\n" \
"v_mfma_f32_16x16x32_bf16 a[192:195], v[140:143], v[148:151], a[192:195]\n" \
"s_lshr_b32 s36, s2, 3\n" \
"v_add_u32_e64 v212, s34, v212\n" \
"v_mfma_f32_16x16x32_bf16 a[196:199], v[76:79], v[88:91], 0\n" \
"s_mul_i32 s34, s5, 0x12000\n" \
"s_or_b32 s40, s34, 0x6000000\n" \
"v_mfma_f32_16x16x32_bf16 a[196:199], v[140:143], v[152:155], a[196:199]\n" \
"s_mul_i32 s34, s36, 0x90000\n" \
"s_lshl_b32 s36, s3, 14\n" \
"v_mfma_f32_16x16x32_bf16 a[200:203], v[76:79], v[92:95], 0\n" \
"s_add_i32 s34, s34, s36\n" \
"s_lshl_b32 s38, s5, 8\n" \
"v_mfma_f32_16x16x32_bf16 a[200:203], v[140:143], v[156:159], a[200:203]\n" \
"s_add_i32 s41, s34, 0x4240000\n" \
"s_mul_i32 s34, s5, 36\n" \
"v_mfma_f32_16x16x32_bf16 a[204:207], v[76:79], v[96:99], 0\n" \
"s_add_i32 s5, s7, s40\n" \
"s_addk_i32 s5, 0x2000\n" \
"v_mfma_f32_16x16x32_bf16 a[204:207], v[140:143], v[160:163], a[204:207]\n" \
"s_mov_b32 s37, 2.0\n" \
"s_or_b32 s38, s38, 0x5be8000\n" \
"v_mfma_f32_16x16x32_bf16 a[236:239], v[80:83], v[96:99], 0\n" \
"s_cmp_lg_u32 s3, 0\n" \
"s_cselect_b32 s36, s37, s5\n" \
"v_mfma_f32_16x16x32_bf16 a[236:239], v[144:147], v[160:163], a[236:239]\n" \
"s_lshl_b32 s5, s6, 2\n" \
"s_or_b32 s5, s38, s5\n" \
"v_mfma_f32_16x16x32_bf16 a[232:235], v[80:83], v[92:95], 0\n" \
"s_lshl_b32 s35, s3, 10\n" \
"v_mfma_f32_16x16x32_bf16 a[232:235], v[144:147], v[156:159], a[232:235]\n" \
"s_cmpk_lt_u32 s3, 3\n" \
"s_cselect_b32 s37, s37, s5\n" \
"v_mfma_f32_16x16x32_bf16 a[228:231], v[80:83], v[88:91], 0\n" \
"s_lshl_b32 s5, s4, 16\n" \
"s_lshl_b32 s4, s4, 11\n" \
"v_mfma_f32_16x16x32_bf16 a[228:231], v[144:147], v[152:155], a[228:231]\n" \
"v_cndmask_b32_e32 v198, v213, v212, vcc\n" \
"v_or_b32_e32 v212, s35, v20\n" \
"v_mfma_f32_16x16x32_bf16 a[224:227], v[80:83], v[84:87], 0\n" \
"s_or_b32 s39, s40, s35\n" \
"s_or_b32 s55, s41, s4\n" \
"v_mfma_f32_16x16x32_bf16 a[224:227], v[144:147], v[148:151], a[224:227]\n" \
"s_waitcnt vmcnt(4) lgkmcnt(0)\n" \
"s_barrier\n" \
"v_mfma_f32_16x16x32_bf16 a[16:19], v[52:55], v[100:103], 0\n" \
"s_add_u32 m0, s30, 0x0\n" \
"s_nop 0\n" \
"s_mov_b32 s85, 0x100\n" \
"v_mfma_f32_16x16x32_bf16 a[16:19], v[116:119], v[164:167], a[16:19]\n" \
"buffer_load_dwordx4 v22, s[64:67], s85 offen lds\n" \
"v_mfma_f32_16x16x32_bf16 a[20:23], v[52:55], v[104:107], 0\n" \
"ds_read_b64_tr_b16 v[84:85], v32 offset:32768\n" \
"v_or_b32_e32 v199, 0x22000, v212\n" \
"v_mfma_f32_16x16x32_bf16 a[20:23], v[116:119], v[168:171], a[20:23]\n" \
"s_add_u32 m0, s30, 0x400\n" \
"s_mov_b32 s84, 0x2100\n" \
"v_mfma_f32_16x16x32_bf16 a[24:27], v[52:55], v[108:111], 0\n" \
"buffer_load_dwordx4 v23, s[64:67], s84 offen lds\n" \
"v_mfma_f32_16x16x32_bf16 a[24:27], v[116:119], v[172:175], a[24:27]\n" \
"ds_read_b64_tr_b16 v[86:87], v32 offset:33792\n" \
"s_add_i32 s35, s39, s7\n" \
"v_mfma_f32_16x16x32_bf16 a[28:31], v[52:55], v[112:115], 0\n" \
"ds_read_b64_tr_b16 v[148:149], v32 offset:40960\n" \
"s_add_i32 s54, s41, s5\n" \
"v_mfma_f32_16x16x32_bf16 a[28:31], v[116:119], v[176:179], a[28:31]\n" \
"ds_read_b128 v[52:55], v28 offset:49152\n" \
"s_add_i32 s55, s55, 0x80000\n" \
"v_mfma_f32_16x16x32_bf16 a[60:63], v[56:59], v[112:115], 0\n" \
"ds_read_b128 v[116:119], v29 offset:49152\n" \
"v_mov_b32_e32 v213, 0xffff\n" \
"v_mfma_f32_16x16x32_bf16 a[60:63], v[120:123], v[176:179], a[60:63]\n" \
"s_add_u32 m0, s31, 0x0\n" \
"s_nop 0\n" \
"s_mov_b32 s85, 0x100000\n" \
"v_mfma_f32_16x16x32_bf16 a[56:59], v[56:59], v[108:111], 0\n" \
"buffer_load_dwordx4 v26, s[68:71], s85 offen lds\n" \
"v_mfma_f32_16x16x32_bf16 a[56:59], v[120:123], v[172:175], a[56:59]\n" \
"s_add_u32 m0, s31, 0x4000\n" \
"s_mov_b32 s84, 0x100000\n" \
"v_mfma_f32_16x16x32_bf16 a[52:55], v[56:59], v[104:107], 0\n" \
"buffer_load_dwordx4 v26, s[72:75], s84 offen lds\n" \
"v_mfma_f32_16x16x32_bf16 a[52:55], v[120:123], v[168:171], a[52:55]\n" \
"s_add_u32 m0, s30, 0x800\n" \
"s_mov_b32 s85, 0x4100\n" \
"v_mfma_f32_16x16x32_bf16 a[48:51], v[56:59], v[100:103], 0\n" \
"buffer_load_dwordx4 v22, s[64:67], s85 offen lds\n" \
"v_mfma_f32_16x16x32_bf16 a[48:51], v[120:123], v[164:167], a[48:51]\n" \
"ds_read_b128 v[56:59], v28 offset:51200\n" \
"s_and_b32 s6, s3, 1\n" \
"v_mfma_f32_16x16x32_bf16 a[80:83], v[60:63], v[100:103], 0\n" \
"ds_read_b128 v[120:123], v29 offset:51200\n" \
"s_lshl_b32 s4, s6, 4\n" \
"v_mfma_f32_16x16x32_bf16 a[80:83], v[124:127], v[164:167], a[80:83]\n" \
"s_add_u32 m0, s30, 0xc00\n" \
"s_mov_b32 s84, 0x6100\n" \
"v_mfma_f32_16x16x32_bf16 a[84:87], v[60:63], v[104:107], 0\n" \
"buffer_load_dwordx4 v23, s[64:67], s84 offen lds\n" \
"v_mfma_f32_16x16x32_bf16 a[84:87], v[124:127], v[168:171], a[84:87]\n" \
"ds_read_b64_tr_b16 v[150:151], v32 offset:41984\n" \
"v_lshrrev_b32_e64 v214, 4, v20\n" \
"v_mfma_f32_16x16x32_bf16 a[88:91], v[60:63], v[108:111], 0\n" \
"s_add_u32 m0, s31, 0x400\n" \
"s_mov_b32 s85, 0x108000\n" \
"v_mfma_f32_16x16x32_bf16 a[88:91], v[124:127], v[172:175], a[88:91]\n" \
"buffer_load_dwordx4 v26, s[68:71], s85 offen lds\n" \
"v_mfma_f32_16x16x32_bf16 a[92:95], v[60:63], v[112:115], 0\n" \
"ds_read_b64_tr_b16 v[88:89], v33 offset:32768\n" \
"v_lshrrev_b32_e64 v215, 6, v20\n" \
"v_mfma_f32_16x16x32_bf16 a[92:95], v[124:127], v[176:179], a[92:95]\n" \
"ds_read_b128 v[60:63], v28 offset:53248\n" \
"v_and_b32_e64 v216, 12, v215\n" \
"v_mfma_f32_16x16x32_bf16 a[124:127], v[64:67], v[112:115], 0\n" \
"ds_read_b128 v[124:127], v29 offset:53248\n" \
"v_and_or_b32 v212, v214, 15, s4\n" \
"v_mfma_f32_16x16x32_bf16 a[124:127], v[128:131], v[176:179], a[124:127]\n" \
"s_add_u32 m0, s31, 0x4400\n" \
"s_mov_b32 s84, 0x108000\n" \
"v_mfma_f32_16x16x32_bf16 a[120:123], v[64:67], v[108:111], 0\n" \
"buffer_load_dwordx4 v26, s[72:75], s84 offen lds\n" \
"v_mfma_f32_16x16x32_bf16 a[120:123], v[128:131], v[172:175], a[120:123]\n" \
"s_add_u32 m0, s30, 0x1000\n" \
"s_mov_b32 s85, 0x8100\n" \
"v_mfma_f32_16x16x32_bf16 a[116:119], v[64:67], v[104:107], 0\n" \
"buffer_load_dwordx4 v22, s[64:67], s85 offen lds\n" \
"v_mfma_f32_16x16x32_bf16 a[116:119], v[128:131], v[168:171], a[116:119]\n" \
"ds_read_b64_tr_b16 v[90:91], v33 offset:33792\n" \
"v_cmp_gt_u32_e64 vcc, v216, v212\n" \
"v_mfma_f32_16x16x32_bf16 a[112:115], v[64:67], v[100:103], 0\n" \
"ds_read_b64_tr_b16 v[152:153], v33 offset:40960\n" \
"v_mov_b32_e32 v218, 0xffff0000\n" \
"v_mfma_f32_16x16x32_bf16 a[112:115], v[128:131], v[164:167], a[112:115]\n" \
"ds_read_b128 v[64:67], v28 offset:55296\n" \
"s_lshl_b32 s4, s2, 2\n" \
"s_nop 0\n" \
"v_mfma_f32_16x16x32_bf16 a[144:147], v[68:71], v[100:103], 0\n" \
"ds_read_b128 v[128:131], v29 offset:55296\n" \
"v_cndmask_b32_e64 v217, v213, 0, vcc\n" \
"v_mfma_f32_16x16x32_bf16 a[144:147], v[132:135], v[164:167], a[144:147]\n" \
"s_add_u32 m0, s30, 0x1400\n" \
"s_mov_b32 s84, 0xa100\n" \
"v_mfma_f32_16x16x32_bf16 a[148:151], v[68:71], v[104:107], 0\n" \
"buffer_load_dwordx4 v23, s[64:67], s84 offen lds\n" \
"v_mfma_f32_16x16x32_bf16 a[148:151], v[132:135], v[168:171], a[148:151]\n" \
"s_add_u32 m0, s31, 0x800\n" \
"s_mov_b32 s85, 0x110000\n" \
"v_mfma_f32_16x16x32_bf16 a[152:155], v[68:71], v[108:111], 0\n" \
"buffer_load_dwordx4 v27, s[68:71], s85 offen lds\n" \
"v_mfma_f32_16x16x32_bf16 a[152:155], v[132:135], v[172:175], a[152:155]\n" \
"s_add_u32 m0, s31, 0x4800\n" \
"s_mov_b32 s84, 0x110000\n" \
"v_mfma_f32_16x16x32_bf16 a[156:159], v[68:71], v[112:115], 0\n" \
"buffer_load_dwordx4 v27, s[72:75], s84 offen lds\n" \
"v_mfma_f32_16x16x32_bf16 a[156:159], v[132:135], v[176:179], a[156:159]\n" \
"ds_read_b128 v[68:71], v28 offset:32768\n" \
"v_cmp_lt_u32_e64 vcc, v216, v212\n" \
"v_mfma_f32_16x16x32_bf16 a[188:191], v[72:75], v[112:115], 0\n" \
"ds_read_b128 v[132:135], v29 offset:32768\n" \
"s_mov_b32 s7, 0\n" \
"v_mfma_f32_16x16x32_bf16 a[188:191], v[136:139], v[176:179], a[188:191]\n" \
"s_add_u32 m0, s30, 0x1800\n" \
"s_mov_b32 s85, 0xc100\n" \
"v_mfma_f32_16x16x32_bf16 a[184:187], v[72:75], v[108:111], 0\n" \
"buffer_load_dwordx4 v22, s[64:67], s85 offen lds\n" \
"v_mfma_f32_16x16x32_bf16 a[184:187], v[136:139], v[172:175], a[184:187]\n" \
"ds_read_b64_tr_b16 v[154:155], v33 offset:41984\n" \
"s_or_b32 s40, s4, s3\n" \
"v_mfma_f32_16x16x32_bf16 a[180:183], v[72:75], v[104:107], 0\n" \
"s_add_u32 m0, s30, 0x1c00\n" \
"s_mov_b32 s84, 0xe100\n" \
"v_mfma_f32_16x16x32_bf16 a[180:183], v[136:139], v[168:171], a[180:183]\n" \
"buffer_load_dwordx4 v23, s[64:67], s84 offen lds\n" \
"v_mfma_f32_16x16x32_bf16 a[176:179], v[72:75], v[100:103], 0\n" \
"ds_read_b64_tr_b16 v[92:93], v34 offset:32768\n" \
"v_cndmask_b32_e64 v219, 0, v218, vcc\n" \
"v_mfma_f32_16x16x32_bf16 a[176:179], v[136:139], v[164:167], a[176:179]\n" \
"ds_read_b128 v[72:75], v28 offset:34816\n" \
"v_or_b32_e64 v12, v217, v219\n" \
"v_mfma_f32_16x16x32_bf16 a[208:211], v[76:79], v[100:103], 0\n" \
"ds_read_b128 v[136:139], v29 offset:34816\n" \
"v_or_b32_e64 v217, 2, v216\n" \
"v_mfma_f32_16x16x32_bf16 a[208:211], v[140:143], v[164:167], a[208:211]\n" \
"ds_read_b64_tr_b16 v[94:95], v34 offset:33792\n" \
"v_cmp_gt_u32_e64 vcc, v217, v212\n" \
"v_mfma_f32_16x16x32_bf16 a[212:215], v[76:79], v[104:107], 0\n" \
"ds_read_b64_tr_b16 v[156:157], v34 offset:40960\n" \
"s_add_i32 s41, s40, 0xffffffaf\n" \
"v_mfma_f32_16x16x32_bf16 a[212:215], v[140:143], v[168:171], a[212:215]\n" \
"ds_read_b64_tr_b16 v[158:159], v34 offset:41984\n" \
"v_mfma_f32_16x16x32_bf16 a[216:219], v[76:79], v[108:111], 0\n" \
"ds_read_b64_tr_b16 v[96:97], v35 offset:32768\n" \
"v_mfma_f32_16x16x32_bf16 a[216:219], v[140:143], v[172:175], a[216:219]\n" \
"ds_read_b64_tr_b16 v[98:99], v35 offset:33792\n" \
"v_mfma_f32_16x16x32_bf16 a[220:223], v[76:79], v[112:115], 0\n" \
"ds_read_b64_tr_b16 v[160:161], v35 offset:40960\n" \
"v_mfma_f32_16x16x32_bf16 a[220:223], v[140:143], v[176:179], a[220:223]\n" \
"ds_read_b128 v[76:79], v28 offset:36864\n" \
"v_mfma_f32_16x16x32_bf16 a[252:255], v[80:83], v[112:115], 0\n" \
"ds_read_b128 v[140:143], v29 offset:36864\n" \
"v_mfma_f32_16x16x32_bf16 a[252:255], v[144:147], v[176:179], a[252:255]\n" \
"ds_read_b64_tr_b16 v[162:163], v35 offset:41984\n" \
"v_mfma_f32_16x16x32_bf16 a[248:251], v[80:83], v[108:111], 0\n" \
"s_cmpk_lt_u32 s40, 0x291\n" \
"s_cselect_b32 s7, s41, s7\n" \
"v_mfma_f32_16x16x32_bf16 a[248:251], v[144:147], v[172:175], a[248:251]\n" \
"v_cndmask_b32_e64 v219, v213, 0, vcc\n" \
"v_cmp_lt_u32_e64 vcc, v217, v212\n" \
"v_mfma_f32_16x16x32_bf16 a[244:247], v[80:83], v[104:107], 0\n" \
"s_cmpk_lt_u32 s40, 0x51\n" \
"s_cselect_b32 s7, s40, s7\n" \
"v_mfma_f32_16x16x32_bf16 a[244:247], v[144:147], v[168:171], a[244:247]\n" \
"s_cmpk_lt_u32 s40, 0x51\n" \
"s_cselect_b32 s5, s23, s25\n" \
"v_mfma_f32_16x16x32_bf16 a[240:243], v[80:83], v[100:103], 0\n" \
"v_cndmask_b32_e32 v217, 0, v218, vcc\n" \
"v_or_b32_e32 v13, v219, v217\n" \
"v_mfma_f32_16x16x32_bf16 a[240:243], v[144:147], v[164:167], a[240:243]\n" \
"ds_read_b128 v[80:83], v28 offset:38912\n" \
"ds_read_b128 v[144:147], v29 offset:38912\n" \
"s_waitcnt lgkmcnt(15)\n" \
"v_mfma_f32_16x16x32_bf16 a[0:3], v[52:55], v[84:87], a[0:3]\n" \
"s_add_u32 m0, s31, 0xc00\n" \
"s_mov_b32 s85, 0x118000\n" \
"v_mfma_f32_16x16x32_bf16 a[0:3], v[116:119], v[148:151], a[0:3]\n" \
"buffer_load_dwordx4 v27, s[68:71], s85 offen lds\n" \
"v_mfma_f32_16x16x32_bf16 a[4:7], v[52:55], v[88:91], a[4:7]\n" \
"ds_read_b64_tr_b16 v[100:101], v36 offset:32768\n" \
"v_or_b32_e32 v217, 16, v216\n" \
"s_waitcnt lgkmcnt(15)\n" \
"v_mfma_f32_16x16x32_bf16 a[4:7], v[116:119], v[152:155], a[4:7]\n" \
"s_add_u32 m0, s31, 0x4c00\n" \
"s_mov_b32 s84, 0x118000\n" \
"s_waitcnt lgkmcnt(11)\n" \
"v_mfma_f32_16x16x32_bf16 a[8:11], v[52:55], v[92:95], a[8:11]\n" \
"buffer_load_dwordx4 v27, s[72:75], s84 offen lds\n" \
"s_waitcnt lgkmcnt(9)\n" \
"v_mfma_f32_16x16x32_bf16 a[8:11], v[116:119], v[156:159], a[8:11]\n" \
"ds_read_b64_tr_b16 v[102:103], v36 offset:33792\n" \
"v_cmp_gt_u32_e32 vcc, v217, v212\n" \
"s_waitcnt lgkmcnt(8)\n" \
"v_mfma_f32_16x16x32_bf16 a[12:15], v[52:55], v[96:99], a[12:15]\n" \
"ds_read_b64_tr_b16 v[164:165], v36 offset:40960\n" \
"s_waitcnt lgkmcnt(5)\n" \
"v_mfma_f32_16x16x32_bf16 a[12:15], v[116:119], v[160:163], a[12:15]\n" \
"s_cmpk_lt_u32 s40, 0x51\n" \
"s_cselect_b32 s4, s22, s24\n" \
"v_mfma_f32_16x16x32_bf16 a[44:47], v[56:59], v[96:99], a[44:47]\n" \
"s_movk_i32 s40, 0xf400\n" \
"v_cndmask_b32_e64 v219, v213, 0, vcc\n" \
"v_mfma_f32_16x16x32_bf16 a[44:47], v[120:123], v[160:163], a[44:47]\n" \
"ds_read_b64_tr_b16 v[166:167], v36 offset:41984\n" \
"v_cmp_lt_u32_e32 vcc, v217, v212\n" \
"v_mfma_f32_16x16x32_bf16 a[40:43], v[56:59], v[92:95], a[40:43]\n" \
"s_nop 0\n" \
"v_cndmask_b32_e32 v217, 0, v218, vcc\n" \
"v_or_b32_e64 v14, v219, v217\n" \
"v_mfma_f32_16x16x32_bf16 a[40:43], v[120:123], v[156:159], a[40:43]\n" \
"v_or_b32_e32 v217, 18, v216\n" \
"v_cmp_gt_u32_e32 vcc, v217, v212\n" \
"v_mfma_f32_16x16x32_bf16 a[36:39], v[56:59], v[88:91], a[36:39]\n" \
"s_nop 0\n" \
"v_cndmask_b32_e64 v213, v213, 0, vcc\n" \
"v_cmp_lt_u32_e32 vcc, v217, v212\n" \
"v_mfma_f32_16x16x32_bf16 a[36:39], v[120:123], v[152:155], a[36:39]\n" \
"ds_read_b64_tr_b16 v[104:105], v37 offset:32768\n" \
"v_cndmask_b32_e64 v212, 0, v218, vcc\n" \
"v_mfma_f32_16x16x32_bf16 a[32:35], v[56:59], v[84:87], a[32:35]\n" \
"v_or_b32_e32 v15, v213, v212\n" \
"v_lshlrev_b32_e32 v212, 7, v214\n" \
"v_mfma_f32_16x16x32_bf16 a[32:35], v[120:123], v[148:151], a[32:35]\n" \
"v_lshl_or_b32 v212, s7, 13, v212\n" \
"v_mov_b32_e64 v213, 0\n" \
"v_mfma_f32_16x16x32_bf16 a[64:67], v[60:63], v[84:87], a[64:67]\n" \
"s_mov_b32 m0, s32\n" \
"ds_read_b64_tr_b16 v[106:107], v37 offset:33792\n" \
"v_mfma_f32_16x16x32_bf16 a[64:67], v[124:127], v[148:151], a[64:67]\n" \
"s_mov_b32 exec_lo, 0x0\n" \
"s_mov_b32 exec_hi, 0xff\n" \
"global_load_lds_dword v[184:185], off\n" \
"s_mov_b64 exec, -1\n" \
"v_mfma_f32_16x16x32_bf16 a[68:71], v[60:63], v[88:91], a[68:71]\n" \
"v_lshl_add_u64 v[16:17], s[4:5], 0, v[212:213]\n" \
"v_mul_u32_u24_e32 v212, 0xe39, v214\n" \
"v_mfma_f32_16x16x32_bf16 a[68:71], v[124:127], v[152:155], a[68:71]\n" \
"ds_read_b64_tr_b16 v[168:169], v37 offset:40960\n" \
"v_lshrrev_b32_e32 v212, 16, v212\n" \
"v_mfma_f32_16x16x32_bf16 a[72:75], v[60:63], v[92:95], a[72:75]\n" \
"s_movk_i32 s5, 0xffee\n" \
"s_lshl_b32 s4, s2, 5\n" \
"v_mfma_f32_16x16x32_bf16 a[72:75], v[124:127], v[156:159], a[72:75]\n" \
"s_mov_b32 m0, s32\n" \
"v_mad_i32_i24 v213, v212, s5, v214\n" \
"v_mfma_f32_16x16x32_bf16 a[76:79], v[60:63], v[96:99], a[76:79]\n" \
"s_mov_b32 exec_lo, 0xffff0000\n" \
"s_mov_b32 exec_hi, 0x0\n" \
"global_load_lds_dword v[186:187], off\n" \
"s_mov_b64 exec, -1\n" \
"v_mfma_f32_16x16x32_bf16 a[76:79], v[124:127], v[160:163], a[76:79]\n" \
"ds_read_b64_tr_b16 v[170:171], v37 offset:41984\n" \
"s_add_i32 s4, s4, 32\n" \
"v_mfma_f32_16x16x32_bf16 a[108:111], v[64:67], v[96:99], a[108:111]\n" \
"v_lshlrev_b32_e32 v217, 4, v213\n" \
"v_add_u32_e32 v218, s4, v217\n" \
"v_mfma_f32_16x16x32_bf16 a[108:111], v[128:131], v[160:163], a[108:111]\n" \
"v_and_b32_e32 v218, 0xf0, v218\n" \
"v_cmp_gt_u32_e64 vcc, 16, v213\n" \
"v_mfma_f32_16x16x32_bf16 a[104:107], v[64:67], v[92:95], a[104:107]\n" \
"ds_read_b64_tr_b16 v[108:109], v38 offset:32768\n" \
"s_movk_i32 s7, 0xf800\n" \
"v_mfma_f32_16x16x32_bf16 a[104:107], v[128:131], v[156:159], a[104:107]\n" \
"v_cndmask_b32_e32 v213, v217, v218, vcc\n" \
"v_or_b32_e64 v217, 64, v214\n" \
"v_mfma_f32_16x16x32_bf16 a[100:103], v[64:67], v[88:91], a[100:103]\n" \
"v_mul_u32_u24_e32 v218, 0xe39, v217\n" \
"v_lshrrev_b32_e64 v218, 16, v218\n" \
"v_mfma_f32_16x16x32_bf16 a[100:103], v[128:131], v[152:155], a[100:103]\n" \
"ds_read_b64_tr_b16 v[110:111], v38 offset:33792\n" \
"v_mad_i32_i24 v217, v218, s5, v217\n" \
"v_mfma_f32_16x16x32_bf16 a[96:99], v[64:67], v[84:87], a[96:99]\n" \
"v_lshlrev_b32_e32 v219, 4, v217\n" \
"v_add_u32_e32 v220, s4, v219\n" \
"v_mfma_f32_16x16x32_bf16 a[96:99], v[128:131], v[148:151], a[96:99]\n" \
"v_and_b32_e32 v220, 0xf0, v220\n" \
"v_cmp_gt_u32_e64 vcc, 16, v217\n" \
"v_mfma_f32_16x16x32_bf16 a[128:131], v[68:71], v[84:87], a[128:131]\n" \
"ds_read_b64_tr_b16 v[172:173], v38 offset:40960\n" \
"v_cndmask_b32_e64 v217, v219, v220, vcc\n" \
"v_mfma_f32_16x16x32_bf16 a[128:131], v[132:135], v[148:151], a[128:131]\n" \
"v_or_b32_e32 v219, 0x80, v214\n" \
"v_mul_u32_u24_e32 v220, 0xe39, v219\n" \
"v_mfma_f32_16x16x32_bf16 a[132:135], v[68:71], v[88:91], a[132:135]\n" \
"v_lshrrev_b32_e64 v220, 16, v220\n" \
"v_mad_i32_i24 v219, v220, s5, v219\n" \
"v_mfma_f32_16x16x32_bf16 a[132:135], v[132:135], v[152:155], a[132:135]\n" \
"ds_read_b64_tr_b16 v[174:175], v38 offset:41984\n" \
"v_lshlrev_b32_e64 v221, 4, v219\n" \
"v_mfma_f32_16x16x32_bf16 a[136:139], v[68:71], v[92:95], a[136:139]\n" \
"v_add_u32_e64 v222, s4, v221\n" \
"v_and_b32_e32 v222, 0xf0, v222\n" \
"v_mfma_f32_16x16x32_bf16 a[136:139], v[132:135], v[156:159], a[136:139]\n" \
"v_cmp_gt_u32_e32 vcc, 16, v219\n" \
"s_nop 1\n" \
"v_cndmask_b32_e64 v219, v221, v222, vcc\n" \
"v_mfma_f32_16x16x32_bf16 a[140:143], v[68:71], v[96:99], a[140:143]\n" \
"v_or_b32_e32 v221, 0xc0, v214\n" \
"v_mul_u32_u24_e32 v222, 0xe39, v221\n" \
"v_mfma_f32_16x16x32_bf16 a[140:143], v[132:135], v[160:163], a[140:143]\n" \
"ds_read_b64_tr_b16 v[112:113], v39 offset:32768\n" \
"v_lshrrev_b32_e64 v222, 16, v222\n" \
"v_mfma_f32_16x16x32_bf16 a[172:175], v[72:75], v[96:99], a[172:175]\n" \
"v_mad_i32_i24 v221, v222, s5, v221\n" \
"v_lshlrev_b32_e64 v223, 4, v221\n" \
"v_mfma_f32_16x16x32_bf16 a[172:175], v[136:139], v[160:163], a[172:175]\n" \
"v_add_u32_e64 v224, s4, v223\n" \
"v_and_b32_e32 v224, 0xf0, v224\n" \
"v_mfma_f32_16x16x32_bf16 a[168:171], v[72:75], v[92:95], a[168:171]\n" \
"ds_read_b64_tr_b16 v[114:115], v39 offset:33792\n" \
"v_cmp_gt_u32_e64 vcc, 16, v221\n" \
"v_mfma_f32_16x16x32_bf16 a[168:171], v[136:139], v[156:159], a[168:171]\n" \
"s_nop 0\n" \
"v_cndmask_b32_e32 v221, v223, v224, vcc\n" \
"v_or_b32_e32 v223, 0x100, v214\n" \
"v_mfma_f32_16x16x32_bf16 a[164:167], v[72:75], v[88:91], a[164:167]\n" \
"v_mul_u32_u24_e32 v224, 0xe39, v223\n" \
"v_lshrrev_b32_e64 v224, 16, v224\n" \
"v_mfma_f32_16x16x32_bf16 a[164:167], v[136:139], v[152:155], a[164:167]\n" \
"ds_read_b64_tr_b16 v[176:177], v39 offset:40960\n" \
"v_mad_i32_i24 v223, v224, s5, v223\n" \
"v_mfma_f32_16x16x32_bf16 a[160:163], v[72:75], v[84:87], a[160:163]\n" \
"v_lshlrev_b32_e32 v225, 4, v223\n" \
"v_add_u32_e32 v226, s4, v225\n" \
"v_mfma_f32_16x16x32_bf16 a[160:163], v[136:139], v[148:151], a[160:163]\n" \
"v_and_b32_e32 v226, 0xf0, v226\n" \
"v_cmp_gt_u32_e64 vcc, 16, v223\n" \
"v_mfma_f32_16x16x32_bf16 a[192:195], v[76:79], v[84:87], a[192:195]\n" \
"ds_read_b64_tr_b16 v[178:179], v39 offset:41984\n" \
"v_cndmask_b32_e64 v223, v225, v226, vcc\n" \
"v_mfma_f32_16x16x32_bf16 a[192:195], v[140:143], v[148:151], a[192:195]\n" \
"v_or_b32_e32 v225, 0x140, v214\n" \
"v_mul_u32_u24_e32 v226, 0xe39, v225\n" \
"v_mfma_f32_16x16x32_bf16 a[196:199], v[76:79], v[88:91], a[196:199]\n" \
"v_lshrrev_b32_e64 v226, 16, v226\n" \
"v_mad_i32_i24 v225, v226, s5, v225\n" \
"v_mfma_f32_16x16x32_bf16 a[196:199], v[140:143], v[152:155], a[196:199]\n" \
"v_lshlrev_b32_e32 v227, 4, v225\n" \
"v_add_u32_e32 v228, s4, v227\n" \
"v_mfma_f32_16x16x32_bf16 a[200:203], v[76:79], v[92:95], a[200:203]\n" \
"v_and_b32_e32 v228, 0xf0, v228\n" \
"v_cmp_gt_u32_e64 vcc, 16, v225\n" \
"v_mfma_f32_16x16x32_bf16 a[200:203], v[140:143], v[156:159], a[200:203]\n" \
"s_nop 0\n" \
"v_cndmask_b32_e32 v225, v227, v228, vcc\n" \
"v_or_b32_e32 v227, 0x180, v214\n" \
"v_mfma_f32_16x16x32_bf16 a[204:207], v[76:79], v[96:99], a[204:207]\n" \
"v_mul_u32_u24_e32 v228, 0xe39, v227\n" \
"v_lshrrev_b32_e64 v228, 16, v228\n" \
"v_mfma_f32_16x16x32_bf16 a[204:207], v[140:143], v[160:163], a[204:207]\n" \
"v_mad_i32_i24 v227, v228, s5, v227\n" \
"v_lshlrev_b32_e32 v229, 4, v227\n" \
"s_waitcnt lgkmcnt(15)\n" \
"v_mfma_f32_16x16x32_bf16 a[236:239], v[80:83], v[96:99], a[236:239]\n" \
"v_add_u32_e64 v230, s4, v229\n" \
"v_and_b32_e32 v230, 0xf0, v230\n" \
"v_mfma_f32_16x16x32_bf16 a[236:239], v[144:147], v[160:163], a[236:239]\n" \
"v_cmp_gt_u32_e32 vcc, 16, v227\n" \
"s_nop 1\n" \
"v_cndmask_b32_e64 v227, v229, v230, vcc\n" \
"v_mfma_f32_16x16x32_bf16 a[232:235], v[80:83], v[92:95], a[232:235]\n" \
"v_or_b32_e32 v229, 0x1c0, v214\n" \
"v_mul_u32_u24_e32 v230, 0xe39, v229\n" \
"v_mfma_f32_16x16x32_bf16 a[232:235], v[144:147], v[156:159], a[232:235]\n" \
"v_lshrrev_b32_e64 v230, 16, v230\n" \
"v_mad_i32_i24 v229, v230, s5, v229\n" \
"v_mfma_f32_16x16x32_bf16 a[228:231], v[80:83], v[88:91], a[228:231]\n" \
"v_lshlrev_b32_e32 v231, 4, v229\n" \
"v_add_u32_e32 v232, s4, v231\n" \
"v_mfma_f32_16x16x32_bf16 a[228:231], v[144:147], v[152:155], a[228:231]\n" \
"v_and_b32_e32 v232, 0xf0, v232\n" \
"v_cmp_gt_u32_e64 vcc, 16, v229\n" \
"v_mfma_f32_16x16x32_bf16 a[224:227], v[80:83], v[84:87], a[224:227]\n" \
"s_nop 0\n" \
"v_cndmask_b32_e32 v229, v231, v232, vcc\n" \
"v_or_b32_e32 v231, 0x200, v214\n" \
"v_mfma_f32_16x16x32_bf16 a[224:227], v[144:147], v[148:151], a[224:227]\n" \
"s_waitcnt vmcnt(20)\n" \
"v_readfirstlane_b32 s62, v180\n" \
"s_nop 3\n" \
"s_add_u32 s62, s62, 0x9E3779B1\n" \
"s_waitcnt vmcnt(2) lgkmcnt(0)\n" \
"s_barrier\n" \
"s_nop 0\n" \
"v_mfma_f32_16x16x32_bf16 a[16:19], v[52:55], v[100:103], a[16:19]\n" \
"s_add_u32 m0, s30, 0x8000\n" \
"s_mov_b32 s85, 0x180\n" \
"v_mfma_f32_16x16x32_bf16 a[16:19], v[116:119], v[164:167], a[16:19]\n" \
"buffer_load_dwordx4 v22, s[64:67], s85 offen lds\n" \
"v_mfma_f32_16x16x32_bf16 a[20:23], v[52:55], v[104:107], a[20:23]\n" \
"ds_read_b64_tr_b16 v[84:85], v32 offset:0\n" \
"v_mul_u32_u24_e32 v232, 0xe39, v231\n" \
"v_mfma_f32_16x16x32_bf16 a[20:23], v[116:119], v[168:171], a[20:23]\n" \
"s_add_u32 m0, s30, 0x8400\n" \
"s_mov_b32 s84, 0x2180\n" \
"v_mfma_f32_16x16x32_bf16 a[24:27], v[52:55], v[108:111], a[24:27]\n" \
"buffer_load_dwordx4 v23, s[64:67], s84 offen lds\n" \
"v_mfma_f32_16x16x32_bf16 a[24:27], v[116:119], v[172:175], a[24:27]\n" \
"ds_read_b64_tr_b16 v[86:87], v32 offset:1024\n" \
"v_lshrrev_b32_e64 v232, 16, v232\n" \
"v_mfma_f32_16x16x32_bf16 a[28:31], v[52:55], v[112:115], a[28:31]\n" \
"ds_read_b64_tr_b16 v[148:149], v32 offset:8192\n" \
"v_mad_i32_i24 v231, v232, s5, v231\n" \
"v_mfma_f32_16x16x32_bf16 a[28:31], v[116:119], v[176:179], a[28:31]\n" \
"ds_read_b128 v[52:55], v28 offset:16384\n" \
"v_lshlrev_b32_e64 v233, 4, v231\n" \
"v_mfma_f32_16x16x32_bf16 a[60:63], v[56:59], v[112:115], a[60:63]\n" \
"ds_read_b128 v[116:119], v29 offset:16384\n" \
"v_add_u32_e64 v234, s4, v233\n" \
"v_mfma_f32_16x16x32_bf16 a[60:63], v[120:123], v[176:179], a[60:63]\n" \
"s_add_u32 m0, s31, 0x8000\n" \
"s_mov_b32 s85, 0x180000\n" \
"v_mfma_f32_16x16x32_bf16 a[56:59], v[56:59], v[108:111], a[56:59]\n" \
"buffer_load_dwordx4 v26, s[68:71], s85 offen lds\n" \
"v_mfma_f32_16x16x32_bf16 a[56:59], v[120:123], v[172:175], a[56:59]\n" \
"s_add_u32 m0, s31, 0xc000\n" \
"s_mov_b32 s84, 0x180000\n" \
"v_mfma_f32_16x16x32_bf16 a[52:55], v[56:59], v[104:107], a[52:55]\n" \
"buffer_load_dwordx4 v26, s[72:75], s84 offen lds\n" \
"v_mfma_f32_16x16x32_bf16 a[52:55], v[120:123], v[168:171], a[52:55]\n" \
"s_add_u32 m0, s30, 0x8800\n" \
"s_mov_b32 s85, 0x4180\n" \
"v_mfma_f32_16x16x32_bf16 a[48:51], v[56:59], v[100:103], a[48:51]\n" \
"buffer_load_dwordx4 v22, s[64:67], s85 offen lds\n" \
"v_mfma_f32_16x16x32_bf16 a[48:51], v[120:123], v[164:167], a[48:51]\n" \
"ds_read_b128 v[56:59], v28 offset:18432\n" \
"s_movk_i32 s4, 0x480\n" \
"s_nop 0\n" \
"v_mfma_f32_16x16x32_bf16 a[80:83], v[60:63], v[100:103], a[80:83]\n" \
"ds_read_b128 v[120:123], v29 offset:18432\n" \
"v_mad_u32_u24 v0, v212, s4, v213\n" \
"v_mfma_f32_16x16x32_bf16 a[80:83], v[124:127], v[164:167], a[80:83]\n" \
"s_add_u32 m0, s30, 0x8c00\n" \
"s_mov_b32 s84, 0x6180\n" \
"v_mfma_f32_16x16x32_bf16 a[84:87], v[60:63], v[104:107], a[84:87]\n" \
"buffer_load_dwordx4 v23, s[64:67], s84 offen lds\n" \
"v_mfma_f32_16x16x32_bf16 a[84:87], v[124:127], v[168:171], a[84:87]\n" \
"ds_read_b64_tr_b16 v[150:151], v32 offset:9216\n" \
"v_mul_u32_u24_e32 v212, 0x480, v218\n" \
"v_mfma_f32_16x16x32_bf16 a[88:91], v[60:63], v[108:111], a[88:91]\n" \
"s_add_u32 m0, s31, 0x8400\n" \
"s_mov_b32 s85, 0x188000\n" \
"v_mfma_f32_16x16x32_bf16 a[88:91], v[124:127], v[172:175], a[88:91]\n" \
"buffer_load_dwordx4 v26, s[68:71], s85 offen lds\n" \
"v_mfma_f32_16x16x32_bf16 a[92:95], v[60:63], v[112:115], a[92:95]\n" \
"ds_read_b64_tr_b16 v[88:89], v33 offset:0\n" \
"s_movk_i32 s5, 0xfc00\n" \
"s_nop 0\n" \
"v_mfma_f32_16x16x32_bf16 a[92:95], v[124:127], v[176:179], a[92:95]\n" \
"ds_read_b128 v[60:63], v28 offset:20480\n" \
"v_add3_u32 v1, v212, v217, s5\n" \
"v_mfma_f32_16x16x32_bf16 a[124:127], v[64:67], v[112:115], a[124:127]\n" \
"ds_read_b128 v[124:127], v29 offset:20480\n" \
"v_mul_u32_u24_e32 v212, 0x480, v220\n" \
"v_mfma_f32_16x16x32_bf16 a[124:127], v[128:131], v[176:179], a[124:127]\n" \
"s_add_u32 m0, s31, 0xc400\n" \
"s_mov_b32 s84, 0x188000\n" \
"v_mfma_f32_16x16x32_bf16 a[120:123], v[64:67], v[108:111], a[120:123]\n" \
"buffer_load_dwordx4 v26, s[72:75], s84 offen lds\n" \
"v_mfma_f32_16x16x32_bf16 a[120:123], v[128:131], v[172:175], a[120:123]\n" \
"s_add_u32 m0, s30, 0x9000\n" \
"s_mov_b32 s85, 0x8180\n" \
"v_mfma_f32_16x16x32_bf16 a[116:119], v[64:67], v[104:107], a[116:119]\n" \
"buffer_load_dwordx4 v22, s[64:67], s85 offen lds\n" \
"v_mfma_f32_16x16x32_bf16 a[116:119], v[128:131], v[168:171], a[116:119]\n" \
"ds_read_b64_tr_b16 v[90:91], v33 offset:1024\n" \
"v_add3_u32 v2, v212, v219, s7\n" \
"v_mfma_f32_16x16x32_bf16 a[112:115], v[64:67], v[100:103], a[112:115]\n" \
"ds_read_b64_tr_b16 v[152:153], v33 offset:8192\n" \
"v_mul_u32_u24_e32 v212, 0x480, v222\n" \
"v_mfma_f32_16x16x32_bf16 a[112:115], v[128:131], v[164:167], a[112:115]\n" \
"ds_read_b128 v[64:67], v28 offset:22528\n" \
"v_add3_u32 v3, v212, v221, s40\n" \
"v_mfma_f32_16x16x32_bf16 a[144:147], v[68:71], v[100:103], a[144:147]\n" \
"ds_read_b128 v[128:131], v29 offset:22528\n" \
"v_mul_u32_u24_e32 v212, 0x480, v226\n" \
"v_mfma_f32_16x16x32_bf16 a[144:147], v[132:135], v[164:167], a[144:147]\n" \
"s_add_u32 m0, s30, 0x9400\n" \
"s_mov_b32 s84, 0xa180\n" \
"v_mfma_f32_16x16x32_bf16 a[148:151], v[68:71], v[104:107], a[148:151]\n" \
"buffer_load_dwordx4 v23, s[64:67], s84 offen lds\n" \
"v_mfma_f32_16x16x32_bf16 a[148:151], v[132:135], v[168:171], a[148:151]\n" \
"s_add_u32 m0, s31, 0x8800\n" \
"s_mov_b32 s85, 0x190000\n" \
"v_mfma_f32_16x16x32_bf16 a[152:155], v[68:71], v[108:111], a[152:155]\n" \
"buffer_load_dwordx4 v27, s[68:71], s85 offen lds\n" \
"v_mfma_f32_16x16x32_bf16 a[152:155], v[132:135], v[172:175], a[152:155]\n" \
"s_add_u32 m0, s31, 0xc800\n" \
"s_mov_b32 s84, 0x190000\n" \
"v_mfma_f32_16x16x32_bf16 a[156:159], v[68:71], v[112:115], a[156:159]\n" \
"buffer_load_dwordx4 v27, s[72:75], s84 offen lds\n" \
"v_mfma_f32_16x16x32_bf16 a[156:159], v[132:135], v[176:179], a[156:159]\n" \
"ds_read_b128 v[68:71], v28 offset:0\n" \
"v_add3_u32 v5, v212, v225, s5\n" \
"v_mfma_f32_16x16x32_bf16 a[188:191], v[72:75], v[112:115], a[188:191]\n" \
"ds_read_b128 v[132:135], v29 offset:0\n" \
"v_mul_u32_u24_e32 v212, 0x480, v228\n" \
"v_mfma_f32_16x16x32_bf16 a[188:191], v[136:139], v[176:179], a[188:191]\n" \
"s_add_u32 m0, s30, 0x9800\n" \
"s_mov_b32 s85, 0xc180\n" \
"v_mfma_f32_16x16x32_bf16 a[184:187], v[72:75], v[108:111], a[184:187]\n" \
"buffer_load_dwordx4 v22, s[64:67], s85 offen lds\n" \
"v_mfma_f32_16x16x32_bf16 a[184:187], v[136:139], v[172:175], a[184:187]\n" \
"ds_read_b64_tr_b16 v[154:155], v33 offset:9216\n" \
"v_add3_u32 v6, v212, v227, s7\n" \
"v_mfma_f32_16x16x32_bf16 a[180:183], v[72:75], v[104:107], a[180:183]\n" \
"s_add_u32 m0, s30, 0x9c00\n" \
"s_mov_b32 s84, 0xe180\n" \
"v_mfma_f32_16x16x32_bf16 a[180:183], v[136:139], v[168:171], a[180:183]\n" \
"buffer_load_dwordx4 v23, s[64:67], s84 offen lds\n" \
"v_mfma_f32_16x16x32_bf16 a[176:179], v[72:75], v[100:103], a[176:179]\n" \
"ds_read_b64_tr_b16 v[92:93], v34 offset:0\n" \
"v_mul_u32_u24_e32 v212, 0x480, v230\n" \
"v_mfma_f32_16x16x32_bf16 a[176:179], v[136:139], v[164:167], a[176:179]\n" \
"ds_read_b128 v[72:75], v28 offset:2048\n" \
"v_add3_u32 v7, v212, v229, s40\n" \
"v_mfma_f32_16x16x32_bf16 a[208:211], v[76:79], v[100:103], a[208:211]\n" \
"ds_read_b128 v[136:139], v29 offset:2048\n" \
"s_lshl_b32 s7, s3, 3\n" \
"v_mfma_f32_16x16x32_bf16 a[208:211], v[140:143], v[164:167], a[208:211]\n" \
"ds_read_b64_tr_b16 v[94:95], v34 offset:1024\n" \
"s_lshl_b32 s40, s3, 1\n" \
"v_mfma_f32_16x16x32_bf16 a[212:215], v[76:79], v[104:107], a[212:215]\n" \
"ds_read_b64_tr_b16 v[156:157], v34 offset:8192\n" \
"s_and_b32 s7, s7, 16\n" \
"v_mfma_f32_16x16x32_bf16 a[212:215], v[140:143], v[168:171], a[212:215]\n" \
"ds_read_b64_tr_b16 v[158:159], v34 offset:9216\n" \
"s_and_b32 s40, s40, 2\n" \
"v_mfma_f32_16x16x32_bf16 a[216:219], v[76:79], v[108:111], a[216:219]\n" \
"ds_read_b64_tr_b16 v[96:97], v35 offset:0\n" \
"s_and_b32 s5, s2, 7\n" \
"v_mfma_f32_16x16x32_bf16 a[216:219], v[140:143], v[172:175], a[216:219]\n" \
"ds_read_b64_tr_b16 v[98:99], v35 offset:1024\n" \
"v_bfe_u32 v212, v20, 7, 1\n" \
"v_mfma_f32_16x16x32_bf16 a[220:223], v[76:79], v[112:115], a[220:223]\n" \
"ds_read_b64_tr_b16 v[160:161], v35 offset:8192\n" \
"s_or_b32 s7, s40, s7\n" \
"v_mfma_f32_16x16x32_bf16 a[220:223], v[140:143], v[176:179], a[220:223]\n" \
"ds_read_b128 v[76:79], v28 offset:4096\n" \
"v_and_b32_e32 v234, 0xf0, v234\n" \
"v_mfma_f32_16x16x32_bf16 a[252:255], v[80:83], v[112:115], a[252:255]\n" \
"ds_read_b128 v[140:143], v29 offset:4096\n" \
"v_cmp_gt_u32_e64 vcc, 16, v231\n" \
"v_mfma_f32_16x16x32_bf16 a[252:255], v[144:147], v[176:179], a[252:255]\n" \
"ds_read_b64_tr_b16 v[162:163], v35 offset:9216\n" \
"v_or3_b32 v212, s7, v212, v216\n" \
"v_mfma_f32_16x16x32_bf16 a[248:251], v[80:83], v[108:111], a[248:251]\n" \
"v_and_b32_e32 v213, 32, v20\n" \
"s_lshl_b32 s5, s5, 5\n" \
"v_mfma_f32_16x16x32_bf16 a[248:251], v[144:147], v[172:175], a[248:251]\n" \
"v_cndmask_b32_e32 v231, v233, v234, vcc\n" \
"v_mul_u32_u24_e32 v212, 18, v212\n" \
"v_mfma_f32_16x16x32_bf16 a[244:247], v[80:83], v[104:107], a[244:247]\n" \
"v_mov_b32_e32 v216, 0x100\n" \
"v_mov_b32_e64 v217, s5\n" \
"v_mfma_f32_16x16x32_bf16 a[244:247], v[144:147], v[168:171], a[244:247]\n" \
"v_cmp_eq_u32_e64 vcc, 0, v213\n" \
"v_and_or_b32 v212, v215, 1, v212\n" \
"v_mfma_f32_16x16x32_bf16 a[240:243], v[80:83], v[100:103], a[240:243]\n" \
"v_mad_u32_u24 v4, v224, s4, v223\n" \
"v_cndmask_b32_e64 v213, v216, v217, vcc\n" \
"v_mfma_f32_16x16x32_bf16 a[240:243], v[144:147], v[164:167], a[240:243]\n" \
"ds_read_b128 v[80:83], v28 offset:6144\n" \
"ds_read_b128 v[144:147], v29 offset:6144\n" \
"s_waitcnt lgkmcnt(15)\n" \
"v_mfma_f32_16x16x32_bf16 a[0:3], v[52:55], v[84:87], a[0:3]\n" \
"s_add_u32 m0, s31, 0x8c00\n" \
"s_mov_b32 s85, 0x198000\n" \
"v_mfma_f32_16x16x32_bf16 a[0:3], v[116:119], v[148:151], a[0:3]\n" \
"buffer_load_dwordx4 v27, s[68:71], s85 offen lds\n" \
"v_mfma_f32_16x16x32_bf16 a[4:7], v[52:55], v[88:91], a[4:7]\n" \
"ds_read_b64_tr_b16 v[100:101], v36 offset:0\n" \
"v_mad_u32_u24 v212, v212, s4, v213\n" \
"s_waitcnt lgkmcnt(15)\n" \
"v_mfma_f32_16x16x32_bf16 a[4:7], v[116:119], v[152:155], a[4:7]\n" \
"s_add_u32 m0, s31, 0xcc00\n" \
"s_mov_b32 s84, 0x198000\n" \
"s_waitcnt lgkmcnt(11)\n" \
"v_mfma_f32_16x16x32_bf16 a[8:11], v[52:55], v[92:95], a[8:11]\n" \
"buffer_load_dwordx4 v27, s[72:75], s84 offen lds\n" \
"s_waitcnt lgkmcnt(9)\n" \
"v_mfma_f32_16x16x32_bf16 a[8:11], v[116:119], v[156:159], a[8:11]\n" \
"ds_read_b64_tr_b16 v[102:103], v36 offset:1024\n" \
"v_and_or_b32 v212, v20, 16, v212\n" \
"s_waitcnt lgkmcnt(8)\n" \
"v_mfma_f32_16x16x32_bf16 a[12:15], v[52:55], v[96:99], a[12:15]\n" \
"ds_read_b64_tr_b16 v[164:165], v36 offset:8192\n" \
"v_add_u32_e32 v9, 0x4800, v212\n" \
"s_waitcnt lgkmcnt(5)\n" \
"v_mfma_f32_16x16x32_bf16 a[12:15], v[116:119], v[160:163], a[12:15]\n" \
"v_mul_u32_u24_e32 v212, 0x120, v215\n" \
"v_lshlrev_b32_e64 v213, 3, v214\n" \
"v_mfma_f32_16x16x32_bf16 a[44:47], v[56:59], v[96:99], a[44:47]\n" \
"v_mad_u32_u24 v8, v232, s4, v231\n" \
"v_and_or_b32 v10, v213, 24, v212\n" \
"v_mfma_f32_16x16x32_bf16 a[44:47], v[120:123], v[160:163], a[44:47]\n" \
"ds_read_b64_tr_b16 v[166:167], v36 offset:9216\n" \
"s_lshl_b32 s4, s6, 6\n" \
"v_mfma_f32_16x16x32_bf16 a[40:43], v[56:59], v[92:95], a[40:43]\n" \
"buffer_load_dwordx4 v[200:203], v192, s[76:79], s34 offen offset:0\n" \
"v_mfma_f32_16x16x32_bf16 a[40:43], v[120:123], v[156:159], a[40:43]\n" \
"v_lshrrev_b32_e32 v212, 2, v20\n" \
"v_and_or_b32 v212, v212, 60, s4\n" \
"v_mfma_f32_16x16x32_bf16 a[36:39], v[56:59], v[88:91], a[36:39]\n" \
"v_or_b32_e32 v18, 0x27000, v212\n" \
"v_lshl_or_b32 v212, s3, 9, v213\n" \
"v_mfma_f32_16x16x32_bf16 a[36:39], v[120:123], v[152:155], a[36:39]\n" \
"ds_read_b64_tr_b16 v[104:105], v37 offset:0\n" \
"v_or_b32_e32 v19, 0x27800, v212\n" \
"v_mfma_f32_16x16x32_bf16 a[32:35], v[56:59], v[84:87], a[32:35]\n" \
"v_mov_b32_e32 v219, 0x1000c0c\n" \
"v_mov_b32_e32 v220, 0xc0c0100\n" \
"v_mfma_f32_16x16x32_bf16 a[32:35], v[120:123], v[148:151], a[32:35]\n" \
"v_bfe_u32 v218, v20, 4, 1\n" \
"v_lshrrev_b32_e64 v213, 8, v20\n" \
"v_mfma_f32_16x16x32_bf16 a[64:67], v[60:63], v[84:87], a[64:67]\n" \
"buffer_load_dwordx4 v[204:207], v193, s[76:79], s34 offen offset:0\n" \
"v_mfma_f32_16x16x32_bf16 a[64:67], v[124:127], v[148:151], a[64:67]\n" \
"s_mov_b32 m0, s32\n" \
"ds_read_b64_tr_b16 v[106:107], v37 offset:1024\n" \
"v_mfma_f32_16x16x32_bf16 a[68:71], v[60:63], v[88:91], a[68:71]\n" \
"s_mov_b32 exec_lo, 0x0\n" \
"s_mov_b32 exec_hi, 0xff00\n" \
"global_load_lds_dword v[184:185], off\n" \
"s_mov_b64 exec, -1\n" \
"v_mfma_f32_16x16x32_bf16 a[68:71], v[124:127], v[152:155], a[68:71]\n" \
"ds_read_b64_tr_b16 v[168:169], v37 offset:8192\n" \
"v_cmp_eq_u32_e32 vcc, 0, v218\n" \
"v_mfma_f32_16x16x32_bf16 a[72:75], v[60:63], v[92:95], a[72:75]\n" \
"s_lshr_b32 s4, s2, 5\n" \
"s_and_b32 s5, s3, 1\n" \
"v_mfma_f32_16x16x32_bf16 a[72:75], v[124:127], v[156:159], a[72:75]\n" \
"buffer_load_dword v208, v194, s[76:79], s34 offen offset:32\n" \
"v_mfma_f32_16x16x32_bf16 a[76:79], v[60:63], v[96:99], a[76:79]\n" \
"ds_read_b64_tr_b16 v[170:171], v37 offset:9216\n" \
"v_bfe_u32 v217, v20, 4, 4\n" \
"v_mfma_f32_16x16x32_bf16 a[76:79], v[124:127], v[160:163], a[76:79]\n" \
"v_cndmask_b32_e64 v219, v219, v220, vcc\n" \
"v_mov_b32_e32 v220, 0x3020c0c\n" \
"v_mfma_f32_16x16x32_bf16 a[108:111], v[64:67], v[96:99], a[108:111]\n" \
"v_mov_b32_e32 v221, 0xc0c0302\n" \
"v_lshl_or_b32 v218, v213, 2, v218\n" \
"v_mfma_f32_16x16x32_bf16 a[108:111], v[128:131], v[160:163], a[108:111]\n" \
"s_xor_b32 s6, s4, 15\n" \
"v_mfma_f32_16x16x32_bf16 a[104:107], v[64:67], v[92:95], a[104:107]\n" \
"buffer_load_dword v209, v194, s[76:79], s34 offen offset:1184\n" \
"v_mfma_f32_16x16x32_bf16 a[104:107], v[128:131], v[156:159], a[104:107]\n" \
"ds_read_b64_tr_b16 v[108:109], v38 offset:0\n" \
"v_mfma_f32_16x16x32_bf16 a[100:103], v[64:67], v[88:91], a[100:103]\n" \
"s_cmpk_lt_u32 s3, 2\n" \
"s_cselect_b32 s7, s6, s4\n" \
"v_mfma_f32_16x16x32_bf16 a[100:103], v[128:131], v[152:155], a[100:103]\n" \
"ds_read_b64_tr_b16 v[110:111], v38 offset:1024\n" \
"s_lshl_b32 s40, s7, 7\n" \
"v_mfma_f32_16x16x32_bf16 a[96:99], v[64:67], v[84:87], a[96:99]\n" \
"s_lshl_b32 s41, s5, 6\n" \
"v_cndmask_b32_e32 v220, v220, v221, vcc\n" \
"v_mfma_f32_16x16x32_bf16 a[96:99], v[128:131], v[148:151], a[96:99]\n" \
"v_mov_b32_e32 v221, 0xc0c0c0c\n" \
"v_cmp_gt_u32_e64 vcc, v218, v217\n" \
"v_mfma_f32_16x16x32_bf16 a[128:131], v[68:71], v[84:87], a[128:131]\n" \
"buffer_load_dword v211, v194, s[76:79], s34 offen offset:2336\n" \
"v_mfma_f32_16x16x32_bf16 a[128:131], v[132:135], v[148:151], a[128:131]\n" \
"ds_read_b64_tr_b16 v[172:173], v38 offset:8192\n" \
"v_or_b32_e64 v218, 2, v218\n" \
"v_mfma_f32_16x16x32_bf16 a[132:135], v[68:71], v[88:91], a[132:135]\n" \
"v_lshrrev_b32_e32 v212, 7, v20\n" \
"s_or_b32 s41, s41, s40\n" \
"v_mfma_f32_16x16x32_bf16 a[132:135], v[132:135], v[152:155], a[132:135]\n" \
"ds_read_b64_tr_b16 v[174:175], v38 offset:9216\n" \
"v_cndmask_b32_e64 v40, v221, v219, vcc\n" \
"v_mfma_f32_16x16x32_bf16 a[136:139], v[68:71], v[92:95], a[136:139]\n" \
"v_cndmask_b32_e32 v42, v219, v221, vcc\n" \
"v_cmp_gt_u32_e32 vcc, v218, v217\n" \
"v_mfma_f32_16x16x32_bf16 a[136:139], v[132:135], v[156:159], a[136:139]\n" \
"s_mov_b32 s76, s20\n" \
"s_and_b32 s77, s21, 0xffff\n" \
"v_mfma_f32_16x16x32_bf16 a[140:143], v[68:71], v[96:99], a[140:143]\n" \
"s_mov_b32 s78, 0x480\n" \
"v_and_b32_e32 v218, 16, v20\n" \
"v_mfma_f32_16x16x32_bf16 a[140:143], v[132:135], v[160:163], a[140:143]\n" \
"buffer_load_ushort v210, v195, s[76:79], 0 offen\n" \
"v_mfma_f32_16x16x32_bf16 a[172:175], v[72:75], v[96:99], a[172:175]\n" \
"ds_read_b64_tr_b16 v[112:113], v39 offset:0\n" \
"s_movk_i32 s42, 0x3f80\n" \
"v_mfma_f32_16x16x32_bf16 a[172:175], v[136:139], v[160:163], a[172:175]\n" \
"v_or_b32_e32 v214, s41, v212\n" \
"v_and_b32_e32 v217, 0x320, v20\n" \
"v_mfma_f32_16x16x32_bf16 a[168:171], v[72:75], v[92:95], a[168:171]\n" \
"s_mov_b32 s76, s8\n" \
"s_and_b32 s77, s9, 0xffff\n" \
"v_mfma_f32_16x16x32_bf16 a[168:171], v[136:139], v[156:159], a[168:171]\n" \
"s_mov_b32 s78, 0x240000\n" \
"ds_read_b64_tr_b16 v[114:115], v39 offset:1024\n" \
"v_mfma_f32_16x16x32_bf16 a[164:167], v[72:75], v[88:91], a[164:167]\n" \
"s_movk_i32 s41, 0x100\n" \
"v_lshlrev_b32_e64 v218, v218, s42\n" \
"v_mfma_f32_16x16x32_bf16 a[164:167], v[136:139], v[152:155], a[164:167]\n" \
"ds_read_b64_tr_b16 v[176:177], v39 offset:8192\n" \
"v_mov_b32_e64 v219, 0\n" \
"v_mfma_f32_16x16x32_bf16 a[160:163], v[72:75], v[84:87], a[160:163]\n" \
"v_lshrrev_b32_e32 v215, 6, v20\n" \
"v_cndmask_b32_e32 v41, v221, v220, vcc\n" \
"v_mfma_f32_16x16x32_bf16 a[160:163], v[136:139], v[148:151], a[160:163]\n" \
"v_cndmask_b32_e32 v43, v220, v221, vcc\n" \
"v_cmp_eq_u32_e32 vcc, s41, v217\n" \
"v_mfma_f32_16x16x32_bf16 a[192:195], v[76:79], v[84:87], a[192:195]\n" \
"ds_read_b64_tr_b16 v[178:179], v39 offset:9216\n" \
"v_and_b32_e32 v217, 0x330, v20\n" \
"v_mfma_f32_16x16x32_bf16 a[192:195], v[140:143], v[148:151], a[192:195]\n" \
"v_bfe_u32 v216, v20, 6, 2\n" \
"v_cndmask_b32_e64 v46, v219, v218, vcc\n" \
"v_mfma_f32_16x16x32_bf16 a[196:199], v[76:79], v[88:91], a[196:199]\n" \
"v_mov_b32_e32 v218, 0x3f80\n" \
"v_cmp_eq_u32_e64 vcc, 32, v217\n" \
"v_mfma_f32_16x16x32_bf16 a[196:199], v[140:143], v[152:155], a[196:199]\n" \
"s_add_i32 s41, s40, -16\n" \
"v_lshrrev_b32_e32 v219, 1, v20\n" \
"v_mfma_f32_16x16x32_bf16 a[200:203], v[76:79], v[92:95], a[200:203]\n" \
"v_bfe_u32 v215, v215, 1, 1\n" \
"v_cndmask_b32_e64 v47, 0, v218, vcc\n" \
"v_mfma_f32_16x16x32_bf16 a[200:203], v[140:143], v[156:159], a[200:203]\n" \
"s_mov_b32 s42, 0\n" \
"v_mfma_f32_16x16x32_bf16 a[204:207], v[76:79], v[96:99], a[204:207]\n" \
"s_cmp_lg_u32 s40, 0\n" \
"s_cselect_b32 s41, s41, s42\n" \
"v_mfma_f32_16x16x32_bf16 a[204:207], v[140:143], v[160:163], a[204:207]\n" \
"v_lshl_or_b32 v217, s5, 3, v212\n" \
"v_lshlrev_b32_e32 v218, 10, v213\n" \
"s_waitcnt lgkmcnt(15)\n" \
"v_mfma_f32_16x16x32_bf16 a[236:239], v[80:83], v[96:99], a[236:239]\n" \
"v_lshlrev_b32_e32 v216, 7, v216\n" \
"v_and_b32_e64 v219, 24, v219\n" \
"v_mfma_f32_16x16x32_bf16 a[236:239], v[144:147], v[160:163], a[236:239]\n" \
"v_and_or_b32 v212, v212, 2, v215\n" \
"v_add_u32_e64 v217, s41, v217\n" \
"v_mfma_f32_16x16x32_bf16 a[232:235], v[80:83], v[92:95], a[232:235]\n" \
"v_or3_b32 v216, v216, v218, v219\n" \
"v_lshlrev_b32_e64 v212, 5, v212\n" \
"v_mfma_f32_16x16x32_bf16 a[232:235], v[144:147], v[156:159], a[232:235]\n" \
"s_mov_b32 s41, 0x14000\n" \
"v_or3_b32 v48, v216, v212, s41\n" \
"v_mfma_f32_16x16x32_bf16 a[228:231], v[80:83], v[88:91], a[228:231]\n" \
"s_lshl_b32 s41, s5, 2\n" \
"s_lshl_b32 s7, s7, 3\n" \
"v_mfma_f32_16x16x32_bf16 a[228:231], v[144:147], v[152:155], a[228:231]\n" \
"v_or_b32_e32 v212, s41, v213\n" \
"s_or_b32 s7, s7, s41\n" \
"v_mfma_f32_16x16x32_bf16 a[224:227], v[80:83], v[84:87], a[224:227]\n" \
"s_mov_b32 s41, 0xff800000\n" \
"v_mfma_f32_16x16x32_bf16 a[224:227], v[144:147], v[148:151], a[224:227]\n" \
"s_waitcnt vmcnt(7) lgkmcnt(0)\n" \
"s_barrier\n" \
"v_mfma_f32_16x16x32_bf16 a[16:19], v[52:55], v[100:103], a[16:19]\n" \
"s_add_u32 m0, s30, 0x0\n" \
"s_nop 0\n" \
"s_mov_b32 s85, 0x200\n" \
"v_mfma_f32_16x16x32_bf16 a[16:19], v[116:119], v[164:167], a[16:19]\n" \
"buffer_load_dwordx4 v22, s[64:67], s85 offen lds\n" \
"v_mfma_f32_16x16x32_bf16 a[20:23], v[52:55], v[104:107], a[20:23]\n" \
"ds_read_b64_tr_b16 v[84:85], v32 offset:32768\n" \
"v_mfma_f32_16x16x32_bf16 a[20:23], v[116:119], v[168:171], a[20:23]\n" \
"s_add_u32 m0, s30, 0x400\n" \
"s_mov_b32 s84, 0x2200\n" \
"v_mfma_f32_16x16x32_bf16 a[24:27], v[52:55], v[108:111], a[24:27]\n" \
"buffer_load_dwordx4 v23, s[64:67], s84 offen lds\n" \
"v_mfma_f32_16x16x32_bf16 a[24:27], v[116:119], v[172:175], a[24:27]\n" \
"ds_read_b64_tr_b16 v[86:87], v32 offset:33792\n" \
"v_mfma_f32_16x16x32_bf16 a[28:31], v[52:55], v[112:115], a[28:31]\n" \
"ds_read_b64_tr_b16 v[148:149], v32 offset:40960\n" \
"v_mfma_f32_16x16x32_bf16 a[28:31], v[116:119], v[176:179], a[28:31]\n" \
"ds_read_b128 v[52:55], v28 offset:49152\n" \
"v_mfma_f32_16x16x32_bf16 a[60:63], v[56:59], v[112:115], a[60:63]\n" \
"ds_read_b128 v[116:119], v29 offset:49152\n" \
"v_mfma_f32_16x16x32_bf16 a[60:63], v[120:123], v[176:179], a[60:63]\n" \
"s_add_u32 m0, s31, 0x0\n" \
"s_nop 0\n" \
"s_mov_b32 s85, 0x200000\n" \
"v_mfma_f32_16x16x32_bf16 a[56:59], v[56:59], v[108:111], a[56:59]\n" \
"buffer_load_dwordx4 v26, s[68:71], s85 offen lds\n" \
"v_mfma_f32_16x16x32_bf16 a[56:59], v[120:123], v[172:175], a[56:59]\n" \
"s_add_u32 m0, s31, 0x4000\n" \
"s_mov_b32 s84, 0x200000\n" \
"v_mfma_f32_16x16x32_bf16 a[52:55], v[56:59], v[104:107], a[52:55]\n" \
"buffer_load_dwordx4 v26, s[72:75], s84 offen lds\n" \
"v_mfma_f32_16x16x32_bf16 a[52:55], v[120:123], v[168:171], a[52:55]\n" \
"s_add_u32 m0, s30, 0x800\n" \
"s_mov_b32 s85, 0x4200\n" \
"v_mfma_f32_16x16x32_bf16 a[48:51], v[56:59], v[100:103], a[48:51]\n" \
"buffer_load_dwordx4 v22, s[64:67], s85 offen lds\n" \
"v_mfma_f32_16x16x32_bf16 a[48:51], v[120:123], v[164:167], a[48:51]\n" \
"ds_read_b128 v[56:59], v28 offset:51200\n" \
"v_mfma_f32_16x16x32_bf16 a[80:83], v[60:63], v[100:103], a[80:83]\n" \
"ds_read_b128 v[120:123], v29 offset:51200\n" \
"v_mfma_f32_16x16x32_bf16 a[80:83], v[124:127], v[164:167], a[80:83]\n" \
"s_add_u32 m0, s30, 0xc00\n" \
"s_mov_b32 s84, 0x6200\n" \
"v_mfma_f32_16x16x32_bf16 a[84:87], v[60:63], v[104:107], a[84:87]\n" \
"buffer_load_dwordx4 v23, s[64:67], s84 offen lds\n" \
"v_mfma_f32_16x16x32_bf16 a[84:87], v[124:127], v[168:171], a[84:87]\n" \
"ds_read_b64_tr_b16 v[150:151], v32 offset:41984\n" \
"v_mfma_f32_16x16x32_bf16 a[88:91], v[60:63], v[108:111], a[88:91]\n" \
"s_add_u32 m0, s31, 0x400\n" \
"s_mov_b32 s85, 0x208000\n" \
"v_mfma_f32_16x16x32_bf16 a[88:91], v[124:127], v[172:175], a[88:91]\n" \
"buffer_load_dwordx4 v26, s[68:71], s85 offen lds\n" \
"v_mfma_f32_16x16x32_bf16 a[92:95], v[60:63], v[112:115], a[92:95]\n" \
"ds_read_b64_tr_b16 v[88:89], v33 offset:32768\n" \
"v_mfma_f32_16x16x32_bf16 a[92:95], v[124:127], v[176:179], a[92:95]\n" \
"ds_read_b128 v[60:63], v28 offset:53248\n" \
"v_mfma_f32_16x16x32_bf16 a[124:127], v[64:67], v[112:115], a[124:127]\n" \
"ds_read_b128 v[124:127], v29 offset:53248\n" \
"v_mfma_f32_16x16x32_bf16 a[124:127], v[128:131], v[176:179], a[124:127]\n" \
"s_add_u32 m0, s31, 0x4400\n" \
"s_mov_b32 s84, 0x208000\n" \
"v_mfma_f32_16x16x32_bf16 a[120:123], v[64:67], v[108:111], a[120:123]\n" \
"buffer_load_dwordx4 v26, s[72:75], s84 offen lds\n" \
"v_mfma_f32_16x16x32_bf16 a[120:123], v[128:131], v[172:175], a[120:123]\n" \
"s_add_u32 m0, s30, 0x1000\n" \
"s_mov_b32 s85, 0x8200\n" \
"v_mfma_f32_16x16x32_bf16 a[116:119], v[64:67], v[104:107], a[116:119]\n" \
"buffer_load_dwordx4 v22, s[64:67], s85 offen lds\n" \
"v_mfma_f32_16x16x32_bf16 a[116:119], v[128:131], v[168:171], a[116:119]\n" \
"ds_read_b64_tr_b16 v[90:91], v33 offset:33792\n" \
"v_mfma_f32_16x16x32_bf16 a[112:115], v[64:67], v[100:103], a[112:115]\n" \
"ds_read_b64_tr_b16 v[152:153], v33 offset:40960\n" \
"v_mfma_f32_16x16x32_bf16 a[112:115], v[128:131], v[164:167], a[112:115]\n" \
"ds_read_b128 v[64:67], v28 offset:55296\n" \
"v_mfma_f32_16x16x32_bf16 a[144:147], v[68:71], v[100:103], a[144:147]\n" \
"ds_read_b128 v[128:131], v29 offset:55296\n" \
"v_mfma_f32_16x16x32_bf16 a[144:147], v[132:135], v[164:167], a[144:147]\n" \
"s_add_u32 m0, s30, 0x1400\n" \
"s_mov_b32 s84, 0xa200\n" \
"v_mfma_f32_16x16x32_bf16 a[148:151], v[68:71], v[104:107], a[148:151]\n" \
"buffer_load_dwordx4 v23, s[64:67], s84 offen lds\n" \
"v_mfma_f32_16x16x32_bf16 a[148:151], v[132:135], v[168:171], a[148:151]\n" \
"s_add_u32 m0, s31, 0x800\n" \
"s_mov_b32 s85, 0x210000\n" \
"v_mfma_f32_16x16x32_bf16 a[152:155], v[68:71], v[108:111], a[152:155]\n" \
"buffer_load_dwordx4 v27, s[68:71], s85 offen lds\n" \
"v_mfma_f32_16x16x32_bf16 a[152:155], v[132:135], v[172:175], a[152:155]\n" \
"s_add_u32 m0, s31, 0x4800\n" \
"s_mov_b32 s84, 0x210000\n" \
"v_mfma_f32_16x16x32_bf16 a[156:159], v[68:71], v[112:115], a[156:159]\n" \
"buffer_load_dwordx4 v27, s[72:75], s84 offen lds\n" \
"v_mfma_f32_16x16x32_bf16 a[156:159], v[132:135], v[176:179], a[156:159]\n" \
"ds_read_b128 v[68:71], v28 offset:32768\n" \
"v_mfma_f32_16x16x32_bf16 a[188:191], v[72:75], v[112:115], a[188:191]\n" \
"ds_read_b128 v[132:135], v29 offset:32768\n" \
"v_mfma_f32_16x16x32_bf16 a[188:191], v[136:139], v[176:179], a[188:191]\n" \
"s_add_u32 m0, s30, 0x1800\n" \
"s_mov_b32 s85, 0xc200\n" \
"v_mfma_f32_16x16x32_bf16 a[184:187], v[72:75], v[108:111], a[184:187]\n" \
"buffer_load_dwordx4 v22, s[64:67], s85 offen lds\n" \
"v_mfma_f32_16x16x32_bf16 a[184:187], v[136:139], v[172:175], a[184:187]\n" \
"ds_read_b64_tr_b16 v[154:155], v33 offset:41984\n" \
"v_mfma_f32_16x16x32_bf16 a[180:183], v[72:75], v[104:107], a[180:183]\n" \
"s_add_u32 m0, s30, 0x1c00\n" \
"s_mov_b32 s84, 0xe200\n" \
"v_mfma_f32_16x16x32_bf16 a[180:183], v[136:139], v[168:171], a[180:183]\n" \
"buffer_load_dwordx4 v23, s[64:67], s84 offen lds\n" \
"v_mfma_f32_16x16x32_bf16 a[176:179], v[72:75], v[100:103], a[176:179]\n" \
"ds_read_b64_tr_b16 v[92:93], v34 offset:32768\n" \
"v_mfma_f32_16x16x32_bf16 a[176:179], v[136:139], v[164:167], a[176:179]\n" \
"ds_read_b128 v[72:75], v28 offset:34816\n" \
"v_mfma_f32_16x16x32_bf16 a[208:211], v[76:79], v[100:103], a[208:211]\n" \
"ds_read_b128 v[136:139], v29 offset:34816\n" \
"v_mfma_f32_16x16x32_bf16 a[208:211], v[140:143], v[164:167], a[208:211]\n" \
"ds_read_b64_tr_b16 v[94:95], v34 offset:33792\n" \
"v_mfma_f32_16x16x32_bf16 a[212:215], v[76:79], v[104:107], a[212:215]\n" \
"ds_read_b64_tr_b16 v[156:157], v34 offset:40960\n" \
"v_mfma_f32_16x16x32_bf16 a[212:215], v[140:143], v[168:171], a[212:215]\n" \
"ds_read_b64_tr_b16 v[158:159], v34 offset:41984\n" \
"v_mfma_f32_16x16x32_bf16 a[216:219], v[76:79], v[108:111], a[216:219]\n" \
"ds_read_b64_tr_b16 v[96:97], v35 offset:32768\n" \
"v_mfma_f32_16x16x32_bf16 a[216:219], v[140:143], v[172:175], a[216:219]\n" \
"ds_read_b64_tr_b16 v[98:99], v35 offset:33792\n" \
"v_mfma_f32_16x16x32_bf16 a[220:223], v[76:79], v[112:115], a[220:223]\n" \
"ds_read_b64_tr_b16 v[160:161], v35 offset:40960\n" \
"v_mfma_f32_16x16x32_bf16 a[220:223], v[140:143], v[176:179], a[220:223]\n" \
"ds_read_b128 v[76:79], v28 offset:36864\n" \
"v_mfma_f32_16x16x32_bf16 a[252:255], v[80:83], v[112:115], a[252:255]\n" \
"ds_read_b128 v[140:143], v29 offset:36864\n" \
"v_mfma_f32_16x16x32_bf16 a[252:255], v[144:147], v[176:179], a[252:255]\n" \
"ds_read_b64_tr_b16 v[162:163], v35 offset:41984\n" \
"v_mfma_f32_16x16x32_bf16 a[248:251], v[80:83], v[108:111], a[248:251]\n" \
"s_cmp_lg_u32 s40, 0\n" \
"s_cselect_b32 s47, s42, s41\n" \
"v_mfma_f32_16x16x32_bf16 a[248:251], v[144:147], v[172:175], a[248:251]\n" \
"s_mov_b32 s40, 0xc000\n" \
"s_mov_b32 s41, 0x8000\n" \
"v_mfma_f32_16x16x32_bf16 a[244:247], v[80:83], v[104:107], a[244:247]\n" \
"s_cmp_lg_u32 s4, 0\n" \
"s_cselect_b32 s51, s40, s41\n" \
"v_mfma_f32_16x16x32_bf16 a[244:247], v[144:147], v[168:171], a[244:247]\n" \
"s_movk_i32 s40, 0x4000\n" \
"v_mfma_f32_16x16x32_bf16 a[240:243], v[80:83], v[100:103], a[240:243]\n" \
"s_cmp_lg_u32 s4, 0\n" \
"s_cselect_b32 s53, s41, s40\n" \
"v_mfma_f32_16x16x32_bf16 a[240:243], v[144:147], v[164:167], a[240:243]\n" \
"ds_read_b128 v[80:83], v28 offset:38912\n" \
"ds_read_b128 v[144:147], v29 offset:38912\n" \
"s_waitcnt lgkmcnt(15)\n" \
"v_mfma_f32_16x16x32_bf16 a[0:3], v[52:55], v[84:87], a[0:3]\n" \
"s_add_u32 m0, s31, 0xc00\n" \
"s_mov_b32 s85, 0x218000\n" \
"v_mfma_f32_16x16x32_bf16 a[0:3], v[116:119], v[148:151], a[0:3]\n" \
"buffer_load_dwordx4 v27, s[68:71], s85 offen lds\n" \
"v_mfma_f32_16x16x32_bf16 a[4:7], v[52:55], v[88:91], a[4:7]\n" \
"ds_read_b64_tr_b16 v[100:101], v36 offset:32768\n" \
"s_lshl_b32 s40, s2, 2\n" \
"s_waitcnt lgkmcnt(15)\n" \
"v_mfma_f32_16x16x32_bf16 a[4:7], v[116:119], v[152:155], a[4:7]\n" \
"s_add_u32 m0, s31, 0x4c00\n" \
"s_mov_b32 s84, 0x218000\n" \
"s_waitcnt lgkmcnt(11)\n" \
"v_mfma_f32_16x16x32_bf16 a[8:11], v[52:55], v[92:95], a[8:11]\n" \
"buffer_load_dwordx4 v27, s[72:75], s84 offen lds\n" \
"s_waitcnt lgkmcnt(9)\n" \
"v_mfma_f32_16x16x32_bf16 a[8:11], v[116:119], v[156:159], a[8:11]\n" \
"ds_read_b64_tr_b16 v[102:103], v36 offset:33792\n" \
"s_and_b32 s40, s40, 28\n" \
"s_waitcnt lgkmcnt(8)\n" \
"v_mfma_f32_16x16x32_bf16 a[12:15], v[52:55], v[96:99], a[12:15]\n" \
"ds_read_b64_tr_b16 v[164:165], v36 offset:40960\n" \
"s_bfe_u32 s45, s2, 0x20003\n" \
"s_waitcnt lgkmcnt(5)\n" \
"v_mfma_f32_16x16x32_bf16 a[12:15], v[116:119], v[160:163], a[12:15]\n" \
"s_lshr_b32 s44, s3, 1\n" \
"s_or_b32 s46, s40, s45\n" \
"v_mfma_f32_16x16x32_bf16 a[44:47], v[56:59], v[96:99], a[44:47]\n" \
"s_lshl_b32 s43, s44, 15\n" \
"s_lshl_b32 s41, s5, 14\n" \
"v_mfma_f32_16x16x32_bf16 a[44:47], v[120:123], v[160:163], a[44:47]\n" \
"ds_read_b64_tr_b16 v[166:167], v36 offset:41984\n" \
"s_lshl_b32 s48, s46, 20\n" \
"v_mfma_f32_16x16x32_bf16 a[40:43], v[56:59], v[92:95], a[40:43]\n" \
"s_or_b32 s41, s43, s41\n" \
"s_add_i32 s49, s48, 0x1000000\n" \
"v_mfma_f32_16x16x32_bf16 a[40:43], v[120:123], v[156:159], a[40:43]\n" \
"s_or_b32 s42, s49, s41\n" \
"s_lshl_b32 s4, s4, 16\n" \
"v_mfma_f32_16x16x32_bf16 a[36:39], v[56:59], v[88:91], a[36:39]\n" \
"s_or_b32 s41, s42, s4\n" \
"s_lshl_b32 s4, s6, 16\n" \
"v_mfma_f32_16x16x32_bf16 a[36:39], v[120:123], v[152:155], a[36:39]\n" \
"ds_read_b64_tr_b16 v[104:105], v37 offset:32768\n" \
"s_or_b32 s42, s42, s4\n" \
"v_mfma_f32_16x16x32_bf16 a[32:35], v[56:59], v[84:87], a[32:35]\n" \
"s_lshl_b32 s4, s5, 11\n" \
"s_mul_i32 s5, s7, 0x24800\n" \
"v_mfma_f32_16x16x32_bf16 a[32:35], v[120:123], v[148:151], a[32:35]\n" \
"s_lshl_b32 s6, s46, 9\n" \
"s_lshl_b32 s7, s40, 6\n" \
"v_mfma_f32_16x16x32_bf16 a[64:67], v[60:63], v[84:87], a[64:67]\n" \
"s_waitcnt vmcnt(16)\n" \
"v_or_b32_e32 v204, v210, v204\n" \
"v_or_b32_e32 v206, v210, v206\n" \
"v_mfma_f32_16x16x32_bf16 a[64:67], v[124:127], v[148:151], a[64:67]\n" \
"v_mov_b32_e64 v210, 0\n" \
"s_add_u32 s88, s35, 0x1000\n" \
"v_mfma_f32_16x16x32_bf16 a[68:71], v[60:63], v[88:91], a[68:71]\n" \
"ds_read_b64_tr_b16 v[106:107], v37 offset:33792\n" \
"s_add_i32 s6, s5, s6\n" \
"v_mfma_f32_16x16x32_bf16 a[68:71], v[124:127], v[152:155], a[68:71]\n" \
"ds_read_b64_tr_b16 v[168:169], v37 offset:40960\n" \
"s_or_b32 s5, s5, s7\n" \
"v_mfma_f32_16x16x32_bf16 a[72:75], v[60:63], v[92:95], a[72:75]\n" \
"s_mov_b32 m0, s32\n" \
"s_lshl_b32 s7, s45, 2\n" \
"v_mfma_f32_16x16x32_bf16 a[72:75], v[124:127], v[156:159], a[72:75]\n" \
"s_mov_b32 exec_lo, 0xffffffff\n" \
"s_mov_b32 exec_hi, 0x0\n" \
"global_load_lds_dword v[188:189], off\n" \
"s_mov_b64 exec, -1\n" \
"v_mfma_f32_16x16x32_bf16 a[76:79], v[60:63], v[96:99], a[76:79]\n" \
"ds_read_b64_tr_b16 v[170:171], v37 offset:41984\n" \
"s_or_b32 s5, s5, s7\n" \
"v_mfma_f32_16x16x32_bf16 a[76:79], v[124:127], v[160:163], a[76:79]\n" \
"s_lshl_b32 s7, s3, 12\n" \
"s_or_b32 s50, s49, s7\n" \
"v_mfma_f32_16x16x32_bf16 a[108:111], v[64:67], v[96:99], a[108:111]\n" \
"ds_write_b128 v196, v[200:203]\n" \
"v_mfma_f32_16x16x32_bf16 a[108:111], v[128:131], v[160:163], a[108:111]\n" \
"s_lshl_b32 s7, s3, 11\n" \
"v_lshrrev_b32_e32 v215, 4, v20\n" \
"v_mfma_f32_16x16x32_bf16 a[104:107], v[64:67], v[92:95], a[104:107]\n" \
"ds_read_b64_tr_b16 v[108:109], v38 offset:32768\n" \
"v_bfe_u32 v216, v20, 4, 3\n" \
"v_mfma_f32_16x16x32_bf16 a[104:107], v[128:131], v[156:159], a[104:107]\n" \
"s_add_i32 s52, s48, s7\n" \
"s_add_i32 s53, s53, s7\n" \
"v_mfma_f32_16x16x32_bf16 a[100:103], v[64:67], v[88:91], a[100:103]\n" \
"v_mul_u32_u24_e32 v214, 0x480, v214\n" \
"v_bitop3_b32 v218, v213, v215, 7 bitop3:0x78\n" \
"v_mfma_f32_16x16x32_bf16 a[100:103], v[128:131], v[152:155], a[100:103]\n" \
"ds_read_b64_tr_b16 v[110:111], v38 offset:33792\n" \
"v_bitop3_b32 v213, v213, v216, 4 bitop3:0x36\n" \
"v_mfma_f32_16x16x32_bf16 a[96:99], v[64:67], v[84:87], a[96:99]\n" \
"s_lshl_b32 s7, s3, 10\n" \
"s_or_b32 s4, s43, s4\n" \
"v_mfma_f32_16x16x32_bf16 a[96:99], v[128:131], v[148:151], a[96:99]\n" \
"v_lshl_or_b32 v25, v213, 4, v214\n" \
"v_mul_u32_u24_e32 v213, 0x480, v217\n" \
"v_mfma_f32_16x16x32_bf16 a[128:131], v[68:71], v[84:87], a[128:131]\n" \
"ds_write_b128 v197, v[204:207]\n" \
"v_mfma_f32_16x16x32_bf16 a[128:131], v[132:135], v[148:151], a[128:131]\n" \
"ds_read_b64_tr_b16 v[172:173], v38 offset:40960\n" \
"v_bitop3_b32 v212, v215, v212, 7 bitop3:0x6c\n" \
"v_mfma_f32_16x16x32_bf16 a[132:135], v[68:71], v[88:91], a[132:135]\n" \
"s_or_b32 s40, s7, 0x14000\n" \
"s_lshl_b32 s43, s44, 13\n" \
"v_mfma_f32_16x16x32_bf16 a[132:135], v[132:135], v[152:155], a[132:135]\n" \
"ds_read_b64_tr_b16 v[174:175], v38 offset:41984\n" \
"s_mulk_i32 s44, 0xe800\n" \
"v_mfma_f32_16x16x32_bf16 a[136:139], v[68:71], v[92:95], a[136:139]\n" \
"s_lshl_b32 s7, s3, 8\n" \
"s_nop 0\n" \
"v_lshl_or_b32 v24, v218, 4, v214\n" \
"v_mfma_f32_16x16x32_bf16 a[136:139], v[132:135], v[156:159], a[136:139]\n" \
"v_lshl_or_b32 v21, v212, 4, v213\n" \
"s_add_i32 s44, s44, 0x17000\n" \
"v_mfma_f32_16x16x32_bf16 a[140:143], v[68:71], v[96:99], a[140:143]\n" \
"s_or_b32 s45, s7, 0x27000\n" \
"s_or_b32 s46, s4, 0x10000\n" \
"v_mfma_f32_16x16x32_bf16 a[140:143], v[132:135], v[160:163], a[140:143]\n" \
"ds_read_b64_tr_b16 v[112:113], v39 offset:32768\n" \
"s_add_i32 s48, s6, 0x3000000\n" \
"v_mfma_f32_16x16x32_bf16 a[172:175], v[72:75], v[96:99], a[172:175]\n" \
"ds_write_b128 v198, v[208:211]\n" \
"v_mfma_f32_16x16x32_bf16 a[172:175], v[136:139], v[160:163], a[172:175]\n" \
"s_add_i32 s49, s5, 0x3004000\n" \
"s_add_i32 s52, s52, 0x1010000\n" \
"v_mfma_f32_16x16x32_bf16 a[168:171], v[72:75], v[92:95], a[168:171]\n" \
"ds_read_b64_tr_b16 v[114:115], v39 offset:33792\n" \
"v_mfma_f32_16x16x32_bf16 a[168:171], v[136:139], v[156:159], a[168:171]\n" \
"v_mfma_f32_16x16x32_bf16 a[164:167], v[72:75], v[88:91], a[164:167]\n" \
"v_mfma_f32_16x16x32_bf16 a[164:167], v[136:139], v[152:155], a[164:167]\n" \
"ds_read_b64_tr_b16 v[176:177], v39 offset:40960\n" \
"v_mfma_f32_16x16x32_bf16 a[160:163], v[72:75], v[84:87], a[160:163]\n" \
"v_mfma_f32_16x16x32_bf16 a[160:163], v[136:139], v[148:151], a[160:163]\n" \
"v_mfma_f32_16x16x32_bf16 a[192:195], v[76:79], v[84:87], a[192:195]\n" \
"ds_read_b64_tr_b16 v[178:179], v39 offset:41984\n" \
"v_mfma_f32_16x16x32_bf16 a[192:195], v[140:143], v[148:151], a[192:195]\n" \
"v_mfma_f32_16x16x32_bf16 a[196:199], v[76:79], v[88:91], a[196:199]\n" \
"v_mfma_f32_16x16x32_bf16 a[196:199], v[140:143], v[152:155], a[196:199]\n" \
"v_mfma_f32_16x16x32_bf16 a[200:203], v[76:79], v[92:95], a[200:203]\n" \
"v_mfma_f32_16x16x32_bf16 a[200:203], v[140:143], v[156:159], a[200:203]\n" \
"v_mfma_f32_16x16x32_bf16 a[204:207], v[76:79], v[96:99], a[204:207]\n" \
"v_mfma_f32_16x16x32_bf16 a[204:207], v[140:143], v[160:163], a[204:207]\n" \
"s_waitcnt lgkmcnt(15)\n" \
"v_mfma_f32_16x16x32_bf16 a[236:239], v[80:83], v[96:99], a[236:239]\n" \
"v_mfma_f32_16x16x32_bf16 a[236:239], v[144:147], v[160:163], a[236:239]\n" \
"v_mfma_f32_16x16x32_bf16 a[232:235], v[80:83], v[92:95], a[232:235]\n" \
"v_mfma_f32_16x16x32_bf16 a[232:235], v[144:147], v[156:159], a[232:235]\n" \
"v_mfma_f32_16x16x32_bf16 a[228:231], v[80:83], v[88:91], a[228:231]\n" \
"v_mfma_f32_16x16x32_bf16 a[228:231], v[144:147], v[152:155], a[228:231]\n" \
"v_mfma_f32_16x16x32_bf16 a[224:227], v[80:83], v[84:87], a[224:227]\n" \
"v_mfma_f32_16x16x32_bf16 a[224:227], v[144:147], v[148:151], a[224:227]\n" \
"s_waitcnt vmcnt(1) lgkmcnt(0)\n" \
"s_barrier\n" \
"s_nop 0\n" \
"v_mfma_f32_16x16x32_bf16 a[16:19], v[52:55], v[100:103], a[16:19]\n" \
"s_add_u32 m0, s30, 0x8000\n" \
"s_mov_b32 s85, 0x280\n" \
"v_mfma_f32_16x16x32_bf16 a[16:19], v[116:119], v[164:167], a[16:19]\n" \
"buffer_load_dwordx4 v22, s[64:67], s85 offen lds\n" \
"v_mfma_f32_16x16x32_bf16 a[20:23], v[52:55], v[104:107], a[20:23]\n" \
"s_add_u32 m0, s30, 0x8400\n" \
"s_mov_b32 s84, 0x2280\n" \
"v_mfma_f32_16x16x32_bf16 a[20:23], v[116:119], v[168:171], a[20:23]\n" \
"buffer_load_dwordx4 v23, s[64:67], s84 offen lds\n" \
"v_mfma_f32_16x16x32_bf16 a[24:27], v[52:55], v[108:111], a[24:27]\n" \
"ds_read_b64_tr_b16 v[84:85], v32 offset:0\n" \
"v_mfma_f32_16x16x32_bf16 a[24:27], v[116:119], v[172:175], a[24:27]\n" \
"s_add_u32 m0, s31, 0x8000\n" \
"s_mov_b32 s85, 0x280000\n" \
"v_mfma_f32_16x16x32_bf16 a[28:31], v[52:55], v[112:115], a[28:31]\n" \
"buffer_load_dwordx4 v26, s[68:71], s85 offen lds\n" \
"v_mfma_f32_16x16x32_bf16 a[28:31], v[116:119], v[176:179], a[28:31]\n" \
"ds_read_b128 v[52:55], v28 offset:16384\n" \
"v_mfma_f32_16x16x32_bf16 a[60:63], v[56:59], v[112:115], a[60:63]\n" \
"ds_read_b128 v[116:119], v29 offset:16384\n" \
"v_mfma_f32_16x16x32_bf16 a[60:63], v[120:123], v[176:179], a[60:63]\n" \
"s_add_u32 m0, s31, 0xc000\n" \
"s_mov_b32 s84, 0x280000\n" \
"v_mfma_f32_16x16x32_bf16 a[56:59], v[56:59], v[108:111], a[56:59]\n" \
"buffer_load_dwordx4 v26, s[72:75], s84 offen lds\n" \
"v_mfma_f32_16x16x32_bf16 a[56:59], v[120:123], v[172:175], a[56:59]\n" \
"s_add_u32 m0, s30, 0x8800\n" \
"s_mov_b32 s85, 0x4280\n" \
"v_mfma_f32_16x16x32_bf16 a[52:55], v[56:59], v[104:107], a[52:55]\n" \
"buffer_load_dwordx4 v22, s[64:67], s85 offen lds\n" \
"v_mfma_f32_16x16x32_bf16 a[52:55], v[120:123], v[168:171], a[52:55]\n" \
"ds_read_b64_tr_b16 v[86:87], v32 offset:1024\n" \
"v_mfma_f32_16x16x32_bf16 a[48:51], v[56:59], v[100:103], a[48:51]\n" \
"ds_read_b64_tr_b16 v[148:149], v32 offset:8192\n" \
"v_mfma_f32_16x16x32_bf16 a[48:51], v[120:123], v[164:167], a[48:51]\n" \
"ds_read_b128 v[56:59], v28 offset:18432\n" \
"v_mfma_f32_16x16x32_bf16 a[80:83], v[60:63], v[100:103], a[80:83]\n" \
"ds_read_b128 v[120:123], v29 offset:18432\n" \
"v_mfma_f32_16x16x32_bf16 a[80:83], v[124:127], v[164:167], a[80:83]\n" \
"s_add_u32 m0, s30, 0x8c00\n" \
"s_mov_b32 s84, 0x6280\n" \
"v_mfma_f32_16x16x32_bf16 a[84:87], v[60:63], v[104:107], a[84:87]\n" \
"buffer_load_dwordx4 v23, s[64:67], s84 offen lds\n" \
"v_mfma_f32_16x16x32_bf16 a[84:87], v[124:127], v[168:171], a[84:87]\n" \
"s_add_u32 m0, s31, 0x8400\n" \
"s_mov_b32 s85, 0x288000\n" \
"v_mfma_f32_16x16x32_bf16 a[88:91], v[60:63], v[108:111], a[88:91]\n" \
"buffer_load_dwordx4 v26, s[68:71], s85 offen lds\n" \
"v_mfma_f32_16x16x32_bf16 a[88:91], v[124:127], v[172:175], a[88:91]\n" \
"s_add_u32 m0, s31, 0xc400\n" \
"s_mov_b32 s84, 0x288000\n" \
"v_mfma_f32_16x16x32_bf16 a[92:95], v[60:63], v[112:115], a[92:95]\n" \
"buffer_load_dwordx4 v26, s[72:75], s84 offen lds\n" \
"v_mfma_f32_16x16x32_bf16 a[92:95], v[124:127], v[176:179], a[92:95]\n" \
"ds_read_b128 v[60:63], v28 offset:20480\n" \
"v_mfma_f32_16x16x32_bf16 a[124:127], v[64:67], v[112:115], a[124:127]\n" \
"ds_read_b128 v[124:127], v29 offset:20480\n" \
"v_mfma_f32_16x16x32_bf16 a[124:127], v[128:131], v[176:179], a[124:127]\n" \
"s_add_u32 m0, s30, 0x9000\n" \
"s_mov_b32 s85, 0x8280\n" \
"v_mfma_f32_16x16x32_bf16 a[120:123], v[64:67], v[108:111], a[120:123]\n" \
"buffer_load_dwordx4 v22, s[64:67], s85 offen lds\n" \
"v_mfma_f32_16x16x32_bf16 a[120:123], v[128:131], v[172:175], a[120:123]\n" \
"s_add_u32 m0, s30, 0x9400\n" \
"s_mov_b32 s84, 0xa280\n" \
"v_mfma_f32_16x16x32_bf16 a[116:119], v[64:67], v[104:107], a[116:119]\n" \
"buffer_load_dwordx4 v23, s[64:67], s84 offen lds\n" \
"v_mfma_f32_16x16x32_bf16 a[116:119], v[128:131], v[168:171], a[116:119]\n" \
"s_add_u32 m0, s31, 0x8800\n" \
"s_mov_b32 s85, 0x290000\n" \
"v_mfma_f32_16x16x32_bf16 a[112:115], v[64:67], v[100:103], a[112:115]\n" \
"buffer_load_dwordx4 v27, s[68:71], s85 offen lds\n" \
"v_mfma_f32_16x16x32_bf16 a[112:115], v[128:131], v[164:167], a[112:115]\n" \
"ds_read_b128 v[64:67], v28 offset:22528\n" \
"v_mfma_f32_16x16x32_bf16 a[144:147], v[68:71], v[100:103], a[144:147]\n" \
"ds_read_b128 v[128:131], v29 offset:22528\n" \
"v_mfma_f32_16x16x32_bf16 a[144:147], v[132:135], v[164:167], a[144:147]\n" \
"s_add_u32 m0, s31, 0xc800\n" \
"s_mov_b32 s84, 0x290000\n" \
"v_mfma_f32_16x16x32_bf16 a[148:151], v[68:71], v[104:107], a[148:151]\n" \
"buffer_load_dwordx4 v27, s[72:75], s84 offen lds\n" \
"v_mfma_f32_16x16x32_bf16 a[148:151], v[132:135], v[168:171], a[148:151]\n" \
"s_add_u32 m0, s30, 0x9800\n" \
"s_mov_b32 s85, 0xc280\n" \
"v_mfma_f32_16x16x32_bf16 a[152:155], v[68:71], v[108:111], a[152:155]\n" \
"buffer_load_dwordx4 v22, s[64:67], s85 offen lds\n" \
"v_mfma_f32_16x16x32_bf16 a[152:155], v[132:135], v[172:175], a[152:155]\n" \
"s_add_u32 m0, s30, 0x9c00\n" \
"s_mov_b32 s84, 0xe280\n" \
"v_mfma_f32_16x16x32_bf16 a[156:159], v[68:71], v[112:115], a[156:159]\n" \
"buffer_load_dwordx4 v23, s[64:67], s84 offen lds\n" \
"v_mfma_f32_16x16x32_bf16 a[156:159], v[132:135], v[176:179], a[156:159]\n" \
"ds_read_b128 v[68:71], v28 offset:0\n" \
"v_mfma_f32_16x16x32_bf16 a[188:191], v[72:75], v[112:115], a[188:191]\n" \
"ds_read_b128 v[132:135], v29 offset:0\n" \
"v_mfma_f32_16x16x32_bf16 a[188:191], v[136:139], v[176:179], a[188:191]\n" \
"ds_read_b128 v[200:203], v199 offset:0\n" \
"v_mfma_f32_16x16x32_bf16 a[184:187], v[72:75], v[108:111], a[184:187]\n" \
"ds_read_b128 v[204:207], v199 offset:4096\n" \
"v_mfma_f32_16x16x32_bf16 a[184:187], v[136:139], v[172:175], a[184:187]\n" \
"ds_read_b64_tr_b16 v[150:151], v32 offset:9216\n" \
"v_mfma_f32_16x16x32_bf16 a[180:183], v[72:75], v[104:107], a[180:183]\n" \
"ds_read_b64_tr_b16 v[88:89], v33 offset:0\n" \
"v_mfma_f32_16x16x32_bf16 a[180:183], v[136:139], v[168:171], a[180:183]\n" \
"ds_read_b128 v[208:211], v199 offset:8192\n" \
"v_mfma_f32_16x16x32_bf16 a[176:179], v[72:75], v[100:103], a[176:179]\n" \
"ds_read_b64_tr_b16 v[90:91], v33 offset:1024\n" \
"v_mfma_f32_16x16x32_bf16 a[176:179], v[136:139], v[164:167], a[176:179]\n" \
"ds_read_b128 v[72:75], v28 offset:2048\n" \
"v_mfma_f32_16x16x32_bf16 a[208:211], v[76:79], v[100:103], a[208:211]\n" \
"ds_read_b128 v[136:139], v29 offset:2048\n" \
"v_mfma_f32_16x16x32_bf16 a[208:211], v[140:143], v[164:167], a[208:211]\n" \
"ds_read_b64_tr_b16 v[152:153], v33 offset:8192\n" \
"v_mfma_f32_16x16x32_bf16 a[212:215], v[76:79], v[104:107], a[212:215]\n" \
"ds_read_b64_tr_b16 v[154:155], v33 offset:9216\n" \
"v_mfma_f32_16x16x32_bf16 a[212:215], v[140:143], v[168:171], a[212:215]\n" \
"ds_read_b64_tr_b16 v[92:93], v34 offset:0\n" \
"v_mfma_f32_16x16x32_bf16 a[216:219], v[76:79], v[108:111], a[216:219]\n" \
"ds_read_b64_tr_b16 v[94:95], v34 offset:1024\n" \
"v_mfma_f32_16x16x32_bf16 a[216:219], v[140:143], v[172:175], a[216:219]\n" \
"ds_read_b64_tr_b16 v[156:157], v34 offset:8192\n" \
"v_mfma_f32_16x16x32_bf16 a[220:223], v[76:79], v[112:115], a[220:223]\n" \
"ds_read_b64_tr_b16 v[158:159], v34 offset:9216\n" \
"v_mfma_f32_16x16x32_bf16 a[220:223], v[140:143], v[176:179], a[220:223]\n" \
"ds_read_b128 v[76:79], v28 offset:4096\n" \
"v_mfma_f32_16x16x32_bf16 a[252:255], v[80:83], v[112:115], a[252:255]\n" \
"ds_read_b128 v[140:143], v29 offset:4096\n" \
"v_mfma_f32_16x16x32_bf16 a[252:255], v[144:147], v[176:179], a[252:255]\n" \
"ds_read_b64_tr_b16 v[96:97], v35 offset:0\n" \
"v_mfma_f32_16x16x32_bf16 a[248:251], v[80:83], v[108:111], a[248:251]\n" \
"ds_read_b64_tr_b16 v[98:99], v35 offset:1024\n" \
"v_mfma_f32_16x16x32_bf16 a[248:251], v[144:147], v[172:175], a[248:251]\n" \
"ds_read_b64_tr_b16 v[160:161], v35 offset:8192\n" \
"v_mfma_f32_16x16x32_bf16 a[244:247], v[80:83], v[104:107], a[244:247]\n" \
"ds_read_b64_tr_b16 v[162:163], v35 offset:9216\n" \
"v_mfma_f32_16x16x32_bf16 a[244:247], v[144:147], v[168:171], a[244:247]\n" \
"v_mfma_f32_16x16x32_bf16 a[240:243], v[80:83], v[100:103], a[240:243]\n" \
"v_mfma_f32_16x16x32_bf16 a[240:243], v[144:147], v[164:167], a[240:243]\n" \
"ds_read_b128 v[80:83], v28 offset:6144\n" \
"ds_read_b128 v[144:147], v29 offset:6144\n" \
"s_waitcnt lgkmcnt(15)\n" \
"v_mfma_f32_16x16x32_bf16 a[0:3], v[52:55], v[84:87], a[0:3]\n" \
"s_add_u32 m0, s31, 0x8c00\n" \
"s_mov_b32 s85, 0x298000\n" \
"v_mfma_f32_16x16x32_bf16 a[0:3], v[116:119], v[148:151], a[0:3]\n" \
"buffer_load_dwordx4 v27, s[68:71], s85 offen lds\n" \
"v_mfma_f32_16x16x32_bf16 a[4:7], v[52:55], v[88:91], a[4:7]\n" \
"ds_read_b64_tr_b16 v[100:101], v36 offset:0\n" \
"s_waitcnt lgkmcnt(13)\n" \
"v_mfma_f32_16x16x32_bf16 a[4:7], v[116:119], v[152:155], a[4:7]\n" \
"s_add_u32 m0, s31, 0xcc00\n" \
"s_mov_b32 s84, 0x298000\n" \
"s_waitcnt lgkmcnt(11)\n" \
"v_mfma_f32_16x16x32_bf16 a[8:11], v[52:55], v[92:95], a[8:11]\n" \
"buffer_load_dwordx4 v27, s[72:75], s84 offen lds\n" \
"s_waitcnt lgkmcnt(9)\n" \
"v_mfma_f32_16x16x32_bf16 a[8:11], v[116:119], v[156:159], a[8:11]\n" \
"ds_read_b64_tr_b16 v[102:103], v36 offset:1024\n" \
"s_waitcnt lgkmcnt(6)\n" \
"v_mfma_f32_16x16x32_bf16 a[12:15], v[52:55], v[96:99], a[12:15]\n" \
"ds_read_b64_tr_b16 v[164:165], v36 offset:8192\n" \
"s_waitcnt lgkmcnt(5)\n" \
"v_mfma_f32_16x16x32_bf16 a[12:15], v[116:119], v[160:163], a[12:15]\n" \
"v_mfma_f32_16x16x32_bf16 a[44:47], v[56:59], v[96:99], a[44:47]\n" \
"v_mfma_f32_16x16x32_bf16 a[44:47], v[120:123], v[160:163], a[44:47]\n" \
"ds_read_b64_tr_b16 v[166:167], v36 offset:9216\n" \
"v_mfma_f32_16x16x32_bf16 a[40:43], v[56:59], v[92:95], a[40:43]\n" \
"v_mfma_f32_16x16x32_bf16 a[40:43], v[120:123], v[156:159], a[40:43]\n" \
"v_mfma_f32_16x16x32_bf16 a[36:39], v[56:59], v[88:91], a[36:39]\n" \
"v_mfma_f32_16x16x32_bf16 a[36:39], v[120:123], v[152:155], a[36:39]\n" \
"ds_read_b64_tr_b16 v[104:105], v37 offset:0\n" \
"v_mfma_f32_16x16x32_bf16 a[32:35], v[56:59], v[84:87], a[32:35]\n" \
"v_mfma_f32_16x16x32_bf16 a[32:35], v[120:123], v[148:151], a[32:35]\n" \
"v_mfma_f32_16x16x32_bf16 a[64:67], v[60:63], v[84:87], a[64:67]\n" \
"ds_read_b64_tr_b16 v[106:107], v37 offset:1024\n" \
"v_mfma_f32_16x16x32_bf16 a[64:67], v[124:127], v[148:151], a[64:67]\n" \
"v_mfma_f32_16x16x32_bf16 a[68:71], v[60:63], v[88:91], a[68:71]\n" \
"v_mfma_f32_16x16x32_bf16 a[68:71], v[124:127], v[152:155], a[68:71]\n" \
"buffer_store_dwordx4 v[200:203], v20, s[80:83], s35 offen sc1\n" \
"v_mfma_f32_16x16x32_bf16 a[72:75], v[60:63], v[92:95], a[72:75]\n" \
"s_mov_b32 m0, s32\n" \
"ds_read_b64_tr_b16 v[168:169], v37 offset:8192\n" \
"v_mfma_f32_16x16x32_bf16 a[72:75], v[124:127], v[156:159], a[72:75]\n" \
"s_mov_b32 exec_lo, 0x0\n" \
"s_mov_b32 exec_hi, 0xffffffff\n" \
"global_load_lds_dword v[188:189], off\n" \
"s_mov_b64 exec, -1\n" \
"v_mfma_f32_16x16x32_bf16 a[76:79], v[60:63], v[96:99], a[76:79]\n" \
"ds_read_b64_tr_b16 v[170:171], v37 offset:9216\n" \
"v_mfma_f32_16x16x32_bf16 a[76:79], v[124:127], v[160:163], a[76:79]\n" \
"v_mfma_f32_16x16x32_bf16 a[108:111], v[64:67], v[96:99], a[108:111]\n" \
"v_mfma_f32_16x16x32_bf16 a[108:111], v[128:131], v[160:163], a[108:111]\n" \
"v_mfma_f32_16x16x32_bf16 a[104:107], v[64:67], v[92:95], a[104:107]\n" \
"ds_read_b64_tr_b16 v[108:109], v38 offset:0\n" \
"v_mfma_f32_16x16x32_bf16 a[104:107], v[128:131], v[156:159], a[104:107]\n" \
"v_mfma_f32_16x16x32_bf16 a[100:103], v[64:67], v[88:91], a[100:103]\n" \
"buffer_store_dwordx4 v[204:207], v20, s[80:83], s88 offen sc1\n" \
"v_mfma_f32_16x16x32_bf16 a[100:103], v[128:131], v[152:155], a[100:103]\n" \
"ds_read_b64_tr_b16 v[110:111], v38 offset:1024\n" \
"v_mfma_f32_16x16x32_bf16 a[96:99], v[64:67], v[84:87], a[96:99]\n" \
"v_mfma_f32_16x16x32_bf16 a[96:99], v[128:131], v[148:151], a[96:99]\n" \
"v_mfma_f32_16x16x32_bf16 a[128:131], v[68:71], v[84:87], a[128:131]\n" \
"ds_read_b64_tr_b16 v[172:173], v38 offset:8192\n" \
"v_mfma_f32_16x16x32_bf16 a[128:131], v[132:135], v[148:151], a[128:131]\n" \
"v_mfma_f32_16x16x32_bf16 a[132:135], v[68:71], v[88:91], a[132:135]\n" \
"v_mfma_f32_16x16x32_bf16 a[132:135], v[132:135], v[152:155], a[132:135]\n" \
"ds_read_b64_tr_b16 v[174:175], v38 offset:9216\n" \
"v_mfma_f32_16x16x32_bf16 a[136:139], v[68:71], v[92:95], a[136:139]\n" \
"v_mfma_f32_16x16x32_bf16 a[136:139], v[132:135], v[156:159], a[136:139]\n" \
"v_mfma_f32_16x16x32_bf16 a[140:143], v[68:71], v[96:99], a[140:143]\n" \
"buffer_store_dwordx4 v[208:211], v20, s[80:83], s36 offen sc1\n" \
"v_mfma_f32_16x16x32_bf16 a[140:143], v[132:135], v[160:163], a[140:143]\n" \
"ds_read_b64_tr_b16 v[112:113], v39 offset:0\n" \
"v_mfma_f32_16x16x32_bf16 a[172:175], v[72:75], v[96:99], a[172:175]\n" \
"v_mfma_f32_16x16x32_bf16 a[172:175], v[136:139], v[160:163], a[172:175]\n" \
"v_mfma_f32_16x16x32_bf16 a[168:171], v[72:75], v[92:95], a[168:171]\n" \
"ds_read_b64_tr_b16 v[114:115], v39 offset:1024\n" \
"v_mfma_f32_16x16x32_bf16 a[168:171], v[136:139], v[156:159], a[168:171]\n" \
"v_mfma_f32_16x16x32_bf16 a[164:167], v[72:75], v[88:91], a[164:167]\n" \
"v_mfma_f32_16x16x32_bf16 a[164:167], v[136:139], v[152:155], a[164:167]\n" \
"ds_read_b64_tr_b16 v[176:177], v39 offset:8192\n" \
"v_mfma_f32_16x16x32_bf16 a[160:163], v[72:75], v[84:87], a[160:163]\n" \
"v_mfma_f32_16x16x32_bf16 a[160:163], v[136:139], v[148:151], a[160:163]\n" \
"v_mfma_f32_16x16x32_bf16 a[192:195], v[76:79], v[84:87], a[192:195]\n" \
"ds_read_b64_tr_b16 v[178:179], v39 offset:9216\n" \
"v_mfma_f32_16x16x32_bf16 a[192:195], v[140:143], v[148:151], a[192:195]\n" \
"v_mfma_f32_16x16x32_bf16 a[196:199], v[76:79], v[88:91], a[196:199]\n" \
"v_mfma_f32_16x16x32_bf16 a[196:199], v[140:143], v[152:155], a[196:199]\n" \
"v_mfma_f32_16x16x32_bf16 a[200:203], v[76:79], v[92:95], a[200:203]\n" \
"v_mfma_f32_16x16x32_bf16 a[200:203], v[140:143], v[156:159], a[200:203]\n" \
"v_mfma_f32_16x16x32_bf16 a[204:207], v[76:79], v[96:99], a[204:207]\n" \
"v_mfma_f32_16x16x32_bf16 a[204:207], v[140:143], v[160:163], a[204:207]\n" \
"s_waitcnt lgkmcnt(15)\n" \
"v_mfma_f32_16x16x32_bf16 a[236:239], v[80:83], v[96:99], a[236:239]\n" \
"v_mfma_f32_16x16x32_bf16 a[236:239], v[144:147], v[160:163], a[236:239]\n" \
"v_mfma_f32_16x16x32_bf16 a[232:235], v[80:83], v[92:95], a[232:235]\n" \
"v_mfma_f32_16x16x32_bf16 a[232:235], v[144:147], v[156:159], a[232:235]\n" \
"v_mfma_f32_16x16x32_bf16 a[228:231], v[80:83], v[88:91], a[228:231]\n" \
"v_mfma_f32_16x16x32_bf16 a[228:231], v[144:147], v[152:155], a[228:231]\n" \
"v_mfma_f32_16x16x32_bf16 a[224:227], v[80:83], v[84:87], a[224:227]\n" \
"v_mfma_f32_16x16x32_bf16 a[224:227], v[144:147], v[148:151], a[224:227]\n" \
"s_waitcnt vmcnt(4) lgkmcnt(0)\n" \
"s_barrier\n" \
"v_mfma_f32_16x16x32_bf16 a[16:19], v[52:55], v[100:103], a[16:19]\n" \
"s_add_u32 m0, s30, 0x0\n" \
"s_mov_b32 s85, 0x300\n" \
"v_mfma_f32_16x16x32_bf16 a[16:19], v[116:119], v[164:167], a[16:19]\n" \
"buffer_load_dwordx4 v22, s[64:67], s85 offen lds\n" \
"v_mfma_f32_16x16x32_bf16 a[20:23], v[52:55], v[104:107], a[20:23]\n" \
"ds_read_b64_tr_b16 v[84:85], v32 offset:32768\n" \
"v_mfma_f32_16x16x32_bf16 a[20:23], v[116:119], v[168:171], a[20:23]\n" \
"s_add_u32 m0, s30, 0x400\n" \
"s_mov_b32 s84, 0x2300\n" \
"v_mfma_f32_16x16x32_bf16 a[24:27], v[52:55], v[108:111], a[24:27]\n" \
"buffer_load_dwordx4 v23, s[64:67], s84 offen lds\n" \
"v_mfma_f32_16x16x32_bf16 a[24:27], v[116:119], v[172:175], a[24:27]\n" \
"ds_read_b64_tr_b16 v[86:87], v32 offset:33792\n" \
"v_mfma_f32_16x16x32_bf16 a[28:31], v[52:55], v[112:115], a[28:31]\n" \
"ds_read_b64_tr_b16 v[148:149], v32 offset:40960\n" \
"v_mfma_f32_16x16x32_bf16 a[28:31], v[116:119], v[176:179], a[28:31]\n" \
"ds_read_b128 v[52:55], v28 offset:49152\n" \
"v_mfma_f32_16x16x32_bf16 a[60:63], v[56:59], v[112:115], a[60:63]\n" \
"ds_read_b128 v[116:119], v29 offset:49152\n" \
"v_mfma_f32_16x16x32_bf16 a[60:63], v[120:123], v[176:179], a[60:63]\n" \
"s_add_u32 m0, s31, 0x0\n" \
"s_nop 0\n" \
"s_mov_b32 s85, 0x300000\n" \
"v_mfma_f32_16x16x32_bf16 a[56:59], v[56:59], v[108:111], a[56:59]\n" \
"buffer_load_dwordx4 v26, s[68:71], s85 offen lds\n" \
"v_mfma_f32_16x16x32_bf16 a[56:59], v[120:123], v[172:175], a[56:59]\n" \
"s_add_u32 m0, s31, 0x4000\n" \
"s_mov_b32 s84, 0x300000\n" \
"v_mfma_f32_16x16x32_bf16 a[52:55], v[56:59], v[104:107], a[52:55]\n" \
"buffer_load_dwordx4 v26, s[72:75], s84 offen lds\n" \
"v_mfma_f32_16x16x32_bf16 a[52:55], v[120:123], v[168:171], a[52:55]\n" \
"s_add_u32 m0, s30, 0x800\n" \
"s_mov_b32 s85, 0x4300\n" \
"v_mfma_f32_16x16x32_bf16 a[48:51], v[56:59], v[100:103], a[48:51]\n" \
"buffer_load_dwordx4 v22, s[64:67], s85 offen lds\n" \
"v_mfma_f32_16x16x32_bf16 a[48:51], v[120:123], v[164:167], a[48:51]\n" \
"ds_read_b128 v[56:59], v28 offset:51200\n" \
"v_mfma_f32_16x16x32_bf16 a[80:83], v[60:63], v[100:103], a[80:83]\n" \
"ds_read_b128 v[120:123], v29 offset:51200\n" \
"v_mfma_f32_16x16x32_bf16 a[80:83], v[124:127], v[164:167], a[80:83]\n" \
"s_add_u32 m0, s30, 0xc00\n" \
"s_mov_b32 s84, 0x6300\n" \
"v_mfma_f32_16x16x32_bf16 a[84:87], v[60:63], v[104:107], a[84:87]\n" \
"buffer_load_dwordx4 v23, s[64:67], s84 offen lds\n" \
"v_mfma_f32_16x16x32_bf16 a[84:87], v[124:127], v[168:171], a[84:87]\n" \
"ds_read_b64_tr_b16 v[150:151], v32 offset:41984\n" \
"v_mfma_f32_16x16x32_bf16 a[88:91], v[60:63], v[108:111], a[88:91]\n" \
"s_add_u32 m0, s31, 0x400\n" \
"s_mov_b32 s85, 0x308000\n" \
"v_mfma_f32_16x16x32_bf16 a[88:91], v[124:127], v[172:175], a[88:91]\n" \
"buffer_load_dwordx4 v26, s[68:71], s85 offen lds\n" \
"v_mfma_f32_16x16x32_bf16 a[92:95], v[60:63], v[112:115], a[92:95]\n" \
"ds_read_b64_tr_b16 v[88:89], v33 offset:32768\n" \
"v_mfma_f32_16x16x32_bf16 a[92:95], v[124:127], v[176:179], a[92:95]\n" \
"ds_read_b128 v[60:63], v28 offset:53248\n" \
"v_mfma_f32_16x16x32_bf16 a[124:127], v[64:67], v[112:115], a[124:127]\n" \
"ds_read_b128 v[124:127], v29 offset:53248\n" \
"v_mfma_f32_16x16x32_bf16 a[124:127], v[128:131], v[176:179], a[124:127]\n" \
"s_add_u32 m0, s31, 0x4400\n" \
"s_mov_b32 s84, 0x308000\n" \
"v_mfma_f32_16x16x32_bf16 a[120:123], v[64:67], v[108:111], a[120:123]\n" \
"buffer_load_dwordx4 v26, s[72:75], s84 offen lds\n" \
"v_mfma_f32_16x16x32_bf16 a[120:123], v[128:131], v[172:175], a[120:123]\n" \
"s_add_u32 m0, s30, 0x1000\n" \
"s_mov_b32 s85, 0x8300\n" \
"v_mfma_f32_16x16x32_bf16 a[116:119], v[64:67], v[104:107], a[116:119]\n" \
"buffer_load_dwordx4 v22, s[64:67], s85 offen lds\n" \
"v_mfma_f32_16x16x32_bf16 a[116:119], v[128:131], v[168:171], a[116:119]\n" \
"ds_read_b64_tr_b16 v[90:91], v33 offset:33792\n" \
"v_mfma_f32_16x16x32_bf16 a[112:115], v[64:67], v[100:103], a[112:115]\n" \
"ds_read_b64_tr_b16 v[152:153], v33 offset:40960\n" \
"v_mfma_f32_16x16x32_bf16 a[112:115], v[128:131], v[164:167], a[112:115]\n" \
"ds_read_b128 v[64:67], v28 offset:55296\n" \
"v_mfma_f32_16x16x32_bf16 a[144:147], v[68:71], v[100:103], a[144:147]\n" \
"ds_read_b128 v[128:131], v29 offset:55296\n" \
"v_mfma_f32_16x16x32_bf16 a[144:147], v[132:135], v[164:167], a[144:147]\n" \
"s_add_u32 m0, s30, 0x1400\n" \
"s_mov_b32 s84, 0xa300\n" \
"v_mfma_f32_16x16x32_bf16 a[148:151], v[68:71], v[104:107], a[148:151]\n" \
"buffer_load_dwordx4 v23, s[64:67], s84 offen lds\n" \
"v_mfma_f32_16x16x32_bf16 a[148:151], v[132:135], v[168:171], a[148:151]\n" \
"s_add_u32 m0, s31, 0x800\n" \
"s_mov_b32 s85, 0x310000\n" \
"v_mfma_f32_16x16x32_bf16 a[152:155], v[68:71], v[108:111], a[152:155]\n" \
"buffer_load_dwordx4 v27, s[68:71], s85 offen lds\n" \
"v_mfma_f32_16x16x32_bf16 a[152:155], v[132:135], v[172:175], a[152:155]\n" \
"s_add_u32 m0, s31, 0x4800\n" \
"s_mov_b32 s84, 0x310000\n" \
"v_mfma_f32_16x16x32_bf16 a[156:159], v[68:71], v[112:115], a[156:159]\n" \
"buffer_load_dwordx4 v27, s[72:75], s84 offen lds\n" \
"v_mfma_f32_16x16x32_bf16 a[156:159], v[132:135], v[176:179], a[156:159]\n" \
"ds_read_b128 v[68:71], v28 offset:32768\n" \
"v_mfma_f32_16x16x32_bf16 a[188:191], v[72:75], v[112:115], a[188:191]\n" \
"ds_read_b128 v[132:135], v29 offset:32768\n" \
"v_mfma_f32_16x16x32_bf16 a[188:191], v[136:139], v[176:179], a[188:191]\n" \
"s_add_u32 m0, s30, 0x1800\n" \
"s_mov_b32 s85, 0xc300\n" \
"v_mfma_f32_16x16x32_bf16 a[184:187], v[72:75], v[108:111], a[184:187]\n" \
"buffer_load_dwordx4 v22, s[64:67], s85 offen lds\n" \
"v_mfma_f32_16x16x32_bf16 a[184:187], v[136:139], v[172:175], a[184:187]\n" \
"ds_read_b64_tr_b16 v[154:155], v33 offset:41984\n" \
"v_mfma_f32_16x16x32_bf16 a[180:183], v[72:75], v[104:107], a[180:183]\n" \
"s_add_u32 m0, s30, 0x1c00\n" \
"s_mov_b32 s84, 0xe300\n" \
"v_mfma_f32_16x16x32_bf16 a[180:183], v[136:139], v[168:171], a[180:183]\n" \
"buffer_load_dwordx4 v23, s[64:67], s84 offen lds\n" \
"v_mfma_f32_16x16x32_bf16 a[176:179], v[72:75], v[100:103], a[176:179]\n" \
"ds_read_b64_tr_b16 v[92:93], v34 offset:32768\n" \
"v_mfma_f32_16x16x32_bf16 a[176:179], v[136:139], v[164:167], a[176:179]\n" \
"ds_read_b128 v[72:75], v28 offset:34816\n" \
"v_mfma_f32_16x16x32_bf16 a[208:211], v[76:79], v[100:103], a[208:211]\n" \
"ds_read_b128 v[136:139], v29 offset:34816\n" \
"v_mfma_f32_16x16x32_bf16 a[208:211], v[140:143], v[164:167], a[208:211]\n" \
"ds_read_b64_tr_b16 v[94:95], v34 offset:33792\n" \
"v_mfma_f32_16x16x32_bf16 a[212:215], v[76:79], v[104:107], a[212:215]\n" \
"ds_read_b64_tr_b16 v[156:157], v34 offset:40960\n" \
"v_mfma_f32_16x16x32_bf16 a[212:215], v[140:143], v[168:171], a[212:215]\n" \
"ds_read_b64_tr_b16 v[158:159], v34 offset:41984\n" \
"v_mfma_f32_16x16x32_bf16 a[216:219], v[76:79], v[108:111], a[216:219]\n" \
"ds_read_b64_tr_b16 v[96:97], v35 offset:32768\n" \
"v_mfma_f32_16x16x32_bf16 a[216:219], v[140:143], v[172:175], a[216:219]\n" \
"ds_read_b64_tr_b16 v[98:99], v35 offset:33792\n" \
"v_mfma_f32_16x16x32_bf16 a[220:223], v[76:79], v[112:115], a[220:223]\n" \
"ds_read_b64_tr_b16 v[160:161], v35 offset:40960\n" \
"v_mfma_f32_16x16x32_bf16 a[220:223], v[140:143], v[176:179], a[220:223]\n" \
"ds_read_b128 v[76:79], v28 offset:36864\n" \
"v_mfma_f32_16x16x32_bf16 a[252:255], v[80:83], v[112:115], a[252:255]\n" \
"ds_read_b128 v[140:143], v29 offset:36864\n" \
"v_mfma_f32_16x16x32_bf16 a[252:255], v[144:147], v[176:179], a[252:255]\n" \
"ds_read_b64_tr_b16 v[162:163], v35 offset:41984\n" \
"v_mfma_f32_16x16x32_bf16 a[248:251], v[80:83], v[108:111], a[248:251]\n" \
"v_mfma_f32_16x16x32_bf16 a[248:251], v[144:147], v[172:175], a[248:251]\n" \
"v_mfma_f32_16x16x32_bf16 a[244:247], v[80:83], v[104:107], a[244:247]\n" \
"v_mfma_f32_16x16x32_bf16 a[244:247], v[144:147], v[168:171], a[244:247]\n" \
"v_mfma_f32_16x16x32_bf16 a[240:243], v[80:83], v[100:103], a[240:243]\n" \
"v_mfma_f32_16x16x32_bf16 a[240:243], v[144:147], v[164:167], a[240:243]\n" \
"ds_read_b128 v[80:83], v28 offset:38912\n" \
"ds_read_b128 v[144:147], v29 offset:38912\n" \
"s_waitcnt lgkmcnt(15)\n" \
"v_mfma_f32_16x16x32_bf16 a[0:3], v[52:55], v[84:87], a[0:3]\n" \
"s_add_u32 m0, s31, 0xc00\n" \
"s_mov_b32 s85, 0x318000\n" \
"v_mfma_f32_16x16x32_bf16 a[0:3], v[116:119], v[148:151], a[0:3]\n" \
"buffer_load_dwordx4 v27, s[68:71], s85 offen lds\n" \
"v_mfma_f32_16x16x32_bf16 a[4:7], v[52:55], v[88:91], a[4:7]\n" \
"ds_read_b64_tr_b16 v[100:101], v36 offset:32768\n" \
"s_waitcnt lgkmcnt(15)\n" \
"v_mfma_f32_16x16x32_bf16 a[4:7], v[116:119], v[152:155], a[4:7]\n" \
"s_add_u32 m0, s31, 0x4c00\n" \
"s_mov_b32 s84, 0x318000\n" \
"s_waitcnt lgkmcnt(11)\n" \
"v_mfma_f32_16x16x32_bf16 a[8:11], v[52:55], v[92:95], a[8:11]\n" \
"buffer_load_dwordx4 v27, s[72:75], s84 offen lds\n" \
"s_waitcnt lgkmcnt(9)\n" \
"v_mfma_f32_16x16x32_bf16 a[8:11], v[116:119], v[156:159], a[8:11]\n" \
"ds_read_b64_tr_b16 v[102:103], v36 offset:33792\n" \
"s_waitcnt lgkmcnt(8)\n" \
"v_mfma_f32_16x16x32_bf16 a[12:15], v[52:55], v[96:99], a[12:15]\n" \
"ds_read_b64_tr_b16 v[164:165], v36 offset:40960\n" \
"s_waitcnt lgkmcnt(5)\n" \
"v_mfma_f32_16x16x32_bf16 a[12:15], v[116:119], v[160:163], a[12:15]\n" \
"v_mfma_f32_16x16x32_bf16 a[44:47], v[56:59], v[96:99], a[44:47]\n" \
"v_mfma_f32_16x16x32_bf16 a[44:47], v[120:123], v[160:163], a[44:47]\n" \
"ds_read_b64_tr_b16 v[166:167], v36 offset:41984\n" \
"v_mfma_f32_16x16x32_bf16 a[40:43], v[56:59], v[92:95], a[40:43]\n" \
"v_mfma_f32_16x16x32_bf16 a[40:43], v[120:123], v[156:159], a[40:43]\n" \
"v_mfma_f32_16x16x32_bf16 a[36:39], v[56:59], v[88:91], a[36:39]\n" \
"v_mfma_f32_16x16x32_bf16 a[36:39], v[120:123], v[152:155], a[36:39]\n" \
"ds_read_b64_tr_b16 v[104:105], v37 offset:32768\n" \
"v_mfma_f32_16x16x32_bf16 a[32:35], v[56:59], v[84:87], a[32:35]\n" \
"v_mfma_f32_16x16x32_bf16 a[32:35], v[120:123], v[148:151], a[32:35]\n" \
"v_mfma_f32_16x16x32_bf16 a[64:67], v[60:63], v[84:87], a[64:67]\n" \
"ds_read_b64_tr_b16 v[106:107], v37 offset:33792\n" \
"v_mfma_f32_16x16x32_bf16 a[64:67], v[124:127], v[148:151], a[64:67]\n" \
"v_mfma_f32_16x16x32_bf16 a[68:71], v[60:63], v[88:91], a[68:71]\n" \
"v_lshlrev_b32_e32 v45, 3, v20\n" \
"v_add_u32_e32 v45, s31, v45\n" \
"v_mfma_f32_16x16x32_bf16 a[68:71], v[124:127], v[152:155], a[68:71]\n" \
"s_getpc_b64 s[86:87]\n" \
"s_sub_u32 s86, s86, 0x10800\n" \
"s_subb_u32 s87, s87, 0\n" \
"s_mov_b32 m0, s32\n" \
"s_nop 0\n" \
"v_mfma_f32_16x16x32_bf16 a[72:75], v[60:63], v[92:95], a[72:75]\n" \
"global_load_lds_dword v45, s[86:87]\n" \
"v_mfma_f32_16x16x32_bf16 a[72:75], v[124:127], v[156:159], a[72:75]\n" \
"ds_read_b64_tr_b16 v[168:169], v37 offset:40960\n" \
"v_mfma_f32_16x16x32_bf16 a[76:79], v[60:63], v[96:99], a[76:79]\n" \
"ds_read_b64_tr_b16 v[170:171], v37 offset:41984\n" \
"v_mfma_f32_16x16x32_bf16 a[76:79], v[124:127], v[160:163], a[76:79]\n" \
"v_mfma_f32_16x16x32_bf16 a[108:111], v[64:67], v[96:99], a[108:111]\n" \
"v_mfma_f32_16x16x32_bf16 a[108:111], v[128:131], v[160:163], a[108:111]\n" \
"v_mfma_f32_16x16x32_bf16 a[104:107], v[64:67], v[92:95], a[104:107]\n" \
"ds_read_b64_tr_b16 v[108:109], v38 offset:32768\n" \
"v_mfma_f32_16x16x32_bf16 a[104:107], v[128:131], v[156:159], a[104:107]\n" \
"v_mfma_f32_16x16x32_bf16 a[100:103], v[64:67], v[88:91], a[100:103]\n" \
"v_mfma_f32_16x16x32_bf16 a[100:103], v[128:131], v[152:155], a[100:103]\n" \
"ds_read_b64_tr_b16 v[110:111], v38 offset:33792\n" \
"v_mfma_f32_16x16x32_bf16 a[96:99], v[64:67], v[84:87], a[96:99]\n" \
"v_mfma_f32_16x16x32_bf16 a[96:99], v[128:131], v[148:151], a[96:99]\n" \
"v_mfma_f32_16x16x32_bf16 a[128:131], v[68:71], v[84:87], a[128:131]\n" \
"ds_read_b64_tr_b16 v[172:173], v38 offset:40960\n" \
"v_mfma_f32_16x16x32_bf16 a[128:131], v[132:135], v[148:151], a[128:131]\n" \
"v_mfma_f32_16x16x32_bf16 a[132:135], v[68:71], v[88:91], a[132:135]\n" \
"v_mfma_f32_16x16x32_bf16 a[132:135], v[132:135], v[152:155], a[132:135]\n" \
"ds_read_b64_tr_b16 v[174:175], v38 offset:41984\n" \
"v_mfma_f32_16x16x32_bf16 a[136:139], v[68:71], v[92:95], a[136:139]\n" \
"v_mfma_f32_16x16x32_bf16 a[136:139], v[132:135], v[156:159], a[136:139]\n" \
"v_mfma_f32_16x16x32_bf16 a[140:143], v[68:71], v[96:99], a[140:143]\n" \
"v_mfma_f32_16x16x32_bf16 a[140:143], v[132:135], v[160:163], a[140:143]\n" \
"ds_read_b64_tr_b16 v[112:113], v39 offset:32768\n" \
"v_mfma_f32_16x16x32_bf16 a[172:175], v[72:75], v[96:99], a[172:175]\n" \
"v_mfma_f32_16x16x32_bf16 a[172:175], v[136:139], v[160:163], a[172:175]\n" \
"v_mfma_f32_16x16x32_bf16 a[168:171], v[72:75], v[92:95], a[168:171]\n" \
"ds_read_b64_tr_b16 v[114:115], v39 offset:33792\n" \
"v_mfma_f32_16x16x32_bf16 a[168:171], v[136:139], v[156:159], a[168:171]\n" \
"v_mfma_f32_16x16x32_bf16 a[164:167], v[72:75], v[88:91], a[164:167]\n" \
"v_mfma_f32_16x16x32_bf16 a[164:167], v[136:139], v[152:155], a[164:167]\n" \
"ds_read_b64_tr_b16 v[176:177], v39 offset:40960\n" \
"v_mfma_f32_16x16x32_bf16 a[160:163], v[72:75], v[84:87], a[160:163]\n" \
"v_mfma_f32_16x16x32_bf16 a[160:163], v[136:139], v[148:151], a[160:163]\n" \
"v_mfma_f32_16x16x32_bf16 a[192:195], v[76:79], v[84:87], a[192:195]\n" \
"ds_read_b64_tr_b16 v[178:179], v39 offset:41984\n" \
"v_mfma_f32_16x16x32_bf16 a[192:195], v[140:143], v[148:151], a[192:195]\n" \
"v_mfma_f32_16x16x32_bf16 a[196:199], v[76:79], v[88:91], a[196:199]\n" \
"v_mfma_f32_16x16x32_bf16 a[196:199], v[140:143], v[152:155], a[196:199]\n" \
"v_mfma_f32_16x16x32_bf16 a[200:203], v[76:79], v[92:95], a[200:203]\n" \
"v_mfma_f32_16x16x32_bf16 a[200:203], v[140:143], v[156:159], a[200:203]\n" \
"v_mfma_f32_16x16x32_bf16 a[204:207], v[76:79], v[96:99], a[204:207]\n" \
"v_mfma_f32_16x16x32_bf16 a[204:207], v[140:143], v[160:163], a[204:207]\n" \
"s_waitcnt lgkmcnt(15)\n" \
"v_mfma_f32_16x16x32_bf16 a[236:239], v[80:83], v[96:99], a[236:239]\n" \
"v_mfma_f32_16x16x32_bf16 a[236:239], v[144:147], v[160:163], a[236:239]\n" \
"v_mfma_f32_16x16x32_bf16 a[232:235], v[80:83], v[92:95], a[232:235]\n" \
"v_mfma_f32_16x16x32_bf16 a[232:235], v[144:147], v[156:159], a[232:235]\n" \
"v_mfma_f32_16x16x32_bf16 a[228:231], v[80:83], v[88:91], a[228:231]\n" \
"v_mfma_f32_16x16x32_bf16 a[228:231], v[144:147], v[152:155], a[228:231]\n" \
"v_mfma_f32_16x16x32_bf16 a[224:227], v[80:83], v[84:87], a[224:227]\n" \
"v_mfma_f32_16x16x32_bf16 a[224:227], v[144:147], v[148:151], a[224:227]\n" \
"s_waitcnt vmcnt(1) lgkmcnt(0)\n" \
"s_barrier\n" \
"v_mov_b32_e32 v45, s62\n" \
"s_mov_b64 exec, 1\n" \
"buffer_store_dword v45, off, s[80:83], s37 offset:128 sc1\n" \
"s_mov_b64 exec, -1\n" \
"v_mfma_f32_16x16x32_bf16 a[16:19], v[52:55], v[100:103], a[16:19]\n" \
"s_add_u32 m0, s30, 0x8000\n" \
"s_mov_b32 s85, 0x380\n" \
"v_mfma_f32_16x16x32_bf16 a[16:19], v[116:119], v[164:167], a[16:19]\n" \
"buffer_load_dwordx4 v22, s[64:67], s85 offen lds\n" \
"v_mfma_f32_16x16x32_bf16 a[20:23], v[52:55], v[104:107], a[20:23]\n" \
"ds_read_b64_tr_b16 v[84:85], v32 offset:0\n" \
"v_mfma_f32_16x16x32_bf16 a[20:23], v[116:119], v[168:171], a[20:23]\n" \
"s_add_u32 m0, s30, 0x8400\n" \
"s_mov_b32 s84, 0x2380\n" \
"v_mfma_f32_16x16x32_bf16 a[24:27], v[52:55], v[108:111], a[24:27]\n" \
"buffer_load_dwordx4 v23, s[64:67], s84 offen lds\n" \
"v_mfma_f32_16x16x32_bf16 a[24:27], v[116:119], v[172:175], a[24:27]\n" \
"ds_read_b64_tr_b16 v[86:87], v32 offset:1024\n" \
"v_mfma_f32_16x16x32_bf16 a[28:31], v[52:55], v[112:115], a[28:31]\n" \
"ds_read_b64_tr_b16 v[148:149], v32 offset:8192\n" \
"v_mfma_f32_16x16x32_bf16 a[28:31], v[116:119], v[176:179], a[28:31]\n" \
"ds_read_b128 v[52:55], v28 offset:16384\n" \
"v_mfma_f32_16x16x32_bf16 a[60:63], v[56:59], v[112:115], a[60:63]\n" \
"ds_read_b128 v[116:119], v29 offset:16384\n" \
"v_mfma_f32_16x16x32_bf16 a[60:63], v[120:123], v[176:179], a[60:63]\n" \
"s_add_u32 m0, s31, 0x8000\n" \
"s_mov_b32 s85, 0x380000\n" \
"v_mfma_f32_16x16x32_bf16 a[56:59], v[56:59], v[108:111], a[56:59]\n" \
"buffer_load_dwordx4 v26, s[68:71], s85 offen lds\n" \
"v_mfma_f32_16x16x32_bf16 a[56:59], v[120:123], v[172:175], a[56:59]\n" \
"s_add_u32 m0, s31, 0xc000\n" \
"s_mov_b32 s84, 0x380000\n" \
"v_mfma_f32_16x16x32_bf16 a[52:55], v[56:59], v[104:107], a[52:55]\n" \
"buffer_load_dwordx4 v26, s[72:75], s84 offen lds\n" \
"v_mfma_f32_16x16x32_bf16 a[52:55], v[120:123], v[168:171], a[52:55]\n" \
"s_add_u32 m0, s30, 0x8800\n" \
"s_mov_b32 s85, 0x4380\n" \
"v_mfma_f32_16x16x32_bf16 a[48:51], v[56:59], v[100:103], a[48:51]\n" \
"buffer_load_dwordx4 v22, s[64:67], s85 offen lds\n" \
"v_mfma_f32_16x16x32_bf16 a[48:51], v[120:123], v[164:167], a[48:51]\n" \
"ds_read_b128 v[56:59], v28 offset:18432\n" \
"v_mfma_f32_16x16x32_bf16 a[80:83], v[60:63], v[100:103], a[80:83]\n" \
"ds_read_b128 v[120:123], v29 offset:18432\n" \
"v_mfma_f32_16x16x32_bf16 a[80:83], v[124:127], v[164:167], a[80:83]\n" \
"s_add_u32 m0, s30, 0x8c00\n" \
"s_mov_b32 s84, 0x6380\n" \
"v_mfma_f32_16x16x32_bf16 a[84:87], v[60:63], v[104:107], a[84:87]\n" \
"buffer_load_dwordx4 v23, s[64:67], s84 offen lds\n" \
"v_mfma_f32_16x16x32_bf16 a[84:87], v[124:127], v[168:171], a[84:87]\n" \
"ds_read_b64_tr_b16 v[150:151], v32 offset:9216\n" \
"v_mfma_f32_16x16x32_bf16 a[88:91], v[60:63], v[108:111], a[88:91]\n" \
"s_add_u32 m0, s31, 0x8400\n" \
"s_mov_b32 s85, 0x388000\n" \
"v_mfma_f32_16x16x32_bf16 a[88:91], v[124:127], v[172:175], a[88:91]\n" \
"buffer_load_dwordx4 v26, s[68:71], s85 offen lds\n" \
"v_mfma_f32_16x16x32_bf16 a[92:95], v[60:63], v[112:115], a[92:95]\n" \
"ds_read_b64_tr_b16 v[88:89], v33 offset:0\n" \
"v_mfma_f32_16x16x32_bf16 a[92:95], v[124:127], v[176:179], a[92:95]\n" \
"ds_read_b128 v[60:63], v28 offset:20480\n" \
"v_mfma_f32_16x16x32_bf16 a[124:127], v[64:67], v[112:115], a[124:127]\n" \
"ds_read_b128 v[124:127], v29 offset:20480\n" \
"v_mfma_f32_16x16x32_bf16 a[124:127], v[128:131], v[176:179], a[124:127]\n" \
"s_add_u32 m0, s31, 0xc400\n" \
"s_mov_b32 s84, 0x388000\n" \
"v_mfma_f32_16x16x32_bf16 a[120:123], v[64:67], v[108:111], a[120:123]\n" \
"buffer_load_dwordx4 v26, s[72:75], s84 offen lds\n" \
"v_mfma_f32_16x16x32_bf16 a[120:123], v[128:131], v[172:175], a[120:123]\n" \
"s_add_u32 m0, s30, 0x9000\n" \
"s_mov_b32 s85, 0x8380\n" \
"v_mfma_f32_16x16x32_bf16 a[116:119], v[64:67], v[104:107], a[116:119]\n" \
"buffer_load_dwordx4 v22, s[64:67], s85 offen lds\n" \
"v_mfma_f32_16x16x32_bf16 a[116:119], v[128:131], v[168:171], a[116:119]\n" \
"ds_read_b64_tr_b16 v[90:91], v33 offset:1024\n" \
"v_mfma_f32_16x16x32_bf16 a[112:115], v[64:67], v[100:103], a[112:115]\n" \
"ds_read_b64_tr_b16 v[152:153], v33 offset:8192\n" \
"v_mfma_f32_16x16x32_bf16 a[112:115], v[128:131], v[164:167], a[112:115]\n" \
"ds_read_b128 v[64:67], v28 offset:22528\n" \
"v_mfma_f32_16x16x32_bf16 a[144:147], v[68:71], v[100:103], a[144:147]\n" \
"ds_read_b128 v[128:131], v29 offset:22528\n" \
"v_mfma_f32_16x16x32_bf16 a[144:147], v[132:135], v[164:167], a[144:147]\n" \
"s_add_u32 m0, s30, 0x9400\n" \
"s_mov_b32 s84, 0xa380\n" \
"v_mfma_f32_16x16x32_bf16 a[148:151], v[68:71], v[104:107], a[148:151]\n" \
"buffer_load_dwordx4 v23, s[64:67], s84 offen lds\n" \
"v_mfma_f32_16x16x32_bf16 a[148:151], v[132:135], v[168:171], a[148:151]\n" \
"s_add_u32 m0, s31, 0x8800\n" \
"s_mov_b32 s85, 0x390000\n" \
"v_mfma_f32_16x16x32_bf16 a[152:155], v[68:71], v[108:111], a[152:155]\n" \
"buffer_load_dwordx4 v27, s[68:71], s85 offen lds\n" \
"v_mfma_f32_16x16x32_bf16 a[152:155], v[132:135], v[172:175], a[152:155]\n" \
"s_add_u32 m0, s31, 0xc800\n" \
"s_mov_b32 s84, 0x390000\n" \
"v_mfma_f32_16x16x32_bf16 a[156:159], v[68:71], v[112:115], a[156:159]\n" \
"buffer_load_dwordx4 v27, s[72:75], s84 offen lds\n" \
"v_mfma_f32_16x16x32_bf16 a[156:159], v[132:135], v[176:179], a[156:159]\n" \
"ds_read_b128 v[68:71], v28 offset:0\n" \
"v_mfma_f32_16x16x32_bf16 a[188:191], v[72:75], v[112:115], a[188:191]\n" \
"ds_read_b128 v[132:135], v29 offset:0\n" \
"v_mfma_f32_16x16x32_bf16 a[188:191], v[136:139], v[176:179], a[188:191]\n" \
"s_add_u32 m0, s30, 0x9800\n" \
"s_mov_b32 s85, 0xc380\n" \
"v_mfma_f32_16x16x32_bf16 a[184:187], v[72:75], v[108:111], a[184:187]\n" \
"buffer_load_dwordx4 v22, s[64:67], s85 offen lds\n" \
"v_mfma_f32_16x16x32_bf16 a[184:187], v[136:139], v[172:175], a[184:187]\n" \
"ds_read_b64_tr_b16 v[154:155], v33 offset:9216\n" \
"v_mfma_f32_16x16x32_bf16 a[180:183], v[72:75], v[104:107], a[180:183]\n" \
"s_add_u32 m0, s30, 0x9c00\n" \
"s_mov_b32 s84, 0xe380\n" \
"v_mfma_f32_16x16x32_bf16 a[180:183], v[136:139], v[168:171], a[180:183]\n" \
"buffer_load_dwordx4 v23, s[64:67], s84 offen lds\n" \
"v_mfma_f32_16x16x32_bf16 a[176:179], v[72:75], v[100:103], a[176:179]\n" \
"ds_read_b64_tr_b16 v[92:93], v34 offset:0\n" \
"v_mfma_f32_16x16x32_bf16 a[176:179], v[136:139], v[164:167], a[176:179]\n" \
"ds_read_b128 v[72:75], v28 offset:2048\n" \
"v_mfma_f32_16x16x32_bf16 a[208:211], v[76:79], v[100:103], a[208:211]\n" \
"ds_read_b128 v[136:139], v29 offset:2048\n" \
"v_mfma_f32_16x16x32_bf16 a[208:211], v[140:143], v[164:167], a[208:211]\n" \
"ds_read_b64_tr_b16 v[94:95], v34 offset:1024\n" \
"v_mfma_f32_16x16x32_bf16 a[212:215], v[76:79], v[104:107], a[212:215]\n" \
"ds_read_b64_tr_b16 v[156:157], v34 offset:8192\n" \
"v_mfma_f32_16x16x32_bf16 a[212:215], v[140:143], v[168:171], a[212:215]\n" \
"ds_read_b64_tr_b16 v[158:159], v34 offset:9216\n" \
"v_mfma_f32_16x16x32_bf16 a[216:219], v[76:79], v[108:111], a[216:219]\n" \
"ds_read_b64_tr_b16 v[96:97], v35 offset:0\n" \
"v_mfma_f32_16x16x32_bf16 a[216:219], v[140:143], v[172:175], a[216:219]\n" \
"ds_read_b64_tr_b16 v[98:99], v35 offset:1024\n" \
"v_mfma_f32_16x16x32_bf16 a[220:223], v[76:79], v[112:115], a[220:223]\n" \
"ds_read_b64_tr_b16 v[160:161], v35 offset:8192\n" \
"v_mfma_f32_16x16x32_bf16 a[220:223], v[140:143], v[176:179], a[220:223]\n" \
"ds_read_b128 v[76:79], v28 offset:4096\n" \
"v_mfma_f32_16x16x32_bf16 a[252:255], v[80:83], v[112:115], a[252:255]\n" \
"ds_read_b128 v[140:143], v29 offset:4096\n" \
"v_mfma_f32_16x16x32_bf16 a[252:255], v[144:147], v[176:179], a[252:255]\n" \
"ds_read_b64_tr_b16 v[162:163], v35 offset:9216\n" \
"v_mfma_f32_16x16x32_bf16 a[248:251], v[80:83], v[108:111], a[248:251]\n" \
"v_mfma_f32_16x16x32_bf16 a[248:251], v[144:147], v[172:175], a[248:251]\n" \
"v_mfma_f32_16x16x32_bf16 a[244:247], v[80:83], v[104:107], a[244:247]\n" \
"v_mfma_f32_16x16x32_bf16 a[244:247], v[144:147], v[168:171], a[244:247]\n" \
"v_mfma_f32_16x16x32_bf16 a[240:243], v[80:83], v[100:103], a[240:243]\n" \
"v_mfma_f32_16x16x32_bf16 a[240:243], v[144:147], v[164:167], a[240:243]\n" \
"ds_read_b128 v[80:83], v28 offset:6144\n" \
"ds_read_b128 v[144:147], v29 offset:6144\n" \
"s_waitcnt lgkmcnt(15)\n" \
"v_mfma_f32_16x16x32_bf16 a[0:3], v[52:55], v[84:87], a[0:3]\n" \
"s_add_u32 m0, s31, 0x8c00\n" \
"s_mov_b32 s85, 0x398000\n" \
"v_mfma_f32_16x16x32_bf16 a[0:3], v[116:119], v[148:151], a[0:3]\n" \
"buffer_load_dwordx4 v27, s[68:71], s85 offen lds\n" \
"v_mfma_f32_16x16x32_bf16 a[4:7], v[52:55], v[88:91], a[4:7]\n" \
"ds_read_b64_tr_b16 v[100:101], v36 offset:0\n" \
"s_waitcnt lgkmcnt(15)\n" \
"v_mfma_f32_16x16x32_bf16 a[4:7], v[116:119], v[152:155], a[4:7]\n" \
"s_add_u32 m0, s31, 0xcc00\n" \
"s_mov_b32 s84, 0x398000\n" \
"s_waitcnt lgkmcnt(11)\n" \
"v_mfma_f32_16x16x32_bf16 a[8:11], v[52:55], v[92:95], a[8:11]\n" \
"buffer_load_dwordx4 v27, s[72:75], s84 offen lds\n" \
"s_waitcnt lgkmcnt(9)\n" \
"v_mfma_f32_16x16x32_bf16 a[8:11], v[116:119], v[156:159], a[8:11]\n" \
"ds_read_b64_tr_b16 v[102:103], v36 offset:1024\n" \
"s_waitcnt lgkmcnt(8)\n" \
"v_mfma_f32_16x16x32_bf16 a[12:15], v[52:55], v[96:99], a[12:15]\n" \
"ds_read_b64_tr_b16 v[164:165], v36 offset:8192\n" \
"s_waitcnt lgkmcnt(5)\n" \
"v_mfma_f32_16x16x32_bf16 a[12:15], v[116:119], v[160:163], a[12:15]\n" \
"v_mfma_f32_16x16x32_bf16 a[44:47], v[56:59], v[96:99], a[44:47]\n" \
"v_mfma_f32_16x16x32_bf16 a[44:47], v[120:123], v[160:163], a[44:47]\n" \
"ds_read_b64_tr_b16 v[166:167], v36 offset:9216\n" \
"v_mfma_f32_16x16x32_bf16 a[40:43], v[56:59], v[92:95], a[40:43]\n" \
"v_mfma_f32_16x16x32_bf16 a[40:43], v[120:123], v[156:159], a[40:43]\n" \
"v_mfma_f32_16x16x32_bf16 a[36:39], v[56:59], v[88:91], a[36:39]\n" \
"v_mfma_f32_16x16x32_bf16 a[36:39], v[120:123], v[152:155], a[36:39]\n" \
"ds_read_b64_tr_b16 v[104:105], v37 offset:0\n" \
"v_mfma_f32_16x16x32_bf16 a[32:35], v[56:59], v[84:87], a[32:35]\n" \
"v_mfma_f32_16x16x32_bf16 a[32:35], v[120:123], v[148:151], a[32:35]\n" \
"v_mfma_f32_16x16x32_bf16 a[64:67], v[60:63], v[84:87], a[64:67]\n" \
"ds_read_b64_tr_b16 v[106:107], v37 offset:1024\n" \
"v_mfma_f32_16x16x32_bf16 a[64:67], v[124:127], v[148:151], a[64:67]\n" \
"v_mfma_f32_16x16x32_bf16 a[68:71], v[60:63], v[88:91], a[68:71]\n" \
"v_mfma_f32_16x16x32_bf16 a[68:71], v[124:127], v[152:155], a[68:71]\n" \
"ds_read_b64_tr_b16 v[168:169], v37 offset:8192\n" \
"v_mfma_f32_16x16x32_bf16 a[72:75], v[60:63], v[92:95], a[72:75]\n" \
"v_mfma_f32_16x16x32_bf16 a[72:75], v[124:127], v[156:159], a[72:75]\n" \
"v_mfma_f32_16x16x32_bf16 a[76:79], v[60:63], v[96:99], a[76:79]\n" \
"ds_read_b64_tr_b16 v[170:171], v37 offset:9216\n" \
"v_mfma_f32_16x16x32_bf16 a[76:79], v[124:127], v[160:163], a[76:79]\n" \
"v_mfma_f32_16x16x32_bf16 a[108:111], v[64:67], v[96:99], a[108:111]\n" \
"v_mfma_f32_16x16x32_bf16 a[108:111], v[128:131], v[160:163], a[108:111]\n" \
"v_mfma_f32_16x16x32_bf16 a[104:107], v[64:67], v[92:95], a[104:107]\n" \
"ds_read_b64_tr_b16 v[108:109], v38 offset:0\n" \
"v_mfma_f32_16x16x32_bf16 a[104:107], v[128:131], v[156:159], a[104:107]\n" \
"v_mfma_f32_16x16x32_bf16 a[100:103], v[64:67], v[88:91], a[100:103]\n" \
"v_mfma_f32_16x16x32_bf16 a[100:103], v[128:131], v[152:155], a[100:103]\n" \
"ds_read_b64_tr_b16 v[110:111], v38 offset:1024\n" \
"v_mfma_f32_16x16x32_bf16 a[96:99], v[64:67], v[84:87], a[96:99]\n" \
"v_mfma_f32_16x16x32_bf16 a[96:99], v[128:131], v[148:151], a[96:99]\n" \
"v_mfma_f32_16x16x32_bf16 a[128:131], v[68:71], v[84:87], a[128:131]\n" \
"v_bfe_u32 v44, v20, 2, 5\n" \
"ds_read_b64_tr_b16 v[172:173], v38 offset:8192\n" \
"v_mfma_f32_16x16x32_bf16 a[128:131], v[132:135], v[148:151], a[128:131]\n" \
"buffer_load_dword v44, v44, s[80:83], s38 offen offset:128 sc1\n" \
"v_mfma_f32_16x16x32_bf16 a[132:135], v[68:71], v[88:91], a[132:135]\n" \
"v_mfma_f32_16x16x32_bf16 a[132:135], v[132:135], v[152:155], a[132:135]\n" \
"ds_read_b64_tr_b16 v[174:175], v38 offset:9216\n" \
"v_mfma_f32_16x16x32_bf16 a[136:139], v[68:71], v[92:95], a[136:139]\n" \
"v_mfma_f32_16x16x32_bf16 a[136:139], v[132:135], v[156:159], a[136:139]\n" \
"v_mfma_f32_16x16x32_bf16 a[140:143], v[68:71], v[96:99], a[140:143]\n" \
"v_mfma_f32_16x16x32_bf16 a[140:143], v[132:135], v[160:163], a[140:143]\n" \
"ds_read_b64_tr_b16 v[112:113], v39 offset:0\n" \
"v_mfma_f32_16x16x32_bf16 a[172:175], v[72:75], v[96:99], a[172:175]\n" \
"v_mfma_f32_16x16x32_bf16 a[172:175], v[136:139], v[160:163], a[172:175]\n" \
"v_mfma_f32_16x16x32_bf16 a[168:171], v[72:75], v[92:95], a[168:171]\n" \
"ds_read_b64_tr_b16 v[114:115], v39 offset:1024\n" \
"v_mfma_f32_16x16x32_bf16 a[168:171], v[136:139], v[156:159], a[168:171]\n" \
"v_mfma_f32_16x16x32_bf16 a[164:167], v[72:75], v[88:91], a[164:167]\n" \
"v_mfma_f32_16x16x32_bf16 a[164:167], v[136:139], v[152:155], a[164:167]\n" \
"ds_read_b64_tr_b16 v[176:177], v39 offset:8192\n" \
"v_mfma_f32_16x16x32_bf16 a[160:163], v[72:75], v[84:87], a[160:163]\n" \
"v_mfma_f32_16x16x32_bf16 a[160:163], v[136:139], v[148:151], a[160:163]\n" \
"v_mfma_f32_16x16x32_bf16 a[192:195], v[76:79], v[84:87], a[192:195]\n" \
"ds_read_b64_tr_b16 v[178:179], v39 offset:9216\n" \
"v_mfma_f32_16x16x32_bf16 a[192:195], v[140:143], v[148:151], a[192:195]\n" \
"v_mfma_f32_16x16x32_bf16 a[196:199], v[76:79], v[88:91], a[196:199]\n" \
"v_mfma_f32_16x16x32_bf16 a[196:199], v[140:143], v[152:155], a[196:199]\n" \
"v_mfma_f32_16x16x32_bf16 a[200:203], v[76:79], v[92:95], a[200:203]\n" \
"v_mfma_f32_16x16x32_bf16 a[200:203], v[140:143], v[156:159], a[200:203]\n" \
"v_mfma_f32_16x16x32_bf16 a[204:207], v[76:79], v[96:99], a[204:207]\n" \
"v_mfma_f32_16x16x32_bf16 a[204:207], v[140:143], v[160:163], a[204:207]\n" \
"s_waitcnt lgkmcnt(15)\n" \
"v_mfma_f32_16x16x32_bf16 a[236:239], v[80:83], v[96:99], a[236:239]\n" \
"v_mfma_f32_16x16x32_bf16 a[236:239], v[144:147], v[160:163], a[236:239]\n" \
"v_mfma_f32_16x16x32_bf16 a[232:235], v[80:83], v[92:95], a[232:235]\n" \
"v_mfma_f32_16x16x32_bf16 a[232:235], v[144:147], v[156:159], a[232:235]\n" \
"v_mfma_f32_16x16x32_bf16 a[228:231], v[80:83], v[88:91], a[228:231]\n" \
"v_mfma_f32_16x16x32_bf16 a[228:231], v[144:147], v[152:155], a[228:231]\n" \
"v_mfma_f32_16x16x32_bf16 a[224:227], v[80:83], v[84:87], a[224:227]\n" \
"v_mfma_f32_16x16x32_bf16 a[224:227], v[144:147], v[148:151], a[224:227]\n" \
"s_waitcnt vmcnt(0) lgkmcnt(0)\n" \
"s_barrier\n" \
"v_cmp_ne_u32_e32 vcc, s62, v44\n" \
"s_nop 4\n" \
"s_cbranch_vccnz L_poll61_%=\n" \
"L_pollr61_%=:\n" \
"s_mov_b32 s68, s12\n" \
"s_and_b32 s69, s13, 0xffff\n" \
"s_mov_b32 s70, 0x480000\n" \
"s_mov_b32 s71, 0x20000\n" \
"v_mfma_f32_16x16x32_bf16 a[16:19], v[52:55], v[100:103], a[16:19]\n" \
"s_add_u32 m0, s40, 0x0\n" \
"s_add_u32 s85, s39, 0x0\n" \
"s_nop 0\n" \
"v_mfma_f32_16x16x32_bf16 a[16:19], v[116:119], v[164:167], a[16:19]\n" \
"buffer_load_dwordx4 v20, s[80:83], s85 offen sc1 lds\n" \
"v_mfma_f32_16x16x32_bf16 a[20:23], v[52:55], v[104:107], a[20:23]\n" \
"ds_read_b64_tr_b16 v[84:85], v32 offset:32768\n" \
"v_mfma_f32_16x16x32_bf16 a[20:23], v[116:119], v[168:171], a[20:23]\n" \
"s_add_u32 m0, s40, 0x1000\n" \
"s_add_u32 s84, s39, 0x1000\n" \
"v_mfma_f32_16x16x32_bf16 a[24:27], v[52:55], v[108:111], a[24:27]\n" \
"buffer_load_dwordx4 v20, s[80:83], s84 offen sc1 lds\n" \
"v_mfma_f32_16x16x32_bf16 a[24:27], v[116:119], v[172:175], a[24:27]\n" \
"ds_read_b64_tr_b16 v[86:87], v32 offset:33792\n" \
"v_mfma_f32_16x16x32_bf16 a[28:31], v[52:55], v[112:115], a[28:31]\n" \
"ds_read_b64_tr_b16 v[148:149], v32 offset:40960\n" \
"v_mfma_f32_16x16x32_bf16 a[28:31], v[116:119], v[176:179], a[28:31]\n" \
"ds_read_b128 v[52:55], v28 offset:49152\n" \
"v_mfma_f32_16x16x32_bf16 a[60:63], v[56:59], v[112:115], a[60:63]\n" \
"ds_read_b128 v[116:119], v29 offset:49152\n" \
"v_mfma_f32_16x16x32_bf16 a[60:63], v[120:123], v[176:179], a[60:63]\n" \
"s_add_u32 m0, s40, 0x3000\n" \
"s_mov_b32 s85, 0x0\n" \
"v_mfma_f32_16x16x32_bf16 a[56:59], v[56:59], v[108:111], a[56:59]\n" \
"buffer_load_dwordx4 v21, s[76:79], s85 offen lds\n" \
"v_mfma_f32_16x16x32_bf16 a[56:59], v[120:123], v[172:175], a[56:59]\n" \
"s_add_u32 m0, s30, 0x0\n" \
"s_mov_b32 s84, 0x0\n" \
"s_nop 0\n" \
"v_mfma_f32_16x16x32_bf16 a[52:55], v[56:59], v[104:107], a[52:55]\n" \
"buffer_load_dwordx4 v24, s[76:79], s84 offen lds\n" \
"v_mfma_f32_16x16x32_bf16 a[52:55], v[120:123], v[168:171], a[52:55]\n" \
"ds_read_b64_tr_b16 v[150:151], v32 offset:41984\n" \
"v_mfma_f32_16x16x32_bf16 a[48:51], v[56:59], v[100:103], a[48:51]\n" \
"ds_read_b64_tr_b16 v[88:89], v33 offset:32768\n" \
"v_mfma_f32_16x16x32_bf16 a[48:51], v[120:123], v[164:167], a[48:51]\n" \
"ds_read_b128 v[56:59], v28 offset:51200\n" \
"v_mfma_f32_16x16x32_bf16 a[80:83], v[60:63], v[100:103], a[80:83]\n" \
"ds_read_b128 v[120:123], v29 offset:51200\n" \
"v_mfma_f32_16x16x32_bf16 a[80:83], v[124:127], v[164:167], a[80:83]\n" \
"s_add_u32 m0, s30, 0x400\n" \
"s_mov_b32 s85, 0x2400\n" \
"v_mfma_f32_16x16x32_bf16 a[84:87], v[60:63], v[104:107], a[84:87]\n" \
"buffer_load_dwordx4 v25, s[76:79], s85 offen lds\n" \
"v_mfma_f32_16x16x32_bf16 a[84:87], v[124:127], v[168:171], a[84:87]\n" \
"s_add_u32 m0, s31, 0x0\n" \
"s_mov_b32 s84, 0x0\n" \
"v_mfma_f32_16x16x32_bf16 a[88:91], v[60:63], v[108:111], a[88:91]\n" \
"buffer_load_dwordx4 v26, s[68:71], s84 offen lds\n" \
"v_mfma_f32_16x16x32_bf16 a[88:91], v[124:127], v[172:175], a[88:91]\n" \
"ds_read_b64_tr_b16 v[90:91], v33 offset:33792\n" \
"v_mfma_f32_16x16x32_bf16 a[92:95], v[60:63], v[112:115], a[92:95]\n" \
"ds_read_b64_tr_b16 v[152:153], v33 offset:40960\n" \
"v_mfma_f32_16x16x32_bf16 a[92:95], v[124:127], v[176:179], a[92:95]\n" \
"ds_read_b128 v[60:63], v28 offset:53248\n" \
"v_mfma_f32_16x16x32_bf16 a[124:127], v[64:67], v[112:115], a[124:127]\n" \
"ds_read_b128 v[124:127], v29 offset:53248\n" \
"v_mfma_f32_16x16x32_bf16 a[124:127], v[128:131], v[176:179], a[124:127]\n" \
"s_add_u32 m0, s30, 0x800\n" \
"s_mov_b32 s85, 0x4800\n" \
"v_mfma_f32_16x16x32_bf16 a[120:123], v[64:67], v[108:111], a[120:123]\n" \
"buffer_load_dwordx4 v24, s[76:79], s85 offen lds\n" \
"v_mfma_f32_16x16x32_bf16 a[120:123], v[128:131], v[172:175], a[120:123]\n" \
"s_add_u32 m0, s30, 0xc00\n" \
"s_mov_b32 s84, 0x6c00\n" \
"v_mfma_f32_16x16x32_bf16 a[116:119], v[64:67], v[104:107], a[116:119]\n" \
"buffer_load_dwordx4 v25, s[76:79], s84 offen lds\n" \
"v_mfma_f32_16x16x32_bf16 a[116:119], v[128:131], v[168:171], a[116:119]\n" \
"s_add_u32 m0, s31, 0x400\n" \
"s_mov_b32 s85, 0x8000\n" \
"v_mfma_f32_16x16x32_bf16 a[112:115], v[64:67], v[100:103], a[112:115]\n" \
"buffer_load_dwordx4 v26, s[68:71], s85 offen lds\n" \
"v_mfma_f32_16x16x32_bf16 a[112:115], v[128:131], v[164:167], a[112:115]\n" \
"ds_read_b128 v[64:67], v28 offset:55296\n" \
"v_mfma_f32_16x16x32_bf16 a[144:147], v[68:71], v[100:103], a[144:147]\n" \
"ds_read_b128 v[128:131], v29 offset:55296\n" \
"v_mfma_f32_16x16x32_bf16 a[144:147], v[132:135], v[164:167], a[144:147]\n" \
"s_add_u32 m0, s30, 0x1000\n" \
"s_mov_b32 s84, 0x9000\n" \
"v_mfma_f32_16x16x32_bf16 a[148:151], v[68:71], v[104:107], a[148:151]\n" \
"buffer_load_dwordx4 v24, s[76:79], s84 offen lds\n" \
"v_mfma_f32_16x16x32_bf16 a[148:151], v[132:135], v[168:171], a[148:151]\n" \
"ds_read_b64_tr_b16 v[154:155], v33 offset:41984\n" \
"v_mfma_f32_16x16x32_bf16 a[152:155], v[68:71], v[108:111], a[152:155]\n" \
"s_add_u32 m0, s30, 0x1400\n" \
"s_mov_b32 s85, 0xb400\n" \
"v_mfma_f32_16x16x32_bf16 a[152:155], v[132:135], v[172:175], a[152:155]\n" \
"buffer_load_dwordx4 v25, s[76:79], s85 offen lds\n" \
"v_mfma_f32_16x16x32_bf16 a[156:159], v[68:71], v[112:115], a[156:159]\n" \
"ds_read_b64_tr_b16 v[92:93], v34 offset:32768\n" \
"v_mfma_f32_16x16x32_bf16 a[156:159], v[132:135], v[176:179], a[156:159]\n" \
"ds_read_b128 v[68:71], v28 offset:32768\n" \
"v_mfma_f32_16x16x32_bf16 a[188:191], v[72:75], v[112:115], a[188:191]\n" \
"ds_read_b128 v[132:135], v29 offset:32768\n" \
"v_mfma_f32_16x16x32_bf16 a[188:191], v[136:139], v[176:179], a[188:191]\n" \
"s_add_u32 m0, s31, 0x800\n" \
"s_mov_b32 s84, 0x10000\n" \
"v_mfma_f32_16x16x32_bf16 a[184:187], v[72:75], v[108:111], a[184:187]\n" \
"buffer_load_dwordx4 v27, s[68:71], s84 offen lds\n" \
"v_mfma_f32_16x16x32_bf16 a[184:187], v[136:139], v[172:175], a[184:187]\n" \
"ds_read_b64_tr_b16 v[94:95], v34 offset:33792\n" \
"v_mfma_f32_16x16x32_bf16 a[180:183], v[72:75], v[104:107], a[180:183]\n" \
"s_add_u32 m0, s30, 0x1800\n" \
"s_mov_b32 s85, 0xd800\n" \
"v_mfma_f32_16x16x32_bf16 a[180:183], v[136:139], v[168:171], a[180:183]\n" \
"buffer_load_dwordx4 v24, s[76:79], s85 offen lds\n" \
"v_mfma_f32_16x16x32_bf16 a[176:179], v[72:75], v[100:103], a[176:179]\n" \
"ds_read_b64_tr_b16 v[156:157], v34 offset:40960\n" \
"v_mfma_f32_16x16x32_bf16 a[176:179], v[136:139], v[164:167], a[176:179]\n" \
"ds_read_b128 v[72:75], v28 offset:34816\n" \
"v_mfma_f32_16x16x32_bf16 a[208:211], v[76:79], v[100:103], a[208:211]\n" \
"ds_read_b128 v[136:139], v29 offset:34816\n" \
"v_mfma_f32_16x16x32_bf16 a[208:211], v[140:143], v[164:167], a[208:211]\n" \
"ds_read_b64_tr_b16 v[158:159], v34 offset:41984\n" \
"v_mfma_f32_16x16x32_bf16 a[212:215], v[76:79], v[104:107], a[212:215]\n" \
"ds_read_b64_tr_b16 v[96:97], v35 offset:32768\n" \
"v_mfma_f32_16x16x32_bf16 a[212:215], v[140:143], v[168:171], a[212:215]\n" \
"ds_read_b64_tr_b16 v[98:99], v35 offset:33792\n" \
"v_mfma_f32_16x16x32_bf16 a[216:219], v[76:79], v[108:111], a[216:219]\n" \
"ds_read_b64_tr_b16 v[160:161], v35 offset:40960\n" \
"v_mfma_f32_16x16x32_bf16 a[216:219], v[140:143], v[172:175], a[216:219]\n" \
"ds_read_b64_tr_b16 v[162:163], v35 offset:41984\n" \
"v_mfma_f32_16x16x32_bf16 a[220:223], v[76:79], v[112:115], a[220:223]\n" \
"v_mfma_f32_16x16x32_bf16 a[220:223], v[140:143], v[176:179], a[220:223]\n" \
"ds_read_b128 v[76:79], v28 offset:36864\n" \
"v_mfma_f32_16x16x32_bf16 a[252:255], v[80:83], v[112:115], a[252:255]\n" \
"ds_read_b128 v[140:143], v29 offset:36864\n" \
"v_mfma_f32_16x16x32_bf16 a[252:255], v[144:147], v[176:179], a[252:255]\n" \
"v_mfma_f32_16x16x32_bf16 a[248:251], v[80:83], v[108:111], a[248:251]\n" \
"v_mfma_f32_16x16x32_bf16 a[248:251], v[144:147], v[172:175], a[248:251]\n" \
"v_mfma_f32_16x16x32_bf16 a[244:247], v[80:83], v[104:107], a[244:247]\n" \
"v_mfma_f32_16x16x32_bf16 a[244:247], v[144:147], v[168:171], a[244:247]\n" \
"v_mfma_f32_16x16x32_bf16 a[240:243], v[80:83], v[100:103], a[240:243]\n" \
"v_mfma_f32_16x16x32_bf16 a[240:243], v[144:147], v[164:167], a[240:243]\n" \
"ds_read_b128 v[80:83], v28 offset:38912\n" \
"ds_read_b128 v[144:147], v29 offset:38912\n" \
"s_waitcnt lgkmcnt(15)\n" \
"v_mfma_f32_16x16x32_bf16 a[0:3], v[52:55], v[84:87], a[0:3]\n" \
"s_add_u32 m0, s30, 0x1c00\n" \
"s_mov_b32 s85, 0xfc00\n" \
"v_mfma_f32_16x16x32_bf16 a[0:3], v[116:119], v[148:151], a[0:3]\n" \
"buffer_load_dwordx4 v25, s[76:79], s85 offen lds\n" \
"v_mfma_f32_16x16x32_bf16 a[4:7], v[52:55], v[88:91], a[4:7]\n" \
"ds_read_b64_tr_b16 v[100:101], v36 offset:32768\n" \
"v_mfma_f32_16x16x32_bf16 a[4:7], v[116:119], v[152:155], a[4:7]\n" \
"s_add_u32 m0, s31, 0xc00\n" \
"s_mov_b32 s84, 0x18000\n" \
"s_waitcnt lgkmcnt(13)\n" \
"v_mfma_f32_16x16x32_bf16 a[8:11], v[52:55], v[92:95], a[8:11]\n" \
"buffer_load_dwordx4 v27, s[68:71], s84 offen lds\n" \
"s_waitcnt lgkmcnt(9)\n" \
"v_mfma_f32_16x16x32_bf16 a[8:11], v[116:119], v[156:159], a[8:11]\n" \
"ds_read_b64_tr_b16 v[102:103], v36 offset:33792\n" \
"s_waitcnt lgkmcnt(8)\n" \
"v_mfma_f32_16x16x32_bf16 a[12:15], v[52:55], v[96:99], a[12:15]\n" \
"ds_read_b64_tr_b16 v[164:165], v36 offset:40960\n" \
"s_waitcnt lgkmcnt(7)\n" \
"v_mfma_f32_16x16x32_bf16 a[12:15], v[116:119], v[160:163], a[12:15]\n" \
"v_mfma_f32_16x16x32_bf16 a[44:47], v[56:59], v[96:99], a[44:47]\n" \
"v_mfma_f32_16x16x32_bf16 a[44:47], v[120:123], v[160:163], a[44:47]\n" \
"ds_read_b64_tr_b16 v[166:167], v36 offset:41984\n" \
"v_mfma_f32_16x16x32_bf16 a[40:43], v[56:59], v[92:95], a[40:43]\n" \
"v_mfma_f32_16x16x32_bf16 a[40:43], v[120:123], v[156:159], a[40:43]\n" \
"v_mfma_f32_16x16x32_bf16 a[36:39], v[56:59], v[88:91], a[36:39]\n" \
"s_nop 1\n" \
"v_accvgpr_read_b32 v184, a12\n" \
"v_accvgpr_read_b32 v185, a13\n" \
"v_mfma_f32_16x16x32_bf16 a[36:39], v[120:123], v[152:155], a[36:39]\n" \
"ds_read_b64_tr_b16 v[104:105], v37 offset:32768\n" \
"v_accvgpr_read_b32 v186, a14\n" \
"v_mfma_f32_16x16x32_bf16 a[32:35], v[56:59], v[84:87], a[32:35]\n" \
"v_accvgpr_read_b32 v187, a15\n" \
"v_accvgpr_read_b32 v180, a44\n" \
"v_mfma_f32_16x16x32_bf16 a[32:35], v[120:123], v[148:151], a[32:35]\n" \
"v_accvgpr_read_b32 v181, a45\n" \
"v_accvgpr_read_b32 v182, a46\n" \
"v_mfma_f32_16x16x32_bf16 a[64:67], v[60:63], v[84:87], a[64:67]\n" \
"ds_read_b64_tr_b16 v[106:107], v37 offset:33792\n" \
"v_accvgpr_read_b32 v183, a47\n" \
"v_mfma_f32_16x16x32_bf16 a[64:67], v[124:127], v[148:151], a[64:67]\n" \
"v_cvt_pk_bf16_f32 v184, v184, v185\n" \
"v_cvt_pk_bf16_f32 v185, v186, v187\n" \
"v_mfma_f32_16x16x32_bf16 a[68:71], v[60:63], v[88:91], a[68:71]\n" \
"v_lshlrev_b32_e32 v45, 3, v20\n" \
"v_add_u32_e32 v45, s31, v45\n" \
"v_mfma_f32_16x16x32_bf16 a[68:71], v[124:127], v[152:155], a[68:71]\n" \
"s_getpc_b64 s[86:87]\n" \
"s_sub_u32 s86, s86, 0x10800\n" \
"s_subb_u32 s87, s87, 0\n" \
"s_mov_b32 m0, s32\n" \
"s_nop 0\n" \
"v_mfma_f32_16x16x32_bf16 a[72:75], v[60:63], v[92:95], a[72:75]\n" \
"global_load_lds_dword v45, s[86:87]\n" \
"v_mfma_f32_16x16x32_bf16 a[72:75], v[124:127], v[156:159], a[72:75]\n" \
"ds_read_b64_tr_b16 v[168:169], v37 offset:40960\n" \
"v_cvt_pk_bf16_f32 v186, v180, v181\n" \
"v_mfma_f32_16x16x32_bf16 a[76:79], v[60:63], v[96:99], a[76:79]\n" \
"ds_read_b64_tr_b16 v[170:171], v37 offset:41984\n" \
"v_cvt_pk_bf16_f32 v187, v182, v183\n" \
"v_mfma_f32_16x16x32_bf16 a[76:79], v[124:127], v[160:163], a[76:79]\n" \
"v_accvgpr_read_b32 v188, a8\n" \
"v_accvgpr_read_b32 v189, a9\n" \
"v_mfma_f32_16x16x32_bf16 a[108:111], v[64:67], v[96:99], a[108:111]\n" \
"v_accvgpr_read_b32 v190, a10\n" \
"v_accvgpr_read_b32 v191, a11\n" \
"v_mfma_f32_16x16x32_bf16 a[108:111], v[128:131], v[160:163], a[108:111]\n" \
"v_accvgpr_read_b32 v180, a40\n" \
"v_accvgpr_read_b32 v181, a41\n" \
"v_mfma_f32_16x16x32_bf16 a[104:107], v[64:67], v[92:95], a[104:107]\n" \
"ds_read_b64_tr_b16 v[108:109], v38 offset:32768\n" \
"v_accvgpr_read_b32 v182, a42\n" \
"v_mfma_f32_16x16x32_bf16 a[104:107], v[128:131], v[156:159], a[104:107]\n" \
"v_accvgpr_read_b32 v183, a43\n" \
"v_cvt_pk_bf16_f32 v188, v188, v189\n" \
"v_mfma_f32_16x16x32_bf16 a[100:103], v[64:67], v[88:91], a[100:103]\n" \
"v_cvt_pk_bf16_f32 v189, v190, v191\n" \
"v_cvt_pk_bf16_f32 v190, v180, v181\n" \
"v_mfma_f32_16x16x32_bf16 a[100:103], v[128:131], v[152:155], a[100:103]\n" \
"ds_read_b64_tr_b16 v[110:111], v38 offset:33792\n" \
"v_cvt_pk_bf16_f32 v191, v182, v183\n" \
"v_mfma_f32_16x16x32_bf16 a[96:99], v[64:67], v[84:87], a[96:99]\n" \
"v_accvgpr_read_b32 v192, a4\n" \
"v_accvgpr_read_b32 v193, a5\n" \
"v_mfma_f32_16x16x32_bf16 a[96:99], v[128:131], v[148:151], a[96:99]\n" \
"v_accvgpr_read_b32 v194, a6\n" \
"v_accvgpr_read_b32 v195, a7\n" \
"v_mfma_f32_16x16x32_bf16 a[128:131], v[68:71], v[84:87], a[128:131]\n" \
"ds_read_b64_tr_b16 v[172:173], v38 offset:40960\n" \
"v_accvgpr_read_b32 v180, a36\n" \
"v_mfma_f32_16x16x32_bf16 a[128:131], v[132:135], v[148:151], a[128:131]\n" \
"v_accvgpr_read_b32 v181, a37\n" \
"v_accvgpr_read_b32 v182, a38\n" \
"v_mfma_f32_16x16x32_bf16 a[132:135], v[68:71], v[88:91], a[132:135]\n" \
"v_accvgpr_read_b32 v183, a39\n" \
"v_cvt_pk_bf16_f32 v192, v192, v193\n" \
"v_mfma_f32_16x16x32_bf16 a[132:135], v[132:135], v[152:155], a[132:135]\n" \
"ds_read_b64_tr_b16 v[174:175], v38 offset:41984\n" \
"v_cvt_pk_bf16_f32 v193, v194, v195\n" \
"v_mfma_f32_16x16x32_bf16 a[136:139], v[68:71], v[92:95], a[136:139]\n" \
"v_cvt_pk_bf16_f32 v194, v180, v181\n" \
"v_cvt_pk_bf16_f32 v195, v182, v183\n" \
"v_mfma_f32_16x16x32_bf16 a[136:139], v[132:135], v[156:159], a[136:139]\n" \
"v_accvgpr_read_b32 v196, a0\n" \
"v_accvgpr_read_b32 v197, a1\n" \
"v_mfma_f32_16x16x32_bf16 a[140:143], v[68:71], v[96:99], a[140:143]\n" \
"v_accvgpr_read_b32 v198, a2\n" \
"v_accvgpr_read_b32 v199, a3\n" \
"v_mfma_f32_16x16x32_bf16 a[140:143], v[132:135], v[160:163], a[140:143]\n" \
"ds_read_b64_tr_b16 v[112:113], v39 offset:32768\n" \
"v_accvgpr_read_b32 v180, a32\n" \
"v_mfma_f32_16x16x32_bf16 a[172:175], v[72:75], v[96:99], a[172:175]\n" \
"s_add_u32 s89, s41, 0x0\n" \
"s_nop 0\n" \
"buffer_store_dwordx4 v[184:187], v20, s[80:83], s89 offen offset:3072 sc1\n" \
"v_mfma_f32_16x16x32_bf16 a[172:175], v[136:139], v[160:163], a[172:175]\n" \
"v_accvgpr_read_b32 v181, a33\n" \
"v_accvgpr_read_b32 v182, a34\n" \
"v_mfma_f32_16x16x32_bf16 a[168:171], v[72:75], v[92:95], a[168:171]\n" \
"ds_read_b64_tr_b16 v[114:115], v39 offset:33792\n" \
"v_accvgpr_read_b32 v183, a35\n" \
"v_mfma_f32_16x16x32_bf16 a[168:171], v[136:139], v[156:159], a[168:171]\n" \
"v_cvt_pk_bf16_f32 v196, v196, v197\n" \
"v_cvt_pk_bf16_f32 v197, v198, v199\n" \
"v_mfma_f32_16x16x32_bf16 a[164:167], v[72:75], v[88:91], a[164:167]\n" \
"v_cvt_pk_bf16_f32 v198, v180, v181\n" \
"v_cvt_pk_bf16_f32 v199, v182, v183\n" \
"v_mfma_f32_16x16x32_bf16 a[164:167], v[136:139], v[152:155], a[164:167]\n" \
"ds_read_b64_tr_b16 v[176:177], v39 offset:40960\n" \
"v_accvgpr_read_b32 v200, a76\n" \
"v_mfma_f32_16x16x32_bf16 a[160:163], v[72:75], v[84:87], a[160:163]\n" \
"s_add_u32 s88, s41, 0x0\n" \
"buffer_store_dwordx4 v[188:191], v20, s[80:83], s88 offen offset:2048 sc1\n" \
"v_mfma_f32_16x16x32_bf16 a[160:163], v[136:139], v[148:151], a[160:163]\n" \
"v_accvgpr_read_b32 v201, a77\n" \
"v_accvgpr_read_b32 v202, a78\n" \
"s_waitcnt lgkmcnt(15)\n" \
"v_mfma_f32_16x16x32_bf16 a[192:195], v[76:79], v[84:87], a[192:195]\n" \
"ds_read_b64_tr_b16 v[178:179], v39 offset:41984\n" \
"v_accvgpr_read_b32 v203, a79\n" \
"v_mfma_f32_16x16x32_bf16 a[192:195], v[140:143], v[148:151], a[192:195]\n" \
"v_accvgpr_read_b32 v180, a108\n" \
"v_accvgpr_read_b32 v181, a109\n" \
"v_mfma_f32_16x16x32_bf16 a[196:199], v[76:79], v[88:91], a[196:199]\n" \
"v_accvgpr_read_b32 v182, a110\n" \
"v_accvgpr_read_b32 v183, a111\n" \
"v_mfma_f32_16x16x32_bf16 a[196:199], v[140:143], v[152:155], a[196:199]\n" \
"v_cvt_pk_bf16_f32 v200, v200, v201\n" \
"v_cvt_pk_bf16_f32 v201, v202, v203\n" \
"v_mfma_f32_16x16x32_bf16 a[200:203], v[76:79], v[92:95], a[200:203]\n" \
"s_add_u32 s89, s41, 0x0\n" \
"s_nop 0\n" \
"buffer_store_dwordx4 v[192:195], v20, s[80:83], s89 offen offset:1024 sc1\n" \
"v_mfma_f32_16x16x32_bf16 a[200:203], v[140:143], v[156:159], a[200:203]\n" \
"v_cvt_pk_bf16_f32 v202, v180, v181\n" \
"v_cvt_pk_bf16_f32 v203, v182, v183\n" \
"v_mfma_f32_16x16x32_bf16 a[204:207], v[76:79], v[96:99], a[204:207]\n" \
"v_accvgpr_read_b32 v204, a72\n" \
"v_accvgpr_read_b32 v205, a73\n" \
"v_mfma_f32_16x16x32_bf16 a[204:207], v[140:143], v[160:163], a[204:207]\n" \
"v_accvgpr_read_b32 v206, a74\n" \
"v_accvgpr_read_b32 v207, a75\n" \
"v_mfma_f32_16x16x32_bf16 a[236:239], v[80:83], v[96:99], a[236:239]\n" \
"v_accvgpr_read_b32 v180, a104\n" \
"v_accvgpr_read_b32 v181, a105\n" \
"v_mfma_f32_16x16x32_bf16 a[236:239], v[144:147], v[160:163], a[236:239]\n" \
"v_accvgpr_read_b32 v182, a106\n" \
"v_accvgpr_read_b32 v183, a107\n" \
"v_mfma_f32_16x16x32_bf16 a[232:235], v[80:83], v[92:95], a[232:235]\n" \
"s_add_u32 s88, s41, 0x0\n" \
"s_nop 0\n" \
"buffer_store_dwordx4 v[196:199], v20, s[80:83], s88 offen offset:0 sc1\n" \
"v_mfma_f32_16x16x32_bf16 a[232:235], v[144:147], v[156:159], a[232:235]\n" \
"v_cvt_pk_bf16_f32 v204, v204, v205\n" \
"v_cvt_pk_bf16_f32 v205, v206, v207\n" \
"v_mfma_f32_16x16x32_bf16 a[228:231], v[80:83], v[88:91], a[228:231]\n" \
"v_cvt_pk_bf16_f32 v206, v180, v181\n" \
"v_cvt_pk_bf16_f32 v207, v182, v183\n" \
"v_mfma_f32_16x16x32_bf16 a[228:231], v[144:147], v[152:155], a[228:231]\n" \
"v_accvgpr_read_b32 v208, a68\n" \
"v_accvgpr_read_b32 v209, a69\n" \
"v_mfma_f32_16x16x32_bf16 a[224:227], v[80:83], v[84:87], a[224:227]\n" \
"v_accvgpr_read_b32 v210, a70\n" \
"v_accvgpr_read_b32 v211, a71\n" \
"v_mfma_f32_16x16x32_bf16 a[224:227], v[144:147], v[148:151], a[224:227]\n" \
"v_accvgpr_read_b32 v180, a100\n" \
"v_accvgpr_read_b32 v181, a101\n" \
"s_waitcnt vmcnt(5) lgkmcnt(0)\n" \
"s_barrier\n" \
"v_xor_b32_e32 v36, 0x4000, v36\n" \
"v_xor_b32_e64 v32, 0x0, v48\n" \
"v_xor_b32_e32 v37, 0x4000, v37\n" \
"v_xor_b32_e64 v33, 0x20, v48\n" \
"v_xor_b32_e32 v38, 0x4000, v38\n" \
"v_xor_b32_e64 v34, 0x40, v48\n" \
"v_xor_b32_e32 v39, 0x4000, v39\n" \
"v_xor_b32_e32 v35, 0x60, v48\n" \
"v_add_u32_e32 v28, s43, v28\n" \
"v_add_u32_e32 v29, s43, v29\n" \
"v_mfma_f32_16x16x32_bf16 a[144:147], v[68:71], v[100:103], a[144:147]\n" \
"s_add_u32 m0, s40, 0x8000\n" \
"s_add_u32 s85, s39, 0x2000\n" \
"v_mfma_f32_16x16x32_bf16 a[144:147], v[132:135], v[164:167], a[144:147]\n" \
"buffer_load_dwordx4 v20, s[80:83], s85 offen sc1 lds\n" \
"v_mfma_f32_16x16x32_bf16 a[148:151], v[68:71], v[104:107], a[148:151]\n" \
"ds_read_b64_tr_b16 v[84:85], v32 offset:0\n" \
"v_accvgpr_read_b32 v182, a102\n" \
"v_mfma_f32_16x16x32_bf16 a[148:151], v[132:135], v[168:171], a[148:151]\n" \
"s_add_u32 m0, s40, 0x9000\n" \
"s_add_u32 s84, s39, 0x3000\n" \
"v_mfma_f32_16x16x32_bf16 a[152:155], v[68:71], v[108:111], a[152:155]\n" \
"buffer_load_dwordx4 v20, s[80:83], s84 offen sc1 lds\n" \
"v_mfma_f32_16x16x32_bf16 a[152:155], v[132:135], v[172:175], a[152:155]\n" \
"ds_read_b64_tr_b16 v[86:87], v32 offset:512\n" \
"v_accvgpr_read_b32 v183, a103\n" \
"v_mfma_f32_16x16x32_bf16 a[156:159], v[68:71], v[112:115], a[156:159]\n" \
"ds_read_b64_tr_b16 v[148:149], v32 offset:4096\n" \
"v_cvt_pk_bf16_f32 v208, v208, v209\n" \
"v_mfma_f32_16x16x32_bf16 a[156:159], v[132:135], v[176:179], a[156:159]\n" \
"ds_read_b128 v[68:71], v28 offset:8192\n" \
"v_cvt_pk_bf16_f32 v209, v210, v211\n" \
"v_mfma_f32_16x16x32_bf16 a[188:191], v[72:75], v[112:115], a[188:191]\n" \
"ds_read_b128 v[132:135], v29 offset:8192\n" \
"v_cvt_pk_bf16_f32 v210, v180, v181\n" \
"v_mfma_f32_16x16x32_bf16 a[188:191], v[136:139], v[176:179], a[188:191]\n" \
"s_add_u32 m0, s40, 0xb000\n" \
"s_mov_b32 s85, 0x80\n" \
"v_mfma_f32_16x16x32_bf16 a[184:187], v[72:75], v[108:111], a[184:187]\n" \
"buffer_load_dwordx4 v21, s[76:79], s85 offen lds\n" \
"v_mfma_f32_16x16x32_bf16 a[184:187], v[136:139], v[172:175], a[184:187]\n" \
"s_add_u32 m0, s30, 0x8000\n" \
"s_mov_b32 s84, 0x80\n" \
"v_mfma_f32_16x16x32_bf16 a[180:183], v[72:75], v[104:107], a[180:183]\n" \
"buffer_load_dwordx4 v24, s[76:79], s84 offen lds\n" \
"v_mfma_f32_16x16x32_bf16 a[180:183], v[136:139], v[168:171], a[180:183]\n" \
"ds_read_b64_tr_b16 v[150:151], v32 offset:4608\n" \
"v_cvt_pk_bf16_f32 v211, v182, v183\n" \
"v_mfma_f32_16x16x32_bf16 a[176:179], v[72:75], v[100:103], a[176:179]\n" \
"ds_read_b64_tr_b16 v[88:89], v33 offset:0\n" \
"v_accvgpr_read_b32 v212, a64\n" \
"v_mfma_f32_16x16x32_bf16 a[176:179], v[136:139], v[164:167], a[176:179]\n" \
"ds_read_b128 v[72:75], v28 offset:10240\n" \
"v_accvgpr_read_b32 v213, a65\n" \
"v_mfma_f32_16x16x32_bf16 a[208:211], v[76:79], v[100:103], a[208:211]\n" \
"ds_read_b128 v[136:139], v29 offset:10240\n" \
"v_accvgpr_read_b32 v214, a66\n" \
"v_mfma_f32_16x16x32_bf16 a[208:211], v[140:143], v[164:167], a[208:211]\n" \
"s_add_u32 m0, s30, 0x8400\n" \
"s_mov_b32 s85, 0x2480\n" \
"v_mfma_f32_16x16x32_bf16 a[212:215], v[76:79], v[104:107], a[212:215]\n" \
"buffer_load_dwordx4 v25, s[76:79], s85 offen lds\n" \
"v_mfma_f32_16x16x32_bf16 a[212:215], v[140:143], v[168:171], a[212:215]\n" \
"s_add_u32 m0, s31, 0x8000\n" \
"s_mov_b32 s84, 0x80000\n" \
"v_mfma_f32_16x16x32_bf16 a[216:219], v[76:79], v[108:111], a[216:219]\n" \
"buffer_load_dwordx4 v26, s[68:71], s84 offen lds\n" \
"v_mfma_f32_16x16x32_bf16 a[216:219], v[140:143], v[172:175], a[216:219]\n" \
"ds_read_b64_tr_b16 v[90:91], v33 offset:512\n" \
"v_accvgpr_read_b32 v215, a67\n" \
"v_mfma_f32_16x16x32_bf16 a[220:223], v[76:79], v[112:115], a[220:223]\n" \
"ds_read_b64_tr_b16 v[152:153], v33 offset:4096\n" \
"v_accvgpr_read_b32 v180, a96\n" \
"v_mfma_f32_16x16x32_bf16 a[220:223], v[140:143], v[176:179], a[220:223]\n" \
"ds_read_b128 v[76:79], v28 offset:12288\n" \
"v_accvgpr_read_b32 v181, a97\n" \
"v_mfma_f32_16x16x32_bf16 a[252:255], v[80:83], v[112:115], a[252:255]\n" \
"ds_read_b128 v[140:143], v29 offset:12288\n" \
"v_accvgpr_read_b32 v182, a98\n" \
"v_mfma_f32_16x16x32_bf16 a[252:255], v[144:147], v[176:179], a[252:255]\n" \
"s_add_u32 m0, s30, 0x8800\n" \
"s_mov_b32 s85, 0x4880\n" \
"v_mfma_f32_16x16x32_bf16 a[248:251], v[80:83], v[108:111], a[248:251]\n" \
"buffer_load_dwordx4 v24, s[76:79], s85 offen lds\n" \
"v_mfma_f32_16x16x32_bf16 a[248:251], v[144:147], v[172:175], a[248:251]\n" \
"s_add_u32 m0, s30, 0x8c00\n" \
"s_mov_b32 s84, 0x6c80\n" \
"v_mfma_f32_16x16x32_bf16 a[244:247], v[80:83], v[104:107], a[244:247]\n" \
"buffer_load_dwordx4 v25, s[76:79], s84 offen lds\n" \
"v_mfma_f32_16x16x32_bf16 a[244:247], v[144:147], v[168:171], a[244:247]\n" \
"s_add_u32 m0, s31, 0x8400\n" \
"s_mov_b32 s85, 0x88000\n" \
"v_mfma_f32_16x16x32_bf16 a[240:243], v[80:83], v[100:103], a[240:243]\n" \
"buffer_load_dwordx4 v26, s[68:71], s85 offen lds\n" \
"v_mfma_f32_16x16x32_bf16 a[240:243], v[144:147], v[164:167], a[240:243]\n" \
"ds_read_b128 v[80:83], v28 offset:14336\n" \
"v_accvgpr_read_b32 v183, a99\n" \
"v_mfma_f32_16x16x32_bf16 a[80:83], v[60:63], v[100:103], a[80:83]\n" \
"ds_read_b128 v[144:147], v29 offset:14336\n" \
"v_cvt_pk_bf16_f32 v212, v212, v213\n" \
"v_mfma_f32_16x16x32_bf16 a[80:83], v[124:127], v[164:167], a[80:83]\n" \
"s_add_u32 m0, s30, 0x9000\n" \
"s_mov_b32 s84, 0x9080\n" \
"v_mfma_f32_16x16x32_bf16 a[84:87], v[60:63], v[104:107], a[84:87]\n" \
"buffer_load_dwordx4 v24, s[76:79], s84 offen lds\n" \
"v_mfma_f32_16x16x32_bf16 a[84:87], v[124:127], v[168:171], a[84:87]\n" \
"ds_read_b64_tr_b16 v[154:155], v33 offset:4608\n" \
"v_cvt_pk_bf16_f32 v213, v214, v215\n" \
"v_mfma_f32_16x16x32_bf16 a[88:91], v[60:63], v[108:111], a[88:91]\n" \
"s_add_u32 m0, s30, 0x9400\n" \
"s_mov_b32 s85, 0xb480\n" \
"v_mfma_f32_16x16x32_bf16 a[88:91], v[124:127], v[172:175], a[88:91]\n" \
"buffer_load_dwordx4 v25, s[76:79], s85 offen lds\n" \
"v_mfma_f32_16x16x32_bf16 a[92:95], v[60:63], v[112:115], a[92:95]\n" \
"ds_read_b64_tr_b16 v[96:97], v35 offset:0\n" \
"v_cvt_pk_bf16_f32 v214, v180, v181\n" \
"v_mfma_f32_16x16x32_bf16 a[92:95], v[124:127], v[176:179], a[92:95]\n" \
"ds_read_b128 v[60:63], v28 offset:4096\n" \
"v_cvt_pk_bf16_f32 v215, v182, v183\n" \
"v_mfma_f32_16x16x32_bf16 a[124:127], v[64:67], v[112:115], a[124:127]\n" \
"ds_read_b128 v[124:127], v29 offset:4096\n" \
"v_accvgpr_read_b32 v216, a140\n" \
"v_mfma_f32_16x16x32_bf16 a[124:127], v[128:131], v[176:179], a[124:127]\n" \
"s_add_u32 m0, s31, 0x8800\n" \
"s_mov_b32 s84, 0x90000\n" \
"v_mfma_f32_16x16x32_bf16 a[120:123], v[64:67], v[108:111], a[120:123]\n" \
"buffer_load_dwordx4 v27, s[68:71], s84 offen lds\n" \
"v_mfma_f32_16x16x32_bf16 a[120:123], v[128:131], v[172:175], a[120:123]\n" \
"ds_read_b64_tr_b16 v[98:99], v35 offset:512\n" \
"v_accvgpr_read_b32 v217, a141\n" \
"v_mfma_f32_16x16x32_bf16 a[116:119], v[64:67], v[104:107], a[116:119]\n" \
"s_add_u32 m0, s30, 0x9800\n" \
"s_mov_b32 s85, 0xd880\n" \
"v_mfma_f32_16x16x32_bf16 a[116:119], v[128:131], v[168:171], a[116:119]\n" \
"buffer_load_dwordx4 v24, s[76:79], s85 offen lds\n" \
"v_mfma_f32_16x16x32_bf16 a[112:115], v[64:67], v[100:103], a[112:115]\n" \
"ds_read_b64_tr_b16 v[160:161], v35 offset:4096\n" \
"v_accvgpr_read_b32 v218, a142\n" \
"v_mfma_f32_16x16x32_bf16 a[112:115], v[128:131], v[164:167], a[112:115]\n" \
"ds_read_b128 v[64:67], v28 offset:6144\n" \
"v_accvgpr_read_b32 v219, a143\n" \
"v_mfma_f32_16x16x32_bf16 a[16:19], v[52:55], v[100:103], a[16:19]\n" \
"ds_read_b128 v[128:131], v29 offset:6144\n" \
"v_accvgpr_read_b32 v180, a172\n" \
"v_mfma_f32_16x16x32_bf16 a[16:19], v[116:119], v[164:167], a[16:19]\n" \
"ds_read_b64_tr_b16 v[162:163], v35 offset:4608\n" \
"v_accvgpr_read_b32 v181, a173\n" \
"v_mfma_f32_16x16x32_bf16 a[20:23], v[52:55], v[104:107], a[20:23]\n" \
"ds_read_b64_tr_b16 v[92:93], v34 offset:0\n" \
"v_accvgpr_read_b32 v182, a174\n" \
"v_mfma_f32_16x16x32_bf16 a[20:23], v[116:119], v[168:171], a[20:23]\n" \
"ds_read_b64_tr_b16 v[94:95], v34 offset:512\n" \
"v_accvgpr_read_b32 v183, a175\n" \
"v_mfma_f32_16x16x32_bf16 a[24:27], v[52:55], v[108:111], a[24:27]\n" \
"ds_read_b64_tr_b16 v[156:157], v34 offset:4096\n" \
"v_cvt_pk_bf16_f32 v216, v216, v217\n" \
"v_mfma_f32_16x16x32_bf16 a[24:27], v[116:119], v[172:175], a[24:27]\n" \
"ds_read_b64_tr_b16 v[158:159], v34 offset:4608\n" \
"v_cvt_pk_bf16_f32 v217, v218, v219\n" \
"v_mfma_f32_16x16x32_bf16 a[28:31], v[52:55], v[112:115], a[28:31]\n" \
"v_cvt_pk_bf16_f32 v218, v180, v181\n" \
"v_cvt_pk_bf16_f32 v219, v182, v183\n" \
"v_mfma_f32_16x16x32_bf16 a[28:31], v[116:119], v[176:179], a[28:31]\n" \
"ds_read_b128 v[52:55], v28 offset:0\n" \
"v_accvgpr_read_b32 v220, a136\n" \
"v_mfma_f32_16x16x32_bf16 a[60:63], v[56:59], v[112:115], a[60:63]\n" \
"ds_read_b128 v[116:119], v29 offset:0\n" \
"v_accvgpr_read_b32 v221, a137\n" \
"v_mfma_f32_16x16x32_bf16 a[60:63], v[120:123], v[176:179], a[60:63]\n" \
"v_accvgpr_read_b32 v222, a138\n" \
"v_accvgpr_read_b32 v223, a139\n" \
"v_mfma_f32_16x16x32_bf16 a[56:59], v[56:59], v[108:111], a[56:59]\n" \
"v_accvgpr_read_b32 v180, a168\n" \
"v_accvgpr_read_b32 v181, a169\n" \
"v_mfma_f32_16x16x32_bf16 a[56:59], v[120:123], v[172:175], a[56:59]\n" \
"v_accvgpr_read_b32 v182, a170\n" \
"v_accvgpr_read_b32 v183, a171\n" \
"v_mfma_f32_16x16x32_bf16 a[52:55], v[56:59], v[104:107], a[52:55]\n" \
"v_cvt_pk_bf16_f32 v220, v220, v221\n" \
"v_cvt_pk_bf16_f32 v221, v222, v223\n" \
"v_mfma_f32_16x16x32_bf16 a[52:55], v[120:123], v[168:171], a[52:55]\n" \
"v_cvt_pk_bf16_f32 v222, v180, v181\n" \
"v_cvt_pk_bf16_f32 v223, v182, v183\n" \
"v_mfma_f32_16x16x32_bf16 a[48:51], v[56:59], v[100:103], a[48:51]\n" \
"v_accvgpr_read_b32 v224, a132\n" \
"v_accvgpr_read_b32 v225, a133\n" \
"v_mfma_f32_16x16x32_bf16 a[48:51], v[120:123], v[164:167], a[48:51]\n" \
"ds_read_b128 v[56:59], v28 offset:2048\n" \
"ds_read_b128 v[120:123], v29 offset:2048\n" \
"s_waitcnt lgkmcnt(15)\n" \
"s_nop 0\n" \
"v_mfma_f32_16x16x32_bf16 a[0:3], v[84:87], v[68:71], 0\n" \
"s_add_u32 m0, s30, 0x9c00\n" \
"s_mov_b32 s85, 0xfc80\n" \
"v_accvgpr_read_b32 v226, a134\n" \
"v_mfma_f32_16x16x32_bf16 a[0:3], v[148:151], v[132:135], a[0:3]\n" \
"buffer_load_dwordx4 v25, s[76:79], s85 offen lds\n" \
"v_accvgpr_read_b32 v227, a135\n" \
"v_mfma_f32_16x16x32_bf16 a[4:7], v[88:91], v[68:71], 0\n" \
"ds_read_b64_tr_b16 v[100:101], v36 offset:0\n" \
"v_accvgpr_read_b32 v180, a164\n" \
"v_mfma_f32_16x16x32_bf16 a[4:7], v[152:155], v[132:135], a[4:7]\n" \
"s_add_u32 m0, s31, 0x8c00\n" \
"s_mov_b32 s84, 0x98000\n" \
"v_accvgpr_read_b32 v181, a165\n" \
"s_waitcnt lgkmcnt(13)\n" \
"v_mfma_f32_16x16x32_bf16 a[36:39], v[96:99], v[68:71], 0\n" \
"buffer_load_dwordx4 v27, s[68:71], s84 offen lds\n" \
"v_accvgpr_read_b32 v182, a166\n" \
"s_waitcnt lgkmcnt(9)\n" \
"v_mfma_f32_16x16x32_bf16 a[36:39], v[160:163], v[132:135], a[36:39]\n" \
"ds_read_b64_tr_b16 v[102:103], v36 offset:1024\n" \
"v_accvgpr_read_b32 v183, a167\n" \
"s_waitcnt lgkmcnt(8)\n" \
"v_mfma_f32_16x16x32_bf16 a[32:35], v[68:71], v[92:95], 0\n" \
"ds_read_b64_tr_b16 v[164:165], v36 offset:8192\n" \
"v_cvt_pk_bf16_f32 v224, v224, v225\n" \
"s_waitcnt lgkmcnt(7)\n" \
"v_mfma_f32_16x16x32_bf16 a[32:35], v[132:135], v[156:159], a[32:35]\n" \
"ds_read_b64_tr_b16 v[166:167], v36 offset:9216\n" \
"v_cvt_pk_bf16_f32 v225, v226, v227\n" \
"v_mfma_f32_16x16x32_bf16 a[96:99], v[72:75], v[92:95], 0\n" \
"ds_read_b64_tr_b16 v[104:105], v37 offset:0\n" \
"v_cvt_pk_bf16_f32 v226, v180, v181\n" \
"v_mfma_f32_16x16x32_bf16 a[96:99], v[136:139], v[156:159], a[96:99]\n" \
"ds_read_b64_tr_b16 v[106:107], v37 offset:1024\n" \
"v_cvt_pk_bf16_f32 v227, v182, v183\n" \
"v_mfma_f32_16x16x32_bf16 a[100:103], v[96:99], v[72:75], 0\n" \
"ds_read_b64_tr_b16 v[168:169], v37 offset:8192\n" \
"v_accvgpr_read_b32 v228, a128\n" \
"v_mfma_f32_16x16x32_bf16 a[100:103], v[160:163], v[136:139], a[100:103]\n" \
"s_add_u32 s89, s41, 0x2000\n" \
"buffer_store_dwordx4 v[200:203], v20, s[80:83], s89 offen offset:3072 sc1\n" \
"v_mfma_f32_16x16x32_bf16 a[68:71], v[88:91], v[72:75], 0\n" \
"ds_read_b64_tr_b16 v[170:171], v37 offset:9216\n" \
"v_accvgpr_read_b32 v229, a129\n" \
"v_mfma_f32_16x16x32_bf16 a[68:71], v[152:155], v[136:139], a[68:71]\n" \
"ds_read_b64_tr_b16 v[108:109], v38 offset:0\n" \
"v_accvgpr_read_b32 v230, a130\n" \
"v_mfma_f32_16x16x32_bf16 a[64:67], v[84:87], v[72:75], 0\n" \
"ds_read_b64_tr_b16 v[110:111], v38 offset:1024\n" \
"v_accvgpr_read_b32 v231, a131\n" \
"v_mfma_f32_16x16x32_bf16 a[64:67], v[148:151], v[136:139], a[64:67]\n" \
"v_accvgpr_read_b32 v180, a160\n" \
"v_accvgpr_read_b32 v181, a161\n" \
"v_accvgpr_read_b32 v182, a162\n" \
"v_accvgpr_read_b32 v183, a163\n" \
"v_cvt_pk_bf16_f32 v228, v228, v229\n" \
"v_cvt_pk_bf16_f32 v229, v230, v231\n" \
"v_cvt_pk_bf16_f32 v230, v180, v181\n" \
"v_cvt_pk_bf16_f32 v231, v182, v183\n" \
"v_mfma_f32_16x16x32_bf16 a[128:131], v[84:87], v[76:79], 0\n" \
"s_add_u32 s88, s41, 0x2000\n" \
"buffer_store_dwordx4 v[204:207], v20, s[80:83], s88 offen offset:2048 sc1\n" \
"v_mfma_f32_16x16x32_bf16 a[128:131], v[148:151], v[140:143], a[128:131]\n" \
"ds_read_b64_tr_b16 v[172:173], v38 offset:8192\n" \
"v_accvgpr_read_b32 v232, a204\n" \
"v_mfma_f32_16x16x32_bf16 a[132:135], v[88:91], v[76:79], 0\n" \
"ds_read_b64_tr_b16 v[174:175], v38 offset:9216\n" \
"v_accvgpr_read_b32 v233, a205\n" \
"v_mfma_f32_16x16x32_bf16 a[132:135], v[152:155], v[140:143], a[132:135]\n" \
"ds_read_b64_tr_b16 v[112:113], v39 offset:0\n" \
"v_accvgpr_read_b32 v234, a206\n" \
"v_mfma_f32_16x16x32_bf16 a[164:167], v[96:99], v[76:79], 0\n" \
"v_accvgpr_read_b32 v235, a207\n" \
"v_accvgpr_read_b32 v180, a236\n" \
"v_accvgpr_read_b32 v181, a237\n" \
"v_mfma_f32_16x16x32_bf16 a[164:167], v[160:163], v[140:143], a[164:167]\n" \
"ds_read_b64_tr_b16 v[114:115], v39 offset:1024\n" \
"v_accvgpr_read_b32 v182, a238\n" \
"v_mfma_f32_16x16x32_bf16 a[160:163], v[76:79], v[92:95], 0\n" \
"s_add_u32 s89, s41, 0x2000\n" \
"buffer_store_dwordx4 v[208:211], v20, s[80:83], s89 offen offset:1024 sc1\n" \
"v_mfma_f32_16x16x32_bf16 a[160:163], v[140:143], v[156:159], a[160:163]\n" \
"ds_read_b64_tr_b16 v[176:177], v39 offset:8192\n" \
"v_accvgpr_read_b32 v183, a239\n" \
"v_cvt_pk_bf16_f32 v232, v232, v233\n" \
"v_cvt_pk_bf16_f32 v233, v234, v235\n" \
"v_cvt_pk_bf16_f32 v234, v180, v181\n" \
"v_cvt_pk_bf16_f32 v235, v182, v183\n" \
"v_accvgpr_read_b32 v236, a200\n" \
"v_accvgpr_read_b32 v237, a201\n" \
"v_accvgpr_read_b32 v238, a202\n" \
"v_accvgpr_read_b32 v239, a203\n" \
"v_accvgpr_read_b32 v180, a232\n" \
"v_accvgpr_read_b32 v181, a233\n" \
"v_accvgpr_read_b32 v182, a234\n" \
"v_accvgpr_read_b32 v183, a235\n" \
"v_cvt_pk_bf16_f32 v236, v236, v237\n" \
"v_cvt_pk_bf16_f32 v237, v238, v239\n" \
"v_cvt_pk_bf16_f32 v238, v180, v181\n" \
"v_cvt_pk_bf16_f32 v239, v182, v183\n" \
"v_accvgpr_read_b32 v240, a196\n" \
"v_accvgpr_read_b32 v241, a197\n" \
"v_accvgpr_read_b32 v242, a198\n" \
"v_accvgpr_read_b32 v243, a199\n" \
"v_accvgpr_read_b32 v180, a228\n" \
"v_accvgpr_read_b32 v181, a229\n" \
"v_accvgpr_read_b32 v182, a230\n" \
"v_accvgpr_read_b32 v183, a231\n" \
"v_cvt_pk_bf16_f32 v240, v240, v241\n" \
"v_cvt_pk_bf16_f32 v241, v242, v243\n" \
"v_cvt_pk_bf16_f32 v242, v180, v181\n" \
"v_cvt_pk_bf16_f32 v243, v182, v183\n" \
"v_accvgpr_read_b32 v244, a192\n" \
"v_accvgpr_read_b32 v245, a193\n" \
"v_accvgpr_read_b32 v246, a194\n" \
"v_accvgpr_read_b32 v247, a195\n" \
"v_accvgpr_read_b32 v180, a224\n" \
"v_accvgpr_read_b32 v181, a225\n" \
"v_accvgpr_read_b32 v182, a226\n" \
"v_accvgpr_read_b32 v183, a227\n" \
"v_cvt_pk_bf16_f32 v244, v244, v245\n" \
"v_cvt_pk_bf16_f32 v245, v246, v247\n" \
"v_cvt_pk_bf16_f32 v246, v180, v181\n" \
"v_cvt_pk_bf16_f32 v247, v182, v183\n" \
"v_mfma_f32_16x16x32_bf16 a[224:227], v[80:83], v[92:95], 0\n" \
"ds_read_b64_tr_b16 v[178:179], v39 offset:9216\n" \
"v_accvgpr_read_b32 v184, a144\n" \
"v_mfma_f32_16x16x32_bf16 a[224:227], v[144:147], v[156:159], a[224:227]\n" \
"v_accvgpr_read_b32 v185, a145\n" \
"v_accvgpr_read_b32 v186, a146\n" \
"v_accvgpr_read_b32 v187, a147\n" \
"v_mfma_f32_16x16x32_bf16 a[228:231], v[96:99], v[80:83], 0\n" \
"v_accvgpr_read_b32 v180, a176\n" \
"v_accvgpr_read_b32 v181, a177\n" \
"v_accvgpr_read_b32 v182, a178\n" \
"v_mfma_f32_16x16x32_bf16 a[228:231], v[160:163], v[144:147], a[228:231]\n" \
"s_add_u32 s88, s41, 0x2000\n" \
"buffer_store_dwordx4 v[212:215], v20, s[80:83], s88 offen offset:0 sc1\n" \
"v_mfma_f32_16x16x32_bf16 a[196:199], v[88:91], v[80:83], 0\n" \
"v_accvgpr_read_b32 v183, a179\n" \
"v_cvt_pk_bf16_f32 v184, v184, v185\n" \
"v_cvt_pk_bf16_f32 v185, v186, v187\n" \
"v_mfma_f32_16x16x32_bf16 a[196:199], v[152:155], v[144:147], a[196:199]\n" \
"v_cvt_pk_bf16_f32 v186, v180, v181\n" \
"v_cvt_pk_bf16_f32 v187, v182, v183\n" \
"v_accvgpr_read_b32 v188, a148\n" \
"v_mfma_f32_16x16x32_bf16 a[192:195], v[84:87], v[80:83], 0\n" \
"v_accvgpr_read_b32 v189, a149\n" \
"v_accvgpr_read_b32 v190, a150\n" \
"v_accvgpr_read_b32 v191, a151\n" \
"v_mfma_f32_16x16x32_bf16 a[192:195], v[148:151], v[144:147], a[192:195]\n" \
"v_accvgpr_read_b32 v180, a180\n" \
"v_accvgpr_read_b32 v181, a181\n" \
"v_accvgpr_read_b32 v182, a182\n" \
"v_mfma_f32_16x16x32_bf16 a[8:11], v[88:91], v[64:67], 0\n" \
"v_accvgpr_read_b32 v183, a183\n" \
"v_cvt_pk_bf16_f32 v188, v188, v189\n" \
"v_cvt_pk_bf16_f32 v189, v190, v191\n" \
"v_mfma_f32_16x16x32_bf16 a[8:11], v[152:155], v[128:131], a[8:11]\n" \
"v_cvt_pk_bf16_f32 v190, v180, v181\n" \
"v_cvt_pk_bf16_f32 v191, v182, v183\n" \
"v_accvgpr_read_b32 v192, a80\n" \
"v_mfma_f32_16x16x32_bf16 a[40:43], v[96:99], v[64:67], 0\n" \
"v_accvgpr_read_b32 v193, a81\n" \
"v_accvgpr_read_b32 v194, a82\n" \
"v_accvgpr_read_b32 v195, a83\n" \
"v_mfma_f32_16x16x32_bf16 a[40:43], v[160:163], v[128:131], a[40:43]\n" \
"v_accvgpr_read_b32 v180, a112\n" \
"v_accvgpr_read_b32 v181, a113\n" \
"v_accvgpr_read_b32 v182, a114\n" \
"v_mfma_f32_16x16x32_bf16 a[12:15], v[64:67], v[92:95], 0\n" \
"v_accvgpr_read_b32 v183, a115\n" \
"v_cvt_pk_bf16_f32 v192, v192, v193\n" \
"v_cvt_pk_bf16_f32 v193, v194, v195\n" \
"v_mfma_f32_16x16x32_bf16 a[12:15], v[128:131], v[156:159], a[12:15]\n" \
"v_cvt_pk_bf16_f32 v194, v180, v181\n" \
"v_cvt_pk_bf16_f32 v195, v182, v183\n" \
"v_accvgpr_read_b32 v196, a84\n" \
"s_waitcnt vmcnt(4) lgkmcnt(0)\n" \
"s_barrier\n" \
"v_mfma_f32_16x16x32_bf16 a[72:75], v[100:103], v[76:79], 0\n" \
"s_add_u32 m0, s40, 0x0\n" \
"s_nop 0\n" \
"s_add_u32 s85, s39, 0x4000\n" \
"v_accvgpr_read_b32 v197, a85\n" \
"v_mfma_f32_16x16x32_bf16 a[72:75], v[164:167], v[140:143], a[72:75]\n" \
"buffer_load_dwordx4 v20, s[80:83], s85 offen sc1 lds\n" \
"v_accvgpr_read_b32 v198, a86\n" \
"v_mfma_f32_16x16x32_bf16 a[76:79], v[104:107], v[76:79], 0\n" \
"ds_read_b64_tr_b16 v[84:85], v32 offset:32768\n" \
"v_accvgpr_read_b32 v199, a87\n" \
"v_mfma_f32_16x16x32_bf16 a[76:79], v[168:171], v[140:143], a[76:79]\n" \
"ds_read_b64_tr_b16 v[86:87], v32 offset:33280\n" \
"v_accvgpr_read_b32 v180, a116\n" \
"v_mfma_f32_16x16x32_bf16 a[104:107], v[108:111], v[76:79], 0\n" \
"s_add_u32 m0, s40, 0x1000\n" \
"s_add_u32 s84, s39, 0x5000\n" \
"v_accvgpr_read_b32 v181, a117\n" \
"v_mfma_f32_16x16x32_bf16 a[104:107], v[172:175], v[140:143], a[104:107]\n" \
"buffer_load_dwordx4 v20, s[80:83], s84 offen sc1 lds\n" \
"v_accvgpr_read_b32 v182, a118\n" \
"v_mfma_f32_16x16x32_bf16 a[108:111], v[112:115], v[76:79], 0\n" \
"ds_read_b64_tr_b16 v[148:149], v32 offset:36864\n" \
"v_accvgpr_read_b32 v183, a119\n" \
"v_mfma_f32_16x16x32_bf16 a[108:111], v[176:179], v[140:143], a[108:111]\n" \
"ds_read_b128 v[76:79], v28 offset:45056\n" \
"v_cvt_pk_bf16_f32 v196, v196, v197\n" \
"v_mfma_f32_16x16x32_bf16 a[172:175], v[112:115], v[80:83], 0\n" \
"ds_read_b128 v[140:143], v29 offset:45056\n" \
"v_cvt_pk_bf16_f32 v197, v198, v199\n" \
"v_mfma_f32_16x16x32_bf16 a[172:175], v[176:179], v[144:147], a[172:175]\n" \
"s_add_u32 m0, s40, 0x3000\n" \
"s_mov_b32 s85, 0x100\n" \
"v_cvt_pk_bf16_f32 v198, v180, v181\n" \
"v_mfma_f32_16x16x32_bf16 a[168:171], v[108:111], v[80:83], 0\n" \
"buffer_load_dwordx4 v21, s[76:79], s85 offen lds\n" \
"v_cvt_pk_bf16_f32 v199, v182, v183\n" \
"v_mfma_f32_16x16x32_bf16 a[168:171], v[172:175], v[144:147], a[168:171]\n" \
"ds_read_b64_tr_b16 v[150:151], v32 offset:37376\n" \
"v_accvgpr_read_b32 v252, a88\n" \
"v_mfma_f32_16x16x32_bf16 a[140:143], v[104:107], v[80:83], 0\n" \
"s_add_u32 m0, s30, 0x0\n" \
"s_nop 0\n" \
"s_mov_b32 s84, 0x100\n" \
"v_accvgpr_read_b32 v253, a89\n" \
"v_mfma_f32_16x16x32_bf16 a[140:143], v[168:171], v[144:147], a[140:143]\n" \
"buffer_load_dwordx4 v24, s[76:79], s84 offen lds\n" \
"v_accvgpr_read_b32 v254, a90\n" \
"v_mfma_f32_16x16x32_bf16 a[136:139], v[100:103], v[80:83], 0\n" \
"ds_read_b64_tr_b16 v[88:89], v33 offset:32768\n" \
"v_accvgpr_read_b32 v255, a91\n" \
"v_mfma_f32_16x16x32_bf16 a[136:139], v[164:167], v[144:147], a[136:139]\n" \
"ds_read_b128 v[80:83], v28 offset:47104\n" \
"v_accvgpr_read_b32 v180, a120\n" \
"v_mfma_f32_16x16x32_bf16 a[144:147], v[100:103], v[68:71], 0\n" \
"ds_read_b128 v[144:147], v29 offset:47104\n" \
"v_accvgpr_read_b32 v181, a121\n" \
"v_mfma_f32_16x16x32_bf16 a[144:147], v[164:167], v[132:135], a[144:147]\n" \
"s_add_u32 m0, s30, 0x400\n" \
"s_mov_b32 s85, 0x2500\n" \
"v_accvgpr_read_b32 v182, a122\n" \
"v_mfma_f32_16x16x32_bf16 a[148:151], v[104:107], v[68:71], 0\n" \
"buffer_load_dwordx4 v25, s[76:79], s85 offen lds\n" \
"v_accvgpr_read_b32 v183, a123\n" \
"v_mfma_f32_16x16x32_bf16 a[148:151], v[168:171], v[132:135], a[148:151]\n" \
"ds_read_b64_tr_b16 v[90:91], v33 offset:33280\n" \
"v_cvt_pk_bf16_f32 v252, v252, v253\n" \
"v_mfma_f32_16x16x32_bf16 a[200:203], v[108:111], v[68:71], 0\n" \
"ds_read_b64_tr_b16 v[152:153], v33 offset:36864\n" \
"v_cvt_pk_bf16_f32 v253, v254, v255\n" \
"v_mfma_f32_16x16x32_bf16 a[200:203], v[172:175], v[132:135], a[200:203]\n" \
"s_add_u32 m0, s31, 0x0\n" \
"s_nop 0\n" \
"s_mov_b32 s84, 0x100000\n" \
"v_cvt_pk_bf16_f32 v254, v180, v181\n" \
"v_mfma_f32_16x16x32_bf16 a[204:207], v[112:115], v[68:71], 0\n" \
"buffer_load_dwordx4 v26, s[68:71], s84 offen lds\n" \
"v_cvt_pk_bf16_f32 v255, v182, v183\n" \
"v_mfma_f32_16x16x32_bf16 a[204:207], v[176:179], v[132:135], a[204:207]\n" \
"ds_read_b128 v[68:71], v28 offset:40960\n" \
"v_accvgpr_write_b32 a44, v252\n" \
"v_mfma_f32_16x16x32_bf16 a[236:239], v[112:115], v[72:75], 0\n" \
"ds_read_b128 v[132:135], v29 offset:40960\n" \
"v_accvgpr_write_b32 a45, v253\n" \
"v_mfma_f32_16x16x32_bf16 a[236:239], v[176:179], v[136:139], a[236:239]\n" \
"s_add_u32 m0, s30, 0x800\n" \
"s_mov_b32 s85, 0x4900\n" \
"v_accvgpr_write_b32 a46, v254\n" \
"v_mfma_f32_16x16x32_bf16 a[232:235], v[108:111], v[72:75], 0\n" \
"buffer_load_dwordx4 v24, s[76:79], s85 offen lds\n" \
"v_accvgpr_write_b32 a47, v255\n" \
"v_mfma_f32_16x16x32_bf16 a[232:235], v[172:175], v[136:139], a[232:235]\n" \
"ds_read_b64_tr_b16 v[154:155], v33 offset:37376\n" \
"v_accvgpr_read_b32 v252, a92\n" \
"v_mfma_f32_16x16x32_bf16 a[180:183], v[104:107], v[72:75], 0\n" \
"ds_read_b64_tr_b16 v[96:97], v35 offset:32768\n" \
"v_accvgpr_read_b32 v253, a93\n" \
"v_mfma_f32_16x16x32_bf16 a[180:183], v[168:171], v[136:139], a[180:183]\n" \
"ds_read_b64_tr_b16 v[98:99], v35 offset:33280\n" \
"v_accvgpr_read_b32 v254, a94\n" \
"v_mfma_f32_16x16x32_bf16 a[176:179], v[100:103], v[72:75], 0\n" \
"v_accvgpr_read_b32 v255, a95\n" \
"v_accvgpr_read_b32 v180, a124\n" \
"v_accvgpr_read_b32 v181, a125\n" \
"v_mfma_f32_16x16x32_bf16 a[176:179], v[164:167], v[136:139], a[176:179]\n" \
"ds_read_b128 v[72:75], v28 offset:43008\n" \
"v_accvgpr_read_b32 v182, a126\n" \
"v_mfma_f32_16x16x32_bf16 a[80:83], v[100:103], v[60:63], 0\n" \
"ds_read_b128 v[136:139], v29 offset:43008\n" \
"v_accvgpr_read_b32 v183, a127\n" \
"v_mfma_f32_16x16x32_bf16 a[80:83], v[164:167], v[124:127], a[80:83]\n" \
"s_add_u32 m0, s30, 0xc00\n" \
"s_mov_b32 s84, 0x6d00\n" \
"v_cvt_pk_bf16_f32 v252, v252, v253\n" \
"v_mfma_f32_16x16x32_bf16 a[84:87], v[104:107], v[60:63], 0\n" \
"buffer_load_dwordx4 v25, s[76:79], s84 offen lds\n" \
"v_cvt_pk_bf16_f32 v253, v254, v255\n" \
"v_mfma_f32_16x16x32_bf16 a[84:87], v[168:171], v[124:127], a[84:87]\n" \
"s_add_u32 m0, s31, 0x400\n" \
"s_mov_b32 s85, 0x108000\n" \
"v_cvt_pk_bf16_f32 v254, v180, v181\n" \
"v_mfma_f32_16x16x32_bf16 a[88:91], v[108:111], v[60:63], 0\n" \
"buffer_load_dwordx4 v26, s[68:71], s85 offen lds\n" \
"v_cvt_pk_bf16_f32 v255, v182, v183\n" \
"v_mfma_f32_16x16x32_bf16 a[88:91], v[172:175], v[124:127], a[88:91]\n" \
"ds_read_b64_tr_b16 v[160:161], v35 offset:36864\n" \
"v_accvgpr_read_b32 v200, a16\n" \
"v_mfma_f32_16x16x32_bf16 a[92:95], v[112:115], v[60:63], 0\n" \
"ds_read_b64_tr_b16 v[162:163], v35 offset:37376\n" \
"v_accvgpr_read_b32 v201, a17\n" \
"v_mfma_f32_16x16x32_bf16 a[92:95], v[176:179], v[124:127], a[92:95]\n" \
"ds_read_b128 v[60:63], v28 offset:36864\n" \
"v_accvgpr_read_b32 v202, a18\n" \
"v_mfma_f32_16x16x32_bf16 a[124:127], v[112:115], v[64:67], 0\n" \
"ds_read_b128 v[124:127], v29 offset:36864\n" \
"v_accvgpr_read_b32 v203, a19\n" \
"v_mfma_f32_16x16x32_bf16 a[124:127], v[176:179], v[128:131], a[124:127]\n" \
"s_add_u32 m0, s30, 0x1000\n" \
"s_mov_b32 s84, 0x9100\n" \
"v_accvgpr_read_b32 v180, a48\n" \
"v_mfma_f32_16x16x32_bf16 a[120:123], v[108:111], v[64:67], 0\n" \
"buffer_load_dwordx4 v24, s[76:79], s84 offen lds\n" \
"v_accvgpr_read_b32 v181, a49\n" \
"v_mfma_f32_16x16x32_bf16 a[120:123], v[172:175], v[128:131], a[120:123]\n" \
"s_add_u32 m0, s30, 0x1400\n" \
"s_mov_b32 s85, 0xb500\n" \
"v_accvgpr_read_b32 v182, a50\n" \
"v_mfma_f32_16x16x32_bf16 a[116:119], v[104:107], v[64:67], 0\n" \
"buffer_load_dwordx4 v25, s[76:79], s85 offen lds\n" \
"v_accvgpr_read_b32 v183, a51\n" \
"v_mfma_f32_16x16x32_bf16 a[116:119], v[168:171], v[128:131], a[116:119]\n" \
"ds_read_b64_tr_b16 v[92:93], v34 offset:32768\n" \
"v_cvt_pk_bf16_f32 v200, v200, v201\n" \
"v_mfma_f32_16x16x32_bf16 a[112:115], v[100:103], v[64:67], 0\n" \
"ds_read_b64_tr_b16 v[94:95], v34 offset:33280\n" \
"v_cvt_pk_bf16_f32 v201, v202, v203\n" \
"v_mfma_f32_16x16x32_bf16 a[112:115], v[164:167], v[128:131], a[112:115]\n" \
"ds_read_b128 v[64:67], v28 offset:38912\n" \
"v_cvt_pk_bf16_f32 v202, v180, v181\n" \
"v_cvt_pk_bf16_f32 v203, v182, v183\n" \
"v_mfma_f32_16x16x32_bf16 a[16:19], v[100:103], v[52:55], 0\n" \
"ds_read_b128 v[128:131], v29 offset:38912\n" \
"v_accvgpr_read_b32 v204, a20\n" \
"v_mfma_f32_16x16x32_bf16 a[16:19], v[164:167], v[116:119], a[16:19]\n" \
"s_add_u32 m0, s31, 0x800\n" \
"s_mov_b32 s84, 0x110000\n" \
"v_accvgpr_read_b32 v205, a21\n" \
"v_accvgpr_read_b32 v206, a22\n" \
"v_accvgpr_read_b32 v207, a23\n" \
"v_accvgpr_read_b32 v180, a52\n" \
"v_accvgpr_read_b32 v181, a53\n" \
"v_accvgpr_read_b32 v182, a54\n" \
"v_accvgpr_read_b32 v183, a55\n" \
"v_cvt_pk_bf16_f32 v204, v204, v205\n" \
"v_cvt_pk_bf16_f32 v205, v206, v207\n" \
"v_cvt_pk_bf16_f32 v206, v180, v181\n" \
"v_cvt_pk_bf16_f32 v207, v182, v183\n" \
"v_mfma_f32_16x16x32_bf16 a[20:23], v[104:107], v[52:55], 0\n" \
"buffer_load_dwordx4 v27, s[68:71], s84 offen lds\n" \
"v_accvgpr_read_b32 v208, a24\n" \
"v_mfma_f32_16x16x32_bf16 a[20:23], v[168:171], v[116:119], a[20:23]\n" \
"s_add_u32 m0, s30, 0x1800\n" \
"s_mov_b32 s85, 0xd900\n" \
"v_accvgpr_read_b32 v209, a25\n" \
"v_accvgpr_read_b32 v210, a26\n" \
"v_accvgpr_read_b32 v211, a27\n" \
"v_accvgpr_read_b32 v180, a56\n" \
"v_accvgpr_read_b32 v181, a57\n" \
"v_accvgpr_read_b32 v182, a58\n" \
"v_accvgpr_read_b32 v183, a59\n" \
"v_cvt_pk_bf16_f32 v208, v208, v209\n" \
"v_cvt_pk_bf16_f32 v209, v210, v211\n" \
"v_cvt_pk_bf16_f32 v210, v180, v181\n" \
"v_cvt_pk_bf16_f32 v211, v182, v183\n" \
"v_mfma_f32_16x16x32_bf16 a[24:27], v[108:111], v[52:55], 0\n" \
"buffer_load_dwordx4 v24, s[76:79], s85 offen lds\n" \
"v_accvgpr_read_b32 v212, a28\n" \
"v_mfma_f32_16x16x32_bf16 a[24:27], v[172:175], v[116:119], a[24:27]\n" \
"ds_read_b64_tr_b16 v[156:157], v34 offset:36864\n" \
"v_accvgpr_read_b32 v213, a29\n" \
"v_accvgpr_read_b32 v214, a30\n" \
"v_accvgpr_read_b32 v215, a31\n" \
"v_accvgpr_read_b32 v180, a60\n" \
"v_accvgpr_read_b32 v181, a61\n" \
"v_accvgpr_read_b32 v182, a62\n" \
"v_accvgpr_read_b32 v183, a63\n" \
"v_cvt_pk_bf16_f32 v212, v212, v213\n" \
"v_cvt_pk_bf16_f32 v213, v214, v215\n" \
"v_cvt_pk_bf16_f32 v214, v180, v181\n" \
"v_cvt_pk_bf16_f32 v215, v182, v183\n" \
"v_mfma_f32_16x16x32_bf16 a[28:31], v[112:115], v[52:55], 0\n" \
"ds_read_b64_tr_b16 v[158:159], v34 offset:37376\n" \
"v_mfma_f32_16x16x32_bf16 a[28:31], v[176:179], v[116:119], a[28:31]\n" \
"ds_read_b128 v[52:55], v28 offset:32768\n" \
"v_mfma_f32_16x16x32_bf16 a[60:63], v[112:115], v[56:59], 0\n" \
"ds_read_b128 v[116:119], v29 offset:32768\n" \
"v_mfma_f32_16x16x32_bf16 a[60:63], v[176:179], v[120:123], a[60:63]\n" \
"v_mfma_f32_16x16x32_bf16 a[56:59], v[108:111], v[56:59], 0\n" \
"v_mfma_f32_16x16x32_bf16 a[56:59], v[172:175], v[120:123], a[56:59]\n" \
"v_mfma_f32_16x16x32_bf16 a[52:55], v[104:107], v[56:59], 0\n" \
"v_mfma_f32_16x16x32_bf16 a[52:55], v[168:171], v[120:123], a[52:55]\n" \
"v_mfma_f32_16x16x32_bf16 a[48:51], v[100:103], v[56:59], 0\n" \
"v_mfma_f32_16x16x32_bf16 a[48:51], v[164:167], v[120:123], a[48:51]\n" \
"ds_read_b128 v[56:59], v28 offset:34816\n" \
"ds_read_b128 v[120:123], v29 offset:34816\n" \
"s_waitcnt lgkmcnt(15)\n" \
"v_mfma_f32_16x16x32_bf16 a[0:3], v[84:87], v[68:71], a[0:3]\n" \
"s_add_u32 m0, s30, 0x1c00\n" \
"s_mov_b32 s85, 0xfd00\n" \
"v_mfma_f32_16x16x32_bf16 a[0:3], v[148:151], v[132:135], a[0:3]\n" \
"buffer_load_dwordx4 v25, s[76:79], s85 offen lds\n" \
"v_mfma_f32_16x16x32_bf16 a[4:7], v[88:91], v[68:71], a[4:7]\n" \
"ds_read_b64_tr_b16 v[100:101], v36 offset:32768\n" \
"v_mov_b32_e32 v11, 0\n" \
"v_mfma_f32_16x16x32_bf16 a[4:7], v[152:155], v[132:135], a[4:7]\n" \
"s_add_u32 m0, s31, 0xc00\n" \
"s_mov_b32 s84, 0x118000\n" \
"v_mfma_f32_16x16x32_bf16 a[36:39], v[96:99], v[68:71], a[36:39]\n" \
"buffer_load_dwordx4 v27, s[68:71], s84 offen lds\n" \
"s_waitcnt lgkmcnt(13)\n" \
"v_mfma_f32_16x16x32_bf16 a[36:39], v[160:163], v[132:135], a[36:39]\n" \
"ds_read_b64_tr_b16 v[102:103], v36 offset:33792\n" \
"s_and_b32 s6, s2, 7\n" \
"s_waitcnt lgkmcnt(10)\n" \
"v_mfma_f32_16x16x32_bf16 a[32:35], v[68:71], v[92:95], a[32:35]\n" \
"ds_read_b64_tr_b16 v[164:165], v36 offset:40960\n" \
"s_lshl_b32 s7, s3, 1\n" \
"s_waitcnt lgkmcnt(7)\n" \
"v_mfma_f32_16x16x32_bf16 a[32:35], v[132:135], v[156:159], a[32:35]\n" \
"ds_read_b64_tr_b16 v[166:167], v36 offset:41984\n" \
"s_lshr_b32 s4, s2, 5\n" \
"v_mfma_f32_16x16x32_bf16 a[96:99], v[72:75], v[92:95], a[96:99]\n" \
"ds_read_b64_tr_b16 v[104:105], v37 offset:32768\n" \
"s_lshl_b32 s5, s6, 2\n" \
"v_mfma_f32_16x16x32_bf16 a[96:99], v[136:139], v[156:159], a[96:99]\n" \
"ds_read_b64_tr_b16 v[106:107], v37 offset:33792\n" \
"s_bfe_u32 s8, s2, 0x20003\n" \
"v_mfma_f32_16x16x32_bf16 a[100:103], v[96:99], v[72:75], a[100:103]\n" \
"ds_read_b64_tr_b16 v[168:169], v37 offset:40960\n" \
"s_and_b32 s7, s7, 4\n" \
"v_mfma_f32_16x16x32_bf16 a[100:103], v[160:163], v[136:139], a[100:103]\n" \
"s_add_u32 s89, s41, 0x3000\n" \
"buffer_store_dwordx4 v[192:195], v20, s[80:83], s89 offen offset:0 sc1\n" \
"v_mfma_f32_16x16x32_bf16 a[68:71], v[88:91], v[72:75], a[68:71]\n" \
"ds_read_b64_tr_b16 v[170:171], v37 offset:41984\n" \
"s_and_b32 s9, s3, 1\n" \
"v_mfma_f32_16x16x32_bf16 a[68:71], v[152:155], v[136:139], a[68:71]\n" \
"ds_read_b64_tr_b16 v[108:109], v38 offset:32768\n" \
"s_or_b32 s13, s5, s8\n" \
"v_mfma_f32_16x16x32_bf16 a[64:67], v[84:87], v[72:75], a[64:67]\n" \
"ds_read_b64_tr_b16 v[110:111], v38 offset:33792\n" \
"s_or_b32 s9, s7, s9\n" \
"v_mfma_f32_16x16x32_bf16 a[64:67], v[148:151], v[136:139], a[64:67]\n" \
"s_mul_i32 s7, s4, 0xa00\n" \
"s_add_i32 s7, s7, 0x5bc1000\n" \
"v_mfma_f32_16x16x32_bf16 a[128:131], v[84:87], v[76:79], a[128:131]\n" \
"s_add_u32 s88, s41, 0x3000\n" \
"buffer_store_dwordx4 v[196:199], v20, s[80:83], s88 offen offset:1024 sc1\n" \
"v_mfma_f32_16x16x32_bf16 a[128:131], v[148:151], v[140:143], a[128:131]\n" \
"ds_read_b64_tr_b16 v[172:173], v38 offset:40960\n" \
"s_lshl_b32 s10, s13, 2\n" \
"v_mfma_f32_16x16x32_bf16 a[132:135], v[88:91], v[76:79], a[132:135]\n" \
"ds_read_b64_tr_b16 v[174:175], v38 offset:41984\n" \
"s_or_b32 s10, s10, s7\n" \
"v_mfma_f32_16x16x32_bf16 a[132:135], v[152:155], v[140:143], a[132:135]\n" \
"ds_read_b64_tr_b16 v[112:113], v39 offset:32768\n" \
"v_writelane_b32 v11, s10, 0\n" \
"v_mfma_f32_16x16x32_bf16 a[164:167], v[96:99], v[76:79], a[164:167]\n" \
"s_lshl_b32 s14, s4, 3\n" \
"v_writelane_b32 v11, s7, 1\n" \
"v_mfma_f32_16x16x32_bf16 a[164:167], v[160:163], v[140:143], a[164:167]\n" \
"ds_read_b64_tr_b16 v[114:115], v39 offset:33792\n" \
"s_or_b32 s7, s14, s6\n" \
"v_mfma_f32_16x16x32_bf16 a[160:163], v[76:79], v[92:95], a[160:163]\n" \
"s_add_u32 s89, s41, 0x3000\n" \
"buffer_store_dwordx4 a[44:47], v20, s[80:83], s89 offen offset:2048 sc1\n" \
"v_mfma_f32_16x16x32_bf16 a[160:163], v[140:143], v[156:159], a[160:163]\n" \
"ds_read_b64_tr_b16 v[176:177], v39 offset:40960\n" \
"s_lshl_b32 s7, s7, 10\n" \
"v_mfma_f32_16x16x32_bf16 a[224:227], v[80:83], v[92:95], a[224:227]\n" \
"ds_read_b64_tr_b16 v[178:179], v39 offset:41984\n" \
"s_lshl_b32 s10, s3, 8\n" \
"v_mfma_f32_16x16x32_bf16 a[224:227], v[144:147], v[156:159], a[224:227]\n" \
"s_or_b32 s7, s7, s10\n" \
"s_or_b32 s10, s7, 0x5c00200\n" \
"v_mfma_f32_16x16x32_bf16 a[228:231], v[96:99], v[80:83], a[228:231]\n" \
"v_writelane_b32 v11, s10, 2\n" \
"s_lshl_b32 s10, s8, 2\n" \
"v_mfma_f32_16x16x32_bf16 a[228:231], v[160:163], v[144:147], a[228:231]\n" \
"s_add_u32 s88, s41, 0x3000\n" \
"buffer_store_dwordx4 v[252:255], v20, s[80:83], s88 offen offset:3072 sc1\n" \
"v_mfma_f32_16x16x32_bf16 a[196:199], v[88:91], v[80:83], a[196:199]\n" \
"s_or_b32 s11, s10, s7\n" \
"s_or_b32 s11, s11, 0x5c00000\n" \
"v_mfma_f32_16x16x32_bf16 a[196:199], v[152:155], v[144:147], a[196:199]\n" \
"v_writelane_b32 v11, s11, 3\n" \
"s_or_b32 s7, s7, 0x5c00080\n" \
"v_mfma_f32_16x16x32_bf16 a[192:195], v[84:87], v[80:83], a[192:195]\n" \
"v_writelane_b32 v11, s7, 4\n" \
"s_or_b32 s7, s7, s10\n" \
"v_mfma_f32_16x16x32_bf16 a[192:195], v[148:151], v[144:147], a[192:195]\n" \
"s_add_i32 s10, s3, s2\n" \
"v_writelane_b32 v11, s7, 5\n" \
"v_mfma_f32_16x16x32_bf16 a[8:11], v[88:91], v[64:67], a[8:11]\n" \
"s_mov_b32 s7, 2.0\n" \
"s_mov_b32 s11, 0x5bc0000\n" \
"v_mfma_f32_16x16x32_bf16 a[8:11], v[152:155], v[128:131], a[8:11]\n" \
"s_cmp_lg_u32 s10, 0\n" \
"s_cselect_b32 s10, s7, s11\n" \
"v_mfma_f32_16x16x32_bf16 a[40:43], v[96:99], v[64:67], a[40:43]\n" \
"s_lshr_b32 s15, s2, 3\n" \
"v_writelane_b32 v11, s10, 53\n" \
"v_mfma_f32_16x16x32_bf16 a[40:43], v[160:163], v[128:131], a[40:43]\n" \
"s_lshl_b32 s10, s6, 5\n" \
"s_min_u32 s16, s3, 2\n" \
"v_mfma_f32_16x16x32_bf16 a[12:15], v[64:67], v[92:95], a[12:15]\n" \
"s_mul_i32 s12, s8, 0x120\n" \
"v_mfma_f32_16x16x32_bf16 a[12:15], v[128:131], v[156:159], a[12:15]\n" \
"s_waitcnt vmcnt(4) lgkmcnt(0)\n" \
"s_barrier\n" \
"v_mfma_f32_16x16x32_bf16 a[144:147], v[100:103], v[68:71], a[144:147]\n" \
"s_add_u32 m0, s40, 0x8000\n" \
"s_add_u32 s85, s39, 0x6000\n" \
"v_mfma_f32_16x16x32_bf16 a[144:147], v[164:167], v[132:135], a[144:147]\n" \
"buffer_load_dwordx4 v20, s[80:83], s85 offen sc1 lds\n" \
"v_mfma_f32_16x16x32_bf16 a[148:151], v[104:107], v[68:71], a[148:151]\n" \
"ds_read_b64_tr_b16 v[84:85], v32 offset:0\n" \
"v_mfma_f32_16x16x32_bf16 a[148:151], v[168:171], v[132:135], a[148:151]\n" \
"ds_read_b64_tr_b16 v[86:87], v32 offset:512\n" \
"v_mfma_f32_16x16x32_bf16 a[200:203], v[108:111], v[68:71], a[200:203]\n" \
"s_add_u32 m0, s40, 0x9000\n" \
"s_add_u32 s84, s39, 0x7000\n" \
"v_mfma_f32_16x16x32_bf16 a[200:203], v[172:175], v[132:135], a[200:203]\n" \
"buffer_load_dwordx4 v20, s[80:83], s84 offen sc1 lds\n" \
"v_mfma_f32_16x16x32_bf16 a[204:207], v[112:115], v[68:71], a[204:207]\n" \
"ds_read_b64_tr_b16 v[148:149], v32 offset:4096\n" \
"v_mfma_f32_16x16x32_bf16 a[204:207], v[176:179], v[132:135], a[204:207]\n" \
"ds_read_b128 v[68:71], v28 offset:8192\n" \
"v_mfma_f32_16x16x32_bf16 a[236:239], v[112:115], v[72:75], a[236:239]\n" \
"ds_read_b128 v[132:135], v29 offset:8192\n" \
"v_mfma_f32_16x16x32_bf16 a[236:239], v[176:179], v[136:139], a[236:239]\n" \
"s_add_u32 m0, s40, 0xb000\n" \
"s_mov_b32 s85, 0x180\n" \
"v_mfma_f32_16x16x32_bf16 a[232:235], v[108:111], v[72:75], a[232:235]\n" \
"buffer_load_dwordx4 v21, s[76:79], s85 offen lds\n" \
"v_mfma_f32_16x16x32_bf16 a[232:235], v[172:175], v[136:139], a[232:235]\n" \
"ds_read_b64_tr_b16 v[150:151], v32 offset:4608\n" \
"v_mfma_f32_16x16x32_bf16 a[180:183], v[104:107], v[72:75], a[180:183]\n" \
"s_add_u32 m0, s30, 0x8000\n" \
"s_mov_b32 s84, 0x180\n" \
"v_mfma_f32_16x16x32_bf16 a[180:183], v[168:171], v[136:139], a[180:183]\n" \
"buffer_load_dwordx4 v24, s[76:79], s84 offen lds\n" \
"v_mfma_f32_16x16x32_bf16 a[176:179], v[100:103], v[72:75], a[176:179]\n" \
"ds_read_b64_tr_b16 v[88:89], v33 offset:0\n" \
"v_mfma_f32_16x16x32_bf16 a[176:179], v[164:167], v[136:139], a[176:179]\n" \
"ds_read_b128 v[72:75], v28 offset:10240\n" \
"v_mfma_f32_16x16x32_bf16 a[72:75], v[100:103], v[76:79], a[72:75]\n" \
"ds_read_b128 v[136:139], v29 offset:10240\n" \
"v_mfma_f32_16x16x32_bf16 a[72:75], v[164:167], v[140:143], a[72:75]\n" \
"s_add_u32 m0, s30, 0x8400\n" \
"s_mov_b32 s85, 0x2580\n" \
"v_mfma_f32_16x16x32_bf16 a[76:79], v[104:107], v[76:79], a[76:79]\n" \
"buffer_load_dwordx4 v25, s[76:79], s85 offen lds\n" \
"v_mfma_f32_16x16x32_bf16 a[76:79], v[168:171], v[140:143], a[76:79]\n" \
"ds_read_b64_tr_b16 v[90:91], v33 offset:512\n" \
"v_mfma_f32_16x16x32_bf16 a[104:107], v[108:111], v[76:79], a[104:107]\n" \
"ds_read_b64_tr_b16 v[152:153], v33 offset:4096\n" \
"v_mfma_f32_16x16x32_bf16 a[104:107], v[172:175], v[140:143], a[104:107]\n" \
"s_add_u32 m0, s31, 0x8000\n" \
"s_mov_b32 s84, 0x180000\n" \
"v_mfma_f32_16x16x32_bf16 a[108:111], v[112:115], v[76:79], a[108:111]\n" \
"buffer_load_dwordx4 v26, s[68:71], s84 offen lds\n" \
"v_mfma_f32_16x16x32_bf16 a[108:111], v[176:179], v[140:143], a[108:111]\n" \
"ds_read_b128 v[76:79], v28 offset:12288\n" \
"v_mfma_f32_16x16x32_bf16 a[172:175], v[112:115], v[80:83], a[172:175]\n" \
"ds_read_b128 v[140:143], v29 offset:12288\n" \
"v_mfma_f32_16x16x32_bf16 a[172:175], v[176:179], v[144:147], a[172:175]\n" \
"s_add_u32 m0, s30, 0x8800\n" \
"s_mov_b32 s85, 0x4980\n" \
"v_mfma_f32_16x16x32_bf16 a[168:171], v[108:111], v[80:83], a[168:171]\n" \
"buffer_load_dwordx4 v24, s[76:79], s85 offen lds\n" \
"v_mfma_f32_16x16x32_bf16 a[168:171], v[172:175], v[144:147], a[168:171]\n" \
"ds_read_b64_tr_b16 v[154:155], v33 offset:4608\n" \
"v_mfma_f32_16x16x32_bf16 a[140:143], v[104:107], v[80:83], a[140:143]\n" \
"ds_read_b64_tr_b16 v[96:97], v35 offset:0\n" \
"v_mfma_f32_16x16x32_bf16 a[140:143], v[168:171], v[144:147], a[140:143]\n" \
"ds_read_b64_tr_b16 v[98:99], v35 offset:512\n" \
"v_mfma_f32_16x16x32_bf16 a[136:139], v[100:103], v[80:83], a[136:139]\n" \
"s_add_u32 s11, s22, s12\n" \
"s_addc_u32 s17, s23, 0\n" \
"v_mfma_f32_16x16x32_bf16 a[136:139], v[164:167], v[144:147], a[136:139]\n" \
"ds_read_b128 v[80:83], v28 offset:14336\n" \
"v_mfma_f32_16x16x32_bf16 a[80:83], v[100:103], v[60:63], a[80:83]\n" \
"ds_read_b128 v[144:147], v29 offset:14336\n" \
"v_mfma_f32_16x16x32_bf16 a[80:83], v[164:167], v[124:127], a[80:83]\n" \
"s_add_u32 m0, s30, 0x8c00\n" \
"s_mov_b32 s84, 0x6d80\n" \
"v_mfma_f32_16x16x32_bf16 a[84:87], v[104:107], v[60:63], a[84:87]\n" \
"buffer_load_dwordx4 v25, s[76:79], s84 offen lds\n" \
"v_mfma_f32_16x16x32_bf16 a[84:87], v[168:171], v[124:127], a[84:87]\n" \
"s_add_u32 m0, s31, 0x8400\n" \
"s_mov_b32 s85, 0x188000\n" \
"v_mfma_f32_16x16x32_bf16 a[88:91], v[108:111], v[60:63], a[88:91]\n" \
"buffer_load_dwordx4 v26, s[68:71], s85 offen lds\n" \
"v_mfma_f32_16x16x32_bf16 a[88:91], v[172:175], v[124:127], a[88:91]\n" \
"ds_read_b64_tr_b16 v[160:161], v35 offset:4096\n" \
"v_mfma_f32_16x16x32_bf16 a[92:95], v[112:115], v[60:63], a[92:95]\n" \
"ds_read_b64_tr_b16 v[162:163], v35 offset:4608\n" \
"v_mfma_f32_16x16x32_bf16 a[92:95], v[176:179], v[124:127], a[92:95]\n" \
"ds_read_b128 v[60:63], v28 offset:4096\n" \
"v_mfma_f32_16x16x32_bf16 a[124:127], v[112:115], v[64:67], a[124:127]\n" \
"ds_read_b128 v[124:127], v29 offset:4096\n" \
"v_mfma_f32_16x16x32_bf16 a[124:127], v[176:179], v[128:131], a[124:127]\n" \
"s_add_u32 m0, s30, 0x9000\n" \
"s_mov_b32 s84, 0x9180\n" \
"v_mfma_f32_16x16x32_bf16 a[120:123], v[108:111], v[64:67], a[120:123]\n" \
"buffer_load_dwordx4 v24, s[76:79], s84 offen lds\n" \
"v_mfma_f32_16x16x32_bf16 a[120:123], v[172:175], v[128:131], a[120:123]\n" \
"s_add_u32 m0, s30, 0x9400\n" \
"s_mov_b32 s85, 0xb580\n" \
"v_mfma_f32_16x16x32_bf16 a[116:119], v[104:107], v[64:67], a[116:119]\n" \
"buffer_load_dwordx4 v25, s[76:79], s85 offen lds\n" \
"v_mfma_f32_16x16x32_bf16 a[116:119], v[168:171], v[128:131], a[116:119]\n" \
"ds_read_b64_tr_b16 v[92:93], v34 offset:0\n" \
"v_mfma_f32_16x16x32_bf16 a[112:115], v[100:103], v[64:67], a[112:115]\n" \
"ds_read_b64_tr_b16 v[94:95], v34 offset:512\n" \
"v_mfma_f32_16x16x32_bf16 a[112:115], v[164:167], v[128:131], a[112:115]\n" \
"ds_read_b128 v[64:67], v28 offset:6144\n" \
"v_mfma_f32_16x16x32_bf16 a[16:19], v[100:103], v[52:55], a[16:19]\n" \
"ds_read_b128 v[128:131], v29 offset:6144\n" \
"v_mfma_f32_16x16x32_bf16 a[16:19], v[164:167], v[116:119], a[16:19]\n" \
"s_add_u32 m0, s31, 0x8800\n" \
"s_mov_b32 s84, 0x190000\n" \
"v_mfma_f32_16x16x32_bf16 a[20:23], v[104:107], v[52:55], a[20:23]\n" \
"buffer_load_dwordx4 v27, s[68:71], s84 offen lds\n" \
"v_mfma_f32_16x16x32_bf16 a[20:23], v[168:171], v[116:119], a[20:23]\n" \
"s_add_u32 m0, s30, 0x9800\n" \
"s_mov_b32 s85, 0xd980\n" \
"v_mfma_f32_16x16x32_bf16 a[24:27], v[108:111], v[52:55], a[24:27]\n" \
"buffer_load_dwordx4 v24, s[76:79], s85 offen lds\n" \
"v_mfma_f32_16x16x32_bf16 a[24:27], v[172:175], v[116:119], a[24:27]\n" \
"ds_read_b64_tr_b16 v[156:157], v34 offset:4096\n" \
"v_mfma_f32_16x16x32_bf16 a[28:31], v[112:115], v[52:55], a[28:31]\n" \
"ds_read_b64_tr_b16 v[158:159], v34 offset:4608\n" \
"v_mfma_f32_16x16x32_bf16 a[28:31], v[176:179], v[116:119], a[28:31]\n" \
"ds_read_b128 v[52:55], v28 offset:0\n" \
"v_mfma_f32_16x16x32_bf16 a[60:63], v[112:115], v[56:59], a[60:63]\n" \
"ds_read_b128 v[116:119], v29 offset:0\n" \
"v_mfma_f32_16x16x32_bf16 a[60:63], v[176:179], v[120:123], a[60:63]\n" \
"s_add_u32 s18, s24, s12\n" \
"v_writelane_b32 v11, s11, 9\n" \
"s_addc_u32 s19, s25, 0\n" \
"v_mfma_f32_16x16x32_bf16 a[56:59], v[108:111], v[56:59], a[56:59]\n" \
"s_and_b32 s20, s17, 0xffff\n" \
"v_writelane_b32 v11, s20, 10\n" \
"v_mfma_f32_16x16x32_bf16 a[56:59], v[172:175], v[120:123], a[56:59]\n" \
"s_and_b32 s20, s19, 0xffff\n" \
"v_writelane_b32 v11, s18, 11\n" \
"v_mfma_f32_16x16x32_bf16 a[52:55], v[104:107], v[56:59], a[52:55]\n" \
"s_cmpk_lt_u32 s3, 3\n" \
"s_cselect_b32 s11, s11, s18\n" \
"v_mfma_f32_16x16x32_bf16 a[52:55], v[168:171], v[120:123], a[52:55]\n" \
"s_movk_i32 s18, 0xfff\n" \
"v_writelane_b32 v11, s20, 12\n" \
"v_mfma_f32_16x16x32_bf16 a[48:51], v[100:103], v[56:59], a[48:51]\n" \
"s_and_b32 s20, s29, 0xffff\n" \
"v_writelane_b32 v11, s28, 13\n" \
"v_mfma_f32_16x16x32_bf16 a[48:51], v[164:167], v[120:123], a[48:51]\n" \
"ds_read_b128 v[56:59], v28 offset:2048\n" \
"ds_read_b128 v[120:123], v29 offset:2048\n" \
"s_waitcnt lgkmcnt(15)\n" \
"v_mfma_f32_16x16x32_bf16 a[0:3], v[84:87], v[68:71], a[0:3]\n" \
"s_add_u32 m0, s30, 0x9c00\n" \
"s_mov_b32 s85, 0xfd80\n" \
"v_mfma_f32_16x16x32_bf16 a[0:3], v[148:151], v[132:135], a[0:3]\n" \
"buffer_load_dwordx4 v25, s[76:79], s85 offen lds\n" \
"v_mfma_f32_16x16x32_bf16 a[4:7], v[88:91], v[68:71], a[4:7]\n" \
"ds_read_b64_tr_b16 v[100:101], v36 offset:0\n" \
"v_writelane_b32 v11, s20, 14\n" \
"v_mfma_f32_16x16x32_bf16 a[4:7], v[152:155], v[132:135], a[4:7]\n" \
"s_add_u32 m0, s31, 0x8c00\n" \
"s_mov_b32 s84, 0x198000\n" \
"v_mfma_f32_16x16x32_bf16 a[36:39], v[96:99], v[68:71], a[36:39]\n" \
"buffer_load_dwordx4 v27, s[68:71], s84 offen lds\n" \
"s_waitcnt lgkmcnt(13)\n" \
"v_mfma_f32_16x16x32_bf16 a[36:39], v[160:163], v[132:135], a[36:39]\n" \
"ds_read_b64_tr_b16 v[102:103], v36 offset:1024\n" \
"v_writelane_b32 v11, s11, 15\n" \
"s_waitcnt lgkmcnt(10)\n" \
"v_mfma_f32_16x16x32_bf16 a[32:35], v[68:71], v[92:95], a[32:35]\n" \
"ds_read_b64_tr_b16 v[164:165], v36 offset:8192\n" \
"s_waitcnt lgkmcnt(7)\n" \
"s_nop 0\n" \
"v_mfma_f32_16x16x32_bf16 a[32:35], v[132:135], v[156:159], a[32:35]\n" \
"ds_read_b64_tr_b16 v[166:167], v36 offset:9216\n" \
"v_mfma_f32_16x16x32_bf16 a[96:99], v[72:75], v[92:95], a[96:99]\n" \
"ds_read_b64_tr_b16 v[104:105], v37 offset:0\n" \
"v_mfma_f32_16x16x32_bf16 a[96:99], v[136:139], v[156:159], a[96:99]\n" \
"ds_read_b64_tr_b16 v[106:107], v37 offset:1024\n" \
"v_mfma_f32_16x16x32_bf16 a[100:103], v[96:99], v[72:75], a[100:103]\n" \
"ds_read_b64_tr_b16 v[168:169], v37 offset:8192\n" \
"v_mfma_f32_16x16x32_bf16 a[100:103], v[160:163], v[136:139], a[100:103]\n" \
"s_add_u32 s89, s41, 0x1000\n" \
"buffer_store_dwordx4 v[200:203], v20, s[80:83], s89 offen offset:0 sc1\n" \
"v_mfma_f32_16x16x32_bf16 a[68:71], v[88:91], v[72:75], a[68:71]\n" \
"ds_read_b64_tr_b16 v[170:171], v37 offset:9216\n" \
"v_mfma_f32_16x16x32_bf16 a[68:71], v[152:155], v[136:139], a[68:71]\n" \
"ds_read_b64_tr_b16 v[108:109], v38 offset:0\n" \
"v_mfma_f32_16x16x32_bf16 a[64:67], v[84:87], v[72:75], a[64:67]\n" \
"ds_read_b64_tr_b16 v[110:111], v38 offset:1024\n" \
"v_mfma_f32_16x16x32_bf16 a[64:67], v[148:151], v[136:139], a[64:67]\n" \
"s_cmpk_lt_u32 s3, 3\n" \
"s_cselect_b32 s11, s17, s19\n" \
"v_mfma_f32_16x16x32_bf16 a[128:131], v[84:87], v[76:79], a[128:131]\n" \
"s_add_u32 s88, s41, 0x1000\n" \
"buffer_store_dwordx4 v[204:207], v20, s[80:83], s88 offen offset:1024 sc1\n" \
"v_mfma_f32_16x16x32_bf16 a[128:131], v[148:151], v[140:143], a[128:131]\n" \
"ds_read_b64_tr_b16 v[172:173], v38 offset:8192\n" \
"s_and_b32 s11, s11, 0xffff\n" \
"v_mfma_f32_16x16x32_bf16 a[132:135], v[88:91], v[76:79], a[132:135]\n" \
"ds_read_b64_tr_b16 v[174:175], v38 offset:9216\n" \
"v_writelane_b32 v11, s11, 16\n" \
"v_mfma_f32_16x16x32_bf16 a[132:135], v[152:155], v[140:143], a[132:135]\n" \
"ds_read_b64_tr_b16 v[112:113], v39 offset:0\n" \
"s_mul_i32 s11, s6, 0x48\n" \
"v_mfma_f32_16x16x32_bf16 a[164:167], v[96:99], v[76:79], a[164:167]\n" \
"s_add_i32 s17, s11, 0x47\n" \
"v_mfma_f32_16x16x32_bf16 a[164:167], v[160:163], v[140:143], a[164:167]\n" \
"ds_read_b64_tr_b16 v[114:115], v39 offset:1024\n" \
"v_mfma_f32_16x16x32_bf16 a[160:163], v[76:79], v[92:95], a[160:163]\n" \
"s_add_u32 s89, s41, 0x1000\n" \
"buffer_store_dwordx4 v[208:211], v20, s[80:83], s89 offen offset:2048 sc1\n" \
"v_mfma_f32_16x16x32_bf16 a[160:163], v[140:143], v[156:159], a[160:163]\n" \
"ds_read_b64_tr_b16 v[176:177], v39 offset:8192\n" \
"v_mfma_f32_16x16x32_bf16 a[224:227], v[80:83], v[92:95], a[224:227]\n" \
"ds_read_b64_tr_b16 v[178:179], v39 offset:9216\n" \
"v_mfma_f32_16x16x32_bf16 a[224:227], v[144:147], v[156:159], a[224:227]\n" \
"s_cmpk_lt_u32 s3, 3\n" \
"s_cselect_b32 s17, s17, s18\n" \
"v_mfma_f32_16x16x32_bf16 a[228:231], v[96:99], v[80:83], a[228:231]\n" \
"s_mulk_i32 s17, 0x480\n" \
"s_addk_i32 s17, 0x120\n" \
"v_mfma_f32_16x16x32_bf16 a[228:231], v[160:163], v[144:147], a[228:231]\n" \
"s_add_u32 s88, s41, 0x1000\n" \
"buffer_store_dwordx4 v[212:215], v20, s[80:83], s88 offen offset:3072 sc1\n" \
"v_mfma_f32_16x16x32_bf16 a[196:199], v[88:91], v[80:83], a[196:199]\n" \
"v_writelane_b32 v11, s17, 17\n" \
"s_lshl_b32 s17, s3, 5\n" \
"v_mfma_f32_16x16x32_bf16 a[196:199], v[152:155], v[144:147], a[196:199]\n" \
"s_add_i32 s11, s11, s17\n" \
"s_mul_i32 s17, s11, 0x480\n" \
"v_mfma_f32_16x16x32_bf16 a[192:195], v[84:87], v[80:83], a[192:195]\n" \
"s_mul_i32 s11, s13, 0x24000\n" \
"s_add_i32 s18, s11, 0x1b000\n" \
"v_mfma_f32_16x16x32_bf16 a[192:195], v[148:151], v[144:147], a[192:195]\n" \
"s_cmpk_lt_u32 s3, 3\n" \
"s_cselect_b32 s17, s17, s18\n" \
"v_mfma_f32_16x16x32_bf16 a[8:11], v[88:91], v[64:67], a[8:11]\n" \
"s_mul_i32 s19, s3, 0x9000\n" \
"v_writelane_b32 v11, s17, 18\n" \
"v_mfma_f32_16x16x32_bf16 a[8:11], v[152:155], v[128:131], a[8:11]\n" \
"s_mul_i32 s17, s16, 0x2400\n" \
"s_mul_i32 s16, s16, 0x9000\n" \
"v_mfma_f32_16x16x32_bf16 a[40:43], v[96:99], v[64:67], a[40:43]\n" \
"s_bitset1_b32 s17, 17\n" \
"v_writelane_b32 v11, s17, 19\n" \
"v_mfma_f32_16x16x32_bf16 a[40:43], v[160:163], v[128:131], a[40:43]\n" \
"s_add_i32 s16, s11, s16\n" \
"v_writelane_b32 v11, s16, 25\n" \
"v_mfma_f32_16x16x32_bf16 a[12:15], v[64:67], v[92:95], a[12:15]\n" \
"s_mul_i32 s16, s3, 0x2400\n" \
"s_add_i32 s17, s16, 0x6000\n" \
"v_mfma_f32_16x16x32_bf16 a[12:15], v[128:131], v[156:159], a[12:15]\n" \
"s_waitcnt vmcnt(4) lgkmcnt(0)\n" \
"s_barrier\n" \
"v_mfma_f32_16x16x32_bf16 a[144:147], v[100:103], v[68:71], a[144:147]\n" \
"s_add_u32 m0, s40, 0x0\n" \
"s_nop 0\n" \
"s_add_u32 s85, s39, 0x8000\n" \
"v_mfma_f32_16x16x32_bf16 a[144:147], v[164:167], v[132:135], a[144:147]\n" \
"buffer_load_dwordx4 v20, s[80:83], s85 offen sc1 lds\n" \
"v_mfma_f32_16x16x32_bf16 a[148:151], v[104:107], v[68:71], a[148:151]\n" \
"ds_read_b64_tr_b16 v[84:85], v32 offset:32768\n" \
"v_writelane_b32 v11, s17, 20\n" \
"v_mfma_f32_16x16x32_bf16 a[148:151], v[168:171], v[132:135], a[148:151]\n" \
"ds_read_b64_tr_b16 v[86:87], v32 offset:33280\n" \
"s_or_b32 s17, s16, 0x10000\n" \
"v_mfma_f32_16x16x32_bf16 a[200:203], v[108:111], v[68:71], a[200:203]\n" \
"s_add_u32 m0, s40, 0x1000\n" \
"s_add_u32 s84, s39, 0x9000\n" \
"v_mfma_f32_16x16x32_bf16 a[200:203], v[172:175], v[132:135], a[200:203]\n" \
"buffer_load_dwordx4 v20, s[80:83], s84 offen sc1 lds\n" \
"v_mfma_f32_16x16x32_bf16 a[204:207], v[112:115], v[68:71], a[204:207]\n" \
"ds_read_b64_tr_b16 v[148:149], v32 offset:36864\n" \
"s_add_i32 s18, s16, 0x19000\n" \
"v_mfma_f32_16x16x32_bf16 a[204:207], v[176:179], v[132:135], a[204:207]\n" \
"ds_read_b128 v[68:71], v28 offset:40960\n" \
"v_writelane_b32 v11, s17, 21\n" \
"v_mfma_f32_16x16x32_bf16 a[236:239], v[112:115], v[72:75], a[236:239]\n" \
"ds_read_b128 v[132:135], v29 offset:40960\n" \
"s_mov_b32 s17, 0\n" \
"v_mfma_f32_16x16x32_bf16 a[236:239], v[176:179], v[136:139], a[236:239]\n" \
"s_add_u32 m0, s40, 0x3000\n" \
"s_mov_b32 s85, 0x200\n" \
"v_mfma_f32_16x16x32_bf16 a[232:235], v[108:111], v[72:75], a[232:235]\n" \
"buffer_load_dwordx4 v21, s[76:79], s85 offen lds\n" \
"v_mfma_f32_16x16x32_bf16 a[232:235], v[172:175], v[136:139], a[232:235]\n" \
"ds_read_b64_tr_b16 v[150:151], v32 offset:37376\n" \
"v_mfma_f32_16x16x32_bf16 a[180:183], v[104:107], v[72:75], a[180:183]\n" \
"s_add_u32 m0, s30, 0x0\n" \
"s_mov_b32 s84, 0x200\n" \
"v_mfma_f32_16x16x32_bf16 a[180:183], v[168:171], v[136:139], a[180:183]\n" \
"buffer_load_dwordx4 v24, s[76:79], s84 offen lds\n" \
"v_mfma_f32_16x16x32_bf16 a[176:179], v[100:103], v[72:75], a[176:179]\n" \
"ds_read_b64_tr_b16 v[88:89], v33 offset:32768\n" \
"v_mfma_f32_16x16x32_bf16 a[176:179], v[164:167], v[136:139], a[176:179]\n" \
"ds_read_b128 v[72:75], v28 offset:43008\n" \
"v_mfma_f32_16x16x32_bf16 a[72:75], v[100:103], v[76:79], a[72:75]\n" \
"ds_read_b128 v[136:139], v29 offset:43008\n" \
"v_mfma_f32_16x16x32_bf16 a[72:75], v[164:167], v[140:143], a[72:75]\n" \
"s_add_u32 m0, s30, 0x400\n" \
"s_mov_b32 s85, 0x2600\n" \
"v_mfma_f32_16x16x32_bf16 a[76:79], v[104:107], v[76:79], a[76:79]\n" \
"buffer_load_dwordx4 v25, s[76:79], s85 offen lds\n" \
"v_mfma_f32_16x16x32_bf16 a[76:79], v[168:171], v[140:143], a[76:79]\n" \
"ds_read_b64_tr_b16 v[90:91], v33 offset:33280\n" \
"v_mfma_f32_16x16x32_bf16 a[104:107], v[108:111], v[76:79], a[104:107]\n" \
"ds_read_b64_tr_b16 v[152:153], v33 offset:36864\n" \
"v_mfma_f32_16x16x32_bf16 a[104:107], v[172:175], v[140:143], a[104:107]\n" \
"s_add_u32 m0, s31, 0x0\n" \
"s_nop 0\n" \
"s_mov_b32 s84, 0x200000\n" \
"v_mfma_f32_16x16x32_bf16 a[108:111], v[112:115], v[76:79], a[108:111]\n" \
"buffer_load_dwordx4 v26, s[68:71], s84 offen lds\n" \
"v_mfma_f32_16x16x32_bf16 a[108:111], v[176:179], v[140:143], a[108:111]\n" \
"ds_read_b128 v[76:79], v28 offset:45056\n" \
"v_mfma_f32_16x16x32_bf16 a[172:175], v[112:115], v[80:83], a[172:175]\n" \
"ds_read_b128 v[140:143], v29 offset:45056\n" \
"v_mfma_f32_16x16x32_bf16 a[172:175], v[176:179], v[144:147], a[172:175]\n" \
"s_add_u32 m0, s30, 0x800\n" \
"s_mov_b32 s85, 0x4a00\n" \
"v_mfma_f32_16x16x32_bf16 a[168:171], v[108:111], v[80:83], a[168:171]\n" \
"buffer_load_dwordx4 v24, s[76:79], s85 offen lds\n" \
"v_mfma_f32_16x16x32_bf16 a[168:171], v[172:175], v[144:147], a[168:171]\n" \
"ds_read_b64_tr_b16 v[154:155], v33 offset:37376\n" \
"v_mfma_f32_16x16x32_bf16 a[140:143], v[104:107], v[80:83], a[140:143]\n" \
"ds_read_b64_tr_b16 v[96:97], v35 offset:32768\n" \
"v_mfma_f32_16x16x32_bf16 a[140:143], v[168:171], v[144:147], a[140:143]\n" \
"ds_read_b64_tr_b16 v[98:99], v35 offset:33280\n" \
"v_mfma_f32_16x16x32_bf16 a[136:139], v[100:103], v[80:83], a[136:139]\n" \
"s_cmpk_lt_u32 s3, 3\n" \
"s_cselect_b32 s18, s18, s17\n" \
"v_mfma_f32_16x16x32_bf16 a[136:139], v[164:167], v[144:147], a[136:139]\n" \
"ds_read_b128 v[80:83], v28 offset:47104\n" \
"s_bitset1_b32 s16, 17\n" \
"s_nop 0\n" \
"v_mfma_f32_16x16x32_bf16 a[80:83], v[100:103], v[60:63], a[80:83]\n" \
"ds_read_b128 v[144:147], v29 offset:47104\n" \
"v_writelane_b32 v11, s18, 22\n" \
"v_mfma_f32_16x16x32_bf16 a[80:83], v[164:167], v[124:127], a[80:83]\n" \
"s_add_u32 m0, s30, 0xc00\n" \
"s_mov_b32 s84, 0x6e00\n" \
"v_mfma_f32_16x16x32_bf16 a[84:87], v[104:107], v[60:63], a[84:87]\n" \
"buffer_load_dwordx4 v25, s[76:79], s84 offen lds\n" \
"v_mfma_f32_16x16x32_bf16 a[84:87], v[168:171], v[124:127], a[84:87]\n" \
"s_add_u32 m0, s31, 0x400\n" \
"s_mov_b32 s85, 0x208000\n" \
"v_mfma_f32_16x16x32_bf16 a[88:91], v[108:111], v[60:63], a[88:91]\n" \
"buffer_load_dwordx4 v26, s[68:71], s85 offen lds\n" \
"v_mfma_f32_16x16x32_bf16 a[88:91], v[172:175], v[124:127], a[88:91]\n" \
"ds_read_b64_tr_b16 v[160:161], v35 offset:36864\n" \
"s_movk_i32 s18, 0x2400\n" \
"s_nop 0\n" \
"v_mfma_f32_16x16x32_bf16 a[92:95], v[112:115], v[60:63], a[92:95]\n" \
"ds_read_b64_tr_b16 v[162:163], v35 offset:37376\n" \
"v_mfma_f32_16x16x32_bf16 a[92:95], v[176:179], v[124:127], a[92:95]\n" \
"ds_read_b128 v[60:63], v28 offset:36864\n" \
"v_mfma_f32_16x16x32_bf16 a[124:127], v[112:115], v[64:67], a[124:127]\n" \
"ds_read_b128 v[124:127], v29 offset:36864\n" \
"v_mfma_f32_16x16x32_bf16 a[124:127], v[176:179], v[128:131], a[124:127]\n" \
"s_add_u32 m0, s30, 0x1000\n" \
"s_mov_b32 s84, 0x9200\n" \
"v_mfma_f32_16x16x32_bf16 a[120:123], v[108:111], v[64:67], a[120:123]\n" \
"buffer_load_dwordx4 v24, s[76:79], s84 offen lds\n" \
"v_mfma_f32_16x16x32_bf16 a[120:123], v[172:175], v[128:131], a[120:123]\n" \
"s_add_u32 m0, s30, 0x1400\n" \
"s_mov_b32 s85, 0xb600\n" \
"v_mfma_f32_16x16x32_bf16 a[116:119], v[104:107], v[64:67], a[116:119]\n" \
"buffer_load_dwordx4 v25, s[76:79], s85 offen lds\n" \
"v_mfma_f32_16x16x32_bf16 a[116:119], v[168:171], v[128:131], a[116:119]\n" \
"ds_read_b64_tr_b16 v[92:93], v34 offset:32768\n" \
"v_mfma_f32_16x16x32_bf16 a[112:115], v[100:103], v[64:67], a[112:115]\n" \
"ds_read_b64_tr_b16 v[94:95], v34 offset:33280\n" \
"v_mfma_f32_16x16x32_bf16 a[112:115], v[164:167], v[128:131], a[112:115]\n" \
"ds_read_b128 v[64:67], v28 offset:38912\n" \
"v_mfma_f32_16x16x32_bf16 a[16:19], v[100:103], v[52:55], a[16:19]\n" \
"ds_read_b128 v[128:131], v29 offset:38912\n" \
"v_mfma_f32_16x16x32_bf16 a[16:19], v[164:167], v[116:119], a[16:19]\n" \
"s_add_u32 m0, s31, 0x800\n" \
"s_mov_b32 s84, 0x210000\n" \
"v_mfma_f32_16x16x32_bf16 a[20:23], v[104:107], v[52:55], a[20:23]\n" \
"buffer_load_dwordx4 v27, s[68:71], s84 offen lds\n" \
"v_mfma_f32_16x16x32_bf16 a[20:23], v[168:171], v[116:119], a[20:23]\n" \
"s_add_u32 m0, s30, 0x1800\n" \
"s_mov_b32 s85, 0xda00\n" \
"v_mfma_f32_16x16x32_bf16 a[24:27], v[108:111], v[52:55], a[24:27]\n" \
"buffer_load_dwordx4 v24, s[76:79], s85 offen lds\n" \
"v_mfma_f32_16x16x32_bf16 a[24:27], v[172:175], v[116:119], a[24:27]\n" \
"ds_read_b64_tr_b16 v[156:157], v34 offset:36864\n" \
"v_mfma_f32_16x16x32_bf16 a[28:31], v[112:115], v[52:55], a[28:31]\n" \
"ds_read_b64_tr_b16 v[158:159], v34 offset:37376\n" \
"v_mfma_f32_16x16x32_bf16 a[28:31], v[176:179], v[116:119], a[28:31]\n" \
"ds_read_b128 v[52:55], v28 offset:32768\n" \
"v_mfma_f32_16x16x32_bf16 a[60:63], v[112:115], v[56:59], a[60:63]\n" \
"ds_read_b128 v[116:119], v29 offset:32768\n" \
"v_mfma_f32_16x16x32_bf16 a[60:63], v[176:179], v[120:123], a[60:63]\n" \
"s_cmpk_lt_u32 s3, 3\n" \
"s_cselect_b32 s16, s16, s18\n" \
"v_mfma_f32_16x16x32_bf16 a[56:59], v[108:111], v[56:59], a[56:59]\n" \
"s_xor_b32 s13, s13, 2\n" \
"v_writelane_b32 v11, s16, 23\n" \
"v_mfma_f32_16x16x32_bf16 a[56:59], v[172:175], v[120:123], a[56:59]\n" \
"s_lshl_b32 s16, s3, 10\n" \
"s_or_b32 s16, s16, 0xf000\n" \
"v_mfma_f32_16x16x32_bf16 a[52:55], v[104:107], v[56:59], a[52:55]\n" \
"v_writelane_b32 v11, s16, 24\n" \
"s_add_i32 s16, s15, 1\n" \
"v_mfma_f32_16x16x32_bf16 a[52:55], v[168:171], v[120:123], a[52:55]\n" \
"s_and_b32 s16, s16, 3\n" \
"s_or_b32 s16, s16, s5\n" \
"v_mfma_f32_16x16x32_bf16 a[48:51], v[100:103], v[56:59], a[48:51]\n" \
"s_mul_i32 s18, s16, 0x24000\n" \
"s_add_i32 s18, s18, s19\n" \
"v_mfma_f32_16x16x32_bf16 a[48:51], v[164:167], v[120:123], a[48:51]\n" \
"ds_read_b128 v[56:59], v28 offset:34816\n" \
"ds_read_b128 v[120:123], v29 offset:34816\n" \
"s_waitcnt lgkmcnt(15)\n" \
"v_mfma_f32_16x16x32_bf16 a[0:3], v[84:87], v[68:71], a[0:3]\n" \
"s_add_u32 m0, s30, 0x1c00\n" \
"s_mov_b32 s85, 0xfe00\n" \
"v_mfma_f32_16x16x32_bf16 a[0:3], v[148:151], v[132:135], a[0:3]\n" \
"buffer_load_dwordx4 v25, s[76:79], s85 offen lds\n" \
"v_mfma_f32_16x16x32_bf16 a[4:7], v[88:91], v[68:71], a[4:7]\n" \
"ds_read_b64_tr_b16 v[100:101], v36 offset:32768\n" \
"s_add_i32 s15, s15, -1\n" \
"v_mfma_f32_16x16x32_bf16 a[4:7], v[152:155], v[132:135], a[4:7]\n" \
"s_add_u32 m0, s31, 0xc00\n" \
"s_mov_b32 s84, 0x218000\n" \
"v_mfma_f32_16x16x32_bf16 a[36:39], v[96:99], v[68:71], a[36:39]\n" \
"buffer_load_dwordx4 v27, s[68:71], s84 offen lds\n" \
"s_waitcnt lgkmcnt(13)\n" \
"v_mfma_f32_16x16x32_bf16 a[36:39], v[160:163], v[132:135], a[36:39]\n" \
"ds_read_b64_tr_b16 v[102:103], v36 offset:33792\n" \
"v_writelane_b32 v11, s18, 26\n" \
"s_waitcnt lgkmcnt(10)\n" \
"v_mfma_f32_16x16x32_bf16 a[32:35], v[68:71], v[92:95], a[32:35]\n" \
"ds_read_b64_tr_b16 v[164:165], v36 offset:40960\n" \
"s_mul_i32 s18, s13, 0x24000\n" \
"s_waitcnt lgkmcnt(7)\n" \
"v_mfma_f32_16x16x32_bf16 a[32:35], v[132:135], v[156:159], a[32:35]\n" \
"ds_read_b64_tr_b16 v[166:167], v36 offset:41984\n" \
"s_and_b32 s15, s15, 3\n" \
"v_mfma_f32_16x16x32_bf16 a[96:99], v[72:75], v[92:95], a[96:99]\n" \
"ds_read_b64_tr_b16 v[104:105], v37 offset:32768\n" \
"s_add_i32 s18, s18, s19\n" \
"v_mfma_f32_16x16x32_bf16 a[96:99], v[136:139], v[156:159], a[96:99]\n" \
"ds_read_b64_tr_b16 v[106:107], v37 offset:33792\n" \
"s_or_b32 s15, s15, s5\n" \
"v_mfma_f32_16x16x32_bf16 a[100:103], v[96:99], v[72:75], a[100:103]\n" \
"ds_read_b64_tr_b16 v[168:169], v37 offset:40960\n" \
"v_writelane_b32 v11, s18, 27\n" \
"v_mfma_f32_16x16x32_bf16 a[100:103], v[160:163], v[136:139], a[100:103]\n" \
"s_add_u32 s89, s42, 0x0\n" \
"buffer_store_dwordx4 v[216:219], v20, s[80:83], s89 offen offset:3072 sc1\n" \
"v_mfma_f32_16x16x32_bf16 a[68:71], v[88:91], v[72:75], a[68:71]\n" \
"ds_read_b64_tr_b16 v[170:171], v37 offset:41984\n" \
"s_mul_i32 s18, s15, 0x24000\n" \
"v_mfma_f32_16x16x32_bf16 a[68:71], v[152:155], v[136:139], a[68:71]\n" \
"ds_read_b64_tr_b16 v[108:109], v38 offset:32768\n" \
"s_add_i32 s18, s18, s19\n" \
"v_mfma_f32_16x16x32_bf16 a[64:67], v[84:87], v[72:75], a[64:67]\n" \
"ds_read_b64_tr_b16 v[110:111], v38 offset:33792\n" \
"v_writelane_b32 v11, s18, 28\n" \
"v_mfma_f32_16x16x32_bf16 a[64:67], v[148:151], v[136:139], a[64:67]\n" \
"s_or_b32 s18, s9, s14\n" \
"s_mul_i32 s18, s18, 0x24800\n" \
"v_mfma_f32_16x16x32_bf16 a[128:131], v[84:87], v[76:79], a[128:131]\n" \
"s_add_u32 s88, s42, 0x0\n" \
"buffer_store_dwordx4 v[220:223], v20, s[80:83], s88 offen offset:2048 sc1\n" \
"v_mfma_f32_16x16x32_bf16 a[128:131], v[148:151], v[140:143], a[128:131]\n" \
"ds_read_b64_tr_b16 v[172:173], v38 offset:40960\n" \
"s_lshl_b32 s16, s16, 12\n" \
"v_mfma_f32_16x16x32_bf16 a[132:135], v[88:91], v[76:79], a[132:135]\n" \
"ds_read_b64_tr_b16 v[174:175], v38 offset:41984\n" \
"s_add_i32 s19, s18, 0x3004800\n" \
"v_mfma_f32_16x16x32_bf16 a[132:135], v[152:155], v[140:143], a[132:135]\n" \
"ds_read_b64_tr_b16 v[112:113], v39 offset:32768\n" \
"s_add_i32 s20, s19, s16\n" \
"v_mfma_f32_16x16x32_bf16 a[164:167], v[96:99], v[76:79], a[164:167]\n" \
"v_writelane_b32 v11, s20, 29\n" \
"s_lshl_b32 s13, s13, 12\n" \
"v_mfma_f32_16x16x32_bf16 a[164:167], v[160:163], v[140:143], a[164:167]\n" \
"ds_read_b64_tr_b16 v[114:115], v39 offset:33792\n" \
"s_lshl_b32 s15, s15, 12\n" \
"v_mfma_f32_16x16x32_bf16 a[160:163], v[76:79], v[92:95], a[160:163]\n" \
"s_add_u32 s89, s42, 0x0\n" \
"buffer_store_dwordx4 v[224:227], v20, s[80:83], s89 offen offset:1024 sc1\n" \
"v_mfma_f32_16x16x32_bf16 a[160:163], v[140:143], v[156:159], a[160:163]\n" \
"ds_read_b64_tr_b16 v[176:177], v39 offset:40960\n" \
"s_sub_i32 s14, s9, s14\n" \
"v_mfma_f32_16x16x32_bf16 a[224:227], v[80:83], v[92:95], a[224:227]\n" \
"ds_read_b64_tr_b16 v[178:179], v39 offset:41984\n" \
"s_add_i32 s20, s19, s13\n" \
"v_mfma_f32_16x16x32_bf16 a[224:227], v[144:147], v[156:159], a[224:227]\n" \
"v_writelane_b32 v11, s20, 30\n" \
"s_add_i32 s19, s19, s15\n" \
"v_mfma_f32_16x16x32_bf16 a[228:231], v[96:99], v[80:83], a[228:231]\n" \
"s_mul_i32 s14, s14, 0x24800\n" \
"v_writelane_b32 v11, s19, 31\n" \
"v_mfma_f32_16x16x32_bf16 a[228:231], v[160:163], v[144:147], a[228:231]\n" \
"s_add_u32 s88, s42, 0x0\n" \
"buffer_store_dwordx4 v[228:231], v20, s[80:83], s88 offen offset:0 sc1\n" \
"v_mfma_f32_16x16x32_bf16 a[196:199], v[88:91], v[80:83], a[196:199]\n" \
"s_add_i32 s19, s14, 0x4120800\n" \
"s_add_i32 s16, s19, s16\n" \
"v_mfma_f32_16x16x32_bf16 a[196:199], v[152:155], v[144:147], a[196:199]\n" \
"v_writelane_b32 v11, s16, 32\n" \
"s_add_i32 s13, s19, s13\n" \
"v_mfma_f32_16x16x32_bf16 a[192:195], v[84:87], v[80:83], a[192:195]\n" \
"v_writelane_b32 v11, s13, 33\n" \
"s_add_i32 s19, s19, s15\n" \
"v_mfma_f32_16x16x32_bf16 a[192:195], v[148:151], v[144:147], a[192:195]\n" \
"s_or_b32 s13, s18, 0x3000000\n" \
"s_lshl_b32 s15, s6, 11\n" \
"v_mfma_f32_16x16x32_bf16 a[8:11], v[88:91], v[64:67], a[8:11]\n" \
"v_writelane_b32 v11, s19, 34\n" \
"s_add_i32 s16, s13, s15\n" \
"v_mfma_f32_16x16x32_bf16 a[8:11], v[152:155], v[128:131], a[8:11]\n" \
"v_writelane_b32 v11, s16, 35\n" \
"s_add_i32 s16, s14, 0x411c000\n" \
"v_mfma_f32_16x16x32_bf16 a[40:43], v[96:99], v[64:67], a[40:43]\n" \
"s_add_i32 s19, s16, s15\n" \
"v_writelane_b32 v11, s19, 36\n" \
"v_mfma_f32_16x16x32_bf16 a[40:43], v[160:163], v[128:131], a[40:43]\n" \
"s_add_i32 s18, s18, 0x3004000\n" \
"v_writelane_b32 v11, s18, 37\n" \
"v_mfma_f32_16x16x32_bf16 a[12:15], v[64:67], v[92:95], a[12:15]\n" \
"s_add_i32 s14, s14, 0x4120000\n" \
"v_writelane_b32 v11, s14, 38\n" \
"v_mfma_f32_16x16x32_bf16 a[12:15], v[128:131], v[156:159], a[12:15]\n" \
"s_waitcnt vmcnt(4) lgkmcnt(0)\n" \
"s_barrier\n" \
"v_mov_b32_e32 v45, s62\n" \
"s_mov_b64 exec, 1\n" \
"buffer_store_dword v45, off, s[80:83], s37 offset:0 sc1\n" \
"s_mov_b64 exec, -1\n" \
"v_mfma_f32_16x16x32_bf16 a[144:147], v[100:103], v[68:71], a[144:147]\n" \
"s_add_u32 m0, s40, 0x8000\n" \
"s_add_u32 s85, s39, 0xa000\n" \
"v_mfma_f32_16x16x32_bf16 a[144:147], v[164:167], v[132:135], a[144:147]\n" \
"buffer_load_dwordx4 v20, s[80:83], s85 offen sc1 lds\n" \
"v_mfma_f32_16x16x32_bf16 a[148:151], v[104:107], v[68:71], a[148:151]\n" \
"ds_read_b64_tr_b16 v[84:85], v32 offset:0\n" \
"v_accvgpr_read_b32 v212, a220\n" \
"v_mfma_f32_16x16x32_bf16 a[148:151], v[168:171], v[132:135], a[148:151]\n" \
"ds_read_b64_tr_b16 v[86:87], v32 offset:512\n" \
"v_accvgpr_read_b32 v213, a221\n" \
"v_mfma_f32_16x16x32_bf16 a[200:203], v[108:111], v[68:71], a[200:203]\n" \
"s_add_u32 m0, s40, 0x9000\n" \
"s_add_u32 s84, s39, 0xb000\n" \
"v_mfma_f32_16x16x32_bf16 a[200:203], v[172:175], v[132:135], a[200:203]\n" \
"buffer_load_dwordx4 v20, s[80:83], s84 offen sc1 lds\n" \
"v_mfma_f32_16x16x32_bf16 a[204:207], v[112:115], v[68:71], a[204:207]\n" \
"ds_read_b64_tr_b16 v[148:149], v32 offset:4096\n" \
"v_accvgpr_read_b32 v214, a222\n" \
"v_mfma_f32_16x16x32_bf16 a[204:207], v[176:179], v[132:135], a[204:207]\n" \
"ds_read_b128 v[68:71], v28 offset:8192\n" \
"v_accvgpr_read_b32 v215, a223\n" \
"v_mfma_f32_16x16x32_bf16 a[236:239], v[112:115], v[72:75], a[236:239]\n" \
"ds_read_b128 v[132:135], v29 offset:8192\n" \
"v_accvgpr_read_b32 v180, a252\n" \
"v_mfma_f32_16x16x32_bf16 a[236:239], v[176:179], v[136:139], a[236:239]\n" \
"s_add_u32 m0, s40, 0xb000\n" \
"s_mov_b32 s85, 0x280\n" \
"v_mfma_f32_16x16x32_bf16 a[232:235], v[108:111], v[72:75], a[232:235]\n" \
"buffer_load_dwordx4 v21, s[76:79], s85 offen lds\n" \
"v_mfma_f32_16x16x32_bf16 a[232:235], v[172:175], v[136:139], a[232:235]\n" \
"ds_read_b64_tr_b16 v[150:151], v32 offset:4608\n" \
"v_accvgpr_read_b32 v181, a253\n" \
"v_mfma_f32_16x16x32_bf16 a[180:183], v[104:107], v[72:75], a[180:183]\n" \
"s_add_u32 m0, s30, 0x8000\n" \
"s_mov_b32 s84, 0x280\n" \
"v_mfma_f32_16x16x32_bf16 a[180:183], v[168:171], v[136:139], a[180:183]\n" \
"buffer_load_dwordx4 v24, s[76:79], s84 offen lds\n" \
"v_mfma_f32_16x16x32_bf16 a[176:179], v[100:103], v[72:75], a[176:179]\n" \
"ds_read_b64_tr_b16 v[88:89], v33 offset:0\n" \
"v_accvgpr_read_b32 v182, a254\n" \
"v_mfma_f32_16x16x32_bf16 a[176:179], v[164:167], v[136:139], a[176:179]\n" \
"ds_read_b128 v[72:75], v28 offset:10240\n" \
"v_accvgpr_read_b32 v183, a255\n" \
"v_mfma_f32_16x16x32_bf16 a[72:75], v[100:103], v[76:79], a[72:75]\n" \
"ds_read_b128 v[136:139], v29 offset:10240\n" \
"v_cvt_pk_bf16_f32 v212, v212, v213\n" \
"v_mfma_f32_16x16x32_bf16 a[72:75], v[164:167], v[140:143], a[72:75]\n" \
"s_add_u32 m0, s30, 0x8400\n" \
"s_mov_b32 s85, 0x2680\n" \
"v_mfma_f32_16x16x32_bf16 a[76:79], v[104:107], v[76:79], a[76:79]\n" \
"buffer_load_dwordx4 v25, s[76:79], s85 offen lds\n" \
"v_mfma_f32_16x16x32_bf16 a[76:79], v[168:171], v[140:143], a[76:79]\n" \
"ds_read_b64_tr_b16 v[90:91], v33 offset:512\n" \
"v_cvt_pk_bf16_f32 v213, v214, v215\n" \
"v_mfma_f32_16x16x32_bf16 a[104:107], v[108:111], v[76:79], a[104:107]\n" \
"ds_read_b64_tr_b16 v[152:153], v33 offset:4096\n" \
"v_cvt_pk_bf16_f32 v214, v180, v181\n" \
"v_mfma_f32_16x16x32_bf16 a[104:107], v[172:175], v[140:143], a[104:107]\n" \
"s_add_u32 m0, s31, 0x8000\n" \
"s_mov_b32 s84, 0x280000\n" \
"v_mfma_f32_16x16x32_bf16 a[108:111], v[112:115], v[76:79], a[108:111]\n" \
"buffer_load_dwordx4 v26, s[68:71], s84 offen lds\n" \
"v_mfma_f32_16x16x32_bf16 a[108:111], v[176:179], v[140:143], a[108:111]\n" \
"ds_read_b128 v[76:79], v28 offset:12288\n" \
"v_cvt_pk_bf16_f32 v215, v182, v183\n" \
"v_mfma_f32_16x16x32_bf16 a[172:175], v[112:115], v[80:83], a[172:175]\n" \
"ds_read_b128 v[140:143], v29 offset:12288\n" \
"v_accvgpr_read_b32 v208, a216\n" \
"v_mfma_f32_16x16x32_bf16 a[172:175], v[176:179], v[144:147], a[172:175]\n" \
"s_add_u32 m0, s30, 0x8800\n" \
"s_mov_b32 s85, 0x4a80\n" \
"v_mfma_f32_16x16x32_bf16 a[168:171], v[108:111], v[80:83], a[168:171]\n" \
"buffer_load_dwordx4 v24, s[76:79], s85 offen lds\n" \
"v_mfma_f32_16x16x32_bf16 a[168:171], v[172:175], v[144:147], a[168:171]\n" \
"ds_read_b64_tr_b16 v[154:155], v33 offset:4608\n" \
"v_accvgpr_read_b32 v209, a217\n" \
"v_mfma_f32_16x16x32_bf16 a[140:143], v[104:107], v[80:83], a[140:143]\n" \
"ds_read_b64_tr_b16 v[96:97], v35 offset:0\n" \
"v_accvgpr_read_b32 v210, a218\n" \
"v_mfma_f32_16x16x32_bf16 a[140:143], v[168:171], v[144:147], a[140:143]\n" \
"ds_read_b64_tr_b16 v[98:99], v35 offset:512\n" \
"v_accvgpr_read_b32 v211, a219\n" \
"v_mfma_f32_16x16x32_bf16 a[136:139], v[100:103], v[80:83], a[136:139]\n" \
"v_accvgpr_read_b32 v180, a248\n" \
"v_accvgpr_read_b32 v181, a249\n" \
"v_mfma_f32_16x16x32_bf16 a[136:139], v[164:167], v[144:147], a[136:139]\n" \
"ds_read_b128 v[80:83], v28 offset:14336\n" \
"v_accvgpr_read_b32 v182, a250\n" \
"v_mfma_f32_16x16x32_bf16 a[80:83], v[100:103], v[60:63], a[80:83]\n" \
"ds_read_b128 v[144:147], v29 offset:14336\n" \
"v_accvgpr_read_b32 v183, a251\n" \
"v_mfma_f32_16x16x32_bf16 a[80:83], v[164:167], v[124:127], a[80:83]\n" \
"s_add_u32 m0, s30, 0x8c00\n" \
"s_mov_b32 s84, 0x6e80\n" \
"v_mfma_f32_16x16x32_bf16 a[84:87], v[104:107], v[60:63], a[84:87]\n" \
"buffer_load_dwordx4 v25, s[76:79], s84 offen lds\n" \
"v_mfma_f32_16x16x32_bf16 a[84:87], v[168:171], v[124:127], a[84:87]\n" \
"s_add_u32 m0, s31, 0x8400\n" \
"s_mov_b32 s85, 0x288000\n" \
"v_mfma_f32_16x16x32_bf16 a[88:91], v[108:111], v[60:63], a[88:91]\n" \
"buffer_load_dwordx4 v26, s[68:71], s85 offen lds\n" \
"v_mfma_f32_16x16x32_bf16 a[88:91], v[172:175], v[124:127], a[88:91]\n" \
"ds_read_b64_tr_b16 v[160:161], v35 offset:4096\n" \
"v_cvt_pk_bf16_f32 v208, v208, v209\n" \
"v_mfma_f32_16x16x32_bf16 a[92:95], v[112:115], v[60:63], a[92:95]\n" \
"ds_read_b64_tr_b16 v[162:163], v35 offset:4608\n" \
"v_cvt_pk_bf16_f32 v209, v210, v211\n" \
"v_mfma_f32_16x16x32_bf16 a[92:95], v[176:179], v[124:127], a[92:95]\n" \
"ds_read_b128 v[60:63], v28 offset:4096\n" \
"v_cvt_pk_bf16_f32 v210, v180, v181\n" \
"v_mfma_f32_16x16x32_bf16 a[124:127], v[112:115], v[64:67], a[124:127]\n" \
"ds_read_b128 v[124:127], v29 offset:4096\n" \
"v_cvt_pk_bf16_f32 v211, v182, v183\n" \
"v_mfma_f32_16x16x32_bf16 a[124:127], v[176:179], v[128:131], a[124:127]\n" \
"s_add_u32 m0, s30, 0x9000\n" \
"s_mov_b32 s84, 0x9280\n" \
"v_mfma_f32_16x16x32_bf16 a[120:123], v[108:111], v[64:67], a[120:123]\n" \
"buffer_load_dwordx4 v24, s[76:79], s84 offen lds\n" \
"v_mfma_f32_16x16x32_bf16 a[120:123], v[172:175], v[128:131], a[120:123]\n" \
"s_add_u32 m0, s30, 0x9400\n" \
"s_mov_b32 s85, 0xb680\n" \
"v_mfma_f32_16x16x32_bf16 a[116:119], v[104:107], v[64:67], a[116:119]\n" \
"buffer_load_dwordx4 v25, s[76:79], s85 offen lds\n" \
"v_mfma_f32_16x16x32_bf16 a[116:119], v[168:171], v[128:131], a[116:119]\n" \
"ds_read_b64_tr_b16 v[92:93], v34 offset:0\n" \
"v_accvgpr_read_b32 v204, a212\n" \
"v_mfma_f32_16x16x32_bf16 a[112:115], v[100:103], v[64:67], a[112:115]\n" \
"ds_read_b64_tr_b16 v[94:95], v34 offset:512\n" \
"v_accvgpr_read_b32 v205, a213\n" \
"v_mfma_f32_16x16x32_bf16 a[112:115], v[164:167], v[128:131], a[112:115]\n" \
"ds_read_b128 v[64:67], v28 offset:6144\n" \
"v_accvgpr_read_b32 v206, a214\n" \
"v_mfma_f32_16x16x32_bf16 a[16:19], v[100:103], v[52:55], a[16:19]\n" \
"ds_read_b128 v[128:131], v29 offset:6144\n" \
"v_accvgpr_read_b32 v207, a215\n" \
"v_mfma_f32_16x16x32_bf16 a[16:19], v[164:167], v[116:119], a[16:19]\n" \
"s_add_u32 m0, s31, 0x8800\n" \
"s_mov_b32 s84, 0x290000\n" \
"v_mfma_f32_16x16x32_bf16 a[20:23], v[104:107], v[52:55], a[20:23]\n" \
"buffer_load_dwordx4 v27, s[68:71], s84 offen lds\n" \
"v_mfma_f32_16x16x32_bf16 a[20:23], v[168:171], v[116:119], a[20:23]\n" \
"s_add_u32 m0, s30, 0x9800\n" \
"s_mov_b32 s85, 0xda80\n" \
"v_mfma_f32_16x16x32_bf16 a[24:27], v[108:111], v[52:55], a[24:27]\n" \
"buffer_load_dwordx4 v24, s[76:79], s85 offen lds\n" \
"v_mfma_f32_16x16x32_bf16 a[24:27], v[172:175], v[116:119], a[24:27]\n" \
"ds_read_b64_tr_b16 v[156:157], v34 offset:4096\n" \
"v_accvgpr_read_b32 v180, a244\n" \
"v_mfma_f32_16x16x32_bf16 a[28:31], v[112:115], v[52:55], a[28:31]\n" \
"ds_read_b64_tr_b16 v[158:159], v34 offset:4608\n" \
"v_accvgpr_read_b32 v181, a245\n" \
"v_mfma_f32_16x16x32_bf16 a[28:31], v[176:179], v[116:119], a[28:31]\n" \
"ds_read_b128 v[52:55], v28 offset:0\n" \
"v_accvgpr_read_b32 v182, a246\n" \
"v_mfma_f32_16x16x32_bf16 a[60:63], v[112:115], v[56:59], a[60:63]\n" \
"ds_read_b128 v[116:119], v29 offset:0\n" \
"v_accvgpr_read_b32 v183, a247\n" \
"v_mfma_f32_16x16x32_bf16 a[60:63], v[176:179], v[120:123], a[60:63]\n" \
"v_cvt_pk_bf16_f32 v204, v204, v205\n" \
"v_cvt_pk_bf16_f32 v205, v206, v207\n" \
"v_mfma_f32_16x16x32_bf16 a[56:59], v[108:111], v[56:59], a[56:59]\n" \
"v_cvt_pk_bf16_f32 v206, v180, v181\n" \
"v_cvt_pk_bf16_f32 v207, v182, v183\n" \
"v_mfma_f32_16x16x32_bf16 a[56:59], v[172:175], v[120:123], a[56:59]\n" \
"v_accvgpr_read_b32 v200, a208\n" \
"v_accvgpr_read_b32 v201, a209\n" \
"v_mfma_f32_16x16x32_bf16 a[52:55], v[104:107], v[56:59], a[52:55]\n" \
"v_accvgpr_read_b32 v202, a210\n" \
"v_accvgpr_read_b32 v203, a211\n" \
"v_mfma_f32_16x16x32_bf16 a[52:55], v[168:171], v[120:123], a[52:55]\n" \
"v_accvgpr_read_b32 v180, a240\n" \
"v_accvgpr_read_b32 v181, a241\n" \
"v_mfma_f32_16x16x32_bf16 a[48:51], v[100:103], v[56:59], a[48:51]\n" \
"v_accvgpr_read_b32 v182, a242\n" \
"v_accvgpr_read_b32 v183, a243\n" \
"v_mfma_f32_16x16x32_bf16 a[48:51], v[164:167], v[120:123], a[48:51]\n" \
"ds_read_b128 v[56:59], v28 offset:2048\n" \
"ds_read_b128 v[120:123], v29 offset:2048\n" \
"s_waitcnt lgkmcnt(15)\n" \
"s_nop 0\n" \
"v_mfma_f32_16x16x32_bf16 a[0:3], v[84:87], v[68:71], a[0:3]\n" \
"s_add_u32 m0, s30, 0x9c00\n" \
"s_mov_b32 s85, 0xfe80\n" \
"v_mfma_f32_16x16x32_bf16 a[0:3], v[148:151], v[132:135], a[0:3]\n" \
"buffer_load_dwordx4 v25, s[76:79], s85 offen lds\n" \
"v_mfma_f32_16x16x32_bf16 a[4:7], v[88:91], v[68:71], a[4:7]\n" \
"ds_read_b64_tr_b16 v[100:101], v36 offset:0\n" \
"v_cvt_pk_bf16_f32 v200, v200, v201\n" \
"v_mfma_f32_16x16x32_bf16 a[4:7], v[152:155], v[132:135], a[4:7]\n" \
"s_add_u32 m0, s31, 0x8c00\n" \
"s_mov_b32 s84, 0x298000\n" \
"v_mfma_f32_16x16x32_bf16 a[36:39], v[96:99], v[68:71], a[36:39]\n" \
"buffer_load_dwordx4 v27, s[68:71], s84 offen lds\n" \
"s_waitcnt lgkmcnt(13)\n" \
"v_mfma_f32_16x16x32_bf16 a[36:39], v[160:163], v[132:135], a[36:39]\n" \
"ds_read_b64_tr_b16 v[102:103], v36 offset:1024\n" \
"v_cvt_pk_bf16_f32 v201, v202, v203\n" \
"s_waitcnt lgkmcnt(10)\n" \
"v_mfma_f32_16x16x32_bf16 a[32:35], v[68:71], v[92:95], a[32:35]\n" \
"ds_read_b64_tr_b16 v[164:165], v36 offset:8192\n" \
"v_cvt_pk_bf16_f32 v202, v180, v181\n" \
"s_waitcnt lgkmcnt(7)\n" \
"s_nop 0\n" \
"v_mfma_f32_16x16x32_bf16 a[32:35], v[132:135], v[156:159], a[32:35]\n" \
"ds_read_b64_tr_b16 v[166:167], v36 offset:9216\n" \
"v_cvt_pk_bf16_f32 v203, v182, v183\n" \
"v_mfma_f32_16x16x32_bf16 a[96:99], v[72:75], v[92:95], a[96:99]\n" \
"ds_read_b64_tr_b16 v[104:105], v37 offset:0\n" \
"v_accvgpr_read_b32 v196, a156\n" \
"v_mfma_f32_16x16x32_bf16 a[96:99], v[136:139], v[156:159], a[96:99]\n" \
"ds_read_b64_tr_b16 v[106:107], v37 offset:1024\n" \
"v_accvgpr_read_b32 v197, a157\n" \
"v_mfma_f32_16x16x32_bf16 a[100:103], v[96:99], v[72:75], a[100:103]\n" \
"ds_read_b64_tr_b16 v[168:169], v37 offset:8192\n" \
"v_accvgpr_read_b32 v198, a158\n" \
"v_mfma_f32_16x16x32_bf16 a[100:103], v[160:163], v[136:139], a[100:103]\n" \
"ds_read_b64_tr_b16 v[170:171], v37 offset:9216\n" \
"v_accvgpr_read_b32 v199, a159\n" \
"v_mfma_f32_16x16x32_bf16 a[68:71], v[88:91], v[72:75], a[68:71]\n" \
"v_accvgpr_read_b32 v180, a188\n" \
"v_accvgpr_read_b32 v181, a189\n" \
"v_mfma_f32_16x16x32_bf16 a[68:71], v[152:155], v[136:139], a[68:71]\n" \
"s_add_u32 s89, s42, 0x2000\n" \
"buffer_store_dwordx4 v[232:235], v20, s[80:83], s89 offen offset:3072 sc1\n" \
"v_mfma_f32_16x16x32_bf16 a[64:67], v[84:87], v[72:75], a[64:67]\n" \
"ds_read_b64_tr_b16 v[108:109], v38 offset:0\n" \
"v_accvgpr_read_b32 v182, a190\n" \
"v_mfma_f32_16x16x32_bf16 a[64:67], v[148:151], v[136:139], a[64:67]\n" \
"ds_read_b64_tr_b16 v[110:111], v38 offset:1024\n" \
"v_accvgpr_read_b32 v183, a191\n" \
"v_mfma_f32_16x16x32_bf16 a[128:131], v[84:87], v[76:79], a[128:131]\n" \
"ds_read_b64_tr_b16 v[172:173], v38 offset:8192\n" \
"v_cvt_pk_bf16_f32 v196, v196, v197\n" \
"v_mfma_f32_16x16x32_bf16 a[128:131], v[148:151], v[140:143], a[128:131]\n" \
"ds_read_b64_tr_b16 v[174:175], v38 offset:9216\n" \
"v_cvt_pk_bf16_f32 v197, v198, v199\n" \
"v_mfma_f32_16x16x32_bf16 a[132:135], v[88:91], v[76:79], a[132:135]\n" \
"v_cvt_pk_bf16_f32 v198, v180, v181\n" \
"v_cvt_pk_bf16_f32 v199, v182, v183\n" \
"v_mfma_f32_16x16x32_bf16 a[132:135], v[152:155], v[140:143], a[132:135]\n" \
"s_add_u32 s88, s42, 0x2000\n" \
"buffer_store_dwordx4 v[236:239], v20, s[80:83], s88 offen offset:2048 sc1\n" \
"v_mfma_f32_16x16x32_bf16 a[164:167], v[96:99], v[76:79], a[164:167]\n" \
"ds_read_b64_tr_b16 v[112:113], v39 offset:0\n" \
"v_accvgpr_read_b32 v192, a152\n" \
"v_mfma_f32_16x16x32_bf16 a[164:167], v[160:163], v[140:143], a[164:167]\n" \
"ds_read_b64_tr_b16 v[114:115], v39 offset:1024\n" \
"v_accvgpr_read_b32 v193, a153\n" \
"v_mfma_f32_16x16x32_bf16 a[160:163], v[76:79], v[92:95], a[160:163]\n" \
"ds_read_b64_tr_b16 v[176:177], v39 offset:8192\n" \
"v_accvgpr_read_b32 v194, a154\n" \
"v_mfma_f32_16x16x32_bf16 a[160:163], v[140:143], v[156:159], a[160:163]\n" \
"v_accvgpr_read_b32 v195, a155\n" \
"v_accvgpr_read_b32 v180, a184\n" \
"v_mfma_f32_16x16x32_bf16 a[224:227], v[80:83], v[92:95], a[224:227]\n" \
"ds_read_b64_tr_b16 v[178:179], v39 offset:9216\n" \
"v_accvgpr_read_b32 v181, a185\n" \
"v_mfma_f32_16x16x32_bf16 a[224:227], v[144:147], v[156:159], a[224:227]\n" \
"v_accvgpr_read_b32 v182, a186\n" \
"v_accvgpr_read_b32 v183, a187\n" \
"v_mfma_f32_16x16x32_bf16 a[228:231], v[96:99], v[80:83], a[228:231]\n" \
"s_add_u32 s89, s42, 0x2000\n" \
"buffer_store_dwordx4 v[240:243], v20, s[80:83], s89 offen offset:1024 sc1\n" \
"v_mfma_f32_16x16x32_bf16 a[228:231], v[160:163], v[144:147], a[228:231]\n" \
"v_cvt_pk_bf16_f32 v192, v192, v193\n" \
"v_cvt_pk_bf16_f32 v193, v194, v195\n" \
"v_mfma_f32_16x16x32_bf16 a[196:199], v[88:91], v[80:83], a[196:199]\n" \
"v_cvt_pk_bf16_f32 v194, v180, v181\n" \
"v_cvt_pk_bf16_f32 v195, v182, v183\n" \
"v_mfma_f32_16x16x32_bf16 a[196:199], v[152:155], v[144:147], a[196:199]\n" \
"s_lshl_b32 s14, s4, 2\n" \
"s_or_b32 s8, s14, s8\n" \
"v_mfma_f32_16x16x32_bf16 a[192:195], v[84:87], v[80:83], a[192:195]\n" \
"s_mul_i32 s8, s8, 0x90000\n" \
"s_lshl_b32 s14, s3, 14\n" \
"v_mfma_f32_16x16x32_bf16 a[192:195], v[148:151], v[144:147], a[192:195]\n" \
"s_add_i32 s8, s8, s14\n" \
"s_lshl_b32 s9, s9, 4\n" \
"v_mfma_f32_16x16x32_bf16 a[8:11], v[88:91], v[64:67], a[8:11]\n" \
"s_lshl_b32 s14, s4, 7\n" \
"s_or_b32 s18, s9, s14\n" \
"v_mfma_f32_16x16x32_bf16 a[8:11], v[152:155], v[128:131], a[8:11]\n" \
"s_sub_i32 s9, s9, s14\n" \
"s_mulk_i32 s18, 0x480\n" \
"v_mfma_f32_16x16x32_bf16 a[40:43], v[96:99], v[64:67], a[40:43]\n" \
"s_mulk_i32 s9, 0x480\n" \
"s_or_b32 s14, s18, s12\n" \
"v_mfma_f32_16x16x32_bf16 a[40:43], v[160:163], v[128:131], a[40:43]\n" \
"s_or_b32 s9, s9, s12\n" \
"s_and_b32 s12, s2, 1\n" \
"v_mfma_f32_16x16x32_bf16 a[12:15], v[64:67], v[92:95], a[12:15]\n" \
"s_cmp_lg_u32 s12, 0\n" \
"s_cselect_b32 s13, s16, s13\n" \
"v_mfma_f32_16x16x32_bf16 a[12:15], v[128:131], v[156:159], a[12:15]\n" \
"s_waitcnt vmcnt(3) lgkmcnt(0)\n" \
"s_barrier\n" \
"v_mfma_f32_16x16x32_bf16 a[144:147], v[100:103], v[68:71], a[144:147]\n" \
"s_add_u32 m0, s40, 0x0\n" \
"s_add_u32 s84, s39, 0xc000\n" \
"v_mfma_f32_16x16x32_bf16 a[144:147], v[164:167], v[132:135], a[144:147]\n" \
"buffer_load_dwordx4 v20, s[80:83], s84 offen sc1 lds\n" \
"v_mfma_f32_16x16x32_bf16 a[148:151], v[104:107], v[68:71], a[148:151]\n" \
"ds_read_b64_tr_b16 v[84:85], v32 offset:32768\n" \
"s_bfe_u32 s16, s2, 0x20001\n" \
"v_mfma_f32_16x16x32_bf16 a[148:151], v[168:171], v[132:135], a[148:151]\n" \
"ds_read_b64_tr_b16 v[86:87], v32 offset:33280\n" \
"s_addk_i32 s13, 0x4000\n" \
"v_mfma_f32_16x16x32_bf16 a[200:203], v[108:111], v[68:71], a[200:203]\n" \
"s_add_u32 m0, s40, 0x1000\n" \
"s_add_u32 s85, s39, 0xd000\n" \
"v_mfma_f32_16x16x32_bf16 a[200:203], v[172:175], v[132:135], a[200:203]\n" \
"buffer_load_dwordx4 v20, s[80:83], s85 offen sc1 lds\n" \
"v_mfma_f32_16x16x32_bf16 a[204:207], v[112:115], v[68:71], a[204:207]\n" \
"ds_read_b64_tr_b16 v[148:149], v32 offset:36864\n" \
"s_xor_b32 s16, s16, s3\n" \
"v_mfma_f32_16x16x32_bf16 a[204:207], v[176:179], v[132:135], a[204:207]\n" \
"ds_read_b128 v[68:71], v28 offset:40960\n" \
"v_mfma_f32_16x16x32_bf16 a[236:239], v[112:115], v[72:75], a[236:239]\n" \
"ds_read_b128 v[132:135], v29 offset:40960\n" \
"v_mfma_f32_16x16x32_bf16 a[236:239], v[176:179], v[136:139], a[236:239]\n" \
"s_add_u32 m0, s40, 0x3000\n" \
"s_mov_b32 s84, 0x300\n" \
"v_mfma_f32_16x16x32_bf16 a[232:235], v[108:111], v[72:75], a[232:235]\n" \
"buffer_load_dwordx4 v21, s[76:79], s84 offen lds\n" \
"v_mfma_f32_16x16x32_bf16 a[232:235], v[172:175], v[136:139], a[232:235]\n" \
"ds_read_b64_tr_b16 v[150:151], v32 offset:37376\n" \
"v_mfma_f32_16x16x32_bf16 a[180:183], v[104:107], v[72:75], a[180:183]\n" \
"s_add_u32 m0, s30, 0x0\n" \
"s_nop 0\n" \
"s_mov_b32 s85, 0x300\n" \
"v_mfma_f32_16x16x32_bf16 a[180:183], v[168:171], v[136:139], a[180:183]\n" \
"buffer_load_dwordx4 v24, s[76:79], s85 offen lds\n" \
"v_mfma_f32_16x16x32_bf16 a[176:179], v[100:103], v[72:75], a[176:179]\n" \
"ds_read_b64_tr_b16 v[88:89], v33 offset:32768\n" \
"v_mfma_f32_16x16x32_bf16 a[176:179], v[164:167], v[136:139], a[176:179]\n" \
"ds_read_b128 v[72:75], v28 offset:43008\n" \
"v_mfma_f32_16x16x32_bf16 a[72:75], v[100:103], v[76:79], a[72:75]\n" \
"ds_read_b128 v[136:139], v29 offset:43008\n" \
"v_mfma_f32_16x16x32_bf16 a[72:75], v[164:167], v[140:143], a[72:75]\n" \
"s_add_u32 m0, s30, 0x400\n" \
"s_mov_b32 s84, 0x2700\n" \
"v_mfma_f32_16x16x32_bf16 a[76:79], v[104:107], v[76:79], a[76:79]\n" \
"buffer_load_dwordx4 v25, s[76:79], s84 offen lds\n" \
"v_mfma_f32_16x16x32_bf16 a[76:79], v[168:171], v[140:143], a[76:79]\n" \
"ds_read_b64_tr_b16 v[90:91], v33 offset:33280\n" \
"v_mfma_f32_16x16x32_bf16 a[104:107], v[108:111], v[76:79], a[104:107]\n" \
"ds_read_b64_tr_b16 v[152:153], v33 offset:36864\n" \
"v_mfma_f32_16x16x32_bf16 a[104:107], v[172:175], v[140:143], a[104:107]\n" \
"s_add_u32 m0, s31, 0x0\n" \
"s_nop 0\n" \
"s_mov_b32 s85, 0x300000\n" \
"v_mfma_f32_16x16x32_bf16 a[108:111], v[112:115], v[76:79], a[108:111]\n" \
"buffer_load_dwordx4 v26, s[68:71], s85 offen lds\n" \
"v_mfma_f32_16x16x32_bf16 a[108:111], v[176:179], v[140:143], a[108:111]\n" \
"ds_read_b128 v[76:79], v28 offset:45056\n" \
"v_mfma_f32_16x16x32_bf16 a[172:175], v[112:115], v[80:83], a[172:175]\n" \
"ds_read_b128 v[140:143], v29 offset:45056\n" \
"v_mfma_f32_16x16x32_bf16 a[172:175], v[176:179], v[144:147], a[172:175]\n" \
"s_add_u32 m0, s30, 0x800\n" \
"s_mov_b32 s84, 0x4b00\n" \
"v_mfma_f32_16x16x32_bf16 a[168:171], v[108:111], v[80:83], a[168:171]\n" \
"buffer_load_dwordx4 v24, s[76:79], s84 offen lds\n" \
"v_mfma_f32_16x16x32_bf16 a[168:171], v[172:175], v[144:147], a[168:171]\n" \
"ds_read_b64_tr_b16 v[154:155], v33 offset:37376\n" \
"v_mfma_f32_16x16x32_bf16 a[140:143], v[104:107], v[80:83], a[140:143]\n" \
"ds_read_b64_tr_b16 v[96:97], v35 offset:32768\n" \
"v_mfma_f32_16x16x32_bf16 a[140:143], v[168:171], v[144:147], a[140:143]\n" \
"ds_read_b64_tr_b16 v[98:99], v35 offset:33280\n" \
"v_mfma_f32_16x16x32_bf16 a[136:139], v[100:103], v[80:83], a[136:139]\n" \
"s_cmp_lg_u32 s16, 0\n" \
"s_cselect_b32 s13, s7, s13\n" \
"v_mfma_f32_16x16x32_bf16 a[136:139], v[164:167], v[144:147], a[136:139]\n" \
"ds_read_b128 v[80:83], v28 offset:47104\n" \
"s_lshl_b32 s6, s6, 16\n" \
"s_nop 0\n" \
"v_mfma_f32_16x16x32_bf16 a[80:83], v[100:103], v[60:63], a[80:83]\n" \
"ds_read_b128 v[144:147], v29 offset:47104\n" \
"v_writelane_b32 v11, s13, 39\n" \
"v_mfma_f32_16x16x32_bf16 a[80:83], v[164:167], v[124:127], a[80:83]\n" \
"s_add_u32 m0, s30, 0xc00\n" \
"s_mov_b32 s85, 0x6f00\n" \
"v_mfma_f32_16x16x32_bf16 a[84:87], v[104:107], v[60:63], a[84:87]\n" \
"buffer_load_dwordx4 v25, s[76:79], s85 offen lds\n" \
"v_mfma_f32_16x16x32_bf16 a[84:87], v[168:171], v[124:127], a[84:87]\n" \
"s_add_u32 m0, s31, 0x400\n" \
"s_mov_b32 s84, 0x308000\n" \
"v_mfma_f32_16x16x32_bf16 a[88:91], v[108:111], v[60:63], a[88:91]\n" \
"buffer_load_dwordx4 v26, s[68:71], s84 offen lds\n" \
"v_mfma_f32_16x16x32_bf16 a[88:91], v[172:175], v[124:127], a[88:91]\n" \
"ds_read_b64_tr_b16 v[160:161], v35 offset:36864\n" \
"s_add_i32 s13, s8, 0x4240000\n" \
"v_mfma_f32_16x16x32_bf16 a[92:95], v[112:115], v[60:63], a[92:95]\n" \
"ds_read_b64_tr_b16 v[162:163], v35 offset:37376\n" \
"s_add_i32 s6, s13, s6\n" \
"s_nop 0\n" \
"v_mfma_f32_16x16x32_bf16 a[92:95], v[176:179], v[124:127], a[92:95]\n" \
"ds_read_b128 v[60:63], v28 offset:36864\n" \
"v_writelane_b32 v11, s6, 40\n" \
"v_mfma_f32_16x16x32_bf16 a[124:127], v[112:115], v[64:67], a[124:127]\n" \
"ds_read_b128 v[124:127], v29 offset:36864\n" \
"s_add_i32 s8, s8, 0x42c0000\n" \
"v_mfma_f32_16x16x32_bf16 a[124:127], v[176:179], v[128:131], a[124:127]\n" \
"s_add_u32 m0, s30, 0x1000\n" \
"s_mov_b32 s85, 0x9300\n" \
"v_mfma_f32_16x16x32_bf16 a[120:123], v[108:111], v[64:67], a[120:123]\n" \
"buffer_load_dwordx4 v24, s[76:79], s85 offen lds\n" \
"v_mfma_f32_16x16x32_bf16 a[120:123], v[172:175], v[128:131], a[120:123]\n" \
"s_add_u32 m0, s30, 0x1400\n" \
"s_mov_b32 s84, 0xb700\n" \
"v_mfma_f32_16x16x32_bf16 a[116:119], v[104:107], v[64:67], a[116:119]\n" \
"buffer_load_dwordx4 v25, s[76:79], s84 offen lds\n" \
"v_mfma_f32_16x16x32_bf16 a[116:119], v[168:171], v[128:131], a[116:119]\n" \
"ds_read_b64_tr_b16 v[92:93], v34 offset:32768\n" \
"s_lshl_b32 s6, s12, 10\n" \
"v_mfma_f32_16x16x32_bf16 a[112:115], v[100:103], v[64:67], a[112:115]\n" \
"ds_read_b64_tr_b16 v[94:95], v34 offset:33280\n" \
"s_or_b32 s6, s8, s6\n" \
"v_mfma_f32_16x16x32_bf16 a[112:115], v[164:167], v[128:131], a[112:115]\n" \
"ds_read_b128 v[64:67], v28 offset:38912\n" \
"v_mfma_f32_16x16x32_bf16 a[16:19], v[100:103], v[52:55], a[16:19]\n" \
"ds_read_b128 v[128:131], v29 offset:38912\n" \
"v_mfma_f32_16x16x32_bf16 a[16:19], v[164:167], v[116:119], a[16:19]\n" \
"s_add_u32 m0, s31, 0x800\n" \
"s_mov_b32 s85, 0x310000\n" \
"v_mfma_f32_16x16x32_bf16 a[20:23], v[104:107], v[52:55], a[20:23]\n" \
"buffer_load_dwordx4 v27, s[68:71], s85 offen lds\n" \
"v_mfma_f32_16x16x32_bf16 a[20:23], v[168:171], v[116:119], a[20:23]\n" \
"s_add_u32 m0, s30, 0x1800\n" \
"s_mov_b32 s84, 0xdb00\n" \
"v_mfma_f32_16x16x32_bf16 a[24:27], v[108:111], v[52:55], a[24:27]\n" \
"buffer_load_dwordx4 v24, s[76:79], s84 offen lds\n" \
"v_mfma_f32_16x16x32_bf16 a[24:27], v[172:175], v[116:119], a[24:27]\n" \
"ds_read_b64_tr_b16 v[156:157], v34 offset:36864\n" \
"v_mfma_f32_16x16x32_bf16 a[28:31], v[112:115], v[52:55], a[28:31]\n" \
"ds_read_b64_tr_b16 v[158:159], v34 offset:37376\n" \
"v_mfma_f32_16x16x32_bf16 a[28:31], v[176:179], v[116:119], a[28:31]\n" \
"ds_read_b128 v[52:55], v28 offset:32768\n" \
"v_mfma_f32_16x16x32_bf16 a[60:63], v[112:115], v[56:59], a[60:63]\n" \
"ds_read_b128 v[116:119], v29 offset:32768\n" \
"v_mfma_f32_16x16x32_bf16 a[60:63], v[176:179], v[120:123], a[60:63]\n" \
"s_cmp_lg_u32 s16, 0\n" \
"s_cselect_b32 s6, s7, s6\n" \
"v_mfma_f32_16x16x32_bf16 a[56:59], v[108:111], v[56:59], a[56:59]\n" \
"s_mulk_i32 s4, 0x4800\n" \
"v_writelane_b32 v11, s6, 41\n" \
"v_mfma_f32_16x16x32_bf16 a[56:59], v[172:175], v[120:123], a[56:59]\n" \
"s_add_i32 s6, s14, s10\n" \
"v_writelane_b32 v11, s6, 42\n" \
"v_mfma_f32_16x16x32_bf16 a[52:55], v[104:107], v[56:59], a[52:55]\n" \
"s_add_i32 s6, s9, 0x21c000\n" \
"s_add_i32 s9, s6, s10\n" \
"v_mfma_f32_16x16x32_bf16 a[52:55], v[168:171], v[120:123], a[52:55]\n" \
"s_cmp_lg_u32 s12, 0\n" \
"s_cselect_b32 s6, s6, s14\n" \
"v_mfma_f32_16x16x32_bf16 a[48:51], v[100:103], v[56:59], a[48:51]\n" \
"s_addk_i32 s6, 0x100\n" \
"v_writelane_b32 v11, s9, 43\n" \
"v_mfma_f32_16x16x32_bf16 a[48:51], v[164:167], v[120:123], a[48:51]\n" \
"ds_read_b128 v[56:59], v28 offset:34816\n" \
"ds_read_b128 v[120:123], v29 offset:34816\n" \
"s_waitcnt lgkmcnt(15)\n" \
"s_nop 0\n" \
"v_mfma_f32_16x16x32_bf16 a[0:3], v[84:87], v[68:71], a[0:3]\n" \
"s_add_u32 m0, s30, 0x1c00\n" \
"s_mov_b32 s84, 0xff00\n" \
"v_mfma_f32_16x16x32_bf16 a[0:3], v[148:151], v[132:135], a[0:3]\n" \
"buffer_load_dwordx4 v25, s[76:79], s84 offen lds\n" \
"v_mfma_f32_16x16x32_bf16 a[4:7], v[88:91], v[68:71], a[4:7]\n" \
"ds_read_b64_tr_b16 v[100:101], v36 offset:32768\n" \
"v_mfma_f32_16x16x32_bf16 a[4:7], v[152:155], v[132:135], a[4:7]\n" \
"s_add_u32 m0, s31, 0xc00\n" \
"s_mov_b32 s85, 0x318000\n" \
"v_mfma_f32_16x16x32_bf16 a[36:39], v[96:99], v[68:71], a[36:39]\n" \
"buffer_load_dwordx4 v27, s[68:71], s85 offen lds\n" \
"s_waitcnt lgkmcnt(13)\n" \
"v_mfma_f32_16x16x32_bf16 a[36:39], v[160:163], v[132:135], a[36:39]\n" \
"ds_read_b64_tr_b16 v[102:103], v36 offset:33792\n" \
"s_waitcnt lgkmcnt(10)\n" \
"v_mfma_f32_16x16x32_bf16 a[32:35], v[68:71], v[92:95], a[32:35]\n" \
"ds_read_b64_tr_b16 v[164:165], v36 offset:40960\n" \
"s_waitcnt lgkmcnt(7)\n" \
"s_nop 0\n" \
"v_mfma_f32_16x16x32_bf16 a[32:35], v[132:135], v[156:159], a[32:35]\n" \
"ds_read_b64_tr_b16 v[166:167], v36 offset:41984\n" \
"v_mfma_f32_16x16x32_bf16 a[96:99], v[72:75], v[92:95], a[96:99]\n" \
"ds_read_b64_tr_b16 v[104:105], v37 offset:32768\n" \
"v_mfma_f32_16x16x32_bf16 a[96:99], v[136:139], v[156:159], a[96:99]\n" \
"ds_read_b64_tr_b16 v[106:107], v37 offset:33792\n" \
"v_mfma_f32_16x16x32_bf16 a[100:103], v[96:99], v[72:75], a[100:103]\n" \
"ds_read_b64_tr_b16 v[168:169], v37 offset:40960\n" \
"v_mfma_f32_16x16x32_bf16 a[100:103], v[160:163], v[136:139], a[100:103]\n" \
"ds_read_b64_tr_b16 v[170:171], v37 offset:41984\n" \
"v_mfma_f32_16x16x32_bf16 a[68:71], v[88:91], v[72:75], a[68:71]\n" \
"s_cmp_lg_u32 s16, 0\n" \
"s_cselect_b32 s6, s7, s6\n" \
"v_mfma_f32_16x16x32_bf16 a[68:71], v[152:155], v[136:139], a[68:71]\n" \
"s_add_u32 s88, s42, 0x2000\n" \
"buffer_store_dwordx4 v[244:247], v20, s[80:83], s88 offen offset:0 sc1\n" \
"v_mfma_f32_16x16x32_bf16 a[64:67], v[84:87], v[72:75], a[64:67]\n" \
"ds_read_b64_tr_b16 v[108:109], v38 offset:32768\n" \
"v_writelane_b32 v11, s6, 44\n" \
"v_mfma_f32_16x16x32_bf16 a[64:67], v[148:151], v[136:139], a[64:67]\n" \
"ds_read_b64_tr_b16 v[110:111], v38 offset:33792\n" \
"s_or_b32 s6, s8, s15\n" \
"v_mfma_f32_16x16x32_bf16 a[128:131], v[84:87], v[76:79], a[128:131]\n" \
"ds_read_b64_tr_b16 v[172:173], v38 offset:40960\n" \
"v_writelane_b32 v11, s6, 52\n" \
"v_mfma_f32_16x16x32_bf16 a[128:131], v[148:151], v[140:143], a[128:131]\n" \
"ds_read_b64_tr_b16 v[174:175], v38 offset:41984\n" \
"s_lshl_b32 s6, s3, 17\n" \
"v_mfma_f32_16x16x32_bf16 a[132:135], v[88:91], v[76:79], a[132:135]\n" \
"s_add_i32 s6, s13, s6\n" \
"s_or_b32 s5, s6, s5\n" \
"v_mfma_f32_16x16x32_bf16 a[132:135], v[152:155], v[140:143], a[132:135]\n" \
"s_add_u32 s89, s42, 0x1000\n" \
"buffer_store_dwordx4 v[184:187], v20, s[80:83], s89 offen offset:0 sc1\n" \
"v_mfma_f32_16x16x32_bf16 a[164:167], v[96:99], v[76:79], a[164:167]\n" \
"ds_read_b64_tr_b16 v[112:113], v39 offset:32768\n" \
"s_or_b32 s5, s5, 0x3840\n" \
"v_mfma_f32_16x16x32_bf16 a[164:167], v[160:163], v[140:143], a[164:167]\n" \
"ds_read_b64_tr_b16 v[114:115], v39 offset:33792\n" \
"v_writelane_b32 v11, s5, 6\n" \
"v_mfma_f32_16x16x32_bf16 a[160:163], v[76:79], v[92:95], a[160:163]\n" \
"ds_read_b64_tr_b16 v[176:177], v39 offset:40960\n" \
"s_mov_b32 s5, 0xff0000\n" \
"v_mfma_f32_16x16x32_bf16 a[160:163], v[140:143], v[156:159], a[160:163]\n" \
"s_cmp_lg_u32 s16, 0\n" \
"s_cselect_b32 s5, s17, s5\n" \
"v_mfma_f32_16x16x32_bf16 a[224:227], v[80:83], v[92:95], a[224:227]\n" \
"ds_read_b64_tr_b16 v[178:179], v39 offset:41984\n" \
"v_writelane_b32 v11, s5, 7\n" \
"v_mfma_f32_16x16x32_bf16 a[224:227], v[144:147], v[156:159], a[224:227]\n" \
"s_mul_i32 s5, s3, 0x1200\n" \
"s_add_i32 s4, s5, s4\n" \
"v_mfma_f32_16x16x32_bf16 a[228:231], v[96:99], v[80:83], a[228:231]\n" \
"s_add_u32 s88, s42, 0x1000\n" \
"buffer_store_dwordx4 v[188:191], v20, s[80:83], s88 offen offset:1024 sc1\n" \
"v_mfma_f32_16x16x32_bf16 a[228:231], v[160:163], v[144:147], a[228:231]\n" \
"s_add_i32 s4, s4, s11\n" \
"v_writelane_b32 v11, s4, 8\n" \
"v_mfma_f32_16x16x32_bf16 a[196:199], v[88:91], v[80:83], a[196:199]\n" \
"s_lshl_b32 s4, s2, 16\n" \
"s_add_i32 s5, s4, 0x10000\n" \
"v_mfma_f32_16x16x32_bf16 a[196:199], v[152:155], v[144:147], a[196:199]\n" \
"s_and_b32 s5, s5, 0x70000\n" \
"s_add_i32 s5, s13, s5\n" \
"v_mfma_f32_16x16x32_bf16 a[192:195], v[84:87], v[80:83], a[192:195]\n" \
"v_writelane_b32 v11, s5, 45\n" \
"s_add_i32 s5, s4, 0x20000\n" \
"v_mfma_f32_16x16x32_bf16 a[192:195], v[148:151], v[144:147], a[192:195]\n" \
"s_and_b32 s5, s5, 0x70000\n" \
"s_add_i32 s5, s13, s5\n" \
"v_mfma_f32_16x16x32_bf16 a[8:11], v[88:91], v[64:67], a[8:11]\n" \
"s_bitset1_b32 s5, 11\n" \
"v_writelane_b32 v11, s5, 46\n" \
"v_mfma_f32_16x16x32_bf16 a[8:11], v[152:155], v[128:131], a[8:11]\n" \
"s_add_i32 s5, s4, 0x30000\n" \
"s_and_b32 s5, s5, 0x70000\n" \
"v_mfma_f32_16x16x32_bf16 a[40:43], v[96:99], v[64:67], a[40:43]\n" \
"s_add_i32 s5, s13, s5\n" \
"s_bitset1_b32 s5, 12\n" \
"v_mfma_f32_16x16x32_bf16 a[40:43], v[160:163], v[128:131], a[40:43]\n" \
"v_writelane_b32 v11, s5, 47\n" \
"s_and_b32 s5, s4, 0x70000\n" \
"v_mfma_f32_16x16x32_bf16 a[12:15], v[64:67], v[92:95], a[12:15]\n" \
"s_xor_b32 s5, s5, 0x40000\n" \
"s_add_i32 s5, s13, s5\n" \
"v_mfma_f32_16x16x32_bf16 a[12:15], v[128:131], v[156:159], a[12:15]\n" \
"s_waitcnt vmcnt(3) lgkmcnt(0)\n" \
"s_barrier\n" \
"s_nop 0\n" \
"v_mfma_f32_16x16x32_bf16 a[144:147], v[100:103], v[68:71], a[144:147]\n" \
"s_add_u32 m0, s40, 0x8000\n" \
"s_add_u32 s85, s39, 0xe000\n" \
"v_mfma_f32_16x16x32_bf16 a[144:147], v[164:167], v[132:135], a[144:147]\n" \
"buffer_load_dwordx4 v20, s[80:83], s85 offen sc1 lds\n" \
"v_mfma_f32_16x16x32_bf16 a[148:151], v[104:107], v[68:71], a[148:151]\n" \
"ds_read_b64_tr_b16 v[84:85], v32 offset:0\n" \
"s_or_b32 s5, s5, 0x1800\n" \
"v_mfma_f32_16x16x32_bf16 a[148:151], v[168:171], v[132:135], a[148:151]\n" \
"ds_read_b64_tr_b16 v[86:87], v32 offset:512\n" \
"v_writelane_b32 v11, s5, 48\n" \
"v_mfma_f32_16x16x32_bf16 a[200:203], v[108:111], v[68:71], a[200:203]\n" \
"s_add_u32 m0, s40, 0x9000\n" \
"s_add_u32 s84, s39, 0xf000\n" \
"v_mfma_f32_16x16x32_bf16 a[200:203], v[172:175], v[132:135], a[200:203]\n" \
"buffer_load_dwordx4 v20, s[80:83], s84 offen sc1 lds\n" \
"v_mfma_f32_16x16x32_bf16 a[204:207], v[112:115], v[68:71], a[204:207]\n" \
"ds_read_b64_tr_b16 v[148:149], v32 offset:4096\n" \
"s_add_i32 s5, s4, 0x50000\n" \
"v_mfma_f32_16x16x32_bf16 a[204:207], v[176:179], v[132:135], a[204:207]\n" \
"ds_read_b128 v[68:71], v28 offset:8192\n" \
"s_and_b32 s5, s5, 0x70000\n" \
"v_mfma_f32_16x16x32_bf16 a[236:239], v[112:115], v[72:75], a[236:239]\n" \
"ds_read_b128 v[132:135], v29 offset:8192\n" \
"s_add_i32 s5, s13, s5\n" \
"v_mfma_f32_16x16x32_bf16 a[236:239], v[176:179], v[136:139], a[236:239]\n" \
"s_add_u32 m0, s40, 0xb000\n" \
"s_mov_b32 s85, 0x380\n" \
"v_mfma_f32_16x16x32_bf16 a[232:235], v[108:111], v[72:75], a[232:235]\n" \
"buffer_load_dwordx4 v21, s[76:79], s85 offen lds\n" \
"v_mfma_f32_16x16x32_bf16 a[232:235], v[172:175], v[136:139], a[232:235]\n" \
"ds_read_b64_tr_b16 v[150:151], v32 offset:4608\n" \
"s_bitset1_b32 s5, 13\n" \
"v_mfma_f32_16x16x32_bf16 a[180:183], v[104:107], v[72:75], a[180:183]\n" \
"s_add_u32 m0, s30, 0x8000\n" \
"s_mov_b32 s84, 0x380\n" \
"v_mfma_f32_16x16x32_bf16 a[180:183], v[168:171], v[136:139], a[180:183]\n" \
"buffer_load_dwordx4 v24, s[76:79], s84 offen lds\n" \
"v_mfma_f32_16x16x32_bf16 a[176:179], v[100:103], v[72:75], a[176:179]\n" \
"ds_read_b64_tr_b16 v[88:89], v33 offset:0\n" \
"v_writelane_b32 v11, s5, 49\n" \
"v_mfma_f32_16x16x32_bf16 a[176:179], v[164:167], v[136:139], a[176:179]\n" \
"ds_read_b128 v[72:75], v28 offset:10240\n" \
"s_add_i32 s5, s4, 0x60000\n" \
"v_mfma_f32_16x16x32_bf16 a[72:75], v[100:103], v[76:79], a[72:75]\n" \
"ds_read_b128 v[136:139], v29 offset:10240\n" \
"s_and_b32 s5, s5, 0x70000\n" \
"v_mfma_f32_16x16x32_bf16 a[72:75], v[164:167], v[140:143], a[72:75]\n" \
"s_add_u32 m0, s30, 0x8400\n" \
"s_mov_b32 s85, 0x2780\n" \
"v_mfma_f32_16x16x32_bf16 a[76:79], v[104:107], v[76:79], a[76:79]\n" \
"buffer_load_dwordx4 v25, s[76:79], s85 offen lds\n" \
"v_mfma_f32_16x16x32_bf16 a[76:79], v[168:171], v[140:143], a[76:79]\n" \
"ds_read_b64_tr_b16 v[90:91], v33 offset:512\n" \
"s_add_i32 s4, s4, 0x70000\n" \
"v_mfma_f32_16x16x32_bf16 a[104:107], v[108:111], v[76:79], a[104:107]\n" \
"ds_read_b64_tr_b16 v[152:153], v33 offset:4096\n" \
"s_add_i32 s5, s13, s5\n" \
"s_nop 0\n" \
"v_mfma_f32_16x16x32_bf16 a[104:107], v[172:175], v[140:143], a[104:107]\n" \
"s_add_u32 m0, s31, 0x8000\n" \
"s_mov_b32 s84, 0x380000\n" \
"v_mfma_f32_16x16x32_bf16 a[108:111], v[112:115], v[76:79], a[108:111]\n" \
"buffer_load_dwordx4 v26, s[68:71], s84 offen lds\n" \
"v_mfma_f32_16x16x32_bf16 a[108:111], v[176:179], v[140:143], a[108:111]\n" \
"ds_read_b128 v[76:79], v28 offset:12288\n" \
"s_and_b32 s4, s4, 0x70000\n" \
"v_mfma_f32_16x16x32_bf16 a[172:175], v[112:115], v[80:83], a[172:175]\n" \
"ds_read_b128 v[140:143], v29 offset:12288\n" \
"s_or_b32 s5, s5, 0x2800\n" \
"v_mfma_f32_16x16x32_bf16 a[172:175], v[176:179], v[144:147], a[172:175]\n" \
"s_add_u32 m0, s30, 0x8800\n" \
"s_mov_b32 s85, 0x4b80\n" \
"v_mfma_f32_16x16x32_bf16 a[168:171], v[108:111], v[80:83], a[168:171]\n" \
"buffer_load_dwordx4 v24, s[76:79], s85 offen lds\n" \
"v_mfma_f32_16x16x32_bf16 a[168:171], v[172:175], v[144:147], a[168:171]\n" \
"ds_read_b64_tr_b16 v[154:155], v33 offset:4608\n" \
"v_writelane_b32 v11, s5, 50\n" \
"v_mfma_f32_16x16x32_bf16 a[140:143], v[104:107], v[80:83], a[140:143]\n" \
"ds_read_b64_tr_b16 v[96:97], v35 offset:0\n" \
"s_add_i32 s13, s13, s4\n" \
"v_mfma_f32_16x16x32_bf16 a[140:143], v[168:171], v[144:147], a[140:143]\n" \
"ds_read_b64_tr_b16 v[98:99], v35 offset:512\n" \
"s_or_b32 s4, s13, 0x3000\n" \
"v_mfma_f32_16x16x32_bf16 a[136:139], v[100:103], v[80:83], a[136:139]\n" \
"v_writelane_b32 v11, s4, 51\n" \
"s_movk_i32 s11, 0x4000\n" \
"v_mfma_f32_16x16x32_bf16 a[136:139], v[164:167], v[144:147], a[136:139]\n" \
"ds_read_b128 v[80:83], v28 offset:14336\n" \
"s_lshl_b32 s4, s2, 2\n" \
"v_mfma_f32_16x16x32_bf16 a[80:83], v[100:103], v[60:63], a[80:83]\n" \
"ds_read_b128 v[144:147], v29 offset:14336\n" \
"s_and_b32 s4, s4, 28\n" \
"v_mfma_f32_16x16x32_bf16 a[80:83], v[164:167], v[124:127], a[80:83]\n" \
"s_add_u32 m0, s30, 0x8c00\n" \
"s_mov_b32 s84, 0x6f80\n" \
"v_mfma_f32_16x16x32_bf16 a[84:87], v[104:107], v[60:63], a[84:87]\n" \
"buffer_load_dwordx4 v25, s[76:79], s84 offen lds\n" \
"v_mfma_f32_16x16x32_bf16 a[84:87], v[168:171], v[124:127], a[84:87]\n" \
"s_add_u32 m0, s31, 0x8400\n" \
"s_mov_b32 s85, 0x388000\n" \
"v_mfma_f32_16x16x32_bf16 a[88:91], v[108:111], v[60:63], a[88:91]\n" \
"buffer_load_dwordx4 v26, s[68:71], s85 offen lds\n" \
"v_mfma_f32_16x16x32_bf16 a[88:91], v[172:175], v[124:127], a[88:91]\n" \
"ds_read_b64_tr_b16 v[160:161], v35 offset:4096\n" \
"s_bfe_u32 s5, s2, 0x20003\n" \
"v_mfma_f32_16x16x32_bf16 a[92:95], v[112:115], v[60:63], a[92:95]\n" \
"ds_read_b64_tr_b16 v[162:163], v35 offset:4608\n" \
"s_or_b32 s4, s4, s5\n" \
"v_mfma_f32_16x16x32_bf16 a[92:95], v[176:179], v[124:127], a[92:95]\n" \
"ds_read_b128 v[60:63], v28 offset:4096\n" \
"s_lshr_b32 s5, s2, 5\n" \
"v_mfma_f32_16x16x32_bf16 a[124:127], v[112:115], v[64:67], a[124:127]\n" \
"ds_read_b128 v[124:127], v29 offset:4096\n" \
"s_lshr_b32 s19, s3, 1\n" \
"s_nop 0\n" \
"v_mfma_f32_16x16x32_bf16 a[124:127], v[176:179], v[128:131], a[124:127]\n" \
"s_add_u32 m0, s30, 0x9000\n" \
"s_mov_b32 s84, 0x9380\n" \
"v_mfma_f32_16x16x32_bf16 a[120:123], v[108:111], v[64:67], a[120:123]\n" \
"buffer_load_dwordx4 v24, s[76:79], s84 offen lds\n" \
"v_mfma_f32_16x16x32_bf16 a[120:123], v[172:175], v[128:131], a[120:123]\n" \
"s_add_u32 m0, s30, 0x9400\n" \
"s_mov_b32 s85, 0xb780\n" \
"v_mfma_f32_16x16x32_bf16 a[116:119], v[104:107], v[64:67], a[116:119]\n" \
"buffer_load_dwordx4 v25, s[76:79], s85 offen lds\n" \
"v_mfma_f32_16x16x32_bf16 a[116:119], v[168:171], v[128:131], a[116:119]\n" \
"ds_read_b64_tr_b16 v[92:93], v34 offset:0\n" \
"s_lshl_b32 s18, s5, 1\n" \
"v_mfma_f32_16x16x32_bf16 a[112:115], v[100:103], v[64:67], a[112:115]\n" \
"ds_read_b64_tr_b16 v[94:95], v34 offset:512\n" \
"s_lshl_b32 s6, s4, 20\n" \
"v_mfma_f32_16x16x32_bf16 a[112:115], v[164:167], v[128:131], a[112:115]\n" \
"ds_read_b128 v[64:67], v28 offset:6144\n" \
"s_lshl_b32 s7, s4, 8\n" \
"v_mfma_f32_16x16x32_bf16 a[16:19], v[100:103], v[52:55], a[16:19]\n" \
"ds_read_b128 v[128:131], v29 offset:6144\n" \
"s_and_b32 s13, s3, 1\n" \
"v_mfma_f32_16x16x32_bf16 a[16:19], v[164:167], v[116:119], a[16:19]\n" \
"s_add_u32 m0, s31, 0x8800\n" \
"s_mov_b32 s84, 0x390000\n" \
"v_mfma_f32_16x16x32_bf16 a[20:23], v[104:107], v[52:55], a[20:23]\n" \
"buffer_load_dwordx4 v27, s[68:71], s84 offen lds\n" \
"v_mfma_f32_16x16x32_bf16 a[20:23], v[168:171], v[116:119], a[20:23]\n" \
"s_add_u32 m0, s30, 0x9800\n" \
"s_mov_b32 s85, 0xdb80\n" \
"v_mfma_f32_16x16x32_bf16 a[24:27], v[108:111], v[52:55], a[24:27]\n" \
"buffer_load_dwordx4 v24, s[76:79], s85 offen lds\n" \
"v_mfma_f32_16x16x32_bf16 a[24:27], v[172:175], v[116:119], a[24:27]\n" \
"ds_read_b64_tr_b16 v[156:157], v34 offset:4096\n" \
"s_add_i32 s14, s18, s19\n" \
"v_mfma_f32_16x16x32_bf16 a[28:31], v[112:115], v[52:55], a[28:31]\n" \
"ds_read_b64_tr_b16 v[158:159], v34 offset:4608\n" \
"v_mfma_f32_16x16x32_bf16 a[28:31], v[176:179], v[116:119], a[28:31]\n" \
"ds_read_b128 v[52:55], v28 offset:0\n" \
"v_mfma_f32_16x16x32_bf16 a[60:63], v[112:115], v[56:59], a[60:63]\n" \
"ds_read_b128 v[116:119], v29 offset:0\n" \
"v_mfma_f32_16x16x32_bf16 a[60:63], v[176:179], v[120:123], a[60:63]\n" \
"s_add_u32 s6, s26, s6\n" \
"s_addc_u32 s9, s27, 0\n" \
"v_mfma_f32_16x16x32_bf16 a[56:59], v[108:111], v[56:59], a[56:59]\n" \
"s_add_u32 s8, s6, 0x1000000\n" \
"s_addc_u32 s6, s9, 0\n" \
"v_mfma_f32_16x16x32_bf16 a[56:59], v[172:175], v[120:123], a[56:59]\n" \
"s_and_b32 s9, s6, 0xffff\n" \
"s_lshl_b32 s6, s5, 16\n" \
"v_mfma_f32_16x16x32_bf16 a[52:55], v[104:107], v[56:59], a[52:55]\n" \
"s_sub_i32 s10, 0x100000, s6\n" \
"s_mov_b32 s6, 0x8000\n" \
"v_mfma_f32_16x16x32_bf16 a[52:55], v[168:171], v[120:123], a[52:55]\n" \
"s_cmp_lg_u32 s5, 0\n" \
"s_cselect_b32 s17, s11, s6\n" \
"v_mfma_f32_16x16x32_bf16 a[48:51], v[100:103], v[56:59], a[48:51]\n" \
"s_lshl_b32 s6, s3, 1\n" \
"s_lshl_b32 s5, s5, 3\n" \
"v_mfma_f32_16x16x32_bf16 a[48:51], v[164:167], v[120:123], a[48:51]\n" \
"ds_read_b128 v[56:59], v28 offset:2048\n" \
"ds_read_b128 v[120:123], v29 offset:2048\n" \
"s_waitcnt lgkmcnt(15)\n" \
"v_mfma_f32_16x16x32_bf16 a[0:3], v[84:87], v[68:71], a[0:3]\n" \
"s_add_u32 m0, s30, 0x9c00\n" \
"s_mov_b32 s85, 0xff80\n" \
"v_mfma_f32_16x16x32_bf16 a[0:3], v[148:151], v[132:135], a[0:3]\n" \
"buffer_load_dwordx4 v25, s[76:79], s85 offen lds\n" \
"v_mfma_f32_16x16x32_bf16 a[4:7], v[88:91], v[68:71], a[4:7]\n" \
"ds_read_b64_tr_b16 v[100:101], v36 offset:0\n" \
"s_and_b32 s6, s6, 4\n" \
"v_mfma_f32_16x16x32_bf16 a[4:7], v[152:155], v[132:135], a[4:7]\n" \
"s_add_u32 m0, s31, 0x8c00\n" \
"s_mov_b32 s84, 0x398000\n" \
"v_mfma_f32_16x16x32_bf16 a[36:39], v[96:99], v[68:71], a[36:39]\n" \
"buffer_load_dwordx4 v27, s[68:71], s84 offen lds\n" \
"s_waitcnt lgkmcnt(13)\n" \
"v_mfma_f32_16x16x32_bf16 a[36:39], v[160:163], v[132:135], a[36:39]\n" \
"ds_read_b64_tr_b16 v[102:103], v36 offset:1024\n" \
"s_or_b32 s12, s5, s6\n" \
"s_waitcnt lgkmcnt(10)\n" \
"v_mfma_f32_16x16x32_bf16 a[32:35], v[68:71], v[92:95], a[32:35]\n" \
"ds_read_b64_tr_b16 v[164:165], v36 offset:8192\n" \
"s_sub_i32 s5, s13, s5\n" \
"s_waitcnt lgkmcnt(7)\n" \
"v_mfma_f32_16x16x32_bf16 a[32:35], v[132:135], v[156:159], a[32:35]\n" \
"ds_read_b64_tr_b16 v[166:167], v36 offset:9216\n" \
"s_or_b32 s5, s5, s6\n" \
"v_mfma_f32_16x16x32_bf16 a[96:99], v[72:75], v[92:95], a[96:99]\n" \
"ds_read_b64_tr_b16 v[104:105], v37 offset:0\n" \
"s_or_b32 s6, s12, s13\n" \
"v_mfma_f32_16x16x32_bf16 a[96:99], v[136:139], v[156:159], a[96:99]\n" \
"ds_read_b64_tr_b16 v[106:107], v37 offset:1024\n" \
"s_lshl_b32 s4, s4, 2\n" \
"v_mfma_f32_16x16x32_bf16 a[100:103], v[96:99], v[72:75], a[100:103]\n" \
"ds_read_b64_tr_b16 v[168:169], v37 offset:8192\n" \
"s_mulk_i32 s6, 0x92\n" \
"v_mfma_f32_16x16x32_bf16 a[100:103], v[160:163], v[136:139], a[100:103]\n" \
"ds_read_b64_tr_b16 v[170:171], v37 offset:9216\n" \
"s_add_i32 s4, s4, 18\n" \
"v_mfma_f32_16x16x32_bf16 a[68:71], v[88:91], v[72:75], a[68:71]\n" \
"s_mulk_i32 s5, 0x92\n" \
"s_add_i32 s6, s4, s6\n" \
"v_mfma_f32_16x16x32_bf16 a[68:71], v[152:155], v[136:139], a[68:71]\n" \
"s_add_u32 s89, s42, 0x1000\n" \
"buffer_store_dwordx4 v[192:195], v20, s[80:83], s89 offen offset:2048 sc1\n" \
"v_mfma_f32_16x16x32_bf16 a[64:67], v[84:87], v[72:75], a[64:67]\n" \
"ds_read_b64_tr_b16 v[108:109], v38 offset:0\n" \
"s_add_i32 s5, s5, s4\n" \
"v_mfma_f32_16x16x32_bf16 a[64:67], v[148:151], v[136:139], a[64:67]\n" \
"ds_read_b64_tr_b16 v[110:111], v38 offset:1024\n" \
"s_lshl_b32 s4, s3, 11\n" \
"v_mfma_f32_16x16x32_bf16 a[128:131], v[84:87], v[76:79], a[128:131]\n" \
"ds_read_b64_tr_b16 v[172:173], v38 offset:8192\n" \
"s_lshl_b32 s15, s13, 12\n" \
"v_mfma_f32_16x16x32_bf16 a[128:131], v[148:151], v[140:143], a[128:131]\n" \
"ds_read_b64_tr_b16 v[174:175], v38 offset:9216\n" \
"s_lshl_b32 s16, s19, 14\n" \
"s_nop 0\n" \
"v_mfma_f32_16x16x32_bf16 a[132:135], v[88:91], v[76:79], a[132:135]\n" \
"s_mul_i32 s19, s19, 0xe000\n" \
"s_mov_b32 s11, 0x20000\n" \
"v_mfma_f32_16x16x32_bf16 a[132:135], v[152:155], v[140:143], a[132:135]\n" \
"s_add_u32 s88, s42, 0x1000\n" \
"buffer_store_dwordx4 v[196:199], v20, s[80:83], s88 offen offset:3072 sc1\n" \
"v_mfma_f32_16x16x32_bf16 a[164:167], v[96:99], v[76:79], a[164:167]\n" \
"ds_read_b64_tr_b16 v[112:113], v39 offset:0\n" \
"s_or_b32 s12, s4, 0xc000\n" \
"v_mfma_f32_16x16x32_bf16 a[164:167], v[160:163], v[140:143], a[164:167]\n" \
"ds_read_b64_tr_b16 v[114:115], v39 offset:1024\n" \
"s_lshl_b32 s4, s3, 8\n" \
"v_mfma_f32_16x16x32_bf16 a[160:163], v[76:79], v[92:95], a[160:163]\n" \
"ds_read_b64_tr_b16 v[176:177], v39 offset:8192\n" \
"s_or_b32 s22, s15, s16\n" \
"v_mfma_f32_16x16x32_bf16 a[160:163], v[140:143], v[156:159], a[160:163]\n" \
"s_or_b32 s23, s19, s15\n" \
"s_lshl_b32 s60, s6, 10\n" \
"v_mfma_f32_16x16x32_bf16 a[224:227], v[80:83], v[92:95], a[224:227]\n" \
"ds_read_b64_tr_b16 v[178:179], v39 offset:9216\n" \
"s_lshl_b32 s61, s5, 10\n" \
"v_mfma_f32_16x16x32_bf16 a[224:227], v[144:147], v[156:159], a[224:227]\n" \
"s_lshl_b32 s14, s14, 15\n" \
"s_or_b32 s20, s4, 0x26c00\n" \
"v_mfma_f32_16x16x32_bf16 a[228:231], v[96:99], v[80:83], a[228:231]\n" \
"s_add_u32 s89, s42, 0x3000\n" \
"buffer_store_dwordx4 v[200:203], v20, s[80:83], s89 offen offset:0 sc1\n" \
"v_mfma_f32_16x16x32_bf16 a[228:231], v[160:163], v[144:147], a[228:231]\n" \
"s_or_b32 s13, s22, 0x8000\n" \
"s_add_i32 s22, s22, 0x14000\n" \
"v_mfma_f32_16x16x32_bf16 a[196:199], v[88:91], v[80:83], a[196:199]\n" \
"s_bitset1_b32 s23, 16\n" \
"s_or_b32 s24, s7, 0x5be8020\n" \
"v_mfma_f32_16x16x32_bf16 a[196:199], v[152:155], v[144:147], a[196:199]\n" \
"s_and_b32 s57, s27, 0xffff\n" \
"s_mov_b32 s58, 0x6000000\n" \
"v_mfma_f32_16x16x32_bf16 a[192:195], v[84:87], v[80:83], a[192:195]\n" \
"s_mov_b32 s56, s26\n" \
"s_mov_b32 s59, s11\n" \
"v_mfma_f32_16x16x32_bf16 a[192:195], v[148:151], v[144:147], a[192:195]\n" \
"s_add_i32 s60, s60, 0x3000000\n" \
"s_add_i32 s61, s61, 0x411c000\n" \
"v_mfma_f32_16x16x32_bf16 a[8:11], v[88:91], v[64:67], a[8:11]\n" \
"s_sub_i32 s19, 14, s18\n" \
"s_mov_b32 s21, 0x71800000\n" \
"v_mfma_f32_16x16x32_bf16 a[8:11], v[152:155], v[128:131], a[8:11]\n" \
"v_mfma_f32_16x16x32_bf16 a[40:43], v[96:99], v[64:67], a[40:43]\n" \
"v_mfma_f32_16x16x32_bf16 a[40:43], v[160:163], v[128:131], a[40:43]\n" \
"v_mfma_f32_16x16x32_bf16 a[12:15], v[64:67], v[92:95], a[12:15]\n" \
"v_mfma_f32_16x16x32_bf16 a[12:15], v[128:131], v[156:159], a[12:15]\n" \
"s_waitcnt vmcnt(3) lgkmcnt(0)\n" \
"s_barrier\n" \
"v_mfma_f32_16x16x32_bf16 a[144:147], v[100:103], v[68:71], a[144:147]\n" \
"s_add_u32 m0, s40, 0x0\n" \
"s_nop 0\n" \
"s_add_u32 s84, s39, 0x10000\n" \
"v_mfma_f32_16x16x32_bf16 a[144:147], v[164:167], v[132:135], a[144:147]\n" \
"buffer_load_dwordx4 v20, s[80:83], s84 offen sc1 lds\n" \
"v_mfma_f32_16x16x32_bf16 a[148:151], v[104:107], v[68:71], a[148:151]\n" \
"ds_read_b64_tr_b16 v[84:85], v32 offset:32768\n" \
"v_mfma_f32_16x16x32_bf16 a[148:151], v[168:171], v[132:135], a[148:151]\n" \
"ds_read_b64_tr_b16 v[86:87], v32 offset:33280\n" \
"v_mfma_f32_16x16x32_bf16 a[200:203], v[108:111], v[68:71], a[200:203]\n" \
"s_add_u32 m0, s40, 0x1000\n" \
"s_add_u32 s85, s39, 0x11000\n" \
"v_mfma_f32_16x16x32_bf16 a[200:203], v[172:175], v[132:135], a[200:203]\n" \
"buffer_load_dwordx4 v20, s[80:83], s85 offen sc1 lds\n" \
"v_mfma_f32_16x16x32_bf16 a[204:207], v[112:115], v[68:71], a[204:207]\n" \
"ds_read_b64_tr_b16 v[148:149], v32 offset:36864\n" \
"v_mfma_f32_16x16x32_bf16 a[204:207], v[176:179], v[132:135], a[204:207]\n" \
"ds_read_b128 v[68:71], v28 offset:40960\n" \
"v_mfma_f32_16x16x32_bf16 a[236:239], v[112:115], v[72:75], a[236:239]\n" \
"ds_read_b128 v[132:135], v29 offset:40960\n" \
"v_mfma_f32_16x16x32_bf16 a[236:239], v[176:179], v[136:139], a[236:239]\n" \
"s_add_u32 m0, s40, 0x3000\n" \
"s_mov_b32 s84, 0x400\n" \
"v_mfma_f32_16x16x32_bf16 a[232:235], v[108:111], v[72:75], a[232:235]\n" \
"buffer_load_dwordx4 v21, s[76:79], s84 offen lds\n" \
"v_mfma_f32_16x16x32_bf16 a[232:235], v[172:175], v[136:139], a[232:235]\n" \
"ds_read_b64_tr_b16 v[150:151], v32 offset:37376\n" \
"v_mfma_f32_16x16x32_bf16 a[180:183], v[104:107], v[72:75], a[180:183]\n" \
"s_add_u32 m0, s30, 0x0\n" \
"s_nop 0\n" \
"s_mov_b32 s85, 0x400\n" \
"v_mfma_f32_16x16x32_bf16 a[180:183], v[168:171], v[136:139], a[180:183]\n" \
"buffer_load_dwordx4 v24, s[76:79], s85 offen lds\n" \
"v_mfma_f32_16x16x32_bf16 a[176:179], v[100:103], v[72:75], a[176:179]\n" \
"ds_read_b64_tr_b16 v[88:89], v33 offset:32768\n" \
"v_mfma_f32_16x16x32_bf16 a[176:179], v[164:167], v[136:139], a[176:179]\n" \
"ds_read_b128 v[72:75], v28 offset:43008\n" \
"v_mfma_f32_16x16x32_bf16 a[72:75], v[100:103], v[76:79], a[72:75]\n" \
"ds_read_b128 v[136:139], v29 offset:43008\n" \
"v_mfma_f32_16x16x32_bf16 a[72:75], v[164:167], v[140:143], a[72:75]\n" \
"s_add_u32 m0, s30, 0x400\n" \
"s_mov_b32 s84, 0x2800\n" \
"v_mfma_f32_16x16x32_bf16 a[76:79], v[104:107], v[76:79], a[76:79]\n" \
"buffer_load_dwordx4 v25, s[76:79], s84 offen lds\n" \
"v_mfma_f32_16x16x32_bf16 a[76:79], v[168:171], v[140:143], a[76:79]\n" \
"ds_read_b64_tr_b16 v[90:91], v33 offset:33280\n" \
"v_mfma_f32_16x16x32_bf16 a[104:107], v[108:111], v[76:79], a[104:107]\n" \
"ds_read_b64_tr_b16 v[152:153], v33 offset:36864\n" \
"v_mfma_f32_16x16x32_bf16 a[104:107], v[172:175], v[140:143], a[104:107]\n" \
"s_add_u32 m0, s31, 0x0\n" \
"s_nop 0\n" \
"s_mov_b32 s85, 0x400000\n" \
"v_mfma_f32_16x16x32_bf16 a[108:111], v[112:115], v[76:79], a[108:111]\n" \
"buffer_load_dwordx4 v26, s[68:71], s85 offen lds\n" \
"v_mfma_f32_16x16x32_bf16 a[108:111], v[176:179], v[140:143], a[108:111]\n" \
"ds_read_b128 v[76:79], v28 offset:45056\n" \
"v_mfma_f32_16x16x32_bf16 a[172:175], v[112:115], v[80:83], a[172:175]\n" \
"ds_read_b128 v[140:143], v29 offset:45056\n" \
"v_mfma_f32_16x16x32_bf16 a[172:175], v[176:179], v[144:147], a[172:175]\n" \
"s_add_u32 m0, s30, 0x800\n" \
"s_mov_b32 s84, 0x4c00\n" \
"v_mfma_f32_16x16x32_bf16 a[168:171], v[108:111], v[80:83], a[168:171]\n" \
"buffer_load_dwordx4 v24, s[76:79], s84 offen lds\n" \
"v_mfma_f32_16x16x32_bf16 a[168:171], v[172:175], v[144:147], a[168:171]\n" \
"ds_read_b64_tr_b16 v[154:155], v33 offset:37376\n" \
"v_mfma_f32_16x16x32_bf16 a[140:143], v[104:107], v[80:83], a[140:143]\n" \
"ds_read_b64_tr_b16 v[96:97], v35 offset:32768\n" \
"v_mfma_f32_16x16x32_bf16 a[140:143], v[168:171], v[144:147], a[140:143]\n" \
"ds_read_b64_tr_b16 v[98:99], v35 offset:33280\n" \
"v_mfma_f32_16x16x32_bf16 a[136:139], v[100:103], v[80:83], a[136:139]\n" \
"v_mfma_f32_16x16x32_bf16 a[136:139], v[164:167], v[144:147], a[136:139]\n" \
"ds_read_b128 v[80:83], v28 offset:47104\n" \
"v_mfma_f32_16x16x32_bf16 a[80:83], v[100:103], v[60:63], a[80:83]\n" \
"ds_read_b128 v[144:147], v29 offset:47104\n" \
"v_mfma_f32_16x16x32_bf16 a[80:83], v[164:167], v[124:127], a[80:83]\n" \
"s_add_u32 m0, s30, 0xc00\n" \
"s_mov_b32 s85, 0x7000\n" \
"v_mfma_f32_16x16x32_bf16 a[84:87], v[104:107], v[60:63], a[84:87]\n" \
"buffer_load_dwordx4 v25, s[76:79], s85 offen lds\n" \
"v_mfma_f32_16x16x32_bf16 a[84:87], v[168:171], v[124:127], a[84:87]\n" \
"s_add_u32 m0, s31, 0x400\n" \
"s_mov_b32 s84, 0x408000\n" \
"v_mfma_f32_16x16x32_bf16 a[88:91], v[108:111], v[60:63], a[88:91]\n" \
"buffer_load_dwordx4 v26, s[68:71], s84 offen lds\n" \
"v_mfma_f32_16x16x32_bf16 a[88:91], v[172:175], v[124:127], a[88:91]\n" \
"ds_read_b64_tr_b16 v[160:161], v35 offset:36864\n" \
"v_mfma_f32_16x16x32_bf16 a[92:95], v[112:115], v[60:63], a[92:95]\n" \
"ds_read_b64_tr_b16 v[162:163], v35 offset:37376\n" \
"v_mfma_f32_16x16x32_bf16 a[92:95], v[176:179], v[124:127], a[92:95]\n" \
"ds_read_b128 v[60:63], v28 offset:36864\n" \
"v_mfma_f32_16x16x32_bf16 a[124:127], v[112:115], v[64:67], a[124:127]\n" \
"ds_read_b128 v[124:127], v29 offset:36864\n" \
"v_mfma_f32_16x16x32_bf16 a[124:127], v[176:179], v[128:131], a[124:127]\n" \
"s_add_u32 m0, s30, 0x1000\n" \
"s_mov_b32 s85, 0x9400\n" \
"v_mfma_f32_16x16x32_bf16 a[120:123], v[108:111], v[64:67], a[120:123]\n" \
"buffer_load_dwordx4 v24, s[76:79], s85 offen lds\n" \
"v_mfma_f32_16x16x32_bf16 a[120:123], v[172:175], v[128:131], a[120:123]\n" \
"s_add_u32 m0, s30, 0x1400\n" \
"s_mov_b32 s84, 0xb800\n" \
"v_mfma_f32_16x16x32_bf16 a[116:119], v[104:107], v[64:67], a[116:119]\n" \
"buffer_load_dwordx4 v25, s[76:79], s84 offen lds\n" \
"v_mfma_f32_16x16x32_bf16 a[116:119], v[168:171], v[128:131], a[116:119]\n" \
"ds_read_b64_tr_b16 v[92:93], v34 offset:32768\n" \
"v_mfma_f32_16x16x32_bf16 a[112:115], v[100:103], v[64:67], a[112:115]\n" \
"ds_read_b64_tr_b16 v[94:95], v34 offset:33280\n" \
"v_mfma_f32_16x16x32_bf16 a[112:115], v[164:167], v[128:131], a[112:115]\n" \
"ds_read_b128 v[64:67], v28 offset:38912\n" \
"v_mfma_f32_16x16x32_bf16 a[16:19], v[100:103], v[52:55], a[16:19]\n" \
"ds_read_b128 v[128:131], v29 offset:38912\n" \
"v_mfma_f32_16x16x32_bf16 a[16:19], v[164:167], v[116:119], a[16:19]\n" \
"s_add_u32 m0, s31, 0x800\n" \
"s_mov_b32 s85, 0x410000\n" \
"v_mfma_f32_16x16x32_bf16 a[20:23], v[104:107], v[52:55], a[20:23]\n" \
"buffer_load_dwordx4 v27, s[68:71], s85 offen lds\n" \
"v_mfma_f32_16x16x32_bf16 a[20:23], v[168:171], v[116:119], a[20:23]\n" \
"s_add_u32 m0, s30, 0x1800\n" \
"s_mov_b32 s84, 0xdc00\n" \
"v_mfma_f32_16x16x32_bf16 a[24:27], v[108:111], v[52:55], a[24:27]\n" \
"buffer_load_dwordx4 v24, s[76:79], s84 offen lds\n" \
"v_mfma_f32_16x16x32_bf16 a[24:27], v[172:175], v[116:119], a[24:27]\n" \
"ds_read_b64_tr_b16 v[156:157], v34 offset:36864\n" \
"v_mfma_f32_16x16x32_bf16 a[28:31], v[112:115], v[52:55], a[28:31]\n" \
"ds_read_b64_tr_b16 v[158:159], v34 offset:37376\n" \
"v_mfma_f32_16x16x32_bf16 a[28:31], v[176:179], v[116:119], a[28:31]\n" \
"ds_read_b128 v[52:55], v28 offset:32768\n" \
"v_mfma_f32_16x16x32_bf16 a[60:63], v[112:115], v[56:59], a[60:63]\n" \
"ds_read_b128 v[116:119], v29 offset:32768\n" \
"v_mfma_f32_16x16x32_bf16 a[60:63], v[176:179], v[120:123], a[60:63]\n" \
"v_mfma_f32_16x16x32_bf16 a[56:59], v[108:111], v[56:59], a[56:59]\n" \
"v_mfma_f32_16x16x32_bf16 a[56:59], v[172:175], v[120:123], a[56:59]\n" \
"v_mfma_f32_16x16x32_bf16 a[52:55], v[104:107], v[56:59], a[52:55]\n" \
"v_mfma_f32_16x16x32_bf16 a[52:55], v[168:171], v[120:123], a[52:55]\n" \
"v_mfma_f32_16x16x32_bf16 a[48:51], v[100:103], v[56:59], a[48:51]\n" \
"v_mfma_f32_16x16x32_bf16 a[48:51], v[164:167], v[120:123], a[48:51]\n" \
"ds_read_b128 v[56:59], v28 offset:34816\n" \
"ds_read_b128 v[120:123], v29 offset:34816\n" \
"s_waitcnt lgkmcnt(15)\n" \
"s_nop 0\n" \
"v_mfma_f32_16x16x32_bf16 a[0:3], v[84:87], v[68:71], a[0:3]\n" \
"s_add_u32 m0, s30, 0x1c00\n" \
"s_mov_b32 s84, 0x10000\n" \
"v_mfma_f32_16x16x32_bf16 a[0:3], v[148:151], v[132:135], a[0:3]\n" \
"buffer_load_dwordx4 v25, s[76:79], s84 offen lds\n" \
"v_mfma_f32_16x16x32_bf16 a[4:7], v[88:91], v[68:71], a[4:7]\n" \
"ds_read_b64_tr_b16 v[100:101], v36 offset:32768\n" \
"v_mfma_f32_16x16x32_bf16 a[4:7], v[152:155], v[132:135], a[4:7]\n" \
"s_add_u32 m0, s31, 0xc00\n" \
"s_mov_b32 s85, 0x418000\n" \
"v_mfma_f32_16x16x32_bf16 a[36:39], v[96:99], v[68:71], a[36:39]\n" \
"buffer_load_dwordx4 v27, s[68:71], s85 offen lds\n" \
"s_waitcnt lgkmcnt(13)\n" \
"v_mfma_f32_16x16x32_bf16 a[36:39], v[160:163], v[132:135], a[36:39]\n" \
"ds_read_b64_tr_b16 v[102:103], v36 offset:33792\n" \
"s_waitcnt lgkmcnt(10)\n" \
"v_mfma_f32_16x16x32_bf16 a[32:35], v[68:71], v[92:95], a[32:35]\n" \
"ds_read_b64_tr_b16 v[164:165], v36 offset:40960\n" \
"s_waitcnt lgkmcnt(7)\n" \
"s_nop 0\n" \
"v_mfma_f32_16x16x32_bf16 a[32:35], v[132:135], v[156:159], a[32:35]\n" \
"ds_read_b64_tr_b16 v[166:167], v36 offset:41984\n" \
"v_mfma_f32_16x16x32_bf16 a[96:99], v[72:75], v[92:95], a[96:99]\n" \
"ds_read_b64_tr_b16 v[104:105], v37 offset:32768\n" \
"v_mfma_f32_16x16x32_bf16 a[96:99], v[136:139], v[156:159], a[96:99]\n" \
"ds_read_b64_tr_b16 v[106:107], v37 offset:33792\n" \
"v_mfma_f32_16x16x32_bf16 a[100:103], v[96:99], v[72:75], a[100:103]\n" \
"ds_read_b64_tr_b16 v[168:169], v37 offset:40960\n" \
"v_mfma_f32_16x16x32_bf16 a[100:103], v[160:163], v[136:139], a[100:103]\n" \
"ds_read_b64_tr_b16 v[170:171], v37 offset:41984\n" \
"v_mfma_f32_16x16x32_bf16 a[68:71], v[88:91], v[72:75], a[68:71]\n" \
"v_mfma_f32_16x16x32_bf16 a[68:71], v[152:155], v[136:139], a[68:71]\n" \
"s_add_u32 s88, s42, 0x3000\n" \
"buffer_store_dwordx4 v[204:207], v20, s[80:83], s88 offen offset:1024 sc1\n" \
"v_mfma_f32_16x16x32_bf16 a[64:67], v[84:87], v[72:75], a[64:67]\n" \
"ds_read_b64_tr_b16 v[108:109], v38 offset:32768\n" \
"v_mfma_f32_16x16x32_bf16 a[64:67], v[148:151], v[136:139], a[64:67]\n" \
"ds_read_b64_tr_b16 v[110:111], v38 offset:33792\n" \
"v_mfma_f32_16x16x32_bf16 a[128:131], v[84:87], v[76:79], a[128:131]\n" \
"ds_read_b64_tr_b16 v[172:173], v38 offset:40960\n" \
"v_mfma_f32_16x16x32_bf16 a[128:131], v[148:151], v[140:143], a[128:131]\n" \
"ds_read_b64_tr_b16 v[174:175], v38 offset:41984\n" \
"v_mfma_f32_16x16x32_bf16 a[132:135], v[88:91], v[76:79], a[132:135]\n" \
"v_mfma_f32_16x16x32_bf16 a[132:135], v[152:155], v[140:143], a[132:135]\n" \
"s_add_u32 s89, s42, 0x3000\n" \
"buffer_store_dwordx4 v[208:211], v20, s[80:83], s89 offen offset:2048 sc1\n" \
"v_mfma_f32_16x16x32_bf16 a[164:167], v[96:99], v[76:79], a[164:167]\n" \
"ds_read_b64_tr_b16 v[112:113], v39 offset:32768\n" \
"v_mfma_f32_16x16x32_bf16 a[164:167], v[160:163], v[140:143], a[164:167]\n" \
"ds_read_b64_tr_b16 v[114:115], v39 offset:33792\n" \
"v_mfma_f32_16x16x32_bf16 a[160:163], v[76:79], v[92:95], a[160:163]\n" \
"ds_read_b64_tr_b16 v[176:177], v39 offset:40960\n" \
"v_mfma_f32_16x16x32_bf16 a[160:163], v[140:143], v[156:159], a[160:163]\n" \
"v_mfma_f32_16x16x32_bf16 a[224:227], v[80:83], v[92:95], a[224:227]\n" \
"ds_read_b64_tr_b16 v[178:179], v39 offset:41984\n" \
"v_mfma_f32_16x16x32_bf16 a[224:227], v[144:147], v[156:159], a[224:227]\n" \
"v_mfma_f32_16x16x32_bf16 a[228:231], v[96:99], v[80:83], a[228:231]\n" \
"s_add_u32 s88, s42, 0x3000\n" \
"buffer_store_dwordx4 v[212:215], v20, s[80:83], s88 offen offset:3072 sc1\n" \
"v_mfma_f32_16x16x32_bf16 a[228:231], v[160:163], v[144:147], a[228:231]\n" \
"v_mfma_f32_16x16x32_bf16 a[196:199], v[88:91], v[80:83], a[196:199]\n" \
"v_mfma_f32_16x16x32_bf16 a[196:199], v[152:155], v[144:147], a[196:199]\n" \
"v_mfma_f32_16x16x32_bf16 a[192:195], v[84:87], v[80:83], a[192:195]\n" \
"v_mfma_f32_16x16x32_bf16 a[192:195], v[148:151], v[144:147], a[192:195]\n" \
"v_mfma_f32_16x16x32_bf16 a[8:11], v[88:91], v[64:67], a[8:11]\n" \
"v_mfma_f32_16x16x32_bf16 a[8:11], v[152:155], v[128:131], a[8:11]\n" \
"v_mfma_f32_16x16x32_bf16 a[40:43], v[96:99], v[64:67], a[40:43]\n" \
"v_mfma_f32_16x16x32_bf16 a[40:43], v[160:163], v[128:131], a[40:43]\n" \
"v_mfma_f32_16x16x32_bf16 a[12:15], v[64:67], v[92:95], a[12:15]\n" \
"v_mfma_f32_16x16x32_bf16 a[12:15], v[128:131], v[156:159], a[12:15]\n" \
"s_waitcnt vmcnt(3) lgkmcnt(0)\n" \
"s_barrier\n" \
"v_mfma_f32_16x16x32_bf16 a[144:147], v[100:103], v[68:71], a[144:147]\n" \
"ds_read_b64_tr_b16 v[84:85], v32 offset:0\n" \
"v_lshrrev_b32_e64 v24, 9, v20\n" \
"v_mfma_f32_16x16x32_bf16 a[144:147], v[164:167], v[132:135], a[144:147]\n" \
"v_and_b32_e32 v26, 0x1ff, v20\n" \
"v_lshl_add_u32 v24, v24, 3, v26\n" \
"v_mfma_f32_16x16x32_bf16 a[148:151], v[104:107], v[68:71], a[148:151]\n" \
"v_lshrrev_b32_e64 v25, 8, v20\n" \
"v_mul_u32_u24_e32 v25, 0x24700, v25\n" \
"v_mfma_f32_16x16x32_bf16 a[148:151], v[168:171], v[132:135], a[148:151]\n" \
"ds_read_b64_tr_b16 v[86:87], v32 offset:512\n" \
"v_add_u32_e64 v25, v25, v20\n" \
"v_mfma_f32_16x16x32_bf16 a[200:203], v[108:111], v[68:71], a[200:203]\n" \
"v_lshrrev_b32_e32 v26, 2, v20\n" \
"v_add_u32_e32 v26, s45, v26\n" \
"v_mfma_f32_16x16x32_bf16 a[200:203], v[172:175], v[132:135], a[200:203]\n" \
"v_add_u32_e32 v27, s46, v20\n" \
"v_add_u32_e32 v21, s43, v27\n" \
"v_mfma_f32_16x16x32_bf16 a[204:207], v[112:115], v[68:71], a[204:207]\n" \
"ds_read_b64_tr_b16 v[148:149], v32 offset:4096\n" \
"v_mfma_f32_16x16x32_bf16 a[204:207], v[176:179], v[132:135], a[204:207]\n" \
"ds_read_b128 v[68:71], v28 offset:8192\n" \
"v_mfma_f32_16x16x32_bf16 a[236:239], v[112:115], v[72:75], a[236:239]\n" \
"ds_read_b128 v[132:135], v29 offset:8192\n" \
"v_mfma_f32_16x16x32_bf16 a[236:239], v[176:179], v[136:139], a[236:239]\n" \
"ds_read_b64_tr_b16 v[150:151], v32 offset:4608\n" \
"v_mfma_f32_16x16x32_bf16 a[232:235], v[108:111], v[72:75], a[232:235]\n" \
"v_mfma_f32_16x16x32_bf16 a[232:235], v[172:175], v[136:139], a[232:235]\n" \
"v_mfma_f32_16x16x32_bf16 a[180:183], v[104:107], v[72:75], a[180:183]\n" \
"v_mfma_f32_16x16x32_bf16 a[180:183], v[168:171], v[136:139], a[180:183]\n" \
"ds_read_b64_tr_b16 v[88:89], v33 offset:0\n" \
"v_mfma_f32_16x16x32_bf16 a[176:179], v[100:103], v[72:75], a[176:179]\n" \
"v_mfma_f32_16x16x32_bf16 a[176:179], v[164:167], v[136:139], a[176:179]\n" \
"ds_read_b128 v[72:75], v28 offset:10240\n" \
"v_mfma_f32_16x16x32_bf16 a[72:75], v[100:103], v[76:79], a[72:75]\n" \
"ds_read_b128 v[136:139], v29 offset:10240\n" \
"v_mfma_f32_16x16x32_bf16 a[72:75], v[164:167], v[140:143], a[72:75]\n" \
"ds_read_b64_tr_b16 v[90:91], v33 offset:512\n" \
"v_mfma_f32_16x16x32_bf16 a[76:79], v[104:107], v[76:79], a[76:79]\n" \
"v_mfma_f32_16x16x32_bf16 a[76:79], v[168:171], v[140:143], a[76:79]\n" \
"ds_read_b64_tr_b16 v[152:153], v33 offset:4096\n" \
"v_mfma_f32_16x16x32_bf16 a[104:107], v[108:111], v[76:79], a[104:107]\n" \
"v_mfma_f32_16x16x32_bf16 a[104:107], v[172:175], v[140:143], a[104:107]\n" \
"v_mfma_f32_16x16x32_bf16 a[108:111], v[112:115], v[76:79], a[108:111]\n" \
"ds_read_b64_tr_b16 v[154:155], v33 offset:4608\n" \
"v_mfma_f32_16x16x32_bf16 a[108:111], v[176:179], v[140:143], a[108:111]\n" \
"ds_read_b128 v[76:79], v28 offset:12288\n" \
"v_mfma_f32_16x16x32_bf16 a[172:175], v[112:115], v[80:83], a[172:175]\n" \
"ds_read_b128 v[140:143], v29 offset:12288\n" \
"v_mfma_f32_16x16x32_bf16 a[172:175], v[176:179], v[144:147], a[172:175]\n" \
"v_mfma_f32_16x16x32_bf16 a[168:171], v[108:111], v[80:83], a[168:171]\n" \
"ds_read_b64_tr_b16 v[96:97], v35 offset:0\n" \
"v_mfma_f32_16x16x32_bf16 a[168:171], v[172:175], v[144:147], a[168:171]\n" \
"v_mfma_f32_16x16x32_bf16 a[140:143], v[104:107], v[80:83], a[140:143]\n" \
"v_mfma_f32_16x16x32_bf16 a[140:143], v[168:171], v[144:147], a[140:143]\n" \
"ds_read_b64_tr_b16 v[98:99], v35 offset:512\n" \
"v_mfma_f32_16x16x32_bf16 a[136:139], v[100:103], v[80:83], a[136:139]\n" \
"v_mfma_f32_16x16x32_bf16 a[136:139], v[164:167], v[144:147], a[136:139]\n" \
"ds_read_b128 v[80:83], v28 offset:14336\n" \
"v_mfma_f32_16x16x32_bf16 a[80:83], v[100:103], v[60:63], a[80:83]\n" \
"ds_read_b128 v[144:147], v29 offset:14336\n" \
"v_mfma_f32_16x16x32_bf16 a[80:83], v[164:167], v[124:127], a[80:83]\n" \
"ds_read_b64_tr_b16 v[160:161], v35 offset:4096\n" \
"v_mfma_f32_16x16x32_bf16 a[84:87], v[104:107], v[60:63], a[84:87]\n" \
"v_mfma_f32_16x16x32_bf16 a[84:87], v[168:171], v[124:127], a[84:87]\n" \
"ds_read_b64_tr_b16 v[162:163], v35 offset:4608\n" \
"v_mfma_f32_16x16x32_bf16 a[88:91], v[108:111], v[60:63], a[88:91]\n" \
"v_mfma_f32_16x16x32_bf16 a[88:91], v[172:175], v[124:127], a[88:91]\n" \
"v_mfma_f32_16x16x32_bf16 a[92:95], v[112:115], v[60:63], a[92:95]\n" \
"v_mfma_f32_16x16x32_bf16 a[92:95], v[176:179], v[124:127], a[92:95]\n" \
"ds_read_b128 v[60:63], v28 offset:4096\n" \
"v_mfma_f32_16x16x32_bf16 a[124:127], v[112:115], v[64:67], a[124:127]\n" \
"ds_read_b128 v[124:127], v29 offset:4096\n" \
"v_mfma_f32_16x16x32_bf16 a[124:127], v[176:179], v[128:131], a[124:127]\n" \
"ds_read_b64_tr_b16 v[92:93], v34 offset:0\n" \
"v_mfma_f32_16x16x32_bf16 a[120:123], v[108:111], v[64:67], a[120:123]\n" \
"ds_read_b64_tr_b16 v[94:95], v34 offset:512\n" \
"v_mfma_f32_16x16x32_bf16 a[120:123], v[172:175], v[128:131], a[120:123]\n" \
"v_mfma_f32_16x16x32_bf16 a[116:119], v[104:107], v[64:67], a[116:119]\n" \
"v_mfma_f32_16x16x32_bf16 a[116:119], v[168:171], v[128:131], a[116:119]\n" \
"ds_read_b64_tr_b16 v[156:157], v34 offset:4096\n" \
"v_mfma_f32_16x16x32_bf16 a[112:115], v[100:103], v[64:67], a[112:115]\n" \
"v_mfma_f32_16x16x32_bf16 a[112:115], v[164:167], v[128:131], a[112:115]\n" \
"ds_read_b128 v[64:67], v28 offset:6144\n" \
"v_mfma_f32_16x16x32_bf16 a[16:19], v[100:103], v[52:55], a[16:19]\n" \
"ds_read_b128 v[128:131], v29 offset:6144\n" \
"v_mfma_f32_16x16x32_bf16 a[16:19], v[164:167], v[116:119], a[16:19]\n" \
"ds_read_b64_tr_b16 v[158:159], v34 offset:4608\n" \
"v_mfma_f32_16x16x32_bf16 a[20:23], v[104:107], v[52:55], a[20:23]\n" \
"v_mfma_f32_16x16x32_bf16 a[20:23], v[168:171], v[116:119], a[20:23]\n" \
"v_mfma_f32_16x16x32_bf16 a[24:27], v[108:111], v[52:55], a[24:27]\n" \
"v_mfma_f32_16x16x32_bf16 a[24:27], v[172:175], v[116:119], a[24:27]\n" \
"v_mfma_f32_16x16x32_bf16 a[28:31], v[112:115], v[52:55], a[28:31]\n" \
"v_mfma_f32_16x16x32_bf16 a[28:31], v[176:179], v[116:119], a[28:31]\n" \
"ds_read_b128 v[52:55], v28 offset:0\n" \
"v_mfma_f32_16x16x32_bf16 a[60:63], v[112:115], v[56:59], a[60:63]\n" \
"ds_read_b128 v[116:119], v29 offset:0\n" \
"v_mfma_f32_16x16x32_bf16 a[60:63], v[176:179], v[120:123], a[60:63]\n" \
"v_mfma_f32_16x16x32_bf16 a[56:59], v[108:111], v[56:59], a[56:59]\n" \
"v_mfma_f32_16x16x32_bf16 a[56:59], v[172:175], v[120:123], a[56:59]\n" \
"v_mfma_f32_16x16x32_bf16 a[52:55], v[104:107], v[56:59], a[52:55]\n" \
"v_mfma_f32_16x16x32_bf16 a[52:55], v[168:171], v[120:123], a[52:55]\n" \
"v_mfma_f32_16x16x32_bf16 a[48:51], v[100:103], v[56:59], a[48:51]\n" \
"v_mfma_f32_16x16x32_bf16 a[48:51], v[164:167], v[120:123], a[48:51]\n" \
"ds_read_b128 v[56:59], v28 offset:2048\n" \
"ds_read_b128 v[120:123], v29 offset:2048\n" \
"s_waitcnt lgkmcnt(15)\n" \
"s_nop 0\n" \
"v_mfma_f32_16x16x32_bf16 a[0:3], v[84:87], v[68:71], a[0:3]\n" \
"v_bfe_u32 v44, v20, 2, 5\n" \
"ds_read_b64_tr_b16 v[100:101], v36 offset:0\n" \
"v_mfma_f32_16x16x32_bf16 a[0:3], v[148:151], v[132:135], a[0:3]\n" \
"buffer_load_dword v44, v44, s[80:83], s38 offen offset:0 sc1\n" \
"v_mfma_f32_16x16x32_bf16 a[4:7], v[88:91], v[68:71], a[4:7]\n" \
"ds_read_b64_tr_b16 v[102:103], v36 offset:1024\n" \
"v_mfma_f32_16x16x32_bf16 a[4:7], v[152:155], v[132:135], a[4:7]\n" \
"ds_read_b64_tr_b16 v[164:165], v36 offset:8192\n" \
"v_mfma_f32_16x16x32_bf16 a[36:39], v[96:99], v[68:71], a[36:39]\n" \
"ds_read_b64_tr_b16 v[166:167], v36 offset:9216\n" \
"s_nop 0\n" \
"v_accvgpr_read_b32 v184, a0\n" \
"s_waitcnt lgkmcnt(15)\n" \
"v_mfma_f32_16x16x32_bf16 a[36:39], v[160:163], v[132:135], a[36:39]\n" \
"v_accvgpr_read_b32 v185, a1\n" \
"v_accvgpr_read_b32 v186, a2\n" \
"s_waitcnt lgkmcnt(12)\n" \
"v_mfma_f32_16x16x32_bf16 a[32:35], v[68:71], v[92:95], a[32:35]\n" \
"ds_read_b64_tr_b16 v[104:105], v37 offset:0\n" \
"v_accvgpr_read_b32 v187, a3\n" \
"s_waitcnt lgkmcnt(9)\n" \
"v_mfma_f32_16x16x32_bf16 a[32:35], v[132:135], v[156:159], a[32:35]\n" \
"v_accvgpr_read_b32 v188, a4\n" \
"v_accvgpr_read_b32 v189, a5\n" \
"v_mfma_f32_16x16x32_bf16 a[96:99], v[72:75], v[92:95], a[96:99]\n" \
"ds_read_b64_tr_b16 v[106:107], v37 offset:1024\n" \
"v_accvgpr_read_b32 v190, a6\n" \
"v_mfma_f32_16x16x32_bf16 a[96:99], v[136:139], v[156:159], a[96:99]\n" \
"ds_read_b64_tr_b16 v[168:169], v37 offset:8192\n" \
"v_accvgpr_read_b32 v191, a7\n" \
"v_mfma_f32_16x16x32_bf16 a[100:103], v[96:99], v[72:75], a[100:103]\n" \
"v_accvgpr_read_b32 v196, a36\n" \
"v_accvgpr_read_b32 v197, a37\n" \
"v_mfma_f32_16x16x32_bf16 a[100:103], v[160:163], v[136:139], a[100:103]\n" \
"ds_read_b64_tr_b16 v[170:171], v37 offset:9216\n" \
"v_accvgpr_read_b32 v198, a38\n" \
"v_mfma_f32_16x16x32_bf16 a[68:71], v[88:91], v[72:75], a[68:71]\n" \
"v_accvgpr_read_b32 v199, a39\n" \
"v_accvgpr_read_b32 v192, a32\n" \
"v_mfma_f32_16x16x32_bf16 a[68:71], v[152:155], v[136:139], a[68:71]\n" \
"ds_read_b64_tr_b16 v[108:109], v38 offset:0\n" \
"v_accvgpr_read_b32 v193, a33\n" \
"v_mfma_f32_16x16x32_bf16 a[64:67], v[84:87], v[72:75], a[64:67]\n" \
"ds_read_b64_tr_b16 v[110:111], v38 offset:1024\n" \
"v_accvgpr_read_b32 v194, a34\n" \
"v_mfma_f32_16x16x32_bf16 a[64:67], v[148:151], v[136:139], a[64:67]\n" \
"v_accvgpr_read_b32 v195, a35\n" \
"v_accvgpr_read_b32 v208, a96\n" \
"v_mfma_f32_16x16x32_bf16 a[128:131], v[84:87], v[76:79], a[128:131]\n" \
"ds_read_b64_tr_b16 v[172:173], v38 offset:8192\n" \
"v_accvgpr_read_b32 v209, a97\n" \
"v_mfma_f32_16x16x32_bf16 a[128:131], v[148:151], v[140:143], a[128:131]\n" \
"ds_read_b64_tr_b16 v[174:175], v38 offset:9216\n" \
"v_accvgpr_read_b32 v210, a98\n" \
"v_mfma_f32_16x16x32_bf16 a[132:135], v[88:91], v[76:79], a[132:135]\n" \
"v_lshlrev_b32_e32 v45, 3, v20\n" \
"v_add_u32_e32 v45, s30, v45\n" \
"v_mfma_f32_16x16x32_bf16 a[132:135], v[152:155], v[140:143], a[132:135]\n" \
"s_getpc_b64 s[86:87]\n" \
"L_ct1_%=:\n" \
"s_add_u32 s86, s86, L_end_%=-L_ct1_%=\n" \
"s_addc_u32 s87, s87, 0\n" \
"s_mov_b32 m0, s32\n" \
"s_nop 0\n" \
"v_mfma_f32_16x16x32_bf16 a[164:167], v[96:99], v[76:79], a[164:167]\n" \
"global_load_lds_dword v45, s[86:87]\n" \
"v_mfma_f32_16x16x32_bf16 a[164:167], v[160:163], v[140:143], a[164:167]\n" \
"ds_read_b64_tr_b16 v[112:113], v39 offset:0\n" \
"v_accvgpr_read_b32 v211, a99\n" \
"v_mfma_f32_16x16x32_bf16 a[160:163], v[76:79], v[92:95], a[160:163]\n" \
"ds_read_b64_tr_b16 v[114:115], v39 offset:1024\n" \
"v_accvgpr_read_b32 v212, a100\n" \
"v_mfma_f32_16x16x32_bf16 a[160:163], v[140:143], v[156:159], a[160:163]\n" \
"ds_read_b64_tr_b16 v[176:177], v39 offset:8192\n" \
"v_accvgpr_read_b32 v213, a101\n" \
"v_mfma_f32_16x16x32_bf16 a[224:227], v[80:83], v[92:95], a[224:227]\n" \
"ds_read_b64_tr_b16 v[178:179], v39 offset:9216\n" \
"v_accvgpr_read_b32 v214, a102\n" \
"v_mfma_f32_16x16x32_bf16 a[224:227], v[144:147], v[156:159], a[224:227]\n" \
"v_accvgpr_read_b32 v215, a103\n" \
"v_accvgpr_read_b32 v204, a68\n" \
"v_mfma_f32_16x16x32_bf16 a[228:231], v[96:99], v[80:83], a[228:231]\n" \
"v_accvgpr_read_b32 v205, a69\n" \
"v_accvgpr_read_b32 v206, a70\n" \
"v_mfma_f32_16x16x32_bf16 a[228:231], v[160:163], v[144:147], a[228:231]\n" \
"v_accvgpr_read_b32 v207, a71\n" \
"v_accvgpr_read_b32 v200, a64\n" \
"v_mfma_f32_16x16x32_bf16 a[196:199], v[88:91], v[80:83], a[196:199]\n" \
"v_accvgpr_read_b32 v201, a65\n" \
"v_accvgpr_read_b32 v202, a66\n" \
"v_mfma_f32_16x16x32_bf16 a[196:199], v[152:155], v[144:147], a[196:199]\n" \
"v_accvgpr_read_b32 v203, a67\n" \
"v_accvgpr_read_b32 v216, a128\n" \
"v_mfma_f32_16x16x32_bf16 a[192:195], v[84:87], v[80:83], a[192:195]\n" \
"v_accvgpr_read_b32 v217, a129\n" \
"v_accvgpr_read_b32 v218, a130\n" \
"v_mfma_f32_16x16x32_bf16 a[192:195], v[148:151], v[144:147], a[192:195]\n" \
"v_accvgpr_read_b32 v219, a131\n" \
"v_accvgpr_read_b32 v220, a132\n" \
"v_mfma_f32_16x16x32_bf16 a[8:11], v[88:91], v[64:67], a[8:11]\n" \
"v_accvgpr_read_b32 v221, a133\n" \
"v_accvgpr_read_b32 v222, a134\n" \
"v_mfma_f32_16x16x32_bf16 a[8:11], v[152:155], v[128:131], a[8:11]\n" \
"v_accvgpr_read_b32 v223, a135\n" \
"v_accvgpr_read_b32 v228, a164\n" \
"v_mfma_f32_16x16x32_bf16 a[40:43], v[96:99], v[64:67], a[40:43]\n" \
"v_accvgpr_read_b32 v229, a165\n" \
"v_accvgpr_read_b32 v230, a166\n" \
"v_mfma_f32_16x16x32_bf16 a[40:43], v[160:163], v[128:131], a[40:43]\n" \
"v_accvgpr_read_b32 v231, a167\n" \
"v_accvgpr_read_b32 v224, a160\n" \
"v_mfma_f32_16x16x32_bf16 a[12:15], v[64:67], v[92:95], a[12:15]\n" \
"v_accvgpr_read_b32 v225, a161\n" \
"v_accvgpr_read_b32 v226, a162\n" \
"v_mfma_f32_16x16x32_bf16 a[12:15], v[128:131], v[156:159], a[12:15]\n" \
"v_accvgpr_read_b32 v227, a163\n" \
"v_accvgpr_read_b32 v240, a224\n" \
"s_waitcnt vmcnt(1) lgkmcnt(0)\n" \
"s_barrier\n" \
"v_mov_b32_e32 v45, s62\n" \
"s_mov_b64 exec, 1\n" \
"buffer_store_dword v45, off, s[80:83], s37 offset:32 sc1\n" \
"s_mov_b64 exec, -1\n" \
"v_cmp_ne_u32_e32 vcc, s62, v44\n" \
"s_nop 4\n" \
"s_cbranch_vccnz L_poll161_%=\n" \
"L_pollr161_%=:\n" \
"v_mfma_f32_16x16x32_bf16 a[144:147], v[100:103], v[68:71], a[144:147]\n" \
"s_mov_b32 s73, s50\n" \
"s_sub_u32 m0, s31, 0x10000\n" \
"v_mfma_f32_16x16x32_bf16 a[144:147], v[164:167], v[132:135], a[144:147]\n" \
"buffer_load_dwordx4 v20, s[80:83], s73 offen offset:0 sc1 lds\n" \
"v_mfma_f32_16x16x32_bf16 a[148:151], v[104:107], v[68:71], a[148:151]\n" \
"s_add_u32 s74, s50, 0x4000\n" \
"s_add_u32 s75, s50, 0x8000\n" \
"v_mfma_f32_16x16x32_bf16 a[148:151], v[168:171], v[132:135], a[148:151]\n" \
"s_mov_b32 s70, 0x3e0293ee\n" \
"s_mov_b32 s76, s52\n" \
"v_mfma_f32_16x16x32_bf16 a[200:203], v[108:111], v[68:71], a[200:203]\n" \
"v_accvgpr_read_b32 v32, a144\n" \
"v_accvgpr_read_b32 v33, a145\n" \
"v_mfma_f32_16x16x32_bf16 a[200:203], v[172:175], v[132:135], a[200:203]\n" \
"buffer_load_dwordx4 v20, s[80:83], s73 offen offset:1024 sc1 lds\n" \
"v_mfma_f32_16x16x32_bf16 a[204:207], v[112:115], v[68:71], a[204:207]\n" \
"v_accvgpr_read_b32 v34, a146\n" \
"v_accvgpr_read_b32 v35, a147\n" \
"v_mfma_f32_16x16x32_bf16 a[204:207], v[176:179], v[132:135], a[204:207]\n" \
"v_mul_f32_e32 v32, s70, v32\n" \
"v_mul_f32_e32 v33, s70, v33\n" \
"v_mfma_f32_16x16x32_bf16 a[236:239], v[112:115], v[72:75], a[236:239]\n" \
"v_accvgpr_read_b32 v36, a148\n" \
"v_accvgpr_read_b32 v37, a149\n" \
"v_mfma_f32_16x16x32_bf16 a[236:239], v[176:179], v[136:139], a[236:239]\n" \
"buffer_load_dwordx4 v20, s[80:83], s73 offen offset:2048 sc1 lds\n" \
"v_mfma_f32_16x16x32_bf16 a[232:235], v[108:111], v[72:75], a[232:235]\n" \
"v_accvgpr_read_b32 v38, a150\n" \
"v_accvgpr_read_b32 v39, a151\n" \
"v_mfma_f32_16x16x32_bf16 a[232:235], v[172:175], v[136:139], a[232:235]\n" \
"v_mul_f32_e32 v34, s70, v34\n" \
"v_mul_f32_e32 v35, s70, v35\n" \
"v_mfma_f32_16x16x32_bf16 a[180:183], v[104:107], v[72:75], a[180:183]\n" \
"v_cvt_pk_bf16_f32 v32, v32, v33\n" \
"v_mul_f32_e64 v36, s70, v36\n" \
"v_mfma_f32_16x16x32_bf16 a[180:183], v[168:171], v[136:139], a[180:183]\n" \
"buffer_load_dwordx4 v20, s[80:83], s73 offen offset:3072 sc1 lds\n" \
"v_mfma_f32_16x16x32_bf16 a[176:179], v[100:103], v[72:75], a[176:179]\n" \
"s_sub_u32 m0, s31, s51\n" \
"v_mul_f32_e32 v37, s70, v37\n" \
"v_mfma_f32_16x16x32_bf16 a[176:179], v[164:167], v[136:139], a[176:179]\n" \
"v_mul_f32_e32 v38, s70, v38\n" \
"v_mul_f32_e32 v39, s70, v39\n" \
"v_mfma_f32_16x16x32_bf16 a[72:75], v[100:103], v[76:79], a[72:75]\n" \
"v_cvt_pk_bf16_f32 v33, v34, v35\n" \
"v_cvt_pk_bf16_f32 v34, v36, v37\n" \
"v_mfma_f32_16x16x32_bf16 a[72:75], v[164:167], v[140:143], a[72:75]\n" \
"buffer_load_dwordx4 v20, s[80:83], s74 offen offset:0 sc1 lds\n" \
"v_mfma_f32_16x16x32_bf16 a[76:79], v[104:107], v[76:79], a[76:79]\n" \
"v_cvt_pk_bf16_f32 v35, v38, v39\n" \
"v_accvgpr_read_b32 v36, a204\n" \
"v_mfma_f32_16x16x32_bf16 a[76:79], v[168:171], v[140:143], a[76:79]\n" \
"ds_write_b128 v21, v[32:35] offset:16384\n" \
"v_mfma_f32_16x16x32_bf16 a[104:107], v[108:111], v[76:79], a[104:107]\n" \
"v_accvgpr_read_b32 v32, a200\n" \
"v_accvgpr_read_b32 v33, a201\n" \
"v_mfma_f32_16x16x32_bf16 a[104:107], v[172:175], v[140:143], a[104:107]\n" \
"buffer_load_dwordx4 v20, s[80:83], s74 offen offset:1024 sc1 lds\n" \
"v_mfma_f32_16x16x32_bf16 a[108:111], v[112:115], v[76:79], a[108:111]\n" \
"v_accvgpr_read_b32 v34, a202\n" \
"v_accvgpr_read_b32 v35, a203\n" \
"v_mfma_f32_16x16x32_bf16 a[108:111], v[176:179], v[140:143], a[108:111]\n" \
"v_mul_f32_e32 v32, s70, v32\n" \
"v_mul_f32_e32 v33, s70, v33\n" \
"v_mfma_f32_16x16x32_bf16 a[172:175], v[112:115], v[80:83], a[172:175]\n" \
"v_accvgpr_read_b32 v37, a205\n" \
"v_accvgpr_read_b32 v38, a206\n" \
"v_mfma_f32_16x16x32_bf16 a[172:175], v[176:179], v[144:147], a[172:175]\n" \
"buffer_load_dwordx4 v20, s[80:83], s74 offen offset:2048 sc1 lds\n" \
"v_mfma_f32_16x16x32_bf16 a[168:171], v[108:111], v[80:83], a[168:171]\n" \
"v_accvgpr_read_b32 v39, a207\n" \
"v_mul_f32_e64 v34, s70, v34\n" \
"v_mfma_f32_16x16x32_bf16 a[168:171], v[172:175], v[144:147], a[168:171]\n" \
"v_mul_f32_e64 v35, s70, v35\n" \
"v_cvt_pk_bf16_f32 v32, v32, v33\n" \
"v_mfma_f32_16x16x32_bf16 a[140:143], v[104:107], v[80:83], a[140:143]\n" \
"v_mul_f32_e32 v36, s70, v36\n" \
"v_mul_f32_e32 v37, s70, v37\n" \
"v_mfma_f32_16x16x32_bf16 a[140:143], v[168:171], v[144:147], a[140:143]\n" \
"buffer_load_dwordx4 v20, s[80:83], s74 offen offset:3072 sc1 lds\n" \
"v_mfma_f32_16x16x32_bf16 a[136:139], v[100:103], v[80:83], a[136:139]\n" \
"s_sub_u32 m0, s31, 0x4000\n" \
"v_mul_f32_e64 v38, s70, v38\n" \
"v_mfma_f32_16x16x32_bf16 a[136:139], v[164:167], v[144:147], a[136:139]\n" \
"v_mul_f32_e64 v39, s70, v39\n" \
"v_cvt_pk_bf16_f32 v33, v34, v35\n" \
"v_mfma_f32_16x16x32_bf16 a[80:83], v[100:103], v[60:63], a[80:83]\n" \
"v_accvgpr_read_b32 v236, a196\n" \
"v_accvgpr_read_b32 v237, a197\n" \
"v_mfma_f32_16x16x32_bf16 a[80:83], v[164:167], v[124:127], a[80:83]\n" \
"buffer_load_dwordx4 v20, s[80:83], s75 offen offset:0 sc1 lds\n" \
"v_mfma_f32_16x16x32_bf16 a[84:87], v[104:107], v[60:63], a[84:87]\n" \
"v_accvgpr_read_b32 v232, a192\n" \
"v_accvgpr_read_b32 v233, a193\n" \
"v_mfma_f32_16x16x32_bf16 a[84:87], v[168:171], v[124:127], a[84:87]\n" \
"v_accvgpr_read_b32 v180, a8\n" \
"v_accvgpr_read_b32 v181, a9\n" \
"v_mfma_f32_16x16x32_bf16 a[88:91], v[108:111], v[60:63], a[88:91]\n" \
"v_cvt_pk_bf16_f32 v34, v36, v37\n" \
"v_cvt_pk_bf16_f32 v35, v38, v39\n" \
"v_mfma_f32_16x16x32_bf16 a[88:91], v[172:175], v[124:127], a[88:91]\n" \
"buffer_load_dwordx4 v20, s[80:83], s75 offen offset:1024 sc1 lds\n" \
"v_mfma_f32_16x16x32_bf16 a[92:95], v[112:115], v[60:63], a[92:95]\n" \
"v_accvgpr_read_b32 v238, a198\n" \
"v_accvgpr_read_b32 v239, a199\n" \
"v_mfma_f32_16x16x32_bf16 a[92:95], v[176:179], v[124:127], a[92:95]\n" \
"v_accvgpr_read_b32 v234, a194\n" \
"v_accvgpr_read_b32 v235, a195\n" \
"v_mfma_f32_16x16x32_bf16 a[124:127], v[112:115], v[64:67], a[124:127]\n" \
"v_accvgpr_read_b32 v182, a10\n" \
"v_accvgpr_read_b32 v183, a11\n" \
"v_mfma_f32_16x16x32_bf16 a[124:127], v[176:179], v[128:131], a[124:127]\n" \
"buffer_load_dwordx4 v20, s[80:83], s75 offen offset:2048 sc1 lds\n" \
"v_mfma_f32_16x16x32_bf16 a[120:123], v[108:111], v[64:67], a[120:123]\n" \
"v_cvt_pk_bf16_f32 v180, v180, v181\n" \
"v_cvt_pk_bf16_f32 v188, v188, v189\n" \
"v_mfma_f32_16x16x32_bf16 a[120:123], v[172:175], v[128:131], a[120:123]\n" \
"v_cvt_pk_bf16_f32 v184, v184, v185\n" \
"v_cvt_pk_bf16_f32 v204, v204, v205\n" \
"v_mfma_f32_16x16x32_bf16 a[116:119], v[104:107], v[64:67], a[116:119]\n" \
"v_cvt_pk_bf16_f32 v200, v200, v201\n" \
"v_cvt_pk_bf16_f32 v220, v220, v221\n" \
"v_mfma_f32_16x16x32_bf16 a[116:119], v[168:171], v[128:131], a[116:119]\n" \
"buffer_load_dwordx4 v20, s[80:83], s75 offen offset:3072 sc1 lds\n" \
"v_mfma_f32_16x16x32_bf16 a[112:115], v[100:103], v[64:67], a[112:115]\n" \
"s_mov_b32 m0, s53\n" \
"s_nop 0\n" \
"v_cvt_pk_bf16_f32 v216, v216, v217\n" \
"v_mfma_f32_16x16x32_bf16 a[112:115], v[164:167], v[128:131], a[112:115]\n" \
"v_cvt_pk_bf16_f32 v236, v236, v237\n" \
"v_cvt_pk_bf16_f32 v232, v232, v233\n" \
"v_mfma_f32_16x16x32_bf16 a[16:19], v[100:103], v[52:55], a[16:19]\n" \
"ds_write_b128 v21, v[32:35] offset:17408\n" \
"v_mfma_f32_16x16x32_bf16 a[16:19], v[164:167], v[116:119], a[16:19]\n" \
"buffer_load_dwordx4 v20, s[80:83], s76 offen offset:0 sc1 lds\n" \
"v_mfma_f32_16x16x32_bf16 a[20:23], v[104:107], v[52:55], a[20:23]\n" \
"v_accvgpr_read_b32 v241, a225\n" \
"v_accvgpr_read_b32 v242, a226\n" \
"v_mfma_f32_16x16x32_bf16 a[20:23], v[168:171], v[116:119], a[20:23]\n" \
"v_accvgpr_read_b32 v243, a227\n" \
"v_accvgpr_read_b32 v244, a228\n" \
"v_accvgpr_read_b32 v245, a229\n" \
"v_accvgpr_read_b32 v246, a230\n" \
"v_accvgpr_read_b32 v247, a231\n" \
"v_accvgpr_read_b32 v50, a42\n" \
"v_accvgpr_read_b32 v51, a43\n" \
"buffer_load_dwordx4 v20, s[80:83], s76 offen offset:1024 sc1 lds\n" \
"v_cvt_pk_bf16_f32 v181, v182, v183\n" \
"v_cvt_pk_bf16_f32 v189, v190, v191\n" \
"v_cvt_pk_bf16_f32 v185, v186, v187\n" \
"v_cvt_pk_bf16_f32 v205, v206, v207\n" \
"v_cvt_pk_bf16_f32 v201, v202, v203\n" \
"v_cvt_pk_bf16_f32 v221, v222, v223\n" \
"v_cvt_pk_bf16_f32 v217, v218, v219\n" \
"v_cvt_pk_bf16_f32 v237, v238, v239\n" \
"v_cvt_pk_bf16_f32 v233, v234, v235\n" \
"v_cvt_pk_bf16_f32 v182, v50, v51\n" \
"v_mov_b32_e32 v183, 0x3f80\n" \
"v_cvt_pk_bf16_f32 v190, v198, v199\n" \
"v_mov_b32_e32 v191, 0x3f80\n" \
"v_cvt_pk_bf16_f32 v186, v196, v197\n" \
"v_mov_b32_e64 v187, 0\n" \
"v_cvt_pk_bf16_f32 v86, v192, v193\n" \
"v_cvt_pk_bf16_f32 v87, v194, v195\n" \
"v_cvt_pk_bf16_f32 v206, v214, v215\n" \
"v_mov_b32_e32 v207, 0x3f80\n" \
"v_cvt_pk_bf16_f32 v202, v212, v213\n" \
"v_mov_b32_e64 v203, 0\n" \
"v_cvt_pk_bf16_f32 v88, v208, v209\n" \
"v_cvt_pk_bf16_f32 v89, v210, v211\n" \
"v_cvt_pk_bf16_f32 v222, v230, v231\n" \
"v_mov_b32_e32 v223, 0x3f80\n" \
"v_cvt_pk_bf16_f32 v218, v228, v229\n" \
"v_mov_b32_e64 v219, 0\n" \
"v_cvt_pk_bf16_f32 v90, v224, v225\n" \
"v_cvt_pk_bf16_f32 v91, v226, v227\n" \
"v_cvt_pk_bf16_f32 v238, v246, v247\n" \
"v_mov_b32_e32 v239, 0x3f80\n" \
"v_cvt_pk_bf16_f32 v234, v244, v245\n" \
"v_mov_b32_e64 v235, 0\n" \
"v_cvt_pk_bf16_f32 v92, v240, v241\n" \
"v_cvt_pk_bf16_f32 v93, v242, v243\n" \
"v_accvgpr_read_b32 v132, a176\n" \
"v_accvgpr_read_b32 v133, a177\n" \
"v_accvgpr_read_b32 v32, a232\n" \
"v_accvgpr_read_b32 v33, a233\n" \
"v_mfma_f32_16x16x32_bf16 a[24:27], v[108:111], v[52:55], a[24:27]\n" \
"v_mfma_f32_16x16x32_bf16 v[192:195], v[188:191], v[184:187], 0\n" \
"v_mfma_f32_16x16x32_bf16 v[148:151], v[180:183], v[184:187], 0\n" \
"v_mfma_f32_16x16x32_bf16 v[208:211], v[204:207], v[200:203], 0\n" \
"v_mfma_f32_16x16x32_bf16 v[152:155], v[188:191], v[200:203], 0\n" \
"v_mfma_f32_16x16x32_bf16 v[224:227], v[220:223], v[216:219], 0\n" \
"v_mfma_f32_16x16x32_bf16 v[156:159], v[204:207], v[216:219], 0\n" \
"v_mfma_f32_16x16x32_bf16 v[240:243], v[236:239], v[232:235], 0\n" \
"v_mfma_f32_16x16x32_bf16 v[160:163], v[220:223], v[232:235], 0\n" \
"v_accvgpr_read_b32 v134, a178\n" \
"v_accvgpr_read_b32 v135, a179\n" \
"v_mul_f32_e32 v132, s70, v132\n" \
"v_mul_f32_e32 v133, s70, v133\n" \
"v_accvgpr_read_b32 v34, a234\n" \
"v_accvgpr_read_b32 v35, a235\n" \
"v_mul_f32_e32 v32, s70, v32\n" \
"v_mul_f32_e32 v33, s70, v33\n" \
"v_accvgpr_read_b32 v136, a180\n" \
"v_accvgpr_read_b32 v137, a181\n" \
"v_accvgpr_read_b32 v138, a182\n" \
"v_accvgpr_read_b32 v139, a183\n" \
"v_mul_f32_e32 v134, s70, v134\n" \
"v_mul_f32_e32 v135, s70, v135\n" \
"v_cvt_pk_bf16_f32 v132, v132, v133\n" \
"v_accvgpr_read_b32 v36, a236\n" \
"v_accvgpr_read_b32 v37, a237\n" \
"v_accvgpr_read_b32 v38, a238\n" \
"v_accvgpr_read_b32 v39, a239\n" \
"v_mul_f32_e32 v34, s70, v34\n" \
"v_mul_f32_e32 v35, s70, v35\n" \
"v_cvt_pk_bf16_f32 v32, v32, v33\n" \
"v_mul_f32_e32 v136, s70, v136\n" \
"v_mul_f32_e32 v137, s70, v137\n" \
"v_mul_f32_e32 v138, s70, v138\n" \
"v_mul_f32_e32 v139, s70, v139\n" \
"v_cvt_pk_bf16_f32 v133, v134, v135\n" \
"v_mul_f32_e32 v36, s70, v36\n" \
"v_mul_f32_e32 v37, s70, v37\n" \
"v_mul_f32_e32 v38, s70, v38\n" \
"v_mul_f32_e32 v39, s70, v39\n" \
"v_cvt_pk_bf16_f32 v33, v34, v35\n" \
"v_cvt_pk_bf16_f32 v134, v136, v137\n" \
"v_cvt_pk_bf16_f32 v135, v138, v139\n" \
"v_cvt_pk_bf16_f32 v34, v36, v37\n" \
"v_cvt_pk_bf16_f32 v35, v38, v39\n" \
"v_mfma_f32_16x16x32_bf16 a[24:27], v[172:175], v[116:119], a[24:27]\n" \
"ds_write_b128 v21, v[132:135] offset:20480\n" \
"ds_write_b128 v21, v[32:35] offset:21504\n" \
"v_accvgpr_read_b32 v132, a72\n" \
"v_accvgpr_read_b32 v133, a73\n" \
"v_accvgpr_read_b32 v32, a104\n" \
"v_accvgpr_read_b32 v33, a105\n" \
"v_accvgpr_read_b32 v140, a136\n" \
"v_accvgpr_read_b32 v141, a137\n" \
"v_accvgpr_read_b32 v134, a74\n" \
"v_accvgpr_read_b32 v135, a75\n" \
"v_mul_f32_e32 v132, s70, v132\n" \
"v_mul_f32_e32 v133, s70, v133\n" \
"v_accvgpr_read_b32 v34, a106\n" \
"v_accvgpr_read_b32 v35, a107\n" \
"v_mul_f32_e32 v32, s70, v32\n" \
"v_mul_f32_e32 v33, s70, v33\n" \
"v_accvgpr_read_b32 v142, a138\n" \
"v_accvgpr_read_b32 v143, a139\n" \
"v_mul_f32_e32 v140, s70, v140\n" \
"v_mul_f32_e32 v141, s70, v141\n" \
"v_mfma_f32_16x16x32_bf16 a[28:31], v[112:115], v[52:55], a[28:31]\n" \
"v_accvgpr_read_b32 v136, a76\n" \
"v_accvgpr_read_b32 v137, a77\n" \
"v_accvgpr_read_b32 v138, a78\n" \
"v_accvgpr_read_b32 v139, a79\n" \
"v_mul_f32_e32 v134, s70, v134\n" \
"v_mul_f32_e32 v135, s70, v135\n" \
"v_cvt_pk_bf16_f32 v132, v132, v133\n" \
"v_accvgpr_read_b32 v36, a108\n" \
"v_accvgpr_read_b32 v37, a109\n" \
"v_accvgpr_read_b32 v38, a110\n" \
"v_accvgpr_read_b32 v39, a111\n" \
"v_mul_f32_e32 v34, s70, v34\n" \
"v_mul_f32_e32 v35, s70, v35\n" \
"v_cvt_pk_bf16_f32 v32, v32, v33\n" \
"v_accvgpr_read_b32 v144, a140\n" \
"v_accvgpr_read_b32 v145, a141\n" \
"v_accvgpr_read_b32 v146, a142\n" \
"v_accvgpr_read_b32 v147, a143\n" \
"v_mul_f32_e32 v142, s70, v142\n" \
"v_mul_f32_e32 v143, s70, v143\n" \
"v_cvt_pk_bf16_f32 v140, v140, v141\n" \
"s_mov_b32 s64, 0xaaaaaaaa\n" \
"s_mov_b32 s65, 0xaaaaaaaa\n" \
"v_mul_f32_e32 v136, s70, v136\n" \
"v_mul_f32_e32 v137, s70, v137\n" \
"v_mul_f32_e32 v138, s70, v138\n" \
"v_mul_f32_e32 v139, s70, v139\n" \
"v_cvt_pk_bf16_f32 v133, v134, v135\n" \
"v_mul_f32_e32 v36, s70, v36\n" \
"v_mul_f32_e32 v37, s70, v37\n" \
"v_mul_f32_e32 v38, s70, v38\n" \
"v_mul_f32_e32 v39, s70, v39\n" \
"v_cvt_pk_bf16_f32 v33, v34, v35\n" \
"v_mul_f32_e32 v144, s70, v144\n" \
"v_mul_f32_e32 v145, s70, v145\n" \
"v_mul_f32_e32 v146, s70, v146\n" \
"v_mul_f32_e32 v147, s70, v147\n" \
"v_cvt_pk_bf16_f32 v141, v142, v143\n" \
"s_mov_b32 s66, 0xfff0ffff\n" \
"s_mov_b32 s67, 0xf000ff00\n" \
"s_mov_b32 s68, 0xffc0fffc\n" \
"s_mov_b32 s69, 0xc000fc00\n" \
"v_cndmask_b32_e64 v192, v192, v193, s[64:65]\n" \
"v_cndmask_b32_e64 v148, v148, v149, s[64:65]\n" \
"v_cndmask_b32_e64 v194, v194, v195, s[64:65]\n" \
"v_cndmask_b32_e64 v150, v150, v151, s[64:65]\n" \
"v_cndmask_b32_e64 v208, v208, v209, s[64:65]\n" \
"v_cndmask_b32_e64 v152, v152, v153, s[64:65]\n" \
"v_cndmask_b32_e64 v210, v210, v211, s[64:65]\n" \
"v_cndmask_b32_e64 v154, v154, v155, s[64:65]\n" \
"v_cndmask_b32_e64 v224, v224, v225, s[64:65]\n" \
"v_cndmask_b32_e64 v156, v156, v157, s[64:65]\n" \
"v_cndmask_b32_e64 v226, v226, v227, s[64:65]\n" \
"v_cndmask_b32_e64 v158, v158, v159, s[64:65]\n" \
"v_cndmask_b32_e64 v240, v240, v241, s[64:65]\n" \
"v_cndmask_b32_e64 v160, v160, v161, s[64:65]\n" \
"v_cndmask_b32_e64 v242, v242, v243, s[64:65]\n" \
"v_cndmask_b32_e64 v162, v162, v163, s[64:65]\n" \
"v_cvt_pk_bf16_f32 v134, v136, v137\n" \
"v_cvt_pk_bf16_f32 v135, v138, v139\n" \
"v_cvt_pk_bf16_f32 v34, v36, v37\n" \
"v_cvt_pk_bf16_f32 v35, v38, v39\n" \
"v_cvt_pk_bf16_f32 v142, v144, v145\n" \
"v_cvt_pk_bf16_f32 v143, v146, v147\n" \
"v_cndmask_b32_e64 v192, v148, v192, s[66:67]\n" \
"v_cndmask_b32_e64 v194, v150, v194, s[68:69]\n" \
"v_cndmask_b32_e64 v208, v152, v208, s[66:67]\n" \
"v_cndmask_b32_e64 v210, v154, v210, s[68:69]\n" \
"v_cndmask_b32_e64 v224, v156, v224, s[66:67]\n" \
"v_cndmask_b32_e64 v226, v158, v226, s[68:69]\n" \
"v_cndmask_b32_e64 v240, v160, v240, s[66:67]\n" \
"v_cndmask_b32_e64 v242, v162, v242, s[68:69]\n" \
"ds_write_b128 v21, v[132:135] offset:24576\n" \
"ds_write_b128 v21, v[32:35] offset:25600\n" \
"ds_write_b128 v21, v[140:143] offset:28672\n" \
"v_mfma_f32_16x16x32_bf16 a[28:31], v[176:179], v[116:119], a[28:31]\n" \
"v_max_f32_e32 v193, v192, v194\n" \
"v_max_f32_e32 v209, v208, v210\n" \
"v_max_f32_e32 v225, v224, v226\n" \
"v_max_f32_e32 v241, v240, v242\n" \
"s_mov_b32 s71, 0x3e0293ee\n" \
"v_permlane32_swap_b32_e32 v193, v225\n" \
"v_permlane32_swap_b32_e32 v209, v241\n" \
"v_accvgpr_read_b32 v132, a168\n" \
"v_accvgpr_read_b32 v133, a169\n" \
"v_accvgpr_read_b32 v32, a80\n" \
"v_accvgpr_read_b32 v33, a81\n" \
"v_accvgpr_read_b32 v140, a88\n" \
"v_accvgpr_read_b32 v141, a89\n" \
"v_accvgpr_read_b32 v124, a112\n" \
"v_accvgpr_read_b32 v125, a113\n" \
"v_permlane32_swap_b32_e32 v196, v228\n" \
"v_permlane32_swap_b32_e32 v212, v244\n" \
"v_max_f32_e32 v193, v193, v225\n" \
"v_max_f32_e32 v209, v209, v241\n" \
"v_accvgpr_read_b32 v134, a170\n" \
"v_accvgpr_read_b32 v135, a171\n" \
"v_mul_f32_e32 v132, s70, v132\n" \
"v_mul_f32_e32 v133, s70, v133\n" \
"v_accvgpr_read_b32 v34, a82\n" \
"v_accvgpr_read_b32 v35, a83\n" \
"v_pk_mul_f32 v[32:33], v[32:33], s[70:71]\n" \
"v_accvgpr_read_b32 v142, a90\n" \
"v_accvgpr_read_b32 v143, a91\n" \
"v_pk_mul_f32 v[140:141], v[140:141], s[70:71]\n" \
"v_accvgpr_read_b32 v126, a114\n" \
"v_accvgpr_read_b32 v127, a115\n" \
"v_pk_mul_f32 v[124:125], v[124:125], s[70:71]\n" \
"v_accvgpr_read_b32 v48, a40\n" \
"v_accvgpr_read_b32 v49, a41\n" \
"v_permlane16_swap_b32_e32 v228, v244\n" \
"v_permlane16_swap_b32_e32 v193, v209\n" \
"v_accvgpr_read_b32 v136, a172\n" \
"v_accvgpr_read_b32 v137, a173\n" \
"v_accvgpr_read_b32 v138, a174\n" \
"v_accvgpr_read_b32 v139, a175\n" \
"v_mul_f32_e32 v134, s70, v134\n" \
"v_mul_f32_e32 v135, s70, v135\n" \
"v_cvt_pk_bf16_f32 v132, v132, v133\n" \
"v_accvgpr_read_b32 v36, a84\n" \
"v_accvgpr_read_b32 v37, a85\n" \
"v_accvgpr_read_b32 v38, a86\n" \
"v_accvgpr_read_b32 v39, a87\n" \
"v_pk_mul_f32 v[34:35], v[34:35], s[70:71]\n" \
"v_cvt_pk_bf16_f32 v32, v32, v33\n" \
"v_accvgpr_read_b32 v144, a92\n" \
"v_accvgpr_read_b32 v145, a93\n" \
"v_accvgpr_read_b32 v146, a94\n" \
"v_accvgpr_read_b32 v147, a95\n" \
"v_pk_mul_f32 v[142:143], v[142:143], s[70:71]\n" \
"v_cvt_pk_bf16_f32 v140, v140, v141\n" \
"v_accvgpr_read_b32 v128, a116\n" \
"v_accvgpr_read_b32 v129, a117\n" \
"v_accvgpr_read_b32 v130, a118\n" \
"v_accvgpr_read_b32 v131, a119\n" \
"v_pk_mul_f32 v[126:127], v[126:127], s[70:71]\n" \
"v_cvt_pk_bf16_f32 v124, v124, v125\n" \
"v_mfma_f32_16x16x32_bf16 a[60:63], v[112:115], v[56:59], a[60:63]\n" \
"s_mov_b32 s72, 0x3eae1a93\n" \
"v_mul_f32_e32 v45, 0xbfb8aa3b, v228\n" \
"v_mfma_f32_16x16x16_bf16 v[48:51], v[182:183], v[46:47], 0\n" \
"v_mfma_f32_16x16x16_bf16 v[196:199], v[190:191], v[46:47], 0\n" \
"v_mfma_f32_16x16x16_bf16 v[212:215], v[206:207], v[46:47], 0\n" \
"v_max_f32_e32 v193, v193, v209\n" \
"v_mul_f32_e32 v136, s70, v136\n" \
"v_mul_f32_e32 v137, s70, v137\n" \
"v_mul_f32_e32 v138, s70, v138\n" \
"v_mul_f32_e64 v139, s70, v139\n" \
"v_cvt_pk_bf16_f32 v133, v134, v135\n" \
"v_pk_mul_f32 v[36:37], v[36:37], s[70:71]\n" \
"v_pk_mul_f32 v[38:39], v[38:39], s[70:71]\n" \
"v_cvt_pk_bf16_f32 v33, v34, v35\n" \
"v_pk_mul_f32 v[144:145], v[144:145], s[70:71]\n" \
"v_pk_mul_f32 v[146:147], v[146:147], s[70:71]\n" \
"v_cvt_pk_bf16_f32 v141, v142, v143\n" \
"v_pk_mul_f32 v[128:129], v[128:129], s[70:71]\n" \
"v_pk_mul_f32 v[130:131], v[130:131], s[70:71]\n" \
"v_cvt_pk_bf16_f32 v125, v126, v127\n" \
"v_mfma_f32_16x16x16_bf16 v[228:231], v[222:223], v[46:47], 0\n" \
"v_mul_f32_e64 v193, s72, v193\n" \
"v_cvt_pk_bf16_f32 v134, v136, v137\n" \
"v_cvt_pk_bf16_f32 v135, v138, v139\n" \
"v_cvt_pk_bf16_f32 v34, v36, v37\n" \
"v_cvt_pk_bf16_f32 v35, v38, v39\n" \
"v_cvt_pk_bf16_f32 v142, v144, v145\n" \
"v_cvt_pk_bf16_f32 v143, v146, v147\n" \
"v_cvt_pk_bf16_f32 v126, v128, v129\n" \
"v_cvt_pk_bf16_f32 v127, v130, v131\n" \
"v_mfma_f32_16x16x16_bf16 v[244:247], v[238:239], v[46:47], 0\n" \
"v_mov_b32_e64 v209, v193\n" \
"v_accvgpr_read_b32 v60, a120\n" \
"ds_write_b128 v21, v[132:135] offset:29696\n" \
"ds_write_b128 v27, v[32:35] offset:8192\n" \
"ds_write_b128 v27, v[140:143] offset:9216\n" \
"ds_write_b128 v27, v[124:127] offset:12288\n" \
"v_permlane16_swap_b32_e32 v193, v209\n" \
"v_mfma_f32_16x16x32_bf16 a[60:63], v[176:179], v[120:123], a[60:63]\n" \
"v_mov_b32_e32 v225, v193\n" \
"v_mov_b32_e64 v241, v209\n" \
"v_accvgpr_read_b32 v61, a121\n" \
"v_accvgpr_read_b32 v132, a16\n" \
"v_accvgpr_read_b32 v133, a17\n" \
"v_accvgpr_read_b32 v32, a24\n" \
"v_accvgpr_read_b32 v33, a25\n" \
"v_permlane32_swap_b32_e32 v193, v225\n" \
"v_permlane32_swap_b32_e32 v209, v241\n" \
"v_accvgpr_read_b32 v62, a122\n" \
"v_accvgpr_read_b32 v63, a123\n" \
"v_pk_mul_f32 v[60:61], v[60:61], s[70:71]\n" \
"v_accvgpr_read_b32 v134, a18\n" \
"v_accvgpr_read_b32 v135, a19\n" \
"v_pk_mul_f32 v[132:133], v[132:133], s[70:71]\n" \
"v_accvgpr_read_b32 v34, a26\n" \
"v_accvgpr_read_b32 v35, a27\n" \
"v_pk_mul_f32 v[32:33], v[32:33], s[70:71]\n" \
"v_fma_f32 v192, v192, s72, -v193\n" \
"v_fma_f32 v194, v194, s72, -v193\n" \
"v_fma_f32 v208, v208, s72, -v209\n" \
"v_fma_f32 v210, v210, s72, -v209\n" \
"v_accvgpr_read_b32 v64, a124\n" \
"v_accvgpr_read_b32 v65, a125\n" \
"v_accvgpr_read_b32 v66, a126\n" \
"v_accvgpr_read_b32 v67, a127\n" \
"v_pk_mul_f32 v[62:63], v[62:63], s[70:71]\n" \
"v_cvt_pk_bf16_f32 v60, v60, v61\n" \
"v_accvgpr_read_b32 v136, a20\n" \
"v_accvgpr_read_b32 v137, a21\n" \
"v_accvgpr_read_b32 v138, a22\n" \
"v_accvgpr_read_b32 v139, a23\n" \
"v_pk_mul_f32 v[134:135], v[134:135], s[70:71]\n" \
"v_cvt_pk_bf16_f32 v132, v132, v133\n" \
"v_accvgpr_read_b32 v36, a28\n" \
"v_accvgpr_read_b32 v37, a29\n" \
"v_accvgpr_read_b32 v38, a30\n" \
"v_accvgpr_read_b32 v39, a31\n" \
"v_pk_mul_f32 v[34:35], v[34:35], s[70:71]\n" \
"v_cvt_pk_bf16_f32 v32, v32, v33\n" \
"v_accvgpr_read_b32 v128, a60\n" \
"v_accvgpr_read_b32 v129, a61\n" \
"v_accvgpr_read_b32 v130, a62\n" \
"v_accvgpr_read_b32 v131, a63\n" \
"v_exp_f32_e32 v192, v192\n" \
"v_exp_f32_e32 v194, v194\n" \
"v_exp_f32_e32 v208, v208\n" \
"v_exp_f32_e32 v210, v210\n" \
"v_fma_f32 v224, v224, s72, -v225\n" \
"v_fma_f32 v226, v226, s72, -v225\n" \
"v_pk_mul_f32 v[64:65], v[64:65], s[70:71]\n" \
"v_pk_mul_f32 v[66:67], v[66:67], s[70:71]\n" \
"v_cvt_pk_bf16_f32 v61, v62, v63\n" \
"v_pk_mul_f32 v[136:137], v[136:137], s[70:71]\n" \
"v_pk_mul_f32 v[138:139], v[138:139], s[70:71]\n" \
"v_cvt_pk_bf16_f32 v133, v134, v135\n" \
"v_pk_mul_f32 v[36:37], v[36:37], s[70:71]\n" \
"v_pk_mul_f32 v[38:39], v[38:39], s[70:71]\n" \
"v_cvt_pk_bf16_f32 v33, v34, v35\n" \
"v_pk_mul_f32 v[128:129], v[128:129], s[70:71]\n" \
"v_pk_mul_f32 v[130:131], v[130:131], s[70:71]\n" \
"v_mfma_f32_16x16x32_bf16 a[56:59], v[108:111], v[56:59], a[56:59]\n" \
"v_exp_f32_e32 v224, v224\n" \
"v_exp_f32_e32 v226, v226\n" \
"v_fma_f32 v240, v240, s72, -v241\n" \
"v_fma_f32 v242, v242, s72, -v241\n" \
"v_cvt_pk_bf16_f32 v62, v64, v65\n" \
"v_cvt_pk_bf16_f32 v63, v66, v67\n" \
"v_cvt_pk_bf16_f32 v134, v136, v137\n" \
"v_cvt_pk_bf16_f32 v135, v138, v139\n" \
"v_cvt_pk_bf16_f32 v34, v36, v37\n" \
"v_cvt_pk_bf16_f32 v35, v38, v39\n" \
"v_cvt_pk_bf16_f32 v192, v192, v194\n" \
"v_cvt_pk_bf16_f32 v208, v208, v210\n" \
"v_exp_f32_e32 v240, v240\n" \
"v_exp_f32_e32 v242, v242\n" \
"ds_write_b128 v27, v[60:63] offset:13312\n" \
"ds_write_b128 v27, v[132:135] offset:0\n" \
"ds_write_b128 v27, v[32:35] offset:1024\n" \
"v_cvt_pk_bf16_f32 v68, v48, v49\n" \
"v_cvt_pk_bf16_f32 v69, v50, v51\n" \
"v_cvt_pk_bf16_f32 v70, v196, v197\n" \
"v_cvt_pk_bf16_f32 v71, v198, v199\n" \
"v_cvt_pk_bf16_f32 v72, v212, v213\n" \
"v_cvt_pk_bf16_f32 v73, v214, v215\n" \
"v_perm_b32 v184, v192, v192, v40\n" \
"v_perm_b32 v185, v192, v192, v41\n" \
"v_perm_b32 v186, v192, v192, v42\n" \
"v_perm_b32 v187, v192, v192, v43\n" \
"v_perm_b32 v200, v208, v208, v40\n" \
"v_perm_b32 v201, v208, v208, v41\n" \
"v_perm_b32 v202, v208, v208, v42\n" \
"v_perm_b32 v203, v208, v208, v43\n" \
"v_cvt_pk_bf16_f32 v224, v224, v226\n" \
"v_cvt_pk_bf16_f32 v74, v228, v229\n" \
"v_cvt_pk_bf16_f32 v75, v230, v231\n" \
"v_mfma_f32_16x16x32_bf16 v[148:151], v[68:71], v[184:187], 0\n" \
"v_mfma_f32_16x16x32_bf16 v[152:155], v[70:73], v[200:203], 0\n" \
"v_perm_b32 v216, v224, v224, v40\n" \
"v_perm_b32 v217, v224, v224, v41\n" \
"v_perm_b32 v218, v224, v224, v42\n" \
"v_perm_b32 v219, v224, v224, v43\n" \
"v_cvt_pk_bf16_f32 v240, v240, v242\n" \
"v_mfma_f32_16x16x32_bf16 a[56:59], v[172:175], v[120:123], a[56:59]\n" \
"v_cvt_pk_bf16_f32 v76, v244, v245\n" \
"v_cvt_pk_bf16_f32 v77, v246, v247\n" \
"v_mfma_f32_16x16x32_bf16 v[156:159], v[72:75], v[216:219], 0\n" \
"v_perm_b32 v232, v240, v240, v40\n" \
"v_perm_b32 v233, v240, v240, v41\n" \
"v_perm_b32 v234, v240, v240, v42\n" \
"v_perm_b32 v235, v240, v240, v43\n" \
"v_mfma_f32_16x16x32_bf16 a[52:55], v[104:107], v[56:59], a[52:55]\n" \
"v_accvgpr_read_b32 v124, a56\n" \
"v_accvgpr_read_b32 v125, a57\n" \
"v_accvgpr_read_b32 v126, a58\n" \
"v_accvgpr_read_b32 v127, a59\n" \
"v_pk_mul_f32 v[124:125], v[124:125], s[70:71]\n" \
"v_pk_mul_f32 v[126:127], v[126:127], s[70:71]\n" \
"v_cvt_pk_bf16_f32 v124, v124, v125\n" \
"v_cvt_pk_bf16_f32 v125, v126, v127\n" \
"v_cvt_pk_bf16_f32 v126, v128, v129\n" \
"v_cvt_pk_bf16_f32 v127, v130, v131\n" \
"ds_write_b128 v27, v[124:127] offset:5120\n" \
"v_mfma_f32_16x16x32_bf16 v[160:163], v[74:77], v[232:235], 0\n" \
"v_accvgpr_read_b32 v252, a12\n" \
"v_accvgpr_read_b32 v253, a13\n" \
"v_accvgpr_read_b32 v254, a14\n" \
"v_accvgpr_read_b32 v255, a15\n" \
"v_mfma_f32_16x16x32_bf16 a[52:55], v[168:171], v[120:123], a[52:55]\n" \
"v_cvt_pk_bf16_f32 v84, v252, v253\n" \
"v_cvt_pk_bf16_f32 v85, v254, v255\n" \
"v_exp_f32_e64 v45, v45\n" \
"v_mfma_f32_16x16x32_bf16 v[208:211], v[86:89], v[200:203], 0\n" \
"v_mfma_f32_16x16x32_bf16 v[192:195], v[84:87], v[184:187], 0\n" \
"v_mfma_f32_16x16x32_bf16 v[224:227], v[88:91], v[216:219], 0\n" \
"v_mfma_f32_16x16x32_bf16 v[240:243], v[90:93], v[232:235], 0\n" \
"v_mov_b32_dpp v150, v154 quad_perm:[0,1,2,3] row_mask:0x2 bank_mask:0xf\n" \
"v_accvgpr_read_b32 v144, a52\n" \
"v_accvgpr_read_b32 v145, a53\n" \
"v_accvgpr_read_b32 v146, a54\n" \
"v_accvgpr_read_b32 v147, a55\n" \
"v_pk_mul_f32 v[144:145], v[144:145], s[70:71]\n" \
"v_pk_mul_f32 v[146:147], v[146:147], s[70:71]\n" \
"v_mov_b32_dpp v150, v158 quad_perm:[0,1,2,3] row_mask:0x4 bank_mask:0xf\n" \
"v_add_f32_e64 v45, 1.0, v45\n" \
"v_mfma_f32_16x16x32_bf16 a[48:51], v[100:103], v[56:59], a[48:51]\n" \
"v_mov_b32_dpp v150, v162 quad_perm:[0,1,2,3] row_mask:0x8 bank_mask:0xf\n" \
"v_mov_b32_dpp v148, v152 quad_perm:[0,1,2,3] row_mask:0x2 bank_mask:0xf\n" \
"v_rcp_f32_e64 v45, v45\n" \
"v_mov_b32_dpp v149, v153 quad_perm:[0,1,2,3] row_mask:0x2 bank_mask:0xf\n" \
"v_rcp_f32_e64 v150, v150\n" \
"v_mov_b32_dpp v148, v156 quad_perm:[0,1,2,3] row_mask:0x4 bank_mask:0xf\n" \
"v_mov_b32_dpp v149, v157 quad_perm:[0,1,2,3] row_mask:0x4 bank_mask:0xf\n" \
"v_mul_f32_e64 v150, v45, v150\n" \
"v_mov_b32_dpp v148, v160 quad_perm:[0,1,2,3] row_mask:0x8 bank_mask:0xf\n" \
"v_mov_b32_dpp v149, v161 quad_perm:[0,1,2,3] row_mask:0x8 bank_mask:0xf\n" \
"v_mul_f32_e32 v148, v150, v148\n" \
"v_mul_f32_e32 v149, v150, v149\n" \
"v_mov_b32_e64 v154, v150\n" \
"v_mfma_f32_16x16x32_bf16 a[48:51], v[164:167], v[120:123], a[48:51]\n" \
"v_sub_f32_e32 v44, 1.0, v45\n" \
"v_permlane16_swap_b32_e32 v150, v154\n" \
"v_mov_b32_e32 v158, v150\n" \
"v_mov_b32_e32 v162, v154\n" \
"v_cvt_pk_bf16_f32 v148, v148, v149\n" \
"v_permlane32_swap_b32_e32 v150, v158\n" \
"v_permlane32_swap_b32_e32 v154, v162\n" \
"v_mul_f32_e64 v192, v150, v192\n" \
"v_accvgpr_read_b32 v140, a48\n" \
"v_accvgpr_read_b32 v141, a49\n" \
"v_accvgpr_read_b32 v142, a50\n" \
"v_accvgpr_read_b32 v143, a51\n" \
"v_pk_mul_f32 v[140:141], v[140:141], s[70:71]\n" \
"v_pk_mul_f32 v[142:143], v[142:143], s[70:71]\n" \
"v_cvt_pk_bf16_f32 v140, v140, v141\n" \
"v_cvt_pk_bf16_f32 v141, v142, v143\n" \
"v_cvt_pk_bf16_f32 v142, v144, v145\n" \
"v_cvt_pk_bf16_f32 v143, v146, v147\n" \
"ds_write_b128 v27, v[140:143] offset:4096\n" \
"v_mul_f32_e32 v193, v150, v193\n" \
"v_mul_f32_e32 v208, v154, v208\n" \
"v_mul_f32_e32 v209, v154, v209\n" \
"v_mul_f32_e32 v224, v158, v224\n" \
"v_mul_f32_e32 v225, v158, v225\n" \
"v_mul_f32_e32 v240, v162, v240\n" \
"v_mul_f32_e32 v241, v162, v241\n" \
"v_mul_f32_e32 v194, v150, v194\n" \
"v_mul_f32_e64 v195, v150, v195\n" \
"v_cvt_pk_bf16_f32 v192, v192, v193\n" \
"v_mul_f32_e32 v210, v154, v210\n" \
"v_mul_f32_e32 v211, v154, v211\n" \
"v_cvt_pk_bf16_f32 v208, v208, v209\n" \
"v_mul_f32_e32 v226, v158, v226\n" \
"v_mul_f32_e32 v227, v158, v227\n" \
"v_cvt_pk_bf16_f32 v224, v224, v225\n" \
"v_mul_f32_e32 v242, v162, v242\n" \
"v_mul_f32_e32 v243, v162, v243\n" \
"v_cvt_pk_bf16_f32 v240, v240, v241\n" \
"v_cvt_pk_bf16_f32 v193, v194, v195\n" \
"s_add_u32 s84, s48, 0x0\n" \
"s_nop 0\n" \
"v_cvt_pk_bf16_f32 v209, v210, v211\n" \
"s_add_u32 s85, s48, 0x24800\n" \
"v_cvt_pk_bf16_f32 v225, v226, v227\n" \
"s_add_u32 s86, s48, 0x49000\n" \
"v_cvt_pk_bf16_f32 v241, v242, v243\n" \
"s_add_u32 s87, s48, 0x6d800\n" \
"ds_write_b32 v26, v44\n" \
"buffer_store_dword v148, v25, s[80:83], s49 offen sc1\n" \
"buffer_store_dwordx2 v[192:193], v24, s[80:83], s84 offen sc1\n" \
"buffer_store_dwordx2 v[208:209], v24, s[80:83], s85 offen sc1\n" \
"buffer_store_dwordx2 v[224:225], v24, s[80:83], s86 offen sc1\n" \
"buffer_store_dwordx2 v[240:241], v24, s[80:83], s87 offen sc1\n" \
"s_branch L_end_%=\n" \
"L_poll61_%=:\n" \
"s_mov_b32 s84, 0\n" \
"L_pl61_%=:\n" \
"s_sleep 1\n" \
"v_bfe_u32 v44, v20, 2, 5\n" \
"buffer_load_dword v44, v44, s[80:83], s38 offen offset:128 sc1\n" \
"s_waitcnt vmcnt(0)\n" \
"v_cmp_ne_u32_e32 vcc, s62, v44\n" \
"s_add_u32 s84, s84, 1\n" \
"s_nop 3\n" \
"s_cbranch_vccz L_pollr61_%=\n" \
"s_cmp_gt_u32 s84, 0x400000\n" \
"s_cbranch_scc0 L_pl61_%=\n" \
"s_branch L_pollr61_%=\n" \
"L_poll161_%=:\n" \
"s_mov_b32 s84, 0\n" \
"L_pl161_%=:\n" \
"s_sleep 1\n" \
"v_bfe_u32 v44, v20, 2, 5\n" \
"buffer_load_dword v44, v44, s[80:83], s38 offen offset:0 sc1\n" \
"s_waitcnt vmcnt(0)\n" \
"v_cmp_ne_u32_e32 vcc, s62, v44\n" \
"s_add_u32 s84, s84, 1\n" \
"s_nop 3\n" \
"s_cbranch_vccz L_pollr161_%=\n" \
"s_cmp_gt_u32 s84, 0x400000\n" \
"s_cbranch_scc0 L_pl161_%=\n" \
"s_branch L_pollr161_%=\n" \
"L_end_%=:\n"
#define PROJ_VCLOB "v21","v24","v25","v28","v29","v32","v33","v34","v35","v36","v37","v38","v39","v40","v41","v42","v43","v44","v45","v46","v47","v48","v49","v50","v51","v52","v53","v54","v55","v56","v57","v58","v59","v60","v61","v62","v63","v64","v65","v66","v67","v68","v69","v70","v71","v72","v73","v74","v75","v76","v77","v78","v79","v80","v81","v82","v83","v84","v85","v86","v87","v88","v89","v90","v91","v92","v93","v94","v95","v96","v97","v98","v99","v100","v101","v102","v103","v104","v105","v106","v107","v108","v109","v110","v111","v112","v113","v114","v115","v116","v117","v118","v119","v120","v121","v122","v123","v124","v125","v126","v127","v128","v129","v130","v131","v132","v133","v134","v135","v136","v137","v138","v139","v140","v141","v142","v143","v144","v145","v146","v147","v148","v149","v150","v151","v152","v153","v154","v155","v156","v157","v158","v159","v160","v161","v162","v163","v164","v165","v166","v167","v168","v169","v170","v171","v172","v173","v174","v175","v176","v177","v178","v179","v180","v181","v182","v183","v184","v185","v186","v187","v188","v189","v190","v192","v193","v194","v195","v196","v197","v198","v199","v200","v201","v202","v203","v204","v205","v206","v207","v208","v209","v210","v211","v212","v213","v214","v215","v216","v217","v218","v219","v220","v221","v222","v223","v224","v225","v226","v227","v228","v229","v230","v231","v232","v233","v234","v235","v236","v237","v238","v239","v240","v241","v242","v243","v244","v245","v246","v247","v248","v249","v250","v251","v252","v253","v254","v255"
#define PROJ_SCLOB "s4","s5","s6","s7","s32","s33","s34","s35","s36","s37","s38","s39","s40","s41","s42","s43","s44","s45","s46","s47","s48","s49","s50","s51","s52","s53","s54","s55","s63","s64","s65","s66","s67","s68","s69","s70","s71","s72","s73","s74","s75","s76","s77","s78","s79","s80","s81","s82","s83","s84","s85","s86","s87","s88","s89","s90","s91","s92","s93","s94","s95","s96","s97","s98","s99","s100","s101"

typedef const u16* const __attribute__((address_space(4)))* kaptr;
__device__ __forceinline__ void proj_phase(kaptr& ka, char*& ws, const unsigned wid, u32x4 (&vp)[5], unsigned& vl, u32x4 (&sa)[5], u32x2 s24, u32x2 s28, unsigned tdum, u32x4& srdA, u32x2& sd, unsigned& tag
    ) {
    const unsigned b = blockIdx.x, h = (b & 7) * 4 + ((b >> 3) & 3), mt = b >> 5;
    const unsigned rb = (wid < 2u ? 15u - mt : mt) * 128u;
    const unsigned lane = threadIdx.x & 63u, wc = wid & 1u;
    u32x2 vAc, vB;
#pragma unroll
    for (int par = 0; par < 2; ++par) {
        const unsigned lr = lane >> 3, cc = (lane & 7u) ^ (4u * par + (lr >> 1)), r0 = rb + wc * 64u + lr;
        vAc[par] = r0 * (DC * 2) + cc * 16u;
        const unsigned br = lane >> 4, p16 = lane & 15u, sc = (((p16 >> 1) ^ (br | (par << 2))) << 1) | (p16 & 1u);
        vB[par] = (16u * wid + br) * (NBIG * 2) + h * (HD * 2) + sc * 16u;
    }
    vl = lane * 16u;
    u32x2 s30 = {wid * 8192u, 65536u + wid * 4096u};
    asm volatile(PROJ_ASM
                 : "={s62}"(tag), "={s[56:59]}"(srdA), "={s[60:61]}"(sd), "+{s[8:11]}"(sa[0]), "+{s[12:15]}"(sa[1]), "+{s[16:19]}"(sa[2]), "+{s[20:23]}"(sa[3]), "+{s[24:25]}"(s24), "+{s[28:29]}"(s28), "+{s[30:31]}"(s30),
                   "={v[0:3]}"(vp[0]), "={v[4:7]}"(vp[1]), "={v[8:11]}"(vp[2]), "={v[12:15]}"(vp[3]), "={v[16:19]}"(vp[4]), "+{v[22:23]}"(vAc), "+{v[26:27]}"(vB), "+{v191}"(tdum), "+{s[26:27]}"(ws), "+{s[0:1]}"(ka)
                 : "{s2}"(b), "{s3}"(wid), "{v20}"(vl)
                 : "memory", "m0", "vcc", "scc", PROJ_VCLOB, PROJ_SCLOB, ACLOB_LO, ACLOB_HI);
    sa[4] = (u32x4){s24[0], 0u, 0u, 0u};
}

namespace att {
#define DI __device__ __forceinline__
typedef __bf16 bfx8 __attribute__((ext_vector_type(8)));
DI unsigned rfl(unsigned x) { return __builtin_amdgcn_readfirstlane(x); }
DI f32x4 mfma(u32x4 a, u32x4 b, f32x4 c) { return __builtin_amdgcn_mfma_f32_16x16x32_bf16(__builtin_bit_cast(bfx8, a), __builtin_bit_cast(bfx8, b), c, 0, 0, 0); }
typedef __bf16 bfx2 __attribute__((ext_vector_type(2)));
typedef float f32x2 __attribute__((ext_vector_type(2)));
DI unsigned cvtpk2(float lo, float hi) { f32x2 v = {lo, hi}; bfx2 b = __builtin_convertvector(v, bfx2); return __builtin_bit_cast(unsigned, b); }
DI void att_exact(const u16* __restrict__ x, const u16* __restrict__ wq, char* __restrict__ ws, unsigned h, unsigned r0, float g1) {
    const unsigned lane = lane_id(), g = lane >> 4, c = lane & 15u;
    f32x4 o[8];
#pragma unroll
    for (int n = 0; n < 8; ++n) o[n] = (f32x4){0.f, 0.f, 0.f, 0.f};
#pragma unroll 1
    for (unsigned kk = 0; kk < (unsigned)DM; kk += 32u) {
        const unsigned k = kk + 8u * g;
        const u32x4 xf = *reinterpret_cast<const u32x4*>(x + (r0 + c) * DM + k);
#pragma unroll
        for (int n = 0; n < 8; ++n) {
            const u16* w = wq + k * NBIG + h * HD + n * 16 + c; u32x4 wf;
#pragma unroll
            for (int e = 0; e < 4; ++e) wf[e] = (unsigned)w[2 * e * NBIG] | ((unsigned)w[(2 * e + 1) * NBIG] << 16);
            o[n] = mfma(wf, xf, o[n]);
        }
    }
    u32x4 q[4];
#pragma unroll
    for (int p = 0; p < 4; ++p) {
        const f32x4 a = o[2 * p] * 0.12751743082f, b = o[2 * p + 1] * 0.12751743082f;
        q[p] = (u32x4){cvtpk2(a[0], a[1]), cvtpk2(a[2], a[3]), cvtpk2(b[0], b[1]), cvtpk2(b[2], b[3])};
    }
#pragma unroll
    for (int n = 0; n < 8; ++n) o[n] = (f32x4){0.f, 0.f, 0.f, 0.f};
    const __amdgpu_buffer_rsrc_t srd = __builtin_amdgcn_make_buffer_rsrc((void*)(ws + WS_K + (size_t)h * 1048576), (short)0, 1048576, 0x00020000);
    float m = -3.0e38f, l = 0.f;
#pragma unroll 1
    for (unsigned k0 = 0; k0 <= r0 + 15u; k0 += 32u) {
        const unsigned tb = (k0 >> 6) * 32768u + (k0 & 32u) * 256u;
        f32x4 s[2]; float mx = m;
#pragma unroll
        for (int kt = 0; kt < 2; ++kt) {
            s[kt] = (f32x4){0.f, 0.f, 0.f, 0.f};
#pragma unroll
            for (int p = 0; p < 4; ++p) s[kt] = mfma(__builtin_amdgcn_raw_buffer_load_b128(srd, lane * 16u + (kt * 4 + p) * 1024u, tb, 16), q[p], s[kt]);
#pragma unroll
            for (int e = 0; e < 4; ++e) { if (k0 + 16u * kt + 4u * g + e > r0 + c) s[kt][e] = -3.0e38f; mx = fmaxf(mx, s[kt][e]); }
        }
        mx = fmaxf(mx, __shfl_xor(mx, 16)); mx = fmaxf(mx, __shfl_xor(mx, 32));
        const float al = __builtin_amdgcn_exp2f(m - mx); m = mx;
        u32x4 pf;
#pragma unroll
        for (int e = 0; e < 4; ++e) pf[e] = cvtpk2(__builtin_amdgcn_exp2f(s[e >> 1][2 * (e & 1)] - m), __builtin_amdgcn_exp2f(s[e >> 1][2 * (e & 1) + 1] - m));
        l *= al;
#pragma unroll
        for (int e = 0; e < 4; ++e) l += __uint_as_float(pf[e] << 16) + __uint_as_float(pf[e] & 0xffff0000u);
#pragma unroll
        for (int n = 0; n < 8; ++n) o[n] = mfma(__builtin_amdgcn_raw_buffer_load_b128(srd, lane * 16u + 16384u + n * 1024u, tb, 16), pf, o[n] * al);
    }
    l += __shfl_xor(l, 16); l += __shfl_xor(l, 32);
    const float f = g1 / l;
    const __amdgpu_buffer_rsrc_t srdA = __builtin_amdgcn_make_buffer_rsrc((void*)(ws + WS_AF), (short)0, 128 * KS_TOT * 1024, 0x00020000);
#pragma unroll
    for (int p = 0; p < 4; ++p) {
        const f32x4 a = o[2 * p] * f, b = o[2 * p + 1] * f;
        __builtin_amdgcn_raw_buffer_store_b128((u32x4){cvtpk2(a[0], a[1]), cvtpk2(a[2], a[3]), cvtpk2(b[0], b[1]), cvtpk2(b[2], b[3])}, srdA, lane * 16u + p * 1024u, ((r0 >> 4) * KS_TOT + 18u + 4u * h) * 1024u, 16);
    }
}
}

#define ATT_ASM \
"s_mov_b32 s101, 0\n" \
"s_waitcnt vmcnt(15) lgkmcnt(0)\n" \
"s_barrier\n" \
"v_accvgpr_write_b32 a216, 0\n" \
"v_accvgpr_write_b32 a217, 0\n" \
"v_accvgpr_write_b32 a218, 0\n" \
"v_accvgpr_write_b32 a219, 0\n" \
"v_bfe_u32 v61, v20, 2, 5\n" \
"s_mov_b32 s96, s24\n" \
"v_mfma_f32_32x32x16_bf16 v[128:143], a[216:219], a[216:219], 0\n" \
"buffer_load_dword v57, v61, s[56:59], s96 offen sc1\n" \
"v_and_b32_e32 v63, 0x300, v20\n" \
"ds_read_b128 v[96:99], v63 offset:0\n" \
"v_add_u32_e32 v58, s22, v20\n" \
"ds_read_b128 a[0:3], v58 offset:0\n" \
"v_mfma_f32_32x32x16_bf16 v[144:159], a[216:219], a[216:219], 0\n" \
"ds_read_b128 a[16:19], v58 offset:8192\n" \
"v_add_u32_e32 v59, s23, v20\n" \
"ds_read_b128 a[32:35], v59 offset:0\n" \
"ds_read_b128 a[48:51], v59 offset:8192\n" \
"ds_read_b128 v[100:103], v63 offset:1024\n" \
"ds_read_b128 a[4:7], v58 offset:1024\n" \
"v_mfma_f32_32x32x16_bf16 v[160:175], a[216:219], a[216:219], 0\n" \
"ds_read_b128 a[20:23], v58 offset:9216\n" \
"ds_read_b128 a[36:39], v59 offset:1024\n" \
"ds_read_b128 a[52:55], v59 offset:9216\n" \
"s_sub_u32 s67, s12, 0xc000\n" \
"ds_read_b128 v[104:107], v63 offset:2048\n" \
"s_mov_b32 s65, s12\n" \
"v_mfma_f32_32x32x16_bf16 v[176:191], a[216:219], a[216:219], 0\n" \
"v_add_u32_e32 v55, s67, v20\n" \
"v_add_u32_e32 v52, 0xc000, v55\n" \
"s_xor_b32 m0, s65, 0x10000\n" \
"ds_read_b128 a[8:11], v58 offset:2048\n" \
"buffer_load_dwordx4 v52, s[8:11], 0 offen offset:0 sc1 lds\n" \
"v_mfma_f32_32x32x16_bf16 v[192:207], a[216:219], a[216:219], 0\n" \
"ds_read_b128 a[24:27], v58 offset:10240\n" \
"ds_read_b128 a[40:43], v59 offset:2048\n" \
"ds_read_b128 a[56:59], v59 offset:10240\n" \
"ds_read_b128 v[108:111], v63 offset:3072\n" \
"buffer_load_dwordx4 v52, s[8:11], 0 offen offset:1024 sc1 lds\n" \
"v_mfma_f32_32x32x16_bf16 v[208:223], a[216:219], a[216:219], 0\n" \
"ds_read_b128 a[12:15], v58 offset:3072\n" \
"ds_read_b128 a[28:31], v58 offset:11264\n" \
"ds_read_b128 a[44:47], v59 offset:3072\n" \
"ds_read_b128 a[60:63], v59 offset:11264\n" \
"s_waitcnt lgkmcnt(15)\n" \
"v_xor_b32_e32 v96, 0x80008000, v96\n" \
"v_mfma_f32_32x32x16_bf16 v[224:239], a[216:219], a[216:219], 0\n" \
"v_xor_b32_e32 v97, 0x80008000, v97\n" \
"v_xor_b32_e32 v98, 0x80008000, v98\n" \
"v_xor_b32_e32 v99, 0x80008000, v99\n" \
"ds_read_b128 a[160:163], v20 offset:0\n" \
"ds_read_b128 a[164:167], v20 offset:1024\n" \
"s_waitcnt lgkmcnt(15)\n" \
"v_xor_b32_e32 v100, 0x80008000, v100\n" \
"v_mfma_f32_32x32x16_bf16 v[240:255], a[216:219], a[216:219], 0\n" \
"v_xor_b32_e32 v101, 0x80008000, v101\n" \
"v_xor_b32_e32 v102, 0x80008000, v102\n" \
"v_xor_b32_e32 v103, 0x80008000, v103\n" \
"ds_read_b128 a[168:171], v20 offset:2048\n" \
"ds_read_b128 a[172:175], v20 offset:3072\n" \
"ds_read_b128 a[188:191], v20 offset:7168\n" \
"v_mfma_f32_16x16x32_bf16 a[192:195], a[216:219], a[216:219], 0\n" \
"ds_read_b128 a[184:187], v20 offset:6144\n" \
"s_waitcnt lgkmcnt(15)\n" \
"v_xor_b32_e32 v104, 0x80008000, v104\n" \
"v_mfma_f32_16x16x32_bf16 a[196:199], a[216:219], a[216:219], 0\n" \
"v_xor_b32_e32 v105, 0x80008000, v105\n" \
"v_xor_b32_e32 v106, 0x80008000, v106\n" \
"v_mfma_f32_16x16x32_bf16 v[32:35], v[96:99], a[0:3], 0\n" \
"v_xor_b32_e32 v107, 0x80008000, v107\n" \
"ds_read_b128 a[180:183], v20 offset:5120\n" \
"v_mfma_f32_16x16x32_bf16 v[36:39], v[96:99], a[16:19], 0\n" \
"ds_read_b128 a[176:179], v20 offset:4096\n" \
"s_waitcnt lgkmcnt(12)\n" \
"v_xor_b32_e32 v108, 0x80008000, v108\n" \
"v_mfma_f32_16x16x32_bf16 v[40:43], v[96:99], a[32:35], 0\n" \
"v_xor_b32_e32 v109, 0x80008000, v109\n" \
"v_xor_b32_e32 v110, 0x80008000, v110\n" \
"v_mfma_f32_16x16x32_bf16 v[44:47], v[96:99], a[48:51], 0\n" \
"v_xor_b32_e32 v111, 0x80008000, v111\n" \
"v_add_u32_e32 v60, s15, v20\n" \
"v_mfma_f32_16x16x32_bf16 v[32:35], v[100:103], a[4:7], v[32:35]\n" \
"v_add_u32_e32 v60, 0x10000, v60\n" \
"ds_read_b128 a[128:131], v20 offset:8192\n" \
"v_mfma_f32_16x16x32_bf16 v[36:39], v[100:103], a[20:23], v[36:39]\n" \
"ds_read_b128 a[144:147], v20 offset:12288\n" \
"ds_read_b128 a[132:135], v20 offset:9216\n" \
"v_mfma_f32_16x16x32_bf16 v[40:43], v[100:103], a[36:39], v[40:43]\n" \
"ds_read_b128 a[148:151], v20 offset:13312\n" \
"ds_read_b128 a[136:139], v20 offset:10240\n" \
"v_mfma_f32_16x16x32_bf16 v[44:47], v[100:103], a[52:55], v[44:47]\n" \
"ds_read_b128 a[152:155], v20 offset:14336\n" \
"ds_read_b128 a[140:143], v20 offset:11264\n" \
"v_mfma_f32_16x16x32_bf16 v[32:35], v[104:107], a[8:11], v[32:35]\n" \
"ds_read_b128 a[156:159], v20 offset:15360\n" \
"ds_read_b128 a[64:67], v60 offset:16384\n" \
"v_mfma_f32_16x16x32_bf16 v[36:39], v[104:107], a[24:27], v[36:39]\n" \
"ds_read_b128 a[68:71], v60 offset:17408\n" \
"ds_read_b128 a[72:75], v60 offset:18432\n" \
"v_mfma_f32_16x16x32_bf16 v[40:43], v[104:107], a[40:43], v[40:43]\n" \
"ds_read_b128 a[76:79], v60 offset:19456\n" \
"ds_read_b128 a[80:83], v60 offset:24576\n" \
"v_mfma_f32_16x16x32_bf16 v[44:47], v[104:107], a[56:59], v[44:47]\n" \
"ds_read_b128 a[84:87], v60 offset:25600\n" \
"s_waitcnt lgkmcnt(15)\n" \
"v_mfma_f32_16x16x32_bf16 v[32:35], v[108:111], a[12:15], v[32:35]\n" \
"ds_read_b128 a[88:91], v60 offset:26624\n" \
"ds_read_b128 a[92:95], v60 offset:27648\n" \
"v_mfma_f32_16x16x32_bf16 v[36:39], v[108:111], a[28:31], v[36:39]\n" \
"ds_read_b128 a[96:99], v60 offset:0\n" \
"ds_read_b128 a[100:103], v60 offset:1024\n" \
"v_mfma_f32_16x16x32_bf16 v[40:43], v[108:111], a[44:47], v[40:43]\n" \
"ds_read_b128 a[104:107], v60 offset:2048\n" \
"ds_read_b128 a[108:111], v60 offset:3072\n" \
"v_mfma_f32_16x16x32_bf16 v[44:47], v[108:111], a[60:63], v[44:47]\n" \
"ds_read_b128 a[112:115], v60 offset:8192\n" \
"ds_read_b128 a[116:119], v60 offset:9216\n" \
"v_mfma_f32_16x16x32_bf16 v[64:67], a[160:163], a[0:3], v[32:35]\n" \
"ds_read_b128 a[120:123], v60 offset:10240\n" \
"ds_read_b128 a[124:127], v60 offset:11264\n" \
"v_mfma_f32_16x16x32_bf16 v[64:67], a[164:167], a[4:7], v[64:67]\n" \
"v_bfe_u32 v26, v20, 8, 2\n" \
"s_mov_b32 m0, s20\n" \
"v_mfma_f32_16x16x32_bf16 v[64:67], a[168:171], a[8:11], v[64:67]\n" \
"v_bfe_u32 v25, v20, 5, 3\n" \
"v_lshlrev_b32_e32 v26, 1, v26\n" \
"v_mfma_f32_16x16x32_bf16 v[64:67], a[172:175], a[12:15], v[64:67]\n" \
"global_load_lds_dword v[16:17], off\n" \
"v_mfma_f32_16x16x32_bf16 v[68:71], a[188:191], a[12:15], v[32:35]\n" \
"v_sub_u32_e32 v25, v25, v26\n" \
"v_and_b32_e32 v26, 16, v20\n" \
"v_mfma_f32_16x16x32_bf16 v[68:71], a[184:187], a[8:11], v[68:71]\n" \
"v_mov_b32_e32 v27, 0x3f80\n" \
"v_lshlrev_b32_e32 v26, v26, v27\n" \
"v_mfma_f32_16x16x32_bf16 v[68:71], a[180:183], a[4:7], v[68:71]\n" \
"v_mov_b32_e32 v54, 0x3f803f80\n" \
"s_waitcnt lgkmcnt(15)\n" \
"v_mfma_f32_16x16x32_bf16 v[68:71], a[176:179], a[0:3], v[68:71]\n" \
"s_mov_b32 s84, 0x33333333\n" \
"s_mov_b32 s85, 0xcccccccc\n" \
"v_mfma_f32_16x16x32_bf16 v[76:79], a[176:179], a[16:19], v[36:39]\n" \
"s_mov_b32 s86, 0x11111111\n" \
"s_mov_b32 s87, 0x11111111\n" \
"v_mfma_f32_16x16x32_bf16 v[76:79], a[180:183], a[20:23], v[76:79]\n" \
"s_mov_b32 s88, 0x44444444\n" \
"s_mov_b32 s89, 0x44444444\n" \
"v_mfma_f32_16x16x32_bf16 v[76:79], a[184:187], a[24:27], v[76:79]\n" \
"v_exp_f32_e32 v70, v70\n" \
"v_mfma_f32_16x16x32_bf16 v[76:79], a[188:191], a[28:31], v[76:79]\n" \
"v_exp_f32_e32 v71, v71\n" \
"v_mfma_f32_16x16x32_bf16 v[72:75], a[172:175], a[28:31], v[36:39]\n" \
"v_exp_f32_e32 v68, v68\n" \
"v_mfma_f32_16x16x32_bf16 v[72:75], a[168:171], a[24:27], v[72:75]\n" \
"v_exp_f32_e32 v69, v69\n" \
"v_mfma_f32_16x16x32_bf16 v[72:75], a[164:167], a[20:23], v[72:75]\n" \
"v_bfe_i32 v21, v20, 4, 1\n" \
"v_cvt_pk_bf16_f32 v71, v70, v71\n" \
"v_mfma_f32_16x16x32_bf16 v[72:75], a[160:163], a[16:19], v[72:75]\n" \
"v_exp_f32_e32 v66, v66\n" \
"v_mfma_f32_16x16x32_bf16 v[80:83], a[160:163], a[32:35], v[40:43]\n" \
"v_exp_f32_e32 v67, v67\n" \
"v_mfma_f32_16x16x32_bf16 v[80:83], a[164:167], a[36:39], v[80:83]\n" \
"v_exp_f32_e32 v64, v64\n" \
"v_mfma_f32_16x16x32_bf16 v[80:83], a[168:171], a[40:43], v[80:83]\n" \
"v_exp_f32_e32 v65, v65\n" \
"v_mfma_f32_16x16x32_bf16 v[80:83], a[172:175], a[44:47], v[80:83]\n" \
"v_exp_f32_e32 v72, v72\n" \
"v_mfma_f32_16x16x32_bf16 v[84:87], a[188:191], a[44:47], v[40:43]\n" \
"s_waitcnt vmcnt(3)\n" \
"v_cmp_ne_u32_e32 vcc, s62, v57\n" \
"v_mfma_f32_16x16x32_bf16 v[84:87], a[184:187], a[40:43], v[84:87]\n" \
"v_exp_f32_e32 v73, v73\n" \
"v_mfma_f32_16x16x32_bf16 v[84:87], a[180:183], a[36:39], v[84:87]\n" \
"v_exp_f32_e32 v74, v74\n" \
"v_mfma_f32_16x16x32_bf16 v[84:87], a[176:179], a[32:35], v[84:87]\n" \
"s_cbranch_vccnz L_skb_%=\n" \
"L_rkb_%=:\n" \
"v_cmp_eq_u32_e32 vcc, 0, v25\n" \
"v_mfma_f32_16x16x32_bf16 v[92:95], a[176:179], a[48:51], v[44:47]\n" \
"v_exp_f32_e32 v75, v75\n" \
"v_mfma_f32_16x16x32_bf16 v[92:95], a[180:183], a[52:55], v[92:95]\n" \
"v_cndmask_b32_e32 v28, 0, v26, vcc\n" \
"v_cvt_pk_bf16_f32 v72, v72, v73\n" \
"v_mfma_f32_16x16x32_bf16 v[92:95], a[184:187], a[56:59], v[92:95]\n" \
"v_exp_f32_e32 v76, v76\n" \
"v_mfma_f32_16x16x32_bf16 v[92:95], a[188:191], a[60:63], v[92:95]\n" \
"v_exp_f32_e32 v77, v77\n" \
"v_mfma_f32_16x16x32_bf16 v[88:91], a[172:175], a[60:63], v[44:47]\n" \
"v_exp_f32_e32 v78, v78\n" \
"v_mfma_f32_16x16x32_bf16 v[88:91], a[168:171], a[56:59], v[88:91]\n" \
"v_exp_f32_e32 v79, v79\n" \
"v_mfma_f32_16x16x32_bf16 v[88:91], a[164:167], a[52:55], v[88:91]\n" \
"v_cndmask_b32_e64 v22, 0, v54, s[84:85]\n" \
"v_cndmask_b32_e64 v23, 0, v54, s[86:87]\n" \
"v_mfma_f32_16x16x32_bf16 v[88:91], a[160:163], a[48:51], v[88:91]\n" \
"v_cndmask_b32_e64 v24, 0, v54, s[88:89]\n" \
"v_and_b32_e32 v21, 0xaaaaaaaa, v21\n" \
"s_waitcnt vmcnt(4) lgkmcnt(0)\n" \
"s_barrier\n" \
"v_cmp_eq_u32_e32 vcc, 1, v25\n" \
"v_cvt_pk_bf16_f32 v70, v68, v69\n" \
"v_cvt_pk_bf16_f32 v73, v74, v75\n" \
"v_mov_b32_e32 v62, 0\n" \
"v_accvgpr_write_b32 a208, 0\n" \
"v_accvgpr_write_b32 a209, 0\n" \
"v_accvgpr_write_b32 a210, 0\n" \
"v_accvgpr_write_b32 a211, 0\n" \
"v_accvgpr_write_b32 a200, v22\n" \
"v_accvgpr_write_b32 a201, v22\n" \
"v_accvgpr_write_b32 a202, v22\n" \
"v_accvgpr_write_b32 a203, v22\n" \
"v_accvgpr_write_b32 a204, v23\n" \
"v_accvgpr_write_b32 a205, v23\n" \
"v_accvgpr_write_b32 a206, v23\n" \
"v_accvgpr_write_b32 a207, v23\n" \
"v_accvgpr_write_b32 a212, v24\n" \
"v_accvgpr_write_b32 a213, v24\n" \
"v_accvgpr_write_b32 a214, v24\n" \
"v_accvgpr_write_b32 a215, v24\n" \
"v_or_b32_e32 v21, 0x44444444, v21\n" \
"s_mov_b32 s64, 0x8000\n" \
"v_add_u32_e32 v50, 0x6000, v55\n" \
"v_add_u32_e32 v51, 0xa000, v55\n" \
"v_add_u32_e32 v53, 0x10000, v55\n" \
"v_add_u32_e32 v48, 0xc000, v20\n" \
"v_add_u32_e32 v49, s17, v20\n" \
"v_cndmask_b32_e32 v29, 0, v26, vcc\n" \
"v_accvgpr_write_b32 a220, v96\n" \
"v_accvgpr_write_b32 a221, v97\n" \
"v_accvgpr_write_b32 a222, v98\n" \
"v_accvgpr_write_b32 a223, v99\n" \
"v_accvgpr_write_b32 a224, v100\n" \
"v_accvgpr_write_b32 a225, v101\n" \
"v_accvgpr_write_b32 a226, v102\n" \
"v_accvgpr_write_b32 a227, v103\n" \
"v_accvgpr_write_b32 a228, v104\n" \
"v_accvgpr_write_b32 a229, v105\n" \
"v_accvgpr_write_b32 a230, v106\n" \
"v_accvgpr_write_b32 a231, v107\n" \
"v_accvgpr_write_b32 a232, v108\n" \
"v_accvgpr_write_b32 a233, v109\n" \
"v_accvgpr_write_b32 a234, v110\n" \
"v_accvgpr_write_b32 a235, v111\n" \
"v_cvt_pk_bf16_f32 v69, v66, v67\n" \
"v_cvt_pk_bf16_f32 v68, v64, v65\n" \
"v_cvt_pk_bf16_f32 v74, v76, v77\n" \
"v_cvt_pk_bf16_f32 v75, v78, v79\n" \
"s_sub_u32 s66, s18, 1\n" \
"s_cbranch_scc1 L_tr_%=\n" \
".p2align 6\n" \
"L_1_%=:\n" \
"v_mfma_f32_16x16x32_bf16 v[96:99], a[128:131], a[0:3], v[32:35]\n" \
"s_xor_b32 m0, s65, 0x12000\n" \
"ds_read_b128 a[160:163], v49 offset:0\n" \
"v_mfma_f32_16x16x32_bf16 v[96:99], a[132:135], a[4:7], v[96:99]\n" \
"buffer_load_dwordx4 v50, s[8:11], s64 offen offset:0 sc1 lds\n" \
"v_mfma_f32_16x16x32_bf16 v[96:99], a[136:139], a[8:11], v[96:99]\n" \
"ds_read_b128 a[164:167], v49 offset:1024\n" \
"ds_read_b128 a[168:171], v49 offset:2048\n" \
"v_mfma_f32_16x16x32_bf16 v[96:99], a[140:143], a[12:15], v[96:99]\n" \
"ds_read_b128 a[172:175], v49 offset:3072\n" \
"ds_read_b128 a[176:179], v49 offset:4096\n" \
"v_mfma_f32_16x16x32_bf16 v[100:103], a[156:159], a[12:15], v[32:35]\n" \
"ds_read_b128 a[180:183], v49 offset:5120\n" \
"ds_read_b128 a[184:187], v49 offset:6144\n" \
"v_mfma_f32_16x16x32_bf16 v[100:103], a[152:155], a[8:11], v[100:103]\n" \
"v_exp_f32_e64 v86, v86\n" \
"v_mfma_f32_16x16x32_bf16 v[100:103], a[148:151], a[4:7], v[100:103]\n" \
"v_exp_f32_e64 v87, v87\n" \
"v_mfma_f32_16x16x32_bf16 v[100:103], a[144:147], a[0:3], v[100:103]\n" \
"ds_read_b128 a[188:191], v49 offset:7168\n" \
"v_cvt_pk_bf16_f32 v87, v86, v87\n" \
"v_mfma_f32_16x16x32_bf16 v[108:111], a[144:147], a[16:19], v[36:39]\n" \
"v_exp_f32_e64 v84, v84\n" \
"v_mfma_f32_16x16x32_bf16 v[108:111], a[148:151], a[20:23], v[108:111]\n" \
"v_exp_f32_e64 v85, v85\n" \
"v_mfma_f32_16x16x32_bf16 v[108:111], a[152:155], a[24:27], v[108:111]\n" \
"v_exp_f32_e64 v82, v82\n" \
"v_mfma_f32_16x16x32_bf16 v[108:111], a[156:159], a[28:31], v[108:111]\n" \
"v_exp_f32_e64 v83, v83\n" \
"v_mfma_f32_16x16x32_bf16 v[104:107], a[140:143], a[28:31], v[36:39]\n" \
"v_exp_f32_e64 v80, v80\n" \
"v_mfma_f32_16x16x32_bf16 v[104:107], a[136:139], a[24:27], v[104:107]\n" \
"v_exp_f32_e64 v81, v81\n" \
"v_mfma_f32_16x16x32_bf16 v[104:107], a[132:135], a[20:23], v[104:107]\n" \
"v_exp_f32_e64 v88, v88\n" \
"v_mfma_f32_16x16x32_bf16 v[104:107], a[128:131], a[16:19], v[104:107]\n" \
"buffer_load_dwordx4 v50, s[8:11], s64 offen offset:1024 sc1 lds\n" \
"v_mfma_f32_16x16x32_bf16 v[112:115], a[128:131], a[32:35], v[40:43]\n" \
"s_xor_b32 m0, s65, 0x6000\n" \
"v_cvt_pk_bf16_f32 v86, v84, v85\n" \
"v_mfma_f32_16x16x32_bf16 v[112:115], a[132:135], a[36:39], v[112:115]\n" \
"v_cvt_pk_bf16_f32 v85, v82, v83\n" \
"v_cvt_pk_bf16_f32 v84, v80, v81\n" \
"v_mfma_f32_16x16x32_bf16 v[112:115], a[136:139], a[40:43], v[112:115]\n" \
"v_exp_f32_e64 v89, v89\n" \
"v_mfma_f32_16x16x32_bf16 v[112:115], a[140:143], a[44:47], v[112:115]\n" \
"v_exp_f32_e64 v90, v90\n" \
"v_mfma_f32_16x16x32_bf16 v[116:119], a[156:159], a[44:47], v[40:43]\n" \
"v_exp_f32_e64 v91, v91\n" \
"v_mfma_f32_16x16x32_bf16 v[116:119], a[152:155], a[40:43], v[116:119]\n" \
"v_exp_f32_e64 v92, v92\n" \
"v_mfma_f32_16x16x32_bf16 v[116:119], a[148:151], a[36:39], v[116:119]\n" \
"v_exp_f32_e64 v93, v93\n" \
"v_mfma_f32_16x16x32_bf16 v[116:119], a[144:147], a[32:35], v[116:119]\n" \
"v_exp_f32_e64 v94, v94\n" \
"v_mfma_f32_16x16x32_bf16 v[124:127], a[144:147], a[48:51], v[44:47]\n" \
"v_cvt_pk_bf16_f32 v88, v88, v89\n" \
"v_cvt_pk_bf16_f32 v89, v90, v91\n" \
"v_mfma_f32_16x16x32_bf16 v[124:127], a[148:151], a[52:55], v[124:127]\n" \
"v_exp_f32_e64 v95, v95\n" \
"v_mfma_f32_16x16x32_bf16 v[124:127], a[152:155], a[56:59], v[124:127]\n" \
"v_cvt_pk_bf16_f32 v90, v92, v93\n" \
"v_cvt_pk_bf16_f32 v91, v94, v95\n" \
"v_mfma_f32_16x16x32_bf16 v[124:127], a[156:159], a[60:63], v[124:127]\n" \
"ds_read_b128 a[144:147], v48 offset:4096\n" \
"ds_read_b128 a[148:151], v48 offset:5120\n" \
"v_mfma_f32_16x16x32_bf16 v[120:123], a[140:143], a[60:63], v[44:47]\n" \
"ds_read_b128 a[152:155], v48 offset:6144\n" \
"ds_read_b128 a[140:143], v48 offset:3072\n" \
"v_mfma_f32_16x16x32_bf16 v[120:123], a[136:139], a[56:59], v[120:123]\n" \
"ds_read_b128 a[136:139], v48 offset:2048\n" \
"ds_read_b128 a[156:159], v48 offset:7168\n" \
"v_mfma_f32_16x16x32_bf16 v[120:123], a[132:135], a[52:55], v[120:123]\n" \
"buffer_load_dwordx4 v51, s[8:11], s64 offen offset:0 sc1 lds\n" \
"v_mfma_f32_16x16x32_bf16 v[120:123], a[128:131], a[48:51], v[120:123]\n" \
"ds_read_b128 a[128:131], v48 offset:0\n" \
"s_waitcnt lgkmcnt(7)\n" \
"v_mfma_f32_16x16x32_bf16 v[128:131], a[160:163], v[68:71], v[128:131]\n" \
"v_exp_f32_e32 v102, v102\n" \
"v_mfma_f32_16x16x32_bf16 v[160:163], a[160:163], v[72:75], v[160:163]\n" \
"v_exp_f32_e64 v103, v103\n" \
"v_mfma_f32_16x16x32_bf16 v[132:135], a[164:167], v[68:71], v[132:135]\n" \
"ds_read_b128 a[132:135], v48 offset:1024\n" \
"v_cvt_pk_bf16_f32 v103, v102, v103\n" \
"v_mfma_f32_16x16x32_bf16 v[164:167], a[164:167], v[72:75], v[164:167]\n" \
"v_exp_f32_e64 v100, v100\n" \
"v_mfma_f32_16x16x32_bf16 v[136:139], a[168:171], v[68:71], v[136:139]\n" \
"v_exp_f32_e64 v101, v101\n" \
"v_mfma_f32_16x16x32_bf16 v[168:171], a[168:171], v[72:75], v[168:171]\n" \
"v_exp_f32_e64 v98, v98\n" \
"v_mfma_f32_16x16x32_bf16 v[140:143], a[172:175], v[68:71], v[140:143]\n" \
"v_exp_f32_e64 v99, v99\n" \
"v_mfma_f32_16x16x32_bf16 v[172:175], a[172:175], v[72:75], v[172:175]\n" \
"v_exp_f32_e64 v96, v96\n" \
"v_mfma_f32_16x16x32_bf16 v[144:147], a[176:179], v[68:71], v[144:147]\n" \
"v_exp_f32_e64 v97, v97\n" \
"v_mfma_f32_16x16x32_bf16 v[176:179], a[176:179], v[72:75], v[176:179]\n" \
"v_exp_f32_e64 v104, v104\n" \
"v_mfma_f32_16x16x32_bf16 v[148:151], a[180:183], v[68:71], v[148:151]\n" \
"v_exp_f32_e64 v105, v105\n" \
"v_mfma_f32_16x16x32_bf16 v[180:183], a[180:183], v[72:75], v[180:183]\n" \
"v_cvt_pk_bf16_f32 v102, v100, v101\n" \
"v_cvt_pk_bf16_f32 v101, v98, v99\n" \
"v_mfma_f32_16x16x32_bf16 v[152:155], a[184:187], v[68:71], v[152:155]\n" \
"v_cvt_pk_bf16_f32 v100, v96, v97\n" \
"v_cvt_pk_bf16_f32 v104, v104, v105\n" \
"v_mfma_f32_16x16x32_bf16 v[184:187], a[184:187], v[72:75], v[184:187]\n" \
"buffer_load_dwordx4 v51, s[8:11], s64 offen offset:1024 sc1 lds\n" \
"v_mfma_f32_16x16x32_bf16 v[156:159], a[188:191], v[68:71], v[156:159]\n" \
"v_exp_f32_e64 v106, v106\n" \
"v_mfma_f32_16x16x32_bf16 v[188:191], a[188:191], v[72:75], v[188:191]\n" \
"v_exp_f32_e64 v107, v107\n" \
"v_smfmac_f32_16x16x64_bf16 a[192:195], a[200:203], v[68:75], v21\n" \
"v_exp_f32_e64 v108, v108\n" \
"v_mfma_f32_16x16x32_bf16 v[220:223], a[188:191], v[84:87], v[220:223]\n" \
"v_exp_f32_e64 v109, v109\n" \
"v_mfma_f32_16x16x32_bf16 v[252:255], a[188:191], v[88:91], v[252:255]\n" \
"v_exp_f32_e64 v110, v110\n" \
"v_mfma_f32_16x16x32_bf16 v[216:219], a[184:187], v[84:87], v[216:219]\n" \
"v_exp_f32_e64 v111, v111\n" \
"v_mfma_f32_16x16x32_bf16 v[248:251], a[184:187], v[88:91], v[248:251]\n" \
"v_exp_f32_e64 v118, v118\n" \
"v_mfma_f32_16x16x32_bf16 v[212:215], a[180:183], v[84:87], v[212:215]\n" \
"v_exp_f32_e64 v119, v119\n" \
"v_mfma_f32_16x16x32_bf16 v[244:247], a[180:183], v[88:91], v[244:247]\n" \
"v_cvt_pk_bf16_f32 v105, v106, v107\n" \
"v_cvt_pk_bf16_f32 v106, v108, v109\n" \
"v_mfma_f32_16x16x32_bf16 v[208:211], a[176:179], v[84:87], v[208:211]\n" \
"v_cvt_pk_bf16_f32 v107, v110, v111\n" \
"v_cvt_pk_bf16_f32 v119, v118, v119\n" \
"v_mfma_f32_16x16x32_bf16 v[240:243], a[176:179], v[88:91], v[240:243]\n" \
"v_exp_f32_e64 v116, v116\n" \
"v_mfma_f32_16x16x32_bf16 v[204:207], a[172:175], v[84:87], v[204:207]\n" \
"v_exp_f32_e64 v117, v117\n" \
"v_mfma_f32_16x16x32_bf16 v[236:239], a[172:175], v[88:91], v[236:239]\n" \
"v_exp_f32_e64 v114, v114\n" \
"v_mfma_f32_16x16x32_bf16 v[200:203], a[168:171], v[84:87], v[200:203]\n" \
"v_exp_f32_e64 v115, v115\n" \
"v_mfma_f32_16x16x32_bf16 v[232:235], a[168:171], v[88:91], v[232:235]\n" \
"v_exp_f32_e64 v112, v112\n" \
"v_mfma_f32_16x16x32_bf16 v[196:199], a[164:167], v[84:87], v[196:199]\n" \
"v_exp_f32_e64 v113, v113\n" \
"v_mfma_f32_16x16x32_bf16 v[228:231], a[164:167], v[88:91], v[228:231]\n" \
"v_exp_f32_e64 v120, v120\n" \
"v_mfma_f32_16x16x32_bf16 v[192:195], a[160:163], v[84:87], v[192:195]\n" \
"v_exp_f32_e64 v121, v121\n" \
"v_mfma_f32_16x16x32_bf16 v[224:227], a[160:163], v[88:91], v[224:227]\n" \
"v_cvt_pk_bf16_f32 v118, v116, v117\n" \
"v_cvt_pk_bf16_f32 v117, v114, v115\n" \
"v_smfmac_f32_16x16x64_bf16 a[196:199], a[200:203], v[84:91], v21\n" \
"v_cvt_pk_bf16_f32 v116, v112, v113\n" \
"v_cvt_pk_bf16_f32 v120, v120, v121\n" \
"s_waitcnt vmcnt(4) lgkmcnt(0)\n" \
"s_barrier\n" \
"v_mfma_f32_16x16x32_bf16 v[64:67], a[128:131], a[0:3], v[32:35]\n" \
"s_xor_b32 m0, s65, 0x14000\n" \
"ds_read_b128 a[160:163], v49 offset:8192\n" \
"v_mfma_f32_16x16x32_bf16 v[64:67], a[132:135], a[4:7], v[64:67]\n" \
"buffer_load_dwordx4 v52, s[8:11], s64 offen offset:0 sc1 lds\n" \
"v_mfma_f32_16x16x32_bf16 v[64:67], a[136:139], a[8:11], v[64:67]\n" \
"ds_read_b128 a[164:167], v49 offset:9216\n" \
"ds_read_b128 a[168:171], v49 offset:10240\n" \
"v_mfma_f32_16x16x32_bf16 v[64:67], a[140:143], a[12:15], v[64:67]\n" \
"ds_read_b128 a[172:175], v49 offset:11264\n" \
"ds_read_b128 a[176:179], v49 offset:12288\n" \
"v_mfma_f32_16x16x32_bf16 v[68:71], a[156:159], a[12:15], v[32:35]\n" \
"ds_read_b128 a[180:183], v49 offset:13312\n" \
"ds_read_b128 a[184:187], v49 offset:14336\n" \
"v_mfma_f32_16x16x32_bf16 v[68:71], a[152:155], a[8:11], v[68:71]\n" \
"v_exp_f32_e64 v122, v122\n" \
"v_mfma_f32_16x16x32_bf16 v[68:71], a[148:151], a[4:7], v[68:71]\n" \
"v_exp_f32_e64 v123, v123\n" \
"v_mfma_f32_16x16x32_bf16 v[68:71], a[144:147], a[0:3], v[68:71]\n" \
"ds_read_b128 a[188:191], v49 offset:15360\n" \
"v_cvt_pk_bf16_f32 v121, v122, v123\n" \
"v_mfma_f32_16x16x32_bf16 v[76:79], a[144:147], a[16:19], v[36:39]\n" \
"v_exp_f32_e64 v124, v124\n" \
"v_mfma_f32_16x16x32_bf16 v[76:79], a[148:151], a[20:23], v[76:79]\n" \
"v_exp_f32_e64 v125, v125\n" \
"v_mfma_f32_16x16x32_bf16 v[76:79], a[152:155], a[24:27], v[76:79]\n" \
"v_exp_f32_e64 v126, v126\n" \
"v_mfma_f32_16x16x32_bf16 v[76:79], a[156:159], a[28:31], v[76:79]\n" \
"v_exp_f32_e64 v127, v127\n" \
"v_mfma_f32_16x16x32_bf16 v[72:75], a[140:143], a[28:31], v[36:39]\n" \
"v_cvt_pk_bf16_f32 v122, v124, v125\n" \
"v_cvt_pk_bf16_f32 v123, v126, v127\n" \
"v_mfma_f32_16x16x32_bf16 v[72:75], a[136:139], a[24:27], v[72:75]\n" \
"v_exp_f32_e64 v70, v70\n" \
"v_mfma_f32_16x16x32_bf16 v[72:75], a[132:135], a[20:23], v[72:75]\n" \
"v_exp_f32_e64 v71, v71\n" \
"v_mfma_f32_16x16x32_bf16 v[72:75], a[128:131], a[16:19], v[72:75]\n" \
"buffer_load_dwordx4 v52, s[8:11], s64 offen offset:1024 sc1 lds\n" \
"v_mfma_f32_16x16x32_bf16 v[80:83], a[128:131], a[32:35], v[40:43]\n" \
"s_mov_b32 m0, s65\n" \
"v_cvt_pk_bf16_f32 v71, v70, v71\n" \
"v_mfma_f32_16x16x32_bf16 v[80:83], a[132:135], a[36:39], v[80:83]\n" \
"v_exp_f32_e32 v68, v68\n" \
"v_mfma_f32_16x16x32_bf16 v[80:83], a[136:139], a[40:43], v[80:83]\n" \
"v_exp_f32_e64 v69, v69\n" \
"v_mfma_f32_16x16x32_bf16 v[80:83], a[140:143], a[44:47], v[80:83]\n" \
"v_exp_f32_e64 v72, v72\n" \
"v_mfma_f32_16x16x32_bf16 v[84:87], a[156:159], a[44:47], v[40:43]\n" \
"v_exp_f32_e64 v73, v73\n" \
"v_mfma_f32_16x16x32_bf16 v[84:87], a[152:155], a[40:43], v[84:87]\n" \
"v_exp_f32_e64 v74, v74\n" \
"v_mfma_f32_16x16x32_bf16 v[84:87], a[148:151], a[36:39], v[84:87]\n" \
"v_exp_f32_e64 v75, v75\n" \
"v_mfma_f32_16x16x32_bf16 v[84:87], a[144:147], a[32:35], v[84:87]\n" \
"v_exp_f32_e64 v66, v66\n" \
"v_mfma_f32_16x16x32_bf16 v[92:95], a[144:147], a[48:51], v[44:47]\n" \
"ds_read_b128 a[144:147], v48 offset:12288\n" \
"v_cvt_pk_bf16_f32 v72, v72, v73\n" \
"v_mfma_f32_16x16x32_bf16 v[92:95], a[148:151], a[52:55], v[92:95]\n" \
"ds_read_b128 a[148:151], v48 offset:13312\n" \
"v_cvt_pk_bf16_f32 v70, v68, v69\n" \
"v_mfma_f32_16x16x32_bf16 v[92:95], a[152:155], a[56:59], v[92:95]\n" \
"ds_read_b128 a[152:155], v48 offset:14336\n" \
"v_cvt_pk_bf16_f32 v73, v74, v75\n" \
"v_mfma_f32_16x16x32_bf16 v[92:95], a[156:159], a[60:63], v[92:95]\n" \
"ds_read_b128 a[156:159], v48 offset:15360\n" \
"v_add_u32_e32 v49, 0x10000, v48\n" \
"v_mfma_f32_16x16x32_bf16 v[88:91], a[140:143], a[60:63], v[44:47]\n" \
"ds_read_b128 a[140:143], v48 offset:11264\n" \
"s_xor_b32 s65, s65, 0x4000\n" \
"v_mfma_f32_16x16x32_bf16 v[88:91], a[136:139], a[56:59], v[88:91]\n" \
"v_exp_f32_e64 v67, v67\n" \
"v_mfma_f32_16x16x32_bf16 v[88:91], a[132:135], a[52:55], v[88:91]\n" \
"buffer_load_dwordx4 v53, s[8:11], s64 offen offset:0 sc1 lds\n" \
"v_mfma_f32_16x16x32_bf16 v[88:91], a[128:131], a[48:51], v[88:91]\n" \
"ds_read_b128 a[128:131], v48 offset:8192\n" \
"s_waitcnt lgkmcnt(6)\n" \
"v_mfma_f32_16x16x32_bf16 v[128:131], a[160:163], v[100:103], v[128:131]\n" \
"ds_read_b128 a[132:135], v48 offset:9216\n" \
"ds_read_b128 a[136:139], v48 offset:10240\n" \
"v_mfma_f32_16x16x32_bf16 v[160:163], a[160:163], v[104:107], v[160:163]\n" \
"v_exp_f32_e32 v64, v64\n" \
"v_mfma_f32_16x16x32_bf16 v[132:135], a[164:167], v[100:103], v[132:135]\n" \
"v_exp_f32_e64 v65, v65\n" \
"v_mfma_f32_16x16x32_bf16 v[164:167], a[164:167], v[104:107], v[164:167]\n" \
"v_exp_f32_e64 v76, v76\n" \
"v_mfma_f32_16x16x32_bf16 v[136:139], a[168:171], v[100:103], v[136:139]\n" \
"v_exp_f32_e64 v77, v77\n" \
"v_mfma_f32_16x16x32_bf16 v[168:171], a[168:171], v[104:107], v[168:171]\n" \
"v_exp_f32_e64 v78, v78\n" \
"v_mfma_f32_16x16x32_bf16 v[140:143], a[172:175], v[100:103], v[140:143]\n" \
"v_exp_f32_e64 v79, v79\n" \
"v_mfma_f32_16x16x32_bf16 v[172:175], a[172:175], v[104:107], v[172:175]\n" \
"v_cvt_pk_bf16_f32 v69, v66, v67\n" \
"v_cvt_pk_bf16_f32 v68, v64, v65\n" \
"v_mfma_f32_16x16x32_bf16 v[144:147], a[176:179], v[100:103], v[144:147]\n" \
"v_cvt_pk_bf16_f32 v74, v76, v77\n" \
"v_cvt_pk_bf16_f32 v75, v78, v79\n" \
"v_mfma_f32_16x16x32_bf16 v[176:179], a[176:179], v[104:107], v[176:179]\n" \
"v_xor_b32_e32 v48, 0x4000, v48\n" \
"v_mfma_f32_16x16x32_bf16 v[148:151], a[180:183], v[100:103], v[148:151]\n" \
"v_mfma_f32_16x16x32_bf16 v[180:183], a[180:183], v[104:107], v[180:183]\n" \
"v_mfma_f32_16x16x32_bf16 v[152:155], a[184:187], v[100:103], v[152:155]\n" \
"v_mfma_f32_16x16x32_bf16 v[184:187], a[184:187], v[104:107], v[184:187]\n" \
"buffer_load_dwordx4 v53, s[8:11], s64 offen offset:1024 sc1 lds\n" \
"v_mfma_f32_16x16x32_bf16 v[156:159], a[188:191], v[100:103], v[156:159]\n" \
"s_add_u32 s64, s64, 0x8000\n" \
"s_sub_u32 s66, s66, 1\n" \
"s_nop 0\n" \
"v_mfma_f32_16x16x32_bf16 v[188:191], a[188:191], v[104:107], v[188:191]\n" \
"v_smfmac_f32_16x16x64_bf16 a[192:195], a[200:203], v[100:107], v21\n" \
"v_mfma_f32_16x16x32_bf16 v[220:223], a[188:191], v[116:119], v[220:223]\n" \
"v_mfma_f32_16x16x32_bf16 v[252:255], a[188:191], v[120:123], v[252:255]\n" \
"v_mfma_f32_16x16x32_bf16 v[216:219], a[184:187], v[116:119], v[216:219]\n" \
"v_mfma_f32_16x16x32_bf16 v[248:251], a[184:187], v[120:123], v[248:251]\n" \
"v_mfma_f32_16x16x32_bf16 v[212:215], a[180:183], v[116:119], v[212:215]\n" \
"v_mfma_f32_16x16x32_bf16 v[244:247], a[180:183], v[120:123], v[244:247]\n" \
"v_mfma_f32_16x16x32_bf16 v[208:211], a[176:179], v[116:119], v[208:211]\n" \
"v_mfma_f32_16x16x32_bf16 v[240:243], a[176:179], v[120:123], v[240:243]\n" \
"v_mfma_f32_16x16x32_bf16 v[204:207], a[172:175], v[116:119], v[204:207]\n" \
"v_mfma_f32_16x16x32_bf16 v[236:239], a[172:175], v[120:123], v[236:239]\n" \
"v_mfma_f32_16x16x32_bf16 v[200:203], a[168:171], v[116:119], v[200:203]\n" \
"v_mfma_f32_16x16x32_bf16 v[232:235], a[168:171], v[120:123], v[232:235]\n" \
"v_mfma_f32_16x16x32_bf16 v[196:199], a[164:167], v[116:119], v[196:199]\n" \
"v_mfma_f32_16x16x32_bf16 v[228:231], a[164:167], v[120:123], v[228:231]\n" \
"v_mfma_f32_16x16x32_bf16 v[192:195], a[160:163], v[116:119], v[192:195]\n" \
"v_mfma_f32_16x16x32_bf16 v[224:227], a[160:163], v[120:123], v[224:227]\n" \
"v_smfmac_f32_16x16x64_bf16 a[196:199], a[200:203], v[116:123], v21\n" \
"s_waitcnt vmcnt(4) lgkmcnt(0)\n" \
"s_barrier\n" \
"s_cbranch_scc0 L_1_%=\n" \
"L_tr_%=:\n" \
"s_cmp_lg_u32 s16, 0\n" \
"s_cbranch_scc1 L_sw_%=\n" \
"v_mfma_f32_16x16x32_bf16 v[96:99], a[128:131], a[0:3], v[32:35]\n" \
"v_mov_b32_e32 v56, s62\n" \
"v_readlane_b32 s68, v11, 0\n" \
"v_mfma_f32_16x16x32_bf16 v[96:99], a[132:135], a[4:7], v[96:99]\n" \
"s_xor_b32 m0, s65, 0x12000\n" \
"s_mov_b32 s65, s13\n" \
"v_mfma_f32_16x16x32_bf16 v[96:99], a[136:139], a[8:11], v[96:99]\n" \
"buffer_load_dwordx4 v50, s[8:11], s64 offen offset:0 sc1 lds\n" \
"v_mfma_f32_16x16x32_bf16 v[96:99], a[140:143], a[12:15], v[96:99]\n" \
"v_add_u32_e32 v63, s15, v20\n" \
"s_add_u32 s67, s14, 40960\n" \
"v_mfma_f32_16x16x32_bf16 v[100:103], a[156:159], a[12:15], v[32:35]\n" \
"buffer_load_dwordx4 v50, s[8:11], s64 offen offset:1024 sc1 lds\n" \
"s_mov_b64 exec, 1\n" \
"buffer_store_dword v56, off, s[56:59], s68 offset:0 sc1\n" \
"s_mov_b64 exec, -1\n" \
"v_mfma_f32_16x16x32_bf16 v[100:103], a[152:155], a[8:11], v[100:103]\n" \
"v_add_u32_e32 v50, 0x6000, v63\n" \
"s_xor_b32 m0, s65, 0x8000\n" \
"v_mfma_f32_16x16x32_bf16 v[100:103], a[148:151], a[4:7], v[100:103]\n" \
"ds_read_b128 a[160:163], v49 offset:0\n" \
"ds_read_b128 a[164:167], v49 offset:1024\n" \
"v_mfma_f32_16x16x32_bf16 v[100:103], a[144:147], a[0:3], v[100:103]\n" \
"ds_read_b128 a[168:171], v49 offset:2048\n" \
"ds_read_b128 a[172:175], v49 offset:3072\n" \
"v_mfma_f32_16x16x32_bf16 v[108:111], a[144:147], a[16:19], v[36:39]\n" \
"buffer_load_dwordx4 v50, s[8:11], s67 offen offset:0 sc1 lds\n" \
"v_mfma_f32_16x16x32_bf16 v[108:111], a[148:151], a[20:23], v[108:111]\n" \
"ds_read_b128 a[176:179], v49 offset:4096\n" \
"ds_read_b128 a[180:183], v49 offset:5120\n" \
"v_mfma_f32_16x16x32_bf16 v[108:111], a[152:155], a[24:27], v[108:111]\n" \
"ds_read_b128 a[184:187], v49 offset:6144\n" \
"ds_read_b128 a[188:191], v49 offset:7168\n" \
"v_mfma_f32_16x16x32_bf16 v[108:111], a[156:159], a[28:31], v[108:111]\n" \
"s_mov_b32 s64, s14\n" \
"v_add_u32_e32 v51, 0x12000, v63\n" \
"v_mfma_f32_16x16x32_bf16 v[104:107], a[140:143], a[28:31], v[36:39]\n" \
"buffer_load_dwordx4 v50, s[8:11], s67 offen offset:1024 sc1 lds\n" \
"v_mfma_f32_16x16x32_bf16 v[104:107], a[136:139], a[24:27], v[104:107]\n" \
"v_exp_f32_e32 v86, v86\n" \
"v_mfma_f32_16x16x32_bf16 v[104:107], a[132:135], a[20:23], v[104:107]\n" \
"v_exp_f32_e32 v87, v87\n" \
"v_mfma_f32_16x16x32_bf16 v[104:107], a[128:131], a[16:19], v[104:107]\n" \
"v_exp_f32_e32 v84, v84\n" \
"v_mfma_f32_16x16x32_bf16 v[112:115], a[128:131], a[32:35], v[40:43]\n" \
"v_exp_f32_e32 v85, v85\n" \
"v_mfma_f32_16x16x32_bf16 v[112:115], a[132:135], a[36:39], v[112:115]\n" \
"buffer_load_dwordx4 v50, s[8:11], s67 offen offset:2048 sc1 lds\n" \
"v_mfma_f32_16x16x32_bf16 v[112:115], a[136:139], a[40:43], v[112:115]\n" \
"v_cvt_pk_bf16_f32 v87, v86, v87\n" \
"v_cvt_pk_bf16_f32 v86, v84, v85\n" \
"v_mfma_f32_16x16x32_bf16 v[112:115], a[140:143], a[44:47], v[112:115]\n" \
"v_exp_f32_e32 v82, v82\n" \
"v_mfma_f32_16x16x32_bf16 v[116:119], a[156:159], a[44:47], v[40:43]\n" \
"v_exp_f32_e32 v83, v83\n" \
"v_mfma_f32_16x16x32_bf16 v[116:119], a[152:155], a[40:43], v[116:119]\n" \
"buffer_load_dwordx4 v50, s[8:11], s67 offen offset:3072 sc1 lds\n" \
"v_mfma_f32_16x16x32_bf16 v[116:119], a[148:151], a[36:39], v[116:119]\n" \
"s_xor_b32 m0, s65, 0xa000\n" \
"v_cvt_pk_bf16_f32 v85, v82, v83\n" \
"v_mfma_f32_16x16x32_bf16 v[116:119], a[144:147], a[32:35], v[116:119]\n" \
"v_exp_f32_e32 v80, v80\n" \
"v_mfma_f32_16x16x32_bf16 v[124:127], a[144:147], a[48:51], v[44:47]\n" \
"v_exp_f32_e32 v81, v81\n" \
"v_mfma_f32_16x16x32_bf16 v[124:127], a[148:151], a[52:55], v[124:127]\n" \
"buffer_load_dwordx4 v51, s[8:11], s64 offen offset:0 sc1 lds\n" \
"v_mfma_f32_16x16x32_bf16 v[124:127], a[152:155], a[56:59], v[124:127]\n" \
"v_cvt_pk_bf16_f32 v84, v80, v81\n" \
"v_readlane_b32 s93, v11, 19\n" \
"v_mfma_f32_16x16x32_bf16 v[124:127], a[156:159], a[60:63], v[124:127]\n" \
"v_exp_f32_e32 v88, v88\n" \
"v_mfma_f32_16x16x32_bf16 v[120:123], a[140:143], a[60:63], v[44:47]\n" \
"v_exp_f32_e32 v89, v89\n" \
"v_mfma_f32_16x16x32_bf16 v[120:123], a[136:139], a[56:59], v[120:123]\n" \
"v_exp_f32_e32 v90, v90\n" \
"v_mfma_f32_16x16x32_bf16 v[120:123], a[132:135], a[52:55], v[120:123]\n" \
"buffer_load_dwordx4 v51, s[8:11], s64 offen offset:1024 sc1 lds\n" \
"v_mfma_f32_16x16x32_bf16 v[120:123], a[128:131], a[48:51], v[120:123]\n" \
"v_cvt_pk_bf16_f32 v88, v88, v89\n" \
"s_waitcnt lgkmcnt(0)\n" \
"v_mfma_f32_16x16x32_bf16 v[128:131], a[160:163], v[68:71], v[128:131]\n" \
"v_exp_f32_e32 v91, v91\n" \
"v_mfma_f32_16x16x32_bf16 v[160:163], a[160:163], v[72:75], v[160:163]\n" \
"v_exp_f32_e32 v92, v92\n" \
"v_mfma_f32_16x16x32_bf16 v[132:135], a[164:167], v[68:71], v[132:135]\n" \
"buffer_load_dwordx4 v51, s[8:11], s64 offen offset:2048 sc1 lds\n" \
"v_mfma_f32_16x16x32_bf16 v[164:167], a[164:167], v[72:75], v[164:167]\n" \
"v_exp_f32_e32 v93, v93\n" \
"v_mfma_f32_16x16x32_bf16 v[136:139], a[168:171], v[68:71], v[136:139]\n" \
"v_exp_f32_e32 v94, v94\n" \
"v_mfma_f32_16x16x32_bf16 v[168:171], a[168:171], v[72:75], v[168:171]\n" \
"v_exp_f32_e32 v95, v95\n" \
"v_mfma_f32_16x16x32_bf16 v[140:143], a[172:175], v[68:71], v[140:143]\n" \
"buffer_load_dwordx4 v51, s[8:11], s64 offen offset:3072 sc1 lds\n" \
"v_mfma_f32_16x16x32_bf16 v[172:175], a[172:175], v[72:75], v[172:175]\n" \
"v_cvt_pk_bf16_f32 v89, v90, v91\n" \
"v_cvt_pk_bf16_f32 v90, v92, v93\n" \
"v_mfma_f32_16x16x32_bf16 v[144:147], a[176:179], v[68:71], v[144:147]\n" \
"v_cvt_pk_bf16_f32 v91, v94, v95\n" \
"v_readlane_b32 s80, v11, 11\n" \
"v_mfma_f32_16x16x32_bf16 v[176:179], a[176:179], v[72:75], v[176:179]\n" \
"v_readlane_b32 s81, v11, 12\n" \
"s_mov_b32 s82, 0x47fca0\n" \
"v_mfma_f32_16x16x32_bf16 v[148:151], a[180:183], v[68:71], v[148:151]\n" \
"s_mov_b32 s83, 0x20000\n" \
"v_readlane_b32 s92, v11, 25\n" \
"v_mfma_f32_16x16x32_bf16 v[180:183], a[180:183], v[72:75], v[180:183]\n" \
"s_add_u32 m0, s93, 0x0\n" \
"ds_read_b128 a[128:131], v48 offset:0\n" \
"v_mfma_f32_16x16x32_bf16 v[152:155], a[184:187], v[68:71], v[152:155]\n" \
"ds_read_b128 a[144:147], v48 offset:4096\n" \
"ds_read_b128 a[132:135], v48 offset:1024\n" \
"v_mfma_f32_16x16x32_bf16 v[184:187], a[184:187], v[72:75], v[184:187]\n" \
"buffer_load_dwordx4 v0, s[80:83], s92 offen offset:0 lds\n" \
"v_mfma_f32_16x16x32_bf16 v[156:159], a[188:191], v[68:71], v[156:159]\n" \
"ds_read_b128 a[148:151], v48 offset:5120\n" \
"ds_read_b128 a[136:139], v48 offset:2048\n" \
"v_mfma_f32_16x16x32_bf16 v[188:191], a[188:191], v[72:75], v[188:191]\n" \
"ds_read_b128 a[152:155], v48 offset:6144\n" \
"ds_read_b128 a[140:143], v48 offset:3072\n" \
"v_smfmac_f32_16x16x64_bf16 a[192:195], a[200:203], v[68:75], v21\n" \
"ds_read_b128 a[156:159], v48 offset:7168\n" \
"v_add_u32_e32 v52, 0x14000, v63\n" \
"v_mfma_f32_16x16x32_bf16 v[220:223], a[188:191], v[84:87], v[220:223]\n" \
"buffer_load_dwordx4 v1, s[80:83], s92 offen offset:1024 lds\n" \
"v_mfma_f32_16x16x32_bf16 v[252:255], a[188:191], v[88:91], v[252:255]\n" \
"v_add_u32_e32 v53, 0x20000, v63\n" \
"s_mov_b32 s66, s19\n" \
"v_mfma_f32_16x16x32_bf16 v[216:219], a[184:187], v[84:87], v[216:219]\n" \
"buffer_load_dwordx4 v2, s[80:83], s92 offen offset:2048 lds\n" \
"v_mfma_f32_16x16x32_bf16 v[248:251], a[184:187], v[88:91], v[248:251]\n" \
"v_exp_f32_e32 v102, v102\n" \
"v_mfma_f32_16x16x32_bf16 v[212:215], a[180:183], v[84:87], v[212:215]\n" \
"v_exp_f32_e32 v103, v103\n" \
"v_mfma_f32_16x16x32_bf16 v[244:247], a[180:183], v[88:91], v[244:247]\n" \
"v_exp_f32_e32 v100, v100\n" \
"v_mfma_f32_16x16x32_bf16 v[208:211], a[176:179], v[84:87], v[208:211]\n" \
"buffer_load_dwordx4 v3, s[80:83], s92 offen offset:3072 lds\n" \
"v_mfma_f32_16x16x32_bf16 v[240:243], a[176:179], v[88:91], v[240:243]\n" \
"v_exp_f32_e32 v101, v101\n" \
"v_mfma_f32_16x16x32_bf16 v[204:207], a[172:175], v[84:87], v[204:207]\n" \
"v_exp_f32_e32 v98, v98\n" \
"v_mfma_f32_16x16x32_bf16 v[236:239], a[172:175], v[88:91], v[236:239]\n" \
"v_exp_f32_e32 v99, v99\n" \
"v_mfma_f32_16x16x32_bf16 v[200:203], a[168:171], v[84:87], v[200:203]\n" \
"v_exp_f32_e32 v96, v96\n" \
"v_mfma_f32_16x16x32_bf16 v[232:235], a[168:171], v[88:91], v[232:235]\n" \
"v_exp_f32_e32 v97, v97\n" \
"v_mfma_f32_16x16x32_bf16 v[196:199], a[164:167], v[84:87], v[196:199]\n" \
"v_exp_f32_e32 v104, v104\n" \
"v_mfma_f32_16x16x32_bf16 v[228:231], a[164:167], v[88:91], v[228:231]\n" \
"v_exp_f32_e32 v105, v105\n" \
"v_mfma_f32_16x16x32_bf16 v[192:195], a[160:163], v[84:87], v[192:195]\n" \
"v_cvt_pk_bf16_f32 v103, v102, v103\n" \
"v_cvt_pk_bf16_f32 v102, v100, v101\n" \
"v_mfma_f32_16x16x32_bf16 v[224:227], a[160:163], v[88:91], v[224:227]\n" \
"v_cvt_pk_bf16_f32 v101, v98, v99\n" \
"v_cvt_pk_bf16_f32 v100, v96, v97\n" \
"v_smfmac_f32_16x16x64_bf16 a[196:199], a[200:203], v[84:91], v21\n" \
"v_exp_f32_e32 v106, v106\n" \
"s_waitcnt vmcnt(15) lgkmcnt(0)\n" \
"s_barrier\n" \
"v_mfma_f32_16x16x32_bf16 v[64:67], a[128:131], a[0:3], v[32:35]\n" \
"s_xor_b32 m0, s65, 0x18000\n" \
"ds_read_b128 a[160:163], v49 offset:8192\n" \
"v_mfma_f32_16x16x32_bf16 v[64:67], a[132:135], a[4:7], v[64:67]\n" \
"buffer_load_dwordx4 v52, s[8:11], s64 offen offset:0 sc1 lds\n" \
"v_mfma_f32_16x16x32_bf16 v[64:67], a[136:139], a[8:11], v[64:67]\n" \
"ds_read_b128 a[164:167], v49 offset:9216\n" \
"ds_read_b128 a[168:171], v49 offset:10240\n" \
"v_mfma_f32_16x16x32_bf16 v[64:67], a[140:143], a[12:15], v[64:67]\n" \
"ds_read_b128 a[172:175], v49 offset:11264\n" \
"ds_read_b128 a[176:179], v49 offset:12288\n" \
"v_mfma_f32_16x16x32_bf16 v[68:71], a[156:159], a[12:15], v[32:35]\n" \
"buffer_load_dwordx4 v52, s[8:11], s64 offen offset:1024 sc1 lds\n" \
"v_mfma_f32_16x16x32_bf16 v[68:71], a[152:155], a[8:11], v[68:71]\n" \
"ds_read_b128 a[180:183], v49 offset:13312\n" \
"ds_read_b128 a[184:187], v49 offset:14336\n" \
"v_mfma_f32_16x16x32_bf16 v[68:71], a[148:151], a[4:7], v[68:71]\n" \
"v_exp_f32_e32 v107, v107\n" \
"v_mfma_f32_16x16x32_bf16 v[68:71], a[144:147], a[0:3], v[68:71]\n" \
"v_cvt_pk_bf16_f32 v104, v104, v105\n" \
"v_cvt_pk_bf16_f32 v105, v106, v107\n" \
"v_mfma_f32_16x16x32_bf16 v[76:79], a[144:147], a[16:19], v[36:39]\n" \
"v_exp_f32_e32 v108, v108\n" \
"v_mfma_f32_16x16x32_bf16 v[76:79], a[148:151], a[20:23], v[76:79]\n" \
"buffer_load_dwordx4 v52, s[8:11], s64 offen offset:2048 sc1 lds\n" \
"v_mfma_f32_16x16x32_bf16 v[76:79], a[152:155], a[24:27], v[76:79]\n" \
"v_exp_f32_e32 v109, v109\n" \
"v_mfma_f32_16x16x32_bf16 v[76:79], a[156:159], a[28:31], v[76:79]\n" \
"v_exp_f32_e32 v110, v110\n" \
"v_mfma_f32_16x16x32_bf16 v[72:75], a[140:143], a[28:31], v[36:39]\n" \
"v_exp_f32_e32 v111, v111\n" \
"v_mfma_f32_16x16x32_bf16 v[72:75], a[136:139], a[24:27], v[72:75]\n" \
"buffer_load_dwordx4 v52, s[8:11], s64 offen offset:3072 sc1 lds\n" \
"v_mfma_f32_16x16x32_bf16 v[72:75], a[132:135], a[20:23], v[72:75]\n" \
"s_mov_b32 m0, s65\n" \
"v_cvt_pk_bf16_f32 v106, v108, v109\n" \
"v_mfma_f32_16x16x32_bf16 v[72:75], a[128:131], a[16:19], v[72:75]\n" \
"v_cvt_pk_bf16_f32 v107, v110, v111\n" \
"ds_read_b128 a[188:191], v49 offset:15360\n" \
"v_mfma_f32_16x16x32_bf16 v[80:83], a[128:131], a[32:35], v[40:43]\n" \
"v_exp_f32_e32 v118, v118\n" \
"v_mfma_f32_16x16x32_bf16 v[80:83], a[132:135], a[36:39], v[80:83]\n" \
"v_exp_f32_e32 v119, v119\n" \
"v_mfma_f32_16x16x32_bf16 v[80:83], a[136:139], a[40:43], v[80:83]\n" \
"buffer_load_dwordx4 v53, s[8:11], s64 offen offset:0 sc1 lds\n" \
"v_mfma_f32_16x16x32_bf16 v[80:83], a[140:143], a[44:47], v[80:83]\n" \
"v_exp_f32_e32 v116, v116\n" \
"v_mfma_f32_16x16x32_bf16 v[84:87], a[156:159], a[44:47], v[40:43]\n" \
"v_exp_f32_e32 v117, v117\n" \
"v_mfma_f32_16x16x32_bf16 v[84:87], a[152:155], a[40:43], v[84:87]\n" \
"v_cvt_pk_bf16_f32 v119, v118, v119\n" \
"v_cvt_pk_bf16_f32 v118, v116, v117\n" \
"v_mfma_f32_16x16x32_bf16 v[84:87], a[148:151], a[36:39], v[84:87]\n" \
"v_exp_f32_e32 v114, v114\n" \
"v_mfma_f32_16x16x32_bf16 v[84:87], a[144:147], a[32:35], v[84:87]\n" \
"buffer_load_dwordx4 v53, s[8:11], s64 offen offset:1024 sc1 lds\n" \
"v_mfma_f32_16x16x32_bf16 v[92:95], a[144:147], a[48:51], v[44:47]\n" \
"v_exp_f32_e32 v115, v115\n" \
"v_mfma_f32_16x16x32_bf16 v[92:95], a[148:151], a[52:55], v[92:95]\n" \
"v_exp_f32_e32 v112, v112\n" \
"v_mfma_f32_16x16x32_bf16 v[92:95], a[152:155], a[56:59], v[92:95]\n" \
"v_exp_f32_e32 v113, v113\n" \
"v_mfma_f32_16x16x32_bf16 v[92:95], a[156:159], a[60:63], v[92:95]\n" \
"buffer_load_dwordx4 v53, s[8:11], s64 offen offset:2048 sc1 lds\n" \
"v_mfma_f32_16x16x32_bf16 v[88:91], a[140:143], a[60:63], v[44:47]\n" \
"v_cvt_pk_bf16_f32 v117, v114, v115\n" \
"v_cvt_pk_bf16_f32 v116, v112, v113\n" \
"v_mfma_f32_16x16x32_bf16 v[88:91], a[136:139], a[56:59], v[88:91]\n" \
"v_exp_f32_e32 v120, v120\n" \
"v_mfma_f32_16x16x32_bf16 v[88:91], a[132:135], a[52:55], v[88:91]\n" \
"v_exp_f32_e32 v121, v121\n" \
"v_mfma_f32_16x16x32_bf16 v[88:91], a[128:131], a[48:51], v[88:91]\n" \
"v_cvt_pk_bf16_f32 v120, v120, v121\n" \
"s_waitcnt lgkmcnt(0)\n" \
"v_mfma_f32_16x16x32_bf16 v[128:131], a[160:163], v[100:103], v[128:131]\n" \
"buffer_load_dwordx4 v53, s[8:11], s64 offen offset:3072 sc1 lds\n" \
"v_mfma_f32_16x16x32_bf16 v[160:163], a[160:163], v[104:107], v[160:163]\n" \
"s_add_u32 m0, s93, 0x1000\n" \
"ds_read_b128 a[128:131], v48 offset:8192\n" \
"v_mfma_f32_16x16x32_bf16 v[132:135], a[164:167], v[100:103], v[132:135]\n" \
"v_exp_f32_e32 v122, v122\n" \
"v_mfma_f32_16x16x32_bf16 v[164:167], a[164:167], v[104:107], v[164:167]\n" \
"v_exp_f32_e32 v123, v123\n" \
"v_mfma_f32_16x16x32_bf16 v[136:139], a[168:171], v[100:103], v[136:139]\n" \
"buffer_load_dwordx4 v4, s[80:83], s92 offen offset:0 lds\n" \
"v_mfma_f32_16x16x32_bf16 v[168:171], a[168:171], v[104:107], v[168:171]\n" \
"v_exp_f32_e32 v124, v124\n" \
"v_mfma_f32_16x16x32_bf16 v[140:143], a[172:175], v[100:103], v[140:143]\n" \
"v_exp_f32_e32 v125, v125\n" \
"v_mfma_f32_16x16x32_bf16 v[172:175], a[172:175], v[104:107], v[172:175]\n" \
"v_exp_f32_e32 v126, v126\n" \
"v_mfma_f32_16x16x32_bf16 v[144:147], a[176:179], v[100:103], v[144:147]\n" \
"v_exp_f32_e32 v127, v127\n" \
"v_mfma_f32_16x16x32_bf16 v[176:179], a[176:179], v[104:107], v[176:179]\n" \
"v_cvt_pk_bf16_f32 v121, v122, v123\n" \
"v_cvt_pk_bf16_f32 v122, v124, v125\n" \
"v_mfma_f32_16x16x32_bf16 v[148:151], a[180:183], v[100:103], v[148:151]\n" \
"buffer_load_dwordx4 v5, s[80:83], s92 offen offset:1024 lds\n" \
"v_mfma_f32_16x16x32_bf16 v[180:183], a[180:183], v[104:107], v[180:183]\n" \
"v_cvt_pk_bf16_f32 v123, v126, v127\n" \
"ds_read_b128 a[144:147], v48 offset:12288\n" \
"v_mfma_f32_16x16x32_bf16 v[152:155], a[184:187], v[100:103], v[152:155]\n" \
"ds_read_b128 a[132:135], v48 offset:9216\n" \
"ds_read_b128 a[148:151], v48 offset:13312\n" \
"v_mfma_f32_16x16x32_bf16 v[184:187], a[184:187], v[104:107], v[184:187]\n" \
"ds_read_b128 a[136:139], v48 offset:10240\n" \
"ds_read_b128 a[152:155], v48 offset:14336\n" \
"v_mfma_f32_16x16x32_bf16 v[156:159], a[188:191], v[100:103], v[156:159]\n" \
"ds_read_b128 a[140:143], v48 offset:11264\n" \
"ds_read_b128 a[156:159], v48 offset:15360\n" \
"v_mfma_f32_16x16x32_bf16 v[188:191], a[188:191], v[104:107], v[188:191]\n" \
"buffer_load_dwordx4 v6, s[80:83], s92 offen offset:2048 lds\n" \
"v_smfmac_f32_16x16x64_bf16 a[192:195], a[200:203], v[100:107], v21\n" \
"v_exp_f32_e32 v70, v70\n" \
"v_mfma_f32_16x16x32_bf16 v[220:223], a[188:191], v[116:119], v[220:223]\n" \
"v_exp_f32_e32 v71, v71\n" \
"v_mfma_f32_16x16x32_bf16 v[252:255], a[188:191], v[120:123], v[252:255]\n" \
"buffer_load_dwordx4 v7, s[80:83], s92 offen offset:3072 lds\n" \
"v_mfma_f32_16x16x32_bf16 v[216:219], a[184:187], v[116:119], v[216:219]\n" \
"s_add_u32 m0, s93, 0x2000\n" \
"v_cvt_pk_bf16_f32 v71, v70, v71\n" \
"v_mfma_f32_16x16x32_bf16 v[248:251], a[184:187], v[120:123], v[248:251]\n" \
"v_exp_f32_e32 v72, v72\n" \
"v_mfma_f32_16x16x32_bf16 v[212:215], a[180:183], v[116:119], v[212:215]\n" \
"v_exp_f32_e32 v73, v73\n" \
"v_mfma_f32_16x16x32_bf16 v[244:247], a[180:183], v[120:123], v[244:247]\n" \
"v_exp_f32_e32 v68, v68\n" \
"v_mfma_f32_16x16x32_bf16 v[208:211], a[176:179], v[116:119], v[208:211]\n" \
"buffer_load_dwordx4 v8, s[80:83], s92 offen offset:0 lds\n" \
"v_mfma_f32_16x16x32_bf16 v[240:243], a[176:179], v[120:123], v[240:243]\n" \
"v_exp_f32_e32 v69, v69\n" \
"v_mfma_f32_16x16x32_bf16 v[204:207], a[172:175], v[116:119], v[204:207]\n" \
"v_exp_f32_e32 v74, v74\n" \
"v_mfma_f32_16x16x32_bf16 v[236:239], a[172:175], v[120:123], v[236:239]\n" \
"v_exp_f32_e32 v75, v75\n" \
"v_mfma_f32_16x16x32_bf16 v[200:203], a[168:171], v[116:119], v[200:203]\n" \
"v_exp_f32_e32 v66, v66\n" \
"v_mfma_f32_16x16x32_bf16 v[232:235], a[168:171], v[120:123], v[232:235]\n" \
"v_exp_f32_e32 v67, v67\n" \
"v_mfma_f32_16x16x32_bf16 v[196:199], a[164:167], v[116:119], v[196:199]\n" \
"v_exp_f32_e32 v64, v64\n" \
"v_mfma_f32_16x16x32_bf16 v[228:231], a[164:167], v[120:123], v[228:231]\n" \
"v_exp_f32_e32 v65, v65\n" \
"v_mfma_f32_16x16x32_bf16 v[192:195], a[160:163], v[116:119], v[192:195]\n" \
"v_cvt_pk_bf16_f32 v72, v72, v73\n" \
"v_cvt_pk_bf16_f32 v70, v68, v69\n" \
"v_mfma_f32_16x16x32_bf16 v[224:227], a[160:163], v[120:123], v[224:227]\n" \
"v_exp_f32_e32 v76, v76\n" \
"v_smfmac_f32_16x16x64_bf16 a[196:199], a[200:203], v[116:123], v21\n" \
"v_exp_f32_e32 v77, v77\n" \
"s_waitcnt vmcnt(13) lgkmcnt(0)\n" \
"s_barrier\n" \
"v_exp_f32_e32 v78, v78\n" \
"v_exp_f32_e32 v79, v79\n" \
"v_cvt_pk_bf16_f32 v73, v74, v75\n" \
"v_add_u32_e32 v49, 0x10000, v48\n" \
"v_cvt_pk_bf16_f32 v69, v66, v67\n" \
"v_cvt_pk_bf16_f32 v68, v64, v65\n" \
"v_cvt_pk_bf16_f32 v74, v76, v77\n" \
"v_cvt_pk_bf16_f32 v75, v78, v79\n" \
"v_add_u32_e32 v48, s16, v20\n" \
"s_xor_b32 s65, s65, 0x8000\n" \
"s_add_u32 s64, s64, 0x10000\n" \
".p2align 6\n" \
"L_2_%=:\n" \
"v_mfma_f32_16x16x32_bf16 v[96:99], a[128:131], a[64:67], v[32:35]\n" \
"s_xor_b32 m0, s65, 0x12000\n" \
"ds_read_b128 a[160:163], v49 offset:0\n" \
"v_mfma_f32_16x16x32_bf16 v[96:99], a[132:135], a[68:71], v[96:99]\n" \
"buffer_load_dwordx4 v50, s[8:11], s64 offen offset:0 sc1 lds\n" \
"v_mfma_f32_16x16x32_bf16 v[96:99], a[136:139], a[72:75], v[96:99]\n" \
"ds_read_b128 a[164:167], v49 offset:1024\n" \
"ds_read_b128 a[168:171], v49 offset:2048\n" \
"v_mfma_f32_16x16x32_bf16 v[96:99], a[140:143], a[76:79], v[96:99]\n" \
"ds_read_b128 a[172:175], v49 offset:3072\n" \
"ds_read_b128 a[176:179], v49 offset:4096\n" \
"v_mfma_f32_16x16x32_bf16 v[100:103], a[156:159], a[76:79], v[32:35]\n" \
"ds_read_b128 a[180:183], v49 offset:5120\n" \
"ds_read_b128 a[184:187], v49 offset:6144\n" \
"v_mfma_f32_16x16x32_bf16 v[100:103], a[152:155], a[72:75], v[100:103]\n" \
"v_exp_f32_e64 v86, v86\n" \
"v_mfma_f32_16x16x32_bf16 v[100:103], a[148:151], a[68:71], v[100:103]\n" \
"v_exp_f32_e64 v87, v87\n" \
"v_mfma_f32_16x16x32_bf16 v[100:103], a[144:147], a[64:67], v[100:103]\n" \
"buffer_load_dwordx4 v50, s[8:11], s64 offen offset:1024 sc1 lds\n" \
"v_mfma_f32_16x16x32_bf16 v[108:111], a[144:147], a[80:83], v[36:39]\n" \
"ds_read_b128 a[188:191], v49 offset:7168\n" \
"v_cvt_pk_bf16_f32 v87, v86, v87\n" \
"v_mfma_f32_16x16x32_bf16 v[108:111], a[148:151], a[84:87], v[108:111]\n" \
"v_exp_f32_e64 v84, v84\n" \
"v_mfma_f32_16x16x32_bf16 v[108:111], a[152:155], a[88:91], v[108:111]\n" \
"v_exp_f32_e64 v85, v85\n" \
"v_mfma_f32_16x16x32_bf16 v[108:111], a[156:159], a[92:95], v[108:111]\n" \
"v_exp_f32_e64 v82, v82\n" \
"v_mfma_f32_16x16x32_bf16 v[104:107], a[140:143], a[92:95], v[36:39]\n" \
"v_exp_f32_e64 v83, v83\n" \
"v_mfma_f32_16x16x32_bf16 v[104:107], a[136:139], a[88:91], v[104:107]\n" \
"v_exp_f32_e64 v80, v80\n" \
"v_mfma_f32_16x16x32_bf16 v[104:107], a[132:135], a[84:87], v[104:107]\n" \
"v_exp_f32_e64 v81, v81\n" \
"v_mfma_f32_16x16x32_bf16 v[104:107], a[128:131], a[80:83], v[104:107]\n" \
"buffer_load_dwordx4 v50, s[8:11], s64 offen offset:2048 sc1 lds\n" \
"v_mfma_f32_16x16x32_bf16 v[112:115], a[128:131], a[96:99], v[40:43]\n" \
"v_exp_f32_e64 v88, v88\n" \
"v_mfma_f32_16x16x32_bf16 v[112:115], a[132:135], a[100:103], v[112:115]\n" \
"v_exp_f32_e64 v89, v89\n" \
"v_mfma_f32_16x16x32_bf16 v[112:115], a[136:139], a[104:107], v[112:115]\n" \
"v_cvt_pk_bf16_f32 v86, v84, v85\n" \
"v_cvt_pk_bf16_f32 v85, v82, v83\n" \
"v_mfma_f32_16x16x32_bf16 v[112:115], a[140:143], a[108:111], v[112:115]\n" \
"v_cvt_pk_bf16_f32 v84, v80, v81\n" \
"v_cvt_pk_bf16_f32 v88, v88, v89\n" \
"v_mfma_f32_16x16x32_bf16 v[116:119], a[156:159], a[108:111], v[40:43]\n" \
"v_exp_f32_e64 v90, v90\n" \
"v_mfma_f32_16x16x32_bf16 v[116:119], a[152:155], a[104:107], v[116:119]\n" \
"v_exp_f32_e64 v91, v91\n" \
"v_mfma_f32_16x16x32_bf16 v[116:119], a[148:151], a[100:103], v[116:119]\n" \
"buffer_load_dwordx4 v50, s[8:11], s64 offen offset:3072 sc1 lds\n" \
"v_mfma_f32_16x16x32_bf16 v[116:119], a[144:147], a[96:99], v[116:119]\n" \
"s_xor_b32 m0, s65, 0xa000\n" \
"v_cvt_pk_bf16_f32 v89, v90, v91\n" \
"v_mfma_f32_16x16x32_bf16 v[124:127], a[144:147], a[112:115], v[44:47]\n" \
"v_exp_f32_e64 v92, v92\n" \
"v_mfma_f32_16x16x32_bf16 v[124:127], a[148:151], a[116:119], v[124:127]\n" \
"v_exp_f32_e64 v93, v93\n" \
"v_mfma_f32_16x16x32_bf16 v[124:127], a[152:155], a[120:123], v[124:127]\n" \
"v_exp_f32_e64 v94, v94\n" \
"v_mfma_f32_16x16x32_bf16 v[124:127], a[156:159], a[124:127], v[124:127]\n" \
"v_exp_f32_e64 v95, v95\n" \
"v_mfma_f32_16x16x32_bf16 v[120:123], a[140:143], a[124:127], v[44:47]\n" \
"v_cvt_pk_bf16_f32 v90, v92, v93\n" \
"v_cvt_pk_bf16_f32 v91, v94, v95\n" \
"v_mfma_f32_16x16x32_bf16 v[120:123], a[136:139], a[120:123], v[120:123]\n" \
"ds_read_b128 a[144:147], v48 offset:4096\n" \
"ds_read_b128 a[148:151], v48 offset:5120\n" \
"v_mfma_f32_16x16x32_bf16 v[120:123], a[132:135], a[116:119], v[120:123]\n" \
"buffer_load_dwordx4 v51, s[8:11], s64 offen offset:0 sc1 lds\n" \
"v_mfma_f32_16x16x32_bf16 v[120:123], a[128:131], a[112:115], v[120:123]\n" \
"ds_read_b128 a[128:131], v48 offset:0\n" \
"s_waitcnt lgkmcnt(3)\n" \
"v_mfma_f32_16x16x32_bf16 v[128:131], a[160:163], v[68:71], v[128:131]\n" \
"ds_read_b128 a[132:135], v48 offset:1024\n" \
"ds_read_b128 a[136:139], v48 offset:2048\n" \
"v_mfma_f32_16x16x32_bf16 v[160:163], a[160:163], v[72:75], v[160:163]\n" \
"ds_read_b128 a[152:155], v48 offset:6144\n" \
"ds_read_b128 a[140:143], v48 offset:3072\n" \
"v_mfma_f32_16x16x32_bf16 v[132:135], a[164:167], v[68:71], v[132:135]\n" \
"v_exp_f32_e32 v102, v102\n" \
"v_mfma_f32_16x16x32_bf16 v[164:167], a[164:167], v[72:75], v[164:167]\n" \
"v_exp_f32_e64 v103, v103\n" \
"v_mfma_f32_16x16x32_bf16 v[136:139], a[168:171], v[68:71], v[136:139]\n" \
"ds_read_b128 a[156:159], v48 offset:7168\n" \
"v_cvt_pk_bf16_f32 v103, v102, v103\n" \
"v_mfma_f32_16x16x32_bf16 v[168:171], a[168:171], v[72:75], v[168:171]\n" \
"buffer_load_dwordx4 v51, s[8:11], s64 offen offset:1024 sc1 lds\n" \
"v_mfma_f32_16x16x32_bf16 v[140:143], a[172:175], v[68:71], v[140:143]\n" \
"v_exp_f32_e64 v100, v100\n" \
"v_mfma_f32_16x16x32_bf16 v[172:175], a[172:175], v[72:75], v[172:175]\n" \
"v_exp_f32_e64 v101, v101\n" \
"v_mfma_f32_16x16x32_bf16 v[144:147], a[176:179], v[68:71], v[144:147]\n" \
"v_exp_f32_e64 v98, v98\n" \
"v_mfma_f32_16x16x32_bf16 v[176:179], a[176:179], v[72:75], v[176:179]\n" \
"v_exp_f32_e64 v99, v99\n" \
"v_mfma_f32_16x16x32_bf16 v[148:151], a[180:183], v[68:71], v[148:151]\n" \
"v_exp_f32_e64 v96, v96\n" \
"v_mfma_f32_16x16x32_bf16 v[180:183], a[180:183], v[72:75], v[180:183]\n" \
"v_exp_f32_e64 v97, v97\n" \
"v_mfma_f32_16x16x32_bf16 v[152:155], a[184:187], v[68:71], v[152:155]\n" \
"v_exp_f32_e64 v104, v104\n" \
"v_mfma_f32_16x16x32_bf16 v[184:187], a[184:187], v[72:75], v[184:187]\n" \
"buffer_load_dwordx4 v51, s[8:11], s64 offen offset:2048 sc1 lds\n" \
"v_mfma_f32_16x16x32_bf16 v[156:159], a[188:191], v[68:71], v[156:159]\n" \
"v_exp_f32_e64 v105, v105\n" \
"v_mfma_f32_16x16x32_bf16 v[188:191], a[188:191], v[72:75], v[188:191]\n" \
"v_cvt_pk_bf16_f32 v102, v100, v101\n" \
"v_cvt_pk_bf16_f32 v101, v98, v99\n" \
"v_smfmac_f32_16x16x64_bf16 a[192:195], a[200:203], v[68:75], v21\n" \
"v_cvt_pk_bf16_f32 v100, v96, v97\n" \
"v_cvt_pk_bf16_f32 v104, v104, v105\n" \
"v_mfma_f32_16x16x32_bf16 v[220:223], a[188:191], v[84:87], v[220:223]\n" \
"v_exp_f32_e64 v106, v106\n" \
"v_mfma_f32_16x16x32_bf16 v[252:255], a[188:191], v[88:91], v[252:255]\n" \
"v_exp_f32_e64 v107, v107\n" \
"v_mfma_f32_16x16x32_bf16 v[216:219], a[184:187], v[84:87], v[216:219]\n" \
"v_exp_f32_e64 v108, v108\n" \
"v_mfma_f32_16x16x32_bf16 v[248:251], a[184:187], v[88:91], v[248:251]\n" \
"buffer_load_dwordx4 v51, s[8:11], s64 offen offset:3072 sc1 lds\n" \
"v_mfma_f32_16x16x32_bf16 v[212:215], a[180:183], v[84:87], v[212:215]\n" \
"v_exp_f32_e64 v109, v109\n" \
"v_mfma_f32_16x16x32_bf16 v[244:247], a[180:183], v[88:91], v[244:247]\n" \
"v_exp_f32_e64 v110, v110\n" \
"v_mfma_f32_16x16x32_bf16 v[208:211], a[176:179], v[84:87], v[208:211]\n" \
"v_exp_f32_e64 v111, v111\n" \
"v_mfma_f32_16x16x32_bf16 v[240:243], a[176:179], v[88:91], v[240:243]\n" \
"v_exp_f32_e64 v118, v118\n" \
"v_mfma_f32_16x16x32_bf16 v[204:207], a[172:175], v[84:87], v[204:207]\n" \
"v_exp_f32_e64 v119, v119\n" \
"v_mfma_f32_16x16x32_bf16 v[236:239], a[172:175], v[88:91], v[236:239]\n" \
"v_cvt_pk_bf16_f32 v105, v106, v107\n" \
"v_cvt_pk_bf16_f32 v106, v108, v109\n" \
"v_mfma_f32_16x16x32_bf16 v[200:203], a[168:171], v[84:87], v[200:203]\n" \
"v_cvt_pk_bf16_f32 v107, v110, v111\n" \
"v_cvt_pk_bf16_f32 v119, v118, v119\n" \
"v_mfma_f32_16x16x32_bf16 v[232:235], a[168:171], v[88:91], v[232:235]\n" \
"v_exp_f32_e64 v116, v116\n" \
"v_mfma_f32_16x16x32_bf16 v[196:199], a[164:167], v[84:87], v[196:199]\n" \
"v_exp_f32_e64 v117, v117\n" \
"v_mfma_f32_16x16x32_bf16 v[228:231], a[164:167], v[88:91], v[228:231]\n" \
"v_exp_f32_e64 v114, v114\n" \
"v_mfma_f32_16x16x32_bf16 v[192:195], a[160:163], v[84:87], v[192:195]\n" \
"v_exp_f32_e64 v115, v115\n" \
"v_mfma_f32_16x16x32_bf16 v[224:227], a[160:163], v[88:91], v[224:227]\n" \
"v_exp_f32_e64 v112, v112\n" \
"v_smfmac_f32_16x16x64_bf16 a[196:199], a[200:203], v[84:91], v21\n" \
"v_exp_f32_e32 v113, v113\n" \
"s_waitcnt vmcnt(8) lgkmcnt(0)\n" \
"s_barrier\n" \
"s_nop 0\n" \
"v_mfma_f32_16x16x32_bf16 v[64:67], a[128:131], a[64:67], v[32:35]\n" \
"s_xor_b32 m0, s65, 0x18000\n" \
"ds_read_b128 a[160:163], v49 offset:8192\n" \
"v_mfma_f32_16x16x32_bf16 v[64:67], a[132:135], a[68:71], v[64:67]\n" \
"buffer_load_dwordx4 v52, s[8:11], s64 offen offset:0 sc1 lds\n" \
"v_mfma_f32_16x16x32_bf16 v[64:67], a[136:139], a[72:75], v[64:67]\n" \
"ds_read_b128 a[164:167], v49 offset:9216\n" \
"ds_read_b128 a[168:171], v49 offset:10240\n" \
"v_mfma_f32_16x16x32_bf16 v[64:67], a[140:143], a[76:79], v[64:67]\n" \
"ds_read_b128 a[172:175], v49 offset:11264\n" \
"ds_read_b128 a[176:179], v49 offset:12288\n" \
"v_mfma_f32_16x16x32_bf16 v[68:71], a[156:159], a[76:79], v[32:35]\n" \
"ds_read_b128 a[180:183], v49 offset:13312\n" \
"ds_read_b128 a[184:187], v49 offset:14336\n" \
"v_mfma_f32_16x16x32_bf16 v[68:71], a[152:155], a[72:75], v[68:71]\n" \
"ds_read_b128 a[188:191], v49 offset:15360\n" \
"v_cvt_pk_bf16_f32 v118, v116, v117\n" \
"v_mfma_f32_16x16x32_bf16 v[68:71], a[148:151], a[68:71], v[68:71]\n" \
"v_cvt_pk_bf16_f32 v117, v114, v115\n" \
"v_cvt_pk_bf16_f32 v116, v112, v113\n" \
"v_mfma_f32_16x16x32_bf16 v[68:71], a[144:147], a[64:67], v[68:71]\n" \
"buffer_load_dwordx4 v52, s[8:11], s64 offen offset:1024 sc1 lds\n" \
"v_mfma_f32_16x16x32_bf16 v[76:79], a[144:147], a[80:83], v[36:39]\n" \
"v_exp_f32_e64 v120, v120\n" \
"v_mfma_f32_16x16x32_bf16 v[76:79], a[148:151], a[84:87], v[76:79]\n" \
"v_exp_f32_e64 v121, v121\n" \
"v_mfma_f32_16x16x32_bf16 v[76:79], a[152:155], a[88:91], v[76:79]\n" \
"v_exp_f32_e64 v122, v122\n" \
"v_mfma_f32_16x16x32_bf16 v[76:79], a[156:159], a[92:95], v[76:79]\n" \
"v_exp_f32_e64 v123, v123\n" \
"v_mfma_f32_16x16x32_bf16 v[72:75], a[140:143], a[92:95], v[36:39]\n" \
"v_cvt_pk_bf16_f32 v120, v120, v121\n" \
"v_cvt_pk_bf16_f32 v121, v122, v123\n" \
"v_mfma_f32_16x16x32_bf16 v[72:75], a[136:139], a[88:91], v[72:75]\n" \
"v_exp_f32_e64 v124, v124\n" \
"v_mfma_f32_16x16x32_bf16 v[72:75], a[132:135], a[84:87], v[72:75]\n" \
"v_exp_f32_e64 v125, v125\n" \
"v_mfma_f32_16x16x32_bf16 v[72:75], a[128:131], a[80:83], v[72:75]\n" \
"buffer_load_dwordx4 v52, s[8:11], s64 offen offset:2048 sc1 lds\n" \
"v_mfma_f32_16x16x32_bf16 v[80:83], a[128:131], a[96:99], v[40:43]\n" \
"v_exp_f32_e64 v126, v126\n" \
"v_mfma_f32_16x16x32_bf16 v[80:83], a[132:135], a[100:103], v[80:83]\n" \
"v_exp_f32_e64 v127, v127\n" \
"v_mfma_f32_16x16x32_bf16 v[80:83], a[136:139], a[104:107], v[80:83]\n" \
"v_cvt_pk_bf16_f32 v122, v124, v125\n" \
"v_cvt_pk_bf16_f32 v123, v126, v127\n" \
"v_mfma_f32_16x16x32_bf16 v[80:83], a[140:143], a[108:111], v[80:83]\n" \
"v_exp_f32_e64 v70, v70\n" \
"v_mfma_f32_16x16x32_bf16 v[84:87], a[156:159], a[108:111], v[40:43]\n" \
"v_exp_f32_e64 v71, v71\n" \
"v_mfma_f32_16x16x32_bf16 v[84:87], a[152:155], a[104:107], v[84:87]\n" \
"v_exp_f32_e64 v72, v72\n" \
"v_mfma_f32_16x16x32_bf16 v[84:87], a[148:151], a[100:103], v[84:87]\n" \
"buffer_load_dwordx4 v52, s[8:11], s64 offen offset:3072 sc1 lds\n" \
"v_mfma_f32_16x16x32_bf16 v[84:87], a[144:147], a[96:99], v[84:87]\n" \
"s_mov_b32 m0, s65\n" \
"v_cvt_pk_bf16_f32 v71, v70, v71\n" \
"v_mfma_f32_16x16x32_bf16 v[92:95], a[144:147], a[112:115], v[44:47]\n" \
"ds_read_b128 a[144:147], v48 offset:12288\n" \
"v_add_u32_e32 v49, 0x10000, v48\n" \
"v_mfma_f32_16x16x32_bf16 v[92:95], a[148:151], a[116:119], v[92:95]\n" \
"ds_read_b128 a[148:151], v48 offset:13312\n" \
"s_xor_b32 s65, s65, 0x8000\n" \
"v_mfma_f32_16x16x32_bf16 v[92:95], a[152:155], a[120:123], v[92:95]\n" \
"v_exp_f32_e32 v73, v73\n" \
"v_mfma_f32_16x16x32_bf16 v[92:95], a[156:159], a[124:127], v[92:95]\n" \
"ds_read_b128 a[152:155], v48 offset:14336\n" \
"ds_read_b128 a[156:159], v48 offset:15360\n" \
"v_mfma_f32_16x16x32_bf16 v[88:91], a[140:143], a[124:127], v[44:47]\n" \
"ds_read_b128 a[140:143], v48 offset:11264\n" \
"v_cvt_pk_bf16_f32 v72, v72, v73\n" \
"v_mfma_f32_16x16x32_bf16 v[88:91], a[136:139], a[120:123], v[88:91]\n" \
"v_exp_f32_e64 v68, v68\n" \
"v_mfma_f32_16x16x32_bf16 v[88:91], a[132:135], a[116:119], v[88:91]\n" \
"buffer_load_dwordx4 v53, s[8:11], s64 offen offset:0 sc1 lds\n" \
"v_mfma_f32_16x16x32_bf16 v[88:91], a[128:131], a[112:115], v[88:91]\n" \
"ds_read_b128 a[128:131], v48 offset:8192\n" \
"s_waitcnt lgkmcnt(6)\n" \
"v_mfma_f32_16x16x32_bf16 v[128:131], a[160:163], v[100:103], v[128:131]\n" \
"ds_read_b128 a[132:135], v48 offset:9216\n" \
"ds_read_b128 a[136:139], v48 offset:10240\n" \
"v_mfma_f32_16x16x32_bf16 v[160:163], a[160:163], v[104:107], v[160:163]\n" \
"v_exp_f32_e32 v69, v69\n" \
"v_mfma_f32_16x16x32_bf16 v[132:135], a[164:167], v[100:103], v[132:135]\n" \
"v_exp_f32_e64 v74, v74\n" \
"v_mfma_f32_16x16x32_bf16 v[164:167], a[164:167], v[104:107], v[164:167]\n" \
"v_exp_f32_e64 v75, v75\n" \
"v_mfma_f32_16x16x32_bf16 v[136:139], a[168:171], v[100:103], v[136:139]\n" \
"v_exp_f32_e64 v66, v66\n" \
"v_mfma_f32_16x16x32_bf16 v[168:171], a[168:171], v[104:107], v[168:171]\n" \
"buffer_load_dwordx4 v53, s[8:11], s64 offen offset:1024 sc1 lds\n" \
"v_mfma_f32_16x16x32_bf16 v[140:143], a[172:175], v[100:103], v[140:143]\n" \
"v_exp_f32_e64 v67, v67\n" \
"v_mfma_f32_16x16x32_bf16 v[172:175], a[172:175], v[104:107], v[172:175]\n" \
"v_exp_f32_e64 v64, v64\n" \
"v_mfma_f32_16x16x32_bf16 v[144:147], a[176:179], v[100:103], v[144:147]\n" \
"v_exp_f32_e64 v65, v65\n" \
"v_mfma_f32_16x16x32_bf16 v[176:179], a[176:179], v[104:107], v[176:179]\n" \
"v_exp_f32_e64 v76, v76\n" \
"v_mfma_f32_16x16x32_bf16 v[148:151], a[180:183], v[100:103], v[148:151]\n" \
"v_exp_f32_e64 v77, v77\n" \
"v_mfma_f32_16x16x32_bf16 v[180:183], a[180:183], v[104:107], v[180:183]\n" \
"v_exp_f32_e64 v78, v78\n" \
"v_mfma_f32_16x16x32_bf16 v[152:155], a[184:187], v[100:103], v[152:155]\n" \
"v_exp_f32_e64 v79, v79\n" \
"v_mfma_f32_16x16x32_bf16 v[184:187], a[184:187], v[104:107], v[184:187]\n" \
"buffer_load_dwordx4 v53, s[8:11], s64 offen offset:2048 sc1 lds\n" \
"v_mfma_f32_16x16x32_bf16 v[156:159], a[188:191], v[100:103], v[156:159]\n" \
"v_cvt_pk_bf16_f32 v70, v68, v69\n" \
"v_cvt_pk_bf16_f32 v73, v74, v75\n" \
"v_mfma_f32_16x16x32_bf16 v[188:191], a[188:191], v[104:107], v[188:191]\n" \
"v_cvt_pk_bf16_f32 v69, v66, v67\n" \
"v_cvt_pk_bf16_f32 v68, v64, v65\n" \
"v_smfmac_f32_16x16x64_bf16 a[192:195], a[200:203], v[100:107], v21\n" \
"v_cvt_pk_bf16_f32 v74, v76, v77\n" \
"v_cvt_pk_bf16_f32 v75, v78, v79\n" \
"v_mfma_f32_16x16x32_bf16 v[220:223], a[188:191], v[116:119], v[220:223]\n" \
"v_xor_b32_e32 v48, 0x8000, v48\n" \
"v_mfma_f32_16x16x32_bf16 v[252:255], a[188:191], v[120:123], v[252:255]\n" \
"v_mfma_f32_16x16x32_bf16 v[216:219], a[184:187], v[116:119], v[216:219]\n" \
"v_mfma_f32_16x16x32_bf16 v[248:251], a[184:187], v[120:123], v[248:251]\n" \
"buffer_load_dwordx4 v53, s[8:11], s64 offen offset:3072 sc1 lds\n" \
"v_mfma_f32_16x16x32_bf16 v[212:215], a[180:183], v[116:119], v[212:215]\n" \
"s_add_u32 s64, s64, 0x10000\n" \
"s_sub_u32 s66, s66, 1\n" \
"s_nop 0\n" \
"v_mfma_f32_16x16x32_bf16 v[244:247], a[180:183], v[120:123], v[244:247]\n" \
"v_mfma_f32_16x16x32_bf16 v[208:211], a[176:179], v[116:119], v[208:211]\n" \
"v_mfma_f32_16x16x32_bf16 v[240:243], a[176:179], v[120:123], v[240:243]\n" \
"v_mfma_f32_16x16x32_bf16 v[204:207], a[172:175], v[116:119], v[204:207]\n" \
"v_mfma_f32_16x16x32_bf16 v[236:239], a[172:175], v[120:123], v[236:239]\n" \
"v_mfma_f32_16x16x32_bf16 v[200:203], a[168:171], v[116:119], v[200:203]\n" \
"v_mfma_f32_16x16x32_bf16 v[232:235], a[168:171], v[120:123], v[232:235]\n" \
"v_mfma_f32_16x16x32_bf16 v[196:199], a[164:167], v[116:119], v[196:199]\n" \
"v_mfma_f32_16x16x32_bf16 v[228:231], a[164:167], v[120:123], v[228:231]\n" \
"v_mfma_f32_16x16x32_bf16 v[192:195], a[160:163], v[116:119], v[192:195]\n" \
"v_mfma_f32_16x16x32_bf16 v[224:227], a[160:163], v[120:123], v[224:227]\n" \
"v_smfmac_f32_16x16x64_bf16 a[196:199], a[200:203], v[116:123], v21\n" \
"s_waitcnt vmcnt(8) lgkmcnt(0)\n" \
"s_barrier\n" \
"s_cbranch_scc0 L_2_%=\n" \
"s_cmp_lg_u32 s16, 0\n" \
"s_cbranch_scc1 L_f1_%=\n" \
"v_bfe_u32 v58, v20, 2, 7\n" \
"v_readlane_b32 s96, v11, 1\n" \
"ds_read_b128 a[160:163], v49 offset:0\n" \
"ds_read_b128 a[164:167], v49 offset:1024\n" \
"ds_read_b128 a[168:171], v49 offset:2048\n" \
"v_mfma_f32_16x16x32_bf16 v[120:123], a[128:131], a[112:115], v[44:47]\n" \
"v_readlane_b32 s93, v11, 20\n" \
"ds_read_b128 a[172:175], v49 offset:3072\n" \
"buffer_load_dword v55, v58, s[56:59], s96 offen sc1\n" \
"v_readlane_b32 s84, v11, 15\n" \
"v_readlane_b32 s85, v11, 16\n" \
"v_readlane_b32 s86, v11, 17\n" \
"s_mov_b32 s87, 0x20000\n" \
"v_readlane_b32 s92, v11, 18\n" \
"s_add_u32 m0, s93, 0x0\n" \
"v_mfma_f32_16x16x32_bf16 v[120:123], a[132:135], a[116:119], v[120:123]\n" \
"ds_read_b128 a[176:179], v49 offset:4096\n" \
"v_add_u32_e32 v54, 0x49000, v20\n" \
"v_mfma_f32_16x16x32_bf16 v[120:123], a[136:139], a[120:123], v[120:123]\n" \
"v_readlane_b32 s97, v11, 35\n" \
"v_readlane_b32 s98, v11, 36\n" \
"v_mfma_f32_16x16x32_bf16 v[120:123], a[140:143], a[124:127], v[120:123]\n" \
"ds_read_b128 a[180:183], v49 offset:5120\n" \
"ds_read_b128 a[184:187], v49 offset:6144\n" \
"v_mfma_f32_16x16x32_bf16 v[124:127], a[156:159], a[124:127], v[44:47]\n" \
"ds_read_b128 a[188:191], v49 offset:7168\n" \
"v_add_u32_e32 v50, s15, v20\n" \
"buffer_load_dwordx4 v0, s[84:87], s92 offen offset:0 lds\n" \
"v_mfma_f32_16x16x32_bf16 v[124:127], a[152:155], a[120:123], v[124:127]\n" \
"v_exp_f32_e32 v86, v86\n" \
"v_mfma_f32_16x16x32_bf16 v[124:127], a[148:151], a[116:119], v[124:127]\n" \
"v_exp_f32_e32 v87, v87\n" \
"v_mfma_f32_16x16x32_bf16 v[124:127], a[144:147], a[112:115], v[124:127]\n" \
"v_cvt_pk_bf16_f32 v87, v86, v87\n" \
"s_waitcnt lgkmcnt(7)\n" \
"v_mfma_f32_16x16x32_bf16 v[128:131], a[160:163], v[68:71], v[128:131]\n" \
"v_exp_f32_e32 v84, v84\n" \
"v_mfma_f32_16x16x32_bf16 v[160:163], a[160:163], v[72:75], v[160:163]\n" \
"v_and_b32_e32 v87, v15, v87\n" \
"s_waitcnt lgkmcnt(6)\n" \
"v_mfma_f32_16x16x32_bf16 v[132:135], a[164:167], v[68:71], v[132:135]\n" \
"buffer_load_dwordx4 v1, s[84:87], s92 offen offset:1024 lds\n" \
"v_mfma_f32_16x16x32_bf16 v[164:167], a[164:167], v[72:75], v[164:167]\n" \
"v_add_u32_e32 v51, 0x10000, v50\n" \
"s_waitcnt lgkmcnt(5)\n" \
"v_mfma_f32_16x16x32_bf16 v[136:139], a[168:171], v[68:71], v[136:139]\n" \
"v_exp_f32_e32 v85, v85\n" \
"v_mfma_f32_16x16x32_bf16 v[168:171], a[168:171], v[72:75], v[168:171]\n" \
"v_cvt_pk_bf16_f32 v86, v84, v85\n" \
"s_waitcnt lgkmcnt(4)\n" \
"v_mfma_f32_16x16x32_bf16 v[140:143], a[172:175], v[68:71], v[140:143]\n" \
"v_exp_f32_e32 v82, v82\n" \
"v_mfma_f32_16x16x32_bf16 v[172:175], a[172:175], v[72:75], v[172:175]\n" \
"v_and_b32_e32 v86, v14, v86\n" \
"s_waitcnt lgkmcnt(3)\n" \
"v_mfma_f32_16x16x32_bf16 v[144:147], a[176:179], v[68:71], v[144:147]\n" \
"buffer_load_dwordx4 v2, s[84:87], s92 offen offset:2048 lds\n" \
"v_mfma_f32_16x16x32_bf16 v[176:179], a[176:179], v[72:75], v[176:179]\n" \
"v_cvt_pk_bf16_f32 v128, v128, v129\n" \
"s_waitcnt lgkmcnt(1)\n" \
"v_mfma_f32_16x16x32_bf16 v[148:151], a[180:183], v[68:71], v[148:151]\n" \
"v_exp_f32_e32 v83, v83\n" \
"v_mfma_f32_16x16x32_bf16 v[180:183], a[180:183], v[72:75], v[180:183]\n" \
"v_exp_f32_e32 v80, v80\n" \
"v_mfma_f32_16x16x32_bf16 v[152:155], a[184:187], v[68:71], v[152:155]\n" \
"v_exp_f32_e32 v81, v81\n" \
"v_mfma_f32_16x16x32_bf16 v[184:187], a[184:187], v[72:75], v[184:187]\n" \
"v_cvt_pk_bf16_f32 v85, v82, v83\n" \
"s_waitcnt lgkmcnt(0)\n" \
"v_mfma_f32_16x16x32_bf16 v[156:159], a[188:191], v[68:71], v[156:159]\n" \
"buffer_load_dwordx4 v3, s[84:87], s92 offen offset:3072 lds\n" \
"s_add_u32 m0, s93, 0x1000\n" \
"v_mfma_f32_16x16x32_bf16 v[188:191], a[188:191], v[72:75], v[188:191]\n" \
"v_cvt_pk_bf16_f32 v84, v80, v81\n" \
"v_and_b32_e32 v84, v12, v84\n" \
"v_smfmac_f32_16x16x64_bf16 a[192:195], a[200:203], v[68:75], v21\n" \
"v_and_b32_e32 v85, v13, v85\n" \
"v_exp_f32_e32 v88, v88\n" \
"v_exp_f32_e32 v89, v89\n" \
"v_mfma_f32_16x16x32_bf16 v[220:223], a[188:191], v[84:87], v[220:223]\n" \
"v_exp_f32_e32 v90, v90\n" \
"v_exp_f32_e32 v91, v91\n" \
"v_cvt_pk_bf16_f32 v88, v88, v89\n" \
"v_exp_f32_e32 v92, v92\n" \
"buffer_load_dwordx4 v4, s[84:87], s92 offen offset:0 lds\n" \
"v_exp_f32_e32 v93, v93\n" \
"v_exp_f32_e32 v94, v94\n" \
"v_exp_f32_e32 v95, v95\n" \
"v_cvt_pk_bf16_f32 v160, v160, v161\n" \
"v_cvt_pk_bf16_f32 v89, v90, v91\n" \
"v_cvt_pk_bf16_f32 v129, v130, v131\n" \
"v_cvt_pk_bf16_f32 v161, v162, v163\n" \
"v_cvt_pk_bf16_f32 v90, v92, v93\n" \
"v_cvt_pk_bf16_f32 v91, v94, v95\n" \
"v_cvt_pk_bf16_f32 v130, v132, v133\n" \
"v_cvt_pk_bf16_f32 v131, v134, v135\n" \
"v_cvt_pk_bf16_f32 v162, v164, v165\n" \
"v_cvt_pk_bf16_f32 v163, v166, v167\n" \
"v_mfma_f32_16x16x32_bf16 v[252:255], a[188:191], v[88:91], v[252:255]\n" \
"v_cvt_pk_bf16_f32 v132, v136, v137\n" \
"v_cvt_pk_bf16_f32 v133, v138, v139\n" \
"v_cvt_pk_bf16_f32 v134, v140, v141\n" \
"buffer_load_dwordx4 v5, s[84:87], s92 offen offset:1024 lds\n" \
"v_cvt_pk_bf16_f32 v135, v142, v143\n" \
"v_cvt_pk_bf16_f32 v164, v168, v169\n" \
"v_cvt_pk_bf16_f32 v165, v170, v171\n" \
"v_cvt_pk_bf16_f32 v166, v172, v173\n" \
"v_cvt_pk_bf16_f32 v167, v174, v175\n" \
"v_cvt_pk_bf16_f32 v136, v144, v145\n" \
"v_cvt_pk_bf16_f32 v137, v146, v147\n" \
"v_cvt_pk_bf16_f32 v138, v148, v149\n" \
"v_cvt_pk_bf16_f32 v139, v150, v151\n" \
"v_cvt_pk_bf16_f32 v140, v152, v153\n" \
"v_cvt_pk_bf16_f32 v141, v154, v155\n" \
"v_cvt_pk_bf16_f32 v142, v156, v157\n" \
"v_cvt_pk_bf16_f32 v143, v158, v159\n" \
"v_cvt_pk_bf16_f32 v168, v176, v177\n" \
"v_cvt_pk_bf16_f32 v169, v178, v179\n" \
"v_cvt_pk_bf16_f32 v170, v180, v181\n" \
"v_cvt_pk_bf16_f32 v171, v182, v183\n" \
"v_cvt_pk_bf16_f32 v172, v184, v185\n" \
"v_cvt_pk_bf16_f32 v173, v186, v187\n" \
"v_cvt_pk_bf16_f32 v174, v188, v189\n" \
"v_cvt_pk_bf16_f32 v175, v190, v191\n" \
"buffer_load_dwordx4 v6, s[84:87], s92 offen offset:2048 lds\n" \
"s_waitcnt vmcnt(8)\n" \
"ds_write_b128 v50, v[128:131] offset:0\n" \
"v_mfma_f32_16x16x32_bf16 v[216:219], a[184:187], v[84:87], v[216:219]\n" \
"ds_read_b32 v60, v18 offset:0\n" \
"ds_read_b32 v61, v18 offset:128\n" \
"v_mfma_f32_16x16x32_bf16 v[248:251], a[184:187], v[88:91], v[248:251]\n" \
"ds_write_b128 v50, v[132:135] offset:1024\n" \
"s_waitcnt vmcnt(7)\n" \
"v_cmp_ne_u32_e32 vcc, s62, v55\n" \
"v_mfma_f32_16x16x32_bf16 v[212:215], a[180:183], v[84:87], v[212:215]\n" \
"ds_read_b128 a[0:3], v49 offset:8192\n" \
"ds_read_b128 a[4:7], v49 offset:9216\n" \
"ds_write_b128 v50, v[136:139] offset:2048\n" \
"v_mfma_f32_16x16x32_bf16 v[244:247], a[180:183], v[88:91], v[244:247]\n" \
"buffer_load_dwordx4 v7, s[84:87], s92 offen offset:3072 lds\n" \
"s_add_u32 m0, s93, 0x2000\n" \
"s_cbranch_vccnz L_so0_%=\n" \
"L_ro0_%=:\n" \
"ds_write_b128 v50, v[140:143] offset:3072\n" \
"ds_read_b128 a[8:11], v49 offset:10240\n" \
"ds_read_b128 a[12:15], v49 offset:11264\n" \
"ds_read_b128 a[16:19], v49 offset:12288\n" \
"ds_read_b128 a[20:23], v49 offset:13312\n" \
"ds_read_b128 a[24:27], v49 offset:14336\n" \
"ds_read_b128 a[28:31], v49 offset:15360\n" \
"ds_write_b128 v51, v[160:163] offset:32768\n" \
"v_mfma_f32_16x16x32_bf16 v[208:211], a[176:179], v[84:87], v[208:211]\n" \
"v_accvgpr_read_b32 v40, a192\n" \
"v_accvgpr_read_b32 v22, a193\n" \
"v_mfma_f32_16x16x32_bf16 v[240:243], a[176:179], v[88:91], v[240:243]\n" \
"ds_write_b128 v51, v[164:167] offset:33792\n" \
"v_mfma_f32_16x16x32_bf16 v[204:207], a[172:175], v[84:87], v[204:207]\n" \
"buffer_load_dwordx4 v8, s[84:87], s92 offen offset:0 lds\n" \
"v_mfma_f32_16x16x32_bf16 v[236:239], a[172:175], v[88:91], v[236:239]\n" \
"ds_write_b128 v51, v[168:171] offset:34816\n" \
"v_mfma_f32_16x16x32_bf16 v[200:203], a[168:171], v[84:87], v[200:203]\n" \
"v_accvgpr_read_b32 v41, a194\n" \
"v_accvgpr_read_b32 v23, a195\n" \
"v_mfma_f32_16x16x32_bf16 v[232:235], a[168:171], v[88:91], v[232:235]\n" \
"ds_write_b128 v51, v[172:175] offset:35840\n" \
"v_mfma_f32_16x16x32_bf16 v[196:199], a[164:167], v[84:87], v[196:199]\n" \
"v_add_f32_e32 v40, v40, v22\n" \
"v_add_f32_e32 v41, v41, v23\n" \
"v_mfma_f32_16x16x32_bf16 v[228:231], a[164:167], v[88:91], v[228:231]\n" \
"ds_write_b64 v19, v[40:41]\n" \
"v_lshl_add_u32 v52, s15, 2, v20\n" \
"v_mfma_f32_16x16x32_bf16 v[192:195], a[160:163], v[84:87], v[192:195]\n" \
"buffer_load_dwordx4 a[220:223], v20, s[56:59], s60 offen offset:0 sc1\n" \
"v_mfma_f32_16x16x32_bf16 v[224:227], a[160:163], v[88:91], v[224:227]\n" \
"v_add_u32_e32 v52, 0x1a000, v52\n" \
"v_xor_b32_e32 v63, 0x400, v19\n" \
"v_smfmac_f32_16x16x64_bf16 a[196:199], a[200:203], v[84:91], v21\n" \
"v_exp_f32_e32 v120, v120\n" \
"v_exp_f32_e32 v121, v121\n" \
"v_exp_f32_e32 v122, v122\n" \
"v_exp_f32_e32 v123, v123\n" \
"v_cvt_pk_bf16_f32 v120, v120, v121\n" \
"v_exp_f32_e32 v124, v124\n" \
"v_exp_f32_e32 v125, v125\n" \
"v_exp_f32_e32 v126, v126\n" \
"buffer_load_dwordx4 a[236:239], v54, s[56:59], s60 offen offset:0 sc1\n" \
"v_exp_f32_e32 v127, v127\n" \
"v_cvt_pk_bf16_f32 v121, v122, v123\n" \
"v_cvt_pk_bf16_f32 v122, v124, v125\n" \
"v_cvt_pk_bf16_f32 v123, v126, v127\n" \
"v_and_b32_e32 v120, v12, v120\n" \
"v_and_b32_e32 v121, v13, v121\n" \
"v_and_b32_e32 v122, v14, v122\n" \
"v_and_b32_e32 v123, v15, v123\n" \
"s_waitcnt lgkmcnt(0)\n" \
"s_barrier\n" \
"ds_read_b128 v[144:147], v52 offset:0\n" \
"ds_read_b128 v[148:151], v52 offset:1024\n" \
"ds_read_b128 v[152:155], v52 offset:2048\n" \
"ds_read_b128 v[156:159], v52 offset:3072\n" \
"buffer_load_dwordx4 a[224:227], v20, s[56:59], s60 offen offset:1024 sc1\n" \
"ds_read_b128 v[176:179], v52 offset:4096\n" \
"ds_read_b128 v[180:183], v52 offset:5120\n" \
"ds_read_b128 v[184:187], v52 offset:6144\n" \
"ds_read_b128 v[188:191], v52 offset:7168\n" \
"v_mfma_f32_16x16x32_bf16 a[196:199], a[212:215], v[120:123], a[196:199]\n" \
"ds_read_b64 v[42:43], v63\n" \
"v_mov_b32_e32 v130, v28\n" \
"v_mfma_f32_16x16x32_bf16 v[224:227], a[0:3], v[120:123], v[224:227]\n" \
"v_mov_b32_e32 v131, v29\n" \
"v_mov_b32_e32 v132, 0\n" \
"v_mfma_f32_16x16x32_bf16 v[228:231], a[4:7], v[120:123], v[228:231]\n" \
"v_mov_b32_e32 v133, 0\n" \
"v_mov_b32_e32 v128, 0\n" \
"v_mfma_f32_16x16x32_bf16 v[232:235], a[8:11], v[120:123], v[232:235]\n" \
"v_mov_b32_e32 v129, 0\n" \
"v_accvgpr_read_b32 v56, a196\n" \
"v_mfma_f32_16x16x32_bf16 v[236:239], a[12:15], v[120:123], v[236:239]\n" \
"buffer_load_dwordx4 a[240:243], v54, s[56:59], s60 offen offset:1024 sc1\n" \
"v_mfma_f32_16x16x32_bf16 v[240:243], a[16:19], v[120:123], v[240:243]\n" \
"v_accvgpr_read_b32 v24, a197\n" \
"v_add_f32_e32 v56, v56, v24\n" \
"v_mfma_f32_16x16x32_bf16 v[244:247], a[20:23], v[120:123], v[244:247]\n" \
"v_accvgpr_read_b32 v57, a198\n" \
"v_accvgpr_read_b32 v25, a199\n" \
"v_mfma_f32_16x16x32_bf16 v[248:251], a[24:27], v[120:123], v[248:251]\n" \
"v_add_f32_e32 v57, v57, v25\n" \
"v_mfma_f32_16x16x32_bf16 v[252:255], a[28:31], v[120:123], v[252:255]\n" \
"s_waitcnt lgkmcnt(1)\n" \
"s_barrier\n" \
"buffer_load_dwordx4 a[228:231], v20, s[56:59], s60 offen offset:2048 sc1\n" \
"v_mfma_f32_16x16x32_bf16 v[192:195], v[130:133], v[144:147], v[192:195]\n" \
"s_waitcnt lgkmcnt(0)\n" \
"v_add_f32_e32 v56, v56, v42\n" \
"v_mfma_f32_16x16x32_bf16 v[196:199], v[128:131], v[144:147], v[196:199]\n" \
"v_max_f32_e32 v62, v62, v56\n" \
"v_add_f32_e32 v57, v57, v43\n" \
"v_mfma_f32_16x16x32_bf16 v[200:203], v[130:133], v[148:151], v[200:203]\n" \
"v_rcp_f32_e32 v56, v56\n" \
"v_mfma_f32_16x16x32_bf16 v[204:207], v[128:131], v[148:151], v[204:207]\n" \
"v_mul_f32_e32 v56, v56, v60\n" \
"v_mul_f32_e32 v192, v56, v192\n" \
"v_mfma_f32_16x16x32_bf16 v[208:211], v[130:133], v[152:155], v[208:211]\n" \
"v_mul_f32_e32 v193, v56, v193\n" \
"v_mul_f32_e32 v194, v56, v194\n" \
"v_mfma_f32_16x16x32_bf16 v[212:215], v[128:131], v[152:155], v[212:215]\n" \
"buffer_load_dwordx4 a[244:247], v54, s[56:59], s60 offen offset:2048 sc1\n" \
"v_mfma_f32_16x16x32_bf16 v[216:219], v[130:133], v[156:159], v[216:219]\n" \
"v_mul_f32_e32 v195, v56, v195\n" \
"v_mul_f32_e32 v196, v56, v196\n" \
"v_mfma_f32_16x16x32_bf16 v[220:223], v[128:131], v[156:159], v[220:223]\n" \
"v_mul_f32_e32 v197, v56, v197\n" \
"v_mul_f32_e32 v198, v56, v198\n" \
"v_mfma_f32_16x16x32_bf16 v[224:227], v[130:133], v[176:179], v[224:227]\n" \
"v_mul_f32_e32 v199, v56, v199\n" \
"v_cvt_pk_bf16_f32 v64, v192, v193\n" \
"v_mfma_f32_16x16x32_bf16 v[228:231], v[128:131], v[176:179], v[228:231]\n" \
"v_cvt_pk_bf16_f32 v65, v194, v195\n" \
"v_cvt_pk_bf16_f32 v66, v196, v197\n" \
"v_mfma_f32_16x16x32_bf16 v[232:235], v[130:133], v[180:183], v[232:235]\n" \
"v_cvt_pk_bf16_f32 v67, v198, v199\n" \
"v_mul_f32_e32 v200, v56, v200\n" \
"v_mfma_f32_16x16x32_bf16 v[236:239], v[128:131], v[180:183], v[236:239]\n" \
"buffer_load_dwordx4 a[232:235], v20, s[56:59], s60 offen offset:3072 sc1\n" \
"v_mfma_f32_16x16x32_bf16 v[240:243], v[130:133], v[184:187], v[240:243]\n" \
"v_mfma_f32_16x16x32_bf16 a[208:211], a[216:219], v[64:67], a[208:211]\n" \
"v_mul_f32_e32 v201, v56, v201\n" \
"v_mul_f32_e32 v202, v56, v202\n" \
"v_mul_f32_e32 v203, v56, v203\n" \
"v_mul_f32_e32 v204, v56, v204\n" \
"v_mul_f32_e32 v205, v56, v205\n" \
"v_mul_f32_e32 v206, v56, v206\n" \
"v_mul_f32_e32 v207, v56, v207\n" \
"v_cvt_pk_bf16_f32 v68, v200, v201\n" \
"v_cvt_pk_bf16_f32 v69, v202, v203\n" \
"v_cvt_pk_bf16_f32 v70, v204, v205\n" \
"v_cvt_pk_bf16_f32 v71, v206, v207\n" \
"v_mul_f32_e32 v208, v56, v208\n" \
"v_mul_f32_e32 v209, v56, v209\n" \
"v_mfma_f32_16x16x32_bf16 a[208:211], a[216:219], v[68:71], a[208:211]\n" \
"buffer_load_dwordx4 a[248:251], v54, s[56:59], s60 offen offset:3072 sc1\n" \
"v_mul_f32_e32 v210, v56, v210\n" \
"v_mul_f32_e32 v211, v56, v211\n" \
"v_mul_f32_e32 v212, v56, v212\n" \
"v_mul_f32_e32 v213, v56, v213\n" \
"v_mul_f32_e32 v214, v56, v214\n" \
"v_mul_f32_e32 v215, v56, v215\n" \
"v_max_f32_e32 v62, v62, v57\n" \
"v_cvt_pk_bf16_f32 v72, v208, v209\n" \
"v_cvt_pk_bf16_f32 v73, v210, v211\n" \
"v_cvt_pk_bf16_f32 v74, v212, v213\n" \
"v_cvt_pk_bf16_f32 v75, v214, v215\n" \
"v_rcp_f32_e32 v57, v57\n" \
"v_mfma_f32_16x16x32_bf16 v[244:247], v[128:131], v[184:187], v[244:247]\n" \
"v_mfma_f32_16x16x32_bf16 a[208:211], a[216:219], v[72:75], a[208:211]\n" \
"v_mul_f32_e32 v216, v56, v216\n" \
"v_mul_f32_e32 v217, v56, v217\n" \
"buffer_load_dwordx4 a[96:99], v20, s[56:59], s97 offen offset:0 sc1\n" \
"v_mul_f32_e32 v218, v56, v218\n" \
"v_mul_f32_e32 v219, v56, v219\n" \
"v_mul_f32_e32 v220, v56, v220\n" \
"v_mul_f32_e32 v221, v56, v221\n" \
"v_mul_f32_e32 v222, v56, v222\n" \
"v_mul_f32_e32 v223, v56, v223\n" \
"v_cvt_pk_bf16_f32 v76, v216, v217\n" \
"v_cvt_pk_bf16_f32 v77, v218, v219\n" \
"v_cvt_pk_bf16_f32 v78, v220, v221\n" \
"v_cvt_pk_bf16_f32 v79, v222, v223\n" \
"v_mul_f32_e32 v57, v57, v61\n" \
"v_mul_f32_e32 v224, v57, v224\n" \
"v_mfma_f32_16x16x32_bf16 a[208:211], a[216:219], v[76:79], a[208:211]\n" \
"v_mul_f32_e32 v225, v57, v225\n" \
"v_mul_f32_e32 v226, v57, v226\n" \
"v_mul_f32_e32 v227, v57, v227\n" \
"v_mul_f32_e32 v228, v57, v228\n" \
"v_mul_f32_e32 v229, v57, v229\n" \
"v_mul_f32_e32 v230, v57, v230\n" \
"v_mul_f32_e32 v231, v57, v231\n" \
"buffer_load_dwordx4 a[104:107], v54, s[56:59], s97 offen offset:0 sc1\n" \
"v_cvt_pk_bf16_f32 v80, v224, v225\n" \
"v_cvt_pk_bf16_f32 v81, v226, v227\n" \
"v_cvt_pk_bf16_f32 v82, v228, v229\n" \
"v_cvt_pk_bf16_f32 v83, v230, v231\n" \
"v_mfma_f32_16x16x32_bf16 v[248:251], v[130:133], v[188:191], v[248:251]\n" \
"v_mul_f32_e32 v232, v57, v232\n" \
"v_mul_f32_e32 v233, v57, v233\n" \
"v_mfma_f32_16x16x32_bf16 a[208:211], a[216:219], v[80:83], a[208:211]\n" \
"v_mul_f32_e32 v234, v57, v234\n" \
"v_mul_f32_e32 v235, v57, v235\n" \
"v_mul_f32_e32 v236, v57, v236\n" \
"v_mul_f32_e32 v237, v57, v237\n" \
"v_mul_f32_e32 v238, v57, v238\n" \
"v_mul_f32_e32 v239, v57, v239\n" \
"v_cvt_pk_bf16_f32 v84, v232, v233\n" \
"v_cvt_pk_bf16_f32 v85, v234, v235\n" \
"v_cvt_pk_bf16_f32 v86, v236, v237\n" \
"v_cvt_pk_bf16_f32 v87, v238, v239\n" \
"v_mul_f32_e32 v240, v57, v240\n" \
"buffer_load_dwordx4 a[112:115], v20, s[56:59], s98 offen offset:0 sc1\n" \
"v_mfma_f32_16x16x32_bf16 a[208:211], a[216:219], v[84:87], a[208:211]\n" \
"v_mul_f32_e32 v241, v57, v241\n" \
"v_mul_f32_e32 v242, v57, v242\n" \
"v_mul_f32_e32 v243, v57, v243\n" \
"v_mul_f32_e32 v244, v57, v244\n" \
"v_mul_f32_e32 v245, v57, v245\n" \
"v_mul_f32_e32 v246, v57, v246\n" \
"v_mul_f32_e32 v247, v57, v247\n" \
"v_cvt_pk_bf16_f32 v96, v240, v241\n" \
"v_cvt_pk_bf16_f32 v97, v242, v243\n" \
"v_cvt_pk_bf16_f32 v98, v244, v245\n" \
"v_cvt_pk_bf16_f32 v99, v246, v247\n" \
"v_mfma_f32_16x16x32_bf16 v[252:255], v[128:131], v[188:191], v[252:255]\n" \
"v_mul_f32_e32 v248, v57, v248\n" \
"v_mfma_f32_16x16x32_bf16 a[208:211], a[216:219], v[96:99], a[208:211]\n" \
"v_mul_f32_e32 v249, v57, v249\n" \
"v_mul_f32_e32 v250, v57, v250\n" \
"buffer_load_dwordx4 a[120:123], v54, s[56:59], s98 offen offset:0 sc1\n" \
"v_mul_f32_e32 v251, v57, v251\n" \
"v_cvt_pk_bf16_f32 v100, v248, v249\n" \
"v_cvt_pk_bf16_f32 v101, v250, v251\n" \
"v_mul_f32_e32 v252, v57, v252\n" \
"v_mul_f32_e32 v253, v57, v253\n" \
"v_mul_f32_e32 v254, v57, v254\n" \
"v_mul_f32_e32 v255, v57, v255\n" \
"v_cvt_pk_bf16_f32 v102, v252, v253\n" \
"v_cvt_pk_bf16_f32 v103, v254, v255\n" \
"v_cmp_lt_f32_e64 s[72:73], s21, v62\n" \
"buffer_load_dwordx4 a[100:103], v20, s[56:59], s97 offen offset:1024 sc1\n" \
"v_mfma_f32_16x16x32_bf16 a[208:211], a[216:219], v[100:103], a[208:211]\n" \
"buffer_load_dwordx4 a[108:111], v54, s[56:59], s97 offen offset:1024 sc1\n" \
"buffer_load_dwordx4 a[116:119], v20, s[56:59], s98 offen offset:1024 sc1\n" \
"buffer_load_dwordx4 a[124:127], v54, s[56:59], s98 offen offset:1024 sc1\n" \
"s_nop 4\n" \
"v_accvgpr_read_b32 v63, a208\n" \
"v_cmp_u_f32_e32 vcc, v63, v63\n" \
"s_nop 0\n" \
"s_or_b64 vcc, s[72:73], vcc\n" \
"s_cbranch_scc0 L_out_%=\n" \
"s_branch L_end_%=\n" \
"L_skb_%=:\n" \
"s_sleep 1\n" \
"buffer_load_dword v57, v61, s[56:59], s96 offen sc1\n" \
"s_add_u32 s101, s101, 1\n" \
"s_waitcnt vmcnt(0)\n" \
"v_cmp_ne_u32_e32 vcc, s62, v57\n" \
"s_cmp_gt_u32 s101, 0x100000\n" \
"s_cbranch_scc1 L_rkb_%=\n" \
"s_nop 2\n" \
"s_cbranch_vccnz L_skb_%=\n" \
"s_branch L_rkb_%=\n" \
"L_so0_%=:\n" \
"s_sleep 1\n" \
"buffer_load_dword v55, v58, s[56:59], s96 offen sc1\n" \
"s_add_u32 s101, s101, 1\n" \
"s_waitcnt vmcnt(0)\n" \
"v_cmp_ne_u32_e32 vcc, s62, v55\n" \
"s_cmp_gt_u32 s101, 0x100000\n" \
"s_cbranch_scc1 L_ro0_%=\n" \
"s_nop 2\n" \
"s_cbranch_vccnz L_so0_%=\n" \
"s_branch L_ro0_%=\n" \
"L_so1_%=:\n" \
"s_sleep 1\n" \
"buffer_load_dword v57, v60, s[56:59], s96 offen sc1\n" \
"s_add_u32 s101, s101, 1\n" \
"s_waitcnt vmcnt(0)\n" \
"v_cmp_ne_u32_e32 vcc, s62, v57\n" \
"s_cmp_gt_u32 s101, 0x100000\n" \
"s_cbranch_scc1 L_ro1_%=\n" \
"s_nop 2\n" \
"s_cbranch_vccnz L_so1_%=\n" \
"s_branch L_ro1_%=\n" \
"L_sa_%=:\n" \
"s_sleep 1\n" \
"buffer_load_dword v26, v38, s[56:59], s64 offen sc1\n" \
"s_add_u32 s101, s101, 1\n" \
"s_waitcnt vmcnt(0)\n" \
"v_cmp_ne_u32_e32 vcc, s62, v26\n" \
"s_cmp_gt_u32 s101, 0x100000\n" \
"s_cbranch_scc1 L_ra_%=\n" \
"s_nop 2\n" \
"s_cbranch_vccnz L_sa_%=\n" \
"s_branch L_ra_%=\n" \
"L_sb_%=:\n" \
"s_sleep 1\n" \
"buffer_load_dword v47, v38, s[56:59], s73 offen sc1\n" \
"s_add_u32 s101, s101, 1\n" \
"s_waitcnt vmcnt(0)\n" \
"v_cmp_ne_u32_e32 vcc, s62, v47\n" \
"s_cmp_gt_u32 s101, 0x100000\n" \
"s_cbranch_scc1 L_rb_%=\n" \
"s_nop 2\n" \
"s_cbranch_vccnz L_sb_%=\n" \
"s_branch L_rb_%=\n" \
"L_sx0_%=:\n" \
"s_sleep 1\n" \
"buffer_load_dword v37, v40, s[56:59], s74 offen sc1\n" \
"s_add_u32 s101, s101, 1\n" \
"s_waitcnt vmcnt(0)\n" \
"v_cmp_eq_u32_e64 s[98:99], s62, v37\n" \
"s_cmp_gt_u32 s101, 0x100000\n" \
"s_cbranch_scc1 L_rx0_%=\n" \
"s_or_b32 s75, 0x7f, s100\n" \
"s_andn2_b32 s75, s75, s98\n" \
"s_cbranch_scc1 L_sx0_%=\n" \
"s_branch L_rx0_%=\n" \
"L_sx1_%=:\n" \
"s_sleep 1\n" \
"buffer_load_dword v37, v40, s[56:59], s74 offen sc1\n" \
"s_add_u32 s101, s101, 1\n" \
"s_waitcnt vmcnt(0)\n" \
"v_cmp_eq_u32_e64 s[98:99], s62, v37\n" \
"s_cmp_gt_u32 s101, 0x100000\n" \
"s_cbranch_scc1 L_rx1_%=\n" \
"s_andn2_b32 s75, 0x100, s98\n" \
"s_cbranch_scc1 L_sx1_%=\n" \
"s_branch L_rx1_%=\n" \
"L_sx2_%=:\n" \
"s_sleep 1\n" \
"buffer_load_dword v37, v40, s[56:59], s74 offen sc1\n" \
"s_add_u32 s101, s101, 1\n" \
"s_waitcnt vmcnt(0)\n" \
"v_cmp_eq_u32_e64 s[98:99], s62, v37\n" \
"s_cmp_gt_u32 s101, 0x100000\n" \
"s_cbranch_scc1 L_rx2_%=\n" \
"s_andn2_b32 s75, 0x200, s98\n" \
"s_cbranch_scc1 L_sx2_%=\n" \
"s_branch L_rx2_%=\n" \
"L_sx3_%=:\n" \
"s_sleep 1\n" \
"buffer_load_dword v37, v40, s[56:59], s74 offen sc1\n" \
"s_add_u32 s101, s101, 1\n" \
"s_waitcnt vmcnt(0)\n" \
"v_cmp_eq_u32_e64 s[98:99], s62, v37\n" \
"s_cmp_gt_u32 s101, 0x100000\n" \
"s_cbranch_scc1 L_rx3_%=\n" \
"s_andn2_b32 s75, 0x400, s98\n" \
"s_cbranch_scc1 L_sx3_%=\n" \
"s_branch L_rx3_%=\n" \
"L_sx4_%=:\n" \
"s_sleep 1\n" \
"buffer_load_dword v37, v40, s[56:59], s74 offen sc1\n" \
"s_add_u32 s101, s101, 1\n" \
"s_waitcnt vmcnt(0)\n" \
"v_cmp_eq_u32_e64 s[98:99], s62, v37\n" \
"s_cmp_gt_u32 s101, 0x100000\n" \
"s_cbranch_scc1 L_rx4_%=\n" \
"s_andn2_b32 s75, 0x800, s98\n" \
"s_cbranch_scc1 L_sx4_%=\n" \
"s_branch L_rx4_%=\n" \
"L_sx5_%=:\n" \
"s_sleep 1\n" \
"buffer_load_dword v37, v40, s[56:59], s74 offen sc1\n" \
"s_add_u32 s101, s101, 1\n" \
"s_waitcnt vmcnt(0)\n" \
"v_cmp_eq_u32_e64 s[98:99], s62, v37\n" \
"s_cmp_gt_u32 s101, 0x100000\n" \
"s_cbranch_scc1 L_rx5_%=\n" \
"s_andn2_b32 s75, 0x1000, s98\n" \
"s_cbranch_scc1 L_sx5_%=\n" \
"s_branch L_rx5_%=\n" \
"L_sx6_%=:\n" \
"s_sleep 1\n" \
"buffer_load_dword v37, v40, s[56:59], s74 offen sc1\n" \
"s_add_u32 s101, s101, 1\n" \
"s_waitcnt vmcnt(0)\n" \
"v_cmp_eq_u32_e64 s[98:99], s62, v37\n" \
"s_cmp_gt_u32 s101, 0x100000\n" \
"s_cbranch_scc1 L_rx6_%=\n" \
"s_andn2_b32 s75, 0x2000, s98\n" \
"s_cbranch_scc1 L_sx6_%=\n" \
"s_branch L_rx6_%=\n" \
"L_sx7_%=:\n" \
"s_sleep 1\n" \
"buffer_load_dword v37, v40, s[56:59], s74 offen sc1\n" \
"s_add_u32 s101, s101, 1\n" \
"s_waitcnt vmcnt(0)\n" \
"v_cmp_eq_u32_e64 s[98:99], s62, v37\n" \
"s_cmp_gt_u32 s101, 0x100000\n" \
"s_cbranch_scc1 L_rx7_%=\n" \
"s_andn2_b32 s75, 0x4000, s98\n" \
"s_cbranch_scc1 L_sx7_%=\n" \
"s_branch L_rx7_%=\n" \
"L_att_stub:\n" \
"s_mov_b32 s101, 0\n" \
"v_add_u32_e32 v32, 0x49000, v20\n" \
"v_readlane_b32 s96, v11, 35\n" \
"v_readlane_b32 s97, v11, 36\n" \
"buffer_load_dwordx4 a[220:223], v20, s[56:59], s60 offen offset:0 sc1\n" \
"buffer_load_dwordx4 a[236:239], v32, s[56:59], s60 offen offset:0 sc1\n" \
"buffer_load_dwordx4 a[224:227], v20, s[56:59], s60 offen offset:1024 sc1\n" \
"buffer_load_dwordx4 a[240:243], v32, s[56:59], s60 offen offset:1024 sc1\n" \
"buffer_load_dwordx4 a[228:231], v20, s[56:59], s60 offen offset:2048 sc1\n" \
"buffer_load_dwordx4 a[244:247], v32, s[56:59], s60 offen offset:2048 sc1\n" \
"buffer_load_dwordx4 a[232:235], v20, s[56:59], s60 offen offset:3072 sc1\n" \
"buffer_load_dwordx4 a[248:251], v32, s[56:59], s60 offen offset:3072 sc1\n" \
"buffer_load_dwordx4 a[96:99], v20, s[56:59], s96 offen offset:0 sc1\n" \
"buffer_load_dwordx4 a[104:107], v32, s[56:59], s96 offen offset:0 sc1\n" \
"buffer_load_dwordx4 a[112:115], v20, s[56:59], s97 offen offset:0 sc1\n" \
"buffer_load_dwordx4 a[120:123], v32, s[56:59], s97 offen offset:0 sc1\n" \
"buffer_load_dwordx4 a[100:103], v20, s[56:59], s96 offen offset:1024 sc1\n" \
"buffer_load_dwordx4 a[108:111], v32, s[56:59], s96 offen offset:1024 sc1\n" \
"buffer_load_dwordx4 a[116:119], v20, s[56:59], s97 offen offset:1024 sc1\n" \
"buffer_load_dwordx4 a[124:127], v32, s[56:59], s97 offen offset:1024 sc1\n" \
"buffer_load_dwordx4 v[64:67], v20, s[56:59], s61 offen offset:0 sc1\n" \
"buffer_load_dwordx4 v[68:71], v20, s[56:59], s61 offen offset:1024 sc1\n" \
"buffer_load_dwordx4 v[72:75], v20, s[56:59], s61 offen offset:2048 sc1\n" \
"buffer_load_dwordx4 v[76:79], v20, s[56:59], s61 offen offset:3072 sc1\n" \
"buffer_load_dwordx4 v[80:83], v32, s[56:59], s61 offen offset:0 sc1\n" \
"buffer_load_dwordx4 v[84:87], v32, s[56:59], s61 offen offset:1024 sc1\n" \
"buffer_load_dwordx4 v[96:99], v32, s[56:59], s61 offen offset:2048 sc1\n" \
"buffer_load_dwordx4 v[100:103], v32, s[56:59], s61 offen offset:3072 sc1\n" \
"v_not_b32_e32 v26, s62\n" \
"s_waitcnt vmcnt(0)\n" \
"s_branch L_out_%=\n" \
"L_sw_%=:\n" \
"v_mfma_f32_16x16x32_bf16 v[104:107], a[128:131], a[16:19], v[36:39]\n" \
"s_add_u32 s68, s60, 0xfff6e000\n" \
"s_add_u32 s69, s60, 0xfffb7000\n" \
"s_add_u32 s70, s60, 0x0\n" \
"s_add_u32 s71, s60, 0x49000\n" \
"v_mfma_f32_16x16x32_bf16 v[104:107], a[132:135], a[20:23], v[104:107]\n" \
"s_xor_b32 m0, s65, 0x12000\n" \
"ds_read_b128 a[160:163], v49 offset:0\n" \
"v_mfma_f32_16x16x32_bf16 v[104:107], a[136:139], a[24:27], v[104:107]\n" \
"buffer_load_dwordx4 v50, s[8:11], s64 offen offset:0 sc1 lds\n" \
"v_mfma_f32_16x16x32_bf16 v[104:107], a[140:143], a[28:31], v[104:107]\n" \
"ds_read_b128 a[164:167], v49 offset:1024\n" \
"ds_read_b128 a[168:171], v49 offset:2048\n" \
"v_mfma_f32_16x16x32_bf16 v[108:111], a[156:159], a[28:31], v[36:39]\n" \
"ds_read_b128 a[172:175], v49 offset:3072\n" \
"ds_read_b128 a[176:179], v49 offset:4096\n" \
"v_mfma_f32_16x16x32_bf16 v[108:111], a[152:155], a[24:27], v[108:111]\n" \
"buffer_load_dwordx4 v50, s[8:11], s64 offen offset:1024 sc1 lds\n" \
"v_mfma_f32_16x16x32_bf16 v[108:111], a[148:151], a[20:23], v[108:111]\n" \
"s_mov_b32 s65, s13\n" \
"v_add_u32_e32 v63, s15, v20\n" \
"v_mfma_f32_16x16x32_bf16 v[108:111], a[144:147], a[16:19], v[108:111]\n" \
"s_add_u32 s67, s14, 40960\n" \
"v_add_u32_e32 v50, 0x6000, v63\n" \
"v_mfma_f32_16x16x32_bf16 v[116:119], a[144:147], a[32:35], v[40:43]\n" \
"s_xor_b32 m0, s65, 0x8000\n" \
"ds_read_b128 a[180:183], v49 offset:5120\n" \
"v_mfma_f32_16x16x32_bf16 v[116:119], a[148:151], a[36:39], v[116:119]\n" \
"ds_read_b128 a[184:187], v49 offset:6144\n" \
"v_and_b32_e32 v68, v12, v68\n" \
"v_mfma_f32_16x16x32_bf16 v[116:119], a[152:155], a[40:43], v[116:119]\n" \
"v_and_b32_e32 v69, v13, v69\n" \
"v_and_b32_e32 v70, v14, v70\n" \
"v_mfma_f32_16x16x32_bf16 v[116:119], a[156:159], a[44:47], v[116:119]\n" \
"buffer_load_dwordx4 v50, s[8:11], s67 offen offset:0 sc1 lds\n" \
"v_mfma_f32_16x16x32_bf16 v[112:115], a[140:143], a[44:47], v[40:43]\n" \
"v_and_b32_e32 v71, v15, v71\n" \
"ds_read_b128 a[188:191], v49 offset:7168\n" \
"v_mfma_f32_16x16x32_bf16 v[112:115], a[136:139], a[40:43], v[112:115]\n" \
"v_exp_f32_e32 v86, v86\n" \
"v_mfma_f32_16x16x32_bf16 v[112:115], a[132:135], a[36:39], v[112:115]\n" \
"v_exp_f32_e32 v87, v87\n" \
"v_mfma_f32_16x16x32_bf16 v[112:115], a[128:131], a[32:35], v[112:115]\n" \
"v_exp_f32_e32 v84, v84\n" \
"v_mfma_f32_16x16x32_bf16 v[120:123], a[128:131], a[48:51], v[44:47]\n" \
"buffer_load_dwordx4 v50, s[8:11], s67 offen offset:1024 sc1 lds\n" \
"v_mfma_f32_16x16x32_bf16 v[120:123], a[132:135], a[52:55], v[120:123]\n" \
"v_exp_f32_e32 v85, v85\n" \
"v_mfma_f32_16x16x32_bf16 v[120:123], a[136:139], a[56:59], v[120:123]\n" \
"v_cvt_pk_bf16_f32 v87, v86, v87\n" \
"v_cvt_pk_bf16_f32 v86, v84, v85\n" \
"v_mfma_f32_16x16x32_bf16 v[120:123], a[140:143], a[60:63], v[120:123]\n" \
"v_exp_f32_e32 v82, v82\n" \
"v_mfma_f32_16x16x32_bf16 v[124:127], a[156:159], a[60:63], v[44:47]\n" \
"v_exp_f32_e32 v83, v83\n" \
"v_mfma_f32_16x16x32_bf16 v[124:127], a[152:155], a[56:59], v[124:127]\n" \
"v_exp_f32_e32 v80, v80\n" \
"v_mfma_f32_16x16x32_bf16 v[124:127], a[148:151], a[52:55], v[124:127]\n" \
"buffer_load_dwordx4 v50, s[8:11], s67 offen offset:2048 sc1 lds\n" \
"v_mfma_f32_16x16x32_bf16 v[124:127], a[144:147], a[48:51], v[124:127]\n" \
"v_cvt_pk_bf16_f32 v85, v82, v83\n" \
"s_waitcnt lgkmcnt(1)\n" \
"v_mfma_f32_16x16x32_bf16 v[128:131], a[160:163], v[68:71], v[128:131]\n" \
"v_exp_f32_e32 v81, v81\n" \
"v_mfma_f32_16x16x32_bf16 v[160:163], a[160:163], v[72:75], v[160:163]\n" \
"v_cvt_pk_bf16_f32 v84, v80, v81\n" \
"s_mov_b32 s64, s14\n" \
"v_mfma_f32_16x16x32_bf16 v[132:135], a[164:167], v[68:71], v[132:135]\n" \
"v_exp_f32_e32 v88, v88\n" \
"v_mfma_f32_16x16x32_bf16 v[164:167], a[164:167], v[72:75], v[164:167]\n" \
"v_exp_f32_e32 v89, v89\n" \
"v_mfma_f32_16x16x32_bf16 v[136:139], a[168:171], v[68:71], v[136:139]\n" \
"buffer_load_dwordx4 v50, s[8:11], s67 offen offset:3072 sc1 lds\n" \
"v_mfma_f32_16x16x32_bf16 v[168:171], a[168:171], v[72:75], v[168:171]\n" \
"v_exp_f32_e32 v90, v90\n" \
"v_mfma_f32_16x16x32_bf16 v[140:143], a[172:175], v[68:71], v[140:143]\n" \
"v_exp_f32_e32 v91, v91\n" \
"v_mfma_f32_16x16x32_bf16 v[172:175], a[172:175], v[72:75], v[172:175]\n" \
"v_cvt_pk_bf16_f32 v88, v88, v89\n" \
"v_cvt_pk_bf16_f32 v89, v90, v91\n" \
"v_mfma_f32_16x16x32_bf16 v[144:147], a[176:179], v[68:71], v[144:147]\n" \
"v_exp_f32_e32 v92, v92\n" \
"v_mfma_f32_16x16x32_bf16 v[176:179], a[176:179], v[72:75], v[176:179]\n" \
"v_exp_f32_e32 v93, v93\n" \
"v_mfma_f32_16x16x32_bf16 v[148:151], a[180:183], v[68:71], v[148:151]\n" \
"v_exp_f32_e32 v94, v94\n" \
"v_mfma_f32_16x16x32_bf16 v[180:183], a[180:183], v[72:75], v[180:183]\n" \
"v_exp_f32_e32 v95, v95\n" \
"v_mfma_f32_16x16x32_bf16 v[152:155], a[184:187], v[68:71], v[152:155]\n" \
"v_cvt_pk_bf16_f32 v90, v92, v93\n" \
"v_cvt_pk_bf16_f32 v91, v94, v95\n" \
"v_mfma_f32_16x16x32_bf16 v[184:187], a[184:187], v[72:75], v[184:187]\n" \
"v_add_u32_e32 v51, 0x12000, v63\n" \
"s_waitcnt lgkmcnt(0)\n" \
"v_mfma_f32_16x16x32_bf16 v[156:159], a[188:191], v[68:71], v[156:159]\n" \
"s_xor_b32 m0, s65, 0xa000\n" \
"ds_read_b32 v58, v18 offset:512\n" \
"v_mfma_f32_16x16x32_bf16 v[188:191], a[188:191], v[72:75], v[188:191]\n" \
"buffer_load_dwordx4 v51, s[8:11], s64 offen offset:0 sc1 lds\n" \
"v_smfmac_f32_16x16x64_bf16 a[192:195], a[200:203], v[68:75], v21\n" \
"buffer_load_dwordx4 v51, s[8:11], s64 offen offset:1024 sc1 lds\n" \
"v_mfma_f32_16x16x32_bf16 v[220:223], a[188:191], v[84:87], v[220:223]\n" \
"ds_read_b32 v59, v18 offset:640\n" \
"ds_read_b32 v60, v18 offset:768\n" \
"v_mfma_f32_16x16x32_bf16 v[252:255], a[188:191], v[88:91], v[252:255]\n" \
"ds_read_b32 v61, v18 offset:896\n" \
"ds_read_b128 a[128:131], v48 offset:0\n" \
"v_mfma_f32_16x16x32_bf16 v[216:219], a[184:187], v[84:87], v[216:219]\n" \
"ds_read_b128 a[144:147], v48 offset:4096\n" \
"ds_read_b128 a[132:135], v48 offset:1024\n" \
"v_mfma_f32_16x16x32_bf16 v[248:251], a[184:187], v[88:91], v[248:251]\n" \
"ds_read_b128 a[148:151], v48 offset:5120\n" \
"ds_read_b128 a[136:139], v48 offset:2048\n" \
"v_mfma_f32_16x16x32_bf16 v[212:215], a[180:183], v[84:87], v[212:215]\n" \
"ds_read_b128 a[152:155], v48 offset:6144\n" \
"ds_read_b128 a[140:143], v48 offset:3072\n" \
"v_mfma_f32_16x16x32_bf16 v[244:247], a[180:183], v[88:91], v[244:247]\n" \
"ds_read_b128 a[156:159], v48 offset:7168\n" \
"v_add_u32_e32 v52, 0x14000, v63\n" \
"v_mfma_f32_16x16x32_bf16 v[208:211], a[176:179], v[84:87], v[208:211]\n" \
"buffer_load_dwordx4 v51, s[8:11], s64 offen offset:2048 sc1 lds\n" \
"v_mfma_f32_16x16x32_bf16 v[240:243], a[176:179], v[88:91], v[240:243]\n" \
"v_exp_f32_e32 v104, v104\n" \
"v_mfma_f32_16x16x32_bf16 v[204:207], a[172:175], v[84:87], v[204:207]\n" \
"v_exp_f32_e32 v105, v105\n" \
"v_mfma_f32_16x16x32_bf16 v[236:239], a[172:175], v[88:91], v[236:239]\n" \
"buffer_load_dwordx4 v51, s[8:11], s64 offen offset:3072 sc1 lds\n" \
"v_mfma_f32_16x16x32_bf16 v[200:203], a[168:171], v[84:87], v[200:203]\n" \
"v_exp_f32_e32 v106, v106\n" \
"v_mfma_f32_16x16x32_bf16 v[232:235], a[168:171], v[88:91], v[232:235]\n" \
"v_exp_f32_e32 v107, v107\n" \
"v_mfma_f32_16x16x32_bf16 v[196:199], a[164:167], v[84:87], v[196:199]\n" \
"v_cvt_pk_bf16_f32 v104, v104, v105\n" \
"v_cvt_pk_bf16_f32 v105, v106, v107\n" \
"v_mfma_f32_16x16x32_bf16 v[228:231], a[164:167], v[88:91], v[228:231]\n" \
"v_exp_f32_e32 v108, v108\n" \
"v_mfma_f32_16x16x32_bf16 v[192:195], a[160:163], v[84:87], v[192:195]\n" \
"v_exp_f32_e32 v109, v109\n" \
"v_mfma_f32_16x16x32_bf16 v[224:227], a[160:163], v[88:91], v[224:227]\n" \
"v_exp_f32_e32 v110, v110\n" \
"v_smfmac_f32_16x16x64_bf16 a[196:199], a[200:203], v[84:91], v21\n" \
"v_exp_f32_e32 v111, v111\n" \
"v_cvt_pk_bf16_f32 v106, v108, v109\n" \
"v_cvt_pk_bf16_f32 v107, v110, v111\n" \
"v_and_b32_e32 v104, v12, v104\n" \
"v_and_b32_e32 v105, v13, v105\n" \
"v_and_b32_e32 v106, v14, v106\n" \
"v_and_b32_e32 v107, v15, v107\n" \
"v_accvgpr_read_b32 v54, a192\n" \
"v_accvgpr_read_b32 v22, a193\n" \
"v_add_u32_e32 v53, 0x20000, v63\n" \
"v_exp_f32_e32 v118, v118\n" \
"v_exp_f32_e32 v119, v119\n" \
"v_exp_f32_e32 v116, v116\n" \
"v_exp_f32_e32 v117, v117\n" \
"v_cvt_pk_bf16_f32 v119, v118, v119\n" \
"v_exp_f32_e32 v114, v114\n" \
"v_exp_f32_e32 v115, v115\n" \
"v_exp_f32_e32 v112, v112\n" \
"v_exp_f32_e32 v113, v113\n" \
"v_cvt_pk_bf16_f32 v118, v116, v117\n" \
"v_cvt_pk_bf16_f32 v117, v114, v115\n" \
"v_cvt_pk_bf16_f32 v116, v112, v113\n" \
"v_readlane_b32 s93, v11, 19\n" \
"s_waitcnt vmcnt(10) lgkmcnt(0)\n" \
"s_barrier\n" \
"v_mfma_f32_16x16x32_bf16 v[80:83], a[128:131], a[32:35], v[40:43]\n" \
"ds_read_b128 a[160:163], v49 offset:8192\n" \
"ds_read_b128 a[164:167], v49 offset:9216\n" \
"v_mfma_f32_16x16x32_bf16 v[80:83], a[132:135], a[36:39], v[80:83]\n" \
"ds_read_b128 a[168:171], v49 offset:10240\n" \
"s_xor_b32 m0, s65, 0x18000\n" \
"v_mfma_f32_16x16x32_bf16 v[80:83], a[136:139], a[40:43], v[80:83]\n" \
"buffer_load_dwordx4 v52, s[8:11], s64 offen offset:0 sc1 lds\n" \
"v_mfma_f32_16x16x32_bf16 v[80:83], a[140:143], a[44:47], v[80:83]\n" \
"ds_read_b128 a[172:175], v49 offset:11264\n" \
"ds_read_b128 a[176:179], v49 offset:12288\n" \
"v_mfma_f32_16x16x32_bf16 v[84:87], a[156:159], a[44:47], v[40:43]\n" \
"ds_read_b128 a[180:183], v49 offset:13312\n" \
"ds_read_b128 a[184:187], v49 offset:14336\n" \
"v_mfma_f32_16x16x32_bf16 v[84:87], a[152:155], a[40:43], v[84:87]\n" \
"buffer_load_dwordx4 v52, s[8:11], s64 offen offset:1024 sc1 lds\n" \
"v_mfma_f32_16x16x32_bf16 v[84:87], a[148:151], a[36:39], v[84:87]\n" \
"ds_read_b128 a[188:191], v49 offset:15360\n" \
"v_readlane_b32 s80, v11, 11\n" \
"v_mfma_f32_16x16x32_bf16 v[84:87], a[144:147], a[32:35], v[84:87]\n" \
"buffer_load_dwordx4 v52, s[8:11], s64 offen offset:2048 sc1 lds\n" \
"v_mfma_f32_16x16x32_bf16 v[92:95], a[144:147], a[48:51], v[44:47]\n" \
"v_exp_f32_e32 v120, v120\n" \
"v_mfma_f32_16x16x32_bf16 v[92:95], a[148:151], a[52:55], v[92:95]\n" \
"v_exp_f32_e32 v121, v121\n" \
"v_mfma_f32_16x16x32_bf16 v[92:95], a[152:155], a[56:59], v[92:95]\n" \
"buffer_load_dwordx4 v52, s[8:11], s64 offen offset:3072 sc1 lds\n" \
"v_mfma_f32_16x16x32_bf16 v[92:95], a[156:159], a[60:63], v[92:95]\n" \
"s_mov_b32 m0, s65\n" \
"v_cvt_pk_bf16_f32 v120, v120, v121\n" \
"v_mfma_f32_16x16x32_bf16 v[88:91], a[140:143], a[60:63], v[44:47]\n" \
"v_exp_f32_e32 v122, v122\n" \
"v_mfma_f32_16x16x32_bf16 v[88:91], a[136:139], a[56:59], v[88:91]\n" \
"buffer_load_dwordx4 v53, s[8:11], s64 offen offset:0 sc1 lds\n" \
"v_mfma_f32_16x16x32_bf16 v[88:91], a[132:135], a[52:55], v[88:91]\n" \
"v_exp_f32_e32 v123, v123\n" \
"v_mfma_f32_16x16x32_bf16 v[88:91], a[128:131], a[48:51], v[88:91]\n" \
"v_exp_f32_e32 v124, v124\n" \
"v_mfma_f32_16x16x32_bf16 a[192:195], a[212:215], v[104:107], a[192:195]\n" \
"v_cvt_pk_bf16_f32 v121, v122, v123\n" \
"s_waitcnt lgkmcnt(5)\n" \
"v_mfma_f32_16x16x32_bf16 v[160:163], a[160:163], v[104:107], v[160:163]\n" \
"buffer_load_dwordx4 v53, s[8:11], s64 offen offset:1024 sc1 lds\n" \
"v_mfma_f32_16x16x32_bf16 v[164:167], a[164:167], v[104:107], v[164:167]\n" \
"v_exp_f32_e32 v125, v125\n" \
"v_mfma_f32_16x16x32_bf16 v[168:171], a[168:171], v[104:107], v[168:171]\n" \
"v_cvt_pk_bf16_f32 v122, v124, v125\n" \
"s_waitcnt lgkmcnt(1)\n" \
"v_mfma_f32_16x16x32_bf16 v[172:175], a[172:175], v[104:107], v[172:175]\n" \
"v_exp_f32_e32 v126, v126\n" \
"v_mfma_f32_16x16x32_bf16 v[176:179], a[176:179], v[104:107], v[176:179]\n" \
"v_exp_f32_e32 v127, v127\n" \
"v_mfma_f32_16x16x32_bf16 v[180:183], a[180:183], v[104:107], v[180:183]\n" \
"v_cvt_pk_bf16_f32 v123, v126, v127\n" \
"v_readlane_b32 s81, v11, 12\n" \
"v_mfma_f32_16x16x32_bf16 v[184:187], a[184:187], v[104:107], v[184:187]\n" \
"s_mov_b32 s82, 0x47fca0\n" \
"s_waitcnt lgkmcnt(0)\n" \
"v_mfma_f32_16x16x32_bf16 v[188:191], a[188:191], v[104:107], v[188:191]\n" \
"buffer_load_dwordx4 v53, s[8:11], s64 offen offset:2048 sc1 lds\n" \
"v_mfma_f32_16x16x32_bf16 v[220:223], a[188:191], v[116:119], v[220:223]\n" \
"buffer_load_dwordx4 v53, s[8:11], s64 offen offset:3072 sc1 lds\n" \
"v_mfma_f32_16x16x32_bf16 v[252:255], a[188:191], v[120:123], v[252:255]\n" \
"s_mov_b32 s83, 0x20000\n" \
"v_readlane_b32 s92, v11, 25\n" \
"v_mfma_f32_16x16x32_bf16 v[216:219], a[184:187], v[116:119], v[216:219]\n" \
"s_add_u32 m0, s93, 0x0\n" \
"ds_read_b128 a[128:131], v48 offset:8192\n" \
"v_mfma_f32_16x16x32_bf16 v[248:251], a[184:187], v[120:123], v[248:251]\n" \
"ds_read_b128 a[144:147], v48 offset:12288\n" \
"ds_read_b128 a[132:135], v48 offset:9216\n" \
"v_mfma_f32_16x16x32_bf16 v[212:215], a[180:183], v[116:119], v[212:215]\n" \
"buffer_load_dwordx4 v0, s[80:83], s92 offen offset:0 lds\n" \
"v_mfma_f32_16x16x32_bf16 v[244:247], a[180:183], v[120:123], v[244:247]\n" \
"buffer_load_dwordx4 v1, s[80:83], s92 offen offset:1024 lds\n" \
"v_mfma_f32_16x16x32_bf16 v[208:211], a[176:179], v[116:119], v[208:211]\n" \
"ds_read_b128 a[148:151], v48 offset:13312\n" \
"ds_read_b128 a[136:139], v48 offset:10240\n" \
"v_mfma_f32_16x16x32_bf16 v[240:243], a[176:179], v[120:123], v[240:243]\n" \
"ds_read_b128 a[152:155], v48 offset:14336\n" \
"ds_read_b128 a[140:143], v48 offset:11264\n" \
"v_mfma_f32_16x16x32_bf16 v[204:207], a[172:175], v[116:119], v[204:207]\n" \
"buffer_load_dwordx4 v2, s[80:83], s92 offen offset:2048 lds\n" \
"v_mfma_f32_16x16x32_bf16 v[236:239], a[172:175], v[120:123], v[236:239]\n" \
"ds_read_b128 a[156:159], v48 offset:15360\n" \
"s_xor_b32 s65, s65, 0x8000\n" \
"v_mfma_f32_16x16x32_bf16 v[200:203], a[168:171], v[116:119], v[200:203]\n" \
"s_add_u32 s64, s64, 0x10000\n" \
"v_add_u32_e32 v49, 0x10000, v48\n" \
"v_mfma_f32_16x16x32_bf16 v[232:235], a[168:171], v[120:123], v[232:235]\n" \
"buffer_load_dwordx4 v3, s[80:83], s92 offen offset:3072 lds\n" \
"v_mfma_f32_16x16x32_bf16 v[196:199], a[164:167], v[116:119], v[196:199]\n" \
"v_exp_f32_e32 v86, v86\n" \
"v_mfma_f32_16x16x32_bf16 v[228:231], a[164:167], v[120:123], v[228:231]\n" \
"v_exp_f32_e32 v87, v87\n" \
"v_mfma_f32_16x16x32_bf16 v[192:195], a[160:163], v[116:119], v[192:195]\n" \
"v_exp_f32_e32 v84, v84\n" \
"v_mfma_f32_16x16x32_bf16 v[224:227], a[160:163], v[120:123], v[224:227]\n" \
"v_exp_f32_e32 v85, v85\n" \
"v_smfmac_f32_16x16x64_bf16 a[196:199], a[200:203], v[116:123], v21\n" \
"v_cvt_pk_bf16_f32 v87, v86, v87\n" \
"v_exp_f32_e32 v82, v82\n" \
"v_exp_f32_e32 v83, v83\n" \
"v_exp_f32_e32 v80, v80\n" \
"v_exp_f32_e32 v81, v81\n" \
"v_cvt_pk_bf16_f32 v86, v84, v85\n" \
"v_cvt_pk_bf16_f32 v85, v82, v83\n" \
"v_cvt_pk_bf16_f32 v84, v80, v81\n" \
"v_and_b32_e32 v84, v12, v84\n" \
"v_and_b32_e32 v85, v13, v85\n" \
"v_and_b32_e32 v86, v14, v86\n" \
"v_and_b32_e32 v87, v15, v87\n" \
"v_exp_f32_e32 v88, v88\n" \
"v_exp_f32_e32 v89, v89\n" \
"v_exp_f32_e32 v90, v90\n" \
"v_exp_f32_e32 v91, v91\n" \
"v_cvt_pk_bf16_f32 v88, v88, v89\n" \
"v_exp_f32_e32 v92, v92\n" \
"v_exp_f32_e32 v93, v93\n" \
"v_exp_f32_e32 v94, v94\n" \
"v_exp_f32_e32 v95, v95\n" \
"v_cvt_pk_bf16_f32 v89, v90, v91\n" \
"v_cvt_pk_bf16_f32 v90, v92, v93\n" \
"v_cvt_pk_bf16_f32 v91, v94, v95\n" \
"v_add_u32_e32 v48, s16, v20\n" \
"v_add_f32_e32 v54, v54, v22\n" \
"v_accvgpr_read_b32 v55, a194\n" \
"v_accvgpr_read_b32 v23, a195\n" \
"v_mfma_f32_16x16x32_bf16 a[192:195], a[216:219], a[216:219], 0\n" \
"v_max_f32_e32 v62, v62, v54\n" \
"v_add_f32_e32 v55, v55, v23\n" \
"v_max_f32_e32 v62, v62, v55\n" \
"v_rcp_f32_e32 v54, v54\n" \
"v_rcp_f32_e32 v55, v55\n" \
"v_mul_f32_e32 v54, v54, v58\n" \
"v_mul_f32_e32 v55, v55, v59\n" \
"v_mul_f32_e32 v128, v54, v128\n" \
"v_mul_f32_e32 v129, v54, v129\n" \
"v_mul_f32_e32 v136, v54, v136\n" \
"v_mul_f32_e32 v137, v54, v137\n" \
"v_mul_f32_e32 v144, v54, v144\n" \
"v_mul_f32_e32 v145, v54, v145\n" \
"v_mul_f32_e32 v152, v54, v152\n" \
"v_mul_f32_e32 v153, v54, v153\n" \
"v_mul_f32_e32 v160, v55, v160\n" \
"v_mul_f32_e32 v161, v55, v161\n" \
"v_mul_f32_e32 v168, v55, v168\n" \
"v_mul_f32_e32 v169, v55, v169\n" \
"v_mul_f32_e32 v176, v55, v176\n" \
"v_mul_f32_e32 v177, v55, v177\n" \
"v_mul_f32_e32 v184, v55, v184\n" \
"v_mul_f32_e32 v185, v55, v185\n" \
"v_cvt_pk_bf16_f32 v128, v128, v129\n" \
"v_mul_f32_e32 v130, v54, v130\n" \
"v_mul_f32_e32 v131, v54, v131\n" \
"v_cvt_pk_bf16_f32 v136, v136, v137\n" \
"v_mul_f32_e32 v138, v54, v138\n" \
"v_mul_f32_e32 v139, v54, v139\n" \
"v_cvt_pk_bf16_f32 v144, v144, v145\n" \
"v_mul_f32_e32 v146, v54, v146\n" \
"v_mul_f32_e32 v147, v54, v147\n" \
"v_cvt_pk_bf16_f32 v152, v152, v153\n" \
"v_mul_f32_e32 v154, v54, v154\n" \
"v_mul_f32_e32 v155, v54, v155\n" \
"v_cvt_pk_bf16_f32 v160, v160, v161\n" \
"v_mul_f32_e32 v162, v55, v162\n" \
"v_mul_f32_e32 v163, v55, v163\n" \
"v_cvt_pk_bf16_f32 v168, v168, v169\n" \
"v_mul_f32_e32 v170, v55, v170\n" \
"v_mul_f32_e32 v171, v55, v171\n" \
"v_cvt_pk_bf16_f32 v176, v176, v177\n" \
"v_mul_f32_e32 v178, v55, v178\n" \
"v_mul_f32_e32 v179, v55, v179\n" \
"v_cvt_pk_bf16_f32 v184, v184, v185\n" \
"v_mul_f32_e32 v186, v55, v186\n" \
"v_mul_f32_e32 v187, v55, v187\n" \
"v_cvt_pk_bf16_f32 v129, v130, v131\n" \
"v_mul_f32_e32 v132, v54, v132\n" \
"v_mul_f32_e32 v133, v54, v133\n" \
"v_mul_f32_e32 v134, v54, v134\n" \
"v_mul_f32_e32 v135, v54, v135\n" \
"v_cvt_pk_bf16_f32 v137, v138, v139\n" \
"v_mul_f32_e32 v140, v54, v140\n" \
"s_waitcnt vmcnt(12) lgkmcnt(0)\n" \
"s_barrier\n" \
"v_mfma_f32_16x16x32_bf16 v[120:123], a[128:131], a[48:51], v[44:47]\n" \
"s_xor_b32 m0, s65, 0x12000\n" \
"ds_read_b128 a[188:191], v49 offset:7168\n" \
"v_mfma_f32_16x16x32_bf16 v[120:123], a[132:135], a[52:55], v[120:123]\n" \
"buffer_load_dwordx4 v50, s[8:11], s64 offen offset:0 sc1 lds\n" \
"v_mfma_f32_16x16x32_bf16 v[120:123], a[136:139], a[56:59], v[120:123]\n" \
"ds_read_b128 a[184:187], v49 offset:6144\n" \
"ds_read_b128 a[180:183], v49 offset:5120\n" \
"v_mfma_f32_16x16x32_bf16 v[120:123], a[140:143], a[60:63], v[120:123]\n" \
"buffer_load_dwordx4 v50, s[8:11], s64 offen offset:1024 sc1 lds\n" \
"v_mfma_f32_16x16x32_bf16 v[124:127], a[156:159], a[60:63], v[44:47]\n" \
"ds_read_b128 a[176:179], v49 offset:4096\n" \
"ds_read_b128 a[172:175], v49 offset:3072\n" \
"v_mfma_f32_16x16x32_bf16 v[124:127], a[152:155], a[56:59], v[124:127]\n" \
"ds_read_b128 a[168:171], v49 offset:2048\n" \
"ds_read_b128 a[164:167], v49 offset:1024\n" \
"v_mfma_f32_16x16x32_bf16 v[124:127], a[148:151], a[52:55], v[124:127]\n" \
"ds_read_b128 a[160:163], v49 offset:0\n" \
"ds_read_b128 a[0:3], v49 offset:8192\n" \
"v_mfma_f32_16x16x32_bf16 v[124:127], a[144:147], a[48:51], v[124:127]\n" \
"buffer_load_dwordx4 v50, s[8:11], s64 offen offset:2048 sc1 lds\n" \
"v_mfma_f32_16x16x32_bf16 v[32:35], a[220:223], a[64:67], 0\n" \
"ds_read_b128 a[4:7], v49 offset:9216\n" \
"ds_read_b128 a[8:11], v49 offset:10240\n" \
"v_mfma_f32_16x16x32_bf16 v[36:39], a[220:223], a[80:83], 0\n" \
"ds_read_b128 a[12:15], v49 offset:11264\n" \
"ds_read_b128 a[16:19], v49 offset:12288\n" \
"v_mfma_f32_16x16x32_bf16 v[40:43], a[220:223], a[96:99], 0\n" \
"ds_read_b128 a[20:23], v49 offset:13312\n" \
"ds_read_b128 a[24:27], v49 offset:14336\n" \
"v_mfma_f32_16x16x32_bf16 v[44:47], a[220:223], a[112:115], 0\n" \
"buffer_load_dwordx4 v50, s[8:11], s64 offen offset:3072 sc1 lds\n" \
"v_mfma_f32_16x16x32_bf16 v[32:35], a[224:227], a[68:71], v[32:35]\n" \
"s_xor_b32 m0, s65, 0xa000\n" \
"ds_read_b128 a[28:31], v49 offset:15360\n" \
"v_mfma_f32_16x16x32_bf16 v[36:39], a[224:227], a[84:87], v[36:39]\n" \
"v_exp_f32_e32 v120, v120\n" \
"v_mfma_f32_16x16x32_bf16 v[40:43], a[224:227], a[100:103], v[40:43]\n" \
"buffer_load_dwordx4 v51, s[8:11], s64 offen offset:0 sc1 lds\n" \
"v_mfma_f32_16x16x32_bf16 v[44:47], a[224:227], a[116:119], v[44:47]\n" \
"v_exp_f32_e32 v121, v121\n" \
"v_mfma_f32_16x16x32_bf16 v[32:35], a[228:231], a[72:75], v[32:35]\n" \
"v_exp_f32_e32 v122, v122\n" \
"v_mfma_f32_16x16x32_bf16 v[36:39], a[228:231], a[88:91], v[36:39]\n" \
"v_exp_f32_e32 v123, v123\n" \
"v_mfma_f32_16x16x32_bf16 v[40:43], a[228:231], a[104:107], v[40:43]\n" \
"buffer_load_dwordx4 v51, s[8:11], s64 offen offset:1024 sc1 lds\n" \
"v_mfma_f32_16x16x32_bf16 v[44:47], a[228:231], a[120:123], v[44:47]\n" \
"v_cvt_pk_bf16_f32 v120, v120, v121\n" \
"v_cvt_pk_bf16_f32 v121, v122, v123\n" \
"v_mfma_f32_16x16x32_bf16 v[32:35], a[232:235], a[76:79], v[32:35]\n" \
"v_exp_f32_e32 v124, v124\n" \
"v_mfma_f32_16x16x32_bf16 v[36:39], a[232:235], a[92:95], v[36:39]\n" \
"v_exp_f32_e32 v125, v125\n" \
"v_mfma_f32_16x16x32_bf16 v[40:43], a[232:235], a[108:111], v[40:43]\n" \
"buffer_load_dwordx4 v51, s[8:11], s64 offen offset:2048 sc1 lds\n" \
"v_mfma_f32_16x16x32_bf16 v[44:47], a[232:235], a[124:127], v[44:47]\n" \
"v_cvt_pk_bf16_f32 v122, v124, v125\n" \
"s_waitcnt lgkmcnt(3)\n" \
"v_mfma_f32_16x16x32_bf16 v[220:223], a[188:191], v[84:87], v[220:223]\n" \
"v_exp_f32_e32 v126, v126\n" \
"v_mfma_f32_16x16x32_bf16 v[252:255], a[188:191], v[88:91], v[252:255]\n" \
"buffer_load_dwordx4 v51, s[8:11], s64 offen offset:3072 sc1 lds\n" \
"v_mfma_f32_16x16x32_bf16 v[216:219], a[184:187], v[84:87], v[216:219]\n" \
"s_add_u32 m0, s93, 0x1000\n" \
"v_and_b32_e32 v120, v12, v120\n" \
"v_mfma_f32_16x16x32_bf16 v[248:251], a[184:187], v[88:91], v[248:251]\n" \
"v_exp_f32_e32 v127, v127\n" \
"v_mfma_f32_16x16x32_bf16 v[212:215], a[180:183], v[84:87], v[212:215]\n" \
"v_cvt_pk_bf16_f32 v123, v126, v127\n" \
"v_and_b32_e32 v121, v13, v121\n" \
"v_mfma_f32_16x16x32_bf16 v[244:247], a[180:183], v[88:91], v[244:247]\n" \
"buffer_load_dwordx4 v4, s[80:83], s92 offen offset:0 lds\n" \
"v_mfma_f32_16x16x32_bf16 v[208:211], a[176:179], v[84:87], v[208:211]\n" \
"v_and_b32_e32 v122, v14, v122\n" \
"v_and_b32_e32 v123, v15, v123\n" \
"v_mfma_f32_16x16x32_bf16 v[240:243], a[176:179], v[88:91], v[240:243]\n" \
"ds_read_b128 a[128:131], v48 offset:0\n" \
"ds_read_b128 a[144:147], v48 offset:4096\n" \
"v_mfma_f32_16x16x32_bf16 v[204:207], a[172:175], v[84:87], v[204:207]\n" \
"ds_read_b128 a[132:135], v48 offset:1024\n" \
"ds_read_b128 a[148:151], v48 offset:5120\n" \
"v_mfma_f32_16x16x32_bf16 v[236:239], a[172:175], v[88:91], v[236:239]\n" \
"buffer_load_dwordx4 v5, s[80:83], s92 offen offset:1024 lds\n" \
"v_mfma_f32_16x16x32_bf16 v[200:203], a[168:171], v[84:87], v[200:203]\n" \
"ds_read_b128 a[136:139], v48 offset:2048\n" \
"ds_read_b128 a[152:155], v48 offset:6144\n" \
"v_mfma_f32_16x16x32_bf16 v[232:235], a[168:171], v[88:91], v[232:235]\n" \
"ds_read_b128 a[140:143], v48 offset:3072\n" \
"ds_read_b128 a[156:159], v48 offset:7168\n" \
"v_mfma_f32_16x16x32_bf16 v[196:199], a[164:167], v[84:87], v[196:199]\n" \
"buffer_load_dwordx4 v6, s[80:83], s92 offen offset:2048 lds\n" \
"v_mfma_f32_16x16x32_bf16 v[228:231], a[164:167], v[88:91], v[228:231]\n" \
"v_mul_f32_e32 v141, v54, v141\n" \
"v_mul_f32_e32 v142, v54, v142\n" \
"v_mfma_f32_16x16x32_bf16 v[192:195], a[160:163], v[84:87], v[192:195]\n" \
"v_mul_f32_e32 v143, v54, v143\n" \
"v_cvt_pk_bf16_f32 v145, v146, v147\n" \
"v_mfma_f32_16x16x32_bf16 v[224:227], a[160:163], v[88:91], v[224:227]\n" \
"v_mul_f32_e32 v148, v54, v148\n" \
"v_mul_f32_e32 v149, v54, v149\n" \
"v_smfmac_f32_16x16x64_bf16 a[196:199], a[200:203], v[84:91], v21\n" \
"buffer_load_dwordx4 v7, s[80:83], s92 offen offset:3072 lds\n" \
"v_mul_f32_e32 v150, v54, v150\n" \
"v_mul_f32_e32 v151, v54, v151\n" \
"v_cvt_pk_bf16_f32 v153, v154, v155\n" \
"v_mul_f32_e32 v156, v54, v156\n" \
"v_mul_f32_e32 v157, v54, v157\n" \
"v_mfma_f32_16x16x32_bf16 a[196:199], a[212:215], v[120:123], a[196:199]\n" \
"v_mul_f32_e32 v158, v54, v158\n" \
"v_mul_f32_e32 v159, v54, v159\n" \
"v_mfma_f32_16x16x32_bf16 v[224:227], a[0:3], v[120:123], v[224:227]\n" \
"v_cvt_pk_bf16_f32 v161, v162, v163\n" \
"v_mul_f32_e32 v164, v55, v164\n" \
"v_mfma_f32_16x16x32_bf16 v[228:231], a[4:7], v[120:123], v[228:231]\n" \
"v_mul_f32_e32 v165, v55, v165\n" \
"v_mul_f32_e32 v166, v55, v166\n" \
"v_mfma_f32_16x16x32_bf16 v[232:235], a[8:11], v[120:123], v[232:235]\n" \
"v_mul_f32_e32 v167, v55, v167\n" \
"v_cvt_pk_bf16_f32 v169, v170, v171\n" \
"v_mfma_f32_16x16x32_bf16 v[236:239], a[12:15], v[120:123], v[236:239]\n" \
"v_mul_f32_e32 v172, v55, v172\n" \
"v_mul_f32_e32 v173, v55, v173\n" \
"v_mfma_f32_16x16x32_bf16 v[240:243], a[16:19], v[120:123], v[240:243]\n" \
"v_mul_f32_e32 v174, v55, v174\n" \
"s_waitcnt lgkmcnt(4)\n" \
"v_mfma_f32_16x16x32_bf16 v[244:247], a[20:23], v[120:123], v[244:247]\n" \
"v_mul_f32_e32 v175, v55, v175\n" \
"v_cvt_pk_bf16_f32 v177, v178, v179\n" \
"v_mfma_f32_16x16x32_bf16 v[248:251], a[24:27], v[120:123], v[248:251]\n" \
"v_mul_f32_e32 v180, v55, v180\n" \
"v_mul_f32_e32 v181, v55, v181\n" \
"v_mfma_f32_16x16x32_bf16 v[252:255], a[28:31], v[120:123], v[252:255]\n" \
"v_mul_f32_e32 v182, v55, v182\n" \
"v_mul_f32_e32 v183, v55, v183\n" \
"v_cvt_pk_bf16_f32 v185, v186, v187\n" \
"v_mul_f32_e32 v188, v55, v188\n" \
"v_mul_f32_e32 v189, v55, v189\n" \
"v_mul_f32_e32 v190, v55, v190\n" \
"v_mul_f32_e32 v191, v55, v191\n" \
"v_cvt_pk_bf16_f32 v130, v132, v133\n" \
"v_cvt_pk_bf16_f32 v131, v134, v135\n" \
"buffer_store_dwordx4 v[128:131], v20, s[56:59], s68 offen offset:0 sc1\n" \
"v_cvt_pk_bf16_f32 v138, v140, v141\n" \
"v_cvt_pk_bf16_f32 v139, v142, v143\n" \
"buffer_store_dwordx4 v[136:139], v20, s[56:59], s68 offen offset:1024 sc1\n" \
"v_cvt_pk_bf16_f32 v146, v148, v149\n" \
"v_cvt_pk_bf16_f32 v147, v150, v151\n" \
"buffer_store_dwordx4 v[144:147], v20, s[56:59], s68 offen offset:2048 sc1\n" \
"v_cvt_pk_bf16_f32 v154, v156, v157\n" \
"v_cvt_pk_bf16_f32 v155, v158, v159\n" \
"buffer_store_dwordx4 v[152:155], v20, s[56:59], s68 offen offset:3072 sc1\n" \
"v_cvt_pk_bf16_f32 v162, v164, v165\n" \
"v_cvt_pk_bf16_f32 v163, v166, v167\n" \
"buffer_store_dwordx4 v[160:163], v20, s[56:59], s69 offen offset:0 sc1\n" \
"v_cvt_pk_bf16_f32 v170, v172, v173\n" \
"v_cvt_pk_bf16_f32 v171, v174, v175\n" \
"buffer_store_dwordx4 v[168:171], v20, s[56:59], s69 offen offset:1024 sc1\n" \
"v_cvt_pk_bf16_f32 v178, v180, v181\n" \
"v_cvt_pk_bf16_f32 v179, v182, v183\n" \
"buffer_store_dwordx4 v[176:179], v20, s[56:59], s69 offen offset:2048 sc1\n" \
"v_cvt_pk_bf16_f32 v186, v188, v189\n" \
"v_cvt_pk_bf16_f32 v187, v190, v191\n" \
"buffer_store_dwordx4 v[184:187], v20, s[56:59], s69 offen offset:3072 sc1\n" \
"s_add_u32 m0, s93, 0x2000\n" \
"v_mfma_f32_16x16x32_bf16 a[208:211], a[216:219], v[128:131], a[208:211]\n" \
"buffer_load_dwordx4 v8, s[80:83], s92 offen offset:0 lds\n" \
"v_mfma_f32_16x16x32_bf16 a[208:211], a[216:219], v[136:139], a[208:211]\n" \
"v_mfma_f32_32x32x16_bf16 v[128:143], a[216:219], a[216:219], 0\n" \
"v_mfma_f32_16x16x32_bf16 a[208:211], a[216:219], v[144:147], a[208:211]\n" \
"v_mfma_f32_16x16x32_bf16 a[208:211], a[216:219], v[152:155], a[208:211]\n" \
"v_mfma_f32_32x32x16_bf16 v[144:159], a[216:219], a[216:219], 0\n" \
"v_accvgpr_read_b32 v56, a196\n" \
"v_accvgpr_read_b32 v24, a197\n" \
"v_add_f32_e32 v56, v56, v24\n" \
"v_mfma_f32_16x16x32_bf16 a[208:211], a[216:219], v[160:163], a[208:211]\n" \
"v_max_f32_e32 v62, v62, v56\n" \
"v_accvgpr_read_b32 v57, a198\n" \
"s_waitcnt vmcnt(21) lgkmcnt(0)\n" \
"s_barrier\n" \
"v_mfma_f32_16x16x32_bf16 v[64:67], a[128:131], a[64:67], v[32:35]\n" \
"s_xor_b32 m0, s65, 0x18000\n" \
"v_accvgpr_read_b32 v25, a199\n" \
"v_mfma_f32_16x16x32_bf16 a[196:199], a[216:219], a[216:219], 0\n" \
"buffer_load_dwordx4 v52, s[8:11], s64 offen offset:0 sc1 lds\n" \
"v_mfma_f32_16x16x32_bf16 v[64:67], a[132:135], a[68:71], v[64:67]\n" \
"v_rcp_f32_e32 v56, v56\n" \
"v_mfma_f32_16x16x32_bf16 v[64:67], a[136:139], a[72:75], v[64:67]\n" \
"v_mfma_f32_16x16x32_bf16 a[208:211], a[216:219], v[168:171], a[208:211]\n" \
"v_mfma_f32_32x32x16_bf16 v[160:175], a[216:219], a[216:219], 0\n" \
"v_mul_f32_e32 v56, v56, v60\n" \
"v_mfma_f32_16x16x32_bf16 a[208:211], a[216:219], v[176:179], a[208:211]\n" \
"v_mul_f32_e32 v192, v56, v192\n" \
"v_mul_f32_e32 v193, v56, v193\n" \
"v_mfma_f32_16x16x32_bf16 v[64:67], a[140:143], a[76:79], v[64:67]\n" \
"buffer_load_dwordx4 v52, s[8:11], s64 offen offset:1024 sc1 lds\n" \
"v_mfma_f32_16x16x32_bf16 v[68:71], a[156:159], a[76:79], v[32:35]\n" \
"v_cvt_pk_bf16_f32 v192, v192, v193\n" \
"v_mul_f32_e32 v194, v56, v194\n" \
"v_mfma_f32_16x16x32_bf16 v[68:71], a[152:155], a[72:75], v[68:71]\n" \
"v_mul_f32_e32 v195, v56, v195\n" \
"v_cvt_pk_bf16_f32 v193, v194, v195\n" \
"v_mfma_f32_16x16x32_bf16 v[68:71], a[148:151], a[68:71], v[68:71]\n" \
"v_mfma_f32_16x16x32_bf16 a[208:211], a[216:219], v[184:187], a[208:211]\n" \
"v_mfma_f32_32x32x16_bf16 v[176:191], a[216:219], a[216:219], 0\n" \
"v_mul_f32_e32 v196, v56, v196\n" \
"v_mul_f32_e32 v197, v56, v197\n" \
"v_mul_f32_e32 v198, v56, v198\n" \
"v_mul_f32_e32 v199, v56, v199\n" \
"v_mul_f32_e32 v200, v56, v200\n" \
"v_mul_f32_e32 v201, v56, v201\n" \
"v_mfma_f32_16x16x32_bf16 v[68:71], a[144:147], a[64:67], v[68:71]\n" \
"buffer_load_dwordx4 v52, s[8:11], s64 offen offset:2048 sc1 lds\n" \
"v_mfma_f32_16x16x32_bf16 v[76:79], a[144:147], a[80:83], v[36:39]\n" \
"v_cvt_pk_bf16_f32 v194, v196, v197\n" \
"v_cvt_pk_bf16_f32 v195, v198, v199\n" \
"buffer_store_dwordx4 v[192:195], v20, s[56:59], s70 offen offset:0 sc1\n" \
"v_mfma_f32_16x16x32_bf16 v[76:79], a[148:151], a[84:87], v[76:79]\n" \
"v_cvt_pk_bf16_f32 v200, v200, v201\n" \
"v_mul_f32_e32 v202, v56, v202\n" \
"v_mfma_f32_16x16x32_bf16 v[76:79], a[152:155], a[88:91], v[76:79]\n" \
"v_mul_f32_e32 v203, v56, v203\n" \
"v_add_f32_e32 v57, v57, v25\n" \
"v_mfma_f32_16x16x32_bf16 v[76:79], a[156:159], a[92:95], v[76:79]\n" \
"buffer_load_dwordx4 v52, s[8:11], s64 offen offset:3072 sc1 lds\n" \
"v_mfma_f32_16x16x32_bf16 v[72:75], a[140:143], a[92:95], v[36:39]\n" \
"s_mov_b32 m0, s65\n" \
"v_cvt_pk_bf16_f32 v201, v202, v203\n" \
"v_mfma_f32_16x16x32_bf16 v[72:75], a[136:139], a[88:91], v[72:75]\n" \
"v_mfma_f32_16x16x32_bf16 a[208:211], a[216:219], v[192:195], a[208:211]\n" \
"v_mul_f32_e32 v204, v56, v204\n" \
"v_mul_f32_e32 v205, v56, v205\n" \
"v_mfma_f32_16x16x32_bf16 v[72:75], a[132:135], a[84:87], v[72:75]\n" \
"v_mul_f32_e32 v206, v56, v206\n" \
"v_mul_f32_e32 v207, v56, v207\n" \
"v_mfma_f32_16x16x32_bf16 v[72:75], a[128:131], a[80:83], v[72:75]\n" \
"buffer_load_dwordx4 v53, s[8:11], s64 offen offset:0 sc1 lds\n" \
"v_mfma_f32_16x16x32_bf16 v[80:83], a[128:131], a[96:99], v[40:43]\n" \
"v_mul_f32_e32 v208, v56, v208\n" \
"v_mul_f32_e32 v209, v56, v209\n" \
"v_mfma_f32_16x16x32_bf16 v[80:83], a[132:135], a[100:103], v[80:83]\n" \
"v_max_f32_e32 v62, v62, v57\n" \
"v_cvt_pk_bf16_f32 v202, v204, v205\n" \
"v_mfma_f32_16x16x32_bf16 v[80:83], a[136:139], a[104:107], v[80:83]\n" \
"buffer_load_dwordx4 v53, s[8:11], s64 offen offset:1024 sc1 lds\n" \
"v_mfma_f32_16x16x32_bf16 v[80:83], a[140:143], a[108:111], v[80:83]\n" \
"v_cvt_pk_bf16_f32 v203, v206, v207\n" \
"v_cvt_pk_bf16_f32 v208, v208, v209\n" \
"buffer_store_dwordx4 v[200:203], v20, s[56:59], s70 offen offset:1024 sc1\n" \
"v_mfma_f32_16x16x32_bf16 v[84:87], a[156:159], a[108:111], v[40:43]\n" \
"v_mul_f32_e32 v210, v56, v210\n" \
"v_mul_f32_e32 v211, v56, v211\n" \
"v_mfma_f32_16x16x32_bf16 v[84:87], a[152:155], a[104:107], v[84:87]\n" \
"v_rcp_f32_e32 v57, v57\n" \
"v_mfma_f32_16x16x32_bf16 v[84:87], a[148:151], a[100:103], v[84:87]\n" \
"buffer_load_dwordx4 v53, s[8:11], s64 offen offset:2048 sc1 lds\n" \
"v_mfma_f32_16x16x32_bf16 v[84:87], a[144:147], a[96:99], v[84:87]\n" \
"v_mfma_f32_16x16x32_bf16 a[208:211], a[216:219], v[200:203], a[208:211]\n" \
"v_mfma_f32_32x32x16_bf16 v[192:207], a[216:219], a[216:219], 0\n" \
"v_cvt_pk_bf16_f32 v209, v210, v211\n" \
"v_mul_f32_e32 v212, v56, v212\n" \
"v_mul_f32_e32 v213, v56, v213\n" \
"v_mul_f32_e32 v214, v56, v214\n" \
"v_mul_f32_e32 v215, v56, v215\n" \
"v_mul_f32_e32 v216, v56, v216\n" \
"v_mfma_f32_16x16x32_bf16 v[92:95], a[144:147], a[112:115], v[44:47]\n" \
"ds_read_b128 a[144:147], v48 offset:12288\n" \
"v_mul_f32_e32 v217, v56, v217\n" \
"v_mfma_f32_16x16x32_bf16 v[92:95], a[148:151], a[116:119], v[92:95]\n" \
"ds_read_b128 a[148:151], v48 offset:13312\n" \
"v_cvt_pk_bf16_f32 v210, v212, v213\n" \
"v_mfma_f32_16x16x32_bf16 v[92:95], a[152:155], a[120:123], v[92:95]\n" \
"ds_read_b128 a[152:155], v48 offset:14336\n" \
"v_cvt_pk_bf16_f32 v211, v214, v215\n" \
"buffer_store_dwordx4 v[208:211], v20, s[56:59], s70 offen offset:2048 sc1\n" \
"v_mfma_f32_16x16x32_bf16 v[92:95], a[156:159], a[124:127], v[92:95]\n" \
"ds_read_b128 a[156:159], v48 offset:15360\n" \
"v_cvt_pk_bf16_f32 v216, v216, v217\n" \
"v_mfma_f32_16x16x32_bf16 v[88:91], a[140:143], a[124:127], v[44:47]\n" \
"ds_read_b128 a[140:143], v48 offset:11264\n" \
"v_mul_f32_e32 v218, v56, v218\n" \
"v_mfma_f32_16x16x32_bf16 v[88:91], a[136:139], a[120:123], v[88:91]\n" \
"ds_read_b128 a[136:139], v48 offset:10240\n" \
"v_mul_f32_e32 v219, v56, v219\n" \
"v_mfma_f32_16x16x32_bf16 v[88:91], a[132:135], a[116:119], v[88:91]\n" \
"ds_read_b128 a[132:135], v48 offset:9216\n" \
"v_mul_f32_e32 v57, v57, v61\n" \
"v_mfma_f32_16x16x32_bf16 v[88:91], a[128:131], a[112:115], v[88:91]\n" \
"ds_read_b128 a[128:131], v48 offset:8192\n" \
"v_mfma_f32_16x16x32_bf16 a[208:211], a[216:219], v[208:211], a[208:211]\n" \
"v_cvt_pk_bf16_f32 v217, v218, v219\n" \
"v_mul_f32_e32 v220, v56, v220\n" \
"v_mul_f32_e32 v221, v56, v221\n" \
"v_mul_f32_e32 v222, v56, v222\n" \
"v_mul_f32_e32 v223, v56, v223\n" \
"v_mul_f32_e32 v224, v57, v224\n" \
"v_mul_f32_e32 v225, v57, v225\n" \
"v_cvt_pk_bf16_f32 v218, v220, v221\n" \
"v_cvt_pk_bf16_f32 v219, v222, v223\n" \
"buffer_store_dwordx4 v[216:219], v20, s[56:59], s70 offen offset:3072 sc1\n" \
"v_cvt_pk_bf16_f32 v224, v224, v225\n" \
"v_mul_f32_e32 v226, v57, v226\n" \
"v_mul_f32_e32 v227, v57, v227\n" \
"v_mfma_f32_16x16x32_bf16 a[208:211], a[216:219], v[216:219], a[208:211]\n" \
"v_mfma_f32_32x32x16_bf16 v[208:223], a[216:219], a[216:219], 0\n" \
"v_cvt_pk_bf16_f32 v225, v226, v227\n" \
"v_mul_f32_e32 v228, v57, v228\n" \
"v_mul_f32_e32 v229, v57, v229\n" \
"v_mul_f32_e32 v230, v57, v230\n" \
"v_mul_f32_e32 v231, v57, v231\n" \
"v_mul_f32_e32 v232, v57, v232\n" \
"v_mul_f32_e32 v233, v57, v233\n" \
"v_cvt_pk_bf16_f32 v226, v228, v229\n" \
"v_cvt_pk_bf16_f32 v227, v230, v231\n" \
"buffer_store_dwordx4 v[224:227], v20, s[56:59], s71 offen offset:0 sc1\n" \
"v_cvt_pk_bf16_f32 v232, v232, v233\n" \
"v_mul_f32_e32 v234, v57, v234\n" \
"v_mul_f32_e32 v235, v57, v235\n" \
"v_mfma_f32_16x16x32_bf16 a[208:211], a[216:219], v[224:227], a[208:211]\n" \
"v_cvt_pk_bf16_f32 v233, v234, v235\n" \
"v_mul_f32_e32 v236, v57, v236\n" \
"v_mul_f32_e32 v237, v57, v237\n" \
"v_mul_f32_e32 v238, v57, v238\n" \
"v_mul_f32_e32 v239, v57, v239\n" \
"v_mul_f32_e32 v240, v57, v240\n" \
"v_mul_f32_e32 v241, v57, v241\n" \
"v_cvt_pk_bf16_f32 v234, v236, v237\n" \
"v_cvt_pk_bf16_f32 v235, v238, v239\n" \
"buffer_store_dwordx4 v[232:235], v20, s[56:59], s71 offen offset:1024 sc1\n" \
"v_cvt_pk_bf16_f32 v240, v240, v241\n" \
"v_mul_f32_e32 v242, v57, v242\n" \
"v_mul_f32_e32 v243, v57, v243\n" \
"v_mfma_f32_16x16x32_bf16 a[208:211], a[216:219], v[232:235], a[208:211]\n" \
"v_mfma_f32_32x32x16_bf16 v[224:239], a[216:219], a[216:219], 0\n" \
"v_cvt_pk_bf16_f32 v241, v242, v243\n" \
"v_mul_f32_e32 v244, v57, v244\n" \
"v_mul_f32_e32 v245, v57, v245\n" \
"v_mul_f32_e32 v246, v57, v246\n" \
"v_mul_f32_e32 v247, v57, v247\n" \
"v_mul_f32_e32 v248, v57, v248\n" \
"v_mul_f32_e32 v249, v57, v249\n" \
"v_cvt_pk_bf16_f32 v242, v244, v245\n" \
"v_cvt_pk_bf16_f32 v243, v246, v247\n" \
"buffer_store_dwordx4 v[240:243], v20, s[56:59], s71 offen offset:2048 sc1\n" \
"v_cvt_pk_bf16_f32 v248, v248, v249\n" \
"v_mul_f32_e32 v250, v57, v250\n" \
"v_mul_f32_e32 v251, v57, v251\n" \
"v_mfma_f32_16x16x32_bf16 a[208:211], a[216:219], v[240:243], a[208:211]\n" \
"v_cvt_pk_bf16_f32 v249, v250, v251\n" \
"v_mul_f32_e32 v252, v57, v252\n" \
"v_mul_f32_e32 v253, v57, v253\n" \
"v_mul_f32_e32 v254, v57, v254\n" \
"v_mul_f32_e32 v255, v57, v255\n" \
"v_cvt_pk_bf16_f32 v250, v252, v253\n" \
"v_cvt_pk_bf16_f32 v251, v254, v255\n" \
"buffer_store_dwordx4 v[248:251], v20, s[56:59], s71 offen offset:3072 sc1\n" \
"buffer_load_dwordx4 v53, s[8:11], s64 offen offset:3072 sc1 lds\n" \
"s_waitcnt vmcnt(16) lgkmcnt(0)\n" \
"s_barrier\n" \
"v_mfma_f32_16x16x32_bf16 a[208:211], a[216:219], v[248:251], a[208:211]\n" \
"v_mfma_f32_32x32x16_bf16 v[240:255], a[216:219], a[216:219], 0\n" \
"v_exp_f32_e32 v70, v70\n" \
"v_exp_f32_e32 v71, v71\n" \
"v_exp_f32_e32 v72, v72\n" \
"v_exp_f32_e32 v73, v73\n" \
"s_add_u32 s64, s64, 0x10000\n" \
"v_cmp_lt_f32_e64 s[72:73], s21, v62\n" \
"v_readlane_b32 s95, v11, 3\n" \
"v_accvgpr_read_b32 v63, a208\n" \
"v_cmp_u_f32_e32 vcc, v63, v63\n" \
"v_exp_f32_e32 v68, v68\n" \
"v_exp_f32_e32 v69, v69\n" \
"v_exp_f32_e32 v74, v74\n" \
"v_exp_f32_e32 v75, v75\n" \
"s_or_b64 vcc, s[72:73], vcc\n" \
"s_cselect_b32 s95, 0x40000000, s95\n" \
"s_cselect_b32 s94, 0x7f800000, 0\n" \
"v_cvt_pk_bf16_f32 v71, v70, v71\n" \
"v_exp_f32_e32 v66, v66\n" \
"v_exp_f32_e32 v67, v67\n" \
"v_exp_f32_e32 v64, v64\n" \
"v_exp_f32_e32 v65, v65\n" \
"v_cvt_pk_bf16_f32 v72, v72, v73\n" \
"v_exp_f32_e32 v76, v76\n" \
"v_exp_f32_e32 v77, v77\n" \
"v_exp_f32_e32 v78, v78\n" \
"v_exp_f32_e32 v79, v79\n" \
"v_cvt_pk_bf16_f32 v70, v68, v69\n" \
"v_cvt_pk_bf16_f32 v73, v74, v75\n" \
"v_add_u32_e32 v49, 0x10000, v48\n" \
"s_xor_b32 s65, s65, 0x8000\n" \
"v_cvt_pk_bf16_f32 v69, v66, v67\n" \
"v_cvt_pk_bf16_f32 v68, v64, v65\n" \
"v_cvt_pk_bf16_f32 v74, v76, v77\n" \
"v_cvt_pk_bf16_f32 v75, v78, v79\n" \
"v_xor_b32_e32 v48, 0x8000, v48\n" \
"s_sub_u32 s66, s19, 1\n" \
"s_cbranch_scc0 L_2_%=\n" \
"L_f1_%=:\n" \
"v_bfe_u32 v60, v20, 2, 7\n" \
"v_readlane_b32 s96, v11, 1\n" \
"ds_read_b128 a[160:163], v49 offset:0\n" \
"ds_read_b128 a[164:167], v49 offset:1024\n" \
"ds_read_b128 a[168:171], v49 offset:2048\n" \
"v_mfma_f32_16x16x32_bf16 v[104:107], a[128:131], a[80:83], v[36:39]\n" \
"v_cvt_pk_bf16_f32 v192, v192, v193\n" \
"v_cvt_pk_bf16_f32 v224, v224, v225\n" \
"buffer_load_dword v57, v60, s[56:59], s96 offen sc1\n" \
"v_readlane_b32 s93, v11, 20\n" \
"v_cvt_pk_bf16_f32 v193, v194, v195\n" \
"v_cvt_pk_bf16_f32 v225, v226, v227\n" \
"ds_read_b128 a[172:175], v49 offset:3072\n" \
"v_readlane_b32 s84, v11, 15\n" \
"v_readlane_b32 s85, v11, 16\n" \
"v_readlane_b32 s86, v11, 17\n" \
"s_mov_b32 s87, 0x20000\n" \
"v_readlane_b32 s92, v11, 18\n" \
"s_add_u32 m0, s93, 0x0\n" \
"v_cvt_pk_bf16_f32 v194, v196, v197\n" \
"v_cvt_pk_bf16_f32 v195, v198, v199\n" \
"v_cvt_pk_bf16_f32 v226, v228, v229\n" \
"v_cvt_pk_bf16_f32 v227, v230, v231\n" \
"v_mfma_f32_16x16x32_bf16 v[104:107], a[132:135], a[84:87], v[104:107]\n" \
"v_lshl_add_u32 v50, s15, 2, v20\n" \
"v_cvt_pk_bf16_f32 v196, v200, v201\n" \
"v_cvt_pk_bf16_f32 v197, v202, v203\n" \
"v_cvt_pk_bf16_f32 v198, v204, v205\n" \
"v_cvt_pk_bf16_f32 v199, v206, v207\n" \
"buffer_load_dwordx4 v0, s[84:87], s92 offen offset:0 lds\n" \
"v_cvt_pk_bf16_f32 v228, v232, v233\n" \
"v_cvt_pk_bf16_f32 v229, v234, v235\n" \
"v_cvt_pk_bf16_f32 v230, v236, v237\n" \
"v_cvt_pk_bf16_f32 v231, v238, v239\n" \
"v_add_u32_e32 v50, 0x1a000, v50\n" \
"v_mov_b32_e32 v61, s62\n" \
"v_cvt_pk_bf16_f32 v200, v208, v209\n" \
"v_cvt_pk_bf16_f32 v201, v210, v211\n" \
"v_cvt_pk_bf16_f32 v202, v212, v213\n" \
"v_cvt_pk_bf16_f32 v203, v214, v215\n" \
"v_cvt_pk_bf16_f32 v204, v216, v217\n" \
"v_cvt_pk_bf16_f32 v205, v218, v219\n" \
"v_cvt_pk_bf16_f32 v206, v220, v221\n" \
"v_cvt_pk_bf16_f32 v207, v222, v223\n" \
"v_cvt_pk_bf16_f32 v232, v240, v241\n" \
"v_cvt_pk_bf16_f32 v233, v242, v243\n" \
"v_cvt_pk_bf16_f32 v234, v244, v245\n" \
"v_cvt_pk_bf16_f32 v235, v246, v247\n" \
"v_cvt_pk_bf16_f32 v236, v248, v249\n" \
"v_cvt_pk_bf16_f32 v237, v250, v251\n" \
"v_cvt_pk_bf16_f32 v238, v252, v253\n" \
"buffer_load_dwordx4 v1, s[84:87], s92 offen offset:1024 lds\n" \
"v_cvt_pk_bf16_f32 v239, v254, v255\n" \
"ds_write_b128 v50, v[192:195] offset:0\n" \
"v_mfma_f32_16x16x32_bf16 v[104:107], a[136:139], a[88:91], v[104:107]\n" \
"ds_read_b128 a[176:179], v49 offset:4096\n" \
"v_add_u32_e32 v56, 0x49000, v20\n" \
"v_mfma_f32_16x16x32_bf16 v[104:107], a[140:143], a[92:95], v[104:107]\n" \
"ds_write_b128 v50, v[196:199] offset:1024\n" \
"v_mfma_f32_16x16x32_bf16 v[108:111], a[156:159], a[92:95], v[36:39]\n" \
"v_readlane_b32 s97, v11, 35\n" \
"v_readlane_b32 s98, v11, 36\n" \
"ds_read_b128 a[180:183], v49 offset:5120\n" \
"v_mfma_f32_16x16x32_bf16 v[108:111], a[152:155], a[88:91], v[108:111]\n" \
"ds_write_b128 v50, v[200:203] offset:2048\n" \
"v_mfma_f32_16x16x32_bf16 v[108:111], a[148:151], a[84:87], v[108:111]\n" \
"buffer_load_dwordx4 v2, s[84:87], s92 offen offset:2048 lds\n" \
"ds_read_b128 a[184:187], v49 offset:6144\n" \
"v_mfma_f32_16x16x32_bf16 v[108:111], a[144:147], a[80:83], v[108:111]\n" \
"ds_write_b128 v50, v[204:207] offset:3072\n" \
"v_and_b32_e32 v68, v12, v68\n" \
"v_and_b32_e32 v69, v13, v69\n" \
"v_and_b32_e32 v70, v14, v70\n" \
"v_and_b32_e32 v71, v15, v71\n" \
"ds_read_b128 a[188:191], v49 offset:7168\n" \
"ds_read_b128 a[0:3], v20 offset:8192\n" \
"ds_write_b128 v50, v[224:227] offset:4096\n" \
"s_waitcnt lgkmcnt(10)\n" \
"v_mfma_f32_16x16x32_bf16 v[128:131], a[160:163], v[68:71], v[128:131]\n" \
"ds_read_b128 a[4:7], v20 offset:9216\n" \
"ds_read_b128 a[8:11], v20 offset:10240\n" \
"v_mfma_f32_16x16x32_bf16 v[160:163], a[160:163], v[72:75], v[160:163]\n" \
"ds_write_b128 v50, v[228:231] offset:5120\n" \
"buffer_load_dwordx4 v3, s[84:87], s92 offen offset:3072 lds\n" \
"s_add_u32 m0, s93, 0x1000\n" \
"v_mfma_f32_16x16x32_bf16 v[132:135], a[164:167], v[68:71], v[132:135]\n" \
"ds_read_b128 a[12:15], v20 offset:11264\n" \
"ds_read_b128 a[28:31], v20 offset:15360\n" \
"ds_write_b128 v50, v[232:235] offset:6144\n" \
"v_mfma_f32_16x16x32_bf16 v[164:167], a[164:167], v[72:75], v[164:167]\n" \
"ds_read_b128 a[24:27], v20 offset:14336\n" \
"ds_read_b128 a[20:23], v20 offset:13312\n" \
"v_mfma_f32_16x16x32_bf16 v[136:139], a[168:171], v[68:71], v[136:139]\n" \
"ds_write_b128 v50, v[236:239] offset:7168\n" \
"v_mfma_f32_16x16x32_bf16 v[168:171], a[168:171], v[72:75], v[168:171]\n" \
"ds_read_b128 a[16:19], v20 offset:12288\n" \
"v_xor_b32_e32 v48, 0x4000, v49\n" \
"v_mfma_f32_16x16x32_bf16 v[140:143], a[172:175], v[68:71], v[140:143]\n" \
"buffer_load_dwordx4 v4, s[84:87], s92 offen offset:0 lds\n" \
"v_mfma_f32_16x16x32_bf16 v[172:175], a[172:175], v[72:75], v[172:175]\n" \
"ds_read_b32 v58, v18 offset:256\n" \
"s_waitcnt lgkmcnt(15)\n" \
"v_mfma_f32_16x16x32_bf16 v[144:147], a[176:179], v[68:71], v[144:147]\n" \
"ds_read_b32 v59, v18 offset:384\n" \
"ds_read_b128 a[32:35], v48 offset:8192\n" \
"v_mfma_f32_16x16x32_bf16 v[176:179], a[176:179], v[72:75], v[176:179]\n" \
"ds_read_b128 a[36:39], v48 offset:9216\n" \
"ds_read_b128 a[40:43], v48 offset:10240\n" \
"v_mfma_f32_16x16x32_bf16 v[148:151], a[180:183], v[68:71], v[148:151]\n" \
"ds_read_b128 a[44:47], v48 offset:11264\n" \
"ds_read_b128 a[48:51], v48 offset:12288\n" \
"v_mfma_f32_16x16x32_bf16 v[180:183], a[180:183], v[72:75], v[180:183]\n" \
"ds_read_b128 a[52:55], v48 offset:13312\n" \
"ds_read_b128 a[56:59], v48 offset:14336\n" \
"v_mfma_f32_16x16x32_bf16 v[152:155], a[184:187], v[68:71], v[152:155]\n" \
"buffer_load_dwordx4 v5, s[84:87], s92 offen offset:1024 lds\n" \
"v_mfma_f32_16x16x32_bf16 v[184:187], a[184:187], v[72:75], v[184:187]\n" \
"ds_read_b128 a[60:63], v48 offset:15360\n" \
"s_waitcnt lgkmcnt(15)\n" \
"v_mfma_f32_16x16x32_bf16 v[156:159], a[188:191], v[68:71], v[156:159]\n" \
"ds_read_b128 a[128:131], v49 offset:8192\n" \
"ds_read_b128 a[132:135], v49 offset:9216\n" \
"v_mfma_f32_16x16x32_bf16 v[188:191], a[188:191], v[72:75], v[188:191]\n" \
"ds_read_b128 a[136:139], v49 offset:10240\n" \
"ds_read_b128 a[140:143], v49 offset:11264\n" \
"v_smfmac_f32_16x16x64_bf16 a[192:195], a[200:203], v[68:75], v21\n" \
"ds_read_b128 a[144:147], v49 offset:12288\n" \
"ds_read_b128 a[148:151], v49 offset:13312\n" \
"v_mfma_f32_16x16x32_bf16 v[112:115], a[0:3], a[64:67], v[32:35]\n" \
"ds_read_b128 a[152:155], v49 offset:14336\n" \
"ds_read_b128 a[156:159], v49 offset:15360\n" \
"v_mfma_f32_16x16x32_bf16 v[112:115], a[4:7], a[68:71], v[112:115]\n" \
"buffer_load_dwordx4 v6, s[84:87], s92 offen offset:2048 lds\n" \
"v_mfma_f32_16x16x32_bf16 v[112:115], a[8:11], a[72:75], v[112:115]\n" \
"v_accvgpr_read_b32 v40, a196\n" \
"v_accvgpr_read_b32 v24, a197\n" \
"v_mfma_f32_16x16x32_bf16 v[112:115], a[12:15], a[76:79], v[112:115]\n" \
"v_accvgpr_read_b32 v41, a198\n" \
"v_accvgpr_read_b32 v25, a199\n" \
"v_mfma_f32_16x16x32_bf16 v[116:119], a[28:31], a[76:79], v[32:35]\n" \
"s_waitcnt vmcnt(7)\n" \
"v_cmp_ne_u32_e32 vcc, s62, v57\n" \
"s_mov_b64 exec, 1\n" \
"buffer_store_dword v61, off, s[56:59], s95 offset:0 sc1\n" \
"s_mov_b64 exec, -1\n" \
"s_waitcnt lgkmcnt(15)\n" \
"v_mfma_f32_16x16x32_bf16 v[116:119], a[24:27], a[72:75], v[116:119]\n" \
"s_cbranch_vccnz L_so1_%=\n" \
"L_ro1_%=:\n" \
"v_add_f32_e32 v40, v40, v24\n" \
"v_mfma_f32_16x16x32_bf16 v[116:119], a[20:23], a[68:71], v[116:119]\n" \
"v_add_f32_e32 v41, v41, v25\n" \
"v_add_f32_e32 v40, s94, v40\n" \
"v_mfma_f32_16x16x32_bf16 v[116:119], a[16:19], a[64:67], v[116:119]\n" \
"v_add_f32_e32 v41, s94, v41\n" \
"ds_write_b64 v19, v[40:41]\n" \
"v_mfma_f32_16x16x32_bf16 v[124:127], a[16:19], a[80:83], v[36:39]\n" \
"v_add_u32_e32 v52, s15, v20\n" \
"v_add_u32_e32 v51, 0x10000, v52\n" \
"v_mfma_f32_16x16x32_bf16 v[124:127], a[20:23], a[84:87], v[124:127]\n" \
"v_exp_f32_e32 v104, v104\n" \
"buffer_load_dwordx4 v7, s[84:87], s92 offen offset:3072 lds\n" \
"s_add_u32 m0, s93, 0x2000\n" \
"v_mfma_f32_16x16x32_bf16 v[124:127], a[24:27], a[88:91], v[124:127]\n" \
"v_exp_f32_e32 v105, v105\n" \
"v_mfma_f32_16x16x32_bf16 v[124:127], a[28:31], a[92:95], v[124:127]\n" \
"v_exp_f32_e32 v106, v106\n" \
"v_mfma_f32_16x16x32_bf16 v[120:123], a[12:15], a[92:95], v[36:39]\n" \
"v_exp_f32_e32 v107, v107\n" \
"v_mfma_f32_16x16x32_bf16 v[120:123], a[8:11], a[88:91], v[120:123]\n" \
"v_cvt_pk_bf16_f32 v104, v104, v105\n" \
"v_cvt_pk_bf16_f32 v105, v106, v107\n" \
"v_mfma_f32_16x16x32_bf16 v[120:123], a[4:7], a[84:87], v[120:123]\n" \
"v_exp_f32_e32 v108, v108\n" \
"buffer_load_dwordx4 v8, s[84:87], s92 offen offset:0 lds\n" \
"v_mfma_f32_16x16x32_bf16 v[120:123], a[0:3], a[80:83], v[120:123]\n" \
"v_exp_f32_e32 v109, v109\n" \
"v_exp_f32_e32 v110, v110\n" \
"v_exp_f32_e32 v111, v111\n" \
"s_waitcnt lgkmcnt(0)\n" \
"s_barrier\n" \
"ds_read_b128 v[208:211], v52 offset:0\n" \
"ds_read_b128 v[212:215], v52 offset:1024\n" \
"ds_read_b128 v[216:219], v52 offset:2048\n" \
"ds_read_b128 v[220:223], v52 offset:3072\n" \
"ds_read_b128 v[240:243], v51 offset:32768\n" \
"ds_read_b128 v[244:247], v51 offset:33792\n" \
"ds_read_b128 v[248:251], v51 offset:34816\n" \
"ds_read_b128 v[252:255], v51 offset:35840\n" \
"buffer_load_dwordx4 a[220:223], v20, s[56:59], s60 offen offset:0 sc1\n" \
"v_cvt_pk_bf16_f32 v106, v108, v109\n" \
"v_cvt_pk_bf16_f32 v107, v110, v111\n" \
"v_and_b32_e32 v104, v12, v104\n" \
"v_and_b32_e32 v105, v13, v105\n" \
"v_and_b32_e32 v106, v14, v106\n" \
"v_and_b32_e32 v107, v15, v107\n" \
"v_xor_b32_e32 v63, 0x400, v19\n" \
"ds_read_b64 v[42:43], v63\n" \
"v_mfma_f32_16x16x32_bf16 a[192:195], a[212:215], v[104:107], a[192:195]\n" \
"v_mov_b32_e32 v194, v28\n" \
"v_mov_b32_e32 v195, v29\n" \
"v_mfma_f32_16x16x32_bf16 v[160:163], a[128:131], v[104:107], v[160:163]\n" \
"v_mov_b32_e32 v196, 0\n" \
"v_mov_b32_e32 v197, 0\n" \
"v_mfma_f32_16x16x32_bf16 v[164:167], a[132:135], v[104:107], v[164:167]\n" \
"v_mov_b32_e32 v192, 0\n" \
"v_mov_b32_e32 v193, 0\n" \
"v_mfma_f32_16x16x32_bf16 v[168:171], a[136:139], v[104:107], v[168:171]\n" \
"buffer_load_dwordx4 a[236:239], v56, s[56:59], s60 offen offset:0 sc1\n" \
"v_mfma_f32_16x16x32_bf16 v[172:175], a[140:143], v[104:107], v[172:175]\n" \
"v_exp_f32_e32 v118, v118\n" \
"v_mfma_f32_16x16x32_bf16 v[176:179], a[144:147], v[104:107], v[176:179]\n" \
"v_exp_f32_e32 v119, v119\n" \
"v_mfma_f32_16x16x32_bf16 v[180:183], a[148:151], v[104:107], v[180:183]\n" \
"v_exp_f32_e32 v120, v120\n" \
"v_mfma_f32_16x16x32_bf16 v[184:187], a[152:155], v[104:107], v[184:187]\n" \
"v_exp_f32_e32 v121, v121\n" \
"v_mfma_f32_16x16x32_bf16 v[188:191], a[156:159], v[104:107], v[188:191]\n" \
"v_exp_f32_e32 v116, v116\n" \
"s_waitcnt lgkmcnt(1)\n" \
"s_barrier\n" \
"buffer_load_dwordx4 a[224:227], v20, s[56:59], s60 offen offset:1024 sc1\n" \
"v_mfma_f32_16x16x32_bf16 v[128:131], v[194:197], v[208:211], v[128:131]\n" \
"v_exp_f32_e32 v117, v117\n" \
"v_mfma_f32_16x16x32_bf16 v[132:135], v[192:195], v[208:211], v[132:135]\n" \
"v_exp_f32_e32 v122, v122\n" \
"v_mfma_f32_16x16x32_bf16 v[136:139], v[194:197], v[212:215], v[136:139]\n" \
"v_exp_f32_e32 v123, v123\n" \
"v_mfma_f32_16x16x32_bf16 v[140:143], v[192:195], v[212:215], v[140:143]\n" \
"v_cvt_pk_bf16_f32 v119, v118, v119\n" \
"v_cvt_pk_bf16_f32 v120, v120, v121\n" \
"v_mfma_f32_16x16x32_bf16 v[144:147], v[194:197], v[216:219], v[144:147]\n" \
"v_exp_f32_e32 v114, v114\n" \
"v_mfma_f32_16x16x32_bf16 v[148:151], v[192:195], v[216:219], v[148:151]\n" \
"buffer_load_dwordx4 a[240:243], v56, s[56:59], s60 offen offset:1024 sc1\n" \
"v_mfma_f32_16x16x32_bf16 v[152:155], v[194:197], v[220:223], v[152:155]\n" \
"v_exp_f32_e32 v115, v115\n" \
"v_mfma_f32_16x16x32_bf16 v[156:159], v[192:195], v[220:223], v[156:159]\n" \
"v_exp_f32_e32 v112, v112\n" \
"v_mfma_f32_16x16x32_bf16 v[160:163], v[194:197], v[240:243], v[160:163]\n" \
"v_exp_f32_e32 v113, v113\n" \
"v_mfma_f32_16x16x32_bf16 v[164:167], v[192:195], v[240:243], v[164:167]\n" \
"v_exp_f32_e32 v124, v124\n" \
"v_mfma_f32_16x16x32_bf16 v[168:171], v[194:197], v[244:247], v[168:171]\n" \
"v_exp_f32_e32 v125, v125\n" \
"v_mfma_f32_16x16x32_bf16 v[172:175], v[192:195], v[244:247], v[172:175]\n" \
"buffer_load_dwordx4 a[228:231], v20, s[56:59], s60 offen offset:2048 sc1\n" \
"v_mfma_f32_16x16x32_bf16 v[176:179], v[194:197], v[248:251], v[176:179]\n" \
"v_exp_f32_e32 v126, v126\n" \
"v_mfma_f32_16x16x32_bf16 v[180:183], v[192:195], v[248:251], v[180:183]\n" \
"v_exp_f32_e32 v127, v127\n" \
"v_mfma_f32_16x16x32_bf16 v[184:187], v[194:197], v[252:255], v[184:187]\n" \
"v_cvt_pk_bf16_f32 v118, v116, v117\n" \
"v_cvt_pk_bf16_f32 v121, v122, v123\n" \
"v_mfma_f32_16x16x32_bf16 v[188:191], v[192:195], v[252:255], v[188:191]\n" \
"v_cvt_pk_bf16_f32 v117, v114, v115\n" \
"v_cvt_pk_bf16_f32 v116, v112, v113\n" \
"v_cvt_pk_bf16_f32 v122, v124, v125\n" \
"v_cvt_pk_bf16_f32 v123, v126, v127\n" \
"s_nop 1\n" \
"v_smfmac_f32_16x16x64_bf16 a[192:195], a[200:203], v[116:123], v21\n" \
"buffer_load_dwordx4 a[244:247], v56, s[56:59], s60 offen offset:2048 sc1\n" \
"v_mfma_f32_16x16x32_bf16 v[128:131], a[32:35], v[116:119], v[128:131]\n" \
"v_mfma_f32_16x16x32_bf16 v[160:163], a[32:35], v[120:123], v[160:163]\n" \
"v_mfma_f32_16x16x32_bf16 v[132:135], a[36:39], v[116:119], v[132:135]\n" \
"v_mfma_f32_16x16x32_bf16 v[164:167], a[36:39], v[120:123], v[164:167]\n" \
"v_mfma_f32_16x16x32_bf16 v[136:139], a[40:43], v[116:119], v[136:139]\n" \
"v_mfma_f32_16x16x32_bf16 v[168:171], a[40:43], v[120:123], v[168:171]\n" \
"buffer_load_dwordx4 a[232:235], v20, s[56:59], s60 offen offset:3072 sc1\n" \
"v_mfma_f32_16x16x32_bf16 v[140:143], a[44:47], v[116:119], v[140:143]\n" \
"v_accvgpr_read_b32 v54, a192\n" \
"v_accvgpr_read_b32 v22, a193\n" \
"v_mfma_f32_16x16x32_bf16 v[172:175], a[44:47], v[120:123], v[172:175]\n" \
"v_add_f32_e32 v54, v54, v22\n" \
"s_waitcnt lgkmcnt(0)\n" \
"v_add_f32_e32 v54, v54, v42\n" \
"v_mfma_f32_16x16x32_bf16 v[144:147], a[48:51], v[116:119], v[144:147]\n" \
"v_max_f32_e32 v62, v62, v54\n" \
"v_accvgpr_read_b32 v55, a194\n" \
"v_mfma_f32_16x16x32_bf16 v[176:179], a[48:51], v[120:123], v[176:179]\n" \
"v_rcp_f32_e32 v54, v54\n" \
"v_mfma_f32_16x16x32_bf16 v[148:151], a[52:55], v[116:119], v[148:151]\n" \
"v_mul_f32_e32 v54, v54, v58\n" \
"v_mul_f32_e32 v128, v54, v128\n" \
"buffer_load_dwordx4 a[248:251], v56, s[56:59], s60 offen offset:3072 sc1\n" \
"v_mfma_f32_16x16x32_bf16 v[180:183], a[52:55], v[120:123], v[180:183]\n" \
"v_mul_f32_e32 v129, v54, v129\n" \
"v_mul_f32_e32 v130, v54, v130\n" \
"v_mul_f32_e32 v131, v54, v131\n" \
"v_mul_f32_e32 v132, v54, v132\n" \
"v_mul_f32_e32 v133, v54, v133\n" \
"v_mul_f32_e32 v134, v54, v134\n" \
"v_mul_f32_e32 v135, v54, v135\n" \
"v_cvt_pk_bf16_f32 v64, v128, v129\n" \
"v_cvt_pk_bf16_f32 v65, v130, v131\n" \
"v_cvt_pk_bf16_f32 v66, v132, v133\n" \
"v_cvt_pk_bf16_f32 v67, v134, v135\n" \
"v_accvgpr_read_b32 v23, a195\n" \
"v_mfma_f32_16x16x32_bf16 v[152:155], a[56:59], v[116:119], v[152:155]\n" \
"v_mfma_f32_16x16x32_bf16 a[208:211], a[216:219], v[64:67], a[208:211]\n" \
"v_mul_f32_e32 v136, v54, v136\n" \
"v_mul_f32_e32 v137, v54, v137\n" \
"buffer_load_dwordx4 a[96:99], v20, s[56:59], s97 offen offset:0 sc1\n" \
"v_mul_f32_e32 v138, v54, v138\n" \
"v_mul_f32_e32 v139, v54, v139\n" \
"v_mul_f32_e32 v140, v54, v140\n" \
"v_mul_f32_e32 v141, v54, v141\n" \
"v_mul_f32_e32 v142, v54, v142\n" \
"v_mul_f32_e32 v143, v54, v143\n" \
"v_add_f32_e32 v55, v55, v23\n" \
"v_cvt_pk_bf16_f32 v68, v136, v137\n" \
"v_cvt_pk_bf16_f32 v69, v138, v139\n" \
"v_cvt_pk_bf16_f32 v70, v140, v141\n" \
"v_cvt_pk_bf16_f32 v71, v142, v143\n" \
"v_add_f32_e32 v55, v55, v43\n" \
"v_mul_f32_e32 v144, v54, v144\n" \
"v_mfma_f32_16x16x32_bf16 a[208:211], a[216:219], v[68:71], a[208:211]\n" \
"v_mul_f32_e32 v145, v54, v145\n" \
"v_mul_f32_e32 v146, v54, v146\n" \
"v_mul_f32_e32 v147, v54, v147\n" \
"v_mul_f32_e32 v148, v54, v148\n" \
"v_mul_f32_e32 v149, v54, v149\n" \
"v_mul_f32_e32 v150, v54, v150\n" \
"buffer_load_dwordx4 a[104:107], v56, s[56:59], s97 offen offset:0 sc1\n" \
"v_mul_f32_e32 v151, v54, v151\n" \
"v_max_f32_e32 v62, v62, v55\n" \
"v_cvt_pk_bf16_f32 v72, v144, v145\n" \
"v_cvt_pk_bf16_f32 v73, v146, v147\n" \
"v_cvt_pk_bf16_f32 v74, v148, v149\n" \
"v_cvt_pk_bf16_f32 v75, v150, v151\n" \
"v_rcp_f32_e32 v55, v55\n" \
"v_mfma_f32_16x16x32_bf16 v[184:187], a[56:59], v[120:123], v[184:187]\n" \
"v_mfma_f32_16x16x32_bf16 a[208:211], a[216:219], v[72:75], a[208:211]\n" \
"v_mul_f32_e32 v152, v54, v152\n" \
"v_mul_f32_e32 v153, v54, v153\n" \
"v_mul_f32_e32 v154, v54, v154\n" \
"v_mul_f32_e32 v155, v54, v155\n" \
"v_cvt_pk_bf16_f32 v76, v152, v153\n" \
"v_cvt_pk_bf16_f32 v77, v154, v155\n" \
"v_mul_f32_e32 v55, v55, v59\n" \
"buffer_load_dwordx4 a[112:115], v20, s[56:59], s98 offen offset:0 sc1\n" \
"v_mul_f32_e32 v160, v55, v160\n" \
"v_mul_f32_e32 v161, v55, v161\n" \
"v_mul_f32_e32 v162, v55, v162\n" \
"v_mul_f32_e32 v163, v55, v163\n" \
"v_mul_f32_e32 v164, v55, v164\n" \
"v_mul_f32_e32 v165, v55, v165\n" \
"v_mul_f32_e32 v166, v55, v166\n" \
"v_mul_f32_e32 v167, v55, v167\n" \
"v_cvt_pk_bf16_f32 v80, v160, v161\n" \
"v_cvt_pk_bf16_f32 v81, v162, v163\n" \
"v_cvt_pk_bf16_f32 v82, v164, v165\n" \
"v_cvt_pk_bf16_f32 v83, v166, v167\n" \
"v_mfma_f32_16x16x32_bf16 v[156:159], a[60:63], v[116:119], v[156:159]\n" \
"v_mul_f32_e32 v168, v55, v168\n" \
"v_mul_f32_e32 v169, v55, v169\n" \
"v_mul_f32_e32 v170, v55, v170\n" \
"v_mul_f32_e32 v171, v55, v171\n" \
"v_mul_f32_e32 v172, v55, v172\n" \
"v_mul_f32_e32 v173, v55, v173\n" \
"v_mul_f32_e32 v174, v55, v174\n" \
"buffer_load_dwordx4 a[120:123], v56, s[56:59], s98 offen offset:0 sc1\n" \
"v_mul_f32_e32 v156, v54, v156\n" \
"v_mul_f32_e32 v157, v54, v157\n" \
"v_mul_f32_e32 v158, v54, v158\n" \
"v_mul_f32_e32 v159, v54, v159\n" \
"v_cvt_pk_bf16_f32 v78, v156, v157\n" \
"v_cvt_pk_bf16_f32 v79, v158, v159\n" \
"v_mul_f32_e32 v175, v55, v175\n" \
"v_cvt_pk_bf16_f32 v84, v168, v169\n" \
"v_mfma_f32_16x16x32_bf16 a[208:211], a[216:219], v[76:79], a[208:211]\n" \
"v_mfma_f32_16x16x32_bf16 a[208:211], a[216:219], v[80:83], a[208:211]\n" \
"v_cvt_pk_bf16_f32 v85, v170, v171\n" \
"v_cvt_pk_bf16_f32 v86, v172, v173\n" \
"v_cvt_pk_bf16_f32 v87, v174, v175\n" \
"v_mul_f32_e32 v176, v55, v176\n" \
"v_mul_f32_e32 v177, v55, v177\n" \
"v_mfma_f32_16x16x32_bf16 a[208:211], a[216:219], v[84:87], a[208:211]\n" \
"buffer_load_dwordx4 a[100:103], v20, s[56:59], s97 offen offset:1024 sc1\n" \
"v_mul_f32_e32 v178, v55, v178\n" \
"v_mul_f32_e32 v179, v55, v179\n" \
"v_mul_f32_e32 v180, v55, v180\n" \
"v_mul_f32_e32 v181, v55, v181\n" \
"v_mul_f32_e32 v182, v55, v182\n" \
"v_mul_f32_e32 v183, v55, v183\n" \
"v_cvt_pk_bf16_f32 v96, v176, v177\n" \
"v_cvt_pk_bf16_f32 v97, v178, v179\n" \
"v_cvt_pk_bf16_f32 v98, v180, v181\n" \
"v_cvt_pk_bf16_f32 v99, v182, v183\n" \
"v_mfma_f32_16x16x32_bf16 v[188:191], a[60:63], v[120:123], v[188:191]\n" \
"v_mul_f32_e32 v184, v55, v184\n" \
"v_mfma_f32_16x16x32_bf16 a[208:211], a[216:219], v[96:99], a[208:211]\n" \
"v_mul_f32_e32 v185, v55, v185\n" \
"v_mul_f32_e32 v186, v55, v186\n" \
"v_mul_f32_e32 v187, v55, v187\n" \
"v_cvt_pk_bf16_f32 v100, v184, v185\n" \
"v_cvt_pk_bf16_f32 v101, v186, v187\n" \
"buffer_load_dwordx4 a[108:111], v56, s[56:59], s97 offen offset:1024 sc1\n" \
"v_mul_f32_e32 v188, v55, v188\n" \
"v_mul_f32_e32 v189, v55, v189\n" \
"v_mul_f32_e32 v190, v55, v190\n" \
"v_mul_f32_e32 v191, v55, v191\n" \
"v_cvt_pk_bf16_f32 v102, v188, v189\n" \
"v_cvt_pk_bf16_f32 v103, v190, v191\n" \
"v_cmp_lt_f32_e64 s[72:73], s21, v62\n" \
"buffer_load_dwordx4 a[116:119], v20, s[56:59], s98 offen offset:1024 sc1\n" \
"v_mfma_f32_16x16x32_bf16 a[208:211], a[216:219], v[100:103], a[208:211]\n" \
"buffer_load_dwordx4 a[124:127], v56, s[56:59], s98 offen offset:1024 sc1\n" \
"s_nop 6\n" \
"v_accvgpr_read_b32 v63, a208\n" \
"v_cmp_u_f32_e32 vcc, v63, v63\n" \
"s_nop 0\n" \
"s_or_b64 vcc, s[72:73], vcc\n" \
"s_cbranch_scc1 L_end_%=\n" \
"L_out_%=:\n" \
"v_bfe_u32 v38, v20, 2, 4\n" \
"v_readlane_b32 s64, v11, 2\n" \
"v_add_u32_e32 v32, 0x49000, v20\n" \
"v_add_u32_e32 v34, 0x20000, v10\n" \
"ds_read_b64_tr_b16 a[0:1], v34 offset:0\n" \
"ds_read_b64_tr_b16 a[2:3], v34 offset:4608\n" \
"ds_read_b64_tr_b16 a[4:5], v34 offset:32\n" \
"buffer_load_dword v26, v38, s[56:59], s64 offen sc1\n" \
"ds_read_b64_tr_b16 a[6:7], v34 offset:4640\n" \
"ds_read_b64_tr_b16 a[8:9], v34 offset:64\n" \
"ds_read_b64_tr_b16 a[10:11], v34 offset:4672\n" \
"ds_read_b64_tr_b16 a[12:13], v34 offset:96\n" \
"buffer_store_dwordx4 v[64:67], v20, s[56:59], s61 offen offset:0 sc1\n" \
"ds_read_b64_tr_b16 a[14:15], v34 offset:4704\n" \
"v_mov_b32_e32 v39, s62\n" \
"v_readlane_b32 s72, v11, 5\n" \
"v_readlane_b32 s73, v11, 4\n" \
"buffer_store_dwordx4 v[68:71], v20, s[56:59], s61 offen offset:1024 sc1\n" \
"ds_read_b64_tr_b16 a[16:17], v34 offset:128\n" \
"ds_read_b64_tr_b16 a[18:19], v34 offset:4736\n" \
"ds_read_b64_tr_b16 a[20:21], v34 offset:160\n" \
"ds_read_b64_tr_b16 a[22:23], v34 offset:4768\n" \
"buffer_store_dwordx4 v[72:75], v20, s[56:59], s61 offen offset:2048 sc1\n" \
"ds_read_b64_tr_b16 a[24:25], v34 offset:192\n" \
"ds_read_b64_tr_b16 a[26:27], v34 offset:4800\n" \
"ds_read_b64_tr_b16 a[28:29], v34 offset:224\n" \
"ds_read_b64_tr_b16 a[30:31], v34 offset:4832\n" \
"buffer_store_dwordx4 v[76:79], v20, s[56:59], s61 offen offset:3072 sc1\n" \
"ds_read_b64_tr_b16 a[192:193], v34 offset:256\n" \
"ds_read_b64_tr_b16 a[194:195], v34 offset:4864\n" \
"ds_read_b64_tr_b16 a[196:197], v34 offset:9216\n" \
"ds_read_b64_tr_b16 a[198:199], v34 offset:13824\n" \
"buffer_store_dwordx4 v[80:83], v32, s[56:59], s61 offen offset:0 sc1\n" \
"ds_read_b64_tr_b16 a[200:201], v34 offset:9248\n" \
"ds_read_b64_tr_b16 a[202:203], v34 offset:13856\n" \
"ds_read_b64_tr_b16 a[204:205], v34 offset:9280\n" \
"ds_read_b64_tr_b16 a[206:207], v34 offset:13888\n" \
"buffer_store_dwordx4 v[84:87], v32, s[56:59], s61 offen offset:1024 sc1\n" \
"ds_read_b64_tr_b16 a[208:209], v34 offset:9312\n" \
"s_waitcnt vmcnt(15) lgkmcnt(15)\n" \
"v_mfma_f32_16x16x32_bf16 v[112:115], a[0:3], a[220:223], 0\n" \
"buffer_store_dwordx4 v[96:99], v32, s[56:59], s61 offen offset:2048 sc1\n" \
"v_mfma_f32_16x16x32_bf16 v[116:119], a[0:3], a[236:239], 0\n" \
"ds_read_b64_tr_b16 a[210:211], v34 offset:13920\n" \
"ds_read_b64_tr_b16 a[212:213], v34 offset:9344\n" \
"buffer_store_dwordx4 v[100:103], v32, s[56:59], s61 offen offset:3072 sc1\n" \
"v_mfma_f32_16x16x32_bf16 v[184:187], a[0:3], v[64:67], 0\n" \
"ds_read_b64_tr_b16 a[214:215], v34 offset:13952\n" \
"ds_read_b64_tr_b16 a[216:217], v34 offset:9376\n" \
"v_mfma_f32_16x16x32_bf16 v[188:191], a[0:3], v[80:83], 0\n" \
"ds_read_b64_tr_b16 a[218:219], v34 offset:13984\n" \
"ds_read_b64_tr_b16 a[0:1], v34 offset:9408\n" \
"v_mfma_f32_16x16x32_bf16 v[120:123], a[4:7], a[220:223], 0\n" \
"ds_read_b64_tr_b16 a[2:3], v34 offset:14016\n" \
"v_readlane_b32 s66, v11, 21\n" \
"v_mfma_f32_16x16x32_bf16 v[124:127], a[4:7], a[236:239], 0\n" \
"v_readlane_b32 s80, v11, 11\n" \
"v_readlane_b32 s81, v11, 12\n" \
"v_mfma_f32_16x16x32_bf16 v[192:195], a[4:7], v[64:67], 0\n" \
"s_mov_b32 s82, 0x47fca0\n" \
"s_mov_b32 s83, 0x20000\n" \
"v_mfma_f32_16x16x32_bf16 v[196:199], a[4:7], v[80:83], 0\n" \
"ds_read_b64_tr_b16 a[4:5], v34 offset:9440\n" \
"ds_read_b64_tr_b16 a[6:7], v34 offset:14048\n" \
"v_mfma_f32_16x16x32_bf16 v[128:131], a[8:11], a[220:223], 0\n" \
"v_readlane_b32 s65, v11, 26\n" \
"s_add_u32 m0, s66, 0x0\n" \
"v_mfma_f32_16x16x32_bf16 v[132:135], a[8:11], a[236:239], 0\n" \
"v_readlane_b32 s67, v11, 29\n" \
"v_lshrrev_b32_e32 v44, 9, v20\n" \
"v_mfma_f32_16x16x32_bf16 v[200:203], a[8:11], v[64:67], 0\n" \
"buffer_load_dwordx4 v0, s[80:83], s65 offen offset:0 lds\n" \
"v_mfma_f32_16x16x32_bf16 v[204:207], a[8:11], v[80:83], 0\n" \
"ds_read_b64_tr_b16 a[8:9], v34 offset:9472\n" \
"ds_read_b64_tr_b16 a[10:11], v34 offset:14080\n" \
"v_mfma_f32_16x16x32_bf16 v[136:139], a[12:15], a[220:223], 0\n" \
"v_mul_u32_u24_e32 v44, 0xb40, v44\n" \
"v_add_u32_e32 v35, v44, v10\n" \
"v_mfma_f32_16x16x32_bf16 v[140:143], a[12:15], a[236:239], 0\n" \
"v_readlane_b32 s69, v11, 22\n" \
"v_readlane_b32 s68, v11, 27\n" \
"v_mfma_f32_16x16x32_bf16 v[208:211], a[12:15], v[64:67], 0\n" \
"v_readlane_b32 s94, v11, 23\n" \
"v_readlane_b32 s93, v11, 28\n" \
"v_mfma_f32_16x16x32_bf16 v[212:215], a[12:15], v[80:83], 0\n" \
"ds_read_b64_tr_b16 a[12:13], v34 offset:18432\n" \
"ds_read_b64_tr_b16 a[14:15], v34 offset:23040\n" \
"v_mfma_f32_16x16x32_bf16 v[144:147], a[16:19], a[220:223], 0\n" \
"buffer_load_dwordx4 v1, s[80:83], s65 offen offset:1024 lds\n" \
"v_mfma_f32_16x16x32_bf16 v[148:151], a[16:19], a[236:239], 0\n" \
"v_readlane_b32 s96, v11, 24\n" \
"v_readlane_b32 s84, v11, 9\n" \
"v_mfma_f32_16x16x32_bf16 v[216:219], a[16:19], v[64:67], 0\n" \
"v_readlane_b32 s85, v11, 10\n" \
"s_mov_b32 s86, 0xa1ca0\n" \
"v_mfma_f32_16x16x32_bf16 v[220:223], a[16:19], v[80:83], 0\n" \
"ds_read_b64_tr_b16 a[16:17], v34 offset:18464\n" \
"s_waitcnt lgkmcnt(7)\n" \
"v_mfma_f32_16x16x32_bf16 v[152:155], a[20:23], a[220:223], 0\n" \
"ds_read_b64_tr_b16 a[18:19], v34 offset:23072\n" \
"s_mov_b32 s87, 0x20000\n" \
"v_mfma_f32_16x16x32_bf16 v[156:159], a[20:23], a[236:239], 0\n" \
"buffer_load_dwordx4 v2, s[80:83], s65 offen offset:2048 lds\n" \
"v_mfma_f32_16x16x32_bf16 v[224:227], a[20:23], v[64:67], 0\n" \
"v_readlane_b32 s70, v11, 30\n" \
"v_add_u32_e32 v33, 0x10000, v10\n" \
"v_mfma_f32_16x16x32_bf16 v[228:231], a[20:23], v[80:83], 0\n" \
"ds_read_b64_tr_b16 a[20:21], v34 offset:18496\n" \
"ds_read_b64_tr_b16 a[22:23], v34 offset:23104\n" \
"v_mfma_f32_16x16x32_bf16 v[160:163], a[24:27], a[220:223], 0\n" \
"v_readlane_b32 s71, v11, 31\n" \
"v_readlane_b32 s92, v11, 33\n" \
"v_mfma_f32_16x16x32_bf16 v[164:167], a[24:27], a[236:239], 0\n" \
"v_readlane_b32 s95, v11, 34\n" \
"v_lshlrev_b32_e32 v41, 12, v20\n" \
"v_mfma_f32_16x16x32_bf16 v[232:235], a[24:27], v[64:67], 0\n" \
"buffer_load_dwordx4 v3, s[80:83], s65 offen offset:3072 lds\n" \
"v_mfma_f32_16x16x32_bf16 v[236:239], a[24:27], v[80:83], 0\n" \
"ds_read_b64_tr_b16 a[24:25], v34 offset:18528\n" \
"ds_read_b64_tr_b16 a[26:27], v34 offset:23136\n" \
"v_mfma_f32_16x16x32_bf16 v[168:171], a[28:31], a[220:223], 0\n" \
"s_add_u32 m0, s66, 0x1000\n" \
"v_readlane_b32 s97, v11, 37\n" \
"v_mfma_f32_16x16x32_bf16 v[172:175], a[28:31], a[236:239], 0\n" \
"v_readlane_b32 s63, v11, 40\n" \
"v_bfe_u32 v40, v20, 2, 7\n" \
"v_mfma_f32_16x16x32_bf16 v[240:243], a[28:31], v[64:67], 0\n" \
"s_add_u32 s74, s63, 0x3800\n" \
"v_readlane_b32 s100, v11, 7\n" \
"v_mfma_f32_16x16x32_bf16 v[244:247], a[28:31], v[80:83], 0\n" \
"ds_read_b64_tr_b16 a[28:29], v34 offset:18560\n" \
"ds_read_b64_tr_b16 a[30:31], v34 offset:23168\n" \
"v_mfma_f32_16x16x32_bf16 v[176:179], a[192:195], a[220:223], 0\n" \
"buffer_load_dwordx4 v4, s[80:83], s65 offen offset:0 lds\n" \
"v_mfma_f32_16x16x32_bf16 v[180:183], a[192:195], a[236:239], 0\n" \
"v_bfe_u32 v44, v20, 4, 4\n" \
"v_bfe_u32 v45, v20, 8, 2\n" \
"v_mfma_f32_16x16x32_bf16 v[248:251], a[192:195], v[64:67], 0\n" \
"v_mul_u32_u24_e32 v42, 0x480, v44\n" \
"v_lshl_add_u32 v42, v45, 3, v42\n" \
"v_mfma_f32_16x16x32_bf16 v[252:255], a[192:195], v[80:83], 0\n" \
"ds_read_b64_tr_b16 a[192:193], v34 offset:18592\n" \
"ds_read_b64_tr_b16 a[194:195], v34 offset:23200\n" \
"v_mfma_f32_16x16x32_bf16 v[112:115], a[196:199], a[224:227], v[112:115]\n" \
"v_lshrrev_b32_e32 v44, 1, v44\n" \
"v_lshlrev_b32_e32 v45, 1, v45\n" \
"v_mfma_f32_16x16x32_bf16 v[116:119], a[196:199], a[240:243], v[116:119]\n" \
"buffer_load_dwordx4 v5, s[80:83], s65 offen offset:1024 lds\n" \
"v_mfma_f32_16x16x32_bf16 v[184:187], a[196:199], v[68:71], v[184:187]\n" \
"v_sub_u32_e32 v44, v44, v45\n" \
"v_and_b32_e32 v45, 16, v20\n" \
"v_mfma_f32_16x16x32_bf16 v[188:191], a[196:199], v[84:87], v[188:191]\n" \
"ds_read_b64_tr_b16 a[196:197], v34 offset:18624\n" \
"ds_read_b64_tr_b16 a[198:199], v34 offset:23232\n" \
"v_mfma_f32_16x16x32_bf16 v[120:123], a[200:203], a[224:227], v[120:123]\n" \
"v_mov_b32_e32 v46, 0x3f80\n" \
"v_lshlrev_b32_e32 v45, v45, v46\n" \
"v_mfma_f32_16x16x32_bf16 v[124:127], a[200:203], a[240:243], v[124:127]\n" \
"v_mov_b32_e32 v52, 0\n" \
"v_mov_b32_e32 v53, 0\n" \
"v_mfma_f32_16x16x32_bf16 v[192:195], a[200:203], v[68:71], v[192:195]\n" \
"buffer_load_dwordx4 v6, s[80:83], s65 offen offset:2048 lds\n" \
"v_mfma_f32_16x16x32_bf16 v[196:199], a[200:203], v[84:87], v[196:199]\n" \
"ds_read_b64_tr_b16 a[200:201], v34 offset:18656\n" \
"ds_read_b64_tr_b16 a[202:203], v34 offset:23264\n" \
"v_mfma_f32_16x16x32_bf16 v[128:131], a[204:207], a[224:227], v[128:131]\n" \
"s_waitcnt vmcnt(15)\n" \
"v_cmp_ne_u32_e32 vcc, s62, v26\n" \
"v_mfma_f32_16x16x32_bf16 v[132:135], a[204:207], a[240:243], v[132:135]\n" \
"v_mov_b32_e32 v48, 0\n" \
"v_mov_b32_e32 v49, 0\n" \
"v_mfma_f32_16x16x32_bf16 v[200:203], a[204:207], v[68:71], v[200:203]\n" \
"v_add_u32_e32 v43, 0x9000, v42\n" \
"s_cbranch_vccnz L_sa_%=\n" \
"L_ra_%=:\n" \
"v_mfma_f32_16x16x32_bf16 v[204:207], a[204:207], v[84:87], v[204:207]\n" \
"ds_read_b64_tr_b16 a[204:205], v34 offset:18688\n" \
"ds_read_b64_tr_b16 a[206:207], v34 offset:23296\n" \
"v_mfma_f32_16x16x32_bf16 v[136:139], a[208:211], a[224:227], v[136:139]\n" \
"buffer_load_dwordx4 v7, s[80:83], s65 offen offset:3072 lds\n" \
"v_mfma_f32_16x16x32_bf16 v[140:143], a[208:211], a[240:243], v[140:143]\n" \
"s_add_u32 m0, s66, 0x2000\n" \
"v_readlane_b32 s66, v11, 32\n" \
"v_mfma_f32_16x16x32_bf16 v[208:211], a[208:211], v[68:71], v[208:211]\n" \
"v_readlane_b32 s64, v11, 38\n" \
"v_readlane_b32 s88, v11, 13\n" \
"v_mfma_f32_16x16x32_bf16 v[212:215], a[208:211], v[84:87], v[212:215]\n" \
"v_readlane_b32 s89, v11, 14\n" \
"s_mov_b32 s90, 0x240000\n" \
"v_mfma_f32_16x16x32_bf16 v[144:147], a[212:215], a[224:227], v[144:147]\n" \
"s_mov_b32 s91, 0x20000\n" \
"v_mov_b32_e32 v46, s62\n" \
"v_mfma_f32_16x16x32_bf16 v[148:151], a[212:215], a[240:243], v[148:151]\n" \
"buffer_load_dwordx4 v8, s[80:83], s65 offen offset:0 lds\n" \
"v_mfma_f32_16x16x32_bf16 v[216:219], a[212:215], v[68:71], v[216:219]\n" \
"s_add_u32 m0, s69, 0x0\n" \
"v_readlane_b32 s65, v11, 39\n" \
"v_mfma_f32_16x16x32_bf16 v[220:223], a[212:215], v[84:87], v[220:223]\n" \
"v_mfma_f32_16x16x32_bf16 v[152:155], a[216:219], a[224:227], v[152:155]\n" \
"v_mfma_f32_16x16x32_bf16 v[156:159], a[216:219], a[240:243], v[156:159]\n" \
"v_mfma_f32_16x16x32_bf16 v[224:227], a[216:219], v[68:71], v[224:227]\n" \
"buffer_load_dwordx4 a[64:67], v20, s[56:59], s67 offen offset:0 sc1\n" \
"v_mfma_f32_16x16x32_bf16 v[228:231], a[216:219], v[84:87], v[228:231]\n" \
"v_mfma_f32_16x16x32_bf16 v[160:163], a[0:3], a[224:227], v[160:163]\n" \
"v_mfma_f32_16x16x32_bf16 v[164:167], a[0:3], a[240:243], v[164:167]\n" \
"v_mfma_f32_16x16x32_bf16 v[232:235], a[0:3], v[68:71], v[232:235]\n" \
"v_mfma_f32_16x16x32_bf16 v[236:239], a[0:3], v[84:87], v[236:239]\n" \
"s_waitcnt lgkmcnt(0)\n" \
"v_mfma_f32_16x16x32_bf16 v[168:171], a[4:7], a[224:227], v[168:171]\n" \
"buffer_load_dwordx4 a[80:83], v32, s[56:59], s67 offen offset:0 sc1\n" \
"v_mfma_f32_16x16x32_bf16 v[172:175], a[4:7], a[240:243], v[172:175]\n" \
"v_mfma_f32_16x16x32_bf16 v[240:243], a[4:7], v[68:71], v[240:243]\n" \
"v_mfma_f32_16x16x32_bf16 v[244:247], a[4:7], v[84:87], v[244:247]\n" \
"v_mfma_f32_16x16x32_bf16 v[176:179], a[8:11], a[224:227], v[176:179]\n" \
"v_mfma_f32_16x16x32_bf16 v[180:183], a[8:11], a[240:243], v[180:183]\n" \
"buffer_load_dwordx4 a[68:71], v20, s[56:59], s67 offen offset:1024 sc1\n" \
"v_mfma_f32_16x16x32_bf16 v[248:251], a[8:11], v[68:71], v[248:251]\n" \
"v_mfma_f32_16x16x32_bf16 v[252:255], a[8:11], v[84:87], v[252:255]\n" \
"v_mfma_f32_16x16x32_bf16 v[112:115], a[12:15], a[228:231], v[112:115]\n" \
"v_mfma_f32_16x16x32_bf16 v[116:119], a[12:15], a[244:247], v[116:119]\n" \
"v_mfma_f32_16x16x32_bf16 v[184:187], a[12:15], v[72:75], v[184:187]\n" \
"buffer_load_dwordx4 a[84:87], v32, s[56:59], s67 offen offset:1024 sc1\n" \
"v_mfma_f32_16x16x32_bf16 v[188:191], a[12:15], v[96:99], v[188:191]\n" \
"v_mfma_f32_16x16x32_bf16 v[120:123], a[16:19], a[228:231], v[120:123]\n" \
"v_mfma_f32_16x16x32_bf16 v[124:127], a[16:19], a[244:247], v[124:127]\n" \
"v_mfma_f32_16x16x32_bf16 v[192:195], a[16:19], v[72:75], v[192:195]\n" \
"v_mfma_f32_16x16x32_bf16 v[196:199], a[16:19], v[96:99], v[196:199]\n" \
"buffer_load_dwordx4 a[72:75], v20, s[56:59], s67 offen offset:2048 sc1\n" \
"v_mfma_f32_16x16x32_bf16 v[128:131], a[20:23], a[228:231], v[128:131]\n" \
"v_mfma_f32_16x16x32_bf16 v[132:135], a[20:23], a[244:247], v[132:135]\n" \
"v_mfma_f32_16x16x32_bf16 v[200:203], a[20:23], v[72:75], v[200:203]\n" \
"v_mfma_f32_16x16x32_bf16 v[204:207], a[20:23], v[96:99], v[204:207]\n" \
"s_barrier\n" \
"v_mfma_f32_16x16x32_bf16 v[136:139], a[24:27], a[228:231], v[136:139]\n" \
"ds_read_b64_tr_b16 a[208:209], v10 offset:52224\n" \
"ds_read_b64_tr_b16 a[210:211], v10 offset:56832\n" \
"v_mfma_f32_16x16x32_bf16 v[140:143], a[24:27], a[244:247], v[140:143]\n" \
"ds_read_b64_tr_b16 a[212:213], v10 offset:52256\n" \
"ds_read_b64_tr_b16 a[214:215], v10 offset:56864\n" \
"v_mfma_f32_16x16x32_bf16 v[208:211], a[24:27], v[72:75], v[208:211]\n" \
"ds_read_b64_tr_b16 a[216:217], v10 offset:52288\n" \
"ds_read_b64_tr_b16 a[218:219], v10 offset:56896\n" \
"v_mfma_f32_16x16x32_bf16 v[212:215], a[24:27], v[96:99], v[212:215]\n" \
"ds_read_b64_tr_b16 a[0:1], v10 offset:52320\n" \
"ds_read_b64_tr_b16 a[2:3], v10 offset:56928\n" \
"v_mfma_f32_16x16x32_bf16 v[144:147], a[28:31], a[228:231], v[144:147]\n" \
"ds_read_b64_tr_b16 a[4:5], v10 offset:52352\n" \
"ds_read_b64_tr_b16 a[6:7], v10 offset:56960\n" \
"v_mfma_f32_16x16x32_bf16 v[148:151], a[28:31], a[244:247], v[148:151]\n" \
"ds_read_b64_tr_b16 a[8:9], v10 offset:52384\n" \
"ds_read_b64_tr_b16 a[10:11], v10 offset:56992\n" \
"v_mfma_f32_16x16x32_bf16 v[216:219], a[28:31], v[72:75], v[216:219]\n" \
"ds_read_b64_tr_b16 a[12:13], v10 offset:52416\n" \
"ds_read_b64_tr_b16 a[14:15], v10 offset:57024\n" \
"v_mfma_f32_16x16x32_bf16 v[220:223], a[28:31], v[96:99], v[220:223]\n" \
"ds_read_b64_tr_b16 a[16:17], v10 offset:52448\n" \
"ds_read_b64_tr_b16 a[18:19], v10 offset:57056\n" \
"v_mfma_f32_16x16x32_bf16 v[152:155], a[192:195], a[228:231], v[152:155]\n" \
"ds_read_b64_tr_b16 a[20:21], v10 offset:52480\n" \
"ds_read_b64_tr_b16 a[22:23], v10 offset:57088\n" \
"v_mfma_f32_16x16x32_bf16 v[156:159], a[192:195], a[244:247], v[156:159]\n" \
"ds_read_b64_tr_b16 a[24:25], v35 offset:24576\n" \
"ds_read_b64_tr_b16 a[26:27], v35 offset:26880\n" \
"v_mfma_f32_16x16x32_bf16 v[224:227], a[192:195], v[72:75], v[224:227]\n" \
"ds_read_b64_tr_b16 a[28:29], v35 offset:24608\n" \
"ds_read_b64_tr_b16 a[30:31], v35 offset:26912\n" \
"v_mfma_f32_16x16x32_bf16 v[228:231], a[192:195], v[96:99], v[228:231]\n" \
"ds_read_b64_tr_b16 a[192:193], v35 offset:24640\n" \
"ds_read_b64_tr_b16 a[194:195], v35 offset:26944\n" \
"v_mfma_f32_16x16x32_bf16 v[160:163], a[196:199], a[228:231], v[160:163]\n" \
"buffer_load_dwordx4 a[88:91], v32, s[56:59], s67 offen offset:2048 sc1\n" \
"v_mfma_f32_16x16x32_bf16 v[164:167], a[196:199], a[244:247], v[164:167]\n" \
"v_mfma_f32_16x16x32_bf16 v[232:235], a[196:199], v[72:75], v[232:235]\n" \
"v_mfma_f32_16x16x32_bf16 v[236:239], a[196:199], v[96:99], v[236:239]\n" \
"ds_read_b64_tr_b16 a[196:197], v35 offset:24672\n" \
"ds_read_b64_tr_b16 a[198:199], v35 offset:26976\n" \
"v_mfma_f32_16x16x32_bf16 v[168:171], a[200:203], a[228:231], v[168:171]\n" \
"v_mfma_f32_16x16x32_bf16 v[172:175], a[200:203], a[244:247], v[172:175]\n" \
"buffer_load_dwordx4 v0, s[80:83], s68 offen offset:0 lds\n" \
"v_mfma_f32_16x16x32_bf16 v[240:243], a[200:203], v[72:75], v[240:243]\n" \
"v_mfma_f32_16x16x32_bf16 v[244:247], a[200:203], v[96:99], v[244:247]\n" \
"ds_read_b64_tr_b16 a[200:201], v35 offset:24704\n" \
"ds_read_b64_tr_b16 a[202:203], v35 offset:27008\n" \
"v_mfma_f32_16x16x32_bf16 v[176:179], a[204:207], a[228:231], v[176:179]\n" \
"v_mfma_f32_16x16x32_bf16 v[180:183], a[204:207], a[244:247], v[180:183]\n" \
"v_mfma_f32_16x16x32_bf16 v[248:251], a[204:207], v[72:75], v[248:251]\n" \
"buffer_load_dwordx4 v1, s[80:83], s68 offen offset:1024 lds\n" \
"v_mfma_f32_16x16x32_bf16 v[252:255], a[204:207], v[96:99], v[252:255]\n" \
"ds_read_b64_tr_b16 a[204:205], v35 offset:24736\n" \
"s_waitcnt lgkmcnt(9)\n" \
"v_mfma_f32_16x16x32_bf16 v[112:115], a[208:211], a[232:235], v[112:115]\n" \
"ds_read_b64_tr_b16 a[206:207], v35 offset:27040\n" \
"v_mfma_f32_16x16x32_bf16 v[116:119], a[208:211], a[248:251], v[116:119]\n" \
"v_mfma_f32_16x16x32_bf16 v[184:187], a[208:211], v[76:79], v[184:187]\n" \
"v_mfma_f32_16x16x32_bf16 v[188:191], a[208:211], v[100:103], v[188:191]\n" \
"ds_read_b64_tr_b16 a[208:209], v35 offset:24768\n" \
"ds_read_b64_tr_b16 a[210:211], v35 offset:27072\n" \
"v_mfma_f32_16x16x32_bf16 v[120:123], a[212:215], a[232:235], v[120:123]\n" \
"buffer_load_dwordx4 v2, s[80:83], s68 offen offset:2048 lds\n" \
"v_mfma_f32_16x16x32_bf16 v[124:127], a[212:215], a[248:251], v[124:127]\n" \
"v_mfma_f32_16x16x32_bf16 v[192:195], a[212:215], v[76:79], v[192:195]\n" \
"s_waitcnt vmcnt(18)\n" \
"s_mov_b64 exec, 1\n" \
"buffer_store_dword v39, off, s[56:59], s72 offset:0 sc1\n" \
"s_mov_b64 exec, -1\n" \
"v_mfma_f32_16x16x32_bf16 v[196:199], a[212:215], v[100:103], v[196:199]\n" \
"ds_read_b64_tr_b16 a[212:213], v35 offset:24800\n" \
"ds_read_b64_tr_b16 a[214:215], v35 offset:27104\n" \
"v_mfma_f32_16x16x32_bf16 v[128:131], a[216:219], a[232:235], v[128:131]\n" \
"v_readlane_b32 s72, v11, 50\n" \
"v_mfma_f32_16x16x32_bf16 v[132:135], a[216:219], a[248:251], v[132:135]\n" \
"v_mfma_f32_16x16x32_bf16 v[200:203], a[216:219], v[76:79], v[200:203]\n" \
"buffer_load_dwordx4 v3, s[80:83], s68 offen offset:3072 lds\n" \
"v_mfma_f32_16x16x32_bf16 v[204:207], a[216:219], v[100:103], v[204:207]\n" \
"ds_read_b64_tr_b16 a[216:217], v35 offset:24832\n" \
"ds_read_b64_tr_b16 a[218:219], v35 offset:27136\n" \
"v_mfma_f32_16x16x32_bf16 v[136:139], a[0:3], a[232:235], v[136:139]\n" \
"s_add_u32 m0, s69, 0x1000\n" \
"v_mfma_f32_16x16x32_bf16 v[140:143], a[0:3], a[248:251], v[140:143]\n" \
"v_mfma_f32_16x16x32_bf16 v[208:211], a[0:3], v[76:79], v[208:211]\n" \
"v_mfma_f32_16x16x32_bf16 v[212:215], a[0:3], v[100:103], v[212:215]\n" \
"ds_read_b64_tr_b16 a[0:1], v35 offset:34944\n" \
"ds_read_b64_tr_b16 a[2:3], v35 offset:37248\n" \
"v_mfma_f32_16x16x32_bf16 v[144:147], a[4:7], a[232:235], v[144:147]\n" \
"buffer_load_dwordx4 v4, s[80:83], s68 offen offset:0 lds\n" \
"v_mfma_f32_16x16x32_bf16 v[148:151], a[4:7], a[248:251], v[148:151]\n" \
"v_mfma_f32_16x16x32_bf16 v[216:219], a[4:7], v[76:79], v[216:219]\n" \
"v_mfma_f32_16x16x32_bf16 v[220:223], a[4:7], v[100:103], v[220:223]\n" \
"ds_read_b64_tr_b16 a[4:5], v35 offset:34976\n" \
"ds_read_b64_tr_b16 a[6:7], v35 offset:37280\n" \
"v_mfma_f32_16x16x32_bf16 v[152:155], a[8:11], a[232:235], v[152:155]\n" \
"v_mfma_f32_16x16x32_bf16 v[156:159], a[8:11], a[248:251], v[156:159]\n" \
"buffer_load_dwordx4 v5, s[80:83], s68 offen offset:1024 lds\n" \
"v_mfma_f32_16x16x32_bf16 v[224:227], a[8:11], v[76:79], v[224:227]\n" \
"v_mfma_f32_16x16x32_bf16 v[228:231], a[8:11], v[100:103], v[228:231]\n" \
"ds_read_b64_tr_b16 a[8:9], v35 offset:35008\n" \
"ds_read_b64_tr_b16 a[10:11], v35 offset:37312\n" \
"v_mfma_f32_16x16x32_bf16 v[160:163], a[12:15], a[232:235], v[160:163]\n" \
"v_mfma_f32_16x16x32_bf16 v[164:167], a[12:15], a[248:251], v[164:167]\n" \
"v_mfma_f32_16x16x32_bf16 v[232:235], a[12:15], v[76:79], v[232:235]\n" \
"buffer_load_dwordx4 v6, s[80:83], s68 offen offset:2048 lds\n" \
"v_mfma_f32_16x16x32_bf16 v[236:239], a[12:15], v[100:103], v[236:239]\n" \
"ds_read_b64_tr_b16 a[12:13], v35 offset:35040\n" \
"ds_read_b64_tr_b16 a[14:15], v35 offset:37344\n" \
"v_mfma_f32_16x16x32_bf16 v[168:171], a[16:19], a[232:235], v[168:171]\n" \
"v_mfma_f32_16x16x32_bf16 v[172:175], a[16:19], a[248:251], v[172:175]\n" \
"v_mfma_f32_16x16x32_bf16 v[240:243], a[16:19], v[76:79], v[240:243]\n" \
"v_mfma_f32_16x16x32_bf16 v[244:247], a[16:19], v[100:103], v[244:247]\n" \
"buffer_load_dwordx4 v7, s[80:83], s68 offen offset:3072 lds\n" \
"v_mfma_f32_16x16x32_bf16 v[176:179], a[20:23], a[232:235], v[176:179]\n" \
"s_add_u32 m0, s69, 0x2000\n" \
"ds_read_b64_tr_b16 a[16:17], v35 offset:35072\n" \
"v_mfma_f32_16x16x32_bf16 v[180:183], a[20:23], a[248:251], v[180:183]\n" \
"ds_read_b64_tr_b16 a[18:19], v35 offset:37376\n" \
"v_readlane_b32 s69, v11, 47\n" \
"v_mfma_f32_16x16x32_bf16 v[248:251], a[20:23], v[76:79], v[248:251]\n" \
"v_mfma_f32_16x16x32_bf16 v[252:255], a[20:23], v[100:103], v[252:255]\n" \
"ds_read_b64_tr_b16 a[20:21], v35 offset:35104\n" \
"ds_read_b64_tr_b16 a[22:23], v35 offset:37408\n" \
"v_mfma_f32_16x16x32_bf16 v[112:115], a[24:27], a[96:99], v[112:115]\n" \
"buffer_load_dwordx4 v8, s[80:83], s68 offen offset:0 lds\n" \
"v_mfma_f32_16x16x32_bf16 v[116:119], a[24:27], a[104:107], v[116:119]\n" \
"s_add_u32 m0, s94, 0x0\n" \
"v_readlane_b32 s68, v11, 46\n" \
"v_mfma_f32_16x16x32_bf16 v[184:187], a[24:27], a[112:115], v[184:187]\n" \
"v_mfma_f32_16x16x32_bf16 v[188:191], a[24:27], a[120:123], v[188:191]\n" \
"ds_read_b64_tr_b16 a[24:25], v35 offset:35136\n" \
"s_waitcnt lgkmcnt(7)\n" \
"v_mfma_f32_16x16x32_bf16 v[120:123], a[28:31], a[96:99], v[120:123]\n" \
"ds_read_b64_tr_b16 a[26:27], v35 offset:37440\n" \
"buffer_load_dword v47, v38, s[56:59], s73 offen sc1\n" \
"v_mfma_f32_16x16x32_bf16 v[124:127], a[28:31], a[104:107], v[124:127]\n" \
"v_mfma_f32_16x16x32_bf16 v[192:195], a[28:31], a[112:115], v[192:195]\n" \
"v_mfma_f32_16x16x32_bf16 v[196:199], a[28:31], a[120:123], v[196:199]\n" \
"ds_read_b64_tr_b16 a[28:29], v35 offset:35168\n" \
"ds_read_b64_tr_b16 a[30:31], v35 offset:37472\n" \
"v_mfma_f32_16x16x32_bf16 v[128:131], a[192:195], a[96:99], v[128:131]\n" \
"v_mfma_f32_16x16x32_bf16 v[132:135], a[192:195], a[104:107], v[132:135]\n" \
"buffer_load_dwordx4 v0, s[80:83], s93 offen offset:0 lds\n" \
"v_mfma_f32_16x16x32_bf16 v[200:203], a[192:195], a[112:115], v[200:203]\n" \
"v_mfma_f32_16x16x32_bf16 v[204:207], a[192:195], a[120:123], v[204:207]\n" \
"ds_read_b64_tr_b16 a[192:193], v35 offset:35200\n" \
"ds_read_b64_tr_b16 a[194:195], v35 offset:37504\n" \
"v_mfma_f32_16x16x32_bf16 v[136:139], a[196:199], a[96:99], v[136:139]\n" \
"v_mfma_f32_16x16x32_bf16 v[140:143], a[196:199], a[104:107], v[140:143]\n" \
"v_mfma_f32_16x16x32_bf16 v[208:211], a[196:199], a[112:115], v[208:211]\n" \
"buffer_load_dwordx4 v1, s[80:83], s93 offen offset:1024 lds\n" \
"v_mfma_f32_16x16x32_bf16 v[212:215], a[196:199], a[120:123], v[212:215]\n" \
"v_mfma_f32_16x16x32_bf16 v[144:147], a[200:203], a[96:99], v[144:147]\n" \
"v_mfma_f32_16x16x32_bf16 v[148:151], a[200:203], a[104:107], v[148:151]\n" \
"v_mfma_f32_16x16x32_bf16 v[216:219], a[200:203], a[112:115], v[216:219]\n" \
"v_mfma_f32_16x16x32_bf16 v[220:223], a[200:203], a[120:123], v[220:223]\n" \
"buffer_load_dwordx4 v2, s[80:83], s93 offen offset:2048 lds\n" \
"v_mfma_f32_16x16x32_bf16 v[152:155], a[204:207], a[96:99], v[152:155]\n" \
"v_mfma_f32_16x16x32_bf16 v[156:159], a[204:207], a[104:107], v[156:159]\n" \
"v_mfma_f32_16x16x32_bf16 v[224:227], a[204:207], a[112:115], v[224:227]\n" \
"v_mfma_f32_16x16x32_bf16 v[228:231], a[204:207], a[120:123], v[228:231]\n" \
"v_mfma_f32_16x16x32_bf16 v[160:163], a[208:211], a[96:99], v[160:163]\n" \
"buffer_load_dwordx4 v3, s[80:83], s93 offen offset:3072 lds\n" \
"v_mfma_f32_16x16x32_bf16 v[164:167], a[208:211], a[104:107], v[164:167]\n" \
"s_add_u32 m0, s94, 0x1000\n" \
"v_mfma_f32_16x16x32_bf16 v[232:235], a[208:211], a[112:115], v[232:235]\n" \
"v_mfma_f32_16x16x32_bf16 v[236:239], a[208:211], a[120:123], v[236:239]\n" \
"v_mfma_f32_16x16x32_bf16 v[168:171], a[212:215], a[96:99], v[168:171]\n" \
"v_mfma_f32_16x16x32_bf16 v[172:175], a[212:215], a[104:107], v[172:175]\n" \
"buffer_load_dwordx4 v4, s[80:83], s93 offen offset:0 lds\n" \
"v_mfma_f32_16x16x32_bf16 v[240:243], a[212:215], a[112:115], v[240:243]\n" \
"v_mfma_f32_16x16x32_bf16 v[244:247], a[212:215], a[120:123], v[244:247]\n" \
"v_mfma_f32_16x16x32_bf16 v[176:179], a[216:219], a[96:99], v[176:179]\n" \
"v_mfma_f32_16x16x32_bf16 v[180:183], a[216:219], a[104:107], v[180:183]\n" \
"v_mfma_f32_16x16x32_bf16 v[248:251], a[216:219], a[112:115], v[248:251]\n" \
"buffer_load_dwordx4 v5, s[80:83], s93 offen offset:1024 lds\n" \
"v_mfma_f32_16x16x32_bf16 v[252:255], a[216:219], a[120:123], v[252:255]\n" \
"v_mfma_f32_16x16x32_bf16 v[112:115], a[0:3], a[100:103], v[112:115]\n" \
"v_mfma_f32_16x16x32_bf16 v[116:119], a[0:3], a[108:111], v[116:119]\n" \
"v_mfma_f32_16x16x32_bf16 v[184:187], a[0:3], a[116:119], v[184:187]\n" \
"v_mfma_f32_16x16x32_bf16 v[188:191], a[0:3], a[124:127], v[188:191]\n" \
"buffer_load_dwordx4 v6, s[80:83], s93 offen offset:2048 lds\n" \
"v_mfma_f32_16x16x32_bf16 v[120:123], a[4:7], a[100:103], v[120:123]\n" \
"v_mfma_f32_16x16x32_bf16 v[124:127], a[4:7], a[108:111], v[124:127]\n" \
"v_mfma_f32_16x16x32_bf16 v[192:195], a[4:7], a[116:119], v[192:195]\n" \
"v_mfma_f32_16x16x32_bf16 v[196:199], a[4:7], a[124:127], v[196:199]\n" \
"v_mfma_f32_16x16x32_bf16 v[128:131], a[8:11], a[100:103], v[128:131]\n" \
"buffer_load_dwordx4 v7, s[80:83], s93 offen offset:3072 lds\n" \
"s_add_u32 m0, s94, 0x2000\n" \
"v_mfma_f32_16x16x32_bf16 v[132:135], a[8:11], a[108:111], v[132:135]\n" \
"v_readlane_b32 s94, v11, 45\n" \
"v_mfma_f32_16x16x32_bf16 v[200:203], a[8:11], a[116:119], v[200:203]\n" \
"v_mfma_f32_16x16x32_bf16 v[204:207], a[8:11], a[124:127], v[204:207]\n" \
"s_waitcnt lgkmcnt(0)\n" \
"v_mfma_f32_16x16x32_bf16 v[136:139], a[12:15], a[100:103], v[136:139]\n" \
"buffer_load_dwordx4 v8, s[80:83], s93 offen offset:0 lds\n" \
"s_mov_b32 m0, s96\n" \
"v_mfma_f32_16x16x32_bf16 v[140:143], a[12:15], a[108:111], v[140:143]\n" \
"v_readlane_b32 s96, v11, 47\n" \
"v_readlane_b32 s93, v11, 6\n" \
"v_mfma_f32_16x16x32_bf16 v[208:211], a[12:15], a[116:119], v[208:211]\n" \
"v_mfma_f32_16x16x32_bf16 v[212:215], a[12:15], a[124:127], v[212:215]\n" \
"s_waitcnt vmcnt(23)\n" \
"s_barrier\n" \
"buffer_load_dwordx4 v9, s[84:87], 0 offen lds\n" \
"v_mfma_f32_16x16x32_bf16 v[144:147], a[16:19], a[100:103], v[144:147]\n" \
"ds_read_b64_tr_b16 a[196:197], v33 offset:0\n" \
"ds_read_b64_tr_b16 a[198:199], v33 offset:4608\n" \
"v_mfma_f32_16x16x32_bf16 v[148:151], a[16:19], a[108:111], v[148:151]\n" \
"ds_read_b64_tr_b16 a[200:201], v33 offset:32\n" \
"ds_read_b64_tr_b16 a[202:203], v33 offset:4640\n" \
"v_mfma_f32_16x16x32_bf16 v[216:219], a[16:19], a[116:119], v[216:219]\n" \
"ds_read_b64_tr_b16 a[204:205], v33 offset:64\n" \
"ds_read_b64_tr_b16 a[206:207], v33 offset:4672\n" \
"v_mfma_f32_16x16x32_bf16 v[220:223], a[16:19], a[124:127], v[220:223]\n" \
"ds_read_b64_tr_b16 a[208:209], v33 offset:96\n" \
"ds_read_b64_tr_b16 a[210:211], v33 offset:4704\n" \
"buffer_load_dwordx4 a[76:79], v20, s[56:59], s67 offen offset:3072 sc1\n" \
"v_mfma_f32_16x16x32_bf16 v[152:155], a[20:23], a[100:103], v[152:155]\n" \
"ds_read_b64_tr_b16 a[212:213], v33 offset:128\n" \
"ds_read_b64_tr_b16 a[214:215], v33 offset:4736\n" \
"v_mfma_f32_16x16x32_bf16 v[156:159], a[20:23], a[108:111], v[156:159]\n" \
"ds_read_b64_tr_b16 a[216:217], v33 offset:160\n" \
"ds_read_b64_tr_b16 a[218:219], v33 offset:4768\n" \
"v_mfma_f32_16x16x32_bf16 v[224:227], a[20:23], a[116:119], v[224:227]\n" \
"ds_read_b64_tr_b16 a[0:1], v33 offset:192\n" \
"ds_read_b64_tr_b16 a[2:3], v33 offset:4800\n" \
"v_mfma_f32_16x16x32_bf16 v[228:231], a[20:23], a[124:127], v[228:231]\n" \
"ds_read_b64_tr_b16 a[4:5], v33 offset:224\n" \
"ds_read_b64_tr_b16 a[6:7], v33 offset:4832\n" \
"buffer_load_dwordx4 a[92:95], v32, s[56:59], s67 offen offset:3072 sc1\n" \
"v_mfma_f32_16x16x32_bf16 v[160:163], a[24:27], a[100:103], v[160:163]\n" \
"v_readlane_b32 s67, v11, 45\n" \
"ds_read_b64_tr_b16 a[8:9], v33 offset:256\n" \
"v_mfma_f32_16x16x32_bf16 v[164:167], a[24:27], a[108:111], v[164:167]\n" \
"ds_read_b64_tr_b16 a[10:11], v33 offset:4864\n" \
"ds_read_b64_tr_b16 a[12:13], v33 offset:9216\n" \
"v_mfma_f32_16x16x32_bf16 v[232:235], a[24:27], a[116:119], v[232:235]\n" \
"ds_read_b64_tr_b16 a[14:15], v33 offset:13824\n" \
"ds_read_b64_tr_b16 a[16:17], v33 offset:9248\n" \
"v_mfma_f32_16x16x32_bf16 v[236:239], a[24:27], a[124:127], v[236:239]\n" \
"ds_read_b64_tr_b16 a[18:19], v33 offset:13856\n" \
"ds_read_b64_tr_b16 a[20:21], v33 offset:9280\n" \
"buffer_load_dwordx4 a[128:131], v20, s[56:59], s70 offen offset:0 sc1\n" \
"v_mfma_f32_16x16x32_bf16 v[168:171], a[28:31], a[100:103], v[168:171]\n" \
"ds_read_b64_tr_b16 a[22:23], v33 offset:13888\n" \
"ds_read_b64_tr_b16 a[24:25], v33 offset:9312\n" \
"v_mfma_f32_16x16x32_bf16 v[172:175], a[28:31], a[108:111], v[172:175]\n" \
"ds_read_b64_tr_b16 a[26:27], v33 offset:13920\n" \
"s_waitcnt vmcnt(13)\n" \
"v_cmp_ne_u32_e32 vcc, s62, v47\n" \
"v_mfma_f32_16x16x32_bf16 v[240:243], a[28:31], a[116:119], v[240:243]\n" \
"v_mfma_f32_16x16x32_bf16 v[244:247], a[28:31], a[124:127], v[244:247]\n" \
"buffer_load_dwordx4 a[144:147], v32, s[56:59], s70 offen offset:0 sc1\n" \
"v_mfma_f32_16x16x32_bf16 v[176:179], a[192:195], a[100:103], v[176:179]\n" \
"ds_read_b64_tr_b16 a[28:29], v33 offset:9344\n" \
"ds_read_b64_tr_b16 a[30:31], v33 offset:13952\n" \
"v_mfma_f32_16x16x32_bf16 v[180:183], a[192:195], a[108:111], v[180:183]\n" \
"s_cbranch_vccnz L_sb_%=\n" \
"L_rb_%=:\n" \
"v_readlane_b32 s73, v11, 51\n" \
"v_mfma_f32_16x16x32_bf16 v[248:251], a[192:195], a[116:119], v[248:251]\n" \
"v_cmp_eq_u32_e32 vcc, 0, v44\n" \
"v_mfma_f32_16x16x32_bf16 v[252:255], a[192:195], a[124:127], v[252:255]\n" \
"ds_read_b64_tr_b16 a[192:193], v33 offset:9376\n" \
"buffer_load_dwordx4 a[132:135], v20, s[56:59], s70 offen offset:1024 sc1\n" \
"s_waitcnt lgkmcnt(14)\n" \
"v_mfma_f32_16x16x32_bf16 v[112:115], a[196:199], a[64:67], v[112:115]\n" \
"ds_read_b64_tr_b16 a[194:195], v33 offset:13984\n" \
"v_cndmask_b32_e32 v50, 0, v45, vcc\n" \
"v_mfma_f32_16x16x32_bf16 v[116:119], a[196:199], a[80:83], v[116:119]\n" \
"ds_read_b64_tr_b16 a[196:197], v33 offset:9408\n" \
"ds_read_b64_tr_b16 a[198:199], v33 offset:14016\n" \
"v_mfma_f32_16x16x32_bf16 v[120:123], a[200:203], a[64:67], v[120:123]\n" \
"v_cmp_eq_u32_e32 vcc, 1, v44\n" \
"v_and_b32_e32 v44, 0x3c0, v20\n" \
"v_mfma_f32_16x16x32_bf16 v[124:127], a[200:203], a[80:83], v[124:127]\n" \
"buffer_load_dwordx4 a[148:151], v32, s[56:59], s70 offen offset:1024 sc1\n" \
"v_mfma_f32_16x16x32_bf16 v[128:131], a[204:207], a[64:67], v[128:131]\n" \
"ds_read_b64_tr_b16 a[200:201], v33 offset:9440\n" \
"ds_read_b64_tr_b16 a[202:203], v33 offset:14048\n" \
"v_mfma_f32_16x16x32_bf16 v[132:135], a[204:207], a[80:83], v[132:135]\n" \
"ds_read_b64_tr_b16 a[204:205], v33 offset:9472\n" \
"ds_read_b64_tr_b16 a[206:207], v33 offset:14080\n" \
"v_mfma_f32_16x16x32_bf16 v[136:139], a[208:211], a[64:67], v[136:139]\n" \
"v_cndmask_b32_e32 v51, 0, v45, vcc\n" \
"v_bfe_u32 v45, v20, 1, 5\n" \
"v_mfma_f32_16x16x32_bf16 v[140:143], a[208:211], a[80:83], v[140:143]\n" \
"ds_read_b64_tr_b16 a[208:209], v33 offset:18432\n" \
"ds_read_b64_tr_b16 a[210:211], v33 offset:23040\n" \
"buffer_load_dwordx4 a[136:139], v20, s[56:59], s70 offen offset:2048 sc1\n" \
"v_mfma_f32_16x16x32_bf16 v[144:147], a[212:215], a[64:67], v[144:147]\n" \
"v_and_b32_e32 v45, 24, v45\n" \
"v_add_u32_e32 v45, 0xf000, v45\n" \
"v_mfma_f32_16x16x32_bf16 v[148:151], a[212:215], a[80:83], v[148:151]\n" \
"ds_read_b64_tr_b16 a[212:213], v33 offset:18464\n" \
"ds_read_b64_tr_b16 a[214:215], v33 offset:23072\n" \
"v_mfma_f32_16x16x32_bf16 v[152:155], a[216:219], a[64:67], v[152:155]\n" \
"v_add_u32_e32 v36, v44, v45\n" \
"v_mfma_f32_16x16x32_bf16 v[156:159], a[216:219], a[80:83], v[156:159]\n" \
"ds_read_b64_tr_b16 a[216:217], v33 offset:18496\n" \
"ds_read_b64_tr_b16 a[218:219], v33 offset:23104\n" \
"buffer_load_dwordx4 a[152:155], v32, s[56:59], s70 offen offset:2048 sc1\n" \
"v_mfma_f32_16x16x32_bf16 v[160:163], a[0:3], a[64:67], v[160:163]\n" \
"v_mfma_f32_16x16x32_bf16 v[164:167], a[0:3], a[80:83], v[164:167]\n" \
"ds_read_b64_tr_b16 a[0:1], v33 offset:18528\n" \
"s_waitcnt lgkmcnt(13)\n" \
"v_mfma_f32_16x16x32_bf16 v[168:171], a[4:7], a[64:67], v[168:171]\n" \
"ds_read_b64_tr_b16 a[2:3], v33 offset:23136\n" \
"v_mfma_f32_16x16x32_bf16 v[172:175], a[4:7], a[80:83], v[172:175]\n" \
"ds_read_b64_tr_b16 a[4:5], v33 offset:18560\n" \
"ds_read_b64_tr_b16 a[6:7], v33 offset:23168\n" \
"buffer_load_dwordx4 a[140:143], v20, s[56:59], s70 offen offset:3072 sc1\n" \
"v_mfma_f32_16x16x32_bf16 v[176:179], a[8:11], a[64:67], v[176:179]\n" \
"v_mfma_f32_16x16x32_bf16 v[180:183], a[8:11], a[80:83], v[180:183]\n" \
"ds_read_b64_tr_b16 a[8:9], v33 offset:18592\n" \
"ds_read_b64_tr_b16 a[10:11], v33 offset:23200\n" \
"v_mfma_f32_16x16x32_bf16 v[112:115], a[12:15], a[68:71], v[112:115]\n" \
"v_mfma_f32_16x16x32_bf16 v[116:119], a[12:15], a[84:87], v[116:119]\n" \
"ds_read_b64_tr_b16 a[12:13], v33 offset:18624\n" \
"ds_read_b64_tr_b16 a[14:15], v33 offset:23232\n" \
"buffer_load_dwordx4 a[156:159], v32, s[56:59], s70 offen offset:3072 sc1\n" \
"v_mfma_f32_16x16x32_bf16 v[120:123], a[16:19], a[68:71], v[120:123]\n" \
"v_readlane_b32 s70, v11, 48\n" \
"v_mfma_f32_16x16x32_bf16 v[124:127], a[16:19], a[84:87], v[124:127]\n" \
"ds_read_b64_tr_b16 a[16:17], v33 offset:18656\n" \
"ds_read_b64_tr_b16 a[18:19], v33 offset:23264\n" \
"v_mfma_f32_16x16x32_bf16 v[128:131], a[20:23], a[68:71], v[128:131]\n" \
"v_mfma_f32_16x16x32_bf16 v[132:135], a[20:23], a[84:87], v[132:135]\n" \
"ds_read_b64_tr_b16 a[20:21], v33 offset:18688\n" \
"ds_read_b64_tr_b16 a[22:23], v33 offset:23296\n" \
"buffer_load_dwordx4 a[32:35], v20, s[56:59], s71 offen offset:0 sc1\n" \
"v_mfma_f32_16x16x32_bf16 v[136:139], a[24:27], a[68:71], v[136:139]\n" \
"v_mfma_f32_16x16x32_bf16 v[140:143], a[24:27], a[84:87], v[140:143]\n" \
"ds_read_b64_tr_b16 a[24:25], v33 offset:27648\n" \
"ds_read_b64_tr_b16 a[26:27], v33 offset:32256\n" \
"v_mfma_f32_16x16x32_bf16 v[144:147], a[28:31], a[68:71], v[144:147]\n" \
"v_mfma_f32_16x16x32_bf16 v[148:151], a[28:31], a[84:87], v[148:151]\n" \
"ds_read_b64_tr_b16 a[28:29], v33 offset:27680\n" \
"ds_read_b64_tr_b16 a[30:31], v33 offset:32288\n" \
"buffer_load_dwordx4 a[48:51], v32, s[56:59], s71 offen offset:0 sc1\n" \
"v_mfma_f32_16x16x32_bf16 v[152:155], a[192:195], a[68:71], v[152:155]\n" \
"v_mfma_f32_16x16x32_bf16 v[156:159], a[192:195], a[84:87], v[156:159]\n" \
"ds_read_b64_tr_b16 a[192:193], v33 offset:27712\n" \
"s_waitcnt lgkmcnt(13)\n" \
"v_mfma_f32_16x16x32_bf16 v[160:163], a[196:199], a[68:71], v[160:163]\n" \
"ds_read_b64_tr_b16 a[194:195], v33 offset:32320\n" \
"v_mfma_f32_16x16x32_bf16 v[164:167], a[196:199], a[84:87], v[164:167]\n" \
"ds_read_b64_tr_b16 a[196:197], v33 offset:27744\n" \
"ds_read_b64_tr_b16 a[198:199], v33 offset:32352\n" \
"buffer_load_dwordx4 a[36:39], v20, s[56:59], s71 offen offset:1024 sc1\n" \
"v_mfma_f32_16x16x32_bf16 v[168:171], a[200:203], a[68:71], v[168:171]\n" \
"v_mfma_f32_16x16x32_bf16 v[172:175], a[200:203], a[84:87], v[172:175]\n" \
"ds_read_b64_tr_b16 a[200:201], v33 offset:27776\n" \
"ds_read_b64_tr_b16 a[202:203], v33 offset:32384\n" \
"v_mfma_f32_16x16x32_bf16 v[176:179], a[204:207], a[68:71], v[176:179]\n" \
"v_mfma_f32_16x16x32_bf16 v[180:183], a[204:207], a[84:87], v[180:183]\n" \
"ds_read_b64_tr_b16 a[204:205], v33 offset:27808\n" \
"ds_read_b64_tr_b16 a[206:207], v33 offset:32416\n" \
"buffer_load_dwordx4 a[52:55], v32, s[56:59], s71 offen offset:1024 sc1\n" \
"v_mfma_f32_16x16x32_bf16 v[112:115], a[208:211], a[72:75], v[112:115]\n" \
"v_mfma_f32_16x16x32_bf16 v[116:119], a[208:211], a[88:91], v[116:119]\n" \
"ds_read_b64_tr_b16 a[208:209], v33 offset:27840\n" \
"ds_read_b64_tr_b16 a[210:211], v33 offset:32448\n" \
"v_mfma_f32_16x16x32_bf16 v[120:123], a[212:215], a[72:75], v[120:123]\n" \
"v_mfma_f32_16x16x32_bf16 v[124:127], a[212:215], a[88:91], v[124:127]\n" \
"ds_read_b64_tr_b16 a[212:213], v33 offset:27872\n" \
"ds_read_b64_tr_b16 a[214:215], v33 offset:32480\n" \
"buffer_load_dwordx4 a[40:43], v20, s[56:59], s71 offen offset:2048 sc1\n" \
"v_mfma_f32_16x16x32_bf16 v[128:131], a[216:219], a[72:75], v[128:131]\n" \
"v_mfma_f32_16x16x32_bf16 v[132:135], a[216:219], a[88:91], v[132:135]\n" \
"ds_read_b64_tr_b16 a[216:217], v33 offset:27904\n" \
"ds_read_b64_tr_b16 a[218:219], v33 offset:32512\n" \
"v_mfma_f32_16x16x32_bf16 v[136:139], a[0:3], a[72:75], v[136:139]\n" \
"v_mfma_f32_16x16x32_bf16 v[140:143], a[0:3], a[88:91], v[140:143]\n" \
"v_mfma_f32_16x16x32_bf16 v[144:147], a[4:7], a[72:75], v[144:147]\n" \
"buffer_load_dwordx4 a[56:59], v32, s[56:59], s71 offen offset:2048 sc1\n" \
"v_mfma_f32_16x16x32_bf16 v[148:151], a[4:7], a[88:91], v[148:151]\n" \
"s_waitcnt lgkmcnt(10)\n" \
"v_mfma_f32_16x16x32_bf16 v[152:155], a[8:11], a[72:75], v[152:155]\n" \
"v_mfma_f32_16x16x32_bf16 v[156:159], a[8:11], a[88:91], v[156:159]\n" \
"v_mfma_f32_16x16x32_bf16 v[160:163], a[12:15], a[72:75], v[160:163]\n" \
"v_mfma_f32_16x16x32_bf16 v[164:167], a[12:15], a[88:91], v[164:167]\n" \
"buffer_load_dwordx4 a[44:47], v20, s[56:59], s71 offen offset:3072 sc1\n" \
"v_mfma_f32_16x16x32_bf16 v[168:171], a[16:19], a[72:75], v[168:171]\n" \
"v_mfma_f32_16x16x32_bf16 v[172:175], a[16:19], a[88:91], v[172:175]\n" \
"v_mfma_f32_16x16x32_bf16 v[176:179], a[20:23], a[72:75], v[176:179]\n" \
"v_mfma_f32_16x16x32_bf16 v[180:183], a[20:23], a[88:91], v[180:183]\n" \
"buffer_load_dwordx4 a[60:63], v32, s[56:59], s71 offen offset:3072 sc1\n" \
"v_readlane_b32 s71, v11, 49\n" \
"buffer_load_dwordx4 a[96:99], v20, s[56:59], s66 offen offset:0 sc1\n" \
"buffer_load_dwordx4 a[112:115], v32, s[56:59], s66 offen offset:0 sc1\n" \
"s_waitcnt vmcnt(20)\n" \
"s_barrier\n" \
"ds_read_b64_tr_b16 a[0:1], v33 offset:36864\n" \
"ds_read_b64_tr_b16 a[2:3], v33 offset:41472\n" \
"ds_read_b64_tr_b16 a[4:5], v33 offset:46080\n" \
"ds_read_b64_tr_b16 a[6:7], v33 offset:50688\n" \
"ds_read_b64_tr_b16 a[8:9], v33 offset:55296\n" \
"ds_read_b64_tr_b16 a[10:11], v33 offset:59904\n" \
"ds_read_b64_tr_b16 a[12:13], v10 offset:0\n" \
"ds_read_b64_tr_b16 a[14:15], v10 offset:4608\n" \
"ds_read_b64_tr_b16 a[16:17], v34 offset:0\n" \
"ds_read_b64_tr_b16 a[18:19], v34 offset:4608\n" \
"ds_read_b64_tr_b16 a[20:21], v34 offset:9216\n" \
"ds_read_b64_tr_b16 a[22:23], v34 offset:13824\n" \
"buffer_load_dwordx4 a[100:103], v20, s[56:59], s66 offen offset:1024 sc1\n" \
"s_waitcnt vmcnt(20)\n" \
"v_mfma_f32_16x16x32_bf16 v[112:115], a[24:27], a[76:79], v[112:115]\n" \
"s_waitcnt vmcnt(19)\n" \
"v_mfma_f32_16x16x32_bf16 v[116:119], a[24:27], a[92:95], v[116:119]\n" \
"ds_read_b64_tr_b16 a[24:25], v34 offset:18432\n" \
"ds_read_b64_tr_b16 a[26:27], v34 offset:23040\n" \
"v_mfma_f32_16x16x32_bf16 v[120:123], a[28:31], a[76:79], v[120:123]\n" \
"buffer_load_dwordx4 a[116:119], v32, s[56:59], s66 offen offset:1024 sc1\n" \
"v_mfma_f32_16x16x32_bf16 v[124:127], a[28:31], a[92:95], v[124:127]\n" \
"ds_read_b64_tr_b16 a[28:29], v10 offset:9216\n" \
"ds_read_b64_tr_b16 a[30:31], v10 offset:13824\n" \
"v_mfma_f32_16x16x32_bf16 v[128:131], a[192:195], a[76:79], v[128:131]\n" \
"v_mfma_f32_16x16x32_bf16 v[132:135], a[192:195], a[92:95], v[132:135]\n" \
"ds_read_b64_tr_b16 a[192:193], v33 offset:36896\n" \
"ds_read_b64_tr_b16 a[194:195], v33 offset:41504\n" \
"v_mfma_f32_16x16x32_bf16 v[136:139], a[196:199], a[76:79], v[136:139]\n" \
"v_mfma_f32_16x16x32_bf16 v[140:143], a[196:199], a[92:95], v[140:143]\n" \
"ds_read_b64_tr_b16 a[196:197], v33 offset:46112\n" \
"s_waitcnt lgkmcnt(15)\n" \
"v_mfma_f32_16x16x32_bf16 v[144:147], a[200:203], a[76:79], v[144:147]\n" \
"buffer_load_dwordx4 a[104:107], v20, s[56:59], s66 offen offset:2048 sc1\n" \
"v_mfma_f32_16x16x32_bf16 v[148:151], a[200:203], a[92:95], v[148:151]\n" \
"ds_read_b64_tr_b16 a[198:199], v33 offset:50720\n" \
"ds_read_b64_tr_b16 a[200:201], v33 offset:55328\n" \
"v_mfma_f32_16x16x32_bf16 v[152:155], a[204:207], a[76:79], v[152:155]\n" \
"ds_read_b64_tr_b16 a[202:203], v33 offset:59936\n" \
"v_mfma_f32_16x16x32_bf16 v[156:159], a[204:207], a[92:95], v[156:159]\n" \
"ds_read_b64_tr_b16 a[204:205], v10 offset:32\n" \
"ds_read_b64_tr_b16 a[206:207], v10 offset:4640\n" \
"v_mfma_f32_16x16x32_bf16 v[160:163], a[208:211], a[76:79], v[160:163]\n" \
"v_mfma_f32_16x16x32_bf16 v[164:167], a[208:211], a[92:95], v[164:167]\n" \
"ds_read_b64_tr_b16 a[208:209], v34 offset:32\n" \
"ds_read_b64_tr_b16 a[210:211], v34 offset:4640\n" \
"v_mfma_f32_16x16x32_bf16 v[168:171], a[212:215], a[76:79], v[168:171]\n" \
"buffer_load_dwordx4 a[120:123], v32, s[56:59], s66 offen offset:2048 sc1\n" \
"v_mfma_f32_16x16x32_bf16 v[172:175], a[212:215], a[92:95], v[172:175]\n" \
"ds_read_b64_tr_b16 a[212:213], v34 offset:9248\n" \
"ds_read_b64_tr_b16 a[214:215], v34 offset:13856\n" \
"v_mfma_f32_16x16x32_bf16 v[176:179], a[216:219], a[76:79], v[176:179]\n" \
"v_mfma_f32_16x16x32_bf16 v[180:183], a[216:219], a[92:95], v[180:183]\n" \
"ds_read_b64_tr_b16 a[216:217], v34 offset:18464\n" \
"s_waitcnt vmcnt(21)\n" \
"v_mfma_f32_16x16x32_bf16 v[112:115], a[0:3], a[128:131], v[112:115]\n" \
"ds_read_b64_tr_b16 a[218:219], v34 offset:23072\n" \
"s_waitcnt vmcnt(20)\n" \
"v_mfma_f32_16x16x32_bf16 v[116:119], a[0:3], a[144:147], v[116:119]\n" \
"ds_read_b64_tr_b16 a[0:1], v10 offset:9248\n" \
"s_waitcnt vmcnt(19)\n" \
"v_mfma_f32_16x16x32_bf16 v[112:115], a[4:7], a[132:135], v[112:115]\n" \
"ds_read_b64_tr_b16 a[2:3], v10 offset:13856\n" \
"s_waitcnt vmcnt(18)\n" \
"v_mfma_f32_16x16x32_bf16 v[116:119], a[4:7], a[148:151], v[116:119]\n" \
"ds_read_b64_tr_b16 a[4:5], v33 offset:36928\n" \
"ds_read_b64_tr_b16 a[6:7], v33 offset:41536\n" \
"buffer_load_dwordx4 a[108:111], v20, s[56:59], s66 offen offset:3072 sc1\n" \
"s_waitcnt vmcnt(18) lgkmcnt(15)\n" \
"v_mfma_f32_16x16x32_bf16 v[112:115], a[8:11], a[136:139], v[112:115]\n" \
"buffer_load_dwordx4 a[124:127], v32, s[56:59], s66 offen offset:3072 sc1\n" \
"s_waitcnt vmcnt(18)\n" \
"v_mfma_f32_16x16x32_bf16 v[116:119], a[8:11], a[152:155], v[116:119]\n" \
"ds_read_b64_tr_b16 a[8:9], v33 offset:46144\n" \
"ds_read_b64_tr_b16 a[10:11], v33 offset:50752\n" \
"buffer_load_dwordx4 a[160:163], v20, s[56:59], s92 offen offset:0 sc1\n" \
"s_waitcnt vmcnt(18)\n" \
"v_mfma_f32_16x16x32_bf16 v[112:115], a[12:15], a[140:143], v[112:115]\n" \
"buffer_load_dwordx4 a[176:179], v32, s[56:59], s92 offen offset:0 sc1\n" \
"s_waitcnt vmcnt(18)\n" \
"v_mfma_f32_16x16x32_bf16 v[116:119], a[12:15], a[156:159], v[116:119]\n" \
"ds_read_b64_tr_b16 a[12:13], v33 offset:55360\n" \
"ds_read_b64_tr_b16 a[14:15], v33 offset:59968\n" \
"buffer_load_dwordx4 a[164:167], v20, s[56:59], s92 offen offset:1024 sc1\n" \
"s_waitcnt vmcnt(18)\n" \
"v_mfma_f32_16x16x32_bf16 v[112:115], a[16:19], a[32:35], v[112:115]\n" \
"buffer_load_dwordx4 a[180:183], v32, s[56:59], s92 offen offset:1024 sc1\n" \
"s_waitcnt vmcnt(18)\n" \
"v_mfma_f32_16x16x32_bf16 v[116:119], a[16:19], a[48:51], v[116:119]\n" \
"ds_read_b64_tr_b16 a[16:17], v10 offset:64\n" \
"ds_read_b64_tr_b16 a[18:19], v10 offset:4672\n" \
"buffer_load_dwordx4 a[168:171], v20, s[56:59], s92 offen offset:2048 sc1\n" \
"s_waitcnt vmcnt(18)\n" \
"v_mfma_f32_16x16x32_bf16 v[112:115], a[20:23], a[36:39], v[112:115]\n" \
"buffer_load_dwordx4 a[184:187], v32, s[56:59], s92 offen offset:2048 sc1\n" \
"s_waitcnt vmcnt(18)\n" \
"v_mfma_f32_16x16x32_bf16 v[116:119], a[20:23], a[52:55], v[116:119]\n" \
"ds_read_b64_tr_b16 a[20:21], v34 offset:64\n" \
"ds_read_b64_tr_b16 a[22:23], v34 offset:4672\n" \
"buffer_load_dwordx4 a[172:175], v20, s[56:59], s92 offen offset:3072 sc1\n" \
"s_waitcnt vmcnt(18)\n" \
"v_mfma_f32_16x16x32_bf16 v[112:115], a[24:27], a[40:43], v[112:115]\n" \
"buffer_load_dwordx4 a[188:191], v32, s[56:59], s92 offen offset:3072 sc1\n" \
"v_readlane_b32 s92, v11, 52\n" \
"s_waitcnt vmcnt(18)\n" \
"v_mfma_f32_16x16x32_bf16 v[116:119], a[24:27], a[56:59], v[116:119]\n" \
"ds_read_b64_tr_b16 a[24:25], v34 offset:9280\n" \
"ds_read_b64_tr_b16 a[26:27], v34 offset:13888\n" \
"buffer_load_dwordx4 a[220:223], v20, s[56:59], s95 offen offset:0 sc1\n" \
"s_waitcnt vmcnt(18)\n" \
"v_mfma_f32_16x16x32_bf16 v[112:115], a[28:31], a[44:47], v[112:115]\n" \
"buffer_load_dwordx4 a[236:239], v32, s[56:59], s95 offen offset:0 sc1\n" \
"s_waitcnt vmcnt(18)\n" \
"v_mfma_f32_16x16x32_bf16 v[116:119], a[28:31], a[60:63], v[116:119]\n" \
"ds_read_b64_tr_b16 a[28:29], v34 offset:18496\n" \
"ds_read_b64_tr_b16 a[30:31], v34 offset:23104\n" \
"v_mfma_f32_16x16x32_bf16 v[120:123], a[192:195], a[128:131], v[120:123]\n" \
"buffer_load_dwordx4 a[224:227], v20, s[56:59], s95 offen offset:1024 sc1\n" \
"v_mfma_f32_16x16x32_bf16 v[124:127], a[192:195], a[144:147], v[124:127]\n" \
"v_cvt_pk_bf16_f32 v112, v112, v113\n" \
"v_cvt_pk_bf16_f32 v113, v114, v115\n" \
"s_waitcnt lgkmcnt(4)\n" \
"v_mfma_f32_16x16x32_bf16 v[120:123], a[196:199], a[132:135], v[120:123]\n" \
"v_cvt_pk_bf16_f32 v114, v116, v117\n" \
"v_cvt_pk_bf16_f32 v115, v118, v119\n" \
"buffer_store_dwordx4 v[112:115], v20, s[56:59], s67 offen offset:0 sc1\n" \
"s_add_u32 s67, s67, 0x3800\n" \
"v_mfma_f32_16x16x32_bf16 v[124:127], a[196:199], a[148:151], v[124:127]\n" \
"ds_read_b64_tr_b16 a[192:193], v10 offset:9280\n" \
"ds_read_b64_tr_b16 a[194:195], v10 offset:13888\n" \
"v_mfma_f32_16x16x32_bf16 v[120:123], a[200:203], a[136:139], v[120:123]\n" \
"ds_read_b64_tr_b16 a[196:197], v33 offset:36960\n" \
"ds_read_b64_tr_b16 a[198:199], v33 offset:41568\n" \
"v_mfma_f32_16x16x32_bf16 v[124:127], a[200:203], a[152:155], v[124:127]\n" \
"ds_read_b64_tr_b16 a[200:201], v33 offset:46176\n" \
"ds_read_b64_tr_b16 a[202:203], v33 offset:50784\n" \
"v_mfma_f32_16x16x32_bf16 v[120:123], a[204:207], a[140:143], v[120:123]\n" \
"buffer_load_dwordx4 a[240:243], v32, s[56:59], s95 offen offset:1024 sc1\n" \
"v_mfma_f32_16x16x32_bf16 v[124:127], a[204:207], a[156:159], v[124:127]\n" \
"ds_read_b64_tr_b16 a[204:205], v33 offset:55392\n" \
"ds_read_b64_tr_b16 a[206:207], v33 offset:60000\n" \
"v_mfma_f32_16x16x32_bf16 v[120:123], a[208:211], a[32:35], v[120:123]\n" \
"v_mfma_f32_16x16x32_bf16 v[124:127], a[208:211], a[48:51], v[124:127]\n" \
"ds_read_b64_tr_b16 a[208:209], v10 offset:96\n" \
"ds_read_b64_tr_b16 a[210:211], v10 offset:4704\n" \
"v_mfma_f32_16x16x32_bf16 v[120:123], a[212:215], a[36:39], v[120:123]\n" \
"v_mfma_f32_16x16x32_bf16 v[124:127], a[212:215], a[52:55], v[124:127]\n" \
"ds_read_b64_tr_b16 a[212:213], v34 offset:96\n" \
"ds_read_b64_tr_b16 a[214:215], v34 offset:4704\n" \
"v_mfma_f32_16x16x32_bf16 v[120:123], a[216:219], a[40:43], v[120:123]\n" \
"buffer_load_dwordx4 a[228:231], v20, s[56:59], s95 offen offset:2048 sc1\n" \
"v_mfma_f32_16x16x32_bf16 v[124:127], a[216:219], a[56:59], v[124:127]\n" \
"ds_read_b64_tr_b16 a[216:217], v34 offset:9312\n" \
"ds_read_b64_tr_b16 a[218:219], v34 offset:13920\n" \
"v_mfma_f32_16x16x32_bf16 v[120:123], a[0:3], a[44:47], v[120:123]\n" \
"v_mfma_f32_16x16x32_bf16 v[124:127], a[0:3], a[60:63], v[124:127]\n" \
"ds_read_b64_tr_b16 a[0:1], v34 offset:18528\n" \
"ds_read_b64_tr_b16 a[2:3], v34 offset:23136\n" \
"v_mfma_f32_16x16x32_bf16 v[128:131], a[4:7], a[128:131], v[128:131]\n" \
"v_mfma_f32_16x16x32_bf16 v[132:135], a[4:7], a[144:147], v[132:135]\n" \
"ds_read_b64_tr_b16 a[4:5], v10 offset:9312\n" \
"ds_read_b64_tr_b16 a[6:7], v10 offset:13920\n" \
"v_mfma_f32_16x16x32_bf16 v[128:131], a[8:11], a[132:135], v[128:131]\n" \
"v_cvt_pk_bf16_f32 v120, v120, v121\n" \
"v_cvt_pk_bf16_f32 v121, v122, v123\n" \
"v_cvt_pk_bf16_f32 v122, v124, v125\n" \
"v_cvt_pk_bf16_f32 v123, v126, v127\n" \
"buffer_store_dwordx4 v[120:123], v20, s[56:59], s68 offen offset:0 sc1\n" \
"s_add_u32 s68, s68, 0x3000\n" \
"v_mfma_f32_16x16x32_bf16 v[132:135], a[8:11], a[148:151], v[132:135]\n" \
"ds_read_b64_tr_b16 a[8:9], v33 offset:36992\n" \
"ds_read_b64_tr_b16 a[10:11], v33 offset:41600\n" \
"v_mfma_f32_16x16x32_bf16 v[128:131], a[12:15], a[136:139], v[128:131]\n" \
"v_mfma_f32_16x16x32_bf16 v[132:135], a[12:15], a[152:155], v[132:135]\n" \
"ds_read_b64_tr_b16 a[12:13], v33 offset:46208\n" \
"ds_read_b64_tr_b16 a[14:15], v33 offset:50816\n" \
"v_mfma_f32_16x16x32_bf16 v[128:131], a[16:19], a[140:143], v[128:131]\n" \
"buffer_load_dwordx4 a[244:247], v32, s[56:59], s95 offen offset:2048 sc1\n" \
"v_mfma_f32_16x16x32_bf16 v[132:135], a[16:19], a[156:159], v[132:135]\n" \
"ds_read_b64_tr_b16 a[16:17], v33 offset:55424\n" \
"ds_read_b64_tr_b16 a[18:19], v33 offset:60032\n" \
"v_mfma_f32_16x16x32_bf16 v[128:131], a[20:23], a[32:35], v[128:131]\n" \
"v_mfma_f32_16x16x32_bf16 v[132:135], a[20:23], a[48:51], v[132:135]\n" \
"ds_read_b64_tr_b16 a[20:21], v10 offset:128\n" \
"s_waitcnt lgkmcnt(13)\n" \
"v_mfma_f32_16x16x32_bf16 v[128:131], a[24:27], a[36:39], v[128:131]\n" \
"ds_read_b64_tr_b16 a[22:23], v10 offset:4736\n" \
"v_mfma_f32_16x16x32_bf16 v[132:135], a[24:27], a[52:55], v[132:135]\n" \
"buffer_load_dwordx4 a[232:235], v20, s[56:59], s95 offen offset:3072 sc1\n" \
"v_mfma_f32_16x16x32_bf16 v[128:131], a[28:31], a[40:43], v[128:131]\n" \
"ds_read_b64_tr_b16 a[24:25], v34 offset:128\n" \
"ds_read_b64_tr_b16 a[26:27], v34 offset:4736\n" \
"v_mfma_f32_16x16x32_bf16 v[132:135], a[28:31], a[56:59], v[132:135]\n" \
"ds_read_b64_tr_b16 a[28:29], v34 offset:9344\n" \
"ds_read_b64_tr_b16 a[30:31], v34 offset:13952\n" \
"v_mfma_f32_16x16x32_bf16 v[128:131], a[192:195], a[44:47], v[128:131]\n" \
"v_mfma_f32_16x16x32_bf16 v[132:135], a[192:195], a[60:63], v[132:135]\n" \
"ds_read_b64_tr_b16 a[192:193], v34 offset:18560\n" \
"ds_read_b64_tr_b16 a[194:195], v34 offset:23168\n" \
"v_mfma_f32_16x16x32_bf16 v[136:139], a[196:199], a[128:131], v[136:139]\n" \
"buffer_load_dwordx4 a[248:251], v32, s[56:59], s95 offen offset:3072 sc1\n" \
"v_mfma_f32_16x16x32_bf16 v[140:143], a[196:199], a[144:147], v[140:143]\n" \
"ds_read_b64_tr_b16 a[196:197], v10 offset:9344\n" \
"ds_read_b64_tr_b16 a[198:199], v10 offset:13952\n" \
"v_cvt_pk_bf16_f32 v128, v128, v129\n" \
"v_cvt_pk_bf16_f32 v129, v130, v131\n" \
"v_cvt_pk_bf16_f32 v130, v132, v133\n" \
"v_cvt_pk_bf16_f32 v131, v134, v135\n" \
"buffer_store_dwordx4 v[128:131], v20, s[56:59], s69 offen offset:0 sc1\n" \
"s_add_u32 s69, s69, 0x2800\n" \
"v_mfma_f32_16x16x32_bf16 v[136:139], a[200:203], a[132:135], v[136:139]\n" \
"v_readlane_b32 s95, v11, 46\n" \
"v_mfma_f32_16x16x32_bf16 v[140:143], a[200:203], a[148:151], v[140:143]\n" \
"ds_read_b64_tr_b16 a[200:201], v33 offset:37024\n" \
"ds_read_b64_tr_b16 a[202:203], v33 offset:41632\n" \
"v_mfma_f32_16x16x32_bf16 v[136:139], a[204:207], a[136:139], v[136:139]\n" \
"v_mfma_f32_16x16x32_bf16 v[140:143], a[204:207], a[152:155], v[140:143]\n" \
"ds_read_b64_tr_b16 a[204:205], v33 offset:46240\n" \
"ds_read_b64_tr_b16 a[206:207], v33 offset:50848\n" \
"v_mfma_f32_16x16x32_bf16 v[136:139], a[208:211], a[140:143], v[136:139]\n" \
"buffer_load_dwordx4 v[64:67], v20, s[56:59], s97 offen offset:0 sc1\n" \
"v_mfma_f32_16x16x32_bf16 v[140:143], a[208:211], a[156:159], v[140:143]\n" \
"ds_read_b64_tr_b16 a[208:209], v33 offset:55456\n" \
"ds_read_b64_tr_b16 a[210:211], v33 offset:60064\n" \
"v_mfma_f32_16x16x32_bf16 v[136:139], a[212:215], a[32:35], v[136:139]\n" \
"v_mfma_f32_16x16x32_bf16 v[140:143], a[212:215], a[48:51], v[140:143]\n" \
"ds_read_b64_tr_b16 a[212:213], v10 offset:160\n" \
"s_waitcnt lgkmcnt(12)\n" \
"v_mfma_f32_16x16x32_bf16 v[136:139], a[216:219], a[36:39], v[136:139]\n" \
"ds_read_b64_tr_b16 a[214:215], v10 offset:4768\n" \
"v_mfma_f32_16x16x32_bf16 v[140:143], a[216:219], a[52:55], v[140:143]\n" \
"ds_read_b64_tr_b16 a[216:217], v34 offset:160\n" \
"ds_read_b64_tr_b16 a[218:219], v34 offset:4768\n" \
"v_mfma_f32_16x16x32_bf16 v[136:139], a[0:3], a[40:43], v[136:139]\n" \
"buffer_load_dwordx4 v[68:71], v20, s[56:59], s97 offen offset:1024 sc1\n" \
"v_mfma_f32_16x16x32_bf16 v[140:143], a[0:3], a[56:59], v[140:143]\n" \
"ds_read_b64_tr_b16 a[0:1], v34 offset:9376\n" \
"ds_read_b64_tr_b16 a[2:3], v34 offset:13984\n" \
"v_mfma_f32_16x16x32_bf16 v[136:139], a[4:7], a[44:47], v[136:139]\n" \
"v_mfma_f32_16x16x32_bf16 v[140:143], a[4:7], a[60:63], v[140:143]\n" \
"ds_read_b64_tr_b16 a[4:5], v34 offset:18592\n" \
"ds_read_b64_tr_b16 a[6:7], v34 offset:23200\n" \
"v_mfma_f32_16x16x32_bf16 v[144:147], a[8:11], a[128:131], v[144:147]\n" \
"v_mfma_f32_16x16x32_bf16 v[148:151], a[8:11], a[144:147], v[148:151]\n" \
"ds_read_b64_tr_b16 a[8:9], v10 offset:9376\n" \
"ds_read_b64_tr_b16 a[10:11], v10 offset:13984\n" \
"v_mfma_f32_16x16x32_bf16 v[144:147], a[12:15], a[132:135], v[144:147]\n" \
"v_cvt_pk_bf16_f32 v136, v136, v137\n" \
"v_cvt_pk_bf16_f32 v137, v138, v139\n" \
"v_cvt_pk_bf16_f32 v138, v140, v141\n" \
"v_cvt_pk_bf16_f32 v139, v142, v143\n" \
"buffer_store_dwordx4 v[136:139], v20, s[56:59], s70 offen offset:0 sc1\n" \
"s_add_u32 s70, s70, 0x2000\n" \
"v_mfma_f32_16x16x32_bf16 v[148:151], a[12:15], a[148:151], v[148:151]\n" \
"ds_read_b64_tr_b16 a[12:13], v33 offset:37056\n" \
"ds_read_b64_tr_b16 a[14:15], v33 offset:41664\n" \
"v_mfma_f32_16x16x32_bf16 v[144:147], a[16:19], a[136:139], v[144:147]\n" \
"v_mfma_f32_16x16x32_bf16 v[148:151], a[16:19], a[152:155], v[148:151]\n" \
"ds_read_b64_tr_b16 a[16:17], v33 offset:46272\n" \
"ds_read_b64_tr_b16 a[18:19], v33 offset:50880\n" \
"v_mfma_f32_16x16x32_bf16 v[144:147], a[20:23], a[140:143], v[144:147]\n" \
"buffer_load_dwordx4 v[72:75], v32, s[56:59], s97 offen offset:0 sc1\n" \
"v_mfma_f32_16x16x32_bf16 v[148:151], a[20:23], a[156:159], v[148:151]\n" \
"ds_read_b64_tr_b16 a[20:21], v33 offset:55488\n" \
"ds_read_b64_tr_b16 a[22:23], v33 offset:60096\n" \
"v_mfma_f32_16x16x32_bf16 v[144:147], a[24:27], a[32:35], v[144:147]\n" \
"v_mfma_f32_16x16x32_bf16 v[148:151], a[24:27], a[48:51], v[148:151]\n" \
"ds_read_b64_tr_b16 a[24:25], v10 offset:192\n" \
"s_waitcnt lgkmcnt(13)\n" \
"v_mfma_f32_16x16x32_bf16 v[144:147], a[28:31], a[36:39], v[144:147]\n" \
"ds_read_b64_tr_b16 a[26:27], v10 offset:4800\n" \
"v_mfma_f32_16x16x32_bf16 v[148:151], a[28:31], a[52:55], v[148:151]\n" \
"ds_read_b64_tr_b16 a[28:29], v34 offset:192\n" \
"ds_read_b64_tr_b16 a[30:31], v34 offset:4800\n" \
"v_mfma_f32_16x16x32_bf16 v[144:147], a[192:195], a[40:43], v[144:147]\n" \
"buffer_load_dwordx4 v[76:79], v32, s[56:59], s97 offen offset:1024 sc1\n" \
"v_mfma_f32_16x16x32_bf16 v[148:151], a[192:195], a[56:59], v[148:151]\n" \
"ds_read_b64_tr_b16 a[192:193], v34 offset:9408\n" \
"ds_read_b64_tr_b16 a[194:195], v34 offset:14016\n" \
"v_mfma_f32_16x16x32_bf16 v[144:147], a[196:199], a[44:47], v[144:147]\n" \
"v_readlane_b32 s97, v11, 48\n" \
"v_mfma_f32_16x16x32_bf16 v[148:151], a[196:199], a[60:63], v[148:151]\n" \
"ds_read_b64_tr_b16 a[196:197], v34 offset:18624\n" \
"ds_read_b64_tr_b16 a[198:199], v34 offset:23232\n" \
"v_mfma_f32_16x16x32_bf16 v[152:155], a[200:203], a[128:131], v[152:155]\n" \
"v_mfma_f32_16x16x32_bf16 v[156:159], a[200:203], a[144:147], v[156:159]\n" \
"ds_read_b64_tr_b16 a[200:201], v10 offset:9408\n" \
"ds_read_b64_tr_b16 a[202:203], v10 offset:14016\n" \
"v_cvt_pk_bf16_f32 v144, v144, v145\n" \
"v_cvt_pk_bf16_f32 v145, v146, v147\n" \
"v_cvt_pk_bf16_f32 v146, v148, v149\n" \
"v_cvt_pk_bf16_f32 v147, v150, v151\n" \
"buffer_store_dwordx4 v[144:147], v20, s[56:59], s71 offen offset:0 sc1\n" \
"s_add_u32 s71, s71, 0x1800\n" \
"v_mfma_f32_16x16x32_bf16 v[152:155], a[204:207], a[132:135], v[152:155]\n" \
"v_mfma_f32_16x16x32_bf16 v[156:159], a[204:207], a[148:151], v[156:159]\n" \
"ds_read_b64_tr_b16 a[204:205], v33 offset:256\n" \
"ds_read_b64_tr_b16 a[206:207], v33 offset:4864\n" \
"v_mfma_f32_16x16x32_bf16 v[152:155], a[208:211], a[136:139], v[152:155]\n" \
"v_mfma_f32_16x16x32_bf16 v[156:159], a[208:211], a[152:155], v[156:159]\n" \
"ds_read_b64_tr_b16 a[208:209], v33 offset:9472\n" \
"ds_read_b64_tr_b16 a[210:211], v33 offset:14080\n" \
"v_mfma_f32_16x16x32_bf16 v[152:155], a[212:215], a[140:143], v[152:155]\n" \
"buffer_load_dwordx4 v[80:83], v20, s[56:59], s64 offen offset:0 sc1\n" \
"v_mfma_f32_16x16x32_bf16 v[156:159], a[212:215], a[156:159], v[156:159]\n" \
"ds_read_b64_tr_b16 a[212:213], v33 offset:18688\n" \
"ds_read_b64_tr_b16 a[214:215], v33 offset:23296\n" \
"v_mfma_f32_16x16x32_bf16 v[152:155], a[216:219], a[32:35], v[152:155]\n" \
"v_mfma_f32_16x16x32_bf16 v[156:159], a[216:219], a[48:51], v[156:159]\n" \
"ds_read_b64_tr_b16 a[216:217], v33 offset:27904\n" \
"s_waitcnt lgkmcnt(12)\n" \
"v_mfma_f32_16x16x32_bf16 v[152:155], a[0:3], a[36:39], v[152:155]\n" \
"ds_read_b64_tr_b16 a[218:219], v33 offset:32512\n" \
"v_mfma_f32_16x16x32_bf16 v[156:159], a[0:3], a[52:55], v[156:159]\n" \
"ds_read_b64_tr_b16 a[0:1], v33 offset:37120\n" \
"ds_read_b64_tr_b16 a[2:3], v33 offset:41728\n" \
"v_mfma_f32_16x16x32_bf16 v[152:155], a[4:7], a[40:43], v[152:155]\n" \
"buffer_load_dwordx4 v[84:87], v20, s[56:59], s64 offen offset:1024 sc1\n" \
"v_mfma_f32_16x16x32_bf16 v[156:159], a[4:7], a[56:59], v[156:159]\n" \
"ds_read_b64_tr_b16 a[4:5], v33 offset:46336\n" \
"ds_read_b64_tr_b16 a[6:7], v33 offset:50944\n" \
"v_mfma_f32_16x16x32_bf16 v[152:155], a[8:11], a[44:47], v[152:155]\n" \
"v_mfma_f32_16x16x32_bf16 v[156:159], a[8:11], a[60:63], v[156:159]\n" \
"ds_read_b64_tr_b16 a[8:9], v33 offset:55552\n" \
"ds_read_b64_tr_b16 a[10:11], v33 offset:60160\n" \
"v_mfma_f32_16x16x32_bf16 v[160:163], a[12:15], a[128:131], v[160:163]\n" \
"v_mfma_f32_16x16x32_bf16 v[164:167], a[12:15], a[144:147], v[164:167]\n" \
"ds_read_b64_tr_b16 a[12:13], v10 offset:256\n" \
"ds_read_b64_tr_b16 a[14:15], v10 offset:4864\n" \
"v_mfma_f32_16x16x32_bf16 v[160:163], a[16:19], a[132:135], v[160:163]\n" \
"v_cvt_pk_bf16_f32 v152, v152, v153\n" \
"v_cvt_pk_bf16_f32 v153, v154, v155\n" \
"v_cvt_pk_bf16_f32 v154, v156, v157\n" \
"v_cvt_pk_bf16_f32 v155, v158, v159\n" \
"buffer_store_dwordx4 v[152:155], v20, s[56:59], s72 offen offset:0 sc1\n" \
"s_add_u32 s72, s72, 0x1000\n" \
"v_mfma_f32_16x16x32_bf16 v[164:167], a[16:19], a[148:151], v[164:167]\n" \
"ds_read_b64_tr_b16 a[16:17], v34 offset:256\n" \
"ds_read_b64_tr_b16 a[18:19], v34 offset:4864\n" \
"v_mfma_f32_16x16x32_bf16 v[160:163], a[20:23], a[136:139], v[160:163]\n" \
"v_mfma_f32_16x16x32_bf16 v[164:167], a[20:23], a[152:155], v[164:167]\n" \
"ds_read_b64_tr_b16 a[20:21], v34 offset:9472\n" \
"ds_read_b64_tr_b16 a[22:23], v34 offset:14080\n" \
"v_mfma_f32_16x16x32_bf16 v[160:163], a[24:27], a[140:143], v[160:163]\n" \
"buffer_load_dwordx4 v[88:91], v32, s[56:59], s64 offen offset:0 sc1\n" \
"v_mfma_f32_16x16x32_bf16 v[164:167], a[24:27], a[156:159], v[164:167]\n" \
"ds_read_b64_tr_b16 a[24:25], v34 offset:18688\n" \
"ds_read_b64_tr_b16 a[26:27], v34 offset:23296\n" \
"v_mfma_f32_16x16x32_bf16 v[160:163], a[28:31], a[32:35], v[160:163]\n" \
"v_mfma_f32_16x16x32_bf16 v[164:167], a[28:31], a[48:51], v[164:167]\n" \
"ds_read_b64_tr_b16 a[28:29], v10 offset:9472\n" \
"s_waitcnt vmcnt(17)\n" \
"s_mov_b64 exec, 1\n" \
"buffer_store_dword v39, off, s[56:59], s67 offset:0 sc1\n" \
"s_mov_b64 exec, -1\n" \
"s_waitcnt lgkmcnt(11)\n" \
"v_mfma_f32_16x16x32_bf16 v[160:163], a[192:195], a[36:39], v[160:163]\n" \
"ds_read_b64_tr_b16 a[30:31], v10 offset:14080\n" \
"v_mfma_f32_16x16x32_bf16 v[164:167], a[192:195], a[52:55], v[164:167]\n" \
"ds_read_b64_tr_b16 a[192:193], v33 offset:0\n" \
"ds_read_b64_tr_b16 a[194:195], v33 offset:4608\n" \
"v_mfma_f32_16x16x32_bf16 v[160:163], a[196:199], a[40:43], v[160:163]\n" \
"v_mfma_f32_16x16x32_bf16 v[164:167], a[196:199], a[56:59], v[164:167]\n" \
"ds_read_b64_tr_b16 a[196:197], v33 offset:9216\n" \
"ds_read_b64_tr_b16 a[198:199], v33 offset:13824\n" \
"v_mfma_f32_16x16x32_bf16 v[160:163], a[200:203], a[44:47], v[160:163]\n" \
"buffer_load_dwordx4 v[92:95], v32, s[56:59], s64 offen offset:1024 sc1\n" \
"v_mfma_f32_16x16x32_bf16 v[164:167], a[200:203], a[60:63], v[164:167]\n" \
"ds_read_b64_tr_b16 a[200:201], v33 offset:18432\n" \
"ds_read_b64_tr_b16 a[202:203], v33 offset:23040\n" \
"v_mfma_f32_16x16x32_bf16 v[248:251], a[204:207], a[96:99], v[248:251]\n" \
"v_readlane_b32 s64, v11, 49\n" \
"v_mfma_f32_16x16x32_bf16 v[252:255], a[204:207], a[112:115], v[252:255]\n" \
"ds_read_b64_tr_b16 a[204:205], v33 offset:27648\n" \
"v_cvt_pk_bf16_f32 v160, v160, v161\n" \
"v_cvt_pk_bf16_f32 v161, v162, v163\n" \
"v_cvt_pk_bf16_f32 v162, v164, v165\n" \
"v_cvt_pk_bf16_f32 v163, v166, v167\n" \
"buffer_store_dwordx4 v[160:163], v20, s[56:59], s73 offen offset:0 sc1\n" \
"s_add_u32 s73, s73, 0x800\n" \
"v_mfma_f32_16x16x32_bf16 v[248:251], a[208:211], a[100:103], v[248:251]\n" \
"ds_read_b64_tr_b16 a[206:207], v33 offset:32256\n" \
"v_mfma_f32_16x16x32_bf16 v[252:255], a[208:211], a[116:119], v[252:255]\n" \
"ds_read_b64_tr_b16 a[208:209], v33 offset:36864\n" \
"ds_read_b64_tr_b16 a[210:211], v33 offset:41472\n" \
"v_mfma_f32_16x16x32_bf16 v[248:251], a[212:215], a[104:107], v[248:251]\n" \
"v_mfma_f32_16x16x32_bf16 v[252:255], a[212:215], a[120:123], v[252:255]\n" \
"ds_read_b64_tr_b16 a[212:213], v33 offset:46080\n" \
"ds_read_b64_tr_b16 a[214:215], v33 offset:50688\n" \
"v_mfma_f32_16x16x32_bf16 v[248:251], a[216:219], a[108:111], v[248:251]\n" \
"buffer_load_dwordx4 v[96:99], v20, s[56:59], s65 offen offset:0 sc1\n" \
"v_mfma_f32_16x16x32_bf16 v[252:255], a[216:219], a[124:127], v[252:255]\n" \
"ds_read_b64_tr_b16 a[216:217], v33 offset:55296\n" \
"ds_read_b64_tr_b16 a[218:219], v33 offset:59904\n" \
"s_waitcnt vmcnt(18)\n" \
"s_mov_b64 exec, 1\n" \
"buffer_store_dword v39, off, s[56:59], s68 offset:4 sc1\n" \
"s_mov_b64 exec, -1\n" \
"v_mfma_f32_16x16x32_bf16 v[176:179], a[0:3], a[128:131], v[176:179]\n" \
"v_mfma_f32_16x16x32_bf16 v[180:183], a[0:3], a[144:147], v[180:183]\n" \
"v_mfma_f32_16x16x32_bf16 v[248:251], a[0:3], a[160:163], v[248:251]\n" \
"v_mfma_f32_16x16x32_bf16 v[252:255], a[0:3], a[176:179], v[252:255]\n" \
"ds_read_b64_tr_b16 a[0:1], v10 offset:0\n" \
"ds_read_b64_tr_b16 a[2:3], v10 offset:4608\n" \
"v_mfma_f32_16x16x32_bf16 v[176:179], a[4:7], a[132:135], v[176:179]\n" \
"buffer_load_dwordx4 v[100:103], v20, s[56:59], s65 offen offset:1024 sc1\n" \
"v_mfma_f32_16x16x32_bf16 v[180:183], a[4:7], a[148:151], v[180:183]\n" \
"v_mfma_f32_16x16x32_bf16 v[248:251], a[4:7], a[164:167], v[248:251]\n" \
"v_mfma_f32_16x16x32_bf16 v[252:255], a[4:7], a[180:183], v[252:255]\n" \
"ds_read_b64_tr_b16 a[4:5], v34 offset:0\n" \
"s_waitcnt lgkmcnt(9)\n" \
"v_mfma_f32_16x16x32_bf16 v[176:179], a[8:11], a[136:139], v[176:179]\n" \
"ds_read_b64_tr_b16 a[6:7], v34 offset:4608\n" \
"v_mfma_f32_16x16x32_bf16 v[180:183], a[8:11], a[152:155], v[180:183]\n" \
"buffer_load_dwordx4 v[104:107], v32, s[56:59], s65 offen offset:0 sc1\n" \
"v_mfma_f32_16x16x32_bf16 v[248:251], a[8:11], a[168:171], v[248:251]\n" \
"v_mfma_f32_16x16x32_bf16 v[252:255], a[8:11], a[184:187], v[252:255]\n" \
"ds_read_b64_tr_b16 a[8:9], v34 offset:9216\n" \
"ds_read_b64_tr_b16 a[10:11], v34 offset:13824\n" \
"v_mfma_f32_16x16x32_bf16 v[176:179], a[12:15], a[140:143], v[176:179]\n" \
"v_mfma_f32_16x16x32_bf16 v[180:183], a[12:15], a[156:159], v[180:183]\n" \
"v_mfma_f32_16x16x32_bf16 v[248:251], a[12:15], a[172:175], v[248:251]\n" \
"buffer_load_dwordx4 v[108:111], v32, s[56:59], s65 offen offset:1024 sc1\n" \
"v_mfma_f32_16x16x32_bf16 v[252:255], a[12:15], a[188:191], v[252:255]\n" \
"ds_read_b64_tr_b16 a[12:13], v34 offset:18432\n" \
"ds_read_b64_tr_b16 a[14:15], v34 offset:23040\n" \
"s_waitcnt vmcnt(18)\n" \
"s_mov_b64 exec, 1\n" \
"buffer_store_dword v39, off, s[56:59], s69 offset:8 sc1\n" \
"s_mov_b64 exec, -1\n" \
"v_mfma_f32_16x16x32_bf16 v[176:179], a[16:19], a[32:35], v[176:179]\n" \
"v_mfma_f32_16x16x32_bf16 v[180:183], a[16:19], a[48:51], v[180:183]\n" \
"v_mfma_f32_16x16x32_bf16 v[248:251], a[16:19], a[220:223], v[248:251]\n" \
"v_mfma_f32_16x16x32_bf16 v[252:255], a[16:19], a[236:239], v[252:255]\n" \
"ds_read_b64_tr_b16 a[16:17], v10 offset:9216\n" \
"ds_read_b64_tr_b16 a[18:19], v10 offset:13824\n" \
"v_mfma_f32_16x16x32_bf16 v[176:179], a[20:23], a[36:39], v[176:179]\n" \
"v_mfma_f32_16x16x32_bf16 v[180:183], a[20:23], a[52:55], v[180:183]\n" \
"v_mfma_f32_16x16x32_bf16 v[248:251], a[20:23], a[224:227], v[248:251]\n" \
"v_mfma_f32_16x16x32_bf16 v[252:255], a[20:23], a[240:243], v[252:255]\n" \
"ds_read_b64_tr_b16 a[20:21], v33 offset:32\n" \
"ds_read_b64_tr_b16 a[22:23], v33 offset:4640\n" \
"v_mfma_f32_16x16x32_bf16 v[176:179], a[24:27], a[40:43], v[176:179]\n" \
"v_mfma_f32_16x16x32_bf16 v[180:183], a[24:27], a[56:59], v[180:183]\n" \
"v_mfma_f32_16x16x32_bf16 v[248:251], a[24:27], a[228:231], v[248:251]\n" \
"v_mfma_f32_16x16x32_bf16 v[252:255], a[24:27], a[244:247], v[252:255]\n" \
"ds_read_b64_tr_b16 a[24:25], v33 offset:9248\n" \
"ds_read_b64_tr_b16 a[26:27], v33 offset:13856\n" \
"v_mfma_f32_16x16x32_bf16 v[176:179], a[28:31], a[44:47], v[176:179]\n" \
"v_mfma_f32_16x16x32_bf16 v[180:183], a[28:31], a[60:63], v[180:183]\n" \
"v_mfma_f32_16x16x32_bf16 v[248:251], a[28:31], a[232:235], v[248:251]\n" \
"v_mfma_f32_16x16x32_bf16 v[252:255], a[28:31], a[248:251], v[252:255]\n" \
"ds_read_b64_tr_b16 a[28:29], v33 offset:18464\n" \
"ds_read_b64_tr_b16 a[30:31], v33 offset:23072\n" \
"v_mfma_f32_16x16x32_bf16 v[184:187], a[192:195], a[96:99], v[184:187]\n" \
"s_waitcnt vmcnt(16)\n" \
"s_mov_b64 exec, 1\n" \
"buffer_store_dword v39, off, s[56:59], s70 offset:12 sc1\n" \
"s_mov_b64 exec, -1\n" \
"v_cvt_pk_bf16_f32 v176, v176, v177\n" \
"v_cvt_pk_bf16_f32 v248, v248, v249\n" \
"v_cvt_pk_bf16_f32 v177, v178, v179\n" \
"v_cvt_pk_bf16_f32 v249, v250, v251\n" \
"v_cvt_pk_bf16_f32 v178, v180, v181\n" \
"v_cvt_pk_bf16_f32 v179, v182, v183\n" \
"v_cvt_pk_bf16_f32 v250, v252, v253\n" \
"v_cvt_pk_bf16_f32 v251, v254, v255\n" \
"buffer_store_dwordx4 v[176:179], v20, s[56:59], s92 offen offset:0 sc1\n" \
"v_mfma_f32_16x16x32_bf16 v[188:191], a[192:195], a[112:115], v[188:191]\n" \
"ds_read_b64_tr_b16 a[192:193], v33 offset:27680\n" \
"ds_read_b64_tr_b16 a[194:195], v33 offset:32288\n" \
"v_mfma_f32_16x16x32_bf16 v[184:187], a[196:199], a[100:103], v[184:187]\n" \
"v_mfma_f32_16x16x32_bf16 v[188:191], a[196:199], a[116:119], v[188:191]\n" \
"buffer_store_dwordx4 v[248:251], v20, s[56:59], s92 offen offset:1024 sc1\n" \
"v_mfma_f32_16x16x32_bf16 v[184:187], a[200:203], a[104:107], v[184:187]\n" \
"v_readlane_b32 s92, v11, 51\n" \
"ds_read_b64_tr_b16 a[196:197], v33 offset:36896\n" \
"v_mfma_f32_16x16x32_bf16 v[188:191], a[200:203], a[120:123], v[188:191]\n" \
"ds_read_b64_tr_b16 a[198:199], v33 offset:41504\n" \
"ds_read_b64_tr_b16 a[200:201], v33 offset:46112\n" \
"v_mfma_f32_16x16x32_bf16 v[184:187], a[204:207], a[108:111], v[184:187]\n" \
"ds_read_b64_tr_b16 a[202:203], v33 offset:50720\n" \
"v_mfma_f32_16x16x32_bf16 v[188:191], a[204:207], a[124:127], v[188:191]\n" \
"ds_read_b64_tr_b16 a[204:205], v33 offset:55328\n" \
"s_waitcnt lgkmcnt(9)\n" \
"v_mfma_f32_16x16x32_bf16 v[184:187], a[208:211], a[160:163], v[184:187]\n" \
"ds_read_b64_tr_b16 a[206:207], v33 offset:59936\n" \
"v_mfma_f32_16x16x32_bf16 v[188:191], a[208:211], a[176:179], v[188:191]\n" \
"ds_read_b64_tr_b16 a[208:209], v10 offset:32\n" \
"ds_read_b64_tr_b16 a[210:211], v10 offset:4640\n" \
"v_mfma_f32_16x16x32_bf16 v[184:187], a[212:215], a[164:167], v[184:187]\n" \
"v_mfma_f32_16x16x32_bf16 v[188:191], a[212:215], a[180:183], v[188:191]\n" \
"ds_read_b64_tr_b16 a[212:213], v34 offset:32\n" \
"ds_read_b64_tr_b16 a[214:215], v34 offset:4640\n" \
"v_mfma_f32_16x16x32_bf16 v[184:187], a[216:219], a[168:171], v[184:187]\n" \
"v_mfma_f32_16x16x32_bf16 v[188:191], a[216:219], a[184:187], v[188:191]\n" \
"ds_read_b64_tr_b16 a[216:217], v34 offset:9248\n" \
"ds_read_b64_tr_b16 a[218:219], v34 offset:13856\n" \
"s_waitcnt vmcnt(16)\n" \
"s_mov_b64 exec, 1\n" \
"buffer_store_dword v39, off, s[56:59], s71 offset:16 sc1\n" \
"s_mov_b64 exec, -1\n" \
"v_mfma_f32_16x16x32_bf16 v[184:187], a[0:3], a[172:175], v[184:187]\n" \
"v_mfma_f32_16x16x32_bf16 v[188:191], a[0:3], a[188:191], v[188:191]\n" \
"ds_read_b64_tr_b16 a[0:1], v34 offset:18464\n" \
"ds_read_b64_tr_b16 a[2:3], v34 offset:23072\n" \
"v_mfma_f32_16x16x32_bf16 v[184:187], a[4:7], a[220:223], v[184:187]\n" \
"v_mfma_f32_16x16x32_bf16 v[188:191], a[4:7], a[236:239], v[188:191]\n" \
"ds_read_b64_tr_b16 a[4:5], v10 offset:9248\n" \
"ds_read_b64_tr_b16 a[6:7], v10 offset:13856\n" \
"v_mfma_f32_16x16x32_bf16 v[184:187], a[8:11], a[224:227], v[184:187]\n" \
"v_mfma_f32_16x16x32_bf16 v[188:191], a[8:11], a[240:243], v[188:191]\n" \
"ds_read_b64_tr_b16 a[8:9], v33 offset:64\n" \
"ds_read_b64_tr_b16 a[10:11], v33 offset:4672\n" \
"v_mfma_f32_16x16x32_bf16 v[184:187], a[12:15], a[228:231], v[184:187]\n" \
"v_mfma_f32_16x16x32_bf16 v[188:191], a[12:15], a[244:247], v[188:191]\n" \
"ds_read_b64_tr_b16 a[12:13], v33 offset:9280\n" \
"ds_read_b64_tr_b16 a[14:15], v33 offset:13888\n" \
"v_mfma_f32_16x16x32_bf16 v[184:187], a[16:19], a[232:235], v[184:187]\n" \
"v_mfma_f32_16x16x32_bf16 v[188:191], a[16:19], a[248:251], v[188:191]\n" \
"ds_read_b64_tr_b16 a[16:17], v33 offset:18496\n" \
"ds_read_b64_tr_b16 a[18:19], v33 offset:23104\n" \
"v_mfma_f32_16x16x32_bf16 v[192:195], a[20:23], a[96:99], v[192:195]\n" \
"v_mfma_f32_16x16x32_bf16 v[196:199], a[20:23], a[112:115], v[196:199]\n" \
"ds_read_b64_tr_b16 a[20:21], v33 offset:27712\n" \
"ds_read_b64_tr_b16 a[22:23], v33 offset:32320\n" \
"v_mfma_f32_16x16x32_bf16 v[192:195], a[24:27], a[100:103], v[192:195]\n" \
"v_cvt_pk_bf16_f32 v184, v184, v185\n" \
"v_cvt_pk_bf16_f32 v185, v186, v187\n" \
"v_cvt_pk_bf16_f32 v186, v188, v189\n" \
"v_cvt_pk_bf16_f32 v187, v190, v191\n" \
"buffer_store_dwordx4 v[184:187], v20, s[56:59], s94 offen offset:1024 sc1\n" \
"s_add_u32 s94, s94, 0x3800\n" \
"v_mfma_f32_16x16x32_bf16 v[196:199], a[24:27], a[116:119], v[196:199]\n" \
"ds_read_b64_tr_b16 a[24:25], v33 offset:36928\n" \
"s_waitcnt lgkmcnt(13)\n" \
"v_mfma_f32_16x16x32_bf16 v[192:195], a[28:31], a[104:107], v[192:195]\n" \
"ds_read_b64_tr_b16 a[26:27], v33 offset:41536\n" \
"v_mfma_f32_16x16x32_bf16 v[196:199], a[28:31], a[120:123], v[196:199]\n" \
"ds_read_b64_tr_b16 a[28:29], v33 offset:46144\n" \
"ds_read_b64_tr_b16 a[30:31], v33 offset:50752\n" \
"s_waitcnt vmcnt(15)\n" \
"s_mov_b64 exec, 1\n" \
"buffer_store_dword v39, off, s[56:59], s72 offset:20 sc1\n" \
"s_mov_b64 exec, -1\n" \
"v_mfma_f32_16x16x32_bf16 v[192:195], a[192:195], a[108:111], v[192:195]\n" \
"v_readlane_b32 s72, v11, 41\n" \
"v_mfma_f32_16x16x32_bf16 v[196:199], a[192:195], a[124:127], v[196:199]\n" \
"ds_read_b64_tr_b16 a[192:193], v33 offset:55360\n" \
"ds_read_b64_tr_b16 a[194:195], v33 offset:59968\n" \
"v_mfma_f32_16x16x32_bf16 v[192:195], a[196:199], a[160:163], v[192:195]\n" \
"v_mfma_f32_16x16x32_bf16 v[196:199], a[196:199], a[176:179], v[196:199]\n" \
"ds_read_b64_tr_b16 a[196:197], v10 offset:64\n" \
"ds_read_b64_tr_b16 a[198:199], v10 offset:4672\n" \
"v_mfma_f32_16x16x32_bf16 v[192:195], a[200:203], a[164:167], v[192:195]\n" \
"v_mfma_f32_16x16x32_bf16 v[196:199], a[200:203], a[180:183], v[196:199]\n" \
"ds_read_b64_tr_b16 a[200:201], v34 offset:64\n" \
"ds_read_b64_tr_b16 a[202:203], v34 offset:4672\n" \
"v_mfma_f32_16x16x32_bf16 v[192:195], a[204:207], a[168:171], v[192:195]\n" \
"v_mfma_f32_16x16x32_bf16 v[196:199], a[204:207], a[184:187], v[196:199]\n" \
"ds_read_b64_tr_b16 a[204:205], v34 offset:9280\n" \
"ds_read_b64_tr_b16 a[206:207], v34 offset:13888\n" \
"v_mfma_f32_16x16x32_bf16 v[192:195], a[208:211], a[172:175], v[192:195]\n" \
"v_mfma_f32_16x16x32_bf16 v[196:199], a[208:211], a[188:191], v[196:199]\n" \
"ds_read_b64_tr_b16 a[208:209], v34 offset:18496\n" \
"ds_read_b64_tr_b16 a[210:211], v34 offset:23104\n" \
"v_mfma_f32_16x16x32_bf16 v[192:195], a[212:215], a[220:223], v[192:195]\n" \
"v_mfma_f32_16x16x32_bf16 v[196:199], a[212:215], a[236:239], v[196:199]\n" \
"ds_read_b64_tr_b16 a[212:213], v10 offset:9280\n" \
"ds_read_b64_tr_b16 a[214:215], v10 offset:13888\n" \
"v_mfma_f32_16x16x32_bf16 v[192:195], a[216:219], a[224:227], v[192:195]\n" \
"v_mfma_f32_16x16x32_bf16 v[196:199], a[216:219], a[240:243], v[196:199]\n" \
"ds_read_b64_tr_b16 a[216:217], v33 offset:96\n" \
"s_waitcnt lgkmcnt(13)\n" \
"v_mfma_f32_16x16x32_bf16 v[192:195], a[0:3], a[228:231], v[192:195]\n" \
"ds_read_b64_tr_b16 a[218:219], v33 offset:4704\n" \
"v_mfma_f32_16x16x32_bf16 v[196:199], a[0:3], a[244:247], v[196:199]\n" \
"ds_read_b64_tr_b16 a[0:1], v33 offset:9312\n" \
"ds_read_b64_tr_b16 a[2:3], v33 offset:13920\n" \
"s_waitcnt vmcnt(12)\n" \
"s_mov_b64 exec, 1\n" \
"buffer_store_dword v39, off, s[56:59], s73 offset:24 sc1\n" \
"s_mov_b64 exec, -1\n" \
"v_readlane_b32 s73, v11, 50\n" \
"v_mfma_f32_16x16x32_bf16 v[192:195], a[4:7], a[232:235], v[192:195]\n" \
"v_mfma_f32_16x16x32_bf16 v[196:199], a[4:7], a[248:251], v[196:199]\n" \
"ds_read_b64_tr_b16 a[4:5], v33 offset:18528\n" \
"ds_read_b64_tr_b16 a[6:7], v33 offset:23136\n" \
"v_mfma_f32_16x16x32_bf16 v[200:203], a[8:11], a[96:99], v[200:203]\n" \
"v_mfma_f32_16x16x32_bf16 v[204:207], a[8:11], a[112:115], v[204:207]\n" \
"ds_read_b64_tr_b16 a[8:9], v33 offset:27744\n" \
"ds_read_b64_tr_b16 a[10:11], v33 offset:32352\n" \
"v_mfma_f32_16x16x32_bf16 v[200:203], a[12:15], a[100:103], v[200:203]\n" \
"v_cvt_pk_bf16_f32 v192, v192, v193\n" \
"v_cvt_pk_bf16_f32 v193, v194, v195\n" \
"v_cvt_pk_bf16_f32 v194, v196, v197\n" \
"v_cvt_pk_bf16_f32 v195, v198, v199\n" \
"buffer_store_dwordx4 v[192:195], v20, s[56:59], s95 offen offset:1024 sc1\n" \
"s_add_u32 s95, s95, 0x3000\n" \
"v_mfma_f32_16x16x32_bf16 v[204:207], a[12:15], a[116:119], v[204:207]\n" \
"ds_read_b64_tr_b16 a[12:13], v33 offset:36960\n" \
"ds_read_b64_tr_b16 a[14:15], v33 offset:41568\n" \
"v_mfma_f32_16x16x32_bf16 v[200:203], a[16:19], a[104:107], v[200:203]\n" \
"v_mfma_f32_16x16x32_bf16 v[204:207], a[16:19], a[120:123], v[204:207]\n" \
"ds_read_b64_tr_b16 a[16:17], v33 offset:46176\n" \
"ds_read_b64_tr_b16 a[18:19], v33 offset:50784\n" \
"v_mfma_f32_16x16x32_bf16 v[200:203], a[20:23], a[108:111], v[200:203]\n" \
"v_mfma_f32_16x16x32_bf16 v[204:207], a[20:23], a[124:127], v[204:207]\n" \
"ds_read_b64_tr_b16 a[20:21], v33 offset:55392\n" \
"ds_read_b64_tr_b16 a[22:23], v33 offset:60000\n" \
"v_mfma_f32_16x16x32_bf16 v[200:203], a[24:27], a[160:163], v[200:203]\n" \
"v_mfma_f32_16x16x32_bf16 v[204:207], a[24:27], a[176:179], v[204:207]\n" \
"ds_read_b64_tr_b16 a[24:25], v10 offset:96\n" \
"ds_read_b64_tr_b16 a[26:27], v10 offset:4704\n" \
"v_mfma_f32_16x16x32_bf16 v[200:203], a[28:31], a[164:167], v[200:203]\n" \
"v_mfma_f32_16x16x32_bf16 v[204:207], a[28:31], a[180:183], v[204:207]\n" \
"ds_read_b64_tr_b16 a[28:29], v34 offset:96\n" \
"s_waitcnt lgkmcnt(13)\n" \
"v_mfma_f32_16x16x32_bf16 v[200:203], a[192:195], a[168:171], v[200:203]\n" \
"ds_read_b64_tr_b16 a[30:31], v34 offset:4704\n" \
"v_mfma_f32_16x16x32_bf16 v[204:207], a[192:195], a[184:187], v[204:207]\n" \
"ds_read_b64_tr_b16 a[192:193], v34 offset:9312\n" \
"ds_read_b64_tr_b16 a[194:195], v34 offset:13920\n" \
"v_mfma_f32_16x16x32_bf16 v[200:203], a[196:199], a[172:175], v[200:203]\n" \
"v_mfma_f32_16x16x32_bf16 v[204:207], a[196:199], a[188:191], v[204:207]\n" \
"ds_read_b64_tr_b16 a[196:197], v34 offset:18528\n" \
"ds_read_b64_tr_b16 a[198:199], v34 offset:23136\n" \
"v_mfma_f32_16x16x32_bf16 v[200:203], a[200:203], a[220:223], v[200:203]\n" \
"v_mfma_f32_16x16x32_bf16 v[204:207], a[200:203], a[236:239], v[204:207]\n" \
"ds_read_b64_tr_b16 a[200:201], v10 offset:9312\n" \
"ds_read_b64_tr_b16 a[202:203], v10 offset:13920\n" \
"v_mfma_f32_16x16x32_bf16 v[200:203], a[204:207], a[224:227], v[200:203]\n" \
"v_mfma_f32_16x16x32_bf16 v[204:207], a[204:207], a[240:243], v[204:207]\n" \
"ds_read_b64_tr_b16 a[204:205], v33 offset:128\n" \
"ds_read_b64_tr_b16 a[206:207], v33 offset:4736\n" \
"v_mfma_f32_16x16x32_bf16 v[200:203], a[208:211], a[228:231], v[200:203]\n" \
"v_mfma_f32_16x16x32_bf16 v[204:207], a[208:211], a[244:247], v[204:207]\n" \
"ds_read_b64_tr_b16 a[208:209], v33 offset:9344\n" \
"ds_read_b64_tr_b16 a[210:211], v33 offset:13952\n" \
"v_mfma_f32_16x16x32_bf16 v[200:203], a[212:215], a[232:235], v[200:203]\n" \
"v_mfma_f32_16x16x32_bf16 v[204:207], a[212:215], a[248:251], v[204:207]\n" \
"ds_read_b64_tr_b16 a[212:213], v33 offset:18560\n" \
"ds_read_b64_tr_b16 a[214:215], v33 offset:23168\n" \
"v_mfma_f32_16x16x32_bf16 v[208:211], a[216:219], a[96:99], v[208:211]\n" \
"v_mfma_f32_16x16x32_bf16 v[212:215], a[216:219], a[112:115], v[212:215]\n" \
"ds_read_b64_tr_b16 a[216:217], v33 offset:27776\n" \
"ds_read_b64_tr_b16 a[218:219], v33 offset:32384\n" \
"v_mfma_f32_16x16x32_bf16 v[208:211], a[0:3], a[100:103], v[208:211]\n" \
"v_cvt_pk_bf16_f32 v200, v200, v201\n" \
"v_cvt_pk_bf16_f32 v201, v202, v203\n" \
"v_cvt_pk_bf16_f32 v202, v204, v205\n" \
"v_cvt_pk_bf16_f32 v203, v206, v207\n" \
"buffer_store_dwordx4 v[200:203], v20, s[56:59], s96 offen offset:1024 sc1\n" \
"s_add_u32 s96, s96, 0x2800\n" \
"v_mfma_f32_16x16x32_bf16 v[212:215], a[0:3], a[116:119], v[212:215]\n" \
"ds_read_b64_tr_b16 a[0:1], v33 offset:36992\n" \
"s_waitcnt lgkmcnt(13)\n" \
"v_mfma_f32_16x16x32_bf16 v[208:211], a[4:7], a[104:107], v[208:211]\n" \
"ds_read_b64_tr_b16 a[2:3], v33 offset:41600\n" \
"v_mfma_f32_16x16x32_bf16 v[212:215], a[4:7], a[120:123], v[212:215]\n" \
"ds_read_b64_tr_b16 a[4:5], v33 offset:46208\n" \
"ds_read_b64_tr_b16 a[6:7], v33 offset:50816\n" \
"v_mfma_f32_16x16x32_bf16 v[208:211], a[8:11], a[108:111], v[208:211]\n" \
"v_mfma_f32_16x16x32_bf16 v[212:215], a[8:11], a[124:127], v[212:215]\n" \
"ds_read_b64_tr_b16 a[8:9], v33 offset:55424\n" \
"ds_read_b64_tr_b16 a[10:11], v33 offset:60032\n" \
"v_mfma_f32_16x16x32_bf16 v[208:211], a[12:15], a[160:163], v[208:211]\n" \
"v_mfma_f32_16x16x32_bf16 v[212:215], a[12:15], a[176:179], v[212:215]\n" \
"ds_read_b64_tr_b16 a[12:13], v10 offset:128\n" \
"ds_read_b64_tr_b16 a[14:15], v10 offset:4736\n" \
"v_mfma_f32_16x16x32_bf16 v[208:211], a[16:19], a[164:167], v[208:211]\n" \
"v_mfma_f32_16x16x32_bf16 v[212:215], a[16:19], a[180:183], v[212:215]\n" \
"ds_read_b64_tr_b16 a[16:17], v34 offset:128\n" \
"ds_read_b64_tr_b16 a[18:19], v34 offset:4736\n" \
"v_mfma_f32_16x16x32_bf16 v[208:211], a[20:23], a[168:171], v[208:211]\n" \
"v_mfma_f32_16x16x32_bf16 v[212:215], a[20:23], a[184:187], v[212:215]\n" \
"ds_read_b64_tr_b16 a[20:21], v34 offset:9344\n" \
"ds_read_b64_tr_b16 a[22:23], v34 offset:13952\n" \
"v_mfma_f32_16x16x32_bf16 v[208:211], a[24:27], a[172:175], v[208:211]\n" \
"v_mfma_f32_16x16x32_bf16 v[212:215], a[24:27], a[188:191], v[212:215]\n" \
"ds_read_b64_tr_b16 a[24:25], v34 offset:18560\n" \
"ds_read_b64_tr_b16 a[26:27], v34 offset:23168\n" \
"v_mfma_f32_16x16x32_bf16 v[208:211], a[28:31], a[220:223], v[208:211]\n" \
"v_mfma_f32_16x16x32_bf16 v[212:215], a[28:31], a[236:239], v[212:215]\n" \
"ds_read_b64_tr_b16 a[28:29], v10 offset:9344\n" \
"ds_read_b64_tr_b16 a[30:31], v10 offset:13952\n" \
"s_waitcnt vmcnt(6)\n" \
"s_mov_b64 exec, 3\n" \
"buffer_store_dword v39, v41, s[56:59], s93 offen sc1\n" \
"s_mov_b64 exec, -1\n" \
"v_mfma_f32_16x16x32_bf16 v[208:211], a[192:195], a[224:227], v[208:211]\n" \
"v_mfma_f32_16x16x32_bf16 v[212:215], a[192:195], a[240:243], v[212:215]\n" \
"ds_read_b64_tr_b16 a[192:193], v33 offset:160\n" \
"s_waitcnt lgkmcnt(13)\n" \
"v_mfma_f32_16x16x32_bf16 v[208:211], a[196:199], a[228:231], v[208:211]\n" \
"ds_read_b64_tr_b16 a[194:195], v33 offset:4768\n" \
"v_mfma_f32_16x16x32_bf16 v[212:215], a[196:199], a[244:247], v[212:215]\n" \
"ds_read_b64_tr_b16 a[196:197], v33 offset:9376\n" \
"ds_read_b64_tr_b16 a[198:199], v33 offset:13984\n" \
"v_mfma_f32_16x16x32_bf16 v[208:211], a[200:203], a[232:235], v[208:211]\n" \
"v_mfma_f32_16x16x32_bf16 v[212:215], a[200:203], a[248:251], v[212:215]\n" \
"ds_read_b64_tr_b16 a[200:201], v33 offset:18592\n" \
"ds_read_b64_tr_b16 a[202:203], v33 offset:23200\n" \
"v_mfma_f32_16x16x32_bf16 v[216:219], a[204:207], a[96:99], v[216:219]\n" \
"v_mfma_f32_16x16x32_bf16 v[220:223], a[204:207], a[112:115], v[220:223]\n" \
"ds_read_b64_tr_b16 a[204:205], v33 offset:27808\n" \
"ds_read_b64_tr_b16 a[206:207], v33 offset:32416\n" \
"v_mfma_f32_16x16x32_bf16 v[216:219], a[208:211], a[100:103], v[216:219]\n" \
"v_cvt_pk_bf16_f32 v208, v208, v209\n" \
"v_cvt_pk_bf16_f32 v209, v210, v211\n" \
"v_cvt_pk_bf16_f32 v210, v212, v213\n" \
"v_cvt_pk_bf16_f32 v211, v214, v215\n" \
"buffer_store_dwordx4 v[208:211], v20, s[56:59], s97 offen offset:1024 sc1\n" \
"s_add_u32 s97, s97, 0x2000\n" \
"v_mfma_f32_16x16x32_bf16 v[220:223], a[208:211], a[116:119], v[220:223]\n" \
"ds_read_b64_tr_b16 a[208:209], v33 offset:37024\n" \
"ds_read_b64_tr_b16 a[210:211], v33 offset:41632\n" \
"v_mfma_f32_16x16x32_bf16 v[216:219], a[212:215], a[104:107], v[216:219]\n" \
"v_mfma_f32_16x16x32_bf16 v[220:223], a[212:215], a[120:123], v[220:223]\n" \
"ds_read_b64_tr_b16 a[212:213], v33 offset:46240\n" \
"ds_read_b64_tr_b16 a[214:215], v33 offset:50848\n" \
"v_mfma_f32_16x16x32_bf16 v[216:219], a[216:219], a[108:111], v[216:219]\n" \
"v_mfma_f32_16x16x32_bf16 v[220:223], a[216:219], a[124:127], v[220:223]\n" \
"ds_read_b64_tr_b16 a[216:217], v33 offset:55456\n" \
"ds_read_b64_tr_b16 a[218:219], v33 offset:60064\n" \
"v_mfma_f32_16x16x32_bf16 v[216:219], a[0:3], a[160:163], v[216:219]\n" \
"v_mfma_f32_16x16x32_bf16 v[220:223], a[0:3], a[176:179], v[220:223]\n" \
"ds_read_b64_tr_b16 a[0:1], v10 offset:160\n" \
"ds_read_b64_tr_b16 a[2:3], v10 offset:4768\n" \
"v_mfma_f32_16x16x32_bf16 v[216:219], a[4:7], a[164:167], v[216:219]\n" \
"v_mfma_f32_16x16x32_bf16 v[220:223], a[4:7], a[180:183], v[220:223]\n" \
"ds_read_b64_tr_b16 a[4:5], v34 offset:160\n" \
"s_waitcnt lgkmcnt(13)\n" \
"v_mfma_f32_16x16x32_bf16 v[216:219], a[8:11], a[168:171], v[216:219]\n" \
"ds_read_b64_tr_b16 a[6:7], v34 offset:4768\n" \
"v_mfma_f32_16x16x32_bf16 v[220:223], a[8:11], a[184:187], v[220:223]\n" \
"ds_read_b64_tr_b16 a[8:9], v34 offset:9376\n" \
"ds_read_b64_tr_b16 a[10:11], v34 offset:13984\n" \
"v_mfma_f32_16x16x32_bf16 v[216:219], a[12:15], a[172:175], v[216:219]\n" \
"v_mfma_f32_16x16x32_bf16 v[220:223], a[12:15], a[188:191], v[220:223]\n" \
"ds_read_b64_tr_b16 a[12:13], v34 offset:18592\n" \
"ds_read_b64_tr_b16 a[14:15], v34 offset:23200\n" \
"v_mfma_f32_16x16x32_bf16 v[216:219], a[16:19], a[220:223], v[216:219]\n" \
"v_mfma_f32_16x16x32_bf16 v[220:223], a[16:19], a[236:239], v[220:223]\n" \
"ds_read_b64_tr_b16 a[16:17], v10 offset:9376\n" \
"ds_read_b64_tr_b16 a[18:19], v10 offset:13984\n" \
"s_waitcnt vmcnt(6)\n" \
"s_mov_b64 exec, 1\n" \
"buffer_store_dword v39, off, s[56:59], s94 offset:32 sc1\n" \
"s_mov_b64 exec, -1\n" \
"v_mfma_f32_16x16x32_bf16 v[216:219], a[20:23], a[224:227], v[216:219]\n" \
"v_mfma_f32_16x16x32_bf16 v[220:223], a[20:23], a[240:243], v[220:223]\n" \
"ds_read_b64_tr_b16 a[20:21], v33 offset:192\n" \
"ds_read_b64_tr_b16 a[22:23], v33 offset:4800\n" \
"v_mfma_f32_16x16x32_bf16 v[216:219], a[24:27], a[228:231], v[216:219]\n" \
"v_mfma_f32_16x16x32_bf16 v[220:223], a[24:27], a[244:247], v[220:223]\n" \
"ds_read_b64_tr_b16 a[24:25], v33 offset:9408\n" \
"ds_read_b64_tr_b16 a[26:27], v33 offset:14016\n" \
"v_mfma_f32_16x16x32_bf16 v[216:219], a[28:31], a[232:235], v[216:219]\n" \
"v_mfma_f32_16x16x32_bf16 v[220:223], a[28:31], a[248:251], v[220:223]\n" \
"buffer_load_dword v37, v40, s[56:59], s74 offen sc1\n" \
"v_mfma_f32_16x16x32_bf16 v[224:227], a[192:195], a[96:99], v[224:227]\n" \
"ds_read_b64_tr_b16 a[28:29], v33 offset:18624\n" \
"ds_read_b64_tr_b16 a[30:31], v33 offset:23232\n" \
"v_mfma_f32_16x16x32_bf16 v[228:231], a[192:195], a[112:115], v[228:231]\n" \
"ds_read_b64_tr_b16 a[192:193], v33 offset:27840\n" \
"ds_read_b64_tr_b16 a[194:195], v33 offset:32448\n" \
"v_cvt_pk_bf16_f32 v216, v216, v217\n" \
"v_cvt_pk_bf16_f32 v217, v218, v219\n" \
"v_cvt_pk_bf16_f32 v218, v220, v221\n" \
"v_cvt_pk_bf16_f32 v219, v222, v223\n" \
"buffer_store_dwordx4 v[216:219], v20, s[56:59], s64 offen offset:1024 sc1\n" \
"s_add_u32 s64, s64, 0x1800\n" \
"v_mfma_f32_16x16x32_bf16 v[224:227], a[196:199], a[100:103], v[224:227]\n" \
"v_mfma_f32_16x16x32_bf16 v[228:231], a[196:199], a[116:119], v[228:231]\n" \
"ds_read_b64_tr_b16 a[196:197], v33 offset:37056\n" \
"s_waitcnt lgkmcnt(11)\n" \
"v_mfma_f32_16x16x32_bf16 v[224:227], a[200:203], a[104:107], v[224:227]\n" \
"ds_read_b64_tr_b16 a[198:199], v33 offset:41664\n" \
"v_mfma_f32_16x16x32_bf16 v[228:231], a[200:203], a[120:123], v[228:231]\n" \
"ds_read_b64_tr_b16 a[200:201], v33 offset:46272\n" \
"ds_read_b64_tr_b16 a[202:203], v33 offset:50880\n" \
"v_mfma_f32_16x16x32_bf16 v[224:227], a[204:207], a[108:111], v[224:227]\n" \
"v_mfma_f32_16x16x32_bf16 v[228:231], a[204:207], a[124:127], v[228:231]\n" \
"ds_read_b64_tr_b16 a[204:205], v33 offset:55488\n" \
"ds_read_b64_tr_b16 a[206:207], v33 offset:60096\n" \
"v_mfma_f32_16x16x32_bf16 v[224:227], a[208:211], a[160:163], v[224:227]\n" \
"v_mfma_f32_16x16x32_bf16 v[228:231], a[208:211], a[176:179], v[228:231]\n" \
"ds_read_b64_tr_b16 a[208:209], v10 offset:192\n" \
"ds_read_b64_tr_b16 a[210:211], v10 offset:4800\n" \
"v_mfma_f32_16x16x32_bf16 v[224:227], a[212:215], a[164:167], v[224:227]\n" \
"v_mfma_f32_16x16x32_bf16 v[228:231], a[212:215], a[180:183], v[228:231]\n" \
"ds_read_b64_tr_b16 a[212:213], v34 offset:192\n" \
"ds_read_b64_tr_b16 a[214:215], v34 offset:4800\n" \
"v_mfma_f32_16x16x32_bf16 v[224:227], a[216:219], a[168:171], v[224:227]\n" \
"v_mfma_f32_16x16x32_bf16 v[228:231], a[216:219], a[184:187], v[228:231]\n" \
"ds_read_b64_tr_b16 a[216:217], v34 offset:9408\n" \
"ds_read_b64_tr_b16 a[218:219], v34 offset:14016\n" \
"v_mfma_f32_16x16x32_bf16 v[224:227], a[0:3], a[172:175], v[224:227]\n" \
"v_mfma_f32_16x16x32_bf16 v[228:231], a[0:3], a[188:191], v[228:231]\n" \
"ds_read_b64_tr_b16 a[0:1], v34 offset:18624\n" \
"ds_read_b64_tr_b16 a[2:3], v34 offset:23232\n" \
"v_mfma_f32_16x16x32_bf16 v[224:227], a[4:7], a[220:223], v[224:227]\n" \
"v_mfma_f32_16x16x32_bf16 v[228:231], a[4:7], a[236:239], v[228:231]\n" \
"ds_read_b64_tr_b16 a[4:5], v10 offset:9408\n" \
"ds_read_b64_tr_b16 a[6:7], v10 offset:14016\n" \
"s_waitcnt vmcnt(6)\n" \
"s_mov_b64 exec, 1\n" \
"buffer_store_dword v39, off, s[56:59], s95 offset:36 sc1\n" \
"s_mov_b64 exec, -1\n" \
"v_mfma_f32_16x16x32_bf16 v[224:227], a[8:11], a[224:227], v[224:227]\n" \
"v_mfma_f32_16x16x32_bf16 v[228:231], a[8:11], a[240:243], v[228:231]\n" \
"ds_read_b64_tr_b16 a[8:9], v33 offset:224\n" \
"ds_read_b64_tr_b16 a[10:11], v33 offset:4832\n" \
"v_mfma_f32_16x16x32_bf16 v[224:227], a[12:15], a[228:231], v[224:227]\n" \
"v_mfma_f32_16x16x32_bf16 v[228:231], a[12:15], a[244:247], v[228:231]\n" \
"ds_read_b64_tr_b16 a[12:13], v33 offset:9440\n" \
"s_waitcnt lgkmcnt(13)\n" \
"v_mfma_f32_16x16x32_bf16 v[224:227], a[16:19], a[232:235], v[224:227]\n" \
"ds_read_b64_tr_b16 a[14:15], v33 offset:14048\n" \
"v_mfma_f32_16x16x32_bf16 v[228:231], a[16:19], a[248:251], v[228:231]\n" \
"ds_read_b64_tr_b16 a[16:17], v33 offset:18656\n" \
"ds_read_b64_tr_b16 a[18:19], v33 offset:23264\n" \
"v_mfma_f32_16x16x32_bf16 v[232:235], a[20:23], a[96:99], v[232:235]\n" \
"v_mfma_f32_16x16x32_bf16 v[236:239], a[20:23], a[112:115], v[236:239]\n" \
"ds_read_b64_tr_b16 a[20:21], v33 offset:27872\n" \
"ds_read_b64_tr_b16 a[22:23], v33 offset:32480\n" \
"v_cvt_pk_bf16_f32 v224, v224, v225\n" \
"v_cvt_pk_bf16_f32 v225, v226, v227\n" \
"v_cvt_pk_bf16_f32 v226, v228, v229\n" \
"v_cvt_pk_bf16_f32 v227, v230, v231\n" \
"buffer_store_dwordx4 v[224:227], v20, s[56:59], s73 offen offset:1024 sc1\n" \
"v_mfma_f32_16x16x32_bf16 v[232:235], a[24:27], a[100:103], v[232:235]\n" \
"v_mfma_f32_16x16x32_bf16 v[236:239], a[24:27], a[116:119], v[236:239]\n" \
"ds_read_b64_tr_b16 a[24:25], v33 offset:37088\n" \
"ds_read_b64_tr_b16 a[26:27], v33 offset:41696\n" \
"v_mfma_f32_16x16x32_bf16 v[232:235], a[28:31], a[104:107], v[232:235]\n" \
"v_mfma_f32_16x16x32_bf16 v[236:239], a[28:31], a[120:123], v[236:239]\n" \
"ds_read_b64_tr_b16 a[28:29], v33 offset:46304\n" \
"ds_read_b64_tr_b16 a[30:31], v33 offset:50912\n" \
"v_mfma_f32_16x16x32_bf16 v[232:235], a[192:195], a[108:111], v[232:235]\n" \
"v_mfma_f32_16x16x32_bf16 v[236:239], a[192:195], a[124:127], v[236:239]\n" \
"ds_read_b64_tr_b16 a[192:193], v33 offset:55520\n" \
"ds_read_b64_tr_b16 a[194:195], v33 offset:60128\n" \
"v_mfma_f32_16x16x32_bf16 v[232:235], a[196:199], a[160:163], v[232:235]\n" \
"v_mfma_f32_16x16x32_bf16 v[236:239], a[196:199], a[176:179], v[236:239]\n" \
"ds_read_b64_tr_b16 a[196:197], v10 offset:224\n" \
"ds_read_b64_tr_b16 a[198:199], v10 offset:4832\n" \
"v_mfma_f32_16x16x32_bf16 v[232:235], a[200:203], a[164:167], v[232:235]\n" \
"v_mfma_f32_16x16x32_bf16 v[236:239], a[200:203], a[180:183], v[236:239]\n" \
"ds_read_b64_tr_b16 a[200:201], v34 offset:224\n" \
"ds_read_b64_tr_b16 a[202:203], v34 offset:4832\n" \
"v_mfma_f32_16x16x32_bf16 v[232:235], a[204:207], a[168:171], v[232:235]\n" \
"v_mfma_f32_16x16x32_bf16 v[236:239], a[204:207], a[184:187], v[236:239]\n" \
"ds_read_b64_tr_b16 a[204:205], v34 offset:9440\n" \
"s_waitcnt lgkmcnt(13)\n" \
"v_mfma_f32_16x16x32_bf16 v[232:235], a[208:211], a[172:175], v[232:235]\n" \
"ds_read_b64_tr_b16 a[206:207], v34 offset:14048\n" \
"v_mfma_f32_16x16x32_bf16 v[236:239], a[208:211], a[188:191], v[236:239]\n" \
"ds_read_b64_tr_b16 a[208:209], v34 offset:18656\n" \
"ds_read_b64_tr_b16 a[210:211], v34 offset:23264\n" \
"v_mfma_f32_16x16x32_bf16 v[232:235], a[212:215], a[220:223], v[232:235]\n" \
"s_waitcnt vmcnt(7)\n" \
"s_mov_b64 exec, 1\n" \
"buffer_store_dword v39, off, s[56:59], s96 offset:40 sc1\n" \
"s_mov_b64 exec, -1\n" \
"v_mfma_f32_16x16x32_bf16 v[236:239], a[212:215], a[236:239], v[236:239]\n" \
"ds_read_b64_tr_b16 a[212:213], v10 offset:9440\n" \
"ds_read_b64_tr_b16 a[214:215], v10 offset:14048\n" \
"v_mfma_f32_16x16x32_bf16 v[232:235], a[216:219], a[224:227], v[232:235]\n" \
"v_mfma_f32_16x16x32_bf16 v[236:239], a[216:219], a[240:243], v[236:239]\n" \
"ds_read_b64_tr_b16 a[216:217], v36 offset:0\n" \
"ds_read_b64_tr_b16 a[218:219], v36 offset:1024\n" \
"v_mfma_f32_16x16x32_bf16 v[232:235], a[0:3], a[228:231], v[232:235]\n" \
"v_mfma_f32_16x16x32_bf16 v[236:239], a[0:3], a[244:247], v[236:239]\n" \
"ds_read_b64_tr_b16 a[0:1], v36 offset:2048\n" \
"ds_read_b64_tr_b16 a[2:3], v36 offset:3072\n" \
"v_mfma_f32_16x16x32_bf16 v[232:235], a[4:7], a[232:235], v[232:235]\n" \
"v_mfma_f32_16x16x32_bf16 v[236:239], a[4:7], a[248:251], v[236:239]\n" \
"ds_read_b64_tr_b16 a[4:5], v36 offset:32\n" \
"ds_read_b64_tr_b16 a[6:7], v36 offset:1056\n" \
"v_mfma_f32_16x16x32_bf16 v[240:243], a[8:11], a[96:99], v[240:243]\n" \
"v_mfma_f32_16x16x32_bf16 v[244:247], a[8:11], a[112:115], v[244:247]\n" \
"ds_read_b64_tr_b16 a[8:9], v36 offset:2080\n" \
"ds_read_b64_tr_b16 a[10:11], v36 offset:3104\n" \
"v_mfma_f32_16x16x32_bf16 v[240:243], a[12:15], a[100:103], v[240:243]\n" \
"v_cvt_pk_bf16_f32 v232, v232, v233\n" \
"v_cvt_pk_bf16_f32 v233, v234, v235\n" \
"v_cvt_pk_bf16_f32 v234, v236, v237\n" \
"v_cvt_pk_bf16_f32 v235, v238, v239\n" \
"buffer_store_dwordx4 v[232:235], v20, s[56:59], s92 offen offset:1024 sc1\n" \
"s_waitcnt vmcnt(5)\n" \
"v_cmp_eq_u32_e64 s[98:99], s62, v37\n" \
"s_or_b32 s75, 0x7f, s100\n" \
"s_andn2_b32 s75, s75, s98\n" \
"s_cbranch_scc1 L_sx0_%=\n" \
"L_rx0_%=:\n" \
"s_mov_b64 exec, 1\n" \
"buffer_store_dword v39, off, s[56:59], s97 offset:44 sc1\n" \
"s_mov_b64 exec, -1\n" \
"s_add_u32 s73, s73, 0x1000\n" \
"s_add_u32 s92, s92, 0x800\n" \
"s_add_u32 s65, s63, 0x0\n" \
"s_add_u32 s66, s63, 0x800\n" \
"s_add_u32 s67, s63, 0x1000\n" \
"s_add_u32 s68, s63, 0x1800\n" \
"s_add_u32 s69, s63, 0x2000\n" \
"s_add_u32 s70, s63, 0x2800\n" \
"s_add_u32 s71, s63, 0x3000\n" \
"v_mfma_f32_16x16x32_bf16 v[244:247], a[12:15], a[116:119], v[244:247]\n" \
"v_mfma_f32_16x16x32_bf16 v[240:243], a[16:19], a[104:107], v[240:243]\n" \
"buffer_load_dwordx4 v[116:119], v20, s[56:59], s65 offen offset:0 sc1\n" \
"v_mfma_f32_16x16x32_bf16 v[244:247], a[16:19], a[120:123], v[244:247]\n" \
"s_waitcnt lgkmcnt(6)\n" \
"v_mfma_f32_16x16x32_bf16 v[240:243], a[20:23], a[108:111], v[240:243]\n" \
"v_mfma_f32_16x16x32_bf16 v[244:247], a[20:23], a[124:127], v[244:247]\n" \
"v_mfma_f32_16x16x32_bf16 v[168:171], a[24:27], a[128:131], v[168:171]\n" \
"buffer_load_dwordx4 v[124:127], v20, s[56:59], s66 offen offset:0 sc1\n" \
"v_mfma_f32_16x16x32_bf16 v[172:175], a[24:27], a[144:147], v[172:175]\n" \
"v_readlane_b32 s66, v11, 53\n" \
"v_mfma_f32_16x16x32_bf16 v[240:243], a[24:27], a[160:163], v[240:243]\n" \
"v_mfma_f32_16x16x32_bf16 v[244:247], a[24:27], a[176:179], v[244:247]\n" \
"v_mfma_f32_16x16x32_bf16 v[168:171], a[28:31], a[132:135], v[168:171]\n" \
"buffer_load_dwordx4 v[132:135], v20, s[56:59], s67 offen offset:0 sc1\n" \
"v_mfma_f32_16x16x32_bf16 v[172:175], a[28:31], a[148:151], v[172:175]\n" \
"v_readlane_b32 s67, v11, 44\n" \
"v_mfma_f32_16x16x32_bf16 v[240:243], a[28:31], a[164:167], v[240:243]\n" \
"v_mfma_f32_16x16x32_bf16 v[244:247], a[28:31], a[180:183], v[244:247]\n" \
"v_mfma_f32_16x16x32_bf16 v[168:171], a[192:195], a[136:139], v[168:171]\n" \
"buffer_load_dwordx4 v[140:143], v20, s[56:59], s68 offen offset:0 sc1\n" \
"v_mfma_f32_16x16x32_bf16 v[172:175], a[192:195], a[152:155], v[172:175]\n" \
"v_readlane_b32 s68, v11, 42\n" \
"v_mfma_f32_16x16x32_bf16 v[240:243], a[192:195], a[168:171], v[240:243]\n" \
"v_mfma_f32_16x16x32_bf16 v[244:247], a[192:195], a[184:187], v[244:247]\n" \
"v_mfma_f32_16x16x32_bf16 v[168:171], a[196:199], a[140:143], v[168:171]\n" \
"buffer_load_dwordx4 v[148:151], v20, s[56:59], s69 offen offset:0 sc1\n" \
"v_mfma_f32_16x16x32_bf16 v[172:175], a[196:199], a[156:159], v[172:175]\n" \
"v_readlane_b32 s69, v11, 43\n" \
"v_mfma_f32_16x16x32_bf16 v[240:243], a[196:199], a[172:175], v[240:243]\n" \
"v_mfma_f32_16x16x32_bf16 v[244:247], a[196:199], a[188:191], v[244:247]\n" \
"v_mfma_f32_16x16x32_bf16 v[168:171], a[200:203], a[32:35], v[168:171]\n" \
"buffer_load_dwordx4 v[156:159], v20, s[56:59], s70 offen offset:0 sc1\n" \
"v_mfma_f32_16x16x32_bf16 v[172:175], a[200:203], a[48:51], v[172:175]\n" \
"v_mfma_f32_16x16x32_bf16 v[240:243], a[200:203], a[220:223], v[240:243]\n" \
"v_mfma_f32_16x16x32_bf16 v[244:247], a[200:203], a[236:239], v[244:247]\n" \
"v_mfma_f32_16x16x32_bf16 v[168:171], a[204:207], a[36:39], v[168:171]\n" \
"buffer_load_dwordx4 v[164:167], v20, s[56:59], s71 offen offset:0 sc1\n" \
"v_mfma_f32_16x16x32_bf16 v[172:175], a[204:207], a[52:55], v[172:175]\n" \
"v_mfma_f32_16x16x32_bf16 v[240:243], a[204:207], a[224:227], v[240:243]\n" \
"v_mfma_f32_16x16x32_bf16 v[244:247], a[204:207], a[240:243], v[244:247]\n" \
"v_mfma_f32_16x16x32_bf16 v[168:171], a[208:211], a[40:43], v[168:171]\n" \
"buffer_load_dwordx4 v[112:115], v20, s[56:59], s72 offen offset:0 sc1\n" \
"v_mfma_f32_16x16x32_bf16 v[172:175], a[208:211], a[56:59], v[172:175]\n" \
"v_mfma_f32_16x16x32_bf16 v[240:243], a[208:211], a[228:231], v[240:243]\n" \
"v_mfma_f32_16x16x32_bf16 v[244:247], a[208:211], a[244:247], v[244:247]\n" \
"v_mfma_f32_16x16x32_bf16 v[168:171], a[212:215], a[44:47], v[168:171]\n" \
"buffer_load_dwordx4 v[184:187], v20, s[56:59], s72 offen offset:2048 sc1\n" \
"s_add_u32 s72, s72, 0x1000\n" \
"v_mfma_f32_16x16x32_bf16 v[172:175], a[212:215], a[60:63], v[172:175]\n" \
"v_mfma_f32_16x16x32_bf16 v[240:243], a[212:215], a[232:235], v[240:243]\n" \
"s_waitcnt vmcnt(14)\n" \
"s_mov_b64 exec, 1\n" \
"buffer_store_dword v39, off, s[56:59], s64 offset:48 sc1\n" \
"s_mov_b64 exec, -1\n" \
"v_mfma_f32_16x16x32_bf16 v[244:247], a[212:215], a[248:251], v[244:247]\n" \
"v_mfma_f32_16x16x32_bf16 v[168:171], a[216:219], v[64:67], v[168:171]\n" \
"v_mfma_f32_16x16x32_bf16 v[172:175], a[216:219], v[72:75], v[172:175]\n" \
"v_mfma_f32_16x16x32_bf16 v[240:243], a[216:219], v[80:83], v[240:243]\n" \
"buffer_load_dwordx4 v[120:123], v20, s[56:59], s72 offen offset:0 sc1\n" \
"v_mfma_f32_16x16x32_bf16 v[244:247], a[216:219], v[88:91], v[244:247]\n" \
"s_waitcnt lgkmcnt(0)\n" \
"v_mfma_f32_16x16x32_bf16 v[168:171], a[0:3], v[68:71], v[168:171]\n" \
"v_mfma_f32_16x16x32_bf16 v[172:175], a[0:3], v[76:79], v[172:175]\n" \
"v_mfma_f32_16x16x32_bf16 v[240:243], a[0:3], v[84:87], v[240:243]\n" \
"buffer_load_dwordx4 v[192:195], v20, s[56:59], s72 offen offset:2048 sc1\n" \
"s_add_u32 s72, s72, 0x1000\n" \
"v_mfma_f32_16x16x32_bf16 v[244:247], a[0:3], v[92:95], v[244:247]\n" \
"v_mfma_f32_16x16x32_bf16 v[56:59], a[4:7], v[96:99], 0\n" \
"v_mfma_f32_16x16x32_bf16 v[60:63], a[4:7], v[104:107], 0\n" \
"v_mfma_f32_16x16x32_bf16 v[56:59], a[8:11], v[100:103], v[56:59]\n" \
"buffer_load_dwordx4 v[128:131], v20, s[56:59], s72 offen offset:0 sc1\n" \
"v_mfma_f32_16x16x32_bf16 v[60:63], a[8:11], v[108:111], v[60:63]\n" \
"buffer_load_dwordx4 v[200:203], v20, s[56:59], s72 offen offset:2048 sc1\n" \
"s_add_u32 s72, s72, 0x1000\n" \
"buffer_load_dwordx4 v[176:179], v20, s[56:59], s72 offen offset:0 sc1\n" \
"buffer_load_dwordx4 v[248:251], v20, s[56:59], s72 offen offset:2048 sc1\n" \
"s_waitcnt vmcnt(15)\n" \
"v_mfma_f32_16x16x32_bf16 v[168:171], v[50:53], v[116:119], v[168:171]\n" \
"s_mov_b64 exec, 1\n" \
"buffer_store_dword v39, off, s[56:59], s73 offset:52 sc1\n" \
"s_mov_b64 exec, -1\n" \
"s_mov_b64 exec, 1\n" \
"buffer_store_dword v39, off, s[56:59], s92 offset:56 sc1\n" \
"s_mov_b64 exec, -1\n" \
"buffer_load_dword v37, v40, s[56:59], s74 offen sc1\n" \
"v_mfma_f32_16x16x32_bf16 v[172:175], v[48:51], v[116:119], v[172:175]\n" \
"s_waitcnt vmcnt(17)\n" \
"v_mfma_f32_16x16x32_bf16 v[168:171], v[50:53], v[124:127], v[168:171]\n" \
"v_mfma_f32_16x16x32_bf16 v[172:175], v[48:51], v[124:127], v[172:175]\n" \
"s_waitcnt vmcnt(16)\n" \
"v_mfma_f32_16x16x32_bf16 v[168:171], v[50:53], v[132:135], v[168:171]\n" \
"v_mfma_f32_16x16x32_bf16 v[172:175], v[48:51], v[132:135], v[172:175]\n" \
"s_waitcnt vmcnt(15)\n" \
"v_mfma_f32_16x16x32_bf16 v[168:171], v[50:53], v[140:143], v[168:171]\n" \
"v_mfma_f32_16x16x32_bf16 v[172:175], v[48:51], v[140:143], v[172:175]\n" \
"s_waitcnt vmcnt(14)\n" \
"v_mfma_f32_16x16x32_bf16 v[168:171], v[50:53], v[148:151], v[168:171]\n" \
"v_mfma_f32_16x16x32_bf16 v[172:175], v[48:51], v[148:151], v[172:175]\n" \
"s_waitcnt vmcnt(13)\n" \
"v_mfma_f32_16x16x32_bf16 v[168:171], v[50:53], v[156:159], v[168:171]\n" \
"v_mfma_f32_16x16x32_bf16 v[172:175], v[48:51], v[156:159], v[172:175]\n" \
"s_waitcnt vmcnt(12)\n" \
"v_mfma_f32_16x16x32_bf16 v[168:171], v[50:53], v[164:167], v[168:171]\n" \
"v_mfma_f32_16x16x32_bf16 v[172:175], v[48:51], v[164:167], v[172:175]\n" \
"s_waitcnt vmcnt(11)\n" \
"v_mfma_f32_16x16x32_bf16 v[56:59], v[50:53], v[112:115], v[56:59]\n" \
"v_mfma_f32_16x16x32_bf16 v[60:63], v[48:51], v[112:115], v[60:63]\n" \
"s_waitcnt vmcnt(10)\n" \
"v_mfma_f32_16x16x32_bf16 v[56:59], v[50:53], v[184:187], v[56:59]\n" \
"v_mfma_f32_16x16x32_bf16 v[60:63], v[48:51], v[184:187], v[60:63]\n" \
"s_waitcnt vmcnt(8)\n" \
"v_mfma_f32_16x16x32_bf16 v[56:59], v[50:53], v[120:123], v[56:59]\n" \
"v_cvt_pk_bf16_f32 v168, v168, v169\n" \
"v_cvt_pk_bf16_f32 v172, v172, v173\n" \
"v_mfma_f32_16x16x32_bf16 v[60:63], v[48:51], v[120:123], v[60:63]\n" \
"v_cvt_pk_bf16_f32 v169, v170, v171\n" \
"v_cvt_pk_bf16_f32 v173, v174, v175\n" \
"buffer_store_dwordx2 v[168:169], v42, s[88:91], s68 offen sc1\n" \
"buffer_store_dwordx2 v[172:173], v43, s[88:91], s68 offen sc1\n" \
"s_waitcnt vmcnt(9)\n" \
"v_mfma_f32_16x16x32_bf16 v[56:59], v[50:53], v[192:195], v[56:59]\n" \
"v_mfma_f32_16x16x32_bf16 v[60:63], v[48:51], v[192:195], v[60:63]\n" \
"s_waitcnt vmcnt(8)\n" \
"v_mfma_f32_16x16x32_bf16 v[56:59], v[50:53], v[128:131], v[56:59]\n" \
"v_mfma_f32_16x16x32_bf16 v[60:63], v[48:51], v[128:131], v[60:63]\n" \
"s_waitcnt vmcnt(7)\n" \
"v_mfma_f32_16x16x32_bf16 v[56:59], v[50:53], v[200:203], v[56:59]\n" \
"v_mfma_f32_16x16x32_bf16 v[60:63], v[48:51], v[200:203], v[60:63]\n" \
"s_waitcnt vmcnt(6)\n" \
"v_mfma_f32_16x16x32_bf16 v[56:59], v[50:53], v[176:179], v[56:59]\n" \
"v_mfma_f32_16x16x32_bf16 v[60:63], v[48:51], v[176:179], v[60:63]\n" \
"s_waitcnt vmcnt(5)\n" \
"v_mfma_f32_16x16x32_bf16 v[56:59], v[50:53], v[248:251], v[56:59]\n" \
"v_mfma_f32_16x16x32_bf16 v[60:63], v[48:51], v[248:251], v[60:63]\n" \
"s_waitcnt vmcnt(2)\n" \
"v_cmp_eq_u32_e64 s[98:99], s62, v37\n" \
"s_nop 0\n" \
"s_andn2_b32 s75, 0x100, s98\n" \
"s_cbranch_scc1 L_sx1_%=\n" \
"L_rx1_%=:\n" \
"s_add_u32 s93, s63, 0x0\n" \
"buffer_load_dwordx4 v[188:191], v20, s[56:59], s93 offen offset:1024 sc1\n" \
"s_andn2_b32 s75, 0x200, s98\n" \
"s_cbranch_scc1 L_sx2_%=\n" \
"L_rx2_%=:\n" \
"s_add_u32 s94, s63, 0x800\n" \
"v_cvt_pk_bf16_f32 v56, v56, v57\n" \
"v_cvt_pk_bf16_f32 v60, v60, v61\n" \
"v_cvt_pk_bf16_f32 v57, v58, v59\n" \
"v_cvt_pk_bf16_f32 v61, v62, v63\n" \
"buffer_store_dwordx2 v[56:57], v42, s[88:91], s67 offen sc1\n" \
"buffer_store_dwordx2 v[60:61], v43, s[88:91], s67 offen sc1\n" \
"buffer_load_dwordx4 v[196:199], v20, s[56:59], s94 offen offset:1024 sc1\n" \
"s_andn2_b32 s75, 0x400, s98\n" \
"s_cbranch_scc1 L_sx3_%=\n" \
"L_rx3_%=:\n" \
"s_add_u32 s95, s63, 0x1000\n" \
"buffer_load_dwordx4 v[204:207], v20, s[56:59], s95 offen offset:1024 sc1\n" \
"s_andn2_b32 s75, 0x800, s98\n" \
"s_cbranch_scc1 L_sx4_%=\n" \
"L_rx4_%=:\n" \
"s_add_u32 s96, s63, 0x1800\n" \
"buffer_load_dwordx4 v[212:215], v20, s[56:59], s96 offen offset:1024 sc1\n" \
"s_andn2_b32 s75, 0x1000, s98\n" \
"s_cbranch_scc1 L_sx5_%=\n" \
"L_rx5_%=:\n" \
"s_add_u32 s97, s63, 0x2000\n" \
"buffer_load_dwordx4 v[220:223], v20, s[56:59], s97 offen offset:1024 sc1\n" \
"s_andn2_b32 s75, 0x2000, s98\n" \
"s_cbranch_scc1 L_sx6_%=\n" \
"L_rx6_%=:\n" \
"s_add_u32 s64, s63, 0x2800\n" \
"buffer_load_dwordx4 v[228:231], v20, s[56:59], s64 offen offset:1024 sc1\n" \
"s_andn2_b32 s75, 0x4000, s98\n" \
"s_cbranch_scc1 L_sx7_%=\n" \
"L_rx7_%=:\n" \
"s_add_u32 s65, s63, 0x3000\n" \
"buffer_load_dwordx4 v[236:239], v20, s[56:59], s65 offen offset:1024 sc1\n" \
"s_mov_b64 exec, 1\n" \
"buffer_store_dword v46, off, s[56:59], s66 offset:0 sc1\n" \
"s_mov_b64 exec, -1\n" \
"s_waitcnt vmcnt(9)\n" \
"v_mfma_f32_16x16x32_bf16 v[240:243], v[50:53], v[188:191], v[240:243]\n" \
"v_mfma_f32_16x16x32_bf16 v[244:247], v[48:51], v[188:191], v[244:247]\n" \
"s_waitcnt vmcnt(6)\n" \
"v_mfma_f32_16x16x32_bf16 v[240:243], v[50:53], v[196:199], v[240:243]\n" \
"v_mfma_f32_16x16x32_bf16 v[244:247], v[48:51], v[196:199], v[244:247]\n" \
"s_waitcnt vmcnt(5)\n" \
"v_mfma_f32_16x16x32_bf16 v[240:243], v[50:53], v[204:207], v[240:243]\n" \
"v_mfma_f32_16x16x32_bf16 v[244:247], v[48:51], v[204:207], v[244:247]\n" \
"s_waitcnt vmcnt(4)\n" \
"v_mfma_f32_16x16x32_bf16 v[240:243], v[50:53], v[212:215], v[240:243]\n" \
"v_mfma_f32_16x16x32_bf16 v[244:247], v[48:51], v[212:215], v[244:247]\n" \
"s_waitcnt vmcnt(3)\n" \
"v_mfma_f32_16x16x32_bf16 v[240:243], v[50:53], v[220:223], v[240:243]\n" \
"v_mfma_f32_16x16x32_bf16 v[244:247], v[48:51], v[220:223], v[244:247]\n" \
"s_waitcnt vmcnt(2)\n" \
"v_mfma_f32_16x16x32_bf16 v[240:243], v[50:53], v[228:231], v[240:243]\n" \
"v_mfma_f32_16x16x32_bf16 v[244:247], v[48:51], v[228:231], v[244:247]\n" \
"s_waitcnt vmcnt(1)\n" \
"v_mfma_f32_16x16x32_bf16 v[240:243], v[50:53], v[236:239], v[240:243]\n" \
"v_mfma_f32_16x16x32_bf16 v[244:247], v[48:51], v[236:239], v[244:247]\n" \
"s_nop 6\n" \
"v_cvt_pk_bf16_f32 v240, v240, v241\n" \
"v_cvt_pk_bf16_f32 v244, v244, v245\n" \
"v_cvt_pk_bf16_f32 v241, v242, v243\n" \
"v_cvt_pk_bf16_f32 v245, v246, v247\n" \
"buffer_store_dwordx2 v[240:241], v42, s[88:91], s69 offen sc1\n" \
"buffer_store_dwordx2 v[244:245], v43, s[88:91], s69 offen sc1\n"
#define ATT_VCLOB "v21","v22","v23","v24","v25","v26","v27","v28","v29","v32","v33","v34","v35","v36","v37","v38","v39","v40","v41","v42","v43","v44","v45","v46","v47","v48","v49","v50","v51","v52","v53","v54","v55","v56","v57","v58","v59","v60","v61","v62","v63","v64","v65","v66","v67","v68","v69","v70","v71","v72","v73","v74","v75","v76","v77","v78","v79","v80","v81","v82","v83","v84","v85","v86","v87","v88","v89","v90","v91","v92","v93","v94","v95","v96","v97","v98","v99","v100","v101","v102","v103","v104","v105","v106","v107","v108","v109","v110","v111","v112","v113","v114","v115","v116","v117","v118","v119","v120","v121","v122","v123","v124","v125","v126","v127","v128","v129","v130","v131","v132","v133","v134","v135","v136","v137","v138","v139","v140","v141","v142","v143","v144","v145","v146","v147","v148","v149","v150","v151","v152","v153","v154","v155","v156","v157","v158","v159","v160","v161","v162","v163","v164","v165","v166","v167","v168","v169","v170","v171","v172","v173","v174","v175","v176","v177","v178","v179","v180","v181","v182","v183","v184","v185","v186","v187","v188","v189","v190","v191","v192","v193","v194","v195","v196","v197","v198","v199","v200","v201","v202","v203","v204","v205","v206","v207","v208","v209","v210","v211","v212","v213","v214","v215","v216","v217","v218","v219","v220","v221","v222","v223","v224","v225","v226","v227","v228","v229","v230","v231","v232","v233","v234","v235","v236","v237","v238","v239","v240","v241","v242","v243","v244","v245","v246","v247","v248","v249","v250","v251","v252","v253","v254","v255"
#define ATT_SCLOB "s63","s64","s65","s66","s67","s68","s69","s70","s71","s72","s73","s74","s75","s76","s77","s78","s79","s80","s81","s82","s83","s84","s85","s86","s87","s88","s89","s90","s91","s92","s93","s94","s95","s96","s97","s98","s99","s100","s101"
#define ATT_WTCH 0
enum { T_fo, T_fj, T_fa, T_fas, T_fb, T_fbs, T_f8, T_mk8, T_wt, T_wol, T_woh, T_wbl, T_wbh, T_outl, T_outh, T_r1l, T_r1h, T_r1n, T_r1s, T_m0, T_m1, T_mw, T_m3, T_m4, T_ml, T_w0, T_w1, T_w2, T_w3, T_A1, T_A2, T_A3, T_B1, T_B2, T_B3, T_Ao, T_Bo, T_Al, T_Bl, T_l8, T_xr, T_x8r, T_oA, T_oB, T_o8, T_x0, T_x1, T_x2, T_x3, T_x4, T_x5, T_x6, T_x8, T_ep };

__device__ __forceinline__ void out_consts(u32x4 (&vp)[3], const u16* wo, const u16* wob, const u16* out, const unsigned b, const unsigned w, const unsigned lane) {
    const unsigned sp = b & 7u, nt = (b >> 3) & 3u, j = b >> 5, head = 4u * sp + nt, OOR = 0x40000000u;
    unsigned vw[9];
#pragma unroll
    for (unsigned i = 0; i < 9u; ++i) {
        const unsigned q = 64u * i + lane, row = __umul24(q, 3641u) >> 16, c = q - __umul24(row, 18u);
        vw[i] = __umul24(row, DM * 2) + (c < 16u ? (c * 16u + sp * 32u + 32u) & 255u : c * 16u) - (i & 3u) * 1024u;
    }
    const unsigned row = 16u * w + (lane >> 2), r9 = __umul24(16u * (row >> 5) + (row & 12u) + 2u * ((row >> 4) & 1u) + ((row >> 1) & 1u), 18u) + 16u + (row & 1u);
    unsigned vwl = __umul24(r9, DM * 2) + ((lane & 2u) ? 256u : sp * 32u) + (lane & 1u) * 16u, vrd = __umul24(lane >> 2, 288u) + (lane & 3u) * 8u;
    unsigned tab = 0;
#define TB(n, v) asm("v_writelane_b32 %0, %1, %2" : "+v"(tab) : "s"((unsigned)(v)), "n"((int)T_##n))
    const unsigned FJ = (unsigned)WS_F + (F_J + 640u * j) * 4u, mt = 2u * (w & 2u) + (w & 1u), w2 = w < 2u ? w : 2u, hw = head * 147456u;
    const unsigned FR = (unsigned)WS_F + F_R * 4u + (8u * j + sp) * 1024u;
    TB(fo, FJ + 4u * head); TB(fj, FJ); TB(fa, FR + (w | 2u) * 256u); TB(fas, FR + w * 256u + nt * 4u);
    TB(fb, FR + 128u + w * 256u); TB(fbs, FR + 128u + w * 256u + nt * 4u);
    TB(ep, ssel(b + w, OOR, (unsigned)WS_F + F_EPOCH * 4u));
    const u16* pwo = wo + nt * 144u; const u16* pwb = wob + nt * 144u;
    TB(wol, PLO(pwo)); TB(woh, PHI(pwo) & 0xffffu); TB(wbl, PLO(pwb)); TB(wbh, PHI(pwb) & 0xffffu); TB(outl, PLO(out)); TB(outh, PHI(out) & 0xffffu); TB(r1l, sslt<3>(w, PLO(pwo), PLO(pwb))); TB(r1h, sslt<3>(w, PHI(pwo), PHI(pwb)) & 0xffffu);
    TB(r1n, sslt<3>(w, 72u * sp + 71u, 4095u) * 1152u + 288u); TB(r1s, sslt<3>(w, (72u * sp + 32u * w) * 1152u, hw + 110592u));
    TB(m0, 131072u + 9216u * w2); TB(w0, hw + 36864u * w2); TB(m1, 24576u + 9216u * w); TB(mw, 65536u + 9216u * w); TB(m3, sslt<3>(w, 102400u + 9216u * w, 0u)); TB(m4, sslt<3>(w, 131072u + 9216u * w, 9216u)); TB(ml, 61440u + 1024u * w);
    const unsigned tA = (unsigned)WS_AF + (8u * j + mt) * (KS_TOT * 1024u), tB = (unsigned)WS_AF + (120u - 8u * j + mt) * (KS_TOT * 1024u);
#define HK(k) (4u * sp + ((nt + k) & 3u))
    TB(w1, HK(1u) * 147456u + w * 36864u); TB(w2, HK(2u) * 147456u + w * 36864u); TB(w3, HK(3u) * 147456u + w * 36864u);
    TB(A1, tA + 18432u + HK(1u) * 4096u); TB(A2, tA + 18432u + HK(2u) * 4096u); TB(A3, tA + 18432u + HK(3u) * 4096u);
    TB(B1, tB + 18432u + HK(1u) * 4096u); TB(B2, tB + 18432u + HK(2u) * 4096u); TB(B3, tB + 18432u + HK(3u) * 4096u);
    TB(Ao, tA + sp * 2048u); TB(Bo, tB + sp * 2048u); TB(Al, tA + 16384u); TB(Bl, tB + 16384u);
    const unsigned nred = w ^ (sp >> 1), blk = sp & 1u;
    const unsigned xg = (unsigned)WS_X + (4u * j + nt) * 589824u + w * 16384u, oA = (128u * j + 16u * mt) * 1152u + nt * 288u, oB = (1920u - 128u * j + 16u * mt) * 1152u + nt * 288u;
    TB(l8, ssel(nred, OOR, ssel(blk, tB, tA) + 16384u)); TB(xr, xg + sp * 65536u); TB(x8r, ssel(nred, OOR, xg + 524288u + (sp & 1u) * 1024u));
    TB(oA, oA + sp * 32u); TB(oB, oB + sp * 32u); TB(o8, ssel(nred, OOR, ssel(blk, oB, oA) + 256u)); TB(x8, xg + 524288u + sp * 2048u);
    TB(f8, xg + w * 131072u + 14400u + sp * 4u); TB(mk8, ssel(nred, 0u, 0xff0000u)); TB(wt, hw + j * 18432u + w * 4608u);
#define XI(i) TB(x##i, xg + ((sp + 1u + i) & 7u) * 65536u + i * 2048u)
    XI(0); XI(1); XI(2); XI(3); XI(4); XI(5); XI(6);
#undef XI
#undef HK
#undef TB
    vp[0] = (u32x4){vw[0], vw[1], vw[2], vw[3]}; vp[1] = (u32x4){vw[4], vw[5], vw[6], vw[7]}; vp[2] = (u32x4){vw[8], vwl, vrd, tab};
}
__global__ __launch_bounds__(256, 1) void fused_kernel(const u16* __restrict__ x, const u16* __restrict__ ckv, const u16* __restrict__ wq, const u16* __restrict__ wuk,
                                                       const u16* __restrict__ wuv, const u16* __restrict__ wqkv, const u16* __restrict__ gw,
                                                       const u16* __restrict__ wo, const u16* __restrict__ wob, char* __restrict__ ws_, u16* __restrict__ out) {
    using namespace att;
    extern __shared__ __attribute__((aligned(16))) char lds[];
    u32x4 sa[5]; u32x2 s24, s28; char* ws; unsigned tdum; kaptr ka = (kaptr)__builtin_amdgcn_kernarg_segment_ptr();
    asm volatile("s_load_dwordx8 s[8:15], %[ka], 0x0\n\ts_load_dwordx8 s[16:23], %[ka], 0x20\n\ts_getpc_b64 s[4:5]\n\tv_lshlrev_b32_e32 v191, 7, %[t]\n\ts_bfe_u32 s6, %[b], 0x20003\n\t"
                 "v_lshl_add_u32 v191, s6, 15, v191\n\tv_min_u32_e32 v191, %[e], v191\n\tglobal_load_dword v191, v191, s[4:5]\n\ts_load_dwordx4 s[24:27], %[ka], 0x40\n\ts_load_dwordx2 s[28:29], %[ka], 0x50"
                 : "={s[8:11]}"(sa[0]), "={s[12:15]}"(sa[1]), "={s[16:19]}"(sa[2]), "={s[20:23]}"(sa[3]), "={s[24:25]}"(s24), "={s[26:27]}"(ws), "={s[28:29]}"(s28), "={v191}"(tdum), [ka] "+{s[0:1]}"(ka)
                 : [t] "v"(threadIdx.x), [b] "s"(blockIdx.x), [e] "i"(CT_END) : "s4", "s5", "s6", "scc");
    SBAR();
    const unsigned wid = rfl(threadIdx.x >> 6);
    unsigned tag, vl;
    const int b = blockIdx.x;
    u32x4 vp[5], srdA; u32x2 sd;
    proj_phase(ka, ws, wid, vp, vl, sa, s24, s28, tdum, srdA, sd, tag
    );
    unsigned* const flags = (unsigned*)(ws + WS_F);
    RTS(2);
#define AST(L) do {} while (0)
#define ATT_TAIL "s_endpgm\n"
    asm volatile(ATT_ASM ATT_TAIL "L_end_%=:\n s_nop 1"
                 :
                 : "{v[0:3]}"(vp[0]), "{v[4:7]}"(vp[1]), "{v[8:11]}"(vp[2]), "{v[12:15]}"(vp[3]), "{v[16:19]}"(vp[4]), "{v20}"(vl), "{s[56:59]}"(srdA), "{s[60:61]}"(sd), "{s62}"(tag),
                   "{s[8:11]}"(sa[0]), "{s[12:15]}"(sa[1]), "{s[16:19]}"(sa[2]), "{s[20:23]}"(sa[3]), "{s24}"(sa[4][0])
                 : "memory", "m0", "vcc", "scc", ATT_VCLOB, ATT_SCLOB, ACLOB_LO, ACLOB_HI);
    {
        unsigned b2 = b, w2 = wid;
        asm volatile("s_waitcnt vmcnt(0)" : "+s"(ka), "+s"(b2), "+s"(w2), "+s"(ws) :: "memory");
        const unsigned j2 = b2 >> 5, p2 = w2 & 1u, h2 = (b2 & 7u) * 4u + ((b2 >> 3) & 3u);
#pragma unroll 1
        for (unsigned i = w2 & 2u; i < 2u + 3u * (w2 & 2u); ++i)
            att_exact(ka[0], ka[2], ws, h2, (i < 4u ? 1920u - 128u * j2 : 128u * j2 - 128u) + 32u * i + 16u * p2, *reinterpret_cast<const float*>(lds + LDS_G + (32u * i + 16u * p2 + (lane_id() & 15u)) * 4u));
        asm volatile("s_waitcnt vmcnt(0)" ::: "memory");
        unsigned* fa = (unsigned*)(ws + WS_F) + F_R + (8u * j2 + (b2 & 7u)) * 256u + (2u + p2) * 64u + ((b2 >> 3) & 3u);
        if (w2 & 2u) st_flag(fa, tag);
        wait_flags(fa, ld_flag(fa), tag);
        const unsigned l2 = lane_id(); u32x4 c[3]; out_consts(c, ka[7], ka[8], ka[10], b2, w2, l2);
        asm volatile("s_waitcnt vmcnt(0) lgkmcnt(0)\n\ts_nop 7\n\ts_branch L_att_stub" :: "{v[0:3]}"(c[0]), "{v[4:7]}"(c[1]), "{v[8:11]}"(c[2]), "{v20}"(l2 * 16u), "{s[56:59]}"(SRDA), "{s60}"(SD(8u * j2, h2, w2)),
                     "{s61}"(SD(120u - 8u * j2, h2, w2)), "{s62}"(tag) : "memory");
    }
    RTS(5);
}

extern "C" void kernel_launch(void* const* d_in, const int* in_sizes, int n_in, void* d_out, int out_size, void* d_ws, size_t ws_size,
                              hipStream_t stream) {
    (void)hipFuncSetAttribute((const void*)fused_kernel, hipFuncAttributeMaxDynamicSharedMemorySize, att::LDS_BYTES);
    fused_kernel<<<dim3(256), 256, att::LDS_BYTES, stream>>>((const u16*)d_in[0], (const u16*)d_in[4], (const u16*)d_in[5], (const u16*)d_in[6], (const u16*)d_in[7], (const u16*)d_in[1],
                                                             (const u16*)d_in[9], (const u16*)d_in[2], (const u16*)d_in[8], (char*)d_ws, (u16*)d_out);
}
```

```cpp
#include <hip/hip_runtime.h>
#include <hip/hip_bf16.h>
#include <stdint.h>
#include <utility>

typedef short bf16x8 __attribute__((ext_vector_type(8)));
typedef short s16x4 __attribute__((ext_vector_type(4)));
typedef float f32x16 __attribute__((ext_vector_type(16)));
typedef float f32x4 __attribute__((ext_vector_type(4)));
typedef unsigned u32x4 __attribute__((ext_vector_type(4)));
typedef unsigned u32x2 __attribute__((ext_vector_type(2)));
typedef unsigned short u16;

constexpr int S = 2048, DM = 576, NH = 32, HD = 128, DC = 512, NQKV = 1728, DHA = 18;
constexpr int NBIG = NH * HD;
constexpr int KS_TOT = 146;

#define SBAR() __builtin_amdgcn_sched_barrier(0)
constexpr size_t WS_Q = 0, WS_K = WS_Q + (size_t)S * NBIG * 2, WS_V = WS_K + (size_t)S * NBIG * 2, WS_AF = WS_V + (size_t)S * NBIG * 2, WS_X = WS_AF + (size_t)128 * KS_TOT * 1024,
                 WS_P = WS_X + (size_t)64 * 16 * 9216, WS_F = WS_P + (size_t)128 * (4 * 32 * 256 + 1024) * 4;
constexpr size_t WS_L = (size_t)96 << 20;
static_assert(WS_F == 96206848, "workspace map is baked into the generated asm");
constexpr int F_EPOCH = 0, F_J = 1024, F_ST = 8192, F_KV = 40960, F_R = 65536;
constexpr unsigned TAG_STEP = 0x9E3779B1u;
__device__ __forceinline__ unsigned ld_flag(const unsigned* p) { return __hip_atomic_load(p, __ATOMIC_RELAXED, __HIP_MEMORY_SCOPE_AGENT); }
__device__ __forceinline__ void st_flag(unsigned* p, unsigned v) { __hip_atomic_store(p, v, __ATOMIC_RELAXED, __HIP_MEMORY_SCOPE_AGENT); }
__device__ __forceinline__ unsigned ld_flag_async(const unsigned* p) { unsigned v; asm volatile("global_load_dword %0, %1, off sc1" : "=v"(v) : "v"(p) : "memory"); return v; }
#define WAITV(v, N) asm volatile("s_waitcnt vmcnt(" #N ")" : "+v"(v) :: "memory")
__device__ __forceinline__ void wait_flags(const unsigned* p, unsigned v, unsigned tag) {
    if (__builtin_expect(__builtin_amdgcn_ballot_w64(v != tag) != 0ull, 0)) {
        unsigned spins = 0;
        do { __builtin_amdgcn_s_sleep(1); } while (__builtin_amdgcn_ballot_w64(ld_flag(p) != tag) != 0ull && ++spins < (1u << 22));
    }
    asm volatile("" ::: "memory");
}

__device__ __forceinline__ unsigned cvtpk(float lo, float hi) {
    unsigned r; asm volatile("v_cvt_pk_bf16_f32 %0, %1, %2" : "=v"(r) : "v"(lo), "v"(hi)); return r;
}
__device__ __forceinline__ float bf2f(u16 v) { return __uint_as_float(((unsigned)v) << 16); }
__device__ __forceinline__ int crow(int r, int hi) { return (r & 3) + 8 * (r >> 2) + 4 * hi; }

typedef __attribute__((address_space(3))) s16x4* lds_s16x4_p;
__device__ __forceinline__ s16x4 trrd(unsigned addr) { return __builtin_amdgcn_ds_read_tr16_b64_v4i16((lds_s16x4_p)addr); }

__device__ __forceinline__ unsigned lane_id() { unsigned l; asm volatile("v_mbcnt_lo_u32_b32 %0, -1, 0\n\tv_mbcnt_hi_u32_b32 %0, -1, %0" : "=v"(l)); return l; }
#define RTS(slot) do {} while (0)
#define CT_END 0x1e380
#define ACLOB_LO "a0","a1","a2","a3","a4","a5","a6","a7","a8","a9","a10","a11","a12","a13","a14","a15","a16","a17","a18","a19","a20","a21","a22","a23","a24","a25","a26","a27","a28","a29","a30","a31","a32","a33","a34","a35","a36","a37","a38","a39","a40","a41","a42","a43","a44","a45","a46","a47","a48","a49","a50","a51","a52","a53","a54","a55","a56","a57","a58","a59","a60","a61","a62","a63","a64","a65","a66","a67","a68","a69","a70","a71","a72","a73","a74","a75","a76","a77","a78","a79","a80","a81","a82","a83","a84","a85","a86","a87","a88","a89","a90","a91","a92","a93","a94","a95","a96","a97","a98","a99","a100","a101","a102","a103","a104","a105","a106","a107","a108","a109","a110","a111","a112","a113","a114","a115","a116","a117","a118","a119","a120","a121","a122","a123","a124","a125","a126","a127"
#define ACLOB_HI "a128","a129","a130","a131","a132","a133","a134","a135","a136","a137","a138","a139","a140","a141","a142","a143","a144","a145","a146","a147","a148","a149","a150","a151","a152","a153","a154","a155","a156","a157","a158","a159","a160","a161","a162","a163","a164","a165","a166","a167","a168","a169","a170","a171","a172","a173","a174","a175","a176","a177","a178","a179","a180","a181","a182","a183","a184","a185","a186","a187","a188","a189","a190","a191","a192","a193","a194","a195","a196","a197","a198","a199","a200","a201","a202","a203","a204","a205","a206","a207","a208","a209","a210","a211","a212","a213","a214","a215","a216","a217","a218","a219","a220","a221","a222","a223","a224","a225","a226","a227","a228","a229","a230","a231","a232","a233","a234","a235","a236","a237","a238","a239","a240","a241","a242","a243","a244","a245","a246","a247","a248","a249","a250","a251","a252","a253","a254","a255"
#define PLO(p) ((unsigned)(size_t)(p))
#define PHI(p) ((unsigned)((size_t)(p) >> 32))
__device__ __forceinline__ unsigned vsel(bool c, unsigned a, unsigned b) { asm("" : "+v"(a), "+v"(b)); return c ? a : b; }
__device__ __forceinline__ unsigned ssel(unsigned c, unsigned a, unsigned b) { unsigned r; asm("s_cmp_lg_u32 %1, 0\n\ts_cselect_b32 %0, %2, %3" : "=s"(r) : "s"(c), "s"(a), "s"(b) : "scc"); return r; }
template <int N> __device__ __forceinline__ unsigned sslt(unsigned c, unsigned a, unsigned b) { unsigned r; asm("s_cmpk_lt_u32 %1, %4\n\ts_cselect_b32 %0, %2, %3" : "=s"(r) : "s"(c), "s"(a), "s"(b), "n"(N) : "scc"); return r; }
namespace att {
constexpr int LDS_BYTES = 163840;
constexpr unsigned LDS_G = 159744u, LDS_L = 161792u;
}
#define SD(J8, H, W) ((unsigned)WS_AF + (((J8) + 2u * ((W) & 2u) + ((W) & 1u)) * KS_TOT + 18u + 4u * (H)) * 1024u)
#define SRDA (u32x4){PLO(ws), PHI(ws) & 0xffffu, (unsigned)WS_L, 0x00020000u}
#define PROJ_ASM \
".p2align 3\n" \
"L_pb:\n" \
"s_waitcnt lgkmcnt(0)\n" \
"s_mov_b32 s64, s10\n" \
"s_and_b32 s65, s11, 0xffff\n" \
"s_mov_b32 s66, 0x200000\n" \
"s_mov_b32 s67, 0x20000\n" \
"s_mov_b32 s68, s14\n" \
"s_and_b32 s69, s15, 0xffff\n" \
"s_mov_b32 s70, 0x400000\n" \
"s_mov_b32 s71, 0x20000\n" \
"s_mov_b32 s72, s16\n" \
"s_and_b32 s73, s17, 0xffff\n" \
"s_mov_b32 s74, 0x400000\n" \
"s_mov_b32 s75, 0x20000\n" \
"s_add_u32 m0, s30, 0x0\n" \
"s_mov_b32 s85, 0x0\n" \
"buffer_load_dwordx4 v22, s[64:67], s85 offen lds\n" \
"s_add_u32 m0, s30, 0x400\n" \
"s_mov_b32 s84, 0x2000\n" \
"buffer_load_dwordx4 v23, s[64:67], s84 offen lds\n" \
"s_add_u32 m0, s31, 0x0\n" \
"s_mov_b32 s85, 0x0\n" \
"buffer_load_dwordx4 v26, s[68:71], s85 offen lds\n" \
"s_add_u32 m0, s31, 0x4000\n" \
"s_mov_b32 s84, 0x0\n" \
"s_nop 0\n" \
"buffer_load_dwordx4 v26, s[72:75], s84 offen lds\n" \
"s_add_u32 m0, s30, 0x800\n" \
"s_mov_b32 s85, 0x4000\n" \
"buffer_load_dwordx4 v22, s[64:67], s85 offen lds\n" \
"s_add_u32 m0, s30, 0xc00\n" \
"s_mov_b32 s84, 0x6000\n" \
"buffer_load_dwordx4 v23, s[64:67], s84 offen lds\n" \
"s_add_u32 m0, s31, 0x400\n" \
"s_mov_b32 s85, 0x8000\n" \
"buffer_load_dwordx4 v26, s[68:71], s85 offen lds\n" \
"s_add_u32 m0, s31, 0x4400\n" \
"s_mov_b32 s84, 0x8000\n" \
"buffer_load_dwordx4 v26, s[72:75], s84 offen lds\n" \
"s_movk_i32 s7, 0x300\n" \
"s_lshr_b32 s4, s2, 1\n" \
"s_and_b32 s4, s4, 60\n" \
"s_or_b32 s4, s4, s3\n" \
"v_lshrrev_b32_e32 v212, 4, v20\n" \
"v_min_u32_e32 v212, 59, v212\n" \
"s_cmpk_lt_u32 s2, 0x80\n" \
"s_cselect_b32 s6, s14, s16\n" \
"v_subrev_u32_e32 v214, 48, v212\n" \
"v_mov_b32_e32 v212, s10\n" \
"v_mov_b32_e32 v215, s6\n" \
"s_cmpk_lt_u32 s2, 0x80\n" \
"s_cselect_b32 s5, s15, s17\n" \
"v_cmp_gt_u32_e32 vcc, s7, v20\n" \
"v_mov_b32_e64 v213, s11\n" \
"v_bfe_u32 v216, v20, 4, 3\n" \
"v_cndmask_b32_e32 v212, v212, v215, vcc\n" \
"v_mov_b32_e32 v215, s5\n" \
"s_lshl_b32 s5, s3, 4\n" \
"v_cndmask_b32_e32 v213, v213, v215, vcc\n" \
"v_lshlrev_b32_e64 v215, 5, v20\n" \
"v_and_b32_e32 v215, 0x7000, v215\n" \
"v_lshl_add_u32 v215, s4, 6, v215\n" \
"s_lshl_b32 s4, s2, 3\n" \
"s_and_b32 s4, s4, 56\n" \
"v_or3_b32 v215, v215, s4, v216\n" \
"s_lshl_b32 s4, s2, 6\n" \
"s_or_b32 s6, s5, s4\n" \
"v_cmp_gt_u32_e64 s[4:5], 6, v214\n" \
"v_add_u32_e32 v215, 0x2000, v215\n" \
"s_nop 0\n" \
"v_cndmask_b32_e64 v216, 4, 2, s[4:5]\n" \
"v_add3_u32 v214, s6, v214, v216\n" \
"s_lshl_b32 s4, s3, 9\n" \
"v_cndmask_b32_e32 v214, v214, v215, vcc\n" \
"v_lshlrev_b32_e32 v214, 7, v214\n" \
"v_mov_b32_e64 v215, 0\n" \
"v_lshl_add_u64 v[184:185], v[212:213], 0, v[214:215]\n" \
"s_or_b32 s32, s4, 0x25000\n" \
"s_add_u32 m0, s30, 0x1000\n" \
"s_mov_b32 s85, 0x8000\n" \
"buffer_load_dwordx4 v22, s[64:67], s85 offen lds\n" \
"s_and_b32 s33, s3, 1\n" \
"v_lshrrev_b32_e32 v212, 4, v20\n" \
"v_lshrrev_b32_e64 v214, 8, v20\n" \
"v_bfe_u32 v212, v212, 1, 3\n" \
"v_lshlrev_b32_e64 v215, 2, v20\n" \
"s_add_u32 m0, s30, 0x1400\n" \
"s_mov_b32 s84, 0xa000\n" \
"buffer_load_dwordx4 v23, s[64:67], s84 offen lds\n" \
"v_lshrrev_b32_e64 v216, 1, v20\n" \
"v_bfe_u32 v213, v20, 4, 4\n" \
"s_lshl_b32 s4, s3, 5\n" \
"v_xor_b32_e32 v212, v212, v214\n" \
"v_lshlrev_b32_e64 v214, 11, v214\n" \
"s_add_u32 m0, s31, 0x800\n" \
"s_mov_b32 s85, 0x10000\n" \
"buffer_load_dwordx4 v27, s[68:71], s85 offen lds\n" \
"v_and_b32_e32 v215, 0x300, v215\n" \
"v_and_b32_e64 v217, 24, v216\n" \
"v_and_or_b32 v213, s4, 64, v213\n" \
"v_or3_b32 v214, v215, v214, v217\n" \
"s_lshl_b32 s4, s33, 14\n" \
"s_add_u32 m0, s31, 0x4800\n" \
"s_mov_b32 s84, 0x10000\n" \
"buffer_load_dwordx4 v27, s[72:75], s84 offen lds\n" \
"v_and_b32_e32 v215, 0xe0, v216\n" \
"v_lshlrev_b32_e32 v213, 7, v213\n" \
"v_lshlrev_b32_e64 v212, 4, v212\n" \
"v_or3_b32 v214, v214, s4, v215\n" \
"v_or_b32_e64 v28, v212, v213\n" \
"s_add_u32 m0, s30, 0x1800\n" \
"s_mov_b32 s85, 0xc000\n" \
"buffer_load_dwordx4 v22, s[64:67], s85 offen lds\n" \
"v_add_u32_e32 v32, 0x10000, v214\n" \
"v_bitop3_b32 v29, v212, 64, v213 bitop3:0x36\n" \
"v_xor_b32_e32 v33, 0x20, v32\n" \
"v_xor_b32_e32 v34, 0x40, v32\n" \
"v_xor_b32_e32 v35, 0x60, v32\n" \
"s_add_u32 m0, s30, 0x1c00\n" \
"s_mov_b32 s84, 0xe000\n" \
"buffer_load_dwordx4 v23, s[64:67], s84 offen lds\n" \
"v_xor_b32_e32 v36, 0x80, v32\n" \
"v_xor_b32_e32 v37, 0xa0, v32\n" \
"v_xor_b32_e32 v38, 0xc0, v32\n" \
"v_xor_b32_e32 v39, 0xe0, v32\n" \
"s_mov_b32 s80, s26\n" \
"s_add_u32 m0, s31, 0xc00\n" \
"s_mov_b32 s85, 0x18000\n" \
"buffer_load_dwordx4 v27, s[68:71], s85 offen lds\n" \
"s_and_b32 s81, s27, 0xffff\n" \
"s_mov_b32 s82, 0x10000000\n" \
"s_mov_b32 s83, 0x20000\n" \
"s_mov_b32 s76, s18\n" \
"s_and_b32 s77, s19, 0xffff\n" \
"s_add_u32 m0, s31, 0x4c00\n" \
"s_mov_b32 s84, 0x18000\n" \
"buffer_load_dwordx4 v27, s[72:75], s84 offen lds\n" \
"s_mov_b32 s78, 0x1e6000\n" \
"s_mov_b32 s79, 0x20000\n" \
"s_branch L_pgo_%=\n" \
"L_pret:\n" \
"s_setpc_b64 s[88:89]\n" \
"L_pgo_%=:\n" \
"s_mul_i32 s88, s33, 0xb000\n" \
"s_lshl_b32 s89, s3, 11\n" \
"s_and_b32 s89, s89, 0x1000\n" \
"s_add_u32 s88, s88, s89\n" \
"s_getpc_b64 s[86:87]\n" \
"L_hpa_%=:\n" \
"s_add_u32 s86, s86, L_pp1-L_hpa_%=\n" \
"s_addc_u32 s87, s87, 0\n" \
"s_add_u32 s86, s86, s88\n" \
"s_addc_u32 s87, s87, 0\n" \
"s_add_u32 m0, s30, 0x8000\n" \
"s_mov_b32 s85, 0x80\n" \
"buffer_load_dwordx4 v22, s[64:67], s85 offen lds\n" \
"s_lshr_b32 s4, s2, 1\n" \
"s_and_b32 s4, s4, 0x7c\n" \
"v_bfe_u32 v214, v20, 4, 5\n" \
"s_add_u32 m0, s30, 0x8400\n" \
"s_mov_b32 s84, 0x2080\n" \
"buffer_load_dwordx4 v23, s[64:67], s84 offen lds\n" \
"s_or_b32 s7, s4, s3\n" \
"v_min_u32_e64 v214, 26, v214\n" \
"v_mad_u64_u32 v[214:215], s[4:5], s7, 27, v[214:215]\n" \
"s_add_u32 m0, s31, 0x8000\n" \
"s_mov_b32 s85, 0x80000\n" \
"buffer_load_dwordx4 v26, s[68:71], s85 offen lds\n" \
"v_mul_u32_u24_e32 v215, 0xaaab, v214\n" \
"v_lshrrev_b32_e64 v215, 18, v215\n" \
"s_mov_b32 s4, 0x1fffffa\n" \
"s_add_u32 m0, s31, 0xc000\n" \
"s_mov_b32 s84, 0x80000\n" \
"buffer_load_dwordx4 v26, s[72:75], s84 offen lds\n" \
"v_mad_u64_u32 v[216:217], s[4:5], v215, s4, v[214:215]\n" \
"s_and_b32 s6, s2, 7\n" \
"v_mul_u32_u24_e32 v215, 27, v215\n" \
"s_mov_b64 exec, 0\n" \
"s_swappc_b64 s[88:89], s[86:87]\n" \
"s_mov_b64 exec, -1\n" \
"s_add_u32 s86, s86, 0x2000\n" \
"s_addc_u32 s87, s87, 0\n" \
"s_add_u32 m0, s30, 0x8800\n" \
"s_mov_b32 s85, 0x4080\n" \
"buffer_load_dwordx4 v22, s[64:67], s85 offen lds\n" \
"v_lshrrev_b32_e32 v216, 1, v216\n" \
"v_and_b32_e32 v214, 1, v214\n" \
"v_mov_b32_e64 v213, s13\n" \
"s_add_u32 m0, s30, 0x8c00\n" \
"s_mov_b32 s84, 0x6080\n" \
"buffer_load_dwordx4 v23, s[64:67], s84 offen lds\n" \
"v_lshrrev_b32_e32 v212, 4, v20\n" \
"v_mul_u32_u24_e32 v216, 9, v216\n" \
"v_add3_u32 v214, v214, s6, v215\n" \
"s_mov_b64 exec, 0\n" \
"s_swappc_b64 s[88:89], s[86:87]\n" \
"s_mov_b64 exec, -1\n" \
"s_add_u32 s86, s86, 0x2000\n" \
"s_addc_u32 s87, s87, 0\n" \
"s_add_u32 m0, s31, 0x8400\n" \
"s_mov_b32 s85, 0x88000\n" \
"buffer_load_dwordx4 v26, s[68:71], s85 offen lds\n" \
"v_add_lshl_u32 v214, v214, v216, 7\n" \
"v_mad_u64_u32 v[216:217], s[4:5], s7, 36, v[212:213]\n" \
"s_movk_i32 s4, 0x240\n" \
"s_add_u32 m0, s31, 0xc400\n" \
"s_mov_b32 s84, 0x88000\n" \
"buffer_load_dwordx4 v26, s[72:75], s84 offen lds\n" \
"v_mov_b32_e32 v219, s9\n" \
"v_mov_b32_e32 v215, 0\n" \
"v_cmp_gt_u32_e32 vcc, s4, v20\n" \
"s_mov_b64 exec, 0\n" \
"s_swappc_b64 s[88:89], s[86:87]\n" \
"s_mov_b64 exec, -1\n" \
"s_add_u32 s86, s86, 0x2000\n" \
"s_addc_u32 s87, s87, 0\n" \
"s_add_u32 m0, s30, 0x9000\n" \
"s_mov_b32 s85, 0x8080\n" \
"buffer_load_dwordx4 v22, s[64:67], s85 offen lds\n" \
"s_lshl_b32 s4, s2, 2\n" \
"v_lshl_add_u64 v[186:187], s[18:19], 0, v[214:215]\n" \
"v_mov_b32_e32 v214, s12\n" \
"s_add_u32 m0, s30, 0x9400\n" \
"s_mov_b32 s84, 0xa080\n" \
"buffer_load_dwordx4 v23, s[64:67], s84 offen lds\n" \
"v_mov_b32_e32 v217, s8\n" \
"s_or_b32 s4, s4, s3\n" \
"v_cndmask_b32_e64 v219, v219, v213, vcc\n" \
"s_add_u32 m0, s31, 0x8800\n" \
"s_mov_b32 s85, 0x90000\n" \
"buffer_load_dwordx4 v27, s[68:71], s85 offen lds\n" \
"v_and_b32_e32 v213, 0x3ff8, v216\n" \
"s_mul_i32 s4, s4, 18\n" \
"v_cndmask_b32_e32 v218, v217, v214, vcc\n" \
"s_mov_b64 exec, 0\n" \
"s_swappc_b64 s[88:89], s[86:87]\n" \
"s_mov_b64 exec, -1\n" \
"s_add_u32 s86, s86, 0x2000\n" \
"s_addc_u32 s87, s87, 0\n" \
"s_add_u32 m0, s31, 0xc800\n" \
"s_mov_b32 s84, 0x90000\n" \
"buffer_load_dwordx4 v27, s[72:75], s84 offen lds\n" \
"v_or_b32_e32 v213, s6, v213\n" \
"v_and_b32_e32 v214, 7, v216\n" \
"v_min_u32_e64 v212, 53, v212\n" \
"s_add_u32 m0, s30, 0x9800\n" \
"s_mov_b32 s85, 0xc080\n" \
"buffer_load_dwordx4 v22, s[64:67], s85 offen lds\n" \
"s_sub_i32 s4, s4, 36\n" \
"v_lshl_or_b32 v213, v213, 3, v214\n" \
"v_add_u32_e32 v212, s4, v212\n" \
"s_mov_b64 exec, 0\n" \
"s_swappc_b64 s[88:89], s[86:87]\n" \
"s_mov_b64 exec, -1\n" \
"s_add_u32 s86, s86, 0x2000\n" \
"s_addc_u32 s87, s87, 0\n" \
"s_add_u32 m0, s30, 0x9c00\n" \
"s_mov_b32 s84, 0xe080\n" \
"buffer_load_dwordx4 v23, s[64:67], s84 offen lds\n" \
"v_cndmask_b32_e32 v212, v212, v213, vcc\n" \
"v_lshlrev_b32_e32 v214, 7, v212\n" \
"v_lshl_add_u64 v[188:189], v[218:219], 0, v[214:215]\n" \
"s_add_u32 m0, s31, 0x8c00\n" \
"s_mov_b32 s85, 0x98000\n" \
"buffer_load_dwordx4 v27, s[68:71], s85 offen lds\n" \
"s_movk_i32 s5, 0xd80\n" \
"s_lshr_b32 s6, s2, 5\n" \
"v_lshrrev_b32_e64 v212, 4, v20\n" \
"s_add_u32 m0, s31, 0xcc00\n" \
"s_mov_b32 s84, 0x98000\n" \
"buffer_load_dwordx4 v27, s[72:75], s84 offen lds\n" \
"v_lshl_or_b32 v215, s3, 6, v212\n" \
"v_mul_u32_u24_e32 v216, 0x2aab, v215\n" \
"v_lshrrev_b32_e32 v216, 16, v216\n" \
"s_mov_b32 m0, s32\n" \
"s_mov_b32 exec_lo, 0xffffff\n" \
"s_mov_b32 exec_hi, 0xfff0000\n" \
"global_load_lds_dword v[184:185], off\n" \
"s_mov_b64 exec, -1\n" \
"s_mov_b32 s84, 0x5bc0000\n" \
"buffer_load_dword v180, off, s[80:83], s84 sc1\n" \
"s_cmp_lg_u32 s33, 0\n" \
"s_cbranch_scc1 L_w1_%=\n" \
"L_w0_%=:\n" \
"s_waitcnt vmcnt(18)\n" \
"s_barrier\n" \
"s_nop 0\n" \
"ds_read_b64_tr_b16 v[84:85], v32 offset:0\n" \
"ds_read_b64_tr_b16 v[86:87], v32 offset:1024\n" \
"ds_read_b64_tr_b16 v[148:149], v32 offset:8192\n" \
"ds_read_b64_tr_b16 v[150:151], v32 offset:9216\n" \
"ds_read_b128 v[52:55], v28 offset:16384\n" \
"ds_read_b128 v[116:119], v29 offset:16384\n" \
"ds_read_b64_tr_b16 v[88:89], v33 offset:0\n" \
"ds_read_b64_tr_b16 v[90:91], v33 offset:1024\n" \
"ds_read_b64_tr_b16 v[152:153], v33 offset:8192\n" \
"ds_read_b64_tr_b16 v[154:155], v33 offset:9216\n" \
"ds_read_b64_tr_b16 v[92:93], v34 offset:0\n" \
"ds_read_b64_tr_b16 v[94:95], v34 offset:1024\n" \
"ds_read_b64_tr_b16 v[156:157], v34 offset:8192\n" \
"ds_read_b64_tr_b16 v[158:159], v34 offset:9216\n" \
"ds_read_b64_tr_b16 v[96:97], v35 offset:0\n" \
"ds_read_b64_tr_b16 v[98:99], v35 offset:1024\n" \
"ds_read_b64_tr_b16 v[160:161], v35 offset:8192\n" \
"ds_read_b64_tr_b16 v[162:163], v35 offset:9216\n" \
"ds_read_b128 v[56:59], v28 offset:18432\n" \
"ds_read_b128 v[120:123], v29 offset:18432\n" \
"ds_read_b128 v[60:63], v28 offset:20480\n" \
"ds_read_b128 v[124:127], v29 offset:20480\n" \
"ds_read_b128 v[64:67], v28 offset:22528\n" \
"ds_read_b128 v[128:131], v29 offset:22528\n" \
"ds_read_b128 v[68:71], v28 offset:0\n" \
"ds_read_b128 v[132:135], v29 offset:0\n" \
"ds_read_b128 v[72:75], v28 offset:2048\n" \
"ds_read_b128 v[136:139], v29 offset:2048\n" \
"ds_read_b128 v[76:79], v28 offset:4096\n" \
"ds_read_b128 v[140:143], v29 offset:4096\n" \
"ds_read_b128 v[80:83], v28 offset:6144\n" \
"ds_read_b128 v[144:147], v29 offset:6144\n" \
"s_waitcnt lgkmcnt(15)\n" \
"v_mfma_f32_16x16x32_bf16 a[0:3], v[84:87], v[52:55], 0\n" \
"ds_read_b64_tr_b16 v[100:101], v36 offset:0\n" \
"v_mad_i32_i24 v217, v216, -6, v215\n" \
"v_mfma_f32_16x16x32_bf16 a[0:3], v[148:151], v[116:119], a[0:3]\n" \
"v_add_u32_e32 v218, 0xfffffe50, v215\n" \
"s_mul_i32 s35, s6, 0x48\n" \
"v_mfma_f32_16x16x32_bf16 a[4:7], v[88:91], v[52:55], 0\n" \
"v_lshrrev_b32_e32 v219, 1, v217\n" \
"v_add_u32_e64 v218, s35, v216\n" \
"v_mfma_f32_16x16x32_bf16 a[4:7], v[152:155], v[116:119], a[4:7]\n" \
"ds_read_b64_tr_b16 v[102:103], v36 offset:1024\n" \
"v_mul_u32_u24_e32 v219, 0x480, v219\n" \
"v_mfma_f32_16x16x32_bf16 a[8:11], v[92:95], v[52:55], 0\n" \
"v_and_b32_e64 v214, 16, v20\n" \
"v_mad_u32_u24 v219, v218, s5, v219\n" \
"v_mfma_f32_16x16x32_bf16 a[8:11], v[156:159], v[116:119], a[8:11]\n" \
"v_or_b32_e32 v192, v219, v214\n" \
"v_mov_b32_e32 v220, 2.0\n" \
"v_mfma_f32_16x16x32_bf16 a[12:15], v[96:99], v[52:55], 0\n" \
"ds_read_b64_tr_b16 v[164:165], v36 offset:8192\n" \
"v_mov_b32_e32 v222, 7\n" \
"s_waitcnt lgkmcnt(15)\n" \
"v_mfma_f32_16x16x32_bf16 a[12:15], v[160:163], v[116:119], a[12:15]\n" \
"v_lshlrev_b32_e32 v220, 7, v218\n" \
"v_lshrrev_b32_e32 v218, 1, v218\n" \
"v_mfma_f32_16x16x32_bf16 a[44:47], v[96:99], v[56:59], 0\n" \
"v_and_b32_e32 v218, 4, v218\n" \
"v_and_b32_e32 v216, 2, v216\n" \
"v_mfma_f32_16x16x32_bf16 a[44:47], v[160:163], v[120:123], a[44:47]\n" \
"ds_read_b64_tr_b16 v[166:167], v36 offset:9216\n" \
"s_mul_i32 s7, s6, 0x2400\n" \
"v_mfma_f32_16x16x32_bf16 a[40:43], v[92:95], v[56:59], 0\n" \
"s_sub_i32 s34, 0x22000, s7\n" \
"v_bitop3_b32 v216, v217, v218, v216 bitop3:0x1e\n" \
"v_mfma_f32_16x16x32_bf16 a[40:43], v[156:159], v[120:123], a[40:43]\n" \
"v_or_b32_e32 v213, 0x24400, v20\n" \
"v_lshlrev_b32_e64 v216, 4, v216\n" \
"v_mfma_f32_16x16x32_bf16 a[36:39], v[88:91], v[56:59], 0\n" \
"v_add3_u32 v196, v220, s34, v216\n" \
"v_mov_b32_e64 v216, v213\n" \
"v_mfma_f32_16x16x32_bf16 a[36:39], v[152:155], v[120:123], a[36:39]\n" \
"ds_read_b64_tr_b16 v[104:105], v37 offset:0\n" \
"s_movk_i32 s36, 0x1b0\n" \
"v_mfma_f32_16x16x32_bf16 a[32:35], v[84:87], v[56:59], 0\n" \
"v_or_b32_e32 v216, 0x100, v215\n" \
"v_mul_u32_u24_e32 v217, 0x2aab, v216\n" \
"v_mfma_f32_16x16x32_bf16 a[32:35], v[148:151], v[120:123], a[32:35]\n" \
"v_add_u32_e32 v215, 0xffffff50, v215\n" \
"v_lshrrev_b32_e32 v217, 16, v217\n" \
"s_waitcnt lgkmcnt(15)\n" \
"v_mfma_f32_16x16x32_bf16 a[64:67], v[84:87], v[60:63], 0\n" \
"s_mov_b32 m0, s32\n" \
"ds_read_b64_tr_b16 v[106:107], v37 offset:1024\n" \
"v_mfma_f32_16x16x32_bf16 a[64:67], v[148:151], v[124:127], a[64:67]\n" \
"s_mov_b32 exec_lo, 0xff000000\n" \
"s_mov_b32 exec_hi, 0x0\n" \
"global_load_lds_dword v[184:185], off\n" \
"s_mov_b64 exec, -1\n" \
"v_mfma_f32_16x16x32_bf16 a[68:71], v[88:91], v[60:63], 0\n" \
"v_mov_b32_e64 v220, v215\n" \
"v_mad_i32_i24 v218, v217, -6, v216\n" \
"v_mfma_f32_16x16x32_bf16 a[68:71], v[152:155], v[124:127], a[68:71]\n" \
"ds_read_b64_tr_b16 v[168:169], v37 offset:8192\n" \
"v_cmp_gt_u32_e64 vcc, s36, v216\n" \
"v_mfma_f32_16x16x32_bf16 a[72:75], v[92:95], v[60:63], 0\n" \
"v_mov_b32_e32 v221, 7\n" \
"v_lshrrev_b32_e32 v222, 1, v218\n" \
"v_mfma_f32_16x16x32_bf16 a[72:75], v[156:159], v[124:127], a[72:75]\n" \
"s_mov_b32 m0, s32\n" \
"v_cndmask_b32_e32 v217, v220, v217, vcc\n" \
"v_mfma_f32_16x16x32_bf16 a[76:79], v[96:99], v[60:63], 0\n" \
"s_mov_b32 exec_lo, 0xffff\n" \
"s_mov_b32 exec_hi, 0x0\n" \
"global_load_lds_dword v[186:187], off\n" \
"s_mov_b64 exec, -1\n" \
"v_mfma_f32_16x16x32_bf16 a[76:79], v[160:163], v[124:127], a[76:79]\n" \
"ds_read_b64_tr_b16 v[170:171], v37 offset:9216\n" \
"s_movk_i32 s4, 0x48\n" \
"s_waitcnt lgkmcnt(15)\n" \
"v_mfma_f32_16x16x32_bf16 a[108:111], v[96:99], v[64:67], 0\n" \
"v_add_u32_e64 v220, s35, v217\n" \
"v_mul_u32_u24_e32 v222, 0x480, v222\n" \
"v_mfma_f32_16x16x32_bf16 a[108:111], v[160:163], v[128:131], a[108:111]\n" \
"v_mad_u32_u24 v222, v220, s5, v222\n" \
"v_cmp_gt_u32_e64 s[4:5], s4, v215\n" \
"v_mfma_f32_16x16x32_bf16 a[104:107], v[92:95], v[64:67], 0\n" \
"ds_read_b64_tr_b16 v[108:109], v38 offset:0\n" \
"v_cndmask_b32_e64 v215, v221, v218, vcc\n" \
"v_mfma_f32_16x16x32_bf16 a[104:107], v[156:159], v[128:131], a[104:107]\n" \
"v_lshrrev_b32_e32 v218, 1, v220\n" \
"v_or_b32_e32 v214, v222, v214\n" \
"v_mfma_f32_16x16x32_bf16 a[100:103], v[88:91], v[64:67], 0\n" \
"v_mov_b32_e32 v222, 2.0\n" \
"v_and_b32_e32 v218, 4, v218\n" \
"v_mfma_f32_16x16x32_bf16 a[100:103], v[152:155], v[128:131], a[100:103]\n" \
"ds_read_b64_tr_b16 v[110:111], v38 offset:1024\n" \
"v_and_b32_e64 v217, 2, v217\n" \
"v_mfma_f32_16x16x32_bf16 a[96:99], v[84:87], v[64:67], 0\n" \
"v_bitop3_b32 v215, v215, v218, v217 bitop3:0x1e\n" \
"v_lshlrev_b32_e64 v223, 1, v220\n" \
"v_mfma_f32_16x16x32_bf16 a[96:99], v[148:151], v[128:131], a[96:99]\n" \
"v_mov_b32_e32 v224, 2.0\n" \
"v_cndmask_b32_e32 v193, v222, v214, vcc\n" \
"v_mfma_f32_16x16x32_bf16 a[128:131], v[84:87], v[68:71], 0\n" \
"ds_read_b64_tr_b16 v[172:173], v38 offset:8192\n" \
"v_lshlrev_b32_e32 v214, 7, v220\n" \
"s_waitcnt lgkmcnt(15)\n" \
"v_mfma_f32_16x16x32_bf16 a[128:131], v[148:151], v[132:135], a[128:131]\n" \
"v_lshlrev_b32_e32 v215, 4, v215\n" \
"s_mul_i32 s39, s3, 18\n" \
"v_mfma_f32_16x16x32_bf16 a[132:135], v[88:91], v[68:71], 0\n" \
"v_add3_u32 v214, v214, s34, v215\n" \
"v_cndmask_b32_e64 v195, v224, v223, s[4:5]\n" \
"v_mfma_f32_16x16x32_bf16 a[132:135], v[152:155], v[132:135], a[132:135]\n" \
"ds_read_b64_tr_b16 v[174:175], v38 offset:9216\n" \
"s_movk_i32 s4, 0x1f8\n" \
"v_mfma_f32_16x16x32_bf16 a[136:139], v[92:95], v[68:71], 0\n" \
"v_mov_b32_e32 v215, v213\n" \
"s_add_i32 s35, s35, s39\n" \
"v_mfma_f32_16x16x32_bf16 a[136:139], v[156:159], v[132:135], a[136:139]\n" \
"v_cmp_gt_u32_e32 vcc, s4, v216\n" \
"v_add_u32_e64 v212, s35, v212\n" \
"v_mfma_f32_16x16x32_bf16 a[140:143], v[96:99], v[68:71], 0\n" \
"v_mov_b32_e32 v219, 2.0\n" \
"v_cndmask_b32_e32 v197, v215, v214, vcc\n" \
"v_mfma_f32_16x16x32_bf16 a[140:143], v[160:163], v[132:135], a[140:143]\n" \
"ds_read_b64_tr_b16 v[112:113], v39 offset:0\n" \
"s_movk_i32 s35, 0x120\n" \
"v_mfma_f32_16x16x32_bf16 a[172:175], v[96:99], v[72:75], 0\n" \
"v_mul_u32_u24_e32 v214, 0xd80, v212\n" \
"v_cmp_gt_u32_e32 vcc, s35, v20\n" \
"v_mfma_f32_16x16x32_bf16 a[172:175], v[160:163], v[136:139], a[172:175]\n" \
"v_lshrrev_b32_e32 v215, 1, v212\n" \
"s_and_b32 s4, s2, 7\n" \
"v_mfma_f32_16x16x32_bf16 a[168:171], v[92:95], v[72:75], 0\n" \
"ds_read_b64_tr_b16 v[114:115], v39 offset:1024\n" \
"v_cndmask_b32_e64 v194, v219, v214, vcc\n" \
"v_mfma_f32_16x16x32_bf16 a[168:171], v[156:159], v[136:139], a[168:171]\n" \
"v_lshlrev_b32_e32 v214, 7, v212\n" \
"v_lshrrev_b32_e32 v212, 2, v212\n" \
"v_mfma_f32_16x16x32_bf16 a[164:167], v[88:91], v[72:75], 0\n" \
"v_and_b32_e64 v212, 2, v212\n" \
"v_and_or_b32 v212, v215, 1, v212\n" \
"v_mfma_f32_16x16x32_bf16 a[164:167], v[152:155], v[136:139], a[164:167]\n" \
"ds_read_b64_tr_b16 v[176:177], v39 offset:8192\n" \
"s_lshl_b32 s5, s4, 2\n" \
"v_mfma_f32_16x16x32_bf16 a[160:163], v[84:87], v[72:75], 0\n" \
"s_bfe_u32 s38, s2, 0x20003\n" \
"v_lshlrev_b32_e32 v212, 5, v212\n" \
"v_mfma_f32_16x16x32_bf16 a[160:163], v[148:151], v[136:139], a[160:163]\n" \
"s_movk_i32 s35, 0x60\n" \
"s_or_b32 s5, s5, s38\n" \
"s_waitcnt lgkmcnt(15)\n" \
"v_mfma_f32_16x16x32_bf16 a[192:195], v[84:87], v[76:79], 0\n" \
"ds_read_b64_tr_b16 v[178:179], v39 offset:9216\n" \
"v_bitop3_b32 v212, v212, s35, v214 bitop3:0x36\n" \
"v_mfma_f32_16x16x32_bf16 a[192:195], v[148:151], v[140:143], a[192:195]\n" \
"s_lshr_b32 s36, s2, 3\n" \
"v_add_u32_e64 v212, s34, v212\n" \
"v_mfma_f32_16x16x32_bf16 a[196:199], v[88:91], v[76:79], 0\n" \
"s_mul_i32 s34, s5, 0x12000\n" \
"s_or_b32 s40, s34, 0x6000000\n" \
"v_mfma_f32_16x16x32_bf16 a[196:199], v[152:155], v[140:143], a[196:199]\n" \
"s_mul_i32 s34, s36, 0x90000\n" \
"s_lshl_b32 s36, s3, 14\n" \
"v_mfma_f32_16x16x32_bf16 a[200:203], v[92:95], v[76:79], 0\n" \
"s_add_i32 s34, s34, s36\n" \
"s_lshl_b32 s38, s5, 8\n" \
"v_mfma_f32_16x16x32_bf16 a[200:203], v[156:159], v[140:143], a[200:203]\n" \
"s_add_i32 s41, s34, 0x4240000\n" \
"s_mul_i32 s34, s5, 36\n" \
"v_mfma_f32_16x16x32_bf16 a[204:207], v[96:99], v[76:79], 0\n" \
"s_add_i32 s5, s7, s40\n" \
"s_addk_i32 s5, 0x2000\n" \
"v_mfma_f32_16x16x32_bf16 a[204:207], v[160:163], v[140:143], a[204:207]\n" \
"s_mov_b32 s37, 2.0\n" \
"s_or_b32 s38, s38, 0x5be8000\n" \
"v_mfma_f32_16x16x32_bf16 a[236:239], v[96:99], v[80:83], 0\n" \
"s_cmp_lg_u32 s3, 0\n" \
"s_cselect_b32 s36, s37, s5\n" \
"v_mfma_f32_16x16x32_bf16 a[236:239], v[160:163], v[144:147], a[236:239]\n" \
"s_lshl_b32 s5, s6, 2\n" \
"s_or_b32 s5, s38, s5\n" \
"v_mfma_f32_16x16x32_bf16 a[232:235], v[92:95], v[80:83], 0\n" \
"s_lshl_b32 s35, s3, 10\n" \
"v_mfma_f32_16x16x32_bf16 a[232:235], v[156:159], v[144:147], a[232:235]\n" \
"s_cmpk_lt_u32 s3, 3\n" \
"s_cselect_b32 s37, s37, s5\n" \
"v_mfma_f32_16x16x32_bf16 a[228:231], v[88:91], v[80:83], 0\n" \
"s_lshl_b32 s5, s4, 16\n" \
"s_lshl_b32 s4, s4, 11\n" \
"v_mfma_f32_16x16x32_bf16 a[228:231], v[152:155], v[144:147], a[228:231]\n" \
"v_cndmask_b32_e32 v198, v213, v212, vcc\n" \
"v_or_b32_e32 v212, s35, v20\n" \
"v_mfma_f32_16x16x32_bf16 a[224:227], v[84:87], v[80:83], 0\n" \
"s_or_b32 s39, s40, s35\n" \
"s_or_b32 s55, s41, s4\n" \
"v_mfma_f32_16x16x32_bf16 a[224:227], v[148:151], v[144:147], a[224:227]\n" \
"s_waitcnt vmcnt(4) lgkmcnt(0)\n" \
"s_barrier\n" \
"v_mfma_f32_16x16x32_bf16 a[16:19], v[100:103], v[52:55], 0\n" \
"s_add_u32 m0, s30, 0x0\n" \
"s_nop 0\n" \
"s_mov_b32 s85, 0x100\n" \
"v_mfma_f32_16x16x32_bf16 a[16:19], v[164:167], v[116:119], a[16:19]\n" \
"buffer_load_dwordx4 v22, s[64:67], s85 offen lds\n" \
"v_mfma_f32_16x16x32_bf16 a[20:23], v[104:107], v[52:55], 0\n" \
"ds_read_b64_tr_b16 v[84:85], v32 offset:32768\n" \
"v_or_b32_e32 v199, 0x22000, v212\n" \
"v_mfma_f32_16x16x32_bf16 a[20:23], v[168:171], v[116:119], a[20:23]\n" \
"s_add_u32 m0, s30, 0x400\n" \
"s_mov_b32 s84, 0x2100\n" \
"v_mfma_f32_16x16x32_bf16 a[24:27], v[108:111], v[52:55], 0\n" \
"buffer_load_dwordx4 v23, s[64:67], s84 offen lds\n" \
"v_mfma_f32_16x16x32_bf16 a[24:27], v[172:175], v[116:119], a[24:27]\n" \
"ds_read_b64_tr_b16 v[86:87], v32 offset:33792\n" \
"s_add_i32 s35, s39, s7\n" \
"v_mfma_f32_16x16x32_bf16 a[28:31], v[112:115], v[52:55], 0\n" \
"ds_read_b64_tr_b16 v[148:149], v32 offset:40960\n" \
"s_add_i32 s54, s41, s5\n" \
"v_mfma_f32_16x16x32_bf16 a[28:31], v[176:179], v[116:119], a[28:31]\n" \
"ds_read_b128 v[52:55], v28 offset:49152\n" \
"s_add_i32 s55, s55, 0x80000\n" \
"v_mfma_f32_16x16x32_bf16 a[60:63], v[112:115], v[56:59], 0\n" \
"ds_read_b128 v[116:119], v29 offset:49152\n" \
"v_mov_b32_e32 v213, 0xffff\n" \
"v_mfma_f32_16x16x32_bf16 a[60:63], v[176:179], v[120:123], a[60:63]\n" \
"s_add_u32 m0, s31, 0x0\n" \
"s_nop 0\n" \
"s_mov_b32 s85, 0x100000\n" \
"v_mfma_f32_16x16x32_bf16 a[56:59], v[108:111], v[56:59], 0\n" \
"buffer_load_dwordx4 v26, s[68:71], s85 offen lds\n" \
"v_mfma_f32_16x16x32_bf16 a[56:59], v[172:175], v[120:123], a[56:59]\n" \
"s_add_u32 m0, s31, 0x4000\n" \
"s_mov_b32 s84, 0x100000\n" \
"v_mfma_f32_16x16x32_bf16 a[52:55], v[104:107], v[56:59], 0\n" \
"buffer_load_dwordx4 v26, s[72:75], s84 offen lds\n" \
"v_mfma_f32_16x16x32_bf16 a[52:55], v[168:171], v[120:123], a[52:55]\n" \
"s_add_u32 m0, s30, 0x800\n" \
"s_mov_b32 s85, 0x4100\n" \
"v_mfma_f32_16x16x32_bf16 a[48:51], v[100:103], v[56:59], 0\n" \
"buffer_load_dwordx4 v22, s[64:67], s85 offen lds\n" \
"v_mfma_f32_16x16x32_bf16 a[48:51], v[164:167], v[120:123], a[48:51]\n" \
"ds_read_b128 v[56:59], v28 offset:51200\n" \
"s_and_b32 s6, s3, 1\n" \
"v_mfma_f32_16x16x32_bf16 a[80:83], v[100:103], v[60:63], 0\n" \
"ds_read_b128 v[120:123], v29 offset:51200\n" \
"s_lshl_b32 s4, s6, 4\n" \
"v_mfma_f32_16x16x32_bf16 a[80:83], v[164:167], v[124:127], a[80:83]\n" \
"s_add_u32 m0, s30, 0xc00\n" \
"s_mov_b32 s84, 0x6100\n" \
"v_mfma_f32_16x16x32_bf16 a[84:87], v[104:107], v[60:63], 0\n" \
"buffer_load_dwordx4 v23, s[64:67], s84 offen lds\n" \
"v_mfma_f32_16x16x32_bf16 a[84:87], v[168:171], v[124:127], a[84:87]\n" \
"ds_read_b64_tr_b16 v[150:151], v32 offset:41984\n" \
"v_lshrrev_b32_e32 v214, 4, v20\n" \
".fill (L_pb + 3808 - .) / 4, 4, 0xBF800000\n" \
"L_pp1:\n" \
"s_cbranch_execz L_pret\n" \
"v_mfma_f32_16x16x32_bf16 a[88:91], v[108:111], v[60:63], 0\n" \
"s_add_u32 m0, s31, 0x400\n" \
"s_mov_b32 s85, 0x108000\n" \
"v_mfma_f32_16x16x32_bf16 a[88:91], v[172:175], v[124:127], a[88:91]\n" \
"buffer_load_dwordx4 v26, s[68:71], s85 offen lds\n" \
"v_mfma_f32_16x16x32_bf16 a[92:95], v[112:115], v[60:63], 0\n" \
"ds_read_b64_tr_b16 v[88:89], v33 offset:32768\n" \
"v_lshrrev_b32_e32 v215, 6, v20\n" \
"v_mfma_f32_16x16x32_bf16 a[92:95], v[176:179], v[124:127], a[92:95]\n" \
"ds_read_b128 v[60:63], v28 offset:53248\n" \
"v_and_b32_e64 v216, 12, v215\n" \
"v_mfma_f32_16x16x32_bf16 a[124:127], v[112:115], v[64:67], 0\n" \
"ds_read_b128 v[124:127], v29 offset:53248\n" \
"v_and_or_b32 v212, v214, 15, s4\n" \
"v_mfma_f32_16x16x32_bf16 a[124:127], v[176:179], v[128:131], a[124:127]\n" \
"s_add_u32 m0, s31, 0x4400\n" \
"s_mov_b32 s84, 0x108000\n" \
"v_mfma_f32_16x16x32_bf16 a[120:123], v[108:111], v[64:67], 0\n" \
"buffer_load_dwordx4 v26, s[72:75], s84 offen lds\n" \
"v_mfma_f32_16x16x32_bf16 a[120:123], v[172:175], v[128:131], a[120:123]\n" \
"s_add_u32 m0, s30, 0x1000\n" \
"s_mov_b32 s85, 0x8100\n" \
"v_mfma_f32_16x16x32_bf16 a[116:119], v[104:107], v[64:67], 0\n" \
"buffer_load_dwordx4 v22, s[64:67], s85 offen lds\n" \
"v_mfma_f32_16x16x32_bf16 a[116:119], v[168:171], v[128:131], a[116:119]\n" \
"ds_read_b64_tr_b16 v[90:91], v33 offset:33792\n" \
"v_cmp_gt_u32_e64 vcc, v216, v212\n" \
"v_mfma_f32_16x16x32_bf16 a[112:115], v[100:103], v[64:67], 0\n" \
"ds_read_b64_tr_b16 v[152:153], v33 offset:40960\n" \
"v_mov_b32_e32 v218, 0xffff0000\n" \
"v_mfma_f32_16x16x32_bf16 a[112:115], v[164:167], v[128:131], a[112:115]\n" \
"ds_read_b128 v[64:67], v28 offset:55296\n" \
"s_lshl_b32 s4, s2, 2\n" \
"s_nop 0\n" \
"v_mfma_f32_16x16x32_bf16 a[144:147], v[100:103], v[68:71], 0\n" \
"ds_read_b128 v[128:131], v29 offset:55296\n" \
"v_cndmask_b32_e64 v217, v213, 0, vcc\n" \
"v_mfma_f32_16x16x32_bf16 a[144:147], v[164:167], v[132:135], a[144:147]\n" \
"s_add_u32 m0, s30, 0x1400\n" \
"s_mov_b32 s84, 0xa100\n" \
"v_mfma_f32_16x16x32_bf16 a[148:151], v[104:107], v[68:71], 0\n" \
"buffer_load_dwordx4 v23, s[64:67], s84 offen lds\n" \
"v_mfma_f32_16x16x32_bf16 a[148:151], v[168:171], v[132:135], a[148:151]\n" \
"s_add_u32 m0, s31, 0x800\n" \
"s_mov_b32 s85, 0x110000\n" \
"v_mfma_f32_16x16x32_bf16 a[152:155], v[108:111], v[68:71], 0\n" \
"buffer_load_dwordx4 v27, s[68:71], s85 offen lds\n" \
"v_mfma_f32_16x16x32_bf16 a[152:155], v[172:175], v[132:135], a[152:155]\n" \
"s_add_u32 m0, s31, 0x4800\n" \
"s_mov_b32 s84, 0x110000\n" \
"v_mfma_f32_16x16x32_bf16 a[156:159], v[112:115], v[68:71], 0\n" \
"buffer_load_dwordx4 v27, s[72:75], s84 offen lds\n" \
"v_mfma_f32_16x16x32_bf16 a[156:159], v[176:179], v[132:135], a[156:159]\n" \
"ds_read_b128 v[68:71], v28 offset:32768\n" \
"v_cmp_lt_u32_e64 vcc, v216, v212\n" \
"v_mfma_f32_16x16x32_bf16 a[188:191], v[112:115], v[72:75], 0\n" \
"ds_read_b128 v[132:135], v29 offset:32768\n" \
"s_mov_b32 s7, 0\n" \
"v_mfma_f32_16x16x32_bf16 a[188:191], v[176:179], v[136:139], a[188:191]\n" \
"s_add_u32 m0, s30, 0x1800\n" \
"s_mov_b32 s85, 0xc100\n" \
"v_mfma_f32_16x16x32_bf16 a[184:187], v[108:111], v[72:75], 0\n" \
"buffer_load_dwordx4 v22, s[64:67], s85 offen lds\n" \
"v_mfma_f32_16x16x32_bf16 a[184:187], v[172:175], v[136:139], a[184:187]\n" \
"ds_read_b64_tr_b16 v[154:155], v33 offset:41984\n" \
"s_or_b32 s40, s4, s3\n" \
"v_mfma_f32_16x16x32_bf16 a[180:183], v[104:107], v[72:75], 0\n" \
"s_add_u32 m0, s30, 0x1c00\n" \
"s_mov_b32 s84, 0xe100\n" \
"v_mfma_f32_16x16x32_bf16 a[180:183], v[168:171], v[136:139], a[180:183]\n" \
"buffer_load_dwordx4 v23, s[64:67], s84 offen lds\n" \
"v_mfma_f32_16x16x32_bf16 a[176:179], v[100:103], v[72:75], 0\n" \
"ds_read_b64_tr_b16 v[92:93], v34 offset:32768\n" \
"v_cndmask_b32_e64 v219, 0, v218, vcc\n" \
"v_mfma_f32_16x16x32_bf16 a[176:179], v[164:167], v[136:139], a[176:179]\n" \
"ds_read_b128 v[72:75], v28 offset:34816\n" \
"v_or_b32_e64 v12, v217, v219\n" \
"v_mfma_f32_16x16x32_bf16 a[208:211], v[100:103], v[76:79], 0\n" \
"ds_read_b128 v[136:139], v29 offset:34816\n" \
"v_or_b32_e64 v217, 2, v216\n" \
"v_mfma_f32_16x16x32_bf16 a[208:211], v[164:167], v[140:143], a[208:211]\n" \
"ds_read_b64_tr_b16 v[94:95], v34 offset:33792\n" \
"v_cmp_gt_u32_e64 vcc, v217, v212\n" \
"v_mfma_f32_16x16x32_bf16 a[212:215], v[104:107], v[76:79], 0\n" \
"ds_read_b64_tr_b16 v[156:157], v34 offset:40960\n" \
"s_add_i32 s41, s40, 0xffffffaf\n" \
"v_mfma_f32_16x16x32_bf16 a[212:215], v[168:171], v[140:143], a[212:215]\n" \
"ds_read_b64_tr_b16 v[158:159], v34 offset:41984\n" \
"v_mfma_f32_16x16x32_bf16 a[216:219], v[108:111], v[76:79], 0\n" \
"ds_read_b64_tr_b16 v[96:97], v35 offset:32768\n" \
"v_mfma_f32_16x16x32_bf16 a[216:219], v[172:175], v[140:143], a[216:219]\n" \
"ds_read_b64_tr_b16 v[98:99], v35 offset:33792\n" \
"v_mfma_f32_16x16x32_bf16 a[220:223], v[112:115], v[76:79], 0\n" \
"ds_read_b64_tr_b16 v[160:161], v35 offset:40960\n" \
"v_mfma_f32_16x16x32_bf16 a[220:223], v[176:179], v[140:143], a[220:223]\n" \
"ds_read_b128 v[76:79], v28 offset:36864\n" \
"v_mfma_f32_16x16x32_bf16 a[252:255], v[112:115], v[80:83], 0\n" \
"ds_read_b128 v[140:143], v29 offset:36864\n" \
"v_mfma_f32_16x16x32_bf16 a[252:255], v[176:179], v[144:147], a[252:255]\n" \
"ds_read_b64_tr_b16 v[162:163], v35 offset:41984\n" \
"v_mfma_f32_16x16x32_bf16 a[248:251], v[108:111], v[80:83], 0\n" \
"s_cmpk_lt_u32 s40, 0x291\n" \
"s_cselect_b32 s7, s41, s7\n" \
"v_mfma_f32_16x16x32_bf16 a[248:251], v[172:175], v[144:147], a[248:251]\n" \
"v_cndmask_b32_e64 v219, v213, 0, vcc\n" \
"v_cmp_lt_u32_e64 vcc, v217, v212\n" \
"v_mfma_f32_16x16x32_bf16 a[244:247], v[104:107], v[80:83], 0\n" \
"s_cmpk_lt_u32 s40, 0x51\n" \
"s_cselect_b32 s7, s40, s7\n" \
"v_mfma_f32_16x16x32_bf16 a[244:247], v[168:171], v[144:147], a[244:247]\n" \
"s_cmpk_lt_u32 s40, 0x51\n" \
"s_cselect_b32 s5, s23, s25\n" \
"v_mfma_f32_16x16x32_bf16 a[240:243], v[100:103], v[80:83], 0\n" \
"v_cndmask_b32_e32 v217, 0, v218, vcc\n" \
"v_or_b32_e32 v13, v219, v217\n" \
"v_mfma_f32_16x16x32_bf16 a[240:243], v[164:167], v[144:147], a[240:243]\n" \
"ds_read_b128 v[80:83], v28 offset:38912\n" \
"ds_read_b128 v[144:147], v29 offset:38912\n" \
"s_waitcnt lgkmcnt(15)\n" \
"v_mfma_f32_16x16x32_bf16 a[0:3], v[84:87], v[52:55], a[0:3]\n" \
"s_add_u32 m0, s31, 0xc00\n" \
"s_mov_b32 s85, 0x118000\n" \
"v_mfma_f32_16x16x32_bf16 a[0:3], v[148:151], v[116:119], a[0:3]\n" \
"buffer_load_dwordx4 v27, s[68:71], s85 offen lds\n" \
"v_mfma_f32_16x16x32_bf16 a[4:7], v[88:91], v[52:55], a[4:7]\n" \
"ds_read_b64_tr_b16 v[100:101], v36 offset:32768\n" \
"v_or_b32_e32 v217, 16, v216\n" \
"s_waitcnt lgkmcnt(15)\n" \
"v_mfma_f32_16x16x32_bf16 a[4:7], v[152:155], v[116:119], a[4:7]\n" \
"s_add_u32 m0, s31, 0x4c00\n" \
"s_mov_b32 s84, 0x118000\n" \
"s_waitcnt lgkmcnt(11)\n" \
"v_mfma_f32_16x16x32_bf16 a[8:11], v[92:95], v[52:55], a[8:11]\n" \
"buffer_load_dwordx4 v27, s[72:75], s84 offen lds\n" \
"s_waitcnt lgkmcnt(9)\n" \
"v_mfma_f32_16x16x32_bf16 a[8:11], v[156:159], v[116:119], a[8:11]\n" \
"ds_read_b64_tr_b16 v[102:103], v36 offset:33792\n" \
"v_cmp_gt_u32_e32 vcc, v217, v212\n" \
"s_waitcnt lgkmcnt(8)\n" \
"v_mfma_f32_16x16x32_bf16 a[12:15], v[96:99], v[52:55], a[12:15]\n" \
"ds_read_b64_tr_b16 v[164:165], v36 offset:40960\n" \
"s_waitcnt lgkmcnt(5)\n" \
"v_mfma_f32_16x16x32_bf16 a[12:15], v[160:163], v[116:119], a[12:15]\n" \
"s_cmpk_lt_u32 s40, 0x51\n" \
"s_cselect_b32 s4, s22, s24\n" \
"v_mfma_f32_16x16x32_bf16 a[44:47], v[96:99], v[56:59], a[44:47]\n" \
"s_movk_i32 s40, 0xf400\n" \
"v_cndmask_b32_e64 v219, v213, 0, vcc\n" \
"v_mfma_f32_16x16x32_bf16 a[44:47], v[160:163], v[120:123], a[44:47]\n" \
"ds_read_b64_tr_b16 v[166:167], v36 offset:41984\n" \
"v_cmp_lt_u32_e32 vcc, v217, v212\n" \
"v_mfma_f32_16x16x32_bf16 a[40:43], v[92:95], v[56:59], a[40:43]\n" \
"s_nop 0\n" \
"v_cndmask_b32_e32 v217, 0, v218, vcc\n" \
"v_or_b32_e64 v14, v219, v217\n" \
"v_mfma_f32_16x16x32_bf16 a[40:43], v[156:159], v[120:123], a[40:43]\n" \
"v_or_b32_e32 v217, 18, v216\n" \
"v_cmp_gt_u32_e32 vcc, v217, v212\n" \
"v_mfma_f32_16x16x32_bf16 a[36:39], v[88:91], v[56:59], a[36:39]\n" \
"s_nop 0\n" \
"v_cndmask_b32_e64 v213, v213, 0, vcc\n" \
"v_cmp_lt_u32_e32 vcc, v217, v212\n" \
"v_mfma_f32_16x16x32_bf16 a[36:39], v[152:155], v[120:123], a[36:39]\n" \
"ds_read_b64_tr_b16 v[104:105], v37 offset:32768\n" \
"v_cndmask_b32_e64 v212, 0, v218, vcc\n" \
"v_mfma_f32_16x16x32_bf16 a[32:35], v[84:87], v[56:59], a[32:35]\n" \
"v_or_b32_e32 v15, v213, v212\n" \
"v_lshlrev_b32_e32 v212, 7, v214\n" \
"v_mfma_f32_16x16x32_bf16 a[32:35], v[148:151], v[120:123], a[32:35]\n" \
"v_lshl_or_b32 v212, s7, 13, v212\n" \
"v_mov_b32_e64 v213, 0\n" \
"v_mfma_f32_16x16x32_bf16 a[64:67], v[84:87], v[60:63], a[64:67]\n" \
"s_mov_b32 m0, s32\n" \
"ds_read_b64_tr_b16 v[106:107], v37 offset:33792\n" \
"v_mfma_f32_16x16x32_bf16 a[64:67], v[148:151], v[124:127], a[64:67]\n" \
"s_mov_b32 exec_lo, 0x0\n" \
"s_mov_b32 exec_hi, 0xff\n" \
"global_load_lds_dword v[184:185], off\n" \
"s_mov_b64 exec, -1\n" \
"v_mfma_f32_16x16x32_bf16 a[68:71], v[88:91], v[60:63], a[68:71]\n" \
"v_lshl_add_u64 v[16:17], s[4:5], 0, v[212:213]\n" \
"v_mul_u32_u24_e32 v212, 0xe39, v214\n" \
"v_mfma_f32_16x16x32_bf16 a[68:71], v[152:155], v[124:127], a[68:71]\n" \
"ds_read_b64_tr_b16 v[168:169], v37 offset:40960\n" \
"v_lshrrev_b32_e32 v212, 16, v212\n" \
"v_mfma_f32_16x16x32_bf16 a[72:75], v[92:95], v[60:63], a[72:75]\n" \
"s_movk_i32 s5, 0xffee\n" \
"s_lshl_b32 s4, s2, 5\n" \
"v_mfma_f32_16x16x32_bf16 a[72:75], v[156:159], v[124:127], a[72:75]\n" \
"s_mov_b32 m0, s32\n" \
"v_mad_i32_i24 v213, v212, s5, v214\n" \
"v_mfma_f32_16x16x32_bf16 a[76:79], v[96:99], v[60:63], a[76:79]\n" \
"s_mov_b32 exec_lo, 0xffff0000\n" \
"s_mov_b32 exec_hi, 0x0\n" \
"global_load_lds_dword v[186:187], off\n" \
"s_mov_b64 exec, -1\n" \
"v_mfma_f32_16x16x32_bf16 a[76:79], v[160:163], v[124:127], a[76:79]\n" \
"ds_read_b64_tr_b16 v[170:171], v37 offset:41984\n" \
"s_add_i32 s4, s4, 32\n" \
"v_mfma_f32_16x16x32_bf16 a[108:111], v[96:99], v[64:67], a[108:111]\n" \
"v_lshlrev_b32_e32 v217, 4, v213\n" \
"v_add_u32_e32 v218, s4, v217\n" \
"v_mfma_f32_16x16x32_bf16 a[108:111], v[160:163], v[128:131], a[108:111]\n" \
"v_and_b32_e32 v218, 0xf0, v218\n" \
"v_cmp_gt_u32_e64 vcc, 16, v213\n" \
"v_mfma_f32_16x16x32_bf16 a[104:107], v[92:95], v[64:67], a[104:107]\n" \
"ds_read_b64_tr_b16 v[108:109], v38 offset:32768\n" \
"s_movk_i32 s7, 0xf800\n" \
"v_mfma_f32_16x16x32_bf16 a[104:107], v[156:159], v[128:131], a[104:107]\n" \
"v_cndmask_b32_e32 v213, v217, v218, vcc\n" \
"v_or_b32_e64 v217, 64, v214\n" \
"v_mfma_f32_16x16x32_bf16 a[100:103], v[88:91], v[64:67], a[100:103]\n" \
"v_mul_u32_u24_e32 v218, 0xe39, v217\n" \
"v_lshrrev_b32_e64 v218, 16, v218\n" \
"v_mfma_f32_16x16x32_bf16 a[100:103], v[152:155], v[128:131], a[100:103]\n" \
"ds_read_b64_tr_b16 v[110:111], v38 offset:33792\n" \
"v_mad_i32_i24 v217, v218, s5, v217\n" \
"v_mfma_f32_16x16x32_bf16 a[96:99], v[84:87], v[64:67], a[96:99]\n" \
"v_lshlrev_b32_e32 v219, 4, v217\n" \
"v_add_u32_e32 v220, s4, v219\n" \
"v_mfma_f32_16x16x32_bf16 a[96:99], v[148:151], v[128:131], a[96:99]\n" \
"v_and_b32_e32 v220, 0xf0, v220\n" \
"v_cmp_gt_u32_e64 vcc, 16, v217\n" \
"v_mfma_f32_16x16x32_bf16 a[128:131], v[84:87], v[68:71], a[128:131]\n" \
"ds_read_b64_tr_b16 v[172:173], v38 offset:40960\n" \
"v_cndmask_b32_e64 v217, v219, v220, vcc\n" \
"v_mfma_f32_16x16x32_bf16 a[128:131], v[148:151], v[132:135], a[128:131]\n" \
"v_or_b32_e32 v219, 0x80, v214\n" \
"v_mul_u32_u24_e32 v220, 0xe39, v219\n" \
"v_mfma_f32_16x16x32_bf16 a[132:135], v[88:91], v[68:71], a[132:135]\n" \
"v_lshrrev_b32_e64 v220, 16, v220\n" \
"v_mad_i32_i24 v219, v220, s5, v219\n" \
"v_mfma_f32_16x16x32_bf16 a[132:135], v[152:155], v[132:135], a[132:135]\n" \
"ds_read_b64_tr_b16 v[174:175], v38 offset:41984\n" \
"v_lshlrev_b32_e64 v221, 4, v219\n" \
"v_mfma_f32_16x16x32_bf16 a[136:139], v[92:95], v[68:71], a[136:139]\n" \
"v_add_u32_e64 v222, s4, v221\n" \
"v_and_b32_e32 v222, 0xf0, v222\n" \
"v_mfma_f32_16x16x32_bf16 a[136:139], v[156:159], v[132:135], a[136:139]\n" \
"v_cmp_gt_u32_e32 vcc, 16, v219\n" \
"s_nop 1\n" \
"v_cndmask_b32_e64 v219, v221, v222, vcc\n" \
"v_mfma_f32_16x16x32_bf16 a[140:143], v[96:99], v[68:71], a[140:143]\n" \
"v_or_b32_e32 v221, 0xc0, v214\n" \
"v_mul_u32_u24_e32 v222, 0xe39, v221\n" \
"v_mfma_f32_16x16x32_bf16 a[140:143], v[160:163], v[132:135], a[140:143]\n" \
"ds_read_b64_tr_b16 v[112:113], v39 offset:32768\n" \
"v_lshrrev_b32_e64 v222, 16, v222\n" \
"v_mfma_f32_16x16x32_bf16 a[172:175], v[96:99], v[72:75], a[172:175]\n" \
"v_mad_i32_i24 v221, v222, s5, v221\n" \
"v_lshlrev_b32_e64 v223, 4, v221\n" \
"v_mfma_f32_16x16x32_bf16 a[172:175], v[160:163], v[136:139], a[172:175]\n" \
"v_add_u32_e64 v224, s4, v223\n" \
"v_and_b32_e32 v224, 0xf0, v224\n" \
"v_mfma_f32_16x16x32_bf16 a[168:171], v[92:95], v[72:75], a[168:171]\n" \
"ds_read_b64_tr_b16 v[114:115], v39 offset:33792\n" \
"v_cmp_gt_u32_e64 vcc, 16, v221\n" \
"v_mfma_f32_16x16x32_bf16 a[168:171], v[156:159], v[136:139], a[168:171]\n" \
"s_nop 0\n" \
"v_cndmask_b32_e32 v221, v223, v224, vcc\n" \
"v_or_b32_e32 v223, 0x100, v214\n" \
"v_mfma_f32_16x16x32_bf16 a[164:167], v[88:91], v[72:75], a[164:167]\n" \
"v_mul_u32_u24_e32 v224, 0xe39, v223\n" \
"v_lshrrev_b32_e64 v224, 16, v224\n" \
"v_mfma_f32_16x16x32_bf16 a[164:167], v[152:155], v[136:139], a[164:167]\n" \
"ds_read_b64_tr_b16 v[176:177], v39 offset:40960\n" \
"v_mad_i32_i24 v223, v224, s5, v223\n" \
"v_mfma_f32_16x16x32_bf16 a[160:163], v[84:87], v[72:75], a[160:163]\n" \
"v_lshlrev_b32_e32 v225, 4, v223\n" \
"v_add_u32_e32 v226, s4, v225\n" \
"v_mfma_f32_16x16x32_bf16 a[160:163], v[148:151], v[136:139], a[160:163]\n" \
"v_and_b32_e32 v226, 0xf0, v226\n" \
"v_cmp_gt_u32_e64 vcc, 16, v223\n" \
"v_mfma_f32_16x16x32_bf16 a[192:195], v[84:87], v[76:79], a[192:195]\n" \
"ds_read_b64_tr_b16 v[178:179], v39 offset:41984\n" \
"v_cndmask_b32_e64 v223, v225, v226, vcc\n" \
"v_mfma_f32_16x16x32_bf16 a[192:195], v[148:151], v[140:143], a[192:195]\n" \
"v_or_b32_e32 v225, 0x140, v214\n" \
"v_mul_u32_u24_e32 v226, 0xe39, v225\n" \
"v_mfma_f32_16x16x32_bf16 a[196:199], v[88:91], v[76:79], a[196:199]\n" \
"v_lshrrev_b32_e64 v226, 16, v226\n" \
"v_mad_i32_i24 v225, v226, s5, v225\n" \
"v_mfma_f32_16x16x32_bf16 a[196:199], v[152:155], v[140:143], a[196:199]\n" \
"v_lshlrev_b32_e32 v227, 4, v225\n" \
"v_add_u32_e32 v228, s4, v227\n" \
"v_mfma_f32_16x16x32_bf16 a[200:203], v[92:95], v[76:79], a[200:203]\n" \
"v_and_b32_e32 v228, 0xf0, v228\n" \
"v_cmp_gt_u32_e64 vcc, 16, v225\n" \
"v_mfma_f32_16x16x32_bf16 a[200:203], v[156:159], v[140:143], a[200:203]\n" \
"s_nop 0\n" \
"v_cndmask_b32_e32 v225, v227, v228, vcc\n" \
"v_or_b32_e32 v227, 0x180, v214\n" \
"v_mfma_f32_16x16x32_bf16 a[204:207], v[96:99], v[76:79], a[204:207]\n" \
"v_mul_u32_u24_e32 v228, 0xe39, v227\n" \
"v_lshrrev_b32_e64 v228, 16, v228\n" \
"v_mfma_f32_16x16x32_bf16 a[204:207], v[160:163], v[140:143], a[204:207]\n" \
"v_mad_i32_i24 v227, v228, s5, v227\n" \
"v_lshlrev_b32_e32 v229, 4, v227\n" \
"s_waitcnt lgkmcnt(15)\n" \
"v_mfma_f32_16x16x32_bf16 a[236:239], v[96:99], v[80:83], a[236:239]\n" \
"v_add_u32_e64 v230, s4, v229\n" \
"v_and_b32_e32 v230, 0xf0, v230\n" \
"v_mfma_f32_16x16x32_bf16 a[236:239], v[160:163], v[144:147], a[236:239]\n" \
"v_cmp_gt_u32_e32 vcc, 16, v227\n" \
"s_nop 1\n" \
"v_cndmask_b32_e64 v227, v229, v230, vcc\n" \
"v_mfma_f32_16x16x32_bf16 a[232:235], v[92:95], v[80:83], a[232:235]\n" \
"v_or_b32_e32 v229, 0x1c0, v214\n" \
"v_mul_u32_u24_e32 v230, 0xe39, v229\n" \
"v_mfma_f32_16x16x32_bf16 a[232:235], v[156:159], v[144:147], a[232:235]\n" \
"v_lshrrev_b32_e64 v230, 16, v230\n" \
"v_mad_i32_i24 v229, v230, s5, v229\n" \
"v_mfma_f32_16x16x32_bf16 a[228:231], v[88:91], v[80:83], a[228:231]\n" \
"v_lshlrev_b32_e32 v231, 4, v229\n" \
"v_add_u32_e32 v232, s4, v231\n" \
"v_mfma_f32_16x16x32_bf16 a[228:231], v[152:155], v[144:147], a[228:231]\n" \
"v_and_b32_e32 v232, 0xf0, v232\n" \
"v_cmp_gt_u32_e64 vcc, 16, v229\n" \
"v_mfma_f32_16x16x32_bf16 a[224:227], v[84:87], v[80:83], a[224:227]\n" \
"s_nop 0\n" \
"v_cndmask_b32_e32 v229, v231, v232, vcc\n" \
"v_or_b32_e32 v231, 0x200, v214\n" \
"v_mfma_f32_16x16x32_bf16 a[224:227], v[148:151], v[144:147], a[224:227]\n" \
"s_waitcnt vmcnt(20)\n" \
"v_readfirstlane_b32 s62, v180\n" \
"s_nop 3\n" \
"s_add_u32 s62, s62, 0x9E3779B1\n" \
"s_waitcnt vmcnt(2) lgkmcnt(0)\n" \
"s_barrier\n" \
"s_nop 0\n" \
"v_mfma_f32_16x16x32_bf16 a[16:19], v[100:103], v[52:55], a[16:19]\n" \
"s_add_u32 m0, s30, 0x8000\n" \
"s_mov_b32 s85, 0x180\n" \
"v_mfma_f32_16x16x32_bf16 a[16:19], v[164:167], v[116:119], a[16:19]\n" \
"buffer_load_dwordx4 v22, s[64:67], s85 offen lds\n" \
"v_mfma_f32_16x16x32_bf16 a[20:23], v[104:107], v[52:55], a[20:23]\n" \
"ds_read_b64_tr_b16 v[84:85], v32 offset:0\n" \
"v_mul_u32_u24_e32 v232, 0xe39, v231\n" \
"v_mfma_f32_16x16x32_bf16 a[20:23], v[168:171], v[116:119], a[20:23]\n" \
"s_add_u32 m0, s30, 0x8400\n" \
"s_mov_b32 s84, 0x2180\n" \
"v_mfma_f32_16x16x32_bf16 a[24:27], v[108:111], v[52:55], a[24:27]\n" \
"buffer_load_dwordx4 v23, s[64:67], s84 offen lds\n" \
"v_mfma_f32_16x16x32_bf16 a[24:27], v[172:175], v[116:119], a[24:27]\n" \
"ds_read_b64_tr_b16 v[86:87], v32 offset:1024\n" \
"v_lshrrev_b32_e64 v232, 16, v232\n" \
"v_mfma_f32_16x16x32_bf16 a[28:31], v[112:115], v[52:55], a[28:31]\n" \
"ds_read_b64_tr_b16 v[148:149], v32 offset:8192\n" \
"v_mad_i32_i24 v231, v232, s5, v231\n" \
"v_mfma_f32_16x16x32_bf16 a[28:31], v[176:179], v[116:119], a[28:31]\n" \
"ds_read_b128 v[52:55], v28 offset:16384\n" \
"v_lshlrev_b32_e64 v233, 4, v231\n" \
"v_mfma_f32_16x16x32_bf16 a[60:63], v[112:115], v[56:59], a[60:63]\n" \
"ds_read_b128 v[116:119], v29 offset:16384\n" \
"v_add_u32_e64 v234, s4, v233\n" \
"v_mfma_f32_16x16x32_bf16 a[60:63], v[176:179], v[120:123], a[60:63]\n" \
"s_add_u32 m0, s31, 0x8000\n" \
"s_mov_b32 s85, 0x180000\n" \
"v_mfma_f32_16x16x32_bf16 a[56:59], v[108:111], v[56:59], a[56:59]\n" \
"buffer_load_dwordx4 v26, s[68:71], s85 offen lds\n" \
"v_mfma_f32_16x16x32_bf16 a[56:59], v[172:175], v[120:123], a[56:59]\n" \
"s_add_u32 m0, s31, 0xc000\n" \
"s_mov_b32 s84, 0x180000\n" \
"v_mfma_f32_16x16x32_bf16 a[52:55], v[104:107], v[56:59], a[52:55]\n" \
"buffer_load_dwordx4 v26, s[72:75], s84 offen lds\n" \
"v_mfma_f32_16x16x32_bf16 a[52:55], v[168:171], v[120:123], a[52:55]\n" \
"s_add_u32 m0, s30, 0x8800\n" \
"s_mov_b32 s85, 0x4180\n" \
"v_mfma_f32_16x16x32_bf16 a[48:51], v[100:103], v[56:59], a[48:51]\n" \
"buffer_load_dwordx4 v22, s[64:67], s85 offen lds\n" \
"v_mfma_f32_16x16x32_bf16 a[48:51], v[164:167], v[120:123], a[48:51]\n" \
"ds_read_b128 v[56:59], v28 offset:18432\n" \
"s_movk_i32 s4, 0x480\n" \
"s_nop 0\n" \
"v_mfma_f32_16x16x32_bf16 a[80:83], v[100:103], v[60:63], a[80:83]\n" \
"ds_read_b128 v[120:123], v29 offset:18432\n" \
"v_mad_u32_u24 v0, v212, s4, v213\n" \
"v_mfma_f32_16x16x32_bf16 a[80:83], v[164:167], v[124:127], a[80:83]\n" \
"s_add_u32 m0, s30, 0x8c00\n" \
"s_mov_b32 s84, 0x6180\n" \
"v_mfma_f32_16x16x32_bf16 a[84:87], v[104:107], v[60:63], a[84:87]\n" \
"buffer_load_dwordx4 v23, s[64:67], s84 offen lds\n" \
"v_mfma_f32_16x16x32_bf16 a[84:87], v[168:171], v[124:127], a[84:87]\n" \
"ds_read_b64_tr_b16 v[150:151], v32 offset:9216\n" \
"v_mul_u32_u24_e32 v212, 0x480, v218\n" \
"v_mfma_f32_16x16x32_bf16 a[88:91], v[108:111], v[60:63], a[88:91]\n" \
"s_add_u32 m0, s31, 0x8400\n" \
"s_mov_b32 s85, 0x188000\n" \
"v_mfma_f32_16x16x32_bf16 a[88:91], v[172:175], v[124:127], a[88:91]\n" \
"buffer_load_dwordx4 v26, s[68:71], s85 offen lds\n" \
"v_mfma_f32_16x16x32_bf16 a[92:95], v[112:115], v[60:63], a[92:95]\n" \
"ds_read_b64_tr_b16 v[88:89], v33 offset:0\n" \
"s_movk_i32 s5, 0xfc00\n" \
"s_nop 0\n" \
"v_mfma_f32_16x16x32_bf16 a[92:95], v[176:179], v[124:127], a[92:95]\n" \
"ds_read_b128 v[60:63], v28 offset:20480\n" \
"v_add3_u32 v1, v212, v217, s5\n" \
"v_mfma_f32_16x16x32_bf16 a[124:127], v[112:115], v[64:67], a[124:127]\n" \
"ds_read_b128 v[124:127], v29 offset:20480\n" \
"v_mul_u32_u24_e32 v212, 0x480, v220\n" \
"v_mfma_f32_16x16x32_bf16 a[124:127], v[176:179], v[128:131], a[124:127]\n" \
"s_add_u32 m0, s31, 0xc400\n" \
"s_mov_b32 s84, 0x188000\n" \
"v_mfma_f32_16x16x32_bf16 a[120:123], v[108:111], v[64:67], a[120:123]\n" \
"buffer_load_dwordx4 v26, s[72:75], s84 offen lds\n" \
"v_mfma_f32_16x16x32_bf16 a[120:123], v[172:175], v[128:131], a[120:123]\n" \
"s_add_u32 m0, s30, 0x9000\n" \
"s_mov_b32 s85, 0x8180\n" \
"v_mfma_f32_16x16x32_bf16 a[116:119], v[104:107], v[64:67], a[116:119]\n" \
"buffer_load_dwordx4 v22, s[64:67], s85 offen lds\n" \
"v_mfma_f32_16x16x32_bf16 a[116:119], v[168:171], v[128:131], a[116:119]\n" \
"ds_read_b64_tr_b16 v[90:91], v33 offset:1024\n" \
"v_add3_u32 v2, v212, v219, s7\n" \
"v_mfma_f32_16x16x32_bf16 a[112:115], v[100:103], v[64:67], a[112:115]\n" \
"ds_read_b64_tr_b16 v[152:153], v33 offset:8192\n" \
"v_mul_u32_u24_e32 v212, 0x480, v222\n" \
"v_mfma_f32_16x16x32_bf16 a[112:115], v[164:167], v[128:131], a[112:115]\n" \
"ds_read_b128 v[64:67], v28 offset:22528\n" \
"v_add3_u32 v3, v212, v221, s40\n" \
"v_mfma_f32_16x16x32_bf16 a[144:147], v[100:103], v[68:71], a[144:147]\n" \
"ds_read_b128 v[128:131], v29 offset:22528\n" \
"v_mul_u32_u24_e32 v212, 0x480, v226\n" \
"v_mfma_f32_16x16x32_bf16 a[144:147], v[164:167], v[132:135], a[144:147]\n" \
"s_add_u32 m0, s30, 0x9400\n" \
"s_mov_b32 s84, 0xa180\n" \
"v_mfma_f32_16x16x32_bf16 a[148:151], v[104:107], v[68:71], a[148:151]\n" \
"buffer_load_dwordx4 v23, s[64:67], s84 offen lds\n" \
"v_mfma_f32_16x16x32_bf16 a[148:151], v[168:171], v[132:135], a[148:151]\n" \
"s_add_u32 m0, s31, 0x8800\n" \
"s_mov_b32 s85, 0x190000\n" \
"v_mfma_f32_16x16x32_bf16 a[152:155], v[108:111], v[68:71], a[152:155]\n" \
"buffer_load_dwordx4 v27, s[68:71], s85 offen lds\n" \
"v_mfma_f32_16x16x32_bf16 a[152:155], v[172:175], v[132:135], a[152:155]\n" \
"s_add_u32 m0, s31, 0xc800\n" \
"s_mov_b32 s84, 0x190000\n" \
"v_mfma_f32_16x16x32_bf16 a[156:159], v[112:115], v[68:71], a[156:159]\n" \
"buffer_load_dwordx4 v27, s[72:75], s84 offen lds\n" \
"v_mfma_f32_16x16x32_bf16 a[156:159], v[176:179], v[132:135], a[156:159]\n" \
"ds_read_b128 v[68:71], v28 offset:0\n" \
"v_add3_u32 v5, v212, v225, s5\n" \
"v_mfma_f32_16x16x32_bf16 a[188:191], v[112:115], v[72:75], a[188:191]\n" \
"ds_read_b128 v[132:135], v29 offset:0\n" \
"v_mul_u32_u24_e32 v212, 0x480, v228\n" \
"v_mfma_f32_16x16x32_bf16 a[188:191], v[176:179], v[136:139], a[188:191]\n" \
"s_add_u32 m0, s30, 0x9800\n" \
"s_mov_b32 s85, 0xc180\n" \
"v_mfma_f32_16x16x32_bf16 a[184:187], v[108:111], v[72:75], a[184:187]\n" \
"buffer_load_dwordx4 v22, s[64:67], s85 offen lds\n" \
"v_mfma_f32_16x16x32_bf16 a[184:187], v[172:175], v[136:139], a[184:187]\n" \
"ds_read_b64_tr_b16 v[154:155], v33 offset:9216\n" \
"v_add3_u32 v6, v212, v227, s7\n" \
"v_mfma_f32_16x16x32_bf16 a[180:183], v[104:107], v[72:75], a[180:183]\n" \
"s_add_u32 m0, s30, 0x9c00\n" \
"s_mov_b32 s84, 0xe180\n" \
"v_mfma_f32_16x16x32_bf16 a[180:183], v[168:171], v[136:139], a[180:183]\n" \
"buffer_load_dwordx4 v23, s[64:67], s84 offen lds\n" \
"v_mfma_f32_16x16x32_bf16 a[176:179], v[100:103], v[72:75], a[176:179]\n" \
"ds_read_b64_tr_b16 v[92:93], v34 offset:0\n" \
"v_mul_u32_u24_e32 v212, 0x480, v230\n" \
"v_mfma_f32_16x16x32_bf16 a[176:179], v[164:167], v[136:139], a[176:179]\n" \
"ds_read_b128 v[72:75], v28 offset:2048\n" \
"v_add3_u32 v7, v212, v229, s40\n" \
"v_mfma_f32_16x16x32_bf16 a[208:211], v[100:103], v[76:79], a[208:211]\n" \
"ds_read_b128 v[136:139], v29 offset:2048\n" \
"s_lshl_b32 s7, s3, 3\n" \
"v_mfma_f32_16x16x32_bf16 a[208:211], v[164:167], v[140:143], a[208:211]\n" \
"ds_read_b64_tr_b16 v[94:95], v34 offset:1024\n" \
"s_lshl_b32 s40, s3, 1\n" \
"v_mfma_f32_16x16x32_bf16 a[212:215], v[104:107], v[76:79], a[212:215]\n" \
"ds_read_b64_tr_b16 v[156:157], v34 offset:8192\n" \
"s_and_b32 s7, s7, 16\n" \
"v_mfma_f32_16x16x32_bf16 a[212:215], v[168:171], v[140:143], a[212:215]\n" \
"ds_read_b64_tr_b16 v[158:159], v34 offset:9216\n" \
"s_and_b32 s40, s40, 2\n" \
"v_mfma_f32_16x16x32_bf16 a[216:219], v[108:111], v[76:79], a[216:219]\n" \
"ds_read_b64_tr_b16 v[96:97], v35 offset:0\n" \
"s_and_b32 s5, s2, 7\n" \
"v_mfma_f32_16x16x32_bf16 a[216:219], v[172:175], v[140:143], a[216:219]\n" \
"ds_read_b64_tr_b16 v[98:99], v35 offset:1024\n" \
"v_bfe_u32 v212, v20, 7, 1\n" \
"v_mfma_f32_16x16x32_bf16 a[220:223], v[112:115], v[76:79], a[220:223]\n" \
"ds_read_b64_tr_b16 v[160:161], v35 offset:8192\n" \
"s_or_b32 s7, s40, s7\n" \
"v_mfma_f32_16x16x32_bf16 a[220:223], v[176:179], v[140:143], a[220:223]\n" \
"ds_read_b128 v[76:79], v28 offset:4096\n" \
"v_and_b32_e32 v234, 0xf0, v234\n" \
"v_mfma_f32_16x16x32_bf16 a[252:255], v[112:115], v[80:83], a[252:255]\n" \
"ds_read_b128 v[140:143], v29 offset:4096\n" \
"v_cmp_gt_u32_e64 vcc, 16, v231\n" \
"v_mfma_f32_16x16x32_bf16 a[252:255], v[176:179], v[144:147], a[252:255]\n" \
"ds_read_b64_tr_b16 v[162:163], v35 offset:9216\n" \
"v_or3_b32 v212, s7, v212, v216\n" \
"v_mfma_f32_16x16x32_bf16 a[248:251], v[108:111], v[80:83], a[248:251]\n" \
"v_and_b32_e32 v213, 32, v20\n" \
"s_lshl_b32 s5, s5, 5\n" \
"v_mfma_f32_16x16x32_bf16 a[248:251], v[172:175], v[144:147], a[248:251]\n" \
"v_cndmask_b32_e32 v231, v233, v234, vcc\n" \
"v_mul_u32_u24_e32 v212, 18, v212\n" \
"v_mfma_f32_16x16x32_bf16 a[244:247], v[104:107], v[80:83], a[244:247]\n" \
"v_mov_b32_e32 v216, 0x100\n" \
"v_mov_b32_e64 v217, s5\n" \
"v_mfma_f32_16x16x32_bf16 a[244:247], v[168:171], v[144:147], a[244:247]\n" \
"v_cmp_eq_u32_e64 vcc, 0, v213\n" \
"v_and_or_b32 v212, v215, 1, v212\n" \
"v_mfma_f32_16x16x32_bf16 a[240:243], v[100:103], v[80:83], a[240:243]\n" \
"v_mad_u32_u24 v4, v224, s4, v223\n" \
"v_cndmask_b32_e64 v213, v216, v217, vcc\n" \
"v_mfma_f32_16x16x32_bf16 a[240:243], v[164:167], v[144:147], a[240:243]\n" \
"ds_read_b128 v[80:83], v28 offset:6144\n" \
"ds_read_b128 v[144:147], v29 offset:6144\n" \
"s_waitcnt lgkmcnt(15)\n" \
"v_mfma_f32_16x16x32_bf16 a[0:3], v[84:87], v[52:55], a[0:3]\n" \
"s_add_u32 m0, s31, 0x8c00\n" \
"s_mov_b32 s85, 0x198000\n" \
"v_mfma_f32_16x16x32_bf16 a[0:3], v[148:151], v[116:119], a[0:3]\n" \
"buffer_load_dwordx4 v27, s[68:71], s85 offen lds\n" \
"v_mfma_f32_16x16x32_bf16 a[4:7], v[88:91], v[52:55], a[4:7]\n" \
"ds_read_b64_tr_b16 v[100:101], v36 offset:0\n" \
"v_mad_u32_u24 v212, v212, s4, v213\n" \
"s_waitcnt lgkmcnt(15)\n" \
"v_mfma_f32_16x16x32_bf16 a[4:7], v[152:155], v[116:119], a[4:7]\n" \
"s_add_u32 m0, s31, 0xcc00\n" \
"s_mov_b32 s84, 0x198000\n" \
"s_waitcnt lgkmcnt(11)\n" \
"v_mfma_f32_16x16x32_bf16 a[8:11], v[92:95], v[52:55], a[8:11]\n" \
"buffer_load_dwordx4 v27, s[72:75], s84 offen lds\n" \
"s_waitcnt lgkmcnt(9)\n" \
"v_mfma_f32_16x16x32_bf16 a[8:11], v[156:159], v[116:119], a[8:11]\n" \
"ds_read_b64_tr_b16 v[102:103], v36 offset:1024\n" \
"v_and_or_b32 v212, v20, 16, v212\n" \
"s_waitcnt lgkmcnt(8)\n" \
"v_mfma_f32_16x16x32_bf16 a[12:15], v[96:99], v[52:55], a[12:15]\n" \
"ds_read_b64_tr_b16 v[164:165], v36 offset:8192\n" \
"v_add_u32_e32 v9, 0x4800, v212\n" \
"s_waitcnt lgkmcnt(5)\n" \
"v_mfma_f32_16x16x32_bf16 a[12:15], v[160:163], v[116:119], a[12:15]\n" \
"v_mul_u32_u24_e32 v212, 0x120, v215\n" \
"v_lshlrev_b32_e64 v213, 3, v214\n" \
"v_mfma_f32_16x16x32_bf16 a[44:47], v[96:99], v[56:59], a[44:47]\n" \
"v_mad_u32_u24 v8, v232, s4, v231\n" \
"v_and_or_b32 v10, v213, 24, v212\n" \
"v_mfma_f32_16x16x32_bf16 a[44:47], v[160:163], v[120:123], a[44:47]\n" \
"ds_read_b64_tr_b16 v[166:167], v36 offset:9216\n" \
"s_lshl_b32 s4, s6, 6\n" \
"v_mfma_f32_16x16x32_bf16 a[40:43], v[92:95], v[56:59], a[40:43]\n" \
"buffer_load_dwordx4 v[200:203], v192, s[76:79], s34 offen offset:0\n" \
"v_mfma_f32_16x16x32_bf16 a[40:43], v[156:159], v[120:123], a[40:43]\n" \
"v_lshrrev_b32_e32 v212, 2, v20\n" \
"v_and_or_b32 v212, v212, 60, s4\n" \
"v_mfma_f32_16x16x32_bf16 a[36:39], v[88:91], v[56:59], a[36:39]\n" \
"v_or_b32_e32 v18, 0x27000, v212\n" \
"v_lshl_or_b32 v212, s3, 9, v213\n" \
"v_mfma_f32_16x16x32_bf16 a[36:39], v[152:155], v[120:123], a[36:39]\n" \
"ds_read_b64_tr_b16 v[104:105], v37 offset:0\n" \
"v_or_b32_e32 v19, 0x27800, v212\n" \
"v_mfma_f32_16x16x32_bf16 a[32:35], v[84:87], v[56:59], a[32:35]\n" \
"v_mov_b32_e32 v219, 0x1000c0c\n" \
"v_mov_b32_e32 v220, 0xc0c0100\n" \
"v_mfma_f32_16x16x32_bf16 a[32:35], v[148:151], v[120:123], a[32:35]\n" \
".fill (L_pb + 7904 - .) / 4, 4, 0xBF800000\n" \
"L_pp2:\n" \
"s_cbranch_execz L_pret\n" \
"v_bfe_u32 v218, v20, 4, 1\n" \
"v_lshrrev_b32_e32 v213, 8, v20\n" \
"v_mfma_f32_16x16x32_bf16 a[64:67], v[84:87], v[60:63], a[64:67]\n" \
"buffer_load_dwordx4 v[204:207], v193, s[76:79], s34 offen offset:0\n" \
"v_mfma_f32_16x16x32_bf16 a[64:67], v[148:151], v[124:127], a[64:67]\n" \
"s_mov_b32 m0, s32\n" \
"ds_read_b64_tr_b16 v[106:107], v37 offset:1024\n" \
"v_mfma_f32_16x16x32_bf16 a[68:71], v[88:91], v[60:63], a[68:71]\n" \
"s_mov_b32 exec_lo, 0x0\n" \
"s_mov_b32 exec_hi, 0xff00\n" \
"global_load_lds_dword v[184:185], off\n" \
"s_mov_b64 exec, -1\n" \
"v_mfma_f32_16x16x32_bf16 a[68:71], v[152:155], v[124:127], a[68:71]\n" \
"ds_read_b64_tr_b16 v[168:169], v37 offset:8192\n" \
"v_cmp_eq_u32_e32 vcc, 0, v218\n" \
"v_mfma_f32_16x16x32_bf16 a[72:75], v[92:95], v[60:63], a[72:75]\n" \
"s_lshr_b32 s4, s2, 5\n" \
"s_and_b32 s5, s3, 1\n" \
"v_mfma_f32_16x16x32_bf16 a[72:75], v[156:159], v[124:127], a[72:75]\n" \
"buffer_load_dword v208, v194, s[76:79], s34 offen offset:32\n" \
"v_mfma_f32_16x16x32_bf16 a[76:79], v[96:99], v[60:63], a[76:79]\n" \
"ds_read_b64_tr_b16 v[170:171], v37 offset:9216\n" \
"v_bfe_u32 v217, v20, 4, 4\n" \
"v_mfma_f32_16x16x32_bf16 a[76:79], v[160:163], v[124:127], a[76:79]\n" \
"v_cndmask_b32_e64 v219, v219, v220, vcc\n" \
"v_mov_b32_e32 v220, 0x3020c0c\n" \
"v_mfma_f32_16x16x32_bf16 a[108:111], v[96:99], v[64:67], a[108:111]\n" \
"v_mov_b32_e32 v221, 0xc0c0302\n" \
"v_lshl_or_b32 v218, v213, 2, v218\n" \
"v_mfma_f32_16x16x32_bf16 a[108:111], v[160:163], v[128:131], a[108:111]\n" \
"s_xor_b32 s6, s4, 15\n" \
"v_mfma_f32_16x16x32_bf16 a[104:107], v[92:95], v[64:67], a[104:107]\n" \
"buffer_load_dword v209, v194, s[76:79], s34 offen offset:1184\n" \
"v_mfma_f32_16x16x32_bf16 a[104:107], v[156:159], v[128:131], a[104:107]\n" \
"ds_read_b64_tr_b16 v[108:109], v38 offset:0\n" \
"v_mfma_f32_16x16x32_bf16 a[100:103], v[88:91], v[64:67], a[100:103]\n" \
"s_cmpk_lt_u32 s3, 2\n" \
"s_cselect_b32 s7, s6, s4\n" \
"v_mfma_f32_16x16x32_bf16 a[100:103], v[152:155], v[128:131], a[100:103]\n" \
"ds_read_b64_tr_b16 v[110:111], v38 offset:1024\n" \
"s_lshl_b32 s40, s7, 7\n" \
"v_mfma_f32_16x16x32_bf16 a[96:99], v[84:87], v[64:67], a[96:99]\n" \
"s_lshl_b32 s41, s5, 6\n" \
"v_cndmask_b32_e32 v220, v220, v221, vcc\n" \
"v_mfma_f32_16x16x32_bf16 a[96:99], v[148:151], v[128:131], a[96:99]\n" \
"v_mov_b32_e32 v221, 0xc0c0c0c\n" \
"v_cmp_gt_u32_e64 vcc, v218, v217\n" \
"v_mfma_f32_16x16x32_bf16 a[128:131], v[84:87], v[68:71], a[128:131]\n" \
"buffer_load_dword v211, v194, s[76:79], s34 offen offset:2336\n" \
"v_mfma_f32_16x16x32_bf16 a[128:131], v[148:151], v[132:135], a[128:131]\n" \
"ds_read_b64_tr_b16 v[172:173], v38 offset:8192\n" \
"v_or_b32_e64 v218, 2, v218\n" \
"v_mfma_f32_16x16x32_bf16 a[132:135], v[88:91], v[68:71], a[132:135]\n" \
"v_lshrrev_b32_e32 v212, 7, v20\n" \
"s_or_b32 s41, s41, s40\n" \
"v_mfma_f32_16x16x32_bf16 a[132:135], v[152:155], v[132:135], a[132:135]\n" \
"ds_read_b64_tr_b16 v[174:175], v38 offset:9216\n" \
"v_cndmask_b32_e64 v40, v221, v219, vcc\n" \
"v_mfma_f32_16x16x32_bf16 a[136:139], v[92:95], v[68:71], a[136:139]\n" \
"v_cndmask_b32_e32 v42, v219, v221, vcc\n" \
"v_cmp_gt_u32_e32 vcc, v218, v217\n" \
"v_mfma_f32_16x16x32_bf16 a[136:139], v[156:159], v[132:135], a[136:139]\n" \
"s_mov_b32 s76, s20\n" \
"s_and_b32 s77, s21, 0xffff\n" \
"v_mfma_f32_16x16x32_bf16 a[140:143], v[96:99], v[68:71], a[140:143]\n" \
"s_mov_b32 s78, 0x480\n" \
"v_and_b32_e32 v218, 16, v20\n" \
"v_mfma_f32_16x16x32_bf16 a[140:143], v[160:163], v[132:135], a[140:143]\n" \
"buffer_load_ushort v210, v195, s[76:79], 0 offen\n" \
"v_mfma_f32_16x16x32_bf16 a[172:175], v[96:99], v[72:75], a[172:175]\n" \
"ds_read_b64_tr_b16 v[112:113], v39 offset:0\n" \
"s_movk_i32 s42, 0x3f80\n" \
"v_mfma_f32_16x16x32_bf16 a[172:175], v[160:163], v[136:139], a[172:175]\n" \
"v_or_b32_e32 v214, s41, v212\n" \
"v_and_b32_e32 v217, 0x320, v20\n" \
"v_mfma_f32_16x16x32_bf16 a[168:171], v[92:95], v[72:75], a[168:171]\n" \
"s_mov_b32 s76, s8\n" \
"s_and_b32 s77, s9, 0xffff\n" \
"v_mfma_f32_16x16x32_bf16 a[168:171], v[156:159], v[136:139], a[168:171]\n" \
"s_mov_b32 s78, 0x240000\n" \
"ds_read_b64_tr_b16 v[114:115], v39 offset:1024\n" \
"v_mfma_f32_16x16x32_bf16 a[164:167], v[88:91], v[72:75], a[164:167]\n" \
"s_movk_i32 s41, 0x100\n" \
"v_lshlrev_b32_e64 v218, v218, s42\n" \
"v_mfma_f32_16x16x32_bf16 a[164:167], v[152:155], v[136:139], a[164:167]\n" \
"ds_read_b64_tr_b16 v[176:177], v39 offset:8192\n" \
"v_mov_b32_e64 v219, 0\n" \
"v_mfma_f32_16x16x32_bf16 a[160:163], v[84:87], v[72:75], a[160:163]\n" \
"v_lshrrev_b32_e32 v215, 6, v20\n" \
"v_cndmask_b32_e32 v41, v221, v220, vcc\n" \
"v_mfma_f32_16x16x32_bf16 a[160:163], v[148:151], v[136:139], a[160:163]\n" \
"v_cndmask_b32_e32 v43, v220, v221, vcc\n" \
"v_cmp_eq_u32_e32 vcc, s41, v217\n" \
"v_mfma_f32_16x16x32_bf16 a[192:195], v[84:87], v[76:79], a[192:195]\n" \
"ds_read_b64_tr_b16 v[178:179], v39 offset:9216\n" \
"v_and_b32_e32 v217, 0x330, v20\n" \
"v_mfma_f32_16x16x32_bf16 a[192:195], v[148:151], v[140:143], a[192:195]\n" \
"v_bfe_u32 v216, v20, 6, 2\n" \
"v_cndmask_b32_e64 v46, v219, v218, vcc\n" \
"v_mfma_f32_16x16x32_bf16 a[196:199], v[88:91], v[76:79], a[196:199]\n" \
"v_mov_b32_e32 v218, 0x3f80\n" \
"v_cmp_eq_u32_e64 vcc, 32, v217\n" \
"v_mfma_f32_16x16x32_bf16 a[196:199], v[152:155], v[140:143], a[196:199]\n" \
"s_add_i32 s41, s40, -16\n" \
"v_lshrrev_b32_e32 v219, 1, v20\n" \
"v_mfma_f32_16x16x32_bf16 a[200:203], v[92:95], v[76:79], a[200:203]\n" \
"v_bfe_u32 v215, v215, 1, 1\n" \
"v_cndmask_b32_e64 v47, 0, v218, vcc\n" \
"v_mfma_f32_16x16x32_bf16 a[200:203], v[156:159], v[140:143], a[200:203]\n" \
"s_mov_b32 s42, 0\n" \
"v_mfma_f32_16x16x32_bf16 a[204:207], v[96:99], v[76:79], a[204:207]\n" \
"s_cmp_lg_u32 s40, 0\n" \
"s_cselect_b32 s41, s41, s42\n" \
"v_mfma_f32_16x16x32_bf16 a[204:207], v[160:163], v[140:143], a[204:207]\n" \
"v_lshl_or_b32 v217, s5, 3, v212\n" \
"v_lshlrev_b32_e32 v218, 10, v213\n" \
"s_waitcnt lgkmcnt(15)\n" \
"v_mfma_f32_16x16x32_bf16 a[236:239], v[96:99], v[80:83], a[236:239]\n" \
"v_lshlrev_b32_e32 v216, 7, v216\n" \
"v_and_b32_e64 v219, 24, v219\n" \
"v_mfma_f32_16x16x32_bf16 a[236:239], v[160:163], v[144:147], a[236:239]\n" \
"v_and_or_b32 v212, v212, 2, v215\n" \
"v_add_u32_e64 v217, s41, v217\n" \
"v_mfma_f32_16x16x32_bf16 a[232:235], v[92:95], v[80:83], a[232:235]\n" \
"v_or3_b32 v216, v216, v218, v219\n" \
"v_lshlrev_b32_e64 v212, 5, v212\n" \
"v_mfma_f32_16x16x32_bf16 a[232:235], v[156:159], v[144:147], a[232:235]\n" \
"s_mov_b32 s41, 0x14000\n" \
"v_or3_b32 v48, v216, v212, s41\n" \
"v_mfma_f32_16x16x32_bf16 a[228:231], v[88:91], v[80:83], a[228:231]\n" \
"s_lshl_b32 s41, s5, 2\n" \
"s_lshl_b32 s7, s7, 3\n" \
"v_mfma_f32_16x16x32_bf16 a[228:231], v[152:155], v[144:147], a[228:231]\n" \
"v_or_b32_e32 v212, s41, v213\n" \
"s_or_b32 s7, s7, s41\n" \
"v_mfma_f32_16x16x32_bf16 a[224:227], v[84:87], v[80:83], a[224:227]\n" \
"s_mov_b32 s41, 0xff800000\n" \
"v_mfma_f32_16x16x32_bf16 a[224:227], v[148:151], v[144:147], a[224:227]\n" \
"s_waitcnt vmcnt(7) lgkmcnt(0)\n" \
"s_barrier\n" \
"v_mfma_f32_16x16x32_bf16 a[16:19], v[100:103], v[52:55], a[16:19]\n" \
"s_add_u32 m0, s30, 0x0\n" \
"s_nop 0\n" \
"s_mov_b32 s85, 0x200\n" \
"v_mfma_f32_16x16x32_bf16 a[16:19], v[164:167], v[116:119], a[16:19]\n" \
"buffer_load_dwordx4 v22, s[64:67], s85 offen lds\n" \
"v_mfma_f32_16x16x32_bf16 a[20:23], v[104:107], v[52:55], a[20:23]\n" \
"ds_read_b64_tr_b16 v[84:85], v32 offset:32768\n" \
"v_mfma_f32_16x16x32_bf16 a[20:23], v[168:171], v[116:119], a[20:23]\n" \
"s_add_u32 m0, s30, 0x400\n" \
"s_mov_b32 s84, 0x2200\n" \
"v_mfma_f32_16x16x32_bf16 a[24:27], v[108:111], v[52:55], a[24:27]\n" \
"buffer_load_dwordx4 v23, s[64:67], s84 offen lds\n" \
"v_mfma_f32_16x16x32_bf16 a[24:27], v[172:175], v[116:119], a[24:27]\n" \
"ds_read_b64_tr_b16 v[86:87], v32 offset:33792\n" \
"v_mfma_f32_16x16x32_bf16 a[28:31], v[112:115], v[52:55], a[28:31]\n" \
"ds_read_b64_tr_b16 v[148:149], v32 offset:40960\n" \
"v_mfma_f32_16x16x32_bf16 a[28:31], v[176:179], v[116:119], a[28:31]\n" \
"ds_read_b128 v[52:55], v28 offset:49152\n" \
"v_mfma_f32_16x16x32_bf16 a[60:63], v[112:115], v[56:59], a[60:63]\n" \
"ds_read_b128 v[116:119], v29 offset:49152\n" \
"v_mfma_f32_16x16x32_bf16 a[60:63], v[176:179], v[120:123], a[60:63]\n" \
"s_add_u32 m0, s31, 0x0\n" \
"s_nop 0\n" \
"s_mov_b32 s85, 0x200000\n" \
"v_mfma_f32_16x16x32_bf16 a[56:59], v[108:111], v[56:59], a[56:59]\n" \
"buffer_load_dwordx4 v26, s[68:71], s85 offen lds\n" \
"v_mfma_f32_16x16x32_bf16 a[56:59], v[172:175], v[120:123], a[56:59]\n" \
"s_add_u32 m0, s31, 0x4000\n" \
"s_mov_b32 s84, 0x200000\n" \
"v_mfma_f32_16x16x32_bf16 a[52:55], v[104:107], v[56:59], a[52:55]\n" \
"buffer_load_dwordx4 v26, s[72:75], s84 offen lds\n" \
"v_mfma_f32_16x16x32_bf16 a[52:55], v[168:171], v[120:123], a[52:55]\n" \
"s_add_u32 m0, s30, 0x800\n" \
"s_mov_b32 s85, 0x4200\n" \
"v_mfma_f32_16x16x32_bf16 a[48:51], v[100:103], v[56:59], a[48:51]\n" \
"buffer_load_dwordx4 v22, s[64:67], s85 offen lds\n" \
"v_mfma_f32_16x16x32_bf16 a[48:51], v[164:167], v[120:123], a[48:51]\n" \
"ds_read_b128 v[56:59], v28 offset:51200\n" \
"v_mfma_f32_16x16x32_bf16 a[80:83], v[100:103], v[60:63], a[80:83]\n" \
"ds_read_b128 v[120:123], v29 offset:51200\n" \
"v_mfma_f32_16x16x32_bf16 a[80:83], v[164:167], v[124:127], a[80:83]\n" \
"s_add_u32 m0, s30, 0xc00\n" \
"s_mov_b32 s84, 0x6200\n" \
"v_mfma_f32_16x16x32_bf16 a[84:87], v[104:107], v[60:63], a[84:87]\n" \
"buffer_load_dwordx4 v23, s[64:67], s84 offen lds\n" \
"v_mfma_f32_16x16x32_bf16 a[84:87], v[168:171], v[124:127], a[84:87]\n" \
"ds_read_b64_tr_b16 v[150:151], v32 offset:41984\n" \
"v_mfma_f32_16x16x32_bf16 a[88:91], v[108:111], v[60:63], a[88:91]\n" \
"s_add_u32 m0, s31, 0x400\n" \
"s_mov_b32 s85, 0x208000\n" \
"v_mfma_f32_16x16x32_bf16 a[88:91], v[172:175], v[124:127], a[88:91]\n" \
"buffer_load_dwordx4 v26, s[68:71], s85 offen lds\n" \
"v_mfma_f32_16x16x32_bf16 a[92:95], v[112:115], v[60:63], a[92:95]\n" \
"ds_read_b64_tr_b16 v[88:89], v33 offset:32768\n" \
"v_mfma_f32_16x16x32_bf16 a[92:95], v[176:179], v[124:127], a[92:95]\n" \
"ds_read_b128 v[60:63], v28 offset:53248\n" \
"v_mfma_f32_16x16x32_bf16 a[124:127], v[112:115], v[64:67], a[124:127]\n" \
"ds_read_b128 v[124:127], v29 offset:53248\n" \
"v_mfma_f32_16x16x32_bf16 a[124:127], v[176:179], v[128:131], a[124:127]\n" \
"s_add_u32 m0, s31, 0x4400\n" \
"s_mov_b32 s84, 0x208000\n" \
"v_mfma_f32_16x16x32_bf16 a[120:123], v[108:111], v[64:67], a[120:123]\n" \
"buffer_load_dwordx4 v26, s[72:75], s84 offen lds\n" \
"v_mfma_f32_16x16x32_bf16 a[120:123], v[172:175], v[128:131], a[120:123]\n" \
"s_add_u32 m0, s30, 0x1000\n" \
"s_mov_b32 s85, 0x8200\n" \
"v_mfma_f32_16x16x32_bf16 a[116:119], v[104:107], v[64:67], a[116:119]\n" \
"buffer_load_dwordx4 v22, s[64:67], s85 offen lds\n" \
"v_mfma_f32_16x16x32_bf16 a[116:119], v[168:171], v[128:131], a[116:119]\n" \
"ds_read_b64_tr_b16 v[90:91], v33 offset:33792\n" \
"v_mfma_f32_16x16x32_bf16 a[112:115], v[100:103], v[64:67], a[112:115]\n" \
"ds_read_b64_tr_b16 v[152:153], v33 offset:40960\n" \
"v_mfma_f32_16x16x32_bf16 a[112:115], v[164:167], v[128:131], a[112:115]\n" \
"ds_read_b128 v[64:67], v28 offset:55296\n" \
"v_mfma_f32_16x16x32_bf16 a[144:147], v[100:103], v[68:71], a[144:147]\n" \
"ds_read_b128 v[128:131], v29 offset:55296\n" \
"v_mfma_f32_16x16x32_bf16 a[144:147], v[164:167], v[132:135], a[144:147]\n" \
"s_add_u32 m0, s30, 0x1400\n" \
"s_mov_b32 s84, 0xa200\n" \
"v_mfma_f32_16x16x32_bf16 a[148:151], v[104:107], v[68:71], a[148:151]\n" \
"buffer_load_dwordx4 v23, s[64:67], s84 offen lds\n" \
"v_mfma_f32_16x16x32_bf16 a[148:151], v[168:171], v[132:135], a[148:151]\n" \
"s_add_u32 m0, s31, 0x800\n" \
"s_mov_b32 s85, 0x210000\n" \
"v_mfma_f32_16x16x32_bf16 a[152:155], v[108:111], v[68:71], a[152:155]\n" \
"buffer_load_dwordx4 v27, s[68:71], s85 offen lds\n" \
"v_mfma_f32_16x16x32_bf16 a[152:155], v[172:175], v[132:135], a[152:155]\n" \
"s_add_u32 m0, s31, 0x4800\n" \
"s_mov_b32 s84, 0x210000\n" \
"v_mfma_f32_16x16x32_bf16 a[156:159], v[112:115], v[68:71], a[156:159]\n" \
"buffer_load_dwordx4 v27, s[72:75], s84 offen lds\n" \
"v_mfma_f32_16x16x32_bf16 a[156:159], v[176:179], v[132:135], a[156:159]\n" \
"ds_read_b128 v[68:71], v28 offset:32768\n" \
"v_mfma_f32_16x16x32_bf16 a[188:191], v[112:115], v[72:75], a[188:191]\n" \
"ds_read_b128 v[132:135], v29 offset:32768\n" \
"v_mfma_f32_16x16x32_bf16 a[188:191], v[176:179], v[136:139], a[188:191]\n" \
"s_add_u32 m0, s30, 0x1800\n" \
"s_mov_b32 s85, 0xc200\n" \
"v_mfma_f32_16x16x32_bf16 a[184:187], v[108:111], v[72:75], a[184:187]\n" \
"buffer_load_dwordx4 v22, s[64:67], s85 offen lds\n" \
"v_mfma_f32_16x16x32_bf16 a[184:187], v[172:175], v[136:139], a[184:187]\n" \
"ds_read_b64_tr_b16 v[154:155], v33 offset:41984\n" \
"v_mfma_f32_16x16x32_bf16 a[180:183], v[104:107], v[72:75], a[180:183]\n" \
"s_add_u32 m0, s30, 0x1c00\n" \
"s_mov_b32 s84, 0xe200\n" \
"v_mfma_f32_16x16x32_bf16 a[180:183], v[168:171], v[136:139], a[180:183]\n" \
"buffer_load_dwordx4 v23, s[64:67], s84 offen lds\n" \
"v_mfma_f32_16x16x32_bf16 a[176:179], v[100:103], v[72:75], a[176:179]\n" \
"ds_read_b64_tr_b16 v[92:93], v34 offset:32768\n" \
"v_mfma_f32_16x16x32_bf16 a[176:179], v[164:167], v[136:139], a[176:179]\n" \
"ds_read_b128 v[72:75], v28 offset:34816\n" \
"v_mfma_f32_16x16x32_bf16 a[208:211], v[100:103], v[76:79], a[208:211]\n" \
"ds_read_b128 v[136:139], v29 offset:34816\n" \
"v_mfma_f32_16x16x32_bf16 a[208:211], v[164:167], v[140:143], a[208:211]\n" \
"ds_read_b64_tr_b16 v[94:95], v34 offset:33792\n" \
"v_mfma_f32_16x16x32_bf16 a[212:215], v[104:107], v[76:79], a[212:215]\n" \
"ds_read_b64_tr_b16 v[156:157], v34 offset:40960\n" \
"v_mfma_f32_16x16x32_bf16 a[212:215], v[168:171], v[140:143], a[212:215]\n" \
"ds_read_b64_tr_b16 v[158:159], v34 offset:41984\n" \
"v_mfma_f32_16x16x32_bf16 a[216:219], v[108:111], v[76:79], a[216:219]\n" \
"ds_read_b64_tr_b16 v[96:97], v35 offset:32768\n" \
"v_mfma_f32_16x16x32_bf16 a[216:219], v[172:175], v[140:143], a[216:219]\n" \
"ds_read_b64_tr_b16 v[98:99], v35 offset:33792\n" \
"v_mfma_f32_16x16x32_bf16 a[220:223], v[112:115], v[76:79], a[220:223]\n" \
"ds_read_b64_tr_b16 v[160:161], v35 offset:40960\n" \
"v_mfma_f32_16x16x32_bf16 a[220:223], v[176:179], v[140:143], a[220:223]\n" \
"ds_read_b128 v[76:79], v28 offset:36864\n" \
"v_mfma_f32_16x16x32_bf16 a[252:255], v[112:115], v[80:83], a[252:255]\n" \
"ds_read_b128 v[140:143], v29 offset:36864\n" \
"v_mfma_f32_16x16x32_bf16 a[252:255], v[176:179], v[144:147], a[252:255]\n" \
"ds_read_b64_tr_b16 v[162:163], v35 offset:41984\n" \
"v_mfma_f32_16x16x32_bf16 a[248:251], v[108:111], v[80:83], a[248:251]\n" \
"s_cmp_lg_u32 s40, 0\n" \
"s_cselect_b32 s47, s42, s41\n" \
"v_mfma_f32_16x16x32_bf16 a[248:251], v[172:175], v[144:147], a[248:251]\n" \
"s_mov_b32 s40, 0xc000\n" \
"s_mov_b32 s41, 0x8000\n" \
"v_mfma_f32_16x16x32_bf16 a[244:247], v[104:107], v[80:83], a[244:247]\n" \
"s_cmp_lg_u32 s4, 0\n" \
"s_cselect_b32 s51, s40, s41\n" \
"v_mfma_f32_16x16x32_bf16 a[244:247], v[168:171], v[144:147], a[244:247]\n" \
"s_movk_i32 s40, 0x4000\n" \
"v_mfma_f32_16x16x32_bf16 a[240:243], v[100:103], v[80:83], a[240:243]\n" \
"s_cmp_lg_u32 s4, 0\n" \
"s_cselect_b32 s53, s41, s40\n" \
"v_mfma_f32_16x16x32_bf16 a[240:243], v[164:167], v[144:147], a[240:243]\n" \
"ds_read_b128 v[80:83], v28 offset:38912\n" \
"ds_read_b128 v[144:147], v29 offset:38912\n" \
"s_waitcnt lgkmcnt(15)\n" \
"v_mfma_f32_16x16x32_bf16 a[0:3], v[84:87], v[52:55], a[0:3]\n" \
"s_add_u32 m0, s31, 0xc00\n" \
"s_mov_b32 s85, 0x218000\n" \
"v_mfma_f32_16x16x32_bf16 a[0:3], v[148:151], v[116:119], a[0:3]\n" \
"buffer_load_dwordx4 v27, s[68:71], s85 offen lds\n" \
"v_mfma_f32_16x16x32_bf16 a[4:7], v[88:91], v[52:55], a[4:7]\n" \
"ds_read_b64_tr_b16 v[100:101], v36 offset:32768\n" \
"s_lshl_b32 s40, s2, 2\n" \
"s_waitcnt lgkmcnt(15)\n" \
"v_mfma_f32_16x16x32_bf16 a[4:7], v[152:155], v[116:119], a[4:7]\n" \
"s_add_u32 m0, s31, 0x4c00\n" \
"s_mov_b32 s84, 0x218000\n" \
"s_waitcnt lgkmcnt(11)\n" \
"v_mfma_f32_16x16x32_bf16 a[8:11], v[92:95], v[52:55], a[8:11]\n" \
"buffer_load_dwordx4 v27, s[72:75], s84 offen lds\n" \
"s_waitcnt lgkmcnt(9)\n" \
"v_mfma_f32_16x16x32_bf16 a[8:11], v[156:159], v[116:119], a[8:11]\n" \
"ds_read_b64_tr_b16 v[102:103], v36 offset:33792\n" \
"s_and_b32 s40, s40, 28\n" \
"s_waitcnt lgkmcnt(8)\n" \
"v_mfma_f32_16x16x32_bf16 a[12:15], v[96:99], v[52:55], a[12:15]\n" \
"ds_read_b64_tr_b16 v[164:165], v36 offset:40960\n" \
"s_bfe_u32 s45, s2, 0x20003\n" \
"s_waitcnt lgkmcnt(5)\n" \
"v_mfma_f32_16x16x32_bf16 a[12:15], v[160:163], v[116:119], a[12:15]\n" \
"s_lshr_b32 s44, s3, 1\n" \
"s_or_b32 s46, s40, s45\n" \
"v_mfma_f32_16x16x32_bf16 a[44:47], v[96:99], v[56:59], a[44:47]\n" \
"s_lshl_b32 s43, s44, 15\n" \
"s_lshl_b32 s41, s5, 14\n" \
"v_mfma_f32_16x16x32_bf16 a[44:47], v[160:163], v[120:123], a[44:47]\n" \
"ds_read_b64_tr_b16 v[166:167], v36 offset:41984\n" \
"s_lshl_b32 s48, s46, 20\n" \
"v_mfma_f32_16x16x32_bf16 a[40:43], v[92:95], v[56:59], a[40:43]\n" \
"s_or_b32 s41, s43, s41\n" \
"s_add_i32 s49, s48, 0x1000000\n" \
"v_mfma_f32_16x16x32_bf16 a[40:43], v[156:159], v[120:123], a[40:43]\n" \
"s_or_b32 s42, s49, s41\n" \
"s_lshl_b32 s4, s4, 16\n" \
"v_mfma_f32_16x16x32_bf16 a[36:39], v[88:91], v[56:59], a[36:39]\n" \
"s_or_b32 s41, s42, s4\n" \
"s_lshl_b32 s4, s6, 16\n" \
"v_mfma_f32_16x16x32_bf16 a[36:39], v[152:155], v[120:123], a[36:39]\n" \
"ds_read_b64_tr_b16 v[104:105], v37 offset:32768\n" \
"s_or_b32 s42, s42, s4\n" \
"v_mfma_f32_16x16x32_bf16 a[32:35], v[84:87], v[56:59], a[32:35]\n" \
"s_lshl_b32 s4, s5, 11\n" \
"s_mul_i32 s5, s7, 0x24800\n" \
"v_mfma_f32_16x16x32_bf16 a[32:35], v[148:151], v[120:123], a[32:35]\n" \
"s_lshl_b32 s6, s46, 9\n" \
"s_lshl_b32 s7, s40, 6\n" \
"v_mfma_f32_16x16x32_bf16 a[64:67], v[84:87], v[60:63], a[64:67]\n" \
"s_waitcnt vmcnt(16)\n" \
"v_or_b32_e32 v204, v210, v204\n" \
"v_or_b32_e32 v206, v210, v206\n" \
"v_mfma_f32_16x16x32_bf16 a[64:67], v[148:151], v[124:127], a[64:67]\n" \
"v_mov_b32_e64 v210, 0\n" \
"s_add_u32 s88, s35, 0x1000\n" \
"v_mfma_f32_16x16x32_bf16 a[68:71], v[88:91], v[60:63], a[68:71]\n" \
"ds_read_b64_tr_b16 v[106:107], v37 offset:33792\n" \
"s_add_i32 s6, s5, s6\n" \
"v_mfma_f32_16x16x32_bf16 a[68:71], v[152:155], v[124:127], a[68:71]\n" \
"ds_read_b64_tr_b16 v[168:169], v37 offset:40960\n" \
"s_or_b32 s5, s5, s7\n" \
"v_mfma_f32_16x16x32_bf16 a[72:75], v[92:95], v[60:63], a[72:75]\n" \
"s_mov_b32 m0, s32\n" \
"s_lshl_b32 s7, s45, 2\n" \
"v_mfma_f32_16x16x32_bf16 a[72:75], v[156:159], v[124:127], a[72:75]\n" \
"s_mov_b32 exec_lo, 0xffffffff\n" \
"s_mov_b32 exec_hi, 0x0\n" \
"global_load_lds_dword v[188:189], off\n" \
"s_mov_b64 exec, -1\n" \
"v_mfma_f32_16x16x32_bf16 a[76:79], v[96:99], v[60:63], a[76:79]\n" \
"ds_read_b64_tr_b16 v[170:171], v37 offset:41984\n" \
"s_or_b32 s5, s5, s7\n" \
"v_mfma_f32_16x16x32_bf16 a[76:79], v[160:163], v[124:127], a[76:79]\n" \
"s_lshl_b32 s7, s3, 12\n" \
"s_or_b32 s50, s49, s7\n" \
"v_mfma_f32_16x16x32_bf16 a[108:111], v[96:99], v[64:67], a[108:111]\n" \
"ds_write_b128 v196, v[200:203]\n" \
"v_mfma_f32_16x16x32_bf16 a[108:111], v[160:163], v[128:131], a[108:111]\n" \
"s_lshl_b32 s7, s3, 11\n" \
"v_lshrrev_b32_e32 v215, 4, v20\n" \
"v_mfma_f32_16x16x32_bf16 a[104:107], v[92:95], v[64:67], a[104:107]\n" \
"ds_read_b64_tr_b16 v[108:109], v38 offset:32768\n" \
"v_bfe_u32 v216, v20, 4, 3\n" \
"v_mfma_f32_16x16x32_bf16 a[104:107], v[156:159], v[128:131], a[104:107]\n" \
"s_add_i32 s52, s48, s7\n" \
"s_add_i32 s53, s53, s7\n" \
"v_mfma_f32_16x16x32_bf16 a[100:103], v[88:91], v[64:67], a[100:103]\n" \
"v_mul_u32_u24_e32 v214, 0x480, v214\n" \
"v_bitop3_b32 v218, v213, v215, 7 bitop3:0x78\n" \
"v_mfma_f32_16x16x32_bf16 a[100:103], v[152:155], v[128:131], a[100:103]\n" \
"ds_read_b64_tr_b16 v[110:111], v38 offset:33792\n" \
"v_bitop3_b32 v213, v213, v216, 4 bitop3:0x36\n" \
"v_mfma_f32_16x16x32_bf16 a[96:99], v[84:87], v[64:67], a[96:99]\n" \
"s_lshl_b32 s7, s3, 10\n" \
"s_or_b32 s4, s43, s4\n" \
"v_mfma_f32_16x16x32_bf16 a[96:99], v[148:151], v[128:131], a[96:99]\n" \
"v_lshl_or_b32 v25, v213, 4, v214\n" \
"v_mul_u32_u24_e32 v213, 0x480, v217\n" \
"v_mfma_f32_16x16x32_bf16 a[128:131], v[84:87], v[68:71], a[128:131]\n" \
"ds_write_b128 v197, v[204:207]\n" \
"v_mfma_f32_16x16x32_bf16 a[128:131], v[148:151], v[132:135], a[128:131]\n" \
"ds_read_b64_tr_b16 v[172:173], v38 offset:40960\n" \
"v_bitop3_b32 v212, v215, v212, 7 bitop3:0x6c\n" \
"v_mfma_f32_16x16x32_bf16 a[132:135], v[88:91], v[68:71], a[132:135]\n" \
"s_or_b32 s40, s7, 0x14000\n" \
"s_lshl_b32 s43, s44, 13\n" \
"v_mfma_f32_16x16x32_bf16 a[132:135], v[152:155], v[132:135], a[132:135]\n" \
"ds_read_b64_tr_b16 v[174:175], v38 offset:41984\n" \
"s_mulk_i32 s44, 0xe800\n" \
"v_mfma_f32_16x16x32_bf16 a[136:139], v[92:95], v[68:71], a[136:139]\n" \
"s_lshl_b32 s7, s3, 8\n" \
"s_nop 0\n" \
"v_lshl_or_b32 v24, v218, 4, v214\n" \
"v_mfma_f32_16x16x32_bf16 a[136:139], v[156:159], v[132:135], a[136:139]\n" \
"v_lshl_or_b32 v21, v212, 4, v213\n" \
"s_add_i32 s44, s44, 0x17000\n" \
"v_mfma_f32_16x16x32_bf16 a[140:143], v[96:99], v[68:71], a[140:143]\n" \
"s_or_b32 s45, s7, 0x27000\n" \
"s_or_b32 s46, s4, 0x10000\n" \
"v_mfma_f32_16x16x32_bf16 a[140:143], v[160:163], v[132:135], a[140:143]\n" \
"ds_read_b64_tr_b16 v[112:113], v39 offset:32768\n" \
"s_add_i32 s48, s6, 0x3000000\n" \
"v_mfma_f32_16x16x32_bf16 a[172:175], v[96:99], v[72:75], a[172:175]\n" \
"ds_write_b128 v198, v[208:211]\n" \
"v_mfma_f32_16x16x32_bf16 a[172:175], v[160:163], v[136:139], a[172:175]\n" \
"s_add_i32 s49, s5, 0x3004000\n" \
"s_add_i32 s52, s52, 0x1010000\n" \
"v_mfma_f32_16x16x32_bf16 a[168:171], v[92:95], v[72:75], a[168:171]\n" \
"ds_read_b64_tr_b16 v[114:115], v39 offset:33792\n" \
"v_mfma_f32_16x16x32_bf16 a[168:171], v[156:159], v[136:139], a[168:171]\n" \
"v_mfma_f32_16x16x32_bf16 a[164:167], v[88:91], v[72:75], a[164:167]\n" \
"v_mfma_f32_16x16x32_bf16 a[164:167], v[152:155], v[136:139], a[164:167]\n" \
"ds_read_b64_tr_b16 v[176:177], v39 offset:40960\n" \
"v_mfma_f32_16x16x32_bf16 a[160:163], v[84:87], v[72:75], a[160:163]\n" \
"v_mfma_f32_16x16x32_bf16 a[160:163], v[148:151], v[136:139], a[160:163]\n" \
"v_mfma_f32_16x16x32_bf16 a[192:195], v[84:87], v[76:79], a[192:195]\n" \
"ds_read_b64_tr_b16 v[178:179], v39 offset:41984\n" \
"v_mfma_f32_16x16x32_bf16 a[192:195], v[148:151], v[140:143], a[192:195]\n" \
"v_mfma_f32_16x16x32_bf16 a[196:199], v[88:91], v[76:79], a[196:199]\n" \
"v_mfma_f32_16x16x32_bf16 a[196:199], v[152:155], v[140:143], a[196:199]\n" \
"v_mfma_f32_16x16x32_bf16 a[200:203], v[92:95], v[76:79], a[200:203]\n" \
"v_mfma_f32_16x16x32_bf16 a[200:203], v[156:159], v[140:143], a[200:203]\n" \
"v_mfma_f32_16x16x32_bf16 a[204:207], v[96:99], v[76:79], a[204:207]\n" \
"v_mfma_f32_16x16x32_bf16 a[204:207], v[160:163], v[140:143], a[204:207]\n" \
"s_waitcnt lgkmcnt(15)\n" \
"v_mfma_f32_16x16x32_bf16 a[236:239], v[96:99], v[80:83], a[236:239]\n" \
"v_mfma_f32_16x16x32_bf16 a[236:239], v[160:163], v[144:147], a[236:239]\n" \
"v_mfma_f32_16x16x32_bf16 a[232:235], v[92:95], v[80:83], a[232:235]\n" \
"v_mfma_f32_16x16x32_bf16 a[232:235], v[156:159], v[144:147], a[232:235]\n" \
"v_mfma_f32_16x16x32_bf16 a[228:231], v[88:91], v[80:83], a[228:231]\n" \
"v_mfma_f32_16x16x32_bf16 a[228:231], v[152:155], v[144:147], a[228:231]\n" \
"v_mfma_f32_16x16x32_bf16 a[224:227], v[84:87], v[80:83], a[224:227]\n" \
"v_mfma_f32_16x16x32_bf16 a[224:227], v[148:151], v[144:147], a[224:227]\n" \
"s_waitcnt vmcnt(1) lgkmcnt(0)\n" \
"s_barrier\n" \
"s_nop 0\n" \
"v_mfma_f32_16x16x32_bf16 a[16:19], v[100:103], v[52:55], a[16:19]\n" \
"s_add_u32 m0, s30, 0x8000\n" \
"s_mov_b32 s85, 0x280\n" \
"v_mfma_f32_16x16x32_bf16 a[16:19], v[164:167], v[116:119], a[16:19]\n" \
"buffer_load_dwordx4 v22, s[64:67], s85 offen lds\n" \
"v_mfma_f32_16x16x32_bf16 a[20:23], v[104:107], v[52:55], a[20:23]\n" \
"s_add_u32 m0, s30, 0x8400\n" \
"s_mov_b32 s84, 0x2280\n" \
"v_mfma_f32_16x16x32_bf16 a[20:23], v[168:171], v[116:119], a[20:23]\n" \
"buffer_load_dwordx4 v23, s[64:67], s84 offen lds\n" \
"v_mfma_f32_16x16x32_bf16 a[24:27], v[108:111], v[52:55], a[24:27]\n" \
"ds_read_b64_tr_b16 v[84:85], v32 offset:0\n" \
"v_mfma_f32_16x16x32_bf16 a[24:27], v[172:175], v[116:119], a[24:27]\n" \
"s_add_u32 m0, s31, 0x8000\n" \
"s_mov_b32 s85, 0x280000\n" \
"v_mfma_f32_16x16x32_bf16 a[28:31], v[112:115], v[52:55], a[28:31]\n" \
"buffer_load_dwordx4 v26, s[68:71], s85 offen lds\n" \
"v_mfma_f32_16x16x32_bf16 a[28:31], v[176:179], v[116:119], a[28:31]\n" \
"ds_read_b128 v[52:55], v28 offset:16384\n" \
"v_mfma_f32_16x16x32_bf16 a[60:63], v[112:115], v[56:59], a[60:63]\n" \
"ds_read_b128 v[116:119], v29 offset:16384\n" \
"v_mfma_f32_16x16x32_bf16 a[60:63], v[176:179], v[120:123], a[60:63]\n" \
"s_add_u32 m0, s31, 0xc000\n" \
"s_mov_b32 s84, 0x280000\n" \
"v_mfma_f32_16x16x32_bf16 a[56:59], v[108:111], v[56:59], a[56:59]\n" \
"buffer_load_dwordx4 v26, s[72:75], s84 offen lds\n" \
"v_mfma_f32_16x16x32_bf16 a[56:59], v[172:175], v[120:123], a[56:59]\n" \
"s_add_u32 m0, s30, 0x8800\n" \
"s_mov_b32 s85, 0x4280\n" \
"v_mfma_f32_16x16x32_bf16 a[52:55], v[104:107], v[56:59], a[52:55]\n" \
"buffer_load_dwordx4 v22, s[64:67], s85 offen lds\n" \
"v_mfma_f32_16x16x32_bf16 a[52:55], v[168:171], v[120:123], a[52:55]\n" \
"ds_read_b64_tr_b16 v[86:87], v32 offset:1024\n" \
"v_mfma_f32_16x16x32_bf16 a[48:51], v[100:103], v[56:59], a[48:51]\n" \
"ds_read_b64_tr_b16 v[148:149], v32 offset:8192\n" \
"v_mfma_f32_16x16x32_bf16 a[48:51], v[164:167], v[120:123], a[48:51]\n" \
"ds_read_b128 v[56:59], v28 offset:18432\n" \
"v_mfma_f32_16x16x32_bf16 a[80:83], v[100:103], v[60:63], a[80:83]\n" \
"ds_read_b128 v[120:123], v29 offset:18432\n" \
"v_mfma_f32_16x16x32_bf16 a[80:83], v[164:167], v[124:127], a[80:83]\n" \
"s_add_u32 m0, s30, 0x8c00\n" \
"s_mov_b32 s84, 0x6280\n" \
"v_mfma_f32_16x16x32_bf16 a[84:87], v[104:107], v[60:63], a[84:87]\n" \
"buffer_load_dwordx4 v23, s[64:67], s84 offen lds\n" \
"v_mfma_f32_16x16x32_bf16 a[84:87], v[168:171], v[124:127], a[84:87]\n" \
"s_add_u32 m0, s31, 0x8400\n" \
"s_mov_b32 s85, 0x288000\n" \
"v_mfma_f32_16x16x32_bf16 a[88:91], v[108:111], v[60:63], a[88:91]\n" \
"buffer_load_dwordx4 v26, s[68:71], s85 offen lds\n" \
"v_mfma_f32_16x16x32_bf16 a[88:91], v[172:175], v[124:127], a[88:91]\n" \
"s_add_u32 m0, s31, 0xc400\n" \
"s_mov_b32 s84, 0x288000\n" \
"v_mfma_f32_16x16x32_bf16 a[92:95], v[112:115], v[60:63], a[92:95]\n" \
"buffer_load_dwordx4 v26, s[72:75], s84 offen lds\n" \
"v_mfma_f32_16x16x32_bf16 a[92:95], v[176:179], v[124:127], a[92:95]\n" \
"ds_read_b128 v[60:63], v28 offset:20480\n" \
"v_mfma_f32_16x16x32_bf16 a[124:127], v[112:115], v[64:67], a[124:127]\n" \
"ds_read_b128 v[124:127], v29 offset:20480\n" \
"v_mfma_f32_16x16x32_bf16 a[124:127], v[176:179], v[128:131], a[124:127]\n" \
"s_add_u32 m0, s30, 0x9000\n" \
"s_mov_b32 s85, 0x8280\n" \
"v_mfma_f32_16x16x32_bf16 a[120:123], v[108:111], v[64:67], a[120:123]\n" \
"buffer_load_dwordx4 v22, s[64:67], s85 offen lds\n" \
"v_mfma_f32_16x16x32_bf16 a[120:123], v[172:175], v[128:131], a[120:123]\n" \
"s_add_u32 m0, s30, 0x9400\n" \
"s_mov_b32 s84, 0xa280\n" \
"v_mfma_f32_16x16x32_bf16 a[116:119], v[104:107], v[64:67], a[116:119]\n" \
"buffer_load_dwordx4 v23, s[64:67], s84 offen lds\n" \
"v_mfma_f32_16x16x32_bf16 a[116:119], v[168:171], v[128:131], a[116:119]\n" \
"s_add_u32 m0, s31, 0x8800\n" \
"s_mov_b32 s85, 0x290000\n" \
"v_mfma_f32_16x16x32_bf16 a[112:115], v[100:103], v[64:67], a[112:115]\n" \
"buffer_load_dwordx4 v27, s[68:71], s85 offen lds\n" \
"v_mfma_f32_16x16x32_bf16 a[112:115], v[164:167], v[128:131], a[112:115]\n" \
"ds_read_b128 v[64:67], v28 offset:22528\n" \
"v_mfma_f32_16x16x32_bf16 a[144:147], v[100:103], v[68:71], a[144:147]\n" \
"ds_read_b128 v[128:131], v29 offset:22528\n" \
"v_mfma_f32_16x16x32_bf16 a[144:147], v[164:167], v[132:135], a[144:147]\n" \
"s_add_u32 m0, s31, 0xc800\n" \
"s_mov_b32 s84, 0x290000\n" \
"v_mfma_f32_16x16x32_bf16 a[148:151], v[104:107], v[68:71], a[148:151]\n" \
"buffer_load_dwordx4 v27, s[72:75], s84 offen lds\n" \
"v_mfma_f32_16x16x32_bf16 a[148:151], v[168:171], v[132:135], a[148:151]\n" \
"s_add_u32 m0, s30, 0x9800\n" \
"s_mov_b32 s85, 0xc280\n" \
"v_mfma_f32_16x16x32_bf16 a[152:155], v[108:111], v[68:71], a[152:155]\n" \
"buffer_load_dwordx4 v22, s[64:67], s85 offen lds\n" \
"v_mfma_f32_16x16x32_bf16 a[152:155], v[172:175], v[132:135], a[152:155]\n" \
"s_add_u32 m0, s30, 0x9c00\n" \
"s_mov_b32 s84, 0xe280\n" \
"v_mfma_f32_16x16x32_bf16 a[156:159], v[112:115], v[68:71], a[156:159]\n" \
"buffer_load_dwordx4 v23, s[64:67], s84 offen lds\n" \
"v_mfma_f32_16x16x32_bf16 a[156:159], v[176:179], v[132:135], a[156:159]\n" \
"ds_read_b128 v[68:71], v28 offset:0\n" \
"v_mfma_f32_16x16x32_bf16 a[188:191], v[112:115], v[72:75], a[188:191]\n" \
"ds_read_b128 v[132:135], v29 offset:0\n" \
"v_mfma_f32_16x16x32_bf16 a[188:191], v[176:179], v[136:139], a[188:191]\n" \
"ds_read_b128 v[200:203], v199 offset:0\n" \
"v_mfma_f32_16x16x32_bf16 a[184:187], v[108:111], v[72:75], a[184:187]\n" \
"ds_read_b128 v[204:207], v199 offset:4096\n" \
"v_mfma_f32_16x16x32_bf16 a[184:187], v[172:175], v[136:139], a[184:187]\n" \
"ds_read_b64_tr_b16 v[150:151], v32 offset:9216\n" \
"v_mfma_f32_16x16x32_bf16 a[180:183], v[104:107], v[72:75], a[180:183]\n" \
"ds_read_b64_tr_b16 v[88:89], v33 offset:0\n" \
"v_mfma_f32_16x16x32_bf16 a[180:183], v[168:171], v[136:139], a[180:183]\n" \
"ds_read_b128 v[208:211], v199 offset:8192\n" \
"v_mfma_f32_16x16x32_bf16 a[176:179], v[100:103], v[72:75], a[176:179]\n" \
"ds_read_b64_tr_b16 v[90:91], v33 offset:1024\n" \
"v_mfma_f32_16x16x32_bf16 a[176:179], v[164:167], v[136:139], a[176:179]\n" \
"ds_read_b128 v[72:75], v28 offset:2048\n" \
"v_mfma_f32_16x16x32_bf16 a[208:211], v[100:103], v[76:79], a[208:211]\n" \
"ds_read_b128 v[136:139], v29 offset:2048\n" \
"v_mfma_f32_16x16x32_bf16 a[208:211], v[164:167], v[140:143], a[208:211]\n" \
".fill (L_pb + 12000 - .) / 4, 4, 0xBF800000\n" \
"L_pp3:\n" \
"s_cbranch_execz L_pret\n" \
"s_nop 0\n" \
"ds_read_b64_tr_b16 v[152:153], v33 offset:8192\n" \
"v_mfma_f32_16x16x32_bf16 a[212:215], v[104:107], v[76:79], a[212:215]\n" \
"ds_read_b64_tr_b16 v[154:155], v33 offset:9216\n" \
"v_mfma_f32_16x16x32_bf16 a[212:215], v[168:171], v[140:143], a[212:215]\n" \
"ds_read_b64_tr_b16 v[92:93], v34 offset:0\n" \
"v_mfma_f32_16x16x32_bf16 a[216:219], v[108:111], v[76:79], a[216:219]\n" \
"ds_read_b64_tr_b16 v[94:95], v34 offset:1024\n" \
"v_mfma_f32_16x16x32_bf16 a[216:219], v[172:175], v[140:143], a[216:219]\n" \
"ds_read_b64_tr_b16 v[156:157], v34 offset:8192\n" \
"v_mfma_f32_16x16x32_bf16 a[220:223], v[112:115], v[76:79], a[220:223]\n" \
"ds_read_b64_tr_b16 v[158:159], v34 offset:9216\n" \
"v_mfma_f32_16x16x32_bf16 a[220:223], v[176:179], v[140:143], a[220:223]\n" \
"ds_read_b128 v[76:79], v28 offset:4096\n" \
"v_mfma_f32_16x16x32_bf16 a[252:255], v[112:115], v[80:83], a[252:255]\n" \
"ds_read_b128 v[140:143], v29 offset:4096\n" \
"v_mfma_f32_16x16x32_bf16 a[252:255], v[176:179], v[144:147], a[252:255]\n" \
"ds_read_b64_tr_b16 v[96:97], v35 offset:0\n" \
"v_mfma_f32_16x16x32_bf16 a[248:251], v[108:111], v[80:83], a[248:251]\n" \
"ds_read_b64_tr_b16 v[98:99], v35 offset:1024\n" \
"v_mfma_f32_16x16x32_bf16 a[248:251], v[172:175], v[144:147], a[248:251]\n" \
"ds_read_b64_tr_b16 v[160:161], v35 offset:8192\n" \
"v_mfma_f32_16x16x32_bf16 a[244:247], v[104:107], v[80:83], a[244:247]\n" \
"ds_read_b64_tr_b16 v[162:163], v35 offset:9216\n" \
"v_mfma_f32_16x16x32_bf16 a[244:247], v[168:171], v[144:147], a[244:247]\n" \
"v_mfma_f32_16x16x32_bf16 a[240:243], v[100:103], v[80:83], a[240:243]\n" \
"v_mfma_f32_16x16x32_bf16 a[240:243], v[164:167], v[144:147], a[240:243]\n" \
"ds_read_b128 v[80:83], v28 offset:6144\n" \
"ds_read_b128 v[144:147], v29 offset:6144\n" \
"s_waitcnt lgkmcnt(15)\n" \
"v_mfma_f32_16x16x32_bf16 a[0:3], v[84:87], v[52:55], a[0:3]\n" \
"s_add_u32 m0, s31, 0x8c00\n" \
"s_mov_b32 s85, 0x298000\n" \
"v_mfma_f32_16x16x32_bf16 a[0:3], v[148:151], v[116:119], a[0:3]\n" \
"buffer_load_dwordx4 v27, s[68:71], s85 offen lds\n" \
"v_mfma_f32_16x16x32_bf16 a[4:7], v[88:91], v[52:55], a[4:7]\n" \
"ds_read_b64_tr_b16 v[100:101], v36 offset:0\n" \
"s_waitcnt lgkmcnt(13)\n" \
"v_mfma_f32_16x16x32_bf16 a[4:7], v[152:155], v[116:119], a[4:7]\n" \
"s_add_u32 m0, s31, 0xcc00\n" \
"s_mov_b32 s84, 0x298000\n" \
"s_waitcnt lgkmcnt(11)\n" \
"v_mfma_f32_16x16x32_bf16 a[8:11], v[92:95], v[52:55], a[8:11]\n" \
"buffer_load_dwordx4 v27, s[72:75], s84 offen lds\n" \
"s_waitcnt lgkmcnt(9)\n" \
"v_mfma_f32_16x16x32_bf16 a[8:11], v[156:159], v[116:119], a[8:11]\n" \
"ds_read_b64_tr_b16 v[102:103], v36 offset:1024\n" \
"s_waitcnt lgkmcnt(6)\n" \
"v_mfma_f32_16x16x32_bf16 a[12:15], v[96:99], v[52:55], a[12:15]\n" \
"ds_read_b64_tr_b16 v[164:165], v36 offset:8192\n" \
"s_waitcnt lgkmcnt(5)\n" \
"v_mfma_f32_16x16x32_bf16 a[12:15], v[160:163], v[116:119], a[12:15]\n" \
"v_mfma_f32_16x16x32_bf16 a[44:47], v[96:99], v[56:59], a[44:47]\n" \
"v_mfma_f32_16x16x32_bf16 a[44:47], v[160:163], v[120:123], a[44:47]\n" \
"ds_read_b64_tr_b16 v[166:167], v36 offset:9216\n" \
"v_mfma_f32_16x16x32_bf16 a[40:43], v[92:95], v[56:59], a[40:43]\n" \
"v_mfma_f32_16x16x32_bf16 a[40:43], v[156:159], v[120:123], a[40:43]\n" \
"v_mfma_f32_16x16x32_bf16 a[36:39], v[88:91], v[56:59], a[36:39]\n" \
"v_mfma_f32_16x16x32_bf16 a[36:39], v[152:155], v[120:123], a[36:39]\n" \
"ds_read_b64_tr_b16 v[104:105], v37 offset:0\n" \
"v_mfma_f32_16x16x32_bf16 a[32:35], v[84:87], v[56:59], a[32:35]\n" \
"v_mfma_f32_16x16x32_bf16 a[32:35], v[148:151], v[120:123], a[32:35]\n" \
"v_mfma_f32_16x16x32_bf16 a[64:67], v[84:87], v[60:63], a[64:67]\n" \
"ds_read_b64_tr_b16 v[106:107], v37 offset:1024\n" \
"v_mfma_f32_16x16x32_bf16 a[64:67], v[148:151], v[124:127], a[64:67]\n" \
"v_mfma_f32_16x16x32_bf16 a[68:71], v[88:91], v[60:63], a[68:71]\n" \
"v_mfma_f32_16x16x32_bf16 a[68:71], v[152:155], v[124:127], a[68:71]\n" \
"buffer_store_dwordx4 v[200:203], v20, s[80:83], s35 offen sc1\n" \
"v_mfma_f32_16x16x32_bf16 a[72:75], v[92:95], v[60:63], a[72:75]\n" \
"s_mov_b32 m0, s32\n" \
"ds_read_b64_tr_b16 v[168:169], v37 offset:8192\n" \
"v_mfma_f32_16x16x32_bf16 a[72:75], v[156:159], v[124:127], a[72:75]\n" \
"s_mov_b32 exec_lo, 0x0\n" \
"s_mov_b32 exec_hi, 0xffffffff\n" \
"global_load_lds_dword v[188:189], off\n" \
"s_mov_b64 exec, -1\n" \
"v_mfma_f32_16x16x32_bf16 a[76:79], v[96:99], v[60:63], a[76:79]\n" \
"ds_read_b64_tr_b16 v[170:171], v37 offset:9216\n" \
"v_mfma_f32_16x16x32_bf16 a[76:79], v[160:163], v[124:127], a[76:79]\n" \
"v_mfma_f32_16x16x32_bf16 a[108:111], v[96:99], v[64:67], a[108:111]\n" \
"v_mfma_f32_16x16x32_bf16 a[108:111], v[160:163], v[128:131], a[108:111]\n" \
"v_mfma_f32_16x16x32_bf16 a[104:107], v[92:95], v[64:67], a[104:107]\n" \
"ds_read_b64_tr_b16 v[108:109], v38 offset:0\n" \
"v_mfma_f32_16x16x32_bf16 a[104:107], v[156:159], v[128:131], a[104:107]\n" \
"v_mfma_f32_16x16x32_bf16 a[100:103], v[88:91], v[64:67], a[100:103]\n" \
"buffer_store_dwordx4 v[204:207], v20, s[80:83], s88 offen sc1\n" \
"v_mfma_f32_16x16x32_bf16 a[100:103], v[152:155], v[128:131], a[100:103]\n" \
"ds_read_b64_tr_b16 v[110:111], v38 offset:1024\n" \
"v_mfma_f32_16x16x32_bf16 a[96:99], v[84:87], v[64:67], a[96:99]\n" \
"v_mfma_f32_16x16x32_bf16 a[96:99], v[148:151], v[128:131], a[96:99]\n" \
"v_mfma_f32_16x16x32_bf16 a[128:131], v[84:87], v[68:71], a[128:131]\n" \
"ds_read_b64_tr_b16 v[172:173], v38 offset:8192\n" \
"v_mfma_f32_16x16x32_bf16 a[128:131], v[148:151], v[132:135], a[128:131]\n" \
"v_mfma_f32_16x16x32_bf16 a[132:135], v[88:91], v[68:71], a[132:135]\n" \
"v_mfma_f32_16x16x32_bf16 a[132:135], v[152:155], v[132:135], a[132:135]\n" \
"ds_read_b64_tr_b16 v[174:175], v38 offset:9216\n" \
"v_mfma_f32_16x16x32_bf16 a[136:139], v[92:95], v[68:71], a[136:139]\n" \
"v_mfma_f32_16x16x32_bf16 a[136:139], v[156:159], v[132:135], a[136:139]\n" \
"v_mfma_f32_16x16x32_bf16 a[140:143], v[96:99], v[68:71], a[140:143]\n" \
"buffer_store_dwordx4 v[208:211], v20, s[80:83], s36 offen sc1\n" \
"v_mfma_f32_16x16x32_bf16 a[140:143], v[160:163], v[132:135], a[140:143]\n" \
"ds_read_b64_tr_b16 v[112:113], v39 offset:0\n" \
"v_mfma_f32_16x16x32_bf16 a[172:175], v[96:99], v[72:75], a[172:175]\n" \
"v_mfma_f32_16x16x32_bf16 a[172:175], v[160:163], v[136:139], a[172:175]\n" \
"v_mfma_f32_16x16x32_bf16 a[168:171], v[92:95], v[72:75], a[168:171]\n" \
"ds_read_b64_tr_b16 v[114:115], v39 offset:1024\n" \
"v_mfma_f32_16x16x32_bf16 a[168:171], v[156:159], v[136:139], a[168:171]\n" \
"v_mfma_f32_16x16x32_bf16 a[164:167], v[88:91], v[72:75], a[164:167]\n" \
"v_mfma_f32_16x16x32_bf16 a[164:167], v[152:155], v[136:139], a[164:167]\n" \
"ds_read_b64_tr_b16 v[176:177], v39 offset:8192\n" \
"v_mfma_f32_16x16x32_bf16 a[160:163], v[84:87], v[72:75], a[160:163]\n" \
"v_mfma_f32_16x16x32_bf16 a[160:163], v[148:151], v[136:139], a[160:163]\n" \
"v_mfma_f32_16x16x32_bf16 a[192:195], v[84:87], v[76:79], a[192:195]\n" \
"ds_read_b64_tr_b16 v[178:179], v39 offset:9216\n" \
"v_mfma_f32_16x16x32_bf16 a[192:195], v[148:151], v[140:143], a[192:195]\n" \
"v_mfma_f32_16x16x32_bf16 a[196:199], v[88:91], v[76:79], a[196:199]\n" \
"v_mfma_f32_16x16x32_bf16 a[196:199], v[152:155], v[140:143], a[196:199]\n" \
"v_mfma_f32_16x16x32_bf16 a[200:203], v[92:95], v[76:79], a[200:203]\n" \
"v_mfma_f32_16x16x32_bf16 a[200:203], v[156:159], v[140:143], a[200:203]\n" \
"v_mfma_f32_16x16x32_bf16 a[204:207], v[96:99], v[76:79], a[204:207]\n" \
"v_mfma_f32_16x16x32_bf16 a[204:207], v[160:163], v[140:143], a[204:207]\n" \
"s_waitcnt lgkmcnt(15)\n" \
"v_mfma_f32_16x16x32_bf16 a[236:239], v[96:99], v[80:83], a[236:239]\n" \
"v_mfma_f32_16x16x32_bf16 a[236:239], v[160:163], v[144:147], a[236:239]\n" \
"v_mfma_f32_16x16x32_bf16 a[232:235], v[92:95], v[80:83], a[232:235]\n" \
"v_mfma_f32_16x16x32_bf16 a[232:235], v[156:159], v[144:147], a[232:235]\n" \
"v_mfma_f32_16x16x32_bf16 a[228:231], v[88:91], v[80:83], a[228:231]\n" \
"v_mfma_f32_16x16x32_bf16 a[228:231], v[152:155], v[144:147], a[228:231]\n" \
"v_mfma_f32_16x16x32_bf16 a[224:227], v[84:87], v[80:83], a[224:227]\n" \
"v_mfma_f32_16x16x32_bf16 a[224:227], v[148:151], v[144:147], a[224:227]\n" \
"s_waitcnt vmcnt(4) lgkmcnt(0)\n" \
"s_barrier\n" \
"v_mfma_f32_16x16x32_bf16 a[16:19], v[100:103], v[52:55], a[16:19]\n" \
"s_add_u32 m0, s30, 0x0\n" \
"s_mov_b32 s85, 0x300\n" \
"v_mfma_f32_16x16x32_bf16 a[16:19], v[164:167], v[116:119], a[16:19]\n" \
"buffer_load_dwordx4 v22, s[64:67], s85 offen lds\n" \
"v_mfma_f32_16x16x32_bf16 a[20:23], v[104:107], v[52:55], a[20:23]\n" \
"ds_read_b64_tr_b16 v[84:85], v32 offset:32768\n" \
"v_mfma_f32_16x16x32_bf16 a[20:23], v[168:171], v[116:119], a[20:23]\n" \
"s_add_u32 m0, s30, 0x400\n" \
"s_mov_b32 s84, 0x2300\n" \
"v_mfma_f32_16x16x32_bf16 a[24:27], v[108:111], v[52:55], a[24:27]\n" \
"buffer_load_dwordx4 v23, s[64:67], s84 offen lds\n" \
"v_mfma_f32_16x16x32_bf16 a[24:27], v[172:175], v[116:119], a[24:27]\n" \
"ds_read_b64_tr_b16 v[86:87], v32 offset:33792\n" \
"v_mfma_f32_16x16x32_bf16 a[28:31], v[112:115], v[52:55], a[28:31]\n" \
"ds_read_b64_tr_b16 v[148:149], v32 offset:40960\n" \
"v_mfma_f32_16x16x32_bf16 a[28:31], v[176:179], v[116:119], a[28:31]\n" \
"ds_read_b128 v[52:55], v28 offset:49152\n" \
"v_mfma_f32_16x16x32_bf16 a[60:63], v[112:115], v[56:59], a[60:63]\n" \
"ds_read_b128 v[116:119], v29 offset:49152\n" \
"v_mfma_f32_16x16x32_bf16 a[60:63], v[176:179], v[120:123], a[60:63]\n" \
"s_add_u32 m0, s31, 0x0\n" \
"s_nop 0\n" \
"s_mov_b32 s85, 0x300000\n" \
"v_mfma_f32_16x16x32_bf16 a[56:59], v[108:111], v[56:59], a[56:59]\n" \
"buffer_load_dwordx4 v26, s[68:71], s85 offen lds\n" \
"v_mfma_f32_16x16x32_bf16 a[56:59], v[172:175], v[120:123], a[56:59]\n" \
"s_add_u32 m0, s31, 0x4000\n" \
"s_mov_b32 s84, 0x300000\n" \
"v_mfma_f32_16x16x32_bf16 a[52:55], v[104:107], v[56:59], a[52:55]\n" \
"buffer_load_dwordx4 v26, s[72:75], s84 offen lds\n" \
"v_mfma_f32_16x16x32_bf16 a[52:55], v[168:171], v[120:123], a[52:55]\n" \
"s_add_u32 m0, s30, 0x800\n" \
"s_mov_b32 s85, 0x4300\n" \
"v_mfma_f32_16x16x32_bf16 a[48:51], v[100:103], v[56:59], a[48:51]\n" \
"buffer_load_dwordx4 v22, s[64:67], s85 offen lds\n" \
"v_mfma_f32_16x16x32_bf16 a[48:51], v[164:167], v[120:123], a[48:51]\n" \
"ds_read_b128 v[56:59], v28 offset:51200\n" \
"v_mfma_f32_16x16x32_bf16 a[80:83], v[100:103], v[60:63], a[80:83]\n" \
"ds_read_b128 v[120:123], v29 offset:51200\n" \
"v_mfma_f32_16x16x32_bf16 a[80:83], v[164:167], v[124:127], a[80:83]\n" \
"s_add_u32 m0, s30, 0xc00\n" \
"s_mov_b32 s84, 0x6300\n" \
"v_mfma_f32_16x16x32_bf16 a[84:87], v[104:107], v[60:63], a[84:87]\n" \
"buffer_load_dwordx4 v23, s[64:67], s84 offen lds\n" \
"v_mfma_f32_16x16x32_bf16 a[84:87], v[168:171], v[124:127], a[84:87]\n" \
"ds_read_b64_tr_b16 v[150:151], v32 offset:41984\n" \
"v_mfma_f32_16x16x32_bf16 a[88:91], v[108:111], v[60:63], a[88:91]\n" \
"s_add_u32 m0, s31, 0x400\n" \
"s_mov_b32 s85, 0x308000\n" \
"v_mfma_f32_16x16x32_bf16 a[88:91], v[172:175], v[124:127], a[88:91]\n" \
"buffer_load_dwordx4 v26, s[68:71], s85 offen lds\n" \
"v_mfma_f32_16x16x32_bf16 a[92:95], v[112:115], v[60:63], a[92:95]\n" \
"ds_read_b64_tr_b16 v[88:89], v33 offset:32768\n" \
"v_mfma_f32_16x16x32_bf16 a[92:95], v[176:179], v[124:127], a[92:95]\n" \
"ds_read_b128 v[60:63], v28 offset:53248\n" \
"v_mfma_f32_16x16x32_bf16 a[124:127], v[112:115], v[64:67], a[124:127]\n" \
"ds_read_b128 v[124:127], v29 offset:53248\n" \
"v_mfma_f32_16x16x32_bf16 a[124:127], v[176:179], v[128:131], a[124:127]\n" \
"s_add_u32 m0, s31, 0x4400\n" \
"s_mov_b32 s84, 0x308000\n" \
"v_mfma_f32_16x16x32_bf16 a[120:123], v[108:111], v[64:67], a[120:123]\n" \
"buffer_load_dwordx4 v26, s[72:75], s84 offen lds\n" \
"v_mfma_f32_16x16x32_bf16 a[120:123], v[172:175], v[128:131], a[120:123]\n" \
"s_add_u32 m0, s30, 0x1000\n" \
"s_mov_b32 s85, 0x8300\n" \
"v_mfma_f32_16x16x32_bf16 a[116:119], v[104:107], v[64:67], a[116:119]\n" \
"buffer_load_dwordx4 v22, s[64:67], s85 offen lds\n" \
"v_mfma_f32_16x16x32_bf16 a[116:119], v[168:171], v[128:131], a[116:119]\n" \
"ds_read_b64_tr_b16 v[90:91], v33 offset:33792\n" \
"v_mfma_f32_16x16x32_bf16 a[112:115], v[100:103], v[64:67], a[112:115]\n" \
"ds_read_b64_tr_b16 v[152:153], v33 offset:40960\n" \
"v_mfma_f32_16x16x32_bf16 a[112:115], v[164:167], v[128:131], a[112:115]\n" \
"ds_read_b128 v[64:67], v28 offset:55296\n" \
"v_mfma_f32_16x16x32_bf16 a[144:147], v[100:103], v[68:71], a[144:147]\n" \
"ds_read_b128 v[128:131], v29 offset:55296\n" \
"v_mfma_f32_16x16x32_bf16 a[144:147], v[164:167], v[132:135], a[144:147]\n" \
"s_add_u32 m0, s30, 0x1400\n" \
"s_mov_b32 s84, 0xa300\n" \
"v_mfma_f32_16x16x32_bf16 a[148:151], v[104:107], v[68:71], a[148:151]\n" \
"buffer_load_dwordx4 v23, s[64:67], s84 offen lds\n" \
"v_mfma_f32_16x16x32_bf16 a[148:151], v[168:171], v[132:135], a[148:151]\n" \
"s_add_u32 m0, s31, 0x800\n" \
"s_mov_b32 s85, 0x310000\n" \
"v_mfma_f32_16x16x32_bf16 a[152:155], v[108:111], v[68:71], a[152:155]\n" \
"buffer_load_dwordx4 v27, s[68:71], s85 offen lds\n" \
"v_mfma_f32_16x16x32_bf16 a[152:155], v[172:175], v[132:135], a[152:155]\n" \
"s_add_u32 m0, s31, 0x4800\n" \
"s_mov_b32 s84, 0x310000\n" \
"v_mfma_f32_16x16x32_bf16 a[156:159], v[112:115], v[68:71], a[156:159]\n" \
"buffer_load_dwordx4 v27, s[72:75], s84 offen lds\n" \
"v_mfma_f32_16x16x32_bf16 a[156:159], v[176:179], v[132:135], a[156:159]\n" \
"ds_read_b128 v[68:71], v28 offset:32768\n" \
"v_mfma_f32_16x16x32_bf16 a[188:191], v[112:115], v[72:75], a[188:191]\n" \
"ds_read_b128 v[132:135], v29 offset:32768\n" \
"v_mfma_f32_16x16x32_bf16 a[188:191], v[176:179], v[136:139], a[188:191]\n" \
"s_add_u32 m0, s30, 0x1800\n" \
"s_mov_b32 s85, 0xc300\n" \
"v_mfma_f32_16x16x32_bf16 a[184:187], v[108:111], v[72:75], a[184:187]\n" \
"buffer_load_dwordx4 v22, s[64:67], s85 offen lds\n" \
"v_mfma_f32_16x16x32_bf16 a[184:187], v[172:175], v[136:139], a[184:187]\n" \
"ds_read_b64_tr_b16 v[154:155], v33 offset:41984\n" \
"v_mfma_f32_16x16x32_bf16 a[180:183], v[104:107], v[72:75], a[180:183]\n" \
"s_add_u32 m0, s30, 0x1c00\n" \
"s_mov_b32 s84, 0xe300\n" \
"v_mfma_f32_16x16x32_bf16 a[180:183], v[168:171], v[136:139], a[180:183]\n" \
"buffer_load_dwordx4 v23, s[64:67], s84 offen lds\n" \
"v_mfma_f32_16x16x32_bf16 a[176:179], v[100:103], v[72:75], a[176:179]\n" \
"ds_read_b64_tr_b16 v[92:93], v34 offset:32768\n" \
"v_mfma_f32_16x16x32_bf16 a[176:179], v[164:167], v[136:139], a[176:179]\n" \
"ds_read_b128 v[72:75], v28 offset:34816\n" \
"v_mfma_f32_16x16x32_bf16 a[208:211], v[100:103], v[76:79], a[208:211]\n" \
"ds_read_b128 v[136:139], v29 offset:34816\n" \
"v_mfma_f32_16x16x32_bf16 a[208:211], v[164:167], v[140:143], a[208:211]\n" \
"ds_read_b64_tr_b16 v[94:95], v34 offset:33792\n" \
"v_mfma_f32_16x16x32_bf16 a[212:215], v[104:107], v[76:79], a[212:215]\n" \
"ds_read_b64_tr_b16 v[156:157], v34 offset:40960\n" \
"v_mfma_f32_16x16x32_bf16 a[212:215], v[168:171], v[140:143], a[212:215]\n" \
"ds_read_b64_tr_b16 v[158:159], v34 offset:41984\n" \
"v_mfma_f32_16x16x32_bf16 a[216:219], v[108:111], v[76:79], a[216:219]\n" \
"ds_read_b64_tr_b16 v[96:97], v35 offset:32768\n" \
"v_mfma_f32_16x16x32_bf16 a[216:219], v[172:175], v[140:143], a[216:219]\n" \
"ds_read_b64_tr_b16 v[98:99], v35 offset:33792\n" \
"v_mfma_f32_16x16x32_bf16 a[220:223], v[112:115], v[76:79], a[220:223]\n" \
"ds_read_b64_tr_b16 v[160:161], v35 offset:40960\n" \
"v_mfma_f32_16x16x32_bf16 a[220:223], v[176:179], v[140:143], a[220:223]\n" \
"ds_read_b128 v[76:79], v28 offset:36864\n" \
"v_mfma_f32_16x16x32_bf16 a[252:255], v[112:115], v[80:83], a[252:255]\n" \
"ds_read_b128 v[140:143], v29 offset:36864\n" \
"v_mfma_f32_16x16x32_bf16 a[252:255], v[176:179], v[144:147], a[252:255]\n" \
"ds_read_b64_tr_b16 v[162:163], v35 offset:41984\n" \
"v_mfma_f32_16x16x32_bf16 a[248:251], v[108:111], v[80:83], a[248:251]\n" \
"v_mfma_f32_16x16x32_bf16 a[248:251], v[172:175], v[144:147], a[248:251]\n" \
"v_mfma_f32_16x16x32_bf16 a[244:247], v[104:107], v[80:83], a[244:247]\n" \
"v_mfma_f32_16x16x32_bf16 a[244:247], v[168:171], v[144:147], a[244:247]\n" \
"v_mfma_f32_16x16x32_bf16 a[240:243], v[100:103], v[80:83], a[240:243]\n" \
"v_mfma_f32_16x16x32_bf16 a[240:243], v[164:167], v[144:147], a[240:243]\n" \
"ds_read_b128 v[80:83], v28 offset:38912\n" \
"ds_read_b128 v[144:147], v29 offset:38912\n" \
"s_waitcnt lgkmcnt(15)\n" \
"v_mfma_f32_16x16x32_bf16 a[0:3], v[84:87], v[52:55], a[0:3]\n" \
"s_add_u32 m0, s31, 0xc00\n" \
"s_mov_b32 s85, 0x318000\n" \
"v_mfma_f32_16x16x32_bf16 a[0:3], v[148:151], v[116:119], a[0:3]\n" \
"buffer_load_dwordx4 v27, s[68:71], s85 offen lds\n" \
"v_mfma_f32_16x16x32_bf16 a[4:7], v[88:91], v[52:55], a[4:7]\n" \
"ds_read_b64_tr_b16 v[100:101], v36 offset:32768\n" \
"s_waitcnt lgkmcnt(15)\n" \
"v_mfma_f32_16x16x32_bf16 a[4:7], v[152:155], v[116:119], a[4:7]\n" \
"s_add_u32 m0, s31, 0x4c00\n" \
"s_mov_b32 s84, 0x318000\n" \
"s_waitcnt lgkmcnt(11)\n" \
"v_mfma_f32_16x16x32_bf16 a[8:11], v[92:95], v[52:55], a[8:11]\n" \
"buffer_load_dwordx4 v27, s[72:75], s84 offen lds\n" \
"s_waitcnt lgkmcnt(9)\n" \
"v_mfma_f32_16x16x32_bf16 a[8:11], v[156:159], v[116:119], a[8:11]\n" \
"ds_read_b64_tr_b16 v[102:103], v36 offset:33792\n" \
"s_waitcnt lgkmcnt(8)\n" \
"v_mfma_f32_16x16x32_bf16 a[12:15], v[96:99], v[52:55], a[12:15]\n" \
"ds_read_b64_tr_b16 v[164:165], v36 offset:40960\n" \
"s_waitcnt lgkmcnt(5)\n" \
"v_mfma_f32_16x16x32_bf16 a[12:15], v[160:163], v[116:119], a[12:15]\n" \
"v_mfma_f32_16x16x32_bf16 a[44:47], v[96:99], v[56:59], a[44:47]\n" \
"v_mfma_f32_16x16x32_bf16 a[44:47], v[160:163], v[120:123], a[44:47]\n" \
"ds_read_b64_tr_b16 v[166:167], v36 offset:41984\n" \
"v_mfma_f32_16x16x32_bf16 a[40:43], v[92:95], v[56:59], a[40:43]\n" \
"v_mfma_f32_16x16x32_bf16 a[40:43], v[156:159], v[120:123], a[40:43]\n" \
"v_mfma_f32_16x16x32_bf16 a[36:39], v[88:91], v[56:59], a[36:39]\n" \
"v_mfma_f32_16x16x32_bf16 a[36:39], v[152:155], v[120:123], a[36:39]\n" \
"ds_read_b64_tr_b16 v[104:105], v37 offset:32768\n" \
"v_mfma_f32_16x16x32_bf16 a[32:35], v[84:87], v[56:59], a[32:35]\n" \
"v_mfma_f32_16x16x32_bf16 a[32:35], v[148:151], v[120:123], a[32:35]\n" \
"v_mfma_f32_16x16x32_bf16 a[64:67], v[84:87], v[60:63], a[64:67]\n" \
"ds_read_b64_tr_b16 v[106:107], v37 offset:33792\n" \
"v_mfma_f32_16x16x32_bf16 a[64:67], v[148:151], v[124:127], a[64:67]\n" \
"v_mfma_f32_16x16x32_bf16 a[68:71], v[88:91], v[60:63], a[68:71]\n" \
"v_lshlrev_b32_e32 v45, 3, v20\n" \
"v_add_u32_e32 v45, s31, v45\n" \
"v_mfma_f32_16x16x32_bf16 a[68:71], v[152:155], v[124:127], a[68:71]\n" \
"s_getpc_b64 s[86:87]\n" \
"s_sub_u32 s86, s86, 0xf800\n" \
"s_subb_u32 s87, s87, 0\n" \
"s_mov_b32 m0, s32\n" \
"s_nop 0\n" \
"v_mfma_f32_16x16x32_bf16 a[72:75], v[92:95], v[60:63], a[72:75]\n" \
"global_load_lds_dword v45, s[86:87]\n" \
"v_mfma_f32_16x16x32_bf16 a[72:75], v[156:159], v[124:127], a[72:75]\n" \
"ds_read_b64_tr_b16 v[168:169], v37 offset:40960\n" \
"v_mfma_f32_16x16x32_bf16 a[76:79], v[96:99], v[60:63], a[76:79]\n" \
"ds_read_b64_tr_b16 v[170:171], v37 offset:41984\n" \
"v_mfma_f32_16x16x32_bf16 a[76:79], v[160:163], v[124:127], a[76:79]\n" \
"v_mfma_f32_16x16x32_bf16 a[108:111], v[96:99], v[64:67], a[108:111]\n" \
"v_mfma_f32_16x16x32_bf16 a[108:111], v[160:163], v[128:131], a[108:111]\n" \
"v_mfma_f32_16x16x32_bf16 a[104:107], v[92:95], v[64:67], a[104:107]\n" \
"ds_read_b64_tr_b16 v[108:109], v38 offset:32768\n" \
"v_mfma_f32_16x16x32_bf16 a[104:107], v[156:159], v[128:131], a[104:107]\n" \
"v_mfma_f32_16x16x32_bf16 a[100:103], v[88:91], v[64:67], a[100:103]\n" \
"v_mfma_f32_16x16x32_bf16 a[100:103], v[152:155], v[128:131], a[100:103]\n" \
"ds_read_b64_tr_b16 v[110:111], v38 offset:33792\n" \
"v_mfma_f32_16x16x32_bf16 a[96:99], v[84:87], v[64:67], a[96:99]\n" \
"v_mfma_f32_16x16x32_bf16 a[96:99], v[148:151], v[128:131], a[96:99]\n" \
"v_mfma_f32_16x16x32_bf16 a[128:131], v[84:87], v[68:71], a[128:131]\n" \
"ds_read_b64_tr_b16 v[172:173], v38 offset:40960\n" \
"v_mfma_f32_16x16x32_bf16 a[128:131], v[148:151], v[132:135], a[128:131]\n" \
"v_mfma_f32_16x16x32_bf16 a[132:135], v[88:91], v[68:71], a[132:135]\n" \
"v_mfma_f32_16x16x32_bf16 a[132:135], v[152:155], v[132:135], a[132:135]\n" \
"ds_read_b64_tr_b16 v[174:175], v38 offset:41984\n" \
"v_mfma_f32_16x16x32_bf16 a[136:139], v[92:95], v[68:71], a[136:139]\n" \
"v_mfma_f32_16x16x32_bf16 a[136:139], v[156:159], v[132:135], a[136:139]\n" \
"v_mfma_f32_16x16x32_bf16 a[140:143], v[96:99], v[68:71], a[140:143]\n" \
"v_mfma_f32_16x16x32_bf16 a[140:143], v[160:163], v[132:135], a[140:143]\n" \
"ds_read_b64_tr_b16 v[112:113], v39 offset:32768\n" \
"v_mfma_f32_16x16x32_bf16 a[172:175], v[96:99], v[72:75], a[172:175]\n" \
"v_mfma_f32_16x16x32_bf16 a[172:175], v[160:163], v[136:139], a[172:175]\n" \
"v_mfma_f32_16x16x32_bf16 a[168:171], v[92:95], v[72:75], a[168:171]\n" \
"ds_read_b64_tr_b16 v[114:115], v39 offset:33792\n" \
"v_mfma_f32_16x16x32_bf16 a[168:171], v[156:159], v[136:139], a[168:171]\n" \
"v_mfma_f32_16x16x32_bf16 a[164:167], v[88:91], v[72:75], a[164:167]\n" \
"v_mfma_f32_16x16x32_bf16 a[164:167], v[152:155], v[136:139], a[164:167]\n" \
"ds_read_b64_tr_b16 v[176:177], v39 offset:40960\n" \
"v_mfma_f32_16x16x32_bf16 a[160:163], v[84:87], v[72:75], a[160:163]\n" \
"v_mfma_f32_16x16x32_bf16 a[160:163], v[148:151], v[136:139], a[160:163]\n" \
"v_mfma_f32_16x16x32_bf16 a[192:195], v[84:87], v[76:79], a[192:195]\n" \
"ds_read_b64_tr_b16 v[178:179], v39 offset:41984\n" \
"v_mfma_f32_16x16x32_bf16 a[192:195], v[148:151], v[140:143], a[192:195]\n" \
"v_mfma_f32_16x16x32_bf16 a[196:199], v[88:91], v[76:79], a[196:199]\n" \
"v_mfma_f32_16x16x32_bf16 a[196:199], v[152:155], v[140:143], a[196:199]\n" \
"v_mfma_f32_16x16x32_bf16 a[200:203], v[92:95], v[76:79], a[200:203]\n" \
"v_mfma_f32_16x16x32_bf16 a[200:203], v[156:159], v[140:143], a[200:203]\n" \
"v_mfma_f32_16x16x32_bf16 a[204:207], v[96:99], v[76:79], a[204:207]\n" \
"v_mfma_f32_16x16x32_bf16 a[204:207], v[160:163], v[140:143], a[204:207]\n" \
"s_waitcnt lgkmcnt(15)\n" \
"v_mfma_f32_16x16x32_bf16 a[236:239], v[96:99], v[80:83], a[236:239]\n" \
"v_mfma_f32_16x16x32_bf16 a[236:239], v[160:163], v[144:147], a[236:239]\n" \
"v_mfma_f32_16x16x32_bf16 a[232:235], v[92:95], v[80:83], a[232:235]\n" \
"v_mfma_f32_16x16x32_bf16 a[232:235], v[156:159], v[144:147], a[232:235]\n" \
"v_mfma_f32_16x16x32_bf16 a[228:231], v[88:91], v[80:83], a[228:231]\n" \
"v_mfma_f32_16x16x32_bf16 a[228:231], v[152:155], v[144:147], a[228:231]\n" \
"v_mfma_f32_16x16x32_bf16 a[224:227], v[84:87], v[80:83], a[224:227]\n" \
"v_mfma_f32_16x16x32_bf16 a[224:227], v[148:151], v[144:147], a[224:227]\n" \
"s_waitcnt vmcnt(1) lgkmcnt(0)\n" \
"s_barrier\n" \
"v_mov_b32_e32 v45, s62\n" \
"s_mov_b64 exec, 1\n" \
"buffer_store_dword v45, off, s[80:83], s37 offset:128 sc1\n" \
"s_mov_b64 exec, -1\n" \
"v_mfma_f32_16x16x32_bf16 a[16:19], v[100:103], v[52:55], a[16:19]\n" \
"s_add_u32 m0, s30, 0x8000\n" \
"s_mov_b32 s85, 0x380\n" \
"v_mfma_f32_16x16x32_bf16 a[16:19], v[164:167], v[116:119], a[16:19]\n" \
"buffer_load_dwordx4 v22, s[64:67], s85 offen lds\n" \
"v_mfma_f32_16x16x32_bf16 a[20:23], v[104:107], v[52:55], a[20:23]\n" \
"ds_read_b64_tr_b16 v[84:85], v32 offset:0\n" \
"v_mfma_f32_16x16x32_bf16 a[20:23], v[168:171], v[116:119], a[20:23]\n" \
"s_add_u32 m0, s30, 0x8400\n" \
"s_mov_b32 s84, 0x2380\n" \
"v_mfma_f32_16x16x32_bf16 a[24:27], v[108:111], v[52:55], a[24:27]\n" \
"buffer_load_dwordx4 v23, s[64:67], s84 offen lds\n" \
"v_mfma_f32_16x16x32_bf16 a[24:27], v[172:175], v[116:119], a[24:27]\n" \
"ds_read_b64_tr_b16 v[86:87], v32 offset:1024\n" \
"v_mfma_f32_16x16x32_bf16 a[28:31], v[112:115], v[52:55], a[28:31]\n" \
"ds_read_b64_tr_b16 v[148:149], v32 offset:8192\n" \
"v_mfma_f32_16x16x32_bf16 a[28:31], v[176:179], v[116:119], a[28:31]\n" \
"ds_read_b128 v[52:55], v28 offset:16384\n" \
"v_mfma_f32_16x16x32_bf16 a[60:63], v[112:115], v[56:59], a[60:63]\n" \
"ds_read_b128 v[116:119], v29 offset:16384\n" \
"v_mfma_f32_16x16x32_bf16 a[60:63], v[176:179], v[120:123], a[60:63]\n" \
"s_add_u32 m0, s31, 0x8000\n" \
"s_mov_b32 s85, 0x380000\n" \
"v_mfma_f32_16x16x32_bf16 a[56:59], v[108:111], v[56:59], a[56:59]\n" \
"buffer_load_dwordx4 v26, s[68:71], s85 offen lds\n" \
"v_mfma_f32_16x16x32_bf16 a[56:59], v[172:175], v[120:123], a[56:59]\n" \
"s_add_u32 m0, s31, 0xc000\n" \
"s_mov_b32 s84, 0x380000\n" \
"v_mfma_f32_16x16x32_bf16 a[52:55], v[104:107], v[56:59], a[52:55]\n" \
"buffer_load_dwordx4 v26, s[72:75], s84 offen lds\n" \
"v_mfma_f32_16x16x32_bf16 a[52:55], v[168:171], v[120:123], a[52:55]\n" \
"s_add_u32 m0, s30, 0x8800\n" \
"s_mov_b32 s85, 0x4380\n" \
"v_mfma_f32_16x16x32_bf16 a[48:51], v[100:103], v[56:59], a[48:51]\n" \
"buffer_load_dwordx4 v22, s[64:67], s85 offen lds\n" \
"v_mfma_f32_16x16x32_bf16 a[48:51], v[164:167], v[120:123], a[48:51]\n" \
"ds_read_b128 v[56:59], v28 offset:18432\n" \
"v_mfma_f32_16x16x32_bf16 a[80:83], v[100:103], v[60:63], a[80:83]\n" \
"ds_read_b128 v[120:123], v29 offset:18432\n" \
"v_mfma_f32_16x16x32_bf16 a[80:83], v[164:167], v[124:127], a[80:83]\n" \
"s_add_u32 m0, s30, 0x8c00\n" \
"s_mov_b32 s84, 0x6380\n" \
"v_mfma_f32_16x16x32_bf16 a[84:87], v[104:107], v[60:63], a[84:87]\n" \
"buffer_load_dwordx4 v23, s[64:67], s84 offen lds\n" \
"v_mfma_f32_16x16x32_bf16 a[84:87], v[168:171], v[124:127], a[84:87]\n" \
"ds_read_b64_tr_b16 v[150:151], v32 offset:9216\n" \
"v_mfma_f32_16x16x32_bf16 a[88:91], v[108:111], v[60:63], a[88:91]\n" \
"s_add_u32 m0, s31, 0x8400\n" \
"s_mov_b32 s85, 0x388000\n" \
"v_mfma_f32_16x16x32_bf16 a[88:91], v[172:175], v[124:127], a[88:91]\n" \
"buffer_load_dwordx4 v26, s[68:71], s85 offen lds\n" \
"v_mfma_f32_16x16x32_bf16 a[92:95], v[112:115], v[60:63], a[92:95]\n" \
"ds_read_b64_tr_b16 v[88:89], v33 offset:0\n" \
"v_mfma_f32_16x16x32_bf16 a[92:95], v[176:179], v[124:127], a[92:95]\n" \
"ds_read_b128 v[60:63], v28 offset:20480\n" \
"v_mfma_f32_16x16x32_bf16 a[124:127], v[112:115], v[64:67], a[124:127]\n" \
"ds_read_b128 v[124:127], v29 offset:20480\n" \
"v_mfma_f32_16x16x32_bf16 a[124:127], v[176:179], v[128:131], a[124:127]\n" \
"s_add_u32 m0, s31, 0xc400\n" \
"s_mov_b32 s84, 0x388000\n" \
"v_mfma_f32_16x16x32_bf16 a[120:123], v[108:111], v[64:67], a[120:123]\n" \
"buffer_load_dwordx4 v26, s[72:75], s84 offen lds\n" \
"v_mfma_f32_16x16x32_bf16 a[120:123], v[172:175], v[128:131], a[120:123]\n" \
"s_add_u32 m0, s30, 0x9000\n" \
"s_mov_b32 s85, 0x8380\n" \
"v_mfma_f32_16x16x32_bf16 a[116:119], v[104:107], v[64:67], a[116:119]\n" \
"buffer_load_dwordx4 v22, s[64:67], s85 offen lds\n" \
"v_mfma_f32_16x16x32_bf16 a[116:119], v[168:171], v[128:131], a[116:119]\n" \
"ds_read_b64_tr_b16 v[90:91], v33 offset:1024\n" \
"v_mfma_f32_16x16x32_bf16 a[112:115], v[100:103], v[64:67], a[112:115]\n" \
"ds_read_b64_tr_b16 v[152:153], v33 offset:8192\n" \
"v_mfma_f32_16x16x32_bf16 a[112:115], v[164:167], v[128:131], a[112:115]\n" \
"ds_read_b128 v[64:67], v28 offset:22528\n" \
"v_mfma_f32_16x16x32_bf16 a[144:147], v[100:103], v[68:71], a[144:147]\n" \
"ds_read_b128 v[128:131], v29 offset:22528\n" \
"v_mfma_f32_16x16x32_bf16 a[144:147], v[164:167], v[132:135], a[144:147]\n" \
"s_add_u32 m0, s30, 0x9400\n" \
"s_mov_b32 s84, 0xa380\n" \
"v_mfma_f32_16x16x32_bf16 a[148:151], v[104:107], v[68:71], a[148:151]\n" \
"buffer_load_dwordx4 v23, s[64:67], s84 offen lds\n" \
"v_mfma_f32_16x16x32_bf16 a[148:151], v[168:171], v[132:135], a[148:151]\n" \
"s_add_u32 m0, s31, 0x8800\n" \
"s_mov_b32 s85, 0x390000\n" \
"v_mfma_f32_16x16x32_bf16 a[152:155], v[108:111], v[68:71], a[152:155]\n" \
"buffer_load_dwordx4 v27, s[68:71], s85 offen lds\n" \
"v_mfma_f32_16x16x32_bf16 a[152:155], v[172:175], v[132:135], a[152:155]\n" \
"s_add_u32 m0, s31, 0xc800\n" \
"s_mov_b32 s84, 0x390000\n" \
"v_mfma_f32_16x16x32_bf16 a[156:159], v[112:115], v[68:71], a[156:159]\n" \
"buffer_load_dwordx4 v27, s[72:75], s84 offen lds\n" \
"v_mfma_f32_16x16x32_bf16 a[156:159], v[176:179], v[132:135], a[156:159]\n" \
"ds_read_b128 v[68:71], v28 offset:0\n" \
"v_mfma_f32_16x16x32_bf16 a[188:191], v[112:115], v[72:75], a[188:191]\n" \
"ds_read_b128 v[132:135], v29 offset:0\n" \
"v_mfma_f32_16x16x32_bf16 a[188:191], v[176:179], v[136:139], a[188:191]\n" \
"s_add_u32 m0, s30, 0x9800\n" \
"s_mov_b32 s85, 0xc380\n" \
"v_mfma_f32_16x16x32_bf16 a[184:187], v[108:111], v[72:75], a[184:187]\n" \
"buffer_load_dwordx4 v22, s[64:67], s85 offen lds\n" \
"v_mfma_f32_16x16x32_bf16 a[184:187], v[172:175], v[136:139], a[184:187]\n" \
"ds_read_b64_tr_b16 v[154:155], v33 offset:9216\n" \
"v_mfma_f32_16x16x32_bf16 a[180:183], v[104:107], v[72:75], a[180:183]\n" \
"s_add_u32 m0, s30, 0x9c00\n" \
"s_mov_b32 s84, 0xe380\n" \
"v_mfma_f32_16x16x32_bf16 a[180:183], v[168:171], v[136:139], a[180:183]\n" \
"buffer_load_dwordx4 v23, s[64:67], s84 offen lds\n" \
"v_mfma_f32_16x16x32_bf16 a[176:179], v[100:103], v[72:75], a[176:179]\n" \
"ds_read_b64_tr_b16 v[92:93], v34 offset:0\n" \
"v_mfma_f32_16x16x32_bf16 a[176:179], v[164:167], v[136:139], a[176:179]\n" \
"ds_read_b128 v[72:75], v28 offset:2048\n" \
"v_mfma_f32_16x16x32_bf16 a[208:211], v[100:103], v[76:79], a[208:211]\n" \
"ds_read_b128 v[136:139], v29 offset:2048\n" \
"v_mfma_f32_16x16x32_bf16 a[208:211], v[164:167], v[140:143], a[208:211]\n" \
"ds_read_b64_tr_b16 v[94:95], v34 offset:1024\n" \
"v_mfma_f32_16x16x32_bf16 a[212:215], v[104:107], v[76:79], a[212:215]\n" \
"ds_read_b64_tr_b16 v[156:157], v34 offset:8192\n" \
"v_mfma_f32_16x16x32_bf16 a[212:215], v[168:171], v[140:143], a[212:215]\n" \
"ds_read_b64_tr_b16 v[158:159], v34 offset:9216\n" \
"v_mfma_f32_16x16x32_bf16 a[216:219], v[108:111], v[76:79], a[216:219]\n" \
"ds_read_b64_tr_b16 v[96:97], v35 offset:0\n" \
"v_mfma_f32_16x16x32_bf16 a[216:219], v[172:175], v[140:143], a[216:219]\n" \
"ds_read_b64_tr_b16 v[98:99], v35 offset:1024\n" \
"v_mfma_f32_16x16x32_bf16 a[220:223], v[112:115], v[76:79], a[220:223]\n" \
"ds_read_b64_tr_b16 v[160:161], v35 offset:8192\n" \
"v_mfma_f32_16x16x32_bf16 a[220:223], v[176:179], v[140:143], a[220:223]\n" \
"ds_read_b128 v[76:79], v28 offset:4096\n" \
"v_mfma_f32_16x16x32_bf16 a[252:255], v[112:115], v[80:83], a[252:255]\n" \
"ds_read_b128 v[140:143], v29 offset:4096\n" \
"v_mfma_f32_16x16x32_bf16 a[252:255], v[176:179], v[144:147], a[252:255]\n" \
"ds_read_b64_tr_b16 v[162:163], v35 offset:9216\n" \
"v_mfma_f32_16x16x32_bf16 a[248:251], v[108:111], v[80:83], a[248:251]\n" \
"v_mfma_f32_16x16x32_bf16 a[248:251], v[172:175], v[144:147], a[248:251]\n" \
"v_mfma_f32_16x16x32_bf16 a[244:247], v[104:107], v[80:83], a[244:247]\n" \
"v_mfma_f32_16x16x32_bf16 a[244:247], v[168:171], v[144:147], a[244:247]\n" \
"v_mfma_f32_16x16x32_bf16 a[240:243], v[100:103], v[80:83], a[240:243]\n" \
"v_mfma_f32_16x16x32_bf16 a[240:243], v[164:167], v[144:147], a[240:243]\n" \
"ds_read_b128 v[80:83], v28 offset:6144\n" \
"ds_read_b128 v[144:147], v29 offset:6144\n" \
"s_waitcnt lgkmcnt(15)\n" \
"v_mfma_f32_16x16x32_bf16 a[0:3], v[84:87], v[52:55], a[0:3]\n" \
"s_add_u32 m0, s31, 0x8c00\n" \
"s_mov_b32 s85, 0x398000\n" \
"v_mfma_f32_16x16x32_bf16 a[0:3], v[148:151], v[116:119], a[0:3]\n" \
"buffer_load_dwordx4 v27, s[68:71], s85 offen lds\n" \
"v_mfma_f32_16x16x32_bf16 a[4:7], v[88:91], v[52:55], a[4:7]\n" \
"ds_read_b64_tr_b16 v[100:101], v36 offset:0\n" \
"s_waitcnt lgkmcnt(15)\n" \
"v_mfma_f32_16x16x32_bf16 a[4:7], v[152:155], v[116:119], a[4:7]\n" \
"s_add_u32 m0, s31, 0xcc00\n" \
"s_mov_b32 s84, 0x398000\n" \
"s_waitcnt lgkmcnt(11)\n" \
"v_mfma_f32_16x16x32_bf16 a[8:11], v[92:95], v[52:55], a[8:11]\n" \
".fill (L_pb + 16096 - .) / 4, 4, 0xBF800000\n" \
"L_pp4:\n" \
"s_cbranch_execz L_pret\n" \
"buffer_load_dwordx4 v27, s[72:75], s84 offen lds\n" \
"s_waitcnt lgkmcnt(9)\n" \
"v_mfma_f32_16x16x32_bf16 a[8:11], v[156:159], v[116:119], a[8:11]\n" \
"ds_read_b64_tr_b16 v[102:103], v36 offset:1024\n" \
"s_waitcnt lgkmcnt(8)\n" \
"v_mfma_f32_16x16x32_bf16 a[12:15], v[96:99], v[52:55], a[12:15]\n" \
"ds_read_b64_tr_b16 v[164:165], v36 offset:8192\n" \
"s_waitcnt lgkmcnt(5)\n" \
"v_mfma_f32_16x16x32_bf16 a[12:15], v[160:163], v[116:119], a[12:15]\n" \
"v_mfma_f32_16x16x32_bf16 a[44:47], v[96:99], v[56:59], a[44:47]\n" \
"v_mfma_f32_16x16x32_bf16 a[44:47], v[160:163], v[120:123], a[44:47]\n" \
"ds_read_b64_tr_b16 v[166:167], v36 offset:9216\n" \
"v_mfma_f32_16x16x32_bf16 a[40:43], v[92:95], v[56:59], a[40:43]\n" \
"v_mfma_f32_16x16x32_bf16 a[40:43], v[156:159], v[120:123], a[40:43]\n" \
"v_mfma_f32_16x16x32_bf16 a[36:39], v[88:91], v[56:59], a[36:39]\n" \
"v_mfma_f32_16x16x32_bf16 a[36:39], v[152:155], v[120:123], a[36:39]\n" \
"ds_read_b64_tr_b16 v[104:105], v37 offset:0\n" \
"v_mfma_f32_16x16x32_bf16 a[32:35], v[84:87], v[56:59], a[32:35]\n" \
"v_mfma_f32_16x16x32_bf16 a[32:35], v[148:151], v[120:123], a[32:35]\n" \
"v_mfma_f32_16x16x32_bf16 a[64:67], v[84:87], v[60:63], a[64:67]\n" \
"ds_read_b64_tr_b16 v[106:107], v37 offset:1024\n" \
"v_mfma_f32_16x16x32_bf16 a[64:67], v[148:151], v[124:127], a[64:67]\n" \
"v_mfma_f32_16x16x32_bf16 a[68:71], v[88:91], v[60:63], a[68:71]\n" \
"v_mfma_f32_16x16x32_bf16 a[68:71], v[152:155], v[124:127], a[68:71]\n" \
"ds_read_b64_tr_b16 v[168:169], v37 offset:8192\n" \
"v_mfma_f32_16x16x32_bf16 a[72:75], v[92:95], v[60:63], a[72:75]\n" \
"v_mfma_f32_16x16x32_bf16 a[72:75], v[156:159], v[124:127], a[72:75]\n" \
"v_mfma_f32_16x16x32_bf16 a[76:79], v[96:99], v[60:63], a[76:79]\n" \
"ds_read_b64_tr_b16 v[170:171], v37 offset:9216\n" \
"v_mfma_f32_16x16x32_bf16 a[76:79], v[160:163], v[124:127], a[76:79]\n" \
"v_mfma_f32_16x16x32_bf16 a[108:111], v[96:99], v[64:67], a[108:111]\n" \
"v_mfma_f32_16x16x32_bf16 a[108:111], v[160:163], v[128:131], a[108:111]\n" \
"v_mfma_f32_16x16x32_bf16 a[104:107], v[92:95], v[64:67], a[104:107]\n" \
"ds_read_b64_tr_b16 v[108:109], v38 offset:0\n" \
"v_mfma_f32_16x16x32_bf16 a[104:107], v[156:159], v[128:131], a[104:107]\n" \
"v_mfma_f32_16x16x32_bf16 a[100:103], v[88:91], v[64:67], a[100:103]\n" \
"v_mfma_f32_16x16x32_bf16 a[100:103], v[152:155], v[128:131], a[100:103]\n" \
"ds_read_b64_tr_b16 v[110:111], v38 offset:1024\n" \
"v_mfma_f32_16x16x32_bf16 a[96:99], v[84:87], v[64:67], a[96:99]\n" \
"v_mfma_f32_16x16x32_bf16 a[96:99], v[148:151], v[128:131], a[96:99]\n" \
"v_mfma_f32_16x16x32_bf16 a[128:131], v[84:87], v[68:71], a[128:131]\n" \
"v_bfe_u32 v44, v20, 2, 5\n" \
"ds_read_b64_tr_b16 v[172:173], v38 offset:8192\n" \
"v_mfma_f32_16x16x32_bf16 a[128:131], v[148:151], v[132:135], a[128:131]\n" \
"buffer_load_dword v44, v44, s[80:83], s38 offen offset:128 sc1\n" \
"v_mfma_f32_16x16x32_bf16 a[132:135], v[88:91], v[68:71], a[132:135]\n" \
"v_mfma_f32_16x16x32_bf16 a[132:135], v[152:155], v[132:135], a[132:135]\n" \
"ds_read_b64_tr_b16 v[174:175], v38 offset:9216\n" \
"v_mfma_f32_16x16x32_bf16 a[136:139], v[92:95], v[68:71], a[136:139]\n" \
"v_mfma_f32_16x16x32_bf16 a[136:139], v[156:159], v[132:135], a[136:139]\n" \
"v_mfma_f32_16x16x32_bf16 a[140:143], v[96:99], v[68:71], a[140:143]\n" \
"v_mfma_f32_16x16x32_bf16 a[140:143], v[160:163], v[132:135], a[140:143]\n" \
"ds_read_b64_tr_b16 v[112:113], v39 offset:0\n" \
"v_mfma_f32_16x16x32_bf16 a[172:175], v[96:99], v[72:75], a[172:175]\n" \
"v_mfma_f32_16x16x32_bf16 a[172:175], v[160:163], v[136:139], a[172:175]\n" \
"v_mfma_f32_16x16x32_bf16 a[168:171], v[92:95], v[72:75], a[168:171]\n" \
"ds_read_b64_tr_b16 v[114:115], v39 offset:1024\n" \
"v_mfma_f32_16x16x32_bf16 a[168:171], v[156:159], v[136:139], a[168:171]\n" \
"v_mfma_f32_16x16x32_bf16 a[164:167], v[88:91], v[72:75], a[164:167]\n" \
"v_mfma_f32_16x16x32_bf16 a[164:167], v[152:155], v[136:139], a[164:167]\n" \
"ds_read_b64_tr_b16 v[176:177], v39 offset:8192\n" \
"v_mfma_f32_16x16x32_bf16 a[160:163], v[84:87], v[72:75], a[160:163]\n" \
"v_mfma_f32_16x16x32_bf16 a[160:163], v[148:151], v[136:139], a[160:163]\n" \
"v_mfma_f32_16x16x32_bf16 a[192:195], v[84:87], v[76:79], a[192:195]\n" \
"ds_read_b64_tr_b16 v[178:179], v39 offset:9216\n" \
"v_mfma_f32_16x16x32_bf16 a[192:195], v[148:151], v[140:143], a[192:195]\n" \
"v_mfma_f32_16x16x32_bf16 a[196:199], v[88:91], v[76:79], a[196:199]\n" \
"v_mfma_f32_16x16x32_bf16 a[196:199], v[152:155], v[140:143], a[196:199]\n" \
"v_mfma_f32_16x16x32_bf16 a[200:203], v[92:95], v[76:79], a[200:203]\n" \
"v_mfma_f32_16x16x32_bf16 a[200:203], v[156:159], v[140:143], a[200:203]\n" \
"v_mfma_f32_16x16x32_bf16 a[204:207], v[96:99], v[76:79], a[204:207]\n" \
"v_mfma_f32_16x16x32_bf16 a[204:207], v[160:163], v[140:143], a[204:207]\n" \
"s_waitcnt lgkmcnt(15)\n" \
"v_mfma_f32_16x16x32_bf16 a[236:239], v[96:99], v[80:83], a[236:239]\n" \
"v_mfma_f32_16x16x32_bf16 a[236:239], v[160:163], v[144:147], a[236:239]\n" \
"v_mfma_f32_16x16x32_bf16 a[232:235], v[92:95], v[80:83], a[232:235]\n" \
"v_mfma_f32_16x16x32_bf16 a[232:235], v[156:159], v[144:147], a[232:235]\n" \
"v_mfma_f32_16x16x32_bf16 a[228:231], v[88:91], v[80:83], a[228:231]\n" \
"v_mfma_f32_16x16x32_bf16 a[228:231], v[152:155], v[144:147], a[228:231]\n" \
"v_mfma_f32_16x16x32_bf16 a[224:227], v[84:87], v[80:83], a[224:227]\n" \
"v_mfma_f32_16x16x32_bf16 a[224:227], v[148:151], v[144:147], a[224:227]\n" \
"s_waitcnt vmcnt(0) lgkmcnt(0)\n" \
"s_barrier\n" \
"v_cmp_ne_u32_e32 vcc, s62, v44\n" \
"s_nop 4\n" \
"s_cbranch_vccnz L_poll60_%=\n" \
"L_pollr60_%=:\n" \
"s_mov_b32 s68, s12\n" \
"s_and_b32 s69, s13, 0xffff\n" \
"s_mov_b32 s70, 0x480000\n" \
"s_mov_b32 s71, 0x20000\n" \
"v_mfma_f32_16x16x32_bf16 a[16:19], v[100:103], v[52:55], a[16:19]\n" \
"s_add_u32 m0, s40, 0x0\n" \
"s_add_u32 s85, s39, 0x0\n" \
"s_nop 0\n" \
"v_mfma_f32_16x16x32_bf16 a[16:19], v[164:167], v[116:119], a[16:19]\n" \
"buffer_load_dwordx4 v20, s[80:83], s85 offen sc1 lds\n" \
"v_mfma_f32_16x16x32_bf16 a[20:23], v[104:107], v[52:55], a[20:23]\n" \
"ds_read_b64_tr_b16 v[84:85], v32 offset:32768\n" \
"v_mfma_f32_16x16x32_bf16 a[20:23], v[168:171], v[116:119], a[20:23]\n" \
"s_add_u32 m0, s40, 0x1000\n" \
"s_add_u32 s84, s39, 0x1000\n" \
"v_mfma_f32_16x16x32_bf16 a[24:27], v[108:111], v[52:55], a[24:27]\n" \
"buffer_load_dwordx4 v20, s[80:83], s84 offen sc1 lds\n" \
"v_mfma_f32_16x16x32_bf16 a[24:27], v[172:175], v[116:119], a[24:27]\n" \
"ds_read_b64_tr_b16 v[86:87], v32 offset:33792\n" \
"v_mfma_f32_16x16x32_bf16 a[28:31], v[112:115], v[52:55], a[28:31]\n" \
"ds_read_b64_tr_b16 v[148:149], v32 offset:40960\n" \
"v_mfma_f32_16x16x32_bf16 a[28:31], v[176:179], v[116:119], a[28:31]\n" \
"ds_read_b128 v[52:55], v28 offset:49152\n" \
"v_mfma_f32_16x16x32_bf16 a[60:63], v[112:115], v[56:59], a[60:63]\n" \
"ds_read_b128 v[116:119], v29 offset:49152\n" \
"v_mfma_f32_16x16x32_bf16 a[60:63], v[176:179], v[120:123], a[60:63]\n" \
"s_add_u32 m0, s40, 0x3000\n" \
"s_mov_b32 s85, 0x0\n" \
"v_mfma_f32_16x16x32_bf16 a[56:59], v[108:111], v[56:59], a[56:59]\n" \
"buffer_load_dwordx4 v21, s[76:79], s85 offen lds\n" \
"v_mfma_f32_16x16x32_bf16 a[56:59], v[172:175], v[120:123], a[56:59]\n" \
"s_add_u32 m0, s30, 0x0\n" \
"s_mov_b32 s84, 0x0\n" \
"s_nop 0\n" \
"v_mfma_f32_16x16x32_bf16 a[52:55], v[104:107], v[56:59], a[52:55]\n" \
"buffer_load_dwordx4 v24, s[76:79], s84 offen lds\n" \
"v_mfma_f32_16x16x32_bf16 a[52:55], v[168:171], v[120:123], a[52:55]\n" \
"ds_read_b64_tr_b16 v[150:151], v32 offset:41984\n" \
"v_mfma_f32_16x16x32_bf16 a[48:51], v[100:103], v[56:59], a[48:51]\n" \
"ds_read_b64_tr_b16 v[88:89], v33 offset:32768\n" \
"v_mfma_f32_16x16x32_bf16 a[48:51], v[164:167], v[120:123], a[48:51]\n" \
"ds_read_b128 v[56:59], v28 offset:51200\n" \
"v_mfma_f32_16x16x32_bf16 a[80:83], v[100:103], v[60:63], a[80:83]\n" \
"ds_read_b128 v[120:123], v29 offset:51200\n" \
"v_mfma_f32_16x16x32_bf16 a[80:83], v[164:167], v[124:127], a[80:83]\n" \
"s_add_u32 m0, s30, 0x400\n" \
"s_mov_b32 s85, 0x2400\n" \
"v_mfma_f32_16x16x32_bf16 a[84:87], v[104:107], v[60:63], a[84:87]\n" \
"buffer_load_dwordx4 v25, s[76:79], s85 offen lds\n" \
"v_mfma_f32_16x16x32_bf16 a[84:87], v[168:171], v[124:127], a[84:87]\n" \
"s_add_u32 m0, s31, 0x0\n" \
"s_mov_b32 s84, 0x0\n" \
"v_mfma_f32_16x16x32_bf16 a[88:91], v[108:111], v[60:63], a[88:91]\n" \
"buffer_load_dwordx4 v26, s[68:71], s84 offen lds\n" \
"v_mfma_f32_16x16x32_bf16 a[88:91], v[172:175], v[124:127], a[88:91]\n" \
"ds_read_b64_tr_b16 v[90:91], v33 offset:33792\n" \
"v_mfma_f32_16x16x32_bf16 a[92:95], v[112:115], v[60:63], a[92:95]\n" \
"ds_read_b64_tr_b16 v[152:153], v33 offset:40960\n" \
"v_mfma_f32_16x16x32_bf16 a[92:95], v[176:179], v[124:127], a[92:95]\n" \
"ds_read_b128 v[60:63], v28 offset:53248\n" \
"v_mfma_f32_16x16x32_bf16 a[124:127], v[112:115], v[64:67], a[124:127]\n" \
"ds_read_b128 v[124:127], v29 offset:53248\n" \
"v_mfma_f32_16x16x32_bf16 a[124:127], v[176:179], v[128:131], a[124:127]\n" \
"s_add_u32 m0, s30, 0x800\n" \
"s_mov_b32 s85, 0x4800\n" \
"v_mfma_f32_16x16x32_bf16 a[120:123], v[108:111], v[64:67], a[120:123]\n" \
"buffer_load_dwordx4 v24, s[76:79], s85 offen lds\n" \
"v_mfma_f32_16x16x32_bf16 a[120:123], v[172:175], v[128:131], a[120:123]\n" \
"s_add_u32 m0, s30, 0xc00\n" \
"s_mov_b32 s84, 0x6c00\n" \
"v_mfma_f32_16x16x32_bf16 a[116:119], v[104:107], v[64:67], a[116:119]\n" \
"buffer_load_dwordx4 v25, s[76:79], s84 offen lds\n" \
"v_mfma_f32_16x16x32_bf16 a[116:119], v[168:171], v[128:131], a[116:119]\n" \
"s_add_u32 m0, s31, 0x400\n" \
"s_mov_b32 s85, 0x8000\n" \
"v_mfma_f32_16x16x32_bf16 a[112:115], v[100:103], v[64:67], a[112:115]\n" \
"buffer_load_dwordx4 v26, s[68:71], s85 offen lds\n" \
"v_mfma_f32_16x16x32_bf16 a[112:115], v[164:167], v[128:131], a[112:115]\n" \
"ds_read_b128 v[64:67], v28 offset:55296\n" \
"v_mfma_f32_16x16x32_bf16 a[144:147], v[100:103], v[68:71], a[144:147]\n" \
"ds_read_b128 v[128:131], v29 offset:55296\n" \
"v_mfma_f32_16x16x32_bf16 a[144:147], v[164:167], v[132:135], a[144:147]\n" \
"s_add_u32 m0, s30, 0x1000\n" \
"s_mov_b32 s84, 0x9000\n" \
"v_mfma_f32_16x16x32_bf16 a[148:151], v[104:107], v[68:71], a[148:151]\n" \
"buffer_load_dwordx4 v24, s[76:79], s84 offen lds\n" \
"v_mfma_f32_16x16x32_bf16 a[148:151], v[168:171], v[132:135], a[148:151]\n" \
"ds_read_b64_tr_b16 v[154:155], v33 offset:41984\n" \
"v_mfma_f32_16x16x32_bf16 a[152:155], v[108:111], v[68:71], a[152:155]\n" \
"s_add_u32 m0, s30, 0x1400\n" \
"s_mov_b32 s85, 0xb400\n" \
"v_mfma_f32_16x16x32_bf16 a[152:155], v[172:175], v[132:135], a[152:155]\n" \
"buffer_load_dwordx4 v25, s[76:79], s85 offen lds\n" \
"v_mfma_f32_16x16x32_bf16 a[156:159], v[112:115], v[68:71], a[156:159]\n" \
"ds_read_b64_tr_b16 v[92:93], v34 offset:32768\n" \
"v_mfma_f32_16x16x32_bf16 a[156:159], v[176:179], v[132:135], a[156:159]\n" \
"ds_read_b128 v[68:71], v28 offset:32768\n" \
"v_mfma_f32_16x16x32_bf16 a[188:191], v[112:115], v[72:75], a[188:191]\n" \
"ds_read_b128 v[132:135], v29 offset:32768\n" \
"v_mfma_f32_16x16x32_bf16 a[188:191], v[176:179], v[136:139], a[188:191]\n" \
"s_add_u32 m0, s31, 0x800\n" \
"s_mov_b32 s84, 0x10000\n" \
"v_mfma_f32_16x16x32_bf16 a[184:187], v[108:111], v[72:75], a[184:187]\n" \
"buffer_load_dwordx4 v27, s[68:71], s84 offen lds\n" \
"v_mfma_f32_16x16x32_bf16 a[184:187], v[172:175], v[136:139], a[184:187]\n" \
"ds_read_b64_tr_b16 v[94:95], v34 offset:33792\n" \
"v_mfma_f32_16x16x32_bf16 a[180:183], v[104:107], v[72:75], a[180:183]\n" \
"s_add_u32 m0, s30, 0x1800\n" \
"s_mov_b32 s85, 0xd800\n" \
"v_mfma_f32_16x16x32_bf16 a[180:183], v[168:171], v[136:139], a[180:183]\n" \
"buffer_load_dwordx4 v24, s[76:79], s85 offen lds\n" \
"v_mfma_f32_16x16x32_bf16 a[176:179], v[100:103], v[72:75], a[176:179]\n" \
"ds_read_b64_tr_b16 v[156:157], v34 offset:40960\n" \
"v_mfma_f32_16x16x32_bf16 a[176:179], v[164:167], v[136:139], a[176:179]\n" \
"ds_read_b128 v[72:75], v28 offset:34816\n" \
"v_mfma_f32_16x16x32_bf16 a[208:211], v[100:103], v[76:79], a[208:211]\n" \
"ds_read_b128 v[136:139], v29 offset:34816\n" \
"v_mfma_f32_16x16x32_bf16 a[208:211], v[164:167], v[140:143], a[208:211]\n" \
"ds_read_b64_tr_b16 v[158:159], v34 offset:41984\n" \
"v_mfma_f32_16x16x32_bf16 a[212:215], v[104:107], v[76:79], a[212:215]\n" \
"ds_read_b64_tr_b16 v[96:97], v35 offset:32768\n" \
"v_mfma_f32_16x16x32_bf16 a[212:215], v[168:171], v[140:143], a[212:215]\n" \
"ds_read_b64_tr_b16 v[98:99], v35 offset:33792\n" \
"v_mfma_f32_16x16x32_bf16 a[216:219], v[108:111], v[76:79], a[216:219]\n" \
"ds_read_b64_tr_b16 v[160:161], v35 offset:40960\n" \
"v_mfma_f32_16x16x32_bf16 a[216:219], v[172:175], v[140:143], a[216:219]\n" \
"ds_read_b64_tr_b16 v[162:163], v35 offset:41984\n" \
"v_mfma_f32_16x16x32_bf16 a[220:223], v[112:115], v[76:79], a[220:223]\n" \
"v_mfma_f32_16x16x32_bf16 a[220:223], v[176:179], v[140:143], a[220:223]\n" \
"ds_read_b128 v[76:79], v28 offset:36864\n" \
"v_mfma_f32_16x16x32_bf16 a[252:255], v[112:115], v[80:83], a[252:255]\n" \
"ds_read_b128 v[140:143], v29 offset:36864\n" \
"v_mfma_f32_16x16x32_bf16 a[252:255], v[176:179], v[144:147], a[252:255]\n" \
"v_mfma_f32_16x16x32_bf16 a[248:251], v[108:111], v[80:83], a[248:251]\n" \
"v_mfma_f32_16x16x32_bf16 a[248:251], v[172:175], v[144:147], a[248:251]\n" \
"v_mfma_f32_16x16x32_bf16 a[244:247], v[104:107], v[80:83], a[244:247]\n" \
"v_mfma_f32_16x16x32_bf16 a[244:247], v[168:171], v[144:147], a[244:247]\n" \
"v_mfma_f32_16x16x32_bf16 a[240:243], v[100:103], v[80:83], a[240:243]\n" \
"v_mfma_f32_16x16x32_bf16 a[240:243], v[164:167], v[144:147], a[240:243]\n" \
"ds_read_b128 v[80:83], v28 offset:38912\n" \
"ds_read_b128 v[144:147], v29 offset:38912\n" \
"s_waitcnt lgkmcnt(15)\n" \
"v_mfma_f32_16x16x32_bf16 a[0:3], v[84:87], v[52:55], a[0:3]\n" \
"s_add_u32 m0, s30, 0x1c00\n" \
"s_mov_b32 s85, 0xfc00\n" \
"v_mfma_f32_16x16x32_bf16 a[0:3], v[148:151], v[116:119], a[0:3]\n" \
"buffer_load_dwordx4 v25, s[76:79], s85 offen lds\n" \
"v_mfma_f32_16x16x32_bf16 a[4:7], v[88:91], v[52:55], a[4:7]\n" \
"ds_read_b64_tr_b16 v[100:101], v36 offset:32768\n" \
"v_mfma_f32_16x16x32_bf16 a[4:7], v[152:155], v[116:119], a[4:7]\n" \
"s_add_u32 m0, s31, 0xc00\n" \
"s_mov_b32 s84, 0x18000\n" \
"s_waitcnt lgkmcnt(13)\n" \
"v_mfma_f32_16x16x32_bf16 a[8:11], v[92:95], v[52:55], a[8:11]\n" \
"buffer_load_dwordx4 v27, s[68:71], s84 offen lds\n" \
"s_waitcnt lgkmcnt(9)\n" \
"v_mfma_f32_16x16x32_bf16 a[8:11], v[156:159], v[116:119], a[8:11]\n" \
"ds_read_b64_tr_b16 v[102:103], v36 offset:33792\n" \
"s_waitcnt lgkmcnt(8)\n" \
"v_mfma_f32_16x16x32_bf16 a[12:15], v[96:99], v[52:55], a[12:15]\n" \
"ds_read_b64_tr_b16 v[164:165], v36 offset:40960\n" \
"v_accvgpr_read_b32 v184, a0\n" \
"s_waitcnt lgkmcnt(7)\n" \
"s_nop 0\n" \
"v_mfma_f32_16x16x32_bf16 a[12:15], v[160:163], v[116:119], a[12:15]\n" \
"v_accvgpr_read_b32 v185, a1\n" \
"v_accvgpr_read_b32 v186, a2\n" \
"v_mfma_f32_16x16x32_bf16 a[44:47], v[96:99], v[56:59], a[44:47]\n" \
"v_accvgpr_read_b32 v187, a3\n" \
"v_accvgpr_read_b32 v180, a4\n" \
"v_mfma_f32_16x16x32_bf16 a[44:47], v[160:163], v[120:123], a[44:47]\n" \
"ds_read_b64_tr_b16 v[166:167], v36 offset:41984\n" \
"v_accvgpr_read_b32 v181, a5\n" \
"v_mfma_f32_16x16x32_bf16 a[40:43], v[92:95], v[56:59], a[40:43]\n" \
"v_accvgpr_read_b32 v182, a6\n" \
"v_accvgpr_read_b32 v183, a7\n" \
"v_mfma_f32_16x16x32_bf16 a[40:43], v[156:159], v[120:123], a[40:43]\n" \
"v_cvt_pk_bf16_f32 v184, v184, v185\n" \
"v_cvt_pk_bf16_f32 v185, v186, v187\n" \
"v_mfma_f32_16x16x32_bf16 a[36:39], v[88:91], v[56:59], a[36:39]\n" \
"v_cvt_pk_bf16_f32 v186, v180, v181\n" \
"v_cvt_pk_bf16_f32 v187, v182, v183\n" \
"v_mfma_f32_16x16x32_bf16 a[36:39], v[152:155], v[120:123], a[36:39]\n" \
"ds_read_b64_tr_b16 v[104:105], v37 offset:32768\n" \
"v_accvgpr_read_b32 v188, a8\n" \
"v_mfma_f32_16x16x32_bf16 a[32:35], v[84:87], v[56:59], a[32:35]\n" \
"v_accvgpr_read_b32 v189, a9\n" \
"v_accvgpr_read_b32 v190, a10\n" \
"v_mfma_f32_16x16x32_bf16 a[32:35], v[148:151], v[120:123], a[32:35]\n" \
"v_accvgpr_read_b32 v191, a11\n" \
"v_accvgpr_read_b32 v180, a12\n" \
"v_mfma_f32_16x16x32_bf16 a[64:67], v[84:87], v[60:63], a[64:67]\n" \
"ds_read_b64_tr_b16 v[106:107], v37 offset:33792\n" \
"v_accvgpr_read_b32 v181, a13\n" \
"v_mfma_f32_16x16x32_bf16 a[64:67], v[148:151], v[124:127], a[64:67]\n" \
"v_accvgpr_read_b32 v182, a14\n" \
"v_accvgpr_read_b32 v183, a15\n" \
"v_mfma_f32_16x16x32_bf16 a[68:71], v[88:91], v[60:63], a[68:71]\n" \
"v_lshlrev_b32_e32 v45, 3, v20\n" \
"v_add_u32_e32 v45, s31, v45\n" \
"v_mfma_f32_16x16x32_bf16 a[68:71], v[152:155], v[124:127], a[68:71]\n" \
"s_getpc_b64 s[86:87]\n" \
"s_sub_u32 s86, s86, 0xf800\n" \
"s_subb_u32 s87, s87, 0\n" \
"s_mov_b32 m0, s32\n" \
"s_nop 0\n" \
"v_mfma_f32_16x16x32_bf16 a[72:75], v[92:95], v[60:63], a[72:75]\n" \
"global_load_lds_dword v45, s[86:87]\n" \
"v_mfma_f32_16x16x32_bf16 a[72:75], v[156:159], v[124:127], a[72:75]\n" \
"ds_read_b64_tr_b16 v[168:169], v37 offset:40960\n" \
"v_cvt_pk_bf16_f32 v188, v188, v189\n" \
"v_mfma_f32_16x16x32_bf16 a[76:79], v[96:99], v[60:63], a[76:79]\n" \
"ds_read_b64_tr_b16 v[170:171], v37 offset:41984\n" \
"v_cvt_pk_bf16_f32 v189, v190, v191\n" \
"v_mfma_f32_16x16x32_bf16 a[76:79], v[160:163], v[124:127], a[76:79]\n" \
"v_cvt_pk_bf16_f32 v190, v180, v181\n" \
"v_cvt_pk_bf16_f32 v191, v182, v183\n" \
"v_mfma_f32_16x16x32_bf16 a[108:111], v[96:99], v[64:67], a[108:111]\n" \
"v_accvgpr_read_b32 v192, a40\n" \
"v_accvgpr_read_b32 v193, a41\n" \
"v_mfma_f32_16x16x32_bf16 a[108:111], v[160:163], v[128:131], a[108:111]\n" \
"v_accvgpr_read_b32 v194, a42\n" \
"v_accvgpr_read_b32 v195, a43\n" \
"v_mfma_f32_16x16x32_bf16 a[104:107], v[92:95], v[64:67], a[104:107]\n" \
"ds_read_b64_tr_b16 v[108:109], v38 offset:32768\n" \
"v_accvgpr_read_b32 v180, a44\n" \
"v_mfma_f32_16x16x32_bf16 a[104:107], v[156:159], v[128:131], a[104:107]\n" \
"v_accvgpr_read_b32 v181, a45\n" \
"v_accvgpr_read_b32 v182, a46\n" \
"v_mfma_f32_16x16x32_bf16 a[100:103], v[88:91], v[64:67], a[100:103]\n" \
"v_accvgpr_read_b32 v183, a47\n" \
"v_cvt_pk_bf16_f32 v192, v192, v193\n" \
"v_mfma_f32_16x16x32_bf16 a[100:103], v[152:155], v[128:131], a[100:103]\n" \
"ds_read_b64_tr_b16 v[110:111], v38 offset:33792\n" \
"v_cvt_pk_bf16_f32 v193, v194, v195\n" \
"v_mfma_f32_16x16x32_bf16 a[96:99], v[84:87], v[64:67], a[96:99]\n" \
"v_cvt_pk_bf16_f32 v194, v180, v181\n" \
"v_cvt_pk_bf16_f32 v195, v182, v183\n" \
"v_mfma_f32_16x16x32_bf16 a[96:99], v[148:151], v[128:131], a[96:99]\n" \
"v_accvgpr_read_b32 v196, a32\n" \
"v_accvgpr_read_b32 v197, a33\n" \
"v_mfma_f32_16x16x32_bf16 a[128:131], v[84:87], v[68:71], a[128:131]\n" \
"ds_read_b64_tr_b16 v[172:173], v38 offset:40960\n" \
"v_accvgpr_read_b32 v198, a34\n" \
"v_mfma_f32_16x16x32_bf16 a[128:131], v[148:151], v[132:135], a[128:131]\n" \
"v_accvgpr_read_b32 v199, a35\n" \
"v_accvgpr_read_b32 v180, a36\n" \
"v_mfma_f32_16x16x32_bf16 a[132:135], v[88:91], v[68:71], a[132:135]\n" \
"v_accvgpr_read_b32 v181, a37\n" \
"v_accvgpr_read_b32 v182, a38\n" \
"v_mfma_f32_16x16x32_bf16 a[132:135], v[152:155], v[132:135], a[132:135]\n" \
"ds_read_b64_tr_b16 v[174:175], v38 offset:41984\n" \
"v_accvgpr_read_b32 v183, a39\n" \
"v_mfma_f32_16x16x32_bf16 a[136:139], v[92:95], v[68:71], a[136:139]\n" \
"v_cvt_pk_bf16_f32 v196, v196, v197\n" \
"v_cvt_pk_bf16_f32 v197, v198, v199\n" \
"v_mfma_f32_16x16x32_bf16 a[136:139], v[156:159], v[132:135], a[136:139]\n" \
"v_cvt_pk_bf16_f32 v198, v180, v181\n" \
"v_cvt_pk_bf16_f32 v199, v182, v183\n" \
"v_mfma_f32_16x16x32_bf16 a[140:143], v[96:99], v[68:71], a[140:143]\n" \
"v_accvgpr_read_b32 v200, a64\n" \
"v_accvgpr_read_b32 v201, a65\n" \
"v_mfma_f32_16x16x32_bf16 a[140:143], v[160:163], v[132:135], a[140:143]\n" \
"ds_read_b64_tr_b16 v[112:113], v39 offset:32768\n" \
"v_accvgpr_read_b32 v202, a66\n" \
"v_mfma_f32_16x16x32_bf16 a[172:175], v[96:99], v[72:75], a[172:175]\n" \
"s_add_u32 s89, s41, 0x0\n" \
"s_nop 0\n" \
"buffer_store_dwordx4 v[184:187], v20, s[80:83], s89 offen offset:0 sc1\n" \
"v_mfma_f32_16x16x32_bf16 a[172:175], v[160:163], v[136:139], a[172:175]\n" \
"v_accvgpr_read_b32 v203, a67\n" \
"v_accvgpr_read_b32 v180, a68\n" \
"v_mfma_f32_16x16x32_bf16 a[168:171], v[92:95], v[72:75], a[168:171]\n" \
"ds_read_b64_tr_b16 v[114:115], v39 offset:33792\n" \
"v_accvgpr_read_b32 v181, a69\n" \
"v_mfma_f32_16x16x32_bf16 a[168:171], v[156:159], v[136:139], a[168:171]\n" \
"v_accvgpr_read_b32 v182, a70\n" \
"v_accvgpr_read_b32 v183, a71\n" \
"v_mfma_f32_16x16x32_bf16 a[164:167], v[88:91], v[72:75], a[164:167]\n" \
"v_cvt_pk_bf16_f32 v200, v200, v201\n" \
"v_cvt_pk_bf16_f32 v201, v202, v203\n" \
"v_mfma_f32_16x16x32_bf16 a[164:167], v[152:155], v[136:139], a[164:167]\n" \
"ds_read_b64_tr_b16 v[176:177], v39 offset:40960\n" \
"v_cvt_pk_bf16_f32 v202, v180, v181\n" \
"v_mfma_f32_16x16x32_bf16 a[160:163], v[84:87], v[72:75], a[160:163]\n" \
"s_add_u32 s88, s41, 0x0\n" \
"buffer_store_dwordx4 v[188:191], v20, s[80:83], s88 offen offset:1024 sc1\n" \
"v_mfma_f32_16x16x32_bf16 a[160:163], v[148:151], v[136:139], a[160:163]\n" \
"v_cvt_pk_bf16_f32 v203, v182, v183\n" \
"v_accvgpr_read_b32 v204, a72\n" \
"s_waitcnt lgkmcnt(15)\n" \
"v_mfma_f32_16x16x32_bf16 a[192:195], v[84:87], v[76:79], a[192:195]\n" \
"ds_read_b64_tr_b16 v[178:179], v39 offset:41984\n" \
"v_accvgpr_read_b32 v205, a73\n" \
"v_mfma_f32_16x16x32_bf16 a[192:195], v[148:151], v[140:143], a[192:195]\n" \
"v_accvgpr_read_b32 v206, a74\n" \
"v_accvgpr_read_b32 v207, a75\n" \
"v_mfma_f32_16x16x32_bf16 a[196:199], v[88:91], v[76:79], a[196:199]\n" \
"v_accvgpr_read_b32 v180, a76\n" \
"v_accvgpr_read_b32 v181, a77\n" \
"v_mfma_f32_16x16x32_bf16 a[196:199], v[152:155], v[140:143], a[196:199]\n" \
"v_accvgpr_read_b32 v182, a78\n" \
"v_accvgpr_read_b32 v183, a79\n" \
"v_mfma_f32_16x16x32_bf16 a[200:203], v[92:95], v[76:79], a[200:203]\n" \
"s_add_u32 s89, s41, 0x1000\n" \
"buffer_store_dwordx4 v[192:195], v20, s[80:83], s89 offen offset:1024 sc1\n" \
"v_mfma_f32_16x16x32_bf16 a[200:203], v[156:159], v[140:143], a[200:203]\n" \
"v_cvt_pk_bf16_f32 v204, v204, v205\n" \
"v_cvt_pk_bf16_f32 v205, v206, v207\n" \
"v_mfma_f32_16x16x32_bf16 a[204:207], v[96:99], v[76:79], a[204:207]\n" \
"v_cvt_pk_bf16_f32 v206, v180, v181\n" \
"v_cvt_pk_bf16_f32 v207, v182, v183\n" \
"v_mfma_f32_16x16x32_bf16 a[204:207], v[160:163], v[140:143], a[204:207]\n" \
"v_accvgpr_read_b32 v208, a104\n" \
"v_accvgpr_read_b32 v209, a105\n" \
"v_mfma_f32_16x16x32_bf16 a[236:239], v[96:99], v[80:83], a[236:239]\n" \
"v_accvgpr_read_b32 v210, a106\n" \
"v_accvgpr_read_b32 v211, a107\n" \
"v_mfma_f32_16x16x32_bf16 a[236:239], v[160:163], v[144:147], a[236:239]\n" \
"v_accvgpr_read_b32 v180, a108\n" \
"v_accvgpr_read_b32 v181, a109\n" \
"v_mfma_f32_16x16x32_bf16 a[232:235], v[92:95], v[80:83], a[232:235]\n" \
"s_add_u32 s88, s41, 0x1000\n" \
"buffer_store_dwordx4 v[196:199], v20, s[80:83], s88 offen offset:0 sc1\n" \
"v_mfma_f32_16x16x32_bf16 a[232:235], v[156:159], v[144:147], a[232:235]\n" \
"v_accvgpr_read_b32 v182, a110\n" \
"v_accvgpr_read_b32 v183, a111\n" \
"v_mfma_f32_16x16x32_bf16 a[228:231], v[88:91], v[80:83], a[228:231]\n" \
"v_cvt_pk_bf16_f32 v208, v208, v209\n" \
"v_cvt_pk_bf16_f32 v209, v210, v211\n" \
"v_mfma_f32_16x16x32_bf16 a[228:231], v[152:155], v[144:147], a[228:231]\n" \
"v_cvt_pk_bf16_f32 v210, v180, v181\n" \
"v_cvt_pk_bf16_f32 v211, v182, v183\n" \
"v_mfma_f32_16x16x32_bf16 a[224:227], v[84:87], v[80:83], a[224:227]\n" \
"v_accvgpr_read_b32 v212, a96\n" \
"v_accvgpr_read_b32 v213, a97\n" \
"v_mfma_f32_16x16x32_bf16 a[224:227], v[148:151], v[144:147], a[224:227]\n" \
"v_accvgpr_read_b32 v214, a98\n" \
"v_accvgpr_read_b32 v215, a99\n" \
"s_waitcnt vmcnt(5) lgkmcnt(0)\n" \
"s_barrier\n" \
"v_mov_b32_e32 v36, v32\n" \
"v_xor_b32_e32 v32, 0x0, v48\n" \
"v_mov_b32_e32 v37, v33\n" \
"v_xor_b32_e32 v33, 0x20, v48\n" \
"v_mov_b32_e32 v38, v34\n" \
"v_xor_b32_e32 v34, 0x40, v48\n" \
"v_mov_b32_e64 v39, v35\n" \
"v_xor_b32_e32 v35, 0x60, v48\n" \
"v_add_u32_e32 v22, s44, v28\n" \
"v_add_u32_e32 v28, s43, v28\n" \
"v_add_u32_e32 v23, s44, v29\n" \
"v_add_u32_e32 v29, s43, v29\n" \
"v_mfma_f32_16x16x32_bf16 a[16:19], v[100:103], v[52:55], a[16:19]\n" \
"s_add_u32 m0, s40, 0x8000\n" \
"s_add_u32 s85, s39, 0x2000\n" \
"v_mfma_f32_16x16x32_bf16 a[16:19], v[164:167], v[116:119], a[16:19]\n" \
"buffer_load_dwordx4 v20, s[80:83], s85 offen sc1 lds\n" \
"v_mfma_f32_16x16x32_bf16 a[20:23], v[104:107], v[52:55], a[20:23]\n" \
"ds_read_b64_tr_b16 v[84:85], v32 offset:0\n" \
"v_accvgpr_read_b32 v180, a100\n" \
"v_mfma_f32_16x16x32_bf16 a[20:23], v[168:171], v[116:119], a[20:23]\n" \
"s_add_u32 m0, s40, 0x9000\n" \
"s_add_u32 s84, s39, 0x3000\n" \
"v_mfma_f32_16x16x32_bf16 a[24:27], v[108:111], v[52:55], a[24:27]\n" \
"buffer_load_dwordx4 v20, s[80:83], s84 offen sc1 lds\n" \
"v_mfma_f32_16x16x32_bf16 a[24:27], v[172:175], v[116:119], a[24:27]\n" \
"ds_read_b64_tr_b16 v[86:87], v32 offset:512\n" \
"v_accvgpr_read_b32 v181, a101\n" \
"v_mfma_f32_16x16x32_bf16 a[28:31], v[112:115], v[52:55], a[28:31]\n" \
"ds_read_b64_tr_b16 v[148:149], v32 offset:4096\n" \
"v_accvgpr_read_b32 v182, a102\n" \
"v_mfma_f32_16x16x32_bf16 a[28:31], v[176:179], v[116:119], a[28:31]\n" \
"ds_read_b128 v[52:55], v28 offset:0\n" \
"v_accvgpr_read_b32 v183, a103\n" \
"v_mfma_f32_16x16x32_bf16 a[60:63], v[112:115], v[56:59], a[60:63]\n" \
"ds_read_b128 v[116:119], v29 offset:0\n" \
"v_cvt_pk_bf16_f32 v212, v212, v213\n" \
"v_mfma_f32_16x16x32_bf16 a[60:63], v[176:179], v[120:123], a[60:63]\n" \
"s_add_u32 m0, s40, 0xb000\n" \
"s_mov_b32 s85, 0x80\n" \
"v_mfma_f32_16x16x32_bf16 a[56:59], v[108:111], v[56:59], a[56:59]\n" \
"buffer_load_dwordx4 v21, s[76:79], s85 offen lds\n" \
"v_mfma_f32_16x16x32_bf16 a[56:59], v[172:175], v[120:123], a[56:59]\n" \
"s_add_u32 m0, s30, 0x8000\n" \
"s_mov_b32 s84, 0x80\n" \
"v_mfma_f32_16x16x32_bf16 a[52:55], v[104:107], v[56:59], a[52:55]\n" \
"buffer_load_dwordx4 v24, s[76:79], s84 offen lds\n" \
"v_mfma_f32_16x16x32_bf16 a[52:55], v[168:171], v[120:123], a[52:55]\n" \
"ds_read_b64_tr_b16 v[150:151], v32 offset:4608\n" \
"v_cvt_pk_bf16_f32 v213, v214, v215\n" \
"v_mfma_f32_16x16x32_bf16 a[48:51], v[100:103], v[56:59], a[48:51]\n" \
"ds_read_b64_tr_b16 v[88:89], v33 offset:0\n" \
"v_cvt_pk_bf16_f32 v214, v180, v181\n" \
"v_mfma_f32_16x16x32_bf16 a[48:51], v[164:167], v[120:123], a[48:51]\n" \
"ds_read_b128 v[56:59], v28 offset:2048\n" \
"v_cvt_pk_bf16_f32 v215, v182, v183\n" \
"v_mfma_f32_16x16x32_bf16 a[80:83], v[100:103], v[60:63], a[80:83]\n" \
"ds_read_b128 v[120:123], v29 offset:2048\n" \
"v_accvgpr_read_b32 v216, a128\n" \
"v_mfma_f32_16x16x32_bf16 a[80:83], v[164:167], v[124:127], a[80:83]\n" \
"s_add_u32 m0, s30, 0x8400\n" \
"s_mov_b32 s85, 0x2480\n" \
"v_mfma_f32_16x16x32_bf16 a[84:87], v[104:107], v[60:63], a[84:87]\n" \
"buffer_load_dwordx4 v25, s[76:79], s85 offen lds\n" \
"v_mfma_f32_16x16x32_bf16 a[84:87], v[168:171], v[124:127], a[84:87]\n" \
"s_add_u32 m0, s31, 0x8000\n" \
"s_mov_b32 s84, 0x80000\n" \
"v_mfma_f32_16x16x32_bf16 a[88:91], v[108:111], v[60:63], a[88:91]\n" \
"buffer_load_dwordx4 v26, s[68:71], s84 offen lds\n" \
"v_mfma_f32_16x16x32_bf16 a[88:91], v[172:175], v[124:127], a[88:91]\n" \
"ds_read_b64_tr_b16 v[90:91], v33 offset:512\n" \
"v_accvgpr_read_b32 v217, a129\n" \
"v_mfma_f32_16x16x32_bf16 a[92:95], v[112:115], v[60:63], a[92:95]\n" \
"ds_read_b64_tr_b16 v[152:153], v33 offset:4096\n" \
"v_accvgpr_read_b32 v218, a130\n" \
"v_mfma_f32_16x16x32_bf16 a[92:95], v[176:179], v[124:127], a[92:95]\n" \
"ds_read_b128 v[60:63], v28 offset:4096\n" \
"v_accvgpr_read_b32 v219, a131\n" \
"v_mfma_f32_16x16x32_bf16 a[124:127], v[112:115], v[64:67], a[124:127]\n" \
"ds_read_b128 v[124:127], v29 offset:4096\n" \
"v_accvgpr_read_b32 v180, a132\n" \
"v_mfma_f32_16x16x32_bf16 a[124:127], v[176:179], v[128:131], a[124:127]\n" \
"s_add_u32 m0, s30, 0x8800\n" \
"s_mov_b32 s85, 0x4880\n" \
"v_mfma_f32_16x16x32_bf16 a[120:123], v[108:111], v[64:67], a[120:123]\n" \
"buffer_load_dwordx4 v24, s[76:79], s85 offen lds\n" \
"v_mfma_f32_16x16x32_bf16 a[120:123], v[172:175], v[128:131], a[120:123]\n" \
"s_add_u32 m0, s30, 0x8c00\n" \
"s_mov_b32 s84, 0x6c80\n" \
"v_mfma_f32_16x16x32_bf16 a[116:119], v[104:107], v[64:67], a[116:119]\n" \
"buffer_load_dwordx4 v25, s[76:79], s84 offen lds\n" \
"v_mfma_f32_16x16x32_bf16 a[116:119], v[168:171], v[128:131], a[116:119]\n" \
"s_add_u32 m0, s31, 0x8400\n" \
"s_mov_b32 s85, 0x88000\n" \
"v_mfma_f32_16x16x32_bf16 a[112:115], v[100:103], v[64:67], a[112:115]\n" \
"buffer_load_dwordx4 v26, s[68:71], s85 offen lds\n" \
"v_mfma_f32_16x16x32_bf16 a[112:115], v[164:167], v[128:131], a[112:115]\n" \
"ds_read_b128 v[64:67], v28 offset:6144\n" \
"v_accvgpr_read_b32 v181, a133\n" \
"v_mfma_f32_16x16x32_bf16 a[144:147], v[100:103], v[68:71], a[144:147]\n" \
"ds_read_b128 v[128:131], v29 offset:6144\n" \
"v_accvgpr_read_b32 v182, a134\n" \
"v_mfma_f32_16x16x32_bf16 a[144:147], v[164:167], v[132:135], a[144:147]\n" \
"s_add_u32 m0, s30, 0x9000\n" \
"s_mov_b32 s84, 0x9080\n" \
"v_mfma_f32_16x16x32_bf16 a[148:151], v[104:107], v[68:71], a[148:151]\n" \
"buffer_load_dwordx4 v24, s[76:79], s84 offen lds\n" \
"v_mfma_f32_16x16x32_bf16 a[148:151], v[168:171], v[132:135], a[148:151]\n" \
".fill (L_pb + 20192 - .) / 4, 4, 0xBF800000\n" \
"L_pp5:\n" \
"s_cbranch_execz L_pret\n" \
"s_nop 0\n" \
"ds_read_b64_tr_b16 v[154:155], v33 offset:4608\n" \
"v_accvgpr_read_b32 v183, a135\n" \
"v_mfma_f32_16x16x32_bf16 a[152:155], v[108:111], v[68:71], a[152:155]\n" \
"s_add_u32 m0, s30, 0x9400\n" \
"s_mov_b32 s85, 0xb480\n" \
"v_mfma_f32_16x16x32_bf16 a[152:155], v[172:175], v[132:135], a[152:155]\n" \
"buffer_load_dwordx4 v25, s[76:79], s85 offen lds\n" \
"v_mfma_f32_16x16x32_bf16 a[156:159], v[112:115], v[68:71], a[156:159]\n" \
"ds_read_b64_tr_b16 v[96:97], v35 offset:0\n" \
"v_cvt_pk_bf16_f32 v216, v216, v217\n" \
"v_mfma_f32_16x16x32_bf16 a[156:159], v[176:179], v[132:135], a[156:159]\n" \
"ds_read_b128 v[68:71], v28 offset:8192\n" \
"v_cvt_pk_bf16_f32 v217, v218, v219\n" \
"v_mfma_f32_16x16x32_bf16 a[188:191], v[112:115], v[72:75], a[188:191]\n" \
"ds_read_b128 v[132:135], v29 offset:8192\n" \
"v_cvt_pk_bf16_f32 v218, v180, v181\n" \
"v_mfma_f32_16x16x32_bf16 a[188:191], v[176:179], v[136:139], a[188:191]\n" \
"s_add_u32 m0, s31, 0x8800\n" \
"s_mov_b32 s84, 0x90000\n" \
"v_mfma_f32_16x16x32_bf16 a[184:187], v[108:111], v[72:75], a[184:187]\n" \
"buffer_load_dwordx4 v27, s[68:71], s84 offen lds\n" \
"v_mfma_f32_16x16x32_bf16 a[184:187], v[172:175], v[136:139], a[184:187]\n" \
"ds_read_b64_tr_b16 v[98:99], v35 offset:512\n" \
"v_cvt_pk_bf16_f32 v219, v182, v183\n" \
"v_mfma_f32_16x16x32_bf16 a[180:183], v[104:107], v[72:75], a[180:183]\n" \
"s_add_u32 m0, s30, 0x9800\n" \
"s_mov_b32 s85, 0xd880\n" \
"v_mfma_f32_16x16x32_bf16 a[180:183], v[168:171], v[136:139], a[180:183]\n" \
"buffer_load_dwordx4 v24, s[76:79], s85 offen lds\n" \
"v_mfma_f32_16x16x32_bf16 a[176:179], v[100:103], v[72:75], a[176:179]\n" \
"ds_read_b64_tr_b16 v[160:161], v35 offset:4096\n" \
"v_accvgpr_read_b32 v220, a136\n" \
"v_mfma_f32_16x16x32_bf16 a[176:179], v[164:167], v[136:139], a[176:179]\n" \
"ds_read_b128 v[72:75], v28 offset:10240\n" \
"v_accvgpr_read_b32 v221, a137\n" \
"v_mfma_f32_16x16x32_bf16 a[208:211], v[100:103], v[76:79], a[208:211]\n" \
"ds_read_b128 v[136:139], v29 offset:10240\n" \
"v_accvgpr_read_b32 v222, a138\n" \
"v_mfma_f32_16x16x32_bf16 a[208:211], v[164:167], v[140:143], a[208:211]\n" \
"ds_read_b64_tr_b16 v[162:163], v35 offset:4608\n" \
"v_accvgpr_read_b32 v223, a139\n" \
"v_mfma_f32_16x16x32_bf16 a[212:215], v[104:107], v[76:79], a[212:215]\n" \
"ds_read_b64_tr_b16 v[92:93], v34 offset:0\n" \
"v_accvgpr_read_b32 v180, a140\n" \
"v_mfma_f32_16x16x32_bf16 a[212:215], v[168:171], v[140:143], a[212:215]\n" \
"ds_read_b64_tr_b16 v[94:95], v34 offset:512\n" \
"v_accvgpr_read_b32 v181, a141\n" \
"v_mfma_f32_16x16x32_bf16 a[216:219], v[108:111], v[76:79], a[216:219]\n" \
"ds_read_b64_tr_b16 v[156:157], v34 offset:4096\n" \
"v_accvgpr_read_b32 v182, a142\n" \
"v_mfma_f32_16x16x32_bf16 a[216:219], v[172:175], v[140:143], a[216:219]\n" \
"ds_read_b64_tr_b16 v[158:159], v34 offset:4608\n" \
"v_accvgpr_read_b32 v183, a143\n" \
"v_mfma_f32_16x16x32_bf16 a[220:223], v[112:115], v[76:79], a[220:223]\n" \
"ds_read_b128 v[248:251], v22 offset:0\n" \
"v_cvt_pk_bf16_f32 v220, v220, v221\n" \
"v_mfma_f32_16x16x32_bf16 a[220:223], v[176:179], v[140:143], a[220:223]\n" \
"ds_read_b128 v[76:79], v28 offset:12288\n" \
"v_cvt_pk_bf16_f32 v221, v222, v223\n" \
"v_mfma_f32_16x16x32_bf16 a[252:255], v[112:115], v[80:83], a[252:255]\n" \
"ds_read_b128 v[140:143], v29 offset:12288\n" \
"v_cvt_pk_bf16_f32 v222, v180, v181\n" \
"v_mfma_f32_16x16x32_bf16 a[252:255], v[176:179], v[144:147], a[252:255]\n" \
"ds_read_b128 v[48:51], v23 offset:0\n" \
"v_cvt_pk_bf16_f32 v223, v182, v183\n" \
"v_mfma_f32_16x16x32_bf16 a[248:251], v[108:111], v[80:83], a[248:251]\n" \
"v_accvgpr_read_b32 v224, a168\n" \
"v_accvgpr_read_b32 v225, a169\n" \
"v_mfma_f32_16x16x32_bf16 a[248:251], v[172:175], v[144:147], a[248:251]\n" \
"v_accvgpr_read_b32 v226, a170\n" \
"v_accvgpr_read_b32 v227, a171\n" \
"v_mfma_f32_16x16x32_bf16 a[244:247], v[104:107], v[80:83], a[244:247]\n" \
"v_accvgpr_read_b32 v180, a172\n" \
"v_accvgpr_read_b32 v181, a173\n" \
"v_mfma_f32_16x16x32_bf16 a[244:247], v[168:171], v[144:147], a[244:247]\n" \
"v_accvgpr_read_b32 v182, a174\n" \
"v_accvgpr_read_b32 v183, a175\n" \
"v_mfma_f32_16x16x32_bf16 a[240:243], v[100:103], v[80:83], a[240:243]\n" \
"v_cvt_pk_bf16_f32 v224, v224, v225\n" \
"v_cvt_pk_bf16_f32 v225, v226, v227\n" \
"v_mfma_f32_16x16x32_bf16 a[240:243], v[164:167], v[144:147], a[240:243]\n" \
"ds_read_b128 v[80:83], v28 offset:14336\n" \
"ds_read_b128 v[144:147], v29 offset:14336\n" \
"s_waitcnt lgkmcnt(15)\n" \
"s_nop 0\n" \
"v_mfma_f32_16x16x32_bf16 a[0:3], v[84:87], v[52:55], 0\n" \
"s_add_u32 m0, s30, 0x9c00\n" \
"s_mov_b32 s85, 0xfc80\n" \
"v_cvt_pk_bf16_f32 v226, v180, v181\n" \
"v_mfma_f32_16x16x32_bf16 a[0:3], v[148:151], v[116:119], a[0:3]\n" \
"buffer_load_dwordx4 v25, s[76:79], s85 offen lds\n" \
"v_cvt_pk_bf16_f32 v227, v182, v183\n" \
"v_mfma_f32_16x16x32_bf16 a[4:7], v[88:91], v[52:55], 0\n" \
"ds_read_b64_tr_b16 v[100:101], v36 offset:0\n" \
"v_accvgpr_read_b32 v228, a160\n" \
"v_mfma_f32_16x16x32_bf16 a[4:7], v[152:155], v[116:119], a[4:7]\n" \
"s_add_u32 m0, s31, 0x8c00\n" \
"s_mov_b32 s84, 0x98000\n" \
"v_accvgpr_read_b32 v229, a161\n" \
"s_waitcnt lgkmcnt(15)\n" \
"v_mfma_f32_16x16x32_bf16 a[36:39], v[96:99], v[52:55], 0\n" \
"buffer_load_dwordx4 v27, s[68:71], s84 offen lds\n" \
"v_accvgpr_read_b32 v230, a162\n" \
"s_waitcnt lgkmcnt(11)\n" \
"v_mfma_f32_16x16x32_bf16 a[36:39], v[160:163], v[116:119], a[36:39]\n" \
"ds_read_b64_tr_b16 v[102:103], v36 offset:1024\n" \
"v_accvgpr_read_b32 v231, a163\n" \
"s_waitcnt lgkmcnt(10)\n" \
"v_mfma_f32_16x16x32_bf16 a[32:35], v[52:55], v[92:95], 0\n" \
"ds_read_b64_tr_b16 v[164:165], v36 offset:8192\n" \
"v_accvgpr_read_b32 v180, a164\n" \
"s_waitcnt lgkmcnt(9)\n" \
"v_mfma_f32_16x16x32_bf16 a[32:35], v[116:119], v[156:159], a[32:35]\n" \
"ds_read_b64_tr_b16 v[166:167], v36 offset:9216\n" \
"v_accvgpr_read_b32 v181, a165\n" \
"v_mfma_f32_16x16x32_bf16 a[96:99], v[56:59], v[92:95], 0\n" \
"ds_read_b64_tr_b16 v[104:105], v37 offset:0\n" \
"v_accvgpr_read_b32 v182, a166\n" \
"v_mfma_f32_16x16x32_bf16 a[96:99], v[120:123], v[156:159], a[96:99]\n" \
"ds_read_b64_tr_b16 v[106:107], v37 offset:1024\n" \
"v_accvgpr_read_b32 v183, a167\n" \
"v_mfma_f32_16x16x32_bf16 a[100:103], v[96:99], v[56:59], 0\n" \
"ds_read_b64_tr_b16 v[168:169], v37 offset:8192\n" \
"v_cvt_pk_bf16_f32 v228, v228, v229\n" \
"v_mfma_f32_16x16x32_bf16 a[100:103], v[160:163], v[120:123], a[100:103]\n" \
"s_add_u32 s89, s41, 0x2000\n" \
"buffer_store_dwordx4 v[200:203], v20, s[80:83], s89 offen offset:0 sc1\n" \
"v_mfma_f32_16x16x32_bf16 a[68:71], v[88:91], v[56:59], 0\n" \
"ds_read_b64_tr_b16 v[170:171], v37 offset:9216\n" \
"v_cvt_pk_bf16_f32 v229, v230, v231\n" \
"v_mfma_f32_16x16x32_bf16 a[68:71], v[152:155], v[120:123], a[68:71]\n" \
"ds_read_b64_tr_b16 v[108:109], v38 offset:0\n" \
"v_cvt_pk_bf16_f32 v230, v180, v181\n" \
"v_mfma_f32_16x16x32_bf16 a[64:67], v[84:87], v[56:59], 0\n" \
"ds_read_b64_tr_b16 v[110:111], v38 offset:1024\n" \
"v_cvt_pk_bf16_f32 v231, v182, v183\n" \
"v_mfma_f32_16x16x32_bf16 a[64:67], v[148:151], v[120:123], a[64:67]\n" \
"v_accvgpr_read_b32 v232, a192\n" \
"v_accvgpr_read_b32 v233, a193\n" \
"v_accvgpr_read_b32 v234, a194\n" \
"v_mfma_f32_16x16x32_bf16 a[128:131], v[84:87], v[60:63], 0\n" \
"s_add_u32 s88, s41, 0x2000\n" \
"buffer_store_dwordx4 v[204:207], v20, s[80:83], s88 offen offset:1024 sc1\n" \
"v_mfma_f32_16x16x32_bf16 a[128:131], v[148:151], v[124:127], a[128:131]\n" \
"ds_read_b64_tr_b16 v[172:173], v38 offset:8192\n" \
"v_accvgpr_read_b32 v235, a195\n" \
"v_mfma_f32_16x16x32_bf16 a[132:135], v[88:91], v[60:63], 0\n" \
"ds_read_b64_tr_b16 v[174:175], v38 offset:9216\n" \
"v_accvgpr_read_b32 v180, a196\n" \
"v_mfma_f32_16x16x32_bf16 a[132:135], v[152:155], v[124:127], a[132:135]\n" \
"ds_read_b64_tr_b16 v[112:113], v39 offset:0\n" \
"v_accvgpr_read_b32 v181, a197\n" \
"v_mfma_f32_16x16x32_bf16 a[164:167], v[96:99], v[60:63], 0\n" \
"v_accvgpr_read_b32 v182, a198\n" \
"v_accvgpr_read_b32 v183, a199\n" \
"v_cvt_pk_bf16_f32 v232, v232, v233\n" \
"v_mfma_f32_16x16x32_bf16 a[164:167], v[160:163], v[124:127], a[164:167]\n" \
"ds_read_b64_tr_b16 v[114:115], v39 offset:1024\n" \
"v_cvt_pk_bf16_f32 v233, v234, v235\n" \
"v_mfma_f32_16x16x32_bf16 a[160:163], v[60:63], v[92:95], 0\n" \
"s_add_u32 s89, s41, 0x3000\n" \
"buffer_store_dwordx4 v[208:211], v20, s[80:83], s89 offen offset:1024 sc1\n" \
"v_mfma_f32_16x16x32_bf16 a[160:163], v[124:127], v[156:159], a[160:163]\n" \
"ds_read_b64_tr_b16 v[176:177], v39 offset:8192\n" \
"v_cvt_pk_bf16_f32 v234, v180, v181\n" \
"v_cvt_pk_bf16_f32 v235, v182, v183\n" \
"v_accvgpr_read_b32 v236, a200\n" \
"v_accvgpr_read_b32 v237, a201\n" \
"v_accvgpr_read_b32 v238, a202\n" \
"v_accvgpr_read_b32 v239, a203\n" \
"v_accvgpr_read_b32 v180, a204\n" \
"v_accvgpr_read_b32 v181, a205\n" \
"v_accvgpr_read_b32 v182, a206\n" \
"v_accvgpr_read_b32 v183, a207\n" \
"v_cvt_pk_bf16_f32 v236, v236, v237\n" \
"v_cvt_pk_bf16_f32 v237, v238, v239\n" \
"v_cvt_pk_bf16_f32 v238, v180, v181\n" \
"v_cvt_pk_bf16_f32 v239, v182, v183\n" \
"v_accvgpr_read_b32 v240, a232\n" \
"v_accvgpr_read_b32 v241, a233\n" \
"v_accvgpr_read_b32 v242, a234\n" \
"v_accvgpr_read_b32 v243, a235\n" \
"v_accvgpr_read_b32 v180, a236\n" \
"v_accvgpr_read_b32 v181, a237\n" \
"v_accvgpr_read_b32 v182, a238\n" \
"v_accvgpr_read_b32 v183, a239\n" \
"v_cvt_pk_bf16_f32 v240, v240, v241\n" \
"v_cvt_pk_bf16_f32 v241, v242, v243\n" \
"v_cvt_pk_bf16_f32 v242, v180, v181\n" \
"v_cvt_pk_bf16_f32 v243, v182, v183\n" \
"v_accvgpr_read_b32 v244, a224\n" \
"v_accvgpr_read_b32 v245, a225\n" \
"v_accvgpr_read_b32 v246, a226\n" \
"v_accvgpr_read_b32 v247, a227\n" \
"v_accvgpr_read_b32 v180, a228\n" \
"v_accvgpr_read_b32 v181, a229\n" \
"v_accvgpr_read_b32 v182, a230\n" \
"v_accvgpr_read_b32 v183, a231\n" \
"v_cvt_pk_bf16_f32 v244, v244, v245\n" \
"v_cvt_pk_bf16_f32 v245, v246, v247\n" \
"v_cvt_pk_bf16_f32 v246, v180, v181\n" \
"v_cvt_pk_bf16_f32 v247, v182, v183\n" \
"v_mfma_f32_16x16x32_bf16 a[224:227], v[64:67], v[92:95], 0\n" \
"ds_read_b64_tr_b16 v[178:179], v39 offset:9216\n" \
"v_accvgpr_read_b32 v184, a24\n" \
"v_mfma_f32_16x16x32_bf16 a[224:227], v[128:131], v[156:159], a[224:227]\n" \
"v_accvgpr_read_b32 v185, a25\n" \
"v_accvgpr_read_b32 v186, a26\n" \
"v_accvgpr_read_b32 v187, a27\n" \
"v_mfma_f32_16x16x32_bf16 a[228:231], v[96:99], v[64:67], 0\n" \
"v_accvgpr_read_b32 v180, a28\n" \
"v_accvgpr_read_b32 v181, a29\n" \
"v_accvgpr_read_b32 v182, a30\n" \
"v_mfma_f32_16x16x32_bf16 a[228:231], v[160:163], v[128:131], a[228:231]\n" \
"s_add_u32 s88, s41, 0x3000\n" \
"buffer_store_dwordx4 v[212:215], v20, s[80:83], s88 offen offset:0 sc1\n" \
"v_mfma_f32_16x16x32_bf16 a[196:199], v[88:91], v[64:67], 0\n" \
"v_accvgpr_read_b32 v183, a31\n" \
"v_cvt_pk_bf16_f32 v184, v184, v185\n" \
"v_cvt_pk_bf16_f32 v185, v186, v187\n" \
"v_mfma_f32_16x16x32_bf16 a[196:199], v[152:155], v[128:131], a[196:199]\n" \
"v_cvt_pk_bf16_f32 v186, v180, v181\n" \
"v_cvt_pk_bf16_f32 v187, v182, v183\n" \
"v_accvgpr_read_b32 v188, a16\n" \
"v_mfma_f32_16x16x32_bf16 a[192:195], v[84:87], v[64:67], 0\n" \
"v_accvgpr_read_b32 v189, a17\n" \
"v_accvgpr_read_b32 v190, a18\n" \
"v_accvgpr_read_b32 v191, a19\n" \
"v_mfma_f32_16x16x32_bf16 a[192:195], v[148:151], v[128:131], a[192:195]\n" \
"v_accvgpr_read_b32 v180, a20\n" \
"v_accvgpr_read_b32 v181, a21\n" \
"v_accvgpr_read_b32 v182, a22\n" \
"s_waitcnt lgkmcnt(15)\n" \
"s_nop 0\n" \
"v_mfma_f32_16x16x32_bf16 a[8:11], v[88:91], v[248:251], 0\n" \
"v_accvgpr_read_b32 v183, a23\n" \
"v_cvt_pk_bf16_f32 v188, v188, v189\n" \
"v_cvt_pk_bf16_f32 v189, v190, v191\n" \
"v_mfma_f32_16x16x32_bf16 a[8:11], v[152:155], v[48:51], a[8:11]\n" \
"v_cvt_pk_bf16_f32 v190, v180, v181\n" \
"v_cvt_pk_bf16_f32 v191, v182, v183\n" \
"v_accvgpr_read_b32 v192, a48\n" \
"v_mfma_f32_16x16x32_bf16 a[40:43], v[96:99], v[248:251], 0\n" \
"v_accvgpr_read_b32 v193, a49\n" \
"v_accvgpr_read_b32 v194, a50\n" \
"v_accvgpr_read_b32 v195, a51\n" \
"v_mfma_f32_16x16x32_bf16 a[40:43], v[160:163], v[48:51], a[40:43]\n" \
"v_accvgpr_read_b32 v180, a52\n" \
"v_accvgpr_read_b32 v181, a53\n" \
"v_accvgpr_read_b32 v182, a54\n" \
"v_mfma_f32_16x16x32_bf16 a[12:15], v[248:251], v[92:95], 0\n" \
"v_accvgpr_read_b32 v183, a55\n" \
"v_cvt_pk_bf16_f32 v192, v192, v193\n" \
"v_cvt_pk_bf16_f32 v193, v194, v195\n" \
"v_mfma_f32_16x16x32_bf16 a[12:15], v[48:51], v[156:159], a[12:15]\n" \
"v_cvt_pk_bf16_f32 v194, v180, v181\n" \
"v_cvt_pk_bf16_f32 v195, v182, v183\n" \
"v_accvgpr_read_b32 v196, a56\n" \
"s_waitcnt vmcnt(4) lgkmcnt(0)\n" \
"s_barrier\n" \
"v_mfma_f32_16x16x32_bf16 a[72:75], v[100:103], v[72:75], 0\n" \
"s_add_u32 m0, s40, 0x0\n" \
"s_nop 0\n" \
"s_add_u32 s85, s39, 0x4000\n" \
"v_accvgpr_read_b32 v197, a57\n" \
"v_mfma_f32_16x16x32_bf16 a[72:75], v[164:167], v[136:139], a[72:75]\n" \
"buffer_load_dwordx4 v20, s[80:83], s85 offen sc1 lds\n" \
"v_accvgpr_read_b32 v198, a58\n" \
"v_mfma_f32_16x16x32_bf16 a[76:79], v[104:107], v[72:75], 0\n" \
"ds_read_b64_tr_b16 v[84:85], v32 offset:32768\n" \
"v_accvgpr_read_b32 v199, a59\n" \
"v_mfma_f32_16x16x32_bf16 a[76:79], v[168:171], v[136:139], a[76:79]\n" \
"ds_read_b64_tr_b16 v[86:87], v32 offset:33280\n" \
"v_accvgpr_read_b32 v180, a60\n" \
"v_mfma_f32_16x16x32_bf16 a[104:107], v[108:111], v[72:75], 0\n" \
"s_add_u32 m0, s40, 0x1000\n" \
"s_add_u32 s84, s39, 0x5000\n" \
"v_accvgpr_read_b32 v181, a61\n" \
"v_mfma_f32_16x16x32_bf16 a[104:107], v[172:175], v[136:139], a[104:107]\n" \
"buffer_load_dwordx4 v20, s[80:83], s84 offen sc1 lds\n" \
"v_accvgpr_read_b32 v182, a62\n" \
"v_mfma_f32_16x16x32_bf16 a[108:111], v[112:115], v[72:75], 0\n" \
"ds_read_b64_tr_b16 v[148:149], v32 offset:36864\n" \
"v_accvgpr_read_b32 v183, a63\n" \
"v_mfma_f32_16x16x32_bf16 a[108:111], v[176:179], v[136:139], a[108:111]\n" \
"ds_read_b128 v[72:75], v28 offset:43008\n" \
"v_cvt_pk_bf16_f32 v196, v196, v197\n" \
"v_mfma_f32_16x16x32_bf16 a[172:175], v[112:115], v[76:79], 0\n" \
"ds_read_b128 v[136:139], v29 offset:43008\n" \
"v_cvt_pk_bf16_f32 v197, v198, v199\n" \
"v_mfma_f32_16x16x32_bf16 a[172:175], v[176:179], v[140:143], a[172:175]\n" \
"s_add_u32 m0, s40, 0x3000\n" \
"s_mov_b32 s85, 0x100\n" \
"v_cvt_pk_bf16_f32 v198, v180, v181\n" \
"v_mfma_f32_16x16x32_bf16 a[168:171], v[108:111], v[76:79], 0\n" \
"buffer_load_dwordx4 v21, s[76:79], s85 offen lds\n" \
"v_cvt_pk_bf16_f32 v199, v182, v183\n" \
"v_mfma_f32_16x16x32_bf16 a[168:171], v[172:175], v[140:143], a[168:171]\n" \
"ds_read_b64_tr_b16 v[150:151], v32 offset:37376\n" \
"v_accvgpr_read_b32 v252, a88\n" \
"v_mfma_f32_16x16x32_bf16 a[140:143], v[104:107], v[76:79], 0\n" \
"s_add_u32 m0, s30, 0x0\n" \
"s_nop 0\n" \
"s_mov_b32 s84, 0x100\n" \
"v_accvgpr_read_b32 v253, a89\n" \
"v_mfma_f32_16x16x32_bf16 a[140:143], v[168:171], v[140:143], a[140:143]\n" \
"buffer_load_dwordx4 v24, s[76:79], s84 offen lds\n" \
"v_accvgpr_read_b32 v254, a90\n" \
"v_mfma_f32_16x16x32_bf16 a[136:139], v[100:103], v[76:79], 0\n" \
"ds_read_b64_tr_b16 v[88:89], v33 offset:32768\n" \
"v_accvgpr_read_b32 v255, a91\n" \
"v_mfma_f32_16x16x32_bf16 a[136:139], v[164:167], v[140:143], a[136:139]\n" \
"ds_read_b128 v[76:79], v28 offset:45056\n" \
"v_accvgpr_read_b32 v180, a92\n" \
"v_mfma_f32_16x16x32_bf16 a[200:203], v[100:103], v[80:83], 0\n" \
"ds_read_b128 v[140:143], v29 offset:45056\n" \
"v_accvgpr_read_b32 v181, a93\n" \
"v_mfma_f32_16x16x32_bf16 a[200:203], v[164:167], v[144:147], a[200:203]\n" \
"s_add_u32 m0, s30, 0x400\n" \
"s_mov_b32 s85, 0x2500\n" \
"v_accvgpr_read_b32 v182, a94\n" \
"v_mfma_f32_16x16x32_bf16 a[204:207], v[104:107], v[80:83], 0\n" \
"buffer_load_dwordx4 v25, s[76:79], s85 offen lds\n" \
"v_accvgpr_read_b32 v183, a95\n" \
"v_mfma_f32_16x16x32_bf16 a[204:207], v[168:171], v[144:147], a[204:207]\n" \
"ds_read_b64_tr_b16 v[90:91], v33 offset:33280\n" \
"v_cvt_pk_bf16_f32 v252, v252, v253\n" \
"v_mfma_f32_16x16x32_bf16 a[232:235], v[108:111], v[80:83], 0\n" \
"ds_read_b64_tr_b16 v[152:153], v33 offset:36864\n" \
"v_cvt_pk_bf16_f32 v253, v254, v255\n" \
"v_mfma_f32_16x16x32_bf16 a[232:235], v[172:175], v[144:147], a[232:235]\n" \
"s_add_u32 m0, s31, 0x0\n" \
"s_nop 0\n" \
"s_mov_b32 s84, 0x100000\n" \
"v_cvt_pk_bf16_f32 v254, v180, v181\n" \
"v_mfma_f32_16x16x32_bf16 a[236:239], v[112:115], v[80:83], 0\n" \
"buffer_load_dwordx4 v26, s[68:71], s84 offen lds\n" \
"v_cvt_pk_bf16_f32 v255, v182, v183\n" \
"v_mfma_f32_16x16x32_bf16 a[236:239], v[176:179], v[144:147], a[236:239]\n" \
"ds_read_b128 v[80:83], v28 offset:47104\n" \
"v_accvgpr_write_b32 a44, v252\n" \
"v_mfma_f32_16x16x32_bf16 a[28:31], v[112:115], v[52:55], 0\n" \
"ds_read_b128 v[144:147], v29 offset:47104\n" \
"v_accvgpr_write_b32 a45, v253\n" \
"v_mfma_f32_16x16x32_bf16 a[28:31], v[176:179], v[116:119], a[28:31]\n" \
"s_add_u32 m0, s30, 0x800\n" \
"s_mov_b32 s85, 0x4900\n" \
"v_accvgpr_write_b32 a46, v254\n" \
"v_mfma_f32_16x16x32_bf16 a[24:27], v[108:111], v[52:55], 0\n" \
"buffer_load_dwordx4 v24, s[76:79], s85 offen lds\n" \
"v_accvgpr_write_b32 a47, v255\n" \
"v_mfma_f32_16x16x32_bf16 a[24:27], v[172:175], v[116:119], a[24:27]\n" \
"ds_read_b64_tr_b16 v[154:155], v33 offset:37376\n" \
"v_accvgpr_read_b32 v252, a80\n" \
"v_mfma_f32_16x16x32_bf16 a[20:23], v[104:107], v[52:55], 0\n" \
"ds_read_b64_tr_b16 v[96:97], v35 offset:32768\n" \
"v_accvgpr_read_b32 v253, a81\n" \
"v_mfma_f32_16x16x32_bf16 a[20:23], v[168:171], v[116:119], a[20:23]\n" \
"ds_read_b64_tr_b16 v[98:99], v35 offset:33280\n" \
"v_accvgpr_read_b32 v254, a82\n" \
"v_mfma_f32_16x16x32_bf16 a[16:19], v[100:103], v[52:55], 0\n" \
"ds_read_b64_tr_b16 v[160:161], v35 offset:36864\n" \
"v_accvgpr_read_b32 v255, a83\n" \
"v_mfma_f32_16x16x32_bf16 a[16:19], v[164:167], v[116:119], a[16:19]\n" \
"ds_read_b128 v[52:55], v28 offset:32768\n" \
"v_accvgpr_read_b32 v180, a84\n" \
"v_mfma_f32_16x16x32_bf16 a[48:51], v[100:103], v[56:59], 0\n" \
"ds_read_b128 v[116:119], v29 offset:32768\n" \
"v_accvgpr_read_b32 v181, a85\n" \
"v_mfma_f32_16x16x32_bf16 a[48:51], v[164:167], v[120:123], a[48:51]\n" \
"s_add_u32 m0, s30, 0xc00\n" \
"s_mov_b32 s84, 0x6d00\n" \
"v_accvgpr_read_b32 v182, a86\n" \
"v_mfma_f32_16x16x32_bf16 a[52:55], v[104:107], v[56:59], 0\n" \
"buffer_load_dwordx4 v25, s[76:79], s84 offen lds\n" \
"v_accvgpr_read_b32 v183, a87\n" \
"v_mfma_f32_16x16x32_bf16 a[52:55], v[168:171], v[120:123], a[52:55]\n" \
"s_add_u32 m0, s31, 0x400\n" \
"s_mov_b32 s85, 0x108000\n" \
"v_cvt_pk_bf16_f32 v252, v252, v253\n" \
"v_mfma_f32_16x16x32_bf16 a[56:59], v[108:111], v[56:59], 0\n" \
"buffer_load_dwordx4 v26, s[68:71], s85 offen lds\n" \
"v_cvt_pk_bf16_f32 v253, v254, v255\n" \
"v_mfma_f32_16x16x32_bf16 a[56:59], v[172:175], v[120:123], a[56:59]\n" \
"ds_read_b64_tr_b16 v[162:163], v35 offset:37376\n" \
"v_cvt_pk_bf16_f32 v254, v180, v181\n" \
"v_mfma_f32_16x16x32_bf16 a[60:63], v[112:115], v[56:59], 0\n" \
"ds_read_b64_tr_b16 v[92:93], v34 offset:32768\n" \
"v_cvt_pk_bf16_f32 v255, v182, v183\n" \
"v_mfma_f32_16x16x32_bf16 a[60:63], v[176:179], v[120:123], a[60:63]\n" \
"ds_read_b128 v[56:59], v28 offset:34816\n" \
"v_accvgpr_read_b32 v200, a112\n" \
"v_mfma_f32_16x16x32_bf16 a[92:95], v[112:115], v[60:63], 0\n" \
"ds_read_b128 v[120:123], v29 offset:34816\n" \
"v_accvgpr_read_b32 v201, a113\n" \
"v_mfma_f32_16x16x32_bf16 a[92:95], v[176:179], v[124:127], a[92:95]\n" \
"s_add_u32 m0, s30, 0x1000\n" \
"s_mov_b32 s84, 0x9100\n" \
"v_accvgpr_read_b32 v202, a114\n" \
"v_mfma_f32_16x16x32_bf16 a[88:91], v[108:111], v[60:63], 0\n" \
"buffer_load_dwordx4 v24, s[76:79], s84 offen lds\n" \
"v_accvgpr_read_b32 v203, a115\n" \
"v_mfma_f32_16x16x32_bf16 a[88:91], v[172:175], v[124:127], a[88:91]\n" \
"s_add_u32 m0, s30, 0x1400\n" \
"s_mov_b32 s85, 0xb500\n" \
"v_accvgpr_read_b32 v180, a116\n" \
"v_mfma_f32_16x16x32_bf16 a[84:87], v[104:107], v[60:63], 0\n" \
"buffer_load_dwordx4 v25, s[76:79], s85 offen lds\n" \
"v_accvgpr_read_b32 v181, a117\n" \
"v_mfma_f32_16x16x32_bf16 a[84:87], v[168:171], v[124:127], a[84:87]\n" \
"ds_read_b64_tr_b16 v[94:95], v34 offset:33280\n" \
"v_accvgpr_read_b32 v182, a118\n" \
"v_mfma_f32_16x16x32_bf16 a[80:83], v[100:103], v[60:63], 0\n" \
"ds_read_b64_tr_b16 v[156:157], v34 offset:36864\n" \
"v_accvgpr_read_b32 v183, a119\n" \
"v_mfma_f32_16x16x32_bf16 a[80:83], v[164:167], v[124:127], a[80:83]\n" \
"ds_read_b128 v[60:63], v28 offset:36864\n" \
"v_cvt_pk_bf16_f32 v200, v200, v201\n" \
"v_cvt_pk_bf16_f32 v201, v202, v203\n" \
"v_cvt_pk_bf16_f32 v202, v180, v181\n" \
"v_cvt_pk_bf16_f32 v203, v182, v183\n" \
"v_mfma_f32_16x16x32_bf16 a[112:115], v[100:103], v[64:67], 0\n" \
"ds_read_b128 v[124:127], v29 offset:36864\n" \
"v_accvgpr_read_b32 v204, a120\n" \
"v_mfma_f32_16x16x32_bf16 a[112:115], v[164:167], v[128:131], a[112:115]\n" \
"s_add_u32 m0, s31, 0x800\n" \
"s_mov_b32 s84, 0x110000\n" \
"v_accvgpr_read_b32 v205, a121\n" \
"v_mfma_f32_16x16x32_bf16 a[116:119], v[104:107], v[64:67], 0\n" \
"buffer_load_dwordx4 v27, s[68:71], s84 offen lds\n" \
"v_accvgpr_read_b32 v206, a122\n" \
"v_mfma_f32_16x16x32_bf16 a[116:119], v[168:171], v[128:131], a[116:119]\n" \
"s_add_u32 m0, s30, 0x1800\n" \
"s_mov_b32 s85, 0xd900\n" \
"v_accvgpr_read_b32 v207, a123\n" \
"v_accvgpr_read_b32 v180, a124\n" \
"v_accvgpr_read_b32 v181, a125\n" \
"v_accvgpr_read_b32 v182, a126\n" \
"v_accvgpr_read_b32 v183, a127\n" \
"v_cvt_pk_bf16_f32 v204, v204, v205\n" \
"v_cvt_pk_bf16_f32 v205, v206, v207\n" \
"v_cvt_pk_bf16_f32 v206, v180, v181\n" \
"v_cvt_pk_bf16_f32 v207, v182, v183\n" \
"v_mfma_f32_16x16x32_bf16 a[120:123], v[108:111], v[64:67], 0\n" \
"buffer_load_dwordx4 v24, s[76:79], s85 offen lds\n" \
"v_accvgpr_read_b32 v208, a152\n" \
"v_mfma_f32_16x16x32_bf16 a[120:123], v[172:175], v[128:131], a[120:123]\n" \
"ds_read_b64_tr_b16 v[158:159], v34 offset:37376\n" \
"v_accvgpr_read_b32 v209, a153\n" \
"v_mfma_f32_16x16x32_bf16 a[124:127], v[112:115], v[64:67], 0\n" \
"ds_read_b128 v[248:251], v22 offset:32768\n" \
"v_accvgpr_read_b32 v210, a154\n" \
"v_mfma_f32_16x16x32_bf16 a[124:127], v[176:179], v[128:131], a[124:127]\n" \
"ds_read_b128 v[64:67], v28 offset:38912\n" \
"v_accvgpr_read_b32 v211, a155\n" \
"v_accvgpr_read_b32 v180, a156\n" \
"v_accvgpr_read_b32 v181, a157\n" \
"v_accvgpr_read_b32 v182, a158\n" \
"v_accvgpr_read_b32 v183, a159\n" \
"v_cvt_pk_bf16_f32 v208, v208, v209\n" \
"v_cvt_pk_bf16_f32 v209, v210, v211\n" \
"v_cvt_pk_bf16_f32 v210, v180, v181\n" \
"v_cvt_pk_bf16_f32 v211, v182, v183\n" \
"v_mfma_f32_16x16x32_bf16 a[156:159], v[112:115], v[68:71], 0\n" \
"ds_read_b128 v[128:131], v29 offset:38912\n" \
"v_accvgpr_read_b32 v212, a144\n" \
"v_mfma_f32_16x16x32_bf16 a[156:159], v[176:179], v[132:135], a[156:159]\n" \
"ds_read_b128 v[48:51], v23 offset:32768\n" \
"v_accvgpr_read_b32 v213, a145\n" \
"v_mfma_f32_16x16x32_bf16 a[152:155], v[108:111], v[68:71], 0\n" \
"v_accvgpr_read_b32 v214, a146\n" \
"v_accvgpr_read_b32 v215, a147\n" \
"v_accvgpr_read_b32 v180, a148\n" \
"v_mfma_f32_16x16x32_bf16 a[152:155], v[172:175], v[132:135], a[152:155]\n" \
"v_accvgpr_read_b32 v181, a149\n" \
"v_accvgpr_read_b32 v182, a150\n" \
"v_accvgpr_read_b32 v183, a151\n" \
"v_cvt_pk_bf16_f32 v212, v212, v213\n" \
"v_cvt_pk_bf16_f32 v213, v214, v215\n" \
"v_cvt_pk_bf16_f32 v214, v180, v181\n" \
"v_cvt_pk_bf16_f32 v215, v182, v183\n" \
"v_mfma_f32_16x16x32_bf16 a[148:151], v[104:107], v[68:71], 0\n" \
"v_mfma_f32_16x16x32_bf16 a[148:151], v[168:171], v[132:135], a[148:151]\n" \
"v_mfma_f32_16x16x32_bf16 a[144:147], v[100:103], v[68:71], 0\n" \
"v_mfma_f32_16x16x32_bf16 a[144:147], v[164:167], v[132:135], a[144:147]\n" \
"ds_read_b128 v[68:71], v28 offset:40960\n" \
"ds_read_b128 v[132:135], v29 offset:40960\n" \
"s_waitcnt lgkmcnt(15)\n" \
"v_mfma_f32_16x16x32_bf16 a[0:3], v[84:87], v[52:55], a[0:3]\n" \
"s_add_u32 m0, s30, 0x1c00\n" \
"s_mov_b32 s85, 0xfd00\n" \
"v_mfma_f32_16x16x32_bf16 a[0:3], v[148:151], v[116:119], a[0:3]\n" \
"buffer_load_dwordx4 v25, s[76:79], s85 offen lds\n" \
"v_mfma_f32_16x16x32_bf16 a[4:7], v[88:91], v[52:55], a[4:7]\n" \
"ds_read_b64_tr_b16 v[100:101], v36 offset:32768\n" \
"v_mov_b32_e32 v11, 0\n" \
"v_mfma_f32_16x16x32_bf16 a[4:7], v[152:155], v[116:119], a[4:7]\n" \
"s_add_u32 m0, s31, 0xc00\n" \
"s_mov_b32 s84, 0x118000\n" \
"v_mfma_f32_16x16x32_bf16 a[36:39], v[96:99], v[52:55], a[36:39]\n" \
"buffer_load_dwordx4 v27, s[68:71], s84 offen lds\n" \
"s_waitcnt lgkmcnt(15)\n" \
"v_mfma_f32_16x16x32_bf16 a[36:39], v[160:163], v[116:119], a[36:39]\n" \
"ds_read_b64_tr_b16 v[102:103], v36 offset:33792\n" \
"s_and_b32 s6, s2, 7\n" \
"s_waitcnt lgkmcnt(12)\n" \
"v_mfma_f32_16x16x32_bf16 a[32:35], v[52:55], v[92:95], a[32:35]\n" \
"ds_read_b64_tr_b16 v[164:165], v36 offset:40960\n" \
"s_lshl_b32 s7, s3, 1\n" \
"s_waitcnt lgkmcnt(9)\n" \
"v_mfma_f32_16x16x32_bf16 a[32:35], v[116:119], v[156:159], a[32:35]\n" \
"ds_read_b64_tr_b16 v[166:167], v36 offset:41984\n" \
"s_lshr_b32 s4, s2, 5\n" \
"v_mfma_f32_16x16x32_bf16 a[96:99], v[56:59], v[92:95], a[96:99]\n" \
"ds_read_b64_tr_b16 v[104:105], v37 offset:32768\n" \
"s_lshl_b32 s5, s6, 2\n" \
"v_mfma_f32_16x16x32_bf16 a[96:99], v[120:123], v[156:159], a[96:99]\n" \
"ds_read_b64_tr_b16 v[106:107], v37 offset:33792\n" \
"s_bfe_u32 s8, s2, 0x20003\n" \
"v_mfma_f32_16x16x32_bf16 a[100:103], v[96:99], v[56:59], a[100:103]\n" \
"ds_read_b64_tr_b16 v[168:169], v37 offset:40960\n" \
"s_and_b32 s7, s7, 4\n" \
"v_mfma_f32_16x16x32_bf16 a[100:103], v[160:163], v[120:123], a[100:103]\n" \
"s_add_u32 s89, s41, 0x0\n" \
".fill (L_pb + 24288 - .) / 4, 4, 0xBF800000\n" \
"L_pp6:\n" \
"s_cbranch_execz L_pret\n" \
"buffer_store_dwordx4 v[184:187], v20, s[80:83], s89 offen offset:3072 sc1\n" \
"v_mfma_f32_16x16x32_bf16 a[68:71], v[88:91], v[56:59], a[68:71]\n" \
"ds_read_b64_tr_b16 v[170:171], v37 offset:41984\n" \
"s_and_b32 s9, s3, 1\n" \
"v_mfma_f32_16x16x32_bf16 a[68:71], v[152:155], v[120:123], a[68:71]\n" \
"ds_read_b64_tr_b16 v[108:109], v38 offset:32768\n" \
"s_or_b32 s13, s5, s8\n" \
"v_mfma_f32_16x16x32_bf16 a[64:67], v[84:87], v[56:59], a[64:67]\n" \
"ds_read_b64_tr_b16 v[110:111], v38 offset:33792\n" \
"s_or_b32 s9, s7, s9\n" \
"v_mfma_f32_16x16x32_bf16 a[64:67], v[148:151], v[120:123], a[64:67]\n" \
"s_mul_i32 s7, s4, 0xa00\n" \
"s_add_i32 s7, s7, 0x5bc1000\n" \
"v_mfma_f32_16x16x32_bf16 a[128:131], v[84:87], v[60:63], a[128:131]\n" \
"s_add_u32 s88, s41, 0x0\n" \
"buffer_store_dwordx4 v[188:191], v20, s[80:83], s88 offen offset:2048 sc1\n" \
"v_mfma_f32_16x16x32_bf16 a[128:131], v[148:151], v[124:127], a[128:131]\n" \
"ds_read_b64_tr_b16 v[172:173], v38 offset:40960\n" \
"s_lshl_b32 s10, s13, 2\n" \
"v_mfma_f32_16x16x32_bf16 a[132:135], v[88:91], v[60:63], a[132:135]\n" \
"ds_read_b64_tr_b16 v[174:175], v38 offset:41984\n" \
"s_or_b32 s10, s10, s7\n" \
"v_mfma_f32_16x16x32_bf16 a[132:135], v[152:155], v[124:127], a[132:135]\n" \
"ds_read_b64_tr_b16 v[112:113], v39 offset:32768\n" \
"v_writelane_b32 v11, s10, 0\n" \
"v_mfma_f32_16x16x32_bf16 a[164:167], v[96:99], v[60:63], a[164:167]\n" \
"s_lshl_b32 s14, s4, 3\n" \
"v_writelane_b32 v11, s7, 1\n" \
"v_mfma_f32_16x16x32_bf16 a[164:167], v[160:163], v[124:127], a[164:167]\n" \
"ds_read_b64_tr_b16 v[114:115], v39 offset:33792\n" \
"s_or_b32 s7, s14, s6\n" \
"v_mfma_f32_16x16x32_bf16 a[160:163], v[60:63], v[92:95], a[160:163]\n" \
"s_add_u32 s89, s41, 0x1000\n" \
"buffer_store_dwordx4 v[192:195], v20, s[80:83], s89 offen offset:2048 sc1\n" \
"v_mfma_f32_16x16x32_bf16 a[160:163], v[124:127], v[156:159], a[160:163]\n" \
"ds_read_b64_tr_b16 v[176:177], v39 offset:40960\n" \
"s_lshl_b32 s7, s7, 10\n" \
"s_waitcnt lgkmcnt(15)\n" \
"v_mfma_f32_16x16x32_bf16 a[224:227], v[64:67], v[92:95], a[224:227]\n" \
"ds_read_b64_tr_b16 v[178:179], v39 offset:41984\n" \
"s_lshl_b32 s10, s3, 8\n" \
"v_mfma_f32_16x16x32_bf16 a[224:227], v[128:131], v[156:159], a[224:227]\n" \
"s_or_b32 s7, s7, s10\n" \
"s_or_b32 s10, s7, 0x5c00200\n" \
"v_mfma_f32_16x16x32_bf16 a[228:231], v[96:99], v[64:67], a[228:231]\n" \
"v_writelane_b32 v11, s10, 2\n" \
"s_lshl_b32 s10, s8, 2\n" \
"v_mfma_f32_16x16x32_bf16 a[228:231], v[160:163], v[128:131], a[228:231]\n" \
"s_add_u32 s88, s41, 0x1000\n" \
"buffer_store_dwordx4 v[196:199], v20, s[80:83], s88 offen offset:3072 sc1\n" \
"v_mfma_f32_16x16x32_bf16 a[196:199], v[88:91], v[64:67], a[196:199]\n" \
"s_or_b32 s11, s10, s7\n" \
"s_or_b32 s11, s11, 0x5c00000\n" \
"v_mfma_f32_16x16x32_bf16 a[196:199], v[152:155], v[128:131], a[196:199]\n" \
"v_writelane_b32 v11, s11, 3\n" \
"s_or_b32 s7, s7, 0x5c00080\n" \
"v_mfma_f32_16x16x32_bf16 a[192:195], v[84:87], v[64:67], a[192:195]\n" \
"v_writelane_b32 v11, s7, 4\n" \
"s_or_b32 s7, s7, s10\n" \
"v_mfma_f32_16x16x32_bf16 a[192:195], v[148:151], v[128:131], a[192:195]\n" \
"s_add_i32 s10, s3, s2\n" \
"v_writelane_b32 v11, s7, 5\n" \
"v_mfma_f32_16x16x32_bf16 a[8:11], v[88:91], v[248:251], a[8:11]\n" \
"s_mov_b32 s7, 2.0\n" \
"s_mov_b32 s11, 0x5bc0000\n" \
"v_mfma_f32_16x16x32_bf16 a[8:11], v[152:155], v[48:51], a[8:11]\n" \
"s_cmp_lg_u32 s10, 0\n" \
"s_cselect_b32 s10, s7, s11\n" \
"v_mfma_f32_16x16x32_bf16 a[40:43], v[96:99], v[248:251], a[40:43]\n" \
"s_lshr_b32 s15, s2, 3\n" \
"v_writelane_b32 v11, s10, 53\n" \
"v_mfma_f32_16x16x32_bf16 a[40:43], v[160:163], v[48:51], a[40:43]\n" \
"s_lshl_b32 s10, s6, 5\n" \
"s_min_u32 s16, s3, 2\n" \
"v_mfma_f32_16x16x32_bf16 a[12:15], v[248:251], v[92:95], a[12:15]\n" \
"s_mul_i32 s12, s8, 0x120\n" \
"v_mfma_f32_16x16x32_bf16 a[12:15], v[48:51], v[156:159], a[12:15]\n" \
"s_waitcnt vmcnt(4) lgkmcnt(0)\n" \
"s_barrier\n" \
"s_nop 0\n" \
"v_mfma_f32_16x16x32_bf16 a[16:19], v[100:103], v[52:55], a[16:19]\n" \
"s_add_u32 m0, s40, 0x8000\n" \
"s_add_u32 s85, s39, 0x6000\n" \
"v_mfma_f32_16x16x32_bf16 a[16:19], v[164:167], v[116:119], a[16:19]\n" \
"buffer_load_dwordx4 v20, s[80:83], s85 offen sc1 lds\n" \
"v_mfma_f32_16x16x32_bf16 a[20:23], v[104:107], v[52:55], a[20:23]\n" \
"ds_read_b64_tr_b16 v[84:85], v32 offset:0\n" \
"v_mfma_f32_16x16x32_bf16 a[20:23], v[168:171], v[116:119], a[20:23]\n" \
"ds_read_b64_tr_b16 v[86:87], v32 offset:512\n" \
"v_mfma_f32_16x16x32_bf16 a[24:27], v[108:111], v[52:55], a[24:27]\n" \
"s_add_u32 m0, s40, 0x9000\n" \
"s_add_u32 s84, s39, 0x7000\n" \
"v_mfma_f32_16x16x32_bf16 a[24:27], v[172:175], v[116:119], a[24:27]\n" \
"buffer_load_dwordx4 v20, s[80:83], s84 offen sc1 lds\n" \
"v_mfma_f32_16x16x32_bf16 a[28:31], v[112:115], v[52:55], a[28:31]\n" \
"ds_read_b64_tr_b16 v[148:149], v32 offset:4096\n" \
"v_mfma_f32_16x16x32_bf16 a[28:31], v[176:179], v[116:119], a[28:31]\n" \
"ds_read_b128 v[52:55], v28 offset:0\n" \
"v_mfma_f32_16x16x32_bf16 a[60:63], v[112:115], v[56:59], a[60:63]\n" \
"ds_read_b128 v[116:119], v29 offset:0\n" \
"v_mfma_f32_16x16x32_bf16 a[60:63], v[176:179], v[120:123], a[60:63]\n" \
"s_add_u32 m0, s40, 0xb000\n" \
"s_mov_b32 s85, 0x180\n" \
"v_mfma_f32_16x16x32_bf16 a[56:59], v[108:111], v[56:59], a[56:59]\n" \
"buffer_load_dwordx4 v21, s[76:79], s85 offen lds\n" \
"v_mfma_f32_16x16x32_bf16 a[56:59], v[172:175], v[120:123], a[56:59]\n" \
"ds_read_b64_tr_b16 v[150:151], v32 offset:4608\n" \
"v_mfma_f32_16x16x32_bf16 a[52:55], v[104:107], v[56:59], a[52:55]\n" \
"s_add_u32 m0, s30, 0x8000\n" \
"s_mov_b32 s84, 0x180\n" \
"v_mfma_f32_16x16x32_bf16 a[52:55], v[168:171], v[120:123], a[52:55]\n" \
"buffer_load_dwordx4 v24, s[76:79], s84 offen lds\n" \
"v_mfma_f32_16x16x32_bf16 a[48:51], v[100:103], v[56:59], a[48:51]\n" \
"ds_read_b64_tr_b16 v[88:89], v33 offset:0\n" \
"v_mfma_f32_16x16x32_bf16 a[48:51], v[164:167], v[120:123], a[48:51]\n" \
"ds_read_b128 v[56:59], v28 offset:2048\n" \
"v_mfma_f32_16x16x32_bf16 a[80:83], v[100:103], v[60:63], a[80:83]\n" \
"ds_read_b128 v[120:123], v29 offset:2048\n" \
"v_mfma_f32_16x16x32_bf16 a[80:83], v[164:167], v[124:127], a[80:83]\n" \
"s_add_u32 m0, s30, 0x8400\n" \
"s_mov_b32 s85, 0x2580\n" \
"v_mfma_f32_16x16x32_bf16 a[84:87], v[104:107], v[60:63], a[84:87]\n" \
"buffer_load_dwordx4 v25, s[76:79], s85 offen lds\n" \
"v_mfma_f32_16x16x32_bf16 a[84:87], v[168:171], v[124:127], a[84:87]\n" \
"ds_read_b64_tr_b16 v[90:91], v33 offset:512\n" \
"v_mfma_f32_16x16x32_bf16 a[88:91], v[108:111], v[60:63], a[88:91]\n" \
"ds_read_b64_tr_b16 v[152:153], v33 offset:4096\n" \
"v_mfma_f32_16x16x32_bf16 a[88:91], v[172:175], v[124:127], a[88:91]\n" \
"s_add_u32 m0, s31, 0x8000\n" \
"s_mov_b32 s84, 0x180000\n" \
"v_mfma_f32_16x16x32_bf16 a[92:95], v[112:115], v[60:63], a[92:95]\n" \
"buffer_load_dwordx4 v26, s[68:71], s84 offen lds\n" \
"v_mfma_f32_16x16x32_bf16 a[92:95], v[176:179], v[124:127], a[92:95]\n" \
"ds_read_b128 v[60:63], v28 offset:4096\n" \
"v_mfma_f32_16x16x32_bf16 a[124:127], v[112:115], v[64:67], a[124:127]\n" \
"ds_read_b128 v[124:127], v29 offset:4096\n" \
"v_mfma_f32_16x16x32_bf16 a[124:127], v[176:179], v[128:131], a[124:127]\n" \
"s_add_u32 m0, s30, 0x8800\n" \
"s_mov_b32 s85, 0x4980\n" \
"v_mfma_f32_16x16x32_bf16 a[120:123], v[108:111], v[64:67], a[120:123]\n" \
"buffer_load_dwordx4 v24, s[76:79], s85 offen lds\n" \
"v_mfma_f32_16x16x32_bf16 a[120:123], v[172:175], v[128:131], a[120:123]\n" \
"ds_read_b64_tr_b16 v[154:155], v33 offset:4608\n" \
"v_mfma_f32_16x16x32_bf16 a[116:119], v[104:107], v[64:67], a[116:119]\n" \
"ds_read_b64_tr_b16 v[96:97], v35 offset:0\n" \
"v_mfma_f32_16x16x32_bf16 a[116:119], v[168:171], v[128:131], a[116:119]\n" \
"ds_read_b64_tr_b16 v[98:99], v35 offset:512\n" \
"v_mfma_f32_16x16x32_bf16 a[112:115], v[100:103], v[64:67], a[112:115]\n" \
"ds_read_b64_tr_b16 v[160:161], v35 offset:4096\n" \
"v_mfma_f32_16x16x32_bf16 a[112:115], v[164:167], v[128:131], a[112:115]\n" \
"ds_read_b128 v[64:67], v28 offset:6144\n" \
"v_mfma_f32_16x16x32_bf16 a[144:147], v[100:103], v[68:71], a[144:147]\n" \
"ds_read_b128 v[128:131], v29 offset:6144\n" \
"v_mfma_f32_16x16x32_bf16 a[144:147], v[164:167], v[132:135], a[144:147]\n" \
"s_add_u32 m0, s30, 0x8c00\n" \
"s_mov_b32 s84, 0x6d80\n" \
"v_mfma_f32_16x16x32_bf16 a[148:151], v[104:107], v[68:71], a[148:151]\n" \
"buffer_load_dwordx4 v25, s[76:79], s84 offen lds\n" \
"v_mfma_f32_16x16x32_bf16 a[148:151], v[168:171], v[132:135], a[148:151]\n" \
"s_add_u32 m0, s31, 0x8400\n" \
"s_mov_b32 s85, 0x188000\n" \
"v_mfma_f32_16x16x32_bf16 a[152:155], v[108:111], v[68:71], a[152:155]\n" \
"buffer_load_dwordx4 v26, s[68:71], s85 offen lds\n" \
"v_mfma_f32_16x16x32_bf16 a[152:155], v[172:175], v[132:135], a[152:155]\n" \
"ds_read_b64_tr_b16 v[162:163], v35 offset:4608\n" \
"v_mfma_f32_16x16x32_bf16 a[156:159], v[112:115], v[68:71], a[156:159]\n" \
"ds_read_b64_tr_b16 v[92:93], v34 offset:0\n" \
"v_mfma_f32_16x16x32_bf16 a[156:159], v[176:179], v[132:135], a[156:159]\n" \
"ds_read_b128 v[68:71], v28 offset:8192\n" \
"v_mfma_f32_16x16x32_bf16 a[108:111], v[112:115], v[72:75], a[108:111]\n" \
"ds_read_b128 v[132:135], v29 offset:8192\n" \
"v_mfma_f32_16x16x32_bf16 a[108:111], v[176:179], v[136:139], a[108:111]\n" \
"s_add_u32 m0, s30, 0x9000\n" \
"s_mov_b32 s84, 0x9180\n" \
"v_mfma_f32_16x16x32_bf16 a[104:107], v[108:111], v[72:75], a[104:107]\n" \
"buffer_load_dwordx4 v24, s[76:79], s84 offen lds\n" \
"v_mfma_f32_16x16x32_bf16 a[104:107], v[172:175], v[136:139], a[104:107]\n" \
"s_add_u32 m0, s30, 0x9400\n" \
"s_mov_b32 s85, 0xb580\n" \
"v_mfma_f32_16x16x32_bf16 a[76:79], v[104:107], v[72:75], a[76:79]\n" \
"buffer_load_dwordx4 v25, s[76:79], s85 offen lds\n" \
"v_mfma_f32_16x16x32_bf16 a[76:79], v[168:171], v[136:139], a[76:79]\n" \
"ds_read_b64_tr_b16 v[94:95], v34 offset:512\n" \
"v_mfma_f32_16x16x32_bf16 a[72:75], v[100:103], v[72:75], a[72:75]\n" \
"ds_read_b64_tr_b16 v[156:157], v34 offset:4096\n" \
"v_mfma_f32_16x16x32_bf16 a[72:75], v[164:167], v[136:139], a[72:75]\n" \
"ds_read_b128 v[72:75], v28 offset:10240\n" \
"v_mfma_f32_16x16x32_bf16 a[136:139], v[100:103], v[76:79], a[136:139]\n" \
"ds_read_b128 v[136:139], v29 offset:10240\n" \
"v_mfma_f32_16x16x32_bf16 a[136:139], v[164:167], v[140:143], a[136:139]\n" \
"s_add_u32 m0, s31, 0x8800\n" \
"s_mov_b32 s84, 0x190000\n" \
"v_mfma_f32_16x16x32_bf16 a[140:143], v[104:107], v[76:79], a[140:143]\n" \
"buffer_load_dwordx4 v27, s[68:71], s84 offen lds\n" \
"v_mfma_f32_16x16x32_bf16 a[140:143], v[168:171], v[140:143], a[140:143]\n" \
"s_add_u32 m0, s30, 0x9800\n" \
"s_mov_b32 s85, 0xd980\n" \
"v_mfma_f32_16x16x32_bf16 a[168:171], v[108:111], v[76:79], a[168:171]\n" \
"buffer_load_dwordx4 v24, s[76:79], s85 offen lds\n" \
"v_mfma_f32_16x16x32_bf16 a[168:171], v[172:175], v[140:143], a[168:171]\n" \
"ds_read_b64_tr_b16 v[158:159], v34 offset:4608\n" \
"v_mfma_f32_16x16x32_bf16 a[172:175], v[112:115], v[76:79], a[172:175]\n" \
"ds_read_b128 v[248:251], v22 offset:0\n" \
"v_mfma_f32_16x16x32_bf16 a[172:175], v[176:179], v[140:143], a[172:175]\n" \
"ds_read_b128 v[76:79], v28 offset:12288\n" \
"v_mfma_f32_16x16x32_bf16 a[236:239], v[112:115], v[80:83], a[236:239]\n" \
"ds_read_b128 v[140:143], v29 offset:12288\n" \
"v_mfma_f32_16x16x32_bf16 a[236:239], v[176:179], v[144:147], a[236:239]\n" \
"ds_read_b128 v[48:51], v23 offset:0\n" \
"v_mfma_f32_16x16x32_bf16 a[232:235], v[108:111], v[80:83], a[232:235]\n" \
"s_add_u32 s11, s22, s12\n" \
"s_addc_u32 s17, s23, 0\n" \
"v_mfma_f32_16x16x32_bf16 a[232:235], v[172:175], v[144:147], a[232:235]\n" \
"s_add_u32 s18, s24, s12\n" \
"v_writelane_b32 v11, s11, 9\n" \
"s_addc_u32 s19, s25, 0\n" \
"v_mfma_f32_16x16x32_bf16 a[204:207], v[104:107], v[80:83], a[204:207]\n" \
"s_and_b32 s20, s17, 0xffff\n" \
"v_writelane_b32 v11, s20, 10\n" \
"v_mfma_f32_16x16x32_bf16 a[204:207], v[168:171], v[144:147], a[204:207]\n" \
"s_and_b32 s20, s19, 0xffff\n" \
"v_writelane_b32 v11, s18, 11\n" \
"v_mfma_f32_16x16x32_bf16 a[200:203], v[100:103], v[80:83], a[200:203]\n" \
"s_cmpk_lt_u32 s3, 3\n" \
"s_cselect_b32 s11, s11, s18\n" \
"v_mfma_f32_16x16x32_bf16 a[200:203], v[164:167], v[144:147], a[200:203]\n" \
"ds_read_b128 v[80:83], v28 offset:14336\n" \
"ds_read_b128 v[144:147], v29 offset:14336\n" \
"s_waitcnt lgkmcnt(15)\n" \
"v_mfma_f32_16x16x32_bf16 a[0:3], v[84:87], v[52:55], a[0:3]\n" \
"s_add_u32 m0, s30, 0x9c00\n" \
"s_mov_b32 s85, 0xfd80\n" \
"v_mfma_f32_16x16x32_bf16 a[0:3], v[148:151], v[116:119], a[0:3]\n" \
"buffer_load_dwordx4 v25, s[76:79], s85 offen lds\n" \
"v_mfma_f32_16x16x32_bf16 a[4:7], v[88:91], v[52:55], a[4:7]\n" \
"ds_read_b64_tr_b16 v[100:101], v36 offset:0\n" \
"s_movk_i32 s18, 0xfff\n" \
"v_mfma_f32_16x16x32_bf16 a[4:7], v[152:155], v[116:119], a[4:7]\n" \
"s_add_u32 m0, s31, 0x8c00\n" \
"s_mov_b32 s84, 0x198000\n" \
"v_mfma_f32_16x16x32_bf16 a[36:39], v[96:99], v[52:55], a[36:39]\n" \
"buffer_load_dwordx4 v27, s[68:71], s84 offen lds\n" \
"s_waitcnt lgkmcnt(15)\n" \
"v_mfma_f32_16x16x32_bf16 a[36:39], v[160:163], v[116:119], a[36:39]\n" \
"ds_read_b64_tr_b16 v[102:103], v36 offset:1024\n" \
"v_writelane_b32 v11, s20, 12\n" \
"s_waitcnt lgkmcnt(12)\n" \
"v_mfma_f32_16x16x32_bf16 a[32:35], v[52:55], v[92:95], a[32:35]\n" \
"ds_read_b64_tr_b16 v[164:165], v36 offset:8192\n" \
"s_and_b32 s20, s29, 0xffff\n" \
"s_waitcnt lgkmcnt(9)\n" \
"s_nop 0\n" \
"v_mfma_f32_16x16x32_bf16 a[32:35], v[116:119], v[156:159], a[32:35]\n" \
"ds_read_b64_tr_b16 v[166:167], v36 offset:9216\n" \
"v_writelane_b32 v11, s28, 13\n" \
"v_mfma_f32_16x16x32_bf16 a[96:99], v[56:59], v[92:95], a[96:99]\n" \
"ds_read_b64_tr_b16 v[104:105], v37 offset:0\n" \
"v_writelane_b32 v11, s20, 14\n" \
"v_mfma_f32_16x16x32_bf16 a[96:99], v[120:123], v[156:159], a[96:99]\n" \
"ds_read_b64_tr_b16 v[106:107], v37 offset:1024\n" \
"v_writelane_b32 v11, s11, 15\n" \
"v_mfma_f32_16x16x32_bf16 a[100:103], v[96:99], v[56:59], a[100:103]\n" \
"ds_read_b64_tr_b16 v[168:169], v37 offset:8192\n" \
"v_mfma_f32_16x16x32_bf16 a[100:103], v[160:163], v[120:123], a[100:103]\n" \
"s_add_u32 s89, s41, 0x2000\n" \
"buffer_store_dwordx4 a[44:47], v20, s[80:83], s89 offen offset:3072 sc1\n" \
"v_mfma_f32_16x16x32_bf16 a[68:71], v[88:91], v[56:59], a[68:71]\n" \
"ds_read_b64_tr_b16 v[170:171], v37 offset:9216\n" \
"v_mfma_f32_16x16x32_bf16 a[68:71], v[152:155], v[120:123], a[68:71]\n" \
"ds_read_b64_tr_b16 v[108:109], v38 offset:0\n" \
"v_mfma_f32_16x16x32_bf16 a[64:67], v[84:87], v[56:59], a[64:67]\n" \
"ds_read_b64_tr_b16 v[110:111], v38 offset:1024\n" \
"v_mfma_f32_16x16x32_bf16 a[64:67], v[148:151], v[120:123], a[64:67]\n" \
"s_cmpk_lt_u32 s3, 3\n" \
"s_cselect_b32 s11, s17, s19\n" \
"v_mfma_f32_16x16x32_bf16 a[128:131], v[84:87], v[60:63], a[128:131]\n" \
"s_add_u32 s88, s41, 0x2000\n" \
"buffer_store_dwordx4 v[252:255], v20, s[80:83], s88 offen offset:2048 sc1\n" \
"v_mfma_f32_16x16x32_bf16 a[128:131], v[148:151], v[124:127], a[128:131]\n" \
"ds_read_b64_tr_b16 v[172:173], v38 offset:8192\n" \
"s_and_b32 s11, s11, 0xffff\n" \
"v_mfma_f32_16x16x32_bf16 a[132:135], v[88:91], v[60:63], a[132:135]\n" \
"ds_read_b64_tr_b16 v[174:175], v38 offset:9216\n" \
"v_writelane_b32 v11, s11, 16\n" \
"v_mfma_f32_16x16x32_bf16 a[132:135], v[152:155], v[124:127], a[132:135]\n" \
"ds_read_b64_tr_b16 v[112:113], v39 offset:0\n" \
"s_mul_i32 s11, s6, 0x48\n" \
"v_mfma_f32_16x16x32_bf16 a[164:167], v[96:99], v[60:63], a[164:167]\n" \
"s_add_i32 s17, s11, 0x47\n" \
"v_mfma_f32_16x16x32_bf16 a[164:167], v[160:163], v[124:127], a[164:167]\n" \
"ds_read_b64_tr_b16 v[114:115], v39 offset:1024\n" \
"v_mfma_f32_16x16x32_bf16 a[160:163], v[60:63], v[92:95], a[160:163]\n" \
"s_add_u32 s89, s41, 0x3000\n" \
"buffer_store_dwordx4 v[200:203], v20, s[80:83], s89 offen offset:2048 sc1\n" \
"v_mfma_f32_16x16x32_bf16 a[160:163], v[124:127], v[156:159], a[160:163]\n" \
"ds_read_b64_tr_b16 v[176:177], v39 offset:8192\n" \
"v_mfma_f32_16x16x32_bf16 a[224:227], v[64:67], v[92:95], a[224:227]\n" \
"ds_read_b64_tr_b16 v[178:179], v39 offset:9216\n" \
"v_mfma_f32_16x16x32_bf16 a[224:227], v[128:131], v[156:159], a[224:227]\n" \
"s_cmpk_lt_u32 s3, 3\n" \
"s_cselect_b32 s17, s17, s18\n" \
"v_mfma_f32_16x16x32_bf16 a[228:231], v[96:99], v[64:67], a[228:231]\n" \
"s_mulk_i32 s17, 0x480\n" \
"s_addk_i32 s17, 0x120\n" \
"v_mfma_f32_16x16x32_bf16 a[228:231], v[160:163], v[128:131], a[228:231]\n" \
"s_add_u32 s88, s41, 0x3000\n" \
"buffer_store_dwordx4 v[204:207], v20, s[80:83], s88 offen offset:3072 sc1\n" \
"v_mfma_f32_16x16x32_bf16 a[196:199], v[88:91], v[64:67], a[196:199]\n" \
"v_writelane_b32 v11, s17, 17\n" \
"s_lshl_b32 s17, s3, 5\n" \
"v_mfma_f32_16x16x32_bf16 a[196:199], v[152:155], v[128:131], a[196:199]\n" \
"s_add_i32 s11, s11, s17\n" \
"s_mul_i32 s17, s11, 0x480\n" \
"v_mfma_f32_16x16x32_bf16 a[192:195], v[84:87], v[64:67], a[192:195]\n" \
"s_mul_i32 s11, s13, 0x24000\n" \
"s_add_i32 s18, s11, 0x1b000\n" \
"v_mfma_f32_16x16x32_bf16 a[192:195], v[148:151], v[128:131], a[192:195]\n" \
"s_cmpk_lt_u32 s3, 3\n" \
"s_cselect_b32 s17, s17, s18\n" \
"s_waitcnt lgkmcnt(15)\n" \
"v_mfma_f32_16x16x32_bf16 a[8:11], v[88:91], v[248:251], a[8:11]\n" \
"s_mul_i32 s19, s3, 0x9000\n" \
"v_writelane_b32 v11, s17, 18\n" \
"v_mfma_f32_16x16x32_bf16 a[8:11], v[152:155], v[48:51], a[8:11]\n" \
"s_mul_i32 s17, s16, 0x2400\n" \
"s_mul_i32 s16, s16, 0x9000\n" \
"v_mfma_f32_16x16x32_bf16 a[40:43], v[96:99], v[248:251], a[40:43]\n" \
"s_bitset1_b32 s17, 17\n" \
"v_writelane_b32 v11, s17, 19\n" \
"v_mfma_f32_16x16x32_bf16 a[40:43], v[160:163], v[48:51], a[40:43]\n" \
"s_add_i32 s16, s11, s16\n" \
"v_writelane_b32 v11, s16, 25\n" \
"v_mfma_f32_16x16x32_bf16 a[12:15], v[248:251], v[92:95], a[12:15]\n" \
"s_mul_i32 s16, s3, 0x2400\n" \
"s_add_i32 s17, s16, 0x6000\n" \
"v_mfma_f32_16x16x32_bf16 a[12:15], v[48:51], v[156:159], a[12:15]\n" \
"s_waitcnt vmcnt(4) lgkmcnt(0)\n" \
"s_barrier\n" \
"v_mfma_f32_16x16x32_bf16 a[16:19], v[100:103], v[52:55], a[16:19]\n" \
"s_add_u32 m0, s40, 0x0\n" \
"s_add_u32 s85, s39, 0x8000\n" \
"v_mfma_f32_16x16x32_bf16 a[16:19], v[164:167], v[116:119], a[16:19]\n" \
"buffer_load_dwordx4 v20, s[80:83], s85 offen sc1 lds\n" \
"v_mfma_f32_16x16x32_bf16 a[20:23], v[104:107], v[52:55], a[20:23]\n" \
"ds_read_b64_tr_b16 v[84:85], v32 offset:32768\n" \
"v_writelane_b32 v11, s17, 20\n" \
"v_mfma_f32_16x16x32_bf16 a[20:23], v[168:171], v[116:119], a[20:23]\n" \
"ds_read_b64_tr_b16 v[86:87], v32 offset:33280\n" \
"s_or_b32 s17, s16, 0x10000\n" \
"v_mfma_f32_16x16x32_bf16 a[24:27], v[108:111], v[52:55], a[24:27]\n" \
"s_add_u32 m0, s40, 0x1000\n" \
"s_add_u32 s84, s39, 0x9000\n" \
"v_mfma_f32_16x16x32_bf16 a[24:27], v[172:175], v[116:119], a[24:27]\n" \
"buffer_load_dwordx4 v20, s[80:83], s84 offen sc1 lds\n" \
"v_mfma_f32_16x16x32_bf16 a[28:31], v[112:115], v[52:55], a[28:31]\n" \
"ds_read_b64_tr_b16 v[148:149], v32 offset:36864\n" \
"s_add_i32 s18, s16, 0x19000\n" \
"v_mfma_f32_16x16x32_bf16 a[28:31], v[176:179], v[116:119], a[28:31]\n" \
"ds_read_b128 v[52:55], v28 offset:32768\n" \
"v_writelane_b32 v11, s17, 21\n" \
"v_mfma_f32_16x16x32_bf16 a[60:63], v[112:115], v[56:59], a[60:63]\n" \
"ds_read_b128 v[116:119], v29 offset:32768\n" \
"s_mov_b32 s17, 0\n" \
"v_mfma_f32_16x16x32_bf16 a[60:63], v[176:179], v[120:123], a[60:63]\n" \
"s_add_u32 m0, s40, 0x3000\n" \
"s_mov_b32 s85, 0x200\n" \
"v_mfma_f32_16x16x32_bf16 a[56:59], v[108:111], v[56:59], a[56:59]\n" \
"buffer_load_dwordx4 v21, s[76:79], s85 offen lds\n" \
"v_mfma_f32_16x16x32_bf16 a[56:59], v[172:175], v[120:123], a[56:59]\n" \
"ds_read_b64_tr_b16 v[150:151], v32 offset:37376\n" \
"v_mfma_f32_16x16x32_bf16 a[52:55], v[104:107], v[56:59], a[52:55]\n" \
"s_add_u32 m0, s30, 0x0\n" \
"s_mov_b32 s84, 0x200\n" \
"v_mfma_f32_16x16x32_bf16 a[52:55], v[168:171], v[120:123], a[52:55]\n" \
"buffer_load_dwordx4 v24, s[76:79], s84 offen lds\n" \
"v_mfma_f32_16x16x32_bf16 a[48:51], v[100:103], v[56:59], a[48:51]\n" \
"ds_read_b64_tr_b16 v[88:89], v33 offset:32768\n" \
"v_mfma_f32_16x16x32_bf16 a[48:51], v[164:167], v[120:123], a[48:51]\n" \
"ds_read_b128 v[56:59], v28 offset:34816\n" \
"v_mfma_f32_16x16x32_bf16 a[80:83], v[100:103], v[60:63], a[80:83]\n" \
"ds_read_b128 v[120:123], v29 offset:34816\n" \
"v_mfma_f32_16x16x32_bf16 a[80:83], v[164:167], v[124:127], a[80:83]\n" \
"s_add_u32 m0, s30, 0x400\n" \
"s_mov_b32 s85, 0x2600\n" \
"v_mfma_f32_16x16x32_bf16 a[84:87], v[104:107], v[60:63], a[84:87]\n" \
"buffer_load_dwordx4 v25, s[76:79], s85 offen lds\n" \
"v_mfma_f32_16x16x32_bf16 a[84:87], v[168:171], v[124:127], a[84:87]\n" \
"ds_read_b64_tr_b16 v[90:91], v33 offset:33280\n" \
"v_mfma_f32_16x16x32_bf16 a[88:91], v[108:111], v[60:63], a[88:91]\n" \
"ds_read_b64_tr_b16 v[152:153], v33 offset:36864\n" \
"v_mfma_f32_16x16x32_bf16 a[88:91], v[172:175], v[124:127], a[88:91]\n" \
"s_add_u32 m0, s31, 0x0\n" \
"s_nop 0\n" \
"s_mov_b32 s84, 0x200000\n" \
"v_mfma_f32_16x16x32_bf16 a[92:95], v[112:115], v[60:63], a[92:95]\n" \
"buffer_load_dwordx4 v26, s[68:71], s84 offen lds\n" \
"v_mfma_f32_16x16x32_bf16 a[92:95], v[176:179], v[124:127], a[92:95]\n" \
"ds_read_b128 v[60:63], v28 offset:36864\n" \
"v_mfma_f32_16x16x32_bf16 a[124:127], v[112:115], v[64:67], a[124:127]\n" \
"ds_read_b128 v[124:127], v29 offset:36864\n" \
"v_mfma_f32_16x16x32_bf16 a[124:127], v[176:179], v[128:131], a[124:127]\n" \
"s_add_u32 m0, s30, 0x800\n" \
"s_mov_b32 s85, 0x4a00\n" \
"v_mfma_f32_16x16x32_bf16 a[120:123], v[108:111], v[64:67], a[120:123]\n" \
"buffer_load_dwordx4 v24, s[76:79], s85 offen lds\n" \
"v_mfma_f32_16x16x32_bf16 a[120:123], v[172:175], v[128:131], a[120:123]\n" \
"ds_read_b64_tr_b16 v[154:155], v33 offset:37376\n" \
"v_mfma_f32_16x16x32_bf16 a[116:119], v[104:107], v[64:67], a[116:119]\n" \
"ds_read_b64_tr_b16 v[96:97], v35 offset:32768\n" \
"v_mfma_f32_16x16x32_bf16 a[116:119], v[168:171], v[128:131], a[116:119]\n" \
"ds_read_b64_tr_b16 v[98:99], v35 offset:33280\n" \
"v_mfma_f32_16x16x32_bf16 a[112:115], v[100:103], v[64:67], a[112:115]\n" \
"ds_read_b64_tr_b16 v[160:161], v35 offset:36864\n" \
"v_mfma_f32_16x16x32_bf16 a[112:115], v[164:167], v[128:131], a[112:115]\n" \
"ds_read_b128 v[64:67], v28 offset:38912\n" \
"v_mfma_f32_16x16x32_bf16 a[144:147], v[100:103], v[68:71], a[144:147]\n" \
"ds_read_b128 v[128:131], v29 offset:38912\n" \
"v_mfma_f32_16x16x32_bf16 a[144:147], v[164:167], v[132:135], a[144:147]\n" \
"s_add_u32 m0, s30, 0xc00\n" \
"s_mov_b32 s84, 0x6e00\n" \
"v_mfma_f32_16x16x32_bf16 a[148:151], v[104:107], v[68:71], a[148:151]\n" \
"buffer_load_dwordx4 v25, s[76:79], s84 offen lds\n" \
"v_mfma_f32_16x16x32_bf16 a[148:151], v[168:171], v[132:135], a[148:151]\n" \
"s_add_u32 m0, s31, 0x400\n" \
"s_mov_b32 s85, 0x208000\n" \
"v_mfma_f32_16x16x32_bf16 a[152:155], v[108:111], v[68:71], a[152:155]\n" \
"buffer_load_dwordx4 v26, s[68:71], s85 offen lds\n" \
"v_mfma_f32_16x16x32_bf16 a[152:155], v[172:175], v[132:135], a[152:155]\n" \
"ds_read_b64_tr_b16 v[162:163], v35 offset:37376\n" \
"v_mfma_f32_16x16x32_bf16 a[156:159], v[112:115], v[68:71], a[156:159]\n" \
"ds_read_b64_tr_b16 v[92:93], v34 offset:32768\n" \
"v_mfma_f32_16x16x32_bf16 a[156:159], v[176:179], v[132:135], a[156:159]\n" \
"ds_read_b128 v[68:71], v28 offset:40960\n" \
"v_mfma_f32_16x16x32_bf16 a[108:111], v[112:115], v[72:75], a[108:111]\n" \
"ds_read_b128 v[132:135], v29 offset:40960\n" \
"v_mfma_f32_16x16x32_bf16 a[108:111], v[176:179], v[136:139], a[108:111]\n" \
"s_add_u32 m0, s30, 0x1000\n" \
"s_mov_b32 s84, 0x9200\n" \
"v_mfma_f32_16x16x32_bf16 a[104:107], v[108:111], v[72:75], a[104:107]\n" \
"buffer_load_dwordx4 v24, s[76:79], s84 offen lds\n" \
"v_mfma_f32_16x16x32_bf16 a[104:107], v[172:175], v[136:139], a[104:107]\n" \
"s_add_u32 m0, s30, 0x1400\n" \
"s_mov_b32 s85, 0xb600\n" \
"v_mfma_f32_16x16x32_bf16 a[76:79], v[104:107], v[72:75], a[76:79]\n" \
"buffer_load_dwordx4 v25, s[76:79], s85 offen lds\n" \
"v_mfma_f32_16x16x32_bf16 a[76:79], v[168:171], v[136:139], a[76:79]\n" \
"ds_read_b64_tr_b16 v[94:95], v34 offset:33280\n" \
"v_mfma_f32_16x16x32_bf16 a[72:75], v[100:103], v[72:75], a[72:75]\n" \
"ds_read_b64_tr_b16 v[156:157], v34 offset:36864\n" \
"v_mfma_f32_16x16x32_bf16 a[72:75], v[164:167], v[136:139], a[72:75]\n" \
"ds_read_b128 v[72:75], v28 offset:43008\n" \
"v_mfma_f32_16x16x32_bf16 a[136:139], v[100:103], v[76:79], a[136:139]\n" \
"ds_read_b128 v[136:139], v29 offset:43008\n" \
"v_mfma_f32_16x16x32_bf16 a[136:139], v[164:167], v[140:143], a[136:139]\n" \
"s_add_u32 m0, s31, 0x800\n" \
"s_mov_b32 s84, 0x210000\n" \
"v_mfma_f32_16x16x32_bf16 a[140:143], v[104:107], v[76:79], a[140:143]\n" \
"buffer_load_dwordx4 v27, s[68:71], s84 offen lds\n" \
"v_mfma_f32_16x16x32_bf16 a[140:143], v[168:171], v[140:143], a[140:143]\n" \
"s_add_u32 m0, s30, 0x1800\n" \
"s_mov_b32 s85, 0xda00\n" \
"v_mfma_f32_16x16x32_bf16 a[168:171], v[108:111], v[76:79], a[168:171]\n" \
"buffer_load_dwordx4 v24, s[76:79], s85 offen lds\n" \
"v_mfma_f32_16x16x32_bf16 a[168:171], v[172:175], v[140:143], a[168:171]\n" \
"ds_read_b64_tr_b16 v[158:159], v34 offset:37376\n" \
"v_mfma_f32_16x16x32_bf16 a[172:175], v[112:115], v[76:79], a[172:175]\n" \
"ds_read_b128 v[248:251], v22 offset:32768\n" \
"v_mfma_f32_16x16x32_bf16 a[172:175], v[176:179], v[140:143], a[172:175]\n" \
"ds_read_b128 v[76:79], v28 offset:45056\n" \
"v_mfma_f32_16x16x32_bf16 a[236:239], v[112:115], v[80:83], a[236:239]\n" \
"ds_read_b128 v[140:143], v29 offset:45056\n" \
"v_mfma_f32_16x16x32_bf16 a[236:239], v[176:179], v[144:147], a[236:239]\n" \
"ds_read_b128 v[48:51], v23 offset:32768\n" \
"v_mfma_f32_16x16x32_bf16 a[232:235], v[108:111], v[80:83], a[232:235]\n" \
"s_cmpk_lt_u32 s3, 3\n" \
"s_cselect_b32 s18, s18, s17\n" \
"v_mfma_f32_16x16x32_bf16 a[232:235], v[172:175], v[144:147], a[232:235]\n" \
"s_bitset1_b32 s16, 17\n" \
"v_writelane_b32 v11, s18, 22\n" \
"v_mfma_f32_16x16x32_bf16 a[204:207], v[104:107], v[80:83], a[204:207]\n" \
"s_movk_i32 s18, 0x2400\n" \
"v_mfma_f32_16x16x32_bf16 a[204:207], v[168:171], v[144:147], a[204:207]\n" \
"s_cmpk_lt_u32 s3, 3\n" \
"s_cselect_b32 s16, s16, s18\n" \
"v_mfma_f32_16x16x32_bf16 a[200:203], v[100:103], v[80:83], a[200:203]\n" \
"s_xor_b32 s13, s13, 2\n" \
"v_writelane_b32 v11, s16, 23\n" \
"v_mfma_f32_16x16x32_bf16 a[200:203], v[164:167], v[144:147], a[200:203]\n" \
"ds_read_b128 v[80:83], v28 offset:47104\n" \
"ds_read_b128 v[144:147], v29 offset:47104\n" \
"s_waitcnt lgkmcnt(15)\n" \
"v_mfma_f32_16x16x32_bf16 a[0:3], v[84:87], v[52:55], a[0:3]\n" \
"s_add_u32 m0, s30, 0x1c00\n" \
"s_mov_b32 s85, 0xfe00\n" \
"v_mfma_f32_16x16x32_bf16 a[0:3], v[148:151], v[116:119], a[0:3]\n" \
"buffer_load_dwordx4 v25, s[76:79], s85 offen lds\n" \
"v_mfma_f32_16x16x32_bf16 a[4:7], v[88:91], v[52:55], a[4:7]\n" \
"ds_read_b64_tr_b16 v[100:101], v36 offset:32768\n" \
"s_lshl_b32 s16, s3, 10\n" \
"v_mfma_f32_16x16x32_bf16 a[4:7], v[152:155], v[116:119], a[4:7]\n" \
"s_add_u32 m0, s31, 0xc00\n" \
"s_mov_b32 s84, 0x218000\n" \
"v_mfma_f32_16x16x32_bf16 a[36:39], v[96:99], v[52:55], a[36:39]\n" \
"buffer_load_dwordx4 v27, s[68:71], s84 offen lds\n" \
"s_waitcnt lgkmcnt(15)\n" \
"v_mfma_f32_16x16x32_bf16 a[36:39], v[160:163], v[116:119], a[36:39]\n" \
"ds_read_b64_tr_b16 v[102:103], v36 offset:33792\n" \
"s_or_b32 s16, s16, 0xf000\n" \
"s_waitcnt lgkmcnt(12)\n" \
"v_mfma_f32_16x16x32_bf16 a[32:35], v[52:55], v[92:95], a[32:35]\n" \
"ds_read_b64_tr_b16 v[164:165], v36 offset:40960\n" \
"v_writelane_b32 v11, s16, 24\n" \
"s_waitcnt lgkmcnt(9)\n" \
"v_mfma_f32_16x16x32_bf16 a[32:35], v[116:119], v[156:159], a[32:35]\n" \
"ds_read_b64_tr_b16 v[166:167], v36 offset:41984\n" \
"s_add_i32 s16, s15, 1\n" \
"v_mfma_f32_16x16x32_bf16 a[96:99], v[56:59], v[92:95], a[96:99]\n" \
"ds_read_b64_tr_b16 v[104:105], v37 offset:32768\n" \
"s_and_b32 s16, s16, 3\n" \
"v_mfma_f32_16x16x32_bf16 a[96:99], v[120:123], v[156:159], a[96:99]\n" \
"ds_read_b64_tr_b16 v[106:107], v37 offset:33792\n" \
"s_or_b32 s16, s16, s5\n" \
"v_mfma_f32_16x16x32_bf16 a[100:103], v[96:99], v[56:59], a[100:103]\n" \
"ds_read_b64_tr_b16 v[168:169], v37 offset:40960\n" \
"s_mul_i32 s18, s16, 0x24000\n" \
"v_mfma_f32_16x16x32_bf16 a[100:103], v[160:163], v[120:123], a[100:103]\n" \
"s_add_u32 s89, s42, 0x0\n" \
"buffer_store_dwordx4 v[216:219], v20, s[80:83], s89 offen offset:0 sc1\n" \
"v_mfma_f32_16x16x32_bf16 a[68:71], v[88:91], v[56:59], a[68:71]\n" \
"ds_read_b64_tr_b16 v[170:171], v37 offset:41984\n" \
"s_add_i32 s18, s18, s19\n" \
"v_mfma_f32_16x16x32_bf16 a[68:71], v[152:155], v[120:123], a[68:71]\n" \
"ds_read_b64_tr_b16 v[108:109], v38 offset:32768\n" \
"s_add_i32 s15, s15, -1\n" \
"v_mfma_f32_16x16x32_bf16 a[64:67], v[84:87], v[56:59], a[64:67]\n" \
"ds_read_b64_tr_b16 v[110:111], v38 offset:33792\n" \
"v_writelane_b32 v11, s18, 26\n" \
"v_mfma_f32_16x16x32_bf16 a[64:67], v[148:151], v[120:123], a[64:67]\n" \
"s_mul_i32 s18, s13, 0x24000\n" \
"s_and_b32 s15, s15, 3\n" \
"v_mfma_f32_16x16x32_bf16 a[128:131], v[84:87], v[60:63], a[128:131]\n" \
"s_add_u32 s88, s42, 0x0\n" \
"buffer_store_dwordx4 v[220:223], v20, s[80:83], s88 offen offset:1024 sc1\n" \
"v_mfma_f32_16x16x32_bf16 a[128:131], v[148:151], v[124:127], a[128:131]\n" \
"ds_read_b64_tr_b16 v[172:173], v38 offset:40960\n" \
"s_add_i32 s18, s18, s19\n" \
"v_mfma_f32_16x16x32_bf16 a[132:135], v[88:91], v[60:63], a[132:135]\n" \
"ds_read_b64_tr_b16 v[174:175], v38 offset:41984\n" \
"s_or_b32 s15, s15, s5\n" \
"v_mfma_f32_16x16x32_bf16 a[132:135], v[152:155], v[124:127], a[132:135]\n" \
"ds_read_b64_tr_b16 v[112:113], v39 offset:32768\n" \
".fill (L_pb + 28384 - .) / 4, 4, 0xBF800000\n" \
"L_pp7:\n" \
"s_cbranch_execz L_pret\n" \
"v_writelane_b32 v11, s18, 27\n" \
"v_mfma_f32_16x16x32_bf16 a[164:167], v[96:99], v[60:63], a[164:167]\n" \
"s_mul_i32 s18, s15, 0x24000\n" \
"s_add_i32 s18, s18, s19\n" \
"v_mfma_f32_16x16x32_bf16 a[164:167], v[160:163], v[124:127], a[164:167]\n" \
"ds_read_b64_tr_b16 v[114:115], v39 offset:33792\n" \
"v_writelane_b32 v11, s18, 28\n" \
"v_mfma_f32_16x16x32_bf16 a[160:163], v[60:63], v[92:95], a[160:163]\n" \
"s_add_u32 s89, s42, 0x1000\n" \
"buffer_store_dwordx4 v[224:227], v20, s[80:83], s89 offen offset:1024 sc1\n" \
"v_mfma_f32_16x16x32_bf16 a[160:163], v[124:127], v[156:159], a[160:163]\n" \
"ds_read_b64_tr_b16 v[176:177], v39 offset:40960\n" \
"s_or_b32 s18, s9, s14\n" \
"v_mfma_f32_16x16x32_bf16 a[224:227], v[64:67], v[92:95], a[224:227]\n" \
"ds_read_b64_tr_b16 v[178:179], v39 offset:41984\n" \
"s_mul_i32 s18, s18, 0x24800\n" \
"v_mfma_f32_16x16x32_bf16 a[224:227], v[128:131], v[156:159], a[224:227]\n" \
"s_lshl_b32 s16, s16, 12\n" \
"s_add_i32 s19, s18, 0x3004800\n" \
"v_mfma_f32_16x16x32_bf16 a[228:231], v[96:99], v[64:67], a[228:231]\n" \
"s_add_i32 s20, s19, s16\n" \
"v_writelane_b32 v11, s20, 29\n" \
"v_mfma_f32_16x16x32_bf16 a[228:231], v[160:163], v[128:131], a[228:231]\n" \
"s_add_u32 s88, s42, 0x1000\n" \
"buffer_store_dwordx4 v[228:231], v20, s[80:83], s88 offen offset:0 sc1\n" \
"v_mfma_f32_16x16x32_bf16 a[196:199], v[88:91], v[64:67], a[196:199]\n" \
"s_lshl_b32 s13, s13, 12\n" \
"s_lshl_b32 s15, s15, 12\n" \
"v_mfma_f32_16x16x32_bf16 a[196:199], v[152:155], v[128:131], a[196:199]\n" \
"s_sub_i32 s14, s9, s14\n" \
"s_add_i32 s20, s19, s13\n" \
"v_mfma_f32_16x16x32_bf16 a[192:195], v[84:87], v[64:67], a[192:195]\n" \
"v_writelane_b32 v11, s20, 30\n" \
"s_add_i32 s19, s19, s15\n" \
"v_mfma_f32_16x16x32_bf16 a[192:195], v[148:151], v[128:131], a[192:195]\n" \
"s_mul_i32 s14, s14, 0x24800\n" \
"v_writelane_b32 v11, s19, 31\n" \
"s_waitcnt lgkmcnt(15)\n" \
"v_mfma_f32_16x16x32_bf16 a[8:11], v[88:91], v[248:251], a[8:11]\n" \
"s_add_i32 s19, s14, 0x4120800\n" \
"s_add_i32 s16, s19, s16\n" \
"v_mfma_f32_16x16x32_bf16 a[8:11], v[152:155], v[48:51], a[8:11]\n" \
"v_writelane_b32 v11, s16, 32\n" \
"s_add_i32 s13, s19, s13\n" \
"v_mfma_f32_16x16x32_bf16 a[40:43], v[96:99], v[248:251], a[40:43]\n" \
"v_writelane_b32 v11, s13, 33\n" \
"s_add_i32 s19, s19, s15\n" \
"v_mfma_f32_16x16x32_bf16 a[40:43], v[160:163], v[48:51], a[40:43]\n" \
"s_or_b32 s13, s18, 0x3000000\n" \
"s_lshl_b32 s15, s6, 11\n" \
"v_mfma_f32_16x16x32_bf16 a[12:15], v[248:251], v[92:95], a[12:15]\n" \
"v_writelane_b32 v11, s19, 34\n" \
"s_add_i32 s16, s13, s15\n" \
"v_mfma_f32_16x16x32_bf16 a[12:15], v[48:51], v[156:159], a[12:15]\n" \
"s_waitcnt vmcnt(4) lgkmcnt(0)\n" \
"s_barrier\n" \
"v_mov_b32_e32 v45, s62\n" \
"s_mov_b64 exec, 1\n" \
"buffer_store_dword v45, off, s[80:83], s37 offset:0 sc1\n" \
"s_mov_b64 exec, -1\n" \
"s_nop 0\n" \
"v_mfma_f32_16x16x32_bf16 a[16:19], v[100:103], v[52:55], a[16:19]\n" \
"s_add_u32 m0, s40, 0x8000\n" \
"s_add_u32 s85, s39, 0xa000\n" \
"v_mfma_f32_16x16x32_bf16 a[16:19], v[164:167], v[116:119], a[16:19]\n" \
"buffer_load_dwordx4 v20, s[80:83], s85 offen sc1 lds\n" \
"v_mfma_f32_16x16x32_bf16 a[20:23], v[104:107], v[52:55], a[20:23]\n" \
"ds_read_b64_tr_b16 v[84:85], v32 offset:0\n" \
"v_accvgpr_read_b32 v204, a248\n" \
"v_mfma_f32_16x16x32_bf16 a[20:23], v[168:171], v[116:119], a[20:23]\n" \
"ds_read_b64_tr_b16 v[86:87], v32 offset:512\n" \
"v_accvgpr_read_b32 v205, a249\n" \
"v_mfma_f32_16x16x32_bf16 a[24:27], v[108:111], v[52:55], a[24:27]\n" \
"s_add_u32 m0, s40, 0x9000\n" \
"s_add_u32 s84, s39, 0xb000\n" \
"v_mfma_f32_16x16x32_bf16 a[24:27], v[172:175], v[116:119], a[24:27]\n" \
"buffer_load_dwordx4 v20, s[80:83], s84 offen sc1 lds\n" \
"v_mfma_f32_16x16x32_bf16 a[28:31], v[112:115], v[52:55], a[28:31]\n" \
"ds_read_b64_tr_b16 v[148:149], v32 offset:4096\n" \
"v_accvgpr_read_b32 v206, a250\n" \
"v_mfma_f32_16x16x32_bf16 a[28:31], v[176:179], v[116:119], a[28:31]\n" \
"ds_read_b128 v[52:55], v28 offset:0\n" \
"v_accvgpr_read_b32 v207, a251\n" \
"v_mfma_f32_16x16x32_bf16 a[60:63], v[112:115], v[56:59], a[60:63]\n" \
"ds_read_b128 v[116:119], v29 offset:0\n" \
"v_accvgpr_read_b32 v180, a252\n" \
"v_mfma_f32_16x16x32_bf16 a[60:63], v[176:179], v[120:123], a[60:63]\n" \
"s_add_u32 m0, s40, 0xb000\n" \
"s_mov_b32 s85, 0x280\n" \
"v_mfma_f32_16x16x32_bf16 a[56:59], v[108:111], v[56:59], a[56:59]\n" \
"buffer_load_dwordx4 v21, s[76:79], s85 offen lds\n" \
"v_mfma_f32_16x16x32_bf16 a[56:59], v[172:175], v[120:123], a[56:59]\n" \
"ds_read_b64_tr_b16 v[150:151], v32 offset:4608\n" \
"v_accvgpr_read_b32 v181, a253\n" \
"v_mfma_f32_16x16x32_bf16 a[52:55], v[104:107], v[56:59], a[52:55]\n" \
"s_add_u32 m0, s30, 0x8000\n" \
"s_mov_b32 s84, 0x280\n" \
"v_mfma_f32_16x16x32_bf16 a[52:55], v[168:171], v[120:123], a[52:55]\n" \
"buffer_load_dwordx4 v24, s[76:79], s84 offen lds\n" \
"v_mfma_f32_16x16x32_bf16 a[48:51], v[100:103], v[56:59], a[48:51]\n" \
"ds_read_b64_tr_b16 v[88:89], v33 offset:0\n" \
"v_accvgpr_read_b32 v182, a254\n" \
"v_mfma_f32_16x16x32_bf16 a[48:51], v[164:167], v[120:123], a[48:51]\n" \
"ds_read_b128 v[56:59], v28 offset:2048\n" \
"v_accvgpr_read_b32 v183, a255\n" \
"v_mfma_f32_16x16x32_bf16 a[80:83], v[100:103], v[60:63], a[80:83]\n" \
"ds_read_b128 v[120:123], v29 offset:2048\n" \
"v_cvt_pk_bf16_f32 v204, v204, v205\n" \
"v_mfma_f32_16x16x32_bf16 a[80:83], v[164:167], v[124:127], a[80:83]\n" \
"s_add_u32 m0, s30, 0x8400\n" \
"s_mov_b32 s85, 0x2680\n" \
"v_mfma_f32_16x16x32_bf16 a[84:87], v[104:107], v[60:63], a[84:87]\n" \
"buffer_load_dwordx4 v25, s[76:79], s85 offen lds\n" \
"v_mfma_f32_16x16x32_bf16 a[84:87], v[168:171], v[124:127], a[84:87]\n" \
"ds_read_b64_tr_b16 v[90:91], v33 offset:512\n" \
"v_cvt_pk_bf16_f32 v205, v206, v207\n" \
"v_mfma_f32_16x16x32_bf16 a[88:91], v[108:111], v[60:63], a[88:91]\n" \
"ds_read_b64_tr_b16 v[152:153], v33 offset:4096\n" \
"v_cvt_pk_bf16_f32 v206, v180, v181\n" \
"v_mfma_f32_16x16x32_bf16 a[88:91], v[172:175], v[124:127], a[88:91]\n" \
"s_add_u32 m0, s31, 0x8000\n" \
"s_mov_b32 s84, 0x280000\n" \
"v_mfma_f32_16x16x32_bf16 a[92:95], v[112:115], v[60:63], a[92:95]\n" \
"buffer_load_dwordx4 v26, s[68:71], s84 offen lds\n" \
"v_mfma_f32_16x16x32_bf16 a[92:95], v[176:179], v[124:127], a[92:95]\n" \
"ds_read_b128 v[60:63], v28 offset:4096\n" \
"v_cvt_pk_bf16_f32 v207, v182, v183\n" \
"v_mfma_f32_16x16x32_bf16 a[124:127], v[112:115], v[64:67], a[124:127]\n" \
"ds_read_b128 v[124:127], v29 offset:4096\n" \
"v_accvgpr_read_b32 v200, a240\n" \
"v_mfma_f32_16x16x32_bf16 a[124:127], v[176:179], v[128:131], a[124:127]\n" \
"s_add_u32 m0, s30, 0x8800\n" \
"s_mov_b32 s85, 0x4a80\n" \
"v_mfma_f32_16x16x32_bf16 a[120:123], v[108:111], v[64:67], a[120:123]\n" \
"buffer_load_dwordx4 v24, s[76:79], s85 offen lds\n" \
"v_mfma_f32_16x16x32_bf16 a[120:123], v[172:175], v[128:131], a[120:123]\n" \
"ds_read_b64_tr_b16 v[154:155], v33 offset:4608\n" \
"v_accvgpr_read_b32 v201, a241\n" \
"v_mfma_f32_16x16x32_bf16 a[116:119], v[104:107], v[64:67], a[116:119]\n" \
"ds_read_b64_tr_b16 v[96:97], v35 offset:0\n" \
"v_accvgpr_read_b32 v202, a242\n" \
"v_mfma_f32_16x16x32_bf16 a[116:119], v[168:171], v[128:131], a[116:119]\n" \
"ds_read_b64_tr_b16 v[98:99], v35 offset:512\n" \
"v_accvgpr_read_b32 v203, a243\n" \
"v_mfma_f32_16x16x32_bf16 a[112:115], v[100:103], v[64:67], a[112:115]\n" \
"ds_read_b64_tr_b16 v[160:161], v35 offset:4096\n" \
"v_accvgpr_read_b32 v180, a244\n" \
"v_mfma_f32_16x16x32_bf16 a[112:115], v[164:167], v[128:131], a[112:115]\n" \
"ds_read_b128 v[64:67], v28 offset:6144\n" \
"v_accvgpr_read_b32 v181, a245\n" \
"v_mfma_f32_16x16x32_bf16 a[144:147], v[100:103], v[68:71], a[144:147]\n" \
"ds_read_b128 v[128:131], v29 offset:6144\n" \
"v_accvgpr_read_b32 v182, a246\n" \
"v_mfma_f32_16x16x32_bf16 a[144:147], v[164:167], v[132:135], a[144:147]\n" \
"s_add_u32 m0, s30, 0x8c00\n" \
"s_mov_b32 s84, 0x6e80\n" \
"v_mfma_f32_16x16x32_bf16 a[148:151], v[104:107], v[68:71], a[148:151]\n" \
"buffer_load_dwordx4 v25, s[76:79], s84 offen lds\n" \
"v_mfma_f32_16x16x32_bf16 a[148:151], v[168:171], v[132:135], a[148:151]\n" \
"s_add_u32 m0, s31, 0x8400\n" \
"s_mov_b32 s85, 0x288000\n" \
"v_mfma_f32_16x16x32_bf16 a[152:155], v[108:111], v[68:71], a[152:155]\n" \
"buffer_load_dwordx4 v26, s[68:71], s85 offen lds\n" \
"v_mfma_f32_16x16x32_bf16 a[152:155], v[172:175], v[132:135], a[152:155]\n" \
"ds_read_b64_tr_b16 v[162:163], v35 offset:4608\n" \
"v_accvgpr_read_b32 v183, a247\n" \
"v_mfma_f32_16x16x32_bf16 a[156:159], v[112:115], v[68:71], a[156:159]\n" \
"ds_read_b64_tr_b16 v[92:93], v34 offset:0\n" \
"v_cvt_pk_bf16_f32 v200, v200, v201\n" \
"v_mfma_f32_16x16x32_bf16 a[156:159], v[176:179], v[132:135], a[156:159]\n" \
"ds_read_b128 v[68:71], v28 offset:8192\n" \
"v_cvt_pk_bf16_f32 v201, v202, v203\n" \
"v_mfma_f32_16x16x32_bf16 a[108:111], v[112:115], v[72:75], a[108:111]\n" \
"ds_read_b128 v[132:135], v29 offset:8192\n" \
"v_cvt_pk_bf16_f32 v202, v180, v181\n" \
"v_mfma_f32_16x16x32_bf16 a[108:111], v[176:179], v[136:139], a[108:111]\n" \
"s_add_u32 m0, s30, 0x9000\n" \
"s_mov_b32 s84, 0x9280\n" \
"v_mfma_f32_16x16x32_bf16 a[104:107], v[108:111], v[72:75], a[104:107]\n" \
"buffer_load_dwordx4 v24, s[76:79], s84 offen lds\n" \
"v_mfma_f32_16x16x32_bf16 a[104:107], v[172:175], v[136:139], a[104:107]\n" \
"s_add_u32 m0, s30, 0x9400\n" \
"s_mov_b32 s85, 0xb680\n" \
"v_mfma_f32_16x16x32_bf16 a[76:79], v[104:107], v[72:75], a[76:79]\n" \
"buffer_load_dwordx4 v25, s[76:79], s85 offen lds\n" \
"v_mfma_f32_16x16x32_bf16 a[76:79], v[168:171], v[136:139], a[76:79]\n" \
"ds_read_b64_tr_b16 v[94:95], v34 offset:512\n" \
"v_cvt_pk_bf16_f32 v203, v182, v183\n" \
"v_mfma_f32_16x16x32_bf16 a[72:75], v[100:103], v[72:75], a[72:75]\n" \
"ds_read_b64_tr_b16 v[156:157], v34 offset:4096\n" \
"v_accvgpr_read_b32 v196, a216\n" \
"v_mfma_f32_16x16x32_bf16 a[72:75], v[164:167], v[136:139], a[72:75]\n" \
"ds_read_b128 v[72:75], v28 offset:10240\n" \
"v_accvgpr_read_b32 v197, a217\n" \
"v_mfma_f32_16x16x32_bf16 a[136:139], v[100:103], v[76:79], a[136:139]\n" \
"ds_read_b128 v[136:139], v29 offset:10240\n" \
"v_accvgpr_read_b32 v198, a218\n" \
"v_mfma_f32_16x16x32_bf16 a[136:139], v[164:167], v[140:143], a[136:139]\n" \
"s_add_u32 m0, s31, 0x8800\n" \
"s_mov_b32 s84, 0x290000\n" \
"v_mfma_f32_16x16x32_bf16 a[140:143], v[104:107], v[76:79], a[140:143]\n" \
"buffer_load_dwordx4 v27, s[68:71], s84 offen lds\n" \
"v_mfma_f32_16x16x32_bf16 a[140:143], v[168:171], v[140:143], a[140:143]\n" \
"s_add_u32 m0, s30, 0x9800\n" \
"s_mov_b32 s85, 0xda80\n" \
"v_mfma_f32_16x16x32_bf16 a[168:171], v[108:111], v[76:79], a[168:171]\n" \
"buffer_load_dwordx4 v24, s[76:79], s85 offen lds\n" \
"v_mfma_f32_16x16x32_bf16 a[168:171], v[172:175], v[140:143], a[168:171]\n" \
"ds_read_b64_tr_b16 v[158:159], v34 offset:4608\n" \
"v_accvgpr_read_b32 v199, a219\n" \
"v_mfma_f32_16x16x32_bf16 a[172:175], v[112:115], v[76:79], a[172:175]\n" \
"ds_read_b128 v[248:251], v22 offset:0\n" \
"v_accvgpr_read_b32 v180, a220\n" \
"v_mfma_f32_16x16x32_bf16 a[172:175], v[176:179], v[140:143], a[172:175]\n" \
"ds_read_b128 v[76:79], v28 offset:12288\n" \
"v_accvgpr_read_b32 v181, a221\n" \
"v_mfma_f32_16x16x32_bf16 a[236:239], v[112:115], v[80:83], a[236:239]\n" \
"ds_read_b128 v[140:143], v29 offset:12288\n" \
"v_accvgpr_read_b32 v182, a222\n" \
"v_mfma_f32_16x16x32_bf16 a[236:239], v[176:179], v[144:147], a[236:239]\n" \
"ds_read_b128 v[48:51], v23 offset:0\n" \
"v_accvgpr_read_b32 v183, a223\n" \
"v_mfma_f32_16x16x32_bf16 a[232:235], v[108:111], v[80:83], a[232:235]\n" \
"v_cvt_pk_bf16_f32 v196, v196, v197\n" \
"v_cvt_pk_bf16_f32 v197, v198, v199\n" \
"v_mfma_f32_16x16x32_bf16 a[232:235], v[172:175], v[144:147], a[232:235]\n" \
"v_cvt_pk_bf16_f32 v198, v180, v181\n" \
"v_cvt_pk_bf16_f32 v199, v182, v183\n" \
"v_mfma_f32_16x16x32_bf16 a[204:207], v[104:107], v[80:83], a[204:207]\n" \
"v_accvgpr_read_b32 v192, a208\n" \
"v_accvgpr_read_b32 v193, a209\n" \
"v_mfma_f32_16x16x32_bf16 a[204:207], v[168:171], v[144:147], a[204:207]\n" \
"v_accvgpr_read_b32 v194, a210\n" \
"v_accvgpr_read_b32 v195, a211\n" \
"v_mfma_f32_16x16x32_bf16 a[200:203], v[100:103], v[80:83], a[200:203]\n" \
"v_accvgpr_read_b32 v180, a212\n" \
"v_accvgpr_read_b32 v181, a213\n" \
"v_mfma_f32_16x16x32_bf16 a[200:203], v[164:167], v[144:147], a[200:203]\n" \
"ds_read_b128 v[80:83], v28 offset:14336\n" \
"ds_read_b128 v[144:147], v29 offset:14336\n" \
"s_waitcnt lgkmcnt(15)\n" \
"s_nop 0\n" \
"v_mfma_f32_16x16x32_bf16 a[0:3], v[84:87], v[52:55], a[0:3]\n" \
"s_add_u32 m0, s30, 0x9c00\n" \
"s_mov_b32 s85, 0xfe80\n" \
"v_mfma_f32_16x16x32_bf16 a[0:3], v[148:151], v[116:119], a[0:3]\n" \
"buffer_load_dwordx4 v25, s[76:79], s85 offen lds\n" \
"v_mfma_f32_16x16x32_bf16 a[4:7], v[88:91], v[52:55], a[4:7]\n" \
"ds_read_b64_tr_b16 v[100:101], v36 offset:0\n" \
"v_accvgpr_read_b32 v182, a214\n" \
"v_mfma_f32_16x16x32_bf16 a[4:7], v[152:155], v[116:119], a[4:7]\n" \
"s_add_u32 m0, s31, 0x8c00\n" \
"s_mov_b32 s84, 0x298000\n" \
"v_mfma_f32_16x16x32_bf16 a[36:39], v[96:99], v[52:55], a[36:39]\n" \
"buffer_load_dwordx4 v27, s[68:71], s84 offen lds\n" \
"s_waitcnt lgkmcnt(15)\n" \
"v_mfma_f32_16x16x32_bf16 a[36:39], v[160:163], v[116:119], a[36:39]\n" \
"ds_read_b64_tr_b16 v[102:103], v36 offset:1024\n" \
"v_accvgpr_read_b32 v183, a215\n" \
"s_waitcnt lgkmcnt(12)\n" \
"v_mfma_f32_16x16x32_bf16 a[32:35], v[52:55], v[92:95], a[32:35]\n" \
"ds_read_b64_tr_b16 v[164:165], v36 offset:8192\n" \
"v_cvt_pk_bf16_f32 v192, v192, v193\n" \
"s_waitcnt lgkmcnt(9)\n" \
"s_nop 0\n" \
"v_mfma_f32_16x16x32_bf16 a[32:35], v[116:119], v[156:159], a[32:35]\n" \
"ds_read_b64_tr_b16 v[166:167], v36 offset:9216\n" \
"v_cvt_pk_bf16_f32 v193, v194, v195\n" \
"v_mfma_f32_16x16x32_bf16 a[96:99], v[56:59], v[92:95], a[96:99]\n" \
"ds_read_b64_tr_b16 v[104:105], v37 offset:0\n" \
"v_cvt_pk_bf16_f32 v194, v180, v181\n" \
"v_mfma_f32_16x16x32_bf16 a[96:99], v[120:123], v[156:159], a[96:99]\n" \
"ds_read_b64_tr_b16 v[106:107], v37 offset:1024\n" \
"v_cvt_pk_bf16_f32 v195, v182, v183\n" \
"v_mfma_f32_16x16x32_bf16 a[100:103], v[96:99], v[56:59], a[100:103]\n" \
"ds_read_b64_tr_b16 v[168:169], v37 offset:8192\n" \
"v_accvgpr_read_b32 v188, a184\n" \
"v_mfma_f32_16x16x32_bf16 a[100:103], v[160:163], v[120:123], a[100:103]\n" \
"ds_read_b64_tr_b16 v[170:171], v37 offset:9216\n" \
"v_accvgpr_read_b32 v189, a185\n" \
"v_mfma_f32_16x16x32_bf16 a[68:71], v[88:91], v[56:59], a[68:71]\n" \
"v_accvgpr_read_b32 v190, a186\n" \
"v_accvgpr_read_b32 v191, a187\n" \
"v_mfma_f32_16x16x32_bf16 a[68:71], v[152:155], v[120:123], a[68:71]\n" \
"s_add_u32 s89, s42, 0x2000\n" \
"buffer_store_dwordx4 v[232:235], v20, s[80:83], s89 offen offset:0 sc1\n" \
"v_mfma_f32_16x16x32_bf16 a[64:67], v[84:87], v[56:59], a[64:67]\n" \
"ds_read_b64_tr_b16 v[108:109], v38 offset:0\n" \
"v_accvgpr_read_b32 v180, a188\n" \
"v_mfma_f32_16x16x32_bf16 a[64:67], v[148:151], v[120:123], a[64:67]\n" \
"ds_read_b64_tr_b16 v[110:111], v38 offset:1024\n" \
"v_accvgpr_read_b32 v181, a189\n" \
"v_mfma_f32_16x16x32_bf16 a[128:131], v[84:87], v[60:63], a[128:131]\n" \
"ds_read_b64_tr_b16 v[172:173], v38 offset:8192\n" \
"v_accvgpr_read_b32 v182, a190\n" \
"v_mfma_f32_16x16x32_bf16 a[128:131], v[148:151], v[124:127], a[128:131]\n" \
"ds_read_b64_tr_b16 v[174:175], v38 offset:9216\n" \
"v_accvgpr_read_b32 v183, a191\n" \
"v_mfma_f32_16x16x32_bf16 a[132:135], v[88:91], v[60:63], a[132:135]\n" \
"v_cvt_pk_bf16_f32 v188, v188, v189\n" \
"v_cvt_pk_bf16_f32 v189, v190, v191\n" \
"v_mfma_f32_16x16x32_bf16 a[132:135], v[152:155], v[124:127], a[132:135]\n" \
"s_add_u32 s88, s42, 0x2000\n" \
"buffer_store_dwordx4 v[236:239], v20, s[80:83], s88 offen offset:1024 sc1\n" \
"v_mfma_f32_16x16x32_bf16 a[164:167], v[96:99], v[60:63], a[164:167]\n" \
"ds_read_b64_tr_b16 v[112:113], v39 offset:0\n" \
"v_cvt_pk_bf16_f32 v190, v180, v181\n" \
"v_mfma_f32_16x16x32_bf16 a[164:167], v[160:163], v[124:127], a[164:167]\n" \
"ds_read_b64_tr_b16 v[114:115], v39 offset:1024\n" \
"v_cvt_pk_bf16_f32 v191, v182, v183\n" \
"v_mfma_f32_16x16x32_bf16 a[160:163], v[60:63], v[92:95], a[160:163]\n" \
"ds_read_b64_tr_b16 v[176:177], v39 offset:8192\n" \
"v_accvgpr_read_b32 v184, a176\n" \
"v_mfma_f32_16x16x32_bf16 a[160:163], v[124:127], v[156:159], a[160:163]\n" \
"v_accvgpr_read_b32 v185, a177\n" \
"v_accvgpr_read_b32 v186, a178\n" \
"v_mfma_f32_16x16x32_bf16 a[224:227], v[64:67], v[92:95], a[224:227]\n" \
"ds_read_b64_tr_b16 v[178:179], v39 offset:9216\n" \
"v_accvgpr_read_b32 v187, a179\n" \
"v_mfma_f32_16x16x32_bf16 a[224:227], v[128:131], v[156:159], a[224:227]\n" \
"v_accvgpr_read_b32 v180, a180\n" \
"v_accvgpr_read_b32 v181, a181\n" \
"v_mfma_f32_16x16x32_bf16 a[228:231], v[96:99], v[64:67], a[228:231]\n" \
"s_add_u32 s89, s42, 0x3000\n" \
"buffer_store_dwordx4 v[240:243], v20, s[80:83], s89 offen offset:1024 sc1\n" \
"v_mfma_f32_16x16x32_bf16 a[228:231], v[160:163], v[128:131], a[228:231]\n" \
"v_accvgpr_read_b32 v182, a182\n" \
"v_accvgpr_read_b32 v183, a183\n" \
"v_mfma_f32_16x16x32_bf16 a[196:199], v[88:91], v[64:67], a[196:199]\n" \
"v_cvt_pk_bf16_f32 v184, v184, v185\n" \
"v_cvt_pk_bf16_f32 v185, v186, v187\n" \
"v_mfma_f32_16x16x32_bf16 a[196:199], v[152:155], v[128:131], a[196:199]\n" \
"v_cvt_pk_bf16_f32 v186, v180, v181\n" \
"v_cvt_pk_bf16_f32 v187, v182, v183\n" \
"v_mfma_f32_16x16x32_bf16 a[192:195], v[84:87], v[64:67], a[192:195]\n" \
"v_writelane_b32 v11, s16, 35\n" \
"s_add_i32 s16, s14, 0x411c000\n" \
"v_mfma_f32_16x16x32_bf16 a[192:195], v[148:151], v[128:131], a[192:195]\n" \
"s_add_i32 s19, s16, s15\n" \
"v_writelane_b32 v11, s19, 36\n" \
"s_waitcnt lgkmcnt(15)\n" \
"v_mfma_f32_16x16x32_bf16 a[8:11], v[88:91], v[248:251], a[8:11]\n" \
"s_add_i32 s18, s18, 0x3004000\n" \
"v_writelane_b32 v11, s18, 37\n" \
"v_mfma_f32_16x16x32_bf16 a[8:11], v[152:155], v[48:51], a[8:11]\n" \
"s_add_i32 s14, s14, 0x4120000\n" \
"v_writelane_b32 v11, s14, 38\n" \
"v_mfma_f32_16x16x32_bf16 a[40:43], v[96:99], v[248:251], a[40:43]\n" \
"s_lshl_b32 s14, s4, 2\n" \
"s_or_b32 s8, s14, s8\n" \
"v_mfma_f32_16x16x32_bf16 a[40:43], v[160:163], v[48:51], a[40:43]\n" \
"s_mul_i32 s8, s8, 0x90000\n" \
"s_lshl_b32 s14, s3, 14\n" \
"v_mfma_f32_16x16x32_bf16 a[12:15], v[248:251], v[92:95], a[12:15]\n" \
"s_add_i32 s8, s8, s14\n" \
"s_lshl_b32 s9, s9, 4\n" \
"v_mfma_f32_16x16x32_bf16 a[12:15], v[48:51], v[156:159], a[12:15]\n" \
"s_waitcnt vmcnt(3) lgkmcnt(0)\n" \
"s_barrier\n" \
"v_mfma_f32_16x16x32_bf16 a[16:19], v[100:103], v[52:55], a[16:19]\n" \
"s_add_u32 m0, s40, 0x0\n" \
"s_add_u32 s84, s39, 0xc000\n" \
"v_mfma_f32_16x16x32_bf16 a[16:19], v[164:167], v[116:119], a[16:19]\n" \
"buffer_load_dwordx4 v20, s[80:83], s84 offen sc1 lds\n" \
"v_mfma_f32_16x16x32_bf16 a[20:23], v[104:107], v[52:55], a[20:23]\n" \
"ds_read_b64_tr_b16 v[84:85], v32 offset:32768\n" \
"s_lshl_b32 s14, s4, 7\n" \
"v_mfma_f32_16x16x32_bf16 a[20:23], v[168:171], v[116:119], a[20:23]\n" \
"ds_read_b64_tr_b16 v[86:87], v32 offset:33280\n" \
"s_or_b32 s18, s9, s14\n" \
"v_mfma_f32_16x16x32_bf16 a[24:27], v[108:111], v[52:55], a[24:27]\n" \
"s_add_u32 m0, s40, 0x1000\n" \
"s_add_u32 s85, s39, 0xd000\n" \
"v_mfma_f32_16x16x32_bf16 a[24:27], v[172:175], v[116:119], a[24:27]\n" \
"buffer_load_dwordx4 v20, s[80:83], s85 offen sc1 lds\n" \
"v_mfma_f32_16x16x32_bf16 a[28:31], v[112:115], v[52:55], a[28:31]\n" \
"ds_read_b64_tr_b16 v[148:149], v32 offset:36864\n" \
"s_sub_i32 s9, s9, s14\n" \
"v_mfma_f32_16x16x32_bf16 a[28:31], v[176:179], v[116:119], a[28:31]\n" \
"ds_read_b128 v[52:55], v28 offset:32768\n" \
"s_mulk_i32 s18, 0x480\n" \
"v_mfma_f32_16x16x32_bf16 a[60:63], v[112:115], v[56:59], a[60:63]\n" \
"ds_read_b128 v[116:119], v29 offset:32768\n" \
"s_mulk_i32 s9, 0x480\n" \
"v_mfma_f32_16x16x32_bf16 a[60:63], v[176:179], v[120:123], a[60:63]\n" \
"s_add_u32 m0, s40, 0x3000\n" \
"s_mov_b32 s84, 0x300\n" \
"v_mfma_f32_16x16x32_bf16 a[56:59], v[108:111], v[56:59], a[56:59]\n" \
"buffer_load_dwordx4 v21, s[76:79], s84 offen lds\n" \
"v_mfma_f32_16x16x32_bf16 a[56:59], v[172:175], v[120:123], a[56:59]\n" \
"ds_read_b64_tr_b16 v[150:151], v32 offset:37376\n" \
"s_or_b32 s14, s18, s12\n" \
"v_mfma_f32_16x16x32_bf16 a[52:55], v[104:107], v[56:59], a[52:55]\n" \
"s_add_u32 m0, s30, 0x0\n" \
"s_mov_b32 s85, 0x300\n" \
"v_mfma_f32_16x16x32_bf16 a[52:55], v[168:171], v[120:123], a[52:55]\n" \
"buffer_load_dwordx4 v24, s[76:79], s85 offen lds\n" \
"v_mfma_f32_16x16x32_bf16 a[48:51], v[100:103], v[56:59], a[48:51]\n" \
"ds_read_b64_tr_b16 v[88:89], v33 offset:32768\n" \
"s_or_b32 s9, s9, s12\n" \
"v_mfma_f32_16x16x32_bf16 a[48:51], v[164:167], v[120:123], a[48:51]\n" \
"ds_read_b128 v[56:59], v28 offset:34816\n" \
"s_and_b32 s12, s2, 1\n" \
"s_nop 0\n" \
"v_mfma_f32_16x16x32_bf16 a[80:83], v[100:103], v[60:63], a[80:83]\n" \
"ds_read_b128 v[120:123], v29 offset:34816\n" \
"v_mfma_f32_16x16x32_bf16 a[80:83], v[164:167], v[124:127], a[80:83]\n" \
"s_add_u32 m0, s30, 0x400\n" \
"s_mov_b32 s84, 0x2700\n" \
"v_mfma_f32_16x16x32_bf16 a[84:87], v[104:107], v[60:63], a[84:87]\n" \
"buffer_load_dwordx4 v25, s[76:79], s84 offen lds\n" \
"v_mfma_f32_16x16x32_bf16 a[84:87], v[168:171], v[124:127], a[84:87]\n" \
"ds_read_b64_tr_b16 v[90:91], v33 offset:33280\n" \
"v_mfma_f32_16x16x32_bf16 a[88:91], v[108:111], v[60:63], a[88:91]\n" \
"ds_read_b64_tr_b16 v[152:153], v33 offset:36864\n" \
"v_mfma_f32_16x16x32_bf16 a[88:91], v[172:175], v[124:127], a[88:91]\n" \
"s_add_u32 m0, s31, 0x0\n" \
"s_nop 0\n" \
"s_mov_b32 s85, 0x300000\n" \
"v_mfma_f32_16x16x32_bf16 a[92:95], v[112:115], v[60:63], a[92:95]\n" \
"buffer_load_dwordx4 v26, s[68:71], s85 offen lds\n" \
"v_mfma_f32_16x16x32_bf16 a[92:95], v[176:179], v[124:127], a[92:95]\n" \
"ds_read_b128 v[60:63], v28 offset:36864\n" \
"v_mfma_f32_16x16x32_bf16 a[124:127], v[112:115], v[64:67], a[124:127]\n" \
"ds_read_b128 v[124:127], v29 offset:36864\n" \
"v_mfma_f32_16x16x32_bf16 a[124:127], v[176:179], v[128:131], a[124:127]\n" \
"s_add_u32 m0, s30, 0x800\n" \
"s_mov_b32 s84, 0x4b00\n" \
"v_mfma_f32_16x16x32_bf16 a[120:123], v[108:111], v[64:67], a[120:123]\n" \
"buffer_load_dwordx4 v24, s[76:79], s84 offen lds\n" \
"v_mfma_f32_16x16x32_bf16 a[120:123], v[172:175], v[128:131], a[120:123]\n" \
"ds_read_b64_tr_b16 v[154:155], v33 offset:37376\n" \
"v_mfma_f32_16x16x32_bf16 a[116:119], v[104:107], v[64:67], a[116:119]\n" \
"ds_read_b64_tr_b16 v[96:97], v35 offset:32768\n" \
"v_mfma_f32_16x16x32_bf16 a[116:119], v[168:171], v[128:131], a[116:119]\n" \
"ds_read_b64_tr_b16 v[98:99], v35 offset:33280\n" \
"v_mfma_f32_16x16x32_bf16 a[112:115], v[100:103], v[64:67], a[112:115]\n" \
"ds_read_b64_tr_b16 v[160:161], v35 offset:36864\n" \
"v_mfma_f32_16x16x32_bf16 a[112:115], v[164:167], v[128:131], a[112:115]\n" \
"ds_read_b128 v[64:67], v28 offset:38912\n" \
"v_mfma_f32_16x16x32_bf16 a[144:147], v[100:103], v[68:71], a[144:147]\n" \
"ds_read_b128 v[128:131], v29 offset:38912\n" \
"v_mfma_f32_16x16x32_bf16 a[144:147], v[164:167], v[132:135], a[144:147]\n" \
"s_add_u32 m0, s30, 0xc00\n" \
"s_mov_b32 s85, 0x6f00\n" \
"v_mfma_f32_16x16x32_bf16 a[148:151], v[104:107], v[68:71], a[148:151]\n" \
"buffer_load_dwordx4 v25, s[76:79], s85 offen lds\n" \
"v_mfma_f32_16x16x32_bf16 a[148:151], v[168:171], v[132:135], a[148:151]\n" \
"s_add_u32 m0, s31, 0x400\n" \
"s_mov_b32 s84, 0x308000\n" \
"v_mfma_f32_16x16x32_bf16 a[152:155], v[108:111], v[68:71], a[152:155]\n" \
"buffer_load_dwordx4 v26, s[68:71], s84 offen lds\n" \
"v_mfma_f32_16x16x32_bf16 a[152:155], v[172:175], v[132:135], a[152:155]\n" \
"ds_read_b64_tr_b16 v[162:163], v35 offset:37376\n" \
"v_mfma_f32_16x16x32_bf16 a[156:159], v[112:115], v[68:71], a[156:159]\n" \
"ds_read_b64_tr_b16 v[92:93], v34 offset:32768\n" \
"v_mfma_f32_16x16x32_bf16 a[156:159], v[176:179], v[132:135], a[156:159]\n" \
"ds_read_b128 v[68:71], v28 offset:40960\n" \
"v_mfma_f32_16x16x32_bf16 a[108:111], v[112:115], v[72:75], a[108:111]\n" \
"ds_read_b128 v[132:135], v29 offset:40960\n" \
"v_mfma_f32_16x16x32_bf16 a[108:111], v[176:179], v[136:139], a[108:111]\n" \
"s_add_u32 m0, s30, 0x1000\n" \
"s_mov_b32 s85, 0x9300\n" \
"v_mfma_f32_16x16x32_bf16 a[104:107], v[108:111], v[72:75], a[104:107]\n" \
"buffer_load_dwordx4 v24, s[76:79], s85 offen lds\n" \
"v_mfma_f32_16x16x32_bf16 a[104:107], v[172:175], v[136:139], a[104:107]\n" \
"s_add_u32 m0, s30, 0x1400\n" \
"s_mov_b32 s84, 0xb700\n" \
"v_mfma_f32_16x16x32_bf16 a[76:79], v[104:107], v[72:75], a[76:79]\n" \
"buffer_load_dwordx4 v25, s[76:79], s84 offen lds\n" \
"v_mfma_f32_16x16x32_bf16 a[76:79], v[168:171], v[136:139], a[76:79]\n" \
"ds_read_b64_tr_b16 v[94:95], v34 offset:33280\n" \
"v_mfma_f32_16x16x32_bf16 a[72:75], v[100:103], v[72:75], a[72:75]\n" \
"ds_read_b64_tr_b16 v[156:157], v34 offset:36864\n" \
"v_mfma_f32_16x16x32_bf16 a[72:75], v[164:167], v[136:139], a[72:75]\n" \
"ds_read_b128 v[72:75], v28 offset:43008\n" \
"v_mfma_f32_16x16x32_bf16 a[136:139], v[100:103], v[76:79], a[136:139]\n" \
"ds_read_b128 v[136:139], v29 offset:43008\n" \
"v_mfma_f32_16x16x32_bf16 a[136:139], v[164:167], v[140:143], a[136:139]\n" \
"s_add_u32 m0, s31, 0x800\n" \
"s_mov_b32 s85, 0x310000\n" \
"v_mfma_f32_16x16x32_bf16 a[140:143], v[104:107], v[76:79], a[140:143]\n" \
"buffer_load_dwordx4 v27, s[68:71], s85 offen lds\n" \
"v_mfma_f32_16x16x32_bf16 a[140:143], v[168:171], v[140:143], a[140:143]\n" \
"s_add_u32 m0, s30, 0x1800\n" \
"s_mov_b32 s84, 0xdb00\n" \
"v_mfma_f32_16x16x32_bf16 a[168:171], v[108:111], v[76:79], a[168:171]\n" \
"buffer_load_dwordx4 v24, s[76:79], s84 offen lds\n" \
"v_mfma_f32_16x16x32_bf16 a[168:171], v[172:175], v[140:143], a[168:171]\n" \
"ds_read_b64_tr_b16 v[158:159], v34 offset:37376\n" \
"v_mfma_f32_16x16x32_bf16 a[172:175], v[112:115], v[76:79], a[172:175]\n" \
"ds_read_b128 v[248:251], v22 offset:32768\n" \
"v_mfma_f32_16x16x32_bf16 a[172:175], v[176:179], v[140:143], a[172:175]\n" \
"ds_read_b128 v[76:79], v28 offset:45056\n" \
"v_mfma_f32_16x16x32_bf16 a[236:239], v[112:115], v[80:83], a[236:239]\n" \
"ds_read_b128 v[140:143], v29 offset:45056\n" \
"v_mfma_f32_16x16x32_bf16 a[236:239], v[176:179], v[144:147], a[236:239]\n" \
"ds_read_b128 v[48:51], v23 offset:32768\n" \
"v_mfma_f32_16x16x32_bf16 a[232:235], v[108:111], v[80:83], a[232:235]\n" \
"s_cmp_lg_u32 s12, 0\n" \
"s_cselect_b32 s13, s16, s13\n" \
"v_mfma_f32_16x16x32_bf16 a[232:235], v[172:175], v[144:147], a[232:235]\n" \
"s_bfe_u32 s16, s2, 0x20001\n" \
"s_addk_i32 s13, 0x4000\n" \
"v_mfma_f32_16x16x32_bf16 a[204:207], v[104:107], v[80:83], a[204:207]\n" \
"s_xor_b32 s16, s16, s3\n" \
"v_mfma_f32_16x16x32_bf16 a[204:207], v[168:171], v[144:147], a[204:207]\n" \
"s_cmp_lg_u32 s16, 0\n" \
"s_cselect_b32 s13, s7, s13\n" \
"v_mfma_f32_16x16x32_bf16 a[200:203], v[100:103], v[80:83], a[200:203]\n" \
"s_lshl_b32 s6, s6, 16\n" \
"v_writelane_b32 v11, s13, 39\n" \
"v_mfma_f32_16x16x32_bf16 a[200:203], v[164:167], v[144:147], a[200:203]\n" \
"ds_read_b128 v[80:83], v28 offset:47104\n" \
"ds_read_b128 v[144:147], v29 offset:47104\n" \
"s_waitcnt lgkmcnt(15)\n" \
"v_mfma_f32_16x16x32_bf16 a[0:3], v[84:87], v[52:55], a[0:3]\n" \
"s_add_u32 m0, s30, 0x1c00\n" \
"s_mov_b32 s84, 0xff00\n" \
"v_mfma_f32_16x16x32_bf16 a[0:3], v[148:151], v[116:119], a[0:3]\n" \
"buffer_load_dwordx4 v25, s[76:79], s84 offen lds\n" \
"v_mfma_f32_16x16x32_bf16 a[4:7], v[88:91], v[52:55], a[4:7]\n" \
"ds_read_b64_tr_b16 v[100:101], v36 offset:32768\n" \
"s_add_i32 s13, s8, 0x4240000\n" \
"v_mfma_f32_16x16x32_bf16 a[4:7], v[152:155], v[116:119], a[4:7]\n" \
"s_add_u32 m0, s31, 0xc00\n" \
"s_mov_b32 s85, 0x318000\n" \
"v_mfma_f32_16x16x32_bf16 a[36:39], v[96:99], v[52:55], a[36:39]\n" \
"buffer_load_dwordx4 v27, s[68:71], s85 offen lds\n" \
"s_waitcnt lgkmcnt(15)\n" \
"v_mfma_f32_16x16x32_bf16 a[36:39], v[160:163], v[116:119], a[36:39]\n" \
"ds_read_b64_tr_b16 v[102:103], v36 offset:33792\n" \
"s_add_i32 s6, s13, s6\n" \
"s_waitcnt lgkmcnt(12)\n" \
"v_mfma_f32_16x16x32_bf16 a[32:35], v[52:55], v[92:95], a[32:35]\n" \
"ds_read_b64_tr_b16 v[164:165], v36 offset:40960\n" \
"v_writelane_b32 v11, s6, 40\n" \
"s_waitcnt lgkmcnt(9)\n" \
"v_mfma_f32_16x16x32_bf16 a[32:35], v[116:119], v[156:159], a[32:35]\n" \
"ds_read_b64_tr_b16 v[166:167], v36 offset:41984\n" \
"s_add_i32 s8, s8, 0x42c0000\n" \
"v_mfma_f32_16x16x32_bf16 a[96:99], v[56:59], v[92:95], a[96:99]\n" \
".fill (L_pb + 32480 - .) / 4, 4, 0xBF800000\n" \
"L_pp8:\n" \
"s_cbranch_execz L_pret\n" \
"ds_read_b64_tr_b16 v[104:105], v37 offset:32768\n" \
"s_lshl_b32 s6, s12, 10\n" \
"v_mfma_f32_16x16x32_bf16 a[96:99], v[120:123], v[156:159], a[96:99]\n" \
"ds_read_b64_tr_b16 v[106:107], v37 offset:33792\n" \
"s_or_b32 s6, s8, s6\n" \
"v_mfma_f32_16x16x32_bf16 a[100:103], v[96:99], v[56:59], a[100:103]\n" \
"ds_read_b64_tr_b16 v[168:169], v37 offset:40960\n" \
"v_mfma_f32_16x16x32_bf16 a[100:103], v[160:163], v[120:123], a[100:103]\n" \
"ds_read_b64_tr_b16 v[170:171], v37 offset:41984\n" \
"v_mfma_f32_16x16x32_bf16 a[68:71], v[88:91], v[56:59], a[68:71]\n" \
"s_cmp_lg_u32 s16, 0\n" \
"s_cselect_b32 s6, s7, s6\n" \
"v_mfma_f32_16x16x32_bf16 a[68:71], v[152:155], v[120:123], a[68:71]\n" \
"s_add_u32 s88, s42, 0x3000\n" \
"buffer_store_dwordx4 v[244:247], v20, s[80:83], s88 offen offset:0 sc1\n" \
"v_mfma_f32_16x16x32_bf16 a[64:67], v[84:87], v[56:59], a[64:67]\n" \
"ds_read_b64_tr_b16 v[108:109], v38 offset:32768\n" \
"s_mulk_i32 s4, 0x4800\n" \
"v_mfma_f32_16x16x32_bf16 a[64:67], v[148:151], v[120:123], a[64:67]\n" \
"ds_read_b64_tr_b16 v[110:111], v38 offset:33792\n" \
"v_writelane_b32 v11, s6, 41\n" \
"v_mfma_f32_16x16x32_bf16 a[128:131], v[84:87], v[60:63], a[128:131]\n" \
"ds_read_b64_tr_b16 v[172:173], v38 offset:40960\n" \
"s_add_i32 s6, s14, s10\n" \
"v_mfma_f32_16x16x32_bf16 a[128:131], v[148:151], v[124:127], a[128:131]\n" \
"ds_read_b64_tr_b16 v[174:175], v38 offset:41984\n" \
"v_writelane_b32 v11, s6, 42\n" \
"v_mfma_f32_16x16x32_bf16 a[132:135], v[88:91], v[60:63], a[132:135]\n" \
"s_add_i32 s6, s9, 0x21c000\n" \
"s_add_i32 s9, s6, s10\n" \
"v_mfma_f32_16x16x32_bf16 a[132:135], v[152:155], v[124:127], a[132:135]\n" \
"s_add_u32 s89, s42, 0x0\n" \
"buffer_store_dwordx4 v[208:211], v20, s[80:83], s89 offen offset:3072 sc1\n" \
"v_mfma_f32_16x16x32_bf16 a[164:167], v[96:99], v[60:63], a[164:167]\n" \
"ds_read_b64_tr_b16 v[112:113], v39 offset:32768\n" \
"v_mfma_f32_16x16x32_bf16 a[164:167], v[160:163], v[124:127], a[164:167]\n" \
"ds_read_b64_tr_b16 v[114:115], v39 offset:33792\n" \
"v_mfma_f32_16x16x32_bf16 a[160:163], v[60:63], v[92:95], a[160:163]\n" \
"ds_read_b64_tr_b16 v[176:177], v39 offset:40960\n" \
"v_mfma_f32_16x16x32_bf16 a[160:163], v[124:127], v[156:159], a[160:163]\n" \
"s_cmp_lg_u32 s12, 0\n" \
"s_cselect_b32 s6, s6, s14\n" \
"v_mfma_f32_16x16x32_bf16 a[224:227], v[64:67], v[92:95], a[224:227]\n" \
"ds_read_b64_tr_b16 v[178:179], v39 offset:41984\n" \
"s_addk_i32 s6, 0x100\n" \
"v_mfma_f32_16x16x32_bf16 a[224:227], v[128:131], v[156:159], a[224:227]\n" \
"v_writelane_b32 v11, s9, 43\n" \
"v_mfma_f32_16x16x32_bf16 a[228:231], v[96:99], v[64:67], a[228:231]\n" \
"s_add_u32 s88, s42, 0x0\n" \
"buffer_store_dwordx4 v[212:215], v20, s[80:83], s88 offen offset:2048 sc1\n" \
"v_mfma_f32_16x16x32_bf16 a[228:231], v[160:163], v[128:131], a[228:231]\n" \
"s_cmp_lg_u32 s16, 0\n" \
"s_cselect_b32 s6, s7, s6\n" \
"v_mfma_f32_16x16x32_bf16 a[196:199], v[88:91], v[64:67], a[196:199]\n" \
"v_writelane_b32 v11, s6, 44\n" \
"s_or_b32 s6, s8, s15\n" \
"v_mfma_f32_16x16x32_bf16 a[196:199], v[152:155], v[128:131], a[196:199]\n" \
"v_writelane_b32 v11, s6, 52\n" \
"s_lshl_b32 s6, s3, 17\n" \
"v_mfma_f32_16x16x32_bf16 a[192:195], v[84:87], v[64:67], a[192:195]\n" \
"s_add_i32 s6, s13, s6\n" \
"s_or_b32 s5, s6, s5\n" \
"v_mfma_f32_16x16x32_bf16 a[192:195], v[148:151], v[128:131], a[192:195]\n" \
"s_or_b32 s5, s5, 0x3840\n" \
"v_writelane_b32 v11, s5, 6\n" \
"s_waitcnt lgkmcnt(15)\n" \
"v_mfma_f32_16x16x32_bf16 a[8:11], v[88:91], v[248:251], a[8:11]\n" \
"s_mov_b32 s5, 0xff0000\n" \
"v_mfma_f32_16x16x32_bf16 a[8:11], v[152:155], v[48:51], a[8:11]\n" \
"s_cmp_lg_u32 s16, 0\n" \
"s_cselect_b32 s5, s17, s5\n" \
"v_mfma_f32_16x16x32_bf16 a[40:43], v[96:99], v[248:251], a[40:43]\n" \
"v_writelane_b32 v11, s5, 7\n" \
"s_mul_i32 s5, s3, 0x1200\n" \
"v_mfma_f32_16x16x32_bf16 a[40:43], v[160:163], v[48:51], a[40:43]\n" \
"s_add_i32 s4, s5, s4\n" \
"s_add_i32 s4, s4, s11\n" \
"v_mfma_f32_16x16x32_bf16 a[12:15], v[248:251], v[92:95], a[12:15]\n" \
"v_writelane_b32 v11, s4, 8\n" \
"s_lshl_b32 s4, s2, 16\n" \
"v_mfma_f32_16x16x32_bf16 a[12:15], v[48:51], v[156:159], a[12:15]\n" \
"s_waitcnt vmcnt(3) lgkmcnt(0)\n" \
"s_barrier\n" \
"s_nop 0\n" \
"v_mfma_f32_16x16x32_bf16 a[16:19], v[100:103], v[52:55], a[16:19]\n" \
"s_add_u32 m0, s40, 0x8000\n" \
"s_add_u32 s85, s39, 0xe000\n" \
"v_mfma_f32_16x16x32_bf16 a[16:19], v[164:167], v[116:119], a[16:19]\n" \
"buffer_load_dwordx4 v20, s[80:83], s85 offen sc1 lds\n" \
"v_mfma_f32_16x16x32_bf16 a[20:23], v[104:107], v[52:55], a[20:23]\n" \
"ds_read_b64_tr_b16 v[84:85], v32 offset:0\n" \
"s_add_i32 s5, s4, 0x10000\n" \
"v_mfma_f32_16x16x32_bf16 a[20:23], v[168:171], v[116:119], a[20:23]\n" \
"ds_read_b64_tr_b16 v[86:87], v32 offset:512\n" \
"s_and_b32 s5, s5, 0x70000\n" \
"v_mfma_f32_16x16x32_bf16 a[24:27], v[108:111], v[52:55], a[24:27]\n" \
"s_add_u32 m0, s40, 0x9000\n" \
"s_add_u32 s84, s39, 0xf000\n" \
"v_mfma_f32_16x16x32_bf16 a[24:27], v[172:175], v[116:119], a[24:27]\n" \
"buffer_load_dwordx4 v20, s[80:83], s84 offen sc1 lds\n" \
"v_mfma_f32_16x16x32_bf16 a[28:31], v[112:115], v[52:55], a[28:31]\n" \
"ds_read_b64_tr_b16 v[148:149], v32 offset:4096\n" \
"s_add_i32 s5, s13, s5\n" \
"s_nop 0\n" \
"v_mfma_f32_16x16x32_bf16 a[28:31], v[176:179], v[116:119], a[28:31]\n" \
"ds_read_b128 v[52:55], v28 offset:0\n" \
"v_writelane_b32 v11, s5, 45\n" \
"v_mfma_f32_16x16x32_bf16 a[60:63], v[112:115], v[56:59], a[60:63]\n" \
"ds_read_b128 v[116:119], v29 offset:0\n" \
"s_add_i32 s5, s4, 0x20000\n" \
"v_mfma_f32_16x16x32_bf16 a[60:63], v[176:179], v[120:123], a[60:63]\n" \
"s_add_u32 m0, s40, 0xb000\n" \
"s_mov_b32 s85, 0x380\n" \
"v_mfma_f32_16x16x32_bf16 a[56:59], v[108:111], v[56:59], a[56:59]\n" \
"buffer_load_dwordx4 v21, s[76:79], s85 offen lds\n" \
"v_mfma_f32_16x16x32_bf16 a[56:59], v[172:175], v[120:123], a[56:59]\n" \
"ds_read_b64_tr_b16 v[150:151], v32 offset:4608\n" \
"s_and_b32 s5, s5, 0x70000\n" \
"v_mfma_f32_16x16x32_bf16 a[52:55], v[104:107], v[56:59], a[52:55]\n" \
"s_add_u32 m0, s30, 0x8000\n" \
"s_mov_b32 s84, 0x380\n" \
"v_mfma_f32_16x16x32_bf16 a[52:55], v[168:171], v[120:123], a[52:55]\n" \
"buffer_load_dwordx4 v24, s[76:79], s84 offen lds\n" \
"v_mfma_f32_16x16x32_bf16 a[48:51], v[100:103], v[56:59], a[48:51]\n" \
"ds_read_b64_tr_b16 v[88:89], v33 offset:0\n" \
"s_add_i32 s5, s13, s5\n" \
"v_mfma_f32_16x16x32_bf16 a[48:51], v[164:167], v[120:123], a[48:51]\n" \
"ds_read_b128 v[56:59], v28 offset:2048\n" \
"s_bitset1_b32 s5, 11\n" \
"v_mfma_f32_16x16x32_bf16 a[80:83], v[100:103], v[60:63], a[80:83]\n" \
"ds_read_b128 v[120:123], v29 offset:2048\n" \
"v_writelane_b32 v11, s5, 46\n" \
"v_mfma_f32_16x16x32_bf16 a[80:83], v[164:167], v[124:127], a[80:83]\n" \
"s_add_u32 m0, s30, 0x8400\n" \
"s_mov_b32 s85, 0x2780\n" \
"v_mfma_f32_16x16x32_bf16 a[84:87], v[104:107], v[60:63], a[84:87]\n" \
"buffer_load_dwordx4 v25, s[76:79], s85 offen lds\n" \
"v_mfma_f32_16x16x32_bf16 a[84:87], v[168:171], v[124:127], a[84:87]\n" \
"ds_read_b64_tr_b16 v[90:91], v33 offset:512\n" \
"s_add_i32 s5, s4, 0x30000\n" \
"v_mfma_f32_16x16x32_bf16 a[88:91], v[108:111], v[60:63], a[88:91]\n" \
"ds_read_b64_tr_b16 v[152:153], v33 offset:4096\n" \
"s_and_b32 s5, s5, 0x70000\n" \
"v_mfma_f32_16x16x32_bf16 a[88:91], v[172:175], v[124:127], a[88:91]\n" \
"s_add_u32 m0, s31, 0x8000\n" \
"s_mov_b32 s84, 0x380000\n" \
"v_mfma_f32_16x16x32_bf16 a[92:95], v[112:115], v[60:63], a[92:95]\n" \
"buffer_load_dwordx4 v26, s[68:71], s84 offen lds\n" \
"v_mfma_f32_16x16x32_bf16 a[92:95], v[176:179], v[124:127], a[92:95]\n" \
"ds_read_b128 v[60:63], v28 offset:4096\n" \
"s_add_i32 s5, s13, s5\n" \
"v_mfma_f32_16x16x32_bf16 a[124:127], v[112:115], v[64:67], a[124:127]\n" \
"ds_read_b128 v[124:127], v29 offset:4096\n" \
"s_bitset1_b32 s5, 12\n" \
"v_mfma_f32_16x16x32_bf16 a[124:127], v[176:179], v[128:131], a[124:127]\n" \
"s_add_u32 m0, s30, 0x8800\n" \
"s_mov_b32 s85, 0x4b80\n" \
"v_mfma_f32_16x16x32_bf16 a[120:123], v[108:111], v[64:67], a[120:123]\n" \
"buffer_load_dwordx4 v24, s[76:79], s85 offen lds\n" \
"v_mfma_f32_16x16x32_bf16 a[120:123], v[172:175], v[128:131], a[120:123]\n" \
"ds_read_b64_tr_b16 v[154:155], v33 offset:4608\n" \
"v_writelane_b32 v11, s5, 47\n" \
"v_mfma_f32_16x16x32_bf16 a[116:119], v[104:107], v[64:67], a[116:119]\n" \
"ds_read_b64_tr_b16 v[96:97], v35 offset:0\n" \
"s_and_b32 s5, s4, 0x70000\n" \
"v_mfma_f32_16x16x32_bf16 a[116:119], v[168:171], v[128:131], a[116:119]\n" \
"ds_read_b64_tr_b16 v[98:99], v35 offset:512\n" \
"s_xor_b32 s5, s5, 0x40000\n" \
"v_mfma_f32_16x16x32_bf16 a[112:115], v[100:103], v[64:67], a[112:115]\n" \
"ds_read_b64_tr_b16 v[160:161], v35 offset:4096\n" \
"s_add_i32 s5, s13, s5\n" \
"s_nop 0\n" \
"v_mfma_f32_16x16x32_bf16 a[112:115], v[164:167], v[128:131], a[112:115]\n" \
"ds_read_b128 v[64:67], v28 offset:6144\n" \
"s_or_b32 s5, s5, 0x1800\n" \
"v_mfma_f32_16x16x32_bf16 a[144:147], v[100:103], v[68:71], a[144:147]\n" \
"ds_read_b128 v[128:131], v29 offset:6144\n" \
"v_writelane_b32 v11, s5, 48\n" \
"v_mfma_f32_16x16x32_bf16 a[144:147], v[164:167], v[132:135], a[144:147]\n" \
"s_add_u32 m0, s30, 0x8c00\n" \
"s_mov_b32 s84, 0x6f80\n" \
"v_mfma_f32_16x16x32_bf16 a[148:151], v[104:107], v[68:71], a[148:151]\n" \
"buffer_load_dwordx4 v25, s[76:79], s84 offen lds\n" \
"v_mfma_f32_16x16x32_bf16 a[148:151], v[168:171], v[132:135], a[148:151]\n" \
"s_add_u32 m0, s31, 0x8400\n" \
"s_mov_b32 s85, 0x388000\n" \
"v_mfma_f32_16x16x32_bf16 a[152:155], v[108:111], v[68:71], a[152:155]\n" \
"buffer_load_dwordx4 v26, s[68:71], s85 offen lds\n" \
"v_mfma_f32_16x16x32_bf16 a[152:155], v[172:175], v[132:135], a[152:155]\n" \
"ds_read_b64_tr_b16 v[162:163], v35 offset:4608\n" \
"s_add_i32 s5, s4, 0x50000\n" \
"v_mfma_f32_16x16x32_bf16 a[156:159], v[112:115], v[68:71], a[156:159]\n" \
"ds_read_b64_tr_b16 v[92:93], v34 offset:0\n" \
"s_and_b32 s5, s5, 0x70000\n" \
"v_mfma_f32_16x16x32_bf16 a[156:159], v[176:179], v[132:135], a[156:159]\n" \
"ds_read_b128 v[68:71], v28 offset:8192\n" \
"s_add_i32 s5, s13, s5\n" \
"v_mfma_f32_16x16x32_bf16 a[108:111], v[112:115], v[72:75], a[108:111]\n" \
"ds_read_b128 v[132:135], v29 offset:8192\n" \
"s_bitset1_b32 s5, 13\n" \
"v_mfma_f32_16x16x32_bf16 a[108:111], v[176:179], v[136:139], a[108:111]\n" \
"s_add_u32 m0, s30, 0x9000\n" \
"s_mov_b32 s84, 0x9380\n" \
"v_mfma_f32_16x16x32_bf16 a[104:107], v[108:111], v[72:75], a[104:107]\n" \
"buffer_load_dwordx4 v24, s[76:79], s84 offen lds\n" \
"v_mfma_f32_16x16x32_bf16 a[104:107], v[172:175], v[136:139], a[104:107]\n" \
"s_add_u32 m0, s30, 0x9400\n" \
"s_mov_b32 s85, 0xb780\n" \
"v_mfma_f32_16x16x32_bf16 a[76:79], v[104:107], v[72:75], a[76:79]\n" \
"buffer_load_dwordx4 v25, s[76:79], s85 offen lds\n" \
"v_mfma_f32_16x16x32_bf16 a[76:79], v[168:171], v[136:139], a[76:79]\n" \
"ds_read_b64_tr_b16 v[94:95], v34 offset:512\n" \
"v_writelane_b32 v11, s5, 49\n" \
"v_mfma_f32_16x16x32_bf16 a[72:75], v[100:103], v[72:75], a[72:75]\n" \
"ds_read_b64_tr_b16 v[156:157], v34 offset:4096\n" \
"s_add_i32 s5, s4, 0x60000\n" \
"v_mfma_f32_16x16x32_bf16 a[72:75], v[164:167], v[136:139], a[72:75]\n" \
"ds_read_b128 v[72:75], v28 offset:10240\n" \
"s_and_b32 s5, s5, 0x70000\n" \
"v_mfma_f32_16x16x32_bf16 a[136:139], v[100:103], v[76:79], a[136:139]\n" \
"ds_read_b128 v[136:139], v29 offset:10240\n" \
"s_add_i32 s4, s4, 0x70000\n" \
"v_mfma_f32_16x16x32_bf16 a[136:139], v[164:167], v[140:143], a[136:139]\n" \
"s_add_u32 m0, s31, 0x8800\n" \
"s_mov_b32 s84, 0x390000\n" \
"v_mfma_f32_16x16x32_bf16 a[140:143], v[104:107], v[76:79], a[140:143]\n" \
"buffer_load_dwordx4 v27, s[68:71], s84 offen lds\n" \
"v_mfma_f32_16x16x32_bf16 a[140:143], v[168:171], v[140:143], a[140:143]\n" \
"s_add_u32 m0, s30, 0x9800\n" \
"s_mov_b32 s85, 0xdb80\n" \
"v_mfma_f32_16x16x32_bf16 a[168:171], v[108:111], v[76:79], a[168:171]\n" \
"buffer_load_dwordx4 v24, s[76:79], s85 offen lds\n" \
"v_mfma_f32_16x16x32_bf16 a[168:171], v[172:175], v[140:143], a[168:171]\n" \
"ds_read_b64_tr_b16 v[158:159], v34 offset:4608\n" \
"s_add_i32 s5, s13, s5\n" \
"s_nop 0\n" \
"v_mfma_f32_16x16x32_bf16 a[172:175], v[112:115], v[76:79], a[172:175]\n" \
"ds_read_b128 v[248:251], v22 offset:0\n" \
"s_and_b32 s4, s4, 0x70000\n" \
"v_mfma_f32_16x16x32_bf16 a[172:175], v[176:179], v[140:143], a[172:175]\n" \
"ds_read_b128 v[76:79], v28 offset:12288\n" \
"s_or_b32 s5, s5, 0x2800\n" \
"v_mfma_f32_16x16x32_bf16 a[236:239], v[112:115], v[80:83], a[236:239]\n" \
"ds_read_b128 v[140:143], v29 offset:12288\n" \
"v_writelane_b32 v11, s5, 50\n" \
"v_mfma_f32_16x16x32_bf16 a[236:239], v[176:179], v[144:147], a[236:239]\n" \
"ds_read_b128 v[48:51], v23 offset:0\n" \
"s_add_i32 s13, s13, s4\n" \
"v_mfma_f32_16x16x32_bf16 a[232:235], v[108:111], v[80:83], a[232:235]\n" \
"s_or_b32 s4, s13, 0x3000\n" \
"v_writelane_b32 v11, s4, 51\n" \
"v_mfma_f32_16x16x32_bf16 a[232:235], v[172:175], v[144:147], a[232:235]\n" \
"s_movk_i32 s11, 0x4000\n" \
"s_lshl_b32 s4, s2, 2\n" \
"v_mfma_f32_16x16x32_bf16 a[204:207], v[104:107], v[80:83], a[204:207]\n" \
"s_and_b32 s4, s4, 28\n" \
"s_bfe_u32 s5, s2, 0x20003\n" \
"v_mfma_f32_16x16x32_bf16 a[204:207], v[168:171], v[144:147], a[204:207]\n" \
"s_or_b32 s4, s4, s5\n" \
"s_lshr_b32 s5, s2, 5\n" \
"v_mfma_f32_16x16x32_bf16 a[200:203], v[100:103], v[80:83], a[200:203]\n" \
"s_lshr_b32 s19, s3, 1\n" \
"s_lshl_b32 s18, s5, 1\n" \
"v_mfma_f32_16x16x32_bf16 a[200:203], v[164:167], v[144:147], a[200:203]\n" \
"ds_read_b128 v[80:83], v28 offset:14336\n" \
"ds_read_b128 v[144:147], v29 offset:14336\n" \
"s_waitcnt lgkmcnt(15)\n" \
"v_mfma_f32_16x16x32_bf16 a[0:3], v[84:87], v[52:55], a[0:3]\n" \
"s_add_u32 m0, s30, 0x9c00\n" \
"s_mov_b32 s85, 0xff80\n" \
"v_mfma_f32_16x16x32_bf16 a[0:3], v[148:151], v[116:119], a[0:3]\n" \
"buffer_load_dwordx4 v25, s[76:79], s85 offen lds\n" \
"v_mfma_f32_16x16x32_bf16 a[4:7], v[88:91], v[52:55], a[4:7]\n" \
"ds_read_b64_tr_b16 v[100:101], v36 offset:0\n" \
"s_lshl_b32 s6, s4, 20\n" \
"v_mfma_f32_16x16x32_bf16 a[4:7], v[152:155], v[116:119], a[4:7]\n" \
"s_add_u32 m0, s31, 0x8c00\n" \
"s_mov_b32 s84, 0x398000\n" \
"v_mfma_f32_16x16x32_bf16 a[36:39], v[96:99], v[52:55], a[36:39]\n" \
"buffer_load_dwordx4 v27, s[68:71], s84 offen lds\n" \
"s_waitcnt lgkmcnt(15)\n" \
"v_mfma_f32_16x16x32_bf16 a[36:39], v[160:163], v[116:119], a[36:39]\n" \
"ds_read_b64_tr_b16 v[102:103], v36 offset:1024\n" \
"s_lshl_b32 s7, s4, 8\n" \
"s_waitcnt lgkmcnt(12)\n" \
"v_mfma_f32_16x16x32_bf16 a[32:35], v[52:55], v[92:95], a[32:35]\n" \
"ds_read_b64_tr_b16 v[164:165], v36 offset:8192\n" \
"s_and_b32 s13, s3, 1\n" \
"s_waitcnt lgkmcnt(9)\n" \
"v_mfma_f32_16x16x32_bf16 a[32:35], v[116:119], v[156:159], a[32:35]\n" \
"ds_read_b64_tr_b16 v[166:167], v36 offset:9216\n" \
"s_add_i32 s14, s18, s19\n" \
"v_mfma_f32_16x16x32_bf16 a[96:99], v[56:59], v[92:95], a[96:99]\n" \
"ds_read_b64_tr_b16 v[104:105], v37 offset:0\n" \
"v_mfma_f32_16x16x32_bf16 a[96:99], v[120:123], v[156:159], a[96:99]\n" \
"ds_read_b64_tr_b16 v[106:107], v37 offset:1024\n" \
"v_mfma_f32_16x16x32_bf16 a[100:103], v[96:99], v[56:59], a[100:103]\n" \
"ds_read_b64_tr_b16 v[168:169], v37 offset:8192\n" \
"v_mfma_f32_16x16x32_bf16 a[100:103], v[160:163], v[120:123], a[100:103]\n" \
"ds_read_b64_tr_b16 v[170:171], v37 offset:9216\n" \
"v_mfma_f32_16x16x32_bf16 a[68:71], v[88:91], v[56:59], a[68:71]\n" \
"s_add_u32 s6, s26, s6\n" \
"s_addc_u32 s9, s27, 0\n" \
"v_mfma_f32_16x16x32_bf16 a[68:71], v[152:155], v[120:123], a[68:71]\n" \
"s_add_u32 s89, s42, 0x1000\n" \
"buffer_store_dwordx4 v[184:187], v20, s[80:83], s89 offen offset:2048 sc1\n" \
"v_mfma_f32_16x16x32_bf16 a[64:67], v[84:87], v[56:59], a[64:67]\n" \
"ds_read_b64_tr_b16 v[108:109], v38 offset:0\n" \
"v_mfma_f32_16x16x32_bf16 a[64:67], v[148:151], v[120:123], a[64:67]\n" \
"ds_read_b64_tr_b16 v[110:111], v38 offset:1024\n" \
"v_mfma_f32_16x16x32_bf16 a[128:131], v[84:87], v[60:63], a[128:131]\n" \
"ds_read_b64_tr_b16 v[172:173], v38 offset:8192\n" \
"v_mfma_f32_16x16x32_bf16 a[128:131], v[148:151], v[124:127], a[128:131]\n" \
"ds_read_b64_tr_b16 v[174:175], v38 offset:9216\n" \
"v_mfma_f32_16x16x32_bf16 a[132:135], v[88:91], v[60:63], a[132:135]\n" \
"s_add_u32 s8, s6, 0x1000000\n" \
"s_addc_u32 s6, s9, 0\n" \
"v_mfma_f32_16x16x32_bf16 a[132:135], v[152:155], v[124:127], a[132:135]\n" \
"s_add_u32 s88, s42, 0x1000\n" \
"buffer_store_dwordx4 v[188:191], v20, s[80:83], s88 offen offset:3072 sc1\n" \
"v_mfma_f32_16x16x32_bf16 a[164:167], v[96:99], v[60:63], a[164:167]\n" \
"ds_read_b64_tr_b16 v[112:113], v39 offset:0\n" \
"s_and_b32 s9, s6, 0xffff\n" \
"v_mfma_f32_16x16x32_bf16 a[164:167], v[160:163], v[124:127], a[164:167]\n" \
"ds_read_b64_tr_b16 v[114:115], v39 offset:1024\n" \
"s_lshl_b32 s6, s5, 16\n" \
"v_mfma_f32_16x16x32_bf16 a[160:163], v[60:63], v[92:95], a[160:163]\n" \
"ds_read_b64_tr_b16 v[176:177], v39 offset:8192\n" \
"s_sub_i32 s10, 0x100000, s6\n" \
"v_mfma_f32_16x16x32_bf16 a[160:163], v[124:127], v[156:159], a[160:163]\n" \
"s_mov_b32 s6, 0x8000\n" \
"v_mfma_f32_16x16x32_bf16 a[224:227], v[64:67], v[92:95], a[224:227]\n" \
"ds_read_b64_tr_b16 v[178:179], v39 offset:9216\n" \
"v_mfma_f32_16x16x32_bf16 a[224:227], v[128:131], v[156:159], a[224:227]\n" \
"s_cmp_lg_u32 s5, 0\n" \
"s_cselect_b32 s17, s11, s6\n" \
"v_mfma_f32_16x16x32_bf16 a[228:231], v[96:99], v[64:67], a[228:231]\n" \
"s_add_u32 s89, s42, 0x2000\n" \
"buffer_store_dwordx4 v[192:195], v20, s[80:83], s89 offen offset:2048 sc1\n" \
"v_mfma_f32_16x16x32_bf16 a[228:231], v[160:163], v[128:131], a[228:231]\n" \
"s_lshl_b32 s6, s3, 1\n" \
"s_lshl_b32 s5, s5, 3\n" \
"v_mfma_f32_16x16x32_bf16 a[196:199], v[88:91], v[64:67], a[196:199]\n" \
"s_and_b32 s6, s6, 4\n" \
"s_or_b32 s12, s5, s6\n" \
"v_mfma_f32_16x16x32_bf16 a[196:199], v[152:155], v[128:131], a[196:199]\n" \
"s_sub_i32 s5, s13, s5\n" \
"s_or_b32 s5, s5, s6\n" \
"v_mfma_f32_16x16x32_bf16 a[192:195], v[84:87], v[64:67], a[192:195]\n" \
"s_or_b32 s6, s12, s13\n" \
"s_lshl_b32 s4, s4, 2\n" \
"v_mfma_f32_16x16x32_bf16 a[192:195], v[148:151], v[128:131], a[192:195]\n" \
"s_mulk_i32 s6, 0x92\n" \
"s_add_i32 s4, s4, 18\n" \
"s_waitcnt lgkmcnt(15)\n" \
"v_mfma_f32_16x16x32_bf16 a[8:11], v[88:91], v[248:251], a[8:11]\n" \
"s_mulk_i32 s5, 0x92\n" \
"s_add_i32 s6, s4, s6\n" \
"v_mfma_f32_16x16x32_bf16 a[8:11], v[152:155], v[48:51], a[8:11]\n" \
"s_add_i32 s5, s5, s4\n" \
"s_lshl_b32 s4, s3, 11\n" \
"v_mfma_f32_16x16x32_bf16 a[40:43], v[96:99], v[248:251], a[40:43]\n" \
"s_lshl_b32 s15, s13, 12\n" \
"s_lshl_b32 s16, s19, 14\n" \
"v_mfma_f32_16x16x32_bf16 a[40:43], v[160:163], v[48:51], a[40:43]\n" \
"s_mul_i32 s19, s19, 0xe000\n" \
"s_mov_b32 s11, 0x20000\n" \
"v_mfma_f32_16x16x32_bf16 a[12:15], v[248:251], v[92:95], a[12:15]\n" \
"s_or_b32 s12, s4, 0xc000\n" \
"s_lshl_b32 s4, s3, 8\n" \
"v_mfma_f32_16x16x32_bf16 a[12:15], v[48:51], v[156:159], a[12:15]\n" \
"s_waitcnt vmcnt(3) lgkmcnt(0)\n" \
"s_barrier\n" \
"v_mfma_f32_16x16x32_bf16 a[16:19], v[100:103], v[52:55], a[16:19]\n" \
"s_add_u32 m0, s40, 0x0\n" \
"s_add_u32 s84, s39, 0x10000\n" \
"v_mfma_f32_16x16x32_bf16 a[16:19], v[164:167], v[116:119], a[16:19]\n" \
"buffer_load_dwordx4 v20, s[80:83], s84 offen sc1 lds\n" \
"v_mfma_f32_16x16x32_bf16 a[20:23], v[104:107], v[52:55], a[20:23]\n" \
"ds_read_b64_tr_b16 v[84:85], v32 offset:32768\n" \
"s_or_b32 s22, s15, s16\n" \
"v_mfma_f32_16x16x32_bf16 a[20:23], v[168:171], v[116:119], a[20:23]\n" \
"ds_read_b64_tr_b16 v[86:87], v32 offset:33280\n" \
"s_or_b32 s23, s19, s15\n" \
"v_mfma_f32_16x16x32_bf16 a[24:27], v[108:111], v[52:55], a[24:27]\n" \
"s_add_u32 m0, s40, 0x1000\n" \
"s_add_u32 s85, s39, 0x11000\n" \
"v_mfma_f32_16x16x32_bf16 a[24:27], v[172:175], v[116:119], a[24:27]\n" \
"buffer_load_dwordx4 v20, s[80:83], s85 offen sc1 lds\n" \
"v_mfma_f32_16x16x32_bf16 a[28:31], v[112:115], v[52:55], a[28:31]\n" \
"ds_read_b64_tr_b16 v[148:149], v32 offset:36864\n" \
"s_lshl_b32 s60, s6, 10\n" \
"v_mfma_f32_16x16x32_bf16 a[28:31], v[176:179], v[116:119], a[28:31]\n" \
"ds_read_b128 v[52:55], v28 offset:32768\n" \
"s_lshl_b32 s61, s5, 10\n" \
"v_mfma_f32_16x16x32_bf16 a[60:63], v[112:115], v[56:59], a[60:63]\n" \
"ds_read_b128 v[116:119], v29 offset:32768\n" \
"s_lshl_b32 s14, s14, 15\n" \
"v_mfma_f32_16x16x32_bf16 a[60:63], v[176:179], v[120:123], a[60:63]\n" \
"s_add_u32 m0, s40, 0x3000\n" \
"s_mov_b32 s84, 0x400\n" \
"v_mfma_f32_16x16x32_bf16 a[56:59], v[108:111], v[56:59], a[56:59]\n" \
"buffer_load_dwordx4 v21, s[76:79], s84 offen lds\n" \
"v_mfma_f32_16x16x32_bf16 a[56:59], v[172:175], v[120:123], a[56:59]\n" \
"ds_read_b64_tr_b16 v[150:151], v32 offset:37376\n" \
"s_or_b32 s20, s4, 0x26c00\n" \
"v_mfma_f32_16x16x32_bf16 a[52:55], v[104:107], v[56:59], a[52:55]\n" \
"s_add_u32 m0, s30, 0x0\n" \
"s_nop 0\n" \
"s_mov_b32 s85, 0x400\n" \
"v_mfma_f32_16x16x32_bf16 a[52:55], v[168:171], v[120:123], a[52:55]\n" \
"buffer_load_dwordx4 v24, s[76:79], s85 offen lds\n" \
"v_mfma_f32_16x16x32_bf16 a[48:51], v[100:103], v[56:59], a[48:51]\n" \
"ds_read_b64_tr_b16 v[88:89], v33 offset:32768\n" \
"s_or_b32 s13, s22, 0x8000\n" \
"v_mfma_f32_16x16x32_bf16 a[48:51], v[164:167], v[120:123], a[48:51]\n" \
"ds_read_b128 v[56:59], v28 offset:34816\n" \
"s_add_i32 s22, s22, 0x14000\n" \
"v_mfma_f32_16x16x32_bf16 a[80:83], v[100:103], v[60:63], a[80:83]\n" \
"ds_read_b128 v[120:123], v29 offset:34816\n" \
"s_bitset1_b32 s23, 16\n" \
"s_nop 0\n" \
"v_mfma_f32_16x16x32_bf16 a[80:83], v[164:167], v[124:127], a[80:83]\n" \
"s_add_u32 m0, s30, 0x400\n" \
"s_mov_b32 s84, 0x2800\n" \
"v_mfma_f32_16x16x32_bf16 a[84:87], v[104:107], v[60:63], a[84:87]\n" \
"buffer_load_dwordx4 v25, s[76:79], s84 offen lds\n" \
"v_mfma_f32_16x16x32_bf16 a[84:87], v[168:171], v[124:127], a[84:87]\n" \
"ds_read_b64_tr_b16 v[90:91], v33 offset:33280\n" \
"s_or_b32 s24, s7, 0x5be8020\n" \
"v_mfma_f32_16x16x32_bf16 a[88:91], v[108:111], v[60:63], a[88:91]\n" \
"ds_read_b64_tr_b16 v[152:153], v33 offset:36864\n" \
"s_and_b32 s57, s27, 0xffff\n" \
"v_mfma_f32_16x16x32_bf16 a[88:91], v[172:175], v[124:127], a[88:91]\n" \
"s_add_u32 m0, s31, 0x0\n" \
"s_mov_b32 s85, 0x400000\n" \
"v_mfma_f32_16x16x32_bf16 a[92:95], v[112:115], v[60:63], a[92:95]\n" \
"buffer_load_dwordx4 v26, s[68:71], s85 offen lds\n" \
"v_mfma_f32_16x16x32_bf16 a[92:95], v[176:179], v[124:127], a[92:95]\n" \
"ds_read_b128 v[60:63], v28 offset:36864\n" \
"s_mov_b32 s58, 0x6000000\n" \
"v_mfma_f32_16x16x32_bf16 a[124:127], v[112:115], v[64:67], a[124:127]\n" \
"ds_read_b128 v[124:127], v29 offset:36864\n" \
"s_mov_b32 s56, s26\n" \
"v_mfma_f32_16x16x32_bf16 a[124:127], v[176:179], v[128:131], a[124:127]\n" \
"s_add_u32 m0, s30, 0x800\n" \
"s_mov_b32 s84, 0x4c00\n" \
"v_mfma_f32_16x16x32_bf16 a[120:123], v[108:111], v[64:67], a[120:123]\n" \
"buffer_load_dwordx4 v24, s[76:79], s84 offen lds\n" \
"v_mfma_f32_16x16x32_bf16 a[120:123], v[172:175], v[128:131], a[120:123]\n" \
"ds_read_b64_tr_b16 v[154:155], v33 offset:37376\n" \
"s_mov_b32 s59, s11\n" \
"v_mfma_f32_16x16x32_bf16 a[116:119], v[104:107], v[64:67], a[116:119]\n" \
"ds_read_b64_tr_b16 v[96:97], v35 offset:32768\n" \
"s_add_i32 s60, s60, 0x3000000\n" \
"v_mfma_f32_16x16x32_bf16 a[116:119], v[168:171], v[128:131], a[116:119]\n" \
"ds_read_b64_tr_b16 v[98:99], v35 offset:33280\n" \
"s_add_i32 s61, s61, 0x411c000\n" \
"v_mfma_f32_16x16x32_bf16 a[112:115], v[100:103], v[64:67], a[112:115]\n" \
"ds_read_b64_tr_b16 v[160:161], v35 offset:36864\n" \
"s_sub_i32 s19, 14, s18\n" \
"v_mfma_f32_16x16x32_bf16 a[112:115], v[164:167], v[128:131], a[112:115]\n" \
"ds_read_b128 v[64:67], v28 offset:38912\n" \
"s_mov_b32 s21, 0x71800000\n" \
"v_mfma_f32_16x16x32_bf16 a[144:147], v[100:103], v[68:71], a[144:147]\n" \
"ds_read_b128 v[128:131], v29 offset:38912\n" \
"v_mfma_f32_16x16x32_bf16 a[144:147], v[164:167], v[132:135], a[144:147]\n" \
"s_add_u32 m0, s30, 0xc00\n" \
"s_mov_b32 s85, 0x7000\n" \
"v_mfma_f32_16x16x32_bf16 a[148:151], v[104:107], v[68:71], a[148:151]\n" \
"buffer_load_dwordx4 v25, s[76:79], s85 offen lds\n" \
"v_mfma_f32_16x16x32_bf16 a[148:151], v[168:171], v[132:135], a[148:151]\n" \
"s_add_u32 m0, s31, 0x400\n" \
"s_mov_b32 s84, 0x408000\n" \
"v_mfma_f32_16x16x32_bf16 a[152:155], v[108:111], v[68:71], a[152:155]\n" \
"buffer_load_dwordx4 v26, s[68:71], s84 offen lds\n" \
"v_mfma_f32_16x16x32_bf16 a[152:155], v[172:175], v[132:135], a[152:155]\n" \
"ds_read_b64_tr_b16 v[162:163], v35 offset:37376\n" \
"v_mfma_f32_16x16x32_bf16 a[156:159], v[112:115], v[68:71], a[156:159]\n" \
"ds_read_b64_tr_b16 v[92:93], v34 offset:32768\n" \
"v_mfma_f32_16x16x32_bf16 a[156:159], v[176:179], v[132:135], a[156:159]\n" \
"ds_read_b128 v[68:71], v28 offset:40960\n" \
"v_mfma_f32_16x16x32_bf16 a[108:111], v[112:115], v[72:75], a[108:111]\n" \
"ds_read_b128 v[132:135], v29 offset:40960\n" \
"v_mfma_f32_16x16x32_bf16 a[108:111], v[176:179], v[136:139], a[108:111]\n" \
"s_add_u32 m0, s30, 0x1000\n" \
"s_mov_b32 s85, 0x9400\n" \
"v_mfma_f32_16x16x32_bf16 a[104:107], v[108:111], v[72:75], a[104:107]\n" \
"buffer_load_dwordx4 v24, s[76:79], s85 offen lds\n" \
"v_mfma_f32_16x16x32_bf16 a[104:107], v[172:175], v[136:139], a[104:107]\n" \
"s_add_u32 m0, s30, 0x1400\n" \
"s_mov_b32 s84, 0xb800\n" \
"v_mfma_f32_16x16x32_bf16 a[76:79], v[104:107], v[72:75], a[76:79]\n" \
"buffer_load_dwordx4 v25, s[76:79], s84 offen lds\n" \
"v_mfma_f32_16x16x32_bf16 a[76:79], v[168:171], v[136:139], a[76:79]\n" \
"ds_read_b64_tr_b16 v[94:95], v34 offset:33280\n" \
"v_mfma_f32_16x16x32_bf16 a[72:75], v[100:103], v[72:75], a[72:75]\n" \
"ds_read_b64_tr_b16 v[156:157], v34 offset:36864\n" \
"v_mfma_f32_16x16x32_bf16 a[72:75], v[164:167], v[136:139], a[72:75]\n" \
"ds_read_b128 v[72:75], v28 offset:43008\n" \
"v_mfma_f32_16x16x32_bf16 a[136:139], v[100:103], v[76:79], a[136:139]\n" \
"ds_read_b128 v[136:139], v29 offset:43008\n" \
"v_mfma_f32_16x16x32_bf16 a[136:139], v[164:167], v[140:143], a[136:139]\n" \
"s_add_u32 m0, s31, 0x800\n" \
"s_mov_b32 s85, 0x410000\n" \
"v_mfma_f32_16x16x32_bf16 a[140:143], v[104:107], v[76:79], a[140:143]\n" \
"buffer_load_dwordx4 v27, s[68:71], s85 offen lds\n" \
"v_mfma_f32_16x16x32_bf16 a[140:143], v[168:171], v[140:143], a[140:143]\n" \
"s_add_u32 m0, s30, 0x1800\n" \
"s_mov_b32 s84, 0xdc00\n" \
"v_mfma_f32_16x16x32_bf16 a[168:171], v[108:111], v[76:79], a[168:171]\n" \
"buffer_load_dwordx4 v24, s[76:79], s84 offen lds\n" \
"v_mfma_f32_16x16x32_bf16 a[168:171], v[172:175], v[140:143], a[168:171]\n" \
"ds_read_b64_tr_b16 v[158:159], v34 offset:37376\n" \
"v_mfma_f32_16x16x32_bf16 a[172:175], v[112:115], v[76:79], a[172:175]\n" \
"ds_read_b128 v[248:251], v22 offset:32768\n" \
"v_mfma_f32_16x16x32_bf16 a[172:175], v[176:179], v[140:143], a[172:175]\n" \
"ds_read_b128 v[76:79], v28 offset:45056\n" \
"v_mfma_f32_16x16x32_bf16 a[236:239], v[112:115], v[80:83], a[236:239]\n" \
"ds_read_b128 v[140:143], v29 offset:45056\n" \
"v_mfma_f32_16x16x32_bf16 a[236:239], v[176:179], v[144:147], a[236:239]\n" \
"ds_read_b128 v[48:51], v23 offset:32768\n" \
"v_mfma_f32_16x16x32_bf16 a[232:235], v[108:111], v[80:83], a[232:235]\n" \
"v_mfma_f32_16x16x32_bf16 a[232:235], v[172:175], v[144:147], a[232:235]\n" \
"v_mfma_f32_16x16x32_bf16 a[204:207], v[104:107], v[80:83], a[204:207]\n" \
"v_mfma_f32_16x16x32_bf16 a[204:207], v[168:171], v[144:147], a[204:207]\n" \
"v_mfma_f32_16x16x32_bf16 a[200:203], v[100:103], v[80:83], a[200:203]\n" \
"v_mfma_f32_16x16x32_bf16 a[200:203], v[164:167], v[144:147], a[200:203]\n" \
"ds_read_b128 v[80:83], v28 offset:47104\n" \
"ds_read_b128 v[144:147], v29 offset:47104\n" \
"s_waitcnt lgkmcnt(15)\n" \
"s_nop 0\n" \
"v_mfma_f32_16x16x32_bf16 a[0:3], v[84:87], v[52:55], a[0:3]\n" \
"s_add_u32 m0, s30, 0x1c00\n" \
"s_mov_b32 s84, 0x10000\n" \
"v_mfma_f32_16x16x32_bf16 a[0:3], v[148:151], v[116:119], a[0:3]\n" \
"buffer_load_dwordx4 v25, s[76:79], s84 offen lds\n" \
"v_mfma_f32_16x16x32_bf16 a[4:7], v[88:91], v[52:55], a[4:7]\n" \
"ds_read_b64_tr_b16 v[100:101], v36 offset:32768\n" \
"v_mfma_f32_16x16x32_bf16 a[4:7], v[152:155], v[116:119], a[4:7]\n" \
"s_add_u32 m0, s31, 0xc00\n" \
"s_mov_b32 s85, 0x418000\n" \
"v_mfma_f32_16x16x32_bf16 a[36:39], v[96:99], v[52:55], a[36:39]\n" \
"buffer_load_dwordx4 v27, s[68:71], s85 offen lds\n" \
"s_waitcnt lgkmcnt(15)\n" \
"v_mfma_f32_16x16x32_bf16 a[36:39], v[160:163], v[116:119], a[36:39]\n" \
"ds_read_b64_tr_b16 v[102:103], v36 offset:33792\n" \
"s_waitcnt lgkmcnt(12)\n" \
"v_mfma_f32_16x16x32_bf16 a[32:35], v[52:55], v[92:95], a[32:35]\n" \
"ds_read_b64_tr_b16 v[164:165], v36 offset:40960\n" \
"s_waitcnt lgkmcnt(9)\n" \
"s_nop 0\n" \
"v_mfma_f32_16x16x32_bf16 a[32:35], v[116:119], v[156:159], a[32:35]\n" \
"ds_read_b64_tr_b16 v[166:167], v36 offset:41984\n" \
"v_mfma_f32_16x16x32_bf16 a[96:99], v[56:59], v[92:95], a[96:99]\n" \
"ds_read_b64_tr_b16 v[104:105], v37 offset:32768\n" \
"v_mfma_f32_16x16x32_bf16 a[96:99], v[120:123], v[156:159], a[96:99]\n" \
"ds_read_b64_tr_b16 v[106:107], v37 offset:33792\n" \
"v_mfma_f32_16x16x32_bf16 a[100:103], v[96:99], v[56:59], a[100:103]\n" \
"ds_read_b64_tr_b16 v[168:169], v37 offset:40960\n" \
"v_mfma_f32_16x16x32_bf16 a[100:103], v[160:163], v[120:123], a[100:103]\n" \
"ds_read_b64_tr_b16 v[170:171], v37 offset:41984\n" \
".fill (L_pb + 36576 - .) / 4, 4, 0xBF800000\n" \
"L_pp9:\n" \
"s_cbranch_execz L_pret\n" \
"s_nop 0\n" \
"v_mfma_f32_16x16x32_bf16 a[68:71], v[88:91], v[56:59], a[68:71]\n" \
"v_mfma_f32_16x16x32_bf16 a[68:71], v[152:155], v[120:123], a[68:71]\n" \
"s_add_u32 s88, s42, 0x2000\n" \
"buffer_store_dwordx4 v[196:199], v20, s[80:83], s88 offen offset:3072 sc1\n" \
"v_mfma_f32_16x16x32_bf16 a[64:67], v[84:87], v[56:59], a[64:67]\n" \
"ds_read_b64_tr_b16 v[108:109], v38 offset:32768\n" \
"v_mfma_f32_16x16x32_bf16 a[64:67], v[148:151], v[120:123], a[64:67]\n" \
"ds_read_b64_tr_b16 v[110:111], v38 offset:33792\n" \
"v_mfma_f32_16x16x32_bf16 a[128:131], v[84:87], v[60:63], a[128:131]\n" \
"ds_read_b64_tr_b16 v[172:173], v38 offset:40960\n" \
"v_mfma_f32_16x16x32_bf16 a[128:131], v[148:151], v[124:127], a[128:131]\n" \
"ds_read_b64_tr_b16 v[174:175], v38 offset:41984\n" \
"v_mfma_f32_16x16x32_bf16 a[132:135], v[88:91], v[60:63], a[132:135]\n" \
"v_lshlrev_b32_e32 v45, 3, v20\n" \
"v_add_u32_e32 v45, s30, v45\n" \
"v_mfma_f32_16x16x32_bf16 a[132:135], v[152:155], v[124:127], a[132:135]\n" \
"s_add_u32 s89, s42, 0x3000\n" \
"buffer_store_dwordx4 v[200:203], v20, s[80:83], s89 offen offset:2048 sc1\n" \
"s_getpc_b64 s[86:87]\n" \
"L_ct0_%=:\n" \
"s_add_u32 s86, s86, L_ap0-L_ct0_%=\n" \
"s_addc_u32 s87, s87, 0\n" \
"s_mov_b32 m0, s32\n" \
"s_nop 0\n" \
"v_mfma_f32_16x16x32_bf16 a[164:167], v[96:99], v[60:63], a[164:167]\n" \
"global_load_lds_dword v45, s[86:87]\n" \
"v_mfma_f32_16x16x32_bf16 a[164:167], v[160:163], v[124:127], a[164:167]\n" \
"ds_read_b64_tr_b16 v[112:113], v39 offset:32768\n" \
"v_mfma_f32_16x16x32_bf16 a[160:163], v[60:63], v[92:95], a[160:163]\n" \
"ds_read_b64_tr_b16 v[114:115], v39 offset:33792\n" \
"v_mfma_f32_16x16x32_bf16 a[160:163], v[124:127], v[156:159], a[160:163]\n" \
"ds_read_b64_tr_b16 v[176:177], v39 offset:40960\n" \
"v_mfma_f32_16x16x32_bf16 a[224:227], v[64:67], v[92:95], a[224:227]\n" \
"ds_read_b64_tr_b16 v[178:179], v39 offset:41984\n" \
"v_mfma_f32_16x16x32_bf16 a[224:227], v[128:131], v[156:159], a[224:227]\n" \
"v_mfma_f32_16x16x32_bf16 a[228:231], v[96:99], v[64:67], a[228:231]\n" \
"s_add_u32 s88, s42, 0x3000\n" \
"buffer_store_dwordx4 v[204:207], v20, s[80:83], s88 offen offset:3072 sc1\n" \
"v_mfma_f32_16x16x32_bf16 a[228:231], v[160:163], v[128:131], a[228:231]\n" \
"v_mfma_f32_16x16x32_bf16 a[196:199], v[88:91], v[64:67], a[196:199]\n" \
"v_mfma_f32_16x16x32_bf16 a[196:199], v[152:155], v[128:131], a[196:199]\n" \
"v_mfma_f32_16x16x32_bf16 a[192:195], v[84:87], v[64:67], a[192:195]\n" \
"v_mfma_f32_16x16x32_bf16 a[192:195], v[148:151], v[128:131], a[192:195]\n" \
"s_waitcnt lgkmcnt(15)\n" \
"v_mfma_f32_16x16x32_bf16 a[8:11], v[88:91], v[248:251], a[8:11]\n" \
"v_mfma_f32_16x16x32_bf16 a[8:11], v[152:155], v[48:51], a[8:11]\n" \
"v_mfma_f32_16x16x32_bf16 a[40:43], v[96:99], v[248:251], a[40:43]\n" \
"v_mfma_f32_16x16x32_bf16 a[40:43], v[160:163], v[48:51], a[40:43]\n" \
"v_mfma_f32_16x16x32_bf16 a[12:15], v[248:251], v[92:95], a[12:15]\n" \
"v_mfma_f32_16x16x32_bf16 a[12:15], v[48:51], v[156:159], a[12:15]\n" \
"s_waitcnt vmcnt(4) lgkmcnt(0)\n" \
"s_barrier\n" \
"v_mfma_f32_16x16x32_bf16 a[16:19], v[100:103], v[52:55], a[16:19]\n" \
"ds_read_b64_tr_b16 v[84:85], v32 offset:0\n" \
"v_lshrrev_b32_e32 v24, 9, v20\n" \
"v_mfma_f32_16x16x32_bf16 a[16:19], v[164:167], v[116:119], a[16:19]\n" \
"v_and_b32_e32 v26, 0x1ff, v20\n" \
"v_lshl_add_u32 v24, v24, 3, v26\n" \
"v_mfma_f32_16x16x32_bf16 a[20:23], v[104:107], v[52:55], a[20:23]\n" \
"ds_read_b64_tr_b16 v[86:87], v32 offset:512\n" \
"v_lshrrev_b32_e64 v25, 8, v20\n" \
"v_mfma_f32_16x16x32_bf16 a[20:23], v[168:171], v[116:119], a[20:23]\n" \
"v_mul_u32_u24_e32 v25, 0x24700, v25\n" \
"v_add_u32_e64 v25, v25, v20\n" \
"v_mfma_f32_16x16x32_bf16 a[24:27], v[108:111], v[52:55], a[24:27]\n" \
"v_lshrrev_b32_e32 v26, 2, v20\n" \
"v_add_u32_e32 v26, s45, v26\n" \
"v_mfma_f32_16x16x32_bf16 a[24:27], v[172:175], v[116:119], a[24:27]\n" \
"ds_read_b64_tr_b16 v[148:149], v32 offset:4096\n" \
"v_add_u32_e64 v27, s46, v20\n" \
"v_mfma_f32_16x16x32_bf16 a[28:31], v[112:115], v[52:55], a[28:31]\n" \
"v_add_u32_e64 v21, s43, v27\n" \
"v_mfma_f32_16x16x32_bf16 a[28:31], v[176:179], v[116:119], a[28:31]\n" \
"ds_read_b128 v[52:55], v28 offset:0\n" \
"v_mfma_f32_16x16x32_bf16 a[60:63], v[112:115], v[56:59], a[60:63]\n" \
"ds_read_b128 v[116:119], v29 offset:0\n" \
"v_mfma_f32_16x16x32_bf16 a[60:63], v[176:179], v[120:123], a[60:63]\n" \
"ds_read_b64_tr_b16 v[150:151], v32 offset:4608\n" \
"v_mfma_f32_16x16x32_bf16 a[56:59], v[108:111], v[56:59], a[56:59]\n" \
"v_mfma_f32_16x16x32_bf16 a[56:59], v[172:175], v[120:123], a[56:59]\n" \
"ds_read_b64_tr_b16 v[88:89], v33 offset:0\n" \
"v_mfma_f32_16x16x32_bf16 a[52:55], v[104:107], v[56:59], a[52:55]\n" \
"v_mfma_f32_16x16x32_bf16 a[52:55], v[168:171], v[120:123], a[52:55]\n" \
"v_mfma_f32_16x16x32_bf16 a[48:51], v[100:103], v[56:59], a[48:51]\n" \
"ds_read_b64_tr_b16 v[90:91], v33 offset:512\n" \
"v_mfma_f32_16x16x32_bf16 a[48:51], v[164:167], v[120:123], a[48:51]\n" \
"ds_read_b128 v[56:59], v28 offset:2048\n" \
"v_mfma_f32_16x16x32_bf16 a[80:83], v[100:103], v[60:63], a[80:83]\n" \
"ds_read_b128 v[120:123], v29 offset:2048\n" \
"v_mfma_f32_16x16x32_bf16 a[80:83], v[164:167], v[124:127], a[80:83]\n" \
"ds_read_b64_tr_b16 v[152:153], v33 offset:4096\n" \
"v_mfma_f32_16x16x32_bf16 a[84:87], v[104:107], v[60:63], a[84:87]\n" \
"v_mfma_f32_16x16x32_bf16 a[84:87], v[168:171], v[124:127], a[84:87]\n" \
"v_mfma_f32_16x16x32_bf16 a[88:91], v[108:111], v[60:63], a[88:91]\n" \
"ds_read_b64_tr_b16 v[154:155], v33 offset:4608\n" \
"v_mfma_f32_16x16x32_bf16 a[88:91], v[172:175], v[124:127], a[88:91]\n" \
"v_mfma_f32_16x16x32_bf16 a[92:95], v[112:115], v[60:63], a[92:95]\n" \
"v_mfma_f32_16x16x32_bf16 a[92:95], v[176:179], v[124:127], a[92:95]\n" \
"ds_read_b128 v[60:63], v28 offset:4096\n" \
"v_mfma_f32_16x16x32_bf16 a[124:127], v[112:115], v[64:67], a[124:127]\n" \
"ds_read_b128 v[124:127], v29 offset:4096\n" \
"v_mfma_f32_16x16x32_bf16 a[124:127], v[176:179], v[128:131], a[124:127]\n" \
"ds_read_b64_tr_b16 v[96:97], v35 offset:0\n" \
"v_mfma_f32_16x16x32_bf16 a[120:123], v[108:111], v[64:67], a[120:123]\n" \
"ds_read_b64_tr_b16 v[98:99], v35 offset:512\n" \
"v_mfma_f32_16x16x32_bf16 a[120:123], v[172:175], v[128:131], a[120:123]\n" \
"v_mfma_f32_16x16x32_bf16 a[116:119], v[104:107], v[64:67], a[116:119]\n" \
"ds_read_b64_tr_b16 v[160:161], v35 offset:4096\n" \
"v_mfma_f32_16x16x32_bf16 a[116:119], v[168:171], v[128:131], a[116:119]\n" \
"v_mfma_f32_16x16x32_bf16 a[112:115], v[100:103], v[64:67], a[112:115]\n" \
"v_mfma_f32_16x16x32_bf16 a[112:115], v[164:167], v[128:131], a[112:115]\n" \
"ds_read_b128 v[64:67], v28 offset:6144\n" \
"v_mfma_f32_16x16x32_bf16 a[144:147], v[100:103], v[68:71], a[144:147]\n" \
"ds_read_b128 v[128:131], v29 offset:6144\n" \
"v_mfma_f32_16x16x32_bf16 a[144:147], v[164:167], v[132:135], a[144:147]\n" \
"ds_read_b64_tr_b16 v[162:163], v35 offset:4608\n" \
"v_mfma_f32_16x16x32_bf16 a[148:151], v[104:107], v[68:71], a[148:151]\n" \
"ds_read_b64_tr_b16 v[92:93], v34 offset:0\n" \
"v_mfma_f32_16x16x32_bf16 a[148:151], v[168:171], v[132:135], a[148:151]\n" \
"v_mfma_f32_16x16x32_bf16 a[152:155], v[108:111], v[68:71], a[152:155]\n" \
"v_mfma_f32_16x16x32_bf16 a[152:155], v[172:175], v[132:135], a[152:155]\n" \
"ds_read_b64_tr_b16 v[94:95], v34 offset:512\n" \
"v_mfma_f32_16x16x32_bf16 a[156:159], v[112:115], v[68:71], a[156:159]\n" \
"v_mfma_f32_16x16x32_bf16 a[156:159], v[176:179], v[132:135], a[156:159]\n" \
"ds_read_b128 v[68:71], v28 offset:8192\n" \
"v_mfma_f32_16x16x32_bf16 a[108:111], v[112:115], v[72:75], a[108:111]\n" \
"ds_read_b128 v[132:135], v29 offset:8192\n" \
"v_mfma_f32_16x16x32_bf16 a[108:111], v[176:179], v[136:139], a[108:111]\n" \
"ds_read_b64_tr_b16 v[156:157], v34 offset:4096\n" \
"v_mfma_f32_16x16x32_bf16 a[104:107], v[108:111], v[72:75], a[104:107]\n" \
"v_mfma_f32_16x16x32_bf16 a[104:107], v[172:175], v[136:139], a[104:107]\n" \
"ds_read_b64_tr_b16 v[158:159], v34 offset:4608\n" \
"v_mfma_f32_16x16x32_bf16 a[76:79], v[104:107], v[72:75], a[76:79]\n" \
"v_mfma_f32_16x16x32_bf16 a[76:79], v[168:171], v[136:139], a[76:79]\n" \
"v_mfma_f32_16x16x32_bf16 a[72:75], v[100:103], v[72:75], a[72:75]\n" \
"ds_read_b128 v[248:251], v22 offset:0\n" \
"v_mfma_f32_16x16x32_bf16 a[72:75], v[164:167], v[136:139], a[72:75]\n" \
"ds_read_b128 v[72:75], v28 offset:10240\n" \
"v_mfma_f32_16x16x32_bf16 a[136:139], v[100:103], v[76:79], a[136:139]\n" \
"ds_read_b128 v[136:139], v29 offset:10240\n" \
"v_mfma_f32_16x16x32_bf16 a[136:139], v[164:167], v[140:143], a[136:139]\n" \
"ds_read_b128 v[48:51], v23 offset:0\n" \
"v_mfma_f32_16x16x32_bf16 a[140:143], v[104:107], v[76:79], a[140:143]\n" \
"v_mfma_f32_16x16x32_bf16 a[140:143], v[168:171], v[140:143], a[140:143]\n" \
"v_mfma_f32_16x16x32_bf16 a[168:171], v[108:111], v[76:79], a[168:171]\n" \
"v_mfma_f32_16x16x32_bf16 a[168:171], v[172:175], v[140:143], a[168:171]\n" \
"v_mfma_f32_16x16x32_bf16 a[172:175], v[112:115], v[76:79], a[172:175]\n" \
"v_mfma_f32_16x16x32_bf16 a[172:175], v[176:179], v[140:143], a[172:175]\n" \
"ds_read_b128 v[76:79], v28 offset:12288\n" \
"v_mfma_f32_16x16x32_bf16 a[236:239], v[112:115], v[80:83], a[236:239]\n" \
"ds_read_b128 v[140:143], v29 offset:12288\n" \
"v_mfma_f32_16x16x32_bf16 a[236:239], v[176:179], v[144:147], a[236:239]\n" \
"v_mfma_f32_16x16x32_bf16 a[232:235], v[108:111], v[80:83], a[232:235]\n" \
"v_mfma_f32_16x16x32_bf16 a[232:235], v[172:175], v[144:147], a[232:235]\n" \
"v_mfma_f32_16x16x32_bf16 a[204:207], v[104:107], v[80:83], a[204:207]\n" \
"v_mfma_f32_16x16x32_bf16 a[204:207], v[168:171], v[144:147], a[204:207]\n" \
"v_mfma_f32_16x16x32_bf16 a[200:203], v[100:103], v[80:83], a[200:203]\n" \
"v_mfma_f32_16x16x32_bf16 a[200:203], v[164:167], v[144:147], a[200:203]\n" \
"ds_read_b128 v[80:83], v28 offset:14336\n" \
"ds_read_b128 v[144:147], v29 offset:14336\n" \
"s_waitcnt lgkmcnt(15)\n" \
"s_nop 0\n" \
"v_mfma_f32_16x16x32_bf16 a[0:3], v[84:87], v[52:55], a[0:3]\n" \
"v_bfe_u32 v44, v20, 2, 5\n" \
"ds_read_b64_tr_b16 v[100:101], v36 offset:0\n" \
"v_mfma_f32_16x16x32_bf16 a[0:3], v[148:151], v[116:119], a[0:3]\n" \
"buffer_load_dword v44, v44, s[80:83], s38 offen offset:0 sc1\n" \
"v_mfma_f32_16x16x32_bf16 a[4:7], v[88:91], v[52:55], a[4:7]\n" \
"ds_read_b64_tr_b16 v[102:103], v36 offset:1024\n" \
"v_mfma_f32_16x16x32_bf16 a[4:7], v[152:155], v[116:119], a[4:7]\n" \
"ds_read_b64_tr_b16 v[164:165], v36 offset:8192\n" \
"v_mfma_f32_16x16x32_bf16 a[36:39], v[96:99], v[52:55], a[36:39]\n" \
"ds_read_b64_tr_b16 v[166:167], v36 offset:9216\n" \
"s_nop 0\n" \
"v_accvgpr_read_b32 v184, a0\n" \
"s_waitcnt lgkmcnt(15)\n" \
"v_mfma_f32_16x16x32_bf16 a[36:39], v[160:163], v[116:119], a[36:39]\n" \
"v_accvgpr_read_b32 v185, a1\n" \
"v_accvgpr_read_b32 v186, a2\n" \
"v_mfma_f32_16x16x32_bf16 a[32:35], v[52:55], v[92:95], a[32:35]\n" \
"ds_read_b64_tr_b16 v[104:105], v37 offset:0\n" \
"v_accvgpr_read_b32 v187, a3\n" \
"s_waitcnt lgkmcnt(13)\n" \
"s_nop 0\n" \
"v_mfma_f32_16x16x32_bf16 a[32:35], v[116:119], v[156:159], a[32:35]\n" \
"v_accvgpr_read_b32 v188, a4\n" \
"v_accvgpr_read_b32 v189, a5\n" \
"v_mfma_f32_16x16x32_bf16 a[96:99], v[56:59], v[92:95], a[96:99]\n" \
"ds_read_b64_tr_b16 v[106:107], v37 offset:1024\n" \
"v_accvgpr_read_b32 v190, a6\n" \
"v_mfma_f32_16x16x32_bf16 a[96:99], v[120:123], v[156:159], a[96:99]\n" \
"ds_read_b64_tr_b16 v[168:169], v37 offset:8192\n" \
"v_accvgpr_read_b32 v191, a7\n" \
"v_mfma_f32_16x16x32_bf16 a[100:103], v[96:99], v[56:59], a[100:103]\n" \
"v_accvgpr_read_b32 v196, a36\n" \
"v_accvgpr_read_b32 v197, a37\n" \
"v_mfma_f32_16x16x32_bf16 a[100:103], v[160:163], v[120:123], a[100:103]\n" \
"ds_read_b64_tr_b16 v[170:171], v37 offset:9216\n" \
"v_accvgpr_read_b32 v198, a38\n" \
"v_mfma_f32_16x16x32_bf16 a[68:71], v[88:91], v[56:59], a[68:71]\n" \
"v_accvgpr_read_b32 v199, a39\n" \
"v_accvgpr_read_b32 v192, a32\n" \
"v_mfma_f32_16x16x32_bf16 a[68:71], v[152:155], v[120:123], a[68:71]\n" \
"ds_read_b64_tr_b16 v[108:109], v38 offset:0\n" \
"v_accvgpr_read_b32 v193, a33\n" \
"v_mfma_f32_16x16x32_bf16 a[64:67], v[84:87], v[56:59], a[64:67]\n" \
"ds_read_b64_tr_b16 v[110:111], v38 offset:1024\n" \
"v_accvgpr_read_b32 v194, a34\n" \
"v_mfma_f32_16x16x32_bf16 a[64:67], v[148:151], v[120:123], a[64:67]\n" \
"v_accvgpr_read_b32 v195, a35\n" \
"v_accvgpr_read_b32 v208, a96\n" \
"v_mfma_f32_16x16x32_bf16 a[128:131], v[84:87], v[60:63], a[128:131]\n" \
"ds_read_b64_tr_b16 v[172:173], v38 offset:8192\n" \
"v_accvgpr_read_b32 v209, a97\n" \
"v_mfma_f32_16x16x32_bf16 a[128:131], v[148:151], v[124:127], a[128:131]\n" \
"ds_read_b64_tr_b16 v[174:175], v38 offset:9216\n" \
"v_accvgpr_read_b32 v210, a98\n" \
"v_mfma_f32_16x16x32_bf16 a[132:135], v[88:91], v[60:63], a[132:135]\n" \
"v_accvgpr_read_b32 v211, a99\n" \
"v_accvgpr_read_b32 v212, a100\n" \
"v_mfma_f32_16x16x32_bf16 a[132:135], v[152:155], v[124:127], a[132:135]\n" \
"ds_read_b64_tr_b16 v[112:113], v39 offset:0\n" \
"v_accvgpr_read_b32 v213, a101\n" \
"v_mfma_f32_16x16x32_bf16 a[164:167], v[96:99], v[60:63], a[164:167]\n" \
"v_accvgpr_read_b32 v214, a102\n" \
"v_accvgpr_read_b32 v215, a103\n" \
"v_mfma_f32_16x16x32_bf16 a[164:167], v[160:163], v[124:127], a[164:167]\n" \
"ds_read_b64_tr_b16 v[114:115], v39 offset:1024\n" \
"v_accvgpr_read_b32 v204, a68\n" \
"v_mfma_f32_16x16x32_bf16 a[160:163], v[60:63], v[92:95], a[160:163]\n" \
"ds_read_b64_tr_b16 v[176:177], v39 offset:8192\n" \
"v_accvgpr_read_b32 v205, a69\n" \
"v_mfma_f32_16x16x32_bf16 a[160:163], v[124:127], v[156:159], a[160:163]\n" \
"v_accvgpr_read_b32 v206, a70\n" \
"v_accvgpr_read_b32 v207, a71\n" \
"v_mfma_f32_16x16x32_bf16 a[224:227], v[64:67], v[92:95], a[224:227]\n" \
"ds_read_b64_tr_b16 v[178:179], v39 offset:9216\n" \
"v_accvgpr_read_b32 v200, a64\n" \
"v_mfma_f32_16x16x32_bf16 a[224:227], v[128:131], v[156:159], a[224:227]\n" \
"v_accvgpr_read_b32 v201, a65\n" \
"v_accvgpr_read_b32 v202, a66\n" \
"v_mfma_f32_16x16x32_bf16 a[228:231], v[96:99], v[64:67], a[228:231]\n" \
"v_accvgpr_read_b32 v203, a67\n" \
"v_accvgpr_read_b32 v216, a128\n" \
"v_mfma_f32_16x16x32_bf16 a[228:231], v[160:163], v[128:131], a[228:231]\n" \
"v_accvgpr_read_b32 v217, a129\n" \
"v_accvgpr_read_b32 v218, a130\n" \
"v_mfma_f32_16x16x32_bf16 a[196:199], v[88:91], v[64:67], a[196:199]\n" \
"v_accvgpr_read_b32 v219, a131\n" \
"v_accvgpr_read_b32 v220, a132\n" \
"v_mfma_f32_16x16x32_bf16 a[196:199], v[152:155], v[128:131], a[196:199]\n" \
"v_accvgpr_read_b32 v221, a133\n" \
"v_accvgpr_read_b32 v222, a134\n" \
"v_mfma_f32_16x16x32_bf16 a[192:195], v[84:87], v[64:67], a[192:195]\n" \
"v_accvgpr_read_b32 v223, a135\n" \
"v_accvgpr_read_b32 v228, a164\n" \
"v_mfma_f32_16x16x32_bf16 a[192:195], v[148:151], v[128:131], a[192:195]\n" \
"v_accvgpr_read_b32 v229, a165\n" \
"v_accvgpr_read_b32 v230, a166\n" \
"s_waitcnt lgkmcnt(15)\n" \
"s_nop 0\n" \
"v_mfma_f32_16x16x32_bf16 a[8:11], v[88:91], v[248:251], a[8:11]\n" \
"v_accvgpr_read_b32 v231, a167\n" \
"v_accvgpr_read_b32 v224, a160\n" \
"v_mfma_f32_16x16x32_bf16 a[8:11], v[152:155], v[48:51], a[8:11]\n" \
"v_accvgpr_read_b32 v225, a161\n" \
"v_accvgpr_read_b32 v226, a162\n" \
"v_mfma_f32_16x16x32_bf16 a[40:43], v[96:99], v[248:251], a[40:43]\n" \
"v_accvgpr_read_b32 v227, a163\n" \
"v_accvgpr_read_b32 v240, a224\n" \
"v_mfma_f32_16x16x32_bf16 a[40:43], v[160:163], v[48:51], a[40:43]\n" \
"v_accvgpr_read_b32 v241, a225\n" \
"v_accvgpr_read_b32 v242, a226\n" \
"v_mfma_f32_16x16x32_bf16 a[12:15], v[248:251], v[92:95], a[12:15]\n" \
"v_accvgpr_read_b32 v243, a227\n" \
"v_accvgpr_read_b32 v244, a228\n" \
"v_mfma_f32_16x16x32_bf16 a[12:15], v[48:51], v[156:159], a[12:15]\n" \
"v_accvgpr_read_b32 v245, a229\n" \
"v_accvgpr_read_b32 v246, a230\n" \
"s_waitcnt vmcnt(0) lgkmcnt(0)\n" \
"s_barrier\n" \
"v_mov_b32_e32 v45, s62\n" \
"s_mov_b64 exec, 1\n" \
"buffer_store_dword v45, off, s[80:83], s37 offset:32 sc1\n" \
"s_mov_b64 exec, -1\n" \
"v_cmp_ne_u32_e32 vcc, s62, v44\n" \
"s_nop 4\n" \
"s_cbranch_vccnz L_poll160_%=\n" \
"L_pollr160_%=:\n" \
"v_mfma_f32_16x16x32_bf16 a[16:19], v[100:103], v[52:55], a[16:19]\n" \
"s_mov_b32 s73, s50\n" \
"s_sub_u32 m0, s31, 0x10000\n" \
"v_mfma_f32_16x16x32_bf16 a[16:19], v[164:167], v[116:119], a[16:19]\n" \
"buffer_load_dwordx4 v20, s[80:83], s73 offen offset:0 sc1 lds\n" \
"v_mfma_f32_16x16x32_bf16 a[20:23], v[104:107], v[52:55], a[20:23]\n" \
"s_add_u32 s74, s50, 0x4000\n" \
"s_add_u32 s75, s50, 0x8000\n" \
"v_mfma_f32_16x16x32_bf16 a[20:23], v[168:171], v[116:119], a[20:23]\n" \
"s_mov_b32 s70, 0x3e0293ee\n" \
"s_mov_b32 s76, s52\n" \
"v_mfma_f32_16x16x32_bf16 a[24:27], v[108:111], v[52:55], a[24:27]\n" \
"v_accvgpr_read_b32 v32, a16\n" \
"v_accvgpr_read_b32 v33, a17\n" \
"v_mfma_f32_16x16x32_bf16 a[24:27], v[172:175], v[116:119], a[24:27]\n" \
"buffer_load_dwordx4 v20, s[80:83], s73 offen offset:1024 sc1 lds\n" \
"v_mfma_f32_16x16x32_bf16 a[28:31], v[112:115], v[52:55], a[28:31]\n" \
"v_accvgpr_read_b32 v34, a18\n" \
"v_accvgpr_read_b32 v35, a19\n" \
"v_mfma_f32_16x16x32_bf16 a[28:31], v[176:179], v[116:119], a[28:31]\n" \
"v_mul_f32_e32 v32, s70, v32\n" \
"v_mul_f32_e32 v33, s70, v33\n" \
"v_mfma_f32_16x16x32_bf16 a[60:63], v[112:115], v[56:59], a[60:63]\n" \
"v_accvgpr_read_b32 v36, a20\n" \
"v_accvgpr_read_b32 v37, a21\n" \
"v_mfma_f32_16x16x32_bf16 a[60:63], v[176:179], v[120:123], a[60:63]\n" \
"buffer_load_dwordx4 v20, s[80:83], s73 offen offset:2048 sc1 lds\n" \
"v_mfma_f32_16x16x32_bf16 a[56:59], v[108:111], v[56:59], a[56:59]\n" \
"v_accvgpr_read_b32 v38, a22\n" \
"v_accvgpr_read_b32 v39, a23\n" \
"v_mfma_f32_16x16x32_bf16 a[56:59], v[172:175], v[120:123], a[56:59]\n" \
"v_mul_f32_e32 v34, s70, v34\n" \
"v_mul_f32_e32 v35, s70, v35\n" \
"v_mfma_f32_16x16x32_bf16 a[52:55], v[104:107], v[56:59], a[52:55]\n" \
"v_cvt_pk_bf16_f32 v32, v32, v33\n" \
"v_mul_f32_e64 v36, s70, v36\n" \
"v_mfma_f32_16x16x32_bf16 a[52:55], v[168:171], v[120:123], a[52:55]\n" \
"buffer_load_dwordx4 v20, s[80:83], s73 offen offset:3072 sc1 lds\n" \
"v_mfma_f32_16x16x32_bf16 a[48:51], v[100:103], v[56:59], a[48:51]\n" \
"s_sub_u32 m0, s31, s51\n" \
"v_mul_f32_e32 v37, s70, v37\n" \
"v_mfma_f32_16x16x32_bf16 a[48:51], v[164:167], v[120:123], a[48:51]\n" \
"v_mul_f32_e32 v38, s70, v38\n" \
"v_mul_f32_e32 v39, s70, v39\n" \
"v_mfma_f32_16x16x32_bf16 a[80:83], v[100:103], v[60:63], a[80:83]\n" \
"v_cvt_pk_bf16_f32 v33, v34, v35\n" \
"v_cvt_pk_bf16_f32 v34, v36, v37\n" \
"v_mfma_f32_16x16x32_bf16 a[80:83], v[164:167], v[124:127], a[80:83]\n" \
"buffer_load_dwordx4 v20, s[80:83], s74 offen offset:0 sc1 lds\n" \
"v_mfma_f32_16x16x32_bf16 a[84:87], v[104:107], v[60:63], a[84:87]\n" \
"v_cvt_pk_bf16_f32 v35, v38, v39\n" \
"v_accvgpr_read_b32 v36, a28\n" \
"v_mfma_f32_16x16x32_bf16 a[84:87], v[168:171], v[124:127], a[84:87]\n" \
"ds_write_b128 v27, v[32:35] offset:0\n" \
"v_mfma_f32_16x16x32_bf16 a[88:91], v[108:111], v[60:63], a[88:91]\n" \
"v_accvgpr_read_b32 v32, a24\n" \
"v_accvgpr_read_b32 v33, a25\n" \
"v_mfma_f32_16x16x32_bf16 a[88:91], v[172:175], v[124:127], a[88:91]\n" \
"buffer_load_dwordx4 v20, s[80:83], s74 offen offset:1024 sc1 lds\n" \
"v_mfma_f32_16x16x32_bf16 a[92:95], v[112:115], v[60:63], a[92:95]\n" \
"v_accvgpr_read_b32 v34, a26\n" \
"v_accvgpr_read_b32 v35, a27\n" \
"v_mfma_f32_16x16x32_bf16 a[92:95], v[176:179], v[124:127], a[92:95]\n" \
"v_mul_f32_e32 v32, s70, v32\n" \
"v_mul_f32_e32 v33, s70, v33\n" \
"v_mfma_f32_16x16x32_bf16 a[124:127], v[112:115], v[64:67], a[124:127]\n" \
"v_accvgpr_read_b32 v37, a29\n" \
"v_accvgpr_read_b32 v38, a30\n" \
"v_mfma_f32_16x16x32_bf16 a[124:127], v[176:179], v[128:131], a[124:127]\n" \
"buffer_load_dwordx4 v20, s[80:83], s74 offen offset:2048 sc1 lds\n" \
"v_mfma_f32_16x16x32_bf16 a[120:123], v[108:111], v[64:67], a[120:123]\n" \
"v_accvgpr_read_b32 v39, a31\n" \
"v_mul_f32_e64 v34, s70, v34\n" \
"v_mfma_f32_16x16x32_bf16 a[120:123], v[172:175], v[128:131], a[120:123]\n" \
"v_mul_f32_e64 v35, s70, v35\n" \
"v_cvt_pk_bf16_f32 v32, v32, v33\n" \
"v_mfma_f32_16x16x32_bf16 a[116:119], v[104:107], v[64:67], a[116:119]\n" \
"v_accvgpr_read_b32 v180, a8\n" \
"v_accvgpr_read_b32 v181, a9\n" \
"v_mfma_f32_16x16x32_bf16 a[116:119], v[168:171], v[128:131], a[116:119]\n" \
"buffer_load_dwordx4 v20, s[80:83], s74 offen offset:3072 sc1 lds\n" \
"v_mfma_f32_16x16x32_bf16 a[112:115], v[100:103], v[64:67], a[112:115]\n" \
"s_sub_u32 m0, s31, 0x4000\n" \
"v_mul_f32_e64 v36, s70, v36\n" \
"v_mfma_f32_16x16x32_bf16 a[112:115], v[164:167], v[128:131], a[112:115]\n" \
"v_mul_f32_e32 v37, s70, v37\n" \
"v_mul_f32_e32 v38, s70, v38\n" \
"v_mfma_f32_16x16x32_bf16 a[144:147], v[100:103], v[68:71], a[144:147]\n" \
"v_mul_f32_e64 v39, s70, v39\n" \
"v_cvt_pk_bf16_f32 v33, v34, v35\n" \
"v_mfma_f32_16x16x32_bf16 a[144:147], v[164:167], v[132:135], a[144:147]\n" \
"buffer_load_dwordx4 v20, s[80:83], s75 offen offset:0 sc1 lds\n" \
"v_mfma_f32_16x16x32_bf16 a[148:151], v[104:107], v[68:71], a[148:151]\n" \
"v_accvgpr_read_b32 v236, a196\n" \
"v_accvgpr_read_b32 v237, a197\n" \
"v_mfma_f32_16x16x32_bf16 a[148:151], v[168:171], v[132:135], a[148:151]\n" \
"v_accvgpr_read_b32 v232, a192\n" \
"v_accvgpr_read_b32 v233, a193\n" \
"v_mfma_f32_16x16x32_bf16 a[152:155], v[108:111], v[68:71], a[152:155]\n" \
"v_accvgpr_read_b32 v182, a10\n" \
"v_accvgpr_read_b32 v183, a11\n" \
"v_mfma_f32_16x16x32_bf16 a[152:155], v[172:175], v[132:135], a[152:155]\n" \
"buffer_load_dwordx4 v20, s[80:83], s75 offen offset:1024 sc1 lds\n" \
"v_mfma_f32_16x16x32_bf16 a[156:159], v[112:115], v[68:71], a[156:159]\n" \
"v_cvt_pk_bf16_f32 v180, v180, v181\n" \
"v_cvt_pk_bf16_f32 v184, v184, v185\n" \
"v_mfma_f32_16x16x32_bf16 a[156:159], v[176:179], v[132:135], a[156:159]\n" \
"v_cvt_pk_bf16_f32 v34, v36, v37\n" \
"v_cvt_pk_bf16_f32 v35, v38, v39\n" \
"v_mfma_f32_16x16x32_bf16 a[108:111], v[112:115], v[72:75], a[108:111]\n" \
"v_accvgpr_read_b32 v238, a198\n" \
"v_accvgpr_read_b32 v239, a199\n" \
"v_mfma_f32_16x16x32_bf16 a[108:111], v[176:179], v[136:139], a[108:111]\n" \
"buffer_load_dwordx4 v20, s[80:83], s75 offen offset:2048 sc1 lds\n" \
"v_mfma_f32_16x16x32_bf16 a[104:107], v[108:111], v[72:75], a[104:107]\n" \
"v_accvgpr_read_b32 v234, a194\n" \
"v_accvgpr_read_b32 v235, a195\n" \
"v_mfma_f32_16x16x32_bf16 a[104:107], v[172:175], v[136:139], a[104:107]\n" \
"v_accvgpr_read_b32 v50, a42\n" \
"v_accvgpr_read_b32 v51, a43\n" \
"v_mfma_f32_16x16x32_bf16 a[76:79], v[104:107], v[72:75], a[76:79]\n" \
"v_cvt_pk_bf16_f32 v181, v182, v183\n" \
"v_cvt_pk_bf16_f32 v188, v188, v189\n" \
"v_mfma_f32_16x16x32_bf16 a[76:79], v[168:171], v[136:139], a[76:79]\n" \
"buffer_load_dwordx4 v20, s[80:83], s75 offen offset:3072 sc1 lds\n" \
"v_mfma_f32_16x16x32_bf16 a[72:75], v[100:103], v[72:75], a[72:75]\n" \
"s_mov_b32 m0, s53\n" \
"s_nop 0\n" \
"v_cvt_pk_bf16_f32 v185, v186, v187\n" \
"v_mfma_f32_16x16x32_bf16 a[72:75], v[164:167], v[136:139], a[72:75]\n" \
"v_cvt_pk_bf16_f32 v204, v204, v205\n" \
"v_cvt_pk_bf16_f32 v200, v200, v201\n" \
"v_mfma_f32_16x16x32_bf16 a[136:139], v[100:103], v[76:79], a[136:139]\n" \
"v_cvt_pk_bf16_f32 v220, v220, v221\n" \
"v_cvt_pk_bf16_f32 v216, v216, v217\n" \
"v_mfma_f32_16x16x32_bf16 a[136:139], v[164:167], v[140:143], a[136:139]\n" \
"buffer_load_dwordx4 v20, s[80:83], s76 offen offset:0 sc1 lds\n" \
"v_mfma_f32_16x16x32_bf16 a[140:143], v[104:107], v[76:79], a[140:143]\n" \
"v_cvt_pk_bf16_f32 v236, v236, v237\n" \
"v_cvt_pk_bf16_f32 v232, v232, v233\n" \
"v_mfma_f32_16x16x32_bf16 a[140:143], v[168:171], v[140:143], a[140:143]\n" \
"ds_write_b128 v27, v[32:35] offset:1024\n" \
"v_accvgpr_read_b32 v247, a231\n" \
"v_cvt_pk_bf16_f32 v182, v50, v51\n" \
"v_mov_b32_e32 v183, 0x3f80\n" \
"v_cvt_pk_bf16_f32 v189, v190, v191\n" \
"v_cvt_pk_bf16_f32 v186, v196, v197\n" \
"buffer_load_dwordx4 v20, s[80:83], s76 offen offset:1024 sc1 lds\n" \
"v_mov_b32_e64 v187, 0\n" \
"v_cvt_pk_bf16_f32 v205, v206, v207\n" \
"v_cvt_pk_bf16_f32 v201, v202, v203\n" \
"v_cvt_pk_bf16_f32 v221, v222, v223\n" \
"v_cvt_pk_bf16_f32 v217, v218, v219\n" \
"v_cvt_pk_bf16_f32 v237, v238, v239\n" \
"v_cvt_pk_bf16_f32 v233, v234, v235\n" \
"v_cvt_pk_bf16_f32 v190, v198, v199\n" \
"v_mov_b32_e32 v191, 0x3f80\n" \
"v_cvt_pk_bf16_f32 v86, v192, v193\n" \
"v_cvt_pk_bf16_f32 v87, v194, v195\n" \
"v_cvt_pk_bf16_f32 v206, v214, v215\n" \
"v_mov_b32_e32 v207, 0x3f80\n" \
"v_cvt_pk_bf16_f32 v202, v212, v213\n" \
"v_mov_b32_e64 v203, 0\n" \
"v_cvt_pk_bf16_f32 v88, v208, v209\n" \
"v_cvt_pk_bf16_f32 v89, v210, v211\n" \
"v_cvt_pk_bf16_f32 v222, v230, v231\n" \
"v_mov_b32_e32 v223, 0x3f80\n" \
"v_cvt_pk_bf16_f32 v218, v228, v229\n" \
"v_mov_b32_e64 v219, 0\n" \
"v_cvt_pk_bf16_f32 v90, v224, v225\n" \
"v_cvt_pk_bf16_f32 v91, v226, v227\n" \
"v_cvt_pk_bf16_f32 v238, v246, v247\n" \
"v_mov_b32_e32 v239, 0x3f80\n" \
"v_cvt_pk_bf16_f32 v234, v244, v245\n" \
"v_mov_b32_e64 v235, 0\n" \
"v_cvt_pk_bf16_f32 v92, v240, v241\n" \
"v_cvt_pk_bf16_f32 v93, v242, v243\n" \
"v_mfma_f32_16x16x32_bf16 v[148:151], v[180:183], v[184:187], 0\n" \
"v_accvgpr_read_b32 v116, a48\n" \
"v_accvgpr_read_b32 v117, a49\n" \
"v_accvgpr_read_b32 v32, a56\n" \
"v_accvgpr_read_b32 v33, a57\n" \
"v_mfma_f32_16x16x32_bf16 a[168:171], v[108:111], v[76:79], a[168:171]\n" \
"v_mfma_f32_16x16x32_bf16 v[192:195], v[188:191], v[184:187], 0\n" \
"v_mfma_f32_16x16x32_bf16 v[208:211], v[204:207], v[200:203], 0\n" \
"v_mfma_f32_16x16x32_bf16 v[152:155], v[188:191], v[200:203], 0\n" \
"v_mfma_f32_16x16x32_bf16 v[224:227], v[220:223], v[216:219], 0\n" \
"v_mfma_f32_16x16x32_bf16 v[156:159], v[204:207], v[216:219], 0\n" \
"v_mfma_f32_16x16x32_bf16 v[240:243], v[236:239], v[232:235], 0\n" \
"v_mfma_f32_16x16x32_bf16 v[160:163], v[220:223], v[232:235], 0\n" \
"v_accvgpr_read_b32 v118, a50\n" \
"v_accvgpr_read_b32 v119, a51\n" \
"v_mul_f32_e32 v116, s70, v116\n" \
"v_mul_f32_e32 v117, s70, v117\n" \
"v_accvgpr_read_b32 v34, a58\n" \
"v_accvgpr_read_b32 v35, a59\n" \
"v_mul_f32_e32 v32, s70, v32\n" \
"v_mul_f32_e32 v33, s70, v33\n" \
"v_accvgpr_read_b32 v120, a52\n" \
"v_accvgpr_read_b32 v121, a53\n" \
"v_accvgpr_read_b32 v122, a54\n" \
"v_accvgpr_read_b32 v123, a55\n" \
"v_mul_f32_e32 v118, s70, v118\n" \
"v_mul_f32_e32 v119, s70, v119\n" \
"v_cvt_pk_bf16_f32 v116, v116, v117\n" \
"v_accvgpr_read_b32 v36, a60\n" \
"v_accvgpr_read_b32 v37, a61\n" \
"v_accvgpr_read_b32 v38, a62\n" \
"v_accvgpr_read_b32 v39, a63\n" \
"v_mul_f32_e32 v34, s70, v34\n" \
"v_mul_f32_e32 v35, s70, v35\n" \
"v_cvt_pk_bf16_f32 v32, v32, v33\n" \
"v_mul_f32_e32 v120, s70, v120\n" \
"v_mul_f32_e32 v121, s70, v121\n" \
"v_mul_f32_e32 v122, s70, v122\n" \
"v_mul_f32_e32 v123, s70, v123\n" \
"v_cvt_pk_bf16_f32 v117, v118, v119\n" \
"v_mul_f32_e32 v36, s70, v36\n" \
"v_mul_f32_e32 v37, s70, v37\n" \
"v_mul_f32_e32 v38, s70, v38\n" \
"v_mul_f32_e32 v39, s70, v39\n" \
"v_cvt_pk_bf16_f32 v33, v34, v35\n" \
"v_cvt_pk_bf16_f32 v118, v120, v121\n" \
"v_cvt_pk_bf16_f32 v119, v122, v123\n" \
"v_cvt_pk_bf16_f32 v34, v36, v37\n" \
"v_cvt_pk_bf16_f32 v35, v38, v39\n" \
"v_mfma_f32_16x16x32_bf16 a[168:171], v[172:175], v[140:143], a[168:171]\n" \
"ds_write_b128 v27, v[116:119] offset:4096\n" \
"ds_write_b128 v27, v[32:35] offset:5120\n" \
"v_accvgpr_read_b32 v116, a80\n" \
"v_accvgpr_read_b32 v117, a81\n" \
"v_accvgpr_read_b32 v32, a88\n" \
"v_accvgpr_read_b32 v33, a89\n" \
"v_accvgpr_read_b32 v124, a112\n" \
"v_accvgpr_read_b32 v125, a113\n" \
"v_accvgpr_read_b32 v118, a82\n" \
"v_accvgpr_read_b32 v119, a83\n" \
"v_mul_f32_e32 v116, s70, v116\n" \
"v_mul_f32_e32 v117, s70, v117\n" \
"v_accvgpr_read_b32 v34, a90\n" \
"v_accvgpr_read_b32 v35, a91\n" \
"v_mul_f32_e32 v32, s70, v32\n" \
"v_mul_f32_e32 v33, s70, v33\n" \
"v_accvgpr_read_b32 v126, a114\n" \
"v_accvgpr_read_b32 v127, a115\n" \
"v_mul_f32_e32 v124, s70, v124\n" \
"v_mul_f32_e32 v125, s70, v125\n" \
"v_mfma_f32_16x16x32_bf16 a[172:175], v[112:115], v[76:79], a[172:175]\n" \
"s_mov_b32 s64, 0xaaaaaaaa\n" \
"s_mov_b32 s65, 0xaaaaaaaa\n" \
"v_accvgpr_read_b32 v120, a84\n" \
"v_accvgpr_read_b32 v121, a85\n" \
"v_accvgpr_read_b32 v122, a86\n" \
"v_accvgpr_read_b32 v123, a87\n" \
"v_mul_f32_e32 v118, s70, v118\n" \
"v_mul_f32_e32 v119, s70, v119\n" \
"v_cvt_pk_bf16_f32 v116, v116, v117\n" \
"v_accvgpr_read_b32 v36, a92\n" \
".fill (L_pb + 40672 - .) / 4, 4, 0xBF800000\n" \
"L_pp10:\n" \
"s_cbranch_execz L_pret\n" \
"v_accvgpr_read_b32 v37, a93\n" \
"v_accvgpr_read_b32 v38, a94\n" \
"v_accvgpr_read_b32 v39, a95\n" \
"v_mul_f32_e32 v34, s70, v34\n" \
"v_mul_f32_e64 v35, s70, v35\n" \
"v_cvt_pk_bf16_f32 v32, v32, v33\n" \
"v_accvgpr_read_b32 v128, a116\n" \
"v_accvgpr_read_b32 v129, a117\n" \
"v_accvgpr_read_b32 v130, a118\n" \
"v_accvgpr_read_b32 v131, a119\n" \
"v_mul_f32_e32 v126, s70, v126\n" \
"v_mul_f32_e32 v127, s70, v127\n" \
"v_cvt_pk_bf16_f32 v124, v124, v125\n" \
"v_cndmask_b32_e64 v148, v148, v149, s[64:65]\n" \
"v_cndmask_b32_e64 v150, v150, v151, s[64:65]\n" \
"v_mul_f32_e32 v120, s70, v120\n" \
"v_mul_f32_e32 v121, s70, v121\n" \
"v_mul_f32_e32 v122, s70, v122\n" \
"v_mul_f32_e32 v123, s70, v123\n" \
"v_cvt_pk_bf16_f32 v117, v118, v119\n" \
"v_mul_f32_e32 v36, s70, v36\n" \
"v_mul_f32_e32 v37, s70, v37\n" \
"v_mul_f32_e32 v38, s70, v38\n" \
"v_mul_f32_e32 v39, s70, v39\n" \
"v_cvt_pk_bf16_f32 v33, v34, v35\n" \
"v_mul_f32_e32 v128, s70, v128\n" \
"v_mul_f32_e32 v129, s70, v129\n" \
"v_mul_f32_e32 v130, s70, v130\n" \
"v_mul_f32_e32 v131, s70, v131\n" \
"v_cvt_pk_bf16_f32 v125, v126, v127\n" \
"s_mov_b32 s66, 0xfff0ffff\n" \
"s_mov_b32 s67, 0xf000ff00\n" \
"s_mov_b32 s68, 0xffc0fffc\n" \
"s_mov_b32 s69, 0xc000fc00\n" \
"v_cndmask_b32_e64 v192, v192, v193, s[64:65]\n" \
"v_add_f32_e64 v148, s47, v148\n" \
"v_cndmask_b32_e64 v194, v194, v195, s[64:65]\n" \
"v_add_f32_e64 v150, s47, v150\n" \
"v_cndmask_b32_e64 v208, v208, v209, s[64:65]\n" \
"v_cndmask_b32_e64 v152, v152, v153, s[64:65]\n" \
"v_cndmask_b32_e64 v210, v210, v211, s[64:65]\n" \
"v_cndmask_b32_e64 v154, v154, v155, s[64:65]\n" \
"v_cndmask_b32_e64 v224, v224, v225, s[64:65]\n" \
"v_cndmask_b32_e64 v156, v156, v157, s[64:65]\n" \
"v_cndmask_b32_e64 v226, v226, v227, s[64:65]\n" \
"v_cndmask_b32_e64 v158, v158, v159, s[64:65]\n" \
"v_cndmask_b32_e64 v240, v240, v241, s[64:65]\n" \
"v_cndmask_b32_e64 v160, v160, v161, s[64:65]\n" \
"v_cndmask_b32_e64 v242, v242, v243, s[64:65]\n" \
"v_cndmask_b32_e64 v162, v162, v163, s[64:65]\n" \
"v_cvt_pk_bf16_f32 v118, v120, v121\n" \
"v_cvt_pk_bf16_f32 v119, v122, v123\n" \
"v_cvt_pk_bf16_f32 v34, v36, v37\n" \
"v_cvt_pk_bf16_f32 v35, v38, v39\n" \
"v_cvt_pk_bf16_f32 v126, v128, v129\n" \
"v_cvt_pk_bf16_f32 v127, v130, v131\n" \
"v_cndmask_b32_e64 v192, v148, v192, s[66:67]\n" \
"v_cndmask_b32_e64 v194, v150, v194, s[68:69]\n" \
"v_cndmask_b32_e64 v208, v152, v208, s[66:67]\n" \
"v_cndmask_b32_e64 v210, v154, v210, s[68:69]\n" \
"v_cndmask_b32_e64 v224, v156, v224, s[66:67]\n" \
"v_cndmask_b32_e64 v226, v158, v226, s[68:69]\n" \
"v_cndmask_b32_e64 v240, v160, v240, s[66:67]\n" \
"v_cndmask_b32_e64 v242, v162, v242, s[68:69]\n" \
"ds_write_b128 v27, v[116:119] offset:8192\n" \
"ds_write_b128 v27, v[32:35] offset:9216\n" \
"ds_write_b128 v27, v[124:127] offset:12288\n" \
"v_mfma_f32_16x16x32_bf16 a[172:175], v[176:179], v[140:143], a[172:175]\n" \
"v_max_f32_e32 v193, v192, v194\n" \
"v_max_f32_e32 v209, v208, v210\n" \
"v_max_f32_e32 v225, v224, v226\n" \
"v_max_f32_e32 v241, v240, v242\n" \
"s_mov_b32 s71, 0x3e0293ee\n" \
"v_permlane32_swap_b32_e32 v193, v225\n" \
"v_permlane32_swap_b32_e32 v209, v241\n" \
"v_accvgpr_read_b32 v116, a120\n" \
"v_accvgpr_read_b32 v117, a121\n" \
"v_accvgpr_read_b32 v32, a144\n" \
"v_accvgpr_read_b32 v33, a145\n" \
"v_accvgpr_read_b32 v124, a152\n" \
"v_accvgpr_read_b32 v125, a153\n" \
"v_accvgpr_read_b32 v132, a72\n" \
"v_accvgpr_read_b32 v133, a73\n" \
"v_permlane32_swap_b32_e32 v196, v228\n" \
"v_permlane32_swap_b32_e32 v212, v244\n" \
"v_max_f32_e32 v193, v193, v225\n" \
"v_max_f32_e32 v209, v209, v241\n" \
"v_accvgpr_read_b32 v118, a122\n" \
"v_accvgpr_read_b32 v119, a123\n" \
"v_mul_f32_e32 v116, s70, v116\n" \
"v_mul_f32_e32 v117, s70, v117\n" \
"v_accvgpr_read_b32 v34, a146\n" \
"v_accvgpr_read_b32 v35, a147\n" \
"v_pk_mul_f32 v[32:33], v[32:33], s[70:71]\n" \
"v_accvgpr_read_b32 v126, a154\n" \
"v_accvgpr_read_b32 v127, a155\n" \
"v_pk_mul_f32 v[124:125], v[124:125], s[70:71]\n" \
"v_accvgpr_read_b32 v134, a74\n" \
"v_accvgpr_read_b32 v135, a75\n" \
"v_pk_mul_f32 v[132:133], v[132:133], s[70:71]\n" \
"v_accvgpr_read_b32 v48, a40\n" \
"v_accvgpr_read_b32 v49, a41\n" \
"v_permlane16_swap_b32_e32 v228, v244\n" \
"v_permlane16_swap_b32_e32 v193, v209\n" \
"v_accvgpr_read_b32 v120, a124\n" \
"v_accvgpr_read_b32 v121, a125\n" \
"v_accvgpr_read_b32 v122, a126\n" \
"v_accvgpr_read_b32 v123, a127\n" \
"v_mul_f32_e32 v118, s70, v118\n" \
"v_mul_f32_e32 v119, s70, v119\n" \
"v_cvt_pk_bf16_f32 v116, v116, v117\n" \
"v_accvgpr_read_b32 v36, a148\n" \
"v_accvgpr_read_b32 v37, a149\n" \
"v_accvgpr_read_b32 v38, a150\n" \
"v_accvgpr_read_b32 v39, a151\n" \
"v_pk_mul_f32 v[34:35], v[34:35], s[70:71]\n" \
"v_cvt_pk_bf16_f32 v32, v32, v33\n" \
"v_accvgpr_read_b32 v128, a156\n" \
"v_accvgpr_read_b32 v129, a157\n" \
"v_accvgpr_read_b32 v130, a158\n" \
"v_accvgpr_read_b32 v131, a159\n" \
"v_pk_mul_f32 v[126:127], v[126:127], s[70:71]\n" \
"v_cvt_pk_bf16_f32 v124, v124, v125\n" \
"v_accvgpr_read_b32 v136, a76\n" \
"v_accvgpr_read_b32 v137, a77\n" \
"v_accvgpr_read_b32 v138, a78\n" \
"v_accvgpr_read_b32 v139, a79\n" \
"v_pk_mul_f32 v[134:135], v[134:135], s[70:71]\n" \
"v_cvt_pk_bf16_f32 v132, v132, v133\n" \
"v_mfma_f32_16x16x32_bf16 a[236:239], v[112:115], v[80:83], a[236:239]\n" \
"s_mov_b32 s72, 0x3eae1a93\n" \
"v_mul_f32_e32 v45, 0xbfb8aa3b, v228\n" \
"v_mfma_f32_16x16x16_bf16 v[48:51], v[182:183], v[46:47], 0\n" \
"v_mfma_f32_16x16x16_bf16 v[196:199], v[190:191], v[46:47], 0\n" \
"v_mfma_f32_16x16x16_bf16 v[212:215], v[206:207], v[46:47], 0\n" \
"v_max_f32_e32 v193, v193, v209\n" \
"v_mul_f32_e32 v120, s70, v120\n" \
"v_mul_f32_e32 v121, s70, v121\n" \
"v_mul_f32_e32 v122, s70, v122\n" \
"v_mul_f32_e64 v123, s70, v123\n" \
"v_cvt_pk_bf16_f32 v117, v118, v119\n" \
"v_pk_mul_f32 v[36:37], v[36:37], s[70:71]\n" \
"v_pk_mul_f32 v[38:39], v[38:39], s[70:71]\n" \
"v_cvt_pk_bf16_f32 v33, v34, v35\n" \
"v_pk_mul_f32 v[128:129], v[128:129], s[70:71]\n" \
"v_pk_mul_f32 v[130:131], v[130:131], s[70:71]\n" \
"v_cvt_pk_bf16_f32 v125, v126, v127\n" \
"v_pk_mul_f32 v[136:137], v[136:137], s[70:71]\n" \
"v_pk_mul_f32 v[138:139], v[138:139], s[70:71]\n" \
"v_cvt_pk_bf16_f32 v133, v134, v135\n" \
"v_mfma_f32_16x16x16_bf16 v[228:231], v[222:223], v[46:47], 0\n" \
"v_mul_f32_e64 v193, s72, v193\n" \
"v_cvt_pk_bf16_f32 v118, v120, v121\n" \
"v_cvt_pk_bf16_f32 v119, v122, v123\n" \
"v_cvt_pk_bf16_f32 v34, v36, v37\n" \
"v_cvt_pk_bf16_f32 v35, v38, v39\n" \
"v_cvt_pk_bf16_f32 v126, v128, v129\n" \
"v_cvt_pk_bf16_f32 v127, v130, v131\n" \
"v_cvt_pk_bf16_f32 v134, v136, v137\n" \
"v_cvt_pk_bf16_f32 v135, v138, v139\n" \
"v_mfma_f32_16x16x16_bf16 v[244:247], v[238:239], v[46:47], 0\n" \
"v_mov_b32_e64 v209, v193\n" \
"v_accvgpr_read_b32 v68, a104\n" \
"ds_write_b128 v27, v[116:119] offset:13312\n" \
"ds_write_b128 v21, v[32:35] offset:16384\n" \
"ds_write_b128 v21, v[124:127] offset:17408\n" \
"ds_write_b128 v21, v[132:135] offset:20480\n" \
"v_permlane16_swap_b32_e32 v193, v209\n" \
"v_mfma_f32_16x16x32_bf16 a[236:239], v[176:179], v[144:147], a[236:239]\n" \
"v_mov_b32_e32 v225, v193\n" \
"v_mov_b32_e64 v241, v209\n" \
"v_accvgpr_read_b32 v69, a105\n" \
"v_accvgpr_read_b32 v116, a136\n" \
"v_accvgpr_read_b32 v117, a137\n" \
"v_accvgpr_read_b32 v32, a168\n" \
"v_accvgpr_read_b32 v33, a169\n" \
"v_permlane32_swap_b32_e32 v193, v225\n" \
"v_permlane32_swap_b32_e32 v209, v241\n" \
"v_accvgpr_read_b32 v70, a106\n" \
"v_accvgpr_read_b32 v71, a107\n" \
"v_pk_mul_f32 v[68:69], v[68:69], s[70:71]\n" \
"v_accvgpr_read_b32 v118, a138\n" \
"v_accvgpr_read_b32 v119, a139\n" \
"v_pk_mul_f32 v[116:117], v[116:117], s[70:71]\n" \
"v_accvgpr_read_b32 v34, a170\n" \
"v_accvgpr_read_b32 v35, a171\n" \
"v_pk_mul_f32 v[32:33], v[32:33], s[70:71]\n" \
"v_fma_f32 v192, v192, s72, -v193\n" \
"v_fma_f32 v194, v194, s72, -v193\n" \
"v_fma_f32 v208, v208, s72, -v209\n" \
"v_fma_f32 v210, v210, s72, -v209\n" \
"v_accvgpr_read_b32 v72, a108\n" \
"v_accvgpr_read_b32 v73, a109\n" \
"v_accvgpr_read_b32 v74, a110\n" \
"v_accvgpr_read_b32 v75, a111\n" \
"v_pk_mul_f32 v[70:71], v[70:71], s[70:71]\n" \
"v_cvt_pk_bf16_f32 v68, v68, v69\n" \
"v_accvgpr_read_b32 v120, a140\n" \
"v_accvgpr_read_b32 v121, a141\n" \
"v_accvgpr_read_b32 v122, a142\n" \
"v_accvgpr_read_b32 v123, a143\n" \
"v_pk_mul_f32 v[118:119], v[118:119], s[70:71]\n" \
"v_cvt_pk_bf16_f32 v116, v116, v117\n" \
"v_accvgpr_read_b32 v36, a172\n" \
"v_accvgpr_read_b32 v37, a173\n" \
"v_accvgpr_read_b32 v38, a174\n" \
"v_accvgpr_read_b32 v39, a175\n" \
"v_pk_mul_f32 v[34:35], v[34:35], s[70:71]\n" \
"v_cvt_pk_bf16_f32 v32, v32, v33\n" \
"v_accvgpr_read_b32 v136, a236\n" \
"v_accvgpr_read_b32 v137, a237\n" \
"v_accvgpr_read_b32 v138, a238\n" \
"v_accvgpr_read_b32 v139, a239\n" \
"v_exp_f32_e32 v192, v192\n" \
"v_exp_f32_e32 v194, v194\n" \
"v_exp_f32_e32 v208, v208\n" \
"v_exp_f32_e32 v210, v210\n" \
"v_fma_f32 v224, v224, s72, -v225\n" \
"v_fma_f32 v226, v226, s72, -v225\n" \
"v_pk_mul_f32 v[72:73], v[72:73], s[70:71]\n" \
"v_pk_mul_f32 v[74:75], v[74:75], s[70:71]\n" \
"v_cvt_pk_bf16_f32 v69, v70, v71\n" \
"v_pk_mul_f32 v[120:121], v[120:121], s[70:71]\n" \
"v_pk_mul_f32 v[122:123], v[122:123], s[70:71]\n" \
"v_cvt_pk_bf16_f32 v117, v118, v119\n" \
"v_pk_mul_f32 v[36:37], v[36:37], s[70:71]\n" \
"v_pk_mul_f32 v[38:39], v[38:39], s[70:71]\n" \
"v_cvt_pk_bf16_f32 v33, v34, v35\n" \
"v_pk_mul_f32 v[136:137], v[136:137], s[70:71]\n" \
"v_pk_mul_f32 v[138:139], v[138:139], s[70:71]\n" \
"v_mfma_f32_16x16x32_bf16 a[232:235], v[108:111], v[80:83], a[232:235]\n" \
"v_exp_f32_e32 v224, v224\n" \
"v_exp_f32_e32 v226, v226\n" \
"v_fma_f32 v240, v240, s72, -v241\n" \
"v_fma_f32 v242, v242, s72, -v241\n" \
"v_cvt_pk_bf16_f32 v70, v72, v73\n" \
"v_cvt_pk_bf16_f32 v71, v74, v75\n" \
"v_cvt_pk_bf16_f32 v118, v120, v121\n" \
"v_cvt_pk_bf16_f32 v119, v122, v123\n" \
"v_cvt_pk_bf16_f32 v34, v36, v37\n" \
"v_cvt_pk_bf16_f32 v35, v38, v39\n" \
"v_cvt_pk_bf16_f32 v192, v192, v194\n" \
"v_cvt_pk_bf16_f32 v208, v208, v210\n" \
"v_exp_f32_e32 v240, v240\n" \
"v_exp_f32_e32 v242, v242\n" \
"ds_write_b128 v21, v[68:71] offset:21504\n" \
"ds_write_b128 v21, v[116:119] offset:24576\n" \
"ds_write_b128 v21, v[32:35] offset:25600\n" \
"v_cvt_pk_bf16_f32 v52, v48, v49\n" \
"v_cvt_pk_bf16_f32 v53, v50, v51\n" \
"v_cvt_pk_bf16_f32 v54, v196, v197\n" \
"v_cvt_pk_bf16_f32 v55, v198, v199\n" \
"v_cvt_pk_bf16_f32 v56, v212, v213\n" \
"v_cvt_pk_bf16_f32 v57, v214, v215\n" \
"v_perm_b32 v184, v192, v192, v40\n" \
"v_perm_b32 v185, v192, v192, v41\n" \
"v_perm_b32 v186, v192, v192, v42\n" \
"v_perm_b32 v187, v192, v192, v43\n" \
"v_perm_b32 v200, v208, v208, v40\n" \
"v_perm_b32 v201, v208, v208, v41\n" \
"v_perm_b32 v202, v208, v208, v42\n" \
"v_perm_b32 v203, v208, v208, v43\n" \
"v_cvt_pk_bf16_f32 v224, v224, v226\n" \
"v_cvt_pk_bf16_f32 v58, v228, v229\n" \
"v_cvt_pk_bf16_f32 v59, v230, v231\n" \
"v_mfma_f32_16x16x32_bf16 v[148:151], v[52:55], v[184:187], 0\n" \
"v_mfma_f32_16x16x32_bf16 v[152:155], v[54:57], v[200:203], 0\n" \
"v_perm_b32 v216, v224, v224, v40\n" \
"v_perm_b32 v217, v224, v224, v41\n" \
"v_perm_b32 v218, v224, v224, v42\n" \
"v_perm_b32 v219, v224, v224, v43\n" \
"v_cvt_pk_bf16_f32 v240, v240, v242\n" \
"v_mfma_f32_16x16x32_bf16 a[232:235], v[172:175], v[144:147], a[232:235]\n" \
"v_cvt_pk_bf16_f32 v60, v244, v245\n" \
"v_cvt_pk_bf16_f32 v61, v246, v247\n" \
"v_mfma_f32_16x16x32_bf16 v[156:159], v[56:59], v[216:219], 0\n" \
"v_perm_b32 v232, v240, v240, v40\n" \
"v_perm_b32 v233, v240, v240, v41\n" \
"v_perm_b32 v234, v240, v240, v42\n" \
"v_perm_b32 v235, v240, v240, v43\n" \
"v_mfma_f32_16x16x32_bf16 a[204:207], v[104:107], v[80:83], a[204:207]\n" \
"v_accvgpr_read_b32 v132, a232\n" \
"v_accvgpr_read_b32 v133, a233\n" \
"v_accvgpr_read_b32 v134, a234\n" \
"v_accvgpr_read_b32 v135, a235\n" \
"v_pk_mul_f32 v[132:133], v[132:133], s[70:71]\n" \
"v_pk_mul_f32 v[134:135], v[134:135], s[70:71]\n" \
"v_cvt_pk_bf16_f32 v132, v132, v133\n" \
"v_cvt_pk_bf16_f32 v133, v134, v135\n" \
"v_cvt_pk_bf16_f32 v134, v136, v137\n" \
"v_cvt_pk_bf16_f32 v135, v138, v139\n" \
"ds_write_b128 v21, v[132:135] offset:29696\n" \
"v_mfma_f32_16x16x32_bf16 v[160:163], v[58:61], v[232:235], 0\n" \
"v_accvgpr_read_b32 v252, a12\n" \
"v_accvgpr_read_b32 v253, a13\n" \
"v_accvgpr_read_b32 v254, a14\n" \
"v_accvgpr_read_b32 v255, a15\n" \
"v_mfma_f32_16x16x32_bf16 a[204:207], v[168:171], v[144:147], a[204:207]\n" \
"v_cvt_pk_bf16_f32 v84, v252, v253\n" \
"v_cvt_pk_bf16_f32 v85, v254, v255\n" \
"v_exp_f32_e64 v45, v45\n" \
"v_mfma_f32_16x16x32_bf16 v[208:211], v[86:89], v[200:203], 0\n" \
"v_mfma_f32_16x16x32_bf16 v[192:195], v[84:87], v[184:187], 0\n" \
"v_mfma_f32_16x16x32_bf16 v[224:227], v[88:91], v[216:219], 0\n" \
"v_mfma_f32_16x16x32_bf16 v[240:243], v[90:93], v[232:235], 0\n" \
"v_mov_b32_dpp v150, v154 quad_perm:[0,1,2,3] row_mask:0x2 bank_mask:0xf\n" \
"v_accvgpr_read_b32 v128, a204\n" \
"v_accvgpr_read_b32 v129, a205\n" \
"v_accvgpr_read_b32 v130, a206\n" \
"v_accvgpr_read_b32 v131, a207\n" \
"v_pk_mul_f32 v[128:129], v[128:129], s[70:71]\n" \
"v_pk_mul_f32 v[130:131], v[130:131], s[70:71]\n" \
"v_mov_b32_dpp v150, v158 quad_perm:[0,1,2,3] row_mask:0x4 bank_mask:0xf\n" \
"v_add_f32_e64 v45, 1.0, v45\n" \
"v_mfma_f32_16x16x32_bf16 a[200:203], v[100:103], v[80:83], a[200:203]\n" \
"v_mov_b32_dpp v150, v162 quad_perm:[0,1,2,3] row_mask:0x8 bank_mask:0xf\n" \
"v_mov_b32_dpp v148, v152 quad_perm:[0,1,2,3] row_mask:0x2 bank_mask:0xf\n" \
"v_rcp_f32_e64 v45, v45\n" \
"v_mov_b32_dpp v149, v153 quad_perm:[0,1,2,3] row_mask:0x2 bank_mask:0xf\n" \
"v_rcp_f32_e64 v150, v150\n" \
"v_mov_b32_dpp v148, v156 quad_perm:[0,1,2,3] row_mask:0x4 bank_mask:0xf\n" \
"v_mov_b32_dpp v149, v157 quad_perm:[0,1,2,3] row_mask:0x4 bank_mask:0xf\n" \
"v_mul_f32_e64 v150, v45, v150\n" \
"v_mov_b32_dpp v148, v160 quad_perm:[0,1,2,3] row_mask:0x8 bank_mask:0xf\n" \
"v_mov_b32_dpp v149, v161 quad_perm:[0,1,2,3] row_mask:0x8 bank_mask:0xf\n" \
"v_mul_f32_e32 v148, v150, v148\n" \
"v_mul_f32_e32 v149, v150, v149\n" \
"v_mov_b32_e64 v154, v150\n" \
"v_mfma_f32_16x16x32_bf16 a[200:203], v[164:167], v[144:147], a[200:203]\n" \
"v_sub_f32_e32 v44, 1.0, v45\n" \
"v_permlane16_swap_b32_e32 v150, v154\n" \
"v_mov_b32_e32 v158, v150\n" \
"v_mov_b32_e32 v162, v154\n" \
"v_cvt_pk_bf16_f32 v148, v148, v149\n" \
"v_permlane32_swap_b32_e32 v150, v158\n" \
"v_permlane32_swap_b32_e32 v154, v162\n" \
"v_mul_f32_e64 v192, v150, v192\n" \
"v_accvgpr_read_b32 v124, a200\n" \
"v_accvgpr_read_b32 v125, a201\n" \
"v_accvgpr_read_b32 v126, a202\n" \
"v_accvgpr_read_b32 v127, a203\n" \
"v_pk_mul_f32 v[124:125], v[124:125], s[70:71]\n" \
"v_pk_mul_f32 v[126:127], v[126:127], s[70:71]\n" \
"v_cvt_pk_bf16_f32 v124, v124, v125\n" \
"v_cvt_pk_bf16_f32 v125, v126, v127\n" \
"v_cvt_pk_bf16_f32 v126, v128, v129\n" \
"v_cvt_pk_bf16_f32 v127, v130, v131\n" \
"ds_write_b128 v21, v[124:127] offset:28672\n" \
"v_mul_f32_e32 v193, v150, v193\n" \
"v_mul_f32_e32 v208, v154, v208\n" \
"v_mul_f32_e32 v209, v154, v209\n" \
"v_mul_f32_e32 v224, v158, v224\n" \
"v_mul_f32_e32 v225, v158, v225\n" \
"v_mul_f32_e32 v240, v162, v240\n" \
"v_mul_f32_e32 v241, v162, v241\n" \
"v_mul_f32_e32 v194, v150, v194\n" \
"v_mul_f32_e64 v195, v150, v195\n" \
"v_cvt_pk_bf16_f32 v192, v192, v193\n" \
"v_mul_f32_e32 v210, v154, v210\n" \
"v_mul_f32_e32 v211, v154, v211\n" \
"v_cvt_pk_bf16_f32 v208, v208, v209\n" \
"v_mul_f32_e32 v226, v158, v226\n" \
"v_mul_f32_e32 v227, v158, v227\n" \
"v_cvt_pk_bf16_f32 v224, v224, v225\n" \
"v_mul_f32_e32 v242, v162, v242\n" \
"v_mul_f32_e32 v243, v162, v243\n" \
"v_cvt_pk_bf16_f32 v240, v240, v241\n" \
"v_cvt_pk_bf16_f32 v193, v194, v195\n" \
"s_add_u32 s84, s48, 0x0\n" \
"s_nop 0\n" \
"v_cvt_pk_bf16_f32 v209, v210, v211\n" \
"s_add_u32 s85, s48, 0x24800\n" \
"v_cvt_pk_bf16_f32 v225, v226, v227\n" \
"s_add_u32 s86, s48, 0x49000\n" \
"v_cvt_pk_bf16_f32 v241, v242, v243\n" \
"s_add_u32 s87, s48, 0x6d800\n" \
"ds_write_b32 v26, v44\n" \
"buffer_store_dword v148, v25, s[80:83], s49 offen sc1\n" \
"buffer_store_dwordx2 v[192:193], v24, s[80:83], s84 offen sc1\n" \
"buffer_store_dwordx2 v[208:209], v24, s[80:83], s85 offen sc1\n" \
"buffer_store_dwordx2 v[224:225], v24, s[80:83], s86 offen sc1\n" \
"buffer_store_dwordx2 v[240:241], v24, s[80:83], s87 offen sc1\n" \
"s_branch L_end_%=\n" \
"L_poll60_%=:\n" \
"s_mov_b32 s84, 0\n" \
"L_pl60_%=:\n" \
"s_sleep 1\n" \
"v_bfe_u32 v44, v20, 2, 5\n" \
"buffer_load_dword v44, v44, s[80:83], s38 offen offset:128 sc1\n" \
"s_waitcnt vmcnt(0)\n" \
"v_cmp_ne_u32_e32 vcc, s62, v44\n" \
"s_add_u32 s84, s84, 1\n" \
"s_nop 3\n" \
"s_cbranch_vccz L_pollr60_%=\n" \
"s_cmp_gt_u32 s84, 0x400000\n" \
"s_cbranch_scc0 L_pl60_%=\n" \
"s_branch L_pollr60_%=\n" \
"L_poll160_%=:\n" \
"s_mov_b32 s84, 0\n" \
"L_pl160_%=:\n" \
"s_sleep 1\n" \
"v_bfe_u32 v44, v20, 2, 5\n" \
"buffer_load_dword v44, v44, s[80:83], s38 offen offset:0 sc1\n" \
"s_waitcnt vmcnt(0)\n" \
"v_cmp_ne_u32_e32 vcc, s62, v44\n" \
"s_add_u32 s84, s84, 1\n" \
"s_nop 3\n" \
"s_cbranch_vccz L_pollr160_%=\n" \
"s_cmp_gt_u32 s84, 0x400000\n" \
"s_cbranch_scc0 L_pl160_%=\n" \
"s_branch L_pollr160_%=\n" \
".fill (L_pb + 46868 - .) / 4, 4, 0xBF800000\n" \
"L_w1_%=:\n" \
"s_waitcnt vmcnt(18)\n" \
"s_barrier\n" \
"s_nop 0\n" \
"ds_read_b64_tr_b16 v[84:85], v32 offset:0\n" \
"ds_read_b64_tr_b16 v[86:87], v32 offset:1024\n" \
"ds_read_b64_tr_b16 v[148:149], v32 offset:8192\n" \
"ds_read_b64_tr_b16 v[150:151], v32 offset:9216\n" \
"ds_read_b128 v[52:55], v28 offset:16384\n" \
"ds_read_b128 v[116:119], v29 offset:16384\n" \
"ds_read_b64_tr_b16 v[88:89], v33 offset:0\n" \
"ds_read_b64_tr_b16 v[90:91], v33 offset:1024\n" \
"ds_read_b64_tr_b16 v[152:153], v33 offset:8192\n" \
"ds_read_b64_tr_b16 v[154:155], v33 offset:9216\n" \
"ds_read_b64_tr_b16 v[92:93], v34 offset:0\n" \
"ds_read_b64_tr_b16 v[94:95], v34 offset:1024\n" \
"ds_read_b64_tr_b16 v[156:157], v34 offset:8192\n" \
"ds_read_b64_tr_b16 v[158:159], v34 offset:9216\n" \
"ds_read_b64_tr_b16 v[96:97], v35 offset:0\n" \
"ds_read_b64_tr_b16 v[98:99], v35 offset:1024\n" \
"ds_read_b64_tr_b16 v[160:161], v35 offset:8192\n" \
"ds_read_b64_tr_b16 v[162:163], v35 offset:9216\n" \
"ds_read_b128 v[56:59], v28 offset:18432\n" \
"ds_read_b128 v[120:123], v29 offset:18432\n" \
"ds_read_b128 v[60:63], v28 offset:20480\n" \
"ds_read_b128 v[124:127], v29 offset:20480\n" \
"ds_read_b128 v[64:67], v28 offset:22528\n" \
"ds_read_b128 v[128:131], v29 offset:22528\n" \
"ds_read_b128 v[68:71], v28 offset:0\n" \
"ds_read_b128 v[132:135], v29 offset:0\n" \
"ds_read_b128 v[72:75], v28 offset:2048\n" \
"ds_read_b128 v[136:139], v29 offset:2048\n" \
"ds_read_b128 v[76:79], v28 offset:4096\n" \
"ds_read_b128 v[140:143], v29 offset:4096\n" \
"ds_read_b128 v[80:83], v28 offset:6144\n" \
"ds_read_b128 v[144:147], v29 offset:6144\n" \
"s_waitcnt lgkmcnt(15)\n" \
"v_mfma_f32_16x16x32_bf16 a[0:3], v[52:55], v[84:87], 0\n" \
"ds_read_b64_tr_b16 v[100:101], v36 offset:0\n" \
"v_mad_i32_i24 v217, v216, -6, v215\n" \
"v_mfma_f32_16x16x32_bf16 a[0:3], v[116:119], v[148:151], a[0:3]\n" \
"v_add_u32_e32 v218, 0xfffffe50, v215\n" \
"s_mul_i32 s35, s6, 0x48\n" \
"v_mfma_f32_16x16x32_bf16 a[4:7], v[52:55], v[88:91], 0\n" \
"v_lshrrev_b32_e32 v219, 1, v217\n" \
"v_add_u32_e64 v218, s35, v216\n" \
"v_mfma_f32_16x16x32_bf16 a[4:7], v[116:119], v[152:155], a[4:7]\n" \
"ds_read_b64_tr_b16 v[102:103], v36 offset:1024\n" \
"v_mul_u32_u24_e32 v219, 0x480, v219\n" \
"v_mfma_f32_16x16x32_bf16 a[8:11], v[52:55], v[92:95], 0\n" \
"v_and_b32_e64 v214, 16, v20\n" \
"v_mad_u32_u24 v219, v218, s5, v219\n" \
"v_mfma_f32_16x16x32_bf16 a[8:11], v[116:119], v[156:159], a[8:11]\n" \
"v_or_b32_e32 v192, v219, v214\n" \
"v_mov_b32_e32 v220, 2.0\n" \
"v_mfma_f32_16x16x32_bf16 a[12:15], v[52:55], v[96:99], 0\n" \
"ds_read_b64_tr_b16 v[164:165], v36 offset:8192\n" \
"v_mov_b32_e32 v222, 7\n" \
"s_waitcnt lgkmcnt(15)\n" \
"v_mfma_f32_16x16x32_bf16 a[12:15], v[116:119], v[160:163], a[12:15]\n" \
"v_lshlrev_b32_e32 v220, 7, v218\n" \
"v_lshrrev_b32_e32 v218, 1, v218\n" \
"v_mfma_f32_16x16x32_bf16 a[44:47], v[56:59], v[96:99], 0\n" \
"v_and_b32_e32 v218, 4, v218\n" \
"v_and_b32_e32 v216, 2, v216\n" \
"v_mfma_f32_16x16x32_bf16 a[44:47], v[120:123], v[160:163], a[44:47]\n" \
"ds_read_b64_tr_b16 v[166:167], v36 offset:9216\n" \
"s_mul_i32 s7, s6, 0x2400\n" \
"v_mfma_f32_16x16x32_bf16 a[40:43], v[56:59], v[92:95], 0\n" \
"s_sub_i32 s34, 0x22000, s7\n" \
"v_bitop3_b32 v216, v217, v218, v216 bitop3:0x1e\n" \
"v_mfma_f32_16x16x32_bf16 a[40:43], v[120:123], v[156:159], a[40:43]\n" \
"v_or_b32_e32 v213, 0x24400, v20\n" \
"v_lshlrev_b32_e64 v216, 4, v216\n" \
"v_mfma_f32_16x16x32_bf16 a[36:39], v[56:59], v[88:91], 0\n" \
"v_add3_u32 v196, v220, s34, v216\n" \
"v_mov_b32_e64 v216, v213\n" \
"v_mfma_f32_16x16x32_bf16 a[36:39], v[120:123], v[152:155], a[36:39]\n" \
"ds_read_b64_tr_b16 v[104:105], v37 offset:0\n" \
"s_movk_i32 s36, 0x1b0\n" \
"v_mfma_f32_16x16x32_bf16 a[32:35], v[56:59], v[84:87], 0\n" \
"v_or_b32_e32 v216, 0x100, v215\n" \
"v_mul_u32_u24_e32 v217, 0x2aab, v216\n" \
"v_mfma_f32_16x16x32_bf16 a[32:35], v[120:123], v[148:151], a[32:35]\n" \
"v_add_u32_e32 v215, 0xffffff50, v215\n" \
"v_lshrrev_b32_e32 v217, 16, v217\n" \
"s_waitcnt lgkmcnt(15)\n" \
"v_mfma_f32_16x16x32_bf16 a[64:67], v[60:63], v[84:87], 0\n" \
"s_mov_b32 m0, s32\n" \
"ds_read_b64_tr_b16 v[106:107], v37 offset:1024\n" \
"v_mfma_f32_16x16x32_bf16 a[64:67], v[124:127], v[148:151], a[64:67]\n" \
"s_mov_b32 exec_lo, 0xff000000\n" \
"s_mov_b32 exec_hi, 0x0\n" \
"global_load_lds_dword v[184:185], off\n" \
"s_mov_b64 exec, -1\n" \
"v_mfma_f32_16x16x32_bf16 a[68:71], v[60:63], v[88:91], 0\n" \
"v_mov_b32_e64 v220, v215\n" \
"v_mad_i32_i24 v218, v217, -6, v216\n" \
"v_mfma_f32_16x16x32_bf16 a[68:71], v[124:127], v[152:155], a[68:71]\n" \
"ds_read_b64_tr_b16 v[168:169], v37 offset:8192\n" \
"v_cmp_gt_u32_e64 vcc, s36, v216\n" \
"v_mfma_f32_16x16x32_bf16 a[72:75], v[60:63], v[92:95], 0\n" \
"v_mov_b32_e32 v221, 7\n" \
"v_lshrrev_b32_e32 v222, 1, v218\n" \
"v_mfma_f32_16x16x32_bf16 a[72:75], v[124:127], v[156:159], a[72:75]\n" \
"s_mov_b32 m0, s32\n" \
"v_cndmask_b32_e32 v217, v220, v217, vcc\n" \
"v_mfma_f32_16x16x32_bf16 a[76:79], v[60:63], v[96:99], 0\n" \
"s_mov_b32 exec_lo, 0xffff\n" \
"s_mov_b32 exec_hi, 0x0\n" \
"global_load_lds_dword v[186:187], off\n" \
"s_mov_b64 exec, -1\n" \
"v_mfma_f32_16x16x32_bf16 a[76:79], v[124:127], v[160:163], a[76:79]\n" \
"ds_read_b64_tr_b16 v[170:171], v37 offset:9216\n" \
"s_movk_i32 s4, 0x48\n" \
"s_waitcnt lgkmcnt(15)\n" \
"v_mfma_f32_16x16x32_bf16 a[108:111], v[64:67], v[96:99], 0\n" \
"v_add_u32_e64 v220, s35, v217\n" \
"v_mul_u32_u24_e32 v222, 0x480, v222\n" \
"v_mfma_f32_16x16x32_bf16 a[108:111], v[128:131], v[160:163], a[108:111]\n" \
"v_mad_u32_u24 v222, v220, s5, v222\n" \
"v_cmp_gt_u32_e64 s[4:5], s4, v215\n" \
"v_mfma_f32_16x16x32_bf16 a[104:107], v[64:67], v[92:95], 0\n" \
"ds_read_b64_tr_b16 v[108:109], v38 offset:0\n" \
"v_cndmask_b32_e64 v215, v221, v218, vcc\n" \
"v_mfma_f32_16x16x32_bf16 a[104:107], v[128:131], v[156:159], a[104:107]\n" \
"v_lshrrev_b32_e32 v218, 1, v220\n" \
"v_or_b32_e32 v214, v222, v214\n" \
"v_mfma_f32_16x16x32_bf16 a[100:103], v[64:67], v[88:91], 0\n" \
"v_mov_b32_e32 v222, 2.0\n" \
"v_and_b32_e32 v218, 4, v218\n" \
"v_mfma_f32_16x16x32_bf16 a[100:103], v[128:131], v[152:155], a[100:103]\n" \
"ds_read_b64_tr_b16 v[110:111], v38 offset:1024\n" \
"v_and_b32_e64 v217, 2, v217\n" \
"v_mfma_f32_16x16x32_bf16 a[96:99], v[64:67], v[84:87], 0\n" \
"v_bitop3_b32 v215, v215, v218, v217 bitop3:0x1e\n" \
"v_lshlrev_b32_e64 v223, 1, v220\n" \
"v_mfma_f32_16x16x32_bf16 a[96:99], v[128:131], v[148:151], a[96:99]\n" \
"v_mov_b32_e32 v224, 2.0\n" \
"v_cndmask_b32_e32 v193, v222, v214, vcc\n" \
"v_mfma_f32_16x16x32_bf16 a[128:131], v[68:71], v[84:87], 0\n" \
"ds_read_b64_tr_b16 v[172:173], v38 offset:8192\n" \
"v_lshlrev_b32_e32 v214, 7, v220\n" \
"s_waitcnt lgkmcnt(15)\n" \
"v_mfma_f32_16x16x32_bf16 a[128:131], v[132:135], v[148:151], a[128:131]\n" \
"v_lshlrev_b32_e32 v215, 4, v215\n" \
"s_mul_i32 s39, s3, 18\n" \
"v_mfma_f32_16x16x32_bf16 a[132:135], v[68:71], v[88:91], 0\n" \
"v_add3_u32 v214, v214, s34, v215\n" \
"v_cndmask_b32_e64 v195, v224, v223, s[4:5]\n" \
"v_mfma_f32_16x16x32_bf16 a[132:135], v[132:135], v[152:155], a[132:135]\n" \
"ds_read_b64_tr_b16 v[174:175], v38 offset:9216\n" \
"s_movk_i32 s4, 0x1f8\n" \
"v_mfma_f32_16x16x32_bf16 a[136:139], v[68:71], v[92:95], 0\n" \
"v_mov_b32_e32 v215, v213\n" \
"s_add_i32 s35, s35, s39\n" \
"v_mfma_f32_16x16x32_bf16 a[136:139], v[132:135], v[156:159], a[136:139]\n" \
"v_cmp_gt_u32_e32 vcc, s4, v216\n" \
"v_add_u32_e64 v212, s35, v212\n" \
"v_mfma_f32_16x16x32_bf16 a[140:143], v[68:71], v[96:99], 0\n" \
"v_mov_b32_e32 v219, 2.0\n" \
"v_cndmask_b32_e32 v197, v215, v214, vcc\n" \
"v_mfma_f32_16x16x32_bf16 a[140:143], v[132:135], v[160:163], a[140:143]\n" \
"ds_read_b64_tr_b16 v[112:113], v39 offset:0\n" \
"s_movk_i32 s35, 0x120\n" \
"v_mfma_f32_16x16x32_bf16 a[172:175], v[72:75], v[96:99], 0\n" \
"v_mul_u32_u24_e32 v214, 0xd80, v212\n" \
"v_cmp_gt_u32_e32 vcc, s35, v20\n" \
"v_mfma_f32_16x16x32_bf16 a[172:175], v[136:139], v[160:163], a[172:175]\n" \
"v_lshrrev_b32_e32 v215, 1, v212\n" \
"s_and_b32 s4, s2, 7\n" \
"v_mfma_f32_16x16x32_bf16 a[168:171], v[72:75], v[92:95], 0\n" \
"ds_read_b64_tr_b16 v[114:115], v39 offset:1024\n" \
"v_cndmask_b32_e64 v194, v219, v214, vcc\n" \
"v_mfma_f32_16x16x32_bf16 a[168:171], v[136:139], v[156:159], a[168:171]\n" \
"v_lshlrev_b32_e32 v214, 7, v212\n" \
"v_lshrrev_b32_e32 v212, 2, v212\n" \
"v_mfma_f32_16x16x32_bf16 a[164:167], v[72:75], v[88:91], 0\n" \
"v_and_b32_e64 v212, 2, v212\n" \
"v_and_or_b32 v212, v215, 1, v212\n" \
"v_mfma_f32_16x16x32_bf16 a[164:167], v[136:139], v[152:155], a[164:167]\n" \
"ds_read_b64_tr_b16 v[176:177], v39 offset:8192\n" \
"s_lshl_b32 s5, s4, 2\n" \
"v_mfma_f32_16x16x32_bf16 a[160:163], v[72:75], v[84:87], 0\n" \
"s_bfe_u32 s38, s2, 0x20003\n" \
"v_lshlrev_b32_e32 v212, 5, v212\n" \
"v_mfma_f32_16x16x32_bf16 a[160:163], v[136:139], v[148:151], a[160:163]\n" \
"s_movk_i32 s35, 0x60\n" \
"s_or_b32 s5, s5, s38\n" \
"s_waitcnt lgkmcnt(15)\n" \
"v_mfma_f32_16x16x32_bf16 a[192:195], v[76:79], v[84:87], 0\n" \
"ds_read_b64_tr_b16 v[178:179], v39 offset:9216\n" \
"v_bitop3_b32 v212, v212, s35, v214 bitop3:0x36\n" \
"v_mfma_f32_16x16x32_bf16 a[192:195], v[140:143], v[148:151], a[192:195]\n" \
"s_lshr_b32 s36, s2, 3\n" \
"v_add_u32_e64 v212, s34, v212\n" \
"v_mfma_f32_16x16x32_bf16 a[196:199], v[76:79], v[88:91], 0\n" \
"s_mul_i32 s34, s5, 0x12000\n" \
"s_or_b32 s40, s34, 0x6000000\n" \
"v_mfma_f32_16x16x32_bf16 a[196:199], v[140:143], v[152:155], a[196:199]\n" \
"s_mul_i32 s34, s36, 0x90000\n" \
"s_lshl_b32 s36, s3, 14\n" \
"v_mfma_f32_16x16x32_bf16 a[200:203], v[76:79], v[92:95], 0\n" \
"s_add_i32 s34, s34, s36\n" \
"s_lshl_b32 s38, s5, 8\n" \
"v_mfma_f32_16x16x32_bf16 a[200:203], v[140:143], v[156:159], a[200:203]\n" \
"s_add_i32 s41, s34, 0x4240000\n" \
"s_mul_i32 s34, s5, 36\n" \
"v_mfma_f32_16x16x32_bf16 a[204:207], v[76:79], v[96:99], 0\n" \
"s_add_i32 s5, s7, s40\n" \
"s_addk_i32 s5, 0x2000\n" \
"v_mfma_f32_16x16x32_bf16 a[204:207], v[140:143], v[160:163], a[204:207]\n" \
"s_mov_b32 s37, 2.0\n" \
"s_or_b32 s38, s38, 0x5be8000\n" \
"v_mfma_f32_16x16x32_bf16 a[236:239], v[80:83], v[96:99], 0\n" \
"s_cmp_lg_u32 s3, 0\n" \
"s_cselect_b32 s36, s37, s5\n" \
"v_mfma_f32_16x16x32_bf16 a[236:239], v[144:147], v[160:163], a[236:239]\n" \
"s_lshl_b32 s5, s6, 2\n" \
"s_or_b32 s5, s38, s5\n" \
"v_mfma_f32_16x16x32_bf16 a[232:235], v[80:83], v[92:95], 0\n" \
"s_lshl_b32 s35, s3, 10\n" \
"v_mfma_f32_16x16x32_bf16 a[232:235], v[144:147], v[156:159], a[232:235]\n" \
"s_cmpk_lt_u32 s3, 3\n" \
"s_cselect_b32 s37, s37, s5\n" \
"v_mfma_f32_16x16x32_bf16 a[228:231], v[80:83], v[88:91], 0\n" \
"s_lshl_b32 s5, s4, 16\n" \
"s_lshl_b32 s4, s4, 11\n" \
"v_mfma_f32_16x16x32_bf16 a[228:231], v[144:147], v[152:155], a[228:231]\n" \
"v_cndmask_b32_e32 v198, v213, v212, vcc\n" \
"v_or_b32_e32 v212, s35, v20\n" \
"v_mfma_f32_16x16x32_bf16 a[224:227], v[80:83], v[84:87], 0\n" \
"s_or_b32 s39, s40, s35\n" \
"s_or_b32 s55, s41, s4\n" \
"v_mfma_f32_16x16x32_bf16 a[224:227], v[144:147], v[148:151], a[224:227]\n" \
"s_waitcnt vmcnt(4) lgkmcnt(0)\n" \
"s_barrier\n" \
"v_mfma_f32_16x16x32_bf16 a[16:19], v[52:55], v[100:103], 0\n" \
"s_add_u32 m0, s30, 0x0\n" \
"s_nop 0\n" \
"s_mov_b32 s85, 0x100\n" \
"v_mfma_f32_16x16x32_bf16 a[16:19], v[116:119], v[164:167], a[16:19]\n" \
"buffer_load_dwordx4 v22, s[64:67], s85 offen lds\n" \
"v_mfma_f32_16x16x32_bf16 a[20:23], v[52:55], v[104:107], 0\n" \
"ds_read_b64_tr_b16 v[84:85], v32 offset:32768\n" \
"v_or_b32_e32 v199, 0x22000, v212\n" \
"v_mfma_f32_16x16x32_bf16 a[20:23], v[116:119], v[168:171], a[20:23]\n" \
"s_add_u32 m0, s30, 0x400\n" \
"s_mov_b32 s84, 0x2100\n" \
"v_mfma_f32_16x16x32_bf16 a[24:27], v[52:55], v[108:111], 0\n" \
"buffer_load_dwordx4 v23, s[64:67], s84 offen lds\n" \
"v_mfma_f32_16x16x32_bf16 a[24:27], v[116:119], v[172:175], a[24:27]\n" \
"ds_read_b64_tr_b16 v[86:87], v32 offset:33792\n" \
"s_add_i32 s35, s39, s7\n" \
"v_mfma_f32_16x16x32_bf16 a[28:31], v[52:55], v[112:115], 0\n" \
"ds_read_b64_tr_b16 v[148:149], v32 offset:40960\n" \
"s_add_i32 s54, s41, s5\n" \
"v_mfma_f32_16x16x32_bf16 a[28:31], v[116:119], v[176:179], a[28:31]\n" \
"ds_read_b128 v[52:55], v28 offset:49152\n" \
"s_add_i32 s55, s55, 0x80000\n" \
"v_mfma_f32_16x16x32_bf16 a[60:63], v[56:59], v[112:115], 0\n" \
"ds_read_b128 v[116:119], v29 offset:49152\n" \
"v_mov_b32_e32 v213, 0xffff\n" \
"v_mfma_f32_16x16x32_bf16 a[60:63], v[120:123], v[176:179], a[60:63]\n" \
"s_add_u32 m0, s31, 0x0\n" \
"s_nop 0\n" \
"s_mov_b32 s85, 0x100000\n" \
"v_mfma_f32_16x16x32_bf16 a[56:59], v[56:59], v[108:111], 0\n" \
"buffer_load_dwordx4 v26, s[68:71], s85 offen lds\n" \
"v_mfma_f32_16x16x32_bf16 a[56:59], v[120:123], v[172:175], a[56:59]\n" \
"s_add_u32 m0, s31, 0x4000\n" \
"s_mov_b32 s84, 0x100000\n" \
"v_mfma_f32_16x16x32_bf16 a[52:55], v[56:59], v[104:107], 0\n" \
"buffer_load_dwordx4 v26, s[72:75], s84 offen lds\n" \
"v_mfma_f32_16x16x32_bf16 a[52:55], v[120:123], v[168:171], a[52:55]\n" \
"s_add_u32 m0, s30, 0x800\n" \
"s_mov_b32 s85, 0x4100\n" \
"v_mfma_f32_16x16x32_bf16 a[48:51], v[56:59], v[100:103], 0\n" \
"buffer_load_dwordx4 v22, s[64:67], s85 offen lds\n" \
"v_mfma_f32_16x16x32_bf16 a[48:51], v[120:123], v[164:167], a[48:51]\n" \
"ds_read_b128 v[56:59], v28 offset:51200\n" \
"s_and_b32 s6, s3, 1\n" \
"v_mfma_f32_16x16x32_bf16 a[80:83], v[60:63], v[100:103], 0\n" \
"ds_read_b128 v[120:123], v29 offset:51200\n" \
"s_lshl_b32 s4, s6, 4\n" \
"v_mfma_f32_16x16x32_bf16 a[80:83], v[124:127], v[164:167], a[80:83]\n" \
"s_add_u32 m0, s30, 0xc00\n" \
"s_mov_b32 s84, 0x6100\n" \
"v_mfma_f32_16x16x32_bf16 a[84:87], v[60:63], v[104:107], 0\n" \
"buffer_load_dwordx4 v23, s[64:67], s84 offen lds\n" \
"v_mfma_f32_16x16x32_bf16 a[84:87], v[124:127], v[168:171], a[84:87]\n" \
"ds_read_b64_tr_b16 v[150:151], v32 offset:41984\n" \
"v_lshrrev_b32_e32 v214, 4, v20\n" \
".fill (L_pb + 48864 - .) / 4, 4, 0xBF800000\n" \
"L_pp12:\n" \
"s_cbranch_execz L_pret\n" \
"v_mfma_f32_16x16x32_bf16 a[88:91], v[60:63], v[108:111], 0\n" \
"s_add_u32 m0, s31, 0x400\n" \
"s_mov_b32 s85, 0x108000\n" \
"v_mfma_f32_16x16x32_bf16 a[88:91], v[124:127], v[172:175], a[88:91]\n" \
"buffer_load_dwordx4 v26, s[68:71], s85 offen lds\n" \
"v_mfma_f32_16x16x32_bf16 a[92:95], v[60:63], v[112:115], 0\n" \
"ds_read_b64_tr_b16 v[88:89], v33 offset:32768\n" \
"v_lshrrev_b32_e32 v215, 6, v20\n" \
"v_mfma_f32_16x16x32_bf16 a[92:95], v[124:127], v[176:179], a[92:95]\n" \
"ds_read_b128 v[60:63], v28 offset:53248\n" \
"v_and_b32_e64 v216, 12, v215\n" \
"v_mfma_f32_16x16x32_bf16 a[124:127], v[64:67], v[112:115], 0\n" \
"ds_read_b128 v[124:127], v29 offset:53248\n" \
"v_and_or_b32 v212, v214, 15, s4\n" \
"v_mfma_f32_16x16x32_bf16 a[124:127], v[128:131], v[176:179], a[124:127]\n" \
"s_add_u32 m0, s31, 0x4400\n" \
"s_mov_b32 s84, 0x108000\n" \
"v_mfma_f32_16x16x32_bf16 a[120:123], v[64:67], v[108:111], 0\n" \
"buffer_load_dwordx4 v26, s[72:75], s84 offen lds\n" \
"v_mfma_f32_16x16x32_bf16 a[120:123], v[128:131], v[172:175], a[120:123]\n" \
"s_add_u32 m0, s30, 0x1000\n" \
"s_mov_b32 s85, 0x8100\n" \
"v_mfma_f32_16x16x32_bf16 a[116:119], v[64:67], v[104:107], 0\n" \
"buffer_load_dwordx4 v22, s[64:67], s85 offen lds\n" \
"v_mfma_f32_16x16x32_bf16 a[116:119], v[128:131], v[168:171], a[116:119]\n" \
"ds_read_b64_tr_b16 v[90:91], v33 offset:33792\n" \
"v_cmp_gt_u32_e64 vcc, v216, v212\n" \
"v_mfma_f32_16x16x32_bf16 a[112:115], v[64:67], v[100:103], 0\n" \
"ds_read_b64_tr_b16 v[152:153], v33 offset:40960\n" \
"v_mov_b32_e32 v218, 0xffff0000\n" \
"v_mfma_f32_16x16x32_bf16 a[112:115], v[128:131], v[164:167], a[112:115]\n" \
"ds_read_b128 v[64:67], v28 offset:55296\n" \
"s_lshl_b32 s4, s2, 2\n" \
"s_nop 0\n" \
"v_mfma_f32_16x16x32_bf16 a[144:147], v[68:71], v[100:103], 0\n" \
"ds_read_b128 v[128:131], v29 offset:55296\n" \
"v_cndmask_b32_e64 v217, v213, 0, vcc\n" \
"v_mfma_f32_16x16x32_bf16 a[144:147], v[132:135], v[164:167], a[144:147]\n" \
"s_add_u32 m0, s30, 0x1400\n" \
"s_mov_b32 s84, 0xa100\n" \
"v_mfma_f32_16x16x32_bf16 a[148:151], v[68:71], v[104:107], 0\n" \
"buffer_load_dwordx4 v23, s[64:67], s84 offen lds\n" \
"v_mfma_f32_16x16x32_bf16 a[148:151], v[132:135], v[168:171], a[148:151]\n" \
"s_add_u32 m0, s31, 0x800\n" \
"s_mov_b32 s85, 0x110000\n" \
"v_mfma_f32_16x16x32_bf16 a[152:155], v[68:71], v[108:111], 0\n" \
"buffer_load_dwordx4 v27, s[68:71], s85 offen lds\n" \
"v_mfma_f32_16x16x32_bf16 a[152:155], v[132:135], v[172:175], a[152:155]\n" \
"s_add_u32 m0, s31, 0x4800\n" \
"s_mov_b32 s84, 0x110000\n" \
"v_mfma_f32_16x16x32_bf16 a[156:159], v[68:71], v[112:115], 0\n" \
"buffer_load_dwordx4 v27, s[72:75], s84 offen lds\n" \
"v_mfma_f32_16x16x32_bf16 a[156:159], v[132:135], v[176:179], a[156:159]\n" \
"ds_read_b128 v[68:71], v28 offset:32768\n" \
"v_cmp_lt_u32_e64 vcc, v216, v212\n" \
"v_mfma_f32_16x16x32_bf16 a[188:191], v[72:75], v[112:115], 0\n" \
"ds_read_b128 v[132:135], v29 offset:32768\n" \
"s_mov_b32 s7, 0\n" \
"v_mfma_f32_16x16x32_bf16 a[188:191], v[136:139], v[176:179], a[188:191]\n" \
"s_add_u32 m0, s30, 0x1800\n" \
"s_mov_b32 s85, 0xc100\n" \
"v_mfma_f32_16x16x32_bf16 a[184:187], v[72:75], v[108:111], 0\n" \
"buffer_load_dwordx4 v22, s[64:67], s85 offen lds\n" \
"v_mfma_f32_16x16x32_bf16 a[184:187], v[136:139], v[172:175], a[184:187]\n" \
"ds_read_b64_tr_b16 v[154:155], v33 offset:41984\n" \
"s_or_b32 s40, s4, s3\n" \
"v_mfma_f32_16x16x32_bf16 a[180:183], v[72:75], v[104:107], 0\n" \
"s_add_u32 m0, s30, 0x1c00\n" \
"s_mov_b32 s84, 0xe100\n" \
"v_mfma_f32_16x16x32_bf16 a[180:183], v[136:139], v[168:171], a[180:183]\n" \
"buffer_load_dwordx4 v23, s[64:67], s84 offen lds\n" \
"v_mfma_f32_16x16x32_bf16 a[176:179], v[72:75], v[100:103], 0\n" \
"ds_read_b64_tr_b16 v[92:93], v34 offset:32768\n" \
"v_cndmask_b32_e64 v219, 0, v218, vcc\n" \
"v_mfma_f32_16x16x32_bf16 a[176:179], v[136:139], v[164:167], a[176:179]\n" \
"ds_read_b128 v[72:75], v28 offset:34816\n" \
"v_or_b32_e64 v12, v217, v219\n" \
"v_mfma_f32_16x16x32_bf16 a[208:211], v[76:79], v[100:103], 0\n" \
"ds_read_b128 v[136:139], v29 offset:34816\n" \
"v_or_b32_e64 v217, 2, v216\n" \
"v_mfma_f32_16x16x32_bf16 a[208:211], v[140:143], v[164:167], a[208:211]\n" \
"ds_read_b64_tr_b16 v[94:95], v34 offset:33792\n" \
"v_cmp_gt_u32_e64 vcc, v217, v212\n" \
"v_mfma_f32_16x16x32_bf16 a[212:215], v[76:79], v[104:107], 0\n" \
"ds_read_b64_tr_b16 v[156:157], v34 offset:40960\n" \
"s_add_i32 s41, s40, 0xffffffaf\n" \
"v_mfma_f32_16x16x32_bf16 a[212:215], v[140:143], v[168:171], a[212:215]\n" \
"ds_read_b64_tr_b16 v[158:159], v34 offset:41984\n" \
"v_mfma_f32_16x16x32_bf16 a[216:219], v[76:79], v[108:111], 0\n" \
"ds_read_b64_tr_b16 v[96:97], v35 offset:32768\n" \
"v_mfma_f32_16x16x32_bf16 a[216:219], v[140:143], v[172:175], a[216:219]\n" \
"ds_read_b64_tr_b16 v[98:99], v35 offset:33792\n" \
"v_mfma_f32_16x16x32_bf16 a[220:223], v[76:79], v[112:115], 0\n" \
"ds_read_b64_tr_b16 v[160:161], v35 offset:40960\n" \
"v_mfma_f32_16x16x32_bf16 a[220:223], v[140:143], v[176:179], a[220:223]\n" \
"ds_read_b128 v[76:79], v28 offset:36864\n" \
"v_mfma_f32_16x16x32_bf16 a[252:255], v[80:83], v[112:115], 0\n" \
"ds_read_b128 v[140:143], v29 offset:36864\n" \
"v_mfma_f32_16x16x32_bf16 a[252:255], v[144:147], v[176:179], a[252:255]\n" \
"ds_read_b64_tr_b16 v[162:163], v35 offset:41984\n" \
"v_mfma_f32_16x16x32_bf16 a[248:251], v[80:83], v[108:111], 0\n" \
"s_cmpk_lt_u32 s40, 0x291\n" \
"s_cselect_b32 s7, s41, s7\n" \
"v_mfma_f32_16x16x32_bf16 a[248:251], v[144:147], v[172:175], a[248:251]\n" \
"v_cndmask_b32_e64 v219, v213, 0, vcc\n" \
"v_cmp_lt_u32_e64 vcc, v217, v212\n" \
"v_mfma_f32_16x16x32_bf16 a[244:247], v[80:83], v[104:107], 0\n" \
"s_cmpk_lt_u32 s40, 0x51\n" \
"s_cselect_b32 s7, s40, s7\n" \
"v_mfma_f32_16x16x32_bf16 a[244:247], v[144:147], v[168:171], a[244:247]\n" \
"s_cmpk_lt_u32 s40, 0x51\n" \
"s_cselect_b32 s5, s23, s25\n" \
"v_mfma_f32_16x16x32_bf16 a[240:243], v[80:83], v[100:103], 0\n" \
"v_cndmask_b32_e32 v217, 0, v218, vcc\n" \
"v_or_b32_e32 v13, v219, v217\n" \
"v_mfma_f32_16x16x32_bf16 a[240:243], v[144:147], v[164:167], a[240:243]\n" \
"ds_read_b128 v[80:83], v28 offset:38912\n" \
"ds_read_b128 v[144:147], v29 offset:38912\n" \
"s_waitcnt lgkmcnt(15)\n" \
"v_mfma_f32_16x16x32_bf16 a[0:3], v[52:55], v[84:87], a[0:3]\n" \
"s_add_u32 m0, s31, 0xc00\n" \
"s_mov_b32 s85, 0x118000\n" \
"v_mfma_f32_16x16x32_bf16 a[0:3], v[116:119], v[148:151], a[0:3]\n" \
"buffer_load_dwordx4 v27, s[68:71], s85 offen lds\n" \
"v_mfma_f32_16x16x32_bf16 a[4:7], v[52:55], v[88:91], a[4:7]\n" \
"ds_read_b64_tr_b16 v[100:101], v36 offset:32768\n" \
"v_or_b32_e32 v217, 16, v216\n" \
"s_waitcnt lgkmcnt(15)\n" \
"v_mfma_f32_16x16x32_bf16 a[4:7], v[116:119], v[152:155], a[4:7]\n" \
"s_add_u32 m0, s31, 0x4c00\n" \
"s_mov_b32 s84, 0x118000\n" \
"s_waitcnt lgkmcnt(11)\n" \
"v_mfma_f32_16x16x32_bf16 a[8:11], v[52:55], v[92:95], a[8:11]\n" \
"buffer_load_dwordx4 v27, s[72:75], s84 offen lds\n" \
"s_waitcnt lgkmcnt(9)\n" \
"v_mfma_f32_16x16x32_bf16 a[8:11], v[116:119], v[156:159], a[8:11]\n" \
"ds_read_b64_tr_b16 v[102:103], v36 offset:33792\n" \
"v_cmp_gt_u32_e32 vcc, v217, v212\n" \
"s_waitcnt lgkmcnt(8)\n" \
"v_mfma_f32_16x16x32_bf16 a[12:15], v[52:55], v[96:99], a[12:15]\n" \
"ds_read_b64_tr_b16 v[164:165], v36 offset:40960\n" \
"s_waitcnt lgkmcnt(5)\n" \
"v_mfma_f32_16x16x32_bf16 a[12:15], v[116:119], v[160:163], a[12:15]\n" \
"s_cmpk_lt_u32 s40, 0x51\n" \
"s_cselect_b32 s4, s22, s24\n" \
"v_mfma_f32_16x16x32_bf16 a[44:47], v[56:59], v[96:99], a[44:47]\n" \
"s_movk_i32 s40, 0xf400\n" \
"v_cndmask_b32_e64 v219, v213, 0, vcc\n" \
"v_mfma_f32_16x16x32_bf16 a[44:47], v[120:123], v[160:163], a[44:47]\n" \
"ds_read_b64_tr_b16 v[166:167], v36 offset:41984\n" \
"v_cmp_lt_u32_e32 vcc, v217, v212\n" \
"v_mfma_f32_16x16x32_bf16 a[40:43], v[56:59], v[92:95], a[40:43]\n" \
"s_nop 0\n" \
"v_cndmask_b32_e32 v217, 0, v218, vcc\n" \
"v_or_b32_e64 v14, v219, v217\n" \
"v_mfma_f32_16x16x32_bf16 a[40:43], v[120:123], v[156:159], a[40:43]\n" \
"v_or_b32_e32 v217, 18, v216\n" \
"v_cmp_gt_u32_e32 vcc, v217, v212\n" \
"v_mfma_f32_16x16x32_bf16 a[36:39], v[56:59], v[88:91], a[36:39]\n" \
"s_nop 0\n" \
"v_cndmask_b32_e64 v213, v213, 0, vcc\n" \
"v_cmp_lt_u32_e32 vcc, v217, v212\n" \
"v_mfma_f32_16x16x32_bf16 a[36:39], v[120:123], v[152:155], a[36:39]\n" \
"ds_read_b64_tr_b16 v[104:105], v37 offset:32768\n" \
"v_cndmask_b32_e64 v212, 0, v218, vcc\n" \
"v_mfma_f32_16x16x32_bf16 a[32:35], v[56:59], v[84:87], a[32:35]\n" \
"v_or_b32_e32 v15, v213, v212\n" \
"v_lshlrev_b32_e32 v212, 7, v214\n" \
"v_mfma_f32_16x16x32_bf16 a[32:35], v[120:123], v[148:151], a[32:35]\n" \
"v_lshl_or_b32 v212, s7, 13, v212\n" \
"v_mov_b32_e64 v213, 0\n" \
"v_mfma_f32_16x16x32_bf16 a[64:67], v[60:63], v[84:87], a[64:67]\n" \
"s_mov_b32 m0, s32\n" \
"ds_read_b64_tr_b16 v[106:107], v37 offset:33792\n" \
"v_mfma_f32_16x16x32_bf16 a[64:67], v[124:127], v[148:151], a[64:67]\n" \
"s_mov_b32 exec_lo, 0x0\n" \
"s_mov_b32 exec_hi, 0xff\n" \
"global_load_lds_dword v[184:185], off\n" \
"s_mov_b64 exec, -1\n" \
"v_mfma_f32_16x16x32_bf16 a[68:71], v[60:63], v[88:91], a[68:71]\n" \
"v_lshl_add_u64 v[16:17], s[4:5], 0, v[212:213]\n" \
"v_mul_u32_u24_e32 v212, 0xe39, v214\n" \
"v_mfma_f32_16x16x32_bf16 a[68:71], v[124:127], v[152:155], a[68:71]\n" \
"ds_read_b64_tr_b16 v[168:169], v37 offset:40960\n" \
"v_lshrrev_b32_e32 v212, 16, v212\n" \
"v_mfma_f32_16x16x32_bf16 a[72:75], v[60:63], v[92:95], a[72:75]\n" \
"s_movk_i32 s5, 0xffee\n" \
"s_lshl_b32 s4, s2, 5\n" \
"v_mfma_f32_16x16x32_bf16 a[72:75], v[124:127], v[156:159], a[72:75]\n" \
"s_mov_b32 m0, s32\n" \
"v_mad_i32_i24 v213, v212, s5, v214\n" \
"v_mfma_f32_16x16x32_bf16 a[76:79], v[60:63], v[96:99], a[76:79]\n" \
"s_mov_b32 exec_lo, 0xffff0000\n" \
"s_mov_b32 exec_hi, 0x0\n" \
"global_load_lds_dword v[186:187], off\n" \
"s_mov_b64 exec, -1\n" \
"v_mfma_f32_16x16x32_bf16 a[76:79], v[124:127], v[160:163], a[76:79]\n" \
"ds_read_b64_tr_b16 v[170:171], v37 offset:41984\n" \
"s_add_i32 s4, s4, 32\n" \
"v_mfma_f32_16x16x32_bf16 a[108:111], v[64:67], v[96:99], a[108:111]\n" \
"v_lshlrev_b32_e32 v217, 4, v213\n" \
"v_add_u32_e32 v218, s4, v217\n" \
"v_mfma_f32_16x16x32_bf16 a[108:111], v[128:131], v[160:163], a[108:111]\n" \
"v_and_b32_e32 v218, 0xf0, v218\n" \
"v_cmp_gt_u32_e64 vcc, 16, v213\n" \
"v_mfma_f32_16x16x32_bf16 a[104:107], v[64:67], v[92:95], a[104:107]\n" \
"ds_read_b64_tr_b16 v[108:109], v38 offset:32768\n" \
"s_movk_i32 s7, 0xf800\n" \
"v_mfma_f32_16x16x32_bf16 a[104:107], v[128:131], v[156:159], a[104:107]\n" \
"v_cndmask_b32_e32 v213, v217, v218, vcc\n" \
"v_or_b32_e64 v217, 64, v214\n" \
"v_mfma_f32_16x16x32_bf16 a[100:103], v[64:67], v[88:91], a[100:103]\n" \
"v_mul_u32_u24_e32 v218, 0xe39, v217\n" \
"v_lshrrev_b32_e64 v218, 16, v218\n" \
"v_mfma_f32_16x16x32_bf16 a[100:103], v[128:131], v[152:155], a[100:103]\n" \
"ds_read_b64_tr_b16 v[110:111], v38 offset:33792\n" \
"v_mad_i32_i24 v217, v218, s5, v217\n" \
"v_mfma_f32_16x16x32_bf16 a[96:99], v[64:67], v[84:87], a[96:99]\n" \
"v_lshlrev_b32_e32 v219, 4, v217\n" \
"v_add_u32_e32 v220, s4, v219\n" \
"v_mfma_f32_16x16x32_bf16 a[96:99], v[128:131], v[148:151], a[96:99]\n" \
"v_and_b32_e32 v220, 0xf0, v220\n" \
"v_cmp_gt_u32_e64 vcc, 16, v217\n" \
"v_mfma_f32_16x16x32_bf16 a[128:131], v[68:71], v[84:87], a[128:131]\n" \
"ds_read_b64_tr_b16 v[172:173], v38 offset:40960\n" \
"v_cndmask_b32_e64 v217, v219, v220, vcc\n" \
"v_mfma_f32_16x16x32_bf16 a[128:131], v[132:135], v[148:151], a[128:131]\n" \
"v_or_b32_e32 v219, 0x80, v214\n" \
"v_mul_u32_u24_e32 v220, 0xe39, v219\n" \
"v_mfma_f32_16x16x32_bf16 a[132:135], v[68:71], v[88:91], a[132:135]\n" \
"v_lshrrev_b32_e64 v220, 16, v220\n" \
"v_mad_i32_i24 v219, v220, s5, v219\n" \
"v_mfma_f32_16x16x32_bf16 a[132:135], v[132:135], v[152:155], a[132:135]\n" \
"ds_read_b64_tr_b16 v[174:175], v38 offset:41984\n" \
"v_lshlrev_b32_e64 v221, 4, v219\n" \
"v_mfma_f32_16x16x32_bf16 a[136:139], v[68:71], v[92:95], a[136:139]\n" \
"v_add_u32_e64 v222, s4, v221\n" \
"v_and_b32_e32 v222, 0xf0, v222\n" \
"v_mfma_f32_16x16x32_bf16 a[136:139], v[132:135], v[156:159], a[136:139]\n" \
"v_cmp_gt_u32_e32 vcc, 16, v219\n" \
"s_nop 1\n" \
"v_cndmask_b32_e64 v219, v221, v222, vcc\n" \
"v_mfma_f32_16x16x32_bf16 a[140:143], v[68:71], v[96:99], a[140:143]\n" \
"v_or_b32_e32 v221, 0xc0, v214\n" \
"v_mul_u32_u24_e32 v222, 0xe39, v221\n" \
"v_mfma_f32_16x16x32_bf16 a[140:143], v[132:135], v[160:163], a[140:143]\n" \
"ds_read_b64_tr_b16 v[112:113], v39 offset:32768\n" \
"v_lshrrev_b32_e64 v222, 16, v222\n" \
"v_mfma_f32_16x16x32_bf16 a[172:175], v[72:75], v[96:99], a[172:175]\n" \
"v_mad_i32_i24 v221, v222, s5, v221\n" \
"v_lshlrev_b32_e64 v223, 4, v221\n" \
"v_mfma_f32_16x16x32_bf16 a[172:175], v[136:139], v[160:163], a[172:175]\n" \
"v_add_u32_e64 v224, s4, v223\n" \
"v_and_b32_e32 v224, 0xf0, v224\n" \
"v_mfma_f32_16x16x32_bf16 a[168:171], v[72:75], v[92:95], a[168:171]\n" \
"ds_read_b64_tr_b16 v[114:115], v39 offset:33792\n" \
"v_cmp_gt_u32_e64 vcc, 16, v221\n" \
"v_mfma_f32_16x16x32_bf16 a[168:171], v[136:139], v[156:159], a[168:171]\n" \
"s_nop 0\n" \
"v_cndmask_b32_e32 v221, v223, v224, vcc\n" \
"v_or_b32_e32 v223, 0x100, v214\n" \
"v_mfma_f32_16x16x32_bf16 a[164:167], v[72:75], v[88:91], a[164:167]\n" \
"v_mul_u32_u24_e32 v224, 0xe39, v223\n" \
"v_lshrrev_b32_e64 v224, 16, v224\n" \
"v_mfma_f32_16x16x32_bf16 a[164:167], v[136:139], v[152:155], a[164:167]\n" \
"ds_read_b64_tr_b16 v[176:177], v39 offset:40960\n" \
"v_mad_i32_i24 v223, v224, s5, v223\n" \
"v_mfma_f32_16x16x32_bf16 a[160:163], v[72:75], v[84:87], a[160:163]\n" \
"v_lshlrev_b32_e32 v225, 4, v223\n" \
"v_add_u32_e32 v226, s4, v225\n" \
"v_mfma_f32_16x16x32_bf16 a[160:163], v[136:139], v[148:151], a[160:163]\n" \
"v_and_b32_e32 v226, 0xf0, v226\n" \
"v_cmp_gt_u32_e64 vcc, 16, v223\n" \
"v_mfma_f32_16x16x32_bf16 a[192:195], v[76:79], v[84:87], a[192:195]\n" \
"ds_read_b64_tr_b16 v[178:179], v39 offset:41984\n" \
"v_cndmask_b32_e64 v223, v225, v226, vcc\n" \
"v_mfma_f32_16x16x32_bf16 a[192:195], v[140:143], v[148:151], a[192:195]\n" \
"v_or_b32_e32 v225, 0x140, v214\n" \
"v_mul_u32_u24_e32 v226, 0xe39, v225\n" \
"v_mfma_f32_16x16x32_bf16 a[196:199], v[76:79], v[88:91], a[196:199]\n" \
"v_lshrrev_b32_e64 v226, 16, v226\n" \
"v_mad_i32_i24 v225, v226, s5, v225\n" \
"v_mfma_f32_16x16x32_bf16 a[196:199], v[140:143], v[152:155], a[196:199]\n" \
"v_lshlrev_b32_e32 v227, 4, v225\n" \
"v_add_u32_e32 v228, s4, v227\n" \
"v_mfma_f32_16x16x32_bf16 a[200:203], v[76:79], v[92:95], a[200:203]\n" \
"v_and_b32_e32 v228, 0xf0, v228\n" \
"v_cmp_gt_u32_e64 vcc, 16, v225\n" \
"v_mfma_f32_16x16x32_bf16 a[200:203], v[140:143], v[156:159], a[200:203]\n" \
"s_nop 0\n" \
"v_cndmask_b32_e32 v225, v227, v228, vcc\n" \
"v_or_b32_e32 v227, 0x180, v214\n" \
"v_mfma_f32_16x16x32_bf16 a[204:207], v[76:79], v[96:99], a[204:207]\n" \
"v_mul_u32_u24_e32 v228, 0xe39, v227\n" \
"v_lshrrev_b32_e64 v228, 16, v228\n" \
"v_mfma_f32_16x16x32_bf16 a[204:207], v[140:143], v[160:163], a[204:207]\n" \
"v_mad_i32_i24 v227, v228, s5, v227\n" \
"v_lshlrev_b32_e32 v229, 4, v227\n" \
"s_waitcnt lgkmcnt(15)\n" \
"v_mfma_f32_16x16x32_bf16 a[236:239], v[80:83], v[96:99], a[236:239]\n" \
"v_add_u32_e64 v230, s4, v229\n" \
"v_and_b32_e32 v230, 0xf0, v230\n" \
"v_mfma_f32_16x16x32_bf16 a[236:239], v[144:147], v[160:163], a[236:239]\n" \
"v_cmp_gt_u32_e32 vcc, 16, v227\n" \
"s_nop 1\n" \
"v_cndmask_b32_e64 v227, v229, v230, vcc\n" \
"v_mfma_f32_16x16x32_bf16 a[232:235], v[80:83], v[92:95], a[232:235]\n" \
"v_or_b32_e32 v229, 0x1c0, v214\n" \
"v_mul_u32_u24_e32 v230, 0xe39, v229\n" \
"v_mfma_f32_16x16x32_bf16 a[232:235], v[144:147], v[156:159], a[232:235]\n" \
"v_lshrrev_b32_e64 v230, 16, v230\n" \
"v_mad_i32_i24 v229, v230, s5, v229\n" \
"v_mfma_f32_16x16x32_bf16 a[228:231], v[80:83], v[88:91], a[228:231]\n" \
"v_lshlrev_b32_e32 v231, 4, v229\n" \
"v_add_u32_e32 v232, s4, v231\n" \
"v_mfma_f32_16x16x32_bf16 a[228:231], v[144:147], v[152:155], a[228:231]\n" \
"v_and_b32_e32 v232, 0xf0, v232\n" \
"v_cmp_gt_u32_e64 vcc, 16, v229\n" \
"v_mfma_f32_16x16x32_bf16 a[224:227], v[80:83], v[84:87], a[224:227]\n" \
"s_nop 0\n" \
"v_cndmask_b32_e32 v229, v231, v232, vcc\n" \
"v_or_b32_e32 v231, 0x200, v214\n" \
"v_mfma_f32_16x16x32_bf16 a[224:227], v[144:147], v[148:151], a[224:227]\n" \
"s_waitcnt vmcnt(20)\n" \
"v_readfirstlane_b32 s62, v180\n" \
"s_nop 3\n" \
"s_add_u32 s62, s62, 0x9E3779B1\n" \
"s_waitcnt vmcnt(2) lgkmcnt(0)\n" \
"s_barrier\n" \
"s_nop 0\n" \
"v_mfma_f32_16x16x32_bf16 a[16:19], v[52:55], v[100:103], a[16:19]\n" \
"s_add_u32 m0, s30, 0x8000\n" \
"s_mov_b32 s85, 0x180\n" \
"v_mfma_f32_16x16x32_bf16 a[16:19], v[116:119], v[164:167], a[16:19]\n" \
"buffer_load_dwordx4 v22, s[64:67], s85 offen lds\n" \
"v_mfma_f32_16x16x32_bf16 a[20:23], v[52:55], v[104:107], a[20:23]\n" \
"ds_read_b64_tr_b16 v[84:85], v32 offset:0\n" \
"v_mul_u32_u24_e32 v232, 0xe39, v231\n" \
"v_mfma_f32_16x16x32_bf16 a[20:23], v[116:119], v[168:171], a[20:23]\n" \
"s_add_u32 m0, s30, 0x8400\n" \
"s_mov_b32 s84, 0x2180\n" \
"v_mfma_f32_16x16x32_bf16 a[24:27], v[52:55], v[108:111], a[24:27]\n" \
"buffer_load_dwordx4 v23, s[64:67], s84 offen lds\n" \
"v_mfma_f32_16x16x32_bf16 a[24:27], v[116:119], v[172:175], a[24:27]\n" \
"ds_read_b64_tr_b16 v[86:87], v32 offset:1024\n" \
"v_lshrrev_b32_e64 v232, 16, v232\n" \
"v_mfma_f32_16x16x32_bf16 a[28:31], v[52:55], v[112:115], a[28:31]\n" \
"ds_read_b64_tr_b16 v[148:149], v32 offset:8192\n" \
"v_mad_i32_i24 v231, v232, s5, v231\n" \
"v_mfma_f32_16x16x32_bf16 a[28:31], v[116:119], v[176:179], a[28:31]\n" \
"ds_read_b128 v[52:55], v28 offset:16384\n" \
"v_lshlrev_b32_e64 v233, 4, v231\n" \
"v_mfma_f32_16x16x32_bf16 a[60:63], v[56:59], v[112:115], a[60:63]\n" \
"ds_read_b128 v[116:119], v29 offset:16384\n" \
"v_add_u32_e64 v234, s4, v233\n" \
"v_mfma_f32_16x16x32_bf16 a[60:63], v[120:123], v[176:179], a[60:63]\n" \
"s_add_u32 m0, s31, 0x8000\n" \
"s_mov_b32 s85, 0x180000\n" \
"v_mfma_f32_16x16x32_bf16 a[56:59], v[56:59], v[108:111], a[56:59]\n" \
"buffer_load_dwordx4 v26, s[68:71], s85 offen lds\n" \
"v_mfma_f32_16x16x32_bf16 a[56:59], v[120:123], v[172:175], a[56:59]\n" \
"s_add_u32 m0, s31, 0xc000\n" \
"s_mov_b32 s84, 0x180000\n" \
"v_mfma_f32_16x16x32_bf16 a[52:55], v[56:59], v[104:107], a[52:55]\n" \
"buffer_load_dwordx4 v26, s[72:75], s84 offen lds\n" \
"v_mfma_f32_16x16x32_bf16 a[52:55], v[120:123], v[168:171], a[52:55]\n" \
"s_add_u32 m0, s30, 0x8800\n" \
"s_mov_b32 s85, 0x4180\n" \
"v_mfma_f32_16x16x32_bf16 a[48:51], v[56:59], v[100:103], a[48:51]\n" \
"buffer_load_dwordx4 v22, s[64:67], s85 offen lds\n" \
"v_mfma_f32_16x16x32_bf16 a[48:51], v[120:123], v[164:167], a[48:51]\n" \
"ds_read_b128 v[56:59], v28 offset:18432\n" \
"s_movk_i32 s4, 0x480\n" \
"s_nop 0\n" \
"v_mfma_f32_16x16x32_bf16 a[80:83], v[60:63], v[100:103], a[80:83]\n" \
"ds_read_b128 v[120:123], v29 offset:18432\n" \
"v_mad_u32_u24 v0, v212, s4, v213\n" \
"v_mfma_f32_16x16x32_bf16 a[80:83], v[124:127], v[164:167], a[80:83]\n" \
"s_add_u32 m0, s30, 0x8c00\n" \
"s_mov_b32 s84, 0x6180\n" \
"v_mfma_f32_16x16x32_bf16 a[84:87], v[60:63], v[104:107], a[84:87]\n" \
"buffer_load_dwordx4 v23, s[64:67], s84 offen lds\n" \
"v_mfma_f32_16x16x32_bf16 a[84:87], v[124:127], v[168:171], a[84:87]\n" \
"ds_read_b64_tr_b16 v[150:151], v32 offset:9216\n" \
"v_mul_u32_u24_e32 v212, 0x480, v218\n" \
"v_mfma_f32_16x16x32_bf16 a[88:91], v[60:63], v[108:111], a[88:91]\n" \
"s_add_u32 m0, s31, 0x8400\n" \
"s_mov_b32 s85, 0x188000\n" \
"v_mfma_f32_16x16x32_bf16 a[88:91], v[124:127], v[172:175], a[88:91]\n" \
"buffer_load_dwordx4 v26, s[68:71], s85 offen lds\n" \
"v_mfma_f32_16x16x32_bf16 a[92:95], v[60:63], v[112:115], a[92:95]\n" \
"ds_read_b64_tr_b16 v[88:89], v33 offset:0\n" \
"s_movk_i32 s5, 0xfc00\n" \
"s_nop 0\n" \
"v_mfma_f32_16x16x32_bf16 a[92:95], v[124:127], v[176:179], a[92:95]\n" \
"ds_read_b128 v[60:63], v28 offset:20480\n" \
"v_add3_u32 v1, v212, v217, s5\n" \
"v_mfma_f32_16x16x32_bf16 a[124:127], v[64:67], v[112:115], a[124:127]\n" \
"ds_read_b128 v[124:127], v29 offset:20480\n" \
"v_mul_u32_u24_e32 v212, 0x480, v220\n" \
"v_mfma_f32_16x16x32_bf16 a[124:127], v[128:131], v[176:179], a[124:127]\n" \
"s_add_u32 m0, s31, 0xc400\n" \
"s_mov_b32 s84, 0x188000\n" \
"v_mfma_f32_16x16x32_bf16 a[120:123], v[64:67], v[108:111], a[120:123]\n" \
"buffer_load_dwordx4 v26, s[72:75], s84 offen lds\n" \
"v_mfma_f32_16x16x32_bf16 a[120:123], v[128:131], v[172:175], a[120:123]\n" \
"s_add_u32 m0, s30, 0x9000\n" \
"s_mov_b32 s85, 0x8180\n" \
"v_mfma_f32_16x16x32_bf16 a[116:119], v[64:67], v[104:107], a[116:119]\n" \
"buffer_load_dwordx4 v22, s[64:67], s85 offen lds\n" \
"v_mfma_f32_16x16x32_bf16 a[116:119], v[128:131], v[168:171], a[116:119]\n" \
"ds_read_b64_tr_b16 v[90:91], v33 offset:1024\n" \
"v_add3_u32 v2, v212, v219, s7\n" \
"v_mfma_f32_16x16x32_bf16 a[112:115], v[64:67], v[100:103], a[112:115]\n" \
"ds_read_b64_tr_b16 v[152:153], v33 offset:8192\n" \
"v_mul_u32_u24_e32 v212, 0x480, v222\n" \
"v_mfma_f32_16x16x32_bf16 a[112:115], v[128:131], v[164:167], a[112:115]\n" \
"ds_read_b128 v[64:67], v28 offset:22528\n" \
"v_add3_u32 v3, v212, v221, s40\n" \
"v_mfma_f32_16x16x32_bf16 a[144:147], v[68:71], v[100:103], a[144:147]\n" \
"ds_read_b128 v[128:131], v29 offset:22528\n" \
"v_mul_u32_u24_e32 v212, 0x480, v226\n" \
"v_mfma_f32_16x16x32_bf16 a[144:147], v[132:135], v[164:167], a[144:147]\n" \
"s_add_u32 m0, s30, 0x9400\n" \
"s_mov_b32 s84, 0xa180\n" \
"v_mfma_f32_16x16x32_bf16 a[148:151], v[68:71], v[104:107], a[148:151]\n" \
"buffer_load_dwordx4 v23, s[64:67], s84 offen lds\n" \
"v_mfma_f32_16x16x32_bf16 a[148:151], v[132:135], v[168:171], a[148:151]\n" \
"s_add_u32 m0, s31, 0x8800\n" \
"s_mov_b32 s85, 0x190000\n" \
"v_mfma_f32_16x16x32_bf16 a[152:155], v[68:71], v[108:111], a[152:155]\n" \
"buffer_load_dwordx4 v27, s[68:71], s85 offen lds\n" \
"v_mfma_f32_16x16x32_bf16 a[152:155], v[132:135], v[172:175], a[152:155]\n" \
"s_add_u32 m0, s31, 0xc800\n" \
"s_mov_b32 s84, 0x190000\n" \
"v_mfma_f32_16x16x32_bf16 a[156:159], v[68:71], v[112:115], a[156:159]\n" \
"buffer_load_dwordx4 v27, s[72:75], s84 offen lds\n" \
"v_mfma_f32_16x16x32_bf16 a[156:159], v[132:135], v[176:179], a[156:159]\n" \
"ds_read_b128 v[68:71], v28 offset:0\n" \
"v_add3_u32 v5, v212, v225, s5\n" \
"v_mfma_f32_16x16x32_bf16 a[188:191], v[72:75], v[112:115], a[188:191]\n" \
"ds_read_b128 v[132:135], v29 offset:0\n" \
"v_mul_u32_u24_e32 v212, 0x480, v228\n" \
"v_mfma_f32_16x16x32_bf16 a[188:191], v[136:139], v[176:179], a[188:191]\n" \
"s_add_u32 m0, s30, 0x9800\n" \
"s_mov_b32 s85, 0xc180\n" \
"v_mfma_f32_16x16x32_bf16 a[184:187], v[72:75], v[108:111], a[184:187]\n" \
"buffer_load_dwordx4 v22, s[64:67], s85 offen lds\n" \
"v_mfma_f32_16x16x32_bf16 a[184:187], v[136:139], v[172:175], a[184:187]\n" \
"ds_read_b64_tr_b16 v[154:155], v33 offset:9216\n" \
"v_add3_u32 v6, v212, v227, s7\n" \
"v_mfma_f32_16x16x32_bf16 a[180:183], v[72:75], v[104:107], a[180:183]\n" \
"s_add_u32 m0, s30, 0x9c00\n" \
"s_mov_b32 s84, 0xe180\n" \
"v_mfma_f32_16x16x32_bf16 a[180:183], v[136:139], v[168:171], a[180:183]\n" \
"buffer_load_dwordx4 v23, s[64:67], s84 offen lds\n" \
"v_mfma_f32_16x16x32_bf16 a[176:179], v[72:75], v[100:103], a[176:179]\n" \
"ds_read_b64_tr_b16 v[92:93], v34 offset:0\n" \
"v_mul_u32_u24_e32 v212, 0x480, v230\n" \
"v_mfma_f32_16x16x32_bf16 a[176:179], v[136:139], v[164:167], a[176:179]\n" \
"ds_read_b128 v[72:75], v28 offset:2048\n" \
"v_add3_u32 v7, v212, v229, s40\n" \
"v_mfma_f32_16x16x32_bf16 a[208:211], v[76:79], v[100:103], a[208:211]\n" \
"ds_read_b128 v[136:139], v29 offset:2048\n" \
"s_lshl_b32 s7, s3, 3\n" \
"v_mfma_f32_16x16x32_bf16 a[208:211], v[140:143], v[164:167], a[208:211]\n" \
"ds_read_b64_tr_b16 v[94:95], v34 offset:1024\n" \
"s_lshl_b32 s40, s3, 1\n" \
"v_mfma_f32_16x16x32_bf16 a[212:215], v[76:79], v[104:107], a[212:215]\n" \
"ds_read_b64_tr_b16 v[156:157], v34 offset:8192\n" \
"s_and_b32 s7, s7, 16\n" \
"v_mfma_f32_16x16x32_bf16 a[212:215], v[140:143], v[168:171], a[212:215]\n" \
"ds_read_b64_tr_b16 v[158:159], v34 offset:9216\n" \
"s_and_b32 s40, s40, 2\n" \
"v_mfma_f32_16x16x32_bf16 a[216:219], v[76:79], v[108:111], a[216:219]\n" \
"ds_read_b64_tr_b16 v[96:97], v35 offset:0\n" \
"s_and_b32 s5, s2, 7\n" \
"v_mfma_f32_16x16x32_bf16 a[216:219], v[140:143], v[172:175], a[216:219]\n" \
"ds_read_b64_tr_b16 v[98:99], v35 offset:1024\n" \
"v_bfe_u32 v212, v20, 7, 1\n" \
"v_mfma_f32_16x16x32_bf16 a[220:223], v[76:79], v[112:115], a[220:223]\n" \
"ds_read_b64_tr_b16 v[160:161], v35 offset:8192\n" \
"s_or_b32 s7, s40, s7\n" \
"v_mfma_f32_16x16x32_bf16 a[220:223], v[140:143], v[176:179], a[220:223]\n" \
"ds_read_b128 v[76:79], v28 offset:4096\n" \
"v_and_b32_e32 v234, 0xf0, v234\n" \
"v_mfma_f32_16x16x32_bf16 a[252:255], v[80:83], v[112:115], a[252:255]\n" \
"ds_read_b128 v[140:143], v29 offset:4096\n" \
"v_cmp_gt_u32_e64 vcc, 16, v231\n" \
"v_mfma_f32_16x16x32_bf16 a[252:255], v[144:147], v[176:179], a[252:255]\n" \
"ds_read_b64_tr_b16 v[162:163], v35 offset:9216\n" \
"v_or3_b32 v212, s7, v212, v216\n" \
"v_mfma_f32_16x16x32_bf16 a[248:251], v[80:83], v[108:111], a[248:251]\n" \
"v_and_b32_e32 v213, 32, v20\n" \
"s_lshl_b32 s5, s5, 5\n" \
"v_mfma_f32_16x16x32_bf16 a[248:251], v[144:147], v[172:175], a[248:251]\n" \
"v_cndmask_b32_e32 v231, v233, v234, vcc\n" \
"v_mul_u32_u24_e32 v212, 18, v212\n" \
"v_mfma_f32_16x16x32_bf16 a[244:247], v[80:83], v[104:107], a[244:247]\n" \
"v_mov_b32_e32 v216, 0x100\n" \
"v_mov_b32_e64 v217, s5\n" \
"v_mfma_f32_16x16x32_bf16 a[244:247], v[144:147], v[168:171], a[244:247]\n" \
"v_cmp_eq_u32_e64 vcc, 0, v213\n" \
"v_and_or_b32 v212, v215, 1, v212\n" \
"v_mfma_f32_16x16x32_bf16 a[240:243], v[80:83], v[100:103], a[240:243]\n" \
"v_mad_u32_u24 v4, v224, s4, v223\n" \
"v_cndmask_b32_e64 v213, v216, v217, vcc\n" \
"v_mfma_f32_16x16x32_bf16 a[240:243], v[144:147], v[164:167], a[240:243]\n" \
"ds_read_b128 v[80:83], v28 offset:6144\n" \
"ds_read_b128 v[144:147], v29 offset:6144\n" \
"s_waitcnt lgkmcnt(15)\n" \
"v_mfma_f32_16x16x32_bf16 a[0:3], v[52:55], v[84:87], a[0:3]\n" \
"s_add_u32 m0, s31, 0x8c00\n" \
"s_mov_b32 s85, 0x198000\n" \
"v_mfma_f32_16x16x32_bf16 a[0:3], v[116:119], v[148:151], a[0:3]\n" \
"buffer_load_dwordx4 v27, s[68:71], s85 offen lds\n" \
"v_mfma_f32_16x16x32_bf16 a[4:7], v[52:55], v[88:91], a[4:7]\n" \
"ds_read_b64_tr_b16 v[100:101], v36 offset:0\n" \
"v_mad_u32_u24 v212, v212, s4, v213\n" \
"s_waitcnt lgkmcnt(15)\n" \
"v_mfma_f32_16x16x32_bf16 a[4:7], v[116:119], v[152:155], a[4:7]\n" \
"s_add_u32 m0, s31, 0xcc00\n" \
"s_mov_b32 s84, 0x198000\n" \
"s_waitcnt lgkmcnt(11)\n" \
"v_mfma_f32_16x16x32_bf16 a[8:11], v[52:55], v[92:95], a[8:11]\n" \
"buffer_load_dwordx4 v27, s[72:75], s84 offen lds\n" \
"s_waitcnt lgkmcnt(9)\n" \
"v_mfma_f32_16x16x32_bf16 a[8:11], v[116:119], v[156:159], a[8:11]\n" \
"ds_read_b64_tr_b16 v[102:103], v36 offset:1024\n" \
"v_and_or_b32 v212, v20, 16, v212\n" \
"s_waitcnt lgkmcnt(8)\n" \
"v_mfma_f32_16x16x32_bf16 a[12:15], v[52:55], v[96:99], a[12:15]\n" \
"ds_read_b64_tr_b16 v[164:165], v36 offset:8192\n" \
"v_add_u32_e32 v9, 0x4800, v212\n" \
"s_waitcnt lgkmcnt(5)\n" \
"v_mfma_f32_16x16x32_bf16 a[12:15], v[116:119], v[160:163], a[12:15]\n" \
"v_mul_u32_u24_e32 v212, 0x120, v215\n" \
"v_lshlrev_b32_e64 v213, 3, v214\n" \
"v_mfma_f32_16x16x32_bf16 a[44:47], v[56:59], v[96:99], a[44:47]\n" \
"v_mad_u32_u24 v8, v232, s4, v231\n" \
"v_and_or_b32 v10, v213, 24, v212\n" \
"v_mfma_f32_16x16x32_bf16 a[44:47], v[120:123], v[160:163], a[44:47]\n" \
"ds_read_b64_tr_b16 v[166:167], v36 offset:9216\n" \
"s_lshl_b32 s4, s6, 6\n" \
"v_mfma_f32_16x16x32_bf16 a[40:43], v[56:59], v[92:95], a[40:43]\n" \
"buffer_load_dwordx4 v[200:203], v192, s[76:79], s34 offen offset:0\n" \
"v_mfma_f32_16x16x32_bf16 a[40:43], v[120:123], v[156:159], a[40:43]\n" \
"v_lshrrev_b32_e32 v212, 2, v20\n" \
"v_and_or_b32 v212, v212, 60, s4\n" \
"v_mfma_f32_16x16x32_bf16 a[36:39], v[56:59], v[88:91], a[36:39]\n" \
"v_or_b32_e32 v18, 0x27000, v212\n" \
"v_lshl_or_b32 v212, s3, 9, v213\n" \
"v_mfma_f32_16x16x32_bf16 a[36:39], v[120:123], v[152:155], a[36:39]\n" \
"ds_read_b64_tr_b16 v[104:105], v37 offset:0\n" \
"v_or_b32_e32 v19, 0x27800, v212\n" \
"v_mfma_f32_16x16x32_bf16 a[32:35], v[56:59], v[84:87], a[32:35]\n" \
"v_mov_b32_e32 v219, 0x1000c0c\n" \
"v_mov_b32_e32 v220, 0xc0c0100\n" \
"v_mfma_f32_16x16x32_bf16 a[32:35], v[120:123], v[148:151], a[32:35]\n" \
".fill (L_pb + 52960 - .) / 4, 4, 0xBF800000\n" \
"L_pp13:\n" \
"s_cbranch_execz L_pret\n" \
"v_bfe_u32 v218, v20, 4, 1\n" \
"v_lshrrev_b32_e32 v213, 8, v20\n" \
"v_mfma_f32_16x16x32_bf16 a[64:67], v[60:63], v[84:87], a[64:67]\n" \
"buffer_load_dwordx4 v[204:207], v193, s[76:79], s34 offen offset:0\n" \
"v_mfma_f32_16x16x32_bf16 a[64:67], v[124:127], v[148:151], a[64:67]\n" \
"s_mov_b32 m0, s32\n" \
"ds_read_b64_tr_b16 v[106:107], v37 offset:1024\n" \
"v_mfma_f32_16x16x32_bf16 a[68:71], v[60:63], v[88:91], a[68:71]\n" \
"s_mov_b32 exec_lo, 0x0\n" \
"s_mov_b32 exec_hi, 0xff00\n" \
"global_load_lds_dword v[184:185], off\n" \
"s_mov_b64 exec, -1\n" \
"v_mfma_f32_16x16x32_bf16 a[68:71], v[124:127], v[152:155], a[68:71]\n" \
"ds_read_b64_tr_b16 v[168:169], v37 offset:8192\n" \
"v_cmp_eq_u32_e32 vcc, 0, v218\n" \
"v_mfma_f32_16x16x32_bf16 a[72:75], v[60:63], v[92:95], a[72:75]\n" \
"s_lshr_b32 s4, s2, 5\n" \
"s_and_b32 s5, s3, 1\n" \
"v_mfma_f32_16x16x32_bf16 a[72:75], v[124:127], v[156:159], a[72:75]\n" \
"buffer_load_dword v208, v194, s[76:79], s34 offen offset:32\n" \
"v_mfma_f32_16x16x32_bf16 a[76:79], v[60:63], v[96:99], a[76:79]\n" \
"ds_read_b64_tr_b16 v[170:171], v37 offset:9216\n" \
"v_bfe_u32 v217, v20, 4, 4\n" \
"v_mfma_f32_16x16x32_bf16 a[76:79], v[124:127], v[160:163], a[76:79]\n" \
"v_cndmask_b32_e64 v219, v219, v220, vcc\n" \
"v_mov_b32_e32 v220, 0x3020c0c\n" \
"v_mfma_f32_16x16x32_bf16 a[108:111], v[64:67], v[96:99], a[108:111]\n" \
"v_mov_b32_e32 v221, 0xc0c0302\n" \
"v_lshl_or_b32 v218, v213, 2, v218\n" \
"v_mfma_f32_16x16x32_bf16 a[108:111], v[128:131], v[160:163], a[108:111]\n" \
"s_xor_b32 s6, s4, 15\n" \
"v_mfma_f32_16x16x32_bf16 a[104:107], v[64:67], v[92:95], a[104:107]\n" \
"buffer_load_dword v209, v194, s[76:79], s34 offen offset:1184\n" \
"v_mfma_f32_16x16x32_bf16 a[104:107], v[128:131], v[156:159], a[104:107]\n" \
"ds_read_b64_tr_b16 v[108:109], v38 offset:0\n" \
"v_mfma_f32_16x16x32_bf16 a[100:103], v[64:67], v[88:91], a[100:103]\n" \
"s_cmpk_lt_u32 s3, 2\n" \
"s_cselect_b32 s7, s6, s4\n" \
"v_mfma_f32_16x16x32_bf16 a[100:103], v[128:131], v[152:155], a[100:103]\n" \
"ds_read_b64_tr_b16 v[110:111], v38 offset:1024\n" \
"s_lshl_b32 s40, s7, 7\n" \
"v_mfma_f32_16x16x32_bf16 a[96:99], v[64:67], v[84:87], a[96:99]\n" \
"s_lshl_b32 s41, s5, 6\n" \
"v_cndmask_b32_e32 v220, v220, v221, vcc\n" \
"v_mfma_f32_16x16x32_bf16 a[96:99], v[128:131], v[148:151], a[96:99]\n" \
"v_mov_b32_e32 v221, 0xc0c0c0c\n" \
"v_cmp_gt_u32_e64 vcc, v218, v217\n" \
"v_mfma_f32_16x16x32_bf16 a[128:131], v[68:71], v[84:87], a[128:131]\n" \
"buffer_load_dword v211, v194, s[76:79], s34 offen offset:2336\n" \
"v_mfma_f32_16x16x32_bf16 a[128:131], v[132:135], v[148:151], a[128:131]\n" \
"ds_read_b64_tr_b16 v[172:173], v38 offset:8192\n" \
"v_or_b32_e64 v218, 2, v218\n" \
"v_mfma_f32_16x16x32_bf16 a[132:135], v[68:71], v[88:91], a[132:135]\n" \
"v_lshrrev_b32_e32 v212, 7, v20\n" \
"s_or_b32 s41, s41, s40\n" \
"v_mfma_f32_16x16x32_bf16 a[132:135], v[132:135], v[152:155], a[132:135]\n" \
"ds_read_b64_tr_b16 v[174:175], v38 offset:9216\n" \
"v_cndmask_b32_e64 v40, v221, v219, vcc\n" \
"v_mfma_f32_16x16x32_bf16 a[136:139], v[68:71], v[92:95], a[136:139]\n" \
"v_cndmask_b32_e32 v42, v219, v221, vcc\n" \
"v_cmp_gt_u32_e32 vcc, v218, v217\n" \
"v_mfma_f32_16x16x32_bf16 a[136:139], v[132:135], v[156:159], a[136:139]\n" \
"s_mov_b32 s76, s20\n" \
"s_and_b32 s77, s21, 0xffff\n" \
"v_mfma_f32_16x16x32_bf16 a[140:143], v[68:71], v[96:99], a[140:143]\n" \
"s_mov_b32 s78, 0x480\n" \
"v_and_b32_e32 v218, 16, v20\n" \
"v_mfma_f32_16x16x32_bf16 a[140:143], v[132:135], v[160:163], a[140:143]\n" \
"buffer_load_ushort v210, v195, s[76:79], 0 offen\n" \
"v_mfma_f32_16x16x32_bf16 a[172:175], v[72:75], v[96:99], a[172:175]\n" \
"ds_read_b64_tr_b16 v[112:113], v39 offset:0\n" \
"s_movk_i32 s42, 0x3f80\n" \
"v_mfma_f32_16x16x32_bf16 a[172:175], v[136:139], v[160:163], a[172:175]\n" \
"v_or_b32_e32 v214, s41, v212\n" \
"v_and_b32_e32 v217, 0x320, v20\n" \
"v_mfma_f32_16x16x32_bf16 a[168:171], v[72:75], v[92:95], a[168:171]\n" \
"s_mov_b32 s76, s8\n" \
"s_and_b32 s77, s9, 0xffff\n" \
"v_mfma_f32_16x16x32_bf16 a[168:171], v[136:139], v[156:159], a[168:171]\n" \
"s_mov_b32 s78, 0x240000\n" \
"ds_read_b64_tr_b16 v[114:115], v39 offset:1024\n" \
"v_mfma_f32_16x16x32_bf16 a[164:167], v[72:75], v[88:91], a[164:167]\n" \
"s_movk_i32 s41, 0x100\n" \
"v_lshlrev_b32_e64 v218, v218, s42\n" \
"v_mfma_f32_16x16x32_bf16 a[164:167], v[136:139], v[152:155], a[164:167]\n" \
"ds_read_b64_tr_b16 v[176:177], v39 offset:8192\n" \
"v_mov_b32_e64 v219, 0\n" \
"v_mfma_f32_16x16x32_bf16 a[160:163], v[72:75], v[84:87], a[160:163]\n" \
"v_lshrrev_b32_e32 v215, 6, v20\n" \
"v_cndmask_b32_e32 v41, v221, v220, vcc\n" \
"v_mfma_f32_16x16x32_bf16 a[160:163], v[136:139], v[148:151], a[160:163]\n" \
"v_cndmask_b32_e32 v43, v220, v221, vcc\n" \
"v_cmp_eq_u32_e32 vcc, s41, v217\n" \
"v_mfma_f32_16x16x32_bf16 a[192:195], v[76:79], v[84:87], a[192:195]\n" \
"ds_read_b64_tr_b16 v[178:179], v39 offset:9216\n" \
"v_and_b32_e32 v217, 0x330, v20\n" \
"v_mfma_f32_16x16x32_bf16 a[192:195], v[140:143], v[148:151], a[192:195]\n" \
"v_bfe_u32 v216, v20, 6, 2\n" \
"v_cndmask_b32_e64 v46, v219, v218, vcc\n" \
"v_mfma_f32_16x16x32_bf16 a[196:199], v[76:79], v[88:91], a[196:199]\n" \
"v_mov_b32_e32 v218, 0x3f80\n" \
"v_cmp_eq_u32_e64 vcc, 32, v217\n" \
"v_mfma_f32_16x16x32_bf16 a[196:199], v[140:143], v[152:155], a[196:199]\n" \
"s_add_i32 s41, s40, -16\n" \
"v_lshrrev_b32_e32 v219, 1, v20\n" \
"v_mfma_f32_16x16x32_bf16 a[200:203], v[76:79], v[92:95], a[200:203]\n" \
"v_bfe_u32 v215, v215, 1, 1\n" \
"v_cndmask_b32_e64 v47, 0, v218, vcc\n" \
"v_mfma_f32_16x16x32_bf16 a[200:203], v[140:143], v[156:159], a[200:203]\n" \
"s_mov_b32 s42, 0\n" \
"v_mfma_f32_16x16x32_bf16 a[204:207], v[76:79], v[96:99], a[204:207]\n" \
"s_cmp_lg_u32 s40, 0\n" \
"s_cselect_b32 s41, s41, s42\n" \
"v_mfma_f32_16x16x32_bf16 a[204:207], v[140:143], v[160:163], a[204:207]\n" \
"v_lshl_or_b32 v217, s5, 3, v212\n" \
"v_lshlrev_b32_e32 v218, 10, v213\n" \
"s_waitcnt lgkmcnt(15)\n" \
"v_mfma_f32_16x16x32_bf16 a[236:239], v[80:83], v[96:99], a[236:239]\n" \
"v_lshlrev_b32_e32 v216, 7, v216\n" \
"v_and_b32_e64 v219, 24, v219\n" \
"v_mfma_f32_16x16x32_bf16 a[236:239], v[144:147], v[160:163], a[236:239]\n" \
"v_and_or_b32 v212, v212, 2, v215\n" \
"v_add_u32_e64 v217, s41, v217\n" \
"v_mfma_f32_16x16x32_bf16 a[232:235], v[80:83], v[92:95], a[232:235]\n" \
"v_or3_b32 v216, v216, v218, v219\n" \
"v_lshlrev_b32_e64 v212, 5, v212\n" \
"v_mfma_f32_16x16x32_bf16 a[232:235], v[144:147], v[156:159], a[232:235]\n" \
"s_mov_b32 s41, 0x14000\n" \
"v_or3_b32 v48, v216, v212, s41\n" \
"v_mfma_f32_16x16x32_bf16 a[228:231], v[80:83], v[88:91], a[228:231]\n" \
"s_lshl_b32 s41, s5, 2\n" \
"s_lshl_b32 s7, s7, 3\n" \
"v_mfma_f32_16x16x32_bf16 a[228:231], v[144:147], v[152:155], a[228:231]\n" \
"v_or_b32_e32 v212, s41, v213\n" \
"s_or_b32 s7, s7, s41\n" \
"v_mfma_f32_16x16x32_bf16 a[224:227], v[80:83], v[84:87], a[224:227]\n" \
"s_mov_b32 s41, 0xff800000\n" \
"v_mfma_f32_16x16x32_bf16 a[224:227], v[144:147], v[148:151], a[224:227]\n" \
"s_waitcnt vmcnt(7) lgkmcnt(0)\n" \
"s_barrier\n" \
"v_mfma_f32_16x16x32_bf16 a[16:19], v[52:55], v[100:103], a[16:19]\n" \
"s_add_u32 m0, s30, 0x0\n" \
"s_nop 0\n" \
"s_mov_b32 s85, 0x200\n" \
"v_mfma_f32_16x16x32_bf16 a[16:19], v[116:119], v[164:167], a[16:19]\n" \
"buffer_load_dwordx4 v22, s[64:67], s85 offen lds\n" \
"v_mfma_f32_16x16x32_bf16 a[20:23], v[52:55], v[104:107], a[20:23]\n" \
"ds_read_b64_tr_b16 v[84:85], v32 offset:32768\n" \
"v_mfma_f32_16x16x32_bf16 a[20:23], v[116:119], v[168:171], a[20:23]\n" \
"s_add_u32 m0, s30, 0x400\n" \
"s_mov_b32 s84, 0x2200\n" \
"v_mfma_f32_16x16x32_bf16 a[24:27], v[52:55], v[108:111], a[24:27]\n" \
"buffer_load_dwordx4 v23, s[64:67], s84 offen lds\n" \
"v_mfma_f32_16x16x32_bf16 a[24:27], v[116:119], v[172:175], a[24:27]\n" \
"ds_read_b64_tr_b16 v[86:87], v32 offset:33792\n" \
"v_mfma_f32_16x16x32_bf16 a[28:31], v[52:55], v[112:115], a[28:31]\n" \
"ds_read_b64_tr_b16 v[148:149], v32 offset:40960\n" \
"v_mfma_f32_16x16x32_bf16 a[28:31], v[116:119], v[176:179], a[28:31]\n" \
"ds_read_b128 v[52:55], v28 offset:49152\n" \
"v_mfma_f32_16x16x32_bf16 a[60:63], v[56:59], v[112:115], a[60:63]\n" \
"ds_read_b128 v[116:119], v29 offset:49152\n" \
"v_mfma_f32_16x16x32_bf16 a[60:63], v[120:123], v[176:179], a[60:63]\n" \
"s_add_u32 m0, s31, 0x0\n" \
"s_nop 0\n" \
"s_mov_b32 s85, 0x200000\n" \
"v_mfma_f32_16x16x32_bf16 a[56:59], v[56:59], v[108:111], a[56:59]\n" \
"buffer_load_dwordx4 v26, s[68:71], s85 offen lds\n" \
"v_mfma_f32_16x16x32_bf16 a[56:59], v[120:123], v[172:175], a[56:59]\n" \
"s_add_u32 m0, s31, 0x4000\n" \
"s_mov_b32 s84, 0x200000\n" \
"v_mfma_f32_16x16x32_bf16 a[52:55], v[56:59], v[104:107], a[52:55]\n" \
"buffer_load_dwordx4 v26, s[72:75], s84 offen lds\n" \
"v_mfma_f32_16x16x32_bf16 a[52:55], v[120:123], v[168:171], a[52:55]\n" \
"s_add_u32 m0, s30, 0x800\n" \
"s_mov_b32 s85, 0x4200\n" \
"v_mfma_f32_16x16x32_bf16 a[48:51], v[56:59], v[100:103], a[48:51]\n" \
"buffer_load_dwordx4 v22, s[64:67], s85 offen lds\n" \
"v_mfma_f32_16x16x32_bf16 a[48:51], v[120:123], v[164:167], a[48:51]\n" \
"ds_read_b128 v[56:59], v28 offset:51200\n" \
"v_mfma_f32_16x16x32_bf16 a[80:83], v[60:63], v[100:103], a[80:83]\n" \
"ds_read_b128 v[120:123], v29 offset:51200\n" \
"v_mfma_f32_16x16x32_bf16 a[80:83], v[124:127], v[164:167], a[80:83]\n" \
"s_add_u32 m0, s30, 0xc00\n" \
"s_mov_b32 s84, 0x6200\n" \
"v_mfma_f32_16x16x32_bf16 a[84:87], v[60:63], v[104:107], a[84:87]\n" \
"buffer_load_dwordx4 v23, s[64:67], s84 offen lds\n" \
"v_mfma_f32_16x16x32_bf16 a[84:87], v[124:127], v[168:171], a[84:87]\n" \
"ds_read_b64_tr_b16 v[150:151], v32 offset:41984\n" \
"v_mfma_f32_16x16x32_bf16 a[88:91], v[60:63], v[108:111], a[88:91]\n" \
"s_add_u32 m0, s31, 0x400\n" \
"s_mov_b32 s85, 0x208000\n" \
"v_mfma_f32_16x16x32_bf16 a[88:91], v[124:127], v[172:175], a[88:91]\n" \
"buffer_load_dwordx4 v26, s[68:71], s85 offen lds\n" \
"v_mfma_f32_16x16x32_bf16 a[92:95], v[60:63], v[112:115], a[92:95]\n" \
"ds_read_b64_tr_b16 v[88:89], v33 offset:32768\n" \
"v_mfma_f32_16x16x32_bf16 a[92:95], v[124:127], v[176:179], a[92:95]\n" \
"ds_read_b128 v[60:63], v28 offset:53248\n" \
"v_mfma_f32_16x16x32_bf16 a[124:127], v[64:67], v[112:115], a[124:127]\n" \
"ds_read_b128 v[124:127], v29 offset:53248\n" \
"v_mfma_f32_16x16x32_bf16 a[124:127], v[128:131], v[176:179], a[124:127]\n" \
"s_add_u32 m0, s31, 0x4400\n" \
"s_mov_b32 s84, 0x208000\n" \
"v_mfma_f32_16x16x32_bf16 a[120:123], v[64:67], v[108:111], a[120:123]\n" \
"buffer_load_dwordx4 v26, s[72:75], s84 offen lds\n" \
"v_mfma_f32_16x16x32_bf16 a[120:123], v[128:131], v[172:175], a[120:123]\n" \
"s_add_u32 m0, s30, 0x1000\n" \
"s_mov_b32 s85, 0x8200\n" \
"v_mfma_f32_16x16x32_bf16 a[116:119], v[64:67], v[104:107], a[116:119]\n" \
"buffer_load_dwordx4 v22, s[64:67], s85 offen lds\n" \
"v_mfma_f32_16x16x32_bf16 a[116:119], v[128:131], v[168:171], a[116:119]\n" \
"ds_read_b64_tr_b16 v[90:91], v33 offset:33792\n" \
"v_mfma_f32_16x16x32_bf16 a[112:115], v[64:67], v[100:103], a[112:115]\n" \
"ds_read_b64_tr_b16 v[152:153], v33 offset:40960\n" \
"v_mfma_f32_16x16x32_bf16 a[112:115], v[128:131], v[164:167], a[112:115]\n" \
"ds_read_b128 v[64:67], v28 offset:55296\n" \
"v_mfma_f32_16x16x32_bf16 a[144:147], v[68:71], v[100:103], a[144:147]\n" \
"ds_read_b128 v[128:131], v29 offset:55296\n" \
"v_mfma_f32_16x16x32_bf16 a[144:147], v[132:135], v[164:167], a[144:147]\n" \
"s_add_u32 m0, s30, 0x1400\n" \
"s_mov_b32 s84, 0xa200\n" \
"v_mfma_f32_16x16x32_bf16 a[148:151], v[68:71], v[104:107], a[148:151]\n" \
"buffer_load_dwordx4 v23, s[64:67], s84 offen lds\n" \
"v_mfma_f32_16x16x32_bf16 a[148:151], v[132:135], v[168:171], a[148:151]\n" \
"s_add_u32 m0, s31, 0x800\n" \
"s_mov_b32 s85, 0x210000\n" \
"v_mfma_f32_16x16x32_bf16 a[152:155], v[68:71], v[108:111], a[152:155]\n" \
"buffer_load_dwordx4 v27, s[68:71], s85 offen lds\n" \
"v_mfma_f32_16x16x32_bf16 a[152:155], v[132:135], v[172:175], a[152:155]\n" \
"s_add_u32 m0, s31, 0x4800\n" \
"s_mov_b32 s84, 0x210000\n" \
"v_mfma_f32_16x16x32_bf16 a[156:159], v[68:71], v[112:115], a[156:159]\n" \
"buffer_load_dwordx4 v27, s[72:75], s84 offen lds\n" \
"v_mfma_f32_16x16x32_bf16 a[156:159], v[132:135], v[176:179], a[156:159]\n" \
"ds_read_b128 v[68:71], v28 offset:32768\n" \
"v_mfma_f32_16x16x32_bf16 a[188:191], v[72:75], v[112:115], a[188:191]\n" \
"ds_read_b128 v[132:135], v29 offset:32768\n" \
"v_mfma_f32_16x16x32_bf16 a[188:191], v[136:139], v[176:179], a[188:191]\n" \
"s_add_u32 m0, s30, 0x1800\n" \
"s_mov_b32 s85, 0xc200\n" \
"v_mfma_f32_16x16x32_bf16 a[184:187], v[72:75], v[108:111], a[184:187]\n" \
"buffer_load_dwordx4 v22, s[64:67], s85 offen lds\n" \
"v_mfma_f32_16x16x32_bf16 a[184:187], v[136:139], v[172:175], a[184:187]\n" \
"ds_read_b64_tr_b16 v[154:155], v33 offset:41984\n" \
"v_mfma_f32_16x16x32_bf16 a[180:183], v[72:75], v[104:107], a[180:183]\n" \
"s_add_u32 m0, s30, 0x1c00\n" \
"s_mov_b32 s84, 0xe200\n" \
"v_mfma_f32_16x16x32_bf16 a[180:183], v[136:139], v[168:171], a[180:183]\n" \
"buffer_load_dwordx4 v23, s[64:67], s84 offen lds\n" \
"v_mfma_f32_16x16x32_bf16 a[176:179], v[72:75], v[100:103], a[176:179]\n" \
"ds_read_b64_tr_b16 v[92:93], v34 offset:32768\n" \
"v_mfma_f32_16x16x32_bf16 a[176:179], v[136:139], v[164:167], a[176:179]\n" \
"ds_read_b128 v[72:75], v28 offset:34816\n" \
"v_mfma_f32_16x16x32_bf16 a[208:211], v[76:79], v[100:103], a[208:211]\n" \
"ds_read_b128 v[136:139], v29 offset:34816\n" \
"v_mfma_f32_16x16x32_bf16 a[208:211], v[140:143], v[164:167], a[208:211]\n" \
"ds_read_b64_tr_b16 v[94:95], v34 offset:33792\n" \
"v_mfma_f32_16x16x32_bf16 a[212:215], v[76:79], v[104:107], a[212:215]\n" \
"ds_read_b64_tr_b16 v[156:157], v34 offset:40960\n" \
"v_mfma_f32_16x16x32_bf16 a[212:215], v[140:143], v[168:171], a[212:215]\n" \
"ds_read_b64_tr_b16 v[158:159], v34 offset:41984\n" \
"v_mfma_f32_16x16x32_bf16 a[216:219], v[76:79], v[108:111], a[216:219]\n" \
"ds_read_b64_tr_b16 v[96:97], v35 offset:32768\n" \
"v_mfma_f32_16x16x32_bf16 a[216:219], v[140:143], v[172:175], a[216:219]\n" \
"ds_read_b64_tr_b16 v[98:99], v35 offset:33792\n" \
"v_mfma_f32_16x16x32_bf16 a[220:223], v[76:79], v[112:115], a[220:223]\n" \
"ds_read_b64_tr_b16 v[160:161], v35 offset:40960\n" \
"v_mfma_f32_16x16x32_bf16 a[220:223], v[140:143], v[176:179], a[220:223]\n" \
"ds_read_b128 v[76:79], v28 offset:36864\n" \
"v_mfma_f32_16x16x32_bf16 a[252:255], v[80:83], v[112:115], a[252:255]\n" \
"ds_read_b128 v[140:143], v29 offset:36864\n" \
"v_mfma_f32_16x16x32_bf16 a[252:255], v[144:147], v[176:179], a[252:255]\n" \
"ds_read_b64_tr_b16 v[162:163], v35 offset:41984\n" \
"v_mfma_f32_16x16x32_bf16 a[248:251], v[80:83], v[108:111], a[248:251]\n" \
"s_cmp_lg_u32 s40, 0\n" \
"s_cselect_b32 s47, s42, s41\n" \
"v_mfma_f32_16x16x32_bf16 a[248:251], v[144:147], v[172:175], a[248:251]\n" \
"s_mov_b32 s40, 0xc000\n" \
"s_mov_b32 s41, 0x8000\n" \
"v_mfma_f32_16x16x32_bf16 a[244:247], v[80:83], v[104:107], a[244:247]\n" \
"s_cmp_lg_u32 s4, 0\n" \
"s_cselect_b32 s51, s40, s41\n" \
"v_mfma_f32_16x16x32_bf16 a[244:247], v[144:147], v[168:171], a[244:247]\n" \
"s_movk_i32 s40, 0x4000\n" \
"v_mfma_f32_16x16x32_bf16 a[240:243], v[80:83], v[100:103], a[240:243]\n" \
"s_cmp_lg_u32 s4, 0\n" \
"s_cselect_b32 s53, s41, s40\n" \
"v_mfma_f32_16x16x32_bf16 a[240:243], v[144:147], v[164:167], a[240:243]\n" \
"ds_read_b128 v[80:83], v28 offset:38912\n" \
"ds_read_b128 v[144:147], v29 offset:38912\n" \
"s_waitcnt lgkmcnt(15)\n" \
"v_mfma_f32_16x16x32_bf16 a[0:3], v[52:55], v[84:87], a[0:3]\n" \
"s_add_u32 m0, s31, 0xc00\n" \
"s_mov_b32 s85, 0x218000\n" \
"v_mfma_f32_16x16x32_bf16 a[0:3], v[116:119], v[148:151], a[0:3]\n" \
"buffer_load_dwordx4 v27, s[68:71], s85 offen lds\n" \
"v_mfma_f32_16x16x32_bf16 a[4:7], v[52:55], v[88:91], a[4:7]\n" \
"ds_read_b64_tr_b16 v[100:101], v36 offset:32768\n" \
"s_lshl_b32 s40, s2, 2\n" \
"s_waitcnt lgkmcnt(15)\n" \
"v_mfma_f32_16x16x32_bf16 a[4:7], v[116:119], v[152:155], a[4:7]\n" \
"s_add_u32 m0, s31, 0x4c00\n" \
"s_mov_b32 s84, 0x218000\n" \
"s_waitcnt lgkmcnt(11)\n" \
"v_mfma_f32_16x16x32_bf16 a[8:11], v[52:55], v[92:95], a[8:11]\n" \
"buffer_load_dwordx4 v27, s[72:75], s84 offen lds\n" \
"s_waitcnt lgkmcnt(9)\n" \
"v_mfma_f32_16x16x32_bf16 a[8:11], v[116:119], v[156:159], a[8:11]\n" \
"ds_read_b64_tr_b16 v[102:103], v36 offset:33792\n" \
"s_and_b32 s40, s40, 28\n" \
"s_waitcnt lgkmcnt(8)\n" \
"v_mfma_f32_16x16x32_bf16 a[12:15], v[52:55], v[96:99], a[12:15]\n" \
"ds_read_b64_tr_b16 v[164:165], v36 offset:40960\n" \
"s_bfe_u32 s45, s2, 0x20003\n" \
"s_waitcnt lgkmcnt(5)\n" \
"v_mfma_f32_16x16x32_bf16 a[12:15], v[116:119], v[160:163], a[12:15]\n" \
"s_lshr_b32 s44, s3, 1\n" \
"s_or_b32 s46, s40, s45\n" \
"v_mfma_f32_16x16x32_bf16 a[44:47], v[56:59], v[96:99], a[44:47]\n" \
"s_lshl_b32 s43, s44, 15\n" \
"s_lshl_b32 s41, s5, 14\n" \
"v_mfma_f32_16x16x32_bf16 a[44:47], v[120:123], v[160:163], a[44:47]\n" \
"ds_read_b64_tr_b16 v[166:167], v36 offset:41984\n" \
"s_lshl_b32 s48, s46, 20\n" \
"v_mfma_f32_16x16x32_bf16 a[40:43], v[56:59], v[92:95], a[40:43]\n" \
"s_or_b32 s41, s43, s41\n" \
"s_add_i32 s49, s48, 0x1000000\n" \
"v_mfma_f32_16x16x32_bf16 a[40:43], v[120:123], v[156:159], a[40:43]\n" \
"s_or_b32 s42, s49, s41\n" \
"s_lshl_b32 s4, s4, 16\n" \
"v_mfma_f32_16x16x32_bf16 a[36:39], v[56:59], v[88:91], a[36:39]\n" \
"s_or_b32 s41, s42, s4\n" \
"s_lshl_b32 s4, s6, 16\n" \
"v_mfma_f32_16x16x32_bf16 a[36:39], v[120:123], v[152:155], a[36:39]\n" \
"ds_read_b64_tr_b16 v[104:105], v37 offset:32768\n" \
"s_or_b32 s42, s42, s4\n" \
"v_mfma_f32_16x16x32_bf16 a[32:35], v[56:59], v[84:87], a[32:35]\n" \
"s_lshl_b32 s4, s5, 11\n" \
"s_mul_i32 s5, s7, 0x24800\n" \
"v_mfma_f32_16x16x32_bf16 a[32:35], v[120:123], v[148:151], a[32:35]\n" \
"s_lshl_b32 s6, s46, 9\n" \
"s_lshl_b32 s7, s40, 6\n" \
"v_mfma_f32_16x16x32_bf16 a[64:67], v[60:63], v[84:87], a[64:67]\n" \
"s_waitcnt vmcnt(16)\n" \
"v_or_b32_e32 v204, v210, v204\n" \
"v_or_b32_e32 v206, v210, v206\n" \
"v_mfma_f32_16x16x32_bf16 a[64:67], v[124:127], v[148:151], a[64:67]\n" \
"v_mov_b32_e64 v210, 0\n" \
"s_add_u32 s88, s35, 0x1000\n" \
"v_mfma_f32_16x16x32_bf16 a[68:71], v[60:63], v[88:91], a[68:71]\n" \
"ds_read_b64_tr_b16 v[106:107], v37 offset:33792\n" \
"s_add_i32 s6, s5, s6\n" \
"v_mfma_f32_16x16x32_bf16 a[68:71], v[124:127], v[152:155], a[68:71]\n" \
"ds_read_b64_tr_b16 v[168:169], v37 offset:40960\n" \
"s_or_b32 s5, s5, s7\n" \
"v_mfma_f32_16x16x32_bf16 a[72:75], v[60:63], v[92:95], a[72:75]\n" \
"s_mov_b32 m0, s32\n" \
"s_lshl_b32 s7, s45, 2\n" \
"v_mfma_f32_16x16x32_bf16 a[72:75], v[124:127], v[156:159], a[72:75]\n" \
"s_mov_b32 exec_lo, 0xffffffff\n" \
"s_mov_b32 exec_hi, 0x0\n" \
"global_load_lds_dword v[188:189], off\n" \
"s_mov_b64 exec, -1\n" \
"v_mfma_f32_16x16x32_bf16 a[76:79], v[60:63], v[96:99], a[76:79]\n" \
"ds_read_b64_tr_b16 v[170:171], v37 offset:41984\n" \
"s_or_b32 s5, s5, s7\n" \
"v_mfma_f32_16x16x32_bf16 a[76:79], v[124:127], v[160:163], a[76:79]\n" \
"s_lshl_b32 s7, s3, 12\n" \
"s_or_b32 s50, s49, s7\n" \
"v_mfma_f32_16x16x32_bf16 a[108:111], v[64:67], v[96:99], a[108:111]\n" \
"ds_write_b128 v196, v[200:203]\n" \
"v_mfma_f32_16x16x32_bf16 a[108:111], v[128:131], v[160:163], a[108:111]\n" \
"s_lshl_b32 s7, s3, 11\n" \
"v_lshrrev_b32_e32 v215, 4, v20\n" \
"v_mfma_f32_16x16x32_bf16 a[104:107], v[64:67], v[92:95], a[104:107]\n" \
"ds_read_b64_tr_b16 v[108:109], v38 offset:32768\n" \
"v_bfe_u32 v216, v20, 4, 3\n" \
"v_mfma_f32_16x16x32_bf16 a[104:107], v[128:131], v[156:159], a[104:107]\n" \
"s_add_i32 s52, s48, s7\n" \
"s_add_i32 s53, s53, s7\n" \
"v_mfma_f32_16x16x32_bf16 a[100:103], v[64:67], v[88:91], a[100:103]\n" \
"v_mul_u32_u24_e32 v214, 0x480, v214\n" \
"v_bitop3_b32 v218, v213, v215, 7 bitop3:0x78\n" \
"v_mfma_f32_16x16x32_bf16 a[100:103], v[128:131], v[152:155], a[100:103]\n" \
"ds_read_b64_tr_b16 v[110:111], v38 offset:33792\n" \
"v_bitop3_b32 v213, v213, v216, 4 bitop3:0x36\n" \
"v_mfma_f32_16x16x32_bf16 a[96:99], v[64:67], v[84:87], a[96:99]\n" \
"s_lshl_b32 s7, s3, 10\n" \
"s_or_b32 s4, s43, s4\n" \
"v_mfma_f32_16x16x32_bf16 a[96:99], v[128:131], v[148:151], a[96:99]\n" \
"v_lshl_or_b32 v25, v213, 4, v214\n" \
"v_mul_u32_u24_e32 v213, 0x480, v217\n" \
"v_mfma_f32_16x16x32_bf16 a[128:131], v[68:71], v[84:87], a[128:131]\n" \
"ds_write_b128 v197, v[204:207]\n" \
"v_mfma_f32_16x16x32_bf16 a[128:131], v[132:135], v[148:151], a[128:131]\n" \
"ds_read_b64_tr_b16 v[172:173], v38 offset:40960\n" \
"v_bitop3_b32 v212, v215, v212, 7 bitop3:0x6c\n" \
"v_mfma_f32_16x16x32_bf16 a[132:135], v[68:71], v[88:91], a[132:135]\n" \
"s_or_b32 s40, s7, 0x14000\n" \
"s_lshl_b32 s43, s44, 13\n" \
"v_mfma_f32_16x16x32_bf16 a[132:135], v[132:135], v[152:155], a[132:135]\n" \
"ds_read_b64_tr_b16 v[174:175], v38 offset:41984\n" \
"s_mulk_i32 s44, 0xe800\n" \
"v_mfma_f32_16x16x32_bf16 a[136:139], v[68:71], v[92:95], a[136:139]\n" \
"s_lshl_b32 s7, s3, 8\n" \
"s_nop 0\n" \
"v_lshl_or_b32 v24, v218, 4, v214\n" \
"v_mfma_f32_16x16x32_bf16 a[136:139], v[132:135], v[156:159], a[136:139]\n" \
"v_lshl_or_b32 v21, v212, 4, v213\n" \
"s_add_i32 s44, s44, 0x17000\n" \
"v_mfma_f32_16x16x32_bf16 a[140:143], v[68:71], v[96:99], a[140:143]\n" \
"s_or_b32 s45, s7, 0x27000\n" \
"s_or_b32 s46, s4, 0x10000\n" \
"v_mfma_f32_16x16x32_bf16 a[140:143], v[132:135], v[160:163], a[140:143]\n" \
"ds_read_b64_tr_b16 v[112:113], v39 offset:32768\n" \
"s_add_i32 s48, s6, 0x3000000\n" \
"v_mfma_f32_16x16x32_bf16 a[172:175], v[72:75], v[96:99], a[172:175]\n" \
"ds_write_b128 v198, v[208:211]\n" \
"v_mfma_f32_16x16x32_bf16 a[172:175], v[136:139], v[160:163], a[172:175]\n" \
"s_add_i32 s49, s5, 0x3004000\n" \
"s_add_i32 s52, s52, 0x1010000\n" \
"v_mfma_f32_16x16x32_bf16 a[168:171], v[72:75], v[92:95], a[168:171]\n" \
"ds_read_b64_tr_b16 v[114:115], v39 offset:33792\n" \
"v_mfma_f32_16x16x32_bf16 a[168:171], v[136:139], v[156:159], a[168:171]\n" \
"v_mfma_f32_16x16x32_bf16 a[164:167], v[72:75], v[88:91], a[164:167]\n" \
"v_mfma_f32_16x16x32_bf16 a[164:167], v[136:139], v[152:155], a[164:167]\n" \
"ds_read_b64_tr_b16 v[176:177], v39 offset:40960\n" \
"v_mfma_f32_16x16x32_bf16 a[160:163], v[72:75], v[84:87], a[160:163]\n" \
"v_mfma_f32_16x16x32_bf16 a[160:163], v[136:139], v[148:151], a[160:163]\n" \
"v_mfma_f32_16x16x32_bf16 a[192:195], v[76:79], v[84:87], a[192:195]\n" \
"ds_read_b64_tr_b16 v[178:179], v39 offset:41984\n" \
"v_mfma_f32_16x16x32_bf16 a[192:195], v[140:143], v[148:151], a[192:195]\n" \
"v_mfma_f32_16x16x32_bf16 a[196:199], v[76:79], v[88:91], a[196:199]\n" \
"v_mfma_f32_16x16x32_bf16 a[196:199], v[140:143], v[152:155], a[196:199]\n" \
"v_mfma_f32_16x16x32_bf16 a[200:203], v[76:79], v[92:95], a[200:203]\n" \
"v_mfma_f32_16x16x32_bf16 a[200:203], v[140:143], v[156:159], a[200:203]\n" \
"v_mfma_f32_16x16x32_bf16 a[204:207], v[76:79], v[96:99], a[204:207]\n" \
"v_mfma_f32_16x16x32_bf16 a[204:207], v[140:143], v[160:163], a[204:207]\n" \
"s_waitcnt lgkmcnt(15)\n" \
"v_mfma_f32_16x16x32_bf16 a[236:239], v[80:83], v[96:99], a[236:239]\n" \
"v_mfma_f32_16x16x32_bf16 a[236:239], v[144:147], v[160:163], a[236:239]\n" \
"v_mfma_f32_16x16x32_bf16 a[232:235], v[80:83], v[92:95], a[232:235]\n" \
"v_mfma_f32_16x16x32_bf16 a[232:235], v[144:147], v[156:159], a[232:235]\n" \
"v_mfma_f32_16x16x32_bf16 a[228:231], v[80:83], v[88:91], a[228:231]\n" \
"v_mfma_f32_16x16x32_bf16 a[228:231], v[144:147], v[152:155], a[228:231]\n" \
"v_mfma_f32_16x16x32_bf16 a[224:227], v[80:83], v[84:87], a[224:227]\n" \
"v_mfma_f32_16x16x32_bf16 a[224:227], v[144:147], v[148:151], a[224:227]\n" \
"s_waitcnt vmcnt(1) lgkmcnt(0)\n" \
"s_barrier\n" \
"s_nop 0\n" \
"v_mfma_f32_16x16x32_bf16 a[16:19], v[52:55], v[100:103], a[16:19]\n" \
"s_add_u32 m0, s30, 0x8000\n" \
"s_mov_b32 s85, 0x280\n" \
"v_mfma_f32_16x16x32_bf16 a[16:19], v[116:119], v[164:167], a[16:19]\n" \
"buffer_load_dwordx4 v22, s[64:67], s85 offen lds\n" \
"v_mfma_f32_16x16x32_bf16 a[20:23], v[52:55], v[104:107], a[20:23]\n" \
"s_add_u32 m0, s30, 0x8400\n" \
"s_mov_b32 s84, 0x2280\n" \
"v_mfma_f32_16x16x32_bf16 a[20:23], v[116:119], v[168:171], a[20:23]\n" \
"buffer_load_dwordx4 v23, s[64:67], s84 offen lds\n" \
"v_mfma_f32_16x16x32_bf16 a[24:27], v[52:55], v[108:111], a[24:27]\n" \
"ds_read_b64_tr_b16 v[84:85], v32 offset:0\n" \
"v_mfma_f32_16x16x32_bf16 a[24:27], v[116:119], v[172:175], a[24:27]\n" \
"s_add_u32 m0, s31, 0x8000\n" \
"s_mov_b32 s85, 0x280000\n" \
"v_mfma_f32_16x16x32_bf16 a[28:31], v[52:55], v[112:115], a[28:31]\n" \
"buffer_load_dwordx4 v26, s[68:71], s85 offen lds\n" \
"v_mfma_f32_16x16x32_bf16 a[28:31], v[116:119], v[176:179], a[28:31]\n" \
"ds_read_b128 v[52:55], v28 offset:16384\n" \
"v_mfma_f32_16x16x32_bf16 a[60:63], v[56:59], v[112:115], a[60:63]\n" \
"ds_read_b128 v[116:119], v29 offset:16384\n" \
"v_mfma_f32_16x16x32_bf16 a[60:63], v[120:123], v[176:179], a[60:63]\n" \
"s_add_u32 m0, s31, 0xc000\n" \
"s_mov_b32 s84, 0x280000\n" \
"v_mfma_f32_16x16x32_bf16 a[56:59], v[56:59], v[108:111], a[56:59]\n" \
"buffer_load_dwordx4 v26, s[72:75], s84 offen lds\n" \
"v_mfma_f32_16x16x32_bf16 a[56:59], v[120:123], v[172:175], a[56:59]\n" \
"s_add_u32 m0, s30, 0x8800\n" \
"s_mov_b32 s85, 0x4280\n" \
"v_mfma_f32_16x16x32_bf16 a[52:55], v[56:59], v[104:107], a[52:55]\n" \
"buffer_load_dwordx4 v22, s[64:67], s85 offen lds\n" \
"v_mfma_f32_16x16x32_bf16 a[52:55], v[120:123], v[168:171], a[52:55]\n" \
"ds_read_b64_tr_b16 v[86:87], v32 offset:1024\n" \
"v_mfma_f32_16x16x32_bf16 a[48:51], v[56:59], v[100:103], a[48:51]\n" \
"ds_read_b64_tr_b16 v[148:149], v32 offset:8192\n" \
"v_mfma_f32_16x16x32_bf16 a[48:51], v[120:123], v[164:167], a[48:51]\n" \
"ds_read_b128 v[56:59], v28 offset:18432\n" \
"v_mfma_f32_16x16x32_bf16 a[80:83], v[60:63], v[100:103], a[80:83]\n" \
"ds_read_b128 v[120:123], v29 offset:18432\n" \
"v_mfma_f32_16x16x32_bf16 a[80:83], v[124:127], v[164:167], a[80:83]\n" \
"s_add_u32 m0, s30, 0x8c00\n" \
"s_mov_b32 s84, 0x6280\n" \
"v_mfma_f32_16x16x32_bf16 a[84:87], v[60:63], v[104:107], a[84:87]\n" \
"buffer_load_dwordx4 v23, s[64:67], s84 offen lds\n" \
"v_mfma_f32_16x16x32_bf16 a[84:87], v[124:127], v[168:171], a[84:87]\n" \
"s_add_u32 m0, s31, 0x8400\n" \
"s_mov_b32 s85, 0x288000\n" \
"v_mfma_f32_16x16x32_bf16 a[88:91], v[60:63], v[108:111], a[88:91]\n" \
"buffer_load_dwordx4 v26, s[68:71], s85 offen lds\n" \
"v_mfma_f32_16x16x32_bf16 a[88:91], v[124:127], v[172:175], a[88:91]\n" \
"s_add_u32 m0, s31, 0xc400\n" \
"s_mov_b32 s84, 0x288000\n" \
"v_mfma_f32_16x16x32_bf16 a[92:95], v[60:63], v[112:115], a[92:95]\n" \
"buffer_load_dwordx4 v26, s[72:75], s84 offen lds\n" \
"v_mfma_f32_16x16x32_bf16 a[92:95], v[124:127], v[176:179], a[92:95]\n" \
"ds_read_b128 v[60:63], v28 offset:20480\n" \
"v_mfma_f32_16x16x32_bf16 a[124:127], v[64:67], v[112:115], a[124:127]\n" \
"ds_read_b128 v[124:127], v29 offset:20480\n" \
"v_mfma_f32_16x16x32_bf16 a[124:127], v[128:131], v[176:179], a[124:127]\n" \
"s_add_u32 m0, s30, 0x9000\n" \
"s_mov_b32 s85, 0x8280\n" \
"v_mfma_f32_16x16x32_bf16 a[120:123], v[64:67], v[108:111], a[120:123]\n" \
"buffer_load_dwordx4 v22, s[64:67], s85 offen lds\n" \
"v_mfma_f32_16x16x32_bf16 a[120:123], v[128:131], v[172:175], a[120:123]\n" \
"s_add_u32 m0, s30, 0x9400\n" \
"s_mov_b32 s84, 0xa280\n" \
"v_mfma_f32_16x16x32_bf16 a[116:119], v[64:67], v[104:107], a[116:119]\n" \
"buffer_load_dwordx4 v23, s[64:67], s84 offen lds\n" \
"v_mfma_f32_16x16x32_bf16 a[116:119], v[128:131], v[168:171], a[116:119]\n" \
"s_add_u32 m0, s31, 0x8800\n" \
"s_mov_b32 s85, 0x290000\n" \
"v_mfma_f32_16x16x32_bf16 a[112:115], v[64:67], v[100:103], a[112:115]\n" \
"buffer_load_dwordx4 v27, s[68:71], s85 offen lds\n" \
"v_mfma_f32_16x16x32_bf16 a[112:115], v[128:131], v[164:167], a[112:115]\n" \
"ds_read_b128 v[64:67], v28 offset:22528\n" \
"v_mfma_f32_16x16x32_bf16 a[144:147], v[68:71], v[100:103], a[144:147]\n" \
"ds_read_b128 v[128:131], v29 offset:22528\n" \
"v_mfma_f32_16x16x32_bf16 a[144:147], v[132:135], v[164:167], a[144:147]\n" \
"s_add_u32 m0, s31, 0xc800\n" \
"s_mov_b32 s84, 0x290000\n" \
"v_mfma_f32_16x16x32_bf16 a[148:151], v[68:71], v[104:107], a[148:151]\n" \
"buffer_load_dwordx4 v27, s[72:75], s84 offen lds\n" \
"v_mfma_f32_16x16x32_bf16 a[148:151], v[132:135], v[168:171], a[148:151]\n" \
"s_add_u32 m0, s30, 0x9800\n" \
"s_mov_b32 s85, 0xc280\n" \
"v_mfma_f32_16x16x32_bf16 a[152:155], v[68:71], v[108:111], a[152:155]\n" \
"buffer_load_dwordx4 v22, s[64:67], s85 offen lds\n" \
"v_mfma_f32_16x16x32_bf16 a[152:155], v[132:135], v[172:175], a[152:155]\n" \
"s_add_u32 m0, s30, 0x9c00\n" \
"s_mov_b32 s84, 0xe280\n" \
"v_mfma_f32_16x16x32_bf16 a[156:159], v[68:71], v[112:115], a[156:159]\n" \
"buffer_load_dwordx4 v23, s[64:67], s84 offen lds\n" \
"v_mfma_f32_16x16x32_bf16 a[156:159], v[132:135], v[176:179], a[156:159]\n" \
"ds_read_b128 v[68:71], v28 offset:0\n" \
"v_mfma_f32_16x16x32_bf16 a[188:191], v[72:75], v[112:115], a[188:191]\n" \
"ds_read_b128 v[132:135], v29 offset:0\n" \
"v_mfma_f32_16x16x32_bf16 a[188:191], v[136:139], v[176:179], a[188:191]\n" \
"ds_read_b128 v[200:203], v199 offset:0\n" \
"v_mfma_f32_16x16x32_bf16 a[184:187], v[72:75], v[108:111], a[184:187]\n" \
"ds_read_b128 v[204:207], v199 offset:4096\n" \
"v_mfma_f32_16x16x32_bf16 a[184:187], v[136:139], v[172:175], a[184:187]\n" \
"ds_read_b64_tr_b16 v[150:151], v32 offset:9216\n" \
"v_mfma_f32_16x16x32_bf16 a[180:183], v[72:75], v[104:107], a[180:183]\n" \
"ds_read_b64_tr_b16 v[88:89], v33 offset:0\n" \
"v_mfma_f32_16x16x32_bf16 a[180:183], v[136:139], v[168:171], a[180:183]\n" \
"ds_read_b128 v[208:211], v199 offset:8192\n" \
"v_mfma_f32_16x16x32_bf16 a[176:179], v[72:75], v[100:103], a[176:179]\n" \
"ds_read_b64_tr_b16 v[90:91], v33 offset:1024\n" \
"v_mfma_f32_16x16x32_bf16 a[176:179], v[136:139], v[164:167], a[176:179]\n" \
"ds_read_b128 v[72:75], v28 offset:2048\n" \
"v_mfma_f32_16x16x32_bf16 a[208:211], v[76:79], v[100:103], a[208:211]\n" \
"ds_read_b128 v[136:139], v29 offset:2048\n" \
"v_mfma_f32_16x16x32_bf16 a[208:211], v[140:143], v[164:167], a[208:211]\n" \
".fill (L_pb + 57056 - .) / 4, 4, 0xBF800000\n" \
"L_pp14:\n" \
"s_cbranch_execz L_pret\n" \
"s_nop 0\n" \
"ds_read_b64_tr_b16 v[152:153], v33 offset:8192\n" \
"v_mfma_f32_16x16x32_bf16 a[212:215], v[76:79], v[104:107], a[212:215]\n" \
"ds_read_b64_tr_b16 v[154:155], v33 offset:9216\n" \
"v_mfma_f32_16x16x32_bf16 a[212:215], v[140:143], v[168:171], a[212:215]\n" \
"ds_read_b64_tr_b16 v[92:93], v34 offset:0\n" \
"v_mfma_f32_16x16x32_bf16 a[216:219], v[76:79], v[108:111], a[216:219]\n" \
"ds_read_b64_tr_b16 v[94:95], v34 offset:1024\n" \
"v_mfma_f32_16x16x32_bf16 a[216:219], v[140:143], v[172:175], a[216:219]\n" \
"ds_read_b64_tr_b16 v[156:157], v34 offset:8192\n" \
"v_mfma_f32_16x16x32_bf16 a[220:223], v[76:79], v[112:115], a[220:223]\n" \
"ds_read_b64_tr_b16 v[158:159], v34 offset:9216\n" \
"v_mfma_f32_16x16x32_bf16 a[220:223], v[140:143], v[176:179], a[220:223]\n" \
"ds_read_b128 v[76:79], v28 offset:4096\n" \
"v_mfma_f32_16x16x32_bf16 a[252:255], v[80:83], v[112:115], a[252:255]\n" \
"ds_read_b128 v[140:143], v29 offset:4096\n" \
"v_mfma_f32_16x16x32_bf16 a[252:255], v[144:147], v[176:179], a[252:255]\n" \
"ds_read_b64_tr_b16 v[96:97], v35 offset:0\n" \
"v_mfma_f32_16x16x32_bf16 a[248:251], v[80:83], v[108:111], a[248:251]\n" \
"ds_read_b64_tr_b16 v[98:99], v35 offset:1024\n" \
"v_mfma_f32_16x16x32_bf16 a[248:251], v[144:147], v[172:175], a[248:251]\n" \
"ds_read_b64_tr_b16 v[160:161], v35 offset:8192\n" \
"v_mfma_f32_16x16x32_bf16 a[244:247], v[80:83], v[104:107], a[244:247]\n" \
"ds_read_b64_tr_b16 v[162:163], v35 offset:9216\n" \
"v_mfma_f32_16x16x32_bf16 a[244:247], v[144:147], v[168:171], a[244:247]\n" \
"v_mfma_f32_16x16x32_bf16 a[240:243], v[80:83], v[100:103], a[240:243]\n" \
"v_mfma_f32_16x16x32_bf16 a[240:243], v[144:147], v[164:167], a[240:243]\n" \
"ds_read_b128 v[80:83], v28 offset:6144\n" \
"ds_read_b128 v[144:147], v29 offset:6144\n" \
"s_waitcnt lgkmcnt(15)\n" \
"v_mfma_f32_16x16x32_bf16 a[0:3], v[52:55], v[84:87], a[0:3]\n" \
"s_add_u32 m0, s31, 0x8c00\n" \
"s_mov_b32 s85, 0x298000\n" \
"v_mfma_f32_16x16x32_bf16 a[0:3], v[116:119], v[148:151], a[0:3]\n" \
"buffer_load_dwordx4 v27, s[68:71], s85 offen lds\n" \
"v_mfma_f32_16x16x32_bf16 a[4:7], v[52:55], v[88:91], a[4:7]\n" \
"ds_read_b64_tr_b16 v[100:101], v36 offset:0\n" \
"s_waitcnt lgkmcnt(13)\n" \
"v_mfma_f32_16x16x32_bf16 a[4:7], v[116:119], v[152:155], a[4:7]\n" \
"s_add_u32 m0, s31, 0xcc00\n" \
"s_mov_b32 s84, 0x298000\n" \
"s_waitcnt lgkmcnt(11)\n" \
"v_mfma_f32_16x16x32_bf16 a[8:11], v[52:55], v[92:95], a[8:11]\n" \
"buffer_load_dwordx4 v27, s[72:75], s84 offen lds\n" \
"s_waitcnt lgkmcnt(9)\n" \
"v_mfma_f32_16x16x32_bf16 a[8:11], v[116:119], v[156:159], a[8:11]\n" \
"ds_read_b64_tr_b16 v[102:103], v36 offset:1024\n" \
"s_waitcnt lgkmcnt(6)\n" \
"v_mfma_f32_16x16x32_bf16 a[12:15], v[52:55], v[96:99], a[12:15]\n" \
"ds_read_b64_tr_b16 v[164:165], v36 offset:8192\n" \
"s_waitcnt lgkmcnt(5)\n" \
"v_mfma_f32_16x16x32_bf16 a[12:15], v[116:119], v[160:163], a[12:15]\n" \
"v_mfma_f32_16x16x32_bf16 a[44:47], v[56:59], v[96:99], a[44:47]\n" \
"v_mfma_f32_16x16x32_bf16 a[44:47], v[120:123], v[160:163], a[44:47]\n" \
"ds_read_b64_tr_b16 v[166:167], v36 offset:9216\n" \
"v_mfma_f32_16x16x32_bf16 a[40:43], v[56:59], v[92:95], a[40:43]\n" \
"v_mfma_f32_16x16x32_bf16 a[40:43], v[120:123], v[156:159], a[40:43]\n" \
"v_mfma_f32_16x16x32_bf16 a[36:39], v[56:59], v[88:91], a[36:39]\n" \
"v_mfma_f32_16x16x32_bf16 a[36:39], v[120:123], v[152:155], a[36:39]\n" \
"ds_read_b64_tr_b16 v[104:105], v37 offset:0\n" \
"v_mfma_f32_16x16x32_bf16 a[32:35], v[56:59], v[84:87], a[32:35]\n" \
"v_mfma_f32_16x16x32_bf16 a[32:35], v[120:123], v[148:151], a[32:35]\n" \
"v_mfma_f32_16x16x32_bf16 a[64:67], v[60:63], v[84:87], a[64:67]\n" \
"ds_read_b64_tr_b16 v[106:107], v37 offset:1024\n" \
"v_mfma_f32_16x16x32_bf16 a[64:67], v[124:127], v[148:151], a[64:67]\n" \
"v_mfma_f32_16x16x32_bf16 a[68:71], v[60:63], v[88:91], a[68:71]\n" \
"v_mfma_f32_16x16x32_bf16 a[68:71], v[124:127], v[152:155], a[68:71]\n" \
"buffer_store_dwordx4 v[200:203], v20, s[80:83], s35 offen sc1\n" \
"v_mfma_f32_16x16x32_bf16 a[72:75], v[60:63], v[92:95], a[72:75]\n" \
"s_mov_b32 m0, s32\n" \
"ds_read_b64_tr_b16 v[168:169], v37 offset:8192\n" \
"v_mfma_f32_16x16x32_bf16 a[72:75], v[124:127], v[156:159], a[72:75]\n" \
"s_mov_b32 exec_lo, 0x0\n" \
"s_mov_b32 exec_hi, 0xffffffff\n" \
"global_load_lds_dword v[188:189], off\n" \
"s_mov_b64 exec, -1\n" \
"v_mfma_f32_16x16x32_bf16 a[76:79], v[60:63], v[96:99], a[76:79]\n" \
"ds_read_b64_tr_b16 v[170:171], v37 offset:9216\n" \
"v_mfma_f32_16x16x32_bf16 a[76:79], v[124:127], v[160:163], a[76:79]\n" \
"v_mfma_f32_16x16x32_bf16 a[108:111], v[64:67], v[96:99], a[108:111]\n" \
"v_mfma_f32_16x16x32_bf16 a[108:111], v[128:131], v[160:163], a[108:111]\n" \
"v_mfma_f32_16x16x32_bf16 a[104:107], v[64:67], v[92:95], a[104:107]\n" \
"ds_read_b64_tr_b16 v[108:109], v38 offset:0\n" \
"v_mfma_f32_16x16x32_bf16 a[104:107], v[128:131], v[156:159], a[104:107]\n" \
"v_mfma_f32_16x16x32_bf16 a[100:103], v[64:67], v[88:91], a[100:103]\n" \
"buffer_store_dwordx4 v[204:207], v20, s[80:83], s88 offen sc1\n" \
"v_mfma_f32_16x16x32_bf16 a[100:103], v[128:131], v[152:155], a[100:103]\n" \
"ds_read_b64_tr_b16 v[110:111], v38 offset:1024\n" \
"v_mfma_f32_16x16x32_bf16 a[96:99], v[64:67], v[84:87], a[96:99]\n" \
"v_mfma_f32_16x16x32_bf16 a[96:99], v[128:131], v[148:151], a[96:99]\n" \
"v_mfma_f32_16x16x32_bf16 a[128:131], v[68:71], v[84:87], a[128:131]\n" \
"ds_read_b64_tr_b16 v[172:173], v38 offset:8192\n" \
"v_mfma_f32_16x16x32_bf16 a[128:131], v[132:135], v[148:151], a[128:131]\n" \
"v_mfma_f32_16x16x32_bf16 a[132:135], v[68:71], v[88:91], a[132:135]\n" \
"v_mfma_f32_16x16x32_bf16 a[132:135], v[132:135], v[152:155], a[132:135]\n" \
"ds_read_b64_tr_b16 v[174:175], v38 offset:9216\n" \
"v_mfma_f32_16x16x32_bf16 a[136:139], v[68:71], v[92:95], a[136:139]\n" \
"v_mfma_f32_16x16x32_bf16 a[136:139], v[132:135], v[156:159], a[136:139]\n" \
"v_mfma_f32_16x16x32_bf16 a[140:143], v[68:71], v[96:99], a[140:143]\n" \
"buffer_store_dwordx4 v[208:211], v20, s[80:83], s36 offen sc1\n" \
"v_mfma_f32_16x16x32_bf16 a[140:143], v[132:135], v[160:163], a[140:143]\n" \
"ds_read_b64_tr_b16 v[112:113], v39 offset:0\n" \
"v_mfma_f32_16x16x32_bf16 a[172:175], v[72:75], v[96:99], a[172:175]\n" \
"v_mfma_f32_16x16x32_bf16 a[172:175], v[136:139], v[160:163], a[172:175]\n" \
"v_mfma_f32_16x16x32_bf16 a[168:171], v[72:75], v[92:95], a[168:171]\n" \
"ds_read_b64_tr_b16 v[114:115], v39 offset:1024\n" \
"v_mfma_f32_16x16x32_bf16 a[168:171], v[136:139], v[156:159], a[168:171]\n" \
"v_mfma_f32_16x16x32_bf16 a[164:167], v[72:75], v[88:91], a[164:167]\n" \
"v_mfma_f32_16x16x32_bf16 a[164:167], v[136:139], v[152:155], a[164:167]\n" \
"ds_read_b64_tr_b16 v[176:177], v39 offset:8192\n" \
"v_mfma_f32_16x16x32_bf16 a[160:163], v[72:75], v[84:87], a[160:163]\n" \
"v_mfma_f32_16x16x32_bf16 a[160:163], v[136:139], v[148:151], a[160:163]\n" \
"v_mfma_f32_16x16x32_bf16 a[192:195], v[76:79], v[84:87], a[192:195]\n" \
"ds_read_b64_tr_b16 v[178:179], v39 offset:9216\n" \
"v_mfma_f32_16x16x32_bf16 a[192:195], v[140:143], v[148:151], a[192:195]\n" \
"v_mfma_f32_16x16x32_bf16 a[196:199], v[76:79], v[88:91], a[196:199]\n" \
"v_mfma_f32_16x16x32_bf16 a[196:199], v[140:143], v[152:155], a[196:199]\n" \
"v_mfma_f32_16x16x32_bf16 a[200:203], v[76:79], v[92:95], a[200:203]\n" \
"v_mfma_f32_16x16x32_bf16 a[200:203], v[140:143], v[156:159], a[200:203]\n" \
"v_mfma_f32_16x16x32_bf16 a[204:207], v[76:79], v[96:99], a[204:207]\n" \
"v_mfma_f32_16x16x32_bf16 a[204:207], v[140:143], v[160:163], a[204:207]\n" \
"s_waitcnt lgkmcnt(15)\n" \
"v_mfma_f32_16x16x32_bf16 a[236:239], v[80:83], v[96:99], a[236:239]\n" \
"v_mfma_f32_16x16x32_bf16 a[236:239], v[144:147], v[160:163], a[236:239]\n" \
"v_mfma_f32_16x16x32_bf16 a[232:235], v[80:83], v[92:95], a[232:235]\n" \
"v_mfma_f32_16x16x32_bf16 a[232:235], v[144:147], v[156:159], a[232:235]\n" \
"v_mfma_f32_16x16x32_bf16 a[228:231], v[80:83], v[88:91], a[228:231]\n" \
"v_mfma_f32_16x16x32_bf16 a[228:231], v[144:147], v[152:155], a[228:231]\n" \
"v_mfma_f32_16x16x32_bf16 a[224:227], v[80:83], v[84:87], a[224:227]\n" \
"v_mfma_f32_16x16x32_bf16 a[224:227], v[144:147], v[148:151], a[224:227]\n" \
"s_waitcnt vmcnt(4) lgkmcnt(0)\n" \
"s_barrier\n" \
"v_mfma_f32_16x16x32_bf16 a[16:19], v[52:55], v[100:103], a[16:19]\n" \
"s_add_u32 m0, s30, 0x0\n" \
"s_mov_b32 s85, 0x300\n" \
"v_mfma_f32_16x16x32_bf16 a[16:19], v[116:119], v[164:167], a[16:19]\n" \
"buffer_load_dwordx4 v22, s[64:67], s85 offen lds\n" \
"v_mfma_f32_16x16x32_bf16 a[20:23], v[52:55], v[104:107], a[20:23]\n" \
"ds_read_b64_tr_b16 v[84:85], v32 offset:32768\n" \
"v_mfma_f32_16x16x32_bf16 a[20:23], v[116:119], v[168:171], a[20:23]\n" \
"s_add_u32 m0, s30, 0x400\n" \
"s_mov_b32 s84, 0x2300\n" \
"v_mfma_f32_16x16x32_bf16 a[24:27], v[52:55], v[108:111], a[24:27]\n" \
"buffer_load_dwordx4 v23, s[64:67], s84 offen lds\n" \
"v_mfma_f32_16x16x32_bf16 a[24:27], v[116:119], v[172:175], a[24:27]\n" \
"ds_read_b64_tr_b16 v[86:87], v32 offset:33792\n" \
"v_mfma_f32_16x16x32_bf16 a[28:31], v[52:55], v[112:115], a[28:31]\n" \
"ds_read_b64_tr_b16 v[148:149], v32 offset:40960\n" \
"v_mfma_f32_16x16x32_bf16 a[28:31], v[116:119], v[176:179], a[28:31]\n" \
"ds_read_b128 v[52:55], v28 offset:49152\n" \
"v_mfma_f32_16x16x32_bf16 a[60:63], v[56:59], v[112:115], a[60:63]\n" \
"ds_read_b128 v[116:119], v29 offset:49152\n" \
"v_mfma_f32_16x16x32_bf16 a[60:63], v[120:123], v[176:179], a[60:63]\n" \
"s_add_u32 m0, s31, 0x0\n" \
"s_nop 0\n" \
"s_mov_b32 s85, 0x300000\n" \
"v_mfma_f32_16x16x32_bf16 a[56:59], v[56:59], v[108:111], a[56:59]\n" \
"buffer_load_dwordx4 v26, s[68:71], s85 offen lds\n" \
"v_mfma_f32_16x16x32_bf16 a[56:59], v[120:123], v[172:175], a[56:59]\n" \
"s_add_u32 m0, s31, 0x4000\n" \
"s_mov_b32 s84, 0x300000\n" \
"v_mfma_f32_16x16x32_bf16 a[52:55], v[56:59], v[104:107], a[52:55]\n" \
"buffer_load_dwordx4 v26, s[72:75], s84 offen lds\n" \
"v_mfma_f32_16x16x32_bf16 a[52:55], v[120:123], v[168:171], a[52:55]\n" \
"s_add_u32 m0, s30, 0x800\n" \
"s_mov_b32 s85, 0x4300\n" \
"v_mfma_f32_16x16x32_bf16 a[48:51], v[56:59], v[100:103], a[48:51]\n" \
"buffer_load_dwordx4 v22, s[64:67], s85 offen lds\n" \
"v_mfma_f32_16x16x32_bf16 a[48:51], v[120:123], v[164:167], a[48:51]\n" \
"ds_read_b128 v[56:59], v28 offset:51200\n" \
"v_mfma_f32_16x16x32_bf16 a[80:83], v[60:63], v[100:103], a[80:83]\n" \
"ds_read_b128 v[120:123], v29 offset:51200\n" \
"v_mfma_f32_16x16x32_bf16 a[80:83], v[124:127], v[164:167], a[80:83]\n" \
"s_add_u32 m0, s30, 0xc00\n" \
"s_mov_b32 s84, 0x6300\n" \
"v_mfma_f32_16x16x32_bf16 a[84:87], v[60:63], v[104:107], a[84:87]\n" \
"buffer_load_dwordx4 v23, s[64:67], s84 offen lds\n" \
"v_mfma_f32_16x16x32_bf16 a[84:87], v[124:127], v[168:171], a[84:87]\n" \
"ds_read_b64_tr_b16 v[150:151], v32 offset:41984\n" \
"v_mfma_f32_16x16x32_bf16 a[88:91], v[60:63], v[108:111], a[88:91]\n" \
"s_add_u32 m0, s31, 0x400\n" \
"s_mov_b32 s85, 0x308000\n" \
"v_mfma_f32_16x16x32_bf16 a[88:91], v[124:127], v[172:175], a[88:91]\n" \
"buffer_load_dwordx4 v26, s[68:71], s85 offen lds\n" \
"v_mfma_f32_16x16x32_bf16 a[92:95], v[60:63], v[112:115], a[92:95]\n" \
"ds_read_b64_tr_b16 v[88:89], v33 offset:32768\n" \
"v_mfma_f32_16x16x32_bf16 a[92:95], v[124:127], v[176:179], a[92:95]\n" \
"ds_read_b128 v[60:63], v28 offset:53248\n" \
"v_mfma_f32_16x16x32_bf16 a[124:127], v[64:67], v[112:115], a[124:127]\n" \
"ds_read_b128 v[124:127], v29 offset:53248\n" \
"v_mfma_f32_16x16x32_bf16 a[124:127], v[128:131], v[176:179], a[124:127]\n" \
"s_add_u32 m0, s31, 0x4400\n" \
"s_mov_b32 s84, 0x308000\n" \
"v_mfma_f32_16x16x32_bf16 a[120:123], v[64:67], v[108:111], a[120:123]\n" \
"buffer_load_dwordx4 v26, s[72:75], s84 offen lds\n" \
"v_mfma_f32_16x16x32_bf16 a[120:123], v[128:131], v[172:175], a[120:123]\n" \
"s_add_u32 m0, s30, 0x1000\n" \
"s_mov_b32 s85, 0x8300\n" \
"v_mfma_f32_16x16x32_bf16 a[116:119], v[64:67], v[104:107], a[116:119]\n" \
"buffer_load_dwordx4 v22, s[64:67], s85 offen lds\n" \
"v_mfma_f32_16x16x32_bf16 a[116:119], v[128:131], v[168:171], a[116:119]\n" \
"ds_read_b64_tr_b16 v[90:91], v33 offset:33792\n" \
"v_mfma_f32_16x16x32_bf16 a[112:115], v[64:67], v[100:103], a[112:115]\n" \
"ds_read_b64_tr_b16 v[152:153], v33 offset:40960\n" \
"v_mfma_f32_16x16x32_bf16 a[112:115], v[128:131], v[164:167], a[112:115]\n" \
"ds_read_b128 v[64:67], v28 offset:55296\n" \
"v_mfma_f32_16x16x32_bf16 a[144:147], v[68:71], v[100:103], a[144:147]\n" \
"ds_read_b128 v[128:131], v29 offset:55296\n" \
"v_mfma_f32_16x16x32_bf16 a[144:147], v[132:135], v[164:167], a[144:147]\n" \
"s_add_u32 m0, s30, 0x1400\n" \
"s_mov_b32 s84, 0xa300\n" \
"v_mfma_f32_16x16x32_bf16 a[148:151], v[68:71], v[104:107], a[148:151]\n" \
"buffer_load_dwordx4 v23, s[64:67], s84 offen lds\n" \
"v_mfma_f32_16x16x32_bf16 a[148:151], v[132:135], v[168:171], a[148:151]\n" \
"s_add_u32 m0, s31, 0x800\n" \
"s_mov_b32 s85, 0x310000\n" \
"v_mfma_f32_16x16x32_bf16 a[152:155], v[68:71], v[108:111], a[152:155]\n" \
"buffer_load_dwordx4 v27, s[68:71], s85 offen lds\n" \
"v_mfma_f32_16x16x32_bf16 a[152:155], v[132:135], v[172:175], a[152:155]\n" \
"s_add_u32 m0, s31, 0x4800\n" \
"s_mov_b32 s84, 0x310000\n" \
"v_mfma_f32_16x16x32_bf16 a[156:159], v[68:71], v[112:115], a[156:159]\n" \
"buffer_load_dwordx4 v27, s[72:75], s84 offen lds\n" \
"v_mfma_f32_16x16x32_bf16 a[156:159], v[132:135], v[176:179], a[156:159]\n" \
"ds_read_b128 v[68:71], v28 offset:32768\n" \
"v_mfma_f32_16x16x32_bf16 a[188:191], v[72:75], v[112:115], a[188:191]\n" \
"ds_read_b128 v[132:135], v29 offset:32768\n" \
"v_mfma_f32_16x16x32_bf16 a[188:191], v[136:139], v[176:179], a[188:191]\n" \
"s_add_u32 m0, s30, 0x1800\n" \
"s_mov_b32 s85, 0xc300\n" \
"v_mfma_f32_16x16x32_bf16 a[184:187], v[72:75], v[108:111], a[184:187]\n" \
"buffer_load_dwordx4 v22, s[64:67], s85 offen lds\n" \
"v_mfma_f32_16x16x32_bf16 a[184:187], v[136:139], v[172:175], a[184:187]\n" \
"ds_read_b64_tr_b16 v[154:155], v33 offset:41984\n" \
"v_mfma_f32_16x16x32_bf16 a[180:183], v[72:75], v[104:107], a[180:183]\n" \
"s_add_u32 m0, s30, 0x1c00\n" \
"s_mov_b32 s84, 0xe300\n" \
"v_mfma_f32_16x16x32_bf16 a[180:183], v[136:139], v[168:171], a[180:183]\n" \
"buffer_load_dwordx4 v23, s[64:67], s84 offen lds\n" \
"v_mfma_f32_16x16x32_bf16 a[176:179], v[72:75], v[100:103], a[176:179]\n" \
"ds_read_b64_tr_b16 v[92:93], v34 offset:32768\n" \
"v_mfma_f32_16x16x32_bf16 a[176:179], v[136:139], v[164:167], a[176:179]\n" \
"ds_read_b128 v[72:75], v28 offset:34816\n" \
"v_mfma_f32_16x16x32_bf16 a[208:211], v[76:79], v[100:103], a[208:211]\n" \
"ds_read_b128 v[136:139], v29 offset:34816\n" \
"v_mfma_f32_16x16x32_bf16 a[208:211], v[140:143], v[164:167], a[208:211]\n" \
"ds_read_b64_tr_b16 v[94:95], v34 offset:33792\n" \
"v_mfma_f32_16x16x32_bf16 a[212:215], v[76:79], v[104:107], a[212:215]\n" \
"ds_read_b64_tr_b16 v[156:157], v34 offset:40960\n" \
"v_mfma_f32_16x16x32_bf16 a[212:215], v[140:143], v[168:171], a[212:215]\n" \
"ds_read_b64_tr_b16 v[158:159], v34 offset:41984\n" \
"v_mfma_f32_16x16x32_bf16 a[216:219], v[76:79], v[108:111], a[216:219]\n" \
"ds_read_b64_tr_b16 v[96:97], v35 offset:32768\n" \
"v_mfma_f32_16x16x32_bf16 a[216:219], v[140:143], v[172:175], a[216:219]\n" \
"ds_read_b64_tr_b16 v[98:99], v35 offset:33792\n" \
"v_mfma_f32_16x16x32_bf16 a[220:223], v[76:79], v[112:115], a[220:223]\n" \
"ds_read_b64_tr_b16 v[160:161], v35 offset:40960\n" \
"v_mfma_f32_16x16x32_bf16 a[220:223], v[140:143], v[176:179], a[220:223]\n" \
"ds_read_b128 v[76:79], v28 offset:36864\n" \
"v_mfma_f32_16x16x32_bf16 a[252:255], v[80:83], v[112:115], a[252:255]\n" \
"ds_read_b128 v[140:143], v29 offset:36864\n" \
"v_mfma_f32_16x16x32_bf16 a[252:255], v[144:147], v[176:179], a[252:255]\n" \
"ds_read_b64_tr_b16 v[162:163], v35 offset:41984\n" \
"v_mfma_f32_16x16x32_bf16 a[248:251], v[80:83], v[108:111], a[248:251]\n" \
"v_mfma_f32_16x16x32_bf16 a[248:251], v[144:147], v[172:175], a[248:251]\n" \
"v_mfma_f32_16x16x32_bf16 a[244:247], v[80:83], v[104:107], a[244:247]\n" \
"v_mfma_f32_16x16x32_bf16 a[244:247], v[144:147], v[168:171], a[244:247]\n" \
"v_mfma_f32_16x16x32_bf16 a[240:243], v[80:83], v[100:103], a[240:243]\n" \
"v_mfma_f32_16x16x32_bf16 a[240:243], v[144:147], v[164:167], a[240:243]\n" \
"ds_read_b128 v[80:83], v28 offset:38912\n" \
"ds_read_b128 v[144:147], v29 offset:38912\n" \
"s_waitcnt lgkmcnt(15)\n" \
"v_mfma_f32_16x16x32_bf16 a[0:3], v[52:55], v[84:87], a[0:3]\n" \
"s_add_u32 m0, s31, 0xc00\n" \
"s_mov_b32 s85, 0x318000\n" \
"v_mfma_f32_16x16x32_bf16 a[0:3], v[116:119], v[148:151], a[0:3]\n" \
"buffer_load_dwordx4 v27, s[68:71], s85 offen lds\n" \
"v_mfma_f32_16x16x32_bf16 a[4:7], v[52:55], v[88:91], a[4:7]\n" \
"ds_read_b64_tr_b16 v[100:101], v36 offset:32768\n" \
"s_waitcnt lgkmcnt(15)\n" \
"v_mfma_f32_16x16x32_bf16 a[4:7], v[116:119], v[152:155], a[4:7]\n" \
"s_add_u32 m0, s31, 0x4c00\n" \
"s_mov_b32 s84, 0x318000\n" \
"s_waitcnt lgkmcnt(11)\n" \
"v_mfma_f32_16x16x32_bf16 a[8:11], v[52:55], v[92:95], a[8:11]\n" \
"buffer_load_dwordx4 v27, s[72:75], s84 offen lds\n" \
"s_waitcnt lgkmcnt(9)\n" \
"v_mfma_f32_16x16x32_bf16 a[8:11], v[116:119], v[156:159], a[8:11]\n" \
"ds_read_b64_tr_b16 v[102:103], v36 offset:33792\n" \
"s_waitcnt lgkmcnt(8)\n" \
"v_mfma_f32_16x16x32_bf16 a[12:15], v[52:55], v[96:99], a[12:15]\n" \
"ds_read_b64_tr_b16 v[164:165], v36 offset:40960\n" \
"s_waitcnt lgkmcnt(5)\n" \
"v_mfma_f32_16x16x32_bf16 a[12:15], v[116:119], v[160:163], a[12:15]\n" \
"v_mfma_f32_16x16x32_bf16 a[44:47], v[56:59], v[96:99], a[44:47]\n" \
"v_mfma_f32_16x16x32_bf16 a[44:47], v[120:123], v[160:163], a[44:47]\n" \
"ds_read_b64_tr_b16 v[166:167], v36 offset:41984\n" \
"v_mfma_f32_16x16x32_bf16 a[40:43], v[56:59], v[92:95], a[40:43]\n" \
"v_mfma_f32_16x16x32_bf16 a[40:43], v[120:123], v[156:159], a[40:43]\n" \
"v_mfma_f32_16x16x32_bf16 a[36:39], v[56:59], v[88:91], a[36:39]\n" \
"v_mfma_f32_16x16x32_bf16 a[36:39], v[120:123], v[152:155], a[36:39]\n" \
"ds_read_b64_tr_b16 v[104:105], v37 offset:32768\n" \
"v_mfma_f32_16x16x32_bf16 a[32:35], v[56:59], v[84:87], a[32:35]\n" \
"v_mfma_f32_16x16x32_bf16 a[32:35], v[120:123], v[148:151], a[32:35]\n" \
"v_mfma_f32_16x16x32_bf16 a[64:67], v[60:63], v[84:87], a[64:67]\n" \
"ds_read_b64_tr_b16 v[106:107], v37 offset:33792\n" \
"v_mfma_f32_16x16x32_bf16 a[64:67], v[124:127], v[148:151], a[64:67]\n" \
"v_mfma_f32_16x16x32_bf16 a[68:71], v[60:63], v[88:91], a[68:71]\n" \
"v_lshlrev_b32_e32 v45, 3, v20\n" \
"v_add_u32_e32 v45, s31, v45\n" \
"v_mfma_f32_16x16x32_bf16 a[68:71], v[124:127], v[152:155], a[68:71]\n" \
"s_getpc_b64 s[86:87]\n" \
"s_sub_u32 s86, s86, 0x10800\n" \
"s_subb_u32 s87, s87, 0\n" \
"s_mov_b32 m0, s32\n" \
"s_nop 0\n" \
"v_mfma_f32_16x16x32_bf16 a[72:75], v[60:63], v[92:95], a[72:75]\n" \
"global_load_lds_dword v45, s[86:87]\n" \
"v_mfma_f32_16x16x32_bf16 a[72:75], v[124:127], v[156:159], a[72:75]\n" \
"ds_read_b64_tr_b16 v[168:169], v37 offset:40960\n" \
"v_mfma_f32_16x16x32_bf16 a[76:79], v[60:63], v[96:99], a[76:79]\n" \
"ds_read_b64_tr_b16 v[170:171], v37 offset:41984\n" \
"v_mfma_f32_16x16x32_bf16 a[76:79], v[124:127], v[160:163], a[76:79]\n" \
"v_mfma_f32_16x16x32_bf16 a[108:111], v[64:67], v[96:99], a[108:111]\n" \
"v_mfma_f32_16x16x32_bf16 a[108:111], v[128:131], v[160:163], a[108:111]\n" \
"v_mfma_f32_16x16x32_bf16 a[104:107], v[64:67], v[92:95], a[104:107]\n" \
"ds_read_b64_tr_b16 v[108:109], v38 offset:32768\n" \
"v_mfma_f32_16x16x32_bf16 a[104:107], v[128:131], v[156:159], a[104:107]\n" \
"v_mfma_f32_16x16x32_bf16 a[100:103], v[64:67], v[88:91], a[100:103]\n" \
"v_mfma_f32_16x16x32_bf16 a[100:103], v[128:131], v[152:155], a[100:103]\n" \
"ds_read_b64_tr_b16 v[110:111], v38 offset:33792\n" \
"v_mfma_f32_16x16x32_bf16 a[96:99], v[64:67], v[84:87], a[96:99]\n" \
"v_mfma_f32_16x16x32_bf16 a[96:99], v[128:131], v[148:151], a[96:99]\n" \
"v_mfma_f32_16x16x32_bf16 a[128:131], v[68:71], v[84:87], a[128:131]\n" \
"ds_read_b64_tr_b16 v[172:173], v38 offset:40960\n" \
"v_mfma_f32_16x16x32_bf16 a[128:131], v[132:135], v[148:151], a[128:131]\n" \
"v_mfma_f32_16x16x32_bf16 a[132:135], v[68:71], v[88:91], a[132:135]\n" \
"v_mfma_f32_16x16x32_bf16 a[132:135], v[132:135], v[152:155], a[132:135]\n" \
"ds_read_b64_tr_b16 v[174:175], v38 offset:41984\n" \
"v_mfma_f32_16x16x32_bf16 a[136:139], v[68:71], v[92:95], a[136:139]\n" \
"v_mfma_f32_16x16x32_bf16 a[136:139], v[132:135], v[156:159], a[136:139]\n" \
"v_mfma_f32_16x16x32_bf16 a[140:143], v[68:71], v[96:99], a[140:143]\n" \
"v_mfma_f32_16x16x32_bf16 a[140:143], v[132:135], v[160:163], a[140:143]\n" \
"ds_read_b64_tr_b16 v[112:113], v39 offset:32768\n" \
"v_mfma_f32_16x16x32_bf16 a[172:175], v[72:75], v[96:99], a[172:175]\n" \
"v_mfma_f32_16x16x32_bf16 a[172:175], v[136:139], v[160:163], a[172:175]\n" \
"v_mfma_f32_16x16x32_bf16 a[168:171], v[72:75], v[92:95], a[168:171]\n" \
"ds_read_b64_tr_b16 v[114:115], v39 offset:33792\n" \
"v_mfma_f32_16x16x32_bf16 a[168:171], v[136:139], v[156:159], a[168:171]\n" \
"v_mfma_f32_16x16x32_bf16 a[164:167], v[72:75], v[88:91], a[164:167]\n" \
"v_mfma_f32_16x16x32_bf16 a[164:167], v[136:139], v[152:155], a[164:167]\n" \
"ds_read_b64_tr_b16 v[176:177], v39 offset:40960\n" \
"v_mfma_f32_16x16x32_bf16 a[160:163], v[72:75], v[84:87], a[160:163]\n" \
"v_mfma_f32_16x16x32_bf16 a[160:163], v[136:139], v[148:151], a[160:163]\n" \
"v_mfma_f32_16x16x32_bf16 a[192:195], v[76:79], v[84:87], a[192:195]\n" \
"ds_read_b64_tr_b16 v[178:179], v39 offset:41984\n" \
"v_mfma_f32_16x16x32_bf16 a[192:195], v[140:143], v[148:151], a[192:195]\n" \
"v_mfma_f32_16x16x32_bf16 a[196:199], v[76:79], v[88:91], a[196:199]\n" \
"v_mfma_f32_16x16x32_bf16 a[196:199], v[140:143], v[152:155], a[196:199]\n" \
"v_mfma_f32_16x16x32_bf16 a[200:203], v[76:79], v[92:95], a[200:203]\n" \
"v_mfma_f32_16x16x32_bf16 a[200:203], v[140:143], v[156:159], a[200:203]\n" \
"v_mfma_f32_16x16x32_bf16 a[204:207], v[76:79], v[96:99], a[204:207]\n" \
"v_mfma_f32_16x16x32_bf16 a[204:207], v[140:143], v[160:163], a[204:207]\n" \
"s_waitcnt lgkmcnt(15)\n" \
"v_mfma_f32_16x16x32_bf16 a[236:239], v[80:83], v[96:99], a[236:239]\n" \
"v_mfma_f32_16x16x32_bf16 a[236:239], v[144:147], v[160:163], a[236:239]\n" \
"v_mfma_f32_16x16x32_bf16 a[232:235], v[80:83], v[92:95], a[232:235]\n" \
"v_mfma_f32_16x16x32_bf16 a[232:235], v[144:147], v[156:159], a[232:235]\n" \
"v_mfma_f32_16x16x32_bf16 a[228:231], v[80:83], v[88:91], a[228:231]\n" \
"v_mfma_f32_16x16x32_bf16 a[228:231], v[144:147], v[152:155], a[228:231]\n" \
"v_mfma_f32_16x16x32_bf16 a[224:227], v[80:83], v[84:87], a[224:227]\n" \
"v_mfma_f32_16x16x32_bf16 a[224:227], v[144:147], v[148:151], a[224:227]\n" \
"s_waitcnt vmcnt(1) lgkmcnt(0)\n" \
"s_barrier\n" \
"v_mov_b32_e32 v45, s62\n" \
"s_mov_b64 exec, 1\n" \
"buffer_store_dword v45, off, s[80:83], s37 offset:128 sc1\n" \
"s_mov_b64 exec, -1\n" \
"v_mfma_f32_16x16x32_bf16 a[16:19], v[52:55], v[100:103], a[16:19]\n" \
"s_add_u32 m0, s30, 0x8000\n" \
"s_mov_b32 s85, 0x380\n" \
"v_mfma_f32_16x16x32_bf16 a[16:19], v[116:119], v[164:167], a[16:19]\n" \
"buffer_load_dwordx4 v22, s[64:67], s85 offen lds\n" \
"v_mfma_f32_16x16x32_bf16 a[20:23], v[52:55], v[104:107], a[20:23]\n" \
"ds_read_b64_tr_b16 v[84:85], v32 offset:0\n" \
"v_mfma_f32_16x16x32_bf16 a[20:23], v[116:119], v[168:171], a[20:23]\n" \
"s_add_u32 m0, s30, 0x8400\n" \
"s_mov_b32 s84, 0x2380\n" \
"v_mfma_f32_16x16x32_bf16 a[24:27], v[52:55], v[108:111], a[24:27]\n" \
"buffer_load_dwordx4 v23, s[64:67], s84 offen lds\n" \
"v_mfma_f32_16x16x32_bf16 a[24:27], v[116:119], v[172:175], a[24:27]\n" \
"ds_read_b64_tr_b16 v[86:87], v32 offset:1024\n" \
"v_mfma_f32_16x16x32_bf16 a[28:31], v[52:55], v[112:115], a[28:31]\n" \
"ds_read_b64_tr_b16 v[148:149], v32 offset:8192\n" \
"v_mfma_f32_16x16x32_bf16 a[28:31], v[116:119], v[176:179], a[28:31]\n" \
"ds_read_b128 v[52:55], v28 offset:16384\n" \
"v_mfma_f32_16x16x32_bf16 a[60:63], v[56:59], v[112:115], a[60:63]\n" \
"ds_read_b128 v[116:119], v29 offset:16384\n" \
"v_mfma_f32_16x16x32_bf16 a[60:63], v[120:123], v[176:179], a[60:63]\n" \
"s_add_u32 m0, s31, 0x8000\n" \
"s_mov_b32 s85, 0x380000\n" \
"v_mfma_f32_16x16x32_bf16 a[56:59], v[56:59], v[108:111], a[56:59]\n" \
"buffer_load_dwordx4 v26, s[68:71], s85 offen lds\n" \
"v_mfma_f32_16x16x32_bf16 a[56:59], v[120:123], v[172:175], a[56:59]\n" \
"s_add_u32 m0, s31, 0xc000\n" \
"s_mov_b32 s84, 0x380000\n" \
"v_mfma_f32_16x16x32_bf16 a[52:55], v[56:59], v[104:107], a[52:55]\n" \
"buffer_load_dwordx4 v26, s[72:75], s84 offen lds\n" \
"v_mfma_f32_16x16x32_bf16 a[52:55], v[120:123], v[168:171], a[52:55]\n" \
"s_add_u32 m0, s30, 0x8800\n" \
"s_mov_b32 s85, 0x4380\n" \
"v_mfma_f32_16x16x32_bf16 a[48:51], v[56:59], v[100:103], a[48:51]\n" \
"buffer_load_dwordx4 v22, s[64:67], s85 offen lds\n" \
"v_mfma_f32_16x16x32_bf16 a[48:51], v[120:123], v[164:167], a[48:51]\n" \
"ds_read_b128 v[56:59], v28 offset:18432\n" \
"v_mfma_f32_16x16x32_bf16 a[80:83], v[60:63], v[100:103], a[80:83]\n" \
"ds_read_b128 v[120:123], v29 offset:18432\n" \
"v_mfma_f32_16x16x32_bf16 a[80:83], v[124:127], v[164:167], a[80:83]\n" \
"s_add_u32 m0, s30, 0x8c00\n" \
"s_mov_b32 s84, 0x6380\n" \
"v_mfma_f32_16x16x32_bf16 a[84:87], v[60:63], v[104:107], a[84:87]\n" \
"buffer_load_dwordx4 v23, s[64:67], s84 offen lds\n" \
"v_mfma_f32_16x16x32_bf16 a[84:87], v[124:127], v[168:171], a[84:87]\n" \
"ds_read_b64_tr_b16 v[150:151], v32 offset:9216\n" \
"v_mfma_f32_16x16x32_bf16 a[88:91], v[60:63], v[108:111], a[88:91]\n" \
"s_add_u32 m0, s31, 0x8400\n" \
"s_mov_b32 s85, 0x388000\n" \
"v_mfma_f32_16x16x32_bf16 a[88:91], v[124:127], v[172:175], a[88:91]\n" \
"buffer_load_dwordx4 v26, s[68:71], s85 offen lds\n" \
"v_mfma_f32_16x16x32_bf16 a[92:95], v[60:63], v[112:115], a[92:95]\n" \
"ds_read_b64_tr_b16 v[88:89], v33 offset:0\n" \
"v_mfma_f32_16x16x32_bf16 a[92:95], v[124:127], v[176:179], a[92:95]\n" \
"ds_read_b128 v[60:63], v28 offset:20480\n" \
"v_mfma_f32_16x16x32_bf16 a[124:127], v[64:67], v[112:115], a[124:127]\n" \
"ds_read_b128 v[124:127], v29 offset:20480\n" \
"v_mfma_f32_16x16x32_bf16 a[124:127], v[128:131], v[176:179], a[124:127]\n" \
"s_add_u32 m0, s31, 0xc400\n" \
"s_mov_b32 s84, 0x388000\n" \
"v_mfma_f32_16x16x32_bf16 a[120:123], v[64:67], v[108:111], a[120:123]\n" \
"buffer_load_dwordx4 v26, s[72:75], s84 offen lds\n" \
"v_mfma_f32_16x16x32_bf16 a[120:123], v[128:131], v[172:175], a[120:123]\n" \
"s_add_u32 m0, s30, 0x9000\n" \
"s_mov_b32 s85, 0x8380\n" \
"v_mfma_f32_16x16x32_bf16 a[116:119], v[64:67], v[104:107], a[116:119]\n" \
"buffer_load_dwordx4 v22, s[64:67], s85 offen lds\n" \
"v_mfma_f32_16x16x32_bf16 a[116:119], v[128:131], v[168:171], a[116:119]\n" \
"ds_read_b64_tr_b16 v[90:91], v33 offset:1024\n" \
"v_mfma_f32_16x16x32_bf16 a[112:115], v[64:67], v[100:103], a[112:115]\n" \
"ds_read_b64_tr_b16 v[152:153], v33 offset:8192\n" \
"v_mfma_f32_16x16x32_bf16 a[112:115], v[128:131], v[164:167], a[112:115]\n" \
"ds_read_b128 v[64:67], v28 offset:22528\n" \
"v_mfma_f32_16x16x32_bf16 a[144:147], v[68:71], v[100:103], a[144:147]\n" \
"ds_read_b128 v[128:131], v29 offset:22528\n" \
"v_mfma_f32_16x16x32_bf16 a[144:147], v[132:135], v[164:167], a[144:147]\n" \
"s_add_u32 m0, s30, 0x9400\n" \
"s_mov_b32 s84, 0xa380\n" \
"v_mfma_f32_16x16x32_bf16 a[148:151], v[68:71], v[104:107], a[148:151]\n" \
"buffer_load_dwordx4 v23, s[64:67], s84 offen lds\n" \
"v_mfma_f32_16x16x32_bf16 a[148:151], v[132:135], v[168:171], a[148:151]\n" \
"s_add_u32 m0, s31, 0x8800\n" \
"s_mov_b32 s85, 0x390000\n" \
"v_mfma_f32_16x16x32_bf16 a[152:155], v[68:71], v[108:111], a[152:155]\n" \
"buffer_load_dwordx4 v27, s[68:71], s85 offen lds\n" \
"v_mfma_f32_16x16x32_bf16 a[152:155], v[132:135], v[172:175], a[152:155]\n" \
"s_add_u32 m0, s31, 0xc800\n" \
"s_mov_b32 s84, 0x390000\n" \
"v_mfma_f32_16x16x32_bf16 a[156:159], v[68:71], v[112:115], a[156:159]\n" \
"buffer_load_dwordx4 v27, s[72:75], s84 offen lds\n" \
"v_mfma_f32_16x16x32_bf16 a[156:159], v[132:135], v[176:179], a[156:159]\n" \
"ds_read_b128 v[68:71], v28 offset:0\n" \
"v_mfma_f32_16x16x32_bf16 a[188:191], v[72:75], v[112:115], a[188:191]\n" \
"ds_read_b128 v[132:135], v29 offset:0\n" \
"v_mfma_f32_16x16x32_bf16 a[188:191], v[136:139], v[176:179], a[188:191]\n" \
"s_add_u32 m0, s30, 0x9800\n" \
"s_mov_b32 s85, 0xc380\n" \
"v_mfma_f32_16x16x32_bf16 a[184:187], v[72:75], v[108:111], a[184:187]\n" \
"buffer_load_dwordx4 v22, s[64:67], s85 offen lds\n" \
"v_mfma_f32_16x16x32_bf16 a[184:187], v[136:139], v[172:175], a[184:187]\n" \
"ds_read_b64_tr_b16 v[154:155], v33 offset:9216\n" \
"v_mfma_f32_16x16x32_bf16 a[180:183], v[72:75], v[104:107], a[180:183]\n" \
"s_add_u32 m0, s30, 0x9c00\n" \
"s_mov_b32 s84, 0xe380\n" \
"v_mfma_f32_16x16x32_bf16 a[180:183], v[136:139], v[168:171], a[180:183]\n" \
"buffer_load_dwordx4 v23, s[64:67], s84 offen lds\n" \
"v_mfma_f32_16x16x32_bf16 a[176:179], v[72:75], v[100:103], a[176:179]\n" \
"ds_read_b64_tr_b16 v[92:93], v34 offset:0\n" \
"v_mfma_f32_16x16x32_bf16 a[176:179], v[136:139], v[164:167], a[176:179]\n" \
"ds_read_b128 v[72:75], v28 offset:2048\n" \
"v_mfma_f32_16x16x32_bf16 a[208:211], v[76:79], v[100:103], a[208:211]\n" \
"ds_read_b128 v[136:139], v29 offset:2048\n" \
"v_mfma_f32_16x16x32_bf16 a[208:211], v[140:143], v[164:167], a[208:211]\n" \
"ds_read_b64_tr_b16 v[94:95], v34 offset:1024\n" \
"v_mfma_f32_16x16x32_bf16 a[212:215], v[76:79], v[104:107], a[212:215]\n" \
"ds_read_b64_tr_b16 v[156:157], v34 offset:8192\n" \
"v_mfma_f32_16x16x32_bf16 a[212:215], v[140:143], v[168:171], a[212:215]\n" \
"ds_read_b64_tr_b16 v[158:159], v34 offset:9216\n" \
"v_mfma_f32_16x16x32_bf16 a[216:219], v[76:79], v[108:111], a[216:219]\n" \
"ds_read_b64_tr_b16 v[96:97], v35 offset:0\n" \
"v_mfma_f32_16x16x32_bf16 a[216:219], v[140:143], v[172:175], a[216:219]\n" \
"ds_read_b64_tr_b16 v[98:99], v35 offset:1024\n" \
"v_mfma_f32_16x16x32_bf16 a[220:223], v[76:79], v[112:115], a[220:223]\n" \
"ds_read_b64_tr_b16 v[160:161], v35 offset:8192\n" \
"v_mfma_f32_16x16x32_bf16 a[220:223], v[140:143], v[176:179], a[220:223]\n" \
"ds_read_b128 v[76:79], v28 offset:4096\n" \
"v_mfma_f32_16x16x32_bf16 a[252:255], v[80:83], v[112:115], a[252:255]\n" \
"ds_read_b128 v[140:143], v29 offset:4096\n" \
"v_mfma_f32_16x16x32_bf16 a[252:255], v[144:147], v[176:179], a[252:255]\n" \
"ds_read_b64_tr_b16 v[162:163], v35 offset:9216\n" \
"v_mfma_f32_16x16x32_bf16 a[248:251], v[80:83], v[108:111], a[248:251]\n" \
"v_mfma_f32_16x16x32_bf16 a[248:251], v[144:147], v[172:175], a[248:251]\n" \
"v_mfma_f32_16x16x32_bf16 a[244:247], v[80:83], v[104:107], a[244:247]\n" \
"v_mfma_f32_16x16x32_bf16 a[244:247], v[144:147], v[168:171], a[244:247]\n" \
"v_mfma_f32_16x16x32_bf16 a[240:243], v[80:83], v[100:103], a[240:243]\n" \
"v_mfma_f32_16x16x32_bf16 a[240:243], v[144:147], v[164:167], a[240:243]\n" \
"ds_read_b128 v[80:83], v28 offset:6144\n" \
"ds_read_b128 v[144:147], v29 offset:6144\n" \
"s_waitcnt lgkmcnt(15)\n" \
"v_mfma_f32_16x16x32_bf16 a[0:3], v[52:55], v[84:87], a[0:3]\n" \
"s_add_u32 m0, s31, 0x8c00\n" \
"s_mov_b32 s85, 0x398000\n" \
"v_mfma_f32_16x16x32_bf16 a[0:3], v[116:119], v[148:151], a[0:3]\n" \
"buffer_load_dwordx4 v27, s[68:71], s85 offen lds\n" \
"v_mfma_f32_16x16x32_bf16 a[4:7], v[52:55], v[88:91], a[4:7]\n" \
"ds_read_b64_tr_b16 v[100:101], v36 offset:0\n" \
"s_waitcnt lgkmcnt(15)\n" \
"v_mfma_f32_16x16x32_bf16 a[4:7], v[116:119], v[152:155], a[4:7]\n" \
"s_add_u32 m0, s31, 0xcc00\n" \
"s_mov_b32 s84, 0x398000\n" \
"s_waitcnt lgkmcnt(11)\n" \
"v_mfma_f32_16x16x32_bf16 a[8:11], v[52:55], v[92:95], a[8:11]\n" \
".fill (L_pb + 61152 - .) / 4, 4, 0xBF800000\n" \
"L_pp15:\n" \
"s_cbranch_execz L_pret\n" \
"buffer_load_dwordx4 v27, s[72:75], s84 offen lds\n" \
"s_waitcnt lgkmcnt(9)\n" \
"v_mfma_f32_16x16x32_bf16 a[8:11], v[116:119], v[156:159], a[8:11]\n" \
"ds_read_b64_tr_b16 v[102:103], v36 offset:1024\n" \
"s_waitcnt lgkmcnt(8)\n" \
"v_mfma_f32_16x16x32_bf16 a[12:15], v[52:55], v[96:99], a[12:15]\n" \
"ds_read_b64_tr_b16 v[164:165], v36 offset:8192\n" \
"s_waitcnt lgkmcnt(5)\n" \
"v_mfma_f32_16x16x32_bf16 a[12:15], v[116:119], v[160:163], a[12:15]\n" \
"v_mfma_f32_16x16x32_bf16 a[44:47], v[56:59], v[96:99], a[44:47]\n" \
"v_mfma_f32_16x16x32_bf16 a[44:47], v[120:123], v[160:163], a[44:47]\n" \
"ds_read_b64_tr_b16 v[166:167], v36 offset:9216\n" \
"v_mfma_f32_16x16x32_bf16 a[40:43], v[56:59], v[92:95], a[40:43]\n" \
"v_mfma_f32_16x16x32_bf16 a[40:43], v[120:123], v[156:159], a[40:43]\n" \
"v_mfma_f32_16x16x32_bf16 a[36:39], v[56:59], v[88:91], a[36:39]\n" \
"v_mfma_f32_16x16x32_bf16 a[36:39], v[120:123], v[152:155], a[36:39]\n" \
"ds_read_b64_tr_b16 v[104:105], v37 offset:0\n" \
"v_mfma_f32_16x16x32_bf16 a[32:35], v[56:59], v[84:87], a[32:35]\n" \
"v_mfma_f32_16x16x32_bf16 a[32:35], v[120:123], v[148:151], a[32:35]\n" \
"v_mfma_f32_16x16x32_bf16 a[64:67], v[60:63], v[84:87], a[64:67]\n" \
"ds_read_b64_tr_b16 v[106:107], v37 offset:1024\n" \
"v_mfma_f32_16x16x32_bf16 a[64:67], v[124:127], v[148:151], a[64:67]\n" \
"v_mfma_f32_16x16x32_bf16 a[68:71], v[60:63], v[88:91], a[68:71]\n" \
"v_mfma_f32_16x16x32_bf16 a[68:71], v[124:127], v[152:155], a[68:71]\n" \
"ds_read_b64_tr_b16 v[168:169], v37 offset:8192\n" \
"v_mfma_f32_16x16x32_bf16 a[72:75], v[60:63], v[92:95], a[72:75]\n" \
"v_mfma_f32_16x16x32_bf16 a[72:75], v[124:127], v[156:159], a[72:75]\n" \
"v_mfma_f32_16x16x32_bf16 a[76:79], v[60:63], v[96:99], a[76:79]\n" \
"ds_read_b64_tr_b16 v[170:171], v37 offset:9216\n" \
"v_mfma_f32_16x16x32_bf16 a[76:79], v[124:127], v[160:163], a[76:79]\n" \
"v_mfma_f32_16x16x32_bf16 a[108:111], v[64:67], v[96:99], a[108:111]\n" \
"v_mfma_f32_16x16x32_bf16 a[108:111], v[128:131], v[160:163], a[108:111]\n" \
"v_mfma_f32_16x16x32_bf16 a[104:107], v[64:67], v[92:95], a[104:107]\n" \
"ds_read_b64_tr_b16 v[108:109], v38 offset:0\n" \
"v_mfma_f32_16x16x32_bf16 a[104:107], v[128:131], v[156:159], a[104:107]\n" \
"v_mfma_f32_16x16x32_bf16 a[100:103], v[64:67], v[88:91], a[100:103]\n" \
"v_mfma_f32_16x16x32_bf16 a[100:103], v[128:131], v[152:155], a[100:103]\n" \
"ds_read_b64_tr_b16 v[110:111], v38 offset:1024\n" \
"v_mfma_f32_16x16x32_bf16 a[96:99], v[64:67], v[84:87], a[96:99]\n" \
"v_mfma_f32_16x16x32_bf16 a[96:99], v[128:131], v[148:151], a[96:99]\n" \
"v_mfma_f32_16x16x32_bf16 a[128:131], v[68:71], v[84:87], a[128:131]\n" \
"v_bfe_u32 v44, v20, 2, 5\n" \
"ds_read_b64_tr_b16 v[172:173], v38 offset:8192\n" \
"v_mfma_f32_16x16x32_bf16 a[128:131], v[132:135], v[148:151], a[128:131]\n" \
"buffer_load_dword v44, v44, s[80:83], s38 offen offset:128 sc1\n" \
"v_mfma_f32_16x16x32_bf16 a[132:135], v[68:71], v[88:91], a[132:135]\n" \
"v_mfma_f32_16x16x32_bf16 a[132:135], v[132:135], v[152:155], a[132:135]\n" \
"ds_read_b64_tr_b16 v[174:175], v38 offset:9216\n" \
"v_mfma_f32_16x16x32_bf16 a[136:139], v[68:71], v[92:95], a[136:139]\n" \
"v_mfma_f32_16x16x32_bf16 a[136:139], v[132:135], v[156:159], a[136:139]\n" \
"v_mfma_f32_16x16x32_bf16 a[140:143], v[68:71], v[96:99], a[140:143]\n" \
"v_mfma_f32_16x16x32_bf16 a[140:143], v[132:135], v[160:163], a[140:143]\n" \
"ds_read_b64_tr_b16 v[112:113], v39 offset:0\n" \
"v_mfma_f32_16x16x32_bf16 a[172:175], v[72:75], v[96:99], a[172:175]\n" \
"v_mfma_f32_16x16x32_bf16 a[172:175], v[136:139], v[160:163], a[172:175]\n" \
"v_mfma_f32_16x16x32_bf16 a[168:171], v[72:75], v[92:95], a[168:171]\n" \
"ds_read_b64_tr_b16 v[114:115], v39 offset:1024\n" \
"v_mfma_f32_16x16x32_bf16 a[168:171], v[136:139], v[156:159], a[168:171]\n" \
"v_mfma_f32_16x16x32_bf16 a[164:167], v[72:75], v[88:91], a[164:167]\n" \
"v_mfma_f32_16x16x32_bf16 a[164:167], v[136:139], v[152:155], a[164:167]\n" \
"ds_read_b64_tr_b16 v[176:177], v39 offset:8192\n" \
"v_mfma_f32_16x16x32_bf16 a[160:163], v[72:75], v[84:87], a[160:163]\n" \
"v_mfma_f32_16x16x32_bf16 a[160:163], v[136:139], v[148:151], a[160:163]\n" \
"v_mfma_f32_16x16x32_bf16 a[192:195], v[76:79], v[84:87], a[192:195]\n" \
"ds_read_b64_tr_b16 v[178:179], v39 offset:9216\n" \
"v_mfma_f32_16x16x32_bf16 a[192:195], v[140:143], v[148:151], a[192:195]\n" \
"v_mfma_f32_16x16x32_bf16 a[196:199], v[76:79], v[88:91], a[196:199]\n" \
"v_mfma_f32_16x16x32_bf16 a[196:199], v[140:143], v[152:155], a[196:199]\n" \
"v_mfma_f32_16x16x32_bf16 a[200:203], v[76:79], v[92:95], a[200:203]\n" \
"v_mfma_f32_16x16x32_bf16 a[200:203], v[140:143], v[156:159], a[200:203]\n" \
"v_mfma_f32_16x16x32_bf16 a[204:207], v[76:79], v[96:99], a[204:207]\n" \
"v_mfma_f32_16x16x32_bf16 a[204:207], v[140:143], v[160:163], a[204:207]\n" \
"s_waitcnt lgkmcnt(15)\n" \
"v_mfma_f32_16x16x32_bf16 a[236:239], v[80:83], v[96:99], a[236:239]\n" \
"v_mfma_f32_16x16x32_bf16 a[236:239], v[144:147], v[160:163], a[236:239]\n" \
"v_mfma_f32_16x16x32_bf16 a[232:235], v[80:83], v[92:95], a[232:235]\n" \
"v_mfma_f32_16x16x32_bf16 a[232:235], v[144:147], v[156:159], a[232:235]\n" \
"v_mfma_f32_16x16x32_bf16 a[228:231], v[80:83], v[88:91], a[228:231]\n" \
"v_mfma_f32_16x16x32_bf16 a[228:231], v[144:147], v[152:155], a[228:231]\n" \
"v_mfma_f32_16x16x32_bf16 a[224:227], v[80:83], v[84:87], a[224:227]\n" \
"v_mfma_f32_16x16x32_bf16 a[224:227], v[144:147], v[148:151], a[224:227]\n" \
"s_waitcnt vmcnt(0) lgkmcnt(0)\n" \
"s_barrier\n" \
"v_cmp_ne_u32_e32 vcc, s62, v44\n" \
"s_nop 4\n" \
"s_cbranch_vccnz L_poll61_%=\n" \
"L_pollr61_%=:\n" \
"s_mov_b32 s68, s12\n" \
"s_and_b32 s69, s13, 0xffff\n" \
"s_mov_b32 s70, 0x480000\n" \
"s_mov_b32 s71, 0x20000\n" \
"v_mfma_f32_16x16x32_bf16 a[16:19], v[52:55], v[100:103], a[16:19]\n" \
"s_add_u32 m0, s40, 0x0\n" \
"s_add_u32 s85, s39, 0x0\n" \
"s_nop 0\n" \
"v_mfma_f32_16x16x32_bf16 a[16:19], v[116:119], v[164:167], a[16:19]\n" \
"buffer_load_dwordx4 v20, s[80:83], s85 offen sc1 lds\n" \
"v_mfma_f32_16x16x32_bf16 a[20:23], v[52:55], v[104:107], a[20:23]\n" \
"ds_read_b64_tr_b16 v[84:85], v32 offset:32768\n" \
"v_mfma_f32_16x16x32_bf16 a[20:23], v[116:119], v[168:171], a[20:23]\n" \
"s_add_u32 m0, s40, 0x1000\n" \
"s_add_u32 s84, s39, 0x1000\n" \
"v_mfma_f32_16x16x32_bf16 a[24:27], v[52:55], v[108:111], a[24:27]\n" \
"buffer_load_dwordx4 v20, s[80:83], s84 offen sc1 lds\n" \
"v_mfma_f32_16x16x32_bf16 a[24:27], v[116:119], v[172:175], a[24:27]\n" \
"ds_read_b64_tr_b16 v[86:87], v32 offset:33792\n" \
"v_mfma_f32_16x16x32_bf16 a[28:31], v[52:55], v[112:115], a[28:31]\n" \
"ds_read_b64_tr_b16 v[148:149], v32 offset:40960\n" \
"v_mfma_f32_16x16x32_bf16 a[28:31], v[116:119], v[176:179], a[28:31]\n" \
"ds_read_b128 v[52:55], v28 offset:49152\n" \
"v_mfma_f32_16x16x32_bf16 a[60:63], v[56:59], v[112:115], a[60:63]\n" \
"ds_read_b128 v[116:119], v29 offset:49152\n" \
"v_mfma_f32_16x16x32_bf16 a[60:63], v[120:123], v[176:179], a[60:63]\n" \
"s_add_u32 m0, s40, 0x3000\n" \
"s_mov_b32 s85, 0x0\n" \
"v_mfma_f32_16x16x32_bf16 a[56:59], v[56:59], v[108:111], a[56:59]\n" \
"buffer_load_dwordx4 v21, s[76:79], s85 offen lds\n" \
"v_mfma_f32_16x16x32_bf16 a[56:59], v[120:123], v[172:175], a[56:59]\n" \
"s_add_u32 m0, s30, 0x0\n" \
"s_mov_b32 s84, 0x0\n" \
"s_nop 0\n" \
"v_mfma_f32_16x16x32_bf16 a[52:55], v[56:59], v[104:107], a[52:55]\n" \
"buffer_load_dwordx4 v24, s[76:79], s84 offen lds\n" \
"v_mfma_f32_16x16x32_bf16 a[52:55], v[120:123], v[168:171], a[52:55]\n" \
"ds_read_b64_tr_b16 v[150:151], v32 offset:41984\n" \
"v_mfma_f32_16x16x32_bf16 a[48:51], v[56:59], v[100:103], a[48:51]\n" \
"ds_read_b64_tr_b16 v[88:89], v33 offset:32768\n" \
"v_mfma_f32_16x16x32_bf16 a[48:51], v[120:123], v[164:167], a[48:51]\n" \
"ds_read_b128 v[56:59], v28 offset:51200\n" \
"v_mfma_f32_16x16x32_bf16 a[80:83], v[60:63], v[100:103], a[80:83]\n" \
"ds_read_b128 v[120:123], v29 offset:51200\n" \
"v_mfma_f32_16x16x32_bf16 a[80:83], v[124:127], v[164:167], a[80:83]\n" \
"s_add_u32 m0, s30, 0x400\n" \
"s_mov_b32 s85, 0x2400\n" \
"v_mfma_f32_16x16x32_bf16 a[84:87], v[60:63], v[104:107], a[84:87]\n" \
"buffer_load_dwordx4 v25, s[76:79], s85 offen lds\n" \
"v_mfma_f32_16x16x32_bf16 a[84:87], v[124:127], v[168:171], a[84:87]\n" \
"s_add_u32 m0, s31, 0x0\n" \
"s_mov_b32 s84, 0x0\n" \
"v_mfma_f32_16x16x32_bf16 a[88:91], v[60:63], v[108:111], a[88:91]\n" \
"buffer_load_dwordx4 v26, s[68:71], s84 offen lds\n" \
"v_mfma_f32_16x16x32_bf16 a[88:91], v[124:127], v[172:175], a[88:91]\n" \
"ds_read_b64_tr_b16 v[90:91], v33 offset:33792\n" \
"v_mfma_f32_16x16x32_bf16 a[92:95], v[60:63], v[112:115], a[92:95]\n" \
"ds_read_b64_tr_b16 v[152:153], v33 offset:40960\n" \
"v_mfma_f32_16x16x32_bf16 a[92:95], v[124:127], v[176:179], a[92:95]\n" \
"ds_read_b128 v[60:63], v28 offset:53248\n" \
"v_mfma_f32_16x16x32_bf16 a[124:127], v[64:67], v[112:115], a[124:127]\n" \
"ds_read_b128 v[124:127], v29 offset:53248\n" \
"v_mfma_f32_16x16x32_bf16 a[124:127], v[128:131], v[176:179], a[124:127]\n" \
"s_add_u32 m0, s30, 0x800\n" \
"s_mov_b32 s85, 0x4800\n" \
"v_mfma_f32_16x16x32_bf16 a[120:123], v[64:67], v[108:111], a[120:123]\n" \
"buffer_load_dwordx4 v24, s[76:79], s85 offen lds\n" \
"v_mfma_f32_16x16x32_bf16 a[120:123], v[128:131], v[172:175], a[120:123]\n" \
"s_add_u32 m0, s30, 0xc00\n" \
"s_mov_b32 s84, 0x6c00\n" \
"v_mfma_f32_16x16x32_bf16 a[116:119], v[64:67], v[104:107], a[116:119]\n" \
"buffer_load_dwordx4 v25, s[76:79], s84 offen lds\n" \
"v_mfma_f32_16x16x32_bf16 a[116:119], v[128:131], v[168:171], a[116:119]\n" \
"s_add_u32 m0, s31, 0x400\n" \
"s_mov_b32 s85, 0x8000\n" \
"v_mfma_f32_16x16x32_bf16 a[112:115], v[64:67], v[100:103], a[112:115]\n" \
"buffer_load_dwordx4 v26, s[68:71], s85 offen lds\n" \
"v_mfma_f32_16x16x32_bf16 a[112:115], v[128:131], v[164:167], a[112:115]\n" \
"ds_read_b128 v[64:67], v28 offset:55296\n" \
"v_mfma_f32_16x16x32_bf16 a[144:147], v[68:71], v[100:103], a[144:147]\n" \
"ds_read_b128 v[128:131], v29 offset:55296\n" \
"v_mfma_f32_16x16x32_bf16 a[144:147], v[132:135], v[164:167], a[144:147]\n" \
"s_add_u32 m0, s30, 0x1000\n" \
"s_mov_b32 s84, 0x9000\n" \
"v_mfma_f32_16x16x32_bf16 a[148:151], v[68:71], v[104:107], a[148:151]\n" \
"buffer_load_dwordx4 v24, s[76:79], s84 offen lds\n" \
"v_mfma_f32_16x16x32_bf16 a[148:151], v[132:135], v[168:171], a[148:151]\n" \
"ds_read_b64_tr_b16 v[154:155], v33 offset:41984\n" \
"v_mfma_f32_16x16x32_bf16 a[152:155], v[68:71], v[108:111], a[152:155]\n" \
"s_add_u32 m0, s30, 0x1400\n" \
"s_mov_b32 s85, 0xb400\n" \
"v_mfma_f32_16x16x32_bf16 a[152:155], v[132:135], v[172:175], a[152:155]\n" \
"buffer_load_dwordx4 v25, s[76:79], s85 offen lds\n" \
"v_mfma_f32_16x16x32_bf16 a[156:159], v[68:71], v[112:115], a[156:159]\n" \
"ds_read_b64_tr_b16 v[92:93], v34 offset:32768\n" \
"v_mfma_f32_16x16x32_bf16 a[156:159], v[132:135], v[176:179], a[156:159]\n" \
"ds_read_b128 v[68:71], v28 offset:32768\n" \
"v_mfma_f32_16x16x32_bf16 a[188:191], v[72:75], v[112:115], a[188:191]\n" \
"ds_read_b128 v[132:135], v29 offset:32768\n" \
"v_mfma_f32_16x16x32_bf16 a[188:191], v[136:139], v[176:179], a[188:191]\n" \
"s_add_u32 m0, s31, 0x800\n" \
"s_mov_b32 s84, 0x10000\n" \
"v_mfma_f32_16x16x32_bf16 a[184:187], v[72:75], v[108:111], a[184:187]\n" \
"buffer_load_dwordx4 v27, s[68:71], s84 offen lds\n" \
"v_mfma_f32_16x16x32_bf16 a[184:187], v[136:139], v[172:175], a[184:187]\n" \
"ds_read_b64_tr_b16 v[94:95], v34 offset:33792\n" \
"v_mfma_f32_16x16x32_bf16 a[180:183], v[72:75], v[104:107], a[180:183]\n" \
"s_add_u32 m0, s30, 0x1800\n" \
"s_mov_b32 s85, 0xd800\n" \
"v_mfma_f32_16x16x32_bf16 a[180:183], v[136:139], v[168:171], a[180:183]\n" \
"buffer_load_dwordx4 v24, s[76:79], s85 offen lds\n" \
"v_mfma_f32_16x16x32_bf16 a[176:179], v[72:75], v[100:103], a[176:179]\n" \
"ds_read_b64_tr_b16 v[156:157], v34 offset:40960\n" \
"v_mfma_f32_16x16x32_bf16 a[176:179], v[136:139], v[164:167], a[176:179]\n" \
"ds_read_b128 v[72:75], v28 offset:34816\n" \
"v_mfma_f32_16x16x32_bf16 a[208:211], v[76:79], v[100:103], a[208:211]\n" \
"ds_read_b128 v[136:139], v29 offset:34816\n" \
"v_mfma_f32_16x16x32_bf16 a[208:211], v[140:143], v[164:167], a[208:211]\n" \
"ds_read_b64_tr_b16 v[158:159], v34 offset:41984\n" \
"v_mfma_f32_16x16x32_bf16 a[212:215], v[76:79], v[104:107], a[212:215]\n" \
"ds_read_b64_tr_b16 v[96:97], v35 offset:32768\n" \
"v_mfma_f32_16x16x32_bf16 a[212:215], v[140:143], v[168:171], a[212:215]\n" \
"ds_read_b64_tr_b16 v[98:99], v35 offset:33792\n" \
"v_mfma_f32_16x16x32_bf16 a[216:219], v[76:79], v[108:111], a[216:219]\n" \
"ds_read_b64_tr_b16 v[160:161], v35 offset:40960\n" \
"v_mfma_f32_16x16x32_bf16 a[216:219], v[140:143], v[172:175], a[216:219]\n" \
"ds_read_b64_tr_b16 v[162:163], v35 offset:41984\n" \
"v_mfma_f32_16x16x32_bf16 a[220:223], v[76:79], v[112:115], a[220:223]\n" \
"v_mfma_f32_16x16x32_bf16 a[220:223], v[140:143], v[176:179], a[220:223]\n" \
"ds_read_b128 v[76:79], v28 offset:36864\n" \
"v_mfma_f32_16x16x32_bf16 a[252:255], v[80:83], v[112:115], a[252:255]\n" \
"ds_read_b128 v[140:143], v29 offset:36864\n" \
"v_mfma_f32_16x16x32_bf16 a[252:255], v[144:147], v[176:179], a[252:255]\n" \
"v_mfma_f32_16x16x32_bf16 a[248:251], v[80:83], v[108:111], a[248:251]\n" \
"v_mfma_f32_16x16x32_bf16 a[248:251], v[144:147], v[172:175], a[248:251]\n" \
"v_mfma_f32_16x16x32_bf16 a[244:247], v[80:83], v[104:107], a[244:247]\n" \
"v_mfma_f32_16x16x32_bf16 a[244:247], v[144:147], v[168:171], a[244:247]\n" \
"v_mfma_f32_16x16x32_bf16 a[240:243], v[80:83], v[100:103], a[240:243]\n" \
"v_mfma_f32_16x16x32_bf16 a[240:243], v[144:147], v[164:167], a[240:243]\n" \
"ds_read_b128 v[80:83], v28 offset:38912\n" \
"ds_read_b128 v[144:147], v29 offset:38912\n" \
"s_waitcnt lgkmcnt(15)\n" \
"v_mfma_f32_16x16x32_bf16 a[0:3], v[52:55], v[84:87], a[0:3]\n" \
"s_add_u32 m0, s30, 0x1c00\n" \
"s_mov_b32 s85, 0xfc00\n" \
"v_mfma_f32_16x16x32_bf16 a[0:3], v[116:119], v[148:151], a[0:3]\n" \
"buffer_load_dwordx4 v25, s[76:79], s85 offen lds\n" \
"v_mfma_f32_16x16x32_bf16 a[4:7], v[52:55], v[88:91], a[4:7]\n" \
"ds_read_b64_tr_b16 v[100:101], v36 offset:32768\n" \
"v_mfma_f32_16x16x32_bf16 a[4:7], v[116:119], v[152:155], a[4:7]\n" \
"s_add_u32 m0, s31, 0xc00\n" \
"s_mov_b32 s84, 0x18000\n" \
"s_waitcnt lgkmcnt(13)\n" \
"v_mfma_f32_16x16x32_bf16 a[8:11], v[52:55], v[92:95], a[8:11]\n" \
"buffer_load_dwordx4 v27, s[68:71], s84 offen lds\n" \
"s_waitcnt lgkmcnt(9)\n" \
"v_mfma_f32_16x16x32_bf16 a[8:11], v[116:119], v[156:159], a[8:11]\n" \
"ds_read_b64_tr_b16 v[102:103], v36 offset:33792\n" \
"s_waitcnt lgkmcnt(8)\n" \
"v_mfma_f32_16x16x32_bf16 a[12:15], v[52:55], v[96:99], a[12:15]\n" \
"ds_read_b64_tr_b16 v[164:165], v36 offset:40960\n" \
"s_waitcnt lgkmcnt(7)\n" \
"v_mfma_f32_16x16x32_bf16 a[12:15], v[116:119], v[160:163], a[12:15]\n" \
"v_mfma_f32_16x16x32_bf16 a[44:47], v[56:59], v[96:99], a[44:47]\n" \
"v_mfma_f32_16x16x32_bf16 a[44:47], v[120:123], v[160:163], a[44:47]\n" \
"ds_read_b64_tr_b16 v[166:167], v36 offset:41984\n" \
"v_mfma_f32_16x16x32_bf16 a[40:43], v[56:59], v[92:95], a[40:43]\n" \
"v_mfma_f32_16x16x32_bf16 a[40:43], v[120:123], v[156:159], a[40:43]\n" \
"v_mfma_f32_16x16x32_bf16 a[36:39], v[56:59], v[88:91], a[36:39]\n" \
"s_nop 1\n" \
"v_accvgpr_read_b32 v184, a12\n" \
"v_accvgpr_read_b32 v185, a13\n" \
"v_mfma_f32_16x16x32_bf16 a[36:39], v[120:123], v[152:155], a[36:39]\n" \
"ds_read_b64_tr_b16 v[104:105], v37 offset:32768\n" \
"v_accvgpr_read_b32 v186, a14\n" \
"v_mfma_f32_16x16x32_bf16 a[32:35], v[56:59], v[84:87], a[32:35]\n" \
"v_accvgpr_read_b32 v187, a15\n" \
"v_accvgpr_read_b32 v180, a44\n" \
"v_mfma_f32_16x16x32_bf16 a[32:35], v[120:123], v[148:151], a[32:35]\n" \
"v_accvgpr_read_b32 v181, a45\n" \
"v_accvgpr_read_b32 v182, a46\n" \
"v_mfma_f32_16x16x32_bf16 a[64:67], v[60:63], v[84:87], a[64:67]\n" \
"ds_read_b64_tr_b16 v[106:107], v37 offset:33792\n" \
"v_accvgpr_read_b32 v183, a47\n" \
"v_mfma_f32_16x16x32_bf16 a[64:67], v[124:127], v[148:151], a[64:67]\n" \
"v_cvt_pk_bf16_f32 v184, v184, v185\n" \
"v_cvt_pk_bf16_f32 v185, v186, v187\n" \
"v_mfma_f32_16x16x32_bf16 a[68:71], v[60:63], v[88:91], a[68:71]\n" \
"v_lshlrev_b32_e32 v45, 3, v20\n" \
"v_add_u32_e32 v45, s31, v45\n" \
"v_mfma_f32_16x16x32_bf16 a[68:71], v[124:127], v[152:155], a[68:71]\n" \
"s_getpc_b64 s[86:87]\n" \
"s_sub_u32 s86, s86, 0x10800\n" \
"s_subb_u32 s87, s87, 0\n" \
"s_mov_b32 m0, s32\n" \
"s_nop 0\n" \
"v_mfma_f32_16x16x32_bf16 a[72:75], v[60:63], v[92:95], a[72:75]\n" \
"global_load_lds_dword v45, s[86:87]\n" \
"v_mfma_f32_16x16x32_bf16 a[72:75], v[124:127], v[156:159], a[72:75]\n" \
"ds_read_b64_tr_b16 v[168:169], v37 offset:40960\n" \
"v_cvt_pk_bf16_f32 v186, v180, v181\n" \
"v_mfma_f32_16x16x32_bf16 a[76:79], v[60:63], v[96:99], a[76:79]\n" \
"ds_read_b64_tr_b16 v[170:171], v37 offset:41984\n" \
"v_cvt_pk_bf16_f32 v187, v182, v183\n" \
"v_mfma_f32_16x16x32_bf16 a[76:79], v[124:127], v[160:163], a[76:79]\n" \
"v_accvgpr_read_b32 v188, a8\n" \
"v_accvgpr_read_b32 v189, a9\n" \
"v_mfma_f32_16x16x32_bf16 a[108:111], v[64:67], v[96:99], a[108:111]\n" \
"v_accvgpr_read_b32 v190, a10\n" \
"v_accvgpr_read_b32 v191, a11\n" \
"v_mfma_f32_16x16x32_bf16 a[108:111], v[128:131], v[160:163], a[108:111]\n" \
"v_accvgpr_read_b32 v180, a40\n" \
"v_accvgpr_read_b32 v181, a41\n" \
"v_mfma_f32_16x16x32_bf16 a[104:107], v[64:67], v[92:95], a[104:107]\n" \
"ds_read_b64_tr_b16 v[108:109], v38 offset:32768\n" \
"v_accvgpr_read_b32 v182, a42\n" \
"v_mfma_f32_16x16x32_bf16 a[104:107], v[128:131], v[156:159], a[104:107]\n" \
"v_accvgpr_read_b32 v183, a43\n" \
"v_cvt_pk_bf16_f32 v188, v188, v189\n" \
"v_mfma_f32_16x16x32_bf16 a[100:103], v[64:67], v[88:91], a[100:103]\n" \
"v_cvt_pk_bf16_f32 v189, v190, v191\n" \
"v_cvt_pk_bf16_f32 v190, v180, v181\n" \
"v_mfma_f32_16x16x32_bf16 a[100:103], v[128:131], v[152:155], a[100:103]\n" \
"ds_read_b64_tr_b16 v[110:111], v38 offset:33792\n" \
"v_cvt_pk_bf16_f32 v191, v182, v183\n" \
"v_mfma_f32_16x16x32_bf16 a[96:99], v[64:67], v[84:87], a[96:99]\n" \
"v_accvgpr_read_b32 v192, a4\n" \
"v_accvgpr_read_b32 v193, a5\n" \
"v_mfma_f32_16x16x32_bf16 a[96:99], v[128:131], v[148:151], a[96:99]\n" \
"v_accvgpr_read_b32 v194, a6\n" \
"v_accvgpr_read_b32 v195, a7\n" \
"v_mfma_f32_16x16x32_bf16 a[128:131], v[68:71], v[84:87], a[128:131]\n" \
"ds_read_b64_tr_b16 v[172:173], v38 offset:40960\n" \
"v_accvgpr_read_b32 v180, a36\n" \
"v_mfma_f32_16x16x32_bf16 a[128:131], v[132:135], v[148:151], a[128:131]\n" \
"v_accvgpr_read_b32 v181, a37\n" \
"v_accvgpr_read_b32 v182, a38\n" \
"v_mfma_f32_16x16x32_bf16 a[132:135], v[68:71], v[88:91], a[132:135]\n" \
"v_accvgpr_read_b32 v183, a39\n" \
"v_cvt_pk_bf16_f32 v192, v192, v193\n" \
"v_mfma_f32_16x16x32_bf16 a[132:135], v[132:135], v[152:155], a[132:135]\n" \
"ds_read_b64_tr_b16 v[174:175], v38 offset:41984\n" \
"v_cvt_pk_bf16_f32 v193, v194, v195\n" \
"v_mfma_f32_16x16x32_bf16 a[136:139], v[68:71], v[92:95], a[136:139]\n" \
"v_cvt_pk_bf16_f32 v194, v180, v181\n" \
"v_cvt_pk_bf16_f32 v195, v182, v183\n" \
"v_mfma_f32_16x16x32_bf16 a[136:139], v[132:135], v[156:159], a[136:139]\n" \
"v_accvgpr_read_b32 v196, a0\n" \
"v_accvgpr_read_b32 v197, a1\n" \
"v_mfma_f32_16x16x32_bf16 a[140:143], v[68:71], v[96:99], a[140:143]\n" \
"v_accvgpr_read_b32 v198, a2\n" \
"v_accvgpr_read_b32 v199, a3\n" \
"v_mfma_f32_16x16x32_bf16 a[140:143], v[132:135], v[160:163], a[140:143]\n" \
"ds_read_b64_tr_b16 v[112:113], v39 offset:32768\n" \
"v_accvgpr_read_b32 v180, a32\n" \
"v_mfma_f32_16x16x32_bf16 a[172:175], v[72:75], v[96:99], a[172:175]\n" \
"s_add_u32 s89, s41, 0x0\n" \
"s_nop 0\n" \
"buffer_store_dwordx4 v[184:187], v20, s[80:83], s89 offen offset:3072 sc1\n" \
"v_mfma_f32_16x16x32_bf16 a[172:175], v[136:139], v[160:163], a[172:175]\n" \
"v_accvgpr_read_b32 v181, a33\n" \
"v_accvgpr_read_b32 v182, a34\n" \
"v_mfma_f32_16x16x32_bf16 a[168:171], v[72:75], v[92:95], a[168:171]\n" \
"ds_read_b64_tr_b16 v[114:115], v39 offset:33792\n" \
"v_accvgpr_read_b32 v183, a35\n" \
"v_mfma_f32_16x16x32_bf16 a[168:171], v[136:139], v[156:159], a[168:171]\n" \
"v_cvt_pk_bf16_f32 v196, v196, v197\n" \
"v_cvt_pk_bf16_f32 v197, v198, v199\n" \
"v_mfma_f32_16x16x32_bf16 a[164:167], v[72:75], v[88:91], a[164:167]\n" \
"v_cvt_pk_bf16_f32 v198, v180, v181\n" \
"v_cvt_pk_bf16_f32 v199, v182, v183\n" \
"v_mfma_f32_16x16x32_bf16 a[164:167], v[136:139], v[152:155], a[164:167]\n" \
"ds_read_b64_tr_b16 v[176:177], v39 offset:40960\n" \
"v_accvgpr_read_b32 v200, a76\n" \
"v_mfma_f32_16x16x32_bf16 a[160:163], v[72:75], v[84:87], a[160:163]\n" \
"s_add_u32 s88, s41, 0x0\n" \
"buffer_store_dwordx4 v[188:191], v20, s[80:83], s88 offen offset:2048 sc1\n" \
"v_mfma_f32_16x16x32_bf16 a[160:163], v[136:139], v[148:151], a[160:163]\n" \
"v_accvgpr_read_b32 v201, a77\n" \
"v_accvgpr_read_b32 v202, a78\n" \
"s_waitcnt lgkmcnt(15)\n" \
"v_mfma_f32_16x16x32_bf16 a[192:195], v[76:79], v[84:87], a[192:195]\n" \
"ds_read_b64_tr_b16 v[178:179], v39 offset:41984\n" \
"v_accvgpr_read_b32 v203, a79\n" \
"v_mfma_f32_16x16x32_bf16 a[192:195], v[140:143], v[148:151], a[192:195]\n" \
"v_accvgpr_read_b32 v180, a108\n" \
"v_accvgpr_read_b32 v181, a109\n" \
"v_mfma_f32_16x16x32_bf16 a[196:199], v[76:79], v[88:91], a[196:199]\n" \
"v_accvgpr_read_b32 v182, a110\n" \
"v_accvgpr_read_b32 v183, a111\n" \
"v_mfma_f32_16x16x32_bf16 a[196:199], v[140:143], v[152:155], a[196:199]\n" \
"v_cvt_pk_bf16_f32 v200, v200, v201\n" \
"v_cvt_pk_bf16_f32 v201, v202, v203\n" \
"v_mfma_f32_16x16x32_bf16 a[200:203], v[76:79], v[92:95], a[200:203]\n" \
"s_add_u32 s89, s41, 0x0\n" \
"s_nop 0\n" \
"buffer_store_dwordx4 v[192:195], v20, s[80:83], s89 offen offset:1024 sc1\n" \
"v_mfma_f32_16x16x32_bf16 a[200:203], v[140:143], v[156:159], a[200:203]\n" \
"v_cvt_pk_bf16_f32 v202, v180, v181\n" \
"v_cvt_pk_bf16_f32 v203, v182, v183\n" \
"v_mfma_f32_16x16x32_bf16 a[204:207], v[76:79], v[96:99], a[204:207]\n" \
"v_accvgpr_read_b32 v204, a72\n" \
"v_accvgpr_read_b32 v205, a73\n" \
"v_mfma_f32_16x16x32_bf16 a[204:207], v[140:143], v[160:163], a[204:207]\n" \
"v_accvgpr_read_b32 v206, a74\n" \
"v_accvgpr_read_b32 v207, a75\n" \
"v_mfma_f32_16x16x32_bf16 a[236:239], v[80:83], v[96:99], a[236:239]\n" \
"v_accvgpr_read_b32 v180, a104\n" \
"v_accvgpr_read_b32 v181, a105\n" \
"v_mfma_f32_16x16x32_bf16 a[236:239], v[144:147], v[160:163], a[236:239]\n" \
"v_accvgpr_read_b32 v182, a106\n" \
"v_accvgpr_read_b32 v183, a107\n" \
"v_mfma_f32_16x16x32_bf16 a[232:235], v[80:83], v[92:95], a[232:235]\n" \
"s_add_u32 s88, s41, 0x0\n" \
"s_nop 0\n" \
"buffer_store_dwordx4 v[196:199], v20, s[80:83], s88 offen offset:0 sc1\n" \
"v_mfma_f32_16x16x32_bf16 a[232:235], v[144:147], v[156:159], a[232:235]\n" \
"v_cvt_pk_bf16_f32 v204, v204, v205\n" \
"v_cvt_pk_bf16_f32 v205, v206, v207\n" \
"v_mfma_f32_16x16x32_bf16 a[228:231], v[80:83], v[88:91], a[228:231]\n" \
"v_cvt_pk_bf16_f32 v206, v180, v181\n" \
"v_cvt_pk_bf16_f32 v207, v182, v183\n" \
"v_mfma_f32_16x16x32_bf16 a[228:231], v[144:147], v[152:155], a[228:231]\n" \
"v_accvgpr_read_b32 v208, a68\n" \
"v_accvgpr_read_b32 v209, a69\n" \
"v_mfma_f32_16x16x32_bf16 a[224:227], v[80:83], v[84:87], a[224:227]\n" \
"v_accvgpr_read_b32 v210, a70\n" \
"v_accvgpr_read_b32 v211, a71\n" \
"v_mfma_f32_16x16x32_bf16 a[224:227], v[144:147], v[148:151], a[224:227]\n" \
"v_accvgpr_read_b32 v180, a100\n" \
"v_accvgpr_read_b32 v181, a101\n" \
"s_waitcnt vmcnt(5) lgkmcnt(0)\n" \
"s_barrier\n" \
"v_xor_b32_e32 v36, 0x4000, v36\n" \
"v_xor_b32_e64 v32, 0x0, v48\n" \
"v_xor_b32_e32 v37, 0x4000, v37\n" \
"v_xor_b32_e64 v33, 0x20, v48\n" \
"v_xor_b32_e32 v38, 0x4000, v38\n" \
"v_xor_b32_e64 v34, 0x40, v48\n" \
"v_xor_b32_e32 v39, 0x4000, v39\n" \
"v_xor_b32_e32 v35, 0x60, v48\n" \
"v_add_u32_e32 v28, s43, v28\n" \
"v_add_u32_e32 v29, s43, v29\n" \
"v_mfma_f32_16x16x32_bf16 a[144:147], v[68:71], v[100:103], a[144:147]\n" \
"s_add_u32 m0, s40, 0x8000\n" \
"s_add_u32 s85, s39, 0x2000\n" \
"v_mfma_f32_16x16x32_bf16 a[144:147], v[132:135], v[164:167], a[144:147]\n" \
"buffer_load_dwordx4 v20, s[80:83], s85 offen sc1 lds\n" \
"v_mfma_f32_16x16x32_bf16 a[148:151], v[68:71], v[104:107], a[148:151]\n" \
"ds_read_b64_tr_b16 v[84:85], v32 offset:0\n" \
"v_accvgpr_read_b32 v182, a102\n" \
"v_mfma_f32_16x16x32_bf16 a[148:151], v[132:135], v[168:171], a[148:151]\n" \
"s_add_u32 m0, s40, 0x9000\n" \
"s_add_u32 s84, s39, 0x3000\n" \
"v_mfma_f32_16x16x32_bf16 a[152:155], v[68:71], v[108:111], a[152:155]\n" \
"buffer_load_dwordx4 v20, s[80:83], s84 offen sc1 lds\n" \
"v_mfma_f32_16x16x32_bf16 a[152:155], v[132:135], v[172:175], a[152:155]\n" \
"ds_read_b64_tr_b16 v[86:87], v32 offset:512\n" \
"v_accvgpr_read_b32 v183, a103\n" \
"v_mfma_f32_16x16x32_bf16 a[156:159], v[68:71], v[112:115], a[156:159]\n" \
"ds_read_b64_tr_b16 v[148:149], v32 offset:4096\n" \
"v_cvt_pk_bf16_f32 v208, v208, v209\n" \
"v_mfma_f32_16x16x32_bf16 a[156:159], v[132:135], v[176:179], a[156:159]\n" \
"ds_read_b128 v[68:71], v28 offset:8192\n" \
"v_cvt_pk_bf16_f32 v209, v210, v211\n" \
"v_mfma_f32_16x16x32_bf16 a[188:191], v[72:75], v[112:115], a[188:191]\n" \
"ds_read_b128 v[132:135], v29 offset:8192\n" \
"v_cvt_pk_bf16_f32 v210, v180, v181\n" \
"v_mfma_f32_16x16x32_bf16 a[188:191], v[136:139], v[176:179], a[188:191]\n" \
"s_add_u32 m0, s40, 0xb000\n" \
"s_mov_b32 s85, 0x80\n" \
"v_mfma_f32_16x16x32_bf16 a[184:187], v[72:75], v[108:111], a[184:187]\n" \
"buffer_load_dwordx4 v21, s[76:79], s85 offen lds\n" \
"v_mfma_f32_16x16x32_bf16 a[184:187], v[136:139], v[172:175], a[184:187]\n" \
"s_add_u32 m0, s30, 0x8000\n" \
"s_mov_b32 s84, 0x80\n" \
"v_mfma_f32_16x16x32_bf16 a[180:183], v[72:75], v[104:107], a[180:183]\n" \
"buffer_load_dwordx4 v24, s[76:79], s84 offen lds\n" \
"v_mfma_f32_16x16x32_bf16 a[180:183], v[136:139], v[168:171], a[180:183]\n" \
"ds_read_b64_tr_b16 v[150:151], v32 offset:4608\n" \
"v_cvt_pk_bf16_f32 v211, v182, v183\n" \
"v_mfma_f32_16x16x32_bf16 a[176:179], v[72:75], v[100:103], a[176:179]\n" \
"ds_read_b64_tr_b16 v[88:89], v33 offset:0\n" \
"v_accvgpr_read_b32 v212, a64\n" \
"v_mfma_f32_16x16x32_bf16 a[176:179], v[136:139], v[164:167], a[176:179]\n" \
"ds_read_b128 v[72:75], v28 offset:10240\n" \
"v_accvgpr_read_b32 v213, a65\n" \
"v_mfma_f32_16x16x32_bf16 a[208:211], v[76:79], v[100:103], a[208:211]\n" \
"ds_read_b128 v[136:139], v29 offset:10240\n" \
"v_accvgpr_read_b32 v214, a66\n" \
"v_mfma_f32_16x16x32_bf16 a[208:211], v[140:143], v[164:167], a[208:211]\n" \
"s_add_u32 m0, s30, 0x8400\n" \
"s_mov_b32 s85, 0x2480\n" \
"v_mfma_f32_16x16x32_bf16 a[212:215], v[76:79], v[104:107], a[212:215]\n" \
"buffer_load_dwordx4 v25, s[76:79], s85 offen lds\n" \
"v_mfma_f32_16x16x32_bf16 a[212:215], v[140:143], v[168:171], a[212:215]\n" \
"s_add_u32 m0, s31, 0x8000\n" \
"s_mov_b32 s84, 0x80000\n" \
"v_mfma_f32_16x16x32_bf16 a[216:219], v[76:79], v[108:111], a[216:219]\n" \
"buffer_load_dwordx4 v26, s[68:71], s84 offen lds\n" \
"v_mfma_f32_16x16x32_bf16 a[216:219], v[140:143], v[172:175], a[216:219]\n" \
"ds_read_b64_tr_b16 v[90:91], v33 offset:512\n" \
"v_accvgpr_read_b32 v215, a67\n" \
"v_mfma_f32_16x16x32_bf16 a[220:223], v[76:79], v[112:115], a[220:223]\n" \
"ds_read_b64_tr_b16 v[152:153], v33 offset:4096\n" \
"v_accvgpr_read_b32 v180, a96\n" \
"v_mfma_f32_16x16x32_bf16 a[220:223], v[140:143], v[176:179], a[220:223]\n" \
"ds_read_b128 v[76:79], v28 offset:12288\n" \
"v_accvgpr_read_b32 v181, a97\n" \
"v_mfma_f32_16x16x32_bf16 a[252:255], v[80:83], v[112:115], a[252:255]\n" \
"ds_read_b128 v[140:143], v29 offset:12288\n" \
"v_accvgpr_read_b32 v182, a98\n" \
"v_mfma_f32_16x16x32_bf16 a[252:255], v[144:147], v[176:179], a[252:255]\n" \
"s_add_u32 m0, s30, 0x8800\n" \
"s_mov_b32 s85, 0x4880\n" \
"v_mfma_f32_16x16x32_bf16 a[248:251], v[80:83], v[108:111], a[248:251]\n" \
"buffer_load_dwordx4 v24, s[76:79], s85 offen lds\n" \
"v_mfma_f32_16x16x32_bf16 a[248:251], v[144:147], v[172:175], a[248:251]\n" \
"s_add_u32 m0, s30, 0x8c00\n" \
"s_mov_b32 s84, 0x6c80\n" \
"v_mfma_f32_16x16x32_bf16 a[244:247], v[80:83], v[104:107], a[244:247]\n" \
"buffer_load_dwordx4 v25, s[76:79], s84 offen lds\n" \
"v_mfma_f32_16x16x32_bf16 a[244:247], v[144:147], v[168:171], a[244:247]\n" \
"s_add_u32 m0, s31, 0x8400\n" \
"s_mov_b32 s85, 0x88000\n" \
"v_mfma_f32_16x16x32_bf16 a[240:243], v[80:83], v[100:103], a[240:243]\n" \
"buffer_load_dwordx4 v26, s[68:71], s85 offen lds\n" \
"v_mfma_f32_16x16x32_bf16 a[240:243], v[144:147], v[164:167], a[240:243]\n" \
"ds_read_b128 v[80:83], v28 offset:14336\n" \
"v_accvgpr_read_b32 v183, a99\n" \
"v_mfma_f32_16x16x32_bf16 a[80:83], v[60:63], v[100:103], a[80:83]\n" \
"ds_read_b128 v[144:147], v29 offset:14336\n" \
"v_cvt_pk_bf16_f32 v212, v212, v213\n" \
"v_mfma_f32_16x16x32_bf16 a[80:83], v[124:127], v[164:167], a[80:83]\n" \
"s_add_u32 m0, s30, 0x9000\n" \
"s_mov_b32 s84, 0x9080\n" \
"v_mfma_f32_16x16x32_bf16 a[84:87], v[60:63], v[104:107], a[84:87]\n" \
"buffer_load_dwordx4 v24, s[76:79], s84 offen lds\n" \
"v_mfma_f32_16x16x32_bf16 a[84:87], v[124:127], v[168:171], a[84:87]\n" \
"ds_read_b64_tr_b16 v[154:155], v33 offset:4608\n" \
"v_cvt_pk_bf16_f32 v213, v214, v215\n" \
"v_mfma_f32_16x16x32_bf16 a[88:91], v[60:63], v[108:111], a[88:91]\n" \
"s_add_u32 m0, s30, 0x9400\n" \
"s_mov_b32 s85, 0xb480\n" \
"v_mfma_f32_16x16x32_bf16 a[88:91], v[124:127], v[172:175], a[88:91]\n" \
"buffer_load_dwordx4 v25, s[76:79], s85 offen lds\n" \
"v_mfma_f32_16x16x32_bf16 a[92:95], v[60:63], v[112:115], a[92:95]\n" \
".fill (L_pb + 65248 - .) / 4, 4, 0xBF800000\n" \
"L_pp16:\n" \
"s_cbranch_execz L_pret\n" \
"s_nop 0\n" \
"ds_read_b64_tr_b16 v[96:97], v35 offset:0\n" \
"v_cvt_pk_bf16_f32 v214, v180, v181\n" \
"v_mfma_f32_16x16x32_bf16 a[92:95], v[124:127], v[176:179], a[92:95]\n" \
"ds_read_b128 v[60:63], v28 offset:4096\n" \
"v_cvt_pk_bf16_f32 v215, v182, v183\n" \
"v_mfma_f32_16x16x32_bf16 a[124:127], v[64:67], v[112:115], a[124:127]\n" \
"ds_read_b128 v[124:127], v29 offset:4096\n" \
"v_accvgpr_read_b32 v216, a140\n" \
"v_mfma_f32_16x16x32_bf16 a[124:127], v[128:131], v[176:179], a[124:127]\n" \
"s_add_u32 m0, s31, 0x8800\n" \
"s_mov_b32 s84, 0x90000\n" \
"v_mfma_f32_16x16x32_bf16 a[120:123], v[64:67], v[108:111], a[120:123]\n" \
"buffer_load_dwordx4 v27, s[68:71], s84 offen lds\n" \
"v_mfma_f32_16x16x32_bf16 a[120:123], v[128:131], v[172:175], a[120:123]\n" \
"ds_read_b64_tr_b16 v[98:99], v35 offset:512\n" \
"v_accvgpr_read_b32 v217, a141\n" \
"v_mfma_f32_16x16x32_bf16 a[116:119], v[64:67], v[104:107], a[116:119]\n" \
"s_add_u32 m0, s30, 0x9800\n" \
"s_mov_b32 s85, 0xd880\n" \
"v_mfma_f32_16x16x32_bf16 a[116:119], v[128:131], v[168:171], a[116:119]\n" \
"buffer_load_dwordx4 v24, s[76:79], s85 offen lds\n" \
"v_mfma_f32_16x16x32_bf16 a[112:115], v[64:67], v[100:103], a[112:115]\n" \
"ds_read_b64_tr_b16 v[160:161], v35 offset:4096\n" \
"v_accvgpr_read_b32 v218, a142\n" \
"v_mfma_f32_16x16x32_bf16 a[112:115], v[128:131], v[164:167], a[112:115]\n" \
"ds_read_b128 v[64:67], v28 offset:6144\n" \
"v_accvgpr_read_b32 v219, a143\n" \
"v_mfma_f32_16x16x32_bf16 a[16:19], v[52:55], v[100:103], a[16:19]\n" \
"ds_read_b128 v[128:131], v29 offset:6144\n" \
"v_accvgpr_read_b32 v180, a172\n" \
"v_mfma_f32_16x16x32_bf16 a[16:19], v[116:119], v[164:167], a[16:19]\n" \
"ds_read_b64_tr_b16 v[162:163], v35 offset:4608\n" \
"v_accvgpr_read_b32 v181, a173\n" \
"v_mfma_f32_16x16x32_bf16 a[20:23], v[52:55], v[104:107], a[20:23]\n" \
"ds_read_b64_tr_b16 v[92:93], v34 offset:0\n" \
"v_accvgpr_read_b32 v182, a174\n" \
"v_mfma_f32_16x16x32_bf16 a[20:23], v[116:119], v[168:171], a[20:23]\n" \
"ds_read_b64_tr_b16 v[94:95], v34 offset:512\n" \
"v_accvgpr_read_b32 v183, a175\n" \
"v_mfma_f32_16x16x32_bf16 a[24:27], v[52:55], v[108:111], a[24:27]\n" \
"ds_read_b64_tr_b16 v[156:157], v34 offset:4096\n" \
"v_cvt_pk_bf16_f32 v216, v216, v217\n" \
"v_mfma_f32_16x16x32_bf16 a[24:27], v[116:119], v[172:175], a[24:27]\n" \
"ds_read_b64_tr_b16 v[158:159], v34 offset:4608\n" \
"v_cvt_pk_bf16_f32 v217, v218, v219\n" \
"v_mfma_f32_16x16x32_bf16 a[28:31], v[52:55], v[112:115], a[28:31]\n" \
"v_cvt_pk_bf16_f32 v218, v180, v181\n" \
"v_cvt_pk_bf16_f32 v219, v182, v183\n" \
"v_mfma_f32_16x16x32_bf16 a[28:31], v[116:119], v[176:179], a[28:31]\n" \
"ds_read_b128 v[52:55], v28 offset:0\n" \
"v_accvgpr_read_b32 v220, a136\n" \
"v_mfma_f32_16x16x32_bf16 a[60:63], v[56:59], v[112:115], a[60:63]\n" \
"ds_read_b128 v[116:119], v29 offset:0\n" \
"v_accvgpr_read_b32 v221, a137\n" \
"v_mfma_f32_16x16x32_bf16 a[60:63], v[120:123], v[176:179], a[60:63]\n" \
"v_accvgpr_read_b32 v222, a138\n" \
"v_accvgpr_read_b32 v223, a139\n" \
"v_mfma_f32_16x16x32_bf16 a[56:59], v[56:59], v[108:111], a[56:59]\n" \
"v_accvgpr_read_b32 v180, a168\n" \
"v_accvgpr_read_b32 v181, a169\n" \
"v_mfma_f32_16x16x32_bf16 a[56:59], v[120:123], v[172:175], a[56:59]\n" \
"v_accvgpr_read_b32 v182, a170\n" \
"v_accvgpr_read_b32 v183, a171\n" \
"v_mfma_f32_16x16x32_bf16 a[52:55], v[56:59], v[104:107], a[52:55]\n" \
"v_cvt_pk_bf16_f32 v220, v220, v221\n" \
"v_cvt_pk_bf16_f32 v221, v222, v223\n" \
"v_mfma_f32_16x16x32_bf16 a[52:55], v[120:123], v[168:171], a[52:55]\n" \
"v_cvt_pk_bf16_f32 v222, v180, v181\n" \
"v_cvt_pk_bf16_f32 v223, v182, v183\n" \
"v_mfma_f32_16x16x32_bf16 a[48:51], v[56:59], v[100:103], a[48:51]\n" \
"v_accvgpr_read_b32 v224, a132\n" \
"v_accvgpr_read_b32 v225, a133\n" \
"v_mfma_f32_16x16x32_bf16 a[48:51], v[120:123], v[164:167], a[48:51]\n" \
"ds_read_b128 v[56:59], v28 offset:2048\n" \
"ds_read_b128 v[120:123], v29 offset:2048\n" \
"s_waitcnt lgkmcnt(15)\n" \
"s_nop 0\n" \
"v_mfma_f32_16x16x32_bf16 a[0:3], v[84:87], v[68:71], 0\n" \
"s_add_u32 m0, s30, 0x9c00\n" \
"s_mov_b32 s85, 0xfc80\n" \
"v_accvgpr_read_b32 v226, a134\n" \
"v_mfma_f32_16x16x32_bf16 a[0:3], v[148:151], v[132:135], a[0:3]\n" \
"buffer_load_dwordx4 v25, s[76:79], s85 offen lds\n" \
"v_accvgpr_read_b32 v227, a135\n" \
"v_mfma_f32_16x16x32_bf16 a[4:7], v[88:91], v[68:71], 0\n" \
"ds_read_b64_tr_b16 v[100:101], v36 offset:0\n" \
"v_accvgpr_read_b32 v180, a164\n" \
"v_mfma_f32_16x16x32_bf16 a[4:7], v[152:155], v[132:135], a[4:7]\n" \
"s_add_u32 m0, s31, 0x8c00\n" \
"s_mov_b32 s84, 0x98000\n" \
"v_accvgpr_read_b32 v181, a165\n" \
"s_waitcnt lgkmcnt(13)\n" \
"v_mfma_f32_16x16x32_bf16 a[36:39], v[96:99], v[68:71], 0\n" \
"buffer_load_dwordx4 v27, s[68:71], s84 offen lds\n" \
"v_accvgpr_read_b32 v182, a166\n" \
"s_waitcnt lgkmcnt(9)\n" \
"v_mfma_f32_16x16x32_bf16 a[36:39], v[160:163], v[132:135], a[36:39]\n" \
"ds_read_b64_tr_b16 v[102:103], v36 offset:1024\n" \
"v_accvgpr_read_b32 v183, a167\n" \
"s_waitcnt lgkmcnt(8)\n" \
"v_mfma_f32_16x16x32_bf16 a[32:35], v[68:71], v[92:95], 0\n" \
"ds_read_b64_tr_b16 v[164:165], v36 offset:8192\n" \
"v_cvt_pk_bf16_f32 v224, v224, v225\n" \
"s_waitcnt lgkmcnt(7)\n" \
"v_mfma_f32_16x16x32_bf16 a[32:35], v[132:135], v[156:159], a[32:35]\n" \
"ds_read_b64_tr_b16 v[166:167], v36 offset:9216\n" \
"v_cvt_pk_bf16_f32 v225, v226, v227\n" \
"v_mfma_f32_16x16x32_bf16 a[96:99], v[72:75], v[92:95], 0\n" \
"ds_read_b64_tr_b16 v[104:105], v37 offset:0\n" \
"v_cvt_pk_bf16_f32 v226, v180, v181\n" \
"v_mfma_f32_16x16x32_bf16 a[96:99], v[136:139], v[156:159], a[96:99]\n" \
"ds_read_b64_tr_b16 v[106:107], v37 offset:1024\n" \
"v_cvt_pk_bf16_f32 v227, v182, v183\n" \
"v_mfma_f32_16x16x32_bf16 a[100:103], v[96:99], v[72:75], 0\n" \
"ds_read_b64_tr_b16 v[168:169], v37 offset:8192\n" \
"v_accvgpr_read_b32 v228, a128\n" \
"v_mfma_f32_16x16x32_bf16 a[100:103], v[160:163], v[136:139], a[100:103]\n" \
"s_add_u32 s89, s41, 0x2000\n" \
"buffer_store_dwordx4 v[200:203], v20, s[80:83], s89 offen offset:3072 sc1\n" \
"v_mfma_f32_16x16x32_bf16 a[68:71], v[88:91], v[72:75], 0\n" \
"ds_read_b64_tr_b16 v[170:171], v37 offset:9216\n" \
"v_accvgpr_read_b32 v229, a129\n" \
"v_mfma_f32_16x16x32_bf16 a[68:71], v[152:155], v[136:139], a[68:71]\n" \
"ds_read_b64_tr_b16 v[108:109], v38 offset:0\n" \
"v_accvgpr_read_b32 v230, a130\n" \
"v_mfma_f32_16x16x32_bf16 a[64:67], v[84:87], v[72:75], 0\n" \
"ds_read_b64_tr_b16 v[110:111], v38 offset:1024\n" \
"v_accvgpr_read_b32 v231, a131\n" \
"v_mfma_f32_16x16x32_bf16 a[64:67], v[148:151], v[136:139], a[64:67]\n" \
"v_accvgpr_read_b32 v180, a160\n" \
"v_accvgpr_read_b32 v181, a161\n" \
"v_accvgpr_read_b32 v182, a162\n" \
"v_accvgpr_read_b32 v183, a163\n" \
"v_cvt_pk_bf16_f32 v228, v228, v229\n" \
"v_cvt_pk_bf16_f32 v229, v230, v231\n" \
"v_cvt_pk_bf16_f32 v230, v180, v181\n" \
"v_cvt_pk_bf16_f32 v231, v182, v183\n" \
"v_mfma_f32_16x16x32_bf16 a[128:131], v[84:87], v[76:79], 0\n" \
"s_add_u32 s88, s41, 0x2000\n" \
"buffer_store_dwordx4 v[204:207], v20, s[80:83], s88 offen offset:2048 sc1\n" \
"v_mfma_f32_16x16x32_bf16 a[128:131], v[148:151], v[140:143], a[128:131]\n" \
"ds_read_b64_tr_b16 v[172:173], v38 offset:8192\n" \
"v_accvgpr_read_b32 v232, a204\n" \
"v_mfma_f32_16x16x32_bf16 a[132:135], v[88:91], v[76:79], 0\n" \
"ds_read_b64_tr_b16 v[174:175], v38 offset:9216\n" \
"v_accvgpr_read_b32 v233, a205\n" \
"v_mfma_f32_16x16x32_bf16 a[132:135], v[152:155], v[140:143], a[132:135]\n" \
"ds_read_b64_tr_b16 v[112:113], v39 offset:0\n" \
"v_accvgpr_read_b32 v234, a206\n" \
"v_mfma_f32_16x16x32_bf16 a[164:167], v[96:99], v[76:79], 0\n" \
"v_accvgpr_read_b32 v235, a207\n" \
"v_accvgpr_read_b32 v180, a236\n" \
"v_accvgpr_read_b32 v181, a237\n" \
"v_mfma_f32_16x16x32_bf16 a[164:167], v[160:163], v[140:143], a[164:167]\n" \
"ds_read_b64_tr_b16 v[114:115], v39 offset:1024\n" \
"v_accvgpr_read_b32 v182, a238\n" \
"v_mfma_f32_16x16x32_bf16 a[160:163], v[76:79], v[92:95], 0\n" \
"s_add_u32 s89, s41, 0x2000\n" \
"buffer_store_dwordx4 v[208:211], v20, s[80:83], s89 offen offset:1024 sc1\n" \
"v_mfma_f32_16x16x32_bf16 a[160:163], v[140:143], v[156:159], a[160:163]\n" \
"ds_read_b64_tr_b16 v[176:177], v39 offset:8192\n" \
"v_accvgpr_read_b32 v183, a239\n" \
"v_cvt_pk_bf16_f32 v232, v232, v233\n" \
"v_cvt_pk_bf16_f32 v233, v234, v235\n" \
"v_cvt_pk_bf16_f32 v234, v180, v181\n" \
"v_cvt_pk_bf16_f32 v235, v182, v183\n" \
"v_accvgpr_read_b32 v236, a200\n" \
"v_accvgpr_read_b32 v237, a201\n" \
"v_accvgpr_read_b32 v238, a202\n" \
"v_accvgpr_read_b32 v239, a203\n" \
"v_accvgpr_read_b32 v180, a232\n" \
"v_accvgpr_read_b32 v181, a233\n" \
"v_accvgpr_read_b32 v182, a234\n" \
"v_accvgpr_read_b32 v183, a235\n" \
"v_cvt_pk_bf16_f32 v236, v236, v237\n" \
"v_cvt_pk_bf16_f32 v237, v238, v239\n" \
"v_cvt_pk_bf16_f32 v238, v180, v181\n" \
"v_cvt_pk_bf16_f32 v239, v182, v183\n" \
"v_accvgpr_read_b32 v240, a196\n" \
"v_accvgpr_read_b32 v241, a197\n" \
"v_accvgpr_read_b32 v242, a198\n" \
"v_accvgpr_read_b32 v243, a199\n" \
"v_accvgpr_read_b32 v180, a228\n" \
"v_accvgpr_read_b32 v181, a229\n" \
"v_accvgpr_read_b32 v182, a230\n" \
"v_accvgpr_read_b32 v183, a231\n" \
"v_cvt_pk_bf16_f32 v240, v240, v241\n" \
"v_cvt_pk_bf16_f32 v241, v242, v243\n" \
"v_cvt_pk_bf16_f32 v242, v180, v181\n" \
"v_cvt_pk_bf16_f32 v243, v182, v183\n" \
"v_accvgpr_read_b32 v244, a192\n" \
"v_accvgpr_read_b32 v245, a193\n" \
"v_accvgpr_read_b32 v246, a194\n" \
"v_accvgpr_read_b32 v247, a195\n" \
"v_accvgpr_read_b32 v180, a224\n" \
"v_accvgpr_read_b32 v181, a225\n" \
"v_accvgpr_read_b32 v182, a226\n" \
"v_accvgpr_read_b32 v183, a227\n" \
"v_cvt_pk_bf16_f32 v244, v244, v245\n" \
"v_cvt_pk_bf16_f32 v245, v246, v247\n" \
"v_cvt_pk_bf16_f32 v246, v180, v181\n" \
"v_cvt_pk_bf16_f32 v247, v182, v183\n" \
"v_mfma_f32_16x16x32_bf16 a[224:227], v[80:83], v[92:95], 0\n" \
"ds_read_b64_tr_b16 v[178:179], v39 offset:9216\n" \
"v_accvgpr_read_b32 v184, a144\n" \
"v_mfma_f32_16x16x32_bf16 a[224:227], v[144:147], v[156:159], a[224:227]\n" \
"v_accvgpr_read_b32 v185, a145\n" \
"v_accvgpr_read_b32 v186, a146\n" \
"v_accvgpr_read_b32 v187, a147\n" \
"v_mfma_f32_16x16x32_bf16 a[228:231], v[96:99], v[80:83], 0\n" \
"v_accvgpr_read_b32 v180, a176\n" \
"v_accvgpr_read_b32 v181, a177\n" \
"v_accvgpr_read_b32 v182, a178\n" \
"v_mfma_f32_16x16x32_bf16 a[228:231], v[160:163], v[144:147], a[228:231]\n" \
"s_add_u32 s88, s41, 0x2000\n" \
"buffer_store_dwordx4 v[212:215], v20, s[80:83], s88 offen offset:0 sc1\n" \
"v_mfma_f32_16x16x32_bf16 a[196:199], v[88:91], v[80:83], 0\n" \
"v_accvgpr_read_b32 v183, a179\n" \
"v_cvt_pk_bf16_f32 v184, v184, v185\n" \
"v_cvt_pk_bf16_f32 v185, v186, v187\n" \
"v_mfma_f32_16x16x32_bf16 a[196:199], v[152:155], v[144:147], a[196:199]\n" \
"v_cvt_pk_bf16_f32 v186, v180, v181\n" \
"v_cvt_pk_bf16_f32 v187, v182, v183\n" \
"v_accvgpr_read_b32 v188, a148\n" \
"v_mfma_f32_16x16x32_bf16 a[192:195], v[84:87], v[80:83], 0\n" \
"v_accvgpr_read_b32 v189, a149\n" \
"v_accvgpr_read_b32 v190, a150\n" \
"v_accvgpr_read_b32 v191, a151\n" \
"v_mfma_f32_16x16x32_bf16 a[192:195], v[148:151], v[144:147], a[192:195]\n" \
"v_accvgpr_read_b32 v180, a180\n" \
"v_accvgpr_read_b32 v181, a181\n" \
"v_accvgpr_read_b32 v182, a182\n" \
"v_mfma_f32_16x16x32_bf16 a[8:11], v[88:91], v[64:67], 0\n" \
"v_accvgpr_read_b32 v183, a183\n" \
"v_cvt_pk_bf16_f32 v188, v188, v189\n" \
"v_cvt_pk_bf16_f32 v189, v190, v191\n" \
"v_mfma_f32_16x16x32_bf16 a[8:11], v[152:155], v[128:131], a[8:11]\n" \
"v_cvt_pk_bf16_f32 v190, v180, v181\n" \
"v_cvt_pk_bf16_f32 v191, v182, v183\n" \
"v_accvgpr_read_b32 v192, a80\n" \
"v_mfma_f32_16x16x32_bf16 a[40:43], v[96:99], v[64:67], 0\n" \
"v_accvgpr_read_b32 v193, a81\n" \
"v_accvgpr_read_b32 v194, a82\n" \
"v_accvgpr_read_b32 v195, a83\n" \
"v_mfma_f32_16x16x32_bf16 a[40:43], v[160:163], v[128:131], a[40:43]\n" \
"v_accvgpr_read_b32 v180, a112\n" \
"v_accvgpr_read_b32 v181, a113\n" \
"v_accvgpr_read_b32 v182, a114\n" \
"v_mfma_f32_16x16x32_bf16 a[12:15], v[64:67], v[92:95], 0\n" \
"v_accvgpr_read_b32 v183, a115\n" \
"v_cvt_pk_bf16_f32 v192, v192, v193\n" \
"v_cvt_pk_bf16_f32 v193, v194, v195\n" \
"v_mfma_f32_16x16x32_bf16 a[12:15], v[128:131], v[156:159], a[12:15]\n" \
"v_cvt_pk_bf16_f32 v194, v180, v181\n" \
"v_cvt_pk_bf16_f32 v195, v182, v183\n" \
"v_accvgpr_read_b32 v196, a84\n" \
"s_waitcnt vmcnt(4) lgkmcnt(0)\n" \
"s_barrier\n" \
"v_mfma_f32_16x16x32_bf16 a[72:75], v[100:103], v[76:79], 0\n" \
"s_add_u32 m0, s40, 0x0\n" \
"s_nop 0\n" \
"s_add_u32 s85, s39, 0x4000\n" \
"v_accvgpr_read_b32 v197, a85\n" \
"v_mfma_f32_16x16x32_bf16 a[72:75], v[164:167], v[140:143], a[72:75]\n" \
"buffer_load_dwordx4 v20, s[80:83], s85 offen sc1 lds\n" \
"v_accvgpr_read_b32 v198, a86\n" \
"v_mfma_f32_16x16x32_bf16 a[76:79], v[104:107], v[76:79], 0\n" \
"ds_read_b64_tr_b16 v[84:85], v32 offset:32768\n" \
"v_accvgpr_read_b32 v199, a87\n" \
"v_mfma_f32_16x16x32_bf16 a[76:79], v[168:171], v[140:143], a[76:79]\n" \
"ds_read_b64_tr_b16 v[86:87], v32 offset:33280\n" \
"v_accvgpr_read_b32 v180, a116\n" \
"v_mfma_f32_16x16x32_bf16 a[104:107], v[108:111], v[76:79], 0\n" \
"s_add_u32 m0, s40, 0x1000\n" \
"s_add_u32 s84, s39, 0x5000\n" \
"v_accvgpr_read_b32 v181, a117\n" \
"v_mfma_f32_16x16x32_bf16 a[104:107], v[172:175], v[140:143], a[104:107]\n" \
"buffer_load_dwordx4 v20, s[80:83], s84 offen sc1 lds\n" \
"v_accvgpr_read_b32 v182, a118\n" \
"v_mfma_f32_16x16x32_bf16 a[108:111], v[112:115], v[76:79], 0\n" \
"ds_read_b64_tr_b16 v[148:149], v32 offset:36864\n" \
"v_accvgpr_read_b32 v183, a119\n" \
"v_mfma_f32_16x16x32_bf16 a[108:111], v[176:179], v[140:143], a[108:111]\n" \
"ds_read_b128 v[76:79], v28 offset:45056\n" \
"v_cvt_pk_bf16_f32 v196, v196, v197\n" \
"v_mfma_f32_16x16x32_bf16 a[172:175], v[112:115], v[80:83], 0\n" \
"ds_read_b128 v[140:143], v29 offset:45056\n" \
"v_cvt_pk_bf16_f32 v197, v198, v199\n" \
"v_mfma_f32_16x16x32_bf16 a[172:175], v[176:179], v[144:147], a[172:175]\n" \
"s_add_u32 m0, s40, 0x3000\n" \
"s_mov_b32 s85, 0x100\n" \
"v_cvt_pk_bf16_f32 v198, v180, v181\n" \
"v_mfma_f32_16x16x32_bf16 a[168:171], v[108:111], v[80:83], 0\n" \
"buffer_load_dwordx4 v21, s[76:79], s85 offen lds\n" \
"v_cvt_pk_bf16_f32 v199, v182, v183\n" \
"v_mfma_f32_16x16x32_bf16 a[168:171], v[172:175], v[144:147], a[168:171]\n" \
"ds_read_b64_tr_b16 v[150:151], v32 offset:37376\n" \
"v_accvgpr_read_b32 v252, a88\n" \
"v_mfma_f32_16x16x32_bf16 a[140:143], v[104:107], v[80:83], 0\n" \
"s_add_u32 m0, s30, 0x0\n" \
"s_nop 0\n" \
"s_mov_b32 s84, 0x100\n" \
"v_accvgpr_read_b32 v253, a89\n" \
"v_mfma_f32_16x16x32_bf16 a[140:143], v[168:171], v[144:147], a[140:143]\n" \
"buffer_load_dwordx4 v24, s[76:79], s84 offen lds\n" \
"v_accvgpr_read_b32 v254, a90\n" \
"v_mfma_f32_16x16x32_bf16 a[136:139], v[100:103], v[80:83], 0\n" \
"ds_read_b64_tr_b16 v[88:89], v33 offset:32768\n" \
"v_accvgpr_read_b32 v255, a91\n" \
"v_mfma_f32_16x16x32_bf16 a[136:139], v[164:167], v[144:147], a[136:139]\n" \
"ds_read_b128 v[80:83], v28 offset:47104\n" \
"v_accvgpr_read_b32 v180, a120\n" \
"v_mfma_f32_16x16x32_bf16 a[144:147], v[100:103], v[68:71], 0\n" \
"ds_read_b128 v[144:147], v29 offset:47104\n" \
"v_accvgpr_read_b32 v181, a121\n" \
"v_mfma_f32_16x16x32_bf16 a[144:147], v[164:167], v[132:135], a[144:147]\n" \
"s_add_u32 m0, s30, 0x400\n" \
"s_mov_b32 s85, 0x2500\n" \
"v_accvgpr_read_b32 v182, a122\n" \
"v_mfma_f32_16x16x32_bf16 a[148:151], v[104:107], v[68:71], 0\n" \
"buffer_load_dwordx4 v25, s[76:79], s85 offen lds\n" \
"v_accvgpr_read_b32 v183, a123\n" \
"v_mfma_f32_16x16x32_bf16 a[148:151], v[168:171], v[132:135], a[148:151]\n" \
"ds_read_b64_tr_b16 v[90:91], v33 offset:33280\n" \
"v_cvt_pk_bf16_f32 v252, v252, v253\n" \
"v_mfma_f32_16x16x32_bf16 a[200:203], v[108:111], v[68:71], 0\n" \
"ds_read_b64_tr_b16 v[152:153], v33 offset:36864\n" \
"v_cvt_pk_bf16_f32 v253, v254, v255\n" \
"v_mfma_f32_16x16x32_bf16 a[200:203], v[172:175], v[132:135], a[200:203]\n" \
"s_add_u32 m0, s31, 0x0\n" \
"s_nop 0\n" \
"s_mov_b32 s84, 0x100000\n" \
"v_cvt_pk_bf16_f32 v254, v180, v181\n" \
"v_mfma_f32_16x16x32_bf16 a[204:207], v[112:115], v[68:71], 0\n" \
"buffer_load_dwordx4 v26, s[68:71], s84 offen lds\n" \
"v_cvt_pk_bf16_f32 v255, v182, v183\n" \
"v_mfma_f32_16x16x32_bf16 a[204:207], v[176:179], v[132:135], a[204:207]\n" \
"ds_read_b128 v[68:71], v28 offset:40960\n" \
"v_accvgpr_write_b32 a44, v252\n" \
"v_mfma_f32_16x16x32_bf16 a[236:239], v[112:115], v[72:75], 0\n" \
"ds_read_b128 v[132:135], v29 offset:40960\n" \
"v_accvgpr_write_b32 a45, v253\n" \
"v_mfma_f32_16x16x32_bf16 a[236:239], v[176:179], v[136:139], a[236:239]\n" \
"s_add_u32 m0, s30, 0x800\n" \
"s_mov_b32 s85, 0x4900\n" \
"v_accvgpr_write_b32 a46, v254\n" \
"v_mfma_f32_16x16x32_bf16 a[232:235], v[108:111], v[72:75], 0\n" \
"buffer_load_dwordx4 v24, s[76:79], s85 offen lds\n" \
"v_accvgpr_write_b32 a47, v255\n" \
"v_mfma_f32_16x16x32_bf16 a[232:235], v[172:175], v[136:139], a[232:235]\n" \
"ds_read_b64_tr_b16 v[154:155], v33 offset:37376\n" \
"v_accvgpr_read_b32 v252, a92\n" \
"v_mfma_f32_16x16x32_bf16 a[180:183], v[104:107], v[72:75], 0\n" \
"ds_read_b64_tr_b16 v[96:97], v35 offset:32768\n" \
"v_accvgpr_read_b32 v253, a93\n" \
"v_mfma_f32_16x16x32_bf16 a[180:183], v[168:171], v[136:139], a[180:183]\n" \
"ds_read_b64_tr_b16 v[98:99], v35 offset:33280\n" \
"v_accvgpr_read_b32 v254, a94\n" \
"v_mfma_f32_16x16x32_bf16 a[176:179], v[100:103], v[72:75], 0\n" \
"v_accvgpr_read_b32 v255, a95\n" \
"v_accvgpr_read_b32 v180, a124\n" \
"v_accvgpr_read_b32 v181, a125\n" \
"v_mfma_f32_16x16x32_bf16 a[176:179], v[164:167], v[136:139], a[176:179]\n" \
"ds_read_b128 v[72:75], v28 offset:43008\n" \
"v_accvgpr_read_b32 v182, a126\n" \
"v_mfma_f32_16x16x32_bf16 a[80:83], v[100:103], v[60:63], 0\n" \
"ds_read_b128 v[136:139], v29 offset:43008\n" \
"v_accvgpr_read_b32 v183, a127\n" \
"v_mfma_f32_16x16x32_bf16 a[80:83], v[164:167], v[124:127], a[80:83]\n" \
"s_add_u32 m0, s30, 0xc00\n" \
"s_mov_b32 s84, 0x6d00\n" \
"v_cvt_pk_bf16_f32 v252, v252, v253\n" \
"v_mfma_f32_16x16x32_bf16 a[84:87], v[104:107], v[60:63], 0\n" \
"buffer_load_dwordx4 v25, s[76:79], s84 offen lds\n" \
"v_cvt_pk_bf16_f32 v253, v254, v255\n" \
"v_mfma_f32_16x16x32_bf16 a[84:87], v[168:171], v[124:127], a[84:87]\n" \
"s_add_u32 m0, s31, 0x400\n" \
"s_mov_b32 s85, 0x108000\n" \
"v_cvt_pk_bf16_f32 v254, v180, v181\n" \
"v_mfma_f32_16x16x32_bf16 a[88:91], v[108:111], v[60:63], 0\n" \
"buffer_load_dwordx4 v26, s[68:71], s85 offen lds\n" \
"v_cvt_pk_bf16_f32 v255, v182, v183\n" \
"v_mfma_f32_16x16x32_bf16 a[88:91], v[172:175], v[124:127], a[88:91]\n" \
"ds_read_b64_tr_b16 v[160:161], v35 offset:36864\n" \
"v_accvgpr_read_b32 v200, a16\n" \
"v_mfma_f32_16x16x32_bf16 a[92:95], v[112:115], v[60:63], 0\n" \
"ds_read_b64_tr_b16 v[162:163], v35 offset:37376\n" \
"v_accvgpr_read_b32 v201, a17\n" \
"v_mfma_f32_16x16x32_bf16 a[92:95], v[176:179], v[124:127], a[92:95]\n" \
"ds_read_b128 v[60:63], v28 offset:36864\n" \
"v_accvgpr_read_b32 v202, a18\n" \
"v_mfma_f32_16x16x32_bf16 a[124:127], v[112:115], v[64:67], 0\n" \
"ds_read_b128 v[124:127], v29 offset:36864\n" \
"v_accvgpr_read_b32 v203, a19\n" \
"v_mfma_f32_16x16x32_bf16 a[124:127], v[176:179], v[128:131], a[124:127]\n" \
"s_add_u32 m0, s30, 0x1000\n" \
"s_mov_b32 s84, 0x9100\n" \
"v_accvgpr_read_b32 v180, a48\n" \
"v_mfma_f32_16x16x32_bf16 a[120:123], v[108:111], v[64:67], 0\n" \
"buffer_load_dwordx4 v24, s[76:79], s84 offen lds\n" \
"v_accvgpr_read_b32 v181, a49\n" \
"v_mfma_f32_16x16x32_bf16 a[120:123], v[172:175], v[128:131], a[120:123]\n" \
"s_add_u32 m0, s30, 0x1400\n" \
"s_mov_b32 s85, 0xb500\n" \
"v_accvgpr_read_b32 v182, a50\n" \
"v_mfma_f32_16x16x32_bf16 a[116:119], v[104:107], v[64:67], 0\n" \
"buffer_load_dwordx4 v25, s[76:79], s85 offen lds\n" \
"v_accvgpr_read_b32 v183, a51\n" \
"v_mfma_f32_16x16x32_bf16 a[116:119], v[168:171], v[128:131], a[116:119]\n" \
"ds_read_b64_tr_b16 v[92:93], v34 offset:32768\n" \
"v_cvt_pk_bf16_f32 v200, v200, v201\n" \
"v_mfma_f32_16x16x32_bf16 a[112:115], v[100:103], v[64:67], 0\n" \
"ds_read_b64_tr_b16 v[94:95], v34 offset:33280\n" \
"v_cvt_pk_bf16_f32 v201, v202, v203\n" \
"v_mfma_f32_16x16x32_bf16 a[112:115], v[164:167], v[128:131], a[112:115]\n" \
"ds_read_b128 v[64:67], v28 offset:38912\n" \
"v_cvt_pk_bf16_f32 v202, v180, v181\n" \
"v_cvt_pk_bf16_f32 v203, v182, v183\n" \
"v_mfma_f32_16x16x32_bf16 a[16:19], v[100:103], v[52:55], 0\n" \
"ds_read_b128 v[128:131], v29 offset:38912\n" \
"v_accvgpr_read_b32 v204, a20\n" \
"v_mfma_f32_16x16x32_bf16 a[16:19], v[164:167], v[116:119], a[16:19]\n" \
"s_add_u32 m0, s31, 0x800\n" \
"s_mov_b32 s84, 0x110000\n" \
"v_accvgpr_read_b32 v205, a21\n" \
"v_accvgpr_read_b32 v206, a22\n" \
"v_accvgpr_read_b32 v207, a23\n" \
"v_accvgpr_read_b32 v180, a52\n" \
"v_accvgpr_read_b32 v181, a53\n" \
"v_accvgpr_read_b32 v182, a54\n" \
"v_accvgpr_read_b32 v183, a55\n" \
"v_cvt_pk_bf16_f32 v204, v204, v205\n" \
"v_cvt_pk_bf16_f32 v205, v206, v207\n" \
"v_cvt_pk_bf16_f32 v206, v180, v181\n" \
"v_cvt_pk_bf16_f32 v207, v182, v183\n" \
"v_mfma_f32_16x16x32_bf16 a[20:23], v[104:107], v[52:55], 0\n" \
"buffer_load_dwordx4 v27, s[68:71], s84 offen lds\n" \
"v_accvgpr_read_b32 v208, a24\n" \
"v_mfma_f32_16x16x32_bf16 a[20:23], v[168:171], v[116:119], a[20:23]\n" \
"s_add_u32 m0, s30, 0x1800\n" \
"s_mov_b32 s85, 0xd900\n" \
"v_accvgpr_read_b32 v209, a25\n" \
"v_accvgpr_read_b32 v210, a26\n" \
"v_accvgpr_read_b32 v211, a27\n" \
"v_accvgpr_read_b32 v180, a56\n" \
"v_accvgpr_read_b32 v181, a57\n" \
"v_accvgpr_read_b32 v182, a58\n" \
"v_accvgpr_read_b32 v183, a59\n" \
"v_cvt_pk_bf16_f32 v208, v208, v209\n" \
"v_cvt_pk_bf16_f32 v209, v210, v211\n" \
"v_cvt_pk_bf16_f32 v210, v180, v181\n" \
"v_cvt_pk_bf16_f32 v211, v182, v183\n" \
"v_mfma_f32_16x16x32_bf16 a[24:27], v[108:111], v[52:55], 0\n" \
"buffer_load_dwordx4 v24, s[76:79], s85 offen lds\n" \
"v_accvgpr_read_b32 v212, a28\n" \
"v_mfma_f32_16x16x32_bf16 a[24:27], v[172:175], v[116:119], a[24:27]\n" \
"ds_read_b64_tr_b16 v[156:157], v34 offset:36864\n" \
"v_accvgpr_read_b32 v213, a29\n" \
"v_accvgpr_read_b32 v214, a30\n" \
"v_accvgpr_read_b32 v215, a31\n" \
"v_accvgpr_read_b32 v180, a60\n" \
"v_accvgpr_read_b32 v181, a61\n" \
"v_accvgpr_read_b32 v182, a62\n" \
"v_accvgpr_read_b32 v183, a63\n" \
"v_cvt_pk_bf16_f32 v212, v212, v213\n" \
"v_cvt_pk_bf16_f32 v213, v214, v215\n" \
"v_cvt_pk_bf16_f32 v214, v180, v181\n" \
"v_cvt_pk_bf16_f32 v215, v182, v183\n" \
"v_mfma_f32_16x16x32_bf16 a[28:31], v[112:115], v[52:55], 0\n" \
"ds_read_b64_tr_b16 v[158:159], v34 offset:37376\n" \
"v_mfma_f32_16x16x32_bf16 a[28:31], v[176:179], v[116:119], a[28:31]\n" \
"ds_read_b128 v[52:55], v28 offset:32768\n" \
"v_mfma_f32_16x16x32_bf16 a[60:63], v[112:115], v[56:59], 0\n" \
"ds_read_b128 v[116:119], v29 offset:32768\n" \
"v_mfma_f32_16x16x32_bf16 a[60:63], v[176:179], v[120:123], a[60:63]\n" \
"v_mfma_f32_16x16x32_bf16 a[56:59], v[108:111], v[56:59], 0\n" \
"v_mfma_f32_16x16x32_bf16 a[56:59], v[172:175], v[120:123], a[56:59]\n" \
"v_mfma_f32_16x16x32_bf16 a[52:55], v[104:107], v[56:59], 0\n" \
"v_mfma_f32_16x16x32_bf16 a[52:55], v[168:171], v[120:123], a[52:55]\n" \
"v_mfma_f32_16x16x32_bf16 a[48:51], v[100:103], v[56:59], 0\n" \
"v_mfma_f32_16x16x32_bf16 a[48:51], v[164:167], v[120:123], a[48:51]\n" \
"ds_read_b128 v[56:59], v28 offset:34816\n" \
"ds_read_b128 v[120:123], v29 offset:34816\n" \
"s_waitcnt lgkmcnt(15)\n" \
"v_mfma_f32_16x16x32_bf16 a[0:3], v[84:87], v[68:71], a[0:3]\n" \
"s_add_u32 m0, s30, 0x1c00\n" \
"s_mov_b32 s85, 0xfd00\n" \
"v_mfma_f32_16x16x32_bf16 a[0:3], v[148:151], v[132:135], a[0:3]\n" \
"buffer_load_dwordx4 v25, s[76:79], s85 offen lds\n" \
"v_mfma_f32_16x16x32_bf16 a[4:7], v[88:91], v[68:71], a[4:7]\n" \
"ds_read_b64_tr_b16 v[100:101], v36 offset:32768\n" \
"v_mov_b32_e32 v11, 0\n" \
"v_mfma_f32_16x16x32_bf16 a[4:7], v[152:155], v[132:135], a[4:7]\n" \
"s_add_u32 m0, s31, 0xc00\n" \
"s_mov_b32 s84, 0x118000\n" \
"v_mfma_f32_16x16x32_bf16 a[36:39], v[96:99], v[68:71], a[36:39]\n" \
"buffer_load_dwordx4 v27, s[68:71], s84 offen lds\n" \
"s_waitcnt lgkmcnt(13)\n" \
"v_mfma_f32_16x16x32_bf16 a[36:39], v[160:163], v[132:135], a[36:39]\n" \
"ds_read_b64_tr_b16 v[102:103], v36 offset:33792\n" \
"s_and_b32 s6, s2, 7\n" \
"s_waitcnt lgkmcnt(10)\n" \
"v_mfma_f32_16x16x32_bf16 a[32:35], v[68:71], v[92:95], a[32:35]\n" \
"ds_read_b64_tr_b16 v[164:165], v36 offset:40960\n" \
"s_lshl_b32 s7, s3, 1\n" \
"s_waitcnt lgkmcnt(7)\n" \
"v_mfma_f32_16x16x32_bf16 a[32:35], v[132:135], v[156:159], a[32:35]\n" \
"ds_read_b64_tr_b16 v[166:167], v36 offset:41984\n" \
"s_lshr_b32 s4, s2, 5\n" \
"v_mfma_f32_16x16x32_bf16 a[96:99], v[72:75], v[92:95], a[96:99]\n" \
"ds_read_b64_tr_b16 v[104:105], v37 offset:32768\n" \
"s_lshl_b32 s5, s6, 2\n" \
"v_mfma_f32_16x16x32_bf16 a[96:99], v[136:139], v[156:159], a[96:99]\n" \
"ds_read_b64_tr_b16 v[106:107], v37 offset:33792\n" \
"s_bfe_u32 s8, s2, 0x20003\n" \
"v_mfma_f32_16x16x32_bf16 a[100:103], v[96:99], v[72:75], a[100:103]\n" \
"ds_read_b64_tr_b16 v[168:169], v37 offset:40960\n" \
"s_and_b32 s7, s7, 4\n" \
"v_mfma_f32_16x16x32_bf16 a[100:103], v[160:163], v[136:139], a[100:103]\n" \
"s_add_u32 s89, s41, 0x3000\n" \
"buffer_store_dwordx4 v[192:195], v20, s[80:83], s89 offen offset:0 sc1\n" \
"v_mfma_f32_16x16x32_bf16 a[68:71], v[88:91], v[72:75], a[68:71]\n" \
"ds_read_b64_tr_b16 v[170:171], v37 offset:41984\n" \
".fill (L_pb + 69344 - .) / 4, 4, 0xBF800000\n" \
"L_pp17:\n" \
"s_cbranch_execz L_pret\n" \
"s_and_b32 s9, s3, 1\n" \
"v_mfma_f32_16x16x32_bf16 a[68:71], v[152:155], v[136:139], a[68:71]\n" \
"ds_read_b64_tr_b16 v[108:109], v38 offset:32768\n" \
"s_or_b32 s13, s5, s8\n" \
"v_mfma_f32_16x16x32_bf16 a[64:67], v[84:87], v[72:75], a[64:67]\n" \
"ds_read_b64_tr_b16 v[110:111], v38 offset:33792\n" \
"s_or_b32 s9, s7, s9\n" \
"v_mfma_f32_16x16x32_bf16 a[64:67], v[148:151], v[136:139], a[64:67]\n" \
"s_mul_i32 s7, s4, 0xa00\n" \
"s_add_i32 s7, s7, 0x5bc1000\n" \
"v_mfma_f32_16x16x32_bf16 a[128:131], v[84:87], v[76:79], a[128:131]\n" \
"s_add_u32 s88, s41, 0x3000\n" \
"buffer_store_dwordx4 v[196:199], v20, s[80:83], s88 offen offset:1024 sc1\n" \
"v_mfma_f32_16x16x32_bf16 a[128:131], v[148:151], v[140:143], a[128:131]\n" \
"ds_read_b64_tr_b16 v[172:173], v38 offset:40960\n" \
"s_lshl_b32 s10, s13, 2\n" \
"v_mfma_f32_16x16x32_bf16 a[132:135], v[88:91], v[76:79], a[132:135]\n" \
"ds_read_b64_tr_b16 v[174:175], v38 offset:41984\n" \
"s_or_b32 s10, s10, s7\n" \
"v_mfma_f32_16x16x32_bf16 a[132:135], v[152:155], v[140:143], a[132:135]\n" \
"ds_read_b64_tr_b16 v[112:113], v39 offset:32768\n" \
"v_writelane_b32 v11, s10, 0\n" \
"v_mfma_f32_16x16x32_bf16 a[164:167], v[96:99], v[76:79], a[164:167]\n" \
"s_lshl_b32 s14, s4, 3\n" \
"v_writelane_b32 v11, s7, 1\n" \
"v_mfma_f32_16x16x32_bf16 a[164:167], v[160:163], v[140:143], a[164:167]\n" \
"ds_read_b64_tr_b16 v[114:115], v39 offset:33792\n" \
"s_or_b32 s7, s14, s6\n" \
"v_mfma_f32_16x16x32_bf16 a[160:163], v[76:79], v[92:95], a[160:163]\n" \
"s_add_u32 s89, s41, 0x3000\n" \
"buffer_store_dwordx4 a[44:47], v20, s[80:83], s89 offen offset:2048 sc1\n" \
"v_mfma_f32_16x16x32_bf16 a[160:163], v[140:143], v[156:159], a[160:163]\n" \
"ds_read_b64_tr_b16 v[176:177], v39 offset:40960\n" \
"s_lshl_b32 s7, s7, 10\n" \
"v_mfma_f32_16x16x32_bf16 a[224:227], v[80:83], v[92:95], a[224:227]\n" \
"ds_read_b64_tr_b16 v[178:179], v39 offset:41984\n" \
"s_lshl_b32 s10, s3, 8\n" \
"v_mfma_f32_16x16x32_bf16 a[224:227], v[144:147], v[156:159], a[224:227]\n" \
"s_or_b32 s7, s7, s10\n" \
"s_or_b32 s10, s7, 0x5c00200\n" \
"v_mfma_f32_16x16x32_bf16 a[228:231], v[96:99], v[80:83], a[228:231]\n" \
"v_writelane_b32 v11, s10, 2\n" \
"s_lshl_b32 s10, s8, 2\n" \
"v_mfma_f32_16x16x32_bf16 a[228:231], v[160:163], v[144:147], a[228:231]\n" \
"s_add_u32 s88, s41, 0x3000\n" \
"buffer_store_dwordx4 v[252:255], v20, s[80:83], s88 offen offset:3072 sc1\n" \
"v_mfma_f32_16x16x32_bf16 a[196:199], v[88:91], v[80:83], a[196:199]\n" \
"s_or_b32 s11, s10, s7\n" \
"s_or_b32 s11, s11, 0x5c00000\n" \
"v_mfma_f32_16x16x32_bf16 a[196:199], v[152:155], v[144:147], a[196:199]\n" \
"v_writelane_b32 v11, s11, 3\n" \
"s_or_b32 s7, s7, 0x5c00080\n" \
"v_mfma_f32_16x16x32_bf16 a[192:195], v[84:87], v[80:83], a[192:195]\n" \
"v_writelane_b32 v11, s7, 4\n" \
"s_or_b32 s7, s7, s10\n" \
"v_mfma_f32_16x16x32_bf16 a[192:195], v[148:151], v[144:147], a[192:195]\n" \
"s_add_i32 s10, s3, s2\n" \
"v_writelane_b32 v11, s7, 5\n" \
"v_mfma_f32_16x16x32_bf16 a[8:11], v[88:91], v[64:67], a[8:11]\n" \
"s_mov_b32 s7, 2.0\n" \
"s_mov_b32 s11, 0x5bc0000\n" \
"v_mfma_f32_16x16x32_bf16 a[8:11], v[152:155], v[128:131], a[8:11]\n" \
"s_cmp_lg_u32 s10, 0\n" \
"s_cselect_b32 s10, s7, s11\n" \
"v_mfma_f32_16x16x32_bf16 a[40:43], v[96:99], v[64:67], a[40:43]\n" \
"s_lshr_b32 s15, s2, 3\n" \
"v_writelane_b32 v11, s10, 53\n" \
"v_mfma_f32_16x16x32_bf16 a[40:43], v[160:163], v[128:131], a[40:43]\n" \
"s_lshl_b32 s10, s6, 5\n" \
"s_min_u32 s16, s3, 2\n" \
"v_mfma_f32_16x16x32_bf16 a[12:15], v[64:67], v[92:95], a[12:15]\n" \
"s_mul_i32 s12, s8, 0x120\n" \
"v_mfma_f32_16x16x32_bf16 a[12:15], v[128:131], v[156:159], a[12:15]\n" \
"s_waitcnt vmcnt(4) lgkmcnt(0)\n" \
"s_barrier\n" \
"s_nop 0\n" \
"v_mfma_f32_16x16x32_bf16 a[144:147], v[100:103], v[68:71], a[144:147]\n" \
"s_add_u32 m0, s40, 0x8000\n" \
"s_add_u32 s85, s39, 0x6000\n" \
"v_mfma_f32_16x16x32_bf16 a[144:147], v[164:167], v[132:135], a[144:147]\n" \
"buffer_load_dwordx4 v20, s[80:83], s85 offen sc1 lds\n" \
"v_mfma_f32_16x16x32_bf16 a[148:151], v[104:107], v[68:71], a[148:151]\n" \
"ds_read_b64_tr_b16 v[84:85], v32 offset:0\n" \
"v_mfma_f32_16x16x32_bf16 a[148:151], v[168:171], v[132:135], a[148:151]\n" \
"ds_read_b64_tr_b16 v[86:87], v32 offset:512\n" \
"v_mfma_f32_16x16x32_bf16 a[200:203], v[108:111], v[68:71], a[200:203]\n" \
"s_add_u32 m0, s40, 0x9000\n" \
"s_add_u32 s84, s39, 0x7000\n" \
"v_mfma_f32_16x16x32_bf16 a[200:203], v[172:175], v[132:135], a[200:203]\n" \
"buffer_load_dwordx4 v20, s[80:83], s84 offen sc1 lds\n" \
"v_mfma_f32_16x16x32_bf16 a[204:207], v[112:115], v[68:71], a[204:207]\n" \
"ds_read_b64_tr_b16 v[148:149], v32 offset:4096\n" \
"v_mfma_f32_16x16x32_bf16 a[204:207], v[176:179], v[132:135], a[204:207]\n" \
"ds_read_b128 v[68:71], v28 offset:8192\n" \
"v_mfma_f32_16x16x32_bf16 a[236:239], v[112:115], v[72:75], a[236:239]\n" \
"ds_read_b128 v[132:135], v29 offset:8192\n" \
"v_mfma_f32_16x16x32_bf16 a[236:239], v[176:179], v[136:139], a[236:239]\n" \
"s_add_u32 m0, s40, 0xb000\n" \
"s_mov_b32 s85, 0x180\n" \
"v_mfma_f32_16x16x32_bf16 a[232:235], v[108:111], v[72:75], a[232:235]\n" \
"buffer_load_dwordx4 v21, s[76:79], s85 offen lds\n" \
"v_mfma_f32_16x16x32_bf16 a[232:235], v[172:175], v[136:139], a[232:235]\n" \
"ds_read_b64_tr_b16 v[150:151], v32 offset:4608\n" \
"v_mfma_f32_16x16x32_bf16 a[180:183], v[104:107], v[72:75], a[180:183]\n" \
"s_add_u32 m0, s30, 0x8000\n" \
"s_mov_b32 s84, 0x180\n" \
"v_mfma_f32_16x16x32_bf16 a[180:183], v[168:171], v[136:139], a[180:183]\n" \
"buffer_load_dwordx4 v24, s[76:79], s84 offen lds\n" \
"v_mfma_f32_16x16x32_bf16 a[176:179], v[100:103], v[72:75], a[176:179]\n" \
"ds_read_b64_tr_b16 v[88:89], v33 offset:0\n" \
"v_mfma_f32_16x16x32_bf16 a[176:179], v[164:167], v[136:139], a[176:179]\n" \
"ds_read_b128 v[72:75], v28 offset:10240\n" \
"v_mfma_f32_16x16x32_bf16 a[72:75], v[100:103], v[76:79], a[72:75]\n" \
"ds_read_b128 v[136:139], v29 offset:10240\n" \
"v_mfma_f32_16x16x32_bf16 a[72:75], v[164:167], v[140:143], a[72:75]\n" \
"s_add_u32 m0, s30, 0x8400\n" \
"s_mov_b32 s85, 0x2580\n" \
"v_mfma_f32_16x16x32_bf16 a[76:79], v[104:107], v[76:79], a[76:79]\n" \
"buffer_load_dwordx4 v25, s[76:79], s85 offen lds\n" \
"v_mfma_f32_16x16x32_bf16 a[76:79], v[168:171], v[140:143], a[76:79]\n" \
"ds_read_b64_tr_b16 v[90:91], v33 offset:512\n" \
"v_mfma_f32_16x16x32_bf16 a[104:107], v[108:111], v[76:79], a[104:107]\n" \
"ds_read_b64_tr_b16 v[152:153], v33 offset:4096\n" \
"v_mfma_f32_16x16x32_bf16 a[104:107], v[172:175], v[140:143], a[104:107]\n" \
"s_add_u32 m0, s31, 0x8000\n" \
"s_mov_b32 s84, 0x180000\n" \
"v_mfma_f32_16x16x32_bf16 a[108:111], v[112:115], v[76:79], a[108:111]\n" \
"buffer_load_dwordx4 v26, s[68:71], s84 offen lds\n" \
"v_mfma_f32_16x16x32_bf16 a[108:111], v[176:179], v[140:143], a[108:111]\n" \
"ds_read_b128 v[76:79], v28 offset:12288\n" \
"v_mfma_f32_16x16x32_bf16 a[172:175], v[112:115], v[80:83], a[172:175]\n" \
"ds_read_b128 v[140:143], v29 offset:12288\n" \
"v_mfma_f32_16x16x32_bf16 a[172:175], v[176:179], v[144:147], a[172:175]\n" \
"s_add_u32 m0, s30, 0x8800\n" \
"s_mov_b32 s85, 0x4980\n" \
"v_mfma_f32_16x16x32_bf16 a[168:171], v[108:111], v[80:83], a[168:171]\n" \
"buffer_load_dwordx4 v24, s[76:79], s85 offen lds\n" \
"v_mfma_f32_16x16x32_bf16 a[168:171], v[172:175], v[144:147], a[168:171]\n" \
"ds_read_b64_tr_b16 v[154:155], v33 offset:4608\n" \
"v_mfma_f32_16x16x32_bf16 a[140:143], v[104:107], v[80:83], a[140:143]\n" \
"ds_read_b64_tr_b16 v[96:97], v35 offset:0\n" \
"v_mfma_f32_16x16x32_bf16 a[140:143], v[168:171], v[144:147], a[140:143]\n" \
"ds_read_b64_tr_b16 v[98:99], v35 offset:512\n" \
"v_mfma_f32_16x16x32_bf16 a[136:139], v[100:103], v[80:83], a[136:139]\n" \
"s_add_u32 s11, s22, s12\n" \
"s_addc_u32 s17, s23, 0\n" \
"v_mfma_f32_16x16x32_bf16 a[136:139], v[164:167], v[144:147], a[136:139]\n" \
"ds_read_b128 v[80:83], v28 offset:14336\n" \
"v_mfma_f32_16x16x32_bf16 a[80:83], v[100:103], v[60:63], a[80:83]\n" \
"ds_read_b128 v[144:147], v29 offset:14336\n" \
"v_mfma_f32_16x16x32_bf16 a[80:83], v[164:167], v[124:127], a[80:83]\n" \
"s_add_u32 m0, s30, 0x8c00\n" \
"s_mov_b32 s84, 0x6d80\n" \
"v_mfma_f32_16x16x32_bf16 a[84:87], v[104:107], v[60:63], a[84:87]\n" \
"buffer_load_dwordx4 v25, s[76:79], s84 offen lds\n" \
"v_mfma_f32_16x16x32_bf16 a[84:87], v[168:171], v[124:127], a[84:87]\n" \
"s_add_u32 m0, s31, 0x8400\n" \
"s_mov_b32 s85, 0x188000\n" \
"v_mfma_f32_16x16x32_bf16 a[88:91], v[108:111], v[60:63], a[88:91]\n" \
"buffer_load_dwordx4 v26, s[68:71], s85 offen lds\n" \
"v_mfma_f32_16x16x32_bf16 a[88:91], v[172:175], v[124:127], a[88:91]\n" \
"ds_read_b64_tr_b16 v[160:161], v35 offset:4096\n" \
"v_mfma_f32_16x16x32_bf16 a[92:95], v[112:115], v[60:63], a[92:95]\n" \
"ds_read_b64_tr_b16 v[162:163], v35 offset:4608\n" \
"v_mfma_f32_16x16x32_bf16 a[92:95], v[176:179], v[124:127], a[92:95]\n" \
"ds_read_b128 v[60:63], v28 offset:4096\n" \
"v_mfma_f32_16x16x32_bf16 a[124:127], v[112:115], v[64:67], a[124:127]\n" \
"ds_read_b128 v[124:127], v29 offset:4096\n" \
"v_mfma_f32_16x16x32_bf16 a[124:127], v[176:179], v[128:131], a[124:127]\n" \
"s_add_u32 m0, s30, 0x9000\n" \
"s_mov_b32 s84, 0x9180\n" \
"v_mfma_f32_16x16x32_bf16 a[120:123], v[108:111], v[64:67], a[120:123]\n" \
"buffer_load_dwordx4 v24, s[76:79], s84 offen lds\n" \
"v_mfma_f32_16x16x32_bf16 a[120:123], v[172:175], v[128:131], a[120:123]\n" \
"s_add_u32 m0, s30, 0x9400\n" \
"s_mov_b32 s85, 0xb580\n" \
"v_mfma_f32_16x16x32_bf16 a[116:119], v[104:107], v[64:67], a[116:119]\n" \
"buffer_load_dwordx4 v25, s[76:79], s85 offen lds\n" \
"v_mfma_f32_16x16x32_bf16 a[116:119], v[168:171], v[128:131], a[116:119]\n" \
"ds_read_b64_tr_b16 v[92:93], v34 offset:0\n" \
"v_mfma_f32_16x16x32_bf16 a[112:115], v[100:103], v[64:67], a[112:115]\n" \
"ds_read_b64_tr_b16 v[94:95], v34 offset:512\n" \
"v_mfma_f32_16x16x32_bf16 a[112:115], v[164:167], v[128:131], a[112:115]\n" \
"ds_read_b128 v[64:67], v28 offset:6144\n" \
"v_mfma_f32_16x16x32_bf16 a[16:19], v[100:103], v[52:55], a[16:19]\n" \
"ds_read_b128 v[128:131], v29 offset:6144\n" \
"v_mfma_f32_16x16x32_bf16 a[16:19], v[164:167], v[116:119], a[16:19]\n" \
"s_add_u32 m0, s31, 0x8800\n" \
"s_mov_b32 s84, 0x190000\n" \
"v_mfma_f32_16x16x32_bf16 a[20:23], v[104:107], v[52:55], a[20:23]\n" \
"buffer_load_dwordx4 v27, s[68:71], s84 offen lds\n" \
"v_mfma_f32_16x16x32_bf16 a[20:23], v[168:171], v[116:119], a[20:23]\n" \
"s_add_u32 m0, s30, 0x9800\n" \
"s_mov_b32 s85, 0xd980\n" \
"v_mfma_f32_16x16x32_bf16 a[24:27], v[108:111], v[52:55], a[24:27]\n" \
"buffer_load_dwordx4 v24, s[76:79], s85 offen lds\n" \
"v_mfma_f32_16x16x32_bf16 a[24:27], v[172:175], v[116:119], a[24:27]\n" \
"ds_read_b64_tr_b16 v[156:157], v34 offset:4096\n" \
"v_mfma_f32_16x16x32_bf16 a[28:31], v[112:115], v[52:55], a[28:31]\n" \
"ds_read_b64_tr_b16 v[158:159], v34 offset:4608\n" \
"v_mfma_f32_16x16x32_bf16 a[28:31], v[176:179], v[116:119], a[28:31]\n" \
"ds_read_b128 v[52:55], v28 offset:0\n" \
"v_mfma_f32_16x16x32_bf16 a[60:63], v[112:115], v[56:59], a[60:63]\n" \
"ds_read_b128 v[116:119], v29 offset:0\n" \
"v_mfma_f32_16x16x32_bf16 a[60:63], v[176:179], v[120:123], a[60:63]\n" \
"s_add_u32 s18, s24, s12\n" \
"v_writelane_b32 v11, s11, 9\n" \
"s_addc_u32 s19, s25, 0\n" \
"v_mfma_f32_16x16x32_bf16 a[56:59], v[108:111], v[56:59], a[56:59]\n" \
"s_and_b32 s20, s17, 0xffff\n" \
"v_writelane_b32 v11, s20, 10\n" \
"v_mfma_f32_16x16x32_bf16 a[56:59], v[172:175], v[120:123], a[56:59]\n" \
"s_and_b32 s20, s19, 0xffff\n" \
"v_writelane_b32 v11, s18, 11\n" \
"v_mfma_f32_16x16x32_bf16 a[52:55], v[104:107], v[56:59], a[52:55]\n" \
"s_cmpk_lt_u32 s3, 3\n" \
"s_cselect_b32 s11, s11, s18\n" \
"v_mfma_f32_16x16x32_bf16 a[52:55], v[168:171], v[120:123], a[52:55]\n" \
"s_movk_i32 s18, 0xfff\n" \
"v_writelane_b32 v11, s20, 12\n" \
"v_mfma_f32_16x16x32_bf16 a[48:51], v[100:103], v[56:59], a[48:51]\n" \
"s_and_b32 s20, s29, 0xffff\n" \
"v_writelane_b32 v11, s28, 13\n" \
"v_mfma_f32_16x16x32_bf16 a[48:51], v[164:167], v[120:123], a[48:51]\n" \
"ds_read_b128 v[56:59], v28 offset:2048\n" \
"ds_read_b128 v[120:123], v29 offset:2048\n" \
"s_waitcnt lgkmcnt(15)\n" \
"v_mfma_f32_16x16x32_bf16 a[0:3], v[84:87], v[68:71], a[0:3]\n" \
"s_add_u32 m0, s30, 0x9c00\n" \
"s_mov_b32 s85, 0xfd80\n" \
"v_mfma_f32_16x16x32_bf16 a[0:3], v[148:151], v[132:135], a[0:3]\n" \
"buffer_load_dwordx4 v25, s[76:79], s85 offen lds\n" \
"v_mfma_f32_16x16x32_bf16 a[4:7], v[88:91], v[68:71], a[4:7]\n" \
"ds_read_b64_tr_b16 v[100:101], v36 offset:0\n" \
"v_writelane_b32 v11, s20, 14\n" \
"v_mfma_f32_16x16x32_bf16 a[4:7], v[152:155], v[132:135], a[4:7]\n" \
"s_add_u32 m0, s31, 0x8c00\n" \
"s_mov_b32 s84, 0x198000\n" \
"v_mfma_f32_16x16x32_bf16 a[36:39], v[96:99], v[68:71], a[36:39]\n" \
"buffer_load_dwordx4 v27, s[68:71], s84 offen lds\n" \
"s_waitcnt lgkmcnt(13)\n" \
"v_mfma_f32_16x16x32_bf16 a[36:39], v[160:163], v[132:135], a[36:39]\n" \
"ds_read_b64_tr_b16 v[102:103], v36 offset:1024\n" \
"v_writelane_b32 v11, s11, 15\n" \
"s_waitcnt lgkmcnt(10)\n" \
"v_mfma_f32_16x16x32_bf16 a[32:35], v[68:71], v[92:95], a[32:35]\n" \
"ds_read_b64_tr_b16 v[164:165], v36 offset:8192\n" \
"s_waitcnt lgkmcnt(7)\n" \
"s_nop 0\n" \
"v_mfma_f32_16x16x32_bf16 a[32:35], v[132:135], v[156:159], a[32:35]\n" \
"ds_read_b64_tr_b16 v[166:167], v36 offset:9216\n" \
"v_mfma_f32_16x16x32_bf16 a[96:99], v[72:75], v[92:95], a[96:99]\n" \
"ds_read_b64_tr_b16 v[104:105], v37 offset:0\n" \
"v_mfma_f32_16x16x32_bf16 a[96:99], v[136:139], v[156:159], a[96:99]\n" \
"ds_read_b64_tr_b16 v[106:107], v37 offset:1024\n" \
"v_mfma_f32_16x16x32_bf16 a[100:103], v[96:99], v[72:75], a[100:103]\n" \
"ds_read_b64_tr_b16 v[168:169], v37 offset:8192\n" \
"v_mfma_f32_16x16x32_bf16 a[100:103], v[160:163], v[136:139], a[100:103]\n" \
"s_add_u32 s89, s41, 0x1000\n" \
"buffer_store_dwordx4 v[200:203], v20, s[80:83], s89 offen offset:0 sc1\n" \
"v_mfma_f32_16x16x32_bf16 a[68:71], v[88:91], v[72:75], a[68:71]\n" \
"ds_read_b64_tr_b16 v[170:171], v37 offset:9216\n" \
"v_mfma_f32_16x16x32_bf16 a[68:71], v[152:155], v[136:139], a[68:71]\n" \
"ds_read_b64_tr_b16 v[108:109], v38 offset:0\n" \
"v_mfma_f32_16x16x32_bf16 a[64:67], v[84:87], v[72:75], a[64:67]\n" \
"ds_read_b64_tr_b16 v[110:111], v38 offset:1024\n" \
"v_mfma_f32_16x16x32_bf16 a[64:67], v[148:151], v[136:139], a[64:67]\n" \
"s_cmpk_lt_u32 s3, 3\n" \
"s_cselect_b32 s11, s17, s19\n" \
"v_mfma_f32_16x16x32_bf16 a[128:131], v[84:87], v[76:79], a[128:131]\n" \
"s_add_u32 s88, s41, 0x1000\n" \
"buffer_store_dwordx4 v[204:207], v20, s[80:83], s88 offen offset:1024 sc1\n" \
"v_mfma_f32_16x16x32_bf16 a[128:131], v[148:151], v[140:143], a[128:131]\n" \
"ds_read_b64_tr_b16 v[172:173], v38 offset:8192\n" \
"s_and_b32 s11, s11, 0xffff\n" \
"v_mfma_f32_16x16x32_bf16 a[132:135], v[88:91], v[76:79], a[132:135]\n" \
"ds_read_b64_tr_b16 v[174:175], v38 offset:9216\n" \
"v_writelane_b32 v11, s11, 16\n" \
"v_mfma_f32_16x16x32_bf16 a[132:135], v[152:155], v[140:143], a[132:135]\n" \
"ds_read_b64_tr_b16 v[112:113], v39 offset:0\n" \
"s_mul_i32 s11, s6, 0x48\n" \
"v_mfma_f32_16x16x32_bf16 a[164:167], v[96:99], v[76:79], a[164:167]\n" \
"s_add_i32 s17, s11, 0x47\n" \
"v_mfma_f32_16x16x32_bf16 a[164:167], v[160:163], v[140:143], a[164:167]\n" \
"ds_read_b64_tr_b16 v[114:115], v39 offset:1024\n" \
"v_mfma_f32_16x16x32_bf16 a[160:163], v[76:79], v[92:95], a[160:163]\n" \
"s_add_u32 s89, s41, 0x1000\n" \
"buffer_store_dwordx4 v[208:211], v20, s[80:83], s89 offen offset:2048 sc1\n" \
"v_mfma_f32_16x16x32_bf16 a[160:163], v[140:143], v[156:159], a[160:163]\n" \
"ds_read_b64_tr_b16 v[176:177], v39 offset:8192\n" \
"v_mfma_f32_16x16x32_bf16 a[224:227], v[80:83], v[92:95], a[224:227]\n" \
"ds_read_b64_tr_b16 v[178:179], v39 offset:9216\n" \
"v_mfma_f32_16x16x32_bf16 a[224:227], v[144:147], v[156:159], a[224:227]\n" \
"s_cmpk_lt_u32 s3, 3\n" \
"s_cselect_b32 s17, s17, s18\n" \
"v_mfma_f32_16x16x32_bf16 a[228:231], v[96:99], v[80:83], a[228:231]\n" \
"s_mulk_i32 s17, 0x480\n" \
"s_addk_i32 s17, 0x120\n" \
"v_mfma_f32_16x16x32_bf16 a[228:231], v[160:163], v[144:147], a[228:231]\n" \
"s_add_u32 s88, s41, 0x1000\n" \
"buffer_store_dwordx4 v[212:215], v20, s[80:83], s88 offen offset:3072 sc1\n" \
"v_mfma_f32_16x16x32_bf16 a[196:199], v[88:91], v[80:83], a[196:199]\n" \
"v_writelane_b32 v11, s17, 17\n" \
"s_lshl_b32 s17, s3, 5\n" \
"v_mfma_f32_16x16x32_bf16 a[196:199], v[152:155], v[144:147], a[196:199]\n" \
"s_add_i32 s11, s11, s17\n" \
"s_mul_i32 s17, s11, 0x480\n" \
"v_mfma_f32_16x16x32_bf16 a[192:195], v[84:87], v[80:83], a[192:195]\n" \
"s_mul_i32 s11, s13, 0x24000\n" \
"s_add_i32 s18, s11, 0x1b000\n" \
"v_mfma_f32_16x16x32_bf16 a[192:195], v[148:151], v[144:147], a[192:195]\n" \
"s_cmpk_lt_u32 s3, 3\n" \
"s_cselect_b32 s17, s17, s18\n" \
"v_mfma_f32_16x16x32_bf16 a[8:11], v[88:91], v[64:67], a[8:11]\n" \
"s_mul_i32 s19, s3, 0x9000\n" \
"v_writelane_b32 v11, s17, 18\n" \
"v_mfma_f32_16x16x32_bf16 a[8:11], v[152:155], v[128:131], a[8:11]\n" \
"s_mul_i32 s17, s16, 0x2400\n" \
"s_mul_i32 s16, s16, 0x9000\n" \
"v_mfma_f32_16x16x32_bf16 a[40:43], v[96:99], v[64:67], a[40:43]\n" \
"s_bitset1_b32 s17, 17\n" \
"v_writelane_b32 v11, s17, 19\n" \
"v_mfma_f32_16x16x32_bf16 a[40:43], v[160:163], v[128:131], a[40:43]\n" \
"s_add_i32 s16, s11, s16\n" \
"v_writelane_b32 v11, s16, 25\n" \
"v_mfma_f32_16x16x32_bf16 a[12:15], v[64:67], v[92:95], a[12:15]\n" \
"s_mul_i32 s16, s3, 0x2400\n" \
"s_add_i32 s17, s16, 0x6000\n" \
"v_mfma_f32_16x16x32_bf16 a[12:15], v[128:131], v[156:159], a[12:15]\n" \
"s_waitcnt vmcnt(4) lgkmcnt(0)\n" \
"s_barrier\n" \
"v_mfma_f32_16x16x32_bf16 a[144:147], v[100:103], v[68:71], a[144:147]\n" \
"s_add_u32 m0, s40, 0x0\n" \
"s_nop 0\n" \
"s_add_u32 s85, s39, 0x8000\n" \
"v_mfma_f32_16x16x32_bf16 a[144:147], v[164:167], v[132:135], a[144:147]\n" \
"buffer_load_dwordx4 v20, s[80:83], s85 offen sc1 lds\n" \
"v_mfma_f32_16x16x32_bf16 a[148:151], v[104:107], v[68:71], a[148:151]\n" \
"ds_read_b64_tr_b16 v[84:85], v32 offset:32768\n" \
"v_writelane_b32 v11, s17, 20\n" \
"v_mfma_f32_16x16x32_bf16 a[148:151], v[168:171], v[132:135], a[148:151]\n" \
"ds_read_b64_tr_b16 v[86:87], v32 offset:33280\n" \
"s_or_b32 s17, s16, 0x10000\n" \
"v_mfma_f32_16x16x32_bf16 a[200:203], v[108:111], v[68:71], a[200:203]\n" \
"s_add_u32 m0, s40, 0x1000\n" \
"s_add_u32 s84, s39, 0x9000\n" \
"v_mfma_f32_16x16x32_bf16 a[200:203], v[172:175], v[132:135], a[200:203]\n" \
"buffer_load_dwordx4 v20, s[80:83], s84 offen sc1 lds\n" \
"v_mfma_f32_16x16x32_bf16 a[204:207], v[112:115], v[68:71], a[204:207]\n" \
"ds_read_b64_tr_b16 v[148:149], v32 offset:36864\n" \
"s_add_i32 s18, s16, 0x19000\n" \
"v_mfma_f32_16x16x32_bf16 a[204:207], v[176:179], v[132:135], a[204:207]\n" \
"ds_read_b128 v[68:71], v28 offset:40960\n" \
"v_writelane_b32 v11, s17, 21\n" \
"v_mfma_f32_16x16x32_bf16 a[236:239], v[112:115], v[72:75], a[236:239]\n" \
"ds_read_b128 v[132:135], v29 offset:40960\n" \
"s_mov_b32 s17, 0\n" \
"v_mfma_f32_16x16x32_bf16 a[236:239], v[176:179], v[136:139], a[236:239]\n" \
"s_add_u32 m0, s40, 0x3000\n" \
"s_mov_b32 s85, 0x200\n" \
"v_mfma_f32_16x16x32_bf16 a[232:235], v[108:111], v[72:75], a[232:235]\n" \
"buffer_load_dwordx4 v21, s[76:79], s85 offen lds\n" \
"v_mfma_f32_16x16x32_bf16 a[232:235], v[172:175], v[136:139], a[232:235]\n" \
"ds_read_b64_tr_b16 v[150:151], v32 offset:37376\n" \
"v_mfma_f32_16x16x32_bf16 a[180:183], v[104:107], v[72:75], a[180:183]\n" \
"s_add_u32 m0, s30, 0x0\n" \
"s_mov_b32 s84, 0x200\n" \
"v_mfma_f32_16x16x32_bf16 a[180:183], v[168:171], v[136:139], a[180:183]\n" \
"buffer_load_dwordx4 v24, s[76:79], s84 offen lds\n" \
"v_mfma_f32_16x16x32_bf16 a[176:179], v[100:103], v[72:75], a[176:179]\n" \
"ds_read_b64_tr_b16 v[88:89], v33 offset:32768\n" \
"v_mfma_f32_16x16x32_bf16 a[176:179], v[164:167], v[136:139], a[176:179]\n" \
"ds_read_b128 v[72:75], v28 offset:43008\n" \
"v_mfma_f32_16x16x32_bf16 a[72:75], v[100:103], v[76:79], a[72:75]\n" \
"ds_read_b128 v[136:139], v29 offset:43008\n" \
"v_mfma_f32_16x16x32_bf16 a[72:75], v[164:167], v[140:143], a[72:75]\n" \
"s_add_u32 m0, s30, 0x400\n" \
"s_mov_b32 s85, 0x2600\n" \
"v_mfma_f32_16x16x32_bf16 a[76:79], v[104:107], v[76:79], a[76:79]\n" \
"buffer_load_dwordx4 v25, s[76:79], s85 offen lds\n" \
"v_mfma_f32_16x16x32_bf16 a[76:79], v[168:171], v[140:143], a[76:79]\n" \
"ds_read_b64_tr_b16 v[90:91], v33 offset:33280\n" \
"v_mfma_f32_16x16x32_bf16 a[104:107], v[108:111], v[76:79], a[104:107]\n" \
"ds_read_b64_tr_b16 v[152:153], v33 offset:36864\n" \
"v_mfma_f32_16x16x32_bf16 a[104:107], v[172:175], v[140:143], a[104:107]\n" \
"s_add_u32 m0, s31, 0x0\n" \
"s_nop 0\n" \
"s_mov_b32 s84, 0x200000\n" \
"v_mfma_f32_16x16x32_bf16 a[108:111], v[112:115], v[76:79], a[108:111]\n" \
"buffer_load_dwordx4 v26, s[68:71], s84 offen lds\n" \
"v_mfma_f32_16x16x32_bf16 a[108:111], v[176:179], v[140:143], a[108:111]\n" \
"ds_read_b128 v[76:79], v28 offset:45056\n" \
"v_mfma_f32_16x16x32_bf16 a[172:175], v[112:115], v[80:83], a[172:175]\n" \
"ds_read_b128 v[140:143], v29 offset:45056\n" \
"v_mfma_f32_16x16x32_bf16 a[172:175], v[176:179], v[144:147], a[172:175]\n" \
"s_add_u32 m0, s30, 0x800\n" \
"s_mov_b32 s85, 0x4a00\n" \
"v_mfma_f32_16x16x32_bf16 a[168:171], v[108:111], v[80:83], a[168:171]\n" \
"buffer_load_dwordx4 v24, s[76:79], s85 offen lds\n" \
"v_mfma_f32_16x16x32_bf16 a[168:171], v[172:175], v[144:147], a[168:171]\n" \
"ds_read_b64_tr_b16 v[154:155], v33 offset:37376\n" \
"v_mfma_f32_16x16x32_bf16 a[140:143], v[104:107], v[80:83], a[140:143]\n" \
"ds_read_b64_tr_b16 v[96:97], v35 offset:32768\n" \
"v_mfma_f32_16x16x32_bf16 a[140:143], v[168:171], v[144:147], a[140:143]\n" \
"ds_read_b64_tr_b16 v[98:99], v35 offset:33280\n" \
"v_mfma_f32_16x16x32_bf16 a[136:139], v[100:103], v[80:83], a[136:139]\n" \
"s_cmpk_lt_u32 s3, 3\n" \
"s_cselect_b32 s18, s18, s17\n" \
"v_mfma_f32_16x16x32_bf16 a[136:139], v[164:167], v[144:147], a[136:139]\n" \
"ds_read_b128 v[80:83], v28 offset:47104\n" \
"s_bitset1_b32 s16, 17\n" \
"s_nop 0\n" \
"v_mfma_f32_16x16x32_bf16 a[80:83], v[100:103], v[60:63], a[80:83]\n" \
"ds_read_b128 v[144:147], v29 offset:47104\n" \
"v_writelane_b32 v11, s18, 22\n" \
"v_mfma_f32_16x16x32_bf16 a[80:83], v[164:167], v[124:127], a[80:83]\n" \
"s_add_u32 m0, s30, 0xc00\n" \
"s_mov_b32 s84, 0x6e00\n" \
"v_mfma_f32_16x16x32_bf16 a[84:87], v[104:107], v[60:63], a[84:87]\n" \
"buffer_load_dwordx4 v25, s[76:79], s84 offen lds\n" \
"v_mfma_f32_16x16x32_bf16 a[84:87], v[168:171], v[124:127], a[84:87]\n" \
"s_add_u32 m0, s31, 0x400\n" \
"s_mov_b32 s85, 0x208000\n" \
"v_mfma_f32_16x16x32_bf16 a[88:91], v[108:111], v[60:63], a[88:91]\n" \
"buffer_load_dwordx4 v26, s[68:71], s85 offen lds\n" \
"v_mfma_f32_16x16x32_bf16 a[88:91], v[172:175], v[124:127], a[88:91]\n" \
"ds_read_b64_tr_b16 v[160:161], v35 offset:36864\n" \
"s_movk_i32 s18, 0x2400\n" \
"s_nop 0\n" \
"v_mfma_f32_16x16x32_bf16 a[92:95], v[112:115], v[60:63], a[92:95]\n" \
"ds_read_b64_tr_b16 v[162:163], v35 offset:37376\n" \
"v_mfma_f32_16x16x32_bf16 a[92:95], v[176:179], v[124:127], a[92:95]\n" \
"ds_read_b128 v[60:63], v28 offset:36864\n" \
"v_mfma_f32_16x16x32_bf16 a[124:127], v[112:115], v[64:67], a[124:127]\n" \
"ds_read_b128 v[124:127], v29 offset:36864\n" \
"v_mfma_f32_16x16x32_bf16 a[124:127], v[176:179], v[128:131], a[124:127]\n" \
"s_add_u32 m0, s30, 0x1000\n" \
"s_mov_b32 s84, 0x9200\n" \
"v_mfma_f32_16x16x32_bf16 a[120:123], v[108:111], v[64:67], a[120:123]\n" \
"buffer_load_dwordx4 v24, s[76:79], s84 offen lds\n" \
"v_mfma_f32_16x16x32_bf16 a[120:123], v[172:175], v[128:131], a[120:123]\n" \
"s_add_u32 m0, s30, 0x1400\n" \
"s_mov_b32 s85, 0xb600\n" \
"v_mfma_f32_16x16x32_bf16 a[116:119], v[104:107], v[64:67], a[116:119]\n" \
"buffer_load_dwordx4 v25, s[76:79], s85 offen lds\n" \
"v_mfma_f32_16x16x32_bf16 a[116:119], v[168:171], v[128:131], a[116:119]\n" \
"ds_read_b64_tr_b16 v[92:93], v34 offset:32768\n" \
"v_mfma_f32_16x16x32_bf16 a[112:115], v[100:103], v[64:67], a[112:115]\n" \
"ds_read_b64_tr_b16 v[94:95], v34 offset:33280\n" \
"v_mfma_f32_16x16x32_bf16 a[112:115], v[164:167], v[128:131], a[112:115]\n" \
"ds_read_b128 v[64:67], v28 offset:38912\n" \
"v_mfma_f32_16x16x32_bf16 a[16:19], v[100:103], v[52:55], a[16:19]\n" \
"ds_read_b128 v[128:131], v29 offset:38912\n" \
"v_mfma_f32_16x16x32_bf16 a[16:19], v[164:167], v[116:119], a[16:19]\n" \
"s_add_u32 m0, s31, 0x800\n" \
"s_mov_b32 s84, 0x210000\n" \
"v_mfma_f32_16x16x32_bf16 a[20:23], v[104:107], v[52:55], a[20:23]\n" \
"buffer_load_dwordx4 v27, s[68:71], s84 offen lds\n" \
"v_mfma_f32_16x16x32_bf16 a[20:23], v[168:171], v[116:119], a[20:23]\n" \
"s_add_u32 m0, s30, 0x1800\n" \
"s_mov_b32 s85, 0xda00\n" \
"v_mfma_f32_16x16x32_bf16 a[24:27], v[108:111], v[52:55], a[24:27]\n" \
"buffer_load_dwordx4 v24, s[76:79], s85 offen lds\n" \
"v_mfma_f32_16x16x32_bf16 a[24:27], v[172:175], v[116:119], a[24:27]\n" \
"ds_read_b64_tr_b16 v[156:157], v34 offset:36864\n" \
"v_mfma_f32_16x16x32_bf16 a[28:31], v[112:115], v[52:55], a[28:31]\n" \
"ds_read_b64_tr_b16 v[158:159], v34 offset:37376\n" \
"v_mfma_f32_16x16x32_bf16 a[28:31], v[176:179], v[116:119], a[28:31]\n" \
"ds_read_b128 v[52:55], v28 offset:32768\n" \
"v_mfma_f32_16x16x32_bf16 a[60:63], v[112:115], v[56:59], a[60:63]\n" \
"ds_read_b128 v[116:119], v29 offset:32768\n" \
"v_mfma_f32_16x16x32_bf16 a[60:63], v[176:179], v[120:123], a[60:63]\n" \
"s_cmpk_lt_u32 s3, 3\n" \
"s_cselect_b32 s16, s16, s18\n" \
"v_mfma_f32_16x16x32_bf16 a[56:59], v[108:111], v[56:59], a[56:59]\n" \
"s_xor_b32 s13, s13, 2\n" \
"v_writelane_b32 v11, s16, 23\n" \
"v_mfma_f32_16x16x32_bf16 a[56:59], v[172:175], v[120:123], a[56:59]\n" \
"s_lshl_b32 s16, s3, 10\n" \
"s_or_b32 s16, s16, 0xf000\n" \
"v_mfma_f32_16x16x32_bf16 a[52:55], v[104:107], v[56:59], a[52:55]\n" \
"v_writelane_b32 v11, s16, 24\n" \
"s_add_i32 s16, s15, 1\n" \
"v_mfma_f32_16x16x32_bf16 a[52:55], v[168:171], v[120:123], a[52:55]\n" \
"s_and_b32 s16, s16, 3\n" \
"s_or_b32 s16, s16, s5\n" \
"v_mfma_f32_16x16x32_bf16 a[48:51], v[100:103], v[56:59], a[48:51]\n" \
"s_mul_i32 s18, s16, 0x24000\n" \
"s_add_i32 s18, s18, s19\n" \
"v_mfma_f32_16x16x32_bf16 a[48:51], v[164:167], v[120:123], a[48:51]\n" \
"ds_read_b128 v[56:59], v28 offset:34816\n" \
"ds_read_b128 v[120:123], v29 offset:34816\n" \
"s_waitcnt lgkmcnt(15)\n" \
"v_mfma_f32_16x16x32_bf16 a[0:3], v[84:87], v[68:71], a[0:3]\n" \
"s_add_u32 m0, s30, 0x1c00\n" \
"s_mov_b32 s85, 0xfe00\n" \
"v_mfma_f32_16x16x32_bf16 a[0:3], v[148:151], v[132:135], a[0:3]\n" \
"buffer_load_dwordx4 v25, s[76:79], s85 offen lds\n" \
"v_mfma_f32_16x16x32_bf16 a[4:7], v[88:91], v[68:71], a[4:7]\n" \
"ds_read_b64_tr_b16 v[100:101], v36 offset:32768\n" \
"s_add_i32 s15, s15, -1\n" \
"v_mfma_f32_16x16x32_bf16 a[4:7], v[152:155], v[132:135], a[4:7]\n" \
"s_add_u32 m0, s31, 0xc00\n" \
"s_mov_b32 s84, 0x218000\n" \
"v_mfma_f32_16x16x32_bf16 a[36:39], v[96:99], v[68:71], a[36:39]\n" \
"buffer_load_dwordx4 v27, s[68:71], s84 offen lds\n" \
"s_waitcnt lgkmcnt(13)\n" \
"v_mfma_f32_16x16x32_bf16 a[36:39], v[160:163], v[132:135], a[36:39]\n" \
"ds_read_b64_tr_b16 v[102:103], v36 offset:33792\n" \
"v_writelane_b32 v11, s18, 26\n" \
"s_waitcnt lgkmcnt(10)\n" \
"v_mfma_f32_16x16x32_bf16 a[32:35], v[68:71], v[92:95], a[32:35]\n" \
"ds_read_b64_tr_b16 v[164:165], v36 offset:40960\n" \
"s_mul_i32 s18, s13, 0x24000\n" \
"s_waitcnt lgkmcnt(7)\n" \
"v_mfma_f32_16x16x32_bf16 a[32:35], v[132:135], v[156:159], a[32:35]\n" \
"ds_read_b64_tr_b16 v[166:167], v36 offset:41984\n" \
"s_and_b32 s15, s15, 3\n" \
"v_mfma_f32_16x16x32_bf16 a[96:99], v[72:75], v[92:95], a[96:99]\n" \
"ds_read_b64_tr_b16 v[104:105], v37 offset:32768\n" \
"s_add_i32 s18, s18, s19\n" \
"v_mfma_f32_16x16x32_bf16 a[96:99], v[136:139], v[156:159], a[96:99]\n" \
"ds_read_b64_tr_b16 v[106:107], v37 offset:33792\n" \
"s_or_b32 s15, s15, s5\n" \
"v_mfma_f32_16x16x32_bf16 a[100:103], v[96:99], v[72:75], a[100:103]\n" \
"ds_read_b64_tr_b16 v[168:169], v37 offset:40960\n" \
"v_writelane_b32 v11, s18, 27\n" \
"v_mfma_f32_16x16x32_bf16 a[100:103], v[160:163], v[136:139], a[100:103]\n" \
"s_add_u32 s89, s42, 0x0\n" \
"buffer_store_dwordx4 v[216:219], v20, s[80:83], s89 offen offset:3072 sc1\n" \
"v_mfma_f32_16x16x32_bf16 a[68:71], v[88:91], v[72:75], a[68:71]\n" \
"ds_read_b64_tr_b16 v[170:171], v37 offset:41984\n" \
"s_mul_i32 s18, s15, 0x24000\n" \
"v_mfma_f32_16x16x32_bf16 a[68:71], v[152:155], v[136:139], a[68:71]\n" \
"ds_read_b64_tr_b16 v[108:109], v38 offset:32768\n" \
"s_add_i32 s18, s18, s19\n" \
"v_mfma_f32_16x16x32_bf16 a[64:67], v[84:87], v[72:75], a[64:67]\n" \
"ds_read_b64_tr_b16 v[110:111], v38 offset:33792\n" \
"v_writelane_b32 v11, s18, 28\n" \
"v_mfma_f32_16x16x32_bf16 a[64:67], v[148:151], v[136:139], a[64:67]\n" \
"s_or_b32 s18, s9, s14\n" \
"s_mul_i32 s18, s18, 0x24800\n" \
"v_mfma_f32_16x16x32_bf16 a[128:131], v[84:87], v[76:79], a[128:131]\n" \
"s_add_u32 s88, s42, 0x0\n" \
"buffer_store_dwordx4 v[220:223], v20, s[80:83], s88 offen offset:2048 sc1\n" \
"v_mfma_f32_16x16x32_bf16 a[128:131], v[148:151], v[140:143], a[128:131]\n" \
"ds_read_b64_tr_b16 v[172:173], v38 offset:40960\n" \
"s_lshl_b32 s16, s16, 12\n" \
"v_mfma_f32_16x16x32_bf16 a[132:135], v[88:91], v[76:79], a[132:135]\n" \
"ds_read_b64_tr_b16 v[174:175], v38 offset:41984\n" \
"s_add_i32 s19, s18, 0x3004800\n" \
"v_mfma_f32_16x16x32_bf16 a[132:135], v[152:155], v[140:143], a[132:135]\n" \
".fill (L_pb + 73440 - .) / 4, 4, 0xBF800000\n" \
"L_pp18:\n" \
"s_cbranch_execz L_pret\n" \
"ds_read_b64_tr_b16 v[112:113], v39 offset:32768\n" \
"s_add_i32 s20, s19, s16\n" \
"v_mfma_f32_16x16x32_bf16 a[164:167], v[96:99], v[76:79], a[164:167]\n" \
"v_writelane_b32 v11, s20, 29\n" \
"s_lshl_b32 s13, s13, 12\n" \
"v_mfma_f32_16x16x32_bf16 a[164:167], v[160:163], v[140:143], a[164:167]\n" \
"ds_read_b64_tr_b16 v[114:115], v39 offset:33792\n" \
"s_lshl_b32 s15, s15, 12\n" \
"v_mfma_f32_16x16x32_bf16 a[160:163], v[76:79], v[92:95], a[160:163]\n" \
"s_add_u32 s89, s42, 0x0\n" \
"buffer_store_dwordx4 v[224:227], v20, s[80:83], s89 offen offset:1024 sc1\n" \
"v_mfma_f32_16x16x32_bf16 a[160:163], v[140:143], v[156:159], a[160:163]\n" \
"ds_read_b64_tr_b16 v[176:177], v39 offset:40960\n" \
"s_sub_i32 s14, s9, s14\n" \
"v_mfma_f32_16x16x32_bf16 a[224:227], v[80:83], v[92:95], a[224:227]\n" \
"ds_read_b64_tr_b16 v[178:179], v39 offset:41984\n" \
"s_add_i32 s20, s19, s13\n" \
"v_mfma_f32_16x16x32_bf16 a[224:227], v[144:147], v[156:159], a[224:227]\n" \
"v_writelane_b32 v11, s20, 30\n" \
"s_add_i32 s19, s19, s15\n" \
"v_mfma_f32_16x16x32_bf16 a[228:231], v[96:99], v[80:83], a[228:231]\n" \
"s_mul_i32 s14, s14, 0x24800\n" \
"v_writelane_b32 v11, s19, 31\n" \
"v_mfma_f32_16x16x32_bf16 a[228:231], v[160:163], v[144:147], a[228:231]\n" \
"s_add_u32 s88, s42, 0x0\n" \
"buffer_store_dwordx4 v[228:231], v20, s[80:83], s88 offen offset:0 sc1\n" \
"v_mfma_f32_16x16x32_bf16 a[196:199], v[88:91], v[80:83], a[196:199]\n" \
"s_add_i32 s19, s14, 0x4120800\n" \
"s_add_i32 s16, s19, s16\n" \
"v_mfma_f32_16x16x32_bf16 a[196:199], v[152:155], v[144:147], a[196:199]\n" \
"v_writelane_b32 v11, s16, 32\n" \
"s_add_i32 s13, s19, s13\n" \
"v_mfma_f32_16x16x32_bf16 a[192:195], v[84:87], v[80:83], a[192:195]\n" \
"v_writelane_b32 v11, s13, 33\n" \
"s_add_i32 s19, s19, s15\n" \
"v_mfma_f32_16x16x32_bf16 a[192:195], v[148:151], v[144:147], a[192:195]\n" \
"s_or_b32 s13, s18, 0x3000000\n" \
"s_lshl_b32 s15, s6, 11\n" \
"v_mfma_f32_16x16x32_bf16 a[8:11], v[88:91], v[64:67], a[8:11]\n" \
"v_writelane_b32 v11, s19, 34\n" \
"s_add_i32 s16, s13, s15\n" \
"v_mfma_f32_16x16x32_bf16 a[8:11], v[152:155], v[128:131], a[8:11]\n" \
"v_writelane_b32 v11, s16, 35\n" \
"s_add_i32 s16, s14, 0x411c000\n" \
"v_mfma_f32_16x16x32_bf16 a[40:43], v[96:99], v[64:67], a[40:43]\n" \
"s_add_i32 s19, s16, s15\n" \
"v_writelane_b32 v11, s19, 36\n" \
"v_mfma_f32_16x16x32_bf16 a[40:43], v[160:163], v[128:131], a[40:43]\n" \
"s_add_i32 s18, s18, 0x3004000\n" \
"v_writelane_b32 v11, s18, 37\n" \
"v_mfma_f32_16x16x32_bf16 a[12:15], v[64:67], v[92:95], a[12:15]\n" \
"s_add_i32 s14, s14, 0x4120000\n" \
"v_writelane_b32 v11, s14, 38\n" \
"v_mfma_f32_16x16x32_bf16 a[12:15], v[128:131], v[156:159], a[12:15]\n" \
"s_waitcnt vmcnt(4) lgkmcnt(0)\n" \
"s_barrier\n" \
"v_mov_b32_e32 v45, s62\n" \
"s_mov_b64 exec, 1\n" \
"buffer_store_dword v45, off, s[80:83], s37 offset:0 sc1\n" \
"s_mov_b64 exec, -1\n" \
"v_mfma_f32_16x16x32_bf16 a[144:147], v[100:103], v[68:71], a[144:147]\n" \
"s_add_u32 m0, s40, 0x8000\n" \
"s_add_u32 s85, s39, 0xa000\n" \
"v_mfma_f32_16x16x32_bf16 a[144:147], v[164:167], v[132:135], a[144:147]\n" \
"buffer_load_dwordx4 v20, s[80:83], s85 offen sc1 lds\n" \
"v_mfma_f32_16x16x32_bf16 a[148:151], v[104:107], v[68:71], a[148:151]\n" \
"ds_read_b64_tr_b16 v[84:85], v32 offset:0\n" \
"v_accvgpr_read_b32 v212, a220\n" \
"v_mfma_f32_16x16x32_bf16 a[148:151], v[168:171], v[132:135], a[148:151]\n" \
"ds_read_b64_tr_b16 v[86:87], v32 offset:512\n" \
"v_accvgpr_read_b32 v213, a221\n" \
"v_mfma_f32_16x16x32_bf16 a[200:203], v[108:111], v[68:71], a[200:203]\n" \
"s_add_u32 m0, s40, 0x9000\n" \
"s_add_u32 s84, s39, 0xb000\n" \
"v_mfma_f32_16x16x32_bf16 a[200:203], v[172:175], v[132:135], a[200:203]\n" \
"buffer_load_dwordx4 v20, s[80:83], s84 offen sc1 lds\n" \
"v_mfma_f32_16x16x32_bf16 a[204:207], v[112:115], v[68:71], a[204:207]\n" \
"ds_read_b64_tr_b16 v[148:149], v32 offset:4096\n" \
"v_accvgpr_read_b32 v214, a222\n" \
"v_mfma_f32_16x16x32_bf16 a[204:207], v[176:179], v[132:135], a[204:207]\n" \
"ds_read_b128 v[68:71], v28 offset:8192\n" \
"v_accvgpr_read_b32 v215, a223\n" \
"v_mfma_f32_16x16x32_bf16 a[236:239], v[112:115], v[72:75], a[236:239]\n" \
"ds_read_b128 v[132:135], v29 offset:8192\n" \
"v_accvgpr_read_b32 v180, a252\n" \
"v_mfma_f32_16x16x32_bf16 a[236:239], v[176:179], v[136:139], a[236:239]\n" \
"s_add_u32 m0, s40, 0xb000\n" \
"s_mov_b32 s85, 0x280\n" \
"v_mfma_f32_16x16x32_bf16 a[232:235], v[108:111], v[72:75], a[232:235]\n" \
"buffer_load_dwordx4 v21, s[76:79], s85 offen lds\n" \
"v_mfma_f32_16x16x32_bf16 a[232:235], v[172:175], v[136:139], a[232:235]\n" \
"ds_read_b64_tr_b16 v[150:151], v32 offset:4608\n" \
"v_accvgpr_read_b32 v181, a253\n" \
"v_mfma_f32_16x16x32_bf16 a[180:183], v[104:107], v[72:75], a[180:183]\n" \
"s_add_u32 m0, s30, 0x8000\n" \
"s_mov_b32 s84, 0x280\n" \
"v_mfma_f32_16x16x32_bf16 a[180:183], v[168:171], v[136:139], a[180:183]\n" \
"buffer_load_dwordx4 v24, s[76:79], s84 offen lds\n" \
"v_mfma_f32_16x16x32_bf16 a[176:179], v[100:103], v[72:75], a[176:179]\n" \
"ds_read_b64_tr_b16 v[88:89], v33 offset:0\n" \
"v_accvgpr_read_b32 v182, a254\n" \
"v_mfma_f32_16x16x32_bf16 a[176:179], v[164:167], v[136:139], a[176:179]\n" \
"ds_read_b128 v[72:75], v28 offset:10240\n" \
"v_accvgpr_read_b32 v183, a255\n" \
"v_mfma_f32_16x16x32_bf16 a[72:75], v[100:103], v[76:79], a[72:75]\n" \
"ds_read_b128 v[136:139], v29 offset:10240\n" \
"v_cvt_pk_bf16_f32 v212, v212, v213\n" \
"v_mfma_f32_16x16x32_bf16 a[72:75], v[164:167], v[140:143], a[72:75]\n" \
"s_add_u32 m0, s30, 0x8400\n" \
"s_mov_b32 s85, 0x2680\n" \
"v_mfma_f32_16x16x32_bf16 a[76:79], v[104:107], v[76:79], a[76:79]\n" \
"buffer_load_dwordx4 v25, s[76:79], s85 offen lds\n" \
"v_mfma_f32_16x16x32_bf16 a[76:79], v[168:171], v[140:143], a[76:79]\n" \
"ds_read_b64_tr_b16 v[90:91], v33 offset:512\n" \
"v_cvt_pk_bf16_f32 v213, v214, v215\n" \
"v_mfma_f32_16x16x32_bf16 a[104:107], v[108:111], v[76:79], a[104:107]\n" \
"ds_read_b64_tr_b16 v[152:153], v33 offset:4096\n" \
"v_cvt_pk_bf16_f32 v214, v180, v181\n" \
"v_mfma_f32_16x16x32_bf16 a[104:107], v[172:175], v[140:143], a[104:107]\n" \
"s_add_u32 m0, s31, 0x8000\n" \
"s_mov_b32 s84, 0x280000\n" \
"v_mfma_f32_16x16x32_bf16 a[108:111], v[112:115], v[76:79], a[108:111]\n" \
"buffer_load_dwordx4 v26, s[68:71], s84 offen lds\n" \
"v_mfma_f32_16x16x32_bf16 a[108:111], v[176:179], v[140:143], a[108:111]\n" \
"ds_read_b128 v[76:79], v28 offset:12288\n" \
"v_cvt_pk_bf16_f32 v215, v182, v183\n" \
"v_mfma_f32_16x16x32_bf16 a[172:175], v[112:115], v[80:83], a[172:175]\n" \
"ds_read_b128 v[140:143], v29 offset:12288\n" \
"v_accvgpr_read_b32 v208, a216\n" \
"v_mfma_f32_16x16x32_bf16 a[172:175], v[176:179], v[144:147], a[172:175]\n" \
"s_add_u32 m0, s30, 0x8800\n" \
"s_mov_b32 s85, 0x4a80\n" \
"v_mfma_f32_16x16x32_bf16 a[168:171], v[108:111], v[80:83], a[168:171]\n" \
"buffer_load_dwordx4 v24, s[76:79], s85 offen lds\n" \
"v_mfma_f32_16x16x32_bf16 a[168:171], v[172:175], v[144:147], a[168:171]\n" \
"ds_read_b64_tr_b16 v[154:155], v33 offset:4608\n" \
"v_accvgpr_read_b32 v209, a217\n" \
"v_mfma_f32_16x16x32_bf16 a[140:143], v[104:107], v[80:83], a[140:143]\n" \
"ds_read_b64_tr_b16 v[96:97], v35 offset:0\n" \
"v_accvgpr_read_b32 v210, a218\n" \
"v_mfma_f32_16x16x32_bf16 a[140:143], v[168:171], v[144:147], a[140:143]\n" \
"ds_read_b64_tr_b16 v[98:99], v35 offset:512\n" \
"v_accvgpr_read_b32 v211, a219\n" \
"v_mfma_f32_16x16x32_bf16 a[136:139], v[100:103], v[80:83], a[136:139]\n" \
"v_accvgpr_read_b32 v180, a248\n" \
"v_accvgpr_read_b32 v181, a249\n" \
"v_mfma_f32_16x16x32_bf16 a[136:139], v[164:167], v[144:147], a[136:139]\n" \
"ds_read_b128 v[80:83], v28 offset:14336\n" \
"v_accvgpr_read_b32 v182, a250\n" \
"v_mfma_f32_16x16x32_bf16 a[80:83], v[100:103], v[60:63], a[80:83]\n" \
"ds_read_b128 v[144:147], v29 offset:14336\n" \
"v_accvgpr_read_b32 v183, a251\n" \
"v_mfma_f32_16x16x32_bf16 a[80:83], v[164:167], v[124:127], a[80:83]\n" \
"s_add_u32 m0, s30, 0x8c00\n" \
"s_mov_b32 s84, 0x6e80\n" \
"v_mfma_f32_16x16x32_bf16 a[84:87], v[104:107], v[60:63], a[84:87]\n" \
"buffer_load_dwordx4 v25, s[76:79], s84 offen lds\n" \
"v_mfma_f32_16x16x32_bf16 a[84:87], v[168:171], v[124:127], a[84:87]\n" \
"s_add_u32 m0, s31, 0x8400\n" \
"s_mov_b32 s85, 0x288000\n" \
"v_mfma_f32_16x16x32_bf16 a[88:91], v[108:111], v[60:63], a[88:91]\n" \
"buffer_load_dwordx4 v26, s[68:71], s85 offen lds\n" \
"v_mfma_f32_16x16x32_bf16 a[88:91], v[172:175], v[124:127], a[88:91]\n" \
"ds_read_b64_tr_b16 v[160:161], v35 offset:4096\n" \
"v_cvt_pk_bf16_f32 v208, v208, v209\n" \
"v_mfma_f32_16x16x32_bf16 a[92:95], v[112:115], v[60:63], a[92:95]\n" \
"ds_read_b64_tr_b16 v[162:163], v35 offset:4608\n" \
"v_cvt_pk_bf16_f32 v209, v210, v211\n" \
"v_mfma_f32_16x16x32_bf16 a[92:95], v[176:179], v[124:127], a[92:95]\n" \
"ds_read_b128 v[60:63], v28 offset:4096\n" \
"v_cvt_pk_bf16_f32 v210, v180, v181\n" \
"v_mfma_f32_16x16x32_bf16 a[124:127], v[112:115], v[64:67], a[124:127]\n" \
"ds_read_b128 v[124:127], v29 offset:4096\n" \
"v_cvt_pk_bf16_f32 v211, v182, v183\n" \
"v_mfma_f32_16x16x32_bf16 a[124:127], v[176:179], v[128:131], a[124:127]\n" \
"s_add_u32 m0, s30, 0x9000\n" \
"s_mov_b32 s84, 0x9280\n" \
"v_mfma_f32_16x16x32_bf16 a[120:123], v[108:111], v[64:67], a[120:123]\n" \
"buffer_load_dwordx4 v24, s[76:79], s84 offen lds\n" \
"v_mfma_f32_16x16x32_bf16 a[120:123], v[172:175], v[128:131], a[120:123]\n" \
"s_add_u32 m0, s30, 0x9400\n" \
"s_mov_b32 s85, 0xb680\n" \
"v_mfma_f32_16x16x32_bf16 a[116:119], v[104:107], v[64:67], a[116:119]\n" \
"buffer_load_dwordx4 v25, s[76:79], s85 offen lds\n" \
"v_mfma_f32_16x16x32_bf16 a[116:119], v[168:171], v[128:131], a[116:119]\n" \
"ds_read_b64_tr_b16 v[92:93], v34 offset:0\n" \
"v_accvgpr_read_b32 v204, a212\n" \
"v_mfma_f32_16x16x32_bf16 a[112:115], v[100:103], v[64:67], a[112:115]\n" \
"ds_read_b64_tr_b16 v[94:95], v34 offset:512\n" \
"v_accvgpr_read_b32 v205, a213\n" \
"v_mfma_f32_16x16x32_bf16 a[112:115], v[164:167], v[128:131], a[112:115]\n" \
"ds_read_b128 v[64:67], v28 offset:6144\n" \
"v_accvgpr_read_b32 v206, a214\n" \
"v_mfma_f32_16x16x32_bf16 a[16:19], v[100:103], v[52:55], a[16:19]\n" \
"ds_read_b128 v[128:131], v29 offset:6144\n" \
"v_accvgpr_read_b32 v207, a215\n" \
"v_mfma_f32_16x16x32_bf16 a[16:19], v[164:167], v[116:119], a[16:19]\n" \
"s_add_u32 m0, s31, 0x8800\n" \
"s_mov_b32 s84, 0x290000\n" \
"v_mfma_f32_16x16x32_bf16 a[20:23], v[104:107], v[52:55], a[20:23]\n" \
"buffer_load_dwordx4 v27, s[68:71], s84 offen lds\n" \
"v_mfma_f32_16x16x32_bf16 a[20:23], v[168:171], v[116:119], a[20:23]\n" \
"s_add_u32 m0, s30, 0x9800\n" \
"s_mov_b32 s85, 0xda80\n" \
"v_mfma_f32_16x16x32_bf16 a[24:27], v[108:111], v[52:55], a[24:27]\n" \
"buffer_load_dwordx4 v24, s[76:79], s85 offen lds\n" \
"v_mfma_f32_16x16x32_bf16 a[24:27], v[172:175], v[116:119], a[24:27]\n" \
"ds_read_b64_tr_b16 v[156:157], v34 offset:4096\n" \
"v_accvgpr_read_b32 v180, a244\n" \
"v_mfma_f32_16x16x32_bf16 a[28:31], v[112:115], v[52:55], a[28:31]\n" \
"ds_read_b64_tr_b16 v[158:159], v34 offset:4608\n" \
"v_accvgpr_read_b32 v181, a245\n" \
"v_mfma_f32_16x16x32_bf16 a[28:31], v[176:179], v[116:119], a[28:31]\n" \
"ds_read_b128 v[52:55], v28 offset:0\n" \
"v_accvgpr_read_b32 v182, a246\n" \
"v_mfma_f32_16x16x32_bf16 a[60:63], v[112:115], v[56:59], a[60:63]\n" \
"ds_read_b128 v[116:119], v29 offset:0\n" \
"v_accvgpr_read_b32 v183, a247\n" \
"v_mfma_f32_16x16x32_bf16 a[60:63], v[176:179], v[120:123], a[60:63]\n" \
"v_cvt_pk_bf16_f32 v204, v204, v205\n" \
"v_cvt_pk_bf16_f32 v205, v206, v207\n" \
"v_mfma_f32_16x16x32_bf16 a[56:59], v[108:111], v[56:59], a[56:59]\n" \
"v_cvt_pk_bf16_f32 v206, v180, v181\n" \
"v_cvt_pk_bf16_f32 v207, v182, v183\n" \
"v_mfma_f32_16x16x32_bf16 a[56:59], v[172:175], v[120:123], a[56:59]\n" \
"v_accvgpr_read_b32 v200, a208\n" \
"v_accvgpr_read_b32 v201, a209\n" \
"v_mfma_f32_16x16x32_bf16 a[52:55], v[104:107], v[56:59], a[52:55]\n" \
"v_accvgpr_read_b32 v202, a210\n" \
"v_accvgpr_read_b32 v203, a211\n" \
"v_mfma_f32_16x16x32_bf16 a[52:55], v[168:171], v[120:123], a[52:55]\n" \
"v_accvgpr_read_b32 v180, a240\n" \
"v_accvgpr_read_b32 v181, a241\n" \
"v_mfma_f32_16x16x32_bf16 a[48:51], v[100:103], v[56:59], a[48:51]\n" \
"v_accvgpr_read_b32 v182, a242\n" \
"v_accvgpr_read_b32 v183, a243\n" \
"v_mfma_f32_16x16x32_bf16 a[48:51], v[164:167], v[120:123], a[48:51]\n" \
"ds_read_b128 v[56:59], v28 offset:2048\n" \
"ds_read_b128 v[120:123], v29 offset:2048\n" \
"s_waitcnt lgkmcnt(15)\n" \
"s_nop 0\n" \
"v_mfma_f32_16x16x32_bf16 a[0:3], v[84:87], v[68:71], a[0:3]\n" \
"s_add_u32 m0, s30, 0x9c00\n" \
"s_mov_b32 s85, 0xfe80\n" \
"v_mfma_f32_16x16x32_bf16 a[0:3], v[148:151], v[132:135], a[0:3]\n" \
"buffer_load_dwordx4 v25, s[76:79], s85 offen lds\n" \
"v_mfma_f32_16x16x32_bf16 a[4:7], v[88:91], v[68:71], a[4:7]\n" \
"ds_read_b64_tr_b16 v[100:101], v36 offset:0\n" \
"v_cvt_pk_bf16_f32 v200, v200, v201\n" \
"v_mfma_f32_16x16x32_bf16 a[4:7], v[152:155], v[132:135], a[4:7]\n" \
"s_add_u32 m0, s31, 0x8c00\n" \
"s_mov_b32 s84, 0x298000\n" \
"v_mfma_f32_16x16x32_bf16 a[36:39], v[96:99], v[68:71], a[36:39]\n" \
"buffer_load_dwordx4 v27, s[68:71], s84 offen lds\n" \
"s_waitcnt lgkmcnt(13)\n" \
"v_mfma_f32_16x16x32_bf16 a[36:39], v[160:163], v[132:135], a[36:39]\n" \
"ds_read_b64_tr_b16 v[102:103], v36 offset:1024\n" \
"v_cvt_pk_bf16_f32 v201, v202, v203\n" \
"s_waitcnt lgkmcnt(10)\n" \
"v_mfma_f32_16x16x32_bf16 a[32:35], v[68:71], v[92:95], a[32:35]\n" \
"ds_read_b64_tr_b16 v[164:165], v36 offset:8192\n" \
"v_cvt_pk_bf16_f32 v202, v180, v181\n" \
"s_waitcnt lgkmcnt(7)\n" \
"s_nop 0\n" \
"v_mfma_f32_16x16x32_bf16 a[32:35], v[132:135], v[156:159], a[32:35]\n" \
"ds_read_b64_tr_b16 v[166:167], v36 offset:9216\n" \
"v_cvt_pk_bf16_f32 v203, v182, v183\n" \
"v_mfma_f32_16x16x32_bf16 a[96:99], v[72:75], v[92:95], a[96:99]\n" \
"ds_read_b64_tr_b16 v[104:105], v37 offset:0\n" \
"v_accvgpr_read_b32 v196, a156\n" \
"v_mfma_f32_16x16x32_bf16 a[96:99], v[136:139], v[156:159], a[96:99]\n" \
"ds_read_b64_tr_b16 v[106:107], v37 offset:1024\n" \
"v_accvgpr_read_b32 v197, a157\n" \
"v_mfma_f32_16x16x32_bf16 a[100:103], v[96:99], v[72:75], a[100:103]\n" \
"ds_read_b64_tr_b16 v[168:169], v37 offset:8192\n" \
"v_accvgpr_read_b32 v198, a158\n" \
"v_mfma_f32_16x16x32_bf16 a[100:103], v[160:163], v[136:139], a[100:103]\n" \
"ds_read_b64_tr_b16 v[170:171], v37 offset:9216\n" \
"v_accvgpr_read_b32 v199, a159\n" \
"v_mfma_f32_16x16x32_bf16 a[68:71], v[88:91], v[72:75], a[68:71]\n" \
"v_accvgpr_read_b32 v180, a188\n" \
"v_accvgpr_read_b32 v181, a189\n" \
"v_mfma_f32_16x16x32_bf16 a[68:71], v[152:155], v[136:139], a[68:71]\n" \
"s_add_u32 s89, s42, 0x2000\n" \
"buffer_store_dwordx4 v[232:235], v20, s[80:83], s89 offen offset:3072 sc1\n" \
"v_mfma_f32_16x16x32_bf16 a[64:67], v[84:87], v[72:75], a[64:67]\n" \
"ds_read_b64_tr_b16 v[108:109], v38 offset:0\n" \
"v_accvgpr_read_b32 v182, a190\n" \
"v_mfma_f32_16x16x32_bf16 a[64:67], v[148:151], v[136:139], a[64:67]\n" \
"ds_read_b64_tr_b16 v[110:111], v38 offset:1024\n" \
"v_accvgpr_read_b32 v183, a191\n" \
"v_mfma_f32_16x16x32_bf16 a[128:131], v[84:87], v[76:79], a[128:131]\n" \
"ds_read_b64_tr_b16 v[172:173], v38 offset:8192\n" \
"v_cvt_pk_bf16_f32 v196, v196, v197\n" \
"v_mfma_f32_16x16x32_bf16 a[128:131], v[148:151], v[140:143], a[128:131]\n" \
"ds_read_b64_tr_b16 v[174:175], v38 offset:9216\n" \
"v_cvt_pk_bf16_f32 v197, v198, v199\n" \
"v_mfma_f32_16x16x32_bf16 a[132:135], v[88:91], v[76:79], a[132:135]\n" \
"v_cvt_pk_bf16_f32 v198, v180, v181\n" \
"v_cvt_pk_bf16_f32 v199, v182, v183\n" \
"v_mfma_f32_16x16x32_bf16 a[132:135], v[152:155], v[140:143], a[132:135]\n" \
"s_add_u32 s88, s42, 0x2000\n" \
"buffer_store_dwordx4 v[236:239], v20, s[80:83], s88 offen offset:2048 sc1\n" \
"v_mfma_f32_16x16x32_bf16 a[164:167], v[96:99], v[76:79], a[164:167]\n" \
"ds_read_b64_tr_b16 v[112:113], v39 offset:0\n" \
"v_accvgpr_read_b32 v192, a152\n" \
"v_mfma_f32_16x16x32_bf16 a[164:167], v[160:163], v[140:143], a[164:167]\n" \
"ds_read_b64_tr_b16 v[114:115], v39 offset:1024\n" \
"v_accvgpr_read_b32 v193, a153\n" \
"v_mfma_f32_16x16x32_bf16 a[160:163], v[76:79], v[92:95], a[160:163]\n" \
"ds_read_b64_tr_b16 v[176:177], v39 offset:8192\n" \
"v_accvgpr_read_b32 v194, a154\n" \
"v_mfma_f32_16x16x32_bf16 a[160:163], v[140:143], v[156:159], a[160:163]\n" \
"v_accvgpr_read_b32 v195, a155\n" \
"v_accvgpr_read_b32 v180, a184\n" \
"v_mfma_f32_16x16x32_bf16 a[224:227], v[80:83], v[92:95], a[224:227]\n" \
"ds_read_b64_tr_b16 v[178:179], v39 offset:9216\n" \
"v_accvgpr_read_b32 v181, a185\n" \
"v_mfma_f32_16x16x32_bf16 a[224:227], v[144:147], v[156:159], a[224:227]\n" \
"v_accvgpr_read_b32 v182, a186\n" \
"v_accvgpr_read_b32 v183, a187\n" \
"v_mfma_f32_16x16x32_bf16 a[228:231], v[96:99], v[80:83], a[228:231]\n" \
"s_add_u32 s89, s42, 0x2000\n" \
"buffer_store_dwordx4 v[240:243], v20, s[80:83], s89 offen offset:1024 sc1\n" \
"v_mfma_f32_16x16x32_bf16 a[228:231], v[160:163], v[144:147], a[228:231]\n" \
"v_cvt_pk_bf16_f32 v192, v192, v193\n" \
"v_cvt_pk_bf16_f32 v193, v194, v195\n" \
"v_mfma_f32_16x16x32_bf16 a[196:199], v[88:91], v[80:83], a[196:199]\n" \
"v_cvt_pk_bf16_f32 v194, v180, v181\n" \
"v_cvt_pk_bf16_f32 v195, v182, v183\n" \
"v_mfma_f32_16x16x32_bf16 a[196:199], v[152:155], v[144:147], a[196:199]\n" \
"s_lshl_b32 s14, s4, 2\n" \
"s_or_b32 s8, s14, s8\n" \
"v_mfma_f32_16x16x32_bf16 a[192:195], v[84:87], v[80:83], a[192:195]\n" \
"s_mul_i32 s8, s8, 0x90000\n" \
"s_lshl_b32 s14, s3, 14\n" \
"v_mfma_f32_16x16x32_bf16 a[192:195], v[148:151], v[144:147], a[192:195]\n" \
"s_add_i32 s8, s8, s14\n" \
"s_lshl_b32 s9, s9, 4\n" \
"v_mfma_f32_16x16x32_bf16 a[8:11], v[88:91], v[64:67], a[8:11]\n" \
"s_lshl_b32 s14, s4, 7\n" \
"s_or_b32 s18, s9, s14\n" \
"v_mfma_f32_16x16x32_bf16 a[8:11], v[152:155], v[128:131], a[8:11]\n" \
"s_sub_i32 s9, s9, s14\n" \
"s_mulk_i32 s18, 0x480\n" \
"v_mfma_f32_16x16x32_bf16 a[40:43], v[96:99], v[64:67], a[40:43]\n" \
"s_mulk_i32 s9, 0x480\n" \
"s_or_b32 s14, s18, s12\n" \
"v_mfma_f32_16x16x32_bf16 a[40:43], v[160:163], v[128:131], a[40:43]\n" \
"s_or_b32 s9, s9, s12\n" \
"s_and_b32 s12, s2, 1\n" \
"v_mfma_f32_16x16x32_bf16 a[12:15], v[64:67], v[92:95], a[12:15]\n" \
"s_cmp_lg_u32 s12, 0\n" \
"s_cselect_b32 s13, s16, s13\n" \
"v_mfma_f32_16x16x32_bf16 a[12:15], v[128:131], v[156:159], a[12:15]\n" \
"s_waitcnt vmcnt(3) lgkmcnt(0)\n" \
"s_barrier\n" \
"v_mfma_f32_16x16x32_bf16 a[144:147], v[100:103], v[68:71], a[144:147]\n" \
"s_add_u32 m0, s40, 0x0\n" \
"s_add_u32 s84, s39, 0xc000\n" \
"v_mfma_f32_16x16x32_bf16 a[144:147], v[164:167], v[132:135], a[144:147]\n" \
"buffer_load_dwordx4 v20, s[80:83], s84 offen sc1 lds\n" \
"v_mfma_f32_16x16x32_bf16 a[148:151], v[104:107], v[68:71], a[148:151]\n" \
"ds_read_b64_tr_b16 v[84:85], v32 offset:32768\n" \
"s_bfe_u32 s16, s2, 0x20001\n" \
"v_mfma_f32_16x16x32_bf16 a[148:151], v[168:171], v[132:135], a[148:151]\n" \
"ds_read_b64_tr_b16 v[86:87], v32 offset:33280\n" \
"s_addk_i32 s13, 0x4000\n" \
"v_mfma_f32_16x16x32_bf16 a[200:203], v[108:111], v[68:71], a[200:203]\n" \
"s_add_u32 m0, s40, 0x1000\n" \
"s_add_u32 s85, s39, 0xd000\n" \
"v_mfma_f32_16x16x32_bf16 a[200:203], v[172:175], v[132:135], a[200:203]\n" \
"buffer_load_dwordx4 v20, s[80:83], s85 offen sc1 lds\n" \
"v_mfma_f32_16x16x32_bf16 a[204:207], v[112:115], v[68:71], a[204:207]\n" \
"ds_read_b64_tr_b16 v[148:149], v32 offset:36864\n" \
"s_xor_b32 s16, s16, s3\n" \
"v_mfma_f32_16x16x32_bf16 a[204:207], v[176:179], v[132:135], a[204:207]\n" \
"ds_read_b128 v[68:71], v28 offset:40960\n" \
"v_mfma_f32_16x16x32_bf16 a[236:239], v[112:115], v[72:75], a[236:239]\n" \
"ds_read_b128 v[132:135], v29 offset:40960\n" \
"v_mfma_f32_16x16x32_bf16 a[236:239], v[176:179], v[136:139], a[236:239]\n" \
"s_add_u32 m0, s40, 0x3000\n" \
"s_mov_b32 s84, 0x300\n" \
"v_mfma_f32_16x16x32_bf16 a[232:235], v[108:111], v[72:75], a[232:235]\n" \
"buffer_load_dwordx4 v21, s[76:79], s84 offen lds\n" \
"v_mfma_f32_16x16x32_bf16 a[232:235], v[172:175], v[136:139], a[232:235]\n" \
"ds_read_b64_tr_b16 v[150:151], v32 offset:37376\n" \
"v_mfma_f32_16x16x32_bf16 a[180:183], v[104:107], v[72:75], a[180:183]\n" \
"s_add_u32 m0, s30, 0x0\n" \
"s_nop 0\n" \
"s_mov_b32 s85, 0x300\n" \
"v_mfma_f32_16x16x32_bf16 a[180:183], v[168:171], v[136:139], a[180:183]\n" \
"buffer_load_dwordx4 v24, s[76:79], s85 offen lds\n" \
"v_mfma_f32_16x16x32_bf16 a[176:179], v[100:103], v[72:75], a[176:179]\n" \
"ds_read_b64_tr_b16 v[88:89], v33 offset:32768\n" \
"v_mfma_f32_16x16x32_bf16 a[176:179], v[164:167], v[136:139], a[176:179]\n" \
"ds_read_b128 v[72:75], v28 offset:43008\n" \
"v_mfma_f32_16x16x32_bf16 a[72:75], v[100:103], v[76:79], a[72:75]\n" \
"ds_read_b128 v[136:139], v29 offset:43008\n" \
"v_mfma_f32_16x16x32_bf16 a[72:75], v[164:167], v[140:143], a[72:75]\n" \
"s_add_u32 m0, s30, 0x400\n" \
"s_mov_b32 s84, 0x2700\n" \
"v_mfma_f32_16x16x32_bf16 a[76:79], v[104:107], v[76:79], a[76:79]\n" \
"buffer_load_dwordx4 v25, s[76:79], s84 offen lds\n" \
"v_mfma_f32_16x16x32_bf16 a[76:79], v[168:171], v[140:143], a[76:79]\n" \
"ds_read_b64_tr_b16 v[90:91], v33 offset:33280\n" \
"v_mfma_f32_16x16x32_bf16 a[104:107], v[108:111], v[76:79], a[104:107]\n" \
"ds_read_b64_tr_b16 v[152:153], v33 offset:36864\n" \
"v_mfma_f32_16x16x32_bf16 a[104:107], v[172:175], v[140:143], a[104:107]\n" \
"s_add_u32 m0, s31, 0x0\n" \
"s_nop 0\n" \
"s_mov_b32 s85, 0x300000\n" \
"v_mfma_f32_16x16x32_bf16 a[108:111], v[112:115], v[76:79], a[108:111]\n" \
"buffer_load_dwordx4 v26, s[68:71], s85 offen lds\n" \
"v_mfma_f32_16x16x32_bf16 a[108:111], v[176:179], v[140:143], a[108:111]\n" \
"ds_read_b128 v[76:79], v28 offset:45056\n" \
"v_mfma_f32_16x16x32_bf16 a[172:175], v[112:115], v[80:83], a[172:175]\n" \
"ds_read_b128 v[140:143], v29 offset:45056\n" \
"v_mfma_f32_16x16x32_bf16 a[172:175], v[176:179], v[144:147], a[172:175]\n" \
"s_add_u32 m0, s30, 0x800\n" \
"s_mov_b32 s84, 0x4b00\n" \
"v_mfma_f32_16x16x32_bf16 a[168:171], v[108:111], v[80:83], a[168:171]\n" \
"buffer_load_dwordx4 v24, s[76:79], s84 offen lds\n" \
"v_mfma_f32_16x16x32_bf16 a[168:171], v[172:175], v[144:147], a[168:171]\n" \
"ds_read_b64_tr_b16 v[154:155], v33 offset:37376\n" \
"v_mfma_f32_16x16x32_bf16 a[140:143], v[104:107], v[80:83], a[140:143]\n" \
"ds_read_b64_tr_b16 v[96:97], v35 offset:32768\n" \
"v_mfma_f32_16x16x32_bf16 a[140:143], v[168:171], v[144:147], a[140:143]\n" \
"ds_read_b64_tr_b16 v[98:99], v35 offset:33280\n" \
"v_mfma_f32_16x16x32_bf16 a[136:139], v[100:103], v[80:83], a[136:139]\n" \
"s_cmp_lg_u32 s16, 0\n" \
"s_cselect_b32 s13, s7, s13\n" \
"v_mfma_f32_16x16x32_bf16 a[136:139], v[164:167], v[144:147], a[136:139]\n" \
"ds_read_b128 v[80:83], v28 offset:47104\n" \
"s_lshl_b32 s6, s6, 16\n" \
"s_nop 0\n" \
"v_mfma_f32_16x16x32_bf16 a[80:83], v[100:103], v[60:63], a[80:83]\n" \
"ds_read_b128 v[144:147], v29 offset:47104\n" \
"v_writelane_b32 v11, s13, 39\n" \
"v_mfma_f32_16x16x32_bf16 a[80:83], v[164:167], v[124:127], a[80:83]\n" \
"s_add_u32 m0, s30, 0xc00\n" \
"s_mov_b32 s85, 0x6f00\n" \
"v_mfma_f32_16x16x32_bf16 a[84:87], v[104:107], v[60:63], a[84:87]\n" \
"buffer_load_dwordx4 v25, s[76:79], s85 offen lds\n" \
"v_mfma_f32_16x16x32_bf16 a[84:87], v[168:171], v[124:127], a[84:87]\n" \
"s_add_u32 m0, s31, 0x400\n" \
"s_mov_b32 s84, 0x308000\n" \
"v_mfma_f32_16x16x32_bf16 a[88:91], v[108:111], v[60:63], a[88:91]\n" \
"buffer_load_dwordx4 v26, s[68:71], s84 offen lds\n" \
"v_mfma_f32_16x16x32_bf16 a[88:91], v[172:175], v[124:127], a[88:91]\n" \
"ds_read_b64_tr_b16 v[160:161], v35 offset:36864\n" \
"s_add_i32 s13, s8, 0x4240000\n" \
"v_mfma_f32_16x16x32_bf16 a[92:95], v[112:115], v[60:63], a[92:95]\n" \
"ds_read_b64_tr_b16 v[162:163], v35 offset:37376\n" \
"s_add_i32 s6, s13, s6\n" \
"s_nop 0\n" \
"v_mfma_f32_16x16x32_bf16 a[92:95], v[176:179], v[124:127], a[92:95]\n" \
"ds_read_b128 v[60:63], v28 offset:36864\n" \
"v_writelane_b32 v11, s6, 40\n" \
"v_mfma_f32_16x16x32_bf16 a[124:127], v[112:115], v[64:67], a[124:127]\n" \
"ds_read_b128 v[124:127], v29 offset:36864\n" \
"s_add_i32 s8, s8, 0x42c0000\n" \
"v_mfma_f32_16x16x32_bf16 a[124:127], v[176:179], v[128:131], a[124:127]\n" \
"s_add_u32 m0, s30, 0x1000\n" \
"s_mov_b32 s85, 0x9300\n" \
"v_mfma_f32_16x16x32_bf16 a[120:123], v[108:111], v[64:67], a[120:123]\n" \
"buffer_load_dwordx4 v24, s[76:79], s85 offen lds\n" \
"v_mfma_f32_16x16x32_bf16 a[120:123], v[172:175], v[128:131], a[120:123]\n" \
"s_add_u32 m0, s30, 0x1400\n" \
"s_mov_b32 s84, 0xb700\n" \
"v_mfma_f32_16x16x32_bf16 a[116:119], v[104:107], v[64:67], a[116:119]\n" \
"buffer_load_dwordx4 v25, s[76:79], s84 offen lds\n" \
"v_mfma_f32_16x16x32_bf16 a[116:119], v[168:171], v[128:131], a[116:119]\n" \
"ds_read_b64_tr_b16 v[92:93], v34 offset:32768\n" \
"s_lshl_b32 s6, s12, 10\n" \
"v_mfma_f32_16x16x32_bf16 a[112:115], v[100:103], v[64:67], a[112:115]\n" \
"ds_read_b64_tr_b16 v[94:95], v34 offset:33280\n" \
"s_or_b32 s6, s8, s6\n" \
"v_mfma_f32_16x16x32_bf16 a[112:115], v[164:167], v[128:131], a[112:115]\n" \
"ds_read_b128 v[64:67], v28 offset:38912\n" \
"v_mfma_f32_16x16x32_bf16 a[16:19], v[100:103], v[52:55], a[16:19]\n" \
"ds_read_b128 v[128:131], v29 offset:38912\n" \
"v_mfma_f32_16x16x32_bf16 a[16:19], v[164:167], v[116:119], a[16:19]\n" \
"s_add_u32 m0, s31, 0x800\n" \
"s_mov_b32 s85, 0x310000\n" \
"v_mfma_f32_16x16x32_bf16 a[20:23], v[104:107], v[52:55], a[20:23]\n" \
"buffer_load_dwordx4 v27, s[68:71], s85 offen lds\n" \
"v_mfma_f32_16x16x32_bf16 a[20:23], v[168:171], v[116:119], a[20:23]\n" \
"s_add_u32 m0, s30, 0x1800\n" \
"s_mov_b32 s84, 0xdb00\n" \
"v_mfma_f32_16x16x32_bf16 a[24:27], v[108:111], v[52:55], a[24:27]\n" \
"buffer_load_dwordx4 v24, s[76:79], s84 offen lds\n" \
"v_mfma_f32_16x16x32_bf16 a[24:27], v[172:175], v[116:119], a[24:27]\n" \
"ds_read_b64_tr_b16 v[156:157], v34 offset:36864\n" \
"v_mfma_f32_16x16x32_bf16 a[28:31], v[112:115], v[52:55], a[28:31]\n" \
"ds_read_b64_tr_b16 v[158:159], v34 offset:37376\n" \
"v_mfma_f32_16x16x32_bf16 a[28:31], v[176:179], v[116:119], a[28:31]\n" \
"ds_read_b128 v[52:55], v28 offset:32768\n" \
"v_mfma_f32_16x16x32_bf16 a[60:63], v[112:115], v[56:59], a[60:63]\n" \
"ds_read_b128 v[116:119], v29 offset:32768\n" \
"v_mfma_f32_16x16x32_bf16 a[60:63], v[176:179], v[120:123], a[60:63]\n" \
"s_cmp_lg_u32 s16, 0\n" \
"s_cselect_b32 s6, s7, s6\n" \
"v_mfma_f32_16x16x32_bf16 a[56:59], v[108:111], v[56:59], a[56:59]\n" \
"s_mulk_i32 s4, 0x4800\n" \
"v_writelane_b32 v11, s6, 41\n" \
"v_mfma_f32_16x16x32_bf16 a[56:59], v[172:175], v[120:123], a[56:59]\n" \
"s_add_i32 s6, s14, s10\n" \
"v_writelane_b32 v11, s6, 42\n" \
"v_mfma_f32_16x16x32_bf16 a[52:55], v[104:107], v[56:59], a[52:55]\n" \
"s_add_i32 s6, s9, 0x21c000\n" \
"s_add_i32 s9, s6, s10\n" \
"v_mfma_f32_16x16x32_bf16 a[52:55], v[168:171], v[120:123], a[52:55]\n" \
"s_cmp_lg_u32 s12, 0\n" \
"s_cselect_b32 s6, s6, s14\n" \
"v_mfma_f32_16x16x32_bf16 a[48:51], v[100:103], v[56:59], a[48:51]\n" \
"s_addk_i32 s6, 0x100\n" \
"v_writelane_b32 v11, s9, 43\n" \
"v_mfma_f32_16x16x32_bf16 a[48:51], v[164:167], v[120:123], a[48:51]\n" \
"ds_read_b128 v[56:59], v28 offset:34816\n" \
"ds_read_b128 v[120:123], v29 offset:34816\n" \
"s_waitcnt lgkmcnt(15)\n" \
"s_nop 0\n" \
"v_mfma_f32_16x16x32_bf16 a[0:3], v[84:87], v[68:71], a[0:3]\n" \
"s_add_u32 m0, s30, 0x1c00\n" \
"s_mov_b32 s84, 0xff00\n" \
"v_mfma_f32_16x16x32_bf16 a[0:3], v[148:151], v[132:135], a[0:3]\n" \
"buffer_load_dwordx4 v25, s[76:79], s84 offen lds\n" \
"v_mfma_f32_16x16x32_bf16 a[4:7], v[88:91], v[68:71], a[4:7]\n" \
"ds_read_b64_tr_b16 v[100:101], v36 offset:32768\n" \
"v_mfma_f32_16x16x32_bf16 a[4:7], v[152:155], v[132:135], a[4:7]\n" \
"s_add_u32 m0, s31, 0xc00\n" \
"s_mov_b32 s85, 0x318000\n" \
"v_mfma_f32_16x16x32_bf16 a[36:39], v[96:99], v[68:71], a[36:39]\n" \
"buffer_load_dwordx4 v27, s[68:71], s85 offen lds\n" \
"s_waitcnt lgkmcnt(13)\n" \
"v_mfma_f32_16x16x32_bf16 a[36:39], v[160:163], v[132:135], a[36:39]\n" \
"ds_read_b64_tr_b16 v[102:103], v36 offset:33792\n" \
"s_waitcnt lgkmcnt(10)\n" \
"v_mfma_f32_16x16x32_bf16 a[32:35], v[68:71], v[92:95], a[32:35]\n" \
"ds_read_b64_tr_b16 v[164:165], v36 offset:40960\n" \
"s_waitcnt lgkmcnt(7)\n" \
"s_nop 0\n" \
"v_mfma_f32_16x16x32_bf16 a[32:35], v[132:135], v[156:159], a[32:35]\n" \
"ds_read_b64_tr_b16 v[166:167], v36 offset:41984\n" \
"v_mfma_f32_16x16x32_bf16 a[96:99], v[72:75], v[92:95], a[96:99]\n" \
"ds_read_b64_tr_b16 v[104:105], v37 offset:32768\n" \
".fill (L_pb + 77536 - .) / 4, 4, 0xBF800000\n" \
"L_pp19:\n" \
"s_cbranch_execz L_pret\n" \
"v_mfma_f32_16x16x32_bf16 a[96:99], v[136:139], v[156:159], a[96:99]\n" \
"ds_read_b64_tr_b16 v[106:107], v37 offset:33792\n" \
"v_mfma_f32_16x16x32_bf16 a[100:103], v[96:99], v[72:75], a[100:103]\n" \
"ds_read_b64_tr_b16 v[168:169], v37 offset:40960\n" \
"v_mfma_f32_16x16x32_bf16 a[100:103], v[160:163], v[136:139], a[100:103]\n" \
"ds_read_b64_tr_b16 v[170:171], v37 offset:41984\n" \
"v_mfma_f32_16x16x32_bf16 a[68:71], v[88:91], v[72:75], a[68:71]\n" \
"s_cmp_lg_u32 s16, 0\n" \
"s_cselect_b32 s6, s7, s6\n" \
"v_mfma_f32_16x16x32_bf16 a[68:71], v[152:155], v[136:139], a[68:71]\n" \
"s_add_u32 s88, s42, 0x2000\n" \
"buffer_store_dwordx4 v[244:247], v20, s[80:83], s88 offen offset:0 sc1\n" \
"v_mfma_f32_16x16x32_bf16 a[64:67], v[84:87], v[72:75], a[64:67]\n" \
"ds_read_b64_tr_b16 v[108:109], v38 offset:32768\n" \
"v_writelane_b32 v11, s6, 44\n" \
"v_mfma_f32_16x16x32_bf16 a[64:67], v[148:151], v[136:139], a[64:67]\n" \
"ds_read_b64_tr_b16 v[110:111], v38 offset:33792\n" \
"s_or_b32 s6, s8, s15\n" \
"v_mfma_f32_16x16x32_bf16 a[128:131], v[84:87], v[76:79], a[128:131]\n" \
"ds_read_b64_tr_b16 v[172:173], v38 offset:40960\n" \
"v_writelane_b32 v11, s6, 52\n" \
"v_mfma_f32_16x16x32_bf16 a[128:131], v[148:151], v[140:143], a[128:131]\n" \
"ds_read_b64_tr_b16 v[174:175], v38 offset:41984\n" \
"s_lshl_b32 s6, s3, 17\n" \
"v_mfma_f32_16x16x32_bf16 a[132:135], v[88:91], v[76:79], a[132:135]\n" \
"s_add_i32 s6, s13, s6\n" \
"s_or_b32 s5, s6, s5\n" \
"s_nop 0\n" \
"v_mfma_f32_16x16x32_bf16 a[132:135], v[152:155], v[140:143], a[132:135]\n" \
"s_add_u32 s89, s42, 0x1000\n" \
"buffer_store_dwordx4 v[184:187], v20, s[80:83], s89 offen offset:0 sc1\n" \
"v_mfma_f32_16x16x32_bf16 a[164:167], v[96:99], v[76:79], a[164:167]\n" \
"ds_read_b64_tr_b16 v[112:113], v39 offset:32768\n" \
"s_or_b32 s5, s5, 0x3840\n" \
"v_mfma_f32_16x16x32_bf16 a[164:167], v[160:163], v[140:143], a[164:167]\n" \
"ds_read_b64_tr_b16 v[114:115], v39 offset:33792\n" \
"v_writelane_b32 v11, s5, 6\n" \
"v_mfma_f32_16x16x32_bf16 a[160:163], v[76:79], v[92:95], a[160:163]\n" \
"ds_read_b64_tr_b16 v[176:177], v39 offset:40960\n" \
"s_mov_b32 s5, 0xff0000\n" \
"v_mfma_f32_16x16x32_bf16 a[160:163], v[140:143], v[156:159], a[160:163]\n" \
"s_cmp_lg_u32 s16, 0\n" \
"s_cselect_b32 s5, s17, s5\n" \
"v_mfma_f32_16x16x32_bf16 a[224:227], v[80:83], v[92:95], a[224:227]\n" \
"ds_read_b64_tr_b16 v[178:179], v39 offset:41984\n" \
"v_writelane_b32 v11, s5, 7\n" \
"v_mfma_f32_16x16x32_bf16 a[224:227], v[144:147], v[156:159], a[224:227]\n" \
"s_mul_i32 s5, s3, 0x1200\n" \
"s_add_i32 s4, s5, s4\n" \
"v_mfma_f32_16x16x32_bf16 a[228:231], v[96:99], v[80:83], a[228:231]\n" \
"s_add_u32 s88, s42, 0x1000\n" \
"buffer_store_dwordx4 v[188:191], v20, s[80:83], s88 offen offset:1024 sc1\n" \
"v_mfma_f32_16x16x32_bf16 a[228:231], v[160:163], v[144:147], a[228:231]\n" \
"s_add_i32 s4, s4, s11\n" \
"v_writelane_b32 v11, s4, 8\n" \
"v_mfma_f32_16x16x32_bf16 a[196:199], v[88:91], v[80:83], a[196:199]\n" \
"s_lshl_b32 s4, s2, 16\n" \
"s_add_i32 s5, s4, 0x10000\n" \
"v_mfma_f32_16x16x32_bf16 a[196:199], v[152:155], v[144:147], a[196:199]\n" \
"s_and_b32 s5, s5, 0x70000\n" \
"s_add_i32 s5, s13, s5\n" \
"v_mfma_f32_16x16x32_bf16 a[192:195], v[84:87], v[80:83], a[192:195]\n" \
"v_writelane_b32 v11, s5, 45\n" \
"s_add_i32 s5, s4, 0x20000\n" \
"v_mfma_f32_16x16x32_bf16 a[192:195], v[148:151], v[144:147], a[192:195]\n" \
"s_and_b32 s5, s5, 0x70000\n" \
"s_add_i32 s5, s13, s5\n" \
"v_mfma_f32_16x16x32_bf16 a[8:11], v[88:91], v[64:67], a[8:11]\n" \
"s_bitset1_b32 s5, 11\n" \
"v_writelane_b32 v11, s5, 46\n" \
"v_mfma_f32_16x16x32_bf16 a[8:11], v[152:155], v[128:131], a[8:11]\n" \
"s_add_i32 s5, s4, 0x30000\n" \
"s_and_b32 s5, s5, 0x70000\n" \
"v_mfma_f32_16x16x32_bf16 a[40:43], v[96:99], v[64:67], a[40:43]\n" \
"s_add_i32 s5, s13, s5\n" \
"s_bitset1_b32 s5, 12\n" \
"v_mfma_f32_16x16x32_bf16 a[40:43], v[160:163], v[128:131], a[40:43]\n" \
"v_writelane_b32 v11, s5, 47\n" \
"s_and_b32 s5, s4, 0x70000\n" \
"v_mfma_f32_16x16x32_bf16 a[12:15], v[64:67], v[92:95], a[12:15]\n" \
"s_xor_b32 s5, s5, 0x40000\n" \
"s_add_i32 s5, s13, s5\n" \
"v_mfma_f32_16x16x32_bf16 a[12:15], v[128:131], v[156:159], a[12:15]\n" \
"s_waitcnt vmcnt(3) lgkmcnt(0)\n" \
"s_barrier\n" \
"s_nop 0\n" \
"v_mfma_f32_16x16x32_bf16 a[144:147], v[100:103], v[68:71], a[144:147]\n" \
"s_add_u32 m0, s40, 0x8000\n" \
"s_add_u32 s85, s39, 0xe000\n" \
"v_mfma_f32_16x16x32_bf16 a[144:147], v[164:167], v[132:135], a[144:147]\n" \
"buffer_load_dwordx4 v20, s[80:83], s85 offen sc1 lds\n" \
"v_mfma_f32_16x16x32_bf16 a[148:151], v[104:107], v[68:71], a[148:151]\n" \
"ds_read_b64_tr_b16 v[84:85], v32 offset:0\n" \
"s_or_b32 s5, s5, 0x1800\n" \
"v_mfma_f32_16x16x32_bf16 a[148:151], v[168:171], v[132:135], a[148:151]\n" \
"ds_read_b64_tr_b16 v[86:87], v32 offset:512\n" \
"v_writelane_b32 v11, s5, 48\n" \
"v_mfma_f32_16x16x32_bf16 a[200:203], v[108:111], v[68:71], a[200:203]\n" \
"s_add_u32 m0, s40, 0x9000\n" \
"s_add_u32 s84, s39, 0xf000\n" \
"v_mfma_f32_16x16x32_bf16 a[200:203], v[172:175], v[132:135], a[200:203]\n" \
"buffer_load_dwordx4 v20, s[80:83], s84 offen sc1 lds\n" \
"v_mfma_f32_16x16x32_bf16 a[204:207], v[112:115], v[68:71], a[204:207]\n" \
"ds_read_b64_tr_b16 v[148:149], v32 offset:4096\n" \
"s_add_i32 s5, s4, 0x50000\n" \
"v_mfma_f32_16x16x32_bf16 a[204:207], v[176:179], v[132:135], a[204:207]\n" \
"ds_read_b128 v[68:71], v28 offset:8192\n" \
"s_and_b32 s5, s5, 0x70000\n" \
"v_mfma_f32_16x16x32_bf16 a[236:239], v[112:115], v[72:75], a[236:239]\n" \
"ds_read_b128 v[132:135], v29 offset:8192\n" \
"s_add_i32 s5, s13, s5\n" \
"v_mfma_f32_16x16x32_bf16 a[236:239], v[176:179], v[136:139], a[236:239]\n" \
"s_add_u32 m0, s40, 0xb000\n" \
"s_mov_b32 s85, 0x380\n" \
"v_mfma_f32_16x16x32_bf16 a[232:235], v[108:111], v[72:75], a[232:235]\n" \
"buffer_load_dwordx4 v21, s[76:79], s85 offen lds\n" \
"v_mfma_f32_16x16x32_bf16 a[232:235], v[172:175], v[136:139], a[232:235]\n" \
"ds_read_b64_tr_b16 v[150:151], v32 offset:4608\n" \
"s_bitset1_b32 s5, 13\n" \
"v_mfma_f32_16x16x32_bf16 a[180:183], v[104:107], v[72:75], a[180:183]\n" \
"s_add_u32 m0, s30, 0x8000\n" \
"s_mov_b32 s84, 0x380\n" \
"v_mfma_f32_16x16x32_bf16 a[180:183], v[168:171], v[136:139], a[180:183]\n" \
"buffer_load_dwordx4 v24, s[76:79], s84 offen lds\n" \
"v_mfma_f32_16x16x32_bf16 a[176:179], v[100:103], v[72:75], a[176:179]\n" \
"ds_read_b64_tr_b16 v[88:89], v33 offset:0\n" \
"v_writelane_b32 v11, s5, 49\n" \
"v_mfma_f32_16x16x32_bf16 a[176:179], v[164:167], v[136:139], a[176:179]\n" \
"ds_read_b128 v[72:75], v28 offset:10240\n" \
"s_add_i32 s5, s4, 0x60000\n" \
"v_mfma_f32_16x16x32_bf16 a[72:75], v[100:103], v[76:79], a[72:75]\n" \
"ds_read_b128 v[136:139], v29 offset:10240\n" \
"s_and_b32 s5, s5, 0x70000\n" \
"v_mfma_f32_16x16x32_bf16 a[72:75], v[164:167], v[140:143], a[72:75]\n" \
"s_add_u32 m0, s30, 0x8400\n" \
"s_mov_b32 s85, 0x2780\n" \
"v_mfma_f32_16x16x32_bf16 a[76:79], v[104:107], v[76:79], a[76:79]\n" \
"buffer_load_dwordx4 v25, s[76:79], s85 offen lds\n" \
"v_mfma_f32_16x16x32_bf16 a[76:79], v[168:171], v[140:143], a[76:79]\n" \
"ds_read_b64_tr_b16 v[90:91], v33 offset:512\n" \
"s_add_i32 s4, s4, 0x70000\n" \
"v_mfma_f32_16x16x32_bf16 a[104:107], v[108:111], v[76:79], a[104:107]\n" \
"ds_read_b64_tr_b16 v[152:153], v33 offset:4096\n" \
"s_add_i32 s5, s13, s5\n" \
"s_nop 0\n" \
"v_mfma_f32_16x16x32_bf16 a[104:107], v[172:175], v[140:143], a[104:107]\n" \
"s_add_u32 m0, s31, 0x8000\n" \
"s_mov_b32 s84, 0x380000\n" \
"v_mfma_f32_16x16x32_bf16 a[108:111], v[112:115], v[76:79], a[108:111]\n" \
"buffer_load_dwordx4 v26, s[68:71], s84 offen lds\n" \
"v_mfma_f32_16x16x32_bf16 a[108:111], v[176:179], v[140:143], a[108:111]\n" \
"ds_read_b128 v[76:79], v28 offset:12288\n" \
"s_and_b32 s4, s4, 0x70000\n" \
"v_mfma_f32_16x16x32_bf16 a[172:175], v[112:115], v[80:83], a[172:175]\n" \
"ds_read_b128 v[140:143], v29 offset:12288\n" \
"s_or_b32 s5, s5, 0x2800\n" \
"v_mfma_f32_16x16x32_bf16 a[172:175], v[176:179], v[144:147], a[172:175]\n" \
"s_add_u32 m0, s30, 0x8800\n" \
"s_mov_b32 s85, 0x4b80\n" \
"v_mfma_f32_16x16x32_bf16 a[168:171], v[108:111], v[80:83], a[168:171]\n" \
"buffer_load_dwordx4 v24, s[76:79], s85 offen lds\n" \
"v_mfma_f32_16x16x32_bf16 a[168:171], v[172:175], v[144:147], a[168:171]\n" \
"ds_read_b64_tr_b16 v[154:155], v33 offset:4608\n" \
"v_writelane_b32 v11, s5, 50\n" \
"v_mfma_f32_16x16x32_bf16 a[140:143], v[104:107], v[80:83], a[140:143]\n" \
"ds_read_b64_tr_b16 v[96:97], v35 offset:0\n" \
"s_add_i32 s13, s13, s4\n" \
"v_mfma_f32_16x16x32_bf16 a[140:143], v[168:171], v[144:147], a[140:143]\n" \
"ds_read_b64_tr_b16 v[98:99], v35 offset:512\n" \
"s_or_b32 s4, s13, 0x3000\n" \
"v_mfma_f32_16x16x32_bf16 a[136:139], v[100:103], v[80:83], a[136:139]\n" \
"v_writelane_b32 v11, s4, 51\n" \
"s_movk_i32 s11, 0x4000\n" \
"v_mfma_f32_16x16x32_bf16 a[136:139], v[164:167], v[144:147], a[136:139]\n" \
"ds_read_b128 v[80:83], v28 offset:14336\n" \
"s_lshl_b32 s4, s2, 2\n" \
"v_mfma_f32_16x16x32_bf16 a[80:83], v[100:103], v[60:63], a[80:83]\n" \
"ds_read_b128 v[144:147], v29 offset:14336\n" \
"s_and_b32 s4, s4, 28\n" \
"v_mfma_f32_16x16x32_bf16 a[80:83], v[164:167], v[124:127], a[80:83]\n" \
"s_add_u32 m0, s30, 0x8c00\n" \
"s_mov_b32 s84, 0x6f80\n" \
"v_mfma_f32_16x16x32_bf16 a[84:87], v[104:107], v[60:63], a[84:87]\n" \
"buffer_load_dwordx4 v25, s[76:79], s84 offen lds\n" \
"v_mfma_f32_16x16x32_bf16 a[84:87], v[168:171], v[124:127], a[84:87]\n" \
"s_add_u32 m0, s31, 0x8400\n" \
"s_mov_b32 s85, 0x388000\n" \
"v_mfma_f32_16x16x32_bf16 a[88:91], v[108:111], v[60:63], a[88:91]\n" \
"buffer_load_dwordx4 v26, s[68:71], s85 offen lds\n" \
"v_mfma_f32_16x16x32_bf16 a[88:91], v[172:175], v[124:127], a[88:91]\n" \
"ds_read_b64_tr_b16 v[160:161], v35 offset:4096\n" \
"s_bfe_u32 s5, s2, 0x20003\n" \
"v_mfma_f32_16x16x32_bf16 a[92:95], v[112:115], v[60:63], a[92:95]\n" \
"ds_read_b64_tr_b16 v[162:163], v35 offset:4608\n" \
"s_or_b32 s4, s4, s5\n" \
"v_mfma_f32_16x16x32_bf16 a[92:95], v[176:179], v[124:127], a[92:95]\n" \
"ds_read_b128 v[60:63], v28 offset:4096\n" \
"s_lshr_b32 s5, s2, 5\n" \
"v_mfma_f32_16x16x32_bf16 a[124:127], v[112:115], v[64:67], a[124:127]\n" \
"ds_read_b128 v[124:127], v29 offset:4096\n" \
"s_lshr_b32 s19, s3, 1\n" \
"s_nop 0\n" \
"v_mfma_f32_16x16x32_bf16 a[124:127], v[176:179], v[128:131], a[124:127]\n" \
"s_add_u32 m0, s30, 0x9000\n" \
"s_mov_b32 s84, 0x9380\n" \
"v_mfma_f32_16x16x32_bf16 a[120:123], v[108:111], v[64:67], a[120:123]\n" \
"buffer_load_dwordx4 v24, s[76:79], s84 offen lds\n" \
"v_mfma_f32_16x16x32_bf16 a[120:123], v[172:175], v[128:131], a[120:123]\n" \
"s_add_u32 m0, s30, 0x9400\n" \
"s_mov_b32 s85, 0xb780\n" \
"v_mfma_f32_16x16x32_bf16 a[116:119], v[104:107], v[64:67], a[116:119]\n" \
"buffer_load_dwordx4 v25, s[76:79], s85 offen lds\n" \
"v_mfma_f32_16x16x32_bf16 a[116:119], v[168:171], v[128:131], a[116:119]\n" \
"ds_read_b64_tr_b16 v[92:93], v34 offset:0\n" \
"s_lshl_b32 s18, s5, 1\n" \
"v_mfma_f32_16x16x32_bf16 a[112:115], v[100:103], v[64:67], a[112:115]\n" \
"ds_read_b64_tr_b16 v[94:95], v34 offset:512\n" \
"s_lshl_b32 s6, s4, 20\n" \
"v_mfma_f32_16x16x32_bf16 a[112:115], v[164:167], v[128:131], a[112:115]\n" \
"ds_read_b128 v[64:67], v28 offset:6144\n" \
"s_lshl_b32 s7, s4, 8\n" \
"v_mfma_f32_16x16x32_bf16 a[16:19], v[100:103], v[52:55], a[16:19]\n" \
"ds_read_b128 v[128:131], v29 offset:6144\n" \
"s_and_b32 s13, s3, 1\n" \
"v_mfma_f32_16x16x32_bf16 a[16:19], v[164:167], v[116:119], a[16:19]\n" \
"s_add_u32 m0, s31, 0x8800\n" \
"s_mov_b32 s84, 0x390000\n" \
"v_mfma_f32_16x16x32_bf16 a[20:23], v[104:107], v[52:55], a[20:23]\n" \
"buffer_load_dwordx4 v27, s[68:71], s84 offen lds\n" \
"v_mfma_f32_16x16x32_bf16 a[20:23], v[168:171], v[116:119], a[20:23]\n" \
"s_add_u32 m0, s30, 0x9800\n" \
"s_mov_b32 s85, 0xdb80\n" \
"v_mfma_f32_16x16x32_bf16 a[24:27], v[108:111], v[52:55], a[24:27]\n" \
"buffer_load_dwordx4 v24, s[76:79], s85 offen lds\n" \
"v_mfma_f32_16x16x32_bf16 a[24:27], v[172:175], v[116:119], a[24:27]\n" \
"ds_read_b64_tr_b16 v[156:157], v34 offset:4096\n" \
"s_add_i32 s14, s18, s19\n" \
"v_mfma_f32_16x16x32_bf16 a[28:31], v[112:115], v[52:55], a[28:31]\n" \
"ds_read_b64_tr_b16 v[158:159], v34 offset:4608\n" \
"v_mfma_f32_16x16x32_bf16 a[28:31], v[176:179], v[116:119], a[28:31]\n" \
"ds_read_b128 v[52:55], v28 offset:0\n" \
"v_mfma_f32_16x16x32_bf16 a[60:63], v[112:115], v[56:59], a[60:63]\n" \
"ds_read_b128 v[116:119], v29 offset:0\n" \
"v_mfma_f32_16x16x32_bf16 a[60:63], v[176:179], v[120:123], a[60:63]\n" \
"s_add_u32 s6, s26, s6\n" \
"s_addc_u32 s9, s27, 0\n" \
"v_mfma_f32_16x16x32_bf16 a[56:59], v[108:111], v[56:59], a[56:59]\n" \
"s_add_u32 s8, s6, 0x1000000\n" \
"s_addc_u32 s6, s9, 0\n" \
"v_mfma_f32_16x16x32_bf16 a[56:59], v[172:175], v[120:123], a[56:59]\n" \
"s_and_b32 s9, s6, 0xffff\n" \
"s_lshl_b32 s6, s5, 16\n" \
"v_mfma_f32_16x16x32_bf16 a[52:55], v[104:107], v[56:59], a[52:55]\n" \
"s_sub_i32 s10, 0x100000, s6\n" \
"s_mov_b32 s6, 0x8000\n" \
"v_mfma_f32_16x16x32_bf16 a[52:55], v[168:171], v[120:123], a[52:55]\n" \
"s_cmp_lg_u32 s5, 0\n" \
"s_cselect_b32 s17, s11, s6\n" \
"v_mfma_f32_16x16x32_bf16 a[48:51], v[100:103], v[56:59], a[48:51]\n" \
"s_lshl_b32 s6, s3, 1\n" \
"s_lshl_b32 s5, s5, 3\n" \
"v_mfma_f32_16x16x32_bf16 a[48:51], v[164:167], v[120:123], a[48:51]\n" \
"ds_read_b128 v[56:59], v28 offset:2048\n" \
"ds_read_b128 v[120:123], v29 offset:2048\n" \
"s_waitcnt lgkmcnt(15)\n" \
"v_mfma_f32_16x16x32_bf16 a[0:3], v[84:87], v[68:71], a[0:3]\n" \
"s_add_u32 m0, s30, 0x9c00\n" \
"s_mov_b32 s85, 0xff80\n" \
"v_mfma_f32_16x16x32_bf16 a[0:3], v[148:151], v[132:135], a[0:3]\n" \
"buffer_load_dwordx4 v25, s[76:79], s85 offen lds\n" \
"v_mfma_f32_16x16x32_bf16 a[4:7], v[88:91], v[68:71], a[4:7]\n" \
"ds_read_b64_tr_b16 v[100:101], v36 offset:0\n" \
"s_and_b32 s6, s6, 4\n" \
"v_mfma_f32_16x16x32_bf16 a[4:7], v[152:155], v[132:135], a[4:7]\n" \
"s_add_u32 m0, s31, 0x8c00\n" \
"s_mov_b32 s84, 0x398000\n" \
"v_mfma_f32_16x16x32_bf16 a[36:39], v[96:99], v[68:71], a[36:39]\n" \
"buffer_load_dwordx4 v27, s[68:71], s84 offen lds\n" \
"s_waitcnt lgkmcnt(13)\n" \
"v_mfma_f32_16x16x32_bf16 a[36:39], v[160:163], v[132:135], a[36:39]\n" \
"ds_read_b64_tr_b16 v[102:103], v36 offset:1024\n" \
"s_or_b32 s12, s5, s6\n" \
"s_waitcnt lgkmcnt(10)\n" \
"v_mfma_f32_16x16x32_bf16 a[32:35], v[68:71], v[92:95], a[32:35]\n" \
"ds_read_b64_tr_b16 v[164:165], v36 offset:8192\n" \
"s_sub_i32 s5, s13, s5\n" \
"s_waitcnt lgkmcnt(7)\n" \
"v_mfma_f32_16x16x32_bf16 a[32:35], v[132:135], v[156:159], a[32:35]\n" \
"ds_read_b64_tr_b16 v[166:167], v36 offset:9216\n" \
"s_or_b32 s5, s5, s6\n" \
"v_mfma_f32_16x16x32_bf16 a[96:99], v[72:75], v[92:95], a[96:99]\n" \
"ds_read_b64_tr_b16 v[104:105], v37 offset:0\n" \
"s_or_b32 s6, s12, s13\n" \
"v_mfma_f32_16x16x32_bf16 a[96:99], v[136:139], v[156:159], a[96:99]\n" \
"ds_read_b64_tr_b16 v[106:107], v37 offset:1024\n" \
"s_lshl_b32 s4, s4, 2\n" \
"v_mfma_f32_16x16x32_bf16 a[100:103], v[96:99], v[72:75], a[100:103]\n" \
"ds_read_b64_tr_b16 v[168:169], v37 offset:8192\n" \
"s_mulk_i32 s6, 0x92\n" \
"v_mfma_f32_16x16x32_bf16 a[100:103], v[160:163], v[136:139], a[100:103]\n" \
"ds_read_b64_tr_b16 v[170:171], v37 offset:9216\n" \
"s_add_i32 s4, s4, 18\n" \
"v_mfma_f32_16x16x32_bf16 a[68:71], v[88:91], v[72:75], a[68:71]\n" \
"s_mulk_i32 s5, 0x92\n" \
"s_add_i32 s6, s4, s6\n" \
"v_mfma_f32_16x16x32_bf16 a[68:71], v[152:155], v[136:139], a[68:71]\n" \
"s_add_u32 s89, s42, 0x1000\n" \
"buffer_store_dwordx4 v[192:195], v20, s[80:83], s89 offen offset:2048 sc1\n" \
"v_mfma_f32_16x16x32_bf16 a[64:67], v[84:87], v[72:75], a[64:67]\n" \
"ds_read_b64_tr_b16 v[108:109], v38 offset:0\n" \
"s_add_i32 s5, s5, s4\n" \
"v_mfma_f32_16x16x32_bf16 a[64:67], v[148:151], v[136:139], a[64:67]\n" \
"ds_read_b64_tr_b16 v[110:111], v38 offset:1024\n" \
"s_lshl_b32 s4, s3, 11\n" \
"v_mfma_f32_16x16x32_bf16 a[128:131], v[84:87], v[76:79], a[128:131]\n" \
"ds_read_b64_tr_b16 v[172:173], v38 offset:8192\n" \
"s_lshl_b32 s15, s13, 12\n" \
"v_mfma_f32_16x16x32_bf16 a[128:131], v[148:151], v[140:143], a[128:131]\n" \
"ds_read_b64_tr_b16 v[174:175], v38 offset:9216\n" \
"s_lshl_b32 s16, s19, 14\n" \
"s_nop 0\n" \
"v_mfma_f32_16x16x32_bf16 a[132:135], v[88:91], v[76:79], a[132:135]\n" \
"s_mul_i32 s19, s19, 0xe000\n" \
"s_mov_b32 s11, 0x20000\n" \
"v_mfma_f32_16x16x32_bf16 a[132:135], v[152:155], v[140:143], a[132:135]\n" \
"s_add_u32 s88, s42, 0x1000\n" \
"buffer_store_dwordx4 v[196:199], v20, s[80:83], s88 offen offset:3072 sc1\n" \
"v_mfma_f32_16x16x32_bf16 a[164:167], v[96:99], v[76:79], a[164:167]\n" \
"ds_read_b64_tr_b16 v[112:113], v39 offset:0\n" \
"s_or_b32 s12, s4, 0xc000\n" \
"v_mfma_f32_16x16x32_bf16 a[164:167], v[160:163], v[140:143], a[164:167]\n" \
"ds_read_b64_tr_b16 v[114:115], v39 offset:1024\n" \
"s_lshl_b32 s4, s3, 8\n" \
"v_mfma_f32_16x16x32_bf16 a[160:163], v[76:79], v[92:95], a[160:163]\n" \
"ds_read_b64_tr_b16 v[176:177], v39 offset:8192\n" \
"s_or_b32 s22, s15, s16\n" \
"v_mfma_f32_16x16x32_bf16 a[160:163], v[140:143], v[156:159], a[160:163]\n" \
"s_or_b32 s23, s19, s15\n" \
"s_lshl_b32 s60, s6, 10\n" \
"v_mfma_f32_16x16x32_bf16 a[224:227], v[80:83], v[92:95], a[224:227]\n" \
"ds_read_b64_tr_b16 v[178:179], v39 offset:9216\n" \
"s_lshl_b32 s61, s5, 10\n" \
"v_mfma_f32_16x16x32_bf16 a[224:227], v[144:147], v[156:159], a[224:227]\n" \
"s_lshl_b32 s14, s14, 15\n" \
"s_or_b32 s20, s4, 0x26c00\n" \
"v_mfma_f32_16x16x32_bf16 a[228:231], v[96:99], v[80:83], a[228:231]\n" \
"s_add_u32 s89, s42, 0x3000\n" \
"buffer_store_dwordx4 v[200:203], v20, s[80:83], s89 offen offset:0 sc1\n" \
"v_mfma_f32_16x16x32_bf16 a[228:231], v[160:163], v[144:147], a[228:231]\n" \
"s_or_b32 s13, s22, 0x8000\n" \
"s_add_i32 s22, s22, 0x14000\n" \
"v_mfma_f32_16x16x32_bf16 a[196:199], v[88:91], v[80:83], a[196:199]\n" \
"s_bitset1_b32 s23, 16\n" \
"s_or_b32 s24, s7, 0x5be8020\n" \
"v_mfma_f32_16x16x32_bf16 a[196:199], v[152:155], v[144:147], a[196:199]\n" \
"s_and_b32 s57, s27, 0xffff\n" \
"s_mov_b32 s58, 0x6000000\n" \
"v_mfma_f32_16x16x32_bf16 a[192:195], v[84:87], v[80:83], a[192:195]\n" \
"s_mov_b32 s56, s26\n" \
"s_mov_b32 s59, s11\n" \
"v_mfma_f32_16x16x32_bf16 a[192:195], v[148:151], v[144:147], a[192:195]\n" \
"s_add_i32 s60, s60, 0x3000000\n" \
"s_add_i32 s61, s61, 0x411c000\n" \
"v_mfma_f32_16x16x32_bf16 a[8:11], v[88:91], v[64:67], a[8:11]\n" \
"s_sub_i32 s19, 14, s18\n" \
"s_mov_b32 s21, 0x71800000\n" \
"v_mfma_f32_16x16x32_bf16 a[8:11], v[152:155], v[128:131], a[8:11]\n" \
"v_mfma_f32_16x16x32_bf16 a[40:43], v[96:99], v[64:67], a[40:43]\n" \
"v_mfma_f32_16x16x32_bf16 a[40:43], v[160:163], v[128:131], a[40:43]\n" \
"v_mfma_f32_16x16x32_bf16 a[12:15], v[64:67], v[92:95], a[12:15]\n" \
"v_mfma_f32_16x16x32_bf16 a[12:15], v[128:131], v[156:159], a[12:15]\n" \
"s_waitcnt vmcnt(3) lgkmcnt(0)\n" \
"s_barrier\n" \
"v_mfma_f32_16x16x32_bf16 a[144:147], v[100:103], v[68:71], a[144:147]\n" \
"s_add_u32 m0, s40, 0x0\n" \
"s_nop 0\n" \
"s_add_u32 s84, s39, 0x10000\n" \
"v_mfma_f32_16x16x32_bf16 a[144:147], v[164:167], v[132:135], a[144:147]\n" \
"buffer_load_dwordx4 v20, s[80:83], s84 offen sc1 lds\n" \
"v_mfma_f32_16x16x32_bf16 a[148:151], v[104:107], v[68:71], a[148:151]\n" \
"ds_read_b64_tr_b16 v[84:85], v32 offset:32768\n" \
"v_mfma_f32_16x16x32_bf16 a[148:151], v[168:171], v[132:135], a[148:151]\n" \
"ds_read_b64_tr_b16 v[86:87], v32 offset:33280\n" \
"v_mfma_f32_16x16x32_bf16 a[200:203], v[108:111], v[68:71], a[200:203]\n" \
"s_add_u32 m0, s40, 0x1000\n" \
"s_add_u32 s85, s39, 0x11000\n" \
"v_mfma_f32_16x16x32_bf16 a[200:203], v[172:175], v[132:135], a[200:203]\n" \
"buffer_load_dwordx4 v20, s[80:83], s85 offen sc1 lds\n" \
"v_mfma_f32_16x16x32_bf16 a[204:207], v[112:115], v[68:71], a[204:207]\n" \
"ds_read_b64_tr_b16 v[148:149], v32 offset:36864\n" \
"v_mfma_f32_16x16x32_bf16 a[204:207], v[176:179], v[132:135], a[204:207]\n" \
"ds_read_b128 v[68:71], v28 offset:40960\n" \
"v_mfma_f32_16x16x32_bf16 a[236:239], v[112:115], v[72:75], a[236:239]\n" \
"ds_read_b128 v[132:135], v29 offset:40960\n" \
"v_mfma_f32_16x16x32_bf16 a[236:239], v[176:179], v[136:139], a[236:239]\n" \
"s_add_u32 m0, s40, 0x3000\n" \
"s_mov_b32 s84, 0x400\n" \
"v_mfma_f32_16x16x32_bf16 a[232:235], v[108:111], v[72:75], a[232:235]\n" \
"buffer_load_dwordx4 v21, s[76:79], s84 offen lds\n" \
"v_mfma_f32_16x16x32_bf16 a[232:235], v[172:175], v[136:139], a[232:235]\n" \
"ds_read_b64_tr_b16 v[150:151], v32 offset:37376\n" \
"v_mfma_f32_16x16x32_bf16 a[180:183], v[104:107], v[72:75], a[180:183]\n" \
"s_add_u32 m0, s30, 0x0\n" \
"s_nop 0\n" \
"s_mov_b32 s85, 0x400\n" \
"v_mfma_f32_16x16x32_bf16 a[180:183], v[168:171], v[136:139], a[180:183]\n" \
"buffer_load_dwordx4 v24, s[76:79], s85 offen lds\n" \
"v_mfma_f32_16x16x32_bf16 a[176:179], v[100:103], v[72:75], a[176:179]\n" \
"ds_read_b64_tr_b16 v[88:89], v33 offset:32768\n" \
"v_mfma_f32_16x16x32_bf16 a[176:179], v[164:167], v[136:139], a[176:179]\n" \
"ds_read_b128 v[72:75], v28 offset:43008\n" \
"v_mfma_f32_16x16x32_bf16 a[72:75], v[100:103], v[76:79], a[72:75]\n" \
"ds_read_b128 v[136:139], v29 offset:43008\n" \
"v_mfma_f32_16x16x32_bf16 a[72:75], v[164:167], v[140:143], a[72:75]\n" \
"s_add_u32 m0, s30, 0x400\n" \
"s_mov_b32 s84, 0x2800\n" \
"v_mfma_f32_16x16x32_bf16 a[76:79], v[104:107], v[76:79], a[76:79]\n" \
"buffer_load_dwordx4 v25, s[76:79], s84 offen lds\n" \
"v_mfma_f32_16x16x32_bf16 a[76:79], v[168:171], v[140:143], a[76:79]\n" \
"ds_read_b64_tr_b16 v[90:91], v33 offset:33280\n" \
"v_mfma_f32_16x16x32_bf16 a[104:107], v[108:111], v[76:79], a[104:107]\n" \
"ds_read_b64_tr_b16 v[152:153], v33 offset:36864\n" \
"v_mfma_f32_16x16x32_bf16 a[104:107], v[172:175], v[140:143], a[104:107]\n" \
"s_add_u32 m0, s31, 0x0\n" \
"s_nop 0\n" \
"s_mov_b32 s85, 0x400000\n" \
"v_mfma_f32_16x16x32_bf16 a[108:111], v[112:115], v[76:79], a[108:111]\n" \
"buffer_load_dwordx4 v26, s[68:71], s85 offen lds\n" \
"v_mfma_f32_16x16x32_bf16 a[108:111], v[176:179], v[140:143], a[108:111]\n" \
"ds_read_b128 v[76:79], v28 offset:45056\n" \
"v_mfma_f32_16x16x32_bf16 a[172:175], v[112:115], v[80:83], a[172:175]\n" \
"ds_read_b128 v[140:143], v29 offset:45056\n" \
"v_mfma_f32_16x16x32_bf16 a[172:175], v[176:179], v[144:147], a[172:175]\n" \
"s_add_u32 m0, s30, 0x800\n" \
"s_mov_b32 s84, 0x4c00\n" \
"v_mfma_f32_16x16x32_bf16 a[168:171], v[108:111], v[80:83], a[168:171]\n" \
"buffer_load_dwordx4 v24, s[76:79], s84 offen lds\n" \
"v_mfma_f32_16x16x32_bf16 a[168:171], v[172:175], v[144:147], a[168:171]\n" \
"ds_read_b64_tr_b16 v[154:155], v33 offset:37376\n" \
"v_mfma_f32_16x16x32_bf16 a[140:143], v[104:107], v[80:83], a[140:143]\n" \
"ds_read_b64_tr_b16 v[96:97], v35 offset:32768\n" \
"v_mfma_f32_16x16x32_bf16 a[140:143], v[168:171], v[144:147], a[140:143]\n" \
"ds_read_b64_tr_b16 v[98:99], v35 offset:33280\n" \
"v_mfma_f32_16x16x32_bf16 a[136:139], v[100:103], v[80:83], a[136:139]\n" \
"v_mfma_f32_16x16x32_bf16 a[136:139], v[164:167], v[144:147], a[136:139]\n" \
"ds_read_b128 v[80:83], v28 offset:47104\n" \
"v_mfma_f32_16x16x32_bf16 a[80:83], v[100:103], v[60:63], a[80:83]\n" \
"ds_read_b128 v[144:147], v29 offset:47104\n" \
"v_mfma_f32_16x16x32_bf16 a[80:83], v[164:167], v[124:127], a[80:83]\n" \
"s_add_u32 m0, s30, 0xc00\n" \
"s_mov_b32 s85, 0x7000\n" \
"v_mfma_f32_16x16x32_bf16 a[84:87], v[104:107], v[60:63], a[84:87]\n" \
"buffer_load_dwordx4 v25, s[76:79], s85 offen lds\n" \
"v_mfma_f32_16x16x32_bf16 a[84:87], v[168:171], v[124:127], a[84:87]\n" \
"s_add_u32 m0, s31, 0x400\n" \
"s_mov_b32 s84, 0x408000\n" \
"v_mfma_f32_16x16x32_bf16 a[88:91], v[108:111], v[60:63], a[88:91]\n" \
"buffer_load_dwordx4 v26, s[68:71], s84 offen lds\n" \
"v_mfma_f32_16x16x32_bf16 a[88:91], v[172:175], v[124:127], a[88:91]\n" \
"ds_read_b64_tr_b16 v[160:161], v35 offset:36864\n" \
"v_mfma_f32_16x16x32_bf16 a[92:95], v[112:115], v[60:63], a[92:95]\n" \
"ds_read_b64_tr_b16 v[162:163], v35 offset:37376\n" \
"v_mfma_f32_16x16x32_bf16 a[92:95], v[176:179], v[124:127], a[92:95]\n" \
"ds_read_b128 v[60:63], v28 offset:36864\n" \
"v_mfma_f32_16x16x32_bf16 a[124:127], v[112:115], v[64:67], a[124:127]\n" \
"ds_read_b128 v[124:127], v29 offset:36864\n" \
"v_mfma_f32_16x16x32_bf16 a[124:127], v[176:179], v[128:131], a[124:127]\n" \
"s_add_u32 m0, s30, 0x1000\n" \
"s_mov_b32 s85, 0x9400\n" \
"v_mfma_f32_16x16x32_bf16 a[120:123], v[108:111], v[64:67], a[120:123]\n" \
"buffer_load_dwordx4 v24, s[76:79], s85 offen lds\n" \
"v_mfma_f32_16x16x32_bf16 a[120:123], v[172:175], v[128:131], a[120:123]\n" \
"s_add_u32 m0, s30, 0x1400\n" \
"s_mov_b32 s84, 0xb800\n" \
"v_mfma_f32_16x16x32_bf16 a[116:119], v[104:107], v[64:67], a[116:119]\n" \
"buffer_load_dwordx4 v25, s[76:79], s84 offen lds\n" \
"v_mfma_f32_16x16x32_bf16 a[116:119], v[168:171], v[128:131], a[116:119]\n" \
"ds_read_b64_tr_b16 v[92:93], v34 offset:32768\n" \
"v_mfma_f32_16x16x32_bf16 a[112:115], v[100:103], v[64:67], a[112:115]\n" \
"ds_read_b64_tr_b16 v[94:95], v34 offset:33280\n" \
"v_mfma_f32_16x16x32_bf16 a[112:115], v[164:167], v[128:131], a[112:115]\n" \
"ds_read_b128 v[64:67], v28 offset:38912\n" \
"v_mfma_f32_16x16x32_bf16 a[16:19], v[100:103], v[52:55], a[16:19]\n" \
"ds_read_b128 v[128:131], v29 offset:38912\n" \
"v_mfma_f32_16x16x32_bf16 a[16:19], v[164:167], v[116:119], a[16:19]\n" \
"s_add_u32 m0, s31, 0x800\n" \
"s_mov_b32 s85, 0x410000\n" \
"v_mfma_f32_16x16x32_bf16 a[20:23], v[104:107], v[52:55], a[20:23]\n" \
"buffer_load_dwordx4 v27, s[68:71], s85 offen lds\n" \
"v_mfma_f32_16x16x32_bf16 a[20:23], v[168:171], v[116:119], a[20:23]\n" \
"s_add_u32 m0, s30, 0x1800\n" \
"s_mov_b32 s84, 0xdc00\n" \
"v_mfma_f32_16x16x32_bf16 a[24:27], v[108:111], v[52:55], a[24:27]\n" \
"buffer_load_dwordx4 v24, s[76:79], s84 offen lds\n" \
"v_mfma_f32_16x16x32_bf16 a[24:27], v[172:175], v[116:119], a[24:27]\n" \
"ds_read_b64_tr_b16 v[156:157], v34 offset:36864\n" \
"v_mfma_f32_16x16x32_bf16 a[28:31], v[112:115], v[52:55], a[28:31]\n" \
"ds_read_b64_tr_b16 v[158:159], v34 offset:37376\n" \
"v_mfma_f32_16x16x32_bf16 a[28:31], v[176:179], v[116:119], a[28:31]\n" \
"ds_read_b128 v[52:55], v28 offset:32768\n" \
"v_mfma_f32_16x16x32_bf16 a[60:63], v[112:115], v[56:59], a[60:63]\n" \
"ds_read_b128 v[116:119], v29 offset:32768\n" \
"v_mfma_f32_16x16x32_bf16 a[60:63], v[176:179], v[120:123], a[60:63]\n" \
"v_mfma_f32_16x16x32_bf16 a[56:59], v[108:111], v[56:59], a[56:59]\n" \
"v_mfma_f32_16x16x32_bf16 a[56:59], v[172:175], v[120:123], a[56:59]\n" \
"v_mfma_f32_16x16x32_bf16 a[52:55], v[104:107], v[56:59], a[52:55]\n" \
"v_mfma_f32_16x16x32_bf16 a[52:55], v[168:171], v[120:123], a[52:55]\n" \
"v_mfma_f32_16x16x32_bf16 a[48:51], v[100:103], v[56:59], a[48:51]\n" \
"v_mfma_f32_16x16x32_bf16 a[48:51], v[164:167], v[120:123], a[48:51]\n" \
"ds_read_b128 v[56:59], v28 offset:34816\n" \
"ds_read_b128 v[120:123], v29 offset:34816\n" \
"s_waitcnt lgkmcnt(15)\n" \
"s_nop 0\n" \
"v_mfma_f32_16x16x32_bf16 a[0:3], v[84:87], v[68:71], a[0:3]\n" \
"s_add_u32 m0, s30, 0x1c00\n" \
"s_mov_b32 s84, 0x10000\n" \
"v_mfma_f32_16x16x32_bf16 a[0:3], v[148:151], v[132:135], a[0:3]\n" \
"buffer_load_dwordx4 v25, s[76:79], s84 offen lds\n" \
"v_mfma_f32_16x16x32_bf16 a[4:7], v[88:91], v[68:71], a[4:7]\n" \
"ds_read_b64_tr_b16 v[100:101], v36 offset:32768\n" \
"v_mfma_f32_16x16x32_bf16 a[4:7], v[152:155], v[132:135], a[4:7]\n" \
"s_add_u32 m0, s31, 0xc00\n" \
"s_mov_b32 s85, 0x418000\n" \
"v_mfma_f32_16x16x32_bf16 a[36:39], v[96:99], v[68:71], a[36:39]\n" \
"buffer_load_dwordx4 v27, s[68:71], s85 offen lds\n" \
"s_waitcnt lgkmcnt(13)\n" \
"v_mfma_f32_16x16x32_bf16 a[36:39], v[160:163], v[132:135], a[36:39]\n" \
"ds_read_b64_tr_b16 v[102:103], v36 offset:33792\n" \
"s_waitcnt lgkmcnt(10)\n" \
"v_mfma_f32_16x16x32_bf16 a[32:35], v[68:71], v[92:95], a[32:35]\n" \
"ds_read_b64_tr_b16 v[164:165], v36 offset:40960\n" \
"s_waitcnt lgkmcnt(7)\n" \
"s_nop 0\n" \
"v_mfma_f32_16x16x32_bf16 a[32:35], v[132:135], v[156:159], a[32:35]\n" \
"ds_read_b64_tr_b16 v[166:167], v36 offset:41984\n" \
"v_mfma_f32_16x16x32_bf16 a[96:99], v[72:75], v[92:95], a[96:99]\n" \
"ds_read_b64_tr_b16 v[104:105], v37 offset:32768\n" \
"v_mfma_f32_16x16x32_bf16 a[96:99], v[136:139], v[156:159], a[96:99]\n" \
"ds_read_b64_tr_b16 v[106:107], v37 offset:33792\n" \
"v_mfma_f32_16x16x32_bf16 a[100:103], v[96:99], v[72:75], a[100:103]\n" \
"ds_read_b64_tr_b16 v[168:169], v37 offset:40960\n" \
"v_mfma_f32_16x16x32_bf16 a[100:103], v[160:163], v[136:139], a[100:103]\n" \
"ds_read_b64_tr_b16 v[170:171], v37 offset:41984\n" \
"v_mfma_f32_16x16x32_bf16 a[68:71], v[88:91], v[72:75], a[68:71]\n" \
"v_mfma_f32_16x16x32_bf16 a[68:71], v[152:155], v[136:139], a[68:71]\n" \
"s_add_u32 s88, s42, 0x3000\n" \
"buffer_store_dwordx4 v[204:207], v20, s[80:83], s88 offen offset:1024 sc1\n" \
"v_mfma_f32_16x16x32_bf16 a[64:67], v[84:87], v[72:75], a[64:67]\n" \
"ds_read_b64_tr_b16 v[108:109], v38 offset:32768\n" \
"v_mfma_f32_16x16x32_bf16 a[64:67], v[148:151], v[136:139], a[64:67]\n" \
"ds_read_b64_tr_b16 v[110:111], v38 offset:33792\n" \
"v_mfma_f32_16x16x32_bf16 a[128:131], v[84:87], v[76:79], a[128:131]\n" \
"ds_read_b64_tr_b16 v[172:173], v38 offset:40960\n" \
"v_mfma_f32_16x16x32_bf16 a[128:131], v[148:151], v[140:143], a[128:131]\n" \
"ds_read_b64_tr_b16 v[174:175], v38 offset:41984\n" \
"v_mfma_f32_16x16x32_bf16 a[132:135], v[88:91], v[76:79], a[132:135]\n" \
".fill (L_pb + 81632 - .) / 4, 4, 0xBF800000\n" \
"L_pp20:\n" \
"s_cbranch_execz L_pret\n" \
"v_lshlrev_b32_e32 v45, 3, v20\n" \
"v_add_u32_e64 v45, s30, v45\n" \
"v_mfma_f32_16x16x32_bf16 a[132:135], v[152:155], v[140:143], a[132:135]\n" \
"s_add_u32 s89, s42, 0x3000\n" \
"buffer_store_dwordx4 v[208:211], v20, s[80:83], s89 offen offset:2048 sc1\n" \
"s_getpc_b64 s[86:87]\n" \
"L_ct1_%=:\n" \
"s_add_u32 s86, s86, L_ap0-L_ct1_%=\n" \
"s_addc_u32 s87, s87, 0\n" \
"s_mov_b32 m0, s32\n" \
"s_nop 0\n" \
"v_mfma_f32_16x16x32_bf16 a[164:167], v[96:99], v[76:79], a[164:167]\n" \
"global_load_lds_dword v45, s[86:87]\n" \
"v_mfma_f32_16x16x32_bf16 a[164:167], v[160:163], v[140:143], a[164:167]\n" \
"ds_read_b64_tr_b16 v[112:113], v39 offset:32768\n" \
"v_mfma_f32_16x16x32_bf16 a[160:163], v[76:79], v[92:95], a[160:163]\n" \
"ds_read_b64_tr_b16 v[114:115], v39 offset:33792\n" \
"v_mfma_f32_16x16x32_bf16 a[160:163], v[140:143], v[156:159], a[160:163]\n" \
"ds_read_b64_tr_b16 v[176:177], v39 offset:40960\n" \
"v_mfma_f32_16x16x32_bf16 a[224:227], v[80:83], v[92:95], a[224:227]\n" \
"ds_read_b64_tr_b16 v[178:179], v39 offset:41984\n" \
"v_mfma_f32_16x16x32_bf16 a[224:227], v[144:147], v[156:159], a[224:227]\n" \
"v_mfma_f32_16x16x32_bf16 a[228:231], v[96:99], v[80:83], a[228:231]\n" \
"s_add_u32 s88, s42, 0x3000\n" \
"buffer_store_dwordx4 v[212:215], v20, s[80:83], s88 offen offset:3072 sc1\n" \
"v_mfma_f32_16x16x32_bf16 a[228:231], v[160:163], v[144:147], a[228:231]\n" \
"v_mfma_f32_16x16x32_bf16 a[196:199], v[88:91], v[80:83], a[196:199]\n" \
"v_mfma_f32_16x16x32_bf16 a[196:199], v[152:155], v[144:147], a[196:199]\n" \
"v_mfma_f32_16x16x32_bf16 a[192:195], v[84:87], v[80:83], a[192:195]\n" \
"v_mfma_f32_16x16x32_bf16 a[192:195], v[148:151], v[144:147], a[192:195]\n" \
"v_mfma_f32_16x16x32_bf16 a[8:11], v[88:91], v[64:67], a[8:11]\n" \
"v_mfma_f32_16x16x32_bf16 a[8:11], v[152:155], v[128:131], a[8:11]\n" \
"v_mfma_f32_16x16x32_bf16 a[40:43], v[96:99], v[64:67], a[40:43]\n" \
"v_mfma_f32_16x16x32_bf16 a[40:43], v[160:163], v[128:131], a[40:43]\n" \
"v_mfma_f32_16x16x32_bf16 a[12:15], v[64:67], v[92:95], a[12:15]\n" \
"v_mfma_f32_16x16x32_bf16 a[12:15], v[128:131], v[156:159], a[12:15]\n" \
"s_waitcnt vmcnt(4) lgkmcnt(0)\n" \
"s_barrier\n" \
"v_mfma_f32_16x16x32_bf16 a[144:147], v[100:103], v[68:71], a[144:147]\n" \
"ds_read_b64_tr_b16 v[84:85], v32 offset:0\n" \
"v_lshrrev_b32_e64 v24, 9, v20\n" \
"v_mfma_f32_16x16x32_bf16 a[144:147], v[164:167], v[132:135], a[144:147]\n" \
"v_and_b32_e32 v26, 0x1ff, v20\n" \
"v_lshl_add_u32 v24, v24, 3, v26\n" \
"v_mfma_f32_16x16x32_bf16 a[148:151], v[104:107], v[68:71], a[148:151]\n" \
"v_lshrrev_b32_e64 v25, 8, v20\n" \
"v_mul_u32_u24_e32 v25, 0x24700, v25\n" \
"v_mfma_f32_16x16x32_bf16 a[148:151], v[168:171], v[132:135], a[148:151]\n" \
"ds_read_b64_tr_b16 v[86:87], v32 offset:512\n" \
"v_add_u32_e64 v25, v25, v20\n" \
"v_mfma_f32_16x16x32_bf16 a[200:203], v[108:111], v[68:71], a[200:203]\n" \
"v_lshrrev_b32_e32 v26, 2, v20\n" \
"v_add_u32_e32 v26, s45, v26\n" \
"v_mfma_f32_16x16x32_bf16 a[200:203], v[172:175], v[132:135], a[200:203]\n" \
"v_add_u32_e32 v27, s46, v20\n" \
"v_add_u32_e32 v21, s43, v27\n" \
"v_mfma_f32_16x16x32_bf16 a[204:207], v[112:115], v[68:71], a[204:207]\n" \
"ds_read_b64_tr_b16 v[148:149], v32 offset:4096\n" \
"v_mfma_f32_16x16x32_bf16 a[204:207], v[176:179], v[132:135], a[204:207]\n" \
"ds_read_b128 v[68:71], v28 offset:8192\n" \
"v_mfma_f32_16x16x32_bf16 a[236:239], v[112:115], v[72:75], a[236:239]\n" \
"ds_read_b128 v[132:135], v29 offset:8192\n" \
"v_mfma_f32_16x16x32_bf16 a[236:239], v[176:179], v[136:139], a[236:239]\n" \
"ds_read_b64_tr_b16 v[150:151], v32 offset:4608\n" \
"v_mfma_f32_16x16x32_bf16 a[232:235], v[108:111], v[72:75], a[232:235]\n" \
"v_mfma_f32_16x16x32_bf16 a[232:235], v[172:175], v[136:139], a[232:235]\n" \
"v_mfma_f32_16x16x32_bf16 a[180:183], v[104:107], v[72:75], a[180:183]\n" \
"v_mfma_f32_16x16x32_bf16 a[180:183], v[168:171], v[136:139], a[180:183]\n" \
"ds_read_b64_tr_b16 v[88:89], v33 offset:0\n" \
"v_mfma_f32_16x16x32_bf16 a[176:179], v[100:103], v[72:75], a[176:179]\n" \
"v_mfma_f32_16x16x32_bf16 a[176:179], v[164:167], v[136:139], a[176:179]\n" \
"ds_read_b128 v[72:75], v28 offset:10240\n" \
"v_mfma_f32_16x16x32_bf16 a[72:75], v[100:103], v[76:79], a[72:75]\n" \
"ds_read_b128 v[136:139], v29 offset:10240\n" \
"v_mfma_f32_16x16x32_bf16 a[72:75], v[164:167], v[140:143], a[72:75]\n" \
"ds_read_b64_tr_b16 v[90:91], v33 offset:512\n" \
"v_mfma_f32_16x16x32_bf16 a[76:79], v[104:107], v[76:79], a[76:79]\n" \
"v_mfma_f32_16x16x32_bf16 a[76:79], v[168:171], v[140:143], a[76:79]\n" \
"ds_read_b64_tr_b16 v[152:153], v33 offset:4096\n" \
"v_mfma_f32_16x16x32_bf16 a[104:107], v[108:111], v[76:79], a[104:107]\n" \
"v_mfma_f32_16x16x32_bf16 a[104:107], v[172:175], v[140:143], a[104:107]\n" \
"v_mfma_f32_16x16x32_bf16 a[108:111], v[112:115], v[76:79], a[108:111]\n" \
"ds_read_b64_tr_b16 v[154:155], v33 offset:4608\n" \
"v_mfma_f32_16x16x32_bf16 a[108:111], v[176:179], v[140:143], a[108:111]\n" \
"ds_read_b128 v[76:79], v28 offset:12288\n" \
"v_mfma_f32_16x16x32_bf16 a[172:175], v[112:115], v[80:83], a[172:175]\n" \
"ds_read_b128 v[140:143], v29 offset:12288\n" \
"v_mfma_f32_16x16x32_bf16 a[172:175], v[176:179], v[144:147], a[172:175]\n" \
"v_mfma_f32_16x16x32_bf16 a[168:171], v[108:111], v[80:83], a[168:171]\n" \
"ds_read_b64_tr_b16 v[96:97], v35 offset:0\n" \
"v_mfma_f32_16x16x32_bf16 a[168:171], v[172:175], v[144:147], a[168:171]\n" \
"v_mfma_f32_16x16x32_bf16 a[140:143], v[104:107], v[80:83], a[140:143]\n" \
"v_mfma_f32_16x16x32_bf16 a[140:143], v[168:171], v[144:147], a[140:143]\n" \
"ds_read_b64_tr_b16 v[98:99], v35 offset:512\n" \
"v_mfma_f32_16x16x32_bf16 a[136:139], v[100:103], v[80:83], a[136:139]\n" \
"v_mfma_f32_16x16x32_bf16 a[136:139], v[164:167], v[144:147], a[136:139]\n" \
"ds_read_b128 v[80:83], v28 offset:14336\n" \
"v_mfma_f32_16x16x32_bf16 a[80:83], v[100:103], v[60:63], a[80:83]\n" \
"ds_read_b128 v[144:147], v29 offset:14336\n" \
"v_mfma_f32_16x16x32_bf16 a[80:83], v[164:167], v[124:127], a[80:83]\n" \
"ds_read_b64_tr_b16 v[160:161], v35 offset:4096\n" \
"v_mfma_f32_16x16x32_bf16 a[84:87], v[104:107], v[60:63], a[84:87]\n" \
"v_mfma_f32_16x16x32_bf16 a[84:87], v[168:171], v[124:127], a[84:87]\n" \
"ds_read_b64_tr_b16 v[162:163], v35 offset:4608\n" \
"v_mfma_f32_16x16x32_bf16 a[88:91], v[108:111], v[60:63], a[88:91]\n" \
"v_mfma_f32_16x16x32_bf16 a[88:91], v[172:175], v[124:127], a[88:91]\n" \
"v_mfma_f32_16x16x32_bf16 a[92:95], v[112:115], v[60:63], a[92:95]\n" \
"v_mfma_f32_16x16x32_bf16 a[92:95], v[176:179], v[124:127], a[92:95]\n" \
"ds_read_b128 v[60:63], v28 offset:4096\n" \
"v_mfma_f32_16x16x32_bf16 a[124:127], v[112:115], v[64:67], a[124:127]\n" \
"ds_read_b128 v[124:127], v29 offset:4096\n" \
"v_mfma_f32_16x16x32_bf16 a[124:127], v[176:179], v[128:131], a[124:127]\n" \
"ds_read_b64_tr_b16 v[92:93], v34 offset:0\n" \
"v_mfma_f32_16x16x32_bf16 a[120:123], v[108:111], v[64:67], a[120:123]\n" \
"ds_read_b64_tr_b16 v[94:95], v34 offset:512\n" \
"v_mfma_f32_16x16x32_bf16 a[120:123], v[172:175], v[128:131], a[120:123]\n" \
"v_mfma_f32_16x16x32_bf16 a[116:119], v[104:107], v[64:67], a[116:119]\n" \
"v_mfma_f32_16x16x32_bf16 a[116:119], v[168:171], v[128:131], a[116:119]\n" \
"ds_read_b64_tr_b16 v[156:157], v34 offset:4096\n" \
"v_mfma_f32_16x16x32_bf16 a[112:115], v[100:103], v[64:67], a[112:115]\n" \
"v_mfma_f32_16x16x32_bf16 a[112:115], v[164:167], v[128:131], a[112:115]\n" \
"ds_read_b128 v[64:67], v28 offset:6144\n" \
"v_mfma_f32_16x16x32_bf16 a[16:19], v[100:103], v[52:55], a[16:19]\n" \
"ds_read_b128 v[128:131], v29 offset:6144\n" \
"v_mfma_f32_16x16x32_bf16 a[16:19], v[164:167], v[116:119], a[16:19]\n" \
"ds_read_b64_tr_b16 v[158:159], v34 offset:4608\n" \
"v_mfma_f32_16x16x32_bf16 a[20:23], v[104:107], v[52:55], a[20:23]\n" \
"v_mfma_f32_16x16x32_bf16 a[20:23], v[168:171], v[116:119], a[20:23]\n" \
"v_mfma_f32_16x16x32_bf16 a[24:27], v[108:111], v[52:55], a[24:27]\n" \
"v_mfma_f32_16x16x32_bf16 a[24:27], v[172:175], v[116:119], a[24:27]\n" \
"v_mfma_f32_16x16x32_bf16 a[28:31], v[112:115], v[52:55], a[28:31]\n" \
"v_mfma_f32_16x16x32_bf16 a[28:31], v[176:179], v[116:119], a[28:31]\n" \
"ds_read_b128 v[52:55], v28 offset:0\n" \
"v_mfma_f32_16x16x32_bf16 a[60:63], v[112:115], v[56:59], a[60:63]\n" \
"ds_read_b128 v[116:119], v29 offset:0\n" \
"v_mfma_f32_16x16x32_bf16 a[60:63], v[176:179], v[120:123], a[60:63]\n" \
"v_mfma_f32_16x16x32_bf16 a[56:59], v[108:111], v[56:59], a[56:59]\n" \
"v_mfma_f32_16x16x32_bf16 a[56:59], v[172:175], v[120:123], a[56:59]\n" \
"v_mfma_f32_16x16x32_bf16 a[52:55], v[104:107], v[56:59], a[52:55]\n" \
"v_mfma_f32_16x16x32_bf16 a[52:55], v[168:171], v[120:123], a[52:55]\n" \
"v_mfma_f32_16x16x32_bf16 a[48:51], v[100:103], v[56:59], a[48:51]\n" \
"v_mfma_f32_16x16x32_bf16 a[48:51], v[164:167], v[120:123], a[48:51]\n" \
"ds_read_b128 v[56:59], v28 offset:2048\n" \
"ds_read_b128 v[120:123], v29 offset:2048\n" \
"s_waitcnt lgkmcnt(15)\n" \
"s_nop 0\n" \
"v_mfma_f32_16x16x32_bf16 a[0:3], v[84:87], v[68:71], a[0:3]\n" \
"v_bfe_u32 v44, v20, 2, 5\n" \
"ds_read_b64_tr_b16 v[100:101], v36 offset:0\n" \
"v_mfma_f32_16x16x32_bf16 a[0:3], v[148:151], v[132:135], a[0:3]\n" \
"buffer_load_dword v44, v44, s[80:83], s38 offen offset:0 sc1\n" \
"v_mfma_f32_16x16x32_bf16 a[4:7], v[88:91], v[68:71], a[4:7]\n" \
"ds_read_b64_tr_b16 v[102:103], v36 offset:1024\n" \
"v_mfma_f32_16x16x32_bf16 a[4:7], v[152:155], v[132:135], a[4:7]\n" \
"ds_read_b64_tr_b16 v[164:165], v36 offset:8192\n" \
"v_mfma_f32_16x16x32_bf16 a[36:39], v[96:99], v[68:71], a[36:39]\n" \
"ds_read_b64_tr_b16 v[166:167], v36 offset:9216\n" \
"s_nop 0\n" \
"v_accvgpr_read_b32 v184, a0\n" \
"s_waitcnt lgkmcnt(15)\n" \
"v_mfma_f32_16x16x32_bf16 a[36:39], v[160:163], v[132:135], a[36:39]\n" \
"v_accvgpr_read_b32 v185, a1\n" \
"v_accvgpr_read_b32 v186, a2\n" \
"s_waitcnt lgkmcnt(12)\n" \
"v_mfma_f32_16x16x32_bf16 a[32:35], v[68:71], v[92:95], a[32:35]\n" \
"ds_read_b64_tr_b16 v[104:105], v37 offset:0\n" \
"v_accvgpr_read_b32 v187, a3\n" \
"s_waitcnt lgkmcnt(9)\n" \
"v_mfma_f32_16x16x32_bf16 a[32:35], v[132:135], v[156:159], a[32:35]\n" \
"v_accvgpr_read_b32 v188, a4\n" \
"v_accvgpr_read_b32 v189, a5\n" \
"v_mfma_f32_16x16x32_bf16 a[96:99], v[72:75], v[92:95], a[96:99]\n" \
"ds_read_b64_tr_b16 v[106:107], v37 offset:1024\n" \
"v_accvgpr_read_b32 v190, a6\n" \
"v_mfma_f32_16x16x32_bf16 a[96:99], v[136:139], v[156:159], a[96:99]\n" \
"ds_read_b64_tr_b16 v[168:169], v37 offset:8192\n" \
"v_accvgpr_read_b32 v191, a7\n" \
"v_mfma_f32_16x16x32_bf16 a[100:103], v[96:99], v[72:75], a[100:103]\n" \
"v_accvgpr_read_b32 v196, a36\n" \
"v_accvgpr_read_b32 v197, a37\n" \
"v_mfma_f32_16x16x32_bf16 a[100:103], v[160:163], v[136:139], a[100:103]\n" \
"ds_read_b64_tr_b16 v[170:171], v37 offset:9216\n" \
"v_accvgpr_read_b32 v198, a38\n" \
"v_mfma_f32_16x16x32_bf16 a[68:71], v[88:91], v[72:75], a[68:71]\n" \
"v_accvgpr_read_b32 v199, a39\n" \
"v_accvgpr_read_b32 v192, a32\n" \
"v_mfma_f32_16x16x32_bf16 a[68:71], v[152:155], v[136:139], a[68:71]\n" \
"ds_read_b64_tr_b16 v[108:109], v38 offset:0\n" \
"v_accvgpr_read_b32 v193, a33\n" \
"v_mfma_f32_16x16x32_bf16 a[64:67], v[84:87], v[72:75], a[64:67]\n" \
"ds_read_b64_tr_b16 v[110:111], v38 offset:1024\n" \
"v_accvgpr_read_b32 v194, a34\n" \
"v_mfma_f32_16x16x32_bf16 a[64:67], v[148:151], v[136:139], a[64:67]\n" \
"v_accvgpr_read_b32 v195, a35\n" \
"v_accvgpr_read_b32 v208, a96\n" \
"v_mfma_f32_16x16x32_bf16 a[128:131], v[84:87], v[76:79], a[128:131]\n" \
"ds_read_b64_tr_b16 v[172:173], v38 offset:8192\n" \
"v_accvgpr_read_b32 v209, a97\n" \
"v_mfma_f32_16x16x32_bf16 a[128:131], v[148:151], v[140:143], a[128:131]\n" \
"ds_read_b64_tr_b16 v[174:175], v38 offset:9216\n" \
"v_accvgpr_read_b32 v210, a98\n" \
"v_mfma_f32_16x16x32_bf16 a[132:135], v[88:91], v[76:79], a[132:135]\n" \
"v_accvgpr_read_b32 v211, a99\n" \
"v_accvgpr_read_b32 v212, a100\n" \
"v_mfma_f32_16x16x32_bf16 a[132:135], v[152:155], v[140:143], a[132:135]\n" \
"ds_read_b64_tr_b16 v[112:113], v39 offset:0\n" \
"v_accvgpr_read_b32 v213, a101\n" \
"v_mfma_f32_16x16x32_bf16 a[164:167], v[96:99], v[76:79], a[164:167]\n" \
"v_accvgpr_read_b32 v214, a102\n" \
"v_accvgpr_read_b32 v215, a103\n" \
"v_mfma_f32_16x16x32_bf16 a[164:167], v[160:163], v[140:143], a[164:167]\n" \
"ds_read_b64_tr_b16 v[114:115], v39 offset:1024\n" \
"v_accvgpr_read_b32 v204, a68\n" \
"v_mfma_f32_16x16x32_bf16 a[160:163], v[76:79], v[92:95], a[160:163]\n" \
"ds_read_b64_tr_b16 v[176:177], v39 offset:8192\n" \
"v_accvgpr_read_b32 v205, a69\n" \
"v_mfma_f32_16x16x32_bf16 a[160:163], v[140:143], v[156:159], a[160:163]\n" \
"v_accvgpr_read_b32 v206, a70\n" \
"v_accvgpr_read_b32 v207, a71\n" \
"v_mfma_f32_16x16x32_bf16 a[224:227], v[80:83], v[92:95], a[224:227]\n" \
"ds_read_b64_tr_b16 v[178:179], v39 offset:9216\n" \
"v_accvgpr_read_b32 v200, a64\n" \
"v_mfma_f32_16x16x32_bf16 a[224:227], v[144:147], v[156:159], a[224:227]\n" \
"v_accvgpr_read_b32 v201, a65\n" \
"v_accvgpr_read_b32 v202, a66\n" \
"v_mfma_f32_16x16x32_bf16 a[228:231], v[96:99], v[80:83], a[228:231]\n" \
"v_accvgpr_read_b32 v203, a67\n" \
"v_accvgpr_read_b32 v216, a128\n" \
"v_mfma_f32_16x16x32_bf16 a[228:231], v[160:163], v[144:147], a[228:231]\n" \
"v_accvgpr_read_b32 v217, a129\n" \
"v_accvgpr_read_b32 v218, a130\n" \
"v_mfma_f32_16x16x32_bf16 a[196:199], v[88:91], v[80:83], a[196:199]\n" \
"v_accvgpr_read_b32 v219, a131\n" \
"v_accvgpr_read_b32 v220, a132\n" \
"v_mfma_f32_16x16x32_bf16 a[196:199], v[152:155], v[144:147], a[196:199]\n" \
"v_accvgpr_read_b32 v221, a133\n" \
"v_accvgpr_read_b32 v222, a134\n" \
"v_mfma_f32_16x16x32_bf16 a[192:195], v[84:87], v[80:83], a[192:195]\n" \
"v_accvgpr_read_b32 v223, a135\n" \
"v_accvgpr_read_b32 v228, a164\n" \
"v_mfma_f32_16x16x32_bf16 a[192:195], v[148:151], v[144:147], a[192:195]\n" \
"v_accvgpr_read_b32 v229, a165\n" \
"v_accvgpr_read_b32 v230, a166\n" \
"v_mfma_f32_16x16x32_bf16 a[8:11], v[88:91], v[64:67], a[8:11]\n" \
"v_accvgpr_read_b32 v231, a167\n" \
"v_accvgpr_read_b32 v224, a160\n" \
"v_mfma_f32_16x16x32_bf16 a[8:11], v[152:155], v[128:131], a[8:11]\n" \
"v_accvgpr_read_b32 v225, a161\n" \
"v_accvgpr_read_b32 v226, a162\n" \
"v_mfma_f32_16x16x32_bf16 a[40:43], v[96:99], v[64:67], a[40:43]\n" \
"v_accvgpr_read_b32 v227, a163\n" \
"v_accvgpr_read_b32 v240, a224\n" \
"v_mfma_f32_16x16x32_bf16 a[40:43], v[160:163], v[128:131], a[40:43]\n" \
"v_accvgpr_read_b32 v241, a225\n" \
"v_accvgpr_read_b32 v242, a226\n" \
"v_mfma_f32_16x16x32_bf16 a[12:15], v[64:67], v[92:95], a[12:15]\n" \
"v_accvgpr_read_b32 v243, a227\n" \
"v_accvgpr_read_b32 v244, a228\n" \
"v_mfma_f32_16x16x32_bf16 a[12:15], v[128:131], v[156:159], a[12:15]\n" \
"v_accvgpr_read_b32 v245, a229\n" \
"v_accvgpr_read_b32 v246, a230\n" \
"s_waitcnt vmcnt(0) lgkmcnt(0)\n" \
"s_barrier\n" \
"v_mov_b32_e32 v45, s62\n" \
"s_mov_b64 exec, 1\n" \
"buffer_store_dword v45, off, s[80:83], s37 offset:32 sc1\n" \
"s_mov_b64 exec, -1\n" \
"v_cmp_ne_u32_e32 vcc, s62, v44\n" \
"s_nop 4\n" \
"s_cbranch_vccnz L_poll161_%=\n" \
"L_pollr161_%=:\n" \
"v_mfma_f32_16x16x32_bf16 a[144:147], v[100:103], v[68:71], a[144:147]\n" \
"s_mov_b32 s73, s50\n" \
"s_sub_u32 m0, s31, 0x10000\n" \
"v_mfma_f32_16x16x32_bf16 a[144:147], v[164:167], v[132:135], a[144:147]\n" \
"buffer_load_dwordx4 v20, s[80:83], s73 offen offset:0 sc1 lds\n" \
"v_mfma_f32_16x16x32_bf16 a[148:151], v[104:107], v[68:71], a[148:151]\n" \
"s_add_u32 s74, s50, 0x4000\n" \
"s_add_u32 s75, s50, 0x8000\n" \
"v_mfma_f32_16x16x32_bf16 a[148:151], v[168:171], v[132:135], a[148:151]\n" \
"s_mov_b32 s70, 0x3e0293ee\n" \
"s_mov_b32 s76, s52\n" \
"v_mfma_f32_16x16x32_bf16 a[200:203], v[108:111], v[68:71], a[200:203]\n" \
"v_accvgpr_read_b32 v32, a144\n" \
"v_accvgpr_read_b32 v33, a145\n" \
"v_mfma_f32_16x16x32_bf16 a[200:203], v[172:175], v[132:135], a[200:203]\n" \
"buffer_load_dwordx4 v20, s[80:83], s73 offen offset:1024 sc1 lds\n" \
"v_mfma_f32_16x16x32_bf16 a[204:207], v[112:115], v[68:71], a[204:207]\n" \
"v_accvgpr_read_b32 v34, a146\n" \
"v_accvgpr_read_b32 v35, a147\n" \
"v_mfma_f32_16x16x32_bf16 a[204:207], v[176:179], v[132:135], a[204:207]\n" \
"v_mul_f32_e32 v32, s70, v32\n" \
"v_mul_f32_e32 v33, s70, v33\n" \
"v_mfma_f32_16x16x32_bf16 a[236:239], v[112:115], v[72:75], a[236:239]\n" \
"v_accvgpr_read_b32 v36, a148\n" \
"v_accvgpr_read_b32 v37, a149\n" \
"v_mfma_f32_16x16x32_bf16 a[236:239], v[176:179], v[136:139], a[236:239]\n" \
"buffer_load_dwordx4 v20, s[80:83], s73 offen offset:2048 sc1 lds\n" \
"v_mfma_f32_16x16x32_bf16 a[232:235], v[108:111], v[72:75], a[232:235]\n" \
"v_accvgpr_read_b32 v38, a150\n" \
"v_accvgpr_read_b32 v39, a151\n" \
"v_mfma_f32_16x16x32_bf16 a[232:235], v[172:175], v[136:139], a[232:235]\n" \
"v_mul_f32_e32 v34, s70, v34\n" \
"v_mul_f32_e32 v35, s70, v35\n" \
"v_mfma_f32_16x16x32_bf16 a[180:183], v[104:107], v[72:75], a[180:183]\n" \
"v_cvt_pk_bf16_f32 v32, v32, v33\n" \
"v_mul_f32_e64 v36, s70, v36\n" \
"v_mfma_f32_16x16x32_bf16 a[180:183], v[168:171], v[136:139], a[180:183]\n" \
"buffer_load_dwordx4 v20, s[80:83], s73 offen offset:3072 sc1 lds\n" \
"v_mfma_f32_16x16x32_bf16 a[176:179], v[100:103], v[72:75], a[176:179]\n" \
"s_sub_u32 m0, s31, s51\n" \
"v_mul_f32_e32 v37, s70, v37\n" \
"v_mfma_f32_16x16x32_bf16 a[176:179], v[164:167], v[136:139], a[176:179]\n" \
"v_mul_f32_e32 v38, s70, v38\n" \
"v_mul_f32_e32 v39, s70, v39\n" \
"v_mfma_f32_16x16x32_bf16 a[72:75], v[100:103], v[76:79], a[72:75]\n" \
"v_cvt_pk_bf16_f32 v33, v34, v35\n" \
"v_cvt_pk_bf16_f32 v34, v36, v37\n" \
"v_mfma_f32_16x16x32_bf16 a[72:75], v[164:167], v[140:143], a[72:75]\n" \
"buffer_load_dwordx4 v20, s[80:83], s74 offen offset:0 sc1 lds\n" \
"v_mfma_f32_16x16x32_bf16 a[76:79], v[104:107], v[76:79], a[76:79]\n" \
"v_cvt_pk_bf16_f32 v35, v38, v39\n" \
"v_accvgpr_read_b32 v36, a204\n" \
"v_mfma_f32_16x16x32_bf16 a[76:79], v[168:171], v[140:143], a[76:79]\n" \
"ds_write_b128 v21, v[32:35] offset:16384\n" \
"v_mfma_f32_16x16x32_bf16 a[104:107], v[108:111], v[76:79], a[104:107]\n" \
"v_accvgpr_read_b32 v32, a200\n" \
"v_accvgpr_read_b32 v33, a201\n" \
"v_mfma_f32_16x16x32_bf16 a[104:107], v[172:175], v[140:143], a[104:107]\n" \
"buffer_load_dwordx4 v20, s[80:83], s74 offen offset:1024 sc1 lds\n" \
"v_mfma_f32_16x16x32_bf16 a[108:111], v[112:115], v[76:79], a[108:111]\n" \
"v_accvgpr_read_b32 v34, a202\n" \
"v_accvgpr_read_b32 v35, a203\n" \
"v_mfma_f32_16x16x32_bf16 a[108:111], v[176:179], v[140:143], a[108:111]\n" \
"v_mul_f32_e32 v32, s70, v32\n" \
"v_mul_f32_e32 v33, s70, v33\n" \
"v_mfma_f32_16x16x32_bf16 a[172:175], v[112:115], v[80:83], a[172:175]\n" \
"v_accvgpr_read_b32 v37, a205\n" \
"v_accvgpr_read_b32 v38, a206\n" \
"v_mfma_f32_16x16x32_bf16 a[172:175], v[176:179], v[144:147], a[172:175]\n" \
"buffer_load_dwordx4 v20, s[80:83], s74 offen offset:2048 sc1 lds\n" \
"v_mfma_f32_16x16x32_bf16 a[168:171], v[108:111], v[80:83], a[168:171]\n" \
"v_accvgpr_read_b32 v39, a207\n" \
"v_mul_f32_e64 v34, s70, v34\n" \
"v_mfma_f32_16x16x32_bf16 a[168:171], v[172:175], v[144:147], a[168:171]\n" \
"v_mul_f32_e64 v35, s70, v35\n" \
"v_cvt_pk_bf16_f32 v32, v32, v33\n" \
"v_mfma_f32_16x16x32_bf16 a[140:143], v[104:107], v[80:83], a[140:143]\n" \
"v_mul_f32_e32 v36, s70, v36\n" \
"v_mul_f32_e32 v37, s70, v37\n" \
"v_mfma_f32_16x16x32_bf16 a[140:143], v[168:171], v[144:147], a[140:143]\n" \
"buffer_load_dwordx4 v20, s[80:83], s74 offen offset:3072 sc1 lds\n" \
"v_mfma_f32_16x16x32_bf16 a[136:139], v[100:103], v[80:83], a[136:139]\n" \
"s_sub_u32 m0, s31, 0x4000\n" \
"v_mul_f32_e64 v38, s70, v38\n" \
"v_mfma_f32_16x16x32_bf16 a[136:139], v[164:167], v[144:147], a[136:139]\n" \
"v_mul_f32_e64 v39, s70, v39\n" \
"v_cvt_pk_bf16_f32 v33, v34, v35\n" \
"v_mfma_f32_16x16x32_bf16 a[80:83], v[100:103], v[60:63], a[80:83]\n" \
"v_accvgpr_read_b32 v236, a196\n" \
"v_accvgpr_read_b32 v237, a197\n" \
"v_mfma_f32_16x16x32_bf16 a[80:83], v[164:167], v[124:127], a[80:83]\n" \
"buffer_load_dwordx4 v20, s[80:83], s75 offen offset:0 sc1 lds\n" \
"v_mfma_f32_16x16x32_bf16 a[84:87], v[104:107], v[60:63], a[84:87]\n" \
"v_accvgpr_read_b32 v232, a192\n" \
"v_accvgpr_read_b32 v233, a193\n" \
"v_mfma_f32_16x16x32_bf16 a[84:87], v[168:171], v[124:127], a[84:87]\n" \
"v_accvgpr_read_b32 v180, a8\n" \
"v_accvgpr_read_b32 v181, a9\n" \
"v_mfma_f32_16x16x32_bf16 a[88:91], v[108:111], v[60:63], a[88:91]\n" \
"v_cvt_pk_bf16_f32 v34, v36, v37\n" \
"v_cvt_pk_bf16_f32 v35, v38, v39\n" \
"v_mfma_f32_16x16x32_bf16 a[88:91], v[172:175], v[124:127], a[88:91]\n" \
"buffer_load_dwordx4 v20, s[80:83], s75 offen offset:1024 sc1 lds\n" \
"v_mfma_f32_16x16x32_bf16 a[92:95], v[112:115], v[60:63], a[92:95]\n" \
"v_accvgpr_read_b32 v238, a198\n" \
"v_accvgpr_read_b32 v239, a199\n" \
"v_mfma_f32_16x16x32_bf16 a[92:95], v[176:179], v[124:127], a[92:95]\n" \
"v_accvgpr_read_b32 v234, a194\n" \
"v_accvgpr_read_b32 v235, a195\n" \
"v_mfma_f32_16x16x32_bf16 a[124:127], v[112:115], v[64:67], a[124:127]\n" \
"v_accvgpr_read_b32 v182, a10\n" \
"v_accvgpr_read_b32 v183, a11\n" \
"v_mfma_f32_16x16x32_bf16 a[124:127], v[176:179], v[128:131], a[124:127]\n" \
"buffer_load_dwordx4 v20, s[80:83], s75 offen offset:2048 sc1 lds\n" \
"v_mfma_f32_16x16x32_bf16 a[120:123], v[108:111], v[64:67], a[120:123]\n" \
"v_cvt_pk_bf16_f32 v180, v180, v181\n" \
"v_cvt_pk_bf16_f32 v188, v188, v189\n" \
"v_mfma_f32_16x16x32_bf16 a[120:123], v[172:175], v[128:131], a[120:123]\n" \
"v_cvt_pk_bf16_f32 v184, v184, v185\n" \
"v_cvt_pk_bf16_f32 v204, v204, v205\n" \
"v_mfma_f32_16x16x32_bf16 a[116:119], v[104:107], v[64:67], a[116:119]\n" \
"v_cvt_pk_bf16_f32 v200, v200, v201\n" \
"v_cvt_pk_bf16_f32 v220, v220, v221\n" \
"v_mfma_f32_16x16x32_bf16 a[116:119], v[168:171], v[128:131], a[116:119]\n" \
"buffer_load_dwordx4 v20, s[80:83], s75 offen offset:3072 sc1 lds\n" \
"v_mfma_f32_16x16x32_bf16 a[112:115], v[100:103], v[64:67], a[112:115]\n" \
"s_mov_b32 m0, s53\n" \
"s_nop 0\n" \
"v_cvt_pk_bf16_f32 v216, v216, v217\n" \
"v_mfma_f32_16x16x32_bf16 a[112:115], v[164:167], v[128:131], a[112:115]\n" \
"v_cvt_pk_bf16_f32 v236, v236, v237\n" \
"v_cvt_pk_bf16_f32 v232, v232, v233\n" \
"v_mfma_f32_16x16x32_bf16 a[16:19], v[100:103], v[52:55], a[16:19]\n" \
"ds_write_b128 v21, v[32:35] offset:17408\n" \
"v_mfma_f32_16x16x32_bf16 a[16:19], v[164:167], v[116:119], a[16:19]\n" \
"buffer_load_dwordx4 v20, s[80:83], s76 offen offset:0 sc1 lds\n" \
"v_mfma_f32_16x16x32_bf16 a[20:23], v[104:107], v[52:55], a[20:23]\n" \
"v_accvgpr_read_b32 v247, a231\n" \
"v_accvgpr_read_b32 v50, a42\n" \
"v_mfma_f32_16x16x32_bf16 a[20:23], v[168:171], v[116:119], a[20:23]\n" \
"v_accvgpr_read_b32 v51, a43\n" \
"v_cvt_pk_bf16_f32 v181, v182, v183\n" \
"v_cvt_pk_bf16_f32 v189, v190, v191\n" \
"v_cvt_pk_bf16_f32 v185, v186, v187\n" \
"v_cvt_pk_bf16_f32 v205, v206, v207\n" \
"v_cvt_pk_bf16_f32 v201, v202, v203\n" \
"v_cvt_pk_bf16_f32 v221, v222, v223\n" \
"buffer_load_dwordx4 v20, s[80:83], s76 offen offset:1024 sc1 lds\n" \
"v_cvt_pk_bf16_f32 v217, v218, v219\n" \
"v_cvt_pk_bf16_f32 v237, v238, v239\n" \
"v_cvt_pk_bf16_f32 v233, v234, v235\n" \
"v_cvt_pk_bf16_f32 v182, v50, v51\n" \
"v_mov_b32_e32 v183, 0x3f80\n" \
"v_cvt_pk_bf16_f32 v190, v198, v199\n" \
"v_mov_b32_e32 v191, 0x3f80\n" \
"v_cvt_pk_bf16_f32 v186, v196, v197\n" \
"v_mov_b32_e64 v187, 0\n" \
"v_cvt_pk_bf16_f32 v86, v192, v193\n" \
"v_cvt_pk_bf16_f32 v87, v194, v195\n" \
"v_cvt_pk_bf16_f32 v206, v214, v215\n" \
"v_mov_b32_e32 v207, 0x3f80\n" \
"v_cvt_pk_bf16_f32 v202, v212, v213\n" \
"v_mov_b32_e64 v203, 0\n" \
"v_cvt_pk_bf16_f32 v88, v208, v209\n" \
"v_cvt_pk_bf16_f32 v89, v210, v211\n" \
"v_cvt_pk_bf16_f32 v222, v230, v231\n" \
"v_mov_b32_e32 v223, 0x3f80\n" \
"v_cvt_pk_bf16_f32 v218, v228, v229\n" \
"v_mov_b32_e64 v219, 0\n" \
"v_cvt_pk_bf16_f32 v90, v224, v225\n" \
"v_cvt_pk_bf16_f32 v91, v226, v227\n" \
"v_cvt_pk_bf16_f32 v238, v246, v247\n" \
"v_mov_b32_e32 v239, 0x3f80\n" \
"v_cvt_pk_bf16_f32 v234, v244, v245\n" \
"v_mov_b32_e64 v235, 0\n" \
"v_cvt_pk_bf16_f32 v92, v240, v241\n" \
"v_cvt_pk_bf16_f32 v93, v242, v243\n" \
"v_accvgpr_read_b32 v132, a176\n" \
"v_accvgpr_read_b32 v133, a177\n" \
"v_accvgpr_read_b32 v32, a232\n" \
"v_accvgpr_read_b32 v33, a233\n" \
"v_mfma_f32_16x16x32_bf16 a[24:27], v[108:111], v[52:55], a[24:27]\n" \
"v_mfma_f32_16x16x32_bf16 v[192:195], v[188:191], v[184:187], 0\n" \
"v_mfma_f32_16x16x32_bf16 v[148:151], v[180:183], v[184:187], 0\n" \
"v_mfma_f32_16x16x32_bf16 v[208:211], v[204:207], v[200:203], 0\n" \
"v_mfma_f32_16x16x32_bf16 v[152:155], v[188:191], v[200:203], 0\n" \
"v_mfma_f32_16x16x32_bf16 v[224:227], v[220:223], v[216:219], 0\n" \
"v_mfma_f32_16x16x32_bf16 v[156:159], v[204:207], v[216:219], 0\n" \
"v_mfma_f32_16x16x32_bf16 v[240:243], v[236:239], v[232:235], 0\n" \
"v_mfma_f32_16x16x32_bf16 v[160:163], v[220:223], v[232:235], 0\n" \
"v_accvgpr_read_b32 v134, a178\n" \
"v_accvgpr_read_b32 v135, a179\n" \
"v_mul_f32_e32 v132, s70, v132\n" \
"v_mul_f32_e32 v133, s70, v133\n" \
"v_accvgpr_read_b32 v34, a234\n" \
"v_accvgpr_read_b32 v35, a235\n" \
"v_mul_f32_e32 v32, s70, v32\n" \
"v_mul_f32_e32 v33, s70, v33\n" \
"v_accvgpr_read_b32 v136, a180\n" \
"v_accvgpr_read_b32 v137, a181\n" \
"v_accvgpr_read_b32 v138, a182\n" \
"v_accvgpr_read_b32 v139, a183\n" \
"v_mul_f32_e32 v134, s70, v134\n" \
"v_mul_f32_e32 v135, s70, v135\n" \
"v_cvt_pk_bf16_f32 v132, v132, v133\n" \
"v_accvgpr_read_b32 v36, a236\n" \
"v_accvgpr_read_b32 v37, a237\n" \
"v_accvgpr_read_b32 v38, a238\n" \
"v_accvgpr_read_b32 v39, a239\n" \
"v_mul_f32_e32 v34, s70, v34\n" \
"v_mul_f32_e32 v35, s70, v35\n" \
"v_cvt_pk_bf16_f32 v32, v32, v33\n" \
"v_mul_f32_e32 v136, s70, v136\n" \
"v_mul_f32_e32 v137, s70, v137\n" \
"v_mul_f32_e32 v138, s70, v138\n" \
"v_mul_f32_e32 v139, s70, v139\n" \
"v_cvt_pk_bf16_f32 v133, v134, v135\n" \
"v_mul_f32_e32 v36, s70, v36\n" \
"v_mul_f32_e32 v37, s70, v37\n" \
"v_mul_f32_e32 v38, s70, v38\n" \
"v_mul_f32_e32 v39, s70, v39\n" \
"v_cvt_pk_bf16_f32 v33, v34, v35\n" \
"v_cvt_pk_bf16_f32 v134, v136, v137\n" \
"v_cvt_pk_bf16_f32 v135, v138, v139\n" \
"v_cvt_pk_bf16_f32 v34, v36, v37\n" \
"v_cvt_pk_bf16_f32 v35, v38, v39\n" \
"v_mfma_f32_16x16x32_bf16 a[24:27], v[172:175], v[116:119], a[24:27]\n" \
"ds_write_b128 v21, v[132:135] offset:20480\n" \
"ds_write_b128 v21, v[32:35] offset:21504\n" \
"v_accvgpr_read_b32 v132, a72\n" \
"v_accvgpr_read_b32 v133, a73\n" \
"v_accvgpr_read_b32 v32, a104\n" \
"v_accvgpr_read_b32 v33, a105\n" \
"v_accvgpr_read_b32 v140, a136\n" \
"v_accvgpr_read_b32 v141, a137\n" \
"v_accvgpr_read_b32 v134, a74\n" \
"v_accvgpr_read_b32 v135, a75\n" \
"v_mul_f32_e32 v132, s70, v132\n" \
"v_mul_f32_e32 v133, s70, v133\n" \
"v_accvgpr_read_b32 v34, a106\n" \
"v_accvgpr_read_b32 v35, a107\n" \
"v_mul_f32_e32 v32, s70, v32\n" \
"v_mul_f32_e32 v33, s70, v33\n" \
"v_accvgpr_read_b32 v142, a138\n" \
"v_accvgpr_read_b32 v143, a139\n" \
"v_mul_f32_e32 v140, s70, v140\n" \
"v_mul_f32_e32 v141, s70, v141\n" \
"v_mfma_f32_16x16x32_bf16 a[28:31], v[112:115], v[52:55], a[28:31]\n" \
"v_accvgpr_read_b32 v136, a76\n" \
"v_accvgpr_read_b32 v137, a77\n" \
"v_accvgpr_read_b32 v138, a78\n" \
"v_accvgpr_read_b32 v139, a79\n" \
"v_mul_f32_e32 v134, s70, v134\n" \
"v_mul_f32_e32 v135, s70, v135\n" \
"v_cvt_pk_bf16_f32 v132, v132, v133\n" \
"v_accvgpr_read_b32 v36, a108\n" \
"v_accvgpr_read_b32 v37, a109\n" \
"v_accvgpr_read_b32 v38, a110\n" \
"v_accvgpr_read_b32 v39, a111\n" \
"v_mul_f32_e32 v34, s70, v34\n" \
"v_mul_f32_e32 v35, s70, v35\n" \
"v_cvt_pk_bf16_f32 v32, v32, v33\n" \
"v_accvgpr_read_b32 v144, a140\n" \
"v_accvgpr_read_b32 v145, a141\n" \
"v_accvgpr_read_b32 v146, a142\n" \
"v_accvgpr_read_b32 v147, a143\n" \
"v_mul_f32_e32 v142, s70, v142\n" \
"v_mul_f32_e32 v143, s70, v143\n" \
"v_cvt_pk_bf16_f32 v140, v140, v141\n" \
"s_mov_b32 s64, 0xaaaaaaaa\n" \
"s_mov_b32 s65, 0xaaaaaaaa\n" \
"v_mul_f32_e32 v136, s70, v136\n" \
"v_mul_f32_e32 v137, s70, v137\n" \
"v_mul_f32_e32 v138, s70, v138\n" \
"v_mul_f32_e32 v139, s70, v139\n" \
"v_cvt_pk_bf16_f32 v133, v134, v135\n" \
"v_mul_f32_e32 v36, s70, v36\n" \
"v_mul_f32_e32 v37, s70, v37\n" \
"v_mul_f32_e32 v38, s70, v38\n" \
"v_mul_f32_e32 v39, s70, v39\n" \
"v_cvt_pk_bf16_f32 v33, v34, v35\n" \
".fill (L_pb + 85728 - .) / 4, 4, 0xBF800000\n" \
"L_pp21:\n" \
"s_cbranch_execz L_pret\n" \
"v_mul_f32_e32 v144, s70, v144\n" \
"v_mul_f32_e32 v145, s70, v145\n" \
"v_mul_f32_e32 v146, s70, v146\n" \
"v_mul_f32_e64 v147, s70, v147\n" \
"v_cvt_pk_bf16_f32 v141, v142, v143\n" \
"s_mov_b32 s66, 0xfff0ffff\n" \
"s_mov_b32 s67, 0xf000ff00\n" \
"s_mov_b32 s68, 0xffc0fffc\n" \
"s_mov_b32 s69, 0xc000fc00\n" \
"v_cndmask_b32_e64 v192, v192, v193, s[64:65]\n" \
"v_cndmask_b32_e64 v148, v148, v149, s[64:65]\n" \
"v_cndmask_b32_e64 v194, v194, v195, s[64:65]\n" \
"v_cndmask_b32_e64 v150, v150, v151, s[64:65]\n" \
"v_cndmask_b32_e64 v208, v208, v209, s[64:65]\n" \
"v_cndmask_b32_e64 v152, v152, v153, s[64:65]\n" \
"v_cndmask_b32_e64 v210, v210, v211, s[64:65]\n" \
"v_cndmask_b32_e64 v154, v154, v155, s[64:65]\n" \
"v_cndmask_b32_e64 v224, v224, v225, s[64:65]\n" \
"v_cndmask_b32_e64 v156, v156, v157, s[64:65]\n" \
"v_cndmask_b32_e64 v226, v226, v227, s[64:65]\n" \
"v_cndmask_b32_e64 v158, v158, v159, s[64:65]\n" \
"v_cndmask_b32_e64 v240, v240, v241, s[64:65]\n" \
"v_cndmask_b32_e64 v160, v160, v161, s[64:65]\n" \
"v_cndmask_b32_e64 v242, v242, v243, s[64:65]\n" \
"v_cndmask_b32_e64 v162, v162, v163, s[64:65]\n" \
"v_cvt_pk_bf16_f32 v134, v136, v137\n" \
"v_cvt_pk_bf16_f32 v135, v138, v139\n" \
"v_cvt_pk_bf16_f32 v34, v36, v37\n" \
"v_cvt_pk_bf16_f32 v35, v38, v39\n" \
"v_cvt_pk_bf16_f32 v142, v144, v145\n" \
"v_cvt_pk_bf16_f32 v143, v146, v147\n" \
"v_cndmask_b32_e64 v192, v148, v192, s[66:67]\n" \
"v_cndmask_b32_e64 v194, v150, v194, s[68:69]\n" \
"v_cndmask_b32_e64 v208, v152, v208, s[66:67]\n" \
"v_cndmask_b32_e64 v210, v154, v210, s[68:69]\n" \
"v_cndmask_b32_e64 v224, v156, v224, s[66:67]\n" \
"v_cndmask_b32_e64 v226, v158, v226, s[68:69]\n" \
"v_cndmask_b32_e64 v240, v160, v240, s[66:67]\n" \
"v_cndmask_b32_e64 v242, v162, v242, s[68:69]\n" \
"ds_write_b128 v21, v[132:135] offset:24576\n" \
"ds_write_b128 v21, v[32:35] offset:25600\n" \
"ds_write_b128 v21, v[140:143] offset:28672\n" \
"v_mfma_f32_16x16x32_bf16 a[28:31], v[176:179], v[116:119], a[28:31]\n" \
"v_max_f32_e32 v193, v192, v194\n" \
"v_max_f32_e32 v209, v208, v210\n" \
"v_max_f32_e32 v225, v224, v226\n" \
"v_max_f32_e32 v241, v240, v242\n" \
"s_mov_b32 s71, 0x3e0293ee\n" \
"v_permlane32_swap_b32_e32 v193, v225\n" \
"v_permlane32_swap_b32_e32 v209, v241\n" \
"v_accvgpr_read_b32 v132, a168\n" \
"v_accvgpr_read_b32 v133, a169\n" \
"v_accvgpr_read_b32 v32, a80\n" \
"v_accvgpr_read_b32 v33, a81\n" \
"v_accvgpr_read_b32 v140, a88\n" \
"v_accvgpr_read_b32 v141, a89\n" \
"v_accvgpr_read_b32 v124, a112\n" \
"v_accvgpr_read_b32 v125, a113\n" \
"v_permlane32_swap_b32_e32 v196, v228\n" \
"v_permlane32_swap_b32_e32 v212, v244\n" \
"v_max_f32_e32 v193, v193, v225\n" \
"v_max_f32_e32 v209, v209, v241\n" \
"v_accvgpr_read_b32 v134, a170\n" \
"v_accvgpr_read_b32 v135, a171\n" \
"v_mul_f32_e32 v132, s70, v132\n" \
"v_mul_f32_e32 v133, s70, v133\n" \
"v_accvgpr_read_b32 v34, a82\n" \
"v_accvgpr_read_b32 v35, a83\n" \
"v_pk_mul_f32 v[32:33], v[32:33], s[70:71]\n" \
"v_accvgpr_read_b32 v142, a90\n" \
"v_accvgpr_read_b32 v143, a91\n" \
"v_pk_mul_f32 v[140:141], v[140:141], s[70:71]\n" \
"v_accvgpr_read_b32 v126, a114\n" \
"v_accvgpr_read_b32 v127, a115\n" \
"v_pk_mul_f32 v[124:125], v[124:125], s[70:71]\n" \
"v_accvgpr_read_b32 v48, a40\n" \
"v_accvgpr_read_b32 v49, a41\n" \
"v_permlane16_swap_b32_e32 v228, v244\n" \
"v_permlane16_swap_b32_e32 v193, v209\n" \
"v_accvgpr_read_b32 v136, a172\n" \
"v_accvgpr_read_b32 v137, a173\n" \
"v_accvgpr_read_b32 v138, a174\n" \
"v_accvgpr_read_b32 v139, a175\n" \
"v_mul_f32_e32 v134, s70, v134\n" \
"v_mul_f32_e32 v135, s70, v135\n" \
"v_cvt_pk_bf16_f32 v132, v132, v133\n" \
"v_accvgpr_read_b32 v36, a84\n" \
"v_accvgpr_read_b32 v37, a85\n" \
"v_accvgpr_read_b32 v38, a86\n" \
"v_accvgpr_read_b32 v39, a87\n" \
"v_pk_mul_f32 v[34:35], v[34:35], s[70:71]\n" \
"v_cvt_pk_bf16_f32 v32, v32, v33\n" \
"v_accvgpr_read_b32 v144, a92\n" \
"v_accvgpr_read_b32 v145, a93\n" \
"v_accvgpr_read_b32 v146, a94\n" \
"v_accvgpr_read_b32 v147, a95\n" \
"v_pk_mul_f32 v[142:143], v[142:143], s[70:71]\n" \
"v_cvt_pk_bf16_f32 v140, v140, v141\n" \
"v_accvgpr_read_b32 v128, a116\n" \
"v_accvgpr_read_b32 v129, a117\n" \
"v_accvgpr_read_b32 v130, a118\n" \
"v_accvgpr_read_b32 v131, a119\n" \
"v_pk_mul_f32 v[126:127], v[126:127], s[70:71]\n" \
"v_cvt_pk_bf16_f32 v124, v124, v125\n" \
"v_mfma_f32_16x16x32_bf16 a[60:63], v[112:115], v[56:59], a[60:63]\n" \
"s_mov_b32 s72, 0x3eae1a93\n" \
"v_mul_f32_e32 v45, 0xbfb8aa3b, v228\n" \
"v_mfma_f32_16x16x16_bf16 v[48:51], v[182:183], v[46:47], 0\n" \
"v_mfma_f32_16x16x16_bf16 v[196:199], v[190:191], v[46:47], 0\n" \
"v_mfma_f32_16x16x16_bf16 v[212:215], v[206:207], v[46:47], 0\n" \
"v_max_f32_e32 v193, v193, v209\n" \
"v_mul_f32_e32 v136, s70, v136\n" \
"v_mul_f32_e32 v137, s70, v137\n" \
"v_mul_f32_e32 v138, s70, v138\n" \
"v_mul_f32_e64 v139, s70, v139\n" \
"v_cvt_pk_bf16_f32 v133, v134, v135\n" \
"v_pk_mul_f32 v[36:37], v[36:37], s[70:71]\n" \
"v_pk_mul_f32 v[38:39], v[38:39], s[70:71]\n" \
"v_cvt_pk_bf16_f32 v33, v34, v35\n" \
"v_pk_mul_f32 v[144:145], v[144:145], s[70:71]\n" \
"v_pk_mul_f32 v[146:147], v[146:147], s[70:71]\n" \
"v_cvt_pk_bf16_f32 v141, v142, v143\n" \
"v_pk_mul_f32 v[128:129], v[128:129], s[70:71]\n" \
"v_pk_mul_f32 v[130:131], v[130:131], s[70:71]\n" \
"v_cvt_pk_bf16_f32 v125, v126, v127\n" \
"v_mfma_f32_16x16x16_bf16 v[228:231], v[222:223], v[46:47], 0\n" \
"v_mul_f32_e64 v193, s72, v193\n" \
"v_cvt_pk_bf16_f32 v134, v136, v137\n" \
"v_cvt_pk_bf16_f32 v135, v138, v139\n" \
"v_cvt_pk_bf16_f32 v34, v36, v37\n" \
"v_cvt_pk_bf16_f32 v35, v38, v39\n" \
"v_cvt_pk_bf16_f32 v142, v144, v145\n" \
"v_cvt_pk_bf16_f32 v143, v146, v147\n" \
"v_cvt_pk_bf16_f32 v126, v128, v129\n" \
"v_cvt_pk_bf16_f32 v127, v130, v131\n" \
"v_mfma_f32_16x16x16_bf16 v[244:247], v[238:239], v[46:47], 0\n" \
"v_mov_b32_e64 v209, v193\n" \
"v_accvgpr_read_b32 v60, a120\n" \
"ds_write_b128 v21, v[132:135] offset:29696\n" \
"ds_write_b128 v27, v[32:35] offset:8192\n" \
"ds_write_b128 v27, v[140:143] offset:9216\n" \
"ds_write_b128 v27, v[124:127] offset:12288\n" \
"v_permlane16_swap_b32_e32 v193, v209\n" \
"v_mfma_f32_16x16x32_bf16 a[60:63], v[176:179], v[120:123], a[60:63]\n" \
"v_mov_b32_e32 v225, v193\n" \
"v_mov_b32_e64 v241, v209\n" \
"v_accvgpr_read_b32 v61, a121\n" \
"v_accvgpr_read_b32 v132, a16\n" \
"v_accvgpr_read_b32 v133, a17\n" \
"v_accvgpr_read_b32 v32, a24\n" \
"v_accvgpr_read_b32 v33, a25\n" \
"v_permlane32_swap_b32_e32 v193, v225\n" \
"v_permlane32_swap_b32_e32 v209, v241\n" \
"v_accvgpr_read_b32 v62, a122\n" \
"v_accvgpr_read_b32 v63, a123\n" \
"v_pk_mul_f32 v[60:61], v[60:61], s[70:71]\n" \
"v_accvgpr_read_b32 v134, a18\n" \
"v_accvgpr_read_b32 v135, a19\n" \
"v_pk_mul_f32 v[132:133], v[132:133], s[70:71]\n" \
"v_accvgpr_read_b32 v34, a26\n" \
"v_accvgpr_read_b32 v35, a27\n" \
"v_pk_mul_f32 v[32:33], v[32:33], s[70:71]\n" \
"v_fma_f32 v192, v192, s72, -v193\n" \
"v_fma_f32 v194, v194, s72, -v193\n" \
"v_fma_f32 v208, v208, s72, -v209\n" \
"v_fma_f32 v210, v210, s72, -v209\n" \
"v_accvgpr_read_b32 v64, a124\n" \
"v_accvgpr_read_b32 v65, a125\n" \
"v_accvgpr_read_b32 v66, a126\n" \
"v_accvgpr_read_b32 v67, a127\n" \
"v_pk_mul_f32 v[62:63], v[62:63], s[70:71]\n" \
"v_cvt_pk_bf16_f32 v60, v60, v61\n" \
"v_accvgpr_read_b32 v136, a20\n" \
"v_accvgpr_read_b32 v137, a21\n" \
"v_accvgpr_read_b32 v138, a22\n" \
"v_accvgpr_read_b32 v139, a23\n" \
"v_pk_mul_f32 v[134:135], v[134:135], s[70:71]\n" \
"v_cvt_pk_bf16_f32 v132, v132, v133\n" \
"v_accvgpr_read_b32 v36, a28\n" \
"v_accvgpr_read_b32 v37, a29\n" \
"v_accvgpr_read_b32 v38, a30\n" \
"v_accvgpr_read_b32 v39, a31\n" \
"v_pk_mul_f32 v[34:35], v[34:35], s[70:71]\n" \
"v_cvt_pk_bf16_f32 v32, v32, v33\n" \
"v_accvgpr_read_b32 v128, a60\n" \
"v_accvgpr_read_b32 v129, a61\n" \
"v_accvgpr_read_b32 v130, a62\n" \
"v_accvgpr_read_b32 v131, a63\n" \
"v_exp_f32_e32 v192, v192\n" \
"v_exp_f32_e32 v194, v194\n" \
"v_exp_f32_e32 v208, v208\n" \
"v_exp_f32_e32 v210, v210\n" \
"v_fma_f32 v224, v224, s72, -v225\n" \
"v_fma_f32 v226, v226, s72, -v225\n" \
"v_pk_mul_f32 v[64:65], v[64:65], s[70:71]\n" \
"v_pk_mul_f32 v[66:67], v[66:67], s[70:71]\n" \
"v_cvt_pk_bf16_f32 v61, v62, v63\n" \
"v_pk_mul_f32 v[136:137], v[136:137], s[70:71]\n" \
"v_pk_mul_f32 v[138:139], v[138:139], s[70:71]\n" \
"v_cvt_pk_bf16_f32 v133, v134, v135\n" \
"v_pk_mul_f32 v[36:37], v[36:37], s[70:71]\n" \
"v_pk_mul_f32 v[38:39], v[38:39], s[70:71]\n" \
"v_cvt_pk_bf16_f32 v33, v34, v35\n" \
"v_pk_mul_f32 v[128:129], v[128:129], s[70:71]\n" \
"v_pk_mul_f32 v[130:131], v[130:131], s[70:71]\n" \
"v_mfma_f32_16x16x32_bf16 a[56:59], v[108:111], v[56:59], a[56:59]\n" \
"v_exp_f32_e32 v224, v224\n" \
"v_exp_f32_e32 v226, v226\n" \
"v_fma_f32 v240, v240, s72, -v241\n" \
"v_fma_f32 v242, v242, s72, -v241\n" \
"v_cvt_pk_bf16_f32 v62, v64, v65\n" \
"v_cvt_pk_bf16_f32 v63, v66, v67\n" \
"v_cvt_pk_bf16_f32 v134, v136, v137\n" \
"v_cvt_pk_bf16_f32 v135, v138, v139\n" \
"v_cvt_pk_bf16_f32 v34, v36, v37\n" \
"v_cvt_pk_bf16_f32 v35, v38, v39\n" \
"v_cvt_pk_bf16_f32 v192, v192, v194\n" \
"v_cvt_pk_bf16_f32 v208, v208, v210\n" \
"v_exp_f32_e32 v240, v240\n" \
"v_exp_f32_e32 v242, v242\n" \
"ds_write_b128 v27, v[60:63] offset:13312\n" \
"ds_write_b128 v27, v[132:135] offset:0\n" \
"ds_write_b128 v27, v[32:35] offset:1024\n" \
"v_cvt_pk_bf16_f32 v68, v48, v49\n" \
"v_cvt_pk_bf16_f32 v69, v50, v51\n" \
"v_cvt_pk_bf16_f32 v70, v196, v197\n" \
"v_cvt_pk_bf16_f32 v71, v198, v199\n" \
"v_cvt_pk_bf16_f32 v72, v212, v213\n" \
"v_cvt_pk_bf16_f32 v73, v214, v215\n" \
"v_perm_b32 v184, v192, v192, v40\n" \
"v_perm_b32 v185, v192, v192, v41\n" \
"v_perm_b32 v186, v192, v192, v42\n" \
"v_perm_b32 v187, v192, v192, v43\n" \
"v_perm_b32 v200, v208, v208, v40\n" \
"v_perm_b32 v201, v208, v208, v41\n" \
"v_perm_b32 v202, v208, v208, v42\n" \
"v_perm_b32 v203, v208, v208, v43\n" \
"v_cvt_pk_bf16_f32 v224, v224, v226\n" \
"v_cvt_pk_bf16_f32 v74, v228, v229\n" \
"v_cvt_pk_bf16_f32 v75, v230, v231\n" \
"v_mfma_f32_16x16x32_bf16 v[148:151], v[68:71], v[184:187], 0\n" \
"v_mfma_f32_16x16x32_bf16 v[152:155], v[70:73], v[200:203], 0\n" \
"v_perm_b32 v216, v224, v224, v40\n" \
"v_perm_b32 v217, v224, v224, v41\n" \
"v_perm_b32 v218, v224, v224, v42\n" \
"v_perm_b32 v219, v224, v224, v43\n" \
"v_cvt_pk_bf16_f32 v240, v240, v242\n" \
"v_mfma_f32_16x16x32_bf16 a[56:59], v[172:175], v[120:123], a[56:59]\n" \
"v_cvt_pk_bf16_f32 v76, v244, v245\n" \
"v_cvt_pk_bf16_f32 v77, v246, v247\n" \
"v_mfma_f32_16x16x32_bf16 v[156:159], v[72:75], v[216:219], 0\n" \
"v_perm_b32 v232, v240, v240, v40\n" \
"v_perm_b32 v233, v240, v240, v41\n" \
"v_perm_b32 v234, v240, v240, v42\n" \
"v_perm_b32 v235, v240, v240, v43\n" \
"v_mfma_f32_16x16x32_bf16 a[52:55], v[104:107], v[56:59], a[52:55]\n" \
"v_accvgpr_read_b32 v124, a56\n" \
"v_accvgpr_read_b32 v125, a57\n" \
"v_accvgpr_read_b32 v126, a58\n" \
"v_accvgpr_read_b32 v127, a59\n" \
"v_pk_mul_f32 v[124:125], v[124:125], s[70:71]\n" \
"v_pk_mul_f32 v[126:127], v[126:127], s[70:71]\n" \
"v_cvt_pk_bf16_f32 v124, v124, v125\n" \
"v_cvt_pk_bf16_f32 v125, v126, v127\n" \
"v_cvt_pk_bf16_f32 v126, v128, v129\n" \
"v_cvt_pk_bf16_f32 v127, v130, v131\n" \
"ds_write_b128 v27, v[124:127] offset:5120\n" \
"v_mfma_f32_16x16x32_bf16 v[160:163], v[74:77], v[232:235], 0\n" \
"v_accvgpr_read_b32 v252, a12\n" \
"v_accvgpr_read_b32 v253, a13\n" \
"v_accvgpr_read_b32 v254, a14\n" \
"v_accvgpr_read_b32 v255, a15\n" \
"v_mfma_f32_16x16x32_bf16 a[52:55], v[168:171], v[120:123], a[52:55]\n" \
"v_cvt_pk_bf16_f32 v84, v252, v253\n" \
"v_cvt_pk_bf16_f32 v85, v254, v255\n" \
"v_exp_f32_e64 v45, v45\n" \
"v_mfma_f32_16x16x32_bf16 v[208:211], v[86:89], v[200:203], 0\n" \
"v_mfma_f32_16x16x32_bf16 v[192:195], v[84:87], v[184:187], 0\n" \
"v_mfma_f32_16x16x32_bf16 v[224:227], v[88:91], v[216:219], 0\n" \
"v_mfma_f32_16x16x32_bf16 v[240:243], v[90:93], v[232:235], 0\n" \
"v_mov_b32_dpp v150, v154 quad_perm:[0,1,2,3] row_mask:0x2 bank_mask:0xf\n" \
"v_accvgpr_read_b32 v144, a52\n" \
"v_accvgpr_read_b32 v145, a53\n" \
"v_accvgpr_read_b32 v146, a54\n" \
"v_accvgpr_read_b32 v147, a55\n" \
"v_pk_mul_f32 v[144:145], v[144:145], s[70:71]\n" \
"v_pk_mul_f32 v[146:147], v[146:147], s[70:71]\n" \
"v_mov_b32_dpp v150, v158 quad_perm:[0,1,2,3] row_mask:0x4 bank_mask:0xf\n" \
"v_add_f32_e64 v45, 1.0, v45\n" \
"v_mfma_f32_16x16x32_bf16 a[48:51], v[100:103], v[56:59], a[48:51]\n" \
"v_mov_b32_dpp v150, v162 quad_perm:[0,1,2,3] row_mask:0x8 bank_mask:0xf\n" \
"v_mov_b32_dpp v148, v152 quad_perm:[0,1,2,3] row_mask:0x2 bank_mask:0xf\n" \
"v_rcp_f32_e64 v45, v45\n" \
"v_mov_b32_dpp v149, v153 quad_perm:[0,1,2,3] row_mask:0x2 bank_mask:0xf\n" \
"v_rcp_f32_e64 v150, v150\n" \
"v_mov_b32_dpp v148, v156 quad_perm:[0,1,2,3] row_mask:0x4 bank_mask:0xf\n" \
"v_mov_b32_dpp v149, v157 quad_perm:[0,1,2,3] row_mask:0x4 bank_mask:0xf\n" \
"v_mul_f32_e64 v150, v45, v150\n" \
"v_mov_b32_dpp v148, v160 quad_perm:[0,1,2,3] row_mask:0x8 bank_mask:0xf\n" \
"v_mov_b32_dpp v149, v161 quad_perm:[0,1,2,3] row_mask:0x8 bank_mask:0xf\n" \
"v_mul_f32_e32 v148, v150, v148\n" \
"v_mul_f32_e32 v149, v150, v149\n" \
"v_mov_b32_e64 v154, v150\n" \
"v_mfma_f32_16x16x32_bf16 a[48:51], v[164:167], v[120:123], a[48:51]\n" \
"v_sub_f32_e32 v44, 1.0, v45\n" \
"v_permlane16_swap_b32_e32 v150, v154\n" \
"v_mov_b32_e32 v158, v150\n" \
"v_mov_b32_e32 v162, v154\n" \
"v_cvt_pk_bf16_f32 v148, v148, v149\n" \
"v_permlane32_swap_b32_e32 v150, v158\n" \
"v_permlane32_swap_b32_e32 v154, v162\n" \
"v_mul_f32_e64 v192, v150, v192\n" \
"v_accvgpr_read_b32 v140, a48\n" \
"v_accvgpr_read_b32 v141, a49\n" \
"v_accvgpr_read_b32 v142, a50\n" \
"v_accvgpr_read_b32 v143, a51\n" \
"v_pk_mul_f32 v[140:141], v[140:141], s[70:71]\n" \
"v_pk_mul_f32 v[142:143], v[142:143], s[70:71]\n" \
"v_cvt_pk_bf16_f32 v140, v140, v141\n" \
"v_cvt_pk_bf16_f32 v141, v142, v143\n" \
"v_cvt_pk_bf16_f32 v142, v144, v145\n" \
"v_cvt_pk_bf16_f32 v143, v146, v147\n" \
"ds_write_b128 v27, v[140:143] offset:4096\n" \
"v_mul_f32_e32 v193, v150, v193\n" \
"v_mul_f32_e32 v208, v154, v208\n" \
"v_mul_f32_e32 v209, v154, v209\n" \
"v_mul_f32_e32 v224, v158, v224\n" \
"v_mul_f32_e32 v225, v158, v225\n" \
"v_mul_f32_e32 v240, v162, v240\n" \
"v_mul_f32_e32 v241, v162, v241\n" \
"v_mul_f32_e32 v194, v150, v194\n" \
"v_mul_f32_e64 v195, v150, v195\n" \
"v_cvt_pk_bf16_f32 v192, v192, v193\n" \
"v_mul_f32_e32 v210, v154, v210\n" \
"v_mul_f32_e32 v211, v154, v211\n" \
"v_cvt_pk_bf16_f32 v208, v208, v209\n" \
"v_mul_f32_e32 v226, v158, v226\n" \
"v_mul_f32_e32 v227, v158, v227\n" \
"v_cvt_pk_bf16_f32 v224, v224, v225\n" \
"v_mul_f32_e32 v242, v162, v242\n" \
"v_mul_f32_e32 v243, v162, v243\n" \
"v_cvt_pk_bf16_f32 v240, v240, v241\n" \
"v_cvt_pk_bf16_f32 v193, v194, v195\n" \
"s_add_u32 s84, s48, 0x0\n" \
"s_nop 0\n" \
"v_cvt_pk_bf16_f32 v209, v210, v211\n" \
"s_add_u32 s85, s48, 0x24800\n" \
"v_cvt_pk_bf16_f32 v225, v226, v227\n" \
"s_add_u32 s86, s48, 0x49000\n" \
"v_cvt_pk_bf16_f32 v241, v242, v243\n" \
"s_add_u32 s87, s48, 0x6d800\n" \
"ds_write_b32 v26, v44\n" \
"buffer_store_dword v148, v25, s[80:83], s49 offen sc1\n" \
"buffer_store_dwordx2 v[192:193], v24, s[80:83], s84 offen sc1\n" \
"buffer_store_dwordx2 v[208:209], v24, s[80:83], s85 offen sc1\n" \
"buffer_store_dwordx2 v[224:225], v24, s[80:83], s86 offen sc1\n" \
"buffer_store_dwordx2 v[240:241], v24, s[80:83], s87 offen sc1\n" \
"s_branch L_end_%=\n" \
"L_poll61_%=:\n" \
"s_mov_b32 s84, 0\n" \
"L_pl61_%=:\n" \
"s_sleep 1\n" \
"v_bfe_u32 v44, v20, 2, 5\n" \
"buffer_load_dword v44, v44, s[80:83], s38 offen offset:128 sc1\n" \
"s_waitcnt vmcnt(0)\n" \
"v_cmp_ne_u32_e32 vcc, s62, v44\n" \
"s_add_u32 s84, s84, 1\n" \
"s_nop 3\n" \
"s_cbranch_vccz L_pollr61_%=\n" \
"s_cmp_gt_u32 s84, 0x400000\n" \
"s_cbranch_scc0 L_pl61_%=\n" \
"s_branch L_pollr61_%=\n" \
"L_poll161_%=:\n" \
"s_mov_b32 s84, 0\n" \
"L_pl161_%=:\n" \
"s_sleep 1\n" \
"v_bfe_u32 v44, v20, 2, 5\n" \
"buffer_load_dword v44, v44, s[80:83], s38 offen offset:0 sc1\n" \
"s_waitcnt vmcnt(0)\n" \
"v_cmp_ne_u32_e32 vcc, s62, v44\n" \
"s_add_u32 s84, s84, 1\n" \
"s_nop 3\n" \
"s_cbranch_vccz L_pollr161_%=\n" \
"s_cmp_gt_u32 s84, 0x400000\n" \
"s_cbranch_scc0 L_pl161_%=\n" \
"s_branch L_pollr161_%=\n" \
"L_end_%=:\n"
#define PROJ_VCLOB "v21","v24","v25","v28","v29","v32","v33","v34","v35","v36","v37","v38","v39","v40","v41","v42","v43","v44","v45","v46","v47","v48","v49","v50","v51","v52","v53","v54","v55","v56","v57","v58","v59","v60","v61","v62","v63","v64","v65","v66","v67","v68","v69","v70","v71","v72","v73","v74","v75","v76","v77","v78","v79","v80","v81","v82","v83","v84","v85","v86","v87","v88","v89","v90","v91","v92","v93","v94","v95","v96","v97","v98","v99","v100","v101","v102","v103","v104","v105","v106","v107","v108","v109","v110","v111","v112","v113","v114","v115","v116","v117","v118","v119","v120","v121","v122","v123","v124","v125","v126","v127","v128","v129","v130","v131","v132","v133","v134","v135","v136","v137","v138","v139","v140","v141","v142","v143","v144","v145","v146","v147","v148","v149","v150","v151","v152","v153","v154","v155","v156","v157","v158","v159","v160","v161","v162","v163","v164","v165","v166","v167","v168","v169","v170","v171","v172","v173","v174","v175","v176","v177","v178","v179","v180","v181","v182","v183","v184","v185","v186","v187","v188","v189","v190","v192","v193","v194","v195","v196","v197","v198","v199","v200","v201","v202","v203","v204","v205","v206","v207","v208","v209","v210","v211","v212","v213","v214","v215","v216","v217","v218","v219","v220","v221","v222","v223","v224","v225","v226","v227","v228","v229","v230","v231","v232","v233","v234","v235","v236","v237","v238","v239","v240","v241","v242","v243","v244","v245","v246","v247","v248","v249","v250","v251","v252","v253","v254","v255"
#define PROJ_SCLOB "s4","s5","s6","s7","s32","s33","s34","s35","s36","s37","s38","s39","s40","s41","s42","s43","s44","s45","s46","s47","s48","s49","s50","s51","s52","s53","s54","s55","s63","s64","s65","s66","s67","s68","s69","s70","s71","s72","s73","s74","s75","s76","s77","s78","s79","s80","s81","s82","s83","s84","s85","s86","s87","s88","s89","s90","s91","s92","s93","s94","s95","s96","s97","s98","s99","s100","s101"

typedef const u16* const __attribute__((address_space(4)))* kaptr;
__device__ __forceinline__ void proj_phase(kaptr& ka, char*& ws, const unsigned wid, u32x4 (&vp)[5], unsigned& vl, u32x4 (&sa)[5], u32x2 s24, u32x2 s28, unsigned tdum, u32x4& srdA, u32x2& sd, unsigned& tag
    ) {
    const unsigned b = blockIdx.x, h = (b & 7) * 4 + ((b >> 3) & 3), mt = b >> 5;
    const unsigned rb = (wid < 2u ? 15u - mt : mt) * 128u;
    const unsigned lane = threadIdx.x & 63u, wc = wid & 1u;
    u32x2 vAc, vB;
#pragma unroll
    for (int par = 0; par < 2; ++par) {
        const unsigned lr = lane >> 3, cc = (lane & 7u) ^ (4u * par + (lr >> 1)), r0 = rb + wc * 64u + lr;
        vAc[par] = r0 * (DC * 2) + cc * 16u;
        const unsigned br = lane >> 4, p16 = lane & 15u, sc = (((p16 >> 1) ^ (br | (par << 2))) << 1) | (p16 & 1u);
        vB[par] = (16u * wid + br) * (NBIG * 2) + h * (HD * 2) + sc * 16u;
    }
    vl = lane * 16u;
    u32x2 s30 = {wid * 8192u, 65536u + wid * 4096u};
    asm volatile(PROJ_ASM
                 : "={s62}"(tag), "={s[56:59]}"(srdA), "={s[60:61]}"(sd), "+{s[8:11]}"(sa[0]), "+{s[12:15]}"(sa[1]), "+{s[16:19]}"(sa[2]), "+{s[20:23]}"(sa[3]), "+{s[24:25]}"(s24), "+{s[28:29]}"(s28), "+{s[30:31]}"(s30),
                   "={v[0:3]}"(vp[0]), "={v[4:7]}"(vp[1]), "={v[8:11]}"(vp[2]), "={v[12:15]}"(vp[3]), "={v[16:19]}"(vp[4]), "+{v[22:23]}"(vAc), "+{v[26:27]}"(vB), "+{v191}"(tdum), "+{s[26:27]}"(ws), "+{s[0:1]}"(ka)
                 : "{s2}"(b), "{s3}"(wid), "{v20}"(vl)
                 : "memory", "m0", "vcc", "scc", PROJ_VCLOB, PROJ_SCLOB, ACLOB_LO, ACLOB_HI);
    sa[4] = (u32x4){s24[0], 0u, 0u, 0u};
}

namespace att {
#define DI __device__ __forceinline__
typedef __bf16 bfx8 __attribute__((ext_vector_type(8)));
DI unsigned rfl(unsigned x) { return __builtin_amdgcn_readfirstlane(x); }
DI f32x4 mfma(u32x4 a, u32x4 b, f32x4 c) { return __builtin_amdgcn_mfma_f32_16x16x32_bf16(__builtin_bit_cast(bfx8, a), __builtin_bit_cast(bfx8, b), c, 0, 0, 0); }
typedef __bf16 bfx2 __attribute__((ext_vector_type(2)));
typedef float f32x2 __attribute__((ext_vector_type(2)));
DI unsigned cvtpk2(float lo, float hi) { f32x2 v = {lo, hi}; bfx2 b = __builtin_convertvector(v, bfx2); return __builtin_bit_cast(unsigned, b); }
DI void att_exact(const u16* __restrict__ x, const u16* __restrict__ wq, char* __restrict__ ws, unsigned h, unsigned r0, float g1) {
    const unsigned lane = lane_id(), g = lane >> 4, c = lane & 15u;
    f32x4 o[8];
#pragma unroll
    for (int n = 0; n < 8; ++n) o[n] = (f32x4){0.f, 0.f, 0.f, 0.f};
#pragma unroll 1
    for (unsigned kk = 0; kk < (unsigned)DM; kk += 32u) {
        const unsigned k = kk + 8u * g;
        const u32x4 xf = *reinterpret_cast<const u32x4*>(x + (r0 + c) * DM + k);
#pragma unroll
        for (int n = 0; n < 8; ++n) {
            const u16* w = wq + k * NBIG + h * HD + n * 16 + c; u32x4 wf;
#pragma unroll
            for (int e = 0; e < 4; ++e) wf[e] = (unsigned)w[2 * e * NBIG] | ((unsigned)w[(2 * e + 1) * NBIG] << 16);
            o[n] = mfma(wf, xf, o[n]);
        }
    }
    u32x4 q[4];
#pragma unroll
    for (int p = 0; p < 4; ++p) {
        const f32x4 a = o[2 * p] * 0.12751743082f, b = o[2 * p + 1] * 0.12751743082f;
        q[p] = (u32x4){cvtpk2(a[0], a[1]), cvtpk2(a[2], a[3]), cvtpk2(b[0], b[1]), cvtpk2(b[2], b[3])};
    }
#pragma unroll
    for (int n = 0; n < 8; ++n) o[n] = (f32x4){0.f, 0.f, 0.f, 0.f};
    const __amdgpu_buffer_rsrc_t srd = __builtin_amdgcn_make_buffer_rsrc((void*)(ws + WS_K + (size_t)h * 1048576), (short)0, 1048576, 0x00020000);
    float m = -3.0e38f, l = 0.f;
#pragma unroll 1
    for (unsigned k0 = 0; k0 <= r0 + 15u; k0 += 32u) {
        const unsigned tb = (k0 >> 6) * 32768u + (k0 & 32u) * 256u;
        f32x4 s[2]; float mx = m;
#pragma unroll
        for (int kt = 0; kt < 2; ++kt) {
            s[kt] = (f32x4){0.f, 0.f, 0.f, 0.f};
#pragma unroll
            for (int p = 0; p < 4; ++p) s[kt] = mfma(__builtin_amdgcn_raw_buffer_load_b128(srd, lane * 16u + (kt * 4 + p) * 1024u, tb, 16), q[p], s[kt]);
#pragma unroll
            for (int e = 0; e < 4; ++e) { if (k0 + 16u * kt + 4u * g + e > r0 + c) s[kt][e] = -3.0e38f; mx = fmaxf(mx, s[kt][e]); }
        }
        mx = fmaxf(mx, __shfl_xor(mx, 16)); mx = fmaxf(mx, __shfl_xor(mx, 32));
        const float al = __builtin_amdgcn_exp2f(m - mx); m = mx;
        u32x4 pf;
#pragma unroll
        for (int e = 0; e < 4; ++e) pf[e] = cvtpk2(__builtin_amdgcn_exp2f(s[e >> 1][2 * (e & 1)] - m), __builtin_amdgcn_exp2f(s[e >> 1][2 * (e & 1) + 1] - m));
        l *= al;
#pragma unroll
        for (int e = 0; e < 4; ++e) l += __uint_as_float(pf[e] << 16) + __uint_as_float(pf[e] & 0xffff0000u);
#pragma unroll
        for (int n = 0; n < 8; ++n) o[n] = mfma(__builtin_amdgcn_raw_buffer_load_b128(srd, lane * 16u + 16384u + n * 1024u, tb, 16), pf, o[n] * al);
    }
    l += __shfl_xor(l, 16); l += __shfl_xor(l, 32);
    const float f = g1 / l;
    const __amdgpu_buffer_rsrc_t srdA = __builtin_amdgcn_make_buffer_rsrc((void*)(ws + WS_AF), (short)0, 128 * KS_TOT * 1024, 0x00020000);
#pragma unroll
    for (int p = 0; p < 4; ++p) {
        const f32x4 a = o[2 * p] * f, b = o[2 * p + 1] * f;
        __builtin_amdgcn_raw_buffer_store_b128((u32x4){cvtpk2(a[0], a[1]), cvtpk2(a[2], a[3]), cvtpk2(b[0], b[1]), cvtpk2(b[2], b[3])}, srdA, lane * 16u + p * 1024u, ((r0 >> 4) * KS_TOT + 18u + 4u * h) * 1024u, 16);
    }
}
}

#define ATT_ASM \
"s_branch L_ago_%=\n" \
".p2align 12\n" \
"L_ab:\n" \
".fill (L_ab + 0 - .) / 4, 4, 0xBF800000\n" \
"L_ap0:\n" \
"s_setpc_b64 s[88:89]\n" \
"L_ago_%=:\n" \
"s_mov_b32 s101, 0\n" \
"s_waitcnt vmcnt(15) lgkmcnt(0)\n" \
"s_barrier\n" \
"v_accvgpr_write_b32 a216, 0\n" \
"v_accvgpr_write_b32 a217, 0\n" \
"v_accvgpr_write_b32 a218, 0\n" \
"v_accvgpr_write_b32 a219, 0\n" \
"v_bfe_u32 v61, v20, 2, 5\n" \
"s_mov_b32 s96, s24\n" \
"v_mfma_f32_32x32x16_bf16 v[128:143], a[216:219], a[216:219], 0\n" \
"buffer_load_dword v57, v61, s[56:59], s96 offen sc1\n" \
"v_and_b32_e32 v63, 0x300, v20\n" \
"ds_read_b128 v[96:99], v63 offset:0\n" \
"v_add_u32_e32 v58, s22, v20\n" \
"ds_read_b128 a[0:3], v58 offset:0\n" \
"v_mfma_f32_32x32x16_bf16 v[144:159], a[216:219], a[216:219], 0\n" \
"ds_read_b128 a[16:19], v58 offset:8192\n" \
"v_add_u32_e32 v59, s23, v20\n" \
"ds_read_b128 a[32:35], v59 offset:0\n" \
"ds_read_b128 a[48:51], v59 offset:8192\n" \
"ds_read_b128 v[100:103], v63 offset:1024\n" \
"ds_read_b128 a[4:7], v58 offset:1024\n" \
"v_mfma_f32_32x32x16_bf16 v[160:175], a[216:219], a[216:219], 0\n" \
"ds_read_b128 a[20:23], v58 offset:9216\n" \
"ds_read_b128 a[36:39], v59 offset:1024\n" \
"ds_read_b128 a[52:55], v59 offset:9216\n" \
"s_sub_u32 s67, s12, 0xc000\n" \
"ds_read_b128 v[104:107], v63 offset:2048\n" \
"s_mov_b32 s65, s12\n" \
"v_mfma_f32_32x32x16_bf16 v[176:191], a[216:219], a[216:219], 0\n" \
"v_add_u32_e32 v55, s67, v20\n" \
"v_add_u32_e32 v52, 0xc000, v55\n" \
"s_xor_b32 m0, s65, 0x10000\n" \
"ds_read_b128 a[8:11], v58 offset:2048\n" \
"buffer_load_dwordx4 v52, s[8:11], 0 offen offset:0 sc1 lds\n" \
"v_mfma_f32_32x32x16_bf16 v[192:207], a[216:219], a[216:219], 0\n" \
"ds_read_b128 a[24:27], v58 offset:10240\n" \
"ds_read_b128 a[40:43], v59 offset:2048\n" \
"ds_read_b128 a[56:59], v59 offset:10240\n" \
"ds_read_b128 v[108:111], v63 offset:3072\n" \
"buffer_load_dwordx4 v52, s[8:11], 0 offen offset:1024 sc1 lds\n" \
"v_mfma_f32_32x32x16_bf16 v[208:223], a[216:219], a[216:219], 0\n" \
"ds_read_b128 a[12:15], v58 offset:3072\n" \
"ds_read_b128 a[28:31], v58 offset:11264\n" \
"ds_read_b128 a[44:47], v59 offset:3072\n" \
"ds_read_b128 a[60:63], v59 offset:11264\n" \
"s_waitcnt lgkmcnt(15)\n" \
"v_xor_b32_e32 v96, 0x80008000, v96\n" \
"v_mfma_f32_32x32x16_bf16 v[224:239], a[216:219], a[216:219], 0\n" \
"v_xor_b32_e32 v97, 0x80008000, v97\n" \
"v_xor_b32_e32 v98, 0x80008000, v98\n" \
"v_xor_b32_e32 v99, 0x80008000, v99\n" \
"ds_read_b128 a[160:163], v20 offset:0\n" \
"ds_read_b128 a[164:167], v20 offset:1024\n" \
"s_waitcnt lgkmcnt(15)\n" \
"v_xor_b32_e32 v100, 0x80008000, v100\n" \
"v_mfma_f32_32x32x16_bf16 v[240:255], a[216:219], a[216:219], 0\n" \
"v_xor_b32_e32 v101, 0x80008000, v101\n" \
"v_xor_b32_e32 v102, 0x80008000, v102\n" \
"v_xor_b32_e32 v103, 0x80008000, v103\n" \
"ds_read_b128 a[168:171], v20 offset:2048\n" \
"ds_read_b128 a[172:175], v20 offset:3072\n" \
"ds_read_b128 a[188:191], v20 offset:7168\n" \
"v_mfma_f32_16x16x32_bf16 a[192:195], a[216:219], a[216:219], 0\n" \
"ds_read_b128 a[184:187], v20 offset:6144\n" \
"s_waitcnt lgkmcnt(15)\n" \
"v_xor_b32_e32 v104, 0x80008000, v104\n" \
"v_mfma_f32_16x16x32_bf16 a[196:199], a[216:219], a[216:219], 0\n" \
"v_xor_b32_e32 v105, 0x80008000, v105\n" \
"v_xor_b32_e32 v106, 0x80008000, v106\n" \
"v_mfma_f32_16x16x32_bf16 v[32:35], v[96:99], a[0:3], 0\n" \
"v_xor_b32_e32 v107, 0x80008000, v107\n" \
"ds_read_b128 a[180:183], v20 offset:5120\n" \
"v_mfma_f32_16x16x32_bf16 v[36:39], v[96:99], a[16:19], 0\n" \
"ds_read_b128 a[176:179], v20 offset:4096\n" \
"s_waitcnt lgkmcnt(12)\n" \
"v_xor_b32_e32 v108, 0x80008000, v108\n" \
"v_mfma_f32_16x16x32_bf16 v[40:43], v[96:99], a[32:35], 0\n" \
"v_xor_b32_e32 v109, 0x80008000, v109\n" \
"v_xor_b32_e32 v110, 0x80008000, v110\n" \
"v_mfma_f32_16x16x32_bf16 v[44:47], v[96:99], a[48:51], 0\n" \
"v_xor_b32_e32 v111, 0x80008000, v111\n" \
"v_add_u32_e32 v60, s15, v20\n" \
"v_mfma_f32_16x16x32_bf16 v[32:35], v[100:103], a[4:7], v[32:35]\n" \
"v_add_u32_e32 v60, 0x10000, v60\n" \
"ds_read_b128 a[128:131], v20 offset:8192\n" \
"v_mfma_f32_16x16x32_bf16 v[36:39], v[100:103], a[20:23], v[36:39]\n" \
"ds_read_b128 a[144:147], v20 offset:12288\n" \
"ds_read_b128 a[132:135], v20 offset:9216\n" \
"v_mfma_f32_16x16x32_bf16 v[40:43], v[100:103], a[36:39], v[40:43]\n" \
"ds_read_b128 a[148:151], v20 offset:13312\n" \
"ds_read_b128 a[136:139], v20 offset:10240\n" \
"v_mfma_f32_16x16x32_bf16 v[44:47], v[100:103], a[52:55], v[44:47]\n" \
"ds_read_b128 a[152:155], v20 offset:14336\n" \
"ds_read_b128 a[140:143], v20 offset:11264\n" \
"v_mfma_f32_16x16x32_bf16 v[32:35], v[104:107], a[8:11], v[32:35]\n" \
"ds_read_b128 a[156:159], v20 offset:15360\n" \
"ds_read_b128 a[64:67], v60 offset:16384\n" \
"v_mfma_f32_16x16x32_bf16 v[36:39], v[104:107], a[24:27], v[36:39]\n" \
"ds_read_b128 a[68:71], v60 offset:17408\n" \
"ds_read_b128 a[72:75], v60 offset:18432\n" \
"v_mfma_f32_16x16x32_bf16 v[40:43], v[104:107], a[40:43], v[40:43]\n" \
"ds_read_b128 a[76:79], v60 offset:19456\n" \
"ds_read_b128 a[80:83], v60 offset:24576\n" \
"v_mfma_f32_16x16x32_bf16 v[44:47], v[104:107], a[56:59], v[44:47]\n" \
"ds_read_b128 a[84:87], v60 offset:25600\n" \
"s_waitcnt lgkmcnt(15)\n" \
"v_mfma_f32_16x16x32_bf16 v[32:35], v[108:111], a[12:15], v[32:35]\n" \
"ds_read_b128 a[88:91], v60 offset:26624\n" \
"ds_read_b128 a[92:95], v60 offset:27648\n" \
"v_mfma_f32_16x16x32_bf16 v[36:39], v[108:111], a[28:31], v[36:39]\n" \
"ds_read_b128 a[96:99], v60 offset:0\n" \
"ds_read_b128 a[100:103], v60 offset:1024\n" \
"v_mfma_f32_16x16x32_bf16 v[40:43], v[108:111], a[44:47], v[40:43]\n" \
"ds_read_b128 a[104:107], v60 offset:2048\n" \
"ds_read_b128 a[108:111], v60 offset:3072\n" \
"v_mfma_f32_16x16x32_bf16 v[44:47], v[108:111], a[60:63], v[44:47]\n" \
"ds_read_b128 a[112:115], v60 offset:8192\n" \
"ds_read_b128 a[116:119], v60 offset:9216\n" \
"v_mfma_f32_16x16x32_bf16 v[64:67], a[160:163], a[0:3], v[32:35]\n" \
"ds_read_b128 a[120:123], v60 offset:10240\n" \
"ds_read_b128 a[124:127], v60 offset:11264\n" \
"v_mfma_f32_16x16x32_bf16 v[64:67], a[164:167], a[4:7], v[64:67]\n" \
"v_bfe_u32 v26, v20, 8, 2\n" \
"s_mov_b32 m0, s20\n" \
"v_mfma_f32_16x16x32_bf16 v[64:67], a[168:171], a[8:11], v[64:67]\n" \
"v_bfe_u32 v25, v20, 5, 3\n" \
"v_lshlrev_b32_e32 v26, 1, v26\n" \
"v_mfma_f32_16x16x32_bf16 v[64:67], a[172:175], a[12:15], v[64:67]\n" \
"global_load_lds_dword v[16:17], off\n" \
"v_mfma_f32_16x16x32_bf16 v[68:71], a[188:191], a[12:15], v[32:35]\n" \
"v_sub_u32_e32 v25, v25, v26\n" \
"v_and_b32_e32 v26, 16, v20\n" \
"v_mfma_f32_16x16x32_bf16 v[68:71], a[184:187], a[8:11], v[68:71]\n" \
"v_mov_b32_e32 v27, 0x3f80\n" \
"v_lshlrev_b32_e32 v26, v26, v27\n" \
"v_mfma_f32_16x16x32_bf16 v[68:71], a[180:183], a[4:7], v[68:71]\n" \
"v_mov_b32_e32 v54, 0x3f803f80\n" \
"s_waitcnt lgkmcnt(15)\n" \
"v_mfma_f32_16x16x32_bf16 v[68:71], a[176:179], a[0:3], v[68:71]\n" \
"s_mov_b32 s84, 0x33333333\n" \
"s_mov_b32 s85, 0xcccccccc\n" \
"v_mfma_f32_16x16x32_bf16 v[76:79], a[176:179], a[16:19], v[36:39]\n" \
"s_mov_b32 s86, 0x11111111\n" \
"s_mov_b32 s87, 0x11111111\n" \
"v_mfma_f32_16x16x32_bf16 v[76:79], a[180:183], a[20:23], v[76:79]\n" \
"s_mov_b32 s88, 0x44444444\n" \
"s_mov_b32 s89, 0x44444444\n" \
"v_mfma_f32_16x16x32_bf16 v[76:79], a[184:187], a[24:27], v[76:79]\n" \
"v_exp_f32_e32 v70, v70\n" \
"v_mfma_f32_16x16x32_bf16 v[76:79], a[188:191], a[28:31], v[76:79]\n" \
"v_exp_f32_e32 v71, v71\n" \
"v_mfma_f32_16x16x32_bf16 v[72:75], a[172:175], a[28:31], v[36:39]\n" \
"v_exp_f32_e32 v68, v68\n" \
"v_mfma_f32_16x16x32_bf16 v[72:75], a[168:171], a[24:27], v[72:75]\n" \
"v_exp_f32_e32 v69, v69\n" \
"v_mfma_f32_16x16x32_bf16 v[72:75], a[164:167], a[20:23], v[72:75]\n" \
"v_bfe_i32 v21, v20, 4, 1\n" \
"v_cvt_pk_bf16_f32 v71, v70, v71\n" \
"v_mfma_f32_16x16x32_bf16 v[72:75], a[160:163], a[16:19], v[72:75]\n" \
"v_exp_f32_e32 v66, v66\n" \
"v_mfma_f32_16x16x32_bf16 v[80:83], a[160:163], a[32:35], v[40:43]\n" \
"v_exp_f32_e32 v67, v67\n" \
"v_mfma_f32_16x16x32_bf16 v[80:83], a[164:167], a[36:39], v[80:83]\n" \
"v_exp_f32_e32 v64, v64\n" \
"v_mfma_f32_16x16x32_bf16 v[80:83], a[168:171], a[40:43], v[80:83]\n" \
"v_exp_f32_e32 v65, v65\n" \
"v_mfma_f32_16x16x32_bf16 v[80:83], a[172:175], a[44:47], v[80:83]\n" \
"v_exp_f32_e32 v72, v72\n" \
"v_mfma_f32_16x16x32_bf16 v[84:87], a[188:191], a[44:47], v[40:43]\n" \
"s_waitcnt vmcnt(3)\n" \
"v_cmp_ne_u32_e32 vcc, s62, v57\n" \
"v_mfma_f32_16x16x32_bf16 v[84:87], a[184:187], a[40:43], v[84:87]\n" \
"v_exp_f32_e32 v73, v73\n" \
"v_mfma_f32_16x16x32_bf16 v[84:87], a[180:183], a[36:39], v[84:87]\n" \
"v_exp_f32_e32 v74, v74\n" \
"v_mfma_f32_16x16x32_bf16 v[84:87], a[176:179], a[32:35], v[84:87]\n" \
"s_cbranch_vccnz L_skb_%=\n" \
"L_rkb_%=:\n" \
"v_cmp_eq_u32_e32 vcc, 0, v25\n" \
"v_mfma_f32_16x16x32_bf16 v[92:95], a[176:179], a[48:51], v[44:47]\n" \
"v_exp_f32_e32 v75, v75\n" \
"v_mfma_f32_16x16x32_bf16 v[92:95], a[180:183], a[52:55], v[92:95]\n" \
"v_cndmask_b32_e32 v28, 0, v26, vcc\n" \
"v_cvt_pk_bf16_f32 v72, v72, v73\n" \
"v_mfma_f32_16x16x32_bf16 v[92:95], a[184:187], a[56:59], v[92:95]\n" \
"v_exp_f32_e32 v76, v76\n" \
"v_mfma_f32_16x16x32_bf16 v[92:95], a[188:191], a[60:63], v[92:95]\n" \
"v_exp_f32_e32 v77, v77\n" \
"v_mfma_f32_16x16x32_bf16 v[88:91], a[172:175], a[60:63], v[44:47]\n" \
"v_exp_f32_e32 v78, v78\n" \
"v_mfma_f32_16x16x32_bf16 v[88:91], a[168:171], a[56:59], v[88:91]\n" \
"v_exp_f32_e32 v79, v79\n" \
"v_mfma_f32_16x16x32_bf16 v[88:91], a[164:167], a[52:55], v[88:91]\n" \
"v_cndmask_b32_e64 v22, 0, v54, s[84:85]\n" \
"v_cndmask_b32_e64 v23, 0, v54, s[86:87]\n" \
"v_mfma_f32_16x16x32_bf16 v[88:91], a[160:163], a[48:51], v[88:91]\n" \
"v_cndmask_b32_e64 v24, 0, v54, s[88:89]\n" \
"v_and_b32_e32 v21, 0xaaaaaaaa, v21\n" \
"s_waitcnt vmcnt(4) lgkmcnt(0)\n" \
"s_barrier\n" \
"v_cmp_eq_u32_e32 vcc, 1, v25\n" \
"v_cvt_pk_bf16_f32 v70, v68, v69\n" \
"v_cvt_pk_bf16_f32 v73, v74, v75\n" \
"v_mov_b32_e32 v62, 0\n" \
"v_accvgpr_write_b32 a208, 0\n" \
"v_accvgpr_write_b32 a209, 0\n" \
"v_accvgpr_write_b32 a210, 0\n" \
"v_accvgpr_write_b32 a211, 0\n" \
"v_accvgpr_write_b32 a200, v22\n" \
"v_accvgpr_write_b32 a201, v22\n" \
"v_accvgpr_write_b32 a202, v22\n" \
"v_accvgpr_write_b32 a203, v22\n" \
"v_accvgpr_write_b32 a204, v23\n" \
"v_accvgpr_write_b32 a205, v23\n" \
"v_accvgpr_write_b32 a206, v23\n" \
"v_accvgpr_write_b32 a207, v23\n" \
"v_accvgpr_write_b32 a212, v24\n" \
"v_accvgpr_write_b32 a213, v24\n" \
"v_accvgpr_write_b32 a214, v24\n" \
"v_accvgpr_write_b32 a215, v24\n" \
"v_or_b32_e32 v21, 0x44444444, v21\n" \
"s_mov_b32 s64, 0x8000\n" \
"v_add_u32_e32 v50, 0x6000, v55\n" \
"v_add_u32_e32 v51, 0xa000, v55\n" \
"v_add_u32_e32 v53, 0x10000, v55\n" \
"v_add_u32_e32 v48, 0xc000, v20\n" \
"v_add_u32_e32 v49, s17, v20\n" \
"v_cndmask_b32_e32 v29, 0, v26, vcc\n" \
"v_accvgpr_write_b32 a220, v96\n" \
"v_accvgpr_write_b32 a221, v97\n" \
"v_accvgpr_write_b32 a222, v98\n" \
"v_accvgpr_write_b32 a223, v99\n" \
"v_accvgpr_write_b32 a224, v100\n" \
"v_accvgpr_write_b32 a225, v101\n" \
"v_accvgpr_write_b32 a226, v102\n" \
"v_accvgpr_write_b32 a227, v103\n" \
"v_accvgpr_write_b32 a228, v104\n" \
"v_accvgpr_write_b32 a229, v105\n" \
"v_accvgpr_write_b32 a230, v106\n" \
"v_accvgpr_write_b32 a231, v107\n" \
"v_accvgpr_write_b32 a232, v108\n" \
"v_accvgpr_write_b32 a233, v109\n" \
"v_accvgpr_write_b32 a234, v110\n" \
"v_accvgpr_write_b32 a235, v111\n" \
"v_cvt_pk_bf16_f32 v69, v66, v67\n" \
"v_cvt_pk_bf16_f32 v68, v64, v65\n" \
"v_cvt_pk_bf16_f32 v74, v76, v77\n" \
"v_cvt_pk_bf16_f32 v75, v78, v79\n" \
"s_sub_u32 s66, s18, 1\n" \
"s_cbranch_scc1 L_tr_%=\n" \
".fill (L_ab + 1856 - .) / 4, 4, 0xBF800000\n" \
"L_1_%=:\n" \
"v_mfma_f32_16x16x32_bf16 v[96:99], a[128:131], a[0:3], v[32:35]\n" \
"s_xor_b32 m0, s65, 0x12000\n" \
"ds_read_b128 a[160:163], v49 offset:0\n" \
"v_mfma_f32_16x16x32_bf16 v[96:99], a[132:135], a[4:7], v[96:99]\n" \
"buffer_load_dwordx4 v50, s[8:11], s64 offen offset:0 sc1 lds\n" \
"v_mfma_f32_16x16x32_bf16 v[96:99], a[136:139], a[8:11], v[96:99]\n" \
"ds_read_b128 a[164:167], v49 offset:1024\n" \
"ds_read_b128 a[168:171], v49 offset:2048\n" \
"v_mfma_f32_16x16x32_bf16 v[96:99], a[140:143], a[12:15], v[96:99]\n" \
"ds_read_b128 a[172:175], v49 offset:3072\n" \
"ds_read_b128 a[176:179], v49 offset:4096\n" \
"v_mfma_f32_16x16x32_bf16 v[100:103], a[156:159], a[12:15], v[32:35]\n" \
"ds_read_b128 a[180:183], v49 offset:5120\n" \
"ds_read_b128 a[184:187], v49 offset:6144\n" \
"v_mfma_f32_16x16x32_bf16 v[100:103], a[152:155], a[8:11], v[100:103]\n" \
"v_exp_f32_e64 v86, v86\n" \
"v_mfma_f32_16x16x32_bf16 v[100:103], a[148:151], a[4:7], v[100:103]\n" \
"v_exp_f32_e64 v87, v87\n" \
"v_mfma_f32_16x16x32_bf16 v[100:103], a[144:147], a[0:3], v[100:103]\n" \
"ds_read_b128 a[188:191], v49 offset:7168\n" \
"v_cvt_pk_bf16_f32 v87, v86, v87\n" \
"v_mfma_f32_16x16x32_bf16 v[108:111], a[144:147], a[16:19], v[36:39]\n" \
"v_exp_f32_e64 v84, v84\n" \
"v_mfma_f32_16x16x32_bf16 v[108:111], a[148:151], a[20:23], v[108:111]\n" \
"v_exp_f32_e64 v85, v85\n" \
"v_mfma_f32_16x16x32_bf16 v[108:111], a[152:155], a[24:27], v[108:111]\n" \
"v_exp_f32_e64 v82, v82\n" \
"v_mfma_f32_16x16x32_bf16 v[108:111], a[156:159], a[28:31], v[108:111]\n" \
"v_exp_f32_e64 v83, v83\n" \
"v_mfma_f32_16x16x32_bf16 v[104:107], a[140:143], a[28:31], v[36:39]\n" \
"v_exp_f32_e64 v80, v80\n" \
"v_mfma_f32_16x16x32_bf16 v[104:107], a[136:139], a[24:27], v[104:107]\n" \
"v_exp_f32_e64 v81, v81\n" \
"v_mfma_f32_16x16x32_bf16 v[104:107], a[132:135], a[20:23], v[104:107]\n" \
"v_exp_f32_e64 v88, v88\n" \
"v_mfma_f32_16x16x32_bf16 v[104:107], a[128:131], a[16:19], v[104:107]\n" \
"buffer_load_dwordx4 v50, s[8:11], s64 offen offset:1024 sc1 lds\n" \
"v_mfma_f32_16x16x32_bf16 v[112:115], a[128:131], a[32:35], v[40:43]\n" \
"s_xor_b32 m0, s65, 0x6000\n" \
"v_cvt_pk_bf16_f32 v86, v84, v85\n" \
"v_mfma_f32_16x16x32_bf16 v[112:115], a[132:135], a[36:39], v[112:115]\n" \
"v_cvt_pk_bf16_f32 v85, v82, v83\n" \
"v_cvt_pk_bf16_f32 v84, v80, v81\n" \
"v_mfma_f32_16x16x32_bf16 v[112:115], a[136:139], a[40:43], v[112:115]\n" \
"v_exp_f32_e64 v89, v89\n" \
"v_mfma_f32_16x16x32_bf16 v[112:115], a[140:143], a[44:47], v[112:115]\n" \
"v_exp_f32_e64 v90, v90\n" \
"v_mfma_f32_16x16x32_bf16 v[116:119], a[156:159], a[44:47], v[40:43]\n" \
"v_exp_f32_e64 v91, v91\n" \
"v_mfma_f32_16x16x32_bf16 v[116:119], a[152:155], a[40:43], v[116:119]\n" \
"v_exp_f32_e64 v92, v92\n" \
"v_mfma_f32_16x16x32_bf16 v[116:119], a[148:151], a[36:39], v[116:119]\n" \
"v_exp_f32_e64 v93, v93\n" \
"v_mfma_f32_16x16x32_bf16 v[116:119], a[144:147], a[32:35], v[116:119]\n" \
"v_exp_f32_e64 v94, v94\n" \
"v_mfma_f32_16x16x32_bf16 v[124:127], a[144:147], a[48:51], v[44:47]\n" \
"v_cvt_pk_bf16_f32 v88, v88, v89\n" \
"v_cvt_pk_bf16_f32 v89, v90, v91\n" \
"v_mfma_f32_16x16x32_bf16 v[124:127], a[148:151], a[52:55], v[124:127]\n" \
"v_exp_f32_e64 v95, v95\n" \
"v_mfma_f32_16x16x32_bf16 v[124:127], a[152:155], a[56:59], v[124:127]\n" \
"v_cvt_pk_bf16_f32 v90, v92, v93\n" \
"v_cvt_pk_bf16_f32 v91, v94, v95\n" \
"v_mfma_f32_16x16x32_bf16 v[124:127], a[156:159], a[60:63], v[124:127]\n" \
"ds_read_b128 a[144:147], v48 offset:4096\n" \
"ds_read_b128 a[148:151], v48 offset:5120\n" \
"v_mfma_f32_16x16x32_bf16 v[120:123], a[140:143], a[60:63], v[44:47]\n" \
"ds_read_b128 a[152:155], v48 offset:6144\n" \
"ds_read_b128 a[140:143], v48 offset:3072\n" \
"v_mfma_f32_16x16x32_bf16 v[120:123], a[136:139], a[56:59], v[120:123]\n" \
"ds_read_b128 a[136:139], v48 offset:2048\n" \
"ds_read_b128 a[156:159], v48 offset:7168\n" \
"v_mfma_f32_16x16x32_bf16 v[120:123], a[132:135], a[52:55], v[120:123]\n" \
"buffer_load_dwordx4 v51, s[8:11], s64 offen offset:0 sc1 lds\n" \
"v_mfma_f32_16x16x32_bf16 v[120:123], a[128:131], a[48:51], v[120:123]\n" \
"ds_read_b128 a[128:131], v48 offset:0\n" \
"s_waitcnt lgkmcnt(7)\n" \
"v_mfma_f32_16x16x32_bf16 v[128:131], a[160:163], v[68:71], v[128:131]\n" \
"v_exp_f32_e32 v102, v102\n" \
"v_mfma_f32_16x16x32_bf16 v[160:163], a[160:163], v[72:75], v[160:163]\n" \
"v_exp_f32_e64 v103, v103\n" \
"v_mfma_f32_16x16x32_bf16 v[132:135], a[164:167], v[68:71], v[132:135]\n" \
"ds_read_b128 a[132:135], v48 offset:1024\n" \
"v_cvt_pk_bf16_f32 v103, v102, v103\n" \
"v_mfma_f32_16x16x32_bf16 v[164:167], a[164:167], v[72:75], v[164:167]\n" \
"v_exp_f32_e64 v100, v100\n" \
"v_mfma_f32_16x16x32_bf16 v[136:139], a[168:171], v[68:71], v[136:139]\n" \
"v_exp_f32_e64 v101, v101\n" \
"v_mfma_f32_16x16x32_bf16 v[168:171], a[168:171], v[72:75], v[168:171]\n" \
"v_exp_f32_e64 v98, v98\n" \
"v_mfma_f32_16x16x32_bf16 v[140:143], a[172:175], v[68:71], v[140:143]\n" \
"v_exp_f32_e64 v99, v99\n" \
"v_mfma_f32_16x16x32_bf16 v[172:175], a[172:175], v[72:75], v[172:175]\n" \
"v_exp_f32_e64 v96, v96\n" \
"v_mfma_f32_16x16x32_bf16 v[144:147], a[176:179], v[68:71], v[144:147]\n" \
"v_exp_f32_e64 v97, v97\n" \
"v_mfma_f32_16x16x32_bf16 v[176:179], a[176:179], v[72:75], v[176:179]\n" \
"v_exp_f32_e64 v104, v104\n" \
"v_mfma_f32_16x16x32_bf16 v[148:151], a[180:183], v[68:71], v[148:151]\n" \
"v_exp_f32_e64 v105, v105\n" \
"v_mfma_f32_16x16x32_bf16 v[180:183], a[180:183], v[72:75], v[180:183]\n" \
"v_cvt_pk_bf16_f32 v102, v100, v101\n" \
"v_cvt_pk_bf16_f32 v101, v98, v99\n" \
"v_mfma_f32_16x16x32_bf16 v[152:155], a[184:187], v[68:71], v[152:155]\n" \
"v_cvt_pk_bf16_f32 v100, v96, v97\n" \
"v_cvt_pk_bf16_f32 v104, v104, v105\n" \
"v_mfma_f32_16x16x32_bf16 v[184:187], a[184:187], v[72:75], v[184:187]\n" \
"buffer_load_dwordx4 v51, s[8:11], s64 offen offset:1024 sc1 lds\n" \
"v_mfma_f32_16x16x32_bf16 v[156:159], a[188:191], v[68:71], v[156:159]\n" \
"v_exp_f32_e64 v106, v106\n" \
"v_mfma_f32_16x16x32_bf16 v[188:191], a[188:191], v[72:75], v[188:191]\n" \
"v_exp_f32_e64 v107, v107\n" \
"v_smfmac_f32_16x16x64_bf16 a[192:195], a[200:203], v[68:75], v21\n" \
"v_exp_f32_e64 v108, v108\n" \
"v_mfma_f32_16x16x32_bf16 v[220:223], a[188:191], v[84:87], v[220:223]\n" \
"v_exp_f32_e64 v109, v109\n" \
"v_mfma_f32_16x16x32_bf16 v[252:255], a[188:191], v[88:91], v[252:255]\n" \
"v_exp_f32_e64 v110, v110\n" \
"v_mfma_f32_16x16x32_bf16 v[216:219], a[184:187], v[84:87], v[216:219]\n" \
"v_exp_f32_e64 v111, v111\n" \
"v_mfma_f32_16x16x32_bf16 v[248:251], a[184:187], v[88:91], v[248:251]\n" \
"v_exp_f32_e64 v118, v118\n" \
"v_mfma_f32_16x16x32_bf16 v[212:215], a[180:183], v[84:87], v[212:215]\n" \
"v_exp_f32_e64 v119, v119\n" \
"v_mfma_f32_16x16x32_bf16 v[244:247], a[180:183], v[88:91], v[244:247]\n" \
"v_cvt_pk_bf16_f32 v105, v106, v107\n" \
"v_cvt_pk_bf16_f32 v106, v108, v109\n" \
"v_mfma_f32_16x16x32_bf16 v[208:211], a[176:179], v[84:87], v[208:211]\n" \
"v_cvt_pk_bf16_f32 v107, v110, v111\n" \
"v_cvt_pk_bf16_f32 v119, v118, v119\n" \
"v_mfma_f32_16x16x32_bf16 v[240:243], a[176:179], v[88:91], v[240:243]\n" \
"v_exp_f32_e64 v116, v116\n" \
"v_mfma_f32_16x16x32_bf16 v[204:207], a[172:175], v[84:87], v[204:207]\n" \
"v_exp_f32_e64 v117, v117\n" \
"v_mfma_f32_16x16x32_bf16 v[236:239], a[172:175], v[88:91], v[236:239]\n" \
"v_exp_f32_e64 v114, v114\n" \
"v_mfma_f32_16x16x32_bf16 v[200:203], a[168:171], v[84:87], v[200:203]\n" \
"v_exp_f32_e64 v115, v115\n" \
"v_mfma_f32_16x16x32_bf16 v[232:235], a[168:171], v[88:91], v[232:235]\n" \
"v_exp_f32_e64 v112, v112\n" \
"v_mfma_f32_16x16x32_bf16 v[196:199], a[164:167], v[84:87], v[196:199]\n" \
"v_exp_f32_e64 v113, v113\n" \
"v_mfma_f32_16x16x32_bf16 v[228:231], a[164:167], v[88:91], v[228:231]\n" \
"v_exp_f32_e64 v120, v120\n" \
"v_mfma_f32_16x16x32_bf16 v[192:195], a[160:163], v[84:87], v[192:195]\n" \
"v_exp_f32_e64 v121, v121\n" \
"v_mfma_f32_16x16x32_bf16 v[224:227], a[160:163], v[88:91], v[224:227]\n" \
"v_cvt_pk_bf16_f32 v118, v116, v117\n" \
"v_cvt_pk_bf16_f32 v117, v114, v115\n" \
"v_smfmac_f32_16x16x64_bf16 a[196:199], a[200:203], v[84:91], v21\n" \
"v_cvt_pk_bf16_f32 v116, v112, v113\n" \
"v_cvt_pk_bf16_f32 v120, v120, v121\n" \
"s_waitcnt vmcnt(4) lgkmcnt(0)\n" \
"s_barrier\n" \
"v_mfma_f32_16x16x32_bf16 v[64:67], a[128:131], a[0:3], v[32:35]\n" \
"s_xor_b32 m0, s65, 0x14000\n" \
"ds_read_b128 a[160:163], v49 offset:8192\n" \
"v_mfma_f32_16x16x32_bf16 v[64:67], a[132:135], a[4:7], v[64:67]\n" \
"buffer_load_dwordx4 v52, s[8:11], s64 offen offset:0 sc1 lds\n" \
"v_mfma_f32_16x16x32_bf16 v[64:67], a[136:139], a[8:11], v[64:67]\n" \
"ds_read_b128 a[164:167], v49 offset:9216\n" \
"ds_read_b128 a[168:171], v49 offset:10240\n" \
"v_mfma_f32_16x16x32_bf16 v[64:67], a[140:143], a[12:15], v[64:67]\n" \
"ds_read_b128 a[172:175], v49 offset:11264\n" \
"ds_read_b128 a[176:179], v49 offset:12288\n" \
"v_mfma_f32_16x16x32_bf16 v[68:71], a[156:159], a[12:15], v[32:35]\n" \
"ds_read_b128 a[180:183], v49 offset:13312\n" \
"ds_read_b128 a[184:187], v49 offset:14336\n" \
"v_mfma_f32_16x16x32_bf16 v[68:71], a[152:155], a[8:11], v[68:71]\n" \
"v_exp_f32_e64 v122, v122\n" \
"v_mfma_f32_16x16x32_bf16 v[68:71], a[148:151], a[4:7], v[68:71]\n" \
"v_exp_f32_e64 v123, v123\n" \
"v_mfma_f32_16x16x32_bf16 v[68:71], a[144:147], a[0:3], v[68:71]\n" \
"ds_read_b128 a[188:191], v49 offset:15360\n" \
"v_cvt_pk_bf16_f32 v121, v122, v123\n" \
"v_mfma_f32_16x16x32_bf16 v[76:79], a[144:147], a[16:19], v[36:39]\n" \
"v_exp_f32_e64 v124, v124\n" \
"v_mfma_f32_16x16x32_bf16 v[76:79], a[148:151], a[20:23], v[76:79]\n" \
"v_exp_f32_e64 v125, v125\n" \
"v_mfma_f32_16x16x32_bf16 v[76:79], a[152:155], a[24:27], v[76:79]\n" \
"v_exp_f32_e64 v126, v126\n" \
"v_mfma_f32_16x16x32_bf16 v[76:79], a[156:159], a[28:31], v[76:79]\n" \
"v_exp_f32_e64 v127, v127\n" \
"v_mfma_f32_16x16x32_bf16 v[72:75], a[140:143], a[28:31], v[36:39]\n" \
"v_cvt_pk_bf16_f32 v122, v124, v125\n" \
"v_cvt_pk_bf16_f32 v123, v126, v127\n" \
"v_mfma_f32_16x16x32_bf16 v[72:75], a[136:139], a[24:27], v[72:75]\n" \
"v_exp_f32_e64 v70, v70\n" \
"v_mfma_f32_16x16x32_bf16 v[72:75], a[132:135], a[20:23], v[72:75]\n" \
"v_exp_f32_e64 v71, v71\n" \
"v_mfma_f32_16x16x32_bf16 v[72:75], a[128:131], a[16:19], v[72:75]\n" \
"buffer_load_dwordx4 v52, s[8:11], s64 offen offset:1024 sc1 lds\n" \
"v_mfma_f32_16x16x32_bf16 v[80:83], a[128:131], a[32:35], v[40:43]\n" \
"s_mov_b32 m0, s65\n" \
"v_cvt_pk_bf16_f32 v71, v70, v71\n" \
"v_mfma_f32_16x16x32_bf16 v[80:83], a[132:135], a[36:39], v[80:83]\n" \
"v_exp_f32_e32 v68, v68\n" \
"v_mfma_f32_16x16x32_bf16 v[80:83], a[136:139], a[40:43], v[80:83]\n" \
"v_exp_f32_e64 v69, v69\n" \
"v_mfma_f32_16x16x32_bf16 v[80:83], a[140:143], a[44:47], v[80:83]\n" \
"v_exp_f32_e64 v72, v72\n" \
"v_mfma_f32_16x16x32_bf16 v[84:87], a[156:159], a[44:47], v[40:43]\n" \
"v_exp_f32_e64 v73, v73\n" \
"v_mfma_f32_16x16x32_bf16 v[84:87], a[152:155], a[40:43], v[84:87]\n" \
"v_exp_f32_e64 v74, v74\n" \
"v_mfma_f32_16x16x32_bf16 v[84:87], a[148:151], a[36:39], v[84:87]\n" \
"v_exp_f32_e64 v75, v75\n" \
"v_mfma_f32_16x16x32_bf16 v[84:87], a[144:147], a[32:35], v[84:87]\n" \
"v_exp_f32_e64 v66, v66\n" \
"v_mfma_f32_16x16x32_bf16 v[92:95], a[144:147], a[48:51], v[44:47]\n" \
"ds_read_b128 a[144:147], v48 offset:12288\n" \
"v_cvt_pk_bf16_f32 v72, v72, v73\n" \
"v_mfma_f32_16x16x32_bf16 v[92:95], a[148:151], a[52:55], v[92:95]\n" \
"ds_read_b128 a[148:151], v48 offset:13312\n" \
"v_cvt_pk_bf16_f32 v70, v68, v69\n" \
"v_mfma_f32_16x16x32_bf16 v[92:95], a[152:155], a[56:59], v[92:95]\n" \
"ds_read_b128 a[152:155], v48 offset:14336\n" \
"v_cvt_pk_bf16_f32 v73, v74, v75\n" \
"v_mfma_f32_16x16x32_bf16 v[92:95], a[156:159], a[60:63], v[92:95]\n" \
"ds_read_b128 a[156:159], v48 offset:15360\n" \
"v_add_u32_e32 v49, 0x10000, v48\n" \
"v_mfma_f32_16x16x32_bf16 v[88:91], a[140:143], a[60:63], v[44:47]\n" \
"ds_read_b128 a[140:143], v48 offset:11264\n" \
"s_xor_b32 s65, s65, 0x4000\n" \
"v_mfma_f32_16x16x32_bf16 v[88:91], a[136:139], a[56:59], v[88:91]\n" \
"v_exp_f32_e64 v67, v67\n" \
"v_mfma_f32_16x16x32_bf16 v[88:91], a[132:135], a[52:55], v[88:91]\n" \
"buffer_load_dwordx4 v53, s[8:11], s64 offen offset:0 sc1 lds\n" \
"v_mfma_f32_16x16x32_bf16 v[88:91], a[128:131], a[48:51], v[88:91]\n" \
"ds_read_b128 a[128:131], v48 offset:8192\n" \
"s_waitcnt lgkmcnt(6)\n" \
"v_mfma_f32_16x16x32_bf16 v[128:131], a[160:163], v[100:103], v[128:131]\n" \
"ds_read_b128 a[132:135], v48 offset:9216\n" \
"ds_read_b128 a[136:139], v48 offset:10240\n" \
"v_mfma_f32_16x16x32_bf16 v[160:163], a[160:163], v[104:107], v[160:163]\n" \
"v_exp_f32_e32 v64, v64\n" \
"v_mfma_f32_16x16x32_bf16 v[132:135], a[164:167], v[100:103], v[132:135]\n" \
"v_exp_f32_e64 v65, v65\n" \
"v_mfma_f32_16x16x32_bf16 v[164:167], a[164:167], v[104:107], v[164:167]\n" \
"v_exp_f32_e64 v76, v76\n" \
"v_mfma_f32_16x16x32_bf16 v[136:139], a[168:171], v[100:103], v[136:139]\n" \
"v_exp_f32_e64 v77, v77\n" \
"v_mfma_f32_16x16x32_bf16 v[168:171], a[168:171], v[104:107], v[168:171]\n" \
"v_exp_f32_e64 v78, v78\n" \
"v_mfma_f32_16x16x32_bf16 v[140:143], a[172:175], v[100:103], v[140:143]\n" \
"v_exp_f32_e64 v79, v79\n" \
"v_mfma_f32_16x16x32_bf16 v[172:175], a[172:175], v[104:107], v[172:175]\n" \
"v_cvt_pk_bf16_f32 v69, v66, v67\n" \
"v_cvt_pk_bf16_f32 v68, v64, v65\n" \
"v_mfma_f32_16x16x32_bf16 v[144:147], a[176:179], v[100:103], v[144:147]\n" \
"v_cvt_pk_bf16_f32 v74, v76, v77\n" \
"v_cvt_pk_bf16_f32 v75, v78, v79\n" \
"v_mfma_f32_16x16x32_bf16 v[176:179], a[176:179], v[104:107], v[176:179]\n" \
"v_xor_b32_e32 v48, 0x4000, v48\n" \
"v_mfma_f32_16x16x32_bf16 v[148:151], a[180:183], v[100:103], v[148:151]\n" \
"v_mfma_f32_16x16x32_bf16 v[180:183], a[180:183], v[104:107], v[180:183]\n" \
"v_mfma_f32_16x16x32_bf16 v[152:155], a[184:187], v[100:103], v[152:155]\n" \
"v_mfma_f32_16x16x32_bf16 v[184:187], a[184:187], v[104:107], v[184:187]\n" \
"buffer_load_dwordx4 v53, s[8:11], s64 offen offset:1024 sc1 lds\n" \
"v_mfma_f32_16x16x32_bf16 v[156:159], a[188:191], v[100:103], v[156:159]\n" \
"s_add_u32 s64, s64, 0x8000\n" \
"s_sub_u32 s66, s66, 1\n" \
"s_nop 0\n" \
"v_mfma_f32_16x16x32_bf16 v[188:191], a[188:191], v[104:107], v[188:191]\n" \
"v_smfmac_f32_16x16x64_bf16 a[192:195], a[200:203], v[100:107], v21\n" \
"v_mfma_f32_16x16x32_bf16 v[220:223], a[188:191], v[116:119], v[220:223]\n" \
"v_mfma_f32_16x16x32_bf16 v[252:255], a[188:191], v[120:123], v[252:255]\n" \
"v_mfma_f32_16x16x32_bf16 v[216:219], a[184:187], v[116:119], v[216:219]\n" \
"v_mfma_f32_16x16x32_bf16 v[248:251], a[184:187], v[120:123], v[248:251]\n" \
"v_mfma_f32_16x16x32_bf16 v[212:215], a[180:183], v[116:119], v[212:215]\n" \
"v_mfma_f32_16x16x32_bf16 v[244:247], a[180:183], v[120:123], v[244:247]\n" \
"v_mfma_f32_16x16x32_bf16 v[208:211], a[176:179], v[116:119], v[208:211]\n" \
"v_mfma_f32_16x16x32_bf16 v[240:243], a[176:179], v[120:123], v[240:243]\n" \
"v_mfma_f32_16x16x32_bf16 v[204:207], a[172:175], v[116:119], v[204:207]\n" \
"v_mfma_f32_16x16x32_bf16 v[236:239], a[172:175], v[120:123], v[236:239]\n" \
"v_mfma_f32_16x16x32_bf16 v[200:203], a[168:171], v[116:119], v[200:203]\n" \
"v_mfma_f32_16x16x32_bf16 v[232:235], a[168:171], v[120:123], v[232:235]\n" \
"v_mfma_f32_16x16x32_bf16 v[196:199], a[164:167], v[116:119], v[196:199]\n" \
"v_mfma_f32_16x16x32_bf16 v[228:231], a[164:167], v[120:123], v[228:231]\n" \
"v_mfma_f32_16x16x32_bf16 v[192:195], a[160:163], v[116:119], v[192:195]\n" \
"v_mfma_f32_16x16x32_bf16 v[224:227], a[160:163], v[120:123], v[224:227]\n" \
"v_smfmac_f32_16x16x64_bf16 a[196:199], a[200:203], v[116:123], v21\n" \
"s_waitcnt vmcnt(4) lgkmcnt(0)\n" \
"s_barrier\n" \
"s_cbranch_scc0 L_1_%=\n" \
"L_tr_%=:\n" \
"s_cmp_lg_u32 s16, 0\n" \
"s_cbranch_scc1 L_sw_%=\n" \
".fill (L_ab + 4096 - .) / 4, 4, 0xBF800000\n" \
"L_ap1:\n" \
"s_cbranch_execz L_ap0\n" \
"v_mfma_f32_16x16x32_bf16 v[96:99], a[128:131], a[0:3], v[32:35]\n" \
"v_mov_b32_e32 v56, s62\n" \
"v_readlane_b32 s68, v11, 0\n" \
"v_mfma_f32_16x16x32_bf16 v[96:99], a[132:135], a[4:7], v[96:99]\n" \
"s_xor_b32 m0, s65, 0x12000\n" \
"s_mov_b32 s65, s13\n" \
"v_mfma_f32_16x16x32_bf16 v[96:99], a[136:139], a[8:11], v[96:99]\n" \
"buffer_load_dwordx4 v50, s[8:11], s64 offen offset:0 sc1 lds\n" \
"v_mfma_f32_16x16x32_bf16 v[96:99], a[140:143], a[12:15], v[96:99]\n" \
"v_add_u32_e32 v63, s15, v20\n" \
"s_add_u32 s67, s14, 40960\n" \
"v_mfma_f32_16x16x32_bf16 v[100:103], a[156:159], a[12:15], v[32:35]\n" \
"buffer_load_dwordx4 v50, s[8:11], s64 offen offset:1024 sc1 lds\n" \
"s_mov_b64 exec, 1\n" \
"buffer_store_dword v56, off, s[56:59], s68 offset:0 sc1\n" \
"s_mov_b64 exec, -1\n" \
"v_mfma_f32_16x16x32_bf16 v[100:103], a[152:155], a[8:11], v[100:103]\n" \
"v_add_u32_e32 v50, 0x6000, v63\n" \
"s_xor_b32 m0, s65, 0x8000\n" \
"v_mfma_f32_16x16x32_bf16 v[100:103], a[148:151], a[4:7], v[100:103]\n" \
"ds_read_b128 a[160:163], v49 offset:0\n" \
"ds_read_b128 a[164:167], v49 offset:1024\n" \
"v_mfma_f32_16x16x32_bf16 v[100:103], a[144:147], a[0:3], v[100:103]\n" \
"ds_read_b128 a[168:171], v49 offset:2048\n" \
"ds_read_b128 a[172:175], v49 offset:3072\n" \
"v_mfma_f32_16x16x32_bf16 v[108:111], a[144:147], a[16:19], v[36:39]\n" \
"buffer_load_dwordx4 v50, s[8:11], s67 offen offset:0 sc1 lds\n" \
"v_mfma_f32_16x16x32_bf16 v[108:111], a[148:151], a[20:23], v[108:111]\n" \
"ds_read_b128 a[176:179], v49 offset:4096\n" \
"ds_read_b128 a[180:183], v49 offset:5120\n" \
"v_mfma_f32_16x16x32_bf16 v[108:111], a[152:155], a[24:27], v[108:111]\n" \
"ds_read_b128 a[184:187], v49 offset:6144\n" \
"ds_read_b128 a[188:191], v49 offset:7168\n" \
"v_mfma_f32_16x16x32_bf16 v[108:111], a[156:159], a[28:31], v[108:111]\n" \
"s_mov_b32 s64, s14\n" \
"v_add_u32_e32 v51, 0x12000, v63\n" \
"v_mfma_f32_16x16x32_bf16 v[104:107], a[140:143], a[28:31], v[36:39]\n" \
"buffer_load_dwordx4 v50, s[8:11], s67 offen offset:1024 sc1 lds\n" \
"v_mfma_f32_16x16x32_bf16 v[104:107], a[136:139], a[24:27], v[104:107]\n" \
"v_exp_f32_e32 v86, v86\n" \
"v_mfma_f32_16x16x32_bf16 v[104:107], a[132:135], a[20:23], v[104:107]\n" \
"v_exp_f32_e32 v87, v87\n" \
"v_mfma_f32_16x16x32_bf16 v[104:107], a[128:131], a[16:19], v[104:107]\n" \
"v_exp_f32_e32 v84, v84\n" \
"v_mfma_f32_16x16x32_bf16 v[112:115], a[128:131], a[32:35], v[40:43]\n" \
"v_exp_f32_e32 v85, v85\n" \
"v_mfma_f32_16x16x32_bf16 v[112:115], a[132:135], a[36:39], v[112:115]\n" \
"buffer_load_dwordx4 v50, s[8:11], s67 offen offset:2048 sc1 lds\n" \
"v_mfma_f32_16x16x32_bf16 v[112:115], a[136:139], a[40:43], v[112:115]\n" \
"v_cvt_pk_bf16_f32 v87, v86, v87\n" \
"v_cvt_pk_bf16_f32 v86, v84, v85\n" \
"v_mfma_f32_16x16x32_bf16 v[112:115], a[140:143], a[44:47], v[112:115]\n" \
"v_exp_f32_e32 v82, v82\n" \
"v_mfma_f32_16x16x32_bf16 v[116:119], a[156:159], a[44:47], v[40:43]\n" \
"v_exp_f32_e32 v83, v83\n" \
"v_mfma_f32_16x16x32_bf16 v[116:119], a[152:155], a[40:43], v[116:119]\n" \
"buffer_load_dwordx4 v50, s[8:11], s67 offen offset:3072 sc1 lds\n" \
"v_mfma_f32_16x16x32_bf16 v[116:119], a[148:151], a[36:39], v[116:119]\n" \
"s_xor_b32 m0, s65, 0xa000\n" \
"v_cvt_pk_bf16_f32 v85, v82, v83\n" \
"v_mfma_f32_16x16x32_bf16 v[116:119], a[144:147], a[32:35], v[116:119]\n" \
"v_exp_f32_e32 v80, v80\n" \
"v_mfma_f32_16x16x32_bf16 v[124:127], a[144:147], a[48:51], v[44:47]\n" \
"v_exp_f32_e32 v81, v81\n" \
"v_mfma_f32_16x16x32_bf16 v[124:127], a[148:151], a[52:55], v[124:127]\n" \
"buffer_load_dwordx4 v51, s[8:11], s64 offen offset:0 sc1 lds\n" \
"v_mfma_f32_16x16x32_bf16 v[124:127], a[152:155], a[56:59], v[124:127]\n" \
"v_cvt_pk_bf16_f32 v84, v80, v81\n" \
"v_readlane_b32 s93, v11, 19\n" \
"v_mfma_f32_16x16x32_bf16 v[124:127], a[156:159], a[60:63], v[124:127]\n" \
"v_exp_f32_e32 v88, v88\n" \
"v_mfma_f32_16x16x32_bf16 v[120:123], a[140:143], a[60:63], v[44:47]\n" \
"v_exp_f32_e32 v89, v89\n" \
"v_mfma_f32_16x16x32_bf16 v[120:123], a[136:139], a[56:59], v[120:123]\n" \
"v_exp_f32_e32 v90, v90\n" \
"v_mfma_f32_16x16x32_bf16 v[120:123], a[132:135], a[52:55], v[120:123]\n" \
"buffer_load_dwordx4 v51, s[8:11], s64 offen offset:1024 sc1 lds\n" \
"v_mfma_f32_16x16x32_bf16 v[120:123], a[128:131], a[48:51], v[120:123]\n" \
"v_cvt_pk_bf16_f32 v88, v88, v89\n" \
"s_waitcnt lgkmcnt(0)\n" \
"v_mfma_f32_16x16x32_bf16 v[128:131], a[160:163], v[68:71], v[128:131]\n" \
"v_exp_f32_e32 v91, v91\n" \
"v_mfma_f32_16x16x32_bf16 v[160:163], a[160:163], v[72:75], v[160:163]\n" \
"v_exp_f32_e32 v92, v92\n" \
"v_mfma_f32_16x16x32_bf16 v[132:135], a[164:167], v[68:71], v[132:135]\n" \
"buffer_load_dwordx4 v51, s[8:11], s64 offen offset:2048 sc1 lds\n" \
"v_mfma_f32_16x16x32_bf16 v[164:167], a[164:167], v[72:75], v[164:167]\n" \
"v_exp_f32_e32 v93, v93\n" \
"v_mfma_f32_16x16x32_bf16 v[136:139], a[168:171], v[68:71], v[136:139]\n" \
"v_exp_f32_e32 v94, v94\n" \
"v_mfma_f32_16x16x32_bf16 v[168:171], a[168:171], v[72:75], v[168:171]\n" \
"v_exp_f32_e32 v95, v95\n" \
"v_mfma_f32_16x16x32_bf16 v[140:143], a[172:175], v[68:71], v[140:143]\n" \
"buffer_load_dwordx4 v51, s[8:11], s64 offen offset:3072 sc1 lds\n" \
"v_mfma_f32_16x16x32_bf16 v[172:175], a[172:175], v[72:75], v[172:175]\n" \
"v_cvt_pk_bf16_f32 v89, v90, v91\n" \
"v_cvt_pk_bf16_f32 v90, v92, v93\n" \
"v_mfma_f32_16x16x32_bf16 v[144:147], a[176:179], v[68:71], v[144:147]\n" \
"v_cvt_pk_bf16_f32 v91, v94, v95\n" \
"v_readlane_b32 s80, v11, 11\n" \
"v_mfma_f32_16x16x32_bf16 v[176:179], a[176:179], v[72:75], v[176:179]\n" \
"v_readlane_b32 s81, v11, 12\n" \
"s_mov_b32 s82, 0x47fca0\n" \
"v_mfma_f32_16x16x32_bf16 v[148:151], a[180:183], v[68:71], v[148:151]\n" \
"s_mov_b32 s83, 0x20000\n" \
"v_readlane_b32 s92, v11, 25\n" \
"v_mfma_f32_16x16x32_bf16 v[180:183], a[180:183], v[72:75], v[180:183]\n" \
"s_add_u32 m0, s93, 0x0\n" \
"ds_read_b128 a[128:131], v48 offset:0\n" \
"v_mfma_f32_16x16x32_bf16 v[152:155], a[184:187], v[68:71], v[152:155]\n" \
"ds_read_b128 a[144:147], v48 offset:4096\n" \
"ds_read_b128 a[132:135], v48 offset:1024\n" \
"v_mfma_f32_16x16x32_bf16 v[184:187], a[184:187], v[72:75], v[184:187]\n" \
"buffer_load_dwordx4 v0, s[80:83], s92 offen offset:0 lds\n" \
"v_mfma_f32_16x16x32_bf16 v[156:159], a[188:191], v[68:71], v[156:159]\n" \
"ds_read_b128 a[148:151], v48 offset:5120\n" \
"ds_read_b128 a[136:139], v48 offset:2048\n" \
"v_mfma_f32_16x16x32_bf16 v[188:191], a[188:191], v[72:75], v[188:191]\n" \
"ds_read_b128 a[152:155], v48 offset:6144\n" \
"ds_read_b128 a[140:143], v48 offset:3072\n" \
"v_smfmac_f32_16x16x64_bf16 a[192:195], a[200:203], v[68:75], v21\n" \
"ds_read_b128 a[156:159], v48 offset:7168\n" \
"v_add_u32_e32 v52, 0x14000, v63\n" \
"v_mfma_f32_16x16x32_bf16 v[220:223], a[188:191], v[84:87], v[220:223]\n" \
"buffer_load_dwordx4 v1, s[80:83], s92 offen offset:1024 lds\n" \
"v_mfma_f32_16x16x32_bf16 v[252:255], a[188:191], v[88:91], v[252:255]\n" \
"v_add_u32_e32 v53, 0x20000, v63\n" \
"s_mov_b32 s66, s19\n" \
"v_mfma_f32_16x16x32_bf16 v[216:219], a[184:187], v[84:87], v[216:219]\n" \
"buffer_load_dwordx4 v2, s[80:83], s92 offen offset:2048 lds\n" \
"v_mfma_f32_16x16x32_bf16 v[248:251], a[184:187], v[88:91], v[248:251]\n" \
"v_exp_f32_e32 v102, v102\n" \
"v_mfma_f32_16x16x32_bf16 v[212:215], a[180:183], v[84:87], v[212:215]\n" \
"v_exp_f32_e32 v103, v103\n" \
"v_mfma_f32_16x16x32_bf16 v[244:247], a[180:183], v[88:91], v[244:247]\n" \
"v_exp_f32_e32 v100, v100\n" \
"v_mfma_f32_16x16x32_bf16 v[208:211], a[176:179], v[84:87], v[208:211]\n" \
"buffer_load_dwordx4 v3, s[80:83], s92 offen offset:3072 lds\n" \
"v_mfma_f32_16x16x32_bf16 v[240:243], a[176:179], v[88:91], v[240:243]\n" \
"v_exp_f32_e32 v101, v101\n" \
"v_mfma_f32_16x16x32_bf16 v[204:207], a[172:175], v[84:87], v[204:207]\n" \
"v_exp_f32_e32 v98, v98\n" \
"v_mfma_f32_16x16x32_bf16 v[236:239], a[172:175], v[88:91], v[236:239]\n" \
"v_exp_f32_e32 v99, v99\n" \
"v_mfma_f32_16x16x32_bf16 v[200:203], a[168:171], v[84:87], v[200:203]\n" \
"v_exp_f32_e32 v96, v96\n" \
"v_mfma_f32_16x16x32_bf16 v[232:235], a[168:171], v[88:91], v[232:235]\n" \
"v_exp_f32_e32 v97, v97\n" \
"v_mfma_f32_16x16x32_bf16 v[196:199], a[164:167], v[84:87], v[196:199]\n" \
"v_exp_f32_e32 v104, v104\n" \
"v_mfma_f32_16x16x32_bf16 v[228:231], a[164:167], v[88:91], v[228:231]\n" \
"v_exp_f32_e32 v105, v105\n" \
"v_mfma_f32_16x16x32_bf16 v[192:195], a[160:163], v[84:87], v[192:195]\n" \
"v_cvt_pk_bf16_f32 v103, v102, v103\n" \
"v_cvt_pk_bf16_f32 v102, v100, v101\n" \
"v_mfma_f32_16x16x32_bf16 v[224:227], a[160:163], v[88:91], v[224:227]\n" \
"v_cvt_pk_bf16_f32 v101, v98, v99\n" \
"v_cvt_pk_bf16_f32 v100, v96, v97\n" \
"v_smfmac_f32_16x16x64_bf16 a[196:199], a[200:203], v[84:91], v21\n" \
"v_exp_f32_e32 v106, v106\n" \
"s_waitcnt vmcnt(15) lgkmcnt(0)\n" \
"s_barrier\n" \
"v_mfma_f32_16x16x32_bf16 v[64:67], a[128:131], a[0:3], v[32:35]\n" \
"s_xor_b32 m0, s65, 0x18000\n" \
"ds_read_b128 a[160:163], v49 offset:8192\n" \
"v_mfma_f32_16x16x32_bf16 v[64:67], a[132:135], a[4:7], v[64:67]\n" \
"buffer_load_dwordx4 v52, s[8:11], s64 offen offset:0 sc1 lds\n" \
"v_mfma_f32_16x16x32_bf16 v[64:67], a[136:139], a[8:11], v[64:67]\n" \
"ds_read_b128 a[164:167], v49 offset:9216\n" \
"ds_read_b128 a[168:171], v49 offset:10240\n" \
"v_mfma_f32_16x16x32_bf16 v[64:67], a[140:143], a[12:15], v[64:67]\n" \
"ds_read_b128 a[172:175], v49 offset:11264\n" \
"ds_read_b128 a[176:179], v49 offset:12288\n" \
"v_mfma_f32_16x16x32_bf16 v[68:71], a[156:159], a[12:15], v[32:35]\n" \
"buffer_load_dwordx4 v52, s[8:11], s64 offen offset:1024 sc1 lds\n" \
"v_mfma_f32_16x16x32_bf16 v[68:71], a[152:155], a[8:11], v[68:71]\n" \
"ds_read_b128 a[180:183], v49 offset:13312\n" \
"ds_read_b128 a[184:187], v49 offset:14336\n" \
"v_mfma_f32_16x16x32_bf16 v[68:71], a[148:151], a[4:7], v[68:71]\n" \
"v_exp_f32_e32 v107, v107\n" \
"v_mfma_f32_16x16x32_bf16 v[68:71], a[144:147], a[0:3], v[68:71]\n" \
"v_cvt_pk_bf16_f32 v104, v104, v105\n" \
"v_cvt_pk_bf16_f32 v105, v106, v107\n" \
"v_mfma_f32_16x16x32_bf16 v[76:79], a[144:147], a[16:19], v[36:39]\n" \
"v_exp_f32_e32 v108, v108\n" \
"v_mfma_f32_16x16x32_bf16 v[76:79], a[148:151], a[20:23], v[76:79]\n" \
"buffer_load_dwordx4 v52, s[8:11], s64 offen offset:2048 sc1 lds\n" \
"v_mfma_f32_16x16x32_bf16 v[76:79], a[152:155], a[24:27], v[76:79]\n" \
"v_exp_f32_e32 v109, v109\n" \
"v_mfma_f32_16x16x32_bf16 v[76:79], a[156:159], a[28:31], v[76:79]\n" \
"v_exp_f32_e32 v110, v110\n" \
"v_mfma_f32_16x16x32_bf16 v[72:75], a[140:143], a[28:31], v[36:39]\n" \
"v_exp_f32_e32 v111, v111\n" \
"v_mfma_f32_16x16x32_bf16 v[72:75], a[136:139], a[24:27], v[72:75]\n" \
"buffer_load_dwordx4 v52, s[8:11], s64 offen offset:3072 sc1 lds\n" \
"v_mfma_f32_16x16x32_bf16 v[72:75], a[132:135], a[20:23], v[72:75]\n" \
"s_mov_b32 m0, s65\n" \
"v_cvt_pk_bf16_f32 v106, v108, v109\n" \
"v_mfma_f32_16x16x32_bf16 v[72:75], a[128:131], a[16:19], v[72:75]\n" \
"v_cvt_pk_bf16_f32 v107, v110, v111\n" \
"ds_read_b128 a[188:191], v49 offset:15360\n" \
"v_mfma_f32_16x16x32_bf16 v[80:83], a[128:131], a[32:35], v[40:43]\n" \
"v_exp_f32_e32 v118, v118\n" \
"v_mfma_f32_16x16x32_bf16 v[80:83], a[132:135], a[36:39], v[80:83]\n" \
"v_exp_f32_e32 v119, v119\n" \
"v_mfma_f32_16x16x32_bf16 v[80:83], a[136:139], a[40:43], v[80:83]\n" \
"buffer_load_dwordx4 v53, s[8:11], s64 offen offset:0 sc1 lds\n" \
"v_mfma_f32_16x16x32_bf16 v[80:83], a[140:143], a[44:47], v[80:83]\n" \
"v_exp_f32_e32 v116, v116\n" \
"v_mfma_f32_16x16x32_bf16 v[84:87], a[156:159], a[44:47], v[40:43]\n" \
"v_exp_f32_e32 v117, v117\n" \
"v_mfma_f32_16x16x32_bf16 v[84:87], a[152:155], a[40:43], v[84:87]\n" \
"v_cvt_pk_bf16_f32 v119, v118, v119\n" \
"v_cvt_pk_bf16_f32 v118, v116, v117\n" \
"v_mfma_f32_16x16x32_bf16 v[84:87], a[148:151], a[36:39], v[84:87]\n" \
"v_exp_f32_e32 v114, v114\n" \
"v_mfma_f32_16x16x32_bf16 v[84:87], a[144:147], a[32:35], v[84:87]\n" \
"buffer_load_dwordx4 v53, s[8:11], s64 offen offset:1024 sc1 lds\n" \
"v_mfma_f32_16x16x32_bf16 v[92:95], a[144:147], a[48:51], v[44:47]\n" \
"v_exp_f32_e32 v115, v115\n" \
"v_mfma_f32_16x16x32_bf16 v[92:95], a[148:151], a[52:55], v[92:95]\n" \
"v_exp_f32_e32 v112, v112\n" \
"v_mfma_f32_16x16x32_bf16 v[92:95], a[152:155], a[56:59], v[92:95]\n" \
"v_exp_f32_e32 v113, v113\n" \
"v_mfma_f32_16x16x32_bf16 v[92:95], a[156:159], a[60:63], v[92:95]\n" \
"buffer_load_dwordx4 v53, s[8:11], s64 offen offset:2048 sc1 lds\n" \
"v_mfma_f32_16x16x32_bf16 v[88:91], a[140:143], a[60:63], v[44:47]\n" \
"v_cvt_pk_bf16_f32 v117, v114, v115\n" \
"v_cvt_pk_bf16_f32 v116, v112, v113\n" \
"v_mfma_f32_16x16x32_bf16 v[88:91], a[136:139], a[56:59], v[88:91]\n" \
"v_exp_f32_e32 v120, v120\n" \
"v_mfma_f32_16x16x32_bf16 v[88:91], a[132:135], a[52:55], v[88:91]\n" \
"v_exp_f32_e32 v121, v121\n" \
"v_mfma_f32_16x16x32_bf16 v[88:91], a[128:131], a[48:51], v[88:91]\n" \
"v_cvt_pk_bf16_f32 v120, v120, v121\n" \
"s_waitcnt lgkmcnt(0)\n" \
"v_mfma_f32_16x16x32_bf16 v[128:131], a[160:163], v[100:103], v[128:131]\n" \
"buffer_load_dwordx4 v53, s[8:11], s64 offen offset:3072 sc1 lds\n" \
"v_mfma_f32_16x16x32_bf16 v[160:163], a[160:163], v[104:107], v[160:163]\n" \
"s_add_u32 m0, s93, 0x1000\n" \
"ds_read_b128 a[128:131], v48 offset:8192\n" \
"v_mfma_f32_16x16x32_bf16 v[132:135], a[164:167], v[100:103], v[132:135]\n" \
"v_exp_f32_e32 v122, v122\n" \
"v_mfma_f32_16x16x32_bf16 v[164:167], a[164:167], v[104:107], v[164:167]\n" \
"v_exp_f32_e32 v123, v123\n" \
"v_mfma_f32_16x16x32_bf16 v[136:139], a[168:171], v[100:103], v[136:139]\n" \
"buffer_load_dwordx4 v4, s[80:83], s92 offen offset:0 lds\n" \
"v_mfma_f32_16x16x32_bf16 v[168:171], a[168:171], v[104:107], v[168:171]\n" \
"v_exp_f32_e32 v124, v124\n" \
"v_mfma_f32_16x16x32_bf16 v[140:143], a[172:175], v[100:103], v[140:143]\n" \
"v_exp_f32_e32 v125, v125\n" \
"v_mfma_f32_16x16x32_bf16 v[172:175], a[172:175], v[104:107], v[172:175]\n" \
"v_exp_f32_e32 v126, v126\n" \
"v_mfma_f32_16x16x32_bf16 v[144:147], a[176:179], v[100:103], v[144:147]\n" \
"v_exp_f32_e32 v127, v127\n" \
"v_mfma_f32_16x16x32_bf16 v[176:179], a[176:179], v[104:107], v[176:179]\n" \
"v_cvt_pk_bf16_f32 v121, v122, v123\n" \
"v_cvt_pk_bf16_f32 v122, v124, v125\n" \
"v_mfma_f32_16x16x32_bf16 v[148:151], a[180:183], v[100:103], v[148:151]\n" \
"buffer_load_dwordx4 v5, s[80:83], s92 offen offset:1024 lds\n" \
"v_mfma_f32_16x16x32_bf16 v[180:183], a[180:183], v[104:107], v[180:183]\n" \
"v_cvt_pk_bf16_f32 v123, v126, v127\n" \
"ds_read_b128 a[144:147], v48 offset:12288\n" \
"v_mfma_f32_16x16x32_bf16 v[152:155], a[184:187], v[100:103], v[152:155]\n" \
"ds_read_b128 a[132:135], v48 offset:9216\n" \
"ds_read_b128 a[148:151], v48 offset:13312\n" \
"v_mfma_f32_16x16x32_bf16 v[184:187], a[184:187], v[104:107], v[184:187]\n" \
"ds_read_b128 a[136:139], v48 offset:10240\n" \
"ds_read_b128 a[152:155], v48 offset:14336\n" \
"v_mfma_f32_16x16x32_bf16 v[156:159], a[188:191], v[100:103], v[156:159]\n" \
"ds_read_b128 a[140:143], v48 offset:11264\n" \
"ds_read_b128 a[156:159], v48 offset:15360\n" \
"v_mfma_f32_16x16x32_bf16 v[188:191], a[188:191], v[104:107], v[188:191]\n" \
"buffer_load_dwordx4 v6, s[80:83], s92 offen offset:2048 lds\n" \
"v_smfmac_f32_16x16x64_bf16 a[192:195], a[200:203], v[100:107], v21\n" \
"v_exp_f32_e32 v70, v70\n" \
"v_mfma_f32_16x16x32_bf16 v[220:223], a[188:191], v[116:119], v[220:223]\n" \
"v_exp_f32_e32 v71, v71\n" \
"v_mfma_f32_16x16x32_bf16 v[252:255], a[188:191], v[120:123], v[252:255]\n" \
"buffer_load_dwordx4 v7, s[80:83], s92 offen offset:3072 lds\n" \
"v_mfma_f32_16x16x32_bf16 v[216:219], a[184:187], v[116:119], v[216:219]\n" \
"s_add_u32 m0, s93, 0x2000\n" \
"v_cvt_pk_bf16_f32 v71, v70, v71\n" \
"v_mfma_f32_16x16x32_bf16 v[248:251], a[184:187], v[120:123], v[248:251]\n" \
"v_exp_f32_e32 v72, v72\n" \
"v_mfma_f32_16x16x32_bf16 v[212:215], a[180:183], v[116:119], v[212:215]\n" \
"v_exp_f32_e32 v73, v73\n" \
"v_mfma_f32_16x16x32_bf16 v[244:247], a[180:183], v[120:123], v[244:247]\n" \
"v_exp_f32_e32 v68, v68\n" \
"v_mfma_f32_16x16x32_bf16 v[208:211], a[176:179], v[116:119], v[208:211]\n" \
"buffer_load_dwordx4 v8, s[80:83], s92 offen offset:0 lds\n" \
"v_mfma_f32_16x16x32_bf16 v[240:243], a[176:179], v[120:123], v[240:243]\n" \
"v_exp_f32_e32 v69, v69\n" \
"v_mfma_f32_16x16x32_bf16 v[204:207], a[172:175], v[116:119], v[204:207]\n" \
"v_exp_f32_e32 v74, v74\n" \
"v_mfma_f32_16x16x32_bf16 v[236:239], a[172:175], v[120:123], v[236:239]\n" \
"v_exp_f32_e32 v75, v75\n" \
"v_mfma_f32_16x16x32_bf16 v[200:203], a[168:171], v[116:119], v[200:203]\n" \
"v_exp_f32_e32 v66, v66\n" \
"v_mfma_f32_16x16x32_bf16 v[232:235], a[168:171], v[120:123], v[232:235]\n" \
"v_exp_f32_e32 v67, v67\n" \
"v_mfma_f32_16x16x32_bf16 v[196:199], a[164:167], v[116:119], v[196:199]\n" \
"v_exp_f32_e32 v64, v64\n" \
"v_mfma_f32_16x16x32_bf16 v[228:231], a[164:167], v[120:123], v[228:231]\n" \
"v_exp_f32_e32 v65, v65\n" \
"v_mfma_f32_16x16x32_bf16 v[192:195], a[160:163], v[116:119], v[192:195]\n" \
"v_cvt_pk_bf16_f32 v72, v72, v73\n" \
"v_cvt_pk_bf16_f32 v70, v68, v69\n" \
"v_mfma_f32_16x16x32_bf16 v[224:227], a[160:163], v[120:123], v[224:227]\n" \
"v_exp_f32_e32 v76, v76\n" \
"v_smfmac_f32_16x16x64_bf16 a[196:199], a[200:203], v[116:123], v21\n" \
"v_exp_f32_e32 v77, v77\n" \
"s_waitcnt vmcnt(13) lgkmcnt(0)\n" \
"s_barrier\n" \
"v_exp_f32_e32 v78, v78\n" \
"v_exp_f32_e32 v79, v79\n" \
"v_cvt_pk_bf16_f32 v73, v74, v75\n" \
"v_add_u32_e32 v49, 0x10000, v48\n" \
"v_cvt_pk_bf16_f32 v69, v66, v67\n" \
"v_cvt_pk_bf16_f32 v68, v64, v65\n" \
"v_cvt_pk_bf16_f32 v74, v76, v77\n" \
"v_cvt_pk_bf16_f32 v75, v78, v79\n" \
"v_add_u32_e32 v48, s16, v20\n" \
"s_xor_b32 s65, s65, 0x8000\n" \
"s_add_u32 s64, s64, 0x10000\n" \
"s_getpc_b64 s[86:87]\n" \
"L_hpt_%=:\n" \
"s_add_u32 s86, s86, L_ap5-L_hpt_%=\n" \
"s_addc_u32 s87, s87, 0\n" \
"s_add_u32 s86, s86, s15\n" \
"s_addc_u32 s87, s87, 0\n" \
"s_mov_b64 exec, 0\n" \
"s_swappc_b64 s[88:89], s[86:87]\n" \
"s_mov_b64 exec, -1\n" \
"s_add_u32 s86, s86, 0x2000\n" \
"s_addc_u32 s87, s87, 0\n" \
"s_mov_b64 exec, 0\n" \
"s_swappc_b64 s[88:89], s[86:87]\n" \
"s_mov_b64 exec, -1\n" \
".fill (L_ab + 6464 - .) / 4, 4, 0xBF800000\n" \
"L_2_%=:\n" \
"v_mfma_f32_16x16x32_bf16 v[96:99], a[128:131], a[64:67], v[32:35]\n" \
"s_xor_b32 m0, s65, 0x12000\n" \
"ds_read_b128 a[160:163], v49 offset:0\n" \
"v_mfma_f32_16x16x32_bf16 v[96:99], a[132:135], a[68:71], v[96:99]\n" \
"buffer_load_dwordx4 v50, s[8:11], s64 offen offset:0 sc1 lds\n" \
"v_mfma_f32_16x16x32_bf16 v[96:99], a[136:139], a[72:75], v[96:99]\n" \
"ds_read_b128 a[164:167], v49 offset:1024\n" \
"ds_read_b128 a[168:171], v49 offset:2048\n" \
"v_mfma_f32_16x16x32_bf16 v[96:99], a[140:143], a[76:79], v[96:99]\n" \
"ds_read_b128 a[172:175], v49 offset:3072\n" \
"ds_read_b128 a[176:179], v49 offset:4096\n" \
"v_mfma_f32_16x16x32_bf16 v[100:103], a[156:159], a[76:79], v[32:35]\n" \
"ds_read_b128 a[180:183], v49 offset:5120\n" \
"ds_read_b128 a[184:187], v49 offset:6144\n" \
"v_mfma_f32_16x16x32_bf16 v[100:103], a[152:155], a[72:75], v[100:103]\n" \
"v_exp_f32_e64 v86, v86\n" \
"v_mfma_f32_16x16x32_bf16 v[100:103], a[148:151], a[68:71], v[100:103]\n" \
"v_exp_f32_e64 v87, v87\n" \
"v_mfma_f32_16x16x32_bf16 v[100:103], a[144:147], a[64:67], v[100:103]\n" \
"buffer_load_dwordx4 v50, s[8:11], s64 offen offset:1024 sc1 lds\n" \
"v_mfma_f32_16x16x32_bf16 v[108:111], a[144:147], a[80:83], v[36:39]\n" \
"ds_read_b128 a[188:191], v49 offset:7168\n" \
"v_cvt_pk_bf16_f32 v87, v86, v87\n" \
"v_mfma_f32_16x16x32_bf16 v[108:111], a[148:151], a[84:87], v[108:111]\n" \
"v_exp_f32_e64 v84, v84\n" \
"v_mfma_f32_16x16x32_bf16 v[108:111], a[152:155], a[88:91], v[108:111]\n" \
"v_exp_f32_e64 v85, v85\n" \
"v_mfma_f32_16x16x32_bf16 v[108:111], a[156:159], a[92:95], v[108:111]\n" \
"v_exp_f32_e64 v82, v82\n" \
"v_mfma_f32_16x16x32_bf16 v[104:107], a[140:143], a[92:95], v[36:39]\n" \
"v_exp_f32_e64 v83, v83\n" \
"v_mfma_f32_16x16x32_bf16 v[104:107], a[136:139], a[88:91], v[104:107]\n" \
"v_exp_f32_e64 v80, v80\n" \
"v_mfma_f32_16x16x32_bf16 v[104:107], a[132:135], a[84:87], v[104:107]\n" \
"v_exp_f32_e64 v81, v81\n" \
"v_mfma_f32_16x16x32_bf16 v[104:107], a[128:131], a[80:83], v[104:107]\n" \
"buffer_load_dwordx4 v50, s[8:11], s64 offen offset:2048 sc1 lds\n" \
"v_mfma_f32_16x16x32_bf16 v[112:115], a[128:131], a[96:99], v[40:43]\n" \
"v_exp_f32_e64 v88, v88\n" \
"v_mfma_f32_16x16x32_bf16 v[112:115], a[132:135], a[100:103], v[112:115]\n" \
"v_exp_f32_e64 v89, v89\n" \
"v_mfma_f32_16x16x32_bf16 v[112:115], a[136:139], a[104:107], v[112:115]\n" \
"v_cvt_pk_bf16_f32 v86, v84, v85\n" \
"v_cvt_pk_bf16_f32 v85, v82, v83\n" \
"v_mfma_f32_16x16x32_bf16 v[112:115], a[140:143], a[108:111], v[112:115]\n" \
"v_cvt_pk_bf16_f32 v84, v80, v81\n" \
"v_cvt_pk_bf16_f32 v88, v88, v89\n" \
"v_mfma_f32_16x16x32_bf16 v[116:119], a[156:159], a[108:111], v[40:43]\n" \
"v_exp_f32_e64 v90, v90\n" \
"v_mfma_f32_16x16x32_bf16 v[116:119], a[152:155], a[104:107], v[116:119]\n" \
"v_exp_f32_e64 v91, v91\n" \
"v_mfma_f32_16x16x32_bf16 v[116:119], a[148:151], a[100:103], v[116:119]\n" \
"buffer_load_dwordx4 v50, s[8:11], s64 offen offset:3072 sc1 lds\n" \
"v_mfma_f32_16x16x32_bf16 v[116:119], a[144:147], a[96:99], v[116:119]\n" \
"s_xor_b32 m0, s65, 0xa000\n" \
"v_cvt_pk_bf16_f32 v89, v90, v91\n" \
"v_mfma_f32_16x16x32_bf16 v[124:127], a[144:147], a[112:115], v[44:47]\n" \
"v_exp_f32_e64 v92, v92\n" \
"v_mfma_f32_16x16x32_bf16 v[124:127], a[148:151], a[116:119], v[124:127]\n" \
"v_exp_f32_e64 v93, v93\n" \
"v_mfma_f32_16x16x32_bf16 v[124:127], a[152:155], a[120:123], v[124:127]\n" \
"v_exp_f32_e64 v94, v94\n" \
"v_mfma_f32_16x16x32_bf16 v[124:127], a[156:159], a[124:127], v[124:127]\n" \
"v_exp_f32_e64 v95, v95\n" \
"v_mfma_f32_16x16x32_bf16 v[120:123], a[140:143], a[124:127], v[44:47]\n" \
"v_cvt_pk_bf16_f32 v90, v92, v93\n" \
"v_cvt_pk_bf16_f32 v91, v94, v95\n" \
"v_mfma_f32_16x16x32_bf16 v[120:123], a[136:139], a[120:123], v[120:123]\n" \
"ds_read_b128 a[144:147], v48 offset:4096\n" \
"ds_read_b128 a[148:151], v48 offset:5120\n" \
"v_mfma_f32_16x16x32_bf16 v[120:123], a[132:135], a[116:119], v[120:123]\n" \
"buffer_load_dwordx4 v51, s[8:11], s64 offen offset:0 sc1 lds\n" \
"v_mfma_f32_16x16x32_bf16 v[120:123], a[128:131], a[112:115], v[120:123]\n" \
"ds_read_b128 a[128:131], v48 offset:0\n" \
"s_waitcnt lgkmcnt(3)\n" \
"v_mfma_f32_16x16x32_bf16 v[128:131], a[160:163], v[68:71], v[128:131]\n" \
"ds_read_b128 a[132:135], v48 offset:1024\n" \
"ds_read_b128 a[136:139], v48 offset:2048\n" \
"v_mfma_f32_16x16x32_bf16 v[160:163], a[160:163], v[72:75], v[160:163]\n" \
"ds_read_b128 a[152:155], v48 offset:6144\n" \
"ds_read_b128 a[140:143], v48 offset:3072\n" \
"v_mfma_f32_16x16x32_bf16 v[132:135], a[164:167], v[68:71], v[132:135]\n" \
"v_exp_f32_e32 v102, v102\n" \
"v_mfma_f32_16x16x32_bf16 v[164:167], a[164:167], v[72:75], v[164:167]\n" \
"v_exp_f32_e64 v103, v103\n" \
"v_mfma_f32_16x16x32_bf16 v[136:139], a[168:171], v[68:71], v[136:139]\n" \
"ds_read_b128 a[156:159], v48 offset:7168\n" \
"v_cvt_pk_bf16_f32 v103, v102, v103\n" \
"v_mfma_f32_16x16x32_bf16 v[168:171], a[168:171], v[72:75], v[168:171]\n" \
"buffer_load_dwordx4 v51, s[8:11], s64 offen offset:1024 sc1 lds\n" \
"v_mfma_f32_16x16x32_bf16 v[140:143], a[172:175], v[68:71], v[140:143]\n" \
"v_exp_f32_e64 v100, v100\n" \
"v_mfma_f32_16x16x32_bf16 v[172:175], a[172:175], v[72:75], v[172:175]\n" \
"v_exp_f32_e64 v101, v101\n" \
"v_mfma_f32_16x16x32_bf16 v[144:147], a[176:179], v[68:71], v[144:147]\n" \
"v_exp_f32_e64 v98, v98\n" \
"v_mfma_f32_16x16x32_bf16 v[176:179], a[176:179], v[72:75], v[176:179]\n" \
"v_exp_f32_e64 v99, v99\n" \
"v_mfma_f32_16x16x32_bf16 v[148:151], a[180:183], v[68:71], v[148:151]\n" \
"v_exp_f32_e64 v96, v96\n" \
"v_mfma_f32_16x16x32_bf16 v[180:183], a[180:183], v[72:75], v[180:183]\n" \
"v_exp_f32_e64 v97, v97\n" \
"v_mfma_f32_16x16x32_bf16 v[152:155], a[184:187], v[68:71], v[152:155]\n" \
"v_exp_f32_e64 v104, v104\n" \
"v_mfma_f32_16x16x32_bf16 v[184:187], a[184:187], v[72:75], v[184:187]\n" \
"buffer_load_dwordx4 v51, s[8:11], s64 offen offset:2048 sc1 lds\n" \
"v_mfma_f32_16x16x32_bf16 v[156:159], a[188:191], v[68:71], v[156:159]\n" \
"v_exp_f32_e64 v105, v105\n" \
"v_mfma_f32_16x16x32_bf16 v[188:191], a[188:191], v[72:75], v[188:191]\n" \
"v_cvt_pk_bf16_f32 v102, v100, v101\n" \
"v_cvt_pk_bf16_f32 v101, v98, v99\n" \
"v_smfmac_f32_16x16x64_bf16 a[192:195], a[200:203], v[68:75], v21\n" \
"v_cvt_pk_bf16_f32 v100, v96, v97\n" \
"v_cvt_pk_bf16_f32 v104, v104, v105\n" \
"v_mfma_f32_16x16x32_bf16 v[220:223], a[188:191], v[84:87], v[220:223]\n" \
"v_exp_f32_e64 v106, v106\n" \
"v_mfma_f32_16x16x32_bf16 v[252:255], a[188:191], v[88:91], v[252:255]\n" \
"v_exp_f32_e64 v107, v107\n" \
"v_mfma_f32_16x16x32_bf16 v[216:219], a[184:187], v[84:87], v[216:219]\n" \
"v_exp_f32_e64 v108, v108\n" \
"v_mfma_f32_16x16x32_bf16 v[248:251], a[184:187], v[88:91], v[248:251]\n" \
"buffer_load_dwordx4 v51, s[8:11], s64 offen offset:3072 sc1 lds\n" \
"v_mfma_f32_16x16x32_bf16 v[212:215], a[180:183], v[84:87], v[212:215]\n" \
"v_exp_f32_e64 v109, v109\n" \
"v_mfma_f32_16x16x32_bf16 v[244:247], a[180:183], v[88:91], v[244:247]\n" \
"v_exp_f32_e64 v110, v110\n" \
"v_mfma_f32_16x16x32_bf16 v[208:211], a[176:179], v[84:87], v[208:211]\n" \
"v_exp_f32_e64 v111, v111\n" \
"v_mfma_f32_16x16x32_bf16 v[240:243], a[176:179], v[88:91], v[240:243]\n" \
"v_exp_f32_e64 v118, v118\n" \
"v_mfma_f32_16x16x32_bf16 v[204:207], a[172:175], v[84:87], v[204:207]\n" \
"v_exp_f32_e64 v119, v119\n" \
"v_mfma_f32_16x16x32_bf16 v[236:239], a[172:175], v[88:91], v[236:239]\n" \
"v_cvt_pk_bf16_f32 v105, v106, v107\n" \
"v_cvt_pk_bf16_f32 v106, v108, v109\n" \
"v_mfma_f32_16x16x32_bf16 v[200:203], a[168:171], v[84:87], v[200:203]\n" \
"v_cvt_pk_bf16_f32 v107, v110, v111\n" \
"v_cvt_pk_bf16_f32 v119, v118, v119\n" \
"v_mfma_f32_16x16x32_bf16 v[232:235], a[168:171], v[88:91], v[232:235]\n" \
"v_exp_f32_e64 v116, v116\n" \
"v_mfma_f32_16x16x32_bf16 v[196:199], a[164:167], v[84:87], v[196:199]\n" \
"v_exp_f32_e64 v117, v117\n" \
"v_mfma_f32_16x16x32_bf16 v[228:231], a[164:167], v[88:91], v[228:231]\n" \
"v_exp_f32_e64 v114, v114\n" \
"v_mfma_f32_16x16x32_bf16 v[192:195], a[160:163], v[84:87], v[192:195]\n" \
"v_exp_f32_e64 v115, v115\n" \
"v_mfma_f32_16x16x32_bf16 v[224:227], a[160:163], v[88:91], v[224:227]\n" \
"v_exp_f32_e64 v112, v112\n" \
"v_smfmac_f32_16x16x64_bf16 a[196:199], a[200:203], v[84:91], v21\n" \
"v_exp_f32_e32 v113, v113\n" \
"s_waitcnt vmcnt(8) lgkmcnt(0)\n" \
"s_barrier\n" \
"s_nop 0\n" \
"v_mfma_f32_16x16x32_bf16 v[64:67], a[128:131], a[64:67], v[32:35]\n" \
"s_xor_b32 m0, s65, 0x18000\n" \
"ds_read_b128 a[160:163], v49 offset:8192\n" \
"v_mfma_f32_16x16x32_bf16 v[64:67], a[132:135], a[68:71], v[64:67]\n" \
"buffer_load_dwordx4 v52, s[8:11], s64 offen offset:0 sc1 lds\n" \
"v_mfma_f32_16x16x32_bf16 v[64:67], a[136:139], a[72:75], v[64:67]\n" \
"ds_read_b128 a[164:167], v49 offset:9216\n" \
"ds_read_b128 a[168:171], v49 offset:10240\n" \
"v_mfma_f32_16x16x32_bf16 v[64:67], a[140:143], a[76:79], v[64:67]\n" \
"ds_read_b128 a[172:175], v49 offset:11264\n" \
"ds_read_b128 a[176:179], v49 offset:12288\n" \
"v_mfma_f32_16x16x32_bf16 v[68:71], a[156:159], a[76:79], v[32:35]\n" \
"ds_read_b128 a[180:183], v49 offset:13312\n" \
"ds_read_b128 a[184:187], v49 offset:14336\n" \
"v_mfma_f32_16x16x32_bf16 v[68:71], a[152:155], a[72:75], v[68:71]\n" \
"ds_read_b128 a[188:191], v49 offset:15360\n" \
"v_cvt_pk_bf16_f32 v118, v116, v117\n" \
"v_mfma_f32_16x16x32_bf16 v[68:71], a[148:151], a[68:71], v[68:71]\n" \
"v_cvt_pk_bf16_f32 v117, v114, v115\n" \
"v_cvt_pk_bf16_f32 v116, v112, v113\n" \
"v_mfma_f32_16x16x32_bf16 v[68:71], a[144:147], a[64:67], v[68:71]\n" \
"buffer_load_dwordx4 v52, s[8:11], s64 offen offset:1024 sc1 lds\n" \
"v_mfma_f32_16x16x32_bf16 v[76:79], a[144:147], a[80:83], v[36:39]\n" \
"v_exp_f32_e64 v120, v120\n" \
"v_mfma_f32_16x16x32_bf16 v[76:79], a[148:151], a[84:87], v[76:79]\n" \
"v_exp_f32_e64 v121, v121\n" \
"v_mfma_f32_16x16x32_bf16 v[76:79], a[152:155], a[88:91], v[76:79]\n" \
"v_exp_f32_e64 v122, v122\n" \
"v_mfma_f32_16x16x32_bf16 v[76:79], a[156:159], a[92:95], v[76:79]\n" \
"v_exp_f32_e64 v123, v123\n" \
"v_mfma_f32_16x16x32_bf16 v[72:75], a[140:143], a[92:95], v[36:39]\n" \
"v_cvt_pk_bf16_f32 v120, v120, v121\n" \
"v_cvt_pk_bf16_f32 v121, v122, v123\n" \
"v_mfma_f32_16x16x32_bf16 v[72:75], a[136:139], a[88:91], v[72:75]\n" \
"v_exp_f32_e64 v124, v124\n" \
"v_mfma_f32_16x16x32_bf16 v[72:75], a[132:135], a[84:87], v[72:75]\n" \
"v_exp_f32_e64 v125, v125\n" \
"v_mfma_f32_16x16x32_bf16 v[72:75], a[128:131], a[80:83], v[72:75]\n" \
"buffer_load_dwordx4 v52, s[8:11], s64 offen offset:2048 sc1 lds\n" \
"v_mfma_f32_16x16x32_bf16 v[80:83], a[128:131], a[96:99], v[40:43]\n" \
"v_exp_f32_e64 v126, v126\n" \
"v_mfma_f32_16x16x32_bf16 v[80:83], a[132:135], a[100:103], v[80:83]\n" \
"v_exp_f32_e64 v127, v127\n" \
"v_mfma_f32_16x16x32_bf16 v[80:83], a[136:139], a[104:107], v[80:83]\n" \
"v_cvt_pk_bf16_f32 v122, v124, v125\n" \
"v_cvt_pk_bf16_f32 v123, v126, v127\n" \
"v_mfma_f32_16x16x32_bf16 v[80:83], a[140:143], a[108:111], v[80:83]\n" \
"v_exp_f32_e64 v70, v70\n" \
"v_mfma_f32_16x16x32_bf16 v[84:87], a[156:159], a[108:111], v[40:43]\n" \
"v_exp_f32_e64 v71, v71\n" \
"v_mfma_f32_16x16x32_bf16 v[84:87], a[152:155], a[104:107], v[84:87]\n" \
"v_exp_f32_e64 v72, v72\n" \
"v_mfma_f32_16x16x32_bf16 v[84:87], a[148:151], a[100:103], v[84:87]\n" \
"buffer_load_dwordx4 v52, s[8:11], s64 offen offset:3072 sc1 lds\n" \
"v_mfma_f32_16x16x32_bf16 v[84:87], a[144:147], a[96:99], v[84:87]\n" \
"s_mov_b32 m0, s65\n" \
"v_cvt_pk_bf16_f32 v71, v70, v71\n" \
"v_mfma_f32_16x16x32_bf16 v[92:95], a[144:147], a[112:115], v[44:47]\n" \
"ds_read_b128 a[144:147], v48 offset:12288\n" \
"v_add_u32_e32 v49, 0x10000, v48\n" \
"v_mfma_f32_16x16x32_bf16 v[92:95], a[148:151], a[116:119], v[92:95]\n" \
"ds_read_b128 a[148:151], v48 offset:13312\n" \
"s_xor_b32 s65, s65, 0x8000\n" \
"v_mfma_f32_16x16x32_bf16 v[92:95], a[152:155], a[120:123], v[92:95]\n" \
"v_exp_f32_e32 v73, v73\n" \
"v_mfma_f32_16x16x32_bf16 v[92:95], a[156:159], a[124:127], v[92:95]\n" \
"ds_read_b128 a[152:155], v48 offset:14336\n" \
".fill (L_ab + 8192 - .) / 4, 4, 0xBF800000\n" \
"L_ap2:\n" \
"s_cbranch_execz L_ap0\n" \
"ds_read_b128 a[156:159], v48 offset:15360\n" \
"v_mfma_f32_16x16x32_bf16 v[88:91], a[140:143], a[124:127], v[44:47]\n" \
"ds_read_b128 a[140:143], v48 offset:11264\n" \
"v_cvt_pk_bf16_f32 v72, v72, v73\n" \
"v_mfma_f32_16x16x32_bf16 v[88:91], a[136:139], a[120:123], v[88:91]\n" \
"v_exp_f32_e32 v68, v68\n" \
"v_mfma_f32_16x16x32_bf16 v[88:91], a[132:135], a[116:119], v[88:91]\n" \
"buffer_load_dwordx4 v53, s[8:11], s64 offen offset:0 sc1 lds\n" \
"v_mfma_f32_16x16x32_bf16 v[88:91], a[128:131], a[112:115], v[88:91]\n" \
"ds_read_b128 a[128:131], v48 offset:8192\n" \
"s_waitcnt lgkmcnt(6)\n" \
"v_mfma_f32_16x16x32_bf16 v[128:131], a[160:163], v[100:103], v[128:131]\n" \
"ds_read_b128 a[132:135], v48 offset:9216\n" \
"ds_read_b128 a[136:139], v48 offset:10240\n" \
"v_mfma_f32_16x16x32_bf16 v[160:163], a[160:163], v[104:107], v[160:163]\n" \
"v_exp_f32_e32 v69, v69\n" \
"v_mfma_f32_16x16x32_bf16 v[132:135], a[164:167], v[100:103], v[132:135]\n" \
"v_exp_f32_e64 v74, v74\n" \
"v_mfma_f32_16x16x32_bf16 v[164:167], a[164:167], v[104:107], v[164:167]\n" \
"v_exp_f32_e64 v75, v75\n" \
"v_mfma_f32_16x16x32_bf16 v[136:139], a[168:171], v[100:103], v[136:139]\n" \
"v_exp_f32_e64 v66, v66\n" \
"v_mfma_f32_16x16x32_bf16 v[168:171], a[168:171], v[104:107], v[168:171]\n" \
"buffer_load_dwordx4 v53, s[8:11], s64 offen offset:1024 sc1 lds\n" \
"v_mfma_f32_16x16x32_bf16 v[140:143], a[172:175], v[100:103], v[140:143]\n" \
"v_exp_f32_e64 v67, v67\n" \
"v_mfma_f32_16x16x32_bf16 v[172:175], a[172:175], v[104:107], v[172:175]\n" \
"v_exp_f32_e64 v64, v64\n" \
"v_mfma_f32_16x16x32_bf16 v[144:147], a[176:179], v[100:103], v[144:147]\n" \
"v_exp_f32_e64 v65, v65\n" \
"v_mfma_f32_16x16x32_bf16 v[176:179], a[176:179], v[104:107], v[176:179]\n" \
"v_exp_f32_e64 v76, v76\n" \
"v_mfma_f32_16x16x32_bf16 v[148:151], a[180:183], v[100:103], v[148:151]\n" \
"v_exp_f32_e64 v77, v77\n" \
"v_mfma_f32_16x16x32_bf16 v[180:183], a[180:183], v[104:107], v[180:183]\n" \
"v_exp_f32_e64 v78, v78\n" \
"v_mfma_f32_16x16x32_bf16 v[152:155], a[184:187], v[100:103], v[152:155]\n" \
"v_exp_f32_e64 v79, v79\n" \
"v_mfma_f32_16x16x32_bf16 v[184:187], a[184:187], v[104:107], v[184:187]\n" \
"buffer_load_dwordx4 v53, s[8:11], s64 offen offset:2048 sc1 lds\n" \
"v_mfma_f32_16x16x32_bf16 v[156:159], a[188:191], v[100:103], v[156:159]\n" \
"v_cvt_pk_bf16_f32 v70, v68, v69\n" \
"v_cvt_pk_bf16_f32 v73, v74, v75\n" \
"v_mfma_f32_16x16x32_bf16 v[188:191], a[188:191], v[104:107], v[188:191]\n" \
"v_cvt_pk_bf16_f32 v69, v66, v67\n" \
"v_cvt_pk_bf16_f32 v68, v64, v65\n" \
"v_smfmac_f32_16x16x64_bf16 a[192:195], a[200:203], v[100:107], v21\n" \
"v_cvt_pk_bf16_f32 v74, v76, v77\n" \
"v_cvt_pk_bf16_f32 v75, v78, v79\n" \
"v_mfma_f32_16x16x32_bf16 v[220:223], a[188:191], v[116:119], v[220:223]\n" \
"v_xor_b32_e32 v48, 0x8000, v48\n" \
"v_mfma_f32_16x16x32_bf16 v[252:255], a[188:191], v[120:123], v[252:255]\n" \
"v_mfma_f32_16x16x32_bf16 v[216:219], a[184:187], v[116:119], v[216:219]\n" \
"v_mfma_f32_16x16x32_bf16 v[248:251], a[184:187], v[120:123], v[248:251]\n" \
"buffer_load_dwordx4 v53, s[8:11], s64 offen offset:3072 sc1 lds\n" \
"v_mfma_f32_16x16x32_bf16 v[212:215], a[180:183], v[116:119], v[212:215]\n" \
"s_add_u32 s64, s64, 0x10000\n" \
"s_sub_u32 s66, s66, 1\n" \
"s_nop 0\n" \
"v_mfma_f32_16x16x32_bf16 v[244:247], a[180:183], v[120:123], v[244:247]\n" \
"v_mfma_f32_16x16x32_bf16 v[208:211], a[176:179], v[116:119], v[208:211]\n" \
"v_mfma_f32_16x16x32_bf16 v[240:243], a[176:179], v[120:123], v[240:243]\n" \
"v_mfma_f32_16x16x32_bf16 v[204:207], a[172:175], v[116:119], v[204:207]\n" \
"v_mfma_f32_16x16x32_bf16 v[236:239], a[172:175], v[120:123], v[236:239]\n" \
"v_mfma_f32_16x16x32_bf16 v[200:203], a[168:171], v[116:119], v[200:203]\n" \
"v_mfma_f32_16x16x32_bf16 v[232:235], a[168:171], v[120:123], v[232:235]\n" \
"v_mfma_f32_16x16x32_bf16 v[196:199], a[164:167], v[116:119], v[196:199]\n" \
"v_mfma_f32_16x16x32_bf16 v[228:231], a[164:167], v[120:123], v[228:231]\n" \
"v_mfma_f32_16x16x32_bf16 v[192:195], a[160:163], v[116:119], v[192:195]\n" \
"v_mfma_f32_16x16x32_bf16 v[224:227], a[160:163], v[120:123], v[224:227]\n" \
"v_smfmac_f32_16x16x64_bf16 a[196:199], a[200:203], v[116:123], v21\n" \
"s_waitcnt vmcnt(8) lgkmcnt(0)\n" \
"s_barrier\n" \
"s_cbranch_scc0 L_2_%=\n" \
"s_cmp_lg_u32 s16, 0\n" \
"s_cbranch_scc1 L_f1_%=\n" \
"v_bfe_u32 v58, v20, 2, 7\n" \
"v_readlane_b32 s96, v11, 1\n" \
"ds_read_b128 a[160:163], v49 offset:0\n" \
"ds_read_b128 a[164:167], v49 offset:1024\n" \
"ds_read_b128 a[168:171], v49 offset:2048\n" \
"v_mfma_f32_16x16x32_bf16 v[120:123], a[128:131], a[112:115], v[44:47]\n" \
"v_readlane_b32 s93, v11, 20\n" \
"ds_read_b128 a[172:175], v49 offset:3072\n" \
"buffer_load_dword v55, v58, s[56:59], s96 offen sc1\n" \
"v_readlane_b32 s84, v11, 15\n" \
"v_readlane_b32 s85, v11, 16\n" \
"v_readlane_b32 s86, v11, 17\n" \
"s_mov_b32 s87, 0x20000\n" \
"v_readlane_b32 s92, v11, 18\n" \
"s_add_u32 m0, s93, 0x0\n" \
"v_mfma_f32_16x16x32_bf16 v[120:123], a[132:135], a[116:119], v[120:123]\n" \
"ds_read_b128 a[176:179], v49 offset:4096\n" \
"v_add_u32_e32 v54, 0x49000, v20\n" \
"v_mfma_f32_16x16x32_bf16 v[120:123], a[136:139], a[120:123], v[120:123]\n" \
"v_readlane_b32 s97, v11, 35\n" \
"v_readlane_b32 s98, v11, 36\n" \
"v_mfma_f32_16x16x32_bf16 v[120:123], a[140:143], a[124:127], v[120:123]\n" \
"ds_read_b128 a[180:183], v49 offset:5120\n" \
"ds_read_b128 a[184:187], v49 offset:6144\n" \
"v_mfma_f32_16x16x32_bf16 v[124:127], a[156:159], a[124:127], v[44:47]\n" \
"ds_read_b128 a[188:191], v49 offset:7168\n" \
"v_add_u32_e32 v50, s15, v20\n" \
"buffer_load_dwordx4 v0, s[84:87], s92 offen offset:0 lds\n" \
"v_mfma_f32_16x16x32_bf16 v[124:127], a[152:155], a[120:123], v[124:127]\n" \
"v_exp_f32_e32 v86, v86\n" \
"v_mfma_f32_16x16x32_bf16 v[124:127], a[148:151], a[116:119], v[124:127]\n" \
"v_exp_f32_e32 v87, v87\n" \
"v_mfma_f32_16x16x32_bf16 v[124:127], a[144:147], a[112:115], v[124:127]\n" \
"v_cvt_pk_bf16_f32 v87, v86, v87\n" \
"s_waitcnt lgkmcnt(7)\n" \
"v_mfma_f32_16x16x32_bf16 v[128:131], a[160:163], v[68:71], v[128:131]\n" \
"v_exp_f32_e32 v84, v84\n" \
"v_mfma_f32_16x16x32_bf16 v[160:163], a[160:163], v[72:75], v[160:163]\n" \
"v_and_b32_e32 v87, v15, v87\n" \
"s_waitcnt lgkmcnt(6)\n" \
"v_mfma_f32_16x16x32_bf16 v[132:135], a[164:167], v[68:71], v[132:135]\n" \
"buffer_load_dwordx4 v1, s[84:87], s92 offen offset:1024 lds\n" \
"v_mfma_f32_16x16x32_bf16 v[164:167], a[164:167], v[72:75], v[164:167]\n" \
"v_add_u32_e32 v51, 0x10000, v50\n" \
"s_waitcnt lgkmcnt(5)\n" \
"v_mfma_f32_16x16x32_bf16 v[136:139], a[168:171], v[68:71], v[136:139]\n" \
"v_exp_f32_e32 v85, v85\n" \
"v_mfma_f32_16x16x32_bf16 v[168:171], a[168:171], v[72:75], v[168:171]\n" \
"v_cvt_pk_bf16_f32 v86, v84, v85\n" \
"s_waitcnt lgkmcnt(4)\n" \
"v_mfma_f32_16x16x32_bf16 v[140:143], a[172:175], v[68:71], v[140:143]\n" \
"v_exp_f32_e32 v82, v82\n" \
"v_mfma_f32_16x16x32_bf16 v[172:175], a[172:175], v[72:75], v[172:175]\n" \
"v_and_b32_e32 v86, v14, v86\n" \
"s_waitcnt lgkmcnt(3)\n" \
"v_mfma_f32_16x16x32_bf16 v[144:147], a[176:179], v[68:71], v[144:147]\n" \
"buffer_load_dwordx4 v2, s[84:87], s92 offen offset:2048 lds\n" \
"v_mfma_f32_16x16x32_bf16 v[176:179], a[176:179], v[72:75], v[176:179]\n" \
"v_cvt_pk_bf16_f32 v128, v128, v129\n" \
"s_waitcnt lgkmcnt(1)\n" \
"v_mfma_f32_16x16x32_bf16 v[148:151], a[180:183], v[68:71], v[148:151]\n" \
"v_exp_f32_e32 v83, v83\n" \
"v_mfma_f32_16x16x32_bf16 v[180:183], a[180:183], v[72:75], v[180:183]\n" \
"v_exp_f32_e32 v80, v80\n" \
"v_mfma_f32_16x16x32_bf16 v[152:155], a[184:187], v[68:71], v[152:155]\n" \
"v_exp_f32_e32 v81, v81\n" \
"v_mfma_f32_16x16x32_bf16 v[184:187], a[184:187], v[72:75], v[184:187]\n" \
"v_cvt_pk_bf16_f32 v85, v82, v83\n" \
"s_waitcnt lgkmcnt(0)\n" \
"v_mfma_f32_16x16x32_bf16 v[156:159], a[188:191], v[68:71], v[156:159]\n" \
"buffer_load_dwordx4 v3, s[84:87], s92 offen offset:3072 lds\n" \
"s_add_u32 m0, s93, 0x1000\n" \
"v_mfma_f32_16x16x32_bf16 v[188:191], a[188:191], v[72:75], v[188:191]\n" \
"v_cvt_pk_bf16_f32 v84, v80, v81\n" \
"v_and_b32_e32 v84, v12, v84\n" \
"v_smfmac_f32_16x16x64_bf16 a[192:195], a[200:203], v[68:75], v21\n" \
"v_and_b32_e32 v85, v13, v85\n" \
"v_exp_f32_e32 v88, v88\n" \
"v_exp_f32_e32 v89, v89\n" \
"v_mfma_f32_16x16x32_bf16 v[220:223], a[188:191], v[84:87], v[220:223]\n" \
"v_exp_f32_e32 v90, v90\n" \
"v_exp_f32_e32 v91, v91\n" \
"v_cvt_pk_bf16_f32 v88, v88, v89\n" \
"v_exp_f32_e32 v92, v92\n" \
"buffer_load_dwordx4 v4, s[84:87], s92 offen offset:0 lds\n" \
"v_exp_f32_e32 v93, v93\n" \
"v_exp_f32_e32 v94, v94\n" \
"v_exp_f32_e32 v95, v95\n" \
"v_cvt_pk_bf16_f32 v160, v160, v161\n" \
"v_cvt_pk_bf16_f32 v89, v90, v91\n" \
"v_cvt_pk_bf16_f32 v129, v130, v131\n" \
"v_cvt_pk_bf16_f32 v161, v162, v163\n" \
"v_cvt_pk_bf16_f32 v90, v92, v93\n" \
"v_cvt_pk_bf16_f32 v91, v94, v95\n" \
"v_cvt_pk_bf16_f32 v130, v132, v133\n" \
"v_cvt_pk_bf16_f32 v131, v134, v135\n" \
"v_cvt_pk_bf16_f32 v162, v164, v165\n" \
"v_cvt_pk_bf16_f32 v163, v166, v167\n" \
"v_mfma_f32_16x16x32_bf16 v[252:255], a[188:191], v[88:91], v[252:255]\n" \
"v_cvt_pk_bf16_f32 v132, v136, v137\n" \
"v_cvt_pk_bf16_f32 v133, v138, v139\n" \
"v_cvt_pk_bf16_f32 v134, v140, v141\n" \
"buffer_load_dwordx4 v5, s[84:87], s92 offen offset:1024 lds\n" \
"v_cvt_pk_bf16_f32 v135, v142, v143\n" \
"v_cvt_pk_bf16_f32 v164, v168, v169\n" \
"v_cvt_pk_bf16_f32 v165, v170, v171\n" \
"v_cvt_pk_bf16_f32 v166, v172, v173\n" \
"v_cvt_pk_bf16_f32 v167, v174, v175\n" \
"v_cvt_pk_bf16_f32 v136, v144, v145\n" \
"v_cvt_pk_bf16_f32 v137, v146, v147\n" \
"v_cvt_pk_bf16_f32 v138, v148, v149\n" \
"v_cvt_pk_bf16_f32 v139, v150, v151\n" \
"v_cvt_pk_bf16_f32 v140, v152, v153\n" \
"v_cvt_pk_bf16_f32 v141, v154, v155\n" \
"v_cvt_pk_bf16_f32 v142, v156, v157\n" \
"v_cvt_pk_bf16_f32 v143, v158, v159\n" \
"v_cvt_pk_bf16_f32 v168, v176, v177\n" \
"v_cvt_pk_bf16_f32 v169, v178, v179\n" \
"v_cvt_pk_bf16_f32 v170, v180, v181\n" \
"v_cvt_pk_bf16_f32 v171, v182, v183\n" \
"v_cvt_pk_bf16_f32 v172, v184, v185\n" \
"v_cvt_pk_bf16_f32 v173, v186, v187\n" \
"v_cvt_pk_bf16_f32 v174, v188, v189\n" \
"v_cvt_pk_bf16_f32 v175, v190, v191\n" \
"buffer_load_dwordx4 v6, s[84:87], s92 offen offset:2048 lds\n" \
"s_waitcnt vmcnt(8)\n" \
"ds_write_b128 v50, v[128:131] offset:0\n" \
"v_mfma_f32_16x16x32_bf16 v[216:219], a[184:187], v[84:87], v[216:219]\n" \
"ds_read_b32 v60, v18 offset:0\n" \
"ds_read_b32 v61, v18 offset:128\n" \
"v_mfma_f32_16x16x32_bf16 v[248:251], a[184:187], v[88:91], v[248:251]\n" \
"ds_write_b128 v50, v[132:135] offset:1024\n" \
"s_waitcnt vmcnt(7)\n" \
"v_cmp_ne_u32_e32 vcc, s62, v55\n" \
"v_mfma_f32_16x16x32_bf16 v[212:215], a[180:183], v[84:87], v[212:215]\n" \
"ds_read_b128 a[0:3], v49 offset:8192\n" \
"ds_read_b128 a[4:7], v49 offset:9216\n" \
"ds_write_b128 v50, v[136:139] offset:2048\n" \
"v_mfma_f32_16x16x32_bf16 v[244:247], a[180:183], v[88:91], v[244:247]\n" \
"buffer_load_dwordx4 v7, s[84:87], s92 offen offset:3072 lds\n" \
"s_add_u32 m0, s93, 0x2000\n" \
"s_cbranch_vccnz L_so0_%=\n" \
"L_ro0_%=:\n" \
"ds_write_b128 v50, v[140:143] offset:3072\n" \
"ds_read_b128 a[8:11], v49 offset:10240\n" \
"ds_read_b128 a[12:15], v49 offset:11264\n" \
"ds_read_b128 a[16:19], v49 offset:12288\n" \
"ds_read_b128 a[20:23], v49 offset:13312\n" \
"ds_read_b128 a[24:27], v49 offset:14336\n" \
"ds_read_b128 a[28:31], v49 offset:15360\n" \
"ds_write_b128 v51, v[160:163] offset:32768\n" \
"v_mfma_f32_16x16x32_bf16 v[208:211], a[176:179], v[84:87], v[208:211]\n" \
"v_accvgpr_read_b32 v40, a192\n" \
"v_accvgpr_read_b32 v22, a193\n" \
"v_mfma_f32_16x16x32_bf16 v[240:243], a[176:179], v[88:91], v[240:243]\n" \
"ds_write_b128 v51, v[164:167] offset:33792\n" \
"v_mfma_f32_16x16x32_bf16 v[204:207], a[172:175], v[84:87], v[204:207]\n" \
"buffer_load_dwordx4 v8, s[84:87], s92 offen offset:0 lds\n" \
"v_mfma_f32_16x16x32_bf16 v[236:239], a[172:175], v[88:91], v[236:239]\n" \
"ds_write_b128 v51, v[168:171] offset:34816\n" \
"v_mfma_f32_16x16x32_bf16 v[200:203], a[168:171], v[84:87], v[200:203]\n" \
"v_accvgpr_read_b32 v41, a194\n" \
"v_accvgpr_read_b32 v23, a195\n" \
"v_mfma_f32_16x16x32_bf16 v[232:235], a[168:171], v[88:91], v[232:235]\n" \
"ds_write_b128 v51, v[172:175] offset:35840\n" \
"v_mfma_f32_16x16x32_bf16 v[196:199], a[164:167], v[84:87], v[196:199]\n" \
"v_add_f32_e32 v40, v40, v22\n" \
"v_add_f32_e32 v41, v41, v23\n" \
"v_mfma_f32_16x16x32_bf16 v[228:231], a[164:167], v[88:91], v[228:231]\n" \
"ds_write_b64 v19, v[40:41]\n" \
"v_lshl_add_u32 v52, s15, 2, v20\n" \
"v_mfma_f32_16x16x32_bf16 v[192:195], a[160:163], v[84:87], v[192:195]\n" \
"buffer_load_dwordx4 a[220:223], v20, s[56:59], s60 offen offset:0 sc1\n" \
"v_mfma_f32_16x16x32_bf16 v[224:227], a[160:163], v[88:91], v[224:227]\n" \
"v_add_u32_e32 v52, 0x1a000, v52\n" \
"v_xor_b32_e32 v63, 0x400, v19\n" \
"v_smfmac_f32_16x16x64_bf16 a[196:199], a[200:203], v[84:91], v21\n" \
"v_exp_f32_e32 v120, v120\n" \
"v_exp_f32_e32 v121, v121\n" \
"v_exp_f32_e32 v122, v122\n" \
"v_exp_f32_e32 v123, v123\n" \
"v_cvt_pk_bf16_f32 v120, v120, v121\n" \
"v_exp_f32_e32 v124, v124\n" \
"v_exp_f32_e32 v125, v125\n" \
"v_exp_f32_e32 v126, v126\n" \
"buffer_load_dwordx4 a[236:239], v54, s[56:59], s60 offen offset:0 sc1\n" \
"v_exp_f32_e32 v127, v127\n" \
"v_cvt_pk_bf16_f32 v121, v122, v123\n" \
"v_cvt_pk_bf16_f32 v122, v124, v125\n" \
"v_cvt_pk_bf16_f32 v123, v126, v127\n" \
"v_and_b32_e32 v120, v12, v120\n" \
"v_and_b32_e32 v121, v13, v121\n" \
"v_and_b32_e32 v122, v14, v122\n" \
"v_and_b32_e32 v123, v15, v123\n" \
"s_waitcnt lgkmcnt(0)\n" \
"s_barrier\n" \
"ds_read_b128 v[144:147], v52 offset:0\n" \
"ds_read_b128 v[148:151], v52 offset:1024\n" \
"ds_read_b128 v[152:155], v52 offset:2048\n" \
"ds_read_b128 v[156:159], v52 offset:3072\n" \
"buffer_load_dwordx4 a[224:227], v20, s[56:59], s60 offen offset:1024 sc1\n" \
"ds_read_b128 v[176:179], v52 offset:4096\n" \
"ds_read_b128 v[180:183], v52 offset:5120\n" \
"ds_read_b128 v[184:187], v52 offset:6144\n" \
"ds_read_b128 v[188:191], v52 offset:7168\n" \
"v_mfma_f32_16x16x32_bf16 a[196:199], a[212:215], v[120:123], a[196:199]\n" \
"ds_read_b64 v[42:43], v63\n" \
"v_mov_b32_e32 v130, v28\n" \
"v_mfma_f32_16x16x32_bf16 v[224:227], a[0:3], v[120:123], v[224:227]\n" \
"v_mov_b32_e32 v131, v29\n" \
"v_mov_b32_e32 v132, 0\n" \
"v_mfma_f32_16x16x32_bf16 v[228:231], a[4:7], v[120:123], v[228:231]\n" \
"v_mov_b32_e32 v133, 0\n" \
"v_mov_b32_e32 v128, 0\n" \
"v_mfma_f32_16x16x32_bf16 v[232:235], a[8:11], v[120:123], v[232:235]\n" \
"v_mov_b32_e32 v129, 0\n" \
"v_accvgpr_read_b32 v56, a196\n" \
"v_mfma_f32_16x16x32_bf16 v[236:239], a[12:15], v[120:123], v[236:239]\n" \
"buffer_load_dwordx4 a[240:243], v54, s[56:59], s60 offen offset:1024 sc1\n" \
"v_mfma_f32_16x16x32_bf16 v[240:243], a[16:19], v[120:123], v[240:243]\n" \
"v_accvgpr_read_b32 v24, a197\n" \
"v_add_f32_e32 v56, v56, v24\n" \
"v_mfma_f32_16x16x32_bf16 v[244:247], a[20:23], v[120:123], v[244:247]\n" \
"v_accvgpr_read_b32 v57, a198\n" \
"v_accvgpr_read_b32 v25, a199\n" \
"v_mfma_f32_16x16x32_bf16 v[248:251], a[24:27], v[120:123], v[248:251]\n" \
"v_add_f32_e32 v57, v57, v25\n" \
"v_mfma_f32_16x16x32_bf16 v[252:255], a[28:31], v[120:123], v[252:255]\n" \
"s_waitcnt lgkmcnt(1)\n" \
"s_barrier\n" \
"buffer_load_dwordx4 a[228:231], v20, s[56:59], s60 offen offset:2048 sc1\n" \
"v_mfma_f32_16x16x32_bf16 v[192:195], v[130:133], v[144:147], v[192:195]\n" \
"s_waitcnt lgkmcnt(0)\n" \
"v_add_f32_e32 v56, v56, v42\n" \
"v_mfma_f32_16x16x32_bf16 v[196:199], v[128:131], v[144:147], v[196:199]\n" \
"v_max_f32_e32 v62, v62, v56\n" \
"v_add_f32_e32 v57, v57, v43\n" \
"v_mfma_f32_16x16x32_bf16 v[200:203], v[130:133], v[148:151], v[200:203]\n" \
"v_rcp_f32_e32 v56, v56\n" \
"v_mfma_f32_16x16x32_bf16 v[204:207], v[128:131], v[148:151], v[204:207]\n" \
"v_mul_f32_e32 v56, v56, v60\n" \
"v_mul_f32_e32 v192, v56, v192\n" \
"v_mfma_f32_16x16x32_bf16 v[208:211], v[130:133], v[152:155], v[208:211]\n" \
"v_mul_f32_e32 v193, v56, v193\n" \
"v_mul_f32_e32 v194, v56, v194\n" \
"v_mfma_f32_16x16x32_bf16 v[212:215], v[128:131], v[152:155], v[212:215]\n" \
"buffer_load_dwordx4 a[244:247], v54, s[56:59], s60 offen offset:2048 sc1\n" \
"v_mfma_f32_16x16x32_bf16 v[216:219], v[130:133], v[156:159], v[216:219]\n" \
"v_mul_f32_e32 v195, v56, v195\n" \
"v_mul_f32_e32 v196, v56, v196\n" \
"v_mfma_f32_16x16x32_bf16 v[220:223], v[128:131], v[156:159], v[220:223]\n" \
"v_mul_f32_e32 v197, v56, v197\n" \
"v_mul_f32_e32 v198, v56, v198\n" \
"v_mfma_f32_16x16x32_bf16 v[224:227], v[130:133], v[176:179], v[224:227]\n" \
"v_mul_f32_e32 v199, v56, v199\n" \
"v_cvt_pk_bf16_f32 v64, v192, v193\n" \
"v_mfma_f32_16x16x32_bf16 v[228:231], v[128:131], v[176:179], v[228:231]\n" \
"v_cvt_pk_bf16_f32 v65, v194, v195\n" \
"v_cvt_pk_bf16_f32 v66, v196, v197\n" \
"v_mfma_f32_16x16x32_bf16 v[232:235], v[130:133], v[180:183], v[232:235]\n" \
"v_cvt_pk_bf16_f32 v67, v198, v199\n" \
"v_mul_f32_e32 v200, v56, v200\n" \
"v_mfma_f32_16x16x32_bf16 v[236:239], v[128:131], v[180:183], v[236:239]\n" \
"buffer_load_dwordx4 a[232:235], v20, s[56:59], s60 offen offset:3072 sc1\n" \
"v_mfma_f32_16x16x32_bf16 v[240:243], v[130:133], v[184:187], v[240:243]\n" \
"v_mfma_f32_16x16x32_bf16 a[208:211], a[216:219], v[64:67], a[208:211]\n" \
"v_mul_f32_e32 v201, v56, v201\n" \
"v_mul_f32_e32 v202, v56, v202\n" \
"v_mul_f32_e32 v203, v56, v203\n" \
"v_mul_f32_e32 v204, v56, v204\n" \
"v_mul_f32_e32 v205, v56, v205\n" \
"v_mul_f32_e32 v206, v56, v206\n" \
"v_mul_f32_e32 v207, v56, v207\n" \
"v_cvt_pk_bf16_f32 v68, v200, v201\n" \
"v_cvt_pk_bf16_f32 v69, v202, v203\n" \
"v_cvt_pk_bf16_f32 v70, v204, v205\n" \
"v_cvt_pk_bf16_f32 v71, v206, v207\n" \
"v_mul_f32_e32 v208, v56, v208\n" \
"v_mul_f32_e32 v209, v56, v209\n" \
"v_mfma_f32_16x16x32_bf16 a[208:211], a[216:219], v[68:71], a[208:211]\n" \
"buffer_load_dwordx4 a[248:251], v54, s[56:59], s60 offen offset:3072 sc1\n" \
"v_mul_f32_e32 v210, v56, v210\n" \
"v_mul_f32_e32 v211, v56, v211\n" \
"v_mul_f32_e32 v212, v56, v212\n" \
"v_mul_f32_e32 v213, v56, v213\n" \
"v_mul_f32_e32 v214, v56, v214\n" \
"v_mul_f32_e32 v215, v56, v215\n" \
"v_max_f32_e32 v62, v62, v57\n" \
"v_cvt_pk_bf16_f32 v72, v208, v209\n" \
"v_cvt_pk_bf16_f32 v73, v210, v211\n" \
"v_cvt_pk_bf16_f32 v74, v212, v213\n" \
"v_cvt_pk_bf16_f32 v75, v214, v215\n" \
"v_rcp_f32_e32 v57, v57\n" \
"v_mfma_f32_16x16x32_bf16 v[244:247], v[128:131], v[184:187], v[244:247]\n" \
"v_mfma_f32_16x16x32_bf16 a[208:211], a[216:219], v[72:75], a[208:211]\n" \
"v_mul_f32_e32 v216, v56, v216\n" \
"v_mul_f32_e32 v217, v56, v217\n" \
"buffer_load_dwordx4 a[96:99], v20, s[56:59], s97 offen offset:0 sc1\n" \
"v_mul_f32_e32 v218, v56, v218\n" \
"v_mul_f32_e32 v219, v56, v219\n" \
"v_mul_f32_e32 v220, v56, v220\n" \
"v_mul_f32_e32 v221, v56, v221\n" \
"v_mul_f32_e32 v222, v56, v222\n" \
"v_mul_f32_e32 v223, v56, v223\n" \
"v_cvt_pk_bf16_f32 v76, v216, v217\n" \
"v_cvt_pk_bf16_f32 v77, v218, v219\n" \
"v_cvt_pk_bf16_f32 v78, v220, v221\n" \
"v_cvt_pk_bf16_f32 v79, v222, v223\n" \
"v_mul_f32_e32 v57, v57, v61\n" \
"v_mul_f32_e32 v224, v57, v224\n" \
"v_mfma_f32_16x16x32_bf16 a[208:211], a[216:219], v[76:79], a[208:211]\n" \
"v_mul_f32_e32 v225, v57, v225\n" \
"v_mul_f32_e32 v226, v57, v226\n" \
"v_mul_f32_e32 v227, v57, v227\n" \
"v_mul_f32_e32 v228, v57, v228\n" \
"v_mul_f32_e32 v229, v57, v229\n" \
"v_mul_f32_e32 v230, v57, v230\n" \
"v_mul_f32_e32 v231, v57, v231\n" \
"buffer_load_dwordx4 a[104:107], v54, s[56:59], s97 offen offset:0 sc1\n" \
"v_cvt_pk_bf16_f32 v80, v224, v225\n" \
"v_cvt_pk_bf16_f32 v81, v226, v227\n" \
"v_cvt_pk_bf16_f32 v82, v228, v229\n" \
"v_cvt_pk_bf16_f32 v83, v230, v231\n" \
"v_mfma_f32_16x16x32_bf16 v[248:251], v[130:133], v[188:191], v[248:251]\n" \
"v_mul_f32_e32 v232, v57, v232\n" \
"v_mul_f32_e32 v233, v57, v233\n" \
"v_mfma_f32_16x16x32_bf16 a[208:211], a[216:219], v[80:83], a[208:211]\n" \
"v_mul_f32_e32 v234, v57, v234\n" \
"v_mul_f32_e32 v235, v57, v235\n" \
"v_mul_f32_e32 v236, v57, v236\n" \
"v_mul_f32_e32 v237, v57, v237\n" \
"v_mul_f32_e32 v238, v57, v238\n" \
"v_mul_f32_e32 v239, v57, v239\n" \
"v_cvt_pk_bf16_f32 v84, v232, v233\n" \
"v_cvt_pk_bf16_f32 v85, v234, v235\n" \
"v_cvt_pk_bf16_f32 v86, v236, v237\n" \
"v_cvt_pk_bf16_f32 v87, v238, v239\n" \
"v_mul_f32_e32 v240, v57, v240\n" \
"buffer_load_dwordx4 a[112:115], v20, s[56:59], s98 offen offset:0 sc1\n" \
"v_mfma_f32_16x16x32_bf16 a[208:211], a[216:219], v[84:87], a[208:211]\n" \
"v_mul_f32_e32 v241, v57, v241\n" \
"v_mul_f32_e32 v242, v57, v242\n" \
"v_mul_f32_e32 v243, v57, v243\n" \
"v_mul_f32_e32 v244, v57, v244\n" \
"v_mul_f32_e32 v245, v57, v245\n" \
"v_mul_f32_e32 v246, v57, v246\n" \
"v_mul_f32_e32 v247, v57, v247\n" \
"v_cvt_pk_bf16_f32 v96, v240, v241\n" \
"v_cvt_pk_bf16_f32 v97, v242, v243\n" \
"v_cvt_pk_bf16_f32 v98, v244, v245\n" \
"v_cvt_pk_bf16_f32 v99, v246, v247\n" \
"v_mfma_f32_16x16x32_bf16 v[252:255], v[128:131], v[188:191], v[252:255]\n" \
"v_mul_f32_e32 v248, v57, v248\n" \
"v_mfma_f32_16x16x32_bf16 a[208:211], a[216:219], v[96:99], a[208:211]\n" \
"v_mul_f32_e32 v249, v57, v249\n" \
"v_mul_f32_e32 v250, v57, v250\n" \
"buffer_load_dwordx4 a[120:123], v54, s[56:59], s98 offen offset:0 sc1\n" \
"v_mul_f32_e32 v251, v57, v251\n" \
"v_cvt_pk_bf16_f32 v100, v248, v249\n" \
"v_cvt_pk_bf16_f32 v101, v250, v251\n" \
"v_mul_f32_e32 v252, v57, v252\n" \
"v_mul_f32_e32 v253, v57, v253\n" \
"v_mul_f32_e32 v254, v57, v254\n" \
"v_mul_f32_e32 v255, v57, v255\n" \
"v_cvt_pk_bf16_f32 v102, v252, v253\n" \
"v_cvt_pk_bf16_f32 v103, v254, v255\n" \
"v_cmp_lt_f32_e64 s[72:73], s21, v62\n" \
"buffer_load_dwordx4 a[100:103], v20, s[56:59], s97 offen offset:1024 sc1\n" \
"v_mfma_f32_16x16x32_bf16 a[208:211], a[216:219], v[100:103], a[208:211]\n" \
"buffer_load_dwordx4 a[108:111], v54, s[56:59], s97 offen offset:1024 sc1\n" \
"buffer_load_dwordx4 a[116:119], v20, s[56:59], s98 offen offset:1024 sc1\n" \
"buffer_load_dwordx4 a[124:127], v54, s[56:59], s98 offen offset:1024 sc1\n" \
"s_nop 4\n" \
"v_accvgpr_read_b32 v63, a208\n" \
"v_cmp_u_f32_e32 vcc, v63, v63\n" \
"s_nop 0\n" \
"s_or_b64 vcc, s[72:73], vcc\n" \
"s_cbranch_scc0 L_out_%=\n" \
"s_branch L_end_%=\n" \
"L_skb_%=:\n" \
"s_sleep 1\n" \
"buffer_load_dword v57, v61, s[56:59], s96 offen sc1\n" \
"s_add_u32 s101, s101, 1\n" \
"s_waitcnt vmcnt(0)\n" \
"v_cmp_ne_u32_e32 vcc, s62, v57\n" \
"s_cmp_gt_u32 s101, 0x100000\n" \
"s_cbranch_scc1 L_rkb_%=\n" \
"s_nop 2\n" \
"s_cbranch_vccnz L_skb_%=\n" \
"s_branch L_rkb_%=\n" \
"L_so0_%=:\n" \
"s_sleep 1\n" \
"buffer_load_dword v55, v58, s[56:59], s96 offen sc1\n" \
"s_add_u32 s101, s101, 1\n" \
"s_waitcnt vmcnt(0)\n" \
"v_cmp_ne_u32_e32 vcc, s62, v55\n" \
"s_cmp_gt_u32 s101, 0x100000\n" \
"s_cbranch_scc1 L_ro0_%=\n" \
"s_nop 2\n" \
"s_cbranch_vccnz L_so0_%=\n" \
"s_branch L_ro0_%=\n" \
"L_so1_%=:\n" \
"s_sleep 1\n" \
"buffer_load_dword v57, v60, s[56:59], s96 offen sc1\n" \
"s_add_u32 s101, s101, 1\n" \
"s_waitcnt vmcnt(0)\n" \
"v_cmp_ne_u32_e32 vcc, s62, v57\n" \
"s_cmp_gt_u32 s101, 0x100000\n" \
"s_cbranch_scc1 L_ro1_%=\n" \
"s_nop 2\n" \
"s_cbranch_vccnz L_so1_%=\n" \
"s_branch L_ro1_%=\n" \
"L_sa_%=:\n" \
"s_sleep 1\n" \
"buffer_load_dword v26, v38, s[56:59], s64 offen sc1\n" \
"s_add_u32 s101, s101, 1\n" \
"s_waitcnt vmcnt(0)\n" \
"v_cmp_ne_u32_e32 vcc, s62, v26\n" \
"s_cmp_gt_u32 s101, 0x100000\n" \
"s_cbranch_scc1 L_ra_%=\n" \
"s_nop 2\n" \
"s_cbranch_vccnz L_sa_%=\n" \
"s_branch L_ra_%=\n" \
"L_sb_%=:\n" \
"s_sleep 1\n" \
"buffer_load_dword v47, v38, s[56:59], s73 offen sc1\n" \
"s_add_u32 s101, s101, 1\n" \
"s_waitcnt vmcnt(0)\n" \
"v_cmp_ne_u32_e32 vcc, s62, v47\n" \
"s_cmp_gt_u32 s101, 0x100000\n" \
"s_cbranch_scc1 L_rb_%=\n" \
"s_nop 2\n" \
"s_cbranch_vccnz L_sb_%=\n" \
"s_branch L_rb_%=\n" \
"L_sx0_%=:\n" \
"s_sleep 1\n" \
"buffer_load_dword v37, v40, s[56:59], s74 offen sc1\n" \
"s_add_u32 s101, s101, 1\n" \
"s_waitcnt vmcnt(0)\n" \
"v_cmp_eq_u32_e64 s[98:99], s62, v37\n" \
"s_cmp_gt_u32 s101, 0x100000\n" \
"s_cbranch_scc1 L_rx0_%=\n" \
"s_or_b32 s75, 0x7f, s100\n" \
"s_andn2_b32 s75, s75, s98\n" \
"s_cbranch_scc1 L_sx0_%=\n" \
"s_branch L_rx0_%=\n" \
"L_sx1_%=:\n" \
"s_sleep 1\n" \
"buffer_load_dword v37, v40, s[56:59], s74 offen sc1\n" \
"s_add_u32 s101, s101, 1\n" \
"s_waitcnt vmcnt(0)\n" \
"v_cmp_eq_u32_e64 s[98:99], s62, v37\n" \
"s_cmp_gt_u32 s101, 0x100000\n" \
"s_cbranch_scc1 L_rx1_%=\n" \
"s_andn2_b32 s75, 0x100, s98\n" \
"s_cbranch_scc1 L_sx1_%=\n" \
"s_branch L_rx1_%=\n" \
"L_sx2_%=:\n" \
"s_sleep 1\n" \
"buffer_load_dword v37, v40, s[56:59], s74 offen sc1\n" \
"s_add_u32 s101, s101, 1\n" \
"s_waitcnt vmcnt(0)\n" \
"v_cmp_eq_u32_e64 s[98:99], s62, v37\n" \
"s_cmp_gt_u32 s101, 0x100000\n" \
"s_cbranch_scc1 L_rx2_%=\n" \
"s_andn2_b32 s75, 0x200, s98\n" \
"s_cbranch_scc1 L_sx2_%=\n" \
"s_branch L_rx2_%=\n" \
"L_sx3_%=:\n" \
"s_sleep 1\n" \
"buffer_load_dword v37, v40, s[56:59], s74 offen sc1\n" \
"s_add_u32 s101, s101, 1\n" \
"s_waitcnt vmcnt(0)\n" \
"v_cmp_eq_u32_e64 s[98:99], s62, v37\n" \
"s_cmp_gt_u32 s101, 0x100000\n" \
"s_cbranch_scc1 L_rx3_%=\n" \
"s_andn2_b32 s75, 0x400, s98\n" \
"s_cbranch_scc1 L_sx3_%=\n" \
"s_branch L_rx3_%=\n" \
"L_sx4_%=:\n" \
"s_sleep 1\n" \
"buffer_load_dword v37, v40, s[56:59], s74 offen sc1\n" \
"s_add_u32 s101, s101, 1\n" \
"s_waitcnt vmcnt(0)\n" \
"v_cmp_eq_u32_e64 s[98:99], s62, v37\n" \
"s_cmp_gt_u32 s101, 0x100000\n" \
"s_cbranch_scc1 L_rx4_%=\n" \
"s_andn2_b32 s75, 0x800, s98\n" \
"s_cbranch_scc1 L_sx4_%=\n" \
"s_branch L_rx4_%=\n" \
"L_sx5_%=:\n" \
"s_sleep 1\n" \
"buffer_load_dword v37, v40, s[56:59], s74 offen sc1\n" \
"s_add_u32 s101, s101, 1\n" \
"s_waitcnt vmcnt(0)\n" \
"v_cmp_eq_u32_e64 s[98:99], s62, v37\n" \
"s_cmp_gt_u32 s101, 0x100000\n" \
"s_cbranch_scc1 L_rx5_%=\n" \
"s_andn2_b32 s75, 0x1000, s98\n" \
"s_cbranch_scc1 L_sx5_%=\n" \
"s_branch L_rx5_%=\n" \
"L_sx6_%=:\n" \
"s_sleep 1\n" \
"buffer_load_dword v37, v40, s[56:59], s74 offen sc1\n" \
"s_add_u32 s101, s101, 1\n" \
"s_waitcnt vmcnt(0)\n" \
"v_cmp_eq_u32_e64 s[98:99], s62, v37\n" \
"s_cmp_gt_u32 s101, 0x100000\n" \
"s_cbranch_scc1 L_rx6_%=\n" \
"s_andn2_b32 s75, 0x2000, s98\n" \
"s_cbranch_scc1 L_sx6_%=\n" \
"s_branch L_rx6_%=\n" \
"L_sx7_%=:\n" \
"s_sleep 1\n" \
"buffer_load_dword v37, v40, s[56:59], s74 offen sc1\n" \
"s_add_u32 s101, s101, 1\n" \
"s_waitcnt vmcnt(0)\n" \
"v_cmp_eq_u32_e64 s[98:99], s62, v37\n" \
"s_cmp_gt_u32 s101, 0x100000\n" \
"s_cbranch_scc1 L_rx7_%=\n" \
"s_andn2_b32 s75, 0x4000, s98\n" \
"s_cbranch_scc1 L_sx7_%=\n" \
"s_branch L_rx7_%=\n" \
"L_att_stub:\n" \
"s_mov_b32 s101, 0\n" \
"v_add_u32_e32 v32, 0x49000, v20\n" \
"v_readlane_b32 s96, v11, 35\n" \
"v_readlane_b32 s97, v11, 36\n" \
"buffer_load_dwordx4 a[220:223], v20, s[56:59], s60 offen offset:0 sc1\n" \
"buffer_load_dwordx4 a[236:239], v32, s[56:59], s60 offen offset:0 sc1\n" \
"buffer_load_dwordx4 a[224:227], v20, s[56:59], s60 offen offset:1024 sc1\n" \
"buffer_load_dwordx4 a[240:243], v32, s[56:59], s60 offen offset:1024 sc1\n" \
"buffer_load_dwordx4 a[228:231], v20, s[56:59], s60 offen offset:2048 sc1\n" \
"buffer_load_dwordx4 a[244:247], v32, s[56:59], s60 offen offset:2048 sc1\n" \
"buffer_load_dwordx4 a[232:235], v20, s[56:59], s60 offen offset:3072 sc1\n" \
"buffer_load_dwordx4 a[248:251], v32, s[56:59], s60 offen offset:3072 sc1\n" \
"buffer_load_dwordx4 a[96:99], v20, s[56:59], s96 offen offset:0 sc1\n" \
"buffer_load_dwordx4 a[104:107], v32, s[56:59], s96 offen offset:0 sc1\n" \
"buffer_load_dwordx4 a[112:115], v20, s[56:59], s97 offen offset:0 sc1\n" \
"buffer_load_dwordx4 a[120:123], v32, s[56:59], s97 offen offset:0 sc1\n" \
"buffer_load_dwordx4 a[100:103], v20, s[56:59], s96 offen offset:1024 sc1\n" \
"buffer_load_dwordx4 a[108:111], v32, s[56:59], s96 offen offset:1024 sc1\n" \
"buffer_load_dwordx4 a[116:119], v20, s[56:59], s97 offen offset:1024 sc1\n" \
"buffer_load_dwordx4 a[124:127], v32, s[56:59], s97 offen offset:1024 sc1\n" \
"buffer_load_dwordx4 v[64:67], v20, s[56:59], s61 offen offset:0 sc1\n" \
"buffer_load_dwordx4 v[68:71], v20, s[56:59], s61 offen offset:1024 sc1\n" \
"buffer_load_dwordx4 v[72:75], v20, s[56:59], s61 offen offset:2048 sc1\n" \
"buffer_load_dwordx4 v[76:79], v20, s[56:59], s61 offen offset:3072 sc1\n" \
"buffer_load_dwordx4 v[80:83], v32, s[56:59], s61 offen offset:0 sc1\n" \
"buffer_load_dwordx4 v[84:87], v32, s[56:59], s61 offen offset:1024 sc1\n" \
"buffer_load_dwordx4 v[96:99], v32, s[56:59], s61 offen offset:2048 sc1\n" \
"buffer_load_dwordx4 v[100:103], v32, s[56:59], s61 offen offset:3072 sc1\n" \
"v_not_b32_e32 v26, s62\n" \
"s_waitcnt vmcnt(0)\n" \
"s_branch L_out_%=\n" \
".fill (L_ab + 12288 - .) / 4, 4, 0xBF800000\n" \
".fill (L_ab + 12288 - .) / 4, 4, 0xBF800000\n" \
"L_ap3:\n" \
"s_setpc_b64 s[88:89]\n" \
"L_sw_%=:\n" \
"v_mfma_f32_16x16x32_bf16 v[104:107], a[128:131], a[16:19], v[36:39]\n" \
"s_add_u32 s68, s60, 0xfff6e000\n" \
"s_add_u32 s69, s60, 0xfffb7000\n" \
"s_add_u32 s70, s60, 0x0\n" \
"s_add_u32 s71, s60, 0x49000\n" \
"v_mfma_f32_16x16x32_bf16 v[104:107], a[132:135], a[20:23], v[104:107]\n" \
"s_xor_b32 m0, s65, 0x12000\n" \
"ds_read_b128 a[160:163], v49 offset:0\n" \
"v_mfma_f32_16x16x32_bf16 v[104:107], a[136:139], a[24:27], v[104:107]\n" \
"buffer_load_dwordx4 v50, s[8:11], s64 offen offset:0 sc1 lds\n" \
"v_mfma_f32_16x16x32_bf16 v[104:107], a[140:143], a[28:31], v[104:107]\n" \
"ds_read_b128 a[164:167], v49 offset:1024\n" \
"ds_read_b128 a[168:171], v49 offset:2048\n" \
"v_mfma_f32_16x16x32_bf16 v[108:111], a[156:159], a[28:31], v[36:39]\n" \
"ds_read_b128 a[172:175], v49 offset:3072\n" \
"ds_read_b128 a[176:179], v49 offset:4096\n" \
"v_mfma_f32_16x16x32_bf16 v[108:111], a[152:155], a[24:27], v[108:111]\n" \
"buffer_load_dwordx4 v50, s[8:11], s64 offen offset:1024 sc1 lds\n" \
"v_mfma_f32_16x16x32_bf16 v[108:111], a[148:151], a[20:23], v[108:111]\n" \
"s_mov_b32 s65, s13\n" \
"v_add_u32_e32 v63, s15, v20\n" \
"v_mfma_f32_16x16x32_bf16 v[108:111], a[144:147], a[16:19], v[108:111]\n" \
"s_add_u32 s67, s14, 40960\n" \
"v_add_u32_e32 v50, 0x6000, v63\n" \
"v_mfma_f32_16x16x32_bf16 v[116:119], a[144:147], a[32:35], v[40:43]\n" \
"s_xor_b32 m0, s65, 0x8000\n" \
"ds_read_b128 a[180:183], v49 offset:5120\n" \
"v_mfma_f32_16x16x32_bf16 v[116:119], a[148:151], a[36:39], v[116:119]\n" \
"ds_read_b128 a[184:187], v49 offset:6144\n" \
"v_and_b32_e32 v68, v12, v68\n" \
"v_mfma_f32_16x16x32_bf16 v[116:119], a[152:155], a[40:43], v[116:119]\n" \
"v_and_b32_e32 v69, v13, v69\n" \
"v_and_b32_e32 v70, v14, v70\n" \
"v_mfma_f32_16x16x32_bf16 v[116:119], a[156:159], a[44:47], v[116:119]\n" \
"buffer_load_dwordx4 v50, s[8:11], s67 offen offset:0 sc1 lds\n" \
"v_mfma_f32_16x16x32_bf16 v[112:115], a[140:143], a[44:47], v[40:43]\n" \
"v_and_b32_e32 v71, v15, v71\n" \
"ds_read_b128 a[188:191], v49 offset:7168\n" \
"v_mfma_f32_16x16x32_bf16 v[112:115], a[136:139], a[40:43], v[112:115]\n" \
"v_exp_f32_e32 v86, v86\n" \
"v_mfma_f32_16x16x32_bf16 v[112:115], a[132:135], a[36:39], v[112:115]\n" \
"v_exp_f32_e32 v87, v87\n" \
"v_mfma_f32_16x16x32_bf16 v[112:115], a[128:131], a[32:35], v[112:115]\n" \
"v_exp_f32_e32 v84, v84\n" \
"v_mfma_f32_16x16x32_bf16 v[120:123], a[128:131], a[48:51], v[44:47]\n" \
"buffer_load_dwordx4 v50, s[8:11], s67 offen offset:1024 sc1 lds\n" \
"v_mfma_f32_16x16x32_bf16 v[120:123], a[132:135], a[52:55], v[120:123]\n" \
"v_exp_f32_e32 v85, v85\n" \
"v_mfma_f32_16x16x32_bf16 v[120:123], a[136:139], a[56:59], v[120:123]\n" \
"v_cvt_pk_bf16_f32 v87, v86, v87\n" \
"v_cvt_pk_bf16_f32 v86, v84, v85\n" \
"v_mfma_f32_16x16x32_bf16 v[120:123], a[140:143], a[60:63], v[120:123]\n" \
"v_exp_f32_e32 v82, v82\n" \
"v_mfma_f32_16x16x32_bf16 v[124:127], a[156:159], a[60:63], v[44:47]\n" \
"v_exp_f32_e32 v83, v83\n" \
"v_mfma_f32_16x16x32_bf16 v[124:127], a[152:155], a[56:59], v[124:127]\n" \
"v_exp_f32_e32 v80, v80\n" \
"v_mfma_f32_16x16x32_bf16 v[124:127], a[148:151], a[52:55], v[124:127]\n" \
"buffer_load_dwordx4 v50, s[8:11], s67 offen offset:2048 sc1 lds\n" \
"v_mfma_f32_16x16x32_bf16 v[124:127], a[144:147], a[48:51], v[124:127]\n" \
"v_cvt_pk_bf16_f32 v85, v82, v83\n" \
"s_waitcnt lgkmcnt(1)\n" \
"v_mfma_f32_16x16x32_bf16 v[128:131], a[160:163], v[68:71], v[128:131]\n" \
"v_exp_f32_e32 v81, v81\n" \
"v_mfma_f32_16x16x32_bf16 v[160:163], a[160:163], v[72:75], v[160:163]\n" \
"v_cvt_pk_bf16_f32 v84, v80, v81\n" \
"s_mov_b32 s64, s14\n" \
"v_mfma_f32_16x16x32_bf16 v[132:135], a[164:167], v[68:71], v[132:135]\n" \
"v_exp_f32_e32 v88, v88\n" \
"v_mfma_f32_16x16x32_bf16 v[164:167], a[164:167], v[72:75], v[164:167]\n" \
"v_exp_f32_e32 v89, v89\n" \
"v_mfma_f32_16x16x32_bf16 v[136:139], a[168:171], v[68:71], v[136:139]\n" \
"buffer_load_dwordx4 v50, s[8:11], s67 offen offset:3072 sc1 lds\n" \
"v_mfma_f32_16x16x32_bf16 v[168:171], a[168:171], v[72:75], v[168:171]\n" \
"v_exp_f32_e32 v90, v90\n" \
"v_mfma_f32_16x16x32_bf16 v[140:143], a[172:175], v[68:71], v[140:143]\n" \
"v_exp_f32_e32 v91, v91\n" \
"v_mfma_f32_16x16x32_bf16 v[172:175], a[172:175], v[72:75], v[172:175]\n" \
"v_cvt_pk_bf16_f32 v88, v88, v89\n" \
"v_cvt_pk_bf16_f32 v89, v90, v91\n" \
"v_mfma_f32_16x16x32_bf16 v[144:147], a[176:179], v[68:71], v[144:147]\n" \
"v_exp_f32_e32 v92, v92\n" \
"v_mfma_f32_16x16x32_bf16 v[176:179], a[176:179], v[72:75], v[176:179]\n" \
"v_exp_f32_e32 v93, v93\n" \
"v_mfma_f32_16x16x32_bf16 v[148:151], a[180:183], v[68:71], v[148:151]\n" \
"v_exp_f32_e32 v94, v94\n" \
"v_mfma_f32_16x16x32_bf16 v[180:183], a[180:183], v[72:75], v[180:183]\n" \
"v_exp_f32_e32 v95, v95\n" \
"v_mfma_f32_16x16x32_bf16 v[152:155], a[184:187], v[68:71], v[152:155]\n" \
"v_cvt_pk_bf16_f32 v90, v92, v93\n" \
"v_cvt_pk_bf16_f32 v91, v94, v95\n" \
"v_mfma_f32_16x16x32_bf16 v[184:187], a[184:187], v[72:75], v[184:187]\n" \
"v_add_u32_e32 v51, 0x12000, v63\n" \
"s_waitcnt lgkmcnt(0)\n" \
"v_mfma_f32_16x16x32_bf16 v[156:159], a[188:191], v[68:71], v[156:159]\n" \
"s_xor_b32 m0, s65, 0xa000\n" \
"ds_read_b32 v58, v18 offset:512\n" \
"v_mfma_f32_16x16x32_bf16 v[188:191], a[188:191], v[72:75], v[188:191]\n" \
"buffer_load_dwordx4 v51, s[8:11], s64 offen offset:0 sc1 lds\n" \
"v_smfmac_f32_16x16x64_bf16 a[192:195], a[200:203], v[68:75], v21\n" \
"buffer_load_dwordx4 v51, s[8:11], s64 offen offset:1024 sc1 lds\n" \
"v_mfma_f32_16x16x32_bf16 v[220:223], a[188:191], v[84:87], v[220:223]\n" \
"ds_read_b32 v59, v18 offset:640\n" \
"ds_read_b32 v60, v18 offset:768\n" \
"v_mfma_f32_16x16x32_bf16 v[252:255], a[188:191], v[88:91], v[252:255]\n" \
"ds_read_b32 v61, v18 offset:896\n" \
"ds_read_b128 a[128:131], v48 offset:0\n" \
"v_mfma_f32_16x16x32_bf16 v[216:219], a[184:187], v[84:87], v[216:219]\n" \
"ds_read_b128 a[144:147], v48 offset:4096\n" \
"ds_read_b128 a[132:135], v48 offset:1024\n" \
"v_mfma_f32_16x16x32_bf16 v[248:251], a[184:187], v[88:91], v[248:251]\n" \
"ds_read_b128 a[148:151], v48 offset:5120\n" \
"ds_read_b128 a[136:139], v48 offset:2048\n" \
"v_mfma_f32_16x16x32_bf16 v[212:215], a[180:183], v[84:87], v[212:215]\n" \
"ds_read_b128 a[152:155], v48 offset:6144\n" \
"ds_read_b128 a[140:143], v48 offset:3072\n" \
"v_mfma_f32_16x16x32_bf16 v[244:247], a[180:183], v[88:91], v[244:247]\n" \
"ds_read_b128 a[156:159], v48 offset:7168\n" \
"v_add_u32_e32 v52, 0x14000, v63\n" \
"v_mfma_f32_16x16x32_bf16 v[208:211], a[176:179], v[84:87], v[208:211]\n" \
"buffer_load_dwordx4 v51, s[8:11], s64 offen offset:2048 sc1 lds\n" \
"v_mfma_f32_16x16x32_bf16 v[240:243], a[176:179], v[88:91], v[240:243]\n" \
"v_exp_f32_e32 v104, v104\n" \
"v_mfma_f32_16x16x32_bf16 v[204:207], a[172:175], v[84:87], v[204:207]\n" \
"v_exp_f32_e32 v105, v105\n" \
"v_mfma_f32_16x16x32_bf16 v[236:239], a[172:175], v[88:91], v[236:239]\n" \
"buffer_load_dwordx4 v51, s[8:11], s64 offen offset:3072 sc1 lds\n" \
"v_mfma_f32_16x16x32_bf16 v[200:203], a[168:171], v[84:87], v[200:203]\n" \
"v_exp_f32_e32 v106, v106\n" \
"v_mfma_f32_16x16x32_bf16 v[232:235], a[168:171], v[88:91], v[232:235]\n" \
"v_exp_f32_e32 v107, v107\n" \
"v_mfma_f32_16x16x32_bf16 v[196:199], a[164:167], v[84:87], v[196:199]\n" \
"v_cvt_pk_bf16_f32 v104, v104, v105\n" \
"v_cvt_pk_bf16_f32 v105, v106, v107\n" \
"v_mfma_f32_16x16x32_bf16 v[228:231], a[164:167], v[88:91], v[228:231]\n" \
"v_exp_f32_e32 v108, v108\n" \
"v_mfma_f32_16x16x32_bf16 v[192:195], a[160:163], v[84:87], v[192:195]\n" \
"v_exp_f32_e32 v109, v109\n" \
"v_mfma_f32_16x16x32_bf16 v[224:227], a[160:163], v[88:91], v[224:227]\n" \
"v_exp_f32_e32 v110, v110\n" \
"v_smfmac_f32_16x16x64_bf16 a[196:199], a[200:203], v[84:91], v21\n" \
"v_exp_f32_e32 v111, v111\n" \
"v_cvt_pk_bf16_f32 v106, v108, v109\n" \
"v_cvt_pk_bf16_f32 v107, v110, v111\n" \
"v_and_b32_e32 v104, v12, v104\n" \
"v_and_b32_e32 v105, v13, v105\n" \
"v_and_b32_e32 v106, v14, v106\n" \
"v_and_b32_e32 v107, v15, v107\n" \
"v_accvgpr_read_b32 v54, a192\n" \
"v_accvgpr_read_b32 v22, a193\n" \
"v_add_u32_e32 v53, 0x20000, v63\n" \
"v_exp_f32_e32 v118, v118\n" \
"v_exp_f32_e32 v119, v119\n" \
"v_exp_f32_e32 v116, v116\n" \
"v_exp_f32_e32 v117, v117\n" \
"v_cvt_pk_bf16_f32 v119, v118, v119\n" \
"v_exp_f32_e32 v114, v114\n" \
"v_exp_f32_e32 v115, v115\n" \
"v_exp_f32_e32 v112, v112\n" \
"v_exp_f32_e32 v113, v113\n" \
"v_cvt_pk_bf16_f32 v118, v116, v117\n" \
"v_cvt_pk_bf16_f32 v117, v114, v115\n" \
"v_cvt_pk_bf16_f32 v116, v112, v113\n" \
"v_readlane_b32 s93, v11, 19\n" \
"s_waitcnt vmcnt(10) lgkmcnt(0)\n" \
"s_barrier\n" \
"v_mfma_f32_16x16x32_bf16 v[80:83], a[128:131], a[32:35], v[40:43]\n" \
"ds_read_b128 a[160:163], v49 offset:8192\n" \
"ds_read_b128 a[164:167], v49 offset:9216\n" \
"v_mfma_f32_16x16x32_bf16 v[80:83], a[132:135], a[36:39], v[80:83]\n" \
"ds_read_b128 a[168:171], v49 offset:10240\n" \
"s_xor_b32 m0, s65, 0x18000\n" \
"v_mfma_f32_16x16x32_bf16 v[80:83], a[136:139], a[40:43], v[80:83]\n" \
"buffer_load_dwordx4 v52, s[8:11], s64 offen offset:0 sc1 lds\n" \
"v_mfma_f32_16x16x32_bf16 v[80:83], a[140:143], a[44:47], v[80:83]\n" \
"ds_read_b128 a[172:175], v49 offset:11264\n" \
"ds_read_b128 a[176:179], v49 offset:12288\n" \
"v_mfma_f32_16x16x32_bf16 v[84:87], a[156:159], a[44:47], v[40:43]\n" \
"ds_read_b128 a[180:183], v49 offset:13312\n" \
"ds_read_b128 a[184:187], v49 offset:14336\n" \
"v_mfma_f32_16x16x32_bf16 v[84:87], a[152:155], a[40:43], v[84:87]\n" \
"buffer_load_dwordx4 v52, s[8:11], s64 offen offset:1024 sc1 lds\n" \
"v_mfma_f32_16x16x32_bf16 v[84:87], a[148:151], a[36:39], v[84:87]\n" \
"ds_read_b128 a[188:191], v49 offset:15360\n" \
"v_readlane_b32 s80, v11, 11\n" \
"v_mfma_f32_16x16x32_bf16 v[84:87], a[144:147], a[32:35], v[84:87]\n" \
"buffer_load_dwordx4 v52, s[8:11], s64 offen offset:2048 sc1 lds\n" \
"v_mfma_f32_16x16x32_bf16 v[92:95], a[144:147], a[48:51], v[44:47]\n" \
"v_exp_f32_e32 v120, v120\n" \
"v_mfma_f32_16x16x32_bf16 v[92:95], a[148:151], a[52:55], v[92:95]\n" \
"v_exp_f32_e32 v121, v121\n" \
"v_mfma_f32_16x16x32_bf16 v[92:95], a[152:155], a[56:59], v[92:95]\n" \
"buffer_load_dwordx4 v52, s[8:11], s64 offen offset:3072 sc1 lds\n" \
"v_mfma_f32_16x16x32_bf16 v[92:95], a[156:159], a[60:63], v[92:95]\n" \
"s_mov_b32 m0, s65\n" \
"v_cvt_pk_bf16_f32 v120, v120, v121\n" \
"v_mfma_f32_16x16x32_bf16 v[88:91], a[140:143], a[60:63], v[44:47]\n" \
"v_exp_f32_e32 v122, v122\n" \
"v_mfma_f32_16x16x32_bf16 v[88:91], a[136:139], a[56:59], v[88:91]\n" \
"buffer_load_dwordx4 v53, s[8:11], s64 offen offset:0 sc1 lds\n" \
"v_mfma_f32_16x16x32_bf16 v[88:91], a[132:135], a[52:55], v[88:91]\n" \
"v_exp_f32_e32 v123, v123\n" \
"v_mfma_f32_16x16x32_bf16 v[88:91], a[128:131], a[48:51], v[88:91]\n" \
"v_exp_f32_e32 v124, v124\n" \
"v_mfma_f32_16x16x32_bf16 a[192:195], a[212:215], v[104:107], a[192:195]\n" \
"v_cvt_pk_bf16_f32 v121, v122, v123\n" \
"s_waitcnt lgkmcnt(5)\n" \
"v_mfma_f32_16x16x32_bf16 v[160:163], a[160:163], v[104:107], v[160:163]\n" \
"buffer_load_dwordx4 v53, s[8:11], s64 offen offset:1024 sc1 lds\n" \
"v_mfma_f32_16x16x32_bf16 v[164:167], a[164:167], v[104:107], v[164:167]\n" \
"v_exp_f32_e32 v125, v125\n" \
"v_mfma_f32_16x16x32_bf16 v[168:171], a[168:171], v[104:107], v[168:171]\n" \
"v_cvt_pk_bf16_f32 v122, v124, v125\n" \
"s_waitcnt lgkmcnt(1)\n" \
"v_mfma_f32_16x16x32_bf16 v[172:175], a[172:175], v[104:107], v[172:175]\n" \
"v_exp_f32_e32 v126, v126\n" \
"v_mfma_f32_16x16x32_bf16 v[176:179], a[176:179], v[104:107], v[176:179]\n" \
"v_exp_f32_e32 v127, v127\n" \
"v_mfma_f32_16x16x32_bf16 v[180:183], a[180:183], v[104:107], v[180:183]\n" \
"v_cvt_pk_bf16_f32 v123, v126, v127\n" \
"v_readlane_b32 s81, v11, 12\n" \
"v_mfma_f32_16x16x32_bf16 v[184:187], a[184:187], v[104:107], v[184:187]\n" \
"s_mov_b32 s82, 0x47fca0\n" \
"s_waitcnt lgkmcnt(0)\n" \
"v_mfma_f32_16x16x32_bf16 v[188:191], a[188:191], v[104:107], v[188:191]\n" \
"buffer_load_dwordx4 v53, s[8:11], s64 offen offset:2048 sc1 lds\n" \
"v_mfma_f32_16x16x32_bf16 v[220:223], a[188:191], v[116:119], v[220:223]\n" \
"buffer_load_dwordx4 v53, s[8:11], s64 offen offset:3072 sc1 lds\n" \
"v_mfma_f32_16x16x32_bf16 v[252:255], a[188:191], v[120:123], v[252:255]\n" \
"s_mov_b32 s83, 0x20000\n" \
"v_readlane_b32 s92, v11, 25\n" \
"v_mfma_f32_16x16x32_bf16 v[216:219], a[184:187], v[116:119], v[216:219]\n" \
"s_add_u32 m0, s93, 0x0\n" \
"ds_read_b128 a[128:131], v48 offset:8192\n" \
"v_mfma_f32_16x16x32_bf16 v[248:251], a[184:187], v[120:123], v[248:251]\n" \
"ds_read_b128 a[144:147], v48 offset:12288\n" \
"ds_read_b128 a[132:135], v48 offset:9216\n" \
"v_mfma_f32_16x16x32_bf16 v[212:215], a[180:183], v[116:119], v[212:215]\n" \
"buffer_load_dwordx4 v0, s[80:83], s92 offen offset:0 lds\n" \
"v_mfma_f32_16x16x32_bf16 v[244:247], a[180:183], v[120:123], v[244:247]\n" \
"buffer_load_dwordx4 v1, s[80:83], s92 offen offset:1024 lds\n" \
"v_mfma_f32_16x16x32_bf16 v[208:211], a[176:179], v[116:119], v[208:211]\n" \
"ds_read_b128 a[148:151], v48 offset:13312\n" \
"ds_read_b128 a[136:139], v48 offset:10240\n" \
"v_mfma_f32_16x16x32_bf16 v[240:243], a[176:179], v[120:123], v[240:243]\n" \
"ds_read_b128 a[152:155], v48 offset:14336\n" \
"ds_read_b128 a[140:143], v48 offset:11264\n" \
"v_mfma_f32_16x16x32_bf16 v[204:207], a[172:175], v[116:119], v[204:207]\n" \
"buffer_load_dwordx4 v2, s[80:83], s92 offen offset:2048 lds\n" \
"v_mfma_f32_16x16x32_bf16 v[236:239], a[172:175], v[120:123], v[236:239]\n" \
"ds_read_b128 a[156:159], v48 offset:15360\n" \
"s_xor_b32 s65, s65, 0x8000\n" \
"v_mfma_f32_16x16x32_bf16 v[200:203], a[168:171], v[116:119], v[200:203]\n" \
"s_add_u32 s64, s64, 0x10000\n" \
"v_add_u32_e32 v49, 0x10000, v48\n" \
"v_mfma_f32_16x16x32_bf16 v[232:235], a[168:171], v[120:123], v[232:235]\n" \
"buffer_load_dwordx4 v3, s[80:83], s92 offen offset:3072 lds\n" \
"v_mfma_f32_16x16x32_bf16 v[196:199], a[164:167], v[116:119], v[196:199]\n" \
"v_exp_f32_e32 v86, v86\n" \
"v_mfma_f32_16x16x32_bf16 v[228:231], a[164:167], v[120:123], v[228:231]\n" \
"v_exp_f32_e32 v87, v87\n" \
"v_mfma_f32_16x16x32_bf16 v[192:195], a[160:163], v[116:119], v[192:195]\n" \
"v_exp_f32_e32 v84, v84\n" \
"v_mfma_f32_16x16x32_bf16 v[224:227], a[160:163], v[120:123], v[224:227]\n" \
"v_exp_f32_e32 v85, v85\n" \
"v_smfmac_f32_16x16x64_bf16 a[196:199], a[200:203], v[116:123], v21\n" \
"v_exp_f32_e32 v88, v88\n" \
"v_exp_f32_e32 v89, v89\n" \
"v_cvt_pk_bf16_f32 v87, v86, v87\n" \
"v_exp_f32_e32 v82, v82\n" \
"v_exp_f32_e32 v83, v83\n" \
"v_exp_f32_e32 v80, v80\n" \
"v_exp_f32_e32 v81, v81\n" \
"v_exp_f32_e32 v90, v90\n" \
"v_exp_f32_e32 v91, v91\n" \
"v_cvt_pk_bf16_f32 v86, v84, v85\n" \
"v_cvt_pk_bf16_f32 v88, v88, v89\n" \
"v_exp_f32_e32 v92, v92\n" \
"v_exp_f32_e32 v93, v93\n" \
"v_exp_f32_e32 v94, v94\n" \
"v_exp_f32_e32 v95, v95\n" \
"v_cvt_pk_bf16_f32 v85, v82, v83\n" \
"v_cvt_pk_bf16_f32 v84, v80, v81\n" \
"v_cvt_pk_bf16_f32 v89, v90, v91\n" \
"v_and_b32_e32 v84, v12, v84\n" \
"v_and_b32_e32 v85, v13, v85\n" \
"v_and_b32_e32 v86, v14, v86\n" \
"v_and_b32_e32 v87, v15, v87\n" \
"v_cvt_pk_bf16_f32 v90, v92, v93\n" \
"v_cvt_pk_bf16_f32 v91, v94, v95\n" \
"v_add_u32_e32 v48, s16, v20\n" \
"v_add_f32_e32 v54, v54, v22\n" \
"v_accvgpr_read_b32 v55, a194\n" \
"v_accvgpr_read_b32 v23, a195\n" \
"v_mfma_f32_16x16x32_bf16 a[192:195], a[216:219], a[216:219], 0\n" \
"v_max_f32_e32 v62, v62, v54\n" \
"v_add_f32_e32 v55, v55, v23\n" \
"v_max_f32_e32 v62, v62, v55\n" \
"v_rcp_f32_e32 v54, v54\n" \
"v_rcp_f32_e32 v55, v55\n" \
"v_mul_f32_e32 v54, v54, v58\n" \
"v_mul_f32_e32 v55, v55, v59\n" \
"v_mul_f32_e32 v128, v54, v128\n" \
"v_mul_f32_e32 v129, v54, v129\n" \
"v_mul_f32_e32 v136, v54, v136\n" \
"v_mul_f32_e32 v137, v54, v137\n" \
"v_mul_f32_e32 v144, v54, v144\n" \
"v_mul_f32_e32 v145, v54, v145\n" \
"v_mul_f32_e32 v152, v54, v152\n" \
"v_mul_f32_e32 v153, v54, v153\n" \
"v_mul_f32_e32 v160, v55, v160\n" \
"v_mul_f32_e32 v161, v55, v161\n" \
"v_mul_f32_e32 v168, v55, v168\n" \
"v_mul_f32_e32 v169, v55, v169\n" \
"v_mul_f32_e32 v176, v55, v176\n" \
"v_mul_f32_e32 v177, v55, v177\n" \
"v_mul_f32_e32 v184, v55, v184\n" \
"v_mul_f32_e32 v185, v55, v185\n" \
"v_cvt_pk_bf16_f32 v128, v128, v129\n" \
"v_mul_f32_e32 v130, v54, v130\n" \
"v_mul_f32_e32 v131, v54, v131\n" \
"v_cvt_pk_bf16_f32 v136, v136, v137\n" \
"v_mul_f32_e32 v138, v54, v138\n" \
"v_mul_f32_e32 v139, v54, v139\n" \
"v_cvt_pk_bf16_f32 v144, v144, v145\n" \
"v_mul_f32_e32 v146, v54, v146\n" \
"v_mul_f32_e32 v147, v54, v147\n" \
"v_cvt_pk_bf16_f32 v152, v152, v153\n" \
"v_mul_f32_e32 v154, v54, v154\n" \
"v_mul_f32_e32 v155, v54, v155\n" \
"v_cvt_pk_bf16_f32 v160, v160, v161\n" \
"v_mul_f32_e32 v162, v55, v162\n" \
"v_mul_f32_e32 v163, v55, v163\n" \
"v_cvt_pk_bf16_f32 v168, v168, v169\n" \
"v_mul_f32_e32 v170, v55, v170\n" \
"v_mul_f32_e32 v171, v55, v171\n" \
"v_cvt_pk_bf16_f32 v176, v176, v177\n" \
"v_mul_f32_e32 v178, v55, v178\n" \
"v_mul_f32_e32 v179, v55, v179\n" \
"v_cvt_pk_bf16_f32 v184, v184, v185\n" \
"v_mul_f32_e32 v186, v55, v186\n" \
"v_mul_f32_e32 v187, v55, v187\n" \
"v_cvt_pk_bf16_f32 v129, v130, v131\n" \
"v_mul_f32_e32 v132, v54, v132\n" \
"v_mul_f32_e32 v133, v54, v133\n" \
"v_mul_f32_e32 v134, v54, v134\n" \
"v_mul_f32_e32 v135, v54, v135\n" \
"v_cvt_pk_bf16_f32 v137, v138, v139\n" \
"v_mul_f32_e32 v140, v54, v140\n" \
"s_waitcnt vmcnt(12) lgkmcnt(0)\n" \
"s_barrier\n" \
"v_mfma_f32_16x16x32_bf16 v[120:123], a[128:131], a[48:51], v[44:47]\n" \
"s_xor_b32 m0, s65, 0x12000\n" \
"ds_read_b128 a[188:191], v49 offset:7168\n" \
"v_mfma_f32_16x16x32_bf16 v[120:123], a[132:135], a[52:55], v[120:123]\n" \
"buffer_load_dwordx4 v50, s[8:11], s64 offen offset:0 sc1 lds\n" \
"v_mfma_f32_16x16x32_bf16 v[120:123], a[136:139], a[56:59], v[120:123]\n" \
"ds_read_b128 a[184:187], v49 offset:6144\n" \
"ds_read_b128 a[180:183], v49 offset:5120\n" \
"v_mfma_f32_16x16x32_bf16 v[120:123], a[140:143], a[60:63], v[120:123]\n" \
"buffer_load_dwordx4 v50, s[8:11], s64 offen offset:1024 sc1 lds\n" \
"v_mfma_f32_16x16x32_bf16 v[124:127], a[156:159], a[60:63], v[44:47]\n" \
"ds_read_b128 a[176:179], v49 offset:4096\n" \
"ds_read_b128 a[172:175], v49 offset:3072\n" \
"v_mfma_f32_16x16x32_bf16 v[124:127], a[152:155], a[56:59], v[124:127]\n" \
"ds_read_b128 a[168:171], v49 offset:2048\n" \
"ds_read_b128 a[164:167], v49 offset:1024\n" \
"v_mfma_f32_16x16x32_bf16 v[124:127], a[148:151], a[52:55], v[124:127]\n" \
"ds_read_b128 a[160:163], v49 offset:0\n" \
"ds_read_b128 a[0:3], v49 offset:8192\n" \
"v_mfma_f32_16x16x32_bf16 v[124:127], a[144:147], a[48:51], v[124:127]\n" \
"buffer_load_dwordx4 v50, s[8:11], s64 offen offset:2048 sc1 lds\n" \
"v_mfma_f32_16x16x32_bf16 v[32:35], a[220:223], a[64:67], 0\n" \
"ds_read_b128 a[4:7], v49 offset:9216\n" \
"ds_read_b128 a[8:11], v49 offset:10240\n" \
"v_mfma_f32_16x16x32_bf16 v[36:39], a[220:223], a[80:83], 0\n" \
"ds_read_b128 a[12:15], v49 offset:11264\n" \
"ds_read_b128 a[16:19], v49 offset:12288\n" \
"v_mfma_f32_16x16x32_bf16 v[40:43], a[220:223], a[96:99], 0\n" \
"ds_read_b128 a[20:23], v49 offset:13312\n" \
"ds_read_b128 a[24:27], v49 offset:14336\n" \
"v_mfma_f32_16x16x32_bf16 v[44:47], a[220:223], a[112:115], 0\n" \
"buffer_load_dwordx4 v50, s[8:11], s64 offen offset:3072 sc1 lds\n" \
"v_mfma_f32_16x16x32_bf16 v[32:35], a[224:227], a[68:71], v[32:35]\n" \
"s_xor_b32 m0, s65, 0xa000\n" \
"ds_read_b128 a[28:31], v49 offset:15360\n" \
"v_mfma_f32_16x16x32_bf16 v[36:39], a[224:227], a[84:87], v[36:39]\n" \
"v_exp_f32_e32 v120, v120\n" \
"v_mfma_f32_16x16x32_bf16 v[40:43], a[224:227], a[100:103], v[40:43]\n" \
"buffer_load_dwordx4 v51, s[8:11], s64 offen offset:0 sc1 lds\n" \
"v_mfma_f32_16x16x32_bf16 v[44:47], a[224:227], a[116:119], v[44:47]\n" \
"v_exp_f32_e32 v121, v121\n" \
"v_mfma_f32_16x16x32_bf16 v[32:35], a[228:231], a[72:75], v[32:35]\n" \
"v_exp_f32_e32 v122, v122\n" \
"v_mfma_f32_16x16x32_bf16 v[36:39], a[228:231], a[88:91], v[36:39]\n" \
"v_exp_f32_e32 v123, v123\n" \
"v_mfma_f32_16x16x32_bf16 v[40:43], a[228:231], a[104:107], v[40:43]\n" \
"buffer_load_dwordx4 v51, s[8:11], s64 offen offset:1024 sc1 lds\n" \
"v_mfma_f32_16x16x32_bf16 v[44:47], a[228:231], a[120:123], v[44:47]\n" \
"v_cvt_pk_bf16_f32 v120, v120, v121\n" \
"v_cvt_pk_bf16_f32 v121, v122, v123\n" \
"v_mfma_f32_16x16x32_bf16 v[32:35], a[232:235], a[76:79], v[32:35]\n" \
"v_exp_f32_e32 v124, v124\n" \
"v_mfma_f32_16x16x32_bf16 v[36:39], a[232:235], a[92:95], v[36:39]\n" \
"v_exp_f32_e32 v125, v125\n" \
"v_mfma_f32_16x16x32_bf16 v[40:43], a[232:235], a[108:111], v[40:43]\n" \
"buffer_load_dwordx4 v51, s[8:11], s64 offen offset:2048 sc1 lds\n" \
"v_mfma_f32_16x16x32_bf16 v[44:47], a[232:235], a[124:127], v[44:47]\n" \
"v_exp_f32_e32 v126, v126\n" \
"v_smfmac_f32_16x16x64_bf16 a[196:199], a[200:203], v[84:91], v21\n" \
"v_cvt_pk_bf16_f32 v122, v124, v125\n" \
"s_waitcnt lgkmcnt(1)\n" \
"v_mfma_f32_16x16x32_bf16 v[220:223], a[188:191], v[84:87], v[220:223]\n" \
"buffer_load_dwordx4 v51, s[8:11], s64 offen offset:3072 sc1 lds\n" \
"v_mfma_f32_16x16x32_bf16 v[252:255], a[188:191], v[88:91], v[252:255]\n" \
"s_add_u32 m0, s93, 0x1000\n" \
"v_and_b32_e32 v120, v12, v120\n" \
"v_mfma_f32_16x16x32_bf16 v[216:219], a[184:187], v[84:87], v[216:219]\n" \
"v_exp_f32_e32 v127, v127\n" \
"v_mfma_f32_16x16x32_bf16 v[248:251], a[184:187], v[88:91], v[248:251]\n" \
"v_cvt_pk_bf16_f32 v123, v126, v127\n" \
"v_and_b32_e32 v121, v13, v121\n" \
"v_mfma_f32_16x16x32_bf16 v[212:215], a[180:183], v[84:87], v[212:215]\n" \
"buffer_load_dwordx4 v4, s[80:83], s92 offen offset:0 lds\n" \
"v_mfma_f32_16x16x32_bf16 v[244:247], a[180:183], v[88:91], v[244:247]\n" \
"v_and_b32_e32 v122, v14, v122\n" \
"v_and_b32_e32 v123, v15, v123\n" \
"v_mfma_f32_16x16x32_bf16 v[208:211], a[176:179], v[84:87], v[208:211]\n" \
"v_accvgpr_read_b32 v56, a196\n" \
"v_accvgpr_read_b32 v24, a197\n" \
"v_mfma_f32_16x16x32_bf16 v[240:243], a[176:179], v[88:91], v[240:243]\n" \
"ds_read_b128 a[128:131], v48 offset:0\n" \
"ds_read_b128 a[144:147], v48 offset:4096\n" \
"v_mfma_f32_16x16x32_bf16 v[204:207], a[172:175], v[84:87], v[204:207]\n" \
"buffer_load_dwordx4 v5, s[80:83], s92 offen offset:1024 lds\n" \
"v_mfma_f32_16x16x32_bf16 v[236:239], a[172:175], v[88:91], v[236:239]\n" \
"ds_read_b128 a[132:135], v48 offset:1024\n" \
"ds_read_b128 a[148:151], v48 offset:5120\n" \
"v_mfma_f32_16x16x32_bf16 v[200:203], a[168:171], v[84:87], v[200:203]\n" \
"ds_read_b128 a[136:139], v48 offset:2048\n" \
"ds_read_b128 a[152:155], v48 offset:6144\n" \
"v_mfma_f32_16x16x32_bf16 v[232:235], a[168:171], v[88:91], v[232:235]\n" \
"buffer_load_dwordx4 v6, s[80:83], s92 offen offset:2048 lds\n" \
"v_mfma_f32_16x16x32_bf16 v[196:199], a[164:167], v[84:87], v[196:199]\n" \
"ds_read_b128 a[140:143], v48 offset:3072\n" \
"ds_read_b128 a[156:159], v48 offset:7168\n" \
"v_mfma_f32_16x16x32_bf16 v[228:231], a[164:167], v[88:91], v[228:231]\n" \
"v_mul_f32_e32 v141, v54, v141\n" \
"v_mul_f32_e32 v142, v54, v142\n" \
"v_mfma_f32_16x16x32_bf16 v[192:195], a[160:163], v[84:87], v[192:195]\n" \
"v_mul_f32_e32 v143, v54, v143\n" \
"v_cvt_pk_bf16_f32 v145, v146, v147\n" \
"v_mfma_f32_16x16x32_bf16 v[224:227], a[160:163], v[88:91], v[224:227]\n" \
"buffer_load_dwordx4 v7, s[80:83], s92 offen offset:3072 lds\n" \
"v_mfma_f32_16x16x32_bf16 a[196:199], a[212:215], v[120:123], a[196:199]\n" \
"v_mul_f32_e32 v148, v54, v148\n" \
"v_mul_f32_e32 v149, v54, v149\n" \
"v_mfma_f32_16x16x32_bf16 v[224:227], a[0:3], v[120:123], v[224:227]\n" \
"v_mul_f32_e32 v150, v54, v150\n" \
"v_mul_f32_e32 v151, v54, v151\n" \
"v_mfma_f32_16x16x32_bf16 v[228:231], a[4:7], v[120:123], v[228:231]\n" \
"v_cvt_pk_bf16_f32 v153, v154, v155\n" \
"v_mul_f32_e32 v156, v54, v156\n" \
"v_mfma_f32_16x16x32_bf16 v[232:235], a[8:11], v[120:123], v[232:235]\n" \
"v_mul_f32_e32 v157, v54, v157\n" \
"v_mul_f32_e32 v158, v54, v158\n" \
"v_mfma_f32_16x16x32_bf16 v[236:239], a[12:15], v[120:123], v[236:239]\n" \
"v_mul_f32_e32 v159, v54, v159\n" \
"v_cvt_pk_bf16_f32 v161, v162, v163\n" \
"v_mfma_f32_16x16x32_bf16 v[240:243], a[16:19], v[120:123], v[240:243]\n" \
"v_mul_f32_e32 v164, v55, v164\n" \
"v_mul_f32_e32 v165, v55, v165\n" \
"v_mfma_f32_16x16x32_bf16 v[244:247], a[20:23], v[120:123], v[244:247]\n" \
"v_mul_f32_e32 v166, v55, v166\n" \
"v_mul_f32_e32 v167, v55, v167\n" \
"v_mfma_f32_16x16x32_bf16 v[248:251], a[24:27], v[120:123], v[248:251]\n" \
"v_cvt_pk_bf16_f32 v169, v170, v171\n" \
"s_waitcnt lgkmcnt(4)\n" \
"v_mfma_f32_16x16x32_bf16 v[252:255], a[28:31], v[120:123], v[252:255]\n" \
"v_mul_f32_e32 v172, v55, v172\n" \
"v_mul_f32_e32 v173, v55, v173\n" \
"v_mul_f32_e32 v174, v55, v174\n" \
"v_mul_f32_e32 v175, v55, v175\n" \
"v_cvt_pk_bf16_f32 v177, v178, v179\n" \
"v_mul_f32_e32 v180, v55, v180\n" \
"v_mul_f32_e32 v181, v55, v181\n" \
"v_mul_f32_e32 v182, v55, v182\n" \
"v_mul_f32_e32 v183, v55, v183\n" \
"v_cvt_pk_bf16_f32 v185, v186, v187\n" \
"v_mul_f32_e32 v188, v55, v188\n" \
"v_mul_f32_e32 v189, v55, v189\n" \
"v_mul_f32_e32 v190, v55, v190\n" \
"v_mul_f32_e32 v191, v55, v191\n" \
"v_cvt_pk_bf16_f32 v130, v132, v133\n" \
"v_cvt_pk_bf16_f32 v131, v134, v135\n" \
"buffer_store_dwordx4 v[128:131], v20, s[56:59], s68 offen offset:0 sc1\n" \
"v_cvt_pk_bf16_f32 v138, v140, v141\n" \
"v_cvt_pk_bf16_f32 v139, v142, v143\n" \
"buffer_store_dwordx4 v[136:139], v20, s[56:59], s68 offen offset:1024 sc1\n" \
"v_cvt_pk_bf16_f32 v146, v148, v149\n" \
"v_cvt_pk_bf16_f32 v147, v150, v151\n" \
"buffer_store_dwordx4 v[144:147], v20, s[56:59], s68 offen offset:2048 sc1\n" \
"v_cvt_pk_bf16_f32 v154, v156, v157\n" \
"v_cvt_pk_bf16_f32 v155, v158, v159\n" \
"buffer_store_dwordx4 v[152:155], v20, s[56:59], s68 offen offset:3072 sc1\n" \
"v_cvt_pk_bf16_f32 v162, v164, v165\n" \
"v_cvt_pk_bf16_f32 v163, v166, v167\n" \
"buffer_store_dwordx4 v[160:163], v20, s[56:59], s69 offen offset:0 sc1\n" \
"v_cvt_pk_bf16_f32 v170, v172, v173\n" \
"v_cvt_pk_bf16_f32 v171, v174, v175\n" \
"buffer_store_dwordx4 v[168:171], v20, s[56:59], s69 offen offset:1024 sc1\n" \
"v_cvt_pk_bf16_f32 v178, v180, v181\n" \
"v_cvt_pk_bf16_f32 v179, v182, v183\n" \
"buffer_store_dwordx4 v[176:179], v20, s[56:59], s69 offen offset:2048 sc1\n" \
"v_cvt_pk_bf16_f32 v186, v188, v189\n" \
"v_cvt_pk_bf16_f32 v187, v190, v191\n" \
"buffer_store_dwordx4 v[184:187], v20, s[56:59], s69 offen offset:3072 sc1\n" \
"s_add_u32 m0, s93, 0x2000\n" \
"v_mfma_f32_16x16x32_bf16 a[208:211], a[216:219], v[128:131], a[208:211]\n" \
"buffer_load_dwordx4 v8, s[80:83], s92 offen offset:0 lds\n" \
"v_mfma_f32_16x16x32_bf16 a[208:211], a[216:219], v[136:139], a[208:211]\n" \
"v_mfma_f32_32x32x16_bf16 v[128:143], a[216:219], a[216:219], 0\n" \
"v_mfma_f32_16x16x32_bf16 a[208:211], a[216:219], v[144:147], a[208:211]\n" \
"v_mfma_f32_16x16x32_bf16 a[208:211], a[216:219], v[152:155], a[208:211]\n" \
"v_mfma_f32_32x32x16_bf16 v[144:159], a[216:219], a[216:219], 0\n" \
"v_add_f32_e32 v56, v56, v24\n" \
"v_mfma_f32_16x16x32_bf16 a[208:211], a[216:219], v[160:163], a[208:211]\n" \
"v_max_f32_e32 v62, v62, v56\n" \
"v_accvgpr_read_b32 v57, a198\n" \
"s_waitcnt vmcnt(21) lgkmcnt(0)\n" \
"s_barrier\n" \
"v_mfma_f32_16x16x32_bf16 v[64:67], a[128:131], a[64:67], v[32:35]\n" \
"s_xor_b32 m0, s65, 0x18000\n" \
"v_accvgpr_read_b32 v25, a199\n" \
"v_mfma_f32_16x16x32_bf16 a[196:199], a[216:219], a[216:219], 0\n" \
"buffer_load_dwordx4 v52, s[8:11], s64 offen offset:0 sc1 lds\n" \
"v_mfma_f32_16x16x32_bf16 v[64:67], a[132:135], a[68:71], v[64:67]\n" \
"v_rcp_f32_e32 v56, v56\n" \
"v_mfma_f32_16x16x32_bf16 v[64:67], a[136:139], a[72:75], v[64:67]\n" \
"v_mfma_f32_16x16x32_bf16 a[208:211], a[216:219], v[168:171], a[208:211]\n" \
"v_mfma_f32_32x32x16_bf16 v[160:175], a[216:219], a[216:219], 0\n" \
"v_mul_f32_e32 v56, v56, v60\n" \
"v_mfma_f32_16x16x32_bf16 a[208:211], a[216:219], v[176:179], a[208:211]\n" \
"v_mul_f32_e32 v192, v56, v192\n" \
"v_mul_f32_e32 v193, v56, v193\n" \
"v_mfma_f32_16x16x32_bf16 v[64:67], a[140:143], a[76:79], v[64:67]\n" \
"buffer_load_dwordx4 v52, s[8:11], s64 offen offset:1024 sc1 lds\n" \
"v_mfma_f32_16x16x32_bf16 v[68:71], a[156:159], a[76:79], v[32:35]\n" \
"v_cvt_pk_bf16_f32 v192, v192, v193\n" \
"v_mul_f32_e32 v194, v56, v194\n" \
"v_mfma_f32_16x16x32_bf16 v[68:71], a[152:155], a[72:75], v[68:71]\n" \
"v_mul_f32_e32 v195, v56, v195\n" \
"v_cvt_pk_bf16_f32 v193, v194, v195\n" \
"v_mfma_f32_16x16x32_bf16 v[68:71], a[148:151], a[68:71], v[68:71]\n" \
"v_mfma_f32_16x16x32_bf16 a[208:211], a[216:219], v[184:187], a[208:211]\n" \
"v_mfma_f32_32x32x16_bf16 v[176:191], a[216:219], a[216:219], 0\n" \
"v_mul_f32_e32 v196, v56, v196\n" \
"v_mul_f32_e32 v197, v56, v197\n" \
"v_mul_f32_e32 v198, v56, v198\n" \
"v_mul_f32_e32 v199, v56, v199\n" \
"v_mul_f32_e32 v200, v56, v200\n" \
"v_mul_f32_e32 v201, v56, v201\n" \
"v_mfma_f32_16x16x32_bf16 v[68:71], a[144:147], a[64:67], v[68:71]\n" \
"buffer_load_dwordx4 v52, s[8:11], s64 offen offset:2048 sc1 lds\n" \
"v_mfma_f32_16x16x32_bf16 v[76:79], a[144:147], a[80:83], v[36:39]\n" \
"v_cvt_pk_bf16_f32 v194, v196, v197\n" \
"v_cvt_pk_bf16_f32 v195, v198, v199\n" \
"buffer_store_dwordx4 v[192:195], v20, s[56:59], s70 offen offset:0 sc1\n" \
"v_mfma_f32_16x16x32_bf16 v[76:79], a[148:151], a[84:87], v[76:79]\n" \
"v_cvt_pk_bf16_f32 v200, v200, v201\n" \
"v_mul_f32_e32 v202, v56, v202\n" \
"v_mfma_f32_16x16x32_bf16 v[76:79], a[152:155], a[88:91], v[76:79]\n" \
"v_mul_f32_e32 v203, v56, v203\n" \
"v_add_f32_e32 v57, v57, v25\n" \
"v_mfma_f32_16x16x32_bf16 v[76:79], a[156:159], a[92:95], v[76:79]\n" \
"buffer_load_dwordx4 v52, s[8:11], s64 offen offset:3072 sc1 lds\n" \
"v_mfma_f32_16x16x32_bf16 v[72:75], a[140:143], a[92:95], v[36:39]\n" \
"s_mov_b32 m0, s65\n" \
"v_cvt_pk_bf16_f32 v201, v202, v203\n" \
"v_mfma_f32_16x16x32_bf16 v[72:75], a[136:139], a[88:91], v[72:75]\n" \
"v_mfma_f32_16x16x32_bf16 a[208:211], a[216:219], v[192:195], a[208:211]\n" \
"v_mul_f32_e32 v204, v56, v204\n" \
"v_mul_f32_e32 v205, v56, v205\n" \
"v_mfma_f32_16x16x32_bf16 v[72:75], a[132:135], a[84:87], v[72:75]\n" \
"v_mul_f32_e32 v206, v56, v206\n" \
"v_mul_f32_e32 v207, v56, v207\n" \
"v_mfma_f32_16x16x32_bf16 v[72:75], a[128:131], a[80:83], v[72:75]\n" \
"buffer_load_dwordx4 v53, s[8:11], s64 offen offset:0 sc1 lds\n" \
"v_mfma_f32_16x16x32_bf16 v[80:83], a[128:131], a[96:99], v[40:43]\n" \
"v_mul_f32_e32 v208, v56, v208\n" \
"v_mul_f32_e32 v209, v56, v209\n" \
"v_mfma_f32_16x16x32_bf16 v[80:83], a[132:135], a[100:103], v[80:83]\n" \
"v_max_f32_e32 v62, v62, v57\n" \
"v_cvt_pk_bf16_f32 v202, v204, v205\n" \
"v_mfma_f32_16x16x32_bf16 v[80:83], a[136:139], a[104:107], v[80:83]\n" \
"buffer_load_dwordx4 v53, s[8:11], s64 offen offset:1024 sc1 lds\n" \
"v_mfma_f32_16x16x32_bf16 v[80:83], a[140:143], a[108:111], v[80:83]\n" \
"v_cvt_pk_bf16_f32 v203, v206, v207\n" \
"v_cvt_pk_bf16_f32 v208, v208, v209\n" \
"buffer_store_dwordx4 v[200:203], v20, s[56:59], s70 offen offset:1024 sc1\n" \
"v_mfma_f32_16x16x32_bf16 v[84:87], a[156:159], a[108:111], v[40:43]\n" \
"v_mul_f32_e32 v210, v56, v210\n" \
"v_mul_f32_e32 v211, v56, v211\n" \
"v_mfma_f32_16x16x32_bf16 v[84:87], a[152:155], a[104:107], v[84:87]\n" \
"v_rcp_f32_e32 v57, v57\n" \
"v_mfma_f32_16x16x32_bf16 v[84:87], a[148:151], a[100:103], v[84:87]\n" \
"buffer_load_dwordx4 v53, s[8:11], s64 offen offset:2048 sc1 lds\n" \
"v_mfma_f32_16x16x32_bf16 v[84:87], a[144:147], a[96:99], v[84:87]\n" \
"v_mfma_f32_16x16x32_bf16 a[208:211], a[216:219], v[200:203], a[208:211]\n" \
"v_mfma_f32_32x32x16_bf16 v[192:207], a[216:219], a[216:219], 0\n" \
"v_cvt_pk_bf16_f32 v209, v210, v211\n" \
"v_mul_f32_e32 v212, v56, v212\n" \
".fill (L_ab + 16384 - .) / 4, 4, 0xBF800000\n" \
"L_ap4:\n" \
"s_cbranch_execz L_ap0\n" \
"v_mul_f32_e32 v213, v56, v213\n" \
"v_mul_f32_e32 v214, v56, v214\n" \
"v_mul_f32_e32 v215, v56, v215\n" \
"v_mul_f32_e32 v216, v56, v216\n" \
"v_mfma_f32_16x16x32_bf16 v[92:95], a[144:147], a[112:115], v[44:47]\n" \
"ds_read_b128 a[144:147], v48 offset:12288\n" \
"v_mul_f32_e32 v217, v56, v217\n" \
"v_mfma_f32_16x16x32_bf16 v[92:95], a[148:151], a[116:119], v[92:95]\n" \
"ds_read_b128 a[148:151], v48 offset:13312\n" \
"v_cvt_pk_bf16_f32 v210, v212, v213\n" \
"v_mfma_f32_16x16x32_bf16 v[92:95], a[152:155], a[120:123], v[92:95]\n" \
"ds_read_b128 a[152:155], v48 offset:14336\n" \
"v_cvt_pk_bf16_f32 v211, v214, v215\n" \
"buffer_store_dwordx4 v[208:211], v20, s[56:59], s70 offen offset:2048 sc1\n" \
"v_mfma_f32_16x16x32_bf16 v[92:95], a[156:159], a[124:127], v[92:95]\n" \
"ds_read_b128 a[156:159], v48 offset:15360\n" \
"v_cvt_pk_bf16_f32 v216, v216, v217\n" \
"v_mfma_f32_16x16x32_bf16 v[88:91], a[140:143], a[124:127], v[44:47]\n" \
"ds_read_b128 a[140:143], v48 offset:11264\n" \
"v_mul_f32_e32 v218, v56, v218\n" \
"v_mfma_f32_16x16x32_bf16 v[88:91], a[136:139], a[120:123], v[88:91]\n" \
"ds_read_b128 a[136:139], v48 offset:10240\n" \
"v_mul_f32_e32 v219, v56, v219\n" \
"v_mfma_f32_16x16x32_bf16 v[88:91], a[132:135], a[116:119], v[88:91]\n" \
"ds_read_b128 a[132:135], v48 offset:9216\n" \
"v_mul_f32_e32 v57, v57, v61\n" \
"v_mfma_f32_16x16x32_bf16 v[88:91], a[128:131], a[112:115], v[88:91]\n" \
"ds_read_b128 a[128:131], v48 offset:8192\n" \
"v_mfma_f32_16x16x32_bf16 a[208:211], a[216:219], v[208:211], a[208:211]\n" \
"v_cvt_pk_bf16_f32 v217, v218, v219\n" \
"v_mul_f32_e32 v220, v56, v220\n" \
"v_mul_f32_e32 v221, v56, v221\n" \
"v_mul_f32_e32 v222, v56, v222\n" \
"v_mul_f32_e32 v223, v56, v223\n" \
"v_mul_f32_e32 v224, v57, v224\n" \
"v_mul_f32_e32 v225, v57, v225\n" \
"v_cvt_pk_bf16_f32 v218, v220, v221\n" \
"v_cvt_pk_bf16_f32 v219, v222, v223\n" \
"buffer_store_dwordx4 v[216:219], v20, s[56:59], s70 offen offset:3072 sc1\n" \
"v_cvt_pk_bf16_f32 v224, v224, v225\n" \
"v_mul_f32_e32 v226, v57, v226\n" \
"v_mul_f32_e32 v227, v57, v227\n" \
"v_mfma_f32_16x16x32_bf16 a[208:211], a[216:219], v[216:219], a[208:211]\n" \
"v_mfma_f32_32x32x16_bf16 v[208:223], a[216:219], a[216:219], 0\n" \
"v_cvt_pk_bf16_f32 v225, v226, v227\n" \
"v_mul_f32_e32 v228, v57, v228\n" \
"v_mul_f32_e32 v229, v57, v229\n" \
"v_mul_f32_e32 v230, v57, v230\n" \
"v_mul_f32_e32 v231, v57, v231\n" \
"v_mul_f32_e32 v232, v57, v232\n" \
"v_mul_f32_e32 v233, v57, v233\n" \
"v_cvt_pk_bf16_f32 v226, v228, v229\n" \
"v_cvt_pk_bf16_f32 v227, v230, v231\n" \
"buffer_store_dwordx4 v[224:227], v20, s[56:59], s71 offen offset:0 sc1\n" \
"v_cvt_pk_bf16_f32 v232, v232, v233\n" \
"v_mul_f32_e32 v234, v57, v234\n" \
"v_mul_f32_e32 v235, v57, v235\n" \
"v_mfma_f32_16x16x32_bf16 a[208:211], a[216:219], v[224:227], a[208:211]\n" \
"v_cvt_pk_bf16_f32 v233, v234, v235\n" \
"v_mul_f32_e32 v236, v57, v236\n" \
"v_mul_f32_e32 v237, v57, v237\n" \
"v_mul_f32_e32 v238, v57, v238\n" \
"v_mul_f32_e32 v239, v57, v239\n" \
"v_mul_f32_e32 v240, v57, v240\n" \
"v_mul_f32_e32 v241, v57, v241\n" \
"v_cvt_pk_bf16_f32 v234, v236, v237\n" \
"v_cvt_pk_bf16_f32 v235, v238, v239\n" \
"buffer_store_dwordx4 v[232:235], v20, s[56:59], s71 offen offset:1024 sc1\n" \
"v_cvt_pk_bf16_f32 v240, v240, v241\n" \
"v_mul_f32_e32 v242, v57, v242\n" \
"v_mul_f32_e32 v243, v57, v243\n" \
"v_mfma_f32_16x16x32_bf16 a[208:211], a[216:219], v[232:235], a[208:211]\n" \
"v_mfma_f32_32x32x16_bf16 v[224:239], a[216:219], a[216:219], 0\n" \
"v_cvt_pk_bf16_f32 v241, v242, v243\n" \
"v_mul_f32_e32 v244, v57, v244\n" \
"v_mul_f32_e32 v245, v57, v245\n" \
"v_mul_f32_e32 v246, v57, v246\n" \
"v_mul_f32_e32 v247, v57, v247\n" \
"v_mul_f32_e32 v248, v57, v248\n" \
"v_mul_f32_e32 v249, v57, v249\n" \
"v_cvt_pk_bf16_f32 v242, v244, v245\n" \
"v_cvt_pk_bf16_f32 v243, v246, v247\n" \
"buffer_store_dwordx4 v[240:243], v20, s[56:59], s71 offen offset:2048 sc1\n" \
"v_cvt_pk_bf16_f32 v248, v248, v249\n" \
"v_mul_f32_e32 v250, v57, v250\n" \
"v_mul_f32_e32 v251, v57, v251\n" \
"v_mfma_f32_16x16x32_bf16 a[208:211], a[216:219], v[240:243], a[208:211]\n" \
"v_cvt_pk_bf16_f32 v249, v250, v251\n" \
"v_mul_f32_e32 v252, v57, v252\n" \
"v_mul_f32_e32 v253, v57, v253\n" \
"v_mul_f32_e32 v254, v57, v254\n" \
"v_mul_f32_e32 v255, v57, v255\n" \
"v_cvt_pk_bf16_f32 v250, v252, v253\n" \
"v_cvt_pk_bf16_f32 v251, v254, v255\n" \
"buffer_store_dwordx4 v[248:251], v20, s[56:59], s71 offen offset:3072 sc1\n" \
"buffer_load_dwordx4 v53, s[8:11], s64 offen offset:3072 sc1 lds\n" \
"s_waitcnt vmcnt(16) lgkmcnt(0)\n" \
"s_barrier\n" \
"v_mfma_f32_16x16x32_bf16 a[208:211], a[216:219], v[248:251], a[208:211]\n" \
"v_mfma_f32_32x32x16_bf16 v[240:255], a[216:219], a[216:219], 0\n" \
"v_exp_f32_e32 v70, v70\n" \
"v_exp_f32_e32 v71, v71\n" \
"v_exp_f32_e32 v72, v72\n" \
"v_exp_f32_e32 v73, v73\n" \
"s_add_u32 s64, s64, 0x10000\n" \
"v_cmp_lt_f32_e64 s[72:73], s21, v62\n" \
"v_readlane_b32 s95, v11, 3\n" \
"v_accvgpr_read_b32 v63, a208\n" \
"v_cmp_u_f32_e32 vcc, v63, v63\n" \
"v_exp_f32_e32 v68, v68\n" \
"v_exp_f32_e32 v69, v69\n" \
"v_exp_f32_e32 v74, v74\n" \
"v_exp_f32_e32 v75, v75\n" \
"s_or_b64 vcc, s[72:73], vcc\n" \
"s_cselect_b32 s95, 0x40000000, s95\n" \
"s_cselect_b32 s94, 0x7f800000, 0\n" \
"v_cvt_pk_bf16_f32 v71, v70, v71\n" \
"v_exp_f32_e32 v66, v66\n" \
"v_exp_f32_e32 v67, v67\n" \
"v_exp_f32_e32 v64, v64\n" \
"v_exp_f32_e32 v65, v65\n" \
"v_cvt_pk_bf16_f32 v72, v72, v73\n" \
"v_exp_f32_e32 v76, v76\n" \
"v_exp_f32_e32 v77, v77\n" \
"v_exp_f32_e32 v78, v78\n" \
"v_exp_f32_e32 v79, v79\n" \
"v_cvt_pk_bf16_f32 v70, v68, v69\n" \
"v_cvt_pk_bf16_f32 v73, v74, v75\n" \
"v_add_u32_e32 v49, 0x10000, v48\n" \
"s_xor_b32 s65, s65, 0x8000\n" \
"v_cvt_pk_bf16_f32 v69, v66, v67\n" \
"v_cvt_pk_bf16_f32 v68, v64, v65\n" \
"v_cvt_pk_bf16_f32 v74, v76, v77\n" \
"v_cvt_pk_bf16_f32 v75, v78, v79\n" \
"v_xor_b32_e32 v48, 0x8000, v48\n" \
"s_sub_u32 s66, s19, 1\n" \
"s_cbranch_scc0 L_2_%=\n" \
"L_f1_%=:\n" \
"v_bfe_u32 v60, v20, 2, 7\n" \
"v_readlane_b32 s96, v11, 1\n" \
"ds_read_b128 a[160:163], v49 offset:0\n" \
"ds_read_b128 a[164:167], v49 offset:1024\n" \
"ds_read_b128 a[168:171], v49 offset:2048\n" \
"v_mfma_f32_16x16x32_bf16 v[104:107], a[128:131], a[80:83], v[36:39]\n" \
"v_cvt_pk_bf16_f32 v192, v192, v193\n" \
"v_cvt_pk_bf16_f32 v224, v224, v225\n" \
"buffer_load_dword v57, v60, s[56:59], s96 offen sc1\n" \
"v_readlane_b32 s93, v11, 20\n" \
"v_cvt_pk_bf16_f32 v193, v194, v195\n" \
"v_cvt_pk_bf16_f32 v225, v226, v227\n" \
"ds_read_b128 a[172:175], v49 offset:3072\n" \
"v_readlane_b32 s84, v11, 15\n" \
"v_readlane_b32 s85, v11, 16\n" \
"v_readlane_b32 s86, v11, 17\n" \
"s_mov_b32 s87, 0x20000\n" \
"v_readlane_b32 s92, v11, 18\n" \
"s_add_u32 m0, s93, 0x0\n" \
"v_cvt_pk_bf16_f32 v194, v196, v197\n" \
"v_cvt_pk_bf16_f32 v195, v198, v199\n" \
"v_cvt_pk_bf16_f32 v226, v228, v229\n" \
"v_cvt_pk_bf16_f32 v227, v230, v231\n" \
"v_mfma_f32_16x16x32_bf16 v[104:107], a[132:135], a[84:87], v[104:107]\n" \
"v_lshl_add_u32 v50, s15, 2, v20\n" \
"v_cvt_pk_bf16_f32 v196, v200, v201\n" \
"v_cvt_pk_bf16_f32 v197, v202, v203\n" \
"v_cvt_pk_bf16_f32 v198, v204, v205\n" \
"v_cvt_pk_bf16_f32 v199, v206, v207\n" \
"buffer_load_dwordx4 v0, s[84:87], s92 offen offset:0 lds\n" \
"v_cvt_pk_bf16_f32 v228, v232, v233\n" \
"v_cvt_pk_bf16_f32 v229, v234, v235\n" \
"v_cvt_pk_bf16_f32 v230, v236, v237\n" \
"v_cvt_pk_bf16_f32 v231, v238, v239\n" \
"v_add_u32_e32 v50, 0x1a000, v50\n" \
"v_mov_b32_e32 v61, s62\n" \
"v_cvt_pk_bf16_f32 v200, v208, v209\n" \
"v_cvt_pk_bf16_f32 v201, v210, v211\n" \
"v_cvt_pk_bf16_f32 v202, v212, v213\n" \
"v_cvt_pk_bf16_f32 v203, v214, v215\n" \
"v_cvt_pk_bf16_f32 v204, v216, v217\n" \
"v_cvt_pk_bf16_f32 v205, v218, v219\n" \
"v_cvt_pk_bf16_f32 v206, v220, v221\n" \
"v_cvt_pk_bf16_f32 v207, v222, v223\n" \
"v_cvt_pk_bf16_f32 v232, v240, v241\n" \
"v_cvt_pk_bf16_f32 v233, v242, v243\n" \
"v_cvt_pk_bf16_f32 v234, v244, v245\n" \
"v_cvt_pk_bf16_f32 v235, v246, v247\n" \
"v_cvt_pk_bf16_f32 v236, v248, v249\n" \
"v_cvt_pk_bf16_f32 v237, v250, v251\n" \
"v_cvt_pk_bf16_f32 v238, v252, v253\n" \
"buffer_load_dwordx4 v1, s[84:87], s92 offen offset:1024 lds\n" \
"v_cvt_pk_bf16_f32 v239, v254, v255\n" \
"ds_write_b128 v50, v[192:195] offset:0\n" \
"v_mfma_f32_16x16x32_bf16 v[104:107], a[136:139], a[88:91], v[104:107]\n" \
"ds_read_b128 a[176:179], v49 offset:4096\n" \
"v_add_u32_e32 v56, 0x49000, v20\n" \
"v_mfma_f32_16x16x32_bf16 v[104:107], a[140:143], a[92:95], v[104:107]\n" \
"ds_write_b128 v50, v[196:199] offset:1024\n" \
"v_mfma_f32_16x16x32_bf16 v[108:111], a[156:159], a[92:95], v[36:39]\n" \
"v_readlane_b32 s97, v11, 35\n" \
"v_readlane_b32 s98, v11, 36\n" \
"ds_read_b128 a[180:183], v49 offset:5120\n" \
"v_mfma_f32_16x16x32_bf16 v[108:111], a[152:155], a[88:91], v[108:111]\n" \
"ds_write_b128 v50, v[200:203] offset:2048\n" \
"v_mfma_f32_16x16x32_bf16 v[108:111], a[148:151], a[84:87], v[108:111]\n" \
"buffer_load_dwordx4 v2, s[84:87], s92 offen offset:2048 lds\n" \
"ds_read_b128 a[184:187], v49 offset:6144\n" \
"v_mfma_f32_16x16x32_bf16 v[108:111], a[144:147], a[80:83], v[108:111]\n" \
"ds_write_b128 v50, v[204:207] offset:3072\n" \
"v_and_b32_e32 v68, v12, v68\n" \
"v_and_b32_e32 v69, v13, v69\n" \
"v_and_b32_e32 v70, v14, v70\n" \
"v_and_b32_e32 v71, v15, v71\n" \
"ds_read_b128 a[188:191], v49 offset:7168\n" \
"ds_read_b128 a[0:3], v20 offset:8192\n" \
"ds_write_b128 v50, v[224:227] offset:4096\n" \
"s_waitcnt lgkmcnt(10)\n" \
"v_mfma_f32_16x16x32_bf16 v[128:131], a[160:163], v[68:71], v[128:131]\n" \
"ds_read_b128 a[4:7], v20 offset:9216\n" \
"ds_read_b128 a[8:11], v20 offset:10240\n" \
"v_mfma_f32_16x16x32_bf16 v[160:163], a[160:163], v[72:75], v[160:163]\n" \
"ds_write_b128 v50, v[228:231] offset:5120\n" \
"buffer_load_dwordx4 v3, s[84:87], s92 offen offset:3072 lds\n" \
"s_add_u32 m0, s93, 0x1000\n" \
"v_mfma_f32_16x16x32_bf16 v[132:135], a[164:167], v[68:71], v[132:135]\n" \
"ds_read_b128 a[12:15], v20 offset:11264\n" \
"ds_read_b128 a[28:31], v20 offset:15360\n" \
"ds_write_b128 v50, v[232:235] offset:6144\n" \
"v_mfma_f32_16x16x32_bf16 v[164:167], a[164:167], v[72:75], v[164:167]\n" \
"ds_read_b128 a[24:27], v20 offset:14336\n" \
"ds_read_b128 a[20:23], v20 offset:13312\n" \
"v_mfma_f32_16x16x32_bf16 v[136:139], a[168:171], v[68:71], v[136:139]\n" \
"ds_write_b128 v50, v[236:239] offset:7168\n" \
"v_mfma_f32_16x16x32_bf16 v[168:171], a[168:171], v[72:75], v[168:171]\n" \
"ds_read_b128 a[16:19], v20 offset:12288\n" \
"v_xor_b32_e32 v48, 0x4000, v49\n" \
"v_mfma_f32_16x16x32_bf16 v[140:143], a[172:175], v[68:71], v[140:143]\n" \
"buffer_load_dwordx4 v4, s[84:87], s92 offen offset:0 lds\n" \
"v_mfma_f32_16x16x32_bf16 v[172:175], a[172:175], v[72:75], v[172:175]\n" \
"ds_read_b32 v58, v18 offset:256\n" \
"s_waitcnt lgkmcnt(15)\n" \
"v_mfma_f32_16x16x32_bf16 v[144:147], a[176:179], v[68:71], v[144:147]\n" \
"ds_read_b32 v59, v18 offset:384\n" \
"ds_read_b128 a[32:35], v48 offset:8192\n" \
"v_mfma_f32_16x16x32_bf16 v[176:179], a[176:179], v[72:75], v[176:179]\n" \
"ds_read_b128 a[36:39], v48 offset:9216\n" \
"ds_read_b128 a[40:43], v48 offset:10240\n" \
"v_mfma_f32_16x16x32_bf16 v[148:151], a[180:183], v[68:71], v[148:151]\n" \
"ds_read_b128 a[44:47], v48 offset:11264\n" \
"ds_read_b128 a[48:51], v48 offset:12288\n" \
"v_mfma_f32_16x16x32_bf16 v[180:183], a[180:183], v[72:75], v[180:183]\n" \
"ds_read_b128 a[52:55], v48 offset:13312\n" \
"ds_read_b128 a[56:59], v48 offset:14336\n" \
"v_mfma_f32_16x16x32_bf16 v[152:155], a[184:187], v[68:71], v[152:155]\n" \
"buffer_load_dwordx4 v5, s[84:87], s92 offen offset:1024 lds\n" \
"v_mfma_f32_16x16x32_bf16 v[184:187], a[184:187], v[72:75], v[184:187]\n" \
"ds_read_b128 a[60:63], v48 offset:15360\n" \
"s_waitcnt lgkmcnt(15)\n" \
"v_mfma_f32_16x16x32_bf16 v[156:159], a[188:191], v[68:71], v[156:159]\n" \
"ds_read_b128 a[128:131], v49 offset:8192\n" \
"ds_read_b128 a[132:135], v49 offset:9216\n" \
"v_mfma_f32_16x16x32_bf16 v[188:191], a[188:191], v[72:75], v[188:191]\n" \
"ds_read_b128 a[136:139], v49 offset:10240\n" \
"ds_read_b128 a[140:143], v49 offset:11264\n" \
"v_smfmac_f32_16x16x64_bf16 a[192:195], a[200:203], v[68:75], v21\n" \
"ds_read_b128 a[144:147], v49 offset:12288\n" \
"ds_read_b128 a[148:151], v49 offset:13312\n" \
"v_mfma_f32_16x16x32_bf16 v[112:115], a[0:3], a[64:67], v[32:35]\n" \
"ds_read_b128 a[152:155], v49 offset:14336\n" \
"ds_read_b128 a[156:159], v49 offset:15360\n" \
"v_mfma_f32_16x16x32_bf16 v[112:115], a[4:7], a[68:71], v[112:115]\n" \
"buffer_load_dwordx4 v6, s[84:87], s92 offen offset:2048 lds\n" \
"v_mfma_f32_16x16x32_bf16 v[112:115], a[8:11], a[72:75], v[112:115]\n" \
"v_accvgpr_read_b32 v40, a196\n" \
"v_accvgpr_read_b32 v24, a197\n" \
"v_mfma_f32_16x16x32_bf16 v[112:115], a[12:15], a[76:79], v[112:115]\n" \
"v_accvgpr_read_b32 v41, a198\n" \
"v_accvgpr_read_b32 v25, a199\n" \
"v_mfma_f32_16x16x32_bf16 v[116:119], a[28:31], a[76:79], v[32:35]\n" \
"s_waitcnt vmcnt(7)\n" \
"v_cmp_ne_u32_e32 vcc, s62, v57\n" \
"s_mov_b64 exec, 1\n" \
"buffer_store_dword v61, off, s[56:59], s95 offset:0 sc1\n" \
"s_mov_b64 exec, -1\n" \
"s_waitcnt lgkmcnt(15)\n" \
"v_mfma_f32_16x16x32_bf16 v[116:119], a[24:27], a[72:75], v[116:119]\n" \
"s_cbranch_vccnz L_so1_%=\n" \
"L_ro1_%=:\n" \
"v_add_f32_e32 v40, v40, v24\n" \
"v_mfma_f32_16x16x32_bf16 v[116:119], a[20:23], a[68:71], v[116:119]\n" \
"v_add_f32_e32 v41, v41, v25\n" \
"v_add_f32_e32 v40, s94, v40\n" \
"v_mfma_f32_16x16x32_bf16 v[116:119], a[16:19], a[64:67], v[116:119]\n" \
"v_add_f32_e32 v41, s94, v41\n" \
"ds_write_b64 v19, v[40:41]\n" \
"v_mfma_f32_16x16x32_bf16 v[124:127], a[16:19], a[80:83], v[36:39]\n" \
"v_add_u32_e32 v52, s15, v20\n" \
"v_add_u32_e32 v51, 0x10000, v52\n" \
"v_mfma_f32_16x16x32_bf16 v[124:127], a[20:23], a[84:87], v[124:127]\n" \
"v_exp_f32_e32 v104, v104\n" \
"buffer_load_dwordx4 v7, s[84:87], s92 offen offset:3072 lds\n" \
"s_add_u32 m0, s93, 0x2000\n" \
"v_mfma_f32_16x16x32_bf16 v[124:127], a[24:27], a[88:91], v[124:127]\n" \
"v_exp_f32_e32 v105, v105\n" \
"v_mfma_f32_16x16x32_bf16 v[124:127], a[28:31], a[92:95], v[124:127]\n" \
"v_exp_f32_e32 v106, v106\n" \
"v_mfma_f32_16x16x32_bf16 v[120:123], a[12:15], a[92:95], v[36:39]\n" \
"v_exp_f32_e32 v107, v107\n" \
"v_mfma_f32_16x16x32_bf16 v[120:123], a[8:11], a[88:91], v[120:123]\n" \
"v_cvt_pk_bf16_f32 v104, v104, v105\n" \
"v_cvt_pk_bf16_f32 v105, v106, v107\n" \
"v_mfma_f32_16x16x32_bf16 v[120:123], a[4:7], a[84:87], v[120:123]\n" \
"v_exp_f32_e32 v108, v108\n" \
"buffer_load_dwordx4 v8, s[84:87], s92 offen offset:0 lds\n" \
"v_mfma_f32_16x16x32_bf16 v[120:123], a[0:3], a[80:83], v[120:123]\n" \
"v_exp_f32_e32 v109, v109\n" \
"v_exp_f32_e32 v110, v110\n" \
"v_exp_f32_e32 v111, v111\n" \
"s_waitcnt lgkmcnt(0)\n" \
"s_barrier\n" \
"ds_read_b128 v[208:211], v52 offset:0\n" \
"ds_read_b128 v[212:215], v52 offset:1024\n" \
"ds_read_b128 v[216:219], v52 offset:2048\n" \
"ds_read_b128 v[220:223], v52 offset:3072\n" \
"ds_read_b128 v[240:243], v51 offset:32768\n" \
"ds_read_b128 v[244:247], v51 offset:33792\n" \
"ds_read_b128 v[248:251], v51 offset:34816\n" \
"ds_read_b128 v[252:255], v51 offset:35840\n" \
"buffer_load_dwordx4 a[220:223], v20, s[56:59], s60 offen offset:0 sc1\n" \
"v_cvt_pk_bf16_f32 v106, v108, v109\n" \
"v_cvt_pk_bf16_f32 v107, v110, v111\n" \
"v_and_b32_e32 v104, v12, v104\n" \
"v_and_b32_e32 v105, v13, v105\n" \
"v_and_b32_e32 v106, v14, v106\n" \
"v_and_b32_e32 v107, v15, v107\n" \
"v_xor_b32_e32 v63, 0x400, v19\n" \
"ds_read_b64 v[42:43], v63\n" \
"v_mfma_f32_16x16x32_bf16 a[192:195], a[212:215], v[104:107], a[192:195]\n" \
"v_mov_b32_e32 v194, v28\n" \
"v_mov_b32_e32 v195, v29\n" \
"v_mfma_f32_16x16x32_bf16 v[160:163], a[128:131], v[104:107], v[160:163]\n" \
"v_mov_b32_e32 v196, 0\n" \
"v_mov_b32_e32 v197, 0\n" \
"v_mfma_f32_16x16x32_bf16 v[164:167], a[132:135], v[104:107], v[164:167]\n" \
"v_mov_b32_e32 v192, 0\n" \
"v_mov_b32_e32 v193, 0\n" \
"v_mfma_f32_16x16x32_bf16 v[168:171], a[136:139], v[104:107], v[168:171]\n" \
"buffer_load_dwordx4 a[236:239], v56, s[56:59], s60 offen offset:0 sc1\n" \
"v_mfma_f32_16x16x32_bf16 v[172:175], a[140:143], v[104:107], v[172:175]\n" \
"v_exp_f32_e32 v118, v118\n" \
"v_mfma_f32_16x16x32_bf16 v[176:179], a[144:147], v[104:107], v[176:179]\n" \
"v_exp_f32_e32 v119, v119\n" \
"v_mfma_f32_16x16x32_bf16 v[180:183], a[148:151], v[104:107], v[180:183]\n" \
"v_exp_f32_e32 v120, v120\n" \
"v_mfma_f32_16x16x32_bf16 v[184:187], a[152:155], v[104:107], v[184:187]\n" \
"v_exp_f32_e32 v121, v121\n" \
"v_mfma_f32_16x16x32_bf16 v[188:191], a[156:159], v[104:107], v[188:191]\n" \
"v_exp_f32_e32 v116, v116\n" \
"s_waitcnt lgkmcnt(1)\n" \
"s_barrier\n" \
"buffer_load_dwordx4 a[224:227], v20, s[56:59], s60 offen offset:1024 sc1\n" \
"v_mfma_f32_16x16x32_bf16 v[128:131], v[194:197], v[208:211], v[128:131]\n" \
"v_exp_f32_e32 v117, v117\n" \
"v_mfma_f32_16x16x32_bf16 v[132:135], v[192:195], v[208:211], v[132:135]\n" \
"v_exp_f32_e32 v122, v122\n" \
"v_mfma_f32_16x16x32_bf16 v[136:139], v[194:197], v[212:215], v[136:139]\n" \
"v_exp_f32_e32 v123, v123\n" \
"v_mfma_f32_16x16x32_bf16 v[140:143], v[192:195], v[212:215], v[140:143]\n" \
"v_cvt_pk_bf16_f32 v119, v118, v119\n" \
"v_cvt_pk_bf16_f32 v120, v120, v121\n" \
"v_mfma_f32_16x16x32_bf16 v[144:147], v[194:197], v[216:219], v[144:147]\n" \
"v_exp_f32_e32 v114, v114\n" \
"v_mfma_f32_16x16x32_bf16 v[148:151], v[192:195], v[216:219], v[148:151]\n" \
"buffer_load_dwordx4 a[240:243], v56, s[56:59], s60 offen offset:1024 sc1\n" \
"v_mfma_f32_16x16x32_bf16 v[152:155], v[194:197], v[220:223], v[152:155]\n" \
"v_exp_f32_e32 v115, v115\n" \
"v_mfma_f32_16x16x32_bf16 v[156:159], v[192:195], v[220:223], v[156:159]\n" \
"v_exp_f32_e32 v112, v112\n" \
"v_mfma_f32_16x16x32_bf16 v[160:163], v[194:197], v[240:243], v[160:163]\n" \
"v_exp_f32_e32 v113, v113\n" \
"v_mfma_f32_16x16x32_bf16 v[164:167], v[192:195], v[240:243], v[164:167]\n" \
"v_exp_f32_e32 v124, v124\n" \
"v_mfma_f32_16x16x32_bf16 v[168:171], v[194:197], v[244:247], v[168:171]\n" \
"v_exp_f32_e32 v125, v125\n" \
"v_mfma_f32_16x16x32_bf16 v[172:175], v[192:195], v[244:247], v[172:175]\n" \
"buffer_load_dwordx4 a[228:231], v20, s[56:59], s60 offen offset:2048 sc1\n" \
"v_mfma_f32_16x16x32_bf16 v[176:179], v[194:197], v[248:251], v[176:179]\n" \
"v_exp_f32_e32 v126, v126\n" \
"v_mfma_f32_16x16x32_bf16 v[180:183], v[192:195], v[248:251], v[180:183]\n" \
"v_exp_f32_e32 v127, v127\n" \
"v_mfma_f32_16x16x32_bf16 v[184:187], v[194:197], v[252:255], v[184:187]\n" \
"v_cvt_pk_bf16_f32 v118, v116, v117\n" \
"v_cvt_pk_bf16_f32 v121, v122, v123\n" \
"v_mfma_f32_16x16x32_bf16 v[188:191], v[192:195], v[252:255], v[188:191]\n" \
"v_cvt_pk_bf16_f32 v117, v114, v115\n" \
"v_cvt_pk_bf16_f32 v116, v112, v113\n" \
"v_cvt_pk_bf16_f32 v122, v124, v125\n" \
"v_cvt_pk_bf16_f32 v123, v126, v127\n" \
"s_nop 1\n" \
"v_smfmac_f32_16x16x64_bf16 a[192:195], a[200:203], v[116:123], v21\n" \
"buffer_load_dwordx4 a[244:247], v56, s[56:59], s60 offen offset:2048 sc1\n" \
"v_mfma_f32_16x16x32_bf16 v[128:131], a[32:35], v[116:119], v[128:131]\n" \
"v_mfma_f32_16x16x32_bf16 v[160:163], a[32:35], v[120:123], v[160:163]\n" \
"v_mfma_f32_16x16x32_bf16 v[132:135], a[36:39], v[116:119], v[132:135]\n" \
"v_mfma_f32_16x16x32_bf16 v[164:167], a[36:39], v[120:123], v[164:167]\n" \
"v_mfma_f32_16x16x32_bf16 v[136:139], a[40:43], v[116:119], v[136:139]\n" \
"v_mfma_f32_16x16x32_bf16 v[168:171], a[40:43], v[120:123], v[168:171]\n" \
"buffer_load_dwordx4 a[232:235], v20, s[56:59], s60 offen offset:3072 sc1\n" \
"v_mfma_f32_16x16x32_bf16 v[140:143], a[44:47], v[116:119], v[140:143]\n" \
"v_accvgpr_read_b32 v54, a192\n" \
"v_accvgpr_read_b32 v22, a193\n" \
"v_mfma_f32_16x16x32_bf16 v[172:175], a[44:47], v[120:123], v[172:175]\n" \
"v_add_f32_e32 v54, v54, v22\n" \
"s_waitcnt lgkmcnt(0)\n" \
"v_add_f32_e32 v54, v54, v42\n" \
"v_mfma_f32_16x16x32_bf16 v[144:147], a[48:51], v[116:119], v[144:147]\n" \
"v_max_f32_e32 v62, v62, v54\n" \
"v_accvgpr_read_b32 v55, a194\n" \
"v_mfma_f32_16x16x32_bf16 v[176:179], a[48:51], v[120:123], v[176:179]\n" \
"v_rcp_f32_e32 v54, v54\n" \
"v_mfma_f32_16x16x32_bf16 v[148:151], a[52:55], v[116:119], v[148:151]\n" \
"v_mul_f32_e32 v54, v54, v58\n" \
"v_mul_f32_e32 v128, v54, v128\n" \
"buffer_load_dwordx4 a[248:251], v56, s[56:59], s60 offen offset:3072 sc1\n" \
"v_mfma_f32_16x16x32_bf16 v[180:183], a[52:55], v[120:123], v[180:183]\n" \
"v_mul_f32_e32 v129, v54, v129\n" \
"v_mul_f32_e32 v130, v54, v130\n" \
"v_mul_f32_e32 v131, v54, v131\n" \
"v_mul_f32_e32 v132, v54, v132\n" \
"v_mul_f32_e32 v133, v54, v133\n" \
"v_mul_f32_e32 v134, v54, v134\n" \
"v_mul_f32_e32 v135, v54, v135\n" \
"v_cvt_pk_bf16_f32 v64, v128, v129\n" \
"v_cvt_pk_bf16_f32 v65, v130, v131\n" \
"v_cvt_pk_bf16_f32 v66, v132, v133\n" \
"v_cvt_pk_bf16_f32 v67, v134, v135\n" \
"v_accvgpr_read_b32 v23, a195\n" \
"v_mfma_f32_16x16x32_bf16 v[152:155], a[56:59], v[116:119], v[152:155]\n" \
"v_mfma_f32_16x16x32_bf16 a[208:211], a[216:219], v[64:67], a[208:211]\n" \
"v_mul_f32_e32 v136, v54, v136\n" \
"v_mul_f32_e32 v137, v54, v137\n" \
"buffer_load_dwordx4 a[96:99], v20, s[56:59], s97 offen offset:0 sc1\n" \
"v_mul_f32_e32 v138, v54, v138\n" \
"v_mul_f32_e32 v139, v54, v139\n" \
"v_mul_f32_e32 v140, v54, v140\n" \
"v_mul_f32_e32 v141, v54, v141\n" \
"v_mul_f32_e32 v142, v54, v142\n" \
"v_mul_f32_e32 v143, v54, v143\n" \
"v_add_f32_e32 v55, v55, v23\n" \
"v_cvt_pk_bf16_f32 v68, v136, v137\n" \
"v_cvt_pk_bf16_f32 v69, v138, v139\n" \
"v_cvt_pk_bf16_f32 v70, v140, v141\n" \
"v_cvt_pk_bf16_f32 v71, v142, v143\n" \
"v_add_f32_e32 v55, v55, v43\n" \
"v_mul_f32_e32 v144, v54, v144\n" \
"v_mfma_f32_16x16x32_bf16 a[208:211], a[216:219], v[68:71], a[208:211]\n" \
"v_mul_f32_e32 v145, v54, v145\n" \
"v_mul_f32_e32 v146, v54, v146\n" \
"v_mul_f32_e32 v147, v54, v147\n" \
"v_mul_f32_e32 v148, v54, v148\n" \
"v_mul_f32_e32 v149, v54, v149\n" \
"v_mul_f32_e32 v150, v54, v150\n" \
"buffer_load_dwordx4 a[104:107], v56, s[56:59], s97 offen offset:0 sc1\n" \
"v_mul_f32_e32 v151, v54, v151\n" \
"v_max_f32_e32 v62, v62, v55\n" \
"v_cvt_pk_bf16_f32 v72, v144, v145\n" \
"v_cvt_pk_bf16_f32 v73, v146, v147\n" \
"v_cvt_pk_bf16_f32 v74, v148, v149\n" \
"v_cvt_pk_bf16_f32 v75, v150, v151\n" \
"v_rcp_f32_e32 v55, v55\n" \
"v_mfma_f32_16x16x32_bf16 v[184:187], a[56:59], v[120:123], v[184:187]\n" \
"v_mfma_f32_16x16x32_bf16 a[208:211], a[216:219], v[72:75], a[208:211]\n" \
"v_mul_f32_e32 v152, v54, v152\n" \
"v_mul_f32_e32 v153, v54, v153\n" \
"v_mul_f32_e32 v154, v54, v154\n" \
"v_mul_f32_e32 v155, v54, v155\n" \
"v_cvt_pk_bf16_f32 v76, v152, v153\n" \
"v_cvt_pk_bf16_f32 v77, v154, v155\n" \
"v_mul_f32_e32 v55, v55, v59\n" \
"buffer_load_dwordx4 a[112:115], v20, s[56:59], s98 offen offset:0 sc1\n" \
"v_mul_f32_e32 v160, v55, v160\n" \
"v_mul_f32_e32 v161, v55, v161\n" \
"v_mul_f32_e32 v162, v55, v162\n" \
"v_mul_f32_e32 v163, v55, v163\n" \
"v_mul_f32_e32 v164, v55, v164\n" \
"v_mul_f32_e32 v165, v55, v165\n" \
"v_mul_f32_e32 v166, v55, v166\n" \
"v_mul_f32_e32 v167, v55, v167\n" \
"v_cvt_pk_bf16_f32 v80, v160, v161\n" \
"v_cvt_pk_bf16_f32 v81, v162, v163\n" \
"v_cvt_pk_bf16_f32 v82, v164, v165\n" \
"v_cvt_pk_bf16_f32 v83, v166, v167\n" \
"v_mfma_f32_16x16x32_bf16 v[156:159], a[60:63], v[116:119], v[156:159]\n" \
"v_mul_f32_e32 v168, v55, v168\n" \
"v_mul_f32_e32 v169, v55, v169\n" \
"v_mul_f32_e32 v170, v55, v170\n" \
"v_mul_f32_e32 v171, v55, v171\n" \
"v_mul_f32_e32 v172, v55, v172\n" \
"v_mul_f32_e32 v173, v55, v173\n" \
"v_mul_f32_e32 v174, v55, v174\n" \
"buffer_load_dwordx4 a[120:123], v56, s[56:59], s98 offen offset:0 sc1\n" \
"v_mul_f32_e32 v156, v54, v156\n" \
"v_mul_f32_e32 v157, v54, v157\n" \
"v_mul_f32_e32 v158, v54, v158\n" \
"v_mul_f32_e32 v159, v54, v159\n" \
"v_cvt_pk_bf16_f32 v78, v156, v157\n" \
"v_cvt_pk_bf16_f32 v79, v158, v159\n" \
"v_mul_f32_e32 v175, v55, v175\n" \
"v_cvt_pk_bf16_f32 v84, v168, v169\n" \
"v_mfma_f32_16x16x32_bf16 a[208:211], a[216:219], v[76:79], a[208:211]\n" \
"v_mfma_f32_16x16x32_bf16 a[208:211], a[216:219], v[80:83], a[208:211]\n" \
"v_cvt_pk_bf16_f32 v85, v170, v171\n" \
"v_cvt_pk_bf16_f32 v86, v172, v173\n" \
"v_cvt_pk_bf16_f32 v87, v174, v175\n" \
"v_mul_f32_e32 v176, v55, v176\n" \
"v_mul_f32_e32 v177, v55, v177\n" \
"v_mfma_f32_16x16x32_bf16 a[208:211], a[216:219], v[84:87], a[208:211]\n" \
"buffer_load_dwordx4 a[100:103], v20, s[56:59], s97 offen offset:1024 sc1\n" \
"v_mul_f32_e32 v178, v55, v178\n" \
"v_mul_f32_e32 v179, v55, v179\n" \
"v_mul_f32_e32 v180, v55, v180\n" \
"v_mul_f32_e32 v181, v55, v181\n" \
"v_mul_f32_e32 v182, v55, v182\n" \
"v_mul_f32_e32 v183, v55, v183\n" \
"v_cvt_pk_bf16_f32 v96, v176, v177\n" \
"v_cvt_pk_bf16_f32 v97, v178, v179\n" \
"v_cvt_pk_bf16_f32 v98, v180, v181\n" \
"v_cvt_pk_bf16_f32 v99, v182, v183\n" \
"v_mfma_f32_16x16x32_bf16 v[188:191], a[60:63], v[120:123], v[188:191]\n" \
"v_mul_f32_e32 v184, v55, v184\n" \
"v_mfma_f32_16x16x32_bf16 a[208:211], a[216:219], v[96:99], a[208:211]\n" \
"v_mul_f32_e32 v185, v55, v185\n" \
"v_mul_f32_e32 v186, v55, v186\n" \
"v_mul_f32_e32 v187, v55, v187\n" \
"v_cvt_pk_bf16_f32 v100, v184, v185\n" \
"v_cvt_pk_bf16_f32 v101, v186, v187\n" \
"buffer_load_dwordx4 a[108:111], v56, s[56:59], s97 offen offset:1024 sc1\n" \
"v_mul_f32_e32 v188, v55, v188\n" \
"v_mul_f32_e32 v189, v55, v189\n" \
"v_mul_f32_e32 v190, v55, v190\n" \
"v_mul_f32_e32 v191, v55, v191\n" \
"v_cvt_pk_bf16_f32 v102, v188, v189\n" \
"v_cvt_pk_bf16_f32 v103, v190, v191\n" \
"v_cmp_lt_f32_e64 s[72:73], s21, v62\n" \
"buffer_load_dwordx4 a[116:119], v20, s[56:59], s98 offen offset:1024 sc1\n" \
"v_mfma_f32_16x16x32_bf16 a[208:211], a[216:219], v[100:103], a[208:211]\n" \
"buffer_load_dwordx4 a[124:127], v56, s[56:59], s98 offen offset:1024 sc1\n" \
"s_nop 6\n" \
"v_accvgpr_read_b32 v63, a208\n" \
"v_cmp_u_f32_e32 vcc, v63, v63\n" \
"s_nop 0\n" \
"s_or_b64 vcc, s[72:73], vcc\n" \
"s_cbranch_scc0 L_out_%=\n" \
"s_branch L_end_%=\n" \
".fill (L_ab + 20480 - .) / 4, 4, 0xBF800000\n" \
".fill (L_ab + 20480 - .) / 4, 4, 0xBF800000\n" \
"L_ap5:\n" \
"s_setpc_b64 s[88:89]\n" \
"L_out_%=:\n" \
"v_bfe_u32 v38, v20, 2, 4\n" \
"v_readlane_b32 s64, v11, 2\n" \
"v_add_u32_e32 v32, 0x49000, v20\n" \
"v_add_u32_e32 v34, 0x20000, v10\n" \
"ds_read_b64_tr_b16 a[0:1], v34 offset:0\n" \
"ds_read_b64_tr_b16 a[2:3], v34 offset:4608\n" \
"ds_read_b64_tr_b16 a[4:5], v34 offset:32\n" \
"buffer_load_dword v26, v38, s[56:59], s64 offen sc1\n" \
"ds_read_b64_tr_b16 a[6:7], v34 offset:4640\n" \
"ds_read_b64_tr_b16 a[8:9], v34 offset:64\n" \
"ds_read_b64_tr_b16 a[10:11], v34 offset:4672\n" \
"ds_read_b64_tr_b16 a[12:13], v34 offset:96\n" \
"buffer_store_dwordx4 v[64:67], v20, s[56:59], s61 offen offset:0 sc1\n" \
"ds_read_b64_tr_b16 a[14:15], v34 offset:4704\n" \
"v_mov_b32_e32 v39, s62\n" \
"v_readlane_b32 s72, v11, 5\n" \
"v_readlane_b32 s73, v11, 4\n" \
"buffer_store_dwordx4 v[68:71], v20, s[56:59], s61 offen offset:1024 sc1\n" \
"ds_read_b64_tr_b16 a[16:17], v34 offset:128\n" \
"ds_read_b64_tr_b16 a[18:19], v34 offset:4736\n" \
"ds_read_b64_tr_b16 a[20:21], v34 offset:160\n" \
"ds_read_b64_tr_b16 a[22:23], v34 offset:4768\n" \
"buffer_store_dwordx4 v[72:75], v20, s[56:59], s61 offen offset:2048 sc1\n" \
"ds_read_b64_tr_b16 a[24:25], v34 offset:192\n" \
"ds_read_b64_tr_b16 a[26:27], v34 offset:4800\n" \
"ds_read_b64_tr_b16 a[28:29], v34 offset:224\n" \
"ds_read_b64_tr_b16 a[30:31], v34 offset:4832\n" \
"buffer_store_dwordx4 v[76:79], v20, s[56:59], s61 offen offset:3072 sc1\n" \
"ds_read_b64_tr_b16 a[192:193], v34 offset:256\n" \
"ds_read_b64_tr_b16 a[194:195], v34 offset:4864\n" \
"ds_read_b64_tr_b16 a[196:197], v34 offset:9216\n" \
"ds_read_b64_tr_b16 a[198:199], v34 offset:13824\n" \
"buffer_store_dwordx4 v[80:83], v32, s[56:59], s61 offen offset:0 sc1\n" \
"ds_read_b64_tr_b16 a[200:201], v34 offset:9248\n" \
"ds_read_b64_tr_b16 a[202:203], v34 offset:13856\n" \
"ds_read_b64_tr_b16 a[204:205], v34 offset:9280\n" \
"ds_read_b64_tr_b16 a[206:207], v34 offset:13888\n" \
"buffer_store_dwordx4 v[84:87], v32, s[56:59], s61 offen offset:1024 sc1\n" \
"ds_read_b64_tr_b16 a[208:209], v34 offset:9312\n" \
"s_waitcnt vmcnt(15) lgkmcnt(15)\n" \
"v_mfma_f32_16x16x32_bf16 v[112:115], a[0:3], a[220:223], 0\n" \
"buffer_store_dwordx4 v[96:99], v32, s[56:59], s61 offen offset:2048 sc1\n" \
"v_mfma_f32_16x16x32_bf16 v[116:119], a[0:3], a[236:239], 0\n" \
"ds_read_b64_tr_b16 a[210:211], v34 offset:13920\n" \
"ds_read_b64_tr_b16 a[212:213], v34 offset:9344\n" \
"buffer_store_dwordx4 v[100:103], v32, s[56:59], s61 offen offset:3072 sc1\n" \
"v_mfma_f32_16x16x32_bf16 v[184:187], a[0:3], v[64:67], 0\n" \
"ds_read_b64_tr_b16 a[214:215], v34 offset:13952\n" \
"ds_read_b64_tr_b16 a[216:217], v34 offset:9376\n" \
"v_mfma_f32_16x16x32_bf16 v[188:191], a[0:3], v[80:83], 0\n" \
"ds_read_b64_tr_b16 a[218:219], v34 offset:13984\n" \
"ds_read_b64_tr_b16 a[0:1], v34 offset:9408\n" \
"v_mfma_f32_16x16x32_bf16 v[120:123], a[4:7], a[220:223], 0\n" \
"ds_read_b64_tr_b16 a[2:3], v34 offset:14016\n" \
"v_readlane_b32 s66, v11, 21\n" \
"v_mfma_f32_16x16x32_bf16 v[124:127], a[4:7], a[236:239], 0\n" \
"v_readlane_b32 s80, v11, 11\n" \
"v_readlane_b32 s81, v11, 12\n" \
"v_mfma_f32_16x16x32_bf16 v[192:195], a[4:7], v[64:67], 0\n" \
"s_mov_b32 s82, 0x47fca0\n" \
"s_mov_b32 s83, 0x20000\n" \
"v_mfma_f32_16x16x32_bf16 v[196:199], a[4:7], v[80:83], 0\n" \
"ds_read_b64_tr_b16 a[4:5], v34 offset:9440\n" \
"ds_read_b64_tr_b16 a[6:7], v34 offset:14048\n" \
"v_mfma_f32_16x16x32_bf16 v[128:131], a[8:11], a[220:223], 0\n" \
"v_readlane_b32 s65, v11, 26\n" \
"s_add_u32 m0, s66, 0x0\n" \
"v_mfma_f32_16x16x32_bf16 v[132:135], a[8:11], a[236:239], 0\n" \
"v_readlane_b32 s67, v11, 29\n" \
"v_lshrrev_b32_e32 v44, 9, v20\n" \
"v_mfma_f32_16x16x32_bf16 v[200:203], a[8:11], v[64:67], 0\n" \
"buffer_load_dwordx4 v0, s[80:83], s65 offen offset:0 lds\n" \
"v_mfma_f32_16x16x32_bf16 v[204:207], a[8:11], v[80:83], 0\n" \
"ds_read_b64_tr_b16 a[8:9], v34 offset:9472\n" \
"ds_read_b64_tr_b16 a[10:11], v34 offset:14080\n" \
"v_mfma_f32_16x16x32_bf16 v[136:139], a[12:15], a[220:223], 0\n" \
"v_mul_u32_u24_e32 v44, 0xb40, v44\n" \
"v_add_u32_e32 v35, v44, v10\n" \
"v_mfma_f32_16x16x32_bf16 v[140:143], a[12:15], a[236:239], 0\n" \
"v_readlane_b32 s69, v11, 22\n" \
"v_readlane_b32 s68, v11, 27\n" \
"v_mfma_f32_16x16x32_bf16 v[208:211], a[12:15], v[64:67], 0\n" \
"v_readlane_b32 s94, v11, 23\n" \
"v_readlane_b32 s93, v11, 28\n" \
"v_mfma_f32_16x16x32_bf16 v[212:215], a[12:15], v[80:83], 0\n" \
"ds_read_b64_tr_b16 a[12:13], v34 offset:18432\n" \
"ds_read_b64_tr_b16 a[14:15], v34 offset:23040\n" \
"v_mfma_f32_16x16x32_bf16 v[144:147], a[16:19], a[220:223], 0\n" \
"buffer_load_dwordx4 v1, s[80:83], s65 offen offset:1024 lds\n" \
"v_mfma_f32_16x16x32_bf16 v[148:151], a[16:19], a[236:239], 0\n" \
"v_readlane_b32 s96, v11, 24\n" \
"v_readlane_b32 s84, v11, 9\n" \
"v_mfma_f32_16x16x32_bf16 v[216:219], a[16:19], v[64:67], 0\n" \
"v_readlane_b32 s85, v11, 10\n" \
"s_mov_b32 s86, 0xa1ca0\n" \
"v_mfma_f32_16x16x32_bf16 v[220:223], a[16:19], v[80:83], 0\n" \
"ds_read_b64_tr_b16 a[16:17], v34 offset:18464\n" \
"s_waitcnt lgkmcnt(7)\n" \
"v_mfma_f32_16x16x32_bf16 v[152:155], a[20:23], a[220:223], 0\n" \
"ds_read_b64_tr_b16 a[18:19], v34 offset:23072\n" \
"s_mov_b32 s87, 0x20000\n" \
"v_mfma_f32_16x16x32_bf16 v[156:159], a[20:23], a[236:239], 0\n" \
"buffer_load_dwordx4 v2, s[80:83], s65 offen offset:2048 lds\n" \
"v_mfma_f32_16x16x32_bf16 v[224:227], a[20:23], v[64:67], 0\n" \
"v_readlane_b32 s70, v11, 30\n" \
"v_add_u32_e32 v33, 0x10000, v10\n" \
"v_mfma_f32_16x16x32_bf16 v[228:231], a[20:23], v[80:83], 0\n" \
"ds_read_b64_tr_b16 a[20:21], v34 offset:18496\n" \
"ds_read_b64_tr_b16 a[22:23], v34 offset:23104\n" \
"v_mfma_f32_16x16x32_bf16 v[160:163], a[24:27], a[220:223], 0\n" \
"v_readlane_b32 s71, v11, 31\n" \
"v_readlane_b32 s92, v11, 33\n" \
"v_mfma_f32_16x16x32_bf16 v[164:167], a[24:27], a[236:239], 0\n" \
"v_readlane_b32 s95, v11, 34\n" \
"v_lshlrev_b32_e32 v41, 12, v20\n" \
"v_mfma_f32_16x16x32_bf16 v[232:235], a[24:27], v[64:67], 0\n" \
"buffer_load_dwordx4 v3, s[80:83], s65 offen offset:3072 lds\n" \
"v_mfma_f32_16x16x32_bf16 v[236:239], a[24:27], v[80:83], 0\n" \
"ds_read_b64_tr_b16 a[24:25], v34 offset:18528\n" \
"ds_read_b64_tr_b16 a[26:27], v34 offset:23136\n" \
"v_mfma_f32_16x16x32_bf16 v[168:171], a[28:31], a[220:223], 0\n" \
"s_add_u32 m0, s66, 0x1000\n" \
"v_readlane_b32 s97, v11, 37\n" \
"v_mfma_f32_16x16x32_bf16 v[172:175], a[28:31], a[236:239], 0\n" \
"v_readlane_b32 s63, v11, 40\n" \
"v_bfe_u32 v40, v20, 2, 7\n" \
"v_mfma_f32_16x16x32_bf16 v[240:243], a[28:31], v[64:67], 0\n" \
"s_add_u32 s74, s63, 0x3800\n" \
"v_readlane_b32 s100, v11, 7\n" \
"v_mfma_f32_16x16x32_bf16 v[244:247], a[28:31], v[80:83], 0\n" \
"ds_read_b64_tr_b16 a[28:29], v34 offset:18560\n" \
"ds_read_b64_tr_b16 a[30:31], v34 offset:23168\n" \
"v_mfma_f32_16x16x32_bf16 v[176:179], a[192:195], a[220:223], 0\n" \
"buffer_load_dwordx4 v4, s[80:83], s65 offen offset:0 lds\n" \
"v_mfma_f32_16x16x32_bf16 v[180:183], a[192:195], a[236:239], 0\n" \
"v_bfe_u32 v44, v20, 4, 4\n" \
"v_bfe_u32 v45, v20, 8, 2\n" \
"v_mfma_f32_16x16x32_bf16 v[248:251], a[192:195], v[64:67], 0\n" \
"v_mul_u32_u24_e32 v42, 0x480, v44\n" \
"v_lshl_add_u32 v42, v45, 3, v42\n" \
"v_mfma_f32_16x16x32_bf16 v[252:255], a[192:195], v[80:83], 0\n" \
"ds_read_b64_tr_b16 a[192:193], v34 offset:18592\n" \
"ds_read_b64_tr_b16 a[194:195], v34 offset:23200\n" \
"v_mfma_f32_16x16x32_bf16 v[112:115], a[196:199], a[224:227], v[112:115]\n" \
"v_lshrrev_b32_e32 v44, 1, v44\n" \
"v_lshlrev_b32_e32 v45, 1, v45\n" \
"v_mfma_f32_16x16x32_bf16 v[116:119], a[196:199], a[240:243], v[116:119]\n" \
"buffer_load_dwordx4 v5, s[80:83], s65 offen offset:1024 lds\n" \
"v_mfma_f32_16x16x32_bf16 v[184:187], a[196:199], v[68:71], v[184:187]\n" \
"v_sub_u32_e32 v44, v44, v45\n" \
"v_and_b32_e32 v45, 16, v20\n" \
"v_mfma_f32_16x16x32_bf16 v[188:191], a[196:199], v[84:87], v[188:191]\n" \
"ds_read_b64_tr_b16 a[196:197], v34 offset:18624\n" \
"ds_read_b64_tr_b16 a[198:199], v34 offset:23232\n" \
"v_mfma_f32_16x16x32_bf16 v[120:123], a[200:203], a[224:227], v[120:123]\n" \
"v_mov_b32_e32 v46, 0x3f80\n" \
"v_lshlrev_b32_e32 v45, v45, v46\n" \
"v_mfma_f32_16x16x32_bf16 v[124:127], a[200:203], a[240:243], v[124:127]\n" \
"v_mov_b32_e32 v52, 0\n" \
"v_mov_b32_e32 v53, 0\n" \
"v_mfma_f32_16x16x32_bf16 v[192:195], a[200:203], v[68:71], v[192:195]\n" \
"buffer_load_dwordx4 v6, s[80:83], s65 offen offset:2048 lds\n" \
"v_mfma_f32_16x16x32_bf16 v[196:199], a[200:203], v[84:87], v[196:199]\n" \
"ds_read_b64_tr_b16 a[200:201], v34 offset:18656\n" \
"ds_read_b64_tr_b16 a[202:203], v34 offset:23264\n" \
"v_mfma_f32_16x16x32_bf16 v[128:131], a[204:207], a[224:227], v[128:131]\n" \
"s_waitcnt vmcnt(15)\n" \
"v_cmp_ne_u32_e32 vcc, s62, v26\n" \
"v_mfma_f32_16x16x32_bf16 v[132:135], a[204:207], a[240:243], v[132:135]\n" \
"v_mov_b32_e32 v48, 0\n" \
"v_mov_b32_e32 v49, 0\n" \
"v_mfma_f32_16x16x32_bf16 v[200:203], a[204:207], v[68:71], v[200:203]\n" \
"v_add_u32_e32 v43, 0x9000, v42\n" \
"s_cbranch_vccnz L_sa_%=\n" \
"L_ra_%=:\n" \
"v_mfma_f32_16x16x32_bf16 v[204:207], a[204:207], v[84:87], v[204:207]\n" \
"ds_read_b64_tr_b16 a[204:205], v34 offset:18688\n" \
"ds_read_b64_tr_b16 a[206:207], v34 offset:23296\n" \
"v_mfma_f32_16x16x32_bf16 v[136:139], a[208:211], a[224:227], v[136:139]\n" \
"buffer_load_dwordx4 v7, s[80:83], s65 offen offset:3072 lds\n" \
"v_mfma_f32_16x16x32_bf16 v[140:143], a[208:211], a[240:243], v[140:143]\n" \
"s_add_u32 m0, s66, 0x2000\n" \
"v_readlane_b32 s66, v11, 32\n" \
"v_mfma_f32_16x16x32_bf16 v[208:211], a[208:211], v[68:71], v[208:211]\n" \
"v_readlane_b32 s64, v11, 38\n" \
"v_readlane_b32 s88, v11, 13\n" \
"v_mfma_f32_16x16x32_bf16 v[212:215], a[208:211], v[84:87], v[212:215]\n" \
"v_readlane_b32 s89, v11, 14\n" \
"s_mov_b32 s90, 0x240000\n" \
"v_mfma_f32_16x16x32_bf16 v[144:147], a[212:215], a[224:227], v[144:147]\n" \
"s_mov_b32 s91, 0x20000\n" \
"v_mov_b32_e32 v46, s62\n" \
"v_mfma_f32_16x16x32_bf16 v[148:151], a[212:215], a[240:243], v[148:151]\n" \
"buffer_load_dwordx4 v8, s[80:83], s65 offen offset:0 lds\n" \
"v_mfma_f32_16x16x32_bf16 v[216:219], a[212:215], v[68:71], v[216:219]\n" \
"s_add_u32 m0, s69, 0x0\n" \
"v_readlane_b32 s65, v11, 39\n" \
"v_mfma_f32_16x16x32_bf16 v[220:223], a[212:215], v[84:87], v[220:223]\n" \
"v_mfma_f32_16x16x32_bf16 v[152:155], a[216:219], a[224:227], v[152:155]\n" \
"v_mfma_f32_16x16x32_bf16 v[156:159], a[216:219], a[240:243], v[156:159]\n" \
"v_mfma_f32_16x16x32_bf16 v[224:227], a[216:219], v[68:71], v[224:227]\n" \
"buffer_load_dwordx4 a[64:67], v20, s[56:59], s67 offen offset:0 sc1\n" \
"v_mfma_f32_16x16x32_bf16 v[228:231], a[216:219], v[84:87], v[228:231]\n" \
"v_mfma_f32_16x16x32_bf16 v[160:163], a[0:3], a[224:227], v[160:163]\n" \
"v_mfma_f32_16x16x32_bf16 v[164:167], a[0:3], a[240:243], v[164:167]\n" \
"v_mfma_f32_16x16x32_bf16 v[232:235], a[0:3], v[68:71], v[232:235]\n" \
"v_mfma_f32_16x16x32_bf16 v[236:239], a[0:3], v[84:87], v[236:239]\n" \
"s_waitcnt lgkmcnt(0)\n" \
"v_mfma_f32_16x16x32_bf16 v[168:171], a[4:7], a[224:227], v[168:171]\n" \
"buffer_load_dwordx4 a[80:83], v32, s[56:59], s67 offen offset:0 sc1\n" \
"v_mfma_f32_16x16x32_bf16 v[172:175], a[4:7], a[240:243], v[172:175]\n" \
"v_mfma_f32_16x16x32_bf16 v[240:243], a[4:7], v[68:71], v[240:243]\n" \
"v_mfma_f32_16x16x32_bf16 v[244:247], a[4:7], v[84:87], v[244:247]\n" \
"v_mfma_f32_16x16x32_bf16 v[176:179], a[8:11], a[224:227], v[176:179]\n" \
"v_mfma_f32_16x16x32_bf16 v[180:183], a[8:11], a[240:243], v[180:183]\n" \
"buffer_load_dwordx4 a[68:71], v20, s[56:59], s67 offen offset:1024 sc1\n" \
"v_mfma_f32_16x16x32_bf16 v[248:251], a[8:11], v[68:71], v[248:251]\n" \
"v_mfma_f32_16x16x32_bf16 v[252:255], a[8:11], v[84:87], v[252:255]\n" \
"v_mfma_f32_16x16x32_bf16 v[112:115], a[12:15], a[228:231], v[112:115]\n" \
"v_mfma_f32_16x16x32_bf16 v[116:119], a[12:15], a[244:247], v[116:119]\n" \
"v_mfma_f32_16x16x32_bf16 v[184:187], a[12:15], v[72:75], v[184:187]\n" \
"buffer_load_dwordx4 a[84:87], v32, s[56:59], s67 offen offset:1024 sc1\n" \
"v_mfma_f32_16x16x32_bf16 v[188:191], a[12:15], v[96:99], v[188:191]\n" \
"v_mfma_f32_16x16x32_bf16 v[120:123], a[16:19], a[228:231], v[120:123]\n" \
"v_mfma_f32_16x16x32_bf16 v[124:127], a[16:19], a[244:247], v[124:127]\n" \
"v_mfma_f32_16x16x32_bf16 v[192:195], a[16:19], v[72:75], v[192:195]\n" \
"v_mfma_f32_16x16x32_bf16 v[196:199], a[16:19], v[96:99], v[196:199]\n" \
"buffer_load_dwordx4 a[72:75], v20, s[56:59], s67 offen offset:2048 sc1\n" \
"v_mfma_f32_16x16x32_bf16 v[128:131], a[20:23], a[228:231], v[128:131]\n" \
"v_mfma_f32_16x16x32_bf16 v[132:135], a[20:23], a[244:247], v[132:135]\n" \
"v_mfma_f32_16x16x32_bf16 v[200:203], a[20:23], v[72:75], v[200:203]\n" \
"v_mfma_f32_16x16x32_bf16 v[204:207], a[20:23], v[96:99], v[204:207]\n" \
"s_barrier\n" \
"v_mfma_f32_16x16x32_bf16 v[136:139], a[24:27], a[228:231], v[136:139]\n" \
"ds_read_b64_tr_b16 a[208:209], v10 offset:52224\n" \
"ds_read_b64_tr_b16 a[210:211], v10 offset:56832\n" \
"v_mfma_f32_16x16x32_bf16 v[140:143], a[24:27], a[244:247], v[140:143]\n" \
"ds_read_b64_tr_b16 a[212:213], v10 offset:52256\n" \
"ds_read_b64_tr_b16 a[214:215], v10 offset:56864\n" \
"v_mfma_f32_16x16x32_bf16 v[208:211], a[24:27], v[72:75], v[208:211]\n" \
"ds_read_b64_tr_b16 a[216:217], v10 offset:52288\n" \
"ds_read_b64_tr_b16 a[218:219], v10 offset:56896\n" \
"v_mfma_f32_16x16x32_bf16 v[212:215], a[24:27], v[96:99], v[212:215]\n" \
"ds_read_b64_tr_b16 a[0:1], v10 offset:52320\n" \
"ds_read_b64_tr_b16 a[2:3], v10 offset:56928\n" \
"v_mfma_f32_16x16x32_bf16 v[144:147], a[28:31], a[228:231], v[144:147]\n" \
"ds_read_b64_tr_b16 a[4:5], v10 offset:52352\n" \
"ds_read_b64_tr_b16 a[6:7], v10 offset:56960\n" \
"v_mfma_f32_16x16x32_bf16 v[148:151], a[28:31], a[244:247], v[148:151]\n" \
"ds_read_b64_tr_b16 a[8:9], v10 offset:52384\n" \
"ds_read_b64_tr_b16 a[10:11], v10 offset:56992\n" \
"v_mfma_f32_16x16x32_bf16 v[216:219], a[28:31], v[72:75], v[216:219]\n" \
"ds_read_b64_tr_b16 a[12:13], v10 offset:52416\n" \
"ds_read_b64_tr_b16 a[14:15], v10 offset:57024\n" \
"v_mfma_f32_16x16x32_bf16 v[220:223], a[28:31], v[96:99], v[220:223]\n" \
"ds_read_b64_tr_b16 a[16:17], v10 offset:52448\n" \
"ds_read_b64_tr_b16 a[18:19], v10 offset:57056\n" \
"v_mfma_f32_16x16x32_bf16 v[152:155], a[192:195], a[228:231], v[152:155]\n" \
"ds_read_b64_tr_b16 a[20:21], v10 offset:52480\n" \
"ds_read_b64_tr_b16 a[22:23], v10 offset:57088\n" \
"v_mfma_f32_16x16x32_bf16 v[156:159], a[192:195], a[244:247], v[156:159]\n" \
"ds_read_b64_tr_b16 a[24:25], v35 offset:24576\n" \
"ds_read_b64_tr_b16 a[26:27], v35 offset:26880\n" \
"v_mfma_f32_16x16x32_bf16 v[224:227], a[192:195], v[72:75], v[224:227]\n" \
"ds_read_b64_tr_b16 a[28:29], v35 offset:24608\n" \
"ds_read_b64_tr_b16 a[30:31], v35 offset:26912\n" \
"v_mfma_f32_16x16x32_bf16 v[228:231], a[192:195], v[96:99], v[228:231]\n" \
"ds_read_b64_tr_b16 a[192:193], v35 offset:24640\n" \
"ds_read_b64_tr_b16 a[194:195], v35 offset:26944\n" \
"v_mfma_f32_16x16x32_bf16 v[160:163], a[196:199], a[228:231], v[160:163]\n" \
"buffer_load_dwordx4 a[88:91], v32, s[56:59], s67 offen offset:2048 sc1\n" \
"v_mfma_f32_16x16x32_bf16 v[164:167], a[196:199], a[244:247], v[164:167]\n" \
"v_mfma_f32_16x16x32_bf16 v[232:235], a[196:199], v[72:75], v[232:235]\n" \
"v_mfma_f32_16x16x32_bf16 v[236:239], a[196:199], v[96:99], v[236:239]\n" \
"ds_read_b64_tr_b16 a[196:197], v35 offset:24672\n" \
"ds_read_b64_tr_b16 a[198:199], v35 offset:26976\n" \
"v_mfma_f32_16x16x32_bf16 v[168:171], a[200:203], a[228:231], v[168:171]\n" \
"v_mfma_f32_16x16x32_bf16 v[172:175], a[200:203], a[244:247], v[172:175]\n" \
"buffer_load_dwordx4 v0, s[80:83], s68 offen offset:0 lds\n" \
"v_mfma_f32_16x16x32_bf16 v[240:243], a[200:203], v[72:75], v[240:243]\n" \
"v_mfma_f32_16x16x32_bf16 v[244:247], a[200:203], v[96:99], v[244:247]\n" \
"ds_read_b64_tr_b16 a[200:201], v35 offset:24704\n" \
"ds_read_b64_tr_b16 a[202:203], v35 offset:27008\n" \
"v_mfma_f32_16x16x32_bf16 v[176:179], a[204:207], a[228:231], v[176:179]\n" \
"v_mfma_f32_16x16x32_bf16 v[180:183], a[204:207], a[244:247], v[180:183]\n" \
"v_mfma_f32_16x16x32_bf16 v[248:251], a[204:207], v[72:75], v[248:251]\n" \
"buffer_load_dwordx4 v1, s[80:83], s68 offen offset:1024 lds\n" \
"v_mfma_f32_16x16x32_bf16 v[252:255], a[204:207], v[96:99], v[252:255]\n" \
"ds_read_b64_tr_b16 a[204:205], v35 offset:24736\n" \
"s_waitcnt lgkmcnt(9)\n" \
"v_mfma_f32_16x16x32_bf16 v[112:115], a[208:211], a[232:235], v[112:115]\n" \
"ds_read_b64_tr_b16 a[206:207], v35 offset:27040\n" \
"v_mfma_f32_16x16x32_bf16 v[116:119], a[208:211], a[248:251], v[116:119]\n" \
"v_mfma_f32_16x16x32_bf16 v[184:187], a[208:211], v[76:79], v[184:187]\n" \
"v_mfma_f32_16x16x32_bf16 v[188:191], a[208:211], v[100:103], v[188:191]\n" \
"ds_read_b64_tr_b16 a[208:209], v35 offset:24768\n" \
"ds_read_b64_tr_b16 a[210:211], v35 offset:27072\n" \
"v_mfma_f32_16x16x32_bf16 v[120:123], a[212:215], a[232:235], v[120:123]\n" \
"buffer_load_dwordx4 v2, s[80:83], s68 offen offset:2048 lds\n" \
"v_mfma_f32_16x16x32_bf16 v[124:127], a[212:215], a[248:251], v[124:127]\n" \
"v_mfma_f32_16x16x32_bf16 v[192:195], a[212:215], v[76:79], v[192:195]\n" \
"s_waitcnt vmcnt(18)\n" \
"s_mov_b64 exec, 1\n" \
"buffer_store_dword v39, off, s[56:59], s72 offset:0 sc1\n" \
"s_mov_b64 exec, -1\n" \
"v_mfma_f32_16x16x32_bf16 v[196:199], a[212:215], v[100:103], v[196:199]\n" \
"ds_read_b64_tr_b16 a[212:213], v35 offset:24800\n" \
"ds_read_b64_tr_b16 a[214:215], v35 offset:27104\n" \
"v_mfma_f32_16x16x32_bf16 v[128:131], a[216:219], a[232:235], v[128:131]\n" \
"v_readlane_b32 s72, v11, 50\n" \
"v_mfma_f32_16x16x32_bf16 v[132:135], a[216:219], a[248:251], v[132:135]\n" \
"v_mfma_f32_16x16x32_bf16 v[200:203], a[216:219], v[76:79], v[200:203]\n" \
"buffer_load_dwordx4 v3, s[80:83], s68 offen offset:3072 lds\n" \
"v_mfma_f32_16x16x32_bf16 v[204:207], a[216:219], v[100:103], v[204:207]\n" \
"ds_read_b64_tr_b16 a[216:217], v35 offset:24832\n" \
"ds_read_b64_tr_b16 a[218:219], v35 offset:27136\n" \
"v_mfma_f32_16x16x32_bf16 v[136:139], a[0:3], a[232:235], v[136:139]\n" \
"s_add_u32 m0, s69, 0x1000\n" \
"v_mfma_f32_16x16x32_bf16 v[140:143], a[0:3], a[248:251], v[140:143]\n" \
"v_mfma_f32_16x16x32_bf16 v[208:211], a[0:3], v[76:79], v[208:211]\n" \
"v_mfma_f32_16x16x32_bf16 v[212:215], a[0:3], v[100:103], v[212:215]\n" \
"ds_read_b64_tr_b16 a[0:1], v35 offset:34944\n" \
"ds_read_b64_tr_b16 a[2:3], v35 offset:37248\n" \
"v_mfma_f32_16x16x32_bf16 v[144:147], a[4:7], a[232:235], v[144:147]\n" \
"buffer_load_dwordx4 v4, s[80:83], s68 offen offset:0 lds\n" \
"v_mfma_f32_16x16x32_bf16 v[148:151], a[4:7], a[248:251], v[148:151]\n" \
"v_mfma_f32_16x16x32_bf16 v[216:219], a[4:7], v[76:79], v[216:219]\n" \
"v_mfma_f32_16x16x32_bf16 v[220:223], a[4:7], v[100:103], v[220:223]\n" \
"ds_read_b64_tr_b16 a[4:5], v35 offset:34976\n" \
"ds_read_b64_tr_b16 a[6:7], v35 offset:37280\n" \
"v_mfma_f32_16x16x32_bf16 v[152:155], a[8:11], a[232:235], v[152:155]\n" \
"v_mfma_f32_16x16x32_bf16 v[156:159], a[8:11], a[248:251], v[156:159]\n" \
"buffer_load_dwordx4 v5, s[80:83], s68 offen offset:1024 lds\n" \
"v_mfma_f32_16x16x32_bf16 v[224:227], a[8:11], v[76:79], v[224:227]\n" \
"v_mfma_f32_16x16x32_bf16 v[228:231], a[8:11], v[100:103], v[228:231]\n" \
"ds_read_b64_tr_b16 a[8:9], v35 offset:35008\n" \
"ds_read_b64_tr_b16 a[10:11], v35 offset:37312\n" \
"v_mfma_f32_16x16x32_bf16 v[160:163], a[12:15], a[232:235], v[160:163]\n" \
"v_mfma_f32_16x16x32_bf16 v[164:167], a[12:15], a[248:251], v[164:167]\n" \
"v_mfma_f32_16x16x32_bf16 v[232:235], a[12:15], v[76:79], v[232:235]\n" \
"buffer_load_dwordx4 v6, s[80:83], s68 offen offset:2048 lds\n" \
"v_mfma_f32_16x16x32_bf16 v[236:239], a[12:15], v[100:103], v[236:239]\n" \
"ds_read_b64_tr_b16 a[12:13], v35 offset:35040\n" \
"ds_read_b64_tr_b16 a[14:15], v35 offset:37344\n" \
"v_mfma_f32_16x16x32_bf16 v[168:171], a[16:19], a[232:235], v[168:171]\n" \
"v_mfma_f32_16x16x32_bf16 v[172:175], a[16:19], a[248:251], v[172:175]\n" \
"v_mfma_f32_16x16x32_bf16 v[240:243], a[16:19], v[76:79], v[240:243]\n" \
"v_mfma_f32_16x16x32_bf16 v[244:247], a[16:19], v[100:103], v[244:247]\n" \
"buffer_load_dwordx4 v7, s[80:83], s68 offen offset:3072 lds\n" \
"v_mfma_f32_16x16x32_bf16 v[176:179], a[20:23], a[232:235], v[176:179]\n" \
"s_add_u32 m0, s69, 0x2000\n" \
"ds_read_b64_tr_b16 a[16:17], v35 offset:35072\n" \
"v_mfma_f32_16x16x32_bf16 v[180:183], a[20:23], a[248:251], v[180:183]\n" \
"ds_read_b64_tr_b16 a[18:19], v35 offset:37376\n" \
"v_readlane_b32 s69, v11, 47\n" \
"v_mfma_f32_16x16x32_bf16 v[248:251], a[20:23], v[76:79], v[248:251]\n" \
"v_mfma_f32_16x16x32_bf16 v[252:255], a[20:23], v[100:103], v[252:255]\n" \
"ds_read_b64_tr_b16 a[20:21], v35 offset:35104\n" \
"ds_read_b64_tr_b16 a[22:23], v35 offset:37408\n" \
"v_mfma_f32_16x16x32_bf16 v[112:115], a[24:27], a[96:99], v[112:115]\n" \
"buffer_load_dwordx4 v8, s[80:83], s68 offen offset:0 lds\n" \
"v_mfma_f32_16x16x32_bf16 v[116:119], a[24:27], a[104:107], v[116:119]\n" \
"s_add_u32 m0, s94, 0x0\n" \
"v_readlane_b32 s68, v11, 46\n" \
"v_mfma_f32_16x16x32_bf16 v[184:187], a[24:27], a[112:115], v[184:187]\n" \
"v_mfma_f32_16x16x32_bf16 v[188:191], a[24:27], a[120:123], v[188:191]\n" \
"ds_read_b64_tr_b16 a[24:25], v35 offset:35136\n" \
"s_waitcnt lgkmcnt(7)\n" \
"v_mfma_f32_16x16x32_bf16 v[120:123], a[28:31], a[96:99], v[120:123]\n" \
"ds_read_b64_tr_b16 a[26:27], v35 offset:37440\n" \
"buffer_load_dword v47, v38, s[56:59], s73 offen sc1\n" \
"v_mfma_f32_16x16x32_bf16 v[124:127], a[28:31], a[104:107], v[124:127]\n" \
"v_mfma_f32_16x16x32_bf16 v[192:195], a[28:31], a[112:115], v[192:195]\n" \
"v_mfma_f32_16x16x32_bf16 v[196:199], a[28:31], a[120:123], v[196:199]\n" \
"ds_read_b64_tr_b16 a[28:29], v35 offset:35168\n" \
"ds_read_b64_tr_b16 a[30:31], v35 offset:37472\n" \
"v_mfma_f32_16x16x32_bf16 v[128:131], a[192:195], a[96:99], v[128:131]\n" \
"v_mfma_f32_16x16x32_bf16 v[132:135], a[192:195], a[104:107], v[132:135]\n" \
"buffer_load_dwordx4 v0, s[80:83], s93 offen offset:0 lds\n" \
"v_mfma_f32_16x16x32_bf16 v[200:203], a[192:195], a[112:115], v[200:203]\n" \
"v_mfma_f32_16x16x32_bf16 v[204:207], a[192:195], a[120:123], v[204:207]\n" \
"ds_read_b64_tr_b16 a[192:193], v35 offset:35200\n" \
"ds_read_b64_tr_b16 a[194:195], v35 offset:37504\n" \
"v_mfma_f32_16x16x32_bf16 v[136:139], a[196:199], a[96:99], v[136:139]\n" \
"v_mfma_f32_16x16x32_bf16 v[140:143], a[196:199], a[104:107], v[140:143]\n" \
"v_mfma_f32_16x16x32_bf16 v[208:211], a[196:199], a[112:115], v[208:211]\n" \
"buffer_load_dwordx4 v1, s[80:83], s93 offen offset:1024 lds\n" \
"v_mfma_f32_16x16x32_bf16 v[212:215], a[196:199], a[120:123], v[212:215]\n" \
"v_mfma_f32_16x16x32_bf16 v[144:147], a[200:203], a[96:99], v[144:147]\n" \
"v_mfma_f32_16x16x32_bf16 v[148:151], a[200:203], a[104:107], v[148:151]\n" \
"v_mfma_f32_16x16x32_bf16 v[216:219], a[200:203], a[112:115], v[216:219]\n" \
"v_mfma_f32_16x16x32_bf16 v[220:223], a[200:203], a[120:123], v[220:223]\n" \
"buffer_load_dwordx4 v2, s[80:83], s93 offen offset:2048 lds\n" \
"v_mfma_f32_16x16x32_bf16 v[152:155], a[204:207], a[96:99], v[152:155]\n" \
"v_mfma_f32_16x16x32_bf16 v[156:159], a[204:207], a[104:107], v[156:159]\n" \
"v_mfma_f32_16x16x32_bf16 v[224:227], a[204:207], a[112:115], v[224:227]\n" \
"v_mfma_f32_16x16x32_bf16 v[228:231], a[204:207], a[120:123], v[228:231]\n" \
"v_mfma_f32_16x16x32_bf16 v[160:163], a[208:211], a[96:99], v[160:163]\n" \
"buffer_load_dwordx4 v3, s[80:83], s93 offen offset:3072 lds\n" \
"v_mfma_f32_16x16x32_bf16 v[164:167], a[208:211], a[104:107], v[164:167]\n" \
"s_add_u32 m0, s94, 0x1000\n" \
"v_mfma_f32_16x16x32_bf16 v[232:235], a[208:211], a[112:115], v[232:235]\n" \
"v_mfma_f32_16x16x32_bf16 v[236:239], a[208:211], a[120:123], v[236:239]\n" \
"v_mfma_f32_16x16x32_bf16 v[168:171], a[212:215], a[96:99], v[168:171]\n" \
"v_mfma_f32_16x16x32_bf16 v[172:175], a[212:215], a[104:107], v[172:175]\n" \
"buffer_load_dwordx4 v4, s[80:83], s93 offen offset:0 lds\n" \
"v_mfma_f32_16x16x32_bf16 v[240:243], a[212:215], a[112:115], v[240:243]\n" \
"v_mfma_f32_16x16x32_bf16 v[244:247], a[212:215], a[120:123], v[244:247]\n" \
"v_mfma_f32_16x16x32_bf16 v[176:179], a[216:219], a[96:99], v[176:179]\n" \
"v_mfma_f32_16x16x32_bf16 v[180:183], a[216:219], a[104:107], v[180:183]\n" \
"v_mfma_f32_16x16x32_bf16 v[248:251], a[216:219], a[112:115], v[248:251]\n" \
"buffer_load_dwordx4 v5, s[80:83], s93 offen offset:1024 lds\n" \
"v_mfma_f32_16x16x32_bf16 v[252:255], a[216:219], a[120:123], v[252:255]\n" \
"v_mfma_f32_16x16x32_bf16 v[112:115], a[0:3], a[100:103], v[112:115]\n" \
"v_mfma_f32_16x16x32_bf16 v[116:119], a[0:3], a[108:111], v[116:119]\n" \
"v_mfma_f32_16x16x32_bf16 v[184:187], a[0:3], a[116:119], v[184:187]\n" \
"v_mfma_f32_16x16x32_bf16 v[188:191], a[0:3], a[124:127], v[188:191]\n" \
"buffer_load_dwordx4 v6, s[80:83], s93 offen offset:2048 lds\n" \
"v_mfma_f32_16x16x32_bf16 v[120:123], a[4:7], a[100:103], v[120:123]\n" \
"v_mfma_f32_16x16x32_bf16 v[124:127], a[4:7], a[108:111], v[124:127]\n" \
"v_mfma_f32_16x16x32_bf16 v[192:195], a[4:7], a[116:119], v[192:195]\n" \
"v_mfma_f32_16x16x32_bf16 v[196:199], a[4:7], a[124:127], v[196:199]\n" \
"v_mfma_f32_16x16x32_bf16 v[128:131], a[8:11], a[100:103], v[128:131]\n" \
"buffer_load_dwordx4 v7, s[80:83], s93 offen offset:3072 lds\n" \
"s_add_u32 m0, s94, 0x2000\n" \
"v_mfma_f32_16x16x32_bf16 v[132:135], a[8:11], a[108:111], v[132:135]\n" \
"v_readlane_b32 s94, v11, 45\n" \
"v_mfma_f32_16x16x32_bf16 v[200:203], a[8:11], a[116:119], v[200:203]\n" \
"v_mfma_f32_16x16x32_bf16 v[204:207], a[8:11], a[124:127], v[204:207]\n" \
"s_waitcnt lgkmcnt(0)\n" \
"v_mfma_f32_16x16x32_bf16 v[136:139], a[12:15], a[100:103], v[136:139]\n" \
"buffer_load_dwordx4 v8, s[80:83], s93 offen offset:0 lds\n" \
"s_mov_b32 m0, s96\n" \
"v_mfma_f32_16x16x32_bf16 v[140:143], a[12:15], a[108:111], v[140:143]\n" \
"v_readlane_b32 s96, v11, 47\n" \
"v_readlane_b32 s93, v11, 6\n" \
"v_mfma_f32_16x16x32_bf16 v[208:211], a[12:15], a[116:119], v[208:211]\n" \
"v_mfma_f32_16x16x32_bf16 v[212:215], a[12:15], a[124:127], v[212:215]\n" \
"s_waitcnt vmcnt(23)\n" \
"s_barrier\n" \
"buffer_load_dwordx4 v9, s[84:87], 0 offen lds\n" \
"v_mfma_f32_16x16x32_bf16 v[144:147], a[16:19], a[100:103], v[144:147]\n" \
"ds_read_b64_tr_b16 a[196:197], v33 offset:0\n" \
"ds_read_b64_tr_b16 a[198:199], v33 offset:4608\n" \
"v_mfma_f32_16x16x32_bf16 v[148:151], a[16:19], a[108:111], v[148:151]\n" \
"ds_read_b64_tr_b16 a[200:201], v33 offset:32\n" \
"ds_read_b64_tr_b16 a[202:203], v33 offset:4640\n" \
"v_mfma_f32_16x16x32_bf16 v[216:219], a[16:19], a[116:119], v[216:219]\n" \
"ds_read_b64_tr_b16 a[204:205], v33 offset:64\n" \
"ds_read_b64_tr_b16 a[206:207], v33 offset:4672\n" \
"v_mfma_f32_16x16x32_bf16 v[220:223], a[16:19], a[124:127], v[220:223]\n" \
"ds_read_b64_tr_b16 a[208:209], v33 offset:96\n" \
"ds_read_b64_tr_b16 a[210:211], v33 offset:4704\n" \
"buffer_load_dwordx4 a[76:79], v20, s[56:59], s67 offen offset:3072 sc1\n" \
"v_mfma_f32_16x16x32_bf16 v[152:155], a[20:23], a[100:103], v[152:155]\n" \
"ds_read_b64_tr_b16 a[212:213], v33 offset:128\n" \
"ds_read_b64_tr_b16 a[214:215], v33 offset:4736\n" \
"v_mfma_f32_16x16x32_bf16 v[156:159], a[20:23], a[108:111], v[156:159]\n" \
"ds_read_b64_tr_b16 a[216:217], v33 offset:160\n" \
"ds_read_b64_tr_b16 a[218:219], v33 offset:4768\n" \
"v_mfma_f32_16x16x32_bf16 v[224:227], a[20:23], a[116:119], v[224:227]\n" \
"ds_read_b64_tr_b16 a[0:1], v33 offset:192\n" \
"ds_read_b64_tr_b16 a[2:3], v33 offset:4800\n" \
"v_mfma_f32_16x16x32_bf16 v[228:231], a[20:23], a[124:127], v[228:231]\n" \
"ds_read_b64_tr_b16 a[4:5], v33 offset:224\n" \
"ds_read_b64_tr_b16 a[6:7], v33 offset:4832\n" \
"buffer_load_dwordx4 a[92:95], v32, s[56:59], s67 offen offset:3072 sc1\n" \
"v_mfma_f32_16x16x32_bf16 v[160:163], a[24:27], a[100:103], v[160:163]\n" \
"v_readlane_b32 s67, v11, 45\n" \
"ds_read_b64_tr_b16 a[8:9], v33 offset:256\n" \
"v_mfma_f32_16x16x32_bf16 v[164:167], a[24:27], a[108:111], v[164:167]\n" \
"ds_read_b64_tr_b16 a[10:11], v33 offset:4864\n" \
"ds_read_b64_tr_b16 a[12:13], v33 offset:9216\n" \
"v_mfma_f32_16x16x32_bf16 v[232:235], a[24:27], a[116:119], v[232:235]\n" \
"ds_read_b64_tr_b16 a[14:15], v33 offset:13824\n" \
"ds_read_b64_tr_b16 a[16:17], v33 offset:9248\n" \
"v_mfma_f32_16x16x32_bf16 v[236:239], a[24:27], a[124:127], v[236:239]\n" \
"ds_read_b64_tr_b16 a[18:19], v33 offset:13856\n" \
"ds_read_b64_tr_b16 a[20:21], v33 offset:9280\n" \
"buffer_load_dwordx4 a[128:131], v20, s[56:59], s70 offen offset:0 sc1\n" \
"v_mfma_f32_16x16x32_bf16 v[168:171], a[28:31], a[100:103], v[168:171]\n" \
"ds_read_b64_tr_b16 a[22:23], v33 offset:13888\n" \
"ds_read_b64_tr_b16 a[24:25], v33 offset:9312\n" \
"v_mfma_f32_16x16x32_bf16 v[172:175], a[28:31], a[108:111], v[172:175]\n" \
"ds_read_b64_tr_b16 a[26:27], v33 offset:13920\n" \
"s_waitcnt vmcnt(13)\n" \
"v_cmp_ne_u32_e32 vcc, s62, v47\n" \
"v_mfma_f32_16x16x32_bf16 v[240:243], a[28:31], a[116:119], v[240:243]\n" \
"v_mfma_f32_16x16x32_bf16 v[244:247], a[28:31], a[124:127], v[244:247]\n" \
"buffer_load_dwordx4 a[144:147], v32, s[56:59], s70 offen offset:0 sc1\n" \
"v_mfma_f32_16x16x32_bf16 v[176:179], a[192:195], a[100:103], v[176:179]\n" \
"ds_read_b64_tr_b16 a[28:29], v33 offset:9344\n" \
"ds_read_b64_tr_b16 a[30:31], v33 offset:13952\n" \
"v_mfma_f32_16x16x32_bf16 v[180:183], a[192:195], a[108:111], v[180:183]\n" \
"s_cbranch_vccnz L_sb_%=\n" \
"L_rb_%=:\n" \
"v_readlane_b32 s73, v11, 51\n" \
"v_mfma_f32_16x16x32_bf16 v[248:251], a[192:195], a[116:119], v[248:251]\n" \
"v_cmp_eq_u32_e32 vcc, 0, v44\n" \
"v_mfma_f32_16x16x32_bf16 v[252:255], a[192:195], a[124:127], v[252:255]\n" \
"ds_read_b64_tr_b16 a[192:193], v33 offset:9376\n" \
"buffer_load_dwordx4 a[132:135], v20, s[56:59], s70 offen offset:1024 sc1\n" \
"s_waitcnt lgkmcnt(14)\n" \
"v_mfma_f32_16x16x32_bf16 v[112:115], a[196:199], a[64:67], v[112:115]\n" \
"ds_read_b64_tr_b16 a[194:195], v33 offset:13984\n" \
"v_cndmask_b32_e32 v50, 0, v45, vcc\n" \
"v_mfma_f32_16x16x32_bf16 v[116:119], a[196:199], a[80:83], v[116:119]\n" \
"ds_read_b64_tr_b16 a[196:197], v33 offset:9408\n" \
"ds_read_b64_tr_b16 a[198:199], v33 offset:14016\n" \
"v_mfma_f32_16x16x32_bf16 v[120:123], a[200:203], a[64:67], v[120:123]\n" \
"v_cmp_eq_u32_e32 vcc, 1, v44\n" \
"v_and_b32_e32 v44, 0x3c0, v20\n" \
"v_mfma_f32_16x16x32_bf16 v[124:127], a[200:203], a[80:83], v[124:127]\n" \
"buffer_load_dwordx4 a[148:151], v32, s[56:59], s70 offen offset:1024 sc1\n" \
"v_mfma_f32_16x16x32_bf16 v[128:131], a[204:207], a[64:67], v[128:131]\n" \
"ds_read_b64_tr_b16 a[200:201], v33 offset:9440\n" \
"ds_read_b64_tr_b16 a[202:203], v33 offset:14048\n" \
"v_mfma_f32_16x16x32_bf16 v[132:135], a[204:207], a[80:83], v[132:135]\n" \
"ds_read_b64_tr_b16 a[204:205], v33 offset:9472\n" \
"ds_read_b64_tr_b16 a[206:207], v33 offset:14080\n" \
"v_mfma_f32_16x16x32_bf16 v[136:139], a[208:211], a[64:67], v[136:139]\n" \
"v_cndmask_b32_e32 v51, 0, v45, vcc\n" \
"v_bfe_u32 v45, v20, 1, 5\n" \
"v_mfma_f32_16x16x32_bf16 v[140:143], a[208:211], a[80:83], v[140:143]\n" \
"ds_read_b64_tr_b16 a[208:209], v33 offset:18432\n" \
"ds_read_b64_tr_b16 a[210:211], v33 offset:23040\n" \
"buffer_load_dwordx4 a[136:139], v20, s[56:59], s70 offen offset:2048 sc1\n" \
"v_mfma_f32_16x16x32_bf16 v[144:147], a[212:215], a[64:67], v[144:147]\n" \
"v_and_b32_e32 v45, 24, v45\n" \
"v_add_u32_e32 v45, 0xf000, v45\n" \
"v_mfma_f32_16x16x32_bf16 v[148:151], a[212:215], a[80:83], v[148:151]\n" \
"ds_read_b64_tr_b16 a[212:213], v33 offset:18464\n" \
"ds_read_b64_tr_b16 a[214:215], v33 offset:23072\n" \
"v_mfma_f32_16x16x32_bf16 v[152:155], a[216:219], a[64:67], v[152:155]\n" \
"v_add_u32_e32 v36, v44, v45\n" \
".fill (L_ab + 24576 - .) / 4, 4, 0xBF800000\n" \
"L_ap6:\n" \
"s_cbranch_execz L_ap0\n" \
"v_mfma_f32_16x16x32_bf16 v[156:159], a[216:219], a[80:83], v[156:159]\n" \
"ds_read_b64_tr_b16 a[216:217], v33 offset:18496\n" \
"ds_read_b64_tr_b16 a[218:219], v33 offset:23104\n" \
"buffer_load_dwordx4 a[152:155], v32, s[56:59], s70 offen offset:2048 sc1\n" \
"v_mfma_f32_16x16x32_bf16 v[160:163], a[0:3], a[64:67], v[160:163]\n" \
"v_mfma_f32_16x16x32_bf16 v[164:167], a[0:3], a[80:83], v[164:167]\n" \
"ds_read_b64_tr_b16 a[0:1], v33 offset:18528\n" \
"s_waitcnt lgkmcnt(13)\n" \
"v_mfma_f32_16x16x32_bf16 v[168:171], a[4:7], a[64:67], v[168:171]\n" \
"ds_read_b64_tr_b16 a[2:3], v33 offset:23136\n" \
"v_mfma_f32_16x16x32_bf16 v[172:175], a[4:7], a[80:83], v[172:175]\n" \
"ds_read_b64_tr_b16 a[4:5], v33 offset:18560\n" \
"ds_read_b64_tr_b16 a[6:7], v33 offset:23168\n" \
"buffer_load_dwordx4 a[140:143], v20, s[56:59], s70 offen offset:3072 sc1\n" \
"v_mfma_f32_16x16x32_bf16 v[176:179], a[8:11], a[64:67], v[176:179]\n" \
"v_mfma_f32_16x16x32_bf16 v[180:183], a[8:11], a[80:83], v[180:183]\n" \
"ds_read_b64_tr_b16 a[8:9], v33 offset:18592\n" \
"ds_read_b64_tr_b16 a[10:11], v33 offset:23200\n" \
"v_mfma_f32_16x16x32_bf16 v[112:115], a[12:15], a[68:71], v[112:115]\n" \
"v_mfma_f32_16x16x32_bf16 v[116:119], a[12:15], a[84:87], v[116:119]\n" \
"ds_read_b64_tr_b16 a[12:13], v33 offset:18624\n" \
"ds_read_b64_tr_b16 a[14:15], v33 offset:23232\n" \
"buffer_load_dwordx4 a[156:159], v32, s[56:59], s70 offen offset:3072 sc1\n" \
"v_mfma_f32_16x16x32_bf16 v[120:123], a[16:19], a[68:71], v[120:123]\n" \
"v_readlane_b32 s70, v11, 48\n" \
"v_mfma_f32_16x16x32_bf16 v[124:127], a[16:19], a[84:87], v[124:127]\n" \
"ds_read_b64_tr_b16 a[16:17], v33 offset:18656\n" \
"ds_read_b64_tr_b16 a[18:19], v33 offset:23264\n" \
"v_mfma_f32_16x16x32_bf16 v[128:131], a[20:23], a[68:71], v[128:131]\n" \
"v_mfma_f32_16x16x32_bf16 v[132:135], a[20:23], a[84:87], v[132:135]\n" \
"ds_read_b64_tr_b16 a[20:21], v33 offset:18688\n" \
"ds_read_b64_tr_b16 a[22:23], v33 offset:23296\n" \
"buffer_load_dwordx4 a[32:35], v20, s[56:59], s71 offen offset:0 sc1\n" \
"v_mfma_f32_16x16x32_bf16 v[136:139], a[24:27], a[68:71], v[136:139]\n" \
"v_mfma_f32_16x16x32_bf16 v[140:143], a[24:27], a[84:87], v[140:143]\n" \
"ds_read_b64_tr_b16 a[24:25], v33 offset:27648\n" \
"ds_read_b64_tr_b16 a[26:27], v33 offset:32256\n" \
"v_mfma_f32_16x16x32_bf16 v[144:147], a[28:31], a[68:71], v[144:147]\n" \
"v_mfma_f32_16x16x32_bf16 v[148:151], a[28:31], a[84:87], v[148:151]\n" \
"ds_read_b64_tr_b16 a[28:29], v33 offset:27680\n" \
"ds_read_b64_tr_b16 a[30:31], v33 offset:32288\n" \
"buffer_load_dwordx4 a[48:51], v32, s[56:59], s71 offen offset:0 sc1\n" \
"v_mfma_f32_16x16x32_bf16 v[152:155], a[192:195], a[68:71], v[152:155]\n" \
"v_mfma_f32_16x16x32_bf16 v[156:159], a[192:195], a[84:87], v[156:159]\n" \
"ds_read_b64_tr_b16 a[192:193], v33 offset:27712\n" \
"s_waitcnt lgkmcnt(13)\n" \
"v_mfma_f32_16x16x32_bf16 v[160:163], a[196:199], a[68:71], v[160:163]\n" \
"ds_read_b64_tr_b16 a[194:195], v33 offset:32320\n" \
"v_mfma_f32_16x16x32_bf16 v[164:167], a[196:199], a[84:87], v[164:167]\n" \
"ds_read_b64_tr_b16 a[196:197], v33 offset:27744\n" \
"ds_read_b64_tr_b16 a[198:199], v33 offset:32352\n" \
"buffer_load_dwordx4 a[36:39], v20, s[56:59], s71 offen offset:1024 sc1\n" \
"v_mfma_f32_16x16x32_bf16 v[168:171], a[200:203], a[68:71], v[168:171]\n" \
"v_mfma_f32_16x16x32_bf16 v[172:175], a[200:203], a[84:87], v[172:175]\n" \
"ds_read_b64_tr_b16 a[200:201], v33 offset:27776\n" \
"ds_read_b64_tr_b16 a[202:203], v33 offset:32384\n" \
"v_mfma_f32_16x16x32_bf16 v[176:179], a[204:207], a[68:71], v[176:179]\n" \
"v_mfma_f32_16x16x32_bf16 v[180:183], a[204:207], a[84:87], v[180:183]\n" \
"ds_read_b64_tr_b16 a[204:205], v33 offset:27808\n" \
"ds_read_b64_tr_b16 a[206:207], v33 offset:32416\n" \
"buffer_load_dwordx4 a[52:55], v32, s[56:59], s71 offen offset:1024 sc1\n" \
"v_mfma_f32_16x16x32_bf16 v[112:115], a[208:211], a[72:75], v[112:115]\n" \
"v_mfma_f32_16x16x32_bf16 v[116:119], a[208:211], a[88:91], v[116:119]\n" \
"ds_read_b64_tr_b16 a[208:209], v33 offset:27840\n" \
"ds_read_b64_tr_b16 a[210:211], v33 offset:32448\n" \
"v_mfma_f32_16x16x32_bf16 v[120:123], a[212:215], a[72:75], v[120:123]\n" \
"v_mfma_f32_16x16x32_bf16 v[124:127], a[212:215], a[88:91], v[124:127]\n" \
"ds_read_b64_tr_b16 a[212:213], v33 offset:27872\n" \
"ds_read_b64_tr_b16 a[214:215], v33 offset:32480\n" \
"buffer_load_dwordx4 a[40:43], v20, s[56:59], s71 offen offset:2048 sc1\n" \
"v_mfma_f32_16x16x32_bf16 v[128:131], a[216:219], a[72:75], v[128:131]\n" \
"v_mfma_f32_16x16x32_bf16 v[132:135], a[216:219], a[88:91], v[132:135]\n" \
"ds_read_b64_tr_b16 a[216:217], v33 offset:27904\n" \
"ds_read_b64_tr_b16 a[218:219], v33 offset:32512\n" \
"v_mfma_f32_16x16x32_bf16 v[136:139], a[0:3], a[72:75], v[136:139]\n" \
"v_mfma_f32_16x16x32_bf16 v[140:143], a[0:3], a[88:91], v[140:143]\n" \
"v_mfma_f32_16x16x32_bf16 v[144:147], a[4:7], a[72:75], v[144:147]\n" \
"buffer_load_dwordx4 a[56:59], v32, s[56:59], s71 offen offset:2048 sc1\n" \
"v_mfma_f32_16x16x32_bf16 v[148:151], a[4:7], a[88:91], v[148:151]\n" \
"s_waitcnt lgkmcnt(10)\n" \
"v_mfma_f32_16x16x32_bf16 v[152:155], a[8:11], a[72:75], v[152:155]\n" \
"v_mfma_f32_16x16x32_bf16 v[156:159], a[8:11], a[88:91], v[156:159]\n" \
"v_mfma_f32_16x16x32_bf16 v[160:163], a[12:15], a[72:75], v[160:163]\n" \
"v_mfma_f32_16x16x32_bf16 v[164:167], a[12:15], a[88:91], v[164:167]\n" \
"buffer_load_dwordx4 a[44:47], v20, s[56:59], s71 offen offset:3072 sc1\n" \
"v_mfma_f32_16x16x32_bf16 v[168:171], a[16:19], a[72:75], v[168:171]\n" \
"v_mfma_f32_16x16x32_bf16 v[172:175], a[16:19], a[88:91], v[172:175]\n" \
"v_mfma_f32_16x16x32_bf16 v[176:179], a[20:23], a[72:75], v[176:179]\n" \
"v_mfma_f32_16x16x32_bf16 v[180:183], a[20:23], a[88:91], v[180:183]\n" \
"buffer_load_dwordx4 a[60:63], v32, s[56:59], s71 offen offset:3072 sc1\n" \
"v_readlane_b32 s71, v11, 49\n" \
"buffer_load_dwordx4 a[96:99], v20, s[56:59], s66 offen offset:0 sc1\n" \
"buffer_load_dwordx4 a[112:115], v32, s[56:59], s66 offen offset:0 sc1\n" \
"s_waitcnt vmcnt(20)\n" \
"s_barrier\n" \
"ds_read_b64_tr_b16 a[0:1], v33 offset:36864\n" \
"ds_read_b64_tr_b16 a[2:3], v33 offset:41472\n" \
"ds_read_b64_tr_b16 a[4:5], v33 offset:46080\n" \
"ds_read_b64_tr_b16 a[6:7], v33 offset:50688\n" \
"ds_read_b64_tr_b16 a[8:9], v33 offset:55296\n" \
"ds_read_b64_tr_b16 a[10:11], v33 offset:59904\n" \
"ds_read_b64_tr_b16 a[12:13], v10 offset:0\n" \
"ds_read_b64_tr_b16 a[14:15], v10 offset:4608\n" \
"ds_read_b64_tr_b16 a[16:17], v34 offset:0\n" \
"ds_read_b64_tr_b16 a[18:19], v34 offset:4608\n" \
"ds_read_b64_tr_b16 a[20:21], v34 offset:9216\n" \
"ds_read_b64_tr_b16 a[22:23], v34 offset:13824\n" \
"buffer_load_dwordx4 a[100:103], v20, s[56:59], s66 offen offset:1024 sc1\n" \
"s_waitcnt vmcnt(20)\n" \
"v_mfma_f32_16x16x32_bf16 v[112:115], a[24:27], a[76:79], v[112:115]\n" \
"s_waitcnt vmcnt(19)\n" \
"v_mfma_f32_16x16x32_bf16 v[116:119], a[24:27], a[92:95], v[116:119]\n" \
"ds_read_b64_tr_b16 a[24:25], v34 offset:18432\n" \
"ds_read_b64_tr_b16 a[26:27], v34 offset:23040\n" \
"v_mfma_f32_16x16x32_bf16 v[120:123], a[28:31], a[76:79], v[120:123]\n" \
"buffer_load_dwordx4 a[116:119], v32, s[56:59], s66 offen offset:1024 sc1\n" \
"v_mfma_f32_16x16x32_bf16 v[124:127], a[28:31], a[92:95], v[124:127]\n" \
"ds_read_b64_tr_b16 a[28:29], v10 offset:9216\n" \
"ds_read_b64_tr_b16 a[30:31], v10 offset:13824\n" \
"v_mfma_f32_16x16x32_bf16 v[128:131], a[192:195], a[76:79], v[128:131]\n" \
"v_mfma_f32_16x16x32_bf16 v[132:135], a[192:195], a[92:95], v[132:135]\n" \
"ds_read_b64_tr_b16 a[192:193], v33 offset:36896\n" \
"ds_read_b64_tr_b16 a[194:195], v33 offset:41504\n" \
"v_mfma_f32_16x16x32_bf16 v[136:139], a[196:199], a[76:79], v[136:139]\n" \
"v_mfma_f32_16x16x32_bf16 v[140:143], a[196:199], a[92:95], v[140:143]\n" \
"ds_read_b64_tr_b16 a[196:197], v33 offset:46112\n" \
"s_waitcnt lgkmcnt(15)\n" \
"v_mfma_f32_16x16x32_bf16 v[144:147], a[200:203], a[76:79], v[144:147]\n" \
"buffer_load_dwordx4 a[104:107], v20, s[56:59], s66 offen offset:2048 sc1\n" \
"v_mfma_f32_16x16x32_bf16 v[148:151], a[200:203], a[92:95], v[148:151]\n" \
"ds_read_b64_tr_b16 a[198:199], v33 offset:50720\n" \
"ds_read_b64_tr_b16 a[200:201], v33 offset:55328\n" \
"v_mfma_f32_16x16x32_bf16 v[152:155], a[204:207], a[76:79], v[152:155]\n" \
"ds_read_b64_tr_b16 a[202:203], v33 offset:59936\n" \
"v_mfma_f32_16x16x32_bf16 v[156:159], a[204:207], a[92:95], v[156:159]\n" \
"ds_read_b64_tr_b16 a[204:205], v10 offset:32\n" \
"ds_read_b64_tr_b16 a[206:207], v10 offset:4640\n" \
"v_mfma_f32_16x16x32_bf16 v[160:163], a[208:211], a[76:79], v[160:163]\n" \
"v_mfma_f32_16x16x32_bf16 v[164:167], a[208:211], a[92:95], v[164:167]\n" \
"ds_read_b64_tr_b16 a[208:209], v34 offset:32\n" \
"ds_read_b64_tr_b16 a[210:211], v34 offset:4640\n" \
"v_mfma_f32_16x16x32_bf16 v[168:171], a[212:215], a[76:79], v[168:171]\n" \
"buffer_load_dwordx4 a[120:123], v32, s[56:59], s66 offen offset:2048 sc1\n" \
"v_mfma_f32_16x16x32_bf16 v[172:175], a[212:215], a[92:95], v[172:175]\n" \
"ds_read_b64_tr_b16 a[212:213], v34 offset:9248\n" \
"ds_read_b64_tr_b16 a[214:215], v34 offset:13856\n" \
"v_mfma_f32_16x16x32_bf16 v[176:179], a[216:219], a[76:79], v[176:179]\n" \
"v_mfma_f32_16x16x32_bf16 v[180:183], a[216:219], a[92:95], v[180:183]\n" \
"ds_read_b64_tr_b16 a[216:217], v34 offset:18464\n" \
"s_waitcnt vmcnt(21)\n" \
"v_mfma_f32_16x16x32_bf16 v[112:115], a[0:3], a[128:131], v[112:115]\n" \
"ds_read_b64_tr_b16 a[218:219], v34 offset:23072\n" \
"s_waitcnt vmcnt(20)\n" \
"v_mfma_f32_16x16x32_bf16 v[116:119], a[0:3], a[144:147], v[116:119]\n" \
"ds_read_b64_tr_b16 a[0:1], v10 offset:9248\n" \
"s_waitcnt vmcnt(19)\n" \
"v_mfma_f32_16x16x32_bf16 v[112:115], a[4:7], a[132:135], v[112:115]\n" \
"ds_read_b64_tr_b16 a[2:3], v10 offset:13856\n" \
"s_waitcnt vmcnt(18)\n" \
"v_mfma_f32_16x16x32_bf16 v[116:119], a[4:7], a[148:151], v[116:119]\n" \
"ds_read_b64_tr_b16 a[4:5], v33 offset:36928\n" \
"ds_read_b64_tr_b16 a[6:7], v33 offset:41536\n" \
"buffer_load_dwordx4 a[108:111], v20, s[56:59], s66 offen offset:3072 sc1\n" \
"s_waitcnt vmcnt(18) lgkmcnt(15)\n" \
"v_mfma_f32_16x16x32_bf16 v[112:115], a[8:11], a[136:139], v[112:115]\n" \
"buffer_load_dwordx4 a[124:127], v32, s[56:59], s66 offen offset:3072 sc1\n" \
"s_waitcnt vmcnt(18)\n" \
"v_mfma_f32_16x16x32_bf16 v[116:119], a[8:11], a[152:155], v[116:119]\n" \
"ds_read_b64_tr_b16 a[8:9], v33 offset:46144\n" \
"ds_read_b64_tr_b16 a[10:11], v33 offset:50752\n" \
"buffer_load_dwordx4 a[160:163], v20, s[56:59], s92 offen offset:0 sc1\n" \
"s_waitcnt vmcnt(18)\n" \
"v_mfma_f32_16x16x32_bf16 v[112:115], a[12:15], a[140:143], v[112:115]\n" \
"buffer_load_dwordx4 a[176:179], v32, s[56:59], s92 offen offset:0 sc1\n" \
"s_waitcnt vmcnt(18)\n" \
"v_mfma_f32_16x16x32_bf16 v[116:119], a[12:15], a[156:159], v[116:119]\n" \
"ds_read_b64_tr_b16 a[12:13], v33 offset:55360\n" \
"ds_read_b64_tr_b16 a[14:15], v33 offset:59968\n" \
"buffer_load_dwordx4 a[164:167], v20, s[56:59], s92 offen offset:1024 sc1\n" \
"s_waitcnt vmcnt(18)\n" \
"v_mfma_f32_16x16x32_bf16 v[112:115], a[16:19], a[32:35], v[112:115]\n" \
"buffer_load_dwordx4 a[180:183], v32, s[56:59], s92 offen offset:1024 sc1\n" \
"s_waitcnt vmcnt(18)\n" \
"v_mfma_f32_16x16x32_bf16 v[116:119], a[16:19], a[48:51], v[116:119]\n" \
"ds_read_b64_tr_b16 a[16:17], v10 offset:64\n" \
"ds_read_b64_tr_b16 a[18:19], v10 offset:4672\n" \
"buffer_load_dwordx4 a[168:171], v20, s[56:59], s92 offen offset:2048 sc1\n" \
"s_waitcnt vmcnt(18)\n" \
"v_mfma_f32_16x16x32_bf16 v[112:115], a[20:23], a[36:39], v[112:115]\n" \
"buffer_load_dwordx4 a[184:187], v32, s[56:59], s92 offen offset:2048 sc1\n" \
"s_waitcnt vmcnt(18)\n" \
"v_mfma_f32_16x16x32_bf16 v[116:119], a[20:23], a[52:55], v[116:119]\n" \
"ds_read_b64_tr_b16 a[20:21], v34 offset:64\n" \
"ds_read_b64_tr_b16 a[22:23], v34 offset:4672\n" \
"buffer_load_dwordx4 a[172:175], v20, s[56:59], s92 offen offset:3072 sc1\n" \
"s_waitcnt vmcnt(18)\n" \
"v_mfma_f32_16x16x32_bf16 v[112:115], a[24:27], a[40:43], v[112:115]\n" \
"buffer_load_dwordx4 a[188:191], v32, s[56:59], s92 offen offset:3072 sc1\n" \
"v_readlane_b32 s92, v11, 52\n" \
"s_waitcnt vmcnt(18)\n" \
"v_mfma_f32_16x16x32_bf16 v[116:119], a[24:27], a[56:59], v[116:119]\n" \
"ds_read_b64_tr_b16 a[24:25], v34 offset:9280\n" \
"ds_read_b64_tr_b16 a[26:27], v34 offset:13888\n" \
"buffer_load_dwordx4 a[220:223], v20, s[56:59], s95 offen offset:0 sc1\n" \
"s_waitcnt vmcnt(18)\n" \
"v_mfma_f32_16x16x32_bf16 v[112:115], a[28:31], a[44:47], v[112:115]\n" \
"buffer_load_dwordx4 a[236:239], v32, s[56:59], s95 offen offset:0 sc1\n" \
"s_waitcnt vmcnt(18)\n" \
"v_mfma_f32_16x16x32_bf16 v[116:119], a[28:31], a[60:63], v[116:119]\n" \
"ds_read_b64_tr_b16 a[28:29], v34 offset:18496\n" \
"ds_read_b64_tr_b16 a[30:31], v34 offset:23104\n" \
"v_mfma_f32_16x16x32_bf16 v[120:123], a[192:195], a[128:131], v[120:123]\n" \
"buffer_load_dwordx4 a[224:227], v20, s[56:59], s95 offen offset:1024 sc1\n" \
"v_mfma_f32_16x16x32_bf16 v[124:127], a[192:195], a[144:147], v[124:127]\n" \
"v_cvt_pk_bf16_f32 v112, v112, v113\n" \
"v_cvt_pk_bf16_f32 v113, v114, v115\n" \
"s_waitcnt lgkmcnt(4)\n" \
"v_mfma_f32_16x16x32_bf16 v[120:123], a[196:199], a[132:135], v[120:123]\n" \
"v_cvt_pk_bf16_f32 v114, v116, v117\n" \
"v_cvt_pk_bf16_f32 v115, v118, v119\n" \
"buffer_store_dwordx4 v[112:115], v20, s[56:59], s67 offen offset:0 sc1\n" \
"s_add_u32 s67, s67, 0x3800\n" \
"v_mfma_f32_16x16x32_bf16 v[124:127], a[196:199], a[148:151], v[124:127]\n" \
"ds_read_b64_tr_b16 a[192:193], v10 offset:9280\n" \
"ds_read_b64_tr_b16 a[194:195], v10 offset:13888\n" \
"v_mfma_f32_16x16x32_bf16 v[120:123], a[200:203], a[136:139], v[120:123]\n" \
"ds_read_b64_tr_b16 a[196:197], v33 offset:36960\n" \
"ds_read_b64_tr_b16 a[198:199], v33 offset:41568\n" \
"v_mfma_f32_16x16x32_bf16 v[124:127], a[200:203], a[152:155], v[124:127]\n" \
"ds_read_b64_tr_b16 a[200:201], v33 offset:46176\n" \
"ds_read_b64_tr_b16 a[202:203], v33 offset:50784\n" \
"v_mfma_f32_16x16x32_bf16 v[120:123], a[204:207], a[140:143], v[120:123]\n" \
"buffer_load_dwordx4 a[240:243], v32, s[56:59], s95 offen offset:1024 sc1\n" \
"v_mfma_f32_16x16x32_bf16 v[124:127], a[204:207], a[156:159], v[124:127]\n" \
"ds_read_b64_tr_b16 a[204:205], v33 offset:55392\n" \
"ds_read_b64_tr_b16 a[206:207], v33 offset:60000\n" \
"v_mfma_f32_16x16x32_bf16 v[120:123], a[208:211], a[32:35], v[120:123]\n" \
"v_mfma_f32_16x16x32_bf16 v[124:127], a[208:211], a[48:51], v[124:127]\n" \
"ds_read_b64_tr_b16 a[208:209], v10 offset:96\n" \
"ds_read_b64_tr_b16 a[210:211], v10 offset:4704\n" \
"v_mfma_f32_16x16x32_bf16 v[120:123], a[212:215], a[36:39], v[120:123]\n" \
"v_mfma_f32_16x16x32_bf16 v[124:127], a[212:215], a[52:55], v[124:127]\n" \
"ds_read_b64_tr_b16 a[212:213], v34 offset:96\n" \
"ds_read_b64_tr_b16 a[214:215], v34 offset:4704\n" \
"v_mfma_f32_16x16x32_bf16 v[120:123], a[216:219], a[40:43], v[120:123]\n" \
"buffer_load_dwordx4 a[228:231], v20, s[56:59], s95 offen offset:2048 sc1\n" \
"v_mfma_f32_16x16x32_bf16 v[124:127], a[216:219], a[56:59], v[124:127]\n" \
"ds_read_b64_tr_b16 a[216:217], v34 offset:9312\n" \
"ds_read_b64_tr_b16 a[218:219], v34 offset:13920\n" \
"v_mfma_f32_16x16x32_bf16 v[120:123], a[0:3], a[44:47], v[120:123]\n" \
"v_mfma_f32_16x16x32_bf16 v[124:127], a[0:3], a[60:63], v[124:127]\n" \
"ds_read_b64_tr_b16 a[0:1], v34 offset:18528\n" \
"ds_read_b64_tr_b16 a[2:3], v34 offset:23136\n" \
"v_mfma_f32_16x16x32_bf16 v[128:131], a[4:7], a[128:131], v[128:131]\n" \
"v_mfma_f32_16x16x32_bf16 v[132:135], a[4:7], a[144:147], v[132:135]\n" \
"ds_read_b64_tr_b16 a[4:5], v10 offset:9312\n" \
"ds_read_b64_tr_b16 a[6:7], v10 offset:13920\n" \
"v_mfma_f32_16x16x32_bf16 v[128:131], a[8:11], a[132:135], v[128:131]\n" \
"v_cvt_pk_bf16_f32 v120, v120, v121\n" \
"v_cvt_pk_bf16_f32 v121, v122, v123\n" \
"v_cvt_pk_bf16_f32 v122, v124, v125\n" \
"v_cvt_pk_bf16_f32 v123, v126, v127\n" \
"buffer_store_dwordx4 v[120:123], v20, s[56:59], s68 offen offset:0 sc1\n" \
"s_add_u32 s68, s68, 0x3000\n" \
"v_mfma_f32_16x16x32_bf16 v[132:135], a[8:11], a[148:151], v[132:135]\n" \
"ds_read_b64_tr_b16 a[8:9], v33 offset:36992\n" \
"ds_read_b64_tr_b16 a[10:11], v33 offset:41600\n" \
"v_mfma_f32_16x16x32_bf16 v[128:131], a[12:15], a[136:139], v[128:131]\n" \
"v_mfma_f32_16x16x32_bf16 v[132:135], a[12:15], a[152:155], v[132:135]\n" \
"ds_read_b64_tr_b16 a[12:13], v33 offset:46208\n" \
"ds_read_b64_tr_b16 a[14:15], v33 offset:50816\n" \
"v_mfma_f32_16x16x32_bf16 v[128:131], a[16:19], a[140:143], v[128:131]\n" \
"buffer_load_dwordx4 a[244:247], v32, s[56:59], s95 offen offset:2048 sc1\n" \
"v_mfma_f32_16x16x32_bf16 v[132:135], a[16:19], a[156:159], v[132:135]\n" \
"ds_read_b64_tr_b16 a[16:17], v33 offset:55424\n" \
"ds_read_b64_tr_b16 a[18:19], v33 offset:60032\n" \
"v_mfma_f32_16x16x32_bf16 v[128:131], a[20:23], a[32:35], v[128:131]\n" \
"v_mfma_f32_16x16x32_bf16 v[132:135], a[20:23], a[48:51], v[132:135]\n" \
"ds_read_b64_tr_b16 a[20:21], v10 offset:128\n" \
"s_waitcnt lgkmcnt(13)\n" \
"v_mfma_f32_16x16x32_bf16 v[128:131], a[24:27], a[36:39], v[128:131]\n" \
"ds_read_b64_tr_b16 a[22:23], v10 offset:4736\n" \
"v_mfma_f32_16x16x32_bf16 v[132:135], a[24:27], a[52:55], v[132:135]\n" \
"buffer_load_dwordx4 a[232:235], v20, s[56:59], s95 offen offset:3072 sc1\n" \
"v_mfma_f32_16x16x32_bf16 v[128:131], a[28:31], a[40:43], v[128:131]\n" \
"ds_read_b64_tr_b16 a[24:25], v34 offset:128\n" \
"ds_read_b64_tr_b16 a[26:27], v34 offset:4736\n" \
"v_mfma_f32_16x16x32_bf16 v[132:135], a[28:31], a[56:59], v[132:135]\n" \
"ds_read_b64_tr_b16 a[28:29], v34 offset:9344\n" \
"ds_read_b64_tr_b16 a[30:31], v34 offset:13952\n" \
"v_mfma_f32_16x16x32_bf16 v[128:131], a[192:195], a[44:47], v[128:131]\n" \
"v_mfma_f32_16x16x32_bf16 v[132:135], a[192:195], a[60:63], v[132:135]\n" \
"ds_read_b64_tr_b16 a[192:193], v34 offset:18560\n" \
"ds_read_b64_tr_b16 a[194:195], v34 offset:23168\n" \
"v_mfma_f32_16x16x32_bf16 v[136:139], a[196:199], a[128:131], v[136:139]\n" \
"buffer_load_dwordx4 a[248:251], v32, s[56:59], s95 offen offset:3072 sc1\n" \
"v_mfma_f32_16x16x32_bf16 v[140:143], a[196:199], a[144:147], v[140:143]\n" \
"ds_read_b64_tr_b16 a[196:197], v10 offset:9344\n" \
"ds_read_b64_tr_b16 a[198:199], v10 offset:13952\n" \
"v_cvt_pk_bf16_f32 v128, v128, v129\n" \
"v_cvt_pk_bf16_f32 v129, v130, v131\n" \
"v_cvt_pk_bf16_f32 v130, v132, v133\n" \
"v_cvt_pk_bf16_f32 v131, v134, v135\n" \
"buffer_store_dwordx4 v[128:131], v20, s[56:59], s69 offen offset:0 sc1\n" \
"s_add_u32 s69, s69, 0x2800\n" \
"v_mfma_f32_16x16x32_bf16 v[136:139], a[200:203], a[132:135], v[136:139]\n" \
"v_readlane_b32 s95, v11, 46\n" \
"v_mfma_f32_16x16x32_bf16 v[140:143], a[200:203], a[148:151], v[140:143]\n" \
"ds_read_b64_tr_b16 a[200:201], v33 offset:37024\n" \
"ds_read_b64_tr_b16 a[202:203], v33 offset:41632\n" \
"v_mfma_f32_16x16x32_bf16 v[136:139], a[204:207], a[136:139], v[136:139]\n" \
"v_mfma_f32_16x16x32_bf16 v[140:143], a[204:207], a[152:155], v[140:143]\n" \
"ds_read_b64_tr_b16 a[204:205], v33 offset:46240\n" \
"ds_read_b64_tr_b16 a[206:207], v33 offset:50848\n" \
"v_mfma_f32_16x16x32_bf16 v[136:139], a[208:211], a[140:143], v[136:139]\n" \
"buffer_load_dwordx4 v[64:67], v20, s[56:59], s97 offen offset:0 sc1\n" \
"v_mfma_f32_16x16x32_bf16 v[140:143], a[208:211], a[156:159], v[140:143]\n" \
"ds_read_b64_tr_b16 a[208:209], v33 offset:55456\n" \
"ds_read_b64_tr_b16 a[210:211], v33 offset:60064\n" \
"v_mfma_f32_16x16x32_bf16 v[136:139], a[212:215], a[32:35], v[136:139]\n" \
"v_mfma_f32_16x16x32_bf16 v[140:143], a[212:215], a[48:51], v[140:143]\n" \
"ds_read_b64_tr_b16 a[212:213], v10 offset:160\n" \
"s_waitcnt lgkmcnt(12)\n" \
"v_mfma_f32_16x16x32_bf16 v[136:139], a[216:219], a[36:39], v[136:139]\n" \
"ds_read_b64_tr_b16 a[214:215], v10 offset:4768\n" \
"v_mfma_f32_16x16x32_bf16 v[140:143], a[216:219], a[52:55], v[140:143]\n" \
"ds_read_b64_tr_b16 a[216:217], v34 offset:160\n" \
"ds_read_b64_tr_b16 a[218:219], v34 offset:4768\n" \
"v_mfma_f32_16x16x32_bf16 v[136:139], a[0:3], a[40:43], v[136:139]\n" \
"buffer_load_dwordx4 v[68:71], v20, s[56:59], s97 offen offset:1024 sc1\n" \
"v_mfma_f32_16x16x32_bf16 v[140:143], a[0:3], a[56:59], v[140:143]\n" \
"ds_read_b64_tr_b16 a[0:1], v34 offset:9376\n" \
"ds_read_b64_tr_b16 a[2:3], v34 offset:13984\n" \
"v_mfma_f32_16x16x32_bf16 v[136:139], a[4:7], a[44:47], v[136:139]\n" \
"v_mfma_f32_16x16x32_bf16 v[140:143], a[4:7], a[60:63], v[140:143]\n" \
"ds_read_b64_tr_b16 a[4:5], v34 offset:18592\n" \
"ds_read_b64_tr_b16 a[6:7], v34 offset:23200\n" \
"v_mfma_f32_16x16x32_bf16 v[144:147], a[8:11], a[128:131], v[144:147]\n" \
"v_mfma_f32_16x16x32_bf16 v[148:151], a[8:11], a[144:147], v[148:151]\n" \
"ds_read_b64_tr_b16 a[8:9], v10 offset:9376\n" \
"ds_read_b64_tr_b16 a[10:11], v10 offset:13984\n" \
"v_mfma_f32_16x16x32_bf16 v[144:147], a[12:15], a[132:135], v[144:147]\n" \
"v_cvt_pk_bf16_f32 v136, v136, v137\n" \
"v_cvt_pk_bf16_f32 v137, v138, v139\n" \
"v_cvt_pk_bf16_f32 v138, v140, v141\n" \
"v_cvt_pk_bf16_f32 v139, v142, v143\n" \
"buffer_store_dwordx4 v[136:139], v20, s[56:59], s70 offen offset:0 sc1\n" \
"s_add_u32 s70, s70, 0x2000\n" \
"v_mfma_f32_16x16x32_bf16 v[148:151], a[12:15], a[148:151], v[148:151]\n" \
"ds_read_b64_tr_b16 a[12:13], v33 offset:37056\n" \
"ds_read_b64_tr_b16 a[14:15], v33 offset:41664\n" \
"v_mfma_f32_16x16x32_bf16 v[144:147], a[16:19], a[136:139], v[144:147]\n" \
"v_mfma_f32_16x16x32_bf16 v[148:151], a[16:19], a[152:155], v[148:151]\n" \
"ds_read_b64_tr_b16 a[16:17], v33 offset:46272\n" \
"ds_read_b64_tr_b16 a[18:19], v33 offset:50880\n" \
"v_mfma_f32_16x16x32_bf16 v[144:147], a[20:23], a[140:143], v[144:147]\n" \
"buffer_load_dwordx4 v[72:75], v32, s[56:59], s97 offen offset:0 sc1\n" \
"v_mfma_f32_16x16x32_bf16 v[148:151], a[20:23], a[156:159], v[148:151]\n" \
"ds_read_b64_tr_b16 a[20:21], v33 offset:55488\n" \
"ds_read_b64_tr_b16 a[22:23], v33 offset:60096\n" \
"v_mfma_f32_16x16x32_bf16 v[144:147], a[24:27], a[32:35], v[144:147]\n" \
"v_mfma_f32_16x16x32_bf16 v[148:151], a[24:27], a[48:51], v[148:151]\n" \
"ds_read_b64_tr_b16 a[24:25], v10 offset:192\n" \
"s_waitcnt lgkmcnt(13)\n" \
"v_mfma_f32_16x16x32_bf16 v[144:147], a[28:31], a[36:39], v[144:147]\n" \
"ds_read_b64_tr_b16 a[26:27], v10 offset:4800\n" \
"v_mfma_f32_16x16x32_bf16 v[148:151], a[28:31], a[52:55], v[148:151]\n" \
"ds_read_b64_tr_b16 a[28:29], v34 offset:192\n" \
"ds_read_b64_tr_b16 a[30:31], v34 offset:4800\n" \
"v_mfma_f32_16x16x32_bf16 v[144:147], a[192:195], a[40:43], v[144:147]\n" \
"buffer_load_dwordx4 v[76:79], v32, s[56:59], s97 offen offset:1024 sc1\n" \
"v_mfma_f32_16x16x32_bf16 v[148:151], a[192:195], a[56:59], v[148:151]\n" \
"ds_read_b64_tr_b16 a[192:193], v34 offset:9408\n" \
"ds_read_b64_tr_b16 a[194:195], v34 offset:14016\n" \
"v_mfma_f32_16x16x32_bf16 v[144:147], a[196:199], a[44:47], v[144:147]\n" \
"v_readlane_b32 s97, v11, 48\n" \
"v_mfma_f32_16x16x32_bf16 v[148:151], a[196:199], a[60:63], v[148:151]\n" \
"ds_read_b64_tr_b16 a[196:197], v34 offset:18624\n" \
"ds_read_b64_tr_b16 a[198:199], v34 offset:23232\n" \
"v_mfma_f32_16x16x32_bf16 v[152:155], a[200:203], a[128:131], v[152:155]\n" \
"v_mfma_f32_16x16x32_bf16 v[156:159], a[200:203], a[144:147], v[156:159]\n" \
"ds_read_b64_tr_b16 a[200:201], v10 offset:9408\n" \
"ds_read_b64_tr_b16 a[202:203], v10 offset:14016\n" \
"v_cvt_pk_bf16_f32 v144, v144, v145\n" \
"v_cvt_pk_bf16_f32 v145, v146, v147\n" \
"v_cvt_pk_bf16_f32 v146, v148, v149\n" \
"v_cvt_pk_bf16_f32 v147, v150, v151\n" \
"buffer_store_dwordx4 v[144:147], v20, s[56:59], s71 offen offset:0 sc1\n" \
"s_add_u32 s71, s71, 0x1800\n" \
"v_mfma_f32_16x16x32_bf16 v[152:155], a[204:207], a[132:135], v[152:155]\n" \
"v_mfma_f32_16x16x32_bf16 v[156:159], a[204:207], a[148:151], v[156:159]\n" \
"ds_read_b64_tr_b16 a[204:205], v33 offset:256\n" \
"ds_read_b64_tr_b16 a[206:207], v33 offset:4864\n" \
"v_mfma_f32_16x16x32_bf16 v[152:155], a[208:211], a[136:139], v[152:155]\n" \
"v_mfma_f32_16x16x32_bf16 v[156:159], a[208:211], a[152:155], v[156:159]\n" \
"ds_read_b64_tr_b16 a[208:209], v33 offset:9472\n" \
"ds_read_b64_tr_b16 a[210:211], v33 offset:14080\n" \
"v_mfma_f32_16x16x32_bf16 v[152:155], a[212:215], a[140:143], v[152:155]\n" \
"buffer_load_dwordx4 v[80:83], v20, s[56:59], s64 offen offset:0 sc1\n" \
"v_mfma_f32_16x16x32_bf16 v[156:159], a[212:215], a[156:159], v[156:159]\n" \
"ds_read_b64_tr_b16 a[212:213], v33 offset:18688\n" \
"ds_read_b64_tr_b16 a[214:215], v33 offset:23296\n" \
"v_mfma_f32_16x16x32_bf16 v[152:155], a[216:219], a[32:35], v[152:155]\n" \
"v_mfma_f32_16x16x32_bf16 v[156:159], a[216:219], a[48:51], v[156:159]\n" \
"ds_read_b64_tr_b16 a[216:217], v33 offset:27904\n" \
"s_waitcnt lgkmcnt(12)\n" \
"v_mfma_f32_16x16x32_bf16 v[152:155], a[0:3], a[36:39], v[152:155]\n" \
"ds_read_b64_tr_b16 a[218:219], v33 offset:32512\n" \
"v_mfma_f32_16x16x32_bf16 v[156:159], a[0:3], a[52:55], v[156:159]\n" \
"ds_read_b64_tr_b16 a[0:1], v33 offset:37120\n" \
"ds_read_b64_tr_b16 a[2:3], v33 offset:41728\n" \
"v_mfma_f32_16x16x32_bf16 v[152:155], a[4:7], a[40:43], v[152:155]\n" \
"buffer_load_dwordx4 v[84:87], v20, s[56:59], s64 offen offset:1024 sc1\n" \
"v_mfma_f32_16x16x32_bf16 v[156:159], a[4:7], a[56:59], v[156:159]\n" \
"ds_read_b64_tr_b16 a[4:5], v33 offset:46336\n" \
"ds_read_b64_tr_b16 a[6:7], v33 offset:50944\n" \
"v_mfma_f32_16x16x32_bf16 v[152:155], a[8:11], a[44:47], v[152:155]\n" \
"v_mfma_f32_16x16x32_bf16 v[156:159], a[8:11], a[60:63], v[156:159]\n" \
"ds_read_b64_tr_b16 a[8:9], v33 offset:55552\n" \
"ds_read_b64_tr_b16 a[10:11], v33 offset:60160\n" \
"v_mfma_f32_16x16x32_bf16 v[160:163], a[12:15], a[128:131], v[160:163]\n" \
"v_mfma_f32_16x16x32_bf16 v[164:167], a[12:15], a[144:147], v[164:167]\n" \
"ds_read_b64_tr_b16 a[12:13], v10 offset:256\n" \
"ds_read_b64_tr_b16 a[14:15], v10 offset:4864\n" \
"v_mfma_f32_16x16x32_bf16 v[160:163], a[16:19], a[132:135], v[160:163]\n" \
"v_cvt_pk_bf16_f32 v152, v152, v153\n" \
"v_cvt_pk_bf16_f32 v153, v154, v155\n" \
"v_cvt_pk_bf16_f32 v154, v156, v157\n" \
"v_cvt_pk_bf16_f32 v155, v158, v159\n" \
"buffer_store_dwordx4 v[152:155], v20, s[56:59], s72 offen offset:0 sc1\n" \
"s_add_u32 s72, s72, 0x1000\n" \
"v_mfma_f32_16x16x32_bf16 v[164:167], a[16:19], a[148:151], v[164:167]\n" \
"ds_read_b64_tr_b16 a[16:17], v34 offset:256\n" \
"ds_read_b64_tr_b16 a[18:19], v34 offset:4864\n" \
"v_mfma_f32_16x16x32_bf16 v[160:163], a[20:23], a[136:139], v[160:163]\n" \
"v_mfma_f32_16x16x32_bf16 v[164:167], a[20:23], a[152:155], v[164:167]\n" \
"ds_read_b64_tr_b16 a[20:21], v34 offset:9472\n" \
"ds_read_b64_tr_b16 a[22:23], v34 offset:14080\n" \
"v_mfma_f32_16x16x32_bf16 v[160:163], a[24:27], a[140:143], v[160:163]\n" \
"buffer_load_dwordx4 v[88:91], v32, s[56:59], s64 offen offset:0 sc1\n" \
"v_mfma_f32_16x16x32_bf16 v[164:167], a[24:27], a[156:159], v[164:167]\n" \
"ds_read_b64_tr_b16 a[24:25], v34 offset:18688\n" \
"ds_read_b64_tr_b16 a[26:27], v34 offset:23296\n" \
"v_mfma_f32_16x16x32_bf16 v[160:163], a[28:31], a[32:35], v[160:163]\n" \
"v_mfma_f32_16x16x32_bf16 v[164:167], a[28:31], a[48:51], v[164:167]\n" \
"ds_read_b64_tr_b16 a[28:29], v10 offset:9472\n" \
"s_waitcnt vmcnt(17)\n" \
"s_mov_b64 exec, 1\n" \
"buffer_store_dword v39, off, s[56:59], s67 offset:0 sc1\n" \
"s_mov_b64 exec, -1\n" \
"s_waitcnt lgkmcnt(11)\n" \
"v_mfma_f32_16x16x32_bf16 v[160:163], a[192:195], a[36:39], v[160:163]\n" \
"ds_read_b64_tr_b16 a[30:31], v10 offset:14080\n" \
"v_mfma_f32_16x16x32_bf16 v[164:167], a[192:195], a[52:55], v[164:167]\n" \
"ds_read_b64_tr_b16 a[192:193], v33 offset:0\n" \
"ds_read_b64_tr_b16 a[194:195], v33 offset:4608\n" \
"v_mfma_f32_16x16x32_bf16 v[160:163], a[196:199], a[40:43], v[160:163]\n" \
"v_mfma_f32_16x16x32_bf16 v[164:167], a[196:199], a[56:59], v[164:167]\n" \
"ds_read_b64_tr_b16 a[196:197], v33 offset:9216\n" \
"ds_read_b64_tr_b16 a[198:199], v33 offset:13824\n" \
"v_mfma_f32_16x16x32_bf16 v[160:163], a[200:203], a[44:47], v[160:163]\n" \
"buffer_load_dwordx4 v[92:95], v32, s[56:59], s64 offen offset:1024 sc1\n" \
"v_mfma_f32_16x16x32_bf16 v[164:167], a[200:203], a[60:63], v[164:167]\n" \
"ds_read_b64_tr_b16 a[200:201], v33 offset:18432\n" \
"ds_read_b64_tr_b16 a[202:203], v33 offset:23040\n" \
"v_mfma_f32_16x16x32_bf16 v[248:251], a[204:207], a[96:99], v[248:251]\n" \
"v_readlane_b32 s64, v11, 49\n" \
"v_mfma_f32_16x16x32_bf16 v[252:255], a[204:207], a[112:115], v[252:255]\n" \
"ds_read_b64_tr_b16 a[204:205], v33 offset:27648\n" \
"v_cvt_pk_bf16_f32 v160, v160, v161\n" \
"v_cvt_pk_bf16_f32 v161, v162, v163\n" \
"v_cvt_pk_bf16_f32 v162, v164, v165\n" \
"v_cvt_pk_bf16_f32 v163, v166, v167\n" \
"buffer_store_dwordx4 v[160:163], v20, s[56:59], s73 offen offset:0 sc1\n" \
"s_add_u32 s73, s73, 0x800\n" \
"v_mfma_f32_16x16x32_bf16 v[248:251], a[208:211], a[100:103], v[248:251]\n" \
"ds_read_b64_tr_b16 a[206:207], v33 offset:32256\n" \
"v_mfma_f32_16x16x32_bf16 v[252:255], a[208:211], a[116:119], v[252:255]\n" \
"ds_read_b64_tr_b16 a[208:209], v33 offset:36864\n" \
"ds_read_b64_tr_b16 a[210:211], v33 offset:41472\n" \
"v_mfma_f32_16x16x32_bf16 v[248:251], a[212:215], a[104:107], v[248:251]\n" \
"v_mfma_f32_16x16x32_bf16 v[252:255], a[212:215], a[120:123], v[252:255]\n" \
"ds_read_b64_tr_b16 a[212:213], v33 offset:46080\n" \
"ds_read_b64_tr_b16 a[214:215], v33 offset:50688\n" \
"v_mfma_f32_16x16x32_bf16 v[248:251], a[216:219], a[108:111], v[248:251]\n" \
"buffer_load_dwordx4 v[96:99], v20, s[56:59], s65 offen offset:0 sc1\n" \
"v_mfma_f32_16x16x32_bf16 v[252:255], a[216:219], a[124:127], v[252:255]\n" \
"ds_read_b64_tr_b16 a[216:217], v33 offset:55296\n" \
"ds_read_b64_tr_b16 a[218:219], v33 offset:59904\n" \
"s_waitcnt vmcnt(18)\n" \
"s_mov_b64 exec, 1\n" \
"buffer_store_dword v39, off, s[56:59], s68 offset:4 sc1\n" \
"s_mov_b64 exec, -1\n" \
"v_mfma_f32_16x16x32_bf16 v[176:179], a[0:3], a[128:131], v[176:179]\n" \
"v_mfma_f32_16x16x32_bf16 v[180:183], a[0:3], a[144:147], v[180:183]\n" \
"v_mfma_f32_16x16x32_bf16 v[248:251], a[0:3], a[160:163], v[248:251]\n" \
"v_mfma_f32_16x16x32_bf16 v[252:255], a[0:3], a[176:179], v[252:255]\n" \
"ds_read_b64_tr_b16 a[0:1], v10 offset:0\n" \
"ds_read_b64_tr_b16 a[2:3], v10 offset:4608\n" \
"v_mfma_f32_16x16x32_bf16 v[176:179], a[4:7], a[132:135], v[176:179]\n" \
"buffer_load_dwordx4 v[100:103], v20, s[56:59], s65 offen offset:1024 sc1\n" \
"v_mfma_f32_16x16x32_bf16 v[180:183], a[4:7], a[148:151], v[180:183]\n" \
"v_mfma_f32_16x16x32_bf16 v[248:251], a[4:7], a[164:167], v[248:251]\n" \
"v_mfma_f32_16x16x32_bf16 v[252:255], a[4:7], a[180:183], v[252:255]\n" \
"ds_read_b64_tr_b16 a[4:5], v34 offset:0\n" \
"s_waitcnt lgkmcnt(9)\n" \
"v_mfma_f32_16x16x32_bf16 v[176:179], a[8:11], a[136:139], v[176:179]\n" \
"ds_read_b64_tr_b16 a[6:7], v34 offset:4608\n" \
"v_mfma_f32_16x16x32_bf16 v[180:183], a[8:11], a[152:155], v[180:183]\n" \
"buffer_load_dwordx4 v[104:107], v32, s[56:59], s65 offen offset:0 sc1\n" \
"v_mfma_f32_16x16x32_bf16 v[248:251], a[8:11], a[168:171], v[248:251]\n" \
"v_mfma_f32_16x16x32_bf16 v[252:255], a[8:11], a[184:187], v[252:255]\n" \
"ds_read_b64_tr_b16 a[8:9], v34 offset:9216\n" \
"ds_read_b64_tr_b16 a[10:11], v34 offset:13824\n" \
"v_mfma_f32_16x16x32_bf16 v[176:179], a[12:15], a[140:143], v[176:179]\n" \
"v_mfma_f32_16x16x32_bf16 v[180:183], a[12:15], a[156:159], v[180:183]\n" \
"v_mfma_f32_16x16x32_bf16 v[248:251], a[12:15], a[172:175], v[248:251]\n" \
"buffer_load_dwordx4 v[108:111], v32, s[56:59], s65 offen offset:1024 sc1\n" \
"v_mfma_f32_16x16x32_bf16 v[252:255], a[12:15], a[188:191], v[252:255]\n" \
"ds_read_b64_tr_b16 a[12:13], v34 offset:18432\n" \
"ds_read_b64_tr_b16 a[14:15], v34 offset:23040\n" \
"s_waitcnt vmcnt(18)\n" \
"s_mov_b64 exec, 1\n" \
"buffer_store_dword v39, off, s[56:59], s69 offset:8 sc1\n" \
"s_mov_b64 exec, -1\n" \
"v_mfma_f32_16x16x32_bf16 v[176:179], a[16:19], a[32:35], v[176:179]\n" \
"v_mfma_f32_16x16x32_bf16 v[180:183], a[16:19], a[48:51], v[180:183]\n" \
"v_mfma_f32_16x16x32_bf16 v[248:251], a[16:19], a[220:223], v[248:251]\n" \
"v_mfma_f32_16x16x32_bf16 v[252:255], a[16:19], a[236:239], v[252:255]\n" \
"ds_read_b64_tr_b16 a[16:17], v10 offset:9216\n" \
"ds_read_b64_tr_b16 a[18:19], v10 offset:13824\n" \
".fill (L_ab + 28672 - .) / 4, 4, 0xBF800000\n" \
"L_ap7:\n" \
"s_cbranch_execz L_ap0\n" \
"v_mfma_f32_16x16x32_bf16 v[176:179], a[20:23], a[36:39], v[176:179]\n" \
"v_mfma_f32_16x16x32_bf16 v[180:183], a[20:23], a[52:55], v[180:183]\n" \
"v_mfma_f32_16x16x32_bf16 v[248:251], a[20:23], a[224:227], v[248:251]\n" \
"v_mfma_f32_16x16x32_bf16 v[252:255], a[20:23], a[240:243], v[252:255]\n" \
"ds_read_b64_tr_b16 a[20:21], v33 offset:32\n" \
"ds_read_b64_tr_b16 a[22:23], v33 offset:4640\n" \
"v_mfma_f32_16x16x32_bf16 v[176:179], a[24:27], a[40:43], v[176:179]\n" \
"v_mfma_f32_16x16x32_bf16 v[180:183], a[24:27], a[56:59], v[180:183]\n" \
"v_mfma_f32_16x16x32_bf16 v[248:251], a[24:27], a[228:231], v[248:251]\n" \
"v_mfma_f32_16x16x32_bf16 v[252:255], a[24:27], a[244:247], v[252:255]\n" \
"ds_read_b64_tr_b16 a[24:25], v33 offset:9248\n" \
"ds_read_b64_tr_b16 a[26:27], v33 offset:13856\n" \
"v_mfma_f32_16x16x32_bf16 v[176:179], a[28:31], a[44:47], v[176:179]\n" \
"v_mfma_f32_16x16x32_bf16 v[180:183], a[28:31], a[60:63], v[180:183]\n" \
"v_mfma_f32_16x16x32_bf16 v[248:251], a[28:31], a[232:235], v[248:251]\n" \
"v_mfma_f32_16x16x32_bf16 v[252:255], a[28:31], a[248:251], v[252:255]\n" \
"ds_read_b64_tr_b16 a[28:29], v33 offset:18464\n" \
"ds_read_b64_tr_b16 a[30:31], v33 offset:23072\n" \
"v_mfma_f32_16x16x32_bf16 v[184:187], a[192:195], a[96:99], v[184:187]\n" \
"s_waitcnt vmcnt(16)\n" \
"s_mov_b64 exec, 1\n" \
"buffer_store_dword v39, off, s[56:59], s70 offset:12 sc1\n" \
"s_mov_b64 exec, -1\n" \
"v_cvt_pk_bf16_f32 v176, v176, v177\n" \
"v_cvt_pk_bf16_f32 v248, v248, v249\n" \
"v_cvt_pk_bf16_f32 v177, v178, v179\n" \
"v_cvt_pk_bf16_f32 v249, v250, v251\n" \
"v_cvt_pk_bf16_f32 v178, v180, v181\n" \
"v_cvt_pk_bf16_f32 v179, v182, v183\n" \
"v_cvt_pk_bf16_f32 v250, v252, v253\n" \
"v_cvt_pk_bf16_f32 v251, v254, v255\n" \
"buffer_store_dwordx4 v[176:179], v20, s[56:59], s92 offen offset:0 sc1\n" \
"v_mfma_f32_16x16x32_bf16 v[188:191], a[192:195], a[112:115], v[188:191]\n" \
"ds_read_b64_tr_b16 a[192:193], v33 offset:27680\n" \
"ds_read_b64_tr_b16 a[194:195], v33 offset:32288\n" \
"v_mfma_f32_16x16x32_bf16 v[184:187], a[196:199], a[100:103], v[184:187]\n" \
"v_mfma_f32_16x16x32_bf16 v[188:191], a[196:199], a[116:119], v[188:191]\n" \
"buffer_store_dwordx4 v[248:251], v20, s[56:59], s92 offen offset:1024 sc1\n" \
"v_mfma_f32_16x16x32_bf16 v[184:187], a[200:203], a[104:107], v[184:187]\n" \
"v_readlane_b32 s92, v11, 51\n" \
"ds_read_b64_tr_b16 a[196:197], v33 offset:36896\n" \
"v_mfma_f32_16x16x32_bf16 v[188:191], a[200:203], a[120:123], v[188:191]\n" \
"ds_read_b64_tr_b16 a[198:199], v33 offset:41504\n" \
"ds_read_b64_tr_b16 a[200:201], v33 offset:46112\n" \
"v_mfma_f32_16x16x32_bf16 v[184:187], a[204:207], a[108:111], v[184:187]\n" \
"ds_read_b64_tr_b16 a[202:203], v33 offset:50720\n" \
"v_mfma_f32_16x16x32_bf16 v[188:191], a[204:207], a[124:127], v[188:191]\n" \
"ds_read_b64_tr_b16 a[204:205], v33 offset:55328\n" \
"s_waitcnt lgkmcnt(9)\n" \
"v_mfma_f32_16x16x32_bf16 v[184:187], a[208:211], a[160:163], v[184:187]\n" \
"ds_read_b64_tr_b16 a[206:207], v33 offset:59936\n" \
"v_mfma_f32_16x16x32_bf16 v[188:191], a[208:211], a[176:179], v[188:191]\n" \
"ds_read_b64_tr_b16 a[208:209], v10 offset:32\n" \
"ds_read_b64_tr_b16 a[210:211], v10 offset:4640\n" \
"v_mfma_f32_16x16x32_bf16 v[184:187], a[212:215], a[164:167], v[184:187]\n" \
"v_mfma_f32_16x16x32_bf16 v[188:191], a[212:215], a[180:183], v[188:191]\n" \
"ds_read_b64_tr_b16 a[212:213], v34 offset:32\n" \
"ds_read_b64_tr_b16 a[214:215], v34 offset:4640\n" \
"v_mfma_f32_16x16x32_bf16 v[184:187], a[216:219], a[168:171], v[184:187]\n" \
"v_mfma_f32_16x16x32_bf16 v[188:191], a[216:219], a[184:187], v[188:191]\n" \
"ds_read_b64_tr_b16 a[216:217], v34 offset:9248\n" \
"ds_read_b64_tr_b16 a[218:219], v34 offset:13856\n" \
"s_waitcnt vmcnt(16)\n" \
"s_mov_b64 exec, 1\n" \
"buffer_store_dword v39, off, s[56:59], s71 offset:16 sc1\n" \
"s_mov_b64 exec, -1\n" \
"v_mfma_f32_16x16x32_bf16 v[184:187], a[0:3], a[172:175], v[184:187]\n" \
"v_mfma_f32_16x16x32_bf16 v[188:191], a[0:3], a[188:191], v[188:191]\n" \
"ds_read_b64_tr_b16 a[0:1], v34 offset:18464\n" \
"ds_read_b64_tr_b16 a[2:3], v34 offset:23072\n" \
"v_mfma_f32_16x16x32_bf16 v[184:187], a[4:7], a[220:223], v[184:187]\n" \
"v_mfma_f32_16x16x32_bf16 v[188:191], a[4:7], a[236:239], v[188:191]\n" \
"ds_read_b64_tr_b16 a[4:5], v10 offset:9248\n" \
"ds_read_b64_tr_b16 a[6:7], v10 offset:13856\n" \
"v_mfma_f32_16x16x32_bf16 v[184:187], a[8:11], a[224:227], v[184:187]\n" \
"v_mfma_f32_16x16x32_bf16 v[188:191], a[8:11], a[240:243], v[188:191]\n" \
"ds_read_b64_tr_b16 a[8:9], v33 offset:64\n" \
"ds_read_b64_tr_b16 a[10:11], v33 offset:4672\n" \
"v_mfma_f32_16x16x32_bf16 v[184:187], a[12:15], a[228:231], v[184:187]\n" \
"v_mfma_f32_16x16x32_bf16 v[188:191], a[12:15], a[244:247], v[188:191]\n" \
"ds_read_b64_tr_b16 a[12:13], v33 offset:9280\n" \
"ds_read_b64_tr_b16 a[14:15], v33 offset:13888\n" \
"v_mfma_f32_16x16x32_bf16 v[184:187], a[16:19], a[232:235], v[184:187]\n" \
"v_mfma_f32_16x16x32_bf16 v[188:191], a[16:19], a[248:251], v[188:191]\n" \
"ds_read_b64_tr_b16 a[16:17], v33 offset:18496\n" \
"ds_read_b64_tr_b16 a[18:19], v33 offset:23104\n" \
"v_mfma_f32_16x16x32_bf16 v[192:195], a[20:23], a[96:99], v[192:195]\n" \
"v_mfma_f32_16x16x32_bf16 v[196:199], a[20:23], a[112:115], v[196:199]\n" \
"ds_read_b64_tr_b16 a[20:21], v33 offset:27712\n" \
"ds_read_b64_tr_b16 a[22:23], v33 offset:32320\n" \
"v_mfma_f32_16x16x32_bf16 v[192:195], a[24:27], a[100:103], v[192:195]\n" \
"v_cvt_pk_bf16_f32 v184, v184, v185\n" \
"v_cvt_pk_bf16_f32 v185, v186, v187\n" \
"v_cvt_pk_bf16_f32 v186, v188, v189\n" \
"v_cvt_pk_bf16_f32 v187, v190, v191\n" \
"buffer_store_dwordx4 v[184:187], v20, s[56:59], s94 offen offset:1024 sc1\n" \
"s_add_u32 s94, s94, 0x3800\n" \
"v_mfma_f32_16x16x32_bf16 v[196:199], a[24:27], a[116:119], v[196:199]\n" \
"ds_read_b64_tr_b16 a[24:25], v33 offset:36928\n" \
"s_waitcnt lgkmcnt(13)\n" \
"v_mfma_f32_16x16x32_bf16 v[192:195], a[28:31], a[104:107], v[192:195]\n" \
"ds_read_b64_tr_b16 a[26:27], v33 offset:41536\n" \
"v_mfma_f32_16x16x32_bf16 v[196:199], a[28:31], a[120:123], v[196:199]\n" \
"ds_read_b64_tr_b16 a[28:29], v33 offset:46144\n" \
"ds_read_b64_tr_b16 a[30:31], v33 offset:50752\n" \
"s_waitcnt vmcnt(15)\n" \
"s_mov_b64 exec, 1\n" \
"buffer_store_dword v39, off, s[56:59], s72 offset:20 sc1\n" \
"s_mov_b64 exec, -1\n" \
"v_mfma_f32_16x16x32_bf16 v[192:195], a[192:195], a[108:111], v[192:195]\n" \
"v_readlane_b32 s72, v11, 41\n" \
"v_mfma_f32_16x16x32_bf16 v[196:199], a[192:195], a[124:127], v[196:199]\n" \
"ds_read_b64_tr_b16 a[192:193], v33 offset:55360\n" \
"ds_read_b64_tr_b16 a[194:195], v33 offset:59968\n" \
"v_mfma_f32_16x16x32_bf16 v[192:195], a[196:199], a[160:163], v[192:195]\n" \
"v_mfma_f32_16x16x32_bf16 v[196:199], a[196:199], a[176:179], v[196:199]\n" \
"ds_read_b64_tr_b16 a[196:197], v10 offset:64\n" \
"ds_read_b64_tr_b16 a[198:199], v10 offset:4672\n" \
"v_mfma_f32_16x16x32_bf16 v[192:195], a[200:203], a[164:167], v[192:195]\n" \
"v_mfma_f32_16x16x32_bf16 v[196:199], a[200:203], a[180:183], v[196:199]\n" \
"ds_read_b64_tr_b16 a[200:201], v34 offset:64\n" \
"ds_read_b64_tr_b16 a[202:203], v34 offset:4672\n" \
"v_mfma_f32_16x16x32_bf16 v[192:195], a[204:207], a[168:171], v[192:195]\n" \
"v_mfma_f32_16x16x32_bf16 v[196:199], a[204:207], a[184:187], v[196:199]\n" \
"ds_read_b64_tr_b16 a[204:205], v34 offset:9280\n" \
"ds_read_b64_tr_b16 a[206:207], v34 offset:13888\n" \
"v_mfma_f32_16x16x32_bf16 v[192:195], a[208:211], a[172:175], v[192:195]\n" \
"v_mfma_f32_16x16x32_bf16 v[196:199], a[208:211], a[188:191], v[196:199]\n" \
"ds_read_b64_tr_b16 a[208:209], v34 offset:18496\n" \
"ds_read_b64_tr_b16 a[210:211], v34 offset:23104\n" \
"v_mfma_f32_16x16x32_bf16 v[192:195], a[212:215], a[220:223], v[192:195]\n" \
"v_mfma_f32_16x16x32_bf16 v[196:199], a[212:215], a[236:239], v[196:199]\n" \
"ds_read_b64_tr_b16 a[212:213], v10 offset:9280\n" \
"ds_read_b64_tr_b16 a[214:215], v10 offset:13888\n" \
"v_mfma_f32_16x16x32_bf16 v[192:195], a[216:219], a[224:227], v[192:195]\n" \
"v_mfma_f32_16x16x32_bf16 v[196:199], a[216:219], a[240:243], v[196:199]\n" \
"ds_read_b64_tr_b16 a[216:217], v33 offset:96\n" \
"s_waitcnt lgkmcnt(13)\n" \
"v_mfma_f32_16x16x32_bf16 v[192:195], a[0:3], a[228:231], v[192:195]\n" \
"ds_read_b64_tr_b16 a[218:219], v33 offset:4704\n" \
"v_mfma_f32_16x16x32_bf16 v[196:199], a[0:3], a[244:247], v[196:199]\n" \
"ds_read_b64_tr_b16 a[0:1], v33 offset:9312\n" \
"ds_read_b64_tr_b16 a[2:3], v33 offset:13920\n" \
"s_waitcnt vmcnt(12)\n" \
"s_mov_b64 exec, 1\n" \
"buffer_store_dword v39, off, s[56:59], s73 offset:24 sc1\n" \
"s_mov_b64 exec, -1\n" \
"v_readlane_b32 s73, v11, 50\n" \
"v_mfma_f32_16x16x32_bf16 v[192:195], a[4:7], a[232:235], v[192:195]\n" \
"v_mfma_f32_16x16x32_bf16 v[196:199], a[4:7], a[248:251], v[196:199]\n" \
"ds_read_b64_tr_b16 a[4:5], v33 offset:18528\n" \
"ds_read_b64_tr_b16 a[6:7], v33 offset:23136\n" \
"v_mfma_f32_16x16x32_bf16 v[200:203], a[8:11], a[96:99], v[200:203]\n" \
"v_mfma_f32_16x16x32_bf16 v[204:207], a[8:11], a[112:115], v[204:207]\n" \
"ds_read_b64_tr_b16 a[8:9], v33 offset:27744\n" \
"ds_read_b64_tr_b16 a[10:11], v33 offset:32352\n" \
"v_mfma_f32_16x16x32_bf16 v[200:203], a[12:15], a[100:103], v[200:203]\n" \
"v_cvt_pk_bf16_f32 v192, v192, v193\n" \
"v_cvt_pk_bf16_f32 v193, v194, v195\n" \
"v_cvt_pk_bf16_f32 v194, v196, v197\n" \
"v_cvt_pk_bf16_f32 v195, v198, v199\n" \
"buffer_store_dwordx4 v[192:195], v20, s[56:59], s95 offen offset:1024 sc1\n" \
"s_add_u32 s95, s95, 0x3000\n" \
"v_mfma_f32_16x16x32_bf16 v[204:207], a[12:15], a[116:119], v[204:207]\n" \
"ds_read_b64_tr_b16 a[12:13], v33 offset:36960\n" \
"ds_read_b64_tr_b16 a[14:15], v33 offset:41568\n" \
"v_mfma_f32_16x16x32_bf16 v[200:203], a[16:19], a[104:107], v[200:203]\n" \
"v_mfma_f32_16x16x32_bf16 v[204:207], a[16:19], a[120:123], v[204:207]\n" \
"ds_read_b64_tr_b16 a[16:17], v33 offset:46176\n" \
"ds_read_b64_tr_b16 a[18:19], v33 offset:50784\n" \
"v_mfma_f32_16x16x32_bf16 v[200:203], a[20:23], a[108:111], v[200:203]\n" \
"v_mfma_f32_16x16x32_bf16 v[204:207], a[20:23], a[124:127], v[204:207]\n" \
"ds_read_b64_tr_b16 a[20:21], v33 offset:55392\n" \
"ds_read_b64_tr_b16 a[22:23], v33 offset:60000\n" \
"v_mfma_f32_16x16x32_bf16 v[200:203], a[24:27], a[160:163], v[200:203]\n" \
"v_mfma_f32_16x16x32_bf16 v[204:207], a[24:27], a[176:179], v[204:207]\n" \
"ds_read_b64_tr_b16 a[24:25], v10 offset:96\n" \
"ds_read_b64_tr_b16 a[26:27], v10 offset:4704\n" \
"v_mfma_f32_16x16x32_bf16 v[200:203], a[28:31], a[164:167], v[200:203]\n" \
"v_mfma_f32_16x16x32_bf16 v[204:207], a[28:31], a[180:183], v[204:207]\n" \
"ds_read_b64_tr_b16 a[28:29], v34 offset:96\n" \
"s_waitcnt lgkmcnt(13)\n" \
"v_mfma_f32_16x16x32_bf16 v[200:203], a[192:195], a[168:171], v[200:203]\n" \
"ds_read_b64_tr_b16 a[30:31], v34 offset:4704\n" \
"v_mfma_f32_16x16x32_bf16 v[204:207], a[192:195], a[184:187], v[204:207]\n" \
"ds_read_b64_tr_b16 a[192:193], v34 offset:9312\n" \
"ds_read_b64_tr_b16 a[194:195], v34 offset:13920\n" \
"v_mfma_f32_16x16x32_bf16 v[200:203], a[196:199], a[172:175], v[200:203]\n" \
"v_mfma_f32_16x16x32_bf16 v[204:207], a[196:199], a[188:191], v[204:207]\n" \
"ds_read_b64_tr_b16 a[196:197], v34 offset:18528\n" \
"ds_read_b64_tr_b16 a[198:199], v34 offset:23136\n" \
"v_mfma_f32_16x16x32_bf16 v[200:203], a[200:203], a[220:223], v[200:203]\n" \
"v_mfma_f32_16x16x32_bf16 v[204:207], a[200:203], a[236:239], v[204:207]\n" \
"ds_read_b64_tr_b16 a[200:201], v10 offset:9312\n" \
"ds_read_b64_tr_b16 a[202:203], v10 offset:13920\n" \
"v_mfma_f32_16x16x32_bf16 v[200:203], a[204:207], a[224:227], v[200:203]\n" \
"v_mfma_f32_16x16x32_bf16 v[204:207], a[204:207], a[240:243], v[204:207]\n" \
"ds_read_b64_tr_b16 a[204:205], v33 offset:128\n" \
"ds_read_b64_tr_b16 a[206:207], v33 offset:4736\n" \
"v_mfma_f32_16x16x32_bf16 v[200:203], a[208:211], a[228:231], v[200:203]\n" \
"v_mfma_f32_16x16x32_bf16 v[204:207], a[208:211], a[244:247], v[204:207]\n" \
"ds_read_b64_tr_b16 a[208:209], v33 offset:9344\n" \
"ds_read_b64_tr_b16 a[210:211], v33 offset:13952\n" \
"v_mfma_f32_16x16x32_bf16 v[200:203], a[212:215], a[232:235], v[200:203]\n" \
"v_mfma_f32_16x16x32_bf16 v[204:207], a[212:215], a[248:251], v[204:207]\n" \
"ds_read_b64_tr_b16 a[212:213], v33 offset:18560\n" \
"ds_read_b64_tr_b16 a[214:215], v33 offset:23168\n" \
"v_mfma_f32_16x16x32_bf16 v[208:211], a[216:219], a[96:99], v[208:211]\n" \
"v_mfma_f32_16x16x32_bf16 v[212:215], a[216:219], a[112:115], v[212:215]\n" \
"ds_read_b64_tr_b16 a[216:217], v33 offset:27776\n" \
"ds_read_b64_tr_b16 a[218:219], v33 offset:32384\n" \
"v_mfma_f32_16x16x32_bf16 v[208:211], a[0:3], a[100:103], v[208:211]\n" \
"v_cvt_pk_bf16_f32 v200, v200, v201\n" \
"v_cvt_pk_bf16_f32 v201, v202, v203\n" \
"v_cvt_pk_bf16_f32 v202, v204, v205\n" \
"v_cvt_pk_bf16_f32 v203, v206, v207\n" \
"buffer_store_dwordx4 v[200:203], v20, s[56:59], s96 offen offset:1024 sc1\n" \
"s_add_u32 s96, s96, 0x2800\n" \
"v_mfma_f32_16x16x32_bf16 v[212:215], a[0:3], a[116:119], v[212:215]\n" \
"ds_read_b64_tr_b16 a[0:1], v33 offset:36992\n" \
"s_waitcnt lgkmcnt(13)\n" \
"v_mfma_f32_16x16x32_bf16 v[208:211], a[4:7], a[104:107], v[208:211]\n" \
"ds_read_b64_tr_b16 a[2:3], v33 offset:41600\n" \
"v_mfma_f32_16x16x32_bf16 v[212:215], a[4:7], a[120:123], v[212:215]\n" \
"ds_read_b64_tr_b16 a[4:5], v33 offset:46208\n" \
"ds_read_b64_tr_b16 a[6:7], v33 offset:50816\n" \
"v_mfma_f32_16x16x32_bf16 v[208:211], a[8:11], a[108:111], v[208:211]\n" \
"v_mfma_f32_16x16x32_bf16 v[212:215], a[8:11], a[124:127], v[212:215]\n" \
"ds_read_b64_tr_b16 a[8:9], v33 offset:55424\n" \
"ds_read_b64_tr_b16 a[10:11], v33 offset:60032\n" \
"v_mfma_f32_16x16x32_bf16 v[208:211], a[12:15], a[160:163], v[208:211]\n" \
"v_mfma_f32_16x16x32_bf16 v[212:215], a[12:15], a[176:179], v[212:215]\n" \
"ds_read_b64_tr_b16 a[12:13], v10 offset:128\n" \
"ds_read_b64_tr_b16 a[14:15], v10 offset:4736\n" \
"v_mfma_f32_16x16x32_bf16 v[208:211], a[16:19], a[164:167], v[208:211]\n" \
"v_mfma_f32_16x16x32_bf16 v[212:215], a[16:19], a[180:183], v[212:215]\n" \
"ds_read_b64_tr_b16 a[16:17], v34 offset:128\n" \
"ds_read_b64_tr_b16 a[18:19], v34 offset:4736\n" \
"v_mfma_f32_16x16x32_bf16 v[208:211], a[20:23], a[168:171], v[208:211]\n" \
"v_mfma_f32_16x16x32_bf16 v[212:215], a[20:23], a[184:187], v[212:215]\n" \
"ds_read_b64_tr_b16 a[20:21], v34 offset:9344\n" \
"ds_read_b64_tr_b16 a[22:23], v34 offset:13952\n" \
"v_mfma_f32_16x16x32_bf16 v[208:211], a[24:27], a[172:175], v[208:211]\n" \
"v_mfma_f32_16x16x32_bf16 v[212:215], a[24:27], a[188:191], v[212:215]\n" \
"ds_read_b64_tr_b16 a[24:25], v34 offset:18560\n" \
"ds_read_b64_tr_b16 a[26:27], v34 offset:23168\n" \
"v_mfma_f32_16x16x32_bf16 v[208:211], a[28:31], a[220:223], v[208:211]\n" \
"v_mfma_f32_16x16x32_bf16 v[212:215], a[28:31], a[236:239], v[212:215]\n" \
"ds_read_b64_tr_b16 a[28:29], v10 offset:9344\n" \
"ds_read_b64_tr_b16 a[30:31], v10 offset:13952\n" \
"s_waitcnt vmcnt(6)\n" \
"s_mov_b64 exec, 3\n" \
"buffer_store_dword v39, v41, s[56:59], s93 offen sc1\n" \
"s_mov_b64 exec, -1\n" \
"v_mfma_f32_16x16x32_bf16 v[208:211], a[192:195], a[224:227], v[208:211]\n" \
"v_mfma_f32_16x16x32_bf16 v[212:215], a[192:195], a[240:243], v[212:215]\n" \
"ds_read_b64_tr_b16 a[192:193], v33 offset:160\n" \
"s_waitcnt lgkmcnt(13)\n" \
"v_mfma_f32_16x16x32_bf16 v[208:211], a[196:199], a[228:231], v[208:211]\n" \
"ds_read_b64_tr_b16 a[194:195], v33 offset:4768\n" \
"v_mfma_f32_16x16x32_bf16 v[212:215], a[196:199], a[244:247], v[212:215]\n" \
"ds_read_b64_tr_b16 a[196:197], v33 offset:9376\n" \
"ds_read_b64_tr_b16 a[198:199], v33 offset:13984\n" \
"v_mfma_f32_16x16x32_bf16 v[208:211], a[200:203], a[232:235], v[208:211]\n" \
"v_mfma_f32_16x16x32_bf16 v[212:215], a[200:203], a[248:251], v[212:215]\n" \
"ds_read_b64_tr_b16 a[200:201], v33 offset:18592\n" \
"ds_read_b64_tr_b16 a[202:203], v33 offset:23200\n" \
"v_mfma_f32_16x16x32_bf16 v[216:219], a[204:207], a[96:99], v[216:219]\n" \
"v_mfma_f32_16x16x32_bf16 v[220:223], a[204:207], a[112:115], v[220:223]\n" \
"ds_read_b64_tr_b16 a[204:205], v33 offset:27808\n" \
"ds_read_b64_tr_b16 a[206:207], v33 offset:32416\n" \
"v_mfma_f32_16x16x32_bf16 v[216:219], a[208:211], a[100:103], v[216:219]\n" \
"v_cvt_pk_bf16_f32 v208, v208, v209\n" \
"v_cvt_pk_bf16_f32 v209, v210, v211\n" \
"v_cvt_pk_bf16_f32 v210, v212, v213\n" \
"v_cvt_pk_bf16_f32 v211, v214, v215\n" \
"buffer_store_dwordx4 v[208:211], v20, s[56:59], s97 offen offset:1024 sc1\n" \
"s_add_u32 s97, s97, 0x2000\n" \
"v_mfma_f32_16x16x32_bf16 v[220:223], a[208:211], a[116:119], v[220:223]\n" \
"ds_read_b64_tr_b16 a[208:209], v33 offset:37024\n" \
"ds_read_b64_tr_b16 a[210:211], v33 offset:41632\n" \
"v_mfma_f32_16x16x32_bf16 v[216:219], a[212:215], a[104:107], v[216:219]\n" \
"v_mfma_f32_16x16x32_bf16 v[220:223], a[212:215], a[120:123], v[220:223]\n" \
"ds_read_b64_tr_b16 a[212:213], v33 offset:46240\n" \
"ds_read_b64_tr_b16 a[214:215], v33 offset:50848\n" \
"v_mfma_f32_16x16x32_bf16 v[216:219], a[216:219], a[108:111], v[216:219]\n" \
"v_mfma_f32_16x16x32_bf16 v[220:223], a[216:219], a[124:127], v[220:223]\n" \
"ds_read_b64_tr_b16 a[216:217], v33 offset:55456\n" \
"ds_read_b64_tr_b16 a[218:219], v33 offset:60064\n" \
"v_mfma_f32_16x16x32_bf16 v[216:219], a[0:3], a[160:163], v[216:219]\n" \
"v_mfma_f32_16x16x32_bf16 v[220:223], a[0:3], a[176:179], v[220:223]\n" \
"ds_read_b64_tr_b16 a[0:1], v10 offset:160\n" \
"ds_read_b64_tr_b16 a[2:3], v10 offset:4768\n" \
"v_mfma_f32_16x16x32_bf16 v[216:219], a[4:7], a[164:167], v[216:219]\n" \
"v_mfma_f32_16x16x32_bf16 v[220:223], a[4:7], a[180:183], v[220:223]\n" \
"ds_read_b64_tr_b16 a[4:5], v34 offset:160\n" \
"s_waitcnt lgkmcnt(13)\n" \
"v_mfma_f32_16x16x32_bf16 v[216:219], a[8:11], a[168:171], v[216:219]\n" \
"ds_read_b64_tr_b16 a[6:7], v34 offset:4768\n" \
"v_mfma_f32_16x16x32_bf16 v[220:223], a[8:11], a[184:187], v[220:223]\n" \
"ds_read_b64_tr_b16 a[8:9], v34 offset:9376\n" \
"ds_read_b64_tr_b16 a[10:11], v34 offset:13984\n" \
"v_mfma_f32_16x16x32_bf16 v[216:219], a[12:15], a[172:175], v[216:219]\n" \
"v_mfma_f32_16x16x32_bf16 v[220:223], a[12:15], a[188:191], v[220:223]\n" \
"ds_read_b64_tr_b16 a[12:13], v34 offset:18592\n" \
"ds_read_b64_tr_b16 a[14:15], v34 offset:23200\n" \
"v_mfma_f32_16x16x32_bf16 v[216:219], a[16:19], a[220:223], v[216:219]\n" \
"v_mfma_f32_16x16x32_bf16 v[220:223], a[16:19], a[236:239], v[220:223]\n" \
"ds_read_b64_tr_b16 a[16:17], v10 offset:9376\n" \
"ds_read_b64_tr_b16 a[18:19], v10 offset:13984\n" \
"s_waitcnt vmcnt(6)\n" \
"s_mov_b64 exec, 1\n" \
"buffer_store_dword v39, off, s[56:59], s94 offset:32 sc1\n" \
"s_mov_b64 exec, -1\n" \
"v_mfma_f32_16x16x32_bf16 v[216:219], a[20:23], a[224:227], v[216:219]\n" \
"v_mfma_f32_16x16x32_bf16 v[220:223], a[20:23], a[240:243], v[220:223]\n" \
"ds_read_b64_tr_b16 a[20:21], v33 offset:192\n" \
"ds_read_b64_tr_b16 a[22:23], v33 offset:4800\n" \
"v_mfma_f32_16x16x32_bf16 v[216:219], a[24:27], a[228:231], v[216:219]\n" \
"v_mfma_f32_16x16x32_bf16 v[220:223], a[24:27], a[244:247], v[220:223]\n" \
"ds_read_b64_tr_b16 a[24:25], v33 offset:9408\n" \
"ds_read_b64_tr_b16 a[26:27], v33 offset:14016\n" \
"v_mfma_f32_16x16x32_bf16 v[216:219], a[28:31], a[232:235], v[216:219]\n" \
"v_mfma_f32_16x16x32_bf16 v[220:223], a[28:31], a[248:251], v[220:223]\n" \
"buffer_load_dword v37, v40, s[56:59], s74 offen sc1\n" \
"v_mfma_f32_16x16x32_bf16 v[224:227], a[192:195], a[96:99], v[224:227]\n" \
"ds_read_b64_tr_b16 a[28:29], v33 offset:18624\n" \
"ds_read_b64_tr_b16 a[30:31], v33 offset:23232\n" \
"v_mfma_f32_16x16x32_bf16 v[228:231], a[192:195], a[112:115], v[228:231]\n" \
"ds_read_b64_tr_b16 a[192:193], v33 offset:27840\n" \
"ds_read_b64_tr_b16 a[194:195], v33 offset:32448\n" \
"v_cvt_pk_bf16_f32 v216, v216, v217\n" \
"v_cvt_pk_bf16_f32 v217, v218, v219\n" \
"v_cvt_pk_bf16_f32 v218, v220, v221\n" \
"v_cvt_pk_bf16_f32 v219, v222, v223\n" \
"buffer_store_dwordx4 v[216:219], v20, s[56:59], s64 offen offset:1024 sc1\n" \
"s_add_u32 s64, s64, 0x1800\n" \
"v_mfma_f32_16x16x32_bf16 v[224:227], a[196:199], a[100:103], v[224:227]\n" \
"v_mfma_f32_16x16x32_bf16 v[228:231], a[196:199], a[116:119], v[228:231]\n" \
"ds_read_b64_tr_b16 a[196:197], v33 offset:37056\n" \
"s_waitcnt lgkmcnt(11)\n" \
"v_mfma_f32_16x16x32_bf16 v[224:227], a[200:203], a[104:107], v[224:227]\n" \
"ds_read_b64_tr_b16 a[198:199], v33 offset:41664\n" \
"v_mfma_f32_16x16x32_bf16 v[228:231], a[200:203], a[120:123], v[228:231]\n" \
"ds_read_b64_tr_b16 a[200:201], v33 offset:46272\n" \
"ds_read_b64_tr_b16 a[202:203], v33 offset:50880\n" \
"v_mfma_f32_16x16x32_bf16 v[224:227], a[204:207], a[108:111], v[224:227]\n" \
"v_mfma_f32_16x16x32_bf16 v[228:231], a[204:207], a[124:127], v[228:231]\n" \
"ds_read_b64_tr_b16 a[204:205], v33 offset:55488\n" \
"ds_read_b64_tr_b16 a[206:207], v33 offset:60096\n" \
"v_mfma_f32_16x16x32_bf16 v[224:227], a[208:211], a[160:163], v[224:227]\n" \
"v_mfma_f32_16x16x32_bf16 v[228:231], a[208:211], a[176:179], v[228:231]\n" \
"ds_read_b64_tr_b16 a[208:209], v10 offset:192\n" \
"ds_read_b64_tr_b16 a[210:211], v10 offset:4800\n" \
"v_mfma_f32_16x16x32_bf16 v[224:227], a[212:215], a[164:167], v[224:227]\n" \
"v_mfma_f32_16x16x32_bf16 v[228:231], a[212:215], a[180:183], v[228:231]\n" \
"ds_read_b64_tr_b16 a[212:213], v34 offset:192\n" \
"ds_read_b64_tr_b16 a[214:215], v34 offset:4800\n" \
"v_mfma_f32_16x16x32_bf16 v[224:227], a[216:219], a[168:171], v[224:227]\n" \
"v_mfma_f32_16x16x32_bf16 v[228:231], a[216:219], a[184:187], v[228:231]\n" \
"ds_read_b64_tr_b16 a[216:217], v34 offset:9408\n" \
"ds_read_b64_tr_b16 a[218:219], v34 offset:14016\n" \
"v_mfma_f32_16x16x32_bf16 v[224:227], a[0:3], a[172:175], v[224:227]\n" \
"v_mfma_f32_16x16x32_bf16 v[228:231], a[0:3], a[188:191], v[228:231]\n" \
"ds_read_b64_tr_b16 a[0:1], v34 offset:18624\n" \
"ds_read_b64_tr_b16 a[2:3], v34 offset:23232\n" \
"v_mfma_f32_16x16x32_bf16 v[224:227], a[4:7], a[220:223], v[224:227]\n" \
"v_mfma_f32_16x16x32_bf16 v[228:231], a[4:7], a[236:239], v[228:231]\n" \
"ds_read_b64_tr_b16 a[4:5], v10 offset:9408\n" \
"ds_read_b64_tr_b16 a[6:7], v10 offset:14016\n" \
"s_waitcnt vmcnt(6)\n" \
"s_mov_b64 exec, 1\n" \
"buffer_store_dword v39, off, s[56:59], s95 offset:36 sc1\n" \
"s_mov_b64 exec, -1\n" \
"v_mfma_f32_16x16x32_bf16 v[224:227], a[8:11], a[224:227], v[224:227]\n" \
"v_mfma_f32_16x16x32_bf16 v[228:231], a[8:11], a[240:243], v[228:231]\n" \
"ds_read_b64_tr_b16 a[8:9], v33 offset:224\n" \
"ds_read_b64_tr_b16 a[10:11], v33 offset:4832\n" \
"v_mfma_f32_16x16x32_bf16 v[224:227], a[12:15], a[228:231], v[224:227]\n" \
"v_mfma_f32_16x16x32_bf16 v[228:231], a[12:15], a[244:247], v[228:231]\n" \
"ds_read_b64_tr_b16 a[12:13], v33 offset:9440\n" \
"s_waitcnt lgkmcnt(13)\n" \
"v_mfma_f32_16x16x32_bf16 v[224:227], a[16:19], a[232:235], v[224:227]\n" \
"ds_read_b64_tr_b16 a[14:15], v33 offset:14048\n" \
"v_mfma_f32_16x16x32_bf16 v[228:231], a[16:19], a[248:251], v[228:231]\n" \
"ds_read_b64_tr_b16 a[16:17], v33 offset:18656\n" \
"ds_read_b64_tr_b16 a[18:19], v33 offset:23264\n" \
"v_mfma_f32_16x16x32_bf16 v[232:235], a[20:23], a[96:99], v[232:235]\n" \
"v_mfma_f32_16x16x32_bf16 v[236:239], a[20:23], a[112:115], v[236:239]\n" \
"ds_read_b64_tr_b16 a[20:21], v33 offset:27872\n" \
"ds_read_b64_tr_b16 a[22:23], v33 offset:32480\n" \
"v_cvt_pk_bf16_f32 v224, v224, v225\n" \
"v_cvt_pk_bf16_f32 v225, v226, v227\n" \
"v_cvt_pk_bf16_f32 v226, v228, v229\n" \
"v_cvt_pk_bf16_f32 v227, v230, v231\n" \
"buffer_store_dwordx4 v[224:227], v20, s[56:59], s73 offen offset:1024 sc1\n" \
"v_mfma_f32_16x16x32_bf16 v[232:235], a[24:27], a[100:103], v[232:235]\n" \
"v_mfma_f32_16x16x32_bf16 v[236:239], a[24:27], a[116:119], v[236:239]\n" \
"ds_read_b64_tr_b16 a[24:25], v33 offset:37088\n" \
"ds_read_b64_tr_b16 a[26:27], v33 offset:41696\n" \
"v_mfma_f32_16x16x32_bf16 v[232:235], a[28:31], a[104:107], v[232:235]\n" \
"v_mfma_f32_16x16x32_bf16 v[236:239], a[28:31], a[120:123], v[236:239]\n" \
"ds_read_b64_tr_b16 a[28:29], v33 offset:46304\n" \
"ds_read_b64_tr_b16 a[30:31], v33 offset:50912\n" \
"v_mfma_f32_16x16x32_bf16 v[232:235], a[192:195], a[108:111], v[232:235]\n" \
"v_mfma_f32_16x16x32_bf16 v[236:239], a[192:195], a[124:127], v[236:239]\n" \
"ds_read_b64_tr_b16 a[192:193], v33 offset:55520\n" \
"ds_read_b64_tr_b16 a[194:195], v33 offset:60128\n" \
"v_mfma_f32_16x16x32_bf16 v[232:235], a[196:199], a[160:163], v[232:235]\n" \
"v_mfma_f32_16x16x32_bf16 v[236:239], a[196:199], a[176:179], v[236:239]\n" \
"ds_read_b64_tr_b16 a[196:197], v10 offset:224\n" \
"ds_read_b64_tr_b16 a[198:199], v10 offset:4832\n" \
"v_mfma_f32_16x16x32_bf16 v[232:235], a[200:203], a[164:167], v[232:235]\n" \
"v_mfma_f32_16x16x32_bf16 v[236:239], a[200:203], a[180:183], v[236:239]\n" \
"ds_read_b64_tr_b16 a[200:201], v34 offset:224\n" \
"ds_read_b64_tr_b16 a[202:203], v34 offset:4832\n" \
"v_mfma_f32_16x16x32_bf16 v[232:235], a[204:207], a[168:171], v[232:235]\n" \
"v_mfma_f32_16x16x32_bf16 v[236:239], a[204:207], a[184:187], v[236:239]\n" \
"ds_read_b64_tr_b16 a[204:205], v34 offset:9440\n" \
"s_waitcnt lgkmcnt(13)\n" \
"v_mfma_f32_16x16x32_bf16 v[232:235], a[208:211], a[172:175], v[232:235]\n" \
"ds_read_b64_tr_b16 a[206:207], v34 offset:14048\n" \
"v_mfma_f32_16x16x32_bf16 v[236:239], a[208:211], a[188:191], v[236:239]\n" \
"ds_read_b64_tr_b16 a[208:209], v34 offset:18656\n" \
"ds_read_b64_tr_b16 a[210:211], v34 offset:23264\n" \
"v_mfma_f32_16x16x32_bf16 v[232:235], a[212:215], a[220:223], v[232:235]\n" \
"s_waitcnt vmcnt(7)\n" \
"s_mov_b64 exec, 1\n" \
"buffer_store_dword v39, off, s[56:59], s96 offset:40 sc1\n" \
"s_mov_b64 exec, -1\n" \
"v_mfma_f32_16x16x32_bf16 v[236:239], a[212:215], a[236:239], v[236:239]\n" \
"ds_read_b64_tr_b16 a[212:213], v10 offset:9440\n" \
"ds_read_b64_tr_b16 a[214:215], v10 offset:14048\n" \
"v_mfma_f32_16x16x32_bf16 v[232:235], a[216:219], a[224:227], v[232:235]\n" \
"v_mfma_f32_16x16x32_bf16 v[236:239], a[216:219], a[240:243], v[236:239]\n" \
"ds_read_b64_tr_b16 a[216:217], v36 offset:0\n" \
"ds_read_b64_tr_b16 a[218:219], v36 offset:1024\n" \
"v_mfma_f32_16x16x32_bf16 v[232:235], a[0:3], a[228:231], v[232:235]\n" \
"v_mfma_f32_16x16x32_bf16 v[236:239], a[0:3], a[244:247], v[236:239]\n" \
"ds_read_b64_tr_b16 a[0:1], v36 offset:2048\n" \
"ds_read_b64_tr_b16 a[2:3], v36 offset:3072\n" \
"v_mfma_f32_16x16x32_bf16 v[232:235], a[4:7], a[232:235], v[232:235]\n" \
"v_mfma_f32_16x16x32_bf16 v[236:239], a[4:7], a[248:251], v[236:239]\n" \
"ds_read_b64_tr_b16 a[4:5], v36 offset:32\n" \
"ds_read_b64_tr_b16 a[6:7], v36 offset:1056\n" \
"v_mfma_f32_16x16x32_bf16 v[240:243], a[8:11], a[96:99], v[240:243]\n" \
"v_mfma_f32_16x16x32_bf16 v[244:247], a[8:11], a[112:115], v[244:247]\n" \
"ds_read_b64_tr_b16 a[8:9], v36 offset:2080\n" \
"ds_read_b64_tr_b16 a[10:11], v36 offset:3104\n" \
"v_mfma_f32_16x16x32_bf16 v[240:243], a[12:15], a[100:103], v[240:243]\n" \
"v_cvt_pk_bf16_f32 v232, v232, v233\n" \
"v_cvt_pk_bf16_f32 v233, v234, v235\n" \
"v_cvt_pk_bf16_f32 v234, v236, v237\n" \
"v_cvt_pk_bf16_f32 v235, v238, v239\n" \
"buffer_store_dwordx4 v[232:235], v20, s[56:59], s92 offen offset:1024 sc1\n" \
"s_waitcnt vmcnt(5)\n" \
"v_cmp_eq_u32_e64 s[98:99], s62, v37\n" \
"s_or_b32 s75, 0x7f, s100\n" \
"s_andn2_b32 s75, s75, s98\n" \
"s_cbranch_scc1 L_sx0_%=\n" \
"L_rx0_%=:\n" \
"s_mov_b64 exec, 1\n" \
"buffer_store_dword v39, off, s[56:59], s97 offset:44 sc1\n" \
"s_mov_b64 exec, -1\n" \
"s_add_u32 s73, s73, 0x1000\n" \
"s_add_u32 s92, s92, 0x800\n" \
"s_add_u32 s65, s63, 0x0\n" \
"s_add_u32 s66, s63, 0x800\n" \
"s_add_u32 s67, s63, 0x1000\n" \
"s_add_u32 s68, s63, 0x1800\n" \
"s_add_u32 s69, s63, 0x2000\n" \
"s_add_u32 s70, s63, 0x2800\n" \
"s_add_u32 s71, s63, 0x3000\n" \
"v_mfma_f32_16x16x32_bf16 v[244:247], a[12:15], a[116:119], v[244:247]\n" \
"v_mfma_f32_16x16x32_bf16 v[240:243], a[16:19], a[104:107], v[240:243]\n" \
"buffer_load_dwordx4 v[116:119], v20, s[56:59], s65 offen offset:0 sc1\n" \
"v_mfma_f32_16x16x32_bf16 v[244:247], a[16:19], a[120:123], v[244:247]\n" \
"s_waitcnt lgkmcnt(6)\n" \
"v_mfma_f32_16x16x32_bf16 v[240:243], a[20:23], a[108:111], v[240:243]\n" \
"v_mfma_f32_16x16x32_bf16 v[244:247], a[20:23], a[124:127], v[244:247]\n" \
"v_mfma_f32_16x16x32_bf16 v[168:171], a[24:27], a[128:131], v[168:171]\n" \
"buffer_load_dwordx4 v[124:127], v20, s[56:59], s66 offen offset:0 sc1\n" \
"v_mfma_f32_16x16x32_bf16 v[172:175], a[24:27], a[144:147], v[172:175]\n" \
"v_readlane_b32 s66, v11, 53\n" \
"v_mfma_f32_16x16x32_bf16 v[240:243], a[24:27], a[160:163], v[240:243]\n" \
"v_mfma_f32_16x16x32_bf16 v[244:247], a[24:27], a[176:179], v[244:247]\n" \
"v_mfma_f32_16x16x32_bf16 v[168:171], a[28:31], a[132:135], v[168:171]\n" \
"buffer_load_dwordx4 v[132:135], v20, s[56:59], s67 offen offset:0 sc1\n" \
"v_mfma_f32_16x16x32_bf16 v[172:175], a[28:31], a[148:151], v[172:175]\n" \
"v_readlane_b32 s67, v11, 44\n" \
"v_mfma_f32_16x16x32_bf16 v[240:243], a[28:31], a[164:167], v[240:243]\n" \
"v_mfma_f32_16x16x32_bf16 v[244:247], a[28:31], a[180:183], v[244:247]\n" \
"v_mfma_f32_16x16x32_bf16 v[168:171], a[192:195], a[136:139], v[168:171]\n" \
"buffer_load_dwordx4 v[140:143], v20, s[56:59], s68 offen offset:0 sc1\n" \
"v_mfma_f32_16x16x32_bf16 v[172:175], a[192:195], a[152:155], v[172:175]\n" \
"v_readlane_b32 s68, v11, 42\n" \
"v_mfma_f32_16x16x32_bf16 v[240:243], a[192:195], a[168:171], v[240:243]\n" \
"v_mfma_f32_16x16x32_bf16 v[244:247], a[192:195], a[184:187], v[244:247]\n" \
"v_mfma_f32_16x16x32_bf16 v[168:171], a[196:199], a[140:143], v[168:171]\n" \
"buffer_load_dwordx4 v[148:151], v20, s[56:59], s69 offen offset:0 sc1\n" \
"v_mfma_f32_16x16x32_bf16 v[172:175], a[196:199], a[156:159], v[172:175]\n" \
"v_readlane_b32 s69, v11, 43\n" \
"v_mfma_f32_16x16x32_bf16 v[240:243], a[196:199], a[172:175], v[240:243]\n" \
"v_mfma_f32_16x16x32_bf16 v[244:247], a[196:199], a[188:191], v[244:247]\n" \
"v_mfma_f32_16x16x32_bf16 v[168:171], a[200:203], a[32:35], v[168:171]\n" \
"buffer_load_dwordx4 v[156:159], v20, s[56:59], s70 offen offset:0 sc1\n" \
"v_mfma_f32_16x16x32_bf16 v[172:175], a[200:203], a[48:51], v[172:175]\n" \
"v_mfma_f32_16x16x32_bf16 v[240:243], a[200:203], a[220:223], v[240:243]\n" \
"v_mfma_f32_16x16x32_bf16 v[244:247], a[200:203], a[236:239], v[244:247]\n" \
"v_mfma_f32_16x16x32_bf16 v[168:171], a[204:207], a[36:39], v[168:171]\n" \
"buffer_load_dwordx4 v[164:167], v20, s[56:59], s71 offen offset:0 sc1\n" \
"v_mfma_f32_16x16x32_bf16 v[172:175], a[204:207], a[52:55], v[172:175]\n" \
"v_mfma_f32_16x16x32_bf16 v[240:243], a[204:207], a[224:227], v[240:243]\n" \
"v_mfma_f32_16x16x32_bf16 v[244:247], a[204:207], a[240:243], v[244:247]\n" \
"v_mfma_f32_16x16x32_bf16 v[168:171], a[208:211], a[40:43], v[168:171]\n" \
"buffer_load_dwordx4 v[112:115], v20, s[56:59], s72 offen offset:0 sc1\n" \
"v_mfma_f32_16x16x32_bf16 v[172:175], a[208:211], a[56:59], v[172:175]\n" \
"v_mfma_f32_16x16x32_bf16 v[240:243], a[208:211], a[228:231], v[240:243]\n" \
"v_mfma_f32_16x16x32_bf16 v[244:247], a[208:211], a[244:247], v[244:247]\n" \
"v_mfma_f32_16x16x32_bf16 v[168:171], a[212:215], a[44:47], v[168:171]\n" \
"buffer_load_dwordx4 v[184:187], v20, s[56:59], s72 offen offset:2048 sc1\n" \
"s_add_u32 s72, s72, 0x1000\n" \
"v_mfma_f32_16x16x32_bf16 v[172:175], a[212:215], a[60:63], v[172:175]\n" \
"v_mfma_f32_16x16x32_bf16 v[240:243], a[212:215], a[232:235], v[240:243]\n" \
"s_waitcnt vmcnt(14)\n" \
"s_mov_b64 exec, 1\n" \
"buffer_store_dword v39, off, s[56:59], s64 offset:48 sc1\n" \
"s_mov_b64 exec, -1\n" \
"v_mfma_f32_16x16x32_bf16 v[244:247], a[212:215], a[248:251], v[244:247]\n" \
"v_mfma_f32_16x16x32_bf16 v[168:171], a[216:219], v[64:67], v[168:171]\n" \
"v_mfma_f32_16x16x32_bf16 v[172:175], a[216:219], v[72:75], v[172:175]\n" \
"v_mfma_f32_16x16x32_bf16 v[240:243], a[216:219], v[80:83], v[240:243]\n" \
"buffer_load_dwordx4 v[120:123], v20, s[56:59], s72 offen offset:0 sc1\n" \
"v_mfma_f32_16x16x32_bf16 v[244:247], a[216:219], v[88:91], v[244:247]\n" \
"s_waitcnt lgkmcnt(0)\n" \
".fill (L_ab + 32768 - .) / 4, 4, 0xBF800000\n" \
"L_ap8:\n" \
"s_cbranch_execz L_ap0\n" \
"v_mfma_f32_16x16x32_bf16 v[168:171], a[0:3], v[68:71], v[168:171]\n" \
"v_mfma_f32_16x16x32_bf16 v[172:175], a[0:3], v[76:79], v[172:175]\n" \
"v_mfma_f32_16x16x32_bf16 v[240:243], a[0:3], v[84:87], v[240:243]\n" \
"buffer_load_dwordx4 v[192:195], v20, s[56:59], s72 offen offset:2048 sc1\n" \
"s_add_u32 s72, s72, 0x1000\n" \
"v_mfma_f32_16x16x32_bf16 v[244:247], a[0:3], v[92:95], v[244:247]\n" \
"v_mfma_f32_16x16x32_bf16 v[56:59], a[4:7], v[96:99], 0\n" \
"v_mfma_f32_16x16x32_bf16 v[60:63], a[4:7], v[104:107], 0\n" \
"v_mfma_f32_16x16x32_bf16 v[56:59], a[8:11], v[100:103], v[56:59]\n" \
"buffer_load_dwordx4 v[128:131], v20, s[56:59], s72 offen offset:0 sc1\n" \
"v_mfma_f32_16x16x32_bf16 v[60:63], a[8:11], v[108:111], v[60:63]\n" \
"buffer_load_dwordx4 v[200:203], v20, s[56:59], s72 offen offset:2048 sc1\n" \
"s_add_u32 s72, s72, 0x1000\n" \
"buffer_load_dwordx4 v[176:179], v20, s[56:59], s72 offen offset:0 sc1\n" \
"buffer_load_dwordx4 v[248:251], v20, s[56:59], s72 offen offset:2048 sc1\n" \
"s_waitcnt vmcnt(15)\n" \
"v_mfma_f32_16x16x32_bf16 v[168:171], v[50:53], v[116:119], v[168:171]\n" \
"s_mov_b64 exec, 1\n" \
"buffer_store_dword v39, off, s[56:59], s73 offset:52 sc1\n" \
"s_mov_b64 exec, -1\n" \
"s_mov_b64 exec, 1\n" \
"buffer_store_dword v39, off, s[56:59], s92 offset:56 sc1\n" \
"s_mov_b64 exec, -1\n" \
"buffer_load_dword v37, v40, s[56:59], s74 offen sc1\n" \
"v_mfma_f32_16x16x32_bf16 v[172:175], v[48:51], v[116:119], v[172:175]\n" \
"s_waitcnt vmcnt(17)\n" \
"v_mfma_f32_16x16x32_bf16 v[168:171], v[50:53], v[124:127], v[168:171]\n" \
"v_mfma_f32_16x16x32_bf16 v[172:175], v[48:51], v[124:127], v[172:175]\n" \
"s_waitcnt vmcnt(16)\n" \
"v_mfma_f32_16x16x32_bf16 v[168:171], v[50:53], v[132:135], v[168:171]\n" \
"v_mfma_f32_16x16x32_bf16 v[172:175], v[48:51], v[132:135], v[172:175]\n" \
"s_waitcnt vmcnt(15)\n" \
"v_mfma_f32_16x16x32_bf16 v[168:171], v[50:53], v[140:143], v[168:171]\n" \
"v_mfma_f32_16x16x32_bf16 v[172:175], v[48:51], v[140:143], v[172:175]\n" \
"s_waitcnt vmcnt(14)\n" \
"v_mfma_f32_16x16x32_bf16 v[168:171], v[50:53], v[148:151], v[168:171]\n" \
"v_mfma_f32_16x16x32_bf16 v[172:175], v[48:51], v[148:151], v[172:175]\n" \
"s_waitcnt vmcnt(13)\n" \
"v_mfma_f32_16x16x32_bf16 v[168:171], v[50:53], v[156:159], v[168:171]\n" \
"v_mfma_f32_16x16x32_bf16 v[172:175], v[48:51], v[156:159], v[172:175]\n" \
"s_waitcnt vmcnt(12)\n" \
"v_mfma_f32_16x16x32_bf16 v[168:171], v[50:53], v[164:167], v[168:171]\n" \
"v_mfma_f32_16x16x32_bf16 v[172:175], v[48:51], v[164:167], v[172:175]\n" \
"s_waitcnt vmcnt(11)\n" \
"v_mfma_f32_16x16x32_bf16 v[56:59], v[50:53], v[112:115], v[56:59]\n" \
"v_mfma_f32_16x16x32_bf16 v[60:63], v[48:51], v[112:115], v[60:63]\n" \
"s_waitcnt vmcnt(10)\n" \
"v_mfma_f32_16x16x32_bf16 v[56:59], v[50:53], v[184:187], v[56:59]\n" \
"v_mfma_f32_16x16x32_bf16 v[60:63], v[48:51], v[184:187], v[60:63]\n" \
"s_waitcnt vmcnt(8)\n" \
"v_mfma_f32_16x16x32_bf16 v[56:59], v[50:53], v[120:123], v[56:59]\n" \
"v_cvt_pk_bf16_f32 v168, v168, v169\n" \
"v_cvt_pk_bf16_f32 v172, v172, v173\n" \
"v_mfma_f32_16x16x32_bf16 v[60:63], v[48:51], v[120:123], v[60:63]\n" \
"v_cvt_pk_bf16_f32 v169, v170, v171\n" \
"v_cvt_pk_bf16_f32 v173, v174, v175\n" \
"buffer_store_dwordx2 v[168:169], v42, s[88:91], s68 offen sc1\n" \
"buffer_store_dwordx2 v[172:173], v43, s[88:91], s68 offen sc1\n" \
"s_waitcnt vmcnt(9)\n" \
"v_mfma_f32_16x16x32_bf16 v[56:59], v[50:53], v[192:195], v[56:59]\n" \
"v_mfma_f32_16x16x32_bf16 v[60:63], v[48:51], v[192:195], v[60:63]\n" \
"s_waitcnt vmcnt(8)\n" \
"v_mfma_f32_16x16x32_bf16 v[56:59], v[50:53], v[128:131], v[56:59]\n" \
"v_mfma_f32_16x16x32_bf16 v[60:63], v[48:51], v[128:131], v[60:63]\n" \
"s_waitcnt vmcnt(7)\n" \
"v_mfma_f32_16x16x32_bf16 v[56:59], v[50:53], v[200:203], v[56:59]\n" \
"v_mfma_f32_16x16x32_bf16 v[60:63], v[48:51], v[200:203], v[60:63]\n" \
"s_waitcnt vmcnt(6)\n" \
"v_mfma_f32_16x16x32_bf16 v[56:59], v[50:53], v[176:179], v[56:59]\n" \
"v_mfma_f32_16x16x32_bf16 v[60:63], v[48:51], v[176:179], v[60:63]\n" \
"s_waitcnt vmcnt(5)\n" \
"v_mfma_f32_16x16x32_bf16 v[56:59], v[50:53], v[248:251], v[56:59]\n" \
"v_mfma_f32_16x16x32_bf16 v[60:63], v[48:51], v[248:251], v[60:63]\n" \
"s_waitcnt vmcnt(2)\n" \
"v_cmp_eq_u32_e64 s[98:99], s62, v37\n" \
"s_nop 0\n" \
"s_andn2_b32 s75, 0x100, s98\n" \
"s_cbranch_scc1 L_sx1_%=\n" \
"L_rx1_%=:\n" \
"s_add_u32 s93, s63, 0x0\n" \
"buffer_load_dwordx4 v[188:191], v20, s[56:59], s93 offen offset:1024 sc1\n" \
"s_andn2_b32 s75, 0x200, s98\n" \
"s_cbranch_scc1 L_sx2_%=\n" \
"L_rx2_%=:\n" \
"s_add_u32 s94, s63, 0x800\n" \
"v_cvt_pk_bf16_f32 v56, v56, v57\n" \
"v_cvt_pk_bf16_f32 v60, v60, v61\n" \
"v_cvt_pk_bf16_f32 v57, v58, v59\n" \
"v_cvt_pk_bf16_f32 v61, v62, v63\n" \
"buffer_store_dwordx2 v[56:57], v42, s[88:91], s67 offen sc1\n" \
"buffer_store_dwordx2 v[60:61], v43, s[88:91], s67 offen sc1\n" \
"buffer_load_dwordx4 v[196:199], v20, s[56:59], s94 offen offset:1024 sc1\n" \
"s_andn2_b32 s75, 0x400, s98\n" \
"s_cbranch_scc1 L_sx3_%=\n" \
"L_rx3_%=:\n" \
"s_add_u32 s95, s63, 0x1000\n" \
"buffer_load_dwordx4 v[204:207], v20, s[56:59], s95 offen offset:1024 sc1\n" \
"s_andn2_b32 s75, 0x800, s98\n" \
"s_cbranch_scc1 L_sx4_%=\n" \
"L_rx4_%=:\n" \
"s_add_u32 s96, s63, 0x1800\n" \
"buffer_load_dwordx4 v[212:215], v20, s[56:59], s96 offen offset:1024 sc1\n" \
"s_andn2_b32 s75, 0x1000, s98\n" \
"s_cbranch_scc1 L_sx5_%=\n" \
"L_rx5_%=:\n" \
"s_add_u32 s97, s63, 0x2000\n" \
"buffer_load_dwordx4 v[220:223], v20, s[56:59], s97 offen offset:1024 sc1\n" \
"s_andn2_b32 s75, 0x2000, s98\n" \
"s_cbranch_scc1 L_sx6_%=\n" \
"L_rx6_%=:\n" \
"s_add_u32 s64, s63, 0x2800\n" \
"buffer_load_dwordx4 v[228:231], v20, s[56:59], s64 offen offset:1024 sc1\n" \
"s_andn2_b32 s75, 0x4000, s98\n" \
"s_cbranch_scc1 L_sx7_%=\n" \
"L_rx7_%=:\n" \
"s_add_u32 s65, s63, 0x3000\n" \
"buffer_load_dwordx4 v[236:239], v20, s[56:59], s65 offen offset:1024 sc1\n" \
"s_mov_b64 exec, 1\n" \
"buffer_store_dword v46, off, s[56:59], s66 offset:0 sc1\n" \
"s_mov_b64 exec, -1\n" \
"s_waitcnt vmcnt(9)\n" \
"v_mfma_f32_16x16x32_bf16 v[240:243], v[50:53], v[188:191], v[240:243]\n" \
"v_mfma_f32_16x16x32_bf16 v[244:247], v[48:51], v[188:191], v[244:247]\n" \
"s_waitcnt vmcnt(6)\n" \
"v_mfma_f32_16x16x32_bf16 v[240:243], v[50:53], v[196:199], v[240:243]\n" \
"v_mfma_f32_16x16x32_bf16 v[244:247], v[48:51], v[196:199], v[244:247]\n" \
"s_waitcnt vmcnt(5)\n" \
"v_mfma_f32_16x16x32_bf16 v[240:243], v[50:53], v[204:207], v[240:243]\n" \
"v_mfma_f32_16x16x32_bf16 v[244:247], v[48:51], v[204:207], v[244:247]\n" \
"s_waitcnt vmcnt(4)\n" \
"v_mfma_f32_16x16x32_bf16 v[240:243], v[50:53], v[212:215], v[240:243]\n" \
"v_mfma_f32_16x16x32_bf16 v[244:247], v[48:51], v[212:215], v[244:247]\n" \
"s_waitcnt vmcnt(3)\n" \
"v_mfma_f32_16x16x32_bf16 v[240:243], v[50:53], v[220:223], v[240:243]\n" \
"v_mfma_f32_16x16x32_bf16 v[244:247], v[48:51], v[220:223], v[244:247]\n" \
"s_waitcnt vmcnt(2)\n" \
"v_mfma_f32_16x16x32_bf16 v[240:243], v[50:53], v[228:231], v[240:243]\n" \
"v_mfma_f32_16x16x32_bf16 v[244:247], v[48:51], v[228:231], v[244:247]\n" \
"s_waitcnt vmcnt(1)\n" \
"v_mfma_f32_16x16x32_bf16 v[240:243], v[50:53], v[236:239], v[240:243]\n" \
"v_mfma_f32_16x16x32_bf16 v[244:247], v[48:51], v[236:239], v[244:247]\n" \
"s_nop 6\n" \
"v_cvt_pk_bf16_f32 v240, v240, v241\n" \
"v_cvt_pk_bf16_f32 v244, v244, v245\n" \
"v_cvt_pk_bf16_f32 v241, v242, v243\n" \
"v_cvt_pk_bf16_f32 v245, v246, v247\n" \
"buffer_store_dwordx2 v[240:241], v42, s[88:91], s69 offen sc1\n" \
"buffer_store_dwordx2 v[244:245], v43, s[88:91], s69 offen sc1\n"
#define ATT_VCLOB "v21","v22","v23","v24","v25","v26","v27","v28","v29","v32","v33","v34","v35","v36","v37","v38","v39","v40","v41","v42","v43","v44","v45","v46","v47","v48","v49","v50","v51","v52","v53","v54","v55","v56","v57","v58","v59","v60","v61","v62","v63","v64","v65","v66","v67","v68","v69","v70","v71","v72","v73","v74","v75","v76","v77","v78","v79","v80","v81","v82","v83","v84","v85","v86","v87","v88","v89","v90","v91","v92","v93","v94","v95","v96","v97","v98","v99","v100","v101","v102","v103","v104","v105","v106","v107","v108","v109","v110","v111","v112","v113","v114","v115","v116","v117","v118","v119","v120","v121","v122","v123","v124","v125","v126","v127","v128","v129","v130","v131","v132","v133","v134","v135","v136","v137","v138","v139","v140","v141","v142","v143","v144","v145","v146","v147","v148","v149","v150","v151","v152","v153","v154","v155","v156","v157","v158","v159","v160","v161","v162","v163","v164","v165","v166","v167","v168","v169","v170","v171","v172","v173","v174","v175","v176","v177","v178","v179","v180","v181","v182","v183","v184","v185","v186","v187","v188","v189","v190","v191","v192","v193","v194","v195","v196","v197","v198","v199","v200","v201","v202","v203","v204","v205","v206","v207","v208","v209","v210","v211","v212","v213","v214","v215","v216","v217","v218","v219","v220","v221","v222","v223","v224","v225","v226","v227","v228","v229","v230","v231","v232","v233","v234","v235","v236","v237","v238","v239","v240","v241","v242","v243","v244","v245","v246","v247","v248","v249","v250","v251","v252","v253","v254","v255"
#define ATT_SCLOB "s63","s64","s65","s66","s67","s68","s69","s70","s71","s72","s73","s74","s75","s76","s77","s78","s79","s80","s81","s82","s83","s84","s85","s86","s87","s88","s89","s90","s91","s92","s93","s94","s95","s96","s97","s98","s99","s100","s101"
#define ATT_WTCH 0
enum { T_fo, T_fj, T_fa, T_fas, T_fb, T_fbs, T_f8, T_mk8, T_wt, T_wol, T_woh, T_wbl, T_wbh, T_outl, T_outh, T_r1l, T_r1h, T_r1n, T_r1s, T_m0, T_m1, T_mw, T_m3, T_m4, T_ml, T_w0, T_w1, T_w2, T_w3, T_A1, T_A2, T_A3, T_B1, T_B2, T_B3, T_Ao, T_Bo, T_Al, T_Bl, T_l8, T_xr, T_x8r, T_oA, T_oB, T_o8, T_x0, T_x1, T_x2, T_x3, T_x4, T_x5, T_x6, T_x8, T_ep };

__device__ __forceinline__ void out_consts(u32x4 (&vp)[3], const u16* wo, const u16* wob, const u16* out, const unsigned b, const unsigned w, const unsigned lane) {
    const unsigned sp = b & 7u, nt = (b >> 3) & 3u, j = b >> 5, head = 4u * sp + nt, OOR = 0x40000000u;
    unsigned vw[9];
#pragma unroll
    for (unsigned i = 0; i < 9u; ++i) {
        const unsigned q = 64u * i + lane, row = __umul24(q, 3641u) >> 16, c = q - __umul24(row, 18u);
        vw[i] = __umul24(row, DM * 2) + (c < 16u ? (c * 16u + sp * 32u + 32u) & 255u : c * 16u) - (i & 3u) * 1024u;
    }
    const unsigned row = 16u * w + (lane >> 2), r9 = __umul24(16u * (row >> 5) + (row & 12u) + 2u * ((row >> 4) & 1u) + ((row >> 1) & 1u), 18u) + 16u + (row & 1u);
    unsigned vwl = __umul24(r9, DM * 2) + ((lane & 2u) ? 256u : sp * 32u) + (lane & 1u) * 16u, vrd = __umul24(lane >> 2, 288u) + (lane & 3u) * 8u;
    unsigned tab = 0;
#define TB(n, v) asm("v_writelane_b32 %0, %1, %2" : "+v"(tab) : "s"((unsigned)(v)), "n"((int)T_##n))
    const unsigned FJ = (unsigned)WS_F + (F_J + 640u * j) * 4u, mt = 2u * (w & 2u) + (w & 1u), w2 = w < 2u ? w : 2u, hw = head * 147456u;
    const unsigned FR = (unsigned)WS_F + F_R * 4u + (8u * j + sp) * 1024u;
    TB(fo, FJ + 4u * head); TB(fj, FJ); TB(fa, FR + (w | 2u) * 256u); TB(fas, FR + w * 256u + nt * 4u);
    TB(fb, FR + 128u + w * 256u); TB(fbs, FR + 128u + w * 256u + nt * 4u);
    TB(ep, ssel(b + w, OOR, (unsigned)WS_F + F_EPOCH * 4u));
    const u16* pwo = wo + nt * 144u; const u16* pwb = wob + nt * 144u;
    TB(wol, PLO(pwo)); TB(woh, PHI(pwo) & 0xffffu); TB(wbl, PLO(pwb)); TB(wbh, PHI(pwb) & 0xffffu); TB(outl, PLO(out)); TB(outh, PHI(out) & 0xffffu); TB(r1l, sslt<3>(w, PLO(pwo), PLO(pwb))); TB(r1h, sslt<3>(w, PHI(pwo), PHI(pwb)) & 0xffffu);
    TB(r1n, sslt<3>(w, 72u * sp + 71u, 4095u) * 1152u + 288u); TB(r1s, sslt<3>(w, (72u * sp + 32u * w) * 1152u, hw + 110592u));
    TB(m0, 131072u + 9216u * w2); TB(w0, hw + 36864u * w2); TB(m1, 24576u + 9216u * w); TB(mw, 65536u + 9216u * w); TB(m3, sslt<3>(w, 102400u + 9216u * w, 0u)); TB(m4, sslt<3>(w, 131072u + 9216u * w, 9216u)); TB(ml, 61440u + 1024u * w);
    const unsigned tA = (unsigned)WS_AF + (8u * j + mt) * (KS_TOT * 1024u), tB = (unsigned)WS_AF + (120u - 8u * j + mt) * (KS_TOT * 1024u);
#define HK(k) (4u * sp + ((nt + k) & 3u))
    TB(w1, HK(1u) * 147456u + w * 36864u); TB(w2, HK(2u) * 147456u + w * 36864u); TB(w3, HK(3u) * 147456u + w * 36864u);
    TB(A1, tA + 18432u + HK(1u) * 4096u); TB(A2, tA + 18432u + HK(2u) * 4096u); TB(A3, tA + 18432u + HK(3u) * 4096u);
    TB(B1, tB + 18432u + HK(1u) * 4096u); TB(B2, tB + 18432u + HK(2u) * 4096u); TB(B3, tB + 18432u + HK(3u) * 4096u);
    TB(Ao, tA + sp * 2048u); TB(Bo, tB + sp * 2048u); TB(Al, tA + 16384u); TB(Bl, tB + 16384u);
    const unsigned nred = w ^ (sp >> 1), blk = sp & 1u;
    const unsigned xg = (unsigned)WS_X + (4u * j + nt) * 589824u + w * 16384u, oA = (128u * j + 16u * mt) * 1152u + nt * 288u, oB = (1920u - 128u * j + 16u * mt) * 1152u + nt * 288u;
    TB(l8, ssel(nred, OOR, ssel(blk, tB, tA) + 16384u)); TB(xr, xg + sp * 65536u); TB(x8r, ssel(nred, OOR, xg + 524288u + (sp & 1u) * 1024u));
    TB(oA, oA + sp * 32u); TB(oB, oB + sp * 32u); TB(o8, ssel(nred, OOR, ssel(blk, oB, oA) + 256u)); TB(x8, xg + 524288u + sp * 2048u);
    TB(f8, xg + w * 131072u + 14400u + sp * 4u); TB(mk8, ssel(nred, 0u, 0xff0000u)); TB(wt, hw + j * 18432u + w * 4608u);
#define XI(i) TB(x##i, xg + ((sp + 1u + i) & 7u) * 65536u + i * 2048u)
    XI(0); XI(1); XI(2); XI(3); XI(4); XI(5); XI(6);
#undef XI
#undef HK
#undef TB
    vp[0] = (u32x4){vw[0], vw[1], vw[2], vw[3]}; vp[1] = (u32x4){vw[4], vw[5], vw[6], vw[7]}; vp[2] = (u32x4){vw[8], vwl, vrd, tab};
}
__global__ __launch_bounds__(256, 1) __attribute__((aligned(4096))) void fused_kernel(const u16* __restrict__ x, const u16* __restrict__ ckv, const u16* __restrict__ wq, const u16* __restrict__ wuk,
                                                       const u16* __restrict__ wuv, const u16* __restrict__ wqkv, const u16* __restrict__ gw,
                                                       const u16* __restrict__ wo, const u16* __restrict__ wob, char* __restrict__ ws_, u16* __restrict__ out) {
    using namespace att;
    extern __shared__ __attribute__((aligned(16))) char lds[];
    u32x4 sa[5]; u32x2 s24, s28; char* ws; unsigned tdum; kaptr ka = (kaptr)__builtin_amdgcn_kernarg_segment_ptr();
    asm volatile("s_load_dwordx8 s[8:15], %[ka], 0x0\n\ts_load_dwordx8 s[16:23], %[ka], 0x20\n\ts_getpc_b64 s[4:5]\n\tv_lshlrev_b32_e32 v191, 7, %[t]\n\ts_bfe_u32 s6, %[b], 0x20003\n\t"
                 "v_lshl_add_u32 v191, s6, 15, v191\n\tv_min_u32_e32 v191, %[e], v191\n\tglobal_load_dword v191, v191, s[4:5]\n\ts_load_dwordx4 s[24:27], %[ka], 0x40\n\ts_load_dwordx2 s[28:29], %[ka], 0x50"
                 : "={s[8:11]}"(sa[0]), "={s[12:15]}"(sa[1]), "={s[16:19]}"(sa[2]), "={s[20:23]}"(sa[3]), "={s[24:25]}"(s24), "={s[26:27]}"(ws), "={s[28:29]}"(s28), "={v191}"(tdum), [ka] "+{s[0:1]}"(ka)
                 : [t] "v"(threadIdx.x), [b] "s"(blockIdx.x), [e] "i"(CT_END) : "s4", "s5", "s6", "scc");
    SBAR();
    const unsigned wid = rfl(threadIdx.x >> 6);
    unsigned tag, vl;
    const int b = blockIdx.x;
    u32x4 vp[5], srdA; u32x2 sd;
    proj_phase(ka, ws, wid, vp, vl, sa, s24, s28, tdum, srdA, sd, tag
    );
    unsigned* const flags = (unsigned*)(ws + WS_F);
    RTS(2);
#define AST(L) do {} while (0)
#define ATT_TAIL "s_endpgm\n"
    asm volatile(ATT_ASM ATT_TAIL "L_end_%=:\n s_nop 1"
                 :
                 : "{v[0:3]}"(vp[0]), "{v[4:7]}"(vp[1]), "{v[8:11]}"(vp[2]), "{v[12:15]}"(vp[3]), "{v[16:19]}"(vp[4]), "{v20}"(vl), "{s[56:59]}"(srdA), "{s[60:61]}"(sd), "{s62}"(tag),
                   "{s[8:11]}"(sa[0]), "{s[12:15]}"(sa[1]), "{s[16:19]}"(sa[2]), "{s[20:23]}"(sa[3]), "{s24}"(sa[4][0]), "{s3}"(wid)
                 : "memory", "m0", "vcc", "scc", ATT_VCLOB, ATT_SCLOB, ACLOB_LO, ACLOB_HI);
    {
        unsigned b2 = b, w2 = wid;
        asm volatile("s_waitcnt vmcnt(0)" : "+s"(ka), "+s"(b2), "+s"(w2), "+s"(ws) :: "memory");
        const unsigned j2 = b2 >> 5, p2 = w2 & 1u, h2 = (b2 & 7u) * 4u + ((b2 >> 3) & 3u);
#pragma unroll 1
        for (unsigned i = w2 & 2u; i < 2u + 3u * (w2 & 2u); ++i)
            att_exact(ka[0], ka[2], ws, h2, (i < 4u ? 1920u - 128u * j2 : 128u * j2 - 128u) + 32u * i + 16u * p2, *reinterpret_cast<const float*>(lds + LDS_G + (32u * i + 16u * p2 + (lane_id() & 15u)) * 4u));
        asm volatile("s_waitcnt vmcnt(0)" ::: "memory");
        unsigned* fa = (unsigned*)(ws + WS_F) + F_R + (8u * j2 + (b2 & 7u)) * 256u + (2u + p2) * 64u + ((b2 >> 3) & 3u);
        if (w2 & 2u) st_flag(fa, tag);
        wait_flags(fa, ld_flag(fa), tag);
        const unsigned l2 = lane_id(); u32x4 c[3]; out_consts(c, ka[7], ka[8], ka[10], b2, w2, l2);
        asm volatile("s_waitcnt vmcnt(0) lgkmcnt(0)\n\ts_nop 7\n\ts_branch L_att_stub" :: "{v[0:3]}"(c[0]), "{v[4:7]}"(c[1]), "{v[8:11]}"(c[2]), "{v20}"(l2 * 16u), "{s[56:59]}"(SRDA), "{s60}"(SD(8u * j2, h2, w2)),
                     "{s61}"(SD(120u - 8u * j2, h2, w2)), "{s62}"(tag) : "memory");
    }
    RTS(5);
}

extern "C" void kernel_launch(void* const* d_in, const int* in_sizes, int n_in, void* d_out, int out_size, void* d_ws, size_t ws_size,
                              hipStream_t stream) {
    (void)hipFuncSetAttribute((const void*)fused_kernel, hipFuncAttributeMaxDynamicSharedMemorySize, att::LDS_BYTES);
    fused_kernel<<<dim3(256), 256, att::LDS_BYTES, stream>>>((const u16*)d_in[0], (const u16*)d_in[4], (const u16*)d_in[5], (const u16*)d_in[6], (const u16*)d_in[7], (const u16*)d_in[1],
                                                             (const u16*)d_in[9], (const u16*)d_in[2], (const u16*)d_in[8], (char*)d_ws, (u16*)d_out);
}
```

```cpp
#include <hip/hip_runtime.h>
#include <hip/hip_bf16.h>
#include <stdint.h>
#include <utility>

typedef short bf16x8 __attribute__((ext_vector_type(8)));
typedef short s16x4 __attribute__((ext_vector_type(4)));
typedef float f32x16 __attribute__((ext_vector_type(16)));
typedef float f32x4 __attribute__((ext_vector_type(4)));
typedef unsigned u32x4 __attribute__((ext_vector_type(4)));
typedef unsigned u32x2 __attribute__((ext_vector_type(2)));
typedef unsigned short u16;

constexpr int S = 2048, DM = 576, NH = 32, HD = 128, DC = 512, NQKV = 1728, DHA = 18;
constexpr int NBIG = NH * HD;
constexpr int KS_TOT = 146;

#define SBAR() __builtin_amdgcn_sched_barrier(0)
constexpr size_t WS_Q = 0, WS_K = WS_Q + (size_t)S * NBIG * 2, WS_V = WS_K + (size_t)S * NBIG * 2, WS_AF = WS_V + (size_t)S * NBIG * 2, WS_X = WS_AF + (size_t)128 * KS_TOT * 1024,
                 WS_P = WS_X + (size_t)64 * 16 * 9216, WS_F = WS_P + (size_t)128 * (4 * 32 * 256 + 1024) * 4;
constexpr size_t WS_L = (size_t)96 << 20;
static_assert(WS_F == 96206848, "workspace map is baked into the generated asm");
constexpr int F_EPOCH = 0, F_J = 1024, F_ST = 8192, F_KV = 40960, F_R = 65536;
constexpr unsigned TAG_STEP = 0x9E3779B1u;
__device__ __forceinline__ unsigned ld_flag(const unsigned* p) { return __hip_atomic_load(p, __ATOMIC_RELAXED, __HIP_MEMORY_SCOPE_AGENT); }
__device__ __forceinline__ void st_flag(unsigned* p, unsigned v) { __hip_atomic_store(p, v, __ATOMIC_RELAXED, __HIP_MEMORY_SCOPE_AGENT); }
__device__ __forceinline__ unsigned ld_flag_async(const unsigned* p) { unsigned v; asm volatile("global_load_dword %0, %1, off sc1" : "=v"(v) : "v"(p) : "memory"); return v; }
#define WAITV(v, N) asm volatile("s_waitcnt vmcnt(" #N ")" : "+v"(v) :: "memory")
__device__ __forceinline__ void wait_flags(const unsigned* p, unsigned v, unsigned tag) {
    if (__builtin_expect(__builtin_amdgcn_ballot_w64(v != tag) != 0ull, 0)) {
        unsigned spins = 0;
        do { __builtin_amdgcn_s_sleep(1); } while (__builtin_amdgcn_ballot_w64(ld_flag(p) != tag) != 0ull && ++spins < (1u << 22));
    }
    asm volatile("" ::: "memory");
}

__device__ __forceinline__ unsigned cvtpk(float lo, float hi) {
    unsigned r; asm volatile("v_cvt_pk_bf16_f32 %0, %1, %2" : "=v"(r) : "v"(lo), "v"(hi)); return r;
}
__device__ __forceinline__ float bf2f(u16 v) { return __uint_as_float(((unsigned)v) << 16); }
__device__ __forceinline__ int crow(int r, int hi) { return (r & 3) + 8 * (r >> 2) + 4 * hi; }

typedef __attribute__((address_space(3))) s16x4* lds_s16x4_p;
__device__ __forceinline__ s16x4 trrd(unsigned addr) { return __builtin_amdgcn_ds_read_tr16_b64_v4i16((lds_s16x4_p)addr); }

__device__ __forceinline__ unsigned lane_id() { unsigned l; asm volatile("v_mbcnt_lo_u32_b32 %0, -1, 0\n\tv_mbcnt_hi_u32_b32 %0, -1, %0" : "=v"(l)); return l; }
#define RTS(slot) do {} while (0)
#define CT_END 0x1e380
#define ACLOB_LO "a0","a1","a2","a3","a4","a5","a6","a7","a8","a9","a10","a11","a12","a13","a14","a15","a16","a17","a18","a19","a20","a21","a22","a23","a24","a25","a26","a27","a28","a29","a30","a31","a32","a33","a34","a35","a36","a37","a38","a39","a40","a41","a42","a43","a44","a45","a46","a47","a48","a49","a50","a51","a52","a53","a54","a55","a56","a57","a58","a59","a60","a61","a62","a63","a64","a65","a66","a67","a68","a69","a70","a71","a72","a73","a74","a75","a76","a77","a78","a79","a80","a81","a82","a83","a84","a85","a86","a87","a88","a89","a90","a91","a92","a93","a94","a95","a96","a97","a98","a99","a100","a101","a102","a103","a104","a105","a106","a107","a108","a109","a110","a111","a112","a113","a114","a115","a116","a117","a118","a119","a120","a121","a122","a123","a124","a125","a126","a127"
#define ACLOB_HI "a128","a129","a130","a131","a132","a133","a134","a135","a136","a137","a138","a139","a140","a141","a142","a143","a144","a145","a146","a147","a148","a149","a150","a151","a152","a153","a154","a155","a156","a157","a158","a159","a160","a161","a162","a163","a164","a165","a166","a167","a168","a169","a170","a171","a172","a173","a174","a175","a176","a177","a178","a179","a180","a181","a182","a183","a184","a185","a186","a187","a188","a189","a190","a191","a192","a193","a194","a195","a196","a197","a198","a199","a200","a201","a202","a203","a204","a205","a206","a207","a208","a209","a210","a211","a212","a213","a214","a215","a216","a217","a218","a219","a220","a221","a222","a223","a224","a225","a226","a227","a228","a229","a230","a231","a232","a233","a234","a235","a236","a237","a238","a239","a240","a241","a242","a243","a244","a245","a246","a247","a248","a249","a250","a251","a252","a253","a254","a255"
#define PLO(p) ((unsigned)(size_t)(p))
#define PHI(p) ((unsigned)((size_t)(p) >> 32))
__device__ __forceinline__ unsigned vsel(bool c, unsigned a, unsigned b) { asm("" : "+v"(a), "+v"(b)); return c ? a : b; }
__device__ __forceinline__ unsigned ssel(unsigned c, unsigned a, unsigned b) { unsigned r; asm("s_cmp_lg_u32 %1, 0\n\ts_cselect_b32 %0, %2, %3" : "=s"(r) : "s"(c), "s"(a), "s"(b) : "scc"); return r; }
template <int N> __device__ __forceinline__ unsigned sslt(unsigned c, unsigned a, unsigned b) { unsigned r; asm("s_cmpk_lt_u32 %1, %4\n\ts_cselect_b32 %0, %2, %3" : "=s"(r) : "s"(c), "s"(a), "s"(b), "n"(N) : "scc"); return r; }
namespace att {
constexpr int LDS_BYTES = 163840;
constexpr unsigned LDS_G = 159744u, LDS_L = 161792u;
}
#define SD(J8, H, W) ((unsigned)WS_AF + (((J8) + 2u * ((W) & 2u) + ((W) & 1u)) * KS_TOT + 18u + 4u * (H)) * 1024u)
#define SRDA (u32x4){PLO(ws), PHI(ws) & 0xffffu, (unsigned)WS_L, 0x00020000u}
#define PROJ_ASM \
".p2align 3\n" \
"L_pb:\n" \
"s_waitcnt lgkmcnt(0)\n" \
"s_mov_b32 s64, s10\n" \
"s_and_b32 s65, s11, 0xffff\n" \
"s_mov_b32 s66, 0x200000\n" \
"s_mov_b32 s67, 0x20000\n" \
"s_mov_b32 s68, s14\n" \
"s_and_b32 s69, s15, 0xffff\n" \
"s_mov_b32 s70, 0x400000\n" \
"s_mov_b32 s71, 0x20000\n" \
"s_mov_b32 s72, s16\n" \
"s_and_b32 s73, s17, 0xffff\n" \
"s_mov_b32 s74, 0x400000\n" \
"s_mov_b32 s75, 0x20000\n" \
"s_add_u32 m0, s30, 0x0\n" \
"s_mov_b32 s85, 0x0\n" \
"buffer_load_dwordx4 v22, s[64:67], s85 offen lds\n" \
"s_add_u32 m0, s30, 0x400\n" \
"s_mov_b32 s84, 0x2000\n" \
"buffer_load_dwordx4 v23, s[64:67], s84 offen lds\n" \
"s_add_u32 m0, s31, 0x0\n" \
"s_mov_b32 s85, 0x0\n" \
"buffer_load_dwordx4 v26, s[68:71], s85 offen lds\n" \
"s_add_u32 m0, s31, 0x4000\n" \
"s_mov_b32 s84, 0x0\n" \
"s_nop 0\n" \
"buffer_load_dwordx4 v26, s[72:75], s84 offen lds\n" \
"s_add_u32 m0, s30, 0x800\n" \
"s_mov_b32 s85, 0x4000\n" \
"buffer_load_dwordx4 v22, s[64:67], s85 offen lds\n" \
"s_add_u32 m0, s30, 0xc00\n" \
"s_mov_b32 s84, 0x6000\n" \
"buffer_load_dwordx4 v23, s[64:67], s84 offen lds\n" \
"s_add_u32 m0, s31, 0x400\n" \
"s_mov_b32 s85, 0x8000\n" \
"buffer_load_dwordx4 v26, s[68:71], s85 offen lds\n" \
"s_add_u32 m0, s31, 0x4400\n" \
"s_mov_b32 s84, 0x8000\n" \
"buffer_load_dwordx4 v26, s[72:75], s84 offen lds\n" \
"s_movk_i32 s7, 0x300\n" \
"s_lshr_b32 s4, s2, 1\n" \
"s_and_b32 s4, s4, 60\n" \
"s_or_b32 s4, s4, s3\n" \
"v_lshrrev_b32_e32 v212, 4, v20\n" \
"v_min_u32_e32 v212, 59, v212\n" \
"s_cmpk_lt_u32 s2, 0x80\n" \
"s_cselect_b32 s6, s14, s16\n" \
"v_subrev_u32_e32 v214, 48, v212\n" \
"v_mov_b32_e32 v212, s10\n" \
"v_mov_b32_e32 v215, s6\n" \
"s_cmpk_lt_u32 s2, 0x80\n" \
"s_cselect_b32 s5, s15, s17\n" \
"v_cmp_gt_u32_e32 vcc, s7, v20\n" \
"v_mov_b32_e64 v213, s11\n" \
"v_bfe_u32 v216, v20, 4, 3\n" \
"v_cndmask_b32_e32 v212, v212, v215, vcc\n" \
"v_mov_b32_e32 v215, s5\n" \
"s_lshl_b32 s5, s3, 4\n" \
"v_cndmask_b32_e32 v213, v213, v215, vcc\n" \
"v_lshlrev_b32_e64 v215, 5, v20\n" \
"v_and_b32_e32 v215, 0x7000, v215\n" \
"v_lshl_add_u32 v215, s4, 6, v215\n" \
"s_lshl_b32 s4, s2, 3\n" \
"s_and_b32 s4, s4, 56\n" \
"v_or3_b32 v215, v215, s4, v216\n" \
"s_lshl_b32 s4, s2, 6\n" \
"s_or_b32 s6, s5, s4\n" \
"v_cmp_gt_u32_e64 s[4:5], 6, v214\n" \
"v_add_u32_e32 v215, 0x2000, v215\n" \
"s_nop 0\n" \
"v_cndmask_b32_e64 v216, 4, 2, s[4:5]\n" \
"v_add3_u32 v214, s6, v214, v216\n" \
"s_lshl_b32 s4, s3, 9\n" \
"v_cndmask_b32_e32 v214, v214, v215, vcc\n" \
"v_lshlrev_b32_e32 v214, 7, v214\n" \
"v_mov_b32_e64 v215, 0\n" \
"v_lshl_add_u64 v[184:185], v[212:213], 0, v[214:215]\n" \
"s_or_b32 s32, s4, 0x25000\n" \
"s_add_u32 m0, s30, 0x1000\n" \
"s_mov_b32 s85, 0x8000\n" \
"buffer_load_dwordx4 v22, s[64:67], s85 offen lds\n" \
"s_and_b32 s33, s3, 1\n" \
"v_lshrrev_b32_e32 v212, 4, v20\n" \
"v_lshrrev_b32_e64 v214, 8, v20\n" \
"v_bfe_u32 v212, v212, 1, 3\n" \
"v_lshlrev_b32_e64 v215, 2, v20\n" \
"s_add_u32 m0, s30, 0x1400\n" \
"s_mov_b32 s84, 0xa000\n" \
"buffer_load_dwordx4 v23, s[64:67], s84 offen lds\n" \
"v_lshrrev_b32_e64 v216, 1, v20\n" \
"v_bfe_u32 v213, v20, 4, 4\n" \
"s_lshl_b32 s4, s3, 5\n" \
"v_xor_b32_e32 v212, v212, v214\n" \
"v_lshlrev_b32_e64 v214, 11, v214\n" \
"s_add_u32 m0, s31, 0x800\n" \
"s_mov_b32 s85, 0x10000\n" \
"buffer_load_dwordx4 v27, s[68:71], s85 offen lds\n" \
"v_and_b32_e32 v215, 0x300, v215\n" \
"v_and_b32_e64 v217, 24, v216\n" \
"v_and_or_b32 v213, s4, 64, v213\n" \
"v_or3_b32 v214, v215, v214, v217\n" \
"s_lshl_b32 s4, s33, 14\n" \
"s_add_u32 m0, s31, 0x4800\n" \
"s_mov_b32 s84, 0x10000\n" \
"buffer_load_dwordx4 v27, s[72:75], s84 offen lds\n" \
"v_and_b32_e32 v215, 0xe0, v216\n" \
"v_lshlrev_b32_e32 v213, 7, v213\n" \
"v_lshlrev_b32_e64 v212, 4, v212\n" \
"v_or3_b32 v214, v214, s4, v215\n" \
"v_or_b32_e64 v28, v212, v213\n" \
"s_add_u32 m0, s30, 0x1800\n" \
"s_mov_b32 s85, 0xc000\n" \
"buffer_load_dwordx4 v22, s[64:67], s85 offen lds\n" \
"v_add_u32_e32 v32, 0x10000, v214\n" \
"v_bitop3_b32 v29, v212, 64, v213 bitop3:0x36\n" \
"v_xor_b32_e32 v33, 0x20, v32\n" \
"v_xor_b32_e32 v34, 0x40, v32\n" \
"v_xor_b32_e32 v35, 0x60, v32\n" \
"s_add_u32 m0, s30, 0x1c00\n" \
"s_mov_b32 s84, 0xe000\n" \
"buffer_load_dwordx4 v23, s[64:67], s84 offen lds\n" \
"v_xor_b32_e32 v36, 0x80, v32\n" \
"v_xor_b32_e32 v37, 0xa0, v32\n" \
"v_xor_b32_e32 v38, 0xc0, v32\n" \
"v_xor_b32_e32 v39, 0xe0, v32\n" \
"s_mov_b32 s80, s26\n" \
"s_add_u32 m0, s31, 0xc00\n" \
"s_mov_b32 s85, 0x18000\n" \
"buffer_load_dwordx4 v27, s[68:71], s85 offen lds\n" \
"s_and_b32 s81, s27, 0xffff\n" \
"s_mov_b32 s82, 0x10000000\n" \
"s_mov_b32 s83, 0x20000\n" \
"s_mov_b32 s76, s18\n" \
"s_and_b32 s77, s19, 0xffff\n" \
"s_add_u32 m0, s31, 0x4c00\n" \
"s_mov_b32 s84, 0x18000\n" \
"buffer_load_dwordx4 v27, s[72:75], s84 offen lds\n" \
"s_mov_b32 s78, 0x1e6000\n" \
"s_mov_b32 s79, 0x20000\n" \
"s_branch L_pgo_%=\n" \
"L_pret:\n" \
"s_setpc_b64 s[88:89]\n" \
"L_pgo_%=:\n" \
"s_mul_i32 s88, s33, 0xb000\n" \
"s_lshl_b32 s89, s3, 11\n" \
"s_and_b32 s89, s89, 0x1000\n" \
"s_add_u32 s88, s88, s89\n" \
"s_getpc_b64 s[86:87]\n" \
"L_hpa_%=:\n" \
"s_add_u32 s86, s86, L_pp1-L_hpa_%=\n" \
"s_addc_u32 s87, s87, 0\n" \
"s_add_u32 s86, s86, s88\n" \
"s_addc_u32 s87, s87, 0\n" \
"s_add_u32 m0, s30, 0x8000\n" \
"s_mov_b32 s85, 0x80\n" \
"buffer_load_dwordx4 v22, s[64:67], s85 offen lds\n" \
"s_lshr_b32 s4, s2, 1\n" \
"s_and_b32 s4, s4, 0x7c\n" \
"v_bfe_u32 v214, v20, 4, 5\n" \
"s_add_u32 m0, s30, 0x8400\n" \
"s_mov_b32 s84, 0x2080\n" \
"buffer_load_dwordx4 v23, s[64:67], s84 offen lds\n" \
"s_or_b32 s7, s4, s3\n" \
"v_min_u32_e64 v214, 26, v214\n" \
"v_mad_u64_u32 v[214:215], s[4:5], s7, 27, v[214:215]\n" \
"s_add_u32 m0, s31, 0x8000\n" \
"s_mov_b32 s85, 0x80000\n" \
"buffer_load_dwordx4 v26, s[68:71], s85 offen lds\n" \
"v_mul_u32_u24_e32 v215, 0xaaab, v214\n" \
"v_lshrrev_b32_e64 v215, 18, v215\n" \
"s_mov_b32 s4, 0x1fffffa\n" \
"s_add_u32 m0, s31, 0xc000\n" \
"s_mov_b32 s84, 0x80000\n" \
"buffer_load_dwordx4 v26, s[72:75], s84 offen lds\n" \
"v_mad_u64_u32 v[216:217], s[4:5], v215, s4, v[214:215]\n" \
"s_and_b32 s6, s2, 7\n" \
"v_mul_u32_u24_e32 v215, 27, v215\n" \
"s_mov_b64 exec, 0\n" \
"s_swappc_b64 s[88:89], s[86:87]\n" \
"s_mov_b64 exec, -1\n" \
"s_add_u32 s86, s86, 0x2000\n" \
"s_addc_u32 s87, s87, 0\n" \
"s_add_u32 m0, s30, 0x8800\n" \
"s_mov_b32 s85, 0x4080\n" \
"buffer_load_dwordx4 v22, s[64:67], s85 offen lds\n" \
"v_lshrrev_b32_e32 v216, 1, v216\n" \
"v_and_b32_e32 v214, 1, v214\n" \
"v_mov_b32_e64 v213, s13\n" \
"s_add_u32 m0, s30, 0x8c00\n" \
"s_mov_b32 s84, 0x6080\n" \
"buffer_load_dwordx4 v23, s[64:67], s84 offen lds\n" \
"v_lshrrev_b32_e32 v212, 4, v20\n" \
"v_mul_u32_u24_e32 v216, 9, v216\n" \
"v_add3_u32 v214, v214, s6, v215\n" \
"s_mov_b64 exec, 0\n" \
"s_swappc_b64 s[88:89], s[86:87]\n" \
"s_mov_b64 exec, -1\n" \
"s_add_u32 s86, s86, 0x2000\n" \
"s_addc_u32 s87, s87, 0\n" \
"s_add_u32 m0, s31, 0x8400\n" \
"s_mov_b32 s85, 0x88000\n" \
"buffer_load_dwordx4 v26, s[68:71], s85 offen lds\n" \
"v_add_lshl_u32 v214, v214, v216, 7\n" \
"v_mad_u64_u32 v[216:217], s[4:5], s7, 36, v[212:213]\n" \
"s_movk_i32 s4, 0x240\n" \
"s_add_u32 m0, s31, 0xc400\n" \
"s_mov_b32 s84, 0x88000\n" \
"buffer_load_dwordx4 v26, s[72:75], s84 offen lds\n" \
"v_mov_b32_e32 v219, s9\n" \
"v_mov_b32_e32 v215, 0\n" \
"v_cmp_gt_u32_e32 vcc, s4, v20\n" \
"s_mov_b64 exec, 0\n" \
"s_swappc_b64 s[88:89], s[86:87]\n" \
"s_mov_b64 exec, -1\n" \
"s_add_u32 s86, s86, 0x2000\n" \
"s_addc_u32 s87, s87, 0\n" \
"s_add_u32 m0, s30, 0x9000\n" \
"s_mov_b32 s85, 0x8080\n" \
"buffer_load_dwordx4 v22, s[64:67], s85 offen lds\n" \
"s_lshl_b32 s4, s2, 2\n" \
"v_lshl_add_u64 v[186:187], s[18:19], 0, v[214:215]\n" \
"v_mov_b32_e32 v214, s12\n" \
"s_add_u32 m0, s30, 0x9400\n" \
"s_mov_b32 s84, 0xa080\n" \
"buffer_load_dwordx4 v23, s[64:67], s84 offen lds\n" \
"v_mov_b32_e32 v217, s8\n" \
"s_or_b32 s4, s4, s3\n" \
"v_cndmask_b32_e64 v219, v219, v213, vcc\n" \
"s_add_u32 m0, s31, 0x8800\n" \
"s_mov_b32 s85, 0x90000\n" \
"buffer_load_dwordx4 v27, s[68:71], s85 offen lds\n" \
"v_and_b32_e32 v213, 0x3ff8, v216\n" \
"s_mul_i32 s4, s4, 18\n" \
"v_cndmask_b32_e32 v218, v217, v214, vcc\n" \
"s_mov_b64 exec, 0\n" \
"s_swappc_b64 s[88:89], s[86:87]\n" \
"s_mov_b64 exec, -1\n" \
"s_add_u32 s86, s86, 0x2000\n" \
"s_addc_u32 s87, s87, 0\n" \
"s_add_u32 m0, s31, 0xc800\n" \
"s_mov_b32 s84, 0x90000\n" \
"buffer_load_dwordx4 v27, s[72:75], s84 offen lds\n" \
"v_or_b32_e32 v213, s6, v213\n" \
"v_and_b32_e32 v214, 7, v216\n" \
"v_min_u32_e64 v212, 53, v212\n" \
"s_add_u32 m0, s30, 0x9800\n" \
"s_mov_b32 s85, 0xc080\n" \
"buffer_load_dwordx4 v22, s[64:67], s85 offen lds\n" \
"s_sub_i32 s4, s4, 36\n" \
"v_lshl_or_b32 v213, v213, 3, v214\n" \
"v_add_u32_e32 v212, s4, v212\n" \
"s_mov_b64 exec, 0\n" \
"s_swappc_b64 s[88:89], s[86:87]\n" \
"s_mov_b64 exec, -1\n" \
"s_add_u32 s86, s86, 0x2000\n" \
"s_addc_u32 s87, s87, 0\n" \
"s_add_u32 m0, s30, 0x9c00\n" \
"s_mov_b32 s84, 0xe080\n" \
"buffer_load_dwordx4 v23, s[64:67], s84 offen lds\n" \
"v_cndmask_b32_e32 v212, v212, v213, vcc\n" \
"v_lshlrev_b32_e32 v214, 7, v212\n" \
"v_lshl_add_u64 v[188:189], v[218:219], 0, v[214:215]\n" \
"s_add_u32 m0, s31, 0x8c00\n" \
"s_mov_b32 s85, 0x98000\n" \
"buffer_load_dwordx4 v27, s[68:71], s85 offen lds\n" \
"s_movk_i32 s5, 0xd80\n" \
"s_lshr_b32 s6, s2, 5\n" \
"v_lshrrev_b32_e64 v212, 4, v20\n" \
"s_add_u32 m0, s31, 0xcc00\n" \
"s_mov_b32 s84, 0x98000\n" \
"buffer_load_dwordx4 v27, s[72:75], s84 offen lds\n" \
"v_lshl_or_b32 v215, s3, 6, v212\n" \
"v_mul_u32_u24_e32 v216, 0x2aab, v215\n" \
"v_lshrrev_b32_e32 v216, 16, v216\n" \
"s_mov_b32 m0, s32\n" \
"s_mov_b32 exec_lo, 0xffffff\n" \
"s_mov_b32 exec_hi, 0xfff0000\n" \
"global_load_lds_dword v[184:185], off\n" \
"s_mov_b64 exec, -1\n" \
"s_mov_b32 s84, 0x5bc0000\n" \
"buffer_load_dword v180, off, s[80:83], s84 sc1\n" \
"s_cmp_lg_u32 s33, 0\n" \
"s_cbranch_scc1 L_w1_%=\n" \
"L_w0_%=:\n" \
"s_waitcnt vmcnt(18)\n" \
"s_barrier\n" \
"s_nop 0\n" \
"ds_read_b64_tr_b16 v[84:85], v32 offset:0\n" \
"ds_read_b64_tr_b16 v[86:87], v32 offset:1024\n" \
"ds_read_b64_tr_b16 v[148:149], v32 offset:8192\n" \
"ds_read_b64_tr_b16 v[150:151], v32 offset:9216\n" \
"ds_read_b128 v[52:55], v28 offset:16384\n" \
"ds_read_b128 v[116:119], v29 offset:16384\n" \
"ds_read_b64_tr_b16 v[88:89], v33 offset:0\n" \
"ds_read_b64_tr_b16 v[90:91], v33 offset:1024\n" \
"ds_read_b64_tr_b16 v[152:153], v33 offset:8192\n" \
"ds_read_b64_tr_b16 v[154:155], v33 offset:9216\n" \
"ds_read_b64_tr_b16 v[92:93], v34 offset:0\n" \
"ds_read_b64_tr_b16 v[94:95], v34 offset:1024\n" \
"ds_read_b64_tr_b16 v[156:157], v34 offset:8192\n" \
"ds_read_b64_tr_b16 v[158:159], v34 offset:9216\n" \
"ds_read_b64_tr_b16 v[96:97], v35 offset:0\n" \
"ds_read_b64_tr_b16 v[98:99], v35 offset:1024\n" \
"ds_read_b64_tr_b16 v[160:161], v35 offset:8192\n" \
"ds_read_b64_tr_b16 v[162:163], v35 offset:9216\n" \
"ds_read_b128 v[56:59], v28 offset:18432\n" \
"ds_read_b128 v[120:123], v29 offset:18432\n" \
"ds_read_b128 v[60:63], v28 offset:20480\n" \
"ds_read_b128 v[124:127], v29 offset:20480\n" \
"ds_read_b128 v[64:67], v28 offset:22528\n" \
"ds_read_b128 v[128:131], v29 offset:22528\n" \
"ds_read_b128 v[68:71], v28 offset:0\n" \
"ds_read_b128 v[132:135], v29 offset:0\n" \
"ds_read_b128 v[72:75], v28 offset:2048\n" \
"ds_read_b128 v[136:139], v29 offset:2048\n" \
"ds_read_b128 v[76:79], v28 offset:4096\n" \
"ds_read_b128 v[140:143], v29 offset:4096\n" \
"ds_read_b128 v[80:83], v28 offset:6144\n" \
"ds_read_b128 v[144:147], v29 offset:6144\n" \
"s_waitcnt lgkmcnt(15)\n" \
"v_mfma_f32_16x16x32_bf16 a[0:3], v[84:87], v[52:55], 0\n" \
"ds_read_b64_tr_b16 v[100:101], v36 offset:0\n" \
"v_mad_i32_i24 v217, v216, -6, v215\n" \
"v_mfma_f32_16x16x32_bf16 a[0:3], v[148:151], v[116:119], a[0:3]\n" \
"v_add_u32_e32 v218, 0xfffffe50, v215\n" \
"s_mul_i32 s35, s6, 0x48\n" \
"v_mfma_f32_16x16x32_bf16 a[4:7], v[88:91], v[52:55], 0\n" \
"v_lshrrev_b32_e32 v219, 1, v217\n" \
"v_add_u32_e64 v218, s35, v216\n" \
"v_mfma_f32_16x16x32_bf16 a[4:7], v[152:155], v[116:119], a[4:7]\n" \
"ds_read_b64_tr_b16 v[102:103], v36 offset:1024\n" \
"v_mul_u32_u24_e32 v219, 0x480, v219\n" \
"v_mfma_f32_16x16x32_bf16 a[8:11], v[92:95], v[52:55], 0\n" \
"v_and_b32_e64 v214, 16, v20\n" \
"v_mad_u32_u24 v219, v218, s5, v219\n" \
"v_mfma_f32_16x16x32_bf16 a[8:11], v[156:159], v[116:119], a[8:11]\n" \
"v_or_b32_e32 v192, v219, v214\n" \
"v_mov_b32_e32 v220, 2.0\n" \
"v_mfma_f32_16x16x32_bf16 a[12:15], v[96:99], v[52:55], 0\n" \
"ds_read_b64_tr_b16 v[164:165], v36 offset:8192\n" \
"v_mov_b32_e32 v222, 7\n" \
"s_waitcnt lgkmcnt(15)\n" \
"v_mfma_f32_16x16x32_bf16 a[12:15], v[160:163], v[116:119], a[12:15]\n" \
"v_lshlrev_b32_e32 v220, 7, v218\n" \
"v_lshrrev_b32_e32 v218, 1, v218\n" \
"v_mfma_f32_16x16x32_bf16 a[44:47], v[96:99], v[56:59], 0\n" \
"v_and_b32_e32 v218, 4, v218\n" \
"v_and_b32_e32 v216, 2, v216\n" \
"v_mfma_f32_16x16x32_bf16 a[44:47], v[160:163], v[120:123], a[44:47]\n" \
"ds_read_b64_tr_b16 v[166:167], v36 offset:9216\n" \
"s_mul_i32 s7, s6, 0x2400\n" \
"v_mfma_f32_16x16x32_bf16 a[40:43], v[92:95], v[56:59], 0\n" \
"s_sub_i32 s34, 0x22000, s7\n" \
"v_bitop3_b32 v216, v217, v218, v216 bitop3:0x1e\n" \
"v_mfma_f32_16x16x32_bf16 a[40:43], v[156:159], v[120:123], a[40:43]\n" \
"v_or_b32_e32 v213, 0x24400, v20\n" \
"v_lshlrev_b32_e64 v216, 4, v216\n" \
"v_mfma_f32_16x16x32_bf16 a[36:39], v[88:91], v[56:59], 0\n" \
"v_add3_u32 v196, v220, s34, v216\n" \
"v_mov_b32_e64 v216, v213\n" \
"v_mfma_f32_16x16x32_bf16 a[36:39], v[152:155], v[120:123], a[36:39]\n" \
"ds_read_b64_tr_b16 v[104:105], v37 offset:0\n" \
"s_movk_i32 s36, 0x1b0\n" \
"v_mfma_f32_16x16x32_bf16 a[32:35], v[84:87], v[56:59], 0\n" \
"v_or_b32_e32 v216, 0x100, v215\n" \
"v_mul_u32_u24_e32 v217, 0x2aab, v216\n" \
"v_mfma_f32_16x16x32_bf16 a[32:35], v[148:151], v[120:123], a[32:35]\n" \
"v_add_u32_e32 v215, 0xffffff50, v215\n" \
"v_lshrrev_b32_e32 v217, 16, v217\n" \
"s_waitcnt lgkmcnt(15)\n" \
"v_mfma_f32_16x16x32_bf16 a[64:67], v[84:87], v[60:63], 0\n" \
"s_mov_b32 m0, s32\n" \
"ds_read_b64_tr_b16 v[106:107], v37 offset:1024\n" \
"v_mfma_f32_16x16x32_bf16 a[64:67], v[148:151], v[124:127], a[64:67]\n" \
"s_mov_b32 exec_lo, 0xff000000\n" \
"s_mov_b32 exec_hi, 0x0\n" \
"global_load_lds_dword v[184:185], off\n" \
"s_mov_b64 exec, -1\n" \
"v_mfma_f32_16x16x32_bf16 a[68:71], v[88:91], v[60:63], 0\n" \
"v_mov_b32_e64 v220, v215\n" \
"v_mad_i32_i24 v218, v217, -6, v216\n" \
"v_mfma_f32_16x16x32_bf16 a[68:71], v[152:155], v[124:127], a[68:71]\n" \
"ds_read_b64_tr_b16 v[168:169], v37 offset:8192\n" \
"v_cmp_gt_u32_e64 vcc, s36, v216\n" \
"v_mfma_f32_16x16x32_bf16 a[72:75], v[92:95], v[60:63], 0\n" \
"v_mov_b32_e32 v221, 7\n" \
"v_lshrrev_b32_e32 v222, 1, v218\n" \
"v_mfma_f32_16x16x32_bf16 a[72:75], v[156:159], v[124:127], a[72:75]\n" \
"s_mov_b32 m0, s32\n" \
"v_cndmask_b32_e32 v217, v220, v217, vcc\n" \
"v_mfma_f32_16x16x32_bf16 a[76:79], v[96:99], v[60:63], 0\n" \
"s_mov_b32 exec_lo, 0xffff\n" \
"s_mov_b32 exec_hi, 0x0\n" \
"global_load_lds_dword v[186:187], off\n" \
"s_mov_b64 exec, -1\n" \
"v_mfma_f32_16x16x32_bf16 a[76:79], v[160:163], v[124:127], a[76:79]\n" \
"ds_read_b64_tr_b16 v[170:171], v37 offset:9216\n" \
"s_movk_i32 s4, 0x48\n" \
"s_waitcnt lgkmcnt(15)\n" \
"v_mfma_f32_16x16x32_bf16 a[108:111], v[96:99], v[64:67], 0\n" \
"v_add_u32_e64 v220, s35, v217\n" \
"v_mul_u32_u24_e32 v222, 0x480, v222\n" \
"v_mfma_f32_16x16x32_bf16 a[108:111], v[160:163], v[128:131], a[108:111]\n" \
"v_mad_u32_u24 v222, v220, s5, v222\n" \
"v_cmp_gt_u32_e64 s[4:5], s4, v215\n" \
"v_mfma_f32_16x16x32_bf16 a[104:107], v[92:95], v[64:67], 0\n" \
"ds_read_b64_tr_b16 v[108:109], v38 offset:0\n" \
"v_cndmask_b32_e64 v215, v221, v218, vcc\n" \
"v_mfma_f32_16x16x32_bf16 a[104:107], v[156:159], v[128:131], a[104:107]\n" \
"v_lshrrev_b32_e32 v218, 1, v220\n" \
"v_or_b32_e32 v214, v222, v214\n" \
"v_mfma_f32_16x16x32_bf16 a[100:103], v[88:91], v[64:67], 0\n" \
"v_mov_b32_e32 v222, 2.0\n" \
"v_and_b32_e32 v218, 4, v218\n" \
"v_mfma_f32_16x16x32_bf16 a[100:103], v[152:155], v[128:131], a[100:103]\n" \
"ds_read_b64_tr_b16 v[110:111], v38 offset:1024\n" \
"v_and_b32_e64 v217, 2, v217\n" \
"v_mfma_f32_16x16x32_bf16 a[96:99], v[84:87], v[64:67], 0\n" \
"v_bitop3_b32 v215, v215, v218, v217 bitop3:0x1e\n" \
"v_lshlrev_b32_e64 v223, 1, v220\n" \
"v_mfma_f32_16x16x32_bf16 a[96:99], v[148:151], v[128:131], a[96:99]\n" \
"v_mov_b32_e32 v224, 2.0\n" \
"v_cndmask_b32_e32 v193, v222, v214, vcc\n" \
"v_mfma_f32_16x16x32_bf16 a[128:131], v[84:87], v[68:71], 0\n" \
"ds_read_b64_tr_b16 v[172:173], v38 offset:8192\n" \
"v_lshlrev_b32_e32 v214, 7, v220\n" \
"s_waitcnt lgkmcnt(15)\n" \
"v_mfma_f32_16x16x32_bf16 a[128:131], v[148:151], v[132:135], a[128:131]\n" \
"v_lshlrev_b32_e32 v215, 4, v215\n" \
"s_mul_i32 s39, s3, 18\n" \
"v_mfma_f32_16x16x32_bf16 a[132:135], v[88:91], v[68:71], 0\n" \
"v_add3_u32 v214, v214, s34, v215\n" \
"v_cndmask_b32_e64 v195, v224, v223, s[4:5]\n" \
"v_mfma_f32_16x16x32_bf16 a[132:135], v[152:155], v[132:135], a[132:135]\n" \
"ds_read_b64_tr_b16 v[174:175], v38 offset:9216\n" \
"s_movk_i32 s4, 0x1f8\n" \
"v_mfma_f32_16x16x32_bf16 a[136:139], v[92:95], v[68:71], 0\n" \
"v_mov_b32_e32 v215, v213\n" \
"s_add_i32 s35, s35, s39\n" \
"v_mfma_f32_16x16x32_bf16 a[136:139], v[156:159], v[132:135], a[136:139]\n" \
"v_cmp_gt_u32_e32 vcc, s4, v216\n" \
"v_add_u32_e64 v212, s35, v212\n" \
"v_mfma_f32_16x16x32_bf16 a[140:143], v[96:99], v[68:71], 0\n" \
"v_mov_b32_e32 v219, 2.0\n" \
"v_cndmask_b32_e32 v197, v215, v214, vcc\n" \
"v_mfma_f32_16x16x32_bf16 a[140:143], v[160:163], v[132:135], a[140:143]\n" \
"ds_read_b64_tr_b16 v[112:113], v39 offset:0\n" \
"s_movk_i32 s35, 0x120\n" \
"v_mfma_f32_16x16x32_bf16 a[172:175], v[96:99], v[72:75], 0\n" \
"v_mul_u32_u24_e32 v214, 0xd80, v212\n" \
"v_cmp_gt_u32_e32 vcc, s35, v20\n" \
"v_mfma_f32_16x16x32_bf16 a[172:175], v[160:163], v[136:139], a[172:175]\n" \
"v_lshrrev_b32_e32 v215, 1, v212\n" \
"s_and_b32 s4, s2, 7\n" \
"v_mfma_f32_16x16x32_bf16 a[168:171], v[92:95], v[72:75], 0\n" \
"ds_read_b64_tr_b16 v[114:115], v39 offset:1024\n" \
"v_cndmask_b32_e64 v194, v219, v214, vcc\n" \
"v_mfma_f32_16x16x32_bf16 a[168:171], v[156:159], v[136:139], a[168:171]\n" \
"v_lshlrev_b32_e32 v214, 7, v212\n" \
"v_lshrrev_b32_e32 v212, 2, v212\n" \
"v_mfma_f32_16x16x32_bf16 a[164:167], v[88:91], v[72:75], 0\n" \
"v_and_b32_e64 v212, 2, v212\n" \
"v_and_or_b32 v212, v215, 1, v212\n" \
"v_mfma_f32_16x16x32_bf16 a[164:167], v[152:155], v[136:139], a[164:167]\n" \
"ds_read_b64_tr_b16 v[176:177], v39 offset:8192\n" \
"s_lshl_b32 s5, s4, 2\n" \
"v_mfma_f32_16x16x32_bf16 a[160:163], v[84:87], v[72:75], 0\n" \
"s_bfe_u32 s38, s2, 0x20003\n" \
"v_lshlrev_b32_e32 v212, 5, v212\n" \
"v_mfma_f32_16x16x32_bf16 a[160:163], v[148:151], v[136:139], a[160:163]\n" \
"s_movk_i32 s35, 0x60\n" \
"s_or_b32 s5, s5, s38\n" \
"s_waitcnt lgkmcnt(15)\n" \
"v_mfma_f32_16x16x32_bf16 a[192:195], v[84:87], v[76:79], 0\n" \
"ds_read_b64_tr_b16 v[178:179], v39 offset:9216\n" \
"v_bitop3_b32 v212, v212, s35, v214 bitop3:0x36\n" \
"v_mfma_f32_16x16x32_bf16 a[192:195], v[148:151], v[140:143], a[192:195]\n" \
"s_lshr_b32 s36, s2, 3\n" \
"v_add_u32_e64 v212, s34, v212\n" \
"v_mfma_f32_16x16x32_bf16 a[196:199], v[88:91], v[76:79], 0\n" \
"s_mul_i32 s34, s5, 0x12000\n" \
"s_or_b32 s40, s34, 0x6000000\n" \
"v_mfma_f32_16x16x32_bf16 a[196:199], v[152:155], v[140:143], a[196:199]\n" \
"s_mul_i32 s34, s36, 0x90000\n" \
"s_lshl_b32 s36, s3, 14\n" \
"v_mfma_f32_16x16x32_bf16 a[200:203], v[92:95], v[76:79], 0\n" \
"s_add_i32 s34, s34, s36\n" \
"s_lshl_b32 s38, s5, 8\n" \
"v_mfma_f32_16x16x32_bf16 a[200:203], v[156:159], v[140:143], a[200:203]\n" \
"s_add_i32 s41, s34, 0x4240000\n" \
"s_mul_i32 s34, s5, 36\n" \
"v_mfma_f32_16x16x32_bf16 a[204:207], v[96:99], v[76:79], 0\n" \
"s_add_i32 s5, s7, s40\n" \
"s_addk_i32 s5, 0x2000\n" \
"v_mfma_f32_16x16x32_bf16 a[204:207], v[160:163], v[140:143], a[204:207]\n" \
"s_mov_b32 s37, 2.0\n" \
"s_or_b32 s38, s38, 0x5be8000\n" \
"v_mfma_f32_16x16x32_bf16 a[236:239], v[96:99], v[80:83], 0\n" \
"s_cmp_lg_u32 s3, 0\n" \
"s_cselect_b32 s36, s37, s5\n" \
"v_mfma_f32_16x16x32_bf16 a[236:239], v[160:163], v[144:147], a[236:239]\n" \
"s_lshl_b32 s5, s6, 2\n" \
"s_or_b32 s5, s38, s5\n" \
"v_mfma_f32_16x16x32_bf16 a[232:235], v[92:95], v[80:83], 0\n" \
"s_lshl_b32 s35, s3, 10\n" \
"v_mfma_f32_16x16x32_bf16 a[232:235], v[156:159], v[144:147], a[232:235]\n" \
"s_cmpk_lt_u32 s3, 3\n" \
"s_cselect_b32 s37, s37, s5\n" \
"v_mfma_f32_16x16x32_bf16 a[228:231], v[88:91], v[80:83], 0\n" \
"s_lshl_b32 s5, s4, 16\n" \
"s_lshl_b32 s4, s4, 11\n" \
"v_mfma_f32_16x16x32_bf16 a[228:231], v[152:155], v[144:147], a[228:231]\n" \
"v_cndmask_b32_e32 v198, v213, v212, vcc\n" \
"v_or_b32_e32 v212, s35, v20\n" \
"v_mfma_f32_16x16x32_bf16 a[224:227], v[84:87], v[80:83], 0\n" \
"s_or_b32 s39, s40, s35\n" \
"s_or_b32 s55, s41, s4\n" \
"v_mfma_f32_16x16x32_bf16 a[224:227], v[148:151], v[144:147], a[224:227]\n" \
"s_waitcnt vmcnt(4) lgkmcnt(0)\n" \
"s_barrier\n" \
"v_mfma_f32_16x16x32_bf16 a[16:19], v[100:103], v[52:55], 0\n" \
"s_add_u32 m0, s30, 0x0\n" \
"s_nop 0\n" \
"s_mov_b32 s85, 0x100\n" \
"v_mfma_f32_16x16x32_bf16 a[16:19], v[164:167], v[116:119], a[16:19]\n" \
"buffer_load_dwordx4 v22, s[64:67], s85 offen lds\n" \
"v_mfma_f32_16x16x32_bf16 a[20:23], v[104:107], v[52:55], 0\n" \
"ds_read_b64_tr_b16 v[84:85], v32 offset:32768\n" \
"v_or_b32_e32 v199, 0x22000, v212\n" \
"v_mfma_f32_16x16x32_bf16 a[20:23], v[168:171], v[116:119], a[20:23]\n" \
"s_add_u32 m0, s30, 0x400\n" \
"s_mov_b32 s84, 0x2100\n" \
"v_mfma_f32_16x16x32_bf16 a[24:27], v[108:111], v[52:55], 0\n" \
"buffer_load_dwordx4 v23, s[64:67], s84 offen lds\n" \
"v_mfma_f32_16x16x32_bf16 a[24:27], v[172:175], v[116:119], a[24:27]\n" \
"ds_read_b64_tr_b16 v[86:87], v32 offset:33792\n" \
"s_add_i32 s35, s39, s7\n" \
"v_mfma_f32_16x16x32_bf16 a[28:31], v[112:115], v[52:55], 0\n" \
"ds_read_b64_tr_b16 v[148:149], v32 offset:40960\n" \
"s_add_i32 s54, s41, s5\n" \
"v_mfma_f32_16x16x32_bf16 a[28:31], v[176:179], v[116:119], a[28:31]\n" \
"ds_read_b128 v[52:55], v28 offset:49152\n" \
"s_add_i32 s55, s55, 0x80000\n" \
"v_mfma_f32_16x16x32_bf16 a[60:63], v[112:115], v[56:59], 0\n" \
"ds_read_b128 v[116:119], v29 offset:49152\n" \
"v_mov_b32_e32 v215, 0xffff\n" \
"v_mfma_f32_16x16x32_bf16 a[60:63], v[176:179], v[120:123], a[60:63]\n" \
"s_add_u32 m0, s31, 0x0\n" \
"s_nop 0\n" \
"s_mov_b32 s85, 0x100000\n" \
"v_mfma_f32_16x16x32_bf16 a[56:59], v[108:111], v[56:59], 0\n" \
"buffer_load_dwordx4 v26, s[68:71], s85 offen lds\n" \
"v_mfma_f32_16x16x32_bf16 a[56:59], v[172:175], v[120:123], a[56:59]\n" \
"s_add_u32 m0, s31, 0x4000\n" \
"s_mov_b32 s84, 0x100000\n" \
"v_mfma_f32_16x16x32_bf16 a[52:55], v[104:107], v[56:59], 0\n" \
"buffer_load_dwordx4 v26, s[72:75], s84 offen lds\n" \
"v_mfma_f32_16x16x32_bf16 a[52:55], v[168:171], v[120:123], a[52:55]\n" \
"s_add_u32 m0, s30, 0x800\n" \
"s_mov_b32 s85, 0x4100\n" \
"v_mfma_f32_16x16x32_bf16 a[48:51], v[100:103], v[56:59], 0\n" \
"buffer_load_dwordx4 v22, s[64:67], s85 offen lds\n" \
"v_mfma_f32_16x16x32_bf16 a[48:51], v[164:167], v[120:123], a[48:51]\n" \
"ds_read_b128 v[56:59], v28 offset:51200\n" \
"s_and_b32 s4, s3, 1\n" \
"v_mfma_f32_16x16x32_bf16 a[80:83], v[100:103], v[60:63], 0\n" \
"ds_read_b128 v[120:123], v29 offset:51200\n" \
"s_lshl_b32 s5, s4, 4\n" \
"v_mfma_f32_16x16x32_bf16 a[80:83], v[164:167], v[124:127], a[80:83]\n" \
"s_add_u32 m0, s30, 0xc00\n" \
"s_mov_b32 s84, 0x6100\n" \
"v_mfma_f32_16x16x32_bf16 a[84:87], v[104:107], v[60:63], 0\n" \
"buffer_load_dwordx4 v23, s[64:67], s84 offen lds\n" \
"v_mfma_f32_16x16x32_bf16 a[84:87], v[168:171], v[124:127], a[84:87]\n" \
"ds_read_b64_tr_b16 v[150:151], v32 offset:41984\n" \
"v_lshrrev_b32_e32 v212, 4, v20\n" \
".fill (L_pb + 3808 - .) / 4, 4, 0xBF800000\n" \
"L_pp1:\n" \
"s_cbranch_execz L_pret\n" \
"v_mfma_f32_16x16x32_bf16 a[88:91], v[108:111], v[60:63], 0\n" \
"s_add_u32 m0, s31, 0x400\n" \
"s_mov_b32 s85, 0x108000\n" \
"v_mfma_f32_16x16x32_bf16 a[88:91], v[172:175], v[124:127], a[88:91]\n" \
"buffer_load_dwordx4 v26, s[68:71], s85 offen lds\n" \
"v_mfma_f32_16x16x32_bf16 a[92:95], v[112:115], v[60:63], 0\n" \
"ds_read_b64_tr_b16 v[88:89], v33 offset:32768\n" \
"v_lshrrev_b32_e32 v218, 6, v20\n" \
"v_mfma_f32_16x16x32_bf16 a[92:95], v[176:179], v[124:127], a[92:95]\n" \
"ds_read_b128 v[60:63], v28 offset:53248\n" \
"v_and_b32_e64 v219, 12, v218\n" \
"v_mfma_f32_16x16x32_bf16 a[124:127], v[112:115], v[64:67], 0\n" \
"ds_read_b128 v[124:127], v29 offset:53248\n" \
"v_and_or_b32 v214, v212, 15, s5\n" \
"v_mfma_f32_16x16x32_bf16 a[124:127], v[176:179], v[128:131], a[124:127]\n" \
"s_add_u32 m0, s31, 0x4400\n" \
"s_mov_b32 s84, 0x108000\n" \
"v_mfma_f32_16x16x32_bf16 a[120:123], v[108:111], v[64:67], 0\n" \
"buffer_load_dwordx4 v26, s[72:75], s84 offen lds\n" \
"v_mfma_f32_16x16x32_bf16 a[120:123], v[172:175], v[128:131], a[120:123]\n" \
"s_add_u32 m0, s30, 0x1000\n" \
"s_mov_b32 s85, 0x8100\n" \
"v_mfma_f32_16x16x32_bf16 a[116:119], v[104:107], v[64:67], 0\n" \
"buffer_load_dwordx4 v22, s[64:67], s85 offen lds\n" \
"v_mfma_f32_16x16x32_bf16 a[116:119], v[168:171], v[128:131], a[116:119]\n" \
"ds_read_b64_tr_b16 v[90:91], v33 offset:33792\n" \
"v_cmp_gt_u32_e64 vcc, v219, v214\n" \
"v_mfma_f32_16x16x32_bf16 a[112:115], v[100:103], v[64:67], 0\n" \
"ds_read_b64_tr_b16 v[152:153], v33 offset:40960\n" \
"v_mov_b32_e32 v220, 0xffff0000\n" \
"v_mfma_f32_16x16x32_bf16 a[112:115], v[164:167], v[128:131], a[112:115]\n" \
"ds_read_b128 v[64:67], v28 offset:55296\n" \
"s_lshl_b32 s5, s2, 2\n" \
"s_nop 0\n" \
"v_mfma_f32_16x16x32_bf16 a[144:147], v[100:103], v[68:71], 0\n" \
"ds_read_b128 v[128:131], v29 offset:55296\n" \
"v_cndmask_b32_e64 v217, v215, 0, vcc\n" \
"v_mfma_f32_16x16x32_bf16 a[144:147], v[164:167], v[132:135], a[144:147]\n" \
"s_add_u32 m0, s30, 0x1400\n" \
"s_mov_b32 s84, 0xa100\n" \
"v_mfma_f32_16x16x32_bf16 a[148:151], v[104:107], v[68:71], 0\n" \
"buffer_load_dwordx4 v23, s[64:67], s84 offen lds\n" \
"v_mfma_f32_16x16x32_bf16 a[148:151], v[168:171], v[132:135], a[148:151]\n" \
"s_add_u32 m0, s31, 0x800\n" \
"s_mov_b32 s85, 0x110000\n" \
"v_mfma_f32_16x16x32_bf16 a[152:155], v[108:111], v[68:71], 0\n" \
"buffer_load_dwordx4 v27, s[68:71], s85 offen lds\n" \
"v_mfma_f32_16x16x32_bf16 a[152:155], v[172:175], v[132:135], a[152:155]\n" \
"s_add_u32 m0, s31, 0x4800\n" \
"s_mov_b32 s84, 0x110000\n" \
"v_mfma_f32_16x16x32_bf16 a[156:159], v[112:115], v[68:71], 0\n" \
"buffer_load_dwordx4 v27, s[72:75], s84 offen lds\n" \
"v_mfma_f32_16x16x32_bf16 a[156:159], v[176:179], v[132:135], a[156:159]\n" \
"ds_read_b128 v[68:71], v28 offset:32768\n" \
"v_cmp_lt_u32_e64 vcc, v219, v214\n" \
"v_mfma_f32_16x16x32_bf16 a[188:191], v[112:115], v[72:75], 0\n" \
"ds_read_b128 v[132:135], v29 offset:32768\n" \
"v_mov_b32_e64 v213, s23\n" \
"v_mfma_f32_16x16x32_bf16 a[188:191], v[176:179], v[136:139], a[188:191]\n" \
"s_add_u32 m0, s30, 0x1800\n" \
"s_mov_b32 s85, 0xc100\n" \
"v_mfma_f32_16x16x32_bf16 a[184:187], v[108:111], v[72:75], 0\n" \
"buffer_load_dwordx4 v22, s[64:67], s85 offen lds\n" \
"v_mfma_f32_16x16x32_bf16 a[184:187], v[172:175], v[136:139], a[184:187]\n" \
"ds_read_b64_tr_b16 v[154:155], v33 offset:41984\n" \
"s_or_b32 s5, s5, s3\n" \
"v_mfma_f32_16x16x32_bf16 a[180:183], v[104:107], v[72:75], 0\n" \
"s_add_u32 m0, s30, 0x1c00\n" \
"s_mov_b32 s84, 0xe100\n" \
"v_mfma_f32_16x16x32_bf16 a[180:183], v[168:171], v[136:139], a[180:183]\n" \
"buffer_load_dwordx4 v23, s[64:67], s84 offen lds\n" \
"v_mfma_f32_16x16x32_bf16 a[176:179], v[100:103], v[72:75], 0\n" \
"ds_read_b64_tr_b16 v[92:93], v34 offset:32768\n" \
"v_cndmask_b32_e32 v221, 0, v220, vcc\n" \
"v_mfma_f32_16x16x32_bf16 a[176:179], v[164:167], v[136:139], a[176:179]\n" \
"ds_read_b128 v[72:75], v28 offset:34816\n" \
"v_or_b32_e64 v12, v217, v221\n" \
"v_mfma_f32_16x16x32_bf16 a[208:211], v[100:103], v[76:79], 0\n" \
"ds_read_b128 v[136:139], v29 offset:34816\n" \
"v_or_b32_e64 v217, 2, v219\n" \
"v_mfma_f32_16x16x32_bf16 a[208:211], v[164:167], v[140:143], a[208:211]\n" \
"ds_read_b64_tr_b16 v[94:95], v34 offset:33792\n" \
"v_cmp_gt_u32_e64 vcc, v217, v214\n" \
"v_mfma_f32_16x16x32_bf16 a[212:215], v[104:107], v[76:79], 0\n" \
"ds_read_b64_tr_b16 v[156:157], v34 offset:40960\n" \
"v_mov_b32_e64 v216, s25\n" \
"v_mfma_f32_16x16x32_bf16 a[212:215], v[168:171], v[140:143], a[212:215]\n" \
"ds_read_b64_tr_b16 v[158:159], v34 offset:41984\n" \
"s_movk_i32 s40, 0xf400\n" \
"v_mfma_f32_16x16x32_bf16 a[216:219], v[108:111], v[76:79], 0\n" \
"ds_read_b64_tr_b16 v[96:97], v35 offset:32768\n" \
"v_cndmask_b32_e64 v221, v215, 0, vcc\n" \
"v_mfma_f32_16x16x32_bf16 a[216:219], v[172:175], v[140:143], a[216:219]\n" \
"ds_read_b64_tr_b16 v[98:99], v35 offset:33792\n" \
"v_cmp_lt_u32_e32 vcc, v217, v214\n" \
"v_mfma_f32_16x16x32_bf16 a[220:223], v[112:115], v[76:79], 0\n" \
"ds_read_b64_tr_b16 v[160:161], v35 offset:40960\n" \
"s_lshl_b32 s4, s4, 6\n" \
"v_mfma_f32_16x16x32_bf16 a[220:223], v[176:179], v[140:143], a[220:223]\n" \
"ds_read_b128 v[76:79], v28 offset:36864\n" \
"v_cndmask_b32_e32 v217, 0, v220, vcc\n" \
"v_mfma_f32_16x16x32_bf16 a[252:255], v[112:115], v[80:83], 0\n" \
"ds_read_b128 v[140:143], v29 offset:36864\n" \
"v_or_b32_e64 v13, v221, v217\n" \
"v_mfma_f32_16x16x32_bf16 a[252:255], v[176:179], v[144:147], a[252:255]\n" \
"ds_read_b64_tr_b16 v[162:163], v35 offset:41984\n" \
"v_or_b32_e64 v217, 16, v219\n" \
"v_mfma_f32_16x16x32_bf16 a[248:251], v[108:111], v[80:83], 0\n" \
"v_cmp_gt_u32_e32 vcc, v217, v214\n" \
"s_nop 1\n" \
"v_cndmask_b32_e64 v221, v215, 0, vcc\n" \
"v_mfma_f32_16x16x32_bf16 a[248:251], v[172:175], v[144:147], a[248:251]\n" \
"v_cmp_lt_u32_e32 vcc, v217, v214\n" \
"s_nop 1\n" \
"v_cndmask_b32_e64 v217, 0, v220, vcc\n" \
"v_mfma_f32_16x16x32_bf16 a[244:247], v[104:107], v[80:83], 0\n" \
"v_or_b32_e32 v14, v221, v217\n" \
"v_or_b32_e32 v217, 18, v219\n" \
"v_mfma_f32_16x16x32_bf16 a[244:247], v[168:171], v[144:147], a[244:247]\n" \
"v_cmp_gt_u32_e32 vcc, v217, v214\n" \
"s_nop 1\n" \
"v_cndmask_b32_e64 v215, v215, 0, vcc\n" \
"v_mfma_f32_16x16x32_bf16 a[240:243], v[100:103], v[80:83], 0\n" \
"v_cmp_lt_u32_e32 vcc, v217, v214\n" \
"s_nop 1\n" \
"v_cndmask_b32_e64 v214, 0, v220, vcc\n" \
"v_mfma_f32_16x16x32_bf16 a[240:243], v[164:167], v[144:147], a[240:243]\n" \
"ds_read_b128 v[80:83], v28 offset:38912\n" \
"ds_read_b128 v[144:147], v29 offset:38912\n" \
"s_waitcnt lgkmcnt(15)\n" \
"v_mfma_f32_16x16x32_bf16 a[0:3], v[84:87], v[52:55], a[0:3]\n" \
"s_add_u32 m0, s31, 0xc00\n" \
"s_mov_b32 s85, 0x118000\n" \
"v_mfma_f32_16x16x32_bf16 a[0:3], v[148:151], v[116:119], a[0:3]\n" \
"buffer_load_dwordx4 v27, s[68:71], s85 offen lds\n" \
"v_mfma_f32_16x16x32_bf16 a[4:7], v[88:91], v[52:55], a[4:7]\n" \
"ds_read_b64_tr_b16 v[100:101], v36 offset:32768\n" \
"v_or_b32_e32 v15, v215, v214\n" \
"s_waitcnt lgkmcnt(15)\n" \
"v_mfma_f32_16x16x32_bf16 a[4:7], v[152:155], v[116:119], a[4:7]\n" \
"s_add_u32 m0, s31, 0x4c00\n" \
"s_mov_b32 s84, 0x118000\n" \
"s_waitcnt lgkmcnt(11)\n" \
"v_mfma_f32_16x16x32_bf16 a[8:11], v[92:95], v[52:55], a[8:11]\n" \
"buffer_load_dwordx4 v27, s[72:75], s84 offen lds\n" \
"s_waitcnt lgkmcnt(9)\n" \
"v_mfma_f32_16x16x32_bf16 a[8:11], v[156:159], v[116:119], a[8:11]\n" \
"ds_read_b64_tr_b16 v[102:103], v36 offset:33792\n" \
"v_mad_u64_u32 v[214:215], s[6:7], s5, 42, v[212:213]\n" \
"s_waitcnt lgkmcnt(8)\n" \
"v_mfma_f32_16x16x32_bf16 a[12:15], v[96:99], v[52:55], a[12:15]\n" \
"ds_read_b64_tr_b16 v[164:165], v36 offset:40960\n" \
"s_movk_i32 s5, 0x1440\n" \
"s_waitcnt lgkmcnt(5)\n" \
"v_mfma_f32_16x16x32_bf16 a[12:15], v[160:163], v[116:119], a[12:15]\n" \
"v_cmp_gt_u32_e64 vcc, s5, v214\n" \
"v_min_u32_e32 v217, 0xa43f, v214\n" \
"v_mfma_f32_16x16x32_bf16 a[44:47], v[96:99], v[56:59], a[44:47]\n" \
"v_mov_b32_e32 v214, s24\n" \
"v_cndmask_b32_e32 v215, v216, v213, vcc\n" \
"v_mfma_f32_16x16x32_bf16 a[44:47], v[160:163], v[120:123], a[44:47]\n" \
"ds_read_b64_tr_b16 v[166:167], v36 offset:41984\n" \
"v_mov_b32_e64 v213, s22\n" \
"v_mfma_f32_16x16x32_bf16 a[40:43], v[92:95], v[56:59], a[40:43]\n" \
"s_movk_i32 s6, 0xffee\n" \
"v_cndmask_b32_e32 v214, v214, v213, vcc\n" \
"v_mfma_f32_16x16x32_bf16 a[40:43], v[156:159], v[120:123], a[40:43]\n" \
"v_add_u32_e32 v213, 0xffffebc0, v217\n" \
"s_lshl_b32 s5, s2, 5\n" \
"v_mfma_f32_16x16x32_bf16 a[36:39], v[88:91], v[56:59], a[36:39]\n" \
"v_cndmask_b32_e32 v213, v213, v217, vcc\n" \
"v_lshlrev_b32_e64 v216, 7, v213\n" \
"v_mfma_f32_16x16x32_bf16 a[36:39], v[152:155], v[120:123], a[36:39]\n" \
"ds_read_b64_tr_b16 v[104:105], v37 offset:32768\n" \
"v_mul_u32_u24_e32 v213, 0xe39, v212\n" \
"v_mfma_f32_16x16x32_bf16 a[32:35], v[84:87], v[56:59], a[32:35]\n" \
"v_mov_b32_e32 v217, 0\n" \
"v_lshrrev_b32_e32 v213, 16, v213\n" \
"v_mfma_f32_16x16x32_bf16 a[32:35], v[148:151], v[120:123], a[32:35]\n" \
"v_lshl_add_u64 v[16:17], v[214:215], 0, v[216:217]\n" \
"v_mad_i32_i24 v214, v213, s6, v212\n" \
"v_mfma_f32_16x16x32_bf16 a[64:67], v[84:87], v[60:63], a[64:67]\n" \
"s_mov_b32 m0, s32\n" \
"ds_read_b64_tr_b16 v[106:107], v37 offset:33792\n" \
"v_mfma_f32_16x16x32_bf16 a[64:67], v[148:151], v[124:127], a[64:67]\n" \
"s_mov_b32 exec_lo, 0x0\n" \
"s_mov_b32 exec_hi, 0xff\n" \
"global_load_lds_dword v[184:185], off\n" \
"s_mov_b64 exec, -1\n" \
"v_mfma_f32_16x16x32_bf16 a[68:71], v[88:91], v[60:63], a[68:71]\n" \
"s_add_i32 s5, s5, 32\n" \
"v_lshlrev_b32_e64 v215, 4, v214\n" \
"v_mfma_f32_16x16x32_bf16 a[68:71], v[152:155], v[124:127], a[68:71]\n" \
"ds_read_b64_tr_b16 v[168:169], v37 offset:40960\n" \
"v_add_u32_e64 v216, s5, v215\n" \
"v_mfma_f32_16x16x32_bf16 a[72:75], v[92:95], v[60:63], a[72:75]\n" \
"v_and_b32_e32 v216, 0xf0, v216\n" \
"v_cmp_gt_u32_e64 vcc, 16, v214\n" \
"v_mfma_f32_16x16x32_bf16 a[72:75], v[156:159], v[124:127], a[72:75]\n" \
"s_mov_b32 m0, s32\n" \
"s_movk_i32 s7, 0xf800\n" \
"v_mfma_f32_16x16x32_bf16 a[76:79], v[96:99], v[60:63], a[76:79]\n" \
"s_mov_b32 exec_lo, 0xffff0000\n" \
"s_mov_b32 exec_hi, 0x0\n" \
"global_load_lds_dword v[186:187], off\n" \
"s_mov_b64 exec, -1\n" \
"v_mfma_f32_16x16x32_bf16 a[76:79], v[160:163], v[124:127], a[76:79]\n" \
"ds_read_b64_tr_b16 v[170:171], v37 offset:41984\n" \
"v_cndmask_b32_e64 v214, v215, v216, vcc\n" \
"v_mfma_f32_16x16x32_bf16 a[108:111], v[96:99], v[64:67], a[108:111]\n" \
"v_or_b32_e64 v215, 64, v212\n" \
"v_mul_u32_u24_e32 v216, 0xe39, v215\n" \
"v_mfma_f32_16x16x32_bf16 a[108:111], v[160:163], v[128:131], a[108:111]\n" \
"v_lshrrev_b32_e64 v216, 16, v216\n" \
"v_mad_i32_i24 v215, v216, s6, v215\n" \
"v_mfma_f32_16x16x32_bf16 a[104:107], v[92:95], v[64:67], a[104:107]\n" \
"ds_read_b64_tr_b16 v[108:109], v38 offset:32768\n" \
"v_lshlrev_b32_e64 v217, 4, v215\n" \
"v_mfma_f32_16x16x32_bf16 a[104:107], v[156:159], v[128:131], a[104:107]\n" \
"v_add_u32_e64 v220, s5, v217\n" \
"v_and_b32_e32 v220, 0xf0, v220\n" \
"v_mfma_f32_16x16x32_bf16 a[100:103], v[88:91], v[64:67], a[100:103]\n" \
"v_cmp_gt_u32_e32 vcc, 16, v215\n" \
"s_nop 1\n" \
"v_cndmask_b32_e64 v215, v217, v220, vcc\n" \
"v_mfma_f32_16x16x32_bf16 a[100:103], v[152:155], v[128:131], a[100:103]\n" \
"ds_read_b64_tr_b16 v[110:111], v38 offset:33792\n" \
"v_or_b32_e32 v217, 0x80, v212\n" \
"v_mfma_f32_16x16x32_bf16 a[96:99], v[84:87], v[64:67], a[96:99]\n" \
"v_mul_u32_u24_e32 v220, 0xe39, v217\n" \
"v_lshrrev_b32_e64 v220, 16, v220\n" \
"v_mfma_f32_16x16x32_bf16 a[96:99], v[148:151], v[128:131], a[96:99]\n" \
"v_mad_i32_i24 v217, v220, s6, v217\n" \
"v_lshlrev_b32_e64 v221, 4, v217\n" \
"v_mfma_f32_16x16x32_bf16 a[128:131], v[84:87], v[68:71], a[128:131]\n" \
"ds_read_b64_tr_b16 v[172:173], v38 offset:40960\n" \
"v_add_u32_e64 v222, s5, v221\n" \
"v_mfma_f32_16x16x32_bf16 a[128:131], v[148:151], v[132:135], a[128:131]\n" \
"v_and_b32_e32 v222, 0xf0, v222\n" \
"v_cmp_gt_u32_e64 vcc, 16, v217\n" \
"v_mfma_f32_16x16x32_bf16 a[132:135], v[88:91], v[68:71], a[132:135]\n" \
"s_nop 0\n" \
"v_cndmask_b32_e32 v217, v221, v222, vcc\n" \
"v_or_b32_e32 v221, 0xc0, v212\n" \
"v_mfma_f32_16x16x32_bf16 a[132:135], v[152:155], v[132:135], a[132:135]\n" \
"ds_read_b64_tr_b16 v[174:175], v38 offset:41984\n" \
"v_mul_u32_u24_e32 v222, 0xe39, v221\n" \
"v_mfma_f32_16x16x32_bf16 a[136:139], v[92:95], v[68:71], a[136:139]\n" \
"v_lshrrev_b32_e64 v222, 16, v222\n" \
"v_mad_i32_i24 v221, v222, s6, v221\n" \
"v_mfma_f32_16x16x32_bf16 a[136:139], v[156:159], v[132:135], a[136:139]\n" \
"v_lshlrev_b32_e32 v223, 4, v221\n" \
"v_add_u32_e32 v224, s5, v223\n" \
"v_mfma_f32_16x16x32_bf16 a[140:143], v[96:99], v[68:71], a[140:143]\n" \
"v_and_b32_e32 v224, 0xf0, v224\n" \
"v_cmp_gt_u32_e64 vcc, 16, v221\n" \
"v_mfma_f32_16x16x32_bf16 a[140:143], v[160:163], v[132:135], a[140:143]\n" \
"ds_read_b64_tr_b16 v[112:113], v39 offset:32768\n" \
"v_cndmask_b32_e64 v221, v223, v224, vcc\n" \
"v_mfma_f32_16x16x32_bf16 a[172:175], v[96:99], v[72:75], a[172:175]\n" \
"v_or_b32_e32 v223, 0x100, v212\n" \
"v_mul_u32_u24_e32 v224, 0xe39, v223\n" \
"v_mfma_f32_16x16x32_bf16 a[172:175], v[160:163], v[136:139], a[172:175]\n" \
"v_lshrrev_b32_e64 v224, 16, v224\n" \
"v_mad_i32_i24 v223, v224, s6, v223\n" \
"v_mfma_f32_16x16x32_bf16 a[168:171], v[92:95], v[72:75], a[168:171]\n" \
"ds_read_b64_tr_b16 v[114:115], v39 offset:33792\n" \
"v_lshlrev_b32_e64 v225, 4, v223\n" \
"v_mfma_f32_16x16x32_bf16 a[168:171], v[156:159], v[136:139], a[168:171]\n" \
"v_add_u32_e64 v226, s5, v225\n" \
"v_and_b32_e32 v226, 0xf0, v226\n" \
"v_mfma_f32_16x16x32_bf16 a[164:167], v[88:91], v[72:75], a[164:167]\n" \
"v_cmp_gt_u32_e32 vcc, 16, v223\n" \
"s_nop 1\n" \
"v_cndmask_b32_e64 v223, v225, v226, vcc\n" \
"v_mfma_f32_16x16x32_bf16 a[164:167], v[152:155], v[136:139], a[164:167]\n" \
"ds_read_b64_tr_b16 v[176:177], v39 offset:40960\n" \
"v_or_b32_e32 v225, 0x140, v212\n" \
"v_mfma_f32_16x16x32_bf16 a[160:163], v[84:87], v[72:75], a[160:163]\n" \
"v_mul_u32_u24_e32 v226, 0xe39, v225\n" \
"v_lshrrev_b32_e64 v226, 16, v226\n" \
"v_mfma_f32_16x16x32_bf16 a[160:163], v[148:151], v[136:139], a[160:163]\n" \
"v_mad_i32_i24 v225, v226, s6, v225\n" \
"v_lshlrev_b32_e64 v227, 4, v225\n" \
"v_mfma_f32_16x16x32_bf16 a[192:195], v[84:87], v[76:79], a[192:195]\n" \
"ds_read_b64_tr_b16 v[178:179], v39 offset:41984\n" \
"v_add_u32_e64 v228, s5, v227\n" \
"v_mfma_f32_16x16x32_bf16 a[192:195], v[148:151], v[140:143], a[192:195]\n" \
"v_and_b32_e32 v228, 0xf0, v228\n" \
"v_cmp_gt_u32_e64 vcc, 16, v225\n" \
"v_mfma_f32_16x16x32_bf16 a[196:199], v[88:91], v[76:79], a[196:199]\n" \
"s_nop 0\n" \
"v_cndmask_b32_e32 v225, v227, v228, vcc\n" \
"v_or_b32_e32 v227, 0x180, v212\n" \
"v_mfma_f32_16x16x32_bf16 a[196:199], v[152:155], v[140:143], a[196:199]\n" \
"v_mul_u32_u24_e32 v228, 0xe39, v227\n" \
"v_lshrrev_b32_e64 v228, 16, v228\n" \
"v_mfma_f32_16x16x32_bf16 a[200:203], v[92:95], v[76:79], a[200:203]\n" \
"v_mad_i32_i24 v227, v228, s6, v227\n" \
"v_lshlrev_b32_e64 v229, 4, v227\n" \
"v_mfma_f32_16x16x32_bf16 a[200:203], v[156:159], v[140:143], a[200:203]\n" \
"v_add_u32_e64 v230, s5, v229\n" \
"v_and_b32_e32 v230, 0xf0, v230\n" \
"v_mfma_f32_16x16x32_bf16 a[204:207], v[96:99], v[76:79], a[204:207]\n" \
"v_cmp_gt_u32_e32 vcc, 16, v227\n" \
"s_nop 1\n" \
"v_cndmask_b32_e64 v227, v229, v230, vcc\n" \
"v_mfma_f32_16x16x32_bf16 a[204:207], v[160:163], v[140:143], a[204:207]\n" \
"v_or_b32_e32 v229, 0x1c0, v212\n" \
"v_mul_u32_u24_e32 v230, 0xe39, v229\n" \
"s_waitcnt lgkmcnt(15)\n" \
"v_mfma_f32_16x16x32_bf16 a[236:239], v[96:99], v[80:83], a[236:239]\n" \
"v_lshrrev_b32_e32 v230, 16, v230\n" \
"v_mad_i32_i24 v229, v230, s6, v229\n" \
"v_mfma_f32_16x16x32_bf16 a[236:239], v[160:163], v[144:147], a[236:239]\n" \
"v_lshlrev_b32_e32 v231, 4, v229\n" \
"v_add_u32_e32 v232, s5, v231\n" \
"v_mfma_f32_16x16x32_bf16 a[232:235], v[92:95], v[80:83], a[232:235]\n" \
"v_and_b32_e32 v232, 0xf0, v232\n" \
"v_cmp_gt_u32_e64 vcc, 16, v229\n" \
"v_mfma_f32_16x16x32_bf16 a[232:235], v[156:159], v[144:147], a[232:235]\n" \
"s_nop 0\n" \
"v_cndmask_b32_e32 v229, v231, v232, vcc\n" \
"v_or_b32_e32 v231, 0x200, v212\n" \
"v_mfma_f32_16x16x32_bf16 a[228:231], v[88:91], v[80:83], a[228:231]\n" \
"v_mul_u32_u24_e32 v232, 0xe39, v231\n" \
"v_lshrrev_b32_e64 v232, 16, v232\n" \
"v_mfma_f32_16x16x32_bf16 a[228:231], v[152:155], v[144:147], a[228:231]\n" \
"v_mad_i32_i24 v231, v232, s6, v231\n" \
"v_lshlrev_b32_e64 v233, 4, v231\n" \
"v_mfma_f32_16x16x32_bf16 a[224:227], v[84:87], v[80:83], a[224:227]\n" \
"v_add_u32_e32 v234, s5, v233\n" \
"s_movk_i32 s5, 0x480\n" \
"v_mfma_f32_16x16x32_bf16 a[224:227], v[148:151], v[144:147], a[224:227]\n" \
"s_waitcnt vmcnt(20)\n" \
"v_readfirstlane_b32 s62, v180\n" \
"s_nop 3\n" \
"s_add_u32 s62, s62, 0x9E3779B1\n" \
"s_waitcnt vmcnt(2) lgkmcnt(0)\n" \
"s_barrier\n" \
"s_nop 0\n" \
"v_mfma_f32_16x16x32_bf16 a[16:19], v[100:103], v[52:55], a[16:19]\n" \
"s_add_u32 m0, s30, 0x8000\n" \
"s_mov_b32 s85, 0x180\n" \
"v_mfma_f32_16x16x32_bf16 a[16:19], v[164:167], v[116:119], a[16:19]\n" \
"buffer_load_dwordx4 v22, s[64:67], s85 offen lds\n" \
"v_mfma_f32_16x16x32_bf16 a[20:23], v[104:107], v[52:55], a[20:23]\n" \
"ds_read_b64_tr_b16 v[84:85], v32 offset:0\n" \
"v_mad_u32_u24 v0, v213, s5, v214\n" \
"v_mfma_f32_16x16x32_bf16 a[20:23], v[168:171], v[116:119], a[20:23]\n" \
"s_add_u32 m0, s30, 0x8400\n" \
"s_mov_b32 s84, 0x2180\n" \
"v_mfma_f32_16x16x32_bf16 a[24:27], v[108:111], v[52:55], a[24:27]\n" \
"buffer_load_dwordx4 v23, s[64:67], s84 offen lds\n" \
"v_mfma_f32_16x16x32_bf16 a[24:27], v[172:175], v[116:119], a[24:27]\n" \
"ds_read_b64_tr_b16 v[86:87], v32 offset:1024\n" \
"v_mul_u32_u24_e32 v213, 0x480, v216\n" \
"v_mfma_f32_16x16x32_bf16 a[28:31], v[112:115], v[52:55], a[28:31]\n" \
"ds_read_b64_tr_b16 v[148:149], v32 offset:8192\n" \
"s_movk_i32 s6, 0xfc00\n" \
"s_nop 0\n" \
"v_mfma_f32_16x16x32_bf16 a[28:31], v[176:179], v[116:119], a[28:31]\n" \
"ds_read_b128 v[52:55], v28 offset:16384\n" \
"v_add3_u32 v1, v213, v215, s6\n" \
"v_mfma_f32_16x16x32_bf16 a[60:63], v[112:115], v[56:59], a[60:63]\n" \
"ds_read_b128 v[116:119], v29 offset:16384\n" \
"v_mul_u32_u24_e32 v213, 0x480, v220\n" \
"v_mfma_f32_16x16x32_bf16 a[60:63], v[176:179], v[120:123], a[60:63]\n" \
"s_add_u32 m0, s31, 0x8000\n" \
"s_mov_b32 s85, 0x180000\n" \
"v_mfma_f32_16x16x32_bf16 a[56:59], v[108:111], v[56:59], a[56:59]\n" \
"buffer_load_dwordx4 v26, s[68:71], s85 offen lds\n" \
"v_mfma_f32_16x16x32_bf16 a[56:59], v[172:175], v[120:123], a[56:59]\n" \
"s_add_u32 m0, s31, 0xc000\n" \
"s_mov_b32 s84, 0x180000\n" \
"v_mfma_f32_16x16x32_bf16 a[52:55], v[104:107], v[56:59], a[52:55]\n" \
"buffer_load_dwordx4 v26, s[72:75], s84 offen lds\n" \
"v_mfma_f32_16x16x32_bf16 a[52:55], v[168:171], v[120:123], a[52:55]\n" \
"s_add_u32 m0, s30, 0x8800\n" \
"s_mov_b32 s85, 0x4180\n" \
"v_mfma_f32_16x16x32_bf16 a[48:51], v[100:103], v[56:59], a[48:51]\n" \
"buffer_load_dwordx4 v22, s[64:67], s85 offen lds\n" \
"v_mfma_f32_16x16x32_bf16 a[48:51], v[164:167], v[120:123], a[48:51]\n" \
"ds_read_b128 v[56:59], v28 offset:18432\n" \
"v_add3_u32 v2, v213, v217, s7\n" \
"v_mfma_f32_16x16x32_bf16 a[80:83], v[100:103], v[60:63], a[80:83]\n" \
"ds_read_b128 v[120:123], v29 offset:18432\n" \
"v_mul_u32_u24_e32 v213, 0x480, v222\n" \
"v_mfma_f32_16x16x32_bf16 a[80:83], v[164:167], v[124:127], a[80:83]\n" \
"s_add_u32 m0, s30, 0x8c00\n" \
"s_mov_b32 s84, 0x6180\n" \
"v_mfma_f32_16x16x32_bf16 a[84:87], v[104:107], v[60:63], a[84:87]\n" \
"buffer_load_dwordx4 v23, s[64:67], s84 offen lds\n" \
"v_mfma_f32_16x16x32_bf16 a[84:87], v[168:171], v[124:127], a[84:87]\n" \
"ds_read_b64_tr_b16 v[150:151], v32 offset:9216\n" \
"v_add3_u32 v3, v213, v221, s40\n" \
"v_mfma_f32_16x16x32_bf16 a[88:91], v[108:111], v[60:63], a[88:91]\n" \
"s_add_u32 m0, s31, 0x8400\n" \
"s_mov_b32 s85, 0x188000\n" \
"v_mfma_f32_16x16x32_bf16 a[88:91], v[172:175], v[124:127], a[88:91]\n" \
"buffer_load_dwordx4 v26, s[68:71], s85 offen lds\n" \
"v_mfma_f32_16x16x32_bf16 a[92:95], v[112:115], v[60:63], a[92:95]\n" \
"ds_read_b64_tr_b16 v[88:89], v33 offset:0\n" \
"v_mul_u32_u24_e32 v213, 0x480, v226\n" \
"v_mfma_f32_16x16x32_bf16 a[92:95], v[176:179], v[124:127], a[92:95]\n" \
"ds_read_b128 v[60:63], v28 offset:20480\n" \
"v_add3_u32 v5, v213, v225, s6\n" \
"v_mfma_f32_16x16x32_bf16 a[124:127], v[112:115], v[64:67], a[124:127]\n" \
"ds_read_b128 v[124:127], v29 offset:20480\n" \
"v_mul_u32_u24_e32 v213, 0x480, v228\n" \
"v_mfma_f32_16x16x32_bf16 a[124:127], v[176:179], v[128:131], a[124:127]\n" \
"s_add_u32 m0, s31, 0xc400\n" \
"s_mov_b32 s84, 0x188000\n" \
"v_mfma_f32_16x16x32_bf16 a[120:123], v[108:111], v[64:67], a[120:123]\n" \
"buffer_load_dwordx4 v26, s[72:75], s84 offen lds\n" \
"v_mfma_f32_16x16x32_bf16 a[120:123], v[172:175], v[128:131], a[120:123]\n" \
"s_add_u32 m0, s30, 0x9000\n" \
"s_mov_b32 s85, 0x8180\n" \
"v_mfma_f32_16x16x32_bf16 a[116:119], v[104:107], v[64:67], a[116:119]\n" \
"buffer_load_dwordx4 v22, s[64:67], s85 offen lds\n" \
"v_mfma_f32_16x16x32_bf16 a[116:119], v[168:171], v[128:131], a[116:119]\n" \
"ds_read_b64_tr_b16 v[90:91], v33 offset:1024\n" \
"v_add3_u32 v6, v213, v227, s7\n" \
"v_mfma_f32_16x16x32_bf16 a[112:115], v[100:103], v[64:67], a[112:115]\n" \
"ds_read_b64_tr_b16 v[152:153], v33 offset:8192\n" \
"v_mul_u32_u24_e32 v213, 0x480, v230\n" \
"v_mfma_f32_16x16x32_bf16 a[112:115], v[164:167], v[128:131], a[112:115]\n" \
"ds_read_b128 v[64:67], v28 offset:22528\n" \
"v_add3_u32 v7, v213, v229, s40\n" \
"v_mfma_f32_16x16x32_bf16 a[144:147], v[100:103], v[68:71], a[144:147]\n" \
"ds_read_b128 v[128:131], v29 offset:22528\n" \
"s_lshl_b32 s7, s3, 3\n" \
"s_nop 0\n" \
"v_mfma_f32_16x16x32_bf16 a[144:147], v[164:167], v[132:135], a[144:147]\n" \
"s_add_u32 m0, s30, 0x9400\n" \
"s_mov_b32 s84, 0xa180\n" \
"v_mfma_f32_16x16x32_bf16 a[148:151], v[104:107], v[68:71], a[148:151]\n" \
"buffer_load_dwordx4 v23, s[64:67], s84 offen lds\n" \
"v_mfma_f32_16x16x32_bf16 a[148:151], v[168:171], v[132:135], a[148:151]\n" \
"s_add_u32 m0, s31, 0x8800\n" \
"s_mov_b32 s85, 0x190000\n" \
"v_mfma_f32_16x16x32_bf16 a[152:155], v[108:111], v[68:71], a[152:155]\n" \
"buffer_load_dwordx4 v27, s[68:71], s85 offen lds\n" \
"v_mfma_f32_16x16x32_bf16 a[152:155], v[172:175], v[132:135], a[152:155]\n" \
"s_add_u32 m0, s31, 0xc800\n" \
"s_mov_b32 s84, 0x190000\n" \
"v_mfma_f32_16x16x32_bf16 a[156:159], v[112:115], v[68:71], a[156:159]\n" \
"buffer_load_dwordx4 v27, s[72:75], s84 offen lds\n" \
"v_mfma_f32_16x16x32_bf16 a[156:159], v[176:179], v[132:135], a[156:159]\n" \
"ds_read_b128 v[68:71], v28 offset:0\n" \
"s_lshl_b32 s40, s3, 1\n" \
"v_mfma_f32_16x16x32_bf16 a[188:191], v[112:115], v[72:75], a[188:191]\n" \
"ds_read_b128 v[132:135], v29 offset:0\n" \
"s_and_b32 s7, s7, 16\n" \
"v_mfma_f32_16x16x32_bf16 a[188:191], v[176:179], v[136:139], a[188:191]\n" \
"s_add_u32 m0, s30, 0x9800\n" \
"s_mov_b32 s85, 0xc180\n" \
"v_mfma_f32_16x16x32_bf16 a[184:187], v[108:111], v[72:75], a[184:187]\n" \
"buffer_load_dwordx4 v22, s[64:67], s85 offen lds\n" \
"v_mfma_f32_16x16x32_bf16 a[184:187], v[172:175], v[136:139], a[184:187]\n" \
"ds_read_b64_tr_b16 v[154:155], v33 offset:9216\n" \
"s_and_b32 s40, s40, 2\n" \
"v_mfma_f32_16x16x32_bf16 a[180:183], v[104:107], v[72:75], a[180:183]\n" \
"s_add_u32 m0, s30, 0x9c00\n" \
"s_mov_b32 s84, 0xe180\n" \
"v_mfma_f32_16x16x32_bf16 a[180:183], v[168:171], v[136:139], a[180:183]\n" \
"buffer_load_dwordx4 v23, s[64:67], s84 offen lds\n" \
"v_mfma_f32_16x16x32_bf16 a[176:179], v[100:103], v[72:75], a[176:179]\n" \
"ds_read_b64_tr_b16 v[92:93], v34 offset:0\n" \
"s_and_b32 s6, s2, 7\n" \
"v_mfma_f32_16x16x32_bf16 a[176:179], v[164:167], v[136:139], a[176:179]\n" \
"ds_read_b128 v[72:75], v28 offset:2048\n" \
"v_bfe_u32 v213, v20, 7, 1\n" \
"v_mfma_f32_16x16x32_bf16 a[208:211], v[100:103], v[76:79], a[208:211]\n" \
"ds_read_b128 v[136:139], v29 offset:2048\n" \
"s_or_b32 s7, s40, s7\n" \
"v_mfma_f32_16x16x32_bf16 a[208:211], v[164:167], v[140:143], a[208:211]\n" \
"ds_read_b64_tr_b16 v[94:95], v34 offset:1024\n" \
"v_and_b32_e32 v234, 0xf0, v234\n" \
"v_mfma_f32_16x16x32_bf16 a[212:215], v[104:107], v[76:79], a[212:215]\n" \
"ds_read_b64_tr_b16 v[156:157], v34 offset:8192\n" \
"v_cmp_gt_u32_e32 vcc, 16, v231\n" \
"v_mfma_f32_16x16x32_bf16 a[212:215], v[168:171], v[140:143], a[212:215]\n" \
"ds_read_b64_tr_b16 v[158:159], v34 offset:9216\n" \
"v_or3_b32 v213, s7, v213, v219\n" \
"v_mfma_f32_16x16x32_bf16 a[216:219], v[108:111], v[76:79], a[216:219]\n" \
"ds_read_b64_tr_b16 v[96:97], v35 offset:0\n" \
"v_and_b32_e64 v214, 32, v20\n" \
"v_mfma_f32_16x16x32_bf16 a[216:219], v[172:175], v[140:143], a[216:219]\n" \
"ds_read_b64_tr_b16 v[98:99], v35 offset:1024\n" \
"s_lshl_b32 s6, s6, 5\n" \
"v_mfma_f32_16x16x32_bf16 a[220:223], v[112:115], v[76:79], a[220:223]\n" \
"ds_read_b64_tr_b16 v[160:161], v35 offset:8192\n" \
"v_cndmask_b32_e32 v231, v233, v234, vcc\n" \
"v_mfma_f32_16x16x32_bf16 a[220:223], v[176:179], v[140:143], a[220:223]\n" \
"ds_read_b128 v[76:79], v28 offset:4096\n" \
"v_mul_u32_u24_e64 v213, 18, v213\n" \
"v_mfma_f32_16x16x32_bf16 a[252:255], v[112:115], v[80:83], a[252:255]\n" \
"ds_read_b128 v[140:143], v29 offset:4096\n" \
"v_mov_b32_e32 v215, 0x100\n" \
"v_mfma_f32_16x16x32_bf16 a[252:255], v[176:179], v[144:147], a[252:255]\n" \
"ds_read_b64_tr_b16 v[162:163], v35 offset:9216\n" \
"v_mov_b32_e64 v216, s6\n" \
"v_mfma_f32_16x16x32_bf16 a[248:251], v[108:111], v[80:83], a[248:251]\n" \
"v_cmp_eq_u32_e64 vcc, 0, v214\n" \
"v_and_or_b32 v213, v218, 1, v213\n" \
"v_mfma_f32_16x16x32_bf16 a[248:251], v[172:175], v[144:147], a[248:251]\n" \
"v_lshlrev_b32_e32 v212, 3, v212\n" \
"v_cndmask_b32_e32 v214, v215, v216, vcc\n" \
"v_mfma_f32_16x16x32_bf16 a[244:247], v[104:107], v[80:83], a[244:247]\n" \
"v_mad_u32_u24 v213, v213, s5, v214\n" \
"v_and_or_b32 v213, v20, 16, v213\n" \
"v_mfma_f32_16x16x32_bf16 a[244:247], v[168:171], v[144:147], a[244:247]\n" \
"v_add_u32_e32 v9, 0x4800, v213\n" \
"v_mul_u32_u24_e32 v213, 0x120, v218\n" \
"v_mfma_f32_16x16x32_bf16 a[240:243], v[100:103], v[80:83], a[240:243]\n" \
"v_and_or_b32 v10, v212, 24, v213\n" \
"v_lshrrev_b32_e64 v213, 2, v20\n" \
"v_mfma_f32_16x16x32_bf16 a[240:243], v[164:167], v[144:147], a[240:243]\n" \
"ds_read_b128 v[80:83], v28 offset:6144\n" \
"ds_read_b128 v[144:147], v29 offset:6144\n" \
"s_waitcnt lgkmcnt(15)\n" \
"v_mfma_f32_16x16x32_bf16 a[0:3], v[84:87], v[52:55], a[0:3]\n" \
"s_add_u32 m0, s31, 0x8c00\n" \
"s_mov_b32 s85, 0x198000\n" \
"v_mfma_f32_16x16x32_bf16 a[0:3], v[148:151], v[116:119], a[0:3]\n" \
"buffer_load_dwordx4 v27, s[68:71], s85 offen lds\n" \
"v_mfma_f32_16x16x32_bf16 a[4:7], v[88:91], v[52:55], a[4:7]\n" \
"ds_read_b64_tr_b16 v[100:101], v36 offset:0\n" \
"v_and_or_b32 v213, v213, 60, s4\n" \
"s_waitcnt lgkmcnt(15)\n" \
"v_mfma_f32_16x16x32_bf16 a[4:7], v[152:155], v[116:119], a[4:7]\n" \
"s_add_u32 m0, s31, 0xcc00\n" \
"s_mov_b32 s84, 0x198000\n" \
"s_waitcnt lgkmcnt(11)\n" \
"v_mfma_f32_16x16x32_bf16 a[8:11], v[92:95], v[52:55], a[8:11]\n" \
"buffer_load_dwordx4 v27, s[72:75], s84 offen lds\n" \
"s_waitcnt lgkmcnt(9)\n" \
"v_mfma_f32_16x16x32_bf16 a[8:11], v[156:159], v[116:119], a[8:11]\n" \
"ds_read_b64_tr_b16 v[102:103], v36 offset:1024\n" \
"v_lshl_or_b32 v212, s3, 9, v212\n" \
"s_waitcnt lgkmcnt(8)\n" \
"v_mfma_f32_16x16x32_bf16 a[12:15], v[96:99], v[52:55], a[12:15]\n" \
"ds_read_b64_tr_b16 v[164:165], v36 offset:8192\n" \
"v_mad_u32_u24 v4, v224, s5, v223\n" \
"s_waitcnt lgkmcnt(5)\n" \
"v_mfma_f32_16x16x32_bf16 a[12:15], v[160:163], v[116:119], a[12:15]\n" \
"v_mad_u32_u24 v8, v232, s5, v231\n" \
"v_or_b32_e32 v18, 0x27000, v213\n" \
"v_mfma_f32_16x16x32_bf16 a[44:47], v[96:99], v[56:59], a[44:47]\n" \
"v_or_b32_e32 v19, 0x27800, v212\n" \
"v_mov_b32_e32 v219, 0x1000c0c\n" \
"v_mfma_f32_16x16x32_bf16 a[44:47], v[160:163], v[120:123], a[44:47]\n" \
"ds_read_b64_tr_b16 v[166:167], v36 offset:9216\n" \
"v_mov_b32_e32 v220, 0xc0c0100\n" \
"v_mfma_f32_16x16x32_bf16 a[40:43], v[92:95], v[56:59], a[40:43]\n" \
".fill (L_pb + 7904 - .) / 4, 4, 0xBF800000\n" \
"L_pp2:\n" \
"s_cbranch_execz L_pret\n" \
"buffer_load_dwordx4 v[200:203], v192, s[76:79], s34 offen offset:0\n" \
"v_mfma_f32_16x16x32_bf16 a[40:43], v[156:159], v[120:123], a[40:43]\n" \
"v_bfe_u32 v218, v20, 4, 1\n" \
"v_lshrrev_b32_e32 v213, 8, v20\n" \
"v_mfma_f32_16x16x32_bf16 a[36:39], v[88:91], v[56:59], a[36:39]\n" \
"v_cmp_eq_u32_e32 vcc, 0, v218\n" \
"s_lshr_b32 s4, s2, 5\n" \
"v_mfma_f32_16x16x32_bf16 a[36:39], v[152:155], v[120:123], a[36:39]\n" \
"ds_read_b64_tr_b16 v[104:105], v37 offset:0\n" \
"s_and_b32 s5, s3, 1\n" \
"v_mfma_f32_16x16x32_bf16 a[32:35], v[84:87], v[56:59], a[32:35]\n" \
"v_bfe_u32 v217, v20, 4, 4\n" \
"v_cndmask_b32_e32 v219, v219, v220, vcc\n" \
"v_mfma_f32_16x16x32_bf16 a[32:35], v[148:151], v[120:123], a[32:35]\n" \
"v_mov_b32_e32 v220, 0x3020c0c\n" \
"v_mov_b32_e32 v221, 0xc0c0302\n" \
"v_mfma_f32_16x16x32_bf16 a[64:67], v[84:87], v[60:63], a[64:67]\n" \
"buffer_load_dwordx4 v[204:207], v193, s[76:79], s34 offen offset:0\n" \
"v_mfma_f32_16x16x32_bf16 a[64:67], v[148:151], v[124:127], a[64:67]\n" \
"s_mov_b32 m0, s32\n" \
"ds_read_b64_tr_b16 v[106:107], v37 offset:1024\n" \
"v_mfma_f32_16x16x32_bf16 a[68:71], v[88:91], v[60:63], a[68:71]\n" \
"s_mov_b32 exec_lo, 0x0\n" \
"s_mov_b32 exec_hi, 0xff00\n" \
"global_load_lds_dword v[184:185], off\n" \
"s_mov_b64 exec, -1\n" \
"v_mfma_f32_16x16x32_bf16 a[68:71], v[152:155], v[124:127], a[68:71]\n" \
"ds_read_b64_tr_b16 v[168:169], v37 offset:8192\n" \
"v_lshl_or_b32 v218, v213, 2, v218\n" \
"v_mfma_f32_16x16x32_bf16 a[72:75], v[92:95], v[60:63], a[72:75]\n" \
"s_xor_b32 s6, s4, 15\n" \
"v_mfma_f32_16x16x32_bf16 a[72:75], v[156:159], v[124:127], a[72:75]\n" \
"buffer_load_dword v208, v194, s[76:79], s34 offen offset:32\n" \
"v_mfma_f32_16x16x32_bf16 a[76:79], v[96:99], v[60:63], a[76:79]\n" \
"ds_read_b64_tr_b16 v[170:171], v37 offset:9216\n" \
"v_mfma_f32_16x16x32_bf16 a[76:79], v[160:163], v[124:127], a[76:79]\n" \
"s_cmpk_lt_u32 s3, 2\n" \
"s_cselect_b32 s7, s6, s4\n" \
"v_mfma_f32_16x16x32_bf16 a[108:111], v[96:99], v[64:67], a[108:111]\n" \
"s_lshl_b32 s40, s7, 7\n" \
"s_lshl_b32 s41, s5, 6\n" \
"v_mfma_f32_16x16x32_bf16 a[108:111], v[160:163], v[128:131], a[108:111]\n" \
"v_cndmask_b32_e64 v220, v220, v221, vcc\n" \
"v_mov_b32_e32 v221, 0xc0c0c0c\n" \
"v_mfma_f32_16x16x32_bf16 a[104:107], v[92:95], v[64:67], a[104:107]\n" \
"buffer_load_dword v209, v194, s[76:79], s34 offen offset:1184\n" \
"v_mfma_f32_16x16x32_bf16 a[104:107], v[156:159], v[128:131], a[104:107]\n" \
"ds_read_b64_tr_b16 v[108:109], v38 offset:0\n" \
"v_cmp_gt_u32_e64 vcc, v218, v217\n" \
"v_mfma_f32_16x16x32_bf16 a[100:103], v[88:91], v[64:67], a[100:103]\n" \
"v_or_b32_e32 v218, 2, v218\n" \
"v_lshrrev_b32_e32 v212, 7, v20\n" \
"v_mfma_f32_16x16x32_bf16 a[100:103], v[152:155], v[128:131], a[100:103]\n" \
"ds_read_b64_tr_b16 v[110:111], v38 offset:1024\n" \
"s_or_b32 s41, s41, s40\n" \
"v_mfma_f32_16x16x32_bf16 a[96:99], v[84:87], v[64:67], a[96:99]\n" \
"v_cndmask_b32_e32 v40, v221, v219, vcc\n" \
"v_cndmask_b32_e64 v42, v219, v221, vcc\n" \
"v_mfma_f32_16x16x32_bf16 a[96:99], v[148:151], v[128:131], a[96:99]\n" \
"v_cmp_gt_u32_e32 vcc, v218, v217\n" \
"v_and_b32_e32 v218, 16, v20\n" \
"v_mfma_f32_16x16x32_bf16 a[128:131], v[84:87], v[68:71], a[128:131]\n" \
"buffer_load_dword v211, v194, s[76:79], s34 offen offset:2336\n" \
"v_mfma_f32_16x16x32_bf16 a[128:131], v[148:151], v[132:135], a[128:131]\n" \
"ds_read_b64_tr_b16 v[172:173], v38 offset:8192\n" \
"s_movk_i32 s42, 0x3f80\n" \
"v_mfma_f32_16x16x32_bf16 a[132:135], v[88:91], v[68:71], a[132:135]\n" \
"v_or_b32_e32 v214, s41, v212\n" \
"v_and_b32_e32 v217, 0x320, v20\n" \
"v_mfma_f32_16x16x32_bf16 a[132:135], v[152:155], v[132:135], a[132:135]\n" \
"ds_read_b64_tr_b16 v[174:175], v38 offset:9216\n" \
"s_movk_i32 s41, 0x100\n" \
"v_mfma_f32_16x16x32_bf16 a[136:139], v[92:95], v[68:71], a[136:139]\n" \
"v_lshlrev_b32_e64 v218, v218, s42\n" \
"v_mov_b32_e32 v219, 0\n" \
"v_mfma_f32_16x16x32_bf16 a[136:139], v[156:159], v[132:135], a[136:139]\n" \
"s_mov_b32 s76, s20\n" \
"s_and_b32 s77, s21, 0xffff\n" \
"v_mfma_f32_16x16x32_bf16 a[140:143], v[96:99], v[68:71], a[140:143]\n" \
"s_mov_b32 s78, 0x480\n" \
"v_lshrrev_b32_e32 v215, 6, v20\n" \
"v_mfma_f32_16x16x32_bf16 a[140:143], v[160:163], v[132:135], a[140:143]\n" \
"buffer_load_ushort v210, v195, s[76:79], 0 offen\n" \
"v_mfma_f32_16x16x32_bf16 a[172:175], v[96:99], v[72:75], a[172:175]\n" \
"ds_read_b64_tr_b16 v[112:113], v39 offset:0\n" \
"v_cndmask_b32_e64 v41, v221, v220, vcc\n" \
"v_mfma_f32_16x16x32_bf16 a[172:175], v[160:163], v[136:139], a[172:175]\n" \
"v_cndmask_b32_e32 v43, v220, v221, vcc\n" \
"v_cmp_eq_u32_e32 vcc, s41, v217\n" \
"v_mfma_f32_16x16x32_bf16 a[168:171], v[92:95], v[72:75], a[168:171]\n" \
"s_mov_b32 s76, s8\n" \
"s_and_b32 s77, s9, 0xffff\n" \
"v_mfma_f32_16x16x32_bf16 a[168:171], v[156:159], v[136:139], a[168:171]\n" \
"s_mov_b32 s78, 0x240000\n" \
"ds_read_b64_tr_b16 v[114:115], v39 offset:1024\n" \
"v_mfma_f32_16x16x32_bf16 a[164:167], v[88:91], v[72:75], a[164:167]\n" \
"v_and_b32_e32 v217, 0x330, v20\n" \
"v_bfe_u32 v216, v20, 6, 2\n" \
"v_mfma_f32_16x16x32_bf16 a[164:167], v[152:155], v[136:139], a[164:167]\n" \
"ds_read_b64_tr_b16 v[176:177], v39 offset:8192\n" \
"v_cndmask_b32_e32 v46, v219, v218, vcc\n" \
"v_mfma_f32_16x16x32_bf16 a[160:163], v[84:87], v[72:75], a[160:163]\n" \
"v_mov_b32_e32 v218, 0x3f80\n" \
"v_cmp_eq_u32_e64 vcc, 32, v217\n" \
"v_mfma_f32_16x16x32_bf16 a[160:163], v[148:151], v[136:139], a[160:163]\n" \
"s_add_i32 s41, s40, -16\n" \
"v_lshrrev_b32_e32 v219, 1, v20\n" \
"v_mfma_f32_16x16x32_bf16 a[192:195], v[84:87], v[76:79], a[192:195]\n" \
"ds_read_b64_tr_b16 v[178:179], v39 offset:9216\n" \
"v_bfe_u32 v215, v215, 1, 1\n" \
"v_mfma_f32_16x16x32_bf16 a[192:195], v[148:151], v[140:143], a[192:195]\n" \
"v_cndmask_b32_e32 v47, 0, v218, vcc\n" \
"s_mov_b32 s42, 0\n" \
"v_mfma_f32_16x16x32_bf16 a[196:199], v[88:91], v[76:79], a[196:199]\n" \
"s_cmp_lg_u32 s40, 0\n" \
"s_cselect_b32 s41, s41, s42\n" \
"v_mfma_f32_16x16x32_bf16 a[196:199], v[152:155], v[140:143], a[196:199]\n" \
"v_lshl_or_b32 v217, s5, 3, v212\n" \
"v_lshlrev_b32_e64 v218, 10, v213\n" \
"v_mfma_f32_16x16x32_bf16 a[200:203], v[92:95], v[76:79], a[200:203]\n" \
"v_lshlrev_b32_e32 v216, 7, v216\n" \
"v_and_b32_e32 v219, 24, v219\n" \
"v_mfma_f32_16x16x32_bf16 a[200:203], v[156:159], v[140:143], a[200:203]\n" \
"v_and_or_b32 v212, v212, 2, v215\n" \
"v_add_u32_e64 v217, s41, v217\n" \
"v_mfma_f32_16x16x32_bf16 a[204:207], v[96:99], v[76:79], a[204:207]\n" \
"v_or3_b32 v216, v216, v218, v219\n" \
"v_lshlrev_b32_e64 v212, 5, v212\n" \
"v_mfma_f32_16x16x32_bf16 a[204:207], v[160:163], v[140:143], a[204:207]\n" \
"s_mov_b32 s41, 0x14000\n" \
"v_or3_b32 v48, v216, v212, s41\n" \
"s_waitcnt lgkmcnt(15)\n" \
"v_mfma_f32_16x16x32_bf16 a[236:239], v[96:99], v[80:83], a[236:239]\n" \
"s_lshl_b32 s41, s5, 2\n" \
"s_lshl_b32 s7, s7, 3\n" \
"v_mfma_f32_16x16x32_bf16 a[236:239], v[160:163], v[144:147], a[236:239]\n" \
"v_or_b32_e32 v212, s41, v213\n" \
"s_or_b32 s7, s7, s41\n" \
"v_mfma_f32_16x16x32_bf16 a[232:235], v[92:95], v[80:83], a[232:235]\n" \
"s_mov_b32 s41, 0xff800000\n" \
"v_mfma_f32_16x16x32_bf16 a[232:235], v[156:159], v[144:147], a[232:235]\n" \
"s_cmp_lg_u32 s40, 0\n" \
"s_cselect_b32 s47, s42, s41\n" \
"v_mfma_f32_16x16x32_bf16 a[228:231], v[88:91], v[80:83], a[228:231]\n" \
"s_mov_b32 s40, 0xc000\n" \
"s_mov_b32 s41, 0x8000\n" \
"v_mfma_f32_16x16x32_bf16 a[228:231], v[152:155], v[144:147], a[228:231]\n" \
"s_cmp_lg_u32 s4, 0\n" \
"s_cselect_b32 s51, s40, s41\n" \
"v_mfma_f32_16x16x32_bf16 a[224:227], v[84:87], v[80:83], a[224:227]\n" \
"s_movk_i32 s40, 0x4000\n" \
"v_mfma_f32_16x16x32_bf16 a[224:227], v[148:151], v[144:147], a[224:227]\n" \
"s_waitcnt vmcnt(7) lgkmcnt(0)\n" \
"s_barrier\n" \
"v_mfma_f32_16x16x32_bf16 a[16:19], v[100:103], v[52:55], a[16:19]\n" \
"s_add_u32 m0, s30, 0x0\n" \
"s_nop 0\n" \
"s_mov_b32 s85, 0x200\n" \
"v_mfma_f32_16x16x32_bf16 a[16:19], v[164:167], v[116:119], a[16:19]\n" \
"buffer_load_dwordx4 v22, s[64:67], s85 offen lds\n" \
"v_mfma_f32_16x16x32_bf16 a[20:23], v[104:107], v[52:55], a[20:23]\n" \
"ds_read_b64_tr_b16 v[84:85], v32 offset:32768\n" \
"v_mfma_f32_16x16x32_bf16 a[20:23], v[168:171], v[116:119], a[20:23]\n" \
"s_add_u32 m0, s30, 0x400\n" \
"s_mov_b32 s84, 0x2200\n" \
"v_mfma_f32_16x16x32_bf16 a[24:27], v[108:111], v[52:55], a[24:27]\n" \
"buffer_load_dwordx4 v23, s[64:67], s84 offen lds\n" \
"v_mfma_f32_16x16x32_bf16 a[24:27], v[172:175], v[116:119], a[24:27]\n" \
"ds_read_b64_tr_b16 v[86:87], v32 offset:33792\n" \
"v_mfma_f32_16x16x32_bf16 a[28:31], v[112:115], v[52:55], a[28:31]\n" \
"ds_read_b64_tr_b16 v[148:149], v32 offset:40960\n" \
"v_mfma_f32_16x16x32_bf16 a[28:31], v[176:179], v[116:119], a[28:31]\n" \
"ds_read_b128 v[52:55], v28 offset:49152\n" \
"v_mfma_f32_16x16x32_bf16 a[60:63], v[112:115], v[56:59], a[60:63]\n" \
"ds_read_b128 v[116:119], v29 offset:49152\n" \
"v_mfma_f32_16x16x32_bf16 a[60:63], v[176:179], v[120:123], a[60:63]\n" \
"s_add_u32 m0, s31, 0x0\n" \
"s_nop 0\n" \
"s_mov_b32 s85, 0x200000\n" \
"v_mfma_f32_16x16x32_bf16 a[56:59], v[108:111], v[56:59], a[56:59]\n" \
"buffer_load_dwordx4 v26, s[68:71], s85 offen lds\n" \
"v_mfma_f32_16x16x32_bf16 a[56:59], v[172:175], v[120:123], a[56:59]\n" \
"s_add_u32 m0, s31, 0x4000\n" \
"s_mov_b32 s84, 0x200000\n" \
"v_mfma_f32_16x16x32_bf16 a[52:55], v[104:107], v[56:59], a[52:55]\n" \
"buffer_load_dwordx4 v26, s[72:75], s84 offen lds\n" \
"v_mfma_f32_16x16x32_bf16 a[52:55], v[168:171], v[120:123], a[52:55]\n" \
"s_add_u32 m0, s30, 0x800\n" \
"s_mov_b32 s85, 0x4200\n" \
"v_mfma_f32_16x16x32_bf16 a[48:51], v[100:103], v[56:59], a[48:51]\n" \
"buffer_load_dwordx4 v22, s[64:67], s85 offen lds\n" \
"v_mfma_f32_16x16x32_bf16 a[48:51], v[164:167], v[120:123], a[48:51]\n" \
"ds_read_b128 v[56:59], v28 offset:51200\n" \
"v_mfma_f32_16x16x32_bf16 a[80:83], v[100:103], v[60:63], a[80:83]\n" \
"ds_read_b128 v[120:123], v29 offset:51200\n" \
"v_mfma_f32_16x16x32_bf16 a[80:83], v[164:167], v[124:127], a[80:83]\n" \
"s_add_u32 m0, s30, 0xc00\n" \
"s_mov_b32 s84, 0x6200\n" \
"v_mfma_f32_16x16x32_bf16 a[84:87], v[104:107], v[60:63], a[84:87]\n" \
"buffer_load_dwordx4 v23, s[64:67], s84 offen lds\n" \
"v_mfma_f32_16x16x32_bf16 a[84:87], v[168:171], v[124:127], a[84:87]\n" \
"ds_read_b64_tr_b16 v[150:151], v32 offset:41984\n" \
"v_mfma_f32_16x16x32_bf16 a[88:91], v[108:111], v[60:63], a[88:91]\n" \
"s_add_u32 m0, s31, 0x400\n" \
"s_mov_b32 s85, 0x208000\n" \
"v_mfma_f32_16x16x32_bf16 a[88:91], v[172:175], v[124:127], a[88:91]\n" \
"buffer_load_dwordx4 v26, s[68:71], s85 offen lds\n" \
"v_mfma_f32_16x16x32_bf16 a[92:95], v[112:115], v[60:63], a[92:95]\n" \
"ds_read_b64_tr_b16 v[88:89], v33 offset:32768\n" \
"v_mfma_f32_16x16x32_bf16 a[92:95], v[176:179], v[124:127], a[92:95]\n" \
"ds_read_b128 v[60:63], v28 offset:53248\n" \
"v_mfma_f32_16x16x32_bf16 a[124:127], v[112:115], v[64:67], a[124:127]\n" \
"ds_read_b128 v[124:127], v29 offset:53248\n" \
"v_mfma_f32_16x16x32_bf16 a[124:127], v[176:179], v[128:131], a[124:127]\n" \
"s_add_u32 m0, s31, 0x4400\n" \
"s_mov_b32 s84, 0x208000\n" \
"v_mfma_f32_16x16x32_bf16 a[120:123], v[108:111], v[64:67], a[120:123]\n" \
"buffer_load_dwordx4 v26, s[72:75], s84 offen lds\n" \
"v_mfma_f32_16x16x32_bf16 a[120:123], v[172:175], v[128:131], a[120:123]\n" \
"s_add_u32 m0, s30, 0x1000\n" \
"s_mov_b32 s85, 0x8200\n" \
"v_mfma_f32_16x16x32_bf16 a[116:119], v[104:107], v[64:67], a[116:119]\n" \
"buffer_load_dwordx4 v22, s[64:67], s85 offen lds\n" \
"v_mfma_f32_16x16x32_bf16 a[116:119], v[168:171], v[128:131], a[116:119]\n" \
"ds_read_b64_tr_b16 v[90:91], v33 offset:33792\n" \
"v_mfma_f32_16x16x32_bf16 a[112:115], v[100:103], v[64:67], a[112:115]\n" \
"ds_read_b64_tr_b16 v[152:153], v33 offset:40960\n" \
"v_mfma_f32_16x16x32_bf16 a[112:115], v[164:167], v[128:131], a[112:115]\n" \
"ds_read_b128 v[64:67], v28 offset:55296\n" \
"v_mfma_f32_16x16x32_bf16 a[144:147], v[100:103], v[68:71], a[144:147]\n" \
"ds_read_b128 v[128:131], v29 offset:55296\n" \
"v_mfma_f32_16x16x32_bf16 a[144:147], v[164:167], v[132:135], a[144:147]\n" \
"s_add_u32 m0, s30, 0x1400\n" \
"s_mov_b32 s84, 0xa200\n" \
"v_mfma_f32_16x16x32_bf16 a[148:151], v[104:107], v[68:71], a[148:151]\n" \
"buffer_load_dwordx4 v23, s[64:67], s84 offen lds\n" \
"v_mfma_f32_16x16x32_bf16 a[148:151], v[168:171], v[132:135], a[148:151]\n" \
"s_add_u32 m0, s31, 0x800\n" \
"s_mov_b32 s85, 0x210000\n" \
"v_mfma_f32_16x16x32_bf16 a[152:155], v[108:111], v[68:71], a[152:155]\n" \
"buffer_load_dwordx4 v27, s[68:71], s85 offen lds\n" \
"v_mfma_f32_16x16x32_bf16 a[152:155], v[172:175], v[132:135], a[152:155]\n" \
"s_add_u32 m0, s31, 0x4800\n" \
"s_mov_b32 s84, 0x210000\n" \
"v_mfma_f32_16x16x32_bf16 a[156:159], v[112:115], v[68:71], a[156:159]\n" \
"buffer_load_dwordx4 v27, s[72:75], s84 offen lds\n" \
"v_mfma_f32_16x16x32_bf16 a[156:159], v[176:179], v[132:135], a[156:159]\n" \
"ds_read_b128 v[68:71], v28 offset:32768\n" \
"v_mfma_f32_16x16x32_bf16 a[188:191], v[112:115], v[72:75], a[188:191]\n" \
"ds_read_b128 v[132:135], v29 offset:32768\n" \
"v_mfma_f32_16x16x32_bf16 a[188:191], v[176:179], v[136:139], a[188:191]\n" \
"s_add_u32 m0, s30, 0x1800\n" \
"s_mov_b32 s85, 0xc200\n" \
"v_mfma_f32_16x16x32_bf16 a[184:187], v[108:111], v[72:75], a[184:187]\n" \
"buffer_load_dwordx4 v22, s[64:67], s85 offen lds\n" \
"v_mfma_f32_16x16x32_bf16 a[184:187], v[172:175], v[136:139], a[184:187]\n" \
"ds_read_b64_tr_b16 v[154:155], v33 offset:41984\n" \
"v_mfma_f32_16x16x32_bf16 a[180:183], v[104:107], v[72:75], a[180:183]\n" \
"s_add_u32 m0, s30, 0x1c00\n" \
"s_mov_b32 s84, 0xe200\n" \
"v_mfma_f32_16x16x32_bf16 a[180:183], v[168:171], v[136:139], a[180:183]\n" \
"buffer_load_dwordx4 v23, s[64:67], s84 offen lds\n" \
"v_mfma_f32_16x16x32_bf16 a[176:179], v[100:103], v[72:75], a[176:179]\n" \
"ds_read_b64_tr_b16 v[92:93], v34 offset:32768\n" \
"v_mfma_f32_16x16x32_bf16 a[176:179], v[164:167], v[136:139], a[176:179]\n" \
"ds_read_b128 v[72:75], v28 offset:34816\n" \
"v_mfma_f32_16x16x32_bf16 a[208:211], v[100:103], v[76:79], a[208:211]\n" \
"ds_read_b128 v[136:139], v29 offset:34816\n" \
"v_mfma_f32_16x16x32_bf16 a[208:211], v[164:167], v[140:143], a[208:211]\n" \
"ds_read_b64_tr_b16 v[94:95], v34 offset:33792\n" \
"v_mfma_f32_16x16x32_bf16 a[212:215], v[104:107], v[76:79], a[212:215]\n" \
"ds_read_b64_tr_b16 v[156:157], v34 offset:40960\n" \
"v_mfma_f32_16x16x32_bf16 a[212:215], v[168:171], v[140:143], a[212:215]\n" \
"ds_read_b64_tr_b16 v[158:159], v34 offset:41984\n" \
"v_mfma_f32_16x16x32_bf16 a[216:219], v[108:111], v[76:79], a[216:219]\n" \
"ds_read_b64_tr_b16 v[96:97], v35 offset:32768\n" \
"v_mfma_f32_16x16x32_bf16 a[216:219], v[172:175], v[140:143], a[216:219]\n" \
"ds_read_b64_tr_b16 v[98:99], v35 offset:33792\n" \
"v_mfma_f32_16x16x32_bf16 a[220:223], v[112:115], v[76:79], a[220:223]\n" \
"ds_read_b64_tr_b16 v[160:161], v35 offset:40960\n" \
"v_mfma_f32_16x16x32_bf16 a[220:223], v[176:179], v[140:143], a[220:223]\n" \
"ds_read_b128 v[76:79], v28 offset:36864\n" \
"v_mfma_f32_16x16x32_bf16 a[252:255], v[112:115], v[80:83], a[252:255]\n" \
"ds_read_b128 v[140:143], v29 offset:36864\n" \
"v_mfma_f32_16x16x32_bf16 a[252:255], v[176:179], v[144:147], a[252:255]\n" \
"ds_read_b64_tr_b16 v[162:163], v35 offset:41984\n" \
"v_mfma_f32_16x16x32_bf16 a[248:251], v[108:111], v[80:83], a[248:251]\n" \
"s_cmp_lg_u32 s4, 0\n" \
"s_cselect_b32 s53, s41, s40\n" \
"v_mfma_f32_16x16x32_bf16 a[248:251], v[172:175], v[144:147], a[248:251]\n" \
"s_lshl_b32 s40, s2, 2\n" \
"s_and_b32 s40, s40, 28\n" \
"v_mfma_f32_16x16x32_bf16 a[244:247], v[104:107], v[80:83], a[244:247]\n" \
"s_bfe_u32 s45, s2, 0x20003\n" \
"s_lshr_b32 s44, s3, 1\n" \
"v_mfma_f32_16x16x32_bf16 a[244:247], v[168:171], v[144:147], a[244:247]\n" \
"s_or_b32 s46, s40, s45\n" \
"s_lshl_b32 s43, s44, 15\n" \
"v_mfma_f32_16x16x32_bf16 a[240:243], v[100:103], v[80:83], a[240:243]\n" \
"s_lshl_b32 s41, s5, 14\n" \
"s_lshl_b32 s48, s46, 20\n" \
"v_mfma_f32_16x16x32_bf16 a[240:243], v[164:167], v[144:147], a[240:243]\n" \
"ds_read_b128 v[80:83], v28 offset:38912\n" \
"ds_read_b128 v[144:147], v29 offset:38912\n" \
"s_waitcnt lgkmcnt(15)\n" \
"v_mfma_f32_16x16x32_bf16 a[0:3], v[84:87], v[52:55], a[0:3]\n" \
"s_add_u32 m0, s31, 0xc00\n" \
"s_mov_b32 s85, 0x218000\n" \
"v_mfma_f32_16x16x32_bf16 a[0:3], v[148:151], v[116:119], a[0:3]\n" \
"buffer_load_dwordx4 v27, s[68:71], s85 offen lds\n" \
"v_mfma_f32_16x16x32_bf16 a[4:7], v[88:91], v[52:55], a[4:7]\n" \
"ds_read_b64_tr_b16 v[100:101], v36 offset:32768\n" \
"s_or_b32 s41, s43, s41\n" \
"s_waitcnt lgkmcnt(15)\n" \
"v_mfma_f32_16x16x32_bf16 a[4:7], v[152:155], v[116:119], a[4:7]\n" \
"s_add_u32 m0, s31, 0x4c00\n" \
"s_mov_b32 s84, 0x218000\n" \
"s_waitcnt lgkmcnt(11)\n" \
"v_mfma_f32_16x16x32_bf16 a[8:11], v[92:95], v[52:55], a[8:11]\n" \
"buffer_load_dwordx4 v27, s[72:75], s84 offen lds\n" \
"s_waitcnt lgkmcnt(9)\n" \
"v_mfma_f32_16x16x32_bf16 a[8:11], v[156:159], v[116:119], a[8:11]\n" \
"ds_read_b64_tr_b16 v[102:103], v36 offset:33792\n" \
"s_add_i32 s49, s48, 0x1000000\n" \
"s_waitcnt lgkmcnt(8)\n" \
"v_mfma_f32_16x16x32_bf16 a[12:15], v[96:99], v[52:55], a[12:15]\n" \
"ds_read_b64_tr_b16 v[164:165], v36 offset:40960\n" \
"s_or_b32 s42, s49, s41\n" \
"s_waitcnt lgkmcnt(5)\n" \
"v_mfma_f32_16x16x32_bf16 a[12:15], v[160:163], v[116:119], a[12:15]\n" \
"s_lshl_b32 s4, s4, 16\n" \
"s_or_b32 s41, s42, s4\n" \
"v_mfma_f32_16x16x32_bf16 a[44:47], v[96:99], v[56:59], a[44:47]\n" \
"s_lshl_b32 s4, s6, 16\n" \
"s_or_b32 s42, s42, s4\n" \
"v_mfma_f32_16x16x32_bf16 a[44:47], v[160:163], v[120:123], a[44:47]\n" \
"ds_read_b64_tr_b16 v[166:167], v36 offset:41984\n" \
"s_lshl_b32 s4, s5, 11\n" \
"v_mfma_f32_16x16x32_bf16 a[40:43], v[92:95], v[56:59], a[40:43]\n" \
"s_mul_i32 s5, s7, 0x24800\n" \
"s_lshl_b32 s6, s46, 9\n" \
"v_mfma_f32_16x16x32_bf16 a[40:43], v[156:159], v[120:123], a[40:43]\n" \
"s_lshl_b32 s7, s40, 6\n" \
"s_add_i32 s6, s5, s6\n" \
"v_mfma_f32_16x16x32_bf16 a[36:39], v[88:91], v[56:59], a[36:39]\n" \
"s_or_b32 s5, s5, s7\n" \
"s_lshl_b32 s7, s45, 2\n" \
"v_mfma_f32_16x16x32_bf16 a[36:39], v[152:155], v[120:123], a[36:39]\n" \
"ds_read_b64_tr_b16 v[104:105], v37 offset:32768\n" \
"s_or_b32 s5, s5, s7\n" \
"v_mfma_f32_16x16x32_bf16 a[32:35], v[84:87], v[56:59], a[32:35]\n" \
"s_lshl_b32 s7, s3, 12\n" \
"s_or_b32 s50, s49, s7\n" \
"v_mfma_f32_16x16x32_bf16 a[32:35], v[148:151], v[120:123], a[32:35]\n" \
"s_lshl_b32 s7, s3, 11\n" \
"v_lshrrev_b32_e32 v215, 4, v20\n" \
"v_mfma_f32_16x16x32_bf16 a[64:67], v[84:87], v[60:63], a[64:67]\n" \
"s_waitcnt vmcnt(16)\n" \
"v_or_b32_e32 v204, v210, v204\n" \
"v_or_b32_e64 v206, v210, v206\n" \
"v_mfma_f32_16x16x32_bf16 a[64:67], v[148:151], v[124:127], a[64:67]\n" \
"v_mov_b32_e64 v210, 0\n" \
"s_add_u32 s88, s35, 0x1000\n" \
"v_mfma_f32_16x16x32_bf16 a[68:71], v[88:91], v[60:63], a[68:71]\n" \
"ds_read_b64_tr_b16 v[106:107], v37 offset:33792\n" \
"v_bfe_u32 v216, v20, 4, 3\n" \
"v_mfma_f32_16x16x32_bf16 a[68:71], v[152:155], v[124:127], a[68:71]\n" \
"ds_read_b64_tr_b16 v[168:169], v37 offset:40960\n" \
"s_add_i32 s52, s48, s7\n" \
"v_mfma_f32_16x16x32_bf16 a[72:75], v[92:95], v[60:63], a[72:75]\n" \
"s_mov_b32 m0, s32\n" \
"s_add_i32 s53, s53, s7\n" \
"v_mfma_f32_16x16x32_bf16 a[72:75], v[156:159], v[124:127], a[72:75]\n" \
"s_mov_b32 exec_lo, 0xffffffff\n" \
"s_mov_b32 exec_hi, 0x0\n" \
"global_load_lds_dword v[188:189], off\n" \
"s_mov_b64 exec, -1\n" \
"v_mfma_f32_16x16x32_bf16 a[76:79], v[96:99], v[60:63], a[76:79]\n" \
"ds_read_b64_tr_b16 v[170:171], v37 offset:41984\n" \
"v_mul_u32_u24_e32 v214, 0x480, v214\n" \
"v_mfma_f32_16x16x32_bf16 a[76:79], v[160:163], v[124:127], a[76:79]\n" \
"v_bitop3_b32 v218, v213, v215, 7 bitop3:0x78\n" \
"v_bitop3_b32 v213, v213, v216, 4 bitop3:0x36\n" \
"v_mfma_f32_16x16x32_bf16 a[108:111], v[96:99], v[64:67], a[108:111]\n" \
"ds_write_b128 v196, v[200:203]\n" \
"v_mfma_f32_16x16x32_bf16 a[108:111], v[160:163], v[128:131], a[108:111]\n" \
"s_lshl_b32 s7, s3, 10\n" \
"s_or_b32 s4, s43, s4\n" \
"v_mfma_f32_16x16x32_bf16 a[104:107], v[92:95], v[64:67], a[104:107]\n" \
"ds_read_b64_tr_b16 v[108:109], v38 offset:32768\n" \
"v_lshl_or_b32 v25, v213, 4, v214\n" \
"v_mfma_f32_16x16x32_bf16 a[104:107], v[156:159], v[128:131], a[104:107]\n" \
"v_mul_u32_u24_e32 v213, 0x480, v217\n" \
"v_bitop3_b32 v212, v215, v212, 7 bitop3:0x6c\n" \
"v_mfma_f32_16x16x32_bf16 a[100:103], v[88:91], v[64:67], a[100:103]\n" \
"s_or_b32 s40, s7, 0x14000\n" \
"s_lshl_b32 s43, s44, 13\n" \
"v_mfma_f32_16x16x32_bf16 a[100:103], v[152:155], v[128:131], a[100:103]\n" \
"ds_read_b64_tr_b16 v[110:111], v38 offset:33792\n" \
"s_mulk_i32 s44, 0xe800\n" \
"v_mfma_f32_16x16x32_bf16 a[96:99], v[84:87], v[64:67], a[96:99]\n" \
"s_lshl_b32 s7, s3, 8\n" \
"s_nop 0\n" \
"v_lshl_or_b32 v24, v218, 4, v214\n" \
"v_mfma_f32_16x16x32_bf16 a[96:99], v[148:151], v[128:131], a[96:99]\n" \
"v_lshl_or_b32 v21, v212, 4, v213\n" \
"s_add_i32 s44, s44, 0x17000\n" \
"v_mfma_f32_16x16x32_bf16 a[128:131], v[84:87], v[68:71], a[128:131]\n" \
"ds_write_b128 v197, v[204:207]\n" \
"v_mfma_f32_16x16x32_bf16 a[128:131], v[148:151], v[132:135], a[128:131]\n" \
"ds_read_b64_tr_b16 v[172:173], v38 offset:40960\n" \
"s_or_b32 s45, s7, 0x27000\n" \
"v_mfma_f32_16x16x32_bf16 a[132:135], v[88:91], v[68:71], a[132:135]\n" \
"s_or_b32 s46, s4, 0x10000\n" \
"s_add_i32 s48, s6, 0x3000000\n" \
"v_mfma_f32_16x16x32_bf16 a[132:135], v[152:155], v[132:135], a[132:135]\n" \
"ds_read_b64_tr_b16 v[174:175], v38 offset:41984\n" \
"s_add_i32 s49, s5, 0x3004000\n" \
"v_mfma_f32_16x16x32_bf16 a[136:139], v[92:95], v[68:71], a[136:139]\n" \
"s_add_i32 s52, s52, 0x1010000\n" \
"v_mfma_f32_16x16x32_bf16 a[136:139], v[156:159], v[132:135], a[136:139]\n" \
"v_mfma_f32_16x16x32_bf16 a[140:143], v[96:99], v[68:71], a[140:143]\n" \
"v_mfma_f32_16x16x32_bf16 a[140:143], v[160:163], v[132:135], a[140:143]\n" \
"ds_read_b64_tr_b16 v[112:113], v39 offset:32768\n" \
"v_mfma_f32_16x16x32_bf16 a[172:175], v[96:99], v[72:75], a[172:175]\n" \
"ds_write_b128 v198, v[208:211]\n" \
"v_mfma_f32_16x16x32_bf16 a[172:175], v[160:163], v[136:139], a[172:175]\n" \
"v_mfma_f32_16x16x32_bf16 a[168:171], v[92:95], v[72:75], a[168:171]\n" \
"ds_read_b64_tr_b16 v[114:115], v39 offset:33792\n" \
"v_mfma_f32_16x16x32_bf16 a[168:171], v[156:159], v[136:139], a[168:171]\n" \
"v_mfma_f32_16x16x32_bf16 a[164:167], v[88:91], v[72:75], a[164:167]\n" \
"v_mfma_f32_16x16x32_bf16 a[164:167], v[152:155], v[136:139], a[164:167]\n" \
"ds_read_b64_tr_b16 v[176:177], v39 offset:40960\n" \
"v_mfma_f32_16x16x32_bf16 a[160:163], v[84:87], v[72:75], a[160:163]\n" \
"v_mfma_f32_16x16x32_bf16 a[160:163], v[148:151], v[136:139], a[160:163]\n" \
"v_mfma_f32_16x16x32_bf16 a[192:195], v[84:87], v[76:79], a[192:195]\n" \
"ds_read_b64_tr_b16 v[178:179], v39 offset:41984\n" \
"v_mfma_f32_16x16x32_bf16 a[192:195], v[148:151], v[140:143], a[192:195]\n" \
"v_mfma_f32_16x16x32_bf16 a[196:199], v[88:91], v[76:79], a[196:199]\n" \
"v_mfma_f32_16x16x32_bf16 a[196:199], v[152:155], v[140:143], a[196:199]\n" \
"v_mfma_f32_16x16x32_bf16 a[200:203], v[92:95], v[76:79], a[200:203]\n" \
"v_mfma_f32_16x16x32_bf16 a[200:203], v[156:159], v[140:143], a[200:203]\n" \
"v_mfma_f32_16x16x32_bf16 a[204:207], v[96:99], v[76:79], a[204:207]\n" \
"v_mfma_f32_16x16x32_bf16 a[204:207], v[160:163], v[140:143], a[204:207]\n" \
"s_waitcnt lgkmcnt(15)\n" \
"v_mfma_f32_16x16x32_bf16 a[236:239], v[96:99], v[80:83], a[236:239]\n" \
"v_mfma_f32_16x16x32_bf16 a[236:239], v[160:163], v[144:147], a[236:239]\n" \
"v_mfma_f32_16x16x32_bf16 a[232:235], v[92:95], v[80:83], a[232:235]\n" \
"v_mfma_f32_16x16x32_bf16 a[232:235], v[156:159], v[144:147], a[232:235]\n" \
"v_mfma_f32_16x16x32_bf16 a[228:231], v[88:91], v[80:83], a[228:231]\n" \
"v_mfma_f32_16x16x32_bf16 a[228:231], v[152:155], v[144:147], a[228:231]\n" \
"v_mfma_f32_16x16x32_bf16 a[224:227], v[84:87], v[80:83], a[224:227]\n" \
"v_mfma_f32_16x16x32_bf16 a[224:227], v[148:151], v[144:147], a[224:227]\n" \
"s_waitcnt vmcnt(1) lgkmcnt(0)\n" \
"s_barrier\n" \
"s_nop 0\n" \
"v_mfma_f32_16x16x32_bf16 a[16:19], v[100:103], v[52:55], a[16:19]\n" \
"s_add_u32 m0, s30, 0x8000\n" \
"s_mov_b32 s85, 0x280\n" \
"v_mfma_f32_16x16x32_bf16 a[16:19], v[164:167], v[116:119], a[16:19]\n" \
"buffer_load_dwordx4 v22, s[64:67], s85 offen lds\n" \
"v_mfma_f32_16x16x32_bf16 a[20:23], v[104:107], v[52:55], a[20:23]\n" \
"s_add_u32 m0, s30, 0x8400\n" \
"s_mov_b32 s84, 0x2280\n" \
"v_mfma_f32_16x16x32_bf16 a[20:23], v[168:171], v[116:119], a[20:23]\n" \
"buffer_load_dwordx4 v23, s[64:67], s84 offen lds\n" \
"v_mfma_f32_16x16x32_bf16 a[24:27], v[108:111], v[52:55], a[24:27]\n" \
"ds_read_b64_tr_b16 v[84:85], v32 offset:0\n" \
"v_mfma_f32_16x16x32_bf16 a[24:27], v[172:175], v[116:119], a[24:27]\n" \
"s_add_u32 m0, s31, 0x8000\n" \
"s_mov_b32 s85, 0x280000\n" \
"v_mfma_f32_16x16x32_bf16 a[28:31], v[112:115], v[52:55], a[28:31]\n" \
"buffer_load_dwordx4 v26, s[68:71], s85 offen lds\n" \
"v_mfma_f32_16x16x32_bf16 a[28:31], v[176:179], v[116:119], a[28:31]\n" \
"ds_read_b128 v[52:55], v28 offset:16384\n" \
"v_mfma_f32_16x16x32_bf16 a[60:63], v[112:115], v[56:59], a[60:63]\n" \
"ds_read_b128 v[116:119], v29 offset:16384\n" \
"v_mfma_f32_16x16x32_bf16 a[60:63], v[176:179], v[120:123], a[60:63]\n" \
"s_add_u32 m0, s31, 0xc000\n" \
"s_mov_b32 s84, 0x280000\n" \
"v_mfma_f32_16x16x32_bf16 a[56:59], v[108:111], v[56:59], a[56:59]\n" \
"buffer_load_dwordx4 v26, s[72:75], s84 offen lds\n" \
"v_mfma_f32_16x16x32_bf16 a[56:59], v[172:175], v[120:123], a[56:59]\n" \
"s_add_u32 m0, s30, 0x8800\n" \
"s_mov_b32 s85, 0x4280\n" \
"v_mfma_f32_16x16x32_bf16 a[52:55], v[104:107], v[56:59], a[52:55]\n" \
"buffer_load_dwordx4 v22, s[64:67], s85 offen lds\n" \
"v_mfma_f32_16x16x32_bf16 a[52:55], v[168:171], v[120:123], a[52:55]\n" \
"ds_read_b64_tr_b16 v[86:87], v32 offset:1024\n" \
"v_mfma_f32_16x16x32_bf16 a[48:51], v[100:103], v[56:59], a[48:51]\n" \
"ds_read_b64_tr_b16 v[148:149], v32 offset:8192\n" \
"v_mfma_f32_16x16x32_bf16 a[48:51], v[164:167], v[120:123], a[48:51]\n" \
"ds_read_b128 v[56:59], v28 offset:18432\n" \
"v_mfma_f32_16x16x32_bf16 a[80:83], v[100:103], v[60:63], a[80:83]\n" \
"ds_read_b128 v[120:123], v29 offset:18432\n" \
"v_mfma_f32_16x16x32_bf16 a[80:83], v[164:167], v[124:127], a[80:83]\n" \
"s_add_u32 m0, s30, 0x8c00\n" \
"s_mov_b32 s84, 0x6280\n" \
"v_mfma_f32_16x16x32_bf16 a[84:87], v[104:107], v[60:63], a[84:87]\n" \
"buffer_load_dwordx4 v23, s[64:67], s84 offen lds\n" \
"v_mfma_f32_16x16x32_bf16 a[84:87], v[168:171], v[124:127], a[84:87]\n" \
"s_add_u32 m0, s31, 0x8400\n" \
"s_mov_b32 s85, 0x288000\n" \
"v_mfma_f32_16x16x32_bf16 a[88:91], v[108:111], v[60:63], a[88:91]\n" \
"buffer_load_dwordx4 v26, s[68:71], s85 offen lds\n" \
"v_mfma_f32_16x16x32_bf16 a[88:91], v[172:175], v[124:127], a[88:91]\n" \
"s_add_u32 m0, s31, 0xc400\n" \
"s_mov_b32 s84, 0x288000\n" \
"v_mfma_f32_16x16x32_bf16 a[92:95], v[112:115], v[60:63], a[92:95]\n" \
"buffer_load_dwordx4 v26, s[72:75], s84 offen lds\n" \
"v_mfma_f32_16x16x32_bf16 a[92:95], v[176:179], v[124:127], a[92:95]\n" \
"ds_read_b128 v[60:63], v28 offset:20480\n" \
"v_mfma_f32_16x16x32_bf16 a[124:127], v[112:115], v[64:67], a[124:127]\n" \
"ds_read_b128 v[124:127], v29 offset:20480\n" \
"v_mfma_f32_16x16x32_bf16 a[124:127], v[176:179], v[128:131], a[124:127]\n" \
"s_add_u32 m0, s30, 0x9000\n" \
"s_mov_b32 s85, 0x8280\n" \
"v_mfma_f32_16x16x32_bf16 a[120:123], v[108:111], v[64:67], a[120:123]\n" \
"buffer_load_dwordx4 v22, s[64:67], s85 offen lds\n" \
"v_mfma_f32_16x16x32_bf16 a[120:123], v[172:175], v[128:131], a[120:123]\n" \
"s_add_u32 m0, s30, 0x9400\n" \
"s_mov_b32 s84, 0xa280\n" \
"v_mfma_f32_16x16x32_bf16 a[116:119], v[104:107], v[64:67], a[116:119]\n" \
"buffer_load_dwordx4 v23, s[64:67], s84 offen lds\n" \
"v_mfma_f32_16x16x32_bf16 a[116:119], v[168:171], v[128:131], a[116:119]\n" \
"s_add_u32 m0, s31, 0x8800\n" \
"s_mov_b32 s85, 0x290000\n" \
"v_mfma_f32_16x16x32_bf16 a[112:115], v[100:103], v[64:67], a[112:115]\n" \
"buffer_load_dwordx4 v27, s[68:71], s85 offen lds\n" \
"v_mfma_f32_16x16x32_bf16 a[112:115], v[164:167], v[128:131], a[112:115]\n" \
"ds_read_b128 v[64:67], v28 offset:22528\n" \
"v_mfma_f32_16x16x32_bf16 a[144:147], v[100:103], v[68:71], a[144:147]\n" \
"ds_read_b128 v[128:131], v29 offset:22528\n" \
"v_mfma_f32_16x16x32_bf16 a[144:147], v[164:167], v[132:135], a[144:147]\n" \
"s_add_u32 m0, s31, 0xc800\n" \
"s_mov_b32 s84, 0x290000\n" \
"v_mfma_f32_16x16x32_bf16 a[148:151], v[104:107], v[68:71], a[148:151]\n" \
"buffer_load_dwordx4 v27, s[72:75], s84 offen lds\n" \
"v_mfma_f32_16x16x32_bf16 a[148:151], v[168:171], v[132:135], a[148:151]\n" \
"s_add_u32 m0, s30, 0x9800\n" \
"s_mov_b32 s85, 0xc280\n" \
"v_mfma_f32_16x16x32_bf16 a[152:155], v[108:111], v[68:71], a[152:155]\n" \
"buffer_load_dwordx4 v22, s[64:67], s85 offen lds\n" \
"v_mfma_f32_16x16x32_bf16 a[152:155], v[172:175], v[132:135], a[152:155]\n" \
"s_add_u32 m0, s30, 0x9c00\n" \
"s_mov_b32 s84, 0xe280\n" \
"v_mfma_f32_16x16x32_bf16 a[156:159], v[112:115], v[68:71], a[156:159]\n" \
"buffer_load_dwordx4 v23, s[64:67], s84 offen lds\n" \
"v_mfma_f32_16x16x32_bf16 a[156:159], v[176:179], v[132:135], a[156:159]\n" \
"ds_read_b128 v[68:71], v28 offset:0\n" \
"v_mfma_f32_16x16x32_bf16 a[188:191], v[112:115], v[72:75], a[188:191]\n" \
"ds_read_b128 v[132:135], v29 offset:0\n" \
"v_mfma_f32_16x16x32_bf16 a[188:191], v[176:179], v[136:139], a[188:191]\n" \
"ds_read_b128 v[200:203], v199 offset:0\n" \
"v_mfma_f32_16x16x32_bf16 a[184:187], v[108:111], v[72:75], a[184:187]\n" \
"ds_read_b128 v[204:207], v199 offset:4096\n" \
"v_mfma_f32_16x16x32_bf16 a[184:187], v[172:175], v[136:139], a[184:187]\n" \
"ds_read_b64_tr_b16 v[150:151], v32 offset:9216\n" \
"v_mfma_f32_16x16x32_bf16 a[180:183], v[104:107], v[72:75], a[180:183]\n" \
"ds_read_b64_tr_b16 v[88:89], v33 offset:0\n" \
"v_mfma_f32_16x16x32_bf16 a[180:183], v[168:171], v[136:139], a[180:183]\n" \
"ds_read_b128 v[208:211], v199 offset:8192\n" \
"v_mfma_f32_16x16x32_bf16 a[176:179], v[100:103], v[72:75], a[176:179]\n" \
".fill (L_pb + 12000 - .) / 4, 4, 0xBF800000\n" \
"L_pp3:\n" \
"s_cbranch_execz L_pret\n" \
"s_nop 0\n" \
"ds_read_b64_tr_b16 v[90:91], v33 offset:1024\n" \
"v_mfma_f32_16x16x32_bf16 a[176:179], v[164:167], v[136:139], a[176:179]\n" \
"ds_read_b128 v[72:75], v28 offset:2048\n" \
"v_mfma_f32_16x16x32_bf16 a[208:211], v[100:103], v[76:79], a[208:211]\n" \
"ds_read_b128 v[136:139], v29 offset:2048\n" \
"v_mfma_f32_16x16x32_bf16 a[208:211], v[164:167], v[140:143], a[208:211]\n" \
"ds_read_b64_tr_b16 v[152:153], v33 offset:8192\n" \
"v_mfma_f32_16x16x32_bf16 a[212:215], v[104:107], v[76:79], a[212:215]\n" \
"ds_read_b64_tr_b16 v[154:155], v33 offset:9216\n" \
"v_mfma_f32_16x16x32_bf16 a[212:215], v[168:171], v[140:143], a[212:215]\n" \
"ds_read_b64_tr_b16 v[92:93], v34 offset:0\n" \
"v_mfma_f32_16x16x32_bf16 a[216:219], v[108:111], v[76:79], a[216:219]\n" \
"ds_read_b64_tr_b16 v[94:95], v34 offset:1024\n" \
"v_mfma_f32_16x16x32_bf16 a[216:219], v[172:175], v[140:143], a[216:219]\n" \
"ds_read_b64_tr_b16 v[156:157], v34 offset:8192\n" \
"v_mfma_f32_16x16x32_bf16 a[220:223], v[112:115], v[76:79], a[220:223]\n" \
"ds_read_b64_tr_b16 v[158:159], v34 offset:9216\n" \
"v_mfma_f32_16x16x32_bf16 a[220:223], v[176:179], v[140:143], a[220:223]\n" \
"ds_read_b128 v[76:79], v28 offset:4096\n" \
"v_mfma_f32_16x16x32_bf16 a[252:255], v[112:115], v[80:83], a[252:255]\n" \
"ds_read_b128 v[140:143], v29 offset:4096\n" \
"v_mfma_f32_16x16x32_bf16 a[252:255], v[176:179], v[144:147], a[252:255]\n" \
"ds_read_b64_tr_b16 v[96:97], v35 offset:0\n" \
"v_mfma_f32_16x16x32_bf16 a[248:251], v[108:111], v[80:83], a[248:251]\n" \
"ds_read_b64_tr_b16 v[98:99], v35 offset:1024\n" \
"v_mfma_f32_16x16x32_bf16 a[248:251], v[172:175], v[144:147], a[248:251]\n" \
"ds_read_b64_tr_b16 v[160:161], v35 offset:8192\n" \
"v_mfma_f32_16x16x32_bf16 a[244:247], v[104:107], v[80:83], a[244:247]\n" \
"ds_read_b64_tr_b16 v[162:163], v35 offset:9216\n" \
"v_mfma_f32_16x16x32_bf16 a[244:247], v[168:171], v[144:147], a[244:247]\n" \
"v_mfma_f32_16x16x32_bf16 a[240:243], v[100:103], v[80:83], a[240:243]\n" \
"v_mfma_f32_16x16x32_bf16 a[240:243], v[164:167], v[144:147], a[240:243]\n" \
"ds_read_b128 v[80:83], v28 offset:6144\n" \
"ds_read_b128 v[144:147], v29 offset:6144\n" \
"s_waitcnt lgkmcnt(15)\n" \
"v_mfma_f32_16x16x32_bf16 a[0:3], v[84:87], v[52:55], a[0:3]\n" \
"s_add_u32 m0, s31, 0x8c00\n" \
"s_mov_b32 s85, 0x298000\n" \
"v_mfma_f32_16x16x32_bf16 a[0:3], v[148:151], v[116:119], a[0:3]\n" \
"buffer_load_dwordx4 v27, s[68:71], s85 offen lds\n" \
"v_mfma_f32_16x16x32_bf16 a[4:7], v[88:91], v[52:55], a[4:7]\n" \
"ds_read_b64_tr_b16 v[100:101], v36 offset:0\n" \
"s_waitcnt lgkmcnt(13)\n" \
"v_mfma_f32_16x16x32_bf16 a[4:7], v[152:155], v[116:119], a[4:7]\n" \
"s_add_u32 m0, s31, 0xcc00\n" \
"s_mov_b32 s84, 0x298000\n" \
"s_waitcnt lgkmcnt(11)\n" \
"v_mfma_f32_16x16x32_bf16 a[8:11], v[92:95], v[52:55], a[8:11]\n" \
"buffer_load_dwordx4 v27, s[72:75], s84 offen lds\n" \
"s_waitcnt lgkmcnt(9)\n" \
"v_mfma_f32_16x16x32_bf16 a[8:11], v[156:159], v[116:119], a[8:11]\n" \
"ds_read_b64_tr_b16 v[102:103], v36 offset:1024\n" \
"s_waitcnt lgkmcnt(6)\n" \
"v_mfma_f32_16x16x32_bf16 a[12:15], v[96:99], v[52:55], a[12:15]\n" \
"ds_read_b64_tr_b16 v[164:165], v36 offset:8192\n" \
"s_waitcnt lgkmcnt(5)\n" \
"v_mfma_f32_16x16x32_bf16 a[12:15], v[160:163], v[116:119], a[12:15]\n" \
"v_mfma_f32_16x16x32_bf16 a[44:47], v[96:99], v[56:59], a[44:47]\n" \
"v_mfma_f32_16x16x32_bf16 a[44:47], v[160:163], v[120:123], a[44:47]\n" \
"ds_read_b64_tr_b16 v[166:167], v36 offset:9216\n" \
"v_mfma_f32_16x16x32_bf16 a[40:43], v[92:95], v[56:59], a[40:43]\n" \
"v_mfma_f32_16x16x32_bf16 a[40:43], v[156:159], v[120:123], a[40:43]\n" \
"v_mfma_f32_16x16x32_bf16 a[36:39], v[88:91], v[56:59], a[36:39]\n" \
"v_mfma_f32_16x16x32_bf16 a[36:39], v[152:155], v[120:123], a[36:39]\n" \
"ds_read_b64_tr_b16 v[104:105], v37 offset:0\n" \
"v_mfma_f32_16x16x32_bf16 a[32:35], v[84:87], v[56:59], a[32:35]\n" \
"v_mfma_f32_16x16x32_bf16 a[32:35], v[148:151], v[120:123], a[32:35]\n" \
"v_mfma_f32_16x16x32_bf16 a[64:67], v[84:87], v[60:63], a[64:67]\n" \
"ds_read_b64_tr_b16 v[106:107], v37 offset:1024\n" \
"v_mfma_f32_16x16x32_bf16 a[64:67], v[148:151], v[124:127], a[64:67]\n" \
"v_mfma_f32_16x16x32_bf16 a[68:71], v[88:91], v[60:63], a[68:71]\n" \
"v_mfma_f32_16x16x32_bf16 a[68:71], v[152:155], v[124:127], a[68:71]\n" \
"buffer_store_dwordx4 v[200:203], v20, s[80:83], s35 offen sc1\n" \
"v_mfma_f32_16x16x32_bf16 a[72:75], v[92:95], v[60:63], a[72:75]\n" \
"s_mov_b32 m0, s32\n" \
"ds_read_b64_tr_b16 v[168:169], v37 offset:8192\n" \
"v_mfma_f32_16x16x32_bf16 a[72:75], v[156:159], v[124:127], a[72:75]\n" \
"s_mov_b32 exec_lo, 0x0\n" \
"s_mov_b32 exec_hi, 0xffffffff\n" \
"global_load_lds_dword v[188:189], off\n" \
"s_mov_b64 exec, -1\n" \
"v_mfma_f32_16x16x32_bf16 a[76:79], v[96:99], v[60:63], a[76:79]\n" \
"ds_read_b64_tr_b16 v[170:171], v37 offset:9216\n" \
"v_mfma_f32_16x16x32_bf16 a[76:79], v[160:163], v[124:127], a[76:79]\n" \
"v_mfma_f32_16x16x32_bf16 a[108:111], v[96:99], v[64:67], a[108:111]\n" \
"v_mfma_f32_16x16x32_bf16 a[108:111], v[160:163], v[128:131], a[108:111]\n" \
"v_mfma_f32_16x16x32_bf16 a[104:107], v[92:95], v[64:67], a[104:107]\n" \
"ds_read_b64_tr_b16 v[108:109], v38 offset:0\n" \
"v_mfma_f32_16x16x32_bf16 a[104:107], v[156:159], v[128:131], a[104:107]\n" \
"v_mfma_f32_16x16x32_bf16 a[100:103], v[88:91], v[64:67], a[100:103]\n" \
"buffer_store_dwordx4 v[204:207], v20, s[80:83], s88 offen sc1\n" \
"v_mfma_f32_16x16x32_bf16 a[100:103], v[152:155], v[128:131], a[100:103]\n" \
"ds_read_b64_tr_b16 v[110:111], v38 offset:1024\n" \
"v_mfma_f32_16x16x32_bf16 a[96:99], v[84:87], v[64:67], a[96:99]\n" \
"v_mfma_f32_16x16x32_bf16 a[96:99], v[148:151], v[128:131], a[96:99]\n" \
"v_mfma_f32_16x16x32_bf16 a[128:131], v[84:87], v[68:71], a[128:131]\n" \
"ds_read_b64_tr_b16 v[172:173], v38 offset:8192\n" \
"v_mfma_f32_16x16x32_bf16 a[128:131], v[148:151], v[132:135], a[128:131]\n" \
"v_mfma_f32_16x16x32_bf16 a[132:135], v[88:91], v[68:71], a[132:135]\n" \
"v_mfma_f32_16x16x32_bf16 a[132:135], v[152:155], v[132:135], a[132:135]\n" \
"ds_read_b64_tr_b16 v[174:175], v38 offset:9216\n" \
"v_mfma_f32_16x16x32_bf16 a[136:139], v[92:95], v[68:71], a[136:139]\n" \
"v_mfma_f32_16x16x32_bf16 a[136:139], v[156:159], v[132:135], a[136:139]\n" \
"v_mfma_f32_16x16x32_bf16 a[140:143], v[96:99], v[68:71], a[140:143]\n" \
"buffer_store_dwordx4 v[208:211], v20, s[80:83], s36 offen sc1\n" \
"v_mfma_f32_16x16x32_bf16 a[140:143], v[160:163], v[132:135], a[140:143]\n" \
"ds_read_b64_tr_b16 v[112:113], v39 offset:0\n" \
"v_mfma_f32_16x16x32_bf16 a[172:175], v[96:99], v[72:75], a[172:175]\n" \
"v_mfma_f32_16x16x32_bf16 a[172:175], v[160:163], v[136:139], a[172:175]\n" \
"v_mfma_f32_16x16x32_bf16 a[168:171], v[92:95], v[72:75], a[168:171]\n" \
"ds_read_b64_tr_b16 v[114:115], v39 offset:1024\n" \
"v_mfma_f32_16x16x32_bf16 a[168:171], v[156:159], v[136:139], a[168:171]\n" \
"v_mfma_f32_16x16x32_bf16 a[164:167], v[88:91], v[72:75], a[164:167]\n" \
"v_mfma_f32_16x16x32_bf16 a[164:167], v[152:155], v[136:139], a[164:167]\n" \
"ds_read_b64_tr_b16 v[176:177], v39 offset:8192\n" \
"v_mfma_f32_16x16x32_bf16 a[160:163], v[84:87], v[72:75], a[160:163]\n" \
"v_mfma_f32_16x16x32_bf16 a[160:163], v[148:151], v[136:139], a[160:163]\n" \
"v_mfma_f32_16x16x32_bf16 a[192:195], v[84:87], v[76:79], a[192:195]\n" \
"ds_read_b64_tr_b16 v[178:179], v39 offset:9216\n" \
"v_mfma_f32_16x16x32_bf16 a[192:195], v[148:151], v[140:143], a[192:195]\n" \
"v_mfma_f32_16x16x32_bf16 a[196:199], v[88:91], v[76:79], a[196:199]\n" \
"v_mfma_f32_16x16x32_bf16 a[196:199], v[152:155], v[140:143], a[196:199]\n" \
"v_mfma_f32_16x16x32_bf16 a[200:203], v[92:95], v[76:79], a[200:203]\n" \
"v_mfma_f32_16x16x32_bf16 a[200:203], v[156:159], v[140:143], a[200:203]\n" \
"v_mfma_f32_16x16x32_bf16 a[204:207], v[96:99], v[76:79], a[204:207]\n" \
"v_mfma_f32_16x16x32_bf16 a[204:207], v[160:163], v[140:143], a[204:207]\n" \
"s_waitcnt lgkmcnt(15)\n" \
"v_mfma_f32_16x16x32_bf16 a[236:239], v[96:99], v[80:83], a[236:239]\n" \
"v_mfma_f32_16x16x32_bf16 a[236:239], v[160:163], v[144:147], a[236:239]\n" \
"v_mfma_f32_16x16x32_bf16 a[232:235], v[92:95], v[80:83], a[232:235]\n" \
"v_mfma_f32_16x16x32_bf16 a[232:235], v[156:159], v[144:147], a[232:235]\n" \
"v_mfma_f32_16x16x32_bf16 a[228:231], v[88:91], v[80:83], a[228:231]\n" \
"v_mfma_f32_16x16x32_bf16 a[228:231], v[152:155], v[144:147], a[228:231]\n" \
"v_mfma_f32_16x16x32_bf16 a[224:227], v[84:87], v[80:83], a[224:227]\n" \
"v_mfma_f32_16x16x32_bf16 a[224:227], v[148:151], v[144:147], a[224:227]\n" \
"s_waitcnt vmcnt(4) lgkmcnt(0)\n" \
"s_barrier\n" \
"v_mfma_f32_16x16x32_bf16 a[16:19], v[100:103], v[52:55], a[16:19]\n" \
"s_add_u32 m0, s30, 0x0\n" \
"s_mov_b32 s85, 0x300\n" \
"v_mfma_f32_16x16x32_bf16 a[16:19], v[164:167], v[116:119], a[16:19]\n" \
"buffer_load_dwordx4 v22, s[64:67], s85 offen lds\n" \
"v_mfma_f32_16x16x32_bf16 a[20:23], v[104:107], v[52:55], a[20:23]\n" \
"ds_read_b64_tr_b16 v[84:85], v32 offset:32768\n" \
"v_mfma_f32_16x16x32_bf16 a[20:23], v[168:171], v[116:119], a[20:23]\n" \
"s_add_u32 m0, s30, 0x400\n" \
"s_mov_b32 s84, 0x2300\n" \
"v_mfma_f32_16x16x32_bf16 a[24:27], v[108:111], v[52:55], a[24:27]\n" \
"buffer_load_dwordx4 v23, s[64:67], s84 offen lds\n" \
"v_mfma_f32_16x16x32_bf16 a[24:27], v[172:175], v[116:119], a[24:27]\n" \
"ds_read_b64_tr_b16 v[86:87], v32 offset:33792\n" \
"v_mfma_f32_16x16x32_bf16 a[28:31], v[112:115], v[52:55], a[28:31]\n" \
"ds_read_b64_tr_b16 v[148:149], v32 offset:40960\n" \
"v_mfma_f32_16x16x32_bf16 a[28:31], v[176:179], v[116:119], a[28:31]\n" \
"ds_read_b128 v[52:55], v28 offset:49152\n" \
"v_mfma_f32_16x16x32_bf16 a[60:63], v[112:115], v[56:59], a[60:63]\n" \
"ds_read_b128 v[116:119], v29 offset:49152\n" \
"v_mfma_f32_16x16x32_bf16 a[60:63], v[176:179], v[120:123], a[60:63]\n" \
"s_add_u32 m0, s31, 0x0\n" \
"s_nop 0\n" \
"s_mov_b32 s85, 0x300000\n" \
"v_mfma_f32_16x16x32_bf16 a[56:59], v[108:111], v[56:59], a[56:59]\n" \
"buffer_load_dwordx4 v26, s[68:71], s85 offen lds\n" \
"v_mfma_f32_16x16x32_bf16 a[56:59], v[172:175], v[120:123], a[56:59]\n" \
"s_add_u32 m0, s31, 0x4000\n" \
"s_mov_b32 s84, 0x300000\n" \
"v_mfma_f32_16x16x32_bf16 a[52:55], v[104:107], v[56:59], a[52:55]\n" \
"buffer_load_dwordx4 v26, s[72:75], s84 offen lds\n" \
"v_mfma_f32_16x16x32_bf16 a[52:55], v[168:171], v[120:123], a[52:55]\n" \
"s_add_u32 m0, s30, 0x800\n" \
"s_mov_b32 s85, 0x4300\n" \
"v_mfma_f32_16x16x32_bf16 a[48:51], v[100:103], v[56:59], a[48:51]\n" \
"buffer_load_dwordx4 v22, s[64:67], s85 offen lds\n" \
"v_mfma_f32_16x16x32_bf16 a[48:51], v[164:167], v[120:123], a[48:51]\n" \
"ds_read_b128 v[56:59], v28 offset:51200\n" \
"v_mfma_f32_16x16x32_bf16 a[80:83], v[100:103], v[60:63], a[80:83]\n" \
"ds_read_b128 v[120:123], v29 offset:51200\n" \
"v_mfma_f32_16x16x32_bf16 a[80:83], v[164:167], v[124:127], a[80:83]\n" \
"s_add_u32 m0, s30, 0xc00\n" \
"s_mov_b32 s84, 0x6300\n" \
"v_mfma_f32_16x16x32_bf16 a[84:87], v[104:107], v[60:63], a[84:87]\n" \
"buffer_load_dwordx4 v23, s[64:67], s84 offen lds\n" \
"v_mfma_f32_16x16x32_bf16 a[84:87], v[168:171], v[124:127], a[84:87]\n" \
"ds_read_b64_tr_b16 v[150:151], v32 offset:41984\n" \
"v_mfma_f32_16x16x32_bf16 a[88:91], v[108:111], v[60:63], a[88:91]\n" \
"s_add_u32 m0, s31, 0x400\n" \
"s_mov_b32 s85, 0x308000\n" \
"v_mfma_f32_16x16x32_bf16 a[88:91], v[172:175], v[124:127], a[88:91]\n" \
"buffer_load_dwordx4 v26, s[68:71], s85 offen lds\n" \
"v_mfma_f32_16x16x32_bf16 a[92:95], v[112:115], v[60:63], a[92:95]\n" \
"ds_read_b64_tr_b16 v[88:89], v33 offset:32768\n" \
"v_mfma_f32_16x16x32_bf16 a[92:95], v[176:179], v[124:127], a[92:95]\n" \
"ds_read_b128 v[60:63], v28 offset:53248\n" \
"v_mfma_f32_16x16x32_bf16 a[124:127], v[112:115], v[64:67], a[124:127]\n" \
"ds_read_b128 v[124:127], v29 offset:53248\n" \
"v_mfma_f32_16x16x32_bf16 a[124:127], v[176:179], v[128:131], a[124:127]\n" \
"s_add_u32 m0, s31, 0x4400\n" \
"s_mov_b32 s84, 0x308000\n" \
"v_mfma_f32_16x16x32_bf16 a[120:123], v[108:111], v[64:67], a[120:123]\n" \
"buffer_load_dwordx4 v26, s[72:75], s84 offen lds\n" \
"v_mfma_f32_16x16x32_bf16 a[120:123], v[172:175], v[128:131], a[120:123]\n" \
"s_add_u32 m0, s30, 0x1000\n" \
"s_mov_b32 s85, 0x8300\n" \
"v_mfma_f32_16x16x32_bf16 a[116:119], v[104:107], v[64:67], a[116:119]\n" \
"buffer_load_dwordx4 v22, s[64:67], s85 offen lds\n" \
"v_mfma_f32_16x16x32_bf16 a[116:119], v[168:171], v[128:131], a[116:119]\n" \
"ds_read_b64_tr_b16 v[90:91], v33 offset:33792\n" \
"v_mfma_f32_16x16x32_bf16 a[112:115], v[100:103], v[64:67], a[112:115]\n" \
"ds_read_b64_tr_b16 v[152:153], v33 offset:40960\n" \
"v_mfma_f32_16x16x32_bf16 a[112:115], v[164:167], v[128:131], a[112:115]\n" \
"ds_read_b128 v[64:67], v28 offset:55296\n" \
"v_mfma_f32_16x16x32_bf16 a[144:147], v[100:103], v[68:71], a[144:147]\n" \
"ds_read_b128 v[128:131], v29 offset:55296\n" \
"v_mfma_f32_16x16x32_bf16 a[144:147], v[164:167], v[132:135], a[144:147]\n" \
"s_add_u32 m0, s30, 0x1400\n" \
"s_mov_b32 s84, 0xa300\n" \
"v_mfma_f32_16x16x32_bf16 a[148:151], v[104:107], v[68:71], a[148:151]\n" \
"buffer_load_dwordx4 v23, s[64:67], s84 offen lds\n" \
"v_mfma_f32_16x16x32_bf16 a[148:151], v[168:171], v[132:135], a[148:151]\n" \
"s_add_u32 m0, s31, 0x800\n" \
"s_mov_b32 s85, 0x310000\n" \
"v_mfma_f32_16x16x32_bf16 a[152:155], v[108:111], v[68:71], a[152:155]\n" \
"buffer_load_dwordx4 v27, s[68:71], s85 offen lds\n" \
"v_mfma_f32_16x16x32_bf16 a[152:155], v[172:175], v[132:135], a[152:155]\n" \
"s_add_u32 m0, s31, 0x4800\n" \
"s_mov_b32 s84, 0x310000\n" \
"v_mfma_f32_16x16x32_bf16 a[156:159], v[112:115], v[68:71], a[156:159]\n" \
"buffer_load_dwordx4 v27, s[72:75], s84 offen lds\n" \
"v_mfma_f32_16x16x32_bf16 a[156:159], v[176:179], v[132:135], a[156:159]\n" \
"ds_read_b128 v[68:71], v28 offset:32768\n" \
"v_mfma_f32_16x16x32_bf16 a[188:191], v[112:115], v[72:75], a[188:191]\n" \
"ds_read_b128 v[132:135], v29 offset:32768\n" \
"v_mfma_f32_16x16x32_bf16 a[188:191], v[176:179], v[136:139], a[188:191]\n" \
"s_add_u32 m0, s30, 0x1800\n" \
"s_mov_b32 s85, 0xc300\n" \
"v_mfma_f32_16x16x32_bf16 a[184:187], v[108:111], v[72:75], a[184:187]\n" \
"buffer_load_dwordx4 v22, s[64:67], s85 offen lds\n" \
"v_mfma_f32_16x16x32_bf16 a[184:187], v[172:175], v[136:139], a[184:187]\n" \
"ds_read_b64_tr_b16 v[154:155], v33 offset:41984\n" \
"v_mfma_f32_16x16x32_bf16 a[180:183], v[104:107], v[72:75], a[180:183]\n" \
"s_add_u32 m0, s30, 0x1c00\n" \
"s_mov_b32 s84, 0xe300\n" \
"v_mfma_f32_16x16x32_bf16 a[180:183], v[168:171], v[136:139], a[180:183]\n" \
"buffer_load_dwordx4 v23, s[64:67], s84 offen lds\n" \
"v_mfma_f32_16x16x32_bf16 a[176:179], v[100:103], v[72:75], a[176:179]\n" \
"ds_read_b64_tr_b16 v[92:93], v34 offset:32768\n" \
"v_mfma_f32_16x16x32_bf16 a[176:179], v[164:167], v[136:139], a[176:179]\n" \
"ds_read_b128 v[72:75], v28 offset:34816\n" \
"v_mfma_f32_16x16x32_bf16 a[208:211], v[100:103], v[76:79], a[208:211]\n" \
"ds_read_b128 v[136:139], v29 offset:34816\n" \
"v_mfma_f32_16x16x32_bf16 a[208:211], v[164:167], v[140:143], a[208:211]\n" \
"ds_read_b64_tr_b16 v[94:95], v34 offset:33792\n" \
"v_mfma_f32_16x16x32_bf16 a[212:215], v[104:107], v[76:79], a[212:215]\n" \
"ds_read_b64_tr_b16 v[156:157], v34 offset:40960\n" \
"v_mfma_f32_16x16x32_bf16 a[212:215], v[168:171], v[140:143], a[212:215]\n" \
"ds_read_b64_tr_b16 v[158:159], v34 offset:41984\n" \
"v_mfma_f32_16x16x32_bf16 a[216:219], v[108:111], v[76:79], a[216:219]\n" \
"ds_read_b64_tr_b16 v[96:97], v35 offset:32768\n" \
"v_mfma_f32_16x16x32_bf16 a[216:219], v[172:175], v[140:143], a[216:219]\n" \
"ds_read_b64_tr_b16 v[98:99], v35 offset:33792\n" \
"v_mfma_f32_16x16x32_bf16 a[220:223], v[112:115], v[76:79], a[220:223]\n" \
"ds_read_b64_tr_b16 v[160:161], v35 offset:40960\n" \
"v_mfma_f32_16x16x32_bf16 a[220:223], v[176:179], v[140:143], a[220:223]\n" \
"ds_read_b128 v[76:79], v28 offset:36864\n" \
"v_mfma_f32_16x16x32_bf16 a[252:255], v[112:115], v[80:83], a[252:255]\n" \
"ds_read_b128 v[140:143], v29 offset:36864\n" \
"v_mfma_f32_16x16x32_bf16 a[252:255], v[176:179], v[144:147], a[252:255]\n" \
"ds_read_b64_tr_b16 v[162:163], v35 offset:41984\n" \
"v_mfma_f32_16x16x32_bf16 a[248:251], v[108:111], v[80:83], a[248:251]\n" \
"v_mfma_f32_16x16x32_bf16 a[248:251], v[172:175], v[144:147], a[248:251]\n" \
"v_mfma_f32_16x16x32_bf16 a[244:247], v[104:107], v[80:83], a[244:247]\n" \
"v_mfma_f32_16x16x32_bf16 a[244:247], v[168:171], v[144:147], a[244:247]\n" \
"v_mfma_f32_16x16x32_bf16 a[240:243], v[100:103], v[80:83], a[240:243]\n" \
"v_mfma_f32_16x16x32_bf16 a[240:243], v[164:167], v[144:147], a[240:243]\n" \
"ds_read_b128 v[80:83], v28 offset:38912\n" \
"ds_read_b128 v[144:147], v29 offset:38912\n" \
"s_waitcnt lgkmcnt(15)\n" \
"v_mfma_f32_16x16x32_bf16 a[0:3], v[84:87], v[52:55], a[0:3]\n" \
"s_add_u32 m0, s31, 0xc00\n" \
"s_mov_b32 s85, 0x318000\n" \
"v_mfma_f32_16x16x32_bf16 a[0:3], v[148:151], v[116:119], a[0:3]\n" \
"buffer_load_dwordx4 v27, s[68:71], s85 offen lds\n" \
"v_mfma_f32_16x16x32_bf16 a[4:7], v[88:91], v[52:55], a[4:7]\n" \
"ds_read_b64_tr_b16 v[100:101], v36 offset:32768\n" \
"s_waitcnt lgkmcnt(15)\n" \
"v_mfma_f32_16x16x32_bf16 a[4:7], v[152:155], v[116:119], a[4:7]\n" \
"s_add_u32 m0, s31, 0x4c00\n" \
"s_mov_b32 s84, 0x318000\n" \
"s_waitcnt lgkmcnt(11)\n" \
"v_mfma_f32_16x16x32_bf16 a[8:11], v[92:95], v[52:55], a[8:11]\n" \
"buffer_load_dwordx4 v27, s[72:75], s84 offen lds\n" \
"s_waitcnt lgkmcnt(9)\n" \
"v_mfma_f32_16x16x32_bf16 a[8:11], v[156:159], v[116:119], a[8:11]\n" \
"ds_read_b64_tr_b16 v[102:103], v36 offset:33792\n" \
"s_waitcnt lgkmcnt(8)\n" \
"v_mfma_f32_16x16x32_bf16 a[12:15], v[96:99], v[52:55], a[12:15]\n" \
"ds_read_b64_tr_b16 v[164:165], v36 offset:40960\n" \
"s_waitcnt lgkmcnt(5)\n" \
"v_mfma_f32_16x16x32_bf16 a[12:15], v[160:163], v[116:119], a[12:15]\n" \
"v_mfma_f32_16x16x32_bf16 a[44:47], v[96:99], v[56:59], a[44:47]\n" \
"v_mfma_f32_16x16x32_bf16 a[44:47], v[160:163], v[120:123], a[44:47]\n" \
"ds_read_b64_tr_b16 v[166:167], v36 offset:41984\n" \
"v_mfma_f32_16x16x32_bf16 a[40:43], v[92:95], v[56:59], a[40:43]\n" \
"v_mfma_f32_16x16x32_bf16 a[40:43], v[156:159], v[120:123], a[40:43]\n" \
"v_mfma_f32_16x16x32_bf16 a[36:39], v[88:91], v[56:59], a[36:39]\n" \
"v_mfma_f32_16x16x32_bf16 a[36:39], v[152:155], v[120:123], a[36:39]\n" \
"ds_read_b64_tr_b16 v[104:105], v37 offset:32768\n" \
"v_mfma_f32_16x16x32_bf16 a[32:35], v[84:87], v[56:59], a[32:35]\n" \
"v_mfma_f32_16x16x32_bf16 a[32:35], v[148:151], v[120:123], a[32:35]\n" \
"v_mfma_f32_16x16x32_bf16 a[64:67], v[84:87], v[60:63], a[64:67]\n" \
"ds_read_b64_tr_b16 v[106:107], v37 offset:33792\n" \
"v_mfma_f32_16x16x32_bf16 a[64:67], v[148:151], v[124:127], a[64:67]\n" \
"v_mfma_f32_16x16x32_bf16 a[68:71], v[88:91], v[60:63], a[68:71]\n" \
"v_lshlrev_b32_e32 v45, 3, v20\n" \
"v_add_u32_e32 v45, s31, v45\n" \
"v_mfma_f32_16x16x32_bf16 a[68:71], v[152:155], v[124:127], a[68:71]\n" \
"s_getpc_b64 s[86:87]\n" \
"s_sub_u32 s86, s86, 0xf800\n" \
"s_subb_u32 s87, s87, 0\n" \
"s_mov_b32 m0, s32\n" \
"s_nop 0\n" \
"v_mfma_f32_16x16x32_bf16 a[72:75], v[92:95], v[60:63], a[72:75]\n" \
"global_load_lds_dword v45, s[86:87]\n" \
"v_mfma_f32_16x16x32_bf16 a[72:75], v[156:159], v[124:127], a[72:75]\n" \
"ds_read_b64_tr_b16 v[168:169], v37 offset:40960\n" \
"v_mfma_f32_16x16x32_bf16 a[76:79], v[96:99], v[60:63], a[76:79]\n" \
"ds_read_b64_tr_b16 v[170:171], v37 offset:41984\n" \
"v_mfma_f32_16x16x32_bf16 a[76:79], v[160:163], v[124:127], a[76:79]\n" \
"v_mfma_f32_16x16x32_bf16 a[108:111], v[96:99], v[64:67], a[108:111]\n" \
"v_mfma_f32_16x16x32_bf16 a[108:111], v[160:163], v[128:131], a[108:111]\n" \
"v_mfma_f32_16x16x32_bf16 a[104:107], v[92:95], v[64:67], a[104:107]\n" \
"ds_read_b64_tr_b16 v[108:109], v38 offset:32768\n" \
"v_mfma_f32_16x16x32_bf16 a[104:107], v[156:159], v[128:131], a[104:107]\n" \
"v_mfma_f32_16x16x32_bf16 a[100:103], v[88:91], v[64:67], a[100:103]\n" \
"v_mfma_f32_16x16x32_bf16 a[100:103], v[152:155], v[128:131], a[100:103]\n" \
"ds_read_b64_tr_b16 v[110:111], v38 offset:33792\n" \
"v_mfma_f32_16x16x32_bf16 a[96:99], v[84:87], v[64:67], a[96:99]\n" \
"v_mfma_f32_16x16x32_bf16 a[96:99], v[148:151], v[128:131], a[96:99]\n" \
"v_mfma_f32_16x16x32_bf16 a[128:131], v[84:87], v[68:71], a[128:131]\n" \
"ds_read_b64_tr_b16 v[172:173], v38 offset:40960\n" \
"v_mfma_f32_16x16x32_bf16 a[128:131], v[148:151], v[132:135], a[128:131]\n" \
"v_mfma_f32_16x16x32_bf16 a[132:135], v[88:91], v[68:71], a[132:135]\n" \
"v_mfma_f32_16x16x32_bf16 a[132:135], v[152:155], v[132:135], a[132:135]\n" \
"ds_read_b64_tr_b16 v[174:175], v38 offset:41984\n" \
"v_mfma_f32_16x16x32_bf16 a[136:139], v[92:95], v[68:71], a[136:139]\n" \
"v_mfma_f32_16x16x32_bf16 a[136:139], v[156:159], v[132:135], a[136:139]\n" \
"v_mfma_f32_16x16x32_bf16 a[140:143], v[96:99], v[68:71], a[140:143]\n" \
"v_mfma_f32_16x16x32_bf16 a[140:143], v[160:163], v[132:135], a[140:143]\n" \
"ds_read_b64_tr_b16 v[112:113], v39 offset:32768\n" \
"v_mfma_f32_16x16x32_bf16 a[172:175], v[96:99], v[72:75], a[172:175]\n" \
"v_mfma_f32_16x16x32_bf16 a[172:175], v[160:163], v[136:139], a[172:175]\n" \
"v_mfma_f32_16x16x32_bf16 a[168:171], v[92:95], v[72:75], a[168:171]\n" \
"ds_read_b64_tr_b16 v[114:115], v39 offset:33792\n" \
"v_mfma_f32_16x16x32_bf16 a[168:171], v[156:159], v[136:139], a[168:171]\n" \
"v_mfma_f32_16x16x32_bf16 a[164:167], v[88:91], v[72:75], a[164:167]\n" \
"v_mfma_f32_16x16x32_bf16 a[164:167], v[152:155], v[136:139], a[164:167]\n" \
"ds_read_b64_tr_b16 v[176:177], v39 offset:40960\n" \
"v_mfma_f32_16x16x32_bf16 a[160:163], v[84:87], v[72:75], a[160:163]\n" \
"v_mfma_f32_16x16x32_bf16 a[160:163], v[148:151], v[136:139], a[160:163]\n" \
"v_mfma_f32_16x16x32_bf16 a[192:195], v[84:87], v[76:79], a[192:195]\n" \
"ds_read_b64_tr_b16 v[178:179], v39 offset:41984\n" \
"v_mfma_f32_16x16x32_bf16 a[192:195], v[148:151], v[140:143], a[192:195]\n" \
"v_mfma_f32_16x16x32_bf16 a[196:199], v[88:91], v[76:79], a[196:199]\n" \
"v_mfma_f32_16x16x32_bf16 a[196:199], v[152:155], v[140:143], a[196:199]\n" \
"v_mfma_f32_16x16x32_bf16 a[200:203], v[92:95], v[76:79], a[200:203]\n" \
"v_mfma_f32_16x16x32_bf16 a[200:203], v[156:159], v[140:143], a[200:203]\n" \
"v_mfma_f32_16x16x32_bf16 a[204:207], v[96:99], v[76:79], a[204:207]\n" \
"v_mfma_f32_16x16x32_bf16 a[204:207], v[160:163], v[140:143], a[204:207]\n" \
"s_waitcnt lgkmcnt(15)\n" \
"v_mfma_f32_16x16x32_bf16 a[236:239], v[96:99], v[80:83], a[236:239]\n" \
"v_mfma_f32_16x16x32_bf16 a[236:239], v[160:163], v[144:147], a[236:239]\n" \
"v_mfma_f32_16x16x32_bf16 a[232:235], v[92:95], v[80:83], a[232:235]\n" \
"v_mfma_f32_16x16x32_bf16 a[232:235], v[156:159], v[144:147], a[232:235]\n" \
"v_mfma_f32_16x16x32_bf16 a[228:231], v[88:91], v[80:83], a[228:231]\n" \
"v_mfma_f32_16x16x32_bf16 a[228:231], v[152:155], v[144:147], a[228:231]\n" \
"v_mfma_f32_16x16x32_bf16 a[224:227], v[84:87], v[80:83], a[224:227]\n" \
"v_mfma_f32_16x16x32_bf16 a[224:227], v[148:151], v[144:147], a[224:227]\n" \
"s_waitcnt vmcnt(1) lgkmcnt(0)\n" \
"s_barrier\n" \
"v_mov_b32_e32 v45, s62\n" \
"s_mov_b64 exec, 1\n" \
"buffer_store_dword v45, off, s[80:83], s37 offset:128 sc1\n" \
"s_mov_b64 exec, -1\n" \
"v_mfma_f32_16x16x32_bf16 a[16:19], v[100:103], v[52:55], a[16:19]\n" \
"s_add_u32 m0, s30, 0x8000\n" \
"s_mov_b32 s85, 0x380\n" \
"v_mfma_f32_16x16x32_bf16 a[16:19], v[164:167], v[116:119], a[16:19]\n" \
"buffer_load_dwordx4 v22, s[64:67], s85 offen lds\n" \
"v_mfma_f32_16x16x32_bf16 a[20:23], v[104:107], v[52:55], a[20:23]\n" \
"ds_read_b64_tr_b16 v[84:85], v32 offset:0\n" \
"v_mfma_f32_16x16x32_bf16 a[20:23], v[168:171], v[116:119], a[20:23]\n" \
"s_add_u32 m0, s30, 0x8400\n" \
"s_mov_b32 s84, 0x2380\n" \
"v_mfma_f32_16x16x32_bf16 a[24:27], v[108:111], v[52:55], a[24:27]\n" \
"buffer_load_dwordx4 v23, s[64:67], s84 offen lds\n" \
"v_mfma_f32_16x16x32_bf16 a[24:27], v[172:175], v[116:119], a[24:27]\n" \
"ds_read_b64_tr_b16 v[86:87], v32 offset:1024\n" \
"v_mfma_f32_16x16x32_bf16 a[28:31], v[112:115], v[52:55], a[28:31]\n" \
"ds_read_b64_tr_b16 v[148:149], v32 offset:8192\n" \
"v_mfma_f32_16x16x32_bf16 a[28:31], v[176:179], v[116:119], a[28:31]\n" \
"ds_read_b128 v[52:55], v28 offset:16384\n" \
"v_mfma_f32_16x16x32_bf16 a[60:63], v[112:115], v[56:59], a[60:63]\n" \
"ds_read_b128 v[116:119], v29 offset:16384\n" \
"v_mfma_f32_16x16x32_bf16 a[60:63], v[176:179], v[120:123], a[60:63]\n" \
"s_add_u32 m0, s31, 0x8000\n" \
"s_mov_b32 s85, 0x380000\n" \
"v_mfma_f32_16x16x32_bf16 a[56:59], v[108:111], v[56:59], a[56:59]\n" \
"buffer_load_dwordx4 v26, s[68:71], s85 offen lds\n" \
"v_mfma_f32_16x16x32_bf16 a[56:59], v[172:175], v[120:123], a[56:59]\n" \
"s_add_u32 m0, s31, 0xc000\n" \
"s_mov_b32 s84, 0x380000\n" \
"v_mfma_f32_16x16x32_bf16 a[52:55], v[104:107], v[56:59], a[52:55]\n" \
"buffer_load_dwordx4 v26, s[72:75], s84 offen lds\n" \
"v_mfma_f32_16x16x32_bf16 a[52:55], v[168:171], v[120:123], a[52:55]\n" \
"s_add_u32 m0, s30, 0x8800\n" \
"s_mov_b32 s85, 0x4380\n" \
"v_mfma_f32_16x16x32_bf16 a[48:51], v[100:103], v[56:59], a[48:51]\n" \
"buffer_load_dwordx4 v22, s[64:67], s85 offen lds\n" \
"v_mfma_f32_16x16x32_bf16 a[48:51], v[164:167], v[120:123], a[48:51]\n" \
"ds_read_b128 v[56:59], v28 offset:18432\n" \
"v_mfma_f32_16x16x32_bf16 a[80:83], v[100:103], v[60:63], a[80:83]\n" \
"ds_read_b128 v[120:123], v29 offset:18432\n" \
"v_mfma_f32_16x16x32_bf16 a[80:83], v[164:167], v[124:127], a[80:83]\n" \
"s_add_u32 m0, s30, 0x8c00\n" \
"s_mov_b32 s84, 0x6380\n" \
"v_mfma_f32_16x16x32_bf16 a[84:87], v[104:107], v[60:63], a[84:87]\n" \
"buffer_load_dwordx4 v23, s[64:67], s84 offen lds\n" \
"v_mfma_f32_16x16x32_bf16 a[84:87], v[168:171], v[124:127], a[84:87]\n" \
"ds_read_b64_tr_b16 v[150:151], v32 offset:9216\n" \
"v_mfma_f32_16x16x32_bf16 a[88:91], v[108:111], v[60:63], a[88:91]\n" \
"s_add_u32 m0, s31, 0x8400\n" \
"s_mov_b32 s85, 0x388000\n" \
"v_mfma_f32_16x16x32_bf16 a[88:91], v[172:175], v[124:127], a[88:91]\n" \
"buffer_load_dwordx4 v26, s[68:71], s85 offen lds\n" \
"v_mfma_f32_16x16x32_bf16 a[92:95], v[112:115], v[60:63], a[92:95]\n" \
"ds_read_b64_tr_b16 v[88:89], v33 offset:0\n" \
"v_mfma_f32_16x16x32_bf16 a[92:95], v[176:179], v[124:127], a[92:95]\n" \
"ds_read_b128 v[60:63], v28 offset:20480\n" \
"v_mfma_f32_16x16x32_bf16 a[124:127], v[112:115], v[64:67], a[124:127]\n" \
"ds_read_b128 v[124:127], v29 offset:20480\n" \
"v_mfma_f32_16x16x32_bf16 a[124:127], v[176:179], v[128:131], a[124:127]\n" \
"s_add_u32 m0, s31, 0xc400\n" \
"s_mov_b32 s84, 0x388000\n" \
"v_mfma_f32_16x16x32_bf16 a[120:123], v[108:111], v[64:67], a[120:123]\n" \
"buffer_load_dwordx4 v26, s[72:75], s84 offen lds\n" \
"v_mfma_f32_16x16x32_bf16 a[120:123], v[172:175], v[128:131], a[120:123]\n" \
"s_add_u32 m0, s30, 0x9000\n" \
"s_mov_b32 s85, 0x8380\n" \
"v_mfma_f32_16x16x32_bf16 a[116:119], v[104:107], v[64:67], a[116:119]\n" \
"buffer_load_dwordx4 v22, s[64:67], s85 offen lds\n" \
"v_mfma_f32_16x16x32_bf16 a[116:119], v[168:171], v[128:131], a[116:119]\n" \
"ds_read_b64_tr_b16 v[90:91], v33 offset:1024\n" \
"v_mfma_f32_16x16x32_bf16 a[112:115], v[100:103], v[64:67], a[112:115]\n" \
"ds_read_b64_tr_b16 v[152:153], v33 offset:8192\n" \
"v_mfma_f32_16x16x32_bf16 a[112:115], v[164:167], v[128:131], a[112:115]\n" \
"ds_read_b128 v[64:67], v28 offset:22528\n" \
"v_mfma_f32_16x16x32_bf16 a[144:147], v[100:103], v[68:71], a[144:147]\n" \
"ds_read_b128 v[128:131], v29 offset:22528\n" \
"v_mfma_f32_16x16x32_bf16 a[144:147], v[164:167], v[132:135], a[144:147]\n" \
"s_add_u32 m0, s30, 0x9400\n" \
"s_mov_b32 s84, 0xa380\n" \
"v_mfma_f32_16x16x32_bf16 a[148:151], v[104:107], v[68:71], a[148:151]\n" \
"buffer_load_dwordx4 v23, s[64:67], s84 offen lds\n" \
"v_mfma_f32_16x16x32_bf16 a[148:151], v[168:171], v[132:135], a[148:151]\n" \
"s_add_u32 m0, s31, 0x8800\n" \
"s_mov_b32 s85, 0x390000\n" \
"v_mfma_f32_16x16x32_bf16 a[152:155], v[108:111], v[68:71], a[152:155]\n" \
"buffer_load_dwordx4 v27, s[68:71], s85 offen lds\n" \
"v_mfma_f32_16x16x32_bf16 a[152:155], v[172:175], v[132:135], a[152:155]\n" \
"s_add_u32 m0, s31, 0xc800\n" \
"s_mov_b32 s84, 0x390000\n" \
"v_mfma_f32_16x16x32_bf16 a[156:159], v[112:115], v[68:71], a[156:159]\n" \
"buffer_load_dwordx4 v27, s[72:75], s84 offen lds\n" \
"v_mfma_f32_16x16x32_bf16 a[156:159], v[176:179], v[132:135], a[156:159]\n" \
"ds_read_b128 v[68:71], v28 offset:0\n" \
"v_mfma_f32_16x16x32_bf16 a[188:191], v[112:115], v[72:75], a[188:191]\n" \
"ds_read_b128 v[132:135], v29 offset:0\n" \
"v_mfma_f32_16x16x32_bf16 a[188:191], v[176:179], v[136:139], a[188:191]\n" \
"s_add_u32 m0, s30, 0x9800\n" \
"s_mov_b32 s85, 0xc380\n" \
"v_mfma_f32_16x16x32_bf16 a[184:187], v[108:111], v[72:75], a[184:187]\n" \
"buffer_load_dwordx4 v22, s[64:67], s85 offen lds\n" \
"v_mfma_f32_16x16x32_bf16 a[184:187], v[172:175], v[136:139], a[184:187]\n" \
"ds_read_b64_tr_b16 v[154:155], v33 offset:9216\n" \
"v_mfma_f32_16x16x32_bf16 a[180:183], v[104:107], v[72:75], a[180:183]\n" \
"s_add_u32 m0, s30, 0x9c00\n" \
"s_mov_b32 s84, 0xe380\n" \
"v_mfma_f32_16x16x32_bf16 a[180:183], v[168:171], v[136:139], a[180:183]\n" \
"buffer_load_dwordx4 v23, s[64:67], s84 offen lds\n" \
"v_mfma_f32_16x16x32_bf16 a[176:179], v[100:103], v[72:75], a[176:179]\n" \
"ds_read_b64_tr_b16 v[92:93], v34 offset:0\n" \
"v_mfma_f32_16x16x32_bf16 a[176:179], v[164:167], v[136:139], a[176:179]\n" \
"ds_read_b128 v[72:75], v28 offset:2048\n" \
"v_mfma_f32_16x16x32_bf16 a[208:211], v[100:103], v[76:79], a[208:211]\n" \
"ds_read_b128 v[136:139], v29 offset:2048\n" \
"v_mfma_f32_16x16x32_bf16 a[208:211], v[164:167], v[140:143], a[208:211]\n" \
"ds_read_b64_tr_b16 v[94:95], v34 offset:1024\n" \
"v_mfma_f32_16x16x32_bf16 a[212:215], v[104:107], v[76:79], a[212:215]\n" \
"ds_read_b64_tr_b16 v[156:157], v34 offset:8192\n" \
"v_mfma_f32_16x16x32_bf16 a[212:215], v[168:171], v[140:143], a[212:215]\n" \
"ds_read_b64_tr_b16 v[158:159], v34 offset:9216\n" \
"v_mfma_f32_16x16x32_bf16 a[216:219], v[108:111], v[76:79], a[216:219]\n" \
"ds_read_b64_tr_b16 v[96:97], v35 offset:0\n" \
"v_mfma_f32_16x16x32_bf16 a[216:219], v[172:175], v[140:143], a[216:219]\n" \
"ds_read_b64_tr_b16 v[98:99], v35 offset:1024\n" \
"v_mfma_f32_16x16x32_bf16 a[220:223], v[112:115], v[76:79], a[220:223]\n" \
"ds_read_b64_tr_b16 v[160:161], v35 offset:8192\n" \
"v_mfma_f32_16x16x32_bf16 a[220:223], v[176:179], v[140:143], a[220:223]\n" \
"ds_read_b128 v[76:79], v28 offset:4096\n" \
"v_mfma_f32_16x16x32_bf16 a[252:255], v[112:115], v[80:83], a[252:255]\n" \
"ds_read_b128 v[140:143], v29 offset:4096\n" \
"v_mfma_f32_16x16x32_bf16 a[252:255], v[176:179], v[144:147], a[252:255]\n" \
"ds_read_b64_tr_b16 v[162:163], v35 offset:9216\n" \
"v_mfma_f32_16x16x32_bf16 a[248:251], v[108:111], v[80:83], a[248:251]\n" \
"v_mfma_f32_16x16x32_bf16 a[248:251], v[172:175], v[144:147], a[248:251]\n" \
"v_mfma_f32_16x16x32_bf16 a[244:247], v[104:107], v[80:83], a[244:247]\n" \
"v_mfma_f32_16x16x32_bf16 a[244:247], v[168:171], v[144:147], a[244:247]\n" \
"v_mfma_f32_16x16x32_bf16 a[240:243], v[100:103], v[80:83], a[240:243]\n" \
"v_mfma_f32_16x16x32_bf16 a[240:243], v[164:167], v[144:147], a[240:243]\n" \
"ds_read_b128 v[80:83], v28 offset:6144\n" \
"ds_read_b128 v[144:147], v29 offset:6144\n" \
"s_waitcnt lgkmcnt(15)\n" \
"v_mfma_f32_16x16x32_bf16 a[0:3], v[84:87], v[52:55], a[0:3]\n" \
"s_add_u32 m0, s31, 0x8c00\n" \
"s_mov_b32 s85, 0x398000\n" \
"v_mfma_f32_16x16x32_bf16 a[0:3], v[148:151], v[116:119], a[0:3]\n" \
"buffer_load_dwordx4 v27, s[68:71], s85 offen lds\n" \
"v_mfma_f32_16x16x32_bf16 a[4:7], v[88:91], v[52:55], a[4:7]\n" \
".fill (L_pb + 16096 - .) / 4, 4, 0xBF800000\n" \
"L_pp4:\n" \
"s_cbranch_execz L_pret\n" \
"ds_read_b64_tr_b16 v[100:101], v36 offset:0\n" \
"s_waitcnt lgkmcnt(15)\n" \
"v_mfma_f32_16x16x32_bf16 a[4:7], v[152:155], v[116:119], a[4:7]\n" \
"s_add_u32 m0, s31, 0xcc00\n" \
"s_mov_b32 s84, 0x398000\n" \
"s_waitcnt lgkmcnt(11)\n" \
"v_mfma_f32_16x16x32_bf16 a[8:11], v[92:95], v[52:55], a[8:11]\n" \
"buffer_load_dwordx4 v27, s[72:75], s84 offen lds\n" \
"s_waitcnt lgkmcnt(9)\n" \
"v_mfma_f32_16x16x32_bf16 a[8:11], v[156:159], v[116:119], a[8:11]\n" \
"ds_read_b64_tr_b16 v[102:103], v36 offset:1024\n" \
"s_waitcnt lgkmcnt(8)\n" \
"v_mfma_f32_16x16x32_bf16 a[12:15], v[96:99], v[52:55], a[12:15]\n" \
"ds_read_b64_tr_b16 v[164:165], v36 offset:8192\n" \
"s_waitcnt lgkmcnt(5)\n" \
"v_mfma_f32_16x16x32_bf16 a[12:15], v[160:163], v[116:119], a[12:15]\n" \
"v_mfma_f32_16x16x32_bf16 a[44:47], v[96:99], v[56:59], a[44:47]\n" \
"v_mfma_f32_16x16x32_bf16 a[44:47], v[160:163], v[120:123], a[44:47]\n" \
"ds_read_b64_tr_b16 v[166:167], v36 offset:9216\n" \
"v_mfma_f32_16x16x32_bf16 a[40:43], v[92:95], v[56:59], a[40:43]\n" \
"v_mfma_f32_16x16x32_bf16 a[40:43], v[156:159], v[120:123], a[40:43]\n" \
"v_mfma_f32_16x16x32_bf16 a[36:39], v[88:91], v[56:59], a[36:39]\n" \
"v_mfma_f32_16x16x32_bf16 a[36:39], v[152:155], v[120:123], a[36:39]\n" \
"ds_read_b64_tr_b16 v[104:105], v37 offset:0\n" \
"v_mfma_f32_16x16x32_bf16 a[32:35], v[84:87], v[56:59], a[32:35]\n" \
"v_mfma_f32_16x16x32_bf16 a[32:35], v[148:151], v[120:123], a[32:35]\n" \
"v_mfma_f32_16x16x32_bf16 a[64:67], v[84:87], v[60:63], a[64:67]\n" \
"ds_read_b64_tr_b16 v[106:107], v37 offset:1024\n" \
"v_mfma_f32_16x16x32_bf16 a[64:67], v[148:151], v[124:127], a[64:67]\n" \
"v_mfma_f32_16x16x32_bf16 a[68:71], v[88:91], v[60:63], a[68:71]\n" \
"v_mfma_f32_16x16x32_bf16 a[68:71], v[152:155], v[124:127], a[68:71]\n" \
"ds_read_b64_tr_b16 v[168:169], v37 offset:8192\n" \
"v_mfma_f32_16x16x32_bf16 a[72:75], v[92:95], v[60:63], a[72:75]\n" \
"v_mfma_f32_16x16x32_bf16 a[72:75], v[156:159], v[124:127], a[72:75]\n" \
"v_mfma_f32_16x16x32_bf16 a[76:79], v[96:99], v[60:63], a[76:79]\n" \
"ds_read_b64_tr_b16 v[170:171], v37 offset:9216\n" \
"v_mfma_f32_16x16x32_bf16 a[76:79], v[160:163], v[124:127], a[76:79]\n" \
"v_mfma_f32_16x16x32_bf16 a[108:111], v[96:99], v[64:67], a[108:111]\n" \
"v_mfma_f32_16x16x32_bf16 a[108:111], v[160:163], v[128:131], a[108:111]\n" \
"v_mfma_f32_16x16x32_bf16 a[104:107], v[92:95], v[64:67], a[104:107]\n" \
"ds_read_b64_tr_b16 v[108:109], v38 offset:0\n" \
"v_mfma_f32_16x16x32_bf16 a[104:107], v[156:159], v[128:131], a[104:107]\n" \
"v_mfma_f32_16x16x32_bf16 a[100:103], v[88:91], v[64:67], a[100:103]\n" \
"v_mfma_f32_16x16x32_bf16 a[100:103], v[152:155], v[128:131], a[100:103]\n" \
"ds_read_b64_tr_b16 v[110:111], v38 offset:1024\n" \
"v_mfma_f32_16x16x32_bf16 a[96:99], v[84:87], v[64:67], a[96:99]\n" \
"v_mfma_f32_16x16x32_bf16 a[96:99], v[148:151], v[128:131], a[96:99]\n" \
"v_mfma_f32_16x16x32_bf16 a[128:131], v[84:87], v[68:71], a[128:131]\n" \
"v_bfe_u32 v44, v20, 2, 5\n" \
"ds_read_b64_tr_b16 v[172:173], v38 offset:8192\n" \
"v_mfma_f32_16x16x32_bf16 a[128:131], v[148:151], v[132:135], a[128:131]\n" \
"buffer_load_dword v44, v44, s[80:83], s38 offen offset:128 sc1\n" \
"v_mfma_f32_16x16x32_bf16 a[132:135], v[88:91], v[68:71], a[132:135]\n" \
"v_mfma_f32_16x16x32_bf16 a[132:135], v[152:155], v[132:135], a[132:135]\n" \
"ds_read_b64_tr_b16 v[174:175], v38 offset:9216\n" \
"v_mfma_f32_16x16x32_bf16 a[136:139], v[92:95], v[68:71], a[136:139]\n" \
"v_mfma_f32_16x16x32_bf16 a[136:139], v[156:159], v[132:135], a[136:139]\n" \
"v_mfma_f32_16x16x32_bf16 a[140:143], v[96:99], v[68:71], a[140:143]\n" \
"v_mfma_f32_16x16x32_bf16 a[140:143], v[160:163], v[132:135], a[140:143]\n" \
"ds_read_b64_tr_b16 v[112:113], v39 offset:0\n" \
"v_mfma_f32_16x16x32_bf16 a[172:175], v[96:99], v[72:75], a[172:175]\n" \
"v_mfma_f32_16x16x32_bf16 a[172:175], v[160:163], v[136:139], a[172:175]\n" \
"v_mfma_f32_16x16x32_bf16 a[168:171], v[92:95], v[72:75], a[168:171]\n" \
"ds_read_b64_tr_b16 v[114:115], v39 offset:1024\n" \
"v_mfma_f32_16x16x32_bf16 a[168:171], v[156:159], v[136:139], a[168:171]\n" \
"v_mfma_f32_16x16x32_bf16 a[164:167], v[88:91], v[72:75], a[164:167]\n" \
"v_mfma_f32_16x16x32_bf16 a[164:167], v[152:155], v[136:139], a[164:167]\n" \
"ds_read_b64_tr_b16 v[176:177], v39 offset:8192\n" \
"v_mfma_f32_16x16x32_bf16 a[160:163], v[84:87], v[72:75], a[160:163]\n" \
"v_mfma_f32_16x16x32_bf16 a[160:163], v[148:151], v[136:139], a[160:163]\n" \
"v_mfma_f32_16x16x32_bf16 a[192:195], v[84:87], v[76:79], a[192:195]\n" \
"ds_read_b64_tr_b16 v[178:179], v39 offset:9216\n" \
"v_mfma_f32_16x16x32_bf16 a[192:195], v[148:151], v[140:143], a[192:195]\n" \
"v_mfma_f32_16x16x32_bf16 a[196:199], v[88:91], v[76:79], a[196:199]\n" \
"v_mfma_f32_16x16x32_bf16 a[196:199], v[152:155], v[140:143], a[196:199]\n" \
"v_mfma_f32_16x16x32_bf16 a[200:203], v[92:95], v[76:79], a[200:203]\n" \
"v_mfma_f32_16x16x32_bf16 a[200:203], v[156:159], v[140:143], a[200:203]\n" \
"v_mfma_f32_16x16x32_bf16 a[204:207], v[96:99], v[76:79], a[204:207]\n" \
"v_mfma_f32_16x16x32_bf16 a[204:207], v[160:163], v[140:143], a[204:207]\n" \
"s_waitcnt lgkmcnt(15)\n" \
"v_mfma_f32_16x16x32_bf16 a[236:239], v[96:99], v[80:83], a[236:239]\n" \
"v_mfma_f32_16x16x32_bf16 a[236:239], v[160:163], v[144:147], a[236:239]\n" \
"v_mfma_f32_16x16x32_bf16 a[232:235], v[92:95], v[80:83], a[232:235]\n" \
"v_mfma_f32_16x16x32_bf16 a[232:235], v[156:159], v[144:147], a[232:235]\n" \
"v_mfma_f32_16x16x32_bf16 a[228:231], v[88:91], v[80:83], a[228:231]\n" \
"v_mfma_f32_16x16x32_bf16 a[228:231], v[152:155], v[144:147], a[228:231]\n" \
"v_mfma_f32_16x16x32_bf16 a[224:227], v[84:87], v[80:83], a[224:227]\n" \
"v_mfma_f32_16x16x32_bf16 a[224:227], v[148:151], v[144:147], a[224:227]\n" \
"s_waitcnt vmcnt(0) lgkmcnt(0)\n" \
"s_barrier\n" \
"v_cmp_ne_u32_e32 vcc, s62, v44\n" \
"s_nop 4\n" \
"s_cbranch_vccnz L_poll60_%=\n" \
"L_pollr60_%=:\n" \
"s_mov_b32 s68, s12\n" \
"s_and_b32 s69, s13, 0xffff\n" \
"s_mov_b32 s70, 0x480000\n" \
"s_mov_b32 s71, 0x20000\n" \
"v_mfma_f32_16x16x32_bf16 a[16:19], v[100:103], v[52:55], a[16:19]\n" \
"s_add_u32 m0, s40, 0x0\n" \
"s_add_u32 s85, s39, 0x0\n" \
"s_nop 0\n" \
"v_mfma_f32_16x16x32_bf16 a[16:19], v[164:167], v[116:119], a[16:19]\n" \
"buffer_load_dwordx4 v20, s[80:83], s85 offen sc1 lds\n" \
"v_mfma_f32_16x16x32_bf16 a[20:23], v[104:107], v[52:55], a[20:23]\n" \
"ds_read_b64_tr_b16 v[84:85], v32 offset:32768\n" \
"v_mfma_f32_16x16x32_bf16 a[20:23], v[168:171], v[116:119], a[20:23]\n" \
"s_add_u32 m0, s40, 0x1000\n" \
"s_add_u32 s84, s39, 0x1000\n" \
"v_mfma_f32_16x16x32_bf16 a[24:27], v[108:111], v[52:55], a[24:27]\n" \
"buffer_load_dwordx4 v20, s[80:83], s84 offen sc1 lds\n" \
"v_mfma_f32_16x16x32_bf16 a[24:27], v[172:175], v[116:119], a[24:27]\n" \
"ds_read_b64_tr_b16 v[86:87], v32 offset:33792\n" \
"v_mfma_f32_16x16x32_bf16 a[28:31], v[112:115], v[52:55], a[28:31]\n" \
"ds_read_b64_tr_b16 v[148:149], v32 offset:40960\n" \
"v_mfma_f32_16x16x32_bf16 a[28:31], v[176:179], v[116:119], a[28:31]\n" \
"ds_read_b128 v[52:55], v28 offset:49152\n" \
"v_mfma_f32_16x16x32_bf16 a[60:63], v[112:115], v[56:59], a[60:63]\n" \
"ds_read_b128 v[116:119], v29 offset:49152\n" \
"v_mfma_f32_16x16x32_bf16 a[60:63], v[176:179], v[120:123], a[60:63]\n" \
"s_add_u32 m0, s40, 0x3000\n" \
"s_mov_b32 s85, 0x0\n" \
"v_mfma_f32_16x16x32_bf16 a[56:59], v[108:111], v[56:59], a[56:59]\n" \
"buffer_load_dwordx4 v21, s[76:79], s85 offen lds\n" \
"v_mfma_f32_16x16x32_bf16 a[56:59], v[172:175], v[120:123], a[56:59]\n" \
"s_add_u32 m0, s30, 0x0\n" \
"s_mov_b32 s84, 0x0\n" \
"s_nop 0\n" \
"v_mfma_f32_16x16x32_bf16 a[52:55], v[104:107], v[56:59], a[52:55]\n" \
"buffer_load_dwordx4 v24, s[76:79], s84 offen lds\n" \
"v_mfma_f32_16x16x32_bf16 a[52:55], v[168:171], v[120:123], a[52:55]\n" \
"ds_read_b64_tr_b16 v[150:151], v32 offset:41984\n" \
"v_mfma_f32_16x16x32_bf16 a[48:51], v[100:103], v[56:59], a[48:51]\n" \
"ds_read_b64_tr_b16 v[88:89], v33 offset:32768\n" \
"v_mfma_f32_16x16x32_bf16 a[48:51], v[164:167], v[120:123], a[48:51]\n" \
"ds_read_b128 v[56:59], v28 offset:51200\n" \
"v_mfma_f32_16x16x32_bf16 a[80:83], v[100:103], v[60:63], a[80:83]\n" \
"ds_read_b128 v[120:123], v29 offset:51200\n" \
"v_mfma_f32_16x16x32_bf16 a[80:83], v[164:167], v[124:127], a[80:83]\n" \
"s_add_u32 m0, s30, 0x400\n" \
"s_mov_b32 s85, 0x2400\n" \
"v_mfma_f32_16x16x32_bf16 a[84:87], v[104:107], v[60:63], a[84:87]\n" \
"buffer_load_dwordx4 v25, s[76:79], s85 offen lds\n" \
"v_mfma_f32_16x16x32_bf16 a[84:87], v[168:171], v[124:127], a[84:87]\n" \
"s_add_u32 m0, s31, 0x0\n" \
"s_mov_b32 s84, 0x0\n" \
"v_mfma_f32_16x16x32_bf16 a[88:91], v[108:111], v[60:63], a[88:91]\n" \
"buffer_load_dwordx4 v26, s[68:71], s84 offen lds\n" \
"v_mfma_f32_16x16x32_bf16 a[88:91], v[172:175], v[124:127], a[88:91]\n" \
"ds_read_b64_tr_b16 v[90:91], v33 offset:33792\n" \
"v_mfma_f32_16x16x32_bf16 a[92:95], v[112:115], v[60:63], a[92:95]\n" \
"ds_read_b64_tr_b16 v[152:153], v33 offset:40960\n" \
"v_mfma_f32_16x16x32_bf16 a[92:95], v[176:179], v[124:127], a[92:95]\n" \
"ds_read_b128 v[60:63], v28 offset:53248\n" \
"v_mfma_f32_16x16x32_bf16 a[124:127], v[112:115], v[64:67], a[124:127]\n" \
"ds_read_b128 v[124:127], v29 offset:53248\n" \
"v_mfma_f32_16x16x32_bf16 a[124:127], v[176:179], v[128:131], a[124:127]\n" \
"s_add_u32 m0, s30, 0x800\n" \
"s_mov_b32 s85, 0x4800\n" \
"v_mfma_f32_16x16x32_bf16 a[120:123], v[108:111], v[64:67], a[120:123]\n" \
"buffer_load_dwordx4 v24, s[76:79], s85 offen lds\n" \
"v_mfma_f32_16x16x32_bf16 a[120:123], v[172:175], v[128:131], a[120:123]\n" \
"s_add_u32 m0, s30, 0xc00\n" \
"s_mov_b32 s84, 0x6c00\n" \
"v_mfma_f32_16x16x32_bf16 a[116:119], v[104:107], v[64:67], a[116:119]\n" \
"buffer_load_dwordx4 v25, s[76:79], s84 offen lds\n" \
"v_mfma_f32_16x16x32_bf16 a[116:119], v[168:171], v[128:131], a[116:119]\n" \
"s_add_u32 m0, s31, 0x400\n" \
"s_mov_b32 s85, 0x8000\n" \
"v_mfma_f32_16x16x32_bf16 a[112:115], v[100:103], v[64:67], a[112:115]\n" \
"buffer_load_dwordx4 v26, s[68:71], s85 offen lds\n" \
"v_mfma_f32_16x16x32_bf16 a[112:115], v[164:167], v[128:131], a[112:115]\n" \
"ds_read_b128 v[64:67], v28 offset:55296\n" \
"v_mfma_f32_16x16x32_bf16 a[144:147], v[100:103], v[68:71], a[144:147]\n" \
"ds_read_b128 v[128:131], v29 offset:55296\n" \
"v_mfma_f32_16x16x32_bf16 a[144:147], v[164:167], v[132:135], a[144:147]\n" \
"s_add_u32 m0, s30, 0x1000\n" \
"s_mov_b32 s84, 0x9000\n" \
"v_mfma_f32_16x16x32_bf16 a[148:151], v[104:107], v[68:71], a[148:151]\n" \
"buffer_load_dwordx4 v24, s[76:79], s84 offen lds\n" \
"v_mfma_f32_16x16x32_bf16 a[148:151], v[168:171], v[132:135], a[148:151]\n" \
"ds_read_b64_tr_b16 v[154:155], v33 offset:41984\n" \
"v_mfma_f32_16x16x32_bf16 a[152:155], v[108:111], v[68:71], a[152:155]\n" \
"s_add_u32 m0, s30, 0x1400\n" \
"s_mov_b32 s85, 0xb400\n" \
"v_mfma_f32_16x16x32_bf16 a[152:155], v[172:175], v[132:135], a[152:155]\n" \
"buffer_load_dwordx4 v25, s[76:79], s85 offen lds\n" \
"v_mfma_f32_16x16x32_bf16 a[156:159], v[112:115], v[68:71], a[156:159]\n" \
"ds_read_b64_tr_b16 v[92:93], v34 offset:32768\n" \
"v_mfma_f32_16x16x32_bf16 a[156:159], v[176:179], v[132:135], a[156:159]\n" \
"ds_read_b128 v[68:71], v28 offset:32768\n" \
"v_mfma_f32_16x16x32_bf16 a[188:191], v[112:115], v[72:75], a[188:191]\n" \
"ds_read_b128 v[132:135], v29 offset:32768\n" \
"v_mfma_f32_16x16x32_bf16 a[188:191], v[176:179], v[136:139], a[188:191]\n" \
"s_add_u32 m0, s31, 0x800\n" \
"s_mov_b32 s84, 0x10000\n" \
"v_mfma_f32_16x16x32_bf16 a[184:187], v[108:111], v[72:75], a[184:187]\n" \
"buffer_load_dwordx4 v27, s[68:71], s84 offen lds\n" \
"v_mfma_f32_16x16x32_bf16 a[184:187], v[172:175], v[136:139], a[184:187]\n" \
"ds_read_b64_tr_b16 v[94:95], v34 offset:33792\n" \
"v_mfma_f32_16x16x32_bf16 a[180:183], v[104:107], v[72:75], a[180:183]\n" \
"s_add_u32 m0, s30, 0x1800\n" \
"s_mov_b32 s85, 0xd800\n" \
"v_mfma_f32_16x16x32_bf16 a[180:183], v[168:171], v[136:139], a[180:183]\n" \
"buffer_load_dwordx4 v24, s[76:79], s85 offen lds\n" \
"v_mfma_f32_16x16x32_bf16 a[176:179], v[100:103], v[72:75], a[176:179]\n" \
"ds_read_b64_tr_b16 v[156:157], v34 offset:40960\n" \
"v_mfma_f32_16x16x32_bf16 a[176:179], v[164:167], v[136:139], a[176:179]\n" \
"ds_read_b128 v[72:75], v28 offset:34816\n" \
"v_mfma_f32_16x16x32_bf16 a[208:211], v[100:103], v[76:79], a[208:211]\n" \
"ds_read_b128 v[136:139], v29 offset:34816\n" \
"v_mfma_f32_16x16x32_bf16 a[208:211], v[164:167], v[140:143], a[208:211]\n" \
"ds_read_b64_tr_b16 v[158:159], v34 offset:41984\n" \
"v_mfma_f32_16x16x32_bf16 a[212:215], v[104:107], v[76:79], a[212:215]\n" \
"ds_read_b64_tr_b16 v[96:97], v35 offset:32768\n" \
"v_mfma_f32_16x16x32_bf16 a[212:215], v[168:171], v[140:143], a[212:215]\n" \
"ds_read_b64_tr_b16 v[98:99], v35 offset:33792\n" \
"v_mfma_f32_16x16x32_bf16 a[216:219], v[108:111], v[76:79], a[216:219]\n" \
"ds_read_b64_tr_b16 v[160:161], v35 offset:40960\n" \
"v_mfma_f32_16x16x32_bf16 a[216:219], v[172:175], v[140:143], a[216:219]\n" \
"ds_read_b64_tr_b16 v[162:163], v35 offset:41984\n" \
"v_mfma_f32_16x16x32_bf16 a[220:223], v[112:115], v[76:79], a[220:223]\n" \
"v_mfma_f32_16x16x32_bf16 a[220:223], v[176:179], v[140:143], a[220:223]\n" \
"ds_read_b128 v[76:79], v28 offset:36864\n" \
"v_mfma_f32_16x16x32_bf16 a[252:255], v[112:115], v[80:83], a[252:255]\n" \
"ds_read_b128 v[140:143], v29 offset:36864\n" \
"v_mfma_f32_16x16x32_bf16 a[252:255], v[176:179], v[144:147], a[252:255]\n" \
"v_mfma_f32_16x16x32_bf16 a[248:251], v[108:111], v[80:83], a[248:251]\n" \
"v_mfma_f32_16x16x32_bf16 a[248:251], v[172:175], v[144:147], a[248:251]\n" \
"v_mfma_f32_16x16x32_bf16 a[244:247], v[104:107], v[80:83], a[244:247]\n" \
"v_mfma_f32_16x16x32_bf16 a[244:247], v[168:171], v[144:147], a[244:247]\n" \
"v_mfma_f32_16x16x32_bf16 a[240:243], v[100:103], v[80:83], a[240:243]\n" \
"v_mfma_f32_16x16x32_bf16 a[240:243], v[164:167], v[144:147], a[240:243]\n" \
"ds_read_b128 v[80:83], v28 offset:38912\n" \
"ds_read_b128 v[144:147], v29 offset:38912\n" \
"s_waitcnt lgkmcnt(15)\n" \
"v_mfma_f32_16x16x32_bf16 a[0:3], v[84:87], v[52:55], a[0:3]\n" \
"s_add_u32 m0, s30, 0x1c00\n" \
"s_mov_b32 s85, 0xfc00\n" \
"v_mfma_f32_16x16x32_bf16 a[0:3], v[148:151], v[116:119], a[0:3]\n" \
"buffer_load_dwordx4 v25, s[76:79], s85 offen lds\n" \
"v_mfma_f32_16x16x32_bf16 a[4:7], v[88:91], v[52:55], a[4:7]\n" \
"ds_read_b64_tr_b16 v[100:101], v36 offset:32768\n" \
"v_mfma_f32_16x16x32_bf16 a[4:7], v[152:155], v[116:119], a[4:7]\n" \
"s_add_u32 m0, s31, 0xc00\n" \
"s_mov_b32 s84, 0x18000\n" \
"s_waitcnt lgkmcnt(13)\n" \
"v_mfma_f32_16x16x32_bf16 a[8:11], v[92:95], v[52:55], a[8:11]\n" \
"buffer_load_dwordx4 v27, s[68:71], s84 offen lds\n" \
"s_waitcnt lgkmcnt(9)\n" \
"v_mfma_f32_16x16x32_bf16 a[8:11], v[156:159], v[116:119], a[8:11]\n" \
"ds_read_b64_tr_b16 v[102:103], v36 offset:33792\n" \
"s_waitcnt lgkmcnt(8)\n" \
"v_mfma_f32_16x16x32_bf16 a[12:15], v[96:99], v[52:55], a[12:15]\n" \
"ds_read_b64_tr_b16 v[164:165], v36 offset:40960\n" \
"v_accvgpr_read_b32 v184, a0\n" \
"s_waitcnt lgkmcnt(7)\n" \
"s_nop 0\n" \
"v_mfma_f32_16x16x32_bf16 a[12:15], v[160:163], v[116:119], a[12:15]\n" \
"v_accvgpr_read_b32 v185, a1\n" \
"v_accvgpr_read_b32 v186, a2\n" \
"v_mfma_f32_16x16x32_bf16 a[44:47], v[96:99], v[56:59], a[44:47]\n" \
"v_accvgpr_read_b32 v187, a3\n" \
"v_accvgpr_read_b32 v180, a4\n" \
"v_mfma_f32_16x16x32_bf16 a[44:47], v[160:163], v[120:123], a[44:47]\n" \
"ds_read_b64_tr_b16 v[166:167], v36 offset:41984\n" \
"v_accvgpr_read_b32 v181, a5\n" \
"v_mfma_f32_16x16x32_bf16 a[40:43], v[92:95], v[56:59], a[40:43]\n" \
"v_accvgpr_read_b32 v182, a6\n" \
"v_accvgpr_read_b32 v183, a7\n" \
"v_mfma_f32_16x16x32_bf16 a[40:43], v[156:159], v[120:123], a[40:43]\n" \
"v_cvt_pk_bf16_f32 v184, v184, v185\n" \
"v_cvt_pk_bf16_f32 v185, v186, v187\n" \
"v_mfma_f32_16x16x32_bf16 a[36:39], v[88:91], v[56:59], a[36:39]\n" \
"v_cvt_pk_bf16_f32 v186, v180, v181\n" \
"v_cvt_pk_bf16_f32 v187, v182, v183\n" \
"v_mfma_f32_16x16x32_bf16 a[36:39], v[152:155], v[120:123], a[36:39]\n" \
"ds_read_b64_tr_b16 v[104:105], v37 offset:32768\n" \
"v_accvgpr_read_b32 v188, a8\n" \
"v_mfma_f32_16x16x32_bf16 a[32:35], v[84:87], v[56:59], a[32:35]\n" \
"v_accvgpr_read_b32 v189, a9\n" \
"v_accvgpr_read_b32 v190, a10\n" \
"v_mfma_f32_16x16x32_bf16 a[32:35], v[148:151], v[120:123], a[32:35]\n" \
"v_accvgpr_read_b32 v191, a11\n" \
"v_accvgpr_read_b32 v180, a12\n" \
"v_mfma_f32_16x16x32_bf16 a[64:67], v[84:87], v[60:63], a[64:67]\n" \
"ds_read_b64_tr_b16 v[106:107], v37 offset:33792\n" \
"v_accvgpr_read_b32 v181, a13\n" \
"v_mfma_f32_16x16x32_bf16 a[64:67], v[148:151], v[124:127], a[64:67]\n" \
"v_accvgpr_read_b32 v182, a14\n" \
"v_accvgpr_read_b32 v183, a15\n" \
"v_mfma_f32_16x16x32_bf16 a[68:71], v[88:91], v[60:63], a[68:71]\n" \
"v_lshlrev_b32_e32 v45, 3, v20\n" \
"v_add_u32_e32 v45, s31, v45\n" \
"v_mfma_f32_16x16x32_bf16 a[68:71], v[152:155], v[124:127], a[68:71]\n" \
"s_getpc_b64 s[86:87]\n" \
"s_sub_u32 s86, s86, 0xf800\n" \
"s_subb_u32 s87, s87, 0\n" \
"s_mov_b32 m0, s32\n" \
"s_nop 0\n" \
"v_mfma_f32_16x16x32_bf16 a[72:75], v[92:95], v[60:63], a[72:75]\n" \
"global_load_lds_dword v45, s[86:87]\n" \
"v_mfma_f32_16x16x32_bf16 a[72:75], v[156:159], v[124:127], a[72:75]\n" \
"ds_read_b64_tr_b16 v[168:169], v37 offset:40960\n" \
"v_cvt_pk_bf16_f32 v188, v188, v189\n" \
"v_mfma_f32_16x16x32_bf16 a[76:79], v[96:99], v[60:63], a[76:79]\n" \
"ds_read_b64_tr_b16 v[170:171], v37 offset:41984\n" \
"v_cvt_pk_bf16_f32 v189, v190, v191\n" \
"v_mfma_f32_16x16x32_bf16 a[76:79], v[160:163], v[124:127], a[76:79]\n" \
"v_cvt_pk_bf16_f32 v190, v180, v181\n" \
"v_cvt_pk_bf16_f32 v191, v182, v183\n" \
"v_mfma_f32_16x16x32_bf16 a[108:111], v[96:99], v[64:67], a[108:111]\n" \
"v_accvgpr_read_b32 v192, a40\n" \
"v_accvgpr_read_b32 v193, a41\n" \
"v_mfma_f32_16x16x32_bf16 a[108:111], v[160:163], v[128:131], a[108:111]\n" \
"v_accvgpr_read_b32 v194, a42\n" \
"v_accvgpr_read_b32 v195, a43\n" \
"v_mfma_f32_16x16x32_bf16 a[104:107], v[92:95], v[64:67], a[104:107]\n" \
"ds_read_b64_tr_b16 v[108:109], v38 offset:32768\n" \
"v_accvgpr_read_b32 v180, a44\n" \
"v_mfma_f32_16x16x32_bf16 a[104:107], v[156:159], v[128:131], a[104:107]\n" \
"v_accvgpr_read_b32 v181, a45\n" \
"v_accvgpr_read_b32 v182, a46\n" \
"v_mfma_f32_16x16x32_bf16 a[100:103], v[88:91], v[64:67], a[100:103]\n" \
"v_accvgpr_read_b32 v183, a47\n" \
"v_cvt_pk_bf16_f32 v192, v192, v193\n" \
"v_mfma_f32_16x16x32_bf16 a[100:103], v[152:155], v[128:131], a[100:103]\n" \
"ds_read_b64_tr_b16 v[110:111], v38 offset:33792\n" \
"v_cvt_pk_bf16_f32 v193, v194, v195\n" \
"v_mfma_f32_16x16x32_bf16 a[96:99], v[84:87], v[64:67], a[96:99]\n" \
"v_cvt_pk_bf16_f32 v194, v180, v181\n" \
"v_cvt_pk_bf16_f32 v195, v182, v183\n" \
"v_mfma_f32_16x16x32_bf16 a[96:99], v[148:151], v[128:131], a[96:99]\n" \
"v_accvgpr_read_b32 v196, a32\n" \
"v_accvgpr_read_b32 v197, a33\n" \
"v_mfma_f32_16x16x32_bf16 a[128:131], v[84:87], v[68:71], a[128:131]\n" \
"ds_read_b64_tr_b16 v[172:173], v38 offset:40960\n" \
"v_accvgpr_read_b32 v198, a34\n" \
"v_mfma_f32_16x16x32_bf16 a[128:131], v[148:151], v[132:135], a[128:131]\n" \
"v_accvgpr_read_b32 v199, a35\n" \
"v_accvgpr_read_b32 v180, a36\n" \
"v_mfma_f32_16x16x32_bf16 a[132:135], v[88:91], v[68:71], a[132:135]\n" \
"v_accvgpr_read_b32 v181, a37\n" \
"v_accvgpr_read_b32 v182, a38\n" \
"v_mfma_f32_16x16x32_bf16 a[132:135], v[152:155], v[132:135], a[132:135]\n" \
"ds_read_b64_tr_b16 v[174:175], v38 offset:41984\n" \
"v_accvgpr_read_b32 v183, a39\n" \
"v_mfma_f32_16x16x32_bf16 a[136:139], v[92:95], v[68:71], a[136:139]\n" \
"v_cvt_pk_bf16_f32 v196, v196, v197\n" \
"v_cvt_pk_bf16_f32 v197, v198, v199\n" \
"v_mfma_f32_16x16x32_bf16 a[136:139], v[156:159], v[132:135], a[136:139]\n" \
"v_cvt_pk_bf16_f32 v198, v180, v181\n" \
"v_cvt_pk_bf16_f32 v199, v182, v183\n" \
"v_mfma_f32_16x16x32_bf16 a[140:143], v[96:99], v[68:71], a[140:143]\n" \
"v_accvgpr_read_b32 v200, a64\n" \
"v_accvgpr_read_b32 v201, a65\n" \
"v_mfma_f32_16x16x32_bf16 a[140:143], v[160:163], v[132:135], a[140:143]\n" \
"ds_read_b64_tr_b16 v[112:113], v39 offset:32768\n" \
"v_accvgpr_read_b32 v202, a66\n" \
"v_mfma_f32_16x16x32_bf16 a[172:175], v[96:99], v[72:75], a[172:175]\n" \
"s_add_u32 s89, s41, 0x0\n" \
"s_nop 0\n" \
"buffer_store_dwordx4 v[184:187], v20, s[80:83], s89 offen offset:0 sc1\n" \
"v_mfma_f32_16x16x32_bf16 a[172:175], v[160:163], v[136:139], a[172:175]\n" \
"v_accvgpr_read_b32 v203, a67\n" \
"v_accvgpr_read_b32 v180, a68\n" \
"v_mfma_f32_16x16x32_bf16 a[168:171], v[92:95], v[72:75], a[168:171]\n" \
"ds_read_b64_tr_b16 v[114:115], v39 offset:33792\n" \
"v_accvgpr_read_b32 v181, a69\n" \
"v_mfma_f32_16x16x32_bf16 a[168:171], v[156:159], v[136:139], a[168:171]\n" \
"v_accvgpr_read_b32 v182, a70\n" \
"v_accvgpr_read_b32 v183, a71\n" \
"v_mfma_f32_16x16x32_bf16 a[164:167], v[88:91], v[72:75], a[164:167]\n" \
"v_cvt_pk_bf16_f32 v200, v200, v201\n" \
"v_cvt_pk_bf16_f32 v201, v202, v203\n" \
"v_mfma_f32_16x16x32_bf16 a[164:167], v[152:155], v[136:139], a[164:167]\n" \
"ds_read_b64_tr_b16 v[176:177], v39 offset:40960\n" \
"v_cvt_pk_bf16_f32 v202, v180, v181\n" \
"v_mfma_f32_16x16x32_bf16 a[160:163], v[84:87], v[72:75], a[160:163]\n" \
"s_add_u32 s88, s41, 0x0\n" \
"buffer_store_dwordx4 v[188:191], v20, s[80:83], s88 offen offset:1024 sc1\n" \
"v_mfma_f32_16x16x32_bf16 a[160:163], v[148:151], v[136:139], a[160:163]\n" \
"v_cvt_pk_bf16_f32 v203, v182, v183\n" \
"v_accvgpr_read_b32 v204, a72\n" \
"s_waitcnt lgkmcnt(15)\n" \
"v_mfma_f32_16x16x32_bf16 a[192:195], v[84:87], v[76:79], a[192:195]\n" \
"ds_read_b64_tr_b16 v[178:179], v39 offset:41984\n" \
"v_accvgpr_read_b32 v205, a73\n" \
"v_mfma_f32_16x16x32_bf16 a[192:195], v[148:151], v[140:143], a[192:195]\n" \
"v_accvgpr_read_b32 v206, a74\n" \
"v_accvgpr_read_b32 v207, a75\n" \
"v_mfma_f32_16x16x32_bf16 a[196:199], v[88:91], v[76:79], a[196:199]\n" \
"v_accvgpr_read_b32 v180, a76\n" \
"v_accvgpr_read_b32 v181, a77\n" \
"v_mfma_f32_16x16x32_bf16 a[196:199], v[152:155], v[140:143], a[196:199]\n" \
"v_accvgpr_read_b32 v182, a78\n" \
"v_accvgpr_read_b32 v183, a79\n" \
"v_mfma_f32_16x16x32_bf16 a[200:203], v[92:95], v[76:79], a[200:203]\n" \
"s_add_u32 s89, s41, 0x1000\n" \
"buffer_store_dwordx4 v[192:195], v20, s[80:83], s89 offen offset:1024 sc1\n" \
"v_mfma_f32_16x16x32_bf16 a[200:203], v[156:159], v[140:143], a[200:203]\n" \
"v_cvt_pk_bf16_f32 v204, v204, v205\n" \
"v_cvt_pk_bf16_f32 v205, v206, v207\n" \
"v_mfma_f32_16x16x32_bf16 a[204:207], v[96:99], v[76:79], a[204:207]\n" \
"v_cvt_pk_bf16_f32 v206, v180, v181\n" \
"v_cvt_pk_bf16_f32 v207, v182, v183\n" \
"v_mfma_f32_16x16x32_bf16 a[204:207], v[160:163], v[140:143], a[204:207]\n" \
"v_accvgpr_read_b32 v208, a104\n" \
"v_accvgpr_read_b32 v209, a105\n" \
"v_mfma_f32_16x16x32_bf16 a[236:239], v[96:99], v[80:83], a[236:239]\n" \
"v_accvgpr_read_b32 v210, a106\n" \
"v_accvgpr_read_b32 v211, a107\n" \
"v_mfma_f32_16x16x32_bf16 a[236:239], v[160:163], v[144:147], a[236:239]\n" \
"v_accvgpr_read_b32 v180, a108\n" \
"v_accvgpr_read_b32 v181, a109\n" \
"v_mfma_f32_16x16x32_bf16 a[232:235], v[92:95], v[80:83], a[232:235]\n" \
"s_add_u32 s88, s41, 0x1000\n" \
"buffer_store_dwordx4 v[196:199], v20, s[80:83], s88 offen offset:0 sc1\n" \
"v_mfma_f32_16x16x32_bf16 a[232:235], v[156:159], v[144:147], a[232:235]\n" \
"v_accvgpr_read_b32 v182, a110\n" \
"v_accvgpr_read_b32 v183, a111\n" \
"v_mfma_f32_16x16x32_bf16 a[228:231], v[88:91], v[80:83], a[228:231]\n" \
"v_cvt_pk_bf16_f32 v208, v208, v209\n" \
"v_cvt_pk_bf16_f32 v209, v210, v211\n" \
"v_mfma_f32_16x16x32_bf16 a[228:231], v[152:155], v[144:147], a[228:231]\n" \
"v_cvt_pk_bf16_f32 v210, v180, v181\n" \
"v_cvt_pk_bf16_f32 v211, v182, v183\n" \
"v_mfma_f32_16x16x32_bf16 a[224:227], v[84:87], v[80:83], a[224:227]\n" \
"v_accvgpr_read_b32 v212, a96\n" \
"v_accvgpr_read_b32 v213, a97\n" \
"v_mfma_f32_16x16x32_bf16 a[224:227], v[148:151], v[144:147], a[224:227]\n" \
"v_accvgpr_read_b32 v214, a98\n" \
"v_accvgpr_read_b32 v215, a99\n" \
"s_waitcnt vmcnt(5) lgkmcnt(0)\n" \
"s_barrier\n" \
"v_mov_b32_e32 v36, v32\n" \
"v_xor_b32_e32 v32, 0x0, v48\n" \
"v_mov_b32_e32 v37, v33\n" \
"v_xor_b32_e32 v33, 0x20, v48\n" \
"v_mov_b32_e32 v38, v34\n" \
"v_xor_b32_e32 v34, 0x40, v48\n" \
"v_mov_b32_e64 v39, v35\n" \
"v_xor_b32_e32 v35, 0x60, v48\n" \
"v_add_u32_e32 v22, s44, v28\n" \
"v_add_u32_e32 v28, s43, v28\n" \
"v_add_u32_e32 v23, s44, v29\n" \
"v_add_u32_e32 v29, s43, v29\n" \
"v_mfma_f32_16x16x32_bf16 a[16:19], v[100:103], v[52:55], a[16:19]\n" \
"s_add_u32 m0, s40, 0x8000\n" \
"s_add_u32 s85, s39, 0x2000\n" \
"v_mfma_f32_16x16x32_bf16 a[16:19], v[164:167], v[116:119], a[16:19]\n" \
"buffer_load_dwordx4 v20, s[80:83], s85 offen sc1 lds\n" \
"v_mfma_f32_16x16x32_bf16 a[20:23], v[104:107], v[52:55], a[20:23]\n" \
"ds_read_b64_tr_b16 v[84:85], v32 offset:0\n" \
"v_accvgpr_read_b32 v180, a100\n" \
"v_mfma_f32_16x16x32_bf16 a[20:23], v[168:171], v[116:119], a[20:23]\n" \
"s_add_u32 m0, s40, 0x9000\n" \
"s_add_u32 s84, s39, 0x3000\n" \
"v_mfma_f32_16x16x32_bf16 a[24:27], v[108:111], v[52:55], a[24:27]\n" \
"buffer_load_dwordx4 v20, s[80:83], s84 offen sc1 lds\n" \
"v_mfma_f32_16x16x32_bf16 a[24:27], v[172:175], v[116:119], a[24:27]\n" \
"ds_read_b64_tr_b16 v[86:87], v32 offset:512\n" \
"v_accvgpr_read_b32 v181, a101\n" \
"v_mfma_f32_16x16x32_bf16 a[28:31], v[112:115], v[52:55], a[28:31]\n" \
"ds_read_b64_tr_b16 v[148:149], v32 offset:4096\n" \
"v_accvgpr_read_b32 v182, a102\n" \
"v_mfma_f32_16x16x32_bf16 a[28:31], v[176:179], v[116:119], a[28:31]\n" \
"ds_read_b128 v[52:55], v28 offset:0\n" \
"v_accvgpr_read_b32 v183, a103\n" \
"v_mfma_f32_16x16x32_bf16 a[60:63], v[112:115], v[56:59], a[60:63]\n" \
"ds_read_b128 v[116:119], v29 offset:0\n" \
"v_cvt_pk_bf16_f32 v212, v212, v213\n" \
"v_mfma_f32_16x16x32_bf16 a[60:63], v[176:179], v[120:123], a[60:63]\n" \
"s_add_u32 m0, s40, 0xb000\n" \
"s_mov_b32 s85, 0x80\n" \
"v_mfma_f32_16x16x32_bf16 a[56:59], v[108:111], v[56:59], a[56:59]\n" \
"buffer_load_dwordx4 v21, s[76:79], s85 offen lds\n" \
"v_mfma_f32_16x16x32_bf16 a[56:59], v[172:175], v[120:123], a[56:59]\n" \
"s_add_u32 m0, s30, 0x8000\n" \
"s_mov_b32 s84, 0x80\n" \
"v_mfma_f32_16x16x32_bf16 a[52:55], v[104:107], v[56:59], a[52:55]\n" \
"buffer_load_dwordx4 v24, s[76:79], s84 offen lds\n" \
"v_mfma_f32_16x16x32_bf16 a[52:55], v[168:171], v[120:123], a[52:55]\n" \
"ds_read_b64_tr_b16 v[150:151], v32 offset:4608\n" \
"v_cvt_pk_bf16_f32 v213, v214, v215\n" \
"v_mfma_f32_16x16x32_bf16 a[48:51], v[100:103], v[56:59], a[48:51]\n" \
"ds_read_b64_tr_b16 v[88:89], v33 offset:0\n" \
"v_cvt_pk_bf16_f32 v214, v180, v181\n" \
"v_mfma_f32_16x16x32_bf16 a[48:51], v[164:167], v[120:123], a[48:51]\n" \
"ds_read_b128 v[56:59], v28 offset:2048\n" \
"v_cvt_pk_bf16_f32 v215, v182, v183\n" \
"v_mfma_f32_16x16x32_bf16 a[80:83], v[100:103], v[60:63], a[80:83]\n" \
"ds_read_b128 v[120:123], v29 offset:2048\n" \
"v_accvgpr_read_b32 v216, a128\n" \
"v_mfma_f32_16x16x32_bf16 a[80:83], v[164:167], v[124:127], a[80:83]\n" \
"s_add_u32 m0, s30, 0x8400\n" \
"s_mov_b32 s85, 0x2480\n" \
"v_mfma_f32_16x16x32_bf16 a[84:87], v[104:107], v[60:63], a[84:87]\n" \
"buffer_load_dwordx4 v25, s[76:79], s85 offen lds\n" \
"v_mfma_f32_16x16x32_bf16 a[84:87], v[168:171], v[124:127], a[84:87]\n" \
"s_add_u32 m0, s31, 0x8000\n" \
"s_mov_b32 s84, 0x80000\n" \
"v_mfma_f32_16x16x32_bf16 a[88:91], v[108:111], v[60:63], a[88:91]\n" \
"buffer_load_dwordx4 v26, s[68:71], s84 offen lds\n" \
"v_mfma_f32_16x16x32_bf16 a[88:91], v[172:175], v[124:127], a[88:91]\n" \
"ds_read_b64_tr_b16 v[90:91], v33 offset:512\n" \
"v_accvgpr_read_b32 v217, a129\n" \
"v_mfma_f32_16x16x32_bf16 a[92:95], v[112:115], v[60:63], a[92:95]\n" \
"ds_read_b64_tr_b16 v[152:153], v33 offset:4096\n" \
"v_accvgpr_read_b32 v218, a130\n" \
"v_mfma_f32_16x16x32_bf16 a[92:95], v[176:179], v[124:127], a[92:95]\n" \
"ds_read_b128 v[60:63], v28 offset:4096\n" \
"v_accvgpr_read_b32 v219, a131\n" \
"v_mfma_f32_16x16x32_bf16 a[124:127], v[112:115], v[64:67], a[124:127]\n" \
"ds_read_b128 v[124:127], v29 offset:4096\n" \
"v_accvgpr_read_b32 v180, a132\n" \
"v_mfma_f32_16x16x32_bf16 a[124:127], v[176:179], v[128:131], a[124:127]\n" \
"s_add_u32 m0, s30, 0x8800\n" \
"s_mov_b32 s85, 0x4880\n" \
"v_mfma_f32_16x16x32_bf16 a[120:123], v[108:111], v[64:67], a[120:123]\n" \
"buffer_load_dwordx4 v24, s[76:79], s85 offen lds\n" \
"v_mfma_f32_16x16x32_bf16 a[120:123], v[172:175], v[128:131], a[120:123]\n" \
"s_add_u32 m0, s30, 0x8c00\n" \
"s_mov_b32 s84, 0x6c80\n" \
"v_mfma_f32_16x16x32_bf16 a[116:119], v[104:107], v[64:67], a[116:119]\n" \
"buffer_load_dwordx4 v25, s[76:79], s84 offen lds\n" \
"v_mfma_f32_16x16x32_bf16 a[116:119], v[168:171], v[128:131], a[116:119]\n" \
"s_add_u32 m0, s31, 0x8400\n" \
"s_mov_b32 s85, 0x88000\n" \
"v_mfma_f32_16x16x32_bf16 a[112:115], v[100:103], v[64:67], a[112:115]\n" \
"buffer_load_dwordx4 v26, s[68:71], s85 offen lds\n" \
"v_mfma_f32_16x16x32_bf16 a[112:115], v[164:167], v[128:131], a[112:115]\n" \
"ds_read_b128 v[64:67], v28 offset:6144\n" \
"v_accvgpr_read_b32 v181, a133\n" \
"v_mfma_f32_16x16x32_bf16 a[144:147], v[100:103], v[68:71], a[144:147]\n" \
"ds_read_b128 v[128:131], v29 offset:6144\n" \
"v_accvgpr_read_b32 v182, a134\n" \
".fill (L_pb + 20192 - .) / 4, 4, 0xBF800000\n" \
"L_pp5:\n" \
"s_cbranch_execz L_pret\n" \
"s_nop 0\n" \
"v_mfma_f32_16x16x32_bf16 a[144:147], v[164:167], v[132:135], a[144:147]\n" \
"s_add_u32 m0, s30, 0x9000\n" \
"s_mov_b32 s84, 0x9080\n" \
"v_mfma_f32_16x16x32_bf16 a[148:151], v[104:107], v[68:71], a[148:151]\n" \
"buffer_load_dwordx4 v24, s[76:79], s84 offen lds\n" \
"v_mfma_f32_16x16x32_bf16 a[148:151], v[168:171], v[132:135], a[148:151]\n" \
"ds_read_b64_tr_b16 v[154:155], v33 offset:4608\n" \
"v_accvgpr_read_b32 v183, a135\n" \
"v_mfma_f32_16x16x32_bf16 a[152:155], v[108:111], v[68:71], a[152:155]\n" \
"s_add_u32 m0, s30, 0x9400\n" \
"s_mov_b32 s85, 0xb480\n" \
"v_mfma_f32_16x16x32_bf16 a[152:155], v[172:175], v[132:135], a[152:155]\n" \
"buffer_load_dwordx4 v25, s[76:79], s85 offen lds\n" \
"v_mfma_f32_16x16x32_bf16 a[156:159], v[112:115], v[68:71], a[156:159]\n" \
"ds_read_b64_tr_b16 v[96:97], v35 offset:0\n" \
"v_cvt_pk_bf16_f32 v216, v216, v217\n" \
"v_mfma_f32_16x16x32_bf16 a[156:159], v[176:179], v[132:135], a[156:159]\n" \
"ds_read_b128 v[68:71], v28 offset:8192\n" \
"v_cvt_pk_bf16_f32 v217, v218, v219\n" \
"v_mfma_f32_16x16x32_bf16 a[188:191], v[112:115], v[72:75], a[188:191]\n" \
"ds_read_b128 v[132:135], v29 offset:8192\n" \
"v_cvt_pk_bf16_f32 v218, v180, v181\n" \
"v_mfma_f32_16x16x32_bf16 a[188:191], v[176:179], v[136:139], a[188:191]\n" \
"s_add_u32 m0, s31, 0x8800\n" \
"s_mov_b32 s84, 0x90000\n" \
"v_mfma_f32_16x16x32_bf16 a[184:187], v[108:111], v[72:75], a[184:187]\n" \
"buffer_load_dwordx4 v27, s[68:71], s84 offen lds\n" \
"v_mfma_f32_16x16x32_bf16 a[184:187], v[172:175], v[136:139], a[184:187]\n" \
"ds_read_b64_tr_b16 v[98:99], v35 offset:512\n" \
"v_cvt_pk_bf16_f32 v219, v182, v183\n" \
"v_mfma_f32_16x16x32_bf16 a[180:183], v[104:107], v[72:75], a[180:183]\n" \
"s_add_u32 m0, s30, 0x9800\n" \
"s_mov_b32 s85, 0xd880\n" \
"v_mfma_f32_16x16x32_bf16 a[180:183], v[168:171], v[136:139], a[180:183]\n" \
"buffer_load_dwordx4 v24, s[76:79], s85 offen lds\n" \
"v_mfma_f32_16x16x32_bf16 a[176:179], v[100:103], v[72:75], a[176:179]\n" \
"ds_read_b64_tr_b16 v[160:161], v35 offset:4096\n" \
"v_accvgpr_read_b32 v220, a136\n" \
"v_mfma_f32_16x16x32_bf16 a[176:179], v[164:167], v[136:139], a[176:179]\n" \
"ds_read_b128 v[72:75], v28 offset:10240\n" \
"v_accvgpr_read_b32 v221, a137\n" \
"v_mfma_f32_16x16x32_bf16 a[208:211], v[100:103], v[76:79], a[208:211]\n" \
"ds_read_b128 v[136:139], v29 offset:10240\n" \
"v_accvgpr_read_b32 v222, a138\n" \
"v_mfma_f32_16x16x32_bf16 a[208:211], v[164:167], v[140:143], a[208:211]\n" \
"ds_read_b64_tr_b16 v[162:163], v35 offset:4608\n" \
"v_accvgpr_read_b32 v223, a139\n" \
"v_mfma_f32_16x16x32_bf16 a[212:215], v[104:107], v[76:79], a[212:215]\n" \
"ds_read_b64_tr_b16 v[92:93], v34 offset:0\n" \
"v_accvgpr_read_b32 v180, a140\n" \
"v_mfma_f32_16x16x32_bf16 a[212:215], v[168:171], v[140:143], a[212:215]\n" \
"ds_read_b64_tr_b16 v[94:95], v34 offset:512\n" \
"v_accvgpr_read_b32 v181, a141\n" \
"v_mfma_f32_16x16x32_bf16 a[216:219], v[108:111], v[76:79], a[216:219]\n" \
"ds_read_b64_tr_b16 v[156:157], v34 offset:4096\n" \
"v_accvgpr_read_b32 v182, a142\n" \
"v_mfma_f32_16x16x32_bf16 a[216:219], v[172:175], v[140:143], a[216:219]\n" \
"ds_read_b64_tr_b16 v[158:159], v34 offset:4608\n" \
"v_accvgpr_read_b32 v183, a143\n" \
"v_mfma_f32_16x16x32_bf16 a[220:223], v[112:115], v[76:79], a[220:223]\n" \
"ds_read_b128 v[248:251], v22 offset:0\n" \
"v_cvt_pk_bf16_f32 v220, v220, v221\n" \
"v_mfma_f32_16x16x32_bf16 a[220:223], v[176:179], v[140:143], a[220:223]\n" \
"ds_read_b128 v[76:79], v28 offset:12288\n" \
"v_cvt_pk_bf16_f32 v221, v222, v223\n" \
"v_mfma_f32_16x16x32_bf16 a[252:255], v[112:115], v[80:83], a[252:255]\n" \
"ds_read_b128 v[140:143], v29 offset:12288\n" \
"v_cvt_pk_bf16_f32 v222, v180, v181\n" \
"v_mfma_f32_16x16x32_bf16 a[252:255], v[176:179], v[144:147], a[252:255]\n" \
"ds_read_b128 v[48:51], v23 offset:0\n" \
"v_cvt_pk_bf16_f32 v223, v182, v183\n" \
"v_mfma_f32_16x16x32_bf16 a[248:251], v[108:111], v[80:83], a[248:251]\n" \
"v_accvgpr_read_b32 v224, a168\n" \
"v_accvgpr_read_b32 v225, a169\n" \
"v_mfma_f32_16x16x32_bf16 a[248:251], v[172:175], v[144:147], a[248:251]\n" \
"v_accvgpr_read_b32 v226, a170\n" \
"v_accvgpr_read_b32 v227, a171\n" \
"v_mfma_f32_16x16x32_bf16 a[244:247], v[104:107], v[80:83], a[244:247]\n" \
"v_accvgpr_read_b32 v180, a172\n" \
"v_accvgpr_read_b32 v181, a173\n" \
"v_mfma_f32_16x16x32_bf16 a[244:247], v[168:171], v[144:147], a[244:247]\n" \
"v_accvgpr_read_b32 v182, a174\n" \
"v_accvgpr_read_b32 v183, a175\n" \
"v_mfma_f32_16x16x32_bf16 a[240:243], v[100:103], v[80:83], a[240:243]\n" \
"v_cvt_pk_bf16_f32 v224, v224, v225\n" \
"v_cvt_pk_bf16_f32 v225, v226, v227\n" \
"v_mfma_f32_16x16x32_bf16 a[240:243], v[164:167], v[144:147], a[240:243]\n" \
"ds_read_b128 v[80:83], v28 offset:14336\n" \
"ds_read_b128 v[144:147], v29 offset:14336\n" \
"s_waitcnt lgkmcnt(15)\n" \
"s_nop 0\n" \
"v_mfma_f32_16x16x32_bf16 a[0:3], v[84:87], v[52:55], 0\n" \
"s_add_u32 m0, s30, 0x9c00\n" \
"s_mov_b32 s85, 0xfc80\n" \
"v_cvt_pk_bf16_f32 v226, v180, v181\n" \
"v_mfma_f32_16x16x32_bf16 a[0:3], v[148:151], v[116:119], a[0:3]\n" \
"buffer_load_dwordx4 v25, s[76:79], s85 offen lds\n" \
"v_cvt_pk_bf16_f32 v227, v182, v183\n" \
"v_mfma_f32_16x16x32_bf16 a[4:7], v[88:91], v[52:55], 0\n" \
"ds_read_b64_tr_b16 v[100:101], v36 offset:0\n" \
"v_accvgpr_read_b32 v228, a160\n" \
"v_mfma_f32_16x16x32_bf16 a[4:7], v[152:155], v[116:119], a[4:7]\n" \
"s_add_u32 m0, s31, 0x8c00\n" \
"s_mov_b32 s84, 0x98000\n" \
"v_accvgpr_read_b32 v229, a161\n" \
"s_waitcnt lgkmcnt(15)\n" \
"v_mfma_f32_16x16x32_bf16 a[36:39], v[96:99], v[52:55], 0\n" \
"buffer_load_dwordx4 v27, s[68:71], s84 offen lds\n" \
"v_accvgpr_read_b32 v230, a162\n" \
"s_waitcnt lgkmcnt(11)\n" \
"v_mfma_f32_16x16x32_bf16 a[36:39], v[160:163], v[116:119], a[36:39]\n" \
"ds_read_b64_tr_b16 v[102:103], v36 offset:1024\n" \
"v_accvgpr_read_b32 v231, a163\n" \
"s_waitcnt lgkmcnt(10)\n" \
"v_mfma_f32_16x16x32_bf16 a[32:35], v[52:55], v[92:95], 0\n" \
"ds_read_b64_tr_b16 v[164:165], v36 offset:8192\n" \
"v_accvgpr_read_b32 v180, a164\n" \
"s_waitcnt lgkmcnt(9)\n" \
"v_mfma_f32_16x16x32_bf16 a[32:35], v[116:119], v[156:159], a[32:35]\n" \
"ds_read_b64_tr_b16 v[166:167], v36 offset:9216\n" \
"v_accvgpr_read_b32 v181, a165\n" \
"v_mfma_f32_16x16x32_bf16 a[96:99], v[56:59], v[92:95], 0\n" \
"ds_read_b64_tr_b16 v[104:105], v37 offset:0\n" \
"v_accvgpr_read_b32 v182, a166\n" \
"v_mfma_f32_16x16x32_bf16 a[96:99], v[120:123], v[156:159], a[96:99]\n" \
"ds_read_b64_tr_b16 v[106:107], v37 offset:1024\n" \
"v_accvgpr_read_b32 v183, a167\n" \
"v_mfma_f32_16x16x32_bf16 a[100:103], v[96:99], v[56:59], 0\n" \
"ds_read_b64_tr_b16 v[168:169], v37 offset:8192\n" \
"v_cvt_pk_bf16_f32 v228, v228, v229\n" \
"v_mfma_f32_16x16x32_bf16 a[100:103], v[160:163], v[120:123], a[100:103]\n" \
"s_add_u32 s89, s41, 0x2000\n" \
"buffer_store_dwordx4 v[200:203], v20, s[80:83], s89 offen offset:0 sc1\n" \
"v_mfma_f32_16x16x32_bf16 a[68:71], v[88:91], v[56:59], 0\n" \
"ds_read_b64_tr_b16 v[170:171], v37 offset:9216\n" \
"v_cvt_pk_bf16_f32 v229, v230, v231\n" \
"v_mfma_f32_16x16x32_bf16 a[68:71], v[152:155], v[120:123], a[68:71]\n" \
"ds_read_b64_tr_b16 v[108:109], v38 offset:0\n" \
"v_cvt_pk_bf16_f32 v230, v180, v181\n" \
"v_mfma_f32_16x16x32_bf16 a[64:67], v[84:87], v[56:59], 0\n" \
"ds_read_b64_tr_b16 v[110:111], v38 offset:1024\n" \
"v_cvt_pk_bf16_f32 v231, v182, v183\n" \
"v_mfma_f32_16x16x32_bf16 a[64:67], v[148:151], v[120:123], a[64:67]\n" \
"v_accvgpr_read_b32 v232, a192\n" \
"v_accvgpr_read_b32 v233, a193\n" \
"v_accvgpr_read_b32 v234, a194\n" \
"v_mfma_f32_16x16x32_bf16 a[128:131], v[84:87], v[60:63], 0\n" \
"s_add_u32 s88, s41, 0x2000\n" \
"buffer_store_dwordx4 v[204:207], v20, s[80:83], s88 offen offset:1024 sc1\n" \
"v_mfma_f32_16x16x32_bf16 a[128:131], v[148:151], v[124:127], a[128:131]\n" \
"ds_read_b64_tr_b16 v[172:173], v38 offset:8192\n" \
"v_accvgpr_read_b32 v235, a195\n" \
"v_mfma_f32_16x16x32_bf16 a[132:135], v[88:91], v[60:63], 0\n" \
"ds_read_b64_tr_b16 v[174:175], v38 offset:9216\n" \
"v_accvgpr_read_b32 v180, a196\n" \
"v_mfma_f32_16x16x32_bf16 a[132:135], v[152:155], v[124:127], a[132:135]\n" \
"ds_read_b64_tr_b16 v[112:113], v39 offset:0\n" \
"v_accvgpr_read_b32 v181, a197\n" \
"v_mfma_f32_16x16x32_bf16 a[164:167], v[96:99], v[60:63], 0\n" \
"v_accvgpr_read_b32 v182, a198\n" \
"v_accvgpr_read_b32 v183, a199\n" \
"v_cvt_pk_bf16_f32 v232, v232, v233\n" \
"v_mfma_f32_16x16x32_bf16 a[164:167], v[160:163], v[124:127], a[164:167]\n" \
"ds_read_b64_tr_b16 v[114:115], v39 offset:1024\n" \
"v_cvt_pk_bf16_f32 v233, v234, v235\n" \
"v_mfma_f32_16x16x32_bf16 a[160:163], v[60:63], v[92:95], 0\n" \
"s_add_u32 s89, s41, 0x3000\n" \
"buffer_store_dwordx4 v[208:211], v20, s[80:83], s89 offen offset:1024 sc1\n" \
"v_mfma_f32_16x16x32_bf16 a[160:163], v[124:127], v[156:159], a[160:163]\n" \
"ds_read_b64_tr_b16 v[176:177], v39 offset:8192\n" \
"v_cvt_pk_bf16_f32 v234, v180, v181\n" \
"v_cvt_pk_bf16_f32 v235, v182, v183\n" \
"v_accvgpr_read_b32 v236, a200\n" \
"v_accvgpr_read_b32 v237, a201\n" \
"v_accvgpr_read_b32 v238, a202\n" \
"v_accvgpr_read_b32 v239, a203\n" \
"v_accvgpr_read_b32 v180, a204\n" \
"v_accvgpr_read_b32 v181, a205\n" \
"v_accvgpr_read_b32 v182, a206\n" \
"v_accvgpr_read_b32 v183, a207\n" \
"v_cvt_pk_bf16_f32 v236, v236, v237\n" \
"v_cvt_pk_bf16_f32 v237, v238, v239\n" \
"v_cvt_pk_bf16_f32 v238, v180, v181\n" \
"v_cvt_pk_bf16_f32 v239, v182, v183\n" \
"v_accvgpr_read_b32 v240, a232\n" \
"v_accvgpr_read_b32 v241, a233\n" \
"v_accvgpr_read_b32 v242, a234\n" \
"v_accvgpr_read_b32 v243, a235\n" \
"v_accvgpr_read_b32 v180, a236\n" \
"v_accvgpr_read_b32 v181, a237\n" \
"v_accvgpr_read_b32 v182, a238\n" \
"v_accvgpr_read_b32 v183, a239\n" \
"v_cvt_pk_bf16_f32 v240, v240, v241\n" \
"v_cvt_pk_bf16_f32 v241, v242, v243\n" \
"v_cvt_pk_bf16_f32 v242, v180, v181\n" \
"v_cvt_pk_bf16_f32 v243, v182, v183\n" \
"v_accvgpr_read_b32 v244, a224\n" \
"v_accvgpr_read_b32 v245, a225\n" \
"v_accvgpr_read_b32 v246, a226\n" \
"v_accvgpr_read_b32 v247, a227\n" \
"v_accvgpr_read_b32 v180, a228\n" \
"v_accvgpr_read_b32 v181, a229\n" \
"v_accvgpr_read_b32 v182, a230\n" \
"v_accvgpr_read_b32 v183, a231\n" \
"v_cvt_pk_bf16_f32 v244, v244, v245\n" \
"v_cvt_pk_bf16_f32 v245, v246, v247\n" \
"v_cvt_pk_bf16_f32 v246, v180, v181\n" \
"v_cvt_pk_bf16_f32 v247, v182, v183\n" \
"v_mfma_f32_16x16x32_bf16 a[224:227], v[64:67], v[92:95], 0\n" \
"ds_read_b64_tr_b16 v[178:179], v39 offset:9216\n" \
"v_accvgpr_read_b32 v184, a24\n" \
"v_mfma_f32_16x16x32_bf16 a[224:227], v[128:131], v[156:159], a[224:227]\n" \
"v_accvgpr_read_b32 v185, a25\n" \
"v_accvgpr_read_b32 v186, a26\n" \
"v_accvgpr_read_b32 v187, a27\n" \
"v_mfma_f32_16x16x32_bf16 a[228:231], v[96:99], v[64:67], 0\n" \
"v_accvgpr_read_b32 v180, a28\n" \
"v_accvgpr_read_b32 v181, a29\n" \
"v_accvgpr_read_b32 v182, a30\n" \
"v_mfma_f32_16x16x32_bf16 a[228:231], v[160:163], v[128:131], a[228:231]\n" \
"s_add_u32 s88, s41, 0x3000\n" \
"buffer_store_dwordx4 v[212:215], v20, s[80:83], s88 offen offset:0 sc1\n" \
"v_mfma_f32_16x16x32_bf16 a[196:199], v[88:91], v[64:67], 0\n" \
"v_accvgpr_read_b32 v183, a31\n" \
"v_cvt_pk_bf16_f32 v184, v184, v185\n" \
"v_cvt_pk_bf16_f32 v185, v186, v187\n" \
"v_mfma_f32_16x16x32_bf16 a[196:199], v[152:155], v[128:131], a[196:199]\n" \
"v_cvt_pk_bf16_f32 v186, v180, v181\n" \
"v_cvt_pk_bf16_f32 v187, v182, v183\n" \
"v_accvgpr_read_b32 v188, a16\n" \
"v_mfma_f32_16x16x32_bf16 a[192:195], v[84:87], v[64:67], 0\n" \
"v_accvgpr_read_b32 v189, a17\n" \
"v_accvgpr_read_b32 v190, a18\n" \
"v_accvgpr_read_b32 v191, a19\n" \
"v_mfma_f32_16x16x32_bf16 a[192:195], v[148:151], v[128:131], a[192:195]\n" \
"v_accvgpr_read_b32 v180, a20\n" \
"v_accvgpr_read_b32 v181, a21\n" \
"v_accvgpr_read_b32 v182, a22\n" \
"s_waitcnt lgkmcnt(15)\n" \
"s_nop 0\n" \
"v_mfma_f32_16x16x32_bf16 a[8:11], v[88:91], v[248:251], 0\n" \
"v_accvgpr_read_b32 v183, a23\n" \
"v_cvt_pk_bf16_f32 v188, v188, v189\n" \
"v_cvt_pk_bf16_f32 v189, v190, v191\n" \
"v_mfma_f32_16x16x32_bf16 a[8:11], v[152:155], v[48:51], a[8:11]\n" \
"v_cvt_pk_bf16_f32 v190, v180, v181\n" \
"v_cvt_pk_bf16_f32 v191, v182, v183\n" \
"v_accvgpr_read_b32 v192, a48\n" \
"v_mfma_f32_16x16x32_bf16 a[40:43], v[96:99], v[248:251], 0\n" \
"v_accvgpr_read_b32 v193, a49\n" \
"v_accvgpr_read_b32 v194, a50\n" \
"v_accvgpr_read_b32 v195, a51\n" \
"v_mfma_f32_16x16x32_bf16 a[40:43], v[160:163], v[48:51], a[40:43]\n" \
"v_accvgpr_read_b32 v180, a52\n" \
"v_accvgpr_read_b32 v181, a53\n" \
"v_accvgpr_read_b32 v182, a54\n" \
"v_mfma_f32_16x16x32_bf16 a[12:15], v[248:251], v[92:95], 0\n" \
"v_accvgpr_read_b32 v183, a55\n" \
"v_cvt_pk_bf16_f32 v192, v192, v193\n" \
"v_cvt_pk_bf16_f32 v193, v194, v195\n" \
"v_mfma_f32_16x16x32_bf16 a[12:15], v[48:51], v[156:159], a[12:15]\n" \
"v_cvt_pk_bf16_f32 v194, v180, v181\n" \
"v_cvt_pk_bf16_f32 v195, v182, v183\n" \
"v_accvgpr_read_b32 v196, a56\n" \
"s_waitcnt vmcnt(4) lgkmcnt(0)\n" \
"s_barrier\n" \
"v_mfma_f32_16x16x32_bf16 a[72:75], v[100:103], v[72:75], 0\n" \
"s_add_u32 m0, s40, 0x0\n" \
"s_nop 0\n" \
"s_add_u32 s85, s39, 0x4000\n" \
"v_accvgpr_read_b32 v197, a57\n" \
"v_mfma_f32_16x16x32_bf16 a[72:75], v[164:167], v[136:139], a[72:75]\n" \
"buffer_load_dwordx4 v20, s[80:83], s85 offen sc1 lds\n" \
"v_accvgpr_read_b32 v198, a58\n" \
"v_mfma_f32_16x16x32_bf16 a[76:79], v[104:107], v[72:75], 0\n" \
"ds_read_b64_tr_b16 v[84:85], v32 offset:32768\n" \
"v_accvgpr_read_b32 v199, a59\n" \
"v_mfma_f32_16x16x32_bf16 a[76:79], v[168:171], v[136:139], a[76:79]\n" \
"ds_read_b64_tr_b16 v[86:87], v32 offset:33280\n" \
"v_accvgpr_read_b32 v180, a60\n" \
"v_mfma_f32_16x16x32_bf16 a[104:107], v[108:111], v[72:75], 0\n" \
"s_add_u32 m0, s40, 0x1000\n" \
"s_add_u32 s84, s39, 0x5000\n" \
"v_accvgpr_read_b32 v181, a61\n" \
"v_mfma_f32_16x16x32_bf16 a[104:107], v[172:175], v[136:139], a[104:107]\n" \
"buffer_load_dwordx4 v20, s[80:83], s84 offen sc1 lds\n" \
"v_accvgpr_read_b32 v182, a62\n" \
"v_mfma_f32_16x16x32_bf16 a[108:111], v[112:115], v[72:75], 0\n" \
"ds_read_b64_tr_b16 v[148:149], v32 offset:36864\n" \
"v_accvgpr_read_b32 v183, a63\n" \
"v_mfma_f32_16x16x32_bf16 a[108:111], v[176:179], v[136:139], a[108:111]\n" \
"ds_read_b128 v[72:75], v28 offset:43008\n" \
"v_cvt_pk_bf16_f32 v196, v196, v197\n" \
"v_mfma_f32_16x16x32_bf16 a[172:175], v[112:115], v[76:79], 0\n" \
"ds_read_b128 v[136:139], v29 offset:43008\n" \
"v_cvt_pk_bf16_f32 v197, v198, v199\n" \
"v_mfma_f32_16x16x32_bf16 a[172:175], v[176:179], v[140:143], a[172:175]\n" \
"s_add_u32 m0, s40, 0x3000\n" \
"s_mov_b32 s85, 0x100\n" \
"v_cvt_pk_bf16_f32 v198, v180, v181\n" \
"v_mfma_f32_16x16x32_bf16 a[168:171], v[108:111], v[76:79], 0\n" \
"buffer_load_dwordx4 v21, s[76:79], s85 offen lds\n" \
"v_cvt_pk_bf16_f32 v199, v182, v183\n" \
"v_mfma_f32_16x16x32_bf16 a[168:171], v[172:175], v[140:143], a[168:171]\n" \
"ds_read_b64_tr_b16 v[150:151], v32 offset:37376\n" \
"v_accvgpr_read_b32 v252, a88\n" \
"v_mfma_f32_16x16x32_bf16 a[140:143], v[104:107], v[76:79], 0\n" \
"s_add_u32 m0, s30, 0x0\n" \
"s_nop 0\n" \
"s_mov_b32 s84, 0x100\n" \
"v_accvgpr_read_b32 v253, a89\n" \
"v_mfma_f32_16x16x32_bf16 a[140:143], v[168:171], v[140:143], a[140:143]\n" \
"buffer_load_dwordx4 v24, s[76:79], s84 offen lds\n" \
"v_accvgpr_read_b32 v254, a90\n" \
"v_mfma_f32_16x16x32_bf16 a[136:139], v[100:103], v[76:79], 0\n" \
"ds_read_b64_tr_b16 v[88:89], v33 offset:32768\n" \
"v_accvgpr_read_b32 v255, a91\n" \
"v_mfma_f32_16x16x32_bf16 a[136:139], v[164:167], v[140:143], a[136:139]\n" \
"ds_read_b128 v[76:79], v28 offset:45056\n" \
"v_accvgpr_read_b32 v180, a92\n" \
"v_mfma_f32_16x16x32_bf16 a[200:203], v[100:103], v[80:83], 0\n" \
"ds_read_b128 v[140:143], v29 offset:45056\n" \
"v_accvgpr_read_b32 v181, a93\n" \
"v_mfma_f32_16x16x32_bf16 a[200:203], v[164:167], v[144:147], a[200:203]\n" \
"s_add_u32 m0, s30, 0x400\n" \
"s_mov_b32 s85, 0x2500\n" \
"v_accvgpr_read_b32 v182, a94\n" \
"v_mfma_f32_16x16x32_bf16 a[204:207], v[104:107], v[80:83], 0\n" \
"buffer_load_dwordx4 v25, s[76:79], s85 offen lds\n" \
"v_accvgpr_read_b32 v183, a95\n" \
"v_mfma_f32_16x16x32_bf16 a[204:207], v[168:171], v[144:147], a[204:207]\n" \
"ds_read_b64_tr_b16 v[90:91], v33 offset:33280\n" \
"v_cvt_pk_bf16_f32 v252, v252, v253\n" \
"v_mfma_f32_16x16x32_bf16 a[232:235], v[108:111], v[80:83], 0\n" \
"ds_read_b64_tr_b16 v[152:153], v33 offset:36864\n" \
"v_cvt_pk_bf16_f32 v253, v254, v255\n" \
"v_mfma_f32_16x16x32_bf16 a[232:235], v[172:175], v[144:147], a[232:235]\n" \
"s_add_u32 m0, s31, 0x0\n" \
"s_nop 0\n" \
"s_mov_b32 s84, 0x100000\n" \
"v_cvt_pk_bf16_f32 v254, v180, v181\n" \
"v_mfma_f32_16x16x32_bf16 a[236:239], v[112:115], v[80:83], 0\n" \
"buffer_load_dwordx4 v26, s[68:71], s84 offen lds\n" \
"v_cvt_pk_bf16_f32 v255, v182, v183\n" \
"v_mfma_f32_16x16x32_bf16 a[236:239], v[176:179], v[144:147], a[236:239]\n" \
"ds_read_b128 v[80:83], v28 offset:47104\n" \
"v_accvgpr_write_b32 a44, v252\n" \
"v_mfma_f32_16x16x32_bf16 a[28:31], v[112:115], v[52:55], 0\n" \
"ds_read_b128 v[144:147], v29 offset:47104\n" \
"v_accvgpr_write_b32 a45, v253\n" \
"v_mfma_f32_16x16x32_bf16 a[28:31], v[176:179], v[116:119], a[28:31]\n" \
"s_add_u32 m0, s30, 0x800\n" \
"s_mov_b32 s85, 0x4900\n" \
"v_accvgpr_write_b32 a46, v254\n" \
"v_mfma_f32_16x16x32_bf16 a[24:27], v[108:111], v[52:55], 0\n" \
"buffer_load_dwordx4 v24, s[76:79], s85 offen lds\n" \
"v_accvgpr_write_b32 a47, v255\n" \
"v_mfma_f32_16x16x32_bf16 a[24:27], v[172:175], v[116:119], a[24:27]\n" \
"ds_read_b64_tr_b16 v[154:155], v33 offset:37376\n" \
"v_accvgpr_read_b32 v252, a80\n" \
"v_mfma_f32_16x16x32_bf16 a[20:23], v[104:107], v[52:55], 0\n" \
"ds_read_b64_tr_b16 v[96:97], v35 offset:32768\n" \
"v_accvgpr_read_b32 v253, a81\n" \
"v_mfma_f32_16x16x32_bf16 a[20:23], v[168:171], v[116:119], a[20:23]\n" \
"ds_read_b64_tr_b16 v[98:99], v35 offset:33280\n" \
"v_accvgpr_read_b32 v254, a82\n" \
"v_mfma_f32_16x16x32_bf16 a[16:19], v[100:103], v[52:55], 0\n" \
"ds_read_b64_tr_b16 v[160:161], v35 offset:36864\n" \
"v_accvgpr_read_b32 v255, a83\n" \
"v_mfma_f32_16x16x32_bf16 a[16:19], v[164:167], v[116:119], a[16:19]\n" \
"ds_read_b128 v[52:55], v28 offset:32768\n" \
"v_accvgpr_read_b32 v180, a84\n" \
"v_mfma_f32_16x16x32_bf16 a[48:51], v[100:103], v[56:59], 0\n" \
"ds_read_b128 v[116:119], v29 offset:32768\n" \
"v_accvgpr_read_b32 v181, a85\n" \
"v_mfma_f32_16x16x32_bf16 a[48:51], v[164:167], v[120:123], a[48:51]\n" \
"s_add_u32 m0, s30, 0xc00\n" \
"s_mov_b32 s84, 0x6d00\n" \
"v_accvgpr_read_b32 v182, a86\n" \
"v_mfma_f32_16x16x32_bf16 a[52:55], v[104:107], v[56:59], 0\n" \
"buffer_load_dwordx4 v25, s[76:79], s84 offen lds\n" \
"v_accvgpr_read_b32 v183, a87\n" \
"v_mfma_f32_16x16x32_bf16 a[52:55], v[168:171], v[120:123], a[52:55]\n" \
"s_add_u32 m0, s31, 0x400\n" \
"s_mov_b32 s85, 0x108000\n" \
"v_cvt_pk_bf16_f32 v252, v252, v253\n" \
"v_mfma_f32_16x16x32_bf16 a[56:59], v[108:111], v[56:59], 0\n" \
"buffer_load_dwordx4 v26, s[68:71], s85 offen lds\n" \
"v_cvt_pk_bf16_f32 v253, v254, v255\n" \
"v_mfma_f32_16x16x32_bf16 a[56:59], v[172:175], v[120:123], a[56:59]\n" \
"ds_read_b64_tr_b16 v[162:163], v35 offset:37376\n" \
"v_cvt_pk_bf16_f32 v254, v180, v181\n" \
"v_mfma_f32_16x16x32_bf16 a[60:63], v[112:115], v[56:59], 0\n" \
"ds_read_b64_tr_b16 v[92:93], v34 offset:32768\n" \
"v_cvt_pk_bf16_f32 v255, v182, v183\n" \
"v_mfma_f32_16x16x32_bf16 a[60:63], v[176:179], v[120:123], a[60:63]\n" \
"ds_read_b128 v[56:59], v28 offset:34816\n" \
"v_accvgpr_read_b32 v200, a112\n" \
"v_mfma_f32_16x16x32_bf16 a[92:95], v[112:115], v[60:63], 0\n" \
"ds_read_b128 v[120:123], v29 offset:34816\n" \
"v_accvgpr_read_b32 v201, a113\n" \
"v_mfma_f32_16x16x32_bf16 a[92:95], v[176:179], v[124:127], a[92:95]\n" \
"s_add_u32 m0, s30, 0x1000\n" \
"s_mov_b32 s84, 0x9100\n" \
"v_accvgpr_read_b32 v202, a114\n" \
"v_mfma_f32_16x16x32_bf16 a[88:91], v[108:111], v[60:63], 0\n" \
"buffer_load_dwordx4 v24, s[76:79], s84 offen lds\n" \
"v_accvgpr_read_b32 v203, a115\n" \
"v_mfma_f32_16x16x32_bf16 a[88:91], v[172:175], v[124:127], a[88:91]\n" \
"s_add_u32 m0, s30, 0x1400\n" \
"s_mov_b32 s85, 0xb500\n" \
"v_accvgpr_read_b32 v180, a116\n" \
"v_mfma_f32_16x16x32_bf16 a[84:87], v[104:107], v[60:63], 0\n" \
"buffer_load_dwordx4 v25, s[76:79], s85 offen lds\n" \
"v_accvgpr_read_b32 v181, a117\n" \
"v_mfma_f32_16x16x32_bf16 a[84:87], v[168:171], v[124:127], a[84:87]\n" \
"ds_read_b64_tr_b16 v[94:95], v34 offset:33280\n" \
"v_accvgpr_read_b32 v182, a118\n" \
"v_mfma_f32_16x16x32_bf16 a[80:83], v[100:103], v[60:63], 0\n" \
"ds_read_b64_tr_b16 v[156:157], v34 offset:36864\n" \
"v_accvgpr_read_b32 v183, a119\n" \
"v_mfma_f32_16x16x32_bf16 a[80:83], v[164:167], v[124:127], a[80:83]\n" \
"ds_read_b128 v[60:63], v28 offset:36864\n" \
"v_cvt_pk_bf16_f32 v200, v200, v201\n" \
"v_cvt_pk_bf16_f32 v201, v202, v203\n" \
"v_cvt_pk_bf16_f32 v202, v180, v181\n" \
"v_cvt_pk_bf16_f32 v203, v182, v183\n" \
"v_mfma_f32_16x16x32_bf16 a[112:115], v[100:103], v[64:67], 0\n" \
"ds_read_b128 v[124:127], v29 offset:36864\n" \
"v_accvgpr_read_b32 v204, a120\n" \
"v_mfma_f32_16x16x32_bf16 a[112:115], v[164:167], v[128:131], a[112:115]\n" \
"s_add_u32 m0, s31, 0x800\n" \
"s_mov_b32 s84, 0x110000\n" \
"v_accvgpr_read_b32 v205, a121\n" \
"v_mfma_f32_16x16x32_bf16 a[116:119], v[104:107], v[64:67], 0\n" \
"buffer_load_dwordx4 v27, s[68:71], s84 offen lds\n" \
"v_accvgpr_read_b32 v206, a122\n" \
"v_mfma_f32_16x16x32_bf16 a[116:119], v[168:171], v[128:131], a[116:119]\n" \
"s_add_u32 m0, s30, 0x1800\n" \
"s_mov_b32 s85, 0xd900\n" \
"v_accvgpr_read_b32 v207, a123\n" \
"v_accvgpr_read_b32 v180, a124\n" \
"v_accvgpr_read_b32 v181, a125\n" \
"v_accvgpr_read_b32 v182, a126\n" \
"v_accvgpr_read_b32 v183, a127\n" \
"v_cvt_pk_bf16_f32 v204, v204, v205\n" \
"v_cvt_pk_bf16_f32 v205, v206, v207\n" \
"v_cvt_pk_bf16_f32 v206, v180, v181\n" \
"v_cvt_pk_bf16_f32 v207, v182, v183\n" \
"v_mfma_f32_16x16x32_bf16 a[120:123], v[108:111], v[64:67], 0\n" \
"buffer_load_dwordx4 v24, s[76:79], s85 offen lds\n" \
"v_accvgpr_read_b32 v208, a152\n" \
"v_mfma_f32_16x16x32_bf16 a[120:123], v[172:175], v[128:131], a[120:123]\n" \
"ds_read_b64_tr_b16 v[158:159], v34 offset:37376\n" \
"v_accvgpr_read_b32 v209, a153\n" \
"v_mfma_f32_16x16x32_bf16 a[124:127], v[112:115], v[64:67], 0\n" \
"ds_read_b128 v[248:251], v22 offset:32768\n" \
"v_accvgpr_read_b32 v210, a154\n" \
"v_mfma_f32_16x16x32_bf16 a[124:127], v[176:179], v[128:131], a[124:127]\n" \
"ds_read_b128 v[64:67], v28 offset:38912\n" \
"v_accvgpr_read_b32 v211, a155\n" \
"v_accvgpr_read_b32 v180, a156\n" \
"v_accvgpr_read_b32 v181, a157\n" \
"v_accvgpr_read_b32 v182, a158\n" \
"v_accvgpr_read_b32 v183, a159\n" \
"v_cvt_pk_bf16_f32 v208, v208, v209\n" \
"v_cvt_pk_bf16_f32 v209, v210, v211\n" \
"v_cvt_pk_bf16_f32 v210, v180, v181\n" \
"v_cvt_pk_bf16_f32 v211, v182, v183\n" \
"v_mfma_f32_16x16x32_bf16 a[156:159], v[112:115], v[68:71], 0\n" \
"ds_read_b128 v[128:131], v29 offset:38912\n" \
"v_accvgpr_read_b32 v212, a144\n" \
"v_mfma_f32_16x16x32_bf16 a[156:159], v[176:179], v[132:135], a[156:159]\n" \
"ds_read_b128 v[48:51], v23 offset:32768\n" \
"v_accvgpr_read_b32 v213, a145\n" \
"v_mfma_f32_16x16x32_bf16 a[152:155], v[108:111], v[68:71], 0\n" \
"v_accvgpr_read_b32 v214, a146\n" \
"v_accvgpr_read_b32 v215, a147\n" \
"v_accvgpr_read_b32 v180, a148\n" \
"v_mfma_f32_16x16x32_bf16 a[152:155], v[172:175], v[132:135], a[152:155]\n" \
"v_accvgpr_read_b32 v181, a149\n" \
"v_accvgpr_read_b32 v182, a150\n" \
"v_accvgpr_read_b32 v183, a151\n" \
"v_cvt_pk_bf16_f32 v212, v212, v213\n" \
"v_cvt_pk_bf16_f32 v213, v214, v215\n" \
"v_cvt_pk_bf16_f32 v214, v180, v181\n" \
"v_cvt_pk_bf16_f32 v215, v182, v183\n" \
"v_mfma_f32_16x16x32_bf16 a[148:151], v[104:107], v[68:71], 0\n" \
"v_mfma_f32_16x16x32_bf16 a[148:151], v[168:171], v[132:135], a[148:151]\n" \
"v_mfma_f32_16x16x32_bf16 a[144:147], v[100:103], v[68:71], 0\n" \
"v_mfma_f32_16x16x32_bf16 a[144:147], v[164:167], v[132:135], a[144:147]\n" \
"ds_read_b128 v[68:71], v28 offset:40960\n" \
"ds_read_b128 v[132:135], v29 offset:40960\n" \
"s_waitcnt lgkmcnt(15)\n" \
"v_mfma_f32_16x16x32_bf16 a[0:3], v[84:87], v[52:55], a[0:3]\n" \
"s_add_u32 m0, s30, 0x1c00\n" \
"s_mov_b32 s85, 0xfd00\n" \
"v_mfma_f32_16x16x32_bf16 a[0:3], v[148:151], v[116:119], a[0:3]\n" \
"buffer_load_dwordx4 v25, s[76:79], s85 offen lds\n" \
"v_mfma_f32_16x16x32_bf16 a[4:7], v[88:91], v[52:55], a[4:7]\n" \
"ds_read_b64_tr_b16 v[100:101], v36 offset:32768\n" \
"v_mov_b32_e32 v11, 0\n" \
"v_mfma_f32_16x16x32_bf16 a[4:7], v[152:155], v[116:119], a[4:7]\n" \
"s_add_u32 m0, s31, 0xc00\n" \
"s_mov_b32 s84, 0x118000\n" \
"v_mfma_f32_16x16x32_bf16 a[36:39], v[96:99], v[52:55], a[36:39]\n" \
"buffer_load_dwordx4 v27, s[68:71], s84 offen lds\n" \
"s_waitcnt lgkmcnt(15)\n" \
"v_mfma_f32_16x16x32_bf16 a[36:39], v[160:163], v[116:119], a[36:39]\n" \
"ds_read_b64_tr_b16 v[102:103], v36 offset:33792\n" \
"s_and_b32 s6, s2, 7\n" \
"s_waitcnt lgkmcnt(12)\n" \
"v_mfma_f32_16x16x32_bf16 a[32:35], v[52:55], v[92:95], a[32:35]\n" \
"ds_read_b64_tr_b16 v[164:165], v36 offset:40960\n" \
"s_lshl_b32 s7, s3, 1\n" \
"s_waitcnt lgkmcnt(9)\n" \
"v_mfma_f32_16x16x32_bf16 a[32:35], v[116:119], v[156:159], a[32:35]\n" \
"ds_read_b64_tr_b16 v[166:167], v36 offset:41984\n" \
"s_lshr_b32 s4, s2, 5\n" \
"v_mfma_f32_16x16x32_bf16 a[96:99], v[56:59], v[92:95], a[96:99]\n" \
"ds_read_b64_tr_b16 v[104:105], v37 offset:32768\n" \
"s_lshl_b32 s5, s6, 2\n" \
"v_mfma_f32_16x16x32_bf16 a[96:99], v[120:123], v[156:159], a[96:99]\n" \
".fill (L_pb + 24288 - .) / 4, 4, 0xBF800000\n" \
"L_pp6:\n" \
"s_cbranch_execz L_pret\n" \
"ds_read_b64_tr_b16 v[106:107], v37 offset:33792\n" \
"s_bfe_u32 s8, s2, 0x20003\n" \
"v_mfma_f32_16x16x32_bf16 a[100:103], v[96:99], v[56:59], a[100:103]\n" \
"ds_read_b64_tr_b16 v[168:169], v37 offset:40960\n" \
"s_and_b32 s7, s7, 4\n" \
"v_mfma_f32_16x16x32_bf16 a[100:103], v[160:163], v[120:123], a[100:103]\n" \
"s_add_u32 s89, s41, 0x0\n" \
"buffer_store_dwordx4 v[184:187], v20, s[80:83], s89 offen offset:3072 sc1\n" \
"v_mfma_f32_16x16x32_bf16 a[68:71], v[88:91], v[56:59], a[68:71]\n" \
"ds_read_b64_tr_b16 v[170:171], v37 offset:41984\n" \
"s_and_b32 s9, s3, 1\n" \
"v_mfma_f32_16x16x32_bf16 a[68:71], v[152:155], v[120:123], a[68:71]\n" \
"ds_read_b64_tr_b16 v[108:109], v38 offset:32768\n" \
"s_or_b32 s13, s5, s8\n" \
"v_mfma_f32_16x16x32_bf16 a[64:67], v[84:87], v[56:59], a[64:67]\n" \
"ds_read_b64_tr_b16 v[110:111], v38 offset:33792\n" \
"s_or_b32 s9, s7, s9\n" \
"v_mfma_f32_16x16x32_bf16 a[64:67], v[148:151], v[120:123], a[64:67]\n" \
"s_mul_i32 s7, s4, 0xa00\n" \
"s_add_i32 s7, s7, 0x5bc1000\n" \
"v_mfma_f32_16x16x32_bf16 a[128:131], v[84:87], v[60:63], a[128:131]\n" \
"s_add_u32 s88, s41, 0x0\n" \
"buffer_store_dwordx4 v[188:191], v20, s[80:83], s88 offen offset:2048 sc1\n" \
"v_mfma_f32_16x16x32_bf16 a[128:131], v[148:151], v[124:127], a[128:131]\n" \
"ds_read_b64_tr_b16 v[172:173], v38 offset:40960\n" \
"s_lshl_b32 s10, s13, 2\n" \
"v_mfma_f32_16x16x32_bf16 a[132:135], v[88:91], v[60:63], a[132:135]\n" \
"ds_read_b64_tr_b16 v[174:175], v38 offset:41984\n" \
"s_or_b32 s10, s10, s7\n" \
"v_mfma_f32_16x16x32_bf16 a[132:135], v[152:155], v[124:127], a[132:135]\n" \
"ds_read_b64_tr_b16 v[112:113], v39 offset:32768\n" \
"v_writelane_b32 v11, s10, 0\n" \
"v_mfma_f32_16x16x32_bf16 a[164:167], v[96:99], v[60:63], a[164:167]\n" \
"s_lshl_b32 s14, s4, 3\n" \
"v_writelane_b32 v11, s7, 1\n" \
"v_mfma_f32_16x16x32_bf16 a[164:167], v[160:163], v[124:127], a[164:167]\n" \
"ds_read_b64_tr_b16 v[114:115], v39 offset:33792\n" \
"s_or_b32 s7, s14, s6\n" \
"v_mfma_f32_16x16x32_bf16 a[160:163], v[60:63], v[92:95], a[160:163]\n" \
"s_add_u32 s89, s41, 0x1000\n" \
"buffer_store_dwordx4 v[192:195], v20, s[80:83], s89 offen offset:2048 sc1\n" \
"v_mfma_f32_16x16x32_bf16 a[160:163], v[124:127], v[156:159], a[160:163]\n" \
"ds_read_b64_tr_b16 v[176:177], v39 offset:40960\n" \
"s_lshl_b32 s7, s7, 10\n" \
"s_waitcnt lgkmcnt(15)\n" \
"v_mfma_f32_16x16x32_bf16 a[224:227], v[64:67], v[92:95], a[224:227]\n" \
"ds_read_b64_tr_b16 v[178:179], v39 offset:41984\n" \
"s_lshl_b32 s10, s3, 8\n" \
"v_mfma_f32_16x16x32_bf16 a[224:227], v[128:131], v[156:159], a[224:227]\n" \
"s_or_b32 s7, s7, s10\n" \
"s_or_b32 s10, s7, 0x5c00200\n" \
"v_mfma_f32_16x16x32_bf16 a[228:231], v[96:99], v[64:67], a[228:231]\n" \
"v_writelane_b32 v11, s10, 2\n" \
"s_lshl_b32 s10, s8, 2\n" \
"v_mfma_f32_16x16x32_bf16 a[228:231], v[160:163], v[128:131], a[228:231]\n" \
"s_add_u32 s88, s41, 0x1000\n" \
"buffer_store_dwordx4 v[196:199], v20, s[80:83], s88 offen offset:3072 sc1\n" \
"v_mfma_f32_16x16x32_bf16 a[196:199], v[88:91], v[64:67], a[196:199]\n" \
"s_or_b32 s11, s10, s7\n" \
"s_or_b32 s11, s11, 0x5c00000\n" \
"v_mfma_f32_16x16x32_bf16 a[196:199], v[152:155], v[128:131], a[196:199]\n" \
"v_writelane_b32 v11, s11, 3\n" \
"s_or_b32 s7, s7, 0x5c00080\n" \
"v_mfma_f32_16x16x32_bf16 a[192:195], v[84:87], v[64:67], a[192:195]\n" \
"v_writelane_b32 v11, s7, 4\n" \
"s_or_b32 s7, s7, s10\n" \
"v_mfma_f32_16x16x32_bf16 a[192:195], v[148:151], v[128:131], a[192:195]\n" \
"s_add_i32 s10, s3, s2\n" \
"v_writelane_b32 v11, s7, 5\n" \
"v_mfma_f32_16x16x32_bf16 a[8:11], v[88:91], v[248:251], a[8:11]\n" \
"s_mov_b32 s7, 2.0\n" \
"s_mov_b32 s11, 0x5bc0000\n" \
"v_mfma_f32_16x16x32_bf16 a[8:11], v[152:155], v[48:51], a[8:11]\n" \
"s_cmp_lg_u32 s10, 0\n" \
"s_cselect_b32 s10, s7, s11\n" \
"v_mfma_f32_16x16x32_bf16 a[40:43], v[96:99], v[248:251], a[40:43]\n" \
"s_lshr_b32 s15, s2, 3\n" \
"v_writelane_b32 v11, s10, 53\n" \
"v_mfma_f32_16x16x32_bf16 a[40:43], v[160:163], v[48:51], a[40:43]\n" \
"s_lshl_b32 s10, s6, 5\n" \
"s_min_u32 s16, s3, 2\n" \
"v_mfma_f32_16x16x32_bf16 a[12:15], v[248:251], v[92:95], a[12:15]\n" \
"s_mul_i32 s12, s8, 0x120\n" \
"v_mfma_f32_16x16x32_bf16 a[12:15], v[48:51], v[156:159], a[12:15]\n" \
"s_waitcnt vmcnt(4) lgkmcnt(0)\n" \
"s_barrier\n" \
"s_nop 0\n" \
"v_mfma_f32_16x16x32_bf16 a[16:19], v[100:103], v[52:55], a[16:19]\n" \
"s_add_u32 m0, s40, 0x8000\n" \
"s_add_u32 s85, s39, 0x6000\n" \
"v_mfma_f32_16x16x32_bf16 a[16:19], v[164:167], v[116:119], a[16:19]\n" \
"buffer_load_dwordx4 v20, s[80:83], s85 offen sc1 lds\n" \
"v_mfma_f32_16x16x32_bf16 a[20:23], v[104:107], v[52:55], a[20:23]\n" \
"ds_read_b64_tr_b16 v[84:85], v32 offset:0\n" \
"v_mfma_f32_16x16x32_bf16 a[20:23], v[168:171], v[116:119], a[20:23]\n" \
"ds_read_b64_tr_b16 v[86:87], v32 offset:512\n" \
"v_mfma_f32_16x16x32_bf16 a[24:27], v[108:111], v[52:55], a[24:27]\n" \
"s_add_u32 m0, s40, 0x9000\n" \
"s_add_u32 s84, s39, 0x7000\n" \
"v_mfma_f32_16x16x32_bf16 a[24:27], v[172:175], v[116:119], a[24:27]\n" \
"buffer_load_dwordx4 v20, s[80:83], s84 offen sc1 lds\n" \
"v_mfma_f32_16x16x32_bf16 a[28:31], v[112:115], v[52:55], a[28:31]\n" \
"ds_read_b64_tr_b16 v[148:149], v32 offset:4096\n" \
"v_mfma_f32_16x16x32_bf16 a[28:31], v[176:179], v[116:119], a[28:31]\n" \
"ds_read_b128 v[52:55], v28 offset:0\n" \
"v_mfma_f32_16x16x32_bf16 a[60:63], v[112:115], v[56:59], a[60:63]\n" \
"ds_read_b128 v[116:119], v29 offset:0\n" \
"v_mfma_f32_16x16x32_bf16 a[60:63], v[176:179], v[120:123], a[60:63]\n" \
"s_add_u32 m0, s40, 0xb000\n" \
"s_mov_b32 s85, 0x180\n" \
"v_mfma_f32_16x16x32_bf16 a[56:59], v[108:111], v[56:59], a[56:59]\n" \
"buffer_load_dwordx4 v21, s[76:79], s85 offen lds\n" \
"v_mfma_f32_16x16x32_bf16 a[56:59], v[172:175], v[120:123], a[56:59]\n" \
"ds_read_b64_tr_b16 v[150:151], v32 offset:4608\n" \
"v_mfma_f32_16x16x32_bf16 a[52:55], v[104:107], v[56:59], a[52:55]\n" \
"s_add_u32 m0, s30, 0x8000\n" \
"s_mov_b32 s84, 0x180\n" \
"v_mfma_f32_16x16x32_bf16 a[52:55], v[168:171], v[120:123], a[52:55]\n" \
"buffer_load_dwordx4 v24, s[76:79], s84 offen lds\n" \
"v_mfma_f32_16x16x32_bf16 a[48:51], v[100:103], v[56:59], a[48:51]\n" \
"ds_read_b64_tr_b16 v[88:89], v33 offset:0\n" \
"v_mfma_f32_16x16x32_bf16 a[48:51], v[164:167], v[120:123], a[48:51]\n" \
"ds_read_b128 v[56:59], v28 offset:2048\n" \
"v_mfma_f32_16x16x32_bf16 a[80:83], v[100:103], v[60:63], a[80:83]\n" \
"ds_read_b128 v[120:123], v29 offset:2048\n" \
"v_mfma_f32_16x16x32_bf16 a[80:83], v[164:167], v[124:127], a[80:83]\n" \
"s_add_u32 m0, s30, 0x8400\n" \
"s_mov_b32 s85, 0x2580\n" \
"v_mfma_f32_16x16x32_bf16 a[84:87], v[104:107], v[60:63], a[84:87]\n" \
"buffer_load_dwordx4 v25, s[76:79], s85 offen lds\n" \
"v_mfma_f32_16x16x32_bf16 a[84:87], v[168:171], v[124:127], a[84:87]\n" \
"ds_read_b64_tr_b16 v[90:91], v33 offset:512\n" \
"v_mfma_f32_16x16x32_bf16 a[88:91], v[108:111], v[60:63], a[88:91]\n" \
"ds_read_b64_tr_b16 v[152:153], v33 offset:4096\n" \
"v_mfma_f32_16x16x32_bf16 a[88:91], v[172:175], v[124:127], a[88:91]\n" \
"s_add_u32 m0, s31, 0x8000\n" \
"s_mov_b32 s84, 0x180000\n" \
"v_mfma_f32_16x16x32_bf16 a[92:95], v[112:115], v[60:63], a[92:95]\n" \
"buffer_load_dwordx4 v26, s[68:71], s84 offen lds\n" \
"v_mfma_f32_16x16x32_bf16 a[92:95], v[176:179], v[124:127], a[92:95]\n" \
"ds_read_b128 v[60:63], v28 offset:4096\n" \
"v_mfma_f32_16x16x32_bf16 a[124:127], v[112:115], v[64:67], a[124:127]\n" \
"ds_read_b128 v[124:127], v29 offset:4096\n" \
"v_mfma_f32_16x16x32_bf16 a[124:127], v[176:179], v[128:131], a[124:127]\n" \
"s_add_u32 m0, s30, 0x8800\n" \
"s_mov_b32 s85, 0x4980\n" \
"v_mfma_f32_16x16x32_bf16 a[120:123], v[108:111], v[64:67], a[120:123]\n" \
"buffer_load_dwordx4 v24, s[76:79], s85 offen lds\n" \
"v_mfma_f32_16x16x32_bf16 a[120:123], v[172:175], v[128:131], a[120:123]\n" \
"ds_read_b64_tr_b16 v[154:155], v33 offset:4608\n" \
"v_mfma_f32_16x16x32_bf16 a[116:119], v[104:107], v[64:67], a[116:119]\n" \
"ds_read_b64_tr_b16 v[96:97], v35 offset:0\n" \
"v_mfma_f32_16x16x32_bf16 a[116:119], v[168:171], v[128:131], a[116:119]\n" \
"ds_read_b64_tr_b16 v[98:99], v35 offset:512\n" \
"v_mfma_f32_16x16x32_bf16 a[112:115], v[100:103], v[64:67], a[112:115]\n" \
"ds_read_b64_tr_b16 v[160:161], v35 offset:4096\n" \
"v_mfma_f32_16x16x32_bf16 a[112:115], v[164:167], v[128:131], a[112:115]\n" \
"ds_read_b128 v[64:67], v28 offset:6144\n" \
"v_mfma_f32_16x16x32_bf16 a[144:147], v[100:103], v[68:71], a[144:147]\n" \
"ds_read_b128 v[128:131], v29 offset:6144\n" \
"v_mfma_f32_16x16x32_bf16 a[144:147], v[164:167], v[132:135], a[144:147]\n" \
"s_add_u32 m0, s30, 0x8c00\n" \
"s_mov_b32 s84, 0x6d80\n" \
"v_mfma_f32_16x16x32_bf16 a[148:151], v[104:107], v[68:71], a[148:151]\n" \
"buffer_load_dwordx4 v25, s[76:79], s84 offen lds\n" \
"v_mfma_f32_16x16x32_bf16 a[148:151], v[168:171], v[132:135], a[148:151]\n" \
"s_add_u32 m0, s31, 0x8400\n" \
"s_mov_b32 s85, 0x188000\n" \
"v_mfma_f32_16x16x32_bf16 a[152:155], v[108:111], v[68:71], a[152:155]\n" \
"buffer_load_dwordx4 v26, s[68:71], s85 offen lds\n" \
"v_mfma_f32_16x16x32_bf16 a[152:155], v[172:175], v[132:135], a[152:155]\n" \
"ds_read_b64_tr_b16 v[162:163], v35 offset:4608\n" \
"v_mfma_f32_16x16x32_bf16 a[156:159], v[112:115], v[68:71], a[156:159]\n" \
"ds_read_b64_tr_b16 v[92:93], v34 offset:0\n" \
"v_mfma_f32_16x16x32_bf16 a[156:159], v[176:179], v[132:135], a[156:159]\n" \
"ds_read_b128 v[68:71], v28 offset:8192\n" \
"v_mfma_f32_16x16x32_bf16 a[108:111], v[112:115], v[72:75], a[108:111]\n" \
"ds_read_b128 v[132:135], v29 offset:8192\n" \
"v_mfma_f32_16x16x32_bf16 a[108:111], v[176:179], v[136:139], a[108:111]\n" \
"s_add_u32 m0, s30, 0x9000\n" \
"s_mov_b32 s84, 0x9180\n" \
"v_mfma_f32_16x16x32_bf16 a[104:107], v[108:111], v[72:75], a[104:107]\n" \
"buffer_load_dwordx4 v24, s[76:79], s84 offen lds\n" \
"v_mfma_f32_16x16x32_bf16 a[104:107], v[172:175], v[136:139], a[104:107]\n" \
"s_add_u32 m0, s30, 0x9400\n" \
"s_mov_b32 s85, 0xb580\n" \
"v_mfma_f32_16x16x32_bf16 a[76:79], v[104:107], v[72:75], a[76:79]\n" \
"buffer_load_dwordx4 v25, s[76:79], s85 offen lds\n" \
"v_mfma_f32_16x16x32_bf16 a[76:79], v[168:171], v[136:139], a[76:79]\n" \
"ds_read_b64_tr_b16 v[94:95], v34 offset:512\n" \
"v_mfma_f32_16x16x32_bf16 a[72:75], v[100:103], v[72:75], a[72:75]\n" \
"ds_read_b64_tr_b16 v[156:157], v34 offset:4096\n" \
"v_mfma_f32_16x16x32_bf16 a[72:75], v[164:167], v[136:139], a[72:75]\n" \
"ds_read_b128 v[72:75], v28 offset:10240\n" \
"v_mfma_f32_16x16x32_bf16 a[136:139], v[100:103], v[76:79], a[136:139]\n" \
"ds_read_b128 v[136:139], v29 offset:10240\n" \
"v_mfma_f32_16x16x32_bf16 a[136:139], v[164:167], v[140:143], a[136:139]\n" \
"s_add_u32 m0, s31, 0x8800\n" \
"s_mov_b32 s84, 0x190000\n" \
"v_mfma_f32_16x16x32_bf16 a[140:143], v[104:107], v[76:79], a[140:143]\n" \
"buffer_load_dwordx4 v27, s[68:71], s84 offen lds\n" \
"v_mfma_f32_16x16x32_bf16 a[140:143], v[168:171], v[140:143], a[140:143]\n" \
"s_add_u32 m0, s30, 0x9800\n" \
"s_mov_b32 s85, 0xd980\n" \
"v_mfma_f32_16x16x32_bf16 a[168:171], v[108:111], v[76:79], a[168:171]\n" \
"buffer_load_dwordx4 v24, s[76:79], s85 offen lds\n" \
"v_mfma_f32_16x16x32_bf16 a[168:171], v[172:175], v[140:143], a[168:171]\n" \
"ds_read_b64_tr_b16 v[158:159], v34 offset:4608\n" \
"v_mfma_f32_16x16x32_bf16 a[172:175], v[112:115], v[76:79], a[172:175]\n" \
"ds_read_b128 v[248:251], v22 offset:0\n" \
"v_mfma_f32_16x16x32_bf16 a[172:175], v[176:179], v[140:143], a[172:175]\n" \
"ds_read_b128 v[76:79], v28 offset:12288\n" \
"v_mfma_f32_16x16x32_bf16 a[236:239], v[112:115], v[80:83], a[236:239]\n" \
"ds_read_b128 v[140:143], v29 offset:12288\n" \
"v_mfma_f32_16x16x32_bf16 a[236:239], v[176:179], v[144:147], a[236:239]\n" \
"ds_read_b128 v[48:51], v23 offset:0\n" \
"v_mfma_f32_16x16x32_bf16 a[232:235], v[108:111], v[80:83], a[232:235]\n" \
"s_add_u32 s11, s22, s12\n" \
"s_addc_u32 s17, s23, 0\n" \
"v_mfma_f32_16x16x32_bf16 a[232:235], v[172:175], v[144:147], a[232:235]\n" \
"s_add_u32 s18, s24, s12\n" \
"v_writelane_b32 v11, s11, 9\n" \
"s_addc_u32 s19, s25, 0\n" \
"v_mfma_f32_16x16x32_bf16 a[204:207], v[104:107], v[80:83], a[204:207]\n" \
"s_and_b32 s20, s17, 0xffff\n" \
"v_writelane_b32 v11, s20, 10\n" \
"v_mfma_f32_16x16x32_bf16 a[204:207], v[168:171], v[144:147], a[204:207]\n" \
"s_and_b32 s20, s19, 0xffff\n" \
"v_writelane_b32 v11, s18, 11\n" \
"v_mfma_f32_16x16x32_bf16 a[200:203], v[100:103], v[80:83], a[200:203]\n" \
"s_cmpk_lt_u32 s3, 3\n" \
"s_cselect_b32 s11, s11, s18\n" \
"v_mfma_f32_16x16x32_bf16 a[200:203], v[164:167], v[144:147], a[200:203]\n" \
"ds_read_b128 v[80:83], v28 offset:14336\n" \
"ds_read_b128 v[144:147], v29 offset:14336\n" \
"s_waitcnt lgkmcnt(15)\n" \
"v_mfma_f32_16x16x32_bf16 a[0:3], v[84:87], v[52:55], a[0:3]\n" \
"s_add_u32 m0, s30, 0x9c00\n" \
"s_mov_b32 s85, 0xfd80\n" \
"v_mfma_f32_16x16x32_bf16 a[0:3], v[148:151], v[116:119], a[0:3]\n" \
"buffer_load_dwordx4 v25, s[76:79], s85 offen lds\n" \
"v_mfma_f32_16x16x32_bf16 a[4:7], v[88:91], v[52:55], a[4:7]\n" \
"ds_read_b64_tr_b16 v[100:101], v36 offset:0\n" \
"s_movk_i32 s18, 0xfff\n" \
"v_mfma_f32_16x16x32_bf16 a[4:7], v[152:155], v[116:119], a[4:7]\n" \
"s_add_u32 m0, s31, 0x8c00\n" \
"s_mov_b32 s84, 0x198000\n" \
"v_mfma_f32_16x16x32_bf16 a[36:39], v[96:99], v[52:55], a[36:39]\n" \
"buffer_load_dwordx4 v27, s[68:71], s84 offen lds\n" \
"s_waitcnt lgkmcnt(15)\n" \
"v_mfma_f32_16x16x32_bf16 a[36:39], v[160:163], v[116:119], a[36:39]\n" \
"ds_read_b64_tr_b16 v[102:103], v36 offset:1024\n" \
"v_writelane_b32 v11, s20, 12\n" \
"s_waitcnt lgkmcnt(12)\n" \
"v_mfma_f32_16x16x32_bf16 a[32:35], v[52:55], v[92:95], a[32:35]\n" \
"ds_read_b64_tr_b16 v[164:165], v36 offset:8192\n" \
"s_and_b32 s20, s29, 0xffff\n" \
"s_waitcnt lgkmcnt(9)\n" \
"s_nop 0\n" \
"v_mfma_f32_16x16x32_bf16 a[32:35], v[116:119], v[156:159], a[32:35]\n" \
"ds_read_b64_tr_b16 v[166:167], v36 offset:9216\n" \
"v_writelane_b32 v11, s28, 13\n" \
"v_mfma_f32_16x16x32_bf16 a[96:99], v[56:59], v[92:95], a[96:99]\n" \
"ds_read_b64_tr_b16 v[104:105], v37 offset:0\n" \
"v_writelane_b32 v11, s20, 14\n" \
"v_mfma_f32_16x16x32_bf16 a[96:99], v[120:123], v[156:159], a[96:99]\n" \
"ds_read_b64_tr_b16 v[106:107], v37 offset:1024\n" \
"v_writelane_b32 v11, s11, 15\n" \
"v_mfma_f32_16x16x32_bf16 a[100:103], v[96:99], v[56:59], a[100:103]\n" \
"ds_read_b64_tr_b16 v[168:169], v37 offset:8192\n" \
"v_mfma_f32_16x16x32_bf16 a[100:103], v[160:163], v[120:123], a[100:103]\n" \
"s_add_u32 s89, s41, 0x2000\n" \
"buffer_store_dwordx4 a[44:47], v20, s[80:83], s89 offen offset:3072 sc1\n" \
"v_mfma_f32_16x16x32_bf16 a[68:71], v[88:91], v[56:59], a[68:71]\n" \
"ds_read_b64_tr_b16 v[170:171], v37 offset:9216\n" \
"v_mfma_f32_16x16x32_bf16 a[68:71], v[152:155], v[120:123], a[68:71]\n" \
"ds_read_b64_tr_b16 v[108:109], v38 offset:0\n" \
"v_mfma_f32_16x16x32_bf16 a[64:67], v[84:87], v[56:59], a[64:67]\n" \
"ds_read_b64_tr_b16 v[110:111], v38 offset:1024\n" \
"v_mfma_f32_16x16x32_bf16 a[64:67], v[148:151], v[120:123], a[64:67]\n" \
"s_cmpk_lt_u32 s3, 3\n" \
"s_cselect_b32 s11, s17, s19\n" \
"v_mfma_f32_16x16x32_bf16 a[128:131], v[84:87], v[60:63], a[128:131]\n" \
"s_add_u32 s88, s41, 0x2000\n" \
"buffer_store_dwordx4 v[252:255], v20, s[80:83], s88 offen offset:2048 sc1\n" \
"v_mfma_f32_16x16x32_bf16 a[128:131], v[148:151], v[124:127], a[128:131]\n" \
"ds_read_b64_tr_b16 v[172:173], v38 offset:8192\n" \
"s_and_b32 s11, s11, 0xffff\n" \
"v_mfma_f32_16x16x32_bf16 a[132:135], v[88:91], v[60:63], a[132:135]\n" \
"ds_read_b64_tr_b16 v[174:175], v38 offset:9216\n" \
"v_writelane_b32 v11, s11, 16\n" \
"v_mfma_f32_16x16x32_bf16 a[132:135], v[152:155], v[124:127], a[132:135]\n" \
"ds_read_b64_tr_b16 v[112:113], v39 offset:0\n" \
"s_mul_i32 s11, s6, 0x48\n" \
"v_mfma_f32_16x16x32_bf16 a[164:167], v[96:99], v[60:63], a[164:167]\n" \
"s_add_i32 s17, s11, 0x47\n" \
"v_mfma_f32_16x16x32_bf16 a[164:167], v[160:163], v[124:127], a[164:167]\n" \
"ds_read_b64_tr_b16 v[114:115], v39 offset:1024\n" \
"v_mfma_f32_16x16x32_bf16 a[160:163], v[60:63], v[92:95], a[160:163]\n" \
"s_add_u32 s89, s41, 0x3000\n" \
"buffer_store_dwordx4 v[200:203], v20, s[80:83], s89 offen offset:2048 sc1\n" \
"v_mfma_f32_16x16x32_bf16 a[160:163], v[124:127], v[156:159], a[160:163]\n" \
"ds_read_b64_tr_b16 v[176:177], v39 offset:8192\n" \
"v_mfma_f32_16x16x32_bf16 a[224:227], v[64:67], v[92:95], a[224:227]\n" \
"ds_read_b64_tr_b16 v[178:179], v39 offset:9216\n" \
"v_mfma_f32_16x16x32_bf16 a[224:227], v[128:131], v[156:159], a[224:227]\n" \
"s_cmpk_lt_u32 s3, 3\n" \
"s_cselect_b32 s17, s17, s18\n" \
"v_mfma_f32_16x16x32_bf16 a[228:231], v[96:99], v[64:67], a[228:231]\n" \
"s_mulk_i32 s17, 0x480\n" \
"s_addk_i32 s17, 0x120\n" \
"v_mfma_f32_16x16x32_bf16 a[228:231], v[160:163], v[128:131], a[228:231]\n" \
"s_add_u32 s88, s41, 0x3000\n" \
"buffer_store_dwordx4 v[204:207], v20, s[80:83], s88 offen offset:3072 sc1\n" \
"v_mfma_f32_16x16x32_bf16 a[196:199], v[88:91], v[64:67], a[196:199]\n" \
"v_writelane_b32 v11, s17, 17\n" \
"s_lshl_b32 s17, s3, 5\n" \
"v_mfma_f32_16x16x32_bf16 a[196:199], v[152:155], v[128:131], a[196:199]\n" \
"s_add_i32 s11, s11, s17\n" \
"s_mul_i32 s17, s11, 0x480\n" \
"v_mfma_f32_16x16x32_bf16 a[192:195], v[84:87], v[64:67], a[192:195]\n" \
"s_mul_i32 s11, s13, 0x24000\n" \
"s_add_i32 s18, s11, 0x1b000\n" \
"v_mfma_f32_16x16x32_bf16 a[192:195], v[148:151], v[128:131], a[192:195]\n" \
"s_cmpk_lt_u32 s3, 3\n" \
"s_cselect_b32 s17, s17, s18\n" \
"s_waitcnt lgkmcnt(15)\n" \
"v_mfma_f32_16x16x32_bf16 a[8:11], v[88:91], v[248:251], a[8:11]\n" \
"s_mul_i32 s19, s3, 0x9000\n" \
"v_writelane_b32 v11, s17, 18\n" \
"v_mfma_f32_16x16x32_bf16 a[8:11], v[152:155], v[48:51], a[8:11]\n" \
"s_mul_i32 s17, s16, 0x2400\n" \
"s_mul_i32 s16, s16, 0x9000\n" \
"v_mfma_f32_16x16x32_bf16 a[40:43], v[96:99], v[248:251], a[40:43]\n" \
"s_bitset1_b32 s17, 17\n" \
"v_writelane_b32 v11, s17, 19\n" \
"v_mfma_f32_16x16x32_bf16 a[40:43], v[160:163], v[48:51], a[40:43]\n" \
"s_add_i32 s16, s11, s16\n" \
"v_writelane_b32 v11, s16, 25\n" \
"v_mfma_f32_16x16x32_bf16 a[12:15], v[248:251], v[92:95], a[12:15]\n" \
"s_mul_i32 s16, s3, 0x2400\n" \
"s_add_i32 s17, s16, 0x6000\n" \
"v_mfma_f32_16x16x32_bf16 a[12:15], v[48:51], v[156:159], a[12:15]\n" \
"s_waitcnt vmcnt(4) lgkmcnt(0)\n" \
"s_barrier\n" \
"v_mfma_f32_16x16x32_bf16 a[16:19], v[100:103], v[52:55], a[16:19]\n" \
"s_add_u32 m0, s40, 0x0\n" \
"s_add_u32 s85, s39, 0x8000\n" \
"v_mfma_f32_16x16x32_bf16 a[16:19], v[164:167], v[116:119], a[16:19]\n" \
"buffer_load_dwordx4 v20, s[80:83], s85 offen sc1 lds\n" \
"v_mfma_f32_16x16x32_bf16 a[20:23], v[104:107], v[52:55], a[20:23]\n" \
"ds_read_b64_tr_b16 v[84:85], v32 offset:32768\n" \
"v_writelane_b32 v11, s17, 20\n" \
"v_mfma_f32_16x16x32_bf16 a[20:23], v[168:171], v[116:119], a[20:23]\n" \
"ds_read_b64_tr_b16 v[86:87], v32 offset:33280\n" \
"s_or_b32 s17, s16, 0x10000\n" \
"v_mfma_f32_16x16x32_bf16 a[24:27], v[108:111], v[52:55], a[24:27]\n" \
"s_add_u32 m0, s40, 0x1000\n" \
"s_add_u32 s84, s39, 0x9000\n" \
"v_mfma_f32_16x16x32_bf16 a[24:27], v[172:175], v[116:119], a[24:27]\n" \
"buffer_load_dwordx4 v20, s[80:83], s84 offen sc1 lds\n" \
"v_mfma_f32_16x16x32_bf16 a[28:31], v[112:115], v[52:55], a[28:31]\n" \
"ds_read_b64_tr_b16 v[148:149], v32 offset:36864\n" \
"s_add_i32 s18, s16, 0x19000\n" \
"v_mfma_f32_16x16x32_bf16 a[28:31], v[176:179], v[116:119], a[28:31]\n" \
"ds_read_b128 v[52:55], v28 offset:32768\n" \
"v_writelane_b32 v11, s17, 21\n" \
"v_mfma_f32_16x16x32_bf16 a[60:63], v[112:115], v[56:59], a[60:63]\n" \
"ds_read_b128 v[116:119], v29 offset:32768\n" \
"s_mov_b32 s17, 0\n" \
"v_mfma_f32_16x16x32_bf16 a[60:63], v[176:179], v[120:123], a[60:63]\n" \
"s_add_u32 m0, s40, 0x3000\n" \
"s_mov_b32 s85, 0x200\n" \
"v_mfma_f32_16x16x32_bf16 a[56:59], v[108:111], v[56:59], a[56:59]\n" \
"buffer_load_dwordx4 v21, s[76:79], s85 offen lds\n" \
"v_mfma_f32_16x16x32_bf16 a[56:59], v[172:175], v[120:123], a[56:59]\n" \
"ds_read_b64_tr_b16 v[150:151], v32 offset:37376\n" \
"v_mfma_f32_16x16x32_bf16 a[52:55], v[104:107], v[56:59], a[52:55]\n" \
"s_add_u32 m0, s30, 0x0\n" \
"s_mov_b32 s84, 0x200\n" \
"v_mfma_f32_16x16x32_bf16 a[52:55], v[168:171], v[120:123], a[52:55]\n" \
"buffer_load_dwordx4 v24, s[76:79], s84 offen lds\n" \
"v_mfma_f32_16x16x32_bf16 a[48:51], v[100:103], v[56:59], a[48:51]\n" \
"ds_read_b64_tr_b16 v[88:89], v33 offset:32768\n" \
"v_mfma_f32_16x16x32_bf16 a[48:51], v[164:167], v[120:123], a[48:51]\n" \
"ds_read_b128 v[56:59], v28 offset:34816\n" \
"v_mfma_f32_16x16x32_bf16 a[80:83], v[100:103], v[60:63], a[80:83]\n" \
"ds_read_b128 v[120:123], v29 offset:34816\n" \
"v_mfma_f32_16x16x32_bf16 a[80:83], v[164:167], v[124:127], a[80:83]\n" \
"s_add_u32 m0, s30, 0x400\n" \
"s_mov_b32 s85, 0x2600\n" \
"v_mfma_f32_16x16x32_bf16 a[84:87], v[104:107], v[60:63], a[84:87]\n" \
"buffer_load_dwordx4 v25, s[76:79], s85 offen lds\n" \
"v_mfma_f32_16x16x32_bf16 a[84:87], v[168:171], v[124:127], a[84:87]\n" \
"ds_read_b64_tr_b16 v[90:91], v33 offset:33280\n" \
"v_mfma_f32_16x16x32_bf16 a[88:91], v[108:111], v[60:63], a[88:91]\n" \
"ds_read_b64_tr_b16 v[152:153], v33 offset:36864\n" \
"v_mfma_f32_16x16x32_bf16 a[88:91], v[172:175], v[124:127], a[88:91]\n" \
"s_add_u32 m0, s31, 0x0\n" \
"s_nop 0\n" \
"s_mov_b32 s84, 0x200000\n" \
"v_mfma_f32_16x16x32_bf16 a[92:95], v[112:115], v[60:63], a[92:95]\n" \
"buffer_load_dwordx4 v26, s[68:71], s84 offen lds\n" \
"v_mfma_f32_16x16x32_bf16 a[92:95], v[176:179], v[124:127], a[92:95]\n" \
"ds_read_b128 v[60:63], v28 offset:36864\n" \
"v_mfma_f32_16x16x32_bf16 a[124:127], v[112:115], v[64:67], a[124:127]\n" \
"ds_read_b128 v[124:127], v29 offset:36864\n" \
"v_mfma_f32_16x16x32_bf16 a[124:127], v[176:179], v[128:131], a[124:127]\n" \
"s_add_u32 m0, s30, 0x800\n" \
"s_mov_b32 s85, 0x4a00\n" \
"v_mfma_f32_16x16x32_bf16 a[120:123], v[108:111], v[64:67], a[120:123]\n" \
"buffer_load_dwordx4 v24, s[76:79], s85 offen lds\n" \
"v_mfma_f32_16x16x32_bf16 a[120:123], v[172:175], v[128:131], a[120:123]\n" \
"ds_read_b64_tr_b16 v[154:155], v33 offset:37376\n" \
"v_mfma_f32_16x16x32_bf16 a[116:119], v[104:107], v[64:67], a[116:119]\n" \
"ds_read_b64_tr_b16 v[96:97], v35 offset:32768\n" \
"v_mfma_f32_16x16x32_bf16 a[116:119], v[168:171], v[128:131], a[116:119]\n" \
"ds_read_b64_tr_b16 v[98:99], v35 offset:33280\n" \
"v_mfma_f32_16x16x32_bf16 a[112:115], v[100:103], v[64:67], a[112:115]\n" \
"ds_read_b64_tr_b16 v[160:161], v35 offset:36864\n" \
"v_mfma_f32_16x16x32_bf16 a[112:115], v[164:167], v[128:131], a[112:115]\n" \
"ds_read_b128 v[64:67], v28 offset:38912\n" \
"v_mfma_f32_16x16x32_bf16 a[144:147], v[100:103], v[68:71], a[144:147]\n" \
"ds_read_b128 v[128:131], v29 offset:38912\n" \
"v_mfma_f32_16x16x32_bf16 a[144:147], v[164:167], v[132:135], a[144:147]\n" \
"s_add_u32 m0, s30, 0xc00\n" \
"s_mov_b32 s84, 0x6e00\n" \
"v_mfma_f32_16x16x32_bf16 a[148:151], v[104:107], v[68:71], a[148:151]\n" \
"buffer_load_dwordx4 v25, s[76:79], s84 offen lds\n" \
"v_mfma_f32_16x16x32_bf16 a[148:151], v[168:171], v[132:135], a[148:151]\n" \
"s_add_u32 m0, s31, 0x400\n" \
"s_mov_b32 s85, 0x208000\n" \
"v_mfma_f32_16x16x32_bf16 a[152:155], v[108:111], v[68:71], a[152:155]\n" \
"buffer_load_dwordx4 v26, s[68:71], s85 offen lds\n" \
"v_mfma_f32_16x16x32_bf16 a[152:155], v[172:175], v[132:135], a[152:155]\n" \
"ds_read_b64_tr_b16 v[162:163], v35 offset:37376\n" \
"v_mfma_f32_16x16x32_bf16 a[156:159], v[112:115], v[68:71], a[156:159]\n" \
"ds_read_b64_tr_b16 v[92:93], v34 offset:32768\n" \
"v_mfma_f32_16x16x32_bf16 a[156:159], v[176:179], v[132:135], a[156:159]\n" \
"ds_read_b128 v[68:71], v28 offset:40960\n" \
"v_mfma_f32_16x16x32_bf16 a[108:111], v[112:115], v[72:75], a[108:111]\n" \
"ds_read_b128 v[132:135], v29 offset:40960\n" \
"v_mfma_f32_16x16x32_bf16 a[108:111], v[176:179], v[136:139], a[108:111]\n" \
"s_add_u32 m0, s30, 0x1000\n" \
"s_mov_b32 s84, 0x9200\n" \
"v_mfma_f32_16x16x32_bf16 a[104:107], v[108:111], v[72:75], a[104:107]\n" \
"buffer_load_dwordx4 v24, s[76:79], s84 offen lds\n" \
"v_mfma_f32_16x16x32_bf16 a[104:107], v[172:175], v[136:139], a[104:107]\n" \
"s_add_u32 m0, s30, 0x1400\n" \
"s_mov_b32 s85, 0xb600\n" \
"v_mfma_f32_16x16x32_bf16 a[76:79], v[104:107], v[72:75], a[76:79]\n" \
"buffer_load_dwordx4 v25, s[76:79], s85 offen lds\n" \
"v_mfma_f32_16x16x32_bf16 a[76:79], v[168:171], v[136:139], a[76:79]\n" \
"ds_read_b64_tr_b16 v[94:95], v34 offset:33280\n" \
"v_mfma_f32_16x16x32_bf16 a[72:75], v[100:103], v[72:75], a[72:75]\n" \
"ds_read_b64_tr_b16 v[156:157], v34 offset:36864\n" \
"v_mfma_f32_16x16x32_bf16 a[72:75], v[164:167], v[136:139], a[72:75]\n" \
"ds_read_b128 v[72:75], v28 offset:43008\n" \
"v_mfma_f32_16x16x32_bf16 a[136:139], v[100:103], v[76:79], a[136:139]\n" \
"ds_read_b128 v[136:139], v29 offset:43008\n" \
"v_mfma_f32_16x16x32_bf16 a[136:139], v[164:167], v[140:143], a[136:139]\n" \
"s_add_u32 m0, s31, 0x800\n" \
"s_mov_b32 s84, 0x210000\n" \
"v_mfma_f32_16x16x32_bf16 a[140:143], v[104:107], v[76:79], a[140:143]\n" \
"buffer_load_dwordx4 v27, s[68:71], s84 offen lds\n" \
"v_mfma_f32_16x16x32_bf16 a[140:143], v[168:171], v[140:143], a[140:143]\n" \
"s_add_u32 m0, s30, 0x1800\n" \
"s_mov_b32 s85, 0xda00\n" \
"v_mfma_f32_16x16x32_bf16 a[168:171], v[108:111], v[76:79], a[168:171]\n" \
"buffer_load_dwordx4 v24, s[76:79], s85 offen lds\n" \
"v_mfma_f32_16x16x32_bf16 a[168:171], v[172:175], v[140:143], a[168:171]\n" \
"ds_read_b64_tr_b16 v[158:159], v34 offset:37376\n" \
"v_mfma_f32_16x16x32_bf16 a[172:175], v[112:115], v[76:79], a[172:175]\n" \
"ds_read_b128 v[248:251], v22 offset:32768\n" \
"v_mfma_f32_16x16x32_bf16 a[172:175], v[176:179], v[140:143], a[172:175]\n" \
"ds_read_b128 v[76:79], v28 offset:45056\n" \
"v_mfma_f32_16x16x32_bf16 a[236:239], v[112:115], v[80:83], a[236:239]\n" \
"ds_read_b128 v[140:143], v29 offset:45056\n" \
"v_mfma_f32_16x16x32_bf16 a[236:239], v[176:179], v[144:147], a[236:239]\n" \
"ds_read_b128 v[48:51], v23 offset:32768\n" \
"v_mfma_f32_16x16x32_bf16 a[232:235], v[108:111], v[80:83], a[232:235]\n" \
"s_cmpk_lt_u32 s3, 3\n" \
"s_cselect_b32 s18, s18, s17\n" \
"v_mfma_f32_16x16x32_bf16 a[232:235], v[172:175], v[144:147], a[232:235]\n" \
"s_bitset1_b32 s16, 17\n" \
"v_writelane_b32 v11, s18, 22\n" \
"v_mfma_f32_16x16x32_bf16 a[204:207], v[104:107], v[80:83], a[204:207]\n" \
"s_movk_i32 s18, 0x2400\n" \
"v_mfma_f32_16x16x32_bf16 a[204:207], v[168:171], v[144:147], a[204:207]\n" \
"s_cmpk_lt_u32 s3, 3\n" \
"s_cselect_b32 s16, s16, s18\n" \
"v_mfma_f32_16x16x32_bf16 a[200:203], v[100:103], v[80:83], a[200:203]\n" \
"s_xor_b32 s13, s13, 2\n" \
"v_writelane_b32 v11, s16, 23\n" \
"v_mfma_f32_16x16x32_bf16 a[200:203], v[164:167], v[144:147], a[200:203]\n" \
"ds_read_b128 v[80:83], v28 offset:47104\n" \
"ds_read_b128 v[144:147], v29 offset:47104\n" \
"s_waitcnt lgkmcnt(15)\n" \
"v_mfma_f32_16x16x32_bf16 a[0:3], v[84:87], v[52:55], a[0:3]\n" \
"s_add_u32 m0, s30, 0x1c00\n" \
"s_mov_b32 s85, 0xfe00\n" \
"v_mfma_f32_16x16x32_bf16 a[0:3], v[148:151], v[116:119], a[0:3]\n" \
"buffer_load_dwordx4 v25, s[76:79], s85 offen lds\n" \
"v_mfma_f32_16x16x32_bf16 a[4:7], v[88:91], v[52:55], a[4:7]\n" \
"ds_read_b64_tr_b16 v[100:101], v36 offset:32768\n" \
"s_lshl_b32 s16, s3, 10\n" \
"v_mfma_f32_16x16x32_bf16 a[4:7], v[152:155], v[116:119], a[4:7]\n" \
"s_add_u32 m0, s31, 0xc00\n" \
"s_mov_b32 s84, 0x218000\n" \
"v_mfma_f32_16x16x32_bf16 a[36:39], v[96:99], v[52:55], a[36:39]\n" \
"buffer_load_dwordx4 v27, s[68:71], s84 offen lds\n" \
"s_waitcnt lgkmcnt(15)\n" \
"v_mfma_f32_16x16x32_bf16 a[36:39], v[160:163], v[116:119], a[36:39]\n" \
"ds_read_b64_tr_b16 v[102:103], v36 offset:33792\n" \
"s_or_b32 s16, s16, 0xf000\n" \
"s_waitcnt lgkmcnt(12)\n" \
"v_mfma_f32_16x16x32_bf16 a[32:35], v[52:55], v[92:95], a[32:35]\n" \
"ds_read_b64_tr_b16 v[164:165], v36 offset:40960\n" \
"v_writelane_b32 v11, s16, 24\n" \
"s_waitcnt lgkmcnt(9)\n" \
"v_mfma_f32_16x16x32_bf16 a[32:35], v[116:119], v[156:159], a[32:35]\n" \
"ds_read_b64_tr_b16 v[166:167], v36 offset:41984\n" \
"s_add_i32 s16, s15, 1\n" \
"v_mfma_f32_16x16x32_bf16 a[96:99], v[56:59], v[92:95], a[96:99]\n" \
"ds_read_b64_tr_b16 v[104:105], v37 offset:32768\n" \
"s_and_b32 s16, s16, 3\n" \
"v_mfma_f32_16x16x32_bf16 a[96:99], v[120:123], v[156:159], a[96:99]\n" \
"ds_read_b64_tr_b16 v[106:107], v37 offset:33792\n" \
"s_or_b32 s16, s16, s5\n" \
"v_mfma_f32_16x16x32_bf16 a[100:103], v[96:99], v[56:59], a[100:103]\n" \
"ds_read_b64_tr_b16 v[168:169], v37 offset:40960\n" \
"s_mul_i32 s18, s16, 0x24000\n" \
"v_mfma_f32_16x16x32_bf16 a[100:103], v[160:163], v[120:123], a[100:103]\n" \
"s_add_u32 s89, s42, 0x0\n" \
"buffer_store_dwordx4 v[216:219], v20, s[80:83], s89 offen offset:0 sc1\n" \
"v_mfma_f32_16x16x32_bf16 a[68:71], v[88:91], v[56:59], a[68:71]\n" \
"ds_read_b64_tr_b16 v[170:171], v37 offset:41984\n" \
"s_add_i32 s18, s18, s19\n" \
"v_mfma_f32_16x16x32_bf16 a[68:71], v[152:155], v[120:123], a[68:71]\n" \
"ds_read_b64_tr_b16 v[108:109], v38 offset:32768\n" \
"s_add_i32 s15, s15, -1\n" \
"v_mfma_f32_16x16x32_bf16 a[64:67], v[84:87], v[56:59], a[64:67]\n" \
"ds_read_b64_tr_b16 v[110:111], v38 offset:33792\n" \
"v_writelane_b32 v11, s18, 26\n" \
"v_mfma_f32_16x16x32_bf16 a[64:67], v[148:151], v[120:123], a[64:67]\n" \
"s_mul_i32 s18, s13, 0x24000\n" \
"s_and_b32 s15, s15, 3\n" \
"v_mfma_f32_16x16x32_bf16 a[128:131], v[84:87], v[60:63], a[128:131]\n" \
"s_add_u32 s88, s42, 0x0\n" \
"buffer_store_dwordx4 v[220:223], v20, s[80:83], s88 offen offset:1024 sc1\n" \
"v_mfma_f32_16x16x32_bf16 a[128:131], v[148:151], v[124:127], a[128:131]\n" \
".fill (L_pb + 28384 - .) / 4, 4, 0xBF800000\n" \
"L_pp7:\n" \
"s_cbranch_execz L_pret\n" \
"ds_read_b64_tr_b16 v[172:173], v38 offset:40960\n" \
"s_add_i32 s18, s18, s19\n" \
"v_mfma_f32_16x16x32_bf16 a[132:135], v[88:91], v[60:63], a[132:135]\n" \
"ds_read_b64_tr_b16 v[174:175], v38 offset:41984\n" \
"s_or_b32 s15, s15, s5\n" \
"v_mfma_f32_16x16x32_bf16 a[132:135], v[152:155], v[124:127], a[132:135]\n" \
"ds_read_b64_tr_b16 v[112:113], v39 offset:32768\n" \
"v_writelane_b32 v11, s18, 27\n" \
"v_mfma_f32_16x16x32_bf16 a[164:167], v[96:99], v[60:63], a[164:167]\n" \
"s_mul_i32 s18, s15, 0x24000\n" \
"s_add_i32 s18, s18, s19\n" \
"v_mfma_f32_16x16x32_bf16 a[164:167], v[160:163], v[124:127], a[164:167]\n" \
"ds_read_b64_tr_b16 v[114:115], v39 offset:33792\n" \
"v_writelane_b32 v11, s18, 28\n" \
"v_mfma_f32_16x16x32_bf16 a[160:163], v[60:63], v[92:95], a[160:163]\n" \
"s_add_u32 s89, s42, 0x1000\n" \
"buffer_store_dwordx4 v[224:227], v20, s[80:83], s89 offen offset:1024 sc1\n" \
"v_mfma_f32_16x16x32_bf16 a[160:163], v[124:127], v[156:159], a[160:163]\n" \
"ds_read_b64_tr_b16 v[176:177], v39 offset:40960\n" \
"s_or_b32 s18, s9, s14\n" \
"v_mfma_f32_16x16x32_bf16 a[224:227], v[64:67], v[92:95], a[224:227]\n" \
"ds_read_b64_tr_b16 v[178:179], v39 offset:41984\n" \
"s_mul_i32 s18, s18, 0x24800\n" \
"v_mfma_f32_16x16x32_bf16 a[224:227], v[128:131], v[156:159], a[224:227]\n" \
"s_lshl_b32 s16, s16, 12\n" \
"s_add_i32 s19, s18, 0x3004800\n" \
"v_mfma_f32_16x16x32_bf16 a[228:231], v[96:99], v[64:67], a[228:231]\n" \
"s_add_i32 s20, s19, s16\n" \
"v_writelane_b32 v11, s20, 29\n" \
"v_mfma_f32_16x16x32_bf16 a[228:231], v[160:163], v[128:131], a[228:231]\n" \
"s_add_u32 s88, s42, 0x1000\n" \
"buffer_store_dwordx4 v[228:231], v20, s[80:83], s88 offen offset:0 sc1\n" \
"v_mfma_f32_16x16x32_bf16 a[196:199], v[88:91], v[64:67], a[196:199]\n" \
"s_lshl_b32 s13, s13, 12\n" \
"s_lshl_b32 s15, s15, 12\n" \
"v_mfma_f32_16x16x32_bf16 a[196:199], v[152:155], v[128:131], a[196:199]\n" \
"s_sub_i32 s14, s9, s14\n" \
"s_add_i32 s20, s19, s13\n" \
"v_mfma_f32_16x16x32_bf16 a[192:195], v[84:87], v[64:67], a[192:195]\n" \
"v_writelane_b32 v11, s20, 30\n" \
"s_add_i32 s19, s19, s15\n" \
"v_mfma_f32_16x16x32_bf16 a[192:195], v[148:151], v[128:131], a[192:195]\n" \
"s_mul_i32 s14, s14, 0x24800\n" \
"v_writelane_b32 v11, s19, 31\n" \
"s_waitcnt lgkmcnt(15)\n" \
"v_mfma_f32_16x16x32_bf16 a[8:11], v[88:91], v[248:251], a[8:11]\n" \
"s_add_i32 s19, s14, 0x4120800\n" \
"s_add_i32 s16, s19, s16\n" \
"v_mfma_f32_16x16x32_bf16 a[8:11], v[152:155], v[48:51], a[8:11]\n" \
"v_writelane_b32 v11, s16, 32\n" \
"s_add_i32 s13, s19, s13\n" \
"v_mfma_f32_16x16x32_bf16 a[40:43], v[96:99], v[248:251], a[40:43]\n" \
"v_writelane_b32 v11, s13, 33\n" \
"s_add_i32 s19, s19, s15\n" \
"v_mfma_f32_16x16x32_bf16 a[40:43], v[160:163], v[48:51], a[40:43]\n" \
"s_or_b32 s13, s18, 0x3000000\n" \
"s_lshl_b32 s15, s6, 11\n" \
"v_mfma_f32_16x16x32_bf16 a[12:15], v[248:251], v[92:95], a[12:15]\n" \
"v_writelane_b32 v11, s19, 34\n" \
"s_add_i32 s16, s13, s15\n" \
"v_mfma_f32_16x16x32_bf16 a[12:15], v[48:51], v[156:159], a[12:15]\n" \
"s_waitcnt vmcnt(4) lgkmcnt(0)\n" \
"s_barrier\n" \
"v_mov_b32_e32 v45, s62\n" \
"s_mov_b64 exec, 1\n" \
"buffer_store_dword v45, off, s[80:83], s37 offset:0 sc1\n" \
"s_mov_b64 exec, -1\n" \
"s_nop 0\n" \
"v_mfma_f32_16x16x32_bf16 a[16:19], v[100:103], v[52:55], a[16:19]\n" \
"s_add_u32 m0, s40, 0x8000\n" \
"s_add_u32 s85, s39, 0xa000\n" \
"v_mfma_f32_16x16x32_bf16 a[16:19], v[164:167], v[116:119], a[16:19]\n" \
"buffer_load_dwordx4 v20, s[80:83], s85 offen sc1 lds\n" \
"v_mfma_f32_16x16x32_bf16 a[20:23], v[104:107], v[52:55], a[20:23]\n" \
"ds_read_b64_tr_b16 v[84:85], v32 offset:0\n" \
"v_accvgpr_read_b32 v204, a248\n" \
"v_mfma_f32_16x16x32_bf16 a[20:23], v[168:171], v[116:119], a[20:23]\n" \
"ds_read_b64_tr_b16 v[86:87], v32 offset:512\n" \
"v_accvgpr_read_b32 v205, a249\n" \
"v_mfma_f32_16x16x32_bf16 a[24:27], v[108:111], v[52:55], a[24:27]\n" \
"s_add_u32 m0, s40, 0x9000\n" \
"s_add_u32 s84, s39, 0xb000\n" \
"v_mfma_f32_16x16x32_bf16 a[24:27], v[172:175], v[116:119], a[24:27]\n" \
"buffer_load_dwordx4 v20, s[80:83], s84 offen sc1 lds\n" \
"v_mfma_f32_16x16x32_bf16 a[28:31], v[112:115], v[52:55], a[28:31]\n" \
"ds_read_b64_tr_b16 v[148:149], v32 offset:4096\n" \
"v_accvgpr_read_b32 v206, a250\n" \
"v_mfma_f32_16x16x32_bf16 a[28:31], v[176:179], v[116:119], a[28:31]\n" \
"ds_read_b128 v[52:55], v28 offset:0\n" \
"v_accvgpr_read_b32 v207, a251\n" \
"v_mfma_f32_16x16x32_bf16 a[60:63], v[112:115], v[56:59], a[60:63]\n" \
"ds_read_b128 v[116:119], v29 offset:0\n" \
"v_accvgpr_read_b32 v180, a252\n" \
"v_mfma_f32_16x16x32_bf16 a[60:63], v[176:179], v[120:123], a[60:63]\n" \
"s_add_u32 m0, s40, 0xb000\n" \
"s_mov_b32 s85, 0x280\n" \
"v_mfma_f32_16x16x32_bf16 a[56:59], v[108:111], v[56:59], a[56:59]\n" \
"buffer_load_dwordx4 v21, s[76:79], s85 offen lds\n" \
"v_mfma_f32_16x16x32_bf16 a[56:59], v[172:175], v[120:123], a[56:59]\n" \
"ds_read_b64_tr_b16 v[150:151], v32 offset:4608\n" \
"v_accvgpr_read_b32 v181, a253\n" \
"v_mfma_f32_16x16x32_bf16 a[52:55], v[104:107], v[56:59], a[52:55]\n" \
"s_add_u32 m0, s30, 0x8000\n" \
"s_mov_b32 s84, 0x280\n" \
"v_mfma_f32_16x16x32_bf16 a[52:55], v[168:171], v[120:123], a[52:55]\n" \
"buffer_load_dwordx4 v24, s[76:79], s84 offen lds\n" \
"v_mfma_f32_16x16x32_bf16 a[48:51], v[100:103], v[56:59], a[48:51]\n" \
"ds_read_b64_tr_b16 v[88:89], v33 offset:0\n" \
"v_accvgpr_read_b32 v182, a254\n" \
"v_mfma_f32_16x16x32_bf16 a[48:51], v[164:167], v[120:123], a[48:51]\n" \
"ds_read_b128 v[56:59], v28 offset:2048\n" \
"v_accvgpr_read_b32 v183, a255\n" \
"v_mfma_f32_16x16x32_bf16 a[80:83], v[100:103], v[60:63], a[80:83]\n" \
"ds_read_b128 v[120:123], v29 offset:2048\n" \
"v_cvt_pk_bf16_f32 v204, v204, v205\n" \
"v_mfma_f32_16x16x32_bf16 a[80:83], v[164:167], v[124:127], a[80:83]\n" \
"s_add_u32 m0, s30, 0x8400\n" \
"s_mov_b32 s85, 0x2680\n" \
"v_mfma_f32_16x16x32_bf16 a[84:87], v[104:107], v[60:63], a[84:87]\n" \
"buffer_load_dwordx4 v25, s[76:79], s85 offen lds\n" \
"v_mfma_f32_16x16x32_bf16 a[84:87], v[168:171], v[124:127], a[84:87]\n" \
"ds_read_b64_tr_b16 v[90:91], v33 offset:512\n" \
"v_cvt_pk_bf16_f32 v205, v206, v207\n" \
"v_mfma_f32_16x16x32_bf16 a[88:91], v[108:111], v[60:63], a[88:91]\n" \
"ds_read_b64_tr_b16 v[152:153], v33 offset:4096\n" \
"v_cvt_pk_bf16_f32 v206, v180, v181\n" \
"v_mfma_f32_16x16x32_bf16 a[88:91], v[172:175], v[124:127], a[88:91]\n" \
"s_add_u32 m0, s31, 0x8000\n" \
"s_mov_b32 s84, 0x280000\n" \
"v_mfma_f32_16x16x32_bf16 a[92:95], v[112:115], v[60:63], a[92:95]\n" \
"buffer_load_dwordx4 v26, s[68:71], s84 offen lds\n" \
"v_mfma_f32_16x16x32_bf16 a[92:95], v[176:179], v[124:127], a[92:95]\n" \
"ds_read_b128 v[60:63], v28 offset:4096\n" \
"v_cvt_pk_bf16_f32 v207, v182, v183\n" \
"v_mfma_f32_16x16x32_bf16 a[124:127], v[112:115], v[64:67], a[124:127]\n" \
"ds_read_b128 v[124:127], v29 offset:4096\n" \
"v_accvgpr_read_b32 v200, a240\n" \
"v_mfma_f32_16x16x32_bf16 a[124:127], v[176:179], v[128:131], a[124:127]\n" \
"s_add_u32 m0, s30, 0x8800\n" \
"s_mov_b32 s85, 0x4a80\n" \
"v_mfma_f32_16x16x32_bf16 a[120:123], v[108:111], v[64:67], a[120:123]\n" \
"buffer_load_dwordx4 v24, s[76:79], s85 offen lds\n" \
"v_mfma_f32_16x16x32_bf16 a[120:123], v[172:175], v[128:131], a[120:123]\n" \
"ds_read_b64_tr_b16 v[154:155], v33 offset:4608\n" \
"v_accvgpr_read_b32 v201, a241\n" \
"v_mfma_f32_16x16x32_bf16 a[116:119], v[104:107], v[64:67], a[116:119]\n" \
"ds_read_b64_tr_b16 v[96:97], v35 offset:0\n" \
"v_accvgpr_read_b32 v202, a242\n" \
"v_mfma_f32_16x16x32_bf16 a[116:119], v[168:171], v[128:131], a[116:119]\n" \
"ds_read_b64_tr_b16 v[98:99], v35 offset:512\n" \
"v_accvgpr_read_b32 v203, a243\n" \
"v_mfma_f32_16x16x32_bf16 a[112:115], v[100:103], v[64:67], a[112:115]\n" \
"ds_read_b64_tr_b16 v[160:161], v35 offset:4096\n" \
"v_accvgpr_read_b32 v180, a244\n" \
"v_mfma_f32_16x16x32_bf16 a[112:115], v[164:167], v[128:131], a[112:115]\n" \
"ds_read_b128 v[64:67], v28 offset:6144\n" \
"v_accvgpr_read_b32 v181, a245\n" \
"v_mfma_f32_16x16x32_bf16 a[144:147], v[100:103], v[68:71], a[144:147]\n" \
"ds_read_b128 v[128:131], v29 offset:6144\n" \
"v_accvgpr_read_b32 v182, a246\n" \
"v_mfma_f32_16x16x32_bf16 a[144:147], v[164:167], v[132:135], a[144:147]\n" \
"s_add_u32 m0, s30, 0x8c00\n" \
"s_mov_b32 s84, 0x6e80\n" \
"v_mfma_f32_16x16x32_bf16 a[148:151], v[104:107], v[68:71], a[148:151]\n" \
"buffer_load_dwordx4 v25, s[76:79], s84 offen lds\n" \
"v_mfma_f32_16x16x32_bf16 a[148:151], v[168:171], v[132:135], a[148:151]\n" \
"s_add_u32 m0, s31, 0x8400\n" \
"s_mov_b32 s85, 0x288000\n" \
"v_mfma_f32_16x16x32_bf16 a[152:155], v[108:111], v[68:71], a[152:155]\n" \
"buffer_load_dwordx4 v26, s[68:71], s85 offen lds\n" \
"v_mfma_f32_16x16x32_bf16 a[152:155], v[172:175], v[132:135], a[152:155]\n" \
"ds_read_b64_tr_b16 v[162:163], v35 offset:4608\n" \
"v_accvgpr_read_b32 v183, a247\n" \
"v_mfma_f32_16x16x32_bf16 a[156:159], v[112:115], v[68:71], a[156:159]\n" \
"ds_read_b64_tr_b16 v[92:93], v34 offset:0\n" \
"v_cvt_pk_bf16_f32 v200, v200, v201\n" \
"v_mfma_f32_16x16x32_bf16 a[156:159], v[176:179], v[132:135], a[156:159]\n" \
"ds_read_b128 v[68:71], v28 offset:8192\n" \
"v_cvt_pk_bf16_f32 v201, v202, v203\n" \
"v_mfma_f32_16x16x32_bf16 a[108:111], v[112:115], v[72:75], a[108:111]\n" \
"ds_read_b128 v[132:135], v29 offset:8192\n" \
"v_cvt_pk_bf16_f32 v202, v180, v181\n" \
"v_mfma_f32_16x16x32_bf16 a[108:111], v[176:179], v[136:139], a[108:111]\n" \
"s_add_u32 m0, s30, 0x9000\n" \
"s_mov_b32 s84, 0x9280\n" \
"v_mfma_f32_16x16x32_bf16 a[104:107], v[108:111], v[72:75], a[104:107]\n" \
"buffer_load_dwordx4 v24, s[76:79], s84 offen lds\n" \
"v_mfma_f32_16x16x32_bf16 a[104:107], v[172:175], v[136:139], a[104:107]\n" \
"s_add_u32 m0, s30, 0x9400\n" \
"s_mov_b32 s85, 0xb680\n" \
"v_mfma_f32_16x16x32_bf16 a[76:79], v[104:107], v[72:75], a[76:79]\n" \
"buffer_load_dwordx4 v25, s[76:79], s85 offen lds\n" \
"v_mfma_f32_16x16x32_bf16 a[76:79], v[168:171], v[136:139], a[76:79]\n" \
"ds_read_b64_tr_b16 v[94:95], v34 offset:512\n" \
"v_cvt_pk_bf16_f32 v203, v182, v183\n" \
"v_mfma_f32_16x16x32_bf16 a[72:75], v[100:103], v[72:75], a[72:75]\n" \
"ds_read_b64_tr_b16 v[156:157], v34 offset:4096\n" \
"v_accvgpr_read_b32 v196, a216\n" \
"v_mfma_f32_16x16x32_bf16 a[72:75], v[164:167], v[136:139], a[72:75]\n" \
"ds_read_b128 v[72:75], v28 offset:10240\n" \
"v_accvgpr_read_b32 v197, a217\n" \
"v_mfma_f32_16x16x32_bf16 a[136:139], v[100:103], v[76:79], a[136:139]\n" \
"ds_read_b128 v[136:139], v29 offset:10240\n" \
"v_accvgpr_read_b32 v198, a218\n" \
"v_mfma_f32_16x16x32_bf16 a[136:139], v[164:167], v[140:143], a[136:139]\n" \
"s_add_u32 m0, s31, 0x8800\n" \
"s_mov_b32 s84, 0x290000\n" \
"v_mfma_f32_16x16x32_bf16 a[140:143], v[104:107], v[76:79], a[140:143]\n" \
"buffer_load_dwordx4 v27, s[68:71], s84 offen lds\n" \
"v_mfma_f32_16x16x32_bf16 a[140:143], v[168:171], v[140:143], a[140:143]\n" \
"s_add_u32 m0, s30, 0x9800\n" \
"s_mov_b32 s85, 0xda80\n" \
"v_mfma_f32_16x16x32_bf16 a[168:171], v[108:111], v[76:79], a[168:171]\n" \
"buffer_load_dwordx4 v24, s[76:79], s85 offen lds\n" \
"v_mfma_f32_16x16x32_bf16 a[168:171], v[172:175], v[140:143], a[168:171]\n" \
"ds_read_b64_tr_b16 v[158:159], v34 offset:4608\n" \
"v_accvgpr_read_b32 v199, a219\n" \
"v_mfma_f32_16x16x32_bf16 a[172:175], v[112:115], v[76:79], a[172:175]\n" \
"ds_read_b128 v[248:251], v22 offset:0\n" \
"v_accvgpr_read_b32 v180, a220\n" \
"v_mfma_f32_16x16x32_bf16 a[172:175], v[176:179], v[140:143], a[172:175]\n" \
"ds_read_b128 v[76:79], v28 offset:12288\n" \
"v_accvgpr_read_b32 v181, a221\n" \
"v_mfma_f32_16x16x32_bf16 a[236:239], v[112:115], v[80:83], a[236:239]\n" \
"ds_read_b128 v[140:143], v29 offset:12288\n" \
"v_accvgpr_read_b32 v182, a222\n" \
"v_mfma_f32_16x16x32_bf16 a[236:239], v[176:179], v[144:147], a[236:239]\n" \
"ds_read_b128 v[48:51], v23 offset:0\n" \
"v_accvgpr_read_b32 v183, a223\n" \
"v_mfma_f32_16x16x32_bf16 a[232:235], v[108:111], v[80:83], a[232:235]\n" \
"v_cvt_pk_bf16_f32 v196, v196, v197\n" \
"v_cvt_pk_bf16_f32 v197, v198, v199\n" \
"v_mfma_f32_16x16x32_bf16 a[232:235], v[172:175], v[144:147], a[232:235]\n" \
"v_cvt_pk_bf16_f32 v198, v180, v181\n" \
"v_cvt_pk_bf16_f32 v199, v182, v183\n" \
"v_mfma_f32_16x16x32_bf16 a[204:207], v[104:107], v[80:83], a[204:207]\n" \
"v_accvgpr_read_b32 v192, a208\n" \
"v_accvgpr_read_b32 v193, a209\n" \
"v_mfma_f32_16x16x32_bf16 a[204:207], v[168:171], v[144:147], a[204:207]\n" \
"v_accvgpr_read_b32 v194, a210\n" \
"v_accvgpr_read_b32 v195, a211\n" \
"v_mfma_f32_16x16x32_bf16 a[200:203], v[100:103], v[80:83], a[200:203]\n" \
"v_accvgpr_read_b32 v180, a212\n" \
"v_accvgpr_read_b32 v181, a213\n" \
"v_mfma_f32_16x16x32_bf16 a[200:203], v[164:167], v[144:147], a[200:203]\n" \
"ds_read_b128 v[80:83], v28 offset:14336\n" \
"ds_read_b128 v[144:147], v29 offset:14336\n" \
"s_waitcnt lgkmcnt(15)\n" \
"s_nop 0\n" \
"v_mfma_f32_16x16x32_bf16 a[0:3], v[84:87], v[52:55], a[0:3]\n" \
"s_add_u32 m0, s30, 0x9c00\n" \
"s_mov_b32 s85, 0xfe80\n" \
"v_mfma_f32_16x16x32_bf16 a[0:3], v[148:151], v[116:119], a[0:3]\n" \
"buffer_load_dwordx4 v25, s[76:79], s85 offen lds\n" \
"v_mfma_f32_16x16x32_bf16 a[4:7], v[88:91], v[52:55], a[4:7]\n" \
"ds_read_b64_tr_b16 v[100:101], v36 offset:0\n" \
"v_accvgpr_read_b32 v182, a214\n" \
"v_mfma_f32_16x16x32_bf16 a[4:7], v[152:155], v[116:119], a[4:7]\n" \
"s_add_u32 m0, s31, 0x8c00\n" \
"s_mov_b32 s84, 0x298000\n" \
"v_mfma_f32_16x16x32_bf16 a[36:39], v[96:99], v[52:55], a[36:39]\n" \
"buffer_load_dwordx4 v27, s[68:71], s84 offen lds\n" \
"s_waitcnt lgkmcnt(15)\n" \
"v_mfma_f32_16x16x32_bf16 a[36:39], v[160:163], v[116:119], a[36:39]\n" \
"ds_read_b64_tr_b16 v[102:103], v36 offset:1024\n" \
"v_accvgpr_read_b32 v183, a215\n" \
"s_waitcnt lgkmcnt(12)\n" \
"v_mfma_f32_16x16x32_bf16 a[32:35], v[52:55], v[92:95], a[32:35]\n" \
"ds_read_b64_tr_b16 v[164:165], v36 offset:8192\n" \
"v_cvt_pk_bf16_f32 v192, v192, v193\n" \
"s_waitcnt lgkmcnt(9)\n" \
"s_nop 0\n" \
"v_mfma_f32_16x16x32_bf16 a[32:35], v[116:119], v[156:159], a[32:35]\n" \
"ds_read_b64_tr_b16 v[166:167], v36 offset:9216\n" \
"v_cvt_pk_bf16_f32 v193, v194, v195\n" \
"v_mfma_f32_16x16x32_bf16 a[96:99], v[56:59], v[92:95], a[96:99]\n" \
"ds_read_b64_tr_b16 v[104:105], v37 offset:0\n" \
"v_cvt_pk_bf16_f32 v194, v180, v181\n" \
"v_mfma_f32_16x16x32_bf16 a[96:99], v[120:123], v[156:159], a[96:99]\n" \
"ds_read_b64_tr_b16 v[106:107], v37 offset:1024\n" \
"v_cvt_pk_bf16_f32 v195, v182, v183\n" \
"v_mfma_f32_16x16x32_bf16 a[100:103], v[96:99], v[56:59], a[100:103]\n" \
"ds_read_b64_tr_b16 v[168:169], v37 offset:8192\n" \
"v_accvgpr_read_b32 v188, a184\n" \
"v_mfma_f32_16x16x32_bf16 a[100:103], v[160:163], v[120:123], a[100:103]\n" \
"ds_read_b64_tr_b16 v[170:171], v37 offset:9216\n" \
"v_accvgpr_read_b32 v189, a185\n" \
"v_mfma_f32_16x16x32_bf16 a[68:71], v[88:91], v[56:59], a[68:71]\n" \
"v_accvgpr_read_b32 v190, a186\n" \
"v_accvgpr_read_b32 v191, a187\n" \
"v_mfma_f32_16x16x32_bf16 a[68:71], v[152:155], v[120:123], a[68:71]\n" \
"s_add_u32 s89, s42, 0x2000\n" \
"buffer_store_dwordx4 v[232:235], v20, s[80:83], s89 offen offset:0 sc1\n" \
"v_mfma_f32_16x16x32_bf16 a[64:67], v[84:87], v[56:59], a[64:67]\n" \
"ds_read_b64_tr_b16 v[108:109], v38 offset:0\n" \
"v_accvgpr_read_b32 v180, a188\n" \
"v_mfma_f32_16x16x32_bf16 a[64:67], v[148:151], v[120:123], a[64:67]\n" \
"ds_read_b64_tr_b16 v[110:111], v38 offset:1024\n" \
"v_accvgpr_read_b32 v181, a189\n" \
"v_mfma_f32_16x16x32_bf16 a[128:131], v[84:87], v[60:63], a[128:131]\n" \
"ds_read_b64_tr_b16 v[172:173], v38 offset:8192\n" \
"v_accvgpr_read_b32 v182, a190\n" \
"v_mfma_f32_16x16x32_bf16 a[128:131], v[148:151], v[124:127], a[128:131]\n" \
"ds_read_b64_tr_b16 v[174:175], v38 offset:9216\n" \
"v_accvgpr_read_b32 v183, a191\n" \
"v_mfma_f32_16x16x32_bf16 a[132:135], v[88:91], v[60:63], a[132:135]\n" \
"v_cvt_pk_bf16_f32 v188, v188, v189\n" \
"v_cvt_pk_bf16_f32 v189, v190, v191\n" \
"v_mfma_f32_16x16x32_bf16 a[132:135], v[152:155], v[124:127], a[132:135]\n" \
"s_add_u32 s88, s42, 0x2000\n" \
"buffer_store_dwordx4 v[236:239], v20, s[80:83], s88 offen offset:1024 sc1\n" \
"v_mfma_f32_16x16x32_bf16 a[164:167], v[96:99], v[60:63], a[164:167]\n" \
"ds_read_b64_tr_b16 v[112:113], v39 offset:0\n" \
"v_cvt_pk_bf16_f32 v190, v180, v181\n" \
"v_mfma_f32_16x16x32_bf16 a[164:167], v[160:163], v[124:127], a[164:167]\n" \
"ds_read_b64_tr_b16 v[114:115], v39 offset:1024\n" \
"v_cvt_pk_bf16_f32 v191, v182, v183\n" \
"v_mfma_f32_16x16x32_bf16 a[160:163], v[60:63], v[92:95], a[160:163]\n" \
"ds_read_b64_tr_b16 v[176:177], v39 offset:8192\n" \
"v_accvgpr_read_b32 v184, a176\n" \
"v_mfma_f32_16x16x32_bf16 a[160:163], v[124:127], v[156:159], a[160:163]\n" \
"v_accvgpr_read_b32 v185, a177\n" \
"v_accvgpr_read_b32 v186, a178\n" \
"v_mfma_f32_16x16x32_bf16 a[224:227], v[64:67], v[92:95], a[224:227]\n" \
"ds_read_b64_tr_b16 v[178:179], v39 offset:9216\n" \
"v_accvgpr_read_b32 v187, a179\n" \
"v_mfma_f32_16x16x32_bf16 a[224:227], v[128:131], v[156:159], a[224:227]\n" \
"v_accvgpr_read_b32 v180, a180\n" \
"v_accvgpr_read_b32 v181, a181\n" \
"v_mfma_f32_16x16x32_bf16 a[228:231], v[96:99], v[64:67], a[228:231]\n" \
"s_add_u32 s89, s42, 0x3000\n" \
"buffer_store_dwordx4 v[240:243], v20, s[80:83], s89 offen offset:1024 sc1\n" \
"v_mfma_f32_16x16x32_bf16 a[228:231], v[160:163], v[128:131], a[228:231]\n" \
"v_accvgpr_read_b32 v182, a182\n" \
"v_accvgpr_read_b32 v183, a183\n" \
"v_mfma_f32_16x16x32_bf16 a[196:199], v[88:91], v[64:67], a[196:199]\n" \
"v_cvt_pk_bf16_f32 v184, v184, v185\n" \
"v_cvt_pk_bf16_f32 v185, v186, v187\n" \
"v_mfma_f32_16x16x32_bf16 a[196:199], v[152:155], v[128:131], a[196:199]\n" \
"v_cvt_pk_bf16_f32 v186, v180, v181\n" \
"v_cvt_pk_bf16_f32 v187, v182, v183\n" \
"v_mfma_f32_16x16x32_bf16 a[192:195], v[84:87], v[64:67], a[192:195]\n" \
"v_writelane_b32 v11, s16, 35\n" \
"s_add_i32 s16, s14, 0x411c000\n" \
"v_mfma_f32_16x16x32_bf16 a[192:195], v[148:151], v[128:131], a[192:195]\n" \
"s_add_i32 s19, s16, s15\n" \
"v_writelane_b32 v11, s19, 36\n" \
"s_waitcnt lgkmcnt(15)\n" \
"v_mfma_f32_16x16x32_bf16 a[8:11], v[88:91], v[248:251], a[8:11]\n" \
"s_add_i32 s18, s18, 0x3004000\n" \
"v_writelane_b32 v11, s18, 37\n" \
"v_mfma_f32_16x16x32_bf16 a[8:11], v[152:155], v[48:51], a[8:11]\n" \
"s_add_i32 s14, s14, 0x4120000\n" \
"v_writelane_b32 v11, s14, 38\n" \
"v_mfma_f32_16x16x32_bf16 a[40:43], v[96:99], v[248:251], a[40:43]\n" \
"s_lshl_b32 s14, s4, 2\n" \
"s_or_b32 s8, s14, s8\n" \
"v_mfma_f32_16x16x32_bf16 a[40:43], v[160:163], v[48:51], a[40:43]\n" \
"s_mul_i32 s8, s8, 0x90000\n" \
"s_lshl_b32 s14, s3, 14\n" \
"v_mfma_f32_16x16x32_bf16 a[12:15], v[248:251], v[92:95], a[12:15]\n" \
"s_add_i32 s8, s8, s14\n" \
"s_lshl_b32 s9, s9, 4\n" \
"v_mfma_f32_16x16x32_bf16 a[12:15], v[48:51], v[156:159], a[12:15]\n" \
"s_waitcnt vmcnt(3) lgkmcnt(0)\n" \
"s_barrier\n" \
"v_mfma_f32_16x16x32_bf16 a[16:19], v[100:103], v[52:55], a[16:19]\n" \
"s_add_u32 m0, s40, 0x0\n" \
"s_add_u32 s84, s39, 0xc000\n" \
"v_mfma_f32_16x16x32_bf16 a[16:19], v[164:167], v[116:119], a[16:19]\n" \
"buffer_load_dwordx4 v20, s[80:83], s84 offen sc1 lds\n" \
"v_mfma_f32_16x16x32_bf16 a[20:23], v[104:107], v[52:55], a[20:23]\n" \
"ds_read_b64_tr_b16 v[84:85], v32 offset:32768\n" \
"s_lshl_b32 s14, s4, 7\n" \
"v_mfma_f32_16x16x32_bf16 a[20:23], v[168:171], v[116:119], a[20:23]\n" \
"ds_read_b64_tr_b16 v[86:87], v32 offset:33280\n" \
"s_or_b32 s18, s9, s14\n" \
"v_mfma_f32_16x16x32_bf16 a[24:27], v[108:111], v[52:55], a[24:27]\n" \
"s_add_u32 m0, s40, 0x1000\n" \
"s_add_u32 s85, s39, 0xd000\n" \
"v_mfma_f32_16x16x32_bf16 a[24:27], v[172:175], v[116:119], a[24:27]\n" \
"buffer_load_dwordx4 v20, s[80:83], s85 offen sc1 lds\n" \
"v_mfma_f32_16x16x32_bf16 a[28:31], v[112:115], v[52:55], a[28:31]\n" \
"ds_read_b64_tr_b16 v[148:149], v32 offset:36864\n" \
"s_sub_i32 s9, s9, s14\n" \
"v_mfma_f32_16x16x32_bf16 a[28:31], v[176:179], v[116:119], a[28:31]\n" \
"ds_read_b128 v[52:55], v28 offset:32768\n" \
"s_mulk_i32 s18, 0x480\n" \
"v_mfma_f32_16x16x32_bf16 a[60:63], v[112:115], v[56:59], a[60:63]\n" \
"ds_read_b128 v[116:119], v29 offset:32768\n" \
"s_mulk_i32 s9, 0x480\n" \
"v_mfma_f32_16x16x32_bf16 a[60:63], v[176:179], v[120:123], a[60:63]\n" \
"s_add_u32 m0, s40, 0x3000\n" \
"s_mov_b32 s84, 0x300\n" \
"v_mfma_f32_16x16x32_bf16 a[56:59], v[108:111], v[56:59], a[56:59]\n" \
"buffer_load_dwordx4 v21, s[76:79], s84 offen lds\n" \
"v_mfma_f32_16x16x32_bf16 a[56:59], v[172:175], v[120:123], a[56:59]\n" \
"ds_read_b64_tr_b16 v[150:151], v32 offset:37376\n" \
"s_or_b32 s14, s18, s12\n" \
"v_mfma_f32_16x16x32_bf16 a[52:55], v[104:107], v[56:59], a[52:55]\n" \
"s_add_u32 m0, s30, 0x0\n" \
"s_mov_b32 s85, 0x300\n" \
"v_mfma_f32_16x16x32_bf16 a[52:55], v[168:171], v[120:123], a[52:55]\n" \
"buffer_load_dwordx4 v24, s[76:79], s85 offen lds\n" \
"v_mfma_f32_16x16x32_bf16 a[48:51], v[100:103], v[56:59], a[48:51]\n" \
"ds_read_b64_tr_b16 v[88:89], v33 offset:32768\n" \
"s_or_b32 s9, s9, s12\n" \
"v_mfma_f32_16x16x32_bf16 a[48:51], v[164:167], v[120:123], a[48:51]\n" \
"ds_read_b128 v[56:59], v28 offset:34816\n" \
"s_and_b32 s12, s2, 1\n" \
"s_nop 0\n" \
"v_mfma_f32_16x16x32_bf16 a[80:83], v[100:103], v[60:63], a[80:83]\n" \
"ds_read_b128 v[120:123], v29 offset:34816\n" \
"v_mfma_f32_16x16x32_bf16 a[80:83], v[164:167], v[124:127], a[80:83]\n" \
"s_add_u32 m0, s30, 0x400\n" \
"s_mov_b32 s84, 0x2700\n" \
"v_mfma_f32_16x16x32_bf16 a[84:87], v[104:107], v[60:63], a[84:87]\n" \
"buffer_load_dwordx4 v25, s[76:79], s84 offen lds\n" \
"v_mfma_f32_16x16x32_bf16 a[84:87], v[168:171], v[124:127], a[84:87]\n" \
"ds_read_b64_tr_b16 v[90:91], v33 offset:33280\n" \
"v_mfma_f32_16x16x32_bf16 a[88:91], v[108:111], v[60:63], a[88:91]\n" \
"ds_read_b64_tr_b16 v[152:153], v33 offset:36864\n" \
"v_mfma_f32_16x16x32_bf16 a[88:91], v[172:175], v[124:127], a[88:91]\n" \
"s_add_u32 m0, s31, 0x0\n" \
"s_nop 0\n" \
"s_mov_b32 s85, 0x300000\n" \
"v_mfma_f32_16x16x32_bf16 a[92:95], v[112:115], v[60:63], a[92:95]\n" \
"buffer_load_dwordx4 v26, s[68:71], s85 offen lds\n" \
"v_mfma_f32_16x16x32_bf16 a[92:95], v[176:179], v[124:127], a[92:95]\n" \
"ds_read_b128 v[60:63], v28 offset:36864\n" \
"v_mfma_f32_16x16x32_bf16 a[124:127], v[112:115], v[64:67], a[124:127]\n" \
"ds_read_b128 v[124:127], v29 offset:36864\n" \
"v_mfma_f32_16x16x32_bf16 a[124:127], v[176:179], v[128:131], a[124:127]\n" \
"s_add_u32 m0, s30, 0x800\n" \
"s_mov_b32 s84, 0x4b00\n" \
"v_mfma_f32_16x16x32_bf16 a[120:123], v[108:111], v[64:67], a[120:123]\n" \
"buffer_load_dwordx4 v24, s[76:79], s84 offen lds\n" \
"v_mfma_f32_16x16x32_bf16 a[120:123], v[172:175], v[128:131], a[120:123]\n" \
"ds_read_b64_tr_b16 v[154:155], v33 offset:37376\n" \
"v_mfma_f32_16x16x32_bf16 a[116:119], v[104:107], v[64:67], a[116:119]\n" \
"ds_read_b64_tr_b16 v[96:97], v35 offset:32768\n" \
"v_mfma_f32_16x16x32_bf16 a[116:119], v[168:171], v[128:131], a[116:119]\n" \
"ds_read_b64_tr_b16 v[98:99], v35 offset:33280\n" \
"v_mfma_f32_16x16x32_bf16 a[112:115], v[100:103], v[64:67], a[112:115]\n" \
"ds_read_b64_tr_b16 v[160:161], v35 offset:36864\n" \
"v_mfma_f32_16x16x32_bf16 a[112:115], v[164:167], v[128:131], a[112:115]\n" \
"ds_read_b128 v[64:67], v28 offset:38912\n" \
"v_mfma_f32_16x16x32_bf16 a[144:147], v[100:103], v[68:71], a[144:147]\n" \
"ds_read_b128 v[128:131], v29 offset:38912\n" \
"v_mfma_f32_16x16x32_bf16 a[144:147], v[164:167], v[132:135], a[144:147]\n" \
"s_add_u32 m0, s30, 0xc00\n" \
"s_mov_b32 s85, 0x6f00\n" \
"v_mfma_f32_16x16x32_bf16 a[148:151], v[104:107], v[68:71], a[148:151]\n" \
"buffer_load_dwordx4 v25, s[76:79], s85 offen lds\n" \
"v_mfma_f32_16x16x32_bf16 a[148:151], v[168:171], v[132:135], a[148:151]\n" \
"s_add_u32 m0, s31, 0x400\n" \
"s_mov_b32 s84, 0x308000\n" \
"v_mfma_f32_16x16x32_bf16 a[152:155], v[108:111], v[68:71], a[152:155]\n" \
"buffer_load_dwordx4 v26, s[68:71], s84 offen lds\n" \
"v_mfma_f32_16x16x32_bf16 a[152:155], v[172:175], v[132:135], a[152:155]\n" \
"ds_read_b64_tr_b16 v[162:163], v35 offset:37376\n" \
"v_mfma_f32_16x16x32_bf16 a[156:159], v[112:115], v[68:71], a[156:159]\n" \
"ds_read_b64_tr_b16 v[92:93], v34 offset:32768\n" \
"v_mfma_f32_16x16x32_bf16 a[156:159], v[176:179], v[132:135], a[156:159]\n" \
"ds_read_b128 v[68:71], v28 offset:40960\n" \
"v_mfma_f32_16x16x32_bf16 a[108:111], v[112:115], v[72:75], a[108:111]\n" \
"ds_read_b128 v[132:135], v29 offset:40960\n" \
"v_mfma_f32_16x16x32_bf16 a[108:111], v[176:179], v[136:139], a[108:111]\n" \
"s_add_u32 m0, s30, 0x1000\n" \
"s_mov_b32 s85, 0x9300\n" \
"v_mfma_f32_16x16x32_bf16 a[104:107], v[108:111], v[72:75], a[104:107]\n" \
"buffer_load_dwordx4 v24, s[76:79], s85 offen lds\n" \
"v_mfma_f32_16x16x32_bf16 a[104:107], v[172:175], v[136:139], a[104:107]\n" \
"s_add_u32 m0, s30, 0x1400\n" \
"s_mov_b32 s84, 0xb700\n" \
"v_mfma_f32_16x16x32_bf16 a[76:79], v[104:107], v[72:75], a[76:79]\n" \
"buffer_load_dwordx4 v25, s[76:79], s84 offen lds\n" \
"v_mfma_f32_16x16x32_bf16 a[76:79], v[168:171], v[136:139], a[76:79]\n" \
"ds_read_b64_tr_b16 v[94:95], v34 offset:33280\n" \
"v_mfma_f32_16x16x32_bf16 a[72:75], v[100:103], v[72:75], a[72:75]\n" \
"ds_read_b64_tr_b16 v[156:157], v34 offset:36864\n" \
"v_mfma_f32_16x16x32_bf16 a[72:75], v[164:167], v[136:139], a[72:75]\n" \
"ds_read_b128 v[72:75], v28 offset:43008\n" \
"v_mfma_f32_16x16x32_bf16 a[136:139], v[100:103], v[76:79], a[136:139]\n" \
"ds_read_b128 v[136:139], v29 offset:43008\n" \
"v_mfma_f32_16x16x32_bf16 a[136:139], v[164:167], v[140:143], a[136:139]\n" \
"s_add_u32 m0, s31, 0x800\n" \
"s_mov_b32 s85, 0x310000\n" \
"v_mfma_f32_16x16x32_bf16 a[140:143], v[104:107], v[76:79], a[140:143]\n" \
"buffer_load_dwordx4 v27, s[68:71], s85 offen lds\n" \
"v_mfma_f32_16x16x32_bf16 a[140:143], v[168:171], v[140:143], a[140:143]\n" \
"s_add_u32 m0, s30, 0x1800\n" \
"s_mov_b32 s84, 0xdb00\n" \
"v_mfma_f32_16x16x32_bf16 a[168:171], v[108:111], v[76:79], a[168:171]\n" \
"buffer_load_dwordx4 v24, s[76:79], s84 offen lds\n" \
"v_mfma_f32_16x16x32_bf16 a[168:171], v[172:175], v[140:143], a[168:171]\n" \
"ds_read_b64_tr_b16 v[158:159], v34 offset:37376\n" \
"v_mfma_f32_16x16x32_bf16 a[172:175], v[112:115], v[76:79], a[172:175]\n" \
"ds_read_b128 v[248:251], v22 offset:32768\n" \
"v_mfma_f32_16x16x32_bf16 a[172:175], v[176:179], v[140:143], a[172:175]\n" \
"ds_read_b128 v[76:79], v28 offset:45056\n" \
"v_mfma_f32_16x16x32_bf16 a[236:239], v[112:115], v[80:83], a[236:239]\n" \
"ds_read_b128 v[140:143], v29 offset:45056\n" \
"v_mfma_f32_16x16x32_bf16 a[236:239], v[176:179], v[144:147], a[236:239]\n" \
"ds_read_b128 v[48:51], v23 offset:32768\n" \
"v_mfma_f32_16x16x32_bf16 a[232:235], v[108:111], v[80:83], a[232:235]\n" \
"s_cmp_lg_u32 s12, 0\n" \
"s_cselect_b32 s13, s16, s13\n" \
"v_mfma_f32_16x16x32_bf16 a[232:235], v[172:175], v[144:147], a[232:235]\n" \
"s_bfe_u32 s16, s2, 0x20001\n" \
"s_addk_i32 s13, 0x4000\n" \
"v_mfma_f32_16x16x32_bf16 a[204:207], v[104:107], v[80:83], a[204:207]\n" \
"s_xor_b32 s16, s16, s3\n" \
"v_mfma_f32_16x16x32_bf16 a[204:207], v[168:171], v[144:147], a[204:207]\n" \
"s_cmp_lg_u32 s16, 0\n" \
"s_cselect_b32 s13, s7, s13\n" \
"v_mfma_f32_16x16x32_bf16 a[200:203], v[100:103], v[80:83], a[200:203]\n" \
"s_lshl_b32 s6, s6, 16\n" \
"v_writelane_b32 v11, s13, 39\n" \
"v_mfma_f32_16x16x32_bf16 a[200:203], v[164:167], v[144:147], a[200:203]\n" \
"ds_read_b128 v[80:83], v28 offset:47104\n" \
"ds_read_b128 v[144:147], v29 offset:47104\n" \
"s_waitcnt lgkmcnt(15)\n" \
"v_mfma_f32_16x16x32_bf16 a[0:3], v[84:87], v[52:55], a[0:3]\n" \
"s_add_u32 m0, s30, 0x1c00\n" \
"s_mov_b32 s84, 0xff00\n" \
"v_mfma_f32_16x16x32_bf16 a[0:3], v[148:151], v[116:119], a[0:3]\n" \
"buffer_load_dwordx4 v25, s[76:79], s84 offen lds\n" \
"v_mfma_f32_16x16x32_bf16 a[4:7], v[88:91], v[52:55], a[4:7]\n" \
"ds_read_b64_tr_b16 v[100:101], v36 offset:32768\n" \
"s_add_i32 s13, s8, 0x4240000\n" \
"v_mfma_f32_16x16x32_bf16 a[4:7], v[152:155], v[116:119], a[4:7]\n" \
"s_add_u32 m0, s31, 0xc00\n" \
"s_mov_b32 s85, 0x318000\n" \
"v_mfma_f32_16x16x32_bf16 a[36:39], v[96:99], v[52:55], a[36:39]\n" \
"buffer_load_dwordx4 v27, s[68:71], s85 offen lds\n" \
"s_waitcnt lgkmcnt(15)\n" \
"v_mfma_f32_16x16x32_bf16 a[36:39], v[160:163], v[116:119], a[36:39]\n" \
"ds_read_b64_tr_b16 v[102:103], v36 offset:33792\n" \
"s_add_i32 s6, s13, s6\n" \
"s_waitcnt lgkmcnt(12)\n" \
"v_mfma_f32_16x16x32_bf16 a[32:35], v[52:55], v[92:95], a[32:35]\n" \
".fill (L_pb + 32480 - .) / 4, 4, 0xBF800000\n" \
"L_pp8:\n" \
"s_cbranch_execz L_pret\n" \
"ds_read_b64_tr_b16 v[164:165], v36 offset:40960\n" \
"v_writelane_b32 v11, s6, 40\n" \
"s_waitcnt lgkmcnt(9)\n" \
"v_mfma_f32_16x16x32_bf16 a[32:35], v[116:119], v[156:159], a[32:35]\n" \
"ds_read_b64_tr_b16 v[166:167], v36 offset:41984\n" \
"s_add_i32 s8, s8, 0x42c0000\n" \
"v_mfma_f32_16x16x32_bf16 a[96:99], v[56:59], v[92:95], a[96:99]\n" \
"ds_read_b64_tr_b16 v[104:105], v37 offset:32768\n" \
"s_lshl_b32 s6, s12, 10\n" \
"v_mfma_f32_16x16x32_bf16 a[96:99], v[120:123], v[156:159], a[96:99]\n" \
"ds_read_b64_tr_b16 v[106:107], v37 offset:33792\n" \
"s_or_b32 s6, s8, s6\n" \
"v_mfma_f32_16x16x32_bf16 a[100:103], v[96:99], v[56:59], a[100:103]\n" \
"ds_read_b64_tr_b16 v[168:169], v37 offset:40960\n" \
"v_mfma_f32_16x16x32_bf16 a[100:103], v[160:163], v[120:123], a[100:103]\n" \
"ds_read_b64_tr_b16 v[170:171], v37 offset:41984\n" \
"v_mfma_f32_16x16x32_bf16 a[68:71], v[88:91], v[56:59], a[68:71]\n" \
"s_cmp_lg_u32 s16, 0\n" \
"s_cselect_b32 s6, s7, s6\n" \
"v_mfma_f32_16x16x32_bf16 a[68:71], v[152:155], v[120:123], a[68:71]\n" \
"s_add_u32 s88, s42, 0x3000\n" \
"buffer_store_dwordx4 v[244:247], v20, s[80:83], s88 offen offset:0 sc1\n" \
"v_mfma_f32_16x16x32_bf16 a[64:67], v[84:87], v[56:59], a[64:67]\n" \
"ds_read_b64_tr_b16 v[108:109], v38 offset:32768\n" \
"s_mulk_i32 s4, 0x4800\n" \
"v_mfma_f32_16x16x32_bf16 a[64:67], v[148:151], v[120:123], a[64:67]\n" \
"ds_read_b64_tr_b16 v[110:111], v38 offset:33792\n" \
"v_writelane_b32 v11, s6, 41\n" \
"v_mfma_f32_16x16x32_bf16 a[128:131], v[84:87], v[60:63], a[128:131]\n" \
"ds_read_b64_tr_b16 v[172:173], v38 offset:40960\n" \
"s_add_i32 s6, s14, s10\n" \
"v_mfma_f32_16x16x32_bf16 a[128:131], v[148:151], v[124:127], a[128:131]\n" \
"ds_read_b64_tr_b16 v[174:175], v38 offset:41984\n" \
"v_writelane_b32 v11, s6, 42\n" \
"v_mfma_f32_16x16x32_bf16 a[132:135], v[88:91], v[60:63], a[132:135]\n" \
"s_add_i32 s6, s9, 0x21c000\n" \
"s_add_i32 s9, s6, s10\n" \
"v_mfma_f32_16x16x32_bf16 a[132:135], v[152:155], v[124:127], a[132:135]\n" \
"s_add_u32 s89, s42, 0x0\n" \
"buffer_store_dwordx4 v[208:211], v20, s[80:83], s89 offen offset:3072 sc1\n" \
"v_mfma_f32_16x16x32_bf16 a[164:167], v[96:99], v[60:63], a[164:167]\n" \
"ds_read_b64_tr_b16 v[112:113], v39 offset:32768\n" \
"v_mfma_f32_16x16x32_bf16 a[164:167], v[160:163], v[124:127], a[164:167]\n" \
"ds_read_b64_tr_b16 v[114:115], v39 offset:33792\n" \
"v_mfma_f32_16x16x32_bf16 a[160:163], v[60:63], v[92:95], a[160:163]\n" \
"ds_read_b64_tr_b16 v[176:177], v39 offset:40960\n" \
"v_mfma_f32_16x16x32_bf16 a[160:163], v[124:127], v[156:159], a[160:163]\n" \
"s_cmp_lg_u32 s12, 0\n" \
"s_cselect_b32 s6, s6, s14\n" \
"v_mfma_f32_16x16x32_bf16 a[224:227], v[64:67], v[92:95], a[224:227]\n" \
"ds_read_b64_tr_b16 v[178:179], v39 offset:41984\n" \
"s_addk_i32 s6, 0x100\n" \
"v_mfma_f32_16x16x32_bf16 a[224:227], v[128:131], v[156:159], a[224:227]\n" \
"v_writelane_b32 v11, s9, 43\n" \
"v_mfma_f32_16x16x32_bf16 a[228:231], v[96:99], v[64:67], a[228:231]\n" \
"s_add_u32 s88, s42, 0x0\n" \
"buffer_store_dwordx4 v[212:215], v20, s[80:83], s88 offen offset:2048 sc1\n" \
"v_mfma_f32_16x16x32_bf16 a[228:231], v[160:163], v[128:131], a[228:231]\n" \
"s_cmp_lg_u32 s16, 0\n" \
"s_cselect_b32 s6, s7, s6\n" \
"v_mfma_f32_16x16x32_bf16 a[196:199], v[88:91], v[64:67], a[196:199]\n" \
"v_writelane_b32 v11, s6, 44\n" \
"s_or_b32 s6, s8, s15\n" \
"v_mfma_f32_16x16x32_bf16 a[196:199], v[152:155], v[128:131], a[196:199]\n" \
"v_writelane_b32 v11, s6, 52\n" \
"s_lshl_b32 s6, s3, 17\n" \
"v_mfma_f32_16x16x32_bf16 a[192:195], v[84:87], v[64:67], a[192:195]\n" \
"s_add_i32 s6, s13, s6\n" \
"s_or_b32 s5, s6, s5\n" \
"v_mfma_f32_16x16x32_bf16 a[192:195], v[148:151], v[128:131], a[192:195]\n" \
"s_or_b32 s5, s5, 0x3840\n" \
"v_writelane_b32 v11, s5, 6\n" \
"s_waitcnt lgkmcnt(15)\n" \
"v_mfma_f32_16x16x32_bf16 a[8:11], v[88:91], v[248:251], a[8:11]\n" \
"s_mov_b32 s5, 0xff0000\n" \
"v_mfma_f32_16x16x32_bf16 a[8:11], v[152:155], v[48:51], a[8:11]\n" \
"s_cmp_lg_u32 s16, 0\n" \
"s_cselect_b32 s5, s17, s5\n" \
"v_mfma_f32_16x16x32_bf16 a[40:43], v[96:99], v[248:251], a[40:43]\n" \
"v_writelane_b32 v11, s5, 7\n" \
"s_mul_i32 s5, s3, 0x1200\n" \
"v_mfma_f32_16x16x32_bf16 a[40:43], v[160:163], v[48:51], a[40:43]\n" \
"s_add_i32 s4, s5, s4\n" \
"s_add_i32 s4, s4, s11\n" \
"v_mfma_f32_16x16x32_bf16 a[12:15], v[248:251], v[92:95], a[12:15]\n" \
"v_writelane_b32 v11, s4, 8\n" \
"s_lshl_b32 s4, s2, 16\n" \
"v_mfma_f32_16x16x32_bf16 a[12:15], v[48:51], v[156:159], a[12:15]\n" \
"s_waitcnt vmcnt(3) lgkmcnt(0)\n" \
"s_barrier\n" \
"v_mfma_f32_16x16x32_bf16 a[16:19], v[100:103], v[52:55], a[16:19]\n" \
"s_add_u32 m0, s40, 0x8000\n" \
"s_add_u32 s85, s39, 0xe000\n" \
"v_mfma_f32_16x16x32_bf16 a[16:19], v[164:167], v[116:119], a[16:19]\n" \
"buffer_load_dwordx4 v20, s[80:83], s85 offen sc1 lds\n" \
"v_mfma_f32_16x16x32_bf16 a[20:23], v[104:107], v[52:55], a[20:23]\n" \
"ds_read_b64_tr_b16 v[84:85], v32 offset:0\n" \
"s_add_i32 s5, s4, 0x10000\n" \
"v_mfma_f32_16x16x32_bf16 a[20:23], v[168:171], v[116:119], a[20:23]\n" \
"ds_read_b64_tr_b16 v[86:87], v32 offset:512\n" \
"s_and_b32 s5, s5, 0x70000\n" \
"v_mfma_f32_16x16x32_bf16 a[24:27], v[108:111], v[52:55], a[24:27]\n" \
"s_add_u32 m0, s40, 0x9000\n" \
"s_add_u32 s84, s39, 0xf000\n" \
"v_mfma_f32_16x16x32_bf16 a[24:27], v[172:175], v[116:119], a[24:27]\n" \
"buffer_load_dwordx4 v20, s[80:83], s84 offen sc1 lds\n" \
"v_mfma_f32_16x16x32_bf16 a[28:31], v[112:115], v[52:55], a[28:31]\n" \
"ds_read_b64_tr_b16 v[148:149], v32 offset:4096\n" \
"s_add_i32 s5, s13, s5\n" \
"s_nop 0\n" \
"v_mfma_f32_16x16x32_bf16 a[28:31], v[176:179], v[116:119], a[28:31]\n" \
"ds_read_b128 v[52:55], v28 offset:0\n" \
"v_writelane_b32 v11, s5, 45\n" \
"v_mfma_f32_16x16x32_bf16 a[60:63], v[112:115], v[56:59], a[60:63]\n" \
"ds_read_b128 v[116:119], v29 offset:0\n" \
"s_add_i32 s5, s4, 0x20000\n" \
"v_mfma_f32_16x16x32_bf16 a[60:63], v[176:179], v[120:123], a[60:63]\n" \
"s_add_u32 m0, s40, 0xb000\n" \
"s_mov_b32 s85, 0x380\n" \
"v_mfma_f32_16x16x32_bf16 a[56:59], v[108:111], v[56:59], a[56:59]\n" \
"buffer_load_dwordx4 v21, s[76:79], s85 offen lds\n" \
"v_mfma_f32_16x16x32_bf16 a[56:59], v[172:175], v[120:123], a[56:59]\n" \
"ds_read_b64_tr_b16 v[150:151], v32 offset:4608\n" \
"s_and_b32 s5, s5, 0x70000\n" \
"v_mfma_f32_16x16x32_bf16 a[52:55], v[104:107], v[56:59], a[52:55]\n" \
"s_add_u32 m0, s30, 0x8000\n" \
"s_mov_b32 s84, 0x380\n" \
"v_mfma_f32_16x16x32_bf16 a[52:55], v[168:171], v[120:123], a[52:55]\n" \
"buffer_load_dwordx4 v24, s[76:79], s84 offen lds\n" \
"v_mfma_f32_16x16x32_bf16 a[48:51], v[100:103], v[56:59], a[48:51]\n" \
"ds_read_b64_tr_b16 v[88:89], v33 offset:0\n" \
"s_add_i32 s5, s13, s5\n" \
"v_mfma_f32_16x16x32_bf16 a[48:51], v[164:167], v[120:123], a[48:51]\n" \
"ds_read_b128 v[56:59], v28 offset:2048\n" \
"s_bitset1_b32 s5, 11\n" \
"v_mfma_f32_16x16x32_bf16 a[80:83], v[100:103], v[60:63], a[80:83]\n" \
"ds_read_b128 v[120:123], v29 offset:2048\n" \
"v_writelane_b32 v11, s5, 46\n" \
"v_mfma_f32_16x16x32_bf16 a[80:83], v[164:167], v[124:127], a[80:83]\n" \
"s_add_u32 m0, s30, 0x8400\n" \
"s_mov_b32 s85, 0x2780\n" \
"v_mfma_f32_16x16x32_bf16 a[84:87], v[104:107], v[60:63], a[84:87]\n" \
"buffer_load_dwordx4 v25, s[76:79], s85 offen lds\n" \
"v_mfma_f32_16x16x32_bf16 a[84:87], v[168:171], v[124:127], a[84:87]\n" \
"ds_read_b64_tr_b16 v[90:91], v33 offset:512\n" \
"s_add_i32 s5, s4, 0x30000\n" \
"v_mfma_f32_16x16x32_bf16 a[88:91], v[108:111], v[60:63], a[88:91]\n" \
"ds_read_b64_tr_b16 v[152:153], v33 offset:4096\n" \
"s_and_b32 s5, s5, 0x70000\n" \
"v_mfma_f32_16x16x32_bf16 a[88:91], v[172:175], v[124:127], a[88:91]\n" \
"s_add_u32 m0, s31, 0x8000\n" \
"s_mov_b32 s84, 0x380000\n" \
"v_mfma_f32_16x16x32_bf16 a[92:95], v[112:115], v[60:63], a[92:95]\n" \
"buffer_load_dwordx4 v26, s[68:71], s84 offen lds\n" \
"v_mfma_f32_16x16x32_bf16 a[92:95], v[176:179], v[124:127], a[92:95]\n" \
"ds_read_b128 v[60:63], v28 offset:4096\n" \
"s_add_i32 s5, s13, s5\n" \
"v_mfma_f32_16x16x32_bf16 a[124:127], v[112:115], v[64:67], a[124:127]\n" \
"ds_read_b128 v[124:127], v29 offset:4096\n" \
"s_bitset1_b32 s5, 12\n" \
"v_mfma_f32_16x16x32_bf16 a[124:127], v[176:179], v[128:131], a[124:127]\n" \
"s_add_u32 m0, s30, 0x8800\n" \
"s_mov_b32 s85, 0x4b80\n" \
"v_mfma_f32_16x16x32_bf16 a[120:123], v[108:111], v[64:67], a[120:123]\n" \
"buffer_load_dwordx4 v24, s[76:79], s85 offen lds\n" \
"v_mfma_f32_16x16x32_bf16 a[120:123], v[172:175], v[128:131], a[120:123]\n" \
"ds_read_b64_tr_b16 v[154:155], v33 offset:4608\n" \
"v_writelane_b32 v11, s5, 47\n" \
"v_mfma_f32_16x16x32_bf16 a[116:119], v[104:107], v[64:67], a[116:119]\n" \
"ds_read_b64_tr_b16 v[96:97], v35 offset:0\n" \
"s_and_b32 s5, s4, 0x70000\n" \
"v_mfma_f32_16x16x32_bf16 a[116:119], v[168:171], v[128:131], a[116:119]\n" \
"ds_read_b64_tr_b16 v[98:99], v35 offset:512\n" \
"s_xor_b32 s5, s5, 0x40000\n" \
"v_mfma_f32_16x16x32_bf16 a[112:115], v[100:103], v[64:67], a[112:115]\n" \
"ds_read_b64_tr_b16 v[160:161], v35 offset:4096\n" \
"s_add_i32 s5, s13, s5\n" \
"s_nop 0\n" \
"v_mfma_f32_16x16x32_bf16 a[112:115], v[164:167], v[128:131], a[112:115]\n" \
"ds_read_b128 v[64:67], v28 offset:6144\n" \
"s_or_b32 s5, s5, 0x1800\n" \
"v_mfma_f32_16x16x32_bf16 a[144:147], v[100:103], v[68:71], a[144:147]\n" \
"ds_read_b128 v[128:131], v29 offset:6144\n" \
"v_writelane_b32 v11, s5, 48\n" \
"v_mfma_f32_16x16x32_bf16 a[144:147], v[164:167], v[132:135], a[144:147]\n" \
"s_add_u32 m0, s30, 0x8c00\n" \
"s_mov_b32 s84, 0x6f80\n" \
"v_mfma_f32_16x16x32_bf16 a[148:151], v[104:107], v[68:71], a[148:151]\n" \
"buffer_load_dwordx4 v25, s[76:79], s84 offen lds\n" \
"v_mfma_f32_16x16x32_bf16 a[148:151], v[168:171], v[132:135], a[148:151]\n" \
"s_add_u32 m0, s31, 0x8400\n" \
"s_mov_b32 s85, 0x388000\n" \
"v_mfma_f32_16x16x32_bf16 a[152:155], v[108:111], v[68:71], a[152:155]\n" \
"buffer_load_dwordx4 v26, s[68:71], s85 offen lds\n" \
"v_mfma_f32_16x16x32_bf16 a[152:155], v[172:175], v[132:135], a[152:155]\n" \
"ds_read_b64_tr_b16 v[162:163], v35 offset:4608\n" \
"s_add_i32 s5, s4, 0x50000\n" \
"v_mfma_f32_16x16x32_bf16 a[156:159], v[112:115], v[68:71], a[156:159]\n" \
"ds_read_b64_tr_b16 v[92:93], v34 offset:0\n" \
"s_and_b32 s5, s5, 0x70000\n" \
"v_mfma_f32_16x16x32_bf16 a[156:159], v[176:179], v[132:135], a[156:159]\n" \
"ds_read_b128 v[68:71], v28 offset:8192\n" \
"s_add_i32 s5, s13, s5\n" \
"v_mfma_f32_16x16x32_bf16 a[108:111], v[112:115], v[72:75], a[108:111]\n" \
"ds_read_b128 v[132:135], v29 offset:8192\n" \
"s_bitset1_b32 s5, 13\n" \
"v_mfma_f32_16x16x32_bf16 a[108:111], v[176:179], v[136:139], a[108:111]\n" \
"s_add_u32 m0, s30, 0x9000\n" \
"s_mov_b32 s84, 0x9380\n" \
"v_mfma_f32_16x16x32_bf16 a[104:107], v[108:111], v[72:75], a[104:107]\n" \
"buffer_load_dwordx4 v24, s[76:79], s84 offen lds\n" \
"v_mfma_f32_16x16x32_bf16 a[104:107], v[172:175], v[136:139], a[104:107]\n" \
"s_add_u32 m0, s30, 0x9400\n" \
"s_mov_b32 s85, 0xb780\n" \
"v_mfma_f32_16x16x32_bf16 a[76:79], v[104:107], v[72:75], a[76:79]\n" \
"buffer_load_dwordx4 v25, s[76:79], s85 offen lds\n" \
"v_mfma_f32_16x16x32_bf16 a[76:79], v[168:171], v[136:139], a[76:79]\n" \
"ds_read_b64_tr_b16 v[94:95], v34 offset:512\n" \
"v_writelane_b32 v11, s5, 49\n" \
"v_mfma_f32_16x16x32_bf16 a[72:75], v[100:103], v[72:75], a[72:75]\n" \
"ds_read_b64_tr_b16 v[156:157], v34 offset:4096\n" \
"s_add_i32 s5, s4, 0x60000\n" \
"v_mfma_f32_16x16x32_bf16 a[72:75], v[164:167], v[136:139], a[72:75]\n" \
"ds_read_b128 v[72:75], v28 offset:10240\n" \
"s_and_b32 s5, s5, 0x70000\n" \
"v_mfma_f32_16x16x32_bf16 a[136:139], v[100:103], v[76:79], a[136:139]\n" \
"ds_read_b128 v[136:139], v29 offset:10240\n" \
"s_add_i32 s4, s4, 0x70000\n" \
"v_mfma_f32_16x16x32_bf16 a[136:139], v[164:167], v[140:143], a[136:139]\n" \
"s_add_u32 m0, s31, 0x8800\n" \
"s_mov_b32 s84, 0x390000\n" \
"v_mfma_f32_16x16x32_bf16 a[140:143], v[104:107], v[76:79], a[140:143]\n" \
"buffer_load_dwordx4 v27, s[68:71], s84 offen lds\n" \
"v_mfma_f32_16x16x32_bf16 a[140:143], v[168:171], v[140:143], a[140:143]\n" \
"s_add_u32 m0, s30, 0x9800\n" \
"s_mov_b32 s85, 0xdb80\n" \
"v_mfma_f32_16x16x32_bf16 a[168:171], v[108:111], v[76:79], a[168:171]\n" \
"buffer_load_dwordx4 v24, s[76:79], s85 offen lds\n" \
"v_mfma_f32_16x16x32_bf16 a[168:171], v[172:175], v[140:143], a[168:171]\n" \
"ds_read_b64_tr_b16 v[158:159], v34 offset:4608\n" \
"s_add_i32 s5, s13, s5\n" \
"s_nop 0\n" \
"v_mfma_f32_16x16x32_bf16 a[172:175], v[112:115], v[76:79], a[172:175]\n" \
"ds_read_b128 v[248:251], v22 offset:0\n" \
"s_and_b32 s4, s4, 0x70000\n" \
"v_mfma_f32_16x16x32_bf16 a[172:175], v[176:179], v[140:143], a[172:175]\n" \
"ds_read_b128 v[76:79], v28 offset:12288\n" \
"s_or_b32 s5, s5, 0x2800\n" \
"v_mfma_f32_16x16x32_bf16 a[236:239], v[112:115], v[80:83], a[236:239]\n" \
"ds_read_b128 v[140:143], v29 offset:12288\n" \
"v_writelane_b32 v11, s5, 50\n" \
"v_mfma_f32_16x16x32_bf16 a[236:239], v[176:179], v[144:147], a[236:239]\n" \
"ds_read_b128 v[48:51], v23 offset:0\n" \
"s_add_i32 s13, s13, s4\n" \
"v_mfma_f32_16x16x32_bf16 a[232:235], v[108:111], v[80:83], a[232:235]\n" \
"s_or_b32 s4, s13, 0x3000\n" \
"v_writelane_b32 v11, s4, 51\n" \
"v_mfma_f32_16x16x32_bf16 a[232:235], v[172:175], v[144:147], a[232:235]\n" \
"s_movk_i32 s11, 0x4000\n" \
"s_lshl_b32 s4, s2, 2\n" \
"v_mfma_f32_16x16x32_bf16 a[204:207], v[104:107], v[80:83], a[204:207]\n" \
"s_and_b32 s4, s4, 28\n" \
"s_bfe_u32 s5, s2, 0x20003\n" \
"v_mfma_f32_16x16x32_bf16 a[204:207], v[168:171], v[144:147], a[204:207]\n" \
"s_or_b32 s4, s4, s5\n" \
"s_lshr_b32 s5, s2, 5\n" \
"v_mfma_f32_16x16x32_bf16 a[200:203], v[100:103], v[80:83], a[200:203]\n" \
"s_lshr_b32 s19, s3, 1\n" \
"s_lshl_b32 s18, s5, 1\n" \
"v_mfma_f32_16x16x32_bf16 a[200:203], v[164:167], v[144:147], a[200:203]\n" \
"ds_read_b128 v[80:83], v28 offset:14336\n" \
"ds_read_b128 v[144:147], v29 offset:14336\n" \
"s_waitcnt lgkmcnt(15)\n" \
"v_mfma_f32_16x16x32_bf16 a[0:3], v[84:87], v[52:55], a[0:3]\n" \
"s_add_u32 m0, s30, 0x9c00\n" \
"s_mov_b32 s85, 0xff80\n" \
"v_mfma_f32_16x16x32_bf16 a[0:3], v[148:151], v[116:119], a[0:3]\n" \
"buffer_load_dwordx4 v25, s[76:79], s85 offen lds\n" \
"v_mfma_f32_16x16x32_bf16 a[4:7], v[88:91], v[52:55], a[4:7]\n" \
"ds_read_b64_tr_b16 v[100:101], v36 offset:0\n" \
"s_lshl_b32 s6, s4, 20\n" \
"v_mfma_f32_16x16x32_bf16 a[4:7], v[152:155], v[116:119], a[4:7]\n" \
"s_add_u32 m0, s31, 0x8c00\n" \
"s_mov_b32 s84, 0x398000\n" \
"v_mfma_f32_16x16x32_bf16 a[36:39], v[96:99], v[52:55], a[36:39]\n" \
"buffer_load_dwordx4 v27, s[68:71], s84 offen lds\n" \
"s_waitcnt lgkmcnt(15)\n" \
"v_mfma_f32_16x16x32_bf16 a[36:39], v[160:163], v[116:119], a[36:39]\n" \
"ds_read_b64_tr_b16 v[102:103], v36 offset:1024\n" \
"s_lshl_b32 s7, s4, 8\n" \
"s_waitcnt lgkmcnt(12)\n" \
"v_mfma_f32_16x16x32_bf16 a[32:35], v[52:55], v[92:95], a[32:35]\n" \
"ds_read_b64_tr_b16 v[164:165], v36 offset:8192\n" \
"s_and_b32 s13, s3, 1\n" \
"s_waitcnt lgkmcnt(9)\n" \
"v_mfma_f32_16x16x32_bf16 a[32:35], v[116:119], v[156:159], a[32:35]\n" \
"ds_read_b64_tr_b16 v[166:167], v36 offset:9216\n" \
"s_add_i32 s14, s18, s19\n" \
"v_mfma_f32_16x16x32_bf16 a[96:99], v[56:59], v[92:95], a[96:99]\n" \
"ds_read_b64_tr_b16 v[104:105], v37 offset:0\n" \
"v_mfma_f32_16x16x32_bf16 a[96:99], v[120:123], v[156:159], a[96:99]\n" \
"ds_read_b64_tr_b16 v[106:107], v37 offset:1024\n" \
"v_mfma_f32_16x16x32_bf16 a[100:103], v[96:99], v[56:59], a[100:103]\n" \
"ds_read_b64_tr_b16 v[168:169], v37 offset:8192\n" \
"v_mfma_f32_16x16x32_bf16 a[100:103], v[160:163], v[120:123], a[100:103]\n" \
"ds_read_b64_tr_b16 v[170:171], v37 offset:9216\n" \
"v_mfma_f32_16x16x32_bf16 a[68:71], v[88:91], v[56:59], a[68:71]\n" \
"s_add_u32 s6, s26, s6\n" \
"s_addc_u32 s9, s27, 0\n" \
"v_mfma_f32_16x16x32_bf16 a[68:71], v[152:155], v[120:123], a[68:71]\n" \
"s_add_u32 s89, s42, 0x1000\n" \
"buffer_store_dwordx4 v[184:187], v20, s[80:83], s89 offen offset:2048 sc1\n" \
"v_mfma_f32_16x16x32_bf16 a[64:67], v[84:87], v[56:59], a[64:67]\n" \
"ds_read_b64_tr_b16 v[108:109], v38 offset:0\n" \
"v_mfma_f32_16x16x32_bf16 a[64:67], v[148:151], v[120:123], a[64:67]\n" \
"ds_read_b64_tr_b16 v[110:111], v38 offset:1024\n" \
"v_mfma_f32_16x16x32_bf16 a[128:131], v[84:87], v[60:63], a[128:131]\n" \
"ds_read_b64_tr_b16 v[172:173], v38 offset:8192\n" \
"v_mfma_f32_16x16x32_bf16 a[128:131], v[148:151], v[124:127], a[128:131]\n" \
"ds_read_b64_tr_b16 v[174:175], v38 offset:9216\n" \
"v_mfma_f32_16x16x32_bf16 a[132:135], v[88:91], v[60:63], a[132:135]\n" \
"s_add_u32 s8, s6, 0x1000000\n" \
"s_addc_u32 s6, s9, 0\n" \
"v_mfma_f32_16x16x32_bf16 a[132:135], v[152:155], v[124:127], a[132:135]\n" \
"s_add_u32 s88, s42, 0x1000\n" \
"buffer_store_dwordx4 v[188:191], v20, s[80:83], s88 offen offset:3072 sc1\n" \
"v_mfma_f32_16x16x32_bf16 a[164:167], v[96:99], v[60:63], a[164:167]\n" \
"ds_read_b64_tr_b16 v[112:113], v39 offset:0\n" \
"s_and_b32 s9, s6, 0xffff\n" \
"v_mfma_f32_16x16x32_bf16 a[164:167], v[160:163], v[124:127], a[164:167]\n" \
"ds_read_b64_tr_b16 v[114:115], v39 offset:1024\n" \
"s_lshl_b32 s6, s5, 16\n" \
"v_mfma_f32_16x16x32_bf16 a[160:163], v[60:63], v[92:95], a[160:163]\n" \
"ds_read_b64_tr_b16 v[176:177], v39 offset:8192\n" \
"s_sub_i32 s10, 0x100000, s6\n" \
"v_mfma_f32_16x16x32_bf16 a[160:163], v[124:127], v[156:159], a[160:163]\n" \
"s_mov_b32 s6, 0x8000\n" \
"v_mfma_f32_16x16x32_bf16 a[224:227], v[64:67], v[92:95], a[224:227]\n" \
"ds_read_b64_tr_b16 v[178:179], v39 offset:9216\n" \
"v_mfma_f32_16x16x32_bf16 a[224:227], v[128:131], v[156:159], a[224:227]\n" \
"s_cmp_lg_u32 s5, 0\n" \
"s_cselect_b32 s17, s11, s6\n" \
"v_mfma_f32_16x16x32_bf16 a[228:231], v[96:99], v[64:67], a[228:231]\n" \
"s_add_u32 s89, s42, 0x2000\n" \
"buffer_store_dwordx4 v[192:195], v20, s[80:83], s89 offen offset:2048 sc1\n" \
"v_mfma_f32_16x16x32_bf16 a[228:231], v[160:163], v[128:131], a[228:231]\n" \
"s_lshl_b32 s6, s3, 1\n" \
"s_lshl_b32 s5, s5, 3\n" \
"v_mfma_f32_16x16x32_bf16 a[196:199], v[88:91], v[64:67], a[196:199]\n" \
"s_and_b32 s6, s6, 4\n" \
"s_or_b32 s12, s5, s6\n" \
"v_mfma_f32_16x16x32_bf16 a[196:199], v[152:155], v[128:131], a[196:199]\n" \
"s_sub_i32 s5, s13, s5\n" \
"s_or_b32 s5, s5, s6\n" \
"v_mfma_f32_16x16x32_bf16 a[192:195], v[84:87], v[64:67], a[192:195]\n" \
"s_or_b32 s6, s12, s13\n" \
"s_lshl_b32 s4, s4, 2\n" \
"v_mfma_f32_16x16x32_bf16 a[192:195], v[148:151], v[128:131], a[192:195]\n" \
"s_mulk_i32 s6, 0x92\n" \
"s_add_i32 s4, s4, 18\n" \
"s_waitcnt lgkmcnt(15)\n" \
"v_mfma_f32_16x16x32_bf16 a[8:11], v[88:91], v[248:251], a[8:11]\n" \
"s_mulk_i32 s5, 0x92\n" \
"s_add_i32 s6, s4, s6\n" \
"v_mfma_f32_16x16x32_bf16 a[8:11], v[152:155], v[48:51], a[8:11]\n" \
"s_add_i32 s5, s5, s4\n" \
"s_lshl_b32 s4, s3, 11\n" \
"v_mfma_f32_16x16x32_bf16 a[40:43], v[96:99], v[248:251], a[40:43]\n" \
"s_lshl_b32 s15, s13, 12\n" \
"s_lshl_b32 s16, s19, 14\n" \
"v_mfma_f32_16x16x32_bf16 a[40:43], v[160:163], v[48:51], a[40:43]\n" \
"s_mul_i32 s19, s19, 0xe000\n" \
"s_mov_b32 s11, 0x20000\n" \
"v_mfma_f32_16x16x32_bf16 a[12:15], v[248:251], v[92:95], a[12:15]\n" \
"s_or_b32 s12, s4, 0xc000\n" \
"s_lshl_b32 s4, s3, 8\n" \
"v_mfma_f32_16x16x32_bf16 a[12:15], v[48:51], v[156:159], a[12:15]\n" \
"s_waitcnt vmcnt(3) lgkmcnt(0)\n" \
"s_barrier\n" \
"v_mfma_f32_16x16x32_bf16 a[16:19], v[100:103], v[52:55], a[16:19]\n" \
"s_add_u32 m0, s40, 0x0\n" \
"s_add_u32 s84, s39, 0x10000\n" \
"v_mfma_f32_16x16x32_bf16 a[16:19], v[164:167], v[116:119], a[16:19]\n" \
"buffer_load_dwordx4 v20, s[80:83], s84 offen sc1 lds\n" \
"v_mfma_f32_16x16x32_bf16 a[20:23], v[104:107], v[52:55], a[20:23]\n" \
"ds_read_b64_tr_b16 v[84:85], v32 offset:32768\n" \
"s_or_b32 s22, s15, s16\n" \
"v_mfma_f32_16x16x32_bf16 a[20:23], v[168:171], v[116:119], a[20:23]\n" \
"ds_read_b64_tr_b16 v[86:87], v32 offset:33280\n" \
"s_or_b32 s23, s19, s15\n" \
"v_mfma_f32_16x16x32_bf16 a[24:27], v[108:111], v[52:55], a[24:27]\n" \
"s_add_u32 m0, s40, 0x1000\n" \
"s_add_u32 s85, s39, 0x11000\n" \
"v_mfma_f32_16x16x32_bf16 a[24:27], v[172:175], v[116:119], a[24:27]\n" \
"buffer_load_dwordx4 v20, s[80:83], s85 offen sc1 lds\n" \
"v_mfma_f32_16x16x32_bf16 a[28:31], v[112:115], v[52:55], a[28:31]\n" \
"ds_read_b64_tr_b16 v[148:149], v32 offset:36864\n" \
"s_lshl_b32 s60, s6, 10\n" \
"v_mfma_f32_16x16x32_bf16 a[28:31], v[176:179], v[116:119], a[28:31]\n" \
"ds_read_b128 v[52:55], v28 offset:32768\n" \
"s_lshl_b32 s61, s5, 10\n" \
"v_mfma_f32_16x16x32_bf16 a[60:63], v[112:115], v[56:59], a[60:63]\n" \
"ds_read_b128 v[116:119], v29 offset:32768\n" \
"s_lshl_b32 s14, s14, 15\n" \
"v_mfma_f32_16x16x32_bf16 a[60:63], v[176:179], v[120:123], a[60:63]\n" \
"s_add_u32 m0, s40, 0x3000\n" \
"s_mov_b32 s84, 0x400\n" \
"v_mfma_f32_16x16x32_bf16 a[56:59], v[108:111], v[56:59], a[56:59]\n" \
"buffer_load_dwordx4 v21, s[76:79], s84 offen lds\n" \
"v_mfma_f32_16x16x32_bf16 a[56:59], v[172:175], v[120:123], a[56:59]\n" \
"ds_read_b64_tr_b16 v[150:151], v32 offset:37376\n" \
"s_or_b32 s20, s4, 0x26c00\n" \
"v_mfma_f32_16x16x32_bf16 a[52:55], v[104:107], v[56:59], a[52:55]\n" \
"s_add_u32 m0, s30, 0x0\n" \
"s_nop 0\n" \
"s_mov_b32 s85, 0x400\n" \
"v_mfma_f32_16x16x32_bf16 a[52:55], v[168:171], v[120:123], a[52:55]\n" \
"buffer_load_dwordx4 v24, s[76:79], s85 offen lds\n" \
"v_mfma_f32_16x16x32_bf16 a[48:51], v[100:103], v[56:59], a[48:51]\n" \
"ds_read_b64_tr_b16 v[88:89], v33 offset:32768\n" \
"s_or_b32 s13, s22, 0x8000\n" \
"v_mfma_f32_16x16x32_bf16 a[48:51], v[164:167], v[120:123], a[48:51]\n" \
"ds_read_b128 v[56:59], v28 offset:34816\n" \
"s_add_i32 s22, s22, 0x14000\n" \
"v_mfma_f32_16x16x32_bf16 a[80:83], v[100:103], v[60:63], a[80:83]\n" \
"ds_read_b128 v[120:123], v29 offset:34816\n" \
"s_bitset1_b32 s23, 16\n" \
"s_nop 0\n" \
"v_mfma_f32_16x16x32_bf16 a[80:83], v[164:167], v[124:127], a[80:83]\n" \
"s_add_u32 m0, s30, 0x400\n" \
"s_mov_b32 s84, 0x2800\n" \
"v_mfma_f32_16x16x32_bf16 a[84:87], v[104:107], v[60:63], a[84:87]\n" \
"buffer_load_dwordx4 v25, s[76:79], s84 offen lds\n" \
"v_mfma_f32_16x16x32_bf16 a[84:87], v[168:171], v[124:127], a[84:87]\n" \
"ds_read_b64_tr_b16 v[90:91], v33 offset:33280\n" \
"s_or_b32 s24, s7, 0x5be8020\n" \
"v_mfma_f32_16x16x32_bf16 a[88:91], v[108:111], v[60:63], a[88:91]\n" \
"ds_read_b64_tr_b16 v[152:153], v33 offset:36864\n" \
"s_and_b32 s57, s27, 0xffff\n" \
"v_mfma_f32_16x16x32_bf16 a[88:91], v[172:175], v[124:127], a[88:91]\n" \
"s_add_u32 m0, s31, 0x0\n" \
"s_mov_b32 s85, 0x400000\n" \
"v_mfma_f32_16x16x32_bf16 a[92:95], v[112:115], v[60:63], a[92:95]\n" \
"buffer_load_dwordx4 v26, s[68:71], s85 offen lds\n" \
"v_mfma_f32_16x16x32_bf16 a[92:95], v[176:179], v[124:127], a[92:95]\n" \
"ds_read_b128 v[60:63], v28 offset:36864\n" \
"s_mov_b32 s58, 0x6000000\n" \
"v_mfma_f32_16x16x32_bf16 a[124:127], v[112:115], v[64:67], a[124:127]\n" \
"ds_read_b128 v[124:127], v29 offset:36864\n" \
"s_mov_b32 s56, s26\n" \
"v_mfma_f32_16x16x32_bf16 a[124:127], v[176:179], v[128:131], a[124:127]\n" \
"s_add_u32 m0, s30, 0x800\n" \
"s_mov_b32 s84, 0x4c00\n" \
"v_mfma_f32_16x16x32_bf16 a[120:123], v[108:111], v[64:67], a[120:123]\n" \
"buffer_load_dwordx4 v24, s[76:79], s84 offen lds\n" \
"v_mfma_f32_16x16x32_bf16 a[120:123], v[172:175], v[128:131], a[120:123]\n" \
"ds_read_b64_tr_b16 v[154:155], v33 offset:37376\n" \
"s_mov_b32 s59, s11\n" \
"v_mfma_f32_16x16x32_bf16 a[116:119], v[104:107], v[64:67], a[116:119]\n" \
"ds_read_b64_tr_b16 v[96:97], v35 offset:32768\n" \
"s_add_i32 s60, s60, 0x3000000\n" \
"v_mfma_f32_16x16x32_bf16 a[116:119], v[168:171], v[128:131], a[116:119]\n" \
"ds_read_b64_tr_b16 v[98:99], v35 offset:33280\n" \
"s_add_i32 s61, s61, 0x411c000\n" \
"v_mfma_f32_16x16x32_bf16 a[112:115], v[100:103], v[64:67], a[112:115]\n" \
"ds_read_b64_tr_b16 v[160:161], v35 offset:36864\n" \
"s_sub_i32 s19, 14, s18\n" \
"v_mfma_f32_16x16x32_bf16 a[112:115], v[164:167], v[128:131], a[112:115]\n" \
"ds_read_b128 v[64:67], v28 offset:38912\n" \
"s_mov_b32 s21, 0x71800000\n" \
"v_mfma_f32_16x16x32_bf16 a[144:147], v[100:103], v[68:71], a[144:147]\n" \
"ds_read_b128 v[128:131], v29 offset:38912\n" \
"v_mfma_f32_16x16x32_bf16 a[144:147], v[164:167], v[132:135], a[144:147]\n" \
"s_add_u32 m0, s30, 0xc00\n" \
"s_mov_b32 s85, 0x7000\n" \
"v_mfma_f32_16x16x32_bf16 a[148:151], v[104:107], v[68:71], a[148:151]\n" \
"buffer_load_dwordx4 v25, s[76:79], s85 offen lds\n" \
"v_mfma_f32_16x16x32_bf16 a[148:151], v[168:171], v[132:135], a[148:151]\n" \
"s_add_u32 m0, s31, 0x400\n" \
"s_mov_b32 s84, 0x408000\n" \
"v_mfma_f32_16x16x32_bf16 a[152:155], v[108:111], v[68:71], a[152:155]\n" \
"buffer_load_dwordx4 v26, s[68:71], s84 offen lds\n" \
"v_mfma_f32_16x16x32_bf16 a[152:155], v[172:175], v[132:135], a[152:155]\n" \
"ds_read_b64_tr_b16 v[162:163], v35 offset:37376\n" \
"v_mfma_f32_16x16x32_bf16 a[156:159], v[112:115], v[68:71], a[156:159]\n" \
"ds_read_b64_tr_b16 v[92:93], v34 offset:32768\n" \
"v_mfma_f32_16x16x32_bf16 a[156:159], v[176:179], v[132:135], a[156:159]\n" \
"ds_read_b128 v[68:71], v28 offset:40960\n" \
"v_mfma_f32_16x16x32_bf16 a[108:111], v[112:115], v[72:75], a[108:111]\n" \
"ds_read_b128 v[132:135], v29 offset:40960\n" \
"v_mfma_f32_16x16x32_bf16 a[108:111], v[176:179], v[136:139], a[108:111]\n" \
"s_add_u32 m0, s30, 0x1000\n" \
"s_mov_b32 s85, 0x9400\n" \
"v_mfma_f32_16x16x32_bf16 a[104:107], v[108:111], v[72:75], a[104:107]\n" \
"buffer_load_dwordx4 v24, s[76:79], s85 offen lds\n" \
"v_mfma_f32_16x16x32_bf16 a[104:107], v[172:175], v[136:139], a[104:107]\n" \
"s_add_u32 m0, s30, 0x1400\n" \
"s_mov_b32 s84, 0xb800\n" \
"v_mfma_f32_16x16x32_bf16 a[76:79], v[104:107], v[72:75], a[76:79]\n" \
"buffer_load_dwordx4 v25, s[76:79], s84 offen lds\n" \
"v_mfma_f32_16x16x32_bf16 a[76:79], v[168:171], v[136:139], a[76:79]\n" \
"ds_read_b64_tr_b16 v[94:95], v34 offset:33280\n" \
"v_mfma_f32_16x16x32_bf16 a[72:75], v[100:103], v[72:75], a[72:75]\n" \
"ds_read_b64_tr_b16 v[156:157], v34 offset:36864\n" \
"v_mfma_f32_16x16x32_bf16 a[72:75], v[164:167], v[136:139], a[72:75]\n" \
"ds_read_b128 v[72:75], v28 offset:43008\n" \
"v_mfma_f32_16x16x32_bf16 a[136:139], v[100:103], v[76:79], a[136:139]\n" \
"ds_read_b128 v[136:139], v29 offset:43008\n" \
"v_mfma_f32_16x16x32_bf16 a[136:139], v[164:167], v[140:143], a[136:139]\n" \
"s_add_u32 m0, s31, 0x800\n" \
"s_mov_b32 s85, 0x410000\n" \
"v_mfma_f32_16x16x32_bf16 a[140:143], v[104:107], v[76:79], a[140:143]\n" \
"buffer_load_dwordx4 v27, s[68:71], s85 offen lds\n" \
"v_mfma_f32_16x16x32_bf16 a[140:143], v[168:171], v[140:143], a[140:143]\n" \
"s_add_u32 m0, s30, 0x1800\n" \
"s_mov_b32 s84, 0xdc00\n" \
"v_mfma_f32_16x16x32_bf16 a[168:171], v[108:111], v[76:79], a[168:171]\n" \
"buffer_load_dwordx4 v24, s[76:79], s84 offen lds\n" \
"v_mfma_f32_16x16x32_bf16 a[168:171], v[172:175], v[140:143], a[168:171]\n" \
"ds_read_b64_tr_b16 v[158:159], v34 offset:37376\n" \
"v_mfma_f32_16x16x32_bf16 a[172:175], v[112:115], v[76:79], a[172:175]\n" \
"ds_read_b128 v[248:251], v22 offset:32768\n" \
"v_mfma_f32_16x16x32_bf16 a[172:175], v[176:179], v[140:143], a[172:175]\n" \
"ds_read_b128 v[76:79], v28 offset:45056\n" \
"v_mfma_f32_16x16x32_bf16 a[236:239], v[112:115], v[80:83], a[236:239]\n" \
"ds_read_b128 v[140:143], v29 offset:45056\n" \
"v_mfma_f32_16x16x32_bf16 a[236:239], v[176:179], v[144:147], a[236:239]\n" \
"ds_read_b128 v[48:51], v23 offset:32768\n" \
"v_mfma_f32_16x16x32_bf16 a[232:235], v[108:111], v[80:83], a[232:235]\n" \
"v_mfma_f32_16x16x32_bf16 a[232:235], v[172:175], v[144:147], a[232:235]\n" \
"v_mfma_f32_16x16x32_bf16 a[204:207], v[104:107], v[80:83], a[204:207]\n" \
"v_mfma_f32_16x16x32_bf16 a[204:207], v[168:171], v[144:147], a[204:207]\n" \
"v_mfma_f32_16x16x32_bf16 a[200:203], v[100:103], v[80:83], a[200:203]\n" \
"v_mfma_f32_16x16x32_bf16 a[200:203], v[164:167], v[144:147], a[200:203]\n" \
"ds_read_b128 v[80:83], v28 offset:47104\n" \
"ds_read_b128 v[144:147], v29 offset:47104\n" \
"s_waitcnt lgkmcnt(15)\n" \
"s_nop 0\n" \
"v_mfma_f32_16x16x32_bf16 a[0:3], v[84:87], v[52:55], a[0:3]\n" \
"s_add_u32 m0, s30, 0x1c00\n" \
"s_mov_b32 s84, 0x10000\n" \
"v_mfma_f32_16x16x32_bf16 a[0:3], v[148:151], v[116:119], a[0:3]\n" \
"buffer_load_dwordx4 v25, s[76:79], s84 offen lds\n" \
"v_mfma_f32_16x16x32_bf16 a[4:7], v[88:91], v[52:55], a[4:7]\n" \
"ds_read_b64_tr_b16 v[100:101], v36 offset:32768\n" \
"v_mfma_f32_16x16x32_bf16 a[4:7], v[152:155], v[116:119], a[4:7]\n" \
"s_add_u32 m0, s31, 0xc00\n" \
"s_mov_b32 s85, 0x418000\n" \
"v_mfma_f32_16x16x32_bf16 a[36:39], v[96:99], v[52:55], a[36:39]\n" \
"buffer_load_dwordx4 v27, s[68:71], s85 offen lds\n" \
"s_waitcnt lgkmcnt(15)\n" \
"v_mfma_f32_16x16x32_bf16 a[36:39], v[160:163], v[116:119], a[36:39]\n" \
"ds_read_b64_tr_b16 v[102:103], v36 offset:33792\n" \
"s_waitcnt lgkmcnt(12)\n" \
"v_mfma_f32_16x16x32_bf16 a[32:35], v[52:55], v[92:95], a[32:35]\n" \
"ds_read_b64_tr_b16 v[164:165], v36 offset:40960\n" \
"s_waitcnt lgkmcnt(9)\n" \
"s_nop 0\n" \
"v_mfma_f32_16x16x32_bf16 a[32:35], v[116:119], v[156:159], a[32:35]\n" \
"ds_read_b64_tr_b16 v[166:167], v36 offset:41984\n" \
"v_mfma_f32_16x16x32_bf16 a[96:99], v[56:59], v[92:95], a[96:99]\n" \
"ds_read_b64_tr_b16 v[104:105], v37 offset:32768\n" \
".fill (L_pb + 36576 - .) / 4, 4, 0xBF800000\n" \
"L_pp9:\n" \
"s_cbranch_execz L_pret\n" \
"s_nop 0\n" \
"v_mfma_f32_16x16x32_bf16 a[96:99], v[120:123], v[156:159], a[96:99]\n" \
"ds_read_b64_tr_b16 v[106:107], v37 offset:33792\n" \
"v_mfma_f32_16x16x32_bf16 a[100:103], v[96:99], v[56:59], a[100:103]\n" \
"ds_read_b64_tr_b16 v[168:169], v37 offset:40960\n" \
"v_mfma_f32_16x16x32_bf16 a[100:103], v[160:163], v[120:123], a[100:103]\n" \
"ds_read_b64_tr_b16 v[170:171], v37 offset:41984\n" \
"v_mfma_f32_16x16x32_bf16 a[68:71], v[88:91], v[56:59], a[68:71]\n" \
"v_mfma_f32_16x16x32_bf16 a[68:71], v[152:155], v[120:123], a[68:71]\n" \
"s_add_u32 s88, s42, 0x2000\n" \
"buffer_store_dwordx4 v[196:199], v20, s[80:83], s88 offen offset:3072 sc1\n" \
"v_mfma_f32_16x16x32_bf16 a[64:67], v[84:87], v[56:59], a[64:67]\n" \
"ds_read_b64_tr_b16 v[108:109], v38 offset:32768\n" \
"v_mfma_f32_16x16x32_bf16 a[64:67], v[148:151], v[120:123], a[64:67]\n" \
"ds_read_b64_tr_b16 v[110:111], v38 offset:33792\n" \
"v_mfma_f32_16x16x32_bf16 a[128:131], v[84:87], v[60:63], a[128:131]\n" \
"ds_read_b64_tr_b16 v[172:173], v38 offset:40960\n" \
"v_mfma_f32_16x16x32_bf16 a[128:131], v[148:151], v[124:127], a[128:131]\n" \
"ds_read_b64_tr_b16 v[174:175], v38 offset:41984\n" \
"v_mfma_f32_16x16x32_bf16 a[132:135], v[88:91], v[60:63], a[132:135]\n" \
"v_lshlrev_b32_e32 v45, 3, v20\n" \
"v_add_u32_e32 v45, s30, v45\n" \
"v_mfma_f32_16x16x32_bf16 a[132:135], v[152:155], v[124:127], a[132:135]\n" \
"s_add_u32 s89, s42, 0x3000\n" \
"buffer_store_dwordx4 v[200:203], v20, s[80:83], s89 offen offset:2048 sc1\n" \
"s_getpc_b64 s[86:87]\n" \
"L_ct0_%=:\n" \
"s_add_u32 s86, s86, L_ap0-L_ct0_%=\n" \
"s_addc_u32 s87, s87, 0\n" \
"s_add_u32 m0, s40, 0x2000\n" \
"v_mfma_f32_16x16x32_bf16 a[164:167], v[96:99], v[60:63], a[164:167]\n" \
"global_load_lds_dword v45, s[86:87]\n" \
"v_mfma_f32_16x16x32_bf16 a[164:167], v[160:163], v[124:127], a[164:167]\n" \
"ds_read_b64_tr_b16 v[112:113], v39 offset:32768\n" \
"v_mfma_f32_16x16x32_bf16 a[160:163], v[60:63], v[92:95], a[160:163]\n" \
"ds_read_b64_tr_b16 v[114:115], v39 offset:33792\n" \
"v_mfma_f32_16x16x32_bf16 a[160:163], v[124:127], v[156:159], a[160:163]\n" \
"ds_read_b64_tr_b16 v[176:177], v39 offset:40960\n" \
"v_mfma_f32_16x16x32_bf16 a[224:227], v[64:67], v[92:95], a[224:227]\n" \
"ds_read_b64_tr_b16 v[178:179], v39 offset:41984\n" \
"v_mfma_f32_16x16x32_bf16 a[224:227], v[128:131], v[156:159], a[224:227]\n" \
"v_mfma_f32_16x16x32_bf16 a[228:231], v[96:99], v[64:67], a[228:231]\n" \
"s_add_u32 s88, s42, 0x3000\n" \
"buffer_store_dwordx4 v[204:207], v20, s[80:83], s88 offen offset:3072 sc1\n" \
"v_mfma_f32_16x16x32_bf16 a[228:231], v[160:163], v[128:131], a[228:231]\n" \
"v_mfma_f32_16x16x32_bf16 a[196:199], v[88:91], v[64:67], a[196:199]\n" \
"v_mfma_f32_16x16x32_bf16 a[196:199], v[152:155], v[128:131], a[196:199]\n" \
"v_mfma_f32_16x16x32_bf16 a[192:195], v[84:87], v[64:67], a[192:195]\n" \
"v_mfma_f32_16x16x32_bf16 a[192:195], v[148:151], v[128:131], a[192:195]\n" \
"s_waitcnt lgkmcnt(15)\n" \
"v_mfma_f32_16x16x32_bf16 a[8:11], v[88:91], v[248:251], a[8:11]\n" \
"v_mfma_f32_16x16x32_bf16 a[8:11], v[152:155], v[48:51], a[8:11]\n" \
"v_mfma_f32_16x16x32_bf16 a[40:43], v[96:99], v[248:251], a[40:43]\n" \
"v_mfma_f32_16x16x32_bf16 a[40:43], v[160:163], v[48:51], a[40:43]\n" \
"v_mfma_f32_16x16x32_bf16 a[12:15], v[248:251], v[92:95], a[12:15]\n" \
"v_mfma_f32_16x16x32_bf16 a[12:15], v[48:51], v[156:159], a[12:15]\n" \
"s_waitcnt vmcnt(4) lgkmcnt(0)\n" \
"s_barrier\n" \
"s_nop 0\n" \
"v_mfma_f32_16x16x32_bf16 a[16:19], v[100:103], v[52:55], a[16:19]\n" \
"ds_read_b64_tr_b16 v[84:85], v32 offset:0\n" \
"v_accvgpr_read_b32 v184, a0\n" \
"v_mfma_f32_16x16x32_bf16 a[16:19], v[164:167], v[116:119], a[16:19]\n" \
"v_accvgpr_read_b32 v185, a1\n" \
"v_accvgpr_read_b32 v186, a2\n" \
"v_mfma_f32_16x16x32_bf16 a[20:23], v[104:107], v[52:55], a[20:23]\n" \
"ds_read_b64_tr_b16 v[86:87], v32 offset:512\n" \
"v_accvgpr_read_b32 v187, a3\n" \
"v_mfma_f32_16x16x32_bf16 a[20:23], v[168:171], v[116:119], a[20:23]\n" \
"v_accvgpr_read_b32 v188, a4\n" \
"v_accvgpr_read_b32 v189, a5\n" \
"v_mfma_f32_16x16x32_bf16 a[24:27], v[108:111], v[52:55], a[24:27]\n" \
"v_accvgpr_read_b32 v190, a6\n" \
"v_accvgpr_read_b32 v191, a7\n" \
"v_mfma_f32_16x16x32_bf16 a[24:27], v[172:175], v[116:119], a[24:27]\n" \
"ds_read_b64_tr_b16 v[148:149], v32 offset:4096\n" \
"v_accvgpr_read_b32 v196, a36\n" \
"v_mfma_f32_16x16x32_bf16 a[28:31], v[112:115], v[52:55], a[28:31]\n" \
"v_accvgpr_read_b32 v197, a37\n" \
"v_accvgpr_read_b32 v198, a38\n" \
"v_mfma_f32_16x16x32_bf16 a[28:31], v[176:179], v[116:119], a[28:31]\n" \
"ds_read_b128 v[52:55], v28 offset:0\n" \
"v_accvgpr_read_b32 v199, a39\n" \
"v_mfma_f32_16x16x32_bf16 a[60:63], v[112:115], v[56:59], a[60:63]\n" \
"ds_read_b128 v[116:119], v29 offset:0\n" \
"v_accvgpr_read_b32 v192, a32\n" \
"v_mfma_f32_16x16x32_bf16 a[60:63], v[176:179], v[120:123], a[60:63]\n" \
"ds_read_b64_tr_b16 v[150:151], v32 offset:4608\n" \
"v_accvgpr_read_b32 v193, a33\n" \
"v_mfma_f32_16x16x32_bf16 a[56:59], v[108:111], v[56:59], a[56:59]\n" \
"v_accvgpr_read_b32 v194, a34\n" \
"v_accvgpr_read_b32 v195, a35\n" \
"v_mfma_f32_16x16x32_bf16 a[56:59], v[172:175], v[120:123], a[56:59]\n" \
"ds_read_b64_tr_b16 v[88:89], v33 offset:0\n" \
"v_accvgpr_read_b32 v208, a96\n" \
"v_mfma_f32_16x16x32_bf16 a[52:55], v[104:107], v[56:59], a[52:55]\n" \
"v_accvgpr_read_b32 v209, a97\n" \
"v_accvgpr_read_b32 v210, a98\n" \
"v_mfma_f32_16x16x32_bf16 a[52:55], v[168:171], v[120:123], a[52:55]\n" \
"v_accvgpr_read_b32 v211, a99\n" \
"v_accvgpr_read_b32 v212, a100\n" \
"v_mfma_f32_16x16x32_bf16 a[48:51], v[100:103], v[56:59], a[48:51]\n" \
"ds_read_b64_tr_b16 v[90:91], v33 offset:512\n" \
"v_accvgpr_read_b32 v213, a101\n" \
"v_mfma_f32_16x16x32_bf16 a[48:51], v[164:167], v[120:123], a[48:51]\n" \
"ds_read_b128 v[56:59], v28 offset:2048\n" \
"v_accvgpr_read_b32 v214, a102\n" \
"v_mfma_f32_16x16x32_bf16 a[80:83], v[100:103], v[60:63], a[80:83]\n" \
"ds_read_b128 v[120:123], v29 offset:2048\n" \
"v_accvgpr_read_b32 v215, a103\n" \
"v_mfma_f32_16x16x32_bf16 a[80:83], v[164:167], v[124:127], a[80:83]\n" \
"ds_read_b64_tr_b16 v[152:153], v33 offset:4096\n" \
"v_accvgpr_read_b32 v204, a68\n" \
"v_mfma_f32_16x16x32_bf16 a[84:87], v[104:107], v[60:63], a[84:87]\n" \
"v_accvgpr_read_b32 v205, a69\n" \
"v_accvgpr_read_b32 v206, a70\n" \
"v_mfma_f32_16x16x32_bf16 a[84:87], v[168:171], v[124:127], a[84:87]\n" \
"v_accvgpr_read_b32 v207, a71\n" \
"v_accvgpr_read_b32 v200, a64\n" \
"v_mfma_f32_16x16x32_bf16 a[88:91], v[108:111], v[60:63], a[88:91]\n" \
"ds_read_b64_tr_b16 v[154:155], v33 offset:4608\n" \
"v_accvgpr_read_b32 v201, a65\n" \
"v_mfma_f32_16x16x32_bf16 a[88:91], v[172:175], v[124:127], a[88:91]\n" \
"v_accvgpr_read_b32 v202, a66\n" \
"v_accvgpr_read_b32 v203, a67\n" \
"v_mfma_f32_16x16x32_bf16 a[92:95], v[112:115], v[60:63], a[92:95]\n" \
"v_accvgpr_read_b32 v216, a128\n" \
"v_accvgpr_read_b32 v217, a129\n" \
"v_mfma_f32_16x16x32_bf16 a[92:95], v[176:179], v[124:127], a[92:95]\n" \
"ds_read_b128 v[60:63], v28 offset:4096\n" \
"v_accvgpr_read_b32 v218, a130\n" \
"v_mfma_f32_16x16x32_bf16 a[124:127], v[112:115], v[64:67], a[124:127]\n" \
"ds_read_b128 v[124:127], v29 offset:4096\n" \
"v_accvgpr_read_b32 v219, a131\n" \
"v_mfma_f32_16x16x32_bf16 a[124:127], v[176:179], v[128:131], a[124:127]\n" \
"ds_read_b64_tr_b16 v[96:97], v35 offset:0\n" \
"v_accvgpr_read_b32 v220, a132\n" \
"v_mfma_f32_16x16x32_bf16 a[120:123], v[108:111], v[64:67], a[120:123]\n" \
"ds_read_b64_tr_b16 v[98:99], v35 offset:512\n" \
"v_accvgpr_read_b32 v221, a133\n" \
"v_mfma_f32_16x16x32_bf16 a[120:123], v[172:175], v[128:131], a[120:123]\n" \
"v_accvgpr_read_b32 v222, a134\n" \
"v_accvgpr_read_b32 v223, a135\n" \
"v_mfma_f32_16x16x32_bf16 a[116:119], v[104:107], v[64:67], a[116:119]\n" \
"ds_read_b64_tr_b16 v[160:161], v35 offset:4096\n" \
"v_accvgpr_read_b32 v228, a164\n" \
"v_mfma_f32_16x16x32_bf16 a[116:119], v[168:171], v[128:131], a[116:119]\n" \
"v_accvgpr_read_b32 v229, a165\n" \
"v_accvgpr_read_b32 v230, a166\n" \
"v_mfma_f32_16x16x32_bf16 a[112:115], v[100:103], v[64:67], a[112:115]\n" \
"v_accvgpr_read_b32 v231, a167\n" \
"v_accvgpr_read_b32 v224, a160\n" \
"v_mfma_f32_16x16x32_bf16 a[112:115], v[164:167], v[128:131], a[112:115]\n" \
"ds_read_b128 v[64:67], v28 offset:6144\n" \
"v_accvgpr_read_b32 v225, a161\n" \
"v_mfma_f32_16x16x32_bf16 a[144:147], v[100:103], v[68:71], a[144:147]\n" \
"ds_read_b128 v[128:131], v29 offset:6144\n" \
"v_accvgpr_read_b32 v226, a162\n" \
"v_mfma_f32_16x16x32_bf16 a[144:147], v[164:167], v[132:135], a[144:147]\n" \
"ds_read_b64_tr_b16 v[162:163], v35 offset:4608\n" \
"v_accvgpr_read_b32 v227, a163\n" \
"v_mfma_f32_16x16x32_bf16 a[148:151], v[104:107], v[68:71], a[148:151]\n" \
"ds_read_b64_tr_b16 v[92:93], v34 offset:0\n" \
"v_accvgpr_read_b32 v240, a224\n" \
"v_mfma_f32_16x16x32_bf16 a[148:151], v[168:171], v[132:135], a[148:151]\n" \
"v_accvgpr_read_b32 v241, a225\n" \
"v_accvgpr_read_b32 v242, a226\n" \
"v_mfma_f32_16x16x32_bf16 a[152:155], v[108:111], v[68:71], a[152:155]\n" \
"v_accvgpr_read_b32 v243, a227\n" \
"v_accvgpr_read_b32 v244, a228\n" \
"v_mfma_f32_16x16x32_bf16 a[152:155], v[172:175], v[132:135], a[152:155]\n" \
"ds_read_b64_tr_b16 v[94:95], v34 offset:512\n" \
"v_accvgpr_read_b32 v245, a229\n" \
"v_mfma_f32_16x16x32_bf16 a[156:159], v[112:115], v[68:71], a[156:159]\n" \
"v_accvgpr_read_b32 v246, a230\n" \
"v_accvgpr_read_b32 v247, a231\n" \
"v_mfma_f32_16x16x32_bf16 a[156:159], v[176:179], v[132:135], a[156:159]\n" \
"ds_read_b128 v[68:71], v28 offset:8192\n" \
"v_accvgpr_read_b32 v236, a196\n" \
"v_mfma_f32_16x16x32_bf16 a[108:111], v[112:115], v[72:75], a[108:111]\n" \
"ds_read_b128 v[132:135], v29 offset:8192\n" \
"v_accvgpr_read_b32 v237, a197\n" \
"v_mfma_f32_16x16x32_bf16 a[108:111], v[176:179], v[136:139], a[108:111]\n" \
"ds_read_b64_tr_b16 v[156:157], v34 offset:4096\n" \
"v_accvgpr_read_b32 v238, a198\n" \
"v_mfma_f32_16x16x32_bf16 a[104:107], v[108:111], v[72:75], a[104:107]\n" \
"v_accvgpr_read_b32 v239, a199\n" \
"v_accvgpr_read_b32 v232, a192\n" \
"v_mfma_f32_16x16x32_bf16 a[104:107], v[172:175], v[136:139], a[104:107]\n" \
"ds_read_b64_tr_b16 v[158:159], v34 offset:4608\n" \
"v_accvgpr_read_b32 v233, a193\n" \
"v_mfma_f32_16x16x32_bf16 a[76:79], v[104:107], v[72:75], a[76:79]\n" \
"v_accvgpr_read_b32 v234, a194\n" \
"v_accvgpr_read_b32 v235, a195\n" \
"v_mfma_f32_16x16x32_bf16 a[76:79], v[168:171], v[136:139], a[76:79]\n" \
"v_accvgpr_read_b32 v180, a8\n" \
"v_accvgpr_read_b32 v181, a9\n" \
"v_mfma_f32_16x16x32_bf16 a[72:75], v[100:103], v[72:75], a[72:75]\n" \
"ds_read_b128 v[248:251], v22 offset:0\n" \
"v_accvgpr_read_b32 v182, a10\n" \
"v_mfma_f32_16x16x32_bf16 a[72:75], v[164:167], v[136:139], a[72:75]\n" \
"ds_read_b128 v[72:75], v28 offset:10240\n" \
"v_accvgpr_read_b32 v183, a11\n" \
"v_mfma_f32_16x16x32_bf16 a[136:139], v[100:103], v[76:79], a[136:139]\n" \
"ds_read_b128 v[136:139], v29 offset:10240\n" \
"v_accvgpr_read_b32 v252, a12\n" \
"v_mfma_f32_16x16x32_bf16 a[136:139], v[164:167], v[140:143], a[136:139]\n" \
"ds_read_b128 v[48:51], v23 offset:0\n" \
"v_accvgpr_read_b32 v253, a13\n" \
"v_mfma_f32_16x16x32_bf16 a[140:143], v[104:107], v[76:79], a[140:143]\n" \
"v_accvgpr_read_b32 v254, a14\n" \
"v_accvgpr_read_b32 v255, a15\n" \
"v_mfma_f32_16x16x32_bf16 a[140:143], v[168:171], v[140:143], a[140:143]\n" \
"v_lshrrev_b32_e64 v24, 9, v20\n" \
"v_and_b32_e32 v26, 0x1ff, v20\n" \
"v_mfma_f32_16x16x32_bf16 a[168:171], v[108:111], v[76:79], a[168:171]\n" \
"v_lshl_add_u32 v24, v24, 3, v26\n" \
"v_lshrrev_b32_e64 v25, 8, v20\n" \
"v_mfma_f32_16x16x32_bf16 a[168:171], v[172:175], v[140:143], a[168:171]\n" \
"v_mul_u32_u24_e32 v25, 0x24700, v25\n" \
"v_add_u32_e64 v25, v25, v20\n" \
"v_mfma_f32_16x16x32_bf16 a[172:175], v[112:115], v[76:79], a[172:175]\n" \
"v_lshrrev_b32_e32 v26, 2, v20\n" \
"v_add_u32_e32 v26, s45, v26\n" \
"v_mfma_f32_16x16x32_bf16 a[172:175], v[176:179], v[140:143], a[172:175]\n" \
"ds_read_b128 v[76:79], v28 offset:12288\n" \
"v_add_u32_e64 v27, s46, v20\n" \
"v_mfma_f32_16x16x32_bf16 a[236:239], v[112:115], v[80:83], a[236:239]\n" \
"ds_read_b128 v[140:143], v29 offset:12288\n" \
"v_add_u32_e64 v21, s43, v27\n" \
"v_mfma_f32_16x16x32_bf16 a[236:239], v[176:179], v[144:147], a[236:239]\n" \
"v_mfma_f32_16x16x32_bf16 a[232:235], v[108:111], v[80:83], a[232:235]\n" \
"v_mfma_f32_16x16x32_bf16 a[232:235], v[172:175], v[144:147], a[232:235]\n" \
"v_mfma_f32_16x16x32_bf16 a[204:207], v[104:107], v[80:83], a[204:207]\n" \
"v_mfma_f32_16x16x32_bf16 a[204:207], v[168:171], v[144:147], a[204:207]\n" \
"v_mfma_f32_16x16x32_bf16 a[200:203], v[100:103], v[80:83], a[200:203]\n" \
"v_mfma_f32_16x16x32_bf16 a[200:203], v[164:167], v[144:147], a[200:203]\n" \
"ds_read_b128 v[80:83], v28 offset:14336\n" \
"ds_read_b128 v[144:147], v29 offset:14336\n" \
"s_waitcnt lgkmcnt(15)\n" \
"s_nop 0\n" \
"v_mfma_f32_16x16x32_bf16 v[184:187], v[84:87], v[52:55], v[184:187]\n" \
"v_bfe_u32 v44, v20, 2, 5\n" \
"ds_read_b64_tr_b16 v[100:101], v36 offset:0\n" \
"v_mfma_f32_16x16x32_bf16 v[184:187], v[148:151], v[116:119], v[184:187]\n" \
"buffer_load_dword v44, v44, s[80:83], s38 offen offset:0 sc1\n" \
"v_mfma_f32_16x16x32_bf16 v[188:191], v[88:91], v[52:55], v[188:191]\n" \
"ds_read_b64_tr_b16 v[102:103], v36 offset:1024\n" \
"v_mfma_f32_16x16x32_bf16 v[188:191], v[152:155], v[116:119], v[188:191]\n" \
"ds_read_b64_tr_b16 v[164:165], v36 offset:8192\n" \
"v_mfma_f32_16x16x32_bf16 v[196:199], v[96:99], v[52:55], v[196:199]\n" \
"ds_read_b64_tr_b16 v[166:167], v36 offset:9216\n" \
"s_waitcnt lgkmcnt(15)\n" \
"v_mfma_f32_16x16x32_bf16 v[196:199], v[160:163], v[116:119], v[196:199]\n" \
"v_mfma_f32_16x16x32_bf16 v[192:195], v[52:55], v[92:95], v[192:195]\n" \
"ds_read_b64_tr_b16 v[104:105], v37 offset:0\n" \
"s_waitcnt lgkmcnt(13)\n" \
"v_mfma_f32_16x16x32_bf16 v[192:195], v[116:119], v[156:159], v[192:195]\n" \
"v_mfma_f32_16x16x32_bf16 v[208:211], v[56:59], v[92:95], v[208:211]\n" \
"ds_read_b64_tr_b16 v[106:107], v37 offset:1024\n" \
"v_mfma_f32_16x16x32_bf16 v[208:211], v[120:123], v[156:159], v[208:211]\n" \
"ds_read_b64_tr_b16 v[168:169], v37 offset:8192\n" \
"v_mfma_f32_16x16x32_bf16 v[212:215], v[96:99], v[56:59], v[212:215]\n" \
"v_mfma_f32_16x16x32_bf16 v[212:215], v[160:163], v[120:123], v[212:215]\n" \
"ds_read_b64_tr_b16 v[170:171], v37 offset:9216\n" \
"v_cvt_pk_bf16_f32 v188, v188, v189\n" \
"v_mfma_f32_16x16x32_bf16 v[204:207], v[88:91], v[56:59], v[204:207]\n" \
"v_cvt_pk_bf16_f32 v189, v190, v191\n" \
"v_cvt_pk_bf16_f32 v190, v198, v199\n" \
"v_mfma_f32_16x16x32_bf16 v[204:207], v[152:155], v[120:123], v[204:207]\n" \
"ds_read_b64_tr_b16 v[108:109], v38 offset:0\n" \
"v_mov_b32_e32 v191, 0x3f80\n" \
"v_mfma_f32_16x16x32_bf16 v[200:203], v[84:87], v[56:59], v[200:203]\n" \
"ds_read_b64_tr_b16 v[110:111], v38 offset:1024\n" \
"v_cvt_pk_bf16_f32 v184, v184, v185\n" \
"v_mfma_f32_16x16x32_bf16 v[200:203], v[148:151], v[120:123], v[200:203]\n" \
"v_cvt_pk_bf16_f32 v185, v186, v187\n" \
"v_cvt_pk_bf16_f32 v186, v196, v197\n" \
"v_mfma_f32_16x16x32_bf16 v[216:219], v[84:87], v[60:63], v[216:219]\n" \
"ds_read_b64_tr_b16 v[172:173], v38 offset:8192\n" \
"v_mov_b32_e64 v187, 0\n" \
"v_mfma_f32_16x16x32_bf16 v[216:219], v[148:151], v[124:127], v[216:219]\n" \
"ds_read_b64_tr_b16 v[174:175], v38 offset:9216\n" \
"v_mfma_f32_16x16x32_bf16 v[220:223], v[88:91], v[60:63], v[220:223]\n" \
"v_mfma_f32_16x16x32_bf16 v[220:223], v[152:155], v[124:127], v[220:223]\n" \
"ds_read_b64_tr_b16 v[112:113], v39 offset:0\n" \
"v_cvt_pk_bf16_f32 v204, v204, v205\n" \
"v_mfma_f32_16x16x32_bf16 v[228:231], v[96:99], v[60:63], v[228:231]\n" \
"v_cvt_pk_bf16_f32 v205, v206, v207\n" \
"v_cvt_pk_bf16_f32 v206, v214, v215\n" \
"v_mfma_f32_16x16x32_bf16 v[228:231], v[160:163], v[124:127], v[228:231]\n" \
"ds_read_b64_tr_b16 v[114:115], v39 offset:1024\n" \
"v_mov_b32_e32 v207, 0x3f80\n" \
"v_mfma_f32_16x16x32_bf16 v[224:227], v[60:63], v[92:95], v[224:227]\n" \
"ds_read_b64_tr_b16 v[176:177], v39 offset:8192\n" \
"v_cvt_pk_bf16_f32 v200, v200, v201\n" \
"v_mfma_f32_16x16x32_bf16 v[224:227], v[124:127], v[156:159], v[224:227]\n" \
"v_cvt_pk_bf16_f32 v201, v202, v203\n" \
"v_cvt_pk_bf16_f32 v202, v212, v213\n" \
"v_mfma_f32_16x16x32_bf16 v[240:243], v[64:67], v[92:95], v[240:243]\n" \
"ds_read_b64_tr_b16 v[178:179], v39 offset:9216\n" \
"v_mov_b32_e64 v203, 0\n" \
"v_mfma_f32_16x16x32_bf16 v[240:243], v[128:131], v[156:159], v[240:243]\n" \
"v_mfma_f32_16x16x32_bf16 v[244:247], v[96:99], v[64:67], v[244:247]\n" \
"v_mfma_f32_16x16x32_bf16 v[244:247], v[160:163], v[128:131], v[244:247]\n" \
"v_cvt_pk_bf16_f32 v220, v220, v221\n" \
"v_cvt_pk_bf16_f32 v221, v222, v223\n" \
"v_mfma_f32_16x16x32_bf16 v[236:239], v[88:91], v[64:67], v[236:239]\n" \
"v_cvt_pk_bf16_f32 v222, v230, v231\n" \
"v_mov_b32_e32 v223, 0x3f80\n" \
"v_mfma_f32_16x16x32_bf16 v[236:239], v[152:155], v[128:131], v[236:239]\n" \
"v_cvt_pk_bf16_f32 v216, v216, v217\n" \
"v_cvt_pk_bf16_f32 v217, v218, v219\n" \
"v_mfma_f32_16x16x32_bf16 v[232:235], v[84:87], v[64:67], v[232:235]\n" \
"v_cvt_pk_bf16_f32 v218, v228, v229\n" \
"v_mov_b32_e64 v219, 0\n" \
"v_mfma_f32_16x16x32_bf16 v[232:235], v[148:151], v[128:131], v[232:235]\n" \
"s_waitcnt lgkmcnt(15)\n" \
"s_nop 0\n" \
"v_mfma_f32_16x16x32_bf16 v[180:183], v[88:91], v[248:251], v[180:183]\n" \
"v_mfma_f32_16x16x32_bf16 v[180:183], v[152:155], v[48:51], v[180:183]\n" \
"v_mfma_f32_16x16x32_bf16 a[40:43], v[96:99], v[248:251], a[40:43]\n" \
"v_mfma_f32_16x16x32_bf16 a[40:43], v[160:163], v[48:51], a[40:43]\n" \
"v_cvt_pk_bf16_f32 v236, v236, v237\n" \
"v_cvt_pk_bf16_f32 v237, v238, v239\n" \
"v_mfma_f32_16x16x32_bf16 v[252:255], v[248:251], v[92:95], v[252:255]\n" \
"v_cvt_pk_bf16_f32 v238, v246, v247\n" \
"v_mov_b32_e32 v239, 0x3f80\n" \
"v_mfma_f32_16x16x32_bf16 v[252:255], v[48:51], v[156:159], v[252:255]\n" \
"v_cvt_pk_bf16_f32 v232, v232, v233\n" \
"v_cvt_pk_bf16_f32 v233, v234, v235\n" \
"s_waitcnt vmcnt(0) lgkmcnt(0)\n" \
"s_barrier\n" \
"v_mov_b32_e32 v45, s62\n" \
"s_mov_b64 exec, 1\n" \
"buffer_store_dword v45, off, s[80:83], s37 offset:32 sc1\n" \
"s_mov_b64 exec, -1\n" \
"v_cmp_ne_u32_e32 vcc, s62, v44\n" \
"s_nop 4\n" \
"s_cbranch_vccnz L_poll160_%=\n" \
"L_pollr160_%=:\n" \
"v_mfma_f32_16x16x32_bf16 a[16:19], v[100:103], v[52:55], a[16:19]\n" \
"s_mov_b32 s73, s50\n" \
"s_sub_u32 m0, s31, 0x10000\n" \
"v_mfma_f32_16x16x32_bf16 a[16:19], v[164:167], v[116:119], a[16:19]\n" \
"buffer_load_dwordx4 v20, s[80:83], s73 offen offset:0 sc1 lds\n" \
"v_mfma_f32_16x16x32_bf16 a[20:23], v[104:107], v[52:55], a[20:23]\n" \
"s_add_u32 s74, s50, 0x4000\n" \
"s_add_u32 s75, s50, 0x8000\n" \
"v_mfma_f32_16x16x32_bf16 a[20:23], v[168:171], v[116:119], a[20:23]\n" \
"s_mov_b32 s70, 0x3e0293ee\n" \
"s_mov_b32 s76, s52\n" \
"v_mfma_f32_16x16x32_bf16 a[24:27], v[108:111], v[52:55], a[24:27]\n" \
"v_accvgpr_read_b32 v32, a16\n" \
"v_accvgpr_read_b32 v33, a17\n" \
"v_mfma_f32_16x16x32_bf16 a[24:27], v[172:175], v[116:119], a[24:27]\n" \
"buffer_load_dwordx4 v20, s[80:83], s73 offen offset:1024 sc1 lds\n" \
"v_mfma_f32_16x16x32_bf16 a[28:31], v[112:115], v[52:55], a[28:31]\n" \
"v_accvgpr_read_b32 v34, a18\n" \
"v_accvgpr_read_b32 v35, a19\n" \
"v_mfma_f32_16x16x32_bf16 a[28:31], v[176:179], v[116:119], a[28:31]\n" \
"v_mul_f32_e32 v32, s70, v32\n" \
"v_mul_f32_e32 v33, s70, v33\n" \
"v_mfma_f32_16x16x32_bf16 a[60:63], v[112:115], v[56:59], a[60:63]\n" \
"v_accvgpr_read_b32 v36, a20\n" \
"v_accvgpr_read_b32 v37, a21\n" \
"v_mfma_f32_16x16x32_bf16 a[60:63], v[176:179], v[120:123], a[60:63]\n" \
"buffer_load_dwordx4 v20, s[80:83], s73 offen offset:2048 sc1 lds\n" \
"v_mfma_f32_16x16x32_bf16 a[56:59], v[108:111], v[56:59], a[56:59]\n" \
"v_accvgpr_read_b32 v38, a22\n" \
"v_accvgpr_read_b32 v39, a23\n" \
"v_mfma_f32_16x16x32_bf16 a[56:59], v[172:175], v[120:123], a[56:59]\n" \
"v_mul_f32_e32 v34, s70, v34\n" \
"v_mul_f32_e32 v35, s70, v35\n" \
"v_mfma_f32_16x16x32_bf16 a[52:55], v[104:107], v[56:59], a[52:55]\n" \
"v_cvt_pk_bf16_f32 v32, v32, v33\n" \
"v_mul_f32_e64 v36, s70, v36\n" \
"v_mfma_f32_16x16x32_bf16 a[52:55], v[168:171], v[120:123], a[52:55]\n" \
"buffer_load_dwordx4 v20, s[80:83], s73 offen offset:3072 sc1 lds\n" \
"v_mfma_f32_16x16x32_bf16 a[48:51], v[100:103], v[56:59], a[48:51]\n" \
"s_sub_u32 m0, s31, s51\n" \
"v_mul_f32_e32 v37, s70, v37\n" \
"v_mfma_f32_16x16x32_bf16 a[48:51], v[164:167], v[120:123], a[48:51]\n" \
"v_mul_f32_e32 v38, s70, v38\n" \
"v_mul_f32_e32 v39, s70, v39\n" \
"v_mfma_f32_16x16x32_bf16 a[80:83], v[100:103], v[60:63], a[80:83]\n" \
"v_cvt_pk_bf16_f32 v33, v34, v35\n" \
"v_cvt_pk_bf16_f32 v34, v36, v37\n" \
"v_mfma_f32_16x16x32_bf16 a[80:83], v[164:167], v[124:127], a[80:83]\n" \
"buffer_load_dwordx4 v20, s[80:83], s74 offen offset:0 sc1 lds\n" \
"v_mfma_f32_16x16x32_bf16 a[84:87], v[104:107], v[60:63], a[84:87]\n" \
"v_cvt_pk_bf16_f32 v35, v38, v39\n" \
"v_accvgpr_read_b32 v36, a28\n" \
"v_mfma_f32_16x16x32_bf16 a[84:87], v[168:171], v[124:127], a[84:87]\n" \
"ds_write_b128 v27, v[32:35] offset:0\n" \
"v_mfma_f32_16x16x32_bf16 a[88:91], v[108:111], v[60:63], a[88:91]\n" \
"v_accvgpr_read_b32 v32, a24\n" \
"v_accvgpr_read_b32 v33, a25\n" \
"v_mfma_f32_16x16x32_bf16 a[88:91], v[172:175], v[124:127], a[88:91]\n" \
"buffer_load_dwordx4 v20, s[80:83], s74 offen offset:1024 sc1 lds\n" \
"v_mfma_f32_16x16x32_bf16 a[92:95], v[112:115], v[60:63], a[92:95]\n" \
"v_accvgpr_read_b32 v34, a26\n" \
"v_accvgpr_read_b32 v35, a27\n" \
"v_mfma_f32_16x16x32_bf16 a[92:95], v[176:179], v[124:127], a[92:95]\n" \
"v_mul_f32_e32 v32, s70, v32\n" \
"v_mul_f32_e32 v33, s70, v33\n" \
"v_mfma_f32_16x16x32_bf16 a[124:127], v[112:115], v[64:67], a[124:127]\n" \
"v_accvgpr_read_b32 v37, a29\n" \
"v_accvgpr_read_b32 v38, a30\n" \
"v_mfma_f32_16x16x32_bf16 a[124:127], v[176:179], v[128:131], a[124:127]\n" \
"buffer_load_dwordx4 v20, s[80:83], s74 offen offset:2048 sc1 lds\n" \
"v_mfma_f32_16x16x32_bf16 a[120:123], v[108:111], v[64:67], a[120:123]\n" \
"v_accvgpr_read_b32 v39, a31\n" \
"v_mul_f32_e64 v34, s70, v34\n" \
"v_mfma_f32_16x16x32_bf16 a[120:123], v[172:175], v[128:131], a[120:123]\n" \
"v_mul_f32_e64 v35, s70, v35\n" \
"v_cvt_pk_bf16_f32 v32, v32, v33\n" \
"v_mfma_f32_16x16x32_bf16 a[116:119], v[104:107], v[64:67], a[116:119]\n" \
"v_mul_f32_e32 v36, s70, v36\n" \
"v_mul_f32_e32 v37, s70, v37\n" \
"v_mfma_f32_16x16x32_bf16 a[116:119], v[168:171], v[128:131], a[116:119]\n" \
"buffer_load_dwordx4 v20, s[80:83], s74 offen offset:3072 sc1 lds\n" \
"v_mfma_f32_16x16x32_bf16 a[112:115], v[100:103], v[64:67], a[112:115]\n" \
"s_sub_u32 m0, s31, 0x4000\n" \
"v_mul_f32_e64 v38, s70, v38\n" \
"v_mfma_f32_16x16x32_bf16 a[112:115], v[164:167], v[128:131], a[112:115]\n" \
"v_mul_f32_e64 v39, s70, v39\n" \
"v_cvt_pk_bf16_f32 v33, v34, v35\n" \
"v_mfma_f32_16x16x32_bf16 a[144:147], v[100:103], v[68:71], a[144:147]\n" \
"v_cvt_pk_bf16_f32 v180, v180, v181\n" \
"v_cvt_pk_bf16_f32 v34, v36, v37\n" \
"v_mfma_f32_16x16x32_bf16 a[144:147], v[164:167], v[132:135], a[144:147]\n" \
"buffer_load_dwordx4 v20, s[80:83], s75 offen offset:0 sc1 lds\n" \
"v_mfma_f32_16x16x32_bf16 a[148:151], v[104:107], v[68:71], a[148:151]\n" \
"v_cvt_pk_bf16_f32 v35, v38, v39\n" \
"v_accvgpr_read_b32 v50, a42\n" \
"v_mfma_f32_16x16x32_bf16 a[148:151], v[168:171], v[132:135], a[148:151]\n" \
"v_accvgpr_read_b32 v51, a43\n" \
"v_cvt_pk_bf16_f32 v181, v182, v183\n" \
"v_mfma_f32_16x16x32_bf16 a[152:155], v[108:111], v[68:71], a[152:155]\n" \
"ds_write_b128 v27, v[32:35] offset:1024\n" \
"v_mfma_f32_16x16x32_bf16 a[152:155], v[172:175], v[132:135], a[152:155]\n" \
"buffer_load_dwordx4 v20, s[80:83], s75 offen offset:1024 sc1 lds\n" \
"v_mfma_f32_16x16x32_bf16 a[156:159], v[112:115], v[68:71], a[156:159]\n" \
"v_cvt_pk_bf16_f32 v182, v50, v51\n" \
"v_mov_b32_e32 v183, 0x3f80\n" \
"v_mfma_f32_16x16x32_bf16 a[156:159], v[176:179], v[132:135], a[156:159]\n" \
"v_cvt_pk_bf16_f32 v234, v244, v245\n" \
"v_mov_b32_e64 v235, 0\n" \
"v_mfma_f32_16x16x32_bf16 a[108:111], v[112:115], v[72:75], a[108:111]\n" \
"v_cvt_pk_bf16_f32 v86, v192, v193\n" \
"v_cvt_pk_bf16_f32 v87, v194, v195\n" \
"v_mfma_f32_16x16x32_bf16 a[108:111], v[176:179], v[136:139], a[108:111]\n" \
"buffer_load_dwordx4 v20, s[80:83], s75 offen offset:2048 sc1 lds\n" \
"v_mfma_f32_16x16x32_bf16 a[104:107], v[108:111], v[72:75], a[104:107]\n" \
"v_cvt_pk_bf16_f32 v88, v208, v209\n" \
"v_cvt_pk_bf16_f32 v89, v210, v211\n" \
"v_mfma_f32_16x16x32_bf16 a[104:107], v[172:175], v[136:139], a[104:107]\n" \
"v_cvt_pk_bf16_f32 v90, v224, v225\n" \
"v_cvt_pk_bf16_f32 v91, v226, v227\n" \
"v_mfma_f32_16x16x32_bf16 a[76:79], v[104:107], v[72:75], a[76:79]\n" \
"v_cvt_pk_bf16_f32 v92, v240, v241\n" \
"v_cvt_pk_bf16_f32 v93, v242, v243\n" \
"v_mfma_f32_16x16x32_bf16 a[76:79], v[168:171], v[136:139], a[76:79]\n" \
"buffer_load_dwordx4 v20, s[80:83], s75 offen offset:3072 sc1 lds\n" \
"v_mfma_f32_16x16x32_bf16 a[72:75], v[100:103], v[72:75], a[72:75]\n" \
"s_mov_b32 m0, s53\n" \
"s_nop 0\n" \
"v_accvgpr_read_b32 v116, a48\n" \
"v_mfma_f32_16x16x32_bf16 a[72:75], v[164:167], v[136:139], a[72:75]\n" \
"v_mfma_f32_16x16x32_bf16 v[148:151], v[180:183], v[184:187], 0\n" \
"v_accvgpr_read_b32 v117, a49\n" \
"v_accvgpr_read_b32 v32, a56\n" \
"v_mfma_f32_16x16x32_bf16 a[136:139], v[100:103], v[76:79], a[136:139]\n" \
"buffer_load_dwordx4 v20, s[80:83], s76 offen offset:0 sc1 lds\n" \
"v_mfma_f32_16x16x32_bf16 a[136:139], v[164:167], v[140:143], a[136:139]\n" \
"v_accvgpr_read_b32 v33, a57\n" \
"v_accvgpr_read_b32 v118, a50\n" \
"v_mfma_f32_16x16x32_bf16 a[140:143], v[104:107], v[76:79], a[140:143]\n" \
"v_mfma_f32_16x16x32_bf16 v[192:195], v[188:191], v[184:187], 0\n" \
"v_mfma_f32_16x16x32_bf16 v[208:211], v[204:207], v[200:203], 0\n" \
"buffer_load_dwordx4 v20, s[80:83], s76 offen offset:1024 sc1 lds\n" \
"v_mfma_f32_16x16x32_bf16 a[140:143], v[168:171], v[140:143], a[140:143]\n" \
"v_mfma_f32_16x16x32_bf16 v[152:155], v[188:191], v[200:203], 0\n" \
"v_mfma_f32_16x16x32_bf16 v[224:227], v[220:223], v[216:219], 0\n" \
"v_mfma_f32_16x16x32_bf16 v[156:159], v[204:207], v[216:219], 0\n" \
"v_mfma_f32_16x16x32_bf16 v[240:243], v[236:239], v[232:235], 0\n" \
"v_mfma_f32_16x16x32_bf16 v[160:163], v[220:223], v[232:235], 0\n" \
"v_accvgpr_read_b32 v119, a51\n" \
"v_mul_f32_e64 v116, s70, v116\n" \
"v_mfma_f32_16x16x32_bf16 a[168:171], v[108:111], v[76:79], a[168:171]\n" \
"v_mul_f32_e64 v117, s70, v117\n" \
"v_accvgpr_read_b32 v34, a58\n" \
"v_accvgpr_read_b32 v35, a59\n" \
"v_mul_f32_e32 v32, s70, v32\n" \
"v_mul_f32_e32 v33, s70, v33\n" \
"v_accvgpr_read_b32 v120, a52\n" \
"v_accvgpr_read_b32 v121, a53\n" \
"v_accvgpr_read_b32 v122, a54\n" \
"v_accvgpr_read_b32 v123, a55\n" \
"v_mul_f32_e32 v118, s70, v118\n" \
"v_mul_f32_e32 v119, s70, v119\n" \
"v_cvt_pk_bf16_f32 v116, v116, v117\n" \
"v_accvgpr_read_b32 v36, a60\n" \
"v_accvgpr_read_b32 v37, a61\n" \
"v_accvgpr_read_b32 v38, a62\n" \
"v_accvgpr_read_b32 v39, a63\n" \
"v_mul_f32_e32 v34, s70, v34\n" \
"v_mul_f32_e32 v35, s70, v35\n" \
"v_cvt_pk_bf16_f32 v32, v32, v33\n" \
"v_mul_f32_e32 v120, s70, v120\n" \
"v_mul_f32_e32 v121, s70, v121\n" \
"v_mul_f32_e32 v122, s70, v122\n" \
"v_mul_f32_e32 v123, s70, v123\n" \
"v_cvt_pk_bf16_f32 v117, v118, v119\n" \
"v_mul_f32_e32 v36, s70, v36\n" \
"v_mul_f32_e32 v37, s70, v37\n" \
"v_mul_f32_e32 v38, s70, v38\n" \
"v_mul_f32_e32 v39, s70, v39\n" \
"v_cvt_pk_bf16_f32 v33, v34, v35\n" \
"v_cvt_pk_bf16_f32 v118, v120, v121\n" \
"v_cvt_pk_bf16_f32 v119, v122, v123\n" \
"v_cvt_pk_bf16_f32 v34, v36, v37\n" \
"v_cvt_pk_bf16_f32 v35, v38, v39\n" \
"v_mfma_f32_16x16x32_bf16 a[168:171], v[172:175], v[140:143], a[168:171]\n" \
"ds_write_b128 v27, v[116:119] offset:4096\n" \
"ds_write_b128 v27, v[32:35] offset:5120\n" \
"v_accvgpr_read_b32 v116, a80\n" \
"v_accvgpr_read_b32 v117, a81\n" \
"v_accvgpr_read_b32 v32, a88\n" \
"v_accvgpr_read_b32 v33, a89\n" \
"v_accvgpr_read_b32 v124, a112\n" \
"v_accvgpr_read_b32 v125, a113\n" \
"v_accvgpr_read_b32 v118, a82\n" \
"v_accvgpr_read_b32 v119, a83\n" \
"v_mul_f32_e32 v116, s70, v116\n" \
"v_mul_f32_e32 v117, s70, v117\n" \
"v_accvgpr_read_b32 v34, a90\n" \
"v_accvgpr_read_b32 v35, a91\n" \
"v_mul_f32_e32 v32, s70, v32\n" \
"v_mul_f32_e32 v33, s70, v33\n" \
"v_accvgpr_read_b32 v126, a114\n" \
"v_accvgpr_read_b32 v127, a115\n" \
".fill (L_pb + 40672 - .) / 4, 4, 0xBF800000\n" \
"L_pp10:\n" \
"s_cbranch_execz L_pret\n" \
"v_mul_f32_e32 v124, s70, v124\n" \
"v_mul_f32_e64 v125, s70, v125\n" \
"v_mfma_f32_16x16x32_bf16 a[172:175], v[112:115], v[76:79], a[172:175]\n" \
"s_mov_b32 s64, 0xaaaaaaaa\n" \
"s_mov_b32 s65, 0xaaaaaaaa\n" \
"v_accvgpr_read_b32 v120, a84\n" \
"v_accvgpr_read_b32 v121, a85\n" \
"v_accvgpr_read_b32 v122, a86\n" \
"v_accvgpr_read_b32 v123, a87\n" \
"v_mul_f32_e32 v118, s70, v118\n" \
"v_mul_f32_e32 v119, s70, v119\n" \
"v_cvt_pk_bf16_f32 v116, v116, v117\n" \
"v_accvgpr_read_b32 v36, a92\n" \
"v_accvgpr_read_b32 v37, a93\n" \
"v_accvgpr_read_b32 v38, a94\n" \
"v_accvgpr_read_b32 v39, a95\n" \
"v_mul_f32_e32 v34, s70, v34\n" \
"v_mul_f32_e32 v35, s70, v35\n" \
"v_cvt_pk_bf16_f32 v32, v32, v33\n" \
"v_accvgpr_read_b32 v128, a116\n" \
"v_accvgpr_read_b32 v129, a117\n" \
"v_accvgpr_read_b32 v130, a118\n" \
"v_accvgpr_read_b32 v131, a119\n" \
"v_mul_f32_e32 v126, s70, v126\n" \
"v_mul_f32_e32 v127, s70, v127\n" \
"v_cvt_pk_bf16_f32 v124, v124, v125\n" \
"v_cndmask_b32_e64 v148, v148, v149, s[64:65]\n" \
"v_cndmask_b32_e64 v150, v150, v151, s[64:65]\n" \
"v_mul_f32_e32 v120, s70, v120\n" \
"v_mul_f32_e32 v121, s70, v121\n" \
"v_mul_f32_e32 v122, s70, v122\n" \
"v_mul_f32_e32 v123, s70, v123\n" \
"v_cvt_pk_bf16_f32 v117, v118, v119\n" \
"v_mul_f32_e32 v36, s70, v36\n" \
"v_mul_f32_e32 v37, s70, v37\n" \
"v_mul_f32_e32 v38, s70, v38\n" \
"v_mul_f32_e32 v39, s70, v39\n" \
"v_cvt_pk_bf16_f32 v33, v34, v35\n" \
"v_mul_f32_e32 v128, s70, v128\n" \
"v_mul_f32_e32 v129, s70, v129\n" \
"v_mul_f32_e32 v130, s70, v130\n" \
"v_mul_f32_e32 v131, s70, v131\n" \
"v_cvt_pk_bf16_f32 v125, v126, v127\n" \
"s_mov_b32 s66, 0xfff0ffff\n" \
"s_mov_b32 s67, 0xf000ff00\n" \
"s_mov_b32 s68, 0xffc0fffc\n" \
"s_mov_b32 s69, 0xc000fc00\n" \
"v_cndmask_b32_e64 v192, v192, v193, s[64:65]\n" \
"v_add_f32_e64 v148, s47, v148\n" \
"v_cndmask_b32_e64 v194, v194, v195, s[64:65]\n" \
"v_add_f32_e64 v150, s47, v150\n" \
"v_cndmask_b32_e64 v208, v208, v209, s[64:65]\n" \
"v_cndmask_b32_e64 v152, v152, v153, s[64:65]\n" \
"v_cndmask_b32_e64 v210, v210, v211, s[64:65]\n" \
"v_cndmask_b32_e64 v154, v154, v155, s[64:65]\n" \
"v_cndmask_b32_e64 v224, v224, v225, s[64:65]\n" \
"v_cndmask_b32_e64 v156, v156, v157, s[64:65]\n" \
"v_cndmask_b32_e64 v226, v226, v227, s[64:65]\n" \
"v_cndmask_b32_e64 v158, v158, v159, s[64:65]\n" \
"v_cndmask_b32_e64 v240, v240, v241, s[64:65]\n" \
"v_cndmask_b32_e64 v160, v160, v161, s[64:65]\n" \
"v_cndmask_b32_e64 v242, v242, v243, s[64:65]\n" \
"v_cndmask_b32_e64 v162, v162, v163, s[64:65]\n" \
"v_cvt_pk_bf16_f32 v118, v120, v121\n" \
"v_cvt_pk_bf16_f32 v119, v122, v123\n" \
"v_cvt_pk_bf16_f32 v34, v36, v37\n" \
"v_cvt_pk_bf16_f32 v35, v38, v39\n" \
"v_cvt_pk_bf16_f32 v126, v128, v129\n" \
"v_cvt_pk_bf16_f32 v127, v130, v131\n" \
"v_cndmask_b32_e64 v192, v148, v192, s[66:67]\n" \
"v_cndmask_b32_e64 v194, v150, v194, s[68:69]\n" \
"v_cndmask_b32_e64 v208, v152, v208, s[66:67]\n" \
"v_cndmask_b32_e64 v210, v154, v210, s[68:69]\n" \
"v_cndmask_b32_e64 v224, v156, v224, s[66:67]\n" \
"v_cndmask_b32_e64 v226, v158, v226, s[68:69]\n" \
"v_cndmask_b32_e64 v240, v160, v240, s[66:67]\n" \
"v_cndmask_b32_e64 v242, v162, v242, s[68:69]\n" \
"ds_write_b128 v27, v[116:119] offset:8192\n" \
"ds_write_b128 v27, v[32:35] offset:9216\n" \
"ds_write_b128 v27, v[124:127] offset:12288\n" \
"v_mfma_f32_16x16x32_bf16 a[172:175], v[176:179], v[140:143], a[172:175]\n" \
"v_max_f32_e32 v193, v192, v194\n" \
"v_max_f32_e32 v209, v208, v210\n" \
"v_max_f32_e32 v225, v224, v226\n" \
"v_max_f32_e32 v241, v240, v242\n" \
"s_mov_b32 s71, 0x3e0293ee\n" \
"v_permlane32_swap_b32_e32 v193, v225\n" \
"v_permlane32_swap_b32_e32 v209, v241\n" \
"v_accvgpr_read_b32 v116, a120\n" \
"v_accvgpr_read_b32 v117, a121\n" \
"v_accvgpr_read_b32 v32, a144\n" \
"v_accvgpr_read_b32 v33, a145\n" \
"v_accvgpr_read_b32 v124, a152\n" \
"v_accvgpr_read_b32 v125, a153\n" \
"v_accvgpr_read_b32 v132, a72\n" \
"v_accvgpr_read_b32 v133, a73\n" \
"v_permlane32_swap_b32_e32 v196, v228\n" \
"v_permlane32_swap_b32_e32 v212, v244\n" \
"v_max_f32_e32 v193, v193, v225\n" \
"v_max_f32_e32 v209, v209, v241\n" \
"v_accvgpr_read_b32 v118, a122\n" \
"v_accvgpr_read_b32 v119, a123\n" \
"v_mul_f32_e32 v116, s70, v116\n" \
"v_mul_f32_e32 v117, s70, v117\n" \
"v_accvgpr_read_b32 v34, a146\n" \
"v_accvgpr_read_b32 v35, a147\n" \
"v_pk_mul_f32 v[32:33], v[32:33], s[70:71]\n" \
"v_accvgpr_read_b32 v126, a154\n" \
"v_accvgpr_read_b32 v127, a155\n" \
"v_pk_mul_f32 v[124:125], v[124:125], s[70:71]\n" \
"v_accvgpr_read_b32 v134, a74\n" \
"v_accvgpr_read_b32 v135, a75\n" \
"v_pk_mul_f32 v[132:133], v[132:133], s[70:71]\n" \
"v_accvgpr_read_b32 v48, a40\n" \
"v_accvgpr_read_b32 v49, a41\n" \
"v_permlane16_swap_b32_e32 v228, v244\n" \
"v_permlane16_swap_b32_e32 v193, v209\n" \
"v_accvgpr_read_b32 v120, a124\n" \
"v_accvgpr_read_b32 v121, a125\n" \
"v_accvgpr_read_b32 v122, a126\n" \
"v_accvgpr_read_b32 v123, a127\n" \
"v_mul_f32_e32 v118, s70, v118\n" \
"v_mul_f32_e32 v119, s70, v119\n" \
"v_cvt_pk_bf16_f32 v116, v116, v117\n" \
"v_accvgpr_read_b32 v36, a148\n" \
"v_accvgpr_read_b32 v37, a149\n" \
"v_accvgpr_read_b32 v38, a150\n" \
"v_accvgpr_read_b32 v39, a151\n" \
"v_pk_mul_f32 v[34:35], v[34:35], s[70:71]\n" \
"v_cvt_pk_bf16_f32 v32, v32, v33\n" \
"v_accvgpr_read_b32 v128, a156\n" \
"v_accvgpr_read_b32 v129, a157\n" \
"v_accvgpr_read_b32 v130, a158\n" \
"v_accvgpr_read_b32 v131, a159\n" \
"v_pk_mul_f32 v[126:127], v[126:127], s[70:71]\n" \
"v_cvt_pk_bf16_f32 v124, v124, v125\n" \
"v_accvgpr_read_b32 v136, a76\n" \
"v_accvgpr_read_b32 v137, a77\n" \
"v_accvgpr_read_b32 v138, a78\n" \
"v_accvgpr_read_b32 v139, a79\n" \
"v_pk_mul_f32 v[134:135], v[134:135], s[70:71]\n" \
"v_cvt_pk_bf16_f32 v132, v132, v133\n" \
"v_mfma_f32_16x16x32_bf16 a[236:239], v[112:115], v[80:83], a[236:239]\n" \
"s_mov_b32 s72, 0x3eae1a93\n" \
"v_mul_f32_e32 v45, 0xbfb8aa3b, v228\n" \
"v_mfma_f32_16x16x16_bf16 v[48:51], v[182:183], v[46:47], 0\n" \
"v_mfma_f32_16x16x16_bf16 v[196:199], v[190:191], v[46:47], 0\n" \
"v_mfma_f32_16x16x16_bf16 v[212:215], v[206:207], v[46:47], 0\n" \
"v_max_f32_e32 v193, v193, v209\n" \
"v_mul_f32_e32 v120, s70, v120\n" \
"v_mul_f32_e32 v121, s70, v121\n" \
"v_mul_f32_e32 v122, s70, v122\n" \
"v_mul_f32_e64 v123, s70, v123\n" \
"v_cvt_pk_bf16_f32 v117, v118, v119\n" \
"v_pk_mul_f32 v[36:37], v[36:37], s[70:71]\n" \
"v_pk_mul_f32 v[38:39], v[38:39], s[70:71]\n" \
"v_cvt_pk_bf16_f32 v33, v34, v35\n" \
"v_pk_mul_f32 v[128:129], v[128:129], s[70:71]\n" \
"v_pk_mul_f32 v[130:131], v[130:131], s[70:71]\n" \
"v_cvt_pk_bf16_f32 v125, v126, v127\n" \
"v_pk_mul_f32 v[136:137], v[136:137], s[70:71]\n" \
"v_pk_mul_f32 v[138:139], v[138:139], s[70:71]\n" \
"v_cvt_pk_bf16_f32 v133, v134, v135\n" \
"v_mfma_f32_16x16x16_bf16 v[228:231], v[222:223], v[46:47], 0\n" \
"v_mul_f32_e64 v193, s72, v193\n" \
"v_cvt_pk_bf16_f32 v118, v120, v121\n" \
"v_cvt_pk_bf16_f32 v119, v122, v123\n" \
"v_cvt_pk_bf16_f32 v34, v36, v37\n" \
"v_cvt_pk_bf16_f32 v35, v38, v39\n" \
"v_cvt_pk_bf16_f32 v126, v128, v129\n" \
"v_cvt_pk_bf16_f32 v127, v130, v131\n" \
"v_cvt_pk_bf16_f32 v134, v136, v137\n" \
"v_cvt_pk_bf16_f32 v135, v138, v139\n" \
"v_mfma_f32_16x16x16_bf16 v[244:247], v[238:239], v[46:47], 0\n" \
"v_mov_b32_e64 v209, v193\n" \
"v_accvgpr_read_b32 v68, a104\n" \
"ds_write_b128 v27, v[116:119] offset:13312\n" \
"ds_write_b128 v21, v[32:35] offset:16384\n" \
"ds_write_b128 v21, v[124:127] offset:17408\n" \
"ds_write_b128 v21, v[132:135] offset:20480\n" \
"v_permlane16_swap_b32_e32 v193, v209\n" \
"v_mfma_f32_16x16x32_bf16 a[236:239], v[176:179], v[144:147], a[236:239]\n" \
"v_mov_b32_e32 v225, v193\n" \
"v_mov_b32_e64 v241, v209\n" \
"v_accvgpr_read_b32 v69, a105\n" \
"v_accvgpr_read_b32 v116, a136\n" \
"v_accvgpr_read_b32 v117, a137\n" \
"v_accvgpr_read_b32 v32, a168\n" \
"v_accvgpr_read_b32 v33, a169\n" \
"v_permlane32_swap_b32_e32 v193, v225\n" \
"v_permlane32_swap_b32_e32 v209, v241\n" \
"v_accvgpr_read_b32 v70, a106\n" \
"v_accvgpr_read_b32 v71, a107\n" \
"v_pk_mul_f32 v[68:69], v[68:69], s[70:71]\n" \
"v_accvgpr_read_b32 v118, a138\n" \
"v_accvgpr_read_b32 v119, a139\n" \
"v_pk_mul_f32 v[116:117], v[116:117], s[70:71]\n" \
"v_accvgpr_read_b32 v34, a170\n" \
"v_accvgpr_read_b32 v35, a171\n" \
"v_pk_mul_f32 v[32:33], v[32:33], s[70:71]\n" \
"v_fma_f32 v192, v192, s72, -v193\n" \
"v_fma_f32 v194, v194, s72, -v193\n" \
"v_fma_f32 v208, v208, s72, -v209\n" \
"v_fma_f32 v210, v210, s72, -v209\n" \
"v_accvgpr_read_b32 v72, a108\n" \
"v_accvgpr_read_b32 v73, a109\n" \
"v_accvgpr_read_b32 v74, a110\n" \
"v_accvgpr_read_b32 v75, a111\n" \
"v_pk_mul_f32 v[70:71], v[70:71], s[70:71]\n" \
"v_cvt_pk_bf16_f32 v68, v68, v69\n" \
"v_accvgpr_read_b32 v120, a140\n" \
"v_accvgpr_read_b32 v121, a141\n" \
"v_accvgpr_read_b32 v122, a142\n" \
"v_accvgpr_read_b32 v123, a143\n" \
"v_pk_mul_f32 v[118:119], v[118:119], s[70:71]\n" \
"v_cvt_pk_bf16_f32 v116, v116, v117\n" \
"v_accvgpr_read_b32 v36, a172\n" \
"v_accvgpr_read_b32 v37, a173\n" \
"v_accvgpr_read_b32 v38, a174\n" \
"v_accvgpr_read_b32 v39, a175\n" \
"v_pk_mul_f32 v[34:35], v[34:35], s[70:71]\n" \
"v_cvt_pk_bf16_f32 v32, v32, v33\n" \
"v_accvgpr_read_b32 v136, a236\n" \
"v_accvgpr_read_b32 v137, a237\n" \
"v_accvgpr_read_b32 v138, a238\n" \
"v_accvgpr_read_b32 v139, a239\n" \
"v_exp_f32_e32 v192, v192\n" \
"v_exp_f32_e32 v194, v194\n" \
"v_exp_f32_e32 v208, v208\n" \
"v_exp_f32_e32 v210, v210\n" \
"v_fma_f32 v224, v224, s72, -v225\n" \
"v_fma_f32 v226, v226, s72, -v225\n" \
"v_pk_mul_f32 v[72:73], v[72:73], s[70:71]\n" \
"v_pk_mul_f32 v[74:75], v[74:75], s[70:71]\n" \
"v_cvt_pk_bf16_f32 v69, v70, v71\n" \
"v_pk_mul_f32 v[120:121], v[120:121], s[70:71]\n" \
"v_pk_mul_f32 v[122:123], v[122:123], s[70:71]\n" \
"v_cvt_pk_bf16_f32 v117, v118, v119\n" \
"v_pk_mul_f32 v[36:37], v[36:37], s[70:71]\n" \
"v_pk_mul_f32 v[38:39], v[38:39], s[70:71]\n" \
"v_cvt_pk_bf16_f32 v33, v34, v35\n" \
"v_pk_mul_f32 v[136:137], v[136:137], s[70:71]\n" \
"v_pk_mul_f32 v[138:139], v[138:139], s[70:71]\n" \
"v_mfma_f32_16x16x32_bf16 a[232:235], v[108:111], v[80:83], a[232:235]\n" \
"v_exp_f32_e32 v224, v224\n" \
"v_exp_f32_e32 v226, v226\n" \
"v_fma_f32 v240, v240, s72, -v241\n" \
"v_fma_f32 v242, v242, s72, -v241\n" \
"v_cvt_pk_bf16_f32 v70, v72, v73\n" \
"v_cvt_pk_bf16_f32 v71, v74, v75\n" \
"v_cvt_pk_bf16_f32 v118, v120, v121\n" \
"v_cvt_pk_bf16_f32 v119, v122, v123\n" \
"v_cvt_pk_bf16_f32 v34, v36, v37\n" \
"v_cvt_pk_bf16_f32 v35, v38, v39\n" \
"v_cvt_pk_bf16_f32 v192, v192, v194\n" \
"v_cvt_pk_bf16_f32 v208, v208, v210\n" \
"v_exp_f32_e32 v240, v240\n" \
"v_exp_f32_e32 v242, v242\n" \
"ds_write_b128 v21, v[68:71] offset:21504\n" \
"ds_write_b128 v21, v[116:119] offset:24576\n" \
"ds_write_b128 v21, v[32:35] offset:25600\n" \
"v_cvt_pk_bf16_f32 v52, v48, v49\n" \
"v_cvt_pk_bf16_f32 v53, v50, v51\n" \
"v_cvt_pk_bf16_f32 v54, v196, v197\n" \
"v_cvt_pk_bf16_f32 v55, v198, v199\n" \
"v_cvt_pk_bf16_f32 v56, v212, v213\n" \
"v_cvt_pk_bf16_f32 v57, v214, v215\n" \
"v_perm_b32 v184, v192, v192, v40\n" \
"v_perm_b32 v185, v192, v192, v41\n" \
"v_perm_b32 v186, v192, v192, v42\n" \
"v_perm_b32 v187, v192, v192, v43\n" \
"v_perm_b32 v200, v208, v208, v40\n" \
"v_perm_b32 v201, v208, v208, v41\n" \
"v_perm_b32 v202, v208, v208, v42\n" \
"v_perm_b32 v203, v208, v208, v43\n" \
"v_cvt_pk_bf16_f32 v224, v224, v226\n" \
"v_cvt_pk_bf16_f32 v58, v228, v229\n" \
"v_cvt_pk_bf16_f32 v59, v230, v231\n" \
"v_mfma_f32_16x16x32_bf16 v[148:151], v[52:55], v[184:187], 0\n" \
"v_mfma_f32_16x16x32_bf16 v[152:155], v[54:57], v[200:203], 0\n" \
"v_perm_b32 v216, v224, v224, v40\n" \
"v_perm_b32 v217, v224, v224, v41\n" \
"v_perm_b32 v218, v224, v224, v42\n" \
"v_perm_b32 v219, v224, v224, v43\n" \
"v_cvt_pk_bf16_f32 v240, v240, v242\n" \
"v_mfma_f32_16x16x32_bf16 a[232:235], v[172:175], v[144:147], a[232:235]\n" \
"v_cvt_pk_bf16_f32 v60, v244, v245\n" \
"v_cvt_pk_bf16_f32 v61, v246, v247\n" \
"v_mfma_f32_16x16x32_bf16 v[156:159], v[56:59], v[216:219], 0\n" \
"v_perm_b32 v232, v240, v240, v40\n" \
"v_perm_b32 v233, v240, v240, v41\n" \
"v_perm_b32 v234, v240, v240, v42\n" \
"v_perm_b32 v235, v240, v240, v43\n" \
"v_mfma_f32_16x16x32_bf16 a[204:207], v[104:107], v[80:83], a[204:207]\n" \
"v_accvgpr_read_b32 v132, a232\n" \
"v_accvgpr_read_b32 v133, a233\n" \
"v_accvgpr_read_b32 v134, a234\n" \
"v_accvgpr_read_b32 v135, a235\n" \
"v_pk_mul_f32 v[132:133], v[132:133], s[70:71]\n" \
"v_pk_mul_f32 v[134:135], v[134:135], s[70:71]\n" \
"v_cvt_pk_bf16_f32 v132, v132, v133\n" \
"v_cvt_pk_bf16_f32 v133, v134, v135\n" \
"v_cvt_pk_bf16_f32 v134, v136, v137\n" \
"v_cvt_pk_bf16_f32 v135, v138, v139\n" \
"ds_write_b128 v21, v[132:135] offset:29696\n" \
"v_mfma_f32_16x16x32_bf16 v[160:163], v[58:61], v[232:235], 0\n" \
"v_cvt_pk_bf16_f32 v84, v252, v253\n" \
"v_cvt_pk_bf16_f32 v85, v254, v255\n" \
"v_mfma_f32_16x16x32_bf16 a[204:207], v[168:171], v[144:147], a[204:207]\n" \
"v_exp_f32_e64 v45, v45\n" \
"v_mfma_f32_16x16x32_bf16 v[192:195], v[84:87], v[184:187], 0\n" \
"v_mfma_f32_16x16x32_bf16 v[208:211], v[86:89], v[200:203], 0\n" \
"v_mfma_f32_16x16x32_bf16 v[224:227], v[88:91], v[216:219], 0\n" \
"v_mfma_f32_16x16x32_bf16 v[240:243], v[90:93], v[232:235], 0\n" \
"v_mov_b32_dpp v150, v154 quad_perm:[0,1,2,3] row_mask:0x2 bank_mask:0xf\n" \
"v_add_f32_e64 v45, 1.0, v45\n" \
"v_mfma_f32_16x16x32_bf16 a[200:203], v[100:103], v[80:83], a[200:203]\n" \
"v_accvgpr_read_b32 v128, a204\n" \
"v_accvgpr_read_b32 v129, a205\n" \
"v_accvgpr_read_b32 v130, a206\n" \
"v_accvgpr_read_b32 v131, a207\n" \
"v_pk_mul_f32 v[128:129], v[128:129], s[70:71]\n" \
"v_pk_mul_f32 v[130:131], v[130:131], s[70:71]\n" \
"v_mov_b32_dpp v150, v158 quad_perm:[0,1,2,3] row_mask:0x4 bank_mask:0xf\n" \
"v_rcp_f32_e64 v45, v45\n" \
"v_mov_b32_dpp v148, v152 quad_perm:[0,1,2,3] row_mask:0x2 bank_mask:0xf\n" \
"v_mov_b32_dpp v150, v162 quad_perm:[0,1,2,3] row_mask:0x8 bank_mask:0xf\n" \
"v_mov_b32_dpp v149, v153 quad_perm:[0,1,2,3] row_mask:0x2 bank_mask:0xf\n" \
"v_rcp_f32_e64 v150, v150\n" \
"v_mov_b32_dpp v148, v156 quad_perm:[0,1,2,3] row_mask:0x4 bank_mask:0xf\n" \
"v_mov_b32_dpp v149, v157 quad_perm:[0,1,2,3] row_mask:0x4 bank_mask:0xf\n" \
"v_mul_f32_e64 v150, v45, v150\n" \
"v_mov_b32_dpp v148, v160 quad_perm:[0,1,2,3] row_mask:0x8 bank_mask:0xf\n" \
"v_mov_b32_dpp v149, v161 quad_perm:[0,1,2,3] row_mask:0x8 bank_mask:0xf\n" \
"v_mul_f32_e32 v148, v150, v148\n" \
"v_mul_f32_e32 v149, v150, v149\n" \
"v_mov_b32_e64 v154, v150\n" \
"v_mfma_f32_16x16x32_bf16 a[200:203], v[164:167], v[144:147], a[200:203]\n" \
"v_sub_f32_e32 v44, 1.0, v45\n" \
"v_permlane16_swap_b32_e32 v150, v154\n" \
"v_mov_b32_e32 v158, v150\n" \
"v_mov_b32_e32 v162, v154\n" \
"v_cvt_pk_bf16_f32 v148, v148, v149\n" \
"v_permlane32_swap_b32_e32 v150, v158\n" \
"v_permlane32_swap_b32_e32 v154, v162\n" \
"v_mul_f32_e64 v192, v150, v192\n" \
"v_accvgpr_read_b32 v124, a200\n" \
"v_accvgpr_read_b32 v125, a201\n" \
"v_accvgpr_read_b32 v126, a202\n" \
"v_accvgpr_read_b32 v127, a203\n" \
"v_pk_mul_f32 v[124:125], v[124:125], s[70:71]\n" \
"v_pk_mul_f32 v[126:127], v[126:127], s[70:71]\n" \
"v_cvt_pk_bf16_f32 v124, v124, v125\n" \
"v_cvt_pk_bf16_f32 v125, v126, v127\n" \
"v_cvt_pk_bf16_f32 v126, v128, v129\n" \
"v_cvt_pk_bf16_f32 v127, v130, v131\n" \
"ds_write_b128 v21, v[124:127] offset:28672\n" \
"v_mul_f32_e32 v193, v150, v193\n" \
"v_mul_f32_e32 v208, v154, v208\n" \
"v_mul_f32_e32 v209, v154, v209\n" \
"v_mul_f32_e32 v224, v158, v224\n" \
"v_mul_f32_e32 v225, v158, v225\n" \
"v_mul_f32_e32 v240, v162, v240\n" \
"v_mul_f32_e32 v241, v162, v241\n" \
"v_mul_f32_e32 v194, v150, v194\n" \
"v_mul_f32_e64 v195, v150, v195\n" \
"v_cvt_pk_bf16_f32 v192, v192, v193\n" \
"v_mul_f32_e32 v210, v154, v210\n" \
"v_mul_f32_e32 v211, v154, v211\n" \
"v_cvt_pk_bf16_f32 v208, v208, v209\n" \
"v_mul_f32_e32 v226, v158, v226\n" \
"v_mul_f32_e32 v227, v158, v227\n" \
"v_cvt_pk_bf16_f32 v224, v224, v225\n" \
"v_mul_f32_e32 v242, v162, v242\n" \
"v_mul_f32_e32 v243, v162, v243\n" \
"v_cvt_pk_bf16_f32 v240, v240, v241\n" \
"v_cvt_pk_bf16_f32 v193, v194, v195\n" \
"s_add_u32 s84, s48, 0x0\n" \
"s_nop 0\n" \
"v_cvt_pk_bf16_f32 v209, v210, v211\n" \
"s_add_u32 s85, s48, 0x24800\n" \
"v_cvt_pk_bf16_f32 v225, v226, v227\n" \
"s_add_u32 s86, s48, 0x49000\n" \
"v_cvt_pk_bf16_f32 v241, v242, v243\n" \
"s_add_u32 s87, s48, 0x6d800\n" \
"ds_write_b32 v26, v44\n" \
"buffer_store_dword v148, v25, s[80:83], s49 offen sc1\n" \
"buffer_store_dwordx2 v[192:193], v24, s[80:83], s84 offen sc1\n" \
"buffer_store_dwordx2 v[208:209], v24, s[80:83], s85 offen sc1\n" \
"buffer_store_dwordx2 v[224:225], v24, s[80:83], s86 offen sc1\n" \
"buffer_store_dwordx2 v[240:241], v24, s[80:83], s87 offen sc1\n" \
"s_branch L_end_%=\n" \
"L_poll60_%=:\n" \
"s_mov_b32 s84, 0\n" \
"L_pl60_%=:\n" \
"s_sleep 1\n" \
"v_bfe_u32 v44, v20, 2, 5\n" \
"buffer_load_dword v44, v44, s[80:83], s38 offen offset:128 sc1\n" \
"s_waitcnt vmcnt(0)\n" \
"v_cmp_ne_u32_e32 vcc, s62, v44\n" \
"s_add_u32 s84, s84, 1\n" \
"s_nop 3\n" \
"s_cbranch_vccz L_pollr60_%=\n" \
"s_cmp_gt_u32 s84, 0x400000\n" \
"s_cbranch_scc0 L_pl60_%=\n" \
"s_branch L_pollr60_%=\n" \
"L_poll160_%=:\n" \
"s_mov_b32 s84, 0\n" \
"L_pl160_%=:\n" \
"s_sleep 1\n" \
"v_bfe_u32 v44, v20, 2, 5\n" \
"buffer_load_dword v44, v44, s[80:83], s38 offen offset:0 sc1\n" \
"s_waitcnt vmcnt(0)\n" \
"v_cmp_ne_u32_e32 vcc, s62, v44\n" \
"s_add_u32 s84, s84, 1\n" \
"s_nop 3\n" \
"s_cbranch_vccz L_pollr160_%=\n" \
"s_cmp_gt_u32 s84, 0x400000\n" \
"s_cbranch_scc0 L_pl160_%=\n" \
"s_branch L_pollr160_%=\n" \
".fill (L_pb + 46868 - .) / 4, 4, 0xBF800000\n" \
"L_w1_%=:\n" \
"s_waitcnt vmcnt(18)\n" \
"s_barrier\n" \
"s_nop 0\n" \
"ds_read_b64_tr_b16 v[84:85], v32 offset:0\n" \
"ds_read_b64_tr_b16 v[86:87], v32 offset:1024\n" \
"ds_read_b64_tr_b16 v[148:149], v32 offset:8192\n" \
"ds_read_b64_tr_b16 v[150:151], v32 offset:9216\n" \
"ds_read_b128 v[52:55], v28 offset:16384\n" \
"ds_read_b128 v[116:119], v29 offset:16384\n" \
"ds_read_b64_tr_b16 v[88:89], v33 offset:0\n" \
"ds_read_b64_tr_b16 v[90:91], v33 offset:1024\n" \
"ds_read_b64_tr_b16 v[152:153], v33 offset:8192\n" \
"ds_read_b64_tr_b16 v[154:155], v33 offset:9216\n" \
"ds_read_b64_tr_b16 v[92:93], v34 offset:0\n" \
"ds_read_b64_tr_b16 v[94:95], v34 offset:1024\n" \
"ds_read_b64_tr_b16 v[156:157], v34 offset:8192\n" \
"ds_read_b64_tr_b16 v[158:159], v34 offset:9216\n" \
"ds_read_b64_tr_b16 v[96:97], v35 offset:0\n" \
"ds_read_b64_tr_b16 v[98:99], v35 offset:1024\n" \
"ds_read_b64_tr_b16 v[160:161], v35 offset:8192\n" \
"ds_read_b64_tr_b16 v[162:163], v35 offset:9216\n" \
"ds_read_b128 v[56:59], v28 offset:18432\n" \
"ds_read_b128 v[120:123], v29 offset:18432\n" \
"ds_read_b128 v[60:63], v28 offset:20480\n" \
"ds_read_b128 v[124:127], v29 offset:20480\n" \
"ds_read_b128 v[64:67], v28 offset:22528\n" \
"ds_read_b128 v[128:131], v29 offset:22528\n" \
"ds_read_b128 v[68:71], v28 offset:0\n" \
"ds_read_b128 v[132:135], v29 offset:0\n" \
"ds_read_b128 v[72:75], v28 offset:2048\n" \
"ds_read_b128 v[136:139], v29 offset:2048\n" \
"ds_read_b128 v[76:79], v28 offset:4096\n" \
"ds_read_b128 v[140:143], v29 offset:4096\n" \
"ds_read_b128 v[80:83], v28 offset:6144\n" \
"ds_read_b128 v[144:147], v29 offset:6144\n" \
"s_waitcnt lgkmcnt(15)\n" \
"v_mfma_f32_16x16x32_bf16 a[0:3], v[52:55], v[84:87], 0\n" \
"ds_read_b64_tr_b16 v[100:101], v36 offset:0\n" \
"v_mad_i32_i24 v217, v216, -6, v215\n" \
"v_mfma_f32_16x16x32_bf16 a[0:3], v[116:119], v[148:151], a[0:3]\n" \
"v_add_u32_e32 v218, 0xfffffe50, v215\n" \
"s_mul_i32 s35, s6, 0x48\n" \
"v_mfma_f32_16x16x32_bf16 a[4:7], v[52:55], v[88:91], 0\n" \
"v_lshrrev_b32_e32 v219, 1, v217\n" \
"v_add_u32_e64 v218, s35, v216\n" \
"v_mfma_f32_16x16x32_bf16 a[4:7], v[116:119], v[152:155], a[4:7]\n" \
"ds_read_b64_tr_b16 v[102:103], v36 offset:1024\n" \
"v_mul_u32_u24_e32 v219, 0x480, v219\n" \
"v_mfma_f32_16x16x32_bf16 a[8:11], v[52:55], v[92:95], 0\n" \
"v_and_b32_e64 v214, 16, v20\n" \
"v_mad_u32_u24 v219, v218, s5, v219\n" \
"v_mfma_f32_16x16x32_bf16 a[8:11], v[116:119], v[156:159], a[8:11]\n" \
"v_or_b32_e32 v192, v219, v214\n" \
"v_mov_b32_e32 v220, 2.0\n" \
"v_mfma_f32_16x16x32_bf16 a[12:15], v[52:55], v[96:99], 0\n" \
"ds_read_b64_tr_b16 v[164:165], v36 offset:8192\n" \
"v_mov_b32_e32 v222, 7\n" \
"s_waitcnt lgkmcnt(15)\n" \
"v_mfma_f32_16x16x32_bf16 a[12:15], v[116:119], v[160:163], a[12:15]\n" \
"v_lshlrev_b32_e32 v220, 7, v218\n" \
"v_lshrrev_b32_e32 v218, 1, v218\n" \
"v_mfma_f32_16x16x32_bf16 a[44:47], v[56:59], v[96:99], 0\n" \
"v_and_b32_e32 v218, 4, v218\n" \
"v_and_b32_e32 v216, 2, v216\n" \
"v_mfma_f32_16x16x32_bf16 a[44:47], v[120:123], v[160:163], a[44:47]\n" \
"ds_read_b64_tr_b16 v[166:167], v36 offset:9216\n" \
"s_mul_i32 s7, s6, 0x2400\n" \
"v_mfma_f32_16x16x32_bf16 a[40:43], v[56:59], v[92:95], 0\n" \
"s_sub_i32 s34, 0x22000, s7\n" \
"v_bitop3_b32 v216, v217, v218, v216 bitop3:0x1e\n" \
"v_mfma_f32_16x16x32_bf16 a[40:43], v[120:123], v[156:159], a[40:43]\n" \
"v_or_b32_e32 v213, 0x24400, v20\n" \
"v_lshlrev_b32_e64 v216, 4, v216\n" \
"v_mfma_f32_16x16x32_bf16 a[36:39], v[56:59], v[88:91], 0\n" \
"v_add3_u32 v196, v220, s34, v216\n" \
"v_mov_b32_e64 v216, v213\n" \
"v_mfma_f32_16x16x32_bf16 a[36:39], v[120:123], v[152:155], a[36:39]\n" \
"ds_read_b64_tr_b16 v[104:105], v37 offset:0\n" \
"s_movk_i32 s36, 0x1b0\n" \
"v_mfma_f32_16x16x32_bf16 a[32:35], v[56:59], v[84:87], 0\n" \
"v_or_b32_e32 v216, 0x100, v215\n" \
"v_mul_u32_u24_e32 v217, 0x2aab, v216\n" \
"v_mfma_f32_16x16x32_bf16 a[32:35], v[120:123], v[148:151], a[32:35]\n" \
"v_add_u32_e32 v215, 0xffffff50, v215\n" \
"v_lshrrev_b32_e32 v217, 16, v217\n" \
"s_waitcnt lgkmcnt(15)\n" \
"v_mfma_f32_16x16x32_bf16 a[64:67], v[60:63], v[84:87], 0\n" \
"s_mov_b32 m0, s32\n" \
"ds_read_b64_tr_b16 v[106:107], v37 offset:1024\n" \
"v_mfma_f32_16x16x32_bf16 a[64:67], v[124:127], v[148:151], a[64:67]\n" \
"s_mov_b32 exec_lo, 0xff000000\n" \
"s_mov_b32 exec_hi, 0x0\n" \
"global_load_lds_dword v[184:185], off\n" \
"s_mov_b64 exec, -1\n" \
"v_mfma_f32_16x16x32_bf16 a[68:71], v[60:63], v[88:91], 0\n" \
"v_mov_b32_e64 v220, v215\n" \
"v_mad_i32_i24 v218, v217, -6, v216\n" \
"v_mfma_f32_16x16x32_bf16 a[68:71], v[124:127], v[152:155], a[68:71]\n" \
"ds_read_b64_tr_b16 v[168:169], v37 offset:8192\n" \
"v_cmp_gt_u32_e64 vcc, s36, v216\n" \
"v_mfma_f32_16x16x32_bf16 a[72:75], v[60:63], v[92:95], 0\n" \
"v_mov_b32_e32 v221, 7\n" \
"v_lshrrev_b32_e32 v222, 1, v218\n" \
"v_mfma_f32_16x16x32_bf16 a[72:75], v[124:127], v[156:159], a[72:75]\n" \
"s_mov_b32 m0, s32\n" \
"v_cndmask_b32_e32 v217, v220, v217, vcc\n" \
"v_mfma_f32_16x16x32_bf16 a[76:79], v[60:63], v[96:99], 0\n" \
"s_mov_b32 exec_lo, 0xffff\n" \
"s_mov_b32 exec_hi, 0x0\n" \
"global_load_lds_dword v[186:187], off\n" \
"s_mov_b64 exec, -1\n" \
"v_mfma_f32_16x16x32_bf16 a[76:79], v[124:127], v[160:163], a[76:79]\n" \
"ds_read_b64_tr_b16 v[170:171], v37 offset:9216\n" \
"s_movk_i32 s4, 0x48\n" \
"s_waitcnt lgkmcnt(15)\n" \
"v_mfma_f32_16x16x32_bf16 a[108:111], v[64:67], v[96:99], 0\n" \
"v_add_u32_e64 v220, s35, v217\n" \
"v_mul_u32_u24_e32 v222, 0x480, v222\n" \
"v_mfma_f32_16x16x32_bf16 a[108:111], v[128:131], v[160:163], a[108:111]\n" \
"v_mad_u32_u24 v222, v220, s5, v222\n" \
"v_cmp_gt_u32_e64 s[4:5], s4, v215\n" \
"v_mfma_f32_16x16x32_bf16 a[104:107], v[64:67], v[92:95], 0\n" \
"ds_read_b64_tr_b16 v[108:109], v38 offset:0\n" \
"v_cndmask_b32_e64 v215, v221, v218, vcc\n" \
"v_mfma_f32_16x16x32_bf16 a[104:107], v[128:131], v[156:159], a[104:107]\n" \
"v_lshrrev_b32_e32 v218, 1, v220\n" \
"v_or_b32_e32 v214, v222, v214\n" \
"v_mfma_f32_16x16x32_bf16 a[100:103], v[64:67], v[88:91], 0\n" \
"v_mov_b32_e32 v222, 2.0\n" \
"v_and_b32_e32 v218, 4, v218\n" \
"v_mfma_f32_16x16x32_bf16 a[100:103], v[128:131], v[152:155], a[100:103]\n" \
"ds_read_b64_tr_b16 v[110:111], v38 offset:1024\n" \
"v_and_b32_e64 v217, 2, v217\n" \
"v_mfma_f32_16x16x32_bf16 a[96:99], v[64:67], v[84:87], 0\n" \
"v_bitop3_b32 v215, v215, v218, v217 bitop3:0x1e\n" \
"v_lshlrev_b32_e64 v223, 1, v220\n" \
"v_mfma_f32_16x16x32_bf16 a[96:99], v[128:131], v[148:151], a[96:99]\n" \
"v_mov_b32_e32 v224, 2.0\n" \
"v_cndmask_b32_e32 v193, v222, v214, vcc\n" \
"v_mfma_f32_16x16x32_bf16 a[128:131], v[68:71], v[84:87], 0\n" \
"ds_read_b64_tr_b16 v[172:173], v38 offset:8192\n" \
"v_lshlrev_b32_e32 v214, 7, v220\n" \
"s_waitcnt lgkmcnt(15)\n" \
"v_mfma_f32_16x16x32_bf16 a[128:131], v[132:135], v[148:151], a[128:131]\n" \
"v_lshlrev_b32_e32 v215, 4, v215\n" \
"s_mul_i32 s39, s3, 18\n" \
"v_mfma_f32_16x16x32_bf16 a[132:135], v[68:71], v[88:91], 0\n" \
"v_add3_u32 v214, v214, s34, v215\n" \
"v_cndmask_b32_e64 v195, v224, v223, s[4:5]\n" \
"v_mfma_f32_16x16x32_bf16 a[132:135], v[132:135], v[152:155], a[132:135]\n" \
"ds_read_b64_tr_b16 v[174:175], v38 offset:9216\n" \
"s_movk_i32 s4, 0x1f8\n" \
"v_mfma_f32_16x16x32_bf16 a[136:139], v[68:71], v[92:95], 0\n" \
"v_mov_b32_e32 v215, v213\n" \
"s_add_i32 s35, s35, s39\n" \
"v_mfma_f32_16x16x32_bf16 a[136:139], v[132:135], v[156:159], a[136:139]\n" \
"v_cmp_gt_u32_e32 vcc, s4, v216\n" \
"v_add_u32_e64 v212, s35, v212\n" \
"v_mfma_f32_16x16x32_bf16 a[140:143], v[68:71], v[96:99], 0\n" \
"v_mov_b32_e32 v219, 2.0\n" \
"v_cndmask_b32_e32 v197, v215, v214, vcc\n" \
"v_mfma_f32_16x16x32_bf16 a[140:143], v[132:135], v[160:163], a[140:143]\n" \
"ds_read_b64_tr_b16 v[112:113], v39 offset:0\n" \
"s_movk_i32 s35, 0x120\n" \
"v_mfma_f32_16x16x32_bf16 a[172:175], v[72:75], v[96:99], 0\n" \
"v_mul_u32_u24_e32 v214, 0xd80, v212\n" \
"v_cmp_gt_u32_e32 vcc, s35, v20\n" \
"v_mfma_f32_16x16x32_bf16 a[172:175], v[136:139], v[160:163], a[172:175]\n" \
"v_lshrrev_b32_e32 v215, 1, v212\n" \
"s_and_b32 s4, s2, 7\n" \
"v_mfma_f32_16x16x32_bf16 a[168:171], v[72:75], v[92:95], 0\n" \
"ds_read_b64_tr_b16 v[114:115], v39 offset:1024\n" \
"v_cndmask_b32_e64 v194, v219, v214, vcc\n" \
"v_mfma_f32_16x16x32_bf16 a[168:171], v[136:139], v[156:159], a[168:171]\n" \
"v_lshlrev_b32_e32 v214, 7, v212\n" \
"v_lshrrev_b32_e32 v212, 2, v212\n" \
"v_mfma_f32_16x16x32_bf16 a[164:167], v[72:75], v[88:91], 0\n" \
"v_and_b32_e64 v212, 2, v212\n" \
"v_and_or_b32 v212, v215, 1, v212\n" \
"v_mfma_f32_16x16x32_bf16 a[164:167], v[136:139], v[152:155], a[164:167]\n" \
"ds_read_b64_tr_b16 v[176:177], v39 offset:8192\n" \
"s_lshl_b32 s5, s4, 2\n" \
"v_mfma_f32_16x16x32_bf16 a[160:163], v[72:75], v[84:87], 0\n" \
"s_bfe_u32 s38, s2, 0x20003\n" \
"v_lshlrev_b32_e32 v212, 5, v212\n" \
"v_mfma_f32_16x16x32_bf16 a[160:163], v[136:139], v[148:151], a[160:163]\n" \
"s_movk_i32 s35, 0x60\n" \
"s_or_b32 s5, s5, s38\n" \
"s_waitcnt lgkmcnt(15)\n" \
"v_mfma_f32_16x16x32_bf16 a[192:195], v[76:79], v[84:87], 0\n" \
"ds_read_b64_tr_b16 v[178:179], v39 offset:9216\n" \
"v_bitop3_b32 v212, v212, s35, v214 bitop3:0x36\n" \
"v_mfma_f32_16x16x32_bf16 a[192:195], v[140:143], v[148:151], a[192:195]\n" \
"s_lshr_b32 s36, s2, 3\n" \
"v_add_u32_e64 v212, s34, v212\n" \
"v_mfma_f32_16x16x32_bf16 a[196:199], v[76:79], v[88:91], 0\n" \
"s_mul_i32 s34, s5, 0x12000\n" \
"s_or_b32 s40, s34, 0x6000000\n" \
"v_mfma_f32_16x16x32_bf16 a[196:199], v[140:143], v[152:155], a[196:199]\n" \
"s_mul_i32 s34, s36, 0x90000\n" \
"s_lshl_b32 s36, s3, 14\n" \
"v_mfma_f32_16x16x32_bf16 a[200:203], v[76:79], v[92:95], 0\n" \
"s_add_i32 s34, s34, s36\n" \
"s_lshl_b32 s38, s5, 8\n" \
"v_mfma_f32_16x16x32_bf16 a[200:203], v[140:143], v[156:159], a[200:203]\n" \
"s_add_i32 s41, s34, 0x4240000\n" \
"s_mul_i32 s34, s5, 36\n" \
"v_mfma_f32_16x16x32_bf16 a[204:207], v[76:79], v[96:99], 0\n" \
"s_add_i32 s5, s7, s40\n" \
"s_addk_i32 s5, 0x2000\n" \
"v_mfma_f32_16x16x32_bf16 a[204:207], v[140:143], v[160:163], a[204:207]\n" \
"s_mov_b32 s37, 2.0\n" \
"s_or_b32 s38, s38, 0x5be8000\n" \
"v_mfma_f32_16x16x32_bf16 a[236:239], v[80:83], v[96:99], 0\n" \
"s_cmp_lg_u32 s3, 0\n" \
"s_cselect_b32 s36, s37, s5\n" \
"v_mfma_f32_16x16x32_bf16 a[236:239], v[144:147], v[160:163], a[236:239]\n" \
"s_lshl_b32 s5, s6, 2\n" \
"s_or_b32 s5, s38, s5\n" \
"v_mfma_f32_16x16x32_bf16 a[232:235], v[80:83], v[92:95], 0\n" \
"s_lshl_b32 s35, s3, 10\n" \
"v_mfma_f32_16x16x32_bf16 a[232:235], v[144:147], v[156:159], a[232:235]\n" \
"s_cmpk_lt_u32 s3, 3\n" \
"s_cselect_b32 s37, s37, s5\n" \
"v_mfma_f32_16x16x32_bf16 a[228:231], v[80:83], v[88:91], 0\n" \
"s_lshl_b32 s5, s4, 16\n" \
"s_lshl_b32 s4, s4, 11\n" \
"v_mfma_f32_16x16x32_bf16 a[228:231], v[144:147], v[152:155], a[228:231]\n" \
"v_cndmask_b32_e32 v198, v213, v212, vcc\n" \
"v_or_b32_e32 v212, s35, v20\n" \
"v_mfma_f32_16x16x32_bf16 a[224:227], v[80:83], v[84:87], 0\n" \
"s_or_b32 s39, s40, s35\n" \
"s_or_b32 s55, s41, s4\n" \
"v_mfma_f32_16x16x32_bf16 a[224:227], v[144:147], v[148:151], a[224:227]\n" \
"s_waitcnt vmcnt(4) lgkmcnt(0)\n" \
"s_barrier\n" \
"v_mfma_f32_16x16x32_bf16 a[16:19], v[52:55], v[100:103], 0\n" \
"s_add_u32 m0, s30, 0x0\n" \
"s_nop 0\n" \
"s_mov_b32 s85, 0x100\n" \
"v_mfma_f32_16x16x32_bf16 a[16:19], v[116:119], v[164:167], a[16:19]\n" \
"buffer_load_dwordx4 v22, s[64:67], s85 offen lds\n" \
"v_mfma_f32_16x16x32_bf16 a[20:23], v[52:55], v[104:107], 0\n" \
"ds_read_b64_tr_b16 v[84:85], v32 offset:32768\n" \
"v_or_b32_e32 v199, 0x22000, v212\n" \
"v_mfma_f32_16x16x32_bf16 a[20:23], v[116:119], v[168:171], a[20:23]\n" \
"s_add_u32 m0, s30, 0x400\n" \
"s_mov_b32 s84, 0x2100\n" \
"v_mfma_f32_16x16x32_bf16 a[24:27], v[52:55], v[108:111], 0\n" \
"buffer_load_dwordx4 v23, s[64:67], s84 offen lds\n" \
"v_mfma_f32_16x16x32_bf16 a[24:27], v[116:119], v[172:175], a[24:27]\n" \
"ds_read_b64_tr_b16 v[86:87], v32 offset:33792\n" \
"s_add_i32 s35, s39, s7\n" \
"v_mfma_f32_16x16x32_bf16 a[28:31], v[52:55], v[112:115], 0\n" \
"ds_read_b64_tr_b16 v[148:149], v32 offset:40960\n" \
"s_add_i32 s54, s41, s5\n" \
"v_mfma_f32_16x16x32_bf16 a[28:31], v[116:119], v[176:179], a[28:31]\n" \
"ds_read_b128 v[52:55], v28 offset:49152\n" \
"s_add_i32 s55, s55, 0x80000\n" \
"v_mfma_f32_16x16x32_bf16 a[60:63], v[56:59], v[112:115], 0\n" \
"ds_read_b128 v[116:119], v29 offset:49152\n" \
"v_mov_b32_e32 v215, 0xffff\n" \
"v_mfma_f32_16x16x32_bf16 a[60:63], v[120:123], v[176:179], a[60:63]\n" \
"s_add_u32 m0, s31, 0x0\n" \
"s_nop 0\n" \
"s_mov_b32 s85, 0x100000\n" \
"v_mfma_f32_16x16x32_bf16 a[56:59], v[56:59], v[108:111], 0\n" \
"buffer_load_dwordx4 v26, s[68:71], s85 offen lds\n" \
"v_mfma_f32_16x16x32_bf16 a[56:59], v[120:123], v[172:175], a[56:59]\n" \
"s_add_u32 m0, s31, 0x4000\n" \
"s_mov_b32 s84, 0x100000\n" \
"v_mfma_f32_16x16x32_bf16 a[52:55], v[56:59], v[104:107], 0\n" \
"buffer_load_dwordx4 v26, s[72:75], s84 offen lds\n" \
"v_mfma_f32_16x16x32_bf16 a[52:55], v[120:123], v[168:171], a[52:55]\n" \
"s_add_u32 m0, s30, 0x800\n" \
"s_mov_b32 s85, 0x4100\n" \
"v_mfma_f32_16x16x32_bf16 a[48:51], v[56:59], v[100:103], 0\n" \
"buffer_load_dwordx4 v22, s[64:67], s85 offen lds\n" \
"v_mfma_f32_16x16x32_bf16 a[48:51], v[120:123], v[164:167], a[48:51]\n" \
"ds_read_b128 v[56:59], v28 offset:51200\n" \
"s_and_b32 s4, s3, 1\n" \
"v_mfma_f32_16x16x32_bf16 a[80:83], v[60:63], v[100:103], 0\n" \
"ds_read_b128 v[120:123], v29 offset:51200\n" \
"s_lshl_b32 s5, s4, 4\n" \
"v_mfma_f32_16x16x32_bf16 a[80:83], v[124:127], v[164:167], a[80:83]\n" \
"s_add_u32 m0, s30, 0xc00\n" \
"s_mov_b32 s84, 0x6100\n" \
"v_mfma_f32_16x16x32_bf16 a[84:87], v[60:63], v[104:107], 0\n" \
"buffer_load_dwordx4 v23, s[64:67], s84 offen lds\n" \
"v_mfma_f32_16x16x32_bf16 a[84:87], v[124:127], v[168:171], a[84:87]\n" \
"ds_read_b64_tr_b16 v[150:151], v32 offset:41984\n" \
"v_lshrrev_b32_e32 v212, 4, v20\n" \
".fill (L_pb + 48864 - .) / 4, 4, 0xBF800000\n" \
"L_pp12:\n" \
"s_cbranch_execz L_pret\n" \
"v_mfma_f32_16x16x32_bf16 a[88:91], v[60:63], v[108:111], 0\n" \
"s_add_u32 m0, s31, 0x400\n" \
"s_mov_b32 s85, 0x108000\n" \
"v_mfma_f32_16x16x32_bf16 a[88:91], v[124:127], v[172:175], a[88:91]\n" \
"buffer_load_dwordx4 v26, s[68:71], s85 offen lds\n" \
"v_mfma_f32_16x16x32_bf16 a[92:95], v[60:63], v[112:115], 0\n" \
"ds_read_b64_tr_b16 v[88:89], v33 offset:32768\n" \
"v_lshrrev_b32_e32 v218, 6, v20\n" \
"v_mfma_f32_16x16x32_bf16 a[92:95], v[124:127], v[176:179], a[92:95]\n" \
"ds_read_b128 v[60:63], v28 offset:53248\n" \
"v_and_b32_e64 v219, 12, v218\n" \
"v_mfma_f32_16x16x32_bf16 a[124:127], v[64:67], v[112:115], 0\n" \
"ds_read_b128 v[124:127], v29 offset:53248\n" \
"v_and_or_b32 v214, v212, 15, s5\n" \
"v_mfma_f32_16x16x32_bf16 a[124:127], v[128:131], v[176:179], a[124:127]\n" \
"s_add_u32 m0, s31, 0x4400\n" \
"s_mov_b32 s84, 0x108000\n" \
"v_mfma_f32_16x16x32_bf16 a[120:123], v[64:67], v[108:111], 0\n" \
"buffer_load_dwordx4 v26, s[72:75], s84 offen lds\n" \
"v_mfma_f32_16x16x32_bf16 a[120:123], v[128:131], v[172:175], a[120:123]\n" \
"s_add_u32 m0, s30, 0x1000\n" \
"s_mov_b32 s85, 0x8100\n" \
"v_mfma_f32_16x16x32_bf16 a[116:119], v[64:67], v[104:107], 0\n" \
"buffer_load_dwordx4 v22, s[64:67], s85 offen lds\n" \
"v_mfma_f32_16x16x32_bf16 a[116:119], v[128:131], v[168:171], a[116:119]\n" \
"ds_read_b64_tr_b16 v[90:91], v33 offset:33792\n" \
"v_cmp_gt_u32_e64 vcc, v219, v214\n" \
"v_mfma_f32_16x16x32_bf16 a[112:115], v[64:67], v[100:103], 0\n" \
"ds_read_b64_tr_b16 v[152:153], v33 offset:40960\n" \
"v_mov_b32_e32 v220, 0xffff0000\n" \
"v_mfma_f32_16x16x32_bf16 a[112:115], v[128:131], v[164:167], a[112:115]\n" \
"ds_read_b128 v[64:67], v28 offset:55296\n" \
"s_lshl_b32 s5, s2, 2\n" \
"s_nop 0\n" \
"v_mfma_f32_16x16x32_bf16 a[144:147], v[68:71], v[100:103], 0\n" \
"ds_read_b128 v[128:131], v29 offset:55296\n" \
"v_cndmask_b32_e64 v217, v215, 0, vcc\n" \
"v_mfma_f32_16x16x32_bf16 a[144:147], v[132:135], v[164:167], a[144:147]\n" \
"s_add_u32 m0, s30, 0x1400\n" \
"s_mov_b32 s84, 0xa100\n" \
"v_mfma_f32_16x16x32_bf16 a[148:151], v[68:71], v[104:107], 0\n" \
"buffer_load_dwordx4 v23, s[64:67], s84 offen lds\n" \
"v_mfma_f32_16x16x32_bf16 a[148:151], v[132:135], v[168:171], a[148:151]\n" \
"s_add_u32 m0, s31, 0x800\n" \
"s_mov_b32 s85, 0x110000\n" \
"v_mfma_f32_16x16x32_bf16 a[152:155], v[68:71], v[108:111], 0\n" \
"buffer_load_dwordx4 v27, s[68:71], s85 offen lds\n" \
"v_mfma_f32_16x16x32_bf16 a[152:155], v[132:135], v[172:175], a[152:155]\n" \
"s_add_u32 m0, s31, 0x4800\n" \
"s_mov_b32 s84, 0x110000\n" \
"v_mfma_f32_16x16x32_bf16 a[156:159], v[68:71], v[112:115], 0\n" \
"buffer_load_dwordx4 v27, s[72:75], s84 offen lds\n" \
"v_mfma_f32_16x16x32_bf16 a[156:159], v[132:135], v[176:179], a[156:159]\n" \
"ds_read_b128 v[68:71], v28 offset:32768\n" \
"v_cmp_lt_u32_e64 vcc, v219, v214\n" \
"v_mfma_f32_16x16x32_bf16 a[188:191], v[72:75], v[112:115], 0\n" \
"ds_read_b128 v[132:135], v29 offset:32768\n" \
"v_mov_b32_e64 v213, s23\n" \
"v_mfma_f32_16x16x32_bf16 a[188:191], v[136:139], v[176:179], a[188:191]\n" \
"s_add_u32 m0, s30, 0x1800\n" \
"s_mov_b32 s85, 0xc100\n" \
"v_mfma_f32_16x16x32_bf16 a[184:187], v[72:75], v[108:111], 0\n" \
"buffer_load_dwordx4 v22, s[64:67], s85 offen lds\n" \
"v_mfma_f32_16x16x32_bf16 a[184:187], v[136:139], v[172:175], a[184:187]\n" \
"ds_read_b64_tr_b16 v[154:155], v33 offset:41984\n" \
"s_or_b32 s5, s5, s3\n" \
"v_mfma_f32_16x16x32_bf16 a[180:183], v[72:75], v[104:107], 0\n" \
"s_add_u32 m0, s30, 0x1c00\n" \
"s_mov_b32 s84, 0xe100\n" \
"v_mfma_f32_16x16x32_bf16 a[180:183], v[136:139], v[168:171], a[180:183]\n" \
"buffer_load_dwordx4 v23, s[64:67], s84 offen lds\n" \
"v_mfma_f32_16x16x32_bf16 a[176:179], v[72:75], v[100:103], 0\n" \
"ds_read_b64_tr_b16 v[92:93], v34 offset:32768\n" \
"v_cndmask_b32_e32 v221, 0, v220, vcc\n" \
"v_mfma_f32_16x16x32_bf16 a[176:179], v[136:139], v[164:167], a[176:179]\n" \
"ds_read_b128 v[72:75], v28 offset:34816\n" \
"v_or_b32_e64 v12, v217, v221\n" \
"v_mfma_f32_16x16x32_bf16 a[208:211], v[76:79], v[100:103], 0\n" \
"ds_read_b128 v[136:139], v29 offset:34816\n" \
"v_or_b32_e64 v217, 2, v219\n" \
"v_mfma_f32_16x16x32_bf16 a[208:211], v[140:143], v[164:167], a[208:211]\n" \
"ds_read_b64_tr_b16 v[94:95], v34 offset:33792\n" \
"v_cmp_gt_u32_e64 vcc, v217, v214\n" \
"v_mfma_f32_16x16x32_bf16 a[212:215], v[76:79], v[104:107], 0\n" \
"ds_read_b64_tr_b16 v[156:157], v34 offset:40960\n" \
"v_mov_b32_e64 v216, s25\n" \
"v_mfma_f32_16x16x32_bf16 a[212:215], v[140:143], v[168:171], a[212:215]\n" \
"ds_read_b64_tr_b16 v[158:159], v34 offset:41984\n" \
"s_movk_i32 s40, 0xf400\n" \
"v_mfma_f32_16x16x32_bf16 a[216:219], v[76:79], v[108:111], 0\n" \
"ds_read_b64_tr_b16 v[96:97], v35 offset:32768\n" \
"v_cndmask_b32_e64 v221, v215, 0, vcc\n" \
"v_mfma_f32_16x16x32_bf16 a[216:219], v[140:143], v[172:175], a[216:219]\n" \
"ds_read_b64_tr_b16 v[98:99], v35 offset:33792\n" \
"v_cmp_lt_u32_e32 vcc, v217, v214\n" \
"v_mfma_f32_16x16x32_bf16 a[220:223], v[76:79], v[112:115], 0\n" \
"ds_read_b64_tr_b16 v[160:161], v35 offset:40960\n" \
"s_lshl_b32 s4, s4, 6\n" \
"v_mfma_f32_16x16x32_bf16 a[220:223], v[140:143], v[176:179], a[220:223]\n" \
"ds_read_b128 v[76:79], v28 offset:36864\n" \
"v_cndmask_b32_e32 v217, 0, v220, vcc\n" \
"v_mfma_f32_16x16x32_bf16 a[252:255], v[80:83], v[112:115], 0\n" \
"ds_read_b128 v[140:143], v29 offset:36864\n" \
"v_or_b32_e64 v13, v221, v217\n" \
"v_mfma_f32_16x16x32_bf16 a[252:255], v[144:147], v[176:179], a[252:255]\n" \
"ds_read_b64_tr_b16 v[162:163], v35 offset:41984\n" \
"v_or_b32_e64 v217, 16, v219\n" \
"v_mfma_f32_16x16x32_bf16 a[248:251], v[80:83], v[108:111], 0\n" \
"v_cmp_gt_u32_e32 vcc, v217, v214\n" \
"s_nop 1\n" \
"v_cndmask_b32_e64 v221, v215, 0, vcc\n" \
"v_mfma_f32_16x16x32_bf16 a[248:251], v[144:147], v[172:175], a[248:251]\n" \
"v_cmp_lt_u32_e32 vcc, v217, v214\n" \
"s_nop 1\n" \
"v_cndmask_b32_e64 v217, 0, v220, vcc\n" \
"v_mfma_f32_16x16x32_bf16 a[244:247], v[80:83], v[104:107], 0\n" \
"v_or_b32_e32 v14, v221, v217\n" \
"v_or_b32_e32 v217, 18, v219\n" \
"v_mfma_f32_16x16x32_bf16 a[244:247], v[144:147], v[168:171], a[244:247]\n" \
"v_cmp_gt_u32_e32 vcc, v217, v214\n" \
"s_nop 1\n" \
"v_cndmask_b32_e64 v215, v215, 0, vcc\n" \
"v_mfma_f32_16x16x32_bf16 a[240:243], v[80:83], v[100:103], 0\n" \
"v_cmp_lt_u32_e32 vcc, v217, v214\n" \
"s_nop 1\n" \
"v_cndmask_b32_e64 v214, 0, v220, vcc\n" \
"v_mfma_f32_16x16x32_bf16 a[240:243], v[144:147], v[164:167], a[240:243]\n" \
"ds_read_b128 v[80:83], v28 offset:38912\n" \
"ds_read_b128 v[144:147], v29 offset:38912\n" \
"s_waitcnt lgkmcnt(15)\n" \
"v_mfma_f32_16x16x32_bf16 a[0:3], v[52:55], v[84:87], a[0:3]\n" \
"s_add_u32 m0, s31, 0xc00\n" \
"s_mov_b32 s85, 0x118000\n" \
"v_mfma_f32_16x16x32_bf16 a[0:3], v[116:119], v[148:151], a[0:3]\n" \
"buffer_load_dwordx4 v27, s[68:71], s85 offen lds\n" \
"v_mfma_f32_16x16x32_bf16 a[4:7], v[52:55], v[88:91], a[4:7]\n" \
"ds_read_b64_tr_b16 v[100:101], v36 offset:32768\n" \
"v_or_b32_e32 v15, v215, v214\n" \
"s_waitcnt lgkmcnt(15)\n" \
"v_mfma_f32_16x16x32_bf16 a[4:7], v[116:119], v[152:155], a[4:7]\n" \
"s_add_u32 m0, s31, 0x4c00\n" \
"s_mov_b32 s84, 0x118000\n" \
"s_waitcnt lgkmcnt(11)\n" \
"v_mfma_f32_16x16x32_bf16 a[8:11], v[52:55], v[92:95], a[8:11]\n" \
"buffer_load_dwordx4 v27, s[72:75], s84 offen lds\n" \
"s_waitcnt lgkmcnt(9)\n" \
"v_mfma_f32_16x16x32_bf16 a[8:11], v[116:119], v[156:159], a[8:11]\n" \
"ds_read_b64_tr_b16 v[102:103], v36 offset:33792\n" \
"v_mad_u64_u32 v[214:215], s[6:7], s5, 42, v[212:213]\n" \
"s_waitcnt lgkmcnt(8)\n" \
"v_mfma_f32_16x16x32_bf16 a[12:15], v[52:55], v[96:99], a[12:15]\n" \
"ds_read_b64_tr_b16 v[164:165], v36 offset:40960\n" \
"s_movk_i32 s5, 0x1440\n" \
"s_waitcnt lgkmcnt(5)\n" \
"v_mfma_f32_16x16x32_bf16 a[12:15], v[116:119], v[160:163], a[12:15]\n" \
"v_cmp_gt_u32_e64 vcc, s5, v214\n" \
"v_min_u32_e32 v217, 0xa43f, v214\n" \
"v_mfma_f32_16x16x32_bf16 a[44:47], v[56:59], v[96:99], a[44:47]\n" \
"v_mov_b32_e32 v214, s24\n" \
"v_cndmask_b32_e32 v215, v216, v213, vcc\n" \
"v_mfma_f32_16x16x32_bf16 a[44:47], v[120:123], v[160:163], a[44:47]\n" \
"ds_read_b64_tr_b16 v[166:167], v36 offset:41984\n" \
"v_mov_b32_e64 v213, s22\n" \
"v_mfma_f32_16x16x32_bf16 a[40:43], v[56:59], v[92:95], a[40:43]\n" \
"s_movk_i32 s6, 0xffee\n" \
"v_cndmask_b32_e32 v214, v214, v213, vcc\n" \
"v_mfma_f32_16x16x32_bf16 a[40:43], v[120:123], v[156:159], a[40:43]\n" \
"v_add_u32_e32 v213, 0xffffebc0, v217\n" \
"s_lshl_b32 s5, s2, 5\n" \
"v_mfma_f32_16x16x32_bf16 a[36:39], v[56:59], v[88:91], a[36:39]\n" \
"v_cndmask_b32_e32 v213, v213, v217, vcc\n" \
"v_lshlrev_b32_e64 v216, 7, v213\n" \
"v_mfma_f32_16x16x32_bf16 a[36:39], v[120:123], v[152:155], a[36:39]\n" \
"ds_read_b64_tr_b16 v[104:105], v37 offset:32768\n" \
"v_mul_u32_u24_e32 v213, 0xe39, v212\n" \
"v_mfma_f32_16x16x32_bf16 a[32:35], v[56:59], v[84:87], a[32:35]\n" \
"v_mov_b32_e32 v217, 0\n" \
"v_lshrrev_b32_e32 v213, 16, v213\n" \
"v_mfma_f32_16x16x32_bf16 a[32:35], v[120:123], v[148:151], a[32:35]\n" \
"v_lshl_add_u64 v[16:17], v[214:215], 0, v[216:217]\n" \
"v_mad_i32_i24 v214, v213, s6, v212\n" \
"v_mfma_f32_16x16x32_bf16 a[64:67], v[60:63], v[84:87], a[64:67]\n" \
"s_mov_b32 m0, s32\n" \
"ds_read_b64_tr_b16 v[106:107], v37 offset:33792\n" \
"v_mfma_f32_16x16x32_bf16 a[64:67], v[124:127], v[148:151], a[64:67]\n" \
"s_mov_b32 exec_lo, 0x0\n" \
"s_mov_b32 exec_hi, 0xff\n" \
"global_load_lds_dword v[184:185], off\n" \
"s_mov_b64 exec, -1\n" \
"v_mfma_f32_16x16x32_bf16 a[68:71], v[60:63], v[88:91], a[68:71]\n" \
"s_add_i32 s5, s5, 32\n" \
"v_lshlrev_b32_e64 v215, 4, v214\n" \
"v_mfma_f32_16x16x32_bf16 a[68:71], v[124:127], v[152:155], a[68:71]\n" \
"ds_read_b64_tr_b16 v[168:169], v37 offset:40960\n" \
"v_add_u32_e64 v216, s5, v215\n" \
"v_mfma_f32_16x16x32_bf16 a[72:75], v[60:63], v[92:95], a[72:75]\n" \
"v_and_b32_e32 v216, 0xf0, v216\n" \
"v_cmp_gt_u32_e64 vcc, 16, v214\n" \
"v_mfma_f32_16x16x32_bf16 a[72:75], v[124:127], v[156:159], a[72:75]\n" \
"s_mov_b32 m0, s32\n" \
"s_movk_i32 s7, 0xf800\n" \
"v_mfma_f32_16x16x32_bf16 a[76:79], v[60:63], v[96:99], a[76:79]\n" \
"s_mov_b32 exec_lo, 0xffff0000\n" \
"s_mov_b32 exec_hi, 0x0\n" \
"global_load_lds_dword v[186:187], off\n" \
"s_mov_b64 exec, -1\n" \
"v_mfma_f32_16x16x32_bf16 a[76:79], v[124:127], v[160:163], a[76:79]\n" \
"ds_read_b64_tr_b16 v[170:171], v37 offset:41984\n" \
"v_cndmask_b32_e64 v214, v215, v216, vcc\n" \
"v_mfma_f32_16x16x32_bf16 a[108:111], v[64:67], v[96:99], a[108:111]\n" \
"v_or_b32_e64 v215, 64, v212\n" \
"v_mul_u32_u24_e32 v216, 0xe39, v215\n" \
"v_mfma_f32_16x16x32_bf16 a[108:111], v[128:131], v[160:163], a[108:111]\n" \
"v_lshrrev_b32_e64 v216, 16, v216\n" \
"v_mad_i32_i24 v215, v216, s6, v215\n" \
"v_mfma_f32_16x16x32_bf16 a[104:107], v[64:67], v[92:95], a[104:107]\n" \
"ds_read_b64_tr_b16 v[108:109], v38 offset:32768\n" \
"v_lshlrev_b32_e64 v217, 4, v215\n" \
"v_mfma_f32_16x16x32_bf16 a[104:107], v[128:131], v[156:159], a[104:107]\n" \
"v_add_u32_e64 v220, s5, v217\n" \
"v_and_b32_e32 v220, 0xf0, v220\n" \
"v_mfma_f32_16x16x32_bf16 a[100:103], v[64:67], v[88:91], a[100:103]\n" \
"v_cmp_gt_u32_e32 vcc, 16, v215\n" \
"s_nop 1\n" \
"v_cndmask_b32_e64 v215, v217, v220, vcc\n" \
"v_mfma_f32_16x16x32_bf16 a[100:103], v[128:131], v[152:155], a[100:103]\n" \
"ds_read_b64_tr_b16 v[110:111], v38 offset:33792\n" \
"v_or_b32_e32 v217, 0x80, v212\n" \
"v_mfma_f32_16x16x32_bf16 a[96:99], v[64:67], v[84:87], a[96:99]\n" \
"v_mul_u32_u24_e32 v220, 0xe39, v217\n" \
"v_lshrrev_b32_e64 v220, 16, v220\n" \
"v_mfma_f32_16x16x32_bf16 a[96:99], v[128:131], v[148:151], a[96:99]\n" \
"v_mad_i32_i24 v217, v220, s6, v217\n" \
"v_lshlrev_b32_e64 v221, 4, v217\n" \
"v_mfma_f32_16x16x32_bf16 a[128:131], v[68:71], v[84:87], a[128:131]\n" \
"ds_read_b64_tr_b16 v[172:173], v38 offset:40960\n" \
"v_add_u32_e64 v222, s5, v221\n" \
"v_mfma_f32_16x16x32_bf16 a[128:131], v[132:135], v[148:151], a[128:131]\n" \
"v_and_b32_e32 v222, 0xf0, v222\n" \
"v_cmp_gt_u32_e64 vcc, 16, v217\n" \
"v_mfma_f32_16x16x32_bf16 a[132:135], v[68:71], v[88:91], a[132:135]\n" \
"s_nop 0\n" \
"v_cndmask_b32_e32 v217, v221, v222, vcc\n" \
"v_or_b32_e32 v221, 0xc0, v212\n" \
"v_mfma_f32_16x16x32_bf16 a[132:135], v[132:135], v[152:155], a[132:135]\n" \
"ds_read_b64_tr_b16 v[174:175], v38 offset:41984\n" \
"v_mul_u32_u24_e32 v222, 0xe39, v221\n" \
"v_mfma_f32_16x16x32_bf16 a[136:139], v[68:71], v[92:95], a[136:139]\n" \
"v_lshrrev_b32_e64 v222, 16, v222\n" \
"v_mad_i32_i24 v221, v222, s6, v221\n" \
"v_mfma_f32_16x16x32_bf16 a[136:139], v[132:135], v[156:159], a[136:139]\n" \
"v_lshlrev_b32_e32 v223, 4, v221\n" \
"v_add_u32_e32 v224, s5, v223\n" \
"v_mfma_f32_16x16x32_bf16 a[140:143], v[68:71], v[96:99], a[140:143]\n" \
"v_and_b32_e32 v224, 0xf0, v224\n" \
"v_cmp_gt_u32_e64 vcc, 16, v221\n" \
"v_mfma_f32_16x16x32_bf16 a[140:143], v[132:135], v[160:163], a[140:143]\n" \
"ds_read_b64_tr_b16 v[112:113], v39 offset:32768\n" \
"v_cndmask_b32_e64 v221, v223, v224, vcc\n" \
"v_mfma_f32_16x16x32_bf16 a[172:175], v[72:75], v[96:99], a[172:175]\n" \
"v_or_b32_e32 v223, 0x100, v212\n" \
"v_mul_u32_u24_e32 v224, 0xe39, v223\n" \
"v_mfma_f32_16x16x32_bf16 a[172:175], v[136:139], v[160:163], a[172:175]\n" \
"v_lshrrev_b32_e64 v224, 16, v224\n" \
"v_mad_i32_i24 v223, v224, s6, v223\n" \
"v_mfma_f32_16x16x32_bf16 a[168:171], v[72:75], v[92:95], a[168:171]\n" \
"ds_read_b64_tr_b16 v[114:115], v39 offset:33792\n" \
"v_lshlrev_b32_e64 v225, 4, v223\n" \
"v_mfma_f32_16x16x32_bf16 a[168:171], v[136:139], v[156:159], a[168:171]\n" \
"v_add_u32_e64 v226, s5, v225\n" \
"v_and_b32_e32 v226, 0xf0, v226\n" \
"v_mfma_f32_16x16x32_bf16 a[164:167], v[72:75], v[88:91], a[164:167]\n" \
"v_cmp_gt_u32_e32 vcc, 16, v223\n" \
"s_nop 1\n" \
"v_cndmask_b32_e64 v223, v225, v226, vcc\n" \
"v_mfma_f32_16x16x32_bf16 a[164:167], v[136:139], v[152:155], a[164:167]\n" \
"ds_read_b64_tr_b16 v[176:177], v39 offset:40960\n" \
"v_or_b32_e32 v225, 0x140, v212\n" \
"v_mfma_f32_16x16x32_bf16 a[160:163], v[72:75], v[84:87], a[160:163]\n" \
"v_mul_u32_u24_e32 v226, 0xe39, v225\n" \
"v_lshrrev_b32_e64 v226, 16, v226\n" \
"v_mfma_f32_16x16x32_bf16 a[160:163], v[136:139], v[148:151], a[160:163]\n" \
"v_mad_i32_i24 v225, v226, s6, v225\n" \
"v_lshlrev_b32_e64 v227, 4, v225\n" \
"v_mfma_f32_16x16x32_bf16 a[192:195], v[76:79], v[84:87], a[192:195]\n" \
"ds_read_b64_tr_b16 v[178:179], v39 offset:41984\n" \
"v_add_u32_e64 v228, s5, v227\n" \
"v_mfma_f32_16x16x32_bf16 a[192:195], v[140:143], v[148:151], a[192:195]\n" \
"v_and_b32_e32 v228, 0xf0, v228\n" \
"v_cmp_gt_u32_e64 vcc, 16, v225\n" \
"v_mfma_f32_16x16x32_bf16 a[196:199], v[76:79], v[88:91], a[196:199]\n" \
"s_nop 0\n" \
"v_cndmask_b32_e32 v225, v227, v228, vcc\n" \
"v_or_b32_e32 v227, 0x180, v212\n" \
"v_mfma_f32_16x16x32_bf16 a[196:199], v[140:143], v[152:155], a[196:199]\n" \
"v_mul_u32_u24_e32 v228, 0xe39, v227\n" \
"v_lshrrev_b32_e64 v228, 16, v228\n" \
"v_mfma_f32_16x16x32_bf16 a[200:203], v[76:79], v[92:95], a[200:203]\n" \
"v_mad_i32_i24 v227, v228, s6, v227\n" \
"v_lshlrev_b32_e64 v229, 4, v227\n" \
"v_mfma_f32_16x16x32_bf16 a[200:203], v[140:143], v[156:159], a[200:203]\n" \
"v_add_u32_e64 v230, s5, v229\n" \
"v_and_b32_e32 v230, 0xf0, v230\n" \
"v_mfma_f32_16x16x32_bf16 a[204:207], v[76:79], v[96:99], a[204:207]\n" \
"v_cmp_gt_u32_e32 vcc, 16, v227\n" \
"s_nop 1\n" \
"v_cndmask_b32_e64 v227, v229, v230, vcc\n" \
"v_mfma_f32_16x16x32_bf16 a[204:207], v[140:143], v[160:163], a[204:207]\n" \
"v_or_b32_e32 v229, 0x1c0, v212\n" \
"v_mul_u32_u24_e32 v230, 0xe39, v229\n" \
"s_waitcnt lgkmcnt(15)\n" \
"v_mfma_f32_16x16x32_bf16 a[236:239], v[80:83], v[96:99], a[236:239]\n" \
"v_lshrrev_b32_e32 v230, 16, v230\n" \
"v_mad_i32_i24 v229, v230, s6, v229\n" \
"v_mfma_f32_16x16x32_bf16 a[236:239], v[144:147], v[160:163], a[236:239]\n" \
"v_lshlrev_b32_e32 v231, 4, v229\n" \
"v_add_u32_e32 v232, s5, v231\n" \
"v_mfma_f32_16x16x32_bf16 a[232:235], v[80:83], v[92:95], a[232:235]\n" \
"v_and_b32_e32 v232, 0xf0, v232\n" \
"v_cmp_gt_u32_e64 vcc, 16, v229\n" \
"v_mfma_f32_16x16x32_bf16 a[232:235], v[144:147], v[156:159], a[232:235]\n" \
"s_nop 0\n" \
"v_cndmask_b32_e32 v229, v231, v232, vcc\n" \
"v_or_b32_e32 v231, 0x200, v212\n" \
"v_mfma_f32_16x16x32_bf16 a[228:231], v[80:83], v[88:91], a[228:231]\n" \
"v_mul_u32_u24_e32 v232, 0xe39, v231\n" \
"v_lshrrev_b32_e64 v232, 16, v232\n" \
"v_mfma_f32_16x16x32_bf16 a[228:231], v[144:147], v[152:155], a[228:231]\n" \
"v_mad_i32_i24 v231, v232, s6, v231\n" \
"v_lshlrev_b32_e64 v233, 4, v231\n" \
"v_mfma_f32_16x16x32_bf16 a[224:227], v[80:83], v[84:87], a[224:227]\n" \
"v_add_u32_e32 v234, s5, v233\n" \
"s_movk_i32 s5, 0x480\n" \
"v_mfma_f32_16x16x32_bf16 a[224:227], v[144:147], v[148:151], a[224:227]\n" \
"s_waitcnt vmcnt(20)\n" \
"v_readfirstlane_b32 s62, v180\n" \
"s_nop 3\n" \
"s_add_u32 s62, s62, 0x9E3779B1\n" \
"s_waitcnt vmcnt(2) lgkmcnt(0)\n" \
"s_barrier\n" \
"s_nop 0\n" \
"v_mfma_f32_16x16x32_bf16 a[16:19], v[52:55], v[100:103], a[16:19]\n" \
"s_add_u32 m0, s30, 0x8000\n" \
"s_mov_b32 s85, 0x180\n" \
"v_mfma_f32_16x16x32_bf16 a[16:19], v[116:119], v[164:167], a[16:19]\n" \
"buffer_load_dwordx4 v22, s[64:67], s85 offen lds\n" \
"v_mfma_f32_16x16x32_bf16 a[20:23], v[52:55], v[104:107], a[20:23]\n" \
"ds_read_b64_tr_b16 v[84:85], v32 offset:0\n" \
"v_mad_u32_u24 v0, v213, s5, v214\n" \
"v_mfma_f32_16x16x32_bf16 a[20:23], v[116:119], v[168:171], a[20:23]\n" \
"s_add_u32 m0, s30, 0x8400\n" \
"s_mov_b32 s84, 0x2180\n" \
"v_mfma_f32_16x16x32_bf16 a[24:27], v[52:55], v[108:111], a[24:27]\n" \
"buffer_load_dwordx4 v23, s[64:67], s84 offen lds\n" \
"v_mfma_f32_16x16x32_bf16 a[24:27], v[116:119], v[172:175], a[24:27]\n" \
"ds_read_b64_tr_b16 v[86:87], v32 offset:1024\n" \
"v_mul_u32_u24_e32 v213, 0x480, v216\n" \
"v_mfma_f32_16x16x32_bf16 a[28:31], v[52:55], v[112:115], a[28:31]\n" \
"ds_read_b64_tr_b16 v[148:149], v32 offset:8192\n" \
"s_movk_i32 s6, 0xfc00\n" \
"s_nop 0\n" \
"v_mfma_f32_16x16x32_bf16 a[28:31], v[116:119], v[176:179], a[28:31]\n" \
"ds_read_b128 v[52:55], v28 offset:16384\n" \
"v_add3_u32 v1, v213, v215, s6\n" \
"v_mfma_f32_16x16x32_bf16 a[60:63], v[56:59], v[112:115], a[60:63]\n" \
"ds_read_b128 v[116:119], v29 offset:16384\n" \
"v_mul_u32_u24_e32 v213, 0x480, v220\n" \
"v_mfma_f32_16x16x32_bf16 a[60:63], v[120:123], v[176:179], a[60:63]\n" \
"s_add_u32 m0, s31, 0x8000\n" \
"s_mov_b32 s85, 0x180000\n" \
"v_mfma_f32_16x16x32_bf16 a[56:59], v[56:59], v[108:111], a[56:59]\n" \
"buffer_load_dwordx4 v26, s[68:71], s85 offen lds\n" \
"v_mfma_f32_16x16x32_bf16 a[56:59], v[120:123], v[172:175], a[56:59]\n" \
"s_add_u32 m0, s31, 0xc000\n" \
"s_mov_b32 s84, 0x180000\n" \
"v_mfma_f32_16x16x32_bf16 a[52:55], v[56:59], v[104:107], a[52:55]\n" \
"buffer_load_dwordx4 v26, s[72:75], s84 offen lds\n" \
"v_mfma_f32_16x16x32_bf16 a[52:55], v[120:123], v[168:171], a[52:55]\n" \
"s_add_u32 m0, s30, 0x8800\n" \
"s_mov_b32 s85, 0x4180\n" \
"v_mfma_f32_16x16x32_bf16 a[48:51], v[56:59], v[100:103], a[48:51]\n" \
"buffer_load_dwordx4 v22, s[64:67], s85 offen lds\n" \
"v_mfma_f32_16x16x32_bf16 a[48:51], v[120:123], v[164:167], a[48:51]\n" \
"ds_read_b128 v[56:59], v28 offset:18432\n" \
"v_add3_u32 v2, v213, v217, s7\n" \
"v_mfma_f32_16x16x32_bf16 a[80:83], v[60:63], v[100:103], a[80:83]\n" \
"ds_read_b128 v[120:123], v29 offset:18432\n" \
"v_mul_u32_u24_e32 v213, 0x480, v222\n" \
"v_mfma_f32_16x16x32_bf16 a[80:83], v[124:127], v[164:167], a[80:83]\n" \
"s_add_u32 m0, s30, 0x8c00\n" \
"s_mov_b32 s84, 0x6180\n" \
"v_mfma_f32_16x16x32_bf16 a[84:87], v[60:63], v[104:107], a[84:87]\n" \
"buffer_load_dwordx4 v23, s[64:67], s84 offen lds\n" \
"v_mfma_f32_16x16x32_bf16 a[84:87], v[124:127], v[168:171], a[84:87]\n" \
"ds_read_b64_tr_b16 v[150:151], v32 offset:9216\n" \
"v_add3_u32 v3, v213, v221, s40\n" \
"v_mfma_f32_16x16x32_bf16 a[88:91], v[60:63], v[108:111], a[88:91]\n" \
"s_add_u32 m0, s31, 0x8400\n" \
"s_mov_b32 s85, 0x188000\n" \
"v_mfma_f32_16x16x32_bf16 a[88:91], v[124:127], v[172:175], a[88:91]\n" \
"buffer_load_dwordx4 v26, s[68:71], s85 offen lds\n" \
"v_mfma_f32_16x16x32_bf16 a[92:95], v[60:63], v[112:115], a[92:95]\n" \
"ds_read_b64_tr_b16 v[88:89], v33 offset:0\n" \
"v_mul_u32_u24_e32 v213, 0x480, v226\n" \
"v_mfma_f32_16x16x32_bf16 a[92:95], v[124:127], v[176:179], a[92:95]\n" \
"ds_read_b128 v[60:63], v28 offset:20480\n" \
"v_add3_u32 v5, v213, v225, s6\n" \
"v_mfma_f32_16x16x32_bf16 a[124:127], v[64:67], v[112:115], a[124:127]\n" \
"ds_read_b128 v[124:127], v29 offset:20480\n" \
"v_mul_u32_u24_e32 v213, 0x480, v228\n" \
"v_mfma_f32_16x16x32_bf16 a[124:127], v[128:131], v[176:179], a[124:127]\n" \
"s_add_u32 m0, s31, 0xc400\n" \
"s_mov_b32 s84, 0x188000\n" \
"v_mfma_f32_16x16x32_bf16 a[120:123], v[64:67], v[108:111], a[120:123]\n" \
"buffer_load_dwordx4 v26, s[72:75], s84 offen lds\n" \
"v_mfma_f32_16x16x32_bf16 a[120:123], v[128:131], v[172:175], a[120:123]\n" \
"s_add_u32 m0, s30, 0x9000\n" \
"s_mov_b32 s85, 0x8180\n" \
"v_mfma_f32_16x16x32_bf16 a[116:119], v[64:67], v[104:107], a[116:119]\n" \
"buffer_load_dwordx4 v22, s[64:67], s85 offen lds\n" \
"v_mfma_f32_16x16x32_bf16 a[116:119], v[128:131], v[168:171], a[116:119]\n" \
"ds_read_b64_tr_b16 v[90:91], v33 offset:1024\n" \
"v_add3_u32 v6, v213, v227, s7\n" \
"v_mfma_f32_16x16x32_bf16 a[112:115], v[64:67], v[100:103], a[112:115]\n" \
"ds_read_b64_tr_b16 v[152:153], v33 offset:8192\n" \
"v_mul_u32_u24_e32 v213, 0x480, v230\n" \
"v_mfma_f32_16x16x32_bf16 a[112:115], v[128:131], v[164:167], a[112:115]\n" \
"ds_read_b128 v[64:67], v28 offset:22528\n" \
"v_add3_u32 v7, v213, v229, s40\n" \
"v_mfma_f32_16x16x32_bf16 a[144:147], v[68:71], v[100:103], a[144:147]\n" \
"ds_read_b128 v[128:131], v29 offset:22528\n" \
"s_lshl_b32 s7, s3, 3\n" \
"s_nop 0\n" \
"v_mfma_f32_16x16x32_bf16 a[144:147], v[132:135], v[164:167], a[144:147]\n" \
"s_add_u32 m0, s30, 0x9400\n" \
"s_mov_b32 s84, 0xa180\n" \
"v_mfma_f32_16x16x32_bf16 a[148:151], v[68:71], v[104:107], a[148:151]\n" \
"buffer_load_dwordx4 v23, s[64:67], s84 offen lds\n" \
"v_mfma_f32_16x16x32_bf16 a[148:151], v[132:135], v[168:171], a[148:151]\n" \
"s_add_u32 m0, s31, 0x8800\n" \
"s_mov_b32 s85, 0x190000\n" \
"v_mfma_f32_16x16x32_bf16 a[152:155], v[68:71], v[108:111], a[152:155]\n" \
"buffer_load_dwordx4 v27, s[68:71], s85 offen lds\n" \
"v_mfma_f32_16x16x32_bf16 a[152:155], v[132:135], v[172:175], a[152:155]\n" \
"s_add_u32 m0, s31, 0xc800\n" \
"s_mov_b32 s84, 0x190000\n" \
"v_mfma_f32_16x16x32_bf16 a[156:159], v[68:71], v[112:115], a[156:159]\n" \
"buffer_load_dwordx4 v27, s[72:75], s84 offen lds\n" \
"v_mfma_f32_16x16x32_bf16 a[156:159], v[132:135], v[176:179], a[156:159]\n" \
"ds_read_b128 v[68:71], v28 offset:0\n" \
"s_lshl_b32 s40, s3, 1\n" \
"v_mfma_f32_16x16x32_bf16 a[188:191], v[72:75], v[112:115], a[188:191]\n" \
"ds_read_b128 v[132:135], v29 offset:0\n" \
"s_and_b32 s7, s7, 16\n" \
"v_mfma_f32_16x16x32_bf16 a[188:191], v[136:139], v[176:179], a[188:191]\n" \
"s_add_u32 m0, s30, 0x9800\n" \
"s_mov_b32 s85, 0xc180\n" \
"v_mfma_f32_16x16x32_bf16 a[184:187], v[72:75], v[108:111], a[184:187]\n" \
"buffer_load_dwordx4 v22, s[64:67], s85 offen lds\n" \
"v_mfma_f32_16x16x32_bf16 a[184:187], v[136:139], v[172:175], a[184:187]\n" \
"ds_read_b64_tr_b16 v[154:155], v33 offset:9216\n" \
"s_and_b32 s40, s40, 2\n" \
"v_mfma_f32_16x16x32_bf16 a[180:183], v[72:75], v[104:107], a[180:183]\n" \
"s_add_u32 m0, s30, 0x9c00\n" \
"s_mov_b32 s84, 0xe180\n" \
"v_mfma_f32_16x16x32_bf16 a[180:183], v[136:139], v[168:171], a[180:183]\n" \
"buffer_load_dwordx4 v23, s[64:67], s84 offen lds\n" \
"v_mfma_f32_16x16x32_bf16 a[176:179], v[72:75], v[100:103], a[176:179]\n" \
"ds_read_b64_tr_b16 v[92:93], v34 offset:0\n" \
"s_and_b32 s6, s2, 7\n" \
"v_mfma_f32_16x16x32_bf16 a[176:179], v[136:139], v[164:167], a[176:179]\n" \
"ds_read_b128 v[72:75], v28 offset:2048\n" \
"v_bfe_u32 v213, v20, 7, 1\n" \
"v_mfma_f32_16x16x32_bf16 a[208:211], v[76:79], v[100:103], a[208:211]\n" \
"ds_read_b128 v[136:139], v29 offset:2048\n" \
"s_or_b32 s7, s40, s7\n" \
"v_mfma_f32_16x16x32_bf16 a[208:211], v[140:143], v[164:167], a[208:211]\n" \
"ds_read_b64_tr_b16 v[94:95], v34 offset:1024\n" \
"v_and_b32_e32 v234, 0xf0, v234\n" \
"v_mfma_f32_16x16x32_bf16 a[212:215], v[76:79], v[104:107], a[212:215]\n" \
"ds_read_b64_tr_b16 v[156:157], v34 offset:8192\n" \
"v_cmp_gt_u32_e32 vcc, 16, v231\n" \
"v_mfma_f32_16x16x32_bf16 a[212:215], v[140:143], v[168:171], a[212:215]\n" \
"ds_read_b64_tr_b16 v[158:159], v34 offset:9216\n" \
"v_or3_b32 v213, s7, v213, v219\n" \
"v_mfma_f32_16x16x32_bf16 a[216:219], v[76:79], v[108:111], a[216:219]\n" \
"ds_read_b64_tr_b16 v[96:97], v35 offset:0\n" \
"v_and_b32_e64 v214, 32, v20\n" \
"v_mfma_f32_16x16x32_bf16 a[216:219], v[140:143], v[172:175], a[216:219]\n" \
"ds_read_b64_tr_b16 v[98:99], v35 offset:1024\n" \
"s_lshl_b32 s6, s6, 5\n" \
"v_mfma_f32_16x16x32_bf16 a[220:223], v[76:79], v[112:115], a[220:223]\n" \
"ds_read_b64_tr_b16 v[160:161], v35 offset:8192\n" \
"v_cndmask_b32_e32 v231, v233, v234, vcc\n" \
"v_mfma_f32_16x16x32_bf16 a[220:223], v[140:143], v[176:179], a[220:223]\n" \
"ds_read_b128 v[76:79], v28 offset:4096\n" \
"v_mul_u32_u24_e64 v213, 18, v213\n" \
"v_mfma_f32_16x16x32_bf16 a[252:255], v[80:83], v[112:115], a[252:255]\n" \
"ds_read_b128 v[140:143], v29 offset:4096\n" \
"v_mov_b32_e32 v215, 0x100\n" \
"v_mfma_f32_16x16x32_bf16 a[252:255], v[144:147], v[176:179], a[252:255]\n" \
"ds_read_b64_tr_b16 v[162:163], v35 offset:9216\n" \
"v_mov_b32_e64 v216, s6\n" \
"v_mfma_f32_16x16x32_bf16 a[248:251], v[80:83], v[108:111], a[248:251]\n" \
"v_cmp_eq_u32_e64 vcc, 0, v214\n" \
"v_and_or_b32 v213, v218, 1, v213\n" \
"v_mfma_f32_16x16x32_bf16 a[248:251], v[144:147], v[172:175], a[248:251]\n" \
"v_lshlrev_b32_e32 v212, 3, v212\n" \
"v_cndmask_b32_e32 v214, v215, v216, vcc\n" \
"v_mfma_f32_16x16x32_bf16 a[244:247], v[80:83], v[104:107], a[244:247]\n" \
"v_mad_u32_u24 v213, v213, s5, v214\n" \
"v_and_or_b32 v213, v20, 16, v213\n" \
"v_mfma_f32_16x16x32_bf16 a[244:247], v[144:147], v[168:171], a[244:247]\n" \
"v_add_u32_e32 v9, 0x4800, v213\n" \
"v_mul_u32_u24_e32 v213, 0x120, v218\n" \
"v_mfma_f32_16x16x32_bf16 a[240:243], v[80:83], v[100:103], a[240:243]\n" \
"v_and_or_b32 v10, v212, 24, v213\n" \
"v_lshrrev_b32_e64 v213, 2, v20\n" \
"v_mfma_f32_16x16x32_bf16 a[240:243], v[144:147], v[164:167], a[240:243]\n" \
"ds_read_b128 v[80:83], v28 offset:6144\n" \
"ds_read_b128 v[144:147], v29 offset:6144\n" \
"s_waitcnt lgkmcnt(15)\n" \
"v_mfma_f32_16x16x32_bf16 a[0:3], v[52:55], v[84:87], a[0:3]\n" \
"s_add_u32 m0, s31, 0x8c00\n" \
"s_mov_b32 s85, 0x198000\n" \
"v_mfma_f32_16x16x32_bf16 a[0:3], v[116:119], v[148:151], a[0:3]\n" \
"buffer_load_dwordx4 v27, s[68:71], s85 offen lds\n" \
"v_mfma_f32_16x16x32_bf16 a[4:7], v[52:55], v[88:91], a[4:7]\n" \
"ds_read_b64_tr_b16 v[100:101], v36 offset:0\n" \
"v_and_or_b32 v213, v213, 60, s4\n" \
"s_waitcnt lgkmcnt(15)\n" \
"v_mfma_f32_16x16x32_bf16 a[4:7], v[116:119], v[152:155], a[4:7]\n" \
"s_add_u32 m0, s31, 0xcc00\n" \
"s_mov_b32 s84, 0x198000\n" \
"s_waitcnt lgkmcnt(11)\n" \
"v_mfma_f32_16x16x32_bf16 a[8:11], v[52:55], v[92:95], a[8:11]\n" \
"buffer_load_dwordx4 v27, s[72:75], s84 offen lds\n" \
"s_waitcnt lgkmcnt(9)\n" \
"v_mfma_f32_16x16x32_bf16 a[8:11], v[116:119], v[156:159], a[8:11]\n" \
"ds_read_b64_tr_b16 v[102:103], v36 offset:1024\n" \
"v_lshl_or_b32 v212, s3, 9, v212\n" \
"s_waitcnt lgkmcnt(8)\n" \
"v_mfma_f32_16x16x32_bf16 a[12:15], v[52:55], v[96:99], a[12:15]\n" \
"ds_read_b64_tr_b16 v[164:165], v36 offset:8192\n" \
"v_mad_u32_u24 v4, v224, s5, v223\n" \
"s_waitcnt lgkmcnt(5)\n" \
"v_mfma_f32_16x16x32_bf16 a[12:15], v[116:119], v[160:163], a[12:15]\n" \
"v_mad_u32_u24 v8, v232, s5, v231\n" \
"v_or_b32_e32 v18, 0x27000, v213\n" \
"v_mfma_f32_16x16x32_bf16 a[44:47], v[56:59], v[96:99], a[44:47]\n" \
"v_or_b32_e32 v19, 0x27800, v212\n" \
"v_mov_b32_e32 v219, 0x1000c0c\n" \
"v_mfma_f32_16x16x32_bf16 a[44:47], v[120:123], v[160:163], a[44:47]\n" \
"ds_read_b64_tr_b16 v[166:167], v36 offset:9216\n" \
"v_mov_b32_e32 v220, 0xc0c0100\n" \
"v_mfma_f32_16x16x32_bf16 a[40:43], v[56:59], v[92:95], a[40:43]\n" \
".fill (L_pb + 52960 - .) / 4, 4, 0xBF800000\n" \
"L_pp13:\n" \
"s_cbranch_execz L_pret\n" \
"buffer_load_dwordx4 v[200:203], v192, s[76:79], s34 offen offset:0\n" \
"v_mfma_f32_16x16x32_bf16 a[40:43], v[120:123], v[156:159], a[40:43]\n" \
"v_bfe_u32 v218, v20, 4, 1\n" \
"v_lshrrev_b32_e32 v213, 8, v20\n" \
"v_mfma_f32_16x16x32_bf16 a[36:39], v[56:59], v[88:91], a[36:39]\n" \
"v_cmp_eq_u32_e32 vcc, 0, v218\n" \
"s_lshr_b32 s4, s2, 5\n" \
"v_mfma_f32_16x16x32_bf16 a[36:39], v[120:123], v[152:155], a[36:39]\n" \
"ds_read_b64_tr_b16 v[104:105], v37 offset:0\n" \
"s_and_b32 s5, s3, 1\n" \
"v_mfma_f32_16x16x32_bf16 a[32:35], v[56:59], v[84:87], a[32:35]\n" \
"v_bfe_u32 v217, v20, 4, 4\n" \
"v_cndmask_b32_e32 v219, v219, v220, vcc\n" \
"v_mfma_f32_16x16x32_bf16 a[32:35], v[120:123], v[148:151], a[32:35]\n" \
"v_mov_b32_e32 v220, 0x3020c0c\n" \
"v_mov_b32_e32 v221, 0xc0c0302\n" \
"v_mfma_f32_16x16x32_bf16 a[64:67], v[60:63], v[84:87], a[64:67]\n" \
"buffer_load_dwordx4 v[204:207], v193, s[76:79], s34 offen offset:0\n" \
"v_mfma_f32_16x16x32_bf16 a[64:67], v[124:127], v[148:151], a[64:67]\n" \
"s_mov_b32 m0, s32\n" \
"ds_read_b64_tr_b16 v[106:107], v37 offset:1024\n" \
"v_mfma_f32_16x16x32_bf16 a[68:71], v[60:63], v[88:91], a[68:71]\n" \
"s_mov_b32 exec_lo, 0x0\n" \
"s_mov_b32 exec_hi, 0xff00\n" \
"global_load_lds_dword v[184:185], off\n" \
"s_mov_b64 exec, -1\n" \
"v_mfma_f32_16x16x32_bf16 a[68:71], v[124:127], v[152:155], a[68:71]\n" \
"ds_read_b64_tr_b16 v[168:169], v37 offset:8192\n" \
"v_lshl_or_b32 v218, v213, 2, v218\n" \
"v_mfma_f32_16x16x32_bf16 a[72:75], v[60:63], v[92:95], a[72:75]\n" \
"s_xor_b32 s6, s4, 15\n" \
"v_mfma_f32_16x16x32_bf16 a[72:75], v[124:127], v[156:159], a[72:75]\n" \
"buffer_load_dword v208, v194, s[76:79], s34 offen offset:32\n" \
"v_mfma_f32_16x16x32_bf16 a[76:79], v[60:63], v[96:99], a[76:79]\n" \
"ds_read_b64_tr_b16 v[170:171], v37 offset:9216\n" \
"v_mfma_f32_16x16x32_bf16 a[76:79], v[124:127], v[160:163], a[76:79]\n" \
"s_cmpk_lt_u32 s3, 2\n" \
"s_cselect_b32 s7, s6, s4\n" \
"v_mfma_f32_16x16x32_bf16 a[108:111], v[64:67], v[96:99], a[108:111]\n" \
"s_lshl_b32 s40, s7, 7\n" \
"s_lshl_b32 s41, s5, 6\n" \
"v_mfma_f32_16x16x32_bf16 a[108:111], v[128:131], v[160:163], a[108:111]\n" \
"v_cndmask_b32_e64 v220, v220, v221, vcc\n" \
"v_mov_b32_e32 v221, 0xc0c0c0c\n" \
"v_mfma_f32_16x16x32_bf16 a[104:107], v[64:67], v[92:95], a[104:107]\n" \
"buffer_load_dword v209, v194, s[76:79], s34 offen offset:1184\n" \
"v_mfma_f32_16x16x32_bf16 a[104:107], v[128:131], v[156:159], a[104:107]\n" \
"ds_read_b64_tr_b16 v[108:109], v38 offset:0\n" \
"v_cmp_gt_u32_e64 vcc, v218, v217\n" \
"v_mfma_f32_16x16x32_bf16 a[100:103], v[64:67], v[88:91], a[100:103]\n" \
"v_or_b32_e32 v218, 2, v218\n" \
"v_lshrrev_b32_e32 v212, 7, v20\n" \
"v_mfma_f32_16x16x32_bf16 a[100:103], v[128:131], v[152:155], a[100:103]\n" \
"ds_read_b64_tr_b16 v[110:111], v38 offset:1024\n" \
"s_or_b32 s41, s41, s40\n" \
"v_mfma_f32_16x16x32_bf16 a[96:99], v[64:67], v[84:87], a[96:99]\n" \
"v_cndmask_b32_e32 v40, v221, v219, vcc\n" \
"v_cndmask_b32_e64 v42, v219, v221, vcc\n" \
"v_mfma_f32_16x16x32_bf16 a[96:99], v[128:131], v[148:151], a[96:99]\n" \
"v_cmp_gt_u32_e32 vcc, v218, v217\n" \
"v_and_b32_e32 v218, 16, v20\n" \
"v_mfma_f32_16x16x32_bf16 a[128:131], v[68:71], v[84:87], a[128:131]\n" \
"buffer_load_dword v211, v194, s[76:79], s34 offen offset:2336\n" \
"v_mfma_f32_16x16x32_bf16 a[128:131], v[132:135], v[148:151], a[128:131]\n" \
"ds_read_b64_tr_b16 v[172:173], v38 offset:8192\n" \
"s_movk_i32 s42, 0x3f80\n" \
"v_mfma_f32_16x16x32_bf16 a[132:135], v[68:71], v[88:91], a[132:135]\n" \
"v_or_b32_e32 v214, s41, v212\n" \
"v_and_b32_e32 v217, 0x320, v20\n" \
"v_mfma_f32_16x16x32_bf16 a[132:135], v[132:135], v[152:155], a[132:135]\n" \
"ds_read_b64_tr_b16 v[174:175], v38 offset:9216\n" \
"s_movk_i32 s41, 0x100\n" \
"v_mfma_f32_16x16x32_bf16 a[136:139], v[68:71], v[92:95], a[136:139]\n" \
"v_lshlrev_b32_e64 v218, v218, s42\n" \
"v_mov_b32_e32 v219, 0\n" \
"v_mfma_f32_16x16x32_bf16 a[136:139], v[132:135], v[156:159], a[136:139]\n" \
"s_mov_b32 s76, s20\n" \
"s_and_b32 s77, s21, 0xffff\n" \
"v_mfma_f32_16x16x32_bf16 a[140:143], v[68:71], v[96:99], a[140:143]\n" \
"s_mov_b32 s78, 0x480\n" \
"v_lshrrev_b32_e32 v215, 6, v20\n" \
"v_mfma_f32_16x16x32_bf16 a[140:143], v[132:135], v[160:163], a[140:143]\n" \
"buffer_load_ushort v210, v195, s[76:79], 0 offen\n" \
"v_mfma_f32_16x16x32_bf16 a[172:175], v[72:75], v[96:99], a[172:175]\n" \
"ds_read_b64_tr_b16 v[112:113], v39 offset:0\n" \
"v_cndmask_b32_e64 v41, v221, v220, vcc\n" \
"v_mfma_f32_16x16x32_bf16 a[172:175], v[136:139], v[160:163], a[172:175]\n" \
"v_cndmask_b32_e32 v43, v220, v221, vcc\n" \
"v_cmp_eq_u32_e32 vcc, s41, v217\n" \
"v_mfma_f32_16x16x32_bf16 a[168:171], v[72:75], v[92:95], a[168:171]\n" \
"s_mov_b32 s76, s8\n" \
"s_and_b32 s77, s9, 0xffff\n" \
"v_mfma_f32_16x16x32_bf16 a[168:171], v[136:139], v[156:159], a[168:171]\n" \
"s_mov_b32 s78, 0x240000\n" \
"ds_read_b64_tr_b16 v[114:115], v39 offset:1024\n" \
"v_mfma_f32_16x16x32_bf16 a[164:167], v[72:75], v[88:91], a[164:167]\n" \
"v_and_b32_e32 v217, 0x330, v20\n" \
"v_bfe_u32 v216, v20, 6, 2\n" \
"v_mfma_f32_16x16x32_bf16 a[164:167], v[136:139], v[152:155], a[164:167]\n" \
"ds_read_b64_tr_b16 v[176:177], v39 offset:8192\n" \
"v_cndmask_b32_e32 v46, v219, v218, vcc\n" \
"v_mfma_f32_16x16x32_bf16 a[160:163], v[72:75], v[84:87], a[160:163]\n" \
"v_mov_b32_e32 v218, 0x3f80\n" \
"v_cmp_eq_u32_e64 vcc, 32, v217\n" \
"v_mfma_f32_16x16x32_bf16 a[160:163], v[136:139], v[148:151], a[160:163]\n" \
"s_add_i32 s41, s40, -16\n" \
"v_lshrrev_b32_e32 v219, 1, v20\n" \
"v_mfma_f32_16x16x32_bf16 a[192:195], v[76:79], v[84:87], a[192:195]\n" \
"ds_read_b64_tr_b16 v[178:179], v39 offset:9216\n" \
"v_bfe_u32 v215, v215, 1, 1\n" \
"v_mfma_f32_16x16x32_bf16 a[192:195], v[140:143], v[148:151], a[192:195]\n" \
"v_cndmask_b32_e32 v47, 0, v218, vcc\n" \
"s_mov_b32 s42, 0\n" \
"v_mfma_f32_16x16x32_bf16 a[196:199], v[76:79], v[88:91], a[196:199]\n" \
"s_cmp_lg_u32 s40, 0\n" \
"s_cselect_b32 s41, s41, s42\n" \
"v_mfma_f32_16x16x32_bf16 a[196:199], v[140:143], v[152:155], a[196:199]\n" \
"v_lshl_or_b32 v217, s5, 3, v212\n" \
"v_lshlrev_b32_e64 v218, 10, v213\n" \
"v_mfma_f32_16x16x32_bf16 a[200:203], v[76:79], v[92:95], a[200:203]\n" \
"v_lshlrev_b32_e32 v216, 7, v216\n" \
"v_and_b32_e32 v219, 24, v219\n" \
"v_mfma_f32_16x16x32_bf16 a[200:203], v[140:143], v[156:159], a[200:203]\n" \
"v_and_or_b32 v212, v212, 2, v215\n" \
"v_add_u32_e64 v217, s41, v217\n" \
"v_mfma_f32_16x16x32_bf16 a[204:207], v[76:79], v[96:99], a[204:207]\n" \
"v_or3_b32 v216, v216, v218, v219\n" \
"v_lshlrev_b32_e64 v212, 5, v212\n" \
"v_mfma_f32_16x16x32_bf16 a[204:207], v[140:143], v[160:163], a[204:207]\n" \
"s_mov_b32 s41, 0x14000\n" \
"v_or3_b32 v48, v216, v212, s41\n" \
"s_waitcnt lgkmcnt(15)\n" \
"v_mfma_f32_16x16x32_bf16 a[236:239], v[80:83], v[96:99], a[236:239]\n" \
"s_lshl_b32 s41, s5, 2\n" \
"s_lshl_b32 s7, s7, 3\n" \
"v_mfma_f32_16x16x32_bf16 a[236:239], v[144:147], v[160:163], a[236:239]\n" \
"v_or_b32_e32 v212, s41, v213\n" \
"s_or_b32 s7, s7, s41\n" \
"v_mfma_f32_16x16x32_bf16 a[232:235], v[80:83], v[92:95], a[232:235]\n" \
"s_mov_b32 s41, 0xff800000\n" \
"v_mfma_f32_16x16x32_bf16 a[232:235], v[144:147], v[156:159], a[232:235]\n" \
"s_cmp_lg_u32 s40, 0\n" \
"s_cselect_b32 s47, s42, s41\n" \
"v_mfma_f32_16x16x32_bf16 a[228:231], v[80:83], v[88:91], a[228:231]\n" \
"s_mov_b32 s40, 0xc000\n" \
"s_mov_b32 s41, 0x8000\n" \
"v_mfma_f32_16x16x32_bf16 a[228:231], v[144:147], v[152:155], a[228:231]\n" \
"s_cmp_lg_u32 s4, 0\n" \
"s_cselect_b32 s51, s40, s41\n" \
"v_mfma_f32_16x16x32_bf16 a[224:227], v[80:83], v[84:87], a[224:227]\n" \
"s_movk_i32 s40, 0x4000\n" \
"v_mfma_f32_16x16x32_bf16 a[224:227], v[144:147], v[148:151], a[224:227]\n" \
"s_waitcnt vmcnt(7) lgkmcnt(0)\n" \
"s_barrier\n" \
"v_mfma_f32_16x16x32_bf16 a[16:19], v[52:55], v[100:103], a[16:19]\n" \
"s_add_u32 m0, s30, 0x0\n" \
"s_nop 0\n" \
"s_mov_b32 s85, 0x200\n" \
"v_mfma_f32_16x16x32_bf16 a[16:19], v[116:119], v[164:167], a[16:19]\n" \
"buffer_load_dwordx4 v22, s[64:67], s85 offen lds\n" \
"v_mfma_f32_16x16x32_bf16 a[20:23], v[52:55], v[104:107], a[20:23]\n" \
"ds_read_b64_tr_b16 v[84:85], v32 offset:32768\n" \
"v_mfma_f32_16x16x32_bf16 a[20:23], v[116:119], v[168:171], a[20:23]\n" \
"s_add_u32 m0, s30, 0x400\n" \
"s_mov_b32 s84, 0x2200\n" \
"v_mfma_f32_16x16x32_bf16 a[24:27], v[52:55], v[108:111], a[24:27]\n" \
"buffer_load_dwordx4 v23, s[64:67], s84 offen lds\n" \
"v_mfma_f32_16x16x32_bf16 a[24:27], v[116:119], v[172:175], a[24:27]\n" \
"ds_read_b64_tr_b16 v[86:87], v32 offset:33792\n" \
"v_mfma_f32_16x16x32_bf16 a[28:31], v[52:55], v[112:115], a[28:31]\n" \
"ds_read_b64_tr_b16 v[148:149], v32 offset:40960\n" \
"v_mfma_f32_16x16x32_bf16 a[28:31], v[116:119], v[176:179], a[28:31]\n" \
"ds_read_b128 v[52:55], v28 offset:49152\n" \
"v_mfma_f32_16x16x32_bf16 a[60:63], v[56:59], v[112:115], a[60:63]\n" \
"ds_read_b128 v[116:119], v29 offset:49152\n" \
"v_mfma_f32_16x16x32_bf16 a[60:63], v[120:123], v[176:179], a[60:63]\n" \
"s_add_u32 m0, s31, 0x0\n" \
"s_nop 0\n" \
"s_mov_b32 s85, 0x200000\n" \
"v_mfma_f32_16x16x32_bf16 a[56:59], v[56:59], v[108:111], a[56:59]\n" \
"buffer_load_dwordx4 v26, s[68:71], s85 offen lds\n" \
"v_mfma_f32_16x16x32_bf16 a[56:59], v[120:123], v[172:175], a[56:59]\n" \
"s_add_u32 m0, s31, 0x4000\n" \
"s_mov_b32 s84, 0x200000\n" \
"v_mfma_f32_16x16x32_bf16 a[52:55], v[56:59], v[104:107], a[52:55]\n" \
"buffer_load_dwordx4 v26, s[72:75], s84 offen lds\n" \
"v_mfma_f32_16x16x32_bf16 a[52:55], v[120:123], v[168:171], a[52:55]\n" \
"s_add_u32 m0, s30, 0x800\n" \
"s_mov_b32 s85, 0x4200\n" \
"v_mfma_f32_16x16x32_bf16 a[48:51], v[56:59], v[100:103], a[48:51]\n" \
"buffer_load_dwordx4 v22, s[64:67], s85 offen lds\n" \
"v_mfma_f32_16x16x32_bf16 a[48:51], v[120:123], v[164:167], a[48:51]\n" \
"ds_read_b128 v[56:59], v28 offset:51200\n" \
"v_mfma_f32_16x16x32_bf16 a[80:83], v[60:63], v[100:103], a[80:83]\n" \
"ds_read_b128 v[120:123], v29 offset:51200\n" \
"v_mfma_f32_16x16x32_bf16 a[80:83], v[124:127], v[164:167], a[80:83]\n" \
"s_add_u32 m0, s30, 0xc00\n" \
"s_mov_b32 s84, 0x6200\n" \
"v_mfma_f32_16x16x32_bf16 a[84:87], v[60:63], v[104:107], a[84:87]\n" \
"buffer_load_dwordx4 v23, s[64:67], s84 offen lds\n" \
"v_mfma_f32_16x16x32_bf16 a[84:87], v[124:127], v[168:171], a[84:87]\n" \
"ds_read_b64_tr_b16 v[150:151], v32 offset:41984\n" \
"v_mfma_f32_16x16x32_bf16 a[88:91], v[60:63], v[108:111], a[88:91]\n" \
"s_add_u32 m0, s31, 0x400\n" \
"s_mov_b32 s85, 0x208000\n" \
"v_mfma_f32_16x16x32_bf16 a[88:91], v[124:127], v[172:175], a[88:91]\n" \
"buffer_load_dwordx4 v26, s[68:71], s85 offen lds\n" \
"v_mfma_f32_16x16x32_bf16 a[92:95], v[60:63], v[112:115], a[92:95]\n" \
"ds_read_b64_tr_b16 v[88:89], v33 offset:32768\n" \
"v_mfma_f32_16x16x32_bf16 a[92:95], v[124:127], v[176:179], a[92:95]\n" \
"ds_read_b128 v[60:63], v28 offset:53248\n" \
"v_mfma_f32_16x16x32_bf16 a[124:127], v[64:67], v[112:115], a[124:127]\n" \
"ds_read_b128 v[124:127], v29 offset:53248\n" \
"v_mfma_f32_16x16x32_bf16 a[124:127], v[128:131], v[176:179], a[124:127]\n" \
"s_add_u32 m0, s31, 0x4400\n" \
"s_mov_b32 s84, 0x208000\n" \
"v_mfma_f32_16x16x32_bf16 a[120:123], v[64:67], v[108:111], a[120:123]\n" \
"buffer_load_dwordx4 v26, s[72:75], s84 offen lds\n" \
"v_mfma_f32_16x16x32_bf16 a[120:123], v[128:131], v[172:175], a[120:123]\n" \
"s_add_u32 m0, s30, 0x1000\n" \
"s_mov_b32 s85, 0x8200\n" \
"v_mfma_f32_16x16x32_bf16 a[116:119], v[64:67], v[104:107], a[116:119]\n" \
"buffer_load_dwordx4 v22, s[64:67], s85 offen lds\n" \
"v_mfma_f32_16x16x32_bf16 a[116:119], v[128:131], v[168:171], a[116:119]\n" \
"ds_read_b64_tr_b16 v[90:91], v33 offset:33792\n" \
"v_mfma_f32_16x16x32_bf16 a[112:115], v[64:67], v[100:103], a[112:115]\n" \
"ds_read_b64_tr_b16 v[152:153], v33 offset:40960\n" \
"v_mfma_f32_16x16x32_bf16 a[112:115], v[128:131], v[164:167], a[112:115]\n" \
"ds_read_b128 v[64:67], v28 offset:55296\n" \
"v_mfma_f32_16x16x32_bf16 a[144:147], v[68:71], v[100:103], a[144:147]\n" \
"ds_read_b128 v[128:131], v29 offset:55296\n" \
"v_mfma_f32_16x16x32_bf16 a[144:147], v[132:135], v[164:167], a[144:147]\n" \
"s_add_u32 m0, s30, 0x1400\n" \
"s_mov_b32 s84, 0xa200\n" \
"v_mfma_f32_16x16x32_bf16 a[148:151], v[68:71], v[104:107], a[148:151]\n" \
"buffer_load_dwordx4 v23, s[64:67], s84 offen lds\n" \
"v_mfma_f32_16x16x32_bf16 a[148:151], v[132:135], v[168:171], a[148:151]\n" \
"s_add_u32 m0, s31, 0x800\n" \
"s_mov_b32 s85, 0x210000\n" \
"v_mfma_f32_16x16x32_bf16 a[152:155], v[68:71], v[108:111], a[152:155]\n" \
"buffer_load_dwordx4 v27, s[68:71], s85 offen lds\n" \
"v_mfma_f32_16x16x32_bf16 a[152:155], v[132:135], v[172:175], a[152:155]\n" \
"s_add_u32 m0, s31, 0x4800\n" \
"s_mov_b32 s84, 0x210000\n" \
"v_mfma_f32_16x16x32_bf16 a[156:159], v[68:71], v[112:115], a[156:159]\n" \
"buffer_load_dwordx4 v27, s[72:75], s84 offen lds\n" \
"v_mfma_f32_16x16x32_bf16 a[156:159], v[132:135], v[176:179], a[156:159]\n" \
"ds_read_b128 v[68:71], v28 offset:32768\n" \
"v_mfma_f32_16x16x32_bf16 a[188:191], v[72:75], v[112:115], a[188:191]\n" \
"ds_read_b128 v[132:135], v29 offset:32768\n" \
"v_mfma_f32_16x16x32_bf16 a[188:191], v[136:139], v[176:179], a[188:191]\n" \
"s_add_u32 m0, s30, 0x1800\n" \
"s_mov_b32 s85, 0xc200\n" \
"v_mfma_f32_16x16x32_bf16 a[184:187], v[72:75], v[108:111], a[184:187]\n" \
"buffer_load_dwordx4 v22, s[64:67], s85 offen lds\n" \
"v_mfma_f32_16x16x32_bf16 a[184:187], v[136:139], v[172:175], a[184:187]\n" \
"ds_read_b64_tr_b16 v[154:155], v33 offset:41984\n" \
"v_mfma_f32_16x16x32_bf16 a[180:183], v[72:75], v[104:107], a[180:183]\n" \
"s_add_u32 m0, s30, 0x1c00\n" \
"s_mov_b32 s84, 0xe200\n" \
"v_mfma_f32_16x16x32_bf16 a[180:183], v[136:139], v[168:171], a[180:183]\n" \
"buffer_load_dwordx4 v23, s[64:67], s84 offen lds\n" \
"v_mfma_f32_16x16x32_bf16 a[176:179], v[72:75], v[100:103], a[176:179]\n" \
"ds_read_b64_tr_b16 v[92:93], v34 offset:32768\n" \
"v_mfma_f32_16x16x32_bf16 a[176:179], v[136:139], v[164:167], a[176:179]\n" \
"ds_read_b128 v[72:75], v28 offset:34816\n" \
"v_mfma_f32_16x16x32_bf16 a[208:211], v[76:79], v[100:103], a[208:211]\n" \
"ds_read_b128 v[136:139], v29 offset:34816\n" \
"v_mfma_f32_16x16x32_bf16 a[208:211], v[140:143], v[164:167], a[208:211]\n" \
"ds_read_b64_tr_b16 v[94:95], v34 offset:33792\n" \
"v_mfma_f32_16x16x32_bf16 a[212:215], v[76:79], v[104:107], a[212:215]\n" \
"ds_read_b64_tr_b16 v[156:157], v34 offset:40960\n" \
"v_mfma_f32_16x16x32_bf16 a[212:215], v[140:143], v[168:171], a[212:215]\n" \
"ds_read_b64_tr_b16 v[158:159], v34 offset:41984\n" \
"v_mfma_f32_16x16x32_bf16 a[216:219], v[76:79], v[108:111], a[216:219]\n" \
"ds_read_b64_tr_b16 v[96:97], v35 offset:32768\n" \
"v_mfma_f32_16x16x32_bf16 a[216:219], v[140:143], v[172:175], a[216:219]\n" \
"ds_read_b64_tr_b16 v[98:99], v35 offset:33792\n" \
"v_mfma_f32_16x16x32_bf16 a[220:223], v[76:79], v[112:115], a[220:223]\n" \
"ds_read_b64_tr_b16 v[160:161], v35 offset:40960\n" \
"v_mfma_f32_16x16x32_bf16 a[220:223], v[140:143], v[176:179], a[220:223]\n" \
"ds_read_b128 v[76:79], v28 offset:36864\n" \
"v_mfma_f32_16x16x32_bf16 a[252:255], v[80:83], v[112:115], a[252:255]\n" \
"ds_read_b128 v[140:143], v29 offset:36864\n" \
"v_mfma_f32_16x16x32_bf16 a[252:255], v[144:147], v[176:179], a[252:255]\n" \
"ds_read_b64_tr_b16 v[162:163], v35 offset:41984\n" \
"v_mfma_f32_16x16x32_bf16 a[248:251], v[80:83], v[108:111], a[248:251]\n" \
"s_cmp_lg_u32 s4, 0\n" \
"s_cselect_b32 s53, s41, s40\n" \
"v_mfma_f32_16x16x32_bf16 a[248:251], v[144:147], v[172:175], a[248:251]\n" \
"s_lshl_b32 s40, s2, 2\n" \
"s_and_b32 s40, s40, 28\n" \
"v_mfma_f32_16x16x32_bf16 a[244:247], v[80:83], v[104:107], a[244:247]\n" \
"s_bfe_u32 s45, s2, 0x20003\n" \
"s_lshr_b32 s44, s3, 1\n" \
"v_mfma_f32_16x16x32_bf16 a[244:247], v[144:147], v[168:171], a[244:247]\n" \
"s_or_b32 s46, s40, s45\n" \
"s_lshl_b32 s43, s44, 15\n" \
"v_mfma_f32_16x16x32_bf16 a[240:243], v[80:83], v[100:103], a[240:243]\n" \
"s_lshl_b32 s41, s5, 14\n" \
"s_lshl_b32 s48, s46, 20\n" \
"v_mfma_f32_16x16x32_bf16 a[240:243], v[144:147], v[164:167], a[240:243]\n" \
"ds_read_b128 v[80:83], v28 offset:38912\n" \
"ds_read_b128 v[144:147], v29 offset:38912\n" \
"s_waitcnt lgkmcnt(15)\n" \
"v_mfma_f32_16x16x32_bf16 a[0:3], v[52:55], v[84:87], a[0:3]\n" \
"s_add_u32 m0, s31, 0xc00\n" \
"s_mov_b32 s85, 0x218000\n" \
"v_mfma_f32_16x16x32_bf16 a[0:3], v[116:119], v[148:151], a[0:3]\n" \
"buffer_load_dwordx4 v27, s[68:71], s85 offen lds\n" \
"v_mfma_f32_16x16x32_bf16 a[4:7], v[52:55], v[88:91], a[4:7]\n" \
"ds_read_b64_tr_b16 v[100:101], v36 offset:32768\n" \
"s_or_b32 s41, s43, s41\n" \
"s_waitcnt lgkmcnt(15)\n" \
"v_mfma_f32_16x16x32_bf16 a[4:7], v[116:119], v[152:155], a[4:7]\n" \
"s_add_u32 m0, s31, 0x4c00\n" \
"s_mov_b32 s84, 0x218000\n" \
"s_waitcnt lgkmcnt(11)\n" \
"v_mfma_f32_16x16x32_bf16 a[8:11], v[52:55], v[92:95], a[8:11]\n" \
"buffer_load_dwordx4 v27, s[72:75], s84 offen lds\n" \
"s_waitcnt lgkmcnt(9)\n" \
"v_mfma_f32_16x16x32_bf16 a[8:11], v[116:119], v[156:159], a[8:11]\n" \
"ds_read_b64_tr_b16 v[102:103], v36 offset:33792\n" \
"s_add_i32 s49, s48, 0x1000000\n" \
"s_waitcnt lgkmcnt(8)\n" \
"v_mfma_f32_16x16x32_bf16 a[12:15], v[52:55], v[96:99], a[12:15]\n" \
"ds_read_b64_tr_b16 v[164:165], v36 offset:40960\n" \
"s_or_b32 s42, s49, s41\n" \
"s_waitcnt lgkmcnt(5)\n" \
"v_mfma_f32_16x16x32_bf16 a[12:15], v[116:119], v[160:163], a[12:15]\n" \
"s_lshl_b32 s4, s4, 16\n" \
"s_or_b32 s41, s42, s4\n" \
"v_mfma_f32_16x16x32_bf16 a[44:47], v[56:59], v[96:99], a[44:47]\n" \
"s_lshl_b32 s4, s6, 16\n" \
"s_or_b32 s42, s42, s4\n" \
"v_mfma_f32_16x16x32_bf16 a[44:47], v[120:123], v[160:163], a[44:47]\n" \
"ds_read_b64_tr_b16 v[166:167], v36 offset:41984\n" \
"s_lshl_b32 s4, s5, 11\n" \
"v_mfma_f32_16x16x32_bf16 a[40:43], v[56:59], v[92:95], a[40:43]\n" \
"s_mul_i32 s5, s7, 0x24800\n" \
"s_lshl_b32 s6, s46, 9\n" \
"v_mfma_f32_16x16x32_bf16 a[40:43], v[120:123], v[156:159], a[40:43]\n" \
"s_lshl_b32 s7, s40, 6\n" \
"s_add_i32 s6, s5, s6\n" \
"v_mfma_f32_16x16x32_bf16 a[36:39], v[56:59], v[88:91], a[36:39]\n" \
"s_or_b32 s5, s5, s7\n" \
"s_lshl_b32 s7, s45, 2\n" \
"v_mfma_f32_16x16x32_bf16 a[36:39], v[120:123], v[152:155], a[36:39]\n" \
"ds_read_b64_tr_b16 v[104:105], v37 offset:32768\n" \
"s_or_b32 s5, s5, s7\n" \
"v_mfma_f32_16x16x32_bf16 a[32:35], v[56:59], v[84:87], a[32:35]\n" \
"s_lshl_b32 s7, s3, 12\n" \
"s_or_b32 s50, s49, s7\n" \
"v_mfma_f32_16x16x32_bf16 a[32:35], v[120:123], v[148:151], a[32:35]\n" \
"s_lshl_b32 s7, s3, 11\n" \
"v_lshrrev_b32_e32 v215, 4, v20\n" \
"v_mfma_f32_16x16x32_bf16 a[64:67], v[60:63], v[84:87], a[64:67]\n" \
"s_waitcnt vmcnt(16)\n" \
"v_or_b32_e32 v204, v210, v204\n" \
"v_or_b32_e64 v206, v210, v206\n" \
"v_mfma_f32_16x16x32_bf16 a[64:67], v[124:127], v[148:151], a[64:67]\n" \
"v_mov_b32_e64 v210, 0\n" \
"s_add_u32 s88, s35, 0x1000\n" \
"v_mfma_f32_16x16x32_bf16 a[68:71], v[60:63], v[88:91], a[68:71]\n" \
"ds_read_b64_tr_b16 v[106:107], v37 offset:33792\n" \
"v_bfe_u32 v216, v20, 4, 3\n" \
"v_mfma_f32_16x16x32_bf16 a[68:71], v[124:127], v[152:155], a[68:71]\n" \
"ds_read_b64_tr_b16 v[168:169], v37 offset:40960\n" \
"s_add_i32 s52, s48, s7\n" \
"v_mfma_f32_16x16x32_bf16 a[72:75], v[60:63], v[92:95], a[72:75]\n" \
"s_mov_b32 m0, s32\n" \
"s_add_i32 s53, s53, s7\n" \
"v_mfma_f32_16x16x32_bf16 a[72:75], v[124:127], v[156:159], a[72:75]\n" \
"s_mov_b32 exec_lo, 0xffffffff\n" \
"s_mov_b32 exec_hi, 0x0\n" \
"global_load_lds_dword v[188:189], off\n" \
"s_mov_b64 exec, -1\n" \
"v_mfma_f32_16x16x32_bf16 a[76:79], v[60:63], v[96:99], a[76:79]\n" \
"ds_read_b64_tr_b16 v[170:171], v37 offset:41984\n" \
"v_mul_u32_u24_e32 v214, 0x480, v214\n" \
"v_mfma_f32_16x16x32_bf16 a[76:79], v[124:127], v[160:163], a[76:79]\n" \
"v_bitop3_b32 v218, v213, v215, 7 bitop3:0x78\n" \
"v_bitop3_b32 v213, v213, v216, 4 bitop3:0x36\n" \
"v_mfma_f32_16x16x32_bf16 a[108:111], v[64:67], v[96:99], a[108:111]\n" \
"ds_write_b128 v196, v[200:203]\n" \
"v_mfma_f32_16x16x32_bf16 a[108:111], v[128:131], v[160:163], a[108:111]\n" \
"s_lshl_b32 s7, s3, 10\n" \
"s_or_b32 s4, s43, s4\n" \
"v_mfma_f32_16x16x32_bf16 a[104:107], v[64:67], v[92:95], a[104:107]\n" \
"ds_read_b64_tr_b16 v[108:109], v38 offset:32768\n" \
"v_lshl_or_b32 v25, v213, 4, v214\n" \
"v_mfma_f32_16x16x32_bf16 a[104:107], v[128:131], v[156:159], a[104:107]\n" \
"v_mul_u32_u24_e32 v213, 0x480, v217\n" \
"v_bitop3_b32 v212, v215, v212, 7 bitop3:0x6c\n" \
"v_mfma_f32_16x16x32_bf16 a[100:103], v[64:67], v[88:91], a[100:103]\n" \
"s_or_b32 s40, s7, 0x14000\n" \
"s_lshl_b32 s43, s44, 13\n" \
"v_mfma_f32_16x16x32_bf16 a[100:103], v[128:131], v[152:155], a[100:103]\n" \
"ds_read_b64_tr_b16 v[110:111], v38 offset:33792\n" \
"s_mulk_i32 s44, 0xe800\n" \
"v_mfma_f32_16x16x32_bf16 a[96:99], v[64:67], v[84:87], a[96:99]\n" \
"s_lshl_b32 s7, s3, 8\n" \
"s_nop 0\n" \
"v_lshl_or_b32 v24, v218, 4, v214\n" \
"v_mfma_f32_16x16x32_bf16 a[96:99], v[128:131], v[148:151], a[96:99]\n" \
"v_lshl_or_b32 v21, v212, 4, v213\n" \
"s_add_i32 s44, s44, 0x17000\n" \
"v_mfma_f32_16x16x32_bf16 a[128:131], v[68:71], v[84:87], a[128:131]\n" \
"ds_write_b128 v197, v[204:207]\n" \
"v_mfma_f32_16x16x32_bf16 a[128:131], v[132:135], v[148:151], a[128:131]\n" \
"ds_read_b64_tr_b16 v[172:173], v38 offset:40960\n" \
"s_or_b32 s45, s7, 0x27000\n" \
"v_mfma_f32_16x16x32_bf16 a[132:135], v[68:71], v[88:91], a[132:135]\n" \
"s_or_b32 s46, s4, 0x10000\n" \
"s_add_i32 s48, s6, 0x3000000\n" \
"v_mfma_f32_16x16x32_bf16 a[132:135], v[132:135], v[152:155], a[132:135]\n" \
"ds_read_b64_tr_b16 v[174:175], v38 offset:41984\n" \
"s_add_i32 s49, s5, 0x3004000\n" \
"v_mfma_f32_16x16x32_bf16 a[136:139], v[68:71], v[92:95], a[136:139]\n" \
"s_add_i32 s52, s52, 0x1010000\n" \
"v_mfma_f32_16x16x32_bf16 a[136:139], v[132:135], v[156:159], a[136:139]\n" \
"v_mfma_f32_16x16x32_bf16 a[140:143], v[68:71], v[96:99], a[140:143]\n" \
"v_mfma_f32_16x16x32_bf16 a[140:143], v[132:135], v[160:163], a[140:143]\n" \
"ds_read_b64_tr_b16 v[112:113], v39 offset:32768\n" \
"v_mfma_f32_16x16x32_bf16 a[172:175], v[72:75], v[96:99], a[172:175]\n" \
"ds_write_b128 v198, v[208:211]\n" \
"v_mfma_f32_16x16x32_bf16 a[172:175], v[136:139], v[160:163], a[172:175]\n" \
"v_mfma_f32_16x16x32_bf16 a[168:171], v[72:75], v[92:95], a[168:171]\n" \
"ds_read_b64_tr_b16 v[114:115], v39 offset:33792\n" \
"v_mfma_f32_16x16x32_bf16 a[168:171], v[136:139], v[156:159], a[168:171]\n" \
"v_mfma_f32_16x16x32_bf16 a[164:167], v[72:75], v[88:91], a[164:167]\n" \
"v_mfma_f32_16x16x32_bf16 a[164:167], v[136:139], v[152:155], a[164:167]\n" \
"ds_read_b64_tr_b16 v[176:177], v39 offset:40960\n" \
"v_mfma_f32_16x16x32_bf16 a[160:163], v[72:75], v[84:87], a[160:163]\n" \
"v_mfma_f32_16x16x32_bf16 a[160:163], v[136:139], v[148:151], a[160:163]\n" \
"v_mfma_f32_16x16x32_bf16 a[192:195], v[76:79], v[84:87], a[192:195]\n" \
"ds_read_b64_tr_b16 v[178:179], v39 offset:41984\n" \
"v_mfma_f32_16x16x32_bf16 a[192:195], v[140:143], v[148:151], a[192:195]\n" \
"v_mfma_f32_16x16x32_bf16 a[196:199], v[76:79], v[88:91], a[196:199]\n" \
"v_mfma_f32_16x16x32_bf16 a[196:199], v[140:143], v[152:155], a[196:199]\n" \
"v_mfma_f32_16x16x32_bf16 a[200:203], v[76:79], v[92:95], a[200:203]\n" \
"v_mfma_f32_16x16x32_bf16 a[200:203], v[140:143], v[156:159], a[200:203]\n" \
"v_mfma_f32_16x16x32_bf16 a[204:207], v[76:79], v[96:99], a[204:207]\n" \
"v_mfma_f32_16x16x32_bf16 a[204:207], v[140:143], v[160:163], a[204:207]\n" \
"s_waitcnt lgkmcnt(15)\n" \
"v_mfma_f32_16x16x32_bf16 a[236:239], v[80:83], v[96:99], a[236:239]\n" \
"v_mfma_f32_16x16x32_bf16 a[236:239], v[144:147], v[160:163], a[236:239]\n" \
"v_mfma_f32_16x16x32_bf16 a[232:235], v[80:83], v[92:95], a[232:235]\n" \
"v_mfma_f32_16x16x32_bf16 a[232:235], v[144:147], v[156:159], a[232:235]\n" \
"v_mfma_f32_16x16x32_bf16 a[228:231], v[80:83], v[88:91], a[228:231]\n" \
"v_mfma_f32_16x16x32_bf16 a[228:231], v[144:147], v[152:155], a[228:231]\n" \
"v_mfma_f32_16x16x32_bf16 a[224:227], v[80:83], v[84:87], a[224:227]\n" \
"v_mfma_f32_16x16x32_bf16 a[224:227], v[144:147], v[148:151], a[224:227]\n" \
"s_waitcnt vmcnt(1) lgkmcnt(0)\n" \
"s_barrier\n" \
"s_nop 0\n" \
"v_mfma_f32_16x16x32_bf16 a[16:19], v[52:55], v[100:103], a[16:19]\n" \
"s_add_u32 m0, s30, 0x8000\n" \
"s_mov_b32 s85, 0x280\n" \
"v_mfma_f32_16x16x32_bf16 a[16:19], v[116:119], v[164:167], a[16:19]\n" \
"buffer_load_dwordx4 v22, s[64:67], s85 offen lds\n" \
"v_mfma_f32_16x16x32_bf16 a[20:23], v[52:55], v[104:107], a[20:23]\n" \
"s_add_u32 m0, s30, 0x8400\n" \
"s_mov_b32 s84, 0x2280\n" \
"v_mfma_f32_16x16x32_bf16 a[20:23], v[116:119], v[168:171], a[20:23]\n" \
"buffer_load_dwordx4 v23, s[64:67], s84 offen lds\n" \
"v_mfma_f32_16x16x32_bf16 a[24:27], v[52:55], v[108:111], a[24:27]\n" \
"ds_read_b64_tr_b16 v[84:85], v32 offset:0\n" \
"v_mfma_f32_16x16x32_bf16 a[24:27], v[116:119], v[172:175], a[24:27]\n" \
"s_add_u32 m0, s31, 0x8000\n" \
"s_mov_b32 s85, 0x280000\n" \
"v_mfma_f32_16x16x32_bf16 a[28:31], v[52:55], v[112:115], a[28:31]\n" \
"buffer_load_dwordx4 v26, s[68:71], s85 offen lds\n" \
"v_mfma_f32_16x16x32_bf16 a[28:31], v[116:119], v[176:179], a[28:31]\n" \
"ds_read_b128 v[52:55], v28 offset:16384\n" \
"v_mfma_f32_16x16x32_bf16 a[60:63], v[56:59], v[112:115], a[60:63]\n" \
"ds_read_b128 v[116:119], v29 offset:16384\n" \
"v_mfma_f32_16x16x32_bf16 a[60:63], v[120:123], v[176:179], a[60:63]\n" \
"s_add_u32 m0, s31, 0xc000\n" \
"s_mov_b32 s84, 0x280000\n" \
"v_mfma_f32_16x16x32_bf16 a[56:59], v[56:59], v[108:111], a[56:59]\n" \
"buffer_load_dwordx4 v26, s[72:75], s84 offen lds\n" \
"v_mfma_f32_16x16x32_bf16 a[56:59], v[120:123], v[172:175], a[56:59]\n" \
"s_add_u32 m0, s30, 0x8800\n" \
"s_mov_b32 s85, 0x4280\n" \
"v_mfma_f32_16x16x32_bf16 a[52:55], v[56:59], v[104:107], a[52:55]\n" \
"buffer_load_dwordx4 v22, s[64:67], s85 offen lds\n" \
"v_mfma_f32_16x16x32_bf16 a[52:55], v[120:123], v[168:171], a[52:55]\n" \
"ds_read_b64_tr_b16 v[86:87], v32 offset:1024\n" \
"v_mfma_f32_16x16x32_bf16 a[48:51], v[56:59], v[100:103], a[48:51]\n" \
"ds_read_b64_tr_b16 v[148:149], v32 offset:8192\n" \
"v_mfma_f32_16x16x32_bf16 a[48:51], v[120:123], v[164:167], a[48:51]\n" \
"ds_read_b128 v[56:59], v28 offset:18432\n" \
"v_mfma_f32_16x16x32_bf16 a[80:83], v[60:63], v[100:103], a[80:83]\n" \
"ds_read_b128 v[120:123], v29 offset:18432\n" \
"v_mfma_f32_16x16x32_bf16 a[80:83], v[124:127], v[164:167], a[80:83]\n" \
"s_add_u32 m0, s30, 0x8c00\n" \
"s_mov_b32 s84, 0x6280\n" \
"v_mfma_f32_16x16x32_bf16 a[84:87], v[60:63], v[104:107], a[84:87]\n" \
"buffer_load_dwordx4 v23, s[64:67], s84 offen lds\n" \
"v_mfma_f32_16x16x32_bf16 a[84:87], v[124:127], v[168:171], a[84:87]\n" \
"s_add_u32 m0, s31, 0x8400\n" \
"s_mov_b32 s85, 0x288000\n" \
"v_mfma_f32_16x16x32_bf16 a[88:91], v[60:63], v[108:111], a[88:91]\n" \
"buffer_load_dwordx4 v26, s[68:71], s85 offen lds\n" \
"v_mfma_f32_16x16x32_bf16 a[88:91], v[124:127], v[172:175], a[88:91]\n" \
"s_add_u32 m0, s31, 0xc400\n" \
"s_mov_b32 s84, 0x288000\n" \
"v_mfma_f32_16x16x32_bf16 a[92:95], v[60:63], v[112:115], a[92:95]\n" \
"buffer_load_dwordx4 v26, s[72:75], s84 offen lds\n" \
"v_mfma_f32_16x16x32_bf16 a[92:95], v[124:127], v[176:179], a[92:95]\n" \
"ds_read_b128 v[60:63], v28 offset:20480\n" \
"v_mfma_f32_16x16x32_bf16 a[124:127], v[64:67], v[112:115], a[124:127]\n" \
"ds_read_b128 v[124:127], v29 offset:20480\n" \
"v_mfma_f32_16x16x32_bf16 a[124:127], v[128:131], v[176:179], a[124:127]\n" \
"s_add_u32 m0, s30, 0x9000\n" \
"s_mov_b32 s85, 0x8280\n" \
"v_mfma_f32_16x16x32_bf16 a[120:123], v[64:67], v[108:111], a[120:123]\n" \
"buffer_load_dwordx4 v22, s[64:67], s85 offen lds\n" \
"v_mfma_f32_16x16x32_bf16 a[120:123], v[128:131], v[172:175], a[120:123]\n" \
"s_add_u32 m0, s30, 0x9400\n" \
"s_mov_b32 s84, 0xa280\n" \
"v_mfma_f32_16x16x32_bf16 a[116:119], v[64:67], v[104:107], a[116:119]\n" \
"buffer_load_dwordx4 v23, s[64:67], s84 offen lds\n" \
"v_mfma_f32_16x16x32_bf16 a[116:119], v[128:131], v[168:171], a[116:119]\n" \
"s_add_u32 m0, s31, 0x8800\n" \
"s_mov_b32 s85, 0x290000\n" \
"v_mfma_f32_16x16x32_bf16 a[112:115], v[64:67], v[100:103], a[112:115]\n" \
"buffer_load_dwordx4 v27, s[68:71], s85 offen lds\n" \
"v_mfma_f32_16x16x32_bf16 a[112:115], v[128:131], v[164:167], a[112:115]\n" \
"ds_read_b128 v[64:67], v28 offset:22528\n" \
"v_mfma_f32_16x16x32_bf16 a[144:147], v[68:71], v[100:103], a[144:147]\n" \
"ds_read_b128 v[128:131], v29 offset:22528\n" \
"v_mfma_f32_16x16x32_bf16 a[144:147], v[132:135], v[164:167], a[144:147]\n" \
"s_add_u32 m0, s31, 0xc800\n" \
"s_mov_b32 s84, 0x290000\n" \
"v_mfma_f32_16x16x32_bf16 a[148:151], v[68:71], v[104:107], a[148:151]\n" \
"buffer_load_dwordx4 v27, s[72:75], s84 offen lds\n" \
"v_mfma_f32_16x16x32_bf16 a[148:151], v[132:135], v[168:171], a[148:151]\n" \
"s_add_u32 m0, s30, 0x9800\n" \
"s_mov_b32 s85, 0xc280\n" \
"v_mfma_f32_16x16x32_bf16 a[152:155], v[68:71], v[108:111], a[152:155]\n" \
"buffer_load_dwordx4 v22, s[64:67], s85 offen lds\n" \
"v_mfma_f32_16x16x32_bf16 a[152:155], v[132:135], v[172:175], a[152:155]\n" \
"s_add_u32 m0, s30, 0x9c00\n" \
"s_mov_b32 s84, 0xe280\n" \
"v_mfma_f32_16x16x32_bf16 a[156:159], v[68:71], v[112:115], a[156:159]\n" \
"buffer_load_dwordx4 v23, s[64:67], s84 offen lds\n" \
"v_mfma_f32_16x16x32_bf16 a[156:159], v[132:135], v[176:179], a[156:159]\n" \
"ds_read_b128 v[68:71], v28 offset:0\n" \
"v_mfma_f32_16x16x32_bf16 a[188:191], v[72:75], v[112:115], a[188:191]\n" \
"ds_read_b128 v[132:135], v29 offset:0\n" \
"v_mfma_f32_16x16x32_bf16 a[188:191], v[136:139], v[176:179], a[188:191]\n" \
"ds_read_b128 v[200:203], v199 offset:0\n" \
"v_mfma_f32_16x16x32_bf16 a[184:187], v[72:75], v[108:111], a[184:187]\n" \
"ds_read_b128 v[204:207], v199 offset:4096\n" \
"v_mfma_f32_16x16x32_bf16 a[184:187], v[136:139], v[172:175], a[184:187]\n" \
"ds_read_b64_tr_b16 v[150:151], v32 offset:9216\n" \
"v_mfma_f32_16x16x32_bf16 a[180:183], v[72:75], v[104:107], a[180:183]\n" \
"ds_read_b64_tr_b16 v[88:89], v33 offset:0\n" \
"v_mfma_f32_16x16x32_bf16 a[180:183], v[136:139], v[168:171], a[180:183]\n" \
"ds_read_b128 v[208:211], v199 offset:8192\n" \
"v_mfma_f32_16x16x32_bf16 a[176:179], v[72:75], v[100:103], a[176:179]\n" \
".fill (L_pb + 57056 - .) / 4, 4, 0xBF800000\n" \
"L_pp14:\n" \
"s_cbranch_execz L_pret\n" \
"s_nop 0\n" \
"ds_read_b64_tr_b16 v[90:91], v33 offset:1024\n" \
"v_mfma_f32_16x16x32_bf16 a[176:179], v[136:139], v[164:167], a[176:179]\n" \
"ds_read_b128 v[72:75], v28 offset:2048\n" \
"v_mfma_f32_16x16x32_bf16 a[208:211], v[76:79], v[100:103], a[208:211]\n" \
"ds_read_b128 v[136:139], v29 offset:2048\n" \
"v_mfma_f32_16x16x32_bf16 a[208:211], v[140:143], v[164:167], a[208:211]\n" \
"ds_read_b64_tr_b16 v[152:153], v33 offset:8192\n" \
"v_mfma_f32_16x16x32_bf16 a[212:215], v[76:79], v[104:107], a[212:215]\n" \
"ds_read_b64_tr_b16 v[154:155], v33 offset:9216\n" \
"v_mfma_f32_16x16x32_bf16 a[212:215], v[140:143], v[168:171], a[212:215]\n" \
"ds_read_b64_tr_b16 v[92:93], v34 offset:0\n" \
"v_mfma_f32_16x16x32_bf16 a[216:219], v[76:79], v[108:111], a[216:219]\n" \
"ds_read_b64_tr_b16 v[94:95], v34 offset:1024\n" \
"v_mfma_f32_16x16x32_bf16 a[216:219], v[140:143], v[172:175], a[216:219]\n" \
"ds_read_b64_tr_b16 v[156:157], v34 offset:8192\n" \
"v_mfma_f32_16x16x32_bf16 a[220:223], v[76:79], v[112:115], a[220:223]\n" \
"ds_read_b64_tr_b16 v[158:159], v34 offset:9216\n" \
"v_mfma_f32_16x16x32_bf16 a[220:223], v[140:143], v[176:179], a[220:223]\n" \
"ds_read_b128 v[76:79], v28 offset:4096\n" \
"v_mfma_f32_16x16x32_bf16 a[252:255], v[80:83], v[112:115], a[252:255]\n" \
"ds_read_b128 v[140:143], v29 offset:4096\n" \
"v_mfma_f32_16x16x32_bf16 a[252:255], v[144:147], v[176:179], a[252:255]\n" \
"ds_read_b64_tr_b16 v[96:97], v35 offset:0\n" \
"v_mfma_f32_16x16x32_bf16 a[248:251], v[80:83], v[108:111], a[248:251]\n" \
"ds_read_b64_tr_b16 v[98:99], v35 offset:1024\n" \
"v_mfma_f32_16x16x32_bf16 a[248:251], v[144:147], v[172:175], a[248:251]\n" \
"ds_read_b64_tr_b16 v[160:161], v35 offset:8192\n" \
"v_mfma_f32_16x16x32_bf16 a[244:247], v[80:83], v[104:107], a[244:247]\n" \
"ds_read_b64_tr_b16 v[162:163], v35 offset:9216\n" \
"v_mfma_f32_16x16x32_bf16 a[244:247], v[144:147], v[168:171], a[244:247]\n" \
"v_mfma_f32_16x16x32_bf16 a[240:243], v[80:83], v[100:103], a[240:243]\n" \
"v_mfma_f32_16x16x32_bf16 a[240:243], v[144:147], v[164:167], a[240:243]\n" \
"ds_read_b128 v[80:83], v28 offset:6144\n" \
"ds_read_b128 v[144:147], v29 offset:6144\n" \
"s_waitcnt lgkmcnt(15)\n" \
"v_mfma_f32_16x16x32_bf16 a[0:3], v[52:55], v[84:87], a[0:3]\n" \
"s_add_u32 m0, s31, 0x8c00\n" \
"s_mov_b32 s85, 0x298000\n" \
"v_mfma_f32_16x16x32_bf16 a[0:3], v[116:119], v[148:151], a[0:3]\n" \
"buffer_load_dwordx4 v27, s[68:71], s85 offen lds\n" \
"v_mfma_f32_16x16x32_bf16 a[4:7], v[52:55], v[88:91], a[4:7]\n" \
"ds_read_b64_tr_b16 v[100:101], v36 offset:0\n" \
"s_waitcnt lgkmcnt(13)\n" \
"v_mfma_f32_16x16x32_bf16 a[4:7], v[116:119], v[152:155], a[4:7]\n" \
"s_add_u32 m0, s31, 0xcc00\n" \
"s_mov_b32 s84, 0x298000\n" \
"s_waitcnt lgkmcnt(11)\n" \
"v_mfma_f32_16x16x32_bf16 a[8:11], v[52:55], v[92:95], a[8:11]\n" \
"buffer_load_dwordx4 v27, s[72:75], s84 offen lds\n" \
"s_waitcnt lgkmcnt(9)\n" \
"v_mfma_f32_16x16x32_bf16 a[8:11], v[116:119], v[156:159], a[8:11]\n" \
"ds_read_b64_tr_b16 v[102:103], v36 offset:1024\n" \
"s_waitcnt lgkmcnt(6)\n" \
"v_mfma_f32_16x16x32_bf16 a[12:15], v[52:55], v[96:99], a[12:15]\n" \
"ds_read_b64_tr_b16 v[164:165], v36 offset:8192\n" \
"s_waitcnt lgkmcnt(5)\n" \
"v_mfma_f32_16x16x32_bf16 a[12:15], v[116:119], v[160:163], a[12:15]\n" \
"v_mfma_f32_16x16x32_bf16 a[44:47], v[56:59], v[96:99], a[44:47]\n" \
"v_mfma_f32_16x16x32_bf16 a[44:47], v[120:123], v[160:163], a[44:47]\n" \
"ds_read_b64_tr_b16 v[166:167], v36 offset:9216\n" \
"v_mfma_f32_16x16x32_bf16 a[40:43], v[56:59], v[92:95], a[40:43]\n" \
"v_mfma_f32_16x16x32_bf16 a[40:43], v[120:123], v[156:159], a[40:43]\n" \
"v_mfma_f32_16x16x32_bf16 a[36:39], v[56:59], v[88:91], a[36:39]\n" \
"v_mfma_f32_16x16x32_bf16 a[36:39], v[120:123], v[152:155], a[36:39]\n" \
"ds_read_b64_tr_b16 v[104:105], v37 offset:0\n" \
"v_mfma_f32_16x16x32_bf16 a[32:35], v[56:59], v[84:87], a[32:35]\n" \
"v_mfma_f32_16x16x32_bf16 a[32:35], v[120:123], v[148:151], a[32:35]\n" \
"v_mfma_f32_16x16x32_bf16 a[64:67], v[60:63], v[84:87], a[64:67]\n" \
"ds_read_b64_tr_b16 v[106:107], v37 offset:1024\n" \
"v_mfma_f32_16x16x32_bf16 a[64:67], v[124:127], v[148:151], a[64:67]\n" \
"v_mfma_f32_16x16x32_bf16 a[68:71], v[60:63], v[88:91], a[68:71]\n" \
"v_mfma_f32_16x16x32_bf16 a[68:71], v[124:127], v[152:155], a[68:71]\n" \
"buffer_store_dwordx4 v[200:203], v20, s[80:83], s35 offen sc1\n" \
"v_mfma_f32_16x16x32_bf16 a[72:75], v[60:63], v[92:95], a[72:75]\n" \
"s_mov_b32 m0, s32\n" \
"ds_read_b64_tr_b16 v[168:169], v37 offset:8192\n" \
"v_mfma_f32_16x16x32_bf16 a[72:75], v[124:127], v[156:159], a[72:75]\n" \
"s_mov_b32 exec_lo, 0x0\n" \
"s_mov_b32 exec_hi, 0xffffffff\n" \
"global_load_lds_dword v[188:189], off\n" \
"s_mov_b64 exec, -1\n" \
"v_mfma_f32_16x16x32_bf16 a[76:79], v[60:63], v[96:99], a[76:79]\n" \
"ds_read_b64_tr_b16 v[170:171], v37 offset:9216\n" \
"v_mfma_f32_16x16x32_bf16 a[76:79], v[124:127], v[160:163], a[76:79]\n" \
"v_mfma_f32_16x16x32_bf16 a[108:111], v[64:67], v[96:99], a[108:111]\n" \
"v_mfma_f32_16x16x32_bf16 a[108:111], v[128:131], v[160:163], a[108:111]\n" \
"v_mfma_f32_16x16x32_bf16 a[104:107], v[64:67], v[92:95], a[104:107]\n" \
"ds_read_b64_tr_b16 v[108:109], v38 offset:0\n" \
"v_mfma_f32_16x16x32_bf16 a[104:107], v[128:131], v[156:159], a[104:107]\n" \
"v_mfma_f32_16x16x32_bf16 a[100:103], v[64:67], v[88:91], a[100:103]\n" \
"buffer_store_dwordx4 v[204:207], v20, s[80:83], s88 offen sc1\n" \
"v_mfma_f32_16x16x32_bf16 a[100:103], v[128:131], v[152:155], a[100:103]\n" \
"ds_read_b64_tr_b16 v[110:111], v38 offset:1024\n" \
"v_mfma_f32_16x16x32_bf16 a[96:99], v[64:67], v[84:87], a[96:99]\n" \
"v_mfma_f32_16x16x32_bf16 a[96:99], v[128:131], v[148:151], a[96:99]\n" \
"v_mfma_f32_16x16x32_bf16 a[128:131], v[68:71], v[84:87], a[128:131]\n" \
"ds_read_b64_tr_b16 v[172:173], v38 offset:8192\n" \
"v_mfma_f32_16x16x32_bf16 a[128:131], v[132:135], v[148:151], a[128:131]\n" \
"v_mfma_f32_16x16x32_bf16 a[132:135], v[68:71], v[88:91], a[132:135]\n" \
"v_mfma_f32_16x16x32_bf16 a[132:135], v[132:135], v[152:155], a[132:135]\n" \
"ds_read_b64_tr_b16 v[174:175], v38 offset:9216\n" \
"v_mfma_f32_16x16x32_bf16 a[136:139], v[68:71], v[92:95], a[136:139]\n" \
"v_mfma_f32_16x16x32_bf16 a[136:139], v[132:135], v[156:159], a[136:139]\n" \
"v_mfma_f32_16x16x32_bf16 a[140:143], v[68:71], v[96:99], a[140:143]\n" \
"buffer_store_dwordx4 v[208:211], v20, s[80:83], s36 offen sc1\n" \
"v_mfma_f32_16x16x32_bf16 a[140:143], v[132:135], v[160:163], a[140:143]\n" \
"ds_read_b64_tr_b16 v[112:113], v39 offset:0\n" \
"v_mfma_f32_16x16x32_bf16 a[172:175], v[72:75], v[96:99], a[172:175]\n" \
"v_mfma_f32_16x16x32_bf16 a[172:175], v[136:139], v[160:163], a[172:175]\n" \
"v_mfma_f32_16x16x32_bf16 a[168:171], v[72:75], v[92:95], a[168:171]\n" \
"ds_read_b64_tr_b16 v[114:115], v39 offset:1024\n" \
"v_mfma_f32_16x16x32_bf16 a[168:171], v[136:139], v[156:159], a[168:171]\n" \
"v_mfma_f32_16x16x32_bf16 a[164:167], v[72:75], v[88:91], a[164:167]\n" \
"v_mfma_f32_16x16x32_bf16 a[164:167], v[136:139], v[152:155], a[164:167]\n" \
"ds_read_b64_tr_b16 v[176:177], v39 offset:8192\n" \
"v_mfma_f32_16x16x32_bf16 a[160:163], v[72:75], v[84:87], a[160:163]\n" \
"v_mfma_f32_16x16x32_bf16 a[160:163], v[136:139], v[148:151], a[160:163]\n" \
"v_mfma_f32_16x16x32_bf16 a[192:195], v[76:79], v[84:87], a[192:195]\n" \
"ds_read_b64_tr_b16 v[178:179], v39 offset:9216\n" \
"v_mfma_f32_16x16x32_bf16 a[192:195], v[140:143], v[148:151], a[192:195]\n" \
"v_mfma_f32_16x16x32_bf16 a[196:199], v[76:79], v[88:91], a[196:199]\n" \
"v_mfma_f32_16x16x32_bf16 a[196:199], v[140:143], v[152:155], a[196:199]\n" \
"v_mfma_f32_16x16x32_bf16 a[200:203], v[76:79], v[92:95], a[200:203]\n" \
"v_mfma_f32_16x16x32_bf16 a[200:203], v[140:143], v[156:159], a[200:203]\n" \
"v_mfma_f32_16x16x32_bf16 a[204:207], v[76:79], v[96:99], a[204:207]\n" \
"v_mfma_f32_16x16x32_bf16 a[204:207], v[140:143], v[160:163], a[204:207]\n" \
"s_waitcnt lgkmcnt(15)\n" \
"v_mfma_f32_16x16x32_bf16 a[236:239], v[80:83], v[96:99], a[236:239]\n" \
"v_mfma_f32_16x16x32_bf16 a[236:239], v[144:147], v[160:163], a[236:239]\n" \
"v_mfma_f32_16x16x32_bf16 a[232:235], v[80:83], v[92:95], a[232:235]\n" \
"v_mfma_f32_16x16x32_bf16 a[232:235], v[144:147], v[156:159], a[232:235]\n" \
"v_mfma_f32_16x16x32_bf16 a[228:231], v[80:83], v[88:91], a[228:231]\n" \
"v_mfma_f32_16x16x32_bf16 a[228:231], v[144:147], v[152:155], a[228:231]\n" \
"v_mfma_f32_16x16x32_bf16 a[224:227], v[80:83], v[84:87], a[224:227]\n" \
"v_mfma_f32_16x16x32_bf16 a[224:227], v[144:147], v[148:151], a[224:227]\n" \
"s_waitcnt vmcnt(4) lgkmcnt(0)\n" \
"s_barrier\n" \
"v_mfma_f32_16x16x32_bf16 a[16:19], v[52:55], v[100:103], a[16:19]\n" \
"s_add_u32 m0, s30, 0x0\n" \
"s_mov_b32 s85, 0x300\n" \
"v_mfma_f32_16x16x32_bf16 a[16:19], v[116:119], v[164:167], a[16:19]\n" \
"buffer_load_dwordx4 v22, s[64:67], s85 offen lds\n" \
"v_mfma_f32_16x16x32_bf16 a[20:23], v[52:55], v[104:107], a[20:23]\n" \
"ds_read_b64_tr_b16 v[84:85], v32 offset:32768\n" \
"v_mfma_f32_16x16x32_bf16 a[20:23], v[116:119], v[168:171], a[20:23]\n" \
"s_add_u32 m0, s30, 0x400\n" \
"s_mov_b32 s84, 0x2300\n" \
"v_mfma_f32_16x16x32_bf16 a[24:27], v[52:55], v[108:111], a[24:27]\n" \
"buffer_load_dwordx4 v23, s[64:67], s84 offen lds\n" \
"v_mfma_f32_16x16x32_bf16 a[24:27], v[116:119], v[172:175], a[24:27]\n" \
"ds_read_b64_tr_b16 v[86:87], v32 offset:33792\n" \
"v_mfma_f32_16x16x32_bf16 a[28:31], v[52:55], v[112:115], a[28:31]\n" \
"ds_read_b64_tr_b16 v[148:149], v32 offset:40960\n" \
"v_mfma_f32_16x16x32_bf16 a[28:31], v[116:119], v[176:179], a[28:31]\n" \
"ds_read_b128 v[52:55], v28 offset:49152\n" \
"v_mfma_f32_16x16x32_bf16 a[60:63], v[56:59], v[112:115], a[60:63]\n" \
"ds_read_b128 v[116:119], v29 offset:49152\n" \
"v_mfma_f32_16x16x32_bf16 a[60:63], v[120:123], v[176:179], a[60:63]\n" \
"s_add_u32 m0, s31, 0x0\n" \
"s_nop 0\n" \
"s_mov_b32 s85, 0x300000\n" \
"v_mfma_f32_16x16x32_bf16 a[56:59], v[56:59], v[108:111], a[56:59]\n" \
"buffer_load_dwordx4 v26, s[68:71], s85 offen lds\n" \
"v_mfma_f32_16x16x32_bf16 a[56:59], v[120:123], v[172:175], a[56:59]\n" \
"s_add_u32 m0, s31, 0x4000\n" \
"s_mov_b32 s84, 0x300000\n" \
"v_mfma_f32_16x16x32_bf16 a[52:55], v[56:59], v[104:107], a[52:55]\n" \
"buffer_load_dwordx4 v26, s[72:75], s84 offen lds\n" \
"v_mfma_f32_16x16x32_bf16 a[52:55], v[120:123], v[168:171], a[52:55]\n" \
"s_add_u32 m0, s30, 0x800\n" \
"s_mov_b32 s85, 0x4300\n" \
"v_mfma_f32_16x16x32_bf16 a[48:51], v[56:59], v[100:103], a[48:51]\n" \
"buffer_load_dwordx4 v22, s[64:67], s85 offen lds\n" \
"v_mfma_f32_16x16x32_bf16 a[48:51], v[120:123], v[164:167], a[48:51]\n" \
"ds_read_b128 v[56:59], v28 offset:51200\n" \
"v_mfma_f32_16x16x32_bf16 a[80:83], v[60:63], v[100:103], a[80:83]\n" \
"ds_read_b128 v[120:123], v29 offset:51200\n" \
"v_mfma_f32_16x16x32_bf16 a[80:83], v[124:127], v[164:167], a[80:83]\n" \
"s_add_u32 m0, s30, 0xc00\n" \
"s_mov_b32 s84, 0x6300\n" \
"v_mfma_f32_16x16x32_bf16 a[84:87], v[60:63], v[104:107], a[84:87]\n" \
"buffer_load_dwordx4 v23, s[64:67], s84 offen lds\n" \
"v_mfma_f32_16x16x32_bf16 a[84:87], v[124:127], v[168:171], a[84:87]\n" \
"ds_read_b64_tr_b16 v[150:151], v32 offset:41984\n" \
"v_mfma_f32_16x16x32_bf16 a[88:91], v[60:63], v[108:111], a[88:91]\n" \
"s_add_u32 m0, s31, 0x400\n" \
"s_mov_b32 s85, 0x308000\n" \
"v_mfma_f32_16x16x32_bf16 a[88:91], v[124:127], v[172:175], a[88:91]\n" \
"buffer_load_dwordx4 v26, s[68:71], s85 offen lds\n" \
"v_mfma_f32_16x16x32_bf16 a[92:95], v[60:63], v[112:115], a[92:95]\n" \
"ds_read_b64_tr_b16 v[88:89], v33 offset:32768\n" \
"v_mfma_f32_16x16x32_bf16 a[92:95], v[124:127], v[176:179], a[92:95]\n" \
"ds_read_b128 v[60:63], v28 offset:53248\n" \
"v_mfma_f32_16x16x32_bf16 a[124:127], v[64:67], v[112:115], a[124:127]\n" \
"ds_read_b128 v[124:127], v29 offset:53248\n" \
"v_mfma_f32_16x16x32_bf16 a[124:127], v[128:131], v[176:179], a[124:127]\n" \
"s_add_u32 m0, s31, 0x4400\n" \
"s_mov_b32 s84, 0x308000\n" \
"v_mfma_f32_16x16x32_bf16 a[120:123], v[64:67], v[108:111], a[120:123]\n" \
"buffer_load_dwordx4 v26, s[72:75], s84 offen lds\n" \
"v_mfma_f32_16x16x32_bf16 a[120:123], v[128:131], v[172:175], a[120:123]\n" \
"s_add_u32 m0, s30, 0x1000\n" \
"s_mov_b32 s85, 0x8300\n" \
"v_mfma_f32_16x16x32_bf16 a[116:119], v[64:67], v[104:107], a[116:119]\n" \
"buffer_load_dwordx4 v22, s[64:67], s85 offen lds\n" \
"v_mfma_f32_16x16x32_bf16 a[116:119], v[128:131], v[168:171], a[116:119]\n" \
"ds_read_b64_tr_b16 v[90:91], v33 offset:33792\n" \
"v_mfma_f32_16x16x32_bf16 a[112:115], v[64:67], v[100:103], a[112:115]\n" \
"ds_read_b64_tr_b16 v[152:153], v33 offset:40960\n" \
"v_mfma_f32_16x16x32_bf16 a[112:115], v[128:131], v[164:167], a[112:115]\n" \
"ds_read_b128 v[64:67], v28 offset:55296\n" \
"v_mfma_f32_16x16x32_bf16 a[144:147], v[68:71], v[100:103], a[144:147]\n" \
"ds_read_b128 v[128:131], v29 offset:55296\n" \
"v_mfma_f32_16x16x32_bf16 a[144:147], v[132:135], v[164:167], a[144:147]\n" \
"s_add_u32 m0, s30, 0x1400\n" \
"s_mov_b32 s84, 0xa300\n" \
"v_mfma_f32_16x16x32_bf16 a[148:151], v[68:71], v[104:107], a[148:151]\n" \
"buffer_load_dwordx4 v23, s[64:67], s84 offen lds\n" \
"v_mfma_f32_16x16x32_bf16 a[148:151], v[132:135], v[168:171], a[148:151]\n" \
"s_add_u32 m0, s31, 0x800\n" \
"s_mov_b32 s85, 0x310000\n" \
"v_mfma_f32_16x16x32_bf16 a[152:155], v[68:71], v[108:111], a[152:155]\n" \
"buffer_load_dwordx4 v27, s[68:71], s85 offen lds\n" \
"v_mfma_f32_16x16x32_bf16 a[152:155], v[132:135], v[172:175], a[152:155]\n" \
"s_add_u32 m0, s31, 0x4800\n" \
"s_mov_b32 s84, 0x310000\n" \
"v_mfma_f32_16x16x32_bf16 a[156:159], v[68:71], v[112:115], a[156:159]\n" \
"buffer_load_dwordx4 v27, s[72:75], s84 offen lds\n" \
"v_mfma_f32_16x16x32_bf16 a[156:159], v[132:135], v[176:179], a[156:159]\n" \
"ds_read_b128 v[68:71], v28 offset:32768\n" \
"v_mfma_f32_16x16x32_bf16 a[188:191], v[72:75], v[112:115], a[188:191]\n" \
"ds_read_b128 v[132:135], v29 offset:32768\n" \
"v_mfma_f32_16x16x32_bf16 a[188:191], v[136:139], v[176:179], a[188:191]\n" \
"s_add_u32 m0, s30, 0x1800\n" \
"s_mov_b32 s85, 0xc300\n" \
"v_mfma_f32_16x16x32_bf16 a[184:187], v[72:75], v[108:111], a[184:187]\n" \
"buffer_load_dwordx4 v22, s[64:67], s85 offen lds\n" \
"v_mfma_f32_16x16x32_bf16 a[184:187], v[136:139], v[172:175], a[184:187]\n" \
"ds_read_b64_tr_b16 v[154:155], v33 offset:41984\n" \
"v_mfma_f32_16x16x32_bf16 a[180:183], v[72:75], v[104:107], a[180:183]\n" \
"s_add_u32 m0, s30, 0x1c00\n" \
"s_mov_b32 s84, 0xe300\n" \
"v_mfma_f32_16x16x32_bf16 a[180:183], v[136:139], v[168:171], a[180:183]\n" \
"buffer_load_dwordx4 v23, s[64:67], s84 offen lds\n" \
"v_mfma_f32_16x16x32_bf16 a[176:179], v[72:75], v[100:103], a[176:179]\n" \
"ds_read_b64_tr_b16 v[92:93], v34 offset:32768\n" \
"v_mfma_f32_16x16x32_bf16 a[176:179], v[136:139], v[164:167], a[176:179]\n" \
"ds_read_b128 v[72:75], v28 offset:34816\n" \
"v_mfma_f32_16x16x32_bf16 a[208:211], v[76:79], v[100:103], a[208:211]\n" \
"ds_read_b128 v[136:139], v29 offset:34816\n" \
"v_mfma_f32_16x16x32_bf16 a[208:211], v[140:143], v[164:167], a[208:211]\n" \
"ds_read_b64_tr_b16 v[94:95], v34 offset:33792\n" \
"v_mfma_f32_16x16x32_bf16 a[212:215], v[76:79], v[104:107], a[212:215]\n" \
"ds_read_b64_tr_b16 v[156:157], v34 offset:40960\n" \
"v_mfma_f32_16x16x32_bf16 a[212:215], v[140:143], v[168:171], a[212:215]\n" \
"ds_read_b64_tr_b16 v[158:159], v34 offset:41984\n" \
"v_mfma_f32_16x16x32_bf16 a[216:219], v[76:79], v[108:111], a[216:219]\n" \
"ds_read_b64_tr_b16 v[96:97], v35 offset:32768\n" \
"v_mfma_f32_16x16x32_bf16 a[216:219], v[140:143], v[172:175], a[216:219]\n" \
"ds_read_b64_tr_b16 v[98:99], v35 offset:33792\n" \
"v_mfma_f32_16x16x32_bf16 a[220:223], v[76:79], v[112:115], a[220:223]\n" \
"ds_read_b64_tr_b16 v[160:161], v35 offset:40960\n" \
"v_mfma_f32_16x16x32_bf16 a[220:223], v[140:143], v[176:179], a[220:223]\n" \
"ds_read_b128 v[76:79], v28 offset:36864\n" \
"v_mfma_f32_16x16x32_bf16 a[252:255], v[80:83], v[112:115], a[252:255]\n" \
"ds_read_b128 v[140:143], v29 offset:36864\n" \
"v_mfma_f32_16x16x32_bf16 a[252:255], v[144:147], v[176:179], a[252:255]\n" \
"ds_read_b64_tr_b16 v[162:163], v35 offset:41984\n" \
"v_mfma_f32_16x16x32_bf16 a[248:251], v[80:83], v[108:111], a[248:251]\n" \
"v_mfma_f32_16x16x32_bf16 a[248:251], v[144:147], v[172:175], a[248:251]\n" \
"v_mfma_f32_16x16x32_bf16 a[244:247], v[80:83], v[104:107], a[244:247]\n" \
"v_mfma_f32_16x16x32_bf16 a[244:247], v[144:147], v[168:171], a[244:247]\n" \
"v_mfma_f32_16x16x32_bf16 a[240:243], v[80:83], v[100:103], a[240:243]\n" \
"v_mfma_f32_16x16x32_bf16 a[240:243], v[144:147], v[164:167], a[240:243]\n" \
"ds_read_b128 v[80:83], v28 offset:38912\n" \
"ds_read_b128 v[144:147], v29 offset:38912\n" \
"s_waitcnt lgkmcnt(15)\n" \
"v_mfma_f32_16x16x32_bf16 a[0:3], v[52:55], v[84:87], a[0:3]\n" \
"s_add_u32 m0, s31, 0xc00\n" \
"s_mov_b32 s85, 0x318000\n" \
"v_mfma_f32_16x16x32_bf16 a[0:3], v[116:119], v[148:151], a[0:3]\n" \
"buffer_load_dwordx4 v27, s[68:71], s85 offen lds\n" \
"v_mfma_f32_16x16x32_bf16 a[4:7], v[52:55], v[88:91], a[4:7]\n" \
"ds_read_b64_tr_b16 v[100:101], v36 offset:32768\n" \
"s_waitcnt lgkmcnt(15)\n" \
"v_mfma_f32_16x16x32_bf16 a[4:7], v[116:119], v[152:155], a[4:7]\n" \
"s_add_u32 m0, s31, 0x4c00\n" \
"s_mov_b32 s84, 0x318000\n" \
"s_waitcnt lgkmcnt(11)\n" \
"v_mfma_f32_16x16x32_bf16 a[8:11], v[52:55], v[92:95], a[8:11]\n" \
"buffer_load_dwordx4 v27, s[72:75], s84 offen lds\n" \
"s_waitcnt lgkmcnt(9)\n" \
"v_mfma_f32_16x16x32_bf16 a[8:11], v[116:119], v[156:159], a[8:11]\n" \
"ds_read_b64_tr_b16 v[102:103], v36 offset:33792\n" \
"s_waitcnt lgkmcnt(8)\n" \
"v_mfma_f32_16x16x32_bf16 a[12:15], v[52:55], v[96:99], a[12:15]\n" \
"ds_read_b64_tr_b16 v[164:165], v36 offset:40960\n" \
"s_waitcnt lgkmcnt(5)\n" \
"v_mfma_f32_16x16x32_bf16 a[12:15], v[116:119], v[160:163], a[12:15]\n" \
"v_mfma_f32_16x16x32_bf16 a[44:47], v[56:59], v[96:99], a[44:47]\n" \
"v_mfma_f32_16x16x32_bf16 a[44:47], v[120:123], v[160:163], a[44:47]\n" \
"ds_read_b64_tr_b16 v[166:167], v36 offset:41984\n" \
"v_mfma_f32_16x16x32_bf16 a[40:43], v[56:59], v[92:95], a[40:43]\n" \
"v_mfma_f32_16x16x32_bf16 a[40:43], v[120:123], v[156:159], a[40:43]\n" \
"v_mfma_f32_16x16x32_bf16 a[36:39], v[56:59], v[88:91], a[36:39]\n" \
"v_mfma_f32_16x16x32_bf16 a[36:39], v[120:123], v[152:155], a[36:39]\n" \
"ds_read_b64_tr_b16 v[104:105], v37 offset:32768\n" \
"v_mfma_f32_16x16x32_bf16 a[32:35], v[56:59], v[84:87], a[32:35]\n" \
"v_mfma_f32_16x16x32_bf16 a[32:35], v[120:123], v[148:151], a[32:35]\n" \
"v_mfma_f32_16x16x32_bf16 a[64:67], v[60:63], v[84:87], a[64:67]\n" \
"ds_read_b64_tr_b16 v[106:107], v37 offset:33792\n" \
"v_mfma_f32_16x16x32_bf16 a[64:67], v[124:127], v[148:151], a[64:67]\n" \
"v_mfma_f32_16x16x32_bf16 a[68:71], v[60:63], v[88:91], a[68:71]\n" \
"v_lshlrev_b32_e32 v45, 3, v20\n" \
"v_add_u32_e32 v45, s31, v45\n" \
"v_mfma_f32_16x16x32_bf16 a[68:71], v[124:127], v[152:155], a[68:71]\n" \
"s_getpc_b64 s[86:87]\n" \
"s_sub_u32 s86, s86, 0x10800\n" \
"s_subb_u32 s87, s87, 0\n" \
"s_mov_b32 m0, s32\n" \
"s_nop 0\n" \
"v_mfma_f32_16x16x32_bf16 a[72:75], v[60:63], v[92:95], a[72:75]\n" \
"global_load_lds_dword v45, s[86:87]\n" \
"v_mfma_f32_16x16x32_bf16 a[72:75], v[124:127], v[156:159], a[72:75]\n" \
"ds_read_b64_tr_b16 v[168:169], v37 offset:40960\n" \
"v_mfma_f32_16x16x32_bf16 a[76:79], v[60:63], v[96:99], a[76:79]\n" \
"ds_read_b64_tr_b16 v[170:171], v37 offset:41984\n" \
"v_mfma_f32_16x16x32_bf16 a[76:79], v[124:127], v[160:163], a[76:79]\n" \
"v_mfma_f32_16x16x32_bf16 a[108:111], v[64:67], v[96:99], a[108:111]\n" \
"v_mfma_f32_16x16x32_bf16 a[108:111], v[128:131], v[160:163], a[108:111]\n" \
"v_mfma_f32_16x16x32_bf16 a[104:107], v[64:67], v[92:95], a[104:107]\n" \
"ds_read_b64_tr_b16 v[108:109], v38 offset:32768\n" \
"v_mfma_f32_16x16x32_bf16 a[104:107], v[128:131], v[156:159], a[104:107]\n" \
"v_mfma_f32_16x16x32_bf16 a[100:103], v[64:67], v[88:91], a[100:103]\n" \
"v_mfma_f32_16x16x32_bf16 a[100:103], v[128:131], v[152:155], a[100:103]\n" \
"ds_read_b64_tr_b16 v[110:111], v38 offset:33792\n" \
"v_mfma_f32_16x16x32_bf16 a[96:99], v[64:67], v[84:87], a[96:99]\n" \
"v_mfma_f32_16x16x32_bf16 a[96:99], v[128:131], v[148:151], a[96:99]\n" \
"v_mfma_f32_16x16x32_bf16 a[128:131], v[68:71], v[84:87], a[128:131]\n" \
"ds_read_b64_tr_b16 v[172:173], v38 offset:40960\n" \
"v_mfma_f32_16x16x32_bf16 a[128:131], v[132:135], v[148:151], a[128:131]\n" \
"v_mfma_f32_16x16x32_bf16 a[132:135], v[68:71], v[88:91], a[132:135]\n" \
"v_mfma_f32_16x16x32_bf16 a[132:135], v[132:135], v[152:155], a[132:135]\n" \
"ds_read_b64_tr_b16 v[174:175], v38 offset:41984\n" \
"v_mfma_f32_16x16x32_bf16 a[136:139], v[68:71], v[92:95], a[136:139]\n" \
"v_mfma_f32_16x16x32_bf16 a[136:139], v[132:135], v[156:159], a[136:139]\n" \
"v_mfma_f32_16x16x32_bf16 a[140:143], v[68:71], v[96:99], a[140:143]\n" \
"v_mfma_f32_16x16x32_bf16 a[140:143], v[132:135], v[160:163], a[140:143]\n" \
"ds_read_b64_tr_b16 v[112:113], v39 offset:32768\n" \
"v_mfma_f32_16x16x32_bf16 a[172:175], v[72:75], v[96:99], a[172:175]\n" \
"v_mfma_f32_16x16x32_bf16 a[172:175], v[136:139], v[160:163], a[172:175]\n" \
"v_mfma_f32_16x16x32_bf16 a[168:171], v[72:75], v[92:95], a[168:171]\n" \
"ds_read_b64_tr_b16 v[114:115], v39 offset:33792\n" \
"v_mfma_f32_16x16x32_bf16 a[168:171], v[136:139], v[156:159], a[168:171]\n" \
"v_mfma_f32_16x16x32_bf16 a[164:167], v[72:75], v[88:91], a[164:167]\n" \
"v_mfma_f32_16x16x32_bf16 a[164:167], v[136:139], v[152:155], a[164:167]\n" \
"ds_read_b64_tr_b16 v[176:177], v39 offset:40960\n" \
"v_mfma_f32_16x16x32_bf16 a[160:163], v[72:75], v[84:87], a[160:163]\n" \
"v_mfma_f32_16x16x32_bf16 a[160:163], v[136:139], v[148:151], a[160:163]\n" \
"v_mfma_f32_16x16x32_bf16 a[192:195], v[76:79], v[84:87], a[192:195]\n" \
"ds_read_b64_tr_b16 v[178:179], v39 offset:41984\n" \
"v_mfma_f32_16x16x32_bf16 a[192:195], v[140:143], v[148:151], a[192:195]\n" \
"v_mfma_f32_16x16x32_bf16 a[196:199], v[76:79], v[88:91], a[196:199]\n" \
"v_mfma_f32_16x16x32_bf16 a[196:199], v[140:143], v[152:155], a[196:199]\n" \
"v_mfma_f32_16x16x32_bf16 a[200:203], v[76:79], v[92:95], a[200:203]\n" \
"v_mfma_f32_16x16x32_bf16 a[200:203], v[140:143], v[156:159], a[200:203]\n" \
"v_mfma_f32_16x16x32_bf16 a[204:207], v[76:79], v[96:99], a[204:207]\n" \
"v_mfma_f32_16x16x32_bf16 a[204:207], v[140:143], v[160:163], a[204:207]\n" \
"s_waitcnt lgkmcnt(15)\n" \
"v_mfma_f32_16x16x32_bf16 a[236:239], v[80:83], v[96:99], a[236:239]\n" \
"v_mfma_f32_16x16x32_bf16 a[236:239], v[144:147], v[160:163], a[236:239]\n" \
"v_mfma_f32_16x16x32_bf16 a[232:235], v[80:83], v[92:95], a[232:235]\n" \
"v_mfma_f32_16x16x32_bf16 a[232:235], v[144:147], v[156:159], a[232:235]\n" \
"v_mfma_f32_16x16x32_bf16 a[228:231], v[80:83], v[88:91], a[228:231]\n" \
"v_mfma_f32_16x16x32_bf16 a[228:231], v[144:147], v[152:155], a[228:231]\n" \
"v_mfma_f32_16x16x32_bf16 a[224:227], v[80:83], v[84:87], a[224:227]\n" \
"v_mfma_f32_16x16x32_bf16 a[224:227], v[144:147], v[148:151], a[224:227]\n" \
"s_waitcnt vmcnt(1) lgkmcnt(0)\n" \
"s_barrier\n" \
"v_mov_b32_e32 v45, s62\n" \
"s_mov_b64 exec, 1\n" \
"buffer_store_dword v45, off, s[80:83], s37 offset:128 sc1\n" \
"s_mov_b64 exec, -1\n" \
"v_mfma_f32_16x16x32_bf16 a[16:19], v[52:55], v[100:103], a[16:19]\n" \
"s_add_u32 m0, s30, 0x8000\n" \
"s_mov_b32 s85, 0x380\n" \
"v_mfma_f32_16x16x32_bf16 a[16:19], v[116:119], v[164:167], a[16:19]\n" \
"buffer_load_dwordx4 v22, s[64:67], s85 offen lds\n" \
"v_mfma_f32_16x16x32_bf16 a[20:23], v[52:55], v[104:107], a[20:23]\n" \
"ds_read_b64_tr_b16 v[84:85], v32 offset:0\n" \
"v_mfma_f32_16x16x32_bf16 a[20:23], v[116:119], v[168:171], a[20:23]\n" \
"s_add_u32 m0, s30, 0x8400\n" \
"s_mov_b32 s84, 0x2380\n" \
"v_mfma_f32_16x16x32_bf16 a[24:27], v[52:55], v[108:111], a[24:27]\n" \
"buffer_load_dwordx4 v23, s[64:67], s84 offen lds\n" \
"v_mfma_f32_16x16x32_bf16 a[24:27], v[116:119], v[172:175], a[24:27]\n" \
"ds_read_b64_tr_b16 v[86:87], v32 offset:1024\n" \
"v_mfma_f32_16x16x32_bf16 a[28:31], v[52:55], v[112:115], a[28:31]\n" \
"ds_read_b64_tr_b16 v[148:149], v32 offset:8192\n" \
"v_mfma_f32_16x16x32_bf16 a[28:31], v[116:119], v[176:179], a[28:31]\n" \
"ds_read_b128 v[52:55], v28 offset:16384\n" \
"v_mfma_f32_16x16x32_bf16 a[60:63], v[56:59], v[112:115], a[60:63]\n" \
"ds_read_b128 v[116:119], v29 offset:16384\n" \
"v_mfma_f32_16x16x32_bf16 a[60:63], v[120:123], v[176:179], a[60:63]\n" \
"s_add_u32 m0, s31, 0x8000\n" \
"s_mov_b32 s85, 0x380000\n" \
"v_mfma_f32_16x16x32_bf16 a[56:59], v[56:59], v[108:111], a[56:59]\n" \
"buffer_load_dwordx4 v26, s[68:71], s85 offen lds\n" \
"v_mfma_f32_16x16x32_bf16 a[56:59], v[120:123], v[172:175], a[56:59]\n" \
"s_add_u32 m0, s31, 0xc000\n" \
"s_mov_b32 s84, 0x380000\n" \
"v_mfma_f32_16x16x32_bf16 a[52:55], v[56:59], v[104:107], a[52:55]\n" \
"buffer_load_dwordx4 v26, s[72:75], s84 offen lds\n" \
"v_mfma_f32_16x16x32_bf16 a[52:55], v[120:123], v[168:171], a[52:55]\n" \
"s_add_u32 m0, s30, 0x8800\n" \
"s_mov_b32 s85, 0x4380\n" \
"v_mfma_f32_16x16x32_bf16 a[48:51], v[56:59], v[100:103], a[48:51]\n" \
"buffer_load_dwordx4 v22, s[64:67], s85 offen lds\n" \
"v_mfma_f32_16x16x32_bf16 a[48:51], v[120:123], v[164:167], a[48:51]\n" \
"ds_read_b128 v[56:59], v28 offset:18432\n" \
"v_mfma_f32_16x16x32_bf16 a[80:83], v[60:63], v[100:103], a[80:83]\n" \
"ds_read_b128 v[120:123], v29 offset:18432\n" \
"v_mfma_f32_16x16x32_bf16 a[80:83], v[124:127], v[164:167], a[80:83]\n" \
"s_add_u32 m0, s30, 0x8c00\n" \
"s_mov_b32 s84, 0x6380\n" \
"v_mfma_f32_16x16x32_bf16 a[84:87], v[60:63], v[104:107], a[84:87]\n" \
"buffer_load_dwordx4 v23, s[64:67], s84 offen lds\n" \
"v_mfma_f32_16x16x32_bf16 a[84:87], v[124:127], v[168:171], a[84:87]\n" \
"ds_read_b64_tr_b16 v[150:151], v32 offset:9216\n" \
"v_mfma_f32_16x16x32_bf16 a[88:91], v[60:63], v[108:111], a[88:91]\n" \
"s_add_u32 m0, s31, 0x8400\n" \
"s_mov_b32 s85, 0x388000\n" \
"v_mfma_f32_16x16x32_bf16 a[88:91], v[124:127], v[172:175], a[88:91]\n" \
"buffer_load_dwordx4 v26, s[68:71], s85 offen lds\n" \
"v_mfma_f32_16x16x32_bf16 a[92:95], v[60:63], v[112:115], a[92:95]\n" \
"ds_read_b64_tr_b16 v[88:89], v33 offset:0\n" \
"v_mfma_f32_16x16x32_bf16 a[92:95], v[124:127], v[176:179], a[92:95]\n" \
"ds_read_b128 v[60:63], v28 offset:20480\n" \
"v_mfma_f32_16x16x32_bf16 a[124:127], v[64:67], v[112:115], a[124:127]\n" \
"ds_read_b128 v[124:127], v29 offset:20480\n" \
"v_mfma_f32_16x16x32_bf16 a[124:127], v[128:131], v[176:179], a[124:127]\n" \
"s_add_u32 m0, s31, 0xc400\n" \
"s_mov_b32 s84, 0x388000\n" \
"v_mfma_f32_16x16x32_bf16 a[120:123], v[64:67], v[108:111], a[120:123]\n" \
"buffer_load_dwordx4 v26, s[72:75], s84 offen lds\n" \
"v_mfma_f32_16x16x32_bf16 a[120:123], v[128:131], v[172:175], a[120:123]\n" \
"s_add_u32 m0, s30, 0x9000\n" \
"s_mov_b32 s85, 0x8380\n" \
"v_mfma_f32_16x16x32_bf16 a[116:119], v[64:67], v[104:107], a[116:119]\n" \
"buffer_load_dwordx4 v22, s[64:67], s85 offen lds\n" \
"v_mfma_f32_16x16x32_bf16 a[116:119], v[128:131], v[168:171], a[116:119]\n" \
"ds_read_b64_tr_b16 v[90:91], v33 offset:1024\n" \
"v_mfma_f32_16x16x32_bf16 a[112:115], v[64:67], v[100:103], a[112:115]\n" \
"ds_read_b64_tr_b16 v[152:153], v33 offset:8192\n" \
"v_mfma_f32_16x16x32_bf16 a[112:115], v[128:131], v[164:167], a[112:115]\n" \
"ds_read_b128 v[64:67], v28 offset:22528\n" \
"v_mfma_f32_16x16x32_bf16 a[144:147], v[68:71], v[100:103], a[144:147]\n" \
"ds_read_b128 v[128:131], v29 offset:22528\n" \
"v_mfma_f32_16x16x32_bf16 a[144:147], v[132:135], v[164:167], a[144:147]\n" \
"s_add_u32 m0, s30, 0x9400\n" \
"s_mov_b32 s84, 0xa380\n" \
"v_mfma_f32_16x16x32_bf16 a[148:151], v[68:71], v[104:107], a[148:151]\n" \
"buffer_load_dwordx4 v23, s[64:67], s84 offen lds\n" \
"v_mfma_f32_16x16x32_bf16 a[148:151], v[132:135], v[168:171], a[148:151]\n" \
"s_add_u32 m0, s31, 0x8800\n" \
"s_mov_b32 s85, 0x390000\n" \
"v_mfma_f32_16x16x32_bf16 a[152:155], v[68:71], v[108:111], a[152:155]\n" \
"buffer_load_dwordx4 v27, s[68:71], s85 offen lds\n" \
"v_mfma_f32_16x16x32_bf16 a[152:155], v[132:135], v[172:175], a[152:155]\n" \
"s_add_u32 m0, s31, 0xc800\n" \
"s_mov_b32 s84, 0x390000\n" \
"v_mfma_f32_16x16x32_bf16 a[156:159], v[68:71], v[112:115], a[156:159]\n" \
"buffer_load_dwordx4 v27, s[72:75], s84 offen lds\n" \
"v_mfma_f32_16x16x32_bf16 a[156:159], v[132:135], v[176:179], a[156:159]\n" \
"ds_read_b128 v[68:71], v28 offset:0\n" \
"v_mfma_f32_16x16x32_bf16 a[188:191], v[72:75], v[112:115], a[188:191]\n" \
"ds_read_b128 v[132:135], v29 offset:0\n" \
"v_mfma_f32_16x16x32_bf16 a[188:191], v[136:139], v[176:179], a[188:191]\n" \
"s_add_u32 m0, s30, 0x9800\n" \
"s_mov_b32 s85, 0xc380\n" \
"v_mfma_f32_16x16x32_bf16 a[184:187], v[72:75], v[108:111], a[184:187]\n" \
"buffer_load_dwordx4 v22, s[64:67], s85 offen lds\n" \
"v_mfma_f32_16x16x32_bf16 a[184:187], v[136:139], v[172:175], a[184:187]\n" \
"ds_read_b64_tr_b16 v[154:155], v33 offset:9216\n" \
"v_mfma_f32_16x16x32_bf16 a[180:183], v[72:75], v[104:107], a[180:183]\n" \
"s_add_u32 m0, s30, 0x9c00\n" \
"s_mov_b32 s84, 0xe380\n" \
"v_mfma_f32_16x16x32_bf16 a[180:183], v[136:139], v[168:171], a[180:183]\n" \
"buffer_load_dwordx4 v23, s[64:67], s84 offen lds\n" \
"v_mfma_f32_16x16x32_bf16 a[176:179], v[72:75], v[100:103], a[176:179]\n" \
"ds_read_b64_tr_b16 v[92:93], v34 offset:0\n" \
"v_mfma_f32_16x16x32_bf16 a[176:179], v[136:139], v[164:167], a[176:179]\n" \
"ds_read_b128 v[72:75], v28 offset:2048\n" \
"v_mfma_f32_16x16x32_bf16 a[208:211], v[76:79], v[100:103], a[208:211]\n" \
"ds_read_b128 v[136:139], v29 offset:2048\n" \
"v_mfma_f32_16x16x32_bf16 a[208:211], v[140:143], v[164:167], a[208:211]\n" \
"ds_read_b64_tr_b16 v[94:95], v34 offset:1024\n" \
"v_mfma_f32_16x16x32_bf16 a[212:215], v[76:79], v[104:107], a[212:215]\n" \
"ds_read_b64_tr_b16 v[156:157], v34 offset:8192\n" \
"v_mfma_f32_16x16x32_bf16 a[212:215], v[140:143], v[168:171], a[212:215]\n" \
"ds_read_b64_tr_b16 v[158:159], v34 offset:9216\n" \
"v_mfma_f32_16x16x32_bf16 a[216:219], v[76:79], v[108:111], a[216:219]\n" \
"ds_read_b64_tr_b16 v[96:97], v35 offset:0\n" \
"v_mfma_f32_16x16x32_bf16 a[216:219], v[140:143], v[172:175], a[216:219]\n" \
"ds_read_b64_tr_b16 v[98:99], v35 offset:1024\n" \
"v_mfma_f32_16x16x32_bf16 a[220:223], v[76:79], v[112:115], a[220:223]\n" \
"ds_read_b64_tr_b16 v[160:161], v35 offset:8192\n" \
"v_mfma_f32_16x16x32_bf16 a[220:223], v[140:143], v[176:179], a[220:223]\n" \
"ds_read_b128 v[76:79], v28 offset:4096\n" \
"v_mfma_f32_16x16x32_bf16 a[252:255], v[80:83], v[112:115], a[252:255]\n" \
"ds_read_b128 v[140:143], v29 offset:4096\n" \
"v_mfma_f32_16x16x32_bf16 a[252:255], v[144:147], v[176:179], a[252:255]\n" \
"ds_read_b64_tr_b16 v[162:163], v35 offset:9216\n" \
"v_mfma_f32_16x16x32_bf16 a[248:251], v[80:83], v[108:111], a[248:251]\n" \
"v_mfma_f32_16x16x32_bf16 a[248:251], v[144:147], v[172:175], a[248:251]\n" \
"v_mfma_f32_16x16x32_bf16 a[244:247], v[80:83], v[104:107], a[244:247]\n" \
"v_mfma_f32_16x16x32_bf16 a[244:247], v[144:147], v[168:171], a[244:247]\n" \
"v_mfma_f32_16x16x32_bf16 a[240:243], v[80:83], v[100:103], a[240:243]\n" \
"v_mfma_f32_16x16x32_bf16 a[240:243], v[144:147], v[164:167], a[240:243]\n" \
"ds_read_b128 v[80:83], v28 offset:6144\n" \
"ds_read_b128 v[144:147], v29 offset:6144\n" \
"s_waitcnt lgkmcnt(15)\n" \
"v_mfma_f32_16x16x32_bf16 a[0:3], v[52:55], v[84:87], a[0:3]\n" \
"s_add_u32 m0, s31, 0x8c00\n" \
"s_mov_b32 s85, 0x398000\n" \
"v_mfma_f32_16x16x32_bf16 a[0:3], v[116:119], v[148:151], a[0:3]\n" \
"buffer_load_dwordx4 v27, s[68:71], s85 offen lds\n" \
"v_mfma_f32_16x16x32_bf16 a[4:7], v[52:55], v[88:91], a[4:7]\n" \
".fill (L_pb + 61152 - .) / 4, 4, 0xBF800000\n" \
"L_pp15:\n" \
"s_cbranch_execz L_pret\n" \
"ds_read_b64_tr_b16 v[100:101], v36 offset:0\n" \
"s_waitcnt lgkmcnt(15)\n" \
"v_mfma_f32_16x16x32_bf16 a[4:7], v[116:119], v[152:155], a[4:7]\n" \
"s_add_u32 m0, s31, 0xcc00\n" \
"s_mov_b32 s84, 0x398000\n" \
"s_waitcnt lgkmcnt(11)\n" \
"v_mfma_f32_16x16x32_bf16 a[8:11], v[52:55], v[92:95], a[8:11]\n" \
"buffer_load_dwordx4 v27, s[72:75], s84 offen lds\n" \
"s_waitcnt lgkmcnt(9)\n" \
"v_mfma_f32_16x16x32_bf16 a[8:11], v[116:119], v[156:159], a[8:11]\n" \
"ds_read_b64_tr_b16 v[102:103], v36 offset:1024\n" \
"s_waitcnt lgkmcnt(8)\n" \
"v_mfma_f32_16x16x32_bf16 a[12:15], v[52:55], v[96:99], a[12:15]\n" \
"ds_read_b64_tr_b16 v[164:165], v36 offset:8192\n" \
"s_waitcnt lgkmcnt(5)\n" \
"v_mfma_f32_16x16x32_bf16 a[12:15], v[116:119], v[160:163], a[12:15]\n" \
"v_mfma_f32_16x16x32_bf16 a[44:47], v[56:59], v[96:99], a[44:47]\n" \
"v_mfma_f32_16x16x32_bf16 a[44:47], v[120:123], v[160:163], a[44:47]\n" \
"ds_read_b64_tr_b16 v[166:167], v36 offset:9216\n" \
"v_mfma_f32_16x16x32_bf16 a[40:43], v[56:59], v[92:95], a[40:43]\n" \
"v_mfma_f32_16x16x32_bf16 a[40:43], v[120:123], v[156:159], a[40:43]\n" \
"v_mfma_f32_16x16x32_bf16 a[36:39], v[56:59], v[88:91], a[36:39]\n" \
"v_mfma_f32_16x16x32_bf16 a[36:39], v[120:123], v[152:155], a[36:39]\n" \
"ds_read_b64_tr_b16 v[104:105], v37 offset:0\n" \
"v_mfma_f32_16x16x32_bf16 a[32:35], v[56:59], v[84:87], a[32:35]\n" \
"v_mfma_f32_16x16x32_bf16 a[32:35], v[120:123], v[148:151], a[32:35]\n" \
"v_mfma_f32_16x16x32_bf16 a[64:67], v[60:63], v[84:87], a[64:67]\n" \
"ds_read_b64_tr_b16 v[106:107], v37 offset:1024\n" \
"v_mfma_f32_16x16x32_bf16 a[64:67], v[124:127], v[148:151], a[64:67]\n" \
"v_mfma_f32_16x16x32_bf16 a[68:71], v[60:63], v[88:91], a[68:71]\n" \
"v_mfma_f32_16x16x32_bf16 a[68:71], v[124:127], v[152:155], a[68:71]\n" \
"ds_read_b64_tr_b16 v[168:169], v37 offset:8192\n" \
"v_mfma_f32_16x16x32_bf16 a[72:75], v[60:63], v[92:95], a[72:75]\n" \
"v_mfma_f32_16x16x32_bf16 a[72:75], v[124:127], v[156:159], a[72:75]\n" \
"v_mfma_f32_16x16x32_bf16 a[76:79], v[60:63], v[96:99], a[76:79]\n" \
"ds_read_b64_tr_b16 v[170:171], v37 offset:9216\n" \
"v_mfma_f32_16x16x32_bf16 a[76:79], v[124:127], v[160:163], a[76:79]\n" \
"v_mfma_f32_16x16x32_bf16 a[108:111], v[64:67], v[96:99], a[108:111]\n" \
"v_mfma_f32_16x16x32_bf16 a[108:111], v[128:131], v[160:163], a[108:111]\n" \
"v_mfma_f32_16x16x32_bf16 a[104:107], v[64:67], v[92:95], a[104:107]\n" \
"ds_read_b64_tr_b16 v[108:109], v38 offset:0\n" \
"v_mfma_f32_16x16x32_bf16 a[104:107], v[128:131], v[156:159], a[104:107]\n" \
"v_mfma_f32_16x16x32_bf16 a[100:103], v[64:67], v[88:91], a[100:103]\n" \
"v_mfma_f32_16x16x32_bf16 a[100:103], v[128:131], v[152:155], a[100:103]\n" \
"ds_read_b64_tr_b16 v[110:111], v38 offset:1024\n" \
"v_mfma_f32_16x16x32_bf16 a[96:99], v[64:67], v[84:87], a[96:99]\n" \
"v_mfma_f32_16x16x32_bf16 a[96:99], v[128:131], v[148:151], a[96:99]\n" \
"v_mfma_f32_16x16x32_bf16 a[128:131], v[68:71], v[84:87], a[128:131]\n" \
"v_bfe_u32 v44, v20, 2, 5\n" \
"ds_read_b64_tr_b16 v[172:173], v38 offset:8192\n" \
"v_mfma_f32_16x16x32_bf16 a[128:131], v[132:135], v[148:151], a[128:131]\n" \
"buffer_load_dword v44, v44, s[80:83], s38 offen offset:128 sc1\n" \
"v_mfma_f32_16x16x32_bf16 a[132:135], v[68:71], v[88:91], a[132:135]\n" \
"v_mfma_f32_16x16x32_bf16 a[132:135], v[132:135], v[152:155], a[132:135]\n" \
"ds_read_b64_tr_b16 v[174:175], v38 offset:9216\n" \
"v_mfma_f32_16x16x32_bf16 a[136:139], v[68:71], v[92:95], a[136:139]\n" \
"v_mfma_f32_16x16x32_bf16 a[136:139], v[132:135], v[156:159], a[136:139]\n" \
"v_mfma_f32_16x16x32_bf16 a[140:143], v[68:71], v[96:99], a[140:143]\n" \
"v_mfma_f32_16x16x32_bf16 a[140:143], v[132:135], v[160:163], a[140:143]\n" \
"ds_read_b64_tr_b16 v[112:113], v39 offset:0\n" \
"v_mfma_f32_16x16x32_bf16 a[172:175], v[72:75], v[96:99], a[172:175]\n" \
"v_mfma_f32_16x16x32_bf16 a[172:175], v[136:139], v[160:163], a[172:175]\n" \
"v_mfma_f32_16x16x32_bf16 a[168:171], v[72:75], v[92:95], a[168:171]\n" \
"ds_read_b64_tr_b16 v[114:115], v39 offset:1024\n" \
"v_mfma_f32_16x16x32_bf16 a[168:171], v[136:139], v[156:159], a[168:171]\n" \
"v_mfma_f32_16x16x32_bf16 a[164:167], v[72:75], v[88:91], a[164:167]\n" \
"v_mfma_f32_16x16x32_bf16 a[164:167], v[136:139], v[152:155], a[164:167]\n" \
"ds_read_b64_tr_b16 v[176:177], v39 offset:8192\n" \
"v_mfma_f32_16x16x32_bf16 a[160:163], v[72:75], v[84:87], a[160:163]\n" \
"v_mfma_f32_16x16x32_bf16 a[160:163], v[136:139], v[148:151], a[160:163]\n" \
"v_mfma_f32_16x16x32_bf16 a[192:195], v[76:79], v[84:87], a[192:195]\n" \
"ds_read_b64_tr_b16 v[178:179], v39 offset:9216\n" \
"v_mfma_f32_16x16x32_bf16 a[192:195], v[140:143], v[148:151], a[192:195]\n" \
"v_mfma_f32_16x16x32_bf16 a[196:199], v[76:79], v[88:91], a[196:199]\n" \
"v_mfma_f32_16x16x32_bf16 a[196:199], v[140:143], v[152:155], a[196:199]\n" \
"v_mfma_f32_16x16x32_bf16 a[200:203], v[76:79], v[92:95], a[200:203]\n" \
"v_mfma_f32_16x16x32_bf16 a[200:203], v[140:143], v[156:159], a[200:203]\n" \
"v_mfma_f32_16x16x32_bf16 a[204:207], v[76:79], v[96:99], a[204:207]\n" \
"v_mfma_f32_16x16x32_bf16 a[204:207], v[140:143], v[160:163], a[204:207]\n" \
"s_waitcnt lgkmcnt(15)\n" \
"v_mfma_f32_16x16x32_bf16 a[236:239], v[80:83], v[96:99], a[236:239]\n" \
"v_mfma_f32_16x16x32_bf16 a[236:239], v[144:147], v[160:163], a[236:239]\n" \
"v_mfma_f32_16x16x32_bf16 a[232:235], v[80:83], v[92:95], a[232:235]\n" \
"v_mfma_f32_16x16x32_bf16 a[232:235], v[144:147], v[156:159], a[232:235]\n" \
"v_mfma_f32_16x16x32_bf16 a[228:231], v[80:83], v[88:91], a[228:231]\n" \
"v_mfma_f32_16x16x32_bf16 a[228:231], v[144:147], v[152:155], a[228:231]\n" \
"v_mfma_f32_16x16x32_bf16 a[224:227], v[80:83], v[84:87], a[224:227]\n" \
"v_mfma_f32_16x16x32_bf16 a[224:227], v[144:147], v[148:151], a[224:227]\n" \
"s_waitcnt vmcnt(0) lgkmcnt(0)\n" \
"s_barrier\n" \
"v_cmp_ne_u32_e32 vcc, s62, v44\n" \
"s_nop 4\n" \
"s_cbranch_vccnz L_poll61_%=\n" \
"L_pollr61_%=:\n" \
"s_mov_b32 s68, s12\n" \
"s_and_b32 s69, s13, 0xffff\n" \
"s_mov_b32 s70, 0x480000\n" \
"s_mov_b32 s71, 0x20000\n" \
"v_mfma_f32_16x16x32_bf16 a[16:19], v[52:55], v[100:103], a[16:19]\n" \
"s_add_u32 m0, s40, 0x0\n" \
"s_add_u32 s85, s39, 0x0\n" \
"s_nop 0\n" \
"v_mfma_f32_16x16x32_bf16 a[16:19], v[116:119], v[164:167], a[16:19]\n" \
"buffer_load_dwordx4 v20, s[80:83], s85 offen sc1 lds\n" \
"v_mfma_f32_16x16x32_bf16 a[20:23], v[52:55], v[104:107], a[20:23]\n" \
"ds_read_b64_tr_b16 v[84:85], v32 offset:32768\n" \
"v_mfma_f32_16x16x32_bf16 a[20:23], v[116:119], v[168:171], a[20:23]\n" \
"s_add_u32 m0, s40, 0x1000\n" \
"s_add_u32 s84, s39, 0x1000\n" \
"v_mfma_f32_16x16x32_bf16 a[24:27], v[52:55], v[108:111], a[24:27]\n" \
"buffer_load_dwordx4 v20, s[80:83], s84 offen sc1 lds\n" \
"v_mfma_f32_16x16x32_bf16 a[24:27], v[116:119], v[172:175], a[24:27]\n" \
"ds_read_b64_tr_b16 v[86:87], v32 offset:33792\n" \
"v_mfma_f32_16x16x32_bf16 a[28:31], v[52:55], v[112:115], a[28:31]\n" \
"ds_read_b64_tr_b16 v[148:149], v32 offset:40960\n" \
"v_mfma_f32_16x16x32_bf16 a[28:31], v[116:119], v[176:179], a[28:31]\n" \
"ds_read_b128 v[52:55], v28 offset:49152\n" \
"v_mfma_f32_16x16x32_bf16 a[60:63], v[56:59], v[112:115], a[60:63]\n" \
"ds_read_b128 v[116:119], v29 offset:49152\n" \
"v_mfma_f32_16x16x32_bf16 a[60:63], v[120:123], v[176:179], a[60:63]\n" \
"s_add_u32 m0, s40, 0x3000\n" \
"s_mov_b32 s85, 0x0\n" \
"v_mfma_f32_16x16x32_bf16 a[56:59], v[56:59], v[108:111], a[56:59]\n" \
"buffer_load_dwordx4 v21, s[76:79], s85 offen lds\n" \
"v_mfma_f32_16x16x32_bf16 a[56:59], v[120:123], v[172:175], a[56:59]\n" \
"s_add_u32 m0, s30, 0x0\n" \
"s_mov_b32 s84, 0x0\n" \
"s_nop 0\n" \
"v_mfma_f32_16x16x32_bf16 a[52:55], v[56:59], v[104:107], a[52:55]\n" \
"buffer_load_dwordx4 v24, s[76:79], s84 offen lds\n" \
"v_mfma_f32_16x16x32_bf16 a[52:55], v[120:123], v[168:171], a[52:55]\n" \
"ds_read_b64_tr_b16 v[150:151], v32 offset:41984\n" \
"v_mfma_f32_16x16x32_bf16 a[48:51], v[56:59], v[100:103], a[48:51]\n" \
"ds_read_b64_tr_b16 v[88:89], v33 offset:32768\n" \
"v_mfma_f32_16x16x32_bf16 a[48:51], v[120:123], v[164:167], a[48:51]\n" \
"ds_read_b128 v[56:59], v28 offset:51200\n" \
"v_mfma_f32_16x16x32_bf16 a[80:83], v[60:63], v[100:103], a[80:83]\n" \
"ds_read_b128 v[120:123], v29 offset:51200\n" \
"v_mfma_f32_16x16x32_bf16 a[80:83], v[124:127], v[164:167], a[80:83]\n" \
"s_add_u32 m0, s30, 0x400\n" \
"s_mov_b32 s85, 0x2400\n" \
"v_mfma_f32_16x16x32_bf16 a[84:87], v[60:63], v[104:107], a[84:87]\n" \
"buffer_load_dwordx4 v25, s[76:79], s85 offen lds\n" \
"v_mfma_f32_16x16x32_bf16 a[84:87], v[124:127], v[168:171], a[84:87]\n" \
"s_add_u32 m0, s31, 0x0\n" \
"s_mov_b32 s84, 0x0\n" \
"v_mfma_f32_16x16x32_bf16 a[88:91], v[60:63], v[108:111], a[88:91]\n" \
"buffer_load_dwordx4 v26, s[68:71], s84 offen lds\n" \
"v_mfma_f32_16x16x32_bf16 a[88:91], v[124:127], v[172:175], a[88:91]\n" \
"ds_read_b64_tr_b16 v[90:91], v33 offset:33792\n" \
"v_mfma_f32_16x16x32_bf16 a[92:95], v[60:63], v[112:115], a[92:95]\n" \
"ds_read_b64_tr_b16 v[152:153], v33 offset:40960\n" \
"v_mfma_f32_16x16x32_bf16 a[92:95], v[124:127], v[176:179], a[92:95]\n" \
"ds_read_b128 v[60:63], v28 offset:53248\n" \
"v_mfma_f32_16x16x32_bf16 a[124:127], v[64:67], v[112:115], a[124:127]\n" \
"ds_read_b128 v[124:127], v29 offset:53248\n" \
"v_mfma_f32_16x16x32_bf16 a[124:127], v[128:131], v[176:179], a[124:127]\n" \
"s_add_u32 m0, s30, 0x800\n" \
"s_mov_b32 s85, 0x4800\n" \
"v_mfma_f32_16x16x32_bf16 a[120:123], v[64:67], v[108:111], a[120:123]\n" \
"buffer_load_dwordx4 v24, s[76:79], s85 offen lds\n" \
"v_mfma_f32_16x16x32_bf16 a[120:123], v[128:131], v[172:175], a[120:123]\n" \
"s_add_u32 m0, s30, 0xc00\n" \
"s_mov_b32 s84, 0x6c00\n" \
"v_mfma_f32_16x16x32_bf16 a[116:119], v[64:67], v[104:107], a[116:119]\n" \
"buffer_load_dwordx4 v25, s[76:79], s84 offen lds\n" \
"v_mfma_f32_16x16x32_bf16 a[116:119], v[128:131], v[168:171], a[116:119]\n" \
"s_add_u32 m0, s31, 0x400\n" \
"s_mov_b32 s85, 0x8000\n" \
"v_mfma_f32_16x16x32_bf16 a[112:115], v[64:67], v[100:103], a[112:115]\n" \
"buffer_load_dwordx4 v26, s[68:71], s85 offen lds\n" \
"v_mfma_f32_16x16x32_bf16 a[112:115], v[128:131], v[164:167], a[112:115]\n" \
"ds_read_b128 v[64:67], v28 offset:55296\n" \
"v_mfma_f32_16x16x32_bf16 a[144:147], v[68:71], v[100:103], a[144:147]\n" \
"ds_read_b128 v[128:131], v29 offset:55296\n" \
"v_mfma_f32_16x16x32_bf16 a[144:147], v[132:135], v[164:167], a[144:147]\n" \
"s_add_u32 m0, s30, 0x1000\n" \
"s_mov_b32 s84, 0x9000\n" \
"v_mfma_f32_16x16x32_bf16 a[148:151], v[68:71], v[104:107], a[148:151]\n" \
"buffer_load_dwordx4 v24, s[76:79], s84 offen lds\n" \
"v_mfma_f32_16x16x32_bf16 a[148:151], v[132:135], v[168:171], a[148:151]\n" \
"ds_read_b64_tr_b16 v[154:155], v33 offset:41984\n" \
"v_mfma_f32_16x16x32_bf16 a[152:155], v[68:71], v[108:111], a[152:155]\n" \
"s_add_u32 m0, s30, 0x1400\n" \
"s_mov_b32 s85, 0xb400\n" \
"v_mfma_f32_16x16x32_bf16 a[152:155], v[132:135], v[172:175], a[152:155]\n" \
"buffer_load_dwordx4 v25, s[76:79], s85 offen lds\n" \
"v_mfma_f32_16x16x32_bf16 a[156:159], v[68:71], v[112:115], a[156:159]\n" \
"ds_read_b64_tr_b16 v[92:93], v34 offset:32768\n" \
"v_mfma_f32_16x16x32_bf16 a[156:159], v[132:135], v[176:179], a[156:159]\n" \
"ds_read_b128 v[68:71], v28 offset:32768\n" \
"v_mfma_f32_16x16x32_bf16 a[188:191], v[72:75], v[112:115], a[188:191]\n" \
"ds_read_b128 v[132:135], v29 offset:32768\n" \
"v_mfma_f32_16x16x32_bf16 a[188:191], v[136:139], v[176:179], a[188:191]\n" \
"s_add_u32 m0, s31, 0x800\n" \
"s_mov_b32 s84, 0x10000\n" \
"v_mfma_f32_16x16x32_bf16 a[184:187], v[72:75], v[108:111], a[184:187]\n" \
"buffer_load_dwordx4 v27, s[68:71], s84 offen lds\n" \
"v_mfma_f32_16x16x32_bf16 a[184:187], v[136:139], v[172:175], a[184:187]\n" \
"ds_read_b64_tr_b16 v[94:95], v34 offset:33792\n" \
"v_mfma_f32_16x16x32_bf16 a[180:183], v[72:75], v[104:107], a[180:183]\n" \
"s_add_u32 m0, s30, 0x1800\n" \
"s_mov_b32 s85, 0xd800\n" \
"v_mfma_f32_16x16x32_bf16 a[180:183], v[136:139], v[168:171], a[180:183]\n" \
"buffer_load_dwordx4 v24, s[76:79], s85 offen lds\n" \
"v_mfma_f32_16x16x32_bf16 a[176:179], v[72:75], v[100:103], a[176:179]\n" \
"ds_read_b64_tr_b16 v[156:157], v34 offset:40960\n" \
"v_mfma_f32_16x16x32_bf16 a[176:179], v[136:139], v[164:167], a[176:179]\n" \
"ds_read_b128 v[72:75], v28 offset:34816\n" \
"v_mfma_f32_16x16x32_bf16 a[208:211], v[76:79], v[100:103], a[208:211]\n" \
"ds_read_b128 v[136:139], v29 offset:34816\n" \
"v_mfma_f32_16x16x32_bf16 a[208:211], v[140:143], v[164:167], a[208:211]\n" \
"ds_read_b64_tr_b16 v[158:159], v34 offset:41984\n" \
"v_mfma_f32_16x16x32_bf16 a[212:215], v[76:79], v[104:107], a[212:215]\n" \
"ds_read_b64_tr_b16 v[96:97], v35 offset:32768\n" \
"v_mfma_f32_16x16x32_bf16 a[212:215], v[140:143], v[168:171], a[212:215]\n" \
"ds_read_b64_tr_b16 v[98:99], v35 offset:33792\n" \
"v_mfma_f32_16x16x32_bf16 a[216:219], v[76:79], v[108:111], a[216:219]\n" \
"ds_read_b64_tr_b16 v[160:161], v35 offset:40960\n" \
"v_mfma_f32_16x16x32_bf16 a[216:219], v[140:143], v[172:175], a[216:219]\n" \
"ds_read_b64_tr_b16 v[162:163], v35 offset:41984\n" \
"v_mfma_f32_16x16x32_bf16 a[220:223], v[76:79], v[112:115], a[220:223]\n" \
"v_mfma_f32_16x16x32_bf16 a[220:223], v[140:143], v[176:179], a[220:223]\n" \
"ds_read_b128 v[76:79], v28 offset:36864\n" \
"v_mfma_f32_16x16x32_bf16 a[252:255], v[80:83], v[112:115], a[252:255]\n" \
"ds_read_b128 v[140:143], v29 offset:36864\n" \
"v_mfma_f32_16x16x32_bf16 a[252:255], v[144:147], v[176:179], a[252:255]\n" \
"v_mfma_f32_16x16x32_bf16 a[248:251], v[80:83], v[108:111], a[248:251]\n" \
"v_mfma_f32_16x16x32_bf16 a[248:251], v[144:147], v[172:175], a[248:251]\n" \
"v_mfma_f32_16x16x32_bf16 a[244:247], v[80:83], v[104:107], a[244:247]\n" \
"v_mfma_f32_16x16x32_bf16 a[244:247], v[144:147], v[168:171], a[244:247]\n" \
"v_mfma_f32_16x16x32_bf16 a[240:243], v[80:83], v[100:103], a[240:243]\n" \
"v_mfma_f32_16x16x32_bf16 a[240:243], v[144:147], v[164:167], a[240:243]\n" \
"ds_read_b128 v[80:83], v28 offset:38912\n" \
"ds_read_b128 v[144:147], v29 offset:38912\n" \
"s_waitcnt lgkmcnt(15)\n" \
"v_mfma_f32_16x16x32_bf16 a[0:3], v[52:55], v[84:87], a[0:3]\n" \
"s_add_u32 m0, s30, 0x1c00\n" \
"s_mov_b32 s85, 0xfc00\n" \
"v_mfma_f32_16x16x32_bf16 a[0:3], v[116:119], v[148:151], a[0:3]\n" \
"buffer_load_dwordx4 v25, s[76:79], s85 offen lds\n" \
"v_mfma_f32_16x16x32_bf16 a[4:7], v[52:55], v[88:91], a[4:7]\n" \
"ds_read_b64_tr_b16 v[100:101], v36 offset:32768\n" \
"v_mfma_f32_16x16x32_bf16 a[4:7], v[116:119], v[152:155], a[4:7]\n" \
"s_add_u32 m0, s31, 0xc00\n" \
"s_mov_b32 s84, 0x18000\n" \
"s_waitcnt lgkmcnt(13)\n" \
"v_mfma_f32_16x16x32_bf16 a[8:11], v[52:55], v[92:95], a[8:11]\n" \
"buffer_load_dwordx4 v27, s[68:71], s84 offen lds\n" \
"s_waitcnt lgkmcnt(9)\n" \
"v_mfma_f32_16x16x32_bf16 a[8:11], v[116:119], v[156:159], a[8:11]\n" \
"ds_read_b64_tr_b16 v[102:103], v36 offset:33792\n" \
"s_waitcnt lgkmcnt(8)\n" \
"v_mfma_f32_16x16x32_bf16 a[12:15], v[52:55], v[96:99], a[12:15]\n" \
"ds_read_b64_tr_b16 v[164:165], v36 offset:40960\n" \
"s_waitcnt lgkmcnt(7)\n" \
"v_mfma_f32_16x16x32_bf16 a[12:15], v[116:119], v[160:163], a[12:15]\n" \
"v_mfma_f32_16x16x32_bf16 a[44:47], v[56:59], v[96:99], a[44:47]\n" \
"v_mfma_f32_16x16x32_bf16 a[44:47], v[120:123], v[160:163], a[44:47]\n" \
"ds_read_b64_tr_b16 v[166:167], v36 offset:41984\n" \
"v_mfma_f32_16x16x32_bf16 a[40:43], v[56:59], v[92:95], a[40:43]\n" \
"v_mfma_f32_16x16x32_bf16 a[40:43], v[120:123], v[156:159], a[40:43]\n" \
"v_mfma_f32_16x16x32_bf16 a[36:39], v[56:59], v[88:91], a[36:39]\n" \
"s_nop 1\n" \
"v_accvgpr_read_b32 v184, a12\n" \
"v_accvgpr_read_b32 v185, a13\n" \
"v_mfma_f32_16x16x32_bf16 a[36:39], v[120:123], v[152:155], a[36:39]\n" \
"ds_read_b64_tr_b16 v[104:105], v37 offset:32768\n" \
"v_accvgpr_read_b32 v186, a14\n" \
"v_mfma_f32_16x16x32_bf16 a[32:35], v[56:59], v[84:87], a[32:35]\n" \
"v_accvgpr_read_b32 v187, a15\n" \
"v_accvgpr_read_b32 v180, a44\n" \
"v_mfma_f32_16x16x32_bf16 a[32:35], v[120:123], v[148:151], a[32:35]\n" \
"v_accvgpr_read_b32 v181, a45\n" \
"v_accvgpr_read_b32 v182, a46\n" \
"v_mfma_f32_16x16x32_bf16 a[64:67], v[60:63], v[84:87], a[64:67]\n" \
"ds_read_b64_tr_b16 v[106:107], v37 offset:33792\n" \
"v_accvgpr_read_b32 v183, a47\n" \
"v_mfma_f32_16x16x32_bf16 a[64:67], v[124:127], v[148:151], a[64:67]\n" \
"v_cvt_pk_bf16_f32 v184, v184, v185\n" \
"v_cvt_pk_bf16_f32 v185, v186, v187\n" \
"v_mfma_f32_16x16x32_bf16 a[68:71], v[60:63], v[88:91], a[68:71]\n" \
"v_lshlrev_b32_e32 v45, 3, v20\n" \
"v_add_u32_e32 v45, s31, v45\n" \
"v_mfma_f32_16x16x32_bf16 a[68:71], v[124:127], v[152:155], a[68:71]\n" \
"s_getpc_b64 s[86:87]\n" \
"s_sub_u32 s86, s86, 0x10800\n" \
"s_subb_u32 s87, s87, 0\n" \
"s_mov_b32 m0, s32\n" \
"s_nop 0\n" \
"v_mfma_f32_16x16x32_bf16 a[72:75], v[60:63], v[92:95], a[72:75]\n" \
"global_load_lds_dword v45, s[86:87]\n" \
"v_mfma_f32_16x16x32_bf16 a[72:75], v[124:127], v[156:159], a[72:75]\n" \
"ds_read_b64_tr_b16 v[168:169], v37 offset:40960\n" \
"v_cvt_pk_bf16_f32 v186, v180, v181\n" \
"v_mfma_f32_16x16x32_bf16 a[76:79], v[60:63], v[96:99], a[76:79]\n" \
"ds_read_b64_tr_b16 v[170:171], v37 offset:41984\n" \
"v_cvt_pk_bf16_f32 v187, v182, v183\n" \
"v_mfma_f32_16x16x32_bf16 a[76:79], v[124:127], v[160:163], a[76:79]\n" \
"v_accvgpr_read_b32 v188, a8\n" \
"v_accvgpr_read_b32 v189, a9\n" \
"v_mfma_f32_16x16x32_bf16 a[108:111], v[64:67], v[96:99], a[108:111]\n" \
"v_accvgpr_read_b32 v190, a10\n" \
"v_accvgpr_read_b32 v191, a11\n" \
"v_mfma_f32_16x16x32_bf16 a[108:111], v[128:131], v[160:163], a[108:111]\n" \
"v_accvgpr_read_b32 v180, a40\n" \
"v_accvgpr_read_b32 v181, a41\n" \
"v_mfma_f32_16x16x32_bf16 a[104:107], v[64:67], v[92:95], a[104:107]\n" \
"ds_read_b64_tr_b16 v[108:109], v38 offset:32768\n" \
"v_accvgpr_read_b32 v182, a42\n" \
"v_mfma_f32_16x16x32_bf16 a[104:107], v[128:131], v[156:159], a[104:107]\n" \
"v_accvgpr_read_b32 v183, a43\n" \
"v_cvt_pk_bf16_f32 v188, v188, v189\n" \
"v_mfma_f32_16x16x32_bf16 a[100:103], v[64:67], v[88:91], a[100:103]\n" \
"v_cvt_pk_bf16_f32 v189, v190, v191\n" \
"v_cvt_pk_bf16_f32 v190, v180, v181\n" \
"v_mfma_f32_16x16x32_bf16 a[100:103], v[128:131], v[152:155], a[100:103]\n" \
"ds_read_b64_tr_b16 v[110:111], v38 offset:33792\n" \
"v_cvt_pk_bf16_f32 v191, v182, v183\n" \
"v_mfma_f32_16x16x32_bf16 a[96:99], v[64:67], v[84:87], a[96:99]\n" \
"v_accvgpr_read_b32 v192, a4\n" \
"v_accvgpr_read_b32 v193, a5\n" \
"v_mfma_f32_16x16x32_bf16 a[96:99], v[128:131], v[148:151], a[96:99]\n" \
"v_accvgpr_read_b32 v194, a6\n" \
"v_accvgpr_read_b32 v195, a7\n" \
"v_mfma_f32_16x16x32_bf16 a[128:131], v[68:71], v[84:87], a[128:131]\n" \
"ds_read_b64_tr_b16 v[172:173], v38 offset:40960\n" \
"v_accvgpr_read_b32 v180, a36\n" \
"v_mfma_f32_16x16x32_bf16 a[128:131], v[132:135], v[148:151], a[128:131]\n" \
"v_accvgpr_read_b32 v181, a37\n" \
"v_accvgpr_read_b32 v182, a38\n" \
"v_mfma_f32_16x16x32_bf16 a[132:135], v[68:71], v[88:91], a[132:135]\n" \
"v_accvgpr_read_b32 v183, a39\n" \
"v_cvt_pk_bf16_f32 v192, v192, v193\n" \
"v_mfma_f32_16x16x32_bf16 a[132:135], v[132:135], v[152:155], a[132:135]\n" \
"ds_read_b64_tr_b16 v[174:175], v38 offset:41984\n" \
"v_cvt_pk_bf16_f32 v193, v194, v195\n" \
"v_mfma_f32_16x16x32_bf16 a[136:139], v[68:71], v[92:95], a[136:139]\n" \
"v_cvt_pk_bf16_f32 v194, v180, v181\n" \
"v_cvt_pk_bf16_f32 v195, v182, v183\n" \
"v_mfma_f32_16x16x32_bf16 a[136:139], v[132:135], v[156:159], a[136:139]\n" \
"v_accvgpr_read_b32 v196, a0\n" \
"v_accvgpr_read_b32 v197, a1\n" \
"v_mfma_f32_16x16x32_bf16 a[140:143], v[68:71], v[96:99], a[140:143]\n" \
"v_accvgpr_read_b32 v198, a2\n" \
"v_accvgpr_read_b32 v199, a3\n" \
"v_mfma_f32_16x16x32_bf16 a[140:143], v[132:135], v[160:163], a[140:143]\n" \
"ds_read_b64_tr_b16 v[112:113], v39 offset:32768\n" \
"v_accvgpr_read_b32 v180, a32\n" \
"v_mfma_f32_16x16x32_bf16 a[172:175], v[72:75], v[96:99], a[172:175]\n" \
"s_add_u32 s89, s41, 0x0\n" \
"s_nop 0\n" \
"buffer_store_dwordx4 v[184:187], v20, s[80:83], s89 offen offset:3072 sc1\n" \
"v_mfma_f32_16x16x32_bf16 a[172:175], v[136:139], v[160:163], a[172:175]\n" \
"v_accvgpr_read_b32 v181, a33\n" \
"v_accvgpr_read_b32 v182, a34\n" \
"v_mfma_f32_16x16x32_bf16 a[168:171], v[72:75], v[92:95], a[168:171]\n" \
"ds_read_b64_tr_b16 v[114:115], v39 offset:33792\n" \
"v_accvgpr_read_b32 v183, a35\n" \
"v_mfma_f32_16x16x32_bf16 a[168:171], v[136:139], v[156:159], a[168:171]\n" \
"v_cvt_pk_bf16_f32 v196, v196, v197\n" \
"v_cvt_pk_bf16_f32 v197, v198, v199\n" \
"v_mfma_f32_16x16x32_bf16 a[164:167], v[72:75], v[88:91], a[164:167]\n" \
"v_cvt_pk_bf16_f32 v198, v180, v181\n" \
"v_cvt_pk_bf16_f32 v199, v182, v183\n" \
"v_mfma_f32_16x16x32_bf16 a[164:167], v[136:139], v[152:155], a[164:167]\n" \
"ds_read_b64_tr_b16 v[176:177], v39 offset:40960\n" \
"v_accvgpr_read_b32 v200, a76\n" \
"v_mfma_f32_16x16x32_bf16 a[160:163], v[72:75], v[84:87], a[160:163]\n" \
"s_add_u32 s88, s41, 0x0\n" \
"buffer_store_dwordx4 v[188:191], v20, s[80:83], s88 offen offset:2048 sc1\n" \
"v_mfma_f32_16x16x32_bf16 a[160:163], v[136:139], v[148:151], a[160:163]\n" \
"v_accvgpr_read_b32 v201, a77\n" \
"v_accvgpr_read_b32 v202, a78\n" \
"s_waitcnt lgkmcnt(15)\n" \
"v_mfma_f32_16x16x32_bf16 a[192:195], v[76:79], v[84:87], a[192:195]\n" \
"ds_read_b64_tr_b16 v[178:179], v39 offset:41984\n" \
"v_accvgpr_read_b32 v203, a79\n" \
"v_mfma_f32_16x16x32_bf16 a[192:195], v[140:143], v[148:151], a[192:195]\n" \
"v_accvgpr_read_b32 v180, a108\n" \
"v_accvgpr_read_b32 v181, a109\n" \
"v_mfma_f32_16x16x32_bf16 a[196:199], v[76:79], v[88:91], a[196:199]\n" \
"v_accvgpr_read_b32 v182, a110\n" \
"v_accvgpr_read_b32 v183, a111\n" \
"v_mfma_f32_16x16x32_bf16 a[196:199], v[140:143], v[152:155], a[196:199]\n" \
"v_cvt_pk_bf16_f32 v200, v200, v201\n" \
"v_cvt_pk_bf16_f32 v201, v202, v203\n" \
"v_mfma_f32_16x16x32_bf16 a[200:203], v[76:79], v[92:95], a[200:203]\n" \
"s_add_u32 s89, s41, 0x0\n" \
"s_nop 0\n" \
"buffer_store_dwordx4 v[192:195], v20, s[80:83], s89 offen offset:1024 sc1\n" \
"v_mfma_f32_16x16x32_bf16 a[200:203], v[140:143], v[156:159], a[200:203]\n" \
"v_cvt_pk_bf16_f32 v202, v180, v181\n" \
"v_cvt_pk_bf16_f32 v203, v182, v183\n" \
"v_mfma_f32_16x16x32_bf16 a[204:207], v[76:79], v[96:99], a[204:207]\n" \
"v_accvgpr_read_b32 v204, a72\n" \
"v_accvgpr_read_b32 v205, a73\n" \
"v_mfma_f32_16x16x32_bf16 a[204:207], v[140:143], v[160:163], a[204:207]\n" \
"v_accvgpr_read_b32 v206, a74\n" \
"v_accvgpr_read_b32 v207, a75\n" \
"v_mfma_f32_16x16x32_bf16 a[236:239], v[80:83], v[96:99], a[236:239]\n" \
"v_accvgpr_read_b32 v180, a104\n" \
"v_accvgpr_read_b32 v181, a105\n" \
"v_mfma_f32_16x16x32_bf16 a[236:239], v[144:147], v[160:163], a[236:239]\n" \
"v_accvgpr_read_b32 v182, a106\n" \
"v_accvgpr_read_b32 v183, a107\n" \
"v_mfma_f32_16x16x32_bf16 a[232:235], v[80:83], v[92:95], a[232:235]\n" \
"s_add_u32 s88, s41, 0x0\n" \
"s_nop 0\n" \
"buffer_store_dwordx4 v[196:199], v20, s[80:83], s88 offen offset:0 sc1\n" \
"v_mfma_f32_16x16x32_bf16 a[232:235], v[144:147], v[156:159], a[232:235]\n" \
"v_cvt_pk_bf16_f32 v204, v204, v205\n" \
"v_cvt_pk_bf16_f32 v205, v206, v207\n" \
"v_mfma_f32_16x16x32_bf16 a[228:231], v[80:83], v[88:91], a[228:231]\n" \
"v_cvt_pk_bf16_f32 v206, v180, v181\n" \
"v_cvt_pk_bf16_f32 v207, v182, v183\n" \
"v_mfma_f32_16x16x32_bf16 a[228:231], v[144:147], v[152:155], a[228:231]\n" \
"v_accvgpr_read_b32 v208, a68\n" \
"v_accvgpr_read_b32 v209, a69\n" \
"v_mfma_f32_16x16x32_bf16 a[224:227], v[80:83], v[84:87], a[224:227]\n" \
"v_accvgpr_read_b32 v210, a70\n" \
"v_accvgpr_read_b32 v211, a71\n" \
"v_mfma_f32_16x16x32_bf16 a[224:227], v[144:147], v[148:151], a[224:227]\n" \
"v_accvgpr_read_b32 v180, a100\n" \
"v_accvgpr_read_b32 v181, a101\n" \
"s_waitcnt vmcnt(5) lgkmcnt(0)\n" \
"s_barrier\n" \
"v_xor_b32_e32 v36, 0x4000, v36\n" \
"v_xor_b32_e64 v32, 0x0, v48\n" \
"v_xor_b32_e32 v37, 0x4000, v37\n" \
"v_xor_b32_e64 v33, 0x20, v48\n" \
"v_xor_b32_e32 v38, 0x4000, v38\n" \
"v_xor_b32_e64 v34, 0x40, v48\n" \
"v_xor_b32_e32 v39, 0x4000, v39\n" \
"v_xor_b32_e32 v35, 0x60, v48\n" \
"v_add_u32_e32 v28, s43, v28\n" \
"v_add_u32_e32 v29, s43, v29\n" \
"v_mfma_f32_16x16x32_bf16 a[144:147], v[68:71], v[100:103], a[144:147]\n" \
"s_add_u32 m0, s40, 0x8000\n" \
"s_add_u32 s85, s39, 0x2000\n" \
"v_mfma_f32_16x16x32_bf16 a[144:147], v[132:135], v[164:167], a[144:147]\n" \
"buffer_load_dwordx4 v20, s[80:83], s85 offen sc1 lds\n" \
"v_mfma_f32_16x16x32_bf16 a[148:151], v[68:71], v[104:107], a[148:151]\n" \
"ds_read_b64_tr_b16 v[84:85], v32 offset:0\n" \
"v_accvgpr_read_b32 v182, a102\n" \
"v_mfma_f32_16x16x32_bf16 a[148:151], v[132:135], v[168:171], a[148:151]\n" \
"s_add_u32 m0, s40, 0x9000\n" \
"s_add_u32 s84, s39, 0x3000\n" \
"v_mfma_f32_16x16x32_bf16 a[152:155], v[68:71], v[108:111], a[152:155]\n" \
"buffer_load_dwordx4 v20, s[80:83], s84 offen sc1 lds\n" \
"v_mfma_f32_16x16x32_bf16 a[152:155], v[132:135], v[172:175], a[152:155]\n" \
"ds_read_b64_tr_b16 v[86:87], v32 offset:512\n" \
"v_accvgpr_read_b32 v183, a103\n" \
"v_mfma_f32_16x16x32_bf16 a[156:159], v[68:71], v[112:115], a[156:159]\n" \
"ds_read_b64_tr_b16 v[148:149], v32 offset:4096\n" \
"v_cvt_pk_bf16_f32 v208, v208, v209\n" \
"v_mfma_f32_16x16x32_bf16 a[156:159], v[132:135], v[176:179], a[156:159]\n" \
"ds_read_b128 v[68:71], v28 offset:8192\n" \
"v_cvt_pk_bf16_f32 v209, v210, v211\n" \
"v_mfma_f32_16x16x32_bf16 a[188:191], v[72:75], v[112:115], a[188:191]\n" \
"ds_read_b128 v[132:135], v29 offset:8192\n" \
"v_cvt_pk_bf16_f32 v210, v180, v181\n" \
"v_mfma_f32_16x16x32_bf16 a[188:191], v[136:139], v[176:179], a[188:191]\n" \
"s_add_u32 m0, s40, 0xb000\n" \
"s_mov_b32 s85, 0x80\n" \
"v_mfma_f32_16x16x32_bf16 a[184:187], v[72:75], v[108:111], a[184:187]\n" \
"buffer_load_dwordx4 v21, s[76:79], s85 offen lds\n" \
"v_mfma_f32_16x16x32_bf16 a[184:187], v[136:139], v[172:175], a[184:187]\n" \
"s_add_u32 m0, s30, 0x8000\n" \
"s_mov_b32 s84, 0x80\n" \
"v_mfma_f32_16x16x32_bf16 a[180:183], v[72:75], v[104:107], a[180:183]\n" \
"buffer_load_dwordx4 v24, s[76:79], s84 offen lds\n" \
"v_mfma_f32_16x16x32_bf16 a[180:183], v[136:139], v[168:171], a[180:183]\n" \
"ds_read_b64_tr_b16 v[150:151], v32 offset:4608\n" \
"v_cvt_pk_bf16_f32 v211, v182, v183\n" \
"v_mfma_f32_16x16x32_bf16 a[176:179], v[72:75], v[100:103], a[176:179]\n" \
"ds_read_b64_tr_b16 v[88:89], v33 offset:0\n" \
"v_accvgpr_read_b32 v212, a64\n" \
"v_mfma_f32_16x16x32_bf16 a[176:179], v[136:139], v[164:167], a[176:179]\n" \
"ds_read_b128 v[72:75], v28 offset:10240\n" \
"v_accvgpr_read_b32 v213, a65\n" \
"v_mfma_f32_16x16x32_bf16 a[208:211], v[76:79], v[100:103], a[208:211]\n" \
"ds_read_b128 v[136:139], v29 offset:10240\n" \
"v_accvgpr_read_b32 v214, a66\n" \
"v_mfma_f32_16x16x32_bf16 a[208:211], v[140:143], v[164:167], a[208:211]\n" \
"s_add_u32 m0, s30, 0x8400\n" \
"s_mov_b32 s85, 0x2480\n" \
"v_mfma_f32_16x16x32_bf16 a[212:215], v[76:79], v[104:107], a[212:215]\n" \
"buffer_load_dwordx4 v25, s[76:79], s85 offen lds\n" \
"v_mfma_f32_16x16x32_bf16 a[212:215], v[140:143], v[168:171], a[212:215]\n" \
"s_add_u32 m0, s31, 0x8000\n" \
"s_mov_b32 s84, 0x80000\n" \
"v_mfma_f32_16x16x32_bf16 a[216:219], v[76:79], v[108:111], a[216:219]\n" \
"buffer_load_dwordx4 v26, s[68:71], s84 offen lds\n" \
"v_mfma_f32_16x16x32_bf16 a[216:219], v[140:143], v[172:175], a[216:219]\n" \
"ds_read_b64_tr_b16 v[90:91], v33 offset:512\n" \
"v_accvgpr_read_b32 v215, a67\n" \
"v_mfma_f32_16x16x32_bf16 a[220:223], v[76:79], v[112:115], a[220:223]\n" \
"ds_read_b64_tr_b16 v[152:153], v33 offset:4096\n" \
"v_accvgpr_read_b32 v180, a96\n" \
"v_mfma_f32_16x16x32_bf16 a[220:223], v[140:143], v[176:179], a[220:223]\n" \
"ds_read_b128 v[76:79], v28 offset:12288\n" \
"v_accvgpr_read_b32 v181, a97\n" \
"v_mfma_f32_16x16x32_bf16 a[252:255], v[80:83], v[112:115], a[252:255]\n" \
"ds_read_b128 v[140:143], v29 offset:12288\n" \
"v_accvgpr_read_b32 v182, a98\n" \
"v_mfma_f32_16x16x32_bf16 a[252:255], v[144:147], v[176:179], a[252:255]\n" \
"s_add_u32 m0, s30, 0x8800\n" \
"s_mov_b32 s85, 0x4880\n" \
"v_mfma_f32_16x16x32_bf16 a[248:251], v[80:83], v[108:111], a[248:251]\n" \
"buffer_load_dwordx4 v24, s[76:79], s85 offen lds\n" \
"v_mfma_f32_16x16x32_bf16 a[248:251], v[144:147], v[172:175], a[248:251]\n" \
"s_add_u32 m0, s30, 0x8c00\n" \
"s_mov_b32 s84, 0x6c80\n" \
"v_mfma_f32_16x16x32_bf16 a[244:247], v[80:83], v[104:107], a[244:247]\n" \
"buffer_load_dwordx4 v25, s[76:79], s84 offen lds\n" \
"v_mfma_f32_16x16x32_bf16 a[244:247], v[144:147], v[168:171], a[244:247]\n" \
"s_add_u32 m0, s31, 0x8400\n" \
"s_mov_b32 s85, 0x88000\n" \
"v_mfma_f32_16x16x32_bf16 a[240:243], v[80:83], v[100:103], a[240:243]\n" \
"buffer_load_dwordx4 v26, s[68:71], s85 offen lds\n" \
"v_mfma_f32_16x16x32_bf16 a[240:243], v[144:147], v[164:167], a[240:243]\n" \
"ds_read_b128 v[80:83], v28 offset:14336\n" \
"v_accvgpr_read_b32 v183, a99\n" \
"v_mfma_f32_16x16x32_bf16 a[80:83], v[60:63], v[100:103], a[80:83]\n" \
"ds_read_b128 v[144:147], v29 offset:14336\n" \
"v_cvt_pk_bf16_f32 v212, v212, v213\n" \
"v_mfma_f32_16x16x32_bf16 a[80:83], v[124:127], v[164:167], a[80:83]\n" \
"s_add_u32 m0, s30, 0x9000\n" \
"s_mov_b32 s84, 0x9080\n" \
"v_mfma_f32_16x16x32_bf16 a[84:87], v[60:63], v[104:107], a[84:87]\n" \
"buffer_load_dwordx4 v24, s[76:79], s84 offen lds\n" \
"v_mfma_f32_16x16x32_bf16 a[84:87], v[124:127], v[168:171], a[84:87]\n" \
"ds_read_b64_tr_b16 v[154:155], v33 offset:4608\n" \
"v_cvt_pk_bf16_f32 v213, v214, v215\n" \
".fill (L_pb + 65248 - .) / 4, 4, 0xBF800000\n" \
"L_pp16:\n" \
"s_cbranch_execz L_pret\n" \
"s_nop 0\n" \
"v_mfma_f32_16x16x32_bf16 a[88:91], v[60:63], v[108:111], a[88:91]\n" \
"s_add_u32 m0, s30, 0x9400\n" \
"s_mov_b32 s85, 0xb480\n" \
"v_mfma_f32_16x16x32_bf16 a[88:91], v[124:127], v[172:175], a[88:91]\n" \
"buffer_load_dwordx4 v25, s[76:79], s85 offen lds\n" \
"v_mfma_f32_16x16x32_bf16 a[92:95], v[60:63], v[112:115], a[92:95]\n" \
"ds_read_b64_tr_b16 v[96:97], v35 offset:0\n" \
"v_cvt_pk_bf16_f32 v214, v180, v181\n" \
"v_mfma_f32_16x16x32_bf16 a[92:95], v[124:127], v[176:179], a[92:95]\n" \
"ds_read_b128 v[60:63], v28 offset:4096\n" \
"v_cvt_pk_bf16_f32 v215, v182, v183\n" \
"v_mfma_f32_16x16x32_bf16 a[124:127], v[64:67], v[112:115], a[124:127]\n" \
"ds_read_b128 v[124:127], v29 offset:4096\n" \
"v_accvgpr_read_b32 v216, a140\n" \
"v_mfma_f32_16x16x32_bf16 a[124:127], v[128:131], v[176:179], a[124:127]\n" \
"s_add_u32 m0, s31, 0x8800\n" \
"s_mov_b32 s84, 0x90000\n" \
"v_mfma_f32_16x16x32_bf16 a[120:123], v[64:67], v[108:111], a[120:123]\n" \
"buffer_load_dwordx4 v27, s[68:71], s84 offen lds\n" \
"v_mfma_f32_16x16x32_bf16 a[120:123], v[128:131], v[172:175], a[120:123]\n" \
"ds_read_b64_tr_b16 v[98:99], v35 offset:512\n" \
"v_accvgpr_read_b32 v217, a141\n" \
"v_mfma_f32_16x16x32_bf16 a[116:119], v[64:67], v[104:107], a[116:119]\n" \
"s_add_u32 m0, s30, 0x9800\n" \
"s_mov_b32 s85, 0xd880\n" \
"v_mfma_f32_16x16x32_bf16 a[116:119], v[128:131], v[168:171], a[116:119]\n" \
"buffer_load_dwordx4 v24, s[76:79], s85 offen lds\n" \
"v_mfma_f32_16x16x32_bf16 a[112:115], v[64:67], v[100:103], a[112:115]\n" \
"ds_read_b64_tr_b16 v[160:161], v35 offset:4096\n" \
"v_accvgpr_read_b32 v218, a142\n" \
"v_mfma_f32_16x16x32_bf16 a[112:115], v[128:131], v[164:167], a[112:115]\n" \
"ds_read_b128 v[64:67], v28 offset:6144\n" \
"v_accvgpr_read_b32 v219, a143\n" \
"v_mfma_f32_16x16x32_bf16 a[16:19], v[52:55], v[100:103], a[16:19]\n" \
"ds_read_b128 v[128:131], v29 offset:6144\n" \
"v_accvgpr_read_b32 v180, a172\n" \
"v_mfma_f32_16x16x32_bf16 a[16:19], v[116:119], v[164:167], a[16:19]\n" \
"ds_read_b64_tr_b16 v[162:163], v35 offset:4608\n" \
"v_accvgpr_read_b32 v181, a173\n" \
"v_mfma_f32_16x16x32_bf16 a[20:23], v[52:55], v[104:107], a[20:23]\n" \
"ds_read_b64_tr_b16 v[92:93], v34 offset:0\n" \
"v_accvgpr_read_b32 v182, a174\n" \
"v_mfma_f32_16x16x32_bf16 a[20:23], v[116:119], v[168:171], a[20:23]\n" \
"ds_read_b64_tr_b16 v[94:95], v34 offset:512\n" \
"v_accvgpr_read_b32 v183, a175\n" \
"v_mfma_f32_16x16x32_bf16 a[24:27], v[52:55], v[108:111], a[24:27]\n" \
"ds_read_b64_tr_b16 v[156:157], v34 offset:4096\n" \
"v_cvt_pk_bf16_f32 v216, v216, v217\n" \
"v_mfma_f32_16x16x32_bf16 a[24:27], v[116:119], v[172:175], a[24:27]\n" \
"ds_read_b64_tr_b16 v[158:159], v34 offset:4608\n" \
"v_cvt_pk_bf16_f32 v217, v218, v219\n" \
"v_mfma_f32_16x16x32_bf16 a[28:31], v[52:55], v[112:115], a[28:31]\n" \
"v_cvt_pk_bf16_f32 v218, v180, v181\n" \
"v_cvt_pk_bf16_f32 v219, v182, v183\n" \
"v_mfma_f32_16x16x32_bf16 a[28:31], v[116:119], v[176:179], a[28:31]\n" \
"ds_read_b128 v[52:55], v28 offset:0\n" \
"v_accvgpr_read_b32 v220, a136\n" \
"v_mfma_f32_16x16x32_bf16 a[60:63], v[56:59], v[112:115], a[60:63]\n" \
"ds_read_b128 v[116:119], v29 offset:0\n" \
"v_accvgpr_read_b32 v221, a137\n" \
"v_mfma_f32_16x16x32_bf16 a[60:63], v[120:123], v[176:179], a[60:63]\n" \
"v_accvgpr_read_b32 v222, a138\n" \
"v_accvgpr_read_b32 v223, a139\n" \
"v_mfma_f32_16x16x32_bf16 a[56:59], v[56:59], v[108:111], a[56:59]\n" \
"v_accvgpr_read_b32 v180, a168\n" \
"v_accvgpr_read_b32 v181, a169\n" \
"v_mfma_f32_16x16x32_bf16 a[56:59], v[120:123], v[172:175], a[56:59]\n" \
"v_accvgpr_read_b32 v182, a170\n" \
"v_accvgpr_read_b32 v183, a171\n" \
"v_mfma_f32_16x16x32_bf16 a[52:55], v[56:59], v[104:107], a[52:55]\n" \
"v_cvt_pk_bf16_f32 v220, v220, v221\n" \
"v_cvt_pk_bf16_f32 v221, v222, v223\n" \
"v_mfma_f32_16x16x32_bf16 a[52:55], v[120:123], v[168:171], a[52:55]\n" \
"v_cvt_pk_bf16_f32 v222, v180, v181\n" \
"v_cvt_pk_bf16_f32 v223, v182, v183\n" \
"v_mfma_f32_16x16x32_bf16 a[48:51], v[56:59], v[100:103], a[48:51]\n" \
"v_accvgpr_read_b32 v224, a132\n" \
"v_accvgpr_read_b32 v225, a133\n" \
"v_mfma_f32_16x16x32_bf16 a[48:51], v[120:123], v[164:167], a[48:51]\n" \
"ds_read_b128 v[56:59], v28 offset:2048\n" \
"ds_read_b128 v[120:123], v29 offset:2048\n" \
"s_waitcnt lgkmcnt(15)\n" \
"s_nop 0\n" \
"v_mfma_f32_16x16x32_bf16 a[0:3], v[84:87], v[68:71], 0\n" \
"s_add_u32 m0, s30, 0x9c00\n" \
"s_mov_b32 s85, 0xfc80\n" \
"v_accvgpr_read_b32 v226, a134\n" \
"v_mfma_f32_16x16x32_bf16 a[0:3], v[148:151], v[132:135], a[0:3]\n" \
"buffer_load_dwordx4 v25, s[76:79], s85 offen lds\n" \
"v_accvgpr_read_b32 v227, a135\n" \
"v_mfma_f32_16x16x32_bf16 a[4:7], v[88:91], v[68:71], 0\n" \
"ds_read_b64_tr_b16 v[100:101], v36 offset:0\n" \
"v_accvgpr_read_b32 v180, a164\n" \
"v_mfma_f32_16x16x32_bf16 a[4:7], v[152:155], v[132:135], a[4:7]\n" \
"s_add_u32 m0, s31, 0x8c00\n" \
"s_mov_b32 s84, 0x98000\n" \
"v_accvgpr_read_b32 v181, a165\n" \
"s_waitcnt lgkmcnt(13)\n" \
"v_mfma_f32_16x16x32_bf16 a[36:39], v[96:99], v[68:71], 0\n" \
"buffer_load_dwordx4 v27, s[68:71], s84 offen lds\n" \
"v_accvgpr_read_b32 v182, a166\n" \
"s_waitcnt lgkmcnt(9)\n" \
"v_mfma_f32_16x16x32_bf16 a[36:39], v[160:163], v[132:135], a[36:39]\n" \
"ds_read_b64_tr_b16 v[102:103], v36 offset:1024\n" \
"v_accvgpr_read_b32 v183, a167\n" \
"s_waitcnt lgkmcnt(8)\n" \
"v_mfma_f32_16x16x32_bf16 a[32:35], v[68:71], v[92:95], 0\n" \
"ds_read_b64_tr_b16 v[164:165], v36 offset:8192\n" \
"v_cvt_pk_bf16_f32 v224, v224, v225\n" \
"s_waitcnt lgkmcnt(7)\n" \
"v_mfma_f32_16x16x32_bf16 a[32:35], v[132:135], v[156:159], a[32:35]\n" \
"ds_read_b64_tr_b16 v[166:167], v36 offset:9216\n" \
"v_cvt_pk_bf16_f32 v225, v226, v227\n" \
"v_mfma_f32_16x16x32_bf16 a[96:99], v[72:75], v[92:95], 0\n" \
"ds_read_b64_tr_b16 v[104:105], v37 offset:0\n" \
"v_cvt_pk_bf16_f32 v226, v180, v181\n" \
"v_mfma_f32_16x16x32_bf16 a[96:99], v[136:139], v[156:159], a[96:99]\n" \
"ds_read_b64_tr_b16 v[106:107], v37 offset:1024\n" \
"v_cvt_pk_bf16_f32 v227, v182, v183\n" \
"v_mfma_f32_16x16x32_bf16 a[100:103], v[96:99], v[72:75], 0\n" \
"ds_read_b64_tr_b16 v[168:169], v37 offset:8192\n" \
"v_accvgpr_read_b32 v228, a128\n" \
"v_mfma_f32_16x16x32_bf16 a[100:103], v[160:163], v[136:139], a[100:103]\n" \
"s_add_u32 s89, s41, 0x2000\n" \
"buffer_store_dwordx4 v[200:203], v20, s[80:83], s89 offen offset:3072 sc1\n" \
"v_mfma_f32_16x16x32_bf16 a[68:71], v[88:91], v[72:75], 0\n" \
"ds_read_b64_tr_b16 v[170:171], v37 offset:9216\n" \
"v_accvgpr_read_b32 v229, a129\n" \
"v_mfma_f32_16x16x32_bf16 a[68:71], v[152:155], v[136:139], a[68:71]\n" \
"ds_read_b64_tr_b16 v[108:109], v38 offset:0\n" \
"v_accvgpr_read_b32 v230, a130\n" \
"v_mfma_f32_16x16x32_bf16 a[64:67], v[84:87], v[72:75], 0\n" \
"ds_read_b64_tr_b16 v[110:111], v38 offset:1024\n" \
"v_accvgpr_read_b32 v231, a131\n" \
"v_mfma_f32_16x16x32_bf16 a[64:67], v[148:151], v[136:139], a[64:67]\n" \
"v_accvgpr_read_b32 v180, a160\n" \
"v_accvgpr_read_b32 v181, a161\n" \
"v_accvgpr_read_b32 v182, a162\n" \
"v_accvgpr_read_b32 v183, a163\n" \
"v_cvt_pk_bf16_f32 v228, v228, v229\n" \
"v_cvt_pk_bf16_f32 v229, v230, v231\n" \
"v_cvt_pk_bf16_f32 v230, v180, v181\n" \
"v_cvt_pk_bf16_f32 v231, v182, v183\n" \
"v_mfma_f32_16x16x32_bf16 a[128:131], v[84:87], v[76:79], 0\n" \
"s_add_u32 s88, s41, 0x2000\n" \
"buffer_store_dwordx4 v[204:207], v20, s[80:83], s88 offen offset:2048 sc1\n" \
"v_mfma_f32_16x16x32_bf16 a[128:131], v[148:151], v[140:143], a[128:131]\n" \
"ds_read_b64_tr_b16 v[172:173], v38 offset:8192\n" \
"v_accvgpr_read_b32 v232, a204\n" \
"v_mfma_f32_16x16x32_bf16 a[132:135], v[88:91], v[76:79], 0\n" \
"ds_read_b64_tr_b16 v[174:175], v38 offset:9216\n" \
"v_accvgpr_read_b32 v233, a205\n" \
"v_mfma_f32_16x16x32_bf16 a[132:135], v[152:155], v[140:143], a[132:135]\n" \
"ds_read_b64_tr_b16 v[112:113], v39 offset:0\n" \
"v_accvgpr_read_b32 v234, a206\n" \
"v_mfma_f32_16x16x32_bf16 a[164:167], v[96:99], v[76:79], 0\n" \
"v_accvgpr_read_b32 v235, a207\n" \
"v_accvgpr_read_b32 v180, a236\n" \
"v_accvgpr_read_b32 v181, a237\n" \
"v_mfma_f32_16x16x32_bf16 a[164:167], v[160:163], v[140:143], a[164:167]\n" \
"ds_read_b64_tr_b16 v[114:115], v39 offset:1024\n" \
"v_accvgpr_read_b32 v182, a238\n" \
"v_mfma_f32_16x16x32_bf16 a[160:163], v[76:79], v[92:95], 0\n" \
"s_add_u32 s89, s41, 0x2000\n" \
"buffer_store_dwordx4 v[208:211], v20, s[80:83], s89 offen offset:1024 sc1\n" \
"v_mfma_f32_16x16x32_bf16 a[160:163], v[140:143], v[156:159], a[160:163]\n" \
"ds_read_b64_tr_b16 v[176:177], v39 offset:8192\n" \
"v_accvgpr_read_b32 v183, a239\n" \
"v_cvt_pk_bf16_f32 v232, v232, v233\n" \
"v_cvt_pk_bf16_f32 v233, v234, v235\n" \
"v_cvt_pk_bf16_f32 v234, v180, v181\n" \
"v_cvt_pk_bf16_f32 v235, v182, v183\n" \
"v_accvgpr_read_b32 v236, a200\n" \
"v_accvgpr_read_b32 v237, a201\n" \
"v_accvgpr_read_b32 v238, a202\n" \
"v_accvgpr_read_b32 v239, a203\n" \
"v_accvgpr_read_b32 v180, a232\n" \
"v_accvgpr_read_b32 v181, a233\n" \
"v_accvgpr_read_b32 v182, a234\n" \
"v_accvgpr_read_b32 v183, a235\n" \
"v_cvt_pk_bf16_f32 v236, v236, v237\n" \
"v_cvt_pk_bf16_f32 v237, v238, v239\n" \
"v_cvt_pk_bf16_f32 v238, v180, v181\n" \
"v_cvt_pk_bf16_f32 v239, v182, v183\n" \
"v_accvgpr_read_b32 v240, a196\n" \
"v_accvgpr_read_b32 v241, a197\n" \
"v_accvgpr_read_b32 v242, a198\n" \
"v_accvgpr_read_b32 v243, a199\n" \
"v_accvgpr_read_b32 v180, a228\n" \
"v_accvgpr_read_b32 v181, a229\n" \
"v_accvgpr_read_b32 v182, a230\n" \
"v_accvgpr_read_b32 v183, a231\n" \
"v_cvt_pk_bf16_f32 v240, v240, v241\n" \
"v_cvt_pk_bf16_f32 v241, v242, v243\n" \
"v_cvt_pk_bf16_f32 v242, v180, v181\n" \
"v_cvt_pk_bf16_f32 v243, v182, v183\n" \
"v_accvgpr_read_b32 v244, a192\n" \
"v_accvgpr_read_b32 v245, a193\n" \
"v_accvgpr_read_b32 v246, a194\n" \
"v_accvgpr_read_b32 v247, a195\n" \
"v_accvgpr_read_b32 v180, a224\n" \
"v_accvgpr_read_b32 v181, a225\n" \
"v_accvgpr_read_b32 v182, a226\n" \
"v_accvgpr_read_b32 v183, a227\n" \
"v_cvt_pk_bf16_f32 v244, v244, v245\n" \
"v_cvt_pk_bf16_f32 v245, v246, v247\n" \
"v_cvt_pk_bf16_f32 v246, v180, v181\n" \
"v_cvt_pk_bf16_f32 v247, v182, v183\n" \
"v_mfma_f32_16x16x32_bf16 a[224:227], v[80:83], v[92:95], 0\n" \
"ds_read_b64_tr_b16 v[178:179], v39 offset:9216\n" \
"v_accvgpr_read_b32 v184, a144\n" \
"v_mfma_f32_16x16x32_bf16 a[224:227], v[144:147], v[156:159], a[224:227]\n" \
"v_accvgpr_read_b32 v185, a145\n" \
"v_accvgpr_read_b32 v186, a146\n" \
"v_accvgpr_read_b32 v187, a147\n" \
"v_mfma_f32_16x16x32_bf16 a[228:231], v[96:99], v[80:83], 0\n" \
"v_accvgpr_read_b32 v180, a176\n" \
"v_accvgpr_read_b32 v181, a177\n" \
"v_accvgpr_read_b32 v182, a178\n" \
"v_mfma_f32_16x16x32_bf16 a[228:231], v[160:163], v[144:147], a[228:231]\n" \
"s_add_u32 s88, s41, 0x2000\n" \
"buffer_store_dwordx4 v[212:215], v20, s[80:83], s88 offen offset:0 sc1\n" \
"v_mfma_f32_16x16x32_bf16 a[196:199], v[88:91], v[80:83], 0\n" \
"v_accvgpr_read_b32 v183, a179\n" \
"v_cvt_pk_bf16_f32 v184, v184, v185\n" \
"v_cvt_pk_bf16_f32 v185, v186, v187\n" \
"v_mfma_f32_16x16x32_bf16 a[196:199], v[152:155], v[144:147], a[196:199]\n" \
"v_cvt_pk_bf16_f32 v186, v180, v181\n" \
"v_cvt_pk_bf16_f32 v187, v182, v183\n" \
"v_accvgpr_read_b32 v188, a148\n" \
"v_mfma_f32_16x16x32_bf16 a[192:195], v[84:87], v[80:83], 0\n" \
"v_accvgpr_read_b32 v189, a149\n" \
"v_accvgpr_read_b32 v190, a150\n" \
"v_accvgpr_read_b32 v191, a151\n" \
"v_mfma_f32_16x16x32_bf16 a[192:195], v[148:151], v[144:147], a[192:195]\n" \
"v_accvgpr_read_b32 v180, a180\n" \
"v_accvgpr_read_b32 v181, a181\n" \
"v_accvgpr_read_b32 v182, a182\n" \
"v_mfma_f32_16x16x32_bf16 a[8:11], v[88:91], v[64:67], 0\n" \
"v_accvgpr_read_b32 v183, a183\n" \
"v_cvt_pk_bf16_f32 v188, v188, v189\n" \
"v_cvt_pk_bf16_f32 v189, v190, v191\n" \
"v_mfma_f32_16x16x32_bf16 a[8:11], v[152:155], v[128:131], a[8:11]\n" \
"v_cvt_pk_bf16_f32 v190, v180, v181\n" \
"v_cvt_pk_bf16_f32 v191, v182, v183\n" \
"v_accvgpr_read_b32 v192, a80\n" \
"v_mfma_f32_16x16x32_bf16 a[40:43], v[96:99], v[64:67], 0\n" \
"v_accvgpr_read_b32 v193, a81\n" \
"v_accvgpr_read_b32 v194, a82\n" \
"v_accvgpr_read_b32 v195, a83\n" \
"v_mfma_f32_16x16x32_bf16 a[40:43], v[160:163], v[128:131], a[40:43]\n" \
"v_accvgpr_read_b32 v180, a112\n" \
"v_accvgpr_read_b32 v181, a113\n" \
"v_accvgpr_read_b32 v182, a114\n" \
"v_mfma_f32_16x16x32_bf16 a[12:15], v[64:67], v[92:95], 0\n" \
"v_accvgpr_read_b32 v183, a115\n" \
"v_cvt_pk_bf16_f32 v192, v192, v193\n" \
"v_cvt_pk_bf16_f32 v193, v194, v195\n" \
"v_mfma_f32_16x16x32_bf16 a[12:15], v[128:131], v[156:159], a[12:15]\n" \
"v_cvt_pk_bf16_f32 v194, v180, v181\n" \
"v_cvt_pk_bf16_f32 v195, v182, v183\n" \
"v_accvgpr_read_b32 v196, a84\n" \
"s_waitcnt vmcnt(4) lgkmcnt(0)\n" \
"s_barrier\n" \
"v_mfma_f32_16x16x32_bf16 a[72:75], v[100:103], v[76:79], 0\n" \
"s_add_u32 m0, s40, 0x0\n" \
"s_nop 0\n" \
"s_add_u32 s85, s39, 0x4000\n" \
"v_accvgpr_read_b32 v197, a85\n" \
"v_mfma_f32_16x16x32_bf16 a[72:75], v[164:167], v[140:143], a[72:75]\n" \
"buffer_load_dwordx4 v20, s[80:83], s85 offen sc1 lds\n" \
"v_accvgpr_read_b32 v198, a86\n" \
"v_mfma_f32_16x16x32_bf16 a[76:79], v[104:107], v[76:79], 0\n" \
"ds_read_b64_tr_b16 v[84:85], v32 offset:32768\n" \
"v_accvgpr_read_b32 v199, a87\n" \
"v_mfma_f32_16x16x32_bf16 a[76:79], v[168:171], v[140:143], a[76:79]\n" \
"ds_read_b64_tr_b16 v[86:87], v32 offset:33280\n" \
"v_accvgpr_read_b32 v180, a116\n" \
"v_mfma_f32_16x16x32_bf16 a[104:107], v[108:111], v[76:79], 0\n" \
"s_add_u32 m0, s40, 0x1000\n" \
"s_add_u32 s84, s39, 0x5000\n" \
"v_accvgpr_read_b32 v181, a117\n" \
"v_mfma_f32_16x16x32_bf16 a[104:107], v[172:175], v[140:143], a[104:107]\n" \
"buffer_load_dwordx4 v20, s[80:83], s84 offen sc1 lds\n" \
"v_accvgpr_read_b32 v182, a118\n" \
"v_mfma_f32_16x16x32_bf16 a[108:111], v[112:115], v[76:79], 0\n" \
"ds_read_b64_tr_b16 v[148:149], v32 offset:36864\n" \
"v_accvgpr_read_b32 v183, a119\n" \
"v_mfma_f32_16x16x32_bf16 a[108:111], v[176:179], v[140:143], a[108:111]\n" \
"ds_read_b128 v[76:79], v28 offset:45056\n" \
"v_cvt_pk_bf16_f32 v196, v196, v197\n" \
"v_mfma_f32_16x16x32_bf16 a[172:175], v[112:115], v[80:83], 0\n" \
"ds_read_b128 v[140:143], v29 offset:45056\n" \
"v_cvt_pk_bf16_f32 v197, v198, v199\n" \
"v_mfma_f32_16x16x32_bf16 a[172:175], v[176:179], v[144:147], a[172:175]\n" \
"s_add_u32 m0, s40, 0x3000\n" \
"s_mov_b32 s85, 0x100\n" \
"v_cvt_pk_bf16_f32 v198, v180, v181\n" \
"v_mfma_f32_16x16x32_bf16 a[168:171], v[108:111], v[80:83], 0\n" \
"buffer_load_dwordx4 v21, s[76:79], s85 offen lds\n" \
"v_cvt_pk_bf16_f32 v199, v182, v183\n" \
"v_mfma_f32_16x16x32_bf16 a[168:171], v[172:175], v[144:147], a[168:171]\n" \
"ds_read_b64_tr_b16 v[150:151], v32 offset:37376\n" \
"v_accvgpr_read_b32 v252, a88\n" \
"v_mfma_f32_16x16x32_bf16 a[140:143], v[104:107], v[80:83], 0\n" \
"s_add_u32 m0, s30, 0x0\n" \
"s_nop 0\n" \
"s_mov_b32 s84, 0x100\n" \
"v_accvgpr_read_b32 v253, a89\n" \
"v_mfma_f32_16x16x32_bf16 a[140:143], v[168:171], v[144:147], a[140:143]\n" \
"buffer_load_dwordx4 v24, s[76:79], s84 offen lds\n" \
"v_accvgpr_read_b32 v254, a90\n" \
"v_mfma_f32_16x16x32_bf16 a[136:139], v[100:103], v[80:83], 0\n" \
"ds_read_b64_tr_b16 v[88:89], v33 offset:32768\n" \
"v_accvgpr_read_b32 v255, a91\n" \
"v_mfma_f32_16x16x32_bf16 a[136:139], v[164:167], v[144:147], a[136:139]\n" \
"ds_read_b128 v[80:83], v28 offset:47104\n" \
"v_accvgpr_read_b32 v180, a120\n" \
"v_mfma_f32_16x16x32_bf16 a[144:147], v[100:103], v[68:71], 0\n" \
"ds_read_b128 v[144:147], v29 offset:47104\n" \
"v_accvgpr_read_b32 v181, a121\n" \
"v_mfma_f32_16x16x32_bf16 a[144:147], v[164:167], v[132:135], a[144:147]\n" \
"s_add_u32 m0, s30, 0x400\n" \
"s_mov_b32 s85, 0x2500\n" \
"v_accvgpr_read_b32 v182, a122\n" \
"v_mfma_f32_16x16x32_bf16 a[148:151], v[104:107], v[68:71], 0\n" \
"buffer_load_dwordx4 v25, s[76:79], s85 offen lds\n" \
"v_accvgpr_read_b32 v183, a123\n" \
"v_mfma_f32_16x16x32_bf16 a[148:151], v[168:171], v[132:135], a[148:151]\n" \
"ds_read_b64_tr_b16 v[90:91], v33 offset:33280\n" \
"v_cvt_pk_bf16_f32 v252, v252, v253\n" \
"v_mfma_f32_16x16x32_bf16 a[200:203], v[108:111], v[68:71], 0\n" \
"ds_read_b64_tr_b16 v[152:153], v33 offset:36864\n" \
"v_cvt_pk_bf16_f32 v253, v254, v255\n" \
"v_mfma_f32_16x16x32_bf16 a[200:203], v[172:175], v[132:135], a[200:203]\n" \
"s_add_u32 m0, s31, 0x0\n" \
"s_nop 0\n" \
"s_mov_b32 s84, 0x100000\n" \
"v_cvt_pk_bf16_f32 v254, v180, v181\n" \
"v_mfma_f32_16x16x32_bf16 a[204:207], v[112:115], v[68:71], 0\n" \
"buffer_load_dwordx4 v26, s[68:71], s84 offen lds\n" \
"v_cvt_pk_bf16_f32 v255, v182, v183\n" \
"v_mfma_f32_16x16x32_bf16 a[204:207], v[176:179], v[132:135], a[204:207]\n" \
"ds_read_b128 v[68:71], v28 offset:40960\n" \
"v_accvgpr_write_b32 a44, v252\n" \
"v_mfma_f32_16x16x32_bf16 a[236:239], v[112:115], v[72:75], 0\n" \
"ds_read_b128 v[132:135], v29 offset:40960\n" \
"v_accvgpr_write_b32 a45, v253\n" \
"v_mfma_f32_16x16x32_bf16 a[236:239], v[176:179], v[136:139], a[236:239]\n" \
"s_add_u32 m0, s30, 0x800\n" \
"s_mov_b32 s85, 0x4900\n" \
"v_accvgpr_write_b32 a46, v254\n" \
"v_mfma_f32_16x16x32_bf16 a[232:235], v[108:111], v[72:75], 0\n" \
"buffer_load_dwordx4 v24, s[76:79], s85 offen lds\n" \
"v_accvgpr_write_b32 a47, v255\n" \
"v_mfma_f32_16x16x32_bf16 a[232:235], v[172:175], v[136:139], a[232:235]\n" \
"ds_read_b64_tr_b16 v[154:155], v33 offset:37376\n" \
"v_accvgpr_read_b32 v252, a92\n" \
"v_mfma_f32_16x16x32_bf16 a[180:183], v[104:107], v[72:75], 0\n" \
"ds_read_b64_tr_b16 v[96:97], v35 offset:32768\n" \
"v_accvgpr_read_b32 v253, a93\n" \
"v_mfma_f32_16x16x32_bf16 a[180:183], v[168:171], v[136:139], a[180:183]\n" \
"ds_read_b64_tr_b16 v[98:99], v35 offset:33280\n" \
"v_accvgpr_read_b32 v254, a94\n" \
"v_mfma_f32_16x16x32_bf16 a[176:179], v[100:103], v[72:75], 0\n" \
"v_accvgpr_read_b32 v255, a95\n" \
"v_accvgpr_read_b32 v180, a124\n" \
"v_accvgpr_read_b32 v181, a125\n" \
"v_mfma_f32_16x16x32_bf16 a[176:179], v[164:167], v[136:139], a[176:179]\n" \
"ds_read_b128 v[72:75], v28 offset:43008\n" \
"v_accvgpr_read_b32 v182, a126\n" \
"v_mfma_f32_16x16x32_bf16 a[80:83], v[100:103], v[60:63], 0\n" \
"ds_read_b128 v[136:139], v29 offset:43008\n" \
"v_accvgpr_read_b32 v183, a127\n" \
"v_mfma_f32_16x16x32_bf16 a[80:83], v[164:167], v[124:127], a[80:83]\n" \
"s_add_u32 m0, s30, 0xc00\n" \
"s_mov_b32 s84, 0x6d00\n" \
"v_cvt_pk_bf16_f32 v252, v252, v253\n" \
"v_mfma_f32_16x16x32_bf16 a[84:87], v[104:107], v[60:63], 0\n" \
"buffer_load_dwordx4 v25, s[76:79], s84 offen lds\n" \
"v_cvt_pk_bf16_f32 v253, v254, v255\n" \
"v_mfma_f32_16x16x32_bf16 a[84:87], v[168:171], v[124:127], a[84:87]\n" \
"s_add_u32 m0, s31, 0x400\n" \
"s_mov_b32 s85, 0x108000\n" \
"v_cvt_pk_bf16_f32 v254, v180, v181\n" \
"v_mfma_f32_16x16x32_bf16 a[88:91], v[108:111], v[60:63], 0\n" \
"buffer_load_dwordx4 v26, s[68:71], s85 offen lds\n" \
"v_cvt_pk_bf16_f32 v255, v182, v183\n" \
"v_mfma_f32_16x16x32_bf16 a[88:91], v[172:175], v[124:127], a[88:91]\n" \
"ds_read_b64_tr_b16 v[160:161], v35 offset:36864\n" \
"v_accvgpr_read_b32 v200, a16\n" \
"v_mfma_f32_16x16x32_bf16 a[92:95], v[112:115], v[60:63], 0\n" \
"ds_read_b64_tr_b16 v[162:163], v35 offset:37376\n" \
"v_accvgpr_read_b32 v201, a17\n" \
"v_mfma_f32_16x16x32_bf16 a[92:95], v[176:179], v[124:127], a[92:95]\n" \
"ds_read_b128 v[60:63], v28 offset:36864\n" \
"v_accvgpr_read_b32 v202, a18\n" \
"v_mfma_f32_16x16x32_bf16 a[124:127], v[112:115], v[64:67], 0\n" \
"ds_read_b128 v[124:127], v29 offset:36864\n" \
"v_accvgpr_read_b32 v203, a19\n" \
"v_mfma_f32_16x16x32_bf16 a[124:127], v[176:179], v[128:131], a[124:127]\n" \
"s_add_u32 m0, s30, 0x1000\n" \
"s_mov_b32 s84, 0x9100\n" \
"v_accvgpr_read_b32 v180, a48\n" \
"v_mfma_f32_16x16x32_bf16 a[120:123], v[108:111], v[64:67], 0\n" \
"buffer_load_dwordx4 v24, s[76:79], s84 offen lds\n" \
"v_accvgpr_read_b32 v181, a49\n" \
"v_mfma_f32_16x16x32_bf16 a[120:123], v[172:175], v[128:131], a[120:123]\n" \
"s_add_u32 m0, s30, 0x1400\n" \
"s_mov_b32 s85, 0xb500\n" \
"v_accvgpr_read_b32 v182, a50\n" \
"v_mfma_f32_16x16x32_bf16 a[116:119], v[104:107], v[64:67], 0\n" \
"buffer_load_dwordx4 v25, s[76:79], s85 offen lds\n" \
"v_accvgpr_read_b32 v183, a51\n" \
"v_mfma_f32_16x16x32_bf16 a[116:119], v[168:171], v[128:131], a[116:119]\n" \
"ds_read_b64_tr_b16 v[92:93], v34 offset:32768\n" \
"v_cvt_pk_bf16_f32 v200, v200, v201\n" \
"v_mfma_f32_16x16x32_bf16 a[112:115], v[100:103], v[64:67], 0\n" \
"ds_read_b64_tr_b16 v[94:95], v34 offset:33280\n" \
"v_cvt_pk_bf16_f32 v201, v202, v203\n" \
"v_mfma_f32_16x16x32_bf16 a[112:115], v[164:167], v[128:131], a[112:115]\n" \
"ds_read_b128 v[64:67], v28 offset:38912\n" \
"v_cvt_pk_bf16_f32 v202, v180, v181\n" \
"v_cvt_pk_bf16_f32 v203, v182, v183\n" \
"v_mfma_f32_16x16x32_bf16 a[16:19], v[100:103], v[52:55], 0\n" \
"ds_read_b128 v[128:131], v29 offset:38912\n" \
"v_accvgpr_read_b32 v204, a20\n" \
"v_mfma_f32_16x16x32_bf16 a[16:19], v[164:167], v[116:119], a[16:19]\n" \
"s_add_u32 m0, s31, 0x800\n" \
"s_mov_b32 s84, 0x110000\n" \
"v_accvgpr_read_b32 v205, a21\n" \
"v_accvgpr_read_b32 v206, a22\n" \
"v_accvgpr_read_b32 v207, a23\n" \
"v_accvgpr_read_b32 v180, a52\n" \
"v_accvgpr_read_b32 v181, a53\n" \
"v_accvgpr_read_b32 v182, a54\n" \
"v_accvgpr_read_b32 v183, a55\n" \
"v_cvt_pk_bf16_f32 v204, v204, v205\n" \
"v_cvt_pk_bf16_f32 v205, v206, v207\n" \
"v_cvt_pk_bf16_f32 v206, v180, v181\n" \
"v_cvt_pk_bf16_f32 v207, v182, v183\n" \
"v_mfma_f32_16x16x32_bf16 a[20:23], v[104:107], v[52:55], 0\n" \
"buffer_load_dwordx4 v27, s[68:71], s84 offen lds\n" \
"v_accvgpr_read_b32 v208, a24\n" \
"v_mfma_f32_16x16x32_bf16 a[20:23], v[168:171], v[116:119], a[20:23]\n" \
"s_add_u32 m0, s30, 0x1800\n" \
"s_mov_b32 s85, 0xd900\n" \
"v_accvgpr_read_b32 v209, a25\n" \
"v_accvgpr_read_b32 v210, a26\n" \
"v_accvgpr_read_b32 v211, a27\n" \
"v_accvgpr_read_b32 v180, a56\n" \
"v_accvgpr_read_b32 v181, a57\n" \
"v_accvgpr_read_b32 v182, a58\n" \
"v_accvgpr_read_b32 v183, a59\n" \
"v_cvt_pk_bf16_f32 v208, v208, v209\n" \
"v_cvt_pk_bf16_f32 v209, v210, v211\n" \
"v_cvt_pk_bf16_f32 v210, v180, v181\n" \
"v_cvt_pk_bf16_f32 v211, v182, v183\n" \
"v_mfma_f32_16x16x32_bf16 a[24:27], v[108:111], v[52:55], 0\n" \
"buffer_load_dwordx4 v24, s[76:79], s85 offen lds\n" \
"v_accvgpr_read_b32 v212, a28\n" \
"v_mfma_f32_16x16x32_bf16 a[24:27], v[172:175], v[116:119], a[24:27]\n" \
"ds_read_b64_tr_b16 v[156:157], v34 offset:36864\n" \
"v_accvgpr_read_b32 v213, a29\n" \
"v_accvgpr_read_b32 v214, a30\n" \
"v_accvgpr_read_b32 v215, a31\n" \
"v_accvgpr_read_b32 v180, a60\n" \
"v_accvgpr_read_b32 v181, a61\n" \
"v_accvgpr_read_b32 v182, a62\n" \
"v_accvgpr_read_b32 v183, a63\n" \
"v_cvt_pk_bf16_f32 v212, v212, v213\n" \
"v_cvt_pk_bf16_f32 v213, v214, v215\n" \
"v_cvt_pk_bf16_f32 v214, v180, v181\n" \
"v_cvt_pk_bf16_f32 v215, v182, v183\n" \
"v_mfma_f32_16x16x32_bf16 a[28:31], v[112:115], v[52:55], 0\n" \
"ds_read_b64_tr_b16 v[158:159], v34 offset:37376\n" \
"v_mfma_f32_16x16x32_bf16 a[28:31], v[176:179], v[116:119], a[28:31]\n" \
"ds_read_b128 v[52:55], v28 offset:32768\n" \
"v_mfma_f32_16x16x32_bf16 a[60:63], v[112:115], v[56:59], 0\n" \
"ds_read_b128 v[116:119], v29 offset:32768\n" \
"v_mfma_f32_16x16x32_bf16 a[60:63], v[176:179], v[120:123], a[60:63]\n" \
"v_mfma_f32_16x16x32_bf16 a[56:59], v[108:111], v[56:59], 0\n" \
"v_mfma_f32_16x16x32_bf16 a[56:59], v[172:175], v[120:123], a[56:59]\n" \
"v_mfma_f32_16x16x32_bf16 a[52:55], v[104:107], v[56:59], 0\n" \
"v_mfma_f32_16x16x32_bf16 a[52:55], v[168:171], v[120:123], a[52:55]\n" \
"v_mfma_f32_16x16x32_bf16 a[48:51], v[100:103], v[56:59], 0\n" \
"v_mfma_f32_16x16x32_bf16 a[48:51], v[164:167], v[120:123], a[48:51]\n" \
"ds_read_b128 v[56:59], v28 offset:34816\n" \
"ds_read_b128 v[120:123], v29 offset:34816\n" \
"s_waitcnt lgkmcnt(15)\n" \
"v_mfma_f32_16x16x32_bf16 a[0:3], v[84:87], v[68:71], a[0:3]\n" \
"s_add_u32 m0, s30, 0x1c00\n" \
"s_mov_b32 s85, 0xfd00\n" \
"v_mfma_f32_16x16x32_bf16 a[0:3], v[148:151], v[132:135], a[0:3]\n" \
"buffer_load_dwordx4 v25, s[76:79], s85 offen lds\n" \
"v_mfma_f32_16x16x32_bf16 a[4:7], v[88:91], v[68:71], a[4:7]\n" \
"ds_read_b64_tr_b16 v[100:101], v36 offset:32768\n" \
"v_mov_b32_e32 v11, 0\n" \
"v_mfma_f32_16x16x32_bf16 a[4:7], v[152:155], v[132:135], a[4:7]\n" \
"s_add_u32 m0, s31, 0xc00\n" \
"s_mov_b32 s84, 0x118000\n" \
"v_mfma_f32_16x16x32_bf16 a[36:39], v[96:99], v[68:71], a[36:39]\n" \
"buffer_load_dwordx4 v27, s[68:71], s84 offen lds\n" \
"s_waitcnt lgkmcnt(13)\n" \
"v_mfma_f32_16x16x32_bf16 a[36:39], v[160:163], v[132:135], a[36:39]\n" \
"ds_read_b64_tr_b16 v[102:103], v36 offset:33792\n" \
"s_and_b32 s6, s2, 7\n" \
"s_waitcnt lgkmcnt(10)\n" \
"v_mfma_f32_16x16x32_bf16 a[32:35], v[68:71], v[92:95], a[32:35]\n" \
"ds_read_b64_tr_b16 v[164:165], v36 offset:40960\n" \
"s_lshl_b32 s7, s3, 1\n" \
"s_waitcnt lgkmcnt(7)\n" \
"v_mfma_f32_16x16x32_bf16 a[32:35], v[132:135], v[156:159], a[32:35]\n" \
"ds_read_b64_tr_b16 v[166:167], v36 offset:41984\n" \
"s_lshr_b32 s4, s2, 5\n" \
"v_mfma_f32_16x16x32_bf16 a[96:99], v[72:75], v[92:95], a[96:99]\n" \
"ds_read_b64_tr_b16 v[104:105], v37 offset:32768\n" \
"s_lshl_b32 s5, s6, 2\n" \
"v_mfma_f32_16x16x32_bf16 a[96:99], v[136:139], v[156:159], a[96:99]\n" \
"ds_read_b64_tr_b16 v[106:107], v37 offset:33792\n" \
"s_bfe_u32 s8, s2, 0x20003\n" \
"v_mfma_f32_16x16x32_bf16 a[100:103], v[96:99], v[72:75], a[100:103]\n" \
".fill (L_pb + 69344 - .) / 4, 4, 0xBF800000\n" \
"L_pp17:\n" \
"s_cbranch_execz L_pret\n" \
"ds_read_b64_tr_b16 v[168:169], v37 offset:40960\n" \
"s_and_b32 s7, s7, 4\n" \
"v_mfma_f32_16x16x32_bf16 a[100:103], v[160:163], v[136:139], a[100:103]\n" \
"s_add_u32 s89, s41, 0x3000\n" \
"buffer_store_dwordx4 v[192:195], v20, s[80:83], s89 offen offset:0 sc1\n" \
"v_mfma_f32_16x16x32_bf16 a[68:71], v[88:91], v[72:75], a[68:71]\n" \
"ds_read_b64_tr_b16 v[170:171], v37 offset:41984\n" \
"s_and_b32 s9, s3, 1\n" \
"v_mfma_f32_16x16x32_bf16 a[68:71], v[152:155], v[136:139], a[68:71]\n" \
"ds_read_b64_tr_b16 v[108:109], v38 offset:32768\n" \
"s_or_b32 s13, s5, s8\n" \
"v_mfma_f32_16x16x32_bf16 a[64:67], v[84:87], v[72:75], a[64:67]\n" \
"ds_read_b64_tr_b16 v[110:111], v38 offset:33792\n" \
"s_or_b32 s9, s7, s9\n" \
"v_mfma_f32_16x16x32_bf16 a[64:67], v[148:151], v[136:139], a[64:67]\n" \
"s_mul_i32 s7, s4, 0xa00\n" \
"s_add_i32 s7, s7, 0x5bc1000\n" \
"v_mfma_f32_16x16x32_bf16 a[128:131], v[84:87], v[76:79], a[128:131]\n" \
"s_add_u32 s88, s41, 0x3000\n" \
"buffer_store_dwordx4 v[196:199], v20, s[80:83], s88 offen offset:1024 sc1\n" \
"v_mfma_f32_16x16x32_bf16 a[128:131], v[148:151], v[140:143], a[128:131]\n" \
"ds_read_b64_tr_b16 v[172:173], v38 offset:40960\n" \
"s_lshl_b32 s10, s13, 2\n" \
"v_mfma_f32_16x16x32_bf16 a[132:135], v[88:91], v[76:79], a[132:135]\n" \
"ds_read_b64_tr_b16 v[174:175], v38 offset:41984\n" \
"s_or_b32 s10, s10, s7\n" \
"v_mfma_f32_16x16x32_bf16 a[132:135], v[152:155], v[140:143], a[132:135]\n" \
"ds_read_b64_tr_b16 v[112:113], v39 offset:32768\n" \
"v_writelane_b32 v11, s10, 0\n" \
"v_mfma_f32_16x16x32_bf16 a[164:167], v[96:99], v[76:79], a[164:167]\n" \
"s_lshl_b32 s14, s4, 3\n" \
"v_writelane_b32 v11, s7, 1\n" \
"v_mfma_f32_16x16x32_bf16 a[164:167], v[160:163], v[140:143], a[164:167]\n" \
"ds_read_b64_tr_b16 v[114:115], v39 offset:33792\n" \
"s_or_b32 s7, s14, s6\n" \
"v_mfma_f32_16x16x32_bf16 a[160:163], v[76:79], v[92:95], a[160:163]\n" \
"s_add_u32 s89, s41, 0x3000\n" \
"buffer_store_dwordx4 a[44:47], v20, s[80:83], s89 offen offset:2048 sc1\n" \
"v_mfma_f32_16x16x32_bf16 a[160:163], v[140:143], v[156:159], a[160:163]\n" \
"ds_read_b64_tr_b16 v[176:177], v39 offset:40960\n" \
"s_lshl_b32 s7, s7, 10\n" \
"v_mfma_f32_16x16x32_bf16 a[224:227], v[80:83], v[92:95], a[224:227]\n" \
"ds_read_b64_tr_b16 v[178:179], v39 offset:41984\n" \
"s_lshl_b32 s10, s3, 8\n" \
"v_mfma_f32_16x16x32_bf16 a[224:227], v[144:147], v[156:159], a[224:227]\n" \
"s_or_b32 s7, s7, s10\n" \
"s_or_b32 s10, s7, 0x5c00200\n" \
"v_mfma_f32_16x16x32_bf16 a[228:231], v[96:99], v[80:83], a[228:231]\n" \
"v_writelane_b32 v11, s10, 2\n" \
"s_lshl_b32 s10, s8, 2\n" \
"v_mfma_f32_16x16x32_bf16 a[228:231], v[160:163], v[144:147], a[228:231]\n" \
"s_add_u32 s88, s41, 0x3000\n" \
"buffer_store_dwordx4 v[252:255], v20, s[80:83], s88 offen offset:3072 sc1\n" \
"v_mfma_f32_16x16x32_bf16 a[196:199], v[88:91], v[80:83], a[196:199]\n" \
"s_or_b32 s11, s10, s7\n" \
"s_or_b32 s11, s11, 0x5c00000\n" \
"v_mfma_f32_16x16x32_bf16 a[196:199], v[152:155], v[144:147], a[196:199]\n" \
"v_writelane_b32 v11, s11, 3\n" \
"s_or_b32 s7, s7, 0x5c00080\n" \
"v_mfma_f32_16x16x32_bf16 a[192:195], v[84:87], v[80:83], a[192:195]\n" \
"v_writelane_b32 v11, s7, 4\n" \
"s_or_b32 s7, s7, s10\n" \
"v_mfma_f32_16x16x32_bf16 a[192:195], v[148:151], v[144:147], a[192:195]\n" \
"s_add_i32 s10, s3, s2\n" \
"v_writelane_b32 v11, s7, 5\n" \
"v_mfma_f32_16x16x32_bf16 a[8:11], v[88:91], v[64:67], a[8:11]\n" \
"s_mov_b32 s7, 2.0\n" \
"s_mov_b32 s11, 0x5bc0000\n" \
"v_mfma_f32_16x16x32_bf16 a[8:11], v[152:155], v[128:131], a[8:11]\n" \
"s_cmp_lg_u32 s10, 0\n" \
"s_cselect_b32 s10, s7, s11\n" \
"v_mfma_f32_16x16x32_bf16 a[40:43], v[96:99], v[64:67], a[40:43]\n" \
"s_lshr_b32 s15, s2, 3\n" \
"v_writelane_b32 v11, s10, 53\n" \
"v_mfma_f32_16x16x32_bf16 a[40:43], v[160:163], v[128:131], a[40:43]\n" \
"s_lshl_b32 s10, s6, 5\n" \
"s_min_u32 s16, s3, 2\n" \
"v_mfma_f32_16x16x32_bf16 a[12:15], v[64:67], v[92:95], a[12:15]\n" \
"s_mul_i32 s12, s8, 0x120\n" \
"v_mfma_f32_16x16x32_bf16 a[12:15], v[128:131], v[156:159], a[12:15]\n" \
"s_waitcnt vmcnt(4) lgkmcnt(0)\n" \
"s_barrier\n" \
"v_mfma_f32_16x16x32_bf16 a[144:147], v[100:103], v[68:71], a[144:147]\n" \
"s_add_u32 m0, s40, 0x8000\n" \
"s_add_u32 s85, s39, 0x6000\n" \
"v_mfma_f32_16x16x32_bf16 a[144:147], v[164:167], v[132:135], a[144:147]\n" \
"buffer_load_dwordx4 v20, s[80:83], s85 offen sc1 lds\n" \
"v_mfma_f32_16x16x32_bf16 a[148:151], v[104:107], v[68:71], a[148:151]\n" \
"ds_read_b64_tr_b16 v[84:85], v32 offset:0\n" \
"v_mfma_f32_16x16x32_bf16 a[148:151], v[168:171], v[132:135], a[148:151]\n" \
"ds_read_b64_tr_b16 v[86:87], v32 offset:512\n" \
"v_mfma_f32_16x16x32_bf16 a[200:203], v[108:111], v[68:71], a[200:203]\n" \
"s_add_u32 m0, s40, 0x9000\n" \
"s_add_u32 s84, s39, 0x7000\n" \
"v_mfma_f32_16x16x32_bf16 a[200:203], v[172:175], v[132:135], a[200:203]\n" \
"buffer_load_dwordx4 v20, s[80:83], s84 offen sc1 lds\n" \
"v_mfma_f32_16x16x32_bf16 a[204:207], v[112:115], v[68:71], a[204:207]\n" \
"ds_read_b64_tr_b16 v[148:149], v32 offset:4096\n" \
"v_mfma_f32_16x16x32_bf16 a[204:207], v[176:179], v[132:135], a[204:207]\n" \
"ds_read_b128 v[68:71], v28 offset:8192\n" \
"v_mfma_f32_16x16x32_bf16 a[236:239], v[112:115], v[72:75], a[236:239]\n" \
"ds_read_b128 v[132:135], v29 offset:8192\n" \
"v_mfma_f32_16x16x32_bf16 a[236:239], v[176:179], v[136:139], a[236:239]\n" \
"s_add_u32 m0, s40, 0xb000\n" \
"s_mov_b32 s85, 0x180\n" \
"v_mfma_f32_16x16x32_bf16 a[232:235], v[108:111], v[72:75], a[232:235]\n" \
"buffer_load_dwordx4 v21, s[76:79], s85 offen lds\n" \
"v_mfma_f32_16x16x32_bf16 a[232:235], v[172:175], v[136:139], a[232:235]\n" \
"ds_read_b64_tr_b16 v[150:151], v32 offset:4608\n" \
"v_mfma_f32_16x16x32_bf16 a[180:183], v[104:107], v[72:75], a[180:183]\n" \
"s_add_u32 m0, s30, 0x8000\n" \
"s_mov_b32 s84, 0x180\n" \
"v_mfma_f32_16x16x32_bf16 a[180:183], v[168:171], v[136:139], a[180:183]\n" \
"buffer_load_dwordx4 v24, s[76:79], s84 offen lds\n" \
"v_mfma_f32_16x16x32_bf16 a[176:179], v[100:103], v[72:75], a[176:179]\n" \
"ds_read_b64_tr_b16 v[88:89], v33 offset:0\n" \
"v_mfma_f32_16x16x32_bf16 a[176:179], v[164:167], v[136:139], a[176:179]\n" \
"ds_read_b128 v[72:75], v28 offset:10240\n" \
"v_mfma_f32_16x16x32_bf16 a[72:75], v[100:103], v[76:79], a[72:75]\n" \
"ds_read_b128 v[136:139], v29 offset:10240\n" \
"v_mfma_f32_16x16x32_bf16 a[72:75], v[164:167], v[140:143], a[72:75]\n" \
"s_add_u32 m0, s30, 0x8400\n" \
"s_mov_b32 s85, 0x2580\n" \
"v_mfma_f32_16x16x32_bf16 a[76:79], v[104:107], v[76:79], a[76:79]\n" \
"buffer_load_dwordx4 v25, s[76:79], s85 offen lds\n" \
"v_mfma_f32_16x16x32_bf16 a[76:79], v[168:171], v[140:143], a[76:79]\n" \
"ds_read_b64_tr_b16 v[90:91], v33 offset:512\n" \
"v_mfma_f32_16x16x32_bf16 a[104:107], v[108:111], v[76:79], a[104:107]\n" \
"ds_read_b64_tr_b16 v[152:153], v33 offset:4096\n" \
"v_mfma_f32_16x16x32_bf16 a[104:107], v[172:175], v[140:143], a[104:107]\n" \
"s_add_u32 m0, s31, 0x8000\n" \
"s_mov_b32 s84, 0x180000\n" \
"v_mfma_f32_16x16x32_bf16 a[108:111], v[112:115], v[76:79], a[108:111]\n" \
"buffer_load_dwordx4 v26, s[68:71], s84 offen lds\n" \
"v_mfma_f32_16x16x32_bf16 a[108:111], v[176:179], v[140:143], a[108:111]\n" \
"ds_read_b128 v[76:79], v28 offset:12288\n" \
"v_mfma_f32_16x16x32_bf16 a[172:175], v[112:115], v[80:83], a[172:175]\n" \
"ds_read_b128 v[140:143], v29 offset:12288\n" \
"v_mfma_f32_16x16x32_bf16 a[172:175], v[176:179], v[144:147], a[172:175]\n" \
"s_add_u32 m0, s30, 0x8800\n" \
"s_mov_b32 s85, 0x4980\n" \
"v_mfma_f32_16x16x32_bf16 a[168:171], v[108:111], v[80:83], a[168:171]\n" \
"buffer_load_dwordx4 v24, s[76:79], s85 offen lds\n" \
"v_mfma_f32_16x16x32_bf16 a[168:171], v[172:175], v[144:147], a[168:171]\n" \
"ds_read_b64_tr_b16 v[154:155], v33 offset:4608\n" \
"v_mfma_f32_16x16x32_bf16 a[140:143], v[104:107], v[80:83], a[140:143]\n" \
"ds_read_b64_tr_b16 v[96:97], v35 offset:0\n" \
"v_mfma_f32_16x16x32_bf16 a[140:143], v[168:171], v[144:147], a[140:143]\n" \
"ds_read_b64_tr_b16 v[98:99], v35 offset:512\n" \
"v_mfma_f32_16x16x32_bf16 a[136:139], v[100:103], v[80:83], a[136:139]\n" \
"s_add_u32 s11, s22, s12\n" \
"s_addc_u32 s17, s23, 0\n" \
"v_mfma_f32_16x16x32_bf16 a[136:139], v[164:167], v[144:147], a[136:139]\n" \
"ds_read_b128 v[80:83], v28 offset:14336\n" \
"v_mfma_f32_16x16x32_bf16 a[80:83], v[100:103], v[60:63], a[80:83]\n" \
"ds_read_b128 v[144:147], v29 offset:14336\n" \
"v_mfma_f32_16x16x32_bf16 a[80:83], v[164:167], v[124:127], a[80:83]\n" \
"s_add_u32 m0, s30, 0x8c00\n" \
"s_mov_b32 s84, 0x6d80\n" \
"v_mfma_f32_16x16x32_bf16 a[84:87], v[104:107], v[60:63], a[84:87]\n" \
"buffer_load_dwordx4 v25, s[76:79], s84 offen lds\n" \
"v_mfma_f32_16x16x32_bf16 a[84:87], v[168:171], v[124:127], a[84:87]\n" \
"s_add_u32 m0, s31, 0x8400\n" \
"s_mov_b32 s85, 0x188000\n" \
"v_mfma_f32_16x16x32_bf16 a[88:91], v[108:111], v[60:63], a[88:91]\n" \
"buffer_load_dwordx4 v26, s[68:71], s85 offen lds\n" \
"v_mfma_f32_16x16x32_bf16 a[88:91], v[172:175], v[124:127], a[88:91]\n" \
"ds_read_b64_tr_b16 v[160:161], v35 offset:4096\n" \
"v_mfma_f32_16x16x32_bf16 a[92:95], v[112:115], v[60:63], a[92:95]\n" \
"ds_read_b64_tr_b16 v[162:163], v35 offset:4608\n" \
"v_mfma_f32_16x16x32_bf16 a[92:95], v[176:179], v[124:127], a[92:95]\n" \
"ds_read_b128 v[60:63], v28 offset:4096\n" \
"v_mfma_f32_16x16x32_bf16 a[124:127], v[112:115], v[64:67], a[124:127]\n" \
"ds_read_b128 v[124:127], v29 offset:4096\n" \
"v_mfma_f32_16x16x32_bf16 a[124:127], v[176:179], v[128:131], a[124:127]\n" \
"s_add_u32 m0, s30, 0x9000\n" \
"s_mov_b32 s84, 0x9180\n" \
"v_mfma_f32_16x16x32_bf16 a[120:123], v[108:111], v[64:67], a[120:123]\n" \
"buffer_load_dwordx4 v24, s[76:79], s84 offen lds\n" \
"v_mfma_f32_16x16x32_bf16 a[120:123], v[172:175], v[128:131], a[120:123]\n" \
"s_add_u32 m0, s30, 0x9400\n" \
"s_mov_b32 s85, 0xb580\n" \
"v_mfma_f32_16x16x32_bf16 a[116:119], v[104:107], v[64:67], a[116:119]\n" \
"buffer_load_dwordx4 v25, s[76:79], s85 offen lds\n" \
"v_mfma_f32_16x16x32_bf16 a[116:119], v[168:171], v[128:131], a[116:119]\n" \
"ds_read_b64_tr_b16 v[92:93], v34 offset:0\n" \
"v_mfma_f32_16x16x32_bf16 a[112:115], v[100:103], v[64:67], a[112:115]\n" \
"ds_read_b64_tr_b16 v[94:95], v34 offset:512\n" \
"v_mfma_f32_16x16x32_bf16 a[112:115], v[164:167], v[128:131], a[112:115]\n" \
"ds_read_b128 v[64:67], v28 offset:6144\n" \
"v_mfma_f32_16x16x32_bf16 a[16:19], v[100:103], v[52:55], a[16:19]\n" \
"ds_read_b128 v[128:131], v29 offset:6144\n" \
"v_mfma_f32_16x16x32_bf16 a[16:19], v[164:167], v[116:119], a[16:19]\n" \
"s_add_u32 m0, s31, 0x8800\n" \
"s_mov_b32 s84, 0x190000\n" \
"v_mfma_f32_16x16x32_bf16 a[20:23], v[104:107], v[52:55], a[20:23]\n" \
"buffer_load_dwordx4 v27, s[68:71], s84 offen lds\n" \
"v_mfma_f32_16x16x32_bf16 a[20:23], v[168:171], v[116:119], a[20:23]\n" \
"s_add_u32 m0, s30, 0x9800\n" \
"s_mov_b32 s85, 0xd980\n" \
"v_mfma_f32_16x16x32_bf16 a[24:27], v[108:111], v[52:55], a[24:27]\n" \
"buffer_load_dwordx4 v24, s[76:79], s85 offen lds\n" \
"v_mfma_f32_16x16x32_bf16 a[24:27], v[172:175], v[116:119], a[24:27]\n" \
"ds_read_b64_tr_b16 v[156:157], v34 offset:4096\n" \
"v_mfma_f32_16x16x32_bf16 a[28:31], v[112:115], v[52:55], a[28:31]\n" \
"ds_read_b64_tr_b16 v[158:159], v34 offset:4608\n" \
"v_mfma_f32_16x16x32_bf16 a[28:31], v[176:179], v[116:119], a[28:31]\n" \
"ds_read_b128 v[52:55], v28 offset:0\n" \
"v_mfma_f32_16x16x32_bf16 a[60:63], v[112:115], v[56:59], a[60:63]\n" \
"ds_read_b128 v[116:119], v29 offset:0\n" \
"v_mfma_f32_16x16x32_bf16 a[60:63], v[176:179], v[120:123], a[60:63]\n" \
"s_add_u32 s18, s24, s12\n" \
"v_writelane_b32 v11, s11, 9\n" \
"s_addc_u32 s19, s25, 0\n" \
"v_mfma_f32_16x16x32_bf16 a[56:59], v[108:111], v[56:59], a[56:59]\n" \
"s_and_b32 s20, s17, 0xffff\n" \
"v_writelane_b32 v11, s20, 10\n" \
"v_mfma_f32_16x16x32_bf16 a[56:59], v[172:175], v[120:123], a[56:59]\n" \
"s_and_b32 s20, s19, 0xffff\n" \
"v_writelane_b32 v11, s18, 11\n" \
"v_mfma_f32_16x16x32_bf16 a[52:55], v[104:107], v[56:59], a[52:55]\n" \
"s_cmpk_lt_u32 s3, 3\n" \
"s_cselect_b32 s11, s11, s18\n" \
"v_mfma_f32_16x16x32_bf16 a[52:55], v[168:171], v[120:123], a[52:55]\n" \
"s_movk_i32 s18, 0xfff\n" \
"v_writelane_b32 v11, s20, 12\n" \
"v_mfma_f32_16x16x32_bf16 a[48:51], v[100:103], v[56:59], a[48:51]\n" \
"s_and_b32 s20, s29, 0xffff\n" \
"v_writelane_b32 v11, s28, 13\n" \
"v_mfma_f32_16x16x32_bf16 a[48:51], v[164:167], v[120:123], a[48:51]\n" \
"ds_read_b128 v[56:59], v28 offset:2048\n" \
"ds_read_b128 v[120:123], v29 offset:2048\n" \
"s_waitcnt lgkmcnt(15)\n" \
"v_mfma_f32_16x16x32_bf16 a[0:3], v[84:87], v[68:71], a[0:3]\n" \
"s_add_u32 m0, s30, 0x9c00\n" \
"s_mov_b32 s85, 0xfd80\n" \
"v_mfma_f32_16x16x32_bf16 a[0:3], v[148:151], v[132:135], a[0:3]\n" \
"buffer_load_dwordx4 v25, s[76:79], s85 offen lds\n" \
"v_mfma_f32_16x16x32_bf16 a[4:7], v[88:91], v[68:71], a[4:7]\n" \
"ds_read_b64_tr_b16 v[100:101], v36 offset:0\n" \
"v_writelane_b32 v11, s20, 14\n" \
"v_mfma_f32_16x16x32_bf16 a[4:7], v[152:155], v[132:135], a[4:7]\n" \
"s_add_u32 m0, s31, 0x8c00\n" \
"s_mov_b32 s84, 0x198000\n" \
"v_mfma_f32_16x16x32_bf16 a[36:39], v[96:99], v[68:71], a[36:39]\n" \
"buffer_load_dwordx4 v27, s[68:71], s84 offen lds\n" \
"s_waitcnt lgkmcnt(13)\n" \
"v_mfma_f32_16x16x32_bf16 a[36:39], v[160:163], v[132:135], a[36:39]\n" \
"ds_read_b64_tr_b16 v[102:103], v36 offset:1024\n" \
"v_writelane_b32 v11, s11, 15\n" \
"s_waitcnt lgkmcnt(10)\n" \
"v_mfma_f32_16x16x32_bf16 a[32:35], v[68:71], v[92:95], a[32:35]\n" \
"ds_read_b64_tr_b16 v[164:165], v36 offset:8192\n" \
"s_waitcnt lgkmcnt(7)\n" \
"s_nop 0\n" \
"v_mfma_f32_16x16x32_bf16 a[32:35], v[132:135], v[156:159], a[32:35]\n" \
"ds_read_b64_tr_b16 v[166:167], v36 offset:9216\n" \
"v_mfma_f32_16x16x32_bf16 a[96:99], v[72:75], v[92:95], a[96:99]\n" \
"ds_read_b64_tr_b16 v[104:105], v37 offset:0\n" \
"v_mfma_f32_16x16x32_bf16 a[96:99], v[136:139], v[156:159], a[96:99]\n" \
"ds_read_b64_tr_b16 v[106:107], v37 offset:1024\n" \
"v_mfma_f32_16x16x32_bf16 a[100:103], v[96:99], v[72:75], a[100:103]\n" \
"ds_read_b64_tr_b16 v[168:169], v37 offset:8192\n" \
"v_mfma_f32_16x16x32_bf16 a[100:103], v[160:163], v[136:139], a[100:103]\n" \
"s_add_u32 s89, s41, 0x1000\n" \
"buffer_store_dwordx4 v[200:203], v20, s[80:83], s89 offen offset:0 sc1\n" \
"v_mfma_f32_16x16x32_bf16 a[68:71], v[88:91], v[72:75], a[68:71]\n" \
"ds_read_b64_tr_b16 v[170:171], v37 offset:9216\n" \
"v_mfma_f32_16x16x32_bf16 a[68:71], v[152:155], v[136:139], a[68:71]\n" \
"ds_read_b64_tr_b16 v[108:109], v38 offset:0\n" \
"v_mfma_f32_16x16x32_bf16 a[64:67], v[84:87], v[72:75], a[64:67]\n" \
"ds_read_b64_tr_b16 v[110:111], v38 offset:1024\n" \
"v_mfma_f32_16x16x32_bf16 a[64:67], v[148:151], v[136:139], a[64:67]\n" \
"s_cmpk_lt_u32 s3, 3\n" \
"s_cselect_b32 s11, s17, s19\n" \
"v_mfma_f32_16x16x32_bf16 a[128:131], v[84:87], v[76:79], a[128:131]\n" \
"s_add_u32 s88, s41, 0x1000\n" \
"buffer_store_dwordx4 v[204:207], v20, s[80:83], s88 offen offset:1024 sc1\n" \
"v_mfma_f32_16x16x32_bf16 a[128:131], v[148:151], v[140:143], a[128:131]\n" \
"ds_read_b64_tr_b16 v[172:173], v38 offset:8192\n" \
"s_and_b32 s11, s11, 0xffff\n" \
"v_mfma_f32_16x16x32_bf16 a[132:135], v[88:91], v[76:79], a[132:135]\n" \
"ds_read_b64_tr_b16 v[174:175], v38 offset:9216\n" \
"v_writelane_b32 v11, s11, 16\n" \
"v_mfma_f32_16x16x32_bf16 a[132:135], v[152:155], v[140:143], a[132:135]\n" \
"ds_read_b64_tr_b16 v[112:113], v39 offset:0\n" \
"s_mul_i32 s11, s6, 0x48\n" \
"v_mfma_f32_16x16x32_bf16 a[164:167], v[96:99], v[76:79], a[164:167]\n" \
"s_add_i32 s17, s11, 0x47\n" \
"v_mfma_f32_16x16x32_bf16 a[164:167], v[160:163], v[140:143], a[164:167]\n" \
"ds_read_b64_tr_b16 v[114:115], v39 offset:1024\n" \
"v_mfma_f32_16x16x32_bf16 a[160:163], v[76:79], v[92:95], a[160:163]\n" \
"s_add_u32 s89, s41, 0x1000\n" \
"buffer_store_dwordx4 v[208:211], v20, s[80:83], s89 offen offset:2048 sc1\n" \
"v_mfma_f32_16x16x32_bf16 a[160:163], v[140:143], v[156:159], a[160:163]\n" \
"ds_read_b64_tr_b16 v[176:177], v39 offset:8192\n" \
"v_mfma_f32_16x16x32_bf16 a[224:227], v[80:83], v[92:95], a[224:227]\n" \
"ds_read_b64_tr_b16 v[178:179], v39 offset:9216\n" \
"v_mfma_f32_16x16x32_bf16 a[224:227], v[144:147], v[156:159], a[224:227]\n" \
"s_cmpk_lt_u32 s3, 3\n" \
"s_cselect_b32 s17, s17, s18\n" \
"v_mfma_f32_16x16x32_bf16 a[228:231], v[96:99], v[80:83], a[228:231]\n" \
"s_mulk_i32 s17, 0x480\n" \
"s_addk_i32 s17, 0x120\n" \
"v_mfma_f32_16x16x32_bf16 a[228:231], v[160:163], v[144:147], a[228:231]\n" \
"s_add_u32 s88, s41, 0x1000\n" \
"buffer_store_dwordx4 v[212:215], v20, s[80:83], s88 offen offset:3072 sc1\n" \
"v_mfma_f32_16x16x32_bf16 a[196:199], v[88:91], v[80:83], a[196:199]\n" \
"v_writelane_b32 v11, s17, 17\n" \
"s_lshl_b32 s17, s3, 5\n" \
"v_mfma_f32_16x16x32_bf16 a[196:199], v[152:155], v[144:147], a[196:199]\n" \
"s_add_i32 s11, s11, s17\n" \
"s_mul_i32 s17, s11, 0x480\n" \
"v_mfma_f32_16x16x32_bf16 a[192:195], v[84:87], v[80:83], a[192:195]\n" \
"s_mul_i32 s11, s13, 0x24000\n" \
"s_add_i32 s18, s11, 0x1b000\n" \
"v_mfma_f32_16x16x32_bf16 a[192:195], v[148:151], v[144:147], a[192:195]\n" \
"s_cmpk_lt_u32 s3, 3\n" \
"s_cselect_b32 s17, s17, s18\n" \
"v_mfma_f32_16x16x32_bf16 a[8:11], v[88:91], v[64:67], a[8:11]\n" \
"s_mul_i32 s19, s3, 0x9000\n" \
"v_writelane_b32 v11, s17, 18\n" \
"v_mfma_f32_16x16x32_bf16 a[8:11], v[152:155], v[128:131], a[8:11]\n" \
"s_mul_i32 s17, s16, 0x2400\n" \
"s_mul_i32 s16, s16, 0x9000\n" \
"v_mfma_f32_16x16x32_bf16 a[40:43], v[96:99], v[64:67], a[40:43]\n" \
"s_bitset1_b32 s17, 17\n" \
"v_writelane_b32 v11, s17, 19\n" \
"v_mfma_f32_16x16x32_bf16 a[40:43], v[160:163], v[128:131], a[40:43]\n" \
"s_add_i32 s16, s11, s16\n" \
"v_writelane_b32 v11, s16, 25\n" \
"v_mfma_f32_16x16x32_bf16 a[12:15], v[64:67], v[92:95], a[12:15]\n" \
"s_mul_i32 s16, s3, 0x2400\n" \
"s_add_i32 s17, s16, 0x6000\n" \
"v_mfma_f32_16x16x32_bf16 a[12:15], v[128:131], v[156:159], a[12:15]\n" \
"s_waitcnt vmcnt(4) lgkmcnt(0)\n" \
"s_barrier\n" \
"v_mfma_f32_16x16x32_bf16 a[144:147], v[100:103], v[68:71], a[144:147]\n" \
"s_add_u32 m0, s40, 0x0\n" \
"s_nop 0\n" \
"s_add_u32 s85, s39, 0x8000\n" \
"v_mfma_f32_16x16x32_bf16 a[144:147], v[164:167], v[132:135], a[144:147]\n" \
"buffer_load_dwordx4 v20, s[80:83], s85 offen sc1 lds\n" \
"v_mfma_f32_16x16x32_bf16 a[148:151], v[104:107], v[68:71], a[148:151]\n" \
"ds_read_b64_tr_b16 v[84:85], v32 offset:32768\n" \
"v_writelane_b32 v11, s17, 20\n" \
"v_mfma_f32_16x16x32_bf16 a[148:151], v[168:171], v[132:135], a[148:151]\n" \
"ds_read_b64_tr_b16 v[86:87], v32 offset:33280\n" \
"s_or_b32 s17, s16, 0x10000\n" \
"v_mfma_f32_16x16x32_bf16 a[200:203], v[108:111], v[68:71], a[200:203]\n" \
"s_add_u32 m0, s40, 0x1000\n" \
"s_add_u32 s84, s39, 0x9000\n" \
"v_mfma_f32_16x16x32_bf16 a[200:203], v[172:175], v[132:135], a[200:203]\n" \
"buffer_load_dwordx4 v20, s[80:83], s84 offen sc1 lds\n" \
"v_mfma_f32_16x16x32_bf16 a[204:207], v[112:115], v[68:71], a[204:207]\n" \
"ds_read_b64_tr_b16 v[148:149], v32 offset:36864\n" \
"s_add_i32 s18, s16, 0x19000\n" \
"v_mfma_f32_16x16x32_bf16 a[204:207], v[176:179], v[132:135], a[204:207]\n" \
"ds_read_b128 v[68:71], v28 offset:40960\n" \
"v_writelane_b32 v11, s17, 21\n" \
"v_mfma_f32_16x16x32_bf16 a[236:239], v[112:115], v[72:75], a[236:239]\n" \
"ds_read_b128 v[132:135], v29 offset:40960\n" \
"s_mov_b32 s17, 0\n" \
"v_mfma_f32_16x16x32_bf16 a[236:239], v[176:179], v[136:139], a[236:239]\n" \
"s_add_u32 m0, s40, 0x3000\n" \
"s_mov_b32 s85, 0x200\n" \
"v_mfma_f32_16x16x32_bf16 a[232:235], v[108:111], v[72:75], a[232:235]\n" \
"buffer_load_dwordx4 v21, s[76:79], s85 offen lds\n" \
"v_mfma_f32_16x16x32_bf16 a[232:235], v[172:175], v[136:139], a[232:235]\n" \
"ds_read_b64_tr_b16 v[150:151], v32 offset:37376\n" \
"v_mfma_f32_16x16x32_bf16 a[180:183], v[104:107], v[72:75], a[180:183]\n" \
"s_add_u32 m0, s30, 0x0\n" \
"s_mov_b32 s84, 0x200\n" \
"v_mfma_f32_16x16x32_bf16 a[180:183], v[168:171], v[136:139], a[180:183]\n" \
"buffer_load_dwordx4 v24, s[76:79], s84 offen lds\n" \
"v_mfma_f32_16x16x32_bf16 a[176:179], v[100:103], v[72:75], a[176:179]\n" \
"ds_read_b64_tr_b16 v[88:89], v33 offset:32768\n" \
"v_mfma_f32_16x16x32_bf16 a[176:179], v[164:167], v[136:139], a[176:179]\n" \
"ds_read_b128 v[72:75], v28 offset:43008\n" \
"v_mfma_f32_16x16x32_bf16 a[72:75], v[100:103], v[76:79], a[72:75]\n" \
"ds_read_b128 v[136:139], v29 offset:43008\n" \
"v_mfma_f32_16x16x32_bf16 a[72:75], v[164:167], v[140:143], a[72:75]\n" \
"s_add_u32 m0, s30, 0x400\n" \
"s_mov_b32 s85, 0x2600\n" \
"v_mfma_f32_16x16x32_bf16 a[76:79], v[104:107], v[76:79], a[76:79]\n" \
"buffer_load_dwordx4 v25, s[76:79], s85 offen lds\n" \
"v_mfma_f32_16x16x32_bf16 a[76:79], v[168:171], v[140:143], a[76:79]\n" \
"ds_read_b64_tr_b16 v[90:91], v33 offset:33280\n" \
"v_mfma_f32_16x16x32_bf16 a[104:107], v[108:111], v[76:79], a[104:107]\n" \
"ds_read_b64_tr_b16 v[152:153], v33 offset:36864\n" \
"v_mfma_f32_16x16x32_bf16 a[104:107], v[172:175], v[140:143], a[104:107]\n" \
"s_add_u32 m0, s31, 0x0\n" \
"s_nop 0\n" \
"s_mov_b32 s84, 0x200000\n" \
"v_mfma_f32_16x16x32_bf16 a[108:111], v[112:115], v[76:79], a[108:111]\n" \
"buffer_load_dwordx4 v26, s[68:71], s84 offen lds\n" \
"v_mfma_f32_16x16x32_bf16 a[108:111], v[176:179], v[140:143], a[108:111]\n" \
"ds_read_b128 v[76:79], v28 offset:45056\n" \
"v_mfma_f32_16x16x32_bf16 a[172:175], v[112:115], v[80:83], a[172:175]\n" \
"ds_read_b128 v[140:143], v29 offset:45056\n" \
"v_mfma_f32_16x16x32_bf16 a[172:175], v[176:179], v[144:147], a[172:175]\n" \
"s_add_u32 m0, s30, 0x800\n" \
"s_mov_b32 s85, 0x4a00\n" \
"v_mfma_f32_16x16x32_bf16 a[168:171], v[108:111], v[80:83], a[168:171]\n" \
"buffer_load_dwordx4 v24, s[76:79], s85 offen lds\n" \
"v_mfma_f32_16x16x32_bf16 a[168:171], v[172:175], v[144:147], a[168:171]\n" \
"ds_read_b64_tr_b16 v[154:155], v33 offset:37376\n" \
"v_mfma_f32_16x16x32_bf16 a[140:143], v[104:107], v[80:83], a[140:143]\n" \
"ds_read_b64_tr_b16 v[96:97], v35 offset:32768\n" \
"v_mfma_f32_16x16x32_bf16 a[140:143], v[168:171], v[144:147], a[140:143]\n" \
"ds_read_b64_tr_b16 v[98:99], v35 offset:33280\n" \
"v_mfma_f32_16x16x32_bf16 a[136:139], v[100:103], v[80:83], a[136:139]\n" \
"s_cmpk_lt_u32 s3, 3\n" \
"s_cselect_b32 s18, s18, s17\n" \
"v_mfma_f32_16x16x32_bf16 a[136:139], v[164:167], v[144:147], a[136:139]\n" \
"ds_read_b128 v[80:83], v28 offset:47104\n" \
"s_bitset1_b32 s16, 17\n" \
"s_nop 0\n" \
"v_mfma_f32_16x16x32_bf16 a[80:83], v[100:103], v[60:63], a[80:83]\n" \
"ds_read_b128 v[144:147], v29 offset:47104\n" \
"v_writelane_b32 v11, s18, 22\n" \
"v_mfma_f32_16x16x32_bf16 a[80:83], v[164:167], v[124:127], a[80:83]\n" \
"s_add_u32 m0, s30, 0xc00\n" \
"s_mov_b32 s84, 0x6e00\n" \
"v_mfma_f32_16x16x32_bf16 a[84:87], v[104:107], v[60:63], a[84:87]\n" \
"buffer_load_dwordx4 v25, s[76:79], s84 offen lds\n" \
"v_mfma_f32_16x16x32_bf16 a[84:87], v[168:171], v[124:127], a[84:87]\n" \
"s_add_u32 m0, s31, 0x400\n" \
"s_mov_b32 s85, 0x208000\n" \
"v_mfma_f32_16x16x32_bf16 a[88:91], v[108:111], v[60:63], a[88:91]\n" \
"buffer_load_dwordx4 v26, s[68:71], s85 offen lds\n" \
"v_mfma_f32_16x16x32_bf16 a[88:91], v[172:175], v[124:127], a[88:91]\n" \
"ds_read_b64_tr_b16 v[160:161], v35 offset:36864\n" \
"s_movk_i32 s18, 0x2400\n" \
"s_nop 0\n" \
"v_mfma_f32_16x16x32_bf16 a[92:95], v[112:115], v[60:63], a[92:95]\n" \
"ds_read_b64_tr_b16 v[162:163], v35 offset:37376\n" \
"v_mfma_f32_16x16x32_bf16 a[92:95], v[176:179], v[124:127], a[92:95]\n" \
"ds_read_b128 v[60:63], v28 offset:36864\n" \
"v_mfma_f32_16x16x32_bf16 a[124:127], v[112:115], v[64:67], a[124:127]\n" \
"ds_read_b128 v[124:127], v29 offset:36864\n" \
"v_mfma_f32_16x16x32_bf16 a[124:127], v[176:179], v[128:131], a[124:127]\n" \
"s_add_u32 m0, s30, 0x1000\n" \
"s_mov_b32 s84, 0x9200\n" \
"v_mfma_f32_16x16x32_bf16 a[120:123], v[108:111], v[64:67], a[120:123]\n" \
"buffer_load_dwordx4 v24, s[76:79], s84 offen lds\n" \
"v_mfma_f32_16x16x32_bf16 a[120:123], v[172:175], v[128:131], a[120:123]\n" \
"s_add_u32 m0, s30, 0x1400\n" \
"s_mov_b32 s85, 0xb600\n" \
"v_mfma_f32_16x16x32_bf16 a[116:119], v[104:107], v[64:67], a[116:119]\n" \
"buffer_load_dwordx4 v25, s[76:79], s85 offen lds\n" \
"v_mfma_f32_16x16x32_bf16 a[116:119], v[168:171], v[128:131], a[116:119]\n" \
"ds_read_b64_tr_b16 v[92:93], v34 offset:32768\n" \
"v_mfma_f32_16x16x32_bf16 a[112:115], v[100:103], v[64:67], a[112:115]\n" \
"ds_read_b64_tr_b16 v[94:95], v34 offset:33280\n" \
"v_mfma_f32_16x16x32_bf16 a[112:115], v[164:167], v[128:131], a[112:115]\n" \
"ds_read_b128 v[64:67], v28 offset:38912\n" \
"v_mfma_f32_16x16x32_bf16 a[16:19], v[100:103], v[52:55], a[16:19]\n" \
"ds_read_b128 v[128:131], v29 offset:38912\n" \
"v_mfma_f32_16x16x32_bf16 a[16:19], v[164:167], v[116:119], a[16:19]\n" \
"s_add_u32 m0, s31, 0x800\n" \
"s_mov_b32 s84, 0x210000\n" \
"v_mfma_f32_16x16x32_bf16 a[20:23], v[104:107], v[52:55], a[20:23]\n" \
"buffer_load_dwordx4 v27, s[68:71], s84 offen lds\n" \
"v_mfma_f32_16x16x32_bf16 a[20:23], v[168:171], v[116:119], a[20:23]\n" \
"s_add_u32 m0, s30, 0x1800\n" \
"s_mov_b32 s85, 0xda00\n" \
"v_mfma_f32_16x16x32_bf16 a[24:27], v[108:111], v[52:55], a[24:27]\n" \
"buffer_load_dwordx4 v24, s[76:79], s85 offen lds\n" \
"v_mfma_f32_16x16x32_bf16 a[24:27], v[172:175], v[116:119], a[24:27]\n" \
"ds_read_b64_tr_b16 v[156:157], v34 offset:36864\n" \
"v_mfma_f32_16x16x32_bf16 a[28:31], v[112:115], v[52:55], a[28:31]\n" \
"ds_read_b64_tr_b16 v[158:159], v34 offset:37376\n" \
"v_mfma_f32_16x16x32_bf16 a[28:31], v[176:179], v[116:119], a[28:31]\n" \
"ds_read_b128 v[52:55], v28 offset:32768\n" \
"v_mfma_f32_16x16x32_bf16 a[60:63], v[112:115], v[56:59], a[60:63]\n" \
"ds_read_b128 v[116:119], v29 offset:32768\n" \
"v_mfma_f32_16x16x32_bf16 a[60:63], v[176:179], v[120:123], a[60:63]\n" \
"s_cmpk_lt_u32 s3, 3\n" \
"s_cselect_b32 s16, s16, s18\n" \
"v_mfma_f32_16x16x32_bf16 a[56:59], v[108:111], v[56:59], a[56:59]\n" \
"s_xor_b32 s13, s13, 2\n" \
"v_writelane_b32 v11, s16, 23\n" \
"v_mfma_f32_16x16x32_bf16 a[56:59], v[172:175], v[120:123], a[56:59]\n" \
"s_lshl_b32 s16, s3, 10\n" \
"s_or_b32 s16, s16, 0xf000\n" \
"v_mfma_f32_16x16x32_bf16 a[52:55], v[104:107], v[56:59], a[52:55]\n" \
"v_writelane_b32 v11, s16, 24\n" \
"s_add_i32 s16, s15, 1\n" \
"v_mfma_f32_16x16x32_bf16 a[52:55], v[168:171], v[120:123], a[52:55]\n" \
"s_and_b32 s16, s16, 3\n" \
"s_or_b32 s16, s16, s5\n" \
"v_mfma_f32_16x16x32_bf16 a[48:51], v[100:103], v[56:59], a[48:51]\n" \
"s_mul_i32 s18, s16, 0x24000\n" \
"s_add_i32 s18, s18, s19\n" \
"v_mfma_f32_16x16x32_bf16 a[48:51], v[164:167], v[120:123], a[48:51]\n" \
"ds_read_b128 v[56:59], v28 offset:34816\n" \
"ds_read_b128 v[120:123], v29 offset:34816\n" \
"s_waitcnt lgkmcnt(15)\n" \
"v_mfma_f32_16x16x32_bf16 a[0:3], v[84:87], v[68:71], a[0:3]\n" \
"s_add_u32 m0, s30, 0x1c00\n" \
"s_mov_b32 s85, 0xfe00\n" \
"v_mfma_f32_16x16x32_bf16 a[0:3], v[148:151], v[132:135], a[0:3]\n" \
"buffer_load_dwordx4 v25, s[76:79], s85 offen lds\n" \
"v_mfma_f32_16x16x32_bf16 a[4:7], v[88:91], v[68:71], a[4:7]\n" \
"ds_read_b64_tr_b16 v[100:101], v36 offset:32768\n" \
"s_add_i32 s15, s15, -1\n" \
"v_mfma_f32_16x16x32_bf16 a[4:7], v[152:155], v[132:135], a[4:7]\n" \
"s_add_u32 m0, s31, 0xc00\n" \
"s_mov_b32 s84, 0x218000\n" \
"v_mfma_f32_16x16x32_bf16 a[36:39], v[96:99], v[68:71], a[36:39]\n" \
"buffer_load_dwordx4 v27, s[68:71], s84 offen lds\n" \
"s_waitcnt lgkmcnt(13)\n" \
"v_mfma_f32_16x16x32_bf16 a[36:39], v[160:163], v[132:135], a[36:39]\n" \
"ds_read_b64_tr_b16 v[102:103], v36 offset:33792\n" \
"v_writelane_b32 v11, s18, 26\n" \
"s_waitcnt lgkmcnt(10)\n" \
"v_mfma_f32_16x16x32_bf16 a[32:35], v[68:71], v[92:95], a[32:35]\n" \
"ds_read_b64_tr_b16 v[164:165], v36 offset:40960\n" \
"s_mul_i32 s18, s13, 0x24000\n" \
"s_waitcnt lgkmcnt(7)\n" \
"v_mfma_f32_16x16x32_bf16 a[32:35], v[132:135], v[156:159], a[32:35]\n" \
"ds_read_b64_tr_b16 v[166:167], v36 offset:41984\n" \
"s_and_b32 s15, s15, 3\n" \
"v_mfma_f32_16x16x32_bf16 a[96:99], v[72:75], v[92:95], a[96:99]\n" \
"ds_read_b64_tr_b16 v[104:105], v37 offset:32768\n" \
"s_add_i32 s18, s18, s19\n" \
"v_mfma_f32_16x16x32_bf16 a[96:99], v[136:139], v[156:159], a[96:99]\n" \
"ds_read_b64_tr_b16 v[106:107], v37 offset:33792\n" \
"s_or_b32 s15, s15, s5\n" \
"v_mfma_f32_16x16x32_bf16 a[100:103], v[96:99], v[72:75], a[100:103]\n" \
"ds_read_b64_tr_b16 v[168:169], v37 offset:40960\n" \
"v_writelane_b32 v11, s18, 27\n" \
"v_mfma_f32_16x16x32_bf16 a[100:103], v[160:163], v[136:139], a[100:103]\n" \
"s_add_u32 s89, s42, 0x0\n" \
"buffer_store_dwordx4 v[216:219], v20, s[80:83], s89 offen offset:3072 sc1\n" \
"v_mfma_f32_16x16x32_bf16 a[68:71], v[88:91], v[72:75], a[68:71]\n" \
"ds_read_b64_tr_b16 v[170:171], v37 offset:41984\n" \
"s_mul_i32 s18, s15, 0x24000\n" \
"v_mfma_f32_16x16x32_bf16 a[68:71], v[152:155], v[136:139], a[68:71]\n" \
"ds_read_b64_tr_b16 v[108:109], v38 offset:32768\n" \
"s_add_i32 s18, s18, s19\n" \
"v_mfma_f32_16x16x32_bf16 a[64:67], v[84:87], v[72:75], a[64:67]\n" \
"ds_read_b64_tr_b16 v[110:111], v38 offset:33792\n" \
"v_writelane_b32 v11, s18, 28\n" \
"v_mfma_f32_16x16x32_bf16 a[64:67], v[148:151], v[136:139], a[64:67]\n" \
"s_or_b32 s18, s9, s14\n" \
"s_mul_i32 s18, s18, 0x24800\n" \
"v_mfma_f32_16x16x32_bf16 a[128:131], v[84:87], v[76:79], a[128:131]\n" \
"s_add_u32 s88, s42, 0x0\n" \
"buffer_store_dwordx4 v[220:223], v20, s[80:83], s88 offen offset:2048 sc1\n" \
"v_mfma_f32_16x16x32_bf16 a[128:131], v[148:151], v[140:143], a[128:131]\n" \
".fill (L_pb + 73440 - .) / 4, 4, 0xBF800000\n" \
"L_pp18:\n" \
"s_cbranch_execz L_pret\n" \
"ds_read_b64_tr_b16 v[172:173], v38 offset:40960\n" \
"s_lshl_b32 s16, s16, 12\n" \
"v_mfma_f32_16x16x32_bf16 a[132:135], v[88:91], v[76:79], a[132:135]\n" \
"ds_read_b64_tr_b16 v[174:175], v38 offset:41984\n" \
"s_add_i32 s19, s18, 0x3004800\n" \
"v_mfma_f32_16x16x32_bf16 a[132:135], v[152:155], v[140:143], a[132:135]\n" \
"ds_read_b64_tr_b16 v[112:113], v39 offset:32768\n" \
"s_add_i32 s20, s19, s16\n" \
"v_mfma_f32_16x16x32_bf16 a[164:167], v[96:99], v[76:79], a[164:167]\n" \
"v_writelane_b32 v11, s20, 29\n" \
"s_lshl_b32 s13, s13, 12\n" \
"v_mfma_f32_16x16x32_bf16 a[164:167], v[160:163], v[140:143], a[164:167]\n" \
"ds_read_b64_tr_b16 v[114:115], v39 offset:33792\n" \
"s_lshl_b32 s15, s15, 12\n" \
"v_mfma_f32_16x16x32_bf16 a[160:163], v[76:79], v[92:95], a[160:163]\n" \
"s_add_u32 s89, s42, 0x0\n" \
"buffer_store_dwordx4 v[224:227], v20, s[80:83], s89 offen offset:1024 sc1\n" \
"v_mfma_f32_16x16x32_bf16 a[160:163], v[140:143], v[156:159], a[160:163]\n" \
"ds_read_b64_tr_b16 v[176:177], v39 offset:40960\n" \
"s_sub_i32 s14, s9, s14\n" \
"v_mfma_f32_16x16x32_bf16 a[224:227], v[80:83], v[92:95], a[224:227]\n" \
"ds_read_b64_tr_b16 v[178:179], v39 offset:41984\n" \
"s_add_i32 s20, s19, s13\n" \
"v_mfma_f32_16x16x32_bf16 a[224:227], v[144:147], v[156:159], a[224:227]\n" \
"v_writelane_b32 v11, s20, 30\n" \
"s_add_i32 s19, s19, s15\n" \
"v_mfma_f32_16x16x32_bf16 a[228:231], v[96:99], v[80:83], a[228:231]\n" \
"s_mul_i32 s14, s14, 0x24800\n" \
"v_writelane_b32 v11, s19, 31\n" \
"v_mfma_f32_16x16x32_bf16 a[228:231], v[160:163], v[144:147], a[228:231]\n" \
"s_add_u32 s88, s42, 0x0\n" \
"buffer_store_dwordx4 v[228:231], v20, s[80:83], s88 offen offset:0 sc1\n" \
"v_mfma_f32_16x16x32_bf16 a[196:199], v[88:91], v[80:83], a[196:199]\n" \
"s_add_i32 s19, s14, 0x4120800\n" \
"s_add_i32 s16, s19, s16\n" \
"v_mfma_f32_16x16x32_bf16 a[196:199], v[152:155], v[144:147], a[196:199]\n" \
"v_writelane_b32 v11, s16, 32\n" \
"s_add_i32 s13, s19, s13\n" \
"v_mfma_f32_16x16x32_bf16 a[192:195], v[84:87], v[80:83], a[192:195]\n" \
"v_writelane_b32 v11, s13, 33\n" \
"s_add_i32 s19, s19, s15\n" \
"v_mfma_f32_16x16x32_bf16 a[192:195], v[148:151], v[144:147], a[192:195]\n" \
"s_or_b32 s13, s18, 0x3000000\n" \
"s_lshl_b32 s15, s6, 11\n" \
"v_mfma_f32_16x16x32_bf16 a[8:11], v[88:91], v[64:67], a[8:11]\n" \
"v_writelane_b32 v11, s19, 34\n" \
"s_add_i32 s16, s13, s15\n" \
"v_mfma_f32_16x16x32_bf16 a[8:11], v[152:155], v[128:131], a[8:11]\n" \
"v_writelane_b32 v11, s16, 35\n" \
"s_add_i32 s16, s14, 0x411c000\n" \
"v_mfma_f32_16x16x32_bf16 a[40:43], v[96:99], v[64:67], a[40:43]\n" \
"s_add_i32 s19, s16, s15\n" \
"v_writelane_b32 v11, s19, 36\n" \
"v_mfma_f32_16x16x32_bf16 a[40:43], v[160:163], v[128:131], a[40:43]\n" \
"s_add_i32 s18, s18, 0x3004000\n" \
"v_writelane_b32 v11, s18, 37\n" \
"v_mfma_f32_16x16x32_bf16 a[12:15], v[64:67], v[92:95], a[12:15]\n" \
"s_add_i32 s14, s14, 0x4120000\n" \
"v_writelane_b32 v11, s14, 38\n" \
"v_mfma_f32_16x16x32_bf16 a[12:15], v[128:131], v[156:159], a[12:15]\n" \
"s_waitcnt vmcnt(4) lgkmcnt(0)\n" \
"s_barrier\n" \
"v_mov_b32_e32 v45, s62\n" \
"s_mov_b64 exec, 1\n" \
"buffer_store_dword v45, off, s[80:83], s37 offset:0 sc1\n" \
"s_mov_b64 exec, -1\n" \
"s_nop 0\n" \
"v_mfma_f32_16x16x32_bf16 a[144:147], v[100:103], v[68:71], a[144:147]\n" \
"s_add_u32 m0, s40, 0x8000\n" \
"s_add_u32 s85, s39, 0xa000\n" \
"v_mfma_f32_16x16x32_bf16 a[144:147], v[164:167], v[132:135], a[144:147]\n" \
"buffer_load_dwordx4 v20, s[80:83], s85 offen sc1 lds\n" \
"v_mfma_f32_16x16x32_bf16 a[148:151], v[104:107], v[68:71], a[148:151]\n" \
"ds_read_b64_tr_b16 v[84:85], v32 offset:0\n" \
"v_accvgpr_read_b32 v212, a220\n" \
"v_mfma_f32_16x16x32_bf16 a[148:151], v[168:171], v[132:135], a[148:151]\n" \
"ds_read_b64_tr_b16 v[86:87], v32 offset:512\n" \
"v_accvgpr_read_b32 v213, a221\n" \
"v_mfma_f32_16x16x32_bf16 a[200:203], v[108:111], v[68:71], a[200:203]\n" \
"s_add_u32 m0, s40, 0x9000\n" \
"s_add_u32 s84, s39, 0xb000\n" \
"v_mfma_f32_16x16x32_bf16 a[200:203], v[172:175], v[132:135], a[200:203]\n" \
"buffer_load_dwordx4 v20, s[80:83], s84 offen sc1 lds\n" \
"v_mfma_f32_16x16x32_bf16 a[204:207], v[112:115], v[68:71], a[204:207]\n" \
"ds_read_b64_tr_b16 v[148:149], v32 offset:4096\n" \
"v_accvgpr_read_b32 v214, a222\n" \
"v_mfma_f32_16x16x32_bf16 a[204:207], v[176:179], v[132:135], a[204:207]\n" \
"ds_read_b128 v[68:71], v28 offset:8192\n" \
"v_accvgpr_read_b32 v215, a223\n" \
"v_mfma_f32_16x16x32_bf16 a[236:239], v[112:115], v[72:75], a[236:239]\n" \
"ds_read_b128 v[132:135], v29 offset:8192\n" \
"v_accvgpr_read_b32 v180, a252\n" \
"v_mfma_f32_16x16x32_bf16 a[236:239], v[176:179], v[136:139], a[236:239]\n" \
"s_add_u32 m0, s40, 0xb000\n" \
"s_mov_b32 s85, 0x280\n" \
"v_mfma_f32_16x16x32_bf16 a[232:235], v[108:111], v[72:75], a[232:235]\n" \
"buffer_load_dwordx4 v21, s[76:79], s85 offen lds\n" \
"v_mfma_f32_16x16x32_bf16 a[232:235], v[172:175], v[136:139], a[232:235]\n" \
"ds_read_b64_tr_b16 v[150:151], v32 offset:4608\n" \
"v_accvgpr_read_b32 v181, a253\n" \
"v_mfma_f32_16x16x32_bf16 a[180:183], v[104:107], v[72:75], a[180:183]\n" \
"s_add_u32 m0, s30, 0x8000\n" \
"s_mov_b32 s84, 0x280\n" \
"v_mfma_f32_16x16x32_bf16 a[180:183], v[168:171], v[136:139], a[180:183]\n" \
"buffer_load_dwordx4 v24, s[76:79], s84 offen lds\n" \
"v_mfma_f32_16x16x32_bf16 a[176:179], v[100:103], v[72:75], a[176:179]\n" \
"ds_read_b64_tr_b16 v[88:89], v33 offset:0\n" \
"v_accvgpr_read_b32 v182, a254\n" \
"v_mfma_f32_16x16x32_bf16 a[176:179], v[164:167], v[136:139], a[176:179]\n" \
"ds_read_b128 v[72:75], v28 offset:10240\n" \
"v_accvgpr_read_b32 v183, a255\n" \
"v_mfma_f32_16x16x32_bf16 a[72:75], v[100:103], v[76:79], a[72:75]\n" \
"ds_read_b128 v[136:139], v29 offset:10240\n" \
"v_cvt_pk_bf16_f32 v212, v212, v213\n" \
"v_mfma_f32_16x16x32_bf16 a[72:75], v[164:167], v[140:143], a[72:75]\n" \
"s_add_u32 m0, s30, 0x8400\n" \
"s_mov_b32 s85, 0x2680\n" \
"v_mfma_f32_16x16x32_bf16 a[76:79], v[104:107], v[76:79], a[76:79]\n" \
"buffer_load_dwordx4 v25, s[76:79], s85 offen lds\n" \
"v_mfma_f32_16x16x32_bf16 a[76:79], v[168:171], v[140:143], a[76:79]\n" \
"ds_read_b64_tr_b16 v[90:91], v33 offset:512\n" \
"v_cvt_pk_bf16_f32 v213, v214, v215\n" \
"v_mfma_f32_16x16x32_bf16 a[104:107], v[108:111], v[76:79], a[104:107]\n" \
"ds_read_b64_tr_b16 v[152:153], v33 offset:4096\n" \
"v_cvt_pk_bf16_f32 v214, v180, v181\n" \
"v_mfma_f32_16x16x32_bf16 a[104:107], v[172:175], v[140:143], a[104:107]\n" \
"s_add_u32 m0, s31, 0x8000\n" \
"s_mov_b32 s84, 0x280000\n" \
"v_mfma_f32_16x16x32_bf16 a[108:111], v[112:115], v[76:79], a[108:111]\n" \
"buffer_load_dwordx4 v26, s[68:71], s84 offen lds\n" \
"v_mfma_f32_16x16x32_bf16 a[108:111], v[176:179], v[140:143], a[108:111]\n" \
"ds_read_b128 v[76:79], v28 offset:12288\n" \
"v_cvt_pk_bf16_f32 v215, v182, v183\n" \
"v_mfma_f32_16x16x32_bf16 a[172:175], v[112:115], v[80:83], a[172:175]\n" \
"ds_read_b128 v[140:143], v29 offset:12288\n" \
"v_accvgpr_read_b32 v208, a216\n" \
"v_mfma_f32_16x16x32_bf16 a[172:175], v[176:179], v[144:147], a[172:175]\n" \
"s_add_u32 m0, s30, 0x8800\n" \
"s_mov_b32 s85, 0x4a80\n" \
"v_mfma_f32_16x16x32_bf16 a[168:171], v[108:111], v[80:83], a[168:171]\n" \
"buffer_load_dwordx4 v24, s[76:79], s85 offen lds\n" \
"v_mfma_f32_16x16x32_bf16 a[168:171], v[172:175], v[144:147], a[168:171]\n" \
"ds_read_b64_tr_b16 v[154:155], v33 offset:4608\n" \
"v_accvgpr_read_b32 v209, a217\n" \
"v_mfma_f32_16x16x32_bf16 a[140:143], v[104:107], v[80:83], a[140:143]\n" \
"ds_read_b64_tr_b16 v[96:97], v35 offset:0\n" \
"v_accvgpr_read_b32 v210, a218\n" \
"v_mfma_f32_16x16x32_bf16 a[140:143], v[168:171], v[144:147], a[140:143]\n" \
"ds_read_b64_tr_b16 v[98:99], v35 offset:512\n" \
"v_accvgpr_read_b32 v211, a219\n" \
"v_mfma_f32_16x16x32_bf16 a[136:139], v[100:103], v[80:83], a[136:139]\n" \
"v_accvgpr_read_b32 v180, a248\n" \
"v_accvgpr_read_b32 v181, a249\n" \
"v_mfma_f32_16x16x32_bf16 a[136:139], v[164:167], v[144:147], a[136:139]\n" \
"ds_read_b128 v[80:83], v28 offset:14336\n" \
"v_accvgpr_read_b32 v182, a250\n" \
"v_mfma_f32_16x16x32_bf16 a[80:83], v[100:103], v[60:63], a[80:83]\n" \
"ds_read_b128 v[144:147], v29 offset:14336\n" \
"v_accvgpr_read_b32 v183, a251\n" \
"v_mfma_f32_16x16x32_bf16 a[80:83], v[164:167], v[124:127], a[80:83]\n" \
"s_add_u32 m0, s30, 0x8c00\n" \
"s_mov_b32 s84, 0x6e80\n" \
"v_mfma_f32_16x16x32_bf16 a[84:87], v[104:107], v[60:63], a[84:87]\n" \
"buffer_load_dwordx4 v25, s[76:79], s84 offen lds\n" \
"v_mfma_f32_16x16x32_bf16 a[84:87], v[168:171], v[124:127], a[84:87]\n" \
"s_add_u32 m0, s31, 0x8400\n" \
"s_mov_b32 s85, 0x288000\n" \
"v_mfma_f32_16x16x32_bf16 a[88:91], v[108:111], v[60:63], a[88:91]\n" \
"buffer_load_dwordx4 v26, s[68:71], s85 offen lds\n" \
"v_mfma_f32_16x16x32_bf16 a[88:91], v[172:175], v[124:127], a[88:91]\n" \
"ds_read_b64_tr_b16 v[160:161], v35 offset:4096\n" \
"v_cvt_pk_bf16_f32 v208, v208, v209\n" \
"v_mfma_f32_16x16x32_bf16 a[92:95], v[112:115], v[60:63], a[92:95]\n" \
"ds_read_b64_tr_b16 v[162:163], v35 offset:4608\n" \
"v_cvt_pk_bf16_f32 v209, v210, v211\n" \
"v_mfma_f32_16x16x32_bf16 a[92:95], v[176:179], v[124:127], a[92:95]\n" \
"ds_read_b128 v[60:63], v28 offset:4096\n" \
"v_cvt_pk_bf16_f32 v210, v180, v181\n" \
"v_mfma_f32_16x16x32_bf16 a[124:127], v[112:115], v[64:67], a[124:127]\n" \
"ds_read_b128 v[124:127], v29 offset:4096\n" \
"v_cvt_pk_bf16_f32 v211, v182, v183\n" \
"v_mfma_f32_16x16x32_bf16 a[124:127], v[176:179], v[128:131], a[124:127]\n" \
"s_add_u32 m0, s30, 0x9000\n" \
"s_mov_b32 s84, 0x9280\n" \
"v_mfma_f32_16x16x32_bf16 a[120:123], v[108:111], v[64:67], a[120:123]\n" \
"buffer_load_dwordx4 v24, s[76:79], s84 offen lds\n" \
"v_mfma_f32_16x16x32_bf16 a[120:123], v[172:175], v[128:131], a[120:123]\n" \
"s_add_u32 m0, s30, 0x9400\n" \
"s_mov_b32 s85, 0xb680\n" \
"v_mfma_f32_16x16x32_bf16 a[116:119], v[104:107], v[64:67], a[116:119]\n" \
"buffer_load_dwordx4 v25, s[76:79], s85 offen lds\n" \
"v_mfma_f32_16x16x32_bf16 a[116:119], v[168:171], v[128:131], a[116:119]\n" \
"ds_read_b64_tr_b16 v[92:93], v34 offset:0\n" \
"v_accvgpr_read_b32 v204, a212\n" \
"v_mfma_f32_16x16x32_bf16 a[112:115], v[100:103], v[64:67], a[112:115]\n" \
"ds_read_b64_tr_b16 v[94:95], v34 offset:512\n" \
"v_accvgpr_read_b32 v205, a213\n" \
"v_mfma_f32_16x16x32_bf16 a[112:115], v[164:167], v[128:131], a[112:115]\n" \
"ds_read_b128 v[64:67], v28 offset:6144\n" \
"v_accvgpr_read_b32 v206, a214\n" \
"v_mfma_f32_16x16x32_bf16 a[16:19], v[100:103], v[52:55], a[16:19]\n" \
"ds_read_b128 v[128:131], v29 offset:6144\n" \
"v_accvgpr_read_b32 v207, a215\n" \
"v_mfma_f32_16x16x32_bf16 a[16:19], v[164:167], v[116:119], a[16:19]\n" \
"s_add_u32 m0, s31, 0x8800\n" \
"s_mov_b32 s84, 0x290000\n" \
"v_mfma_f32_16x16x32_bf16 a[20:23], v[104:107], v[52:55], a[20:23]\n" \
"buffer_load_dwordx4 v27, s[68:71], s84 offen lds\n" \
"v_mfma_f32_16x16x32_bf16 a[20:23], v[168:171], v[116:119], a[20:23]\n" \
"s_add_u32 m0, s30, 0x9800\n" \
"s_mov_b32 s85, 0xda80\n" \
"v_mfma_f32_16x16x32_bf16 a[24:27], v[108:111], v[52:55], a[24:27]\n" \
"buffer_load_dwordx4 v24, s[76:79], s85 offen lds\n" \
"v_mfma_f32_16x16x32_bf16 a[24:27], v[172:175], v[116:119], a[24:27]\n" \
"ds_read_b64_tr_b16 v[156:157], v34 offset:4096\n" \
"v_accvgpr_read_b32 v180, a244\n" \
"v_mfma_f32_16x16x32_bf16 a[28:31], v[112:115], v[52:55], a[28:31]\n" \
"ds_read_b64_tr_b16 v[158:159], v34 offset:4608\n" \
"v_accvgpr_read_b32 v181, a245\n" \
"v_mfma_f32_16x16x32_bf16 a[28:31], v[176:179], v[116:119], a[28:31]\n" \
"ds_read_b128 v[52:55], v28 offset:0\n" \
"v_accvgpr_read_b32 v182, a246\n" \
"v_mfma_f32_16x16x32_bf16 a[60:63], v[112:115], v[56:59], a[60:63]\n" \
"ds_read_b128 v[116:119], v29 offset:0\n" \
"v_accvgpr_read_b32 v183, a247\n" \
"v_mfma_f32_16x16x32_bf16 a[60:63], v[176:179], v[120:123], a[60:63]\n" \
"v_cvt_pk_bf16_f32 v204, v204, v205\n" \
"v_cvt_pk_bf16_f32 v205, v206, v207\n" \
"v_mfma_f32_16x16x32_bf16 a[56:59], v[108:111], v[56:59], a[56:59]\n" \
"v_cvt_pk_bf16_f32 v206, v180, v181\n" \
"v_cvt_pk_bf16_f32 v207, v182, v183\n" \
"v_mfma_f32_16x16x32_bf16 a[56:59], v[172:175], v[120:123], a[56:59]\n" \
"v_accvgpr_read_b32 v200, a208\n" \
"v_accvgpr_read_b32 v201, a209\n" \
"v_mfma_f32_16x16x32_bf16 a[52:55], v[104:107], v[56:59], a[52:55]\n" \
"v_accvgpr_read_b32 v202, a210\n" \
"v_accvgpr_read_b32 v203, a211\n" \
"v_mfma_f32_16x16x32_bf16 a[52:55], v[168:171], v[120:123], a[52:55]\n" \
"v_accvgpr_read_b32 v180, a240\n" \
"v_accvgpr_read_b32 v181, a241\n" \
"v_mfma_f32_16x16x32_bf16 a[48:51], v[100:103], v[56:59], a[48:51]\n" \
"v_accvgpr_read_b32 v182, a242\n" \
"v_accvgpr_read_b32 v183, a243\n" \
"v_mfma_f32_16x16x32_bf16 a[48:51], v[164:167], v[120:123], a[48:51]\n" \
"ds_read_b128 v[56:59], v28 offset:2048\n" \
"ds_read_b128 v[120:123], v29 offset:2048\n" \
"s_waitcnt lgkmcnt(15)\n" \
"s_nop 0\n" \
"v_mfma_f32_16x16x32_bf16 a[0:3], v[84:87], v[68:71], a[0:3]\n" \
"s_add_u32 m0, s30, 0x9c00\n" \
"s_mov_b32 s85, 0xfe80\n" \
"v_mfma_f32_16x16x32_bf16 a[0:3], v[148:151], v[132:135], a[0:3]\n" \
"buffer_load_dwordx4 v25, s[76:79], s85 offen lds\n" \
"v_mfma_f32_16x16x32_bf16 a[4:7], v[88:91], v[68:71], a[4:7]\n" \
"ds_read_b64_tr_b16 v[100:101], v36 offset:0\n" \
"v_cvt_pk_bf16_f32 v200, v200, v201\n" \
"v_mfma_f32_16x16x32_bf16 a[4:7], v[152:155], v[132:135], a[4:7]\n" \
"s_add_u32 m0, s31, 0x8c00\n" \
"s_mov_b32 s84, 0x298000\n" \
"v_mfma_f32_16x16x32_bf16 a[36:39], v[96:99], v[68:71], a[36:39]\n" \
"buffer_load_dwordx4 v27, s[68:71], s84 offen lds\n" \
"s_waitcnt lgkmcnt(13)\n" \
"v_mfma_f32_16x16x32_bf16 a[36:39], v[160:163], v[132:135], a[36:39]\n" \
"ds_read_b64_tr_b16 v[102:103], v36 offset:1024\n" \
"v_cvt_pk_bf16_f32 v201, v202, v203\n" \
"s_waitcnt lgkmcnt(10)\n" \
"v_mfma_f32_16x16x32_bf16 a[32:35], v[68:71], v[92:95], a[32:35]\n" \
"ds_read_b64_tr_b16 v[164:165], v36 offset:8192\n" \
"v_cvt_pk_bf16_f32 v202, v180, v181\n" \
"s_waitcnt lgkmcnt(7)\n" \
"s_nop 0\n" \
"v_mfma_f32_16x16x32_bf16 a[32:35], v[132:135], v[156:159], a[32:35]\n" \
"ds_read_b64_tr_b16 v[166:167], v36 offset:9216\n" \
"v_cvt_pk_bf16_f32 v203, v182, v183\n" \
"v_mfma_f32_16x16x32_bf16 a[96:99], v[72:75], v[92:95], a[96:99]\n" \
"ds_read_b64_tr_b16 v[104:105], v37 offset:0\n" \
"v_accvgpr_read_b32 v196, a156\n" \
"v_mfma_f32_16x16x32_bf16 a[96:99], v[136:139], v[156:159], a[96:99]\n" \
"ds_read_b64_tr_b16 v[106:107], v37 offset:1024\n" \
"v_accvgpr_read_b32 v197, a157\n" \
"v_mfma_f32_16x16x32_bf16 a[100:103], v[96:99], v[72:75], a[100:103]\n" \
"ds_read_b64_tr_b16 v[168:169], v37 offset:8192\n" \
"v_accvgpr_read_b32 v198, a158\n" \
"v_mfma_f32_16x16x32_bf16 a[100:103], v[160:163], v[136:139], a[100:103]\n" \
"ds_read_b64_tr_b16 v[170:171], v37 offset:9216\n" \
"v_accvgpr_read_b32 v199, a159\n" \
"v_mfma_f32_16x16x32_bf16 a[68:71], v[88:91], v[72:75], a[68:71]\n" \
"v_accvgpr_read_b32 v180, a188\n" \
"v_accvgpr_read_b32 v181, a189\n" \
"v_mfma_f32_16x16x32_bf16 a[68:71], v[152:155], v[136:139], a[68:71]\n" \
"s_add_u32 s89, s42, 0x2000\n" \
"buffer_store_dwordx4 v[232:235], v20, s[80:83], s89 offen offset:3072 sc1\n" \
"v_mfma_f32_16x16x32_bf16 a[64:67], v[84:87], v[72:75], a[64:67]\n" \
"ds_read_b64_tr_b16 v[108:109], v38 offset:0\n" \
"v_accvgpr_read_b32 v182, a190\n" \
"v_mfma_f32_16x16x32_bf16 a[64:67], v[148:151], v[136:139], a[64:67]\n" \
"ds_read_b64_tr_b16 v[110:111], v38 offset:1024\n" \
"v_accvgpr_read_b32 v183, a191\n" \
"v_mfma_f32_16x16x32_bf16 a[128:131], v[84:87], v[76:79], a[128:131]\n" \
"ds_read_b64_tr_b16 v[172:173], v38 offset:8192\n" \
"v_cvt_pk_bf16_f32 v196, v196, v197\n" \
"v_mfma_f32_16x16x32_bf16 a[128:131], v[148:151], v[140:143], a[128:131]\n" \
"ds_read_b64_tr_b16 v[174:175], v38 offset:9216\n" \
"v_cvt_pk_bf16_f32 v197, v198, v199\n" \
"v_mfma_f32_16x16x32_bf16 a[132:135], v[88:91], v[76:79], a[132:135]\n" \
"v_cvt_pk_bf16_f32 v198, v180, v181\n" \
"v_cvt_pk_bf16_f32 v199, v182, v183\n" \
"v_mfma_f32_16x16x32_bf16 a[132:135], v[152:155], v[140:143], a[132:135]\n" \
"s_add_u32 s88, s42, 0x2000\n" \
"buffer_store_dwordx4 v[236:239], v20, s[80:83], s88 offen offset:2048 sc1\n" \
"v_mfma_f32_16x16x32_bf16 a[164:167], v[96:99], v[76:79], a[164:167]\n" \
"ds_read_b64_tr_b16 v[112:113], v39 offset:0\n" \
"v_accvgpr_read_b32 v192, a152\n" \
"v_mfma_f32_16x16x32_bf16 a[164:167], v[160:163], v[140:143], a[164:167]\n" \
"ds_read_b64_tr_b16 v[114:115], v39 offset:1024\n" \
"v_accvgpr_read_b32 v193, a153\n" \
"v_mfma_f32_16x16x32_bf16 a[160:163], v[76:79], v[92:95], a[160:163]\n" \
"ds_read_b64_tr_b16 v[176:177], v39 offset:8192\n" \
"v_accvgpr_read_b32 v194, a154\n" \
"v_mfma_f32_16x16x32_bf16 a[160:163], v[140:143], v[156:159], a[160:163]\n" \
"v_accvgpr_read_b32 v195, a155\n" \
"v_accvgpr_read_b32 v180, a184\n" \
"v_mfma_f32_16x16x32_bf16 a[224:227], v[80:83], v[92:95], a[224:227]\n" \
"ds_read_b64_tr_b16 v[178:179], v39 offset:9216\n" \
"v_accvgpr_read_b32 v181, a185\n" \
"v_mfma_f32_16x16x32_bf16 a[224:227], v[144:147], v[156:159], a[224:227]\n" \
"v_accvgpr_read_b32 v182, a186\n" \
"v_accvgpr_read_b32 v183, a187\n" \
"v_mfma_f32_16x16x32_bf16 a[228:231], v[96:99], v[80:83], a[228:231]\n" \
"s_add_u32 s89, s42, 0x2000\n" \
"buffer_store_dwordx4 v[240:243], v20, s[80:83], s89 offen offset:1024 sc1\n" \
"v_mfma_f32_16x16x32_bf16 a[228:231], v[160:163], v[144:147], a[228:231]\n" \
"v_cvt_pk_bf16_f32 v192, v192, v193\n" \
"v_cvt_pk_bf16_f32 v193, v194, v195\n" \
"v_mfma_f32_16x16x32_bf16 a[196:199], v[88:91], v[80:83], a[196:199]\n" \
"v_cvt_pk_bf16_f32 v194, v180, v181\n" \
"v_cvt_pk_bf16_f32 v195, v182, v183\n" \
"v_mfma_f32_16x16x32_bf16 a[196:199], v[152:155], v[144:147], a[196:199]\n" \
"s_lshl_b32 s14, s4, 2\n" \
"s_or_b32 s8, s14, s8\n" \
"v_mfma_f32_16x16x32_bf16 a[192:195], v[84:87], v[80:83], a[192:195]\n" \
"s_mul_i32 s8, s8, 0x90000\n" \
"s_lshl_b32 s14, s3, 14\n" \
"v_mfma_f32_16x16x32_bf16 a[192:195], v[148:151], v[144:147], a[192:195]\n" \
"s_add_i32 s8, s8, s14\n" \
"s_lshl_b32 s9, s9, 4\n" \
"v_mfma_f32_16x16x32_bf16 a[8:11], v[88:91], v[64:67], a[8:11]\n" \
"s_lshl_b32 s14, s4, 7\n" \
"s_or_b32 s18, s9, s14\n" \
"v_mfma_f32_16x16x32_bf16 a[8:11], v[152:155], v[128:131], a[8:11]\n" \
"s_sub_i32 s9, s9, s14\n" \
"s_mulk_i32 s18, 0x480\n" \
"v_mfma_f32_16x16x32_bf16 a[40:43], v[96:99], v[64:67], a[40:43]\n" \
"s_mulk_i32 s9, 0x480\n" \
"s_or_b32 s14, s18, s12\n" \
"v_mfma_f32_16x16x32_bf16 a[40:43], v[160:163], v[128:131], a[40:43]\n" \
"s_or_b32 s9, s9, s12\n" \
"s_and_b32 s12, s2, 1\n" \
"v_mfma_f32_16x16x32_bf16 a[12:15], v[64:67], v[92:95], a[12:15]\n" \
"s_cmp_lg_u32 s12, 0\n" \
"s_cselect_b32 s13, s16, s13\n" \
"v_mfma_f32_16x16x32_bf16 a[12:15], v[128:131], v[156:159], a[12:15]\n" \
"s_waitcnt vmcnt(3) lgkmcnt(0)\n" \
"s_barrier\n" \
"v_mfma_f32_16x16x32_bf16 a[144:147], v[100:103], v[68:71], a[144:147]\n" \
"s_add_u32 m0, s40, 0x0\n" \
"s_add_u32 s84, s39, 0xc000\n" \
"v_mfma_f32_16x16x32_bf16 a[144:147], v[164:167], v[132:135], a[144:147]\n" \
"buffer_load_dwordx4 v20, s[80:83], s84 offen sc1 lds\n" \
"v_mfma_f32_16x16x32_bf16 a[148:151], v[104:107], v[68:71], a[148:151]\n" \
"ds_read_b64_tr_b16 v[84:85], v32 offset:32768\n" \
"s_bfe_u32 s16, s2, 0x20001\n" \
"v_mfma_f32_16x16x32_bf16 a[148:151], v[168:171], v[132:135], a[148:151]\n" \
"ds_read_b64_tr_b16 v[86:87], v32 offset:33280\n" \
"s_addk_i32 s13, 0x4000\n" \
"v_mfma_f32_16x16x32_bf16 a[200:203], v[108:111], v[68:71], a[200:203]\n" \
"s_add_u32 m0, s40, 0x1000\n" \
"s_add_u32 s85, s39, 0xd000\n" \
"v_mfma_f32_16x16x32_bf16 a[200:203], v[172:175], v[132:135], a[200:203]\n" \
"buffer_load_dwordx4 v20, s[80:83], s85 offen sc1 lds\n" \
"v_mfma_f32_16x16x32_bf16 a[204:207], v[112:115], v[68:71], a[204:207]\n" \
"ds_read_b64_tr_b16 v[148:149], v32 offset:36864\n" \
"s_xor_b32 s16, s16, s3\n" \
"v_mfma_f32_16x16x32_bf16 a[204:207], v[176:179], v[132:135], a[204:207]\n" \
"ds_read_b128 v[68:71], v28 offset:40960\n" \
"v_mfma_f32_16x16x32_bf16 a[236:239], v[112:115], v[72:75], a[236:239]\n" \
"ds_read_b128 v[132:135], v29 offset:40960\n" \
"v_mfma_f32_16x16x32_bf16 a[236:239], v[176:179], v[136:139], a[236:239]\n" \
"s_add_u32 m0, s40, 0x3000\n" \
"s_mov_b32 s84, 0x300\n" \
"v_mfma_f32_16x16x32_bf16 a[232:235], v[108:111], v[72:75], a[232:235]\n" \
"buffer_load_dwordx4 v21, s[76:79], s84 offen lds\n" \
"v_mfma_f32_16x16x32_bf16 a[232:235], v[172:175], v[136:139], a[232:235]\n" \
"ds_read_b64_tr_b16 v[150:151], v32 offset:37376\n" \
"v_mfma_f32_16x16x32_bf16 a[180:183], v[104:107], v[72:75], a[180:183]\n" \
"s_add_u32 m0, s30, 0x0\n" \
"s_nop 0\n" \
"s_mov_b32 s85, 0x300\n" \
"v_mfma_f32_16x16x32_bf16 a[180:183], v[168:171], v[136:139], a[180:183]\n" \
"buffer_load_dwordx4 v24, s[76:79], s85 offen lds\n" \
"v_mfma_f32_16x16x32_bf16 a[176:179], v[100:103], v[72:75], a[176:179]\n" \
"ds_read_b64_tr_b16 v[88:89], v33 offset:32768\n" \
"v_mfma_f32_16x16x32_bf16 a[176:179], v[164:167], v[136:139], a[176:179]\n" \
"ds_read_b128 v[72:75], v28 offset:43008\n" \
"v_mfma_f32_16x16x32_bf16 a[72:75], v[100:103], v[76:79], a[72:75]\n" \
"ds_read_b128 v[136:139], v29 offset:43008\n" \
"v_mfma_f32_16x16x32_bf16 a[72:75], v[164:167], v[140:143], a[72:75]\n" \
"s_add_u32 m0, s30, 0x400\n" \
"s_mov_b32 s84, 0x2700\n" \
"v_mfma_f32_16x16x32_bf16 a[76:79], v[104:107], v[76:79], a[76:79]\n" \
"buffer_load_dwordx4 v25, s[76:79], s84 offen lds\n" \
"v_mfma_f32_16x16x32_bf16 a[76:79], v[168:171], v[140:143], a[76:79]\n" \
"ds_read_b64_tr_b16 v[90:91], v33 offset:33280\n" \
"v_mfma_f32_16x16x32_bf16 a[104:107], v[108:111], v[76:79], a[104:107]\n" \
"ds_read_b64_tr_b16 v[152:153], v33 offset:36864\n" \
"v_mfma_f32_16x16x32_bf16 a[104:107], v[172:175], v[140:143], a[104:107]\n" \
"s_add_u32 m0, s31, 0x0\n" \
"s_nop 0\n" \
"s_mov_b32 s85, 0x300000\n" \
"v_mfma_f32_16x16x32_bf16 a[108:111], v[112:115], v[76:79], a[108:111]\n" \
"buffer_load_dwordx4 v26, s[68:71], s85 offen lds\n" \
"v_mfma_f32_16x16x32_bf16 a[108:111], v[176:179], v[140:143], a[108:111]\n" \
"ds_read_b128 v[76:79], v28 offset:45056\n" \
"v_mfma_f32_16x16x32_bf16 a[172:175], v[112:115], v[80:83], a[172:175]\n" \
"ds_read_b128 v[140:143], v29 offset:45056\n" \
"v_mfma_f32_16x16x32_bf16 a[172:175], v[176:179], v[144:147], a[172:175]\n" \
"s_add_u32 m0, s30, 0x800\n" \
"s_mov_b32 s84, 0x4b00\n" \
"v_mfma_f32_16x16x32_bf16 a[168:171], v[108:111], v[80:83], a[168:171]\n" \
"buffer_load_dwordx4 v24, s[76:79], s84 offen lds\n" \
"v_mfma_f32_16x16x32_bf16 a[168:171], v[172:175], v[144:147], a[168:171]\n" \
"ds_read_b64_tr_b16 v[154:155], v33 offset:37376\n" \
"v_mfma_f32_16x16x32_bf16 a[140:143], v[104:107], v[80:83], a[140:143]\n" \
"ds_read_b64_tr_b16 v[96:97], v35 offset:32768\n" \
"v_mfma_f32_16x16x32_bf16 a[140:143], v[168:171], v[144:147], a[140:143]\n" \
"ds_read_b64_tr_b16 v[98:99], v35 offset:33280\n" \
"v_mfma_f32_16x16x32_bf16 a[136:139], v[100:103], v[80:83], a[136:139]\n" \
"s_cmp_lg_u32 s16, 0\n" \
"s_cselect_b32 s13, s7, s13\n" \
"v_mfma_f32_16x16x32_bf16 a[136:139], v[164:167], v[144:147], a[136:139]\n" \
"ds_read_b128 v[80:83], v28 offset:47104\n" \
"s_lshl_b32 s6, s6, 16\n" \
"s_nop 0\n" \
"v_mfma_f32_16x16x32_bf16 a[80:83], v[100:103], v[60:63], a[80:83]\n" \
"ds_read_b128 v[144:147], v29 offset:47104\n" \
"v_writelane_b32 v11, s13, 39\n" \
"v_mfma_f32_16x16x32_bf16 a[80:83], v[164:167], v[124:127], a[80:83]\n" \
"s_add_u32 m0, s30, 0xc00\n" \
"s_mov_b32 s85, 0x6f00\n" \
"v_mfma_f32_16x16x32_bf16 a[84:87], v[104:107], v[60:63], a[84:87]\n" \
"buffer_load_dwordx4 v25, s[76:79], s85 offen lds\n" \
"v_mfma_f32_16x16x32_bf16 a[84:87], v[168:171], v[124:127], a[84:87]\n" \
"s_add_u32 m0, s31, 0x400\n" \
"s_mov_b32 s84, 0x308000\n" \
"v_mfma_f32_16x16x32_bf16 a[88:91], v[108:111], v[60:63], a[88:91]\n" \
"buffer_load_dwordx4 v26, s[68:71], s84 offen lds\n" \
"v_mfma_f32_16x16x32_bf16 a[88:91], v[172:175], v[124:127], a[88:91]\n" \
"ds_read_b64_tr_b16 v[160:161], v35 offset:36864\n" \
"s_add_i32 s13, s8, 0x4240000\n" \
"v_mfma_f32_16x16x32_bf16 a[92:95], v[112:115], v[60:63], a[92:95]\n" \
"ds_read_b64_tr_b16 v[162:163], v35 offset:37376\n" \
"s_add_i32 s6, s13, s6\n" \
"s_nop 0\n" \
"v_mfma_f32_16x16x32_bf16 a[92:95], v[176:179], v[124:127], a[92:95]\n" \
"ds_read_b128 v[60:63], v28 offset:36864\n" \
"v_writelane_b32 v11, s6, 40\n" \
"v_mfma_f32_16x16x32_bf16 a[124:127], v[112:115], v[64:67], a[124:127]\n" \
"ds_read_b128 v[124:127], v29 offset:36864\n" \
"s_add_i32 s8, s8, 0x42c0000\n" \
"v_mfma_f32_16x16x32_bf16 a[124:127], v[176:179], v[128:131], a[124:127]\n" \
"s_add_u32 m0, s30, 0x1000\n" \
"s_mov_b32 s85, 0x9300\n" \
"v_mfma_f32_16x16x32_bf16 a[120:123], v[108:111], v[64:67], a[120:123]\n" \
"buffer_load_dwordx4 v24, s[76:79], s85 offen lds\n" \
"v_mfma_f32_16x16x32_bf16 a[120:123], v[172:175], v[128:131], a[120:123]\n" \
"s_add_u32 m0, s30, 0x1400\n" \
"s_mov_b32 s84, 0xb700\n" \
"v_mfma_f32_16x16x32_bf16 a[116:119], v[104:107], v[64:67], a[116:119]\n" \
"buffer_load_dwordx4 v25, s[76:79], s84 offen lds\n" \
"v_mfma_f32_16x16x32_bf16 a[116:119], v[168:171], v[128:131], a[116:119]\n" \
"ds_read_b64_tr_b16 v[92:93], v34 offset:32768\n" \
"s_lshl_b32 s6, s12, 10\n" \
"v_mfma_f32_16x16x32_bf16 a[112:115], v[100:103], v[64:67], a[112:115]\n" \
"ds_read_b64_tr_b16 v[94:95], v34 offset:33280\n" \
"s_or_b32 s6, s8, s6\n" \
"v_mfma_f32_16x16x32_bf16 a[112:115], v[164:167], v[128:131], a[112:115]\n" \
"ds_read_b128 v[64:67], v28 offset:38912\n" \
"v_mfma_f32_16x16x32_bf16 a[16:19], v[100:103], v[52:55], a[16:19]\n" \
"ds_read_b128 v[128:131], v29 offset:38912\n" \
"v_mfma_f32_16x16x32_bf16 a[16:19], v[164:167], v[116:119], a[16:19]\n" \
"s_add_u32 m0, s31, 0x800\n" \
"s_mov_b32 s85, 0x310000\n" \
"v_mfma_f32_16x16x32_bf16 a[20:23], v[104:107], v[52:55], a[20:23]\n" \
"buffer_load_dwordx4 v27, s[68:71], s85 offen lds\n" \
"v_mfma_f32_16x16x32_bf16 a[20:23], v[168:171], v[116:119], a[20:23]\n" \
"s_add_u32 m0, s30, 0x1800\n" \
"s_mov_b32 s84, 0xdb00\n" \
"v_mfma_f32_16x16x32_bf16 a[24:27], v[108:111], v[52:55], a[24:27]\n" \
"buffer_load_dwordx4 v24, s[76:79], s84 offen lds\n" \
"v_mfma_f32_16x16x32_bf16 a[24:27], v[172:175], v[116:119], a[24:27]\n" \
"ds_read_b64_tr_b16 v[156:157], v34 offset:36864\n" \
"v_mfma_f32_16x16x32_bf16 a[28:31], v[112:115], v[52:55], a[28:31]\n" \
"ds_read_b64_tr_b16 v[158:159], v34 offset:37376\n" \
"v_mfma_f32_16x16x32_bf16 a[28:31], v[176:179], v[116:119], a[28:31]\n" \
"ds_read_b128 v[52:55], v28 offset:32768\n" \
"v_mfma_f32_16x16x32_bf16 a[60:63], v[112:115], v[56:59], a[60:63]\n" \
"ds_read_b128 v[116:119], v29 offset:32768\n" \
"v_mfma_f32_16x16x32_bf16 a[60:63], v[176:179], v[120:123], a[60:63]\n" \
"s_cmp_lg_u32 s16, 0\n" \
"s_cselect_b32 s6, s7, s6\n" \
"v_mfma_f32_16x16x32_bf16 a[56:59], v[108:111], v[56:59], a[56:59]\n" \
"s_mulk_i32 s4, 0x4800\n" \
"v_writelane_b32 v11, s6, 41\n" \
"v_mfma_f32_16x16x32_bf16 a[56:59], v[172:175], v[120:123], a[56:59]\n" \
"s_add_i32 s6, s14, s10\n" \
"v_writelane_b32 v11, s6, 42\n" \
"v_mfma_f32_16x16x32_bf16 a[52:55], v[104:107], v[56:59], a[52:55]\n" \
"s_add_i32 s6, s9, 0x21c000\n" \
"s_add_i32 s9, s6, s10\n" \
"v_mfma_f32_16x16x32_bf16 a[52:55], v[168:171], v[120:123], a[52:55]\n" \
"s_cmp_lg_u32 s12, 0\n" \
"s_cselect_b32 s6, s6, s14\n" \
"v_mfma_f32_16x16x32_bf16 a[48:51], v[100:103], v[56:59], a[48:51]\n" \
"s_addk_i32 s6, 0x100\n" \
"v_writelane_b32 v11, s9, 43\n" \
"v_mfma_f32_16x16x32_bf16 a[48:51], v[164:167], v[120:123], a[48:51]\n" \
"ds_read_b128 v[56:59], v28 offset:34816\n" \
"ds_read_b128 v[120:123], v29 offset:34816\n" \
"s_waitcnt lgkmcnt(15)\n" \
"s_nop 0\n" \
"v_mfma_f32_16x16x32_bf16 a[0:3], v[84:87], v[68:71], a[0:3]\n" \
"s_add_u32 m0, s30, 0x1c00\n" \
"s_mov_b32 s84, 0xff00\n" \
"v_mfma_f32_16x16x32_bf16 a[0:3], v[148:151], v[132:135], a[0:3]\n" \
"buffer_load_dwordx4 v25, s[76:79], s84 offen lds\n" \
"v_mfma_f32_16x16x32_bf16 a[4:7], v[88:91], v[68:71], a[4:7]\n" \
"ds_read_b64_tr_b16 v[100:101], v36 offset:32768\n" \
"v_mfma_f32_16x16x32_bf16 a[4:7], v[152:155], v[132:135], a[4:7]\n" \
"s_add_u32 m0, s31, 0xc00\n" \
"s_mov_b32 s85, 0x318000\n" \
"v_mfma_f32_16x16x32_bf16 a[36:39], v[96:99], v[68:71], a[36:39]\n" \
"buffer_load_dwordx4 v27, s[68:71], s85 offen lds\n" \
"s_waitcnt lgkmcnt(13)\n" \
"v_mfma_f32_16x16x32_bf16 a[36:39], v[160:163], v[132:135], a[36:39]\n" \
"ds_read_b64_tr_b16 v[102:103], v36 offset:33792\n" \
"s_waitcnt lgkmcnt(10)\n" \
"v_mfma_f32_16x16x32_bf16 a[32:35], v[68:71], v[92:95], a[32:35]\n" \
".fill (L_pb + 77536 - .) / 4, 4, 0xBF800000\n" \
"L_pp19:\n" \
"s_cbranch_execz L_pret\n" \
"ds_read_b64_tr_b16 v[164:165], v36 offset:40960\n" \
"s_waitcnt lgkmcnt(7)\n" \
"v_mfma_f32_16x16x32_bf16 a[32:35], v[132:135], v[156:159], a[32:35]\n" \
"ds_read_b64_tr_b16 v[166:167], v36 offset:41984\n" \
"v_mfma_f32_16x16x32_bf16 a[96:99], v[72:75], v[92:95], a[96:99]\n" \
"ds_read_b64_tr_b16 v[104:105], v37 offset:32768\n" \
"v_mfma_f32_16x16x32_bf16 a[96:99], v[136:139], v[156:159], a[96:99]\n" \
"ds_read_b64_tr_b16 v[106:107], v37 offset:33792\n" \
"v_mfma_f32_16x16x32_bf16 a[100:103], v[96:99], v[72:75], a[100:103]\n" \
"ds_read_b64_tr_b16 v[168:169], v37 offset:40960\n" \
"v_mfma_f32_16x16x32_bf16 a[100:103], v[160:163], v[136:139], a[100:103]\n" \
"ds_read_b64_tr_b16 v[170:171], v37 offset:41984\n" \
"v_mfma_f32_16x16x32_bf16 a[68:71], v[88:91], v[72:75], a[68:71]\n" \
"s_cmp_lg_u32 s16, 0\n" \
"s_cselect_b32 s6, s7, s6\n" \
"v_mfma_f32_16x16x32_bf16 a[68:71], v[152:155], v[136:139], a[68:71]\n" \
"s_add_u32 s88, s42, 0x2000\n" \
"buffer_store_dwordx4 v[244:247], v20, s[80:83], s88 offen offset:0 sc1\n" \
"v_mfma_f32_16x16x32_bf16 a[64:67], v[84:87], v[72:75], a[64:67]\n" \
"ds_read_b64_tr_b16 v[108:109], v38 offset:32768\n" \
"v_writelane_b32 v11, s6, 44\n" \
"v_mfma_f32_16x16x32_bf16 a[64:67], v[148:151], v[136:139], a[64:67]\n" \
"ds_read_b64_tr_b16 v[110:111], v38 offset:33792\n" \
"s_or_b32 s6, s8, s15\n" \
"v_mfma_f32_16x16x32_bf16 a[128:131], v[84:87], v[76:79], a[128:131]\n" \
"ds_read_b64_tr_b16 v[172:173], v38 offset:40960\n" \
"v_writelane_b32 v11, s6, 52\n" \
"v_mfma_f32_16x16x32_bf16 a[128:131], v[148:151], v[140:143], a[128:131]\n" \
"ds_read_b64_tr_b16 v[174:175], v38 offset:41984\n" \
"s_lshl_b32 s6, s3, 17\n" \
"v_mfma_f32_16x16x32_bf16 a[132:135], v[88:91], v[76:79], a[132:135]\n" \
"s_add_i32 s6, s13, s6\n" \
"s_or_b32 s5, s6, s5\n" \
"v_mfma_f32_16x16x32_bf16 a[132:135], v[152:155], v[140:143], a[132:135]\n" \
"s_add_u32 s89, s42, 0x1000\n" \
"buffer_store_dwordx4 v[184:187], v20, s[80:83], s89 offen offset:0 sc1\n" \
"v_mfma_f32_16x16x32_bf16 a[164:167], v[96:99], v[76:79], a[164:167]\n" \
"ds_read_b64_tr_b16 v[112:113], v39 offset:32768\n" \
"s_or_b32 s5, s5, 0x3840\n" \
"v_mfma_f32_16x16x32_bf16 a[164:167], v[160:163], v[140:143], a[164:167]\n" \
"ds_read_b64_tr_b16 v[114:115], v39 offset:33792\n" \
"v_writelane_b32 v11, s5, 6\n" \
"v_mfma_f32_16x16x32_bf16 a[160:163], v[76:79], v[92:95], a[160:163]\n" \
"ds_read_b64_tr_b16 v[176:177], v39 offset:40960\n" \
"s_mov_b32 s5, 0xff0000\n" \
"v_mfma_f32_16x16x32_bf16 a[160:163], v[140:143], v[156:159], a[160:163]\n" \
"s_cmp_lg_u32 s16, 0\n" \
"s_cselect_b32 s5, s17, s5\n" \
"v_mfma_f32_16x16x32_bf16 a[224:227], v[80:83], v[92:95], a[224:227]\n" \
"ds_read_b64_tr_b16 v[178:179], v39 offset:41984\n" \
"v_writelane_b32 v11, s5, 7\n" \
"v_mfma_f32_16x16x32_bf16 a[224:227], v[144:147], v[156:159], a[224:227]\n" \
"s_mul_i32 s5, s3, 0x1200\n" \
"s_add_i32 s4, s5, s4\n" \
"v_mfma_f32_16x16x32_bf16 a[228:231], v[96:99], v[80:83], a[228:231]\n" \
"s_add_u32 s88, s42, 0x1000\n" \
"buffer_store_dwordx4 v[188:191], v20, s[80:83], s88 offen offset:1024 sc1\n" \
"v_mfma_f32_16x16x32_bf16 a[228:231], v[160:163], v[144:147], a[228:231]\n" \
"s_add_i32 s4, s4, s11\n" \
"v_writelane_b32 v11, s4, 8\n" \
"v_mfma_f32_16x16x32_bf16 a[196:199], v[88:91], v[80:83], a[196:199]\n" \
"s_lshl_b32 s4, s2, 16\n" \
"s_add_i32 s5, s4, 0x10000\n" \
"v_mfma_f32_16x16x32_bf16 a[196:199], v[152:155], v[144:147], a[196:199]\n" \
"s_and_b32 s5, s5, 0x70000\n" \
"s_add_i32 s5, s13, s5\n" \
"v_mfma_f32_16x16x32_bf16 a[192:195], v[84:87], v[80:83], a[192:195]\n" \
"v_writelane_b32 v11, s5, 45\n" \
"s_add_i32 s5, s4, 0x20000\n" \
"v_mfma_f32_16x16x32_bf16 a[192:195], v[148:151], v[144:147], a[192:195]\n" \
"s_and_b32 s5, s5, 0x70000\n" \
"s_add_i32 s5, s13, s5\n" \
"v_mfma_f32_16x16x32_bf16 a[8:11], v[88:91], v[64:67], a[8:11]\n" \
"s_bitset1_b32 s5, 11\n" \
"v_writelane_b32 v11, s5, 46\n" \
"v_mfma_f32_16x16x32_bf16 a[8:11], v[152:155], v[128:131], a[8:11]\n" \
"s_add_i32 s5, s4, 0x30000\n" \
"s_and_b32 s5, s5, 0x70000\n" \
"v_mfma_f32_16x16x32_bf16 a[40:43], v[96:99], v[64:67], a[40:43]\n" \
"s_add_i32 s5, s13, s5\n" \
"s_bitset1_b32 s5, 12\n" \
"v_mfma_f32_16x16x32_bf16 a[40:43], v[160:163], v[128:131], a[40:43]\n" \
"v_writelane_b32 v11, s5, 47\n" \
"s_and_b32 s5, s4, 0x70000\n" \
"v_mfma_f32_16x16x32_bf16 a[12:15], v[64:67], v[92:95], a[12:15]\n" \
"s_xor_b32 s5, s5, 0x40000\n" \
"s_add_i32 s5, s13, s5\n" \
"v_mfma_f32_16x16x32_bf16 a[12:15], v[128:131], v[156:159], a[12:15]\n" \
"s_waitcnt vmcnt(3) lgkmcnt(0)\n" \
"s_barrier\n" \
"s_nop 0\n" \
"v_mfma_f32_16x16x32_bf16 a[144:147], v[100:103], v[68:71], a[144:147]\n" \
"s_add_u32 m0, s40, 0x8000\n" \
"s_add_u32 s85, s39, 0xe000\n" \
"v_mfma_f32_16x16x32_bf16 a[144:147], v[164:167], v[132:135], a[144:147]\n" \
"buffer_load_dwordx4 v20, s[80:83], s85 offen sc1 lds\n" \
"v_mfma_f32_16x16x32_bf16 a[148:151], v[104:107], v[68:71], a[148:151]\n" \
"ds_read_b64_tr_b16 v[84:85], v32 offset:0\n" \
"s_or_b32 s5, s5, 0x1800\n" \
"v_mfma_f32_16x16x32_bf16 a[148:151], v[168:171], v[132:135], a[148:151]\n" \
"ds_read_b64_tr_b16 v[86:87], v32 offset:512\n" \
"v_writelane_b32 v11, s5, 48\n" \
"v_mfma_f32_16x16x32_bf16 a[200:203], v[108:111], v[68:71], a[200:203]\n" \
"s_add_u32 m0, s40, 0x9000\n" \
"s_add_u32 s84, s39, 0xf000\n" \
"v_mfma_f32_16x16x32_bf16 a[200:203], v[172:175], v[132:135], a[200:203]\n" \
"buffer_load_dwordx4 v20, s[80:83], s84 offen sc1 lds\n" \
"v_mfma_f32_16x16x32_bf16 a[204:207], v[112:115], v[68:71], a[204:207]\n" \
"ds_read_b64_tr_b16 v[148:149], v32 offset:4096\n" \
"s_add_i32 s5, s4, 0x50000\n" \
"v_mfma_f32_16x16x32_bf16 a[204:207], v[176:179], v[132:135], a[204:207]\n" \
"ds_read_b128 v[68:71], v28 offset:8192\n" \
"s_and_b32 s5, s5, 0x70000\n" \
"v_mfma_f32_16x16x32_bf16 a[236:239], v[112:115], v[72:75], a[236:239]\n" \
"ds_read_b128 v[132:135], v29 offset:8192\n" \
"s_add_i32 s5, s13, s5\n" \
"v_mfma_f32_16x16x32_bf16 a[236:239], v[176:179], v[136:139], a[236:239]\n" \
"s_add_u32 m0, s40, 0xb000\n" \
"s_mov_b32 s85, 0x380\n" \
"v_mfma_f32_16x16x32_bf16 a[232:235], v[108:111], v[72:75], a[232:235]\n" \
"buffer_load_dwordx4 v21, s[76:79], s85 offen lds\n" \
"v_mfma_f32_16x16x32_bf16 a[232:235], v[172:175], v[136:139], a[232:235]\n" \
"ds_read_b64_tr_b16 v[150:151], v32 offset:4608\n" \
"s_bitset1_b32 s5, 13\n" \
"v_mfma_f32_16x16x32_bf16 a[180:183], v[104:107], v[72:75], a[180:183]\n" \
"s_add_u32 m0, s30, 0x8000\n" \
"s_mov_b32 s84, 0x380\n" \
"v_mfma_f32_16x16x32_bf16 a[180:183], v[168:171], v[136:139], a[180:183]\n" \
"buffer_load_dwordx4 v24, s[76:79], s84 offen lds\n" \
"v_mfma_f32_16x16x32_bf16 a[176:179], v[100:103], v[72:75], a[176:179]\n" \
"ds_read_b64_tr_b16 v[88:89], v33 offset:0\n" \
"v_writelane_b32 v11, s5, 49\n" \
"v_mfma_f32_16x16x32_bf16 a[176:179], v[164:167], v[136:139], a[176:179]\n" \
"ds_read_b128 v[72:75], v28 offset:10240\n" \
"s_add_i32 s5, s4, 0x60000\n" \
"v_mfma_f32_16x16x32_bf16 a[72:75], v[100:103], v[76:79], a[72:75]\n" \
"ds_read_b128 v[136:139], v29 offset:10240\n" \
"s_and_b32 s5, s5, 0x70000\n" \
"v_mfma_f32_16x16x32_bf16 a[72:75], v[164:167], v[140:143], a[72:75]\n" \
"s_add_u32 m0, s30, 0x8400\n" \
"s_mov_b32 s85, 0x2780\n" \
"v_mfma_f32_16x16x32_bf16 a[76:79], v[104:107], v[76:79], a[76:79]\n" \
"buffer_load_dwordx4 v25, s[76:79], s85 offen lds\n" \
"v_mfma_f32_16x16x32_bf16 a[76:79], v[168:171], v[140:143], a[76:79]\n" \
"ds_read_b64_tr_b16 v[90:91], v33 offset:512\n" \
"s_add_i32 s4, s4, 0x70000\n" \
"v_mfma_f32_16x16x32_bf16 a[104:107], v[108:111], v[76:79], a[104:107]\n" \
"ds_read_b64_tr_b16 v[152:153], v33 offset:4096\n" \
"s_add_i32 s5, s13, s5\n" \
"s_nop 0\n" \
"v_mfma_f32_16x16x32_bf16 a[104:107], v[172:175], v[140:143], a[104:107]\n" \
"s_add_u32 m0, s31, 0x8000\n" \
"s_mov_b32 s84, 0x380000\n" \
"v_mfma_f32_16x16x32_bf16 a[108:111], v[112:115], v[76:79], a[108:111]\n" \
"buffer_load_dwordx4 v26, s[68:71], s84 offen lds\n" \
"v_mfma_f32_16x16x32_bf16 a[108:111], v[176:179], v[140:143], a[108:111]\n" \
"ds_read_b128 v[76:79], v28 offset:12288\n" \
"s_and_b32 s4, s4, 0x70000\n" \
"v_mfma_f32_16x16x32_bf16 a[172:175], v[112:115], v[80:83], a[172:175]\n" \
"ds_read_b128 v[140:143], v29 offset:12288\n" \
"s_or_b32 s5, s5, 0x2800\n" \
"v_mfma_f32_16x16x32_bf16 a[172:175], v[176:179], v[144:147], a[172:175]\n" \
"s_add_u32 m0, s30, 0x8800\n" \
"s_mov_b32 s85, 0x4b80\n" \
"v_mfma_f32_16x16x32_bf16 a[168:171], v[108:111], v[80:83], a[168:171]\n" \
"buffer_load_dwordx4 v24, s[76:79], s85 offen lds\n" \
"v_mfma_f32_16x16x32_bf16 a[168:171], v[172:175], v[144:147], a[168:171]\n" \
"ds_read_b64_tr_b16 v[154:155], v33 offset:4608\n" \
"v_writelane_b32 v11, s5, 50\n" \
"v_mfma_f32_16x16x32_bf16 a[140:143], v[104:107], v[80:83], a[140:143]\n" \
"ds_read_b64_tr_b16 v[96:97], v35 offset:0\n" \
"s_add_i32 s13, s13, s4\n" \
"v_mfma_f32_16x16x32_bf16 a[140:143], v[168:171], v[144:147], a[140:143]\n" \
"ds_read_b64_tr_b16 v[98:99], v35 offset:512\n" \
"s_or_b32 s4, s13, 0x3000\n" \
"v_mfma_f32_16x16x32_bf16 a[136:139], v[100:103], v[80:83], a[136:139]\n" \
"v_writelane_b32 v11, s4, 51\n" \
"s_movk_i32 s11, 0x4000\n" \
"v_mfma_f32_16x16x32_bf16 a[136:139], v[164:167], v[144:147], a[136:139]\n" \
"ds_read_b128 v[80:83], v28 offset:14336\n" \
"s_lshl_b32 s4, s2, 2\n" \
"v_mfma_f32_16x16x32_bf16 a[80:83], v[100:103], v[60:63], a[80:83]\n" \
"ds_read_b128 v[144:147], v29 offset:14336\n" \
"s_and_b32 s4, s4, 28\n" \
"v_mfma_f32_16x16x32_bf16 a[80:83], v[164:167], v[124:127], a[80:83]\n" \
"s_add_u32 m0, s30, 0x8c00\n" \
"s_mov_b32 s84, 0x6f80\n" \
"v_mfma_f32_16x16x32_bf16 a[84:87], v[104:107], v[60:63], a[84:87]\n" \
"buffer_load_dwordx4 v25, s[76:79], s84 offen lds\n" \
"v_mfma_f32_16x16x32_bf16 a[84:87], v[168:171], v[124:127], a[84:87]\n" \
"s_add_u32 m0, s31, 0x8400\n" \
"s_mov_b32 s85, 0x388000\n" \
"v_mfma_f32_16x16x32_bf16 a[88:91], v[108:111], v[60:63], a[88:91]\n" \
"buffer_load_dwordx4 v26, s[68:71], s85 offen lds\n" \
"v_mfma_f32_16x16x32_bf16 a[88:91], v[172:175], v[124:127], a[88:91]\n" \
"ds_read_b64_tr_b16 v[160:161], v35 offset:4096\n" \
"s_bfe_u32 s5, s2, 0x20003\n" \
"v_mfma_f32_16x16x32_bf16 a[92:95], v[112:115], v[60:63], a[92:95]\n" \
"ds_read_b64_tr_b16 v[162:163], v35 offset:4608\n" \
"s_or_b32 s4, s4, s5\n" \
"v_mfma_f32_16x16x32_bf16 a[92:95], v[176:179], v[124:127], a[92:95]\n" \
"ds_read_b128 v[60:63], v28 offset:4096\n" \
"s_lshr_b32 s5, s2, 5\n" \
"v_mfma_f32_16x16x32_bf16 a[124:127], v[112:115], v[64:67], a[124:127]\n" \
"ds_read_b128 v[124:127], v29 offset:4096\n" \
"s_lshr_b32 s19, s3, 1\n" \
"s_nop 0\n" \
"v_mfma_f32_16x16x32_bf16 a[124:127], v[176:179], v[128:131], a[124:127]\n" \
"s_add_u32 m0, s30, 0x9000\n" \
"s_mov_b32 s84, 0x9380\n" \
"v_mfma_f32_16x16x32_bf16 a[120:123], v[108:111], v[64:67], a[120:123]\n" \
"buffer_load_dwordx4 v24, s[76:79], s84 offen lds\n" \
"v_mfma_f32_16x16x32_bf16 a[120:123], v[172:175], v[128:131], a[120:123]\n" \
"s_add_u32 m0, s30, 0x9400\n" \
"s_mov_b32 s85, 0xb780\n" \
"v_mfma_f32_16x16x32_bf16 a[116:119], v[104:107], v[64:67], a[116:119]\n" \
"buffer_load_dwordx4 v25, s[76:79], s85 offen lds\n" \
"v_mfma_f32_16x16x32_bf16 a[116:119], v[168:171], v[128:131], a[116:119]\n" \
"ds_read_b64_tr_b16 v[92:93], v34 offset:0\n" \
"s_lshl_b32 s18, s5, 1\n" \
"v_mfma_f32_16x16x32_bf16 a[112:115], v[100:103], v[64:67], a[112:115]\n" \
"ds_read_b64_tr_b16 v[94:95], v34 offset:512\n" \
"s_lshl_b32 s6, s4, 20\n" \
"v_mfma_f32_16x16x32_bf16 a[112:115], v[164:167], v[128:131], a[112:115]\n" \
"ds_read_b128 v[64:67], v28 offset:6144\n" \
"s_lshl_b32 s7, s4, 8\n" \
"v_mfma_f32_16x16x32_bf16 a[16:19], v[100:103], v[52:55], a[16:19]\n" \
"ds_read_b128 v[128:131], v29 offset:6144\n" \
"s_and_b32 s13, s3, 1\n" \
"v_mfma_f32_16x16x32_bf16 a[16:19], v[164:167], v[116:119], a[16:19]\n" \
"s_add_u32 m0, s31, 0x8800\n" \
"s_mov_b32 s84, 0x390000\n" \
"v_mfma_f32_16x16x32_bf16 a[20:23], v[104:107], v[52:55], a[20:23]\n" \
"buffer_load_dwordx4 v27, s[68:71], s84 offen lds\n" \
"v_mfma_f32_16x16x32_bf16 a[20:23], v[168:171], v[116:119], a[20:23]\n" \
"s_add_u32 m0, s30, 0x9800\n" \
"s_mov_b32 s85, 0xdb80\n" \
"v_mfma_f32_16x16x32_bf16 a[24:27], v[108:111], v[52:55], a[24:27]\n" \
"buffer_load_dwordx4 v24, s[76:79], s85 offen lds\n" \
"v_mfma_f32_16x16x32_bf16 a[24:27], v[172:175], v[116:119], a[24:27]\n" \
"ds_read_b64_tr_b16 v[156:157], v34 offset:4096\n" \
"s_add_i32 s14, s18, s19\n" \
"v_mfma_f32_16x16x32_bf16 a[28:31], v[112:115], v[52:55], a[28:31]\n" \
"ds_read_b64_tr_b16 v[158:159], v34 offset:4608\n" \
"v_mfma_f32_16x16x32_bf16 a[28:31], v[176:179], v[116:119], a[28:31]\n" \
"ds_read_b128 v[52:55], v28 offset:0\n" \
"v_mfma_f32_16x16x32_bf16 a[60:63], v[112:115], v[56:59], a[60:63]\n" \
"ds_read_b128 v[116:119], v29 offset:0\n" \
"v_mfma_f32_16x16x32_bf16 a[60:63], v[176:179], v[120:123], a[60:63]\n" \
"s_add_u32 s6, s26, s6\n" \
"s_addc_u32 s9, s27, 0\n" \
"v_mfma_f32_16x16x32_bf16 a[56:59], v[108:111], v[56:59], a[56:59]\n" \
"s_add_u32 s8, s6, 0x1000000\n" \
"s_addc_u32 s6, s9, 0\n" \
"v_mfma_f32_16x16x32_bf16 a[56:59], v[172:175], v[120:123], a[56:59]\n" \
"s_and_b32 s9, s6, 0xffff\n" \
"s_lshl_b32 s6, s5, 16\n" \
"v_mfma_f32_16x16x32_bf16 a[52:55], v[104:107], v[56:59], a[52:55]\n" \
"s_sub_i32 s10, 0x100000, s6\n" \
"s_mov_b32 s6, 0x8000\n" \
"v_mfma_f32_16x16x32_bf16 a[52:55], v[168:171], v[120:123], a[52:55]\n" \
"s_cmp_lg_u32 s5, 0\n" \
"s_cselect_b32 s17, s11, s6\n" \
"v_mfma_f32_16x16x32_bf16 a[48:51], v[100:103], v[56:59], a[48:51]\n" \
"s_lshl_b32 s6, s3, 1\n" \
"s_lshl_b32 s5, s5, 3\n" \
"v_mfma_f32_16x16x32_bf16 a[48:51], v[164:167], v[120:123], a[48:51]\n" \
"ds_read_b128 v[56:59], v28 offset:2048\n" \
"ds_read_b128 v[120:123], v29 offset:2048\n" \
"s_waitcnt lgkmcnt(15)\n" \
"v_mfma_f32_16x16x32_bf16 a[0:3], v[84:87], v[68:71], a[0:3]\n" \
"s_add_u32 m0, s30, 0x9c00\n" \
"s_mov_b32 s85, 0xff80\n" \
"v_mfma_f32_16x16x32_bf16 a[0:3], v[148:151], v[132:135], a[0:3]\n" \
"buffer_load_dwordx4 v25, s[76:79], s85 offen lds\n" \
"v_mfma_f32_16x16x32_bf16 a[4:7], v[88:91], v[68:71], a[4:7]\n" \
"ds_read_b64_tr_b16 v[100:101], v36 offset:0\n" \
"s_and_b32 s6, s6, 4\n" \
"v_mfma_f32_16x16x32_bf16 a[4:7], v[152:155], v[132:135], a[4:7]\n" \
"s_add_u32 m0, s31, 0x8c00\n" \
"s_mov_b32 s84, 0x398000\n" \
"v_mfma_f32_16x16x32_bf16 a[36:39], v[96:99], v[68:71], a[36:39]\n" \
"buffer_load_dwordx4 v27, s[68:71], s84 offen lds\n" \
"s_waitcnt lgkmcnt(13)\n" \
"v_mfma_f32_16x16x32_bf16 a[36:39], v[160:163], v[132:135], a[36:39]\n" \
"ds_read_b64_tr_b16 v[102:103], v36 offset:1024\n" \
"s_or_b32 s12, s5, s6\n" \
"s_waitcnt lgkmcnt(10)\n" \
"v_mfma_f32_16x16x32_bf16 a[32:35], v[68:71], v[92:95], a[32:35]\n" \
"ds_read_b64_tr_b16 v[164:165], v36 offset:8192\n" \
"s_sub_i32 s5, s13, s5\n" \
"s_waitcnt lgkmcnt(7)\n" \
"v_mfma_f32_16x16x32_bf16 a[32:35], v[132:135], v[156:159], a[32:35]\n" \
"ds_read_b64_tr_b16 v[166:167], v36 offset:9216\n" \
"s_or_b32 s5, s5, s6\n" \
"v_mfma_f32_16x16x32_bf16 a[96:99], v[72:75], v[92:95], a[96:99]\n" \
"ds_read_b64_tr_b16 v[104:105], v37 offset:0\n" \
"s_or_b32 s6, s12, s13\n" \
"v_mfma_f32_16x16x32_bf16 a[96:99], v[136:139], v[156:159], a[96:99]\n" \
"ds_read_b64_tr_b16 v[106:107], v37 offset:1024\n" \
"s_lshl_b32 s4, s4, 2\n" \
"v_mfma_f32_16x16x32_bf16 a[100:103], v[96:99], v[72:75], a[100:103]\n" \
"ds_read_b64_tr_b16 v[168:169], v37 offset:8192\n" \
"s_mulk_i32 s6, 0x92\n" \
"v_mfma_f32_16x16x32_bf16 a[100:103], v[160:163], v[136:139], a[100:103]\n" \
"ds_read_b64_tr_b16 v[170:171], v37 offset:9216\n" \
"s_add_i32 s4, s4, 18\n" \
"v_mfma_f32_16x16x32_bf16 a[68:71], v[88:91], v[72:75], a[68:71]\n" \
"s_mulk_i32 s5, 0x92\n" \
"s_add_i32 s6, s4, s6\n" \
"v_mfma_f32_16x16x32_bf16 a[68:71], v[152:155], v[136:139], a[68:71]\n" \
"s_add_u32 s89, s42, 0x1000\n" \
"buffer_store_dwordx4 v[192:195], v20, s[80:83], s89 offen offset:2048 sc1\n" \
"v_mfma_f32_16x16x32_bf16 a[64:67], v[84:87], v[72:75], a[64:67]\n" \
"ds_read_b64_tr_b16 v[108:109], v38 offset:0\n" \
"s_add_i32 s5, s5, s4\n" \
"v_mfma_f32_16x16x32_bf16 a[64:67], v[148:151], v[136:139], a[64:67]\n" \
"ds_read_b64_tr_b16 v[110:111], v38 offset:1024\n" \
"s_lshl_b32 s4, s3, 11\n" \
"v_mfma_f32_16x16x32_bf16 a[128:131], v[84:87], v[76:79], a[128:131]\n" \
"ds_read_b64_tr_b16 v[172:173], v38 offset:8192\n" \
"s_lshl_b32 s15, s13, 12\n" \
"v_mfma_f32_16x16x32_bf16 a[128:131], v[148:151], v[140:143], a[128:131]\n" \
"ds_read_b64_tr_b16 v[174:175], v38 offset:9216\n" \
"s_lshl_b32 s16, s19, 14\n" \
"s_nop 0\n" \
"v_mfma_f32_16x16x32_bf16 a[132:135], v[88:91], v[76:79], a[132:135]\n" \
"s_mul_i32 s19, s19, 0xe000\n" \
"s_mov_b32 s11, 0x20000\n" \
"v_mfma_f32_16x16x32_bf16 a[132:135], v[152:155], v[140:143], a[132:135]\n" \
"s_add_u32 s88, s42, 0x1000\n" \
"buffer_store_dwordx4 v[196:199], v20, s[80:83], s88 offen offset:3072 sc1\n" \
"v_mfma_f32_16x16x32_bf16 a[164:167], v[96:99], v[76:79], a[164:167]\n" \
"ds_read_b64_tr_b16 v[112:113], v39 offset:0\n" \
"s_or_b32 s12, s4, 0xc000\n" \
"v_mfma_f32_16x16x32_bf16 a[164:167], v[160:163], v[140:143], a[164:167]\n" \
"ds_read_b64_tr_b16 v[114:115], v39 offset:1024\n" \
"s_lshl_b32 s4, s3, 8\n" \
"v_mfma_f32_16x16x32_bf16 a[160:163], v[76:79], v[92:95], a[160:163]\n" \
"ds_read_b64_tr_b16 v[176:177], v39 offset:8192\n" \
"s_or_b32 s22, s15, s16\n" \
"v_mfma_f32_16x16x32_bf16 a[160:163], v[140:143], v[156:159], a[160:163]\n" \
"s_or_b32 s23, s19, s15\n" \
"s_lshl_b32 s60, s6, 10\n" \
"v_mfma_f32_16x16x32_bf16 a[224:227], v[80:83], v[92:95], a[224:227]\n" \
"ds_read_b64_tr_b16 v[178:179], v39 offset:9216\n" \
"s_lshl_b32 s61, s5, 10\n" \
"v_mfma_f32_16x16x32_bf16 a[224:227], v[144:147], v[156:159], a[224:227]\n" \
"s_lshl_b32 s14, s14, 15\n" \
"s_or_b32 s20, s4, 0x26c00\n" \
"v_mfma_f32_16x16x32_bf16 a[228:231], v[96:99], v[80:83], a[228:231]\n" \
"s_add_u32 s89, s42, 0x3000\n" \
"buffer_store_dwordx4 v[200:203], v20, s[80:83], s89 offen offset:0 sc1\n" \
"v_mfma_f32_16x16x32_bf16 a[228:231], v[160:163], v[144:147], a[228:231]\n" \
"s_or_b32 s13, s22, 0x8000\n" \
"s_add_i32 s22, s22, 0x14000\n" \
"v_mfma_f32_16x16x32_bf16 a[196:199], v[88:91], v[80:83], a[196:199]\n" \
"s_bitset1_b32 s23, 16\n" \
"s_or_b32 s24, s7, 0x5be8020\n" \
"v_mfma_f32_16x16x32_bf16 a[196:199], v[152:155], v[144:147], a[196:199]\n" \
"s_and_b32 s57, s27, 0xffff\n" \
"s_mov_b32 s58, 0x6000000\n" \
"v_mfma_f32_16x16x32_bf16 a[192:195], v[84:87], v[80:83], a[192:195]\n" \
"s_mov_b32 s56, s26\n" \
"s_mov_b32 s59, s11\n" \
"v_mfma_f32_16x16x32_bf16 a[192:195], v[148:151], v[144:147], a[192:195]\n" \
"s_add_i32 s60, s60, 0x3000000\n" \
"s_add_i32 s61, s61, 0x411c000\n" \
"v_mfma_f32_16x16x32_bf16 a[8:11], v[88:91], v[64:67], a[8:11]\n" \
"s_sub_i32 s19, 14, s18\n" \
"s_mov_b32 s21, 0x71800000\n" \
"v_mfma_f32_16x16x32_bf16 a[8:11], v[152:155], v[128:131], a[8:11]\n" \
"v_mfma_f32_16x16x32_bf16 a[40:43], v[96:99], v[64:67], a[40:43]\n" \
"v_mfma_f32_16x16x32_bf16 a[40:43], v[160:163], v[128:131], a[40:43]\n" \
"v_mfma_f32_16x16x32_bf16 a[12:15], v[64:67], v[92:95], a[12:15]\n" \
"v_mfma_f32_16x16x32_bf16 a[12:15], v[128:131], v[156:159], a[12:15]\n" \
"s_waitcnt vmcnt(3) lgkmcnt(0)\n" \
"s_barrier\n" \
"v_mfma_f32_16x16x32_bf16 a[144:147], v[100:103], v[68:71], a[144:147]\n" \
"s_add_u32 m0, s40, 0x0\n" \
"s_nop 0\n" \
"s_add_u32 s84, s39, 0x10000\n" \
"v_mfma_f32_16x16x32_bf16 a[144:147], v[164:167], v[132:135], a[144:147]\n" \
"buffer_load_dwordx4 v20, s[80:83], s84 offen sc1 lds\n" \
"v_mfma_f32_16x16x32_bf16 a[148:151], v[104:107], v[68:71], a[148:151]\n" \
"ds_read_b64_tr_b16 v[84:85], v32 offset:32768\n" \
"v_mfma_f32_16x16x32_bf16 a[148:151], v[168:171], v[132:135], a[148:151]\n" \
"ds_read_b64_tr_b16 v[86:87], v32 offset:33280\n" \
"v_mfma_f32_16x16x32_bf16 a[200:203], v[108:111], v[68:71], a[200:203]\n" \
"s_add_u32 m0, s40, 0x1000\n" \
"s_add_u32 s85, s39, 0x11000\n" \
"v_mfma_f32_16x16x32_bf16 a[200:203], v[172:175], v[132:135], a[200:203]\n" \
"buffer_load_dwordx4 v20, s[80:83], s85 offen sc1 lds\n" \
"v_mfma_f32_16x16x32_bf16 a[204:207], v[112:115], v[68:71], a[204:207]\n" \
"ds_read_b64_tr_b16 v[148:149], v32 offset:36864\n" \
"v_mfma_f32_16x16x32_bf16 a[204:207], v[176:179], v[132:135], a[204:207]\n" \
"ds_read_b128 v[68:71], v28 offset:40960\n" \
"v_mfma_f32_16x16x32_bf16 a[236:239], v[112:115], v[72:75], a[236:239]\n" \
"ds_read_b128 v[132:135], v29 offset:40960\n" \
"v_mfma_f32_16x16x32_bf16 a[236:239], v[176:179], v[136:139], a[236:239]\n" \
"s_add_u32 m0, s40, 0x3000\n" \
"s_mov_b32 s84, 0x400\n" \
"v_mfma_f32_16x16x32_bf16 a[232:235], v[108:111], v[72:75], a[232:235]\n" \
"buffer_load_dwordx4 v21, s[76:79], s84 offen lds\n" \
"v_mfma_f32_16x16x32_bf16 a[232:235], v[172:175], v[136:139], a[232:235]\n" \
"ds_read_b64_tr_b16 v[150:151], v32 offset:37376\n" \
"v_mfma_f32_16x16x32_bf16 a[180:183], v[104:107], v[72:75], a[180:183]\n" \
"s_add_u32 m0, s30, 0x0\n" \
"s_nop 0\n" \
"s_mov_b32 s85, 0x400\n" \
"v_mfma_f32_16x16x32_bf16 a[180:183], v[168:171], v[136:139], a[180:183]\n" \
"buffer_load_dwordx4 v24, s[76:79], s85 offen lds\n" \
"v_mfma_f32_16x16x32_bf16 a[176:179], v[100:103], v[72:75], a[176:179]\n" \
"ds_read_b64_tr_b16 v[88:89], v33 offset:32768\n" \
"v_mfma_f32_16x16x32_bf16 a[176:179], v[164:167], v[136:139], a[176:179]\n" \
"ds_read_b128 v[72:75], v28 offset:43008\n" \
"v_mfma_f32_16x16x32_bf16 a[72:75], v[100:103], v[76:79], a[72:75]\n" \
"ds_read_b128 v[136:139], v29 offset:43008\n" \
"v_mfma_f32_16x16x32_bf16 a[72:75], v[164:167], v[140:143], a[72:75]\n" \
"s_add_u32 m0, s30, 0x400\n" \
"s_mov_b32 s84, 0x2800\n" \
"v_mfma_f32_16x16x32_bf16 a[76:79], v[104:107], v[76:79], a[76:79]\n" \
"buffer_load_dwordx4 v25, s[76:79], s84 offen lds\n" \
"v_mfma_f32_16x16x32_bf16 a[76:79], v[168:171], v[140:143], a[76:79]\n" \
"ds_read_b64_tr_b16 v[90:91], v33 offset:33280\n" \
"v_mfma_f32_16x16x32_bf16 a[104:107], v[108:111], v[76:79], a[104:107]\n" \
"ds_read_b64_tr_b16 v[152:153], v33 offset:36864\n" \
"v_mfma_f32_16x16x32_bf16 a[104:107], v[172:175], v[140:143], a[104:107]\n" \
"s_add_u32 m0, s31, 0x0\n" \
"s_nop 0\n" \
"s_mov_b32 s85, 0x400000\n" \
"v_mfma_f32_16x16x32_bf16 a[108:111], v[112:115], v[76:79], a[108:111]\n" \
"buffer_load_dwordx4 v26, s[68:71], s85 offen lds\n" \
"v_mfma_f32_16x16x32_bf16 a[108:111], v[176:179], v[140:143], a[108:111]\n" \
"ds_read_b128 v[76:79], v28 offset:45056\n" \
"v_mfma_f32_16x16x32_bf16 a[172:175], v[112:115], v[80:83], a[172:175]\n" \
"ds_read_b128 v[140:143], v29 offset:45056\n" \
"v_mfma_f32_16x16x32_bf16 a[172:175], v[176:179], v[144:147], a[172:175]\n" \
"s_add_u32 m0, s30, 0x800\n" \
"s_mov_b32 s84, 0x4c00\n" \
"v_mfma_f32_16x16x32_bf16 a[168:171], v[108:111], v[80:83], a[168:171]\n" \
"buffer_load_dwordx4 v24, s[76:79], s84 offen lds\n" \
"v_mfma_f32_16x16x32_bf16 a[168:171], v[172:175], v[144:147], a[168:171]\n" \
"ds_read_b64_tr_b16 v[154:155], v33 offset:37376\n" \
"v_mfma_f32_16x16x32_bf16 a[140:143], v[104:107], v[80:83], a[140:143]\n" \
"ds_read_b64_tr_b16 v[96:97], v35 offset:32768\n" \
"v_mfma_f32_16x16x32_bf16 a[140:143], v[168:171], v[144:147], a[140:143]\n" \
"ds_read_b64_tr_b16 v[98:99], v35 offset:33280\n" \
"v_mfma_f32_16x16x32_bf16 a[136:139], v[100:103], v[80:83], a[136:139]\n" \
"v_mfma_f32_16x16x32_bf16 a[136:139], v[164:167], v[144:147], a[136:139]\n" \
"ds_read_b128 v[80:83], v28 offset:47104\n" \
"v_mfma_f32_16x16x32_bf16 a[80:83], v[100:103], v[60:63], a[80:83]\n" \
"ds_read_b128 v[144:147], v29 offset:47104\n" \
"v_mfma_f32_16x16x32_bf16 a[80:83], v[164:167], v[124:127], a[80:83]\n" \
"s_add_u32 m0, s30, 0xc00\n" \
"s_mov_b32 s85, 0x7000\n" \
"v_mfma_f32_16x16x32_bf16 a[84:87], v[104:107], v[60:63], a[84:87]\n" \
"buffer_load_dwordx4 v25, s[76:79], s85 offen lds\n" \
"v_mfma_f32_16x16x32_bf16 a[84:87], v[168:171], v[124:127], a[84:87]\n" \
"s_add_u32 m0, s31, 0x400\n" \
"s_mov_b32 s84, 0x408000\n" \
"v_mfma_f32_16x16x32_bf16 a[88:91], v[108:111], v[60:63], a[88:91]\n" \
"buffer_load_dwordx4 v26, s[68:71], s84 offen lds\n" \
"v_mfma_f32_16x16x32_bf16 a[88:91], v[172:175], v[124:127], a[88:91]\n" \
"ds_read_b64_tr_b16 v[160:161], v35 offset:36864\n" \
"v_mfma_f32_16x16x32_bf16 a[92:95], v[112:115], v[60:63], a[92:95]\n" \
"ds_read_b64_tr_b16 v[162:163], v35 offset:37376\n" \
"v_mfma_f32_16x16x32_bf16 a[92:95], v[176:179], v[124:127], a[92:95]\n" \
"ds_read_b128 v[60:63], v28 offset:36864\n" \
"v_mfma_f32_16x16x32_bf16 a[124:127], v[112:115], v[64:67], a[124:127]\n" \
"ds_read_b128 v[124:127], v29 offset:36864\n" \
"v_mfma_f32_16x16x32_bf16 a[124:127], v[176:179], v[128:131], a[124:127]\n" \
"s_add_u32 m0, s30, 0x1000\n" \
"s_mov_b32 s85, 0x9400\n" \
"v_mfma_f32_16x16x32_bf16 a[120:123], v[108:111], v[64:67], a[120:123]\n" \
"buffer_load_dwordx4 v24, s[76:79], s85 offen lds\n" \
"v_mfma_f32_16x16x32_bf16 a[120:123], v[172:175], v[128:131], a[120:123]\n" \
"s_add_u32 m0, s30, 0x1400\n" \
"s_mov_b32 s84, 0xb800\n" \
"v_mfma_f32_16x16x32_bf16 a[116:119], v[104:107], v[64:67], a[116:119]\n" \
"buffer_load_dwordx4 v25, s[76:79], s84 offen lds\n" \
"v_mfma_f32_16x16x32_bf16 a[116:119], v[168:171], v[128:131], a[116:119]\n" \
"ds_read_b64_tr_b16 v[92:93], v34 offset:32768\n" \
"v_mfma_f32_16x16x32_bf16 a[112:115], v[100:103], v[64:67], a[112:115]\n" \
"ds_read_b64_tr_b16 v[94:95], v34 offset:33280\n" \
"v_mfma_f32_16x16x32_bf16 a[112:115], v[164:167], v[128:131], a[112:115]\n" \
"ds_read_b128 v[64:67], v28 offset:38912\n" \
"v_mfma_f32_16x16x32_bf16 a[16:19], v[100:103], v[52:55], a[16:19]\n" \
"ds_read_b128 v[128:131], v29 offset:38912\n" \
"v_mfma_f32_16x16x32_bf16 a[16:19], v[164:167], v[116:119], a[16:19]\n" \
"s_add_u32 m0, s31, 0x800\n" \
"s_mov_b32 s85, 0x410000\n" \
"v_mfma_f32_16x16x32_bf16 a[20:23], v[104:107], v[52:55], a[20:23]\n" \
"buffer_load_dwordx4 v27, s[68:71], s85 offen lds\n" \
"v_mfma_f32_16x16x32_bf16 a[20:23], v[168:171], v[116:119], a[20:23]\n" \
"s_add_u32 m0, s30, 0x1800\n" \
"s_mov_b32 s84, 0xdc00\n" \
"v_mfma_f32_16x16x32_bf16 a[24:27], v[108:111], v[52:55], a[24:27]\n" \
"buffer_load_dwordx4 v24, s[76:79], s84 offen lds\n" \
"v_mfma_f32_16x16x32_bf16 a[24:27], v[172:175], v[116:119], a[24:27]\n" \
"ds_read_b64_tr_b16 v[156:157], v34 offset:36864\n" \
"v_mfma_f32_16x16x32_bf16 a[28:31], v[112:115], v[52:55], a[28:31]\n" \
"ds_read_b64_tr_b16 v[158:159], v34 offset:37376\n" \
"v_mfma_f32_16x16x32_bf16 a[28:31], v[176:179], v[116:119], a[28:31]\n" \
"ds_read_b128 v[52:55], v28 offset:32768\n" \
"v_mfma_f32_16x16x32_bf16 a[60:63], v[112:115], v[56:59], a[60:63]\n" \
"ds_read_b128 v[116:119], v29 offset:32768\n" \
"v_mfma_f32_16x16x32_bf16 a[60:63], v[176:179], v[120:123], a[60:63]\n" \
"v_mfma_f32_16x16x32_bf16 a[56:59], v[108:111], v[56:59], a[56:59]\n" \
"v_mfma_f32_16x16x32_bf16 a[56:59], v[172:175], v[120:123], a[56:59]\n" \
"v_mfma_f32_16x16x32_bf16 a[52:55], v[104:107], v[56:59], a[52:55]\n" \
"v_mfma_f32_16x16x32_bf16 a[52:55], v[168:171], v[120:123], a[52:55]\n" \
"v_mfma_f32_16x16x32_bf16 a[48:51], v[100:103], v[56:59], a[48:51]\n" \
"v_mfma_f32_16x16x32_bf16 a[48:51], v[164:167], v[120:123], a[48:51]\n" \
"ds_read_b128 v[56:59], v28 offset:34816\n" \
"ds_read_b128 v[120:123], v29 offset:34816\n" \
"s_waitcnt lgkmcnt(15)\n" \
"s_nop 0\n" \
"v_mfma_f32_16x16x32_bf16 a[0:3], v[84:87], v[68:71], a[0:3]\n" \
"s_add_u32 m0, s30, 0x1c00\n" \
"s_mov_b32 s84, 0x10000\n" \
"v_mfma_f32_16x16x32_bf16 a[0:3], v[148:151], v[132:135], a[0:3]\n" \
"buffer_load_dwordx4 v25, s[76:79], s84 offen lds\n" \
"v_mfma_f32_16x16x32_bf16 a[4:7], v[88:91], v[68:71], a[4:7]\n" \
"ds_read_b64_tr_b16 v[100:101], v36 offset:32768\n" \
"v_mfma_f32_16x16x32_bf16 a[4:7], v[152:155], v[132:135], a[4:7]\n" \
"s_add_u32 m0, s31, 0xc00\n" \
"s_mov_b32 s85, 0x418000\n" \
"v_mfma_f32_16x16x32_bf16 a[36:39], v[96:99], v[68:71], a[36:39]\n" \
"buffer_load_dwordx4 v27, s[68:71], s85 offen lds\n" \
"s_waitcnt lgkmcnt(13)\n" \
"v_mfma_f32_16x16x32_bf16 a[36:39], v[160:163], v[132:135], a[36:39]\n" \
"ds_read_b64_tr_b16 v[102:103], v36 offset:33792\n" \
"s_waitcnt lgkmcnt(10)\n" \
"v_mfma_f32_16x16x32_bf16 a[32:35], v[68:71], v[92:95], a[32:35]\n" \
"ds_read_b64_tr_b16 v[164:165], v36 offset:40960\n" \
"s_waitcnt lgkmcnt(7)\n" \
"s_nop 0\n" \
"v_mfma_f32_16x16x32_bf16 a[32:35], v[132:135], v[156:159], a[32:35]\n" \
"ds_read_b64_tr_b16 v[166:167], v36 offset:41984\n" \
"v_mfma_f32_16x16x32_bf16 a[96:99], v[72:75], v[92:95], a[96:99]\n" \
"ds_read_b64_tr_b16 v[104:105], v37 offset:32768\n" \
"v_mfma_f32_16x16x32_bf16 a[96:99], v[136:139], v[156:159], a[96:99]\n" \
"ds_read_b64_tr_b16 v[106:107], v37 offset:33792\n" \
"v_mfma_f32_16x16x32_bf16 a[100:103], v[96:99], v[72:75], a[100:103]\n" \
"ds_read_b64_tr_b16 v[168:169], v37 offset:40960\n" \
"v_mfma_f32_16x16x32_bf16 a[100:103], v[160:163], v[136:139], a[100:103]\n" \
"ds_read_b64_tr_b16 v[170:171], v37 offset:41984\n" \
"v_mfma_f32_16x16x32_bf16 a[68:71], v[88:91], v[72:75], a[68:71]\n" \
"v_mfma_f32_16x16x32_bf16 a[68:71], v[152:155], v[136:139], a[68:71]\n" \
"s_add_u32 s88, s42, 0x3000\n" \
"buffer_store_dwordx4 v[204:207], v20, s[80:83], s88 offen offset:1024 sc1\n" \
"v_mfma_f32_16x16x32_bf16 a[64:67], v[84:87], v[72:75], a[64:67]\n" \
"ds_read_b64_tr_b16 v[108:109], v38 offset:32768\n" \
"v_mfma_f32_16x16x32_bf16 a[64:67], v[148:151], v[136:139], a[64:67]\n" \
"ds_read_b64_tr_b16 v[110:111], v38 offset:33792\n" \
".fill (L_pb + 81632 - .) / 4, 4, 0xBF800000\n" \
"L_pp20:\n" \
"s_cbranch_execz L_pret\n" \
"v_mfma_f32_16x16x32_bf16 a[128:131], v[84:87], v[76:79], a[128:131]\n" \
"ds_read_b64_tr_b16 v[172:173], v38 offset:40960\n" \
"v_mfma_f32_16x16x32_bf16 a[128:131], v[148:151], v[140:143], a[128:131]\n" \
"ds_read_b64_tr_b16 v[174:175], v38 offset:41984\n" \
"v_mfma_f32_16x16x32_bf16 a[132:135], v[88:91], v[76:79], a[132:135]\n" \
"v_lshlrev_b32_e32 v45, 3, v20\n" \
"v_add_u32_e64 v45, s30, v45\n" \
"v_mfma_f32_16x16x32_bf16 a[132:135], v[152:155], v[140:143], a[132:135]\n" \
"s_add_u32 s89, s42, 0x3000\n" \
"buffer_store_dwordx4 v[208:211], v20, s[80:83], s89 offen offset:2048 sc1\n" \
"s_getpc_b64 s[86:87]\n" \
"L_ct1_%=:\n" \
"s_add_u32 s86, s86, L_ap0-L_ct1_%=\n" \
"s_addc_u32 s87, s87, 0\n" \
"s_add_u32 m0, s40, 0x2000\n" \
"v_mfma_f32_16x16x32_bf16 a[164:167], v[96:99], v[76:79], a[164:167]\n" \
"global_load_lds_dword v45, s[86:87]\n" \
"v_mfma_f32_16x16x32_bf16 a[164:167], v[160:163], v[140:143], a[164:167]\n" \
"ds_read_b64_tr_b16 v[112:113], v39 offset:32768\n" \
"v_mfma_f32_16x16x32_bf16 a[160:163], v[76:79], v[92:95], a[160:163]\n" \
"ds_read_b64_tr_b16 v[114:115], v39 offset:33792\n" \
"v_mfma_f32_16x16x32_bf16 a[160:163], v[140:143], v[156:159], a[160:163]\n" \
"ds_read_b64_tr_b16 v[176:177], v39 offset:40960\n" \
"v_mfma_f32_16x16x32_bf16 a[224:227], v[80:83], v[92:95], a[224:227]\n" \
"ds_read_b64_tr_b16 v[178:179], v39 offset:41984\n" \
"v_mfma_f32_16x16x32_bf16 a[224:227], v[144:147], v[156:159], a[224:227]\n" \
"v_mfma_f32_16x16x32_bf16 a[228:231], v[96:99], v[80:83], a[228:231]\n" \
"s_add_u32 s88, s42, 0x3000\n" \
"buffer_store_dwordx4 v[212:215], v20, s[80:83], s88 offen offset:3072 sc1\n" \
"v_mfma_f32_16x16x32_bf16 a[228:231], v[160:163], v[144:147], a[228:231]\n" \
"v_mfma_f32_16x16x32_bf16 a[196:199], v[88:91], v[80:83], a[196:199]\n" \
"v_mfma_f32_16x16x32_bf16 a[196:199], v[152:155], v[144:147], a[196:199]\n" \
"v_mfma_f32_16x16x32_bf16 a[192:195], v[84:87], v[80:83], a[192:195]\n" \
"v_mfma_f32_16x16x32_bf16 a[192:195], v[148:151], v[144:147], a[192:195]\n" \
"v_mfma_f32_16x16x32_bf16 a[8:11], v[88:91], v[64:67], a[8:11]\n" \
"v_mfma_f32_16x16x32_bf16 a[8:11], v[152:155], v[128:131], a[8:11]\n" \
"v_mfma_f32_16x16x32_bf16 a[40:43], v[96:99], v[64:67], a[40:43]\n" \
"v_mfma_f32_16x16x32_bf16 a[40:43], v[160:163], v[128:131], a[40:43]\n" \
"v_mfma_f32_16x16x32_bf16 a[12:15], v[64:67], v[92:95], a[12:15]\n" \
"v_mfma_f32_16x16x32_bf16 a[12:15], v[128:131], v[156:159], a[12:15]\n" \
"s_waitcnt vmcnt(4) lgkmcnt(0)\n" \
"s_barrier\n" \
"v_mfma_f32_16x16x32_bf16 a[144:147], v[100:103], v[68:71], a[144:147]\n" \
"ds_read_b64_tr_b16 v[84:85], v32 offset:0\n" \
"v_accvgpr_read_b32 v184, a0\n" \
"v_mfma_f32_16x16x32_bf16 a[144:147], v[164:167], v[132:135], a[144:147]\n" \
"v_accvgpr_read_b32 v185, a1\n" \
"v_accvgpr_read_b32 v186, a2\n" \
"v_mfma_f32_16x16x32_bf16 a[148:151], v[104:107], v[68:71], a[148:151]\n" \
"v_accvgpr_read_b32 v187, a3\n" \
"v_accvgpr_read_b32 v188, a4\n" \
"v_mfma_f32_16x16x32_bf16 a[148:151], v[168:171], v[132:135], a[148:151]\n" \
"ds_read_b64_tr_b16 v[86:87], v32 offset:512\n" \
"v_accvgpr_read_b32 v189, a5\n" \
"v_mfma_f32_16x16x32_bf16 a[200:203], v[108:111], v[68:71], a[200:203]\n" \
"v_accvgpr_read_b32 v190, a6\n" \
"v_accvgpr_read_b32 v191, a7\n" \
"v_mfma_f32_16x16x32_bf16 a[200:203], v[172:175], v[132:135], a[200:203]\n" \
"v_accvgpr_read_b32 v196, a36\n" \
"v_accvgpr_read_b32 v197, a37\n" \
"v_mfma_f32_16x16x32_bf16 a[204:207], v[112:115], v[68:71], a[204:207]\n" \
"ds_read_b64_tr_b16 v[148:149], v32 offset:4096\n" \
"v_accvgpr_read_b32 v198, a38\n" \
"v_mfma_f32_16x16x32_bf16 a[204:207], v[176:179], v[132:135], a[204:207]\n" \
"ds_read_b128 v[68:71], v28 offset:8192\n" \
"v_accvgpr_read_b32 v199, a39\n" \
"v_mfma_f32_16x16x32_bf16 a[236:239], v[112:115], v[72:75], a[236:239]\n" \
"ds_read_b128 v[132:135], v29 offset:8192\n" \
"v_accvgpr_read_b32 v192, a32\n" \
"v_mfma_f32_16x16x32_bf16 a[236:239], v[176:179], v[136:139], a[236:239]\n" \
"ds_read_b64_tr_b16 v[150:151], v32 offset:4608\n" \
"v_accvgpr_read_b32 v193, a33\n" \
"v_mfma_f32_16x16x32_bf16 a[232:235], v[108:111], v[72:75], a[232:235]\n" \
"v_accvgpr_read_b32 v194, a34\n" \
"v_accvgpr_read_b32 v195, a35\n" \
"v_mfma_f32_16x16x32_bf16 a[232:235], v[172:175], v[136:139], a[232:235]\n" \
"v_accvgpr_read_b32 v208, a96\n" \
"v_accvgpr_read_b32 v209, a97\n" \
"v_mfma_f32_16x16x32_bf16 a[180:183], v[104:107], v[72:75], a[180:183]\n" \
"v_accvgpr_read_b32 v210, a98\n" \
"v_accvgpr_read_b32 v211, a99\n" \
"v_mfma_f32_16x16x32_bf16 a[180:183], v[168:171], v[136:139], a[180:183]\n" \
"ds_read_b64_tr_b16 v[88:89], v33 offset:0\n" \
"v_accvgpr_read_b32 v212, a100\n" \
"v_mfma_f32_16x16x32_bf16 a[176:179], v[100:103], v[72:75], a[176:179]\n" \
"v_accvgpr_read_b32 v213, a101\n" \
"v_accvgpr_read_b32 v214, a102\n" \
"v_mfma_f32_16x16x32_bf16 a[176:179], v[164:167], v[136:139], a[176:179]\n" \
"ds_read_b128 v[72:75], v28 offset:10240\n" \
"v_accvgpr_read_b32 v215, a103\n" \
"v_mfma_f32_16x16x32_bf16 a[72:75], v[100:103], v[76:79], a[72:75]\n" \
"ds_read_b128 v[136:139], v29 offset:10240\n" \
"v_accvgpr_read_b32 v204, a68\n" \
"v_mfma_f32_16x16x32_bf16 a[72:75], v[164:167], v[140:143], a[72:75]\n" \
"ds_read_b64_tr_b16 v[90:91], v33 offset:512\n" \
"v_accvgpr_read_b32 v205, a69\n" \
"v_mfma_f32_16x16x32_bf16 a[76:79], v[104:107], v[76:79], a[76:79]\n" \
"v_accvgpr_read_b32 v206, a70\n" \
"v_accvgpr_read_b32 v207, a71\n" \
"v_mfma_f32_16x16x32_bf16 a[76:79], v[168:171], v[140:143], a[76:79]\n" \
"ds_read_b64_tr_b16 v[152:153], v33 offset:4096\n" \
"v_accvgpr_read_b32 v200, a64\n" \
"v_mfma_f32_16x16x32_bf16 a[104:107], v[108:111], v[76:79], a[104:107]\n" \
"v_accvgpr_read_b32 v201, a65\n" \
"v_accvgpr_read_b32 v202, a66\n" \
"v_mfma_f32_16x16x32_bf16 a[104:107], v[172:175], v[140:143], a[104:107]\n" \
"v_accvgpr_read_b32 v203, a67\n" \
"v_accvgpr_read_b32 v216, a128\n" \
"v_mfma_f32_16x16x32_bf16 a[108:111], v[112:115], v[76:79], a[108:111]\n" \
"ds_read_b64_tr_b16 v[154:155], v33 offset:4608\n" \
"v_accvgpr_read_b32 v217, a129\n" \
"v_mfma_f32_16x16x32_bf16 a[108:111], v[176:179], v[140:143], a[108:111]\n" \
"ds_read_b128 v[76:79], v28 offset:12288\n" \
"v_accvgpr_read_b32 v218, a130\n" \
"v_mfma_f32_16x16x32_bf16 a[172:175], v[112:115], v[80:83], a[172:175]\n" \
"ds_read_b128 v[140:143], v29 offset:12288\n" \
"v_accvgpr_read_b32 v219, a131\n" \
"v_mfma_f32_16x16x32_bf16 a[172:175], v[176:179], v[144:147], a[172:175]\n" \
"v_accvgpr_read_b32 v220, a132\n" \
"v_accvgpr_read_b32 v221, a133\n" \
"v_mfma_f32_16x16x32_bf16 a[168:171], v[108:111], v[80:83], a[168:171]\n" \
"ds_read_b64_tr_b16 v[96:97], v35 offset:0\n" \
"v_accvgpr_read_b32 v222, a134\n" \
"v_mfma_f32_16x16x32_bf16 a[168:171], v[172:175], v[144:147], a[168:171]\n" \
"v_accvgpr_read_b32 v223, a135\n" \
"v_accvgpr_read_b32 v228, a164\n" \
"v_mfma_f32_16x16x32_bf16 a[140:143], v[104:107], v[80:83], a[140:143]\n" \
"v_accvgpr_read_b32 v229, a165\n" \
"v_accvgpr_read_b32 v230, a166\n" \
"v_mfma_f32_16x16x32_bf16 a[140:143], v[168:171], v[144:147], a[140:143]\n" \
"ds_read_b64_tr_b16 v[98:99], v35 offset:512\n" \
"v_accvgpr_read_b32 v231, a167\n" \
"v_mfma_f32_16x16x32_bf16 a[136:139], v[100:103], v[80:83], a[136:139]\n" \
"v_accvgpr_read_b32 v224, a160\n" \
"v_accvgpr_read_b32 v225, a161\n" \
"v_mfma_f32_16x16x32_bf16 a[136:139], v[164:167], v[144:147], a[136:139]\n" \
"ds_read_b128 v[80:83], v28 offset:14336\n" \
"v_accvgpr_read_b32 v226, a162\n" \
"v_mfma_f32_16x16x32_bf16 a[80:83], v[100:103], v[60:63], a[80:83]\n" \
"ds_read_b128 v[144:147], v29 offset:14336\n" \
"v_accvgpr_read_b32 v227, a163\n" \
"v_mfma_f32_16x16x32_bf16 a[80:83], v[164:167], v[124:127], a[80:83]\n" \
"ds_read_b64_tr_b16 v[160:161], v35 offset:4096\n" \
"v_accvgpr_read_b32 v240, a224\n" \
"v_mfma_f32_16x16x32_bf16 a[84:87], v[104:107], v[60:63], a[84:87]\n" \
"v_accvgpr_read_b32 v241, a225\n" \
"v_accvgpr_read_b32 v242, a226\n" \
"v_mfma_f32_16x16x32_bf16 a[84:87], v[168:171], v[124:127], a[84:87]\n" \
"ds_read_b64_tr_b16 v[162:163], v35 offset:4608\n" \
"v_accvgpr_read_b32 v243, a227\n" \
"v_mfma_f32_16x16x32_bf16 a[88:91], v[108:111], v[60:63], a[88:91]\n" \
"v_accvgpr_read_b32 v244, a228\n" \
"v_accvgpr_read_b32 v245, a229\n" \
"v_mfma_f32_16x16x32_bf16 a[88:91], v[172:175], v[124:127], a[88:91]\n" \
"v_accvgpr_read_b32 v246, a230\n" \
"v_accvgpr_read_b32 v247, a231\n" \
"v_mfma_f32_16x16x32_bf16 a[92:95], v[112:115], v[60:63], a[92:95]\n" \
"v_accvgpr_read_b32 v236, a196\n" \
"v_accvgpr_read_b32 v237, a197\n" \
"v_mfma_f32_16x16x32_bf16 a[92:95], v[176:179], v[124:127], a[92:95]\n" \
"ds_read_b128 v[60:63], v28 offset:4096\n" \
"v_accvgpr_read_b32 v238, a198\n" \
"v_mfma_f32_16x16x32_bf16 a[124:127], v[112:115], v[64:67], a[124:127]\n" \
"ds_read_b128 v[124:127], v29 offset:4096\n" \
"v_accvgpr_read_b32 v239, a199\n" \
"v_mfma_f32_16x16x32_bf16 a[124:127], v[176:179], v[128:131], a[124:127]\n" \
"ds_read_b64_tr_b16 v[92:93], v34 offset:0\n" \
"v_accvgpr_read_b32 v232, a192\n" \
"v_mfma_f32_16x16x32_bf16 a[120:123], v[108:111], v[64:67], a[120:123]\n" \
"ds_read_b64_tr_b16 v[94:95], v34 offset:512\n" \
"v_accvgpr_read_b32 v233, a193\n" \
"v_mfma_f32_16x16x32_bf16 a[120:123], v[172:175], v[128:131], a[120:123]\n" \
"v_accvgpr_read_b32 v234, a194\n" \
"v_accvgpr_read_b32 v235, a195\n" \
"v_mfma_f32_16x16x32_bf16 a[116:119], v[104:107], v[64:67], a[116:119]\n" \
"v_accvgpr_read_b32 v180, a8\n" \
"v_accvgpr_read_b32 v181, a9\n" \
"v_mfma_f32_16x16x32_bf16 a[116:119], v[168:171], v[128:131], a[116:119]\n" \
"ds_read_b64_tr_b16 v[156:157], v34 offset:4096\n" \
"v_accvgpr_read_b32 v182, a10\n" \
"v_mfma_f32_16x16x32_bf16 a[112:115], v[100:103], v[64:67], a[112:115]\n" \
"v_accvgpr_read_b32 v183, a11\n" \
"v_accvgpr_read_b32 v48, a40\n" \
"v_mfma_f32_16x16x32_bf16 a[112:115], v[164:167], v[128:131], a[112:115]\n" \
"ds_read_b128 v[64:67], v28 offset:6144\n" \
"v_accvgpr_read_b32 v49, a41\n" \
"v_mfma_f32_16x16x32_bf16 a[16:19], v[100:103], v[52:55], a[16:19]\n" \
"ds_read_b128 v[128:131], v29 offset:6144\n" \
"v_accvgpr_read_b32 v50, a42\n" \
"v_mfma_f32_16x16x32_bf16 a[16:19], v[164:167], v[116:119], a[16:19]\n" \
"ds_read_b64_tr_b16 v[158:159], v34 offset:4608\n" \
"v_accvgpr_read_b32 v51, a43\n" \
"v_mfma_f32_16x16x32_bf16 a[20:23], v[104:107], v[52:55], a[20:23]\n" \
"v_accvgpr_read_b32 v252, a12\n" \
"v_accvgpr_read_b32 v253, a13\n" \
"v_mfma_f32_16x16x32_bf16 a[20:23], v[168:171], v[116:119], a[20:23]\n" \
"v_accvgpr_read_b32 v254, a14\n" \
"v_accvgpr_read_b32 v255, a15\n" \
"v_mfma_f32_16x16x32_bf16 a[24:27], v[108:111], v[52:55], a[24:27]\n" \
"v_lshrrev_b32_e64 v24, 9, v20\n" \
"v_and_b32_e32 v26, 0x1ff, v20\n" \
"v_mfma_f32_16x16x32_bf16 a[24:27], v[172:175], v[116:119], a[24:27]\n" \
"v_lshl_add_u32 v24, v24, 3, v26\n" \
"v_lshrrev_b32_e64 v25, 8, v20\n" \
"v_mfma_f32_16x16x32_bf16 a[28:31], v[112:115], v[52:55], a[28:31]\n" \
"v_mul_u32_u24_e32 v25, 0x24700, v25\n" \
"v_add_u32_e64 v25, v25, v20\n" \
"v_mfma_f32_16x16x32_bf16 a[28:31], v[176:179], v[116:119], a[28:31]\n" \
"ds_read_b128 v[52:55], v28 offset:0\n" \
"v_lshrrev_b32_e64 v26, 2, v20\n" \
"v_mfma_f32_16x16x32_bf16 a[60:63], v[112:115], v[56:59], a[60:63]\n" \
"ds_read_b128 v[116:119], v29 offset:0\n" \
"v_add_u32_e64 v26, s45, v26\n" \
"v_mfma_f32_16x16x32_bf16 a[60:63], v[176:179], v[120:123], a[60:63]\n" \
"v_add_u32_e32 v27, s46, v20\n" \
"v_add_u32_e32 v21, s43, v27\n" \
"v_mfma_f32_16x16x32_bf16 a[56:59], v[108:111], v[56:59], a[56:59]\n" \
"v_mfma_f32_16x16x32_bf16 a[56:59], v[172:175], v[120:123], a[56:59]\n" \
"v_mfma_f32_16x16x32_bf16 a[52:55], v[104:107], v[56:59], a[52:55]\n" \
"v_mfma_f32_16x16x32_bf16 a[52:55], v[168:171], v[120:123], a[52:55]\n" \
"v_mfma_f32_16x16x32_bf16 a[48:51], v[100:103], v[56:59], a[48:51]\n" \
"v_mfma_f32_16x16x32_bf16 a[48:51], v[164:167], v[120:123], a[48:51]\n" \
"ds_read_b128 v[56:59], v28 offset:2048\n" \
"ds_read_b128 v[120:123], v29 offset:2048\n" \
"s_waitcnt lgkmcnt(15)\n" \
"s_nop 0\n" \
"v_mfma_f32_16x16x32_bf16 v[184:187], v[84:87], v[68:71], v[184:187]\n" \
"v_bfe_u32 v44, v20, 2, 5\n" \
"ds_read_b64_tr_b16 v[100:101], v36 offset:0\n" \
"v_mfma_f32_16x16x32_bf16 v[184:187], v[148:151], v[132:135], v[184:187]\n" \
"buffer_load_dword v44, v44, s[80:83], s38 offen offset:0 sc1\n" \
"v_mfma_f32_16x16x32_bf16 v[188:191], v[88:91], v[68:71], v[188:191]\n" \
"ds_read_b64_tr_b16 v[102:103], v36 offset:1024\n" \
"v_mfma_f32_16x16x32_bf16 v[188:191], v[152:155], v[132:135], v[188:191]\n" \
"ds_read_b64_tr_b16 v[164:165], v36 offset:8192\n" \
"v_mfma_f32_16x16x32_bf16 v[196:199], v[96:99], v[68:71], v[196:199]\n" \
"ds_read_b64_tr_b16 v[166:167], v36 offset:9216\n" \
"s_waitcnt lgkmcnt(15)\n" \
"v_mfma_f32_16x16x32_bf16 v[196:199], v[160:163], v[132:135], v[196:199]\n" \
"s_waitcnt lgkmcnt(12)\n" \
"v_mfma_f32_16x16x32_bf16 v[192:195], v[68:71], v[92:95], v[192:195]\n" \
"ds_read_b64_tr_b16 v[104:105], v37 offset:0\n" \
"s_waitcnt lgkmcnt(9)\n" \
"s_nop 0\n" \
"v_mfma_f32_16x16x32_bf16 v[192:195], v[132:135], v[156:159], v[192:195]\n" \
"v_mfma_f32_16x16x32_bf16 v[208:211], v[72:75], v[92:95], v[208:211]\n" \
"ds_read_b64_tr_b16 v[106:107], v37 offset:1024\n" \
"v_mfma_f32_16x16x32_bf16 v[208:211], v[136:139], v[156:159], v[208:211]\n" \
"ds_read_b64_tr_b16 v[168:169], v37 offset:8192\n" \
"v_mfma_f32_16x16x32_bf16 v[212:215], v[96:99], v[72:75], v[212:215]\n" \
"v_mfma_f32_16x16x32_bf16 v[212:215], v[160:163], v[136:139], v[212:215]\n" \
"ds_read_b64_tr_b16 v[170:171], v37 offset:9216\n" \
"v_cvt_pk_bf16_f32 v188, v188, v189\n" \
"v_mfma_f32_16x16x32_bf16 v[204:207], v[88:91], v[72:75], v[204:207]\n" \
"v_cvt_pk_bf16_f32 v189, v190, v191\n" \
"v_cvt_pk_bf16_f32 v190, v198, v199\n" \
"v_mfma_f32_16x16x32_bf16 v[204:207], v[152:155], v[136:139], v[204:207]\n" \
"ds_read_b64_tr_b16 v[108:109], v38 offset:0\n" \
"v_mov_b32_e32 v191, 0x3f80\n" \
"v_mfma_f32_16x16x32_bf16 v[200:203], v[84:87], v[72:75], v[200:203]\n" \
"ds_read_b64_tr_b16 v[110:111], v38 offset:1024\n" \
"v_cvt_pk_bf16_f32 v184, v184, v185\n" \
"v_mfma_f32_16x16x32_bf16 v[200:203], v[148:151], v[136:139], v[200:203]\n" \
"v_cvt_pk_bf16_f32 v185, v186, v187\n" \
"v_cvt_pk_bf16_f32 v186, v196, v197\n" \
"v_mfma_f32_16x16x32_bf16 v[216:219], v[84:87], v[76:79], v[216:219]\n" \
"ds_read_b64_tr_b16 v[172:173], v38 offset:8192\n" \
"v_mov_b32_e64 v187, 0\n" \
"v_mfma_f32_16x16x32_bf16 v[216:219], v[148:151], v[140:143], v[216:219]\n" \
"ds_read_b64_tr_b16 v[174:175], v38 offset:9216\n" \
"v_mfma_f32_16x16x32_bf16 v[220:223], v[88:91], v[76:79], v[220:223]\n" \
"v_mfma_f32_16x16x32_bf16 v[220:223], v[152:155], v[140:143], v[220:223]\n" \
"ds_read_b64_tr_b16 v[112:113], v39 offset:0\n" \
"v_cvt_pk_bf16_f32 v204, v204, v205\n" \
"v_mfma_f32_16x16x32_bf16 v[228:231], v[96:99], v[76:79], v[228:231]\n" \
"v_cvt_pk_bf16_f32 v205, v206, v207\n" \
"v_cvt_pk_bf16_f32 v206, v214, v215\n" \
"v_mfma_f32_16x16x32_bf16 v[228:231], v[160:163], v[140:143], v[228:231]\n" \
"ds_read_b64_tr_b16 v[114:115], v39 offset:1024\n" \
"v_mov_b32_e32 v207, 0x3f80\n" \
"v_mfma_f32_16x16x32_bf16 v[224:227], v[76:79], v[92:95], v[224:227]\n" \
"ds_read_b64_tr_b16 v[176:177], v39 offset:8192\n" \
"v_cvt_pk_bf16_f32 v200, v200, v201\n" \
"v_mfma_f32_16x16x32_bf16 v[224:227], v[140:143], v[156:159], v[224:227]\n" \
"v_cvt_pk_bf16_f32 v201, v202, v203\n" \
"v_cvt_pk_bf16_f32 v202, v212, v213\n" \
"v_mfma_f32_16x16x32_bf16 v[240:243], v[80:83], v[92:95], v[240:243]\n" \
"ds_read_b64_tr_b16 v[178:179], v39 offset:9216\n" \
"v_mov_b32_e64 v203, 0\n" \
"v_mfma_f32_16x16x32_bf16 v[240:243], v[144:147], v[156:159], v[240:243]\n" \
"v_mfma_f32_16x16x32_bf16 v[244:247], v[96:99], v[80:83], v[244:247]\n" \
"v_mfma_f32_16x16x32_bf16 v[244:247], v[160:163], v[144:147], v[244:247]\n" \
"v_cvt_pk_bf16_f32 v220, v220, v221\n" \
"v_cvt_pk_bf16_f32 v221, v222, v223\n" \
"v_mfma_f32_16x16x32_bf16 v[236:239], v[88:91], v[80:83], v[236:239]\n" \
"v_cvt_pk_bf16_f32 v222, v230, v231\n" \
"v_mov_b32_e32 v223, 0x3f80\n" \
"v_mfma_f32_16x16x32_bf16 v[236:239], v[152:155], v[144:147], v[236:239]\n" \
"v_cvt_pk_bf16_f32 v216, v216, v217\n" \
"v_cvt_pk_bf16_f32 v217, v218, v219\n" \
"v_mfma_f32_16x16x32_bf16 v[232:235], v[84:87], v[80:83], v[232:235]\n" \
"v_cvt_pk_bf16_f32 v218, v228, v229\n" \
"v_mov_b32_e64 v219, 0\n" \
"v_mfma_f32_16x16x32_bf16 v[232:235], v[148:151], v[144:147], v[232:235]\n" \
"v_mfma_f32_16x16x32_bf16 v[180:183], v[88:91], v[64:67], v[180:183]\n" \
"v_mfma_f32_16x16x32_bf16 v[180:183], v[152:155], v[128:131], v[180:183]\n" \
"v_mfma_f32_16x16x32_bf16 v[48:51], v[96:99], v[64:67], v[48:51]\n" \
"v_mfma_f32_16x16x32_bf16 v[48:51], v[160:163], v[128:131], v[48:51]\n" \
"v_cvt_pk_bf16_f32 v236, v236, v237\n" \
"v_cvt_pk_bf16_f32 v237, v238, v239\n" \
"v_mfma_f32_16x16x32_bf16 v[252:255], v[64:67], v[92:95], v[252:255]\n" \
"v_cvt_pk_bf16_f32 v238, v246, v247\n" \
"v_mov_b32_e32 v239, 0x3f80\n" \
"v_mfma_f32_16x16x32_bf16 v[252:255], v[128:131], v[156:159], v[252:255]\n" \
"v_cvt_pk_bf16_f32 v232, v232, v233\n" \
"v_cvt_pk_bf16_f32 v233, v234, v235\n" \
"s_waitcnt vmcnt(0) lgkmcnt(0)\n" \
"s_barrier\n" \
"v_mov_b32_e32 v45, s62\n" \
"s_mov_b64 exec, 1\n" \
"buffer_store_dword v45, off, s[80:83], s37 offset:32 sc1\n" \
"s_mov_b64 exec, -1\n" \
"v_cmp_ne_u32_e32 vcc, s62, v44\n" \
"s_nop 4\n" \
"s_cbranch_vccnz L_poll161_%=\n" \
"L_pollr161_%=:\n" \
"v_mfma_f32_16x16x32_bf16 a[144:147], v[100:103], v[68:71], a[144:147]\n" \
"s_mov_b32 s73, s50\n" \
"s_sub_u32 m0, s31, 0x10000\n" \
"v_mfma_f32_16x16x32_bf16 a[144:147], v[164:167], v[132:135], a[144:147]\n" \
"buffer_load_dwordx4 v20, s[80:83], s73 offen offset:0 sc1 lds\n" \
"v_mfma_f32_16x16x32_bf16 a[148:151], v[104:107], v[68:71], a[148:151]\n" \
"s_add_u32 s74, s50, 0x4000\n" \
"s_add_u32 s75, s50, 0x8000\n" \
"v_mfma_f32_16x16x32_bf16 a[148:151], v[168:171], v[132:135], a[148:151]\n" \
"s_mov_b32 s70, 0x3e0293ee\n" \
"s_mov_b32 s76, s52\n" \
"v_mfma_f32_16x16x32_bf16 a[200:203], v[108:111], v[68:71], a[200:203]\n" \
"v_accvgpr_read_b32 v32, a144\n" \
"v_accvgpr_read_b32 v33, a145\n" \
"v_mfma_f32_16x16x32_bf16 a[200:203], v[172:175], v[132:135], a[200:203]\n" \
"buffer_load_dwordx4 v20, s[80:83], s73 offen offset:1024 sc1 lds\n" \
"v_mfma_f32_16x16x32_bf16 a[204:207], v[112:115], v[68:71], a[204:207]\n" \
"v_accvgpr_read_b32 v34, a146\n" \
"v_accvgpr_read_b32 v35, a147\n" \
"v_mfma_f32_16x16x32_bf16 a[204:207], v[176:179], v[132:135], a[204:207]\n" \
"v_mul_f32_e32 v32, s70, v32\n" \
"v_mul_f32_e32 v33, s70, v33\n" \
"v_mfma_f32_16x16x32_bf16 a[236:239], v[112:115], v[72:75], a[236:239]\n" \
"v_accvgpr_read_b32 v36, a148\n" \
"v_accvgpr_read_b32 v37, a149\n" \
"v_mfma_f32_16x16x32_bf16 a[236:239], v[176:179], v[136:139], a[236:239]\n" \
"buffer_load_dwordx4 v20, s[80:83], s73 offen offset:2048 sc1 lds\n" \
"v_mfma_f32_16x16x32_bf16 a[232:235], v[108:111], v[72:75], a[232:235]\n" \
"v_accvgpr_read_b32 v38, a150\n" \
"v_accvgpr_read_b32 v39, a151\n" \
"v_mfma_f32_16x16x32_bf16 a[232:235], v[172:175], v[136:139], a[232:235]\n" \
"v_mul_f32_e32 v34, s70, v34\n" \
"v_mul_f32_e32 v35, s70, v35\n" \
"v_mfma_f32_16x16x32_bf16 a[180:183], v[104:107], v[72:75], a[180:183]\n" \
"v_cvt_pk_bf16_f32 v32, v32, v33\n" \
"v_mul_f32_e64 v36, s70, v36\n" \
"v_mfma_f32_16x16x32_bf16 a[180:183], v[168:171], v[136:139], a[180:183]\n" \
"buffer_load_dwordx4 v20, s[80:83], s73 offen offset:3072 sc1 lds\n" \
"v_mfma_f32_16x16x32_bf16 a[176:179], v[100:103], v[72:75], a[176:179]\n" \
"s_sub_u32 m0, s31, s51\n" \
"v_mul_f32_e32 v37, s70, v37\n" \
"v_mfma_f32_16x16x32_bf16 a[176:179], v[164:167], v[136:139], a[176:179]\n" \
"v_mul_f32_e32 v38, s70, v38\n" \
"v_mul_f32_e32 v39, s70, v39\n" \
"v_mfma_f32_16x16x32_bf16 a[72:75], v[100:103], v[76:79], a[72:75]\n" \
"v_cvt_pk_bf16_f32 v33, v34, v35\n" \
"v_cvt_pk_bf16_f32 v34, v36, v37\n" \
"v_mfma_f32_16x16x32_bf16 a[72:75], v[164:167], v[140:143], a[72:75]\n" \
"buffer_load_dwordx4 v20, s[80:83], s74 offen offset:0 sc1 lds\n" \
"v_mfma_f32_16x16x32_bf16 a[76:79], v[104:107], v[76:79], a[76:79]\n" \
"v_cvt_pk_bf16_f32 v35, v38, v39\n" \
"v_accvgpr_read_b32 v36, a204\n" \
"v_mfma_f32_16x16x32_bf16 a[76:79], v[168:171], v[140:143], a[76:79]\n" \
"ds_write_b128 v21, v[32:35] offset:16384\n" \
"v_mfma_f32_16x16x32_bf16 a[104:107], v[108:111], v[76:79], a[104:107]\n" \
"v_accvgpr_read_b32 v32, a200\n" \
"v_accvgpr_read_b32 v33, a201\n" \
"v_mfma_f32_16x16x32_bf16 a[104:107], v[172:175], v[140:143], a[104:107]\n" \
"buffer_load_dwordx4 v20, s[80:83], s74 offen offset:1024 sc1 lds\n" \
"v_mfma_f32_16x16x32_bf16 a[108:111], v[112:115], v[76:79], a[108:111]\n" \
"v_accvgpr_read_b32 v34, a202\n" \
"v_accvgpr_read_b32 v35, a203\n" \
"v_mfma_f32_16x16x32_bf16 a[108:111], v[176:179], v[140:143], a[108:111]\n" \
"v_mul_f32_e32 v32, s70, v32\n" \
"v_mul_f32_e32 v33, s70, v33\n" \
"v_mfma_f32_16x16x32_bf16 a[172:175], v[112:115], v[80:83], a[172:175]\n" \
"v_accvgpr_read_b32 v37, a205\n" \
"v_accvgpr_read_b32 v38, a206\n" \
"v_mfma_f32_16x16x32_bf16 a[172:175], v[176:179], v[144:147], a[172:175]\n" \
"buffer_load_dwordx4 v20, s[80:83], s74 offen offset:2048 sc1 lds\n" \
"v_mfma_f32_16x16x32_bf16 a[168:171], v[108:111], v[80:83], a[168:171]\n" \
"v_accvgpr_read_b32 v39, a207\n" \
"v_mul_f32_e64 v34, s70, v34\n" \
"v_mfma_f32_16x16x32_bf16 a[168:171], v[172:175], v[144:147], a[168:171]\n" \
"v_mul_f32_e64 v35, s70, v35\n" \
"v_cvt_pk_bf16_f32 v32, v32, v33\n" \
"v_mfma_f32_16x16x32_bf16 a[140:143], v[104:107], v[80:83], a[140:143]\n" \
"v_mul_f32_e32 v36, s70, v36\n" \
"v_mul_f32_e32 v37, s70, v37\n" \
"v_mfma_f32_16x16x32_bf16 a[140:143], v[168:171], v[144:147], a[140:143]\n" \
"buffer_load_dwordx4 v20, s[80:83], s74 offen offset:3072 sc1 lds\n" \
"v_mfma_f32_16x16x32_bf16 a[136:139], v[100:103], v[80:83], a[136:139]\n" \
"s_sub_u32 m0, s31, 0x4000\n" \
"v_mul_f32_e64 v38, s70, v38\n" \
"v_mfma_f32_16x16x32_bf16 a[136:139], v[164:167], v[144:147], a[136:139]\n" \
"v_mul_f32_e64 v39, s70, v39\n" \
"v_cvt_pk_bf16_f32 v33, v34, v35\n" \
"v_mfma_f32_16x16x32_bf16 a[80:83], v[100:103], v[60:63], a[80:83]\n" \
"v_cvt_pk_bf16_f32 v34, v36, v37\n" \
"v_cvt_pk_bf16_f32 v35, v38, v39\n" \
"v_mfma_f32_16x16x32_bf16 a[80:83], v[164:167], v[124:127], a[80:83]\n" \
"buffer_load_dwordx4 v20, s[80:83], s75 offen offset:0 sc1 lds\n" \
"v_mfma_f32_16x16x32_bf16 a[84:87], v[104:107], v[60:63], a[84:87]\n" \
"v_cvt_pk_bf16_f32 v180, v180, v181\n" \
"v_cvt_pk_bf16_f32 v181, v182, v183\n" \
"v_mfma_f32_16x16x32_bf16 a[84:87], v[168:171], v[124:127], a[84:87]\n" \
"ds_write_b128 v21, v[32:35] offset:17408\n" \
"v_mfma_f32_16x16x32_bf16 a[88:91], v[108:111], v[60:63], a[88:91]\n" \
"v_cvt_pk_bf16_f32 v234, v244, v245\n" \
"v_mov_b32_e64 v235, 0\n" \
"v_mfma_f32_16x16x32_bf16 a[88:91], v[172:175], v[124:127], a[88:91]\n" \
"buffer_load_dwordx4 v20, s[80:83], s75 offen offset:1024 sc1 lds\n" \
"v_mfma_f32_16x16x32_bf16 a[92:95], v[112:115], v[60:63], a[92:95]\n" \
"v_cvt_pk_bf16_f32 v182, v50, v51\n" \
"v_mov_b32_e32 v183, 0x3f80\n" \
"v_mfma_f32_16x16x32_bf16 a[92:95], v[176:179], v[124:127], a[92:95]\n" \
"v_cvt_pk_bf16_f32 v86, v192, v193\n" \
"v_cvt_pk_bf16_f32 v87, v194, v195\n" \
"v_mfma_f32_16x16x32_bf16 a[124:127], v[112:115], v[64:67], a[124:127]\n" \
"v_cvt_pk_bf16_f32 v88, v208, v209\n" \
"v_cvt_pk_bf16_f32 v89, v210, v211\n" \
"v_mfma_f32_16x16x32_bf16 a[124:127], v[176:179], v[128:131], a[124:127]\n" \
"buffer_load_dwordx4 v20, s[80:83], s75 offen offset:2048 sc1 lds\n" \
"v_mfma_f32_16x16x32_bf16 a[120:123], v[108:111], v[64:67], a[120:123]\n" \
"v_cvt_pk_bf16_f32 v90, v224, v225\n" \
"v_cvt_pk_bf16_f32 v91, v226, v227\n" \
"v_mfma_f32_16x16x32_bf16 a[120:123], v[172:175], v[128:131], a[120:123]\n" \
"v_cvt_pk_bf16_f32 v92, v240, v241\n" \
"v_cvt_pk_bf16_f32 v93, v242, v243\n" \
"v_mfma_f32_16x16x32_bf16 a[116:119], v[104:107], v[64:67], a[116:119]\n" \
"v_accvgpr_read_b32 v132, a176\n" \
"v_accvgpr_read_b32 v133, a177\n" \
"v_mfma_f32_16x16x32_bf16 a[116:119], v[168:171], v[128:131], a[116:119]\n" \
"buffer_load_dwordx4 v20, s[80:83], s75 offen offset:3072 sc1 lds\n" \
"v_mfma_f32_16x16x32_bf16 a[112:115], v[100:103], v[64:67], a[112:115]\n" \
"s_mov_b32 m0, s53\n" \
"s_nop 0\n" \
"v_accvgpr_read_b32 v32, a232\n" \
"v_mfma_f32_16x16x32_bf16 a[112:115], v[164:167], v[128:131], a[112:115]\n" \
"v_accvgpr_read_b32 v33, a233\n" \
"v_accvgpr_read_b32 v134, a178\n" \
"v_mfma_f32_16x16x32_bf16 a[16:19], v[100:103], v[52:55], a[16:19]\n" \
"v_mfma_f32_16x16x32_bf16 v[192:195], v[188:191], v[184:187], 0\n" \
"buffer_load_dwordx4 v20, s[80:83], s76 offen offset:0 sc1 lds\n" \
"v_mfma_f32_16x16x32_bf16 a[16:19], v[164:167], v[116:119], a[16:19]\n" \
"v_mfma_f32_16x16x32_bf16 v[148:151], v[180:183], v[184:187], 0\n" \
"v_mfma_f32_16x16x32_bf16 v[208:211], v[204:207], v[200:203], 0\n" \
"v_mfma_f32_16x16x32_bf16 v[152:155], v[188:191], v[200:203], 0\n" \
"buffer_load_dwordx4 v20, s[80:83], s76 offen offset:1024 sc1 lds\n" \
"v_mfma_f32_16x16x32_bf16 a[20:23], v[104:107], v[52:55], a[20:23]\n" \
"v_mfma_f32_16x16x32_bf16 v[224:227], v[220:223], v[216:219], 0\n" \
"v_mfma_f32_16x16x32_bf16 v[156:159], v[204:207], v[216:219], 0\n" \
"v_mfma_f32_16x16x32_bf16 v[240:243], v[236:239], v[232:235], 0\n" \
"v_mfma_f32_16x16x32_bf16 v[160:163], v[220:223], v[232:235], 0\n" \
"v_accvgpr_read_b32 v135, a179\n" \
"v_mul_f32_e64 v132, s70, v132\n" \
"v_mfma_f32_16x16x32_bf16 a[20:23], v[168:171], v[116:119], a[20:23]\n" \
"v_mul_f32_e64 v133, s70, v133\n" \
"v_accvgpr_read_b32 v34, a234\n" \
"v_mfma_f32_16x16x32_bf16 a[24:27], v[108:111], v[52:55], a[24:27]\n" \
"v_accvgpr_read_b32 v35, a235\n" \
"v_mul_f32_e32 v32, s70, v32\n" \
"v_mul_f32_e32 v33, s70, v33\n" \
"v_accvgpr_read_b32 v136, a180\n" \
"v_accvgpr_read_b32 v137, a181\n" \
"v_accvgpr_read_b32 v138, a182\n" \
"v_accvgpr_read_b32 v139, a183\n" \
"v_mul_f32_e32 v134, s70, v134\n" \
"v_mul_f32_e32 v135, s70, v135\n" \
"v_cvt_pk_bf16_f32 v132, v132, v133\n" \
"v_accvgpr_read_b32 v36, a236\n" \
"v_accvgpr_read_b32 v37, a237\n" \
"v_accvgpr_read_b32 v38, a238\n" \
"v_accvgpr_read_b32 v39, a239\n" \
"v_mul_f32_e32 v34, s70, v34\n" \
"v_mul_f32_e32 v35, s70, v35\n" \
"v_cvt_pk_bf16_f32 v32, v32, v33\n" \
"v_mul_f32_e32 v136, s70, v136\n" \
"v_mul_f32_e32 v137, s70, v137\n" \
"v_mul_f32_e32 v138, s70, v138\n" \
"v_mul_f32_e32 v139, s70, v139\n" \
"v_cvt_pk_bf16_f32 v133, v134, v135\n" \
"v_mul_f32_e32 v36, s70, v36\n" \
"v_mul_f32_e32 v37, s70, v37\n" \
"v_mul_f32_e32 v38, s70, v38\n" \
"v_mul_f32_e32 v39, s70, v39\n" \
"v_cvt_pk_bf16_f32 v33, v34, v35\n" \
"v_cvt_pk_bf16_f32 v134, v136, v137\n" \
"v_cvt_pk_bf16_f32 v135, v138, v139\n" \
"v_cvt_pk_bf16_f32 v34, v36, v37\n" \
"v_cvt_pk_bf16_f32 v35, v38, v39\n" \
"v_mfma_f32_16x16x32_bf16 a[24:27], v[172:175], v[116:119], a[24:27]\n" \
"ds_write_b128 v21, v[132:135] offset:20480\n" \
"ds_write_b128 v21, v[32:35] offset:21504\n" \
"v_accvgpr_read_b32 v132, a72\n" \
"v_accvgpr_read_b32 v133, a73\n" \
"v_accvgpr_read_b32 v32, a104\n" \
"v_accvgpr_read_b32 v33, a105\n" \
"v_accvgpr_read_b32 v140, a136\n" \
"v_accvgpr_read_b32 v141, a137\n" \
"v_accvgpr_read_b32 v134, a74\n" \
"v_accvgpr_read_b32 v135, a75\n" \
"v_mul_f32_e32 v132, s70, v132\n" \
"v_mul_f32_e32 v133, s70, v133\n" \
"v_accvgpr_read_b32 v34, a106\n" \
"v_accvgpr_read_b32 v35, a107\n" \
"v_mul_f32_e32 v32, s70, v32\n" \
"v_mul_f32_e32 v33, s70, v33\n" \
"v_accvgpr_read_b32 v142, a138\n" \
"v_accvgpr_read_b32 v143, a139\n" \
"v_mul_f32_e32 v140, s70, v140\n" \
"v_mul_f32_e32 v141, s70, v141\n" \
"v_mfma_f32_16x16x32_bf16 a[28:31], v[112:115], v[52:55], a[28:31]\n" \
"v_accvgpr_read_b32 v136, a76\n" \
"v_accvgpr_read_b32 v137, a77\n" \
"v_accvgpr_read_b32 v138, a78\n" \
"v_accvgpr_read_b32 v139, a79\n" \
"v_mul_f32_e32 v134, s70, v134\n" \
"v_mul_f32_e32 v135, s70, v135\n" \
"v_cvt_pk_bf16_f32 v132, v132, v133\n" \
"v_accvgpr_read_b32 v36, a108\n" \
"v_accvgpr_read_b32 v37, a109\n" \
"v_accvgpr_read_b32 v38, a110\n" \
"v_accvgpr_read_b32 v39, a111\n" \
"v_mul_f32_e32 v34, s70, v34\n" \
"v_mul_f32_e32 v35, s70, v35\n" \
"v_cvt_pk_bf16_f32 v32, v32, v33\n" \
"v_accvgpr_read_b32 v144, a140\n" \
".fill (L_pb + 85728 - .) / 4, 4, 0xBF800000\n" \
"L_pp21:\n" \
"s_cbranch_execz L_pret\n" \
"v_accvgpr_read_b32 v145, a141\n" \
"v_accvgpr_read_b32 v146, a142\n" \
"v_accvgpr_read_b32 v147, a143\n" \
"v_mul_f32_e32 v142, s70, v142\n" \
"v_mul_f32_e64 v143, s70, v143\n" \
"v_cvt_pk_bf16_f32 v140, v140, v141\n" \
"s_mov_b32 s64, 0xaaaaaaaa\n" \
"s_mov_b32 s65, 0xaaaaaaaa\n" \
"v_mul_f32_e32 v136, s70, v136\n" \
"v_mul_f32_e32 v137, s70, v137\n" \
"v_mul_f32_e32 v138, s70, v138\n" \
"v_mul_f32_e32 v139, s70, v139\n" \
"v_cvt_pk_bf16_f32 v133, v134, v135\n" \
"v_mul_f32_e32 v36, s70, v36\n" \
"v_mul_f32_e32 v37, s70, v37\n" \
"v_mul_f32_e32 v38, s70, v38\n" \
"v_mul_f32_e32 v39, s70, v39\n" \
"v_cvt_pk_bf16_f32 v33, v34, v35\n" \
"v_mul_f32_e32 v144, s70, v144\n" \
"v_mul_f32_e32 v145, s70, v145\n" \
"v_mul_f32_e32 v146, s70, v146\n" \
"v_mul_f32_e32 v147, s70, v147\n" \
"v_cvt_pk_bf16_f32 v141, v142, v143\n" \
"s_mov_b32 s66, 0xfff0ffff\n" \
"s_mov_b32 s67, 0xf000ff00\n" \
"s_mov_b32 s68, 0xffc0fffc\n" \
"s_mov_b32 s69, 0xc000fc00\n" \
"v_cndmask_b32_e64 v192, v192, v193, s[64:65]\n" \
"v_cndmask_b32_e64 v148, v148, v149, s[64:65]\n" \
"v_cndmask_b32_e64 v194, v194, v195, s[64:65]\n" \
"v_cndmask_b32_e64 v150, v150, v151, s[64:65]\n" \
"v_cndmask_b32_e64 v208, v208, v209, s[64:65]\n" \
"v_cndmask_b32_e64 v152, v152, v153, s[64:65]\n" \
"v_cndmask_b32_e64 v210, v210, v211, s[64:65]\n" \
"v_cndmask_b32_e64 v154, v154, v155, s[64:65]\n" \
"v_cndmask_b32_e64 v224, v224, v225, s[64:65]\n" \
"v_cndmask_b32_e64 v156, v156, v157, s[64:65]\n" \
"v_cndmask_b32_e64 v226, v226, v227, s[64:65]\n" \
"v_cndmask_b32_e64 v158, v158, v159, s[64:65]\n" \
"v_cndmask_b32_e64 v240, v240, v241, s[64:65]\n" \
"v_cndmask_b32_e64 v160, v160, v161, s[64:65]\n" \
"v_cndmask_b32_e64 v242, v242, v243, s[64:65]\n" \
"v_cndmask_b32_e64 v162, v162, v163, s[64:65]\n" \
"v_cvt_pk_bf16_f32 v134, v136, v137\n" \
"v_cvt_pk_bf16_f32 v135, v138, v139\n" \
"v_cvt_pk_bf16_f32 v34, v36, v37\n" \
"v_cvt_pk_bf16_f32 v35, v38, v39\n" \
"v_cvt_pk_bf16_f32 v142, v144, v145\n" \
"v_cvt_pk_bf16_f32 v143, v146, v147\n" \
"v_cndmask_b32_e64 v192, v148, v192, s[66:67]\n" \
"v_cndmask_b32_e64 v194, v150, v194, s[68:69]\n" \
"v_cndmask_b32_e64 v208, v152, v208, s[66:67]\n" \
"v_cndmask_b32_e64 v210, v154, v210, s[68:69]\n" \
"v_cndmask_b32_e64 v224, v156, v224, s[66:67]\n" \
"v_cndmask_b32_e64 v226, v158, v226, s[68:69]\n" \
"v_cndmask_b32_e64 v240, v160, v240, s[66:67]\n" \
"v_cndmask_b32_e64 v242, v162, v242, s[68:69]\n" \
"ds_write_b128 v21, v[132:135] offset:24576\n" \
"ds_write_b128 v21, v[32:35] offset:25600\n" \
"ds_write_b128 v21, v[140:143] offset:28672\n" \
"v_mfma_f32_16x16x32_bf16 a[28:31], v[176:179], v[116:119], a[28:31]\n" \
"v_max_f32_e32 v193, v192, v194\n" \
"v_max_f32_e32 v209, v208, v210\n" \
"v_max_f32_e32 v225, v224, v226\n" \
"v_max_f32_e32 v241, v240, v242\n" \
"s_mov_b32 s71, 0x3e0293ee\n" \
"v_permlane32_swap_b32_e32 v193, v225\n" \
"v_permlane32_swap_b32_e32 v209, v241\n" \
"v_accvgpr_read_b32 v132, a168\n" \
"v_accvgpr_read_b32 v133, a169\n" \
"v_accvgpr_read_b32 v32, a80\n" \
"v_accvgpr_read_b32 v33, a81\n" \
"v_accvgpr_read_b32 v140, a88\n" \
"v_accvgpr_read_b32 v141, a89\n" \
"v_accvgpr_read_b32 v124, a112\n" \
"v_accvgpr_read_b32 v125, a113\n" \
"v_permlane32_swap_b32_e32 v196, v228\n" \
"v_permlane32_swap_b32_e32 v212, v244\n" \
"v_max_f32_e32 v193, v193, v225\n" \
"v_max_f32_e32 v209, v209, v241\n" \
"v_accvgpr_read_b32 v134, a170\n" \
"v_accvgpr_read_b32 v135, a171\n" \
"v_mul_f32_e32 v132, s70, v132\n" \
"v_mul_f32_e32 v133, s70, v133\n" \
"v_accvgpr_read_b32 v34, a82\n" \
"v_accvgpr_read_b32 v35, a83\n" \
"v_pk_mul_f32 v[32:33], v[32:33], s[70:71]\n" \
"v_accvgpr_read_b32 v142, a90\n" \
"v_accvgpr_read_b32 v143, a91\n" \
"v_pk_mul_f32 v[140:141], v[140:141], s[70:71]\n" \
"v_accvgpr_read_b32 v126, a114\n" \
"v_accvgpr_read_b32 v127, a115\n" \
"v_pk_mul_f32 v[124:125], v[124:125], s[70:71]\n" \
"v_permlane16_swap_b32_e32 v228, v244\n" \
"v_permlane16_swap_b32_e32 v193, v209\n" \
"v_accvgpr_read_b32 v136, a172\n" \
"v_accvgpr_read_b32 v137, a173\n" \
"v_accvgpr_read_b32 v138, a174\n" \
"v_accvgpr_read_b32 v139, a175\n" \
"v_mul_f32_e32 v134, s70, v134\n" \
"v_mul_f32_e32 v135, s70, v135\n" \
"v_cvt_pk_bf16_f32 v132, v132, v133\n" \
"v_accvgpr_read_b32 v36, a84\n" \
"v_accvgpr_read_b32 v37, a85\n" \
"v_accvgpr_read_b32 v38, a86\n" \
"v_accvgpr_read_b32 v39, a87\n" \
"v_pk_mul_f32 v[34:35], v[34:35], s[70:71]\n" \
"v_cvt_pk_bf16_f32 v32, v32, v33\n" \
"v_accvgpr_read_b32 v144, a92\n" \
"v_accvgpr_read_b32 v145, a93\n" \
"v_accvgpr_read_b32 v146, a94\n" \
"v_accvgpr_read_b32 v147, a95\n" \
"v_pk_mul_f32 v[142:143], v[142:143], s[70:71]\n" \
"v_cvt_pk_bf16_f32 v140, v140, v141\n" \
"v_accvgpr_read_b32 v128, a116\n" \
"v_accvgpr_read_b32 v129, a117\n" \
"v_accvgpr_read_b32 v130, a118\n" \
"v_accvgpr_read_b32 v131, a119\n" \
"v_pk_mul_f32 v[126:127], v[126:127], s[70:71]\n" \
"v_cvt_pk_bf16_f32 v124, v124, v125\n" \
"v_mfma_f32_16x16x32_bf16 a[60:63], v[112:115], v[56:59], a[60:63]\n" \
"s_mov_b32 s72, 0x3eae1a93\n" \
"v_mul_f32_e32 v45, 0xbfb8aa3b, v228\n" \
"v_mfma_f32_16x16x16_bf16 v[48:51], v[182:183], v[46:47], 0\n" \
"v_mfma_f32_16x16x16_bf16 v[196:199], v[190:191], v[46:47], 0\n" \
"v_mfma_f32_16x16x16_bf16 v[212:215], v[206:207], v[46:47], 0\n" \
"v_max_f32_e32 v193, v193, v209\n" \
"v_mul_f32_e32 v136, s70, v136\n" \
"v_mul_f32_e32 v137, s70, v137\n" \
"v_mul_f32_e32 v138, s70, v138\n" \
"v_mul_f32_e64 v139, s70, v139\n" \
"v_cvt_pk_bf16_f32 v133, v134, v135\n" \
"v_pk_mul_f32 v[36:37], v[36:37], s[70:71]\n" \
"v_pk_mul_f32 v[38:39], v[38:39], s[70:71]\n" \
"v_cvt_pk_bf16_f32 v33, v34, v35\n" \
"v_pk_mul_f32 v[144:145], v[144:145], s[70:71]\n" \
"v_pk_mul_f32 v[146:147], v[146:147], s[70:71]\n" \
"v_cvt_pk_bf16_f32 v141, v142, v143\n" \
"v_pk_mul_f32 v[128:129], v[128:129], s[70:71]\n" \
"v_pk_mul_f32 v[130:131], v[130:131], s[70:71]\n" \
"v_cvt_pk_bf16_f32 v125, v126, v127\n" \
"v_mfma_f32_16x16x16_bf16 v[228:231], v[222:223], v[46:47], 0\n" \
"v_mul_f32_e64 v193, s72, v193\n" \
"v_cvt_pk_bf16_f32 v134, v136, v137\n" \
"v_cvt_pk_bf16_f32 v135, v138, v139\n" \
"v_cvt_pk_bf16_f32 v34, v36, v37\n" \
"v_cvt_pk_bf16_f32 v35, v38, v39\n" \
"v_cvt_pk_bf16_f32 v142, v144, v145\n" \
"v_cvt_pk_bf16_f32 v143, v146, v147\n" \
"v_cvt_pk_bf16_f32 v126, v128, v129\n" \
"v_cvt_pk_bf16_f32 v127, v130, v131\n" \
"v_mfma_f32_16x16x16_bf16 v[244:247], v[238:239], v[46:47], 0\n" \
"v_mov_b32_e64 v209, v193\n" \
"v_accvgpr_read_b32 v60, a120\n" \
"ds_write_b128 v21, v[132:135] offset:29696\n" \
"ds_write_b128 v27, v[32:35] offset:8192\n" \
"ds_write_b128 v27, v[140:143] offset:9216\n" \
"ds_write_b128 v27, v[124:127] offset:12288\n" \
"v_permlane16_swap_b32_e32 v193, v209\n" \
"v_mfma_f32_16x16x32_bf16 a[60:63], v[176:179], v[120:123], a[60:63]\n" \
"v_mov_b32_e32 v225, v193\n" \
"v_mov_b32_e64 v241, v209\n" \
"v_accvgpr_read_b32 v61, a121\n" \
"v_accvgpr_read_b32 v132, a16\n" \
"v_accvgpr_read_b32 v133, a17\n" \
"v_accvgpr_read_b32 v32, a24\n" \
"v_accvgpr_read_b32 v33, a25\n" \
"v_permlane32_swap_b32_e32 v193, v225\n" \
"v_permlane32_swap_b32_e32 v209, v241\n" \
"v_accvgpr_read_b32 v62, a122\n" \
"v_accvgpr_read_b32 v63, a123\n" \
"v_pk_mul_f32 v[60:61], v[60:61], s[70:71]\n" \
"v_accvgpr_read_b32 v134, a18\n" \
"v_accvgpr_read_b32 v135, a19\n" \
"v_pk_mul_f32 v[132:133], v[132:133], s[70:71]\n" \
"v_accvgpr_read_b32 v34, a26\n" \
"v_accvgpr_read_b32 v35, a27\n" \
"v_pk_mul_f32 v[32:33], v[32:33], s[70:71]\n" \
"v_fma_f32 v192, v192, s72, -v193\n" \
"v_fma_f32 v194, v194, s72, -v193\n" \
"v_fma_f32 v208, v208, s72, -v209\n" \
"v_fma_f32 v210, v210, s72, -v209\n" \
"v_accvgpr_read_b32 v64, a124\n" \
"v_accvgpr_read_b32 v65, a125\n" \
"v_accvgpr_read_b32 v66, a126\n" \
"v_accvgpr_read_b32 v67, a127\n" \
"v_pk_mul_f32 v[62:63], v[62:63], s[70:71]\n" \
"v_cvt_pk_bf16_f32 v60, v60, v61\n" \
"v_accvgpr_read_b32 v136, a20\n" \
"v_accvgpr_read_b32 v137, a21\n" \
"v_accvgpr_read_b32 v138, a22\n" \
"v_accvgpr_read_b32 v139, a23\n" \
"v_pk_mul_f32 v[134:135], v[134:135], s[70:71]\n" \
"v_cvt_pk_bf16_f32 v132, v132, v133\n" \
"v_accvgpr_read_b32 v36, a28\n" \
"v_accvgpr_read_b32 v37, a29\n" \
"v_accvgpr_read_b32 v38, a30\n" \
"v_accvgpr_read_b32 v39, a31\n" \
"v_pk_mul_f32 v[34:35], v[34:35], s[70:71]\n" \
"v_cvt_pk_bf16_f32 v32, v32, v33\n" \
"v_accvgpr_read_b32 v128, a60\n" \
"v_accvgpr_read_b32 v129, a61\n" \
"v_accvgpr_read_b32 v130, a62\n" \
"v_accvgpr_read_b32 v131, a63\n" \
"v_exp_f32_e32 v192, v192\n" \
"v_exp_f32_e32 v194, v194\n" \
"v_exp_f32_e32 v208, v208\n" \
"v_exp_f32_e32 v210, v210\n" \
"v_fma_f32 v224, v224, s72, -v225\n" \
"v_fma_f32 v226, v226, s72, -v225\n" \
"v_pk_mul_f32 v[64:65], v[64:65], s[70:71]\n" \
"v_pk_mul_f32 v[66:67], v[66:67], s[70:71]\n" \
"v_cvt_pk_bf16_f32 v61, v62, v63\n" \
"v_pk_mul_f32 v[136:137], v[136:137], s[70:71]\n" \
"v_pk_mul_f32 v[138:139], v[138:139], s[70:71]\n" \
"v_cvt_pk_bf16_f32 v133, v134, v135\n" \
"v_pk_mul_f32 v[36:37], v[36:37], s[70:71]\n" \
"v_pk_mul_f32 v[38:39], v[38:39], s[70:71]\n" \
"v_cvt_pk_bf16_f32 v33, v34, v35\n" \
"v_pk_mul_f32 v[128:129], v[128:129], s[70:71]\n" \
"v_pk_mul_f32 v[130:131], v[130:131], s[70:71]\n" \
"v_mfma_f32_16x16x32_bf16 a[56:59], v[108:111], v[56:59], a[56:59]\n" \
"v_exp_f32_e32 v224, v224\n" \
"v_exp_f32_e32 v226, v226\n" \
"v_fma_f32 v240, v240, s72, -v241\n" \
"v_fma_f32 v242, v242, s72, -v241\n" \
"v_cvt_pk_bf16_f32 v62, v64, v65\n" \
"v_cvt_pk_bf16_f32 v63, v66, v67\n" \
"v_cvt_pk_bf16_f32 v134, v136, v137\n" \
"v_cvt_pk_bf16_f32 v135, v138, v139\n" \
"v_cvt_pk_bf16_f32 v34, v36, v37\n" \
"v_cvt_pk_bf16_f32 v35, v38, v39\n" \
"v_cvt_pk_bf16_f32 v192, v192, v194\n" \
"v_cvt_pk_bf16_f32 v208, v208, v210\n" \
"v_exp_f32_e32 v240, v240\n" \
"v_exp_f32_e32 v242, v242\n" \
"ds_write_b128 v27, v[60:63] offset:13312\n" \
"ds_write_b128 v27, v[132:135] offset:0\n" \
"ds_write_b128 v27, v[32:35] offset:1024\n" \
"v_cvt_pk_bf16_f32 v68, v48, v49\n" \
"v_cvt_pk_bf16_f32 v69, v50, v51\n" \
"v_cvt_pk_bf16_f32 v70, v196, v197\n" \
"v_cvt_pk_bf16_f32 v71, v198, v199\n" \
"v_cvt_pk_bf16_f32 v72, v212, v213\n" \
"v_cvt_pk_bf16_f32 v73, v214, v215\n" \
"v_perm_b32 v184, v192, v192, v40\n" \
"v_perm_b32 v185, v192, v192, v41\n" \
"v_perm_b32 v186, v192, v192, v42\n" \
"v_perm_b32 v187, v192, v192, v43\n" \
"v_perm_b32 v200, v208, v208, v40\n" \
"v_perm_b32 v201, v208, v208, v41\n" \
"v_perm_b32 v202, v208, v208, v42\n" \
"v_perm_b32 v203, v208, v208, v43\n" \
"v_cvt_pk_bf16_f32 v224, v224, v226\n" \
"v_cvt_pk_bf16_f32 v74, v228, v229\n" \
"v_cvt_pk_bf16_f32 v75, v230, v231\n" \
"v_mfma_f32_16x16x32_bf16 v[148:151], v[68:71], v[184:187], 0\n" \
"v_mfma_f32_16x16x32_bf16 v[152:155], v[70:73], v[200:203], 0\n" \
"v_perm_b32 v216, v224, v224, v40\n" \
"v_perm_b32 v217, v224, v224, v41\n" \
"v_perm_b32 v218, v224, v224, v42\n" \
"v_perm_b32 v219, v224, v224, v43\n" \
"v_cvt_pk_bf16_f32 v240, v240, v242\n" \
"v_mfma_f32_16x16x32_bf16 a[56:59], v[172:175], v[120:123], a[56:59]\n" \
"v_cvt_pk_bf16_f32 v76, v244, v245\n" \
"v_cvt_pk_bf16_f32 v77, v246, v247\n" \
"v_mfma_f32_16x16x32_bf16 v[156:159], v[72:75], v[216:219], 0\n" \
"v_perm_b32 v232, v240, v240, v40\n" \
"v_perm_b32 v233, v240, v240, v41\n" \
"v_perm_b32 v234, v240, v240, v42\n" \
"v_perm_b32 v235, v240, v240, v43\n" \
"v_mfma_f32_16x16x32_bf16 a[52:55], v[104:107], v[56:59], a[52:55]\n" \
"v_accvgpr_read_b32 v124, a56\n" \
"v_accvgpr_read_b32 v125, a57\n" \
"v_accvgpr_read_b32 v126, a58\n" \
"v_accvgpr_read_b32 v127, a59\n" \
"v_pk_mul_f32 v[124:125], v[124:125], s[70:71]\n" \
"v_pk_mul_f32 v[126:127], v[126:127], s[70:71]\n" \
"v_cvt_pk_bf16_f32 v124, v124, v125\n" \
"v_cvt_pk_bf16_f32 v125, v126, v127\n" \
"v_cvt_pk_bf16_f32 v126, v128, v129\n" \
"v_cvt_pk_bf16_f32 v127, v130, v131\n" \
"ds_write_b128 v27, v[124:127] offset:5120\n" \
"v_mfma_f32_16x16x32_bf16 v[160:163], v[74:77], v[232:235], 0\n" \
"v_cvt_pk_bf16_f32 v84, v252, v253\n" \
"v_cvt_pk_bf16_f32 v85, v254, v255\n" \
"v_mfma_f32_16x16x32_bf16 a[52:55], v[168:171], v[120:123], a[52:55]\n" \
"v_exp_f32_e64 v45, v45\n" \
"v_mfma_f32_16x16x32_bf16 v[192:195], v[84:87], v[184:187], 0\n" \
"v_mfma_f32_16x16x32_bf16 v[208:211], v[86:89], v[200:203], 0\n" \
"v_mfma_f32_16x16x32_bf16 v[224:227], v[88:91], v[216:219], 0\n" \
"v_mfma_f32_16x16x32_bf16 v[240:243], v[90:93], v[232:235], 0\n" \
"v_mov_b32_dpp v150, v154 quad_perm:[0,1,2,3] row_mask:0x2 bank_mask:0xf\n" \
"v_add_f32_e64 v45, 1.0, v45\n" \
"v_mfma_f32_16x16x32_bf16 a[48:51], v[100:103], v[56:59], a[48:51]\n" \
"v_accvgpr_read_b32 v144, a52\n" \
"v_accvgpr_read_b32 v145, a53\n" \
"v_accvgpr_read_b32 v146, a54\n" \
"v_accvgpr_read_b32 v147, a55\n" \
"v_pk_mul_f32 v[144:145], v[144:145], s[70:71]\n" \
"v_pk_mul_f32 v[146:147], v[146:147], s[70:71]\n" \
"v_mov_b32_dpp v150, v158 quad_perm:[0,1,2,3] row_mask:0x4 bank_mask:0xf\n" \
"v_rcp_f32_e64 v45, v45\n" \
"v_mov_b32_dpp v148, v152 quad_perm:[0,1,2,3] row_mask:0x2 bank_mask:0xf\n" \
"v_mov_b32_dpp v150, v162 quad_perm:[0,1,2,3] row_mask:0x8 bank_mask:0xf\n" \
"v_mov_b32_dpp v149, v153 quad_perm:[0,1,2,3] row_mask:0x2 bank_mask:0xf\n" \
"v_rcp_f32_e64 v150, v150\n" \
"v_mov_b32_dpp v148, v156 quad_perm:[0,1,2,3] row_mask:0x4 bank_mask:0xf\n" \
"v_mov_b32_dpp v149, v157 quad_perm:[0,1,2,3] row_mask:0x4 bank_mask:0xf\n" \
"v_mul_f32_e64 v150, v45, v150\n" \
"v_mov_b32_dpp v148, v160 quad_perm:[0,1,2,3] row_mask:0x8 bank_mask:0xf\n" \
"v_mov_b32_dpp v149, v161 quad_perm:[0,1,2,3] row_mask:0x8 bank_mask:0xf\n" \
"v_mul_f32_e32 v148, v150, v148\n" \
"v_mul_f32_e32 v149, v150, v149\n" \
"v_mov_b32_e64 v154, v150\n" \
"v_mfma_f32_16x16x32_bf16 a[48:51], v[164:167], v[120:123], a[48:51]\n" \
"v_sub_f32_e32 v44, 1.0, v45\n" \
"v_permlane16_swap_b32_e32 v150, v154\n" \
"v_mov_b32_e32 v158, v150\n" \
"v_mov_b32_e32 v162, v154\n" \
"v_cvt_pk_bf16_f32 v148, v148, v149\n" \
"v_permlane32_swap_b32_e32 v150, v158\n" \
"v_permlane32_swap_b32_e32 v154, v162\n" \
"v_mul_f32_e64 v192, v150, v192\n" \
"v_accvgpr_read_b32 v140, a48\n" \
"v_accvgpr_read_b32 v141, a49\n" \
"v_accvgpr_read_b32 v142, a50\n" \
"v_accvgpr_read_b32 v143, a51\n" \
"v_pk_mul_f32 v[140:141], v[140:141], s[70:71]\n" \
"v_pk_mul_f32 v[142:143], v[142:143], s[70:71]\n" \
"v_cvt_pk_bf16_f32 v140, v140, v141\n" \
"v_cvt_pk_bf16_f32 v141, v142, v143\n" \
"v_cvt_pk_bf16_f32 v142, v144, v145\n" \
"v_cvt_pk_bf16_f32 v143, v146, v147\n" \
"ds_write_b128 v27, v[140:143] offset:4096\n" \
"v_mul_f32_e32 v193, v150, v193\n" \
"v_mul_f32_e32 v208, v154, v208\n" \
"v_mul_f32_e32 v209, v154, v209\n" \
"v_mul_f32_e32 v224, v158, v224\n" \
"v_mul_f32_e32 v225, v158, v225\n" \
"v_mul_f32_e32 v240, v162, v240\n" \
"v_mul_f32_e32 v241, v162, v241\n" \
"v_mul_f32_e32 v194, v150, v194\n" \
"v_mul_f32_e64 v195, v150, v195\n" \
"v_cvt_pk_bf16_f32 v192, v192, v193\n" \
"v_mul_f32_e32 v210, v154, v210\n" \
"v_mul_f32_e32 v211, v154, v211\n" \
"v_cvt_pk_bf16_f32 v208, v208, v209\n" \
"v_mul_f32_e32 v226, v158, v226\n" \
"v_mul_f32_e32 v227, v158, v227\n" \
"v_cvt_pk_bf16_f32 v224, v224, v225\n" \
"v_mul_f32_e32 v242, v162, v242\n" \
"v_mul_f32_e32 v243, v162, v243\n" \
"v_cvt_pk_bf16_f32 v240, v240, v241\n" \
"v_cvt_pk_bf16_f32 v193, v194, v195\n" \
"s_add_u32 s84, s48, 0x0\n" \
"s_nop 0\n" \
"v_cvt_pk_bf16_f32 v209, v210, v211\n" \
"s_add_u32 s85, s48, 0x24800\n" \
"v_cvt_pk_bf16_f32 v225, v226, v227\n" \
"s_add_u32 s86, s48, 0x49000\n" \
"v_cvt_pk_bf16_f32 v241, v242, v243\n" \
"s_add_u32 s87, s48, 0x6d800\n" \
"ds_write_b32 v26, v44\n" \
"buffer_store_dword v148, v25, s[80:83], s49 offen sc1\n" \
"buffer_store_dwordx2 v[192:193], v24, s[80:83], s84 offen sc1\n" \
"buffer_store_dwordx2 v[208:209], v24, s[80:83], s85 offen sc1\n" \
"buffer_store_dwordx2 v[224:225], v24, s[80:83], s86 offen sc1\n" \
"buffer_store_dwordx2 v[240:241], v24, s[80:83], s87 offen sc1\n" \
"s_branch L_end_%=\n" \
"L_poll61_%=:\n" \
"s_mov_b32 s84, 0\n" \
"L_pl61_%=:\n" \
"s_sleep 1\n" \
"v_bfe_u32 v44, v20, 2, 5\n" \
"buffer_load_dword v44, v44, s[80:83], s38 offen offset:128 sc1\n" \
"s_waitcnt vmcnt(0)\n" \
"v_cmp_ne_u32_e32 vcc, s62, v44\n" \
"s_add_u32 s84, s84, 1\n" \
"s_nop 3\n" \
"s_cbranch_vccz L_pollr61_%=\n" \
"s_cmp_gt_u32 s84, 0x400000\n" \
"s_cbranch_scc0 L_pl61_%=\n" \
"s_branch L_pollr61_%=\n" \
"L_poll161_%=:\n" \
"s_mov_b32 s84, 0\n" \
"L_pl161_%=:\n" \
"s_sleep 1\n" \
"v_bfe_u32 v44, v20, 2, 5\n" \
"buffer_load_dword v44, v44, s[80:83], s38 offen offset:0 sc1\n" \
"s_waitcnt vmcnt(0)\n" \
"v_cmp_ne_u32_e32 vcc, s62, v44\n" \
"s_add_u32 s84, s84, 1\n" \
"s_nop 3\n" \
"s_cbranch_vccz L_pollr161_%=\n" \
"s_cmp_gt_u32 s84, 0x400000\n" \
"s_cbranch_scc0 L_pl161_%=\n" \
"s_branch L_pollr161_%=\n" \
"L_end_%=:\n"
#define PROJ_VCLOB "v21","v24","v25","v28","v29","v32","v33","v34","v35","v36","v37","v38","v39","v40","v41","v42","v43","v44","v45","v46","v47","v48","v49","v50","v51","v52","v53","v54","v55","v56","v57","v58","v59","v60","v61","v62","v63","v64","v65","v66","v67","v68","v69","v70","v71","v72","v73","v74","v75","v76","v77","v78","v79","v80","v81","v82","v83","v84","v85","v86","v87","v88","v89","v90","v91","v92","v93","v94","v95","v96","v97","v98","v99","v100","v101","v102","v103","v104","v105","v106","v107","v108","v109","v110","v111","v112","v113","v114","v115","v116","v117","v118","v119","v120","v121","v122","v123","v124","v125","v126","v127","v128","v129","v130","v131","v132","v133","v134","v135","v136","v137","v138","v139","v140","v141","v142","v143","v144","v145","v146","v147","v148","v149","v150","v151","v152","v153","v154","v155","v156","v157","v158","v159","v160","v161","v162","v163","v164","v165","v166","v167","v168","v169","v170","v171","v172","v173","v174","v175","v176","v177","v178","v179","v180","v181","v182","v183","v184","v185","v186","v187","v188","v189","v190","v192","v193","v194","v195","v196","v197","v198","v199","v200","v201","v202","v203","v204","v205","v206","v207","v208","v209","v210","v211","v212","v213","v214","v215","v216","v217","v218","v219","v220","v221","v222","v223","v224","v225","v226","v227","v228","v229","v230","v231","v232","v233","v234","v235","v236","v237","v238","v239","v240","v241","v242","v243","v244","v245","v246","v247","v248","v249","v250","v251","v252","v253","v254","v255"
#define PROJ_SCLOB "s4","s5","s6","s7","s32","s33","s34","s35","s36","s37","s38","s39","s40","s41","s42","s43","s44","s45","s46","s47","s48","s49","s50","s51","s52","s53","s54","s55","s63","s64","s65","s66","s67","s68","s69","s70","s71","s72","s73","s74","s75","s76","s77","s78","s79","s80","s81","s82","s83","s84","s85","s86","s87","s88","s89","s90","s91","s92","s93","s94","s95","s96","s97","s98","s99","s100","s101"

typedef const u16* const __attribute__((address_space(4)))* kaptr;
__device__ __forceinline__ void proj_phase(kaptr& ka, char*& ws, const unsigned wid, u32x4 (&vp)[5], unsigned& vl, u32x4 (&sa)[5], u32x2 s24, u32x2 s28, unsigned tdum, u32x4& srdA, u32x2& sd, unsigned& tag
    ) {
    const unsigned b = blockIdx.x, h = (b & 7) * 4 + ((b >> 3) & 3), mt = b >> 5;
    const unsigned rb = (wid < 2u ? 15u - mt : mt) * 128u;
    const unsigned lane = threadIdx.x & 63u, wc = wid & 1u;
    u32x2 vAc, vB;
#pragma unroll
    for (int par = 0; par < 2; ++par) {
        const unsigned lr = lane >> 3, cc = (lane & 7u) ^ (4u * par + (lr >> 1)), r0 = rb + wc * 64u + lr;
        vAc[par] = r0 * (DC * 2) + cc * 16u;
        const unsigned br = lane >> 4, p16 = lane & 15u, sc = (((p16 >> 1) ^ (br | (par << 2))) << 1) | (p16 & 1u);
        vB[par] = (16u * wid + br) * (NBIG * 2) + h * (HD * 2) + sc * 16u;
    }
    vl = lane * 16u;
    u32x2 s30 = {wid * 8192u, 65536u + wid * 4096u};
    asm volatile(PROJ_ASM
                 : "={s62}"(tag), "={s[56:59]}"(srdA), "={s[60:61]}"(sd), "+{s[8:11]}"(sa[0]), "+{s[12:15]}"(sa[1]), "+{s[16:19]}"(sa[2]), "+{s[20:23]}"(sa[3]), "+{s[24:25]}"(s24), "+{s[28:29]}"(s28), "+{s[30:31]}"(s30),
                   "={v[0:3]}"(vp[0]), "={v[4:7]}"(vp[1]), "={v[8:11]}"(vp[2]), "={v[12:15]}"(vp[3]), "={v[16:19]}"(vp[4]), "+{v[22:23]}"(vAc), "+{v[26:27]}"(vB), "+{v191}"(tdum), "+{s[26:27]}"(ws), "+{s[0:1]}"(ka)
                 : "{s2}"(b), "{s3}"(wid), "{v20}"(vl)
                 : "memory", "m0", "vcc", "scc", PROJ_VCLOB, PROJ_SCLOB, ACLOB_LO, ACLOB_HI);
    sa[4] = (u32x4){s24[0], 0u, 0u, 0u};
}

namespace att {
#define DI __device__ __forceinline__
typedef __bf16 bfx8 __attribute__((ext_vector_type(8)));
DI unsigned rfl(unsigned x) { return __builtin_amdgcn_readfirstlane(x); }
DI f32x4 mfma(u32x4 a, u32x4 b, f32x4 c) { return __builtin_amdgcn_mfma_f32_16x16x32_bf16(__builtin_bit_cast(bfx8, a), __builtin_bit_cast(bfx8, b), c, 0, 0, 0); }
typedef __bf16 bfx2 __attribute__((ext_vector_type(2)));
typedef float f32x2 __attribute__((ext_vector_type(2)));
DI unsigned cvtpk2(float lo, float hi) { f32x2 v = {lo, hi}; bfx2 b = __builtin_convertvector(v, bfx2); return __builtin_bit_cast(unsigned, b); }
DI void att_exact(const u16* __restrict__ x, const u16* __restrict__ wq, char* __restrict__ ws, unsigned h, unsigned r0, float g1) {
    const unsigned lane = lane_id(), g = lane >> 4, c = lane & 15u;
    f32x4 o[8];
#pragma unroll
    for (int n = 0; n < 8; ++n) o[n] = (f32x4){0.f, 0.f, 0.f, 0.f};
#pragma unroll 1
    for (unsigned kk = 0; kk < (unsigned)DM; kk += 32u) {
        const unsigned k = kk + 8u * g;
        const u32x4 xf = *reinterpret_cast<const u32x4*>(x + (r0 + c) * DM + k);
#pragma unroll
        for (int n = 0; n < 8; ++n) {
            const u16* w = wq + k * NBIG + h * HD + n * 16 + c; u32x4 wf;
#pragma unroll
            for (int e = 0; e < 4; ++e) wf[e] = (unsigned)w[2 * e * NBIG] | ((unsigned)w[(2 * e + 1) * NBIG] << 16);
            o[n] = mfma(wf, xf, o[n]);
        }
    }
    u32x4 q[4];
#pragma unroll
    for (int p = 0; p < 4; ++p) {
        const f32x4 a = o[2 * p] * 0.12751743082f, b = o[2 * p + 1] * 0.12751743082f;
        q[p] = (u32x4){cvtpk2(a[0], a[1]), cvtpk2(a[2], a[3]), cvtpk2(b[0], b[1]), cvtpk2(b[2], b[3])};
    }
#pragma unroll
    for (int n = 0; n < 8; ++n) o[n] = (f32x4){0.f, 0.f, 0.f, 0.f};
    const __amdgpu_buffer_rsrc_t srd = __builtin_amdgcn_make_buffer_rsrc((void*)(ws + WS_K + (size_t)h * 1048576), (short)0, 1048576, 0x00020000);
    float m = -3.0e38f, l = 0.f;
#pragma unroll 1
    for (unsigned k0 = 0; k0 <= r0 + 15u; k0 += 32u) {
        const unsigned tb = (k0 >> 6) * 32768u + (k0 & 32u) * 256u;
        f32x4 s[2]; float mx = m;
#pragma unroll
        for (int kt = 0; kt < 2; ++kt) {
            s[kt] = (f32x4){0.f, 0.f, 0.f, 0.f};
#pragma unroll
            for (int p = 0; p < 4; ++p) s[kt] = mfma(__builtin_amdgcn_raw_buffer_load_b128(srd, lane * 16u + (kt * 4 + p) * 1024u, tb, 16), q[p], s[kt]);
#pragma unroll
            for (int e = 0; e < 4; ++e) { if (k0 + 16u * kt + 4u * g + e > r0 + c) s[kt][e] = -3.0e38f; mx = fmaxf(mx, s[kt][e]); }
        }
        mx = fmaxf(mx, __shfl_xor(mx, 16)); mx = fmaxf(mx, __shfl_xor(mx, 32));
        const float al = __builtin_amdgcn_exp2f(m - mx); m = mx;
        u32x4 pf;
#pragma unroll
        for (int e = 0; e < 4; ++e) pf[e] = cvtpk2(__builtin_amdgcn_exp2f(s[e >> 1][2 * (e & 1)] - m), __builtin_amdgcn_exp2f(s[e >> 1][2 * (e & 1) + 1] - m));
        l *= al;
#pragma unroll
        for (int e = 0; e < 4; ++e) l += __uint_as_float(pf[e] << 16) + __uint_as_float(pf[e] & 0xffff0000u);
#pragma unroll
        for (int n = 0; n < 8; ++n) o[n] = mfma(__builtin_amdgcn_raw_buffer_load_b128(srd, lane * 16u + 16384u + n * 1024u, tb, 16), pf, o[n] * al);
    }
    l += __shfl_xor(l, 16); l += __shfl_xor(l, 32);
    const float f = g1 / l;
    const __amdgpu_buffer_rsrc_t srdA = __builtin_amdgcn_make_buffer_rsrc((void*)(ws + WS_AF), (short)0, 128 * KS_TOT * 1024, 0x00020000);
#pragma unroll
    for (int p = 0; p < 4; ++p) {
        const f32x4 a = o[2 * p] * f, b = o[2 * p + 1] * f;
        __builtin_amdgcn_raw_buffer_store_b128((u32x4){cvtpk2(a[0], a[1]), cvtpk2(a[2], a[3]), cvtpk2(b[0], b[1]), cvtpk2(b[2], b[3])}, srdA, lane * 16u + p * 1024u, ((r0 >> 4) * KS_TOT + 18u + 4u * h) * 1024u, 16);
    }
}
}

#define ATT_ASM \
"s_branch L_ago_%=\n" \
".p2align 12\n" \
"L_ab:\n" \
".fill (L_ab + 0 - .) / 4, 4, 0xBF800000\n" \
"L_ap0:\n" \
"s_setpc_b64 s[88:89]\n" \
"L_ago_%=:\n" \
"s_mov_b32 s101, 0\n" \
"s_waitcnt vmcnt(15) lgkmcnt(0)\n" \
"s_barrier\n" \
"v_accvgpr_write_b32 a216, 0\n" \
"v_accvgpr_write_b32 a217, 0\n" \
"v_accvgpr_write_b32 a218, 0\n" \
"v_accvgpr_write_b32 a219, 0\n" \
"v_bfe_u32 v61, v20, 2, 5\n" \
"s_mov_b32 s96, s24\n" \
"v_mfma_f32_32x32x16_bf16 v[128:143], a[216:219], a[216:219], 0\n" \
"buffer_load_dword v57, v61, s[56:59], s96 offen sc1\n" \
"v_and_b32_e32 v63, 0x300, v20\n" \
"ds_read_b128 v[96:99], v63 offset:0\n" \
"v_add_u32_e32 v58, s22, v20\n" \
"ds_read_b128 a[0:3], v58 offset:0\n" \
"v_mfma_f32_32x32x16_bf16 v[144:159], a[216:219], a[216:219], 0\n" \
"ds_read_b128 a[16:19], v58 offset:8192\n" \
"v_add_u32_e32 v59, s23, v20\n" \
"ds_read_b128 a[32:35], v59 offset:0\n" \
"ds_read_b128 a[48:51], v59 offset:8192\n" \
"ds_read_b128 v[100:103], v63 offset:1024\n" \
"ds_read_b128 a[4:7], v58 offset:1024\n" \
"v_mfma_f32_32x32x16_bf16 v[160:175], a[216:219], a[216:219], 0\n" \
"ds_read_b128 a[20:23], v58 offset:9216\n" \
"ds_read_b128 a[36:39], v59 offset:1024\n" \
"ds_read_b128 a[52:55], v59 offset:9216\n" \
"s_sub_u32 s67, s12, 0xc000\n" \
"ds_read_b128 v[104:107], v63 offset:2048\n" \
"s_mov_b32 s65, s12\n" \
"v_mfma_f32_32x32x16_bf16 v[176:191], a[216:219], a[216:219], 0\n" \
"v_add_u32_e32 v55, s67, v20\n" \
"v_add_u32_e32 v52, 0xc000, v55\n" \
"s_xor_b32 m0, s65, 0x10000\n" \
"ds_read_b128 a[8:11], v58 offset:2048\n" \
"buffer_load_dwordx4 v52, s[8:11], 0 offen offset:0 sc1 lds\n" \
"v_mfma_f32_32x32x16_bf16 v[192:207], a[216:219], a[216:219], 0\n" \
"ds_read_b128 a[24:27], v58 offset:10240\n" \
"ds_read_b128 a[40:43], v59 offset:2048\n" \
"ds_read_b128 a[56:59], v59 offset:10240\n" \
"ds_read_b128 v[108:111], v63 offset:3072\n" \
"buffer_load_dwordx4 v52, s[8:11], 0 offen offset:1024 sc1 lds\n" \
"v_mfma_f32_32x32x16_bf16 v[208:223], a[216:219], a[216:219], 0\n" \
"ds_read_b128 a[12:15], v58 offset:3072\n" \
"ds_read_b128 a[28:31], v58 offset:11264\n" \
"ds_read_b128 a[44:47], v59 offset:3072\n" \
"ds_read_b128 a[60:63], v59 offset:11264\n" \
"s_waitcnt lgkmcnt(15)\n" \
"v_xor_b32_e32 v96, 0x80008000, v96\n" \
"v_mfma_f32_32x32x16_bf16 v[224:239], a[216:219], a[216:219], 0\n" \
"v_xor_b32_e32 v97, 0x80008000, v97\n" \
"v_xor_b32_e32 v98, 0x80008000, v98\n" \
"v_xor_b32_e32 v99, 0x80008000, v99\n" \
"ds_read_b128 a[160:163], v20 offset:0\n" \
"ds_read_b128 a[164:167], v20 offset:1024\n" \
"s_waitcnt lgkmcnt(15)\n" \
"v_xor_b32_e32 v100, 0x80008000, v100\n" \
"v_mfma_f32_32x32x16_bf16 v[240:255], a[216:219], a[216:219], 0\n" \
"v_xor_b32_e32 v101, 0x80008000, v101\n" \
"v_xor_b32_e32 v102, 0x80008000, v102\n" \
"v_xor_b32_e32 v103, 0x80008000, v103\n" \
"ds_read_b128 a[168:171], v20 offset:2048\n" \
"ds_read_b128 a[172:175], v20 offset:3072\n" \
"ds_read_b128 a[188:191], v20 offset:7168\n" \
"v_mfma_f32_16x16x32_bf16 a[192:195], a[216:219], a[216:219], 0\n" \
"ds_read_b128 a[184:187], v20 offset:6144\n" \
"s_waitcnt lgkmcnt(15)\n" \
"v_xor_b32_e32 v104, 0x80008000, v104\n" \
"v_mfma_f32_16x16x32_bf16 a[196:199], a[216:219], a[216:219], 0\n" \
"v_xor_b32_e32 v105, 0x80008000, v105\n" \
"v_xor_b32_e32 v106, 0x80008000, v106\n" \
"v_mfma_f32_16x16x32_bf16 v[32:35], v[96:99], a[0:3], 0\n" \
"v_xor_b32_e32 v107, 0x80008000, v107\n" \
"ds_read_b128 a[180:183], v20 offset:5120\n" \
"v_mfma_f32_16x16x32_bf16 v[36:39], v[96:99], a[16:19], 0\n" \
"ds_read_b128 a[176:179], v20 offset:4096\n" \
"s_waitcnt lgkmcnt(12)\n" \
"v_xor_b32_e32 v108, 0x80008000, v108\n" \
"v_mfma_f32_16x16x32_bf16 v[40:43], v[96:99], a[32:35], 0\n" \
"v_xor_b32_e32 v109, 0x80008000, v109\n" \
"v_xor_b32_e32 v110, 0x80008000, v110\n" \
"v_mfma_f32_16x16x32_bf16 v[44:47], v[96:99], a[48:51], 0\n" \
"v_xor_b32_e32 v111, 0x80008000, v111\n" \
"v_add_u32_e32 v60, s15, v20\n" \
"v_mfma_f32_16x16x32_bf16 v[32:35], v[100:103], a[4:7], v[32:35]\n" \
"v_add_u32_e32 v60, 0x10000, v60\n" \
"ds_read_b128 a[128:131], v20 offset:8192\n" \
"v_mfma_f32_16x16x32_bf16 v[36:39], v[100:103], a[20:23], v[36:39]\n" \
"ds_read_b128 a[144:147], v20 offset:12288\n" \
"ds_read_b128 a[132:135], v20 offset:9216\n" \
"v_mfma_f32_16x16x32_bf16 v[40:43], v[100:103], a[36:39], v[40:43]\n" \
"ds_read_b128 a[148:151], v20 offset:13312\n" \
"ds_read_b128 a[136:139], v20 offset:10240\n" \
"v_mfma_f32_16x16x32_bf16 v[44:47], v[100:103], a[52:55], v[44:47]\n" \
"ds_read_b128 a[152:155], v20 offset:14336\n" \
"ds_read_b128 a[140:143], v20 offset:11264\n" \
"v_mfma_f32_16x16x32_bf16 v[32:35], v[104:107], a[8:11], v[32:35]\n" \
"ds_read_b128 a[156:159], v20 offset:15360\n" \
"ds_read_b128 a[64:67], v60 offset:16384\n" \
"v_mfma_f32_16x16x32_bf16 v[36:39], v[104:107], a[24:27], v[36:39]\n" \
"ds_read_b128 a[68:71], v60 offset:17408\n" \
"ds_read_b128 a[72:75], v60 offset:18432\n" \
"v_mfma_f32_16x16x32_bf16 v[40:43], v[104:107], a[40:43], v[40:43]\n" \
"ds_read_b128 a[76:79], v60 offset:19456\n" \
"ds_read_b128 a[80:83], v60 offset:24576\n" \
"v_mfma_f32_16x16x32_bf16 v[44:47], v[104:107], a[56:59], v[44:47]\n" \
"ds_read_b128 a[84:87], v60 offset:25600\n" \
"s_waitcnt lgkmcnt(15)\n" \
"v_mfma_f32_16x16x32_bf16 v[32:35], v[108:111], a[12:15], v[32:35]\n" \
"ds_read_b128 a[88:91], v60 offset:26624\n" \
"ds_read_b128 a[92:95], v60 offset:27648\n" \
"v_mfma_f32_16x16x32_bf16 v[36:39], v[108:111], a[28:31], v[36:39]\n" \
"ds_read_b128 a[96:99], v60 offset:0\n" \
"ds_read_b128 a[100:103], v60 offset:1024\n" \
"v_mfma_f32_16x16x32_bf16 v[40:43], v[108:111], a[44:47], v[40:43]\n" \
"ds_read_b128 a[104:107], v60 offset:2048\n" \
"ds_read_b128 a[108:111], v60 offset:3072\n" \
"v_mfma_f32_16x16x32_bf16 v[44:47], v[108:111], a[60:63], v[44:47]\n" \
"ds_read_b128 a[112:115], v60 offset:8192\n" \
"ds_read_b128 a[116:119], v60 offset:9216\n" \
"v_mfma_f32_16x16x32_bf16 v[64:67], a[160:163], a[0:3], v[32:35]\n" \
"ds_read_b128 a[120:123], v60 offset:10240\n" \
"ds_read_b128 a[124:127], v60 offset:11264\n" \
"v_mfma_f32_16x16x32_bf16 v[64:67], a[164:167], a[4:7], v[64:67]\n" \
"v_bfe_u32 v26, v20, 8, 2\n" \
"s_mov_b32 m0, s20\n" \
"v_mfma_f32_16x16x32_bf16 v[64:67], a[168:171], a[8:11], v[64:67]\n" \
"v_bfe_u32 v25, v20, 5, 3\n" \
"v_lshlrev_b32_e32 v26, 1, v26\n" \
"v_mfma_f32_16x16x32_bf16 v[64:67], a[172:175], a[12:15], v[64:67]\n" \
"global_load_lds_dword v[16:17], off\n" \
"v_mfma_f32_16x16x32_bf16 v[68:71], a[188:191], a[12:15], v[32:35]\n" \
"v_sub_u32_e32 v25, v25, v26\n" \
"v_and_b32_e32 v26, 16, v20\n" \
"v_mfma_f32_16x16x32_bf16 v[68:71], a[184:187], a[8:11], v[68:71]\n" \
"v_mov_b32_e32 v27, 0x3f80\n" \
"v_lshlrev_b32_e32 v26, v26, v27\n" \
"v_mfma_f32_16x16x32_bf16 v[68:71], a[180:183], a[4:7], v[68:71]\n" \
"v_mov_b32_e32 v54, 0x3f803f80\n" \
"s_waitcnt lgkmcnt(15)\n" \
"v_mfma_f32_16x16x32_bf16 v[68:71], a[176:179], a[0:3], v[68:71]\n" \
"s_mov_b32 s84, 0x33333333\n" \
"s_mov_b32 s85, 0xcccccccc\n" \
"v_mfma_f32_16x16x32_bf16 v[76:79], a[176:179], a[16:19], v[36:39]\n" \
"s_mov_b32 s86, 0x11111111\n" \
"s_mov_b32 s87, 0x11111111\n" \
"v_mfma_f32_16x16x32_bf16 v[76:79], a[180:183], a[20:23], v[76:79]\n" \
"s_mov_b32 s88, 0x44444444\n" \
"s_mov_b32 s89, 0x44444444\n" \
"v_mfma_f32_16x16x32_bf16 v[76:79], a[184:187], a[24:27], v[76:79]\n" \
"v_exp_f32_e32 v70, v70\n" \
"v_mfma_f32_16x16x32_bf16 v[76:79], a[188:191], a[28:31], v[76:79]\n" \
"v_exp_f32_e32 v71, v71\n" \
"v_mfma_f32_16x16x32_bf16 v[72:75], a[172:175], a[28:31], v[36:39]\n" \
"v_exp_f32_e32 v68, v68\n" \
"v_mfma_f32_16x16x32_bf16 v[72:75], a[168:171], a[24:27], v[72:75]\n" \
"v_exp_f32_e32 v69, v69\n" \
"v_mfma_f32_16x16x32_bf16 v[72:75], a[164:167], a[20:23], v[72:75]\n" \
"v_bfe_i32 v21, v20, 4, 1\n" \
"v_cvt_pk_bf16_f32 v71, v70, v71\n" \
"v_mfma_f32_16x16x32_bf16 v[72:75], a[160:163], a[16:19], v[72:75]\n" \
"v_exp_f32_e32 v66, v66\n" \
"v_mfma_f32_16x16x32_bf16 v[80:83], a[160:163], a[32:35], v[40:43]\n" \
"v_exp_f32_e32 v67, v67\n" \
"v_mfma_f32_16x16x32_bf16 v[80:83], a[164:167], a[36:39], v[80:83]\n" \
"v_exp_f32_e32 v64, v64\n" \
"v_mfma_f32_16x16x32_bf16 v[80:83], a[168:171], a[40:43], v[80:83]\n" \
"v_exp_f32_e32 v65, v65\n" \
"v_mfma_f32_16x16x32_bf16 v[80:83], a[172:175], a[44:47], v[80:83]\n" \
"v_exp_f32_e32 v72, v72\n" \
"v_mfma_f32_16x16x32_bf16 v[84:87], a[188:191], a[44:47], v[40:43]\n" \
"s_waitcnt vmcnt(3)\n" \
"v_cmp_ne_u32_e32 vcc, s62, v57\n" \
"v_mfma_f32_16x16x32_bf16 v[84:87], a[184:187], a[40:43], v[84:87]\n" \
"v_exp_f32_e32 v73, v73\n" \
"v_mfma_f32_16x16x32_bf16 v[84:87], a[180:183], a[36:39], v[84:87]\n" \
"v_exp_f32_e32 v74, v74\n" \
"v_mfma_f32_16x16x32_bf16 v[84:87], a[176:179], a[32:35], v[84:87]\n" \
"s_cbranch_vccnz L_skb_%=\n" \
"L_rkb_%=:\n" \
"v_cmp_eq_u32_e32 vcc, 0, v25\n" \
"v_mfma_f32_16x16x32_bf16 v[92:95], a[176:179], a[48:51], v[44:47]\n" \
"v_exp_f32_e32 v75, v75\n" \
"v_mfma_f32_16x16x32_bf16 v[92:95], a[180:183], a[52:55], v[92:95]\n" \
"v_cndmask_b32_e32 v28, 0, v26, vcc\n" \
"v_cvt_pk_bf16_f32 v72, v72, v73\n" \
"v_mfma_f32_16x16x32_bf16 v[92:95], a[184:187], a[56:59], v[92:95]\n" \
"v_exp_f32_e32 v76, v76\n" \
"v_mfma_f32_16x16x32_bf16 v[92:95], a[188:191], a[60:63], v[92:95]\n" \
"v_exp_f32_e32 v77, v77\n" \
"v_mfma_f32_16x16x32_bf16 v[88:91], a[172:175], a[60:63], v[44:47]\n" \
"v_exp_f32_e32 v78, v78\n" \
"v_mfma_f32_16x16x32_bf16 v[88:91], a[168:171], a[56:59], v[88:91]\n" \
"v_exp_f32_e32 v79, v79\n" \
"v_mfma_f32_16x16x32_bf16 v[88:91], a[164:167], a[52:55], v[88:91]\n" \
"v_cndmask_b32_e64 v22, 0, v54, s[84:85]\n" \
"v_cndmask_b32_e64 v23, 0, v54, s[86:87]\n" \
"v_mfma_f32_16x16x32_bf16 v[88:91], a[160:163], a[48:51], v[88:91]\n" \
"v_cndmask_b32_e64 v24, 0, v54, s[88:89]\n" \
"v_and_b32_e32 v21, 0xaaaaaaaa, v21\n" \
"s_waitcnt vmcnt(4) lgkmcnt(0)\n" \
"s_barrier\n" \
"v_cmp_eq_u32_e32 vcc, 1, v25\n" \
"v_cvt_pk_bf16_f32 v70, v68, v69\n" \
"v_cvt_pk_bf16_f32 v73, v74, v75\n" \
"v_mov_b32_e32 v62, 0\n" \
"v_accvgpr_write_b32 a208, 0\n" \
"v_accvgpr_write_b32 a209, 0\n" \
"v_accvgpr_write_b32 a210, 0\n" \
"v_accvgpr_write_b32 a211, 0\n" \
"v_accvgpr_write_b32 a200, v22\n" \
"v_accvgpr_write_b32 a201, v22\n" \
"v_accvgpr_write_b32 a202, v22\n" \
"v_accvgpr_write_b32 a203, v22\n" \
"v_accvgpr_write_b32 a204, v23\n" \
"v_accvgpr_write_b32 a205, v23\n" \
"v_accvgpr_write_b32 a206, v23\n" \
"v_accvgpr_write_b32 a207, v23\n" \
"v_accvgpr_write_b32 a212, v24\n" \
"v_accvgpr_write_b32 a213, v24\n" \
"v_accvgpr_write_b32 a214, v24\n" \
"v_accvgpr_write_b32 a215, v24\n" \
"v_or_b32_e32 v21, 0x44444444, v21\n" \
"s_mov_b32 s64, 0x8000\n" \
"v_add_u32_e32 v50, 0x6000, v55\n" \
"v_add_u32_e32 v51, 0xa000, v55\n" \
"v_add_u32_e32 v53, 0x10000, v55\n" \
"v_add_u32_e32 v48, 0xc000, v20\n" \
"v_add_u32_e32 v49, s17, v20\n" \
"v_cndmask_b32_e32 v29, 0, v26, vcc\n" \
"v_accvgpr_write_b32 a220, v96\n" \
"v_accvgpr_write_b32 a221, v97\n" \
"v_accvgpr_write_b32 a222, v98\n" \
"v_accvgpr_write_b32 a223, v99\n" \
"v_accvgpr_write_b32 a224, v100\n" \
"v_accvgpr_write_b32 a225, v101\n" \
"v_accvgpr_write_b32 a226, v102\n" \
"v_accvgpr_write_b32 a227, v103\n" \
"v_accvgpr_write_b32 a228, v104\n" \
"v_accvgpr_write_b32 a229, v105\n" \
"v_accvgpr_write_b32 a230, v106\n" \
"v_accvgpr_write_b32 a231, v107\n" \
"v_accvgpr_write_b32 a232, v108\n" \
"v_accvgpr_write_b32 a233, v109\n" \
"v_accvgpr_write_b32 a234, v110\n" \
"v_accvgpr_write_b32 a235, v111\n" \
"v_cvt_pk_bf16_f32 v69, v66, v67\n" \
"v_cvt_pk_bf16_f32 v68, v64, v65\n" \
"v_cvt_pk_bf16_f32 v74, v76, v77\n" \
"v_cvt_pk_bf16_f32 v75, v78, v79\n" \
"s_sub_u32 s66, s18, 1\n" \
"s_cbranch_scc1 L_tr_%=\n" \
".fill (L_ab + 1856 - .) / 4, 4, 0xBF800000\n" \
"L_1_%=:\n" \
"v_mfma_f32_16x16x32_bf16 v[96:99], a[128:131], a[0:3], v[32:35]\n" \
"s_xor_b32 m0, s65, 0x12000\n" \
"ds_read_b128 a[160:163], v49 offset:0\n" \
"v_mfma_f32_16x16x32_bf16 v[96:99], a[132:135], a[4:7], v[96:99]\n" \
"buffer_load_dwordx4 v50, s[8:11], s64 offen offset:0 sc1 lds\n" \
"v_mfma_f32_16x16x32_bf16 v[96:99], a[136:139], a[8:11], v[96:99]\n" \
"ds_read_b128 a[164:167], v49 offset:1024\n" \
"ds_read_b128 a[168:171], v49 offset:2048\n" \
"v_mfma_f32_16x16x32_bf16 v[96:99], a[140:143], a[12:15], v[96:99]\n" \
"ds_read_b128 a[172:175], v49 offset:3072\n" \
"ds_read_b128 a[176:179], v49 offset:4096\n" \
"v_mfma_f32_16x16x32_bf16 v[100:103], a[156:159], a[12:15], v[32:35]\n" \
"ds_read_b128 a[180:183], v49 offset:5120\n" \
"ds_read_b128 a[184:187], v49 offset:6144\n" \
"v_mfma_f32_16x16x32_bf16 v[100:103], a[152:155], a[8:11], v[100:103]\n" \
"v_exp_f32_e64 v86, v86\n" \
"v_mfma_f32_16x16x32_bf16 v[100:103], a[148:151], a[4:7], v[100:103]\n" \
"v_exp_f32_e64 v87, v87\n" \
"v_mfma_f32_16x16x32_bf16 v[100:103], a[144:147], a[0:3], v[100:103]\n" \
"ds_read_b128 a[188:191], v49 offset:7168\n" \
"v_cvt_pk_bf16_f32 v87, v86, v87\n" \
"v_mfma_f32_16x16x32_bf16 v[108:111], a[144:147], a[16:19], v[36:39]\n" \
"v_exp_f32_e64 v84, v84\n" \
"v_mfma_f32_16x16x32_bf16 v[108:111], a[148:151], a[20:23], v[108:111]\n" \
"v_exp_f32_e64 v85, v85\n" \
"v_mfma_f32_16x16x32_bf16 v[108:111], a[152:155], a[24:27], v[108:111]\n" \
"v_exp_f32_e64 v82, v82\n" \
"v_mfma_f32_16x16x32_bf16 v[108:111], a[156:159], a[28:31], v[108:111]\n" \
"v_exp_f32_e64 v83, v83\n" \
"v_mfma_f32_16x16x32_bf16 v[104:107], a[140:143], a[28:31], v[36:39]\n" \
"v_exp_f32_e64 v80, v80\n" \
"v_mfma_f32_16x16x32_bf16 v[104:107], a[136:139], a[24:27], v[104:107]\n" \
"v_exp_f32_e64 v81, v81\n" \
"v_mfma_f32_16x16x32_bf16 v[104:107], a[132:135], a[20:23], v[104:107]\n" \
"v_exp_f32_e64 v88, v88\n" \
"v_mfma_f32_16x16x32_bf16 v[104:107], a[128:131], a[16:19], v[104:107]\n" \
"buffer_load_dwordx4 v50, s[8:11], s64 offen offset:1024 sc1 lds\n" \
"v_mfma_f32_16x16x32_bf16 v[112:115], a[128:131], a[32:35], v[40:43]\n" \
"s_xor_b32 m0, s65, 0x6000\n" \
"v_cvt_pk_bf16_f32 v86, v84, v85\n" \
"v_mfma_f32_16x16x32_bf16 v[112:115], a[132:135], a[36:39], v[112:115]\n" \
"v_cvt_pk_bf16_f32 v85, v82, v83\n" \
"v_cvt_pk_bf16_f32 v84, v80, v81\n" \
"v_mfma_f32_16x16x32_bf16 v[112:115], a[136:139], a[40:43], v[112:115]\n" \
"v_exp_f32_e64 v89, v89\n" \
"v_mfma_f32_16x16x32_bf16 v[112:115], a[140:143], a[44:47], v[112:115]\n" \
"v_exp_f32_e64 v90, v90\n" \
"v_mfma_f32_16x16x32_bf16 v[116:119], a[156:159], a[44:47], v[40:43]\n" \
"v_exp_f32_e64 v91, v91\n" \
"v_mfma_f32_16x16x32_bf16 v[116:119], a[152:155], a[40:43], v[116:119]\n" \
"v_exp_f32_e64 v92, v92\n" \
"v_mfma_f32_16x16x32_bf16 v[116:119], a[148:151], a[36:39], v[116:119]\n" \
"v_exp_f32_e64 v93, v93\n" \
"v_mfma_f32_16x16x32_bf16 v[116:119], a[144:147], a[32:35], v[116:119]\n" \
"v_exp_f32_e64 v94, v94\n" \
"v_mfma_f32_16x16x32_bf16 v[124:127], a[144:147], a[48:51], v[44:47]\n" \
"v_cvt_pk_bf16_f32 v88, v88, v89\n" \
"v_cvt_pk_bf16_f32 v89, v90, v91\n" \
"v_mfma_f32_16x16x32_bf16 v[124:127], a[148:151], a[52:55], v[124:127]\n" \
"v_exp_f32_e64 v95, v95\n" \
"v_mfma_f32_16x16x32_bf16 v[124:127], a[152:155], a[56:59], v[124:127]\n" \
"v_cvt_pk_bf16_f32 v90, v92, v93\n" \
"v_cvt_pk_bf16_f32 v91, v94, v95\n" \
"v_mfma_f32_16x16x32_bf16 v[124:127], a[156:159], a[60:63], v[124:127]\n" \
"ds_read_b128 a[144:147], v48 offset:4096\n" \
"ds_read_b128 a[148:151], v48 offset:5120\n" \
"v_mfma_f32_16x16x32_bf16 v[120:123], a[140:143], a[60:63], v[44:47]\n" \
"ds_read_b128 a[152:155], v48 offset:6144\n" \
"ds_read_b128 a[140:143], v48 offset:3072\n" \
"v_mfma_f32_16x16x32_bf16 v[120:123], a[136:139], a[56:59], v[120:123]\n" \
"ds_read_b128 a[136:139], v48 offset:2048\n" \
"ds_read_b128 a[156:159], v48 offset:7168\n" \
"v_mfma_f32_16x16x32_bf16 v[120:123], a[132:135], a[52:55], v[120:123]\n" \
"buffer_load_dwordx4 v51, s[8:11], s64 offen offset:0 sc1 lds\n" \
"v_mfma_f32_16x16x32_bf16 v[120:123], a[128:131], a[48:51], v[120:123]\n" \
"ds_read_b128 a[128:131], v48 offset:0\n" \
"s_waitcnt lgkmcnt(7)\n" \
"v_mfma_f32_16x16x32_bf16 v[128:131], a[160:163], v[68:71], v[128:131]\n" \
"v_exp_f32_e32 v102, v102\n" \
"v_mfma_f32_16x16x32_bf16 v[160:163], a[160:163], v[72:75], v[160:163]\n" \
"v_exp_f32_e64 v103, v103\n" \
"v_mfma_f32_16x16x32_bf16 v[132:135], a[164:167], v[68:71], v[132:135]\n" \
"ds_read_b128 a[132:135], v48 offset:1024\n" \
"v_cvt_pk_bf16_f32 v103, v102, v103\n" \
"v_mfma_f32_16x16x32_bf16 v[164:167], a[164:167], v[72:75], v[164:167]\n" \
"v_exp_f32_e64 v100, v100\n" \
"v_mfma_f32_16x16x32_bf16 v[136:139], a[168:171], v[68:71], v[136:139]\n" \
"v_exp_f32_e64 v101, v101\n" \
"v_mfma_f32_16x16x32_bf16 v[168:171], a[168:171], v[72:75], v[168:171]\n" \
"v_exp_f32_e64 v98, v98\n" \
"v_mfma_f32_16x16x32_bf16 v[140:143], a[172:175], v[68:71], v[140:143]\n" \
"v_exp_f32_e64 v99, v99\n" \
"v_mfma_f32_16x16x32_bf16 v[172:175], a[172:175], v[72:75], v[172:175]\n" \
"v_exp_f32_e64 v96, v96\n" \
"v_mfma_f32_16x16x32_bf16 v[144:147], a[176:179], v[68:71], v[144:147]\n" \
"v_exp_f32_e64 v97, v97\n" \
"v_mfma_f32_16x16x32_bf16 v[176:179], a[176:179], v[72:75], v[176:179]\n" \
"v_exp_f32_e64 v104, v104\n" \
"v_mfma_f32_16x16x32_bf16 v[148:151], a[180:183], v[68:71], v[148:151]\n" \
"v_exp_f32_e64 v105, v105\n" \
"v_mfma_f32_16x16x32_bf16 v[180:183], a[180:183], v[72:75], v[180:183]\n" \
"v_cvt_pk_bf16_f32 v102, v100, v101\n" \
"v_cvt_pk_bf16_f32 v101, v98, v99\n" \
"v_mfma_f32_16x16x32_bf16 v[152:155], a[184:187], v[68:71], v[152:155]\n" \
"v_cvt_pk_bf16_f32 v100, v96, v97\n" \
"v_cvt_pk_bf16_f32 v104, v104, v105\n" \
"v_mfma_f32_16x16x32_bf16 v[184:187], a[184:187], v[72:75], v[184:187]\n" \
"buffer_load_dwordx4 v51, s[8:11], s64 offen offset:1024 sc1 lds\n" \
"v_mfma_f32_16x16x32_bf16 v[156:159], a[188:191], v[68:71], v[156:159]\n" \
"v_exp_f32_e64 v106, v106\n" \
"v_mfma_f32_16x16x32_bf16 v[188:191], a[188:191], v[72:75], v[188:191]\n" \
"v_exp_f32_e64 v107, v107\n" \
"v_smfmac_f32_16x16x64_bf16 a[192:195], a[200:203], v[68:75], v21\n" \
"v_exp_f32_e64 v108, v108\n" \
"v_mfma_f32_16x16x32_bf16 v[220:223], a[188:191], v[84:87], v[220:223]\n" \
"v_exp_f32_e64 v109, v109\n" \
"v_mfma_f32_16x16x32_bf16 v[252:255], a[188:191], v[88:91], v[252:255]\n" \
"v_exp_f32_e64 v110, v110\n" \
"v_mfma_f32_16x16x32_bf16 v[216:219], a[184:187], v[84:87], v[216:219]\n" \
"v_exp_f32_e64 v111, v111\n" \
"v_mfma_f32_16x16x32_bf16 v[248:251], a[184:187], v[88:91], v[248:251]\n" \
"v_exp_f32_e64 v118, v118\n" \
"v_mfma_f32_16x16x32_bf16 v[212:215], a[180:183], v[84:87], v[212:215]\n" \
"v_exp_f32_e64 v119, v119\n" \
"v_mfma_f32_16x16x32_bf16 v[244:247], a[180:183], v[88:91], v[244:247]\n" \
"v_cvt_pk_bf16_f32 v105, v106, v107\n" \
"v_cvt_pk_bf16_f32 v106, v108, v109\n" \
"v_mfma_f32_16x16x32_bf16 v[208:211], a[176:179], v[84:87], v[208:211]\n" \
"v_cvt_pk_bf16_f32 v107, v110, v111\n" \
"v_cvt_pk_bf16_f32 v119, v118, v119\n" \
"v_mfma_f32_16x16x32_bf16 v[240:243], a[176:179], v[88:91], v[240:243]\n" \
"v_exp_f32_e64 v116, v116\n" \
"v_mfma_f32_16x16x32_bf16 v[204:207], a[172:175], v[84:87], v[204:207]\n" \
"v_exp_f32_e64 v117, v117\n" \
"v_mfma_f32_16x16x32_bf16 v[236:239], a[172:175], v[88:91], v[236:239]\n" \
"v_exp_f32_e64 v114, v114\n" \
"v_mfma_f32_16x16x32_bf16 v[200:203], a[168:171], v[84:87], v[200:203]\n" \
"v_exp_f32_e64 v115, v115\n" \
"v_mfma_f32_16x16x32_bf16 v[232:235], a[168:171], v[88:91], v[232:235]\n" \
"v_exp_f32_e64 v112, v112\n" \
"v_mfma_f32_16x16x32_bf16 v[196:199], a[164:167], v[84:87], v[196:199]\n" \
"v_exp_f32_e64 v113, v113\n" \
"v_mfma_f32_16x16x32_bf16 v[228:231], a[164:167], v[88:91], v[228:231]\n" \
"v_exp_f32_e64 v120, v120\n" \
"v_mfma_f32_16x16x32_bf16 v[192:195], a[160:163], v[84:87], v[192:195]\n" \
"v_exp_f32_e64 v121, v121\n" \
"v_mfma_f32_16x16x32_bf16 v[224:227], a[160:163], v[88:91], v[224:227]\n" \
"v_cvt_pk_bf16_f32 v118, v116, v117\n" \
"v_cvt_pk_bf16_f32 v117, v114, v115\n" \
"v_smfmac_f32_16x16x64_bf16 a[196:199], a[200:203], v[84:91], v21\n" \
"v_cvt_pk_bf16_f32 v116, v112, v113\n" \
"v_cvt_pk_bf16_f32 v120, v120, v121\n" \
"s_waitcnt vmcnt(4) lgkmcnt(0)\n" \
"s_barrier\n" \
"v_mfma_f32_16x16x32_bf16 v[64:67], a[128:131], a[0:3], v[32:35]\n" \
"s_xor_b32 m0, s65, 0x14000\n" \
"ds_read_b128 a[160:163], v49 offset:8192\n" \
"v_mfma_f32_16x16x32_bf16 v[64:67], a[132:135], a[4:7], v[64:67]\n" \
"buffer_load_dwordx4 v52, s[8:11], s64 offen offset:0 sc1 lds\n" \
"v_mfma_f32_16x16x32_bf16 v[64:67], a[136:139], a[8:11], v[64:67]\n" \
"ds_read_b128 a[164:167], v49 offset:9216\n" \
"ds_read_b128 a[168:171], v49 offset:10240\n" \
"v_mfma_f32_16x16x32_bf16 v[64:67], a[140:143], a[12:15], v[64:67]\n" \
"ds_read_b128 a[172:175], v49 offset:11264\n" \
"ds_read_b128 a[176:179], v49 offset:12288\n" \
"v_mfma_f32_16x16x32_bf16 v[68:71], a[156:159], a[12:15], v[32:35]\n" \
"ds_read_b128 a[180:183], v49 offset:13312\n" \
"ds_read_b128 a[184:187], v49 offset:14336\n" \
"v_mfma_f32_16x16x32_bf16 v[68:71], a[152:155], a[8:11], v[68:71]\n" \
"v_exp_f32_e64 v122, v122\n" \
"v_mfma_f32_16x16x32_bf16 v[68:71], a[148:151], a[4:7], v[68:71]\n" \
"v_exp_f32_e64 v123, v123\n" \
"v_mfma_f32_16x16x32_bf16 v[68:71], a[144:147], a[0:3], v[68:71]\n" \
"ds_read_b128 a[188:191], v49 offset:15360\n" \
"v_cvt_pk_bf16_f32 v121, v122, v123\n" \
"v_mfma_f32_16x16x32_bf16 v[76:79], a[144:147], a[16:19], v[36:39]\n" \
"v_exp_f32_e64 v124, v124\n" \
"v_mfma_f32_16x16x32_bf16 v[76:79], a[148:151], a[20:23], v[76:79]\n" \
"v_exp_f32_e64 v125, v125\n" \
"v_mfma_f32_16x16x32_bf16 v[76:79], a[152:155], a[24:27], v[76:79]\n" \
"v_exp_f32_e64 v126, v126\n" \
"v_mfma_f32_16x16x32_bf16 v[76:79], a[156:159], a[28:31], v[76:79]\n" \
"v_exp_f32_e64 v127, v127\n" \
"v_mfma_f32_16x16x32_bf16 v[72:75], a[140:143], a[28:31], v[36:39]\n" \
"v_cvt_pk_bf16_f32 v122, v124, v125\n" \
"v_cvt_pk_bf16_f32 v123, v126, v127\n" \
"v_mfma_f32_16x16x32_bf16 v[72:75], a[136:139], a[24:27], v[72:75]\n" \
"v_exp_f32_e64 v70, v70\n" \
"v_mfma_f32_16x16x32_bf16 v[72:75], a[132:135], a[20:23], v[72:75]\n" \
"v_exp_f32_e64 v71, v71\n" \
"v_mfma_f32_16x16x32_bf16 v[72:75], a[128:131], a[16:19], v[72:75]\n" \
"buffer_load_dwordx4 v52, s[8:11], s64 offen offset:1024 sc1 lds\n" \
"v_mfma_f32_16x16x32_bf16 v[80:83], a[128:131], a[32:35], v[40:43]\n" \
"s_mov_b32 m0, s65\n" \
"v_cvt_pk_bf16_f32 v71, v70, v71\n" \
"v_mfma_f32_16x16x32_bf16 v[80:83], a[132:135], a[36:39], v[80:83]\n" \
"v_exp_f32_e32 v68, v68\n" \
"v_mfma_f32_16x16x32_bf16 v[80:83], a[136:139], a[40:43], v[80:83]\n" \
"v_exp_f32_e64 v69, v69\n" \
"v_mfma_f32_16x16x32_bf16 v[80:83], a[140:143], a[44:47], v[80:83]\n" \
"v_exp_f32_e64 v72, v72\n" \
"v_mfma_f32_16x16x32_bf16 v[84:87], a[156:159], a[44:47], v[40:43]\n" \
"v_exp_f32_e64 v73, v73\n" \
"v_mfma_f32_16x16x32_bf16 v[84:87], a[152:155], a[40:43], v[84:87]\n" \
"v_exp_f32_e64 v74, v74\n" \
"v_mfma_f32_16x16x32_bf16 v[84:87], a[148:151], a[36:39], v[84:87]\n" \
"v_exp_f32_e64 v75, v75\n" \
"v_mfma_f32_16x16x32_bf16 v[84:87], a[144:147], a[32:35], v[84:87]\n" \
"v_exp_f32_e64 v66, v66\n" \
"v_mfma_f32_16x16x32_bf16 v[92:95], a[144:147], a[48:51], v[44:47]\n" \
"ds_read_b128 a[144:147], v48 offset:12288\n" \
"v_cvt_pk_bf16_f32 v72, v72, v73\n" \
"v_mfma_f32_16x16x32_bf16 v[92:95], a[148:151], a[52:55], v[92:95]\n" \
"ds_read_b128 a[148:151], v48 offset:13312\n" \
"v_cvt_pk_bf16_f32 v70, v68, v69\n" \
"v_mfma_f32_16x16x32_bf16 v[92:95], a[152:155], a[56:59], v[92:95]\n" \
"ds_read_b128 a[152:155], v48 offset:14336\n" \
"v_cvt_pk_bf16_f32 v73, v74, v75\n" \
"v_mfma_f32_16x16x32_bf16 v[92:95], a[156:159], a[60:63], v[92:95]\n" \
"ds_read_b128 a[156:159], v48 offset:15360\n" \
"v_add_u32_e32 v49, 0x10000, v48\n" \
"v_mfma_f32_16x16x32_bf16 v[88:91], a[140:143], a[60:63], v[44:47]\n" \
"ds_read_b128 a[140:143], v48 offset:11264\n" \
"s_xor_b32 s65, s65, 0x4000\n" \
"v_mfma_f32_16x16x32_bf16 v[88:91], a[136:139], a[56:59], v[88:91]\n" \
"v_exp_f32_e64 v67, v67\n" \
"v_mfma_f32_16x16x32_bf16 v[88:91], a[132:135], a[52:55], v[88:91]\n" \
"buffer_load_dwordx4 v53, s[8:11], s64 offen offset:0 sc1 lds\n" \
"v_mfma_f32_16x16x32_bf16 v[88:91], a[128:131], a[48:51], v[88:91]\n" \
"ds_read_b128 a[128:131], v48 offset:8192\n" \
"s_waitcnt lgkmcnt(6)\n" \
"v_mfma_f32_16x16x32_bf16 v[128:131], a[160:163], v[100:103], v[128:131]\n" \
"ds_read_b128 a[132:135], v48 offset:9216\n" \
"ds_read_b128 a[136:139], v48 offset:10240\n" \
"v_mfma_f32_16x16x32_bf16 v[160:163], a[160:163], v[104:107], v[160:163]\n" \
"v_exp_f32_e32 v64, v64\n" \
"v_mfma_f32_16x16x32_bf16 v[132:135], a[164:167], v[100:103], v[132:135]\n" \
"v_exp_f32_e64 v65, v65\n" \
"v_mfma_f32_16x16x32_bf16 v[164:167], a[164:167], v[104:107], v[164:167]\n" \
"v_exp_f32_e64 v76, v76\n" \
"v_mfma_f32_16x16x32_bf16 v[136:139], a[168:171], v[100:103], v[136:139]\n" \
"v_exp_f32_e64 v77, v77\n" \
"v_mfma_f32_16x16x32_bf16 v[168:171], a[168:171], v[104:107], v[168:171]\n" \
"v_exp_f32_e64 v78, v78\n" \
"v_mfma_f32_16x16x32_bf16 v[140:143], a[172:175], v[100:103], v[140:143]\n" \
"v_exp_f32_e64 v79, v79\n" \
"v_mfma_f32_16x16x32_bf16 v[172:175], a[172:175], v[104:107], v[172:175]\n" \
"v_cvt_pk_bf16_f32 v69, v66, v67\n" \
"v_cvt_pk_bf16_f32 v68, v64, v65\n" \
"v_mfma_f32_16x16x32_bf16 v[144:147], a[176:179], v[100:103], v[144:147]\n" \
"v_cvt_pk_bf16_f32 v74, v76, v77\n" \
"v_cvt_pk_bf16_f32 v75, v78, v79\n" \
"v_mfma_f32_16x16x32_bf16 v[176:179], a[176:179], v[104:107], v[176:179]\n" \
"v_xor_b32_e32 v48, 0x4000, v48\n" \
"v_mfma_f32_16x16x32_bf16 v[148:151], a[180:183], v[100:103], v[148:151]\n" \
"v_mfma_f32_16x16x32_bf16 v[180:183], a[180:183], v[104:107], v[180:183]\n" \
"v_mfma_f32_16x16x32_bf16 v[152:155], a[184:187], v[100:103], v[152:155]\n" \
"v_mfma_f32_16x16x32_bf16 v[184:187], a[184:187], v[104:107], v[184:187]\n" \
"buffer_load_dwordx4 v53, s[8:11], s64 offen offset:1024 sc1 lds\n" \
"v_mfma_f32_16x16x32_bf16 v[156:159], a[188:191], v[100:103], v[156:159]\n" \
"s_add_u32 s64, s64, 0x8000\n" \
"s_sub_u32 s66, s66, 1\n" \
"s_nop 0\n" \
"v_mfma_f32_16x16x32_bf16 v[188:191], a[188:191], v[104:107], v[188:191]\n" \
"v_smfmac_f32_16x16x64_bf16 a[192:195], a[200:203], v[100:107], v21\n" \
"v_mfma_f32_16x16x32_bf16 v[220:223], a[188:191], v[116:119], v[220:223]\n" \
"v_mfma_f32_16x16x32_bf16 v[252:255], a[188:191], v[120:123], v[252:255]\n" \
"v_mfma_f32_16x16x32_bf16 v[216:219], a[184:187], v[116:119], v[216:219]\n" \
"v_mfma_f32_16x16x32_bf16 v[248:251], a[184:187], v[120:123], v[248:251]\n" \
"v_mfma_f32_16x16x32_bf16 v[212:215], a[180:183], v[116:119], v[212:215]\n" \
"v_mfma_f32_16x16x32_bf16 v[244:247], a[180:183], v[120:123], v[244:247]\n" \
"v_mfma_f32_16x16x32_bf16 v[208:211], a[176:179], v[116:119], v[208:211]\n" \
"v_mfma_f32_16x16x32_bf16 v[240:243], a[176:179], v[120:123], v[240:243]\n" \
"v_mfma_f32_16x16x32_bf16 v[204:207], a[172:175], v[116:119], v[204:207]\n" \
"v_mfma_f32_16x16x32_bf16 v[236:239], a[172:175], v[120:123], v[236:239]\n" \
"v_mfma_f32_16x16x32_bf16 v[200:203], a[168:171], v[116:119], v[200:203]\n" \
"v_mfma_f32_16x16x32_bf16 v[232:235], a[168:171], v[120:123], v[232:235]\n" \
"v_mfma_f32_16x16x32_bf16 v[196:199], a[164:167], v[116:119], v[196:199]\n" \
"v_mfma_f32_16x16x32_bf16 v[228:231], a[164:167], v[120:123], v[228:231]\n" \
"v_mfma_f32_16x16x32_bf16 v[192:195], a[160:163], v[116:119], v[192:195]\n" \
"v_mfma_f32_16x16x32_bf16 v[224:227], a[160:163], v[120:123], v[224:227]\n" \
"v_smfmac_f32_16x16x64_bf16 a[196:199], a[200:203], v[116:123], v21\n" \
"s_waitcnt vmcnt(4) lgkmcnt(0)\n" \
"s_barrier\n" \
"s_cbranch_scc0 L_1_%=\n" \
"L_tr_%=:\n" \
"s_cmp_lg_u32 s16, 0\n" \
"s_cbranch_scc1 L_sw_%=\n" \
".fill (L_ab + 4096 - .) / 4, 4, 0xBF800000\n" \
"L_ap1:\n" \
"s_cbranch_execz L_ap0\n" \
"v_mfma_f32_16x16x32_bf16 v[96:99], a[128:131], a[0:3], v[32:35]\n" \
"s_xor_b32 m0, s65, 0x12000\n" \
"s_mov_b32 s65, s13\n" \
"v_mfma_f32_16x16x32_bf16 v[96:99], a[132:135], a[4:7], v[96:99]\n" \
"buffer_load_dwordx4 v50, s[8:11], s64 offen offset:0 sc1 lds\n" \
"v_mfma_f32_16x16x32_bf16 v[96:99], a[136:139], a[8:11], v[96:99]\n" \
"v_add_u32_e32 v63, s15, v20\n" \
"s_add_u32 s67, s14, 40960\n" \
"v_mfma_f32_16x16x32_bf16 v[96:99], a[140:143], a[12:15], v[96:99]\n" \
"ds_read_b128 a[160:163], v49 offset:0\n" \
"ds_read_b128 a[164:167], v49 offset:1024\n" \
"v_mfma_f32_16x16x32_bf16 v[100:103], a[156:159], a[12:15], v[32:35]\n" \
"buffer_load_dwordx4 v50, s[8:11], s64 offen offset:1024 sc1 lds\n" \
"v_mfma_f32_16x16x32_bf16 v[100:103], a[152:155], a[8:11], v[100:103]\n" \
"v_add_u32_e32 v50, 0x6000, v63\n" \
"s_xor_b32 m0, s65, 0x8000\n" \
"v_mfma_f32_16x16x32_bf16 v[100:103], a[148:151], a[4:7], v[100:103]\n" \
"ds_read_b128 a[168:171], v49 offset:2048\n" \
"ds_read_b128 a[172:175], v49 offset:3072\n" \
"v_mfma_f32_16x16x32_bf16 v[100:103], a[144:147], a[0:3], v[100:103]\n" \
"ds_read_b128 a[176:179], v49 offset:4096\n" \
"ds_read_b128 a[180:183], v49 offset:5120\n" \
"v_mfma_f32_16x16x32_bf16 v[108:111], a[144:147], a[16:19], v[36:39]\n" \
"buffer_load_dwordx4 v50, s[8:11], s67 offen offset:0 sc1 lds\n" \
"v_mfma_f32_16x16x32_bf16 v[108:111], a[148:151], a[20:23], v[108:111]\n" \
"ds_read_b128 a[184:187], v49 offset:6144\n" \
"ds_read_b128 a[188:191], v49 offset:7168\n" \
"v_mfma_f32_16x16x32_bf16 v[108:111], a[152:155], a[24:27], v[108:111]\n" \
"s_mov_b32 s64, s14\n" \
"v_add_u32_e32 v51, 0x12000, v63\n" \
"v_mfma_f32_16x16x32_bf16 v[108:111], a[156:159], a[28:31], v[108:111]\n" \
"v_exp_f32_e32 v86, v86\n" \
"v_mfma_f32_16x16x32_bf16 v[104:107], a[140:143], a[28:31], v[36:39]\n" \
"buffer_load_dwordx4 v50, s[8:11], s67 offen offset:1024 sc1 lds\n" \
"v_mfma_f32_16x16x32_bf16 v[104:107], a[136:139], a[24:27], v[104:107]\n" \
"v_exp_f32_e32 v87, v87\n" \
"v_mfma_f32_16x16x32_bf16 v[104:107], a[132:135], a[20:23], v[104:107]\n" \
"v_exp_f32_e32 v84, v84\n" \
"v_mfma_f32_16x16x32_bf16 v[104:107], a[128:131], a[16:19], v[104:107]\n" \
"v_exp_f32_e32 v85, v85\n" \
"v_mfma_f32_16x16x32_bf16 v[112:115], a[128:131], a[32:35], v[40:43]\n" \
"v_cvt_pk_bf16_f32 v87, v86, v87\n" \
"v_cvt_pk_bf16_f32 v86, v84, v85\n" \
"v_mfma_f32_16x16x32_bf16 v[112:115], a[132:135], a[36:39], v[112:115]\n" \
"buffer_load_dwordx4 v50, s[8:11], s67 offen offset:2048 sc1 lds\n" \
"v_mfma_f32_16x16x32_bf16 v[112:115], a[136:139], a[40:43], v[112:115]\n" \
"v_exp_f32_e32 v82, v82\n" \
"v_mfma_f32_16x16x32_bf16 v[112:115], a[140:143], a[44:47], v[112:115]\n" \
"v_exp_f32_e32 v83, v83\n" \
"v_mfma_f32_16x16x32_bf16 v[116:119], a[156:159], a[44:47], v[40:43]\n" \
"v_exp_f32_e32 v80, v80\n" \
"v_mfma_f32_16x16x32_bf16 v[116:119], a[152:155], a[40:43], v[116:119]\n" \
"buffer_load_dwordx4 v50, s[8:11], s67 offen offset:3072 sc1 lds\n" \
"v_mfma_f32_16x16x32_bf16 v[116:119], a[148:151], a[36:39], v[116:119]\n" \
"s_xor_b32 m0, s65, 0xa000\n" \
"v_cvt_pk_bf16_f32 v85, v82, v83\n" \
"v_mfma_f32_16x16x32_bf16 v[116:119], a[144:147], a[32:35], v[116:119]\n" \
"v_exp_f32_e32 v81, v81\n" \
"v_mfma_f32_16x16x32_bf16 v[124:127], a[144:147], a[48:51], v[44:47]\n" \
"v_cvt_pk_bf16_f32 v84, v80, v81\n" \
"v_readlane_b32 s93, v11, 19\n" \
"v_mfma_f32_16x16x32_bf16 v[124:127], a[148:151], a[52:55], v[124:127]\n" \
"buffer_load_dwordx4 v51, s[8:11], s64 offen offset:0 sc1 lds\n" \
"v_mfma_f32_16x16x32_bf16 v[124:127], a[152:155], a[56:59], v[124:127]\n" \
"v_exp_f32_e32 v88, v88\n" \
"v_mfma_f32_16x16x32_bf16 v[124:127], a[156:159], a[60:63], v[124:127]\n" \
"v_exp_f32_e32 v89, v89\n" \
"v_mfma_f32_16x16x32_bf16 v[120:123], a[140:143], a[60:63], v[44:47]\n" \
"v_exp_f32_e32 v90, v90\n" \
"v_mfma_f32_16x16x32_bf16 v[120:123], a[136:139], a[56:59], v[120:123]\n" \
"v_exp_f32_e32 v91, v91\n" \
"v_mfma_f32_16x16x32_bf16 v[120:123], a[132:135], a[52:55], v[120:123]\n" \
"buffer_load_dwordx4 v51, s[8:11], s64 offen offset:1024 sc1 lds\n" \
"v_mfma_f32_16x16x32_bf16 v[120:123], a[128:131], a[48:51], v[120:123]\n" \
"v_cvt_pk_bf16_f32 v88, v88, v89\n" \
"s_waitcnt lgkmcnt(0)\n" \
"v_mfma_f32_16x16x32_bf16 v[128:131], a[160:163], v[68:71], v[128:131]\n" \
"v_exp_f32_e32 v92, v92\n" \
"v_mfma_f32_16x16x32_bf16 v[160:163], a[160:163], v[72:75], v[160:163]\n" \
"v_exp_f32_e32 v93, v93\n" \
"v_mfma_f32_16x16x32_bf16 v[132:135], a[164:167], v[68:71], v[132:135]\n" \
"buffer_load_dwordx4 v51, s[8:11], s64 offen offset:2048 sc1 lds\n" \
"v_mfma_f32_16x16x32_bf16 v[164:167], a[164:167], v[72:75], v[164:167]\n" \
"v_exp_f32_e32 v94, v94\n" \
"v_mfma_f32_16x16x32_bf16 v[136:139], a[168:171], v[68:71], v[136:139]\n" \
"v_exp_f32_e32 v95, v95\n" \
"v_mfma_f32_16x16x32_bf16 v[168:171], a[168:171], v[72:75], v[168:171]\n" \
"v_cvt_pk_bf16_f32 v89, v90, v91\n" \
"v_cvt_pk_bf16_f32 v90, v92, v93\n" \
"v_mfma_f32_16x16x32_bf16 v[140:143], a[172:175], v[68:71], v[140:143]\n" \
"buffer_load_dwordx4 v51, s[8:11], s64 offen offset:3072 sc1 lds\n" \
"v_mfma_f32_16x16x32_bf16 v[172:175], a[172:175], v[72:75], v[172:175]\n" \
"v_cvt_pk_bf16_f32 v91, v94, v95\n" \
"v_readlane_b32 s80, v11, 11\n" \
"v_mfma_f32_16x16x32_bf16 v[144:147], a[176:179], v[68:71], v[144:147]\n" \
"v_readlane_b32 s81, v11, 12\n" \
"s_mov_b32 s82, 0x47fca0\n" \
"v_mfma_f32_16x16x32_bf16 v[176:179], a[176:179], v[72:75], v[176:179]\n" \
"s_mov_b32 s83, 0x20000\n" \
"v_readlane_b32 s92, v11, 25\n" \
"v_mfma_f32_16x16x32_bf16 v[148:151], a[180:183], v[68:71], v[148:151]\n" \
"s_add_u32 m0, s93, 0x0\n" \
"ds_read_b128 a[128:131], v48 offset:0\n" \
"v_mfma_f32_16x16x32_bf16 v[180:183], a[180:183], v[72:75], v[180:183]\n" \
"ds_read_b128 a[144:147], v48 offset:4096\n" \
"ds_read_b128 a[132:135], v48 offset:1024\n" \
"v_mfma_f32_16x16x32_bf16 v[152:155], a[184:187], v[68:71], v[152:155]\n" \
"buffer_load_dwordx4 v0, s[80:83], s92 offen offset:0 lds\n" \
"v_mfma_f32_16x16x32_bf16 v[184:187], a[184:187], v[72:75], v[184:187]\n" \
"ds_read_b128 a[148:151], v48 offset:5120\n" \
"ds_read_b128 a[136:139], v48 offset:2048\n" \
"v_mfma_f32_16x16x32_bf16 v[156:159], a[188:191], v[68:71], v[156:159]\n" \
"ds_read_b128 a[152:155], v48 offset:6144\n" \
"ds_read_b128 a[140:143], v48 offset:3072\n" \
"v_mfma_f32_16x16x32_bf16 v[188:191], a[188:191], v[72:75], v[188:191]\n" \
"ds_read_b128 a[156:159], v48 offset:7168\n" \
"v_mov_b32_e32 v56, s62\n" \
"v_smfmac_f32_16x16x64_bf16 a[192:195], a[200:203], v[68:75], v21\n" \
"buffer_load_dwordx4 v1, s[80:83], s92 offen offset:1024 lds\n" \
"v_mfma_f32_16x16x32_bf16 v[220:223], a[188:191], v[84:87], v[220:223]\n" \
"v_readlane_b32 s68, v11, 0\n" \
"v_add_u32_e32 v52, 0x14000, v63\n" \
"v_mfma_f32_16x16x32_bf16 v[252:255], a[188:191], v[88:91], v[252:255]\n" \
"v_add_u32_e32 v53, 0x20000, v63\n" \
"s_mov_b32 s66, s19\n" \
"v_mfma_f32_16x16x32_bf16 v[216:219], a[184:187], v[84:87], v[216:219]\n" \
"buffer_load_dwordx4 v2, s[80:83], s92 offen offset:2048 lds\n" \
"v_mfma_f32_16x16x32_bf16 v[248:251], a[184:187], v[88:91], v[248:251]\n" \
"v_exp_f32_e32 v102, v102\n" \
"v_mfma_f32_16x16x32_bf16 v[212:215], a[180:183], v[84:87], v[212:215]\n" \
"v_exp_f32_e32 v103, v103\n" \
"v_mfma_f32_16x16x32_bf16 v[244:247], a[180:183], v[88:91], v[244:247]\n" \
"v_exp_f32_e32 v100, v100\n" \
"v_mfma_f32_16x16x32_bf16 v[208:211], a[176:179], v[84:87], v[208:211]\n" \
"buffer_load_dwordx4 v3, s[80:83], s92 offen offset:3072 lds\n" \
"v_mfma_f32_16x16x32_bf16 v[240:243], a[176:179], v[88:91], v[240:243]\n" \
"v_exp_f32_e32 v101, v101\n" \
"v_mfma_f32_16x16x32_bf16 v[204:207], a[172:175], v[84:87], v[204:207]\n" \
"v_exp_f32_e32 v98, v98\n" \
"v_mfma_f32_16x16x32_bf16 v[236:239], a[172:175], v[88:91], v[236:239]\n" \
"v_exp_f32_e32 v99, v99\n" \
"v_mfma_f32_16x16x32_bf16 v[200:203], a[168:171], v[84:87], v[200:203]\n" \
"v_exp_f32_e32 v96, v96\n" \
"v_mfma_f32_16x16x32_bf16 v[232:235], a[168:171], v[88:91], v[232:235]\n" \
"v_exp_f32_e32 v97, v97\n" \
"v_mfma_f32_16x16x32_bf16 v[196:199], a[164:167], v[84:87], v[196:199]\n" \
"v_exp_f32_e32 v104, v104\n" \
"v_mfma_f32_16x16x32_bf16 v[228:231], a[164:167], v[88:91], v[228:231]\n" \
"v_exp_f32_e32 v105, v105\n" \
"v_mfma_f32_16x16x32_bf16 v[192:195], a[160:163], v[84:87], v[192:195]\n" \
"v_cvt_pk_bf16_f32 v103, v102, v103\n" \
"v_cvt_pk_bf16_f32 v102, v100, v101\n" \
"v_mfma_f32_16x16x32_bf16 v[224:227], a[160:163], v[88:91], v[224:227]\n" \
"v_cvt_pk_bf16_f32 v101, v98, v99\n" \
"v_cvt_pk_bf16_f32 v100, v96, v97\n" \
"v_smfmac_f32_16x16x64_bf16 a[196:199], a[200:203], v[84:91], v21\n" \
"v_exp_f32_e32 v106, v106\n" \
"s_waitcnt vmcnt(14) lgkmcnt(0)\n" \
"s_barrier\n" \
"v_mfma_f32_16x16x32_bf16 v[64:67], a[128:131], a[0:3], v[32:35]\n" \
"s_xor_b32 m0, s65, 0x18000\n" \
"ds_read_b128 a[160:163], v49 offset:8192\n" \
"v_mfma_f32_16x16x32_bf16 v[64:67], a[132:135], a[4:7], v[64:67]\n" \
"buffer_load_dwordx4 v52, s[8:11], s64 offen offset:0 sc1 lds\n" \
"v_mfma_f32_16x16x32_bf16 v[64:67], a[136:139], a[8:11], v[64:67]\n" \
"ds_read_b128 a[164:167], v49 offset:9216\n" \
"ds_read_b128 a[168:171], v49 offset:10240\n" \
"v_mfma_f32_16x16x32_bf16 v[64:67], a[140:143], a[12:15], v[64:67]\n" \
"ds_read_b128 a[172:175], v49 offset:11264\n" \
"ds_read_b128 a[176:179], v49 offset:12288\n" \
"v_mfma_f32_16x16x32_bf16 v[68:71], a[156:159], a[12:15], v[32:35]\n" \
"buffer_load_dwordx4 v52, s[8:11], s64 offen offset:1024 sc1 lds\n" \
"s_mov_b64 exec, 1\n" \
"buffer_store_dword v56, off, s[56:59], s68 offset:0 sc1\n" \
"s_mov_b64 exec, -1\n" \
"v_mfma_f32_16x16x32_bf16 v[68:71], a[152:155], a[8:11], v[68:71]\n" \
"ds_read_b128 a[180:183], v49 offset:13312\n" \
"ds_read_b128 a[184:187], v49 offset:14336\n" \
"v_mfma_f32_16x16x32_bf16 v[68:71], a[148:151], a[4:7], v[68:71]\n" \
"v_exp_f32_e32 v107, v107\n" \
"v_mfma_f32_16x16x32_bf16 v[68:71], a[144:147], a[0:3], v[68:71]\n" \
"v_cvt_pk_bf16_f32 v104, v104, v105\n" \
"v_cvt_pk_bf16_f32 v105, v106, v107\n" \
"v_mfma_f32_16x16x32_bf16 v[76:79], a[144:147], a[16:19], v[36:39]\n" \
"v_exp_f32_e32 v108, v108\n" \
"v_mfma_f32_16x16x32_bf16 v[76:79], a[148:151], a[20:23], v[76:79]\n" \
"buffer_load_dwordx4 v52, s[8:11], s64 offen offset:2048 sc1 lds\n" \
"v_mfma_f32_16x16x32_bf16 v[76:79], a[152:155], a[24:27], v[76:79]\n" \
"v_exp_f32_e32 v109, v109\n" \
"v_mfma_f32_16x16x32_bf16 v[76:79], a[156:159], a[28:31], v[76:79]\n" \
"v_exp_f32_e32 v110, v110\n" \
"v_mfma_f32_16x16x32_bf16 v[72:75], a[140:143], a[28:31], v[36:39]\n" \
"v_exp_f32_e32 v111, v111\n" \
"v_mfma_f32_16x16x32_bf16 v[72:75], a[136:139], a[24:27], v[72:75]\n" \
"buffer_load_dwordx4 v52, s[8:11], s64 offen offset:3072 sc1 lds\n" \
"v_mfma_f32_16x16x32_bf16 v[72:75], a[132:135], a[20:23], v[72:75]\n" \
"s_mov_b32 m0, s65\n" \
"v_cvt_pk_bf16_f32 v106, v108, v109\n" \
"v_mfma_f32_16x16x32_bf16 v[72:75], a[128:131], a[16:19], v[72:75]\n" \
"v_cvt_pk_bf16_f32 v107, v110, v111\n" \
"ds_read_b128 a[188:191], v49 offset:15360\n" \
"v_mfma_f32_16x16x32_bf16 v[80:83], a[128:131], a[32:35], v[40:43]\n" \
"v_exp_f32_e32 v118, v118\n" \
"v_mfma_f32_16x16x32_bf16 v[80:83], a[132:135], a[36:39], v[80:83]\n" \
"v_exp_f32_e32 v119, v119\n" \
"v_mfma_f32_16x16x32_bf16 v[80:83], a[136:139], a[40:43], v[80:83]\n" \
"buffer_load_dwordx4 v53, s[8:11], s64 offen offset:0 sc1 lds\n" \
"v_mfma_f32_16x16x32_bf16 v[80:83], a[140:143], a[44:47], v[80:83]\n" \
"v_exp_f32_e32 v116, v116\n" \
"v_mfma_f32_16x16x32_bf16 v[84:87], a[156:159], a[44:47], v[40:43]\n" \
"v_exp_f32_e32 v117, v117\n" \
"v_mfma_f32_16x16x32_bf16 v[84:87], a[152:155], a[40:43], v[84:87]\n" \
"v_cvt_pk_bf16_f32 v119, v118, v119\n" \
"v_cvt_pk_bf16_f32 v118, v116, v117\n" \
"v_mfma_f32_16x16x32_bf16 v[84:87], a[148:151], a[36:39], v[84:87]\n" \
"v_exp_f32_e32 v114, v114\n" \
"v_mfma_f32_16x16x32_bf16 v[84:87], a[144:147], a[32:35], v[84:87]\n" \
"buffer_load_dwordx4 v53, s[8:11], s64 offen offset:1024 sc1 lds\n" \
"v_mfma_f32_16x16x32_bf16 v[92:95], a[144:147], a[48:51], v[44:47]\n" \
"v_exp_f32_e32 v115, v115\n" \
"v_mfma_f32_16x16x32_bf16 v[92:95], a[148:151], a[52:55], v[92:95]\n" \
"v_exp_f32_e32 v112, v112\n" \
"v_mfma_f32_16x16x32_bf16 v[92:95], a[152:155], a[56:59], v[92:95]\n" \
"v_exp_f32_e32 v113, v113\n" \
"v_mfma_f32_16x16x32_bf16 v[92:95], a[156:159], a[60:63], v[92:95]\n" \
"buffer_load_dwordx4 v53, s[8:11], s64 offen offset:2048 sc1 lds\n" \
"v_mfma_f32_16x16x32_bf16 v[88:91], a[140:143], a[60:63], v[44:47]\n" \
"v_cvt_pk_bf16_f32 v117, v114, v115\n" \
"v_cvt_pk_bf16_f32 v116, v112, v113\n" \
"v_mfma_f32_16x16x32_bf16 v[88:91], a[136:139], a[56:59], v[88:91]\n" \
"v_exp_f32_e32 v120, v120\n" \
"v_mfma_f32_16x16x32_bf16 v[88:91], a[132:135], a[52:55], v[88:91]\n" \
"v_exp_f32_e32 v121, v121\n" \
"v_mfma_f32_16x16x32_bf16 v[88:91], a[128:131], a[48:51], v[88:91]\n" \
"v_cvt_pk_bf16_f32 v120, v120, v121\n" \
"s_waitcnt lgkmcnt(0)\n" \
"v_mfma_f32_16x16x32_bf16 v[128:131], a[160:163], v[100:103], v[128:131]\n" \
"buffer_load_dwordx4 v53, s[8:11], s64 offen offset:3072 sc1 lds\n" \
"v_mfma_f32_16x16x32_bf16 v[160:163], a[160:163], v[104:107], v[160:163]\n" \
"s_add_u32 m0, s93, 0x1000\n" \
"ds_read_b128 a[128:131], v48 offset:8192\n" \
"v_mfma_f32_16x16x32_bf16 v[132:135], a[164:167], v[100:103], v[132:135]\n" \
"v_exp_f32_e32 v122, v122\n" \
"v_mfma_f32_16x16x32_bf16 v[164:167], a[164:167], v[104:107], v[164:167]\n" \
"v_exp_f32_e32 v123, v123\n" \
"v_mfma_f32_16x16x32_bf16 v[136:139], a[168:171], v[100:103], v[136:139]\n" \
"buffer_load_dwordx4 v4, s[80:83], s92 offen offset:0 lds\n" \
"v_mfma_f32_16x16x32_bf16 v[168:171], a[168:171], v[104:107], v[168:171]\n" \
"v_exp_f32_e32 v124, v124\n" \
"v_mfma_f32_16x16x32_bf16 v[140:143], a[172:175], v[100:103], v[140:143]\n" \
"v_exp_f32_e32 v125, v125\n" \
"v_mfma_f32_16x16x32_bf16 v[172:175], a[172:175], v[104:107], v[172:175]\n" \
"v_exp_f32_e32 v126, v126\n" \
"v_mfma_f32_16x16x32_bf16 v[144:147], a[176:179], v[100:103], v[144:147]\n" \
"v_exp_f32_e32 v127, v127\n" \
"v_mfma_f32_16x16x32_bf16 v[176:179], a[176:179], v[104:107], v[176:179]\n" \
"v_cvt_pk_bf16_f32 v121, v122, v123\n" \
"v_cvt_pk_bf16_f32 v122, v124, v125\n" \
"v_mfma_f32_16x16x32_bf16 v[148:151], a[180:183], v[100:103], v[148:151]\n" \
"buffer_load_dwordx4 v5, s[80:83], s92 offen offset:1024 lds\n" \
"v_mfma_f32_16x16x32_bf16 v[180:183], a[180:183], v[104:107], v[180:183]\n" \
"v_cvt_pk_bf16_f32 v123, v126, v127\n" \
"ds_read_b128 a[144:147], v48 offset:12288\n" \
"v_mfma_f32_16x16x32_bf16 v[152:155], a[184:187], v[100:103], v[152:155]\n" \
"ds_read_b128 a[132:135], v48 offset:9216\n" \
"ds_read_b128 a[148:151], v48 offset:13312\n" \
"v_mfma_f32_16x16x32_bf16 v[184:187], a[184:187], v[104:107], v[184:187]\n" \
"ds_read_b128 a[136:139], v48 offset:10240\n" \
"ds_read_b128 a[152:155], v48 offset:14336\n" \
"v_mfma_f32_16x16x32_bf16 v[156:159], a[188:191], v[100:103], v[156:159]\n" \
"ds_read_b128 a[140:143], v48 offset:11264\n" \
"ds_read_b128 a[156:159], v48 offset:15360\n" \
"v_mfma_f32_16x16x32_bf16 v[188:191], a[188:191], v[104:107], v[188:191]\n" \
"buffer_load_dwordx4 v6, s[80:83], s92 offen offset:2048 lds\n" \
"v_smfmac_f32_16x16x64_bf16 a[192:195], a[200:203], v[100:107], v21\n" \
"v_exp_f32_e32 v70, v70\n" \
"v_mfma_f32_16x16x32_bf16 v[220:223], a[188:191], v[116:119], v[220:223]\n" \
"v_exp_f32_e32 v71, v71\n" \
"v_mfma_f32_16x16x32_bf16 v[252:255], a[188:191], v[120:123], v[252:255]\n" \
"buffer_load_dwordx4 v7, s[80:83], s92 offen offset:3072 lds\n" \
"v_mfma_f32_16x16x32_bf16 v[216:219], a[184:187], v[116:119], v[216:219]\n" \
"s_add_u32 m0, s93, 0x2000\n" \
"v_cvt_pk_bf16_f32 v71, v70, v71\n" \
"v_mfma_f32_16x16x32_bf16 v[248:251], a[184:187], v[120:123], v[248:251]\n" \
"v_exp_f32_e32 v72, v72\n" \
"v_mfma_f32_16x16x32_bf16 v[212:215], a[180:183], v[116:119], v[212:215]\n" \
"v_exp_f32_e32 v73, v73\n" \
"v_mfma_f32_16x16x32_bf16 v[244:247], a[180:183], v[120:123], v[244:247]\n" \
"v_exp_f32_e32 v68, v68\n" \
"v_mfma_f32_16x16x32_bf16 v[208:211], a[176:179], v[116:119], v[208:211]\n" \
"buffer_load_dwordx4 v8, s[80:83], s92 offen offset:0 lds\n" \
"v_mfma_f32_16x16x32_bf16 v[240:243], a[176:179], v[120:123], v[240:243]\n" \
"v_exp_f32_e32 v69, v69\n" \
"v_mfma_f32_16x16x32_bf16 v[204:207], a[172:175], v[116:119], v[204:207]\n" \
"v_exp_f32_e32 v74, v74\n" \
"v_mfma_f32_16x16x32_bf16 v[236:239], a[172:175], v[120:123], v[236:239]\n" \
"v_exp_f32_e32 v75, v75\n" \
"v_mfma_f32_16x16x32_bf16 v[200:203], a[168:171], v[116:119], v[200:203]\n" \
"v_exp_f32_e32 v66, v66\n" \
"v_mfma_f32_16x16x32_bf16 v[232:235], a[168:171], v[120:123], v[232:235]\n" \
"v_exp_f32_e32 v67, v67\n" \
"v_mfma_f32_16x16x32_bf16 v[196:199], a[164:167], v[116:119], v[196:199]\n" \
"v_exp_f32_e32 v64, v64\n" \
"v_mfma_f32_16x16x32_bf16 v[228:231], a[164:167], v[120:123], v[228:231]\n" \
"v_exp_f32_e32 v65, v65\n" \
"v_mfma_f32_16x16x32_bf16 v[192:195], a[160:163], v[116:119], v[192:195]\n" \
"v_cvt_pk_bf16_f32 v72, v72, v73\n" \
"v_cvt_pk_bf16_f32 v70, v68, v69\n" \
"v_mfma_f32_16x16x32_bf16 v[224:227], a[160:163], v[120:123], v[224:227]\n" \
"v_exp_f32_e32 v76, v76\n" \
"v_smfmac_f32_16x16x64_bf16 a[196:199], a[200:203], v[116:123], v21\n" \
"v_exp_f32_e32 v77, v77\n" \
"s_waitcnt vmcnt(14) lgkmcnt(0)\n" \
"s_barrier\n" \
"v_exp_f32_e32 v78, v78\n" \
"v_exp_f32_e32 v79, v79\n" \
"v_cvt_pk_bf16_f32 v73, v74, v75\n" \
"v_add_u32_e32 v49, 0x10000, v48\n" \
"v_cvt_pk_bf16_f32 v69, v66, v67\n" \
"v_cvt_pk_bf16_f32 v68, v64, v65\n" \
"v_cvt_pk_bf16_f32 v74, v76, v77\n" \
"v_cvt_pk_bf16_f32 v75, v78, v79\n" \
"v_add_u32_e32 v48, s16, v20\n" \
"s_xor_b32 s65, s65, 0x8000\n" \
"s_add_u32 s64, s64, 0x10000\n" \
"s_getpc_b64 s[86:87]\n" \
"L_hpt_%=:\n" \
"s_add_u32 s86, s86, L_ap5-L_hpt_%=\n" \
"s_addc_u32 s87, s87, 0\n" \
"s_add_u32 s86, s86, s15\n" \
"s_addc_u32 s87, s87, 0\n" \
"s_mov_b64 exec, 0\n" \
"s_swappc_b64 s[88:89], s[86:87]\n" \
"s_mov_b64 exec, -1\n" \
"s_add_u32 s86, s86, 0x2000\n" \
"s_addc_u32 s87, s87, 0\n" \
"s_mov_b64 exec, 0\n" \
"s_swappc_b64 s[88:89], s[86:87]\n" \
"s_mov_b64 exec, -1\n" \
".fill (L_ab + 6464 - .) / 4, 4, 0xBF800000\n" \
"L_2_%=:\n" \
"v_mfma_f32_16x16x32_bf16 v[96:99], a[128:131], a[64:67], v[32:35]\n" \
"s_xor_b32 m0, s65, 0x12000\n" \
"ds_read_b128 a[160:163], v49 offset:0\n" \
"v_mfma_f32_16x16x32_bf16 v[96:99], a[132:135], a[68:71], v[96:99]\n" \
"buffer_load_dwordx4 v50, s[8:11], s64 offen offset:0 sc1 lds\n" \
"v_mfma_f32_16x16x32_bf16 v[96:99], a[136:139], a[72:75], v[96:99]\n" \
"ds_read_b128 a[164:167], v49 offset:1024\n" \
"ds_read_b128 a[168:171], v49 offset:2048\n" \
"v_mfma_f32_16x16x32_bf16 v[96:99], a[140:143], a[76:79], v[96:99]\n" \
"ds_read_b128 a[172:175], v49 offset:3072\n" \
"ds_read_b128 a[176:179], v49 offset:4096\n" \
"v_mfma_f32_16x16x32_bf16 v[100:103], a[156:159], a[76:79], v[32:35]\n" \
"ds_read_b128 a[180:183], v49 offset:5120\n" \
"ds_read_b128 a[184:187], v49 offset:6144\n" \
"v_mfma_f32_16x16x32_bf16 v[100:103], a[152:155], a[72:75], v[100:103]\n" \
"v_exp_f32_e64 v86, v86\n" \
"v_mfma_f32_16x16x32_bf16 v[100:103], a[148:151], a[68:71], v[100:103]\n" \
"v_exp_f32_e64 v87, v87\n" \
"v_mfma_f32_16x16x32_bf16 v[100:103], a[144:147], a[64:67], v[100:103]\n" \
"buffer_load_dwordx4 v50, s[8:11], s64 offen offset:1024 sc1 lds\n" \
"v_mfma_f32_16x16x32_bf16 v[108:111], a[144:147], a[80:83], v[36:39]\n" \
"ds_read_b128 a[188:191], v49 offset:7168\n" \
"v_cvt_pk_bf16_f32 v87, v86, v87\n" \
"v_mfma_f32_16x16x32_bf16 v[108:111], a[148:151], a[84:87], v[108:111]\n" \
"v_exp_f32_e64 v84, v84\n" \
"v_mfma_f32_16x16x32_bf16 v[108:111], a[152:155], a[88:91], v[108:111]\n" \
"v_exp_f32_e64 v85, v85\n" \
"v_mfma_f32_16x16x32_bf16 v[108:111], a[156:159], a[92:95], v[108:111]\n" \
"v_exp_f32_e64 v82, v82\n" \
"v_mfma_f32_16x16x32_bf16 v[104:107], a[140:143], a[92:95], v[36:39]\n" \
"v_exp_f32_e64 v83, v83\n" \
"v_mfma_f32_16x16x32_bf16 v[104:107], a[136:139], a[88:91], v[104:107]\n" \
"v_exp_f32_e64 v80, v80\n" \
"v_mfma_f32_16x16x32_bf16 v[104:107], a[132:135], a[84:87], v[104:107]\n" \
"v_exp_f32_e64 v81, v81\n" \
"v_mfma_f32_16x16x32_bf16 v[104:107], a[128:131], a[80:83], v[104:107]\n" \
"buffer_load_dwordx4 v50, s[8:11], s64 offen offset:2048 sc1 lds\n" \
"v_mfma_f32_16x16x32_bf16 v[112:115], a[128:131], a[96:99], v[40:43]\n" \
"v_exp_f32_e64 v88, v88\n" \
"v_mfma_f32_16x16x32_bf16 v[112:115], a[132:135], a[100:103], v[112:115]\n" \
"v_exp_f32_e64 v89, v89\n" \
"v_mfma_f32_16x16x32_bf16 v[112:115], a[136:139], a[104:107], v[112:115]\n" \
"v_cvt_pk_bf16_f32 v86, v84, v85\n" \
"v_cvt_pk_bf16_f32 v85, v82, v83\n" \
"v_mfma_f32_16x16x32_bf16 v[112:115], a[140:143], a[108:111], v[112:115]\n" \
"v_cvt_pk_bf16_f32 v84, v80, v81\n" \
"v_cvt_pk_bf16_f32 v88, v88, v89\n" \
"v_mfma_f32_16x16x32_bf16 v[116:119], a[156:159], a[108:111], v[40:43]\n" \
"v_exp_f32_e64 v90, v90\n" \
"v_mfma_f32_16x16x32_bf16 v[116:119], a[152:155], a[104:107], v[116:119]\n" \
"v_exp_f32_e64 v91, v91\n" \
"v_mfma_f32_16x16x32_bf16 v[116:119], a[148:151], a[100:103], v[116:119]\n" \
"buffer_load_dwordx4 v50, s[8:11], s64 offen offset:3072 sc1 lds\n" \
"v_mfma_f32_16x16x32_bf16 v[116:119], a[144:147], a[96:99], v[116:119]\n" \
"s_xor_b32 m0, s65, 0xa000\n" \
"v_cvt_pk_bf16_f32 v89, v90, v91\n" \
"v_mfma_f32_16x16x32_bf16 v[124:127], a[144:147], a[112:115], v[44:47]\n" \
"v_exp_f32_e64 v92, v92\n" \
"v_mfma_f32_16x16x32_bf16 v[124:127], a[148:151], a[116:119], v[124:127]\n" \
"v_exp_f32_e64 v93, v93\n" \
"v_mfma_f32_16x16x32_bf16 v[124:127], a[152:155], a[120:123], v[124:127]\n" \
"v_exp_f32_e64 v94, v94\n" \
"v_mfma_f32_16x16x32_bf16 v[124:127], a[156:159], a[124:127], v[124:127]\n" \
"v_exp_f32_e64 v95, v95\n" \
"v_mfma_f32_16x16x32_bf16 v[120:123], a[140:143], a[124:127], v[44:47]\n" \
"v_cvt_pk_bf16_f32 v90, v92, v93\n" \
"v_cvt_pk_bf16_f32 v91, v94, v95\n" \
"v_mfma_f32_16x16x32_bf16 v[120:123], a[136:139], a[120:123], v[120:123]\n" \
"ds_read_b128 a[144:147], v48 offset:4096\n" \
"ds_read_b128 a[148:151], v48 offset:5120\n" \
"v_mfma_f32_16x16x32_bf16 v[120:123], a[132:135], a[116:119], v[120:123]\n" \
"buffer_load_dwordx4 v51, s[8:11], s64 offen offset:0 sc1 lds\n" \
"v_mfma_f32_16x16x32_bf16 v[120:123], a[128:131], a[112:115], v[120:123]\n" \
"ds_read_b128 a[128:131], v48 offset:0\n" \
"s_waitcnt lgkmcnt(3)\n" \
"v_mfma_f32_16x16x32_bf16 v[128:131], a[160:163], v[68:71], v[128:131]\n" \
"ds_read_b128 a[132:135], v48 offset:1024\n" \
"ds_read_b128 a[136:139], v48 offset:2048\n" \
"v_mfma_f32_16x16x32_bf16 v[160:163], a[160:163], v[72:75], v[160:163]\n" \
"ds_read_b128 a[152:155], v48 offset:6144\n" \
"ds_read_b128 a[140:143], v48 offset:3072\n" \
"v_mfma_f32_16x16x32_bf16 v[132:135], a[164:167], v[68:71], v[132:135]\n" \
"v_exp_f32_e32 v102, v102\n" \
"v_mfma_f32_16x16x32_bf16 v[164:167], a[164:167], v[72:75], v[164:167]\n" \
"v_exp_f32_e64 v103, v103\n" \
"v_mfma_f32_16x16x32_bf16 v[136:139], a[168:171], v[68:71], v[136:139]\n" \
"ds_read_b128 a[156:159], v48 offset:7168\n" \
"v_cvt_pk_bf16_f32 v103, v102, v103\n" \
"v_mfma_f32_16x16x32_bf16 v[168:171], a[168:171], v[72:75], v[168:171]\n" \
"buffer_load_dwordx4 v51, s[8:11], s64 offen offset:1024 sc1 lds\n" \
"v_mfma_f32_16x16x32_bf16 v[140:143], a[172:175], v[68:71], v[140:143]\n" \
"v_exp_f32_e64 v100, v100\n" \
"v_mfma_f32_16x16x32_bf16 v[172:175], a[172:175], v[72:75], v[172:175]\n" \
"v_exp_f32_e64 v101, v101\n" \
"v_mfma_f32_16x16x32_bf16 v[144:147], a[176:179], v[68:71], v[144:147]\n" \
"v_exp_f32_e64 v98, v98\n" \
"v_mfma_f32_16x16x32_bf16 v[176:179], a[176:179], v[72:75], v[176:179]\n" \
"v_exp_f32_e64 v99, v99\n" \
"v_mfma_f32_16x16x32_bf16 v[148:151], a[180:183], v[68:71], v[148:151]\n" \
"v_exp_f32_e64 v96, v96\n" \
"v_mfma_f32_16x16x32_bf16 v[180:183], a[180:183], v[72:75], v[180:183]\n" \
"v_exp_f32_e64 v97, v97\n" \
"v_mfma_f32_16x16x32_bf16 v[152:155], a[184:187], v[68:71], v[152:155]\n" \
"v_exp_f32_e64 v104, v104\n" \
"v_mfma_f32_16x16x32_bf16 v[184:187], a[184:187], v[72:75], v[184:187]\n" \
"buffer_load_dwordx4 v51, s[8:11], s64 offen offset:2048 sc1 lds\n" \
"v_mfma_f32_16x16x32_bf16 v[156:159], a[188:191], v[68:71], v[156:159]\n" \
"v_exp_f32_e64 v105, v105\n" \
"v_mfma_f32_16x16x32_bf16 v[188:191], a[188:191], v[72:75], v[188:191]\n" \
"v_cvt_pk_bf16_f32 v102, v100, v101\n" \
"v_cvt_pk_bf16_f32 v101, v98, v99\n" \
"v_smfmac_f32_16x16x64_bf16 a[192:195], a[200:203], v[68:75], v21\n" \
"v_cvt_pk_bf16_f32 v100, v96, v97\n" \
"v_cvt_pk_bf16_f32 v104, v104, v105\n" \
"v_mfma_f32_16x16x32_bf16 v[220:223], a[188:191], v[84:87], v[220:223]\n" \
"v_exp_f32_e64 v106, v106\n" \
"v_mfma_f32_16x16x32_bf16 v[252:255], a[188:191], v[88:91], v[252:255]\n" \
"v_exp_f32_e64 v107, v107\n" \
"v_mfma_f32_16x16x32_bf16 v[216:219], a[184:187], v[84:87], v[216:219]\n" \
"v_exp_f32_e64 v108, v108\n" \
"v_mfma_f32_16x16x32_bf16 v[248:251], a[184:187], v[88:91], v[248:251]\n" \
"buffer_load_dwordx4 v51, s[8:11], s64 offen offset:3072 sc1 lds\n" \
"v_mfma_f32_16x16x32_bf16 v[212:215], a[180:183], v[84:87], v[212:215]\n" \
"v_exp_f32_e64 v109, v109\n" \
"v_mfma_f32_16x16x32_bf16 v[244:247], a[180:183], v[88:91], v[244:247]\n" \
"v_exp_f32_e64 v110, v110\n" \
"v_mfma_f32_16x16x32_bf16 v[208:211], a[176:179], v[84:87], v[208:211]\n" \
"v_exp_f32_e64 v111, v111\n" \
"v_mfma_f32_16x16x32_bf16 v[240:243], a[176:179], v[88:91], v[240:243]\n" \
"v_exp_f32_e64 v118, v118\n" \
"v_mfma_f32_16x16x32_bf16 v[204:207], a[172:175], v[84:87], v[204:207]\n" \
"v_exp_f32_e64 v119, v119\n" \
"v_mfma_f32_16x16x32_bf16 v[236:239], a[172:175], v[88:91], v[236:239]\n" \
"v_cvt_pk_bf16_f32 v105, v106, v107\n" \
"v_cvt_pk_bf16_f32 v106, v108, v109\n" \
"v_mfma_f32_16x16x32_bf16 v[200:203], a[168:171], v[84:87], v[200:203]\n" \
"v_cvt_pk_bf16_f32 v107, v110, v111\n" \
"v_cvt_pk_bf16_f32 v119, v118, v119\n" \
"v_mfma_f32_16x16x32_bf16 v[232:235], a[168:171], v[88:91], v[232:235]\n" \
"v_exp_f32_e64 v116, v116\n" \
"v_mfma_f32_16x16x32_bf16 v[196:199], a[164:167], v[84:87], v[196:199]\n" \
"v_exp_f32_e64 v117, v117\n" \
"v_mfma_f32_16x16x32_bf16 v[228:231], a[164:167], v[88:91], v[228:231]\n" \
"v_exp_f32_e64 v114, v114\n" \
"v_mfma_f32_16x16x32_bf16 v[192:195], a[160:163], v[84:87], v[192:195]\n" \
"v_exp_f32_e64 v115, v115\n" \
"v_mfma_f32_16x16x32_bf16 v[224:227], a[160:163], v[88:91], v[224:227]\n" \
"v_exp_f32_e64 v112, v112\n" \
"v_smfmac_f32_16x16x64_bf16 a[196:199], a[200:203], v[84:91], v21\n" \
"v_exp_f32_e32 v113, v113\n" \
"s_waitcnt vmcnt(8) lgkmcnt(0)\n" \
"s_barrier\n" \
"s_nop 0\n" \
"v_mfma_f32_16x16x32_bf16 v[64:67], a[128:131], a[64:67], v[32:35]\n" \
"s_xor_b32 m0, s65, 0x18000\n" \
"ds_read_b128 a[160:163], v49 offset:8192\n" \
"v_mfma_f32_16x16x32_bf16 v[64:67], a[132:135], a[68:71], v[64:67]\n" \
"buffer_load_dwordx4 v52, s[8:11], s64 offen offset:0 sc1 lds\n" \
"v_mfma_f32_16x16x32_bf16 v[64:67], a[136:139], a[72:75], v[64:67]\n" \
"ds_read_b128 a[164:167], v49 offset:9216\n" \
"ds_read_b128 a[168:171], v49 offset:10240\n" \
"v_mfma_f32_16x16x32_bf16 v[64:67], a[140:143], a[76:79], v[64:67]\n" \
"ds_read_b128 a[172:175], v49 offset:11264\n" \
"ds_read_b128 a[176:179], v49 offset:12288\n" \
"v_mfma_f32_16x16x32_bf16 v[68:71], a[156:159], a[76:79], v[32:35]\n" \
"ds_read_b128 a[180:183], v49 offset:13312\n" \
"ds_read_b128 a[184:187], v49 offset:14336\n" \
"v_mfma_f32_16x16x32_bf16 v[68:71], a[152:155], a[72:75], v[68:71]\n" \
"ds_read_b128 a[188:191], v49 offset:15360\n" \
"v_cvt_pk_bf16_f32 v118, v116, v117\n" \
"v_mfma_f32_16x16x32_bf16 v[68:71], a[148:151], a[68:71], v[68:71]\n" \
"v_cvt_pk_bf16_f32 v117, v114, v115\n" \
"v_cvt_pk_bf16_f32 v116, v112, v113\n" \
"v_mfma_f32_16x16x32_bf16 v[68:71], a[144:147], a[64:67], v[68:71]\n" \
"buffer_load_dwordx4 v52, s[8:11], s64 offen offset:1024 sc1 lds\n" \
"v_mfma_f32_16x16x32_bf16 v[76:79], a[144:147], a[80:83], v[36:39]\n" \
"v_exp_f32_e64 v120, v120\n" \
"v_mfma_f32_16x16x32_bf16 v[76:79], a[148:151], a[84:87], v[76:79]\n" \
"v_exp_f32_e64 v121, v121\n" \
"v_mfma_f32_16x16x32_bf16 v[76:79], a[152:155], a[88:91], v[76:79]\n" \
"v_exp_f32_e64 v122, v122\n" \
"v_mfma_f32_16x16x32_bf16 v[76:79], a[156:159], a[92:95], v[76:79]\n" \
"v_exp_f32_e64 v123, v123\n" \
"v_mfma_f32_16x16x32_bf16 v[72:75], a[140:143], a[92:95], v[36:39]\n" \
"v_cvt_pk_bf16_f32 v120, v120, v121\n" \
"v_cvt_pk_bf16_f32 v121, v122, v123\n" \
"v_mfma_f32_16x16x32_bf16 v[72:75], a[136:139], a[88:91], v[72:75]\n" \
"v_exp_f32_e64 v124, v124\n" \
"v_mfma_f32_16x16x32_bf16 v[72:75], a[132:135], a[84:87], v[72:75]\n" \
"v_exp_f32_e64 v125, v125\n" \
"v_mfma_f32_16x16x32_bf16 v[72:75], a[128:131], a[80:83], v[72:75]\n" \
"buffer_load_dwordx4 v52, s[8:11], s64 offen offset:2048 sc1 lds\n" \
"v_mfma_f32_16x16x32_bf16 v[80:83], a[128:131], a[96:99], v[40:43]\n" \
"v_exp_f32_e64 v126, v126\n" \
"v_mfma_f32_16x16x32_bf16 v[80:83], a[132:135], a[100:103], v[80:83]\n" \
"v_exp_f32_e64 v127, v127\n" \
"v_mfma_f32_16x16x32_bf16 v[80:83], a[136:139], a[104:107], v[80:83]\n" \
"v_cvt_pk_bf16_f32 v122, v124, v125\n" \
"v_cvt_pk_bf16_f32 v123, v126, v127\n" \
"v_mfma_f32_16x16x32_bf16 v[80:83], a[140:143], a[108:111], v[80:83]\n" \
"v_exp_f32_e64 v70, v70\n" \
"v_mfma_f32_16x16x32_bf16 v[84:87], a[156:159], a[108:111], v[40:43]\n" \
"v_exp_f32_e64 v71, v71\n" \
"v_mfma_f32_16x16x32_bf16 v[84:87], a[152:155], a[104:107], v[84:87]\n" \
"v_exp_f32_e64 v72, v72\n" \
"v_mfma_f32_16x16x32_bf16 v[84:87], a[148:151], a[100:103], v[84:87]\n" \
"buffer_load_dwordx4 v52, s[8:11], s64 offen offset:3072 sc1 lds\n" \
"v_mfma_f32_16x16x32_bf16 v[84:87], a[144:147], a[96:99], v[84:87]\n" \
"s_mov_b32 m0, s65\n" \
"v_cvt_pk_bf16_f32 v71, v70, v71\n" \
"v_mfma_f32_16x16x32_bf16 v[92:95], a[144:147], a[112:115], v[44:47]\n" \
"ds_read_b128 a[144:147], v48 offset:12288\n" \
"v_add_u32_e32 v49, 0x10000, v48\n" \
"v_mfma_f32_16x16x32_bf16 v[92:95], a[148:151], a[116:119], v[92:95]\n" \
"ds_read_b128 a[148:151], v48 offset:13312\n" \
"s_xor_b32 s65, s65, 0x8000\n" \
"v_mfma_f32_16x16x32_bf16 v[92:95], a[152:155], a[120:123], v[92:95]\n" \
"v_exp_f32_e32 v73, v73\n" \
"v_mfma_f32_16x16x32_bf16 v[92:95], a[156:159], a[124:127], v[92:95]\n" \
"ds_read_b128 a[152:155], v48 offset:14336\n" \
".fill (L_ab + 8192 - .) / 4, 4, 0xBF800000\n" \
"L_ap2:\n" \
"s_cbranch_execz L_ap0\n" \
"ds_read_b128 a[156:159], v48 offset:15360\n" \
"v_mfma_f32_16x16x32_bf16 v[88:91], a[140:143], a[124:127], v[44:47]\n" \
"ds_read_b128 a[140:143], v48 offset:11264\n" \
"v_cvt_pk_bf16_f32 v72, v72, v73\n" \
"v_mfma_f32_16x16x32_bf16 v[88:91], a[136:139], a[120:123], v[88:91]\n" \
"v_exp_f32_e32 v68, v68\n" \
"v_mfma_f32_16x16x32_bf16 v[88:91], a[132:135], a[116:119], v[88:91]\n" \
"buffer_load_dwordx4 v53, s[8:11], s64 offen offset:0 sc1 lds\n" \
"v_mfma_f32_16x16x32_bf16 v[88:91], a[128:131], a[112:115], v[88:91]\n" \
"ds_read_b128 a[128:131], v48 offset:8192\n" \
"s_waitcnt lgkmcnt(6)\n" \
"v_mfma_f32_16x16x32_bf16 v[128:131], a[160:163], v[100:103], v[128:131]\n" \
"ds_read_b128 a[132:135], v48 offset:9216\n" \
"ds_read_b128 a[136:139], v48 offset:10240\n" \
"v_mfma_f32_16x16x32_bf16 v[160:163], a[160:163], v[104:107], v[160:163]\n" \
"v_exp_f32_e32 v69, v69\n" \
"v_mfma_f32_16x16x32_bf16 v[132:135], a[164:167], v[100:103], v[132:135]\n" \
"v_exp_f32_e64 v74, v74\n" \
"v_mfma_f32_16x16x32_bf16 v[164:167], a[164:167], v[104:107], v[164:167]\n" \
"v_exp_f32_e64 v75, v75\n" \
"v_mfma_f32_16x16x32_bf16 v[136:139], a[168:171], v[100:103], v[136:139]\n" \
"v_exp_f32_e64 v66, v66\n" \
"v_mfma_f32_16x16x32_bf16 v[168:171], a[168:171], v[104:107], v[168:171]\n" \
"buffer_load_dwordx4 v53, s[8:11], s64 offen offset:1024 sc1 lds\n" \
"v_mfma_f32_16x16x32_bf16 v[140:143], a[172:175], v[100:103], v[140:143]\n" \
"v_exp_f32_e64 v67, v67\n" \
"v_mfma_f32_16x16x32_bf16 v[172:175], a[172:175], v[104:107], v[172:175]\n" \
"v_exp_f32_e64 v64, v64\n" \
"v_mfma_f32_16x16x32_bf16 v[144:147], a[176:179], v[100:103], v[144:147]\n" \
"v_exp_f32_e64 v65, v65\n" \
"v_mfma_f32_16x16x32_bf16 v[176:179], a[176:179], v[104:107], v[176:179]\n" \
"v_exp_f32_e64 v76, v76\n" \
"v_mfma_f32_16x16x32_bf16 v[148:151], a[180:183], v[100:103], v[148:151]\n" \
"v_exp_f32_e64 v77, v77\n" \
"v_mfma_f32_16x16x32_bf16 v[180:183], a[180:183], v[104:107], v[180:183]\n" \
"v_exp_f32_e64 v78, v78\n" \
"v_mfma_f32_16x16x32_bf16 v[152:155], a[184:187], v[100:103], v[152:155]\n" \
"v_exp_f32_e64 v79, v79\n" \
"v_mfma_f32_16x16x32_bf16 v[184:187], a[184:187], v[104:107], v[184:187]\n" \
"buffer_load_dwordx4 v53, s[8:11], s64 offen offset:2048 sc1 lds\n" \
"v_mfma_f32_16x16x32_bf16 v[156:159], a[188:191], v[100:103], v[156:159]\n" \
"v_cvt_pk_bf16_f32 v70, v68, v69\n" \
"v_cvt_pk_bf16_f32 v73, v74, v75\n" \
"v_mfma_f32_16x16x32_bf16 v[188:191], a[188:191], v[104:107], v[188:191]\n" \
"v_cvt_pk_bf16_f32 v69, v66, v67\n" \
"v_cvt_pk_bf16_f32 v68, v64, v65\n" \
"v_smfmac_f32_16x16x64_bf16 a[192:195], a[200:203], v[100:107], v21\n" \
"v_cvt_pk_bf16_f32 v74, v76, v77\n" \
"v_cvt_pk_bf16_f32 v75, v78, v79\n" \
"v_mfma_f32_16x16x32_bf16 v[220:223], a[188:191], v[116:119], v[220:223]\n" \
"v_xor_b32_e32 v48, 0x8000, v48\n" \
"v_mfma_f32_16x16x32_bf16 v[252:255], a[188:191], v[120:123], v[252:255]\n" \
"v_mfma_f32_16x16x32_bf16 v[216:219], a[184:187], v[116:119], v[216:219]\n" \
"v_mfma_f32_16x16x32_bf16 v[248:251], a[184:187], v[120:123], v[248:251]\n" \
"buffer_load_dwordx4 v53, s[8:11], s64 offen offset:3072 sc1 lds\n" \
"v_mfma_f32_16x16x32_bf16 v[212:215], a[180:183], v[116:119], v[212:215]\n" \
"s_add_u32 s64, s64, 0x10000\n" \
"s_sub_u32 s66, s66, 1\n" \
"s_nop 0\n" \
"v_mfma_f32_16x16x32_bf16 v[244:247], a[180:183], v[120:123], v[244:247]\n" \
"v_mfma_f32_16x16x32_bf16 v[208:211], a[176:179], v[116:119], v[208:211]\n" \
"v_mfma_f32_16x16x32_bf16 v[240:243], a[176:179], v[120:123], v[240:243]\n" \
"v_mfma_f32_16x16x32_bf16 v[204:207], a[172:175], v[116:119], v[204:207]\n" \
"v_mfma_f32_16x16x32_bf16 v[236:239], a[172:175], v[120:123], v[236:239]\n" \
"v_mfma_f32_16x16x32_bf16 v[200:203], a[168:171], v[116:119], v[200:203]\n" \
"v_mfma_f32_16x16x32_bf16 v[232:235], a[168:171], v[120:123], v[232:235]\n" \
"v_mfma_f32_16x16x32_bf16 v[196:199], a[164:167], v[116:119], v[196:199]\n" \
"v_mfma_f32_16x16x32_bf16 v[228:231], a[164:167], v[120:123], v[228:231]\n" \
"v_mfma_f32_16x16x32_bf16 v[192:195], a[160:163], v[116:119], v[192:195]\n" \
"v_mfma_f32_16x16x32_bf16 v[224:227], a[160:163], v[120:123], v[224:227]\n" \
"v_smfmac_f32_16x16x64_bf16 a[196:199], a[200:203], v[116:123], v21\n" \
"s_waitcnt vmcnt(8) lgkmcnt(0)\n" \
"s_barrier\n" \
"s_cbranch_scc0 L_2_%=\n" \
"s_cmp_lg_u32 s16, 0\n" \
"s_cbranch_scc1 L_f1_%=\n" \
"v_bfe_u32 v58, v20, 2, 7\n" \
"v_readlane_b32 s96, v11, 1\n" \
"ds_read_b128 a[160:163], v49 offset:0\n" \
"ds_read_b128 a[164:167], v49 offset:1024\n" \
"ds_read_b128 a[168:171], v49 offset:2048\n" \
"v_mfma_f32_16x16x32_bf16 v[120:123], a[128:131], a[112:115], v[44:47]\n" \
"v_readlane_b32 s93, v11, 20\n" \
"ds_read_b128 a[172:175], v49 offset:3072\n" \
"buffer_load_dword v55, v58, s[56:59], s96 offen sc1\n" \
"v_readlane_b32 s84, v11, 15\n" \
"v_readlane_b32 s85, v11, 16\n" \
"v_readlane_b32 s86, v11, 17\n" \
"s_mov_b32 s87, 0x20000\n" \
"v_readlane_b32 s92, v11, 18\n" \
"s_add_u32 m0, s93, 0x0\n" \
"v_mfma_f32_16x16x32_bf16 v[120:123], a[132:135], a[116:119], v[120:123]\n" \
"ds_read_b128 a[176:179], v49 offset:4096\n" \
"v_add_u32_e32 v54, 0x49000, v20\n" \
"v_mfma_f32_16x16x32_bf16 v[120:123], a[136:139], a[120:123], v[120:123]\n" \
"v_readlane_b32 s97, v11, 35\n" \
"v_readlane_b32 s98, v11, 36\n" \
"v_mfma_f32_16x16x32_bf16 v[120:123], a[140:143], a[124:127], v[120:123]\n" \
"ds_read_b128 a[180:183], v49 offset:5120\n" \
"ds_read_b128 a[184:187], v49 offset:6144\n" \
"v_mfma_f32_16x16x32_bf16 v[124:127], a[156:159], a[124:127], v[44:47]\n" \
"ds_read_b128 a[188:191], v49 offset:7168\n" \
"v_add_u32_e32 v50, s15, v20\n" \
"buffer_load_dwordx4 v0, s[84:87], s92 offen offset:0 lds\n" \
"v_mfma_f32_16x16x32_bf16 v[124:127], a[152:155], a[120:123], v[124:127]\n" \
"v_exp_f32_e32 v86, v86\n" \
"v_mfma_f32_16x16x32_bf16 v[124:127], a[148:151], a[116:119], v[124:127]\n" \
"v_exp_f32_e32 v87, v87\n" \
"v_mfma_f32_16x16x32_bf16 v[124:127], a[144:147], a[112:115], v[124:127]\n" \
"v_cvt_pk_bf16_f32 v87, v86, v87\n" \
"s_waitcnt lgkmcnt(7)\n" \
"v_mfma_f32_16x16x32_bf16 v[128:131], a[160:163], v[68:71], v[128:131]\n" \
"v_exp_f32_e32 v84, v84\n" \
"v_mfma_f32_16x16x32_bf16 v[160:163], a[160:163], v[72:75], v[160:163]\n" \
"v_and_b32_e32 v87, v15, v87\n" \
"s_waitcnt lgkmcnt(6)\n" \
"v_mfma_f32_16x16x32_bf16 v[132:135], a[164:167], v[68:71], v[132:135]\n" \
"buffer_load_dwordx4 v1, s[84:87], s92 offen offset:1024 lds\n" \
"v_mfma_f32_16x16x32_bf16 v[164:167], a[164:167], v[72:75], v[164:167]\n" \
"v_add_u32_e32 v51, 0x10000, v50\n" \
"s_waitcnt lgkmcnt(5)\n" \
"v_mfma_f32_16x16x32_bf16 v[136:139], a[168:171], v[68:71], v[136:139]\n" \
"v_exp_f32_e32 v85, v85\n" \
"v_mfma_f32_16x16x32_bf16 v[168:171], a[168:171], v[72:75], v[168:171]\n" \
"v_cvt_pk_bf16_f32 v86, v84, v85\n" \
"s_waitcnt lgkmcnt(4)\n" \
"v_mfma_f32_16x16x32_bf16 v[140:143], a[172:175], v[68:71], v[140:143]\n" \
"v_exp_f32_e32 v82, v82\n" \
"v_mfma_f32_16x16x32_bf16 v[172:175], a[172:175], v[72:75], v[172:175]\n" \
"v_and_b32_e32 v86, v14, v86\n" \
"s_waitcnt lgkmcnt(3)\n" \
"v_mfma_f32_16x16x32_bf16 v[144:147], a[176:179], v[68:71], v[144:147]\n" \
"buffer_load_dwordx4 v2, s[84:87], s92 offen offset:2048 lds\n" \
"v_mfma_f32_16x16x32_bf16 v[176:179], a[176:179], v[72:75], v[176:179]\n" \
"v_cvt_pk_bf16_f32 v128, v128, v129\n" \
"s_waitcnt lgkmcnt(1)\n" \
"v_mfma_f32_16x16x32_bf16 v[148:151], a[180:183], v[68:71], v[148:151]\n" \
"v_exp_f32_e32 v83, v83\n" \
"v_mfma_f32_16x16x32_bf16 v[180:183], a[180:183], v[72:75], v[180:183]\n" \
"v_exp_f32_e32 v80, v80\n" \
"v_mfma_f32_16x16x32_bf16 v[152:155], a[184:187], v[68:71], v[152:155]\n" \
"v_exp_f32_e32 v81, v81\n" \
"v_mfma_f32_16x16x32_bf16 v[184:187], a[184:187], v[72:75], v[184:187]\n" \
"v_cvt_pk_bf16_f32 v85, v82, v83\n" \
"s_waitcnt lgkmcnt(0)\n" \
"v_mfma_f32_16x16x32_bf16 v[156:159], a[188:191], v[68:71], v[156:159]\n" \
"buffer_load_dwordx4 v3, s[84:87], s92 offen offset:3072 lds\n" \
"s_add_u32 m0, s93, 0x1000\n" \
"v_mfma_f32_16x16x32_bf16 v[188:191], a[188:191], v[72:75], v[188:191]\n" \
"v_cvt_pk_bf16_f32 v84, v80, v81\n" \
"v_and_b32_e32 v84, v12, v84\n" \
"v_smfmac_f32_16x16x64_bf16 a[192:195], a[200:203], v[68:75], v21\n" \
"v_and_b32_e32 v85, v13, v85\n" \
"v_exp_f32_e32 v88, v88\n" \
"v_exp_f32_e32 v89, v89\n" \
"v_mfma_f32_16x16x32_bf16 v[220:223], a[188:191], v[84:87], v[220:223]\n" \
"v_exp_f32_e32 v90, v90\n" \
"v_exp_f32_e32 v91, v91\n" \
"v_cvt_pk_bf16_f32 v88, v88, v89\n" \
"v_exp_f32_e32 v92, v92\n" \
"buffer_load_dwordx4 v4, s[84:87], s92 offen offset:0 lds\n" \
"v_exp_f32_e32 v93, v93\n" \
"v_exp_f32_e32 v94, v94\n" \
"v_exp_f32_e32 v95, v95\n" \
"v_cvt_pk_bf16_f32 v160, v160, v161\n" \
"v_cvt_pk_bf16_f32 v89, v90, v91\n" \
"v_cvt_pk_bf16_f32 v129, v130, v131\n" \
"v_cvt_pk_bf16_f32 v161, v162, v163\n" \
"v_cvt_pk_bf16_f32 v90, v92, v93\n" \
"v_cvt_pk_bf16_f32 v91, v94, v95\n" \
"v_cvt_pk_bf16_f32 v130, v132, v133\n" \
"v_cvt_pk_bf16_f32 v131, v134, v135\n" \
"v_cvt_pk_bf16_f32 v162, v164, v165\n" \
"v_cvt_pk_bf16_f32 v163, v166, v167\n" \
"v_mfma_f32_16x16x32_bf16 v[252:255], a[188:191], v[88:91], v[252:255]\n" \
"v_cvt_pk_bf16_f32 v132, v136, v137\n" \
"v_cvt_pk_bf16_f32 v133, v138, v139\n" \
"v_cvt_pk_bf16_f32 v134, v140, v141\n" \
"buffer_load_dwordx4 v5, s[84:87], s92 offen offset:1024 lds\n" \
"v_cvt_pk_bf16_f32 v135, v142, v143\n" \
"v_cvt_pk_bf16_f32 v164, v168, v169\n" \
"v_cvt_pk_bf16_f32 v165, v170, v171\n" \
"v_cvt_pk_bf16_f32 v166, v172, v173\n" \
"v_cvt_pk_bf16_f32 v167, v174, v175\n" \
"v_cvt_pk_bf16_f32 v136, v144, v145\n" \
"v_cvt_pk_bf16_f32 v137, v146, v147\n" \
"v_cvt_pk_bf16_f32 v138, v148, v149\n" \
"v_cvt_pk_bf16_f32 v139, v150, v151\n" \
"v_cvt_pk_bf16_f32 v140, v152, v153\n" \
"v_cvt_pk_bf16_f32 v141, v154, v155\n" \
"v_cvt_pk_bf16_f32 v142, v156, v157\n" \
"v_cvt_pk_bf16_f32 v143, v158, v159\n" \
"v_cvt_pk_bf16_f32 v168, v176, v177\n" \
"v_cvt_pk_bf16_f32 v169, v178, v179\n" \
"v_cvt_pk_bf16_f32 v170, v180, v181\n" \
"v_cvt_pk_bf16_f32 v171, v182, v183\n" \
"v_cvt_pk_bf16_f32 v172, v184, v185\n" \
"v_cvt_pk_bf16_f32 v173, v186, v187\n" \
"v_cvt_pk_bf16_f32 v174, v188, v189\n" \
"v_cvt_pk_bf16_f32 v175, v190, v191\n" \
"buffer_load_dwordx4 v6, s[84:87], s92 offen offset:2048 lds\n" \
"s_waitcnt vmcnt(8)\n" \
"ds_write_b128 v50, v[128:131] offset:0\n" \
"v_mfma_f32_16x16x32_bf16 v[216:219], a[184:187], v[84:87], v[216:219]\n" \
"ds_read_b32 v60, v18 offset:0\n" \
"ds_read_b32 v61, v18 offset:128\n" \
"v_mfma_f32_16x16x32_bf16 v[248:251], a[184:187], v[88:91], v[248:251]\n" \
"ds_write_b128 v50, v[132:135] offset:1024\n" \
"s_waitcnt vmcnt(7)\n" \
"v_cmp_ne_u32_e32 vcc, s62, v55\n" \
"v_mfma_f32_16x16x32_bf16 v[212:215], a[180:183], v[84:87], v[212:215]\n" \
"ds_read_b128 a[0:3], v49 offset:8192\n" \
"ds_read_b128 a[4:7], v49 offset:9216\n" \
"ds_write_b128 v50, v[136:139] offset:2048\n" \
"v_mfma_f32_16x16x32_bf16 v[244:247], a[180:183], v[88:91], v[244:247]\n" \
"buffer_load_dwordx4 v7, s[84:87], s92 offen offset:3072 lds\n" \
"s_add_u32 m0, s93, 0x2000\n" \
"s_cbranch_vccnz L_so0_%=\n" \
"L_ro0_%=:\n" \
"ds_write_b128 v50, v[140:143] offset:3072\n" \
"ds_read_b128 a[8:11], v49 offset:10240\n" \
"ds_read_b128 a[12:15], v49 offset:11264\n" \
"ds_read_b128 a[16:19], v49 offset:12288\n" \
"ds_read_b128 a[20:23], v49 offset:13312\n" \
"ds_read_b128 a[24:27], v49 offset:14336\n" \
"ds_read_b128 a[28:31], v49 offset:15360\n" \
"ds_write_b128 v51, v[160:163] offset:32768\n" \
"v_mfma_f32_16x16x32_bf16 v[208:211], a[176:179], v[84:87], v[208:211]\n" \
"v_accvgpr_read_b32 v40, a192\n" \
"v_accvgpr_read_b32 v22, a193\n" \
"v_mfma_f32_16x16x32_bf16 v[240:243], a[176:179], v[88:91], v[240:243]\n" \
"ds_write_b128 v51, v[164:167] offset:33792\n" \
"v_mfma_f32_16x16x32_bf16 v[204:207], a[172:175], v[84:87], v[204:207]\n" \
"buffer_load_dwordx4 v8, s[84:87], s92 offen offset:0 lds\n" \
"v_mfma_f32_16x16x32_bf16 v[236:239], a[172:175], v[88:91], v[236:239]\n" \
"ds_write_b128 v51, v[168:171] offset:34816\n" \
"v_mfma_f32_16x16x32_bf16 v[200:203], a[168:171], v[84:87], v[200:203]\n" \
"v_accvgpr_read_b32 v41, a194\n" \
"v_accvgpr_read_b32 v23, a195\n" \
"v_mfma_f32_16x16x32_bf16 v[232:235], a[168:171], v[88:91], v[232:235]\n" \
"ds_write_b128 v51, v[172:175] offset:35840\n" \
"v_mfma_f32_16x16x32_bf16 v[196:199], a[164:167], v[84:87], v[196:199]\n" \
"v_add_f32_e32 v40, v40, v22\n" \
"v_add_f32_e32 v41, v41, v23\n" \
"v_mfma_f32_16x16x32_bf16 v[228:231], a[164:167], v[88:91], v[228:231]\n" \
"ds_write_b64 v19, v[40:41]\n" \
"v_lshl_add_u32 v52, s15, 2, v20\n" \
"v_mfma_f32_16x16x32_bf16 v[192:195], a[160:163], v[84:87], v[192:195]\n" \
"buffer_load_dwordx4 a[220:223], v20, s[56:59], s60 offen offset:0 sc1\n" \
"v_mfma_f32_16x16x32_bf16 v[224:227], a[160:163], v[88:91], v[224:227]\n" \
"v_add_u32_e32 v52, 0x1a000, v52\n" \
"v_xor_b32_e32 v63, 0x400, v19\n" \
"v_smfmac_f32_16x16x64_bf16 a[196:199], a[200:203], v[84:91], v21\n" \
"v_exp_f32_e32 v120, v120\n" \
"v_exp_f32_e32 v121, v121\n" \
"v_exp_f32_e32 v122, v122\n" \
"v_exp_f32_e32 v123, v123\n" \
"v_cvt_pk_bf16_f32 v120, v120, v121\n" \
"v_exp_f32_e32 v124, v124\n" \
"v_exp_f32_e32 v125, v125\n" \
"v_exp_f32_e32 v126, v126\n" \
"buffer_load_dwordx4 a[236:239], v54, s[56:59], s60 offen offset:0 sc1\n" \
"v_exp_f32_e32 v127, v127\n" \
"v_cvt_pk_bf16_f32 v121, v122, v123\n" \
"v_cvt_pk_bf16_f32 v122, v124, v125\n" \
"v_cvt_pk_bf16_f32 v123, v126, v127\n" \
"v_and_b32_e32 v120, v12, v120\n" \
"v_and_b32_e32 v121, v13, v121\n" \
"v_and_b32_e32 v122, v14, v122\n" \
"v_and_b32_e32 v123, v15, v123\n" \
"s_waitcnt lgkmcnt(0)\n" \
"s_barrier\n" \
"ds_read_b128 v[144:147], v52 offset:0\n" \
"ds_read_b128 v[148:151], v52 offset:1024\n" \
"ds_read_b128 v[152:155], v52 offset:2048\n" \
"ds_read_b128 v[156:159], v52 offset:3072\n" \
"buffer_load_dwordx4 a[224:227], v20, s[56:59], s60 offen offset:1024 sc1\n" \
"ds_read_b128 v[176:179], v52 offset:4096\n" \
"ds_read_b128 v[180:183], v52 offset:5120\n" \
"ds_read_b128 v[184:187], v52 offset:6144\n" \
"ds_read_b128 v[188:191], v52 offset:7168\n" \
"v_mfma_f32_16x16x32_bf16 a[196:199], a[212:215], v[120:123], a[196:199]\n" \
"ds_read_b64 v[42:43], v63\n" \
"v_mov_b32_e32 v130, v28\n" \
"v_mfma_f32_16x16x32_bf16 v[224:227], a[0:3], v[120:123], v[224:227]\n" \
"v_mov_b32_e32 v131, v29\n" \
"v_mov_b32_e32 v132, 0\n" \
"v_mfma_f32_16x16x32_bf16 v[228:231], a[4:7], v[120:123], v[228:231]\n" \
"v_mov_b32_e32 v133, 0\n" \
"v_mov_b32_e32 v128, 0\n" \
"v_mfma_f32_16x16x32_bf16 v[232:235], a[8:11], v[120:123], v[232:235]\n" \
"v_mov_b32_e32 v129, 0\n" \
"v_accvgpr_read_b32 v56, a196\n" \
"v_mfma_f32_16x16x32_bf16 v[236:239], a[12:15], v[120:123], v[236:239]\n" \
"buffer_load_dwordx4 a[240:243], v54, s[56:59], s60 offen offset:1024 sc1\n" \
"v_mfma_f32_16x16x32_bf16 v[240:243], a[16:19], v[120:123], v[240:243]\n" \
"v_accvgpr_read_b32 v24, a197\n" \
"v_add_f32_e32 v56, v56, v24\n" \
"v_mfma_f32_16x16x32_bf16 v[244:247], a[20:23], v[120:123], v[244:247]\n" \
"v_accvgpr_read_b32 v57, a198\n" \
"v_accvgpr_read_b32 v25, a199\n" \
"v_mfma_f32_16x16x32_bf16 v[248:251], a[24:27], v[120:123], v[248:251]\n" \
"v_add_f32_e32 v57, v57, v25\n" \
"v_mfma_f32_16x16x32_bf16 v[252:255], a[28:31], v[120:123], v[252:255]\n" \
"s_waitcnt lgkmcnt(1)\n" \
"s_barrier\n" \
"buffer_load_dwordx4 a[228:231], v20, s[56:59], s60 offen offset:2048 sc1\n" \
"v_mfma_f32_16x16x32_bf16 v[192:195], v[130:133], v[144:147], v[192:195]\n" \
"s_waitcnt lgkmcnt(0)\n" \
"v_add_f32_e32 v56, v56, v42\n" \
"v_mfma_f32_16x16x32_bf16 v[196:199], v[128:131], v[144:147], v[196:199]\n" \
"v_max_f32_e32 v62, v62, v56\n" \
"v_add_f32_e32 v57, v57, v43\n" \
"v_mfma_f32_16x16x32_bf16 v[200:203], v[130:133], v[148:151], v[200:203]\n" \
"v_rcp_f32_e32 v56, v56\n" \
"v_mfma_f32_16x16x32_bf16 v[204:207], v[128:131], v[148:151], v[204:207]\n" \
"v_mul_f32_e32 v56, v56, v60\n" \
"v_mul_f32_e32 v192, v56, v192\n" \
"v_mfma_f32_16x16x32_bf16 v[208:211], v[130:133], v[152:155], v[208:211]\n" \
"v_mul_f32_e32 v193, v56, v193\n" \
"v_mul_f32_e32 v194, v56, v194\n" \
"v_mfma_f32_16x16x32_bf16 v[212:215], v[128:131], v[152:155], v[212:215]\n" \
"buffer_load_dwordx4 a[244:247], v54, s[56:59], s60 offen offset:2048 sc1\n" \
"v_mfma_f32_16x16x32_bf16 v[216:219], v[130:133], v[156:159], v[216:219]\n" \
"v_mul_f32_e32 v195, v56, v195\n" \
"v_mul_f32_e32 v196, v56, v196\n" \
"v_mfma_f32_16x16x32_bf16 v[220:223], v[128:131], v[156:159], v[220:223]\n" \
"v_mul_f32_e32 v197, v56, v197\n" \
"v_mul_f32_e32 v198, v56, v198\n" \
"v_mfma_f32_16x16x32_bf16 v[224:227], v[130:133], v[176:179], v[224:227]\n" \
"v_mul_f32_e32 v199, v56, v199\n" \
"v_cvt_pk_bf16_f32 v64, v192, v193\n" \
"v_mfma_f32_16x16x32_bf16 v[228:231], v[128:131], v[176:179], v[228:231]\n" \
"v_cvt_pk_bf16_f32 v65, v194, v195\n" \
"v_cvt_pk_bf16_f32 v66, v196, v197\n" \
"v_mfma_f32_16x16x32_bf16 v[232:235], v[130:133], v[180:183], v[232:235]\n" \
"v_cvt_pk_bf16_f32 v67, v198, v199\n" \
"v_mul_f32_e32 v200, v56, v200\n" \
"v_mfma_f32_16x16x32_bf16 v[236:239], v[128:131], v[180:183], v[236:239]\n" \
"buffer_load_dwordx4 a[232:235], v20, s[56:59], s60 offen offset:3072 sc1\n" \
"v_mfma_f32_16x16x32_bf16 v[240:243], v[130:133], v[184:187], v[240:243]\n" \
"v_mfma_f32_16x16x32_bf16 a[208:211], a[216:219], v[64:67], a[208:211]\n" \
"v_mul_f32_e32 v201, v56, v201\n" \
"v_mul_f32_e32 v202, v56, v202\n" \
"v_mul_f32_e32 v203, v56, v203\n" \
"v_mul_f32_e32 v204, v56, v204\n" \
"v_mul_f32_e32 v205, v56, v205\n" \
"v_mul_f32_e32 v206, v56, v206\n" \
"v_mul_f32_e32 v207, v56, v207\n" \
"v_cvt_pk_bf16_f32 v68, v200, v201\n" \
"v_cvt_pk_bf16_f32 v69, v202, v203\n" \
"v_cvt_pk_bf16_f32 v70, v204, v205\n" \
"v_cvt_pk_bf16_f32 v71, v206, v207\n" \
"v_mul_f32_e32 v208, v56, v208\n" \
"v_mul_f32_e32 v209, v56, v209\n" \
"v_mfma_f32_16x16x32_bf16 a[208:211], a[216:219], v[68:71], a[208:211]\n" \
"buffer_load_dwordx4 a[248:251], v54, s[56:59], s60 offen offset:3072 sc1\n" \
"v_mul_f32_e32 v210, v56, v210\n" \
"v_mul_f32_e32 v211, v56, v211\n" \
"v_mul_f32_e32 v212, v56, v212\n" \
"v_mul_f32_e32 v213, v56, v213\n" \
"v_mul_f32_e32 v214, v56, v214\n" \
"v_mul_f32_e32 v215, v56, v215\n" \
"v_max_f32_e32 v62, v62, v57\n" \
"v_cvt_pk_bf16_f32 v72, v208, v209\n" \
"v_cvt_pk_bf16_f32 v73, v210, v211\n" \
"v_cvt_pk_bf16_f32 v74, v212, v213\n" \
"v_cvt_pk_bf16_f32 v75, v214, v215\n" \
"v_rcp_f32_e32 v57, v57\n" \
"v_mfma_f32_16x16x32_bf16 v[244:247], v[128:131], v[184:187], v[244:247]\n" \
"v_mfma_f32_16x16x32_bf16 a[208:211], a[216:219], v[72:75], a[208:211]\n" \
"v_mul_f32_e32 v216, v56, v216\n" \
"v_mul_f32_e32 v217, v56, v217\n" \
"buffer_load_dwordx4 a[96:99], v20, s[56:59], s97 offen offset:0 sc1\n" \
"v_mul_f32_e32 v218, v56, v218\n" \
"v_mul_f32_e32 v219, v56, v219\n" \
"v_mul_f32_e32 v220, v56, v220\n" \
"v_mul_f32_e32 v221, v56, v221\n" \
"v_mul_f32_e32 v222, v56, v222\n" \
"v_mul_f32_e32 v223, v56, v223\n" \
"v_cvt_pk_bf16_f32 v76, v216, v217\n" \
"v_cvt_pk_bf16_f32 v77, v218, v219\n" \
"v_cvt_pk_bf16_f32 v78, v220, v221\n" \
"v_cvt_pk_bf16_f32 v79, v222, v223\n" \
"v_mul_f32_e32 v57, v57, v61\n" \
"v_mul_f32_e32 v224, v57, v224\n" \
"v_mfma_f32_16x16x32_bf16 a[208:211], a[216:219], v[76:79], a[208:211]\n" \
"v_mul_f32_e32 v225, v57, v225\n" \
"v_mul_f32_e32 v226, v57, v226\n" \
"v_mul_f32_e32 v227, v57, v227\n" \
"v_mul_f32_e32 v228, v57, v228\n" \
"v_mul_f32_e32 v229, v57, v229\n" \
"v_mul_f32_e32 v230, v57, v230\n" \
"v_mul_f32_e32 v231, v57, v231\n" \
"buffer_load_dwordx4 a[104:107], v54, s[56:59], s97 offen offset:0 sc1\n" \
"v_cvt_pk_bf16_f32 v80, v224, v225\n" \
"v_cvt_pk_bf16_f32 v81, v226, v227\n" \
"v_cvt_pk_bf16_f32 v82, v228, v229\n" \
"v_cvt_pk_bf16_f32 v83, v230, v231\n" \
"v_mfma_f32_16x16x32_bf16 v[248:251], v[130:133], v[188:191], v[248:251]\n" \
"v_mul_f32_e32 v232, v57, v232\n" \
"v_mul_f32_e32 v233, v57, v233\n" \
"v_mfma_f32_16x16x32_bf16 a[208:211], a[216:219], v[80:83], a[208:211]\n" \
"v_mul_f32_e32 v234, v57, v234\n" \
"v_mul_f32_e32 v235, v57, v235\n" \
"v_mul_f32_e32 v236, v57, v236\n" \
"v_mul_f32_e32 v237, v57, v237\n" \
"v_mul_f32_e32 v238, v57, v238\n" \
"v_mul_f32_e32 v239, v57, v239\n" \
"v_cvt_pk_bf16_f32 v84, v232, v233\n" \
"v_cvt_pk_bf16_f32 v85, v234, v235\n" \
"v_cvt_pk_bf16_f32 v86, v236, v237\n" \
"v_cvt_pk_bf16_f32 v87, v238, v239\n" \
"v_mul_f32_e32 v240, v57, v240\n" \
"buffer_load_dwordx4 a[112:115], v20, s[56:59], s98 offen offset:0 sc1\n" \
"v_mfma_f32_16x16x32_bf16 a[208:211], a[216:219], v[84:87], a[208:211]\n" \
"v_mul_f32_e32 v241, v57, v241\n" \
"v_mul_f32_e32 v242, v57, v242\n" \
"v_mul_f32_e32 v243, v57, v243\n" \
"v_mul_f32_e32 v244, v57, v244\n" \
"v_mul_f32_e32 v245, v57, v245\n" \
"v_mul_f32_e32 v246, v57, v246\n" \
"v_mul_f32_e32 v247, v57, v247\n" \
"v_cvt_pk_bf16_f32 v96, v240, v241\n" \
"v_cvt_pk_bf16_f32 v97, v242, v243\n" \
"v_cvt_pk_bf16_f32 v98, v244, v245\n" \
"v_cvt_pk_bf16_f32 v99, v246, v247\n" \
"v_mfma_f32_16x16x32_bf16 v[252:255], v[128:131], v[188:191], v[252:255]\n" \
"v_mul_f32_e32 v248, v57, v248\n" \
"v_mfma_f32_16x16x32_bf16 a[208:211], a[216:219], v[96:99], a[208:211]\n" \
"v_mul_f32_e32 v249, v57, v249\n" \
"v_mul_f32_e32 v250, v57, v250\n" \
"buffer_load_dwordx4 a[120:123], v54, s[56:59], s98 offen offset:0 sc1\n" \
"v_mul_f32_e32 v251, v57, v251\n" \
"v_cvt_pk_bf16_f32 v100, v248, v249\n" \
"v_cvt_pk_bf16_f32 v101, v250, v251\n" \
"v_mul_f32_e32 v252, v57, v252\n" \
"v_mul_f32_e32 v253, v57, v253\n" \
"v_mul_f32_e32 v254, v57, v254\n" \
"v_mul_f32_e32 v255, v57, v255\n" \
"v_cvt_pk_bf16_f32 v102, v252, v253\n" \
"v_cvt_pk_bf16_f32 v103, v254, v255\n" \
"v_cmp_lt_f32_e64 s[72:73], s21, v62\n" \
"buffer_load_dwordx4 a[100:103], v20, s[56:59], s97 offen offset:1024 sc1\n" \
"v_mfma_f32_16x16x32_bf16 a[208:211], a[216:219], v[100:103], a[208:211]\n" \
"buffer_load_dwordx4 a[108:111], v54, s[56:59], s97 offen offset:1024 sc1\n" \
"buffer_load_dwordx4 a[116:119], v20, s[56:59], s98 offen offset:1024 sc1\n" \
"buffer_load_dwordx4 a[124:127], v54, s[56:59], s98 offen offset:1024 sc1\n" \
"s_nop 4\n" \
"v_accvgpr_read_b32 v63, a208\n" \
"v_cmp_u_f32_e32 vcc, v63, v63\n" \
"s_nop 0\n" \
"s_or_b64 vcc, s[72:73], vcc\n" \
"s_cbranch_scc0 L_out_%=\n" \
"s_branch L_end_%=\n" \
"L_skb_%=:\n" \
"s_sleep 1\n" \
"buffer_load_dword v57, v61, s[56:59], s96 offen sc1\n" \
"s_add_u32 s101, s101, 1\n" \
"s_waitcnt vmcnt(0)\n" \
"v_cmp_ne_u32_e32 vcc, s62, v57\n" \
"s_cmp_gt_u32 s101, 0x100000\n" \
"s_cbranch_scc1 L_rkb_%=\n" \
"s_nop 2\n" \
"s_cbranch_vccnz L_skb_%=\n" \
"s_branch L_rkb_%=\n" \
"L_so0_%=:\n" \
"s_sleep 1\n" \
"buffer_load_dword v55, v58, s[56:59], s96 offen sc1\n" \
"s_add_u32 s101, s101, 1\n" \
"s_waitcnt vmcnt(0)\n" \
"v_cmp_ne_u32_e32 vcc, s62, v55\n" \
"s_cmp_gt_u32 s101, 0x100000\n" \
"s_cbranch_scc1 L_ro0_%=\n" \
"s_nop 2\n" \
"s_cbranch_vccnz L_so0_%=\n" \
"s_branch L_ro0_%=\n" \
"L_so1_%=:\n" \
"s_sleep 1\n" \
"buffer_load_dword v57, v60, s[56:59], s96 offen sc1\n" \
"s_add_u32 s101, s101, 1\n" \
"s_waitcnt vmcnt(0)\n" \
"v_cmp_ne_u32_e32 vcc, s62, v57\n" \
"s_cmp_gt_u32 s101, 0x100000\n" \
"s_cbranch_scc1 L_ro1_%=\n" \
"s_nop 2\n" \
"s_cbranch_vccnz L_so1_%=\n" \
"s_branch L_ro1_%=\n" \
"L_sa_%=:\n" \
"s_sleep 1\n" \
"buffer_load_dword v26, v38, s[56:59], s64 offen sc1\n" \
"s_add_u32 s101, s101, 1\n" \
"s_waitcnt vmcnt(0)\n" \
"v_cmp_ne_u32_e32 vcc, s62, v26\n" \
"s_cmp_gt_u32 s101, 0x100000\n" \
"s_cbranch_scc1 L_ra_%=\n" \
"s_nop 2\n" \
"s_cbranch_vccnz L_sa_%=\n" \
"s_branch L_ra_%=\n" \
"L_sb_%=:\n" \
"s_sleep 1\n" \
"buffer_load_dword v47, v38, s[56:59], s73 offen sc1\n" \
"s_add_u32 s101, s101, 1\n" \
"s_waitcnt vmcnt(0)\n" \
"v_cmp_ne_u32_e32 vcc, s62, v47\n" \
"s_cmp_gt_u32 s101, 0x100000\n" \
"s_cbranch_scc1 L_rb_%=\n" \
"s_nop 2\n" \
"s_cbranch_vccnz L_sb_%=\n" \
"s_branch L_rb_%=\n" \
"L_sx0_%=:\n" \
"s_sleep 1\n" \
"buffer_load_dword v37, v40, s[56:59], s74 offen sc1\n" \
"s_add_u32 s101, s101, 1\n" \
"s_waitcnt vmcnt(0)\n" \
"v_cmp_eq_u32_e64 s[98:99], s62, v37\n" \
"s_cmp_gt_u32 s101, 0x100000\n" \
"s_cbranch_scc1 L_rx0_%=\n" \
"s_or_b32 s75, 0x7f, s100\n" \
"s_andn2_b32 s75, s75, s98\n" \
"s_cbranch_scc1 L_sx0_%=\n" \
"s_branch L_rx0_%=\n" \
"L_sx1_%=:\n" \
"s_sleep 1\n" \
"buffer_load_dword v37, v40, s[56:59], s74 offen sc1\n" \
"s_add_u32 s101, s101, 1\n" \
"s_waitcnt vmcnt(0)\n" \
"v_cmp_eq_u32_e64 s[98:99], s62, v37\n" \
"s_cmp_gt_u32 s101, 0x100000\n" \
"s_cbranch_scc1 L_rx1_%=\n" \
"s_andn2_b32 s75, 0x100, s98\n" \
"s_cbranch_scc1 L_sx1_%=\n" \
"s_branch L_rx1_%=\n" \
"L_sx2_%=:\n" \
"s_sleep 1\n" \
"buffer_load_dword v37, v40, s[56:59], s74 offen sc1\n" \
"s_add_u32 s101, s101, 1\n" \
"s_waitcnt vmcnt(0)\n" \
"v_cmp_eq_u32_e64 s[98:99], s62, v37\n" \
"s_cmp_gt_u32 s101, 0x100000\n" \
"s_cbranch_scc1 L_rx2_%=\n" \
"s_andn2_b32 s75, 0x200, s98\n" \
"s_cbranch_scc1 L_sx2_%=\n" \
"s_branch L_rx2_%=\n" \
"L_sx3_%=:\n" \
"s_sleep 1\n" \
"buffer_load_dword v37, v40, s[56:59], s74 offen sc1\n" \
"s_add_u32 s101, s101, 1\n" \
"s_waitcnt vmcnt(0)\n" \
"v_cmp_eq_u32_e64 s[98:99], s62, v37\n" \
"s_cmp_gt_u32 s101, 0x100000\n" \
"s_cbranch_scc1 L_rx3_%=\n" \
"s_andn2_b32 s75, 0x400, s98\n" \
"s_cbranch_scc1 L_sx3_%=\n" \
"s_branch L_rx3_%=\n" \
"L_sx4_%=:\n" \
"s_sleep 1\n" \
"buffer_load_dword v37, v40, s[56:59], s74 offen sc1\n" \
"s_add_u32 s101, s101, 1\n" \
"s_waitcnt vmcnt(0)\n" \
"v_cmp_eq_u32_e64 s[98:99], s62, v37\n" \
"s_cmp_gt_u32 s101, 0x100000\n" \
"s_cbranch_scc1 L_rx4_%=\n" \
"s_andn2_b32 s75, 0x800, s98\n" \
"s_cbranch_scc1 L_sx4_%=\n" \
"s_branch L_rx4_%=\n" \
"L_sx5_%=:\n" \
"s_sleep 1\n" \
"buffer_load_dword v37, v40, s[56:59], s74 offen sc1\n" \
"s_add_u32 s101, s101, 1\n" \
"s_waitcnt vmcnt(0)\n" \
"v_cmp_eq_u32_e64 s[98:99], s62, v37\n" \
"s_cmp_gt_u32 s101, 0x100000\n" \
"s_cbranch_scc1 L_rx5_%=\n" \
"s_andn2_b32 s75, 0x1000, s98\n" \
"s_cbranch_scc1 L_sx5_%=\n" \
"s_branch L_rx5_%=\n" \
"L_sx6_%=:\n" \
"s_sleep 1\n" \
"buffer_load_dword v37, v40, s[56:59], s74 offen sc1\n" \
"s_add_u32 s101, s101, 1\n" \
"s_waitcnt vmcnt(0)\n" \
"v_cmp_eq_u32_e64 s[98:99], s62, v37\n" \
"s_cmp_gt_u32 s101, 0x100000\n" \
"s_cbranch_scc1 L_rx6_%=\n" \
"s_andn2_b32 s75, 0x2000, s98\n" \
"s_cbranch_scc1 L_sx6_%=\n" \
"s_branch L_rx6_%=\n" \
"L_sx7_%=:\n" \
"s_sleep 1\n" \
"buffer_load_dword v37, v40, s[56:59], s74 offen sc1\n" \
"s_add_u32 s101, s101, 1\n" \
"s_waitcnt vmcnt(0)\n" \
"v_cmp_eq_u32_e64 s[98:99], s62, v37\n" \
"s_cmp_gt_u32 s101, 0x100000\n" \
"s_cbranch_scc1 L_rx7_%=\n" \
"s_andn2_b32 s75, 0x4000, s98\n" \
"s_cbranch_scc1 L_sx7_%=\n" \
"s_branch L_rx7_%=\n" \
".fill (L_ab + 12288 - .) / 4, 4, 0xBF800000\n" \
".fill (L_ab + 12288 - .) / 4, 4, 0xBF800000\n" \
"L_ap3:\n" \
"s_setpc_b64 s[88:89]\n" \
"L_sw_%=:\n" \
"v_mfma_f32_16x16x32_bf16 v[104:107], a[128:131], a[16:19], v[36:39]\n" \
"s_add_u32 s68, s60, 0xfff6e000\n" \
"s_add_u32 s69, s60, 0xfffb7000\n" \
"s_add_u32 s70, s60, 0x0\n" \
"s_add_u32 s71, s60, 0x49000\n" \
"v_mfma_f32_16x16x32_bf16 v[104:107], a[132:135], a[20:23], v[104:107]\n" \
"s_xor_b32 m0, s65, 0x12000\n" \
"ds_read_b128 a[160:163], v49 offset:0\n" \
"v_mfma_f32_16x16x32_bf16 v[104:107], a[136:139], a[24:27], v[104:107]\n" \
"buffer_load_dwordx4 v50, s[8:11], s64 offen offset:0 sc1 lds\n" \
"v_mfma_f32_16x16x32_bf16 v[104:107], a[140:143], a[28:31], v[104:107]\n" \
"ds_read_b128 a[164:167], v49 offset:1024\n" \
"ds_read_b128 a[168:171], v49 offset:2048\n" \
"v_mfma_f32_16x16x32_bf16 v[108:111], a[156:159], a[28:31], v[36:39]\n" \
"ds_read_b128 a[172:175], v49 offset:3072\n" \
"ds_read_b128 a[176:179], v49 offset:4096\n" \
"v_mfma_f32_16x16x32_bf16 v[108:111], a[152:155], a[24:27], v[108:111]\n" \
"buffer_load_dwordx4 v50, s[8:11], s64 offen offset:1024 sc1 lds\n" \
"v_mfma_f32_16x16x32_bf16 v[108:111], a[148:151], a[20:23], v[108:111]\n" \
"s_mov_b32 s65, s13\n" \
"v_add_u32_e32 v63, s15, v20\n" \
"v_mfma_f32_16x16x32_bf16 v[108:111], a[144:147], a[16:19], v[108:111]\n" \
"s_add_u32 s67, s14, 40960\n" \
"v_add_u32_e32 v50, 0x6000, v63\n" \
"v_mfma_f32_16x16x32_bf16 v[116:119], a[144:147], a[32:35], v[40:43]\n" \
"s_xor_b32 m0, s65, 0x8000\n" \
"ds_read_b128 a[180:183], v49 offset:5120\n" \
"v_mfma_f32_16x16x32_bf16 v[116:119], a[148:151], a[36:39], v[116:119]\n" \
"ds_read_b128 a[184:187], v49 offset:6144\n" \
"v_and_b32_e32 v68, v12, v68\n" \
"v_mfma_f32_16x16x32_bf16 v[116:119], a[152:155], a[40:43], v[116:119]\n" \
"v_and_b32_e32 v69, v13, v69\n" \
"v_and_b32_e32 v70, v14, v70\n" \
"v_mfma_f32_16x16x32_bf16 v[116:119], a[156:159], a[44:47], v[116:119]\n" \
"buffer_load_dwordx4 v50, s[8:11], s67 offen offset:0 sc1 lds\n" \
"v_mfma_f32_16x16x32_bf16 v[112:115], a[140:143], a[44:47], v[40:43]\n" \
"v_and_b32_e32 v71, v15, v71\n" \
"ds_read_b128 a[188:191], v49 offset:7168\n" \
"v_mfma_f32_16x16x32_bf16 v[112:115], a[136:139], a[40:43], v[112:115]\n" \
"v_exp_f32_e32 v86, v86\n" \
"v_mfma_f32_16x16x32_bf16 v[112:115], a[132:135], a[36:39], v[112:115]\n" \
"v_exp_f32_e32 v87, v87\n" \
"v_mfma_f32_16x16x32_bf16 v[112:115], a[128:131], a[32:35], v[112:115]\n" \
"v_exp_f32_e32 v84, v84\n" \
"v_mfma_f32_16x16x32_bf16 v[120:123], a[128:131], a[48:51], v[44:47]\n" \
"buffer_load_dwordx4 v50, s[8:11], s67 offen offset:1024 sc1 lds\n" \
"v_mfma_f32_16x16x32_bf16 v[120:123], a[132:135], a[52:55], v[120:123]\n" \
"v_exp_f32_e32 v85, v85\n" \
"v_mfma_f32_16x16x32_bf16 v[120:123], a[136:139], a[56:59], v[120:123]\n" \
"v_cvt_pk_bf16_f32 v87, v86, v87\n" \
"v_cvt_pk_bf16_f32 v86, v84, v85\n" \
"v_mfma_f32_16x16x32_bf16 v[120:123], a[140:143], a[60:63], v[120:123]\n" \
"v_exp_f32_e32 v82, v82\n" \
"v_mfma_f32_16x16x32_bf16 v[124:127], a[156:159], a[60:63], v[44:47]\n" \
"v_exp_f32_e32 v83, v83\n" \
"v_mfma_f32_16x16x32_bf16 v[124:127], a[152:155], a[56:59], v[124:127]\n" \
"v_exp_f32_e32 v80, v80\n" \
"v_mfma_f32_16x16x32_bf16 v[124:127], a[148:151], a[52:55], v[124:127]\n" \
"buffer_load_dwordx4 v50, s[8:11], s67 offen offset:2048 sc1 lds\n" \
"v_mfma_f32_16x16x32_bf16 v[124:127], a[144:147], a[48:51], v[124:127]\n" \
"v_cvt_pk_bf16_f32 v85, v82, v83\n" \
"s_waitcnt lgkmcnt(1)\n" \
"v_mfma_f32_16x16x32_bf16 v[128:131], a[160:163], v[68:71], v[128:131]\n" \
"v_exp_f32_e32 v81, v81\n" \
"v_mfma_f32_16x16x32_bf16 v[160:163], a[160:163], v[72:75], v[160:163]\n" \
"v_cvt_pk_bf16_f32 v84, v80, v81\n" \
"s_mov_b32 s64, s14\n" \
"v_mfma_f32_16x16x32_bf16 v[132:135], a[164:167], v[68:71], v[132:135]\n" \
"v_exp_f32_e32 v88, v88\n" \
"v_mfma_f32_16x16x32_bf16 v[164:167], a[164:167], v[72:75], v[164:167]\n" \
"v_exp_f32_e32 v89, v89\n" \
"v_mfma_f32_16x16x32_bf16 v[136:139], a[168:171], v[68:71], v[136:139]\n" \
"buffer_load_dwordx4 v50, s[8:11], s67 offen offset:3072 sc1 lds\n" \
"v_mfma_f32_16x16x32_bf16 v[168:171], a[168:171], v[72:75], v[168:171]\n" \
"v_exp_f32_e32 v90, v90\n" \
"v_mfma_f32_16x16x32_bf16 v[140:143], a[172:175], v[68:71], v[140:143]\n" \
"v_exp_f32_e32 v91, v91\n" \
"v_mfma_f32_16x16x32_bf16 v[172:175], a[172:175], v[72:75], v[172:175]\n" \
"v_cvt_pk_bf16_f32 v88, v88, v89\n" \
"v_cvt_pk_bf16_f32 v89, v90, v91\n" \
"v_mfma_f32_16x16x32_bf16 v[144:147], a[176:179], v[68:71], v[144:147]\n" \
"v_exp_f32_e32 v92, v92\n" \
"v_mfma_f32_16x16x32_bf16 v[176:179], a[176:179], v[72:75], v[176:179]\n" \
"v_exp_f32_e32 v93, v93\n" \
"v_mfma_f32_16x16x32_bf16 v[148:151], a[180:183], v[68:71], v[148:151]\n" \
"v_exp_f32_e32 v94, v94\n" \
"v_mfma_f32_16x16x32_bf16 v[180:183], a[180:183], v[72:75], v[180:183]\n" \
"v_exp_f32_e32 v95, v95\n" \
"v_mfma_f32_16x16x32_bf16 v[152:155], a[184:187], v[68:71], v[152:155]\n" \
"v_cvt_pk_bf16_f32 v90, v92, v93\n" \
"v_cvt_pk_bf16_f32 v91, v94, v95\n" \
"v_mfma_f32_16x16x32_bf16 v[184:187], a[184:187], v[72:75], v[184:187]\n" \
"v_add_u32_e32 v51, 0x12000, v63\n" \
"s_waitcnt lgkmcnt(0)\n" \
"v_mfma_f32_16x16x32_bf16 v[156:159], a[188:191], v[68:71], v[156:159]\n" \
"s_xor_b32 m0, s65, 0xa000\n" \
"ds_read_b32 v58, v18 offset:512\n" \
"v_mfma_f32_16x16x32_bf16 v[188:191], a[188:191], v[72:75], v[188:191]\n" \
"buffer_load_dwordx4 v51, s[8:11], s64 offen offset:0 sc1 lds\n" \
"v_smfmac_f32_16x16x64_bf16 a[192:195], a[200:203], v[68:75], v21\n" \
"buffer_load_dwordx4 v51, s[8:11], s64 offen offset:1024 sc1 lds\n" \
"v_mfma_f32_16x16x32_bf16 v[220:223], a[188:191], v[84:87], v[220:223]\n" \
"ds_read_b32 v59, v18 offset:640\n" \
"ds_read_b32 v60, v18 offset:768\n" \
"v_mfma_f32_16x16x32_bf16 v[252:255], a[188:191], v[88:91], v[252:255]\n" \
"ds_read_b32 v61, v18 offset:896\n" \
"ds_read_b128 a[128:131], v48 offset:0\n" \
"v_mfma_f32_16x16x32_bf16 v[216:219], a[184:187], v[84:87], v[216:219]\n" \
"ds_read_b128 a[144:147], v48 offset:4096\n" \
"ds_read_b128 a[132:135], v48 offset:1024\n" \
"v_mfma_f32_16x16x32_bf16 v[248:251], a[184:187], v[88:91], v[248:251]\n" \
"ds_read_b128 a[148:151], v48 offset:5120\n" \
"ds_read_b128 a[136:139], v48 offset:2048\n" \
"v_mfma_f32_16x16x32_bf16 v[212:215], a[180:183], v[84:87], v[212:215]\n" \
"ds_read_b128 a[152:155], v48 offset:6144\n" \
"ds_read_b128 a[140:143], v48 offset:3072\n" \
"v_mfma_f32_16x16x32_bf16 v[244:247], a[180:183], v[88:91], v[244:247]\n" \
"ds_read_b128 a[156:159], v48 offset:7168\n" \
"v_add_u32_e32 v52, 0x14000, v63\n" \
"v_mfma_f32_16x16x32_bf16 v[208:211], a[176:179], v[84:87], v[208:211]\n" \
"buffer_load_dwordx4 v51, s[8:11], s64 offen offset:2048 sc1 lds\n" \
"v_mfma_f32_16x16x32_bf16 v[240:243], a[176:179], v[88:91], v[240:243]\n" \
"v_exp_f32_e32 v104, v104\n" \
"v_mfma_f32_16x16x32_bf16 v[204:207], a[172:175], v[84:87], v[204:207]\n" \
"v_exp_f32_e32 v105, v105\n" \
"v_mfma_f32_16x16x32_bf16 v[236:239], a[172:175], v[88:91], v[236:239]\n" \
"buffer_load_dwordx4 v51, s[8:11], s64 offen offset:3072 sc1 lds\n" \
"v_mfma_f32_16x16x32_bf16 v[200:203], a[168:171], v[84:87], v[200:203]\n" \
"v_exp_f32_e32 v106, v106\n" \
"v_mfma_f32_16x16x32_bf16 v[232:235], a[168:171], v[88:91], v[232:235]\n" \
"v_exp_f32_e32 v107, v107\n" \
"v_mfma_f32_16x16x32_bf16 v[196:199], a[164:167], v[84:87], v[196:199]\n" \
"v_cvt_pk_bf16_f32 v104, v104, v105\n" \
"v_cvt_pk_bf16_f32 v105, v106, v107\n" \
"v_mfma_f32_16x16x32_bf16 v[228:231], a[164:167], v[88:91], v[228:231]\n" \
"v_exp_f32_e32 v108, v108\n" \
"v_mfma_f32_16x16x32_bf16 v[192:195], a[160:163], v[84:87], v[192:195]\n" \
"v_exp_f32_e32 v109, v109\n" \
"v_mfma_f32_16x16x32_bf16 v[224:227], a[160:163], v[88:91], v[224:227]\n" \
"v_exp_f32_e32 v110, v110\n" \
"v_smfmac_f32_16x16x64_bf16 a[196:199], a[200:203], v[84:91], v21\n" \
"v_exp_f32_e32 v111, v111\n" \
"v_cvt_pk_bf16_f32 v106, v108, v109\n" \
"v_cvt_pk_bf16_f32 v107, v110, v111\n" \
"v_and_b32_e32 v104, v12, v104\n" \
"v_and_b32_e32 v105, v13, v105\n" \
"v_and_b32_e32 v106, v14, v106\n" \
"v_and_b32_e32 v107, v15, v107\n" \
"v_accvgpr_read_b32 v54, a192\n" \
"v_accvgpr_read_b32 v22, a193\n" \
"v_add_u32_e32 v53, 0x20000, v63\n" \
"v_exp_f32_e32 v118, v118\n" \
"v_exp_f32_e32 v119, v119\n" \
"v_exp_f32_e32 v116, v116\n" \
"v_exp_f32_e32 v117, v117\n" \
"v_cvt_pk_bf16_f32 v119, v118, v119\n" \
"v_exp_f32_e32 v114, v114\n" \
"v_exp_f32_e32 v115, v115\n" \
"v_exp_f32_e32 v112, v112\n" \
"v_exp_f32_e32 v113, v113\n" \
"v_cvt_pk_bf16_f32 v118, v116, v117\n" \
"v_cvt_pk_bf16_f32 v117, v114, v115\n" \
"v_cvt_pk_bf16_f32 v116, v112, v113\n" \
"v_readlane_b32 s93, v11, 19\n" \
"s_waitcnt vmcnt(10) lgkmcnt(0)\n" \
"s_barrier\n" \
"v_mfma_f32_16x16x32_bf16 v[80:83], a[128:131], a[32:35], v[40:43]\n" \
"ds_read_b128 a[160:163], v49 offset:8192\n" \
"ds_read_b128 a[164:167], v49 offset:9216\n" \
"v_mfma_f32_16x16x32_bf16 v[80:83], a[132:135], a[36:39], v[80:83]\n" \
"ds_read_b128 a[168:171], v49 offset:10240\n" \
"s_xor_b32 m0, s65, 0x18000\n" \
"v_mfma_f32_16x16x32_bf16 v[80:83], a[136:139], a[40:43], v[80:83]\n" \
"buffer_load_dwordx4 v52, s[8:11], s64 offen offset:0 sc1 lds\n" \
"v_mfma_f32_16x16x32_bf16 v[80:83], a[140:143], a[44:47], v[80:83]\n" \
"ds_read_b128 a[172:175], v49 offset:11264\n" \
"ds_read_b128 a[176:179], v49 offset:12288\n" \
"v_mfma_f32_16x16x32_bf16 v[84:87], a[156:159], a[44:47], v[40:43]\n" \
"ds_read_b128 a[180:183], v49 offset:13312\n" \
"ds_read_b128 a[184:187], v49 offset:14336\n" \
"v_mfma_f32_16x16x32_bf16 v[84:87], a[152:155], a[40:43], v[84:87]\n" \
"buffer_load_dwordx4 v52, s[8:11], s64 offen offset:1024 sc1 lds\n" \
"v_mfma_f32_16x16x32_bf16 v[84:87], a[148:151], a[36:39], v[84:87]\n" \
"ds_read_b128 a[188:191], v49 offset:15360\n" \
"v_readlane_b32 s80, v11, 11\n" \
"v_mfma_f32_16x16x32_bf16 v[84:87], a[144:147], a[32:35], v[84:87]\n" \
"buffer_load_dwordx4 v52, s[8:11], s64 offen offset:2048 sc1 lds\n" \
"v_mfma_f32_16x16x32_bf16 v[92:95], a[144:147], a[48:51], v[44:47]\n" \
"v_exp_f32_e32 v120, v120\n" \
"v_mfma_f32_16x16x32_bf16 v[92:95], a[148:151], a[52:55], v[92:95]\n" \
"v_exp_f32_e32 v121, v121\n" \
"v_mfma_f32_16x16x32_bf16 v[92:95], a[152:155], a[56:59], v[92:95]\n" \
"buffer_load_dwordx4 v52, s[8:11], s64 offen offset:3072 sc1 lds\n" \
"v_mfma_f32_16x16x32_bf16 v[92:95], a[156:159], a[60:63], v[92:95]\n" \
"s_mov_b32 m0, s65\n" \
"v_cvt_pk_bf16_f32 v120, v120, v121\n" \
"v_mfma_f32_16x16x32_bf16 v[88:91], a[140:143], a[60:63], v[44:47]\n" \
"v_exp_f32_e32 v122, v122\n" \
"v_mfma_f32_16x16x32_bf16 v[88:91], a[136:139], a[56:59], v[88:91]\n" \
"buffer_load_dwordx4 v53, s[8:11], s64 offen offset:0 sc1 lds\n" \
"v_mfma_f32_16x16x32_bf16 v[88:91], a[132:135], a[52:55], v[88:91]\n" \
"v_exp_f32_e32 v123, v123\n" \
"v_mfma_f32_16x16x32_bf16 v[88:91], a[128:131], a[48:51], v[88:91]\n" \
"v_exp_f32_e32 v124, v124\n" \
"v_mfma_f32_16x16x32_bf16 a[192:195], a[212:215], v[104:107], a[192:195]\n" \
"v_cvt_pk_bf16_f32 v121, v122, v123\n" \
"s_waitcnt lgkmcnt(5)\n" \
"v_mfma_f32_16x16x32_bf16 v[160:163], a[160:163], v[104:107], v[160:163]\n" \
"buffer_load_dwordx4 v53, s[8:11], s64 offen offset:1024 sc1 lds\n" \
"v_mfma_f32_16x16x32_bf16 v[164:167], a[164:167], v[104:107], v[164:167]\n" \
"v_exp_f32_e32 v125, v125\n" \
"v_mfma_f32_16x16x32_bf16 v[168:171], a[168:171], v[104:107], v[168:171]\n" \
"v_cvt_pk_bf16_f32 v122, v124, v125\n" \
"s_waitcnt lgkmcnt(1)\n" \
"v_mfma_f32_16x16x32_bf16 v[172:175], a[172:175], v[104:107], v[172:175]\n" \
"v_exp_f32_e32 v126, v126\n" \
"v_mfma_f32_16x16x32_bf16 v[176:179], a[176:179], v[104:107], v[176:179]\n" \
"v_exp_f32_e32 v127, v127\n" \
"v_mfma_f32_16x16x32_bf16 v[180:183], a[180:183], v[104:107], v[180:183]\n" \
"v_cvt_pk_bf16_f32 v123, v126, v127\n" \
"v_readlane_b32 s81, v11, 12\n" \
"v_mfma_f32_16x16x32_bf16 v[184:187], a[184:187], v[104:107], v[184:187]\n" \
"s_mov_b32 s82, 0x47fca0\n" \
"s_waitcnt lgkmcnt(0)\n" \
"v_mfma_f32_16x16x32_bf16 v[188:191], a[188:191], v[104:107], v[188:191]\n" \
"buffer_load_dwordx4 v53, s[8:11], s64 offen offset:2048 sc1 lds\n" \
"v_mfma_f32_16x16x32_bf16 v[220:223], a[188:191], v[116:119], v[220:223]\n" \
"buffer_load_dwordx4 v53, s[8:11], s64 offen offset:3072 sc1 lds\n" \
"v_mfma_f32_16x16x32_bf16 v[252:255], a[188:191], v[120:123], v[252:255]\n" \
"s_mov_b32 s83, 0x20000\n" \
"v_readlane_b32 s92, v11, 25\n" \
"v_mfma_f32_16x16x32_bf16 v[216:219], a[184:187], v[116:119], v[216:219]\n" \
"s_add_u32 m0, s93, 0x0\n" \
"ds_read_b128 a[128:131], v48 offset:8192\n" \
"v_mfma_f32_16x16x32_bf16 v[248:251], a[184:187], v[120:123], v[248:251]\n" \
"ds_read_b128 a[144:147], v48 offset:12288\n" \
"ds_read_b128 a[132:135], v48 offset:9216\n" \
"v_mfma_f32_16x16x32_bf16 v[212:215], a[180:183], v[116:119], v[212:215]\n" \
"buffer_load_dwordx4 v0, s[80:83], s92 offen offset:0 lds\n" \
"v_mfma_f32_16x16x32_bf16 v[244:247], a[180:183], v[120:123], v[244:247]\n" \
"buffer_load_dwordx4 v1, s[80:83], s92 offen offset:1024 lds\n" \
"v_mfma_f32_16x16x32_bf16 v[208:211], a[176:179], v[116:119], v[208:211]\n" \
"ds_read_b128 a[148:151], v48 offset:13312\n" \
"ds_read_b128 a[136:139], v48 offset:10240\n" \
"v_mfma_f32_16x16x32_bf16 v[240:243], a[176:179], v[120:123], v[240:243]\n" \
"ds_read_b128 a[152:155], v48 offset:14336\n" \
"ds_read_b128 a[140:143], v48 offset:11264\n" \
"v_mfma_f32_16x16x32_bf16 v[204:207], a[172:175], v[116:119], v[204:207]\n" \
"buffer_load_dwordx4 v2, s[80:83], s92 offen offset:2048 lds\n" \
"v_mfma_f32_16x16x32_bf16 v[236:239], a[172:175], v[120:123], v[236:239]\n" \
"ds_read_b128 a[156:159], v48 offset:15360\n" \
"s_xor_b32 s65, s65, 0x8000\n" \
"v_mfma_f32_16x16x32_bf16 v[200:203], a[168:171], v[116:119], v[200:203]\n" \
"s_add_u32 s64, s64, 0x10000\n" \
"v_add_u32_e32 v49, 0x10000, v48\n" \
"v_mfma_f32_16x16x32_bf16 v[232:235], a[168:171], v[120:123], v[232:235]\n" \
"buffer_load_dwordx4 v3, s[80:83], s92 offen offset:3072 lds\n" \
"v_mfma_f32_16x16x32_bf16 v[196:199], a[164:167], v[116:119], v[196:199]\n" \
"v_exp_f32_e32 v86, v86\n" \
"v_mfma_f32_16x16x32_bf16 v[228:231], a[164:167], v[120:123], v[228:231]\n" \
"v_exp_f32_e32 v87, v87\n" \
"v_mfma_f32_16x16x32_bf16 v[192:195], a[160:163], v[116:119], v[192:195]\n" \
"v_exp_f32_e32 v84, v84\n" \
"v_mfma_f32_16x16x32_bf16 v[224:227], a[160:163], v[120:123], v[224:227]\n" \
"v_exp_f32_e32 v85, v85\n" \
"v_smfmac_f32_16x16x64_bf16 a[196:199], a[200:203], v[116:123], v21\n" \
"v_exp_f32_e32 v88, v88\n" \
"v_exp_f32_e32 v89, v89\n" \
"v_cvt_pk_bf16_f32 v87, v86, v87\n" \
"v_exp_f32_e32 v82, v82\n" \
"v_exp_f32_e32 v83, v83\n" \
"v_exp_f32_e32 v80, v80\n" \
"v_exp_f32_e32 v81, v81\n" \
"v_exp_f32_e32 v90, v90\n" \
"v_exp_f32_e32 v91, v91\n" \
"v_cvt_pk_bf16_f32 v86, v84, v85\n" \
"v_cvt_pk_bf16_f32 v88, v88, v89\n" \
"v_exp_f32_e32 v92, v92\n" \
"v_exp_f32_e32 v93, v93\n" \
"v_exp_f32_e32 v94, v94\n" \
"v_exp_f32_e32 v95, v95\n" \
"v_cvt_pk_bf16_f32 v85, v82, v83\n" \
"v_cvt_pk_bf16_f32 v84, v80, v81\n" \
"v_cvt_pk_bf16_f32 v89, v90, v91\n" \
"v_and_b32_e32 v84, v12, v84\n" \
"v_and_b32_e32 v85, v13, v85\n" \
"v_and_b32_e32 v86, v14, v86\n" \
"v_and_b32_e32 v87, v15, v87\n" \
"v_cvt_pk_bf16_f32 v90, v92, v93\n" \
"v_cvt_pk_bf16_f32 v91, v94, v95\n" \
"v_add_u32_e32 v48, s16, v20\n" \
"v_add_f32_e32 v54, v54, v22\n" \
"v_accvgpr_read_b32 v55, a194\n" \
"v_accvgpr_read_b32 v23, a195\n" \
"v_mfma_f32_16x16x32_bf16 a[192:195], a[216:219], a[216:219], 0\n" \
"v_max_f32_e32 v62, v62, v54\n" \
"v_add_f32_e32 v55, v55, v23\n" \
"v_max_f32_e32 v62, v62, v55\n" \
"v_rcp_f32_e32 v54, v54\n" \
"v_rcp_f32_e32 v55, v55\n" \
"v_mul_f32_e32 v54, v54, v58\n" \
"v_mul_f32_e32 v55, v55, v59\n" \
"v_mul_f32_e32 v128, v54, v128\n" \
"v_mul_f32_e32 v129, v54, v129\n" \
"v_mul_f32_e32 v136, v54, v136\n" \
"v_mul_f32_e32 v137, v54, v137\n" \
"v_mul_f32_e32 v144, v54, v144\n" \
"v_mul_f32_e32 v145, v54, v145\n" \
"v_mul_f32_e32 v152, v54, v152\n" \
"v_mul_f32_e32 v153, v54, v153\n" \
"v_mul_f32_e32 v160, v55, v160\n" \
"v_mul_f32_e32 v161, v55, v161\n" \
"v_mul_f32_e32 v168, v55, v168\n" \
"v_mul_f32_e32 v169, v55, v169\n" \
"v_mul_f32_e32 v176, v55, v176\n" \
"v_mul_f32_e32 v177, v55, v177\n" \
"v_mul_f32_e32 v184, v55, v184\n" \
"v_mul_f32_e32 v185, v55, v185\n" \
"v_cvt_pk_bf16_f32 v128, v128, v129\n" \
"v_mul_f32_e32 v130, v54, v130\n" \
"v_mul_f32_e32 v131, v54, v131\n" \
"v_cvt_pk_bf16_f32 v136, v136, v137\n" \
"v_mul_f32_e32 v138, v54, v138\n" \
"v_mul_f32_e32 v139, v54, v139\n" \
"v_cvt_pk_bf16_f32 v144, v144, v145\n" \
"v_mul_f32_e32 v146, v54, v146\n" \
"v_mul_f32_e32 v147, v54, v147\n" \
"v_cvt_pk_bf16_f32 v152, v152, v153\n" \
"v_mul_f32_e32 v154, v54, v154\n" \
"v_mul_f32_e32 v155, v54, v155\n" \
"v_cvt_pk_bf16_f32 v160, v160, v161\n" \
"v_mul_f32_e32 v162, v55, v162\n" \
"v_mul_f32_e32 v163, v55, v163\n" \
"v_cvt_pk_bf16_f32 v168, v168, v169\n" \
"v_mul_f32_e32 v170, v55, v170\n" \
"v_mul_f32_e32 v171, v55, v171\n" \
"v_cvt_pk_bf16_f32 v176, v176, v177\n" \
"v_mul_f32_e32 v178, v55, v178\n" \
"v_mul_f32_e32 v179, v55, v179\n" \
"v_cvt_pk_bf16_f32 v184, v184, v185\n" \
"v_mul_f32_e32 v186, v55, v186\n" \
"v_mul_f32_e32 v187, v55, v187\n" \
"v_cvt_pk_bf16_f32 v129, v130, v131\n" \
"v_mul_f32_e32 v132, v54, v132\n" \
"v_mul_f32_e32 v133, v54, v133\n" \
"v_mul_f32_e32 v134, v54, v134\n" \
"v_mul_f32_e32 v135, v54, v135\n" \
"v_cvt_pk_bf16_f32 v137, v138, v139\n" \
"v_mul_f32_e32 v140, v54, v140\n" \
"s_waitcnt vmcnt(12) lgkmcnt(0)\n" \
"s_barrier\n" \
"v_mfma_f32_16x16x32_bf16 v[120:123], a[128:131], a[48:51], v[44:47]\n" \
"s_xor_b32 m0, s65, 0x12000\n" \
"ds_read_b128 a[188:191], v49 offset:7168\n" \
"v_mfma_f32_16x16x32_bf16 v[120:123], a[132:135], a[52:55], v[120:123]\n" \
"buffer_load_dwordx4 v50, s[8:11], s64 offen offset:0 sc1 lds\n" \
"v_mfma_f32_16x16x32_bf16 v[120:123], a[136:139], a[56:59], v[120:123]\n" \
"ds_read_b128 a[184:187], v49 offset:6144\n" \
"ds_read_b128 a[180:183], v49 offset:5120\n" \
"v_mfma_f32_16x16x32_bf16 v[120:123], a[140:143], a[60:63], v[120:123]\n" \
"buffer_load_dwordx4 v50, s[8:11], s64 offen offset:1024 sc1 lds\n" \
"v_mfma_f32_16x16x32_bf16 v[124:127], a[156:159], a[60:63], v[44:47]\n" \
"ds_read_b128 a[176:179], v49 offset:4096\n" \
"ds_read_b128 a[172:175], v49 offset:3072\n" \
"v_mfma_f32_16x16x32_bf16 v[124:127], a[152:155], a[56:59], v[124:127]\n" \
"ds_read_b128 a[168:171], v49 offset:2048\n" \
"ds_read_b128 a[164:167], v49 offset:1024\n" \
"v_mfma_f32_16x16x32_bf16 v[124:127], a[148:151], a[52:55], v[124:127]\n" \
"ds_read_b128 a[160:163], v49 offset:0\n" \
"ds_read_b128 a[0:3], v49 offset:8192\n" \
"v_mfma_f32_16x16x32_bf16 v[124:127], a[144:147], a[48:51], v[124:127]\n" \
"buffer_load_dwordx4 v50, s[8:11], s64 offen offset:2048 sc1 lds\n" \
"v_mfma_f32_16x16x32_bf16 v[32:35], a[220:223], a[64:67], 0\n" \
"ds_read_b128 a[4:7], v49 offset:9216\n" \
"ds_read_b128 a[8:11], v49 offset:10240\n" \
"v_mfma_f32_16x16x32_bf16 v[36:39], a[220:223], a[80:83], 0\n" \
"ds_read_b128 a[12:15], v49 offset:11264\n" \
"ds_read_b128 a[16:19], v49 offset:12288\n" \
"v_mfma_f32_16x16x32_bf16 v[40:43], a[220:223], a[96:99], 0\n" \
"ds_read_b128 a[20:23], v49 offset:13312\n" \
"ds_read_b128 a[24:27], v49 offset:14336\n" \
"v_mfma_f32_16x16x32_bf16 v[44:47], a[220:223], a[112:115], 0\n" \
"buffer_load_dwordx4 v50, s[8:11], s64 offen offset:3072 sc1 lds\n" \
"v_mfma_f32_16x16x32_bf16 v[32:35], a[224:227], a[68:71], v[32:35]\n" \
"s_xor_b32 m0, s65, 0xa000\n" \
"ds_read_b128 a[28:31], v49 offset:15360\n" \
"v_mfma_f32_16x16x32_bf16 v[36:39], a[224:227], a[84:87], v[36:39]\n" \
"v_exp_f32_e32 v120, v120\n" \
"v_mfma_f32_16x16x32_bf16 v[40:43], a[224:227], a[100:103], v[40:43]\n" \
"buffer_load_dwordx4 v51, s[8:11], s64 offen offset:0 sc1 lds\n" \
"v_mfma_f32_16x16x32_bf16 v[44:47], a[224:227], a[116:119], v[44:47]\n" \
"v_exp_f32_e32 v121, v121\n" \
"v_mfma_f32_16x16x32_bf16 v[32:35], a[228:231], a[72:75], v[32:35]\n" \
"v_exp_f32_e32 v122, v122\n" \
"v_mfma_f32_16x16x32_bf16 v[36:39], a[228:231], a[88:91], v[36:39]\n" \
"v_exp_f32_e32 v123, v123\n" \
"v_mfma_f32_16x16x32_bf16 v[40:43], a[228:231], a[104:107], v[40:43]\n" \
"buffer_load_dwordx4 v51, s[8:11], s64 offen offset:1024 sc1 lds\n" \
"v_mfma_f32_16x16x32_bf16 v[44:47], a[228:231], a[120:123], v[44:47]\n" \
"v_cvt_pk_bf16_f32 v120, v120, v121\n" \
"v_cvt_pk_bf16_f32 v121, v122, v123\n" \
"v_mfma_f32_16x16x32_bf16 v[32:35], a[232:235], a[76:79], v[32:35]\n" \
"v_exp_f32_e32 v124, v124\n" \
"v_mfma_f32_16x16x32_bf16 v[36:39], a[232:235], a[92:95], v[36:39]\n" \
"v_exp_f32_e32 v125, v125\n" \
"v_mfma_f32_16x16x32_bf16 v[40:43], a[232:235], a[108:111], v[40:43]\n" \
"buffer_load_dwordx4 v51, s[8:11], s64 offen offset:2048 sc1 lds\n" \
"v_mfma_f32_16x16x32_bf16 v[44:47], a[232:235], a[124:127], v[44:47]\n" \
"v_exp_f32_e32 v126, v126\n" \
"v_smfmac_f32_16x16x64_bf16 a[196:199], a[200:203], v[84:91], v21\n" \
"v_cvt_pk_bf16_f32 v122, v124, v125\n" \
"s_waitcnt lgkmcnt(1)\n" \
"v_mfma_f32_16x16x32_bf16 v[220:223], a[188:191], v[84:87], v[220:223]\n" \
"buffer_load_dwordx4 v51, s[8:11], s64 offen offset:3072 sc1 lds\n" \
"v_mfma_f32_16x16x32_bf16 v[252:255], a[188:191], v[88:91], v[252:255]\n" \
"s_add_u32 m0, s93, 0x1000\n" \
"v_and_b32_e32 v120, v12, v120\n" \
"v_mfma_f32_16x16x32_bf16 v[216:219], a[184:187], v[84:87], v[216:219]\n" \
"v_exp_f32_e32 v127, v127\n" \
"v_mfma_f32_16x16x32_bf16 v[248:251], a[184:187], v[88:91], v[248:251]\n" \
"v_cvt_pk_bf16_f32 v123, v126, v127\n" \
"v_and_b32_e32 v121, v13, v121\n" \
"v_mfma_f32_16x16x32_bf16 v[212:215], a[180:183], v[84:87], v[212:215]\n" \
"buffer_load_dwordx4 v4, s[80:83], s92 offen offset:0 lds\n" \
"v_mfma_f32_16x16x32_bf16 v[244:247], a[180:183], v[88:91], v[244:247]\n" \
"v_and_b32_e32 v122, v14, v122\n" \
"v_and_b32_e32 v123, v15, v123\n" \
"v_mfma_f32_16x16x32_bf16 v[208:211], a[176:179], v[84:87], v[208:211]\n" \
"v_accvgpr_read_b32 v56, a196\n" \
"v_accvgpr_read_b32 v24, a197\n" \
"v_mfma_f32_16x16x32_bf16 v[240:243], a[176:179], v[88:91], v[240:243]\n" \
"ds_read_b128 a[128:131], v48 offset:0\n" \
"ds_read_b128 a[144:147], v48 offset:4096\n" \
"v_mfma_f32_16x16x32_bf16 v[204:207], a[172:175], v[84:87], v[204:207]\n" \
"buffer_load_dwordx4 v5, s[80:83], s92 offen offset:1024 lds\n" \
"v_mfma_f32_16x16x32_bf16 v[236:239], a[172:175], v[88:91], v[236:239]\n" \
"ds_read_b128 a[132:135], v48 offset:1024\n" \
"ds_read_b128 a[148:151], v48 offset:5120\n" \
"v_mfma_f32_16x16x32_bf16 v[200:203], a[168:171], v[84:87], v[200:203]\n" \
"ds_read_b128 a[136:139], v48 offset:2048\n" \
"ds_read_b128 a[152:155], v48 offset:6144\n" \
"v_mfma_f32_16x16x32_bf16 v[232:235], a[168:171], v[88:91], v[232:235]\n" \
"buffer_load_dwordx4 v6, s[80:83], s92 offen offset:2048 lds\n" \
"v_mfma_f32_16x16x32_bf16 v[196:199], a[164:167], v[84:87], v[196:199]\n" \
"ds_read_b128 a[140:143], v48 offset:3072\n" \
"ds_read_b128 a[156:159], v48 offset:7168\n" \
"v_mfma_f32_16x16x32_bf16 v[228:231], a[164:167], v[88:91], v[228:231]\n" \
"v_mul_f32_e32 v141, v54, v141\n" \
"v_mul_f32_e32 v142, v54, v142\n" \
"v_mfma_f32_16x16x32_bf16 v[192:195], a[160:163], v[84:87], v[192:195]\n" \
"v_mul_f32_e32 v143, v54, v143\n" \
"v_cvt_pk_bf16_f32 v145, v146, v147\n" \
"v_mfma_f32_16x16x32_bf16 v[224:227], a[160:163], v[88:91], v[224:227]\n" \
"buffer_load_dwordx4 v7, s[80:83], s92 offen offset:3072 lds\n" \
"v_mfma_f32_16x16x32_bf16 a[196:199], a[212:215], v[120:123], a[196:199]\n" \
"v_mul_f32_e32 v148, v54, v148\n" \
"v_mul_f32_e32 v149, v54, v149\n" \
"v_mfma_f32_16x16x32_bf16 v[224:227], a[0:3], v[120:123], v[224:227]\n" \
"v_mul_f32_e32 v150, v54, v150\n" \
"v_mul_f32_e32 v151, v54, v151\n" \
"v_mfma_f32_16x16x32_bf16 v[228:231], a[4:7], v[120:123], v[228:231]\n" \
"v_cvt_pk_bf16_f32 v153, v154, v155\n" \
"v_mul_f32_e32 v156, v54, v156\n" \
"v_mfma_f32_16x16x32_bf16 v[232:235], a[8:11], v[120:123], v[232:235]\n" \
"v_mul_f32_e32 v157, v54, v157\n" \
"v_mul_f32_e32 v158, v54, v158\n" \
"v_mfma_f32_16x16x32_bf16 v[236:239], a[12:15], v[120:123], v[236:239]\n" \
"v_mul_f32_e32 v159, v54, v159\n" \
"v_cvt_pk_bf16_f32 v161, v162, v163\n" \
"v_mfma_f32_16x16x32_bf16 v[240:243], a[16:19], v[120:123], v[240:243]\n" \
"v_mul_f32_e32 v164, v55, v164\n" \
"v_mul_f32_e32 v165, v55, v165\n" \
"v_mfma_f32_16x16x32_bf16 v[244:247], a[20:23], v[120:123], v[244:247]\n" \
"v_mul_f32_e32 v166, v55, v166\n" \
"v_mul_f32_e32 v167, v55, v167\n" \
"v_mfma_f32_16x16x32_bf16 v[248:251], a[24:27], v[120:123], v[248:251]\n" \
"v_cvt_pk_bf16_f32 v169, v170, v171\n" \
"s_waitcnt lgkmcnt(4)\n" \
"v_mfma_f32_16x16x32_bf16 v[252:255], a[28:31], v[120:123], v[252:255]\n" \
"v_mul_f32_e32 v172, v55, v172\n" \
"v_mul_f32_e32 v173, v55, v173\n" \
"v_mul_f32_e32 v174, v55, v174\n" \
"v_mul_f32_e32 v175, v55, v175\n" \
"v_cvt_pk_bf16_f32 v177, v178, v179\n" \
"v_mul_f32_e32 v180, v55, v180\n" \
"v_mul_f32_e32 v181, v55, v181\n" \
"v_mul_f32_e32 v182, v55, v182\n" \
"v_mul_f32_e32 v183, v55, v183\n" \
"v_cvt_pk_bf16_f32 v185, v186, v187\n" \
"v_mul_f32_e32 v188, v55, v188\n" \
"v_mul_f32_e32 v189, v55, v189\n" \
"v_mul_f32_e32 v190, v55, v190\n" \
"v_mul_f32_e32 v191, v55, v191\n" \
"v_cvt_pk_bf16_f32 v130, v132, v133\n" \
"v_cvt_pk_bf16_f32 v131, v134, v135\n" \
"buffer_store_dwordx4 v[128:131], v20, s[56:59], s68 offen offset:0 sc1\n" \
"v_cvt_pk_bf16_f32 v138, v140, v141\n" \
"v_cvt_pk_bf16_f32 v139, v142, v143\n" \
"buffer_store_dwordx4 v[136:139], v20, s[56:59], s68 offen offset:1024 sc1\n" \
"v_cvt_pk_bf16_f32 v146, v148, v149\n" \
"v_cvt_pk_bf16_f32 v147, v150, v151\n" \
"buffer_store_dwordx4 v[144:147], v20, s[56:59], s68 offen offset:2048 sc1\n" \
"v_cvt_pk_bf16_f32 v154, v156, v157\n" \
"v_cvt_pk_bf16_f32 v155, v158, v159\n" \
"buffer_store_dwordx4 v[152:155], v20, s[56:59], s68 offen offset:3072 sc1\n" \
"v_cvt_pk_bf16_f32 v162, v164, v165\n" \
"v_cvt_pk_bf16_f32 v163, v166, v167\n" \
"buffer_store_dwordx4 v[160:163], v20, s[56:59], s69 offen offset:0 sc1\n" \
"v_cvt_pk_bf16_f32 v170, v172, v173\n" \
"v_cvt_pk_bf16_f32 v171, v174, v175\n" \
"buffer_store_dwordx4 v[168:171], v20, s[56:59], s69 offen offset:1024 sc1\n" \
"v_cvt_pk_bf16_f32 v178, v180, v181\n" \
"v_cvt_pk_bf16_f32 v179, v182, v183\n" \
"buffer_store_dwordx4 v[176:179], v20, s[56:59], s69 offen offset:2048 sc1\n" \
"v_cvt_pk_bf16_f32 v186, v188, v189\n" \
"v_cvt_pk_bf16_f32 v187, v190, v191\n" \
"buffer_store_dwordx4 v[184:187], v20, s[56:59], s69 offen offset:3072 sc1\n" \
"s_add_u32 m0, s93, 0x2000\n" \
"v_mfma_f32_16x16x32_bf16 a[208:211], a[216:219], v[128:131], a[208:211]\n" \
"buffer_load_dwordx4 v8, s[80:83], s92 offen offset:0 lds\n" \
"v_mfma_f32_16x16x32_bf16 a[208:211], a[216:219], v[136:139], a[208:211]\n" \
"v_mfma_f32_32x32x16_bf16 v[128:143], a[216:219], a[216:219], 0\n" \
"v_mfma_f32_16x16x32_bf16 a[208:211], a[216:219], v[144:147], a[208:211]\n" \
"v_mfma_f32_16x16x32_bf16 a[208:211], a[216:219], v[152:155], a[208:211]\n" \
"v_mfma_f32_32x32x16_bf16 v[144:159], a[216:219], a[216:219], 0\n" \
"v_add_f32_e32 v56, v56, v24\n" \
"v_mfma_f32_16x16x32_bf16 a[208:211], a[216:219], v[160:163], a[208:211]\n" \
"v_max_f32_e32 v62, v62, v56\n" \
"v_accvgpr_read_b32 v57, a198\n" \
"s_waitcnt vmcnt(21) lgkmcnt(0)\n" \
"s_barrier\n" \
"v_mfma_f32_16x16x32_bf16 v[64:67], a[128:131], a[64:67], v[32:35]\n" \
"s_xor_b32 m0, s65, 0x18000\n" \
"v_accvgpr_read_b32 v25, a199\n" \
"v_mfma_f32_16x16x32_bf16 a[196:199], a[216:219], a[216:219], 0\n" \
"buffer_load_dwordx4 v52, s[8:11], s64 offen offset:0 sc1 lds\n" \
"v_mfma_f32_16x16x32_bf16 v[64:67], a[132:135], a[68:71], v[64:67]\n" \
"v_rcp_f32_e32 v56, v56\n" \
"v_mfma_f32_16x16x32_bf16 v[64:67], a[136:139], a[72:75], v[64:67]\n" \
"v_mfma_f32_16x16x32_bf16 a[208:211], a[216:219], v[168:171], a[208:211]\n" \
"v_mfma_f32_32x32x16_bf16 v[160:175], a[216:219], a[216:219], 0\n" \
"v_mul_f32_e32 v56, v56, v60\n" \
"v_mfma_f32_16x16x32_bf16 a[208:211], a[216:219], v[176:179], a[208:211]\n" \
"v_mul_f32_e32 v192, v56, v192\n" \
"v_mul_f32_e32 v193, v56, v193\n" \
"v_mfma_f32_16x16x32_bf16 v[64:67], a[140:143], a[76:79], v[64:67]\n" \
"buffer_load_dwordx4 v52, s[8:11], s64 offen offset:1024 sc1 lds\n" \
"v_mfma_f32_16x16x32_bf16 v[68:71], a[156:159], a[76:79], v[32:35]\n" \
"v_cvt_pk_bf16_f32 v192, v192, v193\n" \
"v_mul_f32_e32 v194, v56, v194\n" \
"v_mfma_f32_16x16x32_bf16 v[68:71], a[152:155], a[72:75], v[68:71]\n" \
"v_mul_f32_e32 v195, v56, v195\n" \
"v_cvt_pk_bf16_f32 v193, v194, v195\n" \
"v_mfma_f32_16x16x32_bf16 v[68:71], a[148:151], a[68:71], v[68:71]\n" \
"v_mfma_f32_16x16x32_bf16 a[208:211], a[216:219], v[184:187], a[208:211]\n" \
"v_mfma_f32_32x32x16_bf16 v[176:191], a[216:219], a[216:219], 0\n" \
"v_mul_f32_e32 v196, v56, v196\n" \
"v_mul_f32_e32 v197, v56, v197\n" \
"v_mul_f32_e32 v198, v56, v198\n" \
"v_mul_f32_e32 v199, v56, v199\n" \
"v_mul_f32_e32 v200, v56, v200\n" \
"v_mul_f32_e32 v201, v56, v201\n" \
"v_mfma_f32_16x16x32_bf16 v[68:71], a[144:147], a[64:67], v[68:71]\n" \
"buffer_load_dwordx4 v52, s[8:11], s64 offen offset:2048 sc1 lds\n" \
"v_mfma_f32_16x16x32_bf16 v[76:79], a[144:147], a[80:83], v[36:39]\n" \
"v_cvt_pk_bf16_f32 v194, v196, v197\n" \
"v_cvt_pk_bf16_f32 v195, v198, v199\n" \
"buffer_store_dwordx4 v[192:195], v20, s[56:59], s70 offen offset:0 sc1\n" \
"v_mfma_f32_16x16x32_bf16 v[76:79], a[148:151], a[84:87], v[76:79]\n" \
"v_cvt_pk_bf16_f32 v200, v200, v201\n" \
"v_mul_f32_e32 v202, v56, v202\n" \
"v_mfma_f32_16x16x32_bf16 v[76:79], a[152:155], a[88:91], v[76:79]\n" \
"v_mul_f32_e32 v203, v56, v203\n" \
"v_add_f32_e32 v57, v57, v25\n" \
"v_mfma_f32_16x16x32_bf16 v[76:79], a[156:159], a[92:95], v[76:79]\n" \
"buffer_load_dwordx4 v52, s[8:11], s64 offen offset:3072 sc1 lds\n" \
"v_mfma_f32_16x16x32_bf16 v[72:75], a[140:143], a[92:95], v[36:39]\n" \
"s_mov_b32 m0, s65\n" \
"v_cvt_pk_bf16_f32 v201, v202, v203\n" \
"v_mfma_f32_16x16x32_bf16 v[72:75], a[136:139], a[88:91], v[72:75]\n" \
"v_mfma_f32_16x16x32_bf16 a[208:211], a[216:219], v[192:195], a[208:211]\n" \
"v_mul_f32_e32 v204, v56, v204\n" \
"v_mul_f32_e32 v205, v56, v205\n" \
"v_mfma_f32_16x16x32_bf16 v[72:75], a[132:135], a[84:87], v[72:75]\n" \
"v_mul_f32_e32 v206, v56, v206\n" \
"v_mul_f32_e32 v207, v56, v207\n" \
"v_mfma_f32_16x16x32_bf16 v[72:75], a[128:131], a[80:83], v[72:75]\n" \
"buffer_load_dwordx4 v53, s[8:11], s64 offen offset:0 sc1 lds\n" \
"v_mfma_f32_16x16x32_bf16 v[80:83], a[128:131], a[96:99], v[40:43]\n" \
"v_mul_f32_e32 v208, v56, v208\n" \
"v_mul_f32_e32 v209, v56, v209\n" \
"v_mfma_f32_16x16x32_bf16 v[80:83], a[132:135], a[100:103], v[80:83]\n" \
"v_max_f32_e32 v62, v62, v57\n" \
"v_cvt_pk_bf16_f32 v202, v204, v205\n" \
"v_mfma_f32_16x16x32_bf16 v[80:83], a[136:139], a[104:107], v[80:83]\n" \
"buffer_load_dwordx4 v53, s[8:11], s64 offen offset:1024 sc1 lds\n" \
"v_mfma_f32_16x16x32_bf16 v[80:83], a[140:143], a[108:111], v[80:83]\n" \
"v_cvt_pk_bf16_f32 v203, v206, v207\n" \
"v_cvt_pk_bf16_f32 v208, v208, v209\n" \
"buffer_store_dwordx4 v[200:203], v20, s[56:59], s70 offen offset:1024 sc1\n" \
"v_mfma_f32_16x16x32_bf16 v[84:87], a[156:159], a[108:111], v[40:43]\n" \
"v_mul_f32_e32 v210, v56, v210\n" \
"v_mul_f32_e32 v211, v56, v211\n" \
"v_mfma_f32_16x16x32_bf16 v[84:87], a[152:155], a[104:107], v[84:87]\n" \
"v_rcp_f32_e32 v57, v57\n" \
"v_mfma_f32_16x16x32_bf16 v[84:87], a[148:151], a[100:103], v[84:87]\n" \
"buffer_load_dwordx4 v53, s[8:11], s64 offen offset:2048 sc1 lds\n" \
"v_mfma_f32_16x16x32_bf16 v[84:87], a[144:147], a[96:99], v[84:87]\n" \
"v_mfma_f32_16x16x32_bf16 a[208:211], a[216:219], v[200:203], a[208:211]\n" \
"v_mfma_f32_32x32x16_bf16 v[192:207], a[216:219], a[216:219], 0\n" \
"v_cvt_pk_bf16_f32 v209, v210, v211\n" \
"v_mul_f32_e32 v212, v56, v212\n" \
".fill (L_ab + 16384 - .) / 4, 4, 0xBF800000\n" \
"L_ap4:\n" \
"s_cbranch_execz L_ap0\n" \
"v_mul_f32_e32 v213, v56, v213\n" \
"v_mul_f32_e32 v214, v56, v214\n" \
"v_mul_f32_e32 v215, v56, v215\n" \
"v_mul_f32_e32 v216, v56, v216\n" \
"v_mfma_f32_16x16x32_bf16 v[92:95], a[144:147], a[112:115], v[44:47]\n" \
"ds_read_b128 a[144:147], v48 offset:12288\n" \
"v_mul_f32_e32 v217, v56, v217\n" \
"v_mfma_f32_16x16x32_bf16 v[92:95], a[148:151], a[116:119], v[92:95]\n" \
"ds_read_b128 a[148:151], v48 offset:13312\n" \
"v_cvt_pk_bf16_f32 v210, v212, v213\n" \
"v_mfma_f32_16x16x32_bf16 v[92:95], a[152:155], a[120:123], v[92:95]\n" \
"ds_read_b128 a[152:155], v48 offset:14336\n" \
"v_cvt_pk_bf16_f32 v211, v214, v215\n" \
"buffer_store_dwordx4 v[208:211], v20, s[56:59], s70 offen offset:2048 sc1\n" \
"v_mfma_f32_16x16x32_bf16 v[92:95], a[156:159], a[124:127], v[92:95]\n" \
"ds_read_b128 a[156:159], v48 offset:15360\n" \
"v_cvt_pk_bf16_f32 v216, v216, v217\n" \
"v_mfma_f32_16x16x32_bf16 v[88:91], a[140:143], a[124:127], v[44:47]\n" \
"ds_read_b128 a[140:143], v48 offset:11264\n" \
"v_mul_f32_e32 v218, v56, v218\n" \
"v_mfma_f32_16x16x32_bf16 v[88:91], a[136:139], a[120:123], v[88:91]\n" \
"ds_read_b128 a[136:139], v48 offset:10240\n" \
"v_mul_f32_e32 v219, v56, v219\n" \
"v_mfma_f32_16x16x32_bf16 v[88:91], a[132:135], a[116:119], v[88:91]\n" \
"ds_read_b128 a[132:135], v48 offset:9216\n" \
"v_mul_f32_e32 v57, v57, v61\n" \
"v_mfma_f32_16x16x32_bf16 v[88:91], a[128:131], a[112:115], v[88:91]\n" \
"ds_read_b128 a[128:131], v48 offset:8192\n" \
"v_mfma_f32_16x16x32_bf16 a[208:211], a[216:219], v[208:211], a[208:211]\n" \
"v_cvt_pk_bf16_f32 v217, v218, v219\n" \
"v_mul_f32_e32 v220, v56, v220\n" \
"v_mul_f32_e32 v221, v56, v221\n" \
"v_mul_f32_e32 v222, v56, v222\n" \
"v_mul_f32_e32 v223, v56, v223\n" \
"v_mul_f32_e32 v224, v57, v224\n" \
"v_mul_f32_e32 v225, v57, v225\n" \
"v_cvt_pk_bf16_f32 v218, v220, v221\n" \
"v_cvt_pk_bf16_f32 v219, v222, v223\n" \
"buffer_store_dwordx4 v[216:219], v20, s[56:59], s70 offen offset:3072 sc1\n" \
"v_cvt_pk_bf16_f32 v224, v224, v225\n" \
"v_mul_f32_e32 v226, v57, v226\n" \
"v_mul_f32_e32 v227, v57, v227\n" \
"v_mfma_f32_16x16x32_bf16 a[208:211], a[216:219], v[216:219], a[208:211]\n" \
"v_mfma_f32_32x32x16_bf16 v[208:223], a[216:219], a[216:219], 0\n" \
"v_cvt_pk_bf16_f32 v225, v226, v227\n" \
"v_mul_f32_e32 v228, v57, v228\n" \
"v_mul_f32_e32 v229, v57, v229\n" \
"v_mul_f32_e32 v230, v57, v230\n" \
"v_mul_f32_e32 v231, v57, v231\n" \
"v_mul_f32_e32 v232, v57, v232\n" \
"v_mul_f32_e32 v233, v57, v233\n" \
"v_cvt_pk_bf16_f32 v226, v228, v229\n" \
"v_cvt_pk_bf16_f32 v227, v230, v231\n" \
"buffer_store_dwordx4 v[224:227], v20, s[56:59], s71 offen offset:0 sc1\n" \
"v_cvt_pk_bf16_f32 v232, v232, v233\n" \
"v_mul_f32_e32 v234, v57, v234\n" \
"v_mul_f32_e32 v235, v57, v235\n" \
"v_mfma_f32_16x16x32_bf16 a[208:211], a[216:219], v[224:227], a[208:211]\n" \
"v_cvt_pk_bf16_f32 v233, v234, v235\n" \
"v_mul_f32_e32 v236, v57, v236\n" \
"v_mul_f32_e32 v237, v57, v237\n" \
"v_mul_f32_e32 v238, v57, v238\n" \
"v_mul_f32_e32 v239, v57, v239\n" \
"v_mul_f32_e32 v240, v57, v240\n" \
"v_mul_f32_e32 v241, v57, v241\n" \
"v_cvt_pk_bf16_f32 v234, v236, v237\n" \
"v_cvt_pk_bf16_f32 v235, v238, v239\n" \
"buffer_store_dwordx4 v[232:235], v20, s[56:59], s71 offen offset:1024 sc1\n" \
"v_cvt_pk_bf16_f32 v240, v240, v241\n" \
"v_mul_f32_e32 v242, v57, v242\n" \
"v_mul_f32_e32 v243, v57, v243\n" \
"v_mfma_f32_16x16x32_bf16 a[208:211], a[216:219], v[232:235], a[208:211]\n" \
"v_mfma_f32_32x32x16_bf16 v[224:239], a[216:219], a[216:219], 0\n" \
"v_cvt_pk_bf16_f32 v241, v242, v243\n" \
"v_mul_f32_e32 v244, v57, v244\n" \
"v_mul_f32_e32 v245, v57, v245\n" \
"v_mul_f32_e32 v246, v57, v246\n" \
"v_mul_f32_e32 v247, v57, v247\n" \
"v_mul_f32_e32 v248, v57, v248\n" \
"v_mul_f32_e32 v249, v57, v249\n" \
"v_cvt_pk_bf16_f32 v242, v244, v245\n" \
"v_cvt_pk_bf16_f32 v243, v246, v247\n" \
"buffer_store_dwordx4 v[240:243], v20, s[56:59], s71 offen offset:2048 sc1\n" \
"v_cvt_pk_bf16_f32 v248, v248, v249\n" \
"v_mul_f32_e32 v250, v57, v250\n" \
"v_mul_f32_e32 v251, v57, v251\n" \
"v_mfma_f32_16x16x32_bf16 a[208:211], a[216:219], v[240:243], a[208:211]\n" \
"v_cvt_pk_bf16_f32 v249, v250, v251\n" \
"v_mul_f32_e32 v252, v57, v252\n" \
"v_mul_f32_e32 v253, v57, v253\n" \
"v_mul_f32_e32 v254, v57, v254\n" \
"v_mul_f32_e32 v255, v57, v255\n" \
"v_cvt_pk_bf16_f32 v250, v252, v253\n" \
"v_cvt_pk_bf16_f32 v251, v254, v255\n" \
"buffer_store_dwordx4 v[248:251], v20, s[56:59], s71 offen offset:3072 sc1\n" \
"buffer_load_dwordx4 v53, s[8:11], s64 offen offset:3072 sc1 lds\n" \
"s_waitcnt vmcnt(16) lgkmcnt(0)\n" \
"s_barrier\n" \
"v_mfma_f32_16x16x32_bf16 a[208:211], a[216:219], v[248:251], a[208:211]\n" \
"v_mfma_f32_32x32x16_bf16 v[240:255], a[216:219], a[216:219], 0\n" \
"v_exp_f32_e32 v70, v70\n" \
"v_exp_f32_e32 v71, v71\n" \
"v_exp_f32_e32 v72, v72\n" \
"v_exp_f32_e32 v73, v73\n" \
"s_add_u32 s64, s64, 0x10000\n" \
"v_cmp_lt_f32_e64 s[72:73], s21, v62\n" \
"v_readlane_b32 s95, v11, 3\n" \
"v_accvgpr_read_b32 v63, a208\n" \
"v_cmp_u_f32_e32 vcc, v63, v63\n" \
"v_exp_f32_e32 v68, v68\n" \
"v_exp_f32_e32 v69, v69\n" \
"v_exp_f32_e32 v74, v74\n" \
"v_exp_f32_e32 v75, v75\n" \
"s_or_b64 vcc, s[72:73], vcc\n" \
"s_cselect_b32 s95, 0x40000000, s95\n" \
"s_cselect_b32 s94, 0x7f800000, 0\n" \
"v_cvt_pk_bf16_f32 v71, v70, v71\n" \
"v_exp_f32_e32 v66, v66\n" \
"v_exp_f32_e32 v67, v67\n" \
"v_exp_f32_e32 v64, v64\n" \
"v_exp_f32_e32 v65, v65\n" \
"v_cvt_pk_bf16_f32 v72, v72, v73\n" \
"v_exp_f32_e32 v76, v76\n" \
"v_exp_f32_e32 v77, v77\n" \
"v_exp_f32_e32 v78, v78\n" \
"v_exp_f32_e32 v79, v79\n" \
"v_cvt_pk_bf16_f32 v70, v68, v69\n" \
"v_cvt_pk_bf16_f32 v73, v74, v75\n" \
"v_add_u32_e32 v49, 0x10000, v48\n" \
"s_xor_b32 s65, s65, 0x8000\n" \
"v_cvt_pk_bf16_f32 v69, v66, v67\n" \
"v_cvt_pk_bf16_f32 v68, v64, v65\n" \
"v_cvt_pk_bf16_f32 v74, v76, v77\n" \
"v_cvt_pk_bf16_f32 v75, v78, v79\n" \
"v_xor_b32_e32 v48, 0x8000, v48\n" \
"s_sub_u32 s66, s19, 1\n" \
"s_cbranch_scc0 L_2_%=\n" \
"L_f1_%=:\n" \
"v_bfe_u32 v60, v20, 2, 7\n" \
"v_readlane_b32 s96, v11, 1\n" \
"ds_read_b128 a[160:163], v49 offset:0\n" \
"ds_read_b128 a[164:167], v49 offset:1024\n" \
"ds_read_b128 a[168:171], v49 offset:2048\n" \
"v_mfma_f32_16x16x32_bf16 v[104:107], a[128:131], a[80:83], v[36:39]\n" \
"v_cvt_pk_bf16_f32 v192, v192, v193\n" \
"v_cvt_pk_bf16_f32 v224, v224, v225\n" \
"buffer_load_dword v57, v60, s[56:59], s96 offen sc1\n" \
"v_readlane_b32 s93, v11, 20\n" \
"v_cvt_pk_bf16_f32 v193, v194, v195\n" \
"v_cvt_pk_bf16_f32 v225, v226, v227\n" \
"ds_read_b128 a[172:175], v49 offset:3072\n" \
"v_readlane_b32 s84, v11, 15\n" \
"v_readlane_b32 s85, v11, 16\n" \
"v_readlane_b32 s86, v11, 17\n" \
"s_mov_b32 s87, 0x20000\n" \
"v_readlane_b32 s92, v11, 18\n" \
"s_add_u32 m0, s93, 0x0\n" \
"v_cvt_pk_bf16_f32 v194, v196, v197\n" \
"v_cvt_pk_bf16_f32 v195, v198, v199\n" \
"v_cvt_pk_bf16_f32 v226, v228, v229\n" \
"v_cvt_pk_bf16_f32 v227, v230, v231\n" \
"v_mfma_f32_16x16x32_bf16 v[104:107], a[132:135], a[84:87], v[104:107]\n" \
"v_lshl_add_u32 v50, s15, 2, v20\n" \
"v_cvt_pk_bf16_f32 v196, v200, v201\n" \
"v_cvt_pk_bf16_f32 v197, v202, v203\n" \
"v_cvt_pk_bf16_f32 v198, v204, v205\n" \
"v_cvt_pk_bf16_f32 v199, v206, v207\n" \
"buffer_load_dwordx4 v0, s[84:87], s92 offen offset:0 lds\n" \
"v_cvt_pk_bf16_f32 v228, v232, v233\n" \
"v_cvt_pk_bf16_f32 v229, v234, v235\n" \
"v_cvt_pk_bf16_f32 v230, v236, v237\n" \
"v_cvt_pk_bf16_f32 v231, v238, v239\n" \
"v_add_u32_e32 v50, 0x1a000, v50\n" \
"v_mov_b32_e32 v61, s62\n" \
"v_cvt_pk_bf16_f32 v200, v208, v209\n" \
"v_cvt_pk_bf16_f32 v201, v210, v211\n" \
"v_cvt_pk_bf16_f32 v202, v212, v213\n" \
"v_cvt_pk_bf16_f32 v203, v214, v215\n" \
"v_cvt_pk_bf16_f32 v204, v216, v217\n" \
"v_cvt_pk_bf16_f32 v205, v218, v219\n" \
"v_cvt_pk_bf16_f32 v206, v220, v221\n" \
"v_cvt_pk_bf16_f32 v207, v222, v223\n" \
"v_cvt_pk_bf16_f32 v232, v240, v241\n" \
"v_cvt_pk_bf16_f32 v233, v242, v243\n" \
"v_cvt_pk_bf16_f32 v234, v244, v245\n" \
"v_cvt_pk_bf16_f32 v235, v246, v247\n" \
"v_cvt_pk_bf16_f32 v236, v248, v249\n" \
"v_cvt_pk_bf16_f32 v237, v250, v251\n" \
"v_cvt_pk_bf16_f32 v238, v252, v253\n" \
"buffer_load_dwordx4 v1, s[84:87], s92 offen offset:1024 lds\n" \
"v_cvt_pk_bf16_f32 v239, v254, v255\n" \
"ds_write_b128 v50, v[192:195] offset:0\n" \
"v_mfma_f32_16x16x32_bf16 v[104:107], a[136:139], a[88:91], v[104:107]\n" \
"ds_read_b128 a[176:179], v49 offset:4096\n" \
"v_add_u32_e32 v56, 0x49000, v20\n" \
"v_mfma_f32_16x16x32_bf16 v[104:107], a[140:143], a[92:95], v[104:107]\n" \
"ds_write_b128 v50, v[196:199] offset:1024\n" \
"v_mfma_f32_16x16x32_bf16 v[108:111], a[156:159], a[92:95], v[36:39]\n" \
"v_readlane_b32 s97, v11, 35\n" \
"v_readlane_b32 s98, v11, 36\n" \
"ds_read_b128 a[180:183], v49 offset:5120\n" \
"v_mfma_f32_16x16x32_bf16 v[108:111], a[152:155], a[88:91], v[108:111]\n" \
"ds_write_b128 v50, v[200:203] offset:2048\n" \
"v_mfma_f32_16x16x32_bf16 v[108:111], a[148:151], a[84:87], v[108:111]\n" \
"buffer_load_dwordx4 v2, s[84:87], s92 offen offset:2048 lds\n" \
"ds_read_b128 a[184:187], v49 offset:6144\n" \
"v_mfma_f32_16x16x32_bf16 v[108:111], a[144:147], a[80:83], v[108:111]\n" \
"ds_write_b128 v50, v[204:207] offset:3072\n" \
"v_and_b32_e32 v68, v12, v68\n" \
"v_and_b32_e32 v69, v13, v69\n" \
"v_and_b32_e32 v70, v14, v70\n" \
"v_and_b32_e32 v71, v15, v71\n" \
"ds_read_b128 a[188:191], v49 offset:7168\n" \
"ds_read_b128 a[0:3], v20 offset:8192\n" \
"ds_write_b128 v50, v[224:227] offset:4096\n" \
"s_waitcnt lgkmcnt(10)\n" \
"v_mfma_f32_16x16x32_bf16 v[128:131], a[160:163], v[68:71], v[128:131]\n" \
"ds_read_b128 a[4:7], v20 offset:9216\n" \
"ds_read_b128 a[8:11], v20 offset:10240\n" \
"v_mfma_f32_16x16x32_bf16 v[160:163], a[160:163], v[72:75], v[160:163]\n" \
"ds_write_b128 v50, v[228:231] offset:5120\n" \
"buffer_load_dwordx4 v3, s[84:87], s92 offen offset:3072 lds\n" \
"s_add_u32 m0, s93, 0x1000\n" \
"v_mfma_f32_16x16x32_bf16 v[132:135], a[164:167], v[68:71], v[132:135]\n" \
"ds_read_b128 a[12:15], v20 offset:11264\n" \
"ds_read_b128 a[28:31], v20 offset:15360\n" \
"ds_write_b128 v50, v[232:235] offset:6144\n" \
"v_mfma_f32_16x16x32_bf16 v[164:167], a[164:167], v[72:75], v[164:167]\n" \
"ds_read_b128 a[24:27], v20 offset:14336\n" \
"ds_read_b128 a[20:23], v20 offset:13312\n" \
"v_mfma_f32_16x16x32_bf16 v[136:139], a[168:171], v[68:71], v[136:139]\n" \
"ds_write_b128 v50, v[236:239] offset:7168\n" \
"v_mfma_f32_16x16x32_bf16 v[168:171], a[168:171], v[72:75], v[168:171]\n" \
"ds_read_b128 a[16:19], v20 offset:12288\n" \
"v_xor_b32_e32 v48, 0x4000, v49\n" \
"v_mfma_f32_16x16x32_bf16 v[140:143], a[172:175], v[68:71], v[140:143]\n" \
"buffer_load_dwordx4 v4, s[84:87], s92 offen offset:0 lds\n" \
"v_mfma_f32_16x16x32_bf16 v[172:175], a[172:175], v[72:75], v[172:175]\n" \
"ds_read_b32 v58, v18 offset:256\n" \
"s_waitcnt lgkmcnt(15)\n" \
"v_mfma_f32_16x16x32_bf16 v[144:147], a[176:179], v[68:71], v[144:147]\n" \
"ds_read_b32 v59, v18 offset:384\n" \
"ds_read_b128 a[32:35], v48 offset:8192\n" \
"v_mfma_f32_16x16x32_bf16 v[176:179], a[176:179], v[72:75], v[176:179]\n" \
"ds_read_b128 a[36:39], v48 offset:9216\n" \
"ds_read_b128 a[40:43], v48 offset:10240\n" \
"v_mfma_f32_16x16x32_bf16 v[148:151], a[180:183], v[68:71], v[148:151]\n" \
"ds_read_b128 a[44:47], v48 offset:11264\n" \
"ds_read_b128 a[48:51], v48 offset:12288\n" \
"v_mfma_f32_16x16x32_bf16 v[180:183], a[180:183], v[72:75], v[180:183]\n" \
"ds_read_b128 a[52:55], v48 offset:13312\n" \
"ds_read_b128 a[56:59], v48 offset:14336\n" \
"v_mfma_f32_16x16x32_bf16 v[152:155], a[184:187], v[68:71], v[152:155]\n" \
"buffer_load_dwordx4 v5, s[84:87], s92 offen offset:1024 lds\n" \
"v_mfma_f32_16x16x32_bf16 v[184:187], a[184:187], v[72:75], v[184:187]\n" \
"ds_read_b128 a[60:63], v48 offset:15360\n" \
"s_waitcnt lgkmcnt(15)\n" \
"v_mfma_f32_16x16x32_bf16 v[156:159], a[188:191], v[68:71], v[156:159]\n" \
"ds_read_b128 a[128:131], v49 offset:8192\n" \
"ds_read_b128 a[132:135], v49 offset:9216\n" \
"v_mfma_f32_16x16x32_bf16 v[188:191], a[188:191], v[72:75], v[188:191]\n" \
"ds_read_b128 a[136:139], v49 offset:10240\n" \
"ds_read_b128 a[140:143], v49 offset:11264\n" \
"v_smfmac_f32_16x16x64_bf16 a[192:195], a[200:203], v[68:75], v21\n" \
"ds_read_b128 a[144:147], v49 offset:12288\n" \
"ds_read_b128 a[148:151], v49 offset:13312\n" \
"v_mfma_f32_16x16x32_bf16 v[112:115], a[0:3], a[64:67], v[32:35]\n" \
"ds_read_b128 a[152:155], v49 offset:14336\n" \
"ds_read_b128 a[156:159], v49 offset:15360\n" \
"v_mfma_f32_16x16x32_bf16 v[112:115], a[4:7], a[68:71], v[112:115]\n" \
"buffer_load_dwordx4 v6, s[84:87], s92 offen offset:2048 lds\n" \
"v_mfma_f32_16x16x32_bf16 v[112:115], a[8:11], a[72:75], v[112:115]\n" \
"v_accvgpr_read_b32 v40, a196\n" \
"v_accvgpr_read_b32 v24, a197\n" \
"v_mfma_f32_16x16x32_bf16 v[112:115], a[12:15], a[76:79], v[112:115]\n" \
"v_accvgpr_read_b32 v41, a198\n" \
"v_accvgpr_read_b32 v25, a199\n" \
"v_mfma_f32_16x16x32_bf16 v[116:119], a[28:31], a[76:79], v[32:35]\n" \
"s_waitcnt vmcnt(7)\n" \
"v_cmp_ne_u32_e32 vcc, s62, v57\n" \
"s_mov_b64 exec, 1\n" \
"buffer_store_dword v61, off, s[56:59], s95 offset:0 sc1\n" \
"s_mov_b64 exec, -1\n" \
"s_waitcnt lgkmcnt(15)\n" \
"v_mfma_f32_16x16x32_bf16 v[116:119], a[24:27], a[72:75], v[116:119]\n" \
"s_cbranch_vccnz L_so1_%=\n" \
"L_ro1_%=:\n" \
"v_add_f32_e32 v40, v40, v24\n" \
"v_mfma_f32_16x16x32_bf16 v[116:119], a[20:23], a[68:71], v[116:119]\n" \
"v_add_f32_e32 v41, v41, v25\n" \
"v_add_f32_e32 v40, s94, v40\n" \
"v_mfma_f32_16x16x32_bf16 v[116:119], a[16:19], a[64:67], v[116:119]\n" \
"v_add_f32_e32 v41, s94, v41\n" \
"ds_write_b64 v19, v[40:41]\n" \
"v_mfma_f32_16x16x32_bf16 v[124:127], a[16:19], a[80:83], v[36:39]\n" \
"v_add_u32_e32 v52, s15, v20\n" \
"v_add_u32_e32 v51, 0x10000, v52\n" \
"v_mfma_f32_16x16x32_bf16 v[124:127], a[20:23], a[84:87], v[124:127]\n" \
"v_exp_f32_e32 v104, v104\n" \
"buffer_load_dwordx4 v7, s[84:87], s92 offen offset:3072 lds\n" \
"s_add_u32 m0, s93, 0x2000\n" \
"v_mfma_f32_16x16x32_bf16 v[124:127], a[24:27], a[88:91], v[124:127]\n" \
"v_exp_f32_e32 v105, v105\n" \
"v_mfma_f32_16x16x32_bf16 v[124:127], a[28:31], a[92:95], v[124:127]\n" \
"v_exp_f32_e32 v106, v106\n" \
"v_mfma_f32_16x16x32_bf16 v[120:123], a[12:15], a[92:95], v[36:39]\n" \
"v_exp_f32_e32 v107, v107\n" \
"v_mfma_f32_16x16x32_bf16 v[120:123], a[8:11], a[88:91], v[120:123]\n" \
"v_cvt_pk_bf16_f32 v104, v104, v105\n" \
"v_cvt_pk_bf16_f32 v105, v106, v107\n" \
"v_mfma_f32_16x16x32_bf16 v[120:123], a[4:7], a[84:87], v[120:123]\n" \
"v_exp_f32_e32 v108, v108\n" \
"buffer_load_dwordx4 v8, s[84:87], s92 offen offset:0 lds\n" \
"v_mfma_f32_16x16x32_bf16 v[120:123], a[0:3], a[80:83], v[120:123]\n" \
"v_exp_f32_e32 v109, v109\n" \
"v_exp_f32_e32 v110, v110\n" \
"v_exp_f32_e32 v111, v111\n" \
"s_waitcnt lgkmcnt(0)\n" \
"s_barrier\n" \
"ds_read_b128 v[208:211], v52 offset:0\n" \
"ds_read_b128 v[212:215], v52 offset:1024\n" \
"ds_read_b128 v[216:219], v52 offset:2048\n" \
"ds_read_b128 v[220:223], v52 offset:3072\n" \
"ds_read_b128 v[240:243], v51 offset:32768\n" \
"ds_read_b128 v[244:247], v51 offset:33792\n" \
"ds_read_b128 v[248:251], v51 offset:34816\n" \
"ds_read_b128 v[252:255], v51 offset:35840\n" \
"buffer_load_dwordx4 a[220:223], v20, s[56:59], s60 offen offset:0 sc1\n" \
"v_cvt_pk_bf16_f32 v106, v108, v109\n" \
"v_cvt_pk_bf16_f32 v107, v110, v111\n" \
"v_and_b32_e32 v104, v12, v104\n" \
"v_and_b32_e32 v105, v13, v105\n" \
"v_and_b32_e32 v106, v14, v106\n" \
"v_and_b32_e32 v107, v15, v107\n" \
"v_xor_b32_e32 v63, 0x400, v19\n" \
"ds_read_b64 v[42:43], v63\n" \
"v_mfma_f32_16x16x32_bf16 a[192:195], a[212:215], v[104:107], a[192:195]\n" \
"v_mov_b32_e32 v194, v28\n" \
"v_mov_b32_e32 v195, v29\n" \
"v_mfma_f32_16x16x32_bf16 v[160:163], a[128:131], v[104:107], v[160:163]\n" \
"v_mov_b32_e32 v196, 0\n" \
"v_mov_b32_e32 v197, 0\n" \
"v_mfma_f32_16x16x32_bf16 v[164:167], a[132:135], v[104:107], v[164:167]\n" \
"v_mov_b32_e32 v192, 0\n" \
"v_mov_b32_e32 v193, 0\n" \
"v_mfma_f32_16x16x32_bf16 v[168:171], a[136:139], v[104:107], v[168:171]\n" \
"buffer_load_dwordx4 a[236:239], v56, s[56:59], s60 offen offset:0 sc1\n" \
"v_mfma_f32_16x16x32_bf16 v[172:175], a[140:143], v[104:107], v[172:175]\n" \
"v_exp_f32_e32 v118, v118\n" \
"v_mfma_f32_16x16x32_bf16 v[176:179], a[144:147], v[104:107], v[176:179]\n" \
"v_exp_f32_e32 v119, v119\n" \
"v_mfma_f32_16x16x32_bf16 v[180:183], a[148:151], v[104:107], v[180:183]\n" \
"v_exp_f32_e32 v120, v120\n" \
"v_mfma_f32_16x16x32_bf16 v[184:187], a[152:155], v[104:107], v[184:187]\n" \
"v_exp_f32_e32 v121, v121\n" \
"v_mfma_f32_16x16x32_bf16 v[188:191], a[156:159], v[104:107], v[188:191]\n" \
"v_exp_f32_e32 v116, v116\n" \
"s_waitcnt lgkmcnt(1)\n" \
"s_barrier\n" \
"buffer_load_dwordx4 a[224:227], v20, s[56:59], s60 offen offset:1024 sc1\n" \
"v_mfma_f32_16x16x32_bf16 v[128:131], v[194:197], v[208:211], v[128:131]\n" \
"v_exp_f32_e32 v117, v117\n" \
"v_mfma_f32_16x16x32_bf16 v[132:135], v[192:195], v[208:211], v[132:135]\n" \
"v_exp_f32_e32 v122, v122\n" \
"v_mfma_f32_16x16x32_bf16 v[136:139], v[194:197], v[212:215], v[136:139]\n" \
"v_exp_f32_e32 v123, v123\n" \
"v_mfma_f32_16x16x32_bf16 v[140:143], v[192:195], v[212:215], v[140:143]\n" \
"v_cvt_pk_bf16_f32 v119, v118, v119\n" \
"v_cvt_pk_bf16_f32 v120, v120, v121\n" \
"v_mfma_f32_16x16x32_bf16 v[144:147], v[194:197], v[216:219], v[144:147]\n" \
"v_exp_f32_e32 v114, v114\n" \
"v_mfma_f32_16x16x32_bf16 v[148:151], v[192:195], v[216:219], v[148:151]\n" \
"buffer_load_dwordx4 a[240:243], v56, s[56:59], s60 offen offset:1024 sc1\n" \
"v_mfma_f32_16x16x32_bf16 v[152:155], v[194:197], v[220:223], v[152:155]\n" \
"v_exp_f32_e32 v115, v115\n" \
"v_mfma_f32_16x16x32_bf16 v[156:159], v[192:195], v[220:223], v[156:159]\n" \
"v_exp_f32_e32 v112, v112\n" \
"v_mfma_f32_16x16x32_bf16 v[160:163], v[194:197], v[240:243], v[160:163]\n" \
"v_exp_f32_e32 v113, v113\n" \
"v_mfma_f32_16x16x32_bf16 v[164:167], v[192:195], v[240:243], v[164:167]\n" \
"v_exp_f32_e32 v124, v124\n" \
"v_mfma_f32_16x16x32_bf16 v[168:171], v[194:197], v[244:247], v[168:171]\n" \
"v_exp_f32_e32 v125, v125\n" \
"v_mfma_f32_16x16x32_bf16 v[172:175], v[192:195], v[244:247], v[172:175]\n" \
"buffer_load_dwordx4 a[228:231], v20, s[56:59], s60 offen offset:2048 sc1\n" \
"v_mfma_f32_16x16x32_bf16 v[176:179], v[194:197], v[248:251], v[176:179]\n" \
"v_exp_f32_e32 v126, v126\n" \
"v_mfma_f32_16x16x32_bf16 v[180:183], v[192:195], v[248:251], v[180:183]\n" \
"v_exp_f32_e32 v127, v127\n" \
"v_mfma_f32_16x16x32_bf16 v[184:187], v[194:197], v[252:255], v[184:187]\n" \
"v_cvt_pk_bf16_f32 v118, v116, v117\n" \
"v_cvt_pk_bf16_f32 v121, v122, v123\n" \
"v_mfma_f32_16x16x32_bf16 v[188:191], v[192:195], v[252:255], v[188:191]\n" \
"v_cvt_pk_bf16_f32 v117, v114, v115\n" \
"v_cvt_pk_bf16_f32 v116, v112, v113\n" \
"v_cvt_pk_bf16_f32 v122, v124, v125\n" \
"v_cvt_pk_bf16_f32 v123, v126, v127\n" \
"s_nop 1\n" \
"v_smfmac_f32_16x16x64_bf16 a[192:195], a[200:203], v[116:123], v21\n" \
"buffer_load_dwordx4 a[244:247], v56, s[56:59], s60 offen offset:2048 sc1\n" \
"v_mfma_f32_16x16x32_bf16 v[128:131], a[32:35], v[116:119], v[128:131]\n" \
"v_mfma_f32_16x16x32_bf16 v[160:163], a[32:35], v[120:123], v[160:163]\n" \
"v_mfma_f32_16x16x32_bf16 v[132:135], a[36:39], v[116:119], v[132:135]\n" \
"v_mfma_f32_16x16x32_bf16 v[164:167], a[36:39], v[120:123], v[164:167]\n" \
"v_mfma_f32_16x16x32_bf16 v[136:139], a[40:43], v[116:119], v[136:139]\n" \
"v_mfma_f32_16x16x32_bf16 v[168:171], a[40:43], v[120:123], v[168:171]\n" \
"buffer_load_dwordx4 a[232:235], v20, s[56:59], s60 offen offset:3072 sc1\n" \
"v_mfma_f32_16x16x32_bf16 v[140:143], a[44:47], v[116:119], v[140:143]\n" \
"v_accvgpr_read_b32 v54, a192\n" \
"v_accvgpr_read_b32 v22, a193\n" \
"v_mfma_f32_16x16x32_bf16 v[172:175], a[44:47], v[120:123], v[172:175]\n" \
"v_add_f32_e32 v54, v54, v22\n" \
"s_waitcnt lgkmcnt(0)\n" \
"v_add_f32_e32 v54, v54, v42\n" \
"v_mfma_f32_16x16x32_bf16 v[144:147], a[48:51], v[116:119], v[144:147]\n" \
"v_max_f32_e32 v62, v62, v54\n" \
"v_accvgpr_read_b32 v55, a194\n" \
"v_mfma_f32_16x16x32_bf16 v[176:179], a[48:51], v[120:123], v[176:179]\n" \
"v_rcp_f32_e32 v54, v54\n" \
"v_mfma_f32_16x16x32_bf16 v[148:151], a[52:55], v[116:119], v[148:151]\n" \
"v_mul_f32_e32 v54, v54, v58\n" \
"v_mul_f32_e32 v128, v54, v128\n" \
"buffer_load_dwordx4 a[248:251], v56, s[56:59], s60 offen offset:3072 sc1\n" \
"v_mfma_f32_16x16x32_bf16 v[180:183], a[52:55], v[120:123], v[180:183]\n" \
"v_mul_f32_e32 v129, v54, v129\n" \
"v_mul_f32_e32 v130, v54, v130\n" \
"v_mul_f32_e32 v131, v54, v131\n" \
"v_mul_f32_e32 v132, v54, v132\n" \
"v_mul_f32_e32 v133, v54, v133\n" \
"v_mul_f32_e32 v134, v54, v134\n" \
"v_mul_f32_e32 v135, v54, v135\n" \
"v_cvt_pk_bf16_f32 v64, v128, v129\n" \
"v_cvt_pk_bf16_f32 v65, v130, v131\n" \
"v_cvt_pk_bf16_f32 v66, v132, v133\n" \
"v_cvt_pk_bf16_f32 v67, v134, v135\n" \
"v_accvgpr_read_b32 v23, a195\n" \
"v_mfma_f32_16x16x32_bf16 v[152:155], a[56:59], v[116:119], v[152:155]\n" \
"v_mfma_f32_16x16x32_bf16 a[208:211], a[216:219], v[64:67], a[208:211]\n" \
"v_mul_f32_e32 v136, v54, v136\n" \
"v_mul_f32_e32 v137, v54, v137\n" \
"buffer_load_dwordx4 a[96:99], v20, s[56:59], s97 offen offset:0 sc1\n" \
"v_mul_f32_e32 v138, v54, v138\n" \
"v_mul_f32_e32 v139, v54, v139\n" \
"v_mul_f32_e32 v140, v54, v140\n" \
"v_mul_f32_e32 v141, v54, v141\n" \
"v_mul_f32_e32 v142, v54, v142\n" \
"v_mul_f32_e32 v143, v54, v143\n" \
"v_add_f32_e32 v55, v55, v23\n" \
"v_cvt_pk_bf16_f32 v68, v136, v137\n" \
"v_cvt_pk_bf16_f32 v69, v138, v139\n" \
"v_cvt_pk_bf16_f32 v70, v140, v141\n" \
"v_cvt_pk_bf16_f32 v71, v142, v143\n" \
"v_add_f32_e32 v55, v55, v43\n" \
"v_mul_f32_e32 v144, v54, v144\n" \
"v_mfma_f32_16x16x32_bf16 a[208:211], a[216:219], v[68:71], a[208:211]\n" \
"v_mul_f32_e32 v145, v54, v145\n" \
"v_mul_f32_e32 v146, v54, v146\n" \
"v_mul_f32_e32 v147, v54, v147\n" \
"v_mul_f32_e32 v148, v54, v148\n" \
"v_mul_f32_e32 v149, v54, v149\n" \
"v_mul_f32_e32 v150, v54, v150\n" \
"buffer_load_dwordx4 a[104:107], v56, s[56:59], s97 offen offset:0 sc1\n" \
"v_mul_f32_e32 v151, v54, v151\n" \
"v_max_f32_e32 v62, v62, v55\n" \
"v_cvt_pk_bf16_f32 v72, v144, v145\n" \
"v_cvt_pk_bf16_f32 v73, v146, v147\n" \
"v_cvt_pk_bf16_f32 v74, v148, v149\n" \
"v_cvt_pk_bf16_f32 v75, v150, v151\n" \
"v_rcp_f32_e32 v55, v55\n" \
"v_mfma_f32_16x16x32_bf16 v[184:187], a[56:59], v[120:123], v[184:187]\n" \
"v_mfma_f32_16x16x32_bf16 a[208:211], a[216:219], v[72:75], a[208:211]\n" \
"v_mul_f32_e32 v152, v54, v152\n" \
"v_mul_f32_e32 v153, v54, v153\n" \
"v_mul_f32_e32 v154, v54, v154\n" \
"v_mul_f32_e32 v155, v54, v155\n" \
"v_cvt_pk_bf16_f32 v76, v152, v153\n" \
"v_cvt_pk_bf16_f32 v77, v154, v155\n" \
"v_mul_f32_e32 v55, v55, v59\n" \
"buffer_load_dwordx4 a[112:115], v20, s[56:59], s98 offen offset:0 sc1\n" \
"v_mul_f32_e32 v160, v55, v160\n" \
"v_mul_f32_e32 v161, v55, v161\n" \
"v_mul_f32_e32 v162, v55, v162\n" \
"v_mul_f32_e32 v163, v55, v163\n" \
"v_mul_f32_e32 v164, v55, v164\n" \
"v_mul_f32_e32 v165, v55, v165\n" \
"v_mul_f32_e32 v166, v55, v166\n" \
"v_mul_f32_e32 v167, v55, v167\n" \
"v_cvt_pk_bf16_f32 v80, v160, v161\n" \
"v_cvt_pk_bf16_f32 v81, v162, v163\n" \
"v_cvt_pk_bf16_f32 v82, v164, v165\n" \
"v_cvt_pk_bf16_f32 v83, v166, v167\n" \
"v_mfma_f32_16x16x32_bf16 v[156:159], a[60:63], v[116:119], v[156:159]\n" \
"v_mul_f32_e32 v168, v55, v168\n" \
"v_mul_f32_e32 v169, v55, v169\n" \
"v_mul_f32_e32 v170, v55, v170\n" \
"v_mul_f32_e32 v171, v55, v171\n" \
"v_mul_f32_e32 v172, v55, v172\n" \
"v_mul_f32_e32 v173, v55, v173\n" \
"v_mul_f32_e32 v174, v55, v174\n" \
"buffer_load_dwordx4 a[120:123], v56, s[56:59], s98 offen offset:0 sc1\n" \
"v_mul_f32_e32 v156, v54, v156\n" \
"v_mul_f32_e32 v157, v54, v157\n" \
"v_mul_f32_e32 v158, v54, v158\n" \
"v_mul_f32_e32 v159, v54, v159\n" \
"v_cvt_pk_bf16_f32 v78, v156, v157\n" \
"v_cvt_pk_bf16_f32 v79, v158, v159\n" \
"v_mul_f32_e32 v175, v55, v175\n" \
"v_cvt_pk_bf16_f32 v84, v168, v169\n" \
"v_mfma_f32_16x16x32_bf16 a[208:211], a[216:219], v[76:79], a[208:211]\n" \
"v_mfma_f32_16x16x32_bf16 a[208:211], a[216:219], v[80:83], a[208:211]\n" \
"v_cvt_pk_bf16_f32 v85, v170, v171\n" \
"v_cvt_pk_bf16_f32 v86, v172, v173\n" \
"v_cvt_pk_bf16_f32 v87, v174, v175\n" \
"v_mul_f32_e32 v176, v55, v176\n" \
"v_mul_f32_e32 v177, v55, v177\n" \
"v_mfma_f32_16x16x32_bf16 a[208:211], a[216:219], v[84:87], a[208:211]\n" \
"buffer_load_dwordx4 a[100:103], v20, s[56:59], s97 offen offset:1024 sc1\n" \
"v_mul_f32_e32 v178, v55, v178\n" \
"v_mul_f32_e32 v179, v55, v179\n" \
"v_mul_f32_e32 v180, v55, v180\n" \
"v_mul_f32_e32 v181, v55, v181\n" \
"v_mul_f32_e32 v182, v55, v182\n" \
"v_mul_f32_e32 v183, v55, v183\n" \
"v_cvt_pk_bf16_f32 v96, v176, v177\n" \
"v_cvt_pk_bf16_f32 v97, v178, v179\n" \
"v_cvt_pk_bf16_f32 v98, v180, v181\n" \
"v_cvt_pk_bf16_f32 v99, v182, v183\n" \
"v_mfma_f32_16x16x32_bf16 v[188:191], a[60:63], v[120:123], v[188:191]\n" \
"v_mul_f32_e32 v184, v55, v184\n" \
"v_mfma_f32_16x16x32_bf16 a[208:211], a[216:219], v[96:99], a[208:211]\n" \
"v_mul_f32_e32 v185, v55, v185\n" \
"v_mul_f32_e32 v186, v55, v186\n" \
"v_mul_f32_e32 v187, v55, v187\n" \
"v_cvt_pk_bf16_f32 v100, v184, v185\n" \
"v_cvt_pk_bf16_f32 v101, v186, v187\n" \
"buffer_load_dwordx4 a[108:111], v56, s[56:59], s97 offen offset:1024 sc1\n" \
"v_mul_f32_e32 v188, v55, v188\n" \
"v_mul_f32_e32 v189, v55, v189\n" \
"v_mul_f32_e32 v190, v55, v190\n" \
"v_mul_f32_e32 v191, v55, v191\n" \
"v_cvt_pk_bf16_f32 v102, v188, v189\n" \
"v_cvt_pk_bf16_f32 v103, v190, v191\n" \
"v_cmp_lt_f32_e64 s[72:73], s21, v62\n" \
"buffer_load_dwordx4 a[116:119], v20, s[56:59], s98 offen offset:1024 sc1\n" \
"v_mfma_f32_16x16x32_bf16 a[208:211], a[216:219], v[100:103], a[208:211]\n" \
"buffer_load_dwordx4 a[124:127], v56, s[56:59], s98 offen offset:1024 sc1\n" \
"s_nop 6\n" \
"v_accvgpr_read_b32 v63, a208\n" \
"v_cmp_u_f32_e32 vcc, v63, v63\n" \
"s_nop 0\n" \
"s_or_b64 vcc, s[72:73], vcc\n" \
"s_cbranch_scc0 L_out_%=\n" \
"s_branch L_end_%=\n" \
"L_att_stub:\n" \
"s_mov_b32 s101, 0\n" \
"v_add_u32_e32 v32, 0x49000, v20\n" \
"v_readlane_b32 s96, v11, 35\n" \
"v_readlane_b32 s97, v11, 36\n" \
"buffer_load_dwordx4 a[220:223], v20, s[56:59], s60 offen offset:0 sc1\n" \
"buffer_load_dwordx4 a[236:239], v32, s[56:59], s60 offen offset:0 sc1\n" \
"buffer_load_dwordx4 a[224:227], v20, s[56:59], s60 offen offset:1024 sc1\n" \
"buffer_load_dwordx4 a[240:243], v32, s[56:59], s60 offen offset:1024 sc1\n" \
"buffer_load_dwordx4 a[228:231], v20, s[56:59], s60 offen offset:2048 sc1\n" \
"buffer_load_dwordx4 a[244:247], v32, s[56:59], s60 offen offset:2048 sc1\n" \
"buffer_load_dwordx4 a[232:235], v20, s[56:59], s60 offen offset:3072 sc1\n" \
"buffer_load_dwordx4 a[248:251], v32, s[56:59], s60 offen offset:3072 sc1\n" \
"buffer_load_dwordx4 a[96:99], v20, s[56:59], s96 offen offset:0 sc1\n" \
"buffer_load_dwordx4 a[104:107], v32, s[56:59], s96 offen offset:0 sc1\n" \
"buffer_load_dwordx4 a[112:115], v20, s[56:59], s97 offen offset:0 sc1\n" \
"buffer_load_dwordx4 a[120:123], v32, s[56:59], s97 offen offset:0 sc1\n" \
"buffer_load_dwordx4 a[100:103], v20, s[56:59], s96 offen offset:1024 sc1\n" \
"buffer_load_dwordx4 a[108:111], v32, s[56:59], s96 offen offset:1024 sc1\n" \
"buffer_load_dwordx4 a[116:119], v20, s[56:59], s97 offen offset:1024 sc1\n" \
"buffer_load_dwordx4 a[124:127], v32, s[56:59], s97 offen offset:1024 sc1\n" \
"buffer_load_dwordx4 v[64:67], v20, s[56:59], s61 offen offset:0 sc1\n" \
"buffer_load_dwordx4 v[68:71], v20, s[56:59], s61 offen offset:1024 sc1\n" \
"buffer_load_dwordx4 v[72:75], v20, s[56:59], s61 offen offset:2048 sc1\n" \
"buffer_load_dwordx4 v[76:79], v20, s[56:59], s61 offen offset:3072 sc1\n" \
"buffer_load_dwordx4 v[80:83], v32, s[56:59], s61 offen offset:0 sc1\n" \
"buffer_load_dwordx4 v[84:87], v32, s[56:59], s61 offen offset:1024 sc1\n" \
"buffer_load_dwordx4 v[96:99], v32, s[56:59], s61 offen offset:2048 sc1\n" \
"buffer_load_dwordx4 v[100:103], v32, s[56:59], s61 offen offset:3072 sc1\n" \
"v_not_b32_e32 v26, s62\n" \
"s_waitcnt vmcnt(0)\n" \
"s_branch L_out_%=\n" \
".fill (L_ab + 20480 - .) / 4, 4, 0xBF800000\n" \
".fill (L_ab + 20480 - .) / 4, 4, 0xBF800000\n" \
"L_ap5:\n" \
"s_setpc_b64 s[88:89]\n" \
"L_out_%=:\n" \
"v_bfe_u32 v38, v20, 2, 4\n" \
"v_readlane_b32 s64, v11, 2\n" \
"v_add_u32_e32 v32, 0x49000, v20\n" \
"v_add_u32_e32 v34, 0x20000, v10\n" \
"ds_read_b64_tr_b16 a[0:1], v34 offset:0\n" \
"ds_read_b64_tr_b16 a[2:3], v34 offset:4608\n" \
"ds_read_b64_tr_b16 a[4:5], v34 offset:32\n" \
"buffer_load_dword v26, v38, s[56:59], s64 offen sc1\n" \
"ds_read_b64_tr_b16 a[6:7], v34 offset:4640\n" \
"ds_read_b64_tr_b16 a[8:9], v34 offset:64\n" \
"ds_read_b64_tr_b16 a[10:11], v34 offset:4672\n" \
"ds_read_b64_tr_b16 a[12:13], v34 offset:96\n" \
"buffer_store_dwordx4 v[64:67], v20, s[56:59], s61 offen offset:0 sc1\n" \
"ds_read_b64_tr_b16 a[14:15], v34 offset:4704\n" \
"v_mov_b32_e32 v39, s62\n" \
"v_readlane_b32 s72, v11, 5\n" \
"v_readlane_b32 s73, v11, 4\n" \
"buffer_store_dwordx4 v[68:71], v20, s[56:59], s61 offen offset:1024 sc1\n" \
"ds_read_b64_tr_b16 a[16:17], v34 offset:128\n" \
"ds_read_b64_tr_b16 a[18:19], v34 offset:4736\n" \
"ds_read_b64_tr_b16 a[20:21], v34 offset:160\n" \
"ds_read_b64_tr_b16 a[22:23], v34 offset:4768\n" \
"buffer_store_dwordx4 v[72:75], v20, s[56:59], s61 offen offset:2048 sc1\n" \
"ds_read_b64_tr_b16 a[24:25], v34 offset:192\n" \
"ds_read_b64_tr_b16 a[26:27], v34 offset:4800\n" \
"ds_read_b64_tr_b16 a[28:29], v34 offset:224\n" \
"ds_read_b64_tr_b16 a[30:31], v34 offset:4832\n" \
"buffer_store_dwordx4 v[76:79], v20, s[56:59], s61 offen offset:3072 sc1\n" \
"ds_read_b64_tr_b16 a[192:193], v34 offset:256\n" \
"ds_read_b64_tr_b16 a[194:195], v34 offset:4864\n" \
"ds_read_b64_tr_b16 a[196:197], v34 offset:9216\n" \
"ds_read_b64_tr_b16 a[198:199], v34 offset:13824\n" \
"buffer_store_dwordx4 v[80:83], v32, s[56:59], s61 offen offset:0 sc1\n" \
"ds_read_b64_tr_b16 a[200:201], v34 offset:9248\n" \
"ds_read_b64_tr_b16 a[202:203], v34 offset:13856\n" \
"ds_read_b64_tr_b16 a[204:205], v34 offset:9280\n" \
"ds_read_b64_tr_b16 a[206:207], v34 offset:13888\n" \
"buffer_store_dwordx4 v[84:87], v32, s[56:59], s61 offen offset:1024 sc1\n" \
"ds_read_b64_tr_b16 a[208:209], v34 offset:9312\n" \
"s_waitcnt vmcnt(15) lgkmcnt(15)\n" \
"v_mfma_f32_16x16x32_bf16 v[112:115], a[0:3], a[220:223], 0\n" \
"buffer_store_dwordx4 v[96:99], v32, s[56:59], s61 offen offset:2048 sc1\n" \
"v_mfma_f32_16x16x32_bf16 v[116:119], a[0:3], a[236:239], 0\n" \
"ds_read_b64_tr_b16 a[210:211], v34 offset:13920\n" \
"ds_read_b64_tr_b16 a[212:213], v34 offset:9344\n" \
"buffer_store_dwordx4 v[100:103], v32, s[56:59], s61 offen offset:3072 sc1\n" \
"v_mfma_f32_16x16x32_bf16 v[184:187], a[0:3], v[64:67], 0\n" \
"ds_read_b64_tr_b16 a[214:215], v34 offset:13952\n" \
"ds_read_b64_tr_b16 a[216:217], v34 offset:9376\n" \
"v_mfma_f32_16x16x32_bf16 v[188:191], a[0:3], v[80:83], 0\n" \
"ds_read_b64_tr_b16 a[218:219], v34 offset:13984\n" \
"ds_read_b64_tr_b16 a[0:1], v34 offset:9408\n" \
"v_mfma_f32_16x16x32_bf16 v[120:123], a[4:7], a[220:223], 0\n" \
"ds_read_b64_tr_b16 a[2:3], v34 offset:14016\n" \
"v_readlane_b32 s66, v11, 21\n" \
"v_mfma_f32_16x16x32_bf16 v[124:127], a[4:7], a[236:239], 0\n" \
"v_readlane_b32 s80, v11, 11\n" \
"v_readlane_b32 s81, v11, 12\n" \
"v_mfma_f32_16x16x32_bf16 v[192:195], a[4:7], v[64:67], 0\n" \
"s_mov_b32 s82, 0x47fca0\n" \
"s_mov_b32 s83, 0x20000\n" \
"v_mfma_f32_16x16x32_bf16 v[196:199], a[4:7], v[80:83], 0\n" \
"ds_read_b64_tr_b16 a[4:5], v34 offset:9440\n" \
"ds_read_b64_tr_b16 a[6:7], v34 offset:14048\n" \
"v_mfma_f32_16x16x32_bf16 v[128:131], a[8:11], a[220:223], 0\n" \
"v_readlane_b32 s65, v11, 26\n" \
"s_add_u32 m0, s66, 0x0\n" \
"v_mfma_f32_16x16x32_bf16 v[132:135], a[8:11], a[236:239], 0\n" \
"v_readlane_b32 s67, v11, 29\n" \
"v_lshrrev_b32_e32 v44, 9, v20\n" \
"v_mfma_f32_16x16x32_bf16 v[200:203], a[8:11], v[64:67], 0\n" \
"buffer_load_dwordx4 v0, s[80:83], s65 offen offset:0 lds\n" \
"v_mfma_f32_16x16x32_bf16 v[204:207], a[8:11], v[80:83], 0\n" \
"ds_read_b64_tr_b16 a[8:9], v34 offset:9472\n" \
"ds_read_b64_tr_b16 a[10:11], v34 offset:14080\n" \
"v_mfma_f32_16x16x32_bf16 v[136:139], a[12:15], a[220:223], 0\n" \
"v_mul_u32_u24_e32 v44, 0xb40, v44\n" \
"v_add_u32_e32 v35, v44, v10\n" \
"v_mfma_f32_16x16x32_bf16 v[140:143], a[12:15], a[236:239], 0\n" \
"v_readlane_b32 s69, v11, 22\n" \
"v_readlane_b32 s68, v11, 27\n" \
"v_mfma_f32_16x16x32_bf16 v[208:211], a[12:15], v[64:67], 0\n" \
"v_readlane_b32 s94, v11, 23\n" \
"v_readlane_b32 s93, v11, 28\n" \
"v_mfma_f32_16x16x32_bf16 v[212:215], a[12:15], v[80:83], 0\n" \
"ds_read_b64_tr_b16 a[12:13], v34 offset:18432\n" \
"ds_read_b64_tr_b16 a[14:15], v34 offset:23040\n" \
"v_mfma_f32_16x16x32_bf16 v[144:147], a[16:19], a[220:223], 0\n" \
"buffer_load_dwordx4 v1, s[80:83], s65 offen offset:1024 lds\n" \
"v_mfma_f32_16x16x32_bf16 v[148:151], a[16:19], a[236:239], 0\n" \
"v_readlane_b32 s96, v11, 24\n" \
"v_readlane_b32 s84, v11, 9\n" \
"v_mfma_f32_16x16x32_bf16 v[216:219], a[16:19], v[64:67], 0\n" \
"v_readlane_b32 s85, v11, 10\n" \
"s_mov_b32 s86, 0xa1ca0\n" \
"v_mfma_f32_16x16x32_bf16 v[220:223], a[16:19], v[80:83], 0\n" \
"ds_read_b64_tr_b16 a[16:17], v34 offset:18464\n" \
"s_waitcnt lgkmcnt(7)\n" \
"v_mfma_f32_16x16x32_bf16 v[152:155], a[20:23], a[220:223], 0\n" \
"ds_read_b64_tr_b16 a[18:19], v34 offset:23072\n" \
"s_mov_b32 s87, 0x20000\n" \
"v_mfma_f32_16x16x32_bf16 v[156:159], a[20:23], a[236:239], 0\n" \
"buffer_load_dwordx4 v2, s[80:83], s65 offen offset:2048 lds\n" \
"v_mfma_f32_16x16x32_bf16 v[224:227], a[20:23], v[64:67], 0\n" \
"v_readlane_b32 s70, v11, 30\n" \
"v_add_u32_e32 v33, 0x10000, v10\n" \
"v_mfma_f32_16x16x32_bf16 v[228:231], a[20:23], v[80:83], 0\n" \
"ds_read_b64_tr_b16 a[20:21], v34 offset:18496\n" \
"ds_read_b64_tr_b16 a[22:23], v34 offset:23104\n" \
"v_mfma_f32_16x16x32_bf16 v[160:163], a[24:27], a[220:223], 0\n" \
"v_readlane_b32 s71, v11, 31\n" \
"v_readlane_b32 s92, v11, 33\n" \
"v_mfma_f32_16x16x32_bf16 v[164:167], a[24:27], a[236:239], 0\n" \
"v_readlane_b32 s95, v11, 34\n" \
"v_lshlrev_b32_e32 v41, 12, v20\n" \
"v_mfma_f32_16x16x32_bf16 v[232:235], a[24:27], v[64:67], 0\n" \
"buffer_load_dwordx4 v3, s[80:83], s65 offen offset:3072 lds\n" \
"v_mfma_f32_16x16x32_bf16 v[236:239], a[24:27], v[80:83], 0\n" \
"ds_read_b64_tr_b16 a[24:25], v34 offset:18528\n" \
"ds_read_b64_tr_b16 a[26:27], v34 offset:23136\n" \
"v_mfma_f32_16x16x32_bf16 v[168:171], a[28:31], a[220:223], 0\n" \
"s_add_u32 m0, s66, 0x1000\n" \
"v_readlane_b32 s97, v11, 37\n" \
"v_mfma_f32_16x16x32_bf16 v[172:175], a[28:31], a[236:239], 0\n" \
"v_readlane_b32 s63, v11, 40\n" \
"v_bfe_u32 v40, v20, 2, 7\n" \
"v_mfma_f32_16x16x32_bf16 v[240:243], a[28:31], v[64:67], 0\n" \
"s_add_u32 s74, s63, 0x3800\n" \
"v_readlane_b32 s100, v11, 7\n" \
"v_mfma_f32_16x16x32_bf16 v[244:247], a[28:31], v[80:83], 0\n" \
"ds_read_b64_tr_b16 a[28:29], v34 offset:18560\n" \
"ds_read_b64_tr_b16 a[30:31], v34 offset:23168\n" \
"v_mfma_f32_16x16x32_bf16 v[176:179], a[192:195], a[220:223], 0\n" \
"buffer_load_dwordx4 v4, s[80:83], s65 offen offset:0 lds\n" \
"v_mfma_f32_16x16x32_bf16 v[180:183], a[192:195], a[236:239], 0\n" \
"v_bfe_u32 v44, v20, 4, 4\n" \
"v_bfe_u32 v45, v20, 8, 2\n" \
"v_mfma_f32_16x16x32_bf16 v[248:251], a[192:195], v[64:67], 0\n" \
"v_mul_u32_u24_e32 v42, 0x480, v44\n" \
"v_lshl_add_u32 v42, v45, 3, v42\n" \
"v_mfma_f32_16x16x32_bf16 v[252:255], a[192:195], v[80:83], 0\n" \
"ds_read_b64_tr_b16 a[192:193], v34 offset:18592\n" \
"ds_read_b64_tr_b16 a[194:195], v34 offset:23200\n" \
"v_mfma_f32_16x16x32_bf16 v[112:115], a[196:199], a[224:227], v[112:115]\n" \
"v_lshrrev_b32_e32 v44, 1, v44\n" \
"v_lshlrev_b32_e32 v45, 1, v45\n" \
"v_mfma_f32_16x16x32_bf16 v[116:119], a[196:199], a[240:243], v[116:119]\n" \
"buffer_load_dwordx4 v5, s[80:83], s65 offen offset:1024 lds\n" \
"v_mfma_f32_16x16x32_bf16 v[184:187], a[196:199], v[68:71], v[184:187]\n" \
"v_sub_u32_e32 v44, v44, v45\n" \
"v_and_b32_e32 v45, 16, v20\n" \
"v_mfma_f32_16x16x32_bf16 v[188:191], a[196:199], v[84:87], v[188:191]\n" \
"ds_read_b64_tr_b16 a[196:197], v34 offset:18624\n" \
"ds_read_b64_tr_b16 a[198:199], v34 offset:23232\n" \
"v_mfma_f32_16x16x32_bf16 v[120:123], a[200:203], a[224:227], v[120:123]\n" \
"v_mov_b32_e32 v46, 0x3f80\n" \
"v_lshlrev_b32_e32 v45, v45, v46\n" \
"v_mfma_f32_16x16x32_bf16 v[124:127], a[200:203], a[240:243], v[124:127]\n" \
"v_mov_b32_e32 v52, 0\n" \
"v_mov_b32_e32 v53, 0\n" \
"v_mfma_f32_16x16x32_bf16 v[192:195], a[200:203], v[68:71], v[192:195]\n" \
"buffer_load_dwordx4 v6, s[80:83], s65 offen offset:2048 lds\n" \
"v_mfma_f32_16x16x32_bf16 v[196:199], a[200:203], v[84:87], v[196:199]\n" \
"ds_read_b64_tr_b16 a[200:201], v34 offset:18656\n" \
"ds_read_b64_tr_b16 a[202:203], v34 offset:23264\n" \
"v_mfma_f32_16x16x32_bf16 v[128:131], a[204:207], a[224:227], v[128:131]\n" \
"s_waitcnt vmcnt(15)\n" \
"v_cmp_ne_u32_e32 vcc, s62, v26\n" \
"v_mfma_f32_16x16x32_bf16 v[132:135], a[204:207], a[240:243], v[132:135]\n" \
"v_mov_b32_e32 v48, 0\n" \
"v_mov_b32_e32 v49, 0\n" \
"v_mfma_f32_16x16x32_bf16 v[200:203], a[204:207], v[68:71], v[200:203]\n" \
"v_add_u32_e32 v43, 0x9000, v42\n" \
"s_cbranch_vccnz L_sa_%=\n" \
"L_ra_%=:\n" \
"v_mfma_f32_16x16x32_bf16 v[204:207], a[204:207], v[84:87], v[204:207]\n" \
"ds_read_b64_tr_b16 a[204:205], v34 offset:18688\n" \
"ds_read_b64_tr_b16 a[206:207], v34 offset:23296\n" \
"v_mfma_f32_16x16x32_bf16 v[136:139], a[208:211], a[224:227], v[136:139]\n" \
"buffer_load_dwordx4 v7, s[80:83], s65 offen offset:3072 lds\n" \
"v_mfma_f32_16x16x32_bf16 v[140:143], a[208:211], a[240:243], v[140:143]\n" \
"s_add_u32 m0, s66, 0x2000\n" \
"v_readlane_b32 s66, v11, 32\n" \
"v_mfma_f32_16x16x32_bf16 v[208:211], a[208:211], v[68:71], v[208:211]\n" \
"v_readlane_b32 s64, v11, 38\n" \
"v_readlane_b32 s88, v11, 13\n" \
"v_mfma_f32_16x16x32_bf16 v[212:215], a[208:211], v[84:87], v[212:215]\n" \
"v_readlane_b32 s89, v11, 14\n" \
"s_mov_b32 s90, 0x240000\n" \
"v_mfma_f32_16x16x32_bf16 v[144:147], a[212:215], a[224:227], v[144:147]\n" \
"s_mov_b32 s91, 0x20000\n" \
"v_mov_b32_e32 v46, s62\n" \
"v_mfma_f32_16x16x32_bf16 v[148:151], a[212:215], a[240:243], v[148:151]\n" \
"buffer_load_dwordx4 v8, s[80:83], s65 offen offset:0 lds\n" \
"v_mfma_f32_16x16x32_bf16 v[216:219], a[212:215], v[68:71], v[216:219]\n" \
"s_add_u32 m0, s69, 0x0\n" \
"v_readlane_b32 s65, v11, 39\n" \
"v_mfma_f32_16x16x32_bf16 v[220:223], a[212:215], v[84:87], v[220:223]\n" \
"v_mfma_f32_16x16x32_bf16 v[152:155], a[216:219], a[224:227], v[152:155]\n" \
"v_mfma_f32_16x16x32_bf16 v[156:159], a[216:219], a[240:243], v[156:159]\n" \
"v_mfma_f32_16x16x32_bf16 v[224:227], a[216:219], v[68:71], v[224:227]\n" \
"buffer_load_dwordx4 a[64:67], v20, s[56:59], s67 offen offset:0 sc1\n" \
"v_mfma_f32_16x16x32_bf16 v[228:231], a[216:219], v[84:87], v[228:231]\n" \
"v_mfma_f32_16x16x32_bf16 v[160:163], a[0:3], a[224:227], v[160:163]\n" \
"v_mfma_f32_16x16x32_bf16 v[164:167], a[0:3], a[240:243], v[164:167]\n" \
"v_mfma_f32_16x16x32_bf16 v[232:235], a[0:3], v[68:71], v[232:235]\n" \
"v_mfma_f32_16x16x32_bf16 v[236:239], a[0:3], v[84:87], v[236:239]\n" \
"s_waitcnt lgkmcnt(0)\n" \
"v_mfma_f32_16x16x32_bf16 v[168:171], a[4:7], a[224:227], v[168:171]\n" \
"buffer_load_dwordx4 a[80:83], v32, s[56:59], s67 offen offset:0 sc1\n" \
"v_mfma_f32_16x16x32_bf16 v[172:175], a[4:7], a[240:243], v[172:175]\n" \
"v_mfma_f32_16x16x32_bf16 v[240:243], a[4:7], v[68:71], v[240:243]\n" \
"v_mfma_f32_16x16x32_bf16 v[244:247], a[4:7], v[84:87], v[244:247]\n" \
"v_mfma_f32_16x16x32_bf16 v[176:179], a[8:11], a[224:227], v[176:179]\n" \
"v_mfma_f32_16x16x32_bf16 v[180:183], a[8:11], a[240:243], v[180:183]\n" \
"buffer_load_dwordx4 a[68:71], v20, s[56:59], s67 offen offset:1024 sc1\n" \
"v_mfma_f32_16x16x32_bf16 v[248:251], a[8:11], v[68:71], v[248:251]\n" \
"v_mfma_f32_16x16x32_bf16 v[252:255], a[8:11], v[84:87], v[252:255]\n" \
"v_mfma_f32_16x16x32_bf16 v[112:115], a[12:15], a[228:231], v[112:115]\n" \
"v_mfma_f32_16x16x32_bf16 v[116:119], a[12:15], a[244:247], v[116:119]\n" \
"v_mfma_f32_16x16x32_bf16 v[184:187], a[12:15], v[72:75], v[184:187]\n" \
"buffer_load_dwordx4 a[84:87], v32, s[56:59], s67 offen offset:1024 sc1\n" \
"v_mfma_f32_16x16x32_bf16 v[188:191], a[12:15], v[96:99], v[188:191]\n" \
"v_mfma_f32_16x16x32_bf16 v[120:123], a[16:19], a[228:231], v[120:123]\n" \
"v_mfma_f32_16x16x32_bf16 v[124:127], a[16:19], a[244:247], v[124:127]\n" \
"v_mfma_f32_16x16x32_bf16 v[192:195], a[16:19], v[72:75], v[192:195]\n" \
"v_mfma_f32_16x16x32_bf16 v[196:199], a[16:19], v[96:99], v[196:199]\n" \
"buffer_load_dwordx4 a[72:75], v20, s[56:59], s67 offen offset:2048 sc1\n" \
"v_mfma_f32_16x16x32_bf16 v[128:131], a[20:23], a[228:231], v[128:131]\n" \
"v_mfma_f32_16x16x32_bf16 v[132:135], a[20:23], a[244:247], v[132:135]\n" \
"v_mfma_f32_16x16x32_bf16 v[200:203], a[20:23], v[72:75], v[200:203]\n" \
"v_mfma_f32_16x16x32_bf16 v[204:207], a[20:23], v[96:99], v[204:207]\n" \
"s_barrier\n" \
"v_mfma_f32_16x16x32_bf16 v[136:139], a[24:27], a[228:231], v[136:139]\n" \
"ds_read_b64_tr_b16 a[208:209], v10 offset:52224\n" \
"ds_read_b64_tr_b16 a[210:211], v10 offset:56832\n" \
"v_mfma_f32_16x16x32_bf16 v[140:143], a[24:27], a[244:247], v[140:143]\n" \
"ds_read_b64_tr_b16 a[212:213], v10 offset:52256\n" \
"ds_read_b64_tr_b16 a[214:215], v10 offset:56864\n" \
"v_mfma_f32_16x16x32_bf16 v[208:211], a[24:27], v[72:75], v[208:211]\n" \
"ds_read_b64_tr_b16 a[216:217], v10 offset:52288\n" \
"ds_read_b64_tr_b16 a[218:219], v10 offset:56896\n" \
"v_mfma_f32_16x16x32_bf16 v[212:215], a[24:27], v[96:99], v[212:215]\n" \
"ds_read_b64_tr_b16 a[0:1], v10 offset:52320\n" \
"ds_read_b64_tr_b16 a[2:3], v10 offset:56928\n" \
"v_mfma_f32_16x16x32_bf16 v[144:147], a[28:31], a[228:231], v[144:147]\n" \
"ds_read_b64_tr_b16 a[4:5], v10 offset:52352\n" \
"ds_read_b64_tr_b16 a[6:7], v10 offset:56960\n" \
"v_mfma_f32_16x16x32_bf16 v[148:151], a[28:31], a[244:247], v[148:151]\n" \
"ds_read_b64_tr_b16 a[8:9], v10 offset:52384\n" \
"ds_read_b64_tr_b16 a[10:11], v10 offset:56992\n" \
"v_mfma_f32_16x16x32_bf16 v[216:219], a[28:31], v[72:75], v[216:219]\n" \
"ds_read_b64_tr_b16 a[12:13], v10 offset:52416\n" \
"ds_read_b64_tr_b16 a[14:15], v10 offset:57024\n" \
"v_mfma_f32_16x16x32_bf16 v[220:223], a[28:31], v[96:99], v[220:223]\n" \
"ds_read_b64_tr_b16 a[16:17], v10 offset:52448\n" \
"ds_read_b64_tr_b16 a[18:19], v10 offset:57056\n" \
"v_mfma_f32_16x16x32_bf16 v[152:155], a[192:195], a[228:231], v[152:155]\n" \
"ds_read_b64_tr_b16 a[20:21], v10 offset:52480\n" \
"ds_read_b64_tr_b16 a[22:23], v10 offset:57088\n" \
"v_mfma_f32_16x16x32_bf16 v[156:159], a[192:195], a[244:247], v[156:159]\n" \
"ds_read_b64_tr_b16 a[24:25], v35 offset:24576\n" \
"ds_read_b64_tr_b16 a[26:27], v35 offset:26880\n" \
"v_mfma_f32_16x16x32_bf16 v[224:227], a[192:195], v[72:75], v[224:227]\n" \
"ds_read_b64_tr_b16 a[28:29], v35 offset:24608\n" \
"ds_read_b64_tr_b16 a[30:31], v35 offset:26912\n" \
"v_mfma_f32_16x16x32_bf16 v[228:231], a[192:195], v[96:99], v[228:231]\n" \
"ds_read_b64_tr_b16 a[192:193], v35 offset:24640\n" \
"ds_read_b64_tr_b16 a[194:195], v35 offset:26944\n" \
"v_mfma_f32_16x16x32_bf16 v[160:163], a[196:199], a[228:231], v[160:163]\n" \
"buffer_load_dwordx4 a[88:91], v32, s[56:59], s67 offen offset:2048 sc1\n" \
"v_mfma_f32_16x16x32_bf16 v[164:167], a[196:199], a[244:247], v[164:167]\n" \
"v_mfma_f32_16x16x32_bf16 v[232:235], a[196:199], v[72:75], v[232:235]\n" \
"v_mfma_f32_16x16x32_bf16 v[236:239], a[196:199], v[96:99], v[236:239]\n" \
"ds_read_b64_tr_b16 a[196:197], v35 offset:24672\n" \
"ds_read_b64_tr_b16 a[198:199], v35 offset:26976\n" \
"v_mfma_f32_16x16x32_bf16 v[168:171], a[200:203], a[228:231], v[168:171]\n" \
"v_mfma_f32_16x16x32_bf16 v[172:175], a[200:203], a[244:247], v[172:175]\n" \
"buffer_load_dwordx4 v0, s[80:83], s68 offen offset:0 lds\n" \
"v_mfma_f32_16x16x32_bf16 v[240:243], a[200:203], v[72:75], v[240:243]\n" \
"v_mfma_f32_16x16x32_bf16 v[244:247], a[200:203], v[96:99], v[244:247]\n" \
"ds_read_b64_tr_b16 a[200:201], v35 offset:24704\n" \
"ds_read_b64_tr_b16 a[202:203], v35 offset:27008\n" \
"v_mfma_f32_16x16x32_bf16 v[176:179], a[204:207], a[228:231], v[176:179]\n" \
"v_mfma_f32_16x16x32_bf16 v[180:183], a[204:207], a[244:247], v[180:183]\n" \
"v_mfma_f32_16x16x32_bf16 v[248:251], a[204:207], v[72:75], v[248:251]\n" \
"buffer_load_dwordx4 v1, s[80:83], s68 offen offset:1024 lds\n" \
"v_mfma_f32_16x16x32_bf16 v[252:255], a[204:207], v[96:99], v[252:255]\n" \
"ds_read_b64_tr_b16 a[204:205], v35 offset:24736\n" \
"s_waitcnt lgkmcnt(9)\n" \
"v_mfma_f32_16x16x32_bf16 v[112:115], a[208:211], a[232:235], v[112:115]\n" \
"ds_read_b64_tr_b16 a[206:207], v35 offset:27040\n" \
"v_mfma_f32_16x16x32_bf16 v[116:119], a[208:211], a[248:251], v[116:119]\n" \
"v_mfma_f32_16x16x32_bf16 v[184:187], a[208:211], v[76:79], v[184:187]\n" \
"v_mfma_f32_16x16x32_bf16 v[188:191], a[208:211], v[100:103], v[188:191]\n" \
"ds_read_b64_tr_b16 a[208:209], v35 offset:24768\n" \
"ds_read_b64_tr_b16 a[210:211], v35 offset:27072\n" \
"v_mfma_f32_16x16x32_bf16 v[120:123], a[212:215], a[232:235], v[120:123]\n" \
"buffer_load_dwordx4 v2, s[80:83], s68 offen offset:2048 lds\n" \
"v_mfma_f32_16x16x32_bf16 v[124:127], a[212:215], a[248:251], v[124:127]\n" \
"v_mfma_f32_16x16x32_bf16 v[192:195], a[212:215], v[76:79], v[192:195]\n" \
"s_waitcnt vmcnt(18)\n" \
"s_mov_b64 exec, 1\n" \
"buffer_store_dword v39, off, s[56:59], s72 offset:0 sc1\n" \
"s_mov_b64 exec, -1\n" \
"v_mfma_f32_16x16x32_bf16 v[196:199], a[212:215], v[100:103], v[196:199]\n" \
"ds_read_b64_tr_b16 a[212:213], v35 offset:24800\n" \
"ds_read_b64_tr_b16 a[214:215], v35 offset:27104\n" \
"v_mfma_f32_16x16x32_bf16 v[128:131], a[216:219], a[232:235], v[128:131]\n" \
"v_readlane_b32 s72, v11, 50\n" \
"v_mfma_f32_16x16x32_bf16 v[132:135], a[216:219], a[248:251], v[132:135]\n" \
"v_mfma_f32_16x16x32_bf16 v[200:203], a[216:219], v[76:79], v[200:203]\n" \
"buffer_load_dwordx4 v3, s[80:83], s68 offen offset:3072 lds\n" \
"v_mfma_f32_16x16x32_bf16 v[204:207], a[216:219], v[100:103], v[204:207]\n" \
"ds_read_b64_tr_b16 a[216:217], v35 offset:24832\n" \
"ds_read_b64_tr_b16 a[218:219], v35 offset:27136\n" \
"v_mfma_f32_16x16x32_bf16 v[136:139], a[0:3], a[232:235], v[136:139]\n" \
"s_add_u32 m0, s69, 0x1000\n" \
"v_mfma_f32_16x16x32_bf16 v[140:143], a[0:3], a[248:251], v[140:143]\n" \
"v_mfma_f32_16x16x32_bf16 v[208:211], a[0:3], v[76:79], v[208:211]\n" \
"v_mfma_f32_16x16x32_bf16 v[212:215], a[0:3], v[100:103], v[212:215]\n" \
"ds_read_b64_tr_b16 a[0:1], v35 offset:34944\n" \
"ds_read_b64_tr_b16 a[2:3], v35 offset:37248\n" \
"v_mfma_f32_16x16x32_bf16 v[144:147], a[4:7], a[232:235], v[144:147]\n" \
"buffer_load_dwordx4 v4, s[80:83], s68 offen offset:0 lds\n" \
"v_mfma_f32_16x16x32_bf16 v[148:151], a[4:7], a[248:251], v[148:151]\n" \
"v_mfma_f32_16x16x32_bf16 v[216:219], a[4:7], v[76:79], v[216:219]\n" \
"v_mfma_f32_16x16x32_bf16 v[220:223], a[4:7], v[100:103], v[220:223]\n" \
"ds_read_b64_tr_b16 a[4:5], v35 offset:34976\n" \
"ds_read_b64_tr_b16 a[6:7], v35 offset:37280\n" \
"v_mfma_f32_16x16x32_bf16 v[152:155], a[8:11], a[232:235], v[152:155]\n" \
"v_mfma_f32_16x16x32_bf16 v[156:159], a[8:11], a[248:251], v[156:159]\n" \
"buffer_load_dwordx4 v5, s[80:83], s68 offen offset:1024 lds\n" \
"v_mfma_f32_16x16x32_bf16 v[224:227], a[8:11], v[76:79], v[224:227]\n" \
"v_mfma_f32_16x16x32_bf16 v[228:231], a[8:11], v[100:103], v[228:231]\n" \
"ds_read_b64_tr_b16 a[8:9], v35 offset:35008\n" \
"ds_read_b64_tr_b16 a[10:11], v35 offset:37312\n" \
"v_mfma_f32_16x16x32_bf16 v[160:163], a[12:15], a[232:235], v[160:163]\n" \
"v_mfma_f32_16x16x32_bf16 v[164:167], a[12:15], a[248:251], v[164:167]\n" \
"v_mfma_f32_16x16x32_bf16 v[232:235], a[12:15], v[76:79], v[232:235]\n" \
"buffer_load_dwordx4 v6, s[80:83], s68 offen offset:2048 lds\n" \
"v_mfma_f32_16x16x32_bf16 v[236:239], a[12:15], v[100:103], v[236:239]\n" \
"ds_read_b64_tr_b16 a[12:13], v35 offset:35040\n" \
"ds_read_b64_tr_b16 a[14:15], v35 offset:37344\n" \
"v_mfma_f32_16x16x32_bf16 v[168:171], a[16:19], a[232:235], v[168:171]\n" \
"v_mfma_f32_16x16x32_bf16 v[172:175], a[16:19], a[248:251], v[172:175]\n" \
"v_mfma_f32_16x16x32_bf16 v[240:243], a[16:19], v[76:79], v[240:243]\n" \
"v_mfma_f32_16x16x32_bf16 v[244:247], a[16:19], v[100:103], v[244:247]\n" \
"buffer_load_dwordx4 v7, s[80:83], s68 offen offset:3072 lds\n" \
"v_mfma_f32_16x16x32_bf16 v[176:179], a[20:23], a[232:235], v[176:179]\n" \
"s_add_u32 m0, s69, 0x2000\n" \
"ds_read_b64_tr_b16 a[16:17], v35 offset:35072\n" \
"v_mfma_f32_16x16x32_bf16 v[180:183], a[20:23], a[248:251], v[180:183]\n" \
"ds_read_b64_tr_b16 a[18:19], v35 offset:37376\n" \
"v_readlane_b32 s69, v11, 47\n" \
"v_mfma_f32_16x16x32_bf16 v[248:251], a[20:23], v[76:79], v[248:251]\n" \
"v_mfma_f32_16x16x32_bf16 v[252:255], a[20:23], v[100:103], v[252:255]\n" \
"ds_read_b64_tr_b16 a[20:21], v35 offset:35104\n" \
"ds_read_b64_tr_b16 a[22:23], v35 offset:37408\n" \
"v_mfma_f32_16x16x32_bf16 v[112:115], a[24:27], a[96:99], v[112:115]\n" \
"buffer_load_dwordx4 v8, s[80:83], s68 offen offset:0 lds\n" \
"v_mfma_f32_16x16x32_bf16 v[116:119], a[24:27], a[104:107], v[116:119]\n" \
"s_add_u32 m0, s94, 0x0\n" \
"v_readlane_b32 s68, v11, 46\n" \
"v_mfma_f32_16x16x32_bf16 v[184:187], a[24:27], a[112:115], v[184:187]\n" \
"v_mfma_f32_16x16x32_bf16 v[188:191], a[24:27], a[120:123], v[188:191]\n" \
"ds_read_b64_tr_b16 a[24:25], v35 offset:35136\n" \
"s_waitcnt lgkmcnt(7)\n" \
"v_mfma_f32_16x16x32_bf16 v[120:123], a[28:31], a[96:99], v[120:123]\n" \
"ds_read_b64_tr_b16 a[26:27], v35 offset:37440\n" \
"buffer_load_dword v47, v38, s[56:59], s73 offen sc1\n" \
"v_mfma_f32_16x16x32_bf16 v[124:127], a[28:31], a[104:107], v[124:127]\n" \
"v_mfma_f32_16x16x32_bf16 v[192:195], a[28:31], a[112:115], v[192:195]\n" \
"v_mfma_f32_16x16x32_bf16 v[196:199], a[28:31], a[120:123], v[196:199]\n" \
"ds_read_b64_tr_b16 a[28:29], v35 offset:35168\n" \
"ds_read_b64_tr_b16 a[30:31], v35 offset:37472\n" \
"v_mfma_f32_16x16x32_bf16 v[128:131], a[192:195], a[96:99], v[128:131]\n" \
"v_mfma_f32_16x16x32_bf16 v[132:135], a[192:195], a[104:107], v[132:135]\n" \
"buffer_load_dwordx4 v0, s[80:83], s93 offen offset:0 lds\n" \
"v_mfma_f32_16x16x32_bf16 v[200:203], a[192:195], a[112:115], v[200:203]\n" \
"v_mfma_f32_16x16x32_bf16 v[204:207], a[192:195], a[120:123], v[204:207]\n" \
"ds_read_b64_tr_b16 a[192:193], v35 offset:35200\n" \
"ds_read_b64_tr_b16 a[194:195], v35 offset:37504\n" \
"v_mfma_f32_16x16x32_bf16 v[136:139], a[196:199], a[96:99], v[136:139]\n" \
"v_mfma_f32_16x16x32_bf16 v[140:143], a[196:199], a[104:107], v[140:143]\n" \
"v_mfma_f32_16x16x32_bf16 v[208:211], a[196:199], a[112:115], v[208:211]\n" \
"buffer_load_dwordx4 v1, s[80:83], s93 offen offset:1024 lds\n" \
"v_mfma_f32_16x16x32_bf16 v[212:215], a[196:199], a[120:123], v[212:215]\n" \
"v_mfma_f32_16x16x32_bf16 v[144:147], a[200:203], a[96:99], v[144:147]\n" \
"v_mfma_f32_16x16x32_bf16 v[148:151], a[200:203], a[104:107], v[148:151]\n" \
"v_mfma_f32_16x16x32_bf16 v[216:219], a[200:203], a[112:115], v[216:219]\n" \
"v_mfma_f32_16x16x32_bf16 v[220:223], a[200:203], a[120:123], v[220:223]\n" \
"buffer_load_dwordx4 v2, s[80:83], s93 offen offset:2048 lds\n" \
"v_mfma_f32_16x16x32_bf16 v[152:155], a[204:207], a[96:99], v[152:155]\n" \
"v_mfma_f32_16x16x32_bf16 v[156:159], a[204:207], a[104:107], v[156:159]\n" \
"v_mfma_f32_16x16x32_bf16 v[224:227], a[204:207], a[112:115], v[224:227]\n" \
"v_mfma_f32_16x16x32_bf16 v[228:231], a[204:207], a[120:123], v[228:231]\n" \
"v_mfma_f32_16x16x32_bf16 v[160:163], a[208:211], a[96:99], v[160:163]\n" \
"buffer_load_dwordx4 v3, s[80:83], s93 offen offset:3072 lds\n" \
"v_mfma_f32_16x16x32_bf16 v[164:167], a[208:211], a[104:107], v[164:167]\n" \
"s_add_u32 m0, s94, 0x1000\n" \
"v_mfma_f32_16x16x32_bf16 v[232:235], a[208:211], a[112:115], v[232:235]\n" \
"v_mfma_f32_16x16x32_bf16 v[236:239], a[208:211], a[120:123], v[236:239]\n" \
"v_mfma_f32_16x16x32_bf16 v[168:171], a[212:215], a[96:99], v[168:171]\n" \
"v_mfma_f32_16x16x32_bf16 v[172:175], a[212:215], a[104:107], v[172:175]\n" \
"buffer_load_dwordx4 v4, s[80:83], s93 offen offset:0 lds\n" \
"v_mfma_f32_16x16x32_bf16 v[240:243], a[212:215], a[112:115], v[240:243]\n" \
"v_mfma_f32_16x16x32_bf16 v[244:247], a[212:215], a[120:123], v[244:247]\n" \
"v_mfma_f32_16x16x32_bf16 v[176:179], a[216:219], a[96:99], v[176:179]\n" \
"v_mfma_f32_16x16x32_bf16 v[180:183], a[216:219], a[104:107], v[180:183]\n" \
"v_mfma_f32_16x16x32_bf16 v[248:251], a[216:219], a[112:115], v[248:251]\n" \
"buffer_load_dwordx4 v5, s[80:83], s93 offen offset:1024 lds\n" \
"v_mfma_f32_16x16x32_bf16 v[252:255], a[216:219], a[120:123], v[252:255]\n" \
"v_mfma_f32_16x16x32_bf16 v[112:115], a[0:3], a[100:103], v[112:115]\n" \
"v_mfma_f32_16x16x32_bf16 v[116:119], a[0:3], a[108:111], v[116:119]\n" \
"v_mfma_f32_16x16x32_bf16 v[184:187], a[0:3], a[116:119], v[184:187]\n" \
"v_mfma_f32_16x16x32_bf16 v[188:191], a[0:3], a[124:127], v[188:191]\n" \
"buffer_load_dwordx4 v6, s[80:83], s93 offen offset:2048 lds\n" \
"v_mfma_f32_16x16x32_bf16 v[120:123], a[4:7], a[100:103], v[120:123]\n" \
"v_mfma_f32_16x16x32_bf16 v[124:127], a[4:7], a[108:111], v[124:127]\n" \
"v_mfma_f32_16x16x32_bf16 v[192:195], a[4:7], a[116:119], v[192:195]\n" \
"v_mfma_f32_16x16x32_bf16 v[196:199], a[4:7], a[124:127], v[196:199]\n" \
"v_mfma_f32_16x16x32_bf16 v[128:131], a[8:11], a[100:103], v[128:131]\n" \
"buffer_load_dwordx4 v7, s[80:83], s93 offen offset:3072 lds\n" \
"s_add_u32 m0, s94, 0x2000\n" \
"v_mfma_f32_16x16x32_bf16 v[132:135], a[8:11], a[108:111], v[132:135]\n" \
"v_readlane_b32 s94, v11, 45\n" \
"v_mfma_f32_16x16x32_bf16 v[200:203], a[8:11], a[116:119], v[200:203]\n" \
"v_mfma_f32_16x16x32_bf16 v[204:207], a[8:11], a[124:127], v[204:207]\n" \
"s_waitcnt lgkmcnt(0)\n" \
"v_mfma_f32_16x16x32_bf16 v[136:139], a[12:15], a[100:103], v[136:139]\n" \
"buffer_load_dwordx4 v8, s[80:83], s93 offen offset:0 lds\n" \
"s_mov_b32 m0, s96\n" \
"v_mfma_f32_16x16x32_bf16 v[140:143], a[12:15], a[108:111], v[140:143]\n" \
"v_readlane_b32 s96, v11, 47\n" \
"v_readlane_b32 s93, v11, 6\n" \
"v_mfma_f32_16x16x32_bf16 v[208:211], a[12:15], a[116:119], v[208:211]\n" \
"v_mfma_f32_16x16x32_bf16 v[212:215], a[12:15], a[124:127], v[212:215]\n" \
"s_waitcnt vmcnt(23)\n" \
"s_barrier\n" \
"buffer_load_dwordx4 v9, s[84:87], 0 offen lds\n" \
"v_mfma_f32_16x16x32_bf16 v[144:147], a[16:19], a[100:103], v[144:147]\n" \
"ds_read_b64_tr_b16 a[196:197], v33 offset:0\n" \
"ds_read_b64_tr_b16 a[198:199], v33 offset:4608\n" \
"v_mfma_f32_16x16x32_bf16 v[148:151], a[16:19], a[108:111], v[148:151]\n" \
"ds_read_b64_tr_b16 a[200:201], v33 offset:32\n" \
"ds_read_b64_tr_b16 a[202:203], v33 offset:4640\n" \
"v_mfma_f32_16x16x32_bf16 v[216:219], a[16:19], a[116:119], v[216:219]\n" \
"ds_read_b64_tr_b16 a[204:205], v33 offset:64\n" \
"ds_read_b64_tr_b16 a[206:207], v33 offset:4672\n" \
"v_mfma_f32_16x16x32_bf16 v[220:223], a[16:19], a[124:127], v[220:223]\n" \
"ds_read_b64_tr_b16 a[208:209], v33 offset:96\n" \
"ds_read_b64_tr_b16 a[210:211], v33 offset:4704\n" \
"buffer_load_dwordx4 a[76:79], v20, s[56:59], s67 offen offset:3072 sc1\n" \
"v_mfma_f32_16x16x32_bf16 v[152:155], a[20:23], a[100:103], v[152:155]\n" \
"ds_read_b64_tr_b16 a[212:213], v33 offset:128\n" \
"ds_read_b64_tr_b16 a[214:215], v33 offset:4736\n" \
"v_mfma_f32_16x16x32_bf16 v[156:159], a[20:23], a[108:111], v[156:159]\n" \
"ds_read_b64_tr_b16 a[216:217], v33 offset:160\n" \
"ds_read_b64_tr_b16 a[218:219], v33 offset:4768\n" \
"v_mfma_f32_16x16x32_bf16 v[224:227], a[20:23], a[116:119], v[224:227]\n" \
"ds_read_b64_tr_b16 a[0:1], v33 offset:192\n" \
"ds_read_b64_tr_b16 a[2:3], v33 offset:4800\n" \
"v_mfma_f32_16x16x32_bf16 v[228:231], a[20:23], a[124:127], v[228:231]\n" \
"ds_read_b64_tr_b16 a[4:5], v33 offset:224\n" \
"ds_read_b64_tr_b16 a[6:7], v33 offset:4832\n" \
"buffer_load_dwordx4 a[92:95], v32, s[56:59], s67 offen offset:3072 sc1\n" \
"v_mfma_f32_16x16x32_bf16 v[160:163], a[24:27], a[100:103], v[160:163]\n" \
"v_readlane_b32 s67, v11, 45\n" \
"ds_read_b64_tr_b16 a[8:9], v33 offset:256\n" \
"v_mfma_f32_16x16x32_bf16 v[164:167], a[24:27], a[108:111], v[164:167]\n" \
"ds_read_b64_tr_b16 a[10:11], v33 offset:4864\n" \
"ds_read_b64_tr_b16 a[12:13], v33 offset:9216\n" \
"v_mfma_f32_16x16x32_bf16 v[232:235], a[24:27], a[116:119], v[232:235]\n" \
"ds_read_b64_tr_b16 a[14:15], v33 offset:13824\n" \
"ds_read_b64_tr_b16 a[16:17], v33 offset:9248\n" \
"v_mfma_f32_16x16x32_bf16 v[236:239], a[24:27], a[124:127], v[236:239]\n" \
"ds_read_b64_tr_b16 a[18:19], v33 offset:13856\n" \
"ds_read_b64_tr_b16 a[20:21], v33 offset:9280\n" \
"buffer_load_dwordx4 a[128:131], v20, s[56:59], s70 offen offset:0 sc1\n" \
"v_mfma_f32_16x16x32_bf16 v[168:171], a[28:31], a[100:103], v[168:171]\n" \
"ds_read_b64_tr_b16 a[22:23], v33 offset:13888\n" \
"ds_read_b64_tr_b16 a[24:25], v33 offset:9312\n" \
"v_mfma_f32_16x16x32_bf16 v[172:175], a[28:31], a[108:111], v[172:175]\n" \
"ds_read_b64_tr_b16 a[26:27], v33 offset:13920\n" \
"s_waitcnt vmcnt(13)\n" \
"v_cmp_ne_u32_e32 vcc, s62, v47\n" \
"v_mfma_f32_16x16x32_bf16 v[240:243], a[28:31], a[116:119], v[240:243]\n" \
"v_mfma_f32_16x16x32_bf16 v[244:247], a[28:31], a[124:127], v[244:247]\n" \
"buffer_load_dwordx4 a[144:147], v32, s[56:59], s70 offen offset:0 sc1\n" \
"v_mfma_f32_16x16x32_bf16 v[176:179], a[192:195], a[100:103], v[176:179]\n" \
"ds_read_b64_tr_b16 a[28:29], v33 offset:9344\n" \
"ds_read_b64_tr_b16 a[30:31], v33 offset:13952\n" \
"v_mfma_f32_16x16x32_bf16 v[180:183], a[192:195], a[108:111], v[180:183]\n" \
"s_cbranch_vccnz L_sb_%=\n" \
"L_rb_%=:\n" \
"v_readlane_b32 s73, v11, 51\n" \
"v_mfma_f32_16x16x32_bf16 v[248:251], a[192:195], a[116:119], v[248:251]\n" \
"v_cmp_eq_u32_e32 vcc, 0, v44\n" \
"v_mfma_f32_16x16x32_bf16 v[252:255], a[192:195], a[124:127], v[252:255]\n" \
"ds_read_b64_tr_b16 a[192:193], v33 offset:9376\n" \
"buffer_load_dwordx4 a[132:135], v20, s[56:59], s70 offen offset:1024 sc1\n" \
"s_waitcnt lgkmcnt(14)\n" \
"v_mfma_f32_16x16x32_bf16 v[112:115], a[196:199], a[64:67], v[112:115]\n" \
"ds_read_b64_tr_b16 a[194:195], v33 offset:13984\n" \
"v_cndmask_b32_e32 v50, 0, v45, vcc\n" \
"v_mfma_f32_16x16x32_bf16 v[116:119], a[196:199], a[80:83], v[116:119]\n" \
"ds_read_b64_tr_b16 a[196:197], v33 offset:9408\n" \
"ds_read_b64_tr_b16 a[198:199], v33 offset:14016\n" \
"v_mfma_f32_16x16x32_bf16 v[120:123], a[200:203], a[64:67], v[120:123]\n" \
"v_cmp_eq_u32_e32 vcc, 1, v44\n" \
"v_and_b32_e32 v44, 0x3c0, v20\n" \
"v_mfma_f32_16x16x32_bf16 v[124:127], a[200:203], a[80:83], v[124:127]\n" \
"buffer_load_dwordx4 a[148:151], v32, s[56:59], s70 offen offset:1024 sc1\n" \
"v_mfma_f32_16x16x32_bf16 v[128:131], a[204:207], a[64:67], v[128:131]\n" \
"ds_read_b64_tr_b16 a[200:201], v33 offset:9440\n" \
"ds_read_b64_tr_b16 a[202:203], v33 offset:14048\n" \
"v_mfma_f32_16x16x32_bf16 v[132:135], a[204:207], a[80:83], v[132:135]\n" \
"ds_read_b64_tr_b16 a[204:205], v33 offset:9472\n" \
"ds_read_b64_tr_b16 a[206:207], v33 offset:14080\n" \
"v_mfma_f32_16x16x32_bf16 v[136:139], a[208:211], a[64:67], v[136:139]\n" \
"v_cndmask_b32_e32 v51, 0, v45, vcc\n" \
"v_bfe_u32 v45, v20, 1, 5\n" \
"v_mfma_f32_16x16x32_bf16 v[140:143], a[208:211], a[80:83], v[140:143]\n" \
"ds_read_b64_tr_b16 a[208:209], v33 offset:18432\n" \
"ds_read_b64_tr_b16 a[210:211], v33 offset:23040\n" \
"buffer_load_dwordx4 a[136:139], v20, s[56:59], s70 offen offset:2048 sc1\n" \
"v_mfma_f32_16x16x32_bf16 v[144:147], a[212:215], a[64:67], v[144:147]\n" \
"v_and_b32_e32 v45, 24, v45\n" \
"v_add_u32_e32 v45, 0xf000, v45\n" \
"v_mfma_f32_16x16x32_bf16 v[148:151], a[212:215], a[80:83], v[148:151]\n" \
"ds_read_b64_tr_b16 a[212:213], v33 offset:18464\n" \
"ds_read_b64_tr_b16 a[214:215], v33 offset:23072\n" \
"v_mfma_f32_16x16x32_bf16 v[152:155], a[216:219], a[64:67], v[152:155]\n" \
"v_add_u32_e32 v36, v44, v45\n" \
".fill (L_ab + 24576 - .) / 4, 4, 0xBF800000\n" \
"L_ap6:\n" \
"s_cbranch_execz L_ap0\n" \
"v_mfma_f32_16x16x32_bf16 v[156:159], a[216:219], a[80:83], v[156:159]\n" \
"ds_read_b64_tr_b16 a[216:217], v33 offset:18496\n" \
"ds_read_b64_tr_b16 a[218:219], v33 offset:23104\n" \
"buffer_load_dwordx4 a[152:155], v32, s[56:59], s70 offen offset:2048 sc1\n" \
"v_mfma_f32_16x16x32_bf16 v[160:163], a[0:3], a[64:67], v[160:163]\n" \
"v_mfma_f32_16x16x32_bf16 v[164:167], a[0:3], a[80:83], v[164:167]\n" \
"ds_read_b64_tr_b16 a[0:1], v33 offset:18528\n" \
"s_waitcnt lgkmcnt(13)\n" \
"v_mfma_f32_16x16x32_bf16 v[168:171], a[4:7], a[64:67], v[168:171]\n" \
"ds_read_b64_tr_b16 a[2:3], v33 offset:23136\n" \
"v_mfma_f32_16x16x32_bf16 v[172:175], a[4:7], a[80:83], v[172:175]\n" \
"ds_read_b64_tr_b16 a[4:5], v33 offset:18560\n" \
"ds_read_b64_tr_b16 a[6:7], v33 offset:23168\n" \
"buffer_load_dwordx4 a[140:143], v20, s[56:59], s70 offen offset:3072 sc1\n" \
"v_mfma_f32_16x16x32_bf16 v[176:179], a[8:11], a[64:67], v[176:179]\n" \
"v_mfma_f32_16x16x32_bf16 v[180:183], a[8:11], a[80:83], v[180:183]\n" \
"ds_read_b64_tr_b16 a[8:9], v33 offset:18592\n" \
"ds_read_b64_tr_b16 a[10:11], v33 offset:23200\n" \
"v_mfma_f32_16x16x32_bf16 v[112:115], a[12:15], a[68:71], v[112:115]\n" \
"v_mfma_f32_16x16x32_bf16 v[116:119], a[12:15], a[84:87], v[116:119]\n" \
"ds_read_b64_tr_b16 a[12:13], v33 offset:18624\n" \
"ds_read_b64_tr_b16 a[14:15], v33 offset:23232\n" \
"buffer_load_dwordx4 a[156:159], v32, s[56:59], s70 offen offset:3072 sc1\n" \
"v_mfma_f32_16x16x32_bf16 v[120:123], a[16:19], a[68:71], v[120:123]\n" \
"v_readlane_b32 s70, v11, 48\n" \
"v_mfma_f32_16x16x32_bf16 v[124:127], a[16:19], a[84:87], v[124:127]\n" \
"ds_read_b64_tr_b16 a[16:17], v33 offset:18656\n" \
"ds_read_b64_tr_b16 a[18:19], v33 offset:23264\n" \
"v_mfma_f32_16x16x32_bf16 v[128:131], a[20:23], a[68:71], v[128:131]\n" \
"v_mfma_f32_16x16x32_bf16 v[132:135], a[20:23], a[84:87], v[132:135]\n" \
"ds_read_b64_tr_b16 a[20:21], v33 offset:18688\n" \
"ds_read_b64_tr_b16 a[22:23], v33 offset:23296\n" \
"buffer_load_dwordx4 a[32:35], v20, s[56:59], s71 offen offset:0 sc1\n" \
"v_mfma_f32_16x16x32_bf16 v[136:139], a[24:27], a[68:71], v[136:139]\n" \
"v_mfma_f32_16x16x32_bf16 v[140:143], a[24:27], a[84:87], v[140:143]\n" \
"ds_read_b64_tr_b16 a[24:25], v33 offset:27648\n" \
"ds_read_b64_tr_b16 a[26:27], v33 offset:32256\n" \
"v_mfma_f32_16x16x32_bf16 v[144:147], a[28:31], a[68:71], v[144:147]\n" \
"v_mfma_f32_16x16x32_bf16 v[148:151], a[28:31], a[84:87], v[148:151]\n" \
"ds_read_b64_tr_b16 a[28:29], v33 offset:27680\n" \
"ds_read_b64_tr_b16 a[30:31], v33 offset:32288\n" \
"buffer_load_dwordx4 a[48:51], v32, s[56:59], s71 offen offset:0 sc1\n" \
"v_mfma_f32_16x16x32_bf16 v[152:155], a[192:195], a[68:71], v[152:155]\n" \
"v_mfma_f32_16x16x32_bf16 v[156:159], a[192:195], a[84:87], v[156:159]\n" \
"ds_read_b64_tr_b16 a[192:193], v33 offset:27712\n" \
"s_waitcnt lgkmcnt(13)\n" \
"v_mfma_f32_16x16x32_bf16 v[160:163], a[196:199], a[68:71], v[160:163]\n" \
"ds_read_b64_tr_b16 a[194:195], v33 offset:32320\n" \
"v_mfma_f32_16x16x32_bf16 v[164:167], a[196:199], a[84:87], v[164:167]\n" \
"ds_read_b64_tr_b16 a[196:197], v33 offset:27744\n" \
"ds_read_b64_tr_b16 a[198:199], v33 offset:32352\n" \
"buffer_load_dwordx4 a[36:39], v20, s[56:59], s71 offen offset:1024 sc1\n" \
"v_mfma_f32_16x16x32_bf16 v[168:171], a[200:203], a[68:71], v[168:171]\n" \
"v_mfma_f32_16x16x32_bf16 v[172:175], a[200:203], a[84:87], v[172:175]\n" \
"ds_read_b64_tr_b16 a[200:201], v33 offset:27776\n" \
"ds_read_b64_tr_b16 a[202:203], v33 offset:32384\n" \
"v_mfma_f32_16x16x32_bf16 v[176:179], a[204:207], a[68:71], v[176:179]\n" \
"v_mfma_f32_16x16x32_bf16 v[180:183], a[204:207], a[84:87], v[180:183]\n" \
"ds_read_b64_tr_b16 a[204:205], v33 offset:27808\n" \
"ds_read_b64_tr_b16 a[206:207], v33 offset:32416\n" \
"buffer_load_dwordx4 a[52:55], v32, s[56:59], s71 offen offset:1024 sc1\n" \
"v_mfma_f32_16x16x32_bf16 v[112:115], a[208:211], a[72:75], v[112:115]\n" \
"v_mfma_f32_16x16x32_bf16 v[116:119], a[208:211], a[88:91], v[116:119]\n" \
"ds_read_b64_tr_b16 a[208:209], v33 offset:27840\n" \
"ds_read_b64_tr_b16 a[210:211], v33 offset:32448\n" \
"v_mfma_f32_16x16x32_bf16 v[120:123], a[212:215], a[72:75], v[120:123]\n" \
"v_mfma_f32_16x16x32_bf16 v[124:127], a[212:215], a[88:91], v[124:127]\n" \
"ds_read_b64_tr_b16 a[212:213], v33 offset:27872\n" \
"ds_read_b64_tr_b16 a[214:215], v33 offset:32480\n" \
"buffer_load_dwordx4 a[40:43], v20, s[56:59], s71 offen offset:2048 sc1\n" \
"v_mfma_f32_16x16x32_bf16 v[128:131], a[216:219], a[72:75], v[128:131]\n" \
"v_mfma_f32_16x16x32_bf16 v[132:135], a[216:219], a[88:91], v[132:135]\n" \
"ds_read_b64_tr_b16 a[216:217], v33 offset:27904\n" \
"ds_read_b64_tr_b16 a[218:219], v33 offset:32512\n" \
"v_mfma_f32_16x16x32_bf16 v[136:139], a[0:3], a[72:75], v[136:139]\n" \
"v_mfma_f32_16x16x32_bf16 v[140:143], a[0:3], a[88:91], v[140:143]\n" \
"v_mfma_f32_16x16x32_bf16 v[144:147], a[4:7], a[72:75], v[144:147]\n" \
"buffer_load_dwordx4 a[56:59], v32, s[56:59], s71 offen offset:2048 sc1\n" \
"v_mfma_f32_16x16x32_bf16 v[148:151], a[4:7], a[88:91], v[148:151]\n" \
"s_waitcnt lgkmcnt(10)\n" \
"v_mfma_f32_16x16x32_bf16 v[152:155], a[8:11], a[72:75], v[152:155]\n" \
"v_mfma_f32_16x16x32_bf16 v[156:159], a[8:11], a[88:91], v[156:159]\n" \
"v_mfma_f32_16x16x32_bf16 v[160:163], a[12:15], a[72:75], v[160:163]\n" \
"v_mfma_f32_16x16x32_bf16 v[164:167], a[12:15], a[88:91], v[164:167]\n" \
"buffer_load_dwordx4 a[44:47], v20, s[56:59], s71 offen offset:3072 sc1\n" \
"v_mfma_f32_16x16x32_bf16 v[168:171], a[16:19], a[72:75], v[168:171]\n" \
"v_mfma_f32_16x16x32_bf16 v[172:175], a[16:19], a[88:91], v[172:175]\n" \
"v_mfma_f32_16x16x32_bf16 v[176:179], a[20:23], a[72:75], v[176:179]\n" \
"v_mfma_f32_16x16x32_bf16 v[180:183], a[20:23], a[88:91], v[180:183]\n" \
"buffer_load_dwordx4 a[60:63], v32, s[56:59], s71 offen offset:3072 sc1\n" \
"v_readlane_b32 s71, v11, 49\n" \
"buffer_load_dwordx4 a[96:99], v20, s[56:59], s66 offen offset:0 sc1\n" \
"buffer_load_dwordx4 a[112:115], v32, s[56:59], s66 offen offset:0 sc1\n" \
"s_waitcnt vmcnt(20)\n" \
"s_barrier\n" \
"ds_read_b64_tr_b16 a[0:1], v33 offset:36864\n" \
"ds_read_b64_tr_b16 a[2:3], v33 offset:41472\n" \
"ds_read_b64_tr_b16 a[4:5], v33 offset:46080\n" \
"ds_read_b64_tr_b16 a[6:7], v33 offset:50688\n" \
"ds_read_b64_tr_b16 a[8:9], v33 offset:55296\n" \
"ds_read_b64_tr_b16 a[10:11], v33 offset:59904\n" \
"ds_read_b64_tr_b16 a[12:13], v10 offset:0\n" \
"ds_read_b64_tr_b16 a[14:15], v10 offset:4608\n" \
"ds_read_b64_tr_b16 a[16:17], v34 offset:0\n" \
"ds_read_b64_tr_b16 a[18:19], v34 offset:4608\n" \
"ds_read_b64_tr_b16 a[20:21], v34 offset:9216\n" \
"ds_read_b64_tr_b16 a[22:23], v34 offset:13824\n" \
"buffer_load_dwordx4 a[100:103], v20, s[56:59], s66 offen offset:1024 sc1\n" \
"s_waitcnt vmcnt(20)\n" \
"v_mfma_f32_16x16x32_bf16 v[112:115], a[24:27], a[76:79], v[112:115]\n" \
"s_waitcnt vmcnt(19)\n" \
"v_mfma_f32_16x16x32_bf16 v[116:119], a[24:27], a[92:95], v[116:119]\n" \
"ds_read_b64_tr_b16 a[24:25], v34 offset:18432\n" \
"ds_read_b64_tr_b16 a[26:27], v34 offset:23040\n" \
"v_mfma_f32_16x16x32_bf16 v[120:123], a[28:31], a[76:79], v[120:123]\n" \
"buffer_load_dwordx4 a[116:119], v32, s[56:59], s66 offen offset:1024 sc1\n" \
"v_mfma_f32_16x16x32_bf16 v[124:127], a[28:31], a[92:95], v[124:127]\n" \
"ds_read_b64_tr_b16 a[28:29], v10 offset:9216\n" \
"ds_read_b64_tr_b16 a[30:31], v10 offset:13824\n" \
"v_mfma_f32_16x16x32_bf16 v[128:131], a[192:195], a[76:79], v[128:131]\n" \
"v_mfma_f32_16x16x32_bf16 v[132:135], a[192:195], a[92:95], v[132:135]\n" \
"ds_read_b64_tr_b16 a[192:193], v33 offset:36896\n" \
"ds_read_b64_tr_b16 a[194:195], v33 offset:41504\n" \
"v_mfma_f32_16x16x32_bf16 v[136:139], a[196:199], a[76:79], v[136:139]\n" \
"v_mfma_f32_16x16x32_bf16 v[140:143], a[196:199], a[92:95], v[140:143]\n" \
"ds_read_b64_tr_b16 a[196:197], v33 offset:46112\n" \
"s_waitcnt lgkmcnt(15)\n" \
"v_mfma_f32_16x16x32_bf16 v[144:147], a[200:203], a[76:79], v[144:147]\n" \
"buffer_load_dwordx4 a[104:107], v20, s[56:59], s66 offen offset:2048 sc1\n" \
"v_mfma_f32_16x16x32_bf16 v[148:151], a[200:203], a[92:95], v[148:151]\n" \
"ds_read_b64_tr_b16 a[198:199], v33 offset:50720\n" \
"ds_read_b64_tr_b16 a[200:201], v33 offset:55328\n" \
"v_mfma_f32_16x16x32_bf16 v[152:155], a[204:207], a[76:79], v[152:155]\n" \
"ds_read_b64_tr_b16 a[202:203], v33 offset:59936\n" \
"v_mfma_f32_16x16x32_bf16 v[156:159], a[204:207], a[92:95], v[156:159]\n" \
"ds_read_b64_tr_b16 a[204:205], v10 offset:32\n" \
"ds_read_b64_tr_b16 a[206:207], v10 offset:4640\n" \
"v_mfma_f32_16x16x32_bf16 v[160:163], a[208:211], a[76:79], v[160:163]\n" \
"v_mfma_f32_16x16x32_bf16 v[164:167], a[208:211], a[92:95], v[164:167]\n" \
"ds_read_b64_tr_b16 a[208:209], v34 offset:32\n" \
"ds_read_b64_tr_b16 a[210:211], v34 offset:4640\n" \
"v_mfma_f32_16x16x32_bf16 v[168:171], a[212:215], a[76:79], v[168:171]\n" \
"buffer_load_dwordx4 a[120:123], v32, s[56:59], s66 offen offset:2048 sc1\n" \
"v_mfma_f32_16x16x32_bf16 v[172:175], a[212:215], a[92:95], v[172:175]\n" \
"ds_read_b64_tr_b16 a[212:213], v34 offset:9248\n" \
"ds_read_b64_tr_b16 a[214:215], v34 offset:13856\n" \
"v_mfma_f32_16x16x32_bf16 v[176:179], a[216:219], a[76:79], v[176:179]\n" \
"v_mfma_f32_16x16x32_bf16 v[180:183], a[216:219], a[92:95], v[180:183]\n" \
"ds_read_b64_tr_b16 a[216:217], v34 offset:18464\n" \
"s_waitcnt vmcnt(21)\n" \
"v_mfma_f32_16x16x32_bf16 v[112:115], a[0:3], a[128:131], v[112:115]\n" \
"ds_read_b64_tr_b16 a[218:219], v34 offset:23072\n" \
"s_waitcnt vmcnt(20)\n" \
"v_mfma_f32_16x16x32_bf16 v[116:119], a[0:3], a[144:147], v[116:119]\n" \
"ds_read_b64_tr_b16 a[0:1], v10 offset:9248\n" \
"s_waitcnt vmcnt(19)\n" \
"v_mfma_f32_16x16x32_bf16 v[112:115], a[4:7], a[132:135], v[112:115]\n" \
"ds_read_b64_tr_b16 a[2:3], v10 offset:13856\n" \
"s_waitcnt vmcnt(18)\n" \
"v_mfma_f32_16x16x32_bf16 v[116:119], a[4:7], a[148:151], v[116:119]\n" \
"ds_read_b64_tr_b16 a[4:5], v33 offset:36928\n" \
"ds_read_b64_tr_b16 a[6:7], v33 offset:41536\n" \
"buffer_load_dwordx4 a[108:111], v20, s[56:59], s66 offen offset:3072 sc1\n" \
"s_waitcnt vmcnt(18) lgkmcnt(15)\n" \
"v_mfma_f32_16x16x32_bf16 v[112:115], a[8:11], a[136:139], v[112:115]\n" \
"buffer_load_dwordx4 a[124:127], v32, s[56:59], s66 offen offset:3072 sc1\n" \
"s_waitcnt vmcnt(18)\n" \
"v_mfma_f32_16x16x32_bf16 v[116:119], a[8:11], a[152:155], v[116:119]\n" \
"ds_read_b64_tr_b16 a[8:9], v33 offset:46144\n" \
"ds_read_b64_tr_b16 a[10:11], v33 offset:50752\n" \
"buffer_load_dwordx4 a[160:163], v20, s[56:59], s92 offen offset:0 sc1\n" \
"s_waitcnt vmcnt(18)\n" \
"v_mfma_f32_16x16x32_bf16 v[112:115], a[12:15], a[140:143], v[112:115]\n" \
"buffer_load_dwordx4 a[176:179], v32, s[56:59], s92 offen offset:0 sc1\n" \
"s_waitcnt vmcnt(18)\n" \
"v_mfma_f32_16x16x32_bf16 v[116:119], a[12:15], a[156:159], v[116:119]\n" \
"ds_read_b64_tr_b16 a[12:13], v33 offset:55360\n" \
"ds_read_b64_tr_b16 a[14:15], v33 offset:59968\n" \
"buffer_load_dwordx4 a[164:167], v20, s[56:59], s92 offen offset:1024 sc1\n" \
"s_waitcnt vmcnt(18)\n" \
"v_mfma_f32_16x16x32_bf16 v[112:115], a[16:19], a[32:35], v[112:115]\n" \
"buffer_load_dwordx4 a[180:183], v32, s[56:59], s92 offen offset:1024 sc1\n" \
"s_waitcnt vmcnt(18)\n" \
"v_mfma_f32_16x16x32_bf16 v[116:119], a[16:19], a[48:51], v[116:119]\n" \
"ds_read_b64_tr_b16 a[16:17], v10 offset:64\n" \
"ds_read_b64_tr_b16 a[18:19], v10 offset:4672\n" \
"buffer_load_dwordx4 a[168:171], v20, s[56:59], s92 offen offset:2048 sc1\n" \
"s_waitcnt vmcnt(18)\n" \
"v_mfma_f32_16x16x32_bf16 v[112:115], a[20:23], a[36:39], v[112:115]\n" \
"buffer_load_dwordx4 a[184:187], v32, s[56:59], s92 offen offset:2048 sc1\n" \
"s_waitcnt vmcnt(18)\n" \
"v_mfma_f32_16x16x32_bf16 v[116:119], a[20:23], a[52:55], v[116:119]\n" \
"ds_read_b64_tr_b16 a[20:21], v34 offset:64\n" \
"ds_read_b64_tr_b16 a[22:23], v34 offset:4672\n" \
"buffer_load_dwordx4 a[172:175], v20, s[56:59], s92 offen offset:3072 sc1\n" \
"s_waitcnt vmcnt(18)\n" \
"v_mfma_f32_16x16x32_bf16 v[112:115], a[24:27], a[40:43], v[112:115]\n" \
"buffer_load_dwordx4 a[188:191], v32, s[56:59], s92 offen offset:3072 sc1\n" \
"v_readlane_b32 s92, v11, 52\n" \
"s_waitcnt vmcnt(18)\n" \
"v_mfma_f32_16x16x32_bf16 v[116:119], a[24:27], a[56:59], v[116:119]\n" \
"ds_read_b64_tr_b16 a[24:25], v34 offset:9280\n" \
"ds_read_b64_tr_b16 a[26:27], v34 offset:13888\n" \
"buffer_load_dwordx4 a[220:223], v20, s[56:59], s95 offen offset:0 sc1\n" \
"s_waitcnt vmcnt(18)\n" \
"v_mfma_f32_16x16x32_bf16 v[112:115], a[28:31], a[44:47], v[112:115]\n" \
"buffer_load_dwordx4 a[236:239], v32, s[56:59], s95 offen offset:0 sc1\n" \
"s_waitcnt vmcnt(18)\n" \
"v_mfma_f32_16x16x32_bf16 v[116:119], a[28:31], a[60:63], v[116:119]\n" \
"ds_read_b64_tr_b16 a[28:29], v34 offset:18496\n" \
"ds_read_b64_tr_b16 a[30:31], v34 offset:23104\n" \
"v_mfma_f32_16x16x32_bf16 v[120:123], a[192:195], a[128:131], v[120:123]\n" \
"buffer_load_dwordx4 a[224:227], v20, s[56:59], s95 offen offset:1024 sc1\n" \
"v_mfma_f32_16x16x32_bf16 v[124:127], a[192:195], a[144:147], v[124:127]\n" \
"v_cvt_pk_bf16_f32 v112, v112, v113\n" \
"v_cvt_pk_bf16_f32 v113, v114, v115\n" \
"s_waitcnt lgkmcnt(4)\n" \
"v_mfma_f32_16x16x32_bf16 v[120:123], a[196:199], a[132:135], v[120:123]\n" \
"v_cvt_pk_bf16_f32 v114, v116, v117\n" \
"v_cvt_pk_bf16_f32 v115, v118, v119\n" \
"buffer_store_dwordx4 v[112:115], v20, s[56:59], s67 offen offset:0 sc1\n" \
"s_add_u32 s67, s67, 0x3800\n" \
"v_mfma_f32_16x16x32_bf16 v[124:127], a[196:199], a[148:151], v[124:127]\n" \
"ds_read_b64_tr_b16 a[192:193], v10 offset:9280\n" \
"ds_read_b64_tr_b16 a[194:195], v10 offset:13888\n" \
"v_mfma_f32_16x16x32_bf16 v[120:123], a[200:203], a[136:139], v[120:123]\n" \
"ds_read_b64_tr_b16 a[196:197], v33 offset:36960\n" \
"ds_read_b64_tr_b16 a[198:199], v33 offset:41568\n" \
"v_mfma_f32_16x16x32_bf16 v[124:127], a[200:203], a[152:155], v[124:127]\n" \
"ds_read_b64_tr_b16 a[200:201], v33 offset:46176\n" \
"ds_read_b64_tr_b16 a[202:203], v33 offset:50784\n" \
"v_mfma_f32_16x16x32_bf16 v[120:123], a[204:207], a[140:143], v[120:123]\n" \
"buffer_load_dwordx4 a[240:243], v32, s[56:59], s95 offen offset:1024 sc1\n" \
"v_mfma_f32_16x16x32_bf16 v[124:127], a[204:207], a[156:159], v[124:127]\n" \
"ds_read_b64_tr_b16 a[204:205], v33 offset:55392\n" \
"ds_read_b64_tr_b16 a[206:207], v33 offset:60000\n" \
"v_mfma_f32_16x16x32_bf16 v[120:123], a[208:211], a[32:35], v[120:123]\n" \
"v_mfma_f32_16x16x32_bf16 v[124:127], a[208:211], a[48:51], v[124:127]\n" \
"ds_read_b64_tr_b16 a[208:209], v10 offset:96\n" \
"ds_read_b64_tr_b16 a[210:211], v10 offset:4704\n" \
"v_mfma_f32_16x16x32_bf16 v[120:123], a[212:215], a[36:39], v[120:123]\n" \
"v_mfma_f32_16x16x32_bf16 v[124:127], a[212:215], a[52:55], v[124:127]\n" \
"ds_read_b64_tr_b16 a[212:213], v34 offset:96\n" \
"ds_read_b64_tr_b16 a[214:215], v34 offset:4704\n" \
"v_mfma_f32_16x16x32_bf16 v[120:123], a[216:219], a[40:43], v[120:123]\n" \
"buffer_load_dwordx4 a[228:231], v20, s[56:59], s95 offen offset:2048 sc1\n" \
"v_mfma_f32_16x16x32_bf16 v[124:127], a[216:219], a[56:59], v[124:127]\n" \
"ds_read_b64_tr_b16 a[216:217], v34 offset:9312\n" \
"ds_read_b64_tr_b16 a[218:219], v34 offset:13920\n" \
"v_mfma_f32_16x16x32_bf16 v[120:123], a[0:3], a[44:47], v[120:123]\n" \
"v_mfma_f32_16x16x32_bf16 v[124:127], a[0:3], a[60:63], v[124:127]\n" \
"ds_read_b64_tr_b16 a[0:1], v34 offset:18528\n" \
"ds_read_b64_tr_b16 a[2:3], v34 offset:23136\n" \
"v_mfma_f32_16x16x32_bf16 v[128:131], a[4:7], a[128:131], v[128:131]\n" \
"v_mfma_f32_16x16x32_bf16 v[132:135], a[4:7], a[144:147], v[132:135]\n" \
"ds_read_b64_tr_b16 a[4:5], v10 offset:9312\n" \
"ds_read_b64_tr_b16 a[6:7], v10 offset:13920\n" \
"v_mfma_f32_16x16x32_bf16 v[128:131], a[8:11], a[132:135], v[128:131]\n" \
"v_cvt_pk_bf16_f32 v120, v120, v121\n" \
"v_cvt_pk_bf16_f32 v121, v122, v123\n" \
"v_cvt_pk_bf16_f32 v122, v124, v125\n" \
"v_cvt_pk_bf16_f32 v123, v126, v127\n" \
"buffer_store_dwordx4 v[120:123], v20, s[56:59], s68 offen offset:0 sc1\n" \
"s_add_u32 s68, s68, 0x3000\n" \
"v_mfma_f32_16x16x32_bf16 v[132:135], a[8:11], a[148:151], v[132:135]\n" \
"ds_read_b64_tr_b16 a[8:9], v33 offset:36992\n" \
"ds_read_b64_tr_b16 a[10:11], v33 offset:41600\n" \
"v_mfma_f32_16x16x32_bf16 v[128:131], a[12:15], a[136:139], v[128:131]\n" \
"v_mfma_f32_16x16x32_bf16 v[132:135], a[12:15], a[152:155], v[132:135]\n" \
"ds_read_b64_tr_b16 a[12:13], v33 offset:46208\n" \
"ds_read_b64_tr_b16 a[14:15], v33 offset:50816\n" \
"v_mfma_f32_16x16x32_bf16 v[128:131], a[16:19], a[140:143], v[128:131]\n" \
"buffer_load_dwordx4 a[244:247], v32, s[56:59], s95 offen offset:2048 sc1\n" \
"v_mfma_f32_16x16x32_bf16 v[132:135], a[16:19], a[156:159], v[132:135]\n" \
"ds_read_b64_tr_b16 a[16:17], v33 offset:55424\n" \
"ds_read_b64_tr_b16 a[18:19], v33 offset:60032\n" \
"v_mfma_f32_16x16x32_bf16 v[128:131], a[20:23], a[32:35], v[128:131]\n" \
"v_mfma_f32_16x16x32_bf16 v[132:135], a[20:23], a[48:51], v[132:135]\n" \
"ds_read_b64_tr_b16 a[20:21], v10 offset:128\n" \
"s_waitcnt lgkmcnt(13)\n" \
"v_mfma_f32_16x16x32_bf16 v[128:131], a[24:27], a[36:39], v[128:131]\n" \
"ds_read_b64_tr_b16 a[22:23], v10 offset:4736\n" \
"v_mfma_f32_16x16x32_bf16 v[132:135], a[24:27], a[52:55], v[132:135]\n" \
"buffer_load_dwordx4 a[232:235], v20, s[56:59], s95 offen offset:3072 sc1\n" \
"v_mfma_f32_16x16x32_bf16 v[128:131], a[28:31], a[40:43], v[128:131]\n" \
"ds_read_b64_tr_b16 a[24:25], v34 offset:128\n" \
"ds_read_b64_tr_b16 a[26:27], v34 offset:4736\n" \
"v_mfma_f32_16x16x32_bf16 v[132:135], a[28:31], a[56:59], v[132:135]\n" \
"ds_read_b64_tr_b16 a[28:29], v34 offset:9344\n" \
"ds_read_b64_tr_b16 a[30:31], v34 offset:13952\n" \
"v_mfma_f32_16x16x32_bf16 v[128:131], a[192:195], a[44:47], v[128:131]\n" \
"v_mfma_f32_16x16x32_bf16 v[132:135], a[192:195], a[60:63], v[132:135]\n" \
"ds_read_b64_tr_b16 a[192:193], v34 offset:18560\n" \
"ds_read_b64_tr_b16 a[194:195], v34 offset:23168\n" \
"v_mfma_f32_16x16x32_bf16 v[136:139], a[196:199], a[128:131], v[136:139]\n" \
"buffer_load_dwordx4 a[248:251], v32, s[56:59], s95 offen offset:3072 sc1\n" \
"v_mfma_f32_16x16x32_bf16 v[140:143], a[196:199], a[144:147], v[140:143]\n" \
"ds_read_b64_tr_b16 a[196:197], v10 offset:9344\n" \
"ds_read_b64_tr_b16 a[198:199], v10 offset:13952\n" \
"v_cvt_pk_bf16_f32 v128, v128, v129\n" \
"v_cvt_pk_bf16_f32 v129, v130, v131\n" \
"v_cvt_pk_bf16_f32 v130, v132, v133\n" \
"v_cvt_pk_bf16_f32 v131, v134, v135\n" \
"buffer_store_dwordx4 v[128:131], v20, s[56:59], s69 offen offset:0 sc1\n" \
"s_add_u32 s69, s69, 0x2800\n" \
"v_mfma_f32_16x16x32_bf16 v[136:139], a[200:203], a[132:135], v[136:139]\n" \
"v_readlane_b32 s95, v11, 46\n" \
"v_mfma_f32_16x16x32_bf16 v[140:143], a[200:203], a[148:151], v[140:143]\n" \
"ds_read_b64_tr_b16 a[200:201], v33 offset:37024\n" \
"ds_read_b64_tr_b16 a[202:203], v33 offset:41632\n" \
"v_mfma_f32_16x16x32_bf16 v[136:139], a[204:207], a[136:139], v[136:139]\n" \
"v_mfma_f32_16x16x32_bf16 v[140:143], a[204:207], a[152:155], v[140:143]\n" \
"ds_read_b64_tr_b16 a[204:205], v33 offset:46240\n" \
"ds_read_b64_tr_b16 a[206:207], v33 offset:50848\n" \
"v_mfma_f32_16x16x32_bf16 v[136:139], a[208:211], a[140:143], v[136:139]\n" \
"buffer_load_dwordx4 v[64:67], v20, s[56:59], s97 offen offset:0 sc1\n" \
"v_mfma_f32_16x16x32_bf16 v[140:143], a[208:211], a[156:159], v[140:143]\n" \
"ds_read_b64_tr_b16 a[208:209], v33 offset:55456\n" \
"ds_read_b64_tr_b16 a[210:211], v33 offset:60064\n" \
"v_mfma_f32_16x16x32_bf16 v[136:139], a[212:215], a[32:35], v[136:139]\n" \
"v_mfma_f32_16x16x32_bf16 v[140:143], a[212:215], a[48:51], v[140:143]\n" \
"ds_read_b64_tr_b16 a[212:213], v10 offset:160\n" \
"s_waitcnt lgkmcnt(12)\n" \
"v_mfma_f32_16x16x32_bf16 v[136:139], a[216:219], a[36:39], v[136:139]\n" \
"ds_read_b64_tr_b16 a[214:215], v10 offset:4768\n" \
"v_mfma_f32_16x16x32_bf16 v[140:143], a[216:219], a[52:55], v[140:143]\n" \
"ds_read_b64_tr_b16 a[216:217], v34 offset:160\n" \
"ds_read_b64_tr_b16 a[218:219], v34 offset:4768\n" \
"v_mfma_f32_16x16x32_bf16 v[136:139], a[0:3], a[40:43], v[136:139]\n" \
"buffer_load_dwordx4 v[68:71], v20, s[56:59], s97 offen offset:1024 sc1\n" \
"v_mfma_f32_16x16x32_bf16 v[140:143], a[0:3], a[56:59], v[140:143]\n" \
"ds_read_b64_tr_b16 a[0:1], v34 offset:9376\n" \
"ds_read_b64_tr_b16 a[2:3], v34 offset:13984\n" \
"v_mfma_f32_16x16x32_bf16 v[136:139], a[4:7], a[44:47], v[136:139]\n" \
"v_mfma_f32_16x16x32_bf16 v[140:143], a[4:7], a[60:63], v[140:143]\n" \
"ds_read_b64_tr_b16 a[4:5], v34 offset:18592\n" \
"ds_read_b64_tr_b16 a[6:7], v34 offset:23200\n" \
"v_mfma_f32_16x16x32_bf16 v[144:147], a[8:11], a[128:131], v[144:147]\n" \
"v_mfma_f32_16x16x32_bf16 v[148:151], a[8:11], a[144:147], v[148:151]\n" \
"ds_read_b64_tr_b16 a[8:9], v10 offset:9376\n" \
"ds_read_b64_tr_b16 a[10:11], v10 offset:13984\n" \
"v_mfma_f32_16x16x32_bf16 v[144:147], a[12:15], a[132:135], v[144:147]\n" \
"v_cvt_pk_bf16_f32 v136, v136, v137\n" \
"v_cvt_pk_bf16_f32 v137, v138, v139\n" \
"v_cvt_pk_bf16_f32 v138, v140, v141\n" \
"v_cvt_pk_bf16_f32 v139, v142, v143\n" \
"buffer_store_dwordx4 v[136:139], v20, s[56:59], s70 offen offset:0 sc1\n" \
"s_add_u32 s70, s70, 0x2000\n" \
"v_mfma_f32_16x16x32_bf16 v[148:151], a[12:15], a[148:151], v[148:151]\n" \
"ds_read_b64_tr_b16 a[12:13], v33 offset:37056\n" \
"ds_read_b64_tr_b16 a[14:15], v33 offset:41664\n" \
"v_mfma_f32_16x16x32_bf16 v[144:147], a[16:19], a[136:139], v[144:147]\n" \
"v_mfma_f32_16x16x32_bf16 v[148:151], a[16:19], a[152:155], v[148:151]\n" \
"ds_read_b64_tr_b16 a[16:17], v33 offset:46272\n" \
"ds_read_b64_tr_b16 a[18:19], v33 offset:50880\n" \
"v_mfma_f32_16x16x32_bf16 v[144:147], a[20:23], a[140:143], v[144:147]\n" \
"buffer_load_dwordx4 v[72:75], v32, s[56:59], s97 offen offset:0 sc1\n" \
"v_mfma_f32_16x16x32_bf16 v[148:151], a[20:23], a[156:159], v[148:151]\n" \
"ds_read_b64_tr_b16 a[20:21], v33 offset:55488\n" \
"ds_read_b64_tr_b16 a[22:23], v33 offset:60096\n" \
"v_mfma_f32_16x16x32_bf16 v[144:147], a[24:27], a[32:35], v[144:147]\n" \
"v_mfma_f32_16x16x32_bf16 v[148:151], a[24:27], a[48:51], v[148:151]\n" \
"ds_read_b64_tr_b16 a[24:25], v10 offset:192\n" \
"s_waitcnt lgkmcnt(13)\n" \
"v_mfma_f32_16x16x32_bf16 v[144:147], a[28:31], a[36:39], v[144:147]\n" \
"ds_read_b64_tr_b16 a[26:27], v10 offset:4800\n" \
"v_mfma_f32_16x16x32_bf16 v[148:151], a[28:31], a[52:55], v[148:151]\n" \
"ds_read_b64_tr_b16 a[28:29], v34 offset:192\n" \
"ds_read_b64_tr_b16 a[30:31], v34 offset:4800\n" \
"v_mfma_f32_16x16x32_bf16 v[144:147], a[192:195], a[40:43], v[144:147]\n" \
"buffer_load_dwordx4 v[76:79], v32, s[56:59], s97 offen offset:1024 sc1\n" \
"v_mfma_f32_16x16x32_bf16 v[148:151], a[192:195], a[56:59], v[148:151]\n" \
"ds_read_b64_tr_b16 a[192:193], v34 offset:9408\n" \
"ds_read_b64_tr_b16 a[194:195], v34 offset:14016\n" \
"v_mfma_f32_16x16x32_bf16 v[144:147], a[196:199], a[44:47], v[144:147]\n" \
"v_readlane_b32 s97, v11, 48\n" \
"v_mfma_f32_16x16x32_bf16 v[148:151], a[196:199], a[60:63], v[148:151]\n" \
"ds_read_b64_tr_b16 a[196:197], v34 offset:18624\n" \
"ds_read_b64_tr_b16 a[198:199], v34 offset:23232\n" \
"v_mfma_f32_16x16x32_bf16 v[152:155], a[200:203], a[128:131], v[152:155]\n" \
"v_mfma_f32_16x16x32_bf16 v[156:159], a[200:203], a[144:147], v[156:159]\n" \
"ds_read_b64_tr_b16 a[200:201], v10 offset:9408\n" \
"ds_read_b64_tr_b16 a[202:203], v10 offset:14016\n" \
"v_cvt_pk_bf16_f32 v144, v144, v145\n" \
"v_cvt_pk_bf16_f32 v145, v146, v147\n" \
"v_cvt_pk_bf16_f32 v146, v148, v149\n" \
"v_cvt_pk_bf16_f32 v147, v150, v151\n" \
"buffer_store_dwordx4 v[144:147], v20, s[56:59], s71 offen offset:0 sc1\n" \
"s_add_u32 s71, s71, 0x1800\n" \
"v_mfma_f32_16x16x32_bf16 v[152:155], a[204:207], a[132:135], v[152:155]\n" \
"v_mfma_f32_16x16x32_bf16 v[156:159], a[204:207], a[148:151], v[156:159]\n" \
"ds_read_b64_tr_b16 a[204:205], v33 offset:256\n" \
"ds_read_b64_tr_b16 a[206:207], v33 offset:4864\n" \
"v_mfma_f32_16x16x32_bf16 v[152:155], a[208:211], a[136:139], v[152:155]\n" \
"v_mfma_f32_16x16x32_bf16 v[156:159], a[208:211], a[152:155], v[156:159]\n" \
"ds_read_b64_tr_b16 a[208:209], v33 offset:9472\n" \
"ds_read_b64_tr_b16 a[210:211], v33 offset:14080\n" \
"v_mfma_f32_16x16x32_bf16 v[152:155], a[212:215], a[140:143], v[152:155]\n" \
"buffer_load_dwordx4 v[80:83], v20, s[56:59], s64 offen offset:0 sc1\n" \
"v_mfma_f32_16x16x32_bf16 v[156:159], a[212:215], a[156:159], v[156:159]\n" \
"ds_read_b64_tr_b16 a[212:213], v33 offset:18688\n" \
"ds_read_b64_tr_b16 a[214:215], v33 offset:23296\n" \
"v_mfma_f32_16x16x32_bf16 v[152:155], a[216:219], a[32:35], v[152:155]\n" \
"v_mfma_f32_16x16x32_bf16 v[156:159], a[216:219], a[48:51], v[156:159]\n" \
"ds_read_b64_tr_b16 a[216:217], v33 offset:27904\n" \
"s_waitcnt lgkmcnt(12)\n" \
"v_mfma_f32_16x16x32_bf16 v[152:155], a[0:3], a[36:39], v[152:155]\n" \
"ds_read_b64_tr_b16 a[218:219], v33 offset:32512\n" \
"v_mfma_f32_16x16x32_bf16 v[156:159], a[0:3], a[52:55], v[156:159]\n" \
"ds_read_b64_tr_b16 a[0:1], v33 offset:37120\n" \
"ds_read_b64_tr_b16 a[2:3], v33 offset:41728\n" \
"v_mfma_f32_16x16x32_bf16 v[152:155], a[4:7], a[40:43], v[152:155]\n" \
"buffer_load_dwordx4 v[84:87], v20, s[56:59], s64 offen offset:1024 sc1\n" \
"v_mfma_f32_16x16x32_bf16 v[156:159], a[4:7], a[56:59], v[156:159]\n" \
"ds_read_b64_tr_b16 a[4:5], v33 offset:46336\n" \
"ds_read_b64_tr_b16 a[6:7], v33 offset:50944\n" \
"v_mfma_f32_16x16x32_bf16 v[152:155], a[8:11], a[44:47], v[152:155]\n" \
"v_mfma_f32_16x16x32_bf16 v[156:159], a[8:11], a[60:63], v[156:159]\n" \
"ds_read_b64_tr_b16 a[8:9], v33 offset:55552\n" \
"ds_read_b64_tr_b16 a[10:11], v33 offset:60160\n" \
"v_mfma_f32_16x16x32_bf16 v[160:163], a[12:15], a[128:131], v[160:163]\n" \
"v_mfma_f32_16x16x32_bf16 v[164:167], a[12:15], a[144:147], v[164:167]\n" \
"ds_read_b64_tr_b16 a[12:13], v10 offset:256\n" \
"ds_read_b64_tr_b16 a[14:15], v10 offset:4864\n" \
"v_mfma_f32_16x16x32_bf16 v[160:163], a[16:19], a[132:135], v[160:163]\n" \
"v_cvt_pk_bf16_f32 v152, v152, v153\n" \
"v_cvt_pk_bf16_f32 v153, v154, v155\n" \
"v_cvt_pk_bf16_f32 v154, v156, v157\n" \
"v_cvt_pk_bf16_f32 v155, v158, v159\n" \
"buffer_store_dwordx4 v[152:155], v20, s[56:59], s72 offen offset:0 sc1\n" \
"s_add_u32 s72, s72, 0x1000\n" \
"v_mfma_f32_16x16x32_bf16 v[164:167], a[16:19], a[148:151], v[164:167]\n" \
"ds_read_b64_tr_b16 a[16:17], v34 offset:256\n" \
"ds_read_b64_tr_b16 a[18:19], v34 offset:4864\n" \
"v_mfma_f32_16x16x32_bf16 v[160:163], a[20:23], a[136:139], v[160:163]\n" \
"v_mfma_f32_16x16x32_bf16 v[164:167], a[20:23], a[152:155], v[164:167]\n" \
"ds_read_b64_tr_b16 a[20:21], v34 offset:9472\n" \
"ds_read_b64_tr_b16 a[22:23], v34 offset:14080\n" \
"v_mfma_f32_16x16x32_bf16 v[160:163], a[24:27], a[140:143], v[160:163]\n" \
"buffer_load_dwordx4 v[88:91], v32, s[56:59], s64 offen offset:0 sc1\n" \
"v_mfma_f32_16x16x32_bf16 v[164:167], a[24:27], a[156:159], v[164:167]\n" \
"ds_read_b64_tr_b16 a[24:25], v34 offset:18688\n" \
"ds_read_b64_tr_b16 a[26:27], v34 offset:23296\n" \
"v_mfma_f32_16x16x32_bf16 v[160:163], a[28:31], a[32:35], v[160:163]\n" \
"v_mfma_f32_16x16x32_bf16 v[164:167], a[28:31], a[48:51], v[164:167]\n" \
"ds_read_b64_tr_b16 a[28:29], v10 offset:9472\n" \
"s_waitcnt vmcnt(17)\n" \
"s_mov_b64 exec, 1\n" \
"buffer_store_dword v39, off, s[56:59], s67 offset:0 sc1\n" \
"s_mov_b64 exec, -1\n" \
"s_waitcnt lgkmcnt(11)\n" \
"v_mfma_f32_16x16x32_bf16 v[160:163], a[192:195], a[36:39], v[160:163]\n" \
"ds_read_b64_tr_b16 a[30:31], v10 offset:14080\n" \
"v_mfma_f32_16x16x32_bf16 v[164:167], a[192:195], a[52:55], v[164:167]\n" \
"ds_read_b64_tr_b16 a[192:193], v33 offset:0\n" \
"ds_read_b64_tr_b16 a[194:195], v33 offset:4608\n" \
"v_mfma_f32_16x16x32_bf16 v[160:163], a[196:199], a[40:43], v[160:163]\n" \
"v_mfma_f32_16x16x32_bf16 v[164:167], a[196:199], a[56:59], v[164:167]\n" \
"ds_read_b64_tr_b16 a[196:197], v33 offset:9216\n" \
"ds_read_b64_tr_b16 a[198:199], v33 offset:13824\n" \
"v_mfma_f32_16x16x32_bf16 v[160:163], a[200:203], a[44:47], v[160:163]\n" \
"buffer_load_dwordx4 v[92:95], v32, s[56:59], s64 offen offset:1024 sc1\n" \
"v_mfma_f32_16x16x32_bf16 v[164:167], a[200:203], a[60:63], v[164:167]\n" \
"ds_read_b64_tr_b16 a[200:201], v33 offset:18432\n" \
"ds_read_b64_tr_b16 a[202:203], v33 offset:23040\n" \
"v_mfma_f32_16x16x32_bf16 v[248:251], a[204:207], a[96:99], v[248:251]\n" \
"v_readlane_b32 s64, v11, 49\n" \
"v_mfma_f32_16x16x32_bf16 v[252:255], a[204:207], a[112:115], v[252:255]\n" \
"ds_read_b64_tr_b16 a[204:205], v33 offset:27648\n" \
"v_cvt_pk_bf16_f32 v160, v160, v161\n" \
"v_cvt_pk_bf16_f32 v161, v162, v163\n" \
"v_cvt_pk_bf16_f32 v162, v164, v165\n" \
"v_cvt_pk_bf16_f32 v163, v166, v167\n" \
"buffer_store_dwordx4 v[160:163], v20, s[56:59], s73 offen offset:0 sc1\n" \
"s_add_u32 s73, s73, 0x800\n" \
"v_mfma_f32_16x16x32_bf16 v[248:251], a[208:211], a[100:103], v[248:251]\n" \
"ds_read_b64_tr_b16 a[206:207], v33 offset:32256\n" \
"v_mfma_f32_16x16x32_bf16 v[252:255], a[208:211], a[116:119], v[252:255]\n" \
"ds_read_b64_tr_b16 a[208:209], v33 offset:36864\n" \
"ds_read_b64_tr_b16 a[210:211], v33 offset:41472\n" \
"v_mfma_f32_16x16x32_bf16 v[248:251], a[212:215], a[104:107], v[248:251]\n" \
"v_mfma_f32_16x16x32_bf16 v[252:255], a[212:215], a[120:123], v[252:255]\n" \
"ds_read_b64_tr_b16 a[212:213], v33 offset:46080\n" \
"ds_read_b64_tr_b16 a[214:215], v33 offset:50688\n" \
"v_mfma_f32_16x16x32_bf16 v[248:251], a[216:219], a[108:111], v[248:251]\n" \
"buffer_load_dwordx4 v[96:99], v20, s[56:59], s65 offen offset:0 sc1\n" \
"v_mfma_f32_16x16x32_bf16 v[252:255], a[216:219], a[124:127], v[252:255]\n" \
"ds_read_b64_tr_b16 a[216:217], v33 offset:55296\n" \
"ds_read_b64_tr_b16 a[218:219], v33 offset:59904\n" \
"s_waitcnt vmcnt(18)\n" \
"s_mov_b64 exec, 1\n" \
"buffer_store_dword v39, off, s[56:59], s68 offset:4 sc1\n" \
"s_mov_b64 exec, -1\n" \
"v_mfma_f32_16x16x32_bf16 v[176:179], a[0:3], a[128:131], v[176:179]\n" \
"v_mfma_f32_16x16x32_bf16 v[180:183], a[0:3], a[144:147], v[180:183]\n" \
"v_mfma_f32_16x16x32_bf16 v[248:251], a[0:3], a[160:163], v[248:251]\n" \
"v_mfma_f32_16x16x32_bf16 v[252:255], a[0:3], a[176:179], v[252:255]\n" \
"ds_read_b64_tr_b16 a[0:1], v10 offset:0\n" \
"ds_read_b64_tr_b16 a[2:3], v10 offset:4608\n" \
"v_mfma_f32_16x16x32_bf16 v[176:179], a[4:7], a[132:135], v[176:179]\n" \
"buffer_load_dwordx4 v[104:107], v32, s[56:59], s65 offen offset:0 sc1\n" \
"v_mfma_f32_16x16x32_bf16 v[180:183], a[4:7], a[148:151], v[180:183]\n" \
"v_mfma_f32_16x16x32_bf16 v[248:251], a[4:7], a[164:167], v[248:251]\n" \
"v_mfma_f32_16x16x32_bf16 v[252:255], a[4:7], a[180:183], v[252:255]\n" \
"ds_read_b64_tr_b16 a[4:5], v34 offset:0\n" \
"s_waitcnt lgkmcnt(9)\n" \
"v_mfma_f32_16x16x32_bf16 v[176:179], a[8:11], a[136:139], v[176:179]\n" \
"ds_read_b64_tr_b16 a[6:7], v34 offset:4608\n" \
"v_mfma_f32_16x16x32_bf16 v[180:183], a[8:11], a[152:155], v[180:183]\n" \
"buffer_load_dwordx4 v[100:103], v20, s[56:59], s65 offen offset:1024 sc1\n" \
"v_mfma_f32_16x16x32_bf16 v[248:251], a[8:11], a[168:171], v[248:251]\n" \
"v_mfma_f32_16x16x32_bf16 v[252:255], a[8:11], a[184:187], v[252:255]\n" \
"ds_read_b64_tr_b16 a[8:9], v34 offset:9216\n" \
"ds_read_b64_tr_b16 a[10:11], v34 offset:13824\n" \
"v_mfma_f32_16x16x32_bf16 v[176:179], a[12:15], a[140:143], v[176:179]\n" \
"v_mfma_f32_16x16x32_bf16 v[180:183], a[12:15], a[156:159], v[180:183]\n" \
"v_mfma_f32_16x16x32_bf16 v[248:251], a[12:15], a[172:175], v[248:251]\n" \
"buffer_load_dwordx4 v[108:111], v32, s[56:59], s65 offen offset:1024 sc1\n" \
"v_mfma_f32_16x16x32_bf16 v[252:255], a[12:15], a[188:191], v[252:255]\n" \
"ds_read_b64_tr_b16 a[12:13], v34 offset:18432\n" \
"ds_read_b64_tr_b16 a[14:15], v34 offset:23040\n" \
"s_waitcnt vmcnt(18)\n" \
"s_mov_b64 exec, 1\n" \
"buffer_store_dword v39, off, s[56:59], s69 offset:8 sc1\n" \
"s_mov_b64 exec, -1\n" \
"v_mfma_f32_16x16x32_bf16 v[176:179], a[16:19], a[32:35], v[176:179]\n" \
"v_mfma_f32_16x16x32_bf16 v[180:183], a[16:19], a[48:51], v[180:183]\n" \
"v_mfma_f32_16x16x32_bf16 v[248:251], a[16:19], a[220:223], v[248:251]\n" \
"v_mfma_f32_16x16x32_bf16 v[252:255], a[16:19], a[236:239], v[252:255]\n" \
"ds_read_b64_tr_b16 a[16:17], v10 offset:9216\n" \
"ds_read_b64_tr_b16 a[18:19], v10 offset:13824\n" \
".fill (L_ab + 28672 - .) / 4, 4, 0xBF800000\n" \
"L_ap7:\n" \
"s_cbranch_execz L_ap0\n" \
"v_mfma_f32_16x16x32_bf16 v[176:179], a[20:23], a[36:39], v[176:179]\n" \
"v_mfma_f32_16x16x32_bf16 v[180:183], a[20:23], a[52:55], v[180:183]\n" \
"v_mfma_f32_16x16x32_bf16 v[248:251], a[20:23], a[224:227], v[248:251]\n" \
"v_mfma_f32_16x16x32_bf16 v[252:255], a[20:23], a[240:243], v[252:255]\n" \
"ds_read_b64_tr_b16 a[20:21], v33 offset:32\n" \
"ds_read_b64_tr_b16 a[22:23], v33 offset:4640\n" \
"v_mfma_f32_16x16x32_bf16 v[176:179], a[24:27], a[40:43], v[176:179]\n" \
"v_mfma_f32_16x16x32_bf16 v[180:183], a[24:27], a[56:59], v[180:183]\n" \
"v_mfma_f32_16x16x32_bf16 v[248:251], a[24:27], a[228:231], v[248:251]\n" \
"v_mfma_f32_16x16x32_bf16 v[252:255], a[24:27], a[244:247], v[252:255]\n" \
"ds_read_b64_tr_b16 a[24:25], v33 offset:9248\n" \
"ds_read_b64_tr_b16 a[26:27], v33 offset:13856\n" \
"v_mfma_f32_16x16x32_bf16 v[176:179], a[28:31], a[44:47], v[176:179]\n" \
"v_mfma_f32_16x16x32_bf16 v[180:183], a[28:31], a[60:63], v[180:183]\n" \
"v_mfma_f32_16x16x32_bf16 v[248:251], a[28:31], a[232:235], v[248:251]\n" \
"v_mfma_f32_16x16x32_bf16 v[252:255], a[28:31], a[248:251], v[252:255]\n" \
"ds_read_b64_tr_b16 a[28:29], v33 offset:18464\n" \
"ds_read_b64_tr_b16 a[30:31], v33 offset:23072\n" \
"v_mfma_f32_16x16x32_bf16 v[184:187], a[192:195], a[96:99], v[184:187]\n" \
"s_waitcnt vmcnt(16)\n" \
"s_mov_b64 exec, 1\n" \
"buffer_store_dword v39, off, s[56:59], s70 offset:12 sc1\n" \
"s_mov_b64 exec, -1\n" \
"v_cvt_pk_bf16_f32 v176, v176, v177\n" \
"v_cvt_pk_bf16_f32 v248, v248, v249\n" \
"v_cvt_pk_bf16_f32 v177, v178, v179\n" \
"v_cvt_pk_bf16_f32 v249, v250, v251\n" \
"v_cvt_pk_bf16_f32 v178, v180, v181\n" \
"v_cvt_pk_bf16_f32 v179, v182, v183\n" \
"v_cvt_pk_bf16_f32 v250, v252, v253\n" \
"v_cvt_pk_bf16_f32 v251, v254, v255\n" \
"buffer_store_dwordx4 v[176:179], v20, s[56:59], s92 offen offset:0 sc1\n" \
"v_mfma_f32_16x16x32_bf16 v[188:191], a[192:195], a[112:115], v[188:191]\n" \
"ds_read_b64_tr_b16 a[192:193], v33 offset:27680\n" \
"ds_read_b64_tr_b16 a[194:195], v33 offset:32288\n" \
"v_mfma_f32_16x16x32_bf16 v[184:187], a[196:199], a[100:103], v[184:187]\n" \
"v_mfma_f32_16x16x32_bf16 v[188:191], a[196:199], a[116:119], v[188:191]\n" \
"buffer_store_dwordx4 v[248:251], v20, s[56:59], s92 offen offset:1024 sc1\n" \
"v_mfma_f32_16x16x32_bf16 v[184:187], a[200:203], a[104:107], v[184:187]\n" \
"ds_read_b64_tr_b16 a[196:197], v33 offset:36896\n" \
"ds_read_b64_tr_b16 a[198:199], v33 offset:41504\n" \
"v_mfma_f32_16x16x32_bf16 v[188:191], a[200:203], a[120:123], v[188:191]\n" \
"ds_read_b64_tr_b16 a[200:201], v33 offset:46112\n" \
"ds_read_b64_tr_b16 a[202:203], v33 offset:50720\n" \
"v_mfma_f32_16x16x32_bf16 v[184:187], a[204:207], a[108:111], v[184:187]\n" \
"v_readlane_b32 s92, v11, 51\n" \
"v_mfma_f32_16x16x32_bf16 v[188:191], a[204:207], a[124:127], v[188:191]\n" \
"ds_read_b64_tr_b16 a[204:205], v33 offset:55328\n" \
"s_waitcnt lgkmcnt(9)\n" \
"v_mfma_f32_16x16x32_bf16 v[184:187], a[208:211], a[160:163], v[184:187]\n" \
"ds_read_b64_tr_b16 a[206:207], v33 offset:59936\n" \
"v_mfma_f32_16x16x32_bf16 v[188:191], a[208:211], a[176:179], v[188:191]\n" \
"ds_read_b64_tr_b16 a[208:209], v10 offset:32\n" \
"ds_read_b64_tr_b16 a[210:211], v10 offset:4640\n" \
"v_mfma_f32_16x16x32_bf16 v[184:187], a[212:215], a[164:167], v[184:187]\n" \
"v_mfma_f32_16x16x32_bf16 v[188:191], a[212:215], a[180:183], v[188:191]\n" \
"ds_read_b64_tr_b16 a[212:213], v34 offset:32\n" \
"ds_read_b64_tr_b16 a[214:215], v34 offset:4640\n" \
"v_mfma_f32_16x16x32_bf16 v[184:187], a[216:219], a[168:171], v[184:187]\n" \
"v_mfma_f32_16x16x32_bf16 v[188:191], a[216:219], a[184:187], v[188:191]\n" \
"ds_read_b64_tr_b16 a[216:217], v34 offset:9248\n" \
"ds_read_b64_tr_b16 a[218:219], v34 offset:13856\n" \
"s_waitcnt vmcnt(16)\n" \
"s_mov_b64 exec, 1\n" \
"buffer_store_dword v39, off, s[56:59], s71 offset:16 sc1\n" \
"s_mov_b64 exec, -1\n" \
"v_mfma_f32_16x16x32_bf16 v[184:187], a[0:3], a[172:175], v[184:187]\n" \
"v_mfma_f32_16x16x32_bf16 v[188:191], a[0:3], a[188:191], v[188:191]\n" \
"ds_read_b64_tr_b16 a[0:1], v34 offset:18464\n" \
"ds_read_b64_tr_b16 a[2:3], v34 offset:23072\n" \
"v_mfma_f32_16x16x32_bf16 v[184:187], a[4:7], a[220:223], v[184:187]\n" \
"v_mfma_f32_16x16x32_bf16 v[188:191], a[4:7], a[236:239], v[188:191]\n" \
"ds_read_b64_tr_b16 a[4:5], v10 offset:9248\n" \
"ds_read_b64_tr_b16 a[6:7], v10 offset:13856\n" \
"v_mfma_f32_16x16x32_bf16 v[184:187], a[8:11], a[224:227], v[184:187]\n" \
"v_mfma_f32_16x16x32_bf16 v[188:191], a[8:11], a[240:243], v[188:191]\n" \
"ds_read_b64_tr_b16 a[8:9], v33 offset:64\n" \
"ds_read_b64_tr_b16 a[10:11], v33 offset:4672\n" \
"v_mfma_f32_16x16x32_bf16 v[184:187], a[12:15], a[228:231], v[184:187]\n" \
"v_mfma_f32_16x16x32_bf16 v[188:191], a[12:15], a[244:247], v[188:191]\n" \
"ds_read_b64_tr_b16 a[12:13], v33 offset:9280\n" \
"ds_read_b64_tr_b16 a[14:15], v33 offset:13888\n" \
"v_mfma_f32_16x16x32_bf16 v[184:187], a[16:19], a[232:235], v[184:187]\n" \
"v_mfma_f32_16x16x32_bf16 v[188:191], a[16:19], a[248:251], v[188:191]\n" \
"ds_read_b64_tr_b16 a[16:17], v33 offset:18496\n" \
"ds_read_b64_tr_b16 a[18:19], v33 offset:23104\n" \
"v_mfma_f32_16x16x32_bf16 v[192:195], a[20:23], a[96:99], v[192:195]\n" \
"v_mfma_f32_16x16x32_bf16 v[196:199], a[20:23], a[112:115], v[196:199]\n" \
"ds_read_b64_tr_b16 a[20:21], v33 offset:27712\n" \
"ds_read_b64_tr_b16 a[22:23], v33 offset:32320\n" \
"v_mfma_f32_16x16x32_bf16 v[192:195], a[24:27], a[100:103], v[192:195]\n" \
"v_cvt_pk_bf16_f32 v184, v184, v185\n" \
"v_cvt_pk_bf16_f32 v185, v186, v187\n" \
"v_cvt_pk_bf16_f32 v186, v188, v189\n" \
"v_cvt_pk_bf16_f32 v187, v190, v191\n" \
"buffer_store_dwordx4 v[184:187], v20, s[56:59], s94 offen offset:1024 sc1\n" \
"s_add_u32 s94, s94, 0x3800\n" \
"v_mfma_f32_16x16x32_bf16 v[196:199], a[24:27], a[116:119], v[196:199]\n" \
"ds_read_b64_tr_b16 a[24:25], v33 offset:36928\n" \
"s_waitcnt lgkmcnt(13)\n" \
"v_mfma_f32_16x16x32_bf16 v[192:195], a[28:31], a[104:107], v[192:195]\n" \
"ds_read_b64_tr_b16 a[26:27], v33 offset:41536\n" \
"v_mfma_f32_16x16x32_bf16 v[196:199], a[28:31], a[120:123], v[196:199]\n" \
"ds_read_b64_tr_b16 a[28:29], v33 offset:46144\n" \
"ds_read_b64_tr_b16 a[30:31], v33 offset:50752\n" \
"s_waitcnt vmcnt(15)\n" \
"s_mov_b64 exec, 1\n" \
"buffer_store_dword v39, off, s[56:59], s72 offset:20 sc1\n" \
"s_mov_b64 exec, -1\n" \
"v_mfma_f32_16x16x32_bf16 v[192:195], a[192:195], a[108:111], v[192:195]\n" \
"v_readlane_b32 s72, v11, 41\n" \
"v_mfma_f32_16x16x32_bf16 v[196:199], a[192:195], a[124:127], v[196:199]\n" \
"ds_read_b64_tr_b16 a[192:193], v33 offset:55360\n" \
"ds_read_b64_tr_b16 a[194:195], v33 offset:59968\n" \
"v_mfma_f32_16x16x32_bf16 v[192:195], a[196:199], a[160:163], v[192:195]\n" \
"v_mfma_f32_16x16x32_bf16 v[196:199], a[196:199], a[176:179], v[196:199]\n" \
"ds_read_b64_tr_b16 a[196:197], v10 offset:64\n" \
"ds_read_b64_tr_b16 a[198:199], v10 offset:4672\n" \
"v_mfma_f32_16x16x32_bf16 v[192:195], a[200:203], a[164:167], v[192:195]\n" \
"v_mfma_f32_16x16x32_bf16 v[196:199], a[200:203], a[180:183], v[196:199]\n" \
"ds_read_b64_tr_b16 a[200:201], v34 offset:64\n" \
"ds_read_b64_tr_b16 a[202:203], v34 offset:4672\n" \
"v_mfma_f32_16x16x32_bf16 v[192:195], a[204:207], a[168:171], v[192:195]\n" \
"v_mfma_f32_16x16x32_bf16 v[196:199], a[204:207], a[184:187], v[196:199]\n" \
"ds_read_b64_tr_b16 a[204:205], v34 offset:9280\n" \
"ds_read_b64_tr_b16 a[206:207], v34 offset:13888\n" \
"v_mfma_f32_16x16x32_bf16 v[192:195], a[208:211], a[172:175], v[192:195]\n" \
"v_mfma_f32_16x16x32_bf16 v[196:199], a[208:211], a[188:191], v[196:199]\n" \
"ds_read_b64_tr_b16 a[208:209], v34 offset:18496\n" \
"ds_read_b64_tr_b16 a[210:211], v34 offset:23104\n" \
"v_mfma_f32_16x16x32_bf16 v[192:195], a[212:215], a[220:223], v[192:195]\n" \
"v_mfma_f32_16x16x32_bf16 v[196:199], a[212:215], a[236:239], v[196:199]\n" \
"ds_read_b64_tr_b16 a[212:213], v10 offset:9280\n" \
"ds_read_b64_tr_b16 a[214:215], v10 offset:13888\n" \
"v_mfma_f32_16x16x32_bf16 v[192:195], a[216:219], a[224:227], v[192:195]\n" \
"v_mfma_f32_16x16x32_bf16 v[196:199], a[216:219], a[240:243], v[196:199]\n" \
"ds_read_b64_tr_b16 a[216:217], v33 offset:96\n" \
"s_waitcnt lgkmcnt(13)\n" \
"v_mfma_f32_16x16x32_bf16 v[192:195], a[0:3], a[228:231], v[192:195]\n" \
"ds_read_b64_tr_b16 a[218:219], v33 offset:4704\n" \
"v_mfma_f32_16x16x32_bf16 v[196:199], a[0:3], a[244:247], v[196:199]\n" \
"ds_read_b64_tr_b16 a[0:1], v33 offset:9312\n" \
"ds_read_b64_tr_b16 a[2:3], v33 offset:13920\n" \
"s_waitcnt vmcnt(12)\n" \
"s_mov_b64 exec, 1\n" \
"buffer_store_dword v39, off, s[56:59], s73 offset:24 sc1\n" \
"s_mov_b64 exec, -1\n" \
"v_mfma_f32_16x16x32_bf16 v[192:195], a[4:7], a[232:235], v[192:195]\n" \
"v_readlane_b32 s73, v11, 50\n" \
"v_mfma_f32_16x16x32_bf16 v[196:199], a[4:7], a[248:251], v[196:199]\n" \
"ds_read_b64_tr_b16 a[4:5], v33 offset:18528\n" \
"ds_read_b64_tr_b16 a[6:7], v33 offset:23136\n" \
"v_mfma_f32_16x16x32_bf16 v[200:203], a[8:11], a[96:99], v[200:203]\n" \
"v_mfma_f32_16x16x32_bf16 v[204:207], a[8:11], a[112:115], v[204:207]\n" \
"ds_read_b64_tr_b16 a[8:9], v33 offset:27744\n" \
"ds_read_b64_tr_b16 a[10:11], v33 offset:32352\n" \
"v_cvt_pk_bf16_f32 v192, v192, v193\n" \
"v_cvt_pk_bf16_f32 v193, v194, v195\n" \
"v_cvt_pk_bf16_f32 v194, v196, v197\n" \
"v_cvt_pk_bf16_f32 v195, v198, v199\n" \
"buffer_store_dwordx4 v[192:195], v20, s[56:59], s95 offen offset:1024 sc1\n" \
"s_add_u32 s95, s95, 0x3000\n" \
"v_mfma_f32_16x16x32_bf16 v[200:203], a[12:15], a[100:103], v[200:203]\n" \
"v_mfma_f32_16x16x32_bf16 v[204:207], a[12:15], a[116:119], v[204:207]\n" \
"ds_read_b64_tr_b16 a[12:13], v33 offset:36960\n" \
"ds_read_b64_tr_b16 a[14:15], v33 offset:41568\n" \
"v_mfma_f32_16x16x32_bf16 v[200:203], a[16:19], a[104:107], v[200:203]\n" \
"v_mfma_f32_16x16x32_bf16 v[204:207], a[16:19], a[120:123], v[204:207]\n" \
"ds_read_b64_tr_b16 a[16:17], v33 offset:46176\n" \
"ds_read_b64_tr_b16 a[18:19], v33 offset:50784\n" \
"v_mfma_f32_16x16x32_bf16 v[200:203], a[20:23], a[108:111], v[200:203]\n" \
"v_mfma_f32_16x16x32_bf16 v[204:207], a[20:23], a[124:127], v[204:207]\n" \
"ds_read_b64_tr_b16 a[20:21], v33 offset:55392\n" \
"ds_read_b64_tr_b16 a[22:23], v33 offset:60000\n" \
"v_mfma_f32_16x16x32_bf16 v[200:203], a[24:27], a[160:163], v[200:203]\n" \
"v_mfma_f32_16x16x32_bf16 v[204:207], a[24:27], a[176:179], v[204:207]\n" \
"ds_read_b64_tr_b16 a[24:25], v10 offset:96\n" \
"ds_read_b64_tr_b16 a[26:27], v10 offset:4704\n" \
"v_mfma_f32_16x16x32_bf16 v[200:203], a[28:31], a[164:167], v[200:203]\n" \
"v_mfma_f32_16x16x32_bf16 v[204:207], a[28:31], a[180:183], v[204:207]\n" \
"ds_read_b64_tr_b16 a[28:29], v34 offset:96\n" \
"s_waitcnt lgkmcnt(12)\n" \
"v_mfma_f32_16x16x32_bf16 v[200:203], a[192:195], a[168:171], v[200:203]\n" \
"ds_read_b64_tr_b16 a[30:31], v34 offset:4704\n" \
"v_mfma_f32_16x16x32_bf16 v[204:207], a[192:195], a[184:187], v[204:207]\n" \
"ds_read_b64_tr_b16 a[192:193], v34 offset:9312\n" \
"ds_read_b64_tr_b16 a[194:195], v34 offset:13920\n" \
"v_mfma_f32_16x16x32_bf16 v[200:203], a[196:199], a[172:175], v[200:203]\n" \
"v_mfma_f32_16x16x32_bf16 v[204:207], a[196:199], a[188:191], v[204:207]\n" \
"ds_read_b64_tr_b16 a[196:197], v34 offset:18528\n" \
"ds_read_b64_tr_b16 a[198:199], v34 offset:23136\n" \
"v_mfma_f32_16x16x32_bf16 v[200:203], a[200:203], a[220:223], v[200:203]\n" \
"v_mfma_f32_16x16x32_bf16 v[204:207], a[200:203], a[236:239], v[204:207]\n" \
"ds_read_b64_tr_b16 a[200:201], v10 offset:9312\n" \
"ds_read_b64_tr_b16 a[202:203], v10 offset:13920\n" \
"v_mfma_f32_16x16x32_bf16 v[200:203], a[204:207], a[224:227], v[200:203]\n" \
"v_mfma_f32_16x16x32_bf16 v[204:207], a[204:207], a[240:243], v[204:207]\n" \
"ds_read_b64_tr_b16 a[204:205], v33 offset:128\n" \
"ds_read_b64_tr_b16 a[206:207], v33 offset:4736\n" \
"v_mfma_f32_16x16x32_bf16 v[200:203], a[208:211], a[228:231], v[200:203]\n" \
"v_mfma_f32_16x16x32_bf16 v[204:207], a[208:211], a[244:247], v[204:207]\n" \
"ds_read_b64_tr_b16 a[208:209], v33 offset:9344\n" \
"ds_read_b64_tr_b16 a[210:211], v33 offset:13952\n" \
"v_mfma_f32_16x16x32_bf16 v[200:203], a[212:215], a[232:235], v[200:203]\n" \
"v_mfma_f32_16x16x32_bf16 v[204:207], a[212:215], a[248:251], v[204:207]\n" \
"ds_read_b64_tr_b16 a[212:213], v33 offset:18560\n" \
"ds_read_b64_tr_b16 a[214:215], v33 offset:23168\n" \
"v_mfma_f32_16x16x32_bf16 v[208:211], a[216:219], a[96:99], v[208:211]\n" \
"v_mfma_f32_16x16x32_bf16 v[212:215], a[216:219], a[112:115], v[212:215]\n" \
"ds_read_b64_tr_b16 a[216:217], v33 offset:27776\n" \
"ds_read_b64_tr_b16 a[218:219], v33 offset:32384\n" \
"v_mfma_f32_16x16x32_bf16 v[208:211], a[0:3], a[100:103], v[208:211]\n" \
"v_cvt_pk_bf16_f32 v200, v200, v201\n" \
"v_cvt_pk_bf16_f32 v201, v202, v203\n" \
"v_cvt_pk_bf16_f32 v202, v204, v205\n" \
"v_cvt_pk_bf16_f32 v203, v206, v207\n" \
"buffer_store_dwordx4 v[200:203], v20, s[56:59], s96 offen offset:1024 sc1\n" \
"s_add_u32 s96, s96, 0x2800\n" \
"v_mfma_f32_16x16x32_bf16 v[212:215], a[0:3], a[116:119], v[212:215]\n" \
"ds_read_b64_tr_b16 a[0:1], v33 offset:36992\n" \
"s_waitcnt lgkmcnt(13)\n" \
"v_mfma_f32_16x16x32_bf16 v[208:211], a[4:7], a[104:107], v[208:211]\n" \
"ds_read_b64_tr_b16 a[2:3], v33 offset:41600\n" \
"v_mfma_f32_16x16x32_bf16 v[212:215], a[4:7], a[120:123], v[212:215]\n" \
"ds_read_b64_tr_b16 a[4:5], v33 offset:46208\n" \
"ds_read_b64_tr_b16 a[6:7], v33 offset:50816\n" \
"v_mfma_f32_16x16x32_bf16 v[208:211], a[8:11], a[108:111], v[208:211]\n" \
"v_mfma_f32_16x16x32_bf16 v[212:215], a[8:11], a[124:127], v[212:215]\n" \
"ds_read_b64_tr_b16 a[8:9], v33 offset:55424\n" \
"ds_read_b64_tr_b16 a[10:11], v33 offset:60032\n" \
"v_mfma_f32_16x16x32_bf16 v[208:211], a[12:15], a[160:163], v[208:211]\n" \
"v_mfma_f32_16x16x32_bf16 v[212:215], a[12:15], a[176:179], v[212:215]\n" \
"ds_read_b64_tr_b16 a[12:13], v10 offset:128\n" \
"ds_read_b64_tr_b16 a[14:15], v10 offset:4736\n" \
"v_mfma_f32_16x16x32_bf16 v[208:211], a[16:19], a[164:167], v[208:211]\n" \
"v_mfma_f32_16x16x32_bf16 v[212:215], a[16:19], a[180:183], v[212:215]\n" \
"ds_read_b64_tr_b16 a[16:17], v34 offset:128\n" \
"ds_read_b64_tr_b16 a[18:19], v34 offset:4736\n" \
"v_mfma_f32_16x16x32_bf16 v[208:211], a[20:23], a[168:171], v[208:211]\n" \
"v_mfma_f32_16x16x32_bf16 v[212:215], a[20:23], a[184:187], v[212:215]\n" \
"ds_read_b64_tr_b16 a[20:21], v34 offset:9344\n" \
"ds_read_b64_tr_b16 a[22:23], v34 offset:13952\n" \
"v_mfma_f32_16x16x32_bf16 v[208:211], a[24:27], a[172:175], v[208:211]\n" \
"v_mfma_f32_16x16x32_bf16 v[212:215], a[24:27], a[188:191], v[212:215]\n" \
"ds_read_b64_tr_b16 a[24:25], v34 offset:18560\n" \
"ds_read_b64_tr_b16 a[26:27], v34 offset:23168\n" \
"v_mfma_f32_16x16x32_bf16 v[208:211], a[28:31], a[220:223], v[208:211]\n" \
"v_mfma_f32_16x16x32_bf16 v[212:215], a[28:31], a[236:239], v[212:215]\n" \
"ds_read_b64_tr_b16 a[28:29], v10 offset:9344\n" \
"ds_read_b64_tr_b16 a[30:31], v10 offset:13952\n" \
"s_waitcnt vmcnt(6)\n" \
"s_mov_b64 exec, 3\n" \
"buffer_store_dword v39, v41, s[56:59], s93 offen sc1\n" \
"s_mov_b64 exec, -1\n" \
"v_mfma_f32_16x16x32_bf16 v[208:211], a[192:195], a[224:227], v[208:211]\n" \
"v_mfma_f32_16x16x32_bf16 v[212:215], a[192:195], a[240:243], v[212:215]\n" \
"ds_read_b64_tr_b16 a[192:193], v33 offset:160\n" \
"s_waitcnt lgkmcnt(13)\n" \
"v_mfma_f32_16x16x32_bf16 v[208:211], a[196:199], a[228:231], v[208:211]\n" \
"ds_read_b64_tr_b16 a[194:195], v33 offset:4768\n" \
"v_mfma_f32_16x16x32_bf16 v[212:215], a[196:199], a[244:247], v[212:215]\n" \
"ds_read_b64_tr_b16 a[196:197], v33 offset:9376\n" \
"ds_read_b64_tr_b16 a[198:199], v33 offset:13984\n" \
"v_mfma_f32_16x16x32_bf16 v[208:211], a[200:203], a[232:235], v[208:211]\n" \
"v_mfma_f32_16x16x32_bf16 v[212:215], a[200:203], a[248:251], v[212:215]\n" \
"ds_read_b64_tr_b16 a[200:201], v33 offset:18592\n" \
"ds_read_b64_tr_b16 a[202:203], v33 offset:23200\n" \
"v_mfma_f32_16x16x32_bf16 v[216:219], a[204:207], a[96:99], v[216:219]\n" \
"v_mfma_f32_16x16x32_bf16 v[220:223], a[204:207], a[112:115], v[220:223]\n" \
"ds_read_b64_tr_b16 a[204:205], v33 offset:27808\n" \
"ds_read_b64_tr_b16 a[206:207], v33 offset:32416\n" \
"v_mfma_f32_16x16x32_bf16 v[216:219], a[208:211], a[100:103], v[216:219]\n" \
"v_cvt_pk_bf16_f32 v208, v208, v209\n" \
"v_cvt_pk_bf16_f32 v209, v210, v211\n" \
"v_cvt_pk_bf16_f32 v210, v212, v213\n" \
"v_cvt_pk_bf16_f32 v211, v214, v215\n" \
"buffer_store_dwordx4 v[208:211], v20, s[56:59], s97 offen offset:1024 sc1\n" \
"s_add_u32 s97, s97, 0x2000\n" \
"v_mfma_f32_16x16x32_bf16 v[220:223], a[208:211], a[116:119], v[220:223]\n" \
"ds_read_b64_tr_b16 a[208:209], v33 offset:37024\n" \
"ds_read_b64_tr_b16 a[210:211], v33 offset:41632\n" \
"v_mfma_f32_16x16x32_bf16 v[216:219], a[212:215], a[104:107], v[216:219]\n" \
"v_mfma_f32_16x16x32_bf16 v[220:223], a[212:215], a[120:123], v[220:223]\n" \
"ds_read_b64_tr_b16 a[212:213], v33 offset:46240\n" \
"ds_read_b64_tr_b16 a[214:215], v33 offset:50848\n" \
"v_mfma_f32_16x16x32_bf16 v[216:219], a[216:219], a[108:111], v[216:219]\n" \
"v_mfma_f32_16x16x32_bf16 v[220:223], a[216:219], a[124:127], v[220:223]\n" \
"ds_read_b64_tr_b16 a[216:217], v33 offset:55456\n" \
"ds_read_b64_tr_b16 a[218:219], v33 offset:60064\n" \
"v_mfma_f32_16x16x32_bf16 v[216:219], a[0:3], a[160:163], v[216:219]\n" \
"v_mfma_f32_16x16x32_bf16 v[220:223], a[0:3], a[176:179], v[220:223]\n" \
"ds_read_b64_tr_b16 a[0:1], v10 offset:160\n" \
"ds_read_b64_tr_b16 a[2:3], v10 offset:4768\n" \
"v_mfma_f32_16x16x32_bf16 v[216:219], a[4:7], a[164:167], v[216:219]\n" \
"v_mfma_f32_16x16x32_bf16 v[220:223], a[4:7], a[180:183], v[220:223]\n" \
"ds_read_b64_tr_b16 a[4:5], v34 offset:160\n" \
"s_waitcnt lgkmcnt(13)\n" \
"v_mfma_f32_16x16x32_bf16 v[216:219], a[8:11], a[168:171], v[216:219]\n" \
"ds_read_b64_tr_b16 a[6:7], v34 offset:4768\n" \
"v_mfma_f32_16x16x32_bf16 v[220:223], a[8:11], a[184:187], v[220:223]\n" \
"ds_read_b64_tr_b16 a[8:9], v34 offset:9376\n" \
"ds_read_b64_tr_b16 a[10:11], v34 offset:13984\n" \
"v_mfma_f32_16x16x32_bf16 v[216:219], a[12:15], a[172:175], v[216:219]\n" \
"v_mfma_f32_16x16x32_bf16 v[220:223], a[12:15], a[188:191], v[220:223]\n" \
"ds_read_b64_tr_b16 a[12:13], v34 offset:18592\n" \
"ds_read_b64_tr_b16 a[14:15], v34 offset:23200\n" \
"v_mfma_f32_16x16x32_bf16 v[216:219], a[16:19], a[220:223], v[216:219]\n" \
"s_waitcnt vmcnt(6)\n" \
"s_mov_b64 exec, 1\n" \
"buffer_store_dword v39, off, s[56:59], s94 offset:32 sc1\n" \
"s_mov_b64 exec, -1\n" \
"v_mfma_f32_16x16x32_bf16 v[220:223], a[16:19], a[236:239], v[220:223]\n" \
"ds_read_b64_tr_b16 a[16:17], v10 offset:9376\n" \
"ds_read_b64_tr_b16 a[18:19], v10 offset:13984\n" \
"v_mfma_f32_16x16x32_bf16 v[216:219], a[20:23], a[224:227], v[216:219]\n" \
"v_mfma_f32_16x16x32_bf16 v[220:223], a[20:23], a[240:243], v[220:223]\n" \
"ds_read_b64_tr_b16 a[20:21], v33 offset:192\n" \
"ds_read_b64_tr_b16 a[22:23], v33 offset:4800\n" \
"v_mfma_f32_16x16x32_bf16 v[216:219], a[24:27], a[228:231], v[216:219]\n" \
"v_mfma_f32_16x16x32_bf16 v[220:223], a[24:27], a[244:247], v[220:223]\n" \
"ds_read_b64_tr_b16 a[24:25], v33 offset:9408\n" \
"ds_read_b64_tr_b16 a[26:27], v33 offset:14016\n" \
"v_mfma_f32_16x16x32_bf16 v[216:219], a[28:31], a[232:235], v[216:219]\n" \
"v_mfma_f32_16x16x32_bf16 v[220:223], a[28:31], a[248:251], v[220:223]\n" \
"buffer_load_dword v37, v40, s[56:59], s74 offen sc1\n" \
"v_mfma_f32_16x16x32_bf16 v[224:227], a[192:195], a[96:99], v[224:227]\n" \
"ds_read_b64_tr_b16 a[28:29], v33 offset:18624\n" \
"ds_read_b64_tr_b16 a[30:31], v33 offset:23232\n" \
"v_mfma_f32_16x16x32_bf16 v[228:231], a[192:195], a[112:115], v[228:231]\n" \
"ds_read_b64_tr_b16 a[192:193], v33 offset:27840\n" \
"ds_read_b64_tr_b16 a[194:195], v33 offset:32448\n" \
"v_cvt_pk_bf16_f32 v216, v216, v217\n" \
"v_cvt_pk_bf16_f32 v217, v218, v219\n" \
"v_cvt_pk_bf16_f32 v218, v220, v221\n" \
"v_cvt_pk_bf16_f32 v219, v222, v223\n" \
"buffer_store_dwordx4 v[216:219], v20, s[56:59], s64 offen offset:1024 sc1\n" \
"s_add_u32 s64, s64, 0x1800\n" \
"v_mfma_f32_16x16x32_bf16 v[224:227], a[196:199], a[100:103], v[224:227]\n" \
"v_mfma_f32_16x16x32_bf16 v[228:231], a[196:199], a[116:119], v[228:231]\n" \
"ds_read_b64_tr_b16 a[196:197], v33 offset:37056\n" \
"s_waitcnt lgkmcnt(11)\n" \
"v_mfma_f32_16x16x32_bf16 v[224:227], a[200:203], a[104:107], v[224:227]\n" \
"ds_read_b64_tr_b16 a[198:199], v33 offset:41664\n" \
"v_mfma_f32_16x16x32_bf16 v[228:231], a[200:203], a[120:123], v[228:231]\n" \
"ds_read_b64_tr_b16 a[200:201], v33 offset:46272\n" \
"ds_read_b64_tr_b16 a[202:203], v33 offset:50880\n" \
"v_mfma_f32_16x16x32_bf16 v[224:227], a[204:207], a[108:111], v[224:227]\n" \
"v_mfma_f32_16x16x32_bf16 v[228:231], a[204:207], a[124:127], v[228:231]\n" \
"ds_read_b64_tr_b16 a[204:205], v33 offset:55488\n" \
"ds_read_b64_tr_b16 a[206:207], v33 offset:60096\n" \
"v_mfma_f32_16x16x32_bf16 v[224:227], a[208:211], a[160:163], v[224:227]\n" \
"v_mfma_f32_16x16x32_bf16 v[228:231], a[208:211], a[176:179], v[228:231]\n" \
"ds_read_b64_tr_b16 a[208:209], v10 offset:192\n" \
"ds_read_b64_tr_b16 a[210:211], v10 offset:4800\n" \
"v_mfma_f32_16x16x32_bf16 v[224:227], a[212:215], a[164:167], v[224:227]\n" \
"v_mfma_f32_16x16x32_bf16 v[228:231], a[212:215], a[180:183], v[228:231]\n" \
"ds_read_b64_tr_b16 a[212:213], v34 offset:192\n" \
"ds_read_b64_tr_b16 a[214:215], v34 offset:4800\n" \
"v_mfma_f32_16x16x32_bf16 v[224:227], a[216:219], a[168:171], v[224:227]\n" \
"v_mfma_f32_16x16x32_bf16 v[228:231], a[216:219], a[184:187], v[228:231]\n" \
"ds_read_b64_tr_b16 a[216:217], v34 offset:9408\n" \
"ds_read_b64_tr_b16 a[218:219], v34 offset:14016\n" \
"v_mfma_f32_16x16x32_bf16 v[224:227], a[0:3], a[172:175], v[224:227]\n" \
"v_mfma_f32_16x16x32_bf16 v[228:231], a[0:3], a[188:191], v[228:231]\n" \
"ds_read_b64_tr_b16 a[0:1], v34 offset:18624\n" \
"ds_read_b64_tr_b16 a[2:3], v34 offset:23232\n" \
"v_mfma_f32_16x16x32_bf16 v[224:227], a[4:7], a[220:223], v[224:227]\n" \
"v_mfma_f32_16x16x32_bf16 v[228:231], a[4:7], a[236:239], v[228:231]\n" \
"ds_read_b64_tr_b16 a[4:5], v10 offset:9408\n" \
"ds_read_b64_tr_b16 a[6:7], v10 offset:14016\n" \
"s_waitcnt vmcnt(6)\n" \
"s_mov_b64 exec, 1\n" \
"buffer_store_dword v39, off, s[56:59], s95 offset:36 sc1\n" \
"s_mov_b64 exec, -1\n" \
"v_mfma_f32_16x16x32_bf16 v[224:227], a[8:11], a[224:227], v[224:227]\n" \
"v_mfma_f32_16x16x32_bf16 v[228:231], a[8:11], a[240:243], v[228:231]\n" \
"ds_read_b64_tr_b16 a[8:9], v33 offset:224\n" \
"ds_read_b64_tr_b16 a[10:11], v33 offset:4832\n" \
"v_mfma_f32_16x16x32_bf16 v[224:227], a[12:15], a[228:231], v[224:227]\n" \
"v_mfma_f32_16x16x32_bf16 v[228:231], a[12:15], a[244:247], v[228:231]\n" \
"ds_read_b64_tr_b16 a[12:13], v33 offset:9440\n" \
"s_waitcnt lgkmcnt(13)\n" \
"v_mfma_f32_16x16x32_bf16 v[224:227], a[16:19], a[232:235], v[224:227]\n" \
"ds_read_b64_tr_b16 a[14:15], v33 offset:14048\n" \
"v_mfma_f32_16x16x32_bf16 v[228:231], a[16:19], a[248:251], v[228:231]\n" \
"ds_read_b64_tr_b16 a[16:17], v33 offset:18656\n" \
"ds_read_b64_tr_b16 a[18:19], v33 offset:23264\n" \
"v_mfma_f32_16x16x32_bf16 v[232:235], a[20:23], a[96:99], v[232:235]\n" \
"v_mfma_f32_16x16x32_bf16 v[236:239], a[20:23], a[112:115], v[236:239]\n" \
"ds_read_b64_tr_b16 a[20:21], v33 offset:27872\n" \
"ds_read_b64_tr_b16 a[22:23], v33 offset:32480\n" \
"v_cvt_pk_bf16_f32 v224, v224, v225\n" \
"v_cvt_pk_bf16_f32 v225, v226, v227\n" \
"v_cvt_pk_bf16_f32 v226, v228, v229\n" \
"v_cvt_pk_bf16_f32 v227, v230, v231\n" \
"buffer_store_dwordx4 v[224:227], v20, s[56:59], s73 offen offset:1024 sc1\n" \
"v_mfma_f32_16x16x32_bf16 v[232:235], a[24:27], a[100:103], v[232:235]\n" \
"v_mfma_f32_16x16x32_bf16 v[236:239], a[24:27], a[116:119], v[236:239]\n" \
"ds_read_b64_tr_b16 a[24:25], v33 offset:37088\n" \
"ds_read_b64_tr_b16 a[26:27], v33 offset:41696\n" \
"v_mfma_f32_16x16x32_bf16 v[232:235], a[28:31], a[104:107], v[232:235]\n" \
"v_mfma_f32_16x16x32_bf16 v[236:239], a[28:31], a[120:123], v[236:239]\n" \
"ds_read_b64_tr_b16 a[28:29], v33 offset:46304\n" \
"ds_read_b64_tr_b16 a[30:31], v33 offset:50912\n" \
"v_mfma_f32_16x16x32_bf16 v[232:235], a[192:195], a[108:111], v[232:235]\n" \
"v_mfma_f32_16x16x32_bf16 v[236:239], a[192:195], a[124:127], v[236:239]\n" \
"ds_read_b64_tr_b16 a[192:193], v33 offset:55520\n" \
"ds_read_b64_tr_b16 a[194:195], v33 offset:60128\n" \
"v_mfma_f32_16x16x32_bf16 v[232:235], a[196:199], a[160:163], v[232:235]\n" \
"v_mfma_f32_16x16x32_bf16 v[236:239], a[196:199], a[176:179], v[236:239]\n" \
"ds_read_b64_tr_b16 a[196:197], v10 offset:224\n" \
"ds_read_b64_tr_b16 a[198:199], v10 offset:4832\n" \
"v_mfma_f32_16x16x32_bf16 v[232:235], a[200:203], a[164:167], v[232:235]\n" \
"v_mfma_f32_16x16x32_bf16 v[236:239], a[200:203], a[180:183], v[236:239]\n" \
"ds_read_b64_tr_b16 a[200:201], v34 offset:224\n" \
"ds_read_b64_tr_b16 a[202:203], v34 offset:4832\n" \
"v_mfma_f32_16x16x32_bf16 v[232:235], a[204:207], a[168:171], v[232:235]\n" \
"v_mfma_f32_16x16x32_bf16 v[236:239], a[204:207], a[184:187], v[236:239]\n" \
"ds_read_b64_tr_b16 a[204:205], v34 offset:9440\n" \
"s_waitcnt lgkmcnt(13)\n" \
"v_mfma_f32_16x16x32_bf16 v[232:235], a[208:211], a[172:175], v[232:235]\n" \
"ds_read_b64_tr_b16 a[206:207], v34 offset:14048\n" \
"v_mfma_f32_16x16x32_bf16 v[236:239], a[208:211], a[188:191], v[236:239]\n" \
"ds_read_b64_tr_b16 a[208:209], v34 offset:18656\n" \
"ds_read_b64_tr_b16 a[210:211], v34 offset:23264\n" \
"v_mfma_f32_16x16x32_bf16 v[232:235], a[212:215], a[220:223], v[232:235]\n" \
"v_mfma_f32_16x16x32_bf16 v[236:239], a[212:215], a[236:239], v[236:239]\n" \
"ds_read_b64_tr_b16 a[212:213], v10 offset:9440\n" \
"ds_read_b64_tr_b16 a[214:215], v10 offset:14048\n" \
"s_waitcnt vmcnt(7)\n" \
"s_mov_b64 exec, 1\n" \
"buffer_store_dword v39, off, s[56:59], s96 offset:40 sc1\n" \
"s_mov_b64 exec, -1\n" \
"v_mfma_f32_16x16x32_bf16 v[232:235], a[216:219], a[224:227], v[232:235]\n" \
"v_mfma_f32_16x16x32_bf16 v[236:239], a[216:219], a[240:243], v[236:239]\n" \
"ds_read_b64_tr_b16 a[216:217], v36 offset:0\n" \
"ds_read_b64_tr_b16 a[218:219], v36 offset:1024\n" \
"v_mfma_f32_16x16x32_bf16 v[232:235], a[0:3], a[228:231], v[232:235]\n" \
"v_mfma_f32_16x16x32_bf16 v[236:239], a[0:3], a[244:247], v[236:239]\n" \
"ds_read_b64_tr_b16 a[0:1], v36 offset:2048\n" \
"ds_read_b64_tr_b16 a[2:3], v36 offset:3072\n" \
"v_mfma_f32_16x16x32_bf16 v[232:235], a[4:7], a[232:235], v[232:235]\n" \
"v_mfma_f32_16x16x32_bf16 v[236:239], a[4:7], a[248:251], v[236:239]\n" \
"ds_read_b64_tr_b16 a[4:5], v36 offset:32\n" \
"ds_read_b64_tr_b16 a[6:7], v36 offset:1056\n" \
"v_mfma_f32_16x16x32_bf16 v[240:243], a[8:11], a[96:99], v[240:243]\n" \
"v_mfma_f32_16x16x32_bf16 v[244:247], a[8:11], a[112:115], v[244:247]\n" \
"ds_read_b64_tr_b16 a[8:9], v36 offset:2080\n" \
"ds_read_b64_tr_b16 a[10:11], v36 offset:3104\n" \
"v_mfma_f32_16x16x32_bf16 v[240:243], a[12:15], a[100:103], v[240:243]\n" \
"v_cvt_pk_bf16_f32 v232, v232, v233\n" \
"v_cvt_pk_bf16_f32 v233, v234, v235\n" \
"v_cvt_pk_bf16_f32 v234, v236, v237\n" \
"v_cvt_pk_bf16_f32 v235, v238, v239\n" \
"buffer_store_dwordx4 v[232:235], v20, s[56:59], s92 offen offset:1024 sc1\n" \
"s_waitcnt vmcnt(5)\n" \
"v_cmp_eq_u32_e64 s[98:99], s62, v37\n" \
"s_mov_b64 exec, 1\n" \
"buffer_store_dword v39, off, s[56:59], s97 offset:44 sc1\n" \
"s_mov_b64 exec, -1\n" \
"v_mfma_f32_16x16x32_bf16 v[244:247], a[12:15], a[116:119], v[244:247]\n" \
"s_or_b32 s75, 0x7f, s100\n" \
"s_andn2_b32 s75, s75, s98\n" \
"s_cbranch_scc1 L_sx0_%=\n" \
"L_rx0_%=:\n" \
"buffer_load_dword v37, v40, s[56:59], s74 offen sc1\n" \
"s_add_u32 s65, s63, 0x0\n" \
"s_add_u32 s66, s63, 0x800\n" \
"s_add_u32 s67, s63, 0x1000\n" \
"s_add_u32 s68, s63, 0x1800\n" \
"s_add_u32 s69, s63, 0x2000\n" \
"s_add_u32 s70, s63, 0x2800\n" \
"s_add_u32 s71, s63, 0x3000\n" \
"s_add_u32 s73, s73, 0x1000\n" \
"s_add_u32 s92, s92, 0x800\n" \
"v_mfma_f32_16x16x32_bf16 v[240:243], a[16:19], a[104:107], v[240:243]\n" \
"v_mfma_f32_16x16x32_bf16 v[244:247], a[16:19], a[120:123], v[244:247]\n" \
"buffer_load_dwordx4 v[116:119], v20, s[56:59], s65 offen offset:0 sc1\n" \
"s_waitcnt lgkmcnt(5)\n" \
"v_mfma_f32_16x16x32_bf16 v[240:243], a[20:23], a[108:111], v[240:243]\n" \
"v_mfma_f32_16x16x32_bf16 v[244:247], a[20:23], a[124:127], v[244:247]\n" \
"v_mfma_f32_16x16x32_bf16 v[168:171], a[24:27], a[128:131], v[168:171]\n" \
"v_mfma_f32_16x16x32_bf16 v[172:175], a[24:27], a[144:147], v[172:175]\n" \
"buffer_load_dwordx4 v[124:127], v20, s[56:59], s66 offen offset:0 sc1\n" \
"v_mfma_f32_16x16x32_bf16 v[240:243], a[24:27], a[160:163], v[240:243]\n" \
"v_readlane_b32 s66, v11, 53\n" \
"v_mfma_f32_16x16x32_bf16 v[244:247], a[24:27], a[176:179], v[244:247]\n" \
"v_mfma_f32_16x16x32_bf16 v[168:171], a[28:31], a[132:135], v[168:171]\n" \
"v_mfma_f32_16x16x32_bf16 v[172:175], a[28:31], a[148:151], v[172:175]\n" \
"buffer_load_dwordx4 v[132:135], v20, s[56:59], s67 offen offset:0 sc1\n" \
"v_mfma_f32_16x16x32_bf16 v[240:243], a[28:31], a[164:167], v[240:243]\n" \
"v_readlane_b32 s67, v11, 44\n" \
"v_mfma_f32_16x16x32_bf16 v[244:247], a[28:31], a[180:183], v[244:247]\n" \
"v_mfma_f32_16x16x32_bf16 v[168:171], a[192:195], a[136:139], v[168:171]\n" \
"v_mfma_f32_16x16x32_bf16 v[172:175], a[192:195], a[152:155], v[172:175]\n" \
"buffer_load_dwordx4 v[140:143], v20, s[56:59], s68 offen offset:0 sc1\n" \
"v_mfma_f32_16x16x32_bf16 v[240:243], a[192:195], a[168:171], v[240:243]\n" \
"v_readlane_b32 s68, v11, 42\n" \
"v_mfma_f32_16x16x32_bf16 v[244:247], a[192:195], a[184:187], v[244:247]\n" \
"v_mfma_f32_16x16x32_bf16 v[168:171], a[196:199], a[140:143], v[168:171]\n" \
"v_mfma_f32_16x16x32_bf16 v[172:175], a[196:199], a[156:159], v[172:175]\n" \
"buffer_load_dwordx4 v[148:151], v20, s[56:59], s69 offen offset:0 sc1\n" \
"v_mfma_f32_16x16x32_bf16 v[240:243], a[196:199], a[172:175], v[240:243]\n" \
"v_readlane_b32 s69, v11, 43\n" \
"v_mfma_f32_16x16x32_bf16 v[244:247], a[196:199], a[188:191], v[244:247]\n" \
"v_mfma_f32_16x16x32_bf16 v[168:171], a[200:203], a[32:35], v[168:171]\n" \
"v_mfma_f32_16x16x32_bf16 v[172:175], a[200:203], a[48:51], v[172:175]\n" \
"buffer_load_dwordx4 v[156:159], v20, s[56:59], s70 offen offset:0 sc1\n" \
"v_mfma_f32_16x16x32_bf16 v[240:243], a[200:203], a[220:223], v[240:243]\n" \
"v_mfma_f32_16x16x32_bf16 v[244:247], a[200:203], a[236:239], v[244:247]\n" \
"v_mfma_f32_16x16x32_bf16 v[168:171], a[204:207], a[36:39], v[168:171]\n" \
"v_mfma_f32_16x16x32_bf16 v[172:175], a[204:207], a[52:55], v[172:175]\n" \
"buffer_load_dwordx4 v[164:167], v20, s[56:59], s71 offen offset:0 sc1\n" \
"v_mfma_f32_16x16x32_bf16 v[240:243], a[204:207], a[224:227], v[240:243]\n" \
"v_mfma_f32_16x16x32_bf16 v[244:247], a[204:207], a[240:243], v[244:247]\n" \
"v_mfma_f32_16x16x32_bf16 v[168:171], a[208:211], a[40:43], v[168:171]\n" \
"v_mfma_f32_16x16x32_bf16 v[172:175], a[208:211], a[56:59], v[172:175]\n" \
"buffer_load_dwordx4 v[112:115], v20, s[56:59], s72 offen offset:0 sc1\n" \
"v_mfma_f32_16x16x32_bf16 v[240:243], a[208:211], a[228:231], v[240:243]\n" \
"v_mfma_f32_16x16x32_bf16 v[244:247], a[208:211], a[244:247], v[244:247]\n" \
"v_mfma_f32_16x16x32_bf16 v[168:171], a[212:215], a[44:47], v[168:171]\n" \
"v_mfma_f32_16x16x32_bf16 v[172:175], a[212:215], a[60:63], v[172:175]\n" \
"buffer_load_dwordx4 v[184:187], v20, s[56:59], s72 offen offset:2048 sc1\n" \
"s_waitcnt vmcnt(15)\n" \
"s_mov_b64 exec, 1\n" \
"buffer_store_dword v39, off, s[56:59], s64 offset:48 sc1\n" \
"s_mov_b64 exec, -1\n" \
"s_add_u32 s72, s72, 0x1000\n" \
"v_mfma_f32_16x16x32_bf16 v[240:243], a[212:215], a[232:235], v[240:243]\n" \
"v_mfma_f32_16x16x32_bf16 v[244:247], a[212:215], a[248:251], v[244:247]\n" \
"v_mfma_f32_16x16x32_bf16 v[168:171], a[216:219], v[64:67], v[168:171]\n" \
"v_mfma_f32_16x16x32_bf16 v[172:175], a[216:219], v[72:75], v[172:175]\n" \
"buffer_load_dwordx4 v[120:123], v20, s[56:59], s72 offen offset:0 sc1\n" \
"v_mfma_f32_16x16x32_bf16 v[240:243], a[216:219], v[80:83], v[240:243]\n" \
".fill (L_ab + 32768 - .) / 4, 4, 0xBF800000\n" \
"L_ap8:\n" \
"s_cbranch_execz L_ap0\n" \
"v_mfma_f32_16x16x32_bf16 v[244:247], a[216:219], v[88:91], v[244:247]\n" \
"s_waitcnt lgkmcnt(0)\n" \
"v_mfma_f32_16x16x32_bf16 v[168:171], a[0:3], v[68:71], v[168:171]\n" \
"v_mfma_f32_16x16x32_bf16 v[172:175], a[0:3], v[76:79], v[172:175]\n" \
"buffer_load_dwordx4 v[192:195], v20, s[56:59], s72 offen offset:2048 sc1\n" \
"s_add_u32 s72, s72, 0x1000\n" \
"v_mfma_f32_16x16x32_bf16 v[240:243], a[0:3], v[84:87], v[240:243]\n" \
"v_mfma_f32_16x16x32_bf16 v[244:247], a[0:3], v[92:95], v[244:247]\n" \
"v_mfma_f32_16x16x32_bf16 v[56:59], a[4:7], v[96:99], 0\n" \
"v_mfma_f32_16x16x32_bf16 v[60:63], a[4:7], v[104:107], 0\n" \
"buffer_load_dwordx4 v[128:131], v20, s[56:59], s72 offen offset:0 sc1\n" \
"v_mfma_f32_16x16x32_bf16 v[56:59], a[8:11], v[100:103], v[56:59]\n" \
"v_mfma_f32_16x16x32_bf16 v[60:63], a[8:11], v[108:111], v[60:63]\n" \
"buffer_load_dwordx4 v[200:203], v20, s[56:59], s72 offen offset:2048 sc1\n" \
"s_add_u32 s72, s72, 0x1000\n" \
"buffer_load_dwordx4 v[176:179], v20, s[56:59], s72 offen offset:0 sc1\n" \
"buffer_load_dwordx4 v[248:251], v20, s[56:59], s72 offen offset:2048 sc1\n" \
"s_waitcnt vmcnt(15)\n" \
"v_mfma_f32_16x16x32_bf16 v[168:171], v[50:53], v[116:119], v[168:171]\n" \
"s_mov_b64 exec, 1\n" \
"buffer_store_dword v39, off, s[56:59], s73 offset:52 sc1\n" \
"s_mov_b64 exec, -1\n" \
"s_mov_b64 exec, 1\n" \
"buffer_store_dword v39, off, s[56:59], s92 offset:56 sc1\n" \
"s_mov_b64 exec, -1\n" \
"v_cmp_eq_u32_e64 s[98:99], s62, v37\n" \
"v_mfma_f32_16x16x32_bf16 v[172:175], v[48:51], v[116:119], v[172:175]\n" \
"s_andn2_b32 s75, 0x100, s98\n" \
"s_cbranch_scc1 L_sx1_%=\n" \
"L_rx1_%=:\n" \
"s_add_u32 s93, s63, 0x0\n" \
"s_waitcnt vmcnt(16)\n" \
"v_mfma_f32_16x16x32_bf16 v[168:171], v[50:53], v[124:127], v[168:171]\n" \
"v_mfma_f32_16x16x32_bf16 v[172:175], v[48:51], v[124:127], v[172:175]\n" \
"buffer_load_dwordx4 v[188:191], v20, s[56:59], s93 offen offset:1024 sc1\n" \
"s_andn2_b32 s75, 0x200, s98\n" \
"s_cbranch_scc1 L_sx2_%=\n" \
"L_rx2_%=:\n" \
"s_add_u32 s94, s63, 0x800\n" \
"s_waitcnt vmcnt(16)\n" \
"v_mfma_f32_16x16x32_bf16 v[168:171], v[50:53], v[132:135], v[168:171]\n" \
"v_mfma_f32_16x16x32_bf16 v[172:175], v[48:51], v[132:135], v[172:175]\n" \
"buffer_load_dwordx4 v[196:199], v20, s[56:59], s94 offen offset:1024 sc1\n" \
"s_andn2_b32 s75, 0x400, s98\n" \
"s_cbranch_scc1 L_sx3_%=\n" \
"L_rx3_%=:\n" \
"s_add_u32 s95, s63, 0x1000\n" \
"s_waitcnt vmcnt(16)\n" \
"v_mfma_f32_16x16x32_bf16 v[168:171], v[50:53], v[140:143], v[168:171]\n" \
"v_mfma_f32_16x16x32_bf16 v[172:175], v[48:51], v[140:143], v[172:175]\n" \
"buffer_load_dwordx4 v[204:207], v20, s[56:59], s95 offen offset:1024 sc1\n" \
"s_andn2_b32 s75, 0x800, s98\n" \
"s_cbranch_scc1 L_sx4_%=\n" \
"L_rx4_%=:\n" \
"s_add_u32 s96, s63, 0x1800\n" \
"s_waitcnt vmcnt(16)\n" \
"v_mfma_f32_16x16x32_bf16 v[168:171], v[50:53], v[148:151], v[168:171]\n" \
"v_mfma_f32_16x16x32_bf16 v[172:175], v[48:51], v[148:151], v[172:175]\n" \
"buffer_load_dwordx4 v[212:215], v20, s[56:59], s96 offen offset:1024 sc1\n" \
"s_andn2_b32 s75, 0x1000, s98\n" \
"s_cbranch_scc1 L_sx5_%=\n" \
"L_rx5_%=:\n" \
"s_add_u32 s97, s63, 0x2000\n" \
"s_waitcnt vmcnt(16)\n" \
"v_mfma_f32_16x16x32_bf16 v[168:171], v[50:53], v[156:159], v[168:171]\n" \
"v_mfma_f32_16x16x32_bf16 v[172:175], v[48:51], v[156:159], v[172:175]\n" \
"buffer_load_dwordx4 v[220:223], v20, s[56:59], s97 offen offset:1024 sc1\n" \
"s_andn2_b32 s75, 0x2000, s98\n" \
"s_cbranch_scc1 L_sx6_%=\n" \
"L_rx6_%=:\n" \
"s_add_u32 s64, s63, 0x2800\n" \
"s_waitcnt vmcnt(16)\n" \
"v_mfma_f32_16x16x32_bf16 v[168:171], v[50:53], v[164:167], v[168:171]\n" \
"v_mfma_f32_16x16x32_bf16 v[172:175], v[48:51], v[164:167], v[172:175]\n" \
"buffer_load_dwordx4 v[228:231], v20, s[56:59], s64 offen offset:1024 sc1\n" \
"s_andn2_b32 s75, 0x4000, s98\n" \
"s_cbranch_scc1 L_sx7_%=\n" \
"L_rx7_%=:\n" \
"s_add_u32 s65, s63, 0x3000\n" \
"s_waitcnt vmcnt(16)\n" \
"v_mfma_f32_16x16x32_bf16 v[56:59], v[50:53], v[112:115], v[56:59]\n" \
"v_mfma_f32_16x16x32_bf16 v[60:63], v[48:51], v[112:115], v[60:63]\n" \
"buffer_load_dwordx4 v[236:239], v20, s[56:59], s65 offen offset:1024 sc1\n" \
"v_cvt_pk_bf16_f32 v168, v168, v169\n" \
"v_cvt_pk_bf16_f32 v172, v172, v173\n" \
"v_cvt_pk_bf16_f32 v169, v170, v171\n" \
"v_cvt_pk_bf16_f32 v173, v174, v175\n" \
"buffer_store_dwordx2 v[168:169], v42, s[88:91], s68 offen sc1\n" \
"buffer_store_dwordx2 v[172:173], v43, s[88:91], s68 offen sc1\n" \
"s_waitcnt vmcnt(18)\n" \
"v_mfma_f32_16x16x32_bf16 v[56:59], v[50:53], v[184:187], v[56:59]\n" \
"v_mfma_f32_16x16x32_bf16 v[60:63], v[48:51], v[184:187], v[60:63]\n" \
"s_waitcnt vmcnt(16)\n" \
"v_mfma_f32_16x16x32_bf16 v[56:59], v[50:53], v[120:123], v[56:59]\n" \
"v_mfma_f32_16x16x32_bf16 v[60:63], v[48:51], v[120:123], v[60:63]\n" \
"s_waitcnt vmcnt(15)\n" \
"v_mfma_f32_16x16x32_bf16 v[56:59], v[50:53], v[192:195], v[56:59]\n" \
"v_mfma_f32_16x16x32_bf16 v[60:63], v[48:51], v[192:195], v[60:63]\n" \
"s_waitcnt vmcnt(14)\n" \
"v_mfma_f32_16x16x32_bf16 v[56:59], v[50:53], v[128:131], v[56:59]\n" \
"v_mfma_f32_16x16x32_bf16 v[60:63], v[48:51], v[128:131], v[60:63]\n" \
"s_waitcnt vmcnt(13)\n" \
"v_mfma_f32_16x16x32_bf16 v[56:59], v[50:53], v[200:203], v[56:59]\n" \
"v_mfma_f32_16x16x32_bf16 v[60:63], v[48:51], v[200:203], v[60:63]\n" \
"s_waitcnt vmcnt(12)\n" \
"v_mfma_f32_16x16x32_bf16 v[56:59], v[50:53], v[176:179], v[56:59]\n" \
"v_mfma_f32_16x16x32_bf16 v[60:63], v[48:51], v[176:179], v[60:63]\n" \
"s_waitcnt vmcnt(11)\n" \
"v_mfma_f32_16x16x32_bf16 v[56:59], v[50:53], v[248:251], v[56:59]\n" \
"v_mfma_f32_16x16x32_bf16 v[60:63], v[48:51], v[248:251], v[60:63]\n" \
"s_mov_b64 exec, 1\n" \
"buffer_store_dword v46, off, s[56:59], s66 offset:0 sc1\n" \
"s_mov_b64 exec, -1\n" \
"s_waitcnt vmcnt(9)\n" \
"v_mfma_f32_16x16x32_bf16 v[240:243], v[50:53], v[188:191], v[240:243]\n" \
"v_mfma_f32_16x16x32_bf16 v[244:247], v[48:51], v[188:191], v[244:247]\n" \
"s_waitcnt vmcnt(8)\n" \
"v_mfma_f32_16x16x32_bf16 v[240:243], v[50:53], v[196:199], v[240:243]\n" \
"v_cvt_pk_bf16_f32 v56, v56, v57\n" \
"v_cvt_pk_bf16_f32 v60, v60, v61\n" \
"v_mfma_f32_16x16x32_bf16 v[244:247], v[48:51], v[196:199], v[244:247]\n" \
"v_cvt_pk_bf16_f32 v57, v58, v59\n" \
"v_cvt_pk_bf16_f32 v61, v62, v63\n" \
"buffer_store_dwordx2 v[56:57], v42, s[88:91], s67 offen sc1\n" \
"buffer_store_dwordx2 v[60:61], v43, s[88:91], s67 offen sc1\n" \
"s_waitcnt vmcnt(9)\n" \
"v_mfma_f32_16x16x32_bf16 v[240:243], v[50:53], v[204:207], v[240:243]\n" \
"v_mfma_f32_16x16x32_bf16 v[244:247], v[48:51], v[204:207], v[244:247]\n" \
"s_waitcnt vmcnt(8)\n" \
"v_mfma_f32_16x16x32_bf16 v[240:243], v[50:53], v[212:215], v[240:243]\n" \
"v_mfma_f32_16x16x32_bf16 v[244:247], v[48:51], v[212:215], v[244:247]\n" \
"s_waitcnt vmcnt(7)\n" \
"v_mfma_f32_16x16x32_bf16 v[240:243], v[50:53], v[220:223], v[240:243]\n" \
"v_mfma_f32_16x16x32_bf16 v[244:247], v[48:51], v[220:223], v[244:247]\n" \
"s_waitcnt vmcnt(6)\n" \
"v_mfma_f32_16x16x32_bf16 v[240:243], v[50:53], v[228:231], v[240:243]\n" \
"v_mfma_f32_16x16x32_bf16 v[244:247], v[48:51], v[228:231], v[244:247]\n" \
"s_waitcnt vmcnt(5)\n" \
"v_mfma_f32_16x16x32_bf16 v[240:243], v[50:53], v[236:239], v[240:243]\n" \
"v_mfma_f32_16x16x32_bf16 v[244:247], v[48:51], v[236:239], v[244:247]\n" \
"s_nop 6\n" \
"v_cvt_pk_bf16_f32 v240, v240, v241\n" \
"v_cvt_pk_bf16_f32 v244, v244, v245\n" \
"v_cvt_pk_bf16_f32 v241, v242, v243\n" \
"v_cvt_pk_bf16_f32 v245, v246, v247\n" \
"buffer_store_dwordx2 v[240:241], v42, s[88:91], s69 offen sc1\n" \
"buffer_store_dwordx2 v[244:245], v43, s[88:91], s69 offen sc1\n"
#define ATT_VCLOB "v21","v22","v23","v24","v25","v26","v27","v28","v29","v32","v33","v34","v35","v36","v37","v38","v39","v40","v41","v42","v43","v44","v45","v46","v47","v48","v49","v50","v51","v52","v53","v54","v55","v56","v57","v58","v59","v60","v61","v62","v63","v64","v65","v66","v67","v68","v69","v70","v71","v72","v73","v74","v75","v76","v77","v78","v79","v80","v81","v82","v83","v84","v85","v86","v87","v88","v89","v90","v91","v92","v93","v94","v95","v96","v97","v98","v99","v100","v101","v102","v103","v104","v105","v106","v107","v108","v109","v110","v111","v112","v113","v114","v115","v116","v117","v118","v119","v120","v121","v122","v123","v124","v125","v126","v127","v128","v129","v130","v131","v132","v133","v134","v135","v136","v137","v138","v139","v140","v141","v142","v143","v144","v145","v146","v147","v148","v149","v150","v151","v152","v153","v154","v155","v156","v157","v158","v159","v160","v161","v162","v163","v164","v165","v166","v167","v168","v169","v170","v171","v172","v173","v174","v175","v176","v177","v178","v179","v180","v181","v182","v183","v184","v185","v186","v187","v188","v189","v190","v191","v192","v193","v194","v195","v196","v197","v198","v199","v200","v201","v202","v203","v204","v205","v206","v207","v208","v209","v210","v211","v212","v213","v214","v215","v216","v217","v218","v219","v220","v221","v222","v223","v224","v225","v226","v227","v228","v229","v230","v231","v232","v233","v234","v235","v236","v237","v238","v239","v240","v241","v242","v243","v244","v245","v246","v247","v248","v249","v250","v251","v252","v253","v254","v255"
#define ATT_SCLOB "s63","s64","s65","s66","s67","s68","s69","s70","s71","s72","s73","s74","s75","s76","s77","s78","s79","s80","s81","s82","s83","s84","s85","s86","s87","s88","s89","s90","s91","s92","s93","s95","s96","s97","s98","s99","s100","s101"
#define ATT_WTCH 0
enum { T_fo, T_fj, T_fa, T_fas, T_fb, T_fbs, T_f8, T_mk8, T_wt, T_wol, T_woh, T_wbl, T_wbh, T_outl, T_outh, T_r1l, T_r1h, T_r1n, T_r1s, T_m0, T_m1, T_mw, T_m3, T_m4, T_ml, T_w0, T_w1, T_w2, T_w3, T_A1, T_A2, T_A3, T_B1, T_B2, T_B3, T_Ao, T_Bo, T_Al, T_Bl, T_l8, T_xr, T_x8r, T_oA, T_oB, T_o8, T_x0, T_x1, T_x2, T_x3, T_x4, T_x5, T_x6, T_x8, T_ep };

__device__ __forceinline__ void out_consts(u32x4 (&vp)[3], const u16* wo, const u16* wob, const u16* out, const unsigned b, const unsigned w, const unsigned lane) {
    const unsigned sp = b & 7u, nt = (b >> 3) & 3u, j = b >> 5, head = 4u * sp + nt, OOR = 0x40000000u;
    unsigned vw[9];
#pragma unroll
    for (unsigned i = 0; i < 9u; ++i) {
        const unsigned q = 64u * i + lane, row = __umul24(q, 3641u) >> 16, c = q - __umul24(row, 18u);
        vw[i] = __umul24(row, DM * 2) + (c < 16u ? (c * 16u + sp * 32u + 32u) & 255u : c * 16u) - (i & 3u) * 1024u;
    }
    const unsigned row = 16u * w + (lane >> 2), r9 = __umul24(16u * (row >> 5) + (row & 12u) + 2u * ((row >> 4) & 1u) + ((row >> 1) & 1u), 18u) + 16u + (row & 1u);
    unsigned vwl = __umul24(r9, DM * 2) + ((lane & 2u) ? 256u : sp * 32u) + (lane & 1u) * 16u, vrd = __umul24(lane >> 2, 288u) + (lane & 3u) * 8u;
    unsigned tab = 0;
#define TB(n, v) asm("v_writelane_b32 %0, %1, %2" : "+v"(tab) : "s"((unsigned)(v)), "n"((int)T_##n))
    const unsigned FJ = (unsigned)WS_F + (F_J + 640u * j) * 4u, mt = 2u * (w & 2u) + (w & 1u), w2 = w < 2u ? w : 2u, hw = head * 147456u;
    const unsigned FR = (unsigned)WS_F + F_R * 4u + (8u * j + sp) * 1024u;
    TB(fo, FJ + 4u * head); TB(fj, FJ); TB(fa, FR + (w | 2u) * 256u); TB(fas, FR + w * 256u + nt * 4u);
    TB(fb, FR + 128u + w * 256u); TB(fbs, FR + 128u + w * 256u + nt * 4u);
    TB(ep, ssel(b + w, OOR, (unsigned)WS_F + F_EPOCH * 4u));
    const u16* pwo = wo + nt * 144u; const u16* pwb = wob + nt * 144u;
    TB(wol, PLO(pwo)); TB(woh, PHI(pwo) & 0xffffu); TB(wbl, PLO(pwb)); TB(wbh, PHI(pwb) & 0xffffu); TB(outl, PLO(out)); TB(outh, PHI(out) & 0xffffu); TB(r1l, sslt<3>(w, PLO(pwo), PLO(pwb))); TB(r1h, sslt<3>(w, PHI(pwo), PHI(pwb)) & 0xffffu);
    TB(r1n, sslt<3>(w, 72u * sp + 71u, 4095u) * 1152u + 288u); TB(r1s, sslt<3>(w, (72u * sp + 32u * w) * 1152u, hw + 110592u));
    TB(m0, 131072u + 9216u * w2); TB(w0, hw + 36864u * w2); TB(m1, 24576u + 9216u * w); TB(mw, 65536u + 9216u * w); TB(m3, sslt<3>(w, 102400u + 9216u * w, 0u)); TB(m4, sslt<3>(w, 131072u + 9216u * w, 9216u)); TB(ml, 61440u + 1024u * w);
    const unsigned tA = (unsigned)WS_AF + (8u * j + mt) * (KS_TOT * 1024u), tB = (unsigned)WS_AF + (120u - 8u * j + mt) * (KS_TOT * 1024u);
#define HK(k) (4u * sp + ((nt + k) & 3u))
    TB(w1, HK(1u) * 147456u + w * 36864u); TB(w2, HK(2u) * 147456u + w * 36864u); TB(w3, HK(3u) * 147456u + w * 36864u);
    TB(A1, tA + 18432u + HK(1u) * 4096u); TB(A2, tA + 18432u + HK(2u) * 4096u); TB(A3, tA + 18432u + HK(3u) * 4096u);
    TB(B1, tB + 18432u + HK(1u) * 4096u); TB(B2, tB + 18432u + HK(2u) * 4096u); TB(B3, tB + 18432u + HK(3u) * 4096u);
    TB(Ao, tA + sp * 2048u); TB(Bo, tB + sp * 2048u); TB(Al, tA + 16384u); TB(Bl, tB + 16384u);
    const unsigned nred = w ^ (sp >> 1), blk = sp & 1u;
    const unsigned xg = (unsigned)WS_X + (4u * j + nt) * 589824u + w * 16384u, oA = (128u * j + 16u * mt) * 1152u + nt * 288u, oB = (1920u - 128u * j + 16u * mt) * 1152u + nt * 288u;
    TB(l8, ssel(nred, OOR, ssel(blk, tB, tA) + 16384u)); TB(xr, xg + sp * 65536u); TB(x8r, ssel(nred, OOR, xg + 524288u + (sp & 1u) * 1024u));
    TB(oA, oA + sp * 32u); TB(oB, oB + sp * 32u); TB(o8, ssel(nred, OOR, ssel(blk, oB, oA) + 256u)); TB(x8, xg + 524288u + sp * 2048u);
    TB(f8, xg + w * 131072u + 14400u + sp * 4u); TB(mk8, ssel(nred, 0u, 0xff0000u)); TB(wt, hw + j * 18432u + w * 4608u);
#define XI(i) TB(x##i, xg + ((sp + 1u + i) & 7u) * 65536u + i * 2048u)
    XI(0); XI(1); XI(2); XI(3); XI(4); XI(5); XI(6);
#undef XI
#undef HK
#undef TB
    vp[0] = (u32x4){vw[0], vw[1], vw[2], vw[3]}; vp[1] = (u32x4){vw[4], vw[5], vw[6], vw[7]}; vp[2] = (u32x4){vw[8], vwl, vrd, tab};
}
__global__ __launch_bounds__(256, 1) __attribute__((aligned(4096))) void fused_kernel(const u16* __restrict__ x, const u16* __restrict__ ckv, const u16* __restrict__ wq, const u16* __restrict__ wuk,
                                                       const u16* __restrict__ wuv, const u16* __restrict__ wqkv, const u16* __restrict__ gw,
                                                       const u16* __restrict__ wo, const u16* __restrict__ wob, char* __restrict__ ws_, u16* __restrict__ out) {
    using namespace att;
    extern __shared__ __attribute__((aligned(16))) char lds[];
    u32x4 sa[5]; u32x2 s24, s28; char* ws; unsigned tdum; kaptr ka = (kaptr)__builtin_amdgcn_kernarg_segment_ptr();
    asm volatile("s_load_dwordx8 s[8:15], %[ka], 0x0\n\ts_load_dwordx8 s[16:23], %[ka], 0x20\n\ts_getpc_b64 s[4:5]\n\tv_lshlrev_b32_e32 v191, 7, %[t]\n\ts_bfe_u32 s6, %[b], 0x20003\n\t"
                 "v_lshl_add_u32 v191, s6, 15, v191\n\tv_min_u32_e32 v191, %[e], v191\n\tglobal_load_dword v191, v191, s[4:5]\n\ts_load_dwordx4 s[24:27], %[ka], 0x40\n\ts_load_dwordx2 s[28:29], %[ka], 0x50"
                 : "={s[8:11]}"(sa[0]), "={s[12:15]}"(sa[1]), "={s[16:19]}"(sa[2]), "={s[20:23]}"(sa[3]), "={s[24:25]}"(s24), "={s[26:27]}"(ws), "={s[28:29]}"(s28), "={v191}"(tdum), [ka] "+{s[0:1]}"(ka)
                 : [t] "v"(threadIdx.x), [b] "s"(blockIdx.x), [e] "i"(CT_END) : "s4", "s5", "s6", "scc");
    SBAR();
    const unsigned wid = rfl(threadIdx.x >> 6);
    unsigned tag, vl;
    const int b = blockIdx.x;
    u32x4 vp[5], srdA; u32x2 sd;
    proj_phase(ka, ws, wid, vp, vl, sa, s24, s28, tdum, srdA, sd, tag
    );
    unsigned* const flags = (unsigned*)(ws + WS_F);
    RTS(2);
#define AST(L) do {} while (0)
#define ATT_TAIL "s_endpgm\n"
    unsigned abad;
    asm volatile(ATT_ASM ATT_TAIL "L_end_%=:\n s_nop 1"
                 : "={s94}"(abad)
                 : "{v[0:3]}"(vp[0]), "{v[4:7]}"(vp[1]), "{v[8:11]}"(vp[2]), "{v[12:15]}"(vp[3]), "{v[16:19]}"(vp[4]), "{v20}"(vl), "{s[56:59]}"(srdA), "{s[60:61]}"(sd), "{s62}"(tag),
                   "{s[8:11]}"(sa[0]), "{s[12:15]}"(sa[1]), "{s[16:19]}"(sa[2]), "{s[20:23]}"(sa[3]), "{s24}"(sa[4][0]), "{s3}"(wid)
                 : "memory", "m0", "vcc", "scc", ATT_VCLOB, ATT_SCLOB, ACLOB_LO, ACLOB_HI);
    {
        unsigned b2 = b, w2 = wid;
        asm volatile("s_waitcnt vmcnt(0)" : "+s"(ka), "+s"(b2), "+s"(w2), "+s"(ws) :: "memory");
        const unsigned j2 = b2 >> 5, p2 = w2 & 1u, h2 = (b2 & 7u) * 4u + ((b2 >> 3) & 3u);
#pragma unroll 1
        for (unsigned i = w2 & 2u; i < ((w2 & 2u) ? (abad ? 8u : 4u) : 2u); ++i)
            att_exact(ka[0], ka[2], ws, h2, (i < 4u ? 1920u - 128u * j2 : 128u * j2 - 128u) + 32u * i + 16u * p2, *reinterpret_cast<const float*>(lds + LDS_G + (32u * i + 16u * p2 + (lane_id() & 15u)) * 4u));
        asm volatile("s_waitcnt vmcnt(0)" ::: "memory");
        unsigned* fa = (unsigned*)(ws + WS_F) + F_R + (8u * j2 + (b2 & 7u)) * 256u + (2u + p2) * 64u + ((b2 >> 3) & 3u);
        if (w2 & 2u) st_flag(fa, tag);
        wait_flags(fa, ld_flag(fa), tag);
        const unsigned l2 = lane_id(); u32x4 c[3]; out_consts(c, ka[7], ka[8], ka[10], b2, w2, l2);
        asm volatile("s_waitcnt vmcnt(0) lgkmcnt(0)\n\ts_nop 7\n\ts_branch L_att_stub" :: "{v[0:3]}"(c[0]), "{v[4:7]}"(c[1]), "{v[8:11]}"(c[2]), "{v20}"(l2 * 16u), "{s[56:59]}"(SRDA), "{s60}"(SD(8u * j2, h2, w2)),
                     "{s61}"(SD(120u - 8u * j2, h2, w2)), "{s62}"(tag) : "memory");
    }
    RTS(5);
}

extern "C" void kernel_launch(void* const* d_in, const int* in_sizes, int n_in, void* d_out, int out_size, void* d_ws, size_t ws_size,
                              hipStream_t stream) {
    (void)hipFuncSetAttribute((const void*)fused_kernel, hipFuncAttributeMaxDynamicSharedMemorySize, att::LDS_BYTES);
    fused_kernel<<<dim3(256), 256, att::LDS_BYTES, stream>>>((const u16*)d_in[0], (const u16*)d_in[4], (const u16*)d_in[5], (const u16*)d_in[6], (const u16*)d_in[7], (const u16*)d_in[1],
                                                             (const u16*)d_in[9], (const u16*)d_in[2], (const u16*)d_in[8], (char*)d_ws, (u16*)d_out);
}
```

```cpp
#include <hip/hip_runtime.h>
#include <hip/hip_bf16.h>
#include <stdint.h>
#include <utility>

typedef short bf16x8 __attribute__((ext_vector_type(8)));
typedef short s16x4 __attribute__((ext_vector_type(4)));
typedef float f32x16 __attribute__((ext_vector_type(16)));
typedef float f32x4 __attribute__((ext_vector_type(4)));
typedef unsigned u32x4 __attribute__((ext_vector_type(4)));
typedef unsigned u32x2 __attribute__((ext_vector_type(2)));
typedef unsigned short u16;

constexpr int S = 2048, DM = 576, NH = 32, HD = 128, DC = 512, NQKV = 1728, DHA = 18;
constexpr int NBIG = NH * HD;
constexpr int KS_TOT = 146;

#define SBAR() __builtin_amdgcn_sched_barrier(0)
constexpr size_t WS_Q = 0, WS_K = WS_Q + (size_t)S * NBIG * 2, WS_V = WS_K + (size_t)S * NBIG * 2, WS_AF = WS_V + (size_t)S * NBIG * 2, WS_X = WS_AF + (size_t)128 * KS_TOT * 1024,
                 WS_P = WS_X + (size_t)64 * 16 * 9216, WS_F = WS_P + (size_t)128 * (4 * 32 * 256 + 1024) * 4;
constexpr size_t WS_L = (size_t)96 << 20;
static_assert(WS_F == 96206848, "workspace map is baked into the generated asm");
constexpr int F_EPOCH = 0, F_J = 1024, F_ST = 8192, F_KV = 40960, F_R = 65536;
constexpr unsigned TAG_STEP = 0x9E3779B1u;
__device__ __forceinline__ unsigned ld_flag(const unsigned* p) { return __hip_atomic_load(p, __ATOMIC_RELAXED, __HIP_MEMORY_SCOPE_AGENT); }
__device__ __forceinline__ void st_flag(unsigned* p, unsigned v) { __hip_atomic_store(p, v, __ATOMIC_RELAXED, __HIP_MEMORY_SCOPE_AGENT); }
__device__ __forceinline__ unsigned ld_flag_async(const unsigned* p) { unsigned v; asm volatile("global_load_dword %0, %1, off sc1" : "=v"(v) : "v"(p) : "memory"); return v; }
#define WAITV(v, N) asm volatile("s_waitcnt vmcnt(" #N ")" : "+v"(v) :: "memory")
__device__ __forceinline__ void wait_flags(const unsigned* p, unsigned v, unsigned tag) {
    if (__builtin_expect(__builtin_amdgcn_ballot_w64(v != tag) != 0ull, 0)) {
        unsigned spins = 0;
        do { __builtin_amdgcn_s_sleep(1); } while (__builtin_amdgcn_ballot_w64(ld_flag(p) != tag) != 0ull && ++spins < (1u << 22));
    }
    asm volatile("" ::: "memory");
}

__device__ __forceinline__ unsigned cvtpk(float lo, float hi) {
    unsigned r; asm volatile("v_cvt_pk_bf16_f32 %0, %1, %2" : "=v"(r) : "v"(lo), "v"(hi)); return r;
}
__device__ __forceinline__ float bf2f(u16 v) { return __uint_as_float(((unsigned)v) << 16); }
__device__ __forceinline__ int crow(int r, int hi) { return (r & 3) + 8 * (r >> 2) + 4 * hi; }

typedef __attribute__((address_space(3))) s16x4* lds_s16x4_p;
__device__ __forceinline__ s16x4 trrd(unsigned addr) { return __builtin_amdgcn_ds_read_tr16_b64_v4i16((lds_s16x4_p)addr); }

__device__ __forceinline__ unsigned lane_id() { unsigned l; asm volatile("v_mbcnt_lo_u32_b32 %0, -1, 0\n\tv_mbcnt_hi_u32_b32 %0, -1, %0" : "=v"(l)); return l; }
#define RTS(slot) do {} while (0)
#define CT_END 0x1c380
#define ACLOB_LO "a0","a1","a2","a3","a4","a5","a6","a7","a8","a9","a10","a11","a12","a13","a14","a15","a16","a17","a18","a19","a20","a21","a22","a23","a24","a25","a26","a27","a28","a29","a30","a31","a32","a33","a34","a35","a36","a37","a38","a39","a40","a41","a42","a43","a44","a45","a46","a47","a48","a49","a50","a51","a52","a53","a54","a55","a56","a57","a58","a59","a60","a61","a62","a63","a64","a65","a66","a67","a68","a69","a70","a71","a72","a73","a74","a75","a76","a77","a78","a79","a80","a81","a82","a83","a84","a85","a86","a87","a88","a89","a90","a91","a92","a93","a94","a95","a96","a97","a98","a99","a100","a101","a102","a103","a104","a105","a106","a107","a108","a109","a110","a111","a112","a113","a114","a115","a116","a117","a118","a119","a120","a121","a122","a123","a124","a125","a126","a127"
#define ACLOB_HI "a128","a129","a130","a131","a132","a133","a134","a135","a136","a137","a138","a139","a140","a141","a142","a143","a144","a145","a146","a147","a148","a149","a150","a151","a152","a153","a154","a155","a156","a157","a158","a159","a160","a161","a162","a163","a164","a165","a166","a167","a168","a169","a170","a171","a172","a173","a174","a175","a176","a177","a178","a179","a180","a181","a182","a183","a184","a185","a186","a187","a188","a189","a190","a191","a192","a193","a194","a195","a196","a197","a198","a199","a200","a201","a202","a203","a204","a205","a206","a207","a208","a209","a210","a211","a212","a213","a214","a215","a216","a217","a218","a219","a220","a221","a222","a223","a224","a225","a226","a227","a228","a229","a230","a231","a232","a233","a234","a235","a236","a237","a238","a239","a240","a241","a242","a243","a244","a245","a246","a247","a248","a249","a250","a251","a252","a253","a254","a255"
#define PLO(p) ((unsigned)(size_t)(p))
#define PHI(p) ((unsigned)((size_t)(p) >> 32))
__device__ __forceinline__ unsigned vsel(bool c, unsigned a, unsigned b) { asm("" : "+v"(a), "+v"(b)); return c ? a : b; }
__device__ __forceinline__ unsigned ssel(unsigned c, unsigned a, unsigned b) { unsigned r; asm("s_cmp_lg_u32 %1, 0\n\ts_cselect_b32 %0, %2, %3" : "=s"(r) : "s"(c), "s"(a), "s"(b) : "scc"); return r; }
template <int N> __device__ __forceinline__ unsigned sslt(unsigned c, unsigned a, unsigned b) { unsigned r; asm("s_cmpk_lt_u32 %1, %4\n\ts_cselect_b32 %0, %2, %3" : "=s"(r) : "s"(c), "s"(a), "s"(b), "n"(N) : "scc"); return r; }
namespace att {
constexpr int LDS_BYTES = 163840;
constexpr unsigned LDS_G = 159744u, LDS_L = 161792u;
}
#define SD(J8, H, W) ((unsigned)WS_AF + (((J8) + 2u * ((W) & 2u) + ((W) & 1u)) * KS_TOT + 18u + 4u * (H)) * 1024u)
#define SRDA (u32x4){PLO(ws), PHI(ws) & 0xffffu, (unsigned)WS_L, 0x00020000u}
#define PROJ_ASM \
".p2align 3\n" \
"L_pb:\n" \
"s_waitcnt lgkmcnt(0)\n" \
"s_mov_b32 s64, s10\n" \
"s_and_b32 s65, s11, 0xffff\n" \
"s_mov_b32 s66, 0x200000\n" \
"s_mov_b32 s67, 0x20000\n" \
"s_mov_b32 s68, s14\n" \
"s_and_b32 s69, s15, 0xffff\n" \
"s_mov_b32 s70, 0x400000\n" \
"s_mov_b32 s71, 0x20000\n" \
"s_mov_b32 s72, s16\n" \
"s_and_b32 s73, s17, 0xffff\n" \
"s_mov_b32 s74, 0x400000\n" \
"s_mov_b32 s75, 0x20000\n" \
"s_add_u32 m0, s30, 0x0\n" \
"s_movk_i32 s84, 0x0\n" \
"buffer_load_dwordx4 v22, s[64:67], s84 offen offset:0 lds\n" \
"s_movk_i32 s84, 0x1c00\n" \
"buffer_load_dwordx4 v23, s[64:67], s84 offen offset:1024 lds\n" \
"s_movk_i32 s84, 0x3800\n" \
"buffer_load_dwordx4 v22, s[64:67], s84 offen offset:2048 lds\n" \
"s_movk_i32 s84, 0x5400\n" \
"buffer_load_dwordx4 v23, s[64:67], s84 offen offset:3072 lds\n" \
"s_add_u32 m0, s31, 0x0\n" \
"s_movk_i32 s84, 0x0\n" \
"buffer_load_dwordx4 v26, s[68:71], s84 offen offset:0 lds\n" \
"s_movk_i32 s84, 0x7c00\n" \
"buffer_load_dwordx4 v26, s[68:71], s84 offen offset:1024 lds\n" \
"s_addk_i32 s84, 0x7c00\n" \
"buffer_load_dwordx4 v27, s[68:71], s84 offen offset:2048 lds\n" \
"s_addk_i32 s84, 0x7c00\n" \
"buffer_load_dwordx4 v27, s[68:71], s84 offen offset:3072 lds\n" \
"s_movk_i32 s7, 0x300\n" \
"s_lshr_b32 s4, s2, 1\n" \
"s_and_b32 s4, s4, 60\n" \
"s_or_b32 s4, s4, s3\n" \
"v_lshrrev_b32_e32 v212, 4, v20\n" \
"v_min_u32_e32 v212, 59, v212\n" \
"s_cmpk_lt_u32 s2, 0x80\n" \
"s_cselect_b32 s6, s14, s16\n" \
"v_subrev_u32_e32 v214, 48, v212\n" \
"v_mov_b32_e32 v212, s10\n" \
"v_mov_b32_e32 v215, s6\n" \
"s_cmpk_lt_u32 s2, 0x80\n" \
"s_cselect_b32 s5, s15, s17\n" \
"v_cmp_gt_u32_e32 vcc, s7, v20\n" \
"v_mov_b32_e64 v213, s11\n" \
"v_bfe_u32 v216, v20, 4, 3\n" \
"v_cndmask_b32_e32 v212, v212, v215, vcc\n" \
"v_mov_b32_e32 v215, s5\n" \
"s_lshl_b32 s5, s3, 4\n" \
"v_cndmask_b32_e32 v213, v213, v215, vcc\n" \
"v_lshlrev_b32_e64 v215, 5, v20\n" \
"v_and_b32_e32 v215, 0x7000, v215\n" \
"v_lshl_add_u32 v215, s4, 6, v215\n" \
"s_lshl_b32 s4, s2, 3\n" \
"s_and_b32 s4, s4, 56\n" \
"v_or3_b32 v215, v215, s4, v216\n" \
"s_lshl_b32 s4, s2, 6\n" \
"s_or_b32 s6, s5, s4\n" \
"v_cmp_gt_u32_e64 s[4:5], 6, v214\n" \
"v_add_u32_e32 v215, 0x2000, v215\n" \
"s_nop 0\n" \
"v_cndmask_b32_e64 v216, 4, 2, s[4:5]\n" \
"v_add3_u32 v214, s6, v214, v216\n" \
"s_lshl_b32 s4, s3, 9\n" \
"v_cndmask_b32_e32 v214, v214, v215, vcc\n" \
"v_lshlrev_b32_e32 v214, 7, v214\n" \
"v_mov_b32_e64 v215, 0\n" \
"v_lshl_add_u64 v[184:185], v[212:213], 0, v[214:215]\n" \
"s_or_b32 s32, s4, 0x25000\n" \
"s_add_u32 m0, s30, 0x1000\n" \
"s_mov_b32 s84, 0x8000\n" \
"buffer_load_dwordx4 v22, s[64:67], s84 offen offset:0 lds\n" \
"s_and_b32 s33, s3, 1\n" \
"v_lshrrev_b32_e32 v212, 4, v20\n" \
"v_lshrrev_b32_e64 v214, 8, v20\n" \
"v_bfe_u32 v212, v212, 1, 3\n" \
"v_lshlrev_b32_e32 v215, 2, v20\n" \
"s_addk_i32 s84, 0x1c00\n" \
"buffer_load_dwordx4 v23, s[64:67], s84 offen offset:1024 lds\n" \
"v_lshrrev_b32_e64 v216, 1, v20\n" \
"v_bfe_u32 v213, v20, 4, 4\n" \
"s_lshl_b32 s4, s3, 5\n" \
"v_xor_b32_e32 v212, v212, v214\n" \
"v_lshlrev_b32_e32 v214, 11, v214\n" \
"s_addk_i32 s84, 0x1c00\n" \
"buffer_load_dwordx4 v22, s[64:67], s84 offen offset:2048 lds\n" \
"v_and_b32_e32 v215, 0x300, v215\n" \
"v_and_b32_e64 v217, 24, v216\n" \
"v_and_or_b32 v213, s4, 64, v213\n" \
"v_or3_b32 v214, v215, v214, v217\n" \
"s_lshl_b32 s4, s33, 14\n" \
"s_addk_i32 s84, 0x1c00\n" \
"buffer_load_dwordx4 v23, s[64:67], s84 offen offset:3072 lds\n" \
"v_and_b32_e32 v215, 0xe0, v216\n" \
"v_lshlrev_b32_e32 v213, 7, v213\n" \
"v_lshlrev_b32_e32 v212, 4, v212\n" \
"v_or3_b32 v214, v214, s4, v215\n" \
"v_or_b32_e64 v28, v212, v213\n" \
"s_add_u32 m0, s31, 0x4000\n" \
"s_movk_i32 s84, 0x0\n" \
"buffer_load_dwordx4 v26, s[72:75], s84 offen offset:0 lds\n" \
"v_add_u32_e32 v32, 0x10000, v214\n" \
"v_bitop3_b32 v29, v212, 64, v213 bitop3:0x36\n" \
"v_xor_b32_e32 v33, 0x20, v32\n" \
"v_xor_b32_e64 v34, 0x40, v32\n" \
"v_xor_b32_e32 v35, 0x60, v32\n" \
"s_movk_i32 s84, 0x7c00\n" \
"buffer_load_dwordx4 v26, s[72:75], s84 offen offset:1024 lds\n" \
"v_xor_b32_e32 v36, 0x80, v32\n" \
"v_xor_b32_e32 v37, 0xa0, v32\n" \
"v_xor_b32_e32 v38, 0xc0, v32\n" \
"v_xor_b32_e32 v39, 0xe0, v32\n" \
"s_mov_b32 s80, s26\n" \
"s_addk_i32 s84, 0x7c00\n" \
"buffer_load_dwordx4 v27, s[72:75], s84 offen offset:2048 lds\n" \
"s_and_b32 s81, s27, 0xffff\n" \
"s_mov_b32 s82, 0x10000000\n" \
"s_mov_b32 s83, 0x20000\n" \
"s_mov_b32 s76, s18\n" \
"s_and_b32 s77, s19, 0xffff\n" \
"s_addk_i32 s84, 0x7c00\n" \
"buffer_load_dwordx4 v27, s[72:75], s84 offen offset:3072 lds\n" \
"s_mov_b32 s78, 0x1e6000\n" \
"s_mov_b32 s79, 0x20000\n" \
"s_branch L_pgo_%=\n" \
"L_pret:\n" \
"s_setpc_b64 s[88:89]\n" \
"L_pgo_%=:\n" \
"s_mul_i32 s88, s33, 0xa000\n" \
"s_lshl_b32 s89, s3, 11\n" \
"s_and_b32 s89, s89, 0x1000\n" \
"s_add_u32 s88, s88, s89\n" \
"s_getpc_b64 s[86:87]\n" \
"L_hpa_%=:\n" \
"s_add_u32 s86, s86, L_pp1-L_hpa_%=\n" \
"s_addc_u32 s87, s87, 0\n" \
"s_add_u32 s86, s86, s88\n" \
"s_addc_u32 s87, s87, 0\n" \
"s_add_u32 m0, s30, 0x8000\n" \
"s_movk_i32 s84, 0x80\n" \
"buffer_load_dwordx4 v22, s[64:67], s84 offen offset:0 lds\n" \
"s_lshr_b32 s4, s2, 1\n" \
"s_and_b32 s4, s4, 0x7c\n" \
"v_bfe_u32 v214, v20, 4, 5\n" \
"s_movk_i32 s84, 0x1c80\n" \
"buffer_load_dwordx4 v23, s[64:67], s84 offen offset:1024 lds\n" \
"s_or_b32 s7, s4, s3\n" \
"v_min_u32_e32 v214, 26, v214\n" \
"v_mad_u64_u32 v[214:215], s[4:5], s7, 27, v[214:215]\n" \
"s_movk_i32 s84, 0x3880\n" \
"buffer_load_dwordx4 v22, s[64:67], s84 offen offset:2048 lds\n" \
"v_mul_u32_u24_e32 v215, 0xaaab, v214\n" \
"v_lshrrev_b32_e32 v215, 18, v215\n" \
"s_mov_b32 s4, 0x1fffffa\n" \
"s_movk_i32 s84, 0x5480\n" \
"buffer_load_dwordx4 v23, s[64:67], s84 offen offset:3072 lds\n" \
"v_mad_u64_u32 v[216:217], s[4:5], v215, s4, v[214:215]\n" \
"s_and_b32 s6, s2, 7\n" \
"v_mul_u32_u24_e32 v215, 27, v215\n" \
"s_mov_b64 exec, 0\n" \
"s_swappc_b64 s[88:89], s[86:87]\n" \
"s_mov_b64 exec, -1\n" \
"s_add_u32 s86, s86, 0x2000\n" \
"s_addc_u32 s87, s87, 0\n" \
"s_add_u32 m0, s31, 0x8000\n" \
"s_mov_b32 s84, 0x80000\n" \
"buffer_load_dwordx4 v26, s[68:71], s84 offen offset:0 lds\n" \
"v_lshrrev_b32_e32 v216, 1, v216\n" \
"v_and_b32_e32 v214, 1, v214\n" \
"v_mov_b32_e32 v213, s13\n" \
"s_addk_i32 s84, 0x7c00\n" \
"buffer_load_dwordx4 v26, s[68:71], s84 offen offset:1024 lds\n" \
"v_lshrrev_b32_e32 v212, 4, v20\n" \
"v_mul_u32_u24_e64 v216, 9, v216\n" \
"v_add3_u32 v214, v214, s6, v215\n" \
"s_mov_b64 exec, 0\n" \
"s_swappc_b64 s[88:89], s[86:87]\n" \
"s_mov_b64 exec, -1\n" \
"s_add_u32 s86, s86, 0x2000\n" \
"s_addc_u32 s87, s87, 0\n" \
"s_addk_i32 s84, 0x7c00\n" \
"buffer_load_dwordx4 v27, s[68:71], s84 offen offset:2048 lds\n" \
"v_add_lshl_u32 v214, v214, v216, 7\n" \
"v_mad_u64_u32 v[216:217], s[4:5], s7, 36, v[212:213]\n" \
"s_movk_i32 s4, 0x240\n" \
"s_addk_i32 s84, 0x7c00\n" \
"buffer_load_dwordx4 v27, s[68:71], s84 offen offset:3072 lds\n" \
"v_mov_b32_e32 v219, s9\n" \
"v_mov_b32_e32 v215, 0\n" \
"v_cmp_gt_u32_e32 vcc, s4, v20\n" \
"s_mov_b64 exec, 0\n" \
"s_swappc_b64 s[88:89], s[86:87]\n" \
"s_mov_b64 exec, -1\n" \
"s_add_u32 s86, s86, 0x2000\n" \
"s_addc_u32 s87, s87, 0\n" \
"s_add_u32 m0, s30, 0x9000\n" \
"s_mov_b32 s84, 0x8080\n" \
"buffer_load_dwordx4 v22, s[64:67], s84 offen offset:0 lds\n" \
"s_lshl_b32 s4, s2, 2\n" \
"v_lshl_add_u64 v[186:187], s[18:19], 0, v[214:215]\n" \
"v_mov_b32_e32 v214, s12\n" \
"s_addk_i32 s84, 0x1c00\n" \
"buffer_load_dwordx4 v23, s[64:67], s84 offen offset:1024 lds\n" \
"v_mov_b32_e32 v217, s8\n" \
"s_or_b32 s4, s4, s3\n" \
"v_cndmask_b32_e32 v219, v219, v213, vcc\n" \
"s_addk_i32 s84, 0x1c00\n" \
"buffer_load_dwordx4 v22, s[64:67], s84 offen offset:2048 lds\n" \
"v_and_b32_e32 v213, 0x3ff8, v216\n" \
"s_mul_i32 s4, s4, 18\n" \
"v_cndmask_b32_e32 v218, v217, v214, vcc\n" \
"s_mov_b64 exec, 0\n" \
"s_swappc_b64 s[88:89], s[86:87]\n" \
"s_mov_b64 exec, -1\n" \
"s_andn2_b32 s85, 2, s3\n" \
"s_lshl_b32 s85, s85, 12\n" \
"s_add_u32 s86, s86, s85\n" \
"s_addc_u32 s87, s87, 0\n" \
"s_addk_i32 s84, 0x1c00\n" \
"buffer_load_dwordx4 v23, s[64:67], s84 offen offset:3072 lds\n" \
"v_or_b32_e32 v213, s6, v213\n" \
"v_and_b32_e32 v214, 7, v216\n" \
"v_min_u32_e32 v212, 53, v212\n" \
"s_add_u32 m0, s31, 0xc000\n" \
"s_mov_b32 s84, 0x80000\n" \
"buffer_load_dwordx4 v26, s[72:75], s84 offen offset:0 lds\n" \
"s_sub_i32 s4, s4, 36\n" \
"v_lshl_or_b32 v213, v213, 3, v214\n" \
"v_add_u32_e32 v212, s4, v212\n" \
"s_mov_b64 exec, 0\n" \
"s_swappc_b64 s[88:89], s[86:87]\n" \
"s_mov_b64 exec, -1\n" \
"s_addk_i32 s84, 0x7c00\n" \
"buffer_load_dwordx4 v26, s[72:75], s84 offen offset:1024 lds\n" \
"v_cndmask_b32_e32 v212, v212, v213, vcc\n" \
"v_lshlrev_b32_e32 v214, 7, v212\n" \
"v_lshl_add_u64 v[188:189], v[218:219], 0, v[214:215]\n" \
"s_addk_i32 s84, 0x7c00\n" \
"buffer_load_dwordx4 v27, s[72:75], s84 offen offset:2048 lds\n" \
"s_movk_i32 s5, 0xd80\n" \
"s_lshr_b32 s6, s2, 5\n" \
"v_lshrrev_b32_e32 v212, 4, v20\n" \
"s_addk_i32 s84, 0x7c00\n" \
"buffer_load_dwordx4 v27, s[72:75], s84 offen offset:3072 lds\n" \
"v_lshl_or_b32 v215, s3, 6, v212\n" \
"v_mul_u32_u24_e32 v216, 0x2aab, v215\n" \
"v_lshrrev_b32_e32 v216, 16, v216\n" \
"s_mov_b32 m0, s32\n" \
"s_mov_b32 exec_lo, 0xffffff\n" \
"s_mov_b32 exec_hi, 0xfff0000\n" \
"global_load_lds_dword v[184:185], off\n" \
"s_mov_b64 exec, -1\n" \
"s_mov_b32 s84, 0x5bc0000\n" \
"buffer_load_dword v180, off, s[80:83], s84 sc1\n" \
"s_cmp_lg_u32 s33, 0\n" \
"s_cbranch_scc1 L_w1_%=\n" \
"L_w0_%=:\n" \
"s_waitcnt vmcnt(18)\n" \
"s_barrier\n" \
"ds_read_b64_tr_b16 a[160:161], v32 offset:0\n" \
"ds_read_b64_tr_b16 a[162:163], v32 offset:1024\n" \
"ds_read_b64_tr_b16 a[224:225], v32 offset:8192\n" \
"ds_read_b64_tr_b16 a[226:227], v32 offset:9216\n" \
"ds_read_b128 a[128:131], v28 offset:16384\n" \
"ds_read_b128 a[192:195], v29 offset:16384\n" \
"ds_read_b64_tr_b16 a[164:165], v33 offset:0\n" \
"ds_read_b64_tr_b16 a[166:167], v33 offset:1024\n" \
"ds_read_b64_tr_b16 a[228:229], v33 offset:8192\n" \
"ds_read_b64_tr_b16 a[230:231], v33 offset:9216\n" \
"ds_read_b64_tr_b16 a[168:169], v34 offset:0\n" \
"ds_read_b64_tr_b16 a[170:171], v34 offset:1024\n" \
"ds_read_b64_tr_b16 a[232:233], v34 offset:8192\n" \
"ds_read_b64_tr_b16 a[234:235], v34 offset:9216\n" \
"ds_read_b64_tr_b16 a[172:173], v35 offset:0\n" \
"ds_read_b64_tr_b16 a[174:175], v35 offset:1024\n" \
"ds_read_b64_tr_b16 a[236:237], v35 offset:8192\n" \
"ds_read_b64_tr_b16 a[238:239], v35 offset:9216\n" \
"ds_read_b128 a[132:135], v28 offset:18432\n" \
"ds_read_b128 a[196:199], v29 offset:18432\n" \
"ds_read_b128 a[136:139], v28 offset:20480\n" \
"ds_read_b128 a[200:203], v29 offset:20480\n" \
"ds_read_b128 a[140:143], v28 offset:22528\n" \
"ds_read_b128 a[204:207], v29 offset:22528\n" \
"ds_read_b128 a[144:147], v28 offset:0\n" \
"ds_read_b128 a[208:211], v29 offset:0\n" \
"ds_read_b128 a[148:151], v28 offset:2048\n" \
"ds_read_b128 a[212:215], v29 offset:2048\n" \
"ds_read_b128 a[152:155], v28 offset:4096\n" \
"ds_read_b128 a[216:219], v29 offset:4096\n" \
"ds_read_b128 a[156:159], v28 offset:6144\n" \
"ds_read_b128 a[220:223], v29 offset:6144\n" \
"s_waitcnt lgkmcnt(15)\n" \
"v_mfma_f32_16x16x32_bf16 a[0:3], a[160:163], a[128:131], 0\n" \
"ds_read_b64_tr_b16 a[176:177], v36 offset:0\n" \
"v_mad_i32_i24 v217, v216, -6, v215\n" \
"v_mfma_f32_16x16x32_bf16 a[0:3], a[224:227], a[192:195], a[0:3]\n" \
"v_add_u32_e32 v218, 0xfffffe50, v215\n" \
"s_mul_i32 s35, s6, 0x48\n" \
"v_mfma_f32_16x16x32_bf16 a[4:7], a[164:167], a[128:131], 0\n" \
"v_lshrrev_b32_e32 v219, 1, v217\n" \
"v_add_u32_e64 v218, s35, v216\n" \
"v_mfma_f32_16x16x32_bf16 a[4:7], a[228:231], a[192:195], a[4:7]\n" \
"ds_read_b64_tr_b16 a[178:179], v36 offset:1024\n" \
"v_mul_u32_u24_e32 v219, 0x480, v219\n" \
"v_mfma_f32_16x16x32_bf16 a[8:11], a[168:171], a[128:131], 0\n" \
"v_and_b32_e64 v214, 16, v20\n" \
"v_mad_u32_u24 v219, v218, s5, v219\n" \
"v_mfma_f32_16x16x32_bf16 a[8:11], a[232:235], a[192:195], a[8:11]\n" \
"v_or_b32_e32 v192, v219, v214\n" \
"v_mov_b32_e32 v220, 2.0\n" \
"v_mfma_f32_16x16x32_bf16 a[12:15], a[172:175], a[128:131], 0\n" \
"ds_read_b64_tr_b16 a[240:241], v36 offset:8192\n" \
"v_mov_b32_e32 v222, 7\n" \
"s_waitcnt lgkmcnt(15)\n" \
"v_mfma_f32_16x16x32_bf16 a[12:15], a[236:239], a[192:195], a[12:15]\n" \
"v_lshlrev_b32_e32 v220, 7, v218\n" \
"v_lshrrev_b32_e32 v218, 1, v218\n" \
"v_mfma_f32_16x16x32_bf16 a[28:31], a[172:175], a[132:135], 0\n" \
"v_and_b32_e32 v218, 4, v218\n" \
"v_and_b32_e32 v216, 2, v216\n" \
"v_mfma_f32_16x16x32_bf16 a[28:31], a[236:239], a[196:199], a[28:31]\n" \
"ds_read_b64_tr_b16 a[242:243], v36 offset:9216\n" \
"s_mul_i32 s7, s6, 0x2400\n" \
"v_mfma_f32_16x16x32_bf16 a[24:27], a[168:171], a[132:135], 0\n" \
"s_sub_i32 s34, 0x22000, s7\n" \
"v_bitop3_b32 v216, v217, v218, v216 bitop3:0x1e\n" \
"v_mfma_f32_16x16x32_bf16 a[24:27], a[232:235], a[196:199], a[24:27]\n" \
"v_or_b32_e32 v213, 0x24400, v20\n" \
"v_lshlrev_b32_e64 v216, 4, v216\n" \
"v_mfma_f32_16x16x32_bf16 a[20:23], a[164:167], a[132:135], 0\n" \
"v_add3_u32 v196, v220, s34, v216\n" \
"v_mov_b32_e64 v216, v213\n" \
"v_mfma_f32_16x16x32_bf16 a[20:23], a[228:231], a[196:199], a[20:23]\n" \
"ds_read_b64_tr_b16 a[180:181], v37 offset:0\n" \
"s_movk_i32 s36, 0x1b0\n" \
"v_mfma_f32_16x16x32_bf16 a[16:19], a[160:163], a[132:135], 0\n" \
"v_or_b32_e32 v216, 0x100, v215\n" \
"v_mul_u32_u24_e32 v217, 0x2aab, v216\n" \
"v_mfma_f32_16x16x32_bf16 a[16:19], a[224:227], a[196:199], a[16:19]\n" \
"v_add_u32_e32 v215, 0xffffff50, v215\n" \
"v_lshrrev_b32_e32 v217, 16, v217\n" \
"s_waitcnt lgkmcnt(15)\n" \
"v_mfma_f32_16x16x32_bf16 a[32:35], a[160:163], a[136:139], 0\n" \
"s_mov_b32 m0, s32\n" \
"ds_read_b64_tr_b16 a[182:183], v37 offset:1024\n" \
"v_mfma_f32_16x16x32_bf16 a[32:35], a[224:227], a[200:203], a[32:35]\n" \
"s_mov_b32 exec_lo, 0xff000000\n" \
"s_mov_b32 exec_hi, 0x0\n" \
"global_load_lds_dword v[184:185], off\n" \
"s_mov_b64 exec, -1\n" \
"v_mfma_f32_16x16x32_bf16 a[36:39], a[164:167], a[136:139], 0\n" \
"v_mov_b32_e64 v220, v215\n" \
"v_mad_i32_i24 v218, v217, -6, v216\n" \
"v_mfma_f32_16x16x32_bf16 a[36:39], a[228:231], a[200:203], a[36:39]\n" \
"ds_read_b64_tr_b16 a[244:245], v37 offset:8192\n" \
"v_cmp_gt_u32_e64 vcc, s36, v216\n" \
"v_mfma_f32_16x16x32_bf16 a[40:43], a[168:171], a[136:139], 0\n" \
"v_mov_b32_e32 v221, 7\n" \
"v_lshrrev_b32_e32 v222, 1, v218\n" \
"v_mfma_f32_16x16x32_bf16 a[40:43], a[232:235], a[200:203], a[40:43]\n" \
"s_mov_b32 m0, s32\n" \
"v_cndmask_b32_e32 v217, v220, v217, vcc\n" \
"v_mfma_f32_16x16x32_bf16 a[44:47], a[172:175], a[136:139], 0\n" \
"s_mov_b32 exec_lo, 0xffff\n" \
"s_mov_b32 exec_hi, 0x0\n" \
"global_load_lds_dword v[186:187], off\n" \
"s_mov_b64 exec, -1\n" \
"v_mfma_f32_16x16x32_bf16 a[44:47], a[236:239], a[200:203], a[44:47]\n" \
"ds_read_b64_tr_b16 a[246:247], v37 offset:9216\n" \
"s_movk_i32 s4, 0x48\n" \
"s_waitcnt lgkmcnt(15)\n" \
"v_mfma_f32_16x16x32_bf16 a[60:63], a[172:175], a[140:143], 0\n" \
"v_add_u32_e64 v220, s35, v217\n" \
"v_mul_u32_u24_e32 v222, 0x480, v222\n" \
"v_mfma_f32_16x16x32_bf16 a[60:63], a[236:239], a[204:207], a[60:63]\n" \
"v_mad_u32_u24 v222, v220, s5, v222\n" \
"v_cmp_gt_u32_e64 s[4:5], s4, v215\n" \
"v_mfma_f32_16x16x32_bf16 a[56:59], a[168:171], a[140:143], 0\n" \
"ds_read_b64_tr_b16 a[184:185], v38 offset:0\n" \
"v_cndmask_b32_e64 v215, v221, v218, vcc\n" \
"v_mfma_f32_16x16x32_bf16 a[56:59], a[232:235], a[204:207], a[56:59]\n" \
"v_lshrrev_b32_e32 v218, 1, v220\n" \
"v_or_b32_e32 v214, v222, v214\n" \
"v_mfma_f32_16x16x32_bf16 a[52:55], a[164:167], a[140:143], 0\n" \
"v_mov_b32_e32 v222, 2.0\n" \
"v_and_b32_e32 v218, 4, v218\n" \
"v_mfma_f32_16x16x32_bf16 a[52:55], a[228:231], a[204:207], a[52:55]\n" \
"ds_read_b64_tr_b16 a[186:187], v38 offset:1024\n" \
"v_and_b32_e64 v217, 2, v217\n" \
"v_mfma_f32_16x16x32_bf16 a[48:51], a[160:163], a[140:143], 0\n" \
"v_bitop3_b32 v215, v215, v218, v217 bitop3:0x1e\n" \
"v_lshlrev_b32_e64 v223, 1, v220\n" \
"v_mfma_f32_16x16x32_bf16 a[48:51], a[224:227], a[204:207], a[48:51]\n" \
"v_mov_b32_e32 v224, 2.0\n" \
"v_cndmask_b32_e32 v193, v222, v214, vcc\n" \
"v_mfma_f32_16x16x32_bf16 a[64:67], a[160:163], a[144:147], 0\n" \
"ds_read_b64_tr_b16 a[248:249], v38 offset:8192\n" \
"v_lshlrev_b32_e32 v214, 7, v220\n" \
"s_waitcnt lgkmcnt(15)\n" \
"v_mfma_f32_16x16x32_bf16 a[64:67], a[224:227], a[208:211], a[64:67]\n" \
"v_lshlrev_b32_e32 v215, 4, v215\n" \
"s_mul_i32 s39, s3, 18\n" \
"v_mfma_f32_16x16x32_bf16 a[68:71], a[164:167], a[144:147], 0\n" \
"v_add3_u32 v214, v214, s34, v215\n" \
"v_cndmask_b32_e64 v195, v224, v223, s[4:5]\n" \
"v_mfma_f32_16x16x32_bf16 a[68:71], a[228:231], a[208:211], a[68:71]\n" \
"ds_read_b64_tr_b16 a[250:251], v38 offset:9216\n" \
"s_movk_i32 s4, 0x1f8\n" \
"v_mfma_f32_16x16x32_bf16 a[72:75], a[168:171], a[144:147], 0\n" \
"v_mov_b32_e32 v215, v213\n" \
"s_add_i32 s35, s35, s39\n" \
"v_mfma_f32_16x16x32_bf16 a[72:75], a[232:235], a[208:211], a[72:75]\n" \
"v_cmp_gt_u32_e32 vcc, s4, v216\n" \
"v_add_u32_e64 v212, s35, v212\n" \
"v_mfma_f32_16x16x32_bf16 a[76:79], a[172:175], a[144:147], 0\n" \
"v_mov_b32_e32 v219, 2.0\n" \
"v_cndmask_b32_e32 v197, v215, v214, vcc\n" \
"v_mfma_f32_16x16x32_bf16 a[76:79], a[236:239], a[208:211], a[76:79]\n" \
"ds_read_b64_tr_b16 a[188:189], v39 offset:0\n" \
"s_movk_i32 s35, 0x120\n" \
"v_mfma_f32_16x16x32_bf16 a[92:95], a[172:175], a[148:151], 0\n" \
"v_mul_u32_u24_e32 v214, 0xd80, v212\n" \
"v_cmp_gt_u32_e32 vcc, s35, v20\n" \
"v_mfma_f32_16x16x32_bf16 a[92:95], a[236:239], a[212:215], a[92:95]\n" \
"v_lshrrev_b32_e32 v215, 1, v212\n" \
"s_and_b32 s4, s2, 7\n" \
"v_mfma_f32_16x16x32_bf16 a[88:91], a[168:171], a[148:151], 0\n" \
"ds_read_b64_tr_b16 a[190:191], v39 offset:1024\n" \
"v_cndmask_b32_e64 v194, v219, v214, vcc\n" \
"v_mfma_f32_16x16x32_bf16 a[88:91], a[232:235], a[212:215], a[88:91]\n" \
"v_lshlrev_b32_e32 v214, 7, v212\n" \
"v_lshrrev_b32_e32 v212, 2, v212\n" \
"v_mfma_f32_16x16x32_bf16 a[84:87], a[164:167], a[148:151], 0\n" \
"v_and_b32_e64 v212, 2, v212\n" \
"v_and_or_b32 v212, v215, 1, v212\n" \
"v_mfma_f32_16x16x32_bf16 a[84:87], a[228:231], a[212:215], a[84:87]\n" \
"ds_read_b64_tr_b16 a[252:253], v39 offset:8192\n" \
"s_lshl_b32 s5, s4, 2\n" \
"v_mfma_f32_16x16x32_bf16 a[80:83], a[160:163], a[148:151], 0\n" \
"s_bfe_u32 s38, s2, 0x20003\n" \
"v_lshlrev_b32_e32 v212, 5, v212\n" \
"v_mfma_f32_16x16x32_bf16 a[80:83], a[224:227], a[212:215], a[80:83]\n" \
"s_movk_i32 s35, 0x60\n" \
"s_or_b32 s5, s5, s38\n" \
"s_waitcnt lgkmcnt(15)\n" \
"v_mfma_f32_16x16x32_bf16 a[96:99], a[160:163], a[152:155], 0\n" \
"ds_read_b64_tr_b16 a[254:255], v39 offset:9216\n" \
"v_bitop3_b32 v212, v212, s35, v214 bitop3:0x36\n" \
"v_mfma_f32_16x16x32_bf16 a[96:99], a[224:227], a[216:219], a[96:99]\n" \
"s_lshr_b32 s36, s2, 3\n" \
"v_add_u32_e64 v212, s34, v212\n" \
"v_mfma_f32_16x16x32_bf16 a[100:103], a[164:167], a[152:155], 0\n" \
"s_mul_i32 s34, s5, 0x12000\n" \
"s_or_b32 s40, s34, 0x6000000\n" \
"v_mfma_f32_16x16x32_bf16 a[100:103], a[228:231], a[216:219], a[100:103]\n" \
"s_mul_i32 s34, s36, 0x90000\n" \
"s_lshl_b32 s36, s3, 14\n" \
"v_mfma_f32_16x16x32_bf16 a[104:107], a[168:171], a[152:155], 0\n" \
"s_add_i32 s34, s34, s36\n" \
"s_lshl_b32 s38, s5, 8\n" \
"v_mfma_f32_16x16x32_bf16 a[104:107], a[232:235], a[216:219], a[104:107]\n" \
"s_add_i32 s41, s34, 0x4240000\n" \
"s_mul_i32 s34, s5, 36\n" \
"v_mfma_f32_16x16x32_bf16 a[108:111], a[172:175], a[152:155], 0\n" \
"s_add_i32 s5, s7, s40\n" \
"s_addk_i32 s5, 0x2000\n" \
"v_mfma_f32_16x16x32_bf16 a[108:111], a[236:239], a[216:219], a[108:111]\n" \
"s_mov_b32 s37, 2.0\n" \
"s_or_b32 s38, s38, 0x5be8000\n" \
"v_mfma_f32_16x16x32_bf16 a[124:127], a[172:175], a[156:159], 0\n" \
"s_cmp_lg_u32 s3, 0\n" \
"s_cselect_b32 s36, s37, s5\n" \
"v_mfma_f32_16x16x32_bf16 a[124:127], a[236:239], a[220:223], a[124:127]\n" \
"s_lshl_b32 s5, s6, 2\n" \
"s_or_b32 s5, s38, s5\n" \
"v_mfma_f32_16x16x32_bf16 a[120:123], a[168:171], a[156:159], 0\n" \
"s_lshl_b32 s35, s3, 10\n" \
"v_mfma_f32_16x16x32_bf16 a[120:123], a[232:235], a[220:223], a[120:123]\n" \
"s_cmpk_lt_u32 s3, 3\n" \
"s_cselect_b32 s37, s37, s5\n" \
"v_mfma_f32_16x16x32_bf16 a[116:119], a[164:167], a[156:159], 0\n" \
"s_lshl_b32 s5, s4, 16\n" \
"s_lshl_b32 s4, s4, 11\n" \
"v_mfma_f32_16x16x32_bf16 a[116:119], a[228:231], a[220:223], a[116:119]\n" \
"v_cndmask_b32_e32 v198, v213, v212, vcc\n" \
"v_or_b32_e32 v212, s35, v20\n" \
"v_mfma_f32_16x16x32_bf16 a[112:115], a[160:163], a[156:159], 0\n" \
"s_or_b32 s39, s40, s35\n" \
"s_or_b32 s55, s41, s4\n" \
"v_mfma_f32_16x16x32_bf16 a[112:115], a[224:227], a[220:223], a[112:115]\n" \
"s_waitcnt vmcnt(4) lgkmcnt(0)\n" \
"s_barrier\n" \
"v_mfma_f32_16x16x32_bf16 v[52:55], a[176:179], a[128:131], 0\n" \
"s_add_u32 m0, s30, 0x0\n" \
"s_movk_i32 s84, 0x100\n" \
"v_mfma_f32_16x16x32_bf16 v[52:55], a[240:243], a[192:195], v[52:55]\n" \
"buffer_load_dwordx4 v22, s[64:67], s84 offen offset:0 lds\n" \
"v_mfma_f32_16x16x32_bf16 v[56:59], a[180:183], a[128:131], 0\n" \
"ds_read_b64_tr_b16 a[160:161], v32 offset:32768\n" \
"v_or_b32_e32 v199, 0x22000, v212\n" \
"v_mfma_f32_16x16x32_bf16 v[56:59], a[244:247], a[192:195], v[56:59]\n" \
"s_movk_i32 s84, 0x1d00\n" \
"v_mfma_f32_16x16x32_bf16 v[60:63], a[184:187], a[128:131], 0\n" \
"buffer_load_dwordx4 v23, s[64:67], s84 offen offset:1024 lds\n" \
"v_mfma_f32_16x16x32_bf16 v[60:63], a[248:251], a[192:195], v[60:63]\n" \
"ds_read_b64_tr_b16 a[162:163], v32 offset:33792\n" \
"s_add_i32 s35, s39, s7\n" \
"v_mfma_f32_16x16x32_bf16 v[64:67], a[188:191], a[128:131], 0\n" \
"ds_read_b64_tr_b16 a[224:225], v32 offset:40960\n" \
"s_add_i32 s54, s41, s5\n" \
"v_mfma_f32_16x16x32_bf16 v[64:67], a[252:255], a[192:195], v[64:67]\n" \
"ds_read_b128 a[128:131], v28 offset:49152\n" \
"s_add_i32 s55, s55, 0x80000\n" \
"v_mfma_f32_16x16x32_bf16 v[80:83], a[188:191], a[132:135], 0\n" \
"ds_read_b128 a[192:195], v29 offset:49152\n" \
"v_mov_b32_e32 v215, 0xffff\n" \
"v_mfma_f32_16x16x32_bf16 v[80:83], a[252:255], a[196:199], v[80:83]\n" \
"s_movk_i32 s84, 0x3900\n" \
"v_mfma_f32_16x16x32_bf16 v[76:79], a[184:187], a[132:135], 0\n" \
"buffer_load_dwordx4 v22, s[64:67], s84 offen offset:2048 lds\n" \
"v_mfma_f32_16x16x32_bf16 v[76:79], a[248:251], a[196:199], v[76:79]\n" \
"s_movk_i32 s84, 0x5500\n" \
"v_mfma_f32_16x16x32_bf16 v[72:75], a[180:183], a[132:135], 0\n" \
"buffer_load_dwordx4 v23, s[64:67], s84 offen offset:3072 lds\n" \
"v_mfma_f32_16x16x32_bf16 v[72:75], a[244:247], a[196:199], v[72:75]\n" \
"s_add_u32 m0, s31, 0x0\n" \
"s_mov_b32 s84, 0x100000\n" \
"v_mfma_f32_16x16x32_bf16 v[68:71], a[176:179], a[132:135], 0\n" \
"buffer_load_dwordx4 v26, s[68:71], s84 offen offset:0 lds\n" \
"v_mfma_f32_16x16x32_bf16 v[68:71], a[240:243], a[196:199], v[68:71]\n" \
"ds_read_b128 a[132:135], v28 offset:51200\n" \
"s_and_b32 s4, s3, 1\n" \
"v_mfma_f32_16x16x32_bf16 v[84:87], a[176:179], a[136:139], 0\n" \
"ds_read_b128 a[196:199], v29 offset:51200\n" \
"s_lshl_b32 s5, s4, 4\n" \
"v_mfma_f32_16x16x32_bf16 v[84:87], a[240:243], a[200:203], v[84:87]\n" \
"s_addk_i32 s84, 0x7c00\n" \
"v_mfma_f32_16x16x32_bf16 v[88:91], a[180:183], a[136:139], 0\n" \
"buffer_load_dwordx4 v26, s[68:71], s84 offen offset:1024 lds\n" \
"v_mfma_f32_16x16x32_bf16 v[88:91], a[244:247], a[200:203], v[88:91]\n" \
"ds_read_b64_tr_b16 a[226:227], v32 offset:41984\n" \
"v_lshrrev_b32_e32 v212, 4, v20\n" \
"v_mfma_f32_16x16x32_bf16 v[92:95], a[184:187], a[136:139], 0\n" \
"s_addk_i32 s84, 0x7c00\n" \
"v_mfma_f32_16x16x32_bf16 v[92:95], a[248:251], a[200:203], v[92:95]\n" \
"buffer_load_dwordx4 v27, s[68:71], s84 offen offset:2048 lds\n" \
"v_mfma_f32_16x16x32_bf16 v[96:99], a[188:191], a[136:139], 0\n" \
"ds_read_b64_tr_b16 a[164:165], v33 offset:32768\n" \
"v_lshrrev_b32_e32 v218, 6, v20\n" \
"v_mfma_f32_16x16x32_bf16 v[96:99], a[252:255], a[200:203], v[96:99]\n" \
"ds_read_b128 a[136:139], v28 offset:53248\n" \
"v_and_b32_e64 v219, 12, v218\n" \
"v_mfma_f32_16x16x32_bf16 v[112:115], a[188:191], a[140:143], 0\n" \
"ds_read_b128 a[200:203], v29 offset:53248\n" \
"v_and_or_b32 v214, v212, 15, s5\n" \
"v_mfma_f32_16x16x32_bf16 v[112:115], a[252:255], a[204:207], v[112:115]\n" \
"s_addk_i32 s84, 0x7c00\n" \
"v_mfma_f32_16x16x32_bf16 v[108:111], a[184:187], a[140:143], 0\n" \
"buffer_load_dwordx4 v27, s[68:71], s84 offen offset:3072 lds\n" \
"v_mfma_f32_16x16x32_bf16 v[108:111], a[248:251], a[204:207], v[108:111]\n" \
"s_add_u32 m0, s30, 0x1000\n" \
"s_mov_b32 s84, 0x8100\n" \
"v_mfma_f32_16x16x32_bf16 v[104:107], a[180:183], a[140:143], 0\n" \
"buffer_load_dwordx4 v22, s[64:67], s84 offen offset:0 lds\n" \
"v_mfma_f32_16x16x32_bf16 v[104:107], a[244:247], a[204:207], v[104:107]\n" \
"ds_read_b64_tr_b16 a[166:167], v33 offset:33792\n" \
"v_cmp_gt_u32_e32 vcc, v219, v214\n" \
"v_mfma_f32_16x16x32_bf16 v[100:103], a[176:179], a[140:143], 0\n" \
"ds_read_b64_tr_b16 a[228:229], v33 offset:40960\n" \
"v_mov_b32_e32 v220, 0xffff0000\n" \
"v_mfma_f32_16x16x32_bf16 v[100:103], a[240:243], a[204:207], v[100:103]\n" \
"ds_read_b128 a[140:143], v28 offset:55296\n" \
"s_lshl_b32 s5, s2, 2\n" \
"v_mfma_f32_16x16x32_bf16 v[116:119], a[176:179], a[144:147], 0\n" \
"ds_read_b128 a[204:207], v29 offset:55296\n" \
"v_cndmask_b32_e64 v217, v215, 0, vcc\n" \
"v_mfma_f32_16x16x32_bf16 v[116:119], a[240:243], a[208:211], v[116:119]\n" \
"s_addk_i32 s84, 0x1c00\n" \
"v_mfma_f32_16x16x32_bf16 v[120:123], a[180:183], a[144:147], 0\n" \
"buffer_load_dwordx4 v23, s[64:67], s84 offen offset:1024 lds\n" \
"v_mfma_f32_16x16x32_bf16 v[120:123], a[244:247], a[208:211], v[120:123]\n" \
"s_addk_i32 s84, 0x1c00\n" \
"v_mfma_f32_16x16x32_bf16 v[124:127], a[184:187], a[144:147], 0\n" \
"buffer_load_dwordx4 v22, s[64:67], s84 offen offset:2048 lds\n" \
"v_mfma_f32_16x16x32_bf16 v[124:127], a[248:251], a[208:211], v[124:127]\n" \
"s_addk_i32 s84, 0x1c00\n" \
"v_mfma_f32_16x16x32_bf16 v[128:131], a[188:191], a[144:147], 0\n" \
"buffer_load_dwordx4 v23, s[64:67], s84 offen offset:3072 lds\n" \
"v_mfma_f32_16x16x32_bf16 v[128:131], a[252:255], a[208:211], v[128:131]\n" \
"ds_read_b128 a[144:147], v28 offset:32768\n" \
"v_cmp_lt_u32_e64 vcc, v219, v214\n" \
"v_mfma_f32_16x16x32_bf16 v[144:147], a[188:191], a[148:151], 0\n" \
"ds_read_b128 a[208:211], v29 offset:32768\n" \
"v_mov_b32_e64 v213, s23\n" \
"v_mfma_f32_16x16x32_bf16 v[144:147], a[252:255], a[212:215], v[144:147]\n" \
"s_add_u32 m0, s31, 0x4000\n" \
".fill (L_pb + 3808 - .) / 4, 4, 0xBF800000\n" \
"L_pp1:\n" \
"s_cbranch_execz L_pret\n" \
"s_mov_b32 s84, 0x100000\n" \
"v_mfma_f32_16x16x32_bf16 v[140:143], a[184:187], a[148:151], 0\n" \
"buffer_load_dwordx4 v26, s[72:75], s84 offen offset:0 lds\n" \
"v_mfma_f32_16x16x32_bf16 v[140:143], a[248:251], a[212:215], v[140:143]\n" \
"ds_read_b64_tr_b16 a[230:231], v33 offset:41984\n" \
"s_or_b32 s5, s5, s3\n" \
"v_mfma_f32_16x16x32_bf16 v[136:139], a[180:183], a[148:151], 0\n" \
"s_addk_i32 s84, 0x7c00\n" \
"v_mfma_f32_16x16x32_bf16 v[136:139], a[244:247], a[212:215], v[136:139]\n" \
"buffer_load_dwordx4 v26, s[72:75], s84 offen offset:1024 lds\n" \
"v_mfma_f32_16x16x32_bf16 v[132:135], a[176:179], a[148:151], 0\n" \
"ds_read_b64_tr_b16 a[168:169], v34 offset:32768\n" \
"v_cndmask_b32_e32 v221, 0, v220, vcc\n" \
"v_mfma_f32_16x16x32_bf16 v[132:135], a[240:243], a[212:215], v[132:135]\n" \
"ds_read_b128 a[148:151], v28 offset:34816\n" \
"v_or_b32_e64 v12, v217, v221\n" \
"v_mfma_f32_16x16x32_bf16 v[148:151], a[176:179], a[152:155], 0\n" \
"ds_read_b128 a[212:215], v29 offset:34816\n" \
"v_or_b32_e64 v217, 2, v219\n" \
"v_mfma_f32_16x16x32_bf16 v[148:151], a[240:243], a[216:219], v[148:151]\n" \
"ds_read_b64_tr_b16 a[170:171], v34 offset:33792\n" \
"v_cmp_gt_u32_e64 vcc, v217, v214\n" \
"v_mfma_f32_16x16x32_bf16 v[152:155], a[180:183], a[152:155], 0\n" \
"ds_read_b64_tr_b16 a[232:233], v34 offset:40960\n" \
"v_mov_b32_e64 v216, s25\n" \
"v_mfma_f32_16x16x32_bf16 v[152:155], a[244:247], a[216:219], v[152:155]\n" \
"ds_read_b64_tr_b16 a[234:235], v34 offset:41984\n" \
"s_movk_i32 s40, 0xf400\n" \
"v_mfma_f32_16x16x32_bf16 v[156:159], a[184:187], a[152:155], 0\n" \
"ds_read_b64_tr_b16 a[172:173], v35 offset:32768\n" \
"v_cndmask_b32_e64 v221, v215, 0, vcc\n" \
"v_mfma_f32_16x16x32_bf16 v[156:159], a[248:251], a[216:219], v[156:159]\n" \
"ds_read_b64_tr_b16 a[174:175], v35 offset:33792\n" \
"v_cmp_lt_u32_e32 vcc, v217, v214\n" \
"v_mfma_f32_16x16x32_bf16 v[160:163], a[188:191], a[152:155], 0\n" \
"ds_read_b64_tr_b16 a[236:237], v35 offset:40960\n" \
"s_lshl_b32 s4, s4, 6\n" \
"v_mfma_f32_16x16x32_bf16 v[160:163], a[252:255], a[216:219], v[160:163]\n" \
"ds_read_b128 a[152:155], v28 offset:36864\n" \
"v_cndmask_b32_e32 v217, 0, v220, vcc\n" \
"v_mfma_f32_16x16x32_bf16 v[176:179], a[188:191], a[156:159], 0\n" \
"ds_read_b128 a[216:219], v29 offset:36864\n" \
"v_or_b32_e64 v13, v221, v217\n" \
"v_mfma_f32_16x16x32_bf16 v[176:179], a[252:255], a[220:223], v[176:179]\n" \
"ds_read_b64_tr_b16 a[238:239], v35 offset:41984\n" \
"v_or_b32_e64 v217, 16, v219\n" \
"v_mfma_f32_16x16x32_bf16 v[172:175], a[184:187], a[156:159], 0\n" \
"v_cmp_gt_u32_e32 vcc, v217, v214\n" \
"s_nop 1\n" \
"v_cndmask_b32_e64 v221, v215, 0, vcc\n" \
"v_mfma_f32_16x16x32_bf16 v[172:175], a[248:251], a[220:223], v[172:175]\n" \
"v_cmp_lt_u32_e32 vcc, v217, v214\n" \
"s_nop 1\n" \
"v_cndmask_b32_e64 v217, 0, v220, vcc\n" \
"v_mfma_f32_16x16x32_bf16 v[168:171], a[180:183], a[156:159], 0\n" \
"v_or_b32_e32 v14, v221, v217\n" \
"v_or_b32_e32 v217, 18, v219\n" \
"v_mfma_f32_16x16x32_bf16 v[168:171], a[244:247], a[220:223], v[168:171]\n" \
"v_cmp_gt_u32_e32 vcc, v217, v214\n" \
"s_nop 1\n" \
"v_cndmask_b32_e64 v215, v215, 0, vcc\n" \
"v_mfma_f32_16x16x32_bf16 v[164:167], a[176:179], a[156:159], 0\n" \
"v_cmp_lt_u32_e32 vcc, v217, v214\n" \
"s_nop 1\n" \
"v_cndmask_b32_e64 v214, 0, v220, vcc\n" \
"v_mfma_f32_16x16x32_bf16 v[164:167], a[240:243], a[220:223], v[164:167]\n" \
"ds_read_b128 a[156:159], v28 offset:38912\n" \
"ds_read_b128 a[220:223], v29 offset:38912\n" \
"s_waitcnt lgkmcnt(15)\n" \
"v_mfma_f32_16x16x32_bf16 a[0:3], a[160:163], a[128:131], a[0:3]\n" \
"s_addk_i32 s84, 0x7c00\n" \
"v_mfma_f32_16x16x32_bf16 a[0:3], a[224:227], a[192:195], a[0:3]\n" \
"buffer_load_dwordx4 v27, s[72:75], s84 offen offset:2048 lds\n" \
"v_mfma_f32_16x16x32_bf16 a[4:7], a[164:167], a[128:131], a[4:7]\n" \
"ds_read_b64_tr_b16 a[176:177], v36 offset:32768\n" \
"v_or_b32_e32 v15, v215, v214\n" \
"s_waitcnt lgkmcnt(15)\n" \
"v_mfma_f32_16x16x32_bf16 a[4:7], a[228:231], a[192:195], a[4:7]\n" \
"s_addk_i32 s84, 0x7c00\n" \
"s_waitcnt lgkmcnt(11)\n" \
"v_mfma_f32_16x16x32_bf16 a[8:11], a[168:171], a[128:131], a[8:11]\n" \
"buffer_load_dwordx4 v27, s[72:75], s84 offen offset:3072 lds\n" \
"s_waitcnt lgkmcnt(9)\n" \
"v_mfma_f32_16x16x32_bf16 a[8:11], a[232:235], a[192:195], a[8:11]\n" \
"ds_read_b64_tr_b16 a[178:179], v36 offset:33792\n" \
"v_mad_u64_u32 v[214:215], s[6:7], s5, 42, v[212:213]\n" \
"s_waitcnt lgkmcnt(8)\n" \
"v_mfma_f32_16x16x32_bf16 a[12:15], a[172:175], a[128:131], a[12:15]\n" \
"ds_read_b64_tr_b16 a[240:241], v36 offset:40960\n" \
"s_movk_i32 s5, 0x1440\n" \
"s_waitcnt lgkmcnt(5)\n" \
"v_mfma_f32_16x16x32_bf16 a[12:15], a[236:239], a[192:195], a[12:15]\n" \
"v_cmp_gt_u32_e64 vcc, s5, v214\n" \
"v_min_u32_e32 v217, 0xa43f, v214\n" \
"v_mfma_f32_16x16x32_bf16 a[28:31], a[172:175], a[132:135], a[28:31]\n" \
"v_mov_b32_e32 v214, s24\n" \
"v_cndmask_b32_e32 v215, v216, v213, vcc\n" \
"v_mfma_f32_16x16x32_bf16 a[28:31], a[236:239], a[196:199], a[28:31]\n" \
"ds_read_b64_tr_b16 a[242:243], v36 offset:41984\n" \
"v_mov_b32_e64 v213, s22\n" \
"v_mfma_f32_16x16x32_bf16 a[24:27], a[168:171], a[132:135], a[24:27]\n" \
"s_movk_i32 s6, 0xffee\n" \
"v_cndmask_b32_e32 v214, v214, v213, vcc\n" \
"v_mfma_f32_16x16x32_bf16 a[24:27], a[232:235], a[196:199], a[24:27]\n" \
"v_add_u32_e32 v213, 0xffffebc0, v217\n" \
"s_lshl_b32 s5, s2, 5\n" \
"v_mfma_f32_16x16x32_bf16 a[20:23], a[164:167], a[132:135], a[20:23]\n" \
"v_cndmask_b32_e32 v213, v213, v217, vcc\n" \
"v_lshlrev_b32_e64 v216, 7, v213\n" \
"v_mfma_f32_16x16x32_bf16 a[20:23], a[228:231], a[196:199], a[20:23]\n" \
"ds_read_b64_tr_b16 a[180:181], v37 offset:32768\n" \
"v_mul_u32_u24_e32 v213, 0xe39, v212\n" \
"v_mfma_f32_16x16x32_bf16 a[16:19], a[160:163], a[132:135], a[16:19]\n" \
"v_mov_b32_e32 v217, 0\n" \
"v_lshrrev_b32_e32 v213, 16, v213\n" \
"v_mfma_f32_16x16x32_bf16 a[16:19], a[224:227], a[196:199], a[16:19]\n" \
"v_lshl_add_u64 v[16:17], v[214:215], 0, v[216:217]\n" \
"v_mad_i32_i24 v214, v213, s6, v212\n" \
"v_mfma_f32_16x16x32_bf16 a[32:35], a[160:163], a[136:139], a[32:35]\n" \
"s_mov_b32 m0, s32\n" \
"ds_read_b64_tr_b16 a[182:183], v37 offset:33792\n" \
"v_mfma_f32_16x16x32_bf16 a[32:35], a[224:227], a[200:203], a[32:35]\n" \
"s_mov_b32 exec_lo, 0x0\n" \
"s_mov_b32 exec_hi, 0xff\n" \
"global_load_lds_dword v[184:185], off\n" \
"s_mov_b64 exec, -1\n" \
"v_mfma_f32_16x16x32_bf16 a[36:39], a[164:167], a[136:139], a[36:39]\n" \
"s_add_i32 s5, s5, 32\n" \
"v_lshlrev_b32_e64 v215, 4, v214\n" \
"v_mfma_f32_16x16x32_bf16 a[36:39], a[228:231], a[200:203], a[36:39]\n" \
"ds_read_b64_tr_b16 a[244:245], v37 offset:40960\n" \
"v_add_u32_e64 v216, s5, v215\n" \
"v_mfma_f32_16x16x32_bf16 a[40:43], a[168:171], a[136:139], a[40:43]\n" \
"v_and_b32_e32 v216, 0xf0, v216\n" \
"v_cmp_gt_u32_e64 vcc, 16, v214\n" \
"v_mfma_f32_16x16x32_bf16 a[40:43], a[232:235], a[200:203], a[40:43]\n" \
"s_mov_b32 m0, s32\n" \
"s_movk_i32 s7, 0xf800\n" \
"v_mfma_f32_16x16x32_bf16 a[44:47], a[172:175], a[136:139], a[44:47]\n" \
"s_mov_b32 exec_lo, 0xffff0000\n" \
"s_mov_b32 exec_hi, 0x0\n" \
"global_load_lds_dword v[186:187], off\n" \
"s_mov_b64 exec, -1\n" \
"v_mfma_f32_16x16x32_bf16 a[44:47], a[236:239], a[200:203], a[44:47]\n" \
"ds_read_b64_tr_b16 a[246:247], v37 offset:41984\n" \
"v_cndmask_b32_e64 v214, v215, v216, vcc\n" \
"v_mfma_f32_16x16x32_bf16 a[60:63], a[172:175], a[140:143], a[60:63]\n" \
"v_or_b32_e64 v215, 64, v212\n" \
"v_mul_u32_u24_e32 v216, 0xe39, v215\n" \
"v_mfma_f32_16x16x32_bf16 a[60:63], a[236:239], a[204:207], a[60:63]\n" \
"v_lshrrev_b32_e64 v216, 16, v216\n" \
"v_mad_i32_i24 v215, v216, s6, v215\n" \
"v_mfma_f32_16x16x32_bf16 a[56:59], a[168:171], a[140:143], a[56:59]\n" \
"ds_read_b64_tr_b16 a[184:185], v38 offset:32768\n" \
"v_lshlrev_b32_e64 v217, 4, v215\n" \
"v_mfma_f32_16x16x32_bf16 a[56:59], a[232:235], a[204:207], a[56:59]\n" \
"v_add_u32_e64 v220, s5, v217\n" \
"v_and_b32_e32 v220, 0xf0, v220\n" \
"v_mfma_f32_16x16x32_bf16 a[52:55], a[164:167], a[140:143], a[52:55]\n" \
"v_cmp_gt_u32_e32 vcc, 16, v215\n" \
"s_nop 1\n" \
"v_cndmask_b32_e64 v215, v217, v220, vcc\n" \
"v_mfma_f32_16x16x32_bf16 a[52:55], a[228:231], a[204:207], a[52:55]\n" \
"ds_read_b64_tr_b16 a[186:187], v38 offset:33792\n" \
"v_or_b32_e32 v217, 0x80, v212\n" \
"v_mfma_f32_16x16x32_bf16 a[48:51], a[160:163], a[140:143], a[48:51]\n" \
"v_mul_u32_u24_e32 v220, 0xe39, v217\n" \
"v_lshrrev_b32_e64 v220, 16, v220\n" \
"v_mfma_f32_16x16x32_bf16 a[48:51], a[224:227], a[204:207], a[48:51]\n" \
"v_mad_i32_i24 v217, v220, s6, v217\n" \
"v_lshlrev_b32_e64 v221, 4, v217\n" \
"v_mfma_f32_16x16x32_bf16 a[64:67], a[160:163], a[144:147], a[64:67]\n" \
"ds_read_b64_tr_b16 a[248:249], v38 offset:40960\n" \
"v_add_u32_e64 v222, s5, v221\n" \
"v_mfma_f32_16x16x32_bf16 a[64:67], a[224:227], a[208:211], a[64:67]\n" \
"v_and_b32_e32 v222, 0xf0, v222\n" \
"v_cmp_gt_u32_e64 vcc, 16, v217\n" \
"v_mfma_f32_16x16x32_bf16 a[68:71], a[164:167], a[144:147], a[68:71]\n" \
"s_nop 0\n" \
"v_cndmask_b32_e32 v217, v221, v222, vcc\n" \
"v_or_b32_e32 v221, 0xc0, v212\n" \
"v_mfma_f32_16x16x32_bf16 a[68:71], a[228:231], a[208:211], a[68:71]\n" \
"ds_read_b64_tr_b16 a[250:251], v38 offset:41984\n" \
"v_mul_u32_u24_e32 v222, 0xe39, v221\n" \
"v_mfma_f32_16x16x32_bf16 a[72:75], a[168:171], a[144:147], a[72:75]\n" \
"v_lshrrev_b32_e64 v222, 16, v222\n" \
"v_mad_i32_i24 v221, v222, s6, v221\n" \
"v_mfma_f32_16x16x32_bf16 a[72:75], a[232:235], a[208:211], a[72:75]\n" \
"v_lshlrev_b32_e32 v223, 4, v221\n" \
"v_add_u32_e32 v224, s5, v223\n" \
"v_mfma_f32_16x16x32_bf16 a[76:79], a[172:175], a[144:147], a[76:79]\n" \
"v_and_b32_e32 v224, 0xf0, v224\n" \
"v_cmp_gt_u32_e64 vcc, 16, v221\n" \
"v_mfma_f32_16x16x32_bf16 a[76:79], a[236:239], a[208:211], a[76:79]\n" \
"ds_read_b64_tr_b16 a[188:189], v39 offset:32768\n" \
"v_cndmask_b32_e64 v221, v223, v224, vcc\n" \
"v_mfma_f32_16x16x32_bf16 a[92:95], a[172:175], a[148:151], a[92:95]\n" \
"v_or_b32_e32 v223, 0x100, v212\n" \
"v_mul_u32_u24_e32 v224, 0xe39, v223\n" \
"v_mfma_f32_16x16x32_bf16 a[92:95], a[236:239], a[212:215], a[92:95]\n" \
"v_lshrrev_b32_e64 v224, 16, v224\n" \
"v_mad_i32_i24 v223, v224, s6, v223\n" \
"v_mfma_f32_16x16x32_bf16 a[88:91], a[168:171], a[148:151], a[88:91]\n" \
"ds_read_b64_tr_b16 a[190:191], v39 offset:33792\n" \
"v_lshlrev_b32_e64 v225, 4, v223\n" \
"v_mfma_f32_16x16x32_bf16 a[88:91], a[232:235], a[212:215], a[88:91]\n" \
"v_add_u32_e64 v226, s5, v225\n" \
"v_and_b32_e32 v226, 0xf0, v226\n" \
"v_mfma_f32_16x16x32_bf16 a[84:87], a[164:167], a[148:151], a[84:87]\n" \
"v_cmp_gt_u32_e32 vcc, 16, v223\n" \
"s_nop 1\n" \
"v_cndmask_b32_e64 v223, v225, v226, vcc\n" \
"v_mfma_f32_16x16x32_bf16 a[84:87], a[228:231], a[212:215], a[84:87]\n" \
"ds_read_b64_tr_b16 a[252:253], v39 offset:40960\n" \
"v_or_b32_e32 v225, 0x140, v212\n" \
"v_mfma_f32_16x16x32_bf16 a[80:83], a[160:163], a[148:151], a[80:83]\n" \
"v_mul_u32_u24_e32 v226, 0xe39, v225\n" \
"v_lshrrev_b32_e64 v226, 16, v226\n" \
"v_mfma_f32_16x16x32_bf16 a[80:83], a[224:227], a[212:215], a[80:83]\n" \
"v_mad_i32_i24 v225, v226, s6, v225\n" \
"v_lshlrev_b32_e64 v227, 4, v225\n" \
"v_mfma_f32_16x16x32_bf16 a[96:99], a[160:163], a[152:155], a[96:99]\n" \
"ds_read_b64_tr_b16 a[254:255], v39 offset:41984\n" \
"v_add_u32_e64 v228, s5, v227\n" \
"v_mfma_f32_16x16x32_bf16 a[96:99], a[224:227], a[216:219], a[96:99]\n" \
"v_and_b32_e32 v228, 0xf0, v228\n" \
"v_cmp_gt_u32_e64 vcc, 16, v225\n" \
"v_mfma_f32_16x16x32_bf16 a[100:103], a[164:167], a[152:155], a[100:103]\n" \
"s_nop 0\n" \
"v_cndmask_b32_e32 v225, v227, v228, vcc\n" \
"v_or_b32_e32 v227, 0x180, v212\n" \
"v_mfma_f32_16x16x32_bf16 a[100:103], a[228:231], a[216:219], a[100:103]\n" \
"v_mul_u32_u24_e32 v228, 0xe39, v227\n" \
"v_lshrrev_b32_e64 v228, 16, v228\n" \
"v_mfma_f32_16x16x32_bf16 a[104:107], a[168:171], a[152:155], a[104:107]\n" \
"v_mad_i32_i24 v227, v228, s6, v227\n" \
"v_lshlrev_b32_e64 v229, 4, v227\n" \
"v_mfma_f32_16x16x32_bf16 a[104:107], a[232:235], a[216:219], a[104:107]\n" \
"v_add_u32_e64 v230, s5, v229\n" \
"v_and_b32_e32 v230, 0xf0, v230\n" \
"v_mfma_f32_16x16x32_bf16 a[108:111], a[172:175], a[152:155], a[108:111]\n" \
"v_cmp_gt_u32_e32 vcc, 16, v227\n" \
"s_nop 1\n" \
"v_cndmask_b32_e64 v227, v229, v230, vcc\n" \
"v_mfma_f32_16x16x32_bf16 a[108:111], a[236:239], a[216:219], a[108:111]\n" \
"v_or_b32_e32 v229, 0x1c0, v212\n" \
"v_mul_u32_u24_e32 v230, 0xe39, v229\n" \
"s_waitcnt lgkmcnt(15)\n" \
"v_mfma_f32_16x16x32_bf16 a[124:127], a[172:175], a[156:159], a[124:127]\n" \
"v_lshrrev_b32_e32 v230, 16, v230\n" \
"v_mad_i32_i24 v229, v230, s6, v229\n" \
"v_mfma_f32_16x16x32_bf16 a[124:127], a[236:239], a[220:223], a[124:127]\n" \
"v_lshlrev_b32_e32 v231, 4, v229\n" \
"v_add_u32_e32 v232, s5, v231\n" \
"v_mfma_f32_16x16x32_bf16 a[120:123], a[168:171], a[156:159], a[120:123]\n" \
"v_and_b32_e32 v232, 0xf0, v232\n" \
"v_cmp_gt_u32_e64 vcc, 16, v229\n" \
"v_mfma_f32_16x16x32_bf16 a[120:123], a[232:235], a[220:223], a[120:123]\n" \
"s_nop 0\n" \
"v_cndmask_b32_e32 v229, v231, v232, vcc\n" \
"v_or_b32_e32 v231, 0x200, v212\n" \
"v_mfma_f32_16x16x32_bf16 a[116:119], a[164:167], a[156:159], a[116:119]\n" \
"v_mul_u32_u24_e32 v232, 0xe39, v231\n" \
"v_lshrrev_b32_e64 v232, 16, v232\n" \
"v_mfma_f32_16x16x32_bf16 a[116:119], a[228:231], a[220:223], a[116:119]\n" \
"v_mad_i32_i24 v231, v232, s6, v231\n" \
"v_lshlrev_b32_e64 v233, 4, v231\n" \
"v_mfma_f32_16x16x32_bf16 a[112:115], a[160:163], a[156:159], a[112:115]\n" \
"v_add_u32_e32 v234, s5, v233\n" \
"s_movk_i32 s5, 0x480\n" \
"v_mfma_f32_16x16x32_bf16 a[112:115], a[224:227], a[220:223], a[112:115]\n" \
"s_waitcnt vmcnt(20)\n" \
"v_readfirstlane_b32 s62, v180\n" \
"s_nop 3\n" \
"s_add_u32 s62, s62, 0x9E3779B1\n" \
"s_waitcnt vmcnt(2) lgkmcnt(0)\n" \
"s_barrier\n" \
"v_mfma_f32_16x16x32_bf16 v[52:55], a[176:179], a[128:131], v[52:55]\n" \
"s_add_u32 m0, s30, 0x8000\n" \
"s_movk_i32 s84, 0x180\n" \
"v_mfma_f32_16x16x32_bf16 v[52:55], a[240:243], a[192:195], v[52:55]\n" \
"buffer_load_dwordx4 v22, s[64:67], s84 offen offset:0 lds\n" \
"v_mfma_f32_16x16x32_bf16 v[56:59], a[180:183], a[128:131], v[56:59]\n" \
"ds_read_b64_tr_b16 a[160:161], v32 offset:0\n" \
"v_mad_u32_u24 v0, v213, s5, v214\n" \
"v_mfma_f32_16x16x32_bf16 v[56:59], a[244:247], a[192:195], v[56:59]\n" \
"s_movk_i32 s84, 0x1d80\n" \
"v_mfma_f32_16x16x32_bf16 v[60:63], a[184:187], a[128:131], v[60:63]\n" \
"buffer_load_dwordx4 v23, s[64:67], s84 offen offset:1024 lds\n" \
"v_mfma_f32_16x16x32_bf16 v[60:63], a[248:251], a[192:195], v[60:63]\n" \
"ds_read_b64_tr_b16 a[162:163], v32 offset:1024\n" \
"v_mul_u32_u24_e32 v213, 0x480, v216\n" \
"v_mfma_f32_16x16x32_bf16 v[64:67], a[188:191], a[128:131], v[64:67]\n" \
"ds_read_b64_tr_b16 a[224:225], v32 offset:8192\n" \
"s_movk_i32 s6, 0xfc00\n" \
"v_mfma_f32_16x16x32_bf16 v[64:67], a[252:255], a[192:195], v[64:67]\n" \
"ds_read_b128 a[128:131], v28 offset:16384\n" \
"v_add3_u32 v1, v213, v215, s6\n" \
"v_mfma_f32_16x16x32_bf16 v[80:83], a[188:191], a[132:135], v[80:83]\n" \
"ds_read_b128 a[192:195], v29 offset:16384\n" \
"v_mul_u32_u24_e32 v213, 0x480, v220\n" \
"v_mfma_f32_16x16x32_bf16 v[80:83], a[252:255], a[196:199], v[80:83]\n" \
"s_movk_i32 s84, 0x3980\n" \
"v_mfma_f32_16x16x32_bf16 v[76:79], a[184:187], a[132:135], v[76:79]\n" \
"buffer_load_dwordx4 v22, s[64:67], s84 offen offset:2048 lds\n" \
"v_mfma_f32_16x16x32_bf16 v[76:79], a[248:251], a[196:199], v[76:79]\n" \
"s_movk_i32 s84, 0x5580\n" \
"v_mfma_f32_16x16x32_bf16 v[72:75], a[180:183], a[132:135], v[72:75]\n" \
"buffer_load_dwordx4 v23, s[64:67], s84 offen offset:3072 lds\n" \
"v_mfma_f32_16x16x32_bf16 v[72:75], a[244:247], a[196:199], v[72:75]\n" \
"s_add_u32 m0, s31, 0x8000\n" \
"s_mov_b32 s84, 0x180000\n" \
"v_mfma_f32_16x16x32_bf16 v[68:71], a[176:179], a[132:135], v[68:71]\n" \
"buffer_load_dwordx4 v26, s[68:71], s84 offen offset:0 lds\n" \
"v_mfma_f32_16x16x32_bf16 v[68:71], a[240:243], a[196:199], v[68:71]\n" \
"ds_read_b128 a[132:135], v28 offset:18432\n" \
"v_add3_u32 v2, v213, v217, s7\n" \
"v_mfma_f32_16x16x32_bf16 v[84:87], a[176:179], a[136:139], v[84:87]\n" \
"ds_read_b128 a[196:199], v29 offset:18432\n" \
"v_mul_u32_u24_e32 v213, 0x480, v222\n" \
"v_mfma_f32_16x16x32_bf16 v[84:87], a[240:243], a[200:203], v[84:87]\n" \
"s_addk_i32 s84, 0x7c00\n" \
"v_mfma_f32_16x16x32_bf16 v[88:91], a[180:183], a[136:139], v[88:91]\n" \
"buffer_load_dwordx4 v26, s[68:71], s84 offen offset:1024 lds\n" \
"v_mfma_f32_16x16x32_bf16 v[88:91], a[244:247], a[200:203], v[88:91]\n" \
"ds_read_b64_tr_b16 a[226:227], v32 offset:9216\n" \
"v_add3_u32 v3, v213, v221, s40\n" \
"v_mfma_f32_16x16x32_bf16 v[92:95], a[184:187], a[136:139], v[92:95]\n" \
"s_addk_i32 s84, 0x7c00\n" \
"v_mfma_f32_16x16x32_bf16 v[92:95], a[248:251], a[200:203], v[92:95]\n" \
"buffer_load_dwordx4 v27, s[68:71], s84 offen offset:2048 lds\n" \
"v_mfma_f32_16x16x32_bf16 v[96:99], a[188:191], a[136:139], v[96:99]\n" \
"ds_read_b64_tr_b16 a[164:165], v33 offset:0\n" \
"v_mul_u32_u24_e32 v213, 0x480, v226\n" \
"v_mfma_f32_16x16x32_bf16 v[96:99], a[252:255], a[200:203], v[96:99]\n" \
"ds_read_b128 a[136:139], v28 offset:20480\n" \
"v_add3_u32 v5, v213, v225, s6\n" \
"v_mfma_f32_16x16x32_bf16 v[112:115], a[188:191], a[140:143], v[112:115]\n" \
"ds_read_b128 a[200:203], v29 offset:20480\n" \
"v_mul_u32_u24_e32 v213, 0x480, v228\n" \
"v_mfma_f32_16x16x32_bf16 v[112:115], a[252:255], a[204:207], v[112:115]\n" \
"s_addk_i32 s84, 0x7c00\n" \
"s_nop 0\n" \
"v_mfma_f32_16x16x32_bf16 v[108:111], a[184:187], a[140:143], v[108:111]\n" \
"buffer_load_dwordx4 v27, s[68:71], s84 offen offset:3072 lds\n" \
"v_mfma_f32_16x16x32_bf16 v[108:111], a[248:251], a[204:207], v[108:111]\n" \
"s_add_u32 m0, s30, 0x9000\n" \
"s_mov_b32 s84, 0x8180\n" \
"v_mfma_f32_16x16x32_bf16 v[104:107], a[180:183], a[140:143], v[104:107]\n" \
"buffer_load_dwordx4 v22, s[64:67], s84 offen offset:0 lds\n" \
"v_mfma_f32_16x16x32_bf16 v[104:107], a[244:247], a[204:207], v[104:107]\n" \
"ds_read_b64_tr_b16 a[166:167], v33 offset:1024\n" \
"v_add3_u32 v6, v213, v227, s7\n" \
"v_mfma_f32_16x16x32_bf16 v[100:103], a[176:179], a[140:143], v[100:103]\n" \
"ds_read_b64_tr_b16 a[228:229], v33 offset:8192\n" \
"v_mul_u32_u24_e32 v213, 0x480, v230\n" \
"v_mfma_f32_16x16x32_bf16 v[100:103], a[240:243], a[204:207], v[100:103]\n" \
"ds_read_b128 a[140:143], v28 offset:22528\n" \
"v_add3_u32 v7, v213, v229, s40\n" \
"v_mfma_f32_16x16x32_bf16 v[116:119], a[176:179], a[144:147], v[116:119]\n" \
"ds_read_b128 a[204:207], v29 offset:22528\n" \
"s_lshl_b32 s7, s3, 3\n" \
"v_mfma_f32_16x16x32_bf16 v[116:119], a[240:243], a[208:211], v[116:119]\n" \
"s_addk_i32 s84, 0x1c00\n" \
"v_mfma_f32_16x16x32_bf16 v[120:123], a[180:183], a[144:147], v[120:123]\n" \
"buffer_load_dwordx4 v23, s[64:67], s84 offen offset:1024 lds\n" \
"v_mfma_f32_16x16x32_bf16 v[120:123], a[244:247], a[208:211], v[120:123]\n" \
"s_addk_i32 s84, 0x1c00\n" \
"v_mfma_f32_16x16x32_bf16 v[124:127], a[184:187], a[144:147], v[124:127]\n" \
"buffer_load_dwordx4 v22, s[64:67], s84 offen offset:2048 lds\n" \
"v_mfma_f32_16x16x32_bf16 v[124:127], a[248:251], a[208:211], v[124:127]\n" \
"s_addk_i32 s84, 0x1c00\n" \
"v_mfma_f32_16x16x32_bf16 v[128:131], a[188:191], a[144:147], v[128:131]\n" \
"buffer_load_dwordx4 v23, s[64:67], s84 offen offset:3072 lds\n" \
"v_mfma_f32_16x16x32_bf16 v[128:131], a[252:255], a[208:211], v[128:131]\n" \
"ds_read_b128 a[144:147], v28 offset:0\n" \
"s_lshl_b32 s40, s3, 1\n" \
"v_mfma_f32_16x16x32_bf16 v[144:147], a[188:191], a[148:151], v[144:147]\n" \
"ds_read_b128 a[208:211], v29 offset:0\n" \
"s_and_b32 s7, s7, 16\n" \
"v_mfma_f32_16x16x32_bf16 v[144:147], a[252:255], a[212:215], v[144:147]\n" \
"s_add_u32 m0, s31, 0xc000\n" \
"s_mov_b32 s84, 0x180000\n" \
"v_mfma_f32_16x16x32_bf16 v[140:143], a[184:187], a[148:151], v[140:143]\n" \
"buffer_load_dwordx4 v26, s[72:75], s84 offen offset:0 lds\n" \
"v_mfma_f32_16x16x32_bf16 v[140:143], a[248:251], a[212:215], v[140:143]\n" \
"ds_read_b64_tr_b16 a[230:231], v33 offset:9216\n" \
"s_and_b32 s40, s40, 2\n" \
"v_mfma_f32_16x16x32_bf16 v[136:139], a[180:183], a[148:151], v[136:139]\n" \
"s_addk_i32 s84, 0x7c00\n" \
"v_mfma_f32_16x16x32_bf16 v[136:139], a[244:247], a[212:215], v[136:139]\n" \
"buffer_load_dwordx4 v26, s[72:75], s84 offen offset:1024 lds\n" \
"v_mfma_f32_16x16x32_bf16 v[132:135], a[176:179], a[148:151], v[132:135]\n" \
"ds_read_b64_tr_b16 a[168:169], v34 offset:0\n" \
"s_and_b32 s6, s2, 7\n" \
"v_mfma_f32_16x16x32_bf16 v[132:135], a[240:243], a[212:215], v[132:135]\n" \
"ds_read_b128 a[148:151], v28 offset:2048\n" \
"v_bfe_u32 v213, v20, 7, 1\n" \
"v_mfma_f32_16x16x32_bf16 v[148:151], a[176:179], a[152:155], v[148:151]\n" \
"ds_read_b128 a[212:215], v29 offset:2048\n" \
"s_or_b32 s7, s40, s7\n" \
"v_mfma_f32_16x16x32_bf16 v[148:151], a[240:243], a[216:219], v[148:151]\n" \
"ds_read_b64_tr_b16 a[170:171], v34 offset:1024\n" \
"v_and_b32_e32 v234, 0xf0, v234\n" \
"v_mfma_f32_16x16x32_bf16 v[152:155], a[180:183], a[152:155], v[152:155]\n" \
"ds_read_b64_tr_b16 a[232:233], v34 offset:8192\n" \
"v_cmp_gt_u32_e64 vcc, 16, v231\n" \
"v_mfma_f32_16x16x32_bf16 v[152:155], a[244:247], a[216:219], v[152:155]\n" \
"ds_read_b64_tr_b16 a[234:235], v34 offset:9216\n" \
"v_or3_b32 v213, s7, v213, v219\n" \
"v_mfma_f32_16x16x32_bf16 v[156:159], a[184:187], a[152:155], v[156:159]\n" \
"ds_read_b64_tr_b16 a[172:173], v35 offset:0\n" \
"v_and_b32_e64 v214, 32, v20\n" \
"v_mfma_f32_16x16x32_bf16 v[156:159], a[248:251], a[216:219], v[156:159]\n" \
"ds_read_b64_tr_b16 a[174:175], v35 offset:1024\n" \
"s_lshl_b32 s6, s6, 5\n" \
"v_mfma_f32_16x16x32_bf16 v[160:163], a[188:191], a[152:155], v[160:163]\n" \
"ds_read_b64_tr_b16 a[236:237], v35 offset:8192\n" \
"v_cndmask_b32_e32 v231, v233, v234, vcc\n" \
"v_mfma_f32_16x16x32_bf16 v[160:163], a[252:255], a[216:219], v[160:163]\n" \
"ds_read_b128 a[152:155], v28 offset:4096\n" \
"v_mul_u32_u24_e64 v213, 18, v213\n" \
"v_mfma_f32_16x16x32_bf16 v[176:179], a[188:191], a[156:159], v[176:179]\n" \
"ds_read_b128 a[216:219], v29 offset:4096\n" \
"v_mov_b32_e32 v215, 0x100\n" \
"v_mfma_f32_16x16x32_bf16 v[176:179], a[252:255], a[220:223], v[176:179]\n" \
"ds_read_b64_tr_b16 a[238:239], v35 offset:9216\n" \
"v_mov_b32_e64 v216, s6\n" \
"v_mfma_f32_16x16x32_bf16 v[172:175], a[184:187], a[156:159], v[172:175]\n" \
"v_cmp_eq_u32_e64 vcc, 0, v214\n" \
"v_and_or_b32 v213, v218, 1, v213\n" \
"v_mfma_f32_16x16x32_bf16 v[172:175], a[248:251], a[220:223], v[172:175]\n" \
"v_lshlrev_b32_e32 v212, 3, v212\n" \
"v_cndmask_b32_e32 v214, v215, v216, vcc\n" \
"v_mfma_f32_16x16x32_bf16 v[168:171], a[180:183], a[156:159], v[168:171]\n" \
"v_mad_u32_u24 v213, v213, s5, v214\n" \
"v_and_or_b32 v213, v20, 16, v213\n" \
"v_mfma_f32_16x16x32_bf16 v[168:171], a[244:247], a[220:223], v[168:171]\n" \
"v_add_u32_e32 v9, 0x4800, v213\n" \
"v_mul_u32_u24_e32 v213, 0x120, v218\n" \
"v_mfma_f32_16x16x32_bf16 v[164:167], a[176:179], a[156:159], v[164:167]\n" \
"v_and_or_b32 v10, v212, 24, v213\n" \
"v_lshrrev_b32_e64 v213, 2, v20\n" \
"v_mfma_f32_16x16x32_bf16 v[164:167], a[240:243], a[220:223], v[164:167]\n" \
"ds_read_b128 a[156:159], v28 offset:6144\n" \
"ds_read_b128 a[220:223], v29 offset:6144\n" \
"s_waitcnt lgkmcnt(15)\n" \
"v_mfma_f32_16x16x32_bf16 a[0:3], a[160:163], a[128:131], a[0:3]\n" \
"s_addk_i32 s84, 0x7c00\n" \
"v_mfma_f32_16x16x32_bf16 a[0:3], a[224:227], a[192:195], a[0:3]\n" \
"buffer_load_dwordx4 v27, s[72:75], s84 offen offset:2048 lds\n" \
"v_mfma_f32_16x16x32_bf16 a[4:7], a[164:167], a[128:131], a[4:7]\n" \
"ds_read_b64_tr_b16 a[176:177], v36 offset:0\n" \
"v_and_or_b32 v213, v213, 60, s4\n" \
"s_waitcnt lgkmcnt(15)\n" \
"v_mfma_f32_16x16x32_bf16 a[4:7], a[228:231], a[192:195], a[4:7]\n" \
"s_addk_i32 s84, 0x7c00\n" \
"s_waitcnt lgkmcnt(11)\n" \
"v_mfma_f32_16x16x32_bf16 a[8:11], a[168:171], a[128:131], a[8:11]\n" \
"buffer_load_dwordx4 v27, s[72:75], s84 offen offset:3072 lds\n" \
"s_waitcnt lgkmcnt(9)\n" \
"v_mfma_f32_16x16x32_bf16 a[8:11], a[232:235], a[192:195], a[8:11]\n" \
"ds_read_b64_tr_b16 a[178:179], v36 offset:1024\n" \
"v_lshl_or_b32 v212, s3, 9, v212\n" \
"s_waitcnt lgkmcnt(8)\n" \
"v_mfma_f32_16x16x32_bf16 a[12:15], a[172:175], a[128:131], a[12:15]\n" \
"ds_read_b64_tr_b16 a[240:241], v36 offset:8192\n" \
"v_mad_u32_u24 v4, v224, s5, v223\n" \
"s_waitcnt lgkmcnt(5)\n" \
"v_mfma_f32_16x16x32_bf16 a[12:15], a[236:239], a[192:195], a[12:15]\n" \
"v_mad_u32_u24 v8, v232, s5, v231\n" \
"v_or_b32_e32 v18, 0x27000, v213\n" \
"v_mfma_f32_16x16x32_bf16 a[28:31], a[172:175], a[132:135], a[28:31]\n" \
"v_or_b32_e32 v19, 0x27800, v212\n" \
"v_mov_b32_e32 v219, 0x1000c0c\n" \
"v_mfma_f32_16x16x32_bf16 a[28:31], a[236:239], a[196:199], a[28:31]\n" \
"ds_read_b64_tr_b16 a[242:243], v36 offset:9216\n" \
"v_mov_b32_e32 v220, 0xc0c0100\n" \
"v_mfma_f32_16x16x32_bf16 a[24:27], a[168:171], a[132:135], a[24:27]\n" \
"buffer_load_dwordx4 v[200:203], v192, s[76:79], s34 offen offset:0\n" \
"v_mfma_f32_16x16x32_bf16 a[24:27], a[232:235], a[196:199], a[24:27]\n" \
"v_bfe_u32 v218, v20, 4, 1\n" \
"v_lshrrev_b32_e64 v213, 8, v20\n" \
"v_mfma_f32_16x16x32_bf16 a[20:23], a[164:167], a[132:135], a[20:23]\n" \
"v_cmp_eq_u32_e32 vcc, 0, v218\n" \
"s_lshr_b32 s4, s2, 5\n" \
"v_mfma_f32_16x16x32_bf16 a[20:23], a[228:231], a[196:199], a[20:23]\n" \
"ds_read_b64_tr_b16 a[180:181], v37 offset:0\n" \
"s_and_b32 s5, s3, 1\n" \
"v_mfma_f32_16x16x32_bf16 a[16:19], a[160:163], a[132:135], a[16:19]\n" \
"v_bfe_u32 v217, v20, 4, 4\n" \
"v_cndmask_b32_e32 v219, v219, v220, vcc\n" \
"v_mfma_f32_16x16x32_bf16 a[16:19], a[224:227], a[196:199], a[16:19]\n" \
"v_mov_b32_e32 v220, 0x3020c0c\n" \
"v_mov_b32_e32 v221, 0xc0c0302\n" \
"v_mfma_f32_16x16x32_bf16 a[32:35], a[160:163], a[136:139], a[32:35]\n" \
"buffer_load_dwordx4 v[204:207], v193, s[76:79], s34 offen offset:0\n" \
"v_mfma_f32_16x16x32_bf16 a[32:35], a[224:227], a[200:203], a[32:35]\n" \
"s_mov_b32 m0, s32\n" \
"ds_read_b64_tr_b16 a[182:183], v37 offset:1024\n" \
"v_mfma_f32_16x16x32_bf16 a[36:39], a[164:167], a[136:139], a[36:39]\n" \
"s_mov_b32 exec_lo, 0x0\n" \
"s_mov_b32 exec_hi, 0xff00\n" \
"global_load_lds_dword v[184:185], off\n" \
"s_mov_b64 exec, -1\n" \
"v_mfma_f32_16x16x32_bf16 a[36:39], a[228:231], a[200:203], a[36:39]\n" \
"ds_read_b64_tr_b16 a[244:245], v37 offset:8192\n" \
"v_lshl_or_b32 v218, v213, 2, v218\n" \
"v_mfma_f32_16x16x32_bf16 a[40:43], a[168:171], a[136:139], a[40:43]\n" \
"s_xor_b32 s6, s4, 15\n" \
"v_mfma_f32_16x16x32_bf16 a[40:43], a[232:235], a[200:203], a[40:43]\n" \
"buffer_load_dword v208, v194, s[76:79], s34 offen offset:32\n" \
"v_mfma_f32_16x16x32_bf16 a[44:47], a[172:175], a[136:139], a[44:47]\n" \
"ds_read_b64_tr_b16 a[246:247], v37 offset:9216\n" \
"v_mfma_f32_16x16x32_bf16 a[44:47], a[236:239], a[200:203], a[44:47]\n" \
"s_cmpk_lt_u32 s3, 2\n" \
"s_cselect_b32 s7, s6, s4\n" \
"v_mfma_f32_16x16x32_bf16 a[60:63], a[172:175], a[140:143], a[60:63]\n" \
"s_lshl_b32 s40, s7, 7\n" \
"s_lshl_b32 s41, s5, 6\n" \
"v_mfma_f32_16x16x32_bf16 a[60:63], a[236:239], a[204:207], a[60:63]\n" \
"v_cndmask_b32_e64 v220, v220, v221, vcc\n" \
"v_mov_b32_e32 v221, 0xc0c0c0c\n" \
"v_mfma_f32_16x16x32_bf16 a[56:59], a[168:171], a[140:143], a[56:59]\n" \
"buffer_load_dword v209, v194, s[76:79], s34 offen offset:1184\n" \
"v_mfma_f32_16x16x32_bf16 a[56:59], a[232:235], a[204:207], a[56:59]\n" \
"ds_read_b64_tr_b16 a[184:185], v38 offset:0\n" \
"v_cmp_gt_u32_e64 vcc, v218, v217\n" \
"v_mfma_f32_16x16x32_bf16 a[52:55], a[164:167], a[140:143], a[52:55]\n" \
"v_or_b32_e32 v218, 2, v218\n" \
"v_lshrrev_b32_e32 v212, 7, v20\n" \
"v_mfma_f32_16x16x32_bf16 a[52:55], a[228:231], a[204:207], a[52:55]\n" \
"ds_read_b64_tr_b16 a[186:187], v38 offset:1024\n" \
"s_or_b32 s41, s41, s40\n" \
"v_mfma_f32_16x16x32_bf16 a[48:51], a[160:163], a[140:143], a[48:51]\n" \
"v_cndmask_b32_e32 v40, v221, v219, vcc\n" \
"v_cndmask_b32_e64 v42, v219, v221, vcc\n" \
"v_mfma_f32_16x16x32_bf16 a[48:51], a[224:227], a[204:207], a[48:51]\n" \
"v_cmp_gt_u32_e32 vcc, v218, v217\n" \
"v_and_b32_e32 v218, 16, v20\n" \
"v_mfma_f32_16x16x32_bf16 a[64:67], a[160:163], a[144:147], a[64:67]\n" \
"buffer_load_dword v211, v194, s[76:79], s34 offen offset:2336\n" \
"v_mfma_f32_16x16x32_bf16 a[64:67], a[224:227], a[208:211], a[64:67]\n" \
"ds_read_b64_tr_b16 a[248:249], v38 offset:8192\n" \
"s_movk_i32 s42, 0x3f80\n" \
"v_mfma_f32_16x16x32_bf16 a[68:71], a[164:167], a[144:147], a[68:71]\n" \
"v_or_b32_e32 v214, s41, v212\n" \
"v_and_b32_e32 v217, 0x320, v20\n" \
"v_mfma_f32_16x16x32_bf16 a[68:71], a[228:231], a[208:211], a[68:71]\n" \
"ds_read_b64_tr_b16 a[250:251], v38 offset:9216\n" \
"s_movk_i32 s41, 0x100\n" \
"v_mfma_f32_16x16x32_bf16 a[72:75], a[168:171], a[144:147], a[72:75]\n" \
"v_lshlrev_b32_e64 v218, v218, s42\n" \
"v_mov_b32_e32 v219, 0\n" \
"v_mfma_f32_16x16x32_bf16 a[72:75], a[232:235], a[208:211], a[72:75]\n" \
"s_mov_b32 s76, s20\n" \
"s_and_b32 s77, s21, 0xffff\n" \
"v_mfma_f32_16x16x32_bf16 a[76:79], a[172:175], a[144:147], a[76:79]\n" \
"s_mov_b32 s78, 0x480\n" \
"v_lshrrev_b32_e32 v215, 6, v20\n" \
"v_mfma_f32_16x16x32_bf16 a[76:79], a[236:239], a[208:211], a[76:79]\n" \
"buffer_load_ushort v210, v195, s[76:79], 0 offen\n" \
"v_mfma_f32_16x16x32_bf16 a[92:95], a[172:175], a[148:151], a[92:95]\n" \
"ds_read_b64_tr_b16 a[188:189], v39 offset:0\n" \
"v_cndmask_b32_e64 v41, v221, v220, vcc\n" \
"v_mfma_f32_16x16x32_bf16 a[92:95], a[236:239], a[212:215], a[92:95]\n" \
"v_cndmask_b32_e32 v43, v220, v221, vcc\n" \
"v_cmp_eq_u32_e32 vcc, s41, v217\n" \
"v_mfma_f32_16x16x32_bf16 a[88:91], a[168:171], a[148:151], a[88:91]\n" \
"s_mov_b32 s76, s8\n" \
"s_and_b32 s77, s9, 0xffff\n" \
"v_mfma_f32_16x16x32_bf16 a[88:91], a[232:235], a[212:215], a[88:91]\n" \
"s_mov_b32 s78, 0x240000\n" \
"ds_read_b64_tr_b16 a[190:191], v39 offset:1024\n" \
".fill (L_pb + 7904 - .) / 4, 4, 0xBF800000\n" \
"L_pp2:\n" \
"s_cbranch_execz L_pret\n" \
"v_mfma_f32_16x16x32_bf16 a[84:87], a[164:167], a[148:151], a[84:87]\n" \
"v_and_b32_e32 v217, 0x330, v20\n" \
"v_bfe_u32 v216, v20, 6, 2\n" \
"v_mfma_f32_16x16x32_bf16 a[84:87], a[228:231], a[212:215], a[84:87]\n" \
"ds_read_b64_tr_b16 a[252:253], v39 offset:8192\n" \
"v_cndmask_b32_e32 v46, v219, v218, vcc\n" \
"v_mfma_f32_16x16x32_bf16 a[80:83], a[160:163], a[148:151], a[80:83]\n" \
"v_mov_b32_e32 v218, 0x3f80\n" \
"v_cmp_eq_u32_e64 vcc, 32, v217\n" \
"v_mfma_f32_16x16x32_bf16 a[80:83], a[224:227], a[212:215], a[80:83]\n" \
"s_add_i32 s41, s40, -16\n" \
"v_lshrrev_b32_e32 v219, 1, v20\n" \
"v_mfma_f32_16x16x32_bf16 a[96:99], a[160:163], a[152:155], a[96:99]\n" \
"ds_read_b64_tr_b16 a[254:255], v39 offset:9216\n" \
"v_bfe_u32 v215, v215, 1, 1\n" \
"v_mfma_f32_16x16x32_bf16 a[96:99], a[224:227], a[216:219], a[96:99]\n" \
"v_cndmask_b32_e32 v47, 0, v218, vcc\n" \
"s_mov_b32 s42, 0\n" \
"v_mfma_f32_16x16x32_bf16 a[100:103], a[164:167], a[152:155], a[100:103]\n" \
"s_cmp_lg_u32 s40, 0\n" \
"s_cselect_b32 s41, s41, s42\n" \
"v_mfma_f32_16x16x32_bf16 a[100:103], a[228:231], a[216:219], a[100:103]\n" \
"v_lshl_or_b32 v217, s5, 3, v212\n" \
"v_lshlrev_b32_e64 v218, 10, v213\n" \
"v_mfma_f32_16x16x32_bf16 a[104:107], a[168:171], a[152:155], a[104:107]\n" \
"v_lshlrev_b32_e32 v216, 7, v216\n" \
"v_and_b32_e32 v219, 24, v219\n" \
"v_mfma_f32_16x16x32_bf16 a[104:107], a[232:235], a[216:219], a[104:107]\n" \
"v_and_or_b32 v212, v212, 2, v215\n" \
"v_add_u32_e64 v217, s41, v217\n" \
"v_mfma_f32_16x16x32_bf16 a[108:111], a[172:175], a[152:155], a[108:111]\n" \
"v_or3_b32 v216, v216, v218, v219\n" \
"v_lshlrev_b32_e64 v212, 5, v212\n" \
"v_mfma_f32_16x16x32_bf16 a[108:111], a[236:239], a[216:219], a[108:111]\n" \
"s_mov_b32 s41, 0x14000\n" \
"v_or3_b32 v48, v216, v212, s41\n" \
"s_waitcnt lgkmcnt(15)\n" \
"v_mfma_f32_16x16x32_bf16 a[124:127], a[172:175], a[156:159], a[124:127]\n" \
"s_lshl_b32 s41, s5, 2\n" \
"s_lshl_b32 s7, s7, 3\n" \
"v_mfma_f32_16x16x32_bf16 a[124:127], a[236:239], a[220:223], a[124:127]\n" \
"v_or_b32_e32 v212, s41, v213\n" \
"s_or_b32 s7, s7, s41\n" \
"v_mfma_f32_16x16x32_bf16 a[120:123], a[168:171], a[156:159], a[120:123]\n" \
"s_mov_b32 s41, 0xff800000\n" \
"v_mfma_f32_16x16x32_bf16 a[120:123], a[232:235], a[220:223], a[120:123]\n" \
"s_cmp_lg_u32 s40, 0\n" \
"s_cselect_b32 s47, s42, s41\n" \
"v_mfma_f32_16x16x32_bf16 a[116:119], a[164:167], a[156:159], a[116:119]\n" \
"s_mov_b32 s40, 0xc000\n" \
"s_mov_b32 s41, 0x8000\n" \
"v_mfma_f32_16x16x32_bf16 a[116:119], a[228:231], a[220:223], a[116:119]\n" \
"s_cmp_lg_u32 s4, 0\n" \
"s_cselect_b32 s51, s40, s41\n" \
"v_mfma_f32_16x16x32_bf16 a[112:115], a[160:163], a[156:159], a[112:115]\n" \
"s_movk_i32 s40, 0x4000\n" \
"v_mfma_f32_16x16x32_bf16 a[112:115], a[224:227], a[220:223], a[112:115]\n" \
"s_waitcnt vmcnt(7) lgkmcnt(0)\n" \
"s_barrier\n" \
"v_mfma_f32_16x16x32_bf16 v[52:55], a[176:179], a[128:131], v[52:55]\n" \
"s_add_u32 m0, s30, 0x0\n" \
"s_movk_i32 s84, 0x200\n" \
"v_mfma_f32_16x16x32_bf16 v[52:55], a[240:243], a[192:195], v[52:55]\n" \
"buffer_load_dwordx4 v22, s[64:67], s84 offen offset:0 lds\n" \
"v_mfma_f32_16x16x32_bf16 v[56:59], a[180:183], a[128:131], v[56:59]\n" \
"ds_read_b64_tr_b16 a[160:161], v32 offset:32768\n" \
"v_mfma_f32_16x16x32_bf16 v[56:59], a[244:247], a[192:195], v[56:59]\n" \
"s_movk_i32 s84, 0x1e00\n" \
"s_nop 0\n" \
"v_mfma_f32_16x16x32_bf16 v[60:63], a[184:187], a[128:131], v[60:63]\n" \
"buffer_load_dwordx4 v23, s[64:67], s84 offen offset:1024 lds\n" \
"v_mfma_f32_16x16x32_bf16 v[60:63], a[248:251], a[192:195], v[60:63]\n" \
"ds_read_b64_tr_b16 a[162:163], v32 offset:33792\n" \
"v_mfma_f32_16x16x32_bf16 v[64:67], a[188:191], a[128:131], v[64:67]\n" \
"ds_read_b64_tr_b16 a[224:225], v32 offset:40960\n" \
"v_mfma_f32_16x16x32_bf16 v[64:67], a[252:255], a[192:195], v[64:67]\n" \
"ds_read_b128 a[128:131], v28 offset:49152\n" \
"v_mfma_f32_16x16x32_bf16 v[80:83], a[188:191], a[132:135], v[80:83]\n" \
"ds_read_b128 a[192:195], v29 offset:49152\n" \
"v_mfma_f32_16x16x32_bf16 v[80:83], a[252:255], a[196:199], v[80:83]\n" \
"s_movk_i32 s84, 0x3a00\n" \
"v_mfma_f32_16x16x32_bf16 v[76:79], a[184:187], a[132:135], v[76:79]\n" \
"buffer_load_dwordx4 v22, s[64:67], s84 offen offset:2048 lds\n" \
"v_mfma_f32_16x16x32_bf16 v[76:79], a[248:251], a[196:199], v[76:79]\n" \
"s_movk_i32 s84, 0x5600\n" \
"v_mfma_f32_16x16x32_bf16 v[72:75], a[180:183], a[132:135], v[72:75]\n" \
"buffer_load_dwordx4 v23, s[64:67], s84 offen offset:3072 lds\n" \
"v_mfma_f32_16x16x32_bf16 v[72:75], a[244:247], a[196:199], v[72:75]\n" \
"s_add_u32 m0, s31, 0x0\n" \
"s_mov_b32 s84, 0x200000\n" \
"v_mfma_f32_16x16x32_bf16 v[68:71], a[176:179], a[132:135], v[68:71]\n" \
"buffer_load_dwordx4 v26, s[68:71], s84 offen offset:0 lds\n" \
"v_mfma_f32_16x16x32_bf16 v[68:71], a[240:243], a[196:199], v[68:71]\n" \
"ds_read_b128 a[132:135], v28 offset:51200\n" \
"v_mfma_f32_16x16x32_bf16 v[84:87], a[176:179], a[136:139], v[84:87]\n" \
"ds_read_b128 a[196:199], v29 offset:51200\n" \
"v_mfma_f32_16x16x32_bf16 v[84:87], a[240:243], a[200:203], v[84:87]\n" \
"s_addk_i32 s84, 0x7c00\n" \
"v_mfma_f32_16x16x32_bf16 v[88:91], a[180:183], a[136:139], v[88:91]\n" \
"buffer_load_dwordx4 v26, s[68:71], s84 offen offset:1024 lds\n" \
"v_mfma_f32_16x16x32_bf16 v[88:91], a[244:247], a[200:203], v[88:91]\n" \
"ds_read_b64_tr_b16 a[226:227], v32 offset:41984\n" \
"v_mfma_f32_16x16x32_bf16 v[92:95], a[184:187], a[136:139], v[92:95]\n" \
"s_addk_i32 s84, 0x7c00\n" \
"v_mfma_f32_16x16x32_bf16 v[92:95], a[248:251], a[200:203], v[92:95]\n" \
"buffer_load_dwordx4 v27, s[68:71], s84 offen offset:2048 lds\n" \
"v_mfma_f32_16x16x32_bf16 v[96:99], a[188:191], a[136:139], v[96:99]\n" \
"ds_read_b64_tr_b16 a[164:165], v33 offset:32768\n" \
"v_mfma_f32_16x16x32_bf16 v[96:99], a[252:255], a[200:203], v[96:99]\n" \
"ds_read_b128 a[136:139], v28 offset:53248\n" \
"v_mfma_f32_16x16x32_bf16 v[112:115], a[188:191], a[140:143], v[112:115]\n" \
"ds_read_b128 a[200:203], v29 offset:53248\n" \
"v_mfma_f32_16x16x32_bf16 v[112:115], a[252:255], a[204:207], v[112:115]\n" \
"s_addk_i32 s84, 0x7c00\n" \
"v_mfma_f32_16x16x32_bf16 v[108:111], a[184:187], a[140:143], v[108:111]\n" \
"buffer_load_dwordx4 v27, s[68:71], s84 offen offset:3072 lds\n" \
"v_mfma_f32_16x16x32_bf16 v[108:111], a[248:251], a[204:207], v[108:111]\n" \
"s_add_u32 m0, s30, 0x1000\n" \
"s_mov_b32 s84, 0x8200\n" \
"v_mfma_f32_16x16x32_bf16 v[104:107], a[180:183], a[140:143], v[104:107]\n" \
"buffer_load_dwordx4 v22, s[64:67], s84 offen offset:0 lds\n" \
"v_mfma_f32_16x16x32_bf16 v[104:107], a[244:247], a[204:207], v[104:107]\n" \
"ds_read_b64_tr_b16 a[166:167], v33 offset:33792\n" \
"v_mfma_f32_16x16x32_bf16 v[100:103], a[176:179], a[140:143], v[100:103]\n" \
"ds_read_b64_tr_b16 a[228:229], v33 offset:40960\n" \
"v_mfma_f32_16x16x32_bf16 v[100:103], a[240:243], a[204:207], v[100:103]\n" \
"ds_read_b128 a[140:143], v28 offset:55296\n" \
"v_mfma_f32_16x16x32_bf16 v[116:119], a[176:179], a[144:147], v[116:119]\n" \
"ds_read_b128 a[204:207], v29 offset:55296\n" \
"v_mfma_f32_16x16x32_bf16 v[116:119], a[240:243], a[208:211], v[116:119]\n" \
"s_addk_i32 s84, 0x1c00\n" \
"v_mfma_f32_16x16x32_bf16 v[120:123], a[180:183], a[144:147], v[120:123]\n" \
"buffer_load_dwordx4 v23, s[64:67], s84 offen offset:1024 lds\n" \
"v_mfma_f32_16x16x32_bf16 v[120:123], a[244:247], a[208:211], v[120:123]\n" \
"s_addk_i32 s84, 0x1c00\n" \
"v_mfma_f32_16x16x32_bf16 v[124:127], a[184:187], a[144:147], v[124:127]\n" \
"buffer_load_dwordx4 v22, s[64:67], s84 offen offset:2048 lds\n" \
"v_mfma_f32_16x16x32_bf16 v[124:127], a[248:251], a[208:211], v[124:127]\n" \
"s_addk_i32 s84, 0x1c00\n" \
"s_nop 0\n" \
"v_mfma_f32_16x16x32_bf16 v[128:131], a[188:191], a[144:147], v[128:131]\n" \
"buffer_load_dwordx4 v23, s[64:67], s84 offen offset:3072 lds\n" \
"v_mfma_f32_16x16x32_bf16 v[128:131], a[252:255], a[208:211], v[128:131]\n" \
"ds_read_b128 a[144:147], v28 offset:32768\n" \
"v_mfma_f32_16x16x32_bf16 v[144:147], a[188:191], a[148:151], v[144:147]\n" \
"ds_read_b128 a[208:211], v29 offset:32768\n" \
"v_mfma_f32_16x16x32_bf16 v[144:147], a[252:255], a[212:215], v[144:147]\n" \
"s_add_u32 m0, s31, 0x4000\n" \
"s_mov_b32 s84, 0x200000\n" \
"v_mfma_f32_16x16x32_bf16 v[140:143], a[184:187], a[148:151], v[140:143]\n" \
"buffer_load_dwordx4 v26, s[72:75], s84 offen offset:0 lds\n" \
"v_mfma_f32_16x16x32_bf16 v[140:143], a[248:251], a[212:215], v[140:143]\n" \
"ds_read_b64_tr_b16 a[230:231], v33 offset:41984\n" \
"v_mfma_f32_16x16x32_bf16 v[136:139], a[180:183], a[148:151], v[136:139]\n" \
"s_addk_i32 s84, 0x7c00\n" \
"s_nop 0\n" \
"v_mfma_f32_16x16x32_bf16 v[136:139], a[244:247], a[212:215], v[136:139]\n" \
"buffer_load_dwordx4 v26, s[72:75], s84 offen offset:1024 lds\n" \
"v_mfma_f32_16x16x32_bf16 v[132:135], a[176:179], a[148:151], v[132:135]\n" \
"ds_read_b64_tr_b16 a[168:169], v34 offset:32768\n" \
"v_mfma_f32_16x16x32_bf16 v[132:135], a[240:243], a[212:215], v[132:135]\n" \
"ds_read_b128 a[148:151], v28 offset:34816\n" \
"v_mfma_f32_16x16x32_bf16 v[148:151], a[176:179], a[152:155], v[148:151]\n" \
"ds_read_b128 a[212:215], v29 offset:34816\n" \
"v_mfma_f32_16x16x32_bf16 v[148:151], a[240:243], a[216:219], v[148:151]\n" \
"ds_read_b64_tr_b16 a[170:171], v34 offset:33792\n" \
"v_mfma_f32_16x16x32_bf16 v[152:155], a[180:183], a[152:155], v[152:155]\n" \
"ds_read_b64_tr_b16 a[232:233], v34 offset:40960\n" \
"v_mfma_f32_16x16x32_bf16 v[152:155], a[244:247], a[216:219], v[152:155]\n" \
"ds_read_b64_tr_b16 a[234:235], v34 offset:41984\n" \
"v_mfma_f32_16x16x32_bf16 v[156:159], a[184:187], a[152:155], v[156:159]\n" \
"ds_read_b64_tr_b16 a[172:173], v35 offset:32768\n" \
"v_mfma_f32_16x16x32_bf16 v[156:159], a[248:251], a[216:219], v[156:159]\n" \
"ds_read_b64_tr_b16 a[174:175], v35 offset:33792\n" \
"v_mfma_f32_16x16x32_bf16 v[160:163], a[188:191], a[152:155], v[160:163]\n" \
"ds_read_b64_tr_b16 a[236:237], v35 offset:40960\n" \
"v_mfma_f32_16x16x32_bf16 v[160:163], a[252:255], a[216:219], v[160:163]\n" \
"ds_read_b128 a[152:155], v28 offset:36864\n" \
"v_mfma_f32_16x16x32_bf16 v[176:179], a[188:191], a[156:159], v[176:179]\n" \
"ds_read_b128 a[216:219], v29 offset:36864\n" \
"v_mfma_f32_16x16x32_bf16 v[176:179], a[252:255], a[220:223], v[176:179]\n" \
"ds_read_b64_tr_b16 a[238:239], v35 offset:41984\n" \
"v_mfma_f32_16x16x32_bf16 v[172:175], a[184:187], a[156:159], v[172:175]\n" \
"s_cmp_lg_u32 s4, 0\n" \
"s_cselect_b32 s53, s41, s40\n" \
"v_mfma_f32_16x16x32_bf16 v[172:175], a[248:251], a[220:223], v[172:175]\n" \
"s_lshl_b32 s40, s2, 2\n" \
"s_and_b32 s40, s40, 28\n" \
"v_mfma_f32_16x16x32_bf16 v[168:171], a[180:183], a[156:159], v[168:171]\n" \
"s_bfe_u32 s45, s2, 0x20003\n" \
"s_lshr_b32 s44, s3, 1\n" \
"v_mfma_f32_16x16x32_bf16 v[168:171], a[244:247], a[220:223], v[168:171]\n" \
"s_or_b32 s46, s40, s45\n" \
"s_lshl_b32 s43, s44, 15\n" \
"v_mfma_f32_16x16x32_bf16 v[164:167], a[176:179], a[156:159], v[164:167]\n" \
"s_lshl_b32 s41, s5, 14\n" \
"s_lshl_b32 s48, s46, 20\n" \
"v_mfma_f32_16x16x32_bf16 v[164:167], a[240:243], a[220:223], v[164:167]\n" \
"ds_read_b128 a[156:159], v28 offset:38912\n" \
"ds_read_b128 a[220:223], v29 offset:38912\n" \
"s_waitcnt lgkmcnt(15)\n" \
"v_mfma_f32_16x16x32_bf16 a[0:3], a[160:163], a[128:131], a[0:3]\n" \
"s_addk_i32 s84, 0x7c00\n" \
"v_mfma_f32_16x16x32_bf16 a[0:3], a[224:227], a[192:195], a[0:3]\n" \
"buffer_load_dwordx4 v27, s[72:75], s84 offen offset:2048 lds\n" \
"v_mfma_f32_16x16x32_bf16 a[4:7], a[164:167], a[128:131], a[4:7]\n" \
"ds_read_b64_tr_b16 a[176:177], v36 offset:32768\n" \
"s_or_b32 s41, s43, s41\n" \
"s_waitcnt lgkmcnt(15)\n" \
"v_mfma_f32_16x16x32_bf16 a[4:7], a[228:231], a[192:195], a[4:7]\n" \
"s_addk_i32 s84, 0x7c00\n" \
"s_waitcnt lgkmcnt(11)\n" \
"v_mfma_f32_16x16x32_bf16 a[8:11], a[168:171], a[128:131], a[8:11]\n" \
"buffer_load_dwordx4 v27, s[72:75], s84 offen offset:3072 lds\n" \
"s_waitcnt lgkmcnt(9)\n" \
"v_mfma_f32_16x16x32_bf16 a[8:11], a[232:235], a[192:195], a[8:11]\n" \
"ds_read_b64_tr_b16 a[178:179], v36 offset:33792\n" \
"s_add_i32 s49, s48, 0x1000000\n" \
"s_waitcnt lgkmcnt(8)\n" \
"v_mfma_f32_16x16x32_bf16 a[12:15], a[172:175], a[128:131], a[12:15]\n" \
"ds_read_b64_tr_b16 a[240:241], v36 offset:40960\n" \
"s_or_b32 s42, s49, s41\n" \
"s_waitcnt lgkmcnt(5)\n" \
"v_mfma_f32_16x16x32_bf16 a[12:15], a[236:239], a[192:195], a[12:15]\n" \
"s_lshl_b32 s4, s4, 16\n" \
"s_or_b32 s41, s42, s4\n" \
"v_mfma_f32_16x16x32_bf16 a[28:31], a[172:175], a[132:135], a[28:31]\n" \
"s_lshl_b32 s4, s6, 16\n" \
"s_or_b32 s42, s42, s4\n" \
"v_mfma_f32_16x16x32_bf16 a[28:31], a[236:239], a[196:199], a[28:31]\n" \
"ds_read_b64_tr_b16 a[242:243], v36 offset:41984\n" \
"s_lshl_b32 s4, s5, 11\n" \
"v_mfma_f32_16x16x32_bf16 a[24:27], a[168:171], a[132:135], a[24:27]\n" \
"s_mul_i32 s5, s7, 0x24800\n" \
"s_lshl_b32 s6, s46, 9\n" \
"v_mfma_f32_16x16x32_bf16 a[24:27], a[232:235], a[196:199], a[24:27]\n" \
"s_lshl_b32 s7, s40, 6\n" \
"s_add_i32 s6, s5, s6\n" \
"v_mfma_f32_16x16x32_bf16 a[20:23], a[164:167], a[132:135], a[20:23]\n" \
"s_or_b32 s5, s5, s7\n" \
"s_lshl_b32 s7, s45, 2\n" \
"v_mfma_f32_16x16x32_bf16 a[20:23], a[228:231], a[196:199], a[20:23]\n" \
"ds_read_b64_tr_b16 a[180:181], v37 offset:32768\n" \
"s_or_b32 s5, s5, s7\n" \
"v_mfma_f32_16x16x32_bf16 a[16:19], a[160:163], a[132:135], a[16:19]\n" \
"s_lshl_b32 s7, s3, 12\n" \
"s_or_b32 s50, s49, s7\n" \
"v_mfma_f32_16x16x32_bf16 a[16:19], a[224:227], a[196:199], a[16:19]\n" \
"s_lshl_b32 s7, s3, 11\n" \
"v_lshrrev_b32_e32 v215, 4, v20\n" \
"v_mfma_f32_16x16x32_bf16 a[32:35], a[160:163], a[136:139], a[32:35]\n" \
"s_waitcnt vmcnt(16)\n" \
"v_or_b32_e32 v204, v210, v204\n" \
"v_or_b32_e64 v206, v210, v206\n" \
"v_mfma_f32_16x16x32_bf16 a[32:35], a[224:227], a[200:203], a[32:35]\n" \
"v_mov_b32_e64 v210, 0\n" \
"s_add_u32 s88, s35, 0x1000\n" \
"v_mfma_f32_16x16x32_bf16 a[36:39], a[164:167], a[136:139], a[36:39]\n" \
"ds_read_b64_tr_b16 a[182:183], v37 offset:33792\n" \
"v_bfe_u32 v216, v20, 4, 3\n" \
"v_mfma_f32_16x16x32_bf16 a[36:39], a[228:231], a[200:203], a[36:39]\n" \
"ds_read_b64_tr_b16 a[244:245], v37 offset:40960\n" \
"s_add_i32 s52, s48, s7\n" \
"v_mfma_f32_16x16x32_bf16 a[40:43], a[168:171], a[136:139], a[40:43]\n" \
"s_mov_b32 m0, s32\n" \
"s_add_i32 s53, s53, s7\n" \
"v_mfma_f32_16x16x32_bf16 a[40:43], a[232:235], a[200:203], a[40:43]\n" \
"s_mov_b32 exec_lo, 0xffffffff\n" \
"s_mov_b32 exec_hi, 0x0\n" \
"global_load_lds_dword v[188:189], off\n" \
"s_mov_b64 exec, -1\n" \
"v_mfma_f32_16x16x32_bf16 a[44:47], a[172:175], a[136:139], a[44:47]\n" \
"ds_read_b64_tr_b16 a[246:247], v37 offset:41984\n" \
"v_mul_u32_u24_e32 v214, 0x480, v214\n" \
"v_mfma_f32_16x16x32_bf16 a[44:47], a[236:239], a[200:203], a[44:47]\n" \
"v_bitop3_b32 v218, v213, v215, 7 bitop3:0x78\n" \
"v_bitop3_b32 v213, v213, v216, 4 bitop3:0x36\n" \
"v_mfma_f32_16x16x32_bf16 a[60:63], a[172:175], a[140:143], a[60:63]\n" \
"ds_write_b128 v196, v[200:203]\n" \
"v_mfma_f32_16x16x32_bf16 a[60:63], a[236:239], a[204:207], a[60:63]\n" \
"s_lshl_b32 s7, s3, 10\n" \
"s_or_b32 s4, s43, s4\n" \
"v_mfma_f32_16x16x32_bf16 a[56:59], a[168:171], a[140:143], a[56:59]\n" \
"ds_read_b64_tr_b16 a[184:185], v38 offset:32768\n" \
"v_lshl_or_b32 v25, v213, 4, v214\n" \
"v_mfma_f32_16x16x32_bf16 a[56:59], a[232:235], a[204:207], a[56:59]\n" \
"v_mul_u32_u24_e32 v213, 0x480, v217\n" \
"v_bitop3_b32 v212, v215, v212, 7 bitop3:0x6c\n" \
"v_mfma_f32_16x16x32_bf16 a[52:55], a[164:167], a[140:143], a[52:55]\n" \
"s_or_b32 s40, s7, 0x14000\n" \
"s_lshl_b32 s43, s44, 13\n" \
"v_mfma_f32_16x16x32_bf16 a[52:55], a[228:231], a[204:207], a[52:55]\n" \
"ds_read_b64_tr_b16 a[186:187], v38 offset:33792\n" \
"s_mulk_i32 s44, 0xe800\n" \
"v_mfma_f32_16x16x32_bf16 a[48:51], a[160:163], a[140:143], a[48:51]\n" \
"s_lshl_b32 s7, s3, 8\n" \
"s_nop 0\n" \
"v_lshl_or_b32 v24, v218, 4, v214\n" \
"v_mfma_f32_16x16x32_bf16 a[48:51], a[224:227], a[204:207], a[48:51]\n" \
"v_lshl_or_b32 v21, v212, 4, v213\n" \
"s_add_i32 s44, s44, 0x17000\n" \
"v_mfma_f32_16x16x32_bf16 a[64:67], a[160:163], a[144:147], a[64:67]\n" \
"ds_write_b128 v197, v[204:207]\n" \
"v_mfma_f32_16x16x32_bf16 a[64:67], a[224:227], a[208:211], a[64:67]\n" \
"ds_read_b64_tr_b16 a[248:249], v38 offset:40960\n" \
"s_or_b32 s45, s7, 0x27000\n" \
"v_mfma_f32_16x16x32_bf16 a[68:71], a[164:167], a[144:147], a[68:71]\n" \
"s_or_b32 s46, s4, 0x10000\n" \
"s_add_i32 s48, s6, 0x3000000\n" \
"v_mfma_f32_16x16x32_bf16 a[68:71], a[228:231], a[208:211], a[68:71]\n" \
"ds_read_b64_tr_b16 a[250:251], v38 offset:41984\n" \
"s_add_i32 s49, s5, 0x3004000\n" \
"v_mfma_f32_16x16x32_bf16 a[72:75], a[168:171], a[144:147], a[72:75]\n" \
"s_add_i32 s52, s52, 0x1010000\n" \
"v_mfma_f32_16x16x32_bf16 a[72:75], a[232:235], a[208:211], a[72:75]\n" \
"v_mfma_f32_16x16x32_bf16 a[76:79], a[172:175], a[144:147], a[76:79]\n" \
"v_mfma_f32_16x16x32_bf16 a[76:79], a[236:239], a[208:211], a[76:79]\n" \
"ds_read_b64_tr_b16 a[188:189], v39 offset:32768\n" \
"v_mfma_f32_16x16x32_bf16 a[92:95], a[172:175], a[148:151], a[92:95]\n" \
"ds_write_b128 v198, v[208:211]\n" \
"v_mfma_f32_16x16x32_bf16 a[92:95], a[236:239], a[212:215], a[92:95]\n" \
"v_mfma_f32_16x16x32_bf16 a[88:91], a[168:171], a[148:151], a[88:91]\n" \
"ds_read_b64_tr_b16 a[190:191], v39 offset:33792\n" \
"v_mfma_f32_16x16x32_bf16 a[88:91], a[232:235], a[212:215], a[88:91]\n" \
"v_mfma_f32_16x16x32_bf16 a[84:87], a[164:167], a[148:151], a[84:87]\n" \
"v_mfma_f32_16x16x32_bf16 a[84:87], a[228:231], a[212:215], a[84:87]\n" \
"ds_read_b64_tr_b16 a[252:253], v39 offset:40960\n" \
"v_mfma_f32_16x16x32_bf16 a[80:83], a[160:163], a[148:151], a[80:83]\n" \
"v_mfma_f32_16x16x32_bf16 a[80:83], a[224:227], a[212:215], a[80:83]\n" \
"v_mfma_f32_16x16x32_bf16 a[96:99], a[160:163], a[152:155], a[96:99]\n" \
"ds_read_b64_tr_b16 a[254:255], v39 offset:41984\n" \
"v_mfma_f32_16x16x32_bf16 a[96:99], a[224:227], a[216:219], a[96:99]\n" \
"v_mfma_f32_16x16x32_bf16 a[100:103], a[164:167], a[152:155], a[100:103]\n" \
"v_mfma_f32_16x16x32_bf16 a[100:103], a[228:231], a[216:219], a[100:103]\n" \
"v_mfma_f32_16x16x32_bf16 a[104:107], a[168:171], a[152:155], a[104:107]\n" \
"v_mfma_f32_16x16x32_bf16 a[104:107], a[232:235], a[216:219], a[104:107]\n" \
"v_mfma_f32_16x16x32_bf16 a[108:111], a[172:175], a[152:155], a[108:111]\n" \
"v_mfma_f32_16x16x32_bf16 a[108:111], a[236:239], a[216:219], a[108:111]\n" \
"s_waitcnt lgkmcnt(15)\n" \
"v_mfma_f32_16x16x32_bf16 a[124:127], a[172:175], a[156:159], a[124:127]\n" \
"v_mfma_f32_16x16x32_bf16 a[124:127], a[236:239], a[220:223], a[124:127]\n" \
"v_mfma_f32_16x16x32_bf16 a[120:123], a[168:171], a[156:159], a[120:123]\n" \
"v_mfma_f32_16x16x32_bf16 a[120:123], a[232:235], a[220:223], a[120:123]\n" \
"v_mfma_f32_16x16x32_bf16 a[116:119], a[164:167], a[156:159], a[116:119]\n" \
"v_mfma_f32_16x16x32_bf16 a[116:119], a[228:231], a[220:223], a[116:119]\n" \
"v_mfma_f32_16x16x32_bf16 a[112:115], a[160:163], a[156:159], a[112:115]\n" \
"v_mfma_f32_16x16x32_bf16 a[112:115], a[224:227], a[220:223], a[112:115]\n" \
"s_waitcnt vmcnt(1) lgkmcnt(0)\n" \
"s_barrier\n" \
"v_mfma_f32_16x16x32_bf16 v[52:55], a[176:179], a[128:131], v[52:55]\n" \
"s_add_u32 m0, s30, 0x8000\n" \
"s_movk_i32 s84, 0x280\n" \
"v_mfma_f32_16x16x32_bf16 v[52:55], a[240:243], a[192:195], v[52:55]\n" \
"buffer_load_dwordx4 v22, s[64:67], s84 offen offset:0 lds\n" \
"v_mfma_f32_16x16x32_bf16 v[56:59], a[180:183], a[128:131], v[56:59]\n" \
"s_movk_i32 s84, 0x1e80\n" \
"v_mfma_f32_16x16x32_bf16 v[56:59], a[244:247], a[192:195], v[56:59]\n" \
"buffer_load_dwordx4 v23, s[64:67], s84 offen offset:1024 lds\n" \
"v_mfma_f32_16x16x32_bf16 v[60:63], a[184:187], a[128:131], v[60:63]\n" \
"ds_read_b64_tr_b16 a[160:161], v32 offset:0\n" \
"v_mfma_f32_16x16x32_bf16 v[60:63], a[248:251], a[192:195], v[60:63]\n" \
"s_movk_i32 s84, 0x3a80\n" \
"v_mfma_f32_16x16x32_bf16 v[64:67], a[188:191], a[128:131], v[64:67]\n" \
"buffer_load_dwordx4 v22, s[64:67], s84 offen offset:2048 lds\n" \
"v_mfma_f32_16x16x32_bf16 v[64:67], a[252:255], a[192:195], v[64:67]\n" \
"ds_read_b128 a[128:131], v28 offset:16384\n" \
"v_mfma_f32_16x16x32_bf16 v[80:83], a[188:191], a[132:135], v[80:83]\n" \
"ds_read_b128 a[192:195], v29 offset:16384\n" \
"v_mfma_f32_16x16x32_bf16 v[80:83], a[252:255], a[196:199], v[80:83]\n" \
"s_movk_i32 s84, 0x5680\n" \
"s_nop 0\n" \
"v_mfma_f32_16x16x32_bf16 v[76:79], a[184:187], a[132:135], v[76:79]\n" \
"buffer_load_dwordx4 v23, s[64:67], s84 offen offset:3072 lds\n" \
"v_mfma_f32_16x16x32_bf16 v[76:79], a[248:251], a[196:199], v[76:79]\n" \
"s_add_u32 m0, s31, 0x8000\n" \
"s_mov_b32 s84, 0x280000\n" \
"v_mfma_f32_16x16x32_bf16 v[72:75], a[180:183], a[132:135], v[72:75]\n" \
"buffer_load_dwordx4 v26, s[68:71], s84 offen offset:0 lds\n" \
"v_mfma_f32_16x16x32_bf16 v[72:75], a[244:247], a[196:199], v[72:75]\n" \
"ds_read_b64_tr_b16 a[162:163], v32 offset:1024\n" \
"v_mfma_f32_16x16x32_bf16 v[68:71], a[176:179], a[132:135], v[68:71]\n" \
"ds_read_b64_tr_b16 a[224:225], v32 offset:8192\n" \
"v_mfma_f32_16x16x32_bf16 v[68:71], a[240:243], a[196:199], v[68:71]\n" \
"ds_read_b128 a[132:135], v28 offset:18432\n" \
"v_mfma_f32_16x16x32_bf16 v[84:87], a[176:179], a[136:139], v[84:87]\n" \
"ds_read_b128 a[196:199], v29 offset:18432\n" \
"v_mfma_f32_16x16x32_bf16 v[84:87], a[240:243], a[200:203], v[84:87]\n" \
"s_addk_i32 s84, 0x7c00\n" \
"v_mfma_f32_16x16x32_bf16 v[88:91], a[180:183], a[136:139], v[88:91]\n" \
"buffer_load_dwordx4 v26, s[68:71], s84 offen offset:1024 lds\n" \
"v_mfma_f32_16x16x32_bf16 v[88:91], a[244:247], a[200:203], v[88:91]\n" \
"s_addk_i32 s84, 0x7c00\n" \
"v_mfma_f32_16x16x32_bf16 v[92:95], a[184:187], a[136:139], v[92:95]\n" \
"buffer_load_dwordx4 v27, s[68:71], s84 offen offset:2048 lds\n" \
"v_mfma_f32_16x16x32_bf16 v[92:95], a[248:251], a[200:203], v[92:95]\n" \
"s_addk_i32 s84, 0x7c00\n" \
"s_nop 0\n" \
"v_mfma_f32_16x16x32_bf16 v[96:99], a[188:191], a[136:139], v[96:99]\n" \
"buffer_load_dwordx4 v27, s[68:71], s84 offen offset:3072 lds\n" \
"v_mfma_f32_16x16x32_bf16 v[96:99], a[252:255], a[200:203], v[96:99]\n" \
"ds_read_b128 a[136:139], v28 offset:20480\n" \
"v_mfma_f32_16x16x32_bf16 v[112:115], a[188:191], a[140:143], v[112:115]\n" \
"ds_read_b128 a[200:203], v29 offset:20480\n" \
"v_mfma_f32_16x16x32_bf16 v[112:115], a[252:255], a[204:207], v[112:115]\n" \
"s_add_u32 m0, s30, 0x9000\n" \
"s_mov_b32 s84, 0x8280\n" \
"v_mfma_f32_16x16x32_bf16 v[108:111], a[184:187], a[140:143], v[108:111]\n" \
"buffer_load_dwordx4 v22, s[64:67], s84 offen offset:0 lds\n" \
"v_mfma_f32_16x16x32_bf16 v[108:111], a[248:251], a[204:207], v[108:111]\n" \
"s_addk_i32 s84, 0x1c00\n" \
"v_mfma_f32_16x16x32_bf16 v[104:107], a[180:183], a[140:143], v[104:107]\n" \
"buffer_load_dwordx4 v23, s[64:67], s84 offen offset:1024 lds\n" \
"v_mfma_f32_16x16x32_bf16 v[104:107], a[244:247], a[204:207], v[104:107]\n" \
"s_addk_i32 s84, 0x1c00\n" \
"v_mfma_f32_16x16x32_bf16 v[100:103], a[176:179], a[140:143], v[100:103]\n" \
"buffer_load_dwordx4 v22, s[64:67], s84 offen offset:2048 lds\n" \
"v_mfma_f32_16x16x32_bf16 v[100:103], a[240:243], a[204:207], v[100:103]\n" \
"ds_read_b128 a[140:143], v28 offset:22528\n" \
"v_mfma_f32_16x16x32_bf16 v[116:119], a[176:179], a[144:147], v[116:119]\n" \
"ds_read_b128 a[204:207], v29 offset:22528\n" \
"v_mfma_f32_16x16x32_bf16 v[116:119], a[240:243], a[208:211], v[116:119]\n" \
"s_addk_i32 s84, 0x1c00\n" \
"v_mfma_f32_16x16x32_bf16 v[120:123], a[180:183], a[144:147], v[120:123]\n" \
"buffer_load_dwordx4 v23, s[64:67], s84 offen offset:3072 lds\n" \
"v_mfma_f32_16x16x32_bf16 v[120:123], a[244:247], a[208:211], v[120:123]\n" \
"s_add_u32 m0, s31, 0xc000\n" \
"s_mov_b32 s84, 0x280000\n" \
"v_mfma_f32_16x16x32_bf16 v[124:127], a[184:187], a[144:147], v[124:127]\n" \
"buffer_load_dwordx4 v26, s[72:75], s84 offen offset:0 lds\n" \
"v_mfma_f32_16x16x32_bf16 v[124:127], a[248:251], a[208:211], v[124:127]\n" \
"s_addk_i32 s84, 0x7c00\n" \
"v_mfma_f32_16x16x32_bf16 v[128:131], a[188:191], a[144:147], v[128:131]\n" \
"buffer_load_dwordx4 v26, s[72:75], s84 offen offset:1024 lds\n" \
"v_mfma_f32_16x16x32_bf16 v[128:131], a[252:255], a[208:211], v[128:131]\n" \
"ds_read_b128 a[144:147], v28 offset:0\n" \
"v_mfma_f32_16x16x32_bf16 v[144:147], a[188:191], a[148:151], v[144:147]\n" \
"ds_read_b128 a[208:211], v29 offset:0\n" \
"v_mfma_f32_16x16x32_bf16 v[144:147], a[252:255], a[212:215], v[144:147]\n" \
"ds_read_b128 v[200:203], v199 offset:0\n" \
"v_mfma_f32_16x16x32_bf16 v[140:143], a[184:187], a[148:151], v[140:143]\n" \
"ds_read_b128 v[204:207], v199 offset:4096\n" \
"v_mfma_f32_16x16x32_bf16 v[140:143], a[248:251], a[212:215], v[140:143]\n" \
"ds_read_b64_tr_b16 a[226:227], v32 offset:9216\n" \
"v_mfma_f32_16x16x32_bf16 v[136:139], a[180:183], a[148:151], v[136:139]\n" \
"ds_read_b64_tr_b16 a[164:165], v33 offset:0\n" \
"v_mfma_f32_16x16x32_bf16 v[136:139], a[244:247], a[212:215], v[136:139]\n" \
"ds_read_b128 v[208:211], v199 offset:8192\n" \
"v_mfma_f32_16x16x32_bf16 v[132:135], a[176:179], a[148:151], v[132:135]\n" \
"ds_read_b64_tr_b16 a[166:167], v33 offset:1024\n" \
"v_mfma_f32_16x16x32_bf16 v[132:135], a[240:243], a[212:215], v[132:135]\n" \
"ds_read_b128 a[148:151], v28 offset:2048\n" \
"v_mfma_f32_16x16x32_bf16 v[148:151], a[176:179], a[152:155], v[148:151]\n" \
"ds_read_b128 a[212:215], v29 offset:2048\n" \
"v_mfma_f32_16x16x32_bf16 v[148:151], a[240:243], a[216:219], v[148:151]\n" \
"ds_read_b64_tr_b16 a[228:229], v33 offset:8192\n" \
"v_mfma_f32_16x16x32_bf16 v[152:155], a[180:183], a[152:155], v[152:155]\n" \
"ds_read_b64_tr_b16 a[230:231], v33 offset:9216\n" \
"v_mfma_f32_16x16x32_bf16 v[152:155], a[244:247], a[216:219], v[152:155]\n" \
"ds_read_b64_tr_b16 a[168:169], v34 offset:0\n" \
"v_mfma_f32_16x16x32_bf16 v[156:159], a[184:187], a[152:155], v[156:159]\n" \
"ds_read_b64_tr_b16 a[170:171], v34 offset:1024\n" \
"v_mfma_f32_16x16x32_bf16 v[156:159], a[248:251], a[216:219], v[156:159]\n" \
"ds_read_b64_tr_b16 a[232:233], v34 offset:8192\n" \
"v_mfma_f32_16x16x32_bf16 v[160:163], a[188:191], a[152:155], v[160:163]\n" \
"ds_read_b64_tr_b16 a[234:235], v34 offset:9216\n" \
"v_mfma_f32_16x16x32_bf16 v[160:163], a[252:255], a[216:219], v[160:163]\n" \
"ds_read_b128 a[152:155], v28 offset:4096\n" \
"v_mfma_f32_16x16x32_bf16 v[176:179], a[188:191], a[156:159], v[176:179]\n" \
"ds_read_b128 a[216:219], v29 offset:4096\n" \
"v_mfma_f32_16x16x32_bf16 v[176:179], a[252:255], a[220:223], v[176:179]\n" \
"ds_read_b64_tr_b16 a[172:173], v35 offset:0\n" \
"v_mfma_f32_16x16x32_bf16 v[172:175], a[184:187], a[156:159], v[172:175]\n" \
"ds_read_b64_tr_b16 a[174:175], v35 offset:1024\n" \
"v_mfma_f32_16x16x32_bf16 v[172:175], a[248:251], a[220:223], v[172:175]\n" \
"ds_read_b64_tr_b16 a[236:237], v35 offset:8192\n" \
"v_mfma_f32_16x16x32_bf16 v[168:171], a[180:183], a[156:159], v[168:171]\n" \
"ds_read_b64_tr_b16 a[238:239], v35 offset:9216\n" \
"v_mfma_f32_16x16x32_bf16 v[168:171], a[244:247], a[220:223], v[168:171]\n" \
"v_mfma_f32_16x16x32_bf16 v[164:167], a[176:179], a[156:159], v[164:167]\n" \
"v_mfma_f32_16x16x32_bf16 v[164:167], a[240:243], a[220:223], v[164:167]\n" \
"ds_read_b128 a[156:159], v28 offset:6144\n" \
"ds_read_b128 a[220:223], v29 offset:6144\n" \
"s_waitcnt lgkmcnt(15)\n" \
"v_mfma_f32_16x16x32_bf16 a[0:3], a[160:163], a[128:131], a[0:3]\n" \
"s_addk_i32 s84, 0x7c00\n" \
"v_mfma_f32_16x16x32_bf16 a[0:3], a[224:227], a[192:195], a[0:3]\n" \
"buffer_load_dwordx4 v27, s[72:75], s84 offen offset:2048 lds\n" \
"v_mfma_f32_16x16x32_bf16 a[4:7], a[164:167], a[128:131], a[4:7]\n" \
"ds_read_b64_tr_b16 a[176:177], v36 offset:0\n" \
"s_waitcnt lgkmcnt(13)\n" \
"v_mfma_f32_16x16x32_bf16 a[4:7], a[228:231], a[192:195], a[4:7]\n" \
"s_addk_i32 s84, 0x7c00\n" \
"s_waitcnt lgkmcnt(11)\n" \
"v_mfma_f32_16x16x32_bf16 a[8:11], a[168:171], a[128:131], a[8:11]\n" \
"buffer_load_dwordx4 v27, s[72:75], s84 offen offset:3072 lds\n" \
"s_waitcnt lgkmcnt(9)\n" \
"v_mfma_f32_16x16x32_bf16 a[8:11], a[232:235], a[192:195], a[8:11]\n" \
"ds_read_b64_tr_b16 a[178:179], v36 offset:1024\n" \
"s_waitcnt lgkmcnt(6)\n" \
"v_mfma_f32_16x16x32_bf16 a[12:15], a[172:175], a[128:131], a[12:15]\n" \
"ds_read_b64_tr_b16 a[240:241], v36 offset:8192\n" \
"s_waitcnt lgkmcnt(5)\n" \
"v_mfma_f32_16x16x32_bf16 a[12:15], a[236:239], a[192:195], a[12:15]\n" \
"v_mfma_f32_16x16x32_bf16 a[28:31], a[172:175], a[132:135], a[28:31]\n" \
"v_mfma_f32_16x16x32_bf16 a[28:31], a[236:239], a[196:199], a[28:31]\n" \
"ds_read_b64_tr_b16 a[242:243], v36 offset:9216\n" \
"v_mfma_f32_16x16x32_bf16 a[24:27], a[168:171], a[132:135], a[24:27]\n" \
"v_mfma_f32_16x16x32_bf16 a[24:27], a[232:235], a[196:199], a[24:27]\n" \
"v_mfma_f32_16x16x32_bf16 a[20:23], a[164:167], a[132:135], a[20:23]\n" \
"v_mfma_f32_16x16x32_bf16 a[20:23], a[228:231], a[196:199], a[20:23]\n" \
"ds_read_b64_tr_b16 a[180:181], v37 offset:0\n" \
"v_mfma_f32_16x16x32_bf16 a[16:19], a[160:163], a[132:135], a[16:19]\n" \
"v_mfma_f32_16x16x32_bf16 a[16:19], a[224:227], a[196:199], a[16:19]\n" \
"v_mfma_f32_16x16x32_bf16 a[32:35], a[160:163], a[136:139], a[32:35]\n" \
"ds_read_b64_tr_b16 a[182:183], v37 offset:1024\n" \
"v_mfma_f32_16x16x32_bf16 a[32:35], a[224:227], a[200:203], a[32:35]\n" \
"v_mfma_f32_16x16x32_bf16 a[36:39], a[164:167], a[136:139], a[36:39]\n" \
"v_mfma_f32_16x16x32_bf16 a[36:39], a[228:231], a[200:203], a[36:39]\n" \
"buffer_store_dwordx4 v[200:203], v20, s[80:83], s35 offen sc1\n" \
"v_mfma_f32_16x16x32_bf16 a[40:43], a[168:171], a[136:139], a[40:43]\n" \
"s_mov_b32 m0, s32\n" \
"ds_read_b64_tr_b16 a[244:245], v37 offset:8192\n" \
"v_mfma_f32_16x16x32_bf16 a[40:43], a[232:235], a[200:203], a[40:43]\n" \
"s_mov_b32 exec_lo, 0x0\n" \
"s_mov_b32 exec_hi, 0xffffffff\n" \
"global_load_lds_dword v[188:189], off\n" \
"s_mov_b64 exec, -1\n" \
"v_mfma_f32_16x16x32_bf16 a[44:47], a[172:175], a[136:139], a[44:47]\n" \
"ds_read_b64_tr_b16 a[246:247], v37 offset:9216\n" \
"v_mfma_f32_16x16x32_bf16 a[44:47], a[236:239], a[200:203], a[44:47]\n" \
"v_mfma_f32_16x16x32_bf16 a[60:63], a[172:175], a[140:143], a[60:63]\n" \
"v_mfma_f32_16x16x32_bf16 a[60:63], a[236:239], a[204:207], a[60:63]\n" \
"v_mfma_f32_16x16x32_bf16 a[56:59], a[168:171], a[140:143], a[56:59]\n" \
"ds_read_b64_tr_b16 a[184:185], v38 offset:0\n" \
"v_mfma_f32_16x16x32_bf16 a[56:59], a[232:235], a[204:207], a[56:59]\n" \
"v_mfma_f32_16x16x32_bf16 a[52:55], a[164:167], a[140:143], a[52:55]\n" \
"buffer_store_dwordx4 v[204:207], v20, s[80:83], s88 offen sc1\n" \
"v_mfma_f32_16x16x32_bf16 a[52:55], a[228:231], a[204:207], a[52:55]\n" \
"ds_read_b64_tr_b16 a[186:187], v38 offset:1024\n" \
"v_mfma_f32_16x16x32_bf16 a[48:51], a[160:163], a[140:143], a[48:51]\n" \
"v_mfma_f32_16x16x32_bf16 a[48:51], a[224:227], a[204:207], a[48:51]\n" \
"v_mfma_f32_16x16x32_bf16 a[64:67], a[160:163], a[144:147], a[64:67]\n" \
"ds_read_b64_tr_b16 a[248:249], v38 offset:8192\n" \
"v_mfma_f32_16x16x32_bf16 a[64:67], a[224:227], a[208:211], a[64:67]\n" \
"v_mfma_f32_16x16x32_bf16 a[68:71], a[164:167], a[144:147], a[68:71]\n" \
"v_mfma_f32_16x16x32_bf16 a[68:71], a[228:231], a[208:211], a[68:71]\n" \
"ds_read_b64_tr_b16 a[250:251], v38 offset:9216\n" \
"v_mfma_f32_16x16x32_bf16 a[72:75], a[168:171], a[144:147], a[72:75]\n" \
"v_mfma_f32_16x16x32_bf16 a[72:75], a[232:235], a[208:211], a[72:75]\n" \
"v_mfma_f32_16x16x32_bf16 a[76:79], a[172:175], a[144:147], a[76:79]\n" \
"buffer_store_dwordx4 v[208:211], v20, s[80:83], s36 offen sc1\n" \
"v_mfma_f32_16x16x32_bf16 a[76:79], a[236:239], a[208:211], a[76:79]\n" \
"ds_read_b64_tr_b16 a[188:189], v39 offset:0\n" \
"v_mfma_f32_16x16x32_bf16 a[92:95], a[172:175], a[148:151], a[92:95]\n" \
"v_mfma_f32_16x16x32_bf16 a[92:95], a[236:239], a[212:215], a[92:95]\n" \
"v_mfma_f32_16x16x32_bf16 a[88:91], a[168:171], a[148:151], a[88:91]\n" \
"ds_read_b64_tr_b16 a[190:191], v39 offset:1024\n" \
"v_mfma_f32_16x16x32_bf16 a[88:91], a[232:235], a[212:215], a[88:91]\n" \
"v_mfma_f32_16x16x32_bf16 a[84:87], a[164:167], a[148:151], a[84:87]\n" \
"v_mfma_f32_16x16x32_bf16 a[84:87], a[228:231], a[212:215], a[84:87]\n" \
"ds_read_b64_tr_b16 a[252:253], v39 offset:8192\n" \
"v_mfma_f32_16x16x32_bf16 a[80:83], a[160:163], a[148:151], a[80:83]\n" \
"v_mfma_f32_16x16x32_bf16 a[80:83], a[224:227], a[212:215], a[80:83]\n" \
"v_mfma_f32_16x16x32_bf16 a[96:99], a[160:163], a[152:155], a[96:99]\n" \
"ds_read_b64_tr_b16 a[254:255], v39 offset:9216\n" \
"v_mfma_f32_16x16x32_bf16 a[96:99], a[224:227], a[216:219], a[96:99]\n" \
"v_mfma_f32_16x16x32_bf16 a[100:103], a[164:167], a[152:155], a[100:103]\n" \
"v_mfma_f32_16x16x32_bf16 a[100:103], a[228:231], a[216:219], a[100:103]\n" \
".fill (L_pb + 12000 - .) / 4, 4, 0xBF800000\n" \
"L_pp3:\n" \
"s_cbranch_execz L_pret\n" \
"v_mfma_f32_16x16x32_bf16 a[104:107], a[168:171], a[152:155], a[104:107]\n" \
"v_mfma_f32_16x16x32_bf16 a[104:107], a[232:235], a[216:219], a[104:107]\n" \
"v_mfma_f32_16x16x32_bf16 a[108:111], a[172:175], a[152:155], a[108:111]\n" \
"v_mfma_f32_16x16x32_bf16 a[108:111], a[236:239], a[216:219], a[108:111]\n" \
"s_waitcnt lgkmcnt(15)\n" \
"v_mfma_f32_16x16x32_bf16 a[124:127], a[172:175], a[156:159], a[124:127]\n" \
"v_mfma_f32_16x16x32_bf16 a[124:127], a[236:239], a[220:223], a[124:127]\n" \
"v_mfma_f32_16x16x32_bf16 a[120:123], a[168:171], a[156:159], a[120:123]\n" \
"v_mfma_f32_16x16x32_bf16 a[120:123], a[232:235], a[220:223], a[120:123]\n" \
"v_mfma_f32_16x16x32_bf16 a[116:119], a[164:167], a[156:159], a[116:119]\n" \
"v_mfma_f32_16x16x32_bf16 a[116:119], a[228:231], a[220:223], a[116:119]\n" \
"v_mfma_f32_16x16x32_bf16 a[112:115], a[160:163], a[156:159], a[112:115]\n" \
"v_mfma_f32_16x16x32_bf16 a[112:115], a[224:227], a[220:223], a[112:115]\n" \
"s_waitcnt vmcnt(4) lgkmcnt(0)\n" \
"s_barrier\n" \
"v_mfma_f32_16x16x32_bf16 v[52:55], a[176:179], a[128:131], v[52:55]\n" \
"s_add_u32 m0, s30, 0x0\n" \
"s_movk_i32 s84, 0x300\n" \
"v_mfma_f32_16x16x32_bf16 v[52:55], a[240:243], a[192:195], v[52:55]\n" \
"buffer_load_dwordx4 v22, s[64:67], s84 offen offset:0 lds\n" \
"v_mfma_f32_16x16x32_bf16 v[56:59], a[180:183], a[128:131], v[56:59]\n" \
"ds_read_b64_tr_b16 a[160:161], v32 offset:32768\n" \
"v_mfma_f32_16x16x32_bf16 v[56:59], a[244:247], a[192:195], v[56:59]\n" \
"s_movk_i32 s84, 0x1f00\n" \
"s_nop 0\n" \
"v_mfma_f32_16x16x32_bf16 v[60:63], a[184:187], a[128:131], v[60:63]\n" \
"buffer_load_dwordx4 v23, s[64:67], s84 offen offset:1024 lds\n" \
"v_mfma_f32_16x16x32_bf16 v[60:63], a[248:251], a[192:195], v[60:63]\n" \
"ds_read_b64_tr_b16 a[162:163], v32 offset:33792\n" \
"v_mfma_f32_16x16x32_bf16 v[64:67], a[188:191], a[128:131], v[64:67]\n" \
"ds_read_b64_tr_b16 a[224:225], v32 offset:40960\n" \
"v_mfma_f32_16x16x32_bf16 v[64:67], a[252:255], a[192:195], v[64:67]\n" \
"ds_read_b128 a[128:131], v28 offset:49152\n" \
"v_mfma_f32_16x16x32_bf16 v[80:83], a[188:191], a[132:135], v[80:83]\n" \
"ds_read_b128 a[192:195], v29 offset:49152\n" \
"v_mfma_f32_16x16x32_bf16 v[80:83], a[252:255], a[196:199], v[80:83]\n" \
"s_movk_i32 s84, 0x3b00\n" \
"v_mfma_f32_16x16x32_bf16 v[76:79], a[184:187], a[132:135], v[76:79]\n" \
"buffer_load_dwordx4 v22, s[64:67], s84 offen offset:2048 lds\n" \
"v_mfma_f32_16x16x32_bf16 v[76:79], a[248:251], a[196:199], v[76:79]\n" \
"s_movk_i32 s84, 0x5700\n" \
"v_mfma_f32_16x16x32_bf16 v[72:75], a[180:183], a[132:135], v[72:75]\n" \
"buffer_load_dwordx4 v23, s[64:67], s84 offen offset:3072 lds\n" \
"v_mfma_f32_16x16x32_bf16 v[72:75], a[244:247], a[196:199], v[72:75]\n" \
"s_add_u32 m0, s31, 0x0\n" \
"s_mov_b32 s84, 0x300000\n" \
"v_mfma_f32_16x16x32_bf16 v[68:71], a[176:179], a[132:135], v[68:71]\n" \
"buffer_load_dwordx4 v26, s[68:71], s84 offen offset:0 lds\n" \
"v_mfma_f32_16x16x32_bf16 v[68:71], a[240:243], a[196:199], v[68:71]\n" \
"ds_read_b128 a[132:135], v28 offset:51200\n" \
"v_mfma_f32_16x16x32_bf16 v[84:87], a[176:179], a[136:139], v[84:87]\n" \
"ds_read_b128 a[196:199], v29 offset:51200\n" \
"v_mfma_f32_16x16x32_bf16 v[84:87], a[240:243], a[200:203], v[84:87]\n" \
"s_addk_i32 s84, 0x7c00\n" \
"v_mfma_f32_16x16x32_bf16 v[88:91], a[180:183], a[136:139], v[88:91]\n" \
"buffer_load_dwordx4 v26, s[68:71], s84 offen offset:1024 lds\n" \
"v_mfma_f32_16x16x32_bf16 v[88:91], a[244:247], a[200:203], v[88:91]\n" \
"ds_read_b64_tr_b16 a[226:227], v32 offset:41984\n" \
"v_mfma_f32_16x16x32_bf16 v[92:95], a[184:187], a[136:139], v[92:95]\n" \
"s_addk_i32 s84, 0x7c00\n" \
"v_mfma_f32_16x16x32_bf16 v[92:95], a[248:251], a[200:203], v[92:95]\n" \
"buffer_load_dwordx4 v27, s[68:71], s84 offen offset:2048 lds\n" \
"v_mfma_f32_16x16x32_bf16 v[96:99], a[188:191], a[136:139], v[96:99]\n" \
"ds_read_b64_tr_b16 a[164:165], v33 offset:32768\n" \
"v_mfma_f32_16x16x32_bf16 v[96:99], a[252:255], a[200:203], v[96:99]\n" \
"ds_read_b128 a[136:139], v28 offset:53248\n" \
"v_mfma_f32_16x16x32_bf16 v[112:115], a[188:191], a[140:143], v[112:115]\n" \
"ds_read_b128 a[200:203], v29 offset:53248\n" \
"v_mfma_f32_16x16x32_bf16 v[112:115], a[252:255], a[204:207], v[112:115]\n" \
"s_addk_i32 s84, 0x7c00\n" \
"v_mfma_f32_16x16x32_bf16 v[108:111], a[184:187], a[140:143], v[108:111]\n" \
"buffer_load_dwordx4 v27, s[68:71], s84 offen offset:3072 lds\n" \
"v_mfma_f32_16x16x32_bf16 v[108:111], a[248:251], a[204:207], v[108:111]\n" \
"s_add_u32 m0, s30, 0x1000\n" \
"s_mov_b32 s84, 0x8300\n" \
"v_mfma_f32_16x16x32_bf16 v[104:107], a[180:183], a[140:143], v[104:107]\n" \
"buffer_load_dwordx4 v22, s[64:67], s84 offen offset:0 lds\n" \
"v_mfma_f32_16x16x32_bf16 v[104:107], a[244:247], a[204:207], v[104:107]\n" \
"ds_read_b64_tr_b16 a[166:167], v33 offset:33792\n" \
"v_mfma_f32_16x16x32_bf16 v[100:103], a[176:179], a[140:143], v[100:103]\n" \
"ds_read_b64_tr_b16 a[228:229], v33 offset:40960\n" \
"v_mfma_f32_16x16x32_bf16 v[100:103], a[240:243], a[204:207], v[100:103]\n" \
"ds_read_b128 a[140:143], v28 offset:55296\n" \
"v_mfma_f32_16x16x32_bf16 v[116:119], a[176:179], a[144:147], v[116:119]\n" \
"ds_read_b128 a[204:207], v29 offset:55296\n" \
"v_mfma_f32_16x16x32_bf16 v[116:119], a[240:243], a[208:211], v[116:119]\n" \
"s_addk_i32 s84, 0x1c00\n" \
"v_mfma_f32_16x16x32_bf16 v[120:123], a[180:183], a[144:147], v[120:123]\n" \
"buffer_load_dwordx4 v23, s[64:67], s84 offen offset:1024 lds\n" \
"v_mfma_f32_16x16x32_bf16 v[120:123], a[244:247], a[208:211], v[120:123]\n" \
"s_addk_i32 s84, 0x1c00\n" \
"v_mfma_f32_16x16x32_bf16 v[124:127], a[184:187], a[144:147], v[124:127]\n" \
"buffer_load_dwordx4 v22, s[64:67], s84 offen offset:2048 lds\n" \
"v_mfma_f32_16x16x32_bf16 v[124:127], a[248:251], a[208:211], v[124:127]\n" \
"s_addk_i32 s84, 0x1c00\n" \
"s_nop 0\n" \
"v_mfma_f32_16x16x32_bf16 v[128:131], a[188:191], a[144:147], v[128:131]\n" \
"buffer_load_dwordx4 v23, s[64:67], s84 offen offset:3072 lds\n" \
"v_mfma_f32_16x16x32_bf16 v[128:131], a[252:255], a[208:211], v[128:131]\n" \
"ds_read_b128 a[144:147], v28 offset:32768\n" \
"v_mfma_f32_16x16x32_bf16 v[144:147], a[188:191], a[148:151], v[144:147]\n" \
"ds_read_b128 a[208:211], v29 offset:32768\n" \
"v_mfma_f32_16x16x32_bf16 v[144:147], a[252:255], a[212:215], v[144:147]\n" \
"s_add_u32 m0, s31, 0x4000\n" \
"s_mov_b32 s84, 0x300000\n" \
"v_mfma_f32_16x16x32_bf16 v[140:143], a[184:187], a[148:151], v[140:143]\n" \
"buffer_load_dwordx4 v26, s[72:75], s84 offen offset:0 lds\n" \
"v_mfma_f32_16x16x32_bf16 v[140:143], a[248:251], a[212:215], v[140:143]\n" \
"ds_read_b64_tr_b16 a[230:231], v33 offset:41984\n" \
"v_mfma_f32_16x16x32_bf16 v[136:139], a[180:183], a[148:151], v[136:139]\n" \
"s_addk_i32 s84, 0x7c00\n" \
"s_nop 0\n" \
"v_mfma_f32_16x16x32_bf16 v[136:139], a[244:247], a[212:215], v[136:139]\n" \
"buffer_load_dwordx4 v26, s[72:75], s84 offen offset:1024 lds\n" \
"v_mfma_f32_16x16x32_bf16 v[132:135], a[176:179], a[148:151], v[132:135]\n" \
"ds_read_b64_tr_b16 a[168:169], v34 offset:32768\n" \
"v_mfma_f32_16x16x32_bf16 v[132:135], a[240:243], a[212:215], v[132:135]\n" \
"ds_read_b128 a[148:151], v28 offset:34816\n" \
"v_mfma_f32_16x16x32_bf16 v[148:151], a[176:179], a[152:155], v[148:151]\n" \
"ds_read_b128 a[212:215], v29 offset:34816\n" \
"v_mfma_f32_16x16x32_bf16 v[148:151], a[240:243], a[216:219], v[148:151]\n" \
"ds_read_b64_tr_b16 a[170:171], v34 offset:33792\n" \
"v_mfma_f32_16x16x32_bf16 v[152:155], a[180:183], a[152:155], v[152:155]\n" \
"ds_read_b64_tr_b16 a[232:233], v34 offset:40960\n" \
"v_mfma_f32_16x16x32_bf16 v[152:155], a[244:247], a[216:219], v[152:155]\n" \
"ds_read_b64_tr_b16 a[234:235], v34 offset:41984\n" \
"v_mfma_f32_16x16x32_bf16 v[156:159], a[184:187], a[152:155], v[156:159]\n" \
"ds_read_b64_tr_b16 a[172:173], v35 offset:32768\n" \
"v_mfma_f32_16x16x32_bf16 v[156:159], a[248:251], a[216:219], v[156:159]\n" \
"ds_read_b64_tr_b16 a[174:175], v35 offset:33792\n" \
"v_mfma_f32_16x16x32_bf16 v[160:163], a[188:191], a[152:155], v[160:163]\n" \
"ds_read_b64_tr_b16 a[236:237], v35 offset:40960\n" \
"v_mfma_f32_16x16x32_bf16 v[160:163], a[252:255], a[216:219], v[160:163]\n" \
"ds_read_b128 a[152:155], v28 offset:36864\n" \
"v_mfma_f32_16x16x32_bf16 v[176:179], a[188:191], a[156:159], v[176:179]\n" \
"ds_read_b128 a[216:219], v29 offset:36864\n" \
"v_mfma_f32_16x16x32_bf16 v[176:179], a[252:255], a[220:223], v[176:179]\n" \
"ds_read_b64_tr_b16 a[238:239], v35 offset:41984\n" \
"v_mfma_f32_16x16x32_bf16 v[172:175], a[184:187], a[156:159], v[172:175]\n" \
"v_mfma_f32_16x16x32_bf16 v[172:175], a[248:251], a[220:223], v[172:175]\n" \
"v_mfma_f32_16x16x32_bf16 v[168:171], a[180:183], a[156:159], v[168:171]\n" \
"v_mfma_f32_16x16x32_bf16 v[168:171], a[244:247], a[220:223], v[168:171]\n" \
"v_mfma_f32_16x16x32_bf16 v[164:167], a[176:179], a[156:159], v[164:167]\n" \
"v_mfma_f32_16x16x32_bf16 v[164:167], a[240:243], a[220:223], v[164:167]\n" \
"ds_read_b128 a[156:159], v28 offset:38912\n" \
"ds_read_b128 a[220:223], v29 offset:38912\n" \
"s_waitcnt lgkmcnt(15)\n" \
"v_mfma_f32_16x16x32_bf16 a[0:3], a[160:163], a[128:131], a[0:3]\n" \
"s_addk_i32 s84, 0x7c00\n" \
"v_mfma_f32_16x16x32_bf16 a[0:3], a[224:227], a[192:195], a[0:3]\n" \
"buffer_load_dwordx4 v27, s[72:75], s84 offen offset:2048 lds\n" \
"v_mfma_f32_16x16x32_bf16 a[4:7], a[164:167], a[128:131], a[4:7]\n" \
"ds_read_b64_tr_b16 a[176:177], v36 offset:32768\n" \
"s_waitcnt lgkmcnt(15)\n" \
"v_mfma_f32_16x16x32_bf16 a[4:7], a[228:231], a[192:195], a[4:7]\n" \
"s_addk_i32 s84, 0x7c00\n" \
"s_waitcnt lgkmcnt(11)\n" \
"v_mfma_f32_16x16x32_bf16 a[8:11], a[168:171], a[128:131], a[8:11]\n" \
"buffer_load_dwordx4 v27, s[72:75], s84 offen offset:3072 lds\n" \
"s_waitcnt lgkmcnt(9)\n" \
"v_mfma_f32_16x16x32_bf16 a[8:11], a[232:235], a[192:195], a[8:11]\n" \
"ds_read_b64_tr_b16 a[178:179], v36 offset:33792\n" \
"s_waitcnt lgkmcnt(8)\n" \
"v_mfma_f32_16x16x32_bf16 a[12:15], a[172:175], a[128:131], a[12:15]\n" \
"ds_read_b64_tr_b16 a[240:241], v36 offset:40960\n" \
"s_waitcnt lgkmcnt(5)\n" \
"v_mfma_f32_16x16x32_bf16 a[12:15], a[236:239], a[192:195], a[12:15]\n" \
"v_mfma_f32_16x16x32_bf16 a[28:31], a[172:175], a[132:135], a[28:31]\n" \
"v_mfma_f32_16x16x32_bf16 a[28:31], a[236:239], a[196:199], a[28:31]\n" \
"ds_read_b64_tr_b16 a[242:243], v36 offset:41984\n" \
"v_mfma_f32_16x16x32_bf16 a[24:27], a[168:171], a[132:135], a[24:27]\n" \
"v_mfma_f32_16x16x32_bf16 a[24:27], a[232:235], a[196:199], a[24:27]\n" \
"v_mfma_f32_16x16x32_bf16 a[20:23], a[164:167], a[132:135], a[20:23]\n" \
"v_mfma_f32_16x16x32_bf16 a[20:23], a[228:231], a[196:199], a[20:23]\n" \
"ds_read_b64_tr_b16 a[180:181], v37 offset:32768\n" \
"v_mfma_f32_16x16x32_bf16 a[16:19], a[160:163], a[132:135], a[16:19]\n" \
"v_mfma_f32_16x16x32_bf16 a[16:19], a[224:227], a[196:199], a[16:19]\n" \
"v_mfma_f32_16x16x32_bf16 a[32:35], a[160:163], a[136:139], a[32:35]\n" \
"ds_read_b64_tr_b16 a[182:183], v37 offset:33792\n" \
"v_mfma_f32_16x16x32_bf16 a[32:35], a[224:227], a[200:203], a[32:35]\n" \
"v_mfma_f32_16x16x32_bf16 a[36:39], a[164:167], a[136:139], a[36:39]\n" \
"v_lshlrev_b32_e32 v45, 3, v20\n" \
"v_add_u32_e32 v45, s31, v45\n" \
"v_mfma_f32_16x16x32_bf16 a[36:39], a[228:231], a[200:203], a[36:39]\n" \
"s_getpc_b64 s[86:87]\n" \
"s_sub_u32 s86, s86, 0xf800\n" \
"s_subb_u32 s87, s87, 0\n" \
"s_mov_b32 m0, s32\n" \
"s_nop 0\n" \
"v_mfma_f32_16x16x32_bf16 a[40:43], a[168:171], a[136:139], a[40:43]\n" \
"global_load_lds_dword v45, s[86:87]\n" \
"v_mfma_f32_16x16x32_bf16 a[40:43], a[232:235], a[200:203], a[40:43]\n" \
"ds_read_b64_tr_b16 a[244:245], v37 offset:40960\n" \
"v_mfma_f32_16x16x32_bf16 a[44:47], a[172:175], a[136:139], a[44:47]\n" \
"ds_read_b64_tr_b16 a[246:247], v37 offset:41984\n" \
"v_mfma_f32_16x16x32_bf16 a[44:47], a[236:239], a[200:203], a[44:47]\n" \
"v_mfma_f32_16x16x32_bf16 a[60:63], a[172:175], a[140:143], a[60:63]\n" \
"v_mfma_f32_16x16x32_bf16 a[60:63], a[236:239], a[204:207], a[60:63]\n" \
"v_mfma_f32_16x16x32_bf16 a[56:59], a[168:171], a[140:143], a[56:59]\n" \
"ds_read_b64_tr_b16 a[184:185], v38 offset:32768\n" \
"v_mfma_f32_16x16x32_bf16 a[56:59], a[232:235], a[204:207], a[56:59]\n" \
"v_mfma_f32_16x16x32_bf16 a[52:55], a[164:167], a[140:143], a[52:55]\n" \
"v_mfma_f32_16x16x32_bf16 a[52:55], a[228:231], a[204:207], a[52:55]\n" \
"ds_read_b64_tr_b16 a[186:187], v38 offset:33792\n" \
"v_mfma_f32_16x16x32_bf16 a[48:51], a[160:163], a[140:143], a[48:51]\n" \
"v_mfma_f32_16x16x32_bf16 a[48:51], a[224:227], a[204:207], a[48:51]\n" \
"v_mfma_f32_16x16x32_bf16 a[64:67], a[160:163], a[144:147], a[64:67]\n" \
"ds_read_b64_tr_b16 a[248:249], v38 offset:40960\n" \
"v_mfma_f32_16x16x32_bf16 a[64:67], a[224:227], a[208:211], a[64:67]\n" \
"v_mfma_f32_16x16x32_bf16 a[68:71], a[164:167], a[144:147], a[68:71]\n" \
"v_mfma_f32_16x16x32_bf16 a[68:71], a[228:231], a[208:211], a[68:71]\n" \
"ds_read_b64_tr_b16 a[250:251], v38 offset:41984\n" \
"v_mfma_f32_16x16x32_bf16 a[72:75], a[168:171], a[144:147], a[72:75]\n" \
"v_mfma_f32_16x16x32_bf16 a[72:75], a[232:235], a[208:211], a[72:75]\n" \
"v_mfma_f32_16x16x32_bf16 a[76:79], a[172:175], a[144:147], a[76:79]\n" \
"v_mfma_f32_16x16x32_bf16 a[76:79], a[236:239], a[208:211], a[76:79]\n" \
"ds_read_b64_tr_b16 a[188:189], v39 offset:32768\n" \
"v_mfma_f32_16x16x32_bf16 a[92:95], a[172:175], a[148:151], a[92:95]\n" \
"v_mfma_f32_16x16x32_bf16 a[92:95], a[236:239], a[212:215], a[92:95]\n" \
"v_mfma_f32_16x16x32_bf16 a[88:91], a[168:171], a[148:151], a[88:91]\n" \
"ds_read_b64_tr_b16 a[190:191], v39 offset:33792\n" \
"v_mfma_f32_16x16x32_bf16 a[88:91], a[232:235], a[212:215], a[88:91]\n" \
"v_mfma_f32_16x16x32_bf16 a[84:87], a[164:167], a[148:151], a[84:87]\n" \
"v_mfma_f32_16x16x32_bf16 a[84:87], a[228:231], a[212:215], a[84:87]\n" \
"ds_read_b64_tr_b16 a[252:253], v39 offset:40960\n" \
"v_mfma_f32_16x16x32_bf16 a[80:83], a[160:163], a[148:151], a[80:83]\n" \
"v_mfma_f32_16x16x32_bf16 a[80:83], a[224:227], a[212:215], a[80:83]\n" \
"v_mfma_f32_16x16x32_bf16 a[96:99], a[160:163], a[152:155], a[96:99]\n" \
"ds_read_b64_tr_b16 a[254:255], v39 offset:41984\n" \
"v_mfma_f32_16x16x32_bf16 a[96:99], a[224:227], a[216:219], a[96:99]\n" \
"v_mfma_f32_16x16x32_bf16 a[100:103], a[164:167], a[152:155], a[100:103]\n" \
"v_mfma_f32_16x16x32_bf16 a[100:103], a[228:231], a[216:219], a[100:103]\n" \
"v_mfma_f32_16x16x32_bf16 a[104:107], a[168:171], a[152:155], a[104:107]\n" \
"v_mfma_f32_16x16x32_bf16 a[104:107], a[232:235], a[216:219], a[104:107]\n" \
"v_mfma_f32_16x16x32_bf16 a[108:111], a[172:175], a[152:155], a[108:111]\n" \
"v_mfma_f32_16x16x32_bf16 a[108:111], a[236:239], a[216:219], a[108:111]\n" \
"s_waitcnt lgkmcnt(15)\n" \
"v_mfma_f32_16x16x32_bf16 a[124:127], a[172:175], a[156:159], a[124:127]\n" \
"v_mfma_f32_16x16x32_bf16 a[124:127], a[236:239], a[220:223], a[124:127]\n" \
"v_mfma_f32_16x16x32_bf16 a[120:123], a[168:171], a[156:159], a[120:123]\n" \
"v_mfma_f32_16x16x32_bf16 a[120:123], a[232:235], a[220:223], a[120:123]\n" \
"v_mfma_f32_16x16x32_bf16 a[116:119], a[164:167], a[156:159], a[116:119]\n" \
"v_mfma_f32_16x16x32_bf16 a[116:119], a[228:231], a[220:223], a[116:119]\n" \
"v_mfma_f32_16x16x32_bf16 a[112:115], a[160:163], a[156:159], a[112:115]\n" \
"v_mfma_f32_16x16x32_bf16 a[112:115], a[224:227], a[220:223], a[112:115]\n" \
"s_waitcnt vmcnt(1) lgkmcnt(0)\n" \
"s_barrier\n" \
"v_mov_b32_e32 v45, s62\n" \
"s_mov_b64 exec, 1\n" \
"buffer_store_dword v45, off, s[80:83], s37 offset:128 sc1\n" \
"s_mov_b64 exec, -1\n" \
"v_mfma_f32_16x16x32_bf16 v[52:55], a[176:179], a[128:131], v[52:55]\n" \
"s_add_u32 m0, s30, 0x8000\n" \
"s_movk_i32 s84, 0x380\n" \
"v_mfma_f32_16x16x32_bf16 v[52:55], a[240:243], a[192:195], v[52:55]\n" \
"buffer_load_dwordx4 v22, s[64:67], s84 offen offset:0 lds\n" \
"v_mfma_f32_16x16x32_bf16 v[56:59], a[180:183], a[128:131], v[56:59]\n" \
"ds_read_b64_tr_b16 a[160:161], v32 offset:0\n" \
"v_mfma_f32_16x16x32_bf16 v[56:59], a[244:247], a[192:195], v[56:59]\n" \
"s_movk_i32 s84, 0x1f80\n" \
"v_mfma_f32_16x16x32_bf16 v[60:63], a[184:187], a[128:131], v[60:63]\n" \
"buffer_load_dwordx4 v23, s[64:67], s84 offen offset:1024 lds\n" \
"v_mfma_f32_16x16x32_bf16 v[60:63], a[248:251], a[192:195], v[60:63]\n" \
"ds_read_b64_tr_b16 a[162:163], v32 offset:1024\n" \
"v_mfma_f32_16x16x32_bf16 v[64:67], a[188:191], a[128:131], v[64:67]\n" \
"ds_read_b64_tr_b16 a[224:225], v32 offset:8192\n" \
"v_mfma_f32_16x16x32_bf16 v[64:67], a[252:255], a[192:195], v[64:67]\n" \
"ds_read_b128 a[128:131], v28 offset:16384\n" \
"v_mfma_f32_16x16x32_bf16 v[80:83], a[188:191], a[132:135], v[80:83]\n" \
"ds_read_b128 a[192:195], v29 offset:16384\n" \
"v_mfma_f32_16x16x32_bf16 v[80:83], a[252:255], a[196:199], v[80:83]\n" \
"s_movk_i32 s84, 0x3b80\n" \
"v_mfma_f32_16x16x32_bf16 v[76:79], a[184:187], a[132:135], v[76:79]\n" \
"buffer_load_dwordx4 v22, s[64:67], s84 offen offset:2048 lds\n" \
"v_mfma_f32_16x16x32_bf16 v[76:79], a[248:251], a[196:199], v[76:79]\n" \
"s_movk_i32 s84, 0x5780\n" \
"v_mfma_f32_16x16x32_bf16 v[72:75], a[180:183], a[132:135], v[72:75]\n" \
"buffer_load_dwordx4 v23, s[64:67], s84 offen offset:3072 lds\n" \
"v_mfma_f32_16x16x32_bf16 v[72:75], a[244:247], a[196:199], v[72:75]\n" \
"s_add_u32 m0, s31, 0x8000\n" \
"s_mov_b32 s84, 0x380000\n" \
"v_mfma_f32_16x16x32_bf16 v[68:71], a[176:179], a[132:135], v[68:71]\n" \
"buffer_load_dwordx4 v26, s[68:71], s84 offen offset:0 lds\n" \
"v_mfma_f32_16x16x32_bf16 v[68:71], a[240:243], a[196:199], v[68:71]\n" \
"ds_read_b128 a[132:135], v28 offset:18432\n" \
"v_mfma_f32_16x16x32_bf16 v[84:87], a[176:179], a[136:139], v[84:87]\n" \
"ds_read_b128 a[196:199], v29 offset:18432\n" \
"v_mfma_f32_16x16x32_bf16 v[84:87], a[240:243], a[200:203], v[84:87]\n" \
"s_addk_i32 s84, 0x7c00\n" \
"v_mfma_f32_16x16x32_bf16 v[88:91], a[180:183], a[136:139], v[88:91]\n" \
"buffer_load_dwordx4 v26, s[68:71], s84 offen offset:1024 lds\n" \
"v_mfma_f32_16x16x32_bf16 v[88:91], a[244:247], a[200:203], v[88:91]\n" \
"ds_read_b64_tr_b16 a[226:227], v32 offset:9216\n" \
"v_mfma_f32_16x16x32_bf16 v[92:95], a[184:187], a[136:139], v[92:95]\n" \
"s_addk_i32 s84, 0x7c00\n" \
"v_mfma_f32_16x16x32_bf16 v[92:95], a[248:251], a[200:203], v[92:95]\n" \
"buffer_load_dwordx4 v27, s[68:71], s84 offen offset:2048 lds\n" \
"v_mfma_f32_16x16x32_bf16 v[96:99], a[188:191], a[136:139], v[96:99]\n" \
"ds_read_b64_tr_b16 a[164:165], v33 offset:0\n" \
"v_mfma_f32_16x16x32_bf16 v[96:99], a[252:255], a[200:203], v[96:99]\n" \
"ds_read_b128 a[136:139], v28 offset:20480\n" \
"v_mfma_f32_16x16x32_bf16 v[112:115], a[188:191], a[140:143], v[112:115]\n" \
"ds_read_b128 a[200:203], v29 offset:20480\n" \
"v_mfma_f32_16x16x32_bf16 v[112:115], a[252:255], a[204:207], v[112:115]\n" \
"s_addk_i32 s84, 0x7c00\n" \
"s_nop 0\n" \
"v_mfma_f32_16x16x32_bf16 v[108:111], a[184:187], a[140:143], v[108:111]\n" \
"buffer_load_dwordx4 v27, s[68:71], s84 offen offset:3072 lds\n" \
"v_mfma_f32_16x16x32_bf16 v[108:111], a[248:251], a[204:207], v[108:111]\n" \
"s_add_u32 m0, s30, 0x9000\n" \
"s_mov_b32 s84, 0x8380\n" \
"v_mfma_f32_16x16x32_bf16 v[104:107], a[180:183], a[140:143], v[104:107]\n" \
"buffer_load_dwordx4 v22, s[64:67], s84 offen offset:0 lds\n" \
"v_mfma_f32_16x16x32_bf16 v[104:107], a[244:247], a[204:207], v[104:107]\n" \
"ds_read_b64_tr_b16 a[166:167], v33 offset:1024\n" \
"v_mfma_f32_16x16x32_bf16 v[100:103], a[176:179], a[140:143], v[100:103]\n" \
"ds_read_b64_tr_b16 a[228:229], v33 offset:8192\n" \
"v_mfma_f32_16x16x32_bf16 v[100:103], a[240:243], a[204:207], v[100:103]\n" \
"ds_read_b128 a[140:143], v28 offset:22528\n" \
"v_mfma_f32_16x16x32_bf16 v[116:119], a[176:179], a[144:147], v[116:119]\n" \
"ds_read_b128 a[204:207], v29 offset:22528\n" \
"v_mfma_f32_16x16x32_bf16 v[116:119], a[240:243], a[208:211], v[116:119]\n" \
"s_addk_i32 s84, 0x1c00\n" \
"v_mfma_f32_16x16x32_bf16 v[120:123], a[180:183], a[144:147], v[120:123]\n" \
"buffer_load_dwordx4 v23, s[64:67], s84 offen offset:1024 lds\n" \
"v_mfma_f32_16x16x32_bf16 v[120:123], a[244:247], a[208:211], v[120:123]\n" \
"s_addk_i32 s84, 0x1c00\n" \
"v_mfma_f32_16x16x32_bf16 v[124:127], a[184:187], a[144:147], v[124:127]\n" \
"buffer_load_dwordx4 v22, s[64:67], s84 offen offset:2048 lds\n" \
"v_mfma_f32_16x16x32_bf16 v[124:127], a[248:251], a[208:211], v[124:127]\n" \
"s_addk_i32 s84, 0x1c00\n" \
"s_nop 0\n" \
"v_mfma_f32_16x16x32_bf16 v[128:131], a[188:191], a[144:147], v[128:131]\n" \
"buffer_load_dwordx4 v23, s[64:67], s84 offen offset:3072 lds\n" \
"v_mfma_f32_16x16x32_bf16 v[128:131], a[252:255], a[208:211], v[128:131]\n" \
"ds_read_b128 a[144:147], v28 offset:0\n" \
"v_mfma_f32_16x16x32_bf16 v[144:147], a[188:191], a[148:151], v[144:147]\n" \
"ds_read_b128 a[208:211], v29 offset:0\n" \
"v_mfma_f32_16x16x32_bf16 v[144:147], a[252:255], a[212:215], v[144:147]\n" \
"s_add_u32 m0, s31, 0xc000\n" \
"s_mov_b32 s84, 0x380000\n" \
"v_mfma_f32_16x16x32_bf16 v[140:143], a[184:187], a[148:151], v[140:143]\n" \
"buffer_load_dwordx4 v26, s[72:75], s84 offen offset:0 lds\n" \
"v_mfma_f32_16x16x32_bf16 v[140:143], a[248:251], a[212:215], v[140:143]\n" \
"ds_read_b64_tr_b16 a[230:231], v33 offset:9216\n" \
"v_mfma_f32_16x16x32_bf16 v[136:139], a[180:183], a[148:151], v[136:139]\n" \
"s_addk_i32 s84, 0x7c00\n" \
"s_nop 0\n" \
"v_mfma_f32_16x16x32_bf16 v[136:139], a[244:247], a[212:215], v[136:139]\n" \
"buffer_load_dwordx4 v26, s[72:75], s84 offen offset:1024 lds\n" \
"v_mfma_f32_16x16x32_bf16 v[132:135], a[176:179], a[148:151], v[132:135]\n" \
"ds_read_b64_tr_b16 a[168:169], v34 offset:0\n" \
"v_mfma_f32_16x16x32_bf16 v[132:135], a[240:243], a[212:215], v[132:135]\n" \
"ds_read_b128 a[148:151], v28 offset:2048\n" \
"v_mfma_f32_16x16x32_bf16 v[148:151], a[176:179], a[152:155], v[148:151]\n" \
"ds_read_b128 a[212:215], v29 offset:2048\n" \
"v_mfma_f32_16x16x32_bf16 v[148:151], a[240:243], a[216:219], v[148:151]\n" \
"ds_read_b64_tr_b16 a[170:171], v34 offset:1024\n" \
"v_mfma_f32_16x16x32_bf16 v[152:155], a[180:183], a[152:155], v[152:155]\n" \
"ds_read_b64_tr_b16 a[232:233], v34 offset:8192\n" \
"v_mfma_f32_16x16x32_bf16 v[152:155], a[244:247], a[216:219], v[152:155]\n" \
"ds_read_b64_tr_b16 a[234:235], v34 offset:9216\n" \
"v_mfma_f32_16x16x32_bf16 v[156:159], a[184:187], a[152:155], v[156:159]\n" \
"ds_read_b64_tr_b16 a[172:173], v35 offset:0\n" \
"v_mfma_f32_16x16x32_bf16 v[156:159], a[248:251], a[216:219], v[156:159]\n" \
"ds_read_b64_tr_b16 a[174:175], v35 offset:1024\n" \
"v_mfma_f32_16x16x32_bf16 v[160:163], a[188:191], a[152:155], v[160:163]\n" \
"ds_read_b64_tr_b16 a[236:237], v35 offset:8192\n" \
"v_mfma_f32_16x16x32_bf16 v[160:163], a[252:255], a[216:219], v[160:163]\n" \
"ds_read_b128 a[152:155], v28 offset:4096\n" \
"v_mfma_f32_16x16x32_bf16 v[176:179], a[188:191], a[156:159], v[176:179]\n" \
"ds_read_b128 a[216:219], v29 offset:4096\n" \
"v_mfma_f32_16x16x32_bf16 v[176:179], a[252:255], a[220:223], v[176:179]\n" \
"ds_read_b64_tr_b16 a[238:239], v35 offset:9216\n" \
"v_mfma_f32_16x16x32_bf16 v[172:175], a[184:187], a[156:159], v[172:175]\n" \
"v_mfma_f32_16x16x32_bf16 v[172:175], a[248:251], a[220:223], v[172:175]\n" \
"v_mfma_f32_16x16x32_bf16 v[168:171], a[180:183], a[156:159], v[168:171]\n" \
"v_mfma_f32_16x16x32_bf16 v[168:171], a[244:247], a[220:223], v[168:171]\n" \
"v_mfma_f32_16x16x32_bf16 v[164:167], a[176:179], a[156:159], v[164:167]\n" \
"v_mfma_f32_16x16x32_bf16 v[164:167], a[240:243], a[220:223], v[164:167]\n" \
"ds_read_b128 a[156:159], v28 offset:6144\n" \
"ds_read_b128 a[220:223], v29 offset:6144\n" \
"s_waitcnt lgkmcnt(15)\n" \
"v_mfma_f32_16x16x32_bf16 a[0:3], a[160:163], a[128:131], a[0:3]\n" \
"s_addk_i32 s84, 0x7c00\n" \
"v_mfma_f32_16x16x32_bf16 a[0:3], a[224:227], a[192:195], a[0:3]\n" \
"buffer_load_dwordx4 v27, s[72:75], s84 offen offset:2048 lds\n" \
"v_mfma_f32_16x16x32_bf16 a[4:7], a[164:167], a[128:131], a[4:7]\n" \
"ds_read_b64_tr_b16 a[176:177], v36 offset:0\n" \
"s_waitcnt lgkmcnt(15)\n" \
"v_mfma_f32_16x16x32_bf16 a[4:7], a[228:231], a[192:195], a[4:7]\n" \
"s_addk_i32 s84, 0x7c00\n" \
"s_waitcnt lgkmcnt(11)\n" \
"v_mfma_f32_16x16x32_bf16 a[8:11], a[168:171], a[128:131], a[8:11]\n" \
"buffer_load_dwordx4 v27, s[72:75], s84 offen offset:3072 lds\n" \
"s_waitcnt lgkmcnt(9)\n" \
"v_mfma_f32_16x16x32_bf16 a[8:11], a[232:235], a[192:195], a[8:11]\n" \
"ds_read_b64_tr_b16 a[178:179], v36 offset:1024\n" \
"s_waitcnt lgkmcnt(8)\n" \
"v_mfma_f32_16x16x32_bf16 a[12:15], a[172:175], a[128:131], a[12:15]\n" \
"ds_read_b64_tr_b16 a[240:241], v36 offset:8192\n" \
"s_waitcnt lgkmcnt(5)\n" \
"v_mfma_f32_16x16x32_bf16 a[12:15], a[236:239], a[192:195], a[12:15]\n" \
"v_mfma_f32_16x16x32_bf16 a[28:31], a[172:175], a[132:135], a[28:31]\n" \
"v_mfma_f32_16x16x32_bf16 a[28:31], a[236:239], a[196:199], a[28:31]\n" \
"ds_read_b64_tr_b16 a[242:243], v36 offset:9216\n" \
"v_mfma_f32_16x16x32_bf16 a[24:27], a[168:171], a[132:135], a[24:27]\n" \
"v_mfma_f32_16x16x32_bf16 a[24:27], a[232:235], a[196:199], a[24:27]\n" \
"v_mfma_f32_16x16x32_bf16 a[20:23], a[164:167], a[132:135], a[20:23]\n" \
"v_mfma_f32_16x16x32_bf16 a[20:23], a[228:231], a[196:199], a[20:23]\n" \
"ds_read_b64_tr_b16 a[180:181], v37 offset:0\n" \
"v_mfma_f32_16x16x32_bf16 a[16:19], a[160:163], a[132:135], a[16:19]\n" \
"v_mfma_f32_16x16x32_bf16 a[16:19], a[224:227], a[196:199], a[16:19]\n" \
"v_mfma_f32_16x16x32_bf16 a[32:35], a[160:163], a[136:139], a[32:35]\n" \
"ds_read_b64_tr_b16 a[182:183], v37 offset:1024\n" \
"v_mfma_f32_16x16x32_bf16 a[32:35], a[224:227], a[200:203], a[32:35]\n" \
"v_mfma_f32_16x16x32_bf16 a[36:39], a[164:167], a[136:139], a[36:39]\n" \
"v_mfma_f32_16x16x32_bf16 a[36:39], a[228:231], a[200:203], a[36:39]\n" \
"ds_read_b64_tr_b16 a[244:245], v37 offset:8192\n" \
"v_mfma_f32_16x16x32_bf16 a[40:43], a[168:171], a[136:139], a[40:43]\n" \
"v_mfma_f32_16x16x32_bf16 a[40:43], a[232:235], a[200:203], a[40:43]\n" \
"v_mfma_f32_16x16x32_bf16 a[44:47], a[172:175], a[136:139], a[44:47]\n" \
"ds_read_b64_tr_b16 a[246:247], v37 offset:9216\n" \
"v_mfma_f32_16x16x32_bf16 a[44:47], a[236:239], a[200:203], a[44:47]\n" \
"v_mfma_f32_16x16x32_bf16 a[60:63], a[172:175], a[140:143], a[60:63]\n" \
"v_mfma_f32_16x16x32_bf16 a[60:63], a[236:239], a[204:207], a[60:63]\n" \
"v_mfma_f32_16x16x32_bf16 a[56:59], a[168:171], a[140:143], a[56:59]\n" \
"ds_read_b64_tr_b16 a[184:185], v38 offset:0\n" \
"v_mfma_f32_16x16x32_bf16 a[56:59], a[232:235], a[204:207], a[56:59]\n" \
"v_mfma_f32_16x16x32_bf16 a[52:55], a[164:167], a[140:143], a[52:55]\n" \
"v_mfma_f32_16x16x32_bf16 a[52:55], a[228:231], a[204:207], a[52:55]\n" \
"ds_read_b64_tr_b16 a[186:187], v38 offset:1024\n" \
"v_mfma_f32_16x16x32_bf16 a[48:51], a[160:163], a[140:143], a[48:51]\n" \
"v_mfma_f32_16x16x32_bf16 a[48:51], a[224:227], a[204:207], a[48:51]\n" \
"v_mfma_f32_16x16x32_bf16 a[64:67], a[160:163], a[144:147], a[64:67]\n" \
"v_bfe_u32 v44, v20, 2, 5\n" \
"ds_read_b64_tr_b16 a[248:249], v38 offset:8192\n" \
"v_mfma_f32_16x16x32_bf16 a[64:67], a[224:227], a[208:211], a[64:67]\n" \
"buffer_load_dword v44, v44, s[80:83], s38 offen offset:128 sc1\n" \
"v_mfma_f32_16x16x32_bf16 a[68:71], a[164:167], a[144:147], a[68:71]\n" \
"v_mfma_f32_16x16x32_bf16 a[68:71], a[228:231], a[208:211], a[68:71]\n" \
"ds_read_b64_tr_b16 a[250:251], v38 offset:9216\n" \
"v_mfma_f32_16x16x32_bf16 a[72:75], a[168:171], a[144:147], a[72:75]\n" \
"v_mfma_f32_16x16x32_bf16 a[72:75], a[232:235], a[208:211], a[72:75]\n" \
"v_mfma_f32_16x16x32_bf16 a[76:79], a[172:175], a[144:147], a[76:79]\n" \
"v_mfma_f32_16x16x32_bf16 a[76:79], a[236:239], a[208:211], a[76:79]\n" \
"ds_read_b64_tr_b16 a[188:189], v39 offset:0\n" \
"v_mfma_f32_16x16x32_bf16 a[92:95], a[172:175], a[148:151], a[92:95]\n" \
"v_mfma_f32_16x16x32_bf16 a[92:95], a[236:239], a[212:215], a[92:95]\n" \
"v_mfma_f32_16x16x32_bf16 a[88:91], a[168:171], a[148:151], a[88:91]\n" \
"ds_read_b64_tr_b16 a[190:191], v39 offset:1024\n" \
"v_mfma_f32_16x16x32_bf16 a[88:91], a[232:235], a[212:215], a[88:91]\n" \
"v_mfma_f32_16x16x32_bf16 a[84:87], a[164:167], a[148:151], a[84:87]\n" \
"v_mfma_f32_16x16x32_bf16 a[84:87], a[228:231], a[212:215], a[84:87]\n" \
"ds_read_b64_tr_b16 a[252:253], v39 offset:8192\n" \
"v_mfma_f32_16x16x32_bf16 a[80:83], a[160:163], a[148:151], a[80:83]\n" \
"v_mfma_f32_16x16x32_bf16 a[80:83], a[224:227], a[212:215], a[80:83]\n" \
"v_mfma_f32_16x16x32_bf16 a[96:99], a[160:163], a[152:155], a[96:99]\n" \
"ds_read_b64_tr_b16 a[254:255], v39 offset:9216\n" \
"v_mfma_f32_16x16x32_bf16 a[96:99], a[224:227], a[216:219], a[96:99]\n" \
"v_mfma_f32_16x16x32_bf16 a[100:103], a[164:167], a[152:155], a[100:103]\n" \
"v_mfma_f32_16x16x32_bf16 a[100:103], a[228:231], a[216:219], a[100:103]\n" \
"v_mfma_f32_16x16x32_bf16 a[104:107], a[168:171], a[152:155], a[104:107]\n" \
"v_mfma_f32_16x16x32_bf16 a[104:107], a[232:235], a[216:219], a[104:107]\n" \
"v_mfma_f32_16x16x32_bf16 a[108:111], a[172:175], a[152:155], a[108:111]\n" \
"v_mfma_f32_16x16x32_bf16 a[108:111], a[236:239], a[216:219], a[108:111]\n" \
"s_waitcnt lgkmcnt(15)\n" \
"v_mfma_f32_16x16x32_bf16 a[124:127], a[172:175], a[156:159], a[124:127]\n" \
"v_mfma_f32_16x16x32_bf16 a[124:127], a[236:239], a[220:223], a[124:127]\n" \
"v_mfma_f32_16x16x32_bf16 a[120:123], a[168:171], a[156:159], a[120:123]\n" \
"v_mfma_f32_16x16x32_bf16 a[120:123], a[232:235], a[220:223], a[120:123]\n" \
"v_mfma_f32_16x16x32_bf16 a[116:119], a[164:167], a[156:159], a[116:119]\n" \
"v_mfma_f32_16x16x32_bf16 a[116:119], a[228:231], a[220:223], a[116:119]\n" \
"v_mfma_f32_16x16x32_bf16 a[112:115], a[160:163], a[156:159], a[112:115]\n" \
"v_mfma_f32_16x16x32_bf16 a[112:115], a[224:227], a[220:223], a[112:115]\n" \
"s_waitcnt vmcnt(0) lgkmcnt(0)\n" \
"s_barrier\n" \
"v_cmp_ne_u32_e32 vcc, s62, v44\n" \
"s_nop 4\n" \
"s_cbranch_vccnz L_poll60_%=\n" \
"L_pollr60_%=:\n" \
"s_mov_b32 s68, s12\n" \
"s_and_b32 s69, s13, 0xffff\n" \
"s_mov_b32 s70, 0x480000\n" \
"s_mov_b32 s71, 0x20000\n" \
"v_mfma_f32_16x16x32_bf16 v[52:55], a[176:179], a[128:131], v[52:55]\n" \
"s_add_u32 m0, s40, 0x0\n" \
"s_add_u32 s85, s39, 0x0\n" \
"s_nop 0\n" \
"v_mfma_f32_16x16x32_bf16 v[52:55], a[240:243], a[192:195], v[52:55]\n" \
"buffer_load_dwordx4 v20, s[80:83], s85 offen sc1 lds\n" \
"v_mfma_f32_16x16x32_bf16 v[56:59], a[180:183], a[128:131], v[56:59]\n" \
"ds_read_b64_tr_b16 a[160:161], v32 offset:32768\n" \
"v_mfma_f32_16x16x32_bf16 v[56:59], a[244:247], a[192:195], v[56:59]\n" \
"s_add_u32 m0, s40, 0x1000\n" \
"s_add_u32 s85, s39, 0x1000\n" \
"v_mfma_f32_16x16x32_bf16 v[60:63], a[184:187], a[128:131], v[60:63]\n" \
"buffer_load_dwordx4 v20, s[80:83], s85 offen sc1 lds\n" \
"v_mfma_f32_16x16x32_bf16 v[60:63], a[248:251], a[192:195], v[60:63]\n" \
"ds_read_b64_tr_b16 a[162:163], v32 offset:33792\n" \
"v_mfma_f32_16x16x32_bf16 v[64:67], a[188:191], a[128:131], v[64:67]\n" \
"ds_read_b64_tr_b16 a[224:225], v32 offset:40960\n" \
"v_mfma_f32_16x16x32_bf16 v[64:67], a[252:255], a[192:195], v[64:67]\n" \
"ds_read_b128 a[128:131], v28 offset:49152\n" \
"v_mfma_f32_16x16x32_bf16 v[80:83], a[188:191], a[132:135], v[80:83]\n" \
"ds_read_b128 a[192:195], v29 offset:49152\n" \
"v_mfma_f32_16x16x32_bf16 v[80:83], a[252:255], a[196:199], v[80:83]\n" \
"s_add_u32 m0, s40, 0x3000\n" \
"s_movk_i32 s84, 0x0\n" \
"v_mfma_f32_16x16x32_bf16 v[76:79], a[184:187], a[132:135], v[76:79]\n" \
"buffer_load_dwordx4 v21, s[76:79], s84 offen offset:0 lds\n" \
"v_mfma_f32_16x16x32_bf16 v[76:79], a[248:251], a[196:199], v[76:79]\n" \
"s_add_u32 m0, s30, 0x0\n" \
"s_movk_i32 s84, 0x0\n" \
"s_nop 0\n" \
"v_mfma_f32_16x16x32_bf16 v[72:75], a[180:183], a[132:135], v[72:75]\n" \
"buffer_load_dwordx4 v24, s[76:79], s84 offen offset:0 lds\n" \
"v_mfma_f32_16x16x32_bf16 v[72:75], a[244:247], a[196:199], v[72:75]\n" \
"ds_read_b64_tr_b16 a[226:227], v32 offset:41984\n" \
"v_mfma_f32_16x16x32_bf16 v[68:71], a[176:179], a[132:135], v[68:71]\n" \
"ds_read_b64_tr_b16 a[164:165], v33 offset:32768\n" \
"v_mfma_f32_16x16x32_bf16 v[68:71], a[240:243], a[196:199], v[68:71]\n" \
"ds_read_b128 a[132:135], v28 offset:51200\n" \
"v_mfma_f32_16x16x32_bf16 v[84:87], a[176:179], a[136:139], v[84:87]\n" \
"ds_read_b128 a[196:199], v29 offset:51200\n" \
"v_mfma_f32_16x16x32_bf16 v[84:87], a[240:243], a[200:203], v[84:87]\n" \
"s_movk_i32 s84, 0x2000\n" \
"v_mfma_f32_16x16x32_bf16 v[88:91], a[180:183], a[136:139], v[88:91]\n" \
"buffer_load_dwordx4 v25, s[76:79], s84 offen offset:1024 lds\n" \
"v_mfma_f32_16x16x32_bf16 v[88:91], a[244:247], a[200:203], v[88:91]\n" \
"s_movk_i32 s84, 0x4000\n" \
"v_mfma_f32_16x16x32_bf16 v[92:95], a[184:187], a[136:139], v[92:95]\n" \
"buffer_load_dwordx4 v24, s[76:79], s84 offen offset:2048 lds\n" \
"v_mfma_f32_16x16x32_bf16 v[92:95], a[248:251], a[200:203], v[92:95]\n" \
"ds_read_b64_tr_b16 a[166:167], v33 offset:33792\n" \
"v_mfma_f32_16x16x32_bf16 v[96:99], a[188:191], a[136:139], v[96:99]\n" \
"ds_read_b64_tr_b16 a[228:229], v33 offset:40960\n" \
"v_mfma_f32_16x16x32_bf16 v[96:99], a[252:255], a[200:203], v[96:99]\n" \
"ds_read_b128 a[136:139], v28 offset:53248\n" \
"v_mfma_f32_16x16x32_bf16 v[112:115], a[188:191], a[140:143], v[112:115]\n" \
"ds_read_b128 a[200:203], v29 offset:53248\n" \
"v_mfma_f32_16x16x32_bf16 v[112:115], a[252:255], a[204:207], v[112:115]\n" \
"s_movk_i32 s84, 0x6000\n" \
"v_mfma_f32_16x16x32_bf16 v[108:111], a[184:187], a[140:143], v[108:111]\n" \
"buffer_load_dwordx4 v25, s[76:79], s84 offen offset:3072 lds\n" \
"v_mfma_f32_16x16x32_bf16 v[108:111], a[248:251], a[204:207], v[108:111]\n" \
"s_add_u32 m0, s31, 0x0\n" \
"s_movk_i32 s84, 0x0\n" \
"v_mfma_f32_16x16x32_bf16 v[104:107], a[180:183], a[140:143], v[104:107]\n" \
"buffer_load_dwordx4 v26, s[68:71], s84 offen offset:0 lds\n" \
"v_mfma_f32_16x16x32_bf16 v[104:107], a[244:247], a[204:207], v[104:107]\n" \
"s_movk_i32 s84, 0x7c00\n" \
"v_mfma_f32_16x16x32_bf16 v[100:103], a[176:179], a[140:143], v[100:103]\n" \
".fill (L_pb + 16096 - .) / 4, 4, 0xBF800000\n" \
"L_pp4:\n" \
"s_cbranch_execz L_pret\n" \
"buffer_load_dwordx4 v26, s[68:71], s84 offen offset:1024 lds\n" \
"v_mfma_f32_16x16x32_bf16 v[100:103], a[240:243], a[204:207], v[100:103]\n" \
"ds_read_b128 a[140:143], v28 offset:55296\n" \
"v_mfma_f32_16x16x32_bf16 v[116:119], a[176:179], a[144:147], v[116:119]\n" \
"ds_read_b128 a[204:207], v29 offset:55296\n" \
"v_mfma_f32_16x16x32_bf16 v[116:119], a[240:243], a[208:211], v[116:119]\n" \
"s_addk_i32 s84, 0x7c00\n" \
"v_mfma_f32_16x16x32_bf16 v[120:123], a[180:183], a[144:147], v[120:123]\n" \
"buffer_load_dwordx4 v27, s[68:71], s84 offen offset:2048 lds\n" \
"v_mfma_f32_16x16x32_bf16 v[120:123], a[244:247], a[208:211], v[120:123]\n" \
"ds_read_b64_tr_b16 a[230:231], v33 offset:41984\n" \
"v_mfma_f32_16x16x32_bf16 v[124:127], a[184:187], a[144:147], v[124:127]\n" \
"s_addk_i32 s84, 0x7c00\n" \
"s_nop 0\n" \
"v_mfma_f32_16x16x32_bf16 v[124:127], a[248:251], a[208:211], v[124:127]\n" \
"buffer_load_dwordx4 v27, s[68:71], s84 offen offset:3072 lds\n" \
"v_mfma_f32_16x16x32_bf16 v[128:131], a[188:191], a[144:147], v[128:131]\n" \
"ds_read_b64_tr_b16 a[168:169], v34 offset:32768\n" \
"v_mfma_f32_16x16x32_bf16 v[128:131], a[252:255], a[208:211], v[128:131]\n" \
"ds_read_b128 a[144:147], v28 offset:32768\n" \
"v_mfma_f32_16x16x32_bf16 v[144:147], a[188:191], a[148:151], v[144:147]\n" \
"ds_read_b128 a[208:211], v29 offset:32768\n" \
"v_mfma_f32_16x16x32_bf16 v[144:147], a[252:255], a[212:215], v[144:147]\n" \
"s_add_u32 m0, s30, 0x1000\n" \
"s_mov_b32 s84, 0x9000\n" \
"v_mfma_f32_16x16x32_bf16 v[140:143], a[184:187], a[148:151], v[140:143]\n" \
"buffer_load_dwordx4 v24, s[76:79], s84 offen offset:0 lds\n" \
"v_mfma_f32_16x16x32_bf16 v[140:143], a[248:251], a[212:215], v[140:143]\n" \
"ds_read_b64_tr_b16 a[170:171], v34 offset:33792\n" \
"v_mfma_f32_16x16x32_bf16 v[136:139], a[180:183], a[148:151], v[136:139]\n" \
"s_addk_i32 s84, 0x2000\n" \
"s_nop 0\n" \
"v_mfma_f32_16x16x32_bf16 v[136:139], a[244:247], a[212:215], v[136:139]\n" \
"buffer_load_dwordx4 v25, s[76:79], s84 offen offset:1024 lds\n" \
"v_mfma_f32_16x16x32_bf16 v[132:135], a[176:179], a[148:151], v[132:135]\n" \
"ds_read_b64_tr_b16 a[232:233], v34 offset:40960\n" \
"v_mfma_f32_16x16x32_bf16 v[132:135], a[240:243], a[212:215], v[132:135]\n" \
"ds_read_b128 a[148:151], v28 offset:34816\n" \
"v_mfma_f32_16x16x32_bf16 v[148:151], a[176:179], a[152:155], v[148:151]\n" \
"ds_read_b128 a[212:215], v29 offset:34816\n" \
"v_mfma_f32_16x16x32_bf16 v[148:151], a[240:243], a[216:219], v[148:151]\n" \
"ds_read_b64_tr_b16 a[234:235], v34 offset:41984\n" \
"v_mfma_f32_16x16x32_bf16 v[152:155], a[180:183], a[152:155], v[152:155]\n" \
"ds_read_b64_tr_b16 a[172:173], v35 offset:32768\n" \
"v_mfma_f32_16x16x32_bf16 v[152:155], a[244:247], a[216:219], v[152:155]\n" \
"ds_read_b64_tr_b16 a[174:175], v35 offset:33792\n" \
"v_mfma_f32_16x16x32_bf16 v[156:159], a[184:187], a[152:155], v[156:159]\n" \
"ds_read_b64_tr_b16 a[236:237], v35 offset:40960\n" \
"v_mfma_f32_16x16x32_bf16 v[156:159], a[248:251], a[216:219], v[156:159]\n" \
"ds_read_b64_tr_b16 a[238:239], v35 offset:41984\n" \
"v_mfma_f32_16x16x32_bf16 v[160:163], a[188:191], a[152:155], v[160:163]\n" \
"v_mfma_f32_16x16x32_bf16 v[160:163], a[252:255], a[216:219], v[160:163]\n" \
"ds_read_b128 a[152:155], v28 offset:36864\n" \
"v_mfma_f32_16x16x32_bf16 v[176:179], a[188:191], a[156:159], v[176:179]\n" \
"ds_read_b128 a[216:219], v29 offset:36864\n" \
"v_mfma_f32_16x16x32_bf16 v[176:179], a[252:255], a[220:223], v[176:179]\n" \
"v_mfma_f32_16x16x32_bf16 v[172:175], a[184:187], a[156:159], v[172:175]\n" \
"v_mfma_f32_16x16x32_bf16 v[172:175], a[248:251], a[220:223], v[172:175]\n" \
"v_mfma_f32_16x16x32_bf16 v[168:171], a[180:183], a[156:159], v[168:171]\n" \
"v_mfma_f32_16x16x32_bf16 v[168:171], a[244:247], a[220:223], v[168:171]\n" \
"v_mfma_f32_16x16x32_bf16 v[164:167], a[176:179], a[156:159], v[164:167]\n" \
"v_mfma_f32_16x16x32_bf16 v[164:167], a[240:243], a[220:223], v[164:167]\n" \
"ds_read_b128 a[156:159], v28 offset:38912\n" \
"ds_read_b128 a[220:223], v29 offset:38912\n" \
"s_waitcnt lgkmcnt(15)\n" \
"v_mfma_f32_16x16x32_bf16 a[0:3], a[160:163], a[128:131], a[0:3]\n" \
"s_addk_i32 s84, 0x2000\n" \
"v_mfma_f32_16x16x32_bf16 a[0:3], a[224:227], a[192:195], a[0:3]\n" \
"buffer_load_dwordx4 v24, s[76:79], s84 offen offset:2048 lds\n" \
"v_mfma_f32_16x16x32_bf16 a[4:7], a[164:167], a[128:131], a[4:7]\n" \
"ds_read_b64_tr_b16 a[176:177], v36 offset:32768\n" \
"v_mfma_f32_16x16x32_bf16 a[4:7], a[228:231], a[192:195], a[4:7]\n" \
"s_addk_i32 s84, 0x2000\n" \
"s_waitcnt lgkmcnt(13)\n" \
"v_mfma_f32_16x16x32_bf16 a[8:11], a[168:171], a[128:131], a[8:11]\n" \
"buffer_load_dwordx4 v25, s[76:79], s84 offen offset:3072 lds\n" \
"s_waitcnt lgkmcnt(9)\n" \
"v_mfma_f32_16x16x32_bf16 a[8:11], a[232:235], a[192:195], a[8:11]\n" \
"ds_read_b64_tr_b16 a[178:179], v36 offset:33792\n" \
"s_waitcnt lgkmcnt(8)\n" \
"v_mfma_f32_16x16x32_bf16 a[12:15], a[172:175], a[128:131], a[12:15]\n" \
"ds_read_b64_tr_b16 a[240:241], v36 offset:40960\n" \
"v_accvgpr_read_b32 v184, a0\n" \
"s_waitcnt lgkmcnt(7)\n" \
"s_nop 0\n" \
"v_mfma_f32_16x16x32_bf16 a[12:15], a[236:239], a[192:195], a[12:15]\n" \
"v_accvgpr_read_b32 v185, a1\n" \
"v_accvgpr_read_b32 v186, a2\n" \
"v_mfma_f32_16x16x32_bf16 a[28:31], a[172:175], a[132:135], a[28:31]\n" \
"v_accvgpr_read_b32 v187, a3\n" \
"v_accvgpr_read_b32 v180, a4\n" \
"v_mfma_f32_16x16x32_bf16 a[28:31], a[236:239], a[196:199], a[28:31]\n" \
"ds_read_b64_tr_b16 a[242:243], v36 offset:41984\n" \
"v_accvgpr_read_b32 v181, a5\n" \
"v_mfma_f32_16x16x32_bf16 a[24:27], a[168:171], a[132:135], a[24:27]\n" \
"v_accvgpr_read_b32 v182, a6\n" \
"v_accvgpr_read_b32 v183, a7\n" \
"v_mfma_f32_16x16x32_bf16 a[24:27], a[232:235], a[196:199], a[24:27]\n" \
"v_cvt_pk_bf16_f32 v184, v184, v185\n" \
"v_cvt_pk_bf16_f32 v185, v186, v187\n" \
"v_mfma_f32_16x16x32_bf16 a[20:23], a[164:167], a[132:135], a[20:23]\n" \
"v_cvt_pk_bf16_f32 v186, v180, v181\n" \
"v_cvt_pk_bf16_f32 v187, v182, v183\n" \
"v_mfma_f32_16x16x32_bf16 a[20:23], a[228:231], a[196:199], a[20:23]\n" \
"ds_read_b64_tr_b16 a[180:181], v37 offset:32768\n" \
"v_accvgpr_read_b32 v188, a8\n" \
"v_mfma_f32_16x16x32_bf16 a[16:19], a[160:163], a[132:135], a[16:19]\n" \
"v_accvgpr_read_b32 v189, a9\n" \
"v_accvgpr_read_b32 v190, a10\n" \
"v_mfma_f32_16x16x32_bf16 a[16:19], a[224:227], a[196:199], a[16:19]\n" \
"v_accvgpr_read_b32 v191, a11\n" \
"v_accvgpr_read_b32 v180, a12\n" \
"v_mfma_f32_16x16x32_bf16 a[32:35], a[160:163], a[136:139], a[32:35]\n" \
"ds_read_b64_tr_b16 a[182:183], v37 offset:33792\n" \
"v_accvgpr_read_b32 v181, a13\n" \
"v_mfma_f32_16x16x32_bf16 a[32:35], a[224:227], a[200:203], a[32:35]\n" \
"v_accvgpr_read_b32 v182, a14\n" \
"v_accvgpr_read_b32 v183, a15\n" \
"v_mfma_f32_16x16x32_bf16 a[36:39], a[164:167], a[136:139], a[36:39]\n" \
"v_lshlrev_b32_e32 v45, 3, v20\n" \
"v_add_u32_e32 v45, s31, v45\n" \
"v_mfma_f32_16x16x32_bf16 a[36:39], a[228:231], a[200:203], a[36:39]\n" \
"s_getpc_b64 s[86:87]\n" \
"s_sub_u32 s86, s86, 0xf800\n" \
"s_subb_u32 s87, s87, 0\n" \
"s_mov_b32 m0, s32\n" \
"s_nop 0\n" \
"v_mfma_f32_16x16x32_bf16 a[40:43], a[168:171], a[136:139], a[40:43]\n" \
"global_load_lds_dword v45, s[86:87]\n" \
"v_mfma_f32_16x16x32_bf16 a[40:43], a[232:235], a[200:203], a[40:43]\n" \
"ds_read_b64_tr_b16 a[244:245], v37 offset:40960\n" \
"v_cvt_pk_bf16_f32 v188, v188, v189\n" \
"v_mfma_f32_16x16x32_bf16 a[44:47], a[172:175], a[136:139], a[44:47]\n" \
"ds_read_b64_tr_b16 a[246:247], v37 offset:41984\n" \
"v_cvt_pk_bf16_f32 v189, v190, v191\n" \
"v_mfma_f32_16x16x32_bf16 a[44:47], a[236:239], a[200:203], a[44:47]\n" \
"v_cvt_pk_bf16_f32 v190, v180, v181\n" \
"v_cvt_pk_bf16_f32 v191, v182, v183\n" \
"v_mfma_f32_16x16x32_bf16 a[60:63], a[172:175], a[140:143], a[60:63]\n" \
"v_accvgpr_read_b32 v192, a24\n" \
"v_accvgpr_read_b32 v193, a25\n" \
"v_mfma_f32_16x16x32_bf16 a[60:63], a[236:239], a[204:207], a[60:63]\n" \
"v_accvgpr_read_b32 v194, a26\n" \
"v_accvgpr_read_b32 v195, a27\n" \
"v_mfma_f32_16x16x32_bf16 a[56:59], a[168:171], a[140:143], a[56:59]\n" \
"ds_read_b64_tr_b16 a[184:185], v38 offset:32768\n" \
"v_accvgpr_read_b32 v180, a28\n" \
"v_mfma_f32_16x16x32_bf16 a[56:59], a[232:235], a[204:207], a[56:59]\n" \
"v_accvgpr_read_b32 v181, a29\n" \
"v_accvgpr_read_b32 v182, a30\n" \
"v_mfma_f32_16x16x32_bf16 a[52:55], a[164:167], a[140:143], a[52:55]\n" \
"v_accvgpr_read_b32 v183, a31\n" \
"v_cvt_pk_bf16_f32 v192, v192, v193\n" \
"v_mfma_f32_16x16x32_bf16 a[52:55], a[228:231], a[204:207], a[52:55]\n" \
"ds_read_b64_tr_b16 a[186:187], v38 offset:33792\n" \
"v_cvt_pk_bf16_f32 v193, v194, v195\n" \
"v_mfma_f32_16x16x32_bf16 a[48:51], a[160:163], a[140:143], a[48:51]\n" \
"v_cvt_pk_bf16_f32 v194, v180, v181\n" \
"v_cvt_pk_bf16_f32 v195, v182, v183\n" \
"v_mfma_f32_16x16x32_bf16 a[48:51], a[224:227], a[204:207], a[48:51]\n" \
"v_accvgpr_read_b32 v196, a16\n" \
"v_accvgpr_read_b32 v197, a17\n" \
"v_mfma_f32_16x16x32_bf16 a[64:67], a[160:163], a[144:147], a[64:67]\n" \
"ds_read_b64_tr_b16 a[248:249], v38 offset:40960\n" \
"v_accvgpr_read_b32 v198, a18\n" \
"v_mfma_f32_16x16x32_bf16 a[64:67], a[224:227], a[208:211], a[64:67]\n" \
"v_accvgpr_read_b32 v199, a19\n" \
"v_accvgpr_read_b32 v180, a20\n" \
"v_mfma_f32_16x16x32_bf16 a[68:71], a[164:167], a[144:147], a[68:71]\n" \
"v_accvgpr_read_b32 v181, a21\n" \
"v_accvgpr_read_b32 v182, a22\n" \
"v_mfma_f32_16x16x32_bf16 a[68:71], a[228:231], a[208:211], a[68:71]\n" \
"ds_read_b64_tr_b16 a[250:251], v38 offset:41984\n" \
"v_accvgpr_read_b32 v183, a23\n" \
"v_mfma_f32_16x16x32_bf16 a[72:75], a[168:171], a[144:147], a[72:75]\n" \
"v_cvt_pk_bf16_f32 v196, v196, v197\n" \
"v_cvt_pk_bf16_f32 v197, v198, v199\n" \
"v_mfma_f32_16x16x32_bf16 a[72:75], a[232:235], a[208:211], a[72:75]\n" \
"v_cvt_pk_bf16_f32 v198, v180, v181\n" \
"v_cvt_pk_bf16_f32 v199, v182, v183\n" \
"v_mfma_f32_16x16x32_bf16 a[76:79], a[172:175], a[144:147], a[76:79]\n" \
"v_accvgpr_read_b32 v200, a32\n" \
"v_accvgpr_read_b32 v201, a33\n" \
"v_mfma_f32_16x16x32_bf16 a[76:79], a[236:239], a[208:211], a[76:79]\n" \
"ds_read_b64_tr_b16 a[188:189], v39 offset:32768\n" \
"v_accvgpr_read_b32 v202, a34\n" \
"v_mfma_f32_16x16x32_bf16 a[92:95], a[172:175], a[148:151], a[92:95]\n" \
"s_add_u32 s89, s41, 0x0\n" \
"s_nop 0\n" \
"buffer_store_dwordx4 v[184:187], v20, s[80:83], s89 offen offset:0 sc1\n" \
"v_mfma_f32_16x16x32_bf16 a[92:95], a[236:239], a[212:215], a[92:95]\n" \
"v_accvgpr_read_b32 v203, a35\n" \
"v_accvgpr_read_b32 v180, a36\n" \
"v_mfma_f32_16x16x32_bf16 a[88:91], a[168:171], a[148:151], a[88:91]\n" \
"ds_read_b64_tr_b16 a[190:191], v39 offset:33792\n" \
"v_accvgpr_read_b32 v181, a37\n" \
"v_mfma_f32_16x16x32_bf16 a[88:91], a[232:235], a[212:215], a[88:91]\n" \
"v_accvgpr_read_b32 v182, a38\n" \
"v_accvgpr_read_b32 v183, a39\n" \
"v_mfma_f32_16x16x32_bf16 a[84:87], a[164:167], a[148:151], a[84:87]\n" \
"v_cvt_pk_bf16_f32 v200, v200, v201\n" \
"v_cvt_pk_bf16_f32 v201, v202, v203\n" \
"v_mfma_f32_16x16x32_bf16 a[84:87], a[228:231], a[212:215], a[84:87]\n" \
"ds_read_b64_tr_b16 a[252:253], v39 offset:40960\n" \
"v_cvt_pk_bf16_f32 v202, v180, v181\n" \
"v_mfma_f32_16x16x32_bf16 a[80:83], a[160:163], a[148:151], a[80:83]\n" \
"s_add_u32 s88, s41, 0x0\n" \
"buffer_store_dwordx4 v[188:191], v20, s[80:83], s88 offen offset:1024 sc1\n" \
"v_mfma_f32_16x16x32_bf16 a[80:83], a[224:227], a[212:215], a[80:83]\n" \
"v_cvt_pk_bf16_f32 v203, v182, v183\n" \
"v_accvgpr_read_b32 v204, a40\n" \
"s_waitcnt lgkmcnt(15)\n" \
"v_mfma_f32_16x16x32_bf16 a[96:99], a[160:163], a[152:155], a[96:99]\n" \
"ds_read_b64_tr_b16 a[254:255], v39 offset:41984\n" \
"v_accvgpr_read_b32 v205, a41\n" \
"v_mfma_f32_16x16x32_bf16 a[96:99], a[224:227], a[216:219], a[96:99]\n" \
"v_accvgpr_read_b32 v206, a42\n" \
"v_accvgpr_read_b32 v207, a43\n" \
"v_mfma_f32_16x16x32_bf16 a[100:103], a[164:167], a[152:155], a[100:103]\n" \
"v_accvgpr_read_b32 v180, a44\n" \
"v_accvgpr_read_b32 v181, a45\n" \
"v_mfma_f32_16x16x32_bf16 a[100:103], a[228:231], a[216:219], a[100:103]\n" \
"v_accvgpr_read_b32 v182, a46\n" \
"v_accvgpr_read_b32 v183, a47\n" \
"v_mfma_f32_16x16x32_bf16 a[104:107], a[168:171], a[152:155], a[104:107]\n" \
"s_add_u32 s89, s41, 0x1000\n" \
"buffer_store_dwordx4 v[192:195], v20, s[80:83], s89 offen offset:1024 sc1\n" \
"v_mfma_f32_16x16x32_bf16 a[104:107], a[232:235], a[216:219], a[104:107]\n" \
"v_cvt_pk_bf16_f32 v204, v204, v205\n" \
"v_cvt_pk_bf16_f32 v205, v206, v207\n" \
"v_mfma_f32_16x16x32_bf16 a[108:111], a[172:175], a[152:155], a[108:111]\n" \
"v_cvt_pk_bf16_f32 v206, v180, v181\n" \
"v_cvt_pk_bf16_f32 v207, v182, v183\n" \
"v_mfma_f32_16x16x32_bf16 a[108:111], a[236:239], a[216:219], a[108:111]\n" \
"v_accvgpr_read_b32 v208, a56\n" \
"v_accvgpr_read_b32 v209, a57\n" \
"v_mfma_f32_16x16x32_bf16 a[124:127], a[172:175], a[156:159], a[124:127]\n" \
"v_accvgpr_read_b32 v210, a58\n" \
"v_accvgpr_read_b32 v211, a59\n" \
"v_mfma_f32_16x16x32_bf16 a[124:127], a[236:239], a[220:223], a[124:127]\n" \
"v_accvgpr_read_b32 v180, a60\n" \
"v_accvgpr_read_b32 v181, a61\n" \
"v_mfma_f32_16x16x32_bf16 a[120:123], a[168:171], a[156:159], a[120:123]\n" \
"s_add_u32 s88, s41, 0x1000\n" \
"buffer_store_dwordx4 v[196:199], v20, s[80:83], s88 offen offset:0 sc1\n" \
"v_mfma_f32_16x16x32_bf16 a[120:123], a[232:235], a[220:223], a[120:123]\n" \
"v_accvgpr_read_b32 v182, a62\n" \
"v_accvgpr_read_b32 v183, a63\n" \
"v_mfma_f32_16x16x32_bf16 a[116:119], a[164:167], a[156:159], a[116:119]\n" \
"v_cvt_pk_bf16_f32 v208, v208, v209\n" \
"v_cvt_pk_bf16_f32 v209, v210, v211\n" \
"v_mfma_f32_16x16x32_bf16 a[116:119], a[228:231], a[220:223], a[116:119]\n" \
"v_cvt_pk_bf16_f32 v210, v180, v181\n" \
"v_cvt_pk_bf16_f32 v211, v182, v183\n" \
"v_mfma_f32_16x16x32_bf16 a[112:115], a[160:163], a[156:159], a[112:115]\n" \
"v_accvgpr_read_b32 v212, a48\n" \
"v_accvgpr_read_b32 v213, a49\n" \
"v_mfma_f32_16x16x32_bf16 a[112:115], a[224:227], a[220:223], a[112:115]\n" \
"v_accvgpr_read_b32 v214, a50\n" \
"v_accvgpr_read_b32 v215, a51\n" \
"s_waitcnt vmcnt(5) lgkmcnt(0)\n" \
"s_barrier\n" \
"v_mov_b32_e32 v36, v32\n" \
"v_xor_b32_e32 v32, 0x0, v48\n" \
"v_mov_b32_e32 v37, v33\n" \
"v_xor_b32_e32 v33, 0x20, v48\n" \
"v_mov_b32_e32 v38, v34\n" \
"v_xor_b32_e32 v34, 0x40, v48\n" \
"v_mov_b32_e64 v39, v35\n" \
"v_xor_b32_e32 v35, 0x60, v48\n" \
"v_add_u32_e32 v22, s44, v28\n" \
"v_add_u32_e32 v28, s43, v28\n" \
"v_add_u32_e32 v23, s44, v29\n" \
"v_add_u32_e32 v29, s43, v29\n" \
"v_mfma_f32_16x16x32_bf16 v[52:55], a[176:179], a[128:131], v[52:55]\n" \
"s_add_u32 m0, s40, 0x8000\n" \
"s_add_u32 s85, s39, 0x2000\n" \
"v_mfma_f32_16x16x32_bf16 v[52:55], a[240:243], a[192:195], v[52:55]\n" \
"buffer_load_dwordx4 v20, s[80:83], s85 offen sc1 lds\n" \
"v_mfma_f32_16x16x32_bf16 v[56:59], a[180:183], a[128:131], v[56:59]\n" \
"ds_read_b64_tr_b16 a[160:161], v32 offset:0\n" \
"v_accvgpr_read_b32 v180, a52\n" \
"v_mfma_f32_16x16x32_bf16 v[56:59], a[244:247], a[192:195], v[56:59]\n" \
"s_add_u32 m0, s40, 0x9000\n" \
"s_add_u32 s85, s39, 0x3000\n" \
"v_mfma_f32_16x16x32_bf16 v[60:63], a[184:187], a[128:131], v[60:63]\n" \
"buffer_load_dwordx4 v20, s[80:83], s85 offen sc1 lds\n" \
"v_mfma_f32_16x16x32_bf16 v[60:63], a[248:251], a[192:195], v[60:63]\n" \
"ds_read_b64_tr_b16 a[162:163], v32 offset:512\n" \
"v_accvgpr_read_b32 v181, a53\n" \
"v_mfma_f32_16x16x32_bf16 v[64:67], a[188:191], a[128:131], v[64:67]\n" \
"ds_read_b64_tr_b16 a[224:225], v32 offset:4096\n" \
"v_accvgpr_read_b32 v182, a54\n" \
"v_mfma_f32_16x16x32_bf16 v[64:67], a[252:255], a[192:195], v[64:67]\n" \
"ds_read_b128 a[128:131], v28 offset:0\n" \
"v_accvgpr_read_b32 v183, a55\n" \
"v_mfma_f32_16x16x32_bf16 v[80:83], a[188:191], a[132:135], v[80:83]\n" \
"ds_read_b128 a[192:195], v29 offset:0\n" \
"v_cvt_pk_bf16_f32 v212, v212, v213\n" \
"v_mfma_f32_16x16x32_bf16 v[80:83], a[252:255], a[196:199], v[80:83]\n" \
"s_add_u32 m0, s40, 0xb000\n" \
"s_movk_i32 s84, 0x80\n" \
"v_mfma_f32_16x16x32_bf16 v[76:79], a[184:187], a[132:135], v[76:79]\n" \
"buffer_load_dwordx4 v21, s[76:79], s84 offen offset:0 lds\n" \
"v_mfma_f32_16x16x32_bf16 v[76:79], a[248:251], a[196:199], v[76:79]\n" \
"s_add_u32 m0, s30, 0x8000\n" \
"s_movk_i32 s84, 0x80\n" \
"v_mfma_f32_16x16x32_bf16 v[72:75], a[180:183], a[132:135], v[72:75]\n" \
"buffer_load_dwordx4 v24, s[76:79], s84 offen offset:0 lds\n" \
"v_mfma_f32_16x16x32_bf16 v[72:75], a[244:247], a[196:199], v[72:75]\n" \
"ds_read_b64_tr_b16 a[226:227], v32 offset:4608\n" \
"v_cvt_pk_bf16_f32 v213, v214, v215\n" \
"v_mfma_f32_16x16x32_bf16 v[68:71], a[176:179], a[132:135], v[68:71]\n" \
"ds_read_b64_tr_b16 a[164:165], v33 offset:0\n" \
"v_cvt_pk_bf16_f32 v214, v180, v181\n" \
"v_mfma_f32_16x16x32_bf16 v[68:71], a[240:243], a[196:199], v[68:71]\n" \
"ds_read_b128 a[132:135], v28 offset:2048\n" \
"v_cvt_pk_bf16_f32 v215, v182, v183\n" \
"v_mfma_f32_16x16x32_bf16 v[84:87], a[176:179], a[136:139], v[84:87]\n" \
"ds_read_b128 a[196:199], v29 offset:2048\n" \
"v_accvgpr_read_b32 v216, a64\n" \
"v_mfma_f32_16x16x32_bf16 v[84:87], a[240:243], a[200:203], v[84:87]\n" \
"s_movk_i32 s84, 0x2080\n" \
"v_mfma_f32_16x16x32_bf16 v[88:91], a[180:183], a[136:139], v[88:91]\n" \
"buffer_load_dwordx4 v25, s[76:79], s84 offen offset:1024 lds\n" \
"v_mfma_f32_16x16x32_bf16 v[88:91], a[244:247], a[200:203], v[88:91]\n" \
"s_movk_i32 s84, 0x4080\n" \
"v_mfma_f32_16x16x32_bf16 v[92:95], a[184:187], a[136:139], v[92:95]\n" \
"buffer_load_dwordx4 v24, s[76:79], s84 offen offset:2048 lds\n" \
"v_mfma_f32_16x16x32_bf16 v[92:95], a[248:251], a[200:203], v[92:95]\n" \
"ds_read_b64_tr_b16 a[166:167], v33 offset:512\n" \
"v_accvgpr_read_b32 v217, a65\n" \
"v_mfma_f32_16x16x32_bf16 v[96:99], a[188:191], a[136:139], v[96:99]\n" \
"ds_read_b64_tr_b16 a[228:229], v33 offset:4096\n" \
"v_accvgpr_read_b32 v218, a66\n" \
"v_mfma_f32_16x16x32_bf16 v[96:99], a[252:255], a[200:203], v[96:99]\n" \
"ds_read_b128 a[136:139], v28 offset:4096\n" \
"v_accvgpr_read_b32 v219, a67\n" \
"v_mfma_f32_16x16x32_bf16 v[112:115], a[188:191], a[140:143], v[112:115]\n" \
"ds_read_b128 a[200:203], v29 offset:4096\n" \
"v_accvgpr_read_b32 v180, a68\n" \
"v_mfma_f32_16x16x32_bf16 v[112:115], a[252:255], a[204:207], v[112:115]\n" \
"s_movk_i32 s84, 0x6080\n" \
"v_mfma_f32_16x16x32_bf16 v[108:111], a[184:187], a[140:143], v[108:111]\n" \
"buffer_load_dwordx4 v25, s[76:79], s84 offen offset:3072 lds\n" \
"v_mfma_f32_16x16x32_bf16 v[108:111], a[248:251], a[204:207], v[108:111]\n" \
"s_add_u32 m0, s31, 0x8000\n" \
"s_mov_b32 s84, 0x80000\n" \
"v_mfma_f32_16x16x32_bf16 v[104:107], a[180:183], a[140:143], v[104:107]\n" \
"buffer_load_dwordx4 v26, s[68:71], s84 offen offset:0 lds\n" \
"v_mfma_f32_16x16x32_bf16 v[104:107], a[244:247], a[204:207], v[104:107]\n" \
"s_addk_i32 s84, 0x7c00\n" \
"v_mfma_f32_16x16x32_bf16 v[100:103], a[176:179], a[140:143], v[100:103]\n" \
"buffer_load_dwordx4 v26, s[68:71], s84 offen offset:1024 lds\n" \
"v_mfma_f32_16x16x32_bf16 v[100:103], a[240:243], a[204:207], v[100:103]\n" \
"ds_read_b128 a[140:143], v28 offset:6144\n" \
"v_accvgpr_read_b32 v181, a69\n" \
"v_mfma_f32_16x16x32_bf16 v[116:119], a[176:179], a[144:147], v[116:119]\n" \
"ds_read_b128 a[204:207], v29 offset:6144\n" \
"v_accvgpr_read_b32 v182, a70\n" \
"v_mfma_f32_16x16x32_bf16 v[116:119], a[240:243], a[208:211], v[116:119]\n" \
"s_addk_i32 s84, 0x7c00\n" \
"v_mfma_f32_16x16x32_bf16 v[120:123], a[180:183], a[144:147], v[120:123]\n" \
"buffer_load_dwordx4 v27, s[68:71], s84 offen offset:2048 lds\n" \
"v_mfma_f32_16x16x32_bf16 v[120:123], a[244:247], a[208:211], v[120:123]\n" \
"ds_read_b64_tr_b16 a[230:231], v33 offset:4608\n" \
"v_accvgpr_read_b32 v183, a71\n" \
"v_mfma_f32_16x16x32_bf16 v[124:127], a[184:187], a[144:147], v[124:127]\n" \
"s_addk_i32 s84, 0x7c00\n" \
"v_mfma_f32_16x16x32_bf16 v[124:127], a[248:251], a[208:211], v[124:127]\n" \
"buffer_load_dwordx4 v27, s[68:71], s84 offen offset:3072 lds\n" \
"v_mfma_f32_16x16x32_bf16 v[128:131], a[188:191], a[144:147], v[128:131]\n" \
"ds_read_b64_tr_b16 a[172:173], v35 offset:0\n" \
"v_cvt_pk_bf16_f32 v216, v216, v217\n" \
"v_mfma_f32_16x16x32_bf16 v[128:131], a[252:255], a[208:211], v[128:131]\n" \
"ds_read_b128 a[144:147], v28 offset:8192\n" \
"v_cvt_pk_bf16_f32 v217, v218, v219\n" \
"v_mfma_f32_16x16x32_bf16 v[144:147], a[188:191], a[148:151], v[144:147]\n" \
"ds_read_b128 a[208:211], v29 offset:8192\n" \
"v_cvt_pk_bf16_f32 v218, v180, v181\n" \
"v_mfma_f32_16x16x32_bf16 v[144:147], a[252:255], a[212:215], v[144:147]\n" \
"s_add_u32 m0, s30, 0x9000\n" \
"s_mov_b32 s84, 0x9080\n" \
"v_mfma_f32_16x16x32_bf16 v[140:143], a[184:187], a[148:151], v[140:143]\n" \
"buffer_load_dwordx4 v24, s[76:79], s84 offen offset:0 lds\n" \
"v_mfma_f32_16x16x32_bf16 v[140:143], a[248:251], a[212:215], v[140:143]\n" \
"ds_read_b64_tr_b16 a[174:175], v35 offset:512\n" \
"v_cvt_pk_bf16_f32 v219, v182, v183\n" \
"v_mfma_f32_16x16x32_bf16 v[136:139], a[180:183], a[148:151], v[136:139]\n" \
"s_addk_i32 s84, 0x2000\n" \
"s_nop 0\n" \
"v_mfma_f32_16x16x32_bf16 v[136:139], a[244:247], a[212:215], v[136:139]\n" \
"buffer_load_dwordx4 v25, s[76:79], s84 offen offset:1024 lds\n" \
"v_mfma_f32_16x16x32_bf16 v[132:135], a[176:179], a[148:151], v[132:135]\n" \
"ds_read_b64_tr_b16 a[236:237], v35 offset:4096\n" \
"v_accvgpr_read_b32 v220, a80\n" \
"v_mfma_f32_16x16x32_bf16 v[132:135], a[240:243], a[212:215], v[132:135]\n" \
"ds_read_b128 a[148:151], v28 offset:10240\n" \
"v_accvgpr_read_b32 v221, a81\n" \
"v_mfma_f32_16x16x32_bf16 v[148:151], a[176:179], a[152:155], v[148:151]\n" \
"ds_read_b128 a[212:215], v29 offset:10240\n" \
"v_accvgpr_read_b32 v222, a82\n" \
"v_mfma_f32_16x16x32_bf16 v[148:151], a[240:243], a[216:219], v[148:151]\n" \
"ds_read_b64_tr_b16 a[238:239], v35 offset:4608\n" \
"v_accvgpr_read_b32 v223, a83\n" \
"v_mfma_f32_16x16x32_bf16 v[152:155], a[180:183], a[152:155], v[152:155]\n" \
"ds_read_b64_tr_b16 a[168:169], v34 offset:0\n" \
"v_accvgpr_read_b32 v180, a84\n" \
"v_mfma_f32_16x16x32_bf16 v[152:155], a[244:247], a[216:219], v[152:155]\n" \
"ds_read_b64_tr_b16 a[170:171], v34 offset:512\n" \
"v_accvgpr_read_b32 v181, a85\n" \
"v_mfma_f32_16x16x32_bf16 v[156:159], a[184:187], a[152:155], v[156:159]\n" \
"ds_read_b64_tr_b16 a[232:233], v34 offset:4096\n" \
"v_accvgpr_read_b32 v182, a86\n" \
"v_mfma_f32_16x16x32_bf16 v[156:159], a[248:251], a[216:219], v[156:159]\n" \
"ds_read_b64_tr_b16 a[234:235], v34 offset:4608\n" \
"v_accvgpr_read_b32 v183, a87\n" \
"v_mfma_f32_16x16x32_bf16 v[160:163], a[188:191], a[152:155], v[160:163]\n" \
"ds_read_b128 v[248:251], v22 offset:0\n" \
"v_cvt_pk_bf16_f32 v220, v220, v221\n" \
"v_mfma_f32_16x16x32_bf16 v[160:163], a[252:255], a[216:219], v[160:163]\n" \
"ds_read_b128 a[152:155], v28 offset:12288\n" \
"v_cvt_pk_bf16_f32 v221, v222, v223\n" \
"v_mfma_f32_16x16x32_bf16 v[176:179], a[188:191], a[156:159], v[176:179]\n" \
"ds_read_b128 a[216:219], v29 offset:12288\n" \
"v_cvt_pk_bf16_f32 v222, v180, v181\n" \
"v_mfma_f32_16x16x32_bf16 v[176:179], a[252:255], a[220:223], v[176:179]\n" \
"ds_read_b128 v[48:51], v23 offset:0\n" \
"v_cvt_pk_bf16_f32 v223, v182, v183\n" \
"v_mfma_f32_16x16x32_bf16 v[172:175], a[184:187], a[156:159], v[172:175]\n" \
"v_cvt_pk_bf16_f32 v224, v52, v53\n" \
"v_cvt_pk_bf16_f32 v225, v54, v55\n" \
"v_mfma_f32_16x16x32_bf16 v[172:175], a[248:251], a[220:223], v[172:175]\n" \
"v_cvt_pk_bf16_f32 v226, v56, v57\n" \
"v_cvt_pk_bf16_f32 v227, v58, v59\n" \
"v_mfma_f32_16x16x32_bf16 v[168:171], a[180:183], a[156:159], v[168:171]\n" \
"v_cvt_pk_bf16_f32 v228, v60, v61\n" \
"v_cvt_pk_bf16_f32 v229, v62, v63\n" \
"v_mfma_f32_16x16x32_bf16 v[168:171], a[244:247], a[220:223], v[168:171]\n" \
"v_cvt_pk_bf16_f32 v230, v64, v65\n" \
"v_cvt_pk_bf16_f32 v231, v66, v67\n" \
"v_mfma_f32_16x16x32_bf16 v[164:167], a[176:179], a[156:159], v[164:167]\n" \
"v_cvt_pk_bf16_f32 v232, v76, v77\n" \
"v_cvt_pk_bf16_f32 v233, v78, v79\n" \
"v_mfma_f32_16x16x32_bf16 v[164:167], a[240:243], a[220:223], v[164:167]\n" \
"ds_read_b128 a[156:159], v28 offset:14336\n" \
"ds_read_b128 a[220:223], v29 offset:14336\n" \
"s_waitcnt lgkmcnt(15)\n" \
"v_mfma_f32_16x16x32_bf16 a[0:3], a[160:163], a[128:131], 0\n" \
"s_addk_i32 s84, 0x2000\n" \
"v_cvt_pk_bf16_f32 v234, v80, v81\n" \
"v_mfma_f32_16x16x32_bf16 a[0:3], a[224:227], a[192:195], a[0:3]\n" \
"buffer_load_dwordx4 v24, s[76:79], s84 offen offset:2048 lds\n" \
"v_cvt_pk_bf16_f32 v235, v82, v83\n" \
"v_mfma_f32_16x16x32_bf16 a[4:7], a[164:167], a[128:131], 0\n" \
"ds_read_b64_tr_b16 a[176:177], v36 offset:0\n" \
"v_cvt_pk_bf16_f32 v236, v68, v69\n" \
"v_mfma_f32_16x16x32_bf16 a[4:7], a[228:231], a[192:195], a[4:7]\n" \
"s_addk_i32 s84, 0x2000\n" \
"v_cvt_pk_bf16_f32 v237, v70, v71\n" \
"s_waitcnt lgkmcnt(15)\n" \
"v_mfma_f32_16x16x32_bf16 a[12:15], a[172:175], a[128:131], 0\n" \
"buffer_load_dwordx4 v25, s[76:79], s84 offen offset:3072 lds\n" \
"v_cvt_pk_bf16_f32 v238, v72, v73\n" \
"s_waitcnt lgkmcnt(11)\n" \
"v_mfma_f32_16x16x32_bf16 a[12:15], a[236:239], a[192:195], a[12:15]\n" \
"ds_read_b64_tr_b16 a[178:179], v36 offset:1024\n" \
"v_cvt_pk_bf16_f32 v239, v74, v75\n" \
"s_waitcnt lgkmcnt(10)\n" \
"v_mfma_f32_16x16x32_bf16 a[8:11], a[128:131], a[168:171], 0\n" \
"ds_read_b64_tr_b16 a[240:241], v36 offset:8192\n" \
"v_cvt_pk_bf16_f32 v240, v84, v85\n" \
"s_waitcnt lgkmcnt(9)\n" \
"s_nop 0\n" \
"v_mfma_f32_16x16x32_bf16 a[8:11], a[192:195], a[232:235], a[8:11]\n" \
"ds_read_b64_tr_b16 a[242:243], v36 offset:9216\n" \
"v_cvt_pk_bf16_f32 v241, v86, v87\n" \
"v_mfma_f32_16x16x32_bf16 a[24:27], a[132:135], a[168:171], 0\n" \
"ds_read_b64_tr_b16 a[180:181], v37 offset:0\n" \
"v_cvt_pk_bf16_f32 v242, v88, v89\n" \
"v_mfma_f32_16x16x32_bf16 a[24:27], a[196:199], a[232:235], a[24:27]\n" \
"ds_read_b64_tr_b16 a[182:183], v37 offset:1024\n" \
"v_cvt_pk_bf16_f32 v243, v90, v91\n" \
"v_mfma_f32_16x16x32_bf16 a[28:31], a[172:175], a[132:135], 0\n" \
"ds_read_b64_tr_b16 a[244:245], v37 offset:8192\n" \
"v_cvt_pk_bf16_f32 v244, v92, v93\n" \
"v_mfma_f32_16x16x32_bf16 a[28:31], a[236:239], a[196:199], a[28:31]\n" \
"s_add_u32 s89, s41, 0x2000\n" \
"buffer_store_dwordx4 v[200:203], v20, s[80:83], s89 offen offset:0 sc1\n" \
"v_mfma_f32_16x16x32_bf16 a[20:23], a[164:167], a[132:135], 0\n" \
"ds_read_b64_tr_b16 a[246:247], v37 offset:9216\n" \
"v_cvt_pk_bf16_f32 v245, v94, v95\n" \
"v_mfma_f32_16x16x32_bf16 a[20:23], a[228:231], a[196:199], a[20:23]\n" \
"ds_read_b64_tr_b16 a[184:185], v38 offset:0\n" \
"v_cvt_pk_bf16_f32 v246, v96, v97\n" \
"v_mfma_f32_16x16x32_bf16 a[16:19], a[160:163], a[132:135], 0\n" \
"ds_read_b64_tr_b16 a[186:187], v38 offset:1024\n" \
"v_cvt_pk_bf16_f32 v247, v98, v99\n" \
"v_mfma_f32_16x16x32_bf16 a[16:19], a[224:227], a[196:199], a[16:19]\n" \
"v_cvt_pk_bf16_f32 v184, v108, v109\n" \
"v_cvt_pk_bf16_f32 v185, v110, v111\n" \
"v_cvt_pk_bf16_f32 v186, v112, v113\n" \
"v_mfma_f32_16x16x32_bf16 a[32:35], a[160:163], a[136:139], 0\n" \
"s_add_u32 s88, s41, 0x2000\n" \
"buffer_store_dwordx4 v[204:207], v20, s[80:83], s88 offen offset:1024 sc1\n" \
"v_mfma_f32_16x16x32_bf16 a[32:35], a[224:227], a[200:203], a[32:35]\n" \
"ds_read_b64_tr_b16 a[248:249], v38 offset:8192\n" \
"v_cvt_pk_bf16_f32 v187, v114, v115\n" \
"v_mfma_f32_16x16x32_bf16 a[36:39], a[164:167], a[136:139], 0\n" \
"ds_read_b64_tr_b16 a[250:251], v38 offset:9216\n" \
"v_cvt_pk_bf16_f32 v188, v100, v101\n" \
"v_mfma_f32_16x16x32_bf16 a[36:39], a[228:231], a[200:203], a[36:39]\n" \
"ds_read_b64_tr_b16 a[188:189], v39 offset:0\n" \
"v_cvt_pk_bf16_f32 v189, v102, v103\n" \
"v_mfma_f32_16x16x32_bf16 a[44:47], a[172:175], a[136:139], 0\n" \
"v_cvt_pk_bf16_f32 v190, v104, v105\n" \
"v_cvt_pk_bf16_f32 v191, v106, v107\n" \
"v_cvt_pk_bf16_f32 v192, v116, v117\n" \
"v_mfma_f32_16x16x32_bf16 a[44:47], a[236:239], a[200:203], a[44:47]\n" \
"ds_read_b64_tr_b16 a[190:191], v39 offset:1024\n" \
"v_cvt_pk_bf16_f32 v193, v118, v119\n" \
"v_mfma_f32_16x16x32_bf16 a[40:43], a[136:139], a[168:171], 0\n" \
"s_add_u32 s89, s41, 0x3000\n" \
"buffer_store_dwordx4 v[208:211], v20, s[80:83], s89 offen offset:1024 sc1\n" \
"v_mfma_f32_16x16x32_bf16 a[40:43], a[200:203], a[232:235], a[40:43]\n" \
"ds_read_b64_tr_b16 a[252:253], v39 offset:8192\n" \
"v_cvt_pk_bf16_f32 v194, v120, v121\n" \
"v_mfma_f32_16x16x32_bf16 a[56:59], a[140:143], a[168:171], 0\n" \
"ds_read_b64_tr_b16 a[254:255], v39 offset:9216\n" \
"v_cvt_pk_bf16_f32 v195, v122, v123\n" \
"v_mfma_f32_16x16x32_bf16 a[56:59], a[204:207], a[232:235], a[56:59]\n" \
"v_cvt_pk_bf16_f32 v196, v124, v125\n" \
"v_cvt_pk_bf16_f32 v197, v126, v127\n" \
"v_cvt_pk_bf16_f32 v198, v128, v129\n" \
"v_mfma_f32_16x16x32_bf16 a[60:63], a[172:175], a[140:143], 0\n" \
"v_cvt_pk_bf16_f32 v199, v130, v131\n" \
"v_cvt_pk_bf16_f32 v252, v140, v141\n" \
"v_cvt_pk_bf16_f32 v253, v142, v143\n" \
".fill (L_pb + 20192 - .) / 4, 4, 0xBF800000\n" \
"L_pp5:\n" \
"s_cbranch_execz L_pret\n" \
"s_nop 0\n" \
"v_mfma_f32_16x16x32_bf16 a[60:63], a[236:239], a[204:207], a[60:63]\n" \
"s_add_u32 s88, s41, 0x3000\n" \
"buffer_store_dwordx4 v[212:215], v20, s[80:83], s88 offen offset:0 sc1\n" \
"v_mfma_f32_16x16x32_bf16 a[52:55], a[164:167], a[140:143], 0\n" \
"v_cvt_pk_bf16_f32 v254, v144, v145\n" \
"v_cvt_pk_bf16_f32 v255, v146, v147\n" \
"v_accvgpr_write_b32 a84, v252\n" \
"v_mfma_f32_16x16x32_bf16 a[52:55], a[228:231], a[204:207], a[52:55]\n" \
"v_accvgpr_write_b32 a85, v253\n" \
"v_accvgpr_write_b32 a86, v254\n" \
"v_accvgpr_write_b32 a87, v255\n" \
"v_mfma_f32_16x16x32_bf16 a[48:51], a[160:163], a[140:143], 0\n" \
"v_cvt_pk_bf16_f32 v252, v132, v133\n" \
"v_cvt_pk_bf16_f32 v253, v134, v135\n" \
"v_cvt_pk_bf16_f32 v254, v136, v137\n" \
"v_mfma_f32_16x16x32_bf16 a[48:51], a[224:227], a[204:207], a[48:51]\n" \
"v_cvt_pk_bf16_f32 v255, v138, v139\n" \
"s_waitcnt lgkmcnt(15)\n" \
"v_mfma_f32_16x16x32_bf16 a[64:67], a[164:167], v[248:251], 0\n" \
"v_mfma_f32_16x16x32_bf16 a[64:67], a[228:231], v[48:51], a[64:67]\n" \
"v_mfma_f32_16x16x32_bf16 a[80:83], a[172:175], v[248:251], 0\n" \
"v_mfma_f32_16x16x32_bf16 a[80:83], a[236:239], v[48:51], a[80:83]\n" \
"v_mfma_f32_16x16x32_bf16 a[68:71], v[248:251], a[168:171], 0\n" \
"v_mfma_f32_16x16x32_bf16 a[68:71], v[48:51], a[232:235], a[68:71]\n" \
"s_waitcnt vmcnt(4) lgkmcnt(0)\n" \
"s_barrier\n" \
"v_mfma_f32_16x16x32_bf16 v[52:55], a[176:179], a[128:131], 0\n" \
"s_add_u32 m0, s40, 0x0\n" \
"s_add_u32 s85, s39, 0x4000\n" \
"v_cvt_pk_bf16_f32 v200, v148, v149\n" \
"v_mfma_f32_16x16x32_bf16 v[52:55], a[240:243], a[192:195], v[52:55]\n" \
"buffer_load_dwordx4 v20, s[80:83], s85 offen sc1 lds\n" \
"v_cvt_pk_bf16_f32 v201, v150, v151\n" \
"v_mfma_f32_16x16x32_bf16 v[56:59], a[180:183], a[128:131], 0\n" \
"ds_read_b64_tr_b16 a[160:161], v32 offset:32768\n" \
"v_cvt_pk_bf16_f32 v202, v152, v153\n" \
"v_mfma_f32_16x16x32_bf16 v[56:59], a[244:247], a[192:195], v[56:59]\n" \
"ds_read_b64_tr_b16 a[162:163], v32 offset:33280\n" \
"v_cvt_pk_bf16_f32 v203, v154, v155\n" \
"v_mfma_f32_16x16x32_bf16 v[60:63], a[184:187], a[128:131], 0\n" \
"s_add_u32 m0, s40, 0x1000\n" \
"s_add_u32 s85, s39, 0x5000\n" \
"v_cvt_pk_bf16_f32 v204, v156, v157\n" \
"v_mfma_f32_16x16x32_bf16 v[60:63], a[248:251], a[192:195], v[60:63]\n" \
"buffer_load_dwordx4 v20, s[80:83], s85 offen sc1 lds\n" \
"v_cvt_pk_bf16_f32 v205, v158, v159\n" \
"v_mfma_f32_16x16x32_bf16 v[64:67], a[188:191], a[128:131], 0\n" \
"ds_read_b64_tr_b16 a[224:225], v32 offset:36864\n" \
"v_cvt_pk_bf16_f32 v206, v160, v161\n" \
"v_mfma_f32_16x16x32_bf16 v[64:67], a[252:255], a[192:195], v[64:67]\n" \
"ds_read_b128 a[128:131], v28 offset:32768\n" \
"v_cvt_pk_bf16_f32 v207, v162, v163\n" \
"v_mfma_f32_16x16x32_bf16 v[80:83], a[188:191], a[132:135], 0\n" \
"ds_read_b128 a[192:195], v29 offset:32768\n" \
"v_cvt_pk_bf16_f32 v208, v172, v173\n" \
"v_mfma_f32_16x16x32_bf16 v[80:83], a[252:255], a[196:199], v[80:83]\n" \
"s_add_u32 m0, s40, 0x3000\n" \
"s_movk_i32 s84, 0x100\n" \
"v_cvt_pk_bf16_f32 v209, v174, v175\n" \
"v_mfma_f32_16x16x32_bf16 v[76:79], a[184:187], a[132:135], 0\n" \
"buffer_load_dwordx4 v21, s[76:79], s84 offen offset:0 lds\n" \
"v_cvt_pk_bf16_f32 v210, v176, v177\n" \
"v_mfma_f32_16x16x32_bf16 v[76:79], a[248:251], a[196:199], v[76:79]\n" \
"ds_read_b64_tr_b16 a[226:227], v32 offset:37376\n" \
"v_cvt_pk_bf16_f32 v211, v178, v179\n" \
"v_mfma_f32_16x16x32_bf16 v[72:75], a[180:183], a[132:135], 0\n" \
"s_add_u32 m0, s30, 0x0\n" \
"s_movk_i32 s84, 0x100\n" \
"s_nop 0\n" \
"v_cvt_pk_bf16_f32 v212, v164, v165\n" \
"v_mfma_f32_16x16x32_bf16 v[72:75], a[244:247], a[196:199], v[72:75]\n" \
"buffer_load_dwordx4 v24, s[76:79], s84 offen offset:0 lds\n" \
"v_cvt_pk_bf16_f32 v213, v166, v167\n" \
"v_mfma_f32_16x16x32_bf16 v[68:71], a[176:179], a[132:135], 0\n" \
"ds_read_b64_tr_b16 a[164:165], v33 offset:32768\n" \
"v_cvt_pk_bf16_f32 v214, v168, v169\n" \
"v_mfma_f32_16x16x32_bf16 v[68:71], a[240:243], a[196:199], v[68:71]\n" \
"ds_read_b128 a[132:135], v28 offset:34816\n" \
"v_cvt_pk_bf16_f32 v215, v170, v171\n" \
"v_mfma_f32_16x16x32_bf16 v[84:87], a[176:179], a[136:139], 0\n" \
"ds_read_b128 a[196:199], v29 offset:34816\n" \
"v_mfma_f32_16x16x32_bf16 v[84:87], a[240:243], a[200:203], v[84:87]\n" \
"s_movk_i32 s84, 0x2100\n" \
"v_mfma_f32_16x16x32_bf16 v[88:91], a[180:183], a[136:139], 0\n" \
"buffer_load_dwordx4 v25, s[76:79], s84 offen offset:1024 lds\n" \
"v_mfma_f32_16x16x32_bf16 v[88:91], a[244:247], a[200:203], v[88:91]\n" \
"ds_read_b64_tr_b16 a[166:167], v33 offset:33280\n" \
"v_mfma_f32_16x16x32_bf16 v[92:95], a[184:187], a[136:139], 0\n" \
"ds_read_b64_tr_b16 a[228:229], v33 offset:36864\n" \
"v_mfma_f32_16x16x32_bf16 v[92:95], a[248:251], a[200:203], v[92:95]\n" \
"s_movk_i32 s84, 0x4100\n" \
"v_mfma_f32_16x16x32_bf16 v[96:99], a[188:191], a[136:139], 0\n" \
"buffer_load_dwordx4 v24, s[76:79], s84 offen offset:2048 lds\n" \
"v_mfma_f32_16x16x32_bf16 v[96:99], a[252:255], a[200:203], v[96:99]\n" \
"ds_read_b128 a[136:139], v28 offset:36864\n" \
"v_mfma_f32_16x16x32_bf16 v[112:115], a[188:191], a[140:143], 0\n" \
"ds_read_b128 a[200:203], v29 offset:36864\n" \
"v_mfma_f32_16x16x32_bf16 v[112:115], a[252:255], a[204:207], v[112:115]\n" \
"s_movk_i32 s84, 0x6100\n" \
"s_nop 0\n" \
"v_mfma_f32_16x16x32_bf16 v[108:111], a[184:187], a[140:143], 0\n" \
"buffer_load_dwordx4 v25, s[76:79], s84 offen offset:3072 lds\n" \
"v_mfma_f32_16x16x32_bf16 v[108:111], a[248:251], a[204:207], v[108:111]\n" \
"ds_read_b64_tr_b16 a[230:231], v33 offset:37376\n" \
"v_mfma_f32_16x16x32_bf16 v[104:107], a[180:183], a[140:143], 0\n" \
"ds_read_b64_tr_b16 a[172:173], v35 offset:32768\n" \
"v_mfma_f32_16x16x32_bf16 v[104:107], a[244:247], a[204:207], v[104:107]\n" \
"ds_read_b64_tr_b16 a[174:175], v35 offset:33280\n" \
"v_mfma_f32_16x16x32_bf16 v[100:103], a[176:179], a[140:143], 0\n" \
"ds_read_b64_tr_b16 a[236:237], v35 offset:36864\n" \
"v_mfma_f32_16x16x32_bf16 v[100:103], a[240:243], a[204:207], v[100:103]\n" \
"ds_read_b128 a[140:143], v28 offset:38912\n" \
"v_mfma_f32_16x16x32_bf16 v[116:119], a[176:179], a[144:147], 0\n" \
"ds_read_b128 a[204:207], v29 offset:38912\n" \
"v_mfma_f32_16x16x32_bf16 v[116:119], a[240:243], a[208:211], v[116:119]\n" \
"s_add_u32 m0, s31, 0x0\n" \
"s_mov_b32 s84, 0x100000\n" \
"v_mfma_f32_16x16x32_bf16 v[120:123], a[180:183], a[144:147], 0\n" \
"buffer_load_dwordx4 v26, s[68:71], s84 offen offset:0 lds\n" \
"v_mfma_f32_16x16x32_bf16 v[120:123], a[244:247], a[208:211], v[120:123]\n" \
"s_addk_i32 s84, 0x7c00\n" \
"v_mfma_f32_16x16x32_bf16 v[124:127], a[184:187], a[144:147], 0\n" \
"buffer_load_dwordx4 v26, s[68:71], s84 offen offset:1024 lds\n" \
"v_mfma_f32_16x16x32_bf16 v[124:127], a[248:251], a[208:211], v[124:127]\n" \
"ds_read_b64_tr_b16 a[238:239], v35 offset:37376\n" \
"v_mfma_f32_16x16x32_bf16 v[128:131], a[188:191], a[144:147], 0\n" \
"ds_read_b64_tr_b16 a[168:169], v34 offset:32768\n" \
"v_mfma_f32_16x16x32_bf16 v[128:131], a[252:255], a[208:211], v[128:131]\n" \
"ds_read_b128 a[144:147], v28 offset:40960\n" \
"v_mfma_f32_16x16x32_bf16 v[144:147], a[188:191], a[148:151], 0\n" \
"ds_read_b128 a[208:211], v29 offset:40960\n" \
"v_mfma_f32_16x16x32_bf16 v[144:147], a[252:255], a[212:215], v[144:147]\n" \
"s_addk_i32 s84, 0x7c00\n" \
"v_mfma_f32_16x16x32_bf16 v[140:143], a[184:187], a[148:151], 0\n" \
"buffer_load_dwordx4 v27, s[68:71], s84 offen offset:2048 lds\n" \
"v_mfma_f32_16x16x32_bf16 v[140:143], a[248:251], a[212:215], v[140:143]\n" \
"s_addk_i32 s84, 0x7c00\n" \
"v_mfma_f32_16x16x32_bf16 v[136:139], a[180:183], a[148:151], 0\n" \
"buffer_load_dwordx4 v27, s[68:71], s84 offen offset:3072 lds\n" \
"v_mfma_f32_16x16x32_bf16 v[136:139], a[244:247], a[212:215], v[136:139]\n" \
"ds_read_b64_tr_b16 a[170:171], v34 offset:33280\n" \
"v_mfma_f32_16x16x32_bf16 v[132:135], a[176:179], a[148:151], 0\n" \
"ds_read_b64_tr_b16 a[232:233], v34 offset:36864\n" \
"v_mfma_f32_16x16x32_bf16 v[132:135], a[240:243], a[212:215], v[132:135]\n" \
"ds_read_b128 a[148:151], v28 offset:43008\n" \
"v_mfma_f32_16x16x32_bf16 v[148:151], a[176:179], a[152:155], 0\n" \
"ds_read_b128 a[212:215], v29 offset:43008\n" \
"v_mfma_f32_16x16x32_bf16 v[148:151], a[240:243], a[216:219], v[148:151]\n" \
"s_add_u32 m0, s30, 0x1000\n" \
"s_mov_b32 s84, 0x9100\n" \
"v_mfma_f32_16x16x32_bf16 v[152:155], a[180:183], a[152:155], 0\n" \
"buffer_load_dwordx4 v24, s[76:79], s84 offen offset:0 lds\n" \
"v_mfma_f32_16x16x32_bf16 v[152:155], a[244:247], a[216:219], v[152:155]\n" \
"s_addk_i32 s84, 0x2000\n" \
"s_nop 0\n" \
"v_mfma_f32_16x16x32_bf16 v[156:159], a[184:187], a[152:155], 0\n" \
"buffer_load_dwordx4 v25, s[76:79], s84 offen offset:1024 lds\n" \
"v_mfma_f32_16x16x32_bf16 v[156:159], a[248:251], a[216:219], v[156:159]\n" \
"ds_read_b64_tr_b16 a[234:235], v34 offset:37376\n" \
"v_mfma_f32_16x16x32_bf16 v[160:163], a[188:191], a[152:155], 0\n" \
"ds_read_b128 v[248:251], v22 offset:32768\n" \
"v_mfma_f32_16x16x32_bf16 v[160:163], a[252:255], a[216:219], v[160:163]\n" \
"ds_read_b128 a[152:155], v28 offset:45056\n" \
"v_mfma_f32_16x16x32_bf16 v[176:179], a[188:191], a[156:159], 0\n" \
"ds_read_b128 a[216:219], v29 offset:45056\n" \
"v_mfma_f32_16x16x32_bf16 v[176:179], a[252:255], a[220:223], v[176:179]\n" \
"ds_read_b128 v[48:51], v23 offset:32768\n" \
"v_mfma_f32_16x16x32_bf16 v[172:175], a[184:187], a[156:159], 0\n" \
"v_mfma_f32_16x16x32_bf16 v[172:175], a[248:251], a[220:223], v[172:175]\n" \
"v_mfma_f32_16x16x32_bf16 v[168:171], a[180:183], a[156:159], 0\n" \
"v_mfma_f32_16x16x32_bf16 v[168:171], a[244:247], a[220:223], v[168:171]\n" \
"v_mfma_f32_16x16x32_bf16 v[164:167], a[176:179], a[156:159], 0\n" \
"v_mfma_f32_16x16x32_bf16 v[164:167], a[240:243], a[220:223], v[164:167]\n" \
"ds_read_b128 a[156:159], v28 offset:47104\n" \
"ds_read_b128 a[220:223], v29 offset:47104\n" \
"s_waitcnt lgkmcnt(15)\n" \
"v_mfma_f32_16x16x32_bf16 a[0:3], a[160:163], a[128:131], a[0:3]\n" \
"s_addk_i32 s84, 0x2000\n" \
"v_mfma_f32_16x16x32_bf16 a[0:3], a[224:227], a[192:195], a[0:3]\n" \
"buffer_load_dwordx4 v24, s[76:79], s84 offen offset:2048 lds\n" \
"v_mfma_f32_16x16x32_bf16 a[4:7], a[164:167], a[128:131], a[4:7]\n" \
"ds_read_b64_tr_b16 a[176:177], v36 offset:32768\n" \
"v_mov_b32_e64 v11, 0\n" \
"v_mfma_f32_16x16x32_bf16 a[4:7], a[228:231], a[192:195], a[4:7]\n" \
"s_addk_i32 s84, 0x2000\n" \
"v_mfma_f32_16x16x32_bf16 a[12:15], a[172:175], a[128:131], a[12:15]\n" \
"buffer_load_dwordx4 v25, s[76:79], s84 offen offset:3072 lds\n" \
"s_waitcnt lgkmcnt(15)\n" \
"v_mfma_f32_16x16x32_bf16 a[12:15], a[236:239], a[192:195], a[12:15]\n" \
"ds_read_b64_tr_b16 a[178:179], v36 offset:33792\n" \
"s_and_b32 s6, s2, 7\n" \
"s_waitcnt lgkmcnt(12)\n" \
"v_mfma_f32_16x16x32_bf16 a[8:11], a[128:131], a[168:171], a[8:11]\n" \
"ds_read_b64_tr_b16 a[240:241], v36 offset:40960\n" \
"s_lshl_b32 s7, s3, 1\n" \
"s_waitcnt lgkmcnt(9)\n" \
"v_mfma_f32_16x16x32_bf16 a[8:11], a[192:195], a[232:235], a[8:11]\n" \
"ds_read_b64_tr_b16 a[242:243], v36 offset:41984\n" \
"s_lshr_b32 s4, s2, 5\n" \
"v_mfma_f32_16x16x32_bf16 a[24:27], a[132:135], a[168:171], a[24:27]\n" \
"ds_read_b64_tr_b16 a[180:181], v37 offset:32768\n" \
"s_lshl_b32 s5, s6, 2\n" \
"v_mfma_f32_16x16x32_bf16 a[24:27], a[196:199], a[232:235], a[24:27]\n" \
"ds_read_b64_tr_b16 a[182:183], v37 offset:33792\n" \
"s_bfe_u32 s8, s2, 0x20003\n" \
"v_mfma_f32_16x16x32_bf16 a[28:31], a[172:175], a[132:135], a[28:31]\n" \
"ds_read_b64_tr_b16 a[244:245], v37 offset:40960\n" \
"s_and_b32 s7, s7, 4\n" \
"v_mfma_f32_16x16x32_bf16 a[28:31], a[236:239], a[196:199], a[28:31]\n" \
"s_add_u32 s89, s41, 0x0\n" \
"buffer_store_dwordx4 v[224:227], v20, s[80:83], s89 offen offset:2048 sc1\n" \
"v_mfma_f32_16x16x32_bf16 a[20:23], a[164:167], a[132:135], a[20:23]\n" \
"ds_read_b64_tr_b16 a[246:247], v37 offset:41984\n" \
"s_and_b32 s9, s3, 1\n" \
"v_mfma_f32_16x16x32_bf16 a[20:23], a[228:231], a[196:199], a[20:23]\n" \
"ds_read_b64_tr_b16 a[184:185], v38 offset:32768\n" \
"s_or_b32 s13, s5, s8\n" \
"v_mfma_f32_16x16x32_bf16 a[16:19], a[160:163], a[132:135], a[16:19]\n" \
"ds_read_b64_tr_b16 a[186:187], v38 offset:33792\n" \
"s_or_b32 s9, s7, s9\n" \
"v_mfma_f32_16x16x32_bf16 a[16:19], a[224:227], a[196:199], a[16:19]\n" \
"s_mul_i32 s7, s4, 0xa00\n" \
"s_add_i32 s7, s7, 0x5bc1000\n" \
"v_mfma_f32_16x16x32_bf16 a[32:35], a[160:163], a[136:139], a[32:35]\n" \
"s_add_u32 s88, s41, 0x0\n" \
"buffer_store_dwordx4 v[228:231], v20, s[80:83], s88 offen offset:3072 sc1\n" \
"v_mfma_f32_16x16x32_bf16 a[32:35], a[224:227], a[200:203], a[32:35]\n" \
"ds_read_b64_tr_b16 a[248:249], v38 offset:40960\n" \
"s_lshl_b32 s10, s13, 2\n" \
"v_mfma_f32_16x16x32_bf16 a[36:39], a[164:167], a[136:139], a[36:39]\n" \
"ds_read_b64_tr_b16 a[250:251], v38 offset:41984\n" \
"s_or_b32 s10, s10, s7\n" \
"v_mfma_f32_16x16x32_bf16 a[36:39], a[228:231], a[200:203], a[36:39]\n" \
"ds_read_b64_tr_b16 a[188:189], v39 offset:32768\n" \
"v_writelane_b32 v11, s10, 0\n" \
"v_mfma_f32_16x16x32_bf16 a[44:47], a[172:175], a[136:139], a[44:47]\n" \
"s_lshl_b32 s14, s4, 3\n" \
"v_writelane_b32 v11, s7, 1\n" \
"v_mfma_f32_16x16x32_bf16 a[44:47], a[236:239], a[200:203], a[44:47]\n" \
"ds_read_b64_tr_b16 a[190:191], v39 offset:33792\n" \
"s_or_b32 s7, s14, s6\n" \
"v_mfma_f32_16x16x32_bf16 a[40:43], a[136:139], a[168:171], a[40:43]\n" \
"s_add_u32 s89, s41, 0x1000\n" \
"buffer_store_dwordx4 v[232:235], v20, s[80:83], s89 offen offset:3072 sc1\n" \
"v_mfma_f32_16x16x32_bf16 a[40:43], a[200:203], a[232:235], a[40:43]\n" \
"ds_read_b64_tr_b16 a[252:253], v39 offset:40960\n" \
"s_lshl_b32 s7, s7, 10\n" \
"v_mfma_f32_16x16x32_bf16 a[56:59], a[140:143], a[168:171], a[56:59]\n" \
"ds_read_b64_tr_b16 a[254:255], v39 offset:41984\n" \
"s_lshl_b32 s10, s3, 8\n" \
"v_mfma_f32_16x16x32_bf16 a[56:59], a[204:207], a[232:235], a[56:59]\n" \
"s_or_b32 s7, s7, s10\n" \
"s_or_b32 s10, s7, 0x5c00200\n" \
"v_mfma_f32_16x16x32_bf16 a[60:63], a[172:175], a[140:143], a[60:63]\n" \
"v_writelane_b32 v11, s10, 2\n" \
"s_lshl_b32 s10, s8, 2\n" \
"v_mfma_f32_16x16x32_bf16 a[60:63], a[236:239], a[204:207], a[60:63]\n" \
"s_add_u32 s88, s41, 0x1000\n" \
"buffer_store_dwordx4 v[236:239], v20, s[80:83], s88 offen offset:2048 sc1\n" \
"v_mfma_f32_16x16x32_bf16 a[52:55], a[164:167], a[140:143], a[52:55]\n" \
"s_or_b32 s11, s10, s7\n" \
"s_or_b32 s11, s11, 0x5c00000\n" \
"v_mfma_f32_16x16x32_bf16 a[52:55], a[228:231], a[204:207], a[52:55]\n" \
"v_writelane_b32 v11, s11, 3\n" \
"s_or_b32 s7, s7, 0x5c00080\n" \
"v_mfma_f32_16x16x32_bf16 a[48:51], a[160:163], a[140:143], a[48:51]\n" \
"v_writelane_b32 v11, s7, 4\n" \
"s_or_b32 s7, s7, s10\n" \
"v_mfma_f32_16x16x32_bf16 a[48:51], a[224:227], a[204:207], a[48:51]\n" \
"s_add_i32 s10, s3, s2\n" \
"v_writelane_b32 v11, s7, 5\n" \
"s_waitcnt lgkmcnt(15)\n" \
"v_mfma_f32_16x16x32_bf16 a[64:67], a[164:167], v[248:251], a[64:67]\n" \
"s_mov_b32 s7, 2.0\n" \
"s_mov_b32 s11, 0x5bc0000\n" \
"v_mfma_f32_16x16x32_bf16 a[64:67], a[228:231], v[48:51], a[64:67]\n" \
"s_cmp_lg_u32 s10, 0\n" \
"s_cselect_b32 s10, s7, s11\n" \
"v_mfma_f32_16x16x32_bf16 a[80:83], a[172:175], v[248:251], a[80:83]\n" \
"s_lshr_b32 s15, s2, 3\n" \
"v_writelane_b32 v11, s10, 53\n" \
"v_mfma_f32_16x16x32_bf16 a[80:83], a[236:239], v[48:51], a[80:83]\n" \
"s_lshl_b32 s10, s6, 5\n" \
"s_min_u32 s16, s3, 2\n" \
"v_mfma_f32_16x16x32_bf16 a[68:71], v[248:251], a[168:171], a[68:71]\n" \
"s_mul_i32 s12, s8, 0x120\n" \
"v_mfma_f32_16x16x32_bf16 a[68:71], v[48:51], a[232:235], a[68:71]\n" \
"s_waitcnt vmcnt(4) lgkmcnt(0)\n" \
"s_barrier\n" \
"v_mfma_f32_16x16x32_bf16 v[52:55], a[176:179], a[128:131], v[52:55]\n" \
"s_add_u32 m0, s40, 0x8000\n" \
"s_add_u32 s85, s39, 0x6000\n" \
"v_mfma_f32_16x16x32_bf16 v[52:55], a[240:243], a[192:195], v[52:55]\n" \
"buffer_load_dwordx4 v20, s[80:83], s85 offen sc1 lds\n" \
"v_mfma_f32_16x16x32_bf16 v[56:59], a[180:183], a[128:131], v[56:59]\n" \
"ds_read_b64_tr_b16 a[160:161], v32 offset:0\n" \
"v_mfma_f32_16x16x32_bf16 v[56:59], a[244:247], a[192:195], v[56:59]\n" \
"ds_read_b64_tr_b16 a[162:163], v32 offset:512\n" \
"v_mfma_f32_16x16x32_bf16 v[60:63], a[184:187], a[128:131], v[60:63]\n" \
"s_add_u32 m0, s40, 0x9000\n" \
"s_add_u32 s85, s39, 0x7000\n" \
"v_mfma_f32_16x16x32_bf16 v[60:63], a[248:251], a[192:195], v[60:63]\n" \
"buffer_load_dwordx4 v20, s[80:83], s85 offen sc1 lds\n" \
"v_mfma_f32_16x16x32_bf16 v[64:67], a[188:191], a[128:131], v[64:67]\n" \
"ds_read_b64_tr_b16 a[224:225], v32 offset:4096\n" \
"v_mfma_f32_16x16x32_bf16 v[64:67], a[252:255], a[192:195], v[64:67]\n" \
"ds_read_b128 a[128:131], v28 offset:0\n" \
"v_mfma_f32_16x16x32_bf16 v[80:83], a[188:191], a[132:135], v[80:83]\n" \
"ds_read_b128 a[192:195], v29 offset:0\n" \
"v_mfma_f32_16x16x32_bf16 v[80:83], a[252:255], a[196:199], v[80:83]\n" \
"s_add_u32 m0, s40, 0xb000\n" \
"s_movk_i32 s84, 0x180\n" \
"v_mfma_f32_16x16x32_bf16 v[76:79], a[184:187], a[132:135], v[76:79]\n" \
"buffer_load_dwordx4 v21, s[76:79], s84 offen offset:0 lds\n" \
"v_mfma_f32_16x16x32_bf16 v[76:79], a[248:251], a[196:199], v[76:79]\n" \
"ds_read_b64_tr_b16 a[226:227], v32 offset:4608\n" \
"v_mfma_f32_16x16x32_bf16 v[72:75], a[180:183], a[132:135], v[72:75]\n" \
"s_add_u32 m0, s30, 0x8000\n" \
"s_movk_i32 s84, 0x180\n" \
"v_mfma_f32_16x16x32_bf16 v[72:75], a[244:247], a[196:199], v[72:75]\n" \
"buffer_load_dwordx4 v24, s[76:79], s84 offen offset:0 lds\n" \
"v_mfma_f32_16x16x32_bf16 v[68:71], a[176:179], a[132:135], v[68:71]\n" \
"ds_read_b64_tr_b16 a[164:165], v33 offset:0\n" \
"v_mfma_f32_16x16x32_bf16 v[68:71], a[240:243], a[196:199], v[68:71]\n" \
"ds_read_b128 a[132:135], v28 offset:2048\n" \
"v_mfma_f32_16x16x32_bf16 v[84:87], a[176:179], a[136:139], v[84:87]\n" \
"ds_read_b128 a[196:199], v29 offset:2048\n" \
"v_mfma_f32_16x16x32_bf16 v[84:87], a[240:243], a[200:203], v[84:87]\n" \
"s_movk_i32 s84, 0x2180\n" \
"v_mfma_f32_16x16x32_bf16 v[88:91], a[180:183], a[136:139], v[88:91]\n" \
"buffer_load_dwordx4 v25, s[76:79], s84 offen offset:1024 lds\n" \
"v_mfma_f32_16x16x32_bf16 v[88:91], a[244:247], a[200:203], v[88:91]\n" \
"ds_read_b64_tr_b16 a[166:167], v33 offset:512\n" \
"v_mfma_f32_16x16x32_bf16 v[92:95], a[184:187], a[136:139], v[92:95]\n" \
"ds_read_b64_tr_b16 a[228:229], v33 offset:4096\n" \
"v_mfma_f32_16x16x32_bf16 v[92:95], a[248:251], a[200:203], v[92:95]\n" \
"s_movk_i32 s84, 0x4180\n" \
"v_mfma_f32_16x16x32_bf16 v[96:99], a[188:191], a[136:139], v[96:99]\n" \
"buffer_load_dwordx4 v24, s[76:79], s84 offen offset:2048 lds\n" \
"v_mfma_f32_16x16x32_bf16 v[96:99], a[252:255], a[200:203], v[96:99]\n" \
"ds_read_b128 a[136:139], v28 offset:4096\n" \
"v_mfma_f32_16x16x32_bf16 v[112:115], a[188:191], a[140:143], v[112:115]\n" \
"ds_read_b128 a[200:203], v29 offset:4096\n" \
"v_mfma_f32_16x16x32_bf16 v[112:115], a[252:255], a[204:207], v[112:115]\n" \
"s_movk_i32 s84, 0x6180\n" \
"s_nop 0\n" \
"v_mfma_f32_16x16x32_bf16 v[108:111], a[184:187], a[140:143], v[108:111]\n" \
"buffer_load_dwordx4 v25, s[76:79], s84 offen offset:3072 lds\n" \
"v_mfma_f32_16x16x32_bf16 v[108:111], a[248:251], a[204:207], v[108:111]\n" \
"ds_read_b64_tr_b16 a[230:231], v33 offset:4608\n" \
"v_mfma_f32_16x16x32_bf16 v[104:107], a[180:183], a[140:143], v[104:107]\n" \
"ds_read_b64_tr_b16 a[172:173], v35 offset:0\n" \
"v_mfma_f32_16x16x32_bf16 v[104:107], a[244:247], a[204:207], v[104:107]\n" \
"ds_read_b64_tr_b16 a[174:175], v35 offset:512\n" \
"v_mfma_f32_16x16x32_bf16 v[100:103], a[176:179], a[140:143], v[100:103]\n" \
"ds_read_b64_tr_b16 a[236:237], v35 offset:4096\n" \
"v_mfma_f32_16x16x32_bf16 v[100:103], a[240:243], a[204:207], v[100:103]\n" \
"ds_read_b128 a[140:143], v28 offset:6144\n" \
"v_mfma_f32_16x16x32_bf16 v[116:119], a[176:179], a[144:147], v[116:119]\n" \
"ds_read_b128 a[204:207], v29 offset:6144\n" \
"v_mfma_f32_16x16x32_bf16 v[116:119], a[240:243], a[208:211], v[116:119]\n" \
"s_add_u32 m0, s31, 0x8000\n" \
"s_mov_b32 s84, 0x180000\n" \
"v_mfma_f32_16x16x32_bf16 v[120:123], a[180:183], a[144:147], v[120:123]\n" \
"buffer_load_dwordx4 v26, s[68:71], s84 offen offset:0 lds\n" \
"v_mfma_f32_16x16x32_bf16 v[120:123], a[244:247], a[208:211], v[120:123]\n" \
"s_addk_i32 s84, 0x7c00\n" \
"s_nop 0\n" \
"v_mfma_f32_16x16x32_bf16 v[124:127], a[184:187], a[144:147], v[124:127]\n" \
"buffer_load_dwordx4 v26, s[68:71], s84 offen offset:1024 lds\n" \
"v_mfma_f32_16x16x32_bf16 v[124:127], a[248:251], a[208:211], v[124:127]\n" \
"ds_read_b64_tr_b16 a[238:239], v35 offset:4608\n" \
"v_mfma_f32_16x16x32_bf16 v[128:131], a[188:191], a[144:147], v[128:131]\n" \
"ds_read_b64_tr_b16 a[168:169], v34 offset:0\n" \
"v_mfma_f32_16x16x32_bf16 v[128:131], a[252:255], a[208:211], v[128:131]\n" \
"ds_read_b128 a[144:147], v28 offset:8192\n" \
"v_mfma_f32_16x16x32_bf16 v[144:147], a[188:191], a[148:151], v[144:147]\n" \
"ds_read_b128 a[208:211], v29 offset:8192\n" \
"v_mfma_f32_16x16x32_bf16 v[144:147], a[252:255], a[212:215], v[144:147]\n" \
"s_addk_i32 s84, 0x7c00\n" \
"v_mfma_f32_16x16x32_bf16 v[140:143], a[184:187], a[148:151], v[140:143]\n" \
"buffer_load_dwordx4 v27, s[68:71], s84 offen offset:2048 lds\n" \
"v_mfma_f32_16x16x32_bf16 v[140:143], a[248:251], a[212:215], v[140:143]\n" \
"s_addk_i32 s84, 0x7c00\n" \
"v_mfma_f32_16x16x32_bf16 v[136:139], a[180:183], a[148:151], v[136:139]\n" \
"buffer_load_dwordx4 v27, s[68:71], s84 offen offset:3072 lds\n" \
"v_mfma_f32_16x16x32_bf16 v[136:139], a[244:247], a[212:215], v[136:139]\n" \
"ds_read_b64_tr_b16 a[170:171], v34 offset:512\n" \
"v_mfma_f32_16x16x32_bf16 v[132:135], a[176:179], a[148:151], v[132:135]\n" \
"ds_read_b64_tr_b16 a[232:233], v34 offset:4096\n" \
"v_mfma_f32_16x16x32_bf16 v[132:135], a[240:243], a[212:215], v[132:135]\n" \
"ds_read_b128 a[148:151], v28 offset:10240\n" \
"v_mfma_f32_16x16x32_bf16 v[148:151], a[176:179], a[152:155], v[148:151]\n" \
"ds_read_b128 a[212:215], v29 offset:10240\n" \
"v_mfma_f32_16x16x32_bf16 v[148:151], a[240:243], a[216:219], v[148:151]\n" \
"s_add_u32 m0, s30, 0x9000\n" \
"s_mov_b32 s84, 0x9180\n" \
"v_mfma_f32_16x16x32_bf16 v[152:155], a[180:183], a[152:155], v[152:155]\n" \
"buffer_load_dwordx4 v24, s[76:79], s84 offen offset:0 lds\n" \
"v_mfma_f32_16x16x32_bf16 v[152:155], a[244:247], a[216:219], v[152:155]\n" \
"s_addk_i32 s84, 0x2000\n" \
"s_nop 0\n" \
"v_mfma_f32_16x16x32_bf16 v[156:159], a[184:187], a[152:155], v[156:159]\n" \
"buffer_load_dwordx4 v25, s[76:79], s84 offen offset:1024 lds\n" \
"v_mfma_f32_16x16x32_bf16 v[156:159], a[248:251], a[216:219], v[156:159]\n" \
"ds_read_b64_tr_b16 a[234:235], v34 offset:4608\n" \
"v_mfma_f32_16x16x32_bf16 v[160:163], a[188:191], a[152:155], v[160:163]\n" \
"ds_read_b128 v[248:251], v22 offset:0\n" \
"v_mfma_f32_16x16x32_bf16 v[160:163], a[252:255], a[216:219], v[160:163]\n" \
"ds_read_b128 a[152:155], v28 offset:12288\n" \
"v_mfma_f32_16x16x32_bf16 v[176:179], a[188:191], a[156:159], v[176:179]\n" \
"ds_read_b128 a[216:219], v29 offset:12288\n" \
"v_mfma_f32_16x16x32_bf16 v[176:179], a[252:255], a[220:223], v[176:179]\n" \
"ds_read_b128 v[48:51], v23 offset:0\n" \
"v_mfma_f32_16x16x32_bf16 v[172:175], a[184:187], a[156:159], v[172:175]\n" \
"s_add_u32 s11, s22, s12\n" \
"s_addc_u32 s17, s23, 0\n" \
"v_mfma_f32_16x16x32_bf16 v[172:175], a[248:251], a[220:223], v[172:175]\n" \
"s_add_u32 s18, s24, s12\n" \
"v_writelane_b32 v11, s11, 9\n" \
"s_addc_u32 s19, s25, 0\n" \
"v_mfma_f32_16x16x32_bf16 v[168:171], a[180:183], a[156:159], v[168:171]\n" \
"s_and_b32 s20, s17, 0xffff\n" \
"v_writelane_b32 v11, s20, 10\n" \
"v_mfma_f32_16x16x32_bf16 v[168:171], a[244:247], a[220:223], v[168:171]\n" \
"s_and_b32 s20, s19, 0xffff\n" \
"v_writelane_b32 v11, s18, 11\n" \
"v_mfma_f32_16x16x32_bf16 v[164:167], a[176:179], a[156:159], v[164:167]\n" \
"s_cmpk_lt_u32 s3, 3\n" \
"s_cselect_b32 s11, s11, s18\n" \
"v_mfma_f32_16x16x32_bf16 v[164:167], a[240:243], a[220:223], v[164:167]\n" \
"ds_read_b128 a[156:159], v28 offset:14336\n" \
"ds_read_b128 a[220:223], v29 offset:14336\n" \
"s_waitcnt lgkmcnt(15)\n" \
"v_mfma_f32_16x16x32_bf16 a[0:3], a[160:163], a[128:131], a[0:3]\n" \
"s_addk_i32 s84, 0x2000\n" \
"v_mfma_f32_16x16x32_bf16 a[0:3], a[224:227], a[192:195], a[0:3]\n" \
"buffer_load_dwordx4 v24, s[76:79], s84 offen offset:2048 lds\n" \
"v_mfma_f32_16x16x32_bf16 a[4:7], a[164:167], a[128:131], a[4:7]\n" \
"ds_read_b64_tr_b16 a[176:177], v36 offset:0\n" \
"s_movk_i32 s18, 0xfff\n" \
"v_mfma_f32_16x16x32_bf16 a[4:7], a[228:231], a[192:195], a[4:7]\n" \
"s_addk_i32 s84, 0x2000\n" \
"v_mfma_f32_16x16x32_bf16 a[12:15], a[172:175], a[128:131], a[12:15]\n" \
"buffer_load_dwordx4 v25, s[76:79], s84 offen offset:3072 lds\n" \
"s_waitcnt lgkmcnt(15)\n" \
"v_mfma_f32_16x16x32_bf16 a[12:15], a[236:239], a[192:195], a[12:15]\n" \
"ds_read_b64_tr_b16 a[178:179], v36 offset:1024\n" \
"v_writelane_b32 v11, s20, 12\n" \
"s_waitcnt lgkmcnt(12)\n" \
"v_mfma_f32_16x16x32_bf16 a[8:11], a[128:131], a[168:171], a[8:11]\n" \
"ds_read_b64_tr_b16 a[240:241], v36 offset:8192\n" \
"s_and_b32 s20, s29, 0xffff\n" \
"s_waitcnt lgkmcnt(9)\n" \
"s_nop 0\n" \
"v_mfma_f32_16x16x32_bf16 a[8:11], a[192:195], a[232:235], a[8:11]\n" \
"ds_read_b64_tr_b16 a[242:243], v36 offset:9216\n" \
"v_writelane_b32 v11, s28, 13\n" \
"v_mfma_f32_16x16x32_bf16 a[24:27], a[132:135], a[168:171], a[24:27]\n" \
"ds_read_b64_tr_b16 a[180:181], v37 offset:0\n" \
"v_writelane_b32 v11, s20, 14\n" \
"v_mfma_f32_16x16x32_bf16 a[24:27], a[196:199], a[232:235], a[24:27]\n" \
"ds_read_b64_tr_b16 a[182:183], v37 offset:1024\n" \
"v_writelane_b32 v11, s11, 15\n" \
"v_mfma_f32_16x16x32_bf16 a[28:31], a[172:175], a[132:135], a[28:31]\n" \
"ds_read_b64_tr_b16 a[244:245], v37 offset:8192\n" \
"v_mfma_f32_16x16x32_bf16 a[28:31], a[236:239], a[196:199], a[28:31]\n" \
"s_add_u32 s89, s41, 0x2000\n" \
"buffer_store_dwordx4 v[240:243], v20, s[80:83], s89 offen offset:2048 sc1\n" \
"v_mfma_f32_16x16x32_bf16 a[20:23], a[164:167], a[132:135], a[20:23]\n" \
"ds_read_b64_tr_b16 a[246:247], v37 offset:9216\n" \
"v_mfma_f32_16x16x32_bf16 a[20:23], a[228:231], a[196:199], a[20:23]\n" \
"ds_read_b64_tr_b16 a[184:185], v38 offset:0\n" \
"v_mfma_f32_16x16x32_bf16 a[16:19], a[160:163], a[132:135], a[16:19]\n" \
"ds_read_b64_tr_b16 a[186:187], v38 offset:1024\n" \
"v_mfma_f32_16x16x32_bf16 a[16:19], a[224:227], a[196:199], a[16:19]\n" \
"s_cmpk_lt_u32 s3, 3\n" \
"s_cselect_b32 s11, s17, s19\n" \
"v_mfma_f32_16x16x32_bf16 a[32:35], a[160:163], a[136:139], a[32:35]\n" \
"s_add_u32 s88, s41, 0x2000\n" \
"buffer_store_dwordx4 v[244:247], v20, s[80:83], s88 offen offset:3072 sc1\n" \
"v_mfma_f32_16x16x32_bf16 a[32:35], a[224:227], a[200:203], a[32:35]\n" \
"ds_read_b64_tr_b16 a[248:249], v38 offset:8192\n" \
"s_and_b32 s11, s11, 0xffff\n" \
"v_mfma_f32_16x16x32_bf16 a[36:39], a[164:167], a[136:139], a[36:39]\n" \
"ds_read_b64_tr_b16 a[250:251], v38 offset:9216\n" \
"v_writelane_b32 v11, s11, 16\n" \
"v_mfma_f32_16x16x32_bf16 a[36:39], a[228:231], a[200:203], a[36:39]\n" \
"ds_read_b64_tr_b16 a[188:189], v39 offset:0\n" \
"s_mul_i32 s11, s6, 0x48\n" \
"v_mfma_f32_16x16x32_bf16 a[44:47], a[172:175], a[136:139], a[44:47]\n" \
"s_add_i32 s17, s11, 0x47\n" \
"v_mfma_f32_16x16x32_bf16 a[44:47], a[236:239], a[200:203], a[44:47]\n" \
"ds_read_b64_tr_b16 a[190:191], v39 offset:1024\n" \
"v_mfma_f32_16x16x32_bf16 a[40:43], a[136:139], a[168:171], a[40:43]\n" \
"s_add_u32 s89, s41, 0x3000\n" \
"buffer_store_dwordx4 v[184:187], v20, s[80:83], s89 offen offset:3072 sc1\n" \
"v_mfma_f32_16x16x32_bf16 a[40:43], a[200:203], a[232:235], a[40:43]\n" \
"ds_read_b64_tr_b16 a[252:253], v39 offset:8192\n" \
"v_mfma_f32_16x16x32_bf16 a[56:59], a[140:143], a[168:171], a[56:59]\n" \
"ds_read_b64_tr_b16 a[254:255], v39 offset:9216\n" \
"v_mfma_f32_16x16x32_bf16 a[56:59], a[204:207], a[232:235], a[56:59]\n" \
"s_cmpk_lt_u32 s3, 3\n" \
"s_cselect_b32 s17, s17, s18\n" \
"v_mfma_f32_16x16x32_bf16 a[60:63], a[172:175], a[140:143], a[60:63]\n" \
"s_mulk_i32 s17, 0x480\n" \
"s_addk_i32 s17, 0x120\n" \
"v_mfma_f32_16x16x32_bf16 a[60:63], a[236:239], a[204:207], a[60:63]\n" \
"s_add_u32 s88, s41, 0x3000\n" \
"buffer_store_dwordx4 v[188:191], v20, s[80:83], s88 offen offset:2048 sc1\n" \
"v_mfma_f32_16x16x32_bf16 a[52:55], a[164:167], a[140:143], a[52:55]\n" \
"v_writelane_b32 v11, s17, 17\n" \
"s_lshl_b32 s17, s3, 5\n" \
"v_mfma_f32_16x16x32_bf16 a[52:55], a[228:231], a[204:207], a[52:55]\n" \
"s_add_i32 s11, s11, s17\n" \
"s_mul_i32 s17, s11, 0x480\n" \
"v_mfma_f32_16x16x32_bf16 a[48:51], a[160:163], a[140:143], a[48:51]\n" \
"s_mul_i32 s11, s13, 0x24000\n" \
"s_add_i32 s18, s11, 0x1b000\n" \
"v_mfma_f32_16x16x32_bf16 a[48:51], a[224:227], a[204:207], a[48:51]\n" \
"s_cmpk_lt_u32 s3, 3\n" \
"s_cselect_b32 s17, s17, s18\n" \
"s_waitcnt lgkmcnt(15)\n" \
"v_mfma_f32_16x16x32_bf16 a[64:67], a[164:167], v[248:251], a[64:67]\n" \
"s_mul_i32 s19, s3, 0x9000\n" \
"v_writelane_b32 v11, s17, 18\n" \
"v_mfma_f32_16x16x32_bf16 a[64:67], a[228:231], v[48:51], a[64:67]\n" \
"s_mul_i32 s17, s16, 0x2400\n" \
"s_mul_i32 s16, s16, 0x9000\n" \
"v_mfma_f32_16x16x32_bf16 a[80:83], a[172:175], v[248:251], a[80:83]\n" \
"s_bitset1_b32 s17, 17\n" \
"v_writelane_b32 v11, s17, 19\n" \
"v_mfma_f32_16x16x32_bf16 a[80:83], a[236:239], v[48:51], a[80:83]\n" \
"s_add_i32 s16, s11, s16\n" \
"v_writelane_b32 v11, s16, 25\n" \
"v_mfma_f32_16x16x32_bf16 a[68:71], v[248:251], a[168:171], a[68:71]\n" \
"s_mul_i32 s16, s3, 0x2400\n" \
"s_add_i32 s17, s16, 0x6000\n" \
"v_mfma_f32_16x16x32_bf16 a[68:71], v[48:51], a[232:235], a[68:71]\n" \
"s_waitcnt vmcnt(4) lgkmcnt(0)\n" \
"s_barrier\n" \
"v_mfma_f32_16x16x32_bf16 v[52:55], a[176:179], a[128:131], v[52:55]\n" \
"s_add_u32 m0, s40, 0x0\n" \
"s_add_u32 s85, s39, 0x8000\n" \
"v_mfma_f32_16x16x32_bf16 v[52:55], a[240:243], a[192:195], v[52:55]\n" \
"buffer_load_dwordx4 v20, s[80:83], s85 offen sc1 lds\n" \
"v_mfma_f32_16x16x32_bf16 v[56:59], a[180:183], a[128:131], v[56:59]\n" \
"ds_read_b64_tr_b16 a[160:161], v32 offset:32768\n" \
"v_writelane_b32 v11, s17, 20\n" \
"v_mfma_f32_16x16x32_bf16 v[56:59], a[244:247], a[192:195], v[56:59]\n" \
"ds_read_b64_tr_b16 a[162:163], v32 offset:33280\n" \
"s_or_b32 s17, s16, 0x10000\n" \
"v_mfma_f32_16x16x32_bf16 v[60:63], a[184:187], a[128:131], v[60:63]\n" \
"s_add_u32 m0, s40, 0x1000\n" \
"s_add_u32 s85, s39, 0x9000\n" \
"v_mfma_f32_16x16x32_bf16 v[60:63], a[248:251], a[192:195], v[60:63]\n" \
"buffer_load_dwordx4 v20, s[80:83], s85 offen sc1 lds\n" \
"v_mfma_f32_16x16x32_bf16 v[64:67], a[188:191], a[128:131], v[64:67]\n" \
"ds_read_b64_tr_b16 a[224:225], v32 offset:36864\n" \
".fill (L_pb + 24288 - .) / 4, 4, 0xBF800000\n" \
"L_pp6:\n" \
"s_cbranch_execz L_pret\n" \
"s_add_i32 s18, s16, 0x19000\n" \
"v_mfma_f32_16x16x32_bf16 v[64:67], a[252:255], a[192:195], v[64:67]\n" \
"ds_read_b128 a[128:131], v28 offset:32768\n" \
"v_writelane_b32 v11, s17, 21\n" \
"v_mfma_f32_16x16x32_bf16 v[80:83], a[188:191], a[132:135], v[80:83]\n" \
"ds_read_b128 a[192:195], v29 offset:32768\n" \
"s_mov_b32 s17, 0\n" \
"v_mfma_f32_16x16x32_bf16 v[80:83], a[252:255], a[196:199], v[80:83]\n" \
"s_add_u32 m0, s40, 0x3000\n" \
"s_movk_i32 s84, 0x200\n" \
"v_mfma_f32_16x16x32_bf16 v[76:79], a[184:187], a[132:135], v[76:79]\n" \
"buffer_load_dwordx4 v21, s[76:79], s84 offen offset:0 lds\n" \
"v_mfma_f32_16x16x32_bf16 v[76:79], a[248:251], a[196:199], v[76:79]\n" \
"ds_read_b64_tr_b16 a[226:227], v32 offset:37376\n" \
"v_mfma_f32_16x16x32_bf16 v[72:75], a[180:183], a[132:135], v[72:75]\n" \
"s_add_u32 m0, s30, 0x0\n" \
"s_movk_i32 s84, 0x200\n" \
"v_mfma_f32_16x16x32_bf16 v[72:75], a[244:247], a[196:199], v[72:75]\n" \
"buffer_load_dwordx4 v24, s[76:79], s84 offen offset:0 lds\n" \
"v_mfma_f32_16x16x32_bf16 v[68:71], a[176:179], a[132:135], v[68:71]\n" \
"ds_read_b64_tr_b16 a[164:165], v33 offset:32768\n" \
"v_mfma_f32_16x16x32_bf16 v[68:71], a[240:243], a[196:199], v[68:71]\n" \
"ds_read_b128 a[132:135], v28 offset:34816\n" \
"v_mfma_f32_16x16x32_bf16 v[84:87], a[176:179], a[136:139], v[84:87]\n" \
"ds_read_b128 a[196:199], v29 offset:34816\n" \
"v_mfma_f32_16x16x32_bf16 v[84:87], a[240:243], a[200:203], v[84:87]\n" \
"s_movk_i32 s84, 0x2200\n" \
"v_mfma_f32_16x16x32_bf16 v[88:91], a[180:183], a[136:139], v[88:91]\n" \
"buffer_load_dwordx4 v25, s[76:79], s84 offen offset:1024 lds\n" \
"v_mfma_f32_16x16x32_bf16 v[88:91], a[244:247], a[200:203], v[88:91]\n" \
"ds_read_b64_tr_b16 a[166:167], v33 offset:33280\n" \
"v_mfma_f32_16x16x32_bf16 v[92:95], a[184:187], a[136:139], v[92:95]\n" \
"ds_read_b64_tr_b16 a[228:229], v33 offset:36864\n" \
"v_mfma_f32_16x16x32_bf16 v[92:95], a[248:251], a[200:203], v[92:95]\n" \
"s_movk_i32 s84, 0x4200\n" \
"v_mfma_f32_16x16x32_bf16 v[96:99], a[188:191], a[136:139], v[96:99]\n" \
"buffer_load_dwordx4 v24, s[76:79], s84 offen offset:2048 lds\n" \
"v_mfma_f32_16x16x32_bf16 v[96:99], a[252:255], a[200:203], v[96:99]\n" \
"ds_read_b128 a[136:139], v28 offset:36864\n" \
"v_mfma_f32_16x16x32_bf16 v[112:115], a[188:191], a[140:143], v[112:115]\n" \
"ds_read_b128 a[200:203], v29 offset:36864\n" \
"v_mfma_f32_16x16x32_bf16 v[112:115], a[252:255], a[204:207], v[112:115]\n" \
"s_movk_i32 s84, 0x6200\n" \
"v_mfma_f32_16x16x32_bf16 v[108:111], a[184:187], a[140:143], v[108:111]\n" \
"buffer_load_dwordx4 v25, s[76:79], s84 offen offset:3072 lds\n" \
"v_mfma_f32_16x16x32_bf16 v[108:111], a[248:251], a[204:207], v[108:111]\n" \
"ds_read_b64_tr_b16 a[230:231], v33 offset:37376\n" \
"v_mfma_f32_16x16x32_bf16 v[104:107], a[180:183], a[140:143], v[104:107]\n" \
"ds_read_b64_tr_b16 a[172:173], v35 offset:32768\n" \
"v_mfma_f32_16x16x32_bf16 v[104:107], a[244:247], a[204:207], v[104:107]\n" \
"ds_read_b64_tr_b16 a[174:175], v35 offset:33280\n" \
"v_mfma_f32_16x16x32_bf16 v[100:103], a[176:179], a[140:143], v[100:103]\n" \
"ds_read_b64_tr_b16 a[236:237], v35 offset:36864\n" \
"v_mfma_f32_16x16x32_bf16 v[100:103], a[240:243], a[204:207], v[100:103]\n" \
"ds_read_b128 a[140:143], v28 offset:38912\n" \
"v_mfma_f32_16x16x32_bf16 v[116:119], a[176:179], a[144:147], v[116:119]\n" \
"ds_read_b128 a[204:207], v29 offset:38912\n" \
"v_mfma_f32_16x16x32_bf16 v[116:119], a[240:243], a[208:211], v[116:119]\n" \
"s_add_u32 m0, s31, 0x0\n" \
"s_mov_b32 s84, 0x200000\n" \
"v_mfma_f32_16x16x32_bf16 v[120:123], a[180:183], a[144:147], v[120:123]\n" \
"buffer_load_dwordx4 v26, s[68:71], s84 offen offset:0 lds\n" \
"v_mfma_f32_16x16x32_bf16 v[120:123], a[244:247], a[208:211], v[120:123]\n" \
"s_addk_i32 s84, 0x7c00\n" \
"v_mfma_f32_16x16x32_bf16 v[124:127], a[184:187], a[144:147], v[124:127]\n" \
"buffer_load_dwordx4 v26, s[68:71], s84 offen offset:1024 lds\n" \
"v_mfma_f32_16x16x32_bf16 v[124:127], a[248:251], a[208:211], v[124:127]\n" \
"ds_read_b64_tr_b16 a[238:239], v35 offset:37376\n" \
"v_mfma_f32_16x16x32_bf16 v[128:131], a[188:191], a[144:147], v[128:131]\n" \
"ds_read_b64_tr_b16 a[168:169], v34 offset:32768\n" \
"v_mfma_f32_16x16x32_bf16 v[128:131], a[252:255], a[208:211], v[128:131]\n" \
"ds_read_b128 a[144:147], v28 offset:40960\n" \
"v_mfma_f32_16x16x32_bf16 v[144:147], a[188:191], a[148:151], v[144:147]\n" \
"ds_read_b128 a[208:211], v29 offset:40960\n" \
"v_mfma_f32_16x16x32_bf16 v[144:147], a[252:255], a[212:215], v[144:147]\n" \
"s_addk_i32 s84, 0x7c00\n" \
"v_mfma_f32_16x16x32_bf16 v[140:143], a[184:187], a[148:151], v[140:143]\n" \
"buffer_load_dwordx4 v27, s[68:71], s84 offen offset:2048 lds\n" \
"v_mfma_f32_16x16x32_bf16 v[140:143], a[248:251], a[212:215], v[140:143]\n" \
"s_addk_i32 s84, 0x7c00\n" \
"v_mfma_f32_16x16x32_bf16 v[136:139], a[180:183], a[148:151], v[136:139]\n" \
"buffer_load_dwordx4 v27, s[68:71], s84 offen offset:3072 lds\n" \
"v_mfma_f32_16x16x32_bf16 v[136:139], a[244:247], a[212:215], v[136:139]\n" \
"ds_read_b64_tr_b16 a[170:171], v34 offset:33280\n" \
"v_mfma_f32_16x16x32_bf16 v[132:135], a[176:179], a[148:151], v[132:135]\n" \
"ds_read_b64_tr_b16 a[232:233], v34 offset:36864\n" \
"v_mfma_f32_16x16x32_bf16 v[132:135], a[240:243], a[212:215], v[132:135]\n" \
"ds_read_b128 a[148:151], v28 offset:43008\n" \
"v_mfma_f32_16x16x32_bf16 v[148:151], a[176:179], a[152:155], v[148:151]\n" \
"ds_read_b128 a[212:215], v29 offset:43008\n" \
"v_mfma_f32_16x16x32_bf16 v[148:151], a[240:243], a[216:219], v[148:151]\n" \
"s_add_u32 m0, s30, 0x1000\n" \
"s_mov_b32 s84, 0x9200\n" \
"v_mfma_f32_16x16x32_bf16 v[152:155], a[180:183], a[152:155], v[152:155]\n" \
"buffer_load_dwordx4 v24, s[76:79], s84 offen offset:0 lds\n" \
"v_mfma_f32_16x16x32_bf16 v[152:155], a[244:247], a[216:219], v[152:155]\n" \
"s_addk_i32 s84, 0x2000\n" \
"s_nop 0\n" \
"v_mfma_f32_16x16x32_bf16 v[156:159], a[184:187], a[152:155], v[156:159]\n" \
"buffer_load_dwordx4 v25, s[76:79], s84 offen offset:1024 lds\n" \
"v_mfma_f32_16x16x32_bf16 v[156:159], a[248:251], a[216:219], v[156:159]\n" \
"ds_read_b64_tr_b16 a[234:235], v34 offset:37376\n" \
"v_mfma_f32_16x16x32_bf16 v[160:163], a[188:191], a[152:155], v[160:163]\n" \
"ds_read_b128 v[248:251], v22 offset:32768\n" \
"v_mfma_f32_16x16x32_bf16 v[160:163], a[252:255], a[216:219], v[160:163]\n" \
"ds_read_b128 a[152:155], v28 offset:45056\n" \
"v_mfma_f32_16x16x32_bf16 v[176:179], a[188:191], a[156:159], v[176:179]\n" \
"ds_read_b128 a[216:219], v29 offset:45056\n" \
"v_mfma_f32_16x16x32_bf16 v[176:179], a[252:255], a[220:223], v[176:179]\n" \
"ds_read_b128 v[48:51], v23 offset:32768\n" \
"v_mfma_f32_16x16x32_bf16 v[172:175], a[184:187], a[156:159], v[172:175]\n" \
"s_cmpk_lt_u32 s3, 3\n" \
"s_cselect_b32 s18, s18, s17\n" \
"v_mfma_f32_16x16x32_bf16 v[172:175], a[248:251], a[220:223], v[172:175]\n" \
"s_bitset1_b32 s16, 17\n" \
"v_writelane_b32 v11, s18, 22\n" \
"v_mfma_f32_16x16x32_bf16 v[168:171], a[180:183], a[156:159], v[168:171]\n" \
"s_movk_i32 s18, 0x2400\n" \
"v_mfma_f32_16x16x32_bf16 v[168:171], a[244:247], a[220:223], v[168:171]\n" \
"s_cmpk_lt_u32 s3, 3\n" \
"s_cselect_b32 s16, s16, s18\n" \
"v_mfma_f32_16x16x32_bf16 v[164:167], a[176:179], a[156:159], v[164:167]\n" \
"s_xor_b32 s13, s13, 2\n" \
"v_writelane_b32 v11, s16, 23\n" \
"v_mfma_f32_16x16x32_bf16 v[164:167], a[240:243], a[220:223], v[164:167]\n" \
"ds_read_b128 a[156:159], v28 offset:47104\n" \
"ds_read_b128 a[220:223], v29 offset:47104\n" \
"s_waitcnt lgkmcnt(15)\n" \
"v_mfma_f32_16x16x32_bf16 a[0:3], a[160:163], a[128:131], a[0:3]\n" \
"s_addk_i32 s84, 0x2000\n" \
"v_mfma_f32_16x16x32_bf16 a[0:3], a[224:227], a[192:195], a[0:3]\n" \
"buffer_load_dwordx4 v24, s[76:79], s84 offen offset:2048 lds\n" \
"v_mfma_f32_16x16x32_bf16 a[4:7], a[164:167], a[128:131], a[4:7]\n" \
"ds_read_b64_tr_b16 a[176:177], v36 offset:32768\n" \
"s_lshl_b32 s16, s3, 10\n" \
"v_mfma_f32_16x16x32_bf16 a[4:7], a[228:231], a[192:195], a[4:7]\n" \
"s_addk_i32 s84, 0x2000\n" \
"v_mfma_f32_16x16x32_bf16 a[12:15], a[172:175], a[128:131], a[12:15]\n" \
"buffer_load_dwordx4 v25, s[76:79], s84 offen offset:3072 lds\n" \
"s_waitcnt lgkmcnt(15)\n" \
"v_mfma_f32_16x16x32_bf16 a[12:15], a[236:239], a[192:195], a[12:15]\n" \
"ds_read_b64_tr_b16 a[178:179], v36 offset:33792\n" \
"s_or_b32 s16, s16, 0xf000\n" \
"s_waitcnt lgkmcnt(12)\n" \
"v_mfma_f32_16x16x32_bf16 a[8:11], a[128:131], a[168:171], a[8:11]\n" \
"ds_read_b64_tr_b16 a[240:241], v36 offset:40960\n" \
"v_writelane_b32 v11, s16, 24\n" \
"s_waitcnt lgkmcnt(9)\n" \
"v_mfma_f32_16x16x32_bf16 a[8:11], a[192:195], a[232:235], a[8:11]\n" \
"ds_read_b64_tr_b16 a[242:243], v36 offset:41984\n" \
"s_add_i32 s16, s15, 1\n" \
"v_mfma_f32_16x16x32_bf16 a[24:27], a[132:135], a[168:171], a[24:27]\n" \
"ds_read_b64_tr_b16 a[180:181], v37 offset:32768\n" \
"s_and_b32 s16, s16, 3\n" \
"v_mfma_f32_16x16x32_bf16 a[24:27], a[196:199], a[232:235], a[24:27]\n" \
"ds_read_b64_tr_b16 a[182:183], v37 offset:33792\n" \
"s_or_b32 s16, s16, s5\n" \
"v_mfma_f32_16x16x32_bf16 a[28:31], a[172:175], a[132:135], a[28:31]\n" \
"ds_read_b64_tr_b16 a[244:245], v37 offset:40960\n" \
"s_mul_i32 s18, s16, 0x24000\n" \
"v_mfma_f32_16x16x32_bf16 a[28:31], a[236:239], a[196:199], a[28:31]\n" \
"s_add_u32 s89, s42, 0x0\n" \
"buffer_store_dwordx4 v[216:219], v20, s[80:83], s89 offen offset:0 sc1\n" \
"v_mfma_f32_16x16x32_bf16 a[20:23], a[164:167], a[132:135], a[20:23]\n" \
"ds_read_b64_tr_b16 a[246:247], v37 offset:41984\n" \
"s_add_i32 s18, s18, s19\n" \
"v_mfma_f32_16x16x32_bf16 a[20:23], a[228:231], a[196:199], a[20:23]\n" \
"ds_read_b64_tr_b16 a[184:185], v38 offset:32768\n" \
"s_add_i32 s15, s15, -1\n" \
"v_mfma_f32_16x16x32_bf16 a[16:19], a[160:163], a[132:135], a[16:19]\n" \
"ds_read_b64_tr_b16 a[186:187], v38 offset:33792\n" \
"v_writelane_b32 v11, s18, 26\n" \
"v_mfma_f32_16x16x32_bf16 a[16:19], a[224:227], a[196:199], a[16:19]\n" \
"s_mul_i32 s18, s13, 0x24000\n" \
"s_and_b32 s15, s15, 3\n" \
"v_mfma_f32_16x16x32_bf16 a[32:35], a[160:163], a[136:139], a[32:35]\n" \
"s_add_u32 s88, s42, 0x1000\n" \
"buffer_store_dwordx4 v[220:223], v20, s[80:83], s88 offen offset:0 sc1\n" \
"v_mfma_f32_16x16x32_bf16 a[32:35], a[224:227], a[200:203], a[32:35]\n" \
"ds_read_b64_tr_b16 a[248:249], v38 offset:40960\n" \
"s_add_i32 s18, s18, s19\n" \
"v_mfma_f32_16x16x32_bf16 a[36:39], a[164:167], a[136:139], a[36:39]\n" \
"ds_read_b64_tr_b16 a[250:251], v38 offset:41984\n" \
"s_or_b32 s15, s15, s5\n" \
"v_mfma_f32_16x16x32_bf16 a[36:39], a[228:231], a[200:203], a[36:39]\n" \
"ds_read_b64_tr_b16 a[188:189], v39 offset:32768\n" \
"v_writelane_b32 v11, s18, 27\n" \
"v_mfma_f32_16x16x32_bf16 a[44:47], a[172:175], a[136:139], a[44:47]\n" \
"s_mul_i32 s18, s15, 0x24000\n" \
"s_add_i32 s18, s18, s19\n" \
"v_mfma_f32_16x16x32_bf16 a[44:47], a[236:239], a[200:203], a[44:47]\n" \
"ds_read_b64_tr_b16 a[190:191], v39 offset:33792\n" \
"v_writelane_b32 v11, s18, 28\n" \
"v_mfma_f32_16x16x32_bf16 a[40:43], a[136:139], a[168:171], a[40:43]\n" \
"s_add_u32 s89, s42, 0x0\n" \
"buffer_store_dwordx4 v[192:195], v20, s[80:83], s89 offen offset:2048 sc1\n" \
"v_mfma_f32_16x16x32_bf16 a[40:43], a[200:203], a[232:235], a[40:43]\n" \
"ds_read_b64_tr_b16 a[252:253], v39 offset:40960\n" \
"s_or_b32 s18, s9, s14\n" \
"v_mfma_f32_16x16x32_bf16 a[56:59], a[140:143], a[168:171], a[56:59]\n" \
"ds_read_b64_tr_b16 a[254:255], v39 offset:41984\n" \
"s_mul_i32 s18, s18, 0x24800\n" \
"v_mfma_f32_16x16x32_bf16 a[56:59], a[204:207], a[232:235], a[56:59]\n" \
"s_lshl_b32 s16, s16, 12\n" \
"s_add_i32 s19, s18, 0x3004800\n" \
"v_mfma_f32_16x16x32_bf16 a[60:63], a[172:175], a[140:143], a[60:63]\n" \
"s_add_i32 s20, s19, s16\n" \
"v_writelane_b32 v11, s20, 29\n" \
"v_mfma_f32_16x16x32_bf16 a[60:63], a[236:239], a[204:207], a[60:63]\n" \
"s_add_u32 s88, s42, 0x0\n" \
"buffer_store_dwordx4 v[196:199], v20, s[80:83], s88 offen offset:3072 sc1\n" \
"v_mfma_f32_16x16x32_bf16 a[52:55], a[164:167], a[140:143], a[52:55]\n" \
"s_lshl_b32 s13, s13, 12\n" \
"s_lshl_b32 s15, s15, 12\n" \
"v_mfma_f32_16x16x32_bf16 a[52:55], a[228:231], a[204:207], a[52:55]\n" \
"s_sub_i32 s14, s9, s14\n" \
"s_add_i32 s20, s19, s13\n" \
"v_mfma_f32_16x16x32_bf16 a[48:51], a[160:163], a[140:143], a[48:51]\n" \
"v_writelane_b32 v11, s20, 30\n" \
"s_add_i32 s19, s19, s15\n" \
"v_mfma_f32_16x16x32_bf16 a[48:51], a[224:227], a[204:207], a[48:51]\n" \
"s_mul_i32 s14, s14, 0x24800\n" \
"v_writelane_b32 v11, s19, 31\n" \
"s_waitcnt lgkmcnt(15)\n" \
"v_mfma_f32_16x16x32_bf16 a[64:67], a[164:167], v[248:251], a[64:67]\n" \
"s_add_i32 s19, s14, 0x4120800\n" \
"s_add_i32 s16, s19, s16\n" \
"v_mfma_f32_16x16x32_bf16 a[64:67], a[228:231], v[48:51], a[64:67]\n" \
"v_writelane_b32 v11, s16, 32\n" \
"s_add_i32 s13, s19, s13\n" \
"v_mfma_f32_16x16x32_bf16 a[80:83], a[172:175], v[248:251], a[80:83]\n" \
"v_writelane_b32 v11, s13, 33\n" \
"s_add_i32 s19, s19, s15\n" \
"v_mfma_f32_16x16x32_bf16 a[80:83], a[236:239], v[48:51], a[80:83]\n" \
"s_or_b32 s13, s18, 0x3000000\n" \
"s_lshl_b32 s15, s6, 11\n" \
"v_mfma_f32_16x16x32_bf16 a[68:71], v[248:251], a[168:171], a[68:71]\n" \
"v_writelane_b32 v11, s19, 34\n" \
"s_add_i32 s16, s13, s15\n" \
"v_mfma_f32_16x16x32_bf16 a[68:71], v[48:51], a[232:235], a[68:71]\n" \
"s_waitcnt vmcnt(4) lgkmcnt(0)\n" \
"s_barrier\n" \
"v_mov_b32_e32 v45, s62\n" \
"s_mov_b64 exec, 1\n" \
"buffer_store_dword v45, off, s[80:83], s37 offset:0 sc1\n" \
"s_mov_b64 exec, -1\n" \
"s_nop 0\n" \
"v_mfma_f32_16x16x32_bf16 v[52:55], a[176:179], a[128:131], v[52:55]\n" \
"s_add_u32 m0, s40, 0x8000\n" \
"s_add_u32 s85, s39, 0xa000\n" \
"v_mfma_f32_16x16x32_bf16 v[52:55], a[240:243], a[192:195], v[52:55]\n" \
"buffer_load_dwordx4 v20, s[80:83], s85 offen sc1 lds\n" \
"v_mfma_f32_16x16x32_bf16 v[56:59], a[180:183], a[128:131], v[56:59]\n" \
"ds_read_b64_tr_b16 a[160:161], v32 offset:0\n" \
"v_accvgpr_read_b32 v220, a120\n" \
"v_mfma_f32_16x16x32_bf16 v[56:59], a[244:247], a[192:195], v[56:59]\n" \
"ds_read_b64_tr_b16 a[162:163], v32 offset:512\n" \
"v_accvgpr_read_b32 v221, a121\n" \
"v_mfma_f32_16x16x32_bf16 v[60:63], a[184:187], a[128:131], v[60:63]\n" \
"s_add_u32 m0, s40, 0x9000\n" \
"s_add_u32 s85, s39, 0xb000\n" \
"v_mfma_f32_16x16x32_bf16 v[60:63], a[248:251], a[192:195], v[60:63]\n" \
"buffer_load_dwordx4 v20, s[80:83], s85 offen sc1 lds\n" \
"v_mfma_f32_16x16x32_bf16 v[64:67], a[188:191], a[128:131], v[64:67]\n" \
"ds_read_b64_tr_b16 a[224:225], v32 offset:4096\n" \
"v_accvgpr_read_b32 v222, a122\n" \
"v_mfma_f32_16x16x32_bf16 v[64:67], a[252:255], a[192:195], v[64:67]\n" \
"ds_read_b128 a[128:131], v28 offset:0\n" \
"v_accvgpr_read_b32 v223, a123\n" \
"v_mfma_f32_16x16x32_bf16 v[80:83], a[188:191], a[132:135], v[80:83]\n" \
"ds_read_b128 a[192:195], v29 offset:0\n" \
"v_accvgpr_read_b32 v180, a124\n" \
"v_mfma_f32_16x16x32_bf16 v[80:83], a[252:255], a[196:199], v[80:83]\n" \
"s_add_u32 m0, s40, 0xb000\n" \
"s_movk_i32 s84, 0x280\n" \
"v_mfma_f32_16x16x32_bf16 v[76:79], a[184:187], a[132:135], v[76:79]\n" \
"buffer_load_dwordx4 v21, s[76:79], s84 offen offset:0 lds\n" \
"v_mfma_f32_16x16x32_bf16 v[76:79], a[248:251], a[196:199], v[76:79]\n" \
"ds_read_b64_tr_b16 a[226:227], v32 offset:4608\n" \
"v_accvgpr_read_b32 v181, a125\n" \
"v_mfma_f32_16x16x32_bf16 v[72:75], a[180:183], a[132:135], v[72:75]\n" \
"s_add_u32 m0, s30, 0x8000\n" \
"s_movk_i32 s84, 0x280\n" \
"v_mfma_f32_16x16x32_bf16 v[72:75], a[244:247], a[196:199], v[72:75]\n" \
"buffer_load_dwordx4 v24, s[76:79], s84 offen offset:0 lds\n" \
"v_mfma_f32_16x16x32_bf16 v[68:71], a[176:179], a[132:135], v[68:71]\n" \
"ds_read_b64_tr_b16 a[164:165], v33 offset:0\n" \
"v_accvgpr_read_b32 v182, a126\n" \
"v_mfma_f32_16x16x32_bf16 v[68:71], a[240:243], a[196:199], v[68:71]\n" \
"ds_read_b128 a[132:135], v28 offset:2048\n" \
"v_accvgpr_read_b32 v183, a127\n" \
"v_mfma_f32_16x16x32_bf16 v[84:87], a[176:179], a[136:139], v[84:87]\n" \
"ds_read_b128 a[196:199], v29 offset:2048\n" \
"v_cvt_pk_bf16_f32 v220, v220, v221\n" \
"v_mfma_f32_16x16x32_bf16 v[84:87], a[240:243], a[200:203], v[84:87]\n" \
"s_movk_i32 s84, 0x2280\n" \
"v_mfma_f32_16x16x32_bf16 v[88:91], a[180:183], a[136:139], v[88:91]\n" \
"buffer_load_dwordx4 v25, s[76:79], s84 offen offset:1024 lds\n" \
"v_mfma_f32_16x16x32_bf16 v[88:91], a[244:247], a[200:203], v[88:91]\n" \
"ds_read_b64_tr_b16 a[166:167], v33 offset:512\n" \
"v_cvt_pk_bf16_f32 v221, v222, v223\n" \
"v_mfma_f32_16x16x32_bf16 v[92:95], a[184:187], a[136:139], v[92:95]\n" \
"ds_read_b64_tr_b16 a[228:229], v33 offset:4096\n" \
"v_cvt_pk_bf16_f32 v222, v180, v181\n" \
"v_mfma_f32_16x16x32_bf16 v[92:95], a[248:251], a[200:203], v[92:95]\n" \
"s_movk_i32 s84, 0x4280\n" \
"v_mfma_f32_16x16x32_bf16 v[96:99], a[188:191], a[136:139], v[96:99]\n" \
"buffer_load_dwordx4 v24, s[76:79], s84 offen offset:2048 lds\n" \
"v_mfma_f32_16x16x32_bf16 v[96:99], a[252:255], a[200:203], v[96:99]\n" \
"ds_read_b128 a[136:139], v28 offset:4096\n" \
"v_cvt_pk_bf16_f32 v223, v182, v183\n" \
"v_mfma_f32_16x16x32_bf16 v[112:115], a[188:191], a[140:143], v[112:115]\n" \
"ds_read_b128 a[200:203], v29 offset:4096\n" \
"v_accvgpr_read_b32 v216, a112\n" \
"v_mfma_f32_16x16x32_bf16 v[112:115], a[252:255], a[204:207], v[112:115]\n" \
"s_movk_i32 s84, 0x6280\n" \
"s_nop 0\n" \
"v_mfma_f32_16x16x32_bf16 v[108:111], a[184:187], a[140:143], v[108:111]\n" \
"buffer_load_dwordx4 v25, s[76:79], s84 offen offset:3072 lds\n" \
"v_mfma_f32_16x16x32_bf16 v[108:111], a[248:251], a[204:207], v[108:111]\n" \
"ds_read_b64_tr_b16 a[230:231], v33 offset:4608\n" \
"v_accvgpr_read_b32 v217, a113\n" \
"v_mfma_f32_16x16x32_bf16 v[104:107], a[180:183], a[140:143], v[104:107]\n" \
"ds_read_b64_tr_b16 a[172:173], v35 offset:0\n" \
"v_accvgpr_read_b32 v218, a114\n" \
"v_mfma_f32_16x16x32_bf16 v[104:107], a[244:247], a[204:207], v[104:107]\n" \
"ds_read_b64_tr_b16 a[174:175], v35 offset:512\n" \
"v_accvgpr_read_b32 v219, a115\n" \
"v_mfma_f32_16x16x32_bf16 v[100:103], a[176:179], a[140:143], v[100:103]\n" \
"ds_read_b64_tr_b16 a[236:237], v35 offset:4096\n" \
"v_accvgpr_read_b32 v180, a116\n" \
"v_mfma_f32_16x16x32_bf16 v[100:103], a[240:243], a[204:207], v[100:103]\n" \
"ds_read_b128 a[140:143], v28 offset:6144\n" \
"v_accvgpr_read_b32 v181, a117\n" \
"v_mfma_f32_16x16x32_bf16 v[116:119], a[176:179], a[144:147], v[116:119]\n" \
"ds_read_b128 a[204:207], v29 offset:6144\n" \
"v_accvgpr_read_b32 v182, a118\n" \
"v_mfma_f32_16x16x32_bf16 v[116:119], a[240:243], a[208:211], v[116:119]\n" \
"s_add_u32 m0, s31, 0x8000\n" \
"s_mov_b32 s84, 0x280000\n" \
"v_mfma_f32_16x16x32_bf16 v[120:123], a[180:183], a[144:147], v[120:123]\n" \
"buffer_load_dwordx4 v26, s[68:71], s84 offen offset:0 lds\n" \
"v_mfma_f32_16x16x32_bf16 v[120:123], a[244:247], a[208:211], v[120:123]\n" \
"s_addk_i32 s84, 0x7c00\n" \
"s_nop 0\n" \
"v_mfma_f32_16x16x32_bf16 v[124:127], a[184:187], a[144:147], v[124:127]\n" \
"buffer_load_dwordx4 v26, s[68:71], s84 offen offset:1024 lds\n" \
"v_mfma_f32_16x16x32_bf16 v[124:127], a[248:251], a[208:211], v[124:127]\n" \
"ds_read_b64_tr_b16 a[238:239], v35 offset:4608\n" \
"v_accvgpr_read_b32 v183, a119\n" \
"v_mfma_f32_16x16x32_bf16 v[128:131], a[188:191], a[144:147], v[128:131]\n" \
"ds_read_b64_tr_b16 a[168:169], v34 offset:0\n" \
"v_cvt_pk_bf16_f32 v216, v216, v217\n" \
"v_mfma_f32_16x16x32_bf16 v[128:131], a[252:255], a[208:211], v[128:131]\n" \
"ds_read_b128 a[144:147], v28 offset:8192\n" \
"v_cvt_pk_bf16_f32 v217, v218, v219\n" \
"v_mfma_f32_16x16x32_bf16 v[144:147], a[188:191], a[148:151], v[144:147]\n" \
"ds_read_b128 a[208:211], v29 offset:8192\n" \
"v_cvt_pk_bf16_f32 v218, v180, v181\n" \
"v_mfma_f32_16x16x32_bf16 v[144:147], a[252:255], a[212:215], v[144:147]\n" \
"s_addk_i32 s84, 0x7c00\n" \
"v_mfma_f32_16x16x32_bf16 v[140:143], a[184:187], a[148:151], v[140:143]\n" \
"buffer_load_dwordx4 v27, s[68:71], s84 offen offset:2048 lds\n" \
"v_mfma_f32_16x16x32_bf16 v[140:143], a[248:251], a[212:215], v[140:143]\n" \
"s_addk_i32 s84, 0x7c00\n" \
"v_mfma_f32_16x16x32_bf16 v[136:139], a[180:183], a[148:151], v[136:139]\n" \
"buffer_load_dwordx4 v27, s[68:71], s84 offen offset:3072 lds\n" \
"v_mfma_f32_16x16x32_bf16 v[136:139], a[244:247], a[212:215], v[136:139]\n" \
"ds_read_b64_tr_b16 a[170:171], v34 offset:512\n" \
"v_cvt_pk_bf16_f32 v219, v182, v183\n" \
"v_mfma_f32_16x16x32_bf16 v[132:135], a[176:179], a[148:151], v[132:135]\n" \
"ds_read_b64_tr_b16 a[232:233], v34 offset:4096\n" \
"v_accvgpr_read_b32 v196, a104\n" \
"v_mfma_f32_16x16x32_bf16 v[132:135], a[240:243], a[212:215], v[132:135]\n" \
"ds_read_b128 a[148:151], v28 offset:10240\n" \
"v_accvgpr_read_b32 v197, a105\n" \
"v_mfma_f32_16x16x32_bf16 v[148:151], a[176:179], a[152:155], v[148:151]\n" \
"ds_read_b128 a[212:215], v29 offset:10240\n" \
"v_accvgpr_read_b32 v198, a106\n" \
"v_mfma_f32_16x16x32_bf16 v[148:151], a[240:243], a[216:219], v[148:151]\n" \
"s_add_u32 m0, s30, 0x9000\n" \
"s_mov_b32 s84, 0x9280\n" \
"v_mfma_f32_16x16x32_bf16 v[152:155], a[180:183], a[152:155], v[152:155]\n" \
"buffer_load_dwordx4 v24, s[76:79], s84 offen offset:0 lds\n" \
"v_mfma_f32_16x16x32_bf16 v[152:155], a[244:247], a[216:219], v[152:155]\n" \
"s_addk_i32 s84, 0x2000\n" \
"s_nop 0\n" \
"v_mfma_f32_16x16x32_bf16 v[156:159], a[184:187], a[152:155], v[156:159]\n" \
"buffer_load_dwordx4 v25, s[76:79], s84 offen offset:1024 lds\n" \
"v_mfma_f32_16x16x32_bf16 v[156:159], a[248:251], a[216:219], v[156:159]\n" \
"ds_read_b64_tr_b16 a[234:235], v34 offset:4608\n" \
"v_accvgpr_read_b32 v199, a107\n" \
"v_mfma_f32_16x16x32_bf16 v[160:163], a[188:191], a[152:155], v[160:163]\n" \
"ds_read_b128 v[248:251], v22 offset:0\n" \
"v_accvgpr_read_b32 v180, a108\n" \
"v_mfma_f32_16x16x32_bf16 v[160:163], a[252:255], a[216:219], v[160:163]\n" \
"ds_read_b128 a[152:155], v28 offset:12288\n" \
"v_accvgpr_read_b32 v181, a109\n" \
"v_mfma_f32_16x16x32_bf16 v[176:179], a[188:191], a[156:159], v[176:179]\n" \
"ds_read_b128 a[216:219], v29 offset:12288\n" \
"v_accvgpr_read_b32 v182, a110\n" \
"v_mfma_f32_16x16x32_bf16 v[176:179], a[252:255], a[220:223], v[176:179]\n" \
"ds_read_b128 v[48:51], v23 offset:0\n" \
"v_accvgpr_read_b32 v183, a111\n" \
"v_mfma_f32_16x16x32_bf16 v[172:175], a[184:187], a[156:159], v[172:175]\n" \
"v_cvt_pk_bf16_f32 v196, v196, v197\n" \
"v_cvt_pk_bf16_f32 v197, v198, v199\n" \
"v_mfma_f32_16x16x32_bf16 v[172:175], a[248:251], a[220:223], v[172:175]\n" \
"v_cvt_pk_bf16_f32 v198, v180, v181\n" \
"v_cvt_pk_bf16_f32 v199, v182, v183\n" \
"v_mfma_f32_16x16x32_bf16 v[168:171], a[180:183], a[156:159], v[168:171]\n" \
"v_accvgpr_read_b32 v192, a96\n" \
"v_accvgpr_read_b32 v193, a97\n" \
"v_mfma_f32_16x16x32_bf16 v[168:171], a[244:247], a[220:223], v[168:171]\n" \
"v_accvgpr_read_b32 v194, a98\n" \
"v_accvgpr_read_b32 v195, a99\n" \
"v_mfma_f32_16x16x32_bf16 v[164:167], a[176:179], a[156:159], v[164:167]\n" \
"v_accvgpr_read_b32 v180, a100\n" \
"v_accvgpr_read_b32 v181, a101\n" \
"v_mfma_f32_16x16x32_bf16 v[164:167], a[240:243], a[220:223], v[164:167]\n" \
"ds_read_b128 a[156:159], v28 offset:14336\n" \
"ds_read_b128 a[220:223], v29 offset:14336\n" \
"s_waitcnt lgkmcnt(15)\n" \
"v_mfma_f32_16x16x32_bf16 a[0:3], a[160:163], a[128:131], a[0:3]\n" \
"s_addk_i32 s84, 0x2000\n" \
"v_mfma_f32_16x16x32_bf16 a[0:3], a[224:227], a[192:195], a[0:3]\n" \
"buffer_load_dwordx4 v24, s[76:79], s84 offen offset:2048 lds\n" \
"v_mfma_f32_16x16x32_bf16 a[4:7], a[164:167], a[128:131], a[4:7]\n" \
"ds_read_b64_tr_b16 a[176:177], v36 offset:0\n" \
"v_accvgpr_read_b32 v182, a102\n" \
"v_mfma_f32_16x16x32_bf16 a[4:7], a[228:231], a[192:195], a[4:7]\n" \
"s_addk_i32 s84, 0x2000\n" \
"v_mfma_f32_16x16x32_bf16 a[12:15], a[172:175], a[128:131], a[12:15]\n" \
"buffer_load_dwordx4 v25, s[76:79], s84 offen offset:3072 lds\n" \
"s_waitcnt lgkmcnt(15)\n" \
"v_mfma_f32_16x16x32_bf16 a[12:15], a[236:239], a[192:195], a[12:15]\n" \
"ds_read_b64_tr_b16 a[178:179], v36 offset:1024\n" \
"v_accvgpr_read_b32 v183, a103\n" \
"s_waitcnt lgkmcnt(12)\n" \
"v_mfma_f32_16x16x32_bf16 a[8:11], a[128:131], a[168:171], a[8:11]\n" \
"ds_read_b64_tr_b16 a[240:241], v36 offset:8192\n" \
"v_cvt_pk_bf16_f32 v192, v192, v193\n" \
"s_waitcnt lgkmcnt(9)\n" \
"v_mfma_f32_16x16x32_bf16 a[8:11], a[192:195], a[232:235], a[8:11]\n" \
"ds_read_b64_tr_b16 a[242:243], v36 offset:9216\n" \
"v_cvt_pk_bf16_f32 v193, v194, v195\n" \
"v_mfma_f32_16x16x32_bf16 a[24:27], a[132:135], a[168:171], a[24:27]\n" \
"ds_read_b64_tr_b16 a[180:181], v37 offset:0\n" \
"v_cvt_pk_bf16_f32 v194, v180, v181\n" \
"v_mfma_f32_16x16x32_bf16 a[24:27], a[196:199], a[232:235], a[24:27]\n" \
"ds_read_b64_tr_b16 a[182:183], v37 offset:1024\n" \
"v_cvt_pk_bf16_f32 v195, v182, v183\n" \
"v_mfma_f32_16x16x32_bf16 a[28:31], a[172:175], a[132:135], a[28:31]\n" \
"ds_read_b64_tr_b16 a[244:245], v37 offset:8192\n" \
"v_accvgpr_read_b32 v188, a88\n" \
"v_mfma_f32_16x16x32_bf16 a[28:31], a[236:239], a[196:199], a[28:31]\n" \
"ds_read_b64_tr_b16 a[246:247], v37 offset:9216\n" \
"v_accvgpr_read_b32 v189, a89\n" \
"v_mfma_f32_16x16x32_bf16 a[20:23], a[164:167], a[132:135], a[20:23]\n" \
"v_accvgpr_read_b32 v190, a90\n" \
"v_accvgpr_read_b32 v191, a91\n" \
"v_mfma_f32_16x16x32_bf16 a[20:23], a[228:231], a[196:199], a[20:23]\n" \
"s_add_u32 s89, s42, 0x1000\n" \
"buffer_store_dwordx4 a[84:87], v20, s[80:83], s89 offen offset:3072 sc1\n" \
"v_mfma_f32_16x16x32_bf16 a[16:19], a[160:163], a[132:135], a[16:19]\n" \
"ds_read_b64_tr_b16 a[184:185], v38 offset:0\n" \
"v_accvgpr_read_b32 v180, a92\n" \
"v_mfma_f32_16x16x32_bf16 a[16:19], a[224:227], a[196:199], a[16:19]\n" \
"ds_read_b64_tr_b16 a[186:187], v38 offset:1024\n" \
"v_accvgpr_read_b32 v181, a93\n" \
"v_mfma_f32_16x16x32_bf16 a[32:35], a[160:163], a[136:139], a[32:35]\n" \
"ds_read_b64_tr_b16 a[248:249], v38 offset:8192\n" \
"v_accvgpr_read_b32 v182, a94\n" \
"v_mfma_f32_16x16x32_bf16 a[32:35], a[224:227], a[200:203], a[32:35]\n" \
"ds_read_b64_tr_b16 a[250:251], v38 offset:9216\n" \
"v_accvgpr_read_b32 v183, a95\n" \
"v_mfma_f32_16x16x32_bf16 a[36:39], a[164:167], a[136:139], a[36:39]\n" \
"v_cvt_pk_bf16_f32 v188, v188, v189\n" \
"v_cvt_pk_bf16_f32 v189, v190, v191\n" \
"v_mfma_f32_16x16x32_bf16 a[36:39], a[228:231], a[200:203], a[36:39]\n" \
"s_add_u32 s88, s42, 0x1000\n" \
"buffer_store_dwordx4 v[252:255], v20, s[80:83], s88 offen offset:2048 sc1\n" \
"v_mfma_f32_16x16x32_bf16 a[44:47], a[172:175], a[136:139], a[44:47]\n" \
"ds_read_b64_tr_b16 a[188:189], v39 offset:0\n" \
"v_cvt_pk_bf16_f32 v190, v180, v181\n" \
"v_mfma_f32_16x16x32_bf16 a[44:47], a[236:239], a[200:203], a[44:47]\n" \
"ds_read_b64_tr_b16 a[190:191], v39 offset:1024\n" \
"v_cvt_pk_bf16_f32 v191, v182, v183\n" \
"v_mfma_f32_16x16x32_bf16 a[40:43], a[136:139], a[168:171], a[40:43]\n" \
"ds_read_b64_tr_b16 a[252:253], v39 offset:8192\n" \
"v_accvgpr_read_b32 v184, a72\n" \
"v_mfma_f32_16x16x32_bf16 a[40:43], a[200:203], a[232:235], a[40:43]\n" \
"v_accvgpr_read_b32 v185, a73\n" \
"v_accvgpr_read_b32 v186, a74\n" \
"v_mfma_f32_16x16x32_bf16 a[56:59], a[140:143], a[168:171], a[56:59]\n" \
"ds_read_b64_tr_b16 a[254:255], v39 offset:9216\n" \
"v_accvgpr_read_b32 v187, a75\n" \
"v_mfma_f32_16x16x32_bf16 a[56:59], a[204:207], a[232:235], a[56:59]\n" \
"v_accvgpr_read_b32 v180, a76\n" \
"v_accvgpr_read_b32 v181, a77\n" \
"v_mfma_f32_16x16x32_bf16 a[60:63], a[172:175], a[140:143], a[60:63]\n" \
"s_add_u32 s89, s42, 0x2000\n" \
"buffer_store_dwordx4 v[200:203], v20, s[80:83], s89 offen offset:2048 sc1\n" \
"v_mfma_f32_16x16x32_bf16 a[60:63], a[236:239], a[204:207], a[60:63]\n" \
"v_accvgpr_read_b32 v182, a78\n" \
"v_accvgpr_read_b32 v183, a79\n" \
"v_mfma_f32_16x16x32_bf16 a[52:55], a[164:167], a[140:143], a[52:55]\n" \
"v_cvt_pk_bf16_f32 v184, v184, v185\n" \
"v_cvt_pk_bf16_f32 v185, v186, v187\n" \
"v_mfma_f32_16x16x32_bf16 a[52:55], a[228:231], a[204:207], a[52:55]\n" \
"v_cvt_pk_bf16_f32 v186, v180, v181\n" \
"v_cvt_pk_bf16_f32 v187, v182, v183\n" \
"v_mfma_f32_16x16x32_bf16 a[48:51], a[160:163], a[140:143], a[48:51]\n" \
"v_writelane_b32 v11, s16, 35\n" \
"s_add_i32 s16, s14, 0x411c000\n" \
"v_mfma_f32_16x16x32_bf16 a[48:51], a[224:227], a[204:207], a[48:51]\n" \
"s_add_i32 s19, s16, s15\n" \
"v_writelane_b32 v11, s19, 36\n" \
"s_waitcnt lgkmcnt(15)\n" \
"v_mfma_f32_16x16x32_bf16 a[64:67], a[164:167], v[248:251], a[64:67]\n" \
"s_add_i32 s18, s18, 0x3004000\n" \
"v_writelane_b32 v11, s18, 37\n" \
"v_mfma_f32_16x16x32_bf16 a[64:67], a[228:231], v[48:51], a[64:67]\n" \
"s_add_i32 s14, s14, 0x4120000\n" \
"v_writelane_b32 v11, s14, 38\n" \
"v_mfma_f32_16x16x32_bf16 a[80:83], a[172:175], v[248:251], a[80:83]\n" \
"s_lshl_b32 s14, s4, 2\n" \
"s_or_b32 s8, s14, s8\n" \
"v_mfma_f32_16x16x32_bf16 a[80:83], a[236:239], v[48:51], a[80:83]\n" \
"s_mul_i32 s8, s8, 0x90000\n" \
"s_lshl_b32 s14, s3, 14\n" \
"v_mfma_f32_16x16x32_bf16 a[68:71], v[248:251], a[168:171], a[68:71]\n" \
"s_add_i32 s8, s8, s14\n" \
"s_lshl_b32 s9, s9, 4\n" \
"v_mfma_f32_16x16x32_bf16 a[68:71], v[48:51], a[232:235], a[68:71]\n" \
"s_waitcnt vmcnt(3) lgkmcnt(0)\n" \
"s_barrier\n" \
"v_mfma_f32_16x16x32_bf16 v[52:55], a[176:179], a[128:131], v[52:55]\n" \
"s_add_u32 m0, s40, 0x0\n" \
"s_add_u32 s85, s39, 0xc000\n" \
"v_mfma_f32_16x16x32_bf16 v[52:55], a[240:243], a[192:195], v[52:55]\n" \
"buffer_load_dwordx4 v20, s[80:83], s85 offen sc1 lds\n" \
"v_mfma_f32_16x16x32_bf16 v[56:59], a[180:183], a[128:131], v[56:59]\n" \
"ds_read_b64_tr_b16 a[160:161], v32 offset:32768\n" \
"s_lshl_b32 s14, s4, 7\n" \
"v_mfma_f32_16x16x32_bf16 v[56:59], a[244:247], a[192:195], v[56:59]\n" \
"ds_read_b64_tr_b16 a[162:163], v32 offset:33280\n" \
"s_or_b32 s18, s9, s14\n" \
"v_mfma_f32_16x16x32_bf16 v[60:63], a[184:187], a[128:131], v[60:63]\n" \
"s_add_u32 m0, s40, 0x1000\n" \
"s_add_u32 s85, s39, 0xd000\n" \
"v_mfma_f32_16x16x32_bf16 v[60:63], a[248:251], a[192:195], v[60:63]\n" \
"buffer_load_dwordx4 v20, s[80:83], s85 offen sc1 lds\n" \
"v_mfma_f32_16x16x32_bf16 v[64:67], a[188:191], a[128:131], v[64:67]\n" \
"ds_read_b64_tr_b16 a[224:225], v32 offset:36864\n" \
"s_sub_i32 s9, s9, s14\n" \
".fill (L_pb + 28384 - .) / 4, 4, 0xBF800000\n" \
"L_pp7:\n" \
"s_cbranch_execz L_pret\n" \
"v_mfma_f32_16x16x32_bf16 v[64:67], a[252:255], a[192:195], v[64:67]\n" \
"ds_read_b128 a[128:131], v28 offset:32768\n" \
"s_mulk_i32 s18, 0x480\n" \
"v_mfma_f32_16x16x32_bf16 v[80:83], a[188:191], a[132:135], v[80:83]\n" \
"ds_read_b128 a[192:195], v29 offset:32768\n" \
"s_mulk_i32 s9, 0x480\n" \
"v_mfma_f32_16x16x32_bf16 v[80:83], a[252:255], a[196:199], v[80:83]\n" \
"s_add_u32 m0, s40, 0x3000\n" \
"s_movk_i32 s84, 0x300\n" \
"v_mfma_f32_16x16x32_bf16 v[76:79], a[184:187], a[132:135], v[76:79]\n" \
"buffer_load_dwordx4 v21, s[76:79], s84 offen offset:0 lds\n" \
"v_mfma_f32_16x16x32_bf16 v[76:79], a[248:251], a[196:199], v[76:79]\n" \
"ds_read_b64_tr_b16 a[226:227], v32 offset:37376\n" \
"s_or_b32 s14, s18, s12\n" \
"v_mfma_f32_16x16x32_bf16 v[72:75], a[180:183], a[132:135], v[72:75]\n" \
"s_add_u32 m0, s30, 0x0\n" \
"s_movk_i32 s84, 0x300\n" \
"v_mfma_f32_16x16x32_bf16 v[72:75], a[244:247], a[196:199], v[72:75]\n" \
"buffer_load_dwordx4 v24, s[76:79], s84 offen offset:0 lds\n" \
"v_mfma_f32_16x16x32_bf16 v[68:71], a[176:179], a[132:135], v[68:71]\n" \
"ds_read_b64_tr_b16 a[164:165], v33 offset:32768\n" \
"s_or_b32 s9, s9, s12\n" \
"v_mfma_f32_16x16x32_bf16 v[68:71], a[240:243], a[196:199], v[68:71]\n" \
"ds_read_b128 a[132:135], v28 offset:34816\n" \
"s_and_b32 s12, s2, 1\n" \
"v_mfma_f32_16x16x32_bf16 v[84:87], a[176:179], a[136:139], v[84:87]\n" \
"ds_read_b128 a[196:199], v29 offset:34816\n" \
"v_mfma_f32_16x16x32_bf16 v[84:87], a[240:243], a[200:203], v[84:87]\n" \
"s_movk_i32 s84, 0x2300\n" \
"v_mfma_f32_16x16x32_bf16 v[88:91], a[180:183], a[136:139], v[88:91]\n" \
"buffer_load_dwordx4 v25, s[76:79], s84 offen offset:1024 lds\n" \
"v_mfma_f32_16x16x32_bf16 v[88:91], a[244:247], a[200:203], v[88:91]\n" \
"ds_read_b64_tr_b16 a[166:167], v33 offset:33280\n" \
"v_mfma_f32_16x16x32_bf16 v[92:95], a[184:187], a[136:139], v[92:95]\n" \
"ds_read_b64_tr_b16 a[228:229], v33 offset:36864\n" \
"v_mfma_f32_16x16x32_bf16 v[92:95], a[248:251], a[200:203], v[92:95]\n" \
"s_movk_i32 s84, 0x4300\n" \
"v_mfma_f32_16x16x32_bf16 v[96:99], a[188:191], a[136:139], v[96:99]\n" \
"buffer_load_dwordx4 v24, s[76:79], s84 offen offset:2048 lds\n" \
"v_mfma_f32_16x16x32_bf16 v[96:99], a[252:255], a[200:203], v[96:99]\n" \
"ds_read_b128 a[136:139], v28 offset:36864\n" \
"v_mfma_f32_16x16x32_bf16 v[112:115], a[188:191], a[140:143], v[112:115]\n" \
"ds_read_b128 a[200:203], v29 offset:36864\n" \
"v_mfma_f32_16x16x32_bf16 v[112:115], a[252:255], a[204:207], v[112:115]\n" \
"s_movk_i32 s84, 0x6300\n" \
"v_mfma_f32_16x16x32_bf16 v[108:111], a[184:187], a[140:143], v[108:111]\n" \
"buffer_load_dwordx4 v25, s[76:79], s84 offen offset:3072 lds\n" \
"v_mfma_f32_16x16x32_bf16 v[108:111], a[248:251], a[204:207], v[108:111]\n" \
"ds_read_b64_tr_b16 a[230:231], v33 offset:37376\n" \
"v_mfma_f32_16x16x32_bf16 v[104:107], a[180:183], a[140:143], v[104:107]\n" \
"ds_read_b64_tr_b16 a[172:173], v35 offset:32768\n" \
"v_mfma_f32_16x16x32_bf16 v[104:107], a[244:247], a[204:207], v[104:107]\n" \
"ds_read_b64_tr_b16 a[174:175], v35 offset:33280\n" \
"v_mfma_f32_16x16x32_bf16 v[100:103], a[176:179], a[140:143], v[100:103]\n" \
"ds_read_b64_tr_b16 a[236:237], v35 offset:36864\n" \
"v_mfma_f32_16x16x32_bf16 v[100:103], a[240:243], a[204:207], v[100:103]\n" \
"ds_read_b128 a[140:143], v28 offset:38912\n" \
"v_mfma_f32_16x16x32_bf16 v[116:119], a[176:179], a[144:147], v[116:119]\n" \
"ds_read_b128 a[204:207], v29 offset:38912\n" \
"v_mfma_f32_16x16x32_bf16 v[116:119], a[240:243], a[208:211], v[116:119]\n" \
"s_add_u32 m0, s31, 0x0\n" \
"s_mov_b32 s84, 0x300000\n" \
"v_mfma_f32_16x16x32_bf16 v[120:123], a[180:183], a[144:147], v[120:123]\n" \
"buffer_load_dwordx4 v26, s[68:71], s84 offen offset:0 lds\n" \
"v_mfma_f32_16x16x32_bf16 v[120:123], a[244:247], a[208:211], v[120:123]\n" \
"s_addk_i32 s84, 0x7c00\n" \
"v_mfma_f32_16x16x32_bf16 v[124:127], a[184:187], a[144:147], v[124:127]\n" \
"buffer_load_dwordx4 v26, s[68:71], s84 offen offset:1024 lds\n" \
"v_mfma_f32_16x16x32_bf16 v[124:127], a[248:251], a[208:211], v[124:127]\n" \
"ds_read_b64_tr_b16 a[238:239], v35 offset:37376\n" \
"v_mfma_f32_16x16x32_bf16 v[128:131], a[188:191], a[144:147], v[128:131]\n" \
"ds_read_b64_tr_b16 a[168:169], v34 offset:32768\n" \
"v_mfma_f32_16x16x32_bf16 v[128:131], a[252:255], a[208:211], v[128:131]\n" \
"ds_read_b128 a[144:147], v28 offset:40960\n" \
"v_mfma_f32_16x16x32_bf16 v[144:147], a[188:191], a[148:151], v[144:147]\n" \
"ds_read_b128 a[208:211], v29 offset:40960\n" \
"v_mfma_f32_16x16x32_bf16 v[144:147], a[252:255], a[212:215], v[144:147]\n" \
"s_addk_i32 s84, 0x7c00\n" \
"v_mfma_f32_16x16x32_bf16 v[140:143], a[184:187], a[148:151], v[140:143]\n" \
"buffer_load_dwordx4 v27, s[68:71], s84 offen offset:2048 lds\n" \
"v_mfma_f32_16x16x32_bf16 v[140:143], a[248:251], a[212:215], v[140:143]\n" \
"s_addk_i32 s84, 0x7c00\n" \
"v_mfma_f32_16x16x32_bf16 v[136:139], a[180:183], a[148:151], v[136:139]\n" \
"buffer_load_dwordx4 v27, s[68:71], s84 offen offset:3072 lds\n" \
"v_mfma_f32_16x16x32_bf16 v[136:139], a[244:247], a[212:215], v[136:139]\n" \
"ds_read_b64_tr_b16 a[170:171], v34 offset:33280\n" \
"v_mfma_f32_16x16x32_bf16 v[132:135], a[176:179], a[148:151], v[132:135]\n" \
"ds_read_b64_tr_b16 a[232:233], v34 offset:36864\n" \
"v_mfma_f32_16x16x32_bf16 v[132:135], a[240:243], a[212:215], v[132:135]\n" \
"ds_read_b128 a[148:151], v28 offset:43008\n" \
"v_mfma_f32_16x16x32_bf16 v[148:151], a[176:179], a[152:155], v[148:151]\n" \
"ds_read_b128 a[212:215], v29 offset:43008\n" \
"v_mfma_f32_16x16x32_bf16 v[148:151], a[240:243], a[216:219], v[148:151]\n" \
"s_add_u32 m0, s30, 0x1000\n" \
"s_mov_b32 s84, 0x9300\n" \
"v_mfma_f32_16x16x32_bf16 v[152:155], a[180:183], a[152:155], v[152:155]\n" \
"buffer_load_dwordx4 v24, s[76:79], s84 offen offset:0 lds\n" \
"v_mfma_f32_16x16x32_bf16 v[152:155], a[244:247], a[216:219], v[152:155]\n" \
"s_addk_i32 s84, 0x2000\n" \
"s_nop 0\n" \
"v_mfma_f32_16x16x32_bf16 v[156:159], a[184:187], a[152:155], v[156:159]\n" \
"buffer_load_dwordx4 v25, s[76:79], s84 offen offset:1024 lds\n" \
"v_mfma_f32_16x16x32_bf16 v[156:159], a[248:251], a[216:219], v[156:159]\n" \
"ds_read_b64_tr_b16 a[234:235], v34 offset:37376\n" \
"v_mfma_f32_16x16x32_bf16 v[160:163], a[188:191], a[152:155], v[160:163]\n" \
"ds_read_b128 v[248:251], v22 offset:32768\n" \
"v_mfma_f32_16x16x32_bf16 v[160:163], a[252:255], a[216:219], v[160:163]\n" \
"ds_read_b128 a[152:155], v28 offset:45056\n" \
"v_mfma_f32_16x16x32_bf16 v[176:179], a[188:191], a[156:159], v[176:179]\n" \
"ds_read_b128 a[216:219], v29 offset:45056\n" \
"v_mfma_f32_16x16x32_bf16 v[176:179], a[252:255], a[220:223], v[176:179]\n" \
"ds_read_b128 v[48:51], v23 offset:32768\n" \
"v_mfma_f32_16x16x32_bf16 v[172:175], a[184:187], a[156:159], v[172:175]\n" \
"s_cmp_lg_u32 s12, 0\n" \
"s_cselect_b32 s13, s16, s13\n" \
"v_mfma_f32_16x16x32_bf16 v[172:175], a[248:251], a[220:223], v[172:175]\n" \
"s_bfe_u32 s16, s2, 0x20001\n" \
"s_addk_i32 s13, 0x4000\n" \
"v_mfma_f32_16x16x32_bf16 v[168:171], a[180:183], a[156:159], v[168:171]\n" \
"s_xor_b32 s16, s16, s3\n" \
"v_mfma_f32_16x16x32_bf16 v[168:171], a[244:247], a[220:223], v[168:171]\n" \
"s_cmp_lg_u32 s16, 0\n" \
"s_cselect_b32 s13, s7, s13\n" \
"v_mfma_f32_16x16x32_bf16 v[164:167], a[176:179], a[156:159], v[164:167]\n" \
"s_lshl_b32 s6, s6, 16\n" \
"v_writelane_b32 v11, s13, 39\n" \
"v_mfma_f32_16x16x32_bf16 v[164:167], a[240:243], a[220:223], v[164:167]\n" \
"ds_read_b128 a[156:159], v28 offset:47104\n" \
"ds_read_b128 a[220:223], v29 offset:47104\n" \
"s_waitcnt lgkmcnt(15)\n" \
"v_mfma_f32_16x16x32_bf16 a[0:3], a[160:163], a[128:131], a[0:3]\n" \
"s_addk_i32 s84, 0x2000\n" \
"v_mfma_f32_16x16x32_bf16 a[0:3], a[224:227], a[192:195], a[0:3]\n" \
"buffer_load_dwordx4 v24, s[76:79], s84 offen offset:2048 lds\n" \
"v_mfma_f32_16x16x32_bf16 a[4:7], a[164:167], a[128:131], a[4:7]\n" \
"ds_read_b64_tr_b16 a[176:177], v36 offset:32768\n" \
"s_add_i32 s13, s8, 0x4240000\n" \
"v_mfma_f32_16x16x32_bf16 a[4:7], a[228:231], a[192:195], a[4:7]\n" \
"s_addk_i32 s84, 0x2000\n" \
"v_mfma_f32_16x16x32_bf16 a[12:15], a[172:175], a[128:131], a[12:15]\n" \
"buffer_load_dwordx4 v25, s[76:79], s84 offen offset:3072 lds\n" \
"s_waitcnt lgkmcnt(15)\n" \
"v_mfma_f32_16x16x32_bf16 a[12:15], a[236:239], a[192:195], a[12:15]\n" \
"ds_read_b64_tr_b16 a[178:179], v36 offset:33792\n" \
"s_add_i32 s6, s13, s6\n" \
"s_waitcnt lgkmcnt(12)\n" \
"v_mfma_f32_16x16x32_bf16 a[8:11], a[128:131], a[168:171], a[8:11]\n" \
"ds_read_b64_tr_b16 a[240:241], v36 offset:40960\n" \
"v_writelane_b32 v11, s6, 40\n" \
"s_waitcnt lgkmcnt(9)\n" \
"v_mfma_f32_16x16x32_bf16 a[8:11], a[192:195], a[232:235], a[8:11]\n" \
"ds_read_b64_tr_b16 a[242:243], v36 offset:41984\n" \
"s_add_i32 s8, s8, 0x42c0000\n" \
"v_mfma_f32_16x16x32_bf16 a[24:27], a[132:135], a[168:171], a[24:27]\n" \
"ds_read_b64_tr_b16 a[180:181], v37 offset:32768\n" \
"s_lshl_b32 s6, s12, 10\n" \
"v_mfma_f32_16x16x32_bf16 a[24:27], a[196:199], a[232:235], a[24:27]\n" \
"ds_read_b64_tr_b16 a[182:183], v37 offset:33792\n" \
"s_or_b32 s6, s8, s6\n" \
"v_mfma_f32_16x16x32_bf16 a[28:31], a[172:175], a[132:135], a[28:31]\n" \
"ds_read_b64_tr_b16 a[244:245], v37 offset:40960\n" \
"v_mfma_f32_16x16x32_bf16 a[28:31], a[236:239], a[196:199], a[28:31]\n" \
"ds_read_b64_tr_b16 a[246:247], v37 offset:41984\n" \
"v_mfma_f32_16x16x32_bf16 a[20:23], a[164:167], a[132:135], a[20:23]\n" \
"s_cmp_lg_u32 s16, 0\n" \
"s_cselect_b32 s6, s7, s6\n" \
"v_mfma_f32_16x16x32_bf16 a[20:23], a[228:231], a[196:199], a[20:23]\n" \
"s_add_u32 s88, s42, 0x2000\n" \
"buffer_store_dwordx4 v[204:207], v20, s[80:83], s88 offen offset:3072 sc1\n" \
"v_mfma_f32_16x16x32_bf16 a[16:19], a[160:163], a[132:135], a[16:19]\n" \
"ds_read_b64_tr_b16 a[184:185], v38 offset:32768\n" \
"s_mulk_i32 s4, 0x4800\n" \
"v_mfma_f32_16x16x32_bf16 a[16:19], a[224:227], a[196:199], a[16:19]\n" \
"ds_read_b64_tr_b16 a[186:187], v38 offset:33792\n" \
"v_writelane_b32 v11, s6, 41\n" \
"v_mfma_f32_16x16x32_bf16 a[32:35], a[160:163], a[136:139], a[32:35]\n" \
"ds_read_b64_tr_b16 a[248:249], v38 offset:40960\n" \
"s_add_i32 s6, s14, s10\n" \
"v_mfma_f32_16x16x32_bf16 a[32:35], a[224:227], a[200:203], a[32:35]\n" \
"ds_read_b64_tr_b16 a[250:251], v38 offset:41984\n" \
"v_writelane_b32 v11, s6, 42\n" \
"v_mfma_f32_16x16x32_bf16 a[36:39], a[164:167], a[136:139], a[36:39]\n" \
"s_add_i32 s6, s9, 0x21c000\n" \
"s_add_i32 s9, s6, s10\n" \
"v_mfma_f32_16x16x32_bf16 a[36:39], a[228:231], a[200:203], a[36:39]\n" \
"s_add_u32 s89, s42, 0x3000\n" \
"buffer_store_dwordx4 v[208:211], v20, s[80:83], s89 offen offset:3072 sc1\n" \
"v_mfma_f32_16x16x32_bf16 a[44:47], a[172:175], a[136:139], a[44:47]\n" \
"ds_read_b64_tr_b16 a[188:189], v39 offset:32768\n" \
"v_mfma_f32_16x16x32_bf16 a[44:47], a[236:239], a[200:203], a[44:47]\n" \
"ds_read_b64_tr_b16 a[190:191], v39 offset:33792\n" \
"v_mfma_f32_16x16x32_bf16 a[40:43], a[136:139], a[168:171], a[40:43]\n" \
"ds_read_b64_tr_b16 a[252:253], v39 offset:40960\n" \
"v_mfma_f32_16x16x32_bf16 a[40:43], a[200:203], a[232:235], a[40:43]\n" \
"s_cmp_lg_u32 s12, 0\n" \
"s_cselect_b32 s6, s6, s14\n" \
"v_mfma_f32_16x16x32_bf16 a[56:59], a[140:143], a[168:171], a[56:59]\n" \
"ds_read_b64_tr_b16 a[254:255], v39 offset:41984\n" \
"s_addk_i32 s6, 0x100\n" \
"v_mfma_f32_16x16x32_bf16 a[56:59], a[204:207], a[232:235], a[56:59]\n" \
"v_writelane_b32 v11, s9, 43\n" \
"v_mfma_f32_16x16x32_bf16 a[60:63], a[172:175], a[140:143], a[60:63]\n" \
"s_add_u32 s88, s42, 0x3000\n" \
"buffer_store_dwordx4 v[212:215], v20, s[80:83], s88 offen offset:2048 sc1\n" \
"v_mfma_f32_16x16x32_bf16 a[60:63], a[236:239], a[204:207], a[60:63]\n" \
"s_cmp_lg_u32 s16, 0\n" \
"s_cselect_b32 s6, s7, s6\n" \
"v_mfma_f32_16x16x32_bf16 a[52:55], a[164:167], a[140:143], a[52:55]\n" \
"v_writelane_b32 v11, s6, 44\n" \
"s_or_b32 s6, s8, s15\n" \
"v_mfma_f32_16x16x32_bf16 a[52:55], a[228:231], a[204:207], a[52:55]\n" \
"v_writelane_b32 v11, s6, 52\n" \
"s_lshl_b32 s6, s3, 17\n" \
"v_mfma_f32_16x16x32_bf16 a[48:51], a[160:163], a[140:143], a[48:51]\n" \
"s_add_i32 s6, s13, s6\n" \
"s_or_b32 s5, s6, s5\n" \
"v_mfma_f32_16x16x32_bf16 a[48:51], a[224:227], a[204:207], a[48:51]\n" \
"s_or_b32 s5, s5, 0x3840\n" \
"v_writelane_b32 v11, s5, 6\n" \
"s_waitcnt lgkmcnt(15)\n" \
"v_mfma_f32_16x16x32_bf16 a[64:67], a[164:167], v[248:251], a[64:67]\n" \
"s_mov_b32 s5, 0xff0000\n" \
"v_mfma_f32_16x16x32_bf16 a[64:67], a[228:231], v[48:51], a[64:67]\n" \
"s_cmp_lg_u32 s16, 0\n" \
"s_cselect_b32 s5, s17, s5\n" \
"v_mfma_f32_16x16x32_bf16 a[80:83], a[172:175], v[248:251], a[80:83]\n" \
"v_writelane_b32 v11, s5, 7\n" \
"s_mul_i32 s5, s3, 0x1200\n" \
"v_mfma_f32_16x16x32_bf16 a[80:83], a[236:239], v[48:51], a[80:83]\n" \
"s_add_i32 s4, s5, s4\n" \
"s_add_i32 s4, s4, s11\n" \
"v_mfma_f32_16x16x32_bf16 a[68:71], v[248:251], a[168:171], a[68:71]\n" \
"v_writelane_b32 v11, s4, 8\n" \
"s_lshl_b32 s4, s2, 16\n" \
"v_mfma_f32_16x16x32_bf16 a[68:71], v[48:51], a[232:235], a[68:71]\n" \
"s_waitcnt vmcnt(3) lgkmcnt(0)\n" \
"s_barrier\n" \
"v_mfma_f32_16x16x32_bf16 v[52:55], a[176:179], a[128:131], v[52:55]\n" \
"s_add_u32 m0, s40, 0x8000\n" \
"s_add_u32 s85, s39, 0xe000\n" \
"v_mfma_f32_16x16x32_bf16 v[52:55], a[240:243], a[192:195], v[52:55]\n" \
"buffer_load_dwordx4 v20, s[80:83], s85 offen sc1 lds\n" \
"v_mfma_f32_16x16x32_bf16 v[56:59], a[180:183], a[128:131], v[56:59]\n" \
"ds_read_b64_tr_b16 a[160:161], v32 offset:0\n" \
"s_add_i32 s5, s4, 0x10000\n" \
"v_mfma_f32_16x16x32_bf16 v[56:59], a[244:247], a[192:195], v[56:59]\n" \
"ds_read_b64_tr_b16 a[162:163], v32 offset:512\n" \
"s_and_b32 s5, s5, 0x70000\n" \
"v_mfma_f32_16x16x32_bf16 v[60:63], a[184:187], a[128:131], v[60:63]\n" \
"s_add_u32 m0, s40, 0x9000\n" \
"s_add_u32 s85, s39, 0xf000\n" \
"v_mfma_f32_16x16x32_bf16 v[60:63], a[248:251], a[192:195], v[60:63]\n" \
"buffer_load_dwordx4 v20, s[80:83], s85 offen sc1 lds\n" \
"v_mfma_f32_16x16x32_bf16 v[64:67], a[188:191], a[128:131], v[64:67]\n" \
"ds_read_b64_tr_b16 a[224:225], v32 offset:4096\n" \
"s_add_i32 s5, s13, s5\n" \
"v_mfma_f32_16x16x32_bf16 v[64:67], a[252:255], a[192:195], v[64:67]\n" \
"ds_read_b128 a[128:131], v28 offset:0\n" \
"v_writelane_b32 v11, s5, 45\n" \
"v_mfma_f32_16x16x32_bf16 v[80:83], a[188:191], a[132:135], v[80:83]\n" \
"ds_read_b128 a[192:195], v29 offset:0\n" \
"s_add_i32 s5, s4, 0x20000\n" \
"v_mfma_f32_16x16x32_bf16 v[80:83], a[252:255], a[196:199], v[80:83]\n" \
"s_add_u32 m0, s40, 0xb000\n" \
"s_movk_i32 s84, 0x380\n" \
"v_mfma_f32_16x16x32_bf16 v[76:79], a[184:187], a[132:135], v[76:79]\n" \
"buffer_load_dwordx4 v21, s[76:79], s84 offen offset:0 lds\n" \
"v_mfma_f32_16x16x32_bf16 v[76:79], a[248:251], a[196:199], v[76:79]\n" \
"ds_read_b64_tr_b16 a[226:227], v32 offset:4608\n" \
"s_and_b32 s5, s5, 0x70000\n" \
"v_mfma_f32_16x16x32_bf16 v[72:75], a[180:183], a[132:135], v[72:75]\n" \
"s_add_u32 m0, s30, 0x8000\n" \
"s_movk_i32 s84, 0x380\n" \
"v_mfma_f32_16x16x32_bf16 v[72:75], a[244:247], a[196:199], v[72:75]\n" \
"buffer_load_dwordx4 v24, s[76:79], s84 offen offset:0 lds\n" \
"v_mfma_f32_16x16x32_bf16 v[68:71], a[176:179], a[132:135], v[68:71]\n" \
"ds_read_b64_tr_b16 a[164:165], v33 offset:0\n" \
"s_add_i32 s5, s13, s5\n" \
"v_mfma_f32_16x16x32_bf16 v[68:71], a[240:243], a[196:199], v[68:71]\n" \
"ds_read_b128 a[132:135], v28 offset:2048\n" \
"s_bitset1_b32 s5, 11\n" \
"v_mfma_f32_16x16x32_bf16 v[84:87], a[176:179], a[136:139], v[84:87]\n" \
"ds_read_b128 a[196:199], v29 offset:2048\n" \
"v_writelane_b32 v11, s5, 46\n" \
"v_mfma_f32_16x16x32_bf16 v[84:87], a[240:243], a[200:203], v[84:87]\n" \
"s_movk_i32 s84, 0x2380\n" \
"v_mfma_f32_16x16x32_bf16 v[88:91], a[180:183], a[136:139], v[88:91]\n" \
"buffer_load_dwordx4 v25, s[76:79], s84 offen offset:1024 lds\n" \
"v_mfma_f32_16x16x32_bf16 v[88:91], a[244:247], a[200:203], v[88:91]\n" \
"ds_read_b64_tr_b16 a[166:167], v33 offset:512\n" \
"s_add_i32 s5, s4, 0x30000\n" \
"v_mfma_f32_16x16x32_bf16 v[92:95], a[184:187], a[136:139], v[92:95]\n" \
"ds_read_b64_tr_b16 a[228:229], v33 offset:4096\n" \
"s_and_b32 s5, s5, 0x70000\n" \
"v_mfma_f32_16x16x32_bf16 v[92:95], a[248:251], a[200:203], v[92:95]\n" \
"s_movk_i32 s84, 0x4380\n" \
"v_mfma_f32_16x16x32_bf16 v[96:99], a[188:191], a[136:139], v[96:99]\n" \
"buffer_load_dwordx4 v24, s[76:79], s84 offen offset:2048 lds\n" \
"v_mfma_f32_16x16x32_bf16 v[96:99], a[252:255], a[200:203], v[96:99]\n" \
"ds_read_b128 a[136:139], v28 offset:4096\n" \
"s_add_i32 s5, s13, s5\n" \
"v_mfma_f32_16x16x32_bf16 v[112:115], a[188:191], a[140:143], v[112:115]\n" \
"ds_read_b128 a[200:203], v29 offset:4096\n" \
"s_bitset1_b32 s5, 12\n" \
"v_mfma_f32_16x16x32_bf16 v[112:115], a[252:255], a[204:207], v[112:115]\n" \
"s_movk_i32 s84, 0x6380\n" \
"v_mfma_f32_16x16x32_bf16 v[108:111], a[184:187], a[140:143], v[108:111]\n" \
"buffer_load_dwordx4 v25, s[76:79], s84 offen offset:3072 lds\n" \
"v_mfma_f32_16x16x32_bf16 v[108:111], a[248:251], a[204:207], v[108:111]\n" \
"ds_read_b64_tr_b16 a[230:231], v33 offset:4608\n" \
"v_writelane_b32 v11, s5, 47\n" \
"v_mfma_f32_16x16x32_bf16 v[104:107], a[180:183], a[140:143], v[104:107]\n" \
"ds_read_b64_tr_b16 a[172:173], v35 offset:0\n" \
"s_and_b32 s5, s4, 0x70000\n" \
"v_mfma_f32_16x16x32_bf16 v[104:107], a[244:247], a[204:207], v[104:107]\n" \
"ds_read_b64_tr_b16 a[174:175], v35 offset:512\n" \
"s_xor_b32 s5, s5, 0x40000\n" \
"v_mfma_f32_16x16x32_bf16 v[100:103], a[176:179], a[140:143], v[100:103]\n" \
"ds_read_b64_tr_b16 a[236:237], v35 offset:4096\n" \
"s_add_i32 s5, s13, s5\n" \
"s_nop 0\n" \
"v_mfma_f32_16x16x32_bf16 v[100:103], a[240:243], a[204:207], v[100:103]\n" \
"ds_read_b128 a[140:143], v28 offset:6144\n" \
"s_or_b32 s5, s5, 0x1800\n" \
"v_mfma_f32_16x16x32_bf16 v[116:119], a[176:179], a[144:147], v[116:119]\n" \
"ds_read_b128 a[204:207], v29 offset:6144\n" \
"v_writelane_b32 v11, s5, 48\n" \
"v_mfma_f32_16x16x32_bf16 v[116:119], a[240:243], a[208:211], v[116:119]\n" \
"s_add_u32 m0, s31, 0x8000\n" \
"s_mov_b32 s84, 0x380000\n" \
"v_mfma_f32_16x16x32_bf16 v[120:123], a[180:183], a[144:147], v[120:123]\n" \
"buffer_load_dwordx4 v26, s[68:71], s84 offen offset:0 lds\n" \
"v_mfma_f32_16x16x32_bf16 v[120:123], a[244:247], a[208:211], v[120:123]\n" \
"s_addk_i32 s84, 0x7c00\n" \
"v_mfma_f32_16x16x32_bf16 v[124:127], a[184:187], a[144:147], v[124:127]\n" \
"buffer_load_dwordx4 v26, s[68:71], s84 offen offset:1024 lds\n" \
"v_mfma_f32_16x16x32_bf16 v[124:127], a[248:251], a[208:211], v[124:127]\n" \
"ds_read_b64_tr_b16 a[238:239], v35 offset:4608\n" \
"s_add_i32 s5, s4, 0x50000\n" \
"v_mfma_f32_16x16x32_bf16 v[128:131], a[188:191], a[144:147], v[128:131]\n" \
"ds_read_b64_tr_b16 a[168:169], v34 offset:0\n" \
"s_and_b32 s5, s5, 0x70000\n" \
"v_mfma_f32_16x16x32_bf16 v[128:131], a[252:255], a[208:211], v[128:131]\n" \
"ds_read_b128 a[144:147], v28 offset:8192\n" \
"s_add_i32 s5, s13, s5\n" \
"v_mfma_f32_16x16x32_bf16 v[144:147], a[188:191], a[148:151], v[144:147]\n" \
"ds_read_b128 a[208:211], v29 offset:8192\n" \
"s_bitset1_b32 s5, 13\n" \
"v_mfma_f32_16x16x32_bf16 v[144:147], a[252:255], a[212:215], v[144:147]\n" \
"s_addk_i32 s84, 0x7c00\n" \
"v_mfma_f32_16x16x32_bf16 v[140:143], a[184:187], a[148:151], v[140:143]\n" \
"buffer_load_dwordx4 v27, s[68:71], s84 offen offset:2048 lds\n" \
"v_mfma_f32_16x16x32_bf16 v[140:143], a[248:251], a[212:215], v[140:143]\n" \
"s_addk_i32 s84, 0x7c00\n" \
"s_nop 0\n" \
"v_mfma_f32_16x16x32_bf16 v[136:139], a[180:183], a[148:151], v[136:139]\n" \
"buffer_load_dwordx4 v27, s[68:71], s84 offen offset:3072 lds\n" \
"v_mfma_f32_16x16x32_bf16 v[136:139], a[244:247], a[212:215], v[136:139]\n" \
"ds_read_b64_tr_b16 a[170:171], v34 offset:512\n" \
"v_writelane_b32 v11, s5, 49\n" \
"v_mfma_f32_16x16x32_bf16 v[132:135], a[176:179], a[148:151], v[132:135]\n" \
"ds_read_b64_tr_b16 a[232:233], v34 offset:4096\n" \
"s_add_i32 s5, s4, 0x60000\n" \
"v_mfma_f32_16x16x32_bf16 v[132:135], a[240:243], a[212:215], v[132:135]\n" \
"ds_read_b128 a[148:151], v28 offset:10240\n" \
"s_and_b32 s5, s5, 0x70000\n" \
"v_mfma_f32_16x16x32_bf16 v[148:151], a[176:179], a[152:155], v[148:151]\n" \
"ds_read_b128 a[212:215], v29 offset:10240\n" \
"s_add_i32 s4, s4, 0x70000\n" \
"v_mfma_f32_16x16x32_bf16 v[148:151], a[240:243], a[216:219], v[148:151]\n" \
"s_add_u32 m0, s30, 0x9000\n" \
"s_mov_b32 s84, 0x9380\n" \
"v_mfma_f32_16x16x32_bf16 v[152:155], a[180:183], a[152:155], v[152:155]\n" \
"buffer_load_dwordx4 v24, s[76:79], s84 offen offset:0 lds\n" \
"v_mfma_f32_16x16x32_bf16 v[152:155], a[244:247], a[216:219], v[152:155]\n" \
"s_addk_i32 s84, 0x2000\n" \
"v_mfma_f32_16x16x32_bf16 v[156:159], a[184:187], a[152:155], v[156:159]\n" \
"buffer_load_dwordx4 v25, s[76:79], s84 offen offset:1024 lds\n" \
"v_mfma_f32_16x16x32_bf16 v[156:159], a[248:251], a[216:219], v[156:159]\n" \
"ds_read_b64_tr_b16 a[234:235], v34 offset:4608\n" \
"s_add_i32 s5, s13, s5\n" \
"v_mfma_f32_16x16x32_bf16 v[160:163], a[188:191], a[152:155], v[160:163]\n" \
"ds_read_b128 v[248:251], v22 offset:0\n" \
"s_and_b32 s4, s4, 0x70000\n" \
"v_mfma_f32_16x16x32_bf16 v[160:163], a[252:255], a[216:219], v[160:163]\n" \
"ds_read_b128 a[152:155], v28 offset:12288\n" \
"s_or_b32 s5, s5, 0x2800\n" \
"v_mfma_f32_16x16x32_bf16 v[176:179], a[188:191], a[156:159], v[176:179]\n" \
"ds_read_b128 a[216:219], v29 offset:12288\n" \
"v_writelane_b32 v11, s5, 50\n" \
"v_mfma_f32_16x16x32_bf16 v[176:179], a[252:255], a[220:223], v[176:179]\n" \
"ds_read_b128 v[48:51], v23 offset:0\n" \
"s_add_i32 s13, s13, s4\n" \
"v_mfma_f32_16x16x32_bf16 v[172:175], a[184:187], a[156:159], v[172:175]\n" \
"s_or_b32 s4, s13, 0x3000\n" \
"v_writelane_b32 v11, s4, 51\n" \
"v_mfma_f32_16x16x32_bf16 v[172:175], a[248:251], a[220:223], v[172:175]\n" \
"s_movk_i32 s11, 0x4000\n" \
"s_lshl_b32 s4, s2, 2\n" \
"v_mfma_f32_16x16x32_bf16 v[168:171], a[180:183], a[156:159], v[168:171]\n" \
"s_and_b32 s4, s4, 28\n" \
"s_bfe_u32 s5, s2, 0x20003\n" \
"v_mfma_f32_16x16x32_bf16 v[168:171], a[244:247], a[220:223], v[168:171]\n" \
"s_or_b32 s4, s4, s5\n" \
"s_lshr_b32 s5, s2, 5\n" \
"v_mfma_f32_16x16x32_bf16 v[164:167], a[176:179], a[156:159], v[164:167]\n" \
"s_lshr_b32 s19, s3, 1\n" \
"s_lshl_b32 s18, s5, 1\n" \
"v_mfma_f32_16x16x32_bf16 v[164:167], a[240:243], a[220:223], v[164:167]\n" \
"ds_read_b128 a[156:159], v28 offset:14336\n" \
"ds_read_b128 a[220:223], v29 offset:14336\n" \
"s_waitcnt lgkmcnt(15)\n" \
"v_mfma_f32_16x16x32_bf16 a[0:3], a[160:163], a[128:131], a[0:3]\n" \
"s_addk_i32 s84, 0x2000\n" \
"v_mfma_f32_16x16x32_bf16 a[0:3], a[224:227], a[192:195], a[0:3]\n" \
"buffer_load_dwordx4 v24, s[76:79], s84 offen offset:2048 lds\n" \
"v_mfma_f32_16x16x32_bf16 a[4:7], a[164:167], a[128:131], a[4:7]\n" \
"ds_read_b64_tr_b16 a[176:177], v36 offset:0\n" \
"s_lshl_b32 s6, s4, 20\n" \
"v_mfma_f32_16x16x32_bf16 a[4:7], a[228:231], a[192:195], a[4:7]\n" \
"s_addk_i32 s84, 0x2000\n" \
"v_mfma_f32_16x16x32_bf16 a[12:15], a[172:175], a[128:131], a[12:15]\n" \
"buffer_load_dwordx4 v25, s[76:79], s84 offen offset:3072 lds\n" \
"s_waitcnt lgkmcnt(15)\n" \
"v_mfma_f32_16x16x32_bf16 a[12:15], a[236:239], a[192:195], a[12:15]\n" \
"ds_read_b64_tr_b16 a[178:179], v36 offset:1024\n" \
"s_lshl_b32 s7, s4, 8\n" \
"s_waitcnt lgkmcnt(12)\n" \
"v_mfma_f32_16x16x32_bf16 a[8:11], a[128:131], a[168:171], a[8:11]\n" \
"ds_read_b64_tr_b16 a[240:241], v36 offset:8192\n" \
"s_and_b32 s13, s3, 1\n" \
"s_waitcnt lgkmcnt(9)\n" \
"v_mfma_f32_16x16x32_bf16 a[8:11], a[192:195], a[232:235], a[8:11]\n" \
"ds_read_b64_tr_b16 a[242:243], v36 offset:9216\n" \
"s_add_i32 s14, s18, s19\n" \
"v_mfma_f32_16x16x32_bf16 a[24:27], a[132:135], a[168:171], a[24:27]\n" \
"ds_read_b64_tr_b16 a[180:181], v37 offset:0\n" \
"v_mfma_f32_16x16x32_bf16 a[24:27], a[196:199], a[232:235], a[24:27]\n" \
"ds_read_b64_tr_b16 a[182:183], v37 offset:1024\n" \
"v_mfma_f32_16x16x32_bf16 a[28:31], a[172:175], a[132:135], a[28:31]\n" \
"ds_read_b64_tr_b16 a[244:245], v37 offset:8192\n" \
"v_mfma_f32_16x16x32_bf16 a[28:31], a[236:239], a[196:199], a[28:31]\n" \
"ds_read_b64_tr_b16 a[246:247], v37 offset:9216\n" \
"v_mfma_f32_16x16x32_bf16 a[20:23], a[164:167], a[132:135], a[20:23]\n" \
"s_add_u32 s6, s26, s6\n" \
"s_addc_u32 s9, s27, 0\n" \
"v_mfma_f32_16x16x32_bf16 a[20:23], a[228:231], a[196:199], a[20:23]\n" \
"s_add_u32 s89, s42, 0x0\n" \
"s_nop 0\n" \
"buffer_store_dwordx4 v[184:187], v20, s[80:83], s89 offen offset:1024 sc1\n" \
"v_mfma_f32_16x16x32_bf16 a[16:19], a[160:163], a[132:135], a[16:19]\n" \
"ds_read_b64_tr_b16 a[184:185], v38 offset:0\n" \
"v_mfma_f32_16x16x32_bf16 a[16:19], a[224:227], a[196:199], a[16:19]\n" \
"ds_read_b64_tr_b16 a[186:187], v38 offset:1024\n" \
"v_mfma_f32_16x16x32_bf16 a[32:35], a[160:163], a[136:139], a[32:35]\n" \
"ds_read_b64_tr_b16 a[248:249], v38 offset:8192\n" \
"v_mfma_f32_16x16x32_bf16 a[32:35], a[224:227], a[200:203], a[32:35]\n" \
"ds_read_b64_tr_b16 a[250:251], v38 offset:9216\n" \
"v_mfma_f32_16x16x32_bf16 a[36:39], a[164:167], a[136:139], a[36:39]\n" \
"s_add_u32 s8, s6, 0x1000000\n" \
"s_addc_u32 s6, s9, 0\n" \
"v_mfma_f32_16x16x32_bf16 a[36:39], a[228:231], a[200:203], a[36:39]\n" \
"s_add_u32 s88, s42, 0x1000\n" \
"buffer_store_dwordx4 v[188:191], v20, s[80:83], s88 offen offset:1024 sc1\n" \
"v_mfma_f32_16x16x32_bf16 a[44:47], a[172:175], a[136:139], a[44:47]\n" \
"ds_read_b64_tr_b16 a[188:189], v39 offset:0\n" \
"s_and_b32 s9, s6, 0xffff\n" \
"v_mfma_f32_16x16x32_bf16 a[44:47], a[236:239], a[200:203], a[44:47]\n" \
"ds_read_b64_tr_b16 a[190:191], v39 offset:1024\n" \
"s_lshl_b32 s6, s5, 16\n" \
"v_mfma_f32_16x16x32_bf16 a[40:43], a[136:139], a[168:171], a[40:43]\n" \
"ds_read_b64_tr_b16 a[252:253], v39 offset:8192\n" \
"s_sub_i32 s10, 0x100000, s6\n" \
"v_mfma_f32_16x16x32_bf16 a[40:43], a[200:203], a[232:235], a[40:43]\n" \
"s_mov_b32 s6, 0x8000\n" \
"v_mfma_f32_16x16x32_bf16 a[56:59], a[140:143], a[168:171], a[56:59]\n" \
"ds_read_b64_tr_b16 a[254:255], v39 offset:9216\n" \
"v_mfma_f32_16x16x32_bf16 a[56:59], a[204:207], a[232:235], a[56:59]\n" \
"s_cmp_lg_u32 s5, 0\n" \
"s_cselect_b32 s17, s11, s6\n" \
"v_mfma_f32_16x16x32_bf16 a[60:63], a[172:175], a[140:143], a[60:63]\n" \
"s_add_u32 s89, s42, 0x2000\n" \
"buffer_store_dwordx4 v[192:195], v20, s[80:83], s89 offen offset:0 sc1\n" \
"v_mfma_f32_16x16x32_bf16 a[60:63], a[236:239], a[204:207], a[60:63]\n" \
"s_lshl_b32 s6, s3, 1\n" \
"s_lshl_b32 s5, s5, 3\n" \
"v_mfma_f32_16x16x32_bf16 a[52:55], a[164:167], a[140:143], a[52:55]\n" \
"s_and_b32 s6, s6, 4\n" \
"s_or_b32 s12, s5, s6\n" \
"v_mfma_f32_16x16x32_bf16 a[52:55], a[228:231], a[204:207], a[52:55]\n" \
"s_sub_i32 s5, s13, s5\n" \
"s_or_b32 s5, s5, s6\n" \
"v_mfma_f32_16x16x32_bf16 a[48:51], a[160:163], a[140:143], a[48:51]\n" \
"s_or_b32 s6, s12, s13\n" \
"s_lshl_b32 s4, s4, 2\n" \
"v_mfma_f32_16x16x32_bf16 a[48:51], a[224:227], a[204:207], a[48:51]\n" \
"s_mulk_i32 s6, 0x92\n" \
"s_add_i32 s4, s4, 18\n" \
"s_waitcnt lgkmcnt(15)\n" \
"v_mfma_f32_16x16x32_bf16 a[64:67], a[164:167], v[248:251], a[64:67]\n" \
"s_mulk_i32 s5, 0x92\n" \
"s_add_i32 s6, s4, s6\n" \
"v_mfma_f32_16x16x32_bf16 a[64:67], a[228:231], v[48:51], a[64:67]\n" \
"s_add_i32 s5, s5, s4\n" \
"s_lshl_b32 s4, s3, 11\n" \
"v_mfma_f32_16x16x32_bf16 a[80:83], a[172:175], v[248:251], a[80:83]\n" \
"s_lshl_b32 s15, s13, 12\n" \
"s_lshl_b32 s16, s19, 14\n" \
"v_mfma_f32_16x16x32_bf16 a[80:83], a[236:239], v[48:51], a[80:83]\n" \
"s_mul_i32 s19, s19, 0xe000\n" \
"s_mov_b32 s11, 0x20000\n" \
"v_mfma_f32_16x16x32_bf16 a[68:71], v[248:251], a[168:171], a[68:71]\n" \
"s_or_b32 s12, s4, 0xc000\n" \
"s_lshl_b32 s4, s3, 8\n" \
"v_mfma_f32_16x16x32_bf16 a[68:71], v[48:51], a[232:235], a[68:71]\n" \
"s_waitcnt vmcnt(3) lgkmcnt(0)\n" \
"s_barrier\n" \
"v_mfma_f32_16x16x32_bf16 v[52:55], a[176:179], a[128:131], v[52:55]\n" \
"s_add_u32 m0, s40, 0x0\n" \
"s_add_u32 s85, s39, 0x10000\n" \
"v_mfma_f32_16x16x32_bf16 v[52:55], a[240:243], a[192:195], v[52:55]\n" \
"buffer_load_dwordx4 v20, s[80:83], s85 offen sc1 lds\n" \
"v_mfma_f32_16x16x32_bf16 v[56:59], a[180:183], a[128:131], v[56:59]\n" \
"ds_read_b64_tr_b16 a[160:161], v32 offset:32768\n" \
"s_or_b32 s22, s15, s16\n" \
"v_mfma_f32_16x16x32_bf16 v[56:59], a[244:247], a[192:195], v[56:59]\n" \
"ds_read_b64_tr_b16 a[162:163], v32 offset:33280\n" \
"s_or_b32 s23, s19, s15\n" \
"v_mfma_f32_16x16x32_bf16 v[60:63], a[184:187], a[128:131], v[60:63]\n" \
"s_add_u32 m0, s40, 0x1000\n" \
"s_add_u32 s85, s39, 0x11000\n" \
"v_mfma_f32_16x16x32_bf16 v[60:63], a[248:251], a[192:195], v[60:63]\n" \
"buffer_load_dwordx4 v20, s[80:83], s85 offen sc1 lds\n" \
"v_mfma_f32_16x16x32_bf16 v[64:67], a[188:191], a[128:131], v[64:67]\n" \
"ds_read_b64_tr_b16 a[224:225], v32 offset:36864\n" \
"s_lshl_b32 s60, s6, 10\n" \
"v_mfma_f32_16x16x32_bf16 v[64:67], a[252:255], a[192:195], v[64:67]\n" \
"ds_read_b128 a[128:131], v28 offset:32768\n" \
"s_lshl_b32 s61, s5, 10\n" \
"v_mfma_f32_16x16x32_bf16 v[80:83], a[188:191], a[132:135], v[80:83]\n" \
"ds_read_b128 a[192:195], v29 offset:32768\n" \
"s_lshl_b32 s14, s14, 15\n" \
"v_mfma_f32_16x16x32_bf16 v[80:83], a[252:255], a[196:199], v[80:83]\n" \
"s_add_u32 m0, s40, 0x3000\n" \
"s_movk_i32 s84, 0x400\n" \
"v_mfma_f32_16x16x32_bf16 v[76:79], a[184:187], a[132:135], v[76:79]\n" \
"buffer_load_dwordx4 v21, s[76:79], s84 offen offset:0 lds\n" \
"v_mfma_f32_16x16x32_bf16 v[76:79], a[248:251], a[196:199], v[76:79]\n" \
"ds_read_b64_tr_b16 a[226:227], v32 offset:37376\n" \
"s_or_b32 s20, s4, 0x26c00\n" \
"v_mfma_f32_16x16x32_bf16 v[72:75], a[180:183], a[132:135], v[72:75]\n" \
"s_add_u32 m0, s30, 0x0\n" \
"s_movk_i32 s84, 0x400\n" \
"v_mfma_f32_16x16x32_bf16 v[72:75], a[244:247], a[196:199], v[72:75]\n" \
"buffer_load_dwordx4 v24, s[76:79], s84 offen offset:0 lds\n" \
"v_mfma_f32_16x16x32_bf16 v[68:71], a[176:179], a[132:135], v[68:71]\n" \
"ds_read_b64_tr_b16 a[164:165], v33 offset:32768\n" \
"s_or_b32 s13, s22, 0x8000\n" \
"v_mfma_f32_16x16x32_bf16 v[68:71], a[240:243], a[196:199], v[68:71]\n" \
"ds_read_b128 a[132:135], v28 offset:34816\n" \
"s_add_i32 s22, s22, 0x14000\n" \
"v_mfma_f32_16x16x32_bf16 v[84:87], a[176:179], a[136:139], v[84:87]\n" \
"ds_read_b128 a[196:199], v29 offset:34816\n" \
"s_bitset1_b32 s23, 16\n" \
"v_mfma_f32_16x16x32_bf16 v[84:87], a[240:243], a[200:203], v[84:87]\n" \
"s_movk_i32 s84, 0x2400\n" \
"v_mfma_f32_16x16x32_bf16 v[88:91], a[180:183], a[136:139], v[88:91]\n" \
"buffer_load_dwordx4 v25, s[76:79], s84 offen offset:1024 lds\n" \
"v_mfma_f32_16x16x32_bf16 v[88:91], a[244:247], a[200:203], v[88:91]\n" \
"ds_read_b64_tr_b16 a[166:167], v33 offset:33280\n" \
"s_or_b32 s24, s7, 0x5be8020\n" \
"v_mfma_f32_16x16x32_bf16 v[92:95], a[184:187], a[136:139], v[92:95]\n" \
"ds_read_b64_tr_b16 a[228:229], v33 offset:36864\n" \
"s_and_b32 s57, s27, 0xffff\n" \
"v_mfma_f32_16x16x32_bf16 v[92:95], a[248:251], a[200:203], v[92:95]\n" \
"s_movk_i32 s84, 0x4400\n" \
"v_mfma_f32_16x16x32_bf16 v[96:99], a[188:191], a[136:139], v[96:99]\n" \
"buffer_load_dwordx4 v24, s[76:79], s84 offen offset:2048 lds\n" \
".fill (L_pb + 32480 - .) / 4, 4, 0xBF800000\n" \
"L_pp8:\n" \
"s_cbranch_execz L_pret\n" \
"v_mfma_f32_16x16x32_bf16 v[96:99], a[252:255], a[200:203], v[96:99]\n" \
"ds_read_b128 a[136:139], v28 offset:36864\n" \
"s_mov_b32 s58, 0x6000000\n" \
"v_mfma_f32_16x16x32_bf16 v[112:115], a[188:191], a[140:143], v[112:115]\n" \
"ds_read_b128 a[200:203], v29 offset:36864\n" \
"s_mov_b32 s56, s26\n" \
"v_mfma_f32_16x16x32_bf16 v[112:115], a[252:255], a[204:207], v[112:115]\n" \
"s_movk_i32 s84, 0x6400\n" \
"v_mfma_f32_16x16x32_bf16 v[108:111], a[184:187], a[140:143], v[108:111]\n" \
"buffer_load_dwordx4 v25, s[76:79], s84 offen offset:3072 lds\n" \
"v_mfma_f32_16x16x32_bf16 v[108:111], a[248:251], a[204:207], v[108:111]\n" \
"ds_read_b64_tr_b16 a[230:231], v33 offset:37376\n" \
"s_mov_b32 s59, s11\n" \
"v_mfma_f32_16x16x32_bf16 v[104:107], a[180:183], a[140:143], v[104:107]\n" \
"ds_read_b64_tr_b16 a[172:173], v35 offset:32768\n" \
"s_add_i32 s60, s60, 0x3000000\n" \
"v_mfma_f32_16x16x32_bf16 v[104:107], a[244:247], a[204:207], v[104:107]\n" \
"ds_read_b64_tr_b16 a[174:175], v35 offset:33280\n" \
"s_add_i32 s61, s61, 0x411c000\n" \
"v_mfma_f32_16x16x32_bf16 v[100:103], a[176:179], a[140:143], v[100:103]\n" \
"ds_read_b64_tr_b16 a[236:237], v35 offset:36864\n" \
"s_sub_i32 s19, 14, s18\n" \
"v_mfma_f32_16x16x32_bf16 v[100:103], a[240:243], a[204:207], v[100:103]\n" \
"ds_read_b128 a[140:143], v28 offset:38912\n" \
"s_mov_b32 s21, 0x71800000\n" \
"v_mfma_f32_16x16x32_bf16 v[116:119], a[176:179], a[144:147], v[116:119]\n" \
"ds_read_b128 a[204:207], v29 offset:38912\n" \
"v_mfma_f32_16x16x32_bf16 v[116:119], a[240:243], a[208:211], v[116:119]\n" \
"s_add_u32 m0, s31, 0x0\n" \
"s_mov_b32 s84, 0x400000\n" \
"v_mfma_f32_16x16x32_bf16 v[120:123], a[180:183], a[144:147], v[120:123]\n" \
"buffer_load_dwordx4 v26, s[68:71], s84 offen offset:0 lds\n" \
"v_mfma_f32_16x16x32_bf16 v[120:123], a[244:247], a[208:211], v[120:123]\n" \
"s_addk_i32 s84, 0x7c00\n" \
"s_nop 0\n" \
"v_mfma_f32_16x16x32_bf16 v[124:127], a[184:187], a[144:147], v[124:127]\n" \
"buffer_load_dwordx4 v26, s[68:71], s84 offen offset:1024 lds\n" \
"v_mfma_f32_16x16x32_bf16 v[124:127], a[248:251], a[208:211], v[124:127]\n" \
"ds_read_b64_tr_b16 a[238:239], v35 offset:37376\n" \
"v_mfma_f32_16x16x32_bf16 v[128:131], a[188:191], a[144:147], v[128:131]\n" \
"ds_read_b64_tr_b16 a[168:169], v34 offset:32768\n" \
"v_mfma_f32_16x16x32_bf16 v[128:131], a[252:255], a[208:211], v[128:131]\n" \
"ds_read_b128 a[144:147], v28 offset:40960\n" \
"v_mfma_f32_16x16x32_bf16 v[144:147], a[188:191], a[148:151], v[144:147]\n" \
"ds_read_b128 a[208:211], v29 offset:40960\n" \
"v_mfma_f32_16x16x32_bf16 v[144:147], a[252:255], a[212:215], v[144:147]\n" \
"s_addk_i32 s84, 0x7c00\n" \
"v_mfma_f32_16x16x32_bf16 v[140:143], a[184:187], a[148:151], v[140:143]\n" \
"buffer_load_dwordx4 v27, s[68:71], s84 offen offset:2048 lds\n" \
"v_mfma_f32_16x16x32_bf16 v[140:143], a[248:251], a[212:215], v[140:143]\n" \
"s_addk_i32 s84, 0x7c00\n" \
"v_mfma_f32_16x16x32_bf16 v[136:139], a[180:183], a[148:151], v[136:139]\n" \
"buffer_load_dwordx4 v27, s[68:71], s84 offen offset:3072 lds\n" \
"v_mfma_f32_16x16x32_bf16 v[136:139], a[244:247], a[212:215], v[136:139]\n" \
"ds_read_b64_tr_b16 a[170:171], v34 offset:33280\n" \
"v_mfma_f32_16x16x32_bf16 v[132:135], a[176:179], a[148:151], v[132:135]\n" \
"ds_read_b64_tr_b16 a[232:233], v34 offset:36864\n" \
"v_mfma_f32_16x16x32_bf16 v[132:135], a[240:243], a[212:215], v[132:135]\n" \
"ds_read_b128 a[148:151], v28 offset:43008\n" \
"v_mfma_f32_16x16x32_bf16 v[148:151], a[176:179], a[152:155], v[148:151]\n" \
"ds_read_b128 a[212:215], v29 offset:43008\n" \
"v_mfma_f32_16x16x32_bf16 v[148:151], a[240:243], a[216:219], v[148:151]\n" \
"s_add_u32 m0, s30, 0x1000\n" \
"s_mov_b32 s84, 0x9400\n" \
"v_mfma_f32_16x16x32_bf16 v[152:155], a[180:183], a[152:155], v[152:155]\n" \
"buffer_load_dwordx4 v24, s[76:79], s84 offen offset:0 lds\n" \
"v_mfma_f32_16x16x32_bf16 v[152:155], a[244:247], a[216:219], v[152:155]\n" \
"s_addk_i32 s84, 0x2000\n" \
"s_nop 0\n" \
"v_mfma_f32_16x16x32_bf16 v[156:159], a[184:187], a[152:155], v[156:159]\n" \
"buffer_load_dwordx4 v25, s[76:79], s84 offen offset:1024 lds\n" \
"v_mfma_f32_16x16x32_bf16 v[156:159], a[248:251], a[216:219], v[156:159]\n" \
"ds_read_b64_tr_b16 a[234:235], v34 offset:37376\n" \
"v_mfma_f32_16x16x32_bf16 v[160:163], a[188:191], a[152:155], v[160:163]\n" \
"ds_read_b128 v[248:251], v22 offset:32768\n" \
"v_mfma_f32_16x16x32_bf16 v[160:163], a[252:255], a[216:219], v[160:163]\n" \
"ds_read_b128 a[152:155], v28 offset:45056\n" \
"v_mfma_f32_16x16x32_bf16 v[176:179], a[188:191], a[156:159], v[176:179]\n" \
"ds_read_b128 a[216:219], v29 offset:45056\n" \
"v_mfma_f32_16x16x32_bf16 v[176:179], a[252:255], a[220:223], v[176:179]\n" \
"ds_read_b128 v[48:51], v23 offset:32768\n" \
"v_mfma_f32_16x16x32_bf16 v[172:175], a[184:187], a[156:159], v[172:175]\n" \
"v_mfma_f32_16x16x32_bf16 v[172:175], a[248:251], a[220:223], v[172:175]\n" \
"v_mfma_f32_16x16x32_bf16 v[168:171], a[180:183], a[156:159], v[168:171]\n" \
"v_mfma_f32_16x16x32_bf16 v[168:171], a[244:247], a[220:223], v[168:171]\n" \
"v_mfma_f32_16x16x32_bf16 v[164:167], a[176:179], a[156:159], v[164:167]\n" \
"v_mfma_f32_16x16x32_bf16 v[164:167], a[240:243], a[220:223], v[164:167]\n" \
"ds_read_b128 a[156:159], v28 offset:47104\n" \
"ds_read_b128 a[220:223], v29 offset:47104\n" \
"s_waitcnt lgkmcnt(15)\n" \
"v_mfma_f32_16x16x32_bf16 a[0:3], a[160:163], a[128:131], a[0:3]\n" \
"s_addk_i32 s84, 0x2000\n" \
"v_mfma_f32_16x16x32_bf16 a[0:3], a[224:227], a[192:195], a[0:3]\n" \
"buffer_load_dwordx4 v24, s[76:79], s84 offen offset:2048 lds\n" \
"v_mfma_f32_16x16x32_bf16 a[4:7], a[164:167], a[128:131], a[4:7]\n" \
"ds_read_b64_tr_b16 a[176:177], v36 offset:32768\n" \
"v_mfma_f32_16x16x32_bf16 a[4:7], a[228:231], a[192:195], a[4:7]\n" \
"s_addk_i32 s84, 0x2000\n" \
"v_mfma_f32_16x16x32_bf16 a[12:15], a[172:175], a[128:131], a[12:15]\n" \
"buffer_load_dwordx4 v25, s[76:79], s84 offen offset:3072 lds\n" \
"s_waitcnt lgkmcnt(15)\n" \
"v_mfma_f32_16x16x32_bf16 a[12:15], a[236:239], a[192:195], a[12:15]\n" \
"ds_read_b64_tr_b16 a[178:179], v36 offset:33792\n" \
"s_waitcnt lgkmcnt(12)\n" \
"v_mfma_f32_16x16x32_bf16 a[8:11], a[128:131], a[168:171], a[8:11]\n" \
"ds_read_b64_tr_b16 a[240:241], v36 offset:40960\n" \
"s_waitcnt lgkmcnt(9)\n" \
"v_mfma_f32_16x16x32_bf16 a[8:11], a[192:195], a[232:235], a[8:11]\n" \
"ds_read_b64_tr_b16 a[242:243], v36 offset:41984\n" \
"v_mfma_f32_16x16x32_bf16 a[24:27], a[132:135], a[168:171], a[24:27]\n" \
"ds_read_b64_tr_b16 a[180:181], v37 offset:32768\n" \
"v_mfma_f32_16x16x32_bf16 a[24:27], a[196:199], a[232:235], a[24:27]\n" \
"ds_read_b64_tr_b16 a[182:183], v37 offset:33792\n" \
"v_mfma_f32_16x16x32_bf16 a[28:31], a[172:175], a[132:135], a[28:31]\n" \
"ds_read_b64_tr_b16 a[244:245], v37 offset:40960\n" \
"v_mfma_f32_16x16x32_bf16 a[28:31], a[236:239], a[196:199], a[28:31]\n" \
"ds_read_b64_tr_b16 a[246:247], v37 offset:41984\n" \
"v_mfma_f32_16x16x32_bf16 a[20:23], a[164:167], a[132:135], a[20:23]\n" \
"v_mfma_f32_16x16x32_bf16 a[20:23], a[228:231], a[196:199], a[20:23]\n" \
"s_add_u32 s88, s42, 0x2000\n" \
"buffer_store_dwordx4 v[196:199], v20, s[80:83], s88 offen offset:1024 sc1\n" \
"v_mfma_f32_16x16x32_bf16 a[16:19], a[160:163], a[132:135], a[16:19]\n" \
"ds_read_b64_tr_b16 a[184:185], v38 offset:32768\n" \
"v_mfma_f32_16x16x32_bf16 a[16:19], a[224:227], a[196:199], a[16:19]\n" \
"ds_read_b64_tr_b16 a[186:187], v38 offset:33792\n" \
"v_mfma_f32_16x16x32_bf16 a[32:35], a[160:163], a[136:139], a[32:35]\n" \
"ds_read_b64_tr_b16 a[248:249], v38 offset:40960\n" \
"v_mfma_f32_16x16x32_bf16 a[32:35], a[224:227], a[200:203], a[32:35]\n" \
"ds_read_b64_tr_b16 a[250:251], v38 offset:41984\n" \
"v_mfma_f32_16x16x32_bf16 a[36:39], a[164:167], a[136:139], a[36:39]\n" \
"v_lshlrev_b32_e32 v45, 3, v20\n" \
"v_add_u32_e32 v45, s30, v45\n" \
"v_mfma_f32_16x16x32_bf16 a[36:39], a[228:231], a[200:203], a[36:39]\n" \
"s_add_u32 s89, s42, 0x3000\n" \
"buffer_store_dwordx4 v[216:219], v20, s[80:83], s89 offen offset:0 sc1\n" \
"s_getpc_b64 s[86:87]\n" \
"L_ct0_%=:\n" \
"s_add_u32 s86, s86, L_ap0-L_ct0_%=\n" \
"s_addc_u32 s87, s87, 0\n" \
"s_add_u32 m0, s40, 0x2000\n" \
"v_mfma_f32_16x16x32_bf16 a[44:47], a[172:175], a[136:139], a[44:47]\n" \
"global_load_lds_dword v45, s[86:87]\n" \
"v_mfma_f32_16x16x32_bf16 a[44:47], a[236:239], a[200:203], a[44:47]\n" \
"ds_read_b64_tr_b16 a[188:189], v39 offset:32768\n" \
"v_mfma_f32_16x16x32_bf16 a[40:43], a[136:139], a[168:171], a[40:43]\n" \
"ds_read_b64_tr_b16 a[190:191], v39 offset:33792\n" \
"v_mfma_f32_16x16x32_bf16 a[40:43], a[200:203], a[232:235], a[40:43]\n" \
"ds_read_b64_tr_b16 a[252:253], v39 offset:40960\n" \
"v_mfma_f32_16x16x32_bf16 a[56:59], a[140:143], a[168:171], a[56:59]\n" \
"ds_read_b64_tr_b16 a[254:255], v39 offset:41984\n" \
"v_mfma_f32_16x16x32_bf16 a[56:59], a[204:207], a[232:235], a[56:59]\n" \
"v_mfma_f32_16x16x32_bf16 a[60:63], a[172:175], a[140:143], a[60:63]\n" \
"s_add_u32 s88, s42, 0x3000\n" \
"buffer_store_dwordx4 v[220:223], v20, s[80:83], s88 offen offset:1024 sc1\n" \
"v_mfma_f32_16x16x32_bf16 a[60:63], a[236:239], a[204:207], a[60:63]\n" \
"v_mfma_f32_16x16x32_bf16 a[52:55], a[164:167], a[140:143], a[52:55]\n" \
"v_mfma_f32_16x16x32_bf16 a[52:55], a[228:231], a[204:207], a[52:55]\n" \
"v_mfma_f32_16x16x32_bf16 a[48:51], a[160:163], a[140:143], a[48:51]\n" \
"v_mfma_f32_16x16x32_bf16 a[48:51], a[224:227], a[204:207], a[48:51]\n" \
"s_waitcnt lgkmcnt(15)\n" \
"v_mfma_f32_16x16x32_bf16 a[64:67], a[164:167], v[248:251], a[64:67]\n" \
"v_mfma_f32_16x16x32_bf16 a[64:67], a[228:231], v[48:51], a[64:67]\n" \
"v_mfma_f32_16x16x32_bf16 a[80:83], a[172:175], v[248:251], a[80:83]\n" \
"v_mfma_f32_16x16x32_bf16 a[80:83], a[236:239], v[48:51], a[80:83]\n" \
"v_mfma_f32_16x16x32_bf16 a[68:71], v[248:251], a[168:171], a[68:71]\n" \
"v_mfma_f32_16x16x32_bf16 a[68:71], v[48:51], a[232:235], a[68:71]\n" \
"s_waitcnt vmcnt(4) lgkmcnt(0)\n" \
"s_barrier\n" \
"s_nop 0\n" \
"v_mfma_f32_16x16x32_bf16 v[52:55], a[176:179], a[128:131], v[52:55]\n" \
"ds_read_b64_tr_b16 a[160:161], v32 offset:0\n" \
"v_accvgpr_read_b32 v184, a0\n" \
"v_mfma_f32_16x16x32_bf16 v[52:55], a[240:243], a[192:195], v[52:55]\n" \
"v_accvgpr_read_b32 v185, a1\n" \
"v_accvgpr_read_b32 v186, a2\n" \
"v_mfma_f32_16x16x32_bf16 v[56:59], a[180:183], a[128:131], v[56:59]\n" \
"ds_read_b64_tr_b16 a[162:163], v32 offset:512\n" \
"v_accvgpr_read_b32 v187, a3\n" \
"v_mfma_f32_16x16x32_bf16 v[56:59], a[244:247], a[192:195], v[56:59]\n" \
"v_accvgpr_read_b32 v188, a4\n" \
"v_accvgpr_read_b32 v189, a5\n" \
"v_mfma_f32_16x16x32_bf16 v[60:63], a[184:187], a[128:131], v[60:63]\n" \
"v_accvgpr_read_b32 v190, a6\n" \
"v_accvgpr_read_b32 v191, a7\n" \
"v_mfma_f32_16x16x32_bf16 v[60:63], a[248:251], a[192:195], v[60:63]\n" \
"ds_read_b64_tr_b16 a[224:225], v32 offset:4096\n" \
"v_accvgpr_read_b32 v196, a12\n" \
"v_mfma_f32_16x16x32_bf16 v[64:67], a[188:191], a[128:131], v[64:67]\n" \
"v_accvgpr_read_b32 v197, a13\n" \
"v_accvgpr_read_b32 v198, a14\n" \
"v_mfma_f32_16x16x32_bf16 v[64:67], a[252:255], a[192:195], v[64:67]\n" \
"ds_read_b128 a[128:131], v28 offset:0\n" \
"v_accvgpr_read_b32 v199, a15\n" \
"v_mfma_f32_16x16x32_bf16 v[80:83], a[188:191], a[132:135], v[80:83]\n" \
"ds_read_b128 a[192:195], v29 offset:0\n" \
"v_accvgpr_read_b32 v192, a8\n" \
"v_mfma_f32_16x16x32_bf16 v[80:83], a[252:255], a[196:199], v[80:83]\n" \
"ds_read_b64_tr_b16 a[226:227], v32 offset:4608\n" \
"v_accvgpr_read_b32 v193, a9\n" \
"v_mfma_f32_16x16x32_bf16 v[76:79], a[184:187], a[132:135], v[76:79]\n" \
"v_accvgpr_read_b32 v194, a10\n" \
"v_accvgpr_read_b32 v195, a11\n" \
"v_mfma_f32_16x16x32_bf16 v[76:79], a[248:251], a[196:199], v[76:79]\n" \
"ds_read_b64_tr_b16 a[164:165], v33 offset:0\n" \
"v_accvgpr_read_b32 v208, a24\n" \
"v_mfma_f32_16x16x32_bf16 v[72:75], a[180:183], a[132:135], v[72:75]\n" \
"v_accvgpr_read_b32 v209, a25\n" \
"v_accvgpr_read_b32 v210, a26\n" \
"v_mfma_f32_16x16x32_bf16 v[72:75], a[244:247], a[196:199], v[72:75]\n" \
"v_accvgpr_read_b32 v211, a27\n" \
"v_accvgpr_read_b32 v212, a28\n" \
"v_mfma_f32_16x16x32_bf16 v[68:71], a[176:179], a[132:135], v[68:71]\n" \
"ds_read_b64_tr_b16 a[166:167], v33 offset:512\n" \
"v_accvgpr_read_b32 v213, a29\n" \
"v_mfma_f32_16x16x32_bf16 v[68:71], a[240:243], a[196:199], v[68:71]\n" \
"ds_read_b128 a[132:135], v28 offset:2048\n" \
"v_accvgpr_read_b32 v214, a30\n" \
"v_mfma_f32_16x16x32_bf16 v[84:87], a[176:179], a[136:139], v[84:87]\n" \
"ds_read_b128 a[196:199], v29 offset:2048\n" \
"v_accvgpr_read_b32 v215, a31\n" \
"v_mfma_f32_16x16x32_bf16 v[84:87], a[240:243], a[200:203], v[84:87]\n" \
"ds_read_b64_tr_b16 a[228:229], v33 offset:4096\n" \
"v_accvgpr_read_b32 v204, a20\n" \
"v_mfma_f32_16x16x32_bf16 v[88:91], a[180:183], a[136:139], v[88:91]\n" \
"v_accvgpr_read_b32 v205, a21\n" \
"v_accvgpr_read_b32 v206, a22\n" \
"v_mfma_f32_16x16x32_bf16 v[88:91], a[244:247], a[200:203], v[88:91]\n" \
"v_accvgpr_read_b32 v207, a23\n" \
"v_accvgpr_read_b32 v200, a16\n" \
"v_mfma_f32_16x16x32_bf16 v[92:95], a[184:187], a[136:139], v[92:95]\n" \
"ds_read_b64_tr_b16 a[230:231], v33 offset:4608\n" \
"v_accvgpr_read_b32 v201, a17\n" \
"v_mfma_f32_16x16x32_bf16 v[92:95], a[248:251], a[200:203], v[92:95]\n" \
"v_accvgpr_read_b32 v202, a18\n" \
"v_accvgpr_read_b32 v203, a19\n" \
"v_mfma_f32_16x16x32_bf16 v[96:99], a[188:191], a[136:139], v[96:99]\n" \
"v_accvgpr_read_b32 v216, a32\n" \
"v_accvgpr_read_b32 v217, a33\n" \
"v_mfma_f32_16x16x32_bf16 v[96:99], a[252:255], a[200:203], v[96:99]\n" \
"ds_read_b128 a[136:139], v28 offset:4096\n" \
"v_accvgpr_read_b32 v218, a34\n" \
"v_mfma_f32_16x16x32_bf16 v[112:115], a[188:191], a[140:143], v[112:115]\n" \
"ds_read_b128 a[200:203], v29 offset:4096\n" \
"v_accvgpr_read_b32 v219, a35\n" \
"v_mfma_f32_16x16x32_bf16 v[112:115], a[252:255], a[204:207], v[112:115]\n" \
"ds_read_b64_tr_b16 a[172:173], v35 offset:0\n" \
"v_accvgpr_read_b32 v220, a36\n" \
"v_mfma_f32_16x16x32_bf16 v[108:111], a[184:187], a[140:143], v[108:111]\n" \
"ds_read_b64_tr_b16 a[174:175], v35 offset:512\n" \
"v_accvgpr_read_b32 v221, a37\n" \
"v_mfma_f32_16x16x32_bf16 v[108:111], a[248:251], a[204:207], v[108:111]\n" \
"v_accvgpr_read_b32 v222, a38\n" \
"v_accvgpr_read_b32 v223, a39\n" \
"v_mfma_f32_16x16x32_bf16 v[104:107], a[180:183], a[140:143], v[104:107]\n" \
"ds_read_b64_tr_b16 a[236:237], v35 offset:4096\n" \
"v_accvgpr_read_b32 v228, a44\n" \
"v_mfma_f32_16x16x32_bf16 v[104:107], a[244:247], a[204:207], v[104:107]\n" \
"v_accvgpr_read_b32 v229, a45\n" \
"v_accvgpr_read_b32 v230, a46\n" \
"v_mfma_f32_16x16x32_bf16 v[100:103], a[176:179], a[140:143], v[100:103]\n" \
"v_accvgpr_read_b32 v231, a47\n" \
"v_accvgpr_read_b32 v224, a40\n" \
"v_mfma_f32_16x16x32_bf16 v[100:103], a[240:243], a[204:207], v[100:103]\n" \
"ds_read_b128 a[140:143], v28 offset:6144\n" \
"v_accvgpr_read_b32 v225, a41\n" \
"v_mfma_f32_16x16x32_bf16 v[116:119], a[176:179], a[144:147], v[116:119]\n" \
"ds_read_b128 a[204:207], v29 offset:6144\n" \
"v_accvgpr_read_b32 v226, a42\n" \
"v_mfma_f32_16x16x32_bf16 v[116:119], a[240:243], a[208:211], v[116:119]\n" \
"ds_read_b64_tr_b16 a[238:239], v35 offset:4608\n" \
"v_accvgpr_read_b32 v227, a43\n" \
"v_mfma_f32_16x16x32_bf16 v[120:123], a[180:183], a[144:147], v[120:123]\n" \
"ds_read_b64_tr_b16 a[168:169], v34 offset:0\n" \
"v_accvgpr_read_b32 v240, a56\n" \
"v_mfma_f32_16x16x32_bf16 v[120:123], a[244:247], a[208:211], v[120:123]\n" \
"v_accvgpr_read_b32 v241, a57\n" \
"v_accvgpr_read_b32 v242, a58\n" \
"v_mfma_f32_16x16x32_bf16 v[124:127], a[184:187], a[144:147], v[124:127]\n" \
"v_accvgpr_read_b32 v243, a59\n" \
"v_accvgpr_read_b32 v244, a60\n" \
"v_mfma_f32_16x16x32_bf16 v[124:127], a[248:251], a[208:211], v[124:127]\n" \
"ds_read_b64_tr_b16 a[170:171], v34 offset:512\n" \
"v_accvgpr_read_b32 v245, a61\n" \
"v_mfma_f32_16x16x32_bf16 v[128:131], a[188:191], a[144:147], v[128:131]\n" \
"v_accvgpr_read_b32 v246, a62\n" \
"v_accvgpr_read_b32 v247, a63\n" \
"v_mfma_f32_16x16x32_bf16 v[128:131], a[252:255], a[208:211], v[128:131]\n" \
"ds_read_b128 a[144:147], v28 offset:8192\n" \
"v_accvgpr_read_b32 v236, a52\n" \
"v_mfma_f32_16x16x32_bf16 v[144:147], a[188:191], a[148:151], v[144:147]\n" \
"ds_read_b128 a[208:211], v29 offset:8192\n" \
"v_accvgpr_read_b32 v237, a53\n" \
"v_mfma_f32_16x16x32_bf16 v[144:147], a[252:255], a[212:215], v[144:147]\n" \
"ds_read_b64_tr_b16 a[232:233], v34 offset:4096\n" \
"v_accvgpr_read_b32 v238, a54\n" \
"v_mfma_f32_16x16x32_bf16 v[140:143], a[184:187], a[148:151], v[140:143]\n" \
"v_accvgpr_read_b32 v239, a55\n" \
"v_accvgpr_read_b32 v232, a48\n" \
"v_mfma_f32_16x16x32_bf16 v[140:143], a[248:251], a[212:215], v[140:143]\n" \
"ds_read_b64_tr_b16 a[234:235], v34 offset:4608\n" \
"v_accvgpr_read_b32 v233, a49\n" \
"v_mfma_f32_16x16x32_bf16 v[136:139], a[180:183], a[148:151], v[136:139]\n" \
"v_accvgpr_read_b32 v234, a50\n" \
"v_accvgpr_read_b32 v235, a51\n" \
"v_mfma_f32_16x16x32_bf16 v[136:139], a[244:247], a[212:215], v[136:139]\n" \
"v_accvgpr_read_b32 v180, a64\n" \
"v_accvgpr_read_b32 v181, a65\n" \
"v_mfma_f32_16x16x32_bf16 v[132:135], a[176:179], a[148:151], v[132:135]\n" \
"ds_read_b128 v[248:251], v22 offset:0\n" \
"v_accvgpr_read_b32 v182, a66\n" \
"v_mfma_f32_16x16x32_bf16 v[132:135], a[240:243], a[212:215], v[132:135]\n" \
"ds_read_b128 a[148:151], v28 offset:10240\n" \
"v_accvgpr_read_b32 v183, a67\n" \
"v_mfma_f32_16x16x32_bf16 v[148:151], a[176:179], a[152:155], v[148:151]\n" \
"ds_read_b128 a[212:215], v29 offset:10240\n" \
"v_accvgpr_read_b32 v252, a68\n" \
"v_mfma_f32_16x16x32_bf16 v[148:151], a[240:243], a[216:219], v[148:151]\n" \
"ds_read_b128 v[48:51], v23 offset:0\n" \
"v_accvgpr_read_b32 v253, a69\n" \
"v_mfma_f32_16x16x32_bf16 v[152:155], a[180:183], a[152:155], v[152:155]\n" \
"v_accvgpr_read_b32 v254, a70\n" \
"v_accvgpr_read_b32 v255, a71\n" \
"v_mfma_f32_16x16x32_bf16 v[152:155], a[244:247], a[216:219], v[152:155]\n" \
"v_lshrrev_b32_e64 v24, 9, v20\n" \
"v_and_b32_e32 v26, 0x1ff, v20\n" \
"v_mfma_f32_16x16x32_bf16 v[156:159], a[184:187], a[152:155], v[156:159]\n" \
"v_lshl_add_u32 v24, v24, 3, v26\n" \
"v_lshrrev_b32_e64 v25, 8, v20\n" \
"v_mfma_f32_16x16x32_bf16 v[156:159], a[248:251], a[216:219], v[156:159]\n" \
"v_mul_u32_u24_e32 v25, 0x24700, v25\n" \
"v_add_u32_e64 v25, v25, v20\n" \
"v_mfma_f32_16x16x32_bf16 v[160:163], a[188:191], a[152:155], v[160:163]\n" \
"v_lshrrev_b32_e32 v26, 2, v20\n" \
"v_add_u32_e32 v26, s45, v26\n" \
"v_mfma_f32_16x16x32_bf16 v[160:163], a[252:255], a[216:219], v[160:163]\n" \
"ds_read_b128 a[152:155], v28 offset:12288\n" \
"v_add_u32_e64 v27, s46, v20\n" \
"v_mfma_f32_16x16x32_bf16 v[176:179], a[188:191], a[156:159], v[176:179]\n" \
"ds_read_b128 a[216:219], v29 offset:12288\n" \
"v_add_u32_e64 v21, s43, v27\n" \
"v_mfma_f32_16x16x32_bf16 v[176:179], a[252:255], a[220:223], v[176:179]\n" \
"v_mfma_f32_16x16x32_bf16 v[172:175], a[184:187], a[156:159], v[172:175]\n" \
"v_mfma_f32_16x16x32_bf16 v[172:175], a[248:251], a[220:223], v[172:175]\n" \
"v_mfma_f32_16x16x32_bf16 v[168:171], a[180:183], a[156:159], v[168:171]\n" \
"v_mfma_f32_16x16x32_bf16 v[168:171], a[244:247], a[220:223], v[168:171]\n" \
"v_mfma_f32_16x16x32_bf16 v[164:167], a[176:179], a[156:159], v[164:167]\n" \
"v_mfma_f32_16x16x32_bf16 v[164:167], a[240:243], a[220:223], v[164:167]\n" \
"ds_read_b128 a[156:159], v28 offset:14336\n" \
"ds_read_b128 a[220:223], v29 offset:14336\n" \
"s_waitcnt lgkmcnt(15)\n" \
"s_nop 0\n" \
"v_mfma_f32_16x16x32_bf16 v[184:187], a[160:163], a[128:131], v[184:187]\n" \
"v_bfe_u32 v44, v20, 2, 5\n" \
"ds_read_b64_tr_b16 a[176:177], v36 offset:0\n" \
"v_mfma_f32_16x16x32_bf16 v[184:187], a[224:227], a[192:195], v[184:187]\n" \
"buffer_load_dword v44, v44, s[80:83], s38 offen offset:0 sc1\n" \
"v_mfma_f32_16x16x32_bf16 v[188:191], a[164:167], a[128:131], v[188:191]\n" \
"ds_read_b64_tr_b16 a[178:179], v36 offset:1024\n" \
"v_mfma_f32_16x16x32_bf16 v[188:191], a[228:231], a[192:195], v[188:191]\n" \
"ds_read_b64_tr_b16 a[240:241], v36 offset:8192\n" \
"v_mfma_f32_16x16x32_bf16 v[196:199], a[172:175], a[128:131], v[196:199]\n" \
"ds_read_b64_tr_b16 a[242:243], v36 offset:9216\n" \
"s_waitcnt lgkmcnt(15)\n" \
"v_mfma_f32_16x16x32_bf16 v[196:199], a[236:239], a[192:195], v[196:199]\n" \
"v_mfma_f32_16x16x32_bf16 v[192:195], a[128:131], a[168:171], v[192:195]\n" \
"ds_read_b64_tr_b16 a[180:181], v37 offset:0\n" \
"s_waitcnt lgkmcnt(13)\n" \
"v_mfma_f32_16x16x32_bf16 v[192:195], a[192:195], a[232:235], v[192:195]\n" \
"v_mfma_f32_16x16x32_bf16 v[208:211], a[132:135], a[168:171], v[208:211]\n" \
"ds_read_b64_tr_b16 a[182:183], v37 offset:1024\n" \
"v_mfma_f32_16x16x32_bf16 v[208:211], a[196:199], a[232:235], v[208:211]\n" \
"ds_read_b64_tr_b16 a[244:245], v37 offset:8192\n" \
"v_mfma_f32_16x16x32_bf16 v[212:215], a[172:175], a[132:135], v[212:215]\n" \
"v_mfma_f32_16x16x32_bf16 v[212:215], a[236:239], a[196:199], v[212:215]\n" \
"ds_read_b64_tr_b16 a[246:247], v37 offset:9216\n" \
"v_cvt_pk_bf16_f32 v188, v188, v189\n" \
"v_mfma_f32_16x16x32_bf16 v[204:207], a[164:167], a[132:135], v[204:207]\n" \
"v_cvt_pk_bf16_f32 v189, v190, v191\n" \
"v_cvt_pk_bf16_f32 v190, v198, v199\n" \
"v_mfma_f32_16x16x32_bf16 v[204:207], a[228:231], a[196:199], v[204:207]\n" \
"ds_read_b64_tr_b16 a[184:185], v38 offset:0\n" \
"v_mov_b32_e32 v191, 0x3f80\n" \
"v_mfma_f32_16x16x32_bf16 v[200:203], a[160:163], a[132:135], v[200:203]\n" \
"ds_read_b64_tr_b16 a[186:187], v38 offset:1024\n" \
"v_cvt_pk_bf16_f32 v184, v184, v185\n" \
"v_mfma_f32_16x16x32_bf16 v[200:203], a[224:227], a[196:199], v[200:203]\n" \
"v_cvt_pk_bf16_f32 v185, v186, v187\n" \
"v_cvt_pk_bf16_f32 v186, v196, v197\n" \
"v_mfma_f32_16x16x32_bf16 v[216:219], a[160:163], a[136:139], v[216:219]\n" \
"ds_read_b64_tr_b16 a[248:249], v38 offset:8192\n" \
"v_mov_b32_e64 v187, 0\n" \
"v_mfma_f32_16x16x32_bf16 v[216:219], a[224:227], a[200:203], v[216:219]\n" \
"ds_read_b64_tr_b16 a[250:251], v38 offset:9216\n" \
"v_mfma_f32_16x16x32_bf16 v[220:223], a[164:167], a[136:139], v[220:223]\n" \
"v_mfma_f32_16x16x32_bf16 v[220:223], a[228:231], a[200:203], v[220:223]\n" \
"ds_read_b64_tr_b16 a[188:189], v39 offset:0\n" \
"v_cvt_pk_bf16_f32 v204, v204, v205\n" \
"v_mfma_f32_16x16x32_bf16 v[228:231], a[172:175], a[136:139], v[228:231]\n" \
"v_cvt_pk_bf16_f32 v205, v206, v207\n" \
"v_cvt_pk_bf16_f32 v206, v214, v215\n" \
"v_mfma_f32_16x16x32_bf16 v[228:231], a[236:239], a[200:203], v[228:231]\n" \
"ds_read_b64_tr_b16 a[190:191], v39 offset:1024\n" \
"v_mov_b32_e32 v207, 0x3f80\n" \
"v_mfma_f32_16x16x32_bf16 v[224:227], a[136:139], a[168:171], v[224:227]\n" \
"ds_read_b64_tr_b16 a[252:253], v39 offset:8192\n" \
"v_cvt_pk_bf16_f32 v200, v200, v201\n" \
"v_mfma_f32_16x16x32_bf16 v[224:227], a[200:203], a[232:235], v[224:227]\n" \
"v_cvt_pk_bf16_f32 v201, v202, v203\n" \
"v_cvt_pk_bf16_f32 v202, v212, v213\n" \
"v_mfma_f32_16x16x32_bf16 v[240:243], a[140:143], a[168:171], v[240:243]\n" \
"ds_read_b64_tr_b16 a[254:255], v39 offset:9216\n" \
"v_mov_b32_e64 v203, 0\n" \
"v_mfma_f32_16x16x32_bf16 v[240:243], a[204:207], a[232:235], v[240:243]\n" \
"v_mfma_f32_16x16x32_bf16 v[244:247], a[172:175], a[140:143], v[244:247]\n" \
"v_mfma_f32_16x16x32_bf16 v[244:247], a[236:239], a[204:207], v[244:247]\n" \
"v_cvt_pk_bf16_f32 v220, v220, v221\n" \
"v_cvt_pk_bf16_f32 v221, v222, v223\n" \
"v_mfma_f32_16x16x32_bf16 v[236:239], a[164:167], a[140:143], v[236:239]\n" \
"v_cvt_pk_bf16_f32 v222, v230, v231\n" \
"v_mov_b32_e32 v223, 0x3f80\n" \
"v_mfma_f32_16x16x32_bf16 v[236:239], a[228:231], a[204:207], v[236:239]\n" \
"v_cvt_pk_bf16_f32 v216, v216, v217\n" \
"v_cvt_pk_bf16_f32 v217, v218, v219\n" \
"v_mfma_f32_16x16x32_bf16 v[232:235], a[160:163], a[140:143], v[232:235]\n" \
"v_cvt_pk_bf16_f32 v218, v228, v229\n" \
"v_mov_b32_e64 v219, 0\n" \
"v_mfma_f32_16x16x32_bf16 v[232:235], a[224:227], a[204:207], v[232:235]\n" \
"s_waitcnt lgkmcnt(15)\n" \
"s_nop 0\n" \
"v_mfma_f32_16x16x32_bf16 v[180:183], a[164:167], v[248:251], v[180:183]\n" \
"v_mfma_f32_16x16x32_bf16 v[180:183], a[228:231], v[48:51], v[180:183]\n" \
"v_mfma_f32_16x16x32_bf16 a[80:83], a[172:175], v[248:251], a[80:83]\n" \
"v_mfma_f32_16x16x32_bf16 a[80:83], a[236:239], v[48:51], a[80:83]\n" \
"v_cvt_pk_bf16_f32 v236, v236, v237\n" \
"v_cvt_pk_bf16_f32 v237, v238, v239\n" \
"v_mfma_f32_16x16x32_bf16 v[252:255], v[248:251], a[168:171], v[252:255]\n" \
"v_cvt_pk_bf16_f32 v238, v246, v247\n" \
"v_mov_b32_e32 v239, 0x3f80\n" \
"v_mfma_f32_16x16x32_bf16 v[252:255], v[48:51], a[232:235], v[252:255]\n" \
"v_cvt_pk_bf16_f32 v232, v232, v233\n" \
"v_cvt_pk_bf16_f32 v233, v234, v235\n" \
"s_waitcnt vmcnt(0) lgkmcnt(0)\n" \
"s_barrier\n" \
"v_mov_b32_e32 v45, s62\n" \
"s_mov_b64 exec, 1\n" \
"buffer_store_dword v45, off, s[80:83], s37 offset:32 sc1\n" \
"s_mov_b64 exec, -1\n" \
"v_cmp_ne_u32_e32 vcc, s62, v44\n" \
"s_nop 4\n" \
"s_cbranch_vccnz L_poll160_%=\n" \
"L_pollr160_%=:\n" \
"v_mfma_f32_16x16x32_bf16 v[52:55], a[176:179], a[128:131], v[52:55]\n" \
"s_mov_b32 s73, s50\n" \
"s_sub_u32 m0, s31, 0x10000\n" \
"v_mfma_f32_16x16x32_bf16 v[52:55], a[240:243], a[192:195], v[52:55]\n" \
"buffer_load_dwordx4 v20, s[80:83], s73 offen offset:0 sc1 lds\n" \
"v_mfma_f32_16x16x32_bf16 v[56:59], a[180:183], a[128:131], v[56:59]\n" \
"s_add_u32 s74, s50, 0x4000\n" \
"s_add_u32 s75, s50, 0x8000\n" \
"v_mfma_f32_16x16x32_bf16 v[56:59], a[244:247], a[192:195], v[56:59]\n" \
"s_mov_b32 s70, 0x3e0293ee\n" \
"s_mov_b32 s76, s52\n" \
"v_mfma_f32_16x16x32_bf16 v[60:63], a[184:187], a[128:131], v[60:63]\n" \
"v_mul_f32_e32 v52, s70, v52\n" \
"v_mul_f32_e32 v53, s70, v53\n" \
"v_mfma_f32_16x16x32_bf16 v[60:63], a[248:251], a[192:195], v[60:63]\n" \
"buffer_load_dwordx4 v20, s[80:83], s73 offen offset:1024 sc1 lds\n" \
"v_mfma_f32_16x16x32_bf16 v[64:67], a[188:191], a[128:131], v[64:67]\n" \
"v_mul_f32_e32 v54, s70, v54\n" \
"v_mul_f32_e32 v55, s70, v55\n" \
"v_mfma_f32_16x16x32_bf16 v[64:67], a[252:255], a[192:195], v[64:67]\n" \
"v_cvt_pk_bf16_f32 v52, v52, v53\n" \
"v_mul_f32_e64 v56, s70, v56\n" \
"v_mfma_f32_16x16x32_bf16 v[80:83], a[188:191], a[132:135], v[80:83]\n" \
"v_mul_f32_e32 v60, s70, v60\n" \
"v_mul_f32_e32 v61, s70, v61\n" \
"v_mfma_f32_16x16x32_bf16 v[80:83], a[252:255], a[196:199], v[80:83]\n" \
"buffer_load_dwordx4 v20, s[80:83], s73 offen offset:2048 sc1 lds\n" \
"v_mfma_f32_16x16x32_bf16 v[76:79], a[184:187], a[132:135], v[76:79]\n" \
"v_mul_f32_e32 v62, s70, v62\n" \
"v_mul_f32_e32 v63, s70, v63\n" \
"v_mfma_f32_16x16x32_bf16 v[76:79], a[248:251], a[196:199], v[76:79]\n" \
"v_cvt_pk_bf16_f32 v60, v60, v61\n" \
"v_mul_f32_e64 v57, s70, v57\n" \
"v_mfma_f32_16x16x32_bf16 v[72:75], a[180:183], a[132:135], v[72:75]\n" \
"v_mul_f32_e32 v58, s70, v58\n" \
"v_mul_f32_e32 v59, s70, v59\n" \
"v_mfma_f32_16x16x32_bf16 v[72:75], a[244:247], a[196:199], v[72:75]\n" \
"buffer_load_dwordx4 v20, s[80:83], s73 offen offset:3072 sc1 lds\n" \
"v_mfma_f32_16x16x32_bf16 v[68:71], a[176:179], a[132:135], v[68:71]\n" \
"s_sub_u32 m0, s31, s51\n" \
"v_cvt_pk_bf16_f32 v53, v54, v55\n" \
"v_mfma_f32_16x16x32_bf16 v[68:71], a[240:243], a[196:199], v[68:71]\n" \
"v_mul_f32_e32 v64, s70, v64\n" \
"v_mul_f32_e64 v65, s70, v65\n" \
"v_mfma_f32_16x16x32_bf16 v[84:87], a[176:179], a[136:139], v[84:87]\n" \
"v_mul_f32_e32 v66, s70, v66\n" \
"v_mul_f32_e32 v67, s70, v67\n" \
"v_mfma_f32_16x16x32_bf16 v[84:87], a[240:243], a[200:203], v[84:87]\n" \
"buffer_load_dwordx4 v20, s[80:83], s74 offen offset:0 sc1 lds\n" \
"v_mfma_f32_16x16x32_bf16 v[88:91], a[180:183], a[136:139], v[88:91]\n" \
"v_cvt_pk_bf16_f32 v61, v62, v63\n" \
"v_cvt_pk_bf16_f32 v54, v56, v57\n" \
"v_mfma_f32_16x16x32_bf16 v[88:91], a[244:247], a[200:203], v[88:91]\n" \
"v_cvt_pk_bf16_f32 v55, v58, v59\n" \
"v_cvt_pk_bf16_f32 v62, v64, v65\n" \
"v_mfma_f32_16x16x32_bf16 v[92:95], a[184:187], a[136:139], v[92:95]\n" \
"v_cvt_pk_bf16_f32 v63, v66, v67\n" \
"v_mul_f32_e64 v68, s70, v68\n" \
"v_mfma_f32_16x16x32_bf16 v[92:95], a[248:251], a[200:203], v[92:95]\n" \
"buffer_load_dwordx4 v20, s[80:83], s74 offen offset:1024 sc1 lds\n" \
"v_mfma_f32_16x16x32_bf16 v[96:99], a[188:191], a[136:139], v[96:99]\n" \
"ds_write_b128 v27, v[52:55] offset:0\n" \
"v_mfma_f32_16x16x32_bf16 v[96:99], a[252:255], a[200:203], v[96:99]\n" \
"v_mul_f32_e32 v69, s70, v69\n" \
"v_mul_f32_e32 v76, s70, v76\n" \
"v_mfma_f32_16x16x32_bf16 v[112:115], a[188:191], a[140:143], v[112:115]\n" \
"v_mul_f32_e32 v77, s70, v77\n" \
"v_mul_f32_e32 v70, s70, v70\n" \
"v_mfma_f32_16x16x32_bf16 v[112:115], a[252:255], a[204:207], v[112:115]\n" \
"buffer_load_dwordx4 v20, s[80:83], s74 offen offset:2048 sc1 lds\n" \
"v_mfma_f32_16x16x32_bf16 v[108:111], a[184:187], a[140:143], v[108:111]\n" \
"ds_write_b128 v27, v[60:63] offset:1024\n" \
"v_mfma_f32_16x16x32_bf16 v[108:111], a[248:251], a[204:207], v[108:111]\n" \
"v_mul_f32_e64 v71, s70, v71\n" \
"v_cvt_pk_bf16_f32 v68, v68, v69\n" \
"v_mfma_f32_16x16x32_bf16 v[104:107], a[180:183], a[140:143], v[104:107]\n" \
"v_mul_f32_e32 v78, s70, v78\n" \
"v_mul_f32_e32 v79, s70, v79\n" \
"v_mfma_f32_16x16x32_bf16 v[104:107], a[244:247], a[204:207], v[104:107]\n" \
"buffer_load_dwordx4 v20, s[80:83], s74 offen offset:3072 sc1 lds\n" \
"v_mfma_f32_16x16x32_bf16 v[100:103], a[176:179], a[140:143], v[100:103]\n" \
"s_sub_u32 m0, s31, 0x4000\n" \
"v_cvt_pk_bf16_f32 v76, v76, v77\n" \
"v_mfma_f32_16x16x32_bf16 v[100:103], a[240:243], a[204:207], v[100:103]\n" \
"v_mul_f32_e32 v72, s70, v72\n" \
"v_mul_f32_e32 v73, s70, v73\n" \
"v_mfma_f32_16x16x32_bf16 v[116:119], a[176:179], a[144:147], v[116:119]\n" \
"v_mul_f32_e32 v74, s70, v74\n" \
"v_mul_f32_e32 v75, s70, v75\n" \
"v_mfma_f32_16x16x32_bf16 v[116:119], a[240:243], a[208:211], v[116:119]\n" \
"buffer_load_dwordx4 v20, s[80:83], s75 offen offset:0 sc1 lds\n" \
"v_mfma_f32_16x16x32_bf16 v[120:123], a[180:183], a[144:147], v[120:123]\n" \
"v_cvt_pk_bf16_f32 v69, v70, v71\n" \
"v_mul_f32_e64 v80, s70, v80\n" \
"v_mfma_f32_16x16x32_bf16 v[120:123], a[244:247], a[208:211], v[120:123]\n" \
"v_mul_f32_e32 v81, s70, v81\n" \
"v_mul_f32_e32 v82, s70, v82\n" \
".fill (L_pb + 36576 - .) / 4, 4, 0xBF800000\n" \
"L_pp9:\n" \
"s_cbranch_execz L_pret\n" \
"v_mfma_f32_16x16x32_bf16 v[124:127], a[184:187], a[144:147], v[124:127]\n" \
"v_mul_f32_e32 v83, s70, v83\n" \
"v_cvt_pk_bf16_f32 v77, v78, v79\n" \
"v_mfma_f32_16x16x32_bf16 v[124:127], a[248:251], a[208:211], v[124:127]\n" \
"buffer_load_dwordx4 v20, s[80:83], s75 offen offset:1024 sc1 lds\n" \
"v_mfma_f32_16x16x32_bf16 v[128:131], a[188:191], a[144:147], v[128:131]\n" \
"v_cvt_pk_bf16_f32 v70, v72, v73\n" \
"v_cvt_pk_bf16_f32 v71, v74, v75\n" \
"v_mfma_f32_16x16x32_bf16 v[128:131], a[252:255], a[208:211], v[128:131]\n" \
"v_cvt_pk_bf16_f32 v78, v80, v81\n" \
"v_cvt_pk_bf16_f32 v79, v82, v83\n" \
"v_mfma_f32_16x16x32_bf16 v[144:147], a[188:191], a[148:151], v[144:147]\n" \
"ds_write_b128 v27, v[68:71] offset:4096\n" \
"v_mfma_f32_16x16x32_bf16 v[144:147], a[252:255], a[212:215], v[144:147]\n" \
"buffer_load_dwordx4 v20, s[80:83], s75 offen offset:2048 sc1 lds\n" \
"v_mfma_f32_16x16x32_bf16 v[140:143], a[184:187], a[148:151], v[140:143]\n" \
"v_cvt_pk_bf16_f32 v180, v180, v181\n" \
"v_accvgpr_read_b32 v50, a82\n" \
"v_mfma_f32_16x16x32_bf16 v[140:143], a[248:251], a[212:215], v[140:143]\n" \
"ds_write_b128 v27, v[76:79] offset:5120\n" \
"v_mfma_f32_16x16x32_bf16 v[136:139], a[180:183], a[148:151], v[136:139]\n" \
"v_accvgpr_read_b32 v51, a83\n" \
"v_cvt_pk_bf16_f32 v181, v182, v183\n" \
"v_mfma_f32_16x16x32_bf16 v[136:139], a[244:247], a[212:215], v[136:139]\n" \
"buffer_load_dwordx4 v20, s[80:83], s75 offen offset:3072 sc1 lds\n" \
"v_mfma_f32_16x16x32_bf16 v[132:135], a[176:179], a[148:151], v[132:135]\n" \
"s_mov_b32 m0, s53\n" \
"v_cvt_pk_bf16_f32 v182, v50, v51\n" \
"v_mfma_f32_16x16x32_bf16 v[132:135], a[240:243], a[212:215], v[132:135]\n" \
"v_mov_b32_e32 v183, 0x3f80\n" \
"v_cvt_pk_bf16_f32 v234, v244, v245\n" \
"v_mfma_f32_16x16x32_bf16 v[148:151], a[176:179], a[152:155], v[148:151]\n" \
"v_mov_b32_e32 v235, 0\n" \
"v_cvt_pk_bf16_f32 v54, v192, v193\n" \
"v_mfma_f32_16x16x32_bf16 v[148:151], a[240:243], a[216:219], v[148:151]\n" \
"buffer_load_dwordx4 v20, s[80:83], s76 offen offset:0 sc1 lds\n" \
"v_mfma_f32_16x16x32_bf16 v[152:155], a[180:183], a[152:155], v[152:155]\n" \
"v_cvt_pk_bf16_f32 v55, v194, v195\n" \
"v_cvt_pk_bf16_f32 v56, v208, v209\n" \
"v_mfma_f32_16x16x32_bf16 v[152:155], a[244:247], a[216:219], v[152:155]\n" \
"v_cvt_pk_bf16_f32 v57, v210, v211\n" \
"v_cvt_pk_bf16_f32 v58, v224, v225\n" \
"v_cvt_pk_bf16_f32 v59, v226, v227\n" \
"v_cvt_pk_bf16_f32 v60, v240, v241\n" \
"v_cvt_pk_bf16_f32 v61, v242, v243\n" \
"v_mfma_f32_16x16x32_bf16 v[32:35], v[180:183], v[184:187], 0\n" \
"buffer_load_dwordx4 v20, s[80:83], s76 offen offset:1024 sc1 lds\n" \
"v_mfma_f32_16x16x32_bf16 v[156:159], a[184:187], a[152:155], v[156:159]\n" \
"v_mfma_f32_16x16x32_bf16 v[192:195], v[188:191], v[184:187], 0\n" \
"v_mfma_f32_16x16x32_bf16 v[208:211], v[204:207], v[200:203], 0\n" \
"v_mfma_f32_16x16x32_bf16 v[36:39], v[188:191], v[200:203], 0\n" \
"v_mfma_f32_16x16x32_bf16 v[224:227], v[220:223], v[216:219], 0\n" \
"v_mfma_f32_16x16x32_bf16 v[248:251], v[204:207], v[216:219], 0\n" \
"v_mfma_f32_16x16x32_bf16 v[240:243], v[236:239], v[232:235], 0\n" \
"v_mfma_f32_16x16x32_bf16 v[64:67], v[220:223], v[232:235], 0\n" \
"s_mov_b32 s64, 0xaaaaaaaa\n" \
"s_mov_b32 s65, 0xaaaaaaaa\n" \
"v_mfma_f32_16x16x32_bf16 v[156:159], a[248:251], a[216:219], v[156:159]\n" \
"v_cndmask_b32_e64 v32, v32, v33, s[64:65]\n" \
"v_cndmask_b32_e64 v34, v34, v35, s[64:65]\n" \
"v_mfma_f32_16x16x32_bf16 v[160:163], a[188:191], a[152:155], v[160:163]\n" \
"s_mov_b32 s66, 0xfff0ffff\n" \
"s_mov_b32 s67, 0xf000ff00\n" \
"s_mov_b32 s68, 0xffc0fffc\n" \
"s_mov_b32 s69, 0xc000fc00\n" \
"v_cndmask_b32_e64 v192, v192, v193, s[64:65]\n" \
"v_add_f32_e64 v32, s47, v32\n" \
"v_cndmask_b32_e64 v194, v194, v195, s[64:65]\n" \
"v_add_f32_e64 v34, s47, v34\n" \
"v_cndmask_b32_e64 v208, v208, v209, s[64:65]\n" \
"v_cndmask_b32_e64 v36, v36, v37, s[64:65]\n" \
"v_cndmask_b32_e64 v210, v210, v211, s[64:65]\n" \
"v_cndmask_b32_e64 v38, v38, v39, s[64:65]\n" \
"v_cndmask_b32_e64 v224, v224, v225, s[64:65]\n" \
"v_cndmask_b32_e64 v248, v248, v249, s[64:65]\n" \
"v_cndmask_b32_e64 v226, v226, v227, s[64:65]\n" \
"v_cndmask_b32_e64 v250, v250, v251, s[64:65]\n" \
"v_cndmask_b32_e64 v240, v240, v241, s[64:65]\n" \
"v_cndmask_b32_e64 v64, v64, v65, s[64:65]\n" \
"v_cndmask_b32_e64 v242, v242, v243, s[64:65]\n" \
"v_cndmask_b32_e64 v66, v66, v67, s[64:65]\n" \
"v_cndmask_b32_e64 v192, v32, v192, s[66:67]\n" \
"v_cndmask_b32_e64 v194, v34, v194, s[68:69]\n" \
"v_cndmask_b32_e64 v208, v36, v208, s[66:67]\n" \
"v_cndmask_b32_e64 v210, v38, v210, s[68:69]\n" \
"v_cndmask_b32_e64 v224, v248, v224, s[66:67]\n" \
"v_cndmask_b32_e64 v226, v250, v226, s[68:69]\n" \
"v_cndmask_b32_e64 v240, v64, v240, s[66:67]\n" \
"v_cndmask_b32_e64 v242, v66, v242, s[68:69]\n" \
"v_mfma_f32_16x16x32_bf16 v[160:163], a[252:255], a[216:219], v[160:163]\n" \
"v_max_f32_e32 v193, v192, v194\n" \
"v_max_f32_e32 v209, v208, v210\n" \
"v_max_f32_e32 v225, v224, v226\n" \
"v_max_f32_e32 v241, v240, v242\n" \
"v_mul_f32_e32 v84, s70, v84\n" \
"v_permlane32_swap_b32_e32 v193, v225\n" \
"v_permlane32_swap_b32_e32 v209, v241\n" \
"v_mul_f32_e32 v85, s70, v85\n" \
"v_mul_f32_e32 v92, s70, v92\n" \
"v_mul_f32_e32 v93, s70, v93\n" \
"v_permlane32_swap_b32_e32 v196, v228\n" \
"v_permlane32_swap_b32_e32 v212, v244\n" \
"v_max_f32_e32 v193, v193, v225\n" \
"v_max_f32_e32 v209, v209, v241\n" \
"v_accvgpr_read_b32 v48, a80\n" \
"v_accvgpr_read_b32 v49, a81\n" \
"v_mul_f32_e32 v86, s70, v86\n" \
"v_mul_f32_e32 v87, s70, v87\n" \
"v_cvt_pk_bf16_f32 v84, v84, v85\n" \
"v_mul_f32_e32 v94, s70, v94\n" \
"v_mul_f32_e32 v95, s70, v95\n" \
"v_cvt_pk_bf16_f32 v92, v92, v93\n" \
"v_permlane16_swap_b32_e32 v228, v244\n" \
"v_permlane16_swap_b32_e32 v193, v209\n" \
"v_mfma_f32_16x16x32_bf16 v[176:179], a[188:191], a[156:159], v[176:179]\n" \
"s_mov_b32 s72, 0x3eae1a93\n" \
"v_mul_f32_e32 v88, s70, v88\n" \
"v_mul_f32_e32 v89, s70, v89\n" \
"v_mul_f32_e32 v90, s70, v90\n" \
"v_mul_f32_e32 v91, s70, v91\n" \
"v_cvt_pk_bf16_f32 v85, v86, v87\n" \
"v_mul_f32_e32 v96, s70, v96\n" \
"v_mul_f32_e32 v97, s70, v97\n" \
"v_mul_f32_e32 v98, s70, v98\n" \
"v_mul_f32_e32 v99, s70, v99\n" \
"v_cvt_pk_bf16_f32 v93, v94, v95\n" \
"v_mul_f32_e32 v45, 0xbfb8aa3b, v228\n" \
"v_mfma_f32_16x16x16_bf16 v[48:51], v[182:183], v[46:47], 0\n" \
"v_mfma_f32_16x16x16_bf16 v[196:199], v[190:191], v[46:47], 0\n" \
"v_mfma_f32_16x16x16_bf16 v[212:215], v[206:207], v[46:47], 0\n" \
"v_max_f32_e64 v193, v193, v209\n" \
"v_cvt_pk_bf16_f32 v86, v88, v89\n" \
"v_cvt_pk_bf16_f32 v87, v90, v91\n" \
"v_cvt_pk_bf16_f32 v94, v96, v97\n" \
"v_cvt_pk_bf16_f32 v95, v98, v99\n" \
"v_mfma_f32_16x16x16_bf16 v[228:231], v[222:223], v[46:47], 0\n" \
"v_mul_f32_e32 v193, s72, v193\n" \
"v_mov_b32_e32 v209, v193\n" \
"ds_write_b128 v27, v[84:87] offset:8192\n" \
"v_mfma_f32_16x16x16_bf16 v[244:247], v[238:239], v[46:47], 0\n" \
"v_permlane16_swap_b32_e32 v193, v209\n" \
"s_mov_b32 s71, 0x3e0293ee\n" \
"v_mfma_f32_16x16x32_bf16 v[176:179], a[252:255], a[220:223], v[176:179]\n" \
"v_mov_b32_e32 v225, v193\n" \
"v_mov_b32_e32 v241, v209\n" \
"v_mul_f32_e32 v100, s70, v100\n" \
"v_mul_f32_e64 v101, s70, v101\n" \
"ds_write_b128 v27, v[92:95] offset:9216\n" \
"v_mul_f32_e32 v108, s70, v108\n" \
"v_mul_f32_e32 v109, s70, v109\n" \
"v_pk_mul_f32 v[116:117], v[116:117], s[70:71]\n" \
"v_pk_mul_f32 v[124:125], v[124:125], s[70:71]\n" \
"v_pk_mul_f32 v[132:133], v[132:133], s[70:71]\n" \
"v_pk_mul_f32 v[140:141], v[140:141], s[70:71]\n" \
"v_pk_mul_f32 v[148:149], v[148:149], s[70:71]\n" \
"v_pk_mul_f32 v[156:157], v[156:157], s[70:71]\n" \
"v_permlane32_swap_b32_e32 v193, v225\n" \
"v_permlane32_swap_b32_e32 v209, v241\n" \
"v_mul_f32_e32 v102, s70, v102\n" \
"v_mul_f32_e32 v103, s70, v103\n" \
"v_cvt_pk_bf16_f32 v100, v100, v101\n" \
"v_mul_f32_e32 v110, s70, v110\n" \
"v_mul_f32_e32 v111, s70, v111\n" \
"v_cvt_pk_bf16_f32 v108, v108, v109\n" \
"v_pk_mul_f32 v[118:119], v[118:119], s[70:71]\n" \
"v_cvt_pk_bf16_f32 v116, v116, v117\n" \
"v_pk_mul_f32 v[126:127], v[126:127], s[70:71]\n" \
"v_cvt_pk_bf16_f32 v124, v124, v125\n" \
"v_pk_mul_f32 v[134:135], v[134:135], s[70:71]\n" \
"v_cvt_pk_bf16_f32 v132, v132, v133\n" \
"v_pk_mul_f32 v[142:143], v[142:143], s[70:71]\n" \
"v_cvt_pk_bf16_f32 v140, v140, v141\n" \
"v_pk_mul_f32 v[150:151], v[150:151], s[70:71]\n" \
"v_cvt_pk_bf16_f32 v148, v148, v149\n" \
"v_pk_mul_f32 v[158:159], v[158:159], s[70:71]\n" \
"v_cvt_pk_bf16_f32 v156, v156, v157\n" \
"v_fma_f32 v192, v192, s72, -v193\n" \
"v_fma_f32 v194, v194, s72, -v193\n" \
"v_fma_f32 v208, v208, s72, -v209\n" \
"v_fma_f32 v210, v210, s72, -v209\n" \
"v_mul_f32_e32 v104, s70, v104\n" \
"v_mul_f32_e32 v105, s70, v105\n" \
"v_mul_f32_e32 v106, s70, v106\n" \
"v_mul_f32_e32 v107, s70, v107\n" \
"v_cvt_pk_bf16_f32 v101, v102, v103\n" \
"v_mul_f32_e32 v112, s70, v112\n" \
"v_mul_f32_e32 v113, s70, v113\n" \
"v_mul_f32_e32 v114, s70, v114\n" \
"v_mul_f32_e32 v115, s70, v115\n" \
"v_cvt_pk_bf16_f32 v109, v110, v111\n" \
"v_pk_mul_f32 v[120:121], v[120:121], s[70:71]\n" \
"v_pk_mul_f32 v[122:123], v[122:123], s[70:71]\n" \
"v_cvt_pk_bf16_f32 v117, v118, v119\n" \
"v_pk_mul_f32 v[128:129], v[128:129], s[70:71]\n" \
"v_pk_mul_f32 v[130:131], v[130:131], s[70:71]\n" \
"v_cvt_pk_bf16_f32 v125, v126, v127\n" \
"v_pk_mul_f32 v[136:137], v[136:137], s[70:71]\n" \
"v_pk_mul_f32 v[138:139], v[138:139], s[70:71]\n" \
"v_cvt_pk_bf16_f32 v133, v134, v135\n" \
"v_pk_mul_f32 v[144:145], v[144:145], s[70:71]\n" \
"v_pk_mul_f32 v[146:147], v[146:147], s[70:71]\n" \
"v_cvt_pk_bf16_f32 v141, v142, v143\n" \
"v_pk_mul_f32 v[152:153], v[152:153], s[70:71]\n" \
"v_pk_mul_f32 v[154:155], v[154:155], s[70:71]\n" \
"v_cvt_pk_bf16_f32 v149, v150, v151\n" \
"v_pk_mul_f32 v[160:161], v[160:161], s[70:71]\n" \
"v_pk_mul_f32 v[162:163], v[162:163], s[70:71]\n" \
"v_cvt_pk_bf16_f32 v157, v158, v159\n" \
"v_pk_mul_f32 v[176:177], v[176:177], s[70:71]\n" \
"v_pk_mul_f32 v[178:179], v[178:179], s[70:71]\n" \
"v_exp_f32_e32 v192, v192\n" \
"v_exp_f32_e32 v194, v194\n" \
"v_exp_f32_e32 v208, v208\n" \
"v_exp_f32_e32 v210, v210\n" \
"v_fma_f32 v224, v224, s72, -v225\n" \
"v_fma_f32 v226, v226, s72, -v225\n" \
"v_mfma_f32_16x16x32_bf16 v[172:175], a[184:187], a[156:159], v[172:175]\n" \
"v_cvt_pk_bf16_f32 v102, v104, v105\n" \
"v_cvt_pk_bf16_f32 v103, v106, v107\n" \
"v_cvt_pk_bf16_f32 v110, v112, v113\n" \
"v_cvt_pk_bf16_f32 v111, v114, v115\n" \
"v_cvt_pk_bf16_f32 v118, v120, v121\n" \
"v_cvt_pk_bf16_f32 v119, v122, v123\n" \
"v_cvt_pk_bf16_f32 v126, v128, v129\n" \
"v_cvt_pk_bf16_f32 v127, v130, v131\n" \
"v_cvt_pk_bf16_f32 v134, v136, v137\n" \
"v_cvt_pk_bf16_f32 v135, v138, v139\n" \
"v_cvt_pk_bf16_f32 v142, v144, v145\n" \
"v_cvt_pk_bf16_f32 v143, v146, v147\n" \
"v_cvt_pk_bf16_f32 v150, v152, v153\n" \
"v_cvt_pk_bf16_f32 v151, v154, v155\n" \
"v_cvt_pk_bf16_f32 v158, v160, v161\n" \
"v_cvt_pk_bf16_f32 v159, v162, v163\n" \
"v_exp_f32_e32 v224, v224\n" \
"v_exp_f32_e32 v226, v226\n" \
"v_fma_f32 v240, v240, s72, -v241\n" \
"v_fma_f32 v242, v242, s72, -v241\n" \
"ds_write_b128 v27, v[100:103] offset:12288\n" \
"v_cvt_pk_bf16_f32 v192, v192, v194\n" \
"v_cvt_pk_bf16_f32 v208, v208, v210\n" \
"v_exp_f32_e32 v240, v240\n" \
"v_exp_f32_e32 v242, v242\n" \
"v_cvt_pk_bf16_f32 v68, v48, v49\n" \
"v_cvt_pk_bf16_f32 v69, v50, v51\n" \
"v_cvt_pk_bf16_f32 v70, v196, v197\n" \
"v_cvt_pk_bf16_f32 v71, v198, v199\n" \
"ds_write_b128 v27, v[108:111] offset:13312\n" \
"v_cvt_pk_bf16_f32 v72, v212, v213\n" \
"v_cvt_pk_bf16_f32 v73, v214, v215\n" \
"v_perm_b32 v184, v192, v192, v40\n" \
"v_perm_b32 v185, v192, v192, v41\n" \
"v_perm_b32 v186, v192, v192, v42\n" \
"v_perm_b32 v187, v192, v192, v43\n" \
"v_perm_b32 v200, v208, v208, v40\n" \
"v_perm_b32 v201, v208, v208, v41\n" \
"v_perm_b32 v202, v208, v208, v42\n" \
"v_perm_b32 v203, v208, v208, v43\n" \
"ds_write_b128 v21, v[116:119] offset:16384\n" \
"v_cvt_pk_bf16_f32 v224, v224, v226\n" \
"v_cvt_pk_bf16_f32 v74, v228, v229\n" \
"v_cvt_pk_bf16_f32 v75, v230, v231\n" \
"v_mfma_f32_16x16x32_bf16 v[32:35], v[68:71], v[184:187], 0\n" \
"v_mfma_f32_16x16x32_bf16 v[36:39], v[70:73], v[200:203], 0\n" \
"v_perm_b32 v216, v224, v224, v40\n" \
"v_perm_b32 v217, v224, v224, v41\n" \
"ds_write_b128 v21, v[124:127] offset:17408\n" \
"v_perm_b32 v218, v224, v224, v42\n" \
"v_perm_b32 v219, v224, v224, v43\n" \
"v_cvt_pk_bf16_f32 v240, v240, v242\n" \
"v_mfma_f32_16x16x32_bf16 v[172:175], a[248:251], a[220:223], v[172:175]\n" \
"v_cvt_pk_bf16_f32 v76, v244, v245\n" \
"v_cvt_pk_bf16_f32 v77, v246, v247\n" \
"v_mfma_f32_16x16x32_bf16 v[248:251], v[72:75], v[216:219], 0\n" \
"v_perm_b32 v232, v240, v240, v40\n" \
"v_perm_b32 v233, v240, v240, v41\n" \
"ds_write_b128 v21, v[132:135] offset:20480\n" \
"v_perm_b32 v234, v240, v240, v42\n" \
"v_perm_b32 v235, v240, v240, v43\n" \
"v_pk_mul_f32 v[172:173], v[172:173], s[70:71]\n" \
"v_pk_mul_f32 v[174:175], v[174:175], s[70:71]\n" \
"v_cvt_pk_bf16_f32 v172, v172, v173\n" \
"v_cvt_pk_bf16_f32 v173, v174, v175\n" \
"v_cvt_pk_bf16_f32 v174, v176, v177\n" \
"v_cvt_pk_bf16_f32 v175, v178, v179\n" \
"v_mfma_f32_16x16x32_bf16 v[64:67], v[74:77], v[232:235], 0\n" \
"ds_write_b128 v21, v[140:143] offset:21504\n" \
"v_mfma_f32_16x16x32_bf16 v[168:171], a[180:183], a[156:159], v[168:171]\n" \
"v_cvt_pk_bf16_f32 v52, v252, v253\n" \
"v_cvt_pk_bf16_f32 v53, v254, v255\n" \
"v_mfma_f32_16x16x32_bf16 v[168:171], a[244:247], a[220:223], v[168:171]\n" \
"v_exp_f32_e64 v45, v45\n" \
"v_mfma_f32_16x16x32_bf16 v[192:195], v[52:55], v[184:187], 0\n" \
"ds_write_b128 v21, v[148:151] offset:24576\n" \
"v_mfma_f32_16x16x32_bf16 v[208:211], v[54:57], v[200:203], 0\n" \
"v_mfma_f32_16x16x32_bf16 v[224:227], v[56:59], v[216:219], 0\n" \
"v_mfma_f32_16x16x32_bf16 v[240:243], v[58:61], v[232:235], 0\n" \
"ds_write_b128 v21, v[156:159] offset:25600\n" \
"v_mov_b32_dpp v34, v38 quad_perm:[0,1,2,3] row_mask:0x2 bank_mask:0xf\n" \
"v_pk_mul_f32 v[168:169], v[168:169], s[70:71]\n" \
"v_pk_mul_f32 v[170:171], v[170:171], s[70:71]\n" \
"v_mov_b32_dpp v34, v250 quad_perm:[0,1,2,3] row_mask:0x4 bank_mask:0xf\n" \
"v_add_f32_e64 v45, 1.0, v45\n" \
"v_mfma_f32_16x16x32_bf16 v[164:167], a[176:179], a[156:159], v[164:167]\n" \
"v_mov_b32_dpp v34, v66 quad_perm:[0,1,2,3] row_mask:0x8 bank_mask:0xf\n" \
"v_mov_b32_dpp v32, v36 quad_perm:[0,1,2,3] row_mask:0x2 bank_mask:0xf\n" \
"v_rcp_f32_e64 v45, v45\n" \
"ds_write_b128 v21, v[172:175] offset:29696\n" \
"v_mov_b32_dpp v33, v37 quad_perm:[0,1,2,3] row_mask:0x2 bank_mask:0xf\n" \
"v_rcp_f32_e64 v34, v34\n" \
"v_mov_b32_dpp v32, v248 quad_perm:[0,1,2,3] row_mask:0x4 bank_mask:0xf\n" \
"v_mov_b32_dpp v33, v249 quad_perm:[0,1,2,3] row_mask:0x4 bank_mask:0xf\n" \
"v_mul_f32_e64 v34, v45, v34\n" \
"v_mov_b32_dpp v32, v64 quad_perm:[0,1,2,3] row_mask:0x8 bank_mask:0xf\n" \
"v_mov_b32_dpp v33, v65 quad_perm:[0,1,2,3] row_mask:0x8 bank_mask:0xf\n" \
"v_mul_f32_e32 v32, v34, v32\n" \
"v_mul_f32_e32 v33, v34, v33\n" \
"v_mov_b32_e64 v38, v34\n" \
"v_mfma_f32_16x16x32_bf16 v[164:167], a[240:243], a[220:223], v[164:167]\n" \
"v_sub_f32_e32 v44, 1.0, v45\n" \
"v_permlane16_swap_b32_e32 v34, v38\n" \
"v_mov_b32_e32 v250, v34\n" \
"v_mov_b32_e32 v66, v38\n" \
"v_cvt_pk_bf16_f32 v32, v32, v33\n" \
"v_permlane32_swap_b32_e32 v34, v250\n" \
"v_permlane32_swap_b32_e32 v38, v66\n" \
"v_mul_f32_e64 v192, v34, v192\n" \
"v_pk_mul_f32 v[164:165], v[164:165], s[70:71]\n" \
"v_pk_mul_f32 v[166:167], v[166:167], s[70:71]\n" \
"v_cvt_pk_bf16_f32 v164, v164, v165\n" \
"v_cvt_pk_bf16_f32 v165, v166, v167\n" \
"v_cvt_pk_bf16_f32 v166, v168, v169\n" \
"v_cvt_pk_bf16_f32 v167, v170, v171\n" \
"ds_write_b128 v21, v[164:167] offset:28672\n" \
"v_mul_f32_e32 v193, v34, v193\n" \
"v_mul_f32_e32 v208, v38, v208\n" \
"v_mul_f32_e32 v209, v38, v209\n" \
"v_mul_f32_e32 v224, v250, v224\n" \
"v_mul_f32_e32 v225, v250, v225\n" \
"v_mul_f32_e32 v240, v66, v240\n" \
"v_mul_f32_e32 v241, v66, v241\n" \
"v_mul_f32_e32 v194, v34, v194\n" \
"v_mul_f32_e64 v195, v34, v195\n" \
"v_cvt_pk_bf16_f32 v192, v192, v193\n" \
"v_mul_f32_e32 v210, v38, v210\n" \
"v_mul_f32_e32 v211, v38, v211\n" \
"v_cvt_pk_bf16_f32 v208, v208, v209\n" \
"v_mul_f32_e32 v226, v250, v226\n" \
"v_mul_f32_e32 v227, v250, v227\n" \
"v_cvt_pk_bf16_f32 v224, v224, v225\n" \
"v_mul_f32_e32 v242, v66, v242\n" \
"v_mul_f32_e32 v243, v66, v243\n" \
"v_cvt_pk_bf16_f32 v240, v240, v241\n" \
"v_cvt_pk_bf16_f32 v193, v194, v195\n" \
"s_add_u32 s84, s48, 0x0\n" \
"s_nop 0\n" \
"v_cvt_pk_bf16_f32 v209, v210, v211\n" \
"s_add_u32 s85, s48, 0x24800\n" \
"v_cvt_pk_bf16_f32 v225, v226, v227\n" \
"s_add_u32 s86, s48, 0x49000\n" \
"v_cvt_pk_bf16_f32 v241, v242, v243\n" \
"s_add_u32 s87, s48, 0x6d800\n" \
"ds_write_b32 v26, v44\n" \
"buffer_store_dword v32, v25, s[80:83], s49 offen sc1\n" \
"buffer_store_dwordx2 v[192:193], v24, s[80:83], s84 offen sc1\n" \
"buffer_store_dwordx2 v[208:209], v24, s[80:83], s85 offen sc1\n" \
"buffer_store_dwordx2 v[224:225], v24, s[80:83], s86 offen sc1\n" \
"buffer_store_dwordx2 v[240:241], v24, s[80:83], s87 offen sc1\n" \
"s_branch L_end_%=\n" \
"L_poll60_%=:\n" \
"s_mov_b32 s84, 0\n" \
"L_pl60_%=:\n" \
"s_sleep 1\n" \
"v_bfe_u32 v44, v20, 2, 5\n" \
"buffer_load_dword v44, v44, s[80:83], s38 offen offset:128 sc1\n" \
"s_waitcnt vmcnt(0)\n" \
"v_cmp_ne_u32_e32 vcc, s62, v44\n" \
"s_add_u32 s84, s84, 1\n" \
"s_nop 3\n" \
"s_cbranch_vccz L_pollr60_%=\n" \
"s_cmp_gt_u32 s84, 0x400000\n" \
"s_cbranch_scc0 L_pl60_%=\n" \
"s_branch L_pollr60_%=\n" \
"L_poll160_%=:\n" \
"s_mov_b32 s84, 0\n" \
"L_pl160_%=:\n" \
"s_sleep 1\n" \
"v_bfe_u32 v44, v20, 2, 5\n" \
"buffer_load_dword v44, v44, s[80:83], s38 offen offset:0 sc1\n" \
"s_waitcnt vmcnt(0)\n" \
"v_cmp_ne_u32_e32 vcc, s62, v44\n" \
"s_add_u32 s84, s84, 1\n" \
"s_nop 3\n" \
"s_cbranch_vccz L_pollr160_%=\n" \
"s_cmp_gt_u32 s84, 0x400000\n" \
"s_cbranch_scc0 L_pl160_%=\n" \
"s_branch L_pollr160_%=\n" \
".fill (L_pb + 42440 - .) / 4, 4, 0xBF800000\n" \
"L_w1_%=:\n" \
"s_waitcnt vmcnt(18)\n" \
"s_barrier\n" \
"ds_read_b64_tr_b16 a[160:161], v32 offset:0\n" \
"ds_read_b64_tr_b16 a[162:163], v32 offset:1024\n" \
"ds_read_b64_tr_b16 a[224:225], v32 offset:8192\n" \
"ds_read_b64_tr_b16 a[226:227], v32 offset:9216\n" \
"ds_read_b128 a[128:131], v28 offset:16384\n" \
"ds_read_b128 a[192:195], v29 offset:16384\n" \
"ds_read_b64_tr_b16 a[164:165], v33 offset:0\n" \
"ds_read_b64_tr_b16 a[166:167], v33 offset:1024\n" \
"ds_read_b64_tr_b16 a[228:229], v33 offset:8192\n" \
"ds_read_b64_tr_b16 a[230:231], v33 offset:9216\n" \
"ds_read_b64_tr_b16 a[168:169], v34 offset:0\n" \
"ds_read_b64_tr_b16 a[170:171], v34 offset:1024\n" \
"ds_read_b64_tr_b16 a[232:233], v34 offset:8192\n" \
"ds_read_b64_tr_b16 a[234:235], v34 offset:9216\n" \
"ds_read_b64_tr_b16 a[172:173], v35 offset:0\n" \
"ds_read_b64_tr_b16 a[174:175], v35 offset:1024\n" \
"ds_read_b64_tr_b16 a[236:237], v35 offset:8192\n" \
"ds_read_b64_tr_b16 a[238:239], v35 offset:9216\n" \
"ds_read_b128 a[132:135], v28 offset:18432\n" \
"ds_read_b128 a[196:199], v29 offset:18432\n" \
"ds_read_b128 a[136:139], v28 offset:20480\n" \
"ds_read_b128 a[200:203], v29 offset:20480\n" \
"ds_read_b128 a[140:143], v28 offset:22528\n" \
"ds_read_b128 a[204:207], v29 offset:22528\n" \
"ds_read_b128 a[144:147], v28 offset:0\n" \
"ds_read_b128 a[208:211], v29 offset:0\n" \
"ds_read_b128 a[148:151], v28 offset:2048\n" \
"ds_read_b128 a[212:215], v29 offset:2048\n" \
"ds_read_b128 a[152:155], v28 offset:4096\n" \
"ds_read_b128 a[216:219], v29 offset:4096\n" \
"ds_read_b128 a[156:159], v28 offset:6144\n" \
"ds_read_b128 a[220:223], v29 offset:6144\n" \
"s_waitcnt lgkmcnt(15)\n" \
"v_mfma_f32_16x16x32_bf16 a[0:3], a[128:131], a[160:163], 0\n" \
"ds_read_b64_tr_b16 a[176:177], v36 offset:0\n" \
"v_mad_i32_i24 v217, v216, -6, v215\n" \
"v_mfma_f32_16x16x32_bf16 a[0:3], a[192:195], a[224:227], a[0:3]\n" \
"v_add_u32_e32 v218, 0xfffffe50, v215\n" \
"s_mul_i32 s35, s6, 0x48\n" \
"v_mfma_f32_16x16x32_bf16 a[4:7], a[128:131], a[164:167], 0\n" \
"v_lshrrev_b32_e32 v219, 1, v217\n" \
"v_add_u32_e64 v218, s35, v216\n" \
"v_mfma_f32_16x16x32_bf16 a[4:7], a[192:195], a[228:231], a[4:7]\n" \
"ds_read_b64_tr_b16 a[178:179], v36 offset:1024\n" \
"v_mul_u32_u24_e32 v219, 0x480, v219\n" \
"v_mfma_f32_16x16x32_bf16 a[8:11], a[128:131], a[168:171], 0\n" \
"v_and_b32_e64 v214, 16, v20\n" \
"v_mad_u32_u24 v219, v218, s5, v219\n" \
"v_mfma_f32_16x16x32_bf16 a[8:11], a[192:195], a[232:235], a[8:11]\n" \
"v_or_b32_e32 v192, v219, v214\n" \
"v_mov_b32_e32 v220, 2.0\n" \
"v_mfma_f32_16x16x32_bf16 a[12:15], a[128:131], a[172:175], 0\n" \
"ds_read_b64_tr_b16 a[240:241], v36 offset:8192\n" \
"v_mov_b32_e32 v222, 7\n" \
"s_waitcnt lgkmcnt(15)\n" \
"v_mfma_f32_16x16x32_bf16 a[12:15], a[192:195], a[236:239], a[12:15]\n" \
"v_lshlrev_b32_e32 v220, 7, v218\n" \
"v_lshrrev_b32_e32 v218, 1, v218\n" \
"v_mfma_f32_16x16x32_bf16 a[28:31], a[132:135], a[172:175], 0\n" \
"v_and_b32_e32 v218, 4, v218\n" \
"v_and_b32_e32 v216, 2, v216\n" \
"v_mfma_f32_16x16x32_bf16 a[28:31], a[196:199], a[236:239], a[28:31]\n" \
"ds_read_b64_tr_b16 a[242:243], v36 offset:9216\n" \
"s_mul_i32 s7, s6, 0x2400\n" \
"v_mfma_f32_16x16x32_bf16 a[24:27], a[132:135], a[168:171], 0\n" \
"s_sub_i32 s34, 0x22000, s7\n" \
"v_bitop3_b32 v216, v217, v218, v216 bitop3:0x1e\n" \
"v_mfma_f32_16x16x32_bf16 a[24:27], a[196:199], a[232:235], a[24:27]\n" \
"v_or_b32_e32 v213, 0x24400, v20\n" \
"v_lshlrev_b32_e64 v216, 4, v216\n" \
"v_mfma_f32_16x16x32_bf16 a[20:23], a[132:135], a[164:167], 0\n" \
"v_add3_u32 v196, v220, s34, v216\n" \
"v_mov_b32_e64 v216, v213\n" \
"v_mfma_f32_16x16x32_bf16 a[20:23], a[196:199], a[228:231], a[20:23]\n" \
"ds_read_b64_tr_b16 a[180:181], v37 offset:0\n" \
"s_movk_i32 s36, 0x1b0\n" \
"v_mfma_f32_16x16x32_bf16 a[16:19], a[132:135], a[160:163], 0\n" \
"v_or_b32_e32 v216, 0x100, v215\n" \
"v_mul_u32_u24_e32 v217, 0x2aab, v216\n" \
"v_mfma_f32_16x16x32_bf16 a[16:19], a[196:199], a[224:227], a[16:19]\n" \
"v_add_u32_e32 v215, 0xffffff50, v215\n" \
"v_lshrrev_b32_e32 v217, 16, v217\n" \
"s_waitcnt lgkmcnt(15)\n" \
"v_mfma_f32_16x16x32_bf16 a[32:35], a[136:139], a[160:163], 0\n" \
"s_mov_b32 m0, s32\n" \
"ds_read_b64_tr_b16 a[182:183], v37 offset:1024\n" \
"v_mfma_f32_16x16x32_bf16 a[32:35], a[200:203], a[224:227], a[32:35]\n" \
"s_mov_b32 exec_lo, 0xff000000\n" \
"s_mov_b32 exec_hi, 0x0\n" \
"global_load_lds_dword v[184:185], off\n" \
"s_mov_b64 exec, -1\n" \
"v_mfma_f32_16x16x32_bf16 a[36:39], a[136:139], a[164:167], 0\n" \
"v_mov_b32_e64 v220, v215\n" \
"v_mad_i32_i24 v218, v217, -6, v216\n" \
"v_mfma_f32_16x16x32_bf16 a[36:39], a[200:203], a[228:231], a[36:39]\n" \
"ds_read_b64_tr_b16 a[244:245], v37 offset:8192\n" \
"v_cmp_gt_u32_e64 vcc, s36, v216\n" \
"v_mfma_f32_16x16x32_bf16 a[40:43], a[136:139], a[168:171], 0\n" \
"v_mov_b32_e32 v221, 7\n" \
"v_lshrrev_b32_e32 v222, 1, v218\n" \
"v_mfma_f32_16x16x32_bf16 a[40:43], a[200:203], a[232:235], a[40:43]\n" \
"s_mov_b32 m0, s32\n" \
"v_cndmask_b32_e32 v217, v220, v217, vcc\n" \
"v_mfma_f32_16x16x32_bf16 a[44:47], a[136:139], a[172:175], 0\n" \
"s_mov_b32 exec_lo, 0xffff\n" \
"s_mov_b32 exec_hi, 0x0\n" \
"global_load_lds_dword v[186:187], off\n" \
"s_mov_b64 exec, -1\n" \
"v_mfma_f32_16x16x32_bf16 a[44:47], a[200:203], a[236:239], a[44:47]\n" \
"ds_read_b64_tr_b16 a[246:247], v37 offset:9216\n" \
"s_movk_i32 s4, 0x48\n" \
"s_waitcnt lgkmcnt(15)\n" \
"v_mfma_f32_16x16x32_bf16 a[60:63], a[140:143], a[172:175], 0\n" \
"v_add_u32_e64 v220, s35, v217\n" \
"v_mul_u32_u24_e32 v222, 0x480, v222\n" \
"v_mfma_f32_16x16x32_bf16 a[60:63], a[204:207], a[236:239], a[60:63]\n" \
"v_mad_u32_u24 v222, v220, s5, v222\n" \
"v_cmp_gt_u32_e64 s[4:5], s4, v215\n" \
"v_mfma_f32_16x16x32_bf16 a[56:59], a[140:143], a[168:171], 0\n" \
"ds_read_b64_tr_b16 a[184:185], v38 offset:0\n" \
"v_cndmask_b32_e64 v215, v221, v218, vcc\n" \
"v_mfma_f32_16x16x32_bf16 a[56:59], a[204:207], a[232:235], a[56:59]\n" \
"v_lshrrev_b32_e32 v218, 1, v220\n" \
"v_or_b32_e32 v214, v222, v214\n" \
"v_mfma_f32_16x16x32_bf16 a[52:55], a[140:143], a[164:167], 0\n" \
"v_mov_b32_e32 v222, 2.0\n" \
"v_and_b32_e32 v218, 4, v218\n" \
"v_mfma_f32_16x16x32_bf16 a[52:55], a[204:207], a[228:231], a[52:55]\n" \
"ds_read_b64_tr_b16 a[186:187], v38 offset:1024\n" \
"v_and_b32_e64 v217, 2, v217\n" \
"v_mfma_f32_16x16x32_bf16 a[48:51], a[140:143], a[160:163], 0\n" \
"v_bitop3_b32 v215, v215, v218, v217 bitop3:0x1e\n" \
"v_lshlrev_b32_e64 v223, 1, v220\n" \
"v_mfma_f32_16x16x32_bf16 a[48:51], a[204:207], a[224:227], a[48:51]\n" \
"v_mov_b32_e32 v224, 2.0\n" \
"v_cndmask_b32_e32 v193, v222, v214, vcc\n" \
"v_mfma_f32_16x16x32_bf16 a[64:67], a[144:147], a[160:163], 0\n" \
"ds_read_b64_tr_b16 a[248:249], v38 offset:8192\n" \
"v_lshlrev_b32_e32 v214, 7, v220\n" \
"s_waitcnt lgkmcnt(15)\n" \
"v_mfma_f32_16x16x32_bf16 a[64:67], a[208:211], a[224:227], a[64:67]\n" \
"v_lshlrev_b32_e32 v215, 4, v215\n" \
"s_mul_i32 s39, s3, 18\n" \
"v_mfma_f32_16x16x32_bf16 a[68:71], a[144:147], a[164:167], 0\n" \
"v_add3_u32 v214, v214, s34, v215\n" \
"v_cndmask_b32_e64 v195, v224, v223, s[4:5]\n" \
"v_mfma_f32_16x16x32_bf16 a[68:71], a[208:211], a[228:231], a[68:71]\n" \
"ds_read_b64_tr_b16 a[250:251], v38 offset:9216\n" \
"s_movk_i32 s4, 0x1f8\n" \
"v_mfma_f32_16x16x32_bf16 a[72:75], a[144:147], a[168:171], 0\n" \
"v_mov_b32_e32 v215, v213\n" \
"s_add_i32 s35, s35, s39\n" \
"v_mfma_f32_16x16x32_bf16 a[72:75], a[208:211], a[232:235], a[72:75]\n" \
"v_cmp_gt_u32_e32 vcc, s4, v216\n" \
"v_add_u32_e64 v212, s35, v212\n" \
"v_mfma_f32_16x16x32_bf16 a[76:79], a[144:147], a[172:175], 0\n" \
"v_mov_b32_e32 v219, 2.0\n" \
"v_cndmask_b32_e32 v197, v215, v214, vcc\n" \
"v_mfma_f32_16x16x32_bf16 a[76:79], a[208:211], a[236:239], a[76:79]\n" \
"ds_read_b64_tr_b16 a[188:189], v39 offset:0\n" \
"s_movk_i32 s35, 0x120\n" \
"v_mfma_f32_16x16x32_bf16 a[92:95], a[148:151], a[172:175], 0\n" \
"v_mul_u32_u24_e32 v214, 0xd80, v212\n" \
"v_cmp_gt_u32_e32 vcc, s35, v20\n" \
"v_mfma_f32_16x16x32_bf16 a[92:95], a[212:215], a[236:239], a[92:95]\n" \
"v_lshrrev_b32_e32 v215, 1, v212\n" \
"s_and_b32 s4, s2, 7\n" \
"v_mfma_f32_16x16x32_bf16 a[88:91], a[148:151], a[168:171], 0\n" \
"ds_read_b64_tr_b16 a[190:191], v39 offset:1024\n" \
"v_cndmask_b32_e64 v194, v219, v214, vcc\n" \
"v_mfma_f32_16x16x32_bf16 a[88:91], a[212:215], a[232:235], a[88:91]\n" \
"v_lshlrev_b32_e32 v214, 7, v212\n" \
"v_lshrrev_b32_e32 v212, 2, v212\n" \
"v_mfma_f32_16x16x32_bf16 a[84:87], a[148:151], a[164:167], 0\n" \
"v_and_b32_e64 v212, 2, v212\n" \
"v_and_or_b32 v212, v215, 1, v212\n" \
"v_mfma_f32_16x16x32_bf16 a[84:87], a[212:215], a[228:231], a[84:87]\n" \
"ds_read_b64_tr_b16 a[252:253], v39 offset:8192\n" \
"s_lshl_b32 s5, s4, 2\n" \
"v_mfma_f32_16x16x32_bf16 a[80:83], a[148:151], a[160:163], 0\n" \
"s_bfe_u32 s38, s2, 0x20003\n" \
"v_lshlrev_b32_e32 v212, 5, v212\n" \
"v_mfma_f32_16x16x32_bf16 a[80:83], a[212:215], a[224:227], a[80:83]\n" \
"s_movk_i32 s35, 0x60\n" \
"s_or_b32 s5, s5, s38\n" \
"s_waitcnt lgkmcnt(15)\n" \
"v_mfma_f32_16x16x32_bf16 a[96:99], a[152:155], a[160:163], 0\n" \
"ds_read_b64_tr_b16 a[254:255], v39 offset:9216\n" \
"v_bitop3_b32 v212, v212, s35, v214 bitop3:0x36\n" \
"v_mfma_f32_16x16x32_bf16 a[96:99], a[216:219], a[224:227], a[96:99]\n" \
"s_lshr_b32 s36, s2, 3\n" \
"v_add_u32_e64 v212, s34, v212\n" \
"v_mfma_f32_16x16x32_bf16 a[100:103], a[152:155], a[164:167], 0\n" \
"s_mul_i32 s34, s5, 0x12000\n" \
"s_or_b32 s40, s34, 0x6000000\n" \
"v_mfma_f32_16x16x32_bf16 a[100:103], a[216:219], a[228:231], a[100:103]\n" \
"s_mul_i32 s34, s36, 0x90000\n" \
"s_lshl_b32 s36, s3, 14\n" \
"v_mfma_f32_16x16x32_bf16 a[104:107], a[152:155], a[168:171], 0\n" \
"s_add_i32 s34, s34, s36\n" \
"s_lshl_b32 s38, s5, 8\n" \
"v_mfma_f32_16x16x32_bf16 a[104:107], a[216:219], a[232:235], a[104:107]\n" \
"s_add_i32 s41, s34, 0x4240000\n" \
"s_mul_i32 s34, s5, 36\n" \
"v_mfma_f32_16x16x32_bf16 a[108:111], a[152:155], a[172:175], 0\n" \
"s_add_i32 s5, s7, s40\n" \
"s_addk_i32 s5, 0x2000\n" \
"v_mfma_f32_16x16x32_bf16 a[108:111], a[216:219], a[236:239], a[108:111]\n" \
"s_mov_b32 s37, 2.0\n" \
"s_or_b32 s38, s38, 0x5be8000\n" \
"v_mfma_f32_16x16x32_bf16 a[124:127], a[156:159], a[172:175], 0\n" \
"s_cmp_lg_u32 s3, 0\n" \
"s_cselect_b32 s36, s37, s5\n" \
"v_mfma_f32_16x16x32_bf16 a[124:127], a[220:223], a[236:239], a[124:127]\n" \
"s_lshl_b32 s5, s6, 2\n" \
"s_or_b32 s5, s38, s5\n" \
"v_mfma_f32_16x16x32_bf16 a[120:123], a[156:159], a[168:171], 0\n" \
"s_lshl_b32 s35, s3, 10\n" \
"v_mfma_f32_16x16x32_bf16 a[120:123], a[220:223], a[232:235], a[120:123]\n" \
"s_cmpk_lt_u32 s3, 3\n" \
"s_cselect_b32 s37, s37, s5\n" \
"v_mfma_f32_16x16x32_bf16 a[116:119], a[156:159], a[164:167], 0\n" \
"s_lshl_b32 s5, s4, 16\n" \
"s_lshl_b32 s4, s4, 11\n" \
"v_mfma_f32_16x16x32_bf16 a[116:119], a[220:223], a[228:231], a[116:119]\n" \
"v_cndmask_b32_e32 v198, v213, v212, vcc\n" \
"v_or_b32_e32 v212, s35, v20\n" \
"v_mfma_f32_16x16x32_bf16 a[112:115], a[156:159], a[160:163], 0\n" \
"s_or_b32 s39, s40, s35\n" \
"s_or_b32 s55, s41, s4\n" \
"v_mfma_f32_16x16x32_bf16 a[112:115], a[220:223], a[224:227], a[112:115]\n" \
"s_waitcnt vmcnt(4) lgkmcnt(0)\n" \
"s_barrier\n" \
"v_mfma_f32_16x16x32_bf16 v[52:55], a[128:131], a[176:179], 0\n" \
"s_add_u32 m0, s30, 0x0\n" \
"s_movk_i32 s84, 0x100\n" \
"v_mfma_f32_16x16x32_bf16 v[52:55], a[192:195], a[240:243], v[52:55]\n" \
"buffer_load_dwordx4 v22, s[64:67], s84 offen offset:0 lds\n" \
"v_mfma_f32_16x16x32_bf16 v[56:59], a[128:131], a[180:183], 0\n" \
"ds_read_b64_tr_b16 a[160:161], v32 offset:32768\n" \
"v_or_b32_e32 v199, 0x22000, v212\n" \
"v_mfma_f32_16x16x32_bf16 v[56:59], a[192:195], a[244:247], v[56:59]\n" \
"s_movk_i32 s84, 0x1d00\n" \
"v_mfma_f32_16x16x32_bf16 v[60:63], a[128:131], a[184:187], 0\n" \
"buffer_load_dwordx4 v23, s[64:67], s84 offen offset:1024 lds\n" \
"v_mfma_f32_16x16x32_bf16 v[60:63], a[192:195], a[248:251], v[60:63]\n" \
"ds_read_b64_tr_b16 a[162:163], v32 offset:33792\n" \
"s_add_i32 s35, s39, s7\n" \
"v_mfma_f32_16x16x32_bf16 v[64:67], a[128:131], a[188:191], 0\n" \
"ds_read_b64_tr_b16 a[224:225], v32 offset:40960\n" \
"s_add_i32 s54, s41, s5\n" \
"v_mfma_f32_16x16x32_bf16 v[64:67], a[192:195], a[252:255], v[64:67]\n" \
"ds_read_b128 a[128:131], v28 offset:49152\n" \
"s_add_i32 s55, s55, 0x80000\n" \
"v_mfma_f32_16x16x32_bf16 v[80:83], a[132:135], a[188:191], 0\n" \
"ds_read_b128 a[192:195], v29 offset:49152\n" \
"v_mov_b32_e32 v215, 0xffff\n" \
"v_mfma_f32_16x16x32_bf16 v[80:83], a[196:199], a[252:255], v[80:83]\n" \
"s_movk_i32 s84, 0x3900\n" \
"v_mfma_f32_16x16x32_bf16 v[76:79], a[132:135], a[184:187], 0\n" \
"buffer_load_dwordx4 v22, s[64:67], s84 offen offset:2048 lds\n" \
"v_mfma_f32_16x16x32_bf16 v[76:79], a[196:199], a[248:251], v[76:79]\n" \
"s_movk_i32 s84, 0x5500\n" \
"v_mfma_f32_16x16x32_bf16 v[72:75], a[132:135], a[180:183], 0\n" \
"buffer_load_dwordx4 v23, s[64:67], s84 offen offset:3072 lds\n" \
"v_mfma_f32_16x16x32_bf16 v[72:75], a[196:199], a[244:247], v[72:75]\n" \
"s_add_u32 m0, s31, 0x0\n" \
"s_mov_b32 s84, 0x100000\n" \
"v_mfma_f32_16x16x32_bf16 v[68:71], a[132:135], a[176:179], 0\n" \
"buffer_load_dwordx4 v26, s[68:71], s84 offen offset:0 lds\n" \
"v_mfma_f32_16x16x32_bf16 v[68:71], a[196:199], a[240:243], v[68:71]\n" \
"ds_read_b128 a[132:135], v28 offset:51200\n" \
"s_and_b32 s4, s3, 1\n" \
"v_mfma_f32_16x16x32_bf16 v[84:87], a[136:139], a[176:179], 0\n" \
"ds_read_b128 a[196:199], v29 offset:51200\n" \
"s_lshl_b32 s5, s4, 4\n" \
"v_mfma_f32_16x16x32_bf16 v[84:87], a[200:203], a[240:243], v[84:87]\n" \
"s_addk_i32 s84, 0x7c00\n" \
"v_mfma_f32_16x16x32_bf16 v[88:91], a[136:139], a[180:183], 0\n" \
"buffer_load_dwordx4 v26, s[68:71], s84 offen offset:1024 lds\n" \
"v_mfma_f32_16x16x32_bf16 v[88:91], a[200:203], a[244:247], v[88:91]\n" \
"ds_read_b64_tr_b16 a[226:227], v32 offset:41984\n" \
"v_lshrrev_b32_e32 v212, 4, v20\n" \
"v_mfma_f32_16x16x32_bf16 v[92:95], a[136:139], a[184:187], 0\n" \
"s_addk_i32 s84, 0x7c00\n" \
"v_mfma_f32_16x16x32_bf16 v[92:95], a[200:203], a[248:251], v[92:95]\n" \
"buffer_load_dwordx4 v27, s[68:71], s84 offen offset:2048 lds\n" \
"v_mfma_f32_16x16x32_bf16 v[96:99], a[136:139], a[188:191], 0\n" \
"ds_read_b64_tr_b16 a[164:165], v33 offset:32768\n" \
"v_lshrrev_b32_e32 v218, 6, v20\n" \
"v_mfma_f32_16x16x32_bf16 v[96:99], a[200:203], a[252:255], v[96:99]\n" \
"ds_read_b128 a[136:139], v28 offset:53248\n" \
"v_and_b32_e64 v219, 12, v218\n" \
"v_mfma_f32_16x16x32_bf16 v[112:115], a[140:143], a[188:191], 0\n" \
"ds_read_b128 a[200:203], v29 offset:53248\n" \
"v_and_or_b32 v214, v212, 15, s5\n" \
"v_mfma_f32_16x16x32_bf16 v[112:115], a[204:207], a[252:255], v[112:115]\n" \
"s_addk_i32 s84, 0x7c00\n" \
"v_mfma_f32_16x16x32_bf16 v[108:111], a[140:143], a[184:187], 0\n" \
"buffer_load_dwordx4 v27, s[68:71], s84 offen offset:3072 lds\n" \
"v_mfma_f32_16x16x32_bf16 v[108:111], a[204:207], a[248:251], v[108:111]\n" \
"s_add_u32 m0, s30, 0x1000\n" \
"s_mov_b32 s84, 0x8100\n" \
"v_mfma_f32_16x16x32_bf16 v[104:107], a[140:143], a[180:183], 0\n" \
"buffer_load_dwordx4 v22, s[64:67], s84 offen offset:0 lds\n" \
"v_mfma_f32_16x16x32_bf16 v[104:107], a[204:207], a[244:247], v[104:107]\n" \
"ds_read_b64_tr_b16 a[166:167], v33 offset:33792\n" \
"v_cmp_gt_u32_e32 vcc, v219, v214\n" \
"v_mfma_f32_16x16x32_bf16 v[100:103], a[140:143], a[176:179], 0\n" \
"ds_read_b64_tr_b16 a[228:229], v33 offset:40960\n" \
"v_mov_b32_e32 v220, 0xffff0000\n" \
"v_mfma_f32_16x16x32_bf16 v[100:103], a[204:207], a[240:243], v[100:103]\n" \
"ds_read_b128 a[140:143], v28 offset:55296\n" \
"s_lshl_b32 s5, s2, 2\n" \
"v_mfma_f32_16x16x32_bf16 v[116:119], a[144:147], a[176:179], 0\n" \
"ds_read_b128 a[204:207], v29 offset:55296\n" \
"v_cndmask_b32_e64 v217, v215, 0, vcc\n" \
"v_mfma_f32_16x16x32_bf16 v[116:119], a[208:211], a[240:243], v[116:119]\n" \
"s_addk_i32 s84, 0x1c00\n" \
"v_mfma_f32_16x16x32_bf16 v[120:123], a[144:147], a[180:183], 0\n" \
"buffer_load_dwordx4 v23, s[64:67], s84 offen offset:1024 lds\n" \
"v_mfma_f32_16x16x32_bf16 v[120:123], a[208:211], a[244:247], v[120:123]\n" \
"s_addk_i32 s84, 0x1c00\n" \
"v_mfma_f32_16x16x32_bf16 v[124:127], a[144:147], a[184:187], 0\n" \
"buffer_load_dwordx4 v22, s[64:67], s84 offen offset:2048 lds\n" \
"v_mfma_f32_16x16x32_bf16 v[124:127], a[208:211], a[248:251], v[124:127]\n" \
"s_addk_i32 s84, 0x1c00\n" \
"v_mfma_f32_16x16x32_bf16 v[128:131], a[144:147], a[188:191], 0\n" \
"buffer_load_dwordx4 v23, s[64:67], s84 offen offset:3072 lds\n" \
"v_mfma_f32_16x16x32_bf16 v[128:131], a[208:211], a[252:255], v[128:131]\n" \
"ds_read_b128 a[144:147], v28 offset:32768\n" \
"v_cmp_lt_u32_e64 vcc, v219, v214\n" \
"v_mfma_f32_16x16x32_bf16 v[144:147], a[148:151], a[188:191], 0\n" \
"ds_read_b128 a[208:211], v29 offset:32768\n" \
"v_mov_b32_e64 v213, s23\n" \
"v_mfma_f32_16x16x32_bf16 v[144:147], a[212:215], a[252:255], v[144:147]\n" \
"s_add_u32 m0, s31, 0x4000\n" \
".fill (L_pb + 44768 - .) / 4, 4, 0xBF800000\n" \
"L_pp11:\n" \
"s_cbranch_execz L_pret\n" \
"s_mov_b32 s84, 0x100000\n" \
"v_mfma_f32_16x16x32_bf16 v[140:143], a[148:151], a[184:187], 0\n" \
"buffer_load_dwordx4 v26, s[72:75], s84 offen offset:0 lds\n" \
"v_mfma_f32_16x16x32_bf16 v[140:143], a[212:215], a[248:251], v[140:143]\n" \
"ds_read_b64_tr_b16 a[230:231], v33 offset:41984\n" \
"s_or_b32 s5, s5, s3\n" \
"v_mfma_f32_16x16x32_bf16 v[136:139], a[148:151], a[180:183], 0\n" \
"s_addk_i32 s84, 0x7c00\n" \
"v_mfma_f32_16x16x32_bf16 v[136:139], a[212:215], a[244:247], v[136:139]\n" \
"buffer_load_dwordx4 v26, s[72:75], s84 offen offset:1024 lds\n" \
"v_mfma_f32_16x16x32_bf16 v[132:135], a[148:151], a[176:179], 0\n" \
"ds_read_b64_tr_b16 a[168:169], v34 offset:32768\n" \
"v_cndmask_b32_e32 v221, 0, v220, vcc\n" \
"v_mfma_f32_16x16x32_bf16 v[132:135], a[212:215], a[240:243], v[132:135]\n" \
"ds_read_b128 a[148:151], v28 offset:34816\n" \
"v_or_b32_e64 v12, v217, v221\n" \
"v_mfma_f32_16x16x32_bf16 v[148:151], a[152:155], a[176:179], 0\n" \
"ds_read_b128 a[212:215], v29 offset:34816\n" \
"v_or_b32_e64 v217, 2, v219\n" \
"v_mfma_f32_16x16x32_bf16 v[148:151], a[216:219], a[240:243], v[148:151]\n" \
"ds_read_b64_tr_b16 a[170:171], v34 offset:33792\n" \
"v_cmp_gt_u32_e64 vcc, v217, v214\n" \
"v_mfma_f32_16x16x32_bf16 v[152:155], a[152:155], a[180:183], 0\n" \
"ds_read_b64_tr_b16 a[232:233], v34 offset:40960\n" \
"v_mov_b32_e64 v216, s25\n" \
"v_mfma_f32_16x16x32_bf16 v[152:155], a[216:219], a[244:247], v[152:155]\n" \
"ds_read_b64_tr_b16 a[234:235], v34 offset:41984\n" \
"s_movk_i32 s40, 0xf400\n" \
"v_mfma_f32_16x16x32_bf16 v[156:159], a[152:155], a[184:187], 0\n" \
"ds_read_b64_tr_b16 a[172:173], v35 offset:32768\n" \
"v_cndmask_b32_e64 v221, v215, 0, vcc\n" \
"v_mfma_f32_16x16x32_bf16 v[156:159], a[216:219], a[248:251], v[156:159]\n" \
"ds_read_b64_tr_b16 a[174:175], v35 offset:33792\n" \
"v_cmp_lt_u32_e32 vcc, v217, v214\n" \
"v_mfma_f32_16x16x32_bf16 v[160:163], a[152:155], a[188:191], 0\n" \
"ds_read_b64_tr_b16 a[236:237], v35 offset:40960\n" \
"s_lshl_b32 s4, s4, 6\n" \
"v_mfma_f32_16x16x32_bf16 v[160:163], a[216:219], a[252:255], v[160:163]\n" \
"ds_read_b128 a[152:155], v28 offset:36864\n" \
"v_cndmask_b32_e32 v217, 0, v220, vcc\n" \
"v_mfma_f32_16x16x32_bf16 v[176:179], a[156:159], a[188:191], 0\n" \
"ds_read_b128 a[216:219], v29 offset:36864\n" \
"v_or_b32_e64 v13, v221, v217\n" \
"v_mfma_f32_16x16x32_bf16 v[176:179], a[220:223], a[252:255], v[176:179]\n" \
"ds_read_b64_tr_b16 a[238:239], v35 offset:41984\n" \
"v_or_b32_e64 v217, 16, v219\n" \
"v_mfma_f32_16x16x32_bf16 v[172:175], a[156:159], a[184:187], 0\n" \
"v_cmp_gt_u32_e32 vcc, v217, v214\n" \
"s_nop 1\n" \
"v_cndmask_b32_e64 v221, v215, 0, vcc\n" \
"v_mfma_f32_16x16x32_bf16 v[172:175], a[220:223], a[248:251], v[172:175]\n" \
"v_cmp_lt_u32_e32 vcc, v217, v214\n" \
"s_nop 1\n" \
"v_cndmask_b32_e64 v217, 0, v220, vcc\n" \
"v_mfma_f32_16x16x32_bf16 v[168:171], a[156:159], a[180:183], 0\n" \
"v_or_b32_e32 v14, v221, v217\n" \
"v_or_b32_e32 v217, 18, v219\n" \
"v_mfma_f32_16x16x32_bf16 v[168:171], a[220:223], a[244:247], v[168:171]\n" \
"v_cmp_gt_u32_e32 vcc, v217, v214\n" \
"s_nop 1\n" \
"v_cndmask_b32_e64 v215, v215, 0, vcc\n" \
"v_mfma_f32_16x16x32_bf16 v[164:167], a[156:159], a[176:179], 0\n" \
"v_cmp_lt_u32_e32 vcc, v217, v214\n" \
"s_nop 1\n" \
"v_cndmask_b32_e64 v214, 0, v220, vcc\n" \
"v_mfma_f32_16x16x32_bf16 v[164:167], a[220:223], a[240:243], v[164:167]\n" \
"ds_read_b128 a[156:159], v28 offset:38912\n" \
"ds_read_b128 a[220:223], v29 offset:38912\n" \
"s_waitcnt lgkmcnt(15)\n" \
"v_mfma_f32_16x16x32_bf16 a[0:3], a[128:131], a[160:163], a[0:3]\n" \
"s_addk_i32 s84, 0x7c00\n" \
"v_mfma_f32_16x16x32_bf16 a[0:3], a[192:195], a[224:227], a[0:3]\n" \
"buffer_load_dwordx4 v27, s[72:75], s84 offen offset:2048 lds\n" \
"v_mfma_f32_16x16x32_bf16 a[4:7], a[128:131], a[164:167], a[4:7]\n" \
"ds_read_b64_tr_b16 a[176:177], v36 offset:32768\n" \
"v_or_b32_e32 v15, v215, v214\n" \
"s_waitcnt lgkmcnt(15)\n" \
"v_mfma_f32_16x16x32_bf16 a[4:7], a[192:195], a[228:231], a[4:7]\n" \
"s_addk_i32 s84, 0x7c00\n" \
"s_waitcnt lgkmcnt(11)\n" \
"v_mfma_f32_16x16x32_bf16 a[8:11], a[128:131], a[168:171], a[8:11]\n" \
"buffer_load_dwordx4 v27, s[72:75], s84 offen offset:3072 lds\n" \
"s_waitcnt lgkmcnt(9)\n" \
"v_mfma_f32_16x16x32_bf16 a[8:11], a[192:195], a[232:235], a[8:11]\n" \
"ds_read_b64_tr_b16 a[178:179], v36 offset:33792\n" \
"v_mad_u64_u32 v[214:215], s[6:7], s5, 42, v[212:213]\n" \
"s_waitcnt lgkmcnt(8)\n" \
"v_mfma_f32_16x16x32_bf16 a[12:15], a[128:131], a[172:175], a[12:15]\n" \
"ds_read_b64_tr_b16 a[240:241], v36 offset:40960\n" \
"s_movk_i32 s5, 0x1440\n" \
"s_waitcnt lgkmcnt(5)\n" \
"v_mfma_f32_16x16x32_bf16 a[12:15], a[192:195], a[236:239], a[12:15]\n" \
"v_cmp_gt_u32_e64 vcc, s5, v214\n" \
"v_min_u32_e32 v217, 0xa43f, v214\n" \
"v_mfma_f32_16x16x32_bf16 a[28:31], a[132:135], a[172:175], a[28:31]\n" \
"v_mov_b32_e32 v214, s24\n" \
"v_cndmask_b32_e32 v215, v216, v213, vcc\n" \
"v_mfma_f32_16x16x32_bf16 a[28:31], a[196:199], a[236:239], a[28:31]\n" \
"ds_read_b64_tr_b16 a[242:243], v36 offset:41984\n" \
"v_mov_b32_e64 v213, s22\n" \
"v_mfma_f32_16x16x32_bf16 a[24:27], a[132:135], a[168:171], a[24:27]\n" \
"s_movk_i32 s6, 0xffee\n" \
"v_cndmask_b32_e32 v214, v214, v213, vcc\n" \
"v_mfma_f32_16x16x32_bf16 a[24:27], a[196:199], a[232:235], a[24:27]\n" \
"v_add_u32_e32 v213, 0xffffebc0, v217\n" \
"s_lshl_b32 s5, s2, 5\n" \
"v_mfma_f32_16x16x32_bf16 a[20:23], a[132:135], a[164:167], a[20:23]\n" \
"v_cndmask_b32_e32 v213, v213, v217, vcc\n" \
"v_lshlrev_b32_e64 v216, 7, v213\n" \
"v_mfma_f32_16x16x32_bf16 a[20:23], a[196:199], a[228:231], a[20:23]\n" \
"ds_read_b64_tr_b16 a[180:181], v37 offset:32768\n" \
"v_mul_u32_u24_e32 v213, 0xe39, v212\n" \
"v_mfma_f32_16x16x32_bf16 a[16:19], a[132:135], a[160:163], a[16:19]\n" \
"v_mov_b32_e32 v217, 0\n" \
"v_lshrrev_b32_e32 v213, 16, v213\n" \
"v_mfma_f32_16x16x32_bf16 a[16:19], a[196:199], a[224:227], a[16:19]\n" \
"v_lshl_add_u64 v[16:17], v[214:215], 0, v[216:217]\n" \
"v_mad_i32_i24 v214, v213, s6, v212\n" \
"v_mfma_f32_16x16x32_bf16 a[32:35], a[136:139], a[160:163], a[32:35]\n" \
"s_mov_b32 m0, s32\n" \
"ds_read_b64_tr_b16 a[182:183], v37 offset:33792\n" \
"v_mfma_f32_16x16x32_bf16 a[32:35], a[200:203], a[224:227], a[32:35]\n" \
"s_mov_b32 exec_lo, 0x0\n" \
"s_mov_b32 exec_hi, 0xff\n" \
"global_load_lds_dword v[184:185], off\n" \
"s_mov_b64 exec, -1\n" \
"v_mfma_f32_16x16x32_bf16 a[36:39], a[136:139], a[164:167], a[36:39]\n" \
"s_add_i32 s5, s5, 32\n" \
"v_lshlrev_b32_e64 v215, 4, v214\n" \
"v_mfma_f32_16x16x32_bf16 a[36:39], a[200:203], a[228:231], a[36:39]\n" \
"ds_read_b64_tr_b16 a[244:245], v37 offset:40960\n" \
"v_add_u32_e64 v216, s5, v215\n" \
"v_mfma_f32_16x16x32_bf16 a[40:43], a[136:139], a[168:171], a[40:43]\n" \
"v_and_b32_e32 v216, 0xf0, v216\n" \
"v_cmp_gt_u32_e64 vcc, 16, v214\n" \
"v_mfma_f32_16x16x32_bf16 a[40:43], a[200:203], a[232:235], a[40:43]\n" \
"s_mov_b32 m0, s32\n" \
"s_movk_i32 s7, 0xf800\n" \
"v_mfma_f32_16x16x32_bf16 a[44:47], a[136:139], a[172:175], a[44:47]\n" \
"s_mov_b32 exec_lo, 0xffff0000\n" \
"s_mov_b32 exec_hi, 0x0\n" \
"global_load_lds_dword v[186:187], off\n" \
"s_mov_b64 exec, -1\n" \
"v_mfma_f32_16x16x32_bf16 a[44:47], a[200:203], a[236:239], a[44:47]\n" \
"ds_read_b64_tr_b16 a[246:247], v37 offset:41984\n" \
"v_cndmask_b32_e64 v214, v215, v216, vcc\n" \
"v_mfma_f32_16x16x32_bf16 a[60:63], a[140:143], a[172:175], a[60:63]\n" \
"v_or_b32_e64 v215, 64, v212\n" \
"v_mul_u32_u24_e32 v216, 0xe39, v215\n" \
"v_mfma_f32_16x16x32_bf16 a[60:63], a[204:207], a[236:239], a[60:63]\n" \
"v_lshrrev_b32_e64 v216, 16, v216\n" \
"v_mad_i32_i24 v215, v216, s6, v215\n" \
"v_mfma_f32_16x16x32_bf16 a[56:59], a[140:143], a[168:171], a[56:59]\n" \
"ds_read_b64_tr_b16 a[184:185], v38 offset:32768\n" \
"v_lshlrev_b32_e64 v217, 4, v215\n" \
"v_mfma_f32_16x16x32_bf16 a[56:59], a[204:207], a[232:235], a[56:59]\n" \
"v_add_u32_e64 v220, s5, v217\n" \
"v_and_b32_e32 v220, 0xf0, v220\n" \
"v_mfma_f32_16x16x32_bf16 a[52:55], a[140:143], a[164:167], a[52:55]\n" \
"v_cmp_gt_u32_e32 vcc, 16, v215\n" \
"s_nop 1\n" \
"v_cndmask_b32_e64 v215, v217, v220, vcc\n" \
"v_mfma_f32_16x16x32_bf16 a[52:55], a[204:207], a[228:231], a[52:55]\n" \
"ds_read_b64_tr_b16 a[186:187], v38 offset:33792\n" \
"v_or_b32_e32 v217, 0x80, v212\n" \
"v_mfma_f32_16x16x32_bf16 a[48:51], a[140:143], a[160:163], a[48:51]\n" \
"v_mul_u32_u24_e32 v220, 0xe39, v217\n" \
"v_lshrrev_b32_e64 v220, 16, v220\n" \
"v_mfma_f32_16x16x32_bf16 a[48:51], a[204:207], a[224:227], a[48:51]\n" \
"v_mad_i32_i24 v217, v220, s6, v217\n" \
"v_lshlrev_b32_e64 v221, 4, v217\n" \
"v_mfma_f32_16x16x32_bf16 a[64:67], a[144:147], a[160:163], a[64:67]\n" \
"ds_read_b64_tr_b16 a[248:249], v38 offset:40960\n" \
"v_add_u32_e64 v222, s5, v221\n" \
"v_mfma_f32_16x16x32_bf16 a[64:67], a[208:211], a[224:227], a[64:67]\n" \
"v_and_b32_e32 v222, 0xf0, v222\n" \
"v_cmp_gt_u32_e64 vcc, 16, v217\n" \
"v_mfma_f32_16x16x32_bf16 a[68:71], a[144:147], a[164:167], a[68:71]\n" \
"s_nop 0\n" \
"v_cndmask_b32_e32 v217, v221, v222, vcc\n" \
"v_or_b32_e32 v221, 0xc0, v212\n" \
"v_mfma_f32_16x16x32_bf16 a[68:71], a[208:211], a[228:231], a[68:71]\n" \
"ds_read_b64_tr_b16 a[250:251], v38 offset:41984\n" \
"v_mul_u32_u24_e32 v222, 0xe39, v221\n" \
"v_mfma_f32_16x16x32_bf16 a[72:75], a[144:147], a[168:171], a[72:75]\n" \
"v_lshrrev_b32_e64 v222, 16, v222\n" \
"v_mad_i32_i24 v221, v222, s6, v221\n" \
"v_mfma_f32_16x16x32_bf16 a[72:75], a[208:211], a[232:235], a[72:75]\n" \
"v_lshlrev_b32_e32 v223, 4, v221\n" \
"v_add_u32_e32 v224, s5, v223\n" \
"v_mfma_f32_16x16x32_bf16 a[76:79], a[144:147], a[172:175], a[76:79]\n" \
"v_and_b32_e32 v224, 0xf0, v224\n" \
"v_cmp_gt_u32_e64 vcc, 16, v221\n" \
"v_mfma_f32_16x16x32_bf16 a[76:79], a[208:211], a[236:239], a[76:79]\n" \
"ds_read_b64_tr_b16 a[188:189], v39 offset:32768\n" \
"v_cndmask_b32_e64 v221, v223, v224, vcc\n" \
"v_mfma_f32_16x16x32_bf16 a[92:95], a[148:151], a[172:175], a[92:95]\n" \
"v_or_b32_e32 v223, 0x100, v212\n" \
"v_mul_u32_u24_e32 v224, 0xe39, v223\n" \
"v_mfma_f32_16x16x32_bf16 a[92:95], a[212:215], a[236:239], a[92:95]\n" \
"v_lshrrev_b32_e64 v224, 16, v224\n" \
"v_mad_i32_i24 v223, v224, s6, v223\n" \
"v_mfma_f32_16x16x32_bf16 a[88:91], a[148:151], a[168:171], a[88:91]\n" \
"ds_read_b64_tr_b16 a[190:191], v39 offset:33792\n" \
"v_lshlrev_b32_e64 v225, 4, v223\n" \
"v_mfma_f32_16x16x32_bf16 a[88:91], a[212:215], a[232:235], a[88:91]\n" \
"v_add_u32_e64 v226, s5, v225\n" \
"v_and_b32_e32 v226, 0xf0, v226\n" \
"v_mfma_f32_16x16x32_bf16 a[84:87], a[148:151], a[164:167], a[84:87]\n" \
"v_cmp_gt_u32_e32 vcc, 16, v223\n" \
"s_nop 1\n" \
"v_cndmask_b32_e64 v223, v225, v226, vcc\n" \
"v_mfma_f32_16x16x32_bf16 a[84:87], a[212:215], a[228:231], a[84:87]\n" \
"ds_read_b64_tr_b16 a[252:253], v39 offset:40960\n" \
"v_or_b32_e32 v225, 0x140, v212\n" \
"v_mfma_f32_16x16x32_bf16 a[80:83], a[148:151], a[160:163], a[80:83]\n" \
"v_mul_u32_u24_e32 v226, 0xe39, v225\n" \
"v_lshrrev_b32_e64 v226, 16, v226\n" \
"v_mfma_f32_16x16x32_bf16 a[80:83], a[212:215], a[224:227], a[80:83]\n" \
"v_mad_i32_i24 v225, v226, s6, v225\n" \
"v_lshlrev_b32_e64 v227, 4, v225\n" \
"v_mfma_f32_16x16x32_bf16 a[96:99], a[152:155], a[160:163], a[96:99]\n" \
"ds_read_b64_tr_b16 a[254:255], v39 offset:41984\n" \
"v_add_u32_e64 v228, s5, v227\n" \
"v_mfma_f32_16x16x32_bf16 a[96:99], a[216:219], a[224:227], a[96:99]\n" \
"v_and_b32_e32 v228, 0xf0, v228\n" \
"v_cmp_gt_u32_e64 vcc, 16, v225\n" \
"v_mfma_f32_16x16x32_bf16 a[100:103], a[152:155], a[164:167], a[100:103]\n" \
"s_nop 0\n" \
"v_cndmask_b32_e32 v225, v227, v228, vcc\n" \
"v_or_b32_e32 v227, 0x180, v212\n" \
"v_mfma_f32_16x16x32_bf16 a[100:103], a[216:219], a[228:231], a[100:103]\n" \
"v_mul_u32_u24_e32 v228, 0xe39, v227\n" \
"v_lshrrev_b32_e64 v228, 16, v228\n" \
"v_mfma_f32_16x16x32_bf16 a[104:107], a[152:155], a[168:171], a[104:107]\n" \
"v_mad_i32_i24 v227, v228, s6, v227\n" \
"v_lshlrev_b32_e64 v229, 4, v227\n" \
"v_mfma_f32_16x16x32_bf16 a[104:107], a[216:219], a[232:235], a[104:107]\n" \
"v_add_u32_e64 v230, s5, v229\n" \
"v_and_b32_e32 v230, 0xf0, v230\n" \
"v_mfma_f32_16x16x32_bf16 a[108:111], a[152:155], a[172:175], a[108:111]\n" \
"v_cmp_gt_u32_e32 vcc, 16, v227\n" \
"s_nop 1\n" \
"v_cndmask_b32_e64 v227, v229, v230, vcc\n" \
"v_mfma_f32_16x16x32_bf16 a[108:111], a[216:219], a[236:239], a[108:111]\n" \
"v_or_b32_e32 v229, 0x1c0, v212\n" \
"v_mul_u32_u24_e32 v230, 0xe39, v229\n" \
"s_waitcnt lgkmcnt(15)\n" \
"v_mfma_f32_16x16x32_bf16 a[124:127], a[156:159], a[172:175], a[124:127]\n" \
"v_lshrrev_b32_e32 v230, 16, v230\n" \
"v_mad_i32_i24 v229, v230, s6, v229\n" \
"v_mfma_f32_16x16x32_bf16 a[124:127], a[220:223], a[236:239], a[124:127]\n" \
"v_lshlrev_b32_e32 v231, 4, v229\n" \
"v_add_u32_e32 v232, s5, v231\n" \
"v_mfma_f32_16x16x32_bf16 a[120:123], a[156:159], a[168:171], a[120:123]\n" \
"v_and_b32_e32 v232, 0xf0, v232\n" \
"v_cmp_gt_u32_e64 vcc, 16, v229\n" \
"v_mfma_f32_16x16x32_bf16 a[120:123], a[220:223], a[232:235], a[120:123]\n" \
"s_nop 0\n" \
"v_cndmask_b32_e32 v229, v231, v232, vcc\n" \
"v_or_b32_e32 v231, 0x200, v212\n" \
"v_mfma_f32_16x16x32_bf16 a[116:119], a[156:159], a[164:167], a[116:119]\n" \
"v_mul_u32_u24_e32 v232, 0xe39, v231\n" \
"v_lshrrev_b32_e64 v232, 16, v232\n" \
"v_mfma_f32_16x16x32_bf16 a[116:119], a[220:223], a[228:231], a[116:119]\n" \
"v_mad_i32_i24 v231, v232, s6, v231\n" \
"v_lshlrev_b32_e64 v233, 4, v231\n" \
"v_mfma_f32_16x16x32_bf16 a[112:115], a[156:159], a[160:163], a[112:115]\n" \
"v_add_u32_e32 v234, s5, v233\n" \
"s_movk_i32 s5, 0x480\n" \
"v_mfma_f32_16x16x32_bf16 a[112:115], a[220:223], a[224:227], a[112:115]\n" \
"s_waitcnt vmcnt(20)\n" \
"v_readfirstlane_b32 s62, v180\n" \
"s_nop 3\n" \
"s_add_u32 s62, s62, 0x9E3779B1\n" \
"s_waitcnt vmcnt(2) lgkmcnt(0)\n" \
"s_barrier\n" \
"v_mfma_f32_16x16x32_bf16 v[52:55], a[128:131], a[176:179], v[52:55]\n" \
"s_add_u32 m0, s30, 0x8000\n" \
"s_movk_i32 s84, 0x180\n" \
"v_mfma_f32_16x16x32_bf16 v[52:55], a[192:195], a[240:243], v[52:55]\n" \
"buffer_load_dwordx4 v22, s[64:67], s84 offen offset:0 lds\n" \
"v_mfma_f32_16x16x32_bf16 v[56:59], a[128:131], a[180:183], v[56:59]\n" \
"ds_read_b64_tr_b16 a[160:161], v32 offset:0\n" \
"v_mad_u32_u24 v0, v213, s5, v214\n" \
"v_mfma_f32_16x16x32_bf16 v[56:59], a[192:195], a[244:247], v[56:59]\n" \
"s_movk_i32 s84, 0x1d80\n" \
"v_mfma_f32_16x16x32_bf16 v[60:63], a[128:131], a[184:187], v[60:63]\n" \
"buffer_load_dwordx4 v23, s[64:67], s84 offen offset:1024 lds\n" \
"v_mfma_f32_16x16x32_bf16 v[60:63], a[192:195], a[248:251], v[60:63]\n" \
"ds_read_b64_tr_b16 a[162:163], v32 offset:1024\n" \
"v_mul_u32_u24_e32 v213, 0x480, v216\n" \
"v_mfma_f32_16x16x32_bf16 v[64:67], a[128:131], a[188:191], v[64:67]\n" \
"ds_read_b64_tr_b16 a[224:225], v32 offset:8192\n" \
"s_movk_i32 s6, 0xfc00\n" \
"v_mfma_f32_16x16x32_bf16 v[64:67], a[192:195], a[252:255], v[64:67]\n" \
"ds_read_b128 a[128:131], v28 offset:16384\n" \
"v_add3_u32 v1, v213, v215, s6\n" \
"v_mfma_f32_16x16x32_bf16 v[80:83], a[132:135], a[188:191], v[80:83]\n" \
"ds_read_b128 a[192:195], v29 offset:16384\n" \
"v_mul_u32_u24_e32 v213, 0x480, v220\n" \
"v_mfma_f32_16x16x32_bf16 v[80:83], a[196:199], a[252:255], v[80:83]\n" \
"s_movk_i32 s84, 0x3980\n" \
"v_mfma_f32_16x16x32_bf16 v[76:79], a[132:135], a[184:187], v[76:79]\n" \
"buffer_load_dwordx4 v22, s[64:67], s84 offen offset:2048 lds\n" \
"v_mfma_f32_16x16x32_bf16 v[76:79], a[196:199], a[248:251], v[76:79]\n" \
"s_movk_i32 s84, 0x5580\n" \
"v_mfma_f32_16x16x32_bf16 v[72:75], a[132:135], a[180:183], v[72:75]\n" \
"buffer_load_dwordx4 v23, s[64:67], s84 offen offset:3072 lds\n" \
"v_mfma_f32_16x16x32_bf16 v[72:75], a[196:199], a[244:247], v[72:75]\n" \
"s_add_u32 m0, s31, 0x8000\n" \
"s_mov_b32 s84, 0x180000\n" \
"v_mfma_f32_16x16x32_bf16 v[68:71], a[132:135], a[176:179], v[68:71]\n" \
"buffer_load_dwordx4 v26, s[68:71], s84 offen offset:0 lds\n" \
"v_mfma_f32_16x16x32_bf16 v[68:71], a[196:199], a[240:243], v[68:71]\n" \
"ds_read_b128 a[132:135], v28 offset:18432\n" \
"v_add3_u32 v2, v213, v217, s7\n" \
"v_mfma_f32_16x16x32_bf16 v[84:87], a[136:139], a[176:179], v[84:87]\n" \
"ds_read_b128 a[196:199], v29 offset:18432\n" \
"v_mul_u32_u24_e32 v213, 0x480, v222\n" \
"v_mfma_f32_16x16x32_bf16 v[84:87], a[200:203], a[240:243], v[84:87]\n" \
"s_addk_i32 s84, 0x7c00\n" \
"v_mfma_f32_16x16x32_bf16 v[88:91], a[136:139], a[180:183], v[88:91]\n" \
"buffer_load_dwordx4 v26, s[68:71], s84 offen offset:1024 lds\n" \
"v_mfma_f32_16x16x32_bf16 v[88:91], a[200:203], a[244:247], v[88:91]\n" \
"ds_read_b64_tr_b16 a[226:227], v32 offset:9216\n" \
"v_add3_u32 v3, v213, v221, s40\n" \
"v_mfma_f32_16x16x32_bf16 v[92:95], a[136:139], a[184:187], v[92:95]\n" \
"s_addk_i32 s84, 0x7c00\n" \
"v_mfma_f32_16x16x32_bf16 v[92:95], a[200:203], a[248:251], v[92:95]\n" \
"buffer_load_dwordx4 v27, s[68:71], s84 offen offset:2048 lds\n" \
"v_mfma_f32_16x16x32_bf16 v[96:99], a[136:139], a[188:191], v[96:99]\n" \
"ds_read_b64_tr_b16 a[164:165], v33 offset:0\n" \
"v_mul_u32_u24_e32 v213, 0x480, v226\n" \
"v_mfma_f32_16x16x32_bf16 v[96:99], a[200:203], a[252:255], v[96:99]\n" \
"ds_read_b128 a[136:139], v28 offset:20480\n" \
"v_add3_u32 v5, v213, v225, s6\n" \
"v_mfma_f32_16x16x32_bf16 v[112:115], a[140:143], a[188:191], v[112:115]\n" \
"ds_read_b128 a[200:203], v29 offset:20480\n" \
"v_mul_u32_u24_e32 v213, 0x480, v228\n" \
"v_mfma_f32_16x16x32_bf16 v[112:115], a[204:207], a[252:255], v[112:115]\n" \
"s_addk_i32 s84, 0x7c00\n" \
"s_nop 0\n" \
"v_mfma_f32_16x16x32_bf16 v[108:111], a[140:143], a[184:187], v[108:111]\n" \
"buffer_load_dwordx4 v27, s[68:71], s84 offen offset:3072 lds\n" \
"v_mfma_f32_16x16x32_bf16 v[108:111], a[204:207], a[248:251], v[108:111]\n" \
"s_add_u32 m0, s30, 0x9000\n" \
"s_mov_b32 s84, 0x8180\n" \
"v_mfma_f32_16x16x32_bf16 v[104:107], a[140:143], a[180:183], v[104:107]\n" \
"buffer_load_dwordx4 v22, s[64:67], s84 offen offset:0 lds\n" \
"v_mfma_f32_16x16x32_bf16 v[104:107], a[204:207], a[244:247], v[104:107]\n" \
"ds_read_b64_tr_b16 a[166:167], v33 offset:1024\n" \
"v_add3_u32 v6, v213, v227, s7\n" \
"v_mfma_f32_16x16x32_bf16 v[100:103], a[140:143], a[176:179], v[100:103]\n" \
"ds_read_b64_tr_b16 a[228:229], v33 offset:8192\n" \
"v_mul_u32_u24_e32 v213, 0x480, v230\n" \
"v_mfma_f32_16x16x32_bf16 v[100:103], a[204:207], a[240:243], v[100:103]\n" \
"ds_read_b128 a[140:143], v28 offset:22528\n" \
"v_add3_u32 v7, v213, v229, s40\n" \
"v_mfma_f32_16x16x32_bf16 v[116:119], a[144:147], a[176:179], v[116:119]\n" \
"ds_read_b128 a[204:207], v29 offset:22528\n" \
"s_lshl_b32 s7, s3, 3\n" \
"v_mfma_f32_16x16x32_bf16 v[116:119], a[208:211], a[240:243], v[116:119]\n" \
"s_addk_i32 s84, 0x1c00\n" \
"v_mfma_f32_16x16x32_bf16 v[120:123], a[144:147], a[180:183], v[120:123]\n" \
"buffer_load_dwordx4 v23, s[64:67], s84 offen offset:1024 lds\n" \
"v_mfma_f32_16x16x32_bf16 v[120:123], a[208:211], a[244:247], v[120:123]\n" \
"s_addk_i32 s84, 0x1c00\n" \
"v_mfma_f32_16x16x32_bf16 v[124:127], a[144:147], a[184:187], v[124:127]\n" \
"buffer_load_dwordx4 v22, s[64:67], s84 offen offset:2048 lds\n" \
"v_mfma_f32_16x16x32_bf16 v[124:127], a[208:211], a[248:251], v[124:127]\n" \
"s_addk_i32 s84, 0x1c00\n" \
"v_mfma_f32_16x16x32_bf16 v[128:131], a[144:147], a[188:191], v[128:131]\n" \
"buffer_load_dwordx4 v23, s[64:67], s84 offen offset:3072 lds\n" \
"v_mfma_f32_16x16x32_bf16 v[128:131], a[208:211], a[252:255], v[128:131]\n" \
"ds_read_b128 a[144:147], v28 offset:0\n" \
"s_lshl_b32 s40, s3, 1\n" \
"v_mfma_f32_16x16x32_bf16 v[144:147], a[148:151], a[188:191], v[144:147]\n" \
"ds_read_b128 a[208:211], v29 offset:0\n" \
"s_and_b32 s7, s7, 16\n" \
"v_mfma_f32_16x16x32_bf16 v[144:147], a[212:215], a[252:255], v[144:147]\n" \
"s_add_u32 m0, s31, 0xc000\n" \
"s_mov_b32 s84, 0x180000\n" \
"v_mfma_f32_16x16x32_bf16 v[140:143], a[148:151], a[184:187], v[140:143]\n" \
"buffer_load_dwordx4 v26, s[72:75], s84 offen offset:0 lds\n" \
"v_mfma_f32_16x16x32_bf16 v[140:143], a[212:215], a[248:251], v[140:143]\n" \
"ds_read_b64_tr_b16 a[230:231], v33 offset:9216\n" \
"s_and_b32 s40, s40, 2\n" \
"v_mfma_f32_16x16x32_bf16 v[136:139], a[148:151], a[180:183], v[136:139]\n" \
"s_addk_i32 s84, 0x7c00\n" \
"v_mfma_f32_16x16x32_bf16 v[136:139], a[212:215], a[244:247], v[136:139]\n" \
"buffer_load_dwordx4 v26, s[72:75], s84 offen offset:1024 lds\n" \
"v_mfma_f32_16x16x32_bf16 v[132:135], a[148:151], a[176:179], v[132:135]\n" \
"ds_read_b64_tr_b16 a[168:169], v34 offset:0\n" \
"s_and_b32 s6, s2, 7\n" \
"v_mfma_f32_16x16x32_bf16 v[132:135], a[212:215], a[240:243], v[132:135]\n" \
"ds_read_b128 a[148:151], v28 offset:2048\n" \
"v_bfe_u32 v213, v20, 7, 1\n" \
"v_mfma_f32_16x16x32_bf16 v[148:151], a[152:155], a[176:179], v[148:151]\n" \
"ds_read_b128 a[212:215], v29 offset:2048\n" \
"s_or_b32 s7, s40, s7\n" \
"v_mfma_f32_16x16x32_bf16 v[148:151], a[216:219], a[240:243], v[148:151]\n" \
"ds_read_b64_tr_b16 a[170:171], v34 offset:1024\n" \
"v_and_b32_e32 v234, 0xf0, v234\n" \
"v_mfma_f32_16x16x32_bf16 v[152:155], a[152:155], a[180:183], v[152:155]\n" \
"ds_read_b64_tr_b16 a[232:233], v34 offset:8192\n" \
"v_cmp_gt_u32_e64 vcc, 16, v231\n" \
"v_mfma_f32_16x16x32_bf16 v[152:155], a[216:219], a[244:247], v[152:155]\n" \
"ds_read_b64_tr_b16 a[234:235], v34 offset:9216\n" \
"v_or3_b32 v213, s7, v213, v219\n" \
"v_mfma_f32_16x16x32_bf16 v[156:159], a[152:155], a[184:187], v[156:159]\n" \
"ds_read_b64_tr_b16 a[172:173], v35 offset:0\n" \
"v_and_b32_e64 v214, 32, v20\n" \
"v_mfma_f32_16x16x32_bf16 v[156:159], a[216:219], a[248:251], v[156:159]\n" \
"ds_read_b64_tr_b16 a[174:175], v35 offset:1024\n" \
"s_lshl_b32 s6, s6, 5\n" \
"v_mfma_f32_16x16x32_bf16 v[160:163], a[152:155], a[188:191], v[160:163]\n" \
"ds_read_b64_tr_b16 a[236:237], v35 offset:8192\n" \
"v_cndmask_b32_e32 v231, v233, v234, vcc\n" \
"v_mfma_f32_16x16x32_bf16 v[160:163], a[216:219], a[252:255], v[160:163]\n" \
"ds_read_b128 a[152:155], v28 offset:4096\n" \
"v_mul_u32_u24_e64 v213, 18, v213\n" \
"v_mfma_f32_16x16x32_bf16 v[176:179], a[156:159], a[188:191], v[176:179]\n" \
"ds_read_b128 a[216:219], v29 offset:4096\n" \
"v_mov_b32_e32 v215, 0x100\n" \
"v_mfma_f32_16x16x32_bf16 v[176:179], a[220:223], a[252:255], v[176:179]\n" \
"ds_read_b64_tr_b16 a[238:239], v35 offset:9216\n" \
"v_mov_b32_e64 v216, s6\n" \
"v_mfma_f32_16x16x32_bf16 v[172:175], a[156:159], a[184:187], v[172:175]\n" \
"v_cmp_eq_u32_e64 vcc, 0, v214\n" \
"v_and_or_b32 v213, v218, 1, v213\n" \
"v_mfma_f32_16x16x32_bf16 v[172:175], a[220:223], a[248:251], v[172:175]\n" \
"v_lshlrev_b32_e32 v212, 3, v212\n" \
"v_cndmask_b32_e32 v214, v215, v216, vcc\n" \
"v_mfma_f32_16x16x32_bf16 v[168:171], a[156:159], a[180:183], v[168:171]\n" \
"v_mad_u32_u24 v213, v213, s5, v214\n" \
"v_and_or_b32 v213, v20, 16, v213\n" \
"v_mfma_f32_16x16x32_bf16 v[168:171], a[220:223], a[244:247], v[168:171]\n" \
"v_add_u32_e32 v9, 0x4800, v213\n" \
"v_mul_u32_u24_e32 v213, 0x120, v218\n" \
"v_mfma_f32_16x16x32_bf16 v[164:167], a[156:159], a[176:179], v[164:167]\n" \
"v_and_or_b32 v10, v212, 24, v213\n" \
"v_lshrrev_b32_e64 v213, 2, v20\n" \
"v_mfma_f32_16x16x32_bf16 v[164:167], a[220:223], a[240:243], v[164:167]\n" \
"ds_read_b128 a[156:159], v28 offset:6144\n" \
"ds_read_b128 a[220:223], v29 offset:6144\n" \
"s_waitcnt lgkmcnt(15)\n" \
"v_mfma_f32_16x16x32_bf16 a[0:3], a[128:131], a[160:163], a[0:3]\n" \
"s_addk_i32 s84, 0x7c00\n" \
"v_mfma_f32_16x16x32_bf16 a[0:3], a[192:195], a[224:227], a[0:3]\n" \
"buffer_load_dwordx4 v27, s[72:75], s84 offen offset:2048 lds\n" \
"v_mfma_f32_16x16x32_bf16 a[4:7], a[128:131], a[164:167], a[4:7]\n" \
"ds_read_b64_tr_b16 a[176:177], v36 offset:0\n" \
"v_and_or_b32 v213, v213, 60, s4\n" \
"s_waitcnt lgkmcnt(15)\n" \
"v_mfma_f32_16x16x32_bf16 a[4:7], a[192:195], a[228:231], a[4:7]\n" \
"s_addk_i32 s84, 0x7c00\n" \
"s_waitcnt lgkmcnt(11)\n" \
"v_mfma_f32_16x16x32_bf16 a[8:11], a[128:131], a[168:171], a[8:11]\n" \
"buffer_load_dwordx4 v27, s[72:75], s84 offen offset:3072 lds\n" \
"s_waitcnt lgkmcnt(9)\n" \
"v_mfma_f32_16x16x32_bf16 a[8:11], a[192:195], a[232:235], a[8:11]\n" \
"ds_read_b64_tr_b16 a[178:179], v36 offset:1024\n" \
"v_lshl_or_b32 v212, s3, 9, v212\n" \
"s_waitcnt lgkmcnt(8)\n" \
"v_mfma_f32_16x16x32_bf16 a[12:15], a[128:131], a[172:175], a[12:15]\n" \
"ds_read_b64_tr_b16 a[240:241], v36 offset:8192\n" \
"v_mad_u32_u24 v4, v224, s5, v223\n" \
"s_waitcnt lgkmcnt(5)\n" \
"v_mfma_f32_16x16x32_bf16 a[12:15], a[192:195], a[236:239], a[12:15]\n" \
"v_mad_u32_u24 v8, v232, s5, v231\n" \
"v_or_b32_e32 v18, 0x27000, v213\n" \
"v_mfma_f32_16x16x32_bf16 a[28:31], a[132:135], a[172:175], a[28:31]\n" \
"v_or_b32_e32 v19, 0x27800, v212\n" \
"v_mov_b32_e32 v219, 0x1000c0c\n" \
"v_mfma_f32_16x16x32_bf16 a[28:31], a[196:199], a[236:239], a[28:31]\n" \
"ds_read_b64_tr_b16 a[242:243], v36 offset:9216\n" \
"v_mov_b32_e32 v220, 0xc0c0100\n" \
"v_mfma_f32_16x16x32_bf16 a[24:27], a[132:135], a[168:171], a[24:27]\n" \
"buffer_load_dwordx4 v[200:203], v192, s[76:79], s34 offen offset:0\n" \
"v_mfma_f32_16x16x32_bf16 a[24:27], a[196:199], a[232:235], a[24:27]\n" \
"v_bfe_u32 v218, v20, 4, 1\n" \
"v_lshrrev_b32_e64 v213, 8, v20\n" \
"v_mfma_f32_16x16x32_bf16 a[20:23], a[132:135], a[164:167], a[20:23]\n" \
"v_cmp_eq_u32_e32 vcc, 0, v218\n" \
"s_lshr_b32 s4, s2, 5\n" \
"v_mfma_f32_16x16x32_bf16 a[20:23], a[196:199], a[228:231], a[20:23]\n" \
"ds_read_b64_tr_b16 a[180:181], v37 offset:0\n" \
"s_and_b32 s5, s3, 1\n" \
"v_mfma_f32_16x16x32_bf16 a[16:19], a[132:135], a[160:163], a[16:19]\n" \
"v_bfe_u32 v217, v20, 4, 4\n" \
"v_cndmask_b32_e32 v219, v219, v220, vcc\n" \
"v_mfma_f32_16x16x32_bf16 a[16:19], a[196:199], a[224:227], a[16:19]\n" \
"v_mov_b32_e32 v220, 0x3020c0c\n" \
"v_mov_b32_e32 v221, 0xc0c0302\n" \
"v_mfma_f32_16x16x32_bf16 a[32:35], a[136:139], a[160:163], a[32:35]\n" \
"buffer_load_dwordx4 v[204:207], v193, s[76:79], s34 offen offset:0\n" \
"v_mfma_f32_16x16x32_bf16 a[32:35], a[200:203], a[224:227], a[32:35]\n" \
"s_mov_b32 m0, s32\n" \
"ds_read_b64_tr_b16 a[182:183], v37 offset:1024\n" \
"v_mfma_f32_16x16x32_bf16 a[36:39], a[136:139], a[164:167], a[36:39]\n" \
"s_mov_b32 exec_lo, 0x0\n" \
"s_mov_b32 exec_hi, 0xff00\n" \
"global_load_lds_dword v[184:185], off\n" \
"s_mov_b64 exec, -1\n" \
"v_mfma_f32_16x16x32_bf16 a[36:39], a[200:203], a[228:231], a[36:39]\n" \
"ds_read_b64_tr_b16 a[244:245], v37 offset:8192\n" \
"v_lshl_or_b32 v218, v213, 2, v218\n" \
"v_mfma_f32_16x16x32_bf16 a[40:43], a[136:139], a[168:171], a[40:43]\n" \
"s_xor_b32 s6, s4, 15\n" \
"v_mfma_f32_16x16x32_bf16 a[40:43], a[200:203], a[232:235], a[40:43]\n" \
"buffer_load_dword v208, v194, s[76:79], s34 offen offset:32\n" \
"v_mfma_f32_16x16x32_bf16 a[44:47], a[136:139], a[172:175], a[44:47]\n" \
"ds_read_b64_tr_b16 a[246:247], v37 offset:9216\n" \
"v_mfma_f32_16x16x32_bf16 a[44:47], a[200:203], a[236:239], a[44:47]\n" \
"s_cmpk_lt_u32 s3, 2\n" \
"s_cselect_b32 s7, s6, s4\n" \
"v_mfma_f32_16x16x32_bf16 a[60:63], a[140:143], a[172:175], a[60:63]\n" \
"s_lshl_b32 s40, s7, 7\n" \
"s_lshl_b32 s41, s5, 6\n" \
"v_mfma_f32_16x16x32_bf16 a[60:63], a[204:207], a[236:239], a[60:63]\n" \
"v_cndmask_b32_e64 v220, v220, v221, vcc\n" \
"v_mov_b32_e32 v221, 0xc0c0c0c\n" \
"v_mfma_f32_16x16x32_bf16 a[56:59], a[140:143], a[168:171], a[56:59]\n" \
"buffer_load_dword v209, v194, s[76:79], s34 offen offset:1184\n" \
"v_mfma_f32_16x16x32_bf16 a[56:59], a[204:207], a[232:235], a[56:59]\n" \
"ds_read_b64_tr_b16 a[184:185], v38 offset:0\n" \
"v_cmp_gt_u32_e64 vcc, v218, v217\n" \
"v_mfma_f32_16x16x32_bf16 a[52:55], a[140:143], a[164:167], a[52:55]\n" \
"v_or_b32_e32 v218, 2, v218\n" \
"v_lshrrev_b32_e32 v212, 7, v20\n" \
"v_mfma_f32_16x16x32_bf16 a[52:55], a[204:207], a[228:231], a[52:55]\n" \
"ds_read_b64_tr_b16 a[186:187], v38 offset:1024\n" \
"s_or_b32 s41, s41, s40\n" \
"v_mfma_f32_16x16x32_bf16 a[48:51], a[140:143], a[160:163], a[48:51]\n" \
"v_cndmask_b32_e32 v40, v221, v219, vcc\n" \
"v_cndmask_b32_e64 v42, v219, v221, vcc\n" \
"v_mfma_f32_16x16x32_bf16 a[48:51], a[204:207], a[224:227], a[48:51]\n" \
"v_cmp_gt_u32_e32 vcc, v218, v217\n" \
"v_and_b32_e32 v218, 16, v20\n" \
"v_mfma_f32_16x16x32_bf16 a[64:67], a[144:147], a[160:163], a[64:67]\n" \
"buffer_load_dword v211, v194, s[76:79], s34 offen offset:2336\n" \
"v_mfma_f32_16x16x32_bf16 a[64:67], a[208:211], a[224:227], a[64:67]\n" \
"ds_read_b64_tr_b16 a[248:249], v38 offset:8192\n" \
"s_movk_i32 s42, 0x3f80\n" \
"v_mfma_f32_16x16x32_bf16 a[68:71], a[144:147], a[164:167], a[68:71]\n" \
"v_or_b32_e32 v214, s41, v212\n" \
"v_and_b32_e32 v217, 0x320, v20\n" \
"v_mfma_f32_16x16x32_bf16 a[68:71], a[208:211], a[228:231], a[68:71]\n" \
"ds_read_b64_tr_b16 a[250:251], v38 offset:9216\n" \
"s_movk_i32 s41, 0x100\n" \
"v_mfma_f32_16x16x32_bf16 a[72:75], a[144:147], a[168:171], a[72:75]\n" \
"v_lshlrev_b32_e64 v218, v218, s42\n" \
"v_mov_b32_e32 v219, 0\n" \
"v_mfma_f32_16x16x32_bf16 a[72:75], a[208:211], a[232:235], a[72:75]\n" \
"s_mov_b32 s76, s20\n" \
"s_and_b32 s77, s21, 0xffff\n" \
"v_mfma_f32_16x16x32_bf16 a[76:79], a[144:147], a[172:175], a[76:79]\n" \
"s_mov_b32 s78, 0x480\n" \
"v_lshrrev_b32_e32 v215, 6, v20\n" \
"v_mfma_f32_16x16x32_bf16 a[76:79], a[208:211], a[236:239], a[76:79]\n" \
"buffer_load_ushort v210, v195, s[76:79], 0 offen\n" \
"v_mfma_f32_16x16x32_bf16 a[92:95], a[148:151], a[172:175], a[92:95]\n" \
"ds_read_b64_tr_b16 a[188:189], v39 offset:0\n" \
"v_cndmask_b32_e64 v41, v221, v220, vcc\n" \
"v_mfma_f32_16x16x32_bf16 a[92:95], a[212:215], a[236:239], a[92:95]\n" \
"v_cndmask_b32_e32 v43, v220, v221, vcc\n" \
"v_cmp_eq_u32_e32 vcc, s41, v217\n" \
"v_mfma_f32_16x16x32_bf16 a[88:91], a[148:151], a[168:171], a[88:91]\n" \
"s_mov_b32 s76, s8\n" \
"s_and_b32 s77, s9, 0xffff\n" \
"v_mfma_f32_16x16x32_bf16 a[88:91], a[212:215], a[232:235], a[88:91]\n" \
"s_mov_b32 s78, 0x240000\n" \
"ds_read_b64_tr_b16 a[190:191], v39 offset:1024\n" \
".fill (L_pb + 48864 - .) / 4, 4, 0xBF800000\n" \
"L_pp12:\n" \
"s_cbranch_execz L_pret\n" \
"v_mfma_f32_16x16x32_bf16 a[84:87], a[148:151], a[164:167], a[84:87]\n" \
"v_and_b32_e32 v217, 0x330, v20\n" \
"v_bfe_u32 v216, v20, 6, 2\n" \
"v_mfma_f32_16x16x32_bf16 a[84:87], a[212:215], a[228:231], a[84:87]\n" \
"ds_read_b64_tr_b16 a[252:253], v39 offset:8192\n" \
"v_cndmask_b32_e32 v46, v219, v218, vcc\n" \
"v_mfma_f32_16x16x32_bf16 a[80:83], a[148:151], a[160:163], a[80:83]\n" \
"v_mov_b32_e32 v218, 0x3f80\n" \
"v_cmp_eq_u32_e64 vcc, 32, v217\n" \
"v_mfma_f32_16x16x32_bf16 a[80:83], a[212:215], a[224:227], a[80:83]\n" \
"s_add_i32 s41, s40, -16\n" \
"v_lshrrev_b32_e32 v219, 1, v20\n" \
"v_mfma_f32_16x16x32_bf16 a[96:99], a[152:155], a[160:163], a[96:99]\n" \
"ds_read_b64_tr_b16 a[254:255], v39 offset:9216\n" \
"v_bfe_u32 v215, v215, 1, 1\n" \
"v_mfma_f32_16x16x32_bf16 a[96:99], a[216:219], a[224:227], a[96:99]\n" \
"v_cndmask_b32_e32 v47, 0, v218, vcc\n" \
"s_mov_b32 s42, 0\n" \
"v_mfma_f32_16x16x32_bf16 a[100:103], a[152:155], a[164:167], a[100:103]\n" \
"s_cmp_lg_u32 s40, 0\n" \
"s_cselect_b32 s41, s41, s42\n" \
"v_mfma_f32_16x16x32_bf16 a[100:103], a[216:219], a[228:231], a[100:103]\n" \
"v_lshl_or_b32 v217, s5, 3, v212\n" \
"v_lshlrev_b32_e64 v218, 10, v213\n" \
"v_mfma_f32_16x16x32_bf16 a[104:107], a[152:155], a[168:171], a[104:107]\n" \
"v_lshlrev_b32_e32 v216, 7, v216\n" \
"v_and_b32_e32 v219, 24, v219\n" \
"v_mfma_f32_16x16x32_bf16 a[104:107], a[216:219], a[232:235], a[104:107]\n" \
"v_and_or_b32 v212, v212, 2, v215\n" \
"v_add_u32_e64 v217, s41, v217\n" \
"v_mfma_f32_16x16x32_bf16 a[108:111], a[152:155], a[172:175], a[108:111]\n" \
"v_or3_b32 v216, v216, v218, v219\n" \
"v_lshlrev_b32_e64 v212, 5, v212\n" \
"v_mfma_f32_16x16x32_bf16 a[108:111], a[216:219], a[236:239], a[108:111]\n" \
"s_mov_b32 s41, 0x14000\n" \
"v_or3_b32 v48, v216, v212, s41\n" \
"s_waitcnt lgkmcnt(15)\n" \
"v_mfma_f32_16x16x32_bf16 a[124:127], a[156:159], a[172:175], a[124:127]\n" \
"s_lshl_b32 s41, s5, 2\n" \
"s_lshl_b32 s7, s7, 3\n" \
"v_mfma_f32_16x16x32_bf16 a[124:127], a[220:223], a[236:239], a[124:127]\n" \
"v_or_b32_e32 v212, s41, v213\n" \
"s_or_b32 s7, s7, s41\n" \
"v_mfma_f32_16x16x32_bf16 a[120:123], a[156:159], a[168:171], a[120:123]\n" \
"s_mov_b32 s41, 0xff800000\n" \
"v_mfma_f32_16x16x32_bf16 a[120:123], a[220:223], a[232:235], a[120:123]\n" \
"s_cmp_lg_u32 s40, 0\n" \
"s_cselect_b32 s47, s42, s41\n" \
"v_mfma_f32_16x16x32_bf16 a[116:119], a[156:159], a[164:167], a[116:119]\n" \
"s_mov_b32 s40, 0xc000\n" \
"s_mov_b32 s41, 0x8000\n" \
"v_mfma_f32_16x16x32_bf16 a[116:119], a[220:223], a[228:231], a[116:119]\n" \
"s_cmp_lg_u32 s4, 0\n" \
"s_cselect_b32 s51, s40, s41\n" \
"v_mfma_f32_16x16x32_bf16 a[112:115], a[156:159], a[160:163], a[112:115]\n" \
"s_movk_i32 s40, 0x4000\n" \
"v_mfma_f32_16x16x32_bf16 a[112:115], a[220:223], a[224:227], a[112:115]\n" \
"s_waitcnt vmcnt(7) lgkmcnt(0)\n" \
"s_barrier\n" \
"v_mfma_f32_16x16x32_bf16 v[52:55], a[128:131], a[176:179], v[52:55]\n" \
"s_add_u32 m0, s30, 0x0\n" \
"s_movk_i32 s84, 0x200\n" \
"v_mfma_f32_16x16x32_bf16 v[52:55], a[192:195], a[240:243], v[52:55]\n" \
"buffer_load_dwordx4 v22, s[64:67], s84 offen offset:0 lds\n" \
"v_mfma_f32_16x16x32_bf16 v[56:59], a[128:131], a[180:183], v[56:59]\n" \
"ds_read_b64_tr_b16 a[160:161], v32 offset:32768\n" \
"v_mfma_f32_16x16x32_bf16 v[56:59], a[192:195], a[244:247], v[56:59]\n" \
"s_movk_i32 s84, 0x1e00\n" \
"s_nop 0\n" \
"v_mfma_f32_16x16x32_bf16 v[60:63], a[128:131], a[184:187], v[60:63]\n" \
"buffer_load_dwordx4 v23, s[64:67], s84 offen offset:1024 lds\n" \
"v_mfma_f32_16x16x32_bf16 v[60:63], a[192:195], a[248:251], v[60:63]\n" \
"ds_read_b64_tr_b16 a[162:163], v32 offset:33792\n" \
"v_mfma_f32_16x16x32_bf16 v[64:67], a[128:131], a[188:191], v[64:67]\n" \
"ds_read_b64_tr_b16 a[224:225], v32 offset:40960\n" \
"v_mfma_f32_16x16x32_bf16 v[64:67], a[192:195], a[252:255], v[64:67]\n" \
"ds_read_b128 a[128:131], v28 offset:49152\n" \
"v_mfma_f32_16x16x32_bf16 v[80:83], a[132:135], a[188:191], v[80:83]\n" \
"ds_read_b128 a[192:195], v29 offset:49152\n" \
"v_mfma_f32_16x16x32_bf16 v[80:83], a[196:199], a[252:255], v[80:83]\n" \
"s_movk_i32 s84, 0x3a00\n" \
"v_mfma_f32_16x16x32_bf16 v[76:79], a[132:135], a[184:187], v[76:79]\n" \
"buffer_load_dwordx4 v22, s[64:67], s84 offen offset:2048 lds\n" \
"v_mfma_f32_16x16x32_bf16 v[76:79], a[196:199], a[248:251], v[76:79]\n" \
"s_movk_i32 s84, 0x5600\n" \
"v_mfma_f32_16x16x32_bf16 v[72:75], a[132:135], a[180:183], v[72:75]\n" \
"buffer_load_dwordx4 v23, s[64:67], s84 offen offset:3072 lds\n" \
"v_mfma_f32_16x16x32_bf16 v[72:75], a[196:199], a[244:247], v[72:75]\n" \
"s_add_u32 m0, s31, 0x0\n" \
"s_mov_b32 s84, 0x200000\n" \
"v_mfma_f32_16x16x32_bf16 v[68:71], a[132:135], a[176:179], v[68:71]\n" \
"buffer_load_dwordx4 v26, s[68:71], s84 offen offset:0 lds\n" \
"v_mfma_f32_16x16x32_bf16 v[68:71], a[196:199], a[240:243], v[68:71]\n" \
"ds_read_b128 a[132:135], v28 offset:51200\n" \
"v_mfma_f32_16x16x32_bf16 v[84:87], a[136:139], a[176:179], v[84:87]\n" \
"ds_read_b128 a[196:199], v29 offset:51200\n" \
"v_mfma_f32_16x16x32_bf16 v[84:87], a[200:203], a[240:243], v[84:87]\n" \
"s_addk_i32 s84, 0x7c00\n" \
"v_mfma_f32_16x16x32_bf16 v[88:91], a[136:139], a[180:183], v[88:91]\n" \
"buffer_load_dwordx4 v26, s[68:71], s84 offen offset:1024 lds\n" \
"v_mfma_f32_16x16x32_bf16 v[88:91], a[200:203], a[244:247], v[88:91]\n" \
"ds_read_b64_tr_b16 a[226:227], v32 offset:41984\n" \
"v_mfma_f32_16x16x32_bf16 v[92:95], a[136:139], a[184:187], v[92:95]\n" \
"s_addk_i32 s84, 0x7c00\n" \
"v_mfma_f32_16x16x32_bf16 v[92:95], a[200:203], a[248:251], v[92:95]\n" \
"buffer_load_dwordx4 v27, s[68:71], s84 offen offset:2048 lds\n" \
"v_mfma_f32_16x16x32_bf16 v[96:99], a[136:139], a[188:191], v[96:99]\n" \
"ds_read_b64_tr_b16 a[164:165], v33 offset:32768\n" \
"v_mfma_f32_16x16x32_bf16 v[96:99], a[200:203], a[252:255], v[96:99]\n" \
"ds_read_b128 a[136:139], v28 offset:53248\n" \
"v_mfma_f32_16x16x32_bf16 v[112:115], a[140:143], a[188:191], v[112:115]\n" \
"ds_read_b128 a[200:203], v29 offset:53248\n" \
"v_mfma_f32_16x16x32_bf16 v[112:115], a[204:207], a[252:255], v[112:115]\n" \
"s_addk_i32 s84, 0x7c00\n" \
"v_mfma_f32_16x16x32_bf16 v[108:111], a[140:143], a[184:187], v[108:111]\n" \
"buffer_load_dwordx4 v27, s[68:71], s84 offen offset:3072 lds\n" \
"v_mfma_f32_16x16x32_bf16 v[108:111], a[204:207], a[248:251], v[108:111]\n" \
"s_add_u32 m0, s30, 0x1000\n" \
"s_mov_b32 s84, 0x8200\n" \
"v_mfma_f32_16x16x32_bf16 v[104:107], a[140:143], a[180:183], v[104:107]\n" \
"buffer_load_dwordx4 v22, s[64:67], s84 offen offset:0 lds\n" \
"v_mfma_f32_16x16x32_bf16 v[104:107], a[204:207], a[244:247], v[104:107]\n" \
"ds_read_b64_tr_b16 a[166:167], v33 offset:33792\n" \
"v_mfma_f32_16x16x32_bf16 v[100:103], a[140:143], a[176:179], v[100:103]\n" \
"ds_read_b64_tr_b16 a[228:229], v33 offset:40960\n" \
"v_mfma_f32_16x16x32_bf16 v[100:103], a[204:207], a[240:243], v[100:103]\n" \
"ds_read_b128 a[140:143], v28 offset:55296\n" \
"v_mfma_f32_16x16x32_bf16 v[116:119], a[144:147], a[176:179], v[116:119]\n" \
"ds_read_b128 a[204:207], v29 offset:55296\n" \
"v_mfma_f32_16x16x32_bf16 v[116:119], a[208:211], a[240:243], v[116:119]\n" \
"s_addk_i32 s84, 0x1c00\n" \
"v_mfma_f32_16x16x32_bf16 v[120:123], a[144:147], a[180:183], v[120:123]\n" \
"buffer_load_dwordx4 v23, s[64:67], s84 offen offset:1024 lds\n" \
"v_mfma_f32_16x16x32_bf16 v[120:123], a[208:211], a[244:247], v[120:123]\n" \
"s_addk_i32 s84, 0x1c00\n" \
"v_mfma_f32_16x16x32_bf16 v[124:127], a[144:147], a[184:187], v[124:127]\n" \
"buffer_load_dwordx4 v22, s[64:67], s84 offen offset:2048 lds\n" \
"v_mfma_f32_16x16x32_bf16 v[124:127], a[208:211], a[248:251], v[124:127]\n" \
"s_addk_i32 s84, 0x1c00\n" \
"s_nop 0\n" \
"v_mfma_f32_16x16x32_bf16 v[128:131], a[144:147], a[188:191], v[128:131]\n" \
"buffer_load_dwordx4 v23, s[64:67], s84 offen offset:3072 lds\n" \
"v_mfma_f32_16x16x32_bf16 v[128:131], a[208:211], a[252:255], v[128:131]\n" \
"ds_read_b128 a[144:147], v28 offset:32768\n" \
"v_mfma_f32_16x16x32_bf16 v[144:147], a[148:151], a[188:191], v[144:147]\n" \
"ds_read_b128 a[208:211], v29 offset:32768\n" \
"v_mfma_f32_16x16x32_bf16 v[144:147], a[212:215], a[252:255], v[144:147]\n" \
"s_add_u32 m0, s31, 0x4000\n" \
"s_mov_b32 s84, 0x200000\n" \
"v_mfma_f32_16x16x32_bf16 v[140:143], a[148:151], a[184:187], v[140:143]\n" \
"buffer_load_dwordx4 v26, s[72:75], s84 offen offset:0 lds\n" \
"v_mfma_f32_16x16x32_bf16 v[140:143], a[212:215], a[248:251], v[140:143]\n" \
"ds_read_b64_tr_b16 a[230:231], v33 offset:41984\n" \
"v_mfma_f32_16x16x32_bf16 v[136:139], a[148:151], a[180:183], v[136:139]\n" \
"s_addk_i32 s84, 0x7c00\n" \
"s_nop 0\n" \
"v_mfma_f32_16x16x32_bf16 v[136:139], a[212:215], a[244:247], v[136:139]\n" \
"buffer_load_dwordx4 v26, s[72:75], s84 offen offset:1024 lds\n" \
"v_mfma_f32_16x16x32_bf16 v[132:135], a[148:151], a[176:179], v[132:135]\n" \
"ds_read_b64_tr_b16 a[168:169], v34 offset:32768\n" \
"v_mfma_f32_16x16x32_bf16 v[132:135], a[212:215], a[240:243], v[132:135]\n" \
"ds_read_b128 a[148:151], v28 offset:34816\n" \
"v_mfma_f32_16x16x32_bf16 v[148:151], a[152:155], a[176:179], v[148:151]\n" \
"ds_read_b128 a[212:215], v29 offset:34816\n" \
"v_mfma_f32_16x16x32_bf16 v[148:151], a[216:219], a[240:243], v[148:151]\n" \
"ds_read_b64_tr_b16 a[170:171], v34 offset:33792\n" \
"v_mfma_f32_16x16x32_bf16 v[152:155], a[152:155], a[180:183], v[152:155]\n" \
"ds_read_b64_tr_b16 a[232:233], v34 offset:40960\n" \
"v_mfma_f32_16x16x32_bf16 v[152:155], a[216:219], a[244:247], v[152:155]\n" \
"ds_read_b64_tr_b16 a[234:235], v34 offset:41984\n" \
"v_mfma_f32_16x16x32_bf16 v[156:159], a[152:155], a[184:187], v[156:159]\n" \
"ds_read_b64_tr_b16 a[172:173], v35 offset:32768\n" \
"v_mfma_f32_16x16x32_bf16 v[156:159], a[216:219], a[248:251], v[156:159]\n" \
"ds_read_b64_tr_b16 a[174:175], v35 offset:33792\n" \
"v_mfma_f32_16x16x32_bf16 v[160:163], a[152:155], a[188:191], v[160:163]\n" \
"ds_read_b64_tr_b16 a[236:237], v35 offset:40960\n" \
"v_mfma_f32_16x16x32_bf16 v[160:163], a[216:219], a[252:255], v[160:163]\n" \
"ds_read_b128 a[152:155], v28 offset:36864\n" \
"v_mfma_f32_16x16x32_bf16 v[176:179], a[156:159], a[188:191], v[176:179]\n" \
"ds_read_b128 a[216:219], v29 offset:36864\n" \
"v_mfma_f32_16x16x32_bf16 v[176:179], a[220:223], a[252:255], v[176:179]\n" \
"ds_read_b64_tr_b16 a[238:239], v35 offset:41984\n" \
"v_mfma_f32_16x16x32_bf16 v[172:175], a[156:159], a[184:187], v[172:175]\n" \
"s_cmp_lg_u32 s4, 0\n" \
"s_cselect_b32 s53, s41, s40\n" \
"v_mfma_f32_16x16x32_bf16 v[172:175], a[220:223], a[248:251], v[172:175]\n" \
"s_lshl_b32 s40, s2, 2\n" \
"s_and_b32 s40, s40, 28\n" \
"v_mfma_f32_16x16x32_bf16 v[168:171], a[156:159], a[180:183], v[168:171]\n" \
"s_bfe_u32 s45, s2, 0x20003\n" \
"s_lshr_b32 s44, s3, 1\n" \
"v_mfma_f32_16x16x32_bf16 v[168:171], a[220:223], a[244:247], v[168:171]\n" \
"s_or_b32 s46, s40, s45\n" \
"s_lshl_b32 s43, s44, 15\n" \
"v_mfma_f32_16x16x32_bf16 v[164:167], a[156:159], a[176:179], v[164:167]\n" \
"s_lshl_b32 s41, s5, 14\n" \
"s_lshl_b32 s48, s46, 20\n" \
"v_mfma_f32_16x16x32_bf16 v[164:167], a[220:223], a[240:243], v[164:167]\n" \
"ds_read_b128 a[156:159], v28 offset:38912\n" \
"ds_read_b128 a[220:223], v29 offset:38912\n" \
"s_waitcnt lgkmcnt(15)\n" \
"v_mfma_f32_16x16x32_bf16 a[0:3], a[128:131], a[160:163], a[0:3]\n" \
"s_addk_i32 s84, 0x7c00\n" \
"v_mfma_f32_16x16x32_bf16 a[0:3], a[192:195], a[224:227], a[0:3]\n" \
"buffer_load_dwordx4 v27, s[72:75], s84 offen offset:2048 lds\n" \
"v_mfma_f32_16x16x32_bf16 a[4:7], a[128:131], a[164:167], a[4:7]\n" \
"ds_read_b64_tr_b16 a[176:177], v36 offset:32768\n" \
"s_or_b32 s41, s43, s41\n" \
"s_waitcnt lgkmcnt(15)\n" \
"v_mfma_f32_16x16x32_bf16 a[4:7], a[192:195], a[228:231], a[4:7]\n" \
"s_addk_i32 s84, 0x7c00\n" \
"s_waitcnt lgkmcnt(11)\n" \
"v_mfma_f32_16x16x32_bf16 a[8:11], a[128:131], a[168:171], a[8:11]\n" \
"buffer_load_dwordx4 v27, s[72:75], s84 offen offset:3072 lds\n" \
"s_waitcnt lgkmcnt(9)\n" \
"v_mfma_f32_16x16x32_bf16 a[8:11], a[192:195], a[232:235], a[8:11]\n" \
"ds_read_b64_tr_b16 a[178:179], v36 offset:33792\n" \
"s_add_i32 s49, s48, 0x1000000\n" \
"s_waitcnt lgkmcnt(8)\n" \
"v_mfma_f32_16x16x32_bf16 a[12:15], a[128:131], a[172:175], a[12:15]\n" \
"ds_read_b64_tr_b16 a[240:241], v36 offset:40960\n" \
"s_or_b32 s42, s49, s41\n" \
"s_waitcnt lgkmcnt(5)\n" \
"v_mfma_f32_16x16x32_bf16 a[12:15], a[192:195], a[236:239], a[12:15]\n" \
"s_lshl_b32 s4, s4, 16\n" \
"s_or_b32 s41, s42, s4\n" \
"v_mfma_f32_16x16x32_bf16 a[28:31], a[132:135], a[172:175], a[28:31]\n" \
"s_lshl_b32 s4, s6, 16\n" \
"s_or_b32 s42, s42, s4\n" \
"v_mfma_f32_16x16x32_bf16 a[28:31], a[196:199], a[236:239], a[28:31]\n" \
"ds_read_b64_tr_b16 a[242:243], v36 offset:41984\n" \
"s_lshl_b32 s4, s5, 11\n" \
"v_mfma_f32_16x16x32_bf16 a[24:27], a[132:135], a[168:171], a[24:27]\n" \
"s_mul_i32 s5, s7, 0x24800\n" \
"s_lshl_b32 s6, s46, 9\n" \
"v_mfma_f32_16x16x32_bf16 a[24:27], a[196:199], a[232:235], a[24:27]\n" \
"s_lshl_b32 s7, s40, 6\n" \
"s_add_i32 s6, s5, s6\n" \
"v_mfma_f32_16x16x32_bf16 a[20:23], a[132:135], a[164:167], a[20:23]\n" \
"s_or_b32 s5, s5, s7\n" \
"s_lshl_b32 s7, s45, 2\n" \
"v_mfma_f32_16x16x32_bf16 a[20:23], a[196:199], a[228:231], a[20:23]\n" \
"ds_read_b64_tr_b16 a[180:181], v37 offset:32768\n" \
"s_or_b32 s5, s5, s7\n" \
"v_mfma_f32_16x16x32_bf16 a[16:19], a[132:135], a[160:163], a[16:19]\n" \
"s_lshl_b32 s7, s3, 12\n" \
"s_or_b32 s50, s49, s7\n" \
"v_mfma_f32_16x16x32_bf16 a[16:19], a[196:199], a[224:227], a[16:19]\n" \
"s_lshl_b32 s7, s3, 11\n" \
"v_lshrrev_b32_e32 v215, 4, v20\n" \
"v_mfma_f32_16x16x32_bf16 a[32:35], a[136:139], a[160:163], a[32:35]\n" \
"s_waitcnt vmcnt(16)\n" \
"v_or_b32_e32 v204, v210, v204\n" \
"v_or_b32_e64 v206, v210, v206\n" \
"v_mfma_f32_16x16x32_bf16 a[32:35], a[200:203], a[224:227], a[32:35]\n" \
"v_mov_b32_e64 v210, 0\n" \
"s_add_u32 s88, s35, 0x1000\n" \
"v_mfma_f32_16x16x32_bf16 a[36:39], a[136:139], a[164:167], a[36:39]\n" \
"ds_read_b64_tr_b16 a[182:183], v37 offset:33792\n" \
"v_bfe_u32 v216, v20, 4, 3\n" \
"v_mfma_f32_16x16x32_bf16 a[36:39], a[200:203], a[228:231], a[36:39]\n" \
"ds_read_b64_tr_b16 a[244:245], v37 offset:40960\n" \
"s_add_i32 s52, s48, s7\n" \
"v_mfma_f32_16x16x32_bf16 a[40:43], a[136:139], a[168:171], a[40:43]\n" \
"s_mov_b32 m0, s32\n" \
"s_add_i32 s53, s53, s7\n" \
"v_mfma_f32_16x16x32_bf16 a[40:43], a[200:203], a[232:235], a[40:43]\n" \
"s_mov_b32 exec_lo, 0xffffffff\n" \
"s_mov_b32 exec_hi, 0x0\n" \
"global_load_lds_dword v[188:189], off\n" \
"s_mov_b64 exec, -1\n" \
"v_mfma_f32_16x16x32_bf16 a[44:47], a[136:139], a[172:175], a[44:47]\n" \
"ds_read_b64_tr_b16 a[246:247], v37 offset:41984\n" \
"v_mul_u32_u24_e32 v214, 0x480, v214\n" \
"v_mfma_f32_16x16x32_bf16 a[44:47], a[200:203], a[236:239], a[44:47]\n" \
"v_bitop3_b32 v218, v213, v215, 7 bitop3:0x78\n" \
"v_bitop3_b32 v213, v213, v216, 4 bitop3:0x36\n" \
"v_mfma_f32_16x16x32_bf16 a[60:63], a[140:143], a[172:175], a[60:63]\n" \
"ds_write_b128 v196, v[200:203]\n" \
"v_mfma_f32_16x16x32_bf16 a[60:63], a[204:207], a[236:239], a[60:63]\n" \
"s_lshl_b32 s7, s3, 10\n" \
"s_or_b32 s4, s43, s4\n" \
"v_mfma_f32_16x16x32_bf16 a[56:59], a[140:143], a[168:171], a[56:59]\n" \
"ds_read_b64_tr_b16 a[184:185], v38 offset:32768\n" \
"v_lshl_or_b32 v25, v213, 4, v214\n" \
"v_mfma_f32_16x16x32_bf16 a[56:59], a[204:207], a[232:235], a[56:59]\n" \
"v_mul_u32_u24_e32 v213, 0x480, v217\n" \
"v_bitop3_b32 v212, v215, v212, 7 bitop3:0x6c\n" \
"v_mfma_f32_16x16x32_bf16 a[52:55], a[140:143], a[164:167], a[52:55]\n" \
"s_or_b32 s40, s7, 0x14000\n" \
"s_lshl_b32 s43, s44, 13\n" \
"v_mfma_f32_16x16x32_bf16 a[52:55], a[204:207], a[228:231], a[52:55]\n" \
"ds_read_b64_tr_b16 a[186:187], v38 offset:33792\n" \
"s_mulk_i32 s44, 0xe800\n" \
"v_mfma_f32_16x16x32_bf16 a[48:51], a[140:143], a[160:163], a[48:51]\n" \
"s_lshl_b32 s7, s3, 8\n" \
"s_nop 0\n" \
"v_lshl_or_b32 v24, v218, 4, v214\n" \
"v_mfma_f32_16x16x32_bf16 a[48:51], a[204:207], a[224:227], a[48:51]\n" \
"v_lshl_or_b32 v21, v212, 4, v213\n" \
"s_add_i32 s44, s44, 0x17000\n" \
"v_mfma_f32_16x16x32_bf16 a[64:67], a[144:147], a[160:163], a[64:67]\n" \
"ds_write_b128 v197, v[204:207]\n" \
"v_mfma_f32_16x16x32_bf16 a[64:67], a[208:211], a[224:227], a[64:67]\n" \
"ds_read_b64_tr_b16 a[248:249], v38 offset:40960\n" \
"s_or_b32 s45, s7, 0x27000\n" \
"v_mfma_f32_16x16x32_bf16 a[68:71], a[144:147], a[164:167], a[68:71]\n" \
"s_or_b32 s46, s4, 0x10000\n" \
"s_add_i32 s48, s6, 0x3000000\n" \
"v_mfma_f32_16x16x32_bf16 a[68:71], a[208:211], a[228:231], a[68:71]\n" \
"ds_read_b64_tr_b16 a[250:251], v38 offset:41984\n" \
"s_add_i32 s49, s5, 0x3004000\n" \
"v_mfma_f32_16x16x32_bf16 a[72:75], a[144:147], a[168:171], a[72:75]\n" \
"s_add_i32 s52, s52, 0x1010000\n" \
"v_mfma_f32_16x16x32_bf16 a[72:75], a[208:211], a[232:235], a[72:75]\n" \
"v_mfma_f32_16x16x32_bf16 a[76:79], a[144:147], a[172:175], a[76:79]\n" \
"v_mfma_f32_16x16x32_bf16 a[76:79], a[208:211], a[236:239], a[76:79]\n" \
"ds_read_b64_tr_b16 a[188:189], v39 offset:32768\n" \
"v_mfma_f32_16x16x32_bf16 a[92:95], a[148:151], a[172:175], a[92:95]\n" \
"ds_write_b128 v198, v[208:211]\n" \
"v_mfma_f32_16x16x32_bf16 a[92:95], a[212:215], a[236:239], a[92:95]\n" \
"v_mfma_f32_16x16x32_bf16 a[88:91], a[148:151], a[168:171], a[88:91]\n" \
"ds_read_b64_tr_b16 a[190:191], v39 offset:33792\n" \
"v_mfma_f32_16x16x32_bf16 a[88:91], a[212:215], a[232:235], a[88:91]\n" \
"v_mfma_f32_16x16x32_bf16 a[84:87], a[148:151], a[164:167], a[84:87]\n" \
"v_mfma_f32_16x16x32_bf16 a[84:87], a[212:215], a[228:231], a[84:87]\n" \
"ds_read_b64_tr_b16 a[252:253], v39 offset:40960\n" \
"v_mfma_f32_16x16x32_bf16 a[80:83], a[148:151], a[160:163], a[80:83]\n" \
"v_mfma_f32_16x16x32_bf16 a[80:83], a[212:215], a[224:227], a[80:83]\n" \
"v_mfma_f32_16x16x32_bf16 a[96:99], a[152:155], a[160:163], a[96:99]\n" \
"ds_read_b64_tr_b16 a[254:255], v39 offset:41984\n" \
"v_mfma_f32_16x16x32_bf16 a[96:99], a[216:219], a[224:227], a[96:99]\n" \
"v_mfma_f32_16x16x32_bf16 a[100:103], a[152:155], a[164:167], a[100:103]\n" \
"v_mfma_f32_16x16x32_bf16 a[100:103], a[216:219], a[228:231], a[100:103]\n" \
"v_mfma_f32_16x16x32_bf16 a[104:107], a[152:155], a[168:171], a[104:107]\n" \
"v_mfma_f32_16x16x32_bf16 a[104:107], a[216:219], a[232:235], a[104:107]\n" \
"v_mfma_f32_16x16x32_bf16 a[108:111], a[152:155], a[172:175], a[108:111]\n" \
"v_mfma_f32_16x16x32_bf16 a[108:111], a[216:219], a[236:239], a[108:111]\n" \
"s_waitcnt lgkmcnt(15)\n" \
"v_mfma_f32_16x16x32_bf16 a[124:127], a[156:159], a[172:175], a[124:127]\n" \
"v_mfma_f32_16x16x32_bf16 a[124:127], a[220:223], a[236:239], a[124:127]\n" \
"v_mfma_f32_16x16x32_bf16 a[120:123], a[156:159], a[168:171], a[120:123]\n" \
"v_mfma_f32_16x16x32_bf16 a[120:123], a[220:223], a[232:235], a[120:123]\n" \
"v_mfma_f32_16x16x32_bf16 a[116:119], a[156:159], a[164:167], a[116:119]\n" \
"v_mfma_f32_16x16x32_bf16 a[116:119], a[220:223], a[228:231], a[116:119]\n" \
"v_mfma_f32_16x16x32_bf16 a[112:115], a[156:159], a[160:163], a[112:115]\n" \
"v_mfma_f32_16x16x32_bf16 a[112:115], a[220:223], a[224:227], a[112:115]\n" \
"s_waitcnt vmcnt(1) lgkmcnt(0)\n" \
"s_barrier\n" \
"v_mfma_f32_16x16x32_bf16 v[52:55], a[128:131], a[176:179], v[52:55]\n" \
"s_add_u32 m0, s30, 0x8000\n" \
"s_movk_i32 s84, 0x280\n" \
"v_mfma_f32_16x16x32_bf16 v[52:55], a[192:195], a[240:243], v[52:55]\n" \
"buffer_load_dwordx4 v22, s[64:67], s84 offen offset:0 lds\n" \
"v_mfma_f32_16x16x32_bf16 v[56:59], a[128:131], a[180:183], v[56:59]\n" \
"s_movk_i32 s84, 0x1e80\n" \
"v_mfma_f32_16x16x32_bf16 v[56:59], a[192:195], a[244:247], v[56:59]\n" \
"buffer_load_dwordx4 v23, s[64:67], s84 offen offset:1024 lds\n" \
"v_mfma_f32_16x16x32_bf16 v[60:63], a[128:131], a[184:187], v[60:63]\n" \
"ds_read_b64_tr_b16 a[160:161], v32 offset:0\n" \
"v_mfma_f32_16x16x32_bf16 v[60:63], a[192:195], a[248:251], v[60:63]\n" \
"s_movk_i32 s84, 0x3a80\n" \
"v_mfma_f32_16x16x32_bf16 v[64:67], a[128:131], a[188:191], v[64:67]\n" \
"buffer_load_dwordx4 v22, s[64:67], s84 offen offset:2048 lds\n" \
"v_mfma_f32_16x16x32_bf16 v[64:67], a[192:195], a[252:255], v[64:67]\n" \
"ds_read_b128 a[128:131], v28 offset:16384\n" \
"v_mfma_f32_16x16x32_bf16 v[80:83], a[132:135], a[188:191], v[80:83]\n" \
"ds_read_b128 a[192:195], v29 offset:16384\n" \
"v_mfma_f32_16x16x32_bf16 v[80:83], a[196:199], a[252:255], v[80:83]\n" \
"s_movk_i32 s84, 0x5680\n" \
"s_nop 0\n" \
"v_mfma_f32_16x16x32_bf16 v[76:79], a[132:135], a[184:187], v[76:79]\n" \
"buffer_load_dwordx4 v23, s[64:67], s84 offen offset:3072 lds\n" \
"v_mfma_f32_16x16x32_bf16 v[76:79], a[196:199], a[248:251], v[76:79]\n" \
"s_add_u32 m0, s31, 0x8000\n" \
"s_mov_b32 s84, 0x280000\n" \
"v_mfma_f32_16x16x32_bf16 v[72:75], a[132:135], a[180:183], v[72:75]\n" \
"buffer_load_dwordx4 v26, s[68:71], s84 offen offset:0 lds\n" \
"v_mfma_f32_16x16x32_bf16 v[72:75], a[196:199], a[244:247], v[72:75]\n" \
"ds_read_b64_tr_b16 a[162:163], v32 offset:1024\n" \
"v_mfma_f32_16x16x32_bf16 v[68:71], a[132:135], a[176:179], v[68:71]\n" \
"ds_read_b64_tr_b16 a[224:225], v32 offset:8192\n" \
"v_mfma_f32_16x16x32_bf16 v[68:71], a[196:199], a[240:243], v[68:71]\n" \
"ds_read_b128 a[132:135], v28 offset:18432\n" \
"v_mfma_f32_16x16x32_bf16 v[84:87], a[136:139], a[176:179], v[84:87]\n" \
"ds_read_b128 a[196:199], v29 offset:18432\n" \
"v_mfma_f32_16x16x32_bf16 v[84:87], a[200:203], a[240:243], v[84:87]\n" \
"s_addk_i32 s84, 0x7c00\n" \
"v_mfma_f32_16x16x32_bf16 v[88:91], a[136:139], a[180:183], v[88:91]\n" \
"buffer_load_dwordx4 v26, s[68:71], s84 offen offset:1024 lds\n" \
"v_mfma_f32_16x16x32_bf16 v[88:91], a[200:203], a[244:247], v[88:91]\n" \
"s_addk_i32 s84, 0x7c00\n" \
"v_mfma_f32_16x16x32_bf16 v[92:95], a[136:139], a[184:187], v[92:95]\n" \
"buffer_load_dwordx4 v27, s[68:71], s84 offen offset:2048 lds\n" \
"v_mfma_f32_16x16x32_bf16 v[92:95], a[200:203], a[248:251], v[92:95]\n" \
"s_addk_i32 s84, 0x7c00\n" \
"s_nop 0\n" \
"v_mfma_f32_16x16x32_bf16 v[96:99], a[136:139], a[188:191], v[96:99]\n" \
"buffer_load_dwordx4 v27, s[68:71], s84 offen offset:3072 lds\n" \
"v_mfma_f32_16x16x32_bf16 v[96:99], a[200:203], a[252:255], v[96:99]\n" \
"ds_read_b128 a[136:139], v28 offset:20480\n" \
"v_mfma_f32_16x16x32_bf16 v[112:115], a[140:143], a[188:191], v[112:115]\n" \
"ds_read_b128 a[200:203], v29 offset:20480\n" \
"v_mfma_f32_16x16x32_bf16 v[112:115], a[204:207], a[252:255], v[112:115]\n" \
"s_add_u32 m0, s30, 0x9000\n" \
"s_mov_b32 s84, 0x8280\n" \
"v_mfma_f32_16x16x32_bf16 v[108:111], a[140:143], a[184:187], v[108:111]\n" \
"buffer_load_dwordx4 v22, s[64:67], s84 offen offset:0 lds\n" \
"v_mfma_f32_16x16x32_bf16 v[108:111], a[204:207], a[248:251], v[108:111]\n" \
"s_addk_i32 s84, 0x1c00\n" \
"v_mfma_f32_16x16x32_bf16 v[104:107], a[140:143], a[180:183], v[104:107]\n" \
"buffer_load_dwordx4 v23, s[64:67], s84 offen offset:1024 lds\n" \
"v_mfma_f32_16x16x32_bf16 v[104:107], a[204:207], a[244:247], v[104:107]\n" \
"s_addk_i32 s84, 0x1c00\n" \
"v_mfma_f32_16x16x32_bf16 v[100:103], a[140:143], a[176:179], v[100:103]\n" \
"buffer_load_dwordx4 v22, s[64:67], s84 offen offset:2048 lds\n" \
"v_mfma_f32_16x16x32_bf16 v[100:103], a[204:207], a[240:243], v[100:103]\n" \
"ds_read_b128 a[140:143], v28 offset:22528\n" \
"v_mfma_f32_16x16x32_bf16 v[116:119], a[144:147], a[176:179], v[116:119]\n" \
"ds_read_b128 a[204:207], v29 offset:22528\n" \
"v_mfma_f32_16x16x32_bf16 v[116:119], a[208:211], a[240:243], v[116:119]\n" \
"s_addk_i32 s84, 0x1c00\n" \
"v_mfma_f32_16x16x32_bf16 v[120:123], a[144:147], a[180:183], v[120:123]\n" \
"buffer_load_dwordx4 v23, s[64:67], s84 offen offset:3072 lds\n" \
"v_mfma_f32_16x16x32_bf16 v[120:123], a[208:211], a[244:247], v[120:123]\n" \
"s_add_u32 m0, s31, 0xc000\n" \
"s_mov_b32 s84, 0x280000\n" \
"v_mfma_f32_16x16x32_bf16 v[124:127], a[144:147], a[184:187], v[124:127]\n" \
"buffer_load_dwordx4 v26, s[72:75], s84 offen offset:0 lds\n" \
"v_mfma_f32_16x16x32_bf16 v[124:127], a[208:211], a[248:251], v[124:127]\n" \
"s_addk_i32 s84, 0x7c00\n" \
"v_mfma_f32_16x16x32_bf16 v[128:131], a[144:147], a[188:191], v[128:131]\n" \
"buffer_load_dwordx4 v26, s[72:75], s84 offen offset:1024 lds\n" \
"v_mfma_f32_16x16x32_bf16 v[128:131], a[208:211], a[252:255], v[128:131]\n" \
"ds_read_b128 a[144:147], v28 offset:0\n" \
"v_mfma_f32_16x16x32_bf16 v[144:147], a[148:151], a[188:191], v[144:147]\n" \
"ds_read_b128 a[208:211], v29 offset:0\n" \
"v_mfma_f32_16x16x32_bf16 v[144:147], a[212:215], a[252:255], v[144:147]\n" \
"ds_read_b128 v[200:203], v199 offset:0\n" \
"v_mfma_f32_16x16x32_bf16 v[140:143], a[148:151], a[184:187], v[140:143]\n" \
"ds_read_b128 v[204:207], v199 offset:4096\n" \
"v_mfma_f32_16x16x32_bf16 v[140:143], a[212:215], a[248:251], v[140:143]\n" \
"ds_read_b64_tr_b16 a[226:227], v32 offset:9216\n" \
"v_mfma_f32_16x16x32_bf16 v[136:139], a[148:151], a[180:183], v[136:139]\n" \
"ds_read_b64_tr_b16 a[164:165], v33 offset:0\n" \
"v_mfma_f32_16x16x32_bf16 v[136:139], a[212:215], a[244:247], v[136:139]\n" \
"ds_read_b128 v[208:211], v199 offset:8192\n" \
"v_mfma_f32_16x16x32_bf16 v[132:135], a[148:151], a[176:179], v[132:135]\n" \
"ds_read_b64_tr_b16 a[166:167], v33 offset:1024\n" \
"v_mfma_f32_16x16x32_bf16 v[132:135], a[212:215], a[240:243], v[132:135]\n" \
"ds_read_b128 a[148:151], v28 offset:2048\n" \
"v_mfma_f32_16x16x32_bf16 v[148:151], a[152:155], a[176:179], v[148:151]\n" \
"ds_read_b128 a[212:215], v29 offset:2048\n" \
"v_mfma_f32_16x16x32_bf16 v[148:151], a[216:219], a[240:243], v[148:151]\n" \
"ds_read_b64_tr_b16 a[228:229], v33 offset:8192\n" \
"v_mfma_f32_16x16x32_bf16 v[152:155], a[152:155], a[180:183], v[152:155]\n" \
"ds_read_b64_tr_b16 a[230:231], v33 offset:9216\n" \
"v_mfma_f32_16x16x32_bf16 v[152:155], a[216:219], a[244:247], v[152:155]\n" \
"ds_read_b64_tr_b16 a[168:169], v34 offset:0\n" \
"v_mfma_f32_16x16x32_bf16 v[156:159], a[152:155], a[184:187], v[156:159]\n" \
"ds_read_b64_tr_b16 a[170:171], v34 offset:1024\n" \
"v_mfma_f32_16x16x32_bf16 v[156:159], a[216:219], a[248:251], v[156:159]\n" \
"ds_read_b64_tr_b16 a[232:233], v34 offset:8192\n" \
"v_mfma_f32_16x16x32_bf16 v[160:163], a[152:155], a[188:191], v[160:163]\n" \
"ds_read_b64_tr_b16 a[234:235], v34 offset:9216\n" \
"v_mfma_f32_16x16x32_bf16 v[160:163], a[216:219], a[252:255], v[160:163]\n" \
"ds_read_b128 a[152:155], v28 offset:4096\n" \
"v_mfma_f32_16x16x32_bf16 v[176:179], a[156:159], a[188:191], v[176:179]\n" \
"ds_read_b128 a[216:219], v29 offset:4096\n" \
"v_mfma_f32_16x16x32_bf16 v[176:179], a[220:223], a[252:255], v[176:179]\n" \
"ds_read_b64_tr_b16 a[172:173], v35 offset:0\n" \
"v_mfma_f32_16x16x32_bf16 v[172:175], a[156:159], a[184:187], v[172:175]\n" \
"ds_read_b64_tr_b16 a[174:175], v35 offset:1024\n" \
"v_mfma_f32_16x16x32_bf16 v[172:175], a[220:223], a[248:251], v[172:175]\n" \
"ds_read_b64_tr_b16 a[236:237], v35 offset:8192\n" \
"v_mfma_f32_16x16x32_bf16 v[168:171], a[156:159], a[180:183], v[168:171]\n" \
"ds_read_b64_tr_b16 a[238:239], v35 offset:9216\n" \
"v_mfma_f32_16x16x32_bf16 v[168:171], a[220:223], a[244:247], v[168:171]\n" \
"v_mfma_f32_16x16x32_bf16 v[164:167], a[156:159], a[176:179], v[164:167]\n" \
"v_mfma_f32_16x16x32_bf16 v[164:167], a[220:223], a[240:243], v[164:167]\n" \
"ds_read_b128 a[156:159], v28 offset:6144\n" \
"ds_read_b128 a[220:223], v29 offset:6144\n" \
"s_waitcnt lgkmcnt(15)\n" \
"v_mfma_f32_16x16x32_bf16 a[0:3], a[128:131], a[160:163], a[0:3]\n" \
"s_addk_i32 s84, 0x7c00\n" \
"v_mfma_f32_16x16x32_bf16 a[0:3], a[192:195], a[224:227], a[0:3]\n" \
"buffer_load_dwordx4 v27, s[72:75], s84 offen offset:2048 lds\n" \
"v_mfma_f32_16x16x32_bf16 a[4:7], a[128:131], a[164:167], a[4:7]\n" \
"ds_read_b64_tr_b16 a[176:177], v36 offset:0\n" \
"s_waitcnt lgkmcnt(13)\n" \
"v_mfma_f32_16x16x32_bf16 a[4:7], a[192:195], a[228:231], a[4:7]\n" \
"s_addk_i32 s84, 0x7c00\n" \
"s_waitcnt lgkmcnt(11)\n" \
"v_mfma_f32_16x16x32_bf16 a[8:11], a[128:131], a[168:171], a[8:11]\n" \
"buffer_load_dwordx4 v27, s[72:75], s84 offen offset:3072 lds\n" \
"s_waitcnt lgkmcnt(9)\n" \
"v_mfma_f32_16x16x32_bf16 a[8:11], a[192:195], a[232:235], a[8:11]\n" \
"ds_read_b64_tr_b16 a[178:179], v36 offset:1024\n" \
"s_waitcnt lgkmcnt(6)\n" \
"v_mfma_f32_16x16x32_bf16 a[12:15], a[128:131], a[172:175], a[12:15]\n" \
"ds_read_b64_tr_b16 a[240:241], v36 offset:8192\n" \
"s_waitcnt lgkmcnt(5)\n" \
"v_mfma_f32_16x16x32_bf16 a[12:15], a[192:195], a[236:239], a[12:15]\n" \
"v_mfma_f32_16x16x32_bf16 a[28:31], a[132:135], a[172:175], a[28:31]\n" \
"v_mfma_f32_16x16x32_bf16 a[28:31], a[196:199], a[236:239], a[28:31]\n" \
"ds_read_b64_tr_b16 a[242:243], v36 offset:9216\n" \
"v_mfma_f32_16x16x32_bf16 a[24:27], a[132:135], a[168:171], a[24:27]\n" \
"v_mfma_f32_16x16x32_bf16 a[24:27], a[196:199], a[232:235], a[24:27]\n" \
"v_mfma_f32_16x16x32_bf16 a[20:23], a[132:135], a[164:167], a[20:23]\n" \
"v_mfma_f32_16x16x32_bf16 a[20:23], a[196:199], a[228:231], a[20:23]\n" \
"ds_read_b64_tr_b16 a[180:181], v37 offset:0\n" \
"v_mfma_f32_16x16x32_bf16 a[16:19], a[132:135], a[160:163], a[16:19]\n" \
"v_mfma_f32_16x16x32_bf16 a[16:19], a[196:199], a[224:227], a[16:19]\n" \
"v_mfma_f32_16x16x32_bf16 a[32:35], a[136:139], a[160:163], a[32:35]\n" \
"ds_read_b64_tr_b16 a[182:183], v37 offset:1024\n" \
"v_mfma_f32_16x16x32_bf16 a[32:35], a[200:203], a[224:227], a[32:35]\n" \
"v_mfma_f32_16x16x32_bf16 a[36:39], a[136:139], a[164:167], a[36:39]\n" \
"v_mfma_f32_16x16x32_bf16 a[36:39], a[200:203], a[228:231], a[36:39]\n" \
"buffer_store_dwordx4 v[200:203], v20, s[80:83], s35 offen sc1\n" \
"v_mfma_f32_16x16x32_bf16 a[40:43], a[136:139], a[168:171], a[40:43]\n" \
"s_mov_b32 m0, s32\n" \
"ds_read_b64_tr_b16 a[244:245], v37 offset:8192\n" \
"v_mfma_f32_16x16x32_bf16 a[40:43], a[200:203], a[232:235], a[40:43]\n" \
"s_mov_b32 exec_lo, 0x0\n" \
"s_mov_b32 exec_hi, 0xffffffff\n" \
"global_load_lds_dword v[188:189], off\n" \
"s_mov_b64 exec, -1\n" \
"v_mfma_f32_16x16x32_bf16 a[44:47], a[136:139], a[172:175], a[44:47]\n" \
"ds_read_b64_tr_b16 a[246:247], v37 offset:9216\n" \
"v_mfma_f32_16x16x32_bf16 a[44:47], a[200:203], a[236:239], a[44:47]\n" \
"v_mfma_f32_16x16x32_bf16 a[60:63], a[140:143], a[172:175], a[60:63]\n" \
"v_mfma_f32_16x16x32_bf16 a[60:63], a[204:207], a[236:239], a[60:63]\n" \
"v_mfma_f32_16x16x32_bf16 a[56:59], a[140:143], a[168:171], a[56:59]\n" \
"ds_read_b64_tr_b16 a[184:185], v38 offset:0\n" \
"v_mfma_f32_16x16x32_bf16 a[56:59], a[204:207], a[232:235], a[56:59]\n" \
"v_mfma_f32_16x16x32_bf16 a[52:55], a[140:143], a[164:167], a[52:55]\n" \
"buffer_store_dwordx4 v[204:207], v20, s[80:83], s88 offen sc1\n" \
"v_mfma_f32_16x16x32_bf16 a[52:55], a[204:207], a[228:231], a[52:55]\n" \
"ds_read_b64_tr_b16 a[186:187], v38 offset:1024\n" \
"v_mfma_f32_16x16x32_bf16 a[48:51], a[140:143], a[160:163], a[48:51]\n" \
"v_mfma_f32_16x16x32_bf16 a[48:51], a[204:207], a[224:227], a[48:51]\n" \
"v_mfma_f32_16x16x32_bf16 a[64:67], a[144:147], a[160:163], a[64:67]\n" \
"ds_read_b64_tr_b16 a[248:249], v38 offset:8192\n" \
"v_mfma_f32_16x16x32_bf16 a[64:67], a[208:211], a[224:227], a[64:67]\n" \
"v_mfma_f32_16x16x32_bf16 a[68:71], a[144:147], a[164:167], a[68:71]\n" \
"v_mfma_f32_16x16x32_bf16 a[68:71], a[208:211], a[228:231], a[68:71]\n" \
"ds_read_b64_tr_b16 a[250:251], v38 offset:9216\n" \
"v_mfma_f32_16x16x32_bf16 a[72:75], a[144:147], a[168:171], a[72:75]\n" \
"v_mfma_f32_16x16x32_bf16 a[72:75], a[208:211], a[232:235], a[72:75]\n" \
"v_mfma_f32_16x16x32_bf16 a[76:79], a[144:147], a[172:175], a[76:79]\n" \
"buffer_store_dwordx4 v[208:211], v20, s[80:83], s36 offen sc1\n" \
"v_mfma_f32_16x16x32_bf16 a[76:79], a[208:211], a[236:239], a[76:79]\n" \
"ds_read_b64_tr_b16 a[188:189], v39 offset:0\n" \
"v_mfma_f32_16x16x32_bf16 a[92:95], a[148:151], a[172:175], a[92:95]\n" \
"v_mfma_f32_16x16x32_bf16 a[92:95], a[212:215], a[236:239], a[92:95]\n" \
"v_mfma_f32_16x16x32_bf16 a[88:91], a[148:151], a[168:171], a[88:91]\n" \
"ds_read_b64_tr_b16 a[190:191], v39 offset:1024\n" \
"v_mfma_f32_16x16x32_bf16 a[88:91], a[212:215], a[232:235], a[88:91]\n" \
"v_mfma_f32_16x16x32_bf16 a[84:87], a[148:151], a[164:167], a[84:87]\n" \
"v_mfma_f32_16x16x32_bf16 a[84:87], a[212:215], a[228:231], a[84:87]\n" \
"ds_read_b64_tr_b16 a[252:253], v39 offset:8192\n" \
"v_mfma_f32_16x16x32_bf16 a[80:83], a[148:151], a[160:163], a[80:83]\n" \
"v_mfma_f32_16x16x32_bf16 a[80:83], a[212:215], a[224:227], a[80:83]\n" \
"v_mfma_f32_16x16x32_bf16 a[96:99], a[152:155], a[160:163], a[96:99]\n" \
"ds_read_b64_tr_b16 a[254:255], v39 offset:9216\n" \
"v_mfma_f32_16x16x32_bf16 a[96:99], a[216:219], a[224:227], a[96:99]\n" \
"v_mfma_f32_16x16x32_bf16 a[100:103], a[152:155], a[164:167], a[100:103]\n" \
"v_mfma_f32_16x16x32_bf16 a[100:103], a[216:219], a[228:231], a[100:103]\n" \
".fill (L_pb + 52960 - .) / 4, 4, 0xBF800000\n" \
"L_pp13:\n" \
"s_cbranch_execz L_pret\n" \
"v_mfma_f32_16x16x32_bf16 a[104:107], a[152:155], a[168:171], a[104:107]\n" \
"v_mfma_f32_16x16x32_bf16 a[104:107], a[216:219], a[232:235], a[104:107]\n" \
"v_mfma_f32_16x16x32_bf16 a[108:111], a[152:155], a[172:175], a[108:111]\n" \
"v_mfma_f32_16x16x32_bf16 a[108:111], a[216:219], a[236:239], a[108:111]\n" \
"s_waitcnt lgkmcnt(15)\n" \
"v_mfma_f32_16x16x32_bf16 a[124:127], a[156:159], a[172:175], a[124:127]\n" \
"v_mfma_f32_16x16x32_bf16 a[124:127], a[220:223], a[236:239], a[124:127]\n" \
"v_mfma_f32_16x16x32_bf16 a[120:123], a[156:159], a[168:171], a[120:123]\n" \
"v_mfma_f32_16x16x32_bf16 a[120:123], a[220:223], a[232:235], a[120:123]\n" \
"v_mfma_f32_16x16x32_bf16 a[116:119], a[156:159], a[164:167], a[116:119]\n" \
"v_mfma_f32_16x16x32_bf16 a[116:119], a[220:223], a[228:231], a[116:119]\n" \
"v_mfma_f32_16x16x32_bf16 a[112:115], a[156:159], a[160:163], a[112:115]\n" \
"v_mfma_f32_16x16x32_bf16 a[112:115], a[220:223], a[224:227], a[112:115]\n" \
"s_waitcnt vmcnt(4) lgkmcnt(0)\n" \
"s_barrier\n" \
"v_mfma_f32_16x16x32_bf16 v[52:55], a[128:131], a[176:179], v[52:55]\n" \
"s_add_u32 m0, s30, 0x0\n" \
"s_movk_i32 s84, 0x300\n" \
"v_mfma_f32_16x16x32_bf16 v[52:55], a[192:195], a[240:243], v[52:55]\n" \
"buffer_load_dwordx4 v22, s[64:67], s84 offen offset:0 lds\n" \
"v_mfma_f32_16x16x32_bf16 v[56:59], a[128:131], a[180:183], v[56:59]\n" \
"ds_read_b64_tr_b16 a[160:161], v32 offset:32768\n" \
"v_mfma_f32_16x16x32_bf16 v[56:59], a[192:195], a[244:247], v[56:59]\n" \
"s_movk_i32 s84, 0x1f00\n" \
"s_nop 0\n" \
"v_mfma_f32_16x16x32_bf16 v[60:63], a[128:131], a[184:187], v[60:63]\n" \
"buffer_load_dwordx4 v23, s[64:67], s84 offen offset:1024 lds\n" \
"v_mfma_f32_16x16x32_bf16 v[60:63], a[192:195], a[248:251], v[60:63]\n" \
"ds_read_b64_tr_b16 a[162:163], v32 offset:33792\n" \
"v_mfma_f32_16x16x32_bf16 v[64:67], a[128:131], a[188:191], v[64:67]\n" \
"ds_read_b64_tr_b16 a[224:225], v32 offset:40960\n" \
"v_mfma_f32_16x16x32_bf16 v[64:67], a[192:195], a[252:255], v[64:67]\n" \
"ds_read_b128 a[128:131], v28 offset:49152\n" \
"v_mfma_f32_16x16x32_bf16 v[80:83], a[132:135], a[188:191], v[80:83]\n" \
"ds_read_b128 a[192:195], v29 offset:49152\n" \
"v_mfma_f32_16x16x32_bf16 v[80:83], a[196:199], a[252:255], v[80:83]\n" \
"s_movk_i32 s84, 0x3b00\n" \
"v_mfma_f32_16x16x32_bf16 v[76:79], a[132:135], a[184:187], v[76:79]\n" \
"buffer_load_dwordx4 v22, s[64:67], s84 offen offset:2048 lds\n" \
"v_mfma_f32_16x16x32_bf16 v[76:79], a[196:199], a[248:251], v[76:79]\n" \
"s_movk_i32 s84, 0x5700\n" \
"v_mfma_f32_16x16x32_bf16 v[72:75], a[132:135], a[180:183], v[72:75]\n" \
"buffer_load_dwordx4 v23, s[64:67], s84 offen offset:3072 lds\n" \
"v_mfma_f32_16x16x32_bf16 v[72:75], a[196:199], a[244:247], v[72:75]\n" \
"s_add_u32 m0, s31, 0x0\n" \
"s_mov_b32 s84, 0x300000\n" \
"v_mfma_f32_16x16x32_bf16 v[68:71], a[132:135], a[176:179], v[68:71]\n" \
"buffer_load_dwordx4 v26, s[68:71], s84 offen offset:0 lds\n" \
"v_mfma_f32_16x16x32_bf16 v[68:71], a[196:199], a[240:243], v[68:71]\n" \
"ds_read_b128 a[132:135], v28 offset:51200\n" \
"v_mfma_f32_16x16x32_bf16 v[84:87], a[136:139], a[176:179], v[84:87]\n" \
"ds_read_b128 a[196:199], v29 offset:51200\n" \
"v_mfma_f32_16x16x32_bf16 v[84:87], a[200:203], a[240:243], v[84:87]\n" \
"s_addk_i32 s84, 0x7c00\n" \
"v_mfma_f32_16x16x32_bf16 v[88:91], a[136:139], a[180:183], v[88:91]\n" \
"buffer_load_dwordx4 v26, s[68:71], s84 offen offset:1024 lds\n" \
"v_mfma_f32_16x16x32_bf16 v[88:91], a[200:203], a[244:247], v[88:91]\n" \
"ds_read_b64_tr_b16 a[226:227], v32 offset:41984\n" \
"v_mfma_f32_16x16x32_bf16 v[92:95], a[136:139], a[184:187], v[92:95]\n" \
"s_addk_i32 s84, 0x7c00\n" \
"v_mfma_f32_16x16x32_bf16 v[92:95], a[200:203], a[248:251], v[92:95]\n" \
"buffer_load_dwordx4 v27, s[68:71], s84 offen offset:2048 lds\n" \
"v_mfma_f32_16x16x32_bf16 v[96:99], a[136:139], a[188:191], v[96:99]\n" \
"ds_read_b64_tr_b16 a[164:165], v33 offset:32768\n" \
"v_mfma_f32_16x16x32_bf16 v[96:99], a[200:203], a[252:255], v[96:99]\n" \
"ds_read_b128 a[136:139], v28 offset:53248\n" \
"v_mfma_f32_16x16x32_bf16 v[112:115], a[140:143], a[188:191], v[112:115]\n" \
"ds_read_b128 a[200:203], v29 offset:53248\n" \
"v_mfma_f32_16x16x32_bf16 v[112:115], a[204:207], a[252:255], v[112:115]\n" \
"s_addk_i32 s84, 0x7c00\n" \
"v_mfma_f32_16x16x32_bf16 v[108:111], a[140:143], a[184:187], v[108:111]\n" \
"buffer_load_dwordx4 v27, s[68:71], s84 offen offset:3072 lds\n" \
"v_mfma_f32_16x16x32_bf16 v[108:111], a[204:207], a[248:251], v[108:111]\n" \
"s_add_u32 m0, s30, 0x1000\n" \
"s_mov_b32 s84, 0x8300\n" \
"v_mfma_f32_16x16x32_bf16 v[104:107], a[140:143], a[180:183], v[104:107]\n" \
"buffer_load_dwordx4 v22, s[64:67], s84 offen offset:0 lds\n" \
"v_mfma_f32_16x16x32_bf16 v[104:107], a[204:207], a[244:247], v[104:107]\n" \
"ds_read_b64_tr_b16 a[166:167], v33 offset:33792\n" \
"v_mfma_f32_16x16x32_bf16 v[100:103], a[140:143], a[176:179], v[100:103]\n" \
"ds_read_b64_tr_b16 a[228:229], v33 offset:40960\n" \
"v_mfma_f32_16x16x32_bf16 v[100:103], a[204:207], a[240:243], v[100:103]\n" \
"ds_read_b128 a[140:143], v28 offset:55296\n" \
"v_mfma_f32_16x16x32_bf16 v[116:119], a[144:147], a[176:179], v[116:119]\n" \
"ds_read_b128 a[204:207], v29 offset:55296\n" \
"v_mfma_f32_16x16x32_bf16 v[116:119], a[208:211], a[240:243], v[116:119]\n" \
"s_addk_i32 s84, 0x1c00\n" \
"v_mfma_f32_16x16x32_bf16 v[120:123], a[144:147], a[180:183], v[120:123]\n" \
"buffer_load_dwordx4 v23, s[64:67], s84 offen offset:1024 lds\n" \
"v_mfma_f32_16x16x32_bf16 v[120:123], a[208:211], a[244:247], v[120:123]\n" \
"s_addk_i32 s84, 0x1c00\n" \
"v_mfma_f32_16x16x32_bf16 v[124:127], a[144:147], a[184:187], v[124:127]\n" \
"buffer_load_dwordx4 v22, s[64:67], s84 offen offset:2048 lds\n" \
"v_mfma_f32_16x16x32_bf16 v[124:127], a[208:211], a[248:251], v[124:127]\n" \
"s_addk_i32 s84, 0x1c00\n" \
"s_nop 0\n" \
"v_mfma_f32_16x16x32_bf16 v[128:131], a[144:147], a[188:191], v[128:131]\n" \
"buffer_load_dwordx4 v23, s[64:67], s84 offen offset:3072 lds\n" \
"v_mfma_f32_16x16x32_bf16 v[128:131], a[208:211], a[252:255], v[128:131]\n" \
"ds_read_b128 a[144:147], v28 offset:32768\n" \
"v_mfma_f32_16x16x32_bf16 v[144:147], a[148:151], a[188:191], v[144:147]\n" \
"ds_read_b128 a[208:211], v29 offset:32768\n" \
"v_mfma_f32_16x16x32_bf16 v[144:147], a[212:215], a[252:255], v[144:147]\n" \
"s_add_u32 m0, s31, 0x4000\n" \
"s_mov_b32 s84, 0x300000\n" \
"v_mfma_f32_16x16x32_bf16 v[140:143], a[148:151], a[184:187], v[140:143]\n" \
"buffer_load_dwordx4 v26, s[72:75], s84 offen offset:0 lds\n" \
"v_mfma_f32_16x16x32_bf16 v[140:143], a[212:215], a[248:251], v[140:143]\n" \
"ds_read_b64_tr_b16 a[230:231], v33 offset:41984\n" \
"v_mfma_f32_16x16x32_bf16 v[136:139], a[148:151], a[180:183], v[136:139]\n" \
"s_addk_i32 s84, 0x7c00\n" \
"s_nop 0\n" \
"v_mfma_f32_16x16x32_bf16 v[136:139], a[212:215], a[244:247], v[136:139]\n" \
"buffer_load_dwordx4 v26, s[72:75], s84 offen offset:1024 lds\n" \
"v_mfma_f32_16x16x32_bf16 v[132:135], a[148:151], a[176:179], v[132:135]\n" \
"ds_read_b64_tr_b16 a[168:169], v34 offset:32768\n" \
"v_mfma_f32_16x16x32_bf16 v[132:135], a[212:215], a[240:243], v[132:135]\n" \
"ds_read_b128 a[148:151], v28 offset:34816\n" \
"v_mfma_f32_16x16x32_bf16 v[148:151], a[152:155], a[176:179], v[148:151]\n" \
"ds_read_b128 a[212:215], v29 offset:34816\n" \
"v_mfma_f32_16x16x32_bf16 v[148:151], a[216:219], a[240:243], v[148:151]\n" \
"ds_read_b64_tr_b16 a[170:171], v34 offset:33792\n" \
"v_mfma_f32_16x16x32_bf16 v[152:155], a[152:155], a[180:183], v[152:155]\n" \
"ds_read_b64_tr_b16 a[232:233], v34 offset:40960\n" \
"v_mfma_f32_16x16x32_bf16 v[152:155], a[216:219], a[244:247], v[152:155]\n" \
"ds_read_b64_tr_b16 a[234:235], v34 offset:41984\n" \
"v_mfma_f32_16x16x32_bf16 v[156:159], a[152:155], a[184:187], v[156:159]\n" \
"ds_read_b64_tr_b16 a[172:173], v35 offset:32768\n" \
"v_mfma_f32_16x16x32_bf16 v[156:159], a[216:219], a[248:251], v[156:159]\n" \
"ds_read_b64_tr_b16 a[174:175], v35 offset:33792\n" \
"v_mfma_f32_16x16x32_bf16 v[160:163], a[152:155], a[188:191], v[160:163]\n" \
"ds_read_b64_tr_b16 a[236:237], v35 offset:40960\n" \
"v_mfma_f32_16x16x32_bf16 v[160:163], a[216:219], a[252:255], v[160:163]\n" \
"ds_read_b128 a[152:155], v28 offset:36864\n" \
"v_mfma_f32_16x16x32_bf16 v[176:179], a[156:159], a[188:191], v[176:179]\n" \
"ds_read_b128 a[216:219], v29 offset:36864\n" \
"v_mfma_f32_16x16x32_bf16 v[176:179], a[220:223], a[252:255], v[176:179]\n" \
"ds_read_b64_tr_b16 a[238:239], v35 offset:41984\n" \
"v_mfma_f32_16x16x32_bf16 v[172:175], a[156:159], a[184:187], v[172:175]\n" \
"v_mfma_f32_16x16x32_bf16 v[172:175], a[220:223], a[248:251], v[172:175]\n" \
"v_mfma_f32_16x16x32_bf16 v[168:171], a[156:159], a[180:183], v[168:171]\n" \
"v_mfma_f32_16x16x32_bf16 v[168:171], a[220:223], a[244:247], v[168:171]\n" \
"v_mfma_f32_16x16x32_bf16 v[164:167], a[156:159], a[176:179], v[164:167]\n" \
"v_mfma_f32_16x16x32_bf16 v[164:167], a[220:223], a[240:243], v[164:167]\n" \
"ds_read_b128 a[156:159], v28 offset:38912\n" \
"ds_read_b128 a[220:223], v29 offset:38912\n" \
"s_waitcnt lgkmcnt(15)\n" \
"v_mfma_f32_16x16x32_bf16 a[0:3], a[128:131], a[160:163], a[0:3]\n" \
"s_addk_i32 s84, 0x7c00\n" \
"v_mfma_f32_16x16x32_bf16 a[0:3], a[192:195], a[224:227], a[0:3]\n" \
"buffer_load_dwordx4 v27, s[72:75], s84 offen offset:2048 lds\n" \
"v_mfma_f32_16x16x32_bf16 a[4:7], a[128:131], a[164:167], a[4:7]\n" \
"ds_read_b64_tr_b16 a[176:177], v36 offset:32768\n" \
"s_waitcnt lgkmcnt(15)\n" \
"v_mfma_f32_16x16x32_bf16 a[4:7], a[192:195], a[228:231], a[4:7]\n" \
"s_addk_i32 s84, 0x7c00\n" \
"s_waitcnt lgkmcnt(11)\n" \
"v_mfma_f32_16x16x32_bf16 a[8:11], a[128:131], a[168:171], a[8:11]\n" \
"buffer_load_dwordx4 v27, s[72:75], s84 offen offset:3072 lds\n" \
"s_waitcnt lgkmcnt(9)\n" \
"v_mfma_f32_16x16x32_bf16 a[8:11], a[192:195], a[232:235], a[8:11]\n" \
"ds_read_b64_tr_b16 a[178:179], v36 offset:33792\n" \
"s_waitcnt lgkmcnt(8)\n" \
"v_mfma_f32_16x16x32_bf16 a[12:15], a[128:131], a[172:175], a[12:15]\n" \
"ds_read_b64_tr_b16 a[240:241], v36 offset:40960\n" \
"s_waitcnt lgkmcnt(5)\n" \
"v_mfma_f32_16x16x32_bf16 a[12:15], a[192:195], a[236:239], a[12:15]\n" \
"v_mfma_f32_16x16x32_bf16 a[28:31], a[132:135], a[172:175], a[28:31]\n" \
"v_mfma_f32_16x16x32_bf16 a[28:31], a[196:199], a[236:239], a[28:31]\n" \
"ds_read_b64_tr_b16 a[242:243], v36 offset:41984\n" \
"v_mfma_f32_16x16x32_bf16 a[24:27], a[132:135], a[168:171], a[24:27]\n" \
"v_mfma_f32_16x16x32_bf16 a[24:27], a[196:199], a[232:235], a[24:27]\n" \
"v_mfma_f32_16x16x32_bf16 a[20:23], a[132:135], a[164:167], a[20:23]\n" \
"v_mfma_f32_16x16x32_bf16 a[20:23], a[196:199], a[228:231], a[20:23]\n" \
"ds_read_b64_tr_b16 a[180:181], v37 offset:32768\n" \
"v_mfma_f32_16x16x32_bf16 a[16:19], a[132:135], a[160:163], a[16:19]\n" \
"v_mfma_f32_16x16x32_bf16 a[16:19], a[196:199], a[224:227], a[16:19]\n" \
"v_mfma_f32_16x16x32_bf16 a[32:35], a[136:139], a[160:163], a[32:35]\n" \
"ds_read_b64_tr_b16 a[182:183], v37 offset:33792\n" \
"v_mfma_f32_16x16x32_bf16 a[32:35], a[200:203], a[224:227], a[32:35]\n" \
"v_mfma_f32_16x16x32_bf16 a[36:39], a[136:139], a[164:167], a[36:39]\n" \
"v_lshlrev_b32_e32 v45, 3, v20\n" \
"v_add_u32_e32 v45, s31, v45\n" \
"v_mfma_f32_16x16x32_bf16 a[36:39], a[200:203], a[228:231], a[36:39]\n" \
"s_getpc_b64 s[86:87]\n" \
"s_sub_u32 s86, s86, 0x10800\n" \
"s_subb_u32 s87, s87, 0\n" \
"s_mov_b32 m0, s32\n" \
"s_nop 0\n" \
"v_mfma_f32_16x16x32_bf16 a[40:43], a[136:139], a[168:171], a[40:43]\n" \
"global_load_lds_dword v45, s[86:87]\n" \
"v_mfma_f32_16x16x32_bf16 a[40:43], a[200:203], a[232:235], a[40:43]\n" \
"ds_read_b64_tr_b16 a[244:245], v37 offset:40960\n" \
"v_mfma_f32_16x16x32_bf16 a[44:47], a[136:139], a[172:175], a[44:47]\n" \
"ds_read_b64_tr_b16 a[246:247], v37 offset:41984\n" \
"v_mfma_f32_16x16x32_bf16 a[44:47], a[200:203], a[236:239], a[44:47]\n" \
"v_mfma_f32_16x16x32_bf16 a[60:63], a[140:143], a[172:175], a[60:63]\n" \
"v_mfma_f32_16x16x32_bf16 a[60:63], a[204:207], a[236:239], a[60:63]\n" \
"v_mfma_f32_16x16x32_bf16 a[56:59], a[140:143], a[168:171], a[56:59]\n" \
"ds_read_b64_tr_b16 a[184:185], v38 offset:32768\n" \
"v_mfma_f32_16x16x32_bf16 a[56:59], a[204:207], a[232:235], a[56:59]\n" \
"v_mfma_f32_16x16x32_bf16 a[52:55], a[140:143], a[164:167], a[52:55]\n" \
"v_mfma_f32_16x16x32_bf16 a[52:55], a[204:207], a[228:231], a[52:55]\n" \
"ds_read_b64_tr_b16 a[186:187], v38 offset:33792\n" \
"v_mfma_f32_16x16x32_bf16 a[48:51], a[140:143], a[160:163], a[48:51]\n" \
"v_mfma_f32_16x16x32_bf16 a[48:51], a[204:207], a[224:227], a[48:51]\n" \
"v_mfma_f32_16x16x32_bf16 a[64:67], a[144:147], a[160:163], a[64:67]\n" \
"ds_read_b64_tr_b16 a[248:249], v38 offset:40960\n" \
"v_mfma_f32_16x16x32_bf16 a[64:67], a[208:211], a[224:227], a[64:67]\n" \
"v_mfma_f32_16x16x32_bf16 a[68:71], a[144:147], a[164:167], a[68:71]\n" \
"v_mfma_f32_16x16x32_bf16 a[68:71], a[208:211], a[228:231], a[68:71]\n" \
"ds_read_b64_tr_b16 a[250:251], v38 offset:41984\n" \
"v_mfma_f32_16x16x32_bf16 a[72:75], a[144:147], a[168:171], a[72:75]\n" \
"v_mfma_f32_16x16x32_bf16 a[72:75], a[208:211], a[232:235], a[72:75]\n" \
"v_mfma_f32_16x16x32_bf16 a[76:79], a[144:147], a[172:175], a[76:79]\n" \
"v_mfma_f32_16x16x32_bf16 a[76:79], a[208:211], a[236:239], a[76:79]\n" \
"ds_read_b64_tr_b16 a[188:189], v39 offset:32768\n" \
"v_mfma_f32_16x16x32_bf16 a[92:95], a[148:151], a[172:175], a[92:95]\n" \
"v_mfma_f32_16x16x32_bf16 a[92:95], a[212:215], a[236:239], a[92:95]\n" \
"v_mfma_f32_16x16x32_bf16 a[88:91], a[148:151], a[168:171], a[88:91]\n" \
"ds_read_b64_tr_b16 a[190:191], v39 offset:33792\n" \
"v_mfma_f32_16x16x32_bf16 a[88:91], a[212:215], a[232:235], a[88:91]\n" \
"v_mfma_f32_16x16x32_bf16 a[84:87], a[148:151], a[164:167], a[84:87]\n" \
"v_mfma_f32_16x16x32_bf16 a[84:87], a[212:215], a[228:231], a[84:87]\n" \
"ds_read_b64_tr_b16 a[252:253], v39 offset:40960\n" \
"v_mfma_f32_16x16x32_bf16 a[80:83], a[148:151], a[160:163], a[80:83]\n" \
"v_mfma_f32_16x16x32_bf16 a[80:83], a[212:215], a[224:227], a[80:83]\n" \
"v_mfma_f32_16x16x32_bf16 a[96:99], a[152:155], a[160:163], a[96:99]\n" \
"ds_read_b64_tr_b16 a[254:255], v39 offset:41984\n" \
"v_mfma_f32_16x16x32_bf16 a[96:99], a[216:219], a[224:227], a[96:99]\n" \
"v_mfma_f32_16x16x32_bf16 a[100:103], a[152:155], a[164:167], a[100:103]\n" \
"v_mfma_f32_16x16x32_bf16 a[100:103], a[216:219], a[228:231], a[100:103]\n" \
"v_mfma_f32_16x16x32_bf16 a[104:107], a[152:155], a[168:171], a[104:107]\n" \
"v_mfma_f32_16x16x32_bf16 a[104:107], a[216:219], a[232:235], a[104:107]\n" \
"v_mfma_f32_16x16x32_bf16 a[108:111], a[152:155], a[172:175], a[108:111]\n" \
"v_mfma_f32_16x16x32_bf16 a[108:111], a[216:219], a[236:239], a[108:111]\n" \
"s_waitcnt lgkmcnt(15)\n" \
"v_mfma_f32_16x16x32_bf16 a[124:127], a[156:159], a[172:175], a[124:127]\n" \
"v_mfma_f32_16x16x32_bf16 a[124:127], a[220:223], a[236:239], a[124:127]\n" \
"v_mfma_f32_16x16x32_bf16 a[120:123], a[156:159], a[168:171], a[120:123]\n" \
"v_mfma_f32_16x16x32_bf16 a[120:123], a[220:223], a[232:235], a[120:123]\n" \
"v_mfma_f32_16x16x32_bf16 a[116:119], a[156:159], a[164:167], a[116:119]\n" \
"v_mfma_f32_16x16x32_bf16 a[116:119], a[220:223], a[228:231], a[116:119]\n" \
"v_mfma_f32_16x16x32_bf16 a[112:115], a[156:159], a[160:163], a[112:115]\n" \
"v_mfma_f32_16x16x32_bf16 a[112:115], a[220:223], a[224:227], a[112:115]\n" \
"s_waitcnt vmcnt(1) lgkmcnt(0)\n" \
"s_barrier\n" \
"v_mov_b32_e32 v45, s62\n" \
"s_mov_b64 exec, 1\n" \
"buffer_store_dword v45, off, s[80:83], s37 offset:128 sc1\n" \
"s_mov_b64 exec, -1\n" \
"v_mfma_f32_16x16x32_bf16 v[52:55], a[128:131], a[176:179], v[52:55]\n" \
"s_add_u32 m0, s30, 0x8000\n" \
"s_movk_i32 s84, 0x380\n" \
"v_mfma_f32_16x16x32_bf16 v[52:55], a[192:195], a[240:243], v[52:55]\n" \
"buffer_load_dwordx4 v22, s[64:67], s84 offen offset:0 lds\n" \
"v_mfma_f32_16x16x32_bf16 v[56:59], a[128:131], a[180:183], v[56:59]\n" \
"ds_read_b64_tr_b16 a[160:161], v32 offset:0\n" \
"v_mfma_f32_16x16x32_bf16 v[56:59], a[192:195], a[244:247], v[56:59]\n" \
"s_movk_i32 s84, 0x1f80\n" \
"v_mfma_f32_16x16x32_bf16 v[60:63], a[128:131], a[184:187], v[60:63]\n" \
"buffer_load_dwordx4 v23, s[64:67], s84 offen offset:1024 lds\n" \
"v_mfma_f32_16x16x32_bf16 v[60:63], a[192:195], a[248:251], v[60:63]\n" \
"ds_read_b64_tr_b16 a[162:163], v32 offset:1024\n" \
"v_mfma_f32_16x16x32_bf16 v[64:67], a[128:131], a[188:191], v[64:67]\n" \
"ds_read_b64_tr_b16 a[224:225], v32 offset:8192\n" \
"v_mfma_f32_16x16x32_bf16 v[64:67], a[192:195], a[252:255], v[64:67]\n" \
"ds_read_b128 a[128:131], v28 offset:16384\n" \
"v_mfma_f32_16x16x32_bf16 v[80:83], a[132:135], a[188:191], v[80:83]\n" \
"ds_read_b128 a[192:195], v29 offset:16384\n" \
"v_mfma_f32_16x16x32_bf16 v[80:83], a[196:199], a[252:255], v[80:83]\n" \
"s_movk_i32 s84, 0x3b80\n" \
"v_mfma_f32_16x16x32_bf16 v[76:79], a[132:135], a[184:187], v[76:79]\n" \
"buffer_load_dwordx4 v22, s[64:67], s84 offen offset:2048 lds\n" \
"v_mfma_f32_16x16x32_bf16 v[76:79], a[196:199], a[248:251], v[76:79]\n" \
"s_movk_i32 s84, 0x5780\n" \
"v_mfma_f32_16x16x32_bf16 v[72:75], a[132:135], a[180:183], v[72:75]\n" \
"buffer_load_dwordx4 v23, s[64:67], s84 offen offset:3072 lds\n" \
"v_mfma_f32_16x16x32_bf16 v[72:75], a[196:199], a[244:247], v[72:75]\n" \
"s_add_u32 m0, s31, 0x8000\n" \
"s_mov_b32 s84, 0x380000\n" \
"v_mfma_f32_16x16x32_bf16 v[68:71], a[132:135], a[176:179], v[68:71]\n" \
"buffer_load_dwordx4 v26, s[68:71], s84 offen offset:0 lds\n" \
"v_mfma_f32_16x16x32_bf16 v[68:71], a[196:199], a[240:243], v[68:71]\n" \
"ds_read_b128 a[132:135], v28 offset:18432\n" \
"v_mfma_f32_16x16x32_bf16 v[84:87], a[136:139], a[176:179], v[84:87]\n" \
"ds_read_b128 a[196:199], v29 offset:18432\n" \
"v_mfma_f32_16x16x32_bf16 v[84:87], a[200:203], a[240:243], v[84:87]\n" \
"s_addk_i32 s84, 0x7c00\n" \
"v_mfma_f32_16x16x32_bf16 v[88:91], a[136:139], a[180:183], v[88:91]\n" \
"buffer_load_dwordx4 v26, s[68:71], s84 offen offset:1024 lds\n" \
"v_mfma_f32_16x16x32_bf16 v[88:91], a[200:203], a[244:247], v[88:91]\n" \
"ds_read_b64_tr_b16 a[226:227], v32 offset:9216\n" \
"v_mfma_f32_16x16x32_bf16 v[92:95], a[136:139], a[184:187], v[92:95]\n" \
"s_addk_i32 s84, 0x7c00\n" \
"v_mfma_f32_16x16x32_bf16 v[92:95], a[200:203], a[248:251], v[92:95]\n" \
"buffer_load_dwordx4 v27, s[68:71], s84 offen offset:2048 lds\n" \
"v_mfma_f32_16x16x32_bf16 v[96:99], a[136:139], a[188:191], v[96:99]\n" \
"ds_read_b64_tr_b16 a[164:165], v33 offset:0\n" \
"v_mfma_f32_16x16x32_bf16 v[96:99], a[200:203], a[252:255], v[96:99]\n" \
"ds_read_b128 a[136:139], v28 offset:20480\n" \
"v_mfma_f32_16x16x32_bf16 v[112:115], a[140:143], a[188:191], v[112:115]\n" \
"ds_read_b128 a[200:203], v29 offset:20480\n" \
"v_mfma_f32_16x16x32_bf16 v[112:115], a[204:207], a[252:255], v[112:115]\n" \
"s_addk_i32 s84, 0x7c00\n" \
"s_nop 0\n" \
"v_mfma_f32_16x16x32_bf16 v[108:111], a[140:143], a[184:187], v[108:111]\n" \
"buffer_load_dwordx4 v27, s[68:71], s84 offen offset:3072 lds\n" \
"v_mfma_f32_16x16x32_bf16 v[108:111], a[204:207], a[248:251], v[108:111]\n" \
"s_add_u32 m0, s30, 0x9000\n" \
"s_mov_b32 s84, 0x8380\n" \
"v_mfma_f32_16x16x32_bf16 v[104:107], a[140:143], a[180:183], v[104:107]\n" \
"buffer_load_dwordx4 v22, s[64:67], s84 offen offset:0 lds\n" \
"v_mfma_f32_16x16x32_bf16 v[104:107], a[204:207], a[244:247], v[104:107]\n" \
"ds_read_b64_tr_b16 a[166:167], v33 offset:1024\n" \
"v_mfma_f32_16x16x32_bf16 v[100:103], a[140:143], a[176:179], v[100:103]\n" \
"ds_read_b64_tr_b16 a[228:229], v33 offset:8192\n" \
"v_mfma_f32_16x16x32_bf16 v[100:103], a[204:207], a[240:243], v[100:103]\n" \
"ds_read_b128 a[140:143], v28 offset:22528\n" \
"v_mfma_f32_16x16x32_bf16 v[116:119], a[144:147], a[176:179], v[116:119]\n" \
"ds_read_b128 a[204:207], v29 offset:22528\n" \
"v_mfma_f32_16x16x32_bf16 v[116:119], a[208:211], a[240:243], v[116:119]\n" \
"s_addk_i32 s84, 0x1c00\n" \
"v_mfma_f32_16x16x32_bf16 v[120:123], a[144:147], a[180:183], v[120:123]\n" \
"buffer_load_dwordx4 v23, s[64:67], s84 offen offset:1024 lds\n" \
"v_mfma_f32_16x16x32_bf16 v[120:123], a[208:211], a[244:247], v[120:123]\n" \
"s_addk_i32 s84, 0x1c00\n" \
"v_mfma_f32_16x16x32_bf16 v[124:127], a[144:147], a[184:187], v[124:127]\n" \
"buffer_load_dwordx4 v22, s[64:67], s84 offen offset:2048 lds\n" \
"v_mfma_f32_16x16x32_bf16 v[124:127], a[208:211], a[248:251], v[124:127]\n" \
"s_addk_i32 s84, 0x1c00\n" \
"s_nop 0\n" \
"v_mfma_f32_16x16x32_bf16 v[128:131], a[144:147], a[188:191], v[128:131]\n" \
"buffer_load_dwordx4 v23, s[64:67], s84 offen offset:3072 lds\n" \
"v_mfma_f32_16x16x32_bf16 v[128:131], a[208:211], a[252:255], v[128:131]\n" \
"ds_read_b128 a[144:147], v28 offset:0\n" \
"v_mfma_f32_16x16x32_bf16 v[144:147], a[148:151], a[188:191], v[144:147]\n" \
"ds_read_b128 a[208:211], v29 offset:0\n" \
"v_mfma_f32_16x16x32_bf16 v[144:147], a[212:215], a[252:255], v[144:147]\n" \
"s_add_u32 m0, s31, 0xc000\n" \
"s_mov_b32 s84, 0x380000\n" \
"v_mfma_f32_16x16x32_bf16 v[140:143], a[148:151], a[184:187], v[140:143]\n" \
"buffer_load_dwordx4 v26, s[72:75], s84 offen offset:0 lds\n" \
"v_mfma_f32_16x16x32_bf16 v[140:143], a[212:215], a[248:251], v[140:143]\n" \
"ds_read_b64_tr_b16 a[230:231], v33 offset:9216\n" \
"v_mfma_f32_16x16x32_bf16 v[136:139], a[148:151], a[180:183], v[136:139]\n" \
"s_addk_i32 s84, 0x7c00\n" \
"s_nop 0\n" \
"v_mfma_f32_16x16x32_bf16 v[136:139], a[212:215], a[244:247], v[136:139]\n" \
"buffer_load_dwordx4 v26, s[72:75], s84 offen offset:1024 lds\n" \
"v_mfma_f32_16x16x32_bf16 v[132:135], a[148:151], a[176:179], v[132:135]\n" \
"ds_read_b64_tr_b16 a[168:169], v34 offset:0\n" \
"v_mfma_f32_16x16x32_bf16 v[132:135], a[212:215], a[240:243], v[132:135]\n" \
"ds_read_b128 a[148:151], v28 offset:2048\n" \
"v_mfma_f32_16x16x32_bf16 v[148:151], a[152:155], a[176:179], v[148:151]\n" \
"ds_read_b128 a[212:215], v29 offset:2048\n" \
"v_mfma_f32_16x16x32_bf16 v[148:151], a[216:219], a[240:243], v[148:151]\n" \
"ds_read_b64_tr_b16 a[170:171], v34 offset:1024\n" \
"v_mfma_f32_16x16x32_bf16 v[152:155], a[152:155], a[180:183], v[152:155]\n" \
"ds_read_b64_tr_b16 a[232:233], v34 offset:8192\n" \
"v_mfma_f32_16x16x32_bf16 v[152:155], a[216:219], a[244:247], v[152:155]\n" \
"ds_read_b64_tr_b16 a[234:235], v34 offset:9216\n" \
"v_mfma_f32_16x16x32_bf16 v[156:159], a[152:155], a[184:187], v[156:159]\n" \
"ds_read_b64_tr_b16 a[172:173], v35 offset:0\n" \
"v_mfma_f32_16x16x32_bf16 v[156:159], a[216:219], a[248:251], v[156:159]\n" \
"ds_read_b64_tr_b16 a[174:175], v35 offset:1024\n" \
"v_mfma_f32_16x16x32_bf16 v[160:163], a[152:155], a[188:191], v[160:163]\n" \
"ds_read_b64_tr_b16 a[236:237], v35 offset:8192\n" \
"v_mfma_f32_16x16x32_bf16 v[160:163], a[216:219], a[252:255], v[160:163]\n" \
"ds_read_b128 a[152:155], v28 offset:4096\n" \
"v_mfma_f32_16x16x32_bf16 v[176:179], a[156:159], a[188:191], v[176:179]\n" \
"ds_read_b128 a[216:219], v29 offset:4096\n" \
"v_mfma_f32_16x16x32_bf16 v[176:179], a[220:223], a[252:255], v[176:179]\n" \
"ds_read_b64_tr_b16 a[238:239], v35 offset:9216\n" \
"v_mfma_f32_16x16x32_bf16 v[172:175], a[156:159], a[184:187], v[172:175]\n" \
"v_mfma_f32_16x16x32_bf16 v[172:175], a[220:223], a[248:251], v[172:175]\n" \
"v_mfma_f32_16x16x32_bf16 v[168:171], a[156:159], a[180:183], v[168:171]\n" \
"v_mfma_f32_16x16x32_bf16 v[168:171], a[220:223], a[244:247], v[168:171]\n" \
"v_mfma_f32_16x16x32_bf16 v[164:167], a[156:159], a[176:179], v[164:167]\n" \
"v_mfma_f32_16x16x32_bf16 v[164:167], a[220:223], a[240:243], v[164:167]\n" \
"ds_read_b128 a[156:159], v28 offset:6144\n" \
"ds_read_b128 a[220:223], v29 offset:6144\n" \
"s_waitcnt lgkmcnt(15)\n" \
"v_mfma_f32_16x16x32_bf16 a[0:3], a[128:131], a[160:163], a[0:3]\n" \
"s_addk_i32 s84, 0x7c00\n" \
"v_mfma_f32_16x16x32_bf16 a[0:3], a[192:195], a[224:227], a[0:3]\n" \
"buffer_load_dwordx4 v27, s[72:75], s84 offen offset:2048 lds\n" \
"v_mfma_f32_16x16x32_bf16 a[4:7], a[128:131], a[164:167], a[4:7]\n" \
"ds_read_b64_tr_b16 a[176:177], v36 offset:0\n" \
"s_waitcnt lgkmcnt(15)\n" \
"v_mfma_f32_16x16x32_bf16 a[4:7], a[192:195], a[228:231], a[4:7]\n" \
"s_addk_i32 s84, 0x7c00\n" \
"s_waitcnt lgkmcnt(11)\n" \
"v_mfma_f32_16x16x32_bf16 a[8:11], a[128:131], a[168:171], a[8:11]\n" \
"buffer_load_dwordx4 v27, s[72:75], s84 offen offset:3072 lds\n" \
"s_waitcnt lgkmcnt(9)\n" \
"v_mfma_f32_16x16x32_bf16 a[8:11], a[192:195], a[232:235], a[8:11]\n" \
"ds_read_b64_tr_b16 a[178:179], v36 offset:1024\n" \
"s_waitcnt lgkmcnt(8)\n" \
"v_mfma_f32_16x16x32_bf16 a[12:15], a[128:131], a[172:175], a[12:15]\n" \
"ds_read_b64_tr_b16 a[240:241], v36 offset:8192\n" \
"s_waitcnt lgkmcnt(5)\n" \
"v_mfma_f32_16x16x32_bf16 a[12:15], a[192:195], a[236:239], a[12:15]\n" \
"v_mfma_f32_16x16x32_bf16 a[28:31], a[132:135], a[172:175], a[28:31]\n" \
"v_mfma_f32_16x16x32_bf16 a[28:31], a[196:199], a[236:239], a[28:31]\n" \
"ds_read_b64_tr_b16 a[242:243], v36 offset:9216\n" \
"v_mfma_f32_16x16x32_bf16 a[24:27], a[132:135], a[168:171], a[24:27]\n" \
"v_mfma_f32_16x16x32_bf16 a[24:27], a[196:199], a[232:235], a[24:27]\n" \
"v_mfma_f32_16x16x32_bf16 a[20:23], a[132:135], a[164:167], a[20:23]\n" \
"v_mfma_f32_16x16x32_bf16 a[20:23], a[196:199], a[228:231], a[20:23]\n" \
"ds_read_b64_tr_b16 a[180:181], v37 offset:0\n" \
"v_mfma_f32_16x16x32_bf16 a[16:19], a[132:135], a[160:163], a[16:19]\n" \
"v_mfma_f32_16x16x32_bf16 a[16:19], a[196:199], a[224:227], a[16:19]\n" \
"v_mfma_f32_16x16x32_bf16 a[32:35], a[136:139], a[160:163], a[32:35]\n" \
"ds_read_b64_tr_b16 a[182:183], v37 offset:1024\n" \
"v_mfma_f32_16x16x32_bf16 a[32:35], a[200:203], a[224:227], a[32:35]\n" \
"v_mfma_f32_16x16x32_bf16 a[36:39], a[136:139], a[164:167], a[36:39]\n" \
"v_mfma_f32_16x16x32_bf16 a[36:39], a[200:203], a[228:231], a[36:39]\n" \
"ds_read_b64_tr_b16 a[244:245], v37 offset:8192\n" \
"v_mfma_f32_16x16x32_bf16 a[40:43], a[136:139], a[168:171], a[40:43]\n" \
"v_mfma_f32_16x16x32_bf16 a[40:43], a[200:203], a[232:235], a[40:43]\n" \
"v_mfma_f32_16x16x32_bf16 a[44:47], a[136:139], a[172:175], a[44:47]\n" \
"ds_read_b64_tr_b16 a[246:247], v37 offset:9216\n" \
"v_mfma_f32_16x16x32_bf16 a[44:47], a[200:203], a[236:239], a[44:47]\n" \
"v_mfma_f32_16x16x32_bf16 a[60:63], a[140:143], a[172:175], a[60:63]\n" \
"v_mfma_f32_16x16x32_bf16 a[60:63], a[204:207], a[236:239], a[60:63]\n" \
"v_mfma_f32_16x16x32_bf16 a[56:59], a[140:143], a[168:171], a[56:59]\n" \
"ds_read_b64_tr_b16 a[184:185], v38 offset:0\n" \
"v_mfma_f32_16x16x32_bf16 a[56:59], a[204:207], a[232:235], a[56:59]\n" \
"v_mfma_f32_16x16x32_bf16 a[52:55], a[140:143], a[164:167], a[52:55]\n" \
"v_mfma_f32_16x16x32_bf16 a[52:55], a[204:207], a[228:231], a[52:55]\n" \
"ds_read_b64_tr_b16 a[186:187], v38 offset:1024\n" \
"v_mfma_f32_16x16x32_bf16 a[48:51], a[140:143], a[160:163], a[48:51]\n" \
"v_mfma_f32_16x16x32_bf16 a[48:51], a[204:207], a[224:227], a[48:51]\n" \
"v_mfma_f32_16x16x32_bf16 a[64:67], a[144:147], a[160:163], a[64:67]\n" \
"v_bfe_u32 v44, v20, 2, 5\n" \
"ds_read_b64_tr_b16 a[248:249], v38 offset:8192\n" \
"v_mfma_f32_16x16x32_bf16 a[64:67], a[208:211], a[224:227], a[64:67]\n" \
"buffer_load_dword v44, v44, s[80:83], s38 offen offset:128 sc1\n" \
"v_mfma_f32_16x16x32_bf16 a[68:71], a[144:147], a[164:167], a[68:71]\n" \
"v_mfma_f32_16x16x32_bf16 a[68:71], a[208:211], a[228:231], a[68:71]\n" \
"ds_read_b64_tr_b16 a[250:251], v38 offset:9216\n" \
"v_mfma_f32_16x16x32_bf16 a[72:75], a[144:147], a[168:171], a[72:75]\n" \
"v_mfma_f32_16x16x32_bf16 a[72:75], a[208:211], a[232:235], a[72:75]\n" \
"v_mfma_f32_16x16x32_bf16 a[76:79], a[144:147], a[172:175], a[76:79]\n" \
"v_mfma_f32_16x16x32_bf16 a[76:79], a[208:211], a[236:239], a[76:79]\n" \
"ds_read_b64_tr_b16 a[188:189], v39 offset:0\n" \
"v_mfma_f32_16x16x32_bf16 a[92:95], a[148:151], a[172:175], a[92:95]\n" \
"v_mfma_f32_16x16x32_bf16 a[92:95], a[212:215], a[236:239], a[92:95]\n" \
"v_mfma_f32_16x16x32_bf16 a[88:91], a[148:151], a[168:171], a[88:91]\n" \
"ds_read_b64_tr_b16 a[190:191], v39 offset:1024\n" \
"v_mfma_f32_16x16x32_bf16 a[88:91], a[212:215], a[232:235], a[88:91]\n" \
"v_mfma_f32_16x16x32_bf16 a[84:87], a[148:151], a[164:167], a[84:87]\n" \
"v_mfma_f32_16x16x32_bf16 a[84:87], a[212:215], a[228:231], a[84:87]\n" \
"ds_read_b64_tr_b16 a[252:253], v39 offset:8192\n" \
"v_mfma_f32_16x16x32_bf16 a[80:83], a[148:151], a[160:163], a[80:83]\n" \
"v_mfma_f32_16x16x32_bf16 a[80:83], a[212:215], a[224:227], a[80:83]\n" \
"v_mfma_f32_16x16x32_bf16 a[96:99], a[152:155], a[160:163], a[96:99]\n" \
"ds_read_b64_tr_b16 a[254:255], v39 offset:9216\n" \
"v_mfma_f32_16x16x32_bf16 a[96:99], a[216:219], a[224:227], a[96:99]\n" \
"v_mfma_f32_16x16x32_bf16 a[100:103], a[152:155], a[164:167], a[100:103]\n" \
"v_mfma_f32_16x16x32_bf16 a[100:103], a[216:219], a[228:231], a[100:103]\n" \
"v_mfma_f32_16x16x32_bf16 a[104:107], a[152:155], a[168:171], a[104:107]\n" \
"v_mfma_f32_16x16x32_bf16 a[104:107], a[216:219], a[232:235], a[104:107]\n" \
"v_mfma_f32_16x16x32_bf16 a[108:111], a[152:155], a[172:175], a[108:111]\n" \
"v_mfma_f32_16x16x32_bf16 a[108:111], a[216:219], a[236:239], a[108:111]\n" \
"s_waitcnt lgkmcnt(15)\n" \
"v_mfma_f32_16x16x32_bf16 a[124:127], a[156:159], a[172:175], a[124:127]\n" \
"v_mfma_f32_16x16x32_bf16 a[124:127], a[220:223], a[236:239], a[124:127]\n" \
"v_mfma_f32_16x16x32_bf16 a[120:123], a[156:159], a[168:171], a[120:123]\n" \
"v_mfma_f32_16x16x32_bf16 a[120:123], a[220:223], a[232:235], a[120:123]\n" \
"v_mfma_f32_16x16x32_bf16 a[116:119], a[156:159], a[164:167], a[116:119]\n" \
"v_mfma_f32_16x16x32_bf16 a[116:119], a[220:223], a[228:231], a[116:119]\n" \
"v_mfma_f32_16x16x32_bf16 a[112:115], a[156:159], a[160:163], a[112:115]\n" \
"v_mfma_f32_16x16x32_bf16 a[112:115], a[220:223], a[224:227], a[112:115]\n" \
"s_waitcnt vmcnt(0) lgkmcnt(0)\n" \
"s_barrier\n" \
"v_cmp_ne_u32_e32 vcc, s62, v44\n" \
"s_nop 4\n" \
"s_cbranch_vccnz L_poll61_%=\n" \
"L_pollr61_%=:\n" \
"s_mov_b32 s68, s12\n" \
"s_and_b32 s69, s13, 0xffff\n" \
"s_mov_b32 s70, 0x480000\n" \
"s_mov_b32 s71, 0x20000\n" \
"v_mfma_f32_16x16x32_bf16 v[52:55], a[128:131], a[176:179], v[52:55]\n" \
"s_add_u32 m0, s40, 0x0\n" \
"s_add_u32 s85, s39, 0x0\n" \
"s_nop 0\n" \
"v_mfma_f32_16x16x32_bf16 v[52:55], a[192:195], a[240:243], v[52:55]\n" \
"buffer_load_dwordx4 v20, s[80:83], s85 offen sc1 lds\n" \
"v_mfma_f32_16x16x32_bf16 v[56:59], a[128:131], a[180:183], v[56:59]\n" \
"ds_read_b64_tr_b16 a[160:161], v32 offset:32768\n" \
"v_mfma_f32_16x16x32_bf16 v[56:59], a[192:195], a[244:247], v[56:59]\n" \
"s_add_u32 m0, s40, 0x1000\n" \
"s_add_u32 s85, s39, 0x1000\n" \
"v_mfma_f32_16x16x32_bf16 v[60:63], a[128:131], a[184:187], v[60:63]\n" \
"buffer_load_dwordx4 v20, s[80:83], s85 offen sc1 lds\n" \
"v_mfma_f32_16x16x32_bf16 v[60:63], a[192:195], a[248:251], v[60:63]\n" \
"ds_read_b64_tr_b16 a[162:163], v32 offset:33792\n" \
"v_mfma_f32_16x16x32_bf16 v[64:67], a[128:131], a[188:191], v[64:67]\n" \
"ds_read_b64_tr_b16 a[224:225], v32 offset:40960\n" \
"v_mfma_f32_16x16x32_bf16 v[64:67], a[192:195], a[252:255], v[64:67]\n" \
"ds_read_b128 a[128:131], v28 offset:49152\n" \
"v_mfma_f32_16x16x32_bf16 v[80:83], a[132:135], a[188:191], v[80:83]\n" \
"ds_read_b128 a[192:195], v29 offset:49152\n" \
"v_mfma_f32_16x16x32_bf16 v[80:83], a[196:199], a[252:255], v[80:83]\n" \
"s_add_u32 m0, s40, 0x3000\n" \
"s_movk_i32 s84, 0x0\n" \
"v_mfma_f32_16x16x32_bf16 v[76:79], a[132:135], a[184:187], v[76:79]\n" \
"buffer_load_dwordx4 v21, s[76:79], s84 offen offset:0 lds\n" \
"v_mfma_f32_16x16x32_bf16 v[76:79], a[196:199], a[248:251], v[76:79]\n" \
"s_add_u32 m0, s30, 0x0\n" \
"s_movk_i32 s84, 0x0\n" \
"s_nop 0\n" \
"v_mfma_f32_16x16x32_bf16 v[72:75], a[132:135], a[180:183], v[72:75]\n" \
"buffer_load_dwordx4 v24, s[76:79], s84 offen offset:0 lds\n" \
"v_mfma_f32_16x16x32_bf16 v[72:75], a[196:199], a[244:247], v[72:75]\n" \
"ds_read_b64_tr_b16 a[226:227], v32 offset:41984\n" \
"v_mfma_f32_16x16x32_bf16 v[68:71], a[132:135], a[176:179], v[68:71]\n" \
"ds_read_b64_tr_b16 a[164:165], v33 offset:32768\n" \
"v_mfma_f32_16x16x32_bf16 v[68:71], a[196:199], a[240:243], v[68:71]\n" \
"ds_read_b128 a[132:135], v28 offset:51200\n" \
"v_mfma_f32_16x16x32_bf16 v[84:87], a[136:139], a[176:179], v[84:87]\n" \
"ds_read_b128 a[196:199], v29 offset:51200\n" \
"v_mfma_f32_16x16x32_bf16 v[84:87], a[200:203], a[240:243], v[84:87]\n" \
"s_movk_i32 s84, 0x2000\n" \
"v_mfma_f32_16x16x32_bf16 v[88:91], a[136:139], a[180:183], v[88:91]\n" \
"buffer_load_dwordx4 v25, s[76:79], s84 offen offset:1024 lds\n" \
"v_mfma_f32_16x16x32_bf16 v[88:91], a[200:203], a[244:247], v[88:91]\n" \
"s_movk_i32 s84, 0x4000\n" \
"v_mfma_f32_16x16x32_bf16 v[92:95], a[136:139], a[184:187], v[92:95]\n" \
"buffer_load_dwordx4 v24, s[76:79], s84 offen offset:2048 lds\n" \
"v_mfma_f32_16x16x32_bf16 v[92:95], a[200:203], a[248:251], v[92:95]\n" \
"ds_read_b64_tr_b16 a[166:167], v33 offset:33792\n" \
"v_mfma_f32_16x16x32_bf16 v[96:99], a[136:139], a[188:191], v[96:99]\n" \
"ds_read_b64_tr_b16 a[228:229], v33 offset:40960\n" \
"v_mfma_f32_16x16x32_bf16 v[96:99], a[200:203], a[252:255], v[96:99]\n" \
"ds_read_b128 a[136:139], v28 offset:53248\n" \
"v_mfma_f32_16x16x32_bf16 v[112:115], a[140:143], a[188:191], v[112:115]\n" \
"ds_read_b128 a[200:203], v29 offset:53248\n" \
"v_mfma_f32_16x16x32_bf16 v[112:115], a[204:207], a[252:255], v[112:115]\n" \
"s_movk_i32 s84, 0x6000\n" \
"v_mfma_f32_16x16x32_bf16 v[108:111], a[140:143], a[184:187], v[108:111]\n" \
"buffer_load_dwordx4 v25, s[76:79], s84 offen offset:3072 lds\n" \
"v_mfma_f32_16x16x32_bf16 v[108:111], a[204:207], a[248:251], v[108:111]\n" \
"s_add_u32 m0, s31, 0x0\n" \
"s_movk_i32 s84, 0x0\n" \
"v_mfma_f32_16x16x32_bf16 v[104:107], a[140:143], a[180:183], v[104:107]\n" \
"buffer_load_dwordx4 v26, s[68:71], s84 offen offset:0 lds\n" \
"v_mfma_f32_16x16x32_bf16 v[104:107], a[204:207], a[244:247], v[104:107]\n" \
"s_movk_i32 s84, 0x7c00\n" \
"v_mfma_f32_16x16x32_bf16 v[100:103], a[140:143], a[176:179], v[100:103]\n" \
".fill (L_pb + 57056 - .) / 4, 4, 0xBF800000\n" \
"L_pp14:\n" \
"s_cbranch_execz L_pret\n" \
"buffer_load_dwordx4 v26, s[68:71], s84 offen offset:1024 lds\n" \
"v_mfma_f32_16x16x32_bf16 v[100:103], a[204:207], a[240:243], v[100:103]\n" \
"ds_read_b128 a[140:143], v28 offset:55296\n" \
"v_mfma_f32_16x16x32_bf16 v[116:119], a[144:147], a[176:179], v[116:119]\n" \
"ds_read_b128 a[204:207], v29 offset:55296\n" \
"v_mfma_f32_16x16x32_bf16 v[116:119], a[208:211], a[240:243], v[116:119]\n" \
"s_addk_i32 s84, 0x7c00\n" \
"v_mfma_f32_16x16x32_bf16 v[120:123], a[144:147], a[180:183], v[120:123]\n" \
"buffer_load_dwordx4 v27, s[68:71], s84 offen offset:2048 lds\n" \
"v_mfma_f32_16x16x32_bf16 v[120:123], a[208:211], a[244:247], v[120:123]\n" \
"ds_read_b64_tr_b16 a[230:231], v33 offset:41984\n" \
"v_mfma_f32_16x16x32_bf16 v[124:127], a[144:147], a[184:187], v[124:127]\n" \
"s_addk_i32 s84, 0x7c00\n" \
"s_nop 0\n" \
"v_mfma_f32_16x16x32_bf16 v[124:127], a[208:211], a[248:251], v[124:127]\n" \
"buffer_load_dwordx4 v27, s[68:71], s84 offen offset:3072 lds\n" \
"v_mfma_f32_16x16x32_bf16 v[128:131], a[144:147], a[188:191], v[128:131]\n" \
"ds_read_b64_tr_b16 a[168:169], v34 offset:32768\n" \
"v_mfma_f32_16x16x32_bf16 v[128:131], a[208:211], a[252:255], v[128:131]\n" \
"ds_read_b128 a[144:147], v28 offset:32768\n" \
"v_mfma_f32_16x16x32_bf16 v[144:147], a[148:151], a[188:191], v[144:147]\n" \
"ds_read_b128 a[208:211], v29 offset:32768\n" \
"v_mfma_f32_16x16x32_bf16 v[144:147], a[212:215], a[252:255], v[144:147]\n" \
"s_add_u32 m0, s30, 0x1000\n" \
"s_mov_b32 s84, 0x9000\n" \
"v_mfma_f32_16x16x32_bf16 v[140:143], a[148:151], a[184:187], v[140:143]\n" \
"buffer_load_dwordx4 v24, s[76:79], s84 offen offset:0 lds\n" \
"v_mfma_f32_16x16x32_bf16 v[140:143], a[212:215], a[248:251], v[140:143]\n" \
"ds_read_b64_tr_b16 a[170:171], v34 offset:33792\n" \
"v_mfma_f32_16x16x32_bf16 v[136:139], a[148:151], a[180:183], v[136:139]\n" \
"s_addk_i32 s84, 0x2000\n" \
"s_nop 0\n" \
"v_mfma_f32_16x16x32_bf16 v[136:139], a[212:215], a[244:247], v[136:139]\n" \
"buffer_load_dwordx4 v25, s[76:79], s84 offen offset:1024 lds\n" \
"v_mfma_f32_16x16x32_bf16 v[132:135], a[148:151], a[176:179], v[132:135]\n" \
"ds_read_b64_tr_b16 a[232:233], v34 offset:40960\n" \
"v_mfma_f32_16x16x32_bf16 v[132:135], a[212:215], a[240:243], v[132:135]\n" \
"ds_read_b128 a[148:151], v28 offset:34816\n" \
"v_mfma_f32_16x16x32_bf16 v[148:151], a[152:155], a[176:179], v[148:151]\n" \
"ds_read_b128 a[212:215], v29 offset:34816\n" \
"v_mfma_f32_16x16x32_bf16 v[148:151], a[216:219], a[240:243], v[148:151]\n" \
"ds_read_b64_tr_b16 a[234:235], v34 offset:41984\n" \
"v_mfma_f32_16x16x32_bf16 v[152:155], a[152:155], a[180:183], v[152:155]\n" \
"ds_read_b64_tr_b16 a[172:173], v35 offset:32768\n" \
"v_mfma_f32_16x16x32_bf16 v[152:155], a[216:219], a[244:247], v[152:155]\n" \
"ds_read_b64_tr_b16 a[174:175], v35 offset:33792\n" \
"v_mfma_f32_16x16x32_bf16 v[156:159], a[152:155], a[184:187], v[156:159]\n" \
"ds_read_b64_tr_b16 a[236:237], v35 offset:40960\n" \
"v_mfma_f32_16x16x32_bf16 v[156:159], a[216:219], a[248:251], v[156:159]\n" \
"ds_read_b64_tr_b16 a[238:239], v35 offset:41984\n" \
"v_mfma_f32_16x16x32_bf16 v[160:163], a[152:155], a[188:191], v[160:163]\n" \
"v_mfma_f32_16x16x32_bf16 v[160:163], a[216:219], a[252:255], v[160:163]\n" \
"ds_read_b128 a[152:155], v28 offset:36864\n" \
"v_mfma_f32_16x16x32_bf16 v[176:179], a[156:159], a[188:191], v[176:179]\n" \
"ds_read_b128 a[216:219], v29 offset:36864\n" \
"v_mfma_f32_16x16x32_bf16 v[176:179], a[220:223], a[252:255], v[176:179]\n" \
"v_mfma_f32_16x16x32_bf16 v[172:175], a[156:159], a[184:187], v[172:175]\n" \
"v_mfma_f32_16x16x32_bf16 v[172:175], a[220:223], a[248:251], v[172:175]\n" \
"v_mfma_f32_16x16x32_bf16 v[168:171], a[156:159], a[180:183], v[168:171]\n" \
"v_mfma_f32_16x16x32_bf16 v[168:171], a[220:223], a[244:247], v[168:171]\n" \
"v_mfma_f32_16x16x32_bf16 v[164:167], a[156:159], a[176:179], v[164:167]\n" \
"v_mfma_f32_16x16x32_bf16 v[164:167], a[220:223], a[240:243], v[164:167]\n" \
"ds_read_b128 a[156:159], v28 offset:38912\n" \
"ds_read_b128 a[220:223], v29 offset:38912\n" \
"s_waitcnt lgkmcnt(15)\n" \
"v_mfma_f32_16x16x32_bf16 a[0:3], a[128:131], a[160:163], a[0:3]\n" \
"s_addk_i32 s84, 0x2000\n" \
"v_mfma_f32_16x16x32_bf16 a[0:3], a[192:195], a[224:227], a[0:3]\n" \
"buffer_load_dwordx4 v24, s[76:79], s84 offen offset:2048 lds\n" \
"v_mfma_f32_16x16x32_bf16 a[4:7], a[128:131], a[164:167], a[4:7]\n" \
"ds_read_b64_tr_b16 a[176:177], v36 offset:32768\n" \
"v_mfma_f32_16x16x32_bf16 a[4:7], a[192:195], a[228:231], a[4:7]\n" \
"s_addk_i32 s84, 0x2000\n" \
"s_waitcnt lgkmcnt(13)\n" \
"v_mfma_f32_16x16x32_bf16 a[8:11], a[128:131], a[168:171], a[8:11]\n" \
"buffer_load_dwordx4 v25, s[76:79], s84 offen offset:3072 lds\n" \
"s_waitcnt lgkmcnt(9)\n" \
"v_mfma_f32_16x16x32_bf16 a[8:11], a[192:195], a[232:235], a[8:11]\n" \
"ds_read_b64_tr_b16 a[178:179], v36 offset:33792\n" \
"s_waitcnt lgkmcnt(8)\n" \
"v_mfma_f32_16x16x32_bf16 a[12:15], a[128:131], a[172:175], a[12:15]\n" \
"ds_read_b64_tr_b16 a[240:241], v36 offset:40960\n" \
"s_waitcnt lgkmcnt(7)\n" \
"v_mfma_f32_16x16x32_bf16 a[12:15], a[192:195], a[236:239], a[12:15]\n" \
"v_mfma_f32_16x16x32_bf16 a[28:31], a[132:135], a[172:175], a[28:31]\n" \
"v_mfma_f32_16x16x32_bf16 a[28:31], a[196:199], a[236:239], a[28:31]\n" \
"ds_read_b64_tr_b16 a[242:243], v36 offset:41984\n" \
"v_mfma_f32_16x16x32_bf16 a[24:27], a[132:135], a[168:171], a[24:27]\n" \
"v_mfma_f32_16x16x32_bf16 a[24:27], a[196:199], a[232:235], a[24:27]\n" \
"v_mfma_f32_16x16x32_bf16 a[20:23], a[132:135], a[164:167], a[20:23]\n" \
"s_nop 1\n" \
"v_accvgpr_read_b32 v184, a12\n" \
"v_accvgpr_read_b32 v185, a13\n" \
"v_mfma_f32_16x16x32_bf16 a[20:23], a[196:199], a[228:231], a[20:23]\n" \
"ds_read_b64_tr_b16 a[180:181], v37 offset:32768\n" \
"v_accvgpr_read_b32 v186, a14\n" \
"v_mfma_f32_16x16x32_bf16 a[16:19], a[132:135], a[160:163], a[16:19]\n" \
"v_accvgpr_read_b32 v187, a15\n" \
"v_accvgpr_read_b32 v180, a28\n" \
"v_mfma_f32_16x16x32_bf16 a[16:19], a[196:199], a[224:227], a[16:19]\n" \
"v_accvgpr_read_b32 v181, a29\n" \
"v_accvgpr_read_b32 v182, a30\n" \
"v_mfma_f32_16x16x32_bf16 a[32:35], a[136:139], a[160:163], a[32:35]\n" \
"ds_read_b64_tr_b16 a[182:183], v37 offset:33792\n" \
"v_accvgpr_read_b32 v183, a31\n" \
"v_mfma_f32_16x16x32_bf16 a[32:35], a[200:203], a[224:227], a[32:35]\n" \
"v_cvt_pk_bf16_f32 v184, v184, v185\n" \
"v_cvt_pk_bf16_f32 v185, v186, v187\n" \
"v_mfma_f32_16x16x32_bf16 a[36:39], a[136:139], a[164:167], a[36:39]\n" \
"v_lshlrev_b32_e32 v45, 3, v20\n" \
"v_add_u32_e32 v45, s31, v45\n" \
"v_mfma_f32_16x16x32_bf16 a[36:39], a[200:203], a[228:231], a[36:39]\n" \
"s_getpc_b64 s[86:87]\n" \
"s_sub_u32 s86, s86, 0x10800\n" \
"s_subb_u32 s87, s87, 0\n" \
"s_mov_b32 m0, s32\n" \
"s_nop 0\n" \
"v_mfma_f32_16x16x32_bf16 a[40:43], a[136:139], a[168:171], a[40:43]\n" \
"global_load_lds_dword v45, s[86:87]\n" \
"v_mfma_f32_16x16x32_bf16 a[40:43], a[200:203], a[232:235], a[40:43]\n" \
"ds_read_b64_tr_b16 a[244:245], v37 offset:40960\n" \
"v_cvt_pk_bf16_f32 v186, v180, v181\n" \
"v_mfma_f32_16x16x32_bf16 a[44:47], a[136:139], a[172:175], a[44:47]\n" \
"ds_read_b64_tr_b16 a[246:247], v37 offset:41984\n" \
"v_cvt_pk_bf16_f32 v187, v182, v183\n" \
"v_mfma_f32_16x16x32_bf16 a[44:47], a[200:203], a[236:239], a[44:47]\n" \
"v_accvgpr_read_b32 v188, a8\n" \
"v_accvgpr_read_b32 v189, a9\n" \
"v_mfma_f32_16x16x32_bf16 a[60:63], a[140:143], a[172:175], a[60:63]\n" \
"v_accvgpr_read_b32 v190, a10\n" \
"v_accvgpr_read_b32 v191, a11\n" \
"v_mfma_f32_16x16x32_bf16 a[60:63], a[204:207], a[236:239], a[60:63]\n" \
"v_accvgpr_read_b32 v180, a24\n" \
"v_accvgpr_read_b32 v181, a25\n" \
"v_mfma_f32_16x16x32_bf16 a[56:59], a[140:143], a[168:171], a[56:59]\n" \
"ds_read_b64_tr_b16 a[184:185], v38 offset:32768\n" \
"v_accvgpr_read_b32 v182, a26\n" \
"v_mfma_f32_16x16x32_bf16 a[56:59], a[204:207], a[232:235], a[56:59]\n" \
"v_accvgpr_read_b32 v183, a27\n" \
"v_cvt_pk_bf16_f32 v188, v188, v189\n" \
"v_mfma_f32_16x16x32_bf16 a[52:55], a[140:143], a[164:167], a[52:55]\n" \
"v_cvt_pk_bf16_f32 v189, v190, v191\n" \
"v_cvt_pk_bf16_f32 v190, v180, v181\n" \
"v_mfma_f32_16x16x32_bf16 a[52:55], a[204:207], a[228:231], a[52:55]\n" \
"ds_read_b64_tr_b16 a[186:187], v38 offset:33792\n" \
"v_cvt_pk_bf16_f32 v191, v182, v183\n" \
"v_mfma_f32_16x16x32_bf16 a[48:51], a[140:143], a[160:163], a[48:51]\n" \
"v_accvgpr_read_b32 v192, a4\n" \
"v_accvgpr_read_b32 v193, a5\n" \
"v_mfma_f32_16x16x32_bf16 a[48:51], a[204:207], a[224:227], a[48:51]\n" \
"v_accvgpr_read_b32 v194, a6\n" \
"v_accvgpr_read_b32 v195, a7\n" \
"v_mfma_f32_16x16x32_bf16 a[64:67], a[144:147], a[160:163], a[64:67]\n" \
"ds_read_b64_tr_b16 a[248:249], v38 offset:40960\n" \
"v_accvgpr_read_b32 v180, a20\n" \
"v_mfma_f32_16x16x32_bf16 a[64:67], a[208:211], a[224:227], a[64:67]\n" \
"v_accvgpr_read_b32 v181, a21\n" \
"v_accvgpr_read_b32 v182, a22\n" \
"v_mfma_f32_16x16x32_bf16 a[68:71], a[144:147], a[164:167], a[68:71]\n" \
"v_accvgpr_read_b32 v183, a23\n" \
"v_cvt_pk_bf16_f32 v192, v192, v193\n" \
"v_mfma_f32_16x16x32_bf16 a[68:71], a[208:211], a[228:231], a[68:71]\n" \
"ds_read_b64_tr_b16 a[250:251], v38 offset:41984\n" \
"v_cvt_pk_bf16_f32 v193, v194, v195\n" \
"v_mfma_f32_16x16x32_bf16 a[72:75], a[144:147], a[168:171], a[72:75]\n" \
"v_cvt_pk_bf16_f32 v194, v180, v181\n" \
"v_cvt_pk_bf16_f32 v195, v182, v183\n" \
"v_mfma_f32_16x16x32_bf16 a[72:75], a[208:211], a[232:235], a[72:75]\n" \
"v_accvgpr_read_b32 v196, a0\n" \
"v_accvgpr_read_b32 v197, a1\n" \
"v_mfma_f32_16x16x32_bf16 a[76:79], a[144:147], a[172:175], a[76:79]\n" \
"v_accvgpr_read_b32 v198, a2\n" \
"v_accvgpr_read_b32 v199, a3\n" \
"v_mfma_f32_16x16x32_bf16 a[76:79], a[208:211], a[236:239], a[76:79]\n" \
"ds_read_b64_tr_b16 a[188:189], v39 offset:32768\n" \
"v_accvgpr_read_b32 v180, a16\n" \
"v_mfma_f32_16x16x32_bf16 a[92:95], a[148:151], a[172:175], a[92:95]\n" \
"s_add_u32 s89, s41, 0x0\n" \
"s_nop 0\n" \
"buffer_store_dwordx4 v[184:187], v20, s[80:83], s89 offen offset:3072 sc1\n" \
"v_mfma_f32_16x16x32_bf16 a[92:95], a[212:215], a[236:239], a[92:95]\n" \
"v_accvgpr_read_b32 v181, a17\n" \
"v_accvgpr_read_b32 v182, a18\n" \
"v_mfma_f32_16x16x32_bf16 a[88:91], a[148:151], a[168:171], a[88:91]\n" \
"ds_read_b64_tr_b16 a[190:191], v39 offset:33792\n" \
"v_accvgpr_read_b32 v183, a19\n" \
"v_mfma_f32_16x16x32_bf16 a[88:91], a[212:215], a[232:235], a[88:91]\n" \
"v_cvt_pk_bf16_f32 v196, v196, v197\n" \
"v_cvt_pk_bf16_f32 v197, v198, v199\n" \
"v_mfma_f32_16x16x32_bf16 a[84:87], a[148:151], a[164:167], a[84:87]\n" \
"v_cvt_pk_bf16_f32 v198, v180, v181\n" \
"v_cvt_pk_bf16_f32 v199, v182, v183\n" \
"v_mfma_f32_16x16x32_bf16 a[84:87], a[212:215], a[228:231], a[84:87]\n" \
"ds_read_b64_tr_b16 a[252:253], v39 offset:40960\n" \
"v_accvgpr_read_b32 v200, a44\n" \
"v_mfma_f32_16x16x32_bf16 a[80:83], a[148:151], a[160:163], a[80:83]\n" \
"s_add_u32 s88, s41, 0x0\n" \
"buffer_store_dwordx4 v[188:191], v20, s[80:83], s88 offen offset:2048 sc1\n" \
"v_mfma_f32_16x16x32_bf16 a[80:83], a[212:215], a[224:227], a[80:83]\n" \
"v_accvgpr_read_b32 v201, a45\n" \
"v_accvgpr_read_b32 v202, a46\n" \
"s_waitcnt lgkmcnt(15)\n" \
"v_mfma_f32_16x16x32_bf16 a[96:99], a[152:155], a[160:163], a[96:99]\n" \
"ds_read_b64_tr_b16 a[254:255], v39 offset:41984\n" \
"v_accvgpr_read_b32 v203, a47\n" \
"v_mfma_f32_16x16x32_bf16 a[96:99], a[216:219], a[224:227], a[96:99]\n" \
"v_accvgpr_read_b32 v180, a60\n" \
"v_accvgpr_read_b32 v181, a61\n" \
"v_mfma_f32_16x16x32_bf16 a[100:103], a[152:155], a[164:167], a[100:103]\n" \
"v_accvgpr_read_b32 v182, a62\n" \
"v_accvgpr_read_b32 v183, a63\n" \
"v_mfma_f32_16x16x32_bf16 a[100:103], a[216:219], a[228:231], a[100:103]\n" \
"v_cvt_pk_bf16_f32 v200, v200, v201\n" \
"v_cvt_pk_bf16_f32 v201, v202, v203\n" \
"v_mfma_f32_16x16x32_bf16 a[104:107], a[152:155], a[168:171], a[104:107]\n" \
"s_add_u32 s89, s41, 0x0\n" \
"s_nop 0\n" \
"buffer_store_dwordx4 v[192:195], v20, s[80:83], s89 offen offset:1024 sc1\n" \
"v_mfma_f32_16x16x32_bf16 a[104:107], a[216:219], a[232:235], a[104:107]\n" \
"v_cvt_pk_bf16_f32 v202, v180, v181\n" \
"v_cvt_pk_bf16_f32 v203, v182, v183\n" \
"v_mfma_f32_16x16x32_bf16 a[108:111], a[152:155], a[172:175], a[108:111]\n" \
"v_accvgpr_read_b32 v204, a40\n" \
"v_accvgpr_read_b32 v205, a41\n" \
"v_mfma_f32_16x16x32_bf16 a[108:111], a[216:219], a[236:239], a[108:111]\n" \
"v_accvgpr_read_b32 v206, a42\n" \
"v_accvgpr_read_b32 v207, a43\n" \
"v_mfma_f32_16x16x32_bf16 a[124:127], a[156:159], a[172:175], a[124:127]\n" \
"v_accvgpr_read_b32 v180, a56\n" \
"v_accvgpr_read_b32 v181, a57\n" \
"v_mfma_f32_16x16x32_bf16 a[124:127], a[220:223], a[236:239], a[124:127]\n" \
"v_accvgpr_read_b32 v182, a58\n" \
"v_accvgpr_read_b32 v183, a59\n" \
"v_mfma_f32_16x16x32_bf16 a[120:123], a[156:159], a[168:171], a[120:123]\n" \
"s_add_u32 s88, s41, 0x0\n" \
"s_nop 0\n" \
"buffer_store_dwordx4 v[196:199], v20, s[80:83], s88 offen offset:0 sc1\n" \
"v_mfma_f32_16x16x32_bf16 a[120:123], a[220:223], a[232:235], a[120:123]\n" \
"v_cvt_pk_bf16_f32 v204, v204, v205\n" \
"v_cvt_pk_bf16_f32 v205, v206, v207\n" \
"v_mfma_f32_16x16x32_bf16 a[116:119], a[156:159], a[164:167], a[116:119]\n" \
"v_cvt_pk_bf16_f32 v206, v180, v181\n" \
"v_cvt_pk_bf16_f32 v207, v182, v183\n" \
"v_mfma_f32_16x16x32_bf16 a[116:119], a[220:223], a[228:231], a[116:119]\n" \
"v_accvgpr_read_b32 v208, a36\n" \
"v_accvgpr_read_b32 v209, a37\n" \
"v_mfma_f32_16x16x32_bf16 a[112:115], a[156:159], a[160:163], a[112:115]\n" \
"v_accvgpr_read_b32 v210, a38\n" \
"v_accvgpr_read_b32 v211, a39\n" \
"v_mfma_f32_16x16x32_bf16 a[112:115], a[220:223], a[224:227], a[112:115]\n" \
"v_accvgpr_read_b32 v180, a52\n" \
"v_accvgpr_read_b32 v181, a53\n" \
"s_waitcnt vmcnt(5) lgkmcnt(0)\n" \
"s_barrier\n" \
"v_xor_b32_e32 v36, 0x4000, v36\n" \
"v_xor_b32_e64 v32, 0x0, v48\n" \
"v_xor_b32_e32 v37, 0x4000, v37\n" \
"v_xor_b32_e64 v33, 0x20, v48\n" \
"v_xor_b32_e32 v38, 0x4000, v38\n" \
"v_xor_b32_e64 v34, 0x40, v48\n" \
"v_xor_b32_e32 v39, 0x4000, v39\n" \
"v_xor_b32_e32 v35, 0x60, v48\n" \
"v_add_u32_e32 v28, s43, v28\n" \
"v_add_u32_e32 v29, s43, v29\n" \
"v_mfma_f32_16x16x32_bf16 v[116:119], a[144:147], a[176:179], v[116:119]\n" \
"s_add_u32 m0, s40, 0x8000\n" \
"s_add_u32 s85, s39, 0x2000\n" \
"v_mfma_f32_16x16x32_bf16 v[116:119], a[208:211], a[240:243], v[116:119]\n" \
"buffer_load_dwordx4 v20, s[80:83], s85 offen sc1 lds\n" \
"v_mfma_f32_16x16x32_bf16 v[120:123], a[144:147], a[180:183], v[120:123]\n" \
"ds_read_b64_tr_b16 a[160:161], v32 offset:0\n" \
"v_accvgpr_read_b32 v182, a54\n" \
"v_mfma_f32_16x16x32_bf16 v[120:123], a[208:211], a[244:247], v[120:123]\n" \
"s_add_u32 m0, s40, 0x9000\n" \
"s_add_u32 s85, s39, 0x3000\n" \
"v_mfma_f32_16x16x32_bf16 v[124:127], a[144:147], a[184:187], v[124:127]\n" \
"buffer_load_dwordx4 v20, s[80:83], s85 offen sc1 lds\n" \
"v_mfma_f32_16x16x32_bf16 v[124:127], a[208:211], a[248:251], v[124:127]\n" \
"ds_read_b64_tr_b16 a[162:163], v32 offset:512\n" \
"v_accvgpr_read_b32 v183, a55\n" \
"v_mfma_f32_16x16x32_bf16 v[128:131], a[144:147], a[188:191], v[128:131]\n" \
"ds_read_b64_tr_b16 a[224:225], v32 offset:4096\n" \
"v_cvt_pk_bf16_f32 v208, v208, v209\n" \
"v_mfma_f32_16x16x32_bf16 v[128:131], a[208:211], a[252:255], v[128:131]\n" \
"ds_read_b128 a[144:147], v28 offset:8192\n" \
"v_cvt_pk_bf16_f32 v209, v210, v211\n" \
"v_mfma_f32_16x16x32_bf16 v[144:147], a[148:151], a[188:191], v[144:147]\n" \
"ds_read_b128 a[208:211], v29 offset:8192\n" \
"v_cvt_pk_bf16_f32 v210, v180, v181\n" \
"v_mfma_f32_16x16x32_bf16 v[144:147], a[212:215], a[252:255], v[144:147]\n" \
"s_add_u32 m0, s40, 0xb000\n" \
"s_movk_i32 s84, 0x80\n" \
"v_mfma_f32_16x16x32_bf16 v[140:143], a[148:151], a[184:187], v[140:143]\n" \
"buffer_load_dwordx4 v21, s[76:79], s84 offen offset:0 lds\n" \
"v_mfma_f32_16x16x32_bf16 v[140:143], a[212:215], a[248:251], v[140:143]\n" \
"s_add_u32 m0, s30, 0x8000\n" \
"s_movk_i32 s84, 0x80\n" \
"v_mfma_f32_16x16x32_bf16 v[136:139], a[148:151], a[180:183], v[136:139]\n" \
"buffer_load_dwordx4 v24, s[76:79], s84 offen offset:0 lds\n" \
"v_mfma_f32_16x16x32_bf16 v[136:139], a[212:215], a[244:247], v[136:139]\n" \
"ds_read_b64_tr_b16 a[226:227], v32 offset:4608\n" \
"v_cvt_pk_bf16_f32 v211, v182, v183\n" \
"v_mfma_f32_16x16x32_bf16 v[132:135], a[148:151], a[176:179], v[132:135]\n" \
"ds_read_b64_tr_b16 a[164:165], v33 offset:0\n" \
"v_accvgpr_read_b32 v212, a32\n" \
"v_mfma_f32_16x16x32_bf16 v[132:135], a[212:215], a[240:243], v[132:135]\n" \
"ds_read_b128 a[148:151], v28 offset:10240\n" \
"v_accvgpr_read_b32 v213, a33\n" \
"v_mfma_f32_16x16x32_bf16 v[148:151], a[152:155], a[176:179], v[148:151]\n" \
"ds_read_b128 a[212:215], v29 offset:10240\n" \
"v_accvgpr_read_b32 v214, a34\n" \
"v_mfma_f32_16x16x32_bf16 v[148:151], a[216:219], a[240:243], v[148:151]\n" \
"s_movk_i32 s84, 0x2080\n" \
"v_mfma_f32_16x16x32_bf16 v[152:155], a[152:155], a[180:183], v[152:155]\n" \
"buffer_load_dwordx4 v25, s[76:79], s84 offen offset:1024 lds\n" \
"v_mfma_f32_16x16x32_bf16 v[152:155], a[216:219], a[244:247], v[152:155]\n" \
"s_movk_i32 s84, 0x4080\n" \
"v_mfma_f32_16x16x32_bf16 v[156:159], a[152:155], a[184:187], v[156:159]\n" \
"buffer_load_dwordx4 v24, s[76:79], s84 offen offset:2048 lds\n" \
"v_mfma_f32_16x16x32_bf16 v[156:159], a[216:219], a[248:251], v[156:159]\n" \
"ds_read_b64_tr_b16 a[166:167], v33 offset:512\n" \
"v_accvgpr_read_b32 v215, a35\n" \
"v_mfma_f32_16x16x32_bf16 v[160:163], a[152:155], a[188:191], v[160:163]\n" \
"ds_read_b64_tr_b16 a[228:229], v33 offset:4096\n" \
"v_accvgpr_read_b32 v180, a48\n" \
"v_mfma_f32_16x16x32_bf16 v[160:163], a[216:219], a[252:255], v[160:163]\n" \
"ds_read_b128 a[152:155], v28 offset:12288\n" \
"v_accvgpr_read_b32 v181, a49\n" \
"v_mfma_f32_16x16x32_bf16 v[176:179], a[156:159], a[188:191], v[176:179]\n" \
"ds_read_b128 a[216:219], v29 offset:12288\n" \
"v_accvgpr_read_b32 v182, a50\n" \
"v_mfma_f32_16x16x32_bf16 v[176:179], a[220:223], a[252:255], v[176:179]\n" \
"s_movk_i32 s84, 0x6080\n" \
"v_mfma_f32_16x16x32_bf16 v[172:175], a[156:159], a[184:187], v[172:175]\n" \
"buffer_load_dwordx4 v25, s[76:79], s84 offen offset:3072 lds\n" \
"v_mfma_f32_16x16x32_bf16 v[172:175], a[220:223], a[248:251], v[172:175]\n" \
"s_add_u32 m0, s31, 0x8000\n" \
"s_mov_b32 s84, 0x80000\n" \
"v_mfma_f32_16x16x32_bf16 v[168:171], a[156:159], a[180:183], v[168:171]\n" \
"buffer_load_dwordx4 v26, s[68:71], s84 offen offset:0 lds\n" \
"v_mfma_f32_16x16x32_bf16 v[168:171], a[220:223], a[244:247], v[168:171]\n" \
"s_addk_i32 s84, 0x7c00\n" \
"v_mfma_f32_16x16x32_bf16 v[164:167], a[156:159], a[176:179], v[164:167]\n" \
"buffer_load_dwordx4 v26, s[68:71], s84 offen offset:1024 lds\n" \
"v_mfma_f32_16x16x32_bf16 v[164:167], a[220:223], a[240:243], v[164:167]\n" \
"ds_read_b128 a[156:159], v28 offset:14336\n" \
"v_accvgpr_read_b32 v183, a51\n" \
"v_mfma_f32_16x16x32_bf16 v[84:87], a[136:139], a[176:179], v[84:87]\n" \
"ds_read_b128 a[220:223], v29 offset:14336\n" \
"v_cvt_pk_bf16_f32 v212, v212, v213\n" \
"v_mfma_f32_16x16x32_bf16 v[84:87], a[200:203], a[240:243], v[84:87]\n" \
"s_addk_i32 s84, 0x7c00\n" \
"v_mfma_f32_16x16x32_bf16 v[88:91], a[136:139], a[180:183], v[88:91]\n" \
"buffer_load_dwordx4 v27, s[68:71], s84 offen offset:2048 lds\n" \
"v_mfma_f32_16x16x32_bf16 v[88:91], a[200:203], a[244:247], v[88:91]\n" \
"ds_read_b64_tr_b16 a[230:231], v33 offset:4608\n" \
"v_cvt_pk_bf16_f32 v213, v214, v215\n" \
"v_mfma_f32_16x16x32_bf16 v[92:95], a[136:139], a[184:187], v[92:95]\n" \
"s_addk_i32 s84, 0x7c00\n" \
"v_mfma_f32_16x16x32_bf16 v[92:95], a[200:203], a[248:251], v[92:95]\n" \
"buffer_load_dwordx4 v27, s[68:71], s84 offen offset:3072 lds\n" \
"v_mfma_f32_16x16x32_bf16 v[96:99], a[136:139], a[188:191], v[96:99]\n" \
"ds_read_b64_tr_b16 a[172:173], v35 offset:0\n" \
"v_cvt_pk_bf16_f32 v214, v180, v181\n" \
"v_mfma_f32_16x16x32_bf16 v[96:99], a[200:203], a[252:255], v[96:99]\n" \
"ds_read_b128 a[136:139], v28 offset:4096\n" \
"v_cvt_pk_bf16_f32 v215, v182, v183\n" \
"v_mfma_f32_16x16x32_bf16 v[112:115], a[140:143], a[188:191], v[112:115]\n" \
"ds_read_b128 a[200:203], v29 offset:4096\n" \
"v_accvgpr_read_b32 v216, a68\n" \
"v_mfma_f32_16x16x32_bf16 v[112:115], a[204:207], a[252:255], v[112:115]\n" \
"s_add_u32 m0, s30, 0x9000\n" \
"s_mov_b32 s84, 0x9080\n" \
"v_mfma_f32_16x16x32_bf16 v[108:111], a[140:143], a[184:187], v[108:111]\n" \
"buffer_load_dwordx4 v24, s[76:79], s84 offen offset:0 lds\n" \
"v_mfma_f32_16x16x32_bf16 v[108:111], a[204:207], a[248:251], v[108:111]\n" \
"ds_read_b64_tr_b16 a[174:175], v35 offset:512\n" \
"v_accvgpr_read_b32 v217, a69\n" \
"v_mfma_f32_16x16x32_bf16 v[104:107], a[140:143], a[180:183], v[104:107]\n" \
"s_addk_i32 s84, 0x2000\n" \
"s_nop 0\n" \
"v_mfma_f32_16x16x32_bf16 v[104:107], a[204:207], a[244:247], v[104:107]\n" \
"buffer_load_dwordx4 v25, s[76:79], s84 offen offset:1024 lds\n" \
"v_mfma_f32_16x16x32_bf16 v[100:103], a[140:143], a[176:179], v[100:103]\n" \
"ds_read_b64_tr_b16 a[236:237], v35 offset:4096\n" \
"v_accvgpr_read_b32 v218, a70\n" \
"v_mfma_f32_16x16x32_bf16 v[100:103], a[204:207], a[240:243], v[100:103]\n" \
"ds_read_b128 a[140:143], v28 offset:6144\n" \
"v_accvgpr_read_b32 v219, a71\n" \
"v_mfma_f32_16x16x32_bf16 v[52:55], a[128:131], a[176:179], v[52:55]\n" \
"ds_read_b128 a[204:207], v29 offset:6144\n" \
"v_accvgpr_read_b32 v180, a84\n" \
"v_mfma_f32_16x16x32_bf16 v[52:55], a[192:195], a[240:243], v[52:55]\n" \
"ds_read_b64_tr_b16 a[238:239], v35 offset:4608\n" \
"v_accvgpr_read_b32 v181, a85\n" \
"v_mfma_f32_16x16x32_bf16 v[56:59], a[128:131], a[180:183], v[56:59]\n" \
"ds_read_b64_tr_b16 a[168:169], v34 offset:0\n" \
"v_accvgpr_read_b32 v182, a86\n" \
"v_mfma_f32_16x16x32_bf16 v[56:59], a[192:195], a[244:247], v[56:59]\n" \
"ds_read_b64_tr_b16 a[170:171], v34 offset:512\n" \
"v_accvgpr_read_b32 v183, a87\n" \
"v_mfma_f32_16x16x32_bf16 v[60:63], a[128:131], a[184:187], v[60:63]\n" \
"ds_read_b64_tr_b16 a[232:233], v34 offset:4096\n" \
"v_cvt_pk_bf16_f32 v216, v216, v217\n" \
"v_mfma_f32_16x16x32_bf16 v[60:63], a[192:195], a[248:251], v[60:63]\n" \
"ds_read_b64_tr_b16 a[234:235], v34 offset:4608\n" \
"v_cvt_pk_bf16_f32 v217, v218, v219\n" \
"v_mfma_f32_16x16x32_bf16 v[64:67], a[128:131], a[188:191], v[64:67]\n" \
"v_cvt_pk_bf16_f32 v218, v180, v181\n" \
"v_cvt_pk_bf16_f32 v219, v182, v183\n" \
"v_mfma_f32_16x16x32_bf16 v[64:67], a[192:195], a[252:255], v[64:67]\n" \
"ds_read_b128 a[128:131], v28 offset:0\n" \
"v_accvgpr_read_b32 v220, a64\n" \
"v_mfma_f32_16x16x32_bf16 v[80:83], a[132:135], a[188:191], v[80:83]\n" \
"ds_read_b128 a[192:195], v29 offset:0\n" \
"v_accvgpr_read_b32 v221, a65\n" \
"v_mfma_f32_16x16x32_bf16 v[80:83], a[196:199], a[252:255], v[80:83]\n" \
"v_accvgpr_read_b32 v222, a66\n" \
"v_accvgpr_read_b32 v223, a67\n" \
"v_mfma_f32_16x16x32_bf16 v[76:79], a[132:135], a[184:187], v[76:79]\n" \
"v_accvgpr_read_b32 v180, a80\n" \
"v_accvgpr_read_b32 v181, a81\n" \
"v_mfma_f32_16x16x32_bf16 v[76:79], a[196:199], a[248:251], v[76:79]\n" \
"v_accvgpr_read_b32 v182, a82\n" \
"v_accvgpr_read_b32 v183, a83\n" \
"v_mfma_f32_16x16x32_bf16 v[72:75], a[132:135], a[180:183], v[72:75]\n" \
"v_cvt_pk_bf16_f32 v220, v220, v221\n" \
"v_cvt_pk_bf16_f32 v221, v222, v223\n" \
"v_mfma_f32_16x16x32_bf16 v[72:75], a[196:199], a[244:247], v[72:75]\n" \
"v_cvt_pk_bf16_f32 v222, v180, v181\n" \
"v_cvt_pk_bf16_f32 v223, v182, v183\n" \
"v_mfma_f32_16x16x32_bf16 v[68:71], a[132:135], a[176:179], v[68:71]\n" \
"v_cvt_pk_bf16_f32 v224, v128, v129\n" \
"v_cvt_pk_bf16_f32 v225, v130, v131\n" \
"v_mfma_f32_16x16x32_bf16 v[68:71], a[196:199], a[240:243], v[68:71]\n" \
"ds_read_b128 a[132:135], v28 offset:2048\n" \
"ds_read_b128 a[196:199], v29 offset:2048\n" \
"s_waitcnt lgkmcnt(15)\n" \
"v_mfma_f32_16x16x32_bf16 a[0:3], a[160:163], a[144:147], 0\n" \
"s_addk_i32 s84, 0x2000\n" \
"v_cvt_pk_bf16_f32 v226, v144, v145\n" \
"v_mfma_f32_16x16x32_bf16 a[0:3], a[224:227], a[208:211], a[0:3]\n" \
"buffer_load_dwordx4 v24, s[76:79], s84 offen offset:2048 lds\n" \
"v_cvt_pk_bf16_f32 v227, v146, v147\n" \
"v_mfma_f32_16x16x32_bf16 a[4:7], a[164:167], a[144:147], 0\n" \
"ds_read_b64_tr_b16 a[176:177], v36 offset:0\n" \
"v_cvt_pk_bf16_f32 v228, v124, v125\n" \
"v_mfma_f32_16x16x32_bf16 a[4:7], a[228:231], a[208:211], a[4:7]\n" \
"s_addk_i32 s84, 0x2000\n" \
"v_cvt_pk_bf16_f32 v229, v126, v127\n" \
"s_waitcnt lgkmcnt(13)\n" \
"v_mfma_f32_16x16x32_bf16 a[12:15], a[172:175], a[144:147], 0\n" \
"buffer_load_dwordx4 v25, s[76:79], s84 offen offset:3072 lds\n" \
"v_cvt_pk_bf16_f32 v230, v140, v141\n" \
"s_waitcnt lgkmcnt(9)\n" \
"v_mfma_f32_16x16x32_bf16 a[12:15], a[236:239], a[208:211], a[12:15]\n" \
"ds_read_b64_tr_b16 a[178:179], v36 offset:1024\n" \
"v_cvt_pk_bf16_f32 v231, v142, v143\n" \
"s_waitcnt lgkmcnt(8)\n" \
"v_mfma_f32_16x16x32_bf16 a[8:11], a[144:147], a[168:171], 0\n" \
"ds_read_b64_tr_b16 a[240:241], v36 offset:8192\n" \
"v_cvt_pk_bf16_f32 v232, v120, v121\n" \
"s_waitcnt lgkmcnt(7)\n" \
"s_nop 0\n" \
"v_mfma_f32_16x16x32_bf16 a[8:11], a[208:211], a[232:235], a[8:11]\n" \
"ds_read_b64_tr_b16 a[242:243], v36 offset:9216\n" \
"v_cvt_pk_bf16_f32 v233, v122, v123\n" \
"v_mfma_f32_16x16x32_bf16 a[24:27], a[148:151], a[168:171], 0\n" \
"ds_read_b64_tr_b16 a[180:181], v37 offset:0\n" \
"v_cvt_pk_bf16_f32 v234, v136, v137\n" \
"v_mfma_f32_16x16x32_bf16 a[24:27], a[212:215], a[232:235], a[24:27]\n" \
"ds_read_b64_tr_b16 a[182:183], v37 offset:1024\n" \
"v_cvt_pk_bf16_f32 v235, v138, v139\n" \
"v_mfma_f32_16x16x32_bf16 a[28:31], a[172:175], a[148:151], 0\n" \
"ds_read_b64_tr_b16 a[244:245], v37 offset:8192\n" \
"v_cvt_pk_bf16_f32 v236, v116, v117\n" \
"v_mfma_f32_16x16x32_bf16 a[28:31], a[236:239], a[212:215], a[28:31]\n" \
"s_add_u32 s89, s41, 0x2000\n" \
"buffer_store_dwordx4 v[200:203], v20, s[80:83], s89 offen offset:3072 sc1\n" \
"v_mfma_f32_16x16x32_bf16 a[20:23], a[164:167], a[148:151], 0\n" \
"ds_read_b64_tr_b16 a[246:247], v37 offset:9216\n" \
"v_cvt_pk_bf16_f32 v237, v118, v119\n" \
"v_mfma_f32_16x16x32_bf16 a[20:23], a[228:231], a[212:215], a[20:23]\n" \
"ds_read_b64_tr_b16 a[184:185], v38 offset:0\n" \
"v_cvt_pk_bf16_f32 v238, v132, v133\n" \
"v_mfma_f32_16x16x32_bf16 a[16:19], a[160:163], a[148:151], 0\n" \
"ds_read_b64_tr_b16 a[186:187], v38 offset:1024\n" \
"v_cvt_pk_bf16_f32 v239, v134, v135\n" \
"v_mfma_f32_16x16x32_bf16 a[16:19], a[224:227], a[212:215], a[16:19]\n" \
"v_cvt_pk_bf16_f32 v240, v160, v161\n" \
"v_cvt_pk_bf16_f32 v241, v162, v163\n" \
"v_cvt_pk_bf16_f32 v242, v176, v177\n" \
"v_mfma_f32_16x16x32_bf16 a[32:35], a[160:163], a[152:155], 0\n" \
"s_add_u32 s88, s41, 0x2000\n" \
"buffer_store_dwordx4 v[204:207], v20, s[80:83], s88 offen offset:2048 sc1\n" \
"v_mfma_f32_16x16x32_bf16 a[32:35], a[224:227], a[216:219], a[32:35]\n" \
"ds_read_b64_tr_b16 a[248:249], v38 offset:8192\n" \
"v_cvt_pk_bf16_f32 v243, v178, v179\n" \
"v_mfma_f32_16x16x32_bf16 a[36:39], a[164:167], a[152:155], 0\n" \
"ds_read_b64_tr_b16 a[250:251], v38 offset:9216\n" \
"v_cvt_pk_bf16_f32 v244, v156, v157\n" \
"v_mfma_f32_16x16x32_bf16 a[36:39], a[228:231], a[216:219], a[36:39]\n" \
"ds_read_b64_tr_b16 a[188:189], v39 offset:0\n" \
"v_cvt_pk_bf16_f32 v245, v158, v159\n" \
"v_mfma_f32_16x16x32_bf16 a[44:47], a[172:175], a[152:155], 0\n" \
"v_cvt_pk_bf16_f32 v246, v172, v173\n" \
"v_cvt_pk_bf16_f32 v247, v174, v175\n" \
"v_cvt_pk_bf16_f32 v184, v152, v153\n" \
"v_mfma_f32_16x16x32_bf16 a[44:47], a[236:239], a[216:219], a[44:47]\n" \
"ds_read_b64_tr_b16 a[190:191], v39 offset:1024\n" \
"v_cvt_pk_bf16_f32 v185, v154, v155\n" \
"v_mfma_f32_16x16x32_bf16 a[40:43], a[152:155], a[168:171], 0\n" \
"s_add_u32 s89, s41, 0x2000\n" \
"buffer_store_dwordx4 v[208:211], v20, s[80:83], s89 offen offset:1024 sc1\n" \
"v_mfma_f32_16x16x32_bf16 a[40:43], a[216:219], a[232:235], a[40:43]\n" \
"ds_read_b64_tr_b16 a[252:253], v39 offset:8192\n" \
"v_cvt_pk_bf16_f32 v186, v168, v169\n" \
"v_mfma_f32_16x16x32_bf16 a[56:59], a[156:159], a[168:171], 0\n" \
"ds_read_b64_tr_b16 a[254:255], v39 offset:9216\n" \
"v_cvt_pk_bf16_f32 v187, v170, v171\n" \
"v_mfma_f32_16x16x32_bf16 a[56:59], a[220:223], a[232:235], a[56:59]\n" \
"v_cvt_pk_bf16_f32 v188, v148, v149\n" \
"v_cvt_pk_bf16_f32 v189, v150, v151\n" \
"v_cvt_pk_bf16_f32 v190, v164, v165\n" \
"v_mfma_f32_16x16x32_bf16 a[60:63], a[172:175], a[156:159], 0\n" \
"v_cvt_pk_bf16_f32 v191, v166, v167\n" \
"v_cvt_pk_bf16_f32 v192, v96, v97\n" \
"v_cvt_pk_bf16_f32 v193, v98, v99\n" \
"v_mfma_f32_16x16x32_bf16 a[60:63], a[236:239], a[220:223], a[60:63]\n" \
"s_add_u32 s88, s41, 0x2000\n" \
"buffer_store_dwordx4 v[212:215], v20, s[80:83], s88 offen offset:0 sc1\n" \
"v_mfma_f32_16x16x32_bf16 a[52:55], a[164:167], a[156:159], 0\n" \
"v_cvt_pk_bf16_f32 v194, v112, v113\n" \
"v_cvt_pk_bf16_f32 v195, v114, v115\n" \
"v_cvt_pk_bf16_f32 v196, v92, v93\n" \
"v_mfma_f32_16x16x32_bf16 a[52:55], a[228:231], a[220:223], a[52:55]\n" \
".fill (L_pb + 61152 - .) / 4, 4, 0xBF800000\n" \
"L_pp15:\n" \
"s_cbranch_execz L_pret\n" \
"s_nop 0\n" \
"v_cvt_pk_bf16_f32 v197, v94, v95\n" \
"v_cvt_pk_bf16_f32 v198, v108, v109\n" \
"v_cvt_pk_bf16_f32 v199, v110, v111\n" \
"v_mfma_f32_16x16x32_bf16 a[48:51], a[160:163], a[156:159], 0\n" \
"v_cvt_pk_bf16_f32 v252, v88, v89\n" \
"v_cvt_pk_bf16_f32 v253, v90, v91\n" \
"v_cvt_pk_bf16_f32 v254, v104, v105\n" \
"v_mfma_f32_16x16x32_bf16 a[48:51], a[224:227], a[220:223], a[48:51]\n" \
"v_cvt_pk_bf16_f32 v255, v106, v107\n" \
"v_accvgpr_write_b32 a84, v252\n" \
"v_accvgpr_write_b32 a85, v253\n" \
"v_mfma_f32_16x16x32_bf16 a[64:67], a[164:167], a[140:143], 0\n" \
"v_accvgpr_write_b32 a86, v254\n" \
"v_accvgpr_write_b32 a87, v255\n" \
"v_cvt_pk_bf16_f32 v252, v84, v85\n" \
"v_mfma_f32_16x16x32_bf16 a[64:67], a[228:231], a[204:207], a[64:67]\n" \
"v_cvt_pk_bf16_f32 v253, v86, v87\n" \
"v_cvt_pk_bf16_f32 v254, v100, v101\n" \
"v_cvt_pk_bf16_f32 v255, v102, v103\n" \
"v_mfma_f32_16x16x32_bf16 a[80:83], a[172:175], a[140:143], 0\n" \
"v_mfma_f32_16x16x32_bf16 a[80:83], a[236:239], a[204:207], a[80:83]\n" \
"v_mfma_f32_16x16x32_bf16 a[68:71], a[140:143], a[168:171], 0\n" \
"v_mfma_f32_16x16x32_bf16 a[68:71], a[204:207], a[232:235], a[68:71]\n" \
"s_waitcnt vmcnt(4) lgkmcnt(0)\n" \
"s_barrier\n" \
"v_mfma_f32_16x16x32_bf16 v[116:119], a[176:179], a[144:147], 0\n" \
"s_add_u32 m0, s40, 0x0\n" \
"s_nop 0\n" \
"s_add_u32 s85, s39, 0x4000\n" \
"v_cvt_pk_bf16_f32 v200, v64, v65\n" \
"v_mfma_f32_16x16x32_bf16 v[116:119], a[240:243], a[208:211], v[116:119]\n" \
"buffer_load_dwordx4 v20, s[80:83], s85 offen sc1 lds\n" \
"v_cvt_pk_bf16_f32 v201, v66, v67\n" \
"v_mfma_f32_16x16x32_bf16 v[120:123], a[180:183], a[144:147], 0\n" \
"ds_read_b64_tr_b16 a[160:161], v32 offset:32768\n" \
"v_cvt_pk_bf16_f32 v202, v80, v81\n" \
"v_mfma_f32_16x16x32_bf16 v[120:123], a[244:247], a[208:211], v[120:123]\n" \
"ds_read_b64_tr_b16 a[162:163], v32 offset:33280\n" \
"v_cvt_pk_bf16_f32 v203, v82, v83\n" \
"v_mfma_f32_16x16x32_bf16 v[124:127], a[184:187], a[144:147], 0\n" \
"s_add_u32 m0, s40, 0x1000\n" \
"s_add_u32 s85, s39, 0x5000\n" \
"v_cvt_pk_bf16_f32 v204, v60, v61\n" \
"v_mfma_f32_16x16x32_bf16 v[124:127], a[248:251], a[208:211], v[124:127]\n" \
"buffer_load_dwordx4 v20, s[80:83], s85 offen sc1 lds\n" \
"v_cvt_pk_bf16_f32 v205, v62, v63\n" \
"v_mfma_f32_16x16x32_bf16 v[128:131], a[188:191], a[144:147], 0\n" \
"ds_read_b64_tr_b16 a[224:225], v32 offset:36864\n" \
"v_cvt_pk_bf16_f32 v206, v76, v77\n" \
"v_mfma_f32_16x16x32_bf16 v[128:131], a[252:255], a[208:211], v[128:131]\n" \
"ds_read_b128 a[144:147], v28 offset:40960\n" \
"v_cvt_pk_bf16_f32 v207, v78, v79\n" \
"v_mfma_f32_16x16x32_bf16 v[144:147], a[188:191], a[148:151], 0\n" \
"ds_read_b128 a[208:211], v29 offset:40960\n" \
"v_cvt_pk_bf16_f32 v208, v56, v57\n" \
"v_mfma_f32_16x16x32_bf16 v[144:147], a[252:255], a[212:215], v[144:147]\n" \
"s_add_u32 m0, s40, 0x3000\n" \
"s_movk_i32 s84, 0x100\n" \
"v_cvt_pk_bf16_f32 v209, v58, v59\n" \
"v_mfma_f32_16x16x32_bf16 v[140:143], a[184:187], a[148:151], 0\n" \
"buffer_load_dwordx4 v21, s[76:79], s84 offen offset:0 lds\n" \
"v_cvt_pk_bf16_f32 v210, v72, v73\n" \
"v_mfma_f32_16x16x32_bf16 v[140:143], a[248:251], a[212:215], v[140:143]\n" \
"ds_read_b64_tr_b16 a[226:227], v32 offset:37376\n" \
"v_cvt_pk_bf16_f32 v211, v74, v75\n" \
"v_mfma_f32_16x16x32_bf16 v[136:139], a[180:183], a[148:151], 0\n" \
"s_add_u32 m0, s30, 0x0\n" \
"s_movk_i32 s84, 0x100\n" \
"s_nop 0\n" \
"v_cvt_pk_bf16_f32 v212, v52, v53\n" \
"v_mfma_f32_16x16x32_bf16 v[136:139], a[244:247], a[212:215], v[136:139]\n" \
"buffer_load_dwordx4 v24, s[76:79], s84 offen offset:0 lds\n" \
"v_cvt_pk_bf16_f32 v213, v54, v55\n" \
"v_mfma_f32_16x16x32_bf16 v[132:135], a[176:179], a[148:151], 0\n" \
"ds_read_b64_tr_b16 a[164:165], v33 offset:32768\n" \
"v_cvt_pk_bf16_f32 v214, v68, v69\n" \
"v_mfma_f32_16x16x32_bf16 v[132:135], a[240:243], a[212:215], v[132:135]\n" \
"ds_read_b128 a[148:151], v28 offset:43008\n" \
"v_cvt_pk_bf16_f32 v215, v70, v71\n" \
"v_mfma_f32_16x16x32_bf16 v[148:151], a[176:179], a[152:155], 0\n" \
"ds_read_b128 a[212:215], v29 offset:43008\n" \
"v_mfma_f32_16x16x32_bf16 v[148:151], a[240:243], a[216:219], v[148:151]\n" \
"s_movk_i32 s84, 0x2100\n" \
"v_mfma_f32_16x16x32_bf16 v[152:155], a[180:183], a[152:155], 0\n" \
"buffer_load_dwordx4 v25, s[76:79], s84 offen offset:1024 lds\n" \
"v_mfma_f32_16x16x32_bf16 v[152:155], a[244:247], a[216:219], v[152:155]\n" \
"ds_read_b64_tr_b16 a[166:167], v33 offset:33280\n" \
"v_mfma_f32_16x16x32_bf16 v[156:159], a[184:187], a[152:155], 0\n" \
"ds_read_b64_tr_b16 a[228:229], v33 offset:36864\n" \
"v_mfma_f32_16x16x32_bf16 v[156:159], a[248:251], a[216:219], v[156:159]\n" \
"s_movk_i32 s84, 0x4100\n" \
"v_mfma_f32_16x16x32_bf16 v[160:163], a[188:191], a[152:155], 0\n" \
"buffer_load_dwordx4 v24, s[76:79], s84 offen offset:2048 lds\n" \
"v_mfma_f32_16x16x32_bf16 v[160:163], a[252:255], a[216:219], v[160:163]\n" \
"ds_read_b128 a[152:155], v28 offset:45056\n" \
"v_mfma_f32_16x16x32_bf16 v[176:179], a[188:191], a[156:159], 0\n" \
"ds_read_b128 a[216:219], v29 offset:45056\n" \
"v_mfma_f32_16x16x32_bf16 v[176:179], a[252:255], a[220:223], v[176:179]\n" \
"s_movk_i32 s84, 0x6100\n" \
"s_nop 0\n" \
"v_mfma_f32_16x16x32_bf16 v[172:175], a[184:187], a[156:159], 0\n" \
"buffer_load_dwordx4 v25, s[76:79], s84 offen offset:3072 lds\n" \
"v_mfma_f32_16x16x32_bf16 v[172:175], a[248:251], a[220:223], v[172:175]\n" \
"ds_read_b64_tr_b16 a[230:231], v33 offset:37376\n" \
"v_mfma_f32_16x16x32_bf16 v[168:171], a[180:183], a[156:159], 0\n" \
"ds_read_b64_tr_b16 a[172:173], v35 offset:32768\n" \
"v_mfma_f32_16x16x32_bf16 v[168:171], a[244:247], a[220:223], v[168:171]\n" \
"ds_read_b64_tr_b16 a[174:175], v35 offset:33280\n" \
"v_mfma_f32_16x16x32_bf16 v[164:167], a[176:179], a[156:159], 0\n" \
"v_mfma_f32_16x16x32_bf16 v[164:167], a[240:243], a[220:223], v[164:167]\n" \
"ds_read_b128 a[156:159], v28 offset:47104\n" \
"v_mfma_f32_16x16x32_bf16 v[84:87], a[176:179], a[136:139], 0\n" \
"ds_read_b128 a[220:223], v29 offset:47104\n" \
"v_mfma_f32_16x16x32_bf16 v[84:87], a[240:243], a[200:203], v[84:87]\n" \
"s_add_u32 m0, s31, 0x0\n" \
"s_mov_b32 s84, 0x100000\n" \
"v_mfma_f32_16x16x32_bf16 v[88:91], a[180:183], a[136:139], 0\n" \
"buffer_load_dwordx4 v26, s[68:71], s84 offen offset:0 lds\n" \
"v_mfma_f32_16x16x32_bf16 v[88:91], a[244:247], a[200:203], v[88:91]\n" \
"s_addk_i32 s84, 0x7c00\n" \
"v_mfma_f32_16x16x32_bf16 v[92:95], a[184:187], a[136:139], 0\n" \
"buffer_load_dwordx4 v26, s[68:71], s84 offen offset:1024 lds\n" \
"v_mfma_f32_16x16x32_bf16 v[92:95], a[248:251], a[200:203], v[92:95]\n" \
"ds_read_b64_tr_b16 a[236:237], v35 offset:36864\n" \
"v_mfma_f32_16x16x32_bf16 v[96:99], a[188:191], a[136:139], 0\n" \
"ds_read_b64_tr_b16 a[238:239], v35 offset:37376\n" \
"v_mfma_f32_16x16x32_bf16 v[96:99], a[252:255], a[200:203], v[96:99]\n" \
"ds_read_b128 a[136:139], v28 offset:36864\n" \
"v_mfma_f32_16x16x32_bf16 v[112:115], a[188:191], a[140:143], 0\n" \
"ds_read_b128 a[200:203], v29 offset:36864\n" \
"v_mfma_f32_16x16x32_bf16 v[112:115], a[252:255], a[204:207], v[112:115]\n" \
"s_addk_i32 s84, 0x7c00\n" \
"v_mfma_f32_16x16x32_bf16 v[108:111], a[184:187], a[140:143], 0\n" \
"buffer_load_dwordx4 v27, s[68:71], s84 offen offset:2048 lds\n" \
"v_mfma_f32_16x16x32_bf16 v[108:111], a[248:251], a[204:207], v[108:111]\n" \
"s_addk_i32 s84, 0x7c00\n" \
"v_mfma_f32_16x16x32_bf16 v[104:107], a[180:183], a[140:143], 0\n" \
"buffer_load_dwordx4 v27, s[68:71], s84 offen offset:3072 lds\n" \
"v_mfma_f32_16x16x32_bf16 v[104:107], a[244:247], a[204:207], v[104:107]\n" \
"ds_read_b64_tr_b16 a[168:169], v34 offset:32768\n" \
"v_mfma_f32_16x16x32_bf16 v[100:103], a[176:179], a[140:143], 0\n" \
"ds_read_b64_tr_b16 a[170:171], v34 offset:33280\n" \
"v_mfma_f32_16x16x32_bf16 v[100:103], a[240:243], a[204:207], v[100:103]\n" \
"ds_read_b128 a[140:143], v28 offset:38912\n" \
"v_mfma_f32_16x16x32_bf16 v[52:55], a[176:179], a[128:131], 0\n" \
"ds_read_b128 a[204:207], v29 offset:38912\n" \
"v_mfma_f32_16x16x32_bf16 v[52:55], a[240:243], a[192:195], v[52:55]\n" \
"s_add_u32 m0, s30, 0x1000\n" \
"s_mov_b32 s84, 0x9100\n" \
"v_mfma_f32_16x16x32_bf16 v[56:59], a[180:183], a[128:131], 0\n" \
"buffer_load_dwordx4 v24, s[76:79], s84 offen offset:0 lds\n" \
"v_mfma_f32_16x16x32_bf16 v[56:59], a[244:247], a[192:195], v[56:59]\n" \
"s_addk_i32 s84, 0x2000\n" \
"s_nop 0\n" \
"v_mfma_f32_16x16x32_bf16 v[60:63], a[184:187], a[128:131], 0\n" \
"buffer_load_dwordx4 v25, s[76:79], s84 offen offset:1024 lds\n" \
"v_mfma_f32_16x16x32_bf16 v[60:63], a[248:251], a[192:195], v[60:63]\n" \
"ds_read_b64_tr_b16 a[232:233], v34 offset:36864\n" \
"v_mfma_f32_16x16x32_bf16 v[64:67], a[188:191], a[128:131], 0\n" \
"ds_read_b64_tr_b16 a[234:235], v34 offset:37376\n" \
"v_mfma_f32_16x16x32_bf16 v[64:67], a[252:255], a[192:195], v[64:67]\n" \
"ds_read_b128 a[128:131], v28 offset:32768\n" \
"v_mfma_f32_16x16x32_bf16 v[80:83], a[188:191], a[132:135], 0\n" \
"ds_read_b128 a[192:195], v29 offset:32768\n" \
"v_mfma_f32_16x16x32_bf16 v[80:83], a[252:255], a[196:199], v[80:83]\n" \
"v_mfma_f32_16x16x32_bf16 v[76:79], a[184:187], a[132:135], 0\n" \
"v_mfma_f32_16x16x32_bf16 v[76:79], a[248:251], a[196:199], v[76:79]\n" \
"v_mfma_f32_16x16x32_bf16 v[72:75], a[180:183], a[132:135], 0\n" \
"v_mfma_f32_16x16x32_bf16 v[72:75], a[244:247], a[196:199], v[72:75]\n" \
"v_mfma_f32_16x16x32_bf16 v[68:71], a[176:179], a[132:135], 0\n" \
"v_mfma_f32_16x16x32_bf16 v[68:71], a[240:243], a[196:199], v[68:71]\n" \
"ds_read_b128 a[132:135], v28 offset:34816\n" \
"ds_read_b128 a[196:199], v29 offset:34816\n" \
"s_waitcnt lgkmcnt(15)\n" \
"v_mfma_f32_16x16x32_bf16 a[0:3], a[160:163], a[144:147], a[0:3]\n" \
"s_addk_i32 s84, 0x2000\n" \
"v_mfma_f32_16x16x32_bf16 a[0:3], a[224:227], a[208:211], a[0:3]\n" \
"buffer_load_dwordx4 v24, s[76:79], s84 offen offset:2048 lds\n" \
"v_mfma_f32_16x16x32_bf16 a[4:7], a[164:167], a[144:147], a[4:7]\n" \
"ds_read_b64_tr_b16 a[176:177], v36 offset:32768\n" \
"v_mov_b32_e64 v11, 0\n" \
"v_mfma_f32_16x16x32_bf16 a[4:7], a[228:231], a[208:211], a[4:7]\n" \
"s_addk_i32 s84, 0x2000\n" \
"v_mfma_f32_16x16x32_bf16 a[12:15], a[172:175], a[144:147], a[12:15]\n" \
"buffer_load_dwordx4 v25, s[76:79], s84 offen offset:3072 lds\n" \
"s_waitcnt lgkmcnt(13)\n" \
"v_mfma_f32_16x16x32_bf16 a[12:15], a[236:239], a[208:211], a[12:15]\n" \
"ds_read_b64_tr_b16 a[178:179], v36 offset:33792\n" \
"s_and_b32 s6, s2, 7\n" \
"s_waitcnt lgkmcnt(10)\n" \
"v_mfma_f32_16x16x32_bf16 a[8:11], a[144:147], a[168:171], a[8:11]\n" \
"ds_read_b64_tr_b16 a[240:241], v36 offset:40960\n" \
"s_lshl_b32 s7, s3, 1\n" \
"s_waitcnt lgkmcnt(7)\n" \
"v_mfma_f32_16x16x32_bf16 a[8:11], a[208:211], a[232:235], a[8:11]\n" \
"ds_read_b64_tr_b16 a[242:243], v36 offset:41984\n" \
"s_lshr_b32 s4, s2, 5\n" \
"v_mfma_f32_16x16x32_bf16 a[24:27], a[148:151], a[168:171], a[24:27]\n" \
"ds_read_b64_tr_b16 a[180:181], v37 offset:32768\n" \
"s_lshl_b32 s5, s6, 2\n" \
"v_mfma_f32_16x16x32_bf16 a[24:27], a[212:215], a[232:235], a[24:27]\n" \
"ds_read_b64_tr_b16 a[182:183], v37 offset:33792\n" \
"s_bfe_u32 s8, s2, 0x20003\n" \
"v_mfma_f32_16x16x32_bf16 a[28:31], a[172:175], a[148:151], a[28:31]\n" \
"ds_read_b64_tr_b16 a[244:245], v37 offset:40960\n" \
"s_and_b32 s7, s7, 4\n" \
"v_mfma_f32_16x16x32_bf16 a[28:31], a[236:239], a[212:215], a[28:31]\n" \
"s_add_u32 s89, s41, 0x3000\n" \
"buffer_store_dwordx4 v[192:195], v20, s[80:83], s89 offen offset:3072 sc1\n" \
"v_mfma_f32_16x16x32_bf16 a[20:23], a[164:167], a[148:151], a[20:23]\n" \
"ds_read_b64_tr_b16 a[246:247], v37 offset:41984\n" \
"s_and_b32 s9, s3, 1\n" \
"v_mfma_f32_16x16x32_bf16 a[20:23], a[228:231], a[212:215], a[20:23]\n" \
"ds_read_b64_tr_b16 a[184:185], v38 offset:32768\n" \
"s_or_b32 s13, s5, s8\n" \
"v_mfma_f32_16x16x32_bf16 a[16:19], a[160:163], a[148:151], a[16:19]\n" \
"ds_read_b64_tr_b16 a[186:187], v38 offset:33792\n" \
"s_or_b32 s9, s7, s9\n" \
"v_mfma_f32_16x16x32_bf16 a[16:19], a[224:227], a[212:215], a[16:19]\n" \
"s_mul_i32 s7, s4, 0xa00\n" \
"s_add_i32 s7, s7, 0x5bc1000\n" \
"v_mfma_f32_16x16x32_bf16 a[32:35], a[160:163], a[152:155], a[32:35]\n" \
"s_add_u32 s88, s41, 0x3000\n" \
"buffer_store_dwordx4 v[196:199], v20, s[80:83], s88 offen offset:2048 sc1\n" \
"v_mfma_f32_16x16x32_bf16 a[32:35], a[224:227], a[216:219], a[32:35]\n" \
"ds_read_b64_tr_b16 a[248:249], v38 offset:40960\n" \
"s_lshl_b32 s10, s13, 2\n" \
"v_mfma_f32_16x16x32_bf16 a[36:39], a[164:167], a[152:155], a[36:39]\n" \
"ds_read_b64_tr_b16 a[250:251], v38 offset:41984\n" \
"s_or_b32 s10, s10, s7\n" \
"v_mfma_f32_16x16x32_bf16 a[36:39], a[228:231], a[216:219], a[36:39]\n" \
"ds_read_b64_tr_b16 a[188:189], v39 offset:32768\n" \
"v_writelane_b32 v11, s10, 0\n" \
"v_mfma_f32_16x16x32_bf16 a[44:47], a[172:175], a[152:155], a[44:47]\n" \
"s_lshl_b32 s14, s4, 3\n" \
"v_writelane_b32 v11, s7, 1\n" \
"v_mfma_f32_16x16x32_bf16 a[44:47], a[236:239], a[216:219], a[44:47]\n" \
"ds_read_b64_tr_b16 a[190:191], v39 offset:33792\n" \
"s_or_b32 s7, s14, s6\n" \
"v_mfma_f32_16x16x32_bf16 a[40:43], a[152:155], a[168:171], a[40:43]\n" \
"s_add_u32 s89, s41, 0x3000\n" \
"buffer_store_dwordx4 a[84:87], v20, s[80:83], s89 offen offset:1024 sc1\n" \
"v_mfma_f32_16x16x32_bf16 a[40:43], a[216:219], a[232:235], a[40:43]\n" \
"ds_read_b64_tr_b16 a[252:253], v39 offset:40960\n" \
"s_lshl_b32 s7, s7, 10\n" \
"v_mfma_f32_16x16x32_bf16 a[56:59], a[156:159], a[168:171], a[56:59]\n" \
"ds_read_b64_tr_b16 a[254:255], v39 offset:41984\n" \
"s_lshl_b32 s10, s3, 8\n" \
"v_mfma_f32_16x16x32_bf16 a[56:59], a[220:223], a[232:235], a[56:59]\n" \
"s_or_b32 s7, s7, s10\n" \
"s_or_b32 s10, s7, 0x5c00200\n" \
"v_mfma_f32_16x16x32_bf16 a[60:63], a[172:175], a[156:159], a[60:63]\n" \
"v_writelane_b32 v11, s10, 2\n" \
"s_lshl_b32 s10, s8, 2\n" \
"v_mfma_f32_16x16x32_bf16 a[60:63], a[236:239], a[220:223], a[60:63]\n" \
"s_add_u32 s88, s41, 0x3000\n" \
"buffer_store_dwordx4 v[252:255], v20, s[80:83], s88 offen offset:0 sc1\n" \
"v_mfma_f32_16x16x32_bf16 a[52:55], a[164:167], a[156:159], a[52:55]\n" \
"s_or_b32 s11, s10, s7\n" \
"s_or_b32 s11, s11, 0x5c00000\n" \
"v_mfma_f32_16x16x32_bf16 a[52:55], a[228:231], a[220:223], a[52:55]\n" \
"v_writelane_b32 v11, s11, 3\n" \
"s_or_b32 s7, s7, 0x5c00080\n" \
"v_mfma_f32_16x16x32_bf16 a[48:51], a[160:163], a[156:159], a[48:51]\n" \
"v_writelane_b32 v11, s7, 4\n" \
"s_or_b32 s7, s7, s10\n" \
"v_mfma_f32_16x16x32_bf16 a[48:51], a[224:227], a[220:223], a[48:51]\n" \
"s_add_i32 s10, s3, s2\n" \
"v_writelane_b32 v11, s7, 5\n" \
"v_mfma_f32_16x16x32_bf16 a[64:67], a[164:167], a[140:143], a[64:67]\n" \
"s_mov_b32 s7, 2.0\n" \
"s_mov_b32 s11, 0x5bc0000\n" \
"v_mfma_f32_16x16x32_bf16 a[64:67], a[228:231], a[204:207], a[64:67]\n" \
"s_cmp_lg_u32 s10, 0\n" \
"s_cselect_b32 s10, s7, s11\n" \
"v_mfma_f32_16x16x32_bf16 a[80:83], a[172:175], a[140:143], a[80:83]\n" \
"s_lshr_b32 s15, s2, 3\n" \
"v_writelane_b32 v11, s10, 53\n" \
"v_mfma_f32_16x16x32_bf16 a[80:83], a[236:239], a[204:207], a[80:83]\n" \
"s_lshl_b32 s10, s6, 5\n" \
"s_min_u32 s16, s3, 2\n" \
"v_mfma_f32_16x16x32_bf16 a[68:71], a[140:143], a[168:171], a[68:71]\n" \
"s_mul_i32 s12, s8, 0x120\n" \
"v_mfma_f32_16x16x32_bf16 a[68:71], a[204:207], a[232:235], a[68:71]\n" \
"s_waitcnt vmcnt(4) lgkmcnt(0)\n" \
"s_barrier\n" \
"s_nop 0\n" \
"v_mfma_f32_16x16x32_bf16 v[116:119], a[176:179], a[144:147], v[116:119]\n" \
"s_add_u32 m0, s40, 0x8000\n" \
"s_add_u32 s85, s39, 0x6000\n" \
"v_mfma_f32_16x16x32_bf16 v[116:119], a[240:243], a[208:211], v[116:119]\n" \
"buffer_load_dwordx4 v20, s[80:83], s85 offen sc1 lds\n" \
"v_mfma_f32_16x16x32_bf16 v[120:123], a[180:183], a[144:147], v[120:123]\n" \
"ds_read_b64_tr_b16 a[160:161], v32 offset:0\n" \
"v_mfma_f32_16x16x32_bf16 v[120:123], a[244:247], a[208:211], v[120:123]\n" \
"ds_read_b64_tr_b16 a[162:163], v32 offset:512\n" \
"v_mfma_f32_16x16x32_bf16 v[124:127], a[184:187], a[144:147], v[124:127]\n" \
"s_add_u32 m0, s40, 0x9000\n" \
"s_add_u32 s85, s39, 0x7000\n" \
"v_mfma_f32_16x16x32_bf16 v[124:127], a[248:251], a[208:211], v[124:127]\n" \
"buffer_load_dwordx4 v20, s[80:83], s85 offen sc1 lds\n" \
"v_mfma_f32_16x16x32_bf16 v[128:131], a[188:191], a[144:147], v[128:131]\n" \
"ds_read_b64_tr_b16 a[224:225], v32 offset:4096\n" \
"v_mfma_f32_16x16x32_bf16 v[128:131], a[252:255], a[208:211], v[128:131]\n" \
"ds_read_b128 a[144:147], v28 offset:8192\n" \
"v_mfma_f32_16x16x32_bf16 v[144:147], a[188:191], a[148:151], v[144:147]\n" \
"ds_read_b128 a[208:211], v29 offset:8192\n" \
"v_mfma_f32_16x16x32_bf16 v[144:147], a[252:255], a[212:215], v[144:147]\n" \
"s_add_u32 m0, s40, 0xb000\n" \
"s_movk_i32 s84, 0x180\n" \
"v_mfma_f32_16x16x32_bf16 v[140:143], a[184:187], a[148:151], v[140:143]\n" \
"buffer_load_dwordx4 v21, s[76:79], s84 offen offset:0 lds\n" \
"v_mfma_f32_16x16x32_bf16 v[140:143], a[248:251], a[212:215], v[140:143]\n" \
"ds_read_b64_tr_b16 a[226:227], v32 offset:4608\n" \
"v_mfma_f32_16x16x32_bf16 v[136:139], a[180:183], a[148:151], v[136:139]\n" \
"s_add_u32 m0, s30, 0x8000\n" \
"s_movk_i32 s84, 0x180\n" \
"v_mfma_f32_16x16x32_bf16 v[136:139], a[244:247], a[212:215], v[136:139]\n" \
"buffer_load_dwordx4 v24, s[76:79], s84 offen offset:0 lds\n" \
"v_mfma_f32_16x16x32_bf16 v[132:135], a[176:179], a[148:151], v[132:135]\n" \
"ds_read_b64_tr_b16 a[164:165], v33 offset:0\n" \
"v_mfma_f32_16x16x32_bf16 v[132:135], a[240:243], a[212:215], v[132:135]\n" \
"ds_read_b128 a[148:151], v28 offset:10240\n" \
"v_mfma_f32_16x16x32_bf16 v[148:151], a[176:179], a[152:155], v[148:151]\n" \
"ds_read_b128 a[212:215], v29 offset:10240\n" \
"v_mfma_f32_16x16x32_bf16 v[148:151], a[240:243], a[216:219], v[148:151]\n" \
"s_movk_i32 s84, 0x2180\n" \
"v_mfma_f32_16x16x32_bf16 v[152:155], a[180:183], a[152:155], v[152:155]\n" \
"buffer_load_dwordx4 v25, s[76:79], s84 offen offset:1024 lds\n" \
"v_mfma_f32_16x16x32_bf16 v[152:155], a[244:247], a[216:219], v[152:155]\n" \
"ds_read_b64_tr_b16 a[166:167], v33 offset:512\n" \
"v_mfma_f32_16x16x32_bf16 v[156:159], a[184:187], a[152:155], v[156:159]\n" \
"ds_read_b64_tr_b16 a[228:229], v33 offset:4096\n" \
"v_mfma_f32_16x16x32_bf16 v[156:159], a[248:251], a[216:219], v[156:159]\n" \
"s_movk_i32 s84, 0x4180\n" \
"v_mfma_f32_16x16x32_bf16 v[160:163], a[188:191], a[152:155], v[160:163]\n" \
"buffer_load_dwordx4 v24, s[76:79], s84 offen offset:2048 lds\n" \
"v_mfma_f32_16x16x32_bf16 v[160:163], a[252:255], a[216:219], v[160:163]\n" \
"ds_read_b128 a[152:155], v28 offset:12288\n" \
"v_mfma_f32_16x16x32_bf16 v[176:179], a[188:191], a[156:159], v[176:179]\n" \
"ds_read_b128 a[216:219], v29 offset:12288\n" \
"v_mfma_f32_16x16x32_bf16 v[176:179], a[252:255], a[220:223], v[176:179]\n" \
"s_movk_i32 s84, 0x6180\n" \
"v_mfma_f32_16x16x32_bf16 v[172:175], a[184:187], a[156:159], v[172:175]\n" \
"buffer_load_dwordx4 v25, s[76:79], s84 offen offset:3072 lds\n" \
"v_mfma_f32_16x16x32_bf16 v[172:175], a[248:251], a[220:223], v[172:175]\n" \
"ds_read_b64_tr_b16 a[230:231], v33 offset:4608\n" \
"v_mfma_f32_16x16x32_bf16 v[168:171], a[180:183], a[156:159], v[168:171]\n" \
"ds_read_b64_tr_b16 a[172:173], v35 offset:0\n" \
"v_mfma_f32_16x16x32_bf16 v[168:171], a[244:247], a[220:223], v[168:171]\n" \
"ds_read_b64_tr_b16 a[174:175], v35 offset:512\n" \
"v_mfma_f32_16x16x32_bf16 v[164:167], a[176:179], a[156:159], v[164:167]\n" \
"s_add_u32 s11, s22, s12\n" \
"s_addc_u32 s17, s23, 0\n" \
"v_mfma_f32_16x16x32_bf16 v[164:167], a[240:243], a[220:223], v[164:167]\n" \
"ds_read_b128 a[156:159], v28 offset:14336\n" \
"v_mfma_f32_16x16x32_bf16 v[84:87], a[176:179], a[136:139], v[84:87]\n" \
"ds_read_b128 a[220:223], v29 offset:14336\n" \
"v_mfma_f32_16x16x32_bf16 v[84:87], a[240:243], a[200:203], v[84:87]\n" \
"s_add_u32 m0, s31, 0x8000\n" \
"s_mov_b32 s84, 0x180000\n" \
"v_mfma_f32_16x16x32_bf16 v[88:91], a[180:183], a[136:139], v[88:91]\n" \
"buffer_load_dwordx4 v26, s[68:71], s84 offen offset:0 lds\n" \
"v_mfma_f32_16x16x32_bf16 v[88:91], a[244:247], a[200:203], v[88:91]\n" \
"s_addk_i32 s84, 0x7c00\n" \
"v_mfma_f32_16x16x32_bf16 v[92:95], a[184:187], a[136:139], v[92:95]\n" \
"buffer_load_dwordx4 v26, s[68:71], s84 offen offset:1024 lds\n" \
"v_mfma_f32_16x16x32_bf16 v[92:95], a[248:251], a[200:203], v[92:95]\n" \
"ds_read_b64_tr_b16 a[236:237], v35 offset:4096\n" \
"v_mfma_f32_16x16x32_bf16 v[96:99], a[188:191], a[136:139], v[96:99]\n" \
"ds_read_b64_tr_b16 a[238:239], v35 offset:4608\n" \
"v_mfma_f32_16x16x32_bf16 v[96:99], a[252:255], a[200:203], v[96:99]\n" \
"ds_read_b128 a[136:139], v28 offset:4096\n" \
"v_mfma_f32_16x16x32_bf16 v[112:115], a[188:191], a[140:143], v[112:115]\n" \
"ds_read_b128 a[200:203], v29 offset:4096\n" \
"v_mfma_f32_16x16x32_bf16 v[112:115], a[252:255], a[204:207], v[112:115]\n" \
"s_addk_i32 s84, 0x7c00\n" \
"v_mfma_f32_16x16x32_bf16 v[108:111], a[184:187], a[140:143], v[108:111]\n" \
"buffer_load_dwordx4 v27, s[68:71], s84 offen offset:2048 lds\n" \
"v_mfma_f32_16x16x32_bf16 v[108:111], a[248:251], a[204:207], v[108:111]\n" \
"s_addk_i32 s84, 0x7c00\n" \
"v_mfma_f32_16x16x32_bf16 v[104:107], a[180:183], a[140:143], v[104:107]\n" \
"buffer_load_dwordx4 v27, s[68:71], s84 offen offset:3072 lds\n" \
"v_mfma_f32_16x16x32_bf16 v[104:107], a[244:247], a[204:207], v[104:107]\n" \
"ds_read_b64_tr_b16 a[168:169], v34 offset:0\n" \
"v_mfma_f32_16x16x32_bf16 v[100:103], a[176:179], a[140:143], v[100:103]\n" \
"ds_read_b64_tr_b16 a[170:171], v34 offset:512\n" \
"v_mfma_f32_16x16x32_bf16 v[100:103], a[240:243], a[204:207], v[100:103]\n" \
"ds_read_b128 a[140:143], v28 offset:6144\n" \
"v_mfma_f32_16x16x32_bf16 v[52:55], a[176:179], a[128:131], v[52:55]\n" \
"ds_read_b128 a[204:207], v29 offset:6144\n" \
"v_mfma_f32_16x16x32_bf16 v[52:55], a[240:243], a[192:195], v[52:55]\n" \
"s_add_u32 m0, s30, 0x9000\n" \
"s_mov_b32 s84, 0x9180\n" \
"v_mfma_f32_16x16x32_bf16 v[56:59], a[180:183], a[128:131], v[56:59]\n" \
"buffer_load_dwordx4 v24, s[76:79], s84 offen offset:0 lds\n" \
"v_mfma_f32_16x16x32_bf16 v[56:59], a[244:247], a[192:195], v[56:59]\n" \
"s_addk_i32 s84, 0x2000\n" \
"s_nop 0\n" \
"v_mfma_f32_16x16x32_bf16 v[60:63], a[184:187], a[128:131], v[60:63]\n" \
"buffer_load_dwordx4 v25, s[76:79], s84 offen offset:1024 lds\n" \
"v_mfma_f32_16x16x32_bf16 v[60:63], a[248:251], a[192:195], v[60:63]\n" \
"ds_read_b64_tr_b16 a[232:233], v34 offset:4096\n" \
"v_mfma_f32_16x16x32_bf16 v[64:67], a[188:191], a[128:131], v[64:67]\n" \
"ds_read_b64_tr_b16 a[234:235], v34 offset:4608\n" \
"v_mfma_f32_16x16x32_bf16 v[64:67], a[252:255], a[192:195], v[64:67]\n" \
"ds_read_b128 a[128:131], v28 offset:0\n" \
"v_mfma_f32_16x16x32_bf16 v[80:83], a[188:191], a[132:135], v[80:83]\n" \
"ds_read_b128 a[192:195], v29 offset:0\n" \
"v_mfma_f32_16x16x32_bf16 v[80:83], a[252:255], a[196:199], v[80:83]\n" \
"s_add_u32 s18, s24, s12\n" \
"v_writelane_b32 v11, s11, 9\n" \
"s_addc_u32 s19, s25, 0\n" \
"v_mfma_f32_16x16x32_bf16 v[76:79], a[184:187], a[132:135], v[76:79]\n" \
"s_and_b32 s20, s17, 0xffff\n" \
"v_writelane_b32 v11, s20, 10\n" \
"v_mfma_f32_16x16x32_bf16 v[76:79], a[248:251], a[196:199], v[76:79]\n" \
"s_and_b32 s20, s19, 0xffff\n" \
"v_writelane_b32 v11, s18, 11\n" \
"v_mfma_f32_16x16x32_bf16 v[72:75], a[180:183], a[132:135], v[72:75]\n" \
"s_cmpk_lt_u32 s3, 3\n" \
"s_cselect_b32 s11, s11, s18\n" \
"v_mfma_f32_16x16x32_bf16 v[72:75], a[244:247], a[196:199], v[72:75]\n" \
"s_movk_i32 s18, 0xfff\n" \
"v_writelane_b32 v11, s20, 12\n" \
"v_mfma_f32_16x16x32_bf16 v[68:71], a[176:179], a[132:135], v[68:71]\n" \
"s_and_b32 s20, s29, 0xffff\n" \
"v_writelane_b32 v11, s28, 13\n" \
"v_mfma_f32_16x16x32_bf16 v[68:71], a[240:243], a[196:199], v[68:71]\n" \
"ds_read_b128 a[132:135], v28 offset:2048\n" \
"ds_read_b128 a[196:199], v29 offset:2048\n" \
"s_waitcnt lgkmcnt(15)\n" \
"v_mfma_f32_16x16x32_bf16 a[0:3], a[160:163], a[144:147], a[0:3]\n" \
"s_addk_i32 s84, 0x2000\n" \
"v_mfma_f32_16x16x32_bf16 a[0:3], a[224:227], a[208:211], a[0:3]\n" \
"buffer_load_dwordx4 v24, s[76:79], s84 offen offset:2048 lds\n" \
"v_mfma_f32_16x16x32_bf16 a[4:7], a[164:167], a[144:147], a[4:7]\n" \
"ds_read_b64_tr_b16 a[176:177], v36 offset:0\n" \
"v_writelane_b32 v11, s20, 14\n" \
"v_mfma_f32_16x16x32_bf16 a[4:7], a[228:231], a[208:211], a[4:7]\n" \
"s_addk_i32 s84, 0x2000\n" \
"v_mfma_f32_16x16x32_bf16 a[12:15], a[172:175], a[144:147], a[12:15]\n" \
"buffer_load_dwordx4 v25, s[76:79], s84 offen offset:3072 lds\n" \
"s_waitcnt lgkmcnt(13)\n" \
"v_mfma_f32_16x16x32_bf16 a[12:15], a[236:239], a[208:211], a[12:15]\n" \
"ds_read_b64_tr_b16 a[178:179], v36 offset:1024\n" \
"v_writelane_b32 v11, s11, 15\n" \
"s_waitcnt lgkmcnt(10)\n" \
"v_mfma_f32_16x16x32_bf16 a[8:11], a[144:147], a[168:171], a[8:11]\n" \
"ds_read_b64_tr_b16 a[240:241], v36 offset:8192\n" \
"s_waitcnt lgkmcnt(7)\n" \
"s_nop 0\n" \
"v_mfma_f32_16x16x32_bf16 a[8:11], a[208:211], a[232:235], a[8:11]\n" \
"ds_read_b64_tr_b16 a[242:243], v36 offset:9216\n" \
"v_mfma_f32_16x16x32_bf16 a[24:27], a[148:151], a[168:171], a[24:27]\n" \
"ds_read_b64_tr_b16 a[180:181], v37 offset:0\n" \
"v_mfma_f32_16x16x32_bf16 a[24:27], a[212:215], a[232:235], a[24:27]\n" \
"ds_read_b64_tr_b16 a[182:183], v37 offset:1024\n" \
"v_mfma_f32_16x16x32_bf16 a[28:31], a[172:175], a[148:151], a[28:31]\n" \
"ds_read_b64_tr_b16 a[244:245], v37 offset:8192\n" \
"v_mfma_f32_16x16x32_bf16 a[28:31], a[236:239], a[212:215], a[28:31]\n" \
"s_add_u32 s89, s41, 0x1000\n" \
"buffer_store_dwordx4 v[200:203], v20, s[80:83], s89 offen offset:3072 sc1\n" \
"v_mfma_f32_16x16x32_bf16 a[20:23], a[164:167], a[148:151], a[20:23]\n" \
"ds_read_b64_tr_b16 a[246:247], v37 offset:9216\n" \
"v_mfma_f32_16x16x32_bf16 a[20:23], a[228:231], a[212:215], a[20:23]\n" \
"ds_read_b64_tr_b16 a[184:185], v38 offset:0\n" \
"v_mfma_f32_16x16x32_bf16 a[16:19], a[160:163], a[148:151], a[16:19]\n" \
"ds_read_b64_tr_b16 a[186:187], v38 offset:1024\n" \
"v_mfma_f32_16x16x32_bf16 a[16:19], a[224:227], a[212:215], a[16:19]\n" \
"s_cmpk_lt_u32 s3, 3\n" \
"s_cselect_b32 s11, s17, s19\n" \
"v_mfma_f32_16x16x32_bf16 a[32:35], a[160:163], a[152:155], a[32:35]\n" \
"s_add_u32 s88, s41, 0x1000\n" \
"buffer_store_dwordx4 v[204:207], v20, s[80:83], s88 offen offset:2048 sc1\n" \
"v_mfma_f32_16x16x32_bf16 a[32:35], a[224:227], a[216:219], a[32:35]\n" \
"ds_read_b64_tr_b16 a[248:249], v38 offset:8192\n" \
"s_and_b32 s11, s11, 0xffff\n" \
"v_mfma_f32_16x16x32_bf16 a[36:39], a[164:167], a[152:155], a[36:39]\n" \
"ds_read_b64_tr_b16 a[250:251], v38 offset:9216\n" \
"v_writelane_b32 v11, s11, 16\n" \
"v_mfma_f32_16x16x32_bf16 a[36:39], a[228:231], a[216:219], a[36:39]\n" \
"ds_read_b64_tr_b16 a[188:189], v39 offset:0\n" \
"s_mul_i32 s11, s6, 0x48\n" \
"v_mfma_f32_16x16x32_bf16 a[44:47], a[172:175], a[152:155], a[44:47]\n" \
"s_add_i32 s17, s11, 0x47\n" \
"v_mfma_f32_16x16x32_bf16 a[44:47], a[236:239], a[216:219], a[44:47]\n" \
"ds_read_b64_tr_b16 a[190:191], v39 offset:1024\n" \
"v_mfma_f32_16x16x32_bf16 a[40:43], a[152:155], a[168:171], a[40:43]\n" \
"s_add_u32 s89, s41, 0x1000\n" \
"buffer_store_dwordx4 v[208:211], v20, s[80:83], s89 offen offset:1024 sc1\n" \
"v_mfma_f32_16x16x32_bf16 a[40:43], a[216:219], a[232:235], a[40:43]\n" \
"ds_read_b64_tr_b16 a[252:253], v39 offset:8192\n" \
"v_mfma_f32_16x16x32_bf16 a[56:59], a[156:159], a[168:171], a[56:59]\n" \
"ds_read_b64_tr_b16 a[254:255], v39 offset:9216\n" \
"v_mfma_f32_16x16x32_bf16 a[56:59], a[220:223], a[232:235], a[56:59]\n" \
"s_cmpk_lt_u32 s3, 3\n" \
"s_cselect_b32 s17, s17, s18\n" \
"v_mfma_f32_16x16x32_bf16 a[60:63], a[172:175], a[156:159], a[60:63]\n" \
"s_mulk_i32 s17, 0x480\n" \
"s_addk_i32 s17, 0x120\n" \
"v_mfma_f32_16x16x32_bf16 a[60:63], a[236:239], a[220:223], a[60:63]\n" \
"s_add_u32 s88, s41, 0x1000\n" \
"buffer_store_dwordx4 v[212:215], v20, s[80:83], s88 offen offset:0 sc1\n" \
"v_mfma_f32_16x16x32_bf16 a[52:55], a[164:167], a[156:159], a[52:55]\n" \
"v_writelane_b32 v11, s17, 17\n" \
"s_lshl_b32 s17, s3, 5\n" \
"v_mfma_f32_16x16x32_bf16 a[52:55], a[228:231], a[220:223], a[52:55]\n" \
"s_add_i32 s11, s11, s17\n" \
"s_mul_i32 s17, s11, 0x480\n" \
"v_mfma_f32_16x16x32_bf16 a[48:51], a[160:163], a[156:159], a[48:51]\n" \
"s_mul_i32 s11, s13, 0x24000\n" \
"s_add_i32 s18, s11, 0x1b000\n" \
"v_mfma_f32_16x16x32_bf16 a[48:51], a[224:227], a[220:223], a[48:51]\n" \
"s_cmpk_lt_u32 s3, 3\n" \
"s_cselect_b32 s17, s17, s18\n" \
"v_mfma_f32_16x16x32_bf16 a[64:67], a[164:167], a[140:143], a[64:67]\n" \
"s_mul_i32 s19, s3, 0x9000\n" \
"v_writelane_b32 v11, s17, 18\n" \
"v_mfma_f32_16x16x32_bf16 a[64:67], a[228:231], a[204:207], a[64:67]\n" \
"s_mul_i32 s17, s16, 0x2400\n" \
"s_mul_i32 s16, s16, 0x9000\n" \
"v_mfma_f32_16x16x32_bf16 a[80:83], a[172:175], a[140:143], a[80:83]\n" \
"s_bitset1_b32 s17, 17\n" \
"v_writelane_b32 v11, s17, 19\n" \
"v_mfma_f32_16x16x32_bf16 a[80:83], a[236:239], a[204:207], a[80:83]\n" \
"s_add_i32 s16, s11, s16\n" \
"v_writelane_b32 v11, s16, 25\n" \
"v_mfma_f32_16x16x32_bf16 a[68:71], a[140:143], a[168:171], a[68:71]\n" \
"s_mul_i32 s16, s3, 0x2400\n" \
"s_add_i32 s17, s16, 0x6000\n" \
"v_mfma_f32_16x16x32_bf16 a[68:71], a[204:207], a[232:235], a[68:71]\n" \
"s_waitcnt vmcnt(4) lgkmcnt(0)\n" \
"s_barrier\n" \
"v_mfma_f32_16x16x32_bf16 v[116:119], a[176:179], a[144:147], v[116:119]\n" \
"s_add_u32 m0, s40, 0x0\n" \
"s_nop 0\n" \
"s_add_u32 s85, s39, 0x8000\n" \
"v_mfma_f32_16x16x32_bf16 v[116:119], a[240:243], a[208:211], v[116:119]\n" \
"buffer_load_dwordx4 v20, s[80:83], s85 offen sc1 lds\n" \
"v_mfma_f32_16x16x32_bf16 v[120:123], a[180:183], a[144:147], v[120:123]\n" \
"ds_read_b64_tr_b16 a[160:161], v32 offset:32768\n" \
"v_writelane_b32 v11, s17, 20\n" \
"v_mfma_f32_16x16x32_bf16 v[120:123], a[244:247], a[208:211], v[120:123]\n" \
"ds_read_b64_tr_b16 a[162:163], v32 offset:33280\n" \
"s_or_b32 s17, s16, 0x10000\n" \
"v_mfma_f32_16x16x32_bf16 v[124:127], a[184:187], a[144:147], v[124:127]\n" \
"s_add_u32 m0, s40, 0x1000\n" \
"s_add_u32 s85, s39, 0x9000\n" \
"v_mfma_f32_16x16x32_bf16 v[124:127], a[248:251], a[208:211], v[124:127]\n" \
"buffer_load_dwordx4 v20, s[80:83], s85 offen sc1 lds\n" \
"v_mfma_f32_16x16x32_bf16 v[128:131], a[188:191], a[144:147], v[128:131]\n" \
"ds_read_b64_tr_b16 a[224:225], v32 offset:36864\n" \
"s_add_i32 s18, s16, 0x19000\n" \
"v_mfma_f32_16x16x32_bf16 v[128:131], a[252:255], a[208:211], v[128:131]\n" \
"ds_read_b128 a[144:147], v28 offset:40960\n" \
"v_writelane_b32 v11, s17, 21\n" \
".fill (L_pb + 65248 - .) / 4, 4, 0xBF800000\n" \
"L_pp16:\n" \
"s_cbranch_execz L_pret\n" \
"v_mfma_f32_16x16x32_bf16 v[144:147], a[188:191], a[148:151], v[144:147]\n" \
"ds_read_b128 a[208:211], v29 offset:40960\n" \
"s_mov_b32 s17, 0\n" \
"v_mfma_f32_16x16x32_bf16 v[144:147], a[252:255], a[212:215], v[144:147]\n" \
"s_add_u32 m0, s40, 0x3000\n" \
"s_movk_i32 s84, 0x200\n" \
"v_mfma_f32_16x16x32_bf16 v[140:143], a[184:187], a[148:151], v[140:143]\n" \
"buffer_load_dwordx4 v21, s[76:79], s84 offen offset:0 lds\n" \
"v_mfma_f32_16x16x32_bf16 v[140:143], a[248:251], a[212:215], v[140:143]\n" \
"ds_read_b64_tr_b16 a[226:227], v32 offset:37376\n" \
"v_mfma_f32_16x16x32_bf16 v[136:139], a[180:183], a[148:151], v[136:139]\n" \
"s_add_u32 m0, s30, 0x0\n" \
"s_movk_i32 s84, 0x200\n" \
"v_mfma_f32_16x16x32_bf16 v[136:139], a[244:247], a[212:215], v[136:139]\n" \
"buffer_load_dwordx4 v24, s[76:79], s84 offen offset:0 lds\n" \
"v_mfma_f32_16x16x32_bf16 v[132:135], a[176:179], a[148:151], v[132:135]\n" \
"ds_read_b64_tr_b16 a[164:165], v33 offset:32768\n" \
"v_mfma_f32_16x16x32_bf16 v[132:135], a[240:243], a[212:215], v[132:135]\n" \
"ds_read_b128 a[148:151], v28 offset:43008\n" \
"v_mfma_f32_16x16x32_bf16 v[148:151], a[176:179], a[152:155], v[148:151]\n" \
"ds_read_b128 a[212:215], v29 offset:43008\n" \
"v_mfma_f32_16x16x32_bf16 v[148:151], a[240:243], a[216:219], v[148:151]\n" \
"s_movk_i32 s84, 0x2200\n" \
"v_mfma_f32_16x16x32_bf16 v[152:155], a[180:183], a[152:155], v[152:155]\n" \
"buffer_load_dwordx4 v25, s[76:79], s84 offen offset:1024 lds\n" \
"v_mfma_f32_16x16x32_bf16 v[152:155], a[244:247], a[216:219], v[152:155]\n" \
"ds_read_b64_tr_b16 a[166:167], v33 offset:33280\n" \
"v_mfma_f32_16x16x32_bf16 v[156:159], a[184:187], a[152:155], v[156:159]\n" \
"ds_read_b64_tr_b16 a[228:229], v33 offset:36864\n" \
"v_mfma_f32_16x16x32_bf16 v[156:159], a[248:251], a[216:219], v[156:159]\n" \
"s_movk_i32 s84, 0x4200\n" \
"v_mfma_f32_16x16x32_bf16 v[160:163], a[188:191], a[152:155], v[160:163]\n" \
"buffer_load_dwordx4 v24, s[76:79], s84 offen offset:2048 lds\n" \
"v_mfma_f32_16x16x32_bf16 v[160:163], a[252:255], a[216:219], v[160:163]\n" \
"ds_read_b128 a[152:155], v28 offset:45056\n" \
"v_mfma_f32_16x16x32_bf16 v[176:179], a[188:191], a[156:159], v[176:179]\n" \
"ds_read_b128 a[216:219], v29 offset:45056\n" \
"v_mfma_f32_16x16x32_bf16 v[176:179], a[252:255], a[220:223], v[176:179]\n" \
"s_movk_i32 s84, 0x6200\n" \
"v_mfma_f32_16x16x32_bf16 v[172:175], a[184:187], a[156:159], v[172:175]\n" \
"buffer_load_dwordx4 v25, s[76:79], s84 offen offset:3072 lds\n" \
"v_mfma_f32_16x16x32_bf16 v[172:175], a[248:251], a[220:223], v[172:175]\n" \
"ds_read_b64_tr_b16 a[230:231], v33 offset:37376\n" \
"v_mfma_f32_16x16x32_bf16 v[168:171], a[180:183], a[156:159], v[168:171]\n" \
"ds_read_b64_tr_b16 a[172:173], v35 offset:32768\n" \
"v_mfma_f32_16x16x32_bf16 v[168:171], a[244:247], a[220:223], v[168:171]\n" \
"ds_read_b64_tr_b16 a[174:175], v35 offset:33280\n" \
"v_mfma_f32_16x16x32_bf16 v[164:167], a[176:179], a[156:159], v[164:167]\n" \
"s_cmpk_lt_u32 s3, 3\n" \
"s_cselect_b32 s18, s18, s17\n" \
"v_mfma_f32_16x16x32_bf16 v[164:167], a[240:243], a[220:223], v[164:167]\n" \
"ds_read_b128 a[156:159], v28 offset:47104\n" \
"s_bitset1_b32 s16, 17\n" \
"v_mfma_f32_16x16x32_bf16 v[84:87], a[176:179], a[136:139], v[84:87]\n" \
"ds_read_b128 a[220:223], v29 offset:47104\n" \
"v_writelane_b32 v11, s18, 22\n" \
"v_mfma_f32_16x16x32_bf16 v[84:87], a[240:243], a[200:203], v[84:87]\n" \
"s_add_u32 m0, s31, 0x0\n" \
"s_mov_b32 s84, 0x200000\n" \
"v_mfma_f32_16x16x32_bf16 v[88:91], a[180:183], a[136:139], v[88:91]\n" \
"buffer_load_dwordx4 v26, s[68:71], s84 offen offset:0 lds\n" \
"v_mfma_f32_16x16x32_bf16 v[88:91], a[244:247], a[200:203], v[88:91]\n" \
"s_addk_i32 s84, 0x7c00\n" \
"v_mfma_f32_16x16x32_bf16 v[92:95], a[184:187], a[136:139], v[92:95]\n" \
"buffer_load_dwordx4 v26, s[68:71], s84 offen offset:1024 lds\n" \
"v_mfma_f32_16x16x32_bf16 v[92:95], a[248:251], a[200:203], v[92:95]\n" \
"ds_read_b64_tr_b16 a[236:237], v35 offset:36864\n" \
"s_movk_i32 s18, 0x2400\n" \
"v_mfma_f32_16x16x32_bf16 v[96:99], a[188:191], a[136:139], v[96:99]\n" \
"ds_read_b64_tr_b16 a[238:239], v35 offset:37376\n" \
"v_mfma_f32_16x16x32_bf16 v[96:99], a[252:255], a[200:203], v[96:99]\n" \
"ds_read_b128 a[136:139], v28 offset:36864\n" \
"v_mfma_f32_16x16x32_bf16 v[112:115], a[188:191], a[140:143], v[112:115]\n" \
"ds_read_b128 a[200:203], v29 offset:36864\n" \
"v_mfma_f32_16x16x32_bf16 v[112:115], a[252:255], a[204:207], v[112:115]\n" \
"s_addk_i32 s84, 0x7c00\n" \
"v_mfma_f32_16x16x32_bf16 v[108:111], a[184:187], a[140:143], v[108:111]\n" \
"buffer_load_dwordx4 v27, s[68:71], s84 offen offset:2048 lds\n" \
"v_mfma_f32_16x16x32_bf16 v[108:111], a[248:251], a[204:207], v[108:111]\n" \
"s_addk_i32 s84, 0x7c00\n" \
"v_mfma_f32_16x16x32_bf16 v[104:107], a[180:183], a[140:143], v[104:107]\n" \
"buffer_load_dwordx4 v27, s[68:71], s84 offen offset:3072 lds\n" \
"v_mfma_f32_16x16x32_bf16 v[104:107], a[244:247], a[204:207], v[104:107]\n" \
"ds_read_b64_tr_b16 a[168:169], v34 offset:32768\n" \
"v_mfma_f32_16x16x32_bf16 v[100:103], a[176:179], a[140:143], v[100:103]\n" \
"ds_read_b64_tr_b16 a[170:171], v34 offset:33280\n" \
"v_mfma_f32_16x16x32_bf16 v[100:103], a[240:243], a[204:207], v[100:103]\n" \
"ds_read_b128 a[140:143], v28 offset:38912\n" \
"v_mfma_f32_16x16x32_bf16 v[52:55], a[176:179], a[128:131], v[52:55]\n" \
"ds_read_b128 a[204:207], v29 offset:38912\n" \
"v_mfma_f32_16x16x32_bf16 v[52:55], a[240:243], a[192:195], v[52:55]\n" \
"s_add_u32 m0, s30, 0x1000\n" \
"s_mov_b32 s84, 0x9200\n" \
"v_mfma_f32_16x16x32_bf16 v[56:59], a[180:183], a[128:131], v[56:59]\n" \
"buffer_load_dwordx4 v24, s[76:79], s84 offen offset:0 lds\n" \
"v_mfma_f32_16x16x32_bf16 v[56:59], a[244:247], a[192:195], v[56:59]\n" \
"s_addk_i32 s84, 0x2000\n" \
"s_nop 0\n" \
"v_mfma_f32_16x16x32_bf16 v[60:63], a[184:187], a[128:131], v[60:63]\n" \
"buffer_load_dwordx4 v25, s[76:79], s84 offen offset:1024 lds\n" \
"v_mfma_f32_16x16x32_bf16 v[60:63], a[248:251], a[192:195], v[60:63]\n" \
"ds_read_b64_tr_b16 a[232:233], v34 offset:36864\n" \
"v_mfma_f32_16x16x32_bf16 v[64:67], a[188:191], a[128:131], v[64:67]\n" \
"ds_read_b64_tr_b16 a[234:235], v34 offset:37376\n" \
"v_mfma_f32_16x16x32_bf16 v[64:67], a[252:255], a[192:195], v[64:67]\n" \
"ds_read_b128 a[128:131], v28 offset:32768\n" \
"v_mfma_f32_16x16x32_bf16 v[80:83], a[188:191], a[132:135], v[80:83]\n" \
"ds_read_b128 a[192:195], v29 offset:32768\n" \
"v_mfma_f32_16x16x32_bf16 v[80:83], a[252:255], a[196:199], v[80:83]\n" \
"s_cmpk_lt_u32 s3, 3\n" \
"s_cselect_b32 s16, s16, s18\n" \
"v_mfma_f32_16x16x32_bf16 v[76:79], a[184:187], a[132:135], v[76:79]\n" \
"s_xor_b32 s13, s13, 2\n" \
"v_writelane_b32 v11, s16, 23\n" \
"v_mfma_f32_16x16x32_bf16 v[76:79], a[248:251], a[196:199], v[76:79]\n" \
"s_lshl_b32 s16, s3, 10\n" \
"s_or_b32 s16, s16, 0xf000\n" \
"v_mfma_f32_16x16x32_bf16 v[72:75], a[180:183], a[132:135], v[72:75]\n" \
"v_writelane_b32 v11, s16, 24\n" \
"s_add_i32 s16, s15, 1\n" \
"v_mfma_f32_16x16x32_bf16 v[72:75], a[244:247], a[196:199], v[72:75]\n" \
"s_and_b32 s16, s16, 3\n" \
"s_or_b32 s16, s16, s5\n" \
"v_mfma_f32_16x16x32_bf16 v[68:71], a[176:179], a[132:135], v[68:71]\n" \
"s_mul_i32 s18, s16, 0x24000\n" \
"s_add_i32 s18, s18, s19\n" \
"v_mfma_f32_16x16x32_bf16 v[68:71], a[240:243], a[196:199], v[68:71]\n" \
"ds_read_b128 a[132:135], v28 offset:34816\n" \
"ds_read_b128 a[196:199], v29 offset:34816\n" \
"s_waitcnt lgkmcnt(15)\n" \
"v_mfma_f32_16x16x32_bf16 a[0:3], a[160:163], a[144:147], a[0:3]\n" \
"s_addk_i32 s84, 0x2000\n" \
"v_mfma_f32_16x16x32_bf16 a[0:3], a[224:227], a[208:211], a[0:3]\n" \
"buffer_load_dwordx4 v24, s[76:79], s84 offen offset:2048 lds\n" \
"v_mfma_f32_16x16x32_bf16 a[4:7], a[164:167], a[144:147], a[4:7]\n" \
"ds_read_b64_tr_b16 a[176:177], v36 offset:32768\n" \
"s_add_i32 s15, s15, -1\n" \
"v_mfma_f32_16x16x32_bf16 a[4:7], a[228:231], a[208:211], a[4:7]\n" \
"s_addk_i32 s84, 0x2000\n" \
"v_mfma_f32_16x16x32_bf16 a[12:15], a[172:175], a[144:147], a[12:15]\n" \
"buffer_load_dwordx4 v25, s[76:79], s84 offen offset:3072 lds\n" \
"s_waitcnt lgkmcnt(13)\n" \
"v_mfma_f32_16x16x32_bf16 a[12:15], a[236:239], a[208:211], a[12:15]\n" \
"ds_read_b64_tr_b16 a[178:179], v36 offset:33792\n" \
"v_writelane_b32 v11, s18, 26\n" \
"s_waitcnt lgkmcnt(10)\n" \
"v_mfma_f32_16x16x32_bf16 a[8:11], a[144:147], a[168:171], a[8:11]\n" \
"ds_read_b64_tr_b16 a[240:241], v36 offset:40960\n" \
"s_mul_i32 s18, s13, 0x24000\n" \
"s_waitcnt lgkmcnt(7)\n" \
"v_mfma_f32_16x16x32_bf16 a[8:11], a[208:211], a[232:235], a[8:11]\n" \
"ds_read_b64_tr_b16 a[242:243], v36 offset:41984\n" \
"s_and_b32 s15, s15, 3\n" \
"v_mfma_f32_16x16x32_bf16 a[24:27], a[148:151], a[168:171], a[24:27]\n" \
"ds_read_b64_tr_b16 a[180:181], v37 offset:32768\n" \
"s_add_i32 s18, s18, s19\n" \
"v_mfma_f32_16x16x32_bf16 a[24:27], a[212:215], a[232:235], a[24:27]\n" \
"ds_read_b64_tr_b16 a[182:183], v37 offset:33792\n" \
"s_or_b32 s15, s15, s5\n" \
"v_mfma_f32_16x16x32_bf16 a[28:31], a[172:175], a[148:151], a[28:31]\n" \
"ds_read_b64_tr_b16 a[244:245], v37 offset:40960\n" \
"v_writelane_b32 v11, s18, 27\n" \
"v_mfma_f32_16x16x32_bf16 a[28:31], a[236:239], a[212:215], a[28:31]\n" \
"s_add_u32 s89, s42, 0x0\n" \
"buffer_store_dwordx4 v[216:219], v20, s[80:83], s89 offen offset:1024 sc1\n" \
"v_mfma_f32_16x16x32_bf16 a[20:23], a[164:167], a[148:151], a[20:23]\n" \
"ds_read_b64_tr_b16 a[246:247], v37 offset:41984\n" \
"s_mul_i32 s18, s15, 0x24000\n" \
"v_mfma_f32_16x16x32_bf16 a[20:23], a[228:231], a[212:215], a[20:23]\n" \
"ds_read_b64_tr_b16 a[184:185], v38 offset:32768\n" \
"s_add_i32 s18, s18, s19\n" \
"v_mfma_f32_16x16x32_bf16 a[16:19], a[160:163], a[148:151], a[16:19]\n" \
"ds_read_b64_tr_b16 a[186:187], v38 offset:33792\n" \
"v_writelane_b32 v11, s18, 28\n" \
"v_mfma_f32_16x16x32_bf16 a[16:19], a[224:227], a[212:215], a[16:19]\n" \
"s_or_b32 s18, s9, s14\n" \
"s_mul_i32 s18, s18, 0x24800\n" \
"v_mfma_f32_16x16x32_bf16 a[32:35], a[160:163], a[152:155], a[32:35]\n" \
"s_add_u32 s88, s42, 0x0\n" \
"buffer_store_dwordx4 v[220:223], v20, s[80:83], s88 offen offset:0 sc1\n" \
"v_mfma_f32_16x16x32_bf16 a[32:35], a[224:227], a[216:219], a[32:35]\n" \
"ds_read_b64_tr_b16 a[248:249], v38 offset:40960\n" \
"s_lshl_b32 s16, s16, 12\n" \
"v_mfma_f32_16x16x32_bf16 a[36:39], a[164:167], a[152:155], a[36:39]\n" \
"ds_read_b64_tr_b16 a[250:251], v38 offset:41984\n" \
"s_add_i32 s19, s18, 0x3004800\n" \
"v_mfma_f32_16x16x32_bf16 a[36:39], a[228:231], a[216:219], a[36:39]\n" \
"ds_read_b64_tr_b16 a[188:189], v39 offset:32768\n" \
"s_add_i32 s20, s19, s16\n" \
"v_mfma_f32_16x16x32_bf16 a[44:47], a[172:175], a[152:155], a[44:47]\n" \
"v_writelane_b32 v11, s20, 29\n" \
"s_lshl_b32 s13, s13, 12\n" \
"v_mfma_f32_16x16x32_bf16 a[44:47], a[236:239], a[216:219], a[44:47]\n" \
"ds_read_b64_tr_b16 a[190:191], v39 offset:33792\n" \
"s_lshl_b32 s15, s15, 12\n" \
"v_mfma_f32_16x16x32_bf16 a[40:43], a[152:155], a[168:171], a[40:43]\n" \
"s_add_u32 s89, s42, 0x1000\n" \
"buffer_store_dwordx4 v[224:227], v20, s[80:83], s89 offen offset:3072 sc1\n" \
"v_mfma_f32_16x16x32_bf16 a[40:43], a[216:219], a[232:235], a[40:43]\n" \
"ds_read_b64_tr_b16 a[252:253], v39 offset:40960\n" \
"s_sub_i32 s14, s9, s14\n" \
"v_mfma_f32_16x16x32_bf16 a[56:59], a[156:159], a[168:171], a[56:59]\n" \
"ds_read_b64_tr_b16 a[254:255], v39 offset:41984\n" \
"s_add_i32 s20, s19, s13\n" \
"v_mfma_f32_16x16x32_bf16 a[56:59], a[220:223], a[232:235], a[56:59]\n" \
"v_writelane_b32 v11, s20, 30\n" \
"s_add_i32 s19, s19, s15\n" \
"v_mfma_f32_16x16x32_bf16 a[60:63], a[172:175], a[156:159], a[60:63]\n" \
"s_mul_i32 s14, s14, 0x24800\n" \
"v_writelane_b32 v11, s19, 31\n" \
"v_mfma_f32_16x16x32_bf16 a[60:63], a[236:239], a[220:223], a[60:63]\n" \
"s_add_u32 s88, s42, 0x1000\n" \
"buffer_store_dwordx4 v[228:231], v20, s[80:83], s88 offen offset:2048 sc1\n" \
"v_mfma_f32_16x16x32_bf16 a[52:55], a[164:167], a[156:159], a[52:55]\n" \
"s_add_i32 s19, s14, 0x4120800\n" \
"s_add_i32 s16, s19, s16\n" \
"v_mfma_f32_16x16x32_bf16 a[52:55], a[228:231], a[220:223], a[52:55]\n" \
"v_writelane_b32 v11, s16, 32\n" \
"s_add_i32 s13, s19, s13\n" \
"v_mfma_f32_16x16x32_bf16 a[48:51], a[160:163], a[156:159], a[48:51]\n" \
"v_writelane_b32 v11, s13, 33\n" \
"s_add_i32 s19, s19, s15\n" \
"v_mfma_f32_16x16x32_bf16 a[48:51], a[224:227], a[220:223], a[48:51]\n" \
"s_or_b32 s13, s18, 0x3000000\n" \
"s_lshl_b32 s15, s6, 11\n" \
"v_mfma_f32_16x16x32_bf16 a[64:67], a[164:167], a[140:143], a[64:67]\n" \
"v_writelane_b32 v11, s19, 34\n" \
"s_add_i32 s16, s13, s15\n" \
"v_mfma_f32_16x16x32_bf16 a[64:67], a[228:231], a[204:207], a[64:67]\n" \
"v_writelane_b32 v11, s16, 35\n" \
"s_add_i32 s16, s14, 0x411c000\n" \
"v_mfma_f32_16x16x32_bf16 a[80:83], a[172:175], a[140:143], a[80:83]\n" \
"s_add_i32 s19, s16, s15\n" \
"v_writelane_b32 v11, s19, 36\n" \
"v_mfma_f32_16x16x32_bf16 a[80:83], a[236:239], a[204:207], a[80:83]\n" \
"s_add_i32 s18, s18, 0x3004000\n" \
"v_writelane_b32 v11, s18, 37\n" \
"v_mfma_f32_16x16x32_bf16 a[68:71], a[140:143], a[168:171], a[68:71]\n" \
"s_add_i32 s14, s14, 0x4120000\n" \
"v_writelane_b32 v11, s14, 38\n" \
"v_mfma_f32_16x16x32_bf16 a[68:71], a[204:207], a[232:235], a[68:71]\n" \
"s_waitcnt vmcnt(4) lgkmcnt(0)\n" \
"s_barrier\n" \
"v_mov_b32_e32 v45, s62\n" \
"s_mov_b64 exec, 1\n" \
"buffer_store_dword v45, off, s[80:83], s37 offset:0 sc1\n" \
"s_mov_b64 exec, -1\n" \
"v_mfma_f32_16x16x32_bf16 v[116:119], a[176:179], a[144:147], v[116:119]\n" \
"s_add_u32 m0, s40, 0x8000\n" \
"s_add_u32 s85, s39, 0xa000\n" \
"v_mfma_f32_16x16x32_bf16 v[116:119], a[240:243], a[208:211], v[116:119]\n" \
"buffer_load_dwordx4 v20, s[80:83], s85 offen sc1 lds\n" \
"v_mfma_f32_16x16x32_bf16 v[120:123], a[180:183], a[144:147], v[120:123]\n" \
"ds_read_b64_tr_b16 a[160:161], v32 offset:0\n" \
"v_accvgpr_read_b32 v212, a108\n" \
"v_mfma_f32_16x16x32_bf16 v[120:123], a[244:247], a[208:211], v[120:123]\n" \
"ds_read_b64_tr_b16 a[162:163], v32 offset:512\n" \
"v_accvgpr_read_b32 v213, a109\n" \
"v_mfma_f32_16x16x32_bf16 v[124:127], a[184:187], a[144:147], v[124:127]\n" \
"s_add_u32 m0, s40, 0x9000\n" \
"s_add_u32 s85, s39, 0xb000\n" \
"v_mfma_f32_16x16x32_bf16 v[124:127], a[248:251], a[208:211], v[124:127]\n" \
"buffer_load_dwordx4 v20, s[80:83], s85 offen sc1 lds\n" \
"v_mfma_f32_16x16x32_bf16 v[128:131], a[188:191], a[144:147], v[128:131]\n" \
"ds_read_b64_tr_b16 a[224:225], v32 offset:4096\n" \
"v_accvgpr_read_b32 v214, a110\n" \
"v_mfma_f32_16x16x32_bf16 v[128:131], a[252:255], a[208:211], v[128:131]\n" \
"ds_read_b128 a[144:147], v28 offset:8192\n" \
"v_accvgpr_read_b32 v215, a111\n" \
"v_mfma_f32_16x16x32_bf16 v[144:147], a[188:191], a[148:151], v[144:147]\n" \
"ds_read_b128 a[208:211], v29 offset:8192\n" \
"v_accvgpr_read_b32 v180, a124\n" \
"v_mfma_f32_16x16x32_bf16 v[144:147], a[252:255], a[212:215], v[144:147]\n" \
"s_add_u32 m0, s40, 0xb000\n" \
"s_movk_i32 s84, 0x280\n" \
"v_mfma_f32_16x16x32_bf16 v[140:143], a[184:187], a[148:151], v[140:143]\n" \
"buffer_load_dwordx4 v21, s[76:79], s84 offen offset:0 lds\n" \
"v_mfma_f32_16x16x32_bf16 v[140:143], a[248:251], a[212:215], v[140:143]\n" \
"ds_read_b64_tr_b16 a[226:227], v32 offset:4608\n" \
"v_accvgpr_read_b32 v181, a125\n" \
"v_mfma_f32_16x16x32_bf16 v[136:139], a[180:183], a[148:151], v[136:139]\n" \
"s_add_u32 m0, s30, 0x8000\n" \
"s_movk_i32 s84, 0x280\n" \
"v_mfma_f32_16x16x32_bf16 v[136:139], a[244:247], a[212:215], v[136:139]\n" \
"buffer_load_dwordx4 v24, s[76:79], s84 offen offset:0 lds\n" \
"v_mfma_f32_16x16x32_bf16 v[132:135], a[176:179], a[148:151], v[132:135]\n" \
"ds_read_b64_tr_b16 a[164:165], v33 offset:0\n" \
"v_accvgpr_read_b32 v182, a126\n" \
"v_mfma_f32_16x16x32_bf16 v[132:135], a[240:243], a[212:215], v[132:135]\n" \
"ds_read_b128 a[148:151], v28 offset:10240\n" \
"v_accvgpr_read_b32 v183, a127\n" \
"v_mfma_f32_16x16x32_bf16 v[148:151], a[176:179], a[152:155], v[148:151]\n" \
"ds_read_b128 a[212:215], v29 offset:10240\n" \
"v_cvt_pk_bf16_f32 v212, v212, v213\n" \
"v_mfma_f32_16x16x32_bf16 v[148:151], a[240:243], a[216:219], v[148:151]\n" \
"s_movk_i32 s84, 0x2280\n" \
"v_mfma_f32_16x16x32_bf16 v[152:155], a[180:183], a[152:155], v[152:155]\n" \
"buffer_load_dwordx4 v25, s[76:79], s84 offen offset:1024 lds\n" \
"v_mfma_f32_16x16x32_bf16 v[152:155], a[244:247], a[216:219], v[152:155]\n" \
"ds_read_b64_tr_b16 a[166:167], v33 offset:512\n" \
"v_cvt_pk_bf16_f32 v213, v214, v215\n" \
"v_mfma_f32_16x16x32_bf16 v[156:159], a[184:187], a[152:155], v[156:159]\n" \
"ds_read_b64_tr_b16 a[228:229], v33 offset:4096\n" \
"v_cvt_pk_bf16_f32 v214, v180, v181\n" \
"v_mfma_f32_16x16x32_bf16 v[156:159], a[248:251], a[216:219], v[156:159]\n" \
"s_movk_i32 s84, 0x4280\n" \
"v_mfma_f32_16x16x32_bf16 v[160:163], a[188:191], a[152:155], v[160:163]\n" \
"buffer_load_dwordx4 v24, s[76:79], s84 offen offset:2048 lds\n" \
"v_mfma_f32_16x16x32_bf16 v[160:163], a[252:255], a[216:219], v[160:163]\n" \
"ds_read_b128 a[152:155], v28 offset:12288\n" \
"v_cvt_pk_bf16_f32 v215, v182, v183\n" \
"v_mfma_f32_16x16x32_bf16 v[176:179], a[188:191], a[156:159], v[176:179]\n" \
"ds_read_b128 a[216:219], v29 offset:12288\n" \
"v_accvgpr_read_b32 v208, a104\n" \
"v_mfma_f32_16x16x32_bf16 v[176:179], a[252:255], a[220:223], v[176:179]\n" \
"s_movk_i32 s84, 0x6280\n" \
"s_nop 0\n" \
"v_mfma_f32_16x16x32_bf16 v[172:175], a[184:187], a[156:159], v[172:175]\n" \
"buffer_load_dwordx4 v25, s[76:79], s84 offen offset:3072 lds\n" \
"v_mfma_f32_16x16x32_bf16 v[172:175], a[248:251], a[220:223], v[172:175]\n" \
"ds_read_b64_tr_b16 a[230:231], v33 offset:4608\n" \
"v_accvgpr_read_b32 v209, a105\n" \
"v_mfma_f32_16x16x32_bf16 v[168:171], a[180:183], a[156:159], v[168:171]\n" \
"ds_read_b64_tr_b16 a[172:173], v35 offset:0\n" \
"v_accvgpr_read_b32 v210, a106\n" \
"v_mfma_f32_16x16x32_bf16 v[168:171], a[244:247], a[220:223], v[168:171]\n" \
"ds_read_b64_tr_b16 a[174:175], v35 offset:512\n" \
"v_accvgpr_read_b32 v211, a107\n" \
"v_mfma_f32_16x16x32_bf16 v[164:167], a[176:179], a[156:159], v[164:167]\n" \
"v_accvgpr_read_b32 v180, a120\n" \
"v_accvgpr_read_b32 v181, a121\n" \
"v_mfma_f32_16x16x32_bf16 v[164:167], a[240:243], a[220:223], v[164:167]\n" \
"ds_read_b128 a[156:159], v28 offset:14336\n" \
"v_accvgpr_read_b32 v182, a122\n" \
"v_mfma_f32_16x16x32_bf16 v[84:87], a[176:179], a[136:139], v[84:87]\n" \
"ds_read_b128 a[220:223], v29 offset:14336\n" \
"v_accvgpr_read_b32 v183, a123\n" \
"v_mfma_f32_16x16x32_bf16 v[84:87], a[240:243], a[200:203], v[84:87]\n" \
"s_add_u32 m0, s31, 0x8000\n" \
"s_mov_b32 s84, 0x280000\n" \
"v_mfma_f32_16x16x32_bf16 v[88:91], a[180:183], a[136:139], v[88:91]\n" \
"buffer_load_dwordx4 v26, s[68:71], s84 offen offset:0 lds\n" \
"v_mfma_f32_16x16x32_bf16 v[88:91], a[244:247], a[200:203], v[88:91]\n" \
"s_addk_i32 s84, 0x7c00\n" \
"s_nop 0\n" \
"v_mfma_f32_16x16x32_bf16 v[92:95], a[184:187], a[136:139], v[92:95]\n" \
"buffer_load_dwordx4 v26, s[68:71], s84 offen offset:1024 lds\n" \
"v_mfma_f32_16x16x32_bf16 v[92:95], a[248:251], a[200:203], v[92:95]\n" \
"ds_read_b64_tr_b16 a[236:237], v35 offset:4096\n" \
"v_cvt_pk_bf16_f32 v208, v208, v209\n" \
"v_mfma_f32_16x16x32_bf16 v[96:99], a[188:191], a[136:139], v[96:99]\n" \
"ds_read_b64_tr_b16 a[238:239], v35 offset:4608\n" \
"v_cvt_pk_bf16_f32 v209, v210, v211\n" \
"v_mfma_f32_16x16x32_bf16 v[96:99], a[252:255], a[200:203], v[96:99]\n" \
"ds_read_b128 a[136:139], v28 offset:4096\n" \
"v_cvt_pk_bf16_f32 v210, v180, v181\n" \
"v_mfma_f32_16x16x32_bf16 v[112:115], a[188:191], a[140:143], v[112:115]\n" \
"ds_read_b128 a[200:203], v29 offset:4096\n" \
"v_cvt_pk_bf16_f32 v211, v182, v183\n" \
"v_mfma_f32_16x16x32_bf16 v[112:115], a[252:255], a[204:207], v[112:115]\n" \
"s_addk_i32 s84, 0x7c00\n" \
"v_mfma_f32_16x16x32_bf16 v[108:111], a[184:187], a[140:143], v[108:111]\n" \
"buffer_load_dwordx4 v27, s[68:71], s84 offen offset:2048 lds\n" \
"v_mfma_f32_16x16x32_bf16 v[108:111], a[248:251], a[204:207], v[108:111]\n" \
"s_addk_i32 s84, 0x7c00\n" \
"v_mfma_f32_16x16x32_bf16 v[104:107], a[180:183], a[140:143], v[104:107]\n" \
"buffer_load_dwordx4 v27, s[68:71], s84 offen offset:3072 lds\n" \
"v_mfma_f32_16x16x32_bf16 v[104:107], a[244:247], a[204:207], v[104:107]\n" \
"ds_read_b64_tr_b16 a[168:169], v34 offset:0\n" \
"v_accvgpr_read_b32 v204, a100\n" \
"v_mfma_f32_16x16x32_bf16 v[100:103], a[176:179], a[140:143], v[100:103]\n" \
"ds_read_b64_tr_b16 a[170:171], v34 offset:512\n" \
"v_accvgpr_read_b32 v205, a101\n" \
"v_mfma_f32_16x16x32_bf16 v[100:103], a[240:243], a[204:207], v[100:103]\n" \
"ds_read_b128 a[140:143], v28 offset:6144\n" \
"v_accvgpr_read_b32 v206, a102\n" \
"v_mfma_f32_16x16x32_bf16 v[52:55], a[176:179], a[128:131], v[52:55]\n" \
"ds_read_b128 a[204:207], v29 offset:6144\n" \
"v_accvgpr_read_b32 v207, a103\n" \
"v_mfma_f32_16x16x32_bf16 v[52:55], a[240:243], a[192:195], v[52:55]\n" \
"s_add_u32 m0, s30, 0x9000\n" \
"s_mov_b32 s84, 0x9280\n" \
"v_mfma_f32_16x16x32_bf16 v[56:59], a[180:183], a[128:131], v[56:59]\n" \
"buffer_load_dwordx4 v24, s[76:79], s84 offen offset:0 lds\n" \
"v_mfma_f32_16x16x32_bf16 v[56:59], a[244:247], a[192:195], v[56:59]\n" \
"s_addk_i32 s84, 0x2000\n" \
"s_nop 0\n" \
"v_mfma_f32_16x16x32_bf16 v[60:63], a[184:187], a[128:131], v[60:63]\n" \
"buffer_load_dwordx4 v25, s[76:79], s84 offen offset:1024 lds\n" \
"v_mfma_f32_16x16x32_bf16 v[60:63], a[248:251], a[192:195], v[60:63]\n" \
"ds_read_b64_tr_b16 a[232:233], v34 offset:4096\n" \
"v_accvgpr_read_b32 v180, a116\n" \
"v_mfma_f32_16x16x32_bf16 v[64:67], a[188:191], a[128:131], v[64:67]\n" \
"ds_read_b64_tr_b16 a[234:235], v34 offset:4608\n" \
"v_accvgpr_read_b32 v181, a117\n" \
"v_mfma_f32_16x16x32_bf16 v[64:67], a[252:255], a[192:195], v[64:67]\n" \
"ds_read_b128 a[128:131], v28 offset:0\n" \
"v_accvgpr_read_b32 v182, a118\n" \
"v_mfma_f32_16x16x32_bf16 v[80:83], a[188:191], a[132:135], v[80:83]\n" \
"ds_read_b128 a[192:195], v29 offset:0\n" \
"v_accvgpr_read_b32 v183, a119\n" \
"v_mfma_f32_16x16x32_bf16 v[80:83], a[252:255], a[196:199], v[80:83]\n" \
"v_cvt_pk_bf16_f32 v204, v204, v205\n" \
"v_cvt_pk_bf16_f32 v205, v206, v207\n" \
"v_mfma_f32_16x16x32_bf16 v[76:79], a[184:187], a[132:135], v[76:79]\n" \
"v_cvt_pk_bf16_f32 v206, v180, v181\n" \
"v_cvt_pk_bf16_f32 v207, v182, v183\n" \
"v_mfma_f32_16x16x32_bf16 v[76:79], a[248:251], a[196:199], v[76:79]\n" \
"v_accvgpr_read_b32 v200, a96\n" \
"v_accvgpr_read_b32 v201, a97\n" \
"v_mfma_f32_16x16x32_bf16 v[72:75], a[180:183], a[132:135], v[72:75]\n" \
"v_accvgpr_read_b32 v202, a98\n" \
"v_accvgpr_read_b32 v203, a99\n" \
"v_mfma_f32_16x16x32_bf16 v[72:75], a[244:247], a[196:199], v[72:75]\n" \
"v_accvgpr_read_b32 v180, a112\n" \
"v_accvgpr_read_b32 v181, a113\n" \
"v_mfma_f32_16x16x32_bf16 v[68:71], a[176:179], a[132:135], v[68:71]\n" \
"v_accvgpr_read_b32 v182, a114\n" \
"v_accvgpr_read_b32 v183, a115\n" \
"v_mfma_f32_16x16x32_bf16 v[68:71], a[240:243], a[196:199], v[68:71]\n" \
"ds_read_b128 a[132:135], v28 offset:2048\n" \
"ds_read_b128 a[196:199], v29 offset:2048\n" \
"s_waitcnt lgkmcnt(15)\n" \
"v_mfma_f32_16x16x32_bf16 a[0:3], a[160:163], a[144:147], a[0:3]\n" \
"s_addk_i32 s84, 0x2000\n" \
"v_mfma_f32_16x16x32_bf16 a[0:3], a[224:227], a[208:211], a[0:3]\n" \
"buffer_load_dwordx4 v24, s[76:79], s84 offen offset:2048 lds\n" \
"v_mfma_f32_16x16x32_bf16 a[4:7], a[164:167], a[144:147], a[4:7]\n" \
"ds_read_b64_tr_b16 a[176:177], v36 offset:0\n" \
"v_cvt_pk_bf16_f32 v200, v200, v201\n" \
"v_mfma_f32_16x16x32_bf16 a[4:7], a[228:231], a[208:211], a[4:7]\n" \
"s_addk_i32 s84, 0x2000\n" \
"v_mfma_f32_16x16x32_bf16 a[12:15], a[172:175], a[144:147], a[12:15]\n" \
"buffer_load_dwordx4 v25, s[76:79], s84 offen offset:3072 lds\n" \
"s_waitcnt lgkmcnt(13)\n" \
"v_mfma_f32_16x16x32_bf16 a[12:15], a[236:239], a[208:211], a[12:15]\n" \
"ds_read_b64_tr_b16 a[178:179], v36 offset:1024\n" \
"v_cvt_pk_bf16_f32 v201, v202, v203\n" \
"s_waitcnt lgkmcnt(10)\n" \
"v_mfma_f32_16x16x32_bf16 a[8:11], a[144:147], a[168:171], a[8:11]\n" \
"ds_read_b64_tr_b16 a[240:241], v36 offset:8192\n" \
"v_cvt_pk_bf16_f32 v202, v180, v181\n" \
"s_waitcnt lgkmcnt(7)\n" \
"v_mfma_f32_16x16x32_bf16 a[8:11], a[208:211], a[232:235], a[8:11]\n" \
"ds_read_b64_tr_b16 a[242:243], v36 offset:9216\n" \
"v_cvt_pk_bf16_f32 v203, v182, v183\n" \
"v_mfma_f32_16x16x32_bf16 a[24:27], a[148:151], a[168:171], a[24:27]\n" \
"ds_read_b64_tr_b16 a[180:181], v37 offset:0\n" \
"v_accvgpr_read_b32 v196, a76\n" \
"v_mfma_f32_16x16x32_bf16 a[24:27], a[212:215], a[232:235], a[24:27]\n" \
"ds_read_b64_tr_b16 a[182:183], v37 offset:1024\n" \
"v_accvgpr_read_b32 v197, a77\n" \
"v_mfma_f32_16x16x32_bf16 a[28:31], a[172:175], a[148:151], a[28:31]\n" \
"ds_read_b64_tr_b16 a[244:245], v37 offset:8192\n" \
"v_accvgpr_read_b32 v198, a78\n" \
"v_mfma_f32_16x16x32_bf16 a[28:31], a[236:239], a[212:215], a[28:31]\n" \
"ds_read_b64_tr_b16 a[246:247], v37 offset:9216\n" \
"v_accvgpr_read_b32 v199, a79\n" \
"v_mfma_f32_16x16x32_bf16 a[20:23], a[164:167], a[148:151], a[20:23]\n" \
"v_accvgpr_read_b32 v180, a92\n" \
"v_accvgpr_read_b32 v181, a93\n" \
"v_mfma_f32_16x16x32_bf16 a[20:23], a[228:231], a[212:215], a[20:23]\n" \
"s_add_u32 s89, s42, 0x1000\n" \
"buffer_store_dwordx4 v[232:235], v20, s[80:83], s89 offen offset:1024 sc1\n" \
"v_mfma_f32_16x16x32_bf16 a[16:19], a[160:163], a[148:151], a[16:19]\n" \
"ds_read_b64_tr_b16 a[184:185], v38 offset:0\n" \
"v_accvgpr_read_b32 v182, a94\n" \
"v_mfma_f32_16x16x32_bf16 a[16:19], a[224:227], a[212:215], a[16:19]\n" \
"ds_read_b64_tr_b16 a[186:187], v38 offset:1024\n" \
"v_accvgpr_read_b32 v183, a95\n" \
"v_mfma_f32_16x16x32_bf16 a[32:35], a[160:163], a[152:155], a[32:35]\n" \
"ds_read_b64_tr_b16 a[248:249], v38 offset:8192\n" \
"v_cvt_pk_bf16_f32 v196, v196, v197\n" \
"v_mfma_f32_16x16x32_bf16 a[32:35], a[224:227], a[216:219], a[32:35]\n" \
"ds_read_b64_tr_b16 a[250:251], v38 offset:9216\n" \
"v_cvt_pk_bf16_f32 v197, v198, v199\n" \
"v_mfma_f32_16x16x32_bf16 a[36:39], a[164:167], a[152:155], a[36:39]\n" \
"v_cvt_pk_bf16_f32 v198, v180, v181\n" \
"v_cvt_pk_bf16_f32 v199, v182, v183\n" \
"v_mfma_f32_16x16x32_bf16 a[36:39], a[228:231], a[216:219], a[36:39]\n" \
"s_add_u32 s88, s42, 0x1000\n" \
"buffer_store_dwordx4 v[236:239], v20, s[80:83], s88 offen offset:0 sc1\n" \
"v_mfma_f32_16x16x32_bf16 a[44:47], a[172:175], a[152:155], a[44:47]\n" \
"ds_read_b64_tr_b16 a[188:189], v39 offset:0\n" \
"v_accvgpr_read_b32 v192, a72\n" \
"v_mfma_f32_16x16x32_bf16 a[44:47], a[236:239], a[216:219], a[44:47]\n" \
"ds_read_b64_tr_b16 a[190:191], v39 offset:1024\n" \
"v_accvgpr_read_b32 v193, a73\n" \
"v_mfma_f32_16x16x32_bf16 a[40:43], a[152:155], a[168:171], a[40:43]\n" \
"ds_read_b64_tr_b16 a[252:253], v39 offset:8192\n" \
"v_accvgpr_read_b32 v194, a74\n" \
"v_mfma_f32_16x16x32_bf16 a[40:43], a[216:219], a[232:235], a[40:43]\n" \
"v_accvgpr_read_b32 v195, a75\n" \
"v_accvgpr_read_b32 v180, a88\n" \
"v_mfma_f32_16x16x32_bf16 a[56:59], a[156:159], a[168:171], a[56:59]\n" \
"ds_read_b64_tr_b16 a[254:255], v39 offset:9216\n" \
"v_accvgpr_read_b32 v181, a89\n" \
"v_mfma_f32_16x16x32_bf16 a[56:59], a[220:223], a[232:235], a[56:59]\n" \
"v_accvgpr_read_b32 v182, a90\n" \
"v_accvgpr_read_b32 v183, a91\n" \
"v_mfma_f32_16x16x32_bf16 a[60:63], a[172:175], a[156:159], a[60:63]\n" \
"s_add_u32 s89, s42, 0x3000\n" \
"buffer_store_dwordx4 v[240:243], v20, s[80:83], s89 offen offset:3072 sc1\n" \
"v_mfma_f32_16x16x32_bf16 a[60:63], a[236:239], a[220:223], a[60:63]\n" \
"v_cvt_pk_bf16_f32 v192, v192, v193\n" \
"v_cvt_pk_bf16_f32 v193, v194, v195\n" \
"v_mfma_f32_16x16x32_bf16 a[52:55], a[164:167], a[156:159], a[52:55]\n" \
"v_cvt_pk_bf16_f32 v194, v180, v181\n" \
"v_cvt_pk_bf16_f32 v195, v182, v183\n" \
"v_mfma_f32_16x16x32_bf16 a[52:55], a[228:231], a[220:223], a[52:55]\n" \
"s_lshl_b32 s14, s4, 2\n" \
"s_or_b32 s8, s14, s8\n" \
"v_mfma_f32_16x16x32_bf16 a[48:51], a[160:163], a[156:159], a[48:51]\n" \
"s_mul_i32 s8, s8, 0x90000\n" \
"s_lshl_b32 s14, s3, 14\n" \
"v_mfma_f32_16x16x32_bf16 a[48:51], a[224:227], a[220:223], a[48:51]\n" \
"s_add_i32 s8, s8, s14\n" \
"s_lshl_b32 s9, s9, 4\n" \
"v_mfma_f32_16x16x32_bf16 a[64:67], a[164:167], a[140:143], a[64:67]\n" \
"s_lshl_b32 s14, s4, 7\n" \
"s_or_b32 s18, s9, s14\n" \
"v_mfma_f32_16x16x32_bf16 a[64:67], a[228:231], a[204:207], a[64:67]\n" \
"s_sub_i32 s9, s9, s14\n" \
"s_mulk_i32 s18, 0x480\n" \
"v_mfma_f32_16x16x32_bf16 a[80:83], a[172:175], a[140:143], a[80:83]\n" \
"s_mulk_i32 s9, 0x480\n" \
"s_or_b32 s14, s18, s12\n" \
"v_mfma_f32_16x16x32_bf16 a[80:83], a[236:239], a[204:207], a[80:83]\n" \
"s_or_b32 s9, s9, s12\n" \
"s_and_b32 s12, s2, 1\n" \
"v_mfma_f32_16x16x32_bf16 a[68:71], a[140:143], a[168:171], a[68:71]\n" \
"s_cmp_lg_u32 s12, 0\n" \
"s_cselect_b32 s13, s16, s13\n" \
"v_mfma_f32_16x16x32_bf16 a[68:71], a[204:207], a[232:235], a[68:71]\n" \
"s_waitcnt vmcnt(3) lgkmcnt(0)\n" \
"s_barrier\n" \
"v_mfma_f32_16x16x32_bf16 v[116:119], a[176:179], a[144:147], v[116:119]\n" \
"s_add_u32 m0, s40, 0x0\n" \
"s_add_u32 s85, s39, 0xc000\n" \
"v_mfma_f32_16x16x32_bf16 v[116:119], a[240:243], a[208:211], v[116:119]\n" \
"buffer_load_dwordx4 v20, s[80:83], s85 offen sc1 lds\n" \
"v_mfma_f32_16x16x32_bf16 v[120:123], a[180:183], a[144:147], v[120:123]\n" \
"ds_read_b64_tr_b16 a[160:161], v32 offset:32768\n" \
"s_bfe_u32 s16, s2, 0x20001\n" \
"v_mfma_f32_16x16x32_bf16 v[120:123], a[244:247], a[208:211], v[120:123]\n" \
"ds_read_b64_tr_b16 a[162:163], v32 offset:33280\n" \
"s_addk_i32 s13, 0x4000\n" \
"v_mfma_f32_16x16x32_bf16 v[124:127], a[184:187], a[144:147], v[124:127]\n" \
"s_add_u32 m0, s40, 0x1000\n" \
"s_add_u32 s85, s39, 0xd000\n" \
"v_mfma_f32_16x16x32_bf16 v[124:127], a[248:251], a[208:211], v[124:127]\n" \
"buffer_load_dwordx4 v20, s[80:83], s85 offen sc1 lds\n" \
"v_mfma_f32_16x16x32_bf16 v[128:131], a[188:191], a[144:147], v[128:131]\n" \
"ds_read_b64_tr_b16 a[224:225], v32 offset:36864\n" \
"s_xor_b32 s16, s16, s3\n" \
"v_mfma_f32_16x16x32_bf16 v[128:131], a[252:255], a[208:211], v[128:131]\n" \
"ds_read_b128 a[144:147], v28 offset:40960\n" \
"v_mfma_f32_16x16x32_bf16 v[144:147], a[188:191], a[148:151], v[144:147]\n" \
"ds_read_b128 a[208:211], v29 offset:40960\n" \
".fill (L_pb + 69344 - .) / 4, 4, 0xBF800000\n" \
"L_pp17:\n" \
"s_cbranch_execz L_pret\n" \
"v_mfma_f32_16x16x32_bf16 v[144:147], a[252:255], a[212:215], v[144:147]\n" \
"s_add_u32 m0, s40, 0x3000\n" \
"s_movk_i32 s84, 0x300\n" \
"v_mfma_f32_16x16x32_bf16 v[140:143], a[184:187], a[148:151], v[140:143]\n" \
"buffer_load_dwordx4 v21, s[76:79], s84 offen offset:0 lds\n" \
"v_mfma_f32_16x16x32_bf16 v[140:143], a[248:251], a[212:215], v[140:143]\n" \
"ds_read_b64_tr_b16 a[226:227], v32 offset:37376\n" \
"v_mfma_f32_16x16x32_bf16 v[136:139], a[180:183], a[148:151], v[136:139]\n" \
"s_add_u32 m0, s30, 0x0\n" \
"s_movk_i32 s84, 0x300\n" \
"v_mfma_f32_16x16x32_bf16 v[136:139], a[244:247], a[212:215], v[136:139]\n" \
"buffer_load_dwordx4 v24, s[76:79], s84 offen offset:0 lds\n" \
"v_mfma_f32_16x16x32_bf16 v[132:135], a[176:179], a[148:151], v[132:135]\n" \
"ds_read_b64_tr_b16 a[164:165], v33 offset:32768\n" \
"v_mfma_f32_16x16x32_bf16 v[132:135], a[240:243], a[212:215], v[132:135]\n" \
"ds_read_b128 a[148:151], v28 offset:43008\n" \
"v_mfma_f32_16x16x32_bf16 v[148:151], a[176:179], a[152:155], v[148:151]\n" \
"ds_read_b128 a[212:215], v29 offset:43008\n" \
"v_mfma_f32_16x16x32_bf16 v[148:151], a[240:243], a[216:219], v[148:151]\n" \
"s_movk_i32 s84, 0x2300\n" \
"v_mfma_f32_16x16x32_bf16 v[152:155], a[180:183], a[152:155], v[152:155]\n" \
"buffer_load_dwordx4 v25, s[76:79], s84 offen offset:1024 lds\n" \
"v_mfma_f32_16x16x32_bf16 v[152:155], a[244:247], a[216:219], v[152:155]\n" \
"ds_read_b64_tr_b16 a[166:167], v33 offset:33280\n" \
"v_mfma_f32_16x16x32_bf16 v[156:159], a[184:187], a[152:155], v[156:159]\n" \
"ds_read_b64_tr_b16 a[228:229], v33 offset:36864\n" \
"v_mfma_f32_16x16x32_bf16 v[156:159], a[248:251], a[216:219], v[156:159]\n" \
"s_movk_i32 s84, 0x4300\n" \
"v_mfma_f32_16x16x32_bf16 v[160:163], a[188:191], a[152:155], v[160:163]\n" \
"buffer_load_dwordx4 v24, s[76:79], s84 offen offset:2048 lds\n" \
"v_mfma_f32_16x16x32_bf16 v[160:163], a[252:255], a[216:219], v[160:163]\n" \
"ds_read_b128 a[152:155], v28 offset:45056\n" \
"v_mfma_f32_16x16x32_bf16 v[176:179], a[188:191], a[156:159], v[176:179]\n" \
"ds_read_b128 a[216:219], v29 offset:45056\n" \
"v_mfma_f32_16x16x32_bf16 v[176:179], a[252:255], a[220:223], v[176:179]\n" \
"s_movk_i32 s84, 0x6300\n" \
"v_mfma_f32_16x16x32_bf16 v[172:175], a[184:187], a[156:159], v[172:175]\n" \
"buffer_load_dwordx4 v25, s[76:79], s84 offen offset:3072 lds\n" \
"v_mfma_f32_16x16x32_bf16 v[172:175], a[248:251], a[220:223], v[172:175]\n" \
"ds_read_b64_tr_b16 a[230:231], v33 offset:37376\n" \
"v_mfma_f32_16x16x32_bf16 v[168:171], a[180:183], a[156:159], v[168:171]\n" \
"ds_read_b64_tr_b16 a[172:173], v35 offset:32768\n" \
"v_mfma_f32_16x16x32_bf16 v[168:171], a[244:247], a[220:223], v[168:171]\n" \
"ds_read_b64_tr_b16 a[174:175], v35 offset:33280\n" \
"v_mfma_f32_16x16x32_bf16 v[164:167], a[176:179], a[156:159], v[164:167]\n" \
"s_cmp_lg_u32 s16, 0\n" \
"s_cselect_b32 s13, s7, s13\n" \
"v_mfma_f32_16x16x32_bf16 v[164:167], a[240:243], a[220:223], v[164:167]\n" \
"ds_read_b128 a[156:159], v28 offset:47104\n" \
"s_lshl_b32 s6, s6, 16\n" \
"v_mfma_f32_16x16x32_bf16 v[84:87], a[176:179], a[136:139], v[84:87]\n" \
"ds_read_b128 a[220:223], v29 offset:47104\n" \
"v_writelane_b32 v11, s13, 39\n" \
"v_mfma_f32_16x16x32_bf16 v[84:87], a[240:243], a[200:203], v[84:87]\n" \
"s_add_u32 m0, s31, 0x0\n" \
"s_mov_b32 s84, 0x300000\n" \
"v_mfma_f32_16x16x32_bf16 v[88:91], a[180:183], a[136:139], v[88:91]\n" \
"buffer_load_dwordx4 v26, s[68:71], s84 offen offset:0 lds\n" \
"v_mfma_f32_16x16x32_bf16 v[88:91], a[244:247], a[200:203], v[88:91]\n" \
"s_addk_i32 s84, 0x7c00\n" \
"v_mfma_f32_16x16x32_bf16 v[92:95], a[184:187], a[136:139], v[92:95]\n" \
"buffer_load_dwordx4 v26, s[68:71], s84 offen offset:1024 lds\n" \
"v_mfma_f32_16x16x32_bf16 v[92:95], a[248:251], a[200:203], v[92:95]\n" \
"ds_read_b64_tr_b16 a[236:237], v35 offset:36864\n" \
"s_add_i32 s13, s8, 0x4240000\n" \
"v_mfma_f32_16x16x32_bf16 v[96:99], a[188:191], a[136:139], v[96:99]\n" \
"ds_read_b64_tr_b16 a[238:239], v35 offset:37376\n" \
"s_add_i32 s6, s13, s6\n" \
"v_mfma_f32_16x16x32_bf16 v[96:99], a[252:255], a[200:203], v[96:99]\n" \
"ds_read_b128 a[136:139], v28 offset:36864\n" \
"v_writelane_b32 v11, s6, 40\n" \
"v_mfma_f32_16x16x32_bf16 v[112:115], a[188:191], a[140:143], v[112:115]\n" \
"ds_read_b128 a[200:203], v29 offset:36864\n" \
"s_add_i32 s8, s8, 0x42c0000\n" \
"v_mfma_f32_16x16x32_bf16 v[112:115], a[252:255], a[204:207], v[112:115]\n" \
"s_addk_i32 s84, 0x7c00\n" \
"v_mfma_f32_16x16x32_bf16 v[108:111], a[184:187], a[140:143], v[108:111]\n" \
"buffer_load_dwordx4 v27, s[68:71], s84 offen offset:2048 lds\n" \
"v_mfma_f32_16x16x32_bf16 v[108:111], a[248:251], a[204:207], v[108:111]\n" \
"s_addk_i32 s84, 0x7c00\n" \
"v_mfma_f32_16x16x32_bf16 v[104:107], a[180:183], a[140:143], v[104:107]\n" \
"buffer_load_dwordx4 v27, s[68:71], s84 offen offset:3072 lds\n" \
"v_mfma_f32_16x16x32_bf16 v[104:107], a[244:247], a[204:207], v[104:107]\n" \
"ds_read_b64_tr_b16 a[168:169], v34 offset:32768\n" \
"s_lshl_b32 s6, s12, 10\n" \
"v_mfma_f32_16x16x32_bf16 v[100:103], a[176:179], a[140:143], v[100:103]\n" \
"ds_read_b64_tr_b16 a[170:171], v34 offset:33280\n" \
"s_or_b32 s6, s8, s6\n" \
"v_mfma_f32_16x16x32_bf16 v[100:103], a[240:243], a[204:207], v[100:103]\n" \
"ds_read_b128 a[140:143], v28 offset:38912\n" \
"v_mfma_f32_16x16x32_bf16 v[52:55], a[176:179], a[128:131], v[52:55]\n" \
"ds_read_b128 a[204:207], v29 offset:38912\n" \
"v_mfma_f32_16x16x32_bf16 v[52:55], a[240:243], a[192:195], v[52:55]\n" \
"s_add_u32 m0, s30, 0x1000\n" \
"s_mov_b32 s84, 0x9300\n" \
"v_mfma_f32_16x16x32_bf16 v[56:59], a[180:183], a[128:131], v[56:59]\n" \
"buffer_load_dwordx4 v24, s[76:79], s84 offen offset:0 lds\n" \
"v_mfma_f32_16x16x32_bf16 v[56:59], a[244:247], a[192:195], v[56:59]\n" \
"s_addk_i32 s84, 0x2000\n" \
"v_mfma_f32_16x16x32_bf16 v[60:63], a[184:187], a[128:131], v[60:63]\n" \
"buffer_load_dwordx4 v25, s[76:79], s84 offen offset:1024 lds\n" \
"v_mfma_f32_16x16x32_bf16 v[60:63], a[248:251], a[192:195], v[60:63]\n" \
"ds_read_b64_tr_b16 a[232:233], v34 offset:36864\n" \
"v_mfma_f32_16x16x32_bf16 v[64:67], a[188:191], a[128:131], v[64:67]\n" \
"ds_read_b64_tr_b16 a[234:235], v34 offset:37376\n" \
"v_mfma_f32_16x16x32_bf16 v[64:67], a[252:255], a[192:195], v[64:67]\n" \
"ds_read_b128 a[128:131], v28 offset:32768\n" \
"v_mfma_f32_16x16x32_bf16 v[80:83], a[188:191], a[132:135], v[80:83]\n" \
"ds_read_b128 a[192:195], v29 offset:32768\n" \
"v_mfma_f32_16x16x32_bf16 v[80:83], a[252:255], a[196:199], v[80:83]\n" \
"s_cmp_lg_u32 s16, 0\n" \
"s_cselect_b32 s6, s7, s6\n" \
"v_mfma_f32_16x16x32_bf16 v[76:79], a[184:187], a[132:135], v[76:79]\n" \
"s_mulk_i32 s4, 0x4800\n" \
"v_writelane_b32 v11, s6, 41\n" \
"v_mfma_f32_16x16x32_bf16 v[76:79], a[248:251], a[196:199], v[76:79]\n" \
"s_add_i32 s6, s14, s10\n" \
"v_writelane_b32 v11, s6, 42\n" \
"v_mfma_f32_16x16x32_bf16 v[72:75], a[180:183], a[132:135], v[72:75]\n" \
"s_add_i32 s6, s9, 0x21c000\n" \
"s_add_i32 s9, s6, s10\n" \
"v_mfma_f32_16x16x32_bf16 v[72:75], a[244:247], a[196:199], v[72:75]\n" \
"s_cmp_lg_u32 s12, 0\n" \
"s_cselect_b32 s6, s6, s14\n" \
"v_mfma_f32_16x16x32_bf16 v[68:71], a[176:179], a[132:135], v[68:71]\n" \
"s_addk_i32 s6, 0x100\n" \
"v_writelane_b32 v11, s9, 43\n" \
"v_mfma_f32_16x16x32_bf16 v[68:71], a[240:243], a[196:199], v[68:71]\n" \
"ds_read_b128 a[132:135], v28 offset:34816\n" \
"ds_read_b128 a[196:199], v29 offset:34816\n" \
"s_waitcnt lgkmcnt(15)\n" \
"v_mfma_f32_16x16x32_bf16 a[0:3], a[160:163], a[144:147], a[0:3]\n" \
"s_addk_i32 s84, 0x2000\n" \
"v_mfma_f32_16x16x32_bf16 a[0:3], a[224:227], a[208:211], a[0:3]\n" \
"buffer_load_dwordx4 v24, s[76:79], s84 offen offset:2048 lds\n" \
"v_mfma_f32_16x16x32_bf16 a[4:7], a[164:167], a[144:147], a[4:7]\n" \
"ds_read_b64_tr_b16 a[176:177], v36 offset:32768\n" \
"v_mfma_f32_16x16x32_bf16 a[4:7], a[228:231], a[208:211], a[4:7]\n" \
"s_addk_i32 s84, 0x2000\n" \
"v_mfma_f32_16x16x32_bf16 a[12:15], a[172:175], a[144:147], a[12:15]\n" \
"buffer_load_dwordx4 v25, s[76:79], s84 offen offset:3072 lds\n" \
"s_waitcnt lgkmcnt(13)\n" \
"v_mfma_f32_16x16x32_bf16 a[12:15], a[236:239], a[208:211], a[12:15]\n" \
"ds_read_b64_tr_b16 a[178:179], v36 offset:33792\n" \
"s_waitcnt lgkmcnt(10)\n" \
"v_mfma_f32_16x16x32_bf16 a[8:11], a[144:147], a[168:171], a[8:11]\n" \
"ds_read_b64_tr_b16 a[240:241], v36 offset:40960\n" \
"s_waitcnt lgkmcnt(7)\n" \
"v_mfma_f32_16x16x32_bf16 a[8:11], a[208:211], a[232:235], a[8:11]\n" \
"ds_read_b64_tr_b16 a[242:243], v36 offset:41984\n" \
"v_mfma_f32_16x16x32_bf16 a[24:27], a[148:151], a[168:171], a[24:27]\n" \
"ds_read_b64_tr_b16 a[180:181], v37 offset:32768\n" \
"v_mfma_f32_16x16x32_bf16 a[24:27], a[212:215], a[232:235], a[24:27]\n" \
"ds_read_b64_tr_b16 a[182:183], v37 offset:33792\n" \
"v_mfma_f32_16x16x32_bf16 a[28:31], a[172:175], a[148:151], a[28:31]\n" \
"ds_read_b64_tr_b16 a[244:245], v37 offset:40960\n" \
"v_mfma_f32_16x16x32_bf16 a[28:31], a[236:239], a[212:215], a[28:31]\n" \
"ds_read_b64_tr_b16 a[246:247], v37 offset:41984\n" \
"v_mfma_f32_16x16x32_bf16 a[20:23], a[164:167], a[148:151], a[20:23]\n" \
"s_cmp_lg_u32 s16, 0\n" \
"s_cselect_b32 s6, s7, s6\n" \
"v_mfma_f32_16x16x32_bf16 a[20:23], a[228:231], a[212:215], a[20:23]\n" \
"s_add_u32 s88, s42, 0x3000\n" \
"buffer_store_dwordx4 v[244:247], v20, s[80:83], s88 offen offset:2048 sc1\n" \
"v_mfma_f32_16x16x32_bf16 a[16:19], a[160:163], a[148:151], a[16:19]\n" \
"ds_read_b64_tr_b16 a[184:185], v38 offset:32768\n" \
"v_writelane_b32 v11, s6, 44\n" \
"v_mfma_f32_16x16x32_bf16 a[16:19], a[224:227], a[212:215], a[16:19]\n" \
"ds_read_b64_tr_b16 a[186:187], v38 offset:33792\n" \
"s_or_b32 s6, s8, s15\n" \
"v_mfma_f32_16x16x32_bf16 a[32:35], a[160:163], a[152:155], a[32:35]\n" \
"ds_read_b64_tr_b16 a[248:249], v38 offset:40960\n" \
"v_writelane_b32 v11, s6, 52\n" \
"v_mfma_f32_16x16x32_bf16 a[32:35], a[224:227], a[216:219], a[32:35]\n" \
"ds_read_b64_tr_b16 a[250:251], v38 offset:41984\n" \
"s_lshl_b32 s6, s3, 17\n" \
"v_mfma_f32_16x16x32_bf16 a[36:39], a[164:167], a[152:155], a[36:39]\n" \
"s_add_i32 s6, s13, s6\n" \
"s_or_b32 s5, s6, s5\n" \
"v_mfma_f32_16x16x32_bf16 a[36:39], a[228:231], a[216:219], a[36:39]\n" \
"s_add_u32 s89, s42, 0x3000\n" \
"buffer_store_dwordx4 v[184:187], v20, s[80:83], s89 offen offset:1024 sc1\n" \
"v_mfma_f32_16x16x32_bf16 a[44:47], a[172:175], a[152:155], a[44:47]\n" \
"ds_read_b64_tr_b16 a[188:189], v39 offset:32768\n" \
"s_or_b32 s5, s5, 0x3840\n" \
"v_mfma_f32_16x16x32_bf16 a[44:47], a[236:239], a[216:219], a[44:47]\n" \
"ds_read_b64_tr_b16 a[190:191], v39 offset:33792\n" \
"v_writelane_b32 v11, s5, 6\n" \
"v_mfma_f32_16x16x32_bf16 a[40:43], a[152:155], a[168:171], a[40:43]\n" \
"ds_read_b64_tr_b16 a[252:253], v39 offset:40960\n" \
"s_mov_b32 s5, 0xff0000\n" \
"v_mfma_f32_16x16x32_bf16 a[40:43], a[216:219], a[232:235], a[40:43]\n" \
"s_cmp_lg_u32 s16, 0\n" \
"s_cselect_b32 s5, s17, s5\n" \
"v_mfma_f32_16x16x32_bf16 a[56:59], a[156:159], a[168:171], a[56:59]\n" \
"ds_read_b64_tr_b16 a[254:255], v39 offset:41984\n" \
"v_writelane_b32 v11, s5, 7\n" \
"v_mfma_f32_16x16x32_bf16 a[56:59], a[220:223], a[232:235], a[56:59]\n" \
"s_mul_i32 s5, s3, 0x1200\n" \
"s_add_i32 s4, s5, s4\n" \
"v_mfma_f32_16x16x32_bf16 a[60:63], a[172:175], a[156:159], a[60:63]\n" \
"s_add_u32 s88, s42, 0x3000\n" \
"buffer_store_dwordx4 v[188:191], v20, s[80:83], s88 offen offset:0 sc1\n" \
"v_mfma_f32_16x16x32_bf16 a[60:63], a[236:239], a[220:223], a[60:63]\n" \
"s_add_i32 s4, s4, s11\n" \
"v_writelane_b32 v11, s4, 8\n" \
"v_mfma_f32_16x16x32_bf16 a[52:55], a[164:167], a[156:159], a[52:55]\n" \
"s_lshl_b32 s4, s2, 16\n" \
"s_add_i32 s5, s4, 0x10000\n" \
"v_mfma_f32_16x16x32_bf16 a[52:55], a[228:231], a[220:223], a[52:55]\n" \
"s_and_b32 s5, s5, 0x70000\n" \
"s_add_i32 s5, s13, s5\n" \
"v_mfma_f32_16x16x32_bf16 a[48:51], a[160:163], a[156:159], a[48:51]\n" \
"v_writelane_b32 v11, s5, 45\n" \
"s_add_i32 s5, s4, 0x20000\n" \
"v_mfma_f32_16x16x32_bf16 a[48:51], a[224:227], a[220:223], a[48:51]\n" \
"s_and_b32 s5, s5, 0x70000\n" \
"s_add_i32 s5, s13, s5\n" \
"v_mfma_f32_16x16x32_bf16 a[64:67], a[164:167], a[140:143], a[64:67]\n" \
"s_bitset1_b32 s5, 11\n" \
"v_writelane_b32 v11, s5, 46\n" \
"v_mfma_f32_16x16x32_bf16 a[64:67], a[228:231], a[204:207], a[64:67]\n" \
"s_add_i32 s5, s4, 0x30000\n" \
"s_and_b32 s5, s5, 0x70000\n" \
"v_mfma_f32_16x16x32_bf16 a[80:83], a[172:175], a[140:143], a[80:83]\n" \
"s_add_i32 s5, s13, s5\n" \
"s_bitset1_b32 s5, 12\n" \
"v_mfma_f32_16x16x32_bf16 a[80:83], a[236:239], a[204:207], a[80:83]\n" \
"v_writelane_b32 v11, s5, 47\n" \
"s_and_b32 s5, s4, 0x70000\n" \
"v_mfma_f32_16x16x32_bf16 a[68:71], a[140:143], a[168:171], a[68:71]\n" \
"s_xor_b32 s5, s5, 0x40000\n" \
"s_add_i32 s5, s13, s5\n" \
"v_mfma_f32_16x16x32_bf16 a[68:71], a[204:207], a[232:235], a[68:71]\n" \
"s_waitcnt vmcnt(3) lgkmcnt(0)\n" \
"s_barrier\n" \
"s_nop 0\n" \
"v_mfma_f32_16x16x32_bf16 v[116:119], a[176:179], a[144:147], v[116:119]\n" \
"s_add_u32 m0, s40, 0x8000\n" \
"s_add_u32 s85, s39, 0xe000\n" \
"v_mfma_f32_16x16x32_bf16 v[116:119], a[240:243], a[208:211], v[116:119]\n" \
"buffer_load_dwordx4 v20, s[80:83], s85 offen sc1 lds\n" \
"v_mfma_f32_16x16x32_bf16 v[120:123], a[180:183], a[144:147], v[120:123]\n" \
"ds_read_b64_tr_b16 a[160:161], v32 offset:0\n" \
"s_or_b32 s5, s5, 0x1800\n" \
"v_mfma_f32_16x16x32_bf16 v[120:123], a[244:247], a[208:211], v[120:123]\n" \
"ds_read_b64_tr_b16 a[162:163], v32 offset:512\n" \
"v_writelane_b32 v11, s5, 48\n" \
"v_mfma_f32_16x16x32_bf16 v[124:127], a[184:187], a[144:147], v[124:127]\n" \
"s_add_u32 m0, s40, 0x9000\n" \
"s_add_u32 s85, s39, 0xf000\n" \
"v_mfma_f32_16x16x32_bf16 v[124:127], a[248:251], a[208:211], v[124:127]\n" \
"buffer_load_dwordx4 v20, s[80:83], s85 offen sc1 lds\n" \
"v_mfma_f32_16x16x32_bf16 v[128:131], a[188:191], a[144:147], v[128:131]\n" \
"ds_read_b64_tr_b16 a[224:225], v32 offset:4096\n" \
"s_add_i32 s5, s4, 0x50000\n" \
"v_mfma_f32_16x16x32_bf16 v[128:131], a[252:255], a[208:211], v[128:131]\n" \
"ds_read_b128 a[144:147], v28 offset:8192\n" \
"s_and_b32 s5, s5, 0x70000\n" \
"v_mfma_f32_16x16x32_bf16 v[144:147], a[188:191], a[148:151], v[144:147]\n" \
"ds_read_b128 a[208:211], v29 offset:8192\n" \
"s_add_i32 s5, s13, s5\n" \
"v_mfma_f32_16x16x32_bf16 v[144:147], a[252:255], a[212:215], v[144:147]\n" \
"s_add_u32 m0, s40, 0xb000\n" \
"s_movk_i32 s84, 0x380\n" \
"v_mfma_f32_16x16x32_bf16 v[140:143], a[184:187], a[148:151], v[140:143]\n" \
"buffer_load_dwordx4 v21, s[76:79], s84 offen offset:0 lds\n" \
"v_mfma_f32_16x16x32_bf16 v[140:143], a[248:251], a[212:215], v[140:143]\n" \
"ds_read_b64_tr_b16 a[226:227], v32 offset:4608\n" \
"s_bitset1_b32 s5, 13\n" \
"v_mfma_f32_16x16x32_bf16 v[136:139], a[180:183], a[148:151], v[136:139]\n" \
"s_add_u32 m0, s30, 0x8000\n" \
"s_movk_i32 s84, 0x380\n" \
"v_mfma_f32_16x16x32_bf16 v[136:139], a[244:247], a[212:215], v[136:139]\n" \
"buffer_load_dwordx4 v24, s[76:79], s84 offen offset:0 lds\n" \
"v_mfma_f32_16x16x32_bf16 v[132:135], a[176:179], a[148:151], v[132:135]\n" \
"ds_read_b64_tr_b16 a[164:165], v33 offset:0\n" \
"v_writelane_b32 v11, s5, 49\n" \
"v_mfma_f32_16x16x32_bf16 v[132:135], a[240:243], a[212:215], v[132:135]\n" \
"ds_read_b128 a[148:151], v28 offset:10240\n" \
"s_add_i32 s5, s4, 0x60000\n" \
"v_mfma_f32_16x16x32_bf16 v[148:151], a[176:179], a[152:155], v[148:151]\n" \
"ds_read_b128 a[212:215], v29 offset:10240\n" \
"s_and_b32 s5, s5, 0x70000\n" \
"v_mfma_f32_16x16x32_bf16 v[148:151], a[240:243], a[216:219], v[148:151]\n" \
"s_movk_i32 s84, 0x2380\n" \
"v_mfma_f32_16x16x32_bf16 v[152:155], a[180:183], a[152:155], v[152:155]\n" \
"buffer_load_dwordx4 v25, s[76:79], s84 offen offset:1024 lds\n" \
"v_mfma_f32_16x16x32_bf16 v[152:155], a[244:247], a[216:219], v[152:155]\n" \
"ds_read_b64_tr_b16 a[166:167], v33 offset:512\n" \
"s_add_i32 s4, s4, 0x70000\n" \
"v_mfma_f32_16x16x32_bf16 v[156:159], a[184:187], a[152:155], v[156:159]\n" \
"ds_read_b64_tr_b16 a[228:229], v33 offset:4096\n" \
"s_add_i32 s5, s13, s5\n" \
"v_mfma_f32_16x16x32_bf16 v[156:159], a[248:251], a[216:219], v[156:159]\n" \
"s_movk_i32 s84, 0x4380\n" \
"v_mfma_f32_16x16x32_bf16 v[160:163], a[188:191], a[152:155], v[160:163]\n" \
"buffer_load_dwordx4 v24, s[76:79], s84 offen offset:2048 lds\n" \
"v_mfma_f32_16x16x32_bf16 v[160:163], a[252:255], a[216:219], v[160:163]\n" \
"ds_read_b128 a[152:155], v28 offset:12288\n" \
"s_and_b32 s4, s4, 0x70000\n" \
"v_mfma_f32_16x16x32_bf16 v[176:179], a[188:191], a[156:159], v[176:179]\n" \
"ds_read_b128 a[216:219], v29 offset:12288\n" \
"s_or_b32 s5, s5, 0x2800\n" \
"v_mfma_f32_16x16x32_bf16 v[176:179], a[252:255], a[220:223], v[176:179]\n" \
"s_movk_i32 s84, 0x6380\n" \
"v_mfma_f32_16x16x32_bf16 v[172:175], a[184:187], a[156:159], v[172:175]\n" \
"buffer_load_dwordx4 v25, s[76:79], s84 offen offset:3072 lds\n" \
"v_mfma_f32_16x16x32_bf16 v[172:175], a[248:251], a[220:223], v[172:175]\n" \
"ds_read_b64_tr_b16 a[230:231], v33 offset:4608\n" \
"v_writelane_b32 v11, s5, 50\n" \
"v_mfma_f32_16x16x32_bf16 v[168:171], a[180:183], a[156:159], v[168:171]\n" \
"ds_read_b64_tr_b16 a[172:173], v35 offset:0\n" \
"s_add_i32 s13, s13, s4\n" \
"v_mfma_f32_16x16x32_bf16 v[168:171], a[244:247], a[220:223], v[168:171]\n" \
"ds_read_b64_tr_b16 a[174:175], v35 offset:512\n" \
"s_or_b32 s4, s13, 0x3000\n" \
"v_mfma_f32_16x16x32_bf16 v[164:167], a[176:179], a[156:159], v[164:167]\n" \
"v_writelane_b32 v11, s4, 51\n" \
"s_movk_i32 s11, 0x4000\n" \
"v_mfma_f32_16x16x32_bf16 v[164:167], a[240:243], a[220:223], v[164:167]\n" \
"ds_read_b128 a[156:159], v28 offset:14336\n" \
"s_lshl_b32 s4, s2, 2\n" \
"v_mfma_f32_16x16x32_bf16 v[84:87], a[176:179], a[136:139], v[84:87]\n" \
"ds_read_b128 a[220:223], v29 offset:14336\n" \
"s_and_b32 s4, s4, 28\n" \
"v_mfma_f32_16x16x32_bf16 v[84:87], a[240:243], a[200:203], v[84:87]\n" \
"s_add_u32 m0, s31, 0x8000\n" \
"s_mov_b32 s84, 0x380000\n" \
"v_mfma_f32_16x16x32_bf16 v[88:91], a[180:183], a[136:139], v[88:91]\n" \
"buffer_load_dwordx4 v26, s[68:71], s84 offen offset:0 lds\n" \
"v_mfma_f32_16x16x32_bf16 v[88:91], a[244:247], a[200:203], v[88:91]\n" \
"s_addk_i32 s84, 0x7c00\n" \
"v_mfma_f32_16x16x32_bf16 v[92:95], a[184:187], a[136:139], v[92:95]\n" \
"buffer_load_dwordx4 v26, s[68:71], s84 offen offset:1024 lds\n" \
"v_mfma_f32_16x16x32_bf16 v[92:95], a[248:251], a[200:203], v[92:95]\n" \
"ds_read_b64_tr_b16 a[236:237], v35 offset:4096\n" \
"s_bfe_u32 s5, s2, 0x20003\n" \
"v_mfma_f32_16x16x32_bf16 v[96:99], a[188:191], a[136:139], v[96:99]\n" \
"ds_read_b64_tr_b16 a[238:239], v35 offset:4608\n" \
"s_or_b32 s4, s4, s5\n" \
"v_mfma_f32_16x16x32_bf16 v[96:99], a[252:255], a[200:203], v[96:99]\n" \
"ds_read_b128 a[136:139], v28 offset:4096\n" \
"s_lshr_b32 s5, s2, 5\n" \
"v_mfma_f32_16x16x32_bf16 v[112:115], a[188:191], a[140:143], v[112:115]\n" \
"ds_read_b128 a[200:203], v29 offset:4096\n" \
"s_lshr_b32 s19, s3, 1\n" \
"v_mfma_f32_16x16x32_bf16 v[112:115], a[252:255], a[204:207], v[112:115]\n" \
"s_addk_i32 s84, 0x7c00\n" \
"v_mfma_f32_16x16x32_bf16 v[108:111], a[184:187], a[140:143], v[108:111]\n" \
"buffer_load_dwordx4 v27, s[68:71], s84 offen offset:2048 lds\n" \
"v_mfma_f32_16x16x32_bf16 v[108:111], a[248:251], a[204:207], v[108:111]\n" \
"s_addk_i32 s84, 0x7c00\n" \
"v_mfma_f32_16x16x32_bf16 v[104:107], a[180:183], a[140:143], v[104:107]\n" \
"buffer_load_dwordx4 v27, s[68:71], s84 offen offset:3072 lds\n" \
"v_mfma_f32_16x16x32_bf16 v[104:107], a[244:247], a[204:207], v[104:107]\n" \
"ds_read_b64_tr_b16 a[168:169], v34 offset:0\n" \
"s_lshl_b32 s18, s5, 1\n" \
"v_mfma_f32_16x16x32_bf16 v[100:103], a[176:179], a[140:143], v[100:103]\n" \
"ds_read_b64_tr_b16 a[170:171], v34 offset:512\n" \
"s_lshl_b32 s6, s4, 20\n" \
"v_mfma_f32_16x16x32_bf16 v[100:103], a[240:243], a[204:207], v[100:103]\n" \
"ds_read_b128 a[140:143], v28 offset:6144\n" \
"s_lshl_b32 s7, s4, 8\n" \
"v_mfma_f32_16x16x32_bf16 v[52:55], a[176:179], a[128:131], v[52:55]\n" \
"ds_read_b128 a[204:207], v29 offset:6144\n" \
"s_and_b32 s13, s3, 1\n" \
"v_mfma_f32_16x16x32_bf16 v[52:55], a[240:243], a[192:195], v[52:55]\n" \
"s_add_u32 m0, s30, 0x9000\n" \
"s_mov_b32 s84, 0x9380\n" \
"v_mfma_f32_16x16x32_bf16 v[56:59], a[180:183], a[128:131], v[56:59]\n" \
"buffer_load_dwordx4 v24, s[76:79], s84 offen offset:0 lds\n" \
"v_mfma_f32_16x16x32_bf16 v[56:59], a[244:247], a[192:195], v[56:59]\n" \
"s_addk_i32 s84, 0x2000\n" \
"v_mfma_f32_16x16x32_bf16 v[60:63], a[184:187], a[128:131], v[60:63]\n" \
"buffer_load_dwordx4 v25, s[76:79], s84 offen offset:1024 lds\n" \
"v_mfma_f32_16x16x32_bf16 v[60:63], a[248:251], a[192:195], v[60:63]\n" \
"ds_read_b64_tr_b16 a[232:233], v34 offset:4096\n" \
"s_add_i32 s14, s18, s19\n" \
"v_mfma_f32_16x16x32_bf16 v[64:67], a[188:191], a[128:131], v[64:67]\n" \
"ds_read_b64_tr_b16 a[234:235], v34 offset:4608\n" \
"v_mfma_f32_16x16x32_bf16 v[64:67], a[252:255], a[192:195], v[64:67]\n" \
"ds_read_b128 a[128:131], v28 offset:0\n" \
"v_mfma_f32_16x16x32_bf16 v[80:83], a[188:191], a[132:135], v[80:83]\n" \
"ds_read_b128 a[192:195], v29 offset:0\n" \
"v_mfma_f32_16x16x32_bf16 v[80:83], a[252:255], a[196:199], v[80:83]\n" \
"s_add_u32 s6, s26, s6\n" \
"s_addc_u32 s9, s27, 0\n" \
"v_mfma_f32_16x16x32_bf16 v[76:79], a[184:187], a[132:135], v[76:79]\n" \
"s_add_u32 s8, s6, 0x1000000\n" \
"s_addc_u32 s6, s9, 0\n" \
"v_mfma_f32_16x16x32_bf16 v[76:79], a[248:251], a[196:199], v[76:79]\n" \
"s_and_b32 s9, s6, 0xffff\n" \
"s_lshl_b32 s6, s5, 16\n" \
"v_mfma_f32_16x16x32_bf16 v[72:75], a[180:183], a[132:135], v[72:75]\n" \
"s_sub_i32 s10, 0x100000, s6\n" \
"s_mov_b32 s6, 0x8000\n" \
"v_mfma_f32_16x16x32_bf16 v[72:75], a[244:247], a[196:199], v[72:75]\n" \
"s_cmp_lg_u32 s5, 0\n" \
"s_cselect_b32 s17, s11, s6\n" \
"v_mfma_f32_16x16x32_bf16 v[68:71], a[176:179], a[132:135], v[68:71]\n" \
"s_lshl_b32 s6, s3, 1\n" \
"s_lshl_b32 s5, s5, 3\n" \
"v_mfma_f32_16x16x32_bf16 v[68:71], a[240:243], a[196:199], v[68:71]\n" \
"ds_read_b128 a[132:135], v28 offset:2048\n" \
"ds_read_b128 a[196:199], v29 offset:2048\n" \
"s_waitcnt lgkmcnt(15)\n" \
"v_mfma_f32_16x16x32_bf16 a[0:3], a[160:163], a[144:147], a[0:3]\n" \
"s_addk_i32 s84, 0x2000\n" \
"v_mfma_f32_16x16x32_bf16 a[0:3], a[224:227], a[208:211], a[0:3]\n" \
"buffer_load_dwordx4 v24, s[76:79], s84 offen offset:2048 lds\n" \
"v_mfma_f32_16x16x32_bf16 a[4:7], a[164:167], a[144:147], a[4:7]\n" \
"ds_read_b64_tr_b16 a[176:177], v36 offset:0\n" \
"s_and_b32 s6, s6, 4\n" \
"v_mfma_f32_16x16x32_bf16 a[4:7], a[228:231], a[208:211], a[4:7]\n" \
"s_addk_i32 s84, 0x2000\n" \
"v_mfma_f32_16x16x32_bf16 a[12:15], a[172:175], a[144:147], a[12:15]\n" \
"buffer_load_dwordx4 v25, s[76:79], s84 offen offset:3072 lds\n" \
"s_waitcnt lgkmcnt(13)\n" \
"v_mfma_f32_16x16x32_bf16 a[12:15], a[236:239], a[208:211], a[12:15]\n" \
"ds_read_b64_tr_b16 a[178:179], v36 offset:1024\n" \
"s_or_b32 s12, s5, s6\n" \
"s_waitcnt lgkmcnt(10)\n" \
"v_mfma_f32_16x16x32_bf16 a[8:11], a[144:147], a[168:171], a[8:11]\n" \
"ds_read_b64_tr_b16 a[240:241], v36 offset:8192\n" \
"s_sub_i32 s5, s13, s5\n" \
"s_waitcnt lgkmcnt(7)\n" \
"v_mfma_f32_16x16x32_bf16 a[8:11], a[208:211], a[232:235], a[8:11]\n" \
"ds_read_b64_tr_b16 a[242:243], v36 offset:9216\n" \
"s_or_b32 s5, s5, s6\n" \
"v_mfma_f32_16x16x32_bf16 a[24:27], a[148:151], a[168:171], a[24:27]\n" \
"ds_read_b64_tr_b16 a[180:181], v37 offset:0\n" \
"s_or_b32 s6, s12, s13\n" \
"v_mfma_f32_16x16x32_bf16 a[24:27], a[212:215], a[232:235], a[24:27]\n" \
"ds_read_b64_tr_b16 a[182:183], v37 offset:1024\n" \
"s_lshl_b32 s4, s4, 2\n" \
"v_mfma_f32_16x16x32_bf16 a[28:31], a[172:175], a[148:151], a[28:31]\n" \
"ds_read_b64_tr_b16 a[244:245], v37 offset:8192\n" \
"s_mulk_i32 s6, 0x92\n" \
"v_mfma_f32_16x16x32_bf16 a[28:31], a[236:239], a[212:215], a[28:31]\n" \
"ds_read_b64_tr_b16 a[246:247], v37 offset:9216\n" \
"s_add_i32 s4, s4, 18\n" \
"v_mfma_f32_16x16x32_bf16 a[20:23], a[164:167], a[148:151], a[20:23]\n" \
"s_mulk_i32 s5, 0x92\n" \
"s_add_i32 s6, s4, s6\n" \
"v_mfma_f32_16x16x32_bf16 a[20:23], a[228:231], a[212:215], a[20:23]\n" \
"s_add_u32 s89, s42, 0x0\n" \
"buffer_store_dwordx4 v[192:195], v20, s[80:83], s89 offen offset:2048 sc1\n" \
"v_mfma_f32_16x16x32_bf16 a[16:19], a[160:163], a[148:151], a[16:19]\n" \
"ds_read_b64_tr_b16 a[184:185], v38 offset:0\n" \
"s_add_i32 s5, s5, s4\n" \
"v_mfma_f32_16x16x32_bf16 a[16:19], a[224:227], a[212:215], a[16:19]\n" \
"ds_read_b64_tr_b16 a[186:187], v38 offset:1024\n" \
"s_lshl_b32 s4, s3, 11\n" \
"v_mfma_f32_16x16x32_bf16 a[32:35], a[160:163], a[152:155], a[32:35]\n" \
"ds_read_b64_tr_b16 a[248:249], v38 offset:8192\n" \
"s_lshl_b32 s15, s13, 12\n" \
"v_mfma_f32_16x16x32_bf16 a[32:35], a[224:227], a[216:219], a[32:35]\n" \
"ds_read_b64_tr_b16 a[250:251], v38 offset:9216\n" \
"s_lshl_b32 s16, s19, 14\n" \
"v_mfma_f32_16x16x32_bf16 a[36:39], a[164:167], a[152:155], a[36:39]\n" \
"s_mul_i32 s19, s19, 0xe000\n" \
"s_mov_b32 s11, 0x20000\n" \
"v_mfma_f32_16x16x32_bf16 a[36:39], a[228:231], a[216:219], a[36:39]\n" \
"s_add_u32 s88, s42, 0x0\n" \
"buffer_store_dwordx4 v[196:199], v20, s[80:83], s88 offen offset:3072 sc1\n" \
"v_mfma_f32_16x16x32_bf16 a[44:47], a[172:175], a[152:155], a[44:47]\n" \
"ds_read_b64_tr_b16 a[188:189], v39 offset:0\n" \
"s_or_b32 s12, s4, 0xc000\n" \
"v_mfma_f32_16x16x32_bf16 a[44:47], a[236:239], a[216:219], a[44:47]\n" \
"ds_read_b64_tr_b16 a[190:191], v39 offset:1024\n" \
"s_lshl_b32 s4, s3, 8\n" \
"v_mfma_f32_16x16x32_bf16 a[40:43], a[152:155], a[168:171], a[40:43]\n" \
"ds_read_b64_tr_b16 a[252:253], v39 offset:8192\n" \
"s_or_b32 s22, s15, s16\n" \
"v_mfma_f32_16x16x32_bf16 a[40:43], a[216:219], a[232:235], a[40:43]\n" \
"s_or_b32 s23, s19, s15\n" \
"s_lshl_b32 s60, s6, 10\n" \
"v_mfma_f32_16x16x32_bf16 a[56:59], a[156:159], a[168:171], a[56:59]\n" \
"ds_read_b64_tr_b16 a[254:255], v39 offset:9216\n" \
"s_lshl_b32 s61, s5, 10\n" \
"v_mfma_f32_16x16x32_bf16 a[56:59], a[220:223], a[232:235], a[56:59]\n" \
"s_lshl_b32 s14, s14, 15\n" \
"s_or_b32 s20, s4, 0x26c00\n" \
"v_mfma_f32_16x16x32_bf16 a[60:63], a[172:175], a[156:159], a[60:63]\n" \
"s_add_u32 s89, s42, 0x2000\n" \
"buffer_store_dwordx4 v[200:203], v20, s[80:83], s89 offen offset:0 sc1\n" \
"v_mfma_f32_16x16x32_bf16 a[60:63], a[236:239], a[220:223], a[60:63]\n" \
"s_or_b32 s13, s22, 0x8000\n" \
"s_add_i32 s22, s22, 0x14000\n" \
"v_mfma_f32_16x16x32_bf16 a[52:55], a[164:167], a[156:159], a[52:55]\n" \
"s_bitset1_b32 s23, 16\n" \
"s_or_b32 s24, s7, 0x5be8020\n" \
"v_mfma_f32_16x16x32_bf16 a[52:55], a[228:231], a[220:223], a[52:55]\n" \
"s_and_b32 s57, s27, 0xffff\n" \
"s_mov_b32 s58, 0x6000000\n" \
"v_mfma_f32_16x16x32_bf16 a[48:51], a[160:163], a[156:159], a[48:51]\n" \
"s_mov_b32 s56, s26\n" \
"s_mov_b32 s59, s11\n" \
"v_mfma_f32_16x16x32_bf16 a[48:51], a[224:227], a[220:223], a[48:51]\n" \
"s_add_i32 s60, s60, 0x3000000\n" \
"s_add_i32 s61, s61, 0x411c000\n" \
"v_mfma_f32_16x16x32_bf16 a[64:67], a[164:167], a[140:143], a[64:67]\n" \
"s_sub_i32 s19, 14, s18\n" \
"s_mov_b32 s21, 0x71800000\n" \
"v_mfma_f32_16x16x32_bf16 a[64:67], a[228:231], a[204:207], a[64:67]\n" \
"v_mfma_f32_16x16x32_bf16 a[80:83], a[172:175], a[140:143], a[80:83]\n" \
"v_mfma_f32_16x16x32_bf16 a[80:83], a[236:239], a[204:207], a[80:83]\n" \
"v_mfma_f32_16x16x32_bf16 a[68:71], a[140:143], a[168:171], a[68:71]\n" \
"v_mfma_f32_16x16x32_bf16 a[68:71], a[204:207], a[232:235], a[68:71]\n" \
"s_waitcnt vmcnt(3) lgkmcnt(0)\n" \
"s_barrier\n" \
"v_mfma_f32_16x16x32_bf16 v[116:119], a[176:179], a[144:147], v[116:119]\n" \
"s_add_u32 m0, s40, 0x0\n" \
"s_nop 0\n" \
"s_add_u32 s85, s39, 0x10000\n" \
"v_mfma_f32_16x16x32_bf16 v[116:119], a[240:243], a[208:211], v[116:119]\n" \
"buffer_load_dwordx4 v20, s[80:83], s85 offen sc1 lds\n" \
"v_mfma_f32_16x16x32_bf16 v[120:123], a[180:183], a[144:147], v[120:123]\n" \
"ds_read_b64_tr_b16 a[160:161], v32 offset:32768\n" \
"v_mfma_f32_16x16x32_bf16 v[120:123], a[244:247], a[208:211], v[120:123]\n" \
"ds_read_b64_tr_b16 a[162:163], v32 offset:33280\n" \
"v_mfma_f32_16x16x32_bf16 v[124:127], a[184:187], a[144:147], v[124:127]\n" \
"s_add_u32 m0, s40, 0x1000\n" \
"s_add_u32 s85, s39, 0x11000\n" \
"v_mfma_f32_16x16x32_bf16 v[124:127], a[248:251], a[208:211], v[124:127]\n" \
"buffer_load_dwordx4 v20, s[80:83], s85 offen sc1 lds\n" \
"v_mfma_f32_16x16x32_bf16 v[128:131], a[188:191], a[144:147], v[128:131]\n" \
"ds_read_b64_tr_b16 a[224:225], v32 offset:36864\n" \
"v_mfma_f32_16x16x32_bf16 v[128:131], a[252:255], a[208:211], v[128:131]\n" \
"ds_read_b128 a[144:147], v28 offset:40960\n" \
"v_mfma_f32_16x16x32_bf16 v[144:147], a[188:191], a[148:151], v[144:147]\n" \
"ds_read_b128 a[208:211], v29 offset:40960\n" \
"v_mfma_f32_16x16x32_bf16 v[144:147], a[252:255], a[212:215], v[144:147]\n" \
"s_add_u32 m0, s40, 0x3000\n" \
"s_movk_i32 s84, 0x400\n" \
"v_mfma_f32_16x16x32_bf16 v[140:143], a[184:187], a[148:151], v[140:143]\n" \
"buffer_load_dwordx4 v21, s[76:79], s84 offen offset:0 lds\n" \
"v_mfma_f32_16x16x32_bf16 v[140:143], a[248:251], a[212:215], v[140:143]\n" \
"ds_read_b64_tr_b16 a[226:227], v32 offset:37376\n" \
"v_mfma_f32_16x16x32_bf16 v[136:139], a[180:183], a[148:151], v[136:139]\n" \
"s_add_u32 m0, s30, 0x0\n" \
"s_movk_i32 s84, 0x400\n" \
"v_mfma_f32_16x16x32_bf16 v[136:139], a[244:247], a[212:215], v[136:139]\n" \
"buffer_load_dwordx4 v24, s[76:79], s84 offen offset:0 lds\n" \
"v_mfma_f32_16x16x32_bf16 v[132:135], a[176:179], a[148:151], v[132:135]\n" \
"ds_read_b64_tr_b16 a[164:165], v33 offset:32768\n" \
"v_mfma_f32_16x16x32_bf16 v[132:135], a[240:243], a[212:215], v[132:135]\n" \
"ds_read_b128 a[148:151], v28 offset:43008\n" \
"v_mfma_f32_16x16x32_bf16 v[148:151], a[176:179], a[152:155], v[148:151]\n" \
"ds_read_b128 a[212:215], v29 offset:43008\n" \
"v_mfma_f32_16x16x32_bf16 v[148:151], a[240:243], a[216:219], v[148:151]\n" \
"s_movk_i32 s84, 0x2400\n" \
"v_mfma_f32_16x16x32_bf16 v[152:155], a[180:183], a[152:155], v[152:155]\n" \
"buffer_load_dwordx4 v25, s[76:79], s84 offen offset:1024 lds\n" \
"v_mfma_f32_16x16x32_bf16 v[152:155], a[244:247], a[216:219], v[152:155]\n" \
"ds_read_b64_tr_b16 a[166:167], v33 offset:33280\n" \
"v_mfma_f32_16x16x32_bf16 v[156:159], a[184:187], a[152:155], v[156:159]\n" \
"ds_read_b64_tr_b16 a[228:229], v33 offset:36864\n" \
"v_mfma_f32_16x16x32_bf16 v[156:159], a[248:251], a[216:219], v[156:159]\n" \
"s_movk_i32 s84, 0x4400\n" \
"v_mfma_f32_16x16x32_bf16 v[160:163], a[188:191], a[152:155], v[160:163]\n" \
"buffer_load_dwordx4 v24, s[76:79], s84 offen offset:2048 lds\n" \
"v_mfma_f32_16x16x32_bf16 v[160:163], a[252:255], a[216:219], v[160:163]\n" \
"ds_read_b128 a[152:155], v28 offset:45056\n" \
"v_mfma_f32_16x16x32_bf16 v[176:179], a[188:191], a[156:159], v[176:179]\n" \
"ds_read_b128 a[216:219], v29 offset:45056\n" \
"v_mfma_f32_16x16x32_bf16 v[176:179], a[252:255], a[220:223], v[176:179]\n" \
"s_movk_i32 s84, 0x6400\n" \
"v_mfma_f32_16x16x32_bf16 v[172:175], a[184:187], a[156:159], v[172:175]\n" \
"buffer_load_dwordx4 v25, s[76:79], s84 offen offset:3072 lds\n" \
"v_mfma_f32_16x16x32_bf16 v[172:175], a[248:251], a[220:223], v[172:175]\n" \
"ds_read_b64_tr_b16 a[230:231], v33 offset:37376\n" \
"v_mfma_f32_16x16x32_bf16 v[168:171], a[180:183], a[156:159], v[168:171]\n" \
".fill (L_pb + 73440 - .) / 4, 4, 0xBF800000\n" \
"L_pp18:\n" \
"s_cbranch_execz L_pret\n" \
"ds_read_b64_tr_b16 a[172:173], v35 offset:32768\n" \
"v_mfma_f32_16x16x32_bf16 v[168:171], a[244:247], a[220:223], v[168:171]\n" \
"ds_read_b64_tr_b16 a[174:175], v35 offset:33280\n" \
"v_mfma_f32_16x16x32_bf16 v[164:167], a[176:179], a[156:159], v[164:167]\n" \
"v_mfma_f32_16x16x32_bf16 v[164:167], a[240:243], a[220:223], v[164:167]\n" \
"ds_read_b128 a[156:159], v28 offset:47104\n" \
"v_mfma_f32_16x16x32_bf16 v[84:87], a[176:179], a[136:139], v[84:87]\n" \
"ds_read_b128 a[220:223], v29 offset:47104\n" \
"v_mfma_f32_16x16x32_bf16 v[84:87], a[240:243], a[200:203], v[84:87]\n" \
"s_add_u32 m0, s31, 0x0\n" \
"s_mov_b32 s84, 0x400000\n" \
"v_mfma_f32_16x16x32_bf16 v[88:91], a[180:183], a[136:139], v[88:91]\n" \
"buffer_load_dwordx4 v26, s[68:71], s84 offen offset:0 lds\n" \
"v_mfma_f32_16x16x32_bf16 v[88:91], a[244:247], a[200:203], v[88:91]\n" \
"s_addk_i32 s84, 0x7c00\n" \
"s_nop 0\n" \
"v_mfma_f32_16x16x32_bf16 v[92:95], a[184:187], a[136:139], v[92:95]\n" \
"buffer_load_dwordx4 v26, s[68:71], s84 offen offset:1024 lds\n" \
"v_mfma_f32_16x16x32_bf16 v[92:95], a[248:251], a[200:203], v[92:95]\n" \
"ds_read_b64_tr_b16 a[236:237], v35 offset:36864\n" \
"v_mfma_f32_16x16x32_bf16 v[96:99], a[188:191], a[136:139], v[96:99]\n" \
"ds_read_b64_tr_b16 a[238:239], v35 offset:37376\n" \
"v_mfma_f32_16x16x32_bf16 v[96:99], a[252:255], a[200:203], v[96:99]\n" \
"ds_read_b128 a[136:139], v28 offset:36864\n" \
"v_mfma_f32_16x16x32_bf16 v[112:115], a[188:191], a[140:143], v[112:115]\n" \
"ds_read_b128 a[200:203], v29 offset:36864\n" \
"v_mfma_f32_16x16x32_bf16 v[112:115], a[252:255], a[204:207], v[112:115]\n" \
"s_addk_i32 s84, 0x7c00\n" \
"v_mfma_f32_16x16x32_bf16 v[108:111], a[184:187], a[140:143], v[108:111]\n" \
"buffer_load_dwordx4 v27, s[68:71], s84 offen offset:2048 lds\n" \
"v_mfma_f32_16x16x32_bf16 v[108:111], a[248:251], a[204:207], v[108:111]\n" \
"s_addk_i32 s84, 0x7c00\n" \
"v_mfma_f32_16x16x32_bf16 v[104:107], a[180:183], a[140:143], v[104:107]\n" \
"buffer_load_dwordx4 v27, s[68:71], s84 offen offset:3072 lds\n" \
"v_mfma_f32_16x16x32_bf16 v[104:107], a[244:247], a[204:207], v[104:107]\n" \
"ds_read_b64_tr_b16 a[168:169], v34 offset:32768\n" \
"v_mfma_f32_16x16x32_bf16 v[100:103], a[176:179], a[140:143], v[100:103]\n" \
"ds_read_b64_tr_b16 a[170:171], v34 offset:33280\n" \
"v_mfma_f32_16x16x32_bf16 v[100:103], a[240:243], a[204:207], v[100:103]\n" \
"ds_read_b128 a[140:143], v28 offset:38912\n" \
"v_mfma_f32_16x16x32_bf16 v[52:55], a[176:179], a[128:131], v[52:55]\n" \
"ds_read_b128 a[204:207], v29 offset:38912\n" \
"v_mfma_f32_16x16x32_bf16 v[52:55], a[240:243], a[192:195], v[52:55]\n" \
"s_add_u32 m0, s30, 0x1000\n" \
"s_mov_b32 s84, 0x9400\n" \
"v_mfma_f32_16x16x32_bf16 v[56:59], a[180:183], a[128:131], v[56:59]\n" \
"buffer_load_dwordx4 v24, s[76:79], s84 offen offset:0 lds\n" \
"v_mfma_f32_16x16x32_bf16 v[56:59], a[244:247], a[192:195], v[56:59]\n" \
"s_addk_i32 s84, 0x2000\n" \
"s_nop 0\n" \
"v_mfma_f32_16x16x32_bf16 v[60:63], a[184:187], a[128:131], v[60:63]\n" \
"buffer_load_dwordx4 v25, s[76:79], s84 offen offset:1024 lds\n" \
"v_mfma_f32_16x16x32_bf16 v[60:63], a[248:251], a[192:195], v[60:63]\n" \
"ds_read_b64_tr_b16 a[232:233], v34 offset:36864\n" \
"v_mfma_f32_16x16x32_bf16 v[64:67], a[188:191], a[128:131], v[64:67]\n" \
"ds_read_b64_tr_b16 a[234:235], v34 offset:37376\n" \
"v_mfma_f32_16x16x32_bf16 v[64:67], a[252:255], a[192:195], v[64:67]\n" \
"ds_read_b128 a[128:131], v28 offset:32768\n" \
"v_mfma_f32_16x16x32_bf16 v[80:83], a[188:191], a[132:135], v[80:83]\n" \
"ds_read_b128 a[192:195], v29 offset:32768\n" \
"v_mfma_f32_16x16x32_bf16 v[80:83], a[252:255], a[196:199], v[80:83]\n" \
"v_mfma_f32_16x16x32_bf16 v[76:79], a[184:187], a[132:135], v[76:79]\n" \
"v_mfma_f32_16x16x32_bf16 v[76:79], a[248:251], a[196:199], v[76:79]\n" \
"v_mfma_f32_16x16x32_bf16 v[72:75], a[180:183], a[132:135], v[72:75]\n" \
"v_mfma_f32_16x16x32_bf16 v[72:75], a[244:247], a[196:199], v[72:75]\n" \
"v_mfma_f32_16x16x32_bf16 v[68:71], a[176:179], a[132:135], v[68:71]\n" \
"v_mfma_f32_16x16x32_bf16 v[68:71], a[240:243], a[196:199], v[68:71]\n" \
"ds_read_b128 a[132:135], v28 offset:34816\n" \
"ds_read_b128 a[196:199], v29 offset:34816\n" \
"s_waitcnt lgkmcnt(15)\n" \
"v_mfma_f32_16x16x32_bf16 a[0:3], a[160:163], a[144:147], a[0:3]\n" \
"s_addk_i32 s84, 0x2000\n" \
"v_mfma_f32_16x16x32_bf16 a[0:3], a[224:227], a[208:211], a[0:3]\n" \
"buffer_load_dwordx4 v24, s[76:79], s84 offen offset:2048 lds\n" \
"v_mfma_f32_16x16x32_bf16 a[4:7], a[164:167], a[144:147], a[4:7]\n" \
"ds_read_b64_tr_b16 a[176:177], v36 offset:32768\n" \
"v_mfma_f32_16x16x32_bf16 a[4:7], a[228:231], a[208:211], a[4:7]\n" \
"s_addk_i32 s84, 0x2000\n" \
"v_mfma_f32_16x16x32_bf16 a[12:15], a[172:175], a[144:147], a[12:15]\n" \
"buffer_load_dwordx4 v25, s[76:79], s84 offen offset:3072 lds\n" \
"s_waitcnt lgkmcnt(13)\n" \
"v_mfma_f32_16x16x32_bf16 a[12:15], a[236:239], a[208:211], a[12:15]\n" \
"ds_read_b64_tr_b16 a[178:179], v36 offset:33792\n" \
"s_waitcnt lgkmcnt(10)\n" \
"v_mfma_f32_16x16x32_bf16 a[8:11], a[144:147], a[168:171], a[8:11]\n" \
"ds_read_b64_tr_b16 a[240:241], v36 offset:40960\n" \
"s_waitcnt lgkmcnt(7)\n" \
"v_mfma_f32_16x16x32_bf16 a[8:11], a[208:211], a[232:235], a[8:11]\n" \
"ds_read_b64_tr_b16 a[242:243], v36 offset:41984\n" \
"v_mfma_f32_16x16x32_bf16 a[24:27], a[148:151], a[168:171], a[24:27]\n" \
"ds_read_b64_tr_b16 a[180:181], v37 offset:32768\n" \
"v_mfma_f32_16x16x32_bf16 a[24:27], a[212:215], a[232:235], a[24:27]\n" \
"ds_read_b64_tr_b16 a[182:183], v37 offset:33792\n" \
"v_mfma_f32_16x16x32_bf16 a[28:31], a[172:175], a[148:151], a[28:31]\n" \
"ds_read_b64_tr_b16 a[244:245], v37 offset:40960\n" \
"v_mfma_f32_16x16x32_bf16 a[28:31], a[236:239], a[212:215], a[28:31]\n" \
"ds_read_b64_tr_b16 a[246:247], v37 offset:41984\n" \
"v_mfma_f32_16x16x32_bf16 a[20:23], a[164:167], a[148:151], a[20:23]\n" \
"v_mfma_f32_16x16x32_bf16 a[20:23], a[228:231], a[212:215], a[20:23]\n" \
"s_add_u32 s88, s42, 0x2000\n" \
"buffer_store_dwordx4 v[204:207], v20, s[80:83], s88 offen offset:1024 sc1\n" \
"v_mfma_f32_16x16x32_bf16 a[16:19], a[160:163], a[148:151], a[16:19]\n" \
"ds_read_b64_tr_b16 a[184:185], v38 offset:32768\n" \
"v_mfma_f32_16x16x32_bf16 a[16:19], a[224:227], a[212:215], a[16:19]\n" \
"ds_read_b64_tr_b16 a[186:187], v38 offset:33792\n" \
"v_mfma_f32_16x16x32_bf16 a[32:35], a[160:163], a[152:155], a[32:35]\n" \
"ds_read_b64_tr_b16 a[248:249], v38 offset:40960\n" \
"v_mfma_f32_16x16x32_bf16 a[32:35], a[224:227], a[216:219], a[32:35]\n" \
"ds_read_b64_tr_b16 a[250:251], v38 offset:41984\n" \
"v_mfma_f32_16x16x32_bf16 a[36:39], a[164:167], a[152:155], a[36:39]\n" \
"v_lshlrev_b32_e32 v45, 3, v20\n" \
"v_add_u32_e32 v45, s30, v45\n" \
"v_mfma_f32_16x16x32_bf16 a[36:39], a[228:231], a[216:219], a[36:39]\n" \
"s_add_u32 s89, s42, 0x2000\n" \
"buffer_store_dwordx4 v[208:211], v20, s[80:83], s89 offen offset:2048 sc1\n" \
"s_getpc_b64 s[86:87]\n" \
"L_ct1_%=:\n" \
"s_add_u32 s86, s86, L_ap0-L_ct1_%=\n" \
"s_addc_u32 s87, s87, 0\n" \
"s_add_u32 m0, s40, 0x2000\n" \
"v_mfma_f32_16x16x32_bf16 a[44:47], a[172:175], a[152:155], a[44:47]\n" \
"global_load_lds_dword v45, s[86:87]\n" \
"v_mfma_f32_16x16x32_bf16 a[44:47], a[236:239], a[216:219], a[44:47]\n" \
"ds_read_b64_tr_b16 a[188:189], v39 offset:32768\n" \
"v_mfma_f32_16x16x32_bf16 a[40:43], a[152:155], a[168:171], a[40:43]\n" \
"ds_read_b64_tr_b16 a[190:191], v39 offset:33792\n" \
"v_mfma_f32_16x16x32_bf16 a[40:43], a[216:219], a[232:235], a[40:43]\n" \
"ds_read_b64_tr_b16 a[252:253], v39 offset:40960\n" \
"v_mfma_f32_16x16x32_bf16 a[56:59], a[156:159], a[168:171], a[56:59]\n" \
"ds_read_b64_tr_b16 a[254:255], v39 offset:41984\n" \
"v_mfma_f32_16x16x32_bf16 a[56:59], a[220:223], a[232:235], a[56:59]\n" \
"v_mfma_f32_16x16x32_bf16 a[60:63], a[172:175], a[156:159], a[60:63]\n" \
"s_add_u32 s88, s42, 0x2000\n" \
"buffer_store_dwordx4 v[212:215], v20, s[80:83], s88 offen offset:3072 sc1\n" \
"v_mfma_f32_16x16x32_bf16 a[60:63], a[236:239], a[220:223], a[60:63]\n" \
"v_mfma_f32_16x16x32_bf16 a[52:55], a[164:167], a[156:159], a[52:55]\n" \
"v_mfma_f32_16x16x32_bf16 a[52:55], a[228:231], a[220:223], a[52:55]\n" \
"v_mfma_f32_16x16x32_bf16 a[48:51], a[160:163], a[156:159], a[48:51]\n" \
"v_mfma_f32_16x16x32_bf16 a[48:51], a[224:227], a[220:223], a[48:51]\n" \
"v_mfma_f32_16x16x32_bf16 a[64:67], a[164:167], a[140:143], a[64:67]\n" \
"v_mfma_f32_16x16x32_bf16 a[64:67], a[228:231], a[204:207], a[64:67]\n" \
"v_mfma_f32_16x16x32_bf16 a[80:83], a[172:175], a[140:143], a[80:83]\n" \
"v_mfma_f32_16x16x32_bf16 a[80:83], a[236:239], a[204:207], a[80:83]\n" \
"v_mfma_f32_16x16x32_bf16 a[68:71], a[140:143], a[168:171], a[68:71]\n" \
"v_mfma_f32_16x16x32_bf16 a[68:71], a[204:207], a[232:235], a[68:71]\n" \
"s_waitcnt vmcnt(4) lgkmcnt(0)\n" \
"s_barrier\n" \
"v_mfma_f32_16x16x32_bf16 v[116:119], a[176:179], a[144:147], v[116:119]\n" \
"ds_read_b64_tr_b16 a[160:161], v32 offset:0\n" \
"v_accvgpr_read_b32 v184, a0\n" \
"v_mfma_f32_16x16x32_bf16 v[116:119], a[240:243], a[208:211], v[116:119]\n" \
"v_accvgpr_read_b32 v185, a1\n" \
"v_accvgpr_read_b32 v186, a2\n" \
"v_mfma_f32_16x16x32_bf16 v[120:123], a[180:183], a[144:147], v[120:123]\n" \
"v_accvgpr_read_b32 v187, a3\n" \
"v_accvgpr_read_b32 v188, a4\n" \
"v_mfma_f32_16x16x32_bf16 v[120:123], a[244:247], a[208:211], v[120:123]\n" \
"ds_read_b64_tr_b16 a[162:163], v32 offset:512\n" \
"v_accvgpr_read_b32 v189, a5\n" \
"v_mfma_f32_16x16x32_bf16 v[124:127], a[184:187], a[144:147], v[124:127]\n" \
"v_accvgpr_read_b32 v190, a6\n" \
"v_accvgpr_read_b32 v191, a7\n" \
"v_mfma_f32_16x16x32_bf16 v[124:127], a[248:251], a[208:211], v[124:127]\n" \
"v_accvgpr_read_b32 v196, a12\n" \
"v_accvgpr_read_b32 v197, a13\n" \
"v_mfma_f32_16x16x32_bf16 v[128:131], a[188:191], a[144:147], v[128:131]\n" \
"ds_read_b64_tr_b16 a[224:225], v32 offset:4096\n" \
"v_accvgpr_read_b32 v198, a14\n" \
"v_mfma_f32_16x16x32_bf16 v[128:131], a[252:255], a[208:211], v[128:131]\n" \
"ds_read_b128 a[144:147], v28 offset:8192\n" \
"v_accvgpr_read_b32 v199, a15\n" \
"v_mfma_f32_16x16x32_bf16 v[144:147], a[188:191], a[148:151], v[144:147]\n" \
"ds_read_b128 a[208:211], v29 offset:8192\n" \
"v_accvgpr_read_b32 v192, a8\n" \
"v_mfma_f32_16x16x32_bf16 v[144:147], a[252:255], a[212:215], v[144:147]\n" \
"ds_read_b64_tr_b16 a[226:227], v32 offset:4608\n" \
"v_accvgpr_read_b32 v193, a9\n" \
"v_mfma_f32_16x16x32_bf16 v[140:143], a[184:187], a[148:151], v[140:143]\n" \
"v_accvgpr_read_b32 v194, a10\n" \
"v_accvgpr_read_b32 v195, a11\n" \
"v_mfma_f32_16x16x32_bf16 v[140:143], a[248:251], a[212:215], v[140:143]\n" \
"v_accvgpr_read_b32 v208, a24\n" \
"v_accvgpr_read_b32 v209, a25\n" \
"v_mfma_f32_16x16x32_bf16 v[136:139], a[180:183], a[148:151], v[136:139]\n" \
"v_accvgpr_read_b32 v210, a26\n" \
"v_accvgpr_read_b32 v211, a27\n" \
"v_mfma_f32_16x16x32_bf16 v[136:139], a[244:247], a[212:215], v[136:139]\n" \
"ds_read_b64_tr_b16 a[164:165], v33 offset:0\n" \
"v_accvgpr_read_b32 v212, a28\n" \
"v_mfma_f32_16x16x32_bf16 v[132:135], a[176:179], a[148:151], v[132:135]\n" \
"v_accvgpr_read_b32 v213, a29\n" \
"v_accvgpr_read_b32 v214, a30\n" \
"v_mfma_f32_16x16x32_bf16 v[132:135], a[240:243], a[212:215], v[132:135]\n" \
"ds_read_b128 a[148:151], v28 offset:10240\n" \
"v_accvgpr_read_b32 v215, a31\n" \
"v_mfma_f32_16x16x32_bf16 v[148:151], a[176:179], a[152:155], v[148:151]\n" \
"ds_read_b128 a[212:215], v29 offset:10240\n" \
"v_accvgpr_read_b32 v204, a20\n" \
"v_mfma_f32_16x16x32_bf16 v[148:151], a[240:243], a[216:219], v[148:151]\n" \
"ds_read_b64_tr_b16 a[166:167], v33 offset:512\n" \
"v_accvgpr_read_b32 v205, a21\n" \
"v_mfma_f32_16x16x32_bf16 v[152:155], a[180:183], a[152:155], v[152:155]\n" \
"v_accvgpr_read_b32 v206, a22\n" \
"v_accvgpr_read_b32 v207, a23\n" \
"v_mfma_f32_16x16x32_bf16 v[152:155], a[244:247], a[216:219], v[152:155]\n" \
"ds_read_b64_tr_b16 a[228:229], v33 offset:4096\n" \
"v_accvgpr_read_b32 v200, a16\n" \
"v_mfma_f32_16x16x32_bf16 v[156:159], a[184:187], a[152:155], v[156:159]\n" \
"v_accvgpr_read_b32 v201, a17\n" \
"v_accvgpr_read_b32 v202, a18\n" \
"v_mfma_f32_16x16x32_bf16 v[156:159], a[248:251], a[216:219], v[156:159]\n" \
"v_accvgpr_read_b32 v203, a19\n" \
"v_accvgpr_read_b32 v216, a32\n" \
"v_mfma_f32_16x16x32_bf16 v[160:163], a[188:191], a[152:155], v[160:163]\n" \
"ds_read_b64_tr_b16 a[230:231], v33 offset:4608\n" \
"v_accvgpr_read_b32 v217, a33\n" \
"v_mfma_f32_16x16x32_bf16 v[160:163], a[252:255], a[216:219], v[160:163]\n" \
"ds_read_b128 a[152:155], v28 offset:12288\n" \
"v_accvgpr_read_b32 v218, a34\n" \
"v_mfma_f32_16x16x32_bf16 v[176:179], a[188:191], a[156:159], v[176:179]\n" \
"ds_read_b128 a[216:219], v29 offset:12288\n" \
"v_accvgpr_read_b32 v219, a35\n" \
"v_mfma_f32_16x16x32_bf16 v[176:179], a[252:255], a[220:223], v[176:179]\n" \
"v_accvgpr_read_b32 v220, a36\n" \
"v_accvgpr_read_b32 v221, a37\n" \
"v_mfma_f32_16x16x32_bf16 v[172:175], a[184:187], a[156:159], v[172:175]\n" \
"ds_read_b64_tr_b16 a[172:173], v35 offset:0\n" \
"v_accvgpr_read_b32 v222, a38\n" \
"v_mfma_f32_16x16x32_bf16 v[172:175], a[248:251], a[220:223], v[172:175]\n" \
"v_accvgpr_read_b32 v223, a39\n" \
"v_accvgpr_read_b32 v228, a44\n" \
"v_mfma_f32_16x16x32_bf16 v[168:171], a[180:183], a[156:159], v[168:171]\n" \
"v_accvgpr_read_b32 v229, a45\n" \
"v_accvgpr_read_b32 v230, a46\n" \
"v_mfma_f32_16x16x32_bf16 v[168:171], a[244:247], a[220:223], v[168:171]\n" \
"ds_read_b64_tr_b16 a[174:175], v35 offset:512\n" \
"v_accvgpr_read_b32 v231, a47\n" \
"v_mfma_f32_16x16x32_bf16 v[164:167], a[176:179], a[156:159], v[164:167]\n" \
"v_accvgpr_read_b32 v224, a40\n" \
"v_accvgpr_read_b32 v225, a41\n" \
"v_mfma_f32_16x16x32_bf16 v[164:167], a[240:243], a[220:223], v[164:167]\n" \
"ds_read_b128 a[156:159], v28 offset:14336\n" \
"v_accvgpr_read_b32 v226, a42\n" \
"v_mfma_f32_16x16x32_bf16 v[84:87], a[176:179], a[136:139], v[84:87]\n" \
"ds_read_b128 a[220:223], v29 offset:14336\n" \
"v_accvgpr_read_b32 v227, a43\n" \
"v_mfma_f32_16x16x32_bf16 v[84:87], a[240:243], a[200:203], v[84:87]\n" \
"ds_read_b64_tr_b16 a[236:237], v35 offset:4096\n" \
"v_accvgpr_read_b32 v240, a56\n" \
"v_mfma_f32_16x16x32_bf16 v[88:91], a[180:183], a[136:139], v[88:91]\n" \
"v_accvgpr_read_b32 v241, a57\n" \
"v_accvgpr_read_b32 v242, a58\n" \
"v_mfma_f32_16x16x32_bf16 v[88:91], a[244:247], a[200:203], v[88:91]\n" \
"ds_read_b64_tr_b16 a[238:239], v35 offset:4608\n" \
"v_accvgpr_read_b32 v243, a59\n" \
"v_mfma_f32_16x16x32_bf16 v[92:95], a[184:187], a[136:139], v[92:95]\n" \
"v_accvgpr_read_b32 v244, a60\n" \
"v_accvgpr_read_b32 v245, a61\n" \
"v_mfma_f32_16x16x32_bf16 v[92:95], a[248:251], a[200:203], v[92:95]\n" \
"v_accvgpr_read_b32 v246, a62\n" \
"v_accvgpr_read_b32 v247, a63\n" \
"v_mfma_f32_16x16x32_bf16 v[96:99], a[188:191], a[136:139], v[96:99]\n" \
"v_accvgpr_read_b32 v236, a52\n" \
"v_accvgpr_read_b32 v237, a53\n" \
"v_mfma_f32_16x16x32_bf16 v[96:99], a[252:255], a[200:203], v[96:99]\n" \
"ds_read_b128 a[136:139], v28 offset:4096\n" \
"v_accvgpr_read_b32 v238, a54\n" \
"v_mfma_f32_16x16x32_bf16 v[112:115], a[188:191], a[140:143], v[112:115]\n" \
"ds_read_b128 a[200:203], v29 offset:4096\n" \
"v_accvgpr_read_b32 v239, a55\n" \
"v_mfma_f32_16x16x32_bf16 v[112:115], a[252:255], a[204:207], v[112:115]\n" \
"ds_read_b64_tr_b16 a[168:169], v34 offset:0\n" \
"v_accvgpr_read_b32 v232, a48\n" \
"v_mfma_f32_16x16x32_bf16 v[108:111], a[184:187], a[140:143], v[108:111]\n" \
"ds_read_b64_tr_b16 a[170:171], v34 offset:512\n" \
"v_accvgpr_read_b32 v233, a49\n" \
"v_mfma_f32_16x16x32_bf16 v[108:111], a[248:251], a[204:207], v[108:111]\n" \
"v_accvgpr_read_b32 v234, a50\n" \
"v_accvgpr_read_b32 v235, a51\n" \
"v_mfma_f32_16x16x32_bf16 v[104:107], a[180:183], a[140:143], v[104:107]\n" \
"v_accvgpr_read_b32 v180, a64\n" \
"v_accvgpr_read_b32 v181, a65\n" \
"v_mfma_f32_16x16x32_bf16 v[104:107], a[244:247], a[204:207], v[104:107]\n" \
"ds_read_b64_tr_b16 a[232:233], v34 offset:4096\n" \
"v_accvgpr_read_b32 v182, a66\n" \
"v_mfma_f32_16x16x32_bf16 v[100:103], a[176:179], a[140:143], v[100:103]\n" \
"v_accvgpr_read_b32 v183, a67\n" \
"v_accvgpr_read_b32 v48, a80\n" \
"v_mfma_f32_16x16x32_bf16 v[100:103], a[240:243], a[204:207], v[100:103]\n" \
"ds_read_b128 a[140:143], v28 offset:6144\n" \
"v_accvgpr_read_b32 v49, a81\n" \
"v_mfma_f32_16x16x32_bf16 v[52:55], a[176:179], a[128:131], v[52:55]\n" \
"ds_read_b128 a[204:207], v29 offset:6144\n" \
"v_accvgpr_read_b32 v50, a82\n" \
"v_mfma_f32_16x16x32_bf16 v[52:55], a[240:243], a[192:195], v[52:55]\n" \
"ds_read_b64_tr_b16 a[234:235], v34 offset:4608\n" \
"v_accvgpr_read_b32 v51, a83\n" \
"v_mfma_f32_16x16x32_bf16 v[56:59], a[180:183], a[128:131], v[56:59]\n" \
"v_accvgpr_read_b32 v252, a68\n" \
"v_accvgpr_read_b32 v253, a69\n" \
"v_mfma_f32_16x16x32_bf16 v[56:59], a[244:247], a[192:195], v[56:59]\n" \
"v_accvgpr_read_b32 v254, a70\n" \
"v_accvgpr_read_b32 v255, a71\n" \
"v_mfma_f32_16x16x32_bf16 v[60:63], a[184:187], a[128:131], v[60:63]\n" \
"v_lshrrev_b32_e64 v24, 9, v20\n" \
"v_and_b32_e32 v26, 0x1ff, v20\n" \
"v_mfma_f32_16x16x32_bf16 v[60:63], a[248:251], a[192:195], v[60:63]\n" \
"v_lshl_add_u32 v24, v24, 3, v26\n" \
"v_lshrrev_b32_e64 v25, 8, v20\n" \
"v_mfma_f32_16x16x32_bf16 v[64:67], a[188:191], a[128:131], v[64:67]\n" \
"v_mul_u32_u24_e32 v25, 0x24700, v25\n" \
"v_add_u32_e64 v25, v25, v20\n" \
"v_mfma_f32_16x16x32_bf16 v[64:67], a[252:255], a[192:195], v[64:67]\n" \
"ds_read_b128 a[128:131], v28 offset:0\n" \
"v_lshrrev_b32_e64 v26, 2, v20\n" \
"v_mfma_f32_16x16x32_bf16 v[80:83], a[188:191], a[132:135], v[80:83]\n" \
"ds_read_b128 a[192:195], v29 offset:0\n" \
"v_add_u32_e64 v26, s45, v26\n" \
"v_mfma_f32_16x16x32_bf16 v[80:83], a[252:255], a[196:199], v[80:83]\n" \
"v_add_u32_e32 v27, s46, v20\n" \
"v_add_u32_e32 v21, s43, v27\n" \
"v_mfma_f32_16x16x32_bf16 v[76:79], a[184:187], a[132:135], v[76:79]\n" \
"v_mfma_f32_16x16x32_bf16 v[76:79], a[248:251], a[196:199], v[76:79]\n" \
"v_mfma_f32_16x16x32_bf16 v[72:75], a[180:183], a[132:135], v[72:75]\n" \
"v_mfma_f32_16x16x32_bf16 v[72:75], a[244:247], a[196:199], v[72:75]\n" \
"v_mfma_f32_16x16x32_bf16 v[68:71], a[176:179], a[132:135], v[68:71]\n" \
"v_mfma_f32_16x16x32_bf16 v[68:71], a[240:243], a[196:199], v[68:71]\n" \
"ds_read_b128 a[132:135], v28 offset:2048\n" \
"ds_read_b128 a[196:199], v29 offset:2048\n" \
"s_waitcnt lgkmcnt(15)\n" \
"s_nop 0\n" \
"v_mfma_f32_16x16x32_bf16 v[184:187], a[160:163], a[144:147], v[184:187]\n" \
"v_bfe_u32 v44, v20, 2, 5\n" \
"ds_read_b64_tr_b16 a[176:177], v36 offset:0\n" \
"v_mfma_f32_16x16x32_bf16 v[184:187], a[224:227], a[208:211], v[184:187]\n" \
"buffer_load_dword v44, v44, s[80:83], s38 offen offset:0 sc1\n" \
"v_mfma_f32_16x16x32_bf16 v[188:191], a[164:167], a[144:147], v[188:191]\n" \
"ds_read_b64_tr_b16 a[178:179], v36 offset:1024\n" \
"v_mfma_f32_16x16x32_bf16 v[188:191], a[228:231], a[208:211], v[188:191]\n" \
"ds_read_b64_tr_b16 a[240:241], v36 offset:8192\n" \
"v_mfma_f32_16x16x32_bf16 v[196:199], a[172:175], a[144:147], v[196:199]\n" \
"ds_read_b64_tr_b16 a[242:243], v36 offset:9216\n" \
"s_waitcnt lgkmcnt(15)\n" \
"v_mfma_f32_16x16x32_bf16 v[196:199], a[236:239], a[208:211], v[196:199]\n" \
"s_waitcnt lgkmcnt(12)\n" \
"v_mfma_f32_16x16x32_bf16 v[192:195], a[144:147], a[168:171], v[192:195]\n" \
"ds_read_b64_tr_b16 a[180:181], v37 offset:0\n" \
"s_waitcnt lgkmcnt(9)\n" \
"s_nop 0\n" \
"v_mfma_f32_16x16x32_bf16 v[192:195], a[208:211], a[232:235], v[192:195]\n" \
"v_mfma_f32_16x16x32_bf16 v[208:211], a[148:151], a[168:171], v[208:211]\n" \
"ds_read_b64_tr_b16 a[182:183], v37 offset:1024\n" \
"v_mfma_f32_16x16x32_bf16 v[208:211], a[212:215], a[232:235], v[208:211]\n" \
"ds_read_b64_tr_b16 a[244:245], v37 offset:8192\n" \
"v_mfma_f32_16x16x32_bf16 v[212:215], a[172:175], a[148:151], v[212:215]\n" \
"v_mfma_f32_16x16x32_bf16 v[212:215], a[236:239], a[212:215], v[212:215]\n" \
"ds_read_b64_tr_b16 a[246:247], v37 offset:9216\n" \
"v_cvt_pk_bf16_f32 v188, v188, v189\n" \
"v_mfma_f32_16x16x32_bf16 v[204:207], a[164:167], a[148:151], v[204:207]\n" \
"v_cvt_pk_bf16_f32 v189, v190, v191\n" \
"v_cvt_pk_bf16_f32 v190, v198, v199\n" \
"v_mfma_f32_16x16x32_bf16 v[204:207], a[228:231], a[212:215], v[204:207]\n" \
"ds_read_b64_tr_b16 a[184:185], v38 offset:0\n" \
"v_mov_b32_e32 v191, 0x3f80\n" \
"v_mfma_f32_16x16x32_bf16 v[200:203], a[160:163], a[148:151], v[200:203]\n" \
"ds_read_b64_tr_b16 a[186:187], v38 offset:1024\n" \
"v_cvt_pk_bf16_f32 v184, v184, v185\n" \
"v_mfma_f32_16x16x32_bf16 v[200:203], a[224:227], a[212:215], v[200:203]\n" \
"v_cvt_pk_bf16_f32 v185, v186, v187\n" \
"v_cvt_pk_bf16_f32 v186, v196, v197\n" \
"v_mfma_f32_16x16x32_bf16 v[216:219], a[160:163], a[152:155], v[216:219]\n" \
"ds_read_b64_tr_b16 a[248:249], v38 offset:8192\n" \
"v_mov_b32_e64 v187, 0\n" \
"v_mfma_f32_16x16x32_bf16 v[216:219], a[224:227], a[216:219], v[216:219]\n" \
"ds_read_b64_tr_b16 a[250:251], v38 offset:9216\n" \
"v_mfma_f32_16x16x32_bf16 v[220:223], a[164:167], a[152:155], v[220:223]\n" \
"v_mfma_f32_16x16x32_bf16 v[220:223], a[228:231], a[216:219], v[220:223]\n" \
"ds_read_b64_tr_b16 a[188:189], v39 offset:0\n" \
"v_cvt_pk_bf16_f32 v204, v204, v205\n" \
"v_mfma_f32_16x16x32_bf16 v[228:231], a[172:175], a[152:155], v[228:231]\n" \
"v_cvt_pk_bf16_f32 v205, v206, v207\n" \
"v_cvt_pk_bf16_f32 v206, v214, v215\n" \
"v_mfma_f32_16x16x32_bf16 v[228:231], a[236:239], a[216:219], v[228:231]\n" \
"ds_read_b64_tr_b16 a[190:191], v39 offset:1024\n" \
"v_mov_b32_e32 v207, 0x3f80\n" \
"v_mfma_f32_16x16x32_bf16 v[224:227], a[152:155], a[168:171], v[224:227]\n" \
"ds_read_b64_tr_b16 a[252:253], v39 offset:8192\n" \
"v_cvt_pk_bf16_f32 v200, v200, v201\n" \
"v_mfma_f32_16x16x32_bf16 v[224:227], a[216:219], a[232:235], v[224:227]\n" \
"v_cvt_pk_bf16_f32 v201, v202, v203\n" \
"v_cvt_pk_bf16_f32 v202, v212, v213\n" \
"v_mfma_f32_16x16x32_bf16 v[240:243], a[156:159], a[168:171], v[240:243]\n" \
"ds_read_b64_tr_b16 a[254:255], v39 offset:9216\n" \
"v_mov_b32_e64 v203, 0\n" \
"v_mfma_f32_16x16x32_bf16 v[240:243], a[220:223], a[232:235], v[240:243]\n" \
"v_mfma_f32_16x16x32_bf16 v[244:247], a[172:175], a[156:159], v[244:247]\n" \
"v_mfma_f32_16x16x32_bf16 v[244:247], a[236:239], a[220:223], v[244:247]\n" \
"v_cvt_pk_bf16_f32 v220, v220, v221\n" \
"v_cvt_pk_bf16_f32 v221, v222, v223\n" \
"v_mfma_f32_16x16x32_bf16 v[236:239], a[164:167], a[156:159], v[236:239]\n" \
"v_cvt_pk_bf16_f32 v222, v230, v231\n" \
"v_mov_b32_e32 v223, 0x3f80\n" \
"v_mfma_f32_16x16x32_bf16 v[236:239], a[228:231], a[220:223], v[236:239]\n" \
"v_cvt_pk_bf16_f32 v216, v216, v217\n" \
"v_cvt_pk_bf16_f32 v217, v218, v219\n" \
"v_mfma_f32_16x16x32_bf16 v[232:235], a[160:163], a[156:159], v[232:235]\n" \
"v_cvt_pk_bf16_f32 v218, v228, v229\n" \
"v_mov_b32_e64 v219, 0\n" \
"v_mfma_f32_16x16x32_bf16 v[232:235], a[224:227], a[220:223], v[232:235]\n" \
"v_mfma_f32_16x16x32_bf16 v[180:183], a[164:167], a[140:143], v[180:183]\n" \
"v_mfma_f32_16x16x32_bf16 v[180:183], a[228:231], a[204:207], v[180:183]\n" \
"v_mfma_f32_16x16x32_bf16 v[48:51], a[172:175], a[140:143], v[48:51]\n" \
"v_mfma_f32_16x16x32_bf16 v[48:51], a[236:239], a[204:207], v[48:51]\n" \
"v_cvt_pk_bf16_f32 v236, v236, v237\n" \
"v_cvt_pk_bf16_f32 v237, v238, v239\n" \
"v_mfma_f32_16x16x32_bf16 v[252:255], a[140:143], a[168:171], v[252:255]\n" \
"v_cvt_pk_bf16_f32 v238, v246, v247\n" \
"v_mov_b32_e32 v239, 0x3f80\n" \
"v_mfma_f32_16x16x32_bf16 v[252:255], a[204:207], a[232:235], v[252:255]\n" \
"v_cvt_pk_bf16_f32 v232, v232, v233\n" \
"v_cvt_pk_bf16_f32 v233, v234, v235\n" \
"s_waitcnt vmcnt(0) lgkmcnt(0)\n" \
"s_barrier\n" \
"v_mov_b32_e32 v45, s62\n" \
"s_mov_b64 exec, 1\n" \
"buffer_store_dword v45, off, s[80:83], s37 offset:32 sc1\n" \
"s_mov_b64 exec, -1\n" \
"v_cmp_ne_u32_e32 vcc, s62, v44\n" \
"s_nop 4\n" \
"s_cbranch_vccnz L_poll161_%=\n" \
"L_pollr161_%=:\n" \
"v_mfma_f32_16x16x32_bf16 v[116:119], a[176:179], a[144:147], v[116:119]\n" \
"s_mov_b32 s73, s50\n" \
"s_sub_u32 m0, s31, 0x10000\n" \
"v_mfma_f32_16x16x32_bf16 v[116:119], a[240:243], a[208:211], v[116:119]\n" \
"buffer_load_dwordx4 v20, s[80:83], s73 offen offset:0 sc1 lds\n" \
"v_mfma_f32_16x16x32_bf16 v[120:123], a[180:183], a[144:147], v[120:123]\n" \
"s_add_u32 s74, s50, 0x4000\n" \
"s_add_u32 s75, s50, 0x8000\n" \
"v_mfma_f32_16x16x32_bf16 v[120:123], a[244:247], a[208:211], v[120:123]\n" \
"s_mov_b32 s70, 0x3e0293ee\n" \
"s_mov_b32 s76, s52\n" \
"v_mfma_f32_16x16x32_bf16 v[124:127], a[184:187], a[144:147], v[124:127]\n" \
"v_mul_f32_e32 v116, s70, v116\n" \
"v_mul_f32_e32 v117, s70, v117\n" \
"v_mfma_f32_16x16x32_bf16 v[124:127], a[248:251], a[208:211], v[124:127]\n" \
"buffer_load_dwordx4 v20, s[80:83], s73 offen offset:1024 sc1 lds\n" \
"v_mfma_f32_16x16x32_bf16 v[128:131], a[188:191], a[144:147], v[128:131]\n" \
"v_mul_f32_e32 v118, s70, v118\n" \
"v_mul_f32_e32 v119, s70, v119\n" \
"v_mfma_f32_16x16x32_bf16 v[128:131], a[252:255], a[208:211], v[128:131]\n" \
"v_cvt_pk_bf16_f32 v116, v116, v117\n" \
"v_mul_f32_e64 v120, s70, v120\n" \
"v_mfma_f32_16x16x32_bf16 v[144:147], a[188:191], a[148:151], v[144:147]\n" \
"v_mul_f32_e32 v124, s70, v124\n" \
"v_mul_f32_e32 v125, s70, v125\n" \
"v_mfma_f32_16x16x32_bf16 v[144:147], a[252:255], a[212:215], v[144:147]\n" \
"buffer_load_dwordx4 v20, s[80:83], s73 offen offset:2048 sc1 lds\n" \
"v_mfma_f32_16x16x32_bf16 v[140:143], a[184:187], a[148:151], v[140:143]\n" \
"v_mul_f32_e32 v126, s70, v126\n" \
"v_mul_f32_e32 v127, s70, v127\n" \
"v_mfma_f32_16x16x32_bf16 v[140:143], a[248:251], a[212:215], v[140:143]\n" \
"v_cvt_pk_bf16_f32 v124, v124, v125\n" \
"v_mul_f32_e64 v121, s70, v121\n" \
"v_mfma_f32_16x16x32_bf16 v[136:139], a[180:183], a[148:151], v[136:139]\n" \
"v_mul_f32_e32 v122, s70, v122\n" \
"v_mul_f32_e32 v123, s70, v123\n" \
"v_mfma_f32_16x16x32_bf16 v[136:139], a[244:247], a[212:215], v[136:139]\n" \
"buffer_load_dwordx4 v20, s[80:83], s73 offen offset:3072 sc1 lds\n" \
"v_mfma_f32_16x16x32_bf16 v[132:135], a[176:179], a[148:151], v[132:135]\n" \
"s_sub_u32 m0, s31, s51\n" \
"v_cvt_pk_bf16_f32 v117, v118, v119\n" \
"v_mfma_f32_16x16x32_bf16 v[132:135], a[240:243], a[212:215], v[132:135]\n" \
"v_mul_f32_e32 v128, s70, v128\n" \
"v_mul_f32_e64 v129, s70, v129\n" \
"v_mfma_f32_16x16x32_bf16 v[148:151], a[176:179], a[152:155], v[148:151]\n" \
"v_mul_f32_e32 v130, s70, v130\n" \
"v_mul_f32_e32 v131, s70, v131\n" \
"v_mfma_f32_16x16x32_bf16 v[148:151], a[240:243], a[216:219], v[148:151]\n" \
"buffer_load_dwordx4 v20, s[80:83], s74 offen offset:0 sc1 lds\n" \
"v_mfma_f32_16x16x32_bf16 v[152:155], a[180:183], a[152:155], v[152:155]\n" \
"v_cvt_pk_bf16_f32 v125, v126, v127\n" \
"v_cvt_pk_bf16_f32 v118, v120, v121\n" \
"v_mfma_f32_16x16x32_bf16 v[152:155], a[244:247], a[216:219], v[152:155]\n" \
"v_cvt_pk_bf16_f32 v119, v122, v123\n" \
"v_cvt_pk_bf16_f32 v126, v128, v129\n" \
"v_mfma_f32_16x16x32_bf16 v[156:159], a[184:187], a[152:155], v[156:159]\n" \
"v_cvt_pk_bf16_f32 v127, v130, v131\n" \
"v_mul_f32_e64 v132, s70, v132\n" \
"v_mfma_f32_16x16x32_bf16 v[156:159], a[248:251], a[216:219], v[156:159]\n" \
"buffer_load_dwordx4 v20, s[80:83], s74 offen offset:1024 sc1 lds\n" \
"v_mfma_f32_16x16x32_bf16 v[160:163], a[188:191], a[152:155], v[160:163]\n" \
"ds_write_b128 v21, v[116:119] offset:16384\n" \
"v_mfma_f32_16x16x32_bf16 v[160:163], a[252:255], a[216:219], v[160:163]\n" \
"v_mul_f32_e32 v133, s70, v133\n" \
"v_mul_f32_e32 v140, s70, v140\n" \
"v_mfma_f32_16x16x32_bf16 v[176:179], a[188:191], a[156:159], v[176:179]\n" \
"v_mul_f32_e32 v141, s70, v141\n" \
"v_mul_f32_e32 v134, s70, v134\n" \
"v_mfma_f32_16x16x32_bf16 v[176:179], a[252:255], a[220:223], v[176:179]\n" \
"buffer_load_dwordx4 v20, s[80:83], s74 offen offset:2048 sc1 lds\n" \
"v_mfma_f32_16x16x32_bf16 v[172:175], a[184:187], a[156:159], v[172:175]\n" \
"ds_write_b128 v21, v[124:127] offset:17408\n" \
"v_mfma_f32_16x16x32_bf16 v[172:175], a[248:251], a[220:223], v[172:175]\n" \
"v_mul_f32_e64 v135, s70, v135\n" \
"v_cvt_pk_bf16_f32 v132, v132, v133\n" \
"v_mfma_f32_16x16x32_bf16 v[168:171], a[180:183], a[156:159], v[168:171]\n" \
"v_mul_f32_e32 v142, s70, v142\n" \
"v_mul_f32_e32 v143, s70, v143\n" \
"v_mfma_f32_16x16x32_bf16 v[168:171], a[244:247], a[220:223], v[168:171]\n" \
"buffer_load_dwordx4 v20, s[80:83], s74 offen offset:3072 sc1 lds\n" \
"v_mfma_f32_16x16x32_bf16 v[164:167], a[176:179], a[156:159], v[164:167]\n" \
"s_sub_u32 m0, s31, 0x4000\n" \
"v_cvt_pk_bf16_f32 v140, v140, v141\n" \
"v_mfma_f32_16x16x32_bf16 v[164:167], a[240:243], a[220:223], v[164:167]\n" \
"v_mul_f32_e32 v136, s70, v136\n" \
"v_mul_f32_e32 v137, s70, v137\n" \
"v_mfma_f32_16x16x32_bf16 v[84:87], a[176:179], a[136:139], v[84:87]\n" \
"v_mul_f32_e32 v138, s70, v138\n" \
"v_mul_f32_e32 v139, s70, v139\n" \
"v_mfma_f32_16x16x32_bf16 v[84:87], a[240:243], a[200:203], v[84:87]\n" \
"buffer_load_dwordx4 v20, s[80:83], s75 offen offset:0 sc1 lds\n" \
"v_mfma_f32_16x16x32_bf16 v[88:91], a[180:183], a[136:139], v[88:91]\n" \
"v_cvt_pk_bf16_f32 v133, v134, v135\n" \
"v_mul_f32_e64 v144, s70, v144\n" \
"v_mfma_f32_16x16x32_bf16 v[88:91], a[244:247], a[200:203], v[88:91]\n" \
"v_mul_f32_e32 v145, s70, v145\n" \
"v_mul_f32_e32 v146, s70, v146\n" \
"v_mfma_f32_16x16x32_bf16 v[92:95], a[184:187], a[136:139], v[92:95]\n" \
"v_mul_f32_e64 v147, s70, v147\n" \
"v_cvt_pk_bf16_f32 v141, v142, v143\n" \
"v_mfma_f32_16x16x32_bf16 v[92:95], a[248:251], a[200:203], v[92:95]\n" \
"buffer_load_dwordx4 v20, s[80:83], s75 offen offset:1024 sc1 lds\n" \
"v_mfma_f32_16x16x32_bf16 v[96:99], a[188:191], a[136:139], v[96:99]\n" \
"v_cvt_pk_bf16_f32 v134, v136, v137\n" \
"v_cvt_pk_bf16_f32 v135, v138, v139\n" \
"v_mfma_f32_16x16x32_bf16 v[96:99], a[252:255], a[200:203], v[96:99]\n" \
"v_cvt_pk_bf16_f32 v142, v144, v145\n" \
"v_cvt_pk_bf16_f32 v143, v146, v147\n" \
"v_mfma_f32_16x16x32_bf16 v[112:115], a[188:191], a[140:143], v[112:115]\n" \
"ds_write_b128 v21, v[132:135] offset:20480\n" \
"v_mfma_f32_16x16x32_bf16 v[112:115], a[252:255], a[204:207], v[112:115]\n" \
"buffer_load_dwordx4 v20, s[80:83], s75 offen offset:2048 sc1 lds\n" \
"v_mfma_f32_16x16x32_bf16 v[108:111], a[184:187], a[140:143], v[108:111]\n" \
"v_cvt_pk_bf16_f32 v180, v180, v181\n" \
".fill (L_pb + 77536 - .) / 4, 4, 0xBF800000\n" \
"L_pp19:\n" \
"s_cbranch_execz L_pret\n" \
"v_cvt_pk_bf16_f32 v181, v182, v183\n" \
"v_mfma_f32_16x16x32_bf16 v[108:111], a[248:251], a[204:207], v[108:111]\n" \
"ds_write_b128 v21, v[140:143] offset:21504\n" \
"v_mfma_f32_16x16x32_bf16 v[104:107], a[180:183], a[140:143], v[104:107]\n" \
"v_cvt_pk_bf16_f32 v234, v244, v245\n" \
"v_mov_b32_e32 v235, 0\n" \
"v_mfma_f32_16x16x32_bf16 v[104:107], a[244:247], a[204:207], v[104:107]\n" \
"buffer_load_dwordx4 v20, s[80:83], s75 offen offset:3072 sc1 lds\n" \
"v_mfma_f32_16x16x32_bf16 v[100:103], a[176:179], a[140:143], v[100:103]\n" \
"s_mov_b32 m0, s53\n" \
"s_nop 0\n" \
"v_cvt_pk_bf16_f32 v182, v50, v51\n" \
"v_mfma_f32_16x16x32_bf16 v[100:103], a[240:243], a[204:207], v[100:103]\n" \
"v_mov_b32_e32 v183, 0x3f80\n" \
"v_cvt_pk_bf16_f32 v118, v192, v193\n" \
"v_mfma_f32_16x16x32_bf16 v[52:55], a[176:179], a[128:131], v[52:55]\n" \
"v_cvt_pk_bf16_f32 v119, v194, v195\n" \
"v_cvt_pk_bf16_f32 v120, v208, v209\n" \
"v_mfma_f32_16x16x32_bf16 v[52:55], a[240:243], a[192:195], v[52:55]\n" \
"buffer_load_dwordx4 v20, s[80:83], s76 offen offset:0 sc1 lds\n" \
"v_mfma_f32_16x16x32_bf16 v[56:59], a[180:183], a[128:131], v[56:59]\n" \
"v_cvt_pk_bf16_f32 v121, v210, v211\n" \
"v_cvt_pk_bf16_f32 v122, v224, v225\n" \
"v_mfma_f32_16x16x32_bf16 v[56:59], a[244:247], a[192:195], v[56:59]\n" \
"v_cvt_pk_bf16_f32 v123, v226, v227\n" \
"v_cvt_pk_bf16_f32 v124, v240, v241\n" \
"v_cvt_pk_bf16_f32 v125, v242, v243\n" \
"v_mfma_f32_16x16x32_bf16 v[60:63], a[184:187], a[128:131], v[60:63]\n" \
"v_mfma_f32_16x16x32_bf16 v[192:195], v[188:191], v[184:187], 0\n" \
"buffer_load_dwordx4 v20, s[80:83], s76 offen offset:1024 sc1 lds\n" \
"v_mfma_f32_16x16x32_bf16 v[32:35], v[180:183], v[184:187], 0\n" \
"v_mfma_f32_16x16x32_bf16 v[208:211], v[204:207], v[200:203], 0\n" \
"v_mfma_f32_16x16x32_bf16 v[36:39], v[188:191], v[200:203], 0\n" \
"v_mfma_f32_16x16x32_bf16 v[224:227], v[220:223], v[216:219], 0\n" \
"v_mfma_f32_16x16x32_bf16 v[248:251], v[204:207], v[216:219], 0\n" \
"v_mfma_f32_16x16x32_bf16 v[240:243], v[236:239], v[232:235], 0\n" \
"v_mfma_f32_16x16x32_bf16 v[128:131], v[220:223], v[232:235], 0\n" \
"s_mov_b32 s64, 0xaaaaaaaa\n" \
"s_mov_b32 s65, 0xaaaaaaaa\n" \
"v_mfma_f32_16x16x32_bf16 v[60:63], a[248:251], a[192:195], v[60:63]\n" \
"s_mov_b32 s66, 0xfff0ffff\n" \
"s_mov_b32 s67, 0xf000ff00\n" \
"v_mfma_f32_16x16x32_bf16 v[64:67], a[188:191], a[128:131], v[64:67]\n" \
"s_mov_b32 s68, 0xffc0fffc\n" \
"s_mov_b32 s69, 0xc000fc00\n" \
"v_cndmask_b32_e64 v192, v192, v193, s[64:65]\n" \
"v_cndmask_b32_e64 v32, v32, v33, s[64:65]\n" \
"v_cndmask_b32_e64 v194, v194, v195, s[64:65]\n" \
"v_cndmask_b32_e64 v34, v34, v35, s[64:65]\n" \
"v_cndmask_b32_e64 v208, v208, v209, s[64:65]\n" \
"v_cndmask_b32_e64 v36, v36, v37, s[64:65]\n" \
"v_cndmask_b32_e64 v210, v210, v211, s[64:65]\n" \
"v_cndmask_b32_e64 v38, v38, v39, s[64:65]\n" \
"v_cndmask_b32_e64 v224, v224, v225, s[64:65]\n" \
"v_cndmask_b32_e64 v248, v248, v249, s[64:65]\n" \
"v_cndmask_b32_e64 v226, v226, v227, s[64:65]\n" \
"v_cndmask_b32_e64 v250, v250, v251, s[64:65]\n" \
"v_cndmask_b32_e64 v240, v240, v241, s[64:65]\n" \
"v_cndmask_b32_e64 v128, v128, v129, s[64:65]\n" \
"v_cndmask_b32_e64 v242, v242, v243, s[64:65]\n" \
"v_cndmask_b32_e64 v130, v130, v131, s[64:65]\n" \
"v_cndmask_b32_e64 v192, v32, v192, s[66:67]\n" \
"v_cndmask_b32_e64 v194, v34, v194, s[68:69]\n" \
"v_cndmask_b32_e64 v208, v36, v208, s[66:67]\n" \
"v_cndmask_b32_e64 v210, v38, v210, s[68:69]\n" \
"v_cndmask_b32_e64 v224, v248, v224, s[66:67]\n" \
"v_cndmask_b32_e64 v226, v250, v226, s[68:69]\n" \
"v_cndmask_b32_e64 v240, v128, v240, s[66:67]\n" \
"v_cndmask_b32_e64 v242, v130, v242, s[68:69]\n" \
"v_mfma_f32_16x16x32_bf16 v[64:67], a[252:255], a[192:195], v[64:67]\n" \
"v_max_f32_e32 v193, v192, v194\n" \
"v_max_f32_e32 v209, v208, v210\n" \
"v_max_f32_e32 v225, v224, v226\n" \
"v_max_f32_e32 v241, v240, v242\n" \
"v_mul_f32_e32 v148, s70, v148\n" \
"v_permlane32_swap_b32_e32 v193, v225\n" \
"v_permlane32_swap_b32_e32 v209, v241\n" \
"v_mul_f32_e32 v149, s70, v149\n" \
"v_mul_f32_e32 v156, s70, v156\n" \
"v_mul_f32_e32 v157, s70, v157\n" \
"v_permlane32_swap_b32_e32 v196, v228\n" \
"v_permlane32_swap_b32_e32 v212, v244\n" \
"v_max_f32_e32 v193, v193, v225\n" \
"v_max_f32_e32 v209, v209, v241\n" \
"v_mul_f32_e32 v150, s70, v150\n" \
"v_mul_f32_e32 v151, s70, v151\n" \
"v_cvt_pk_bf16_f32 v148, v148, v149\n" \
"v_mul_f32_e32 v158, s70, v158\n" \
"v_mul_f32_e32 v159, s70, v159\n" \
"v_cvt_pk_bf16_f32 v156, v156, v157\n" \
"v_permlane16_swap_b32_e32 v228, v244\n" \
"v_permlane16_swap_b32_e32 v193, v209\n" \
"v_mfma_f32_16x16x32_bf16 v[80:83], a[188:191], a[132:135], v[80:83]\n" \
"s_mov_b32 s72, 0x3eae1a93\n" \
"v_mul_f32_e32 v152, s70, v152\n" \
"v_mul_f32_e32 v153, s70, v153\n" \
"v_mul_f32_e32 v154, s70, v154\n" \
"v_mul_f32_e32 v155, s70, v155\n" \
"v_cvt_pk_bf16_f32 v149, v150, v151\n" \
"v_mul_f32_e32 v160, s70, v160\n" \
"v_mul_f32_e32 v161, s70, v161\n" \
"v_mul_f32_e32 v162, s70, v162\n" \
"v_mul_f32_e32 v163, s70, v163\n" \
"v_cvt_pk_bf16_f32 v157, v158, v159\n" \
"v_mul_f32_e32 v45, 0xbfb8aa3b, v228\n" \
"v_mfma_f32_16x16x16_bf16 v[48:51], v[182:183], v[46:47], 0\n" \
"v_mfma_f32_16x16x16_bf16 v[196:199], v[190:191], v[46:47], 0\n" \
"v_mfma_f32_16x16x16_bf16 v[212:215], v[206:207], v[46:47], 0\n" \
"v_max_f32_e64 v193, v193, v209\n" \
"v_cvt_pk_bf16_f32 v150, v152, v153\n" \
"v_cvt_pk_bf16_f32 v151, v154, v155\n" \
"v_cvt_pk_bf16_f32 v158, v160, v161\n" \
"v_cvt_pk_bf16_f32 v159, v162, v163\n" \
"v_mfma_f32_16x16x16_bf16 v[228:231], v[222:223], v[46:47], 0\n" \
"v_mul_f32_e32 v193, s72, v193\n" \
"v_mov_b32_e32 v209, v193\n" \
"ds_write_b128 v21, v[148:151] offset:24576\n" \
"v_mfma_f32_16x16x16_bf16 v[244:247], v[238:239], v[46:47], 0\n" \
"v_permlane16_swap_b32_e32 v193, v209\n" \
"s_mov_b32 s71, 0x3e0293ee\n" \
"v_mfma_f32_16x16x32_bf16 v[80:83], a[252:255], a[196:199], v[80:83]\n" \
"v_mov_b32_e32 v225, v193\n" \
"v_mov_b32_e32 v241, v209\n" \
"v_mul_f32_e32 v164, s70, v164\n" \
"v_mul_f32_e64 v165, s70, v165\n" \
"ds_write_b128 v21, v[156:159] offset:25600\n" \
"v_mul_f32_e32 v172, s70, v172\n" \
"v_mul_f32_e32 v173, s70, v173\n" \
"v_pk_mul_f32 v[84:85], v[84:85], s[70:71]\n" \
"v_pk_mul_f32 v[92:93], v[92:93], s[70:71]\n" \
"v_pk_mul_f32 v[100:101], v[100:101], s[70:71]\n" \
"v_pk_mul_f32 v[108:109], v[108:109], s[70:71]\n" \
"v_pk_mul_f32 v[52:53], v[52:53], s[70:71]\n" \
"v_pk_mul_f32 v[60:61], v[60:61], s[70:71]\n" \
"v_permlane32_swap_b32_e32 v193, v225\n" \
"v_permlane32_swap_b32_e32 v209, v241\n" \
"v_mul_f32_e32 v166, s70, v166\n" \
"v_mul_f32_e32 v167, s70, v167\n" \
"v_cvt_pk_bf16_f32 v164, v164, v165\n" \
"v_mul_f32_e32 v174, s70, v174\n" \
"v_mul_f32_e32 v175, s70, v175\n" \
"v_cvt_pk_bf16_f32 v172, v172, v173\n" \
"v_pk_mul_f32 v[86:87], v[86:87], s[70:71]\n" \
"v_cvt_pk_bf16_f32 v84, v84, v85\n" \
"v_pk_mul_f32 v[94:95], v[94:95], s[70:71]\n" \
"v_cvt_pk_bf16_f32 v92, v92, v93\n" \
"v_pk_mul_f32 v[102:103], v[102:103], s[70:71]\n" \
"v_cvt_pk_bf16_f32 v100, v100, v101\n" \
"v_pk_mul_f32 v[110:111], v[110:111], s[70:71]\n" \
"v_cvt_pk_bf16_f32 v108, v108, v109\n" \
"v_pk_mul_f32 v[54:55], v[54:55], s[70:71]\n" \
"v_cvt_pk_bf16_f32 v52, v52, v53\n" \
"v_pk_mul_f32 v[62:63], v[62:63], s[70:71]\n" \
"v_cvt_pk_bf16_f32 v60, v60, v61\n" \
"v_fma_f32 v192, v192, s72, -v193\n" \
"v_fma_f32 v194, v194, s72, -v193\n" \
"v_fma_f32 v208, v208, s72, -v209\n" \
"v_fma_f32 v210, v210, s72, -v209\n" \
"v_mul_f32_e32 v168, s70, v168\n" \
"v_mul_f32_e32 v169, s70, v169\n" \
"v_mul_f32_e32 v170, s70, v170\n" \
"v_mul_f32_e32 v171, s70, v171\n" \
"v_cvt_pk_bf16_f32 v165, v166, v167\n" \
"v_mul_f32_e32 v176, s70, v176\n" \
"v_mul_f32_e32 v177, s70, v177\n" \
"v_mul_f32_e32 v178, s70, v178\n" \
"v_mul_f32_e32 v179, s70, v179\n" \
"v_cvt_pk_bf16_f32 v173, v174, v175\n" \
"v_pk_mul_f32 v[88:89], v[88:89], s[70:71]\n" \
"v_pk_mul_f32 v[90:91], v[90:91], s[70:71]\n" \
"v_cvt_pk_bf16_f32 v85, v86, v87\n" \
"v_pk_mul_f32 v[96:97], v[96:97], s[70:71]\n" \
"v_pk_mul_f32 v[98:99], v[98:99], s[70:71]\n" \
"v_cvt_pk_bf16_f32 v93, v94, v95\n" \
"v_pk_mul_f32 v[104:105], v[104:105], s[70:71]\n" \
"v_pk_mul_f32 v[106:107], v[106:107], s[70:71]\n" \
"v_cvt_pk_bf16_f32 v101, v102, v103\n" \
"v_pk_mul_f32 v[112:113], v[112:113], s[70:71]\n" \
"v_pk_mul_f32 v[114:115], v[114:115], s[70:71]\n" \
"v_cvt_pk_bf16_f32 v109, v110, v111\n" \
"v_pk_mul_f32 v[56:57], v[56:57], s[70:71]\n" \
"v_pk_mul_f32 v[58:59], v[58:59], s[70:71]\n" \
"v_cvt_pk_bf16_f32 v53, v54, v55\n" \
"v_pk_mul_f32 v[64:65], v[64:65], s[70:71]\n" \
"v_pk_mul_f32 v[66:67], v[66:67], s[70:71]\n" \
"v_cvt_pk_bf16_f32 v61, v62, v63\n" \
"v_pk_mul_f32 v[80:81], v[80:81], s[70:71]\n" \
"v_pk_mul_f32 v[82:83], v[82:83], s[70:71]\n" \
"v_exp_f32_e32 v192, v192\n" \
"v_exp_f32_e32 v194, v194\n" \
"v_exp_f32_e32 v208, v208\n" \
"v_exp_f32_e32 v210, v210\n" \
"v_fma_f32 v224, v224, s72, -v225\n" \
"v_fma_f32 v226, v226, s72, -v225\n" \
"v_mfma_f32_16x16x32_bf16 v[76:79], a[184:187], a[132:135], v[76:79]\n" \
"v_cvt_pk_bf16_f32 v166, v168, v169\n" \
"v_cvt_pk_bf16_f32 v167, v170, v171\n" \
"v_cvt_pk_bf16_f32 v174, v176, v177\n" \
"v_cvt_pk_bf16_f32 v175, v178, v179\n" \
"v_cvt_pk_bf16_f32 v86, v88, v89\n" \
"v_cvt_pk_bf16_f32 v87, v90, v91\n" \
"v_cvt_pk_bf16_f32 v94, v96, v97\n" \
"v_cvt_pk_bf16_f32 v95, v98, v99\n" \
"v_cvt_pk_bf16_f32 v102, v104, v105\n" \
"v_cvt_pk_bf16_f32 v103, v106, v107\n" \
"v_cvt_pk_bf16_f32 v110, v112, v113\n" \
"v_cvt_pk_bf16_f32 v111, v114, v115\n" \
"v_cvt_pk_bf16_f32 v54, v56, v57\n" \
"v_cvt_pk_bf16_f32 v55, v58, v59\n" \
"v_cvt_pk_bf16_f32 v62, v64, v65\n" \
"v_cvt_pk_bf16_f32 v63, v66, v67\n" \
"v_exp_f32_e32 v224, v224\n" \
"v_exp_f32_e32 v226, v226\n" \
"v_fma_f32 v240, v240, s72, -v241\n" \
"v_fma_f32 v242, v242, s72, -v241\n" \
"ds_write_b128 v21, v[164:167] offset:28672\n" \
"v_cvt_pk_bf16_f32 v192, v192, v194\n" \
"v_cvt_pk_bf16_f32 v208, v208, v210\n" \
"v_exp_f32_e32 v240, v240\n" \
"v_exp_f32_e32 v242, v242\n" \
"v_cvt_pk_bf16_f32 v132, v48, v49\n" \
"v_cvt_pk_bf16_f32 v133, v50, v51\n" \
"v_cvt_pk_bf16_f32 v134, v196, v197\n" \
"v_cvt_pk_bf16_f32 v135, v198, v199\n" \
"ds_write_b128 v21, v[172:175] offset:29696\n" \
"v_cvt_pk_bf16_f32 v136, v212, v213\n" \
"v_cvt_pk_bf16_f32 v137, v214, v215\n" \
"v_perm_b32 v184, v192, v192, v40\n" \
"v_perm_b32 v185, v192, v192, v41\n" \
"v_perm_b32 v186, v192, v192, v42\n" \
"v_perm_b32 v187, v192, v192, v43\n" \
"v_perm_b32 v200, v208, v208, v40\n" \
"v_perm_b32 v201, v208, v208, v41\n" \
"v_perm_b32 v202, v208, v208, v42\n" \
"v_perm_b32 v203, v208, v208, v43\n" \
"ds_write_b128 v27, v[84:87] offset:8192\n" \
"v_cvt_pk_bf16_f32 v224, v224, v226\n" \
"v_cvt_pk_bf16_f32 v138, v228, v229\n" \
"v_cvt_pk_bf16_f32 v139, v230, v231\n" \
"v_mfma_f32_16x16x32_bf16 v[32:35], v[132:135], v[184:187], 0\n" \
"v_mfma_f32_16x16x32_bf16 v[36:39], v[134:137], v[200:203], 0\n" \
"v_perm_b32 v216, v224, v224, v40\n" \
"v_perm_b32 v217, v224, v224, v41\n" \
"ds_write_b128 v27, v[92:95] offset:9216\n" \
"v_perm_b32 v218, v224, v224, v42\n" \
"v_perm_b32 v219, v224, v224, v43\n" \
"v_cvt_pk_bf16_f32 v240, v240, v242\n" \
"v_mfma_f32_16x16x32_bf16 v[76:79], a[248:251], a[196:199], v[76:79]\n" \
"v_cvt_pk_bf16_f32 v140, v244, v245\n" \
"v_cvt_pk_bf16_f32 v141, v246, v247\n" \
"v_mfma_f32_16x16x32_bf16 v[248:251], v[136:139], v[216:219], 0\n" \
"v_perm_b32 v232, v240, v240, v40\n" \
"v_perm_b32 v233, v240, v240, v41\n" \
"ds_write_b128 v27, v[100:103] offset:12288\n" \
"v_perm_b32 v234, v240, v240, v42\n" \
"v_perm_b32 v235, v240, v240, v43\n" \
"v_pk_mul_f32 v[76:77], v[76:77], s[70:71]\n" \
"v_pk_mul_f32 v[78:79], v[78:79], s[70:71]\n" \
"v_cvt_pk_bf16_f32 v76, v76, v77\n" \
"v_cvt_pk_bf16_f32 v77, v78, v79\n" \
"v_cvt_pk_bf16_f32 v78, v80, v81\n" \
"v_cvt_pk_bf16_f32 v79, v82, v83\n" \
"v_mfma_f32_16x16x32_bf16 v[128:131], v[138:141], v[232:235], 0\n" \
"ds_write_b128 v27, v[108:111] offset:13312\n" \
"v_mfma_f32_16x16x32_bf16 v[72:75], a[180:183], a[132:135], v[72:75]\n" \
"v_cvt_pk_bf16_f32 v116, v252, v253\n" \
"v_cvt_pk_bf16_f32 v117, v254, v255\n" \
"v_mfma_f32_16x16x32_bf16 v[72:75], a[244:247], a[196:199], v[72:75]\n" \
"v_exp_f32_e64 v45, v45\n" \
"v_mfma_f32_16x16x32_bf16 v[192:195], v[116:119], v[184:187], 0\n" \
"ds_write_b128 v27, v[52:55] offset:0\n" \
"v_mfma_f32_16x16x32_bf16 v[208:211], v[118:121], v[200:203], 0\n" \
"v_mfma_f32_16x16x32_bf16 v[224:227], v[120:123], v[216:219], 0\n" \
"v_mfma_f32_16x16x32_bf16 v[240:243], v[122:125], v[232:235], 0\n" \
"ds_write_b128 v27, v[60:63] offset:1024\n" \
"v_mov_b32_dpp v34, v38 quad_perm:[0,1,2,3] row_mask:0x2 bank_mask:0xf\n" \
"v_pk_mul_f32 v[72:73], v[72:73], s[70:71]\n" \
"v_pk_mul_f32 v[74:75], v[74:75], s[70:71]\n" \
"v_mov_b32_dpp v34, v250 quad_perm:[0,1,2,3] row_mask:0x4 bank_mask:0xf\n" \
"v_add_f32_e64 v45, 1.0, v45\n" \
"v_mfma_f32_16x16x32_bf16 v[68:71], a[176:179], a[132:135], v[68:71]\n" \
"v_mov_b32_dpp v34, v130 quad_perm:[0,1,2,3] row_mask:0x8 bank_mask:0xf\n" \
"v_mov_b32_dpp v32, v36 quad_perm:[0,1,2,3] row_mask:0x2 bank_mask:0xf\n" \
"v_rcp_f32_e64 v45, v45\n" \
"ds_write_b128 v27, v[76:79] offset:5120\n" \
"v_mov_b32_dpp v33, v37 quad_perm:[0,1,2,3] row_mask:0x2 bank_mask:0xf\n" \
"v_rcp_f32_e64 v34, v34\n" \
"v_mov_b32_dpp v32, v248 quad_perm:[0,1,2,3] row_mask:0x4 bank_mask:0xf\n" \
"v_mov_b32_dpp v33, v249 quad_perm:[0,1,2,3] row_mask:0x4 bank_mask:0xf\n" \
"v_mul_f32_e64 v34, v45, v34\n" \
"v_mov_b32_dpp v32, v128 quad_perm:[0,1,2,3] row_mask:0x8 bank_mask:0xf\n" \
"v_mov_b32_dpp v33, v129 quad_perm:[0,1,2,3] row_mask:0x8 bank_mask:0xf\n" \
"v_mul_f32_e32 v32, v34, v32\n" \
"v_mul_f32_e32 v33, v34, v33\n" \
"v_mov_b32_e64 v38, v34\n" \
"v_mfma_f32_16x16x32_bf16 v[68:71], a[240:243], a[196:199], v[68:71]\n" \
"v_sub_f32_e32 v44, 1.0, v45\n" \
"v_permlane16_swap_b32_e32 v34, v38\n" \
"v_mov_b32_e32 v250, v34\n" \
"v_mov_b32_e32 v130, v38\n" \
"v_cvt_pk_bf16_f32 v32, v32, v33\n" \
"v_permlane32_swap_b32_e32 v34, v250\n" \
"v_permlane32_swap_b32_e32 v38, v130\n" \
"v_mul_f32_e64 v192, v34, v192\n" \
"v_pk_mul_f32 v[68:69], v[68:69], s[70:71]\n" \
"v_pk_mul_f32 v[70:71], v[70:71], s[70:71]\n" \
"v_cvt_pk_bf16_f32 v68, v68, v69\n" \
"v_cvt_pk_bf16_f32 v69, v70, v71\n" \
"v_cvt_pk_bf16_f32 v70, v72, v73\n" \
"v_cvt_pk_bf16_f32 v71, v74, v75\n" \
"ds_write_b128 v27, v[68:71] offset:4096\n" \
"v_mul_f32_e32 v193, v34, v193\n" \
"v_mul_f32_e32 v208, v38, v208\n" \
"v_mul_f32_e32 v209, v38, v209\n" \
"v_mul_f32_e32 v224, v250, v224\n" \
"v_mul_f32_e32 v225, v250, v225\n" \
"v_mul_f32_e32 v240, v130, v240\n" \
"v_mul_f32_e32 v241, v130, v241\n" \
"v_mul_f32_e32 v194, v34, v194\n" \
"v_mul_f32_e64 v195, v34, v195\n" \
"v_cvt_pk_bf16_f32 v192, v192, v193\n" \
"v_mul_f32_e32 v210, v38, v210\n" \
"v_mul_f32_e32 v211, v38, v211\n" \
"v_cvt_pk_bf16_f32 v208, v208, v209\n" \
"v_mul_f32_e32 v226, v250, v226\n" \
"v_mul_f32_e32 v227, v250, v227\n" \
"v_cvt_pk_bf16_f32 v224, v224, v225\n" \
"v_mul_f32_e32 v242, v130, v242\n" \
"v_mul_f32_e32 v243, v130, v243\n" \
"v_cvt_pk_bf16_f32 v240, v240, v241\n" \
"v_cvt_pk_bf16_f32 v193, v194, v195\n" \
"s_add_u32 s84, s48, 0x0\n" \
"s_nop 0\n" \
"v_cvt_pk_bf16_f32 v209, v210, v211\n" \
"s_add_u32 s85, s48, 0x24800\n" \
"v_cvt_pk_bf16_f32 v225, v226, v227\n" \
"s_add_u32 s86, s48, 0x49000\n" \
"v_cvt_pk_bf16_f32 v241, v242, v243\n" \
"s_add_u32 s87, s48, 0x6d800\n" \
"ds_write_b32 v26, v44\n" \
"buffer_store_dword v32, v25, s[80:83], s49 offen sc1\n" \
"buffer_store_dwordx2 v[192:193], v24, s[80:83], s84 offen sc1\n" \
"buffer_store_dwordx2 v[208:209], v24, s[80:83], s85 offen sc1\n" \
"buffer_store_dwordx2 v[224:225], v24, s[80:83], s86 offen sc1\n" \
"buffer_store_dwordx2 v[240:241], v24, s[80:83], s87 offen sc1\n" \
"s_branch L_end_%=\n" \
"L_poll61_%=:\n" \
"s_mov_b32 s84, 0\n" \
"L_pl61_%=:\n" \
"s_sleep 1\n" \
"v_bfe_u32 v44, v20, 2, 5\n" \
"buffer_load_dword v44, v44, s[80:83], s38 offen offset:128 sc1\n" \
"s_waitcnt vmcnt(0)\n" \
"v_cmp_ne_u32_e32 vcc, s62, v44\n" \
"s_add_u32 s84, s84, 1\n" \
"s_nop 3\n" \
"s_cbranch_vccz L_pollr61_%=\n" \
"s_cmp_gt_u32 s84, 0x400000\n" \
"s_cbranch_scc0 L_pl61_%=\n" \
"s_branch L_pollr61_%=\n" \
"L_poll161_%=:\n" \
"s_mov_b32 s84, 0\n" \
"L_pl161_%=:\n" \
"s_sleep 1\n" \
"v_bfe_u32 v44, v20, 2, 5\n" \
"buffer_load_dword v44, v44, s[80:83], s38 offen offset:0 sc1\n" \
"s_waitcnt vmcnt(0)\n" \
"v_cmp_ne_u32_e32 vcc, s62, v44\n" \
"s_add_u32 s84, s84, 1\n" \
"s_nop 3\n" \
"s_cbranch_vccz L_pollr161_%=\n" \
"s_cmp_gt_u32 s84, 0x400000\n" \
"s_cbranch_scc0 L_pl161_%=\n" \
"s_branch L_pollr161_%=\n" \
"L_end_%=:\n"
#define PROJ_VCLOB "v21","v24","v25","v28","v29","v32","v33","v34","v35","v36","v37","v38","v39","v40","v41","v42","v43","v44","v45","v46","v47","v48","v49","v50","v51","v52","v53","v54","v55","v56","v57","v58","v59","v60","v61","v62","v63","v64","v65","v66","v67","v68","v69","v70","v71","v72","v73","v74","v75","v76","v77","v78","v79","v80","v81","v82","v83","v84","v85","v86","v87","v88","v89","v90","v91","v92","v93","v94","v95","v96","v97","v98","v99","v100","v101","v102","v103","v104","v105","v106","v107","v108","v109","v110","v111","v112","v113","v114","v115","v116","v117","v118","v119","v120","v121","v122","v123","v124","v125","v126","v127","v128","v129","v130","v131","v132","v133","v134","v135","v136","v137","v138","v139","v140","v141","v142","v143","v144","v145","v146","v147","v148","v149","v150","v151","v152","v153","v154","v155","v156","v157","v158","v159","v160","v161","v162","v163","v164","v165","v166","v167","v168","v169","v170","v171","v172","v173","v174","v175","v176","v177","v178","v179","v180","v181","v182","v183","v184","v185","v186","v187","v188","v189","v190","v192","v193","v194","v195","v196","v197","v198","v199","v200","v201","v202","v203","v204","v205","v206","v207","v208","v209","v210","v211","v212","v213","v214","v215","v216","v217","v218","v219","v220","v221","v222","v223","v224","v225","v226","v227","v228","v229","v230","v231","v232","v233","v234","v235","v236","v237","v238","v239","v240","v241","v242","v243","v244","v245","v246","v247","v248","v249","v250","v251","v252","v253","v254","v255"
#define PROJ_SCLOB "s4","s5","s6","s7","s32","s33","s34","s35","s36","s37","s38","s39","s40","s41","s42","s43","s44","s45","s46","s47","s48","s49","s50","s51","s52","s53","s54","s55","s63","s64","s65","s66","s67","s68","s69","s70","s71","s72","s73","s74","s75","s76","s77","s78","s79","s80","s81","s82","s83","s84","s85","s86","s87","s88","s89","s90","s91","s92","s93","s94","s95","s96","s97","s98","s99","s100","s101"

typedef const u16* const __attribute__((address_space(4)))* kaptr;
__device__ __forceinline__ void proj_phase(kaptr& ka, char*& ws, const unsigned wid, u32x4 (&vp)[5], unsigned& vl, u32x4 (&sa)[5], u32x2 s24, u32x2 s28, unsigned tdum, u32x4& srdA, u32x2& sd, unsigned& tag
    ) {
    const unsigned b = blockIdx.x, h = (b & 7) * 4 + ((b >> 3) & 3), mt = b >> 5;
    const unsigned rb = (wid < 2u ? 15u - mt : mt) * 128u;
    const unsigned lane = threadIdx.x & 63u, wc = wid & 1u;
    u32x2 vAc, vB;
#pragma unroll
    for (int par = 0; par < 2; ++par) {
        const unsigned lr = lane >> 3, cc = (lane & 7u) ^ (4u * par + (lr >> 1)), r0 = rb + wc * 64u + lr;
        vAc[par] = r0 * (DC * 2) + cc * 16u;
        const unsigned br = lane >> 4, p16 = lane & 15u, sc = (((p16 >> 1) ^ (br | (par << 2))) << 1) | (p16 & 1u);
        vB[par] = (16u * wid + br) * (NBIG * 2) + h * (HD * 2) + sc * 16u;
    }
    vl = lane * 16u;
    u32x2 s30 = {wid * 8192u, 65536u + wid * 4096u};
    asm volatile(PROJ_ASM
                 : "={s62}"(tag), "={s[56:59]}"(srdA), "={s[60:61]}"(sd), "+{s[8:11]}"(sa[0]), "+{s[12:15]}"(sa[1]), "+{s[16:19]}"(sa[2]), "+{s[20:23]}"(sa[3]), "+{s[24:25]}"(s24), "+{s[28:29]}"(s28), "+{s[30:31]}"(s30),
                   "={v[0:3]}"(vp[0]), "={v[4:7]}"(vp[1]), "={v[8:11]}"(vp[2]), "={v[12:15]}"(vp[3]), "={v[16:19]}"(vp[4]), "+{v[22:23]}"(vAc), "+{v[26:27]}"(vB), "+{v191}"(tdum), "+{s[26:27]}"(ws), "+{s[0:1]}"(ka)
                 : "{s2}"(b), "{s3}"(wid), "{v20}"(vl)
                 : "memory", "m0", "vcc", "scc", PROJ_VCLOB, PROJ_SCLOB, ACLOB_LO, ACLOB_HI);
    sa[4] = (u32x4){s24[0], 0u, 0u, 0u};
}

namespace att {
#define DI __device__ __forceinline__
typedef __bf16 bfx8 __attribute__((ext_vector_type(8)));
DI unsigned rfl(unsigned x) { return __builtin_amdgcn_readfirstlane(x); }
DI f32x4 mfma(u32x4 a, u32x4 b, f32x4 c) { return __builtin_amdgcn_mfma_f32_16x16x32_bf16(__builtin_bit_cast(bfx8, a), __builtin_bit_cast(bfx8, b), c, 0, 0, 0); }
typedef __bf16 bfx2 __attribute__((ext_vector_type(2)));
typedef float f32x2 __attribute__((ext_vector_type(2)));
DI unsigned cvtpk2(float lo, float hi) { f32x2 v = {lo, hi}; bfx2 b = __builtin_convertvector(v, bfx2); return __builtin_bit_cast(unsigned, b); }
DI void att_exact(const u16* __restrict__ x, const u16* __restrict__ wq, char* __restrict__ ws, unsigned h, unsigned r0, float g1) {
    const unsigned lane = lane_id(), g = lane >> 4, c = lane & 15u;
    f32x4 o[8];
#pragma unroll
    for (int n = 0; n < 8; ++n) o[n] = (f32x4){0.f, 0.f, 0.f, 0.f};
#pragma unroll 1
    for (unsigned kk = 0; kk < (unsigned)DM; kk += 32u) {
        const unsigned k = kk + 8u * g;
        const u32x4 xf = *reinterpret_cast<const u32x4*>(x + (r0 + c) * DM + k);
#pragma unroll
        for (int n = 0; n < 8; ++n) {
            const u16* w = wq + k * NBIG + h * HD + n * 16 + c; u32x4 wf;
#pragma unroll
            for (int e = 0; e < 4; ++e) wf[e] = (unsigned)w[2 * e * NBIG] | ((unsigned)w[(2 * e + 1) * NBIG] << 16);
            o[n] = mfma(wf, xf, o[n]);
        }
    }
    u32x4 q[4];
#pragma unroll
    for (int p = 0; p < 4; ++p) {
        const f32x4 a = o[2 * p] * 0.12751743082f, b = o[2 * p + 1] * 0.12751743082f;
        q[p] = (u32x4){cvtpk2(a[0], a[1]), cvtpk2(a[2], a[3]), cvtpk2(b[0], b[1]), cvtpk2(b[2], b[3])};
    }
#pragma unroll
    for (int n = 0; n < 8; ++n) o[n] = (f32x4){0.f, 0.f, 0.f, 0.f};
    const __amdgpu_buffer_rsrc_t srd = __builtin_amdgcn_make_buffer_rsrc((void*)(ws + WS_K + (size_t)h * 1048576), (short)0, 1048576, 0x00020000);
    float m = -3.0e38f, l = 0.f;
#pragma unroll 1
    for (unsigned k0 = 0; k0 <= r0 + 15u; k0 += 32u) {
        const unsigned tb = (k0 >> 6) * 32768u + (k0 & 32u) * 256u;
        f32x4 s[2]; float mx = m;
#pragma unroll
        for (int kt = 0; kt < 2; ++kt) {
            s[kt] = (f32x4){0.f, 0.f, 0.f, 0.f};
#pragma unroll
            for (int p = 0; p < 4; ++p) s[kt] = mfma(__builtin_amdgcn_raw_buffer_load_b128(srd, lane * 16u + (kt * 4 + p) * 1024u, tb, 16), q[p], s[kt]);
#pragma unroll
            for (int e = 0; e < 4; ++e) { if (k0 + 16u * kt + 4u * g + e > r0 + c) s[kt][e] = -3.0e38f; mx = fmaxf(mx, s[kt][e]); }
        }
        mx = fmaxf(mx, __shfl_xor(mx, 16)); mx = fmaxf(mx, __shfl_xor(mx, 32));
        const float al = __builtin_amdgcn_exp2f(m - mx); m = mx;
        u32x4 pf;
#pragma unroll
        for (int e = 0; e < 4; ++e) pf[e] = cvtpk2(__builtin_amdgcn_exp2f(s[e >> 1][2 * (e & 1)] - m), __builtin_amdgcn_exp2f(s[e >> 1][2 * (e & 1) + 1] - m));
        l *= al;
#pragma unroll
        for (int e = 0; e < 4; ++e) l += __uint_as_float(pf[e] << 16) + __uint_as_float(pf[e] & 0xffff0000u);
#pragma unroll
        for (int n = 0; n < 8; ++n) o[n] = mfma(__builtin_amdgcn_raw_buffer_load_b128(srd, lane * 16u + 16384u + n * 1024u, tb, 16), pf, o[n] * al);
    }
    l += __shfl_xor(l, 16); l += __shfl_xor(l, 32);
    const float f = g1 / l;
    const __amdgpu_buffer_rsrc_t srdA = __builtin_amdgcn_make_buffer_rsrc((void*)(ws + WS_AF), (short)0, 128 * KS_TOT * 1024, 0x00020000);
#pragma unroll
    for (int p = 0; p < 4; ++p) {
        const f32x4 a = o[2 * p] * f, b = o[2 * p + 1] * f;
        __builtin_amdgcn_raw_buffer_store_b128((u32x4){cvtpk2(a[0], a[1]), cvtpk2(a[2], a[3]), cvtpk2(b[0], b[1]), cvtpk2(b[2], b[3])}, srdA, lane * 16u + p * 1024u, ((r0 >> 4) * KS_TOT + 18u + 4u * h) * 1024u, 16);
    }
}
}

#define ATT_ASM \
"s_branch L_ago_%=\n" \
".p2align 12\n" \
"L_ab:\n" \
".fill (L_ab + 0 - .) / 4, 4, 0xBF800000\n" \
"L_ap0:\n" \
"s_setpc_b64 s[88:89]\n" \
"L_ago_%=:\n" \
"s_mov_b32 s101, 0\n" \
"s_waitcnt vmcnt(15) lgkmcnt(0)\n" \
"s_barrier\n" \
"v_accvgpr_write_b32 a216, 0\n" \
"v_accvgpr_write_b32 a217, 0\n" \
"v_accvgpr_write_b32 a218, 0\n" \
"v_accvgpr_write_b32 a219, 0\n" \
"v_bfe_u32 v61, v20, 2, 5\n" \
"s_mov_b32 s96, s24\n" \
"v_mfma_f32_32x32x16_bf16 v[128:143], a[216:219], a[216:219], 0\n" \
"buffer_load_dword v57, v61, s[56:59], s96 offen sc1\n" \
"v_and_b32_e32 v63, 0x300, v20\n" \
"ds_read_b128 v[96:99], v63 offset:0\n" \
"v_add_u32_e32 v58, s22, v20\n" \
"ds_read_b128 a[0:3], v58 offset:0\n" \
"v_mfma_f32_32x32x16_bf16 v[144:159], a[216:219], a[216:219], 0\n" \
"ds_read_b128 a[16:19], v58 offset:8192\n" \
"v_add_u32_e32 v59, s23, v20\n" \
"ds_read_b128 a[32:35], v59 offset:0\n" \
"ds_read_b128 a[48:51], v59 offset:8192\n" \
"ds_read_b128 v[100:103], v63 offset:1024\n" \
"ds_read_b128 a[4:7], v58 offset:1024\n" \
"v_mfma_f32_32x32x16_bf16 v[160:175], a[216:219], a[216:219], 0\n" \
"ds_read_b128 a[20:23], v58 offset:9216\n" \
"ds_read_b128 a[36:39], v59 offset:1024\n" \
"ds_read_b128 a[52:55], v59 offset:9216\n" \
"s_sub_u32 s67, s12, 0xc000\n" \
"ds_read_b128 v[104:107], v63 offset:2048\n" \
"s_mov_b32 s65, s12\n" \
"v_mfma_f32_32x32x16_bf16 v[176:191], a[216:219], a[216:219], 0\n" \
"v_add_u32_e32 v55, s67, v20\n" \
"v_add_u32_e32 v52, 0xc000, v55\n" \
"s_xor_b32 m0, s65, 0x10000\n" \
"ds_read_b128 a[8:11], v58 offset:2048\n" \
"buffer_load_dwordx4 v52, s[8:11], 0 offen offset:0 sc1 lds\n" \
"v_mfma_f32_32x32x16_bf16 v[192:207], a[216:219], a[216:219], 0\n" \
"ds_read_b128 a[24:27], v58 offset:10240\n" \
"ds_read_b128 a[40:43], v59 offset:2048\n" \
"ds_read_b128 a[56:59], v59 offset:10240\n" \
"ds_read_b128 v[108:111], v63 offset:3072\n" \
"buffer_load_dwordx4 v52, s[8:11], 0 offen offset:1024 sc1 lds\n" \
"v_mfma_f32_32x32x16_bf16 v[208:223], a[216:219], a[216:219], 0\n" \
"ds_read_b128 a[12:15], v58 offset:3072\n" \
"ds_read_b128 a[28:31], v58 offset:11264\n" \
"ds_read_b128 a[44:47], v59 offset:3072\n" \
"ds_read_b128 a[60:63], v59 offset:11264\n" \
"s_waitcnt lgkmcnt(15)\n" \
"v_xor_b32_e32 v96, 0x80008000, v96\n" \
"v_mfma_f32_32x32x16_bf16 v[224:239], a[216:219], a[216:219], 0\n" \
"v_xor_b32_e32 v97, 0x80008000, v97\n" \
"v_xor_b32_e32 v98, 0x80008000, v98\n" \
"v_xor_b32_e32 v99, 0x80008000, v99\n" \
"ds_read_b128 a[160:163], v20 offset:0\n" \
"ds_read_b128 a[164:167], v20 offset:1024\n" \
"s_waitcnt lgkmcnt(15)\n" \
"v_xor_b32_e32 v100, 0x80008000, v100\n" \
"v_mfma_f32_32x32x16_bf16 v[240:255], a[216:219], a[216:219], 0\n" \
"v_xor_b32_e32 v101, 0x80008000, v101\n" \
"v_xor_b32_e32 v102, 0x80008000, v102\n" \
"v_xor_b32_e32 v103, 0x80008000, v103\n" \
"ds_read_b128 a[168:171], v20 offset:2048\n" \
"ds_read_b128 a[172:175], v20 offset:3072\n" \
"ds_read_b128 a[188:191], v20 offset:7168\n" \
"v_mfma_f32_16x16x32_bf16 a[192:195], a[216:219], a[216:219], 0\n" \
"ds_read_b128 a[184:187], v20 offset:6144\n" \
"s_waitcnt lgkmcnt(15)\n" \
"v_xor_b32_e32 v104, 0x80008000, v104\n" \
"v_mfma_f32_16x16x32_bf16 a[196:199], a[216:219], a[216:219], 0\n" \
"v_xor_b32_e32 v105, 0x80008000, v105\n" \
"v_xor_b32_e32 v106, 0x80008000, v106\n" \
"v_mfma_f32_16x16x32_bf16 v[32:35], v[96:99], a[0:3], 0\n" \
"v_xor_b32_e32 v107, 0x80008000, v107\n" \
"ds_read_b128 a[180:183], v20 offset:5120\n" \
"v_mfma_f32_16x16x32_bf16 v[36:39], v[96:99], a[16:19], 0\n" \
"ds_read_b128 a[176:179], v20 offset:4096\n" \
"s_waitcnt lgkmcnt(12)\n" \
"v_xor_b32_e32 v108, 0x80008000, v108\n" \
"v_mfma_f32_16x16x32_bf16 v[40:43], v[96:99], a[32:35], 0\n" \
"v_xor_b32_e32 v109, 0x80008000, v109\n" \
"v_xor_b32_e32 v110, 0x80008000, v110\n" \
"v_mfma_f32_16x16x32_bf16 v[44:47], v[96:99], a[48:51], 0\n" \
"v_xor_b32_e32 v111, 0x80008000, v111\n" \
"v_add_u32_e32 v60, s15, v20\n" \
"v_mfma_f32_16x16x32_bf16 v[32:35], v[100:103], a[4:7], v[32:35]\n" \
"v_add_u32_e32 v60, 0x10000, v60\n" \
"ds_read_b128 a[128:131], v20 offset:8192\n" \
"v_mfma_f32_16x16x32_bf16 v[36:39], v[100:103], a[20:23], v[36:39]\n" \
"ds_read_b128 a[144:147], v20 offset:12288\n" \
"ds_read_b128 a[132:135], v20 offset:9216\n" \
"v_mfma_f32_16x16x32_bf16 v[40:43], v[100:103], a[36:39], v[40:43]\n" \
"ds_read_b128 a[148:151], v20 offset:13312\n" \
"ds_read_b128 a[136:139], v20 offset:10240\n" \
"v_mfma_f32_16x16x32_bf16 v[44:47], v[100:103], a[52:55], v[44:47]\n" \
"ds_read_b128 a[152:155], v20 offset:14336\n" \
"ds_read_b128 a[140:143], v20 offset:11264\n" \
"v_mfma_f32_16x16x32_bf16 v[32:35], v[104:107], a[8:11], v[32:35]\n" \
"ds_read_b128 a[156:159], v20 offset:15360\n" \
"ds_read_b128 a[64:67], v60 offset:16384\n" \
"v_mfma_f32_16x16x32_bf16 v[36:39], v[104:107], a[24:27], v[36:39]\n" \
"ds_read_b128 a[68:71], v60 offset:17408\n" \
"ds_read_b128 a[72:75], v60 offset:18432\n" \
"v_mfma_f32_16x16x32_bf16 v[40:43], v[104:107], a[40:43], v[40:43]\n" \
"ds_read_b128 a[76:79], v60 offset:19456\n" \
"ds_read_b128 a[80:83], v60 offset:24576\n" \
"v_mfma_f32_16x16x32_bf16 v[44:47], v[104:107], a[56:59], v[44:47]\n" \
"ds_read_b128 a[84:87], v60 offset:25600\n" \
"s_waitcnt lgkmcnt(15)\n" \
"v_mfma_f32_16x16x32_bf16 v[32:35], v[108:111], a[12:15], v[32:35]\n" \
"ds_read_b128 a[88:91], v60 offset:26624\n" \
"ds_read_b128 a[92:95], v60 offset:27648\n" \
"v_mfma_f32_16x16x32_bf16 v[36:39], v[108:111], a[28:31], v[36:39]\n" \
"ds_read_b128 a[96:99], v60 offset:0\n" \
"ds_read_b128 a[100:103], v60 offset:1024\n" \
"v_mfma_f32_16x16x32_bf16 v[40:43], v[108:111], a[44:47], v[40:43]\n" \
"ds_read_b128 a[104:107], v60 offset:2048\n" \
"ds_read_b128 a[108:111], v60 offset:3072\n" \
"v_mfma_f32_16x16x32_bf16 v[44:47], v[108:111], a[60:63], v[44:47]\n" \
"ds_read_b128 a[112:115], v60 offset:8192\n" \
"ds_read_b128 a[116:119], v60 offset:9216\n" \
"v_mfma_f32_16x16x32_bf16 v[64:67], a[160:163], a[0:3], v[32:35]\n" \
"ds_read_b128 a[120:123], v60 offset:10240\n" \
"ds_read_b128 a[124:127], v60 offset:11264\n" \
"v_mfma_f32_16x16x32_bf16 v[64:67], a[164:167], a[4:7], v[64:67]\n" \
"v_bfe_u32 v26, v20, 8, 2\n" \
"s_mov_b32 m0, s20\n" \
"v_mfma_f32_16x16x32_bf16 v[64:67], a[168:171], a[8:11], v[64:67]\n" \
"v_bfe_u32 v25, v20, 5, 3\n" \
"v_lshlrev_b32_e32 v26, 1, v26\n" \
"v_mfma_f32_16x16x32_bf16 v[64:67], a[172:175], a[12:15], v[64:67]\n" \
"global_load_lds_dword v[16:17], off\n" \
"v_mfma_f32_16x16x32_bf16 v[68:71], a[188:191], a[12:15], v[32:35]\n" \
"v_sub_u32_e32 v25, v25, v26\n" \
"v_and_b32_e32 v26, 16, v20\n" \
"v_mfma_f32_16x16x32_bf16 v[68:71], a[184:187], a[8:11], v[68:71]\n" \
"v_mov_b32_e32 v27, 0x3f80\n" \
"v_lshlrev_b32_e32 v26, v26, v27\n" \
"v_mfma_f32_16x16x32_bf16 v[68:71], a[180:183], a[4:7], v[68:71]\n" \
"v_mov_b32_e32 v54, 0x3f803f80\n" \
"s_waitcnt lgkmcnt(15)\n" \
"v_mfma_f32_16x16x32_bf16 v[68:71], a[176:179], a[0:3], v[68:71]\n" \
"s_mov_b32 s84, 0x33333333\n" \
"s_mov_b32 s85, 0xcccccccc\n" \
"v_mfma_f32_16x16x32_bf16 v[76:79], a[176:179], a[16:19], v[36:39]\n" \
"s_mov_b32 s86, 0x11111111\n" \
"s_mov_b32 s87, 0x11111111\n" \
"v_mfma_f32_16x16x32_bf16 v[76:79], a[180:183], a[20:23], v[76:79]\n" \
"s_mov_b32 s88, 0x44444444\n" \
"s_mov_b32 s89, 0x44444444\n" \
"v_mfma_f32_16x16x32_bf16 v[76:79], a[184:187], a[24:27], v[76:79]\n" \
"v_exp_f32_e32 v70, v70\n" \
"v_mfma_f32_16x16x32_bf16 v[76:79], a[188:191], a[28:31], v[76:79]\n" \
"v_exp_f32_e32 v71, v71\n" \
"v_mfma_f32_16x16x32_bf16 v[72:75], a[172:175], a[28:31], v[36:39]\n" \
"v_exp_f32_e32 v68, v68\n" \
"v_mfma_f32_16x16x32_bf16 v[72:75], a[168:171], a[24:27], v[72:75]\n" \
"v_exp_f32_e32 v69, v69\n" \
"v_mfma_f32_16x16x32_bf16 v[72:75], a[164:167], a[20:23], v[72:75]\n" \
"v_bfe_i32 v21, v20, 4, 1\n" \
"v_cvt_pk_bf16_f32 v71, v70, v71\n" \
"v_mfma_f32_16x16x32_bf16 v[72:75], a[160:163], a[16:19], v[72:75]\n" \
"v_exp_f32_e32 v66, v66\n" \
"v_mfma_f32_16x16x32_bf16 v[80:83], a[160:163], a[32:35], v[40:43]\n" \
"v_exp_f32_e32 v67, v67\n" \
"v_mfma_f32_16x16x32_bf16 v[80:83], a[164:167], a[36:39], v[80:83]\n" \
"v_exp_f32_e32 v64, v64\n" \
"v_mfma_f32_16x16x32_bf16 v[80:83], a[168:171], a[40:43], v[80:83]\n" \
"v_exp_f32_e32 v65, v65\n" \
"v_mfma_f32_16x16x32_bf16 v[80:83], a[172:175], a[44:47], v[80:83]\n" \
"v_exp_f32_e32 v72, v72\n" \
"v_mfma_f32_16x16x32_bf16 v[84:87], a[188:191], a[44:47], v[40:43]\n" \
"s_waitcnt vmcnt(3)\n" \
"v_cmp_ne_u32_e32 vcc, s62, v57\n" \
"v_mfma_f32_16x16x32_bf16 v[84:87], a[184:187], a[40:43], v[84:87]\n" \
"v_exp_f32_e32 v73, v73\n" \
"v_mfma_f32_16x16x32_bf16 v[84:87], a[180:183], a[36:39], v[84:87]\n" \
"v_exp_f32_e32 v74, v74\n" \
"v_mfma_f32_16x16x32_bf16 v[84:87], a[176:179], a[32:35], v[84:87]\n" \
"s_cbranch_vccnz L_skb_%=\n" \
"L_rkb_%=:\n" \
"v_cmp_eq_u32_e32 vcc, 0, v25\n" \
"v_mfma_f32_16x16x32_bf16 v[92:95], a[176:179], a[48:51], v[44:47]\n" \
"v_exp_f32_e32 v75, v75\n" \
"v_mfma_f32_16x16x32_bf16 v[92:95], a[180:183], a[52:55], v[92:95]\n" \
"v_cndmask_b32_e32 v28, 0, v26, vcc\n" \
"v_cvt_pk_bf16_f32 v72, v72, v73\n" \
"v_mfma_f32_16x16x32_bf16 v[92:95], a[184:187], a[56:59], v[92:95]\n" \
"v_exp_f32_e32 v76, v76\n" \
"v_mfma_f32_16x16x32_bf16 v[92:95], a[188:191], a[60:63], v[92:95]\n" \
"v_exp_f32_e32 v77, v77\n" \
"v_mfma_f32_16x16x32_bf16 v[88:91], a[172:175], a[60:63], v[44:47]\n" \
"v_exp_f32_e32 v78, v78\n" \
"v_mfma_f32_16x16x32_bf16 v[88:91], a[168:171], a[56:59], v[88:91]\n" \
"v_exp_f32_e32 v79, v79\n" \
"v_mfma_f32_16x16x32_bf16 v[88:91], a[164:167], a[52:55], v[88:91]\n" \
"v_cndmask_b32_e64 v22, 0, v54, s[84:85]\n" \
"v_cndmask_b32_e64 v23, 0, v54, s[86:87]\n" \
"v_mfma_f32_16x16x32_bf16 v[88:91], a[160:163], a[48:51], v[88:91]\n" \
"v_cndmask_b32_e64 v24, 0, v54, s[88:89]\n" \
"v_and_b32_e32 v21, 0xaaaaaaaa, v21\n" \
"s_waitcnt vmcnt(4) lgkmcnt(0)\n" \
"s_barrier\n" \
"v_cmp_eq_u32_e32 vcc, 1, v25\n" \
"v_cvt_pk_bf16_f32 v70, v68, v69\n" \
"v_cvt_pk_bf16_f32 v73, v74, v75\n" \
"v_mov_b32_e32 v62, 0\n" \
"v_accvgpr_write_b32 a208, 0\n" \
"v_accvgpr_write_b32 a209, 0\n" \
"v_accvgpr_write_b32 a210, 0\n" \
"v_accvgpr_write_b32 a211, 0\n" \
"v_accvgpr_write_b32 a200, v22\n" \
"v_accvgpr_write_b32 a201, v22\n" \
"v_accvgpr_write_b32 a202, v22\n" \
"v_accvgpr_write_b32 a203, v22\n" \
"v_accvgpr_write_b32 a204, v23\n" \
"v_accvgpr_write_b32 a205, v23\n" \
"v_accvgpr_write_b32 a206, v23\n" \
"v_accvgpr_write_b32 a207, v23\n" \
"v_accvgpr_write_b32 a212, v24\n" \
"v_accvgpr_write_b32 a213, v24\n" \
"v_accvgpr_write_b32 a214, v24\n" \
"v_accvgpr_write_b32 a215, v24\n" \
"v_or_b32_e32 v21, 0x44444444, v21\n" \
"s_mov_b32 s64, 0x8000\n" \
"v_add_u32_e32 v50, 0x6000, v55\n" \
"v_add_u32_e32 v51, 0xa000, v55\n" \
"v_add_u32_e32 v53, 0x10000, v55\n" \
"v_add_u32_e32 v48, 0xc000, v20\n" \
"v_add_u32_e32 v49, s17, v20\n" \
"v_cndmask_b32_e32 v29, 0, v26, vcc\n" \
"v_accvgpr_write_b32 a220, v96\n" \
"v_accvgpr_write_b32 a221, v97\n" \
"v_accvgpr_write_b32 a222, v98\n" \
"v_accvgpr_write_b32 a223, v99\n" \
"v_accvgpr_write_b32 a224, v100\n" \
"v_accvgpr_write_b32 a225, v101\n" \
"v_accvgpr_write_b32 a226, v102\n" \
"v_accvgpr_write_b32 a227, v103\n" \
"v_accvgpr_write_b32 a228, v104\n" \
"v_accvgpr_write_b32 a229, v105\n" \
"v_accvgpr_write_b32 a230, v106\n" \
"v_accvgpr_write_b32 a231, v107\n" \
"v_accvgpr_write_b32 a232, v108\n" \
"v_accvgpr_write_b32 a233, v109\n" \
"v_accvgpr_write_b32 a234, v110\n" \
"v_accvgpr_write_b32 a235, v111\n" \
"v_cvt_pk_bf16_f32 v69, v66, v67\n" \
"v_cvt_pk_bf16_f32 v68, v64, v65\n" \
"v_cvt_pk_bf16_f32 v74, v76, v77\n" \
"v_cvt_pk_bf16_f32 v75, v78, v79\n" \
"s_sub_u32 s66, s18, 1\n" \
"s_cbranch_scc1 L_tr_%=\n" \
".fill (L_ab + 1856 - .) / 4, 4, 0xBF800000\n" \
"L_1_%=:\n" \
"v_mfma_f32_16x16x32_bf16 v[96:99], a[128:131], a[0:3], v[32:35]\n" \
"s_xor_b32 m0, s65, 0x12000\n" \
"ds_read_b128 a[160:163], v49 offset:0\n" \
"v_mfma_f32_16x16x32_bf16 v[96:99], a[132:135], a[4:7], v[96:99]\n" \
"buffer_load_dwordx4 v50, s[8:11], s64 offen offset:0 sc1 lds\n" \
"v_mfma_f32_16x16x32_bf16 v[96:99], a[136:139], a[8:11], v[96:99]\n" \
"ds_read_b128 a[164:167], v49 offset:1024\n" \
"ds_read_b128 a[168:171], v49 offset:2048\n" \
"v_mfma_f32_16x16x32_bf16 v[96:99], a[140:143], a[12:15], v[96:99]\n" \
"ds_read_b128 a[172:175], v49 offset:3072\n" \
"ds_read_b128 a[176:179], v49 offset:4096\n" \
"v_mfma_f32_16x16x32_bf16 v[100:103], a[156:159], a[12:15], v[32:35]\n" \
"ds_read_b128 a[180:183], v49 offset:5120\n" \
"ds_read_b128 a[184:187], v49 offset:6144\n" \
"v_mfma_f32_16x16x32_bf16 v[100:103], a[152:155], a[8:11], v[100:103]\n" \
"v_exp_f32_e64 v86, v86\n" \
"v_mfma_f32_16x16x32_bf16 v[100:103], a[148:151], a[4:7], v[100:103]\n" \
"v_exp_f32_e64 v87, v87\n" \
"v_mfma_f32_16x16x32_bf16 v[100:103], a[144:147], a[0:3], v[100:103]\n" \
"ds_read_b128 a[188:191], v49 offset:7168\n" \
"v_cvt_pk_bf16_f32 v87, v86, v87\n" \
"v_mfma_f32_16x16x32_bf16 v[108:111], a[144:147], a[16:19], v[36:39]\n" \
"v_exp_f32_e64 v84, v84\n" \
"v_mfma_f32_16x16x32_bf16 v[108:111], a[148:151], a[20:23], v[108:111]\n" \
"v_exp_f32_e64 v85, v85\n" \
"v_mfma_f32_16x16x32_bf16 v[108:111], a[152:155], a[24:27], v[108:111]\n" \
"v_exp_f32_e64 v82, v82\n" \
"v_mfma_f32_16x16x32_bf16 v[108:111], a[156:159], a[28:31], v[108:111]\n" \
"v_exp_f32_e64 v83, v83\n" \
"v_mfma_f32_16x16x32_bf16 v[104:107], a[140:143], a[28:31], v[36:39]\n" \
"v_exp_f32_e64 v80, v80\n" \
"v_mfma_f32_16x16x32_bf16 v[104:107], a[136:139], a[24:27], v[104:107]\n" \
"v_exp_f32_e64 v81, v81\n" \
"v_mfma_f32_16x16x32_bf16 v[104:107], a[132:135], a[20:23], v[104:107]\n" \
"v_exp_f32_e64 v88, v88\n" \
"v_mfma_f32_16x16x32_bf16 v[104:107], a[128:131], a[16:19], v[104:107]\n" \
"buffer_load_dwordx4 v50, s[8:11], s64 offen offset:1024 sc1 lds\n" \
"v_mfma_f32_16x16x32_bf16 v[112:115], a[128:131], a[32:35], v[40:43]\n" \
"s_xor_b32 m0, s65, 0x6000\n" \
"v_cvt_pk_bf16_f32 v86, v84, v85\n" \
"v_mfma_f32_16x16x32_bf16 v[112:115], a[132:135], a[36:39], v[112:115]\n" \
"v_cvt_pk_bf16_f32 v85, v82, v83\n" \
"v_cvt_pk_bf16_f32 v84, v80, v81\n" \
"v_mfma_f32_16x16x32_bf16 v[112:115], a[136:139], a[40:43], v[112:115]\n" \
"v_exp_f32_e64 v89, v89\n" \
"v_mfma_f32_16x16x32_bf16 v[112:115], a[140:143], a[44:47], v[112:115]\n" \
"v_exp_f32_e64 v90, v90\n" \
"v_mfma_f32_16x16x32_bf16 v[116:119], a[156:159], a[44:47], v[40:43]\n" \
"v_exp_f32_e64 v91, v91\n" \
"v_mfma_f32_16x16x32_bf16 v[116:119], a[152:155], a[40:43], v[116:119]\n" \
"v_exp_f32_e64 v92, v92\n" \
"v_mfma_f32_16x16x32_bf16 v[116:119], a[148:151], a[36:39], v[116:119]\n" \
"v_exp_f32_e64 v93, v93\n" \
"v_mfma_f32_16x16x32_bf16 v[116:119], a[144:147], a[32:35], v[116:119]\n" \
"v_exp_f32_e64 v94, v94\n" \
"v_mfma_f32_16x16x32_bf16 v[124:127], a[144:147], a[48:51], v[44:47]\n" \
"v_cvt_pk_bf16_f32 v88, v88, v89\n" \
"v_cvt_pk_bf16_f32 v89, v90, v91\n" \
"v_mfma_f32_16x16x32_bf16 v[124:127], a[148:151], a[52:55], v[124:127]\n" \
"v_exp_f32_e64 v95, v95\n" \
"v_mfma_f32_16x16x32_bf16 v[124:127], a[152:155], a[56:59], v[124:127]\n" \
"v_cvt_pk_bf16_f32 v90, v92, v93\n" \
"v_cvt_pk_bf16_f32 v91, v94, v95\n" \
"v_mfma_f32_16x16x32_bf16 v[124:127], a[156:159], a[60:63], v[124:127]\n" \
"ds_read_b128 a[144:147], v48 offset:4096\n" \
"ds_read_b128 a[148:151], v48 offset:5120\n" \
"v_mfma_f32_16x16x32_bf16 v[120:123], a[140:143], a[60:63], v[44:47]\n" \
"ds_read_b128 a[152:155], v48 offset:6144\n" \
"ds_read_b128 a[140:143], v48 offset:3072\n" \
"v_mfma_f32_16x16x32_bf16 v[120:123], a[136:139], a[56:59], v[120:123]\n" \
"ds_read_b128 a[136:139], v48 offset:2048\n" \
"ds_read_b128 a[156:159], v48 offset:7168\n" \
"v_mfma_f32_16x16x32_bf16 v[120:123], a[132:135], a[52:55], v[120:123]\n" \
"buffer_load_dwordx4 v51, s[8:11], s64 offen offset:0 sc1 lds\n" \
"v_mfma_f32_16x16x32_bf16 v[120:123], a[128:131], a[48:51], v[120:123]\n" \
"ds_read_b128 a[128:131], v48 offset:0\n" \
"s_waitcnt lgkmcnt(7)\n" \
"v_mfma_f32_16x16x32_bf16 v[128:131], a[160:163], v[68:71], v[128:131]\n" \
"v_exp_f32_e32 v102, v102\n" \
"v_mfma_f32_16x16x32_bf16 v[160:163], a[160:163], v[72:75], v[160:163]\n" \
"v_exp_f32_e64 v103, v103\n" \
"v_mfma_f32_16x16x32_bf16 v[132:135], a[164:167], v[68:71], v[132:135]\n" \
"ds_read_b128 a[132:135], v48 offset:1024\n" \
"v_cvt_pk_bf16_f32 v103, v102, v103\n" \
"v_mfma_f32_16x16x32_bf16 v[164:167], a[164:167], v[72:75], v[164:167]\n" \
"v_exp_f32_e64 v100, v100\n" \
"v_mfma_f32_16x16x32_bf16 v[136:139], a[168:171], v[68:71], v[136:139]\n" \
"v_exp_f32_e64 v101, v101\n" \
"v_mfma_f32_16x16x32_bf16 v[168:171], a[168:171], v[72:75], v[168:171]\n" \
"v_exp_f32_e64 v98, v98\n" \
"v_mfma_f32_16x16x32_bf16 v[140:143], a[172:175], v[68:71], v[140:143]\n" \
"v_exp_f32_e64 v99, v99\n" \
"v_mfma_f32_16x16x32_bf16 v[172:175], a[172:175], v[72:75], v[172:175]\n" \
"v_exp_f32_e64 v96, v96\n" \
"v_mfma_f32_16x16x32_bf16 v[144:147], a[176:179], v[68:71], v[144:147]\n" \
"v_exp_f32_e64 v97, v97\n" \
"v_mfma_f32_16x16x32_bf16 v[176:179], a[176:179], v[72:75], v[176:179]\n" \
"v_exp_f32_e64 v104, v104\n" \
"v_mfma_f32_16x16x32_bf16 v[148:151], a[180:183], v[68:71], v[148:151]\n" \
"v_exp_f32_e64 v105, v105\n" \
"v_mfma_f32_16x16x32_bf16 v[180:183], a[180:183], v[72:75], v[180:183]\n" \
"v_cvt_pk_bf16_f32 v102, v100, v101\n" \
"v_cvt_pk_bf16_f32 v101, v98, v99\n" \
"v_mfma_f32_16x16x32_bf16 v[152:155], a[184:187], v[68:71], v[152:155]\n" \
"v_cvt_pk_bf16_f32 v100, v96, v97\n" \
"v_cvt_pk_bf16_f32 v104, v104, v105\n" \
"v_mfma_f32_16x16x32_bf16 v[184:187], a[184:187], v[72:75], v[184:187]\n" \
"buffer_load_dwordx4 v51, s[8:11], s64 offen offset:1024 sc1 lds\n" \
"v_mfma_f32_16x16x32_bf16 v[156:159], a[188:191], v[68:71], v[156:159]\n" \
"v_exp_f32_e64 v106, v106\n" \
"v_mfma_f32_16x16x32_bf16 v[188:191], a[188:191], v[72:75], v[188:191]\n" \
"v_exp_f32_e64 v107, v107\n" \
"v_smfmac_f32_16x16x64_bf16 a[192:195], a[200:203], v[68:75], v21\n" \
"v_exp_f32_e64 v108, v108\n" \
"v_mfma_f32_16x16x32_bf16 v[220:223], a[188:191], v[84:87], v[220:223]\n" \
"v_exp_f32_e64 v109, v109\n" \
"v_mfma_f32_16x16x32_bf16 v[252:255], a[188:191], v[88:91], v[252:255]\n" \
"v_exp_f32_e64 v110, v110\n" \
"v_mfma_f32_16x16x32_bf16 v[216:219], a[184:187], v[84:87], v[216:219]\n" \
"v_exp_f32_e64 v111, v111\n" \
"v_mfma_f32_16x16x32_bf16 v[248:251], a[184:187], v[88:91], v[248:251]\n" \
"v_exp_f32_e64 v118, v118\n" \
"v_mfma_f32_16x16x32_bf16 v[212:215], a[180:183], v[84:87], v[212:215]\n" \
"v_exp_f32_e64 v119, v119\n" \
"v_mfma_f32_16x16x32_bf16 v[244:247], a[180:183], v[88:91], v[244:247]\n" \
"v_cvt_pk_bf16_f32 v105, v106, v107\n" \
"v_cvt_pk_bf16_f32 v106, v108, v109\n" \
"v_mfma_f32_16x16x32_bf16 v[208:211], a[176:179], v[84:87], v[208:211]\n" \
"v_cvt_pk_bf16_f32 v107, v110, v111\n" \
"v_cvt_pk_bf16_f32 v119, v118, v119\n" \
"v_mfma_f32_16x16x32_bf16 v[240:243], a[176:179], v[88:91], v[240:243]\n" \
"v_exp_f32_e64 v116, v116\n" \
"v_mfma_f32_16x16x32_bf16 v[204:207], a[172:175], v[84:87], v[204:207]\n" \
"v_exp_f32_e64 v117, v117\n" \
"v_mfma_f32_16x16x32_bf16 v[236:239], a[172:175], v[88:91], v[236:239]\n" \
"v_exp_f32_e64 v114, v114\n" \
"v_mfma_f32_16x16x32_bf16 v[200:203], a[168:171], v[84:87], v[200:203]\n" \
"v_exp_f32_e64 v115, v115\n" \
"v_mfma_f32_16x16x32_bf16 v[232:235], a[168:171], v[88:91], v[232:235]\n" \
"v_exp_f32_e64 v112, v112\n" \
"v_mfma_f32_16x16x32_bf16 v[196:199], a[164:167], v[84:87], v[196:199]\n" \
"v_exp_f32_e64 v113, v113\n" \
"v_mfma_f32_16x16x32_bf16 v[228:231], a[164:167], v[88:91], v[228:231]\n" \
"v_exp_f32_e64 v120, v120\n" \
"v_mfma_f32_16x16x32_bf16 v[192:195], a[160:163], v[84:87], v[192:195]\n" \
"v_exp_f32_e64 v121, v121\n" \
"v_mfma_f32_16x16x32_bf16 v[224:227], a[160:163], v[88:91], v[224:227]\n" \
"v_cvt_pk_bf16_f32 v118, v116, v117\n" \
"v_cvt_pk_bf16_f32 v117, v114, v115\n" \
"v_smfmac_f32_16x16x64_bf16 a[196:199], a[200:203], v[84:91], v21\n" \
"v_cvt_pk_bf16_f32 v116, v112, v113\n" \
"v_cvt_pk_bf16_f32 v120, v120, v121\n" \
"s_waitcnt vmcnt(4) lgkmcnt(0)\n" \
"s_barrier\n" \
"v_mfma_f32_16x16x32_bf16 v[64:67], a[128:131], a[0:3], v[32:35]\n" \
"s_xor_b32 m0, s65, 0x14000\n" \
"ds_read_b128 a[160:163], v49 offset:8192\n" \
"v_mfma_f32_16x16x32_bf16 v[64:67], a[132:135], a[4:7], v[64:67]\n" \
"buffer_load_dwordx4 v52, s[8:11], s64 offen offset:0 sc1 lds\n" \
"v_mfma_f32_16x16x32_bf16 v[64:67], a[136:139], a[8:11], v[64:67]\n" \
"ds_read_b128 a[164:167], v49 offset:9216\n" \
"ds_read_b128 a[168:171], v49 offset:10240\n" \
"v_mfma_f32_16x16x32_bf16 v[64:67], a[140:143], a[12:15], v[64:67]\n" \
"ds_read_b128 a[172:175], v49 offset:11264\n" \
"ds_read_b128 a[176:179], v49 offset:12288\n" \
"v_mfma_f32_16x16x32_bf16 v[68:71], a[156:159], a[12:15], v[32:35]\n" \
"ds_read_b128 a[180:183], v49 offset:13312\n" \
"ds_read_b128 a[184:187], v49 offset:14336\n" \
"v_mfma_f32_16x16x32_bf16 v[68:71], a[152:155], a[8:11], v[68:71]\n" \
"v_exp_f32_e64 v122, v122\n" \
"v_mfma_f32_16x16x32_bf16 v[68:71], a[148:151], a[4:7], v[68:71]\n" \
"v_exp_f32_e64 v123, v123\n" \
"v_mfma_f32_16x16x32_bf16 v[68:71], a[144:147], a[0:3], v[68:71]\n" \
"ds_read_b128 a[188:191], v49 offset:15360\n" \
"v_cvt_pk_bf16_f32 v121, v122, v123\n" \
"v_mfma_f32_16x16x32_bf16 v[76:79], a[144:147], a[16:19], v[36:39]\n" \
"v_exp_f32_e64 v124, v124\n" \
"v_mfma_f32_16x16x32_bf16 v[76:79], a[148:151], a[20:23], v[76:79]\n" \
"v_exp_f32_e64 v125, v125\n" \
"v_mfma_f32_16x16x32_bf16 v[76:79], a[152:155], a[24:27], v[76:79]\n" \
"v_exp_f32_e64 v126, v126\n" \
"v_mfma_f32_16x16x32_bf16 v[76:79], a[156:159], a[28:31], v[76:79]\n" \
"v_exp_f32_e64 v127, v127\n" \
"v_mfma_f32_16x16x32_bf16 v[72:75], a[140:143], a[28:31], v[36:39]\n" \
"v_cvt_pk_bf16_f32 v122, v124, v125\n" \
"v_cvt_pk_bf16_f32 v123, v126, v127\n" \
"v_mfma_f32_16x16x32_bf16 v[72:75], a[136:139], a[24:27], v[72:75]\n" \
"v_exp_f32_e64 v70, v70\n" \
"v_mfma_f32_16x16x32_bf16 v[72:75], a[132:135], a[20:23], v[72:75]\n" \
"v_exp_f32_e64 v71, v71\n" \
"v_mfma_f32_16x16x32_bf16 v[72:75], a[128:131], a[16:19], v[72:75]\n" \
"buffer_load_dwordx4 v52, s[8:11], s64 offen offset:1024 sc1 lds\n" \
"v_mfma_f32_16x16x32_bf16 v[80:83], a[128:131], a[32:35], v[40:43]\n" \
"s_mov_b32 m0, s65\n" \
"v_cvt_pk_bf16_f32 v71, v70, v71\n" \
"v_mfma_f32_16x16x32_bf16 v[80:83], a[132:135], a[36:39], v[80:83]\n" \
"v_exp_f32_e32 v68, v68\n" \
"v_mfma_f32_16x16x32_bf16 v[80:83], a[136:139], a[40:43], v[80:83]\n" \
"v_exp_f32_e64 v69, v69\n" \
"v_mfma_f32_16x16x32_bf16 v[80:83], a[140:143], a[44:47], v[80:83]\n" \
"v_exp_f32_e64 v72, v72\n" \
"v_mfma_f32_16x16x32_bf16 v[84:87], a[156:159], a[44:47], v[40:43]\n" \
"v_exp_f32_e64 v73, v73\n" \
"v_mfma_f32_16x16x32_bf16 v[84:87], a[152:155], a[40:43], v[84:87]\n" \
"v_exp_f32_e64 v74, v74\n" \
"v_mfma_f32_16x16x32_bf16 v[84:87], a[148:151], a[36:39], v[84:87]\n" \
"v_exp_f32_e64 v75, v75\n" \
"v_mfma_f32_16x16x32_bf16 v[84:87], a[144:147], a[32:35], v[84:87]\n" \
"v_exp_f32_e64 v66, v66\n" \
"v_mfma_f32_16x16x32_bf16 v[92:95], a[144:147], a[48:51], v[44:47]\n" \
"ds_read_b128 a[144:147], v48 offset:12288\n" \
"v_cvt_pk_bf16_f32 v72, v72, v73\n" \
"v_mfma_f32_16x16x32_bf16 v[92:95], a[148:151], a[52:55], v[92:95]\n" \
"ds_read_b128 a[148:151], v48 offset:13312\n" \
"v_cvt_pk_bf16_f32 v70, v68, v69\n" \
"v_mfma_f32_16x16x32_bf16 v[92:95], a[152:155], a[56:59], v[92:95]\n" \
"ds_read_b128 a[152:155], v48 offset:14336\n" \
"v_cvt_pk_bf16_f32 v73, v74, v75\n" \
"v_mfma_f32_16x16x32_bf16 v[92:95], a[156:159], a[60:63], v[92:95]\n" \
"ds_read_b128 a[156:159], v48 offset:15360\n" \
"v_add_u32_e32 v49, 0x10000, v48\n" \
"v_mfma_f32_16x16x32_bf16 v[88:91], a[140:143], a[60:63], v[44:47]\n" \
"ds_read_b128 a[140:143], v48 offset:11264\n" \
"s_xor_b32 s65, s65, 0x4000\n" \
"v_mfma_f32_16x16x32_bf16 v[88:91], a[136:139], a[56:59], v[88:91]\n" \
"v_exp_f32_e64 v67, v67\n" \
"v_mfma_f32_16x16x32_bf16 v[88:91], a[132:135], a[52:55], v[88:91]\n" \
"buffer_load_dwordx4 v53, s[8:11], s64 offen offset:0 sc1 lds\n" \
"v_mfma_f32_16x16x32_bf16 v[88:91], a[128:131], a[48:51], v[88:91]\n" \
"ds_read_b128 a[128:131], v48 offset:8192\n" \
"s_waitcnt lgkmcnt(6)\n" \
"v_mfma_f32_16x16x32_bf16 v[128:131], a[160:163], v[100:103], v[128:131]\n" \
"ds_read_b128 a[132:135], v48 offset:9216\n" \
"ds_read_b128 a[136:139], v48 offset:10240\n" \
"v_mfma_f32_16x16x32_bf16 v[160:163], a[160:163], v[104:107], v[160:163]\n" \
"v_exp_f32_e32 v64, v64\n" \
"v_mfma_f32_16x16x32_bf16 v[132:135], a[164:167], v[100:103], v[132:135]\n" \
"v_exp_f32_e64 v65, v65\n" \
"v_mfma_f32_16x16x32_bf16 v[164:167], a[164:167], v[104:107], v[164:167]\n" \
"v_exp_f32_e64 v76, v76\n" \
"v_mfma_f32_16x16x32_bf16 v[136:139], a[168:171], v[100:103], v[136:139]\n" \
"v_exp_f32_e64 v77, v77\n" \
"v_mfma_f32_16x16x32_bf16 v[168:171], a[168:171], v[104:107], v[168:171]\n" \
"v_exp_f32_e64 v78, v78\n" \
"v_mfma_f32_16x16x32_bf16 v[140:143], a[172:175], v[100:103], v[140:143]\n" \
"v_exp_f32_e64 v79, v79\n" \
"v_mfma_f32_16x16x32_bf16 v[172:175], a[172:175], v[104:107], v[172:175]\n" \
"v_cvt_pk_bf16_f32 v69, v66, v67\n" \
"v_cvt_pk_bf16_f32 v68, v64, v65\n" \
"v_mfma_f32_16x16x32_bf16 v[144:147], a[176:179], v[100:103], v[144:147]\n" \
"v_cvt_pk_bf16_f32 v74, v76, v77\n" \
"v_cvt_pk_bf16_f32 v75, v78, v79\n" \
"v_mfma_f32_16x16x32_bf16 v[176:179], a[176:179], v[104:107], v[176:179]\n" \
"v_xor_b32_e32 v48, 0x4000, v48\n" \
"v_mfma_f32_16x16x32_bf16 v[148:151], a[180:183], v[100:103], v[148:151]\n" \
"v_mfma_f32_16x16x32_bf16 v[180:183], a[180:183], v[104:107], v[180:183]\n" \
"v_mfma_f32_16x16x32_bf16 v[152:155], a[184:187], v[100:103], v[152:155]\n" \
"v_mfma_f32_16x16x32_bf16 v[184:187], a[184:187], v[104:107], v[184:187]\n" \
"buffer_load_dwordx4 v53, s[8:11], s64 offen offset:1024 sc1 lds\n" \
"v_mfma_f32_16x16x32_bf16 v[156:159], a[188:191], v[100:103], v[156:159]\n" \
"s_add_u32 s64, s64, 0x8000\n" \
"s_sub_u32 s66, s66, 1\n" \
"s_nop 0\n" \
"v_mfma_f32_16x16x32_bf16 v[188:191], a[188:191], v[104:107], v[188:191]\n" \
"v_smfmac_f32_16x16x64_bf16 a[192:195], a[200:203], v[100:107], v21\n" \
"v_mfma_f32_16x16x32_bf16 v[220:223], a[188:191], v[116:119], v[220:223]\n" \
"v_mfma_f32_16x16x32_bf16 v[252:255], a[188:191], v[120:123], v[252:255]\n" \
"v_mfma_f32_16x16x32_bf16 v[216:219], a[184:187], v[116:119], v[216:219]\n" \
"v_mfma_f32_16x16x32_bf16 v[248:251], a[184:187], v[120:123], v[248:251]\n" \
"v_mfma_f32_16x16x32_bf16 v[212:215], a[180:183], v[116:119], v[212:215]\n" \
"v_mfma_f32_16x16x32_bf16 v[244:247], a[180:183], v[120:123], v[244:247]\n" \
"v_mfma_f32_16x16x32_bf16 v[208:211], a[176:179], v[116:119], v[208:211]\n" \
"v_mfma_f32_16x16x32_bf16 v[240:243], a[176:179], v[120:123], v[240:243]\n" \
"v_mfma_f32_16x16x32_bf16 v[204:207], a[172:175], v[116:119], v[204:207]\n" \
"v_mfma_f32_16x16x32_bf16 v[236:239], a[172:175], v[120:123], v[236:239]\n" \
"v_mfma_f32_16x16x32_bf16 v[200:203], a[168:171], v[116:119], v[200:203]\n" \
"v_mfma_f32_16x16x32_bf16 v[232:235], a[168:171], v[120:123], v[232:235]\n" \
"v_mfma_f32_16x16x32_bf16 v[196:199], a[164:167], v[116:119], v[196:199]\n" \
"v_mfma_f32_16x16x32_bf16 v[228:231], a[164:167], v[120:123], v[228:231]\n" \
"v_mfma_f32_16x16x32_bf16 v[192:195], a[160:163], v[116:119], v[192:195]\n" \
"v_mfma_f32_16x16x32_bf16 v[224:227], a[160:163], v[120:123], v[224:227]\n" \
"v_smfmac_f32_16x16x64_bf16 a[196:199], a[200:203], v[116:123], v21\n" \
"s_waitcnt vmcnt(4) lgkmcnt(0)\n" \
"s_barrier\n" \
"s_cbranch_scc0 L_1_%=\n" \
"L_tr_%=:\n" \
"s_cmp_lg_u32 s16, 0\n" \
"s_cbranch_scc1 L_sw_%=\n" \
".fill (L_ab + 4096 - .) / 4, 4, 0xBF800000\n" \
"L_ap1:\n" \
"s_cbranch_execz L_ap0\n" \
"v_mfma_f32_16x16x32_bf16 v[96:99], a[128:131], a[0:3], v[32:35]\n" \
"s_xor_b32 m0, s65, 0x12000\n" \
"s_mov_b32 s65, s13\n" \
"v_mfma_f32_16x16x32_bf16 v[96:99], a[132:135], a[4:7], v[96:99]\n" \
"buffer_load_dwordx4 v50, s[8:11], s64 offen offset:0 sc1 lds\n" \
"v_mfma_f32_16x16x32_bf16 v[96:99], a[136:139], a[8:11], v[96:99]\n" \
"v_add_u32_e32 v63, s15, v20\n" \
"s_add_u32 s67, s14, 40960\n" \
"v_mfma_f32_16x16x32_bf16 v[96:99], a[140:143], a[12:15], v[96:99]\n" \
"ds_read_b128 a[160:163], v49 offset:0\n" \
"ds_read_b128 a[164:167], v49 offset:1024\n" \
"v_mfma_f32_16x16x32_bf16 v[100:103], a[156:159], a[12:15], v[32:35]\n" \
"buffer_load_dwordx4 v50, s[8:11], s64 offen offset:1024 sc1 lds\n" \
"v_mfma_f32_16x16x32_bf16 v[100:103], a[152:155], a[8:11], v[100:103]\n" \
"v_add_u32_e32 v50, 0x6000, v63\n" \
"s_xor_b32 m0, s65, 0x8000\n" \
"v_mfma_f32_16x16x32_bf16 v[100:103], a[148:151], a[4:7], v[100:103]\n" \
"ds_read_b128 a[168:171], v49 offset:2048\n" \
"ds_read_b128 a[172:175], v49 offset:3072\n" \
"v_mfma_f32_16x16x32_bf16 v[100:103], a[144:147], a[0:3], v[100:103]\n" \
"ds_read_b128 a[176:179], v49 offset:4096\n" \
"ds_read_b128 a[180:183], v49 offset:5120\n" \
"v_mfma_f32_16x16x32_bf16 v[108:111], a[144:147], a[16:19], v[36:39]\n" \
"buffer_load_dwordx4 v50, s[8:11], s67 offen offset:0 sc1 lds\n" \
"v_mfma_f32_16x16x32_bf16 v[108:111], a[148:151], a[20:23], v[108:111]\n" \
"ds_read_b128 a[184:187], v49 offset:6144\n" \
"ds_read_b128 a[188:191], v49 offset:7168\n" \
"v_mfma_f32_16x16x32_bf16 v[108:111], a[152:155], a[24:27], v[108:111]\n" \
"s_mov_b32 s64, s14\n" \
"v_add_u32_e32 v51, 0x12000, v63\n" \
"v_mfma_f32_16x16x32_bf16 v[108:111], a[156:159], a[28:31], v[108:111]\n" \
"v_exp_f32_e32 v86, v86\n" \
"v_mfma_f32_16x16x32_bf16 v[104:107], a[140:143], a[28:31], v[36:39]\n" \
"buffer_load_dwordx4 v50, s[8:11], s67 offen offset:1024 sc1 lds\n" \
"v_mfma_f32_16x16x32_bf16 v[104:107], a[136:139], a[24:27], v[104:107]\n" \
"v_exp_f32_e32 v87, v87\n" \
"v_mfma_f32_16x16x32_bf16 v[104:107], a[132:135], a[20:23], v[104:107]\n" \
"v_exp_f32_e32 v84, v84\n" \
"v_mfma_f32_16x16x32_bf16 v[104:107], a[128:131], a[16:19], v[104:107]\n" \
"v_exp_f32_e32 v85, v85\n" \
"v_mfma_f32_16x16x32_bf16 v[112:115], a[128:131], a[32:35], v[40:43]\n" \
"v_cvt_pk_bf16_f32 v87, v86, v87\n" \
"v_cvt_pk_bf16_f32 v86, v84, v85\n" \
"v_mfma_f32_16x16x32_bf16 v[112:115], a[132:135], a[36:39], v[112:115]\n" \
"buffer_load_dwordx4 v50, s[8:11], s67 offen offset:2048 sc1 lds\n" \
"v_mfma_f32_16x16x32_bf16 v[112:115], a[136:139], a[40:43], v[112:115]\n" \
"v_exp_f32_e32 v82, v82\n" \
"v_mfma_f32_16x16x32_bf16 v[112:115], a[140:143], a[44:47], v[112:115]\n" \
"v_exp_f32_e32 v83, v83\n" \
"v_mfma_f32_16x16x32_bf16 v[116:119], a[156:159], a[44:47], v[40:43]\n" \
"v_exp_f32_e32 v80, v80\n" \
"v_mfma_f32_16x16x32_bf16 v[116:119], a[152:155], a[40:43], v[116:119]\n" \
"buffer_load_dwordx4 v50, s[8:11], s67 offen offset:3072 sc1 lds\n" \
"v_mfma_f32_16x16x32_bf16 v[116:119], a[148:151], a[36:39], v[116:119]\n" \
"s_xor_b32 m0, s65, 0xa000\n" \
"v_cvt_pk_bf16_f32 v85, v82, v83\n" \
"v_mfma_f32_16x16x32_bf16 v[116:119], a[144:147], a[32:35], v[116:119]\n" \
"v_exp_f32_e32 v81, v81\n" \
"v_mfma_f32_16x16x32_bf16 v[124:127], a[144:147], a[48:51], v[44:47]\n" \
"v_cvt_pk_bf16_f32 v84, v80, v81\n" \
"v_readlane_b32 s93, v11, 19\n" \
"v_mfma_f32_16x16x32_bf16 v[124:127], a[148:151], a[52:55], v[124:127]\n" \
"buffer_load_dwordx4 v51, s[8:11], s64 offen offset:0 sc1 lds\n" \
"v_mfma_f32_16x16x32_bf16 v[124:127], a[152:155], a[56:59], v[124:127]\n" \
"v_exp_f32_e32 v88, v88\n" \
"v_mfma_f32_16x16x32_bf16 v[124:127], a[156:159], a[60:63], v[124:127]\n" \
"v_exp_f32_e32 v89, v89\n" \
"v_mfma_f32_16x16x32_bf16 v[120:123], a[140:143], a[60:63], v[44:47]\n" \
"v_exp_f32_e32 v90, v90\n" \
"v_mfma_f32_16x16x32_bf16 v[120:123], a[136:139], a[56:59], v[120:123]\n" \
"v_exp_f32_e32 v91, v91\n" \
"v_mfma_f32_16x16x32_bf16 v[120:123], a[132:135], a[52:55], v[120:123]\n" \
"buffer_load_dwordx4 v51, s[8:11], s64 offen offset:1024 sc1 lds\n" \
"v_mfma_f32_16x16x32_bf16 v[120:123], a[128:131], a[48:51], v[120:123]\n" \
"v_cvt_pk_bf16_f32 v88, v88, v89\n" \
"s_waitcnt lgkmcnt(0)\n" \
"v_mfma_f32_16x16x32_bf16 v[128:131], a[160:163], v[68:71], v[128:131]\n" \
"v_exp_f32_e32 v92, v92\n" \
"v_mfma_f32_16x16x32_bf16 v[160:163], a[160:163], v[72:75], v[160:163]\n" \
"v_exp_f32_e32 v93, v93\n" \
"v_mfma_f32_16x16x32_bf16 v[132:135], a[164:167], v[68:71], v[132:135]\n" \
"buffer_load_dwordx4 v51, s[8:11], s64 offen offset:2048 sc1 lds\n" \
"v_mfma_f32_16x16x32_bf16 v[164:167], a[164:167], v[72:75], v[164:167]\n" \
"v_exp_f32_e32 v94, v94\n" \
"v_mfma_f32_16x16x32_bf16 v[136:139], a[168:171], v[68:71], v[136:139]\n" \
"v_exp_f32_e32 v95, v95\n" \
"v_mfma_f32_16x16x32_bf16 v[168:171], a[168:171], v[72:75], v[168:171]\n" \
"v_cvt_pk_bf16_f32 v89, v90, v91\n" \
"v_cvt_pk_bf16_f32 v90, v92, v93\n" \
"v_mfma_f32_16x16x32_bf16 v[140:143], a[172:175], v[68:71], v[140:143]\n" \
"buffer_load_dwordx4 v51, s[8:11], s64 offen offset:3072 sc1 lds\n" \
"v_mfma_f32_16x16x32_bf16 v[172:175], a[172:175], v[72:75], v[172:175]\n" \
"v_cvt_pk_bf16_f32 v91, v94, v95\n" \
"v_readlane_b32 s80, v11, 11\n" \
"v_mfma_f32_16x16x32_bf16 v[144:147], a[176:179], v[68:71], v[144:147]\n" \
"v_readlane_b32 s81, v11, 12\n" \
"s_mov_b32 s82, 0x47fca0\n" \
"v_mfma_f32_16x16x32_bf16 v[176:179], a[176:179], v[72:75], v[176:179]\n" \
"s_mov_b32 s83, 0x20000\n" \
"v_readlane_b32 s92, v11, 25\n" \
"v_mfma_f32_16x16x32_bf16 v[148:151], a[180:183], v[68:71], v[148:151]\n" \
"s_add_u32 m0, s93, 0x0\n" \
"ds_read_b128 a[128:131], v48 offset:0\n" \
"v_mfma_f32_16x16x32_bf16 v[180:183], a[180:183], v[72:75], v[180:183]\n" \
"ds_read_b128 a[144:147], v48 offset:4096\n" \
"ds_read_b128 a[132:135], v48 offset:1024\n" \
"v_mfma_f32_16x16x32_bf16 v[152:155], a[184:187], v[68:71], v[152:155]\n" \
"buffer_load_dwordx4 v0, s[80:83], s92 offen offset:0 lds\n" \
"v_mfma_f32_16x16x32_bf16 v[184:187], a[184:187], v[72:75], v[184:187]\n" \
"ds_read_b128 a[148:151], v48 offset:5120\n" \
"ds_read_b128 a[136:139], v48 offset:2048\n" \
"v_mfma_f32_16x16x32_bf16 v[156:159], a[188:191], v[68:71], v[156:159]\n" \
"ds_read_b128 a[152:155], v48 offset:6144\n" \
"ds_read_b128 a[140:143], v48 offset:3072\n" \
"v_mfma_f32_16x16x32_bf16 v[188:191], a[188:191], v[72:75], v[188:191]\n" \
"ds_read_b128 a[156:159], v48 offset:7168\n" \
"v_mov_b32_e32 v56, s62\n" \
"v_smfmac_f32_16x16x64_bf16 a[192:195], a[200:203], v[68:75], v21\n" \
"buffer_load_dwordx4 v1, s[80:83], s92 offen offset:1024 lds\n" \
"v_mfma_f32_16x16x32_bf16 v[220:223], a[188:191], v[84:87], v[220:223]\n" \
"v_readlane_b32 s68, v11, 0\n" \
"v_add_u32_e32 v52, 0x14000, v63\n" \
"v_mfma_f32_16x16x32_bf16 v[252:255], a[188:191], v[88:91], v[252:255]\n" \
"v_add_u32_e32 v53, 0x20000, v63\n" \
"s_mov_b32 s66, s19\n" \
"v_mfma_f32_16x16x32_bf16 v[216:219], a[184:187], v[84:87], v[216:219]\n" \
"buffer_load_dwordx4 v2, s[80:83], s92 offen offset:2048 lds\n" \
"v_mfma_f32_16x16x32_bf16 v[248:251], a[184:187], v[88:91], v[248:251]\n" \
"v_exp_f32_e32 v102, v102\n" \
"v_mfma_f32_16x16x32_bf16 v[212:215], a[180:183], v[84:87], v[212:215]\n" \
"v_exp_f32_e32 v103, v103\n" \
"v_mfma_f32_16x16x32_bf16 v[244:247], a[180:183], v[88:91], v[244:247]\n" \
"v_exp_f32_e32 v100, v100\n" \
"v_mfma_f32_16x16x32_bf16 v[208:211], a[176:179], v[84:87], v[208:211]\n" \
"buffer_load_dwordx4 v3, s[80:83], s92 offen offset:3072 lds\n" \
"v_mfma_f32_16x16x32_bf16 v[240:243], a[176:179], v[88:91], v[240:243]\n" \
"v_exp_f32_e32 v101, v101\n" \
"v_mfma_f32_16x16x32_bf16 v[204:207], a[172:175], v[84:87], v[204:207]\n" \
"v_exp_f32_e32 v98, v98\n" \
"v_mfma_f32_16x16x32_bf16 v[236:239], a[172:175], v[88:91], v[236:239]\n" \
"v_exp_f32_e32 v99, v99\n" \
"v_mfma_f32_16x16x32_bf16 v[200:203], a[168:171], v[84:87], v[200:203]\n" \
"v_exp_f32_e32 v96, v96\n" \
"v_mfma_f32_16x16x32_bf16 v[232:235], a[168:171], v[88:91], v[232:235]\n" \
"v_exp_f32_e32 v97, v97\n" \
"v_mfma_f32_16x16x32_bf16 v[196:199], a[164:167], v[84:87], v[196:199]\n" \
"v_exp_f32_e32 v104, v104\n" \
"v_mfma_f32_16x16x32_bf16 v[228:231], a[164:167], v[88:91], v[228:231]\n" \
"v_exp_f32_e32 v105, v105\n" \
"v_mfma_f32_16x16x32_bf16 v[192:195], a[160:163], v[84:87], v[192:195]\n" \
"v_cvt_pk_bf16_f32 v103, v102, v103\n" \
"v_cvt_pk_bf16_f32 v102, v100, v101\n" \
"v_mfma_f32_16x16x32_bf16 v[224:227], a[160:163], v[88:91], v[224:227]\n" \
"v_cvt_pk_bf16_f32 v101, v98, v99\n" \
"v_cvt_pk_bf16_f32 v100, v96, v97\n" \
"v_smfmac_f32_16x16x64_bf16 a[196:199], a[200:203], v[84:91], v21\n" \
"v_exp_f32_e32 v106, v106\n" \
"s_waitcnt vmcnt(14) lgkmcnt(0)\n" \
"s_barrier\n" \
"v_mfma_f32_16x16x32_bf16 v[64:67], a[128:131], a[0:3], v[32:35]\n" \
"s_xor_b32 m0, s65, 0x18000\n" \
"ds_read_b128 a[160:163], v49 offset:8192\n" \
"v_mfma_f32_16x16x32_bf16 v[64:67], a[132:135], a[4:7], v[64:67]\n" \
"buffer_load_dwordx4 v52, s[8:11], s64 offen offset:0 sc1 lds\n" \
"v_mfma_f32_16x16x32_bf16 v[64:67], a[136:139], a[8:11], v[64:67]\n" \
"ds_read_b128 a[164:167], v49 offset:9216\n" \
"ds_read_b128 a[168:171], v49 offset:10240\n" \
"v_mfma_f32_16x16x32_bf16 v[64:67], a[140:143], a[12:15], v[64:67]\n" \
"ds_read_b128 a[172:175], v49 offset:11264\n" \
"ds_read_b128 a[176:179], v49 offset:12288\n" \
"v_mfma_f32_16x16x32_bf16 v[68:71], a[156:159], a[12:15], v[32:35]\n" \
"buffer_load_dwordx4 v52, s[8:11], s64 offen offset:1024 sc1 lds\n" \
"s_mov_b64 exec, 1\n" \
"buffer_store_dword v56, off, s[56:59], s68 offset:0 sc1\n" \
"s_mov_b64 exec, -1\n" \
"v_mfma_f32_16x16x32_bf16 v[68:71], a[152:155], a[8:11], v[68:71]\n" \
"ds_read_b128 a[180:183], v49 offset:13312\n" \
"ds_read_b128 a[184:187], v49 offset:14336\n" \
"v_mfma_f32_16x16x32_bf16 v[68:71], a[148:151], a[4:7], v[68:71]\n" \
"v_exp_f32_e32 v107, v107\n" \
"v_mfma_f32_16x16x32_bf16 v[68:71], a[144:147], a[0:3], v[68:71]\n" \
"v_cvt_pk_bf16_f32 v104, v104, v105\n" \
"v_cvt_pk_bf16_f32 v105, v106, v107\n" \
"v_mfma_f32_16x16x32_bf16 v[76:79], a[144:147], a[16:19], v[36:39]\n" \
"v_exp_f32_e32 v108, v108\n" \
"v_mfma_f32_16x16x32_bf16 v[76:79], a[148:151], a[20:23], v[76:79]\n" \
"buffer_load_dwordx4 v52, s[8:11], s64 offen offset:2048 sc1 lds\n" \
"v_mfma_f32_16x16x32_bf16 v[76:79], a[152:155], a[24:27], v[76:79]\n" \
"v_exp_f32_e32 v109, v109\n" \
"v_mfma_f32_16x16x32_bf16 v[76:79], a[156:159], a[28:31], v[76:79]\n" \
"v_exp_f32_e32 v110, v110\n" \
"v_mfma_f32_16x16x32_bf16 v[72:75], a[140:143], a[28:31], v[36:39]\n" \
"v_exp_f32_e32 v111, v111\n" \
"v_mfma_f32_16x16x32_bf16 v[72:75], a[136:139], a[24:27], v[72:75]\n" \
"buffer_load_dwordx4 v52, s[8:11], s64 offen offset:3072 sc1 lds\n" \
"v_mfma_f32_16x16x32_bf16 v[72:75], a[132:135], a[20:23], v[72:75]\n" \
"s_mov_b32 m0, s65\n" \
"v_cvt_pk_bf16_f32 v106, v108, v109\n" \
"v_mfma_f32_16x16x32_bf16 v[72:75], a[128:131], a[16:19], v[72:75]\n" \
"v_cvt_pk_bf16_f32 v107, v110, v111\n" \
"ds_read_b128 a[188:191], v49 offset:15360\n" \
"v_mfma_f32_16x16x32_bf16 v[80:83], a[128:131], a[32:35], v[40:43]\n" \
"v_exp_f32_e32 v118, v118\n" \
"v_mfma_f32_16x16x32_bf16 v[80:83], a[132:135], a[36:39], v[80:83]\n" \
"v_exp_f32_e32 v119, v119\n" \
"v_mfma_f32_16x16x32_bf16 v[80:83], a[136:139], a[40:43], v[80:83]\n" \
"buffer_load_dwordx4 v53, s[8:11], s64 offen offset:0 sc1 lds\n" \
"v_mfma_f32_16x16x32_bf16 v[80:83], a[140:143], a[44:47], v[80:83]\n" \
"v_exp_f32_e32 v116, v116\n" \
"v_mfma_f32_16x16x32_bf16 v[84:87], a[156:159], a[44:47], v[40:43]\n" \
"v_exp_f32_e32 v117, v117\n" \
"v_mfma_f32_16x16x32_bf16 v[84:87], a[152:155], a[40:43], v[84:87]\n" \
"v_cvt_pk_bf16_f32 v119, v118, v119\n" \
"v_cvt_pk_bf16_f32 v118, v116, v117\n" \
"v_mfma_f32_16x16x32_bf16 v[84:87], a[148:151], a[36:39], v[84:87]\n" \
"v_exp_f32_e32 v114, v114\n" \
"v_mfma_f32_16x16x32_bf16 v[84:87], a[144:147], a[32:35], v[84:87]\n" \
"buffer_load_dwordx4 v53, s[8:11], s64 offen offset:1024 sc1 lds\n" \
"v_mfma_f32_16x16x32_bf16 v[92:95], a[144:147], a[48:51], v[44:47]\n" \
"v_exp_f32_e32 v115, v115\n" \
"v_mfma_f32_16x16x32_bf16 v[92:95], a[148:151], a[52:55], v[92:95]\n" \
"v_exp_f32_e32 v112, v112\n" \
"v_mfma_f32_16x16x32_bf16 v[92:95], a[152:155], a[56:59], v[92:95]\n" \
"v_exp_f32_e32 v113, v113\n" \
"v_mfma_f32_16x16x32_bf16 v[92:95], a[156:159], a[60:63], v[92:95]\n" \
"buffer_load_dwordx4 v53, s[8:11], s64 offen offset:2048 sc1 lds\n" \
"v_mfma_f32_16x16x32_bf16 v[88:91], a[140:143], a[60:63], v[44:47]\n" \
"v_cvt_pk_bf16_f32 v117, v114, v115\n" \
"v_cvt_pk_bf16_f32 v116, v112, v113\n" \
"v_mfma_f32_16x16x32_bf16 v[88:91], a[136:139], a[56:59], v[88:91]\n" \
"v_exp_f32_e32 v120, v120\n" \
"v_mfma_f32_16x16x32_bf16 v[88:91], a[132:135], a[52:55], v[88:91]\n" \
"v_exp_f32_e32 v121, v121\n" \
"v_mfma_f32_16x16x32_bf16 v[88:91], a[128:131], a[48:51], v[88:91]\n" \
"v_cvt_pk_bf16_f32 v120, v120, v121\n" \
"s_waitcnt lgkmcnt(0)\n" \
"v_mfma_f32_16x16x32_bf16 v[128:131], a[160:163], v[100:103], v[128:131]\n" \
"buffer_load_dwordx4 v53, s[8:11], s64 offen offset:3072 sc1 lds\n" \
"v_mfma_f32_16x16x32_bf16 v[160:163], a[160:163], v[104:107], v[160:163]\n" \
"s_add_u32 m0, s93, 0x1000\n" \
"ds_read_b128 a[128:131], v48 offset:8192\n" \
"v_mfma_f32_16x16x32_bf16 v[132:135], a[164:167], v[100:103], v[132:135]\n" \
"v_exp_f32_e32 v122, v122\n" \
"v_mfma_f32_16x16x32_bf16 v[164:167], a[164:167], v[104:107], v[164:167]\n" \
"v_exp_f32_e32 v123, v123\n" \
"v_mfma_f32_16x16x32_bf16 v[136:139], a[168:171], v[100:103], v[136:139]\n" \
"buffer_load_dwordx4 v4, s[80:83], s92 offen offset:0 lds\n" \
"v_mfma_f32_16x16x32_bf16 v[168:171], a[168:171], v[104:107], v[168:171]\n" \
"v_exp_f32_e32 v124, v124\n" \
"v_mfma_f32_16x16x32_bf16 v[140:143], a[172:175], v[100:103], v[140:143]\n" \
"v_exp_f32_e32 v125, v125\n" \
"v_mfma_f32_16x16x32_bf16 v[172:175], a[172:175], v[104:107], v[172:175]\n" \
"v_exp_f32_e32 v126, v126\n" \
"v_mfma_f32_16x16x32_bf16 v[144:147], a[176:179], v[100:103], v[144:147]\n" \
"v_exp_f32_e32 v127, v127\n" \
"v_mfma_f32_16x16x32_bf16 v[176:179], a[176:179], v[104:107], v[176:179]\n" \
"v_cvt_pk_bf16_f32 v121, v122, v123\n" \
"v_cvt_pk_bf16_f32 v122, v124, v125\n" \
"v_mfma_f32_16x16x32_bf16 v[148:151], a[180:183], v[100:103], v[148:151]\n" \
"buffer_load_dwordx4 v5, s[80:83], s92 offen offset:1024 lds\n" \
"v_mfma_f32_16x16x32_bf16 v[180:183], a[180:183], v[104:107], v[180:183]\n" \
"v_cvt_pk_bf16_f32 v123, v126, v127\n" \
"ds_read_b128 a[144:147], v48 offset:12288\n" \
"v_mfma_f32_16x16x32_bf16 v[152:155], a[184:187], v[100:103], v[152:155]\n" \
"ds_read_b128 a[132:135], v48 offset:9216\n" \
"ds_read_b128 a[148:151], v48 offset:13312\n" \
"v_mfma_f32_16x16x32_bf16 v[184:187], a[184:187], v[104:107], v[184:187]\n" \
"ds_read_b128 a[136:139], v48 offset:10240\n" \
"ds_read_b128 a[152:155], v48 offset:14336\n" \
"v_mfma_f32_16x16x32_bf16 v[156:159], a[188:191], v[100:103], v[156:159]\n" \
"ds_read_b128 a[140:143], v48 offset:11264\n" \
"ds_read_b128 a[156:159], v48 offset:15360\n" \
"v_mfma_f32_16x16x32_bf16 v[188:191], a[188:191], v[104:107], v[188:191]\n" \
"buffer_load_dwordx4 v6, s[80:83], s92 offen offset:2048 lds\n" \
"v_smfmac_f32_16x16x64_bf16 a[192:195], a[200:203], v[100:107], v21\n" \
"v_exp_f32_e32 v70, v70\n" \
"v_mfma_f32_16x16x32_bf16 v[220:223], a[188:191], v[116:119], v[220:223]\n" \
"v_exp_f32_e32 v71, v71\n" \
"v_mfma_f32_16x16x32_bf16 v[252:255], a[188:191], v[120:123], v[252:255]\n" \
"buffer_load_dwordx4 v7, s[80:83], s92 offen offset:3072 lds\n" \
"v_mfma_f32_16x16x32_bf16 v[216:219], a[184:187], v[116:119], v[216:219]\n" \
"s_add_u32 m0, s93, 0x2000\n" \
"v_cvt_pk_bf16_f32 v71, v70, v71\n" \
"v_mfma_f32_16x16x32_bf16 v[248:251], a[184:187], v[120:123], v[248:251]\n" \
"v_exp_f32_e32 v72, v72\n" \
"v_mfma_f32_16x16x32_bf16 v[212:215], a[180:183], v[116:119], v[212:215]\n" \
"v_exp_f32_e32 v73, v73\n" \
"v_mfma_f32_16x16x32_bf16 v[244:247], a[180:183], v[120:123], v[244:247]\n" \
"v_exp_f32_e32 v68, v68\n" \
"v_mfma_f32_16x16x32_bf16 v[208:211], a[176:179], v[116:119], v[208:211]\n" \
"buffer_load_dwordx4 v8, s[80:83], s92 offen offset:0 lds\n" \
"v_mfma_f32_16x16x32_bf16 v[240:243], a[176:179], v[120:123], v[240:243]\n" \
"v_exp_f32_e32 v69, v69\n" \
"v_mfma_f32_16x16x32_bf16 v[204:207], a[172:175], v[116:119], v[204:207]\n" \
"v_exp_f32_e32 v74, v74\n" \
"v_mfma_f32_16x16x32_bf16 v[236:239], a[172:175], v[120:123], v[236:239]\n" \
"v_exp_f32_e32 v75, v75\n" \
"v_mfma_f32_16x16x32_bf16 v[200:203], a[168:171], v[116:119], v[200:203]\n" \
"v_exp_f32_e32 v66, v66\n" \
"v_mfma_f32_16x16x32_bf16 v[232:235], a[168:171], v[120:123], v[232:235]\n" \
"v_exp_f32_e32 v67, v67\n" \
"v_mfma_f32_16x16x32_bf16 v[196:199], a[164:167], v[116:119], v[196:199]\n" \
"v_exp_f32_e32 v64, v64\n" \
"v_mfma_f32_16x16x32_bf16 v[228:231], a[164:167], v[120:123], v[228:231]\n" \
"v_exp_f32_e32 v65, v65\n" \
"v_mfma_f32_16x16x32_bf16 v[192:195], a[160:163], v[116:119], v[192:195]\n" \
"v_cvt_pk_bf16_f32 v72, v72, v73\n" \
"v_cvt_pk_bf16_f32 v70, v68, v69\n" \
"v_mfma_f32_16x16x32_bf16 v[224:227], a[160:163], v[120:123], v[224:227]\n" \
"v_exp_f32_e32 v76, v76\n" \
"v_smfmac_f32_16x16x64_bf16 a[196:199], a[200:203], v[116:123], v21\n" \
"v_exp_f32_e32 v77, v77\n" \
"s_waitcnt vmcnt(14) lgkmcnt(0)\n" \
"s_barrier\n" \
"v_exp_f32_e32 v78, v78\n" \
"v_exp_f32_e32 v79, v79\n" \
"v_cvt_pk_bf16_f32 v73, v74, v75\n" \
"v_add_u32_e32 v49, 0x10000, v48\n" \
"v_cvt_pk_bf16_f32 v69, v66, v67\n" \
"v_cvt_pk_bf16_f32 v68, v64, v65\n" \
"v_cvt_pk_bf16_f32 v74, v76, v77\n" \
"v_cvt_pk_bf16_f32 v75, v78, v79\n" \
"v_add_u32_e32 v48, s16, v20\n" \
"s_xor_b32 s65, s65, 0x8000\n" \
"s_add_u32 s64, s64, 0x10000\n" \
"s_getpc_b64 s[86:87]\n" \
"L_hpt_%=:\n" \
"s_add_u32 s86, s86, L_ap5-L_hpt_%=\n" \
"s_addc_u32 s87, s87, 0\n" \
"s_add_u32 s86, s86, s15\n" \
"s_addc_u32 s87, s87, 0\n" \
"s_mov_b64 exec, 0\n" \
"s_swappc_b64 s[88:89], s[86:87]\n" \
"s_mov_b64 exec, -1\n" \
"s_add_u32 s86, s86, 0x2000\n" \
"s_addc_u32 s87, s87, 0\n" \
"s_mov_b64 exec, 0\n" \
"s_swappc_b64 s[88:89], s[86:87]\n" \
"s_mov_b64 exec, -1\n" \
".fill (L_ab + 6464 - .) / 4, 4, 0xBF800000\n" \
"L_2_%=:\n" \
"v_mfma_f32_16x16x32_bf16 v[96:99], a[128:131], a[64:67], v[32:35]\n" \
"s_xor_b32 m0, s65, 0x12000\n" \
"ds_read_b128 a[160:163], v49 offset:0\n" \
"v_mfma_f32_16x16x32_bf16 v[96:99], a[132:135], a[68:71], v[96:99]\n" \
"buffer_load_dwordx4 v50, s[8:11], s64 offen offset:0 sc1 lds\n" \
"v_mfma_f32_16x16x32_bf16 v[96:99], a[136:139], a[72:75], v[96:99]\n" \
"ds_read_b128 a[164:167], v49 offset:1024\n" \
"ds_read_b128 a[168:171], v49 offset:2048\n" \
"v_mfma_f32_16x16x32_bf16 v[96:99], a[140:143], a[76:79], v[96:99]\n" \
"ds_read_b128 a[172:175], v49 offset:3072\n" \
"ds_read_b128 a[176:179], v49 offset:4096\n" \
"v_mfma_f32_16x16x32_bf16 v[100:103], a[156:159], a[76:79], v[32:35]\n" \
"ds_read_b128 a[180:183], v49 offset:5120\n" \
"ds_read_b128 a[184:187], v49 offset:6144\n" \
"v_mfma_f32_16x16x32_bf16 v[100:103], a[152:155], a[72:75], v[100:103]\n" \
"v_exp_f32_e64 v86, v86\n" \
"v_mfma_f32_16x16x32_bf16 v[100:103], a[148:151], a[68:71], v[100:103]\n" \
"v_exp_f32_e64 v87, v87\n" \
"v_mfma_f32_16x16x32_bf16 v[100:103], a[144:147], a[64:67], v[100:103]\n" \
"buffer_load_dwordx4 v50, s[8:11], s64 offen offset:1024 sc1 lds\n" \
"v_mfma_f32_16x16x32_bf16 v[108:111], a[144:147], a[80:83], v[36:39]\n" \
"ds_read_b128 a[188:191], v49 offset:7168\n" \
"v_cvt_pk_bf16_f32 v87, v86, v87\n" \
"v_mfma_f32_16x16x32_bf16 v[108:111], a[148:151], a[84:87], v[108:111]\n" \
"v_exp_f32_e64 v84, v84\n" \
"v_mfma_f32_16x16x32_bf16 v[108:111], a[152:155], a[88:91], v[108:111]\n" \
"v_exp_f32_e64 v85, v85\n" \
"v_mfma_f32_16x16x32_bf16 v[108:111], a[156:159], a[92:95], v[108:111]\n" \
"v_exp_f32_e64 v82, v82\n" \
"v_mfma_f32_16x16x32_bf16 v[104:107], a[140:143], a[92:95], v[36:39]\n" \
"v_exp_f32_e64 v83, v83\n" \
"v_mfma_f32_16x16x32_bf16 v[104:107], a[136:139], a[88:91], v[104:107]\n" \
"v_exp_f32_e64 v80, v80\n" \
"v_mfma_f32_16x16x32_bf16 v[104:107], a[132:135], a[84:87], v[104:107]\n" \
"v_exp_f32_e64 v81, v81\n" \
"v_mfma_f32_16x16x32_bf16 v[104:107], a[128:131], a[80:83], v[104:107]\n" \
"buffer_load_dwordx4 v50, s[8:11], s64 offen offset:2048 sc1 lds\n" \
"v_mfma_f32_16x16x32_bf16 v[112:115], a[128:131], a[96:99], v[40:43]\n" \
"v_exp_f32_e64 v88, v88\n" \
"v_mfma_f32_16x16x32_bf16 v[112:115], a[132:135], a[100:103], v[112:115]\n" \
"v_exp_f32_e64 v89, v89\n" \
"v_mfma_f32_16x16x32_bf16 v[112:115], a[136:139], a[104:107], v[112:115]\n" \
"v_cvt_pk_bf16_f32 v86, v84, v85\n" \
"v_cvt_pk_bf16_f32 v85, v82, v83\n" \
"v_mfma_f32_16x16x32_bf16 v[112:115], a[140:143], a[108:111], v[112:115]\n" \
"v_cvt_pk_bf16_f32 v84, v80, v81\n" \
"v_cvt_pk_bf16_f32 v88, v88, v89\n" \
"v_mfma_f32_16x16x32_bf16 v[116:119], a[156:159], a[108:111], v[40:43]\n" \
"v_exp_f32_e64 v90, v90\n" \
"v_mfma_f32_16x16x32_bf16 v[116:119], a[152:155], a[104:107], v[116:119]\n" \
"v_exp_f32_e64 v91, v91\n" \
"v_mfma_f32_16x16x32_bf16 v[116:119], a[148:151], a[100:103], v[116:119]\n" \
"buffer_load_dwordx4 v50, s[8:11], s64 offen offset:3072 sc1 lds\n" \
"v_mfma_f32_16x16x32_bf16 v[116:119], a[144:147], a[96:99], v[116:119]\n" \
"s_xor_b32 m0, s65, 0xa000\n" \
"v_cvt_pk_bf16_f32 v89, v90, v91\n" \
"v_mfma_f32_16x16x32_bf16 v[124:127], a[144:147], a[112:115], v[44:47]\n" \
"v_exp_f32_e64 v92, v92\n" \
"v_mfma_f32_16x16x32_bf16 v[124:127], a[148:151], a[116:119], v[124:127]\n" \
"v_exp_f32_e64 v93, v93\n" \
"v_mfma_f32_16x16x32_bf16 v[124:127], a[152:155], a[120:123], v[124:127]\n" \
"v_exp_f32_e64 v94, v94\n" \
"v_mfma_f32_16x16x32_bf16 v[124:127], a[156:159], a[124:127], v[124:127]\n" \
"v_exp_f32_e64 v95, v95\n" \
"v_mfma_f32_16x16x32_bf16 v[120:123], a[140:143], a[124:127], v[44:47]\n" \
"v_cvt_pk_bf16_f32 v90, v92, v93\n" \
"v_cvt_pk_bf16_f32 v91, v94, v95\n" \
"v_mfma_f32_16x16x32_bf16 v[120:123], a[136:139], a[120:123], v[120:123]\n" \
"ds_read_b128 a[144:147], v48 offset:4096\n" \
"ds_read_b128 a[148:151], v48 offset:5120\n" \
"v_mfma_f32_16x16x32_bf16 v[120:123], a[132:135], a[116:119], v[120:123]\n" \
"buffer_load_dwordx4 v51, s[8:11], s64 offen offset:0 sc1 lds\n" \
"v_mfma_f32_16x16x32_bf16 v[120:123], a[128:131], a[112:115], v[120:123]\n" \
"ds_read_b128 a[128:131], v48 offset:0\n" \
"s_waitcnt lgkmcnt(3)\n" \
"v_mfma_f32_16x16x32_bf16 v[128:131], a[160:163], v[68:71], v[128:131]\n" \
"ds_read_b128 a[132:135], v48 offset:1024\n" \
"ds_read_b128 a[136:139], v48 offset:2048\n" \
"v_mfma_f32_16x16x32_bf16 v[160:163], a[160:163], v[72:75], v[160:163]\n" \
"ds_read_b128 a[152:155], v48 offset:6144\n" \
"ds_read_b128 a[140:143], v48 offset:3072\n" \
"v_mfma_f32_16x16x32_bf16 v[132:135], a[164:167], v[68:71], v[132:135]\n" \
"v_exp_f32_e32 v102, v102\n" \
"v_mfma_f32_16x16x32_bf16 v[164:167], a[164:167], v[72:75], v[164:167]\n" \
"v_exp_f32_e64 v103, v103\n" \
"v_mfma_f32_16x16x32_bf16 v[136:139], a[168:171], v[68:71], v[136:139]\n" \
"ds_read_b128 a[156:159], v48 offset:7168\n" \
"v_cvt_pk_bf16_f32 v103, v102, v103\n" \
"v_mfma_f32_16x16x32_bf16 v[168:171], a[168:171], v[72:75], v[168:171]\n" \
"buffer_load_dwordx4 v51, s[8:11], s64 offen offset:1024 sc1 lds\n" \
"v_mfma_f32_16x16x32_bf16 v[140:143], a[172:175], v[68:71], v[140:143]\n" \
"v_exp_f32_e64 v100, v100\n" \
"v_mfma_f32_16x16x32_bf16 v[172:175], a[172:175], v[72:75], v[172:175]\n" \
"v_exp_f32_e64 v101, v101\n" \
"v_mfma_f32_16x16x32_bf16 v[144:147], a[176:179], v[68:71], v[144:147]\n" \
"v_exp_f32_e64 v98, v98\n" \
"v_mfma_f32_16x16x32_bf16 v[176:179], a[176:179], v[72:75], v[176:179]\n" \
"v_exp_f32_e64 v99, v99\n" \
"v_mfma_f32_16x16x32_bf16 v[148:151], a[180:183], v[68:71], v[148:151]\n" \
"v_exp_f32_e64 v96, v96\n" \
"v_mfma_f32_16x16x32_bf16 v[180:183], a[180:183], v[72:75], v[180:183]\n" \
"v_exp_f32_e64 v97, v97\n" \
"v_mfma_f32_16x16x32_bf16 v[152:155], a[184:187], v[68:71], v[152:155]\n" \
"v_exp_f32_e64 v104, v104\n" \
"v_mfma_f32_16x16x32_bf16 v[184:187], a[184:187], v[72:75], v[184:187]\n" \
"buffer_load_dwordx4 v51, s[8:11], s64 offen offset:2048 sc1 lds\n" \
"v_mfma_f32_16x16x32_bf16 v[156:159], a[188:191], v[68:71], v[156:159]\n" \
"v_exp_f32_e64 v105, v105\n" \
"v_mfma_f32_16x16x32_bf16 v[188:191], a[188:191], v[72:75], v[188:191]\n" \
"v_cvt_pk_bf16_f32 v102, v100, v101\n" \
"v_cvt_pk_bf16_f32 v101, v98, v99\n" \
"v_smfmac_f32_16x16x64_bf16 a[192:195], a[200:203], v[68:75], v21\n" \
"v_cvt_pk_bf16_f32 v100, v96, v97\n" \
"v_cvt_pk_bf16_f32 v104, v104, v105\n" \
"v_mfma_f32_16x16x32_bf16 v[220:223], a[188:191], v[84:87], v[220:223]\n" \
"v_exp_f32_e64 v106, v106\n" \
"v_mfma_f32_16x16x32_bf16 v[252:255], a[188:191], v[88:91], v[252:255]\n" \
"v_exp_f32_e64 v107, v107\n" \
"v_mfma_f32_16x16x32_bf16 v[216:219], a[184:187], v[84:87], v[216:219]\n" \
"v_exp_f32_e64 v108, v108\n" \
"v_mfma_f32_16x16x32_bf16 v[248:251], a[184:187], v[88:91], v[248:251]\n" \
"buffer_load_dwordx4 v51, s[8:11], s64 offen offset:3072 sc1 lds\n" \
"v_mfma_f32_16x16x32_bf16 v[212:215], a[180:183], v[84:87], v[212:215]\n" \
"v_exp_f32_e64 v109, v109\n" \
"v_mfma_f32_16x16x32_bf16 v[244:247], a[180:183], v[88:91], v[244:247]\n" \
"v_exp_f32_e64 v110, v110\n" \
"v_mfma_f32_16x16x32_bf16 v[208:211], a[176:179], v[84:87], v[208:211]\n" \
"v_exp_f32_e64 v111, v111\n" \
"v_mfma_f32_16x16x32_bf16 v[240:243], a[176:179], v[88:91], v[240:243]\n" \
"v_exp_f32_e64 v118, v118\n" \
"v_mfma_f32_16x16x32_bf16 v[204:207], a[172:175], v[84:87], v[204:207]\n" \
"v_exp_f32_e64 v119, v119\n" \
"v_mfma_f32_16x16x32_bf16 v[236:239], a[172:175], v[88:91], v[236:239]\n" \
"v_cvt_pk_bf16_f32 v105, v106, v107\n" \
"v_cvt_pk_bf16_f32 v106, v108, v109\n" \
"v_mfma_f32_16x16x32_bf16 v[200:203], a[168:171], v[84:87], v[200:203]\n" \
"v_cvt_pk_bf16_f32 v107, v110, v111\n" \
"v_cvt_pk_bf16_f32 v119, v118, v119\n" \
"v_mfma_f32_16x16x32_bf16 v[232:235], a[168:171], v[88:91], v[232:235]\n" \
"v_exp_f32_e64 v116, v116\n" \
"v_mfma_f32_16x16x32_bf16 v[196:199], a[164:167], v[84:87], v[196:199]\n" \
"v_exp_f32_e64 v117, v117\n" \
"v_mfma_f32_16x16x32_bf16 v[228:231], a[164:167], v[88:91], v[228:231]\n" \
"v_exp_f32_e64 v114, v114\n" \
"v_mfma_f32_16x16x32_bf16 v[192:195], a[160:163], v[84:87], v[192:195]\n" \
"v_exp_f32_e64 v115, v115\n" \
"v_mfma_f32_16x16x32_bf16 v[224:227], a[160:163], v[88:91], v[224:227]\n" \
"v_exp_f32_e64 v112, v112\n" \
"v_smfmac_f32_16x16x64_bf16 a[196:199], a[200:203], v[84:91], v21\n" \
"v_exp_f32_e32 v113, v113\n" \
"s_waitcnt vmcnt(8) lgkmcnt(0)\n" \
"s_barrier\n" \
"s_nop 0\n" \
"v_mfma_f32_16x16x32_bf16 v[64:67], a[128:131], a[64:67], v[32:35]\n" \
"s_xor_b32 m0, s65, 0x18000\n" \
"ds_read_b128 a[160:163], v49 offset:8192\n" \
"v_mfma_f32_16x16x32_bf16 v[64:67], a[132:135], a[68:71], v[64:67]\n" \
"buffer_load_dwordx4 v52, s[8:11], s64 offen offset:0 sc1 lds\n" \
"v_mfma_f32_16x16x32_bf16 v[64:67], a[136:139], a[72:75], v[64:67]\n" \
"ds_read_b128 a[164:167], v49 offset:9216\n" \
"ds_read_b128 a[168:171], v49 offset:10240\n" \
"v_mfma_f32_16x16x32_bf16 v[64:67], a[140:143], a[76:79], v[64:67]\n" \
"ds_read_b128 a[172:175], v49 offset:11264\n" \
"ds_read_b128 a[176:179], v49 offset:12288\n" \
"v_mfma_f32_16x16x32_bf16 v[68:71], a[156:159], a[76:79], v[32:35]\n" \
"ds_read_b128 a[180:183], v49 offset:13312\n" \
"ds_read_b128 a[184:187], v49 offset:14336\n" \
"v_mfma_f32_16x16x32_bf16 v[68:71], a[152:155], a[72:75], v[68:71]\n" \
"ds_read_b128 a[188:191], v49 offset:15360\n" \
"v_cvt_pk_bf16_f32 v118, v116, v117\n" \
"v_mfma_f32_16x16x32_bf16 v[68:71], a[148:151], a[68:71], v[68:71]\n" \
"v_cvt_pk_bf16_f32 v117, v114, v115\n" \
"v_cvt_pk_bf16_f32 v116, v112, v113\n" \
"v_mfma_f32_16x16x32_bf16 v[68:71], a[144:147], a[64:67], v[68:71]\n" \
"buffer_load_dwordx4 v52, s[8:11], s64 offen offset:1024 sc1 lds\n" \
"v_mfma_f32_16x16x32_bf16 v[76:79], a[144:147], a[80:83], v[36:39]\n" \
"v_exp_f32_e64 v120, v120\n" \
"v_mfma_f32_16x16x32_bf16 v[76:79], a[148:151], a[84:87], v[76:79]\n" \
"v_exp_f32_e64 v121, v121\n" \
"v_mfma_f32_16x16x32_bf16 v[76:79], a[152:155], a[88:91], v[76:79]\n" \
"v_exp_f32_e64 v122, v122\n" \
"v_mfma_f32_16x16x32_bf16 v[76:79], a[156:159], a[92:95], v[76:79]\n" \
"v_exp_f32_e64 v123, v123\n" \
"v_mfma_f32_16x16x32_bf16 v[72:75], a[140:143], a[92:95], v[36:39]\n" \
"v_cvt_pk_bf16_f32 v120, v120, v121\n" \
"v_cvt_pk_bf16_f32 v121, v122, v123\n" \
"v_mfma_f32_16x16x32_bf16 v[72:75], a[136:139], a[88:91], v[72:75]\n" \
"v_exp_f32_e64 v124, v124\n" \
"v_mfma_f32_16x16x32_bf16 v[72:75], a[132:135], a[84:87], v[72:75]\n" \
"v_exp_f32_e64 v125, v125\n" \
"v_mfma_f32_16x16x32_bf16 v[72:75], a[128:131], a[80:83], v[72:75]\n" \
"buffer_load_dwordx4 v52, s[8:11], s64 offen offset:2048 sc1 lds\n" \
"v_mfma_f32_16x16x32_bf16 v[80:83], a[128:131], a[96:99], v[40:43]\n" \
"v_exp_f32_e64 v126, v126\n" \
"v_mfma_f32_16x16x32_bf16 v[80:83], a[132:135], a[100:103], v[80:83]\n" \
"v_exp_f32_e64 v127, v127\n" \
"v_mfma_f32_16x16x32_bf16 v[80:83], a[136:139], a[104:107], v[80:83]\n" \
"v_cvt_pk_bf16_f32 v122, v124, v125\n" \
"v_cvt_pk_bf16_f32 v123, v126, v127\n" \
"v_mfma_f32_16x16x32_bf16 v[80:83], a[140:143], a[108:111], v[80:83]\n" \
"v_exp_f32_e64 v70, v70\n" \
"v_mfma_f32_16x16x32_bf16 v[84:87], a[156:159], a[108:111], v[40:43]\n" \
"v_exp_f32_e64 v71, v71\n" \
"v_mfma_f32_16x16x32_bf16 v[84:87], a[152:155], a[104:107], v[84:87]\n" \
"v_exp_f32_e64 v72, v72\n" \
"v_mfma_f32_16x16x32_bf16 v[84:87], a[148:151], a[100:103], v[84:87]\n" \
"buffer_load_dwordx4 v52, s[8:11], s64 offen offset:3072 sc1 lds\n" \
"v_mfma_f32_16x16x32_bf16 v[84:87], a[144:147], a[96:99], v[84:87]\n" \
"s_mov_b32 m0, s65\n" \
"v_cvt_pk_bf16_f32 v71, v70, v71\n" \
"v_mfma_f32_16x16x32_bf16 v[92:95], a[144:147], a[112:115], v[44:47]\n" \
"ds_read_b128 a[144:147], v48 offset:12288\n" \
"v_add_u32_e32 v49, 0x10000, v48\n" \
"v_mfma_f32_16x16x32_bf16 v[92:95], a[148:151], a[116:119], v[92:95]\n" \
"ds_read_b128 a[148:151], v48 offset:13312\n" \
"s_xor_b32 s65, s65, 0x8000\n" \
"v_mfma_f32_16x16x32_bf16 v[92:95], a[152:155], a[120:123], v[92:95]\n" \
"v_exp_f32_e32 v73, v73\n" \
"v_mfma_f32_16x16x32_bf16 v[92:95], a[156:159], a[124:127], v[92:95]\n" \
"ds_read_b128 a[152:155], v48 offset:14336\n" \
".fill (L_ab + 8192 - .) / 4, 4, 0xBF800000\n" \
"L_ap2:\n" \
"s_cbranch_execz L_ap0\n" \
"ds_read_b128 a[156:159], v48 offset:15360\n" \
"v_mfma_f32_16x16x32_bf16 v[88:91], a[140:143], a[124:127], v[44:47]\n" \
"ds_read_b128 a[140:143], v48 offset:11264\n" \
"v_cvt_pk_bf16_f32 v72, v72, v73\n" \
"v_mfma_f32_16x16x32_bf16 v[88:91], a[136:139], a[120:123], v[88:91]\n" \
"v_exp_f32_e32 v68, v68\n" \
"v_mfma_f32_16x16x32_bf16 v[88:91], a[132:135], a[116:119], v[88:91]\n" \
"buffer_load_dwordx4 v53, s[8:11], s64 offen offset:0 sc1 lds\n" \
"v_mfma_f32_16x16x32_bf16 v[88:91], a[128:131], a[112:115], v[88:91]\n" \
"ds_read_b128 a[128:131], v48 offset:8192\n" \
"s_waitcnt lgkmcnt(6)\n" \
"v_mfma_f32_16x16x32_bf16 v[128:131], a[160:163], v[100:103], v[128:131]\n" \
"ds_read_b128 a[132:135], v48 offset:9216\n" \
"ds_read_b128 a[136:139], v48 offset:10240\n" \
"v_mfma_f32_16x16x32_bf16 v[160:163], a[160:163], v[104:107], v[160:163]\n" \
"v_exp_f32_e32 v69, v69\n" \
"v_mfma_f32_16x16x32_bf16 v[132:135], a[164:167], v[100:103], v[132:135]\n" \
"v_exp_f32_e64 v74, v74\n" \
"v_mfma_f32_16x16x32_bf16 v[164:167], a[164:167], v[104:107], v[164:167]\n" \
"v_exp_f32_e64 v75, v75\n" \
"v_mfma_f32_16x16x32_bf16 v[136:139], a[168:171], v[100:103], v[136:139]\n" \
"v_exp_f32_e64 v66, v66\n" \
"v_mfma_f32_16x16x32_bf16 v[168:171], a[168:171], v[104:107], v[168:171]\n" \
"buffer_load_dwordx4 v53, s[8:11], s64 offen offset:1024 sc1 lds\n" \
"v_mfma_f32_16x16x32_bf16 v[140:143], a[172:175], v[100:103], v[140:143]\n" \
"v_exp_f32_e64 v67, v67\n" \
"v_mfma_f32_16x16x32_bf16 v[172:175], a[172:175], v[104:107], v[172:175]\n" \
"v_exp_f32_e64 v64, v64\n" \
"v_mfma_f32_16x16x32_bf16 v[144:147], a[176:179], v[100:103], v[144:147]\n" \
"v_exp_f32_e64 v65, v65\n" \
"v_mfma_f32_16x16x32_bf16 v[176:179], a[176:179], v[104:107], v[176:179]\n" \
"v_exp_f32_e64 v76, v76\n" \
"v_mfma_f32_16x16x32_bf16 v[148:151], a[180:183], v[100:103], v[148:151]\n" \
"v_exp_f32_e64 v77, v77\n" \
"v_mfma_f32_16x16x32_bf16 v[180:183], a[180:183], v[104:107], v[180:183]\n" \
"v_exp_f32_e64 v78, v78\n" \
"v_mfma_f32_16x16x32_bf16 v[152:155], a[184:187], v[100:103], v[152:155]\n" \
"v_exp_f32_e64 v79, v79\n" \
"v_mfma_f32_16x16x32_bf16 v[184:187], a[184:187], v[104:107], v[184:187]\n" \
"buffer_load_dwordx4 v53, s[8:11], s64 offen offset:2048 sc1 lds\n" \
"v_mfma_f32_16x16x32_bf16 v[156:159], a[188:191], v[100:103], v[156:159]\n" \
"v_cvt_pk_bf16_f32 v70, v68, v69\n" \
"v_cvt_pk_bf16_f32 v73, v74, v75\n" \
"v_mfma_f32_16x16x32_bf16 v[188:191], a[188:191], v[104:107], v[188:191]\n" \
"v_cvt_pk_bf16_f32 v69, v66, v67\n" \
"v_cvt_pk_bf16_f32 v68, v64, v65\n" \
"v_smfmac_f32_16x16x64_bf16 a[192:195], a[200:203], v[100:107], v21\n" \
"v_cvt_pk_bf16_f32 v74, v76, v77\n" \
"v_cvt_pk_bf16_f32 v75, v78, v79\n" \
"v_mfma_f32_16x16x32_bf16 v[220:223], a[188:191], v[116:119], v[220:223]\n" \
"v_xor_b32_e32 v48, 0x8000, v48\n" \
"v_mfma_f32_16x16x32_bf16 v[252:255], a[188:191], v[120:123], v[252:255]\n" \
"v_mfma_f32_16x16x32_bf16 v[216:219], a[184:187], v[116:119], v[216:219]\n" \
"v_mfma_f32_16x16x32_bf16 v[248:251], a[184:187], v[120:123], v[248:251]\n" \
"buffer_load_dwordx4 v53, s[8:11], s64 offen offset:3072 sc1 lds\n" \
"v_mfma_f32_16x16x32_bf16 v[212:215], a[180:183], v[116:119], v[212:215]\n" \
"s_add_u32 s64, s64, 0x10000\n" \
"s_sub_u32 s66, s66, 1\n" \
"s_nop 0\n" \
"v_mfma_f32_16x16x32_bf16 v[244:247], a[180:183], v[120:123], v[244:247]\n" \
"v_mfma_f32_16x16x32_bf16 v[208:211], a[176:179], v[116:119], v[208:211]\n" \
"v_mfma_f32_16x16x32_bf16 v[240:243], a[176:179], v[120:123], v[240:243]\n" \
"v_mfma_f32_16x16x32_bf16 v[204:207], a[172:175], v[116:119], v[204:207]\n" \
"v_mfma_f32_16x16x32_bf16 v[236:239], a[172:175], v[120:123], v[236:239]\n" \
"v_mfma_f32_16x16x32_bf16 v[200:203], a[168:171], v[116:119], v[200:203]\n" \
"v_mfma_f32_16x16x32_bf16 v[232:235], a[168:171], v[120:123], v[232:235]\n" \
"v_mfma_f32_16x16x32_bf16 v[196:199], a[164:167], v[116:119], v[196:199]\n" \
"v_mfma_f32_16x16x32_bf16 v[228:231], a[164:167], v[120:123], v[228:231]\n" \
"v_mfma_f32_16x16x32_bf16 v[192:195], a[160:163], v[116:119], v[192:195]\n" \
"v_mfma_f32_16x16x32_bf16 v[224:227], a[160:163], v[120:123], v[224:227]\n" \
"v_smfmac_f32_16x16x64_bf16 a[196:199], a[200:203], v[116:123], v21\n" \
"s_waitcnt vmcnt(8) lgkmcnt(0)\n" \
"s_barrier\n" \
"s_cbranch_scc0 L_2_%=\n" \
"s_cmp_lg_u32 s16, 0\n" \
"s_cbranch_scc1 L_f1_%=\n" \
"v_bfe_u32 v58, v20, 2, 7\n" \
"v_readlane_b32 s96, v11, 1\n" \
"ds_read_b128 a[160:163], v49 offset:0\n" \
"ds_read_b128 a[164:167], v49 offset:1024\n" \
"ds_read_b128 a[168:171], v49 offset:2048\n" \
"v_mfma_f32_16x16x32_bf16 v[120:123], a[128:131], a[112:115], v[44:47]\n" \
"v_readlane_b32 s93, v11, 20\n" \
"ds_read_b128 a[172:175], v49 offset:3072\n" \
"buffer_load_dword v55, v58, s[56:59], s96 offen sc1\n" \
"v_readlane_b32 s84, v11, 15\n" \
"v_readlane_b32 s85, v11, 16\n" \
"v_readlane_b32 s86, v11, 17\n" \
"s_mov_b32 s87, 0x20000\n" \
"v_readlane_b32 s92, v11, 18\n" \
"s_add_u32 m0, s93, 0x0\n" \
"v_mfma_f32_16x16x32_bf16 v[120:123], a[132:135], a[116:119], v[120:123]\n" \
"ds_read_b128 a[176:179], v49 offset:4096\n" \
"v_add_u32_e32 v54, 0x49000, v20\n" \
"v_mfma_f32_16x16x32_bf16 v[120:123], a[136:139], a[120:123], v[120:123]\n" \
"v_readlane_b32 s97, v11, 35\n" \
"v_readlane_b32 s98, v11, 36\n" \
"v_mfma_f32_16x16x32_bf16 v[120:123], a[140:143], a[124:127], v[120:123]\n" \
"ds_read_b128 a[180:183], v49 offset:5120\n" \
"ds_read_b128 a[184:187], v49 offset:6144\n" \
"v_mfma_f32_16x16x32_bf16 v[124:127], a[156:159], a[124:127], v[44:47]\n" \
"ds_read_b128 a[188:191], v49 offset:7168\n" \
"v_add_u32_e32 v50, s15, v20\n" \
"buffer_load_dwordx4 v0, s[84:87], s92 offen offset:0 lds\n" \
"v_mfma_f32_16x16x32_bf16 v[124:127], a[152:155], a[120:123], v[124:127]\n" \
"v_exp_f32_e32 v86, v86\n" \
"v_mfma_f32_16x16x32_bf16 v[124:127], a[148:151], a[116:119], v[124:127]\n" \
"v_exp_f32_e32 v87, v87\n" \
"v_mfma_f32_16x16x32_bf16 v[124:127], a[144:147], a[112:115], v[124:127]\n" \
"v_cvt_pk_bf16_f32 v87, v86, v87\n" \
"s_waitcnt lgkmcnt(7)\n" \
"v_mfma_f32_16x16x32_bf16 v[128:131], a[160:163], v[68:71], v[128:131]\n" \
"v_exp_f32_e32 v84, v84\n" \
"v_mfma_f32_16x16x32_bf16 v[160:163], a[160:163], v[72:75], v[160:163]\n" \
"v_and_b32_e32 v87, v15, v87\n" \
"s_waitcnt lgkmcnt(6)\n" \
"v_mfma_f32_16x16x32_bf16 v[132:135], a[164:167], v[68:71], v[132:135]\n" \
"buffer_load_dwordx4 v1, s[84:87], s92 offen offset:1024 lds\n" \
"v_mfma_f32_16x16x32_bf16 v[164:167], a[164:167], v[72:75], v[164:167]\n" \
"v_add_u32_e32 v51, 0x10000, v50\n" \
"s_waitcnt lgkmcnt(5)\n" \
"v_mfma_f32_16x16x32_bf16 v[136:139], a[168:171], v[68:71], v[136:139]\n" \
"v_exp_f32_e32 v85, v85\n" \
"v_mfma_f32_16x16x32_bf16 v[168:171], a[168:171], v[72:75], v[168:171]\n" \
"v_cvt_pk_bf16_f32 v86, v84, v85\n" \
"s_waitcnt lgkmcnt(4)\n" \
"v_mfma_f32_16x16x32_bf16 v[140:143], a[172:175], v[68:71], v[140:143]\n" \
"v_exp_f32_e32 v82, v82\n" \
"v_mfma_f32_16x16x32_bf16 v[172:175], a[172:175], v[72:75], v[172:175]\n" \
"v_and_b32_e32 v86, v14, v86\n" \
"s_waitcnt lgkmcnt(3)\n" \
"v_mfma_f32_16x16x32_bf16 v[144:147], a[176:179], v[68:71], v[144:147]\n" \
"buffer_load_dwordx4 v2, s[84:87], s92 offen offset:2048 lds\n" \
"v_mfma_f32_16x16x32_bf16 v[176:179], a[176:179], v[72:75], v[176:179]\n" \
"v_cvt_pk_bf16_f32 v128, v128, v129\n" \
"s_waitcnt lgkmcnt(1)\n" \
"v_mfma_f32_16x16x32_bf16 v[148:151], a[180:183], v[68:71], v[148:151]\n" \
"v_exp_f32_e32 v83, v83\n" \
"v_mfma_f32_16x16x32_bf16 v[180:183], a[180:183], v[72:75], v[180:183]\n" \
"v_exp_f32_e32 v80, v80\n" \
"v_mfma_f32_16x16x32_bf16 v[152:155], a[184:187], v[68:71], v[152:155]\n" \
"v_exp_f32_e32 v81, v81\n" \
"v_mfma_f32_16x16x32_bf16 v[184:187], a[184:187], v[72:75], v[184:187]\n" \
"v_cvt_pk_bf16_f32 v85, v82, v83\n" \
"s_waitcnt lgkmcnt(0)\n" \
"v_mfma_f32_16x16x32_bf16 v[156:159], a[188:191], v[68:71], v[156:159]\n" \
"buffer_load_dwordx4 v3, s[84:87], s92 offen offset:3072 lds\n" \
"s_add_u32 m0, s93, 0x1000\n" \
"v_mfma_f32_16x16x32_bf16 v[188:191], a[188:191], v[72:75], v[188:191]\n" \
"v_cvt_pk_bf16_f32 v84, v80, v81\n" \
"v_and_b32_e32 v84, v12, v84\n" \
"v_smfmac_f32_16x16x64_bf16 a[192:195], a[200:203], v[68:75], v21\n" \
"v_and_b32_e32 v85, v13, v85\n" \
"v_exp_f32_e32 v88, v88\n" \
"v_exp_f32_e32 v89, v89\n" \
"v_mfma_f32_16x16x32_bf16 v[220:223], a[188:191], v[84:87], v[220:223]\n" \
"v_exp_f32_e32 v90, v90\n" \
"v_exp_f32_e32 v91, v91\n" \
"v_cvt_pk_bf16_f32 v88, v88, v89\n" \
"v_exp_f32_e32 v92, v92\n" \
"buffer_load_dwordx4 v4, s[84:87], s92 offen offset:0 lds\n" \
"v_exp_f32_e32 v93, v93\n" \
"v_exp_f32_e32 v94, v94\n" \
"v_exp_f32_e32 v95, v95\n" \
"v_cvt_pk_bf16_f32 v160, v160, v161\n" \
"v_cvt_pk_bf16_f32 v89, v90, v91\n" \
"v_cvt_pk_bf16_f32 v129, v130, v131\n" \
"v_cvt_pk_bf16_f32 v161, v162, v163\n" \
"v_cvt_pk_bf16_f32 v90, v92, v93\n" \
"v_cvt_pk_bf16_f32 v91, v94, v95\n" \
"v_cvt_pk_bf16_f32 v130, v132, v133\n" \
"v_cvt_pk_bf16_f32 v131, v134, v135\n" \
"v_cvt_pk_bf16_f32 v162, v164, v165\n" \
"v_cvt_pk_bf16_f32 v163, v166, v167\n" \
"v_mfma_f32_16x16x32_bf16 v[252:255], a[188:191], v[88:91], v[252:255]\n" \
"v_cvt_pk_bf16_f32 v132, v136, v137\n" \
"v_cvt_pk_bf16_f32 v133, v138, v139\n" \
"v_cvt_pk_bf16_f32 v134, v140, v141\n" \
"buffer_load_dwordx4 v5, s[84:87], s92 offen offset:1024 lds\n" \
"v_cvt_pk_bf16_f32 v135, v142, v143\n" \
"v_cvt_pk_bf16_f32 v164, v168, v169\n" \
"v_cvt_pk_bf16_f32 v165, v170, v171\n" \
"v_cvt_pk_bf16_f32 v166, v172, v173\n" \
"v_cvt_pk_bf16_f32 v167, v174, v175\n" \
"v_cvt_pk_bf16_f32 v136, v144, v145\n" \
"v_cvt_pk_bf16_f32 v137, v146, v147\n" \
"v_cvt_pk_bf16_f32 v138, v148, v149\n" \
"v_cvt_pk_bf16_f32 v139, v150, v151\n" \
"v_cvt_pk_bf16_f32 v140, v152, v153\n" \
"v_cvt_pk_bf16_f32 v141, v154, v155\n" \
"v_cvt_pk_bf16_f32 v142, v156, v157\n" \
"v_cvt_pk_bf16_f32 v143, v158, v159\n" \
"v_cvt_pk_bf16_f32 v168, v176, v177\n" \
"v_cvt_pk_bf16_f32 v169, v178, v179\n" \
"v_cvt_pk_bf16_f32 v170, v180, v181\n" \
"v_cvt_pk_bf16_f32 v171, v182, v183\n" \
"v_cvt_pk_bf16_f32 v172, v184, v185\n" \
"v_cvt_pk_bf16_f32 v173, v186, v187\n" \
"v_cvt_pk_bf16_f32 v174, v188, v189\n" \
"v_cvt_pk_bf16_f32 v175, v190, v191\n" \
"buffer_load_dwordx4 v6, s[84:87], s92 offen offset:2048 lds\n" \
"s_waitcnt vmcnt(8)\n" \
"ds_write_b128 v50, v[128:131] offset:0\n" \
"v_mfma_f32_16x16x32_bf16 v[216:219], a[184:187], v[84:87], v[216:219]\n" \
"ds_read_b32 v60, v18 offset:0\n" \
"ds_read_b32 v61, v18 offset:128\n" \
"v_mfma_f32_16x16x32_bf16 v[248:251], a[184:187], v[88:91], v[248:251]\n" \
"ds_write_b128 v50, v[132:135] offset:1024\n" \
"s_waitcnt vmcnt(7)\n" \
"v_cmp_ne_u32_e32 vcc, s62, v55\n" \
"v_mfma_f32_16x16x32_bf16 v[212:215], a[180:183], v[84:87], v[212:215]\n" \
"ds_read_b128 a[0:3], v49 offset:8192\n" \
"ds_read_b128 a[4:7], v49 offset:9216\n" \
"ds_write_b128 v50, v[136:139] offset:2048\n" \
"v_mfma_f32_16x16x32_bf16 v[244:247], a[180:183], v[88:91], v[244:247]\n" \
"buffer_load_dwordx4 v7, s[84:87], s92 offen offset:3072 lds\n" \
"s_add_u32 m0, s93, 0x2000\n" \
"s_cbranch_vccnz L_so0_%=\n" \
"L_ro0_%=:\n" \
"ds_write_b128 v50, v[140:143] offset:3072\n" \
"ds_read_b128 a[8:11], v49 offset:10240\n" \
"ds_read_b128 a[12:15], v49 offset:11264\n" \
"ds_read_b128 a[16:19], v49 offset:12288\n" \
"ds_read_b128 a[20:23], v49 offset:13312\n" \
"ds_read_b128 a[24:27], v49 offset:14336\n" \
"ds_read_b128 a[28:31], v49 offset:15360\n" \
"ds_write_b128 v51, v[160:163] offset:32768\n" \
"v_mfma_f32_16x16x32_bf16 v[208:211], a[176:179], v[84:87], v[208:211]\n" \
"v_accvgpr_read_b32 v40, a192\n" \
"v_accvgpr_read_b32 v22, a193\n" \
"v_mfma_f32_16x16x32_bf16 v[240:243], a[176:179], v[88:91], v[240:243]\n" \
"ds_write_b128 v51, v[164:167] offset:33792\n" \
"v_mfma_f32_16x16x32_bf16 v[204:207], a[172:175], v[84:87], v[204:207]\n" \
"buffer_load_dwordx4 v8, s[84:87], s92 offen offset:0 lds\n" \
"v_mfma_f32_16x16x32_bf16 v[236:239], a[172:175], v[88:91], v[236:239]\n" \
"ds_write_b128 v51, v[168:171] offset:34816\n" \
"v_mfma_f32_16x16x32_bf16 v[200:203], a[168:171], v[84:87], v[200:203]\n" \
"v_accvgpr_read_b32 v41, a194\n" \
"v_accvgpr_read_b32 v23, a195\n" \
"v_mfma_f32_16x16x32_bf16 v[232:235], a[168:171], v[88:91], v[232:235]\n" \
"ds_write_b128 v51, v[172:175] offset:35840\n" \
"v_mfma_f32_16x16x32_bf16 v[196:199], a[164:167], v[84:87], v[196:199]\n" \
"v_add_f32_e32 v40, v40, v22\n" \
"v_add_f32_e32 v41, v41, v23\n" \
"v_mfma_f32_16x16x32_bf16 v[228:231], a[164:167], v[88:91], v[228:231]\n" \
"ds_write_b64 v19, v[40:41]\n" \
"v_lshl_add_u32 v52, s15, 2, v20\n" \
"v_mfma_f32_16x16x32_bf16 v[192:195], a[160:163], v[84:87], v[192:195]\n" \
"buffer_load_dwordx4 a[220:223], v20, s[56:59], s60 offen offset:0 sc1\n" \
"v_mfma_f32_16x16x32_bf16 v[224:227], a[160:163], v[88:91], v[224:227]\n" \
"v_add_u32_e32 v52, 0x1a000, v52\n" \
"v_xor_b32_e32 v63, 0x400, v19\n" \
"v_smfmac_f32_16x16x64_bf16 a[196:199], a[200:203], v[84:91], v21\n" \
"v_exp_f32_e32 v120, v120\n" \
"v_exp_f32_e32 v121, v121\n" \
"v_exp_f32_e32 v122, v122\n" \
"v_exp_f32_e32 v123, v123\n" \
"v_cvt_pk_bf16_f32 v120, v120, v121\n" \
"v_exp_f32_e32 v124, v124\n" \
"v_exp_f32_e32 v125, v125\n" \
"v_exp_f32_e32 v126, v126\n" \
"buffer_load_dwordx4 a[236:239], v54, s[56:59], s60 offen offset:0 sc1\n" \
"v_exp_f32_e32 v127, v127\n" \
"v_cvt_pk_bf16_f32 v121, v122, v123\n" \
"v_cvt_pk_bf16_f32 v122, v124, v125\n" \
"v_cvt_pk_bf16_f32 v123, v126, v127\n" \
"v_and_b32_e32 v120, v12, v120\n" \
"v_and_b32_e32 v121, v13, v121\n" \
"v_and_b32_e32 v122, v14, v122\n" \
"v_and_b32_e32 v123, v15, v123\n" \
"s_waitcnt lgkmcnt(0)\n" \
"s_barrier\n" \
"ds_read_b128 v[144:147], v52 offset:0\n" \
"ds_read_b128 v[148:151], v52 offset:1024\n" \
"ds_read_b128 v[152:155], v52 offset:2048\n" \
"ds_read_b128 v[156:159], v52 offset:3072\n" \
"buffer_load_dwordx4 a[224:227], v20, s[56:59], s60 offen offset:1024 sc1\n" \
"ds_read_b128 v[176:179], v52 offset:4096\n" \
"ds_read_b128 v[180:183], v52 offset:5120\n" \
"ds_read_b128 v[184:187], v52 offset:6144\n" \
"ds_read_b128 v[188:191], v52 offset:7168\n" \
"v_mfma_f32_16x16x32_bf16 a[196:199], a[212:215], v[120:123], a[196:199]\n" \
"ds_read_b64 v[42:43], v63\n" \
"v_mov_b32_e32 v130, v28\n" \
"v_mfma_f32_16x16x32_bf16 v[224:227], a[0:3], v[120:123], v[224:227]\n" \
"v_mov_b32_e32 v131, v29\n" \
"v_mov_b32_e32 v132, 0\n" \
"v_mfma_f32_16x16x32_bf16 v[228:231], a[4:7], v[120:123], v[228:231]\n" \
"v_mov_b32_e32 v133, 0\n" \
"v_mov_b32_e32 v128, 0\n" \
"v_mfma_f32_16x16x32_bf16 v[232:235], a[8:11], v[120:123], v[232:235]\n" \
"v_mov_b32_e32 v129, 0\n" \
"v_accvgpr_read_b32 v56, a196\n" \
"v_mfma_f32_16x16x32_bf16 v[236:239], a[12:15], v[120:123], v[236:239]\n" \
"buffer_load_dwordx4 a[240:243], v54, s[56:59], s60 offen offset:1024 sc1\n" \
"v_mfma_f32_16x16x32_bf16 v[240:243], a[16:19], v[120:123], v[240:243]\n" \
"v_accvgpr_read_b32 v24, a197\n" \
"v_add_f32_e32 v56, v56, v24\n" \
"v_mfma_f32_16x16x32_bf16 v[244:247], a[20:23], v[120:123], v[244:247]\n" \
"v_accvgpr_read_b32 v57, a198\n" \
"v_accvgpr_read_b32 v25, a199\n" \
"v_mfma_f32_16x16x32_bf16 v[248:251], a[24:27], v[120:123], v[248:251]\n" \
"v_add_f32_e32 v57, v57, v25\n" \
"v_mfma_f32_16x16x32_bf16 v[252:255], a[28:31], v[120:123], v[252:255]\n" \
"s_waitcnt lgkmcnt(1)\n" \
"s_barrier\n" \
"buffer_load_dwordx4 a[228:231], v20, s[56:59], s60 offen offset:2048 sc1\n" \
"v_mfma_f32_16x16x32_bf16 v[192:195], v[130:133], v[144:147], v[192:195]\n" \
"s_waitcnt lgkmcnt(0)\n" \
"v_add_f32_e32 v56, v56, v42\n" \
"v_mfma_f32_16x16x32_bf16 v[196:199], v[128:131], v[144:147], v[196:199]\n" \
"v_max_f32_e32 v62, v62, v56\n" \
"v_add_f32_e32 v57, v57, v43\n" \
"v_mfma_f32_16x16x32_bf16 v[200:203], v[130:133], v[148:151], v[200:203]\n" \
"v_rcp_f32_e32 v56, v56\n" \
"v_mfma_f32_16x16x32_bf16 v[204:207], v[128:131], v[148:151], v[204:207]\n" \
"v_mul_f32_e32 v56, v56, v60\n" \
"v_mul_f32_e32 v192, v56, v192\n" \
"v_mfma_f32_16x16x32_bf16 v[208:211], v[130:133], v[152:155], v[208:211]\n" \
"v_mul_f32_e32 v193, v56, v193\n" \
"v_mul_f32_e32 v194, v56, v194\n" \
"v_mfma_f32_16x16x32_bf16 v[212:215], v[128:131], v[152:155], v[212:215]\n" \
"buffer_load_dwordx4 a[244:247], v54, s[56:59], s60 offen offset:2048 sc1\n" \
"v_mfma_f32_16x16x32_bf16 v[216:219], v[130:133], v[156:159], v[216:219]\n" \
"v_mul_f32_e32 v195, v56, v195\n" \
"v_mul_f32_e32 v196, v56, v196\n" \
"v_mfma_f32_16x16x32_bf16 v[220:223], v[128:131], v[156:159], v[220:223]\n" \
"v_mul_f32_e32 v197, v56, v197\n" \
"v_mul_f32_e32 v198, v56, v198\n" \
"v_mfma_f32_16x16x32_bf16 v[224:227], v[130:133], v[176:179], v[224:227]\n" \
"v_mul_f32_e32 v199, v56, v199\n" \
"v_cvt_pk_bf16_f32 v64, v192, v193\n" \
"v_mfma_f32_16x16x32_bf16 v[228:231], v[128:131], v[176:179], v[228:231]\n" \
"v_cvt_pk_bf16_f32 v65, v194, v195\n" \
"v_cvt_pk_bf16_f32 v66, v196, v197\n" \
"v_mfma_f32_16x16x32_bf16 v[232:235], v[130:133], v[180:183], v[232:235]\n" \
"v_cvt_pk_bf16_f32 v67, v198, v199\n" \
"v_mul_f32_e32 v200, v56, v200\n" \
"v_mfma_f32_16x16x32_bf16 v[236:239], v[128:131], v[180:183], v[236:239]\n" \
"buffer_load_dwordx4 a[232:235], v20, s[56:59], s60 offen offset:3072 sc1\n" \
"v_mfma_f32_16x16x32_bf16 v[240:243], v[130:133], v[184:187], v[240:243]\n" \
"v_mfma_f32_16x16x32_bf16 a[208:211], a[216:219], v[64:67], a[208:211]\n" \
"v_mul_f32_e32 v201, v56, v201\n" \
"v_mul_f32_e32 v202, v56, v202\n" \
"v_mul_f32_e32 v203, v56, v203\n" \
"v_mul_f32_e32 v204, v56, v204\n" \
"v_mul_f32_e32 v205, v56, v205\n" \
"v_mul_f32_e32 v206, v56, v206\n" \
"v_mul_f32_e32 v207, v56, v207\n" \
"v_cvt_pk_bf16_f32 v68, v200, v201\n" \
"v_cvt_pk_bf16_f32 v69, v202, v203\n" \
"v_cvt_pk_bf16_f32 v70, v204, v205\n" \
"v_cvt_pk_bf16_f32 v71, v206, v207\n" \
"v_mul_f32_e32 v208, v56, v208\n" \
"v_mul_f32_e32 v209, v56, v209\n" \
"v_mfma_f32_16x16x32_bf16 a[208:211], a[216:219], v[68:71], a[208:211]\n" \
"buffer_load_dwordx4 a[248:251], v54, s[56:59], s60 offen offset:3072 sc1\n" \
"v_mul_f32_e32 v210, v56, v210\n" \
"v_mul_f32_e32 v211, v56, v211\n" \
"v_mul_f32_e32 v212, v56, v212\n" \
"v_mul_f32_e32 v213, v56, v213\n" \
"v_mul_f32_e32 v214, v56, v214\n" \
"v_mul_f32_e32 v215, v56, v215\n" \
"v_max_f32_e32 v62, v62, v57\n" \
"v_cvt_pk_bf16_f32 v72, v208, v209\n" \
"v_cvt_pk_bf16_f32 v73, v210, v211\n" \
"v_cvt_pk_bf16_f32 v74, v212, v213\n" \
"v_cvt_pk_bf16_f32 v75, v214, v215\n" \
"v_rcp_f32_e32 v57, v57\n" \
"v_mfma_f32_16x16x32_bf16 v[244:247], v[128:131], v[184:187], v[244:247]\n" \
"v_mfma_f32_16x16x32_bf16 a[208:211], a[216:219], v[72:75], a[208:211]\n" \
"v_mul_f32_e32 v216, v56, v216\n" \
"v_mul_f32_e32 v217, v56, v217\n" \
"buffer_load_dwordx4 a[96:99], v20, s[56:59], s97 offen offset:0 sc1\n" \
"v_mul_f32_e32 v218, v56, v218\n" \
"v_mul_f32_e32 v219, v56, v219\n" \
"v_mul_f32_e32 v220, v56, v220\n" \
"v_mul_f32_e32 v221, v56, v221\n" \
"v_mul_f32_e32 v222, v56, v222\n" \
"v_mul_f32_e32 v223, v56, v223\n" \
"v_cvt_pk_bf16_f32 v76, v216, v217\n" \
"v_cvt_pk_bf16_f32 v77, v218, v219\n" \
"v_cvt_pk_bf16_f32 v78, v220, v221\n" \
"v_cvt_pk_bf16_f32 v79, v222, v223\n" \
"v_mul_f32_e32 v57, v57, v61\n" \
"v_mul_f32_e32 v224, v57, v224\n" \
"v_mfma_f32_16x16x32_bf16 a[208:211], a[216:219], v[76:79], a[208:211]\n" \
"v_mul_f32_e32 v225, v57, v225\n" \
"v_mul_f32_e32 v226, v57, v226\n" \
"v_mul_f32_e32 v227, v57, v227\n" \
"v_mul_f32_e32 v228, v57, v228\n" \
"v_mul_f32_e32 v229, v57, v229\n" \
"v_mul_f32_e32 v230, v57, v230\n" \
"v_mul_f32_e32 v231, v57, v231\n" \
"buffer_load_dwordx4 a[104:107], v54, s[56:59], s97 offen offset:0 sc1\n" \
"v_cvt_pk_bf16_f32 v80, v224, v225\n" \
"v_cvt_pk_bf16_f32 v81, v226, v227\n" \
"v_cvt_pk_bf16_f32 v82, v228, v229\n" \
"v_cvt_pk_bf16_f32 v83, v230, v231\n" \
"v_mfma_f32_16x16x32_bf16 v[248:251], v[130:133], v[188:191], v[248:251]\n" \
"v_mul_f32_e32 v232, v57, v232\n" \
"v_mul_f32_e32 v233, v57, v233\n" \
"v_mfma_f32_16x16x32_bf16 a[208:211], a[216:219], v[80:83], a[208:211]\n" \
"v_mul_f32_e32 v234, v57, v234\n" \
"v_mul_f32_e32 v235, v57, v235\n" \
"v_mul_f32_e32 v236, v57, v236\n" \
"v_mul_f32_e32 v237, v57, v237\n" \
"v_mul_f32_e32 v238, v57, v238\n" \
"v_mul_f32_e32 v239, v57, v239\n" \
"v_cvt_pk_bf16_f32 v84, v232, v233\n" \
"v_cvt_pk_bf16_f32 v85, v234, v235\n" \
"v_cvt_pk_bf16_f32 v86, v236, v237\n" \
"v_cvt_pk_bf16_f32 v87, v238, v239\n" \
"v_mul_f32_e32 v240, v57, v240\n" \
"buffer_load_dwordx4 a[112:115], v20, s[56:59], s98 offen offset:0 sc1\n" \
"v_mfma_f32_16x16x32_bf16 a[208:211], a[216:219], v[84:87], a[208:211]\n" \
"v_mul_f32_e32 v241, v57, v241\n" \
"v_mul_f32_e32 v242, v57, v242\n" \
"v_mul_f32_e32 v243, v57, v243\n" \
"v_mul_f32_e32 v244, v57, v244\n" \
"v_mul_f32_e32 v245, v57, v245\n" \
"v_mul_f32_e32 v246, v57, v246\n" \
"v_mul_f32_e32 v247, v57, v247\n" \
"v_cvt_pk_bf16_f32 v96, v240, v241\n" \
"v_cvt_pk_bf16_f32 v97, v242, v243\n" \
"v_cvt_pk_bf16_f32 v98, v244, v245\n" \
"v_cvt_pk_bf16_f32 v99, v246, v247\n" \
"v_mfma_f32_16x16x32_bf16 v[252:255], v[128:131], v[188:191], v[252:255]\n" \
"v_mul_f32_e32 v248, v57, v248\n" \
"v_mfma_f32_16x16x32_bf16 a[208:211], a[216:219], v[96:99], a[208:211]\n" \
"v_mul_f32_e32 v249, v57, v249\n" \
"v_mul_f32_e32 v250, v57, v250\n" \
"buffer_load_dwordx4 a[120:123], v54, s[56:59], s98 offen offset:0 sc1\n" \
"v_mul_f32_e32 v251, v57, v251\n" \
"v_cvt_pk_bf16_f32 v100, v248, v249\n" \
"v_cvt_pk_bf16_f32 v101, v250, v251\n" \
"v_mul_f32_e32 v252, v57, v252\n" \
"v_mul_f32_e32 v253, v57, v253\n" \
"v_mul_f32_e32 v254, v57, v254\n" \
"v_mul_f32_e32 v255, v57, v255\n" \
"v_cvt_pk_bf16_f32 v102, v252, v253\n" \
"v_cvt_pk_bf16_f32 v103, v254, v255\n" \
"v_cmp_lt_f32_e64 s[72:73], s21, v62\n" \
"buffer_load_dwordx4 a[100:103], v20, s[56:59], s97 offen offset:1024 sc1\n" \
"v_mfma_f32_16x16x32_bf16 a[208:211], a[216:219], v[100:103], a[208:211]\n" \
"buffer_load_dwordx4 a[108:111], v54, s[56:59], s97 offen offset:1024 sc1\n" \
"buffer_load_dwordx4 a[116:119], v20, s[56:59], s98 offen offset:1024 sc1\n" \
"buffer_load_dwordx4 a[124:127], v54, s[56:59], s98 offen offset:1024 sc1\n" \
"s_nop 4\n" \
"v_accvgpr_read_b32 v63, a208\n" \
"v_cmp_u_f32_e32 vcc, v63, v63\n" \
"s_nop 0\n" \
"s_or_b64 vcc, s[72:73], vcc\n" \
"s_cbranch_scc0 L_out_%=\n" \
"s_branch L_end_%=\n" \
"L_skb_%=:\n" \
"s_sleep 1\n" \
"buffer_load_dword v57, v61, s[56:59], s96 offen sc1\n" \
"s_add_u32 s101, s101, 1\n" \
"s_waitcnt vmcnt(0)\n" \
"v_cmp_ne_u32_e32 vcc, s62, v57\n" \
"s_cmp_gt_u32 s101, 0x100000\n" \
"s_cbranch_scc1 L_rkb_%=\n" \
"s_nop 2\n" \
"s_cbranch_vccnz L_skb_%=\n" \
"s_branch L_rkb_%=\n" \
"L_so0_%=:\n" \
"s_sleep 1\n" \
"buffer_load_dword v55, v58, s[56:59], s96 offen sc1\n" \
"s_add_u32 s101, s101, 1\n" \
"s_waitcnt vmcnt(0)\n" \
"v_cmp_ne_u32_e32 vcc, s62, v55\n" \
"s_cmp_gt_u32 s101, 0x100000\n" \
"s_cbranch_scc1 L_ro0_%=\n" \
"s_nop 2\n" \
"s_cbranch_vccnz L_so0_%=\n" \
"s_branch L_ro0_%=\n" \
"L_so1_%=:\n" \
"s_sleep 1\n" \
"buffer_load_dword v57, v60, s[56:59], s96 offen sc1\n" \
"s_add_u32 s101, s101, 1\n" \
"s_waitcnt vmcnt(0)\n" \
"v_cmp_ne_u32_e32 vcc, s62, v57\n" \
"s_cmp_gt_u32 s101, 0x100000\n" \
"s_cbranch_scc1 L_ro1_%=\n" \
"s_nop 2\n" \
"s_cbranch_vccnz L_so1_%=\n" \
"s_branch L_ro1_%=\n" \
"L_sa_%=:\n" \
"s_sleep 1\n" \
"buffer_load_dword v26, v38, s[56:59], s64 offen sc1\n" \
"s_add_u32 s101, s101, 1\n" \
"s_waitcnt vmcnt(0)\n" \
"v_cmp_ne_u32_e32 vcc, s62, v26\n" \
"s_cmp_gt_u32 s101, 0x100000\n" \
"s_cbranch_scc1 L_ra_%=\n" \
"s_nop 2\n" \
"s_cbranch_vccnz L_sa_%=\n" \
"s_branch L_ra_%=\n" \
"L_sb_%=:\n" \
"s_sleep 1\n" \
"buffer_load_dword v47, v38, s[56:59], s73 offen sc1\n" \
"s_add_u32 s101, s101, 1\n" \
"s_waitcnt vmcnt(0)\n" \
"v_cmp_ne_u32_e32 vcc, s62, v47\n" \
"s_cmp_gt_u32 s101, 0x100000\n" \
"s_cbranch_scc1 L_rb_%=\n" \
"s_nop 2\n" \
"s_cbranch_vccnz L_sb_%=\n" \
"s_branch L_rb_%=\n" \
"L_sx0_%=:\n" \
"s_sleep 1\n" \
"buffer_load_dword v37, v40, s[56:59], s74 offen sc1\n" \
"s_add_u32 s101, s101, 1\n" \
"s_waitcnt vmcnt(0)\n" \
"v_cmp_eq_u32_e64 s[98:99], s62, v37\n" \
"s_cmp_gt_u32 s101, 0x100000\n" \
"s_cbranch_scc1 L_rx0_%=\n" \
"s_or_b32 s75, 0x7f, s100\n" \
"s_andn2_b32 s75, s75, s98\n" \
"s_cbranch_scc1 L_sx0_%=\n" \
"s_branch L_rx0_%=\n" \
"L_sx1_%=:\n" \
"s_sleep 1\n" \
"buffer_load_dword v37, v40, s[56:59], s74 offen sc1\n" \
"s_add_u32 s101, s101, 1\n" \
"s_waitcnt vmcnt(0)\n" \
"v_cmp_eq_u32_e64 s[98:99], s62, v37\n" \
"s_cmp_gt_u32 s101, 0x100000\n" \
"s_cbranch_scc1 L_rx1_%=\n" \
"s_andn2_b32 s75, 0x100, s98\n" \
"s_cbranch_scc1 L_sx1_%=\n" \
"s_branch L_rx1_%=\n" \
"L_sx2_%=:\n" \
"s_sleep 1\n" \
"buffer_load_dword v37, v40, s[56:59], s74 offen sc1\n" \
"s_add_u32 s101, s101, 1\n" \
"s_waitcnt vmcnt(0)\n" \
"v_cmp_eq_u32_e64 s[98:99], s62, v37\n" \
"s_cmp_gt_u32 s101, 0x100000\n" \
"s_cbranch_scc1 L_rx2_%=\n" \
"s_andn2_b32 s75, 0x200, s98\n" \
"s_cbranch_scc1 L_sx2_%=\n" \
"s_branch L_rx2_%=\n" \
"L_sx3_%=:\n" \
"s_sleep 1\n" \
"buffer_load_dword v37, v40, s[56:59], s74 offen sc1\n" \
"s_add_u32 s101, s101, 1\n" \
"s_waitcnt vmcnt(0)\n" \
"v_cmp_eq_u32_e64 s[98:99], s62, v37\n" \
"s_cmp_gt_u32 s101, 0x100000\n" \
"s_cbranch_scc1 L_rx3_%=\n" \
"s_andn2_b32 s75, 0x400, s98\n" \
"s_cbranch_scc1 L_sx3_%=\n" \
"s_branch L_rx3_%=\n" \
"L_sx4_%=:\n" \
"s_sleep 1\n" \
"buffer_load_dword v37, v40, s[56:59], s74 offen sc1\n" \
"s_add_u32 s101, s101, 1\n" \
"s_waitcnt vmcnt(0)\n" \
"v_cmp_eq_u32_e64 s[98:99], s62, v37\n" \
"s_cmp_gt_u32 s101, 0x100000\n" \
"s_cbranch_scc1 L_rx4_%=\n" \
"s_andn2_b32 s75, 0x800, s98\n" \
"s_cbranch_scc1 L_sx4_%=\n" \
"s_branch L_rx4_%=\n" \
"L_sx5_%=:\n" \
"s_sleep 1\n" \
"buffer_load_dword v37, v40, s[56:59], s74 offen sc1\n" \
"s_add_u32 s101, s101, 1\n" \
"s_waitcnt vmcnt(0)\n" \
"v_cmp_eq_u32_e64 s[98:99], s62, v37\n" \
"s_cmp_gt_u32 s101, 0x100000\n" \
"s_cbranch_scc1 L_rx5_%=\n" \
"s_andn2_b32 s75, 0x1000, s98\n" \
"s_cbranch_scc1 L_sx5_%=\n" \
"s_branch L_rx5_%=\n" \
"L_sx6_%=:\n" \
"s_sleep 1\n" \
"buffer_load_dword v37, v40, s[56:59], s74 offen sc1\n" \
"s_add_u32 s101, s101, 1\n" \
"s_waitcnt vmcnt(0)\n" \
"v_cmp_eq_u32_e64 s[98:99], s62, v37\n" \
"s_cmp_gt_u32 s101, 0x100000\n" \
"s_cbranch_scc1 L_rx6_%=\n" \
"s_andn2_b32 s75, 0x2000, s98\n" \
"s_cbranch_scc1 L_sx6_%=\n" \
"s_branch L_rx6_%=\n" \
"L_sx7_%=:\n" \
"s_sleep 1\n" \
"buffer_load_dword v37, v40, s[56:59], s74 offen sc1\n" \
"s_add_u32 s101, s101, 1\n" \
"s_waitcnt vmcnt(0)\n" \
"v_cmp_eq_u32_e64 s[98:99], s62, v37\n" \
"s_cmp_gt_u32 s101, 0x100000\n" \
"s_cbranch_scc1 L_rx7_%=\n" \
"s_andn2_b32 s75, 0x4000, s98\n" \
"s_cbranch_scc1 L_sx7_%=\n" \
"s_branch L_rx7_%=\n" \
".fill (L_ab + 12288 - .) / 4, 4, 0xBF800000\n" \
".fill (L_ab + 12288 - .) / 4, 4, 0xBF800000\n" \
"L_ap3:\n" \
"s_setpc_b64 s[88:89]\n" \
"L_sw_%=:\n" \
"v_mfma_f32_16x16x32_bf16 v[104:107], a[128:131], a[16:19], v[36:39]\n" \
"s_add_u32 s68, s60, 0xfff6e000\n" \
"s_add_u32 s69, s60, 0xfffb7000\n" \
"s_add_u32 s70, s60, 0x0\n" \
"s_add_u32 s71, s60, 0x49000\n" \
"v_mfma_f32_16x16x32_bf16 v[104:107], a[132:135], a[20:23], v[104:107]\n" \
"s_xor_b32 m0, s65, 0x12000\n" \
"ds_read_b128 a[160:163], v49 offset:0\n" \
"v_mfma_f32_16x16x32_bf16 v[104:107], a[136:139], a[24:27], v[104:107]\n" \
"buffer_load_dwordx4 v50, s[8:11], s64 offen offset:0 sc1 lds\n" \
"v_mfma_f32_16x16x32_bf16 v[104:107], a[140:143], a[28:31], v[104:107]\n" \
"ds_read_b128 a[164:167], v49 offset:1024\n" \
"ds_read_b128 a[168:171], v49 offset:2048\n" \
"v_mfma_f32_16x16x32_bf16 v[108:111], a[156:159], a[28:31], v[36:39]\n" \
"ds_read_b128 a[172:175], v49 offset:3072\n" \
"ds_read_b128 a[176:179], v49 offset:4096\n" \
"v_mfma_f32_16x16x32_bf16 v[108:111], a[152:155], a[24:27], v[108:111]\n" \
"buffer_load_dwordx4 v50, s[8:11], s64 offen offset:1024 sc1 lds\n" \
"v_mfma_f32_16x16x32_bf16 v[108:111], a[148:151], a[20:23], v[108:111]\n" \
"s_mov_b32 s65, s13\n" \
"v_add_u32_e32 v63, s15, v20\n" \
"v_mfma_f32_16x16x32_bf16 v[108:111], a[144:147], a[16:19], v[108:111]\n" \
"s_add_u32 s67, s14, 40960\n" \
"v_add_u32_e32 v50, 0x6000, v63\n" \
"v_mfma_f32_16x16x32_bf16 v[116:119], a[144:147], a[32:35], v[40:43]\n" \
"s_xor_b32 m0, s65, 0x8000\n" \
"ds_read_b128 a[180:183], v49 offset:5120\n" \
"v_mfma_f32_16x16x32_bf16 v[116:119], a[148:151], a[36:39], v[116:119]\n" \
"ds_read_b128 a[184:187], v49 offset:6144\n" \
"v_and_b32_e32 v68, v12, v68\n" \
"v_mfma_f32_16x16x32_bf16 v[116:119], a[152:155], a[40:43], v[116:119]\n" \
"v_and_b32_e32 v69, v13, v69\n" \
"v_and_b32_e32 v70, v14, v70\n" \
"v_mfma_f32_16x16x32_bf16 v[116:119], a[156:159], a[44:47], v[116:119]\n" \
"buffer_load_dwordx4 v50, s[8:11], s67 offen offset:0 sc1 lds\n" \
"v_mfma_f32_16x16x32_bf16 v[112:115], a[140:143], a[44:47], v[40:43]\n" \
"v_and_b32_e32 v71, v15, v71\n" \
"ds_read_b128 a[188:191], v49 offset:7168\n" \
"v_mfma_f32_16x16x32_bf16 v[112:115], a[136:139], a[40:43], v[112:115]\n" \
"v_exp_f32_e32 v86, v86\n" \
"v_mfma_f32_16x16x32_bf16 v[112:115], a[132:135], a[36:39], v[112:115]\n" \
"v_exp_f32_e32 v87, v87\n" \
"v_mfma_f32_16x16x32_bf16 v[112:115], a[128:131], a[32:35], v[112:115]\n" \
"v_exp_f32_e32 v84, v84\n" \
"v_mfma_f32_16x16x32_bf16 v[120:123], a[128:131], a[48:51], v[44:47]\n" \
"buffer_load_dwordx4 v50, s[8:11], s67 offen offset:1024 sc1 lds\n" \
"v_mfma_f32_16x16x32_bf16 v[120:123], a[132:135], a[52:55], v[120:123]\n" \
"v_exp_f32_e32 v85, v85\n" \
"v_mfma_f32_16x16x32_bf16 v[120:123], a[136:139], a[56:59], v[120:123]\n" \
"v_cvt_pk_bf16_f32 v87, v86, v87\n" \
"v_cvt_pk_bf16_f32 v86, v84, v85\n" \
"v_mfma_f32_16x16x32_bf16 v[120:123], a[140:143], a[60:63], v[120:123]\n" \
"v_exp_f32_e32 v82, v82\n" \
"v_mfma_f32_16x16x32_bf16 v[124:127], a[156:159], a[60:63], v[44:47]\n" \
"v_exp_f32_e32 v83, v83\n" \
"v_mfma_f32_16x16x32_bf16 v[124:127], a[152:155], a[56:59], v[124:127]\n" \
"v_exp_f32_e32 v80, v80\n" \
"v_mfma_f32_16x16x32_bf16 v[124:127], a[148:151], a[52:55], v[124:127]\n" \
"buffer_load_dwordx4 v50, s[8:11], s67 offen offset:2048 sc1 lds\n" \
"v_mfma_f32_16x16x32_bf16 v[124:127], a[144:147], a[48:51], v[124:127]\n" \
"v_cvt_pk_bf16_f32 v85, v82, v83\n" \
"s_waitcnt lgkmcnt(1)\n" \
"v_mfma_f32_16x16x32_bf16 v[128:131], a[160:163], v[68:71], v[128:131]\n" \
"v_exp_f32_e32 v81, v81\n" \
"v_mfma_f32_16x16x32_bf16 v[160:163], a[160:163], v[72:75], v[160:163]\n" \
"v_cvt_pk_bf16_f32 v84, v80, v81\n" \
"s_mov_b32 s64, s14\n" \
"v_mfma_f32_16x16x32_bf16 v[132:135], a[164:167], v[68:71], v[132:135]\n" \
"v_exp_f32_e32 v88, v88\n" \
"v_mfma_f32_16x16x32_bf16 v[164:167], a[164:167], v[72:75], v[164:167]\n" \
"v_exp_f32_e32 v89, v89\n" \
"v_mfma_f32_16x16x32_bf16 v[136:139], a[168:171], v[68:71], v[136:139]\n" \
"buffer_load_dwordx4 v50, s[8:11], s67 offen offset:3072 sc1 lds\n" \
"v_mfma_f32_16x16x32_bf16 v[168:171], a[168:171], v[72:75], v[168:171]\n" \
"v_exp_f32_e32 v90, v90\n" \
"v_mfma_f32_16x16x32_bf16 v[140:143], a[172:175], v[68:71], v[140:143]\n" \
"v_exp_f32_e32 v91, v91\n" \
"v_mfma_f32_16x16x32_bf16 v[172:175], a[172:175], v[72:75], v[172:175]\n" \
"v_cvt_pk_bf16_f32 v88, v88, v89\n" \
"v_cvt_pk_bf16_f32 v89, v90, v91\n" \
"v_mfma_f32_16x16x32_bf16 v[144:147], a[176:179], v[68:71], v[144:147]\n" \
"v_exp_f32_e32 v92, v92\n" \
"v_mfma_f32_16x16x32_bf16 v[176:179], a[176:179], v[72:75], v[176:179]\n" \
"v_exp_f32_e32 v93, v93\n" \
"v_mfma_f32_16x16x32_bf16 v[148:151], a[180:183], v[68:71], v[148:151]\n" \
"v_exp_f32_e32 v94, v94\n" \
"v_mfma_f32_16x16x32_bf16 v[180:183], a[180:183], v[72:75], v[180:183]\n" \
"v_exp_f32_e32 v95, v95\n" \
"v_mfma_f32_16x16x32_bf16 v[152:155], a[184:187], v[68:71], v[152:155]\n" \
"v_cvt_pk_bf16_f32 v90, v92, v93\n" \
"v_cvt_pk_bf16_f32 v91, v94, v95\n" \
"v_mfma_f32_16x16x32_bf16 v[184:187], a[184:187], v[72:75], v[184:187]\n" \
"v_add_u32_e32 v51, 0x12000, v63\n" \
"s_waitcnt lgkmcnt(0)\n" \
"v_mfma_f32_16x16x32_bf16 v[156:159], a[188:191], v[68:71], v[156:159]\n" \
"s_xor_b32 m0, s65, 0xa000\n" \
"ds_read_b32 v58, v18 offset:512\n" \
"v_mfma_f32_16x16x32_bf16 v[188:191], a[188:191], v[72:75], v[188:191]\n" \
"buffer_load_dwordx4 v51, s[8:11], s64 offen offset:0 sc1 lds\n" \
"v_smfmac_f32_16x16x64_bf16 a[192:195], a[200:203], v[68:75], v21\n" \
"buffer_load_dwordx4 v51, s[8:11], s64 offen offset:1024 sc1 lds\n" \
"v_mfma_f32_16x16x32_bf16 v[220:223], a[188:191], v[84:87], v[220:223]\n" \
"ds_read_b32 v59, v18 offset:640\n" \
"ds_read_b32 v60, v18 offset:768\n" \
"v_mfma_f32_16x16x32_bf16 v[252:255], a[188:191], v[88:91], v[252:255]\n" \
"ds_read_b32 v61, v18 offset:896\n" \
"ds_read_b128 a[128:131], v48 offset:0\n" \
"v_mfma_f32_16x16x32_bf16 v[216:219], a[184:187], v[84:87], v[216:219]\n" \
"ds_read_b128 a[144:147], v48 offset:4096\n" \
"ds_read_b128 a[132:135], v48 offset:1024\n" \
"v_mfma_f32_16x16x32_bf16 v[248:251], a[184:187], v[88:91], v[248:251]\n" \
"ds_read_b128 a[148:151], v48 offset:5120\n" \
"ds_read_b128 a[136:139], v48 offset:2048\n" \
"v_mfma_f32_16x16x32_bf16 v[212:215], a[180:183], v[84:87], v[212:215]\n" \
"ds_read_b128 a[152:155], v48 offset:6144\n" \
"ds_read_b128 a[140:143], v48 offset:3072\n" \
"v_mfma_f32_16x16x32_bf16 v[244:247], a[180:183], v[88:91], v[244:247]\n" \
"ds_read_b128 a[156:159], v48 offset:7168\n" \
"v_add_u32_e32 v52, 0x14000, v63\n" \
"v_mfma_f32_16x16x32_bf16 v[208:211], a[176:179], v[84:87], v[208:211]\n" \
"buffer_load_dwordx4 v51, s[8:11], s64 offen offset:2048 sc1 lds\n" \
"v_mfma_f32_16x16x32_bf16 v[240:243], a[176:179], v[88:91], v[240:243]\n" \
"v_exp_f32_e32 v104, v104\n" \
"v_mfma_f32_16x16x32_bf16 v[204:207], a[172:175], v[84:87], v[204:207]\n" \
"v_exp_f32_e32 v105, v105\n" \
"v_mfma_f32_16x16x32_bf16 v[236:239], a[172:175], v[88:91], v[236:239]\n" \
"buffer_load_dwordx4 v51, s[8:11], s64 offen offset:3072 sc1 lds\n" \
"v_mfma_f32_16x16x32_bf16 v[200:203], a[168:171], v[84:87], v[200:203]\n" \
"v_exp_f32_e32 v106, v106\n" \
"v_mfma_f32_16x16x32_bf16 v[232:235], a[168:171], v[88:91], v[232:235]\n" \
"v_exp_f32_e32 v107, v107\n" \
"v_mfma_f32_16x16x32_bf16 v[196:199], a[164:167], v[84:87], v[196:199]\n" \
"v_cvt_pk_bf16_f32 v104, v104, v105\n" \
"v_cvt_pk_bf16_f32 v105, v106, v107\n" \
"v_mfma_f32_16x16x32_bf16 v[228:231], a[164:167], v[88:91], v[228:231]\n" \
"v_exp_f32_e32 v108, v108\n" \
"v_mfma_f32_16x16x32_bf16 v[192:195], a[160:163], v[84:87], v[192:195]\n" \
"v_exp_f32_e32 v109, v109\n" \
"v_mfma_f32_16x16x32_bf16 v[224:227], a[160:163], v[88:91], v[224:227]\n" \
"v_exp_f32_e32 v110, v110\n" \
"v_smfmac_f32_16x16x64_bf16 a[196:199], a[200:203], v[84:91], v21\n" \
"v_exp_f32_e32 v111, v111\n" \
"v_cvt_pk_bf16_f32 v106, v108, v109\n" \
"v_cvt_pk_bf16_f32 v107, v110, v111\n" \
"v_and_b32_e32 v104, v12, v104\n" \
"v_and_b32_e32 v105, v13, v105\n" \
"v_and_b32_e32 v106, v14, v106\n" \
"v_and_b32_e32 v107, v15, v107\n" \
"v_accvgpr_read_b32 v54, a192\n" \
"v_accvgpr_read_b32 v22, a193\n" \
"v_add_u32_e32 v53, 0x20000, v63\n" \
"v_exp_f32_e32 v118, v118\n" \
"v_exp_f32_e32 v119, v119\n" \
"v_exp_f32_e32 v116, v116\n" \
"v_exp_f32_e32 v117, v117\n" \
"v_cvt_pk_bf16_f32 v119, v118, v119\n" \
"v_exp_f32_e32 v114, v114\n" \
"v_exp_f32_e32 v115, v115\n" \
"v_exp_f32_e32 v112, v112\n" \
"v_exp_f32_e32 v113, v113\n" \
"v_cvt_pk_bf16_f32 v118, v116, v117\n" \
"v_cvt_pk_bf16_f32 v117, v114, v115\n" \
"v_cvt_pk_bf16_f32 v116, v112, v113\n" \
"v_readlane_b32 s93, v11, 19\n" \
"s_waitcnt vmcnt(10) lgkmcnt(0)\n" \
"s_barrier\n" \
"v_mfma_f32_16x16x32_bf16 v[80:83], a[128:131], a[32:35], v[40:43]\n" \
"ds_read_b128 a[160:163], v49 offset:8192\n" \
"ds_read_b128 a[164:167], v49 offset:9216\n" \
"v_mfma_f32_16x16x32_bf16 v[80:83], a[132:135], a[36:39], v[80:83]\n" \
"ds_read_b128 a[168:171], v49 offset:10240\n" \
"s_xor_b32 m0, s65, 0x18000\n" \
"v_mfma_f32_16x16x32_bf16 v[80:83], a[136:139], a[40:43], v[80:83]\n" \
"buffer_load_dwordx4 v52, s[8:11], s64 offen offset:0 sc1 lds\n" \
"v_mfma_f32_16x16x32_bf16 v[80:83], a[140:143], a[44:47], v[80:83]\n" \
"ds_read_b128 a[172:175], v49 offset:11264\n" \
"ds_read_b128 a[176:179], v49 offset:12288\n" \
"v_mfma_f32_16x16x32_bf16 v[84:87], a[156:159], a[44:47], v[40:43]\n" \
"ds_read_b128 a[180:183], v49 offset:13312\n" \
"ds_read_b128 a[184:187], v49 offset:14336\n" \
"v_mfma_f32_16x16x32_bf16 v[84:87], a[152:155], a[40:43], v[84:87]\n" \
"buffer_load_dwordx4 v52, s[8:11], s64 offen offset:1024 sc1 lds\n" \
"v_mfma_f32_16x16x32_bf16 v[84:87], a[148:151], a[36:39], v[84:87]\n" \
"ds_read_b128 a[188:191], v49 offset:15360\n" \
"v_readlane_b32 s80, v11, 11\n" \
"v_mfma_f32_16x16x32_bf16 v[84:87], a[144:147], a[32:35], v[84:87]\n" \
"buffer_load_dwordx4 v52, s[8:11], s64 offen offset:2048 sc1 lds\n" \
"v_mfma_f32_16x16x32_bf16 v[92:95], a[144:147], a[48:51], v[44:47]\n" \
"v_exp_f32_e32 v120, v120\n" \
"v_mfma_f32_16x16x32_bf16 v[92:95], a[148:151], a[52:55], v[92:95]\n" \
"v_exp_f32_e32 v121, v121\n" \
"v_mfma_f32_16x16x32_bf16 v[92:95], a[152:155], a[56:59], v[92:95]\n" \
"buffer_load_dwordx4 v52, s[8:11], s64 offen offset:3072 sc1 lds\n" \
"v_mfma_f32_16x16x32_bf16 v[92:95], a[156:159], a[60:63], v[92:95]\n" \
"s_mov_b32 m0, s65\n" \
"v_cvt_pk_bf16_f32 v120, v120, v121\n" \
"v_mfma_f32_16x16x32_bf16 v[88:91], a[140:143], a[60:63], v[44:47]\n" \
"v_exp_f32_e32 v122, v122\n" \
"v_mfma_f32_16x16x32_bf16 v[88:91], a[136:139], a[56:59], v[88:91]\n" \
"buffer_load_dwordx4 v53, s[8:11], s64 offen offset:0 sc1 lds\n" \
"v_mfma_f32_16x16x32_bf16 v[88:91], a[132:135], a[52:55], v[88:91]\n" \
"v_exp_f32_e32 v123, v123\n" \
"v_mfma_f32_16x16x32_bf16 v[88:91], a[128:131], a[48:51], v[88:91]\n" \
"v_exp_f32_e32 v124, v124\n" \
"v_mfma_f32_16x16x32_bf16 a[192:195], a[212:215], v[104:107], a[192:195]\n" \
"v_cvt_pk_bf16_f32 v121, v122, v123\n" \
"s_waitcnt lgkmcnt(5)\n" \
"v_mfma_f32_16x16x32_bf16 v[160:163], a[160:163], v[104:107], v[160:163]\n" \
"buffer_load_dwordx4 v53, s[8:11], s64 offen offset:1024 sc1 lds\n" \
"v_mfma_f32_16x16x32_bf16 v[164:167], a[164:167], v[104:107], v[164:167]\n" \
"v_exp_f32_e32 v125, v125\n" \
"v_mfma_f32_16x16x32_bf16 v[168:171], a[168:171], v[104:107], v[168:171]\n" \
"v_cvt_pk_bf16_f32 v122, v124, v125\n" \
"s_waitcnt lgkmcnt(1)\n" \
"v_mfma_f32_16x16x32_bf16 v[172:175], a[172:175], v[104:107], v[172:175]\n" \
"v_exp_f32_e32 v126, v126\n" \
"v_mfma_f32_16x16x32_bf16 v[176:179], a[176:179], v[104:107], v[176:179]\n" \
"v_exp_f32_e32 v127, v127\n" \
"v_mfma_f32_16x16x32_bf16 v[180:183], a[180:183], v[104:107], v[180:183]\n" \
"v_cvt_pk_bf16_f32 v123, v126, v127\n" \
"v_readlane_b32 s81, v11, 12\n" \
"v_mfma_f32_16x16x32_bf16 v[184:187], a[184:187], v[104:107], v[184:187]\n" \
"s_mov_b32 s82, 0x47fca0\n" \
"s_waitcnt lgkmcnt(0)\n" \
"v_mfma_f32_16x16x32_bf16 v[188:191], a[188:191], v[104:107], v[188:191]\n" \
"buffer_load_dwordx4 v53, s[8:11], s64 offen offset:2048 sc1 lds\n" \
"v_mfma_f32_16x16x32_bf16 v[220:223], a[188:191], v[116:119], v[220:223]\n" \
"buffer_load_dwordx4 v53, s[8:11], s64 offen offset:3072 sc1 lds\n" \
"v_mfma_f32_16x16x32_bf16 v[252:255], a[188:191], v[120:123], v[252:255]\n" \
"s_mov_b32 s83, 0x20000\n" \
"v_readlane_b32 s92, v11, 25\n" \
"v_mfma_f32_16x16x32_bf16 v[216:219], a[184:187], v[116:119], v[216:219]\n" \
"s_add_u32 m0, s93, 0x0\n" \
"ds_read_b128 a[128:131], v48 offset:8192\n" \
"v_mfma_f32_16x16x32_bf16 v[248:251], a[184:187], v[120:123], v[248:251]\n" \
"ds_read_b128 a[144:147], v48 offset:12288\n" \
"ds_read_b128 a[132:135], v48 offset:9216\n" \
"v_mfma_f32_16x16x32_bf16 v[212:215], a[180:183], v[116:119], v[212:215]\n" \
"buffer_load_dwordx4 v0, s[80:83], s92 offen offset:0 lds\n" \
"v_mfma_f32_16x16x32_bf16 v[244:247], a[180:183], v[120:123], v[244:247]\n" \
"buffer_load_dwordx4 v1, s[80:83], s92 offen offset:1024 lds\n" \
"v_mfma_f32_16x16x32_bf16 v[208:211], a[176:179], v[116:119], v[208:211]\n" \
"ds_read_b128 a[148:151], v48 offset:13312\n" \
"ds_read_b128 a[136:139], v48 offset:10240\n" \
"v_mfma_f32_16x16x32_bf16 v[240:243], a[176:179], v[120:123], v[240:243]\n" \
"ds_read_b128 a[152:155], v48 offset:14336\n" \
"ds_read_b128 a[140:143], v48 offset:11264\n" \
"v_mfma_f32_16x16x32_bf16 v[204:207], a[172:175], v[116:119], v[204:207]\n" \
"buffer_load_dwordx4 v2, s[80:83], s92 offen offset:2048 lds\n" \
"v_mfma_f32_16x16x32_bf16 v[236:239], a[172:175], v[120:123], v[236:239]\n" \
"ds_read_b128 a[156:159], v48 offset:15360\n" \
"s_xor_b32 s65, s65, 0x8000\n" \
"v_mfma_f32_16x16x32_bf16 v[200:203], a[168:171], v[116:119], v[200:203]\n" \
"s_add_u32 s64, s64, 0x10000\n" \
"v_add_u32_e32 v49, 0x10000, v48\n" \
"v_mfma_f32_16x16x32_bf16 v[232:235], a[168:171], v[120:123], v[232:235]\n" \
"buffer_load_dwordx4 v3, s[80:83], s92 offen offset:3072 lds\n" \
"v_mfma_f32_16x16x32_bf16 v[196:199], a[164:167], v[116:119], v[196:199]\n" \
"v_exp_f32_e32 v86, v86\n" \
"v_mfma_f32_16x16x32_bf16 v[228:231], a[164:167], v[120:123], v[228:231]\n" \
"v_exp_f32_e32 v87, v87\n" \
"v_mfma_f32_16x16x32_bf16 v[192:195], a[160:163], v[116:119], v[192:195]\n" \
"v_exp_f32_e32 v84, v84\n" \
"v_mfma_f32_16x16x32_bf16 v[224:227], a[160:163], v[120:123], v[224:227]\n" \
"v_exp_f32_e32 v85, v85\n" \
"v_smfmac_f32_16x16x64_bf16 a[196:199], a[200:203], v[116:123], v21\n" \
"v_exp_f32_e32 v88, v88\n" \
"v_exp_f32_e32 v89, v89\n" \
"v_cvt_pk_bf16_f32 v87, v86, v87\n" \
"v_exp_f32_e32 v82, v82\n" \
"v_exp_f32_e32 v83, v83\n" \
"v_exp_f32_e32 v80, v80\n" \
"v_exp_f32_e32 v81, v81\n" \
"v_exp_f32_e32 v90, v90\n" \
"v_exp_f32_e32 v91, v91\n" \
"v_cvt_pk_bf16_f32 v86, v84, v85\n" \
"v_cvt_pk_bf16_f32 v88, v88, v89\n" \
"v_exp_f32_e32 v92, v92\n" \
"v_exp_f32_e32 v93, v93\n" \
"v_exp_f32_e32 v94, v94\n" \
"v_exp_f32_e32 v95, v95\n" \
"v_cvt_pk_bf16_f32 v85, v82, v83\n" \
"v_cvt_pk_bf16_f32 v84, v80, v81\n" \
"v_cvt_pk_bf16_f32 v89, v90, v91\n" \
"v_and_b32_e32 v84, v12, v84\n" \
"v_and_b32_e32 v85, v13, v85\n" \
"v_and_b32_e32 v86, v14, v86\n" \
"v_and_b32_e32 v87, v15, v87\n" \
"v_cvt_pk_bf16_f32 v90, v92, v93\n" \
"v_cvt_pk_bf16_f32 v91, v94, v95\n" \
"v_add_u32_e32 v48, s16, v20\n" \
"v_add_f32_e32 v54, v54, v22\n" \
"v_accvgpr_read_b32 v55, a194\n" \
"v_accvgpr_read_b32 v23, a195\n" \
"v_mfma_f32_16x16x32_bf16 a[192:195], a[216:219], a[216:219], 0\n" \
"v_max_f32_e32 v62, v62, v54\n" \
"v_add_f32_e32 v55, v55, v23\n" \
"v_max_f32_e32 v62, v62, v55\n" \
"v_rcp_f32_e32 v54, v54\n" \
"v_rcp_f32_e32 v55, v55\n" \
"v_mul_f32_e32 v54, v54, v58\n" \
"v_mul_f32_e32 v55, v55, v59\n" \
"v_mul_f32_e32 v128, v54, v128\n" \
"v_mul_f32_e32 v129, v54, v129\n" \
"v_mul_f32_e32 v136, v54, v136\n" \
"v_mul_f32_e32 v137, v54, v137\n" \
"v_mul_f32_e32 v144, v54, v144\n" \
"v_mul_f32_e32 v145, v54, v145\n" \
"v_mul_f32_e32 v152, v54, v152\n" \
"v_mul_f32_e32 v153, v54, v153\n" \
"v_mul_f32_e32 v160, v55, v160\n" \
"v_mul_f32_e32 v161, v55, v161\n" \
"v_mul_f32_e32 v168, v55, v168\n" \
"v_mul_f32_e32 v169, v55, v169\n" \
"v_mul_f32_e32 v176, v55, v176\n" \
"v_mul_f32_e32 v177, v55, v177\n" \
"v_mul_f32_e32 v184, v55, v184\n" \
"v_mul_f32_e32 v185, v55, v185\n" \
"v_cvt_pk_bf16_f32 v128, v128, v129\n" \
"v_mul_f32_e32 v130, v54, v130\n" \
"v_mul_f32_e32 v131, v54, v131\n" \
"v_cvt_pk_bf16_f32 v136, v136, v137\n" \
"v_mul_f32_e32 v138, v54, v138\n" \
"v_mul_f32_e32 v139, v54, v139\n" \
"v_cvt_pk_bf16_f32 v144, v144, v145\n" \
"v_mul_f32_e32 v146, v54, v146\n" \
"v_mul_f32_e32 v147, v54, v147\n" \
"v_cvt_pk_bf16_f32 v152, v152, v153\n" \
"v_mul_f32_e32 v154, v54, v154\n" \
"v_mul_f32_e32 v155, v54, v155\n" \
"v_cvt_pk_bf16_f32 v160, v160, v161\n" \
"v_mul_f32_e32 v162, v55, v162\n" \
"v_mul_f32_e32 v163, v55, v163\n" \
"v_cvt_pk_bf16_f32 v168, v168, v169\n" \
"v_mul_f32_e32 v170, v55, v170\n" \
"v_mul_f32_e32 v171, v55, v171\n" \
"v_cvt_pk_bf16_f32 v176, v176, v177\n" \
"v_mul_f32_e32 v178, v55, v178\n" \
"v_mul_f32_e32 v179, v55, v179\n" \
"v_cvt_pk_bf16_f32 v184, v184, v185\n" \
"v_mul_f32_e32 v186, v55, v186\n" \
"v_mul_f32_e32 v187, v55, v187\n" \
"v_cvt_pk_bf16_f32 v129, v130, v131\n" \
"v_mul_f32_e32 v132, v54, v132\n" \
"v_mul_f32_e32 v133, v54, v133\n" \
"v_mul_f32_e32 v134, v54, v134\n" \
"v_mul_f32_e32 v135, v54, v135\n" \
"v_cvt_pk_bf16_f32 v137, v138, v139\n" \
"v_mul_f32_e32 v140, v54, v140\n" \
"s_waitcnt vmcnt(12) lgkmcnt(0)\n" \
"s_barrier\n" \
"v_mfma_f32_16x16x32_bf16 v[120:123], a[128:131], a[48:51], v[44:47]\n" \
"s_xor_b32 m0, s65, 0x12000\n" \
"ds_read_b128 a[188:191], v49 offset:7168\n" \
"v_mfma_f32_16x16x32_bf16 v[120:123], a[132:135], a[52:55], v[120:123]\n" \
"buffer_load_dwordx4 v50, s[8:11], s64 offen offset:0 sc1 lds\n" \
"v_mfma_f32_16x16x32_bf16 v[120:123], a[136:139], a[56:59], v[120:123]\n" \
"ds_read_b128 a[184:187], v49 offset:6144\n" \
"ds_read_b128 a[180:183], v49 offset:5120\n" \
"v_mfma_f32_16x16x32_bf16 v[120:123], a[140:143], a[60:63], v[120:123]\n" \
"buffer_load_dwordx4 v50, s[8:11], s64 offen offset:1024 sc1 lds\n" \
"v_mfma_f32_16x16x32_bf16 v[124:127], a[156:159], a[60:63], v[44:47]\n" \
"ds_read_b128 a[176:179], v49 offset:4096\n" \
"ds_read_b128 a[172:175], v49 offset:3072\n" \
"v_mfma_f32_16x16x32_bf16 v[124:127], a[152:155], a[56:59], v[124:127]\n" \
"ds_read_b128 a[168:171], v49 offset:2048\n" \
"ds_read_b128 a[164:167], v49 offset:1024\n" \
"v_mfma_f32_16x16x32_bf16 v[124:127], a[148:151], a[52:55], v[124:127]\n" \
"ds_read_b128 a[160:163], v49 offset:0\n" \
"ds_read_b128 a[0:3], v49 offset:8192\n" \
"v_mfma_f32_16x16x32_bf16 v[124:127], a[144:147], a[48:51], v[124:127]\n" \
"buffer_load_dwordx4 v50, s[8:11], s64 offen offset:2048 sc1 lds\n" \
"v_mfma_f32_16x16x32_bf16 v[32:35], a[220:223], a[64:67], 0\n" \
"ds_read_b128 a[4:7], v49 offset:9216\n" \
"ds_read_b128 a[8:11], v49 offset:10240\n" \
"v_mfma_f32_16x16x32_bf16 v[36:39], a[220:223], a[80:83], 0\n" \
"ds_read_b128 a[12:15], v49 offset:11264\n" \
"ds_read_b128 a[16:19], v49 offset:12288\n" \
"v_mfma_f32_16x16x32_bf16 v[40:43], a[220:223], a[96:99], 0\n" \
"ds_read_b128 a[20:23], v49 offset:13312\n" \
"ds_read_b128 a[24:27], v49 offset:14336\n" \
"v_mfma_f32_16x16x32_bf16 v[44:47], a[220:223], a[112:115], 0\n" \
"buffer_load_dwordx4 v50, s[8:11], s64 offen offset:3072 sc1 lds\n" \
"v_mfma_f32_16x16x32_bf16 v[32:35], a[224:227], a[68:71], v[32:35]\n" \
"s_xor_b32 m0, s65, 0xa000\n" \
"ds_read_b128 a[28:31], v49 offset:15360\n" \
"v_mfma_f32_16x16x32_bf16 v[36:39], a[224:227], a[84:87], v[36:39]\n" \
"v_exp_f32_e32 v120, v120\n" \
"v_mfma_f32_16x16x32_bf16 v[40:43], a[224:227], a[100:103], v[40:43]\n" \
"buffer_load_dwordx4 v51, s[8:11], s64 offen offset:0 sc1 lds\n" \
"v_mfma_f32_16x16x32_bf16 v[44:47], a[224:227], a[116:119], v[44:47]\n" \
"v_exp_f32_e32 v121, v121\n" \
"v_mfma_f32_16x16x32_bf16 v[32:35], a[228:231], a[72:75], v[32:35]\n" \
"v_exp_f32_e32 v122, v122\n" \
"v_mfma_f32_16x16x32_bf16 v[36:39], a[228:231], a[88:91], v[36:39]\n" \
"v_exp_f32_e32 v123, v123\n" \
"v_mfma_f32_16x16x32_bf16 v[40:43], a[228:231], a[104:107], v[40:43]\n" \
"buffer_load_dwordx4 v51, s[8:11], s64 offen offset:1024 sc1 lds\n" \
"v_mfma_f32_16x16x32_bf16 v[44:47], a[228:231], a[120:123], v[44:47]\n" \
"v_cvt_pk_bf16_f32 v120, v120, v121\n" \
"v_cvt_pk_bf16_f32 v121, v122, v123\n" \
"v_mfma_f32_16x16x32_bf16 v[32:35], a[232:235], a[76:79], v[32:35]\n" \
"v_exp_f32_e32 v124, v124\n" \
"v_mfma_f32_16x16x32_bf16 v[36:39], a[232:235], a[92:95], v[36:39]\n" \
"v_exp_f32_e32 v125, v125\n" \
"v_mfma_f32_16x16x32_bf16 v[40:43], a[232:235], a[108:111], v[40:43]\n" \
"buffer_load_dwordx4 v51, s[8:11], s64 offen offset:2048 sc1 lds\n" \
"v_mfma_f32_16x16x32_bf16 v[44:47], a[232:235], a[124:127], v[44:47]\n" \
"v_exp_f32_e32 v126, v126\n" \
"v_smfmac_f32_16x16x64_bf16 a[196:199], a[200:203], v[84:91], v21\n" \
"v_cvt_pk_bf16_f32 v122, v124, v125\n" \
"s_waitcnt lgkmcnt(1)\n" \
"v_mfma_f32_16x16x32_bf16 v[220:223], a[188:191], v[84:87], v[220:223]\n" \
"buffer_load_dwordx4 v51, s[8:11], s64 offen offset:3072 sc1 lds\n" \
"v_mfma_f32_16x16x32_bf16 v[252:255], a[188:191], v[88:91], v[252:255]\n" \
"s_add_u32 m0, s93, 0x1000\n" \
"v_and_b32_e32 v120, v12, v120\n" \
"v_mfma_f32_16x16x32_bf16 v[216:219], a[184:187], v[84:87], v[216:219]\n" \
"v_exp_f32_e32 v127, v127\n" \
"v_mfma_f32_16x16x32_bf16 v[248:251], a[184:187], v[88:91], v[248:251]\n" \
"v_cvt_pk_bf16_f32 v123, v126, v127\n" \
"v_and_b32_e32 v121, v13, v121\n" \
"v_mfma_f32_16x16x32_bf16 v[212:215], a[180:183], v[84:87], v[212:215]\n" \
"buffer_load_dwordx4 v4, s[80:83], s92 offen offset:0 lds\n" \
"v_mfma_f32_16x16x32_bf16 v[244:247], a[180:183], v[88:91], v[244:247]\n" \
"v_and_b32_e32 v122, v14, v122\n" \
"v_and_b32_e32 v123, v15, v123\n" \
"v_mfma_f32_16x16x32_bf16 v[208:211], a[176:179], v[84:87], v[208:211]\n" \
"v_accvgpr_read_b32 v56, a196\n" \
"v_accvgpr_read_b32 v24, a197\n" \
"v_mfma_f32_16x16x32_bf16 v[240:243], a[176:179], v[88:91], v[240:243]\n" \
"ds_read_b128 a[128:131], v48 offset:0\n" \
"ds_read_b128 a[144:147], v48 offset:4096\n" \
"v_mfma_f32_16x16x32_bf16 v[204:207], a[172:175], v[84:87], v[204:207]\n" \
"buffer_load_dwordx4 v5, s[80:83], s92 offen offset:1024 lds\n" \
"v_mfma_f32_16x16x32_bf16 v[236:239], a[172:175], v[88:91], v[236:239]\n" \
"ds_read_b128 a[132:135], v48 offset:1024\n" \
"ds_read_b128 a[148:151], v48 offset:5120\n" \
"v_mfma_f32_16x16x32_bf16 v[200:203], a[168:171], v[84:87], v[200:203]\n" \
"ds_read_b128 a[136:139], v48 offset:2048\n" \
"ds_read_b128 a[152:155], v48 offset:6144\n" \
"v_mfma_f32_16x16x32_bf16 v[232:235], a[168:171], v[88:91], v[232:235]\n" \
"buffer_load_dwordx4 v6, s[80:83], s92 offen offset:2048 lds\n" \
"v_mfma_f32_16x16x32_bf16 v[196:199], a[164:167], v[84:87], v[196:199]\n" \
"ds_read_b128 a[140:143], v48 offset:3072\n" \
"ds_read_b128 a[156:159], v48 offset:7168\n" \
"v_mfma_f32_16x16x32_bf16 v[228:231], a[164:167], v[88:91], v[228:231]\n" \
"v_mul_f32_e32 v141, v54, v141\n" \
"v_mul_f32_e32 v142, v54, v142\n" \
"v_mfma_f32_16x16x32_bf16 v[192:195], a[160:163], v[84:87], v[192:195]\n" \
"v_mul_f32_e32 v143, v54, v143\n" \
"v_cvt_pk_bf16_f32 v145, v146, v147\n" \
"v_mfma_f32_16x16x32_bf16 v[224:227], a[160:163], v[88:91], v[224:227]\n" \
"buffer_load_dwordx4 v7, s[80:83], s92 offen offset:3072 lds\n" \
"v_mfma_f32_16x16x32_bf16 a[196:199], a[212:215], v[120:123], a[196:199]\n" \
"v_mul_f32_e32 v148, v54, v148\n" \
"v_mul_f32_e32 v149, v54, v149\n" \
"v_mfma_f32_16x16x32_bf16 v[224:227], a[0:3], v[120:123], v[224:227]\n" \
"v_mul_f32_e32 v150, v54, v150\n" \
"v_mul_f32_e32 v151, v54, v151\n" \
"v_mfma_f32_16x16x32_bf16 v[228:231], a[4:7], v[120:123], v[228:231]\n" \
"v_cvt_pk_bf16_f32 v153, v154, v155\n" \
"v_mul_f32_e32 v156, v54, v156\n" \
"v_mfma_f32_16x16x32_bf16 v[232:235], a[8:11], v[120:123], v[232:235]\n" \
"v_mul_f32_e32 v157, v54, v157\n" \
"v_mul_f32_e32 v158, v54, v158\n" \
"v_mfma_f32_16x16x32_bf16 v[236:239], a[12:15], v[120:123], v[236:239]\n" \
"v_mul_f32_e32 v159, v54, v159\n" \
"v_cvt_pk_bf16_f32 v161, v162, v163\n" \
"v_mfma_f32_16x16x32_bf16 v[240:243], a[16:19], v[120:123], v[240:243]\n" \
"v_mul_f32_e32 v164, v55, v164\n" \
"v_mul_f32_e32 v165, v55, v165\n" \
"v_mfma_f32_16x16x32_bf16 v[244:247], a[20:23], v[120:123], v[244:247]\n" \
"v_mul_f32_e32 v166, v55, v166\n" \
"v_mul_f32_e32 v167, v55, v167\n" \
"v_mfma_f32_16x16x32_bf16 v[248:251], a[24:27], v[120:123], v[248:251]\n" \
"v_cvt_pk_bf16_f32 v169, v170, v171\n" \
"s_waitcnt lgkmcnt(4)\n" \
"v_mfma_f32_16x16x32_bf16 v[252:255], a[28:31], v[120:123], v[252:255]\n" \
"v_mul_f32_e32 v172, v55, v172\n" \
"v_mul_f32_e32 v173, v55, v173\n" \
"v_mul_f32_e32 v174, v55, v174\n" \
"v_mul_f32_e32 v175, v55, v175\n" \
"v_cvt_pk_bf16_f32 v177, v178, v179\n" \
"v_mul_f32_e32 v180, v55, v180\n" \
"v_mul_f32_e32 v181, v55, v181\n" \
"v_mul_f32_e32 v182, v55, v182\n" \
"v_mul_f32_e32 v183, v55, v183\n" \
"v_cvt_pk_bf16_f32 v185, v186, v187\n" \
"v_mul_f32_e32 v188, v55, v188\n" \
"v_mul_f32_e32 v189, v55, v189\n" \
"v_mul_f32_e32 v190, v55, v190\n" \
"v_mul_f32_e32 v191, v55, v191\n" \
"v_cvt_pk_bf16_f32 v130, v132, v133\n" \
"v_cvt_pk_bf16_f32 v131, v134, v135\n" \
"buffer_store_dwordx4 v[128:131], v20, s[56:59], s68 offen offset:0 sc1\n" \
"v_cvt_pk_bf16_f32 v138, v140, v141\n" \
"v_cvt_pk_bf16_f32 v139, v142, v143\n" \
"buffer_store_dwordx4 v[136:139], v20, s[56:59], s68 offen offset:1024 sc1\n" \
"v_cvt_pk_bf16_f32 v146, v148, v149\n" \
"v_cvt_pk_bf16_f32 v147, v150, v151\n" \
"buffer_store_dwordx4 v[144:147], v20, s[56:59], s68 offen offset:2048 sc1\n" \
"v_cvt_pk_bf16_f32 v154, v156, v157\n" \
"v_cvt_pk_bf16_f32 v155, v158, v159\n" \
"buffer_store_dwordx4 v[152:155], v20, s[56:59], s68 offen offset:3072 sc1\n" \
"v_cvt_pk_bf16_f32 v162, v164, v165\n" \
"v_cvt_pk_bf16_f32 v163, v166, v167\n" \
"buffer_store_dwordx4 v[160:163], v20, s[56:59], s69 offen offset:0 sc1\n" \
"v_cvt_pk_bf16_f32 v170, v172, v173\n" \
"v_cvt_pk_bf16_f32 v171, v174, v175\n" \
"buffer_store_dwordx4 v[168:171], v20, s[56:59], s69 offen offset:1024 sc1\n" \
"v_cvt_pk_bf16_f32 v178, v180, v181\n" \
"v_cvt_pk_bf16_f32 v179, v182, v183\n" \
"buffer_store_dwordx4 v[176:179], v20, s[56:59], s69 offen offset:2048 sc1\n" \
"v_cvt_pk_bf16_f32 v186, v188, v189\n" \
"v_cvt_pk_bf16_f32 v187, v190, v191\n" \
"buffer_store_dwordx4 v[184:187], v20, s[56:59], s69 offen offset:3072 sc1\n" \
"s_add_u32 m0, s93, 0x2000\n" \
"v_mfma_f32_16x16x32_bf16 a[208:211], a[216:219], v[128:131], a[208:211]\n" \
"buffer_load_dwordx4 v8, s[80:83], s92 offen offset:0 lds\n" \
"v_mfma_f32_16x16x32_bf16 a[208:211], a[216:219], v[136:139], a[208:211]\n" \
"v_mfma_f32_32x32x16_bf16 v[128:143], a[216:219], a[216:219], 0\n" \
"v_mfma_f32_16x16x32_bf16 a[208:211], a[216:219], v[144:147], a[208:211]\n" \
"v_mfma_f32_16x16x32_bf16 a[208:211], a[216:219], v[152:155], a[208:211]\n" \
"v_mfma_f32_32x32x16_bf16 v[144:159], a[216:219], a[216:219], 0\n" \
"v_add_f32_e32 v56, v56, v24\n" \
"v_mfma_f32_16x16x32_bf16 a[208:211], a[216:219], v[160:163], a[208:211]\n" \
"v_max_f32_e32 v62, v62, v56\n" \
"v_accvgpr_read_b32 v57, a198\n" \
"s_waitcnt vmcnt(21) lgkmcnt(0)\n" \
"s_barrier\n" \
"v_mfma_f32_16x16x32_bf16 v[64:67], a[128:131], a[64:67], v[32:35]\n" \
"s_xor_b32 m0, s65, 0x18000\n" \
"v_accvgpr_read_b32 v25, a199\n" \
"v_mfma_f32_16x16x32_bf16 a[196:199], a[216:219], a[216:219], 0\n" \
"buffer_load_dwordx4 v52, s[8:11], s64 offen offset:0 sc1 lds\n" \
"v_mfma_f32_16x16x32_bf16 v[64:67], a[132:135], a[68:71], v[64:67]\n" \
"v_rcp_f32_e32 v56, v56\n" \
"v_mfma_f32_16x16x32_bf16 v[64:67], a[136:139], a[72:75], v[64:67]\n" \
"v_mfma_f32_16x16x32_bf16 a[208:211], a[216:219], v[168:171], a[208:211]\n" \
"v_mfma_f32_32x32x16_bf16 v[160:175], a[216:219], a[216:219], 0\n" \
"v_mul_f32_e32 v56, v56, v60\n" \
"v_mfma_f32_16x16x32_bf16 a[208:211], a[216:219], v[176:179], a[208:211]\n" \
"v_mul_f32_e32 v192, v56, v192\n" \
"v_mul_f32_e32 v193, v56, v193\n" \
"v_mfma_f32_16x16x32_bf16 v[64:67], a[140:143], a[76:79], v[64:67]\n" \
"buffer_load_dwordx4 v52, s[8:11], s64 offen offset:1024 sc1 lds\n" \
"v_mfma_f32_16x16x32_bf16 v[68:71], a[156:159], a[76:79], v[32:35]\n" \
"v_cvt_pk_bf16_f32 v192, v192, v193\n" \
"v_mul_f32_e32 v194, v56, v194\n" \
"v_mfma_f32_16x16x32_bf16 v[68:71], a[152:155], a[72:75], v[68:71]\n" \
"v_mul_f32_e32 v195, v56, v195\n" \
"v_cvt_pk_bf16_f32 v193, v194, v195\n" \
"v_mfma_f32_16x16x32_bf16 v[68:71], a[148:151], a[68:71], v[68:71]\n" \
"v_mfma_f32_16x16x32_bf16 a[208:211], a[216:219], v[184:187], a[208:211]\n" \
"v_mfma_f32_32x32x16_bf16 v[176:191], a[216:219], a[216:219], 0\n" \
"v_mul_f32_e32 v196, v56, v196\n" \
"v_mul_f32_e32 v197, v56, v197\n" \
"v_mul_f32_e32 v198, v56, v198\n" \
"v_mul_f32_e32 v199, v56, v199\n" \
"v_mul_f32_e32 v200, v56, v200\n" \
"v_mul_f32_e32 v201, v56, v201\n" \
"v_mfma_f32_16x16x32_bf16 v[68:71], a[144:147], a[64:67], v[68:71]\n" \
"buffer_load_dwordx4 v52, s[8:11], s64 offen offset:2048 sc1 lds\n" \
"v_mfma_f32_16x16x32_bf16 v[76:79], a[144:147], a[80:83], v[36:39]\n" \
"v_cvt_pk_bf16_f32 v194, v196, v197\n" \
"v_cvt_pk_bf16_f32 v195, v198, v199\n" \
"buffer_store_dwordx4 v[192:195], v20, s[56:59], s70 offen offset:0 sc1\n" \
"v_mfma_f32_16x16x32_bf16 v[76:79], a[148:151], a[84:87], v[76:79]\n" \
"v_cvt_pk_bf16_f32 v200, v200, v201\n" \
"v_mul_f32_e32 v202, v56, v202\n" \
"v_mfma_f32_16x16x32_bf16 v[76:79], a[152:155], a[88:91], v[76:79]\n" \
"v_mul_f32_e32 v203, v56, v203\n" \
"v_add_f32_e32 v57, v57, v25\n" \
"v_mfma_f32_16x16x32_bf16 v[76:79], a[156:159], a[92:95], v[76:79]\n" \
"buffer_load_dwordx4 v52, s[8:11], s64 offen offset:3072 sc1 lds\n" \
"v_mfma_f32_16x16x32_bf16 v[72:75], a[140:143], a[92:95], v[36:39]\n" \
"s_mov_b32 m0, s65\n" \
"v_cvt_pk_bf16_f32 v201, v202, v203\n" \
"v_mfma_f32_16x16x32_bf16 v[72:75], a[136:139], a[88:91], v[72:75]\n" \
"v_mfma_f32_16x16x32_bf16 a[208:211], a[216:219], v[192:195], a[208:211]\n" \
"v_mul_f32_e32 v204, v56, v204\n" \
"v_mul_f32_e32 v205, v56, v205\n" \
"v_mfma_f32_16x16x32_bf16 v[72:75], a[132:135], a[84:87], v[72:75]\n" \
"v_mul_f32_e32 v206, v56, v206\n" \
"v_mul_f32_e32 v207, v56, v207\n" \
"v_mfma_f32_16x16x32_bf16 v[72:75], a[128:131], a[80:83], v[72:75]\n" \
"buffer_load_dwordx4 v53, s[8:11], s64 offen offset:0 sc1 lds\n" \
"v_mfma_f32_16x16x32_bf16 v[80:83], a[128:131], a[96:99], v[40:43]\n" \
"v_mul_f32_e32 v208, v56, v208\n" \
"v_mul_f32_e32 v209, v56, v209\n" \
"v_mfma_f32_16x16x32_bf16 v[80:83], a[132:135], a[100:103], v[80:83]\n" \
"v_max_f32_e32 v62, v62, v57\n" \
"v_cvt_pk_bf16_f32 v202, v204, v205\n" \
"v_mfma_f32_16x16x32_bf16 v[80:83], a[136:139], a[104:107], v[80:83]\n" \
"buffer_load_dwordx4 v53, s[8:11], s64 offen offset:1024 sc1 lds\n" \
"v_mfma_f32_16x16x32_bf16 v[80:83], a[140:143], a[108:111], v[80:83]\n" \
"v_cvt_pk_bf16_f32 v203, v206, v207\n" \
"v_cvt_pk_bf16_f32 v208, v208, v209\n" \
"buffer_store_dwordx4 v[200:203], v20, s[56:59], s70 offen offset:1024 sc1\n" \
"v_mfma_f32_16x16x32_bf16 v[84:87], a[156:159], a[108:111], v[40:43]\n" \
"v_mul_f32_e32 v210, v56, v210\n" \
"v_mul_f32_e32 v211, v56, v211\n" \
"v_mfma_f32_16x16x32_bf16 v[84:87], a[152:155], a[104:107], v[84:87]\n" \
"v_rcp_f32_e32 v57, v57\n" \
"v_mfma_f32_16x16x32_bf16 v[84:87], a[148:151], a[100:103], v[84:87]\n" \
"buffer_load_dwordx4 v53, s[8:11], s64 offen offset:2048 sc1 lds\n" \
"v_mfma_f32_16x16x32_bf16 v[84:87], a[144:147], a[96:99], v[84:87]\n" \
"v_mfma_f32_16x16x32_bf16 a[208:211], a[216:219], v[200:203], a[208:211]\n" \
"v_mfma_f32_32x32x16_bf16 v[192:207], a[216:219], a[216:219], 0\n" \
"v_cvt_pk_bf16_f32 v209, v210, v211\n" \
"v_mul_f32_e32 v212, v56, v212\n" \
".fill (L_ab + 16384 - .) / 4, 4, 0xBF800000\n" \
"L_ap4:\n" \
"s_cbranch_execz L_ap0\n" \
"v_mul_f32_e32 v213, v56, v213\n" \
"v_mul_f32_e32 v214, v56, v214\n" \
"v_mul_f32_e32 v215, v56, v215\n" \
"v_mul_f32_e32 v216, v56, v216\n" \
"v_mfma_f32_16x16x32_bf16 v[92:95], a[144:147], a[112:115], v[44:47]\n" \
"ds_read_b128 a[144:147], v48 offset:12288\n" \
"v_mul_f32_e32 v217, v56, v217\n" \
"v_mfma_f32_16x16x32_bf16 v[92:95], a[148:151], a[116:119], v[92:95]\n" \
"ds_read_b128 a[148:151], v48 offset:13312\n" \
"v_cvt_pk_bf16_f32 v210, v212, v213\n" \
"v_mfma_f32_16x16x32_bf16 v[92:95], a[152:155], a[120:123], v[92:95]\n" \
"ds_read_b128 a[152:155], v48 offset:14336\n" \
"v_cvt_pk_bf16_f32 v211, v214, v215\n" \
"buffer_store_dwordx4 v[208:211], v20, s[56:59], s70 offen offset:2048 sc1\n" \
"v_mfma_f32_16x16x32_bf16 v[92:95], a[156:159], a[124:127], v[92:95]\n" \
"ds_read_b128 a[156:159], v48 offset:15360\n" \
"v_cvt_pk_bf16_f32 v216, v216, v217\n" \
"v_mfma_f32_16x16x32_bf16 v[88:91], a[140:143], a[124:127], v[44:47]\n" \
"ds_read_b128 a[140:143], v48 offset:11264\n" \
"v_mul_f32_e32 v218, v56, v218\n" \
"v_mfma_f32_16x16x32_bf16 v[88:91], a[136:139], a[120:123], v[88:91]\n" \
"ds_read_b128 a[136:139], v48 offset:10240\n" \
"v_mul_f32_e32 v219, v56, v219\n" \
"v_mfma_f32_16x16x32_bf16 v[88:91], a[132:135], a[116:119], v[88:91]\n" \
"ds_read_b128 a[132:135], v48 offset:9216\n" \
"v_mul_f32_e32 v57, v57, v61\n" \
"v_mfma_f32_16x16x32_bf16 v[88:91], a[128:131], a[112:115], v[88:91]\n" \
"ds_read_b128 a[128:131], v48 offset:8192\n" \
"v_mfma_f32_16x16x32_bf16 a[208:211], a[216:219], v[208:211], a[208:211]\n" \
"v_cvt_pk_bf16_f32 v217, v218, v219\n" \
"v_mul_f32_e32 v220, v56, v220\n" \
"v_mul_f32_e32 v221, v56, v221\n" \
"v_mul_f32_e32 v222, v56, v222\n" \
"v_mul_f32_e32 v223, v56, v223\n" \
"v_mul_f32_e32 v224, v57, v224\n" \
"v_mul_f32_e32 v225, v57, v225\n" \
"v_cvt_pk_bf16_f32 v218, v220, v221\n" \
"v_cvt_pk_bf16_f32 v219, v222, v223\n" \
"buffer_store_dwordx4 v[216:219], v20, s[56:59], s70 offen offset:3072 sc1\n" \
"v_cvt_pk_bf16_f32 v224, v224, v225\n" \
"v_mul_f32_e32 v226, v57, v226\n" \
"v_mul_f32_e32 v227, v57, v227\n" \
"v_mfma_f32_16x16x32_bf16 a[208:211], a[216:219], v[216:219], a[208:211]\n" \
"v_mfma_f32_32x32x16_bf16 v[208:223], a[216:219], a[216:219], 0\n" \
"v_cvt_pk_bf16_f32 v225, v226, v227\n" \
"v_mul_f32_e32 v228, v57, v228\n" \
"v_mul_f32_e32 v229, v57, v229\n" \
"v_mul_f32_e32 v230, v57, v230\n" \
"v_mul_f32_e32 v231, v57, v231\n" \
"v_mul_f32_e32 v232, v57, v232\n" \
"v_mul_f32_e32 v233, v57, v233\n" \
"v_cvt_pk_bf16_f32 v226, v228, v229\n" \
"v_cvt_pk_bf16_f32 v227, v230, v231\n" \
"buffer_store_dwordx4 v[224:227], v20, s[56:59], s71 offen offset:0 sc1\n" \
"v_cvt_pk_bf16_f32 v232, v232, v233\n" \
"v_mul_f32_e32 v234, v57, v234\n" \
"v_mul_f32_e32 v235, v57, v235\n" \
"v_mfma_f32_16x16x32_bf16 a[208:211], a[216:219], v[224:227], a[208:211]\n" \
"v_cvt_pk_bf16_f32 v233, v234, v235\n" \
"v_mul_f32_e32 v236, v57, v236\n" \
"v_mul_f32_e32 v237, v57, v237\n" \
"v_mul_f32_e32 v238, v57, v238\n" \
"v_mul_f32_e32 v239, v57, v239\n" \
"v_mul_f32_e32 v240, v57, v240\n" \
"v_mul_f32_e32 v241, v57, v241\n" \
"v_cvt_pk_bf16_f32 v234, v236, v237\n" \
"v_cvt_pk_bf16_f32 v235, v238, v239\n" \
"buffer_store_dwordx4 v[232:235], v20, s[56:59], s71 offen offset:1024 sc1\n" \
"v_cvt_pk_bf16_f32 v240, v240, v241\n" \
"v_mul_f32_e32 v242, v57, v242\n" \
"v_mul_f32_e32 v243, v57, v243\n" \
"v_mfma_f32_16x16x32_bf16 a[208:211], a[216:219], v[232:235], a[208:211]\n" \
"v_mfma_f32_32x32x16_bf16 v[224:239], a[216:219], a[216:219], 0\n" \
"v_cvt_pk_bf16_f32 v241, v242, v243\n" \
"v_mul_f32_e32 v244, v57, v244\n" \
"v_mul_f32_e32 v245, v57, v245\n" \
"v_mul_f32_e32 v246, v57, v246\n" \
"v_mul_f32_e32 v247, v57, v247\n" \
"v_mul_f32_e32 v248, v57, v248\n" \
"v_mul_f32_e32 v249, v57, v249\n" \
"v_cvt_pk_bf16_f32 v242, v244, v245\n" \
"v_cvt_pk_bf16_f32 v243, v246, v247\n" \
"buffer_store_dwordx4 v[240:243], v20, s[56:59], s71 offen offset:2048 sc1\n" \
"v_cvt_pk_bf16_f32 v248, v248, v249\n" \
"v_mul_f32_e32 v250, v57, v250\n" \
"v_mul_f32_e32 v251, v57, v251\n" \
"v_mfma_f32_16x16x32_bf16 a[208:211], a[216:219], v[240:243], a[208:211]\n" \
"v_cvt_pk_bf16_f32 v249, v250, v251\n" \
"v_mul_f32_e32 v252, v57, v252\n" \
"v_mul_f32_e32 v253, v57, v253\n" \
"v_mul_f32_e32 v254, v57, v254\n" \
"v_mul_f32_e32 v255, v57, v255\n" \
"v_cvt_pk_bf16_f32 v250, v252, v253\n" \
"v_cvt_pk_bf16_f32 v251, v254, v255\n" \
"buffer_store_dwordx4 v[248:251], v20, s[56:59], s71 offen offset:3072 sc1\n" \
"buffer_load_dwordx4 v53, s[8:11], s64 offen offset:3072 sc1 lds\n" \
"s_waitcnt vmcnt(16) lgkmcnt(0)\n" \
"s_barrier\n" \
"v_mfma_f32_16x16x32_bf16 a[208:211], a[216:219], v[248:251], a[208:211]\n" \
"v_mfma_f32_32x32x16_bf16 v[240:255], a[216:219], a[216:219], 0\n" \
"v_exp_f32_e32 v70, v70\n" \
"v_exp_f32_e32 v71, v71\n" \
"v_exp_f32_e32 v72, v72\n" \
"v_exp_f32_e32 v73, v73\n" \
"s_add_u32 s64, s64, 0x10000\n" \
"v_cmp_lt_f32_e64 s[72:73], s21, v62\n" \
"v_readlane_b32 s95, v11, 3\n" \
"v_accvgpr_read_b32 v63, a208\n" \
"v_cmp_u_f32_e32 vcc, v63, v63\n" \
"v_exp_f32_e32 v68, v68\n" \
"v_exp_f32_e32 v69, v69\n" \
"v_exp_f32_e32 v74, v74\n" \
"v_exp_f32_e32 v75, v75\n" \
"s_or_b64 vcc, s[72:73], vcc\n" \
"s_cselect_b32 s95, 0x40000000, s95\n" \
"s_cselect_b32 s94, 0x7f800000, 0\n" \
"v_cvt_pk_bf16_f32 v71, v70, v71\n" \
"v_exp_f32_e32 v66, v66\n" \
"v_exp_f32_e32 v67, v67\n" \
"v_exp_f32_e32 v64, v64\n" \
"v_exp_f32_e32 v65, v65\n" \
"v_cvt_pk_bf16_f32 v72, v72, v73\n" \
"v_exp_f32_e32 v76, v76\n" \
"v_exp_f32_e32 v77, v77\n" \
"v_exp_f32_e32 v78, v78\n" \
"v_exp_f32_e32 v79, v79\n" \
"v_cvt_pk_bf16_f32 v70, v68, v69\n" \
"v_cvt_pk_bf16_f32 v73, v74, v75\n" \
"v_add_u32_e32 v49, 0x10000, v48\n" \
"s_xor_b32 s65, s65, 0x8000\n" \
"v_cvt_pk_bf16_f32 v69, v66, v67\n" \
"v_cvt_pk_bf16_f32 v68, v64, v65\n" \
"v_cvt_pk_bf16_f32 v74, v76, v77\n" \
"v_cvt_pk_bf16_f32 v75, v78, v79\n" \
"v_xor_b32_e32 v48, 0x8000, v48\n" \
"s_sub_u32 s66, s19, 1\n" \
"s_cbranch_scc0 L_2_%=\n" \
"L_f1_%=:\n" \
"v_bfe_u32 v60, v20, 2, 7\n" \
"v_readlane_b32 s96, v11, 1\n" \
"ds_read_b128 a[160:163], v49 offset:0\n" \
"ds_read_b128 a[164:167], v49 offset:1024\n" \
"ds_read_b128 a[168:171], v49 offset:2048\n" \
"v_mfma_f32_16x16x32_bf16 v[104:107], a[128:131], a[80:83], v[36:39]\n" \
"v_cvt_pk_bf16_f32 v192, v192, v193\n" \
"v_cvt_pk_bf16_f32 v224, v224, v225\n" \
"buffer_load_dword v57, v60, s[56:59], s96 offen sc1\n" \
"v_readlane_b32 s93, v11, 20\n" \
"v_cvt_pk_bf16_f32 v193, v194, v195\n" \
"v_cvt_pk_bf16_f32 v225, v226, v227\n" \
"ds_read_b128 a[172:175], v49 offset:3072\n" \
"v_readlane_b32 s84, v11, 15\n" \
"v_readlane_b32 s85, v11, 16\n" \
"v_readlane_b32 s86, v11, 17\n" \
"s_mov_b32 s87, 0x20000\n" \
"v_readlane_b32 s92, v11, 18\n" \
"s_add_u32 m0, s93, 0x0\n" \
"v_cvt_pk_bf16_f32 v194, v196, v197\n" \
"v_cvt_pk_bf16_f32 v195, v198, v199\n" \
"v_cvt_pk_bf16_f32 v226, v228, v229\n" \
"v_cvt_pk_bf16_f32 v227, v230, v231\n" \
"v_mfma_f32_16x16x32_bf16 v[104:107], a[132:135], a[84:87], v[104:107]\n" \
"v_lshl_add_u32 v50, s15, 2, v20\n" \
"v_cvt_pk_bf16_f32 v196, v200, v201\n" \
"v_cvt_pk_bf16_f32 v197, v202, v203\n" \
"v_cvt_pk_bf16_f32 v198, v204, v205\n" \
"v_cvt_pk_bf16_f32 v199, v206, v207\n" \
"buffer_load_dwordx4 v0, s[84:87], s92 offen offset:0 lds\n" \
"v_cvt_pk_bf16_f32 v228, v232, v233\n" \
"v_cvt_pk_bf16_f32 v229, v234, v235\n" \
"v_cvt_pk_bf16_f32 v230, v236, v237\n" \
"v_cvt_pk_bf16_f32 v231, v238, v239\n" \
"v_add_u32_e32 v50, 0x1a000, v50\n" \
"v_mov_b32_e32 v61, s62\n" \
"v_cvt_pk_bf16_f32 v200, v208, v209\n" \
"v_cvt_pk_bf16_f32 v201, v210, v211\n" \
"v_cvt_pk_bf16_f32 v202, v212, v213\n" \
"v_cvt_pk_bf16_f32 v203, v214, v215\n" \
"v_cvt_pk_bf16_f32 v204, v216, v217\n" \
"v_cvt_pk_bf16_f32 v205, v218, v219\n" \
"v_cvt_pk_bf16_f32 v206, v220, v221\n" \
"v_cvt_pk_bf16_f32 v207, v222, v223\n" \
"v_cvt_pk_bf16_f32 v232, v240, v241\n" \
"v_cvt_pk_bf16_f32 v233, v242, v243\n" \
"v_cvt_pk_bf16_f32 v234, v244, v245\n" \
"v_cvt_pk_bf16_f32 v235, v246, v247\n" \
"v_cvt_pk_bf16_f32 v236, v248, v249\n" \
"v_cvt_pk_bf16_f32 v237, v250, v251\n" \
"v_cvt_pk_bf16_f32 v238, v252, v253\n" \
"buffer_load_dwordx4 v1, s[84:87], s92 offen offset:1024 lds\n" \
"v_cvt_pk_bf16_f32 v239, v254, v255\n" \
"ds_write_b128 v50, v[192:195] offset:0\n" \
"v_mfma_f32_16x16x32_bf16 v[104:107], a[136:139], a[88:91], v[104:107]\n" \
"ds_read_b128 a[176:179], v49 offset:4096\n" \
"v_add_u32_e32 v56, 0x49000, v20\n" \
"v_mfma_f32_16x16x32_bf16 v[104:107], a[140:143], a[92:95], v[104:107]\n" \
"ds_write_b128 v50, v[196:199] offset:1024\n" \
"v_mfma_f32_16x16x32_bf16 v[108:111], a[156:159], a[92:95], v[36:39]\n" \
"v_readlane_b32 s97, v11, 35\n" \
"v_readlane_b32 s98, v11, 36\n" \
"ds_read_b128 a[180:183], v49 offset:5120\n" \
"v_mfma_f32_16x16x32_bf16 v[108:111], a[152:155], a[88:91], v[108:111]\n" \
"ds_write_b128 v50, v[200:203] offset:2048\n" \
"v_mfma_f32_16x16x32_bf16 v[108:111], a[148:151], a[84:87], v[108:111]\n" \
"buffer_load_dwordx4 v2, s[84:87], s92 offen offset:2048 lds\n" \
"ds_read_b128 a[184:187], v49 offset:6144\n" \
"v_mfma_f32_16x16x32_bf16 v[108:111], a[144:147], a[80:83], v[108:111]\n" \
"ds_write_b128 v50, v[204:207] offset:3072\n" \
"v_and_b32_e32 v68, v12, v68\n" \
"v_and_b32_e32 v69, v13, v69\n" \
"v_and_b32_e32 v70, v14, v70\n" \
"v_and_b32_e32 v71, v15, v71\n" \
"ds_read_b128 a[188:191], v49 offset:7168\n" \
"ds_read_b128 a[0:3], v20 offset:8192\n" \
"ds_write_b128 v50, v[224:227] offset:4096\n" \
"s_waitcnt lgkmcnt(10)\n" \
"v_mfma_f32_16x16x32_bf16 v[128:131], a[160:163], v[68:71], v[128:131]\n" \
"ds_read_b128 a[4:7], v20 offset:9216\n" \
"ds_read_b128 a[8:11], v20 offset:10240\n" \
"v_mfma_f32_16x16x32_bf16 v[160:163], a[160:163], v[72:75], v[160:163]\n" \
"ds_write_b128 v50, v[228:231] offset:5120\n" \
"buffer_load_dwordx4 v3, s[84:87], s92 offen offset:3072 lds\n" \
"s_add_u32 m0, s93, 0x1000\n" \
"v_mfma_f32_16x16x32_bf16 v[132:135], a[164:167], v[68:71], v[132:135]\n" \
"ds_read_b128 a[12:15], v20 offset:11264\n" \
"ds_read_b128 a[28:31], v20 offset:15360\n" \
"ds_write_b128 v50, v[232:235] offset:6144\n" \
"v_mfma_f32_16x16x32_bf16 v[164:167], a[164:167], v[72:75], v[164:167]\n" \
"ds_read_b128 a[24:27], v20 offset:14336\n" \
"ds_read_b128 a[20:23], v20 offset:13312\n" \
"v_mfma_f32_16x16x32_bf16 v[136:139], a[168:171], v[68:71], v[136:139]\n" \
"ds_write_b128 v50, v[236:239] offset:7168\n" \
"v_mfma_f32_16x16x32_bf16 v[168:171], a[168:171], v[72:75], v[168:171]\n" \
"ds_read_b128 a[16:19], v20 offset:12288\n" \
"v_xor_b32_e32 v48, 0x4000, v49\n" \
"v_mfma_f32_16x16x32_bf16 v[140:143], a[172:175], v[68:71], v[140:143]\n" \
"buffer_load_dwordx4 v4, s[84:87], s92 offen offset:0 lds\n" \
"v_mfma_f32_16x16x32_bf16 v[172:175], a[172:175], v[72:75], v[172:175]\n" \
"ds_read_b32 v58, v18 offset:256\n" \
"s_waitcnt lgkmcnt(15)\n" \
"v_mfma_f32_16x16x32_bf16 v[144:147], a[176:179], v[68:71], v[144:147]\n" \
"ds_read_b32 v59, v18 offset:384\n" \
"ds_read_b128 a[32:35], v48 offset:8192\n" \
"v_mfma_f32_16x16x32_bf16 v[176:179], a[176:179], v[72:75], v[176:179]\n" \
"ds_read_b128 a[36:39], v48 offset:9216\n" \
"ds_read_b128 a[40:43], v48 offset:10240\n" \
"v_mfma_f32_16x16x32_bf16 v[148:151], a[180:183], v[68:71], v[148:151]\n" \
"ds_read_b128 a[44:47], v48 offset:11264\n" \
"ds_read_b128 a[48:51], v48 offset:12288\n" \
"v_mfma_f32_16x16x32_bf16 v[180:183], a[180:183], v[72:75], v[180:183]\n" \
"ds_read_b128 a[52:55], v48 offset:13312\n" \
"ds_read_b128 a[56:59], v48 offset:14336\n" \
"v_mfma_f32_16x16x32_bf16 v[152:155], a[184:187], v[68:71], v[152:155]\n" \
"buffer_load_dwordx4 v5, s[84:87], s92 offen offset:1024 lds\n" \
"v_mfma_f32_16x16x32_bf16 v[184:187], a[184:187], v[72:75], v[184:187]\n" \
"ds_read_b128 a[60:63], v48 offset:15360\n" \
"s_waitcnt lgkmcnt(15)\n" \
"v_mfma_f32_16x16x32_bf16 v[156:159], a[188:191], v[68:71], v[156:159]\n" \
"ds_read_b128 a[128:131], v49 offset:8192\n" \
"ds_read_b128 a[132:135], v49 offset:9216\n" \
"v_mfma_f32_16x16x32_bf16 v[188:191], a[188:191], v[72:75], v[188:191]\n" \
"ds_read_b128 a[136:139], v49 offset:10240\n" \
"ds_read_b128 a[140:143], v49 offset:11264\n" \
"v_smfmac_f32_16x16x64_bf16 a[192:195], a[200:203], v[68:75], v21\n" \
"ds_read_b128 a[144:147], v49 offset:12288\n" \
"ds_read_b128 a[148:151], v49 offset:13312\n" \
"v_mfma_f32_16x16x32_bf16 v[112:115], a[0:3], a[64:67], v[32:35]\n" \
"ds_read_b128 a[152:155], v49 offset:14336\n" \
"ds_read_b128 a[156:159], v49 offset:15360\n" \
"v_mfma_f32_16x16x32_bf16 v[112:115], a[4:7], a[68:71], v[112:115]\n" \
"buffer_load_dwordx4 v6, s[84:87], s92 offen offset:2048 lds\n" \
"v_mfma_f32_16x16x32_bf16 v[112:115], a[8:11], a[72:75], v[112:115]\n" \
"v_accvgpr_read_b32 v40, a196\n" \
"v_accvgpr_read_b32 v24, a197\n" \
"v_mfma_f32_16x16x32_bf16 v[112:115], a[12:15], a[76:79], v[112:115]\n" \
"v_accvgpr_read_b32 v41, a198\n" \
"v_accvgpr_read_b32 v25, a199\n" \
"v_mfma_f32_16x16x32_bf16 v[116:119], a[28:31], a[76:79], v[32:35]\n" \
"s_waitcnt vmcnt(7)\n" \
"v_cmp_ne_u32_e32 vcc, s62, v57\n" \
"s_mov_b64 exec, 1\n" \
"buffer_store_dword v61, off, s[56:59], s95 offset:0 sc1\n" \
"s_mov_b64 exec, -1\n" \
"s_waitcnt lgkmcnt(15)\n" \
"v_mfma_f32_16x16x32_bf16 v[116:119], a[24:27], a[72:75], v[116:119]\n" \
"s_cbranch_vccnz L_so1_%=\n" \
"L_ro1_%=:\n" \
"v_add_f32_e32 v40, v40, v24\n" \
"v_mfma_f32_16x16x32_bf16 v[116:119], a[20:23], a[68:71], v[116:119]\n" \
"v_add_f32_e32 v41, v41, v25\n" \
"v_add_f32_e32 v40, s94, v40\n" \
"v_mfma_f32_16x16x32_bf16 v[116:119], a[16:19], a[64:67], v[116:119]\n" \
"v_add_f32_e32 v41, s94, v41\n" \
"ds_write_b64 v19, v[40:41]\n" \
"v_mfma_f32_16x16x32_bf16 v[124:127], a[16:19], a[80:83], v[36:39]\n" \
"v_add_u32_e32 v52, s15, v20\n" \
"v_add_u32_e32 v51, 0x10000, v52\n" \
"v_mfma_f32_16x16x32_bf16 v[124:127], a[20:23], a[84:87], v[124:127]\n" \
"v_exp_f32_e32 v104, v104\n" \
"buffer_load_dwordx4 v7, s[84:87], s92 offen offset:3072 lds\n" \
"s_add_u32 m0, s93, 0x2000\n" \
"v_mfma_f32_16x16x32_bf16 v[124:127], a[24:27], a[88:91], v[124:127]\n" \
"v_exp_f32_e32 v105, v105\n" \
"v_mfma_f32_16x16x32_bf16 v[124:127], a[28:31], a[92:95], v[124:127]\n" \
"v_exp_f32_e32 v106, v106\n" \
"v_mfma_f32_16x16x32_bf16 v[120:123], a[12:15], a[92:95], v[36:39]\n" \
"v_exp_f32_e32 v107, v107\n" \
"v_mfma_f32_16x16x32_bf16 v[120:123], a[8:11], a[88:91], v[120:123]\n" \
"v_cvt_pk_bf16_f32 v104, v104, v105\n" \
"v_cvt_pk_bf16_f32 v105, v106, v107\n" \
"v_mfma_f32_16x16x32_bf16 v[120:123], a[4:7], a[84:87], v[120:123]\n" \
"v_exp_f32_e32 v108, v108\n" \
"buffer_load_dwordx4 v8, s[84:87], s92 offen offset:0 lds\n" \
"v_mfma_f32_16x16x32_bf16 v[120:123], a[0:3], a[80:83], v[120:123]\n" \
"v_exp_f32_e32 v109, v109\n" \
"v_exp_f32_e32 v110, v110\n" \
"v_exp_f32_e32 v111, v111\n" \
"s_waitcnt lgkmcnt(0)\n" \
"s_barrier\n" \
"ds_read_b128 v[208:211], v52 offset:0\n" \
"ds_read_b128 v[212:215], v52 offset:1024\n" \
"ds_read_b128 v[216:219], v52 offset:2048\n" \
"ds_read_b128 v[220:223], v52 offset:3072\n" \
"ds_read_b128 v[240:243], v51 offset:32768\n" \
"ds_read_b128 v[244:247], v51 offset:33792\n" \
"ds_read_b128 v[248:251], v51 offset:34816\n" \
"ds_read_b128 v[252:255], v51 offset:35840\n" \
"buffer_load_dwordx4 a[220:223], v20, s[56:59], s60 offen offset:0 sc1\n" \
"v_cvt_pk_bf16_f32 v106, v108, v109\n" \
"v_cvt_pk_bf16_f32 v107, v110, v111\n" \
"v_and_b32_e32 v104, v12, v104\n" \
"v_and_b32_e32 v105, v13, v105\n" \
"v_and_b32_e32 v106, v14, v106\n" \
"v_and_b32_e32 v107, v15, v107\n" \
"v_xor_b32_e32 v63, 0x400, v19\n" \
"ds_read_b64 v[42:43], v63\n" \
"v_mfma_f32_16x16x32_bf16 a[192:195], a[212:215], v[104:107], a[192:195]\n" \
"v_mov_b32_e32 v194, v28\n" \
"v_mov_b32_e32 v195, v29\n" \
"v_mfma_f32_16x16x32_bf16 v[160:163], a[128:131], v[104:107], v[160:163]\n" \
"v_mov_b32_e32 v196, 0\n" \
"v_mov_b32_e32 v197, 0\n" \
"v_mfma_f32_16x16x32_bf16 v[164:167], a[132:135], v[104:107], v[164:167]\n" \
"v_mov_b32_e32 v192, 0\n" \
"v_mov_b32_e32 v193, 0\n" \
"v_mfma_f32_16x16x32_bf16 v[168:171], a[136:139], v[104:107], v[168:171]\n" \
"buffer_load_dwordx4 a[236:239], v56, s[56:59], s60 offen offset:0 sc1\n" \
"v_mfma_f32_16x16x32_bf16 v[172:175], a[140:143], v[104:107], v[172:175]\n" \
"v_exp_f32_e32 v118, v118\n" \
"v_mfma_f32_16x16x32_bf16 v[176:179], a[144:147], v[104:107], v[176:179]\n" \
"v_exp_f32_e32 v119, v119\n" \
"v_mfma_f32_16x16x32_bf16 v[180:183], a[148:151], v[104:107], v[180:183]\n" \
"v_exp_f32_e32 v120, v120\n" \
"v_mfma_f32_16x16x32_bf16 v[184:187], a[152:155], v[104:107], v[184:187]\n" \
"v_exp_f32_e32 v121, v121\n" \
"v_mfma_f32_16x16x32_bf16 v[188:191], a[156:159], v[104:107], v[188:191]\n" \
"v_exp_f32_e32 v116, v116\n" \
"s_waitcnt lgkmcnt(1)\n" \
"s_barrier\n" \
"buffer_load_dwordx4 a[224:227], v20, s[56:59], s60 offen offset:1024 sc1\n" \
"v_mfma_f32_16x16x32_bf16 v[128:131], v[194:197], v[208:211], v[128:131]\n" \
"v_exp_f32_e32 v117, v117\n" \
"v_mfma_f32_16x16x32_bf16 v[132:135], v[192:195], v[208:211], v[132:135]\n" \
"v_exp_f32_e32 v122, v122\n" \
"v_mfma_f32_16x16x32_bf16 v[136:139], v[194:197], v[212:215], v[136:139]\n" \
"v_exp_f32_e32 v123, v123\n" \
"v_mfma_f32_16x16x32_bf16 v[140:143], v[192:195], v[212:215], v[140:143]\n" \
"v_cvt_pk_bf16_f32 v119, v118, v119\n" \
"v_cvt_pk_bf16_f32 v120, v120, v121\n" \
"v_mfma_f32_16x16x32_bf16 v[144:147], v[194:197], v[216:219], v[144:147]\n" \
"v_exp_f32_e32 v114, v114\n" \
"v_mfma_f32_16x16x32_bf16 v[148:151], v[192:195], v[216:219], v[148:151]\n" \
"buffer_load_dwordx4 a[240:243], v56, s[56:59], s60 offen offset:1024 sc1\n" \
"v_mfma_f32_16x16x32_bf16 v[152:155], v[194:197], v[220:223], v[152:155]\n" \
"v_exp_f32_e32 v115, v115\n" \
"v_mfma_f32_16x16x32_bf16 v[156:159], v[192:195], v[220:223], v[156:159]\n" \
"v_exp_f32_e32 v112, v112\n" \
"v_mfma_f32_16x16x32_bf16 v[160:163], v[194:197], v[240:243], v[160:163]\n" \
"v_exp_f32_e32 v113, v113\n" \
"v_mfma_f32_16x16x32_bf16 v[164:167], v[192:195], v[240:243], v[164:167]\n" \
"v_exp_f32_e32 v124, v124\n" \
"v_mfma_f32_16x16x32_bf16 v[168:171], v[194:197], v[244:247], v[168:171]\n" \
"v_exp_f32_e32 v125, v125\n" \
"v_mfma_f32_16x16x32_bf16 v[172:175], v[192:195], v[244:247], v[172:175]\n" \
"buffer_load_dwordx4 a[228:231], v20, s[56:59], s60 offen offset:2048 sc1\n" \
"v_mfma_f32_16x16x32_bf16 v[176:179], v[194:197], v[248:251], v[176:179]\n" \
"v_exp_f32_e32 v126, v126\n" \
"v_mfma_f32_16x16x32_bf16 v[180:183], v[192:195], v[248:251], v[180:183]\n" \
"v_exp_f32_e32 v127, v127\n" \
"v_mfma_f32_16x16x32_bf16 v[184:187], v[194:197], v[252:255], v[184:187]\n" \
"v_cvt_pk_bf16_f32 v118, v116, v117\n" \
"v_cvt_pk_bf16_f32 v121, v122, v123\n" \
"v_mfma_f32_16x16x32_bf16 v[188:191], v[192:195], v[252:255], v[188:191]\n" \
"v_cvt_pk_bf16_f32 v117, v114, v115\n" \
"v_cvt_pk_bf16_f32 v116, v112, v113\n" \
"v_cvt_pk_bf16_f32 v122, v124, v125\n" \
"v_cvt_pk_bf16_f32 v123, v126, v127\n" \
"s_nop 1\n" \
"v_smfmac_f32_16x16x64_bf16 a[192:195], a[200:203], v[116:123], v21\n" \
"buffer_load_dwordx4 a[244:247], v56, s[56:59], s60 offen offset:2048 sc1\n" \
"v_mfma_f32_16x16x32_bf16 v[128:131], a[32:35], v[116:119], v[128:131]\n" \
"v_mfma_f32_16x16x32_bf16 v[160:163], a[32:35], v[120:123], v[160:163]\n" \
"v_mfma_f32_16x16x32_bf16 v[132:135], a[36:39], v[116:119], v[132:135]\n" \
"v_mfma_f32_16x16x32_bf16 v[164:167], a[36:39], v[120:123], v[164:167]\n" \
"v_mfma_f32_16x16x32_bf16 v[136:139], a[40:43], v[116:119], v[136:139]\n" \
"v_mfma_f32_16x16x32_bf16 v[168:171], a[40:43], v[120:123], v[168:171]\n" \
"buffer_load_dwordx4 a[232:235], v20, s[56:59], s60 offen offset:3072 sc1\n" \
"v_mfma_f32_16x16x32_bf16 v[140:143], a[44:47], v[116:119], v[140:143]\n" \
"v_accvgpr_read_b32 v54, a192\n" \
"v_accvgpr_read_b32 v22, a193\n" \
"v_mfma_f32_16x16x32_bf16 v[172:175], a[44:47], v[120:123], v[172:175]\n" \
"v_add_f32_e32 v54, v54, v22\n" \
"s_waitcnt lgkmcnt(0)\n" \
"v_add_f32_e32 v54, v54, v42\n" \
"v_mfma_f32_16x16x32_bf16 v[144:147], a[48:51], v[116:119], v[144:147]\n" \
"v_max_f32_e32 v62, v62, v54\n" \
"v_accvgpr_read_b32 v55, a194\n" \
"v_mfma_f32_16x16x32_bf16 v[176:179], a[48:51], v[120:123], v[176:179]\n" \
"v_rcp_f32_e32 v54, v54\n" \
"v_mfma_f32_16x16x32_bf16 v[148:151], a[52:55], v[116:119], v[148:151]\n" \
"v_mul_f32_e32 v54, v54, v58\n" \
"v_mul_f32_e32 v128, v54, v128\n" \
"buffer_load_dwordx4 a[248:251], v56, s[56:59], s60 offen offset:3072 sc1\n" \
"v_mfma_f32_16x16x32_bf16 v[180:183], a[52:55], v[120:123], v[180:183]\n" \
"v_mul_f32_e32 v129, v54, v129\n" \
"v_mul_f32_e32 v130, v54, v130\n" \
"v_mul_f32_e32 v131, v54, v131\n" \
"v_mul_f32_e32 v132, v54, v132\n" \
"v_mul_f32_e32 v133, v54, v133\n" \
"v_mul_f32_e32 v134, v54, v134\n" \
"v_mul_f32_e32 v135, v54, v135\n" \
"v_cvt_pk_bf16_f32 v64, v128, v129\n" \
"v_cvt_pk_bf16_f32 v65, v130, v131\n" \
"v_cvt_pk_bf16_f32 v66, v132, v133\n" \
"v_cvt_pk_bf16_f32 v67, v134, v135\n" \
"v_accvgpr_read_b32 v23, a195\n" \
"v_mfma_f32_16x16x32_bf16 v[152:155], a[56:59], v[116:119], v[152:155]\n" \
"v_mfma_f32_16x16x32_bf16 a[208:211], a[216:219], v[64:67], a[208:211]\n" \
"v_mul_f32_e32 v136, v54, v136\n" \
"v_mul_f32_e32 v137, v54, v137\n" \
"buffer_load_dwordx4 a[96:99], v20, s[56:59], s97 offen offset:0 sc1\n" \
"v_mul_f32_e32 v138, v54, v138\n" \
"v_mul_f32_e32 v139, v54, v139\n" \
"v_mul_f32_e32 v140, v54, v140\n" \
"v_mul_f32_e32 v141, v54, v141\n" \
"v_mul_f32_e32 v142, v54, v142\n" \
"v_mul_f32_e32 v143, v54, v143\n" \
"v_add_f32_e32 v55, v55, v23\n" \
"v_cvt_pk_bf16_f32 v68, v136, v137\n" \
"v_cvt_pk_bf16_f32 v69, v138, v139\n" \
"v_cvt_pk_bf16_f32 v70, v140, v141\n" \
"v_cvt_pk_bf16_f32 v71, v142, v143\n" \
"v_add_f32_e32 v55, v55, v43\n" \
"v_mul_f32_e32 v144, v54, v144\n" \
"v_mfma_f32_16x16x32_bf16 a[208:211], a[216:219], v[68:71], a[208:211]\n" \
"v_mul_f32_e32 v145, v54, v145\n" \
"v_mul_f32_e32 v146, v54, v146\n" \
"v_mul_f32_e32 v147, v54, v147\n" \
"v_mul_f32_e32 v148, v54, v148\n" \
"v_mul_f32_e32 v149, v54, v149\n" \
"v_mul_f32_e32 v150, v54, v150\n" \
"buffer_load_dwordx4 a[104:107], v56, s[56:59], s97 offen offset:0 sc1\n" \
"v_mul_f32_e32 v151, v54, v151\n" \
"v_max_f32_e32 v62, v62, v55\n" \
"v_cvt_pk_bf16_f32 v72, v144, v145\n" \
"v_cvt_pk_bf16_f32 v73, v146, v147\n" \
"v_cvt_pk_bf16_f32 v74, v148, v149\n" \
"v_cvt_pk_bf16_f32 v75, v150, v151\n" \
"v_rcp_f32_e32 v55, v55\n" \
"v_mfma_f32_16x16x32_bf16 v[184:187], a[56:59], v[120:123], v[184:187]\n" \
"v_mfma_f32_16x16x32_bf16 a[208:211], a[216:219], v[72:75], a[208:211]\n" \
"v_mul_f32_e32 v152, v54, v152\n" \
"v_mul_f32_e32 v153, v54, v153\n" \
"v_mul_f32_e32 v154, v54, v154\n" \
"v_mul_f32_e32 v155, v54, v155\n" \
"v_cvt_pk_bf16_f32 v76, v152, v153\n" \
"v_cvt_pk_bf16_f32 v77, v154, v155\n" \
"v_mul_f32_e32 v55, v55, v59\n" \
"buffer_load_dwordx4 a[112:115], v20, s[56:59], s98 offen offset:0 sc1\n" \
"v_mul_f32_e32 v160, v55, v160\n" \
"v_mul_f32_e32 v161, v55, v161\n" \
"v_mul_f32_e32 v162, v55, v162\n" \
"v_mul_f32_e32 v163, v55, v163\n" \
"v_mul_f32_e32 v164, v55, v164\n" \
"v_mul_f32_e32 v165, v55, v165\n" \
"v_mul_f32_e32 v166, v55, v166\n" \
"v_mul_f32_e32 v167, v55, v167\n" \
"v_cvt_pk_bf16_f32 v80, v160, v161\n" \
"v_cvt_pk_bf16_f32 v81, v162, v163\n" \
"v_cvt_pk_bf16_f32 v82, v164, v165\n" \
"v_cvt_pk_bf16_f32 v83, v166, v167\n" \
"v_mfma_f32_16x16x32_bf16 v[156:159], a[60:63], v[116:119], v[156:159]\n" \
"v_mul_f32_e32 v168, v55, v168\n" \
"v_mul_f32_e32 v169, v55, v169\n" \
"v_mul_f32_e32 v170, v55, v170\n" \
"v_mul_f32_e32 v171, v55, v171\n" \
"v_mul_f32_e32 v172, v55, v172\n" \
"v_mul_f32_e32 v173, v55, v173\n" \
"v_mul_f32_e32 v174, v55, v174\n" \
"buffer_load_dwordx4 a[120:123], v56, s[56:59], s98 offen offset:0 sc1\n" \
"v_mul_f32_e32 v156, v54, v156\n" \
"v_mul_f32_e32 v157, v54, v157\n" \
"v_mul_f32_e32 v158, v54, v158\n" \
"v_mul_f32_e32 v159, v54, v159\n" \
"v_cvt_pk_bf16_f32 v78, v156, v157\n" \
"v_cvt_pk_bf16_f32 v79, v158, v159\n" \
"v_mul_f32_e32 v175, v55, v175\n" \
"v_cvt_pk_bf16_f32 v84, v168, v169\n" \
"v_mfma_f32_16x16x32_bf16 a[208:211], a[216:219], v[76:79], a[208:211]\n" \
"v_mfma_f32_16x16x32_bf16 a[208:211], a[216:219], v[80:83], a[208:211]\n" \
"v_cvt_pk_bf16_f32 v85, v170, v171\n" \
"v_cvt_pk_bf16_f32 v86, v172, v173\n" \
"v_cvt_pk_bf16_f32 v87, v174, v175\n" \
"v_mul_f32_e32 v176, v55, v176\n" \
"v_mul_f32_e32 v177, v55, v177\n" \
"v_mfma_f32_16x16x32_bf16 a[208:211], a[216:219], v[84:87], a[208:211]\n" \
"buffer_load_dwordx4 a[100:103], v20, s[56:59], s97 offen offset:1024 sc1\n" \
"v_mul_f32_e32 v178, v55, v178\n" \
"v_mul_f32_e32 v179, v55, v179\n" \
"v_mul_f32_e32 v180, v55, v180\n" \
"v_mul_f32_e32 v181, v55, v181\n" \
"v_mul_f32_e32 v182, v55, v182\n" \
"v_mul_f32_e32 v183, v55, v183\n" \
"v_cvt_pk_bf16_f32 v96, v176, v177\n" \
"v_cvt_pk_bf16_f32 v97, v178, v179\n" \
"v_cvt_pk_bf16_f32 v98, v180, v181\n" \
"v_cvt_pk_bf16_f32 v99, v182, v183\n" \
"v_mfma_f32_16x16x32_bf16 v[188:191], a[60:63], v[120:123], v[188:191]\n" \
"v_mul_f32_e32 v184, v55, v184\n" \
"v_mfma_f32_16x16x32_bf16 a[208:211], a[216:219], v[96:99], a[208:211]\n" \
"v_mul_f32_e32 v185, v55, v185\n" \
"v_mul_f32_e32 v186, v55, v186\n" \
"v_mul_f32_e32 v187, v55, v187\n" \
"v_cvt_pk_bf16_f32 v100, v184, v185\n" \
"v_cvt_pk_bf16_f32 v101, v186, v187\n" \
"buffer_load_dwordx4 a[108:111], v56, s[56:59], s97 offen offset:1024 sc1\n" \
"v_mul_f32_e32 v188, v55, v188\n" \
"v_mul_f32_e32 v189, v55, v189\n" \
"v_mul_f32_e32 v190, v55, v190\n" \
"v_mul_f32_e32 v191, v55, v191\n" \
"v_cvt_pk_bf16_f32 v102, v188, v189\n" \
"v_cvt_pk_bf16_f32 v103, v190, v191\n" \
"v_cmp_lt_f32_e64 s[72:73], s21, v62\n" \
"buffer_load_dwordx4 a[116:119], v20, s[56:59], s98 offen offset:1024 sc1\n" \
"v_mfma_f32_16x16x32_bf16 a[208:211], a[216:219], v[100:103], a[208:211]\n" \
"buffer_load_dwordx4 a[124:127], v56, s[56:59], s98 offen offset:1024 sc1\n" \
"s_nop 6\n" \
"v_accvgpr_read_b32 v63, a208\n" \
"v_cmp_u_f32_e32 vcc, v63, v63\n" \
"s_nop 0\n" \
"s_or_b64 vcc, s[72:73], vcc\n" \
"s_cbranch_scc0 L_out_%=\n" \
"s_branch L_end_%=\n" \
"L_att_stub:\n" \
"s_mov_b32 s101, 0\n" \
"v_add_u32_e32 v32, 0x49000, v20\n" \
"v_readlane_b32 s96, v11, 35\n" \
"v_readlane_b32 s97, v11, 36\n" \
"buffer_load_dwordx4 a[220:223], v20, s[56:59], s60 offen offset:0 sc1\n" \
"buffer_load_dwordx4 a[236:239], v32, s[56:59], s60 offen offset:0 sc1\n" \
"buffer_load_dwordx4 a[224:227], v20, s[56:59], s60 offen offset:1024 sc1\n" \
"buffer_load_dwordx4 a[240:243], v32, s[56:59], s60 offen offset:1024 sc1\n" \
"buffer_load_dwordx4 a[228:231], v20, s[56:59], s60 offen offset:2048 sc1\n" \
"buffer_load_dwordx4 a[244:247], v32, s[56:59], s60 offen offset:2048 sc1\n" \
"buffer_load_dwordx4 a[232:235], v20, s[56:59], s60 offen offset:3072 sc1\n" \
"buffer_load_dwordx4 a[248:251], v32, s[56:59], s60 offen offset:3072 sc1\n" \
"buffer_load_dwordx4 a[96:99], v20, s[56:59], s96 offen offset:0 sc1\n" \
"buffer_load_dwordx4 a[104:107], v32, s[56:59], s96 offen offset:0 sc1\n" \
"buffer_load_dwordx4 a[112:115], v20, s[56:59], s97 offen offset:0 sc1\n" \
"buffer_load_dwordx4 a[120:123], v32, s[56:59], s97 offen offset:0 sc1\n" \
"buffer_load_dwordx4 a[100:103], v20, s[56:59], s96 offen offset:1024 sc1\n" \
"buffer_load_dwordx4 a[108:111], v32, s[56:59], s96 offen offset:1024 sc1\n" \
"buffer_load_dwordx4 a[116:119], v20, s[56:59], s97 offen offset:1024 sc1\n" \
"buffer_load_dwordx4 a[124:127], v32, s[56:59], s97 offen offset:1024 sc1\n" \
"buffer_load_dwordx4 v[64:67], v20, s[56:59], s61 offen offset:0 sc1\n" \
"buffer_load_dwordx4 v[68:71], v20, s[56:59], s61 offen offset:1024 sc1\n" \
"buffer_load_dwordx4 v[72:75], v20, s[56:59], s61 offen offset:2048 sc1\n" \
"buffer_load_dwordx4 v[76:79], v20, s[56:59], s61 offen offset:3072 sc1\n" \
"buffer_load_dwordx4 v[80:83], v32, s[56:59], s61 offen offset:0 sc1\n" \
"buffer_load_dwordx4 v[84:87], v32, s[56:59], s61 offen offset:1024 sc1\n" \
"buffer_load_dwordx4 v[96:99], v32, s[56:59], s61 offen offset:2048 sc1\n" \
"buffer_load_dwordx4 v[100:103], v32, s[56:59], s61 offen offset:3072 sc1\n" \
"v_not_b32_e32 v26, s62\n" \
"s_waitcnt vmcnt(0)\n" \
"s_branch L_out_%=\n" \
".fill (L_ab + 20480 - .) / 4, 4, 0xBF800000\n" \
".fill (L_ab + 20480 - .) / 4, 4, 0xBF800000\n" \
"L_ap5:\n" \
"s_setpc_b64 s[88:89]\n" \
"L_out_%=:\n" \
"v_bfe_u32 v38, v20, 2, 4\n" \
"v_readlane_b32 s64, v11, 2\n" \
"v_add_u32_e32 v32, 0x49000, v20\n" \
"v_add_u32_e32 v34, 0x20000, v10\n" \
"ds_read_b64_tr_b16 a[0:1], v34 offset:0\n" \
"ds_read_b64_tr_b16 a[2:3], v34 offset:4608\n" \
"ds_read_b64_tr_b16 a[4:5], v34 offset:32\n" \
"buffer_load_dword v26, v38, s[56:59], s64 offen sc1\n" \
"ds_read_b64_tr_b16 a[6:7], v34 offset:4640\n" \
"ds_read_b64_tr_b16 a[8:9], v34 offset:64\n" \
"ds_read_b64_tr_b16 a[10:11], v34 offset:4672\n" \
"ds_read_b64_tr_b16 a[12:13], v34 offset:96\n" \
"buffer_store_dwordx4 v[64:67], v20, s[56:59], s61 offen offset:0 sc1\n" \
"ds_read_b64_tr_b16 a[14:15], v34 offset:4704\n" \
"v_mov_b32_e32 v39, s62\n" \
"v_readlane_b32 s72, v11, 5\n" \
"v_readlane_b32 s73, v11, 4\n" \
"buffer_store_dwordx4 v[68:71], v20, s[56:59], s61 offen offset:1024 sc1\n" \
"ds_read_b64_tr_b16 a[16:17], v34 offset:128\n" \
"ds_read_b64_tr_b16 a[18:19], v34 offset:4736\n" \
"ds_read_b64_tr_b16 a[20:21], v34 offset:160\n" \
"ds_read_b64_tr_b16 a[22:23], v34 offset:4768\n" \
"buffer_store_dwordx4 v[72:75], v20, s[56:59], s61 offen offset:2048 sc1\n" \
"ds_read_b64_tr_b16 a[24:25], v34 offset:192\n" \
"ds_read_b64_tr_b16 a[26:27], v34 offset:4800\n" \
"ds_read_b64_tr_b16 a[28:29], v34 offset:224\n" \
"ds_read_b64_tr_b16 a[30:31], v34 offset:4832\n" \
"buffer_store_dwordx4 v[76:79], v20, s[56:59], s61 offen offset:3072 sc1\n" \
"ds_read_b64_tr_b16 a[192:193], v34 offset:256\n" \
"ds_read_b64_tr_b16 a[194:195], v34 offset:4864\n" \
"ds_read_b64_tr_b16 a[196:197], v34 offset:9216\n" \
"ds_read_b64_tr_b16 a[198:199], v34 offset:13824\n" \
"buffer_store_dwordx4 v[80:83], v32, s[56:59], s61 offen offset:0 sc1\n" \
"ds_read_b64_tr_b16 a[200:201], v34 offset:9248\n" \
"ds_read_b64_tr_b16 a[202:203], v34 offset:13856\n" \
"ds_read_b64_tr_b16 a[204:205], v34 offset:9280\n" \
"ds_read_b64_tr_b16 a[206:207], v34 offset:13888\n" \
"buffer_store_dwordx4 v[84:87], v32, s[56:59], s61 offen offset:1024 sc1\n" \
"ds_read_b64_tr_b16 a[208:209], v34 offset:9312\n" \
"s_waitcnt vmcnt(15) lgkmcnt(15)\n" \
"v_mfma_f32_16x16x32_bf16 v[112:115], a[0:3], a[220:223], 0\n" \
"buffer_store_dwordx4 v[96:99], v32, s[56:59], s61 offen offset:2048 sc1\n" \
"v_mfma_f32_16x16x32_bf16 v[116:119], a[0:3], a[236:239], 0\n" \
"ds_read_b64_tr_b16 a[210:211], v34 offset:13920\n" \
"ds_read_b64_tr_b16 a[212:213], v34 offset:9344\n" \
"buffer_store_dwordx4 v[100:103], v32, s[56:59], s61 offen offset:3072 sc1\n" \
"v_mfma_f32_16x16x32_bf16 v[184:187], a[0:3], v[64:67], 0\n" \
"ds_read_b64_tr_b16 a[214:215], v34 offset:13952\n" \
"ds_read_b64_tr_b16 a[216:217], v34 offset:9376\n" \
"v_mfma_f32_16x16x32_bf16 v[188:191], a[0:3], v[80:83], 0\n" \
"ds_read_b64_tr_b16 a[218:219], v34 offset:13984\n" \
"ds_read_b64_tr_b16 a[0:1], v34 offset:9408\n" \
"v_mfma_f32_16x16x32_bf16 v[120:123], a[4:7], a[220:223], 0\n" \
"ds_read_b64_tr_b16 a[2:3], v34 offset:14016\n" \
"v_readlane_b32 s66, v11, 21\n" \
"v_mfma_f32_16x16x32_bf16 v[124:127], a[4:7], a[236:239], 0\n" \
"v_readlane_b32 s80, v11, 11\n" \
"v_readlane_b32 s81, v11, 12\n" \
"v_mfma_f32_16x16x32_bf16 v[192:195], a[4:7], v[64:67], 0\n" \
"s_mov_b32 s82, 0x47fca0\n" \
"s_mov_b32 s83, 0x20000\n" \
"v_mfma_f32_16x16x32_bf16 v[196:199], a[4:7], v[80:83], 0\n" \
"ds_read_b64_tr_b16 a[4:5], v34 offset:9440\n" \
"ds_read_b64_tr_b16 a[6:7], v34 offset:14048\n" \
"v_mfma_f32_16x16x32_bf16 v[128:131], a[8:11], a[220:223], 0\n" \
"v_readlane_b32 s65, v11, 26\n" \
"s_add_u32 m0, s66, 0x0\n" \
"v_mfma_f32_16x16x32_bf16 v[132:135], a[8:11], a[236:239], 0\n" \
"v_readlane_b32 s67, v11, 29\n" \
"v_lshrrev_b32_e32 v44, 9, v20\n" \
"v_mfma_f32_16x16x32_bf16 v[200:203], a[8:11], v[64:67], 0\n" \
"buffer_load_dwordx4 v0, s[80:83], s65 offen offset:0 lds\n" \
"v_mfma_f32_16x16x32_bf16 v[204:207], a[8:11], v[80:83], 0\n" \
"ds_read_b64_tr_b16 a[8:9], v34 offset:9472\n" \
"ds_read_b64_tr_b16 a[10:11], v34 offset:14080\n" \
"v_mfma_f32_16x16x32_bf16 v[136:139], a[12:15], a[220:223], 0\n" \
"v_mul_u32_u24_e32 v44, 0xb40, v44\n" \
"v_add_u32_e32 v35, v44, v10\n" \
"v_mfma_f32_16x16x32_bf16 v[140:143], a[12:15], a[236:239], 0\n" \
"v_readlane_b32 s69, v11, 22\n" \
"v_readlane_b32 s68, v11, 27\n" \
"v_mfma_f32_16x16x32_bf16 v[208:211], a[12:15], v[64:67], 0\n" \
"v_readlane_b32 s94, v11, 23\n" \
"v_readlane_b32 s93, v11, 28\n" \
"v_mfma_f32_16x16x32_bf16 v[212:215], a[12:15], v[80:83], 0\n" \
"ds_read_b64_tr_b16 a[12:13], v34 offset:18432\n" \
"ds_read_b64_tr_b16 a[14:15], v34 offset:23040\n" \
"v_mfma_f32_16x16x32_bf16 v[144:147], a[16:19], a[220:223], 0\n" \
"buffer_load_dwordx4 v1, s[80:83], s65 offen offset:1024 lds\n" \
"v_mfma_f32_16x16x32_bf16 v[148:151], a[16:19], a[236:239], 0\n" \
"v_readlane_b32 s96, v11, 24\n" \
"v_readlane_b32 s84, v11, 9\n" \
"v_mfma_f32_16x16x32_bf16 v[216:219], a[16:19], v[64:67], 0\n" \
"v_readlane_b32 s85, v11, 10\n" \
"s_mov_b32 s86, 0xa1ca0\n" \
"v_mfma_f32_16x16x32_bf16 v[220:223], a[16:19], v[80:83], 0\n" \
"ds_read_b64_tr_b16 a[16:17], v34 offset:18464\n" \
"s_waitcnt lgkmcnt(7)\n" \
"v_mfma_f32_16x16x32_bf16 v[152:155], a[20:23], a[220:223], 0\n" \
"ds_read_b64_tr_b16 a[18:19], v34 offset:23072\n" \
"s_mov_b32 s87, 0x20000\n" \
"v_mfma_f32_16x16x32_bf16 v[156:159], a[20:23], a[236:239], 0\n" \
"buffer_load_dwordx4 v2, s[80:83], s65 offen offset:2048 lds\n" \
"v_mfma_f32_16x16x32_bf16 v[224:227], a[20:23], v[64:67], 0\n" \
"v_readlane_b32 s70, v11, 30\n" \
"v_add_u32_e32 v33, 0x10000, v10\n" \
"v_mfma_f32_16x16x32_bf16 v[228:231], a[20:23], v[80:83], 0\n" \
"ds_read_b64_tr_b16 a[20:21], v34 offset:18496\n" \
"ds_read_b64_tr_b16 a[22:23], v34 offset:23104\n" \
"v_mfma_f32_16x16x32_bf16 v[160:163], a[24:27], a[220:223], 0\n" \
"v_readlane_b32 s71, v11, 31\n" \
"v_readlane_b32 s92, v11, 33\n" \
"v_mfma_f32_16x16x32_bf16 v[164:167], a[24:27], a[236:239], 0\n" \
"v_readlane_b32 s95, v11, 34\n" \
"v_lshlrev_b32_e32 v41, 12, v20\n" \
"v_mfma_f32_16x16x32_bf16 v[232:235], a[24:27], v[64:67], 0\n" \
"buffer_load_dwordx4 v3, s[80:83], s65 offen offset:3072 lds\n" \
"v_mfma_f32_16x16x32_bf16 v[236:239], a[24:27], v[80:83], 0\n" \
"ds_read_b64_tr_b16 a[24:25], v34 offset:18528\n" \
"ds_read_b64_tr_b16 a[26:27], v34 offset:23136\n" \
"v_mfma_f32_16x16x32_bf16 v[168:171], a[28:31], a[220:223], 0\n" \
"s_add_u32 m0, s66, 0x1000\n" \
"v_readlane_b32 s97, v11, 37\n" \
"v_mfma_f32_16x16x32_bf16 v[172:175], a[28:31], a[236:239], 0\n" \
"v_readlane_b32 s63, v11, 40\n" \
"v_bfe_u32 v40, v20, 2, 7\n" \
"v_mfma_f32_16x16x32_bf16 v[240:243], a[28:31], v[64:67], 0\n" \
"s_add_u32 s74, s63, 0x3800\n" \
"v_readlane_b32 s100, v11, 7\n" \
"v_mfma_f32_16x16x32_bf16 v[244:247], a[28:31], v[80:83], 0\n" \
"ds_read_b64_tr_b16 a[28:29], v34 offset:18560\n" \
"ds_read_b64_tr_b16 a[30:31], v34 offset:23168\n" \
"v_mfma_f32_16x16x32_bf16 v[176:179], a[192:195], a[220:223], 0\n" \
"buffer_load_dwordx4 v4, s[80:83], s65 offen offset:0 lds\n" \
"v_mfma_f32_16x16x32_bf16 v[180:183], a[192:195], a[236:239], 0\n" \
"v_bfe_u32 v44, v20, 4, 4\n" \
"v_bfe_u32 v45, v20, 8, 2\n" \
"v_mfma_f32_16x16x32_bf16 v[248:251], a[192:195], v[64:67], 0\n" \
"v_mul_u32_u24_e32 v42, 0x480, v44\n" \
"v_lshl_add_u32 v42, v45, 3, v42\n" \
"v_mfma_f32_16x16x32_bf16 v[252:255], a[192:195], v[80:83], 0\n" \
"ds_read_b64_tr_b16 a[192:193], v34 offset:18592\n" \
"ds_read_b64_tr_b16 a[194:195], v34 offset:23200\n" \
"v_mfma_f32_16x16x32_bf16 v[112:115], a[196:199], a[224:227], v[112:115]\n" \
"v_lshrrev_b32_e32 v44, 1, v44\n" \
"v_lshlrev_b32_e32 v45, 1, v45\n" \
"v_mfma_f32_16x16x32_bf16 v[116:119], a[196:199], a[240:243], v[116:119]\n" \
"buffer_load_dwordx4 v5, s[80:83], s65 offen offset:1024 lds\n" \
"v_mfma_f32_16x16x32_bf16 v[184:187], a[196:199], v[68:71], v[184:187]\n" \
"v_sub_u32_e32 v44, v44, v45\n" \
"v_and_b32_e32 v45, 16, v20\n" \
"v_mfma_f32_16x16x32_bf16 v[188:191], a[196:199], v[84:87], v[188:191]\n" \
"ds_read_b64_tr_b16 a[196:197], v34 offset:18624\n" \
"ds_read_b64_tr_b16 a[198:199], v34 offset:23232\n" \
"v_mfma_f32_16x16x32_bf16 v[120:123], a[200:203], a[224:227], v[120:123]\n" \
"v_mov_b32_e32 v46, 0x3f80\n" \
"v_lshlrev_b32_e32 v45, v45, v46\n" \
"v_mfma_f32_16x16x32_bf16 v[124:127], a[200:203], a[240:243], v[124:127]\n" \
"v_mov_b32_e32 v52, 0\n" \
"v_mov_b32_e32 v53, 0\n" \
"v_mfma_f32_16x16x32_bf16 v[192:195], a[200:203], v[68:71], v[192:195]\n" \
"buffer_load_dwordx4 v6, s[80:83], s65 offen offset:2048 lds\n" \
"v_mfma_f32_16x16x32_bf16 v[196:199], a[200:203], v[84:87], v[196:199]\n" \
"ds_read_b64_tr_b16 a[200:201], v34 offset:18656\n" \
"ds_read_b64_tr_b16 a[202:203], v34 offset:23264\n" \
"v_mfma_f32_16x16x32_bf16 v[128:131], a[204:207], a[224:227], v[128:131]\n" \
"s_waitcnt vmcnt(15)\n" \
"v_cmp_ne_u32_e32 vcc, s62, v26\n" \
"v_mfma_f32_16x16x32_bf16 v[132:135], a[204:207], a[240:243], v[132:135]\n" \
"v_mov_b32_e32 v48, 0\n" \
"v_mov_b32_e32 v49, 0\n" \
"v_mfma_f32_16x16x32_bf16 v[200:203], a[204:207], v[68:71], v[200:203]\n" \
"v_add_u32_e32 v43, 0x9000, v42\n" \
"s_cbranch_vccnz L_sa_%=\n" \
"L_ra_%=:\n" \
"v_mfma_f32_16x16x32_bf16 v[204:207], a[204:207], v[84:87], v[204:207]\n" \
"ds_read_b64_tr_b16 a[204:205], v34 offset:18688\n" \
"ds_read_b64_tr_b16 a[206:207], v34 offset:23296\n" \
"v_mfma_f32_16x16x32_bf16 v[136:139], a[208:211], a[224:227], v[136:139]\n" \
"buffer_load_dwordx4 v7, s[80:83], s65 offen offset:3072 lds\n" \
"v_mfma_f32_16x16x32_bf16 v[140:143], a[208:211], a[240:243], v[140:143]\n" \
"s_add_u32 m0, s66, 0x2000\n" \
"v_readlane_b32 s66, v11, 32\n" \
"v_mfma_f32_16x16x32_bf16 v[208:211], a[208:211], v[68:71], v[208:211]\n" \
"v_readlane_b32 s64, v11, 38\n" \
"v_readlane_b32 s88, v11, 13\n" \
"v_mfma_f32_16x16x32_bf16 v[212:215], a[208:211], v[84:87], v[212:215]\n" \
"v_readlane_b32 s89, v11, 14\n" \
"s_mov_b32 s90, 0x240000\n" \
"v_mfma_f32_16x16x32_bf16 v[144:147], a[212:215], a[224:227], v[144:147]\n" \
"s_mov_b32 s91, 0x20000\n" \
"v_mov_b32_e32 v46, s62\n" \
"v_mfma_f32_16x16x32_bf16 v[148:151], a[212:215], a[240:243], v[148:151]\n" \
"buffer_load_dwordx4 v8, s[80:83], s65 offen offset:0 lds\n" \
"v_mfma_f32_16x16x32_bf16 v[216:219], a[212:215], v[68:71], v[216:219]\n" \
"s_add_u32 m0, s69, 0x0\n" \
"v_readlane_b32 s65, v11, 39\n" \
"v_mfma_f32_16x16x32_bf16 v[220:223], a[212:215], v[84:87], v[220:223]\n" \
"v_mfma_f32_16x16x32_bf16 v[152:155], a[216:219], a[224:227], v[152:155]\n" \
"v_mfma_f32_16x16x32_bf16 v[156:159], a[216:219], a[240:243], v[156:159]\n" \
"v_mfma_f32_16x16x32_bf16 v[224:227], a[216:219], v[68:71], v[224:227]\n" \
"buffer_load_dwordx4 a[64:67], v20, s[56:59], s67 offen offset:0 sc1\n" \
"v_mfma_f32_16x16x32_bf16 v[228:231], a[216:219], v[84:87], v[228:231]\n" \
"v_mfma_f32_16x16x32_bf16 v[160:163], a[0:3], a[224:227], v[160:163]\n" \
"v_mfma_f32_16x16x32_bf16 v[164:167], a[0:3], a[240:243], v[164:167]\n" \
"v_mfma_f32_16x16x32_bf16 v[232:235], a[0:3], v[68:71], v[232:235]\n" \
"v_mfma_f32_16x16x32_bf16 v[236:239], a[0:3], v[84:87], v[236:239]\n" \
"s_waitcnt lgkmcnt(0)\n" \
"v_mfma_f32_16x16x32_bf16 v[168:171], a[4:7], a[224:227], v[168:171]\n" \
"buffer_load_dwordx4 a[80:83], v32, s[56:59], s67 offen offset:0 sc1\n" \
"v_mfma_f32_16x16x32_bf16 v[172:175], a[4:7], a[240:243], v[172:175]\n" \
"v_mfma_f32_16x16x32_bf16 v[240:243], a[4:7], v[68:71], v[240:243]\n" \
"v_mfma_f32_16x16x32_bf16 v[244:247], a[4:7], v[84:87], v[244:247]\n" \
"v_mfma_f32_16x16x32_bf16 v[176:179], a[8:11], a[224:227], v[176:179]\n" \
"v_mfma_f32_16x16x32_bf16 v[180:183], a[8:11], a[240:243], v[180:183]\n" \
"buffer_load_dwordx4 a[68:71], v20, s[56:59], s67 offen offset:1024 sc1\n" \
"v_mfma_f32_16x16x32_bf16 v[248:251], a[8:11], v[68:71], v[248:251]\n" \
"v_mfma_f32_16x16x32_bf16 v[252:255], a[8:11], v[84:87], v[252:255]\n" \
"v_mfma_f32_16x16x32_bf16 v[112:115], a[12:15], a[228:231], v[112:115]\n" \
"v_mfma_f32_16x16x32_bf16 v[116:119], a[12:15], a[244:247], v[116:119]\n" \
"v_mfma_f32_16x16x32_bf16 v[184:187], a[12:15], v[72:75], v[184:187]\n" \
"buffer_load_dwordx4 a[84:87], v32, s[56:59], s67 offen offset:1024 sc1\n" \
"v_mfma_f32_16x16x32_bf16 v[188:191], a[12:15], v[96:99], v[188:191]\n" \
"v_mfma_f32_16x16x32_bf16 v[120:123], a[16:19], a[228:231], v[120:123]\n" \
"v_mfma_f32_16x16x32_bf16 v[124:127], a[16:19], a[244:247], v[124:127]\n" \
"v_mfma_f32_16x16x32_bf16 v[192:195], a[16:19], v[72:75], v[192:195]\n" \
"v_mfma_f32_16x16x32_bf16 v[196:199], a[16:19], v[96:99], v[196:199]\n" \
"buffer_load_dwordx4 a[72:75], v20, s[56:59], s67 offen offset:2048 sc1\n" \
"v_mfma_f32_16x16x32_bf16 v[128:131], a[20:23], a[228:231], v[128:131]\n" \
"v_mfma_f32_16x16x32_bf16 v[132:135], a[20:23], a[244:247], v[132:135]\n" \
"v_mfma_f32_16x16x32_bf16 v[200:203], a[20:23], v[72:75], v[200:203]\n" \
"v_mfma_f32_16x16x32_bf16 v[204:207], a[20:23], v[96:99], v[204:207]\n" \
"s_barrier\n" \
"v_mfma_f32_16x16x32_bf16 v[136:139], a[24:27], a[228:231], v[136:139]\n" \
"ds_read_b64_tr_b16 a[208:209], v10 offset:52224\n" \
"ds_read_b64_tr_b16 a[210:211], v10 offset:56832\n" \
"v_mfma_f32_16x16x32_bf16 v[140:143], a[24:27], a[244:247], v[140:143]\n" \
"ds_read_b64_tr_b16 a[212:213], v10 offset:52256\n" \
"ds_read_b64_tr_b16 a[214:215], v10 offset:56864\n" \
"v_mfma_f32_16x16x32_bf16 v[208:211], a[24:27], v[72:75], v[208:211]\n" \
"ds_read_b64_tr_b16 a[216:217], v10 offset:52288\n" \
"ds_read_b64_tr_b16 a[218:219], v10 offset:56896\n" \
"v_mfma_f32_16x16x32_bf16 v[212:215], a[24:27], v[96:99], v[212:215]\n" \
"ds_read_b64_tr_b16 a[0:1], v10 offset:52320\n" \
"ds_read_b64_tr_b16 a[2:3], v10 offset:56928\n" \
"v_mfma_f32_16x16x32_bf16 v[144:147], a[28:31], a[228:231], v[144:147]\n" \
"ds_read_b64_tr_b16 a[4:5], v10 offset:52352\n" \
"ds_read_b64_tr_b16 a[6:7], v10 offset:56960\n" \
"v_mfma_f32_16x16x32_bf16 v[148:151], a[28:31], a[244:247], v[148:151]\n" \
"ds_read_b64_tr_b16 a[8:9], v10 offset:52384\n" \
"ds_read_b64_tr_b16 a[10:11], v10 offset:56992\n" \
"v_mfma_f32_16x16x32_bf16 v[216:219], a[28:31], v[72:75], v[216:219]\n" \
"ds_read_b64_tr_b16 a[12:13], v10 offset:52416\n" \
"ds_read_b64_tr_b16 a[14:15], v10 offset:57024\n" \
"v_mfma_f32_16x16x32_bf16 v[220:223], a[28:31], v[96:99], v[220:223]\n" \
"ds_read_b64_tr_b16 a[16:17], v10 offset:52448\n" \
"ds_read_b64_tr_b16 a[18:19], v10 offset:57056\n" \
"v_mfma_f32_16x16x32_bf16 v[152:155], a[192:195], a[228:231], v[152:155]\n" \
"ds_read_b64_tr_b16 a[20:21], v10 offset:52480\n" \
"ds_read_b64_tr_b16 a[22:23], v10 offset:57088\n" \
"v_mfma_f32_16x16x32_bf16 v[156:159], a[192:195], a[244:247], v[156:159]\n" \
"ds_read_b64_tr_b16 a[24:25], v35 offset:24576\n" \
"ds_read_b64_tr_b16 a[26:27], v35 offset:26880\n" \
"v_mfma_f32_16x16x32_bf16 v[224:227], a[192:195], v[72:75], v[224:227]\n" \
"ds_read_b64_tr_b16 a[28:29], v35 offset:24608\n" \
"ds_read_b64_tr_b16 a[30:31], v35 offset:26912\n" \
"v_mfma_f32_16x16x32_bf16 v[228:231], a[192:195], v[96:99], v[228:231]\n" \
"ds_read_b64_tr_b16 a[192:193], v35 offset:24640\n" \
"ds_read_b64_tr_b16 a[194:195], v35 offset:26944\n" \
"v_mfma_f32_16x16x32_bf16 v[160:163], a[196:199], a[228:231], v[160:163]\n" \
"buffer_load_dwordx4 a[88:91], v32, s[56:59], s67 offen offset:2048 sc1\n" \
"v_mfma_f32_16x16x32_bf16 v[164:167], a[196:199], a[244:247], v[164:167]\n" \
"v_mfma_f32_16x16x32_bf16 v[232:235], a[196:199], v[72:75], v[232:235]\n" \
"v_mfma_f32_16x16x32_bf16 v[236:239], a[196:199], v[96:99], v[236:239]\n" \
"ds_read_b64_tr_b16 a[196:197], v35 offset:24672\n" \
"ds_read_b64_tr_b16 a[198:199], v35 offset:26976\n" \
"v_mfma_f32_16x16x32_bf16 v[168:171], a[200:203], a[228:231], v[168:171]\n" \
"v_mfma_f32_16x16x32_bf16 v[172:175], a[200:203], a[244:247], v[172:175]\n" \
"buffer_load_dwordx4 v0, s[80:83], s68 offen offset:0 lds\n" \
"v_mfma_f32_16x16x32_bf16 v[240:243], a[200:203], v[72:75], v[240:243]\n" \
"v_mfma_f32_16x16x32_bf16 v[244:247], a[200:203], v[96:99], v[244:247]\n" \
"ds_read_b64_tr_b16 a[200:201], v35 offset:24704\n" \
"ds_read_b64_tr_b16 a[202:203], v35 offset:27008\n" \
"v_mfma_f32_16x16x32_bf16 v[176:179], a[204:207], a[228:231], v[176:179]\n" \
"v_mfma_f32_16x16x32_bf16 v[180:183], a[204:207], a[244:247], v[180:183]\n" \
"v_mfma_f32_16x16x32_bf16 v[248:251], a[204:207], v[72:75], v[248:251]\n" \
"buffer_load_dwordx4 v1, s[80:83], s68 offen offset:1024 lds\n" \
"v_mfma_f32_16x16x32_bf16 v[252:255], a[204:207], v[96:99], v[252:255]\n" \
"ds_read_b64_tr_b16 a[204:205], v35 offset:24736\n" \
"s_waitcnt lgkmcnt(9)\n" \
"v_mfma_f32_16x16x32_bf16 v[112:115], a[208:211], a[232:235], v[112:115]\n" \
"ds_read_b64_tr_b16 a[206:207], v35 offset:27040\n" \
"v_mfma_f32_16x16x32_bf16 v[116:119], a[208:211], a[248:251], v[116:119]\n" \
"v_mfma_f32_16x16x32_bf16 v[184:187], a[208:211], v[76:79], v[184:187]\n" \
"v_mfma_f32_16x16x32_bf16 v[188:191], a[208:211], v[100:103], v[188:191]\n" \
"ds_read_b64_tr_b16 a[208:209], v35 offset:24768\n" \
"ds_read_b64_tr_b16 a[210:211], v35 offset:27072\n" \
"v_mfma_f32_16x16x32_bf16 v[120:123], a[212:215], a[232:235], v[120:123]\n" \
"buffer_load_dwordx4 v2, s[80:83], s68 offen offset:2048 lds\n" \
"v_mfma_f32_16x16x32_bf16 v[124:127], a[212:215], a[248:251], v[124:127]\n" \
"v_mfma_f32_16x16x32_bf16 v[192:195], a[212:215], v[76:79], v[192:195]\n" \
"s_waitcnt vmcnt(18)\n" \
"s_mov_b64 exec, 1\n" \
"buffer_store_dword v39, off, s[56:59], s72 offset:0 sc1\n" \
"s_mov_b64 exec, -1\n" \
"v_mfma_f32_16x16x32_bf16 v[196:199], a[212:215], v[100:103], v[196:199]\n" \
"ds_read_b64_tr_b16 a[212:213], v35 offset:24800\n" \
"ds_read_b64_tr_b16 a[214:215], v35 offset:27104\n" \
"v_mfma_f32_16x16x32_bf16 v[128:131], a[216:219], a[232:235], v[128:131]\n" \
"v_readlane_b32 s72, v11, 50\n" \
"v_mfma_f32_16x16x32_bf16 v[132:135], a[216:219], a[248:251], v[132:135]\n" \
"v_mfma_f32_16x16x32_bf16 v[200:203], a[216:219], v[76:79], v[200:203]\n" \
"buffer_load_dwordx4 v3, s[80:83], s68 offen offset:3072 lds\n" \
"v_mfma_f32_16x16x32_bf16 v[204:207], a[216:219], v[100:103], v[204:207]\n" \
"ds_read_b64_tr_b16 a[216:217], v35 offset:24832\n" \
"ds_read_b64_tr_b16 a[218:219], v35 offset:27136\n" \
"v_mfma_f32_16x16x32_bf16 v[136:139], a[0:3], a[232:235], v[136:139]\n" \
"s_add_u32 m0, s69, 0x1000\n" \
"v_mfma_f32_16x16x32_bf16 v[140:143], a[0:3], a[248:251], v[140:143]\n" \
"v_mfma_f32_16x16x32_bf16 v[208:211], a[0:3], v[76:79], v[208:211]\n" \
"v_mfma_f32_16x16x32_bf16 v[212:215], a[0:3], v[100:103], v[212:215]\n" \
"ds_read_b64_tr_b16 a[0:1], v35 offset:34944\n" \
"ds_read_b64_tr_b16 a[2:3], v35 offset:37248\n" \
"v_mfma_f32_16x16x32_bf16 v[144:147], a[4:7], a[232:235], v[144:147]\n" \
"buffer_load_dwordx4 v4, s[80:83], s68 offen offset:0 lds\n" \
"v_mfma_f32_16x16x32_bf16 v[148:151], a[4:7], a[248:251], v[148:151]\n" \
"v_mfma_f32_16x16x32_bf16 v[216:219], a[4:7], v[76:79], v[216:219]\n" \
"v_mfma_f32_16x16x32_bf16 v[220:223], a[4:7], v[100:103], v[220:223]\n" \
"ds_read_b64_tr_b16 a[4:5], v35 offset:34976\n" \
"ds_read_b64_tr_b16 a[6:7], v35 offset:37280\n" \
"v_mfma_f32_16x16x32_bf16 v[152:155], a[8:11], a[232:235], v[152:155]\n" \
"v_mfma_f32_16x16x32_bf16 v[156:159], a[8:11], a[248:251], v[156:159]\n" \
"buffer_load_dwordx4 v5, s[80:83], s68 offen offset:1024 lds\n" \
"v_mfma_f32_16x16x32_bf16 v[224:227], a[8:11], v[76:79], v[224:227]\n" \
"v_mfma_f32_16x16x32_bf16 v[228:231], a[8:11], v[100:103], v[228:231]\n" \
"ds_read_b64_tr_b16 a[8:9], v35 offset:35008\n" \
"ds_read_b64_tr_b16 a[10:11], v35 offset:37312\n" \
"v_mfma_f32_16x16x32_bf16 v[160:163], a[12:15], a[232:235], v[160:163]\n" \
"v_mfma_f32_16x16x32_bf16 v[164:167], a[12:15], a[248:251], v[164:167]\n" \
"v_mfma_f32_16x16x32_bf16 v[232:235], a[12:15], v[76:79], v[232:235]\n" \
"buffer_load_dwordx4 v6, s[80:83], s68 offen offset:2048 lds\n" \
"v_mfma_f32_16x16x32_bf16 v[236:239], a[12:15], v[100:103], v[236:239]\n" \
"ds_read_b64_tr_b16 a[12:13], v35 offset:35040\n" \
"ds_read_b64_tr_b16 a[14:15], v35 offset:37344\n" \
"v_mfma_f32_16x16x32_bf16 v[168:171], a[16:19], a[232:235], v[168:171]\n" \
"v_mfma_f32_16x16x32_bf16 v[172:175], a[16:19], a[248:251], v[172:175]\n" \
"v_mfma_f32_16x16x32_bf16 v[240:243], a[16:19], v[76:79], v[240:243]\n" \
"v_mfma_f32_16x16x32_bf16 v[244:247], a[16:19], v[100:103], v[244:247]\n" \
"buffer_load_dwordx4 v7, s[80:83], s68 offen offset:3072 lds\n" \
"v_mfma_f32_16x16x32_bf16 v[176:179], a[20:23], a[232:235], v[176:179]\n" \
"s_add_u32 m0, s69, 0x2000\n" \
"ds_read_b64_tr_b16 a[16:17], v35 offset:35072\n" \
"v_mfma_f32_16x16x32_bf16 v[180:183], a[20:23], a[248:251], v[180:183]\n" \
"ds_read_b64_tr_b16 a[18:19], v35 offset:37376\n" \
"v_readlane_b32 s69, v11, 47\n" \
"v_mfma_f32_16x16x32_bf16 v[248:251], a[20:23], v[76:79], v[248:251]\n" \
"v_mfma_f32_16x16x32_bf16 v[252:255], a[20:23], v[100:103], v[252:255]\n" \
"ds_read_b64_tr_b16 a[20:21], v35 offset:35104\n" \
"ds_read_b64_tr_b16 a[22:23], v35 offset:37408\n" \
"v_mfma_f32_16x16x32_bf16 v[112:115], a[24:27], a[96:99], v[112:115]\n" \
"buffer_load_dwordx4 v8, s[80:83], s68 offen offset:0 lds\n" \
"v_mfma_f32_16x16x32_bf16 v[116:119], a[24:27], a[104:107], v[116:119]\n" \
"s_add_u32 m0, s94, 0x0\n" \
"v_readlane_b32 s68, v11, 46\n" \
"v_mfma_f32_16x16x32_bf16 v[184:187], a[24:27], a[112:115], v[184:187]\n" \
"v_mfma_f32_16x16x32_bf16 v[188:191], a[24:27], a[120:123], v[188:191]\n" \
"ds_read_b64_tr_b16 a[24:25], v35 offset:35136\n" \
"s_waitcnt lgkmcnt(7)\n" \
"v_mfma_f32_16x16x32_bf16 v[120:123], a[28:31], a[96:99], v[120:123]\n" \
"ds_read_b64_tr_b16 a[26:27], v35 offset:37440\n" \
"buffer_load_dword v47, v38, s[56:59], s73 offen sc1\n" \
"v_mfma_f32_16x16x32_bf16 v[124:127], a[28:31], a[104:107], v[124:127]\n" \
"v_mfma_f32_16x16x32_bf16 v[192:195], a[28:31], a[112:115], v[192:195]\n" \
"v_mfma_f32_16x16x32_bf16 v[196:199], a[28:31], a[120:123], v[196:199]\n" \
"ds_read_b64_tr_b16 a[28:29], v35 offset:35168\n" \
"ds_read_b64_tr_b16 a[30:31], v35 offset:37472\n" \
"v_mfma_f32_16x16x32_bf16 v[128:131], a[192:195], a[96:99], v[128:131]\n" \
"v_mfma_f32_16x16x32_bf16 v[132:135], a[192:195], a[104:107], v[132:135]\n" \
"buffer_load_dwordx4 v0, s[80:83], s93 offen offset:0 lds\n" \
"v_mfma_f32_16x16x32_bf16 v[200:203], a[192:195], a[112:115], v[200:203]\n" \
"v_mfma_f32_16x16x32_bf16 v[204:207], a[192:195], a[120:123], v[204:207]\n" \
"ds_read_b64_tr_b16 a[192:193], v35 offset:35200\n" \
"ds_read_b64_tr_b16 a[194:195], v35 offset:37504\n" \
"v_mfma_f32_16x16x32_bf16 v[136:139], a[196:199], a[96:99], v[136:139]\n" \
"v_mfma_f32_16x16x32_bf16 v[140:143], a[196:199], a[104:107], v[140:143]\n" \
"v_mfma_f32_16x16x32_bf16 v[208:211], a[196:199], a[112:115], v[208:211]\n" \
"buffer_load_dwordx4 v1, s[80:83], s93 offen offset:1024 lds\n" \
"v_mfma_f32_16x16x32_bf16 v[212:215], a[196:199], a[120:123], v[212:215]\n" \
"v_mfma_f32_16x16x32_bf16 v[144:147], a[200:203], a[96:99], v[144:147]\n" \
"v_mfma_f32_16x16x32_bf16 v[148:151], a[200:203], a[104:107], v[148:151]\n" \
"v_mfma_f32_16x16x32_bf16 v[216:219], a[200:203], a[112:115], v[216:219]\n" \
"v_mfma_f32_16x16x32_bf16 v[220:223], a[200:203], a[120:123], v[220:223]\n" \
"buffer_load_dwordx4 v2, s[80:83], s93 offen offset:2048 lds\n" \
"v_mfma_f32_16x16x32_bf16 v[152:155], a[204:207], a[96:99], v[152:155]\n" \
"v_mfma_f32_16x16x32_bf16 v[156:159], a[204:207], a[104:107], v[156:159]\n" \
"v_mfma_f32_16x16x32_bf16 v[224:227], a[204:207], a[112:115], v[224:227]\n" \
"v_mfma_f32_16x16x32_bf16 v[228:231], a[204:207], a[120:123], v[228:231]\n" \
"v_mfma_f32_16x16x32_bf16 v[160:163], a[208:211], a[96:99], v[160:163]\n" \
"buffer_load_dwordx4 v3, s[80:83], s93 offen offset:3072 lds\n" \
"v_mfma_f32_16x16x32_bf16 v[164:167], a[208:211], a[104:107], v[164:167]\n" \
"s_add_u32 m0, s94, 0x1000\n" \
"v_mfma_f32_16x16x32_bf16 v[232:235], a[208:211], a[112:115], v[232:235]\n" \
"v_mfma_f32_16x16x32_bf16 v[236:239], a[208:211], a[120:123], v[236:239]\n" \
"v_mfma_f32_16x16x32_bf16 v[168:171], a[212:215], a[96:99], v[168:171]\n" \
"v_mfma_f32_16x16x32_bf16 v[172:175], a[212:215], a[104:107], v[172:175]\n" \
"buffer_load_dwordx4 v4, s[80:83], s93 offen offset:0 lds\n" \
"v_mfma_f32_16x16x32_bf16 v[240:243], a[212:215], a[112:115], v[240:243]\n" \
"v_mfma_f32_16x16x32_bf16 v[244:247], a[212:215], a[120:123], v[244:247]\n" \
"v_mfma_f32_16x16x32_bf16 v[176:179], a[216:219], a[96:99], v[176:179]\n" \
"v_mfma_f32_16x16x32_bf16 v[180:183], a[216:219], a[104:107], v[180:183]\n" \
"v_mfma_f32_16x16x32_bf16 v[248:251], a[216:219], a[112:115], v[248:251]\n" \
"buffer_load_dwordx4 v5, s[80:83], s93 offen offset:1024 lds\n" \
"v_mfma_f32_16x16x32_bf16 v[252:255], a[216:219], a[120:123], v[252:255]\n" \
"v_mfma_f32_16x16x32_bf16 v[112:115], a[0:3], a[100:103], v[112:115]\n" \
"v_mfma_f32_16x16x32_bf16 v[116:119], a[0:3], a[108:111], v[116:119]\n" \
"v_mfma_f32_16x16x32_bf16 v[184:187], a[0:3], a[116:119], v[184:187]\n" \
"v_mfma_f32_16x16x32_bf16 v[188:191], a[0:3], a[124:127], v[188:191]\n" \
"buffer_load_dwordx4 v6, s[80:83], s93 offen offset:2048 lds\n" \
"v_mfma_f32_16x16x32_bf16 v[120:123], a[4:7], a[100:103], v[120:123]\n" \
"v_mfma_f32_16x16x32_bf16 v[124:127], a[4:7], a[108:111], v[124:127]\n" \
"v_mfma_f32_16x16x32_bf16 v[192:195], a[4:7], a[116:119], v[192:195]\n" \
"v_mfma_f32_16x16x32_bf16 v[196:199], a[4:7], a[124:127], v[196:199]\n" \
"v_mfma_f32_16x16x32_bf16 v[128:131], a[8:11], a[100:103], v[128:131]\n" \
"buffer_load_dwordx4 v7, s[80:83], s93 offen offset:3072 lds\n" \
"s_add_u32 m0, s94, 0x2000\n" \
"v_mfma_f32_16x16x32_bf16 v[132:135], a[8:11], a[108:111], v[132:135]\n" \
"v_readlane_b32 s94, v11, 45\n" \
"v_mfma_f32_16x16x32_bf16 v[200:203], a[8:11], a[116:119], v[200:203]\n" \
"v_mfma_f32_16x16x32_bf16 v[204:207], a[8:11], a[124:127], v[204:207]\n" \
"s_waitcnt lgkmcnt(0)\n" \
"v_mfma_f32_16x16x32_bf16 v[136:139], a[12:15], a[100:103], v[136:139]\n" \
"buffer_load_dwordx4 v8, s[80:83], s93 offen offset:0 lds\n" \
"s_mov_b32 m0, s96\n" \
"v_mfma_f32_16x16x32_bf16 v[140:143], a[12:15], a[108:111], v[140:143]\n" \
"v_readlane_b32 s96, v11, 47\n" \
"v_readlane_b32 s93, v11, 6\n" \
"v_mfma_f32_16x16x32_bf16 v[208:211], a[12:15], a[116:119], v[208:211]\n" \
"v_mfma_f32_16x16x32_bf16 v[212:215], a[12:15], a[124:127], v[212:215]\n" \
"s_waitcnt vmcnt(23)\n" \
"s_barrier\n" \
"buffer_load_dwordx4 v9, s[84:87], 0 offen lds\n" \
"v_mfma_f32_16x16x32_bf16 v[144:147], a[16:19], a[100:103], v[144:147]\n" \
"ds_read_b64_tr_b16 a[196:197], v33 offset:0\n" \
"ds_read_b64_tr_b16 a[198:199], v33 offset:4608\n" \
"v_mfma_f32_16x16x32_bf16 v[148:151], a[16:19], a[108:111], v[148:151]\n" \
"ds_read_b64_tr_b16 a[200:201], v33 offset:32\n" \
"ds_read_b64_tr_b16 a[202:203], v33 offset:4640\n" \
"v_mfma_f32_16x16x32_bf16 v[216:219], a[16:19], a[116:119], v[216:219]\n" \
"ds_read_b64_tr_b16 a[204:205], v33 offset:64\n" \
"ds_read_b64_tr_b16 a[206:207], v33 offset:4672\n" \
"v_mfma_f32_16x16x32_bf16 v[220:223], a[16:19], a[124:127], v[220:223]\n" \
"ds_read_b64_tr_b16 a[208:209], v33 offset:96\n" \
"ds_read_b64_tr_b16 a[210:211], v33 offset:4704\n" \
"buffer_load_dwordx4 a[76:79], v20, s[56:59], s67 offen offset:3072 sc1\n" \
"v_mfma_f32_16x16x32_bf16 v[152:155], a[20:23], a[100:103], v[152:155]\n" \
"ds_read_b64_tr_b16 a[212:213], v33 offset:128\n" \
"ds_read_b64_tr_b16 a[214:215], v33 offset:4736\n" \
"v_mfma_f32_16x16x32_bf16 v[156:159], a[20:23], a[108:111], v[156:159]\n" \
"ds_read_b64_tr_b16 a[216:217], v33 offset:160\n" \
"ds_read_b64_tr_b16 a[218:219], v33 offset:4768\n" \
"v_mfma_f32_16x16x32_bf16 v[224:227], a[20:23], a[116:119], v[224:227]\n" \
"ds_read_b64_tr_b16 a[0:1], v33 offset:192\n" \
"ds_read_b64_tr_b16 a[2:3], v33 offset:4800\n" \
"v_mfma_f32_16x16x32_bf16 v[228:231], a[20:23], a[124:127], v[228:231]\n" \
"ds_read_b64_tr_b16 a[4:5], v33 offset:224\n" \
"ds_read_b64_tr_b16 a[6:7], v33 offset:4832\n" \
"buffer_load_dwordx4 a[92:95], v32, s[56:59], s67 offen offset:3072 sc1\n" \
"v_mfma_f32_16x16x32_bf16 v[160:163], a[24:27], a[100:103], v[160:163]\n" \
"v_readlane_b32 s67, v11, 45\n" \
"ds_read_b64_tr_b16 a[8:9], v33 offset:256\n" \
"v_mfma_f32_16x16x32_bf16 v[164:167], a[24:27], a[108:111], v[164:167]\n" \
"ds_read_b64_tr_b16 a[10:11], v33 offset:4864\n" \
"ds_read_b64_tr_b16 a[12:13], v33 offset:9216\n" \
"v_mfma_f32_16x16x32_bf16 v[232:235], a[24:27], a[116:119], v[232:235]\n" \
"ds_read_b64_tr_b16 a[14:15], v33 offset:13824\n" \
"ds_read_b64_tr_b16 a[16:17], v33 offset:9248\n" \
"v_mfma_f32_16x16x32_bf16 v[236:239], a[24:27], a[124:127], v[236:239]\n" \
"ds_read_b64_tr_b16 a[18:19], v33 offset:13856\n" \
"ds_read_b64_tr_b16 a[20:21], v33 offset:9280\n" \
"buffer_load_dwordx4 a[128:131], v20, s[56:59], s70 offen offset:0 sc1\n" \
"v_mfma_f32_16x16x32_bf16 v[168:171], a[28:31], a[100:103], v[168:171]\n" \
"ds_read_b64_tr_b16 a[22:23], v33 offset:13888\n" \
"ds_read_b64_tr_b16 a[24:25], v33 offset:9312\n" \
"v_mfma_f32_16x16x32_bf16 v[172:175], a[28:31], a[108:111], v[172:175]\n" \
"ds_read_b64_tr_b16 a[26:27], v33 offset:13920\n" \
"s_waitcnt vmcnt(13)\n" \
"v_cmp_ne_u32_e32 vcc, s62, v47\n" \
"v_mfma_f32_16x16x32_bf16 v[240:243], a[28:31], a[116:119], v[240:243]\n" \
"v_mfma_f32_16x16x32_bf16 v[244:247], a[28:31], a[124:127], v[244:247]\n" \
"buffer_load_dwordx4 a[144:147], v32, s[56:59], s70 offen offset:0 sc1\n" \
"v_mfma_f32_16x16x32_bf16 v[176:179], a[192:195], a[100:103], v[176:179]\n" \
"ds_read_b64_tr_b16 a[28:29], v33 offset:9344\n" \
"ds_read_b64_tr_b16 a[30:31], v33 offset:13952\n" \
"v_mfma_f32_16x16x32_bf16 v[180:183], a[192:195], a[108:111], v[180:183]\n" \
"s_cbranch_vccnz L_sb_%=\n" \
"L_rb_%=:\n" \
"v_readlane_b32 s73, v11, 51\n" \
"v_mfma_f32_16x16x32_bf16 v[248:251], a[192:195], a[116:119], v[248:251]\n" \
"v_cmp_eq_u32_e32 vcc, 0, v44\n" \
"v_mfma_f32_16x16x32_bf16 v[252:255], a[192:195], a[124:127], v[252:255]\n" \
"ds_read_b64_tr_b16 a[192:193], v33 offset:9376\n" \
"buffer_load_dwordx4 a[132:135], v20, s[56:59], s70 offen offset:1024 sc1\n" \
"s_waitcnt lgkmcnt(14)\n" \
"v_mfma_f32_16x16x32_bf16 v[112:115], a[196:199], a[64:67], v[112:115]\n" \
"ds_read_b64_tr_b16 a[194:195], v33 offset:13984\n" \
"v_cndmask_b32_e32 v50, 0, v45, vcc\n" \
"v_mfma_f32_16x16x32_bf16 v[116:119], a[196:199], a[80:83], v[116:119]\n" \
"ds_read_b64_tr_b16 a[196:197], v33 offset:9408\n" \
"ds_read_b64_tr_b16 a[198:199], v33 offset:14016\n" \
"v_mfma_f32_16x16x32_bf16 v[120:123], a[200:203], a[64:67], v[120:123]\n" \
"v_cmp_eq_u32_e32 vcc, 1, v44\n" \
"v_and_b32_e32 v44, 0x3c0, v20\n" \
"v_mfma_f32_16x16x32_bf16 v[124:127], a[200:203], a[80:83], v[124:127]\n" \
"buffer_load_dwordx4 a[148:151], v32, s[56:59], s70 offen offset:1024 sc1\n" \
"v_mfma_f32_16x16x32_bf16 v[128:131], a[204:207], a[64:67], v[128:131]\n" \
"ds_read_b64_tr_b16 a[200:201], v33 offset:9440\n" \
"ds_read_b64_tr_b16 a[202:203], v33 offset:14048\n" \
"v_mfma_f32_16x16x32_bf16 v[132:135], a[204:207], a[80:83], v[132:135]\n" \
"ds_read_b64_tr_b16 a[204:205], v33 offset:9472\n" \
"ds_read_b64_tr_b16 a[206:207], v33 offset:14080\n" \
"v_mfma_f32_16x16x32_bf16 v[136:139], a[208:211], a[64:67], v[136:139]\n" \
"v_cndmask_b32_e32 v51, 0, v45, vcc\n" \
"v_bfe_u32 v45, v20, 1, 5\n" \
"v_mfma_f32_16x16x32_bf16 v[140:143], a[208:211], a[80:83], v[140:143]\n" \
"ds_read_b64_tr_b16 a[208:209], v33 offset:18432\n" \
"ds_read_b64_tr_b16 a[210:211], v33 offset:23040\n" \
"buffer_load_dwordx4 a[136:139], v20, s[56:59], s70 offen offset:2048 sc1\n" \
"v_mfma_f32_16x16x32_bf16 v[144:147], a[212:215], a[64:67], v[144:147]\n" \
"v_and_b32_e32 v45, 24, v45\n" \
"v_add_u32_e32 v45, 0xf000, v45\n" \
"v_mfma_f32_16x16x32_bf16 v[148:151], a[212:215], a[80:83], v[148:151]\n" \
"ds_read_b64_tr_b16 a[212:213], v33 offset:18464\n" \
"ds_read_b64_tr_b16 a[214:215], v33 offset:23072\n" \
"v_mfma_f32_16x16x32_bf16 v[152:155], a[216:219], a[64:67], v[152:155]\n" \
"v_add_u32_e32 v36, v44, v45\n" \
".fill (L_ab + 24576 - .) / 4, 4, 0xBF800000\n" \
"L_ap6:\n" \
"s_cbranch_execz L_ap0\n" \
"v_mfma_f32_16x16x32_bf16 v[156:159], a[216:219], a[80:83], v[156:159]\n" \
"ds_read_b64_tr_b16 a[216:217], v33 offset:18496\n" \
"ds_read_b64_tr_b16 a[218:219], v33 offset:23104\n" \
"buffer_load_dwordx4 a[152:155], v32, s[56:59], s70 offen offset:2048 sc1\n" \
"v_mfma_f32_16x16x32_bf16 v[160:163], a[0:3], a[64:67], v[160:163]\n" \
"v_mfma_f32_16x16x32_bf16 v[164:167], a[0:3], a[80:83], v[164:167]\n" \
"ds_read_b64_tr_b16 a[0:1], v33 offset:18528\n" \
"s_waitcnt lgkmcnt(13)\n" \
"v_mfma_f32_16x16x32_bf16 v[168:171], a[4:7], a[64:67], v[168:171]\n" \
"ds_read_b64_tr_b16 a[2:3], v33 offset:23136\n" \
"v_mfma_f32_16x16x32_bf16 v[172:175], a[4:7], a[80:83], v[172:175]\n" \
"ds_read_b64_tr_b16 a[4:5], v33 offset:18560\n" \
"ds_read_b64_tr_b16 a[6:7], v33 offset:23168\n" \
"buffer_load_dwordx4 a[140:143], v20, s[56:59], s70 offen offset:3072 sc1\n" \
"v_mfma_f32_16x16x32_bf16 v[176:179], a[8:11], a[64:67], v[176:179]\n" \
"v_mfma_f32_16x16x32_bf16 v[180:183], a[8:11], a[80:83], v[180:183]\n" \
"ds_read_b64_tr_b16 a[8:9], v33 offset:18592\n" \
"ds_read_b64_tr_b16 a[10:11], v33 offset:23200\n" \
"v_mfma_f32_16x16x32_bf16 v[112:115], a[12:15], a[68:71], v[112:115]\n" \
"v_mfma_f32_16x16x32_bf16 v[116:119], a[12:15], a[84:87], v[116:119]\n" \
"ds_read_b64_tr_b16 a[12:13], v33 offset:18624\n" \
"ds_read_b64_tr_b16 a[14:15], v33 offset:23232\n" \
"buffer_load_dwordx4 a[156:159], v32, s[56:59], s70 offen offset:3072 sc1\n" \
"v_mfma_f32_16x16x32_bf16 v[120:123], a[16:19], a[68:71], v[120:123]\n" \
"v_readlane_b32 s70, v11, 48\n" \
"v_mfma_f32_16x16x32_bf16 v[124:127], a[16:19], a[84:87], v[124:127]\n" \
"ds_read_b64_tr_b16 a[16:17], v33 offset:18656\n" \
"ds_read_b64_tr_b16 a[18:19], v33 offset:23264\n" \
"v_mfma_f32_16x16x32_bf16 v[128:131], a[20:23], a[68:71], v[128:131]\n" \
"v_mfma_f32_16x16x32_bf16 v[132:135], a[20:23], a[84:87], v[132:135]\n" \
"ds_read_b64_tr_b16 a[20:21], v33 offset:18688\n" \
"ds_read_b64_tr_b16 a[22:23], v33 offset:23296\n" \
"buffer_load_dwordx4 a[32:35], v20, s[56:59], s71 offen offset:0 sc1\n" \
"v_mfma_f32_16x16x32_bf16 v[136:139], a[24:27], a[68:71], v[136:139]\n" \
"v_mfma_f32_16x16x32_bf16 v[140:143], a[24:27], a[84:87], v[140:143]\n" \
"ds_read_b64_tr_b16 a[24:25], v33 offset:27648\n" \
"ds_read_b64_tr_b16 a[26:27], v33 offset:32256\n" \
"v_mfma_f32_16x16x32_bf16 v[144:147], a[28:31], a[68:71], v[144:147]\n" \
"v_mfma_f32_16x16x32_bf16 v[148:151], a[28:31], a[84:87], v[148:151]\n" \
"ds_read_b64_tr_b16 a[28:29], v33 offset:27680\n" \
"ds_read_b64_tr_b16 a[30:31], v33 offset:32288\n" \
"buffer_load_dwordx4 a[48:51], v32, s[56:59], s71 offen offset:0 sc1\n" \
"v_mfma_f32_16x16x32_bf16 v[152:155], a[192:195], a[68:71], v[152:155]\n" \
"v_mfma_f32_16x16x32_bf16 v[156:159], a[192:195], a[84:87], v[156:159]\n" \
"ds_read_b64_tr_b16 a[192:193], v33 offset:27712\n" \
"s_waitcnt lgkmcnt(13)\n" \
"v_mfma_f32_16x16x32_bf16 v[160:163], a[196:199], a[68:71], v[160:163]\n" \
"ds_read_b64_tr_b16 a[194:195], v33 offset:32320\n" \
"v_mfma_f32_16x16x32_bf16 v[164:167], a[196:199], a[84:87], v[164:167]\n" \
"ds_read_b64_tr_b16 a[196:197], v33 offset:27744\n" \
"ds_read_b64_tr_b16 a[198:199], v33 offset:32352\n" \
"buffer_load_dwordx4 a[36:39], v20, s[56:59], s71 offen offset:1024 sc1\n" \
"v_mfma_f32_16x16x32_bf16 v[168:171], a[200:203], a[68:71], v[168:171]\n" \
"v_mfma_f32_16x16x32_bf16 v[172:175], a[200:203], a[84:87], v[172:175]\n" \
"ds_read_b64_tr_b16 a[200:201], v33 offset:27776\n" \
"ds_read_b64_tr_b16 a[202:203], v33 offset:32384\n" \
"v_mfma_f32_16x16x32_bf16 v[176:179], a[204:207], a[68:71], v[176:179]\n" \
"v_mfma_f32_16x16x32_bf16 v[180:183], a[204:207], a[84:87], v[180:183]\n" \
"ds_read_b64_tr_b16 a[204:205], v33 offset:27808\n" \
"ds_read_b64_tr_b16 a[206:207], v33 offset:32416\n" \
"buffer_load_dwordx4 a[52:55], v32, s[56:59], s71 offen offset:1024 sc1\n" \
"v_mfma_f32_16x16x32_bf16 v[112:115], a[208:211], a[72:75], v[112:115]\n" \
"v_mfma_f32_16x16x32_bf16 v[116:119], a[208:211], a[88:91], v[116:119]\n" \
"ds_read_b64_tr_b16 a[208:209], v33 offset:27840\n" \
"ds_read_b64_tr_b16 a[210:211], v33 offset:32448\n" \
"v_mfma_f32_16x16x32_bf16 v[120:123], a[212:215], a[72:75], v[120:123]\n" \
"v_mfma_f32_16x16x32_bf16 v[124:127], a[212:215], a[88:91], v[124:127]\n" \
"ds_read_b64_tr_b16 a[212:213], v33 offset:27872\n" \
"ds_read_b64_tr_b16 a[214:215], v33 offset:32480\n" \
"buffer_load_dwordx4 a[40:43], v20, s[56:59], s71 offen offset:2048 sc1\n" \
"v_mfma_f32_16x16x32_bf16 v[128:131], a[216:219], a[72:75], v[128:131]\n" \
"v_mfma_f32_16x16x32_bf16 v[132:135], a[216:219], a[88:91], v[132:135]\n" \
"ds_read_b64_tr_b16 a[216:217], v33 offset:27904\n" \
"ds_read_b64_tr_b16 a[218:219], v33 offset:32512\n" \
"v_mfma_f32_16x16x32_bf16 v[136:139], a[0:3], a[72:75], v[136:139]\n" \
"v_mfma_f32_16x16x32_bf16 v[140:143], a[0:3], a[88:91], v[140:143]\n" \
"v_mfma_f32_16x16x32_bf16 v[144:147], a[4:7], a[72:75], v[144:147]\n" \
"buffer_load_dwordx4 a[56:59], v32, s[56:59], s71 offen offset:2048 sc1\n" \
"v_mfma_f32_16x16x32_bf16 v[148:151], a[4:7], a[88:91], v[148:151]\n" \
"s_waitcnt lgkmcnt(10)\n" \
"v_mfma_f32_16x16x32_bf16 v[152:155], a[8:11], a[72:75], v[152:155]\n" \
"v_mfma_f32_16x16x32_bf16 v[156:159], a[8:11], a[88:91], v[156:159]\n" \
"v_mfma_f32_16x16x32_bf16 v[160:163], a[12:15], a[72:75], v[160:163]\n" \
"v_mfma_f32_16x16x32_bf16 v[164:167], a[12:15], a[88:91], v[164:167]\n" \
"buffer_load_dwordx4 a[44:47], v20, s[56:59], s71 offen offset:3072 sc1\n" \
"v_mfma_f32_16x16x32_bf16 v[168:171], a[16:19], a[72:75], v[168:171]\n" \
"v_mfma_f32_16x16x32_bf16 v[172:175], a[16:19], a[88:91], v[172:175]\n" \
"v_mfma_f32_16x16x32_bf16 v[176:179], a[20:23], a[72:75], v[176:179]\n" \
"v_mfma_f32_16x16x32_bf16 v[180:183], a[20:23], a[88:91], v[180:183]\n" \
"buffer_load_dwordx4 a[60:63], v32, s[56:59], s71 offen offset:3072 sc1\n" \
"v_readlane_b32 s71, v11, 49\n" \
"buffer_load_dwordx4 a[96:99], v20, s[56:59], s66 offen offset:0 sc1\n" \
"buffer_load_dwordx4 a[112:115], v32, s[56:59], s66 offen offset:0 sc1\n" \
"s_waitcnt vmcnt(20)\n" \
"s_barrier\n" \
"ds_read_b64_tr_b16 a[0:1], v33 offset:36864\n" \
"ds_read_b64_tr_b16 a[2:3], v33 offset:41472\n" \
"ds_read_b64_tr_b16 a[4:5], v33 offset:46080\n" \
"ds_read_b64_tr_b16 a[6:7], v33 offset:50688\n" \
"ds_read_b64_tr_b16 a[8:9], v33 offset:55296\n" \
"ds_read_b64_tr_b16 a[10:11], v33 offset:59904\n" \
"ds_read_b64_tr_b16 a[12:13], v10 offset:0\n" \
"ds_read_b64_tr_b16 a[14:15], v10 offset:4608\n" \
"ds_read_b64_tr_b16 a[16:17], v34 offset:0\n" \
"ds_read_b64_tr_b16 a[18:19], v34 offset:4608\n" \
"ds_read_b64_tr_b16 a[20:21], v34 offset:9216\n" \
"ds_read_b64_tr_b16 a[22:23], v34 offset:13824\n" \
"buffer_load_dwordx4 a[100:103], v20, s[56:59], s66 offen offset:1024 sc1\n" \
"s_waitcnt vmcnt(20)\n" \
"v_mfma_f32_16x16x32_bf16 v[112:115], a[24:27], a[76:79], v[112:115]\n" \
"s_waitcnt vmcnt(19)\n" \
"v_mfma_f32_16x16x32_bf16 v[116:119], a[24:27], a[92:95], v[116:119]\n" \
"ds_read_b64_tr_b16 a[24:25], v34 offset:18432\n" \
"ds_read_b64_tr_b16 a[26:27], v34 offset:23040\n" \
"v_mfma_f32_16x16x32_bf16 v[120:123], a[28:31], a[76:79], v[120:123]\n" \
"buffer_load_dwordx4 a[116:119], v32, s[56:59], s66 offen offset:1024 sc1\n" \
"v_mfma_f32_16x16x32_bf16 v[124:127], a[28:31], a[92:95], v[124:127]\n" \
"ds_read_b64_tr_b16 a[28:29], v10 offset:9216\n" \
"ds_read_b64_tr_b16 a[30:31], v10 offset:13824\n" \
"v_mfma_f32_16x16x32_bf16 v[128:131], a[192:195], a[76:79], v[128:131]\n" \
"v_mfma_f32_16x16x32_bf16 v[132:135], a[192:195], a[92:95], v[132:135]\n" \
"ds_read_b64_tr_b16 a[192:193], v33 offset:36896\n" \
"ds_read_b64_tr_b16 a[194:195], v33 offset:41504\n" \
"v_mfma_f32_16x16x32_bf16 v[136:139], a[196:199], a[76:79], v[136:139]\n" \
"v_mfma_f32_16x16x32_bf16 v[140:143], a[196:199], a[92:95], v[140:143]\n" \
"ds_read_b64_tr_b16 a[196:197], v33 offset:46112\n" \
"s_waitcnt lgkmcnt(15)\n" \
"v_mfma_f32_16x16x32_bf16 v[144:147], a[200:203], a[76:79], v[144:147]\n" \
"buffer_load_dwordx4 a[104:107], v20, s[56:59], s66 offen offset:2048 sc1\n" \
"v_mfma_f32_16x16x32_bf16 v[148:151], a[200:203], a[92:95], v[148:151]\n" \
"ds_read_b64_tr_b16 a[198:199], v33 offset:50720\n" \
"ds_read_b64_tr_b16 a[200:201], v33 offset:55328\n" \
"v_mfma_f32_16x16x32_bf16 v[152:155], a[204:207], a[76:79], v[152:155]\n" \
"ds_read_b64_tr_b16 a[202:203], v33 offset:59936\n" \
"v_mfma_f32_16x16x32_bf16 v[156:159], a[204:207], a[92:95], v[156:159]\n" \
"ds_read_b64_tr_b16 a[204:205], v10 offset:32\n" \
"ds_read_b64_tr_b16 a[206:207], v10 offset:4640\n" \
"v_mfma_f32_16x16x32_bf16 v[160:163], a[208:211], a[76:79], v[160:163]\n" \
"v_mfma_f32_16x16x32_bf16 v[164:167], a[208:211], a[92:95], v[164:167]\n" \
"ds_read_b64_tr_b16 a[208:209], v34 offset:32\n" \
"ds_read_b64_tr_b16 a[210:211], v34 offset:4640\n" \
"v_mfma_f32_16x16x32_bf16 v[168:171], a[212:215], a[76:79], v[168:171]\n" \
"buffer_load_dwordx4 a[120:123], v32, s[56:59], s66 offen offset:2048 sc1\n" \
"v_mfma_f32_16x16x32_bf16 v[172:175], a[212:215], a[92:95], v[172:175]\n" \
"ds_read_b64_tr_b16 a[212:213], v34 offset:9248\n" \
"ds_read_b64_tr_b16 a[214:215], v34 offset:13856\n" \
"v_mfma_f32_16x16x32_bf16 v[176:179], a[216:219], a[76:79], v[176:179]\n" \
"v_mfma_f32_16x16x32_bf16 v[180:183], a[216:219], a[92:95], v[180:183]\n" \
"ds_read_b64_tr_b16 a[216:217], v34 offset:18464\n" \
"s_waitcnt vmcnt(21)\n" \
"v_mfma_f32_16x16x32_bf16 v[112:115], a[0:3], a[128:131], v[112:115]\n" \
"ds_read_b64_tr_b16 a[218:219], v34 offset:23072\n" \
"s_waitcnt vmcnt(20)\n" \
"v_mfma_f32_16x16x32_bf16 v[116:119], a[0:3], a[144:147], v[116:119]\n" \
"ds_read_b64_tr_b16 a[0:1], v10 offset:9248\n" \
"s_waitcnt vmcnt(19)\n" \
"v_mfma_f32_16x16x32_bf16 v[112:115], a[4:7], a[132:135], v[112:115]\n" \
"ds_read_b64_tr_b16 a[2:3], v10 offset:13856\n" \
"s_waitcnt vmcnt(18)\n" \
"v_mfma_f32_16x16x32_bf16 v[116:119], a[4:7], a[148:151], v[116:119]\n" \
"ds_read_b64_tr_b16 a[4:5], v33 offset:36928\n" \
"ds_read_b64_tr_b16 a[6:7], v33 offset:41536\n" \
"buffer_load_dwordx4 a[108:111], v20, s[56:59], s66 offen offset:3072 sc1\n" \
"s_waitcnt vmcnt(18) lgkmcnt(15)\n" \
"v_mfma_f32_16x16x32_bf16 v[112:115], a[8:11], a[136:139], v[112:115]\n" \
"buffer_load_dwordx4 a[124:127], v32, s[56:59], s66 offen offset:3072 sc1\n" \
"s_waitcnt vmcnt(18)\n" \
"v_mfma_f32_16x16x32_bf16 v[116:119], a[8:11], a[152:155], v[116:119]\n" \
"ds_read_b64_tr_b16 a[8:9], v33 offset:46144\n" \
"ds_read_b64_tr_b16 a[10:11], v33 offset:50752\n" \
"buffer_load_dwordx4 a[160:163], v20, s[56:59], s92 offen offset:0 sc1\n" \
"s_waitcnt vmcnt(18)\n" \
"v_mfma_f32_16x16x32_bf16 v[112:115], a[12:15], a[140:143], v[112:115]\n" \
"buffer_load_dwordx4 a[176:179], v32, s[56:59], s92 offen offset:0 sc1\n" \
"s_waitcnt vmcnt(18)\n" \
"v_mfma_f32_16x16x32_bf16 v[116:119], a[12:15], a[156:159], v[116:119]\n" \
"ds_read_b64_tr_b16 a[12:13], v33 offset:55360\n" \
"ds_read_b64_tr_b16 a[14:15], v33 offset:59968\n" \
"buffer_load_dwordx4 a[164:167], v20, s[56:59], s92 offen offset:1024 sc1\n" \
"s_waitcnt vmcnt(18)\n" \
"v_mfma_f32_16x16x32_bf16 v[112:115], a[16:19], a[32:35], v[112:115]\n" \
"buffer_load_dwordx4 a[180:183], v32, s[56:59], s92 offen offset:1024 sc1\n" \
"s_waitcnt vmcnt(18)\n" \
"v_mfma_f32_16x16x32_bf16 v[116:119], a[16:19], a[48:51], v[116:119]\n" \
"ds_read_b64_tr_b16 a[16:17], v10 offset:64\n" \
"ds_read_b64_tr_b16 a[18:19], v10 offset:4672\n" \
"buffer_load_dwordx4 a[168:171], v20, s[56:59], s92 offen offset:2048 sc1\n" \
"s_waitcnt vmcnt(18)\n" \
"v_mfma_f32_16x16x32_bf16 v[112:115], a[20:23], a[36:39], v[112:115]\n" \
"buffer_load_dwordx4 a[184:187], v32, s[56:59], s92 offen offset:2048 sc1\n" \
"s_waitcnt vmcnt(18)\n" \
"v_mfma_f32_16x16x32_bf16 v[116:119], a[20:23], a[52:55], v[116:119]\n" \
"ds_read_b64_tr_b16 a[20:21], v34 offset:64\n" \
"ds_read_b64_tr_b16 a[22:23], v34 offset:4672\n" \
"buffer_load_dwordx4 a[172:175], v20, s[56:59], s92 offen offset:3072 sc1\n" \
"s_waitcnt vmcnt(18)\n" \
"v_mfma_f32_16x16x32_bf16 v[112:115], a[24:27], a[40:43], v[112:115]\n" \
"buffer_load_dwordx4 a[188:191], v32, s[56:59], s92 offen offset:3072 sc1\n" \
"v_readlane_b32 s92, v11, 52\n" \
"s_waitcnt vmcnt(18)\n" \
"v_mfma_f32_16x16x32_bf16 v[116:119], a[24:27], a[56:59], v[116:119]\n" \
"ds_read_b64_tr_b16 a[24:25], v34 offset:9280\n" \
"ds_read_b64_tr_b16 a[26:27], v34 offset:13888\n" \
"buffer_load_dwordx4 a[220:223], v20, s[56:59], s95 offen offset:0 sc1\n" \
"s_waitcnt vmcnt(18)\n" \
"v_mfma_f32_16x16x32_bf16 v[112:115], a[28:31], a[44:47], v[112:115]\n" \
"buffer_load_dwordx4 a[236:239], v32, s[56:59], s95 offen offset:0 sc1\n" \
"s_waitcnt vmcnt(18)\n" \
"v_mfma_f32_16x16x32_bf16 v[116:119], a[28:31], a[60:63], v[116:119]\n" \
"ds_read_b64_tr_b16 a[28:29], v34 offset:18496\n" \
"ds_read_b64_tr_b16 a[30:31], v34 offset:23104\n" \
"v_mfma_f32_16x16x32_bf16 v[120:123], a[192:195], a[128:131], v[120:123]\n" \
"buffer_load_dwordx4 a[224:227], v20, s[56:59], s95 offen offset:1024 sc1\n" \
"v_mfma_f32_16x16x32_bf16 v[124:127], a[192:195], a[144:147], v[124:127]\n" \
"v_cvt_pk_bf16_f32 v112, v112, v113\n" \
"v_cvt_pk_bf16_f32 v113, v114, v115\n" \
"s_waitcnt lgkmcnt(4)\n" \
"v_mfma_f32_16x16x32_bf16 v[120:123], a[196:199], a[132:135], v[120:123]\n" \
"v_cvt_pk_bf16_f32 v114, v116, v117\n" \
"v_cvt_pk_bf16_f32 v115, v118, v119\n" \
"buffer_store_dwordx4 v[112:115], v20, s[56:59], s67 offen offset:0 sc1\n" \
"s_add_u32 s67, s67, 0x3800\n" \
"v_mfma_f32_16x16x32_bf16 v[124:127], a[196:199], a[148:151], v[124:127]\n" \
"ds_read_b64_tr_b16 a[192:193], v10 offset:9280\n" \
"ds_read_b64_tr_b16 a[194:195], v10 offset:13888\n" \
"v_mfma_f32_16x16x32_bf16 v[120:123], a[200:203], a[136:139], v[120:123]\n" \
"ds_read_b64_tr_b16 a[196:197], v33 offset:36960\n" \
"ds_read_b64_tr_b16 a[198:199], v33 offset:41568\n" \
"v_mfma_f32_16x16x32_bf16 v[124:127], a[200:203], a[152:155], v[124:127]\n" \
"ds_read_b64_tr_b16 a[200:201], v33 offset:46176\n" \
"ds_read_b64_tr_b16 a[202:203], v33 offset:50784\n" \
"v_mfma_f32_16x16x32_bf16 v[120:123], a[204:207], a[140:143], v[120:123]\n" \
"buffer_load_dwordx4 a[240:243], v32, s[56:59], s95 offen offset:1024 sc1\n" \
"v_mfma_f32_16x16x32_bf16 v[124:127], a[204:207], a[156:159], v[124:127]\n" \
"ds_read_b64_tr_b16 a[204:205], v33 offset:55392\n" \
"ds_read_b64_tr_b16 a[206:207], v33 offset:60000\n" \
"v_mfma_f32_16x16x32_bf16 v[120:123], a[208:211], a[32:35], v[120:123]\n" \
"v_mfma_f32_16x16x32_bf16 v[124:127], a[208:211], a[48:51], v[124:127]\n" \
"ds_read_b64_tr_b16 a[208:209], v10 offset:96\n" \
"ds_read_b64_tr_b16 a[210:211], v10 offset:4704\n" \
"v_mfma_f32_16x16x32_bf16 v[120:123], a[212:215], a[36:39], v[120:123]\n" \
"v_mfma_f32_16x16x32_bf16 v[124:127], a[212:215], a[52:55], v[124:127]\n" \
"ds_read_b64_tr_b16 a[212:213], v34 offset:96\n" \
"ds_read_b64_tr_b16 a[214:215], v34 offset:4704\n" \
"v_mfma_f32_16x16x32_bf16 v[120:123], a[216:219], a[40:43], v[120:123]\n" \
"buffer_load_dwordx4 a[228:231], v20, s[56:59], s95 offen offset:2048 sc1\n" \
"v_mfma_f32_16x16x32_bf16 v[124:127], a[216:219], a[56:59], v[124:127]\n" \
"ds_read_b64_tr_b16 a[216:217], v34 offset:9312\n" \
"ds_read_b64_tr_b16 a[218:219], v34 offset:13920\n" \
"v_mfma_f32_16x16x32_bf16 v[120:123], a[0:3], a[44:47], v[120:123]\n" \
"v_mfma_f32_16x16x32_bf16 v[124:127], a[0:3], a[60:63], v[124:127]\n" \
"ds_read_b64_tr_b16 a[0:1], v34 offset:18528\n" \
"ds_read_b64_tr_b16 a[2:3], v34 offset:23136\n" \
"v_mfma_f32_16x16x32_bf16 v[128:131], a[4:7], a[128:131], v[128:131]\n" \
"v_mfma_f32_16x16x32_bf16 v[132:135], a[4:7], a[144:147], v[132:135]\n" \
"ds_read_b64_tr_b16 a[4:5], v10 offset:9312\n" \
"ds_read_b64_tr_b16 a[6:7], v10 offset:13920\n" \
"v_mfma_f32_16x16x32_bf16 v[128:131], a[8:11], a[132:135], v[128:131]\n" \
"v_cvt_pk_bf16_f32 v120, v120, v121\n" \
"v_cvt_pk_bf16_f32 v121, v122, v123\n" \
"v_cvt_pk_bf16_f32 v122, v124, v125\n" \
"v_cvt_pk_bf16_f32 v123, v126, v127\n" \
"buffer_store_dwordx4 v[120:123], v20, s[56:59], s68 offen offset:0 sc1\n" \
"s_add_u32 s68, s68, 0x3000\n" \
"v_mfma_f32_16x16x32_bf16 v[132:135], a[8:11], a[148:151], v[132:135]\n" \
"ds_read_b64_tr_b16 a[8:9], v33 offset:36992\n" \
"ds_read_b64_tr_b16 a[10:11], v33 offset:41600\n" \
"v_mfma_f32_16x16x32_bf16 v[128:131], a[12:15], a[136:139], v[128:131]\n" \
"v_mfma_f32_16x16x32_bf16 v[132:135], a[12:15], a[152:155], v[132:135]\n" \
"ds_read_b64_tr_b16 a[12:13], v33 offset:46208\n" \
"ds_read_b64_tr_b16 a[14:15], v33 offset:50816\n" \
"v_mfma_f32_16x16x32_bf16 v[128:131], a[16:19], a[140:143], v[128:131]\n" \
"buffer_load_dwordx4 a[244:247], v32, s[56:59], s95 offen offset:2048 sc1\n" \
"v_mfma_f32_16x16x32_bf16 v[132:135], a[16:19], a[156:159], v[132:135]\n" \
"ds_read_b64_tr_b16 a[16:17], v33 offset:55424\n" \
"ds_read_b64_tr_b16 a[18:19], v33 offset:60032\n" \
"v_mfma_f32_16x16x32_bf16 v[128:131], a[20:23], a[32:35], v[128:131]\n" \
"v_mfma_f32_16x16x32_bf16 v[132:135], a[20:23], a[48:51], v[132:135]\n" \
"ds_read_b64_tr_b16 a[20:21], v10 offset:128\n" \
"s_waitcnt lgkmcnt(13)\n" \
"v_mfma_f32_16x16x32_bf16 v[128:131], a[24:27], a[36:39], v[128:131]\n" \
"ds_read_b64_tr_b16 a[22:23], v10 offset:4736\n" \
"v_mfma_f32_16x16x32_bf16 v[132:135], a[24:27], a[52:55], v[132:135]\n" \
"buffer_load_dwordx4 a[232:235], v20, s[56:59], s95 offen offset:3072 sc1\n" \
"v_mfma_f32_16x16x32_bf16 v[128:131], a[28:31], a[40:43], v[128:131]\n" \
"ds_read_b64_tr_b16 a[24:25], v34 offset:128\n" \
"ds_read_b64_tr_b16 a[26:27], v34 offset:4736\n" \
"v_mfma_f32_16x16x32_bf16 v[132:135], a[28:31], a[56:59], v[132:135]\n" \
"ds_read_b64_tr_b16 a[28:29], v34 offset:9344\n" \
"ds_read_b64_tr_b16 a[30:31], v34 offset:13952\n" \
"v_mfma_f32_16x16x32_bf16 v[128:131], a[192:195], a[44:47], v[128:131]\n" \
"v_mfma_f32_16x16x32_bf16 v[132:135], a[192:195], a[60:63], v[132:135]\n" \
"ds_read_b64_tr_b16 a[192:193], v34 offset:18560\n" \
"ds_read_b64_tr_b16 a[194:195], v34 offset:23168\n" \
"v_mfma_f32_16x16x32_bf16 v[136:139], a[196:199], a[128:131], v[136:139]\n" \
"buffer_load_dwordx4 a[248:251], v32, s[56:59], s95 offen offset:3072 sc1\n" \
"v_mfma_f32_16x16x32_bf16 v[140:143], a[196:199], a[144:147], v[140:143]\n" \
"ds_read_b64_tr_b16 a[196:197], v10 offset:9344\n" \
"ds_read_b64_tr_b16 a[198:199], v10 offset:13952\n" \
"v_cvt_pk_bf16_f32 v128, v128, v129\n" \
"v_cvt_pk_bf16_f32 v129, v130, v131\n" \
"v_cvt_pk_bf16_f32 v130, v132, v133\n" \
"v_cvt_pk_bf16_f32 v131, v134, v135\n" \
"buffer_store_dwordx4 v[128:131], v20, s[56:59], s69 offen offset:0 sc1\n" \
"s_add_u32 s69, s69, 0x2800\n" \
"v_mfma_f32_16x16x32_bf16 v[136:139], a[200:203], a[132:135], v[136:139]\n" \
"v_readlane_b32 s95, v11, 46\n" \
"v_mfma_f32_16x16x32_bf16 v[140:143], a[200:203], a[148:151], v[140:143]\n" \
"ds_read_b64_tr_b16 a[200:201], v33 offset:37024\n" \
"ds_read_b64_tr_b16 a[202:203], v33 offset:41632\n" \
"v_mfma_f32_16x16x32_bf16 v[136:139], a[204:207], a[136:139], v[136:139]\n" \
"v_mfma_f32_16x16x32_bf16 v[140:143], a[204:207], a[152:155], v[140:143]\n" \
"ds_read_b64_tr_b16 a[204:205], v33 offset:46240\n" \
"ds_read_b64_tr_b16 a[206:207], v33 offset:50848\n" \
"v_mfma_f32_16x16x32_bf16 v[136:139], a[208:211], a[140:143], v[136:139]\n" \
"buffer_load_dwordx4 v[64:67], v20, s[56:59], s97 offen offset:0 sc1\n" \
"v_mfma_f32_16x16x32_bf16 v[140:143], a[208:211], a[156:159], v[140:143]\n" \
"ds_read_b64_tr_b16 a[208:209], v33 offset:55456\n" \
"ds_read_b64_tr_b16 a[210:211], v33 offset:60064\n" \
"v_mfma_f32_16x16x32_bf16 v[136:139], a[212:215], a[32:35], v[136:139]\n" \
"v_mfma_f32_16x16x32_bf16 v[140:143], a[212:215], a[48:51], v[140:143]\n" \
"ds_read_b64_tr_b16 a[212:213], v10 offset:160\n" \
"s_waitcnt lgkmcnt(12)\n" \
"v_mfma_f32_16x16x32_bf16 v[136:139], a[216:219], a[36:39], v[136:139]\n" \
"ds_read_b64_tr_b16 a[214:215], v10 offset:4768\n" \
"v_mfma_f32_16x16x32_bf16 v[140:143], a[216:219], a[52:55], v[140:143]\n" \
"ds_read_b64_tr_b16 a[216:217], v34 offset:160\n" \
"ds_read_b64_tr_b16 a[218:219], v34 offset:4768\n" \
"v_mfma_f32_16x16x32_bf16 v[136:139], a[0:3], a[40:43], v[136:139]\n" \
"buffer_load_dwordx4 v[68:71], v20, s[56:59], s97 offen offset:1024 sc1\n" \
"v_mfma_f32_16x16x32_bf16 v[140:143], a[0:3], a[56:59], v[140:143]\n" \
"ds_read_b64_tr_b16 a[0:1], v34 offset:9376\n" \
"ds_read_b64_tr_b16 a[2:3], v34 offset:13984\n" \
"v_mfma_f32_16x16x32_bf16 v[136:139], a[4:7], a[44:47], v[136:139]\n" \
"v_mfma_f32_16x16x32_bf16 v[140:143], a[4:7], a[60:63], v[140:143]\n" \
"ds_read_b64_tr_b16 a[4:5], v34 offset:18592\n" \
"ds_read_b64_tr_b16 a[6:7], v34 offset:23200\n" \
"v_mfma_f32_16x16x32_bf16 v[144:147], a[8:11], a[128:131], v[144:147]\n" \
"v_mfma_f32_16x16x32_bf16 v[148:151], a[8:11], a[144:147], v[148:151]\n" \
"ds_read_b64_tr_b16 a[8:9], v10 offset:9376\n" \
"ds_read_b64_tr_b16 a[10:11], v10 offset:13984\n" \
"v_mfma_f32_16x16x32_bf16 v[144:147], a[12:15], a[132:135], v[144:147]\n" \
"v_cvt_pk_bf16_f32 v136, v136, v137\n" \
"v_cvt_pk_bf16_f32 v137, v138, v139\n" \
"v_cvt_pk_bf16_f32 v138, v140, v141\n" \
"v_cvt_pk_bf16_f32 v139, v142, v143\n" \
"buffer_store_dwordx4 v[136:139], v20, s[56:59], s70 offen offset:0 sc1\n" \
"s_add_u32 s70, s70, 0x2000\n" \
"v_mfma_f32_16x16x32_bf16 v[148:151], a[12:15], a[148:151], v[148:151]\n" \
"ds_read_b64_tr_b16 a[12:13], v33 offset:37056\n" \
"ds_read_b64_tr_b16 a[14:15], v33 offset:41664\n" \
"v_mfma_f32_16x16x32_bf16 v[144:147], a[16:19], a[136:139], v[144:147]\n" \
"v_mfma_f32_16x16x32_bf16 v[148:151], a[16:19], a[152:155], v[148:151]\n" \
"ds_read_b64_tr_b16 a[16:17], v33 offset:46272\n" \
"ds_read_b64_tr_b16 a[18:19], v33 offset:50880\n" \
"v_mfma_f32_16x16x32_bf16 v[144:147], a[20:23], a[140:143], v[144:147]\n" \
"buffer_load_dwordx4 v[72:75], v32, s[56:59], s97 offen offset:0 sc1\n" \
"v_mfma_f32_16x16x32_bf16 v[148:151], a[20:23], a[156:159], v[148:151]\n" \
"ds_read_b64_tr_b16 a[20:21], v33 offset:55488\n" \
"ds_read_b64_tr_b16 a[22:23], v33 offset:60096\n" \
"v_mfma_f32_16x16x32_bf16 v[144:147], a[24:27], a[32:35], v[144:147]\n" \
"v_mfma_f32_16x16x32_bf16 v[148:151], a[24:27], a[48:51], v[148:151]\n" \
"ds_read_b64_tr_b16 a[24:25], v10 offset:192\n" \
"s_waitcnt lgkmcnt(13)\n" \
"v_mfma_f32_16x16x32_bf16 v[144:147], a[28:31], a[36:39], v[144:147]\n" \
"ds_read_b64_tr_b16 a[26:27], v10 offset:4800\n" \
"v_mfma_f32_16x16x32_bf16 v[148:151], a[28:31], a[52:55], v[148:151]\n" \
"ds_read_b64_tr_b16 a[28:29], v34 offset:192\n" \
"ds_read_b64_tr_b16 a[30:31], v34 offset:4800\n" \
"v_mfma_f32_16x16x32_bf16 v[144:147], a[192:195], a[40:43], v[144:147]\n" \
"buffer_load_dwordx4 v[76:79], v32, s[56:59], s97 offen offset:1024 sc1\n" \
"v_mfma_f32_16x16x32_bf16 v[148:151], a[192:195], a[56:59], v[148:151]\n" \
"ds_read_b64_tr_b16 a[192:193], v34 offset:9408\n" \
"ds_read_b64_tr_b16 a[194:195], v34 offset:14016\n" \
"v_mfma_f32_16x16x32_bf16 v[144:147], a[196:199], a[44:47], v[144:147]\n" \
"v_readlane_b32 s97, v11, 48\n" \
"v_mfma_f32_16x16x32_bf16 v[148:151], a[196:199], a[60:63], v[148:151]\n" \
"ds_read_b64_tr_b16 a[196:197], v34 offset:18624\n" \
"ds_read_b64_tr_b16 a[198:199], v34 offset:23232\n" \
"v_mfma_f32_16x16x32_bf16 v[152:155], a[200:203], a[128:131], v[152:155]\n" \
"v_mfma_f32_16x16x32_bf16 v[156:159], a[200:203], a[144:147], v[156:159]\n" \
"ds_read_b64_tr_b16 a[200:201], v10 offset:9408\n" \
"ds_read_b64_tr_b16 a[202:203], v10 offset:14016\n" \
"v_cvt_pk_bf16_f32 v144, v144, v145\n" \
"v_cvt_pk_bf16_f32 v145, v146, v147\n" \
"v_cvt_pk_bf16_f32 v146, v148, v149\n" \
"v_cvt_pk_bf16_f32 v147, v150, v151\n" \
"buffer_store_dwordx4 v[144:147], v20, s[56:59], s71 offen offset:0 sc1\n" \
"s_add_u32 s71, s71, 0x1800\n" \
"v_mfma_f32_16x16x32_bf16 v[152:155], a[204:207], a[132:135], v[152:155]\n" \
"v_mfma_f32_16x16x32_bf16 v[156:159], a[204:207], a[148:151], v[156:159]\n" \
"ds_read_b64_tr_b16 a[204:205], v33 offset:256\n" \
"ds_read_b64_tr_b16 a[206:207], v33 offset:4864\n" \
"v_mfma_f32_16x16x32_bf16 v[152:155], a[208:211], a[136:139], v[152:155]\n" \
"v_mfma_f32_16x16x32_bf16 v[156:159], a[208:211], a[152:155], v[156:159]\n" \
"ds_read_b64_tr_b16 a[208:209], v33 offset:9472\n" \
"ds_read_b64_tr_b16 a[210:211], v33 offset:14080\n" \
"v_mfma_f32_16x16x32_bf16 v[152:155], a[212:215], a[140:143], v[152:155]\n" \
"buffer_load_dwordx4 v[80:83], v20, s[56:59], s64 offen offset:0 sc1\n" \
"v_mfma_f32_16x16x32_bf16 v[156:159], a[212:215], a[156:159], v[156:159]\n" \
"ds_read_b64_tr_b16 a[212:213], v33 offset:18688\n" \
"ds_read_b64_tr_b16 a[214:215], v33 offset:23296\n" \
"v_mfma_f32_16x16x32_bf16 v[152:155], a[216:219], a[32:35], v[152:155]\n" \
"v_mfma_f32_16x16x32_bf16 v[156:159], a[216:219], a[48:51], v[156:159]\n" \
"ds_read_b64_tr_b16 a[216:217], v33 offset:27904\n" \
"s_waitcnt lgkmcnt(12)\n" \
"v_mfma_f32_16x16x32_bf16 v[152:155], a[0:3], a[36:39], v[152:155]\n" \
"ds_read_b64_tr_b16 a[218:219], v33 offset:32512\n" \
"v_mfma_f32_16x16x32_bf16 v[156:159], a[0:3], a[52:55], v[156:159]\n" \
"ds_read_b64_tr_b16 a[0:1], v33 offset:37120\n" \
"ds_read_b64_tr_b16 a[2:3], v33 offset:41728\n" \
"v_mfma_f32_16x16x32_bf16 v[152:155], a[4:7], a[40:43], v[152:155]\n" \
"buffer_load_dwordx4 v[84:87], v20, s[56:59], s64 offen offset:1024 sc1\n" \
"v_mfma_f32_16x16x32_bf16 v[156:159], a[4:7], a[56:59], v[156:159]\n" \
"ds_read_b64_tr_b16 a[4:5], v33 offset:46336\n" \
"ds_read_b64_tr_b16 a[6:7], v33 offset:50944\n" \
"v_mfma_f32_16x16x32_bf16 v[152:155], a[8:11], a[44:47], v[152:155]\n" \
"v_mfma_f32_16x16x32_bf16 v[156:159], a[8:11], a[60:63], v[156:159]\n" \
"ds_read_b64_tr_b16 a[8:9], v33 offset:55552\n" \
"ds_read_b64_tr_b16 a[10:11], v33 offset:60160\n" \
"v_mfma_f32_16x16x32_bf16 v[160:163], a[12:15], a[128:131], v[160:163]\n" \
"v_mfma_f32_16x16x32_bf16 v[164:167], a[12:15], a[144:147], v[164:167]\n" \
"ds_read_b64_tr_b16 a[12:13], v10 offset:256\n" \
"ds_read_b64_tr_b16 a[14:15], v10 offset:4864\n" \
"v_mfma_f32_16x16x32_bf16 v[160:163], a[16:19], a[132:135], v[160:163]\n" \
"v_cvt_pk_bf16_f32 v152, v152, v153\n" \
"v_cvt_pk_bf16_f32 v153, v154, v155\n" \
"v_cvt_pk_bf16_f32 v154, v156, v157\n" \
"v_cvt_pk_bf16_f32 v155, v158, v159\n" \
"buffer_store_dwordx4 v[152:155], v20, s[56:59], s72 offen offset:0 sc1\n" \
"s_add_u32 s72, s72, 0x1000\n" \
"v_mfma_f32_16x16x32_bf16 v[164:167], a[16:19], a[148:151], v[164:167]\n" \
"ds_read_b64_tr_b16 a[16:17], v34 offset:256\n" \
"ds_read_b64_tr_b16 a[18:19], v34 offset:4864\n" \
"v_mfma_f32_16x16x32_bf16 v[160:163], a[20:23], a[136:139], v[160:163]\n" \
"v_mfma_f32_16x16x32_bf16 v[164:167], a[20:23], a[152:155], v[164:167]\n" \
"ds_read_b64_tr_b16 a[20:21], v34 offset:9472\n" \
"ds_read_b64_tr_b16 a[22:23], v34 offset:14080\n" \
"v_mfma_f32_16x16x32_bf16 v[160:163], a[24:27], a[140:143], v[160:163]\n" \
"buffer_load_dwordx4 v[88:91], v32, s[56:59], s64 offen offset:0 sc1\n" \
"v_mfma_f32_16x16x32_bf16 v[164:167], a[24:27], a[156:159], v[164:167]\n" \
"ds_read_b64_tr_b16 a[24:25], v34 offset:18688\n" \
"ds_read_b64_tr_b16 a[26:27], v34 offset:23296\n" \
"v_mfma_f32_16x16x32_bf16 v[160:163], a[28:31], a[32:35], v[160:163]\n" \
"v_mfma_f32_16x16x32_bf16 v[164:167], a[28:31], a[48:51], v[164:167]\n" \
"ds_read_b64_tr_b16 a[28:29], v10 offset:9472\n" \
"s_waitcnt vmcnt(17)\n" \
"s_mov_b64 exec, 1\n" \
"buffer_store_dword v39, off, s[56:59], s67 offset:0 sc1\n" \
"s_mov_b64 exec, -1\n" \
"s_waitcnt lgkmcnt(11)\n" \
"v_mfma_f32_16x16x32_bf16 v[160:163], a[192:195], a[36:39], v[160:163]\n" \
"ds_read_b64_tr_b16 a[30:31], v10 offset:14080\n" \
"v_mfma_f32_16x16x32_bf16 v[164:167], a[192:195], a[52:55], v[164:167]\n" \
"ds_read_b64_tr_b16 a[192:193], v33 offset:0\n" \
"ds_read_b64_tr_b16 a[194:195], v33 offset:4608\n" \
"v_mfma_f32_16x16x32_bf16 v[160:163], a[196:199], a[40:43], v[160:163]\n" \
"v_mfma_f32_16x16x32_bf16 v[164:167], a[196:199], a[56:59], v[164:167]\n" \
"ds_read_b64_tr_b16 a[196:197], v33 offset:9216\n" \
"ds_read_b64_tr_b16 a[198:199], v33 offset:13824\n" \
"v_mfma_f32_16x16x32_bf16 v[160:163], a[200:203], a[44:47], v[160:163]\n" \
"buffer_load_dwordx4 v[92:95], v32, s[56:59], s64 offen offset:1024 sc1\n" \
"v_mfma_f32_16x16x32_bf16 v[164:167], a[200:203], a[60:63], v[164:167]\n" \
"ds_read_b64_tr_b16 a[200:201], v33 offset:18432\n" \
"ds_read_b64_tr_b16 a[202:203], v33 offset:23040\n" \
"v_mfma_f32_16x16x32_bf16 v[248:251], a[204:207], a[96:99], v[248:251]\n" \
"v_readlane_b32 s64, v11, 49\n" \
"v_mfma_f32_16x16x32_bf16 v[252:255], a[204:207], a[112:115], v[252:255]\n" \
"ds_read_b64_tr_b16 a[204:205], v33 offset:27648\n" \
"v_cvt_pk_bf16_f32 v160, v160, v161\n" \
"v_cvt_pk_bf16_f32 v161, v162, v163\n" \
"v_cvt_pk_bf16_f32 v162, v164, v165\n" \
"v_cvt_pk_bf16_f32 v163, v166, v167\n" \
"buffer_store_dwordx4 v[160:163], v20, s[56:59], s73 offen offset:0 sc1\n" \
"s_add_u32 s73, s73, 0x800\n" \
"v_mfma_f32_16x16x32_bf16 v[248:251], a[208:211], a[100:103], v[248:251]\n" \
"ds_read_b64_tr_b16 a[206:207], v33 offset:32256\n" \
"v_mfma_f32_16x16x32_bf16 v[252:255], a[208:211], a[116:119], v[252:255]\n" \
"ds_read_b64_tr_b16 a[208:209], v33 offset:36864\n" \
"ds_read_b64_tr_b16 a[210:211], v33 offset:41472\n" \
"v_mfma_f32_16x16x32_bf16 v[248:251], a[212:215], a[104:107], v[248:251]\n" \
"v_mfma_f32_16x16x32_bf16 v[252:255], a[212:215], a[120:123], v[252:255]\n" \
"ds_read_b64_tr_b16 a[212:213], v33 offset:46080\n" \
"ds_read_b64_tr_b16 a[214:215], v33 offset:50688\n" \
"v_mfma_f32_16x16x32_bf16 v[248:251], a[216:219], a[108:111], v[248:251]\n" \
"buffer_load_dwordx4 v[96:99], v20, s[56:59], s65 offen offset:0 sc1\n" \
"v_mfma_f32_16x16x32_bf16 v[252:255], a[216:219], a[124:127], v[252:255]\n" \
"ds_read_b64_tr_b16 a[216:217], v33 offset:55296\n" \
"ds_read_b64_tr_b16 a[218:219], v33 offset:59904\n" \
"s_waitcnt vmcnt(18)\n" \
"s_mov_b64 exec, 1\n" \
"buffer_store_dword v39, off, s[56:59], s68 offset:4 sc1\n" \
"s_mov_b64 exec, -1\n" \
"v_mfma_f32_16x16x32_bf16 v[176:179], a[0:3], a[128:131], v[176:179]\n" \
"v_mfma_f32_16x16x32_bf16 v[180:183], a[0:3], a[144:147], v[180:183]\n" \
"v_mfma_f32_16x16x32_bf16 v[248:251], a[0:3], a[160:163], v[248:251]\n" \
"v_mfma_f32_16x16x32_bf16 v[252:255], a[0:3], a[176:179], v[252:255]\n" \
"ds_read_b64_tr_b16 a[0:1], v10 offset:0\n" \
"ds_read_b64_tr_b16 a[2:3], v10 offset:4608\n" \
"v_mfma_f32_16x16x32_bf16 v[176:179], a[4:7], a[132:135], v[176:179]\n" \
"buffer_load_dwordx4 v[104:107], v32, s[56:59], s65 offen offset:0 sc1\n" \
"v_mfma_f32_16x16x32_bf16 v[180:183], a[4:7], a[148:151], v[180:183]\n" \
"v_mfma_f32_16x16x32_bf16 v[248:251], a[4:7], a[164:167], v[248:251]\n" \
"v_mfma_f32_16x16x32_bf16 v[252:255], a[4:7], a[180:183], v[252:255]\n" \
"ds_read_b64_tr_b16 a[4:5], v34 offset:0\n" \
"s_waitcnt lgkmcnt(9)\n" \
"v_mfma_f32_16x16x32_bf16 v[176:179], a[8:11], a[136:139], v[176:179]\n" \
"ds_read_b64_tr_b16 a[6:7], v34 offset:4608\n" \
"v_mfma_f32_16x16x32_bf16 v[180:183], a[8:11], a[152:155], v[180:183]\n" \
"buffer_load_dwordx4 v[100:103], v20, s[56:59], s65 offen offset:1024 sc1\n" \
"v_mfma_f32_16x16x32_bf16 v[248:251], a[8:11], a[168:171], v[248:251]\n" \
"v_mfma_f32_16x16x32_bf16 v[252:255], a[8:11], a[184:187], v[252:255]\n" \
"ds_read_b64_tr_b16 a[8:9], v34 offset:9216\n" \
"ds_read_b64_tr_b16 a[10:11], v34 offset:13824\n" \
"v_mfma_f32_16x16x32_bf16 v[176:179], a[12:15], a[140:143], v[176:179]\n" \
"v_mfma_f32_16x16x32_bf16 v[180:183], a[12:15], a[156:159], v[180:183]\n" \
"v_mfma_f32_16x16x32_bf16 v[248:251], a[12:15], a[172:175], v[248:251]\n" \
"buffer_load_dwordx4 v[108:111], v32, s[56:59], s65 offen offset:1024 sc1\n" \
"v_mfma_f32_16x16x32_bf16 v[252:255], a[12:15], a[188:191], v[252:255]\n" \
"ds_read_b64_tr_b16 a[12:13], v34 offset:18432\n" \
"ds_read_b64_tr_b16 a[14:15], v34 offset:23040\n" \
"s_waitcnt vmcnt(18)\n" \
"s_mov_b64 exec, 1\n" \
"buffer_store_dword v39, off, s[56:59], s69 offset:8 sc1\n" \
"s_mov_b64 exec, -1\n" \
"v_mfma_f32_16x16x32_bf16 v[176:179], a[16:19], a[32:35], v[176:179]\n" \
"v_mfma_f32_16x16x32_bf16 v[180:183], a[16:19], a[48:51], v[180:183]\n" \
"v_mfma_f32_16x16x32_bf16 v[248:251], a[16:19], a[220:223], v[248:251]\n" \
"v_mfma_f32_16x16x32_bf16 v[252:255], a[16:19], a[236:239], v[252:255]\n" \
"ds_read_b64_tr_b16 a[16:17], v10 offset:9216\n" \
"ds_read_b64_tr_b16 a[18:19], v10 offset:13824\n" \
".fill (L_ab + 28672 - .) / 4, 4, 0xBF800000\n" \
"L_ap7:\n" \
"s_cbranch_execz L_ap0\n" \
"v_mfma_f32_16x16x32_bf16 v[176:179], a[20:23], a[36:39], v[176:179]\n" \
"v_mfma_f32_16x16x32_bf16 v[180:183], a[20:23], a[52:55], v[180:183]\n" \
"v_mfma_f32_16x16x32_bf16 v[248:251], a[20:23], a[224:227], v[248:251]\n" \
"v_mfma_f32_16x16x32_bf16 v[252:255], a[20:23], a[240:243], v[252:255]\n" \
"ds_read_b64_tr_b16 a[20:21], v33 offset:32\n" \
"ds_read_b64_tr_b16 a[22:23], v33 offset:4640\n" \
"v_mfma_f32_16x16x32_bf16 v[176:179], a[24:27], a[40:43], v[176:179]\n" \
"v_mfma_f32_16x16x32_bf16 v[180:183], a[24:27], a[56:59], v[180:183]\n" \
"v_mfma_f32_16x16x32_bf16 v[248:251], a[24:27], a[228:231], v[248:251]\n" \
"v_mfma_f32_16x16x32_bf16 v[252:255], a[24:27], a[244:247], v[252:255]\n" \
"ds_read_b64_tr_b16 a[24:25], v33 offset:9248\n" \
"ds_read_b64_tr_b16 a[26:27], v33 offset:13856\n" \
"v_mfma_f32_16x16x32_bf16 v[176:179], a[28:31], a[44:47], v[176:179]\n" \
"v_mfma_f32_16x16x32_bf16 v[180:183], a[28:31], a[60:63], v[180:183]\n" \
"v_mfma_f32_16x16x32_bf16 v[248:251], a[28:31], a[232:235], v[248:251]\n" \
"v_mfma_f32_16x16x32_bf16 v[252:255], a[28:31], a[248:251], v[252:255]\n" \
"ds_read_b64_tr_b16 a[28:29], v33 offset:18464\n" \
"ds_read_b64_tr_b16 a[30:31], v33 offset:23072\n" \
"v_mfma_f32_16x16x32_bf16 v[184:187], a[192:195], a[96:99], v[184:187]\n" \
"s_waitcnt vmcnt(16)\n" \
"s_mov_b64 exec, 1\n" \
"buffer_store_dword v39, off, s[56:59], s70 offset:12 sc1\n" \
"s_mov_b64 exec, -1\n" \
"v_cvt_pk_bf16_f32 v176, v176, v177\n" \
"v_cvt_pk_bf16_f32 v248, v248, v249\n" \
"v_cvt_pk_bf16_f32 v177, v178, v179\n" \
"v_cvt_pk_bf16_f32 v249, v250, v251\n" \
"v_cvt_pk_bf16_f32 v178, v180, v181\n" \
"v_cvt_pk_bf16_f32 v179, v182, v183\n" \
"v_cvt_pk_bf16_f32 v250, v252, v253\n" \
"v_cvt_pk_bf16_f32 v251, v254, v255\n" \
"buffer_store_dwordx4 v[176:179], v20, s[56:59], s92 offen offset:0 sc1\n" \
"v_mfma_f32_16x16x32_bf16 v[188:191], a[192:195], a[112:115], v[188:191]\n" \
"ds_read_b64_tr_b16 a[192:193], v33 offset:27680\n" \
"ds_read_b64_tr_b16 a[194:195], v33 offset:32288\n" \
"v_mfma_f32_16x16x32_bf16 v[184:187], a[196:199], a[100:103], v[184:187]\n" \
"v_mfma_f32_16x16x32_bf16 v[188:191], a[196:199], a[116:119], v[188:191]\n" \
"buffer_store_dwordx4 v[248:251], v20, s[56:59], s92 offen offset:1024 sc1\n" \
"v_mfma_f32_16x16x32_bf16 v[184:187], a[200:203], a[104:107], v[184:187]\n" \
"ds_read_b64_tr_b16 a[196:197], v33 offset:36896\n" \
"ds_read_b64_tr_b16 a[198:199], v33 offset:41504\n" \
"v_mfma_f32_16x16x32_bf16 v[188:191], a[200:203], a[120:123], v[188:191]\n" \
"ds_read_b64_tr_b16 a[200:201], v33 offset:46112\n" \
"ds_read_b64_tr_b16 a[202:203], v33 offset:50720\n" \
"v_mfma_f32_16x16x32_bf16 v[184:187], a[204:207], a[108:111], v[184:187]\n" \
"v_readlane_b32 s92, v11, 51\n" \
"v_mfma_f32_16x16x32_bf16 v[188:191], a[204:207], a[124:127], v[188:191]\n" \
"ds_read_b64_tr_b16 a[204:205], v33 offset:55328\n" \
"s_waitcnt lgkmcnt(9)\n" \
"v_mfma_f32_16x16x32_bf16 v[184:187], a[208:211], a[160:163], v[184:187]\n" \
"ds_read_b64_tr_b16 a[206:207], v33 offset:59936\n" \
"v_mfma_f32_16x16x32_bf16 v[188:191], a[208:211], a[176:179], v[188:191]\n" \
"ds_read_b64_tr_b16 a[208:209], v10 offset:32\n" \
"ds_read_b64_tr_b16 a[210:211], v10 offset:4640\n" \
"v_mfma_f32_16x16x32_bf16 v[184:187], a[212:215], a[164:167], v[184:187]\n" \
"v_mfma_f32_16x16x32_bf16 v[188:191], a[212:215], a[180:183], v[188:191]\n" \
"ds_read_b64_tr_b16 a[212:213], v34 offset:32\n" \
"ds_read_b64_tr_b16 a[214:215], v34 offset:4640\n" \
"v_mfma_f32_16x16x32_bf16 v[184:187], a[216:219], a[168:171], v[184:187]\n" \
"v_mfma_f32_16x16x32_bf16 v[188:191], a[216:219], a[184:187], v[188:191]\n" \
"ds_read_b64_tr_b16 a[216:217], v34 offset:9248\n" \
"ds_read_b64_tr_b16 a[218:219], v34 offset:13856\n" \
"s_waitcnt vmcnt(16)\n" \
"s_mov_b64 exec, 1\n" \
"buffer_store_dword v39, off, s[56:59], s71 offset:16 sc1\n" \
"s_mov_b64 exec, -1\n" \
"v_mfma_f32_16x16x32_bf16 v[184:187], a[0:3], a[172:175], v[184:187]\n" \
"v_mfma_f32_16x16x32_bf16 v[188:191], a[0:3], a[188:191], v[188:191]\n" \
"ds_read_b64_tr_b16 a[0:1], v34 offset:18464\n" \
"ds_read_b64_tr_b16 a[2:3], v34 offset:23072\n" \
"v_mfma_f32_16x16x32_bf16 v[184:187], a[4:7], a[220:223], v[184:187]\n" \
"v_mfma_f32_16x16x32_bf16 v[188:191], a[4:7], a[236:239], v[188:191]\n" \
"ds_read_b64_tr_b16 a[4:5], v10 offset:9248\n" \
"ds_read_b64_tr_b16 a[6:7], v10 offset:13856\n" \
"v_mfma_f32_16x16x32_bf16 v[184:187], a[8:11], a[224:227], v[184:187]\n" \
"v_mfma_f32_16x16x32_bf16 v[188:191], a[8:11], a[240:243], v[188:191]\n" \
"ds_read_b64_tr_b16 a[8:9], v33 offset:64\n" \
"ds_read_b64_tr_b16 a[10:11], v33 offset:4672\n" \
"v_mfma_f32_16x16x32_bf16 v[184:187], a[12:15], a[228:231], v[184:187]\n" \
"v_mfma_f32_16x16x32_bf16 v[188:191], a[12:15], a[244:247], v[188:191]\n" \
"ds_read_b64_tr_b16 a[12:13], v33 offset:9280\n" \
"ds_read_b64_tr_b16 a[14:15], v33 offset:13888\n" \
"v_mfma_f32_16x16x32_bf16 v[184:187], a[16:19], a[232:235], v[184:187]\n" \
"v_mfma_f32_16x16x32_bf16 v[188:191], a[16:19], a[248:251], v[188:191]\n" \
"ds_read_b64_tr_b16 a[16:17], v33 offset:18496\n" \
"ds_read_b64_tr_b16 a[18:19], v33 offset:23104\n" \
"v_mfma_f32_16x16x32_bf16 v[192:195], a[20:23], a[96:99], v[192:195]\n" \
"v_mfma_f32_16x16x32_bf16 v[196:199], a[20:23], a[112:115], v[196:199]\n" \
"ds_read_b64_tr_b16 a[20:21], v33 offset:27712\n" \
"ds_read_b64_tr_b16 a[22:23], v33 offset:32320\n" \
"v_mfma_f32_16x16x32_bf16 v[192:195], a[24:27], a[100:103], v[192:195]\n" \
"v_cvt_pk_bf16_f32 v184, v184, v185\n" \
"v_cvt_pk_bf16_f32 v185, v186, v187\n" \
"v_cvt_pk_bf16_f32 v186, v188, v189\n" \
"v_cvt_pk_bf16_f32 v187, v190, v191\n" \
"buffer_store_dwordx4 v[184:187], v20, s[56:59], s94 offen offset:1024 sc1\n" \
"s_add_u32 s94, s94, 0x3800\n" \
"v_mfma_f32_16x16x32_bf16 v[196:199], a[24:27], a[116:119], v[196:199]\n" \
"ds_read_b64_tr_b16 a[24:25], v33 offset:36928\n" \
"s_waitcnt lgkmcnt(13)\n" \
"v_mfma_f32_16x16x32_bf16 v[192:195], a[28:31], a[104:107], v[192:195]\n" \
"ds_read_b64_tr_b16 a[26:27], v33 offset:41536\n" \
"v_mfma_f32_16x16x32_bf16 v[196:199], a[28:31], a[120:123], v[196:199]\n" \
"ds_read_b64_tr_b16 a[28:29], v33 offset:46144\n" \
"ds_read_b64_tr_b16 a[30:31], v33 offset:50752\n" \
"s_waitcnt vmcnt(15)\n" \
"s_mov_b64 exec, 1\n" \
"buffer_store_dword v39, off, s[56:59], s72 offset:20 sc1\n" \
"s_mov_b64 exec, -1\n" \
"v_mfma_f32_16x16x32_bf16 v[192:195], a[192:195], a[108:111], v[192:195]\n" \
"v_readlane_b32 s72, v11, 41\n" \
"v_mfma_f32_16x16x32_bf16 v[196:199], a[192:195], a[124:127], v[196:199]\n" \
"ds_read_b64_tr_b16 a[192:193], v33 offset:55360\n" \
"ds_read_b64_tr_b16 a[194:195], v33 offset:59968\n" \
"v_mfma_f32_16x16x32_bf16 v[192:195], a[196:199], a[160:163], v[192:195]\n" \
"v_mfma_f32_16x16x32_bf16 v[196:199], a[196:199], a[176:179], v[196:199]\n" \
"ds_read_b64_tr_b16 a[196:197], v10 offset:64\n" \
"ds_read_b64_tr_b16 a[198:199], v10 offset:4672\n" \
"v_mfma_f32_16x16x32_bf16 v[192:195], a[200:203], a[164:167], v[192:195]\n" \
"v_mfma_f32_16x16x32_bf16 v[196:199], a[200:203], a[180:183], v[196:199]\n" \
"ds_read_b64_tr_b16 a[200:201], v34 offset:64\n" \
"ds_read_b64_tr_b16 a[202:203], v34 offset:4672\n" \
"v_mfma_f32_16x16x32_bf16 v[192:195], a[204:207], a[168:171], v[192:195]\n" \
"v_mfma_f32_16x16x32_bf16 v[196:199], a[204:207], a[184:187], v[196:199]\n" \
"ds_read_b64_tr_b16 a[204:205], v34 offset:9280\n" \
"ds_read_b64_tr_b16 a[206:207], v34 offset:13888\n" \
"v_mfma_f32_16x16x32_bf16 v[192:195], a[208:211], a[172:175], v[192:195]\n" \
"v_mfma_f32_16x16x32_bf16 v[196:199], a[208:211], a[188:191], v[196:199]\n" \
"ds_read_b64_tr_b16 a[208:209], v34 offset:18496\n" \
"ds_read_b64_tr_b16 a[210:211], v34 offset:23104\n" \
"v_mfma_f32_16x16x32_bf16 v[192:195], a[212:215], a[220:223], v[192:195]\n" \
"v_mfma_f32_16x16x32_bf16 v[196:199], a[212:215], a[236:239], v[196:199]\n" \
"ds_read_b64_tr_b16 a[212:213], v10 offset:9280\n" \
"ds_read_b64_tr_b16 a[214:215], v10 offset:13888\n" \
"v_mfma_f32_16x16x32_bf16 v[192:195], a[216:219], a[224:227], v[192:195]\n" \
"v_mfma_f32_16x16x32_bf16 v[196:199], a[216:219], a[240:243], v[196:199]\n" \
"ds_read_b64_tr_b16 a[216:217], v33 offset:96\n" \
"s_waitcnt lgkmcnt(13)\n" \
"v_mfma_f32_16x16x32_bf16 v[192:195], a[0:3], a[228:231], v[192:195]\n" \
"ds_read_b64_tr_b16 a[218:219], v33 offset:4704\n" \
"v_mfma_f32_16x16x32_bf16 v[196:199], a[0:3], a[244:247], v[196:199]\n" \
"ds_read_b64_tr_b16 a[0:1], v33 offset:9312\n" \
"ds_read_b64_tr_b16 a[2:3], v33 offset:13920\n" \
"s_waitcnt vmcnt(12)\n" \
"s_mov_b64 exec, 1\n" \
"buffer_store_dword v39, off, s[56:59], s73 offset:24 sc1\n" \
"s_mov_b64 exec, -1\n" \
"v_mfma_f32_16x16x32_bf16 v[192:195], a[4:7], a[232:235], v[192:195]\n" \
"v_readlane_b32 s73, v11, 50\n" \
"v_mfma_f32_16x16x32_bf16 v[196:199], a[4:7], a[248:251], v[196:199]\n" \
"ds_read_b64_tr_b16 a[4:5], v33 offset:18528\n" \
"ds_read_b64_tr_b16 a[6:7], v33 offset:23136\n" \
"v_mfma_f32_16x16x32_bf16 v[200:203], a[8:11], a[96:99], v[200:203]\n" \
"v_mfma_f32_16x16x32_bf16 v[204:207], a[8:11], a[112:115], v[204:207]\n" \
"ds_read_b64_tr_b16 a[8:9], v33 offset:27744\n" \
"ds_read_b64_tr_b16 a[10:11], v33 offset:32352\n" \
"v_cvt_pk_bf16_f32 v192, v192, v193\n" \
"v_cvt_pk_bf16_f32 v193, v194, v195\n" \
"v_cvt_pk_bf16_f32 v194, v196, v197\n" \
"v_cvt_pk_bf16_f32 v195, v198, v199\n" \
"buffer_store_dwordx4 v[192:195], v20, s[56:59], s95 offen offset:1024 sc1\n" \
"s_add_u32 s95, s95, 0x3000\n" \
"v_mfma_f32_16x16x32_bf16 v[200:203], a[12:15], a[100:103], v[200:203]\n" \
"v_mfma_f32_16x16x32_bf16 v[204:207], a[12:15], a[116:119], v[204:207]\n" \
"ds_read_b64_tr_b16 a[12:13], v33 offset:36960\n" \
"ds_read_b64_tr_b16 a[14:15], v33 offset:41568\n" \
"v_mfma_f32_16x16x32_bf16 v[200:203], a[16:19], a[104:107], v[200:203]\n" \
"v_mfma_f32_16x16x32_bf16 v[204:207], a[16:19], a[120:123], v[204:207]\n" \
"ds_read_b64_tr_b16 a[16:17], v33 offset:46176\n" \
"ds_read_b64_tr_b16 a[18:19], v33 offset:50784\n" \
"v_mfma_f32_16x16x32_bf16 v[200:203], a[20:23], a[108:111], v[200:203]\n" \
"v_mfma_f32_16x16x32_bf16 v[204:207], a[20:23], a[124:127], v[204:207]\n" \
"ds_read_b64_tr_b16 a[20:21], v33 offset:55392\n" \
"ds_read_b64_tr_b16 a[22:23], v33 offset:60000\n" \
"v_mfma_f32_16x16x32_bf16 v[200:203], a[24:27], a[160:163], v[200:203]\n" \
"v_mfma_f32_16x16x32_bf16 v[204:207], a[24:27], a[176:179], v[204:207]\n" \
"ds_read_b64_tr_b16 a[24:25], v10 offset:96\n" \
"ds_read_b64_tr_b16 a[26:27], v10 offset:4704\n" \
"v_mfma_f32_16x16x32_bf16 v[200:203], a[28:31], a[164:167], v[200:203]\n" \
"v_mfma_f32_16x16x32_bf16 v[204:207], a[28:31], a[180:183], v[204:207]\n" \
"ds_read_b64_tr_b16 a[28:29], v34 offset:96\n" \
"s_waitcnt lgkmcnt(12)\n" \
"v_mfma_f32_16x16x32_bf16 v[200:203], a[192:195], a[168:171], v[200:203]\n" \
"ds_read_b64_tr_b16 a[30:31], v34 offset:4704\n" \
"v_mfma_f32_16x16x32_bf16 v[204:207], a[192:195], a[184:187], v[204:207]\n" \
"ds_read_b64_tr_b16 a[192:193], v34 offset:9312\n" \
"ds_read_b64_tr_b16 a[194:195], v34 offset:13920\n" \
"v_mfma_f32_16x16x32_bf16 v[200:203], a[196:199], a[172:175], v[200:203]\n" \
"v_mfma_f32_16x16x32_bf16 v[204:207], a[196:199], a[188:191], v[204:207]\n" \
"ds_read_b64_tr_b16 a[196:197], v34 offset:18528\n" \
"ds_read_b64_tr_b16 a[198:199], v34 offset:23136\n" \
"v_mfma_f32_16x16x32_bf16 v[200:203], a[200:203], a[220:223], v[200:203]\n" \
"v_mfma_f32_16x16x32_bf16 v[204:207], a[200:203], a[236:239], v[204:207]\n" \
"ds_read_b64_tr_b16 a[200:201], v10 offset:9312\n" \
"ds_read_b64_tr_b16 a[202:203], v10 offset:13920\n" \
"v_mfma_f32_16x16x32_bf16 v[200:203], a[204:207], a[224:227], v[200:203]\n" \
"v_mfma_f32_16x16x32_bf16 v[204:207], a[204:207], a[240:243], v[204:207]\n" \
"ds_read_b64_tr_b16 a[204:205], v33 offset:128\n" \
"ds_read_b64_tr_b16 a[206:207], v33 offset:4736\n" \
"v_mfma_f32_16x16x32_bf16 v[200:203], a[208:211], a[228:231], v[200:203]\n" \
"v_mfma_f32_16x16x32_bf16 v[204:207], a[208:211], a[244:247], v[204:207]\n" \
"ds_read_b64_tr_b16 a[208:209], v33 offset:9344\n" \
"ds_read_b64_tr_b16 a[210:211], v33 offset:13952\n" \
"v_mfma_f32_16x16x32_bf16 v[200:203], a[212:215], a[232:235], v[200:203]\n" \
"v_mfma_f32_16x16x32_bf16 v[204:207], a[212:215], a[248:251], v[204:207]\n" \
"ds_read_b64_tr_b16 a[212:213], v33 offset:18560\n" \
"ds_read_b64_tr_b16 a[214:215], v33 offset:23168\n" \
"v_mfma_f32_16x16x32_bf16 v[208:211], a[216:219], a[96:99], v[208:211]\n" \
"v_mfma_f32_16x16x32_bf16 v[212:215], a[216:219], a[112:115], v[212:215]\n" \
"ds_read_b64_tr_b16 a[216:217], v33 offset:27776\n" \
"ds_read_b64_tr_b16 a[218:219], v33 offset:32384\n" \
"v_mfma_f32_16x16x32_bf16 v[208:211], a[0:3], a[100:103], v[208:211]\n" \
"v_cvt_pk_bf16_f32 v200, v200, v201\n" \
"v_cvt_pk_bf16_f32 v201, v202, v203\n" \
"v_cvt_pk_bf16_f32 v202, v204, v205\n" \
"v_cvt_pk_bf16_f32 v203, v206, v207\n" \
"buffer_store_dwordx4 v[200:203], v20, s[56:59], s96 offen offset:1024 sc1\n" \
"s_add_u32 s96, s96, 0x2800\n" \
"v_mfma_f32_16x16x32_bf16 v[212:215], a[0:3], a[116:119], v[212:215]\n" \
"ds_read_b64_tr_b16 a[0:1], v33 offset:36992\n" \
"s_waitcnt lgkmcnt(13)\n" \
"v_mfma_f32_16x16x32_bf16 v[208:211], a[4:7], a[104:107], v[208:211]\n" \
"ds_read_b64_tr_b16 a[2:3], v33 offset:41600\n" \
"v_mfma_f32_16x16x32_bf16 v[212:215], a[4:7], a[120:123], v[212:215]\n" \
"ds_read_b64_tr_b16 a[4:5], v33 offset:46208\n" \
"ds_read_b64_tr_b16 a[6:7], v33 offset:50816\n" \
"v_mfma_f32_16x16x32_bf16 v[208:211], a[8:11], a[108:111], v[208:211]\n" \
"v_mfma_f32_16x16x32_bf16 v[212:215], a[8:11], a[124:127], v[212:215]\n" \
"ds_read_b64_tr_b16 a[8:9], v33 offset:55424\n" \
"ds_read_b64_tr_b16 a[10:11], v33 offset:60032\n" \
"v_mfma_f32_16x16x32_bf16 v[208:211], a[12:15], a[160:163], v[208:211]\n" \
"v_mfma_f32_16x16x32_bf16 v[212:215], a[12:15], a[176:179], v[212:215]\n" \
"ds_read_b64_tr_b16 a[12:13], v10 offset:128\n" \
"ds_read_b64_tr_b16 a[14:15], v10 offset:4736\n" \
"v_mfma_f32_16x16x32_bf16 v[208:211], a[16:19], a[164:167], v[208:211]\n" \
"v_mfma_f32_16x16x32_bf16 v[212:215], a[16:19], a[180:183], v[212:215]\n" \
"ds_read_b64_tr_b16 a[16:17], v34 offset:128\n" \
"ds_read_b64_tr_b16 a[18:19], v34 offset:4736\n" \
"v_mfma_f32_16x16x32_bf16 v[208:211], a[20:23], a[168:171], v[208:211]\n" \
"v_mfma_f32_16x16x32_bf16 v[212:215], a[20:23], a[184:187], v[212:215]\n" \
"ds_read_b64_tr_b16 a[20:21], v34 offset:9344\n" \
"ds_read_b64_tr_b16 a[22:23], v34 offset:13952\n" \
"v_mfma_f32_16x16x32_bf16 v[208:211], a[24:27], a[172:175], v[208:211]\n" \
"v_mfma_f32_16x16x32_bf16 v[212:215], a[24:27], a[188:191], v[212:215]\n" \
"ds_read_b64_tr_b16 a[24:25], v34 offset:18560\n" \
"ds_read_b64_tr_b16 a[26:27], v34 offset:23168\n" \
"v_mfma_f32_16x16x32_bf16 v[208:211], a[28:31], a[220:223], v[208:211]\n" \
"v_mfma_f32_16x16x32_bf16 v[212:215], a[28:31], a[236:239], v[212:215]\n" \
"ds_read_b64_tr_b16 a[28:29], v10 offset:9344\n" \
"ds_read_b64_tr_b16 a[30:31], v10 offset:13952\n" \
"s_waitcnt vmcnt(6)\n" \
"s_mov_b64 exec, 3\n" \
"buffer_store_dword v39, v41, s[56:59], s93 offen sc1\n" \
"s_mov_b64 exec, -1\n" \
"v_mfma_f32_16x16x32_bf16 v[208:211], a[192:195], a[224:227], v[208:211]\n" \
"v_mfma_f32_16x16x32_bf16 v[212:215], a[192:195], a[240:243], v[212:215]\n" \
"ds_read_b64_tr_b16 a[192:193], v33 offset:160\n" \
"s_waitcnt lgkmcnt(13)\n" \
"v_mfma_f32_16x16x32_bf16 v[208:211], a[196:199], a[228:231], v[208:211]\n" \
"ds_read_b64_tr_b16 a[194:195], v33 offset:4768\n" \
"v_mfma_f32_16x16x32_bf16 v[212:215], a[196:199], a[244:247], v[212:215]\n" \
"ds_read_b64_tr_b16 a[196:197], v33 offset:9376\n" \
"ds_read_b64_tr_b16 a[198:199], v33 offset:13984\n" \
"v_mfma_f32_16x16x32_bf16 v[208:211], a[200:203], a[232:235], v[208:211]\n" \
"v_mfma_f32_16x16x32_bf16 v[212:215], a[200:203], a[248:251], v[212:215]\n" \
"ds_read_b64_tr_b16 a[200:201], v33 offset:18592\n" \
"ds_read_b64_tr_b16 a[202:203], v33 offset:23200\n" \
"v_mfma_f32_16x16x32_bf16 v[216:219], a[204:207], a[96:99], v[216:219]\n" \
"v_mfma_f32_16x16x32_bf16 v[220:223], a[204:207], a[112:115], v[220:223]\n" \
"ds_read_b64_tr_b16 a[204:205], v33 offset:27808\n" \
"ds_read_b64_tr_b16 a[206:207], v33 offset:32416\n" \
"v_mfma_f32_16x16x32_bf16 v[216:219], a[208:211], a[100:103], v[216:219]\n" \
"v_cvt_pk_bf16_f32 v208, v208, v209\n" \
"v_cvt_pk_bf16_f32 v209, v210, v211\n" \
"v_cvt_pk_bf16_f32 v210, v212, v213\n" \
"v_cvt_pk_bf16_f32 v211, v214, v215\n" \
"buffer_store_dwordx4 v[208:211], v20, s[56:59], s97 offen offset:1024 sc1\n" \
"s_add_u32 s97, s97, 0x2000\n" \
"v_mfma_f32_16x16x32_bf16 v[220:223], a[208:211], a[116:119], v[220:223]\n" \
"ds_read_b64_tr_b16 a[208:209], v33 offset:37024\n" \
"ds_read_b64_tr_b16 a[210:211], v33 offset:41632\n" \
"v_mfma_f32_16x16x32_bf16 v[216:219], a[212:215], a[104:107], v[216:219]\n" \
"v_mfma_f32_16x16x32_bf16 v[220:223], a[212:215], a[120:123], v[220:223]\n" \
"ds_read_b64_tr_b16 a[212:213], v33 offset:46240\n" \
"ds_read_b64_tr_b16 a[214:215], v33 offset:50848\n" \
"v_mfma_f32_16x16x32_bf16 v[216:219], a[216:219], a[108:111], v[216:219]\n" \
"v_mfma_f32_16x16x32_bf16 v[220:223], a[216:219], a[124:127], v[220:223]\n" \
"ds_read_b64_tr_b16 a[216:217], v33 offset:55456\n" \
"ds_read_b64_tr_b16 a[218:219], v33 offset:60064\n" \
"v_mfma_f32_16x16x32_bf16 v[216:219], a[0:3], a[160:163], v[216:219]\n" \
"v_mfma_f32_16x16x32_bf16 v[220:223], a[0:3], a[176:179], v[220:223]\n" \
"ds_read_b64_tr_b16 a[0:1], v10 offset:160\n" \
"ds_read_b64_tr_b16 a[2:3], v10 offset:4768\n" \
"v_mfma_f32_16x16x32_bf16 v[216:219], a[4:7], a[164:167], v[216:219]\n" \
"v_mfma_f32_16x16x32_bf16 v[220:223], a[4:7], a[180:183], v[220:223]\n" \
"ds_read_b64_tr_b16 a[4:5], v34 offset:160\n" \
"s_waitcnt lgkmcnt(13)\n" \
"v_mfma_f32_16x16x32_bf16 v[216:219], a[8:11], a[168:171], v[216:219]\n" \
"ds_read_b64_tr_b16 a[6:7], v34 offset:4768\n" \
"v_mfma_f32_16x16x32_bf16 v[220:223], a[8:11], a[184:187], v[220:223]\n" \
"ds_read_b64_tr_b16 a[8:9], v34 offset:9376\n" \
"ds_read_b64_tr_b16 a[10:11], v34 offset:13984\n" \
"v_mfma_f32_16x16x32_bf16 v[216:219], a[12:15], a[172:175], v[216:219]\n" \
"v_mfma_f32_16x16x32_bf16 v[220:223], a[12:15], a[188:191], v[220:223]\n" \
"ds_read_b64_tr_b16 a[12:13], v34 offset:18592\n" \
"ds_read_b64_tr_b16 a[14:15], v34 offset:23200\n" \
"v_mfma_f32_16x16x32_bf16 v[216:219], a[16:19], a[220:223], v[216:219]\n" \
"s_waitcnt vmcnt(6)\n" \
"s_mov_b64 exec, 1\n" \
"buffer_store_dword v39, off, s[56:59], s94 offset:32 sc1\n" \
"s_mov_b64 exec, -1\n" \
"v_mfma_f32_16x16x32_bf16 v[220:223], a[16:19], a[236:239], v[220:223]\n" \
"ds_read_b64_tr_b16 a[16:17], v10 offset:9376\n" \
"ds_read_b64_tr_b16 a[18:19], v10 offset:13984\n" \
"v_mfma_f32_16x16x32_bf16 v[216:219], a[20:23], a[224:227], v[216:219]\n" \
"v_mfma_f32_16x16x32_bf16 v[220:223], a[20:23], a[240:243], v[220:223]\n" \
"ds_read_b64_tr_b16 a[20:21], v33 offset:192\n" \
"ds_read_b64_tr_b16 a[22:23], v33 offset:4800\n" \
"v_mfma_f32_16x16x32_bf16 v[216:219], a[24:27], a[228:231], v[216:219]\n" \
"v_mfma_f32_16x16x32_bf16 v[220:223], a[24:27], a[244:247], v[220:223]\n" \
"ds_read_b64_tr_b16 a[24:25], v33 offset:9408\n" \
"ds_read_b64_tr_b16 a[26:27], v33 offset:14016\n" \
"v_mfma_f32_16x16x32_bf16 v[216:219], a[28:31], a[232:235], v[216:219]\n" \
"v_mfma_f32_16x16x32_bf16 v[220:223], a[28:31], a[248:251], v[220:223]\n" \
"buffer_load_dword v37, v40, s[56:59], s74 offen sc1\n" \
"v_mfma_f32_16x16x32_bf16 v[224:227], a[192:195], a[96:99], v[224:227]\n" \
"ds_read_b64_tr_b16 a[28:29], v33 offset:18624\n" \
"ds_read_b64_tr_b16 a[30:31], v33 offset:23232\n" \
"v_mfma_f32_16x16x32_bf16 v[228:231], a[192:195], a[112:115], v[228:231]\n" \
"ds_read_b64_tr_b16 a[192:193], v33 offset:27840\n" \
"ds_read_b64_tr_b16 a[194:195], v33 offset:32448\n" \
"v_cvt_pk_bf16_f32 v216, v216, v217\n" \
"v_cvt_pk_bf16_f32 v217, v218, v219\n" \
"v_cvt_pk_bf16_f32 v218, v220, v221\n" \
"v_cvt_pk_bf16_f32 v219, v222, v223\n" \
"buffer_store_dwordx4 v[216:219], v20, s[56:59], s64 offen offset:1024 sc1\n" \
"s_add_u32 s64, s64, 0x1800\n" \
"v_mfma_f32_16x16x32_bf16 v[224:227], a[196:199], a[100:103], v[224:227]\n" \
"v_mfma_f32_16x16x32_bf16 v[228:231], a[196:199], a[116:119], v[228:231]\n" \
"ds_read_b64_tr_b16 a[196:197], v33 offset:37056\n" \
"s_waitcnt lgkmcnt(11)\n" \
"v_mfma_f32_16x16x32_bf16 v[224:227], a[200:203], a[104:107], v[224:227]\n" \
"ds_read_b64_tr_b16 a[198:199], v33 offset:41664\n" \
"v_mfma_f32_16x16x32_bf16 v[228:231], a[200:203], a[120:123], v[228:231]\n" \
"ds_read_b64_tr_b16 a[200:201], v33 offset:46272\n" \
"ds_read_b64_tr_b16 a[202:203], v33 offset:50880\n" \
"v_mfma_f32_16x16x32_bf16 v[224:227], a[204:207], a[108:111], v[224:227]\n" \
"v_mfma_f32_16x16x32_bf16 v[228:231], a[204:207], a[124:127], v[228:231]\n" \
"ds_read_b64_tr_b16 a[204:205], v33 offset:55488\n" \
"ds_read_b64_tr_b16 a[206:207], v33 offset:60096\n" \
"v_mfma_f32_16x16x32_bf16 v[224:227], a[208:211], a[160:163], v[224:227]\n" \
"v_mfma_f32_16x16x32_bf16 v[228:231], a[208:211], a[176:179], v[228:231]\n" \
"ds_read_b64_tr_b16 a[208:209], v10 offset:192\n" \
"ds_read_b64_tr_b16 a[210:211], v10 offset:4800\n" \
"v_mfma_f32_16x16x32_bf16 v[224:227], a[212:215], a[164:167], v[224:227]\n" \
"v_mfma_f32_16x16x32_bf16 v[228:231], a[212:215], a[180:183], v[228:231]\n" \
"ds_read_b64_tr_b16 a[212:213], v34 offset:192\n" \
"ds_read_b64_tr_b16 a[214:215], v34 offset:4800\n" \
"v_mfma_f32_16x16x32_bf16 v[224:227], a[216:219], a[168:171], v[224:227]\n" \
"v_mfma_f32_16x16x32_bf16 v[228:231], a[216:219], a[184:187], v[228:231]\n" \
"ds_read_b64_tr_b16 a[216:217], v34 offset:9408\n" \
"ds_read_b64_tr_b16 a[218:219], v34 offset:14016\n" \
"v_mfma_f32_16x16x32_bf16 v[224:227], a[0:3], a[172:175], v[224:227]\n" \
"v_mfma_f32_16x16x32_bf16 v[228:231], a[0:3], a[188:191], v[228:231]\n" \
"ds_read_b64_tr_b16 a[0:1], v34 offset:18624\n" \
"ds_read_b64_tr_b16 a[2:3], v34 offset:23232\n" \
"v_mfma_f32_16x16x32_bf16 v[224:227], a[4:7], a[220:223], v[224:227]\n" \
"v_mfma_f32_16x16x32_bf16 v[228:231], a[4:7], a[236:239], v[228:231]\n" \
"ds_read_b64_tr_b16 a[4:5], v10 offset:9408\n" \
"ds_read_b64_tr_b16 a[6:7], v10 offset:14016\n" \
"s_waitcnt vmcnt(6)\n" \
"s_mov_b64 exec, 1\n" \
"buffer_store_dword v39, off, s[56:59], s95 offset:36 sc1\n" \
"s_mov_b64 exec, -1\n" \
"v_mfma_f32_16x16x32_bf16 v[224:227], a[8:11], a[224:227], v[224:227]\n" \
"v_mfma_f32_16x16x32_bf16 v[228:231], a[8:11], a[240:243], v[228:231]\n" \
"ds_read_b64_tr_b16 a[8:9], v33 offset:224\n" \
"ds_read_b64_tr_b16 a[10:11], v33 offset:4832\n" \
"v_mfma_f32_16x16x32_bf16 v[224:227], a[12:15], a[228:231], v[224:227]\n" \
"v_mfma_f32_16x16x32_bf16 v[228:231], a[12:15], a[244:247], v[228:231]\n" \
"ds_read_b64_tr_b16 a[12:13], v33 offset:9440\n" \
"s_waitcnt lgkmcnt(13)\n" \
"v_mfma_f32_16x16x32_bf16 v[224:227], a[16:19], a[232:235], v[224:227]\n" \
"ds_read_b64_tr_b16 a[14:15], v33 offset:14048\n" \
"v_mfma_f32_16x16x32_bf16 v[228:231], a[16:19], a[248:251], v[228:231]\n" \
"ds_read_b64_tr_b16 a[16:17], v33 offset:18656\n" \
"ds_read_b64_tr_b16 a[18:19], v33 offset:23264\n" \
"v_mfma_f32_16x16x32_bf16 v[232:235], a[20:23], a[96:99], v[232:235]\n" \
"v_mfma_f32_16x16x32_bf16 v[236:239], a[20:23], a[112:115], v[236:239]\n" \
"ds_read_b64_tr_b16 a[20:21], v33 offset:27872\n" \
"ds_read_b64_tr_b16 a[22:23], v33 offset:32480\n" \
"v_cvt_pk_bf16_f32 v224, v224, v225\n" \
"v_cvt_pk_bf16_f32 v225, v226, v227\n" \
"v_cvt_pk_bf16_f32 v226, v228, v229\n" \
"v_cvt_pk_bf16_f32 v227, v230, v231\n" \
"buffer_store_dwordx4 v[224:227], v20, s[56:59], s73 offen offset:1024 sc1\n" \
"v_mfma_f32_16x16x32_bf16 v[232:235], a[24:27], a[100:103], v[232:235]\n" \
"v_mfma_f32_16x16x32_bf16 v[236:239], a[24:27], a[116:119], v[236:239]\n" \
"ds_read_b64_tr_b16 a[24:25], v33 offset:37088\n" \
"ds_read_b64_tr_b16 a[26:27], v33 offset:41696\n" \
"v_mfma_f32_16x16x32_bf16 v[232:235], a[28:31], a[104:107], v[232:235]\n" \
"v_mfma_f32_16x16x32_bf16 v[236:239], a[28:31], a[120:123], v[236:239]\n" \
"ds_read_b64_tr_b16 a[28:29], v33 offset:46304\n" \
"ds_read_b64_tr_b16 a[30:31], v33 offset:50912\n" \
"v_mfma_f32_16x16x32_bf16 v[232:235], a[192:195], a[108:111], v[232:235]\n" \
"v_mfma_f32_16x16x32_bf16 v[236:239], a[192:195], a[124:127], v[236:239]\n" \
"ds_read_b64_tr_b16 a[192:193], v33 offset:55520\n" \
"ds_read_b64_tr_b16 a[194:195], v33 offset:60128\n" \
"v_mfma_f32_16x16x32_bf16 v[232:235], a[196:199], a[160:163], v[232:235]\n" \
"v_mfma_f32_16x16x32_bf16 v[236:239], a[196:199], a[176:179], v[236:239]\n" \
"ds_read_b64_tr_b16 a[196:197], v10 offset:224\n" \
"ds_read_b64_tr_b16 a[198:199], v10 offset:4832\n" \
"v_mfma_f32_16x16x32_bf16 v[232:235], a[200:203], a[164:167], v[232:235]\n" \
"v_mfma_f32_16x16x32_bf16 v[236:239], a[200:203], a[180:183], v[236:239]\n" \
"ds_read_b64_tr_b16 a[200:201], v34 offset:224\n" \
"ds_read_b64_tr_b16 a[202:203], v34 offset:4832\n" \
"v_mfma_f32_16x16x32_bf16 v[232:235], a[204:207], a[168:171], v[232:235]\n" \
"v_mfma_f32_16x16x32_bf16 v[236:239], a[204:207], a[184:187], v[236:239]\n" \
"ds_read_b64_tr_b16 a[204:205], v34 offset:9440\n" \
"s_waitcnt lgkmcnt(13)\n" \
"v_mfma_f32_16x16x32_bf16 v[232:235], a[208:211], a[172:175], v[232:235]\n" \
"ds_read_b64_tr_b16 a[206:207], v34 offset:14048\n" \
"v_mfma_f32_16x16x32_bf16 v[236:239], a[208:211], a[188:191], v[236:239]\n" \
"ds_read_b64_tr_b16 a[208:209], v34 offset:18656\n" \
"ds_read_b64_tr_b16 a[210:211], v34 offset:23264\n" \
"v_mfma_f32_16x16x32_bf16 v[232:235], a[212:215], a[220:223], v[232:235]\n" \
"v_mfma_f32_16x16x32_bf16 v[236:239], a[212:215], a[236:239], v[236:239]\n" \
"ds_read_b64_tr_b16 a[212:213], v10 offset:9440\n" \
"ds_read_b64_tr_b16 a[214:215], v10 offset:14048\n" \
"s_waitcnt vmcnt(7)\n" \
"s_mov_b64 exec, 1\n" \
"buffer_store_dword v39, off, s[56:59], s96 offset:40 sc1\n" \
"s_mov_b64 exec, -1\n" \
"v_mfma_f32_16x16x32_bf16 v[232:235], a[216:219], a[224:227], v[232:235]\n" \
"v_mfma_f32_16x16x32_bf16 v[236:239], a[216:219], a[240:243], v[236:239]\n" \
"ds_read_b64_tr_b16 a[216:217], v36 offset:0\n" \
"ds_read_b64_tr_b16 a[218:219], v36 offset:1024\n" \
"v_mfma_f32_16x16x32_bf16 v[232:235], a[0:3], a[228:231], v[232:235]\n" \
"v_mfma_f32_16x16x32_bf16 v[236:239], a[0:3], a[244:247], v[236:239]\n" \
"ds_read_b64_tr_b16 a[0:1], v36 offset:2048\n" \
"ds_read_b64_tr_b16 a[2:3], v36 offset:3072\n" \
"v_mfma_f32_16x16x32_bf16 v[232:235], a[4:7], a[232:235], v[232:235]\n" \
"v_mfma_f32_16x16x32_bf16 v[236:239], a[4:7], a[248:251], v[236:239]\n" \
"ds_read_b64_tr_b16 a[4:5], v36 offset:32\n" \
"ds_read_b64_tr_b16 a[6:7], v36 offset:1056\n" \
"v_mfma_f32_16x16x32_bf16 v[240:243], a[8:11], a[96:99], v[240:243]\n" \
"v_mfma_f32_16x16x32_bf16 v[244:247], a[8:11], a[112:115], v[244:247]\n" \
"ds_read_b64_tr_b16 a[8:9], v36 offset:2080\n" \
"ds_read_b64_tr_b16 a[10:11], v36 offset:3104\n" \
"v_mfma_f32_16x16x32_bf16 v[240:243], a[12:15], a[100:103], v[240:243]\n" \
"v_cvt_pk_bf16_f32 v232, v232, v233\n" \
"v_cvt_pk_bf16_f32 v233, v234, v235\n" \
"v_cvt_pk_bf16_f32 v234, v236, v237\n" \
"v_cvt_pk_bf16_f32 v235, v238, v239\n" \
"buffer_store_dwordx4 v[232:235], v20, s[56:59], s92 offen offset:1024 sc1\n" \
"s_waitcnt vmcnt(5)\n" \
"v_cmp_eq_u32_e64 s[98:99], s62, v37\n" \
"s_mov_b64 exec, 1\n" \
"buffer_store_dword v39, off, s[56:59], s97 offset:44 sc1\n" \
"s_mov_b64 exec, -1\n" \
"v_mfma_f32_16x16x32_bf16 v[244:247], a[12:15], a[116:119], v[244:247]\n" \
"s_or_b32 s75, 0x7f, s100\n" \
"s_andn2_b32 s75, s75, s98\n" \
"s_cbranch_scc1 L_sx0_%=\n" \
"L_rx0_%=:\n" \
"buffer_load_dword v37, v40, s[56:59], s74 offen sc1\n" \
"s_add_u32 s65, s63, 0x0\n" \
"s_add_u32 s66, s63, 0x800\n" \
"s_add_u32 s67, s63, 0x1000\n" \
"s_add_u32 s68, s63, 0x1800\n" \
"s_add_u32 s69, s63, 0x2000\n" \
"s_add_u32 s70, s63, 0x2800\n" \
"s_add_u32 s71, s63, 0x3000\n" \
"s_add_u32 s73, s73, 0x1000\n" \
"s_add_u32 s92, s92, 0x800\n" \
"v_mfma_f32_16x16x32_bf16 v[240:243], a[16:19], a[104:107], v[240:243]\n" \
"v_mfma_f32_16x16x32_bf16 v[244:247], a[16:19], a[120:123], v[244:247]\n" \
"buffer_load_dwordx4 v[116:119], v20, s[56:59], s65 offen offset:0 sc1\n" \
"s_waitcnt lgkmcnt(5)\n" \
"v_mfma_f32_16x16x32_bf16 v[240:243], a[20:23], a[108:111], v[240:243]\n" \
"v_mfma_f32_16x16x32_bf16 v[244:247], a[20:23], a[124:127], v[244:247]\n" \
"v_mfma_f32_16x16x32_bf16 v[168:171], a[24:27], a[128:131], v[168:171]\n" \
"v_mfma_f32_16x16x32_bf16 v[172:175], a[24:27], a[144:147], v[172:175]\n" \
"buffer_load_dwordx4 v[124:127], v20, s[56:59], s66 offen offset:0 sc1\n" \
"v_mfma_f32_16x16x32_bf16 v[240:243], a[24:27], a[160:163], v[240:243]\n" \
"v_readlane_b32 s66, v11, 53\n" \
"v_mfma_f32_16x16x32_bf16 v[244:247], a[24:27], a[176:179], v[244:247]\n" \
"v_mfma_f32_16x16x32_bf16 v[168:171], a[28:31], a[132:135], v[168:171]\n" \
"v_mfma_f32_16x16x32_bf16 v[172:175], a[28:31], a[148:151], v[172:175]\n" \
"buffer_load_dwordx4 v[132:135], v20, s[56:59], s67 offen offset:0 sc1\n" \
"v_mfma_f32_16x16x32_bf16 v[240:243], a[28:31], a[164:167], v[240:243]\n" \
"v_readlane_b32 s67, v11, 44\n" \
"v_mfma_f32_16x16x32_bf16 v[244:247], a[28:31], a[180:183], v[244:247]\n" \
"v_mfma_f32_16x16x32_bf16 v[168:171], a[192:195], a[136:139], v[168:171]\n" \
"v_mfma_f32_16x16x32_bf16 v[172:175], a[192:195], a[152:155], v[172:175]\n" \
"buffer_load_dwordx4 v[140:143], v20, s[56:59], s68 offen offset:0 sc1\n" \
"v_mfma_f32_16x16x32_bf16 v[240:243], a[192:195], a[168:171], v[240:243]\n" \
"v_readlane_b32 s68, v11, 42\n" \
"v_mfma_f32_16x16x32_bf16 v[244:247], a[192:195], a[184:187], v[244:247]\n" \
"v_mfma_f32_16x16x32_bf16 v[168:171], a[196:199], a[140:143], v[168:171]\n" \
"v_mfma_f32_16x16x32_bf16 v[172:175], a[196:199], a[156:159], v[172:175]\n" \
"buffer_load_dwordx4 v[148:151], v20, s[56:59], s69 offen offset:0 sc1\n" \
"v_mfma_f32_16x16x32_bf16 v[240:243], a[196:199], a[172:175], v[240:243]\n" \
"v_readlane_b32 s69, v11, 43\n" \
"v_mfma_f32_16x16x32_bf16 v[244:247], a[196:199], a[188:191], v[244:247]\n" \
"v_mfma_f32_16x16x32_bf16 v[168:171], a[200:203], a[32:35], v[168:171]\n" \
"v_mfma_f32_16x16x32_bf16 v[172:175], a[200:203], a[48:51], v[172:175]\n" \
"buffer_load_dwordx4 v[156:159], v20, s[56:59], s70 offen offset:0 sc1\n" \
"v_mfma_f32_16x16x32_bf16 v[240:243], a[200:203], a[220:223], v[240:243]\n" \
"v_mfma_f32_16x16x32_bf16 v[244:247], a[200:203], a[236:239], v[244:247]\n" \
"v_mfma_f32_16x16x32_bf16 v[168:171], a[204:207], a[36:39], v[168:171]\n" \
"v_mfma_f32_16x16x32_bf16 v[172:175], a[204:207], a[52:55], v[172:175]\n" \
"buffer_load_dwordx4 v[164:167], v20, s[56:59], s71 offen offset:0 sc1\n" \
"v_mfma_f32_16x16x32_bf16 v[240:243], a[204:207], a[224:227], v[240:243]\n" \
"v_mfma_f32_16x16x32_bf16 v[244:247], a[204:207], a[240:243], v[244:247]\n" \
"v_mfma_f32_16x16x32_bf16 v[168:171], a[208:211], a[40:43], v[168:171]\n" \
"v_mfma_f32_16x16x32_bf16 v[172:175], a[208:211], a[56:59], v[172:175]\n" \
"buffer_load_dwordx4 v[112:115], v20, s[56:59], s72 offen offset:0 sc1\n" \
"v_mfma_f32_16x16x32_bf16 v[240:243], a[208:211], a[228:231], v[240:243]\n" \
"v_mfma_f32_16x16x32_bf16 v[244:247], a[208:211], a[244:247], v[244:247]\n" \
"v_mfma_f32_16x16x32_bf16 v[168:171], a[212:215], a[44:47], v[168:171]\n" \
"v_mfma_f32_16x16x32_bf16 v[172:175], a[212:215], a[60:63], v[172:175]\n" \
"buffer_load_dwordx4 v[184:187], v20, s[56:59], s72 offen offset:2048 sc1\n" \
"s_waitcnt vmcnt(15)\n" \
"s_mov_b64 exec, 1\n" \
"buffer_store_dword v39, off, s[56:59], s64 offset:48 sc1\n" \
"s_mov_b64 exec, -1\n" \
"s_add_u32 s72, s72, 0x1000\n" \
"v_mfma_f32_16x16x32_bf16 v[240:243], a[212:215], a[232:235], v[240:243]\n" \
"v_mfma_f32_16x16x32_bf16 v[244:247], a[212:215], a[248:251], v[244:247]\n" \
"v_mfma_f32_16x16x32_bf16 v[168:171], a[216:219], v[64:67], v[168:171]\n" \
"v_mfma_f32_16x16x32_bf16 v[172:175], a[216:219], v[72:75], v[172:175]\n" \
"buffer_load_dwordx4 v[120:123], v20, s[56:59], s72 offen offset:0 sc1\n" \
"v_mfma_f32_16x16x32_bf16 v[240:243], a[216:219], v[80:83], v[240:243]\n" \
".fill (L_ab + 32768 - .) / 4, 4, 0xBF800000\n" \
"L_ap8:\n" \
"s_cbranch_execz L_ap0\n" \
"v_mfma_f32_16x16x32_bf16 v[244:247], a[216:219], v[88:91], v[244:247]\n" \
"s_waitcnt lgkmcnt(0)\n" \
"v_mfma_f32_16x16x32_bf16 v[168:171], a[0:3], v[68:71], v[168:171]\n" \
"v_mfma_f32_16x16x32_bf16 v[172:175], a[0:3], v[76:79], v[172:175]\n" \
"buffer_load_dwordx4 v[192:195], v20, s[56:59], s72 offen offset:2048 sc1\n" \
"s_add_u32 s72, s72, 0x1000\n" \
"v_mfma_f32_16x16x32_bf16 v[240:243], a[0:3], v[84:87], v[240:243]\n" \
"v_mfma_f32_16x16x32_bf16 v[244:247], a[0:3], v[92:95], v[244:247]\n" \
"v_mfma_f32_16x16x32_bf16 v[56:59], a[4:7], v[96:99], 0\n" \
"v_mfma_f32_16x16x32_bf16 v[60:63], a[4:7], v[104:107], 0\n" \
"buffer_load_dwordx4 v[128:131], v20, s[56:59], s72 offen offset:0 sc1\n" \
"v_mfma_f32_16x16x32_bf16 v[56:59], a[8:11], v[100:103], v[56:59]\n" \
"v_mfma_f32_16x16x32_bf16 v[60:63], a[8:11], v[108:111], v[60:63]\n" \
"buffer_load_dwordx4 v[200:203], v20, s[56:59], s72 offen offset:2048 sc1\n" \
"s_add_u32 s72, s72, 0x1000\n" \
"buffer_load_dwordx4 v[176:179], v20, s[56:59], s72 offen offset:0 sc1\n" \
"buffer_load_dwordx4 v[248:251], v20, s[56:59], s72 offen offset:2048 sc1\n" \
"s_waitcnt vmcnt(15)\n" \
"v_mfma_f32_16x16x32_bf16 v[168:171], v[50:53], v[116:119], v[168:171]\n" \
"s_mov_b64 exec, 1\n" \
"buffer_store_dword v39, off, s[56:59], s73 offset:52 sc1\n" \
"s_mov_b64 exec, -1\n" \
"s_mov_b64 exec, 1\n" \
"buffer_store_dword v39, off, s[56:59], s92 offset:56 sc1\n" \
"s_mov_b64 exec, -1\n" \
"v_cmp_eq_u32_e64 s[98:99], s62, v37\n" \
"v_mfma_f32_16x16x32_bf16 v[172:175], v[48:51], v[116:119], v[172:175]\n" \
"s_andn2_b32 s75, 0x100, s98\n" \
"s_cbranch_scc1 L_sx1_%=\n" \
"L_rx1_%=:\n" \
"s_add_u32 s93, s63, 0x0\n" \
"s_waitcnt vmcnt(16)\n" \
"v_mfma_f32_16x16x32_bf16 v[168:171], v[50:53], v[124:127], v[168:171]\n" \
"v_mfma_f32_16x16x32_bf16 v[172:175], v[48:51], v[124:127], v[172:175]\n" \
"buffer_load_dwordx4 v[188:191], v20, s[56:59], s93 offen offset:1024 sc1\n" \
"s_andn2_b32 s75, 0x200, s98\n" \
"s_cbranch_scc1 L_sx2_%=\n" \
"L_rx2_%=:\n" \
"s_add_u32 s94, s63, 0x800\n" \
"s_waitcnt vmcnt(16)\n" \
"v_mfma_f32_16x16x32_bf16 v[168:171], v[50:53], v[132:135], v[168:171]\n" \
"v_mfma_f32_16x16x32_bf16 v[172:175], v[48:51], v[132:135], v[172:175]\n" \
"buffer_load_dwordx4 v[196:199], v20, s[56:59], s94 offen offset:1024 sc1\n" \
"s_andn2_b32 s75, 0x400, s98\n" \
"s_cbranch_scc1 L_sx3_%=\n" \
"L_rx3_%=:\n" \
"s_add_u32 s95, s63, 0x1000\n" \
"s_waitcnt vmcnt(16)\n" \
"v_mfma_f32_16x16x32_bf16 v[168:171], v[50:53], v[140:143], v[168:171]\n" \
"v_mfma_f32_16x16x32_bf16 v[172:175], v[48:51], v[140:143], v[172:175]\n" \
"buffer_load_dwordx4 v[204:207], v20, s[56:59], s95 offen offset:1024 sc1\n" \
"s_andn2_b32 s75, 0x800, s98\n" \
"s_cbranch_scc1 L_sx4_%=\n" \
"L_rx4_%=:\n" \
"s_add_u32 s96, s63, 0x1800\n" \
"s_waitcnt vmcnt(16)\n" \
"v_mfma_f32_16x16x32_bf16 v[168:171], v[50:53], v[148:151], v[168:171]\n" \
"v_mfma_f32_16x16x32_bf16 v[172:175], v[48:51], v[148:151], v[172:175]\n" \
"buffer_load_dwordx4 v[212:215], v20, s[56:59], s96 offen offset:1024 sc1\n" \
"s_andn2_b32 s75, 0x1000, s98\n" \
"s_cbranch_scc1 L_sx5_%=\n" \
"L_rx5_%=:\n" \
"s_add_u32 s97, s63, 0x2000\n" \
"s_waitcnt vmcnt(16)\n" \
"v_mfma_f32_16x16x32_bf16 v[168:171], v[50:53], v[156:159], v[168:171]\n" \
"v_mfma_f32_16x16x32_bf16 v[172:175], v[48:51], v[156:159], v[172:175]\n" \
"buffer_load_dwordx4 v[220:223], v20, s[56:59], s97 offen offset:1024 sc1\n" \
"s_andn2_b32 s75, 0x2000, s98\n" \
"s_cbranch_scc1 L_sx6_%=\n" \
"L_rx6_%=:\n" \
"s_add_u32 s64, s63, 0x2800\n" \
"s_waitcnt vmcnt(16)\n" \
"v_mfma_f32_16x16x32_bf16 v[168:171], v[50:53], v[164:167], v[168:171]\n" \
"v_mfma_f32_16x16x32_bf16 v[172:175], v[48:51], v[164:167], v[172:175]\n" \
"buffer_load_dwordx4 v[228:231], v20, s[56:59], s64 offen offset:1024 sc1\n" \
"s_andn2_b32 s75, 0x4000, s98\n" \
"s_cbranch_scc1 L_sx7_%=\n" \
"L_rx7_%=:\n" \
"s_add_u32 s65, s63, 0x3000\n" \
"s_waitcnt vmcnt(16)\n" \
"v_mfma_f32_16x16x32_bf16 v[56:59], v[50:53], v[112:115], v[56:59]\n" \
"v_mfma_f32_16x16x32_bf16 v[60:63], v[48:51], v[112:115], v[60:63]\n" \
"buffer_load_dwordx4 v[236:239], v20, s[56:59], s65 offen offset:1024 sc1\n" \
"v_cvt_pk_bf16_f32 v168, v168, v169\n" \
"v_cvt_pk_bf16_f32 v172, v172, v173\n" \
"v_cvt_pk_bf16_f32 v169, v170, v171\n" \
"v_cvt_pk_bf16_f32 v173, v174, v175\n" \
"buffer_store_dwordx2 v[168:169], v42, s[88:91], s68 offen sc1\n" \
"buffer_store_dwordx2 v[172:173], v43, s[88:91], s68 offen sc1\n" \
"s_waitcnt vmcnt(18)\n" \
"v_mfma_f32_16x16x32_bf16 v[56:59], v[50:53], v[184:187], v[56:59]\n" \
"v_mfma_f32_16x16x32_bf16 v[60:63], v[48:51], v[184:187], v[60:63]\n" \
"s_waitcnt vmcnt(16)\n" \
"v_mfma_f32_16x16x32_bf16 v[56:59], v[50:53], v[120:123], v[56:59]\n" \
"v_mfma_f32_16x16x32_bf16 v[60:63], v[48:51], v[120:123], v[60:63]\n" \
"s_waitcnt vmcnt(15)\n" \
"v_mfma_f32_16x16x32_bf16 v[56:59], v[50:53], v[192:195], v[56:59]\n" \
"v_mfma_f32_16x16x32_bf16 v[60:63], v[48:51], v[192:195], v[60:63]\n" \
"s_waitcnt vmcnt(14)\n" \
"v_mfma_f32_16x16x32_bf16 v[56:59], v[50:53], v[128:131], v[56:59]\n" \
"v_mfma_f32_16x16x32_bf16 v[60:63], v[48:51], v[128:131], v[60:63]\n" \
"s_waitcnt vmcnt(13)\n" \
"v_mfma_f32_16x16x32_bf16 v[56:59], v[50:53], v[200:203], v[56:59]\n" \
"v_mfma_f32_16x16x32_bf16 v[60:63], v[48:51], v[200:203], v[60:63]\n" \
"s_waitcnt vmcnt(12)\n" \
"v_mfma_f32_16x16x32_bf16 v[56:59], v[50:53], v[176:179], v[56:59]\n" \
"v_mfma_f32_16x16x32_bf16 v[60:63], v[48:51], v[176:179], v[60:63]\n" \
"s_waitcnt vmcnt(11)\n" \
"v_mfma_f32_16x16x32_bf16 v[56:59], v[50:53], v[248:251], v[56:59]\n" \
"v_mfma_f32_16x16x32_bf16 v[60:63], v[48:51], v[248:251], v[60:63]\n" \
"s_mov_b64 exec, 1\n" \
"buffer_store_dword v46, off, s[56:59], s66 offset:0 sc1\n" \
"s_mov_b64 exec, -1\n" \
"s_waitcnt vmcnt(9)\n" \
"v_mfma_f32_16x16x32_bf16 v[240:243], v[50:53], v[188:191], v[240:243]\n" \
"v_mfma_f32_16x16x32_bf16 v[244:247], v[48:51], v[188:191], v[244:247]\n" \
"s_waitcnt vmcnt(8)\n" \
"v_mfma_f32_16x16x32_bf16 v[240:243], v[50:53], v[196:199], v[240:243]\n" \
"v_cvt_pk_bf16_f32 v56, v56, v57\n" \
"v_cvt_pk_bf16_f32 v60, v60, v61\n" \
"v_mfma_f32_16x16x32_bf16 v[244:247], v[48:51], v[196:199], v[244:247]\n" \
"v_cvt_pk_bf16_f32 v57, v58, v59\n" \
"v_cvt_pk_bf16_f32 v61, v62, v63\n" \
"buffer_store_dwordx2 v[56:57], v42, s[88:91], s67 offen sc1\n" \
"buffer_store_dwordx2 v[60:61], v43, s[88:91], s67 offen sc1\n" \
"s_waitcnt vmcnt(9)\n" \
"v_mfma_f32_16x16x32_bf16 v[240:243], v[50:53], v[204:207], v[240:243]\n" \
"v_mfma_f32_16x16x32_bf16 v[244:247], v[48:51], v[204:207], v[244:247]\n" \
"s_waitcnt vmcnt(8)\n" \
"v_mfma_f32_16x16x32_bf16 v[240:243], v[50:53], v[212:215], v[240:243]\n" \
"v_mfma_f32_16x16x32_bf16 v[244:247], v[48:51], v[212:215], v[244:247]\n" \
"s_waitcnt vmcnt(7)\n" \
"v_mfma_f32_16x16x32_bf16 v[240:243], v[50:53], v[220:223], v[240:243]\n" \
"v_mfma_f32_16x16x32_bf16 v[244:247], v[48:51], v[220:223], v[244:247]\n" \
"s_waitcnt vmcnt(6)\n" \
"v_mfma_f32_16x16x32_bf16 v[240:243], v[50:53], v[228:231], v[240:243]\n" \
"v_mfma_f32_16x16x32_bf16 v[244:247], v[48:51], v[228:231], v[244:247]\n" \
"s_waitcnt vmcnt(5)\n" \
"v_mfma_f32_16x16x32_bf16 v[240:243], v[50:53], v[236:239], v[240:243]\n" \
"v_mfma_f32_16x16x32_bf16 v[244:247], v[48:51], v[236:239], v[244:247]\n" \
"s_nop 6\n" \
"v_cvt_pk_bf16_f32 v240, v240, v241\n" \
"v_cvt_pk_bf16_f32 v244, v244, v245\n" \
"v_cvt_pk_bf16_f32 v241, v242, v243\n" \
"v_cvt_pk_bf16_f32 v245, v246, v247\n" \
"buffer_store_dwordx2 v[240:241], v42, s[88:91], s69 offen sc1\n" \
"buffer_store_dwordx2 v[244:245], v43, s[88:91], s69 offen sc1\n"
#define ATT_VCLOB "v21","v22","v23","v24","v25","v26","v27","v28","v29","v32","v33","v34","v35","v36","v37","v38","v39","v40","v41","v42","v43","v44","v45","v46","v47","v48","v49","v50","v51","v52","v53","v54","v55","v56","v57","v58","v59","v60","v61","v62","v63","v64","v65","v66","v67","v68","v69","v70","v71","v72","v73","v74","v75","v76","v77","v78","v79","v80","v81","v82","v83","v84","v85","v86","v87","v88","v89","v90","v91","v92","v93","v94","v95","v96","v97","v98","v99","v100","v101","v102","v103","v104","v105","v106","v107","v108","v109","v110","v111","v112","v113","v114","v115","v116","v117","v118","v119","v120","v121","v122","v123","v124","v125","v126","v127","v128","v129","v130","v131","v132","v133","v134","v135","v136","v137","v138","v139","v140","v141","v142","v143","v144","v145","v146","v147","v148","v149","v150","v151","v152","v153","v154","v155","v156","v157","v158","v159","v160","v161","v162","v163","v164","v165","v166","v167","v168","v169","v170","v171","v172","v173","v174","v175","v176","v177","v178","v179","v180","v181","v182","v183","v184","v185","v186","v187","v188","v189","v190","v191","v192","v193","v194","v195","v196","v197","v198","v199","v200","v201","v202","v203","v204","v205","v206","v207","v208","v209","v210","v211","v212","v213","v214","v215","v216","v217","v218","v219","v220","v221","v222","v223","v224","v225","v226","v227","v228","v229","v230","v231","v232","v233","v234","v235","v236","v237","v238","v239","v240","v241","v242","v243","v244","v245","v246","v247","v248","v249","v250","v251","v252","v253","v254","v255"
#define ATT_SCLOB "s63","s64","s65","s66","s67","s68","s69","s70","s71","s72","s73","s74","s75","s76","s77","s78","s79","s80","s81","s82","s83","s84","s85","s86","s87","s88","s89","s90","s91","s92","s93","s95","s96","s97","s98","s99","s100","s101"
#define ATT_WTCH 0
enum { T_fo, T_fj, T_fa, T_fas, T_fb, T_fbs, T_f8, T_mk8, T_wt, T_wol, T_woh, T_wbl, T_wbh, T_outl, T_outh, T_r1l, T_r1h, T_r1n, T_r1s, T_m0, T_m1, T_mw, T_m3, T_m4, T_ml, T_w0, T_w1, T_w2, T_w3, T_A1, T_A2, T_A3, T_B1, T_B2, T_B3, T_Ao, T_Bo, T_Al, T_Bl, T_l8, T_xr, T_x8r, T_oA, T_oB, T_o8, T_x0, T_x1, T_x2, T_x3, T_x4, T_x5, T_x6, T_x8, T_ep };

__device__ __forceinline__ void out_consts(u32x4 (&vp)[3], const u16* wo, const u16* wob, const u16* out, const unsigned b, const unsigned w, const unsigned lane) {
    const unsigned sp = b & 7u, nt = (b >> 3) & 3u, j = b >> 5, head = 4u * sp + nt, OOR = 0x40000000u;
    unsigned vw[9];
#pragma unroll
    for (unsigned i = 0; i < 9u; ++i) {
        const unsigned q = 64u * i + lane, row = __umul24(q, 3641u) >> 16, c = q - __umul24(row, 18u);
        vw[i] = __umul24(row, DM * 2) + (c < 16u ? (c * 16u + sp * 32u + 32u) & 255u : c * 16u) - (i & 3u) * 1024u;
    }
    const unsigned row = 16u * w + (lane >> 2), r9 = __umul24(16u * (row >> 5) + (row & 12u) + 2u * ((row >> 4) & 1u) + ((row >> 1) & 1u), 18u) + 16u + (row & 1u);
    unsigned vwl = __umul24(r9, DM * 2) + ((lane & 2u) ? 256u : sp * 32u) + (lane & 1u) * 16u, vrd = __umul24(lane >> 2, 288u) + (lane & 3u) * 8u;
    unsigned tab = 0;
#define TB(n, v) asm("v_writelane_b32 %0, %1, %2" : "+v"(tab) : "s"((unsigned)(v)), "n"((int)T_##n))
    const unsigned FJ = (unsigned)WS_F + (F_J + 640u * j) * 4u, mt = 2u * (w & 2u) + (w & 1u), w2 = w < 2u ? w : 2u, hw = head * 147456u;
    const unsigned FR = (unsigned)WS_F + F_R * 4u + (8u * j + sp) * 1024u;
    TB(fo, FJ + 4u * head); TB(fj, FJ); TB(fa, FR + (w | 2u) * 256u); TB(fas, FR + w * 256u + nt * 4u);
    TB(fb, FR + 128u + w * 256u); TB(fbs, FR + 128u + w * 256u + nt * 4u);
    TB(ep, ssel(b + w, OOR, (unsigned)WS_F + F_EPOCH * 4u));
    const u16* pwo = wo + nt * 144u; const u16* pwb = wob + nt * 144u;
    TB(wol, PLO(pwo)); TB(woh, PHI(pwo) & 0xffffu); TB(wbl, PLO(pwb)); TB(wbh, PHI(pwb) & 0xffffu); TB(outl, PLO(out)); TB(outh, PHI(out) & 0xffffu); TB(r1l, sslt<3>(w, PLO(pwo), PLO(pwb))); TB(r1h, sslt<3>(w, PHI(pwo), PHI(pwb)) & 0xffffu);
    TB(r1n, sslt<3>(w, 72u * sp + 71u, 4095u) * 1152u + 288u); TB(r1s, sslt<3>(w, (72u * sp + 32u * w) * 1152u, hw + 110592u));
    TB(m0, 131072u + 9216u * w2); TB(w0, hw + 36864u * w2); TB(m1, 24576u + 9216u * w); TB(mw, 65536u + 9216u * w); TB(m3, sslt<3>(w, 102400u + 9216u * w, 0u)); TB(m4, sslt<3>(w, 131072u + 9216u * w, 9216u)); TB(ml, 61440u + 1024u * w);
    const unsigned tA = (unsigned)WS_AF + (8u * j + mt) * (KS_TOT * 1024u), tB = (unsigned)WS_AF + (120u - 8u * j + mt) * (KS_TOT * 1024u);
#define HK(k) (4u * sp + ((nt + k) & 3u))
    TB(w1, HK(1u) * 147456u + w * 36864u); TB(w2, HK(2u) * 147456u + w * 36864u); TB(w3, HK(3u) * 147456u + w * 36864u);
    TB(A1, tA + 18432u + HK(1u) * 4096u); TB(A2, tA + 18432u + HK(2u) * 4096u); TB(A3, tA + 18432u + HK(3u) * 4096u);
    TB(B1, tB + 18432u + HK(1u) * 4096u); TB(B2, tB + 18432u + HK(2u) * 4096u); TB(B3, tB + 18432u + HK(3u) * 4096u);
    TB(Ao, tA + sp * 2048u); TB(Bo, tB + sp * 2048u); TB(Al, tA + 16384u); TB(Bl, tB + 16384u);
    const unsigned nred = w ^ (sp >> 1), blk = sp & 1u;
    const unsigned xg = (unsigned)WS_X + (4u * j + nt) * 589824u + w * 16384u, oA = (128u * j + 16u * mt) * 1152u + nt * 288u, oB = (1920u - 128u * j + 16u * mt) * 1152u + nt * 288u;
    TB(l8, ssel(nred, OOR, ssel(blk, tB, tA) + 16384u)); TB(xr, xg + sp * 65536u); TB(x8r, ssel(nred, OOR, xg + 524288u + (sp & 1u) * 1024u));
    TB(oA, oA + sp * 32u); TB(oB, oB + sp * 32u); TB(o8, ssel(nred, OOR, ssel(blk, oB, oA) + 256u)); TB(x8, xg + 524288u + sp * 2048u);
    TB(f8, xg + w * 131072u + 14400u + sp * 4u); TB(mk8, ssel(nred, 0u, 0xff0000u)); TB(wt, hw + j * 18432u + w * 4608u);
#define XI(i) TB(x##i, xg + ((sp + 1u + i) & 7u) * 65536u + i * 2048u)
    XI(0); XI(1); XI(2); XI(3); XI(4); XI(5); XI(6);
#undef XI
#undef HK
#undef TB
    vp[0] = (u32x4){vw[0], vw[1], vw[2], vw[3]}; vp[1] = (u32x4){vw[4], vw[5], vw[6], vw[7]}; vp[2] = (u32x4){vw[8], vwl, vrd, tab};
}
__global__ __launch_bounds__(256, 1) __attribute__((aligned(4096))) void fused_kernel(const u16* __restrict__ x, const u16* __restrict__ ckv, const u16* __restrict__ wq, const u16* __restrict__ wuk,
                                                       const u16* __restrict__ wuv, const u16* __restrict__ wqkv, const u16* __restrict__ gw,
                                                       const u16* __restrict__ wo, const u16* __restrict__ wob, char* __restrict__ ws_, u16* __restrict__ out) {
    using namespace att;
    extern __shared__ __attribute__((aligned(16))) char lds[];
    u32x4 sa[5]; u32x2 s24, s28; char* ws; unsigned tdum; kaptr ka = (kaptr)__builtin_amdgcn_kernarg_segment_ptr();
    asm volatile("s_load_dwordx8 s[8:15], %[ka], 0x0\n\ts_load_dwordx8 s[16:23], %[ka], 0x20\n\ts_getpc_b64 s[4:5]\n\tv_lshlrev_b32_e32 v191, 7, %[t]\n\ts_bfe_u32 s6, %[b], 0x20003\n\t"
                 "v_lshl_add_u32 v191, s6, 15, v191\n\tv_min_u32_e32 v191, %[e], v191\n\tglobal_load_dword v191, v191, s[4:5]\n\ts_load_dwordx4 s[24:27], %[ka], 0x40\n\ts_load_dwordx2 s[28:29], %[ka], 0x50"
                 : "={s[8:11]}"(sa[0]), "={s[12:15]}"(sa[1]), "={s[16:19]}"(sa[2]), "={s[20:23]}"(sa[3]), "={s[24:25]}"(s24), "={s[26:27]}"(ws), "={s[28:29]}"(s28), "={v191}"(tdum), [ka] "+{s[0:1]}"(ka)
                 : [t] "v"(threadIdx.x), [b] "s"(blockIdx.x), [e] "i"(CT_END) : "s4", "s5", "s6", "scc");
    SBAR();
    const unsigned wid = rfl(threadIdx.x >> 6);
    unsigned tag, vl;
    const int b = blockIdx.x;
    u32x4 vp[5], srdA; u32x2 sd;
    proj_phase(ka, ws, wid, vp, vl, sa, s24, s28, tdum, srdA, sd, tag
    );
    unsigned* const flags = (unsigned*)(ws + WS_F);
    RTS(2);
#define AST(L) do {} while (0)
#define ATT_TAIL "s_endpgm\n"
    unsigned abad;
    asm volatile(ATT_ASM ATT_TAIL "L_end_%=:\n s_nop 1"
                 : "={s94}"(abad)
                 : "{v[0:3]}"(vp[0]), "{v[4:7]}"(vp[1]), "{v[8:11]}"(vp[2]), "{v[12:15]}"(vp[3]), "{v[16:19]}"(vp[4]), "{v20}"(vl), "{s[56:59]}"(srdA), "{s[60:61]}"(sd), "{s62}"(tag),
                   "{s[8:11]}"(sa[0]), "{s[12:15]}"(sa[1]), "{s[16:19]}"(sa[2]), "{s[20:23]}"(sa[3]), "{s24}"(sa[4][0]), "{s3}"(wid)
                 : "memory", "m0", "vcc", "scc", ATT_VCLOB, ATT_SCLOB, ACLOB_LO, ACLOB_HI);
    {
        unsigned b2 = b, w2 = wid;
        asm volatile("s_waitcnt vmcnt(0)" : "+s"(ka), "+s"(b2), "+s"(w2), "+s"(ws) :: "memory");
        const unsigned j2 = b2 >> 5, p2 = w2 & 1u, h2 = (b2 & 7u) * 4u + ((b2 >> 3) & 3u);
#pragma unroll 1
        for (unsigned i = w2 & 2u; i < ((w2 & 2u) ? (abad ? 8u : 4u) : 2u); ++i)
            att_exact(ka[0], ka[2], ws, h2, (i < 4u ? 1920u - 128u * j2 : 128u * j2 - 128u) + 32u * i + 16u * p2, *reinterpret_cast<const float*>(lds + LDS_G + (32u * i + 16u * p2 + (lane_id() & 15u)) * 4u));
        asm volatile("s_waitcnt vmcnt(0)" ::: "memory");
        unsigned* fa = (unsigned*)(ws + WS_F) + F_R + (8u * j2 + (b2 & 7u)) * 256u + (2u + p2) * 64u + ((b2 >> 3) & 3u);
        if (w2 & 2u) st_flag(fa, tag);
        wait_flags(fa, ld_flag(fa), tag);
        const unsigned l2 = lane_id(); u32x4 c[3]; out_consts(c, ka[7], ka[8], ka[10], b2, w2, l2);
        asm volatile("s_waitcnt vmcnt(0) lgkmcnt(0)\n\ts_nop 7\n\ts_branch L_att_stub" :: "{v[0:3]}"(c[0]), "{v[4:7]}"(c[1]), "{v[8:11]}"(c[2]), "{v20}"(l2 * 16u), "{s[56:59]}"(SRDA), "{s60}"(SD(8u * j2, h2, w2)),
                     "{s61}"(SD(120u - 8u * j2, h2, w2)), "{s62}"(tag) : "memory");
    }
    RTS(5);
}

extern "C" void kernel_launch(void* const* d_in, const int* in_sizes, int n_in, void* d_out, int out_size, void* d_ws, size_t ws_size,
                              hipStream_t stream) {
    (void)hipFuncSetAttribute((const void*)fused_kernel, hipFuncAttributeMaxDynamicSharedMemorySize, att::LDS_BYTES);
    fused_kernel<<<dim3(256), 256, att::LDS_BYTES, stream>>>((const u16*)d_in[0], (const u16*)d_in[4], (const u16*)d_in[5], (const u16*)d_in[6], (const u16*)d_in[7], (const u16*)d_in[1],
                                                             (const u16*)d_in[9], (const u16*)d_in[2], (const u16*)d_in[8], (char*)d_ws, (u16*)d_out);
}
```

```cpp
#include <hip/hip_runtime.h>
#include <hip/hip_bf16.h>
#include <stdint.h>
#include <utility>

typedef short bf16x8 __attribute__((ext_vector_type(8)));
typedef short s16x4 __attribute__((ext_vector_type(4)));
typedef float f32x16 __attribute__((ext_vector_type(16)));
typedef float f32x4 __attribute__((ext_vector_type(4)));
typedef unsigned u32x4 __attribute__((ext_vector_type(4)));
typedef unsigned u32x2 __attribute__((ext_vector_type(2)));
typedef unsigned short u16;

constexpr int S = 2048, DM = 576, NH = 32, HD = 128, DC = 512, NQKV = 1728, DHA = 18;
constexpr int NBIG = NH * HD;
constexpr int KS_TOT = 146;

#define SBAR() __builtin_amdgcn_sched_barrier(0)
constexpr size_t WS_Q = 0, WS_K = WS_Q + (size_t)S * NBIG * 2, WS_V = WS_K + (size_t)S * NBIG * 2, WS_AF = WS_V + (size_t)S * NBIG * 2, WS_X = WS_AF + (size_t)128 * KS_TOT * 1024,
                 WS_P = WS_X + (size_t)64 * 16 * 9216, WS_F = WS_P + (size_t)128 * (4 * 32 * 256 + 1024) * 4;
constexpr size_t WS_L = (size_t)96 << 20;
static_assert(WS_F == 96206848, "workspace map is baked into the generated asm");
constexpr int F_EPOCH = 0, F_J = 1024, F_ST = 8192, F_KV = 40960, F_R = 65536;
constexpr unsigned TAG_STEP = 0x9E3779B1u;
__device__ __forceinline__ unsigned ld_flag(const unsigned* p) { return __hip_atomic_load(p, __ATOMIC_RELAXED, __HIP_MEMORY_SCOPE_AGENT); }
__device__ __forceinline__ void st_flag(unsigned* p, unsigned v) { __hip_atomic_store(p, v, __ATOMIC_RELAXED, __HIP_MEMORY_SCOPE_AGENT); }
__device__ __forceinline__ unsigned ld_flag_async(const unsigned* p) { unsigned v; asm volatile("global_load_dword %0, %1, off sc1" : "=v"(v) : "v"(p) : "memory"); return v; }
#define WAITV(v, N) asm volatile("s_waitcnt vmcnt(" #N ")" : "+v"(v) :: "memory")
__device__ __forceinline__ void wait_flags(const unsigned* p, unsigned v, unsigned tag) {
    if (__builtin_expect(__builtin_amdgcn_ballot_w64(v != tag) != 0ull, 0)) {
        unsigned spins = 0;
        do { __builtin_amdgcn_s_sleep(1); } while (__builtin_amdgcn_ballot_w64(ld_flag(p) != tag) != 0ull && ++spins < (1u << 22));
    }
    asm volatile("" ::: "memory");
}

__device__ __forceinline__ unsigned cvtpk(float lo, float hi) {
    unsigned r; asm volatile("v_cvt_pk_bf16_f32 %0, %1, %2" : "=v"(r) : "v"(lo), "v"(hi)); return r;
}
__device__ __forceinline__ float bf2f(u16 v) { return __uint_as_float(((unsigned)v) << 16); }
__device__ __forceinline__ int crow(int r, int hi) { return (r & 3) + 8 * (r >> 2) + 4 * hi; }

typedef __attribute__((address_space(3))) s16x4* lds_s16x4_p;
__device__ __forceinline__ s16x4 trrd(unsigned addr) { return __builtin_amdgcn_ds_read_tr16_b64_v4i16((lds_s16x4_p)addr); }

__device__ __forceinline__ unsigned lane_id() { unsigned l; asm volatile("v_mbcnt_lo_u32_b32 %0, -1, 0\n\tv_mbcnt_hi_u32_b32 %0, -1, %0" : "=v"(l)); return l; }
#define RTS(slot) do {} while (0)
#define CT_END 0x1c380
#define ACLOB_LO "a0","a1","a2","a3","a4","a5","a6","a7","a8","a9","a10","a11","a12","a13","a14","a15","a16","a17","a18","a19","a20","a21","a22","a23","a24","a25","a26","a27","a28","a29","a30","a31","a32","a33","a34","a35","a36","a37","a38","a39","a40","a41","a42","a43","a44","a45","a46","a47","a48","a49","a50","a51","a52","a53","a54","a55","a56","a57","a58","a59","a60","a61","a62","a63","a64","a65","a66","a67","a68","a69","a70","a71","a72","a73","a74","a75","a76","a77","a78","a79","a80","a81","a82","a83","a84","a85","a86","a87","a88","a89","a90","a91","a92","a93","a94","a95","a96","a97","a98","a99","a100","a101","a102","a103","a104","a105","a106","a107","a108","a109","a110","a111","a112","a113","a114","a115","a116","a117","a118","a119","a120","a121","a122","a123","a124","a125","a126","a127"
#define ACLOB_HI "a128","a129","a130","a131","a132","a133","a134","a135","a136","a137","a138","a139","a140","a141","a142","a143","a144","a145","a146","a147","a148","a149","a150","a151","a152","a153","a154","a155","a156","a157","a158","a159","a160","a161","a162","a163","a164","a165","a166","a167","a168","a169","a170","a171","a172","a173","a174","a175","a176","a177","a178","a179","a180","a181","a182","a183","a184","a185","a186","a187","a188","a189","a190","a191","a192","a193","a194","a195","a196","a197","a198","a199","a200","a201","a202","a203","a204","a205","a206","a207","a208","a209","a210","a211","a212","a213","a214","a215","a216","a217","a218","a219","a220","a221","a222","a223","a224","a225","a226","a227","a228","a229","a230","a231","a232","a233","a234","a235","a236","a237","a238","a239","a240","a241","a242","a243","a244","a245","a246","a247","a248","a249","a250","a251","a252","a253","a254","a255"
#define PLO(p) ((unsigned)(size_t)(p))
#define PHI(p) ((unsigned)((size_t)(p) >> 32))
__device__ __forceinline__ unsigned vsel(bool c, unsigned a, unsigned b) { asm("" : "+v"(a), "+v"(b)); return c ? a : b; }
__device__ __forceinline__ unsigned ssel(unsigned c, unsigned a, unsigned b) { unsigned r; asm("s_cmp_lg_u32 %1, 0\n\ts_cselect_b32 %0, %2, %3" : "=s"(r) : "s"(c), "s"(a), "s"(b) : "scc"); return r; }
template <int N> __device__ __forceinline__ unsigned sslt(unsigned c, unsigned a, unsigned b) { unsigned r; asm("s_cmpk_lt_u32 %1, %4\n\ts_cselect_b32 %0, %2, %3" : "=s"(r) : "s"(c), "s"(a), "s"(b), "n"(N) : "scc"); return r; }
namespace att {
constexpr int LDS_BYTES = 163840;
constexpr unsigned LDS_G = 159744u, LDS_L = 161792u;
}
#define SD(J8, H, W) ((unsigned)WS_AF + (((J8) + 2u * ((W) & 2u) + ((W) & 1u)) * KS_TOT + 18u + 4u * (H)) * 1024u)
#define SRDA (u32x4){PLO(ws), PHI(ws) & 0xffffu, (unsigned)WS_L, 0x00020000u}
#define PROJ_ASM \
".p2align 3\n" \
"L_pb:\n" \
"s_waitcnt lgkmcnt(0)\n" \
"s_mov_b32 s64, s10\n" \
"s_and_b32 s65, s11, 0xffff\n" \
"s_mov_b32 s66, 0x200000\n" \
"s_mov_b32 s67, 0x20000\n" \
"s_mov_b32 s68, s14\n" \
"s_and_b32 s69, s15, 0xffff\n" \
"s_mov_b32 s70, 0x400000\n" \
"s_mov_b32 s71, 0x20000\n" \
"s_mov_b32 s72, s16\n" \
"s_and_b32 s73, s17, 0xffff\n" \
"s_mov_b32 s74, 0x400000\n" \
"s_mov_b32 s75, 0x20000\n" \
"s_add_u32 m0, s30, 0x0\n" \
"s_movk_i32 s84, 0x0\n" \
"buffer_load_dwordx4 v22, s[64:67], s84 offen offset:0 lds\n" \
"s_movk_i32 s84, 0x1c00\n" \
"buffer_load_dwordx4 v23, s[64:67], s84 offen offset:1024 lds\n" \
"s_movk_i32 s84, 0x3800\n" \
"buffer_load_dwordx4 v22, s[64:67], s84 offen offset:2048 lds\n" \
"s_movk_i32 s84, 0x5400\n" \
"buffer_load_dwordx4 v23, s[64:67], s84 offen offset:3072 lds\n" \
"s_add_u32 m0, s31, 0x0\n" \
"s_movk_i32 s84, 0x0\n" \
"buffer_load_dwordx4 v26, s[68:71], s84 offen offset:0 lds\n" \
"s_movk_i32 s84, 0x7c00\n" \
"buffer_load_dwordx4 v26, s[68:71], s84 offen offset:1024 lds\n" \
"s_addk_i32 s84, 0x7c00\n" \
"buffer_load_dwordx4 v27, s[68:71], s84 offen offset:2048 lds\n" \
"s_addk_i32 s84, 0x7c00\n" \
"buffer_load_dwordx4 v27, s[68:71], s84 offen offset:3072 lds\n" \
"s_movk_i32 s7, 0x300\n" \
"s_lshr_b32 s4, s2, 1\n" \
"s_and_b32 s4, s4, 60\n" \
"s_or_b32 s4, s4, s3\n" \
"v_lshrrev_b32_e32 v212, 4, v20\n" \
"v_min_u32_e32 v212, 59, v212\n" \
"s_cmpk_lt_u32 s2, 0x80\n" \
"s_cselect_b32 s6, s14, s16\n" \
"v_subrev_u32_e32 v214, 48, v212\n" \
"v_mov_b32_e32 v212, s10\n" \
"v_mov_b32_e32 v215, s6\n" \
"s_cmpk_lt_u32 s2, 0x80\n" \
"s_cselect_b32 s5, s15, s17\n" \
"v_cmp_gt_u32_e32 vcc, s7, v20\n" \
"v_mov_b32_e64 v213, s11\n" \
"v_bfe_u32 v216, v20, 4, 3\n" \
"v_cndmask_b32_e32 v212, v212, v215, vcc\n" \
"v_mov_b32_e32 v215, s5\n" \
"s_lshl_b32 s5, s3, 4\n" \
"v_cndmask_b32_e32 v213, v213, v215, vcc\n" \
"v_lshlrev_b32_e64 v215, 5, v20\n" \
"v_and_b32_e32 v215, 0x7000, v215\n" \
"v_lshl_add_u32 v215, s4, 6, v215\n" \
"s_lshl_b32 s4, s2, 3\n" \
"s_and_b32 s4, s4, 56\n" \
"v_or3_b32 v215, v215, s4, v216\n" \
"s_lshl_b32 s4, s2, 6\n" \
"s_or_b32 s6, s5, s4\n" \
"v_cmp_gt_u32_e64 s[4:5], 6, v214\n" \
"v_add_u32_e32 v215, 0x2000, v215\n" \
"s_nop 0\n" \
"v_cndmask_b32_e64 v216, 4, 2, s[4:5]\n" \
"v_add3_u32 v214, s6, v214, v216\n" \
"s_lshl_b32 s4, s3, 9\n" \
"v_cndmask_b32_e32 v214, v214, v215, vcc\n" \
"v_lshlrev_b32_e32 v214, 7, v214\n" \
"v_mov_b32_e64 v215, 0\n" \
"v_lshl_add_u64 v[184:185], v[212:213], 0, v[214:215]\n" \
"s_or_b32 s32, s4, 0x25000\n" \
"s_add_u32 m0, s30, 0x1000\n" \
"s_mov_b32 s84, 0x8000\n" \
"buffer_load_dwordx4 v22, s[64:67], s84 offen offset:0 lds\n" \
"s_and_b32 s33, s3, 1\n" \
"v_lshrrev_b32_e32 v212, 4, v20\n" \
"v_lshrrev_b32_e64 v214, 8, v20\n" \
"v_bfe_u32 v212, v212, 1, 3\n" \
"v_lshlrev_b32_e32 v215, 2, v20\n" \
"s_addk_i32 s84, 0x1c00\n" \
"buffer_load_dwordx4 v23, s[64:67], s84 offen offset:1024 lds\n" \
"v_lshrrev_b32_e64 v216, 1, v20\n" \
"v_bfe_u32 v213, v20, 4, 4\n" \
"s_lshl_b32 s4, s3, 5\n" \
"v_xor_b32_e32 v212, v212, v214\n" \
"v_lshlrev_b32_e32 v214, 11, v214\n" \
"s_addk_i32 s84, 0x1c00\n" \
"buffer_load_dwordx4 v22, s[64:67], s84 offen offset:2048 lds\n" \
"v_and_b32_e32 v215, 0x300, v215\n" \
"v_and_b32_e64 v217, 24, v216\n" \
"v_and_or_b32 v213, s4, 64, v213\n" \
"v_or3_b32 v214, v215, v214, v217\n" \
"s_lshl_b32 s4, s33, 14\n" \
"s_addk_i32 s84, 0x1c00\n" \
"buffer_load_dwordx4 v23, s[64:67], s84 offen offset:3072 lds\n" \
"v_and_b32_e32 v215, 0xe0, v216\n" \
"v_lshlrev_b32_e32 v213, 7, v213\n" \
"v_lshlrev_b32_e32 v212, 4, v212\n" \
"v_or3_b32 v214, v214, s4, v215\n" \
"v_or_b32_e64 v28, v212, v213\n" \
"s_add_u32 m0, s31, 0x4000\n" \
"s_movk_i32 s84, 0x0\n" \
"buffer_load_dwordx4 v26, s[72:75], s84 offen offset:0 lds\n" \
"v_add_u32_e32 v32, 0x10000, v214\n" \
"v_bitop3_b32 v29, v212, 64, v213 bitop3:0x36\n" \
"v_xor_b32_e32 v33, 0x20, v32\n" \
"v_xor_b32_e64 v34, 0x40, v32\n" \
"v_xor_b32_e32 v35, 0x60, v32\n" \
"s_movk_i32 s84, 0x7c00\n" \
"buffer_load_dwordx4 v26, s[72:75], s84 offen offset:1024 lds\n" \
"v_xor_b32_e32 v36, 0x80, v32\n" \
"v_xor_b32_e32 v37, 0xa0, v32\n" \
"v_xor_b32_e32 v38, 0xc0, v32\n" \
"v_xor_b32_e32 v39, 0xe0, v32\n" \
"s_mov_b32 s80, s26\n" \
"s_addk_i32 s84, 0x7c00\n" \
"buffer_load_dwordx4 v27, s[72:75], s84 offen offset:2048 lds\n" \
"s_and_b32 s81, s27, 0xffff\n" \
"s_mov_b32 s82, 0x10000000\n" \
"s_mov_b32 s83, 0x20000\n" \
"s_mov_b32 s76, s18\n" \
"s_and_b32 s77, s19, 0xffff\n" \
"s_addk_i32 s84, 0x7c00\n" \
"buffer_load_dwordx4 v27, s[72:75], s84 offen offset:3072 lds\n" \
"s_mov_b32 s78, 0x1e6000\n" \
"s_mov_b32 s79, 0x20000\n" \
"s_branch L_pgo_%=\n" \
"L_pret:\n" \
"s_setpc_b64 s[88:89]\n" \
"L_pgo_%=:\n" \
"s_mul_i32 s88, s33, 0xa000\n" \
"s_lshl_b32 s89, s3, 11\n" \
"s_and_b32 s89, s89, 0x1000\n" \
"s_add_u32 s88, s88, s89\n" \
"s_getpc_b64 s[86:87]\n" \
"L_hpa_%=:\n" \
"s_add_u32 s86, s86, L_pp1-L_hpa_%=\n" \
"s_addc_u32 s87, s87, 0\n" \
"s_add_u32 s86, s86, s88\n" \
"s_addc_u32 s87, s87, 0\n" \
"s_add_u32 m0, s30, 0x8000\n" \
"s_movk_i32 s84, 0x80\n" \
"buffer_load_dwordx4 v22, s[64:67], s84 offen offset:0 lds\n" \
"s_lshr_b32 s4, s2, 1\n" \
"s_and_b32 s4, s4, 0x7c\n" \
"v_bfe_u32 v214, v20, 4, 5\n" \
"s_movk_i32 s84, 0x1c80\n" \
"buffer_load_dwordx4 v23, s[64:67], s84 offen offset:1024 lds\n" \
"s_or_b32 s7, s4, s3\n" \
"v_min_u32_e32 v214, 26, v214\n" \
"v_mad_u64_u32 v[214:215], s[4:5], s7, 27, v[214:215]\n" \
"s_movk_i32 s84, 0x3880\n" \
"buffer_load_dwordx4 v22, s[64:67], s84 offen offset:2048 lds\n" \
"v_mul_u32_u24_e32 v215, 0xaaab, v214\n" \
"v_lshrrev_b32_e32 v215, 18, v215\n" \
"s_mov_b32 s4, 0x1fffffa\n" \
"s_movk_i32 s84, 0x5480\n" \
"buffer_load_dwordx4 v23, s[64:67], s84 offen offset:3072 lds\n" \
"v_mad_u64_u32 v[216:217], s[4:5], v215, s4, v[214:215]\n" \
"s_and_b32 s6, s2, 7\n" \
"v_mul_u32_u24_e32 v215, 27, v215\n" \
"s_mov_b64 exec, 0\n" \
"s_swappc_b64 s[88:89], s[86:87]\n" \
"s_mov_b64 exec, -1\n" \
"s_add_u32 s86, s86, 0x2000\n" \
"s_addc_u32 s87, s87, 0\n" \
"s_add_u32 m0, s31, 0x8000\n" \
"s_mov_b32 s84, 0x80000\n" \
"buffer_load_dwordx4 v26, s[68:71], s84 offen offset:0 lds\n" \
"v_lshrrev_b32_e32 v216, 1, v216\n" \
"v_and_b32_e32 v214, 1, v214\n" \
"v_mov_b32_e32 v213, s13\n" \
"s_addk_i32 s84, 0x7c00\n" \
"buffer_load_dwordx4 v26, s[68:71], s84 offen offset:1024 lds\n" \
"v_lshrrev_b32_e32 v212, 4, v20\n" \
"v_mul_u32_u24_e64 v216, 9, v216\n" \
"v_add3_u32 v214, v214, s6, v215\n" \
"s_mov_b64 exec, 0\n" \
"s_swappc_b64 s[88:89], s[86:87]\n" \
"s_mov_b64 exec, -1\n" \
"s_add_u32 s86, s86, 0x2000\n" \
"s_addc_u32 s87, s87, 0\n" \
"s_addk_i32 s84, 0x7c00\n" \
"buffer_load_dwordx4 v27, s[68:71], s84 offen offset:2048 lds\n" \
"v_add_lshl_u32 v214, v214, v216, 7\n" \
"v_mad_u64_u32 v[216:217], s[4:5], s7, 36, v[212:213]\n" \
"s_movk_i32 s4, 0x240\n" \
"s_addk_i32 s84, 0x7c00\n" \
"buffer_load_dwordx4 v27, s[68:71], s84 offen offset:3072 lds\n" \
"v_mov_b32_e32 v219, s9\n" \
"v_mov_b32_e32 v215, 0\n" \
"v_cmp_gt_u32_e32 vcc, s4, v20\n" \
"s_mov_b64 exec, 0\n" \
"s_swappc_b64 s[88:89], s[86:87]\n" \
"s_mov_b64 exec, -1\n" \
"s_add_u32 s86, s86, 0x2000\n" \
"s_addc_u32 s87, s87, 0\n" \
"s_add_u32 m0, s30, 0x9000\n" \
"s_mov_b32 s84, 0x8080\n" \
"buffer_load_dwordx4 v22, s[64:67], s84 offen offset:0 lds\n" \
"s_lshl_b32 s4, s2, 2\n" \
"v_lshl_add_u64 v[186:187], s[18:19], 0, v[214:215]\n" \
"v_mov_b32_e32 v214, s12\n" \
"s_addk_i32 s84, 0x1c00\n" \
"buffer_load_dwordx4 v23, s[64:67], s84 offen offset:1024 lds\n" \
"v_mov_b32_e32 v217, s8\n" \
"s_or_b32 s4, s4, s3\n" \
"v_cndmask_b32_e32 v219, v219, v213, vcc\n" \
"s_addk_i32 s84, 0x1c00\n" \
"buffer_load_dwordx4 v22, s[64:67], s84 offen offset:2048 lds\n" \
"v_and_b32_e32 v213, 0x3ff8, v216\n" \
"s_mul_i32 s4, s4, 18\n" \
"v_cndmask_b32_e32 v218, v217, v214, vcc\n" \
"s_mov_b64 exec, 0\n" \
"s_swappc_b64 s[88:89], s[86:87]\n" \
"s_mov_b64 exec, -1\n" \
"s_andn2_b32 s85, 2, s3\n" \
"s_lshl_b32 s85, s85, 12\n" \
"s_add_u32 s86, s86, s85\n" \
"s_addc_u32 s87, s87, 0\n" \
"s_addk_i32 s84, 0x1c00\n" \
"buffer_load_dwordx4 v23, s[64:67], s84 offen offset:3072 lds\n" \
"v_or_b32_e32 v213, s6, v213\n" \
"v_and_b32_e32 v214, 7, v216\n" \
"v_min_u32_e32 v212, 53, v212\n" \
"s_add_u32 m0, s31, 0xc000\n" \
"s_mov_b32 s84, 0x80000\n" \
"buffer_load_dwordx4 v26, s[72:75], s84 offen offset:0 lds\n" \
"s_sub_i32 s4, s4, 36\n" \
"v_lshl_or_b32 v213, v213, 3, v214\n" \
"v_add_u32_e32 v212, s4, v212\n" \
"s_mov_b64 exec, 0\n" \
"s_swappc_b64 s[88:89], s[86:87]\n" \
"s_mov_b64 exec, -1\n" \
"s_addk_i32 s84, 0x7c00\n" \
"buffer_load_dwordx4 v26, s[72:75], s84 offen offset:1024 lds\n" \
"v_cndmask_b32_e32 v212, v212, v213, vcc\n" \
"v_lshlrev_b32_e32 v214, 7, v212\n" \
"v_lshl_add_u64 v[188:189], v[218:219], 0, v[214:215]\n" \
"s_addk_i32 s84, 0x7c00\n" \
"buffer_load_dwordx4 v27, s[72:75], s84 offen offset:2048 lds\n" \
"s_movk_i32 s5, 0xd80\n" \
"s_lshr_b32 s6, s2, 5\n" \
"v_lshrrev_b32_e32 v212, 4, v20\n" \
"s_addk_i32 s84, 0x7c00\n" \
"buffer_load_dwordx4 v27, s[72:75], s84 offen offset:3072 lds\n" \
"v_lshl_or_b32 v215, s3, 6, v212\n" \
"v_mul_u32_u24_e32 v216, 0x2aab, v215\n" \
"v_lshrrev_b32_e32 v216, 16, v216\n" \
"s_mov_b32 m0, s32\n" \
"s_mov_b32 exec_lo, 0xffffff\n" \
"s_mov_b32 exec_hi, 0xfff0000\n" \
"global_load_lds_dword v[184:185], off\n" \
"s_mov_b64 exec, -1\n" \
"s_mov_b32 s84, 0x5bc0000\n" \
"buffer_load_dword v180, off, s[80:83], s84 sc1\n" \
"s_cmp_lg_u32 s33, 0\n" \
"s_cbranch_scc1 L_w1_%=\n" \
"L_w0_%=:\n" \
"s_waitcnt vmcnt(18)\n" \
"s_barrier\n" \
"ds_read_b64_tr_b16 a[160:161], v32 offset:0\n" \
"ds_read_b64_tr_b16 a[162:163], v32 offset:1024\n" \
"ds_read_b128 a[128:131], v28 offset:16384\n" \
"ds_read_b64_tr_b16 a[224:225], v32 offset:8192\n" \
"ds_read_b64_tr_b16 a[226:227], v32 offset:9216\n" \
"ds_read_b128 a[192:195], v29 offset:16384\n" \
"ds_read_b128 a[132:135], v28 offset:18432\n" \
"ds_read_b128 a[196:199], v29 offset:18432\n" \
"ds_read_b64_tr_b16 a[164:165], v33 offset:0\n" \
"ds_read_b64_tr_b16 a[166:167], v33 offset:1024\n" \
"ds_read_b64_tr_b16 a[228:229], v33 offset:8192\n" \
"ds_read_b64_tr_b16 a[230:231], v33 offset:9216\n" \
"ds_read_b128 a[136:139], v28 offset:20480\n" \
"ds_read_b128 a[200:203], v29 offset:20480\n" \
"s_waitcnt lgkmcnt(11)\n" \
"v_mfma_f32_16x16x32_bf16 a[0:3], a[160:163], a[128:131], 0\n" \
"ds_read_b64_tr_b16 a[168:169], v34 offset:0\n" \
"v_mad_i32_i24 v217, v216, -6, v215\n" \
"s_waitcnt lgkmcnt(9)\n" \
"v_mfma_f32_16x16x32_bf16 a[0:3], a[224:227], a[192:195], a[0:3]\n" \
"ds_read_b64_tr_b16 a[170:171], v34 offset:1024\n" \
"v_add_u32_e32 v218, 0xfffffe50, v215\n" \
"s_waitcnt lgkmcnt(9)\n" \
"v_mfma_f32_16x16x32_bf16 a[16:19], a[160:163], a[132:135], 0\n" \
"ds_read_b64_tr_b16 a[232:233], v34 offset:8192\n" \
"s_mul_i32 s35, s6, 0x48\n" \
"s_waitcnt lgkmcnt(9)\n" \
"v_mfma_f32_16x16x32_bf16 a[16:19], a[224:227], a[196:199], a[16:19]\n" \
"ds_read_b64_tr_b16 a[234:235], v34 offset:9216\n" \
"v_lshrrev_b32_e32 v219, 1, v217\n" \
"s_waitcnt lgkmcnt(8)\n" \
"v_mfma_f32_16x16x32_bf16 a[20:23], a[164:167], a[132:135], 0\n" \
"ds_read_b128 a[140:143], v28 offset:22528\n" \
"v_add_u32_e32 v218, s35, v216\n" \
"s_waitcnt lgkmcnt(7)\n" \
"v_mfma_f32_16x16x32_bf16 a[20:23], a[228:231], a[196:199], a[20:23]\n" \
"ds_read_b128 a[204:207], v29 offset:22528\n" \
"v_mul_u32_u24_e32 v219, 0x480, v219\n" \
"v_mfma_f32_16x16x32_bf16 a[4:7], a[164:167], a[128:131], 0\n" \
"ds_read_b64_tr_b16 a[172:173], v35 offset:0\n" \
"v_and_b32_e64 v214, 16, v20\n" \
"v_mfma_f32_16x16x32_bf16 a[4:7], a[228:231], a[192:195], a[4:7]\n" \
"ds_read_b64_tr_b16 a[174:175], v35 offset:1024\n" \
"v_mad_u32_u24 v219, v218, s5, v219\n" \
"s_waitcnt lgkmcnt(9)\n" \
"v_mfma_f32_16x16x32_bf16 a[32:35], a[160:163], a[136:139], 0\n" \
"ds_read_b64_tr_b16 a[236:237], v35 offset:8192\n" \
"v_or_b32_e32 v192, v219, v214\n" \
"s_waitcnt lgkmcnt(9)\n" \
"v_mfma_f32_16x16x32_bf16 a[32:35], a[224:227], a[200:203], a[32:35]\n" \
"ds_read_b64_tr_b16 a[238:239], v35 offset:9216\n" \
"v_mov_b32_e32 v220, 2.0\n" \
"v_mfma_f32_16x16x32_bf16 a[36:39], a[164:167], a[136:139], 0\n" \
"ds_read_b128 a[144:147], v28 offset:0\n" \
"v_mov_b32_e64 v222, 7\n" \
"v_mfma_f32_16x16x32_bf16 a[36:39], a[228:231], a[200:203], a[36:39]\n" \
"ds_read_b128 a[208:211], v29 offset:0\n" \
"v_lshlrev_b32_e32 v220, 7, v218\n" \
"s_waitcnt lgkmcnt(10)\n" \
"v_mfma_f32_16x16x32_bf16 a[40:43], a[168:171], a[136:139], 0\n" \
"ds_read_b128 a[148:151], v28 offset:2048\n" \
"v_lshrrev_b32_e32 v218, 1, v218\n" \
"s_waitcnt lgkmcnt(9)\n" \
"v_mfma_f32_16x16x32_bf16 a[40:43], a[232:235], a[200:203], a[40:43]\n" \
"ds_read_b128 a[212:215], v29 offset:2048\n" \
"v_and_b32_e64 v218, 4, v218\n" \
"v_mfma_f32_16x16x32_bf16 a[24:27], a[168:171], a[132:135], 0\n" \
"ds_read_b128 a[152:155], v28 offset:4096\n" \
"v_and_b32_e64 v216, 2, v216\n" \
"v_mfma_f32_16x16x32_bf16 a[24:27], a[232:235], a[196:199], a[24:27]\n" \
"ds_read_b128 a[216:219], v29 offset:4096\n" \
"s_mul_i32 s7, s6, 0x2400\n" \
"v_mfma_f32_16x16x32_bf16 a[8:11], a[168:171], a[128:131], 0\n" \
"ds_read_b128 a[156:159], v28 offset:6144\n" \
"s_sub_i32 s34, 0x22000, s7\n" \
"v_mfma_f32_16x16x32_bf16 a[8:11], a[232:235], a[192:195], a[8:11]\n" \
"ds_read_b128 a[220:223], v29 offset:6144\n" \
"s_mov_b32 m0, s32\n" \
"s_waitcnt lgkmcnt(13)\n" \
"v_mfma_f32_16x16x32_bf16 a[48:51], a[160:163], a[140:143], 0\n" \
"s_mov_b32 exec_lo, 0xff000000\n" \
"s_mov_b32 exec_hi, 0x0\n" \
"global_load_lds_dword v[184:185], off\n" \
"s_mov_b64 exec, -1\n" \
"s_waitcnt lgkmcnt(12)\n" \
"v_mfma_f32_16x16x32_bf16 a[48:51], a[224:227], a[204:207], a[48:51]\n" \
"ds_read_b64_tr_b16 a[176:177], v36 offset:0\n" \
"v_bitop3_b32 v216, v217, v218, v216 bitop3:0x1e\n" \
"v_mfma_f32_16x16x32_bf16 a[52:55], a[164:167], a[140:143], 0\n" \
"ds_read_b64_tr_b16 a[178:179], v36 offset:1024\n" \
"v_or_b32_e32 v213, 0x24400, v20\n" \
"v_mfma_f32_16x16x32_bf16 a[52:55], a[228:231], a[204:207], a[52:55]\n" \
"s_mov_b32 m0, s32\n" \
"ds_read_b64_tr_b16 a[240:241], v36 offset:8192\n" \
"v_mfma_f32_16x16x32_bf16 a[56:59], a[168:171], a[140:143], 0\n" \
"s_mov_b32 exec_lo, 0xffff\n" \
"s_mov_b32 exec_hi, 0x0\n" \
"global_load_lds_dword v[186:187], off\n" \
"s_mov_b64 exec, -1\n" \
"v_mfma_f32_16x16x32_bf16 a[56:59], a[232:235], a[204:207], a[56:59]\n" \
"ds_read_b64_tr_b16 a[242:243], v36 offset:9216\n" \
"v_lshlrev_b32_e32 v216, 4, v216\n" \
"s_waitcnt lgkmcnt(14)\n" \
"v_mfma_f32_16x16x32_bf16 a[60:63], a[172:175], a[140:143], 0\n" \
"ds_read_b64_tr_b16 a[180:181], v37 offset:0\n" \
"v_add3_u32 v196, v220, s34, v216\n" \
"s_waitcnt lgkmcnt(13)\n" \
"v_mfma_f32_16x16x32_bf16 a[60:63], a[236:239], a[204:207], a[60:63]\n" \
"ds_read_b64_tr_b16 a[182:183], v37 offset:1024\n" \
"v_mov_b32_e32 v216, v213\n" \
"v_mfma_f32_16x16x32_bf16 a[44:47], a[172:175], a[136:139], 0\n" \
"ds_read_b64_tr_b16 a[244:245], v37 offset:8192\n" \
"s_movk_i32 s36, 0x1b0\n" \
"v_mfma_f32_16x16x32_bf16 a[44:47], a[236:239], a[200:203], a[44:47]\n" \
"ds_read_b64_tr_b16 a[246:247], v37 offset:9216\n" \
"v_or_b32_e32 v216, 0x100, v215\n" \
"v_mfma_f32_16x16x32_bf16 a[28:31], a[172:175], a[132:135], 0\n" \
"ds_read_b64_tr_b16 a[184:185], v38 offset:0\n" \
"v_mul_u32_u24_e32 v217, 0x2aab, v216\n" \
"v_mfma_f32_16x16x32_bf16 a[28:31], a[236:239], a[196:199], a[28:31]\n" \
"ds_read_b64_tr_b16 a[186:187], v38 offset:1024\n" \
"v_add_u32_e32 v215, 0xffffff50, v215\n" \
"v_mfma_f32_16x16x32_bf16 a[12:15], a[172:175], a[128:131], 0\n" \
"v_lshrrev_b32_e32 v217, 16, v217\n" \
"v_mov_b32_e64 v220, v215\n" \
"v_mfma_f32_16x16x32_bf16 a[12:15], a[236:239], a[192:195], a[12:15]\n" \
"v_mad_i32_i24 v218, v217, -6, v216\n" \
"v_cmp_gt_u32_e32 vcc, s36, v216\n" \
"s_waitcnt lgkmcnt(15)\n" \
"v_mfma_f32_16x16x32_bf16 a[76:79], a[172:175], a[144:147], 0\n" \
"ds_read_b64_tr_b16 a[248:249], v38 offset:8192\n" \
"v_mov_b32_e64 v221, 7\n" \
"v_mfma_f32_16x16x32_bf16 a[76:79], a[236:239], a[208:211], a[76:79]\n" \
"v_lshrrev_b32_e32 v222, 1, v218\n" \
"v_cndmask_b32_e32 v217, v220, v217, vcc\n" \
"v_mfma_f32_16x16x32_bf16 a[72:75], a[168:171], a[144:147], 0\n" \
"s_movk_i32 s4, 0x48\n" \
"v_add_u32_e32 v220, s35, v217\n" \
"v_mfma_f32_16x16x32_bf16 a[72:75], a[232:235], a[208:211], a[72:75]\n" \
"ds_read_b64_tr_b16 a[250:251], v38 offset:9216\n" \
"v_mul_u32_u24_e32 v222, 0x480, v222\n" \
"v_mfma_f32_16x16x32_bf16 a[68:71], a[164:167], a[144:147], 0\n" \
"v_mad_u32_u24 v222, v220, s5, v222\n" \
"v_cmp_gt_u32_e64 s[4:5], s4, v215\n" \
"v_mfma_f32_16x16x32_bf16 a[68:71], a[228:231], a[208:211], a[68:71]\n" \
"v_cndmask_b32_e32 v215, v221, v218, vcc\n" \
"v_lshrrev_b32_e32 v218, 1, v220\n" \
"v_mfma_f32_16x16x32_bf16 a[64:67], a[160:163], a[144:147], 0\n" \
"v_or_b32_e32 v214, v222, v214\n" \
"v_mov_b32_e32 v222, 2.0\n" \
"v_mfma_f32_16x16x32_bf16 a[64:67], a[224:227], a[208:211], a[64:67]\n" \
"ds_read_b64_tr_b16 a[188:189], v39 offset:0\n" \
"v_and_b32_e64 v218, 4, v218\n" \
"v_mfma_f32_16x16x32_bf16 a[80:83], a[160:163], a[148:151], 0\n" \
"v_and_b32_e64 v217, 2, v217\n" \
"v_bitop3_b32 v215, v215, v218, v217 bitop3:0x1e\n" \
"s_waitcnt lgkmcnt(15)\n" \
"v_mfma_f32_16x16x32_bf16 a[80:83], a[224:227], a[212:215], a[80:83]\n" \
"v_lshlrev_b32_e32 v223, 1, v220\n" \
"v_mov_b32_e64 v224, 2.0\n" \
"v_mfma_f32_16x16x32_bf16 a[84:87], a[164:167], a[148:151], 0\n" \
"ds_read_b64_tr_b16 a[190:191], v39 offset:1024\n" \
"v_cndmask_b32_e64 v193, v222, v214, vcc\n" \
"v_mfma_f32_16x16x32_bf16 a[84:87], a[228:231], a[212:215], a[84:87]\n" \
"v_lshlrev_b32_e32 v214, 7, v220\n" \
"v_lshlrev_b32_e32 v215, 4, v215\n" \
"v_mfma_f32_16x16x32_bf16 a[88:91], a[168:171], a[148:151], 0\n" \
"s_mul_i32 s39, s3, 18\n" \
"v_add3_u32 v214, v214, s34, v215\n" \
"v_mfma_f32_16x16x32_bf16 a[88:91], a[232:235], a[212:215], a[88:91]\n" \
"ds_read_b64_tr_b16 a[252:253], v39 offset:8192\n" \
"v_cndmask_b32_e64 v195, v224, v223, s[4:5]\n" \
"v_mfma_f32_16x16x32_bf16 a[92:95], a[172:175], a[148:151], 0\n" \
"s_movk_i32 s4, 0x1f8\n" \
"v_mov_b32_e64 v215, v213\n" \
"v_mfma_f32_16x16x32_bf16 a[92:95], a[236:239], a[212:215], a[92:95]\n" \
"s_add_i32 s35, s35, s39\n" \
"v_cmp_gt_u32_e32 vcc, s4, v216\n" \
"v_mfma_f32_16x16x32_bf16 a[108:111], a[172:175], a[152:155], 0\n" \
"ds_read_b64_tr_b16 a[254:255], v39 offset:9216\n" \
"v_add_u32_e64 v212, s35, v212\n" \
"v_mfma_f32_16x16x32_bf16 a[108:111], a[236:239], a[216:219], a[108:111]\n" \
"v_mov_b32_e32 v219, 2.0\n" \
"v_cndmask_b32_e32 v197, v215, v214, vcc\n" \
"v_mfma_f32_16x16x32_bf16 a[104:107], a[168:171], a[152:155], 0\n" \
"s_movk_i32 s35, 0x120\n" \
"v_mul_u32_u24_e32 v214, 0xd80, v212\n" \
"v_mfma_f32_16x16x32_bf16 a[104:107], a[232:235], a[216:219], a[104:107]\n" \
"v_cmp_gt_u32_e32 vcc, s35, v20\n" \
"v_lshrrev_b32_e64 v215, 1, v212\n" \
"v_mfma_f32_16x16x32_bf16 a[100:103], a[164:167], a[152:155], 0\n" \
"s_and_b32 s4, s2, 7\n" \
"v_cndmask_b32_e32 v194, v219, v214, vcc\n" \
"v_mfma_f32_16x16x32_bf16 a[100:103], a[228:231], a[216:219], a[100:103]\n" \
"v_lshlrev_b32_e32 v214, 7, v212\n" \
"v_lshrrev_b32_e32 v212, 2, v212\n" \
"v_mfma_f32_16x16x32_bf16 a[96:99], a[160:163], a[152:155], 0\n" \
"v_and_b32_e64 v212, 2, v212\n" \
"v_and_or_b32 v212, v215, 1, v212\n" \
"v_mfma_f32_16x16x32_bf16 a[96:99], a[224:227], a[216:219], a[96:99]\n" \
"s_lshl_b32 s5, s4, 2\n" \
"s_bfe_u32 s38, s2, 0x20003\n" \
"s_waitcnt lgkmcnt(15)\n" \
"v_mfma_f32_16x16x32_bf16 a[112:115], a[160:163], a[156:159], 0\n" \
"v_lshlrev_b32_e32 v212, 5, v212\n" \
"s_movk_i32 s35, 0x60\n" \
"v_mfma_f32_16x16x32_bf16 a[112:115], a[224:227], a[220:223], a[112:115]\n" \
"s_or_b32 s5, s5, s38\n" \
"v_bitop3_b32 v212, v212, s35, v214 bitop3:0x36\n" \
"v_mfma_f32_16x16x32_bf16 a[116:119], a[164:167], a[156:159], 0\n" \
"s_lshr_b32 s36, s2, 3\n" \
"v_add_u32_e64 v212, s34, v212\n" \
"v_mfma_f32_16x16x32_bf16 a[116:119], a[228:231], a[220:223], a[116:119]\n" \
"s_mul_i32 s34, s5, 0x12000\n" \
"s_or_b32 s40, s34, 0x6000000\n" \
"v_mfma_f32_16x16x32_bf16 a[120:123], a[168:171], a[156:159], 0\n" \
"s_mul_i32 s34, s36, 0x90000\n" \
"s_lshl_b32 s36, s3, 14\n" \
"v_mfma_f32_16x16x32_bf16 a[120:123], a[232:235], a[220:223], a[120:123]\n" \
"s_add_i32 s34, s34, s36\n" \
"s_lshl_b32 s38, s5, 8\n" \
"v_mfma_f32_16x16x32_bf16 a[124:127], a[172:175], a[156:159], 0\n" \
"s_add_i32 s41, s34, 0x4240000\n" \
"s_mul_i32 s34, s5, 36\n" \
"v_mfma_f32_16x16x32_bf16 a[124:127], a[236:239], a[220:223], a[124:127]\n" \
"s_waitcnt vmcnt(4) lgkmcnt(0)\n" \
"s_barrier\n" \
"v_mfma_f32_16x16x32_bf16 v[52:55], a[176:179], a[128:131], 0\n" \
"s_add_u32 m0, s30, 0x0\n" \
"s_movk_i32 s84, 0x100\n" \
"v_mfma_f32_16x16x32_bf16 v[52:55], a[240:243], a[192:195], v[52:55]\n" \
"buffer_load_dwordx4 v22, s[64:67], s84 offen offset:0 lds\n" \
"v_mfma_f32_16x16x32_bf16 v[56:59], a[180:183], a[128:131], 0\n" \
"ds_read_b64_tr_b16 a[160:161], v32 offset:32768\n" \
"s_add_i32 s5, s7, s40\n" \
"v_mfma_f32_16x16x32_bf16 v[56:59], a[244:247], a[192:195], v[56:59]\n" \
"s_movk_i32 s84, 0x1d00\n" \
"v_mfma_f32_16x16x32_bf16 v[60:63], a[184:187], a[128:131], 0\n" \
"buffer_load_dwordx4 v23, s[64:67], s84 offen offset:1024 lds\n" \
"v_mfma_f32_16x16x32_bf16 v[60:63], a[248:251], a[192:195], v[60:63]\n" \
"ds_read_b64_tr_b16 a[162:163], v32 offset:33792\n" \
"s_addk_i32 s5, 0x2000\n" \
"v_mfma_f32_16x16x32_bf16 v[64:67], a[188:191], a[128:131], 0\n" \
"ds_read_b64_tr_b16 a[224:225], v32 offset:40960\n" \
"s_mov_b32 s37, 2.0\n" \
"v_mfma_f32_16x16x32_bf16 v[64:67], a[252:255], a[192:195], v[64:67]\n" \
"ds_read_b128 a[128:131], v28 offset:49152\n" \
"s_or_b32 s38, s38, 0x5be8000\n" \
"v_mfma_f32_16x16x32_bf16 v[80:83], a[188:191], a[132:135], 0\n" \
"ds_read_b128 a[192:195], v29 offset:49152\n" \
"v_mfma_f32_16x16x32_bf16 v[80:83], a[252:255], a[196:199], v[80:83]\n" \
"s_movk_i32 s84, 0x3900\n" \
"v_mfma_f32_16x16x32_bf16 v[76:79], a[184:187], a[132:135], 0\n" \
"buffer_load_dwordx4 v22, s[64:67], s84 offen offset:2048 lds\n" \
"v_mfma_f32_16x16x32_bf16 v[76:79], a[248:251], a[196:199], v[76:79]\n" \
"s_movk_i32 s84, 0x5500\n" \
"v_mfma_f32_16x16x32_bf16 v[72:75], a[180:183], a[132:135], 0\n" \
"buffer_load_dwordx4 v23, s[64:67], s84 offen offset:3072 lds\n" \
"v_mfma_f32_16x16x32_bf16 v[72:75], a[244:247], a[196:199], v[72:75]\n" \
"s_add_u32 m0, s31, 0x0\n" \
"s_mov_b32 s84, 0x100000\n" \
"v_mfma_f32_16x16x32_bf16 v[68:71], a[176:179], a[132:135], 0\n" \
"buffer_load_dwordx4 v26, s[68:71], s84 offen offset:0 lds\n" \
"v_mfma_f32_16x16x32_bf16 v[68:71], a[240:243], a[196:199], v[68:71]\n" \
"ds_read_b128 a[132:135], v28 offset:51200\n" \
"v_mfma_f32_16x16x32_bf16 v[84:87], a[176:179], a[136:139], 0\n" \
"ds_read_b128 a[196:199], v29 offset:51200\n" \
"v_mfma_f32_16x16x32_bf16 v[84:87], a[240:243], a[200:203], v[84:87]\n" \
"s_addk_i32 s84, 0x7c00\n" \
"v_mfma_f32_16x16x32_bf16 v[88:91], a[180:183], a[136:139], 0\n" \
"buffer_load_dwordx4 v26, s[68:71], s84 offen offset:1024 lds\n" \
"v_mfma_f32_16x16x32_bf16 v[88:91], a[244:247], a[200:203], v[88:91]\n" \
"ds_read_b64_tr_b16 a[226:227], v32 offset:41984\n" \
"v_mfma_f32_16x16x32_bf16 v[92:95], a[184:187], a[136:139], 0\n" \
"s_addk_i32 s84, 0x7c00\n" \
"v_mfma_f32_16x16x32_bf16 v[92:95], a[248:251], a[200:203], v[92:95]\n" \
"buffer_load_dwordx4 v27, s[68:71], s84 offen offset:2048 lds\n" \
"v_mfma_f32_16x16x32_bf16 v[96:99], a[188:191], a[136:139], 0\n" \
"ds_read_b64_tr_b16 a[164:165], v33 offset:32768\n" \
"v_mfma_f32_16x16x32_bf16 v[96:99], a[252:255], a[200:203], v[96:99]\n" \
"ds_read_b128 a[136:139], v28 offset:53248\n" \
"v_mfma_f32_16x16x32_bf16 v[112:115], a[188:191], a[140:143], 0\n" \
"ds_read_b128 a[200:203], v29 offset:53248\n" \
"v_mfma_f32_16x16x32_bf16 v[112:115], a[252:255], a[204:207], v[112:115]\n" \
"s_addk_i32 s84, 0x7c00\n" \
"v_mfma_f32_16x16x32_bf16 v[108:111], a[184:187], a[140:143], 0\n" \
"buffer_load_dwordx4 v27, s[68:71], s84 offen offset:3072 lds\n" \
"v_mfma_f32_16x16x32_bf16 v[108:111], a[248:251], a[204:207], v[108:111]\n" \
"s_add_u32 m0, s30, 0x1000\n" \
"s_mov_b32 s84, 0x8100\n" \
"v_mfma_f32_16x16x32_bf16 v[104:107], a[180:183], a[140:143], 0\n" \
"buffer_load_dwordx4 v22, s[64:67], s84 offen offset:0 lds\n" \
"v_mfma_f32_16x16x32_bf16 v[104:107], a[244:247], a[204:207], v[104:107]\n" \
"ds_read_b64_tr_b16 a[166:167], v33 offset:33792\n" \
"v_mfma_f32_16x16x32_bf16 v[100:103], a[176:179], a[140:143], 0\n" \
"ds_read_b64_tr_b16 a[228:229], v33 offset:40960\n" \
"v_mfma_f32_16x16x32_bf16 v[100:103], a[240:243], a[204:207], v[100:103]\n" \
"ds_read_b128 a[140:143], v28 offset:55296\n" \
"v_mfma_f32_16x16x32_bf16 v[116:119], a[176:179], a[144:147], 0\n" \
"ds_read_b128 a[204:207], v29 offset:55296\n" \
"v_mfma_f32_16x16x32_bf16 v[116:119], a[240:243], a[208:211], v[116:119]\n" \
"s_addk_i32 s84, 0x1c00\n" \
"v_mfma_f32_16x16x32_bf16 v[120:123], a[180:183], a[144:147], 0\n" \
"buffer_load_dwordx4 v23, s[64:67], s84 offen offset:1024 lds\n" \
"v_mfma_f32_16x16x32_bf16 v[120:123], a[244:247], a[208:211], v[120:123]\n" \
"s_addk_i32 s84, 0x1c00\n" \
"v_mfma_f32_16x16x32_bf16 v[124:127], a[184:187], a[144:147], 0\n" \
"buffer_load_dwordx4 v22, s[64:67], s84 offen offset:2048 lds\n" \
"v_mfma_f32_16x16x32_bf16 v[124:127], a[248:251], a[208:211], v[124:127]\n" \
"s_addk_i32 s84, 0x1c00\n" \
"s_nop 0\n" \
"v_mfma_f32_16x16x32_bf16 v[128:131], a[188:191], a[144:147], 0\n" \
"buffer_load_dwordx4 v23, s[64:67], s84 offen offset:3072 lds\n" \
"v_mfma_f32_16x16x32_bf16 v[128:131], a[252:255], a[208:211], v[128:131]\n" \
"ds_read_b128 a[144:147], v28 offset:32768\n" \
"v_mfma_f32_16x16x32_bf16 v[144:147], a[188:191], a[148:151], 0\n" \
"ds_read_b128 a[208:211], v29 offset:32768\n" \
"v_mfma_f32_16x16x32_bf16 v[144:147], a[252:255], a[212:215], v[144:147]\n" \
"s_add_u32 m0, s31, 0x4000\n" \
"s_mov_b32 s84, 0x100000\n" \
"v_mfma_f32_16x16x32_bf16 v[140:143], a[184:187], a[148:151], 0\n" \
"buffer_load_dwordx4 v26, s[72:75], s84 offen offset:0 lds\n" \
"v_mfma_f32_16x16x32_bf16 v[140:143], a[248:251], a[212:215], v[140:143]\n" \
"ds_read_b64_tr_b16 a[230:231], v33 offset:41984\n" \
"v_mfma_f32_16x16x32_bf16 v[136:139], a[180:183], a[148:151], 0\n" \
"s_addk_i32 s84, 0x7c00\n" \
"s_nop 0\n" \
"v_mfma_f32_16x16x32_bf16 v[136:139], a[244:247], a[212:215], v[136:139]\n" \
"buffer_load_dwordx4 v26, s[72:75], s84 offen offset:1024 lds\n" \
"v_mfma_f32_16x16x32_bf16 v[132:135], a[176:179], a[148:151], 0\n" \
"ds_read_b64_tr_b16 a[168:169], v34 offset:32768\n" \
"v_mfma_f32_16x16x32_bf16 v[132:135], a[240:243], a[212:215], v[132:135]\n" \
"ds_read_b128 a[148:151], v28 offset:34816\n" \
".fill (L_pb + 3808 - .) / 4, 4, 0xBF800000\n" \
"L_pp1:\n" \
"s_cbranch_execz L_pret\n" \
"s_nop 0\n" \
"v_mfma_f32_16x16x32_bf16 v[148:151], a[176:179], a[152:155], 0\n" \
"ds_read_b128 a[212:215], v29 offset:34816\n" \
"v_mfma_f32_16x16x32_bf16 v[148:151], a[240:243], a[216:219], v[148:151]\n" \
"ds_read_b64_tr_b16 a[170:171], v34 offset:33792\n" \
"v_mfma_f32_16x16x32_bf16 v[152:155], a[180:183], a[152:155], 0\n" \
"ds_read_b64_tr_b16 a[232:233], v34 offset:40960\n" \
"v_mfma_f32_16x16x32_bf16 v[152:155], a[244:247], a[216:219], v[152:155]\n" \
"ds_read_b64_tr_b16 a[234:235], v34 offset:41984\n" \
"v_mfma_f32_16x16x32_bf16 v[156:159], a[184:187], a[152:155], 0\n" \
"ds_read_b64_tr_b16 a[172:173], v35 offset:32768\n" \
"v_mfma_f32_16x16x32_bf16 v[156:159], a[248:251], a[216:219], v[156:159]\n" \
"ds_read_b64_tr_b16 a[174:175], v35 offset:33792\n" \
"v_mfma_f32_16x16x32_bf16 v[160:163], a[188:191], a[152:155], 0\n" \
"ds_read_b64_tr_b16 a[236:237], v35 offset:40960\n" \
"v_mfma_f32_16x16x32_bf16 v[160:163], a[252:255], a[216:219], v[160:163]\n" \
"ds_read_b128 a[152:155], v28 offset:36864\n" \
"v_mfma_f32_16x16x32_bf16 v[176:179], a[188:191], a[156:159], 0\n" \
"ds_read_b128 a[216:219], v29 offset:36864\n" \
"v_mfma_f32_16x16x32_bf16 v[176:179], a[252:255], a[220:223], v[176:179]\n" \
"ds_read_b64_tr_b16 a[238:239], v35 offset:41984\n" \
"v_mfma_f32_16x16x32_bf16 v[172:175], a[184:187], a[156:159], 0\n" \
"s_cmp_lg_u32 s3, 0\n" \
"s_cselect_b32 s36, s37, s5\n" \
"v_mfma_f32_16x16x32_bf16 v[172:175], a[248:251], a[220:223], v[172:175]\n" \
"s_lshl_b32 s5, s6, 2\n" \
"s_or_b32 s5, s38, s5\n" \
"v_mfma_f32_16x16x32_bf16 v[168:171], a[180:183], a[156:159], 0\n" \
"s_lshl_b32 s35, s3, 10\n" \
"v_mfma_f32_16x16x32_bf16 v[168:171], a[244:247], a[220:223], v[168:171]\n" \
"s_cmpk_lt_u32 s3, 3\n" \
"s_cselect_b32 s37, s37, s5\n" \
"v_mfma_f32_16x16x32_bf16 v[164:167], a[176:179], a[156:159], 0\n" \
"s_lshl_b32 s5, s4, 16\n" \
"s_lshl_b32 s4, s4, 11\n" \
"v_mfma_f32_16x16x32_bf16 v[164:167], a[240:243], a[220:223], v[164:167]\n" \
"ds_read_b128 a[156:159], v28 offset:38912\n" \
"ds_read_b128 a[220:223], v29 offset:38912\n" \
"s_waitcnt lgkmcnt(15)\n" \
"v_mfma_f32_16x16x32_bf16 a[0:3], a[160:163], a[128:131], a[0:3]\n" \
"s_addk_i32 s84, 0x7c00\n" \
"v_mfma_f32_16x16x32_bf16 a[0:3], a[224:227], a[192:195], a[0:3]\n" \
"buffer_load_dwordx4 v27, s[72:75], s84 offen offset:2048 lds\n" \
"v_mfma_f32_16x16x32_bf16 a[4:7], a[164:167], a[128:131], a[4:7]\n" \
"ds_read_b64_tr_b16 a[176:177], v36 offset:32768\n" \
"v_cndmask_b32_e32 v198, v213, v212, vcc\n" \
"s_waitcnt lgkmcnt(15)\n" \
"v_mfma_f32_16x16x32_bf16 a[4:7], a[228:231], a[192:195], a[4:7]\n" \
"s_addk_i32 s84, 0x7c00\n" \
"s_waitcnt lgkmcnt(11)\n" \
"v_mfma_f32_16x16x32_bf16 a[8:11], a[168:171], a[128:131], a[8:11]\n" \
"buffer_load_dwordx4 v27, s[72:75], s84 offen offset:3072 lds\n" \
"s_waitcnt lgkmcnt(9)\n" \
"v_mfma_f32_16x16x32_bf16 a[8:11], a[232:235], a[192:195], a[8:11]\n" \
"ds_read_b64_tr_b16 a[178:179], v36 offset:33792\n" \
"v_or_b32_e32 v212, s35, v20\n" \
"s_waitcnt lgkmcnt(8)\n" \
"v_mfma_f32_16x16x32_bf16 a[12:15], a[172:175], a[128:131], a[12:15]\n" \
"ds_read_b64_tr_b16 a[240:241], v36 offset:40960\n" \
"s_or_b32 s39, s40, s35\n" \
"s_waitcnt lgkmcnt(5)\n" \
"v_mfma_f32_16x16x32_bf16 a[12:15], a[236:239], a[192:195], a[12:15]\n" \
"s_or_b32 s55, s41, s4\n" \
"v_or_b32_e32 v199, 0x22000, v212\n" \
"v_mfma_f32_16x16x32_bf16 a[28:31], a[172:175], a[132:135], a[28:31]\n" \
"s_add_i32 s35, s39, s7\n" \
"s_add_i32 s54, s41, s5\n" \
"v_mfma_f32_16x16x32_bf16 a[28:31], a[236:239], a[196:199], a[28:31]\n" \
"ds_read_b64_tr_b16 a[242:243], v36 offset:41984\n" \
"s_add_i32 s55, s55, 0x80000\n" \
"v_mfma_f32_16x16x32_bf16 a[24:27], a[168:171], a[132:135], a[24:27]\n" \
"v_mov_b32_e32 v215, 0xffff\n" \
"s_and_b32 s4, s3, 1\n" \
"v_mfma_f32_16x16x32_bf16 a[24:27], a[232:235], a[196:199], a[24:27]\n" \
"s_lshl_b32 s5, s4, 4\n" \
"v_lshrrev_b32_e32 v212, 4, v20\n" \
"v_mfma_f32_16x16x32_bf16 a[20:23], a[164:167], a[132:135], a[20:23]\n" \
"v_lshrrev_b32_e32 v218, 6, v20\n" \
"v_and_b32_e32 v219, 12, v218\n" \
"v_mfma_f32_16x16x32_bf16 a[20:23], a[228:231], a[196:199], a[20:23]\n" \
"ds_read_b64_tr_b16 a[180:181], v37 offset:32768\n" \
"v_and_or_b32 v214, v212, 15, s5\n" \
"v_mfma_f32_16x16x32_bf16 a[16:19], a[160:163], a[132:135], a[16:19]\n" \
"v_cmp_gt_u32_e64 vcc, v219, v214\n" \
"v_mov_b32_e32 v220, 0xffff0000\n" \
"v_mfma_f32_16x16x32_bf16 a[16:19], a[224:227], a[196:199], a[16:19]\n" \
"s_lshl_b32 s5, s2, 2\n" \
"v_cndmask_b32_e64 v217, v215, 0, vcc\n" \
"v_mfma_f32_16x16x32_bf16 a[32:35], a[160:163], a[136:139], a[32:35]\n" \
"s_mov_b32 m0, s32\n" \
"ds_read_b64_tr_b16 a[182:183], v37 offset:33792\n" \
"v_mfma_f32_16x16x32_bf16 a[32:35], a[224:227], a[200:203], a[32:35]\n" \
"s_mov_b32 exec_lo, 0x0\n" \
"s_mov_b32 exec_hi, 0xff\n" \
"global_load_lds_dword v[184:185], off\n" \
"s_mov_b64 exec, -1\n" \
"v_mfma_f32_16x16x32_bf16 a[36:39], a[164:167], a[136:139], a[36:39]\n" \
"v_cmp_lt_u32_e32 vcc, v219, v214\n" \
"v_mov_b32_e32 v213, s23\n" \
"v_mfma_f32_16x16x32_bf16 a[36:39], a[228:231], a[200:203], a[36:39]\n" \
"ds_read_b64_tr_b16 a[244:245], v37 offset:40960\n" \
"s_or_b32 s5, s5, s3\n" \
"v_mfma_f32_16x16x32_bf16 a[40:43], a[168:171], a[136:139], a[40:43]\n" \
"v_cndmask_b32_e32 v221, 0, v220, vcc\n" \
"v_or_b32_e64 v12, v217, v221\n" \
"v_mfma_f32_16x16x32_bf16 a[40:43], a[232:235], a[200:203], a[40:43]\n" \
"s_mov_b32 m0, s32\n" \
"v_or_b32_e32 v217, 2, v219\n" \
"v_mfma_f32_16x16x32_bf16 a[44:47], a[172:175], a[136:139], a[44:47]\n" \
"s_mov_b32 exec_lo, 0xffff0000\n" \
"s_mov_b32 exec_hi, 0x0\n" \
"global_load_lds_dword v[186:187], off\n" \
"s_mov_b64 exec, -1\n" \
"v_mfma_f32_16x16x32_bf16 a[44:47], a[236:239], a[200:203], a[44:47]\n" \
"ds_read_b64_tr_b16 a[246:247], v37 offset:41984\n" \
"v_cmp_gt_u32_e64 vcc, v217, v214\n" \
"v_mfma_f32_16x16x32_bf16 a[60:63], a[172:175], a[140:143], a[60:63]\n" \
"v_mov_b32_e32 v216, s25\n" \
"s_movk_i32 s40, 0xf400\n" \
"v_mfma_f32_16x16x32_bf16 a[60:63], a[236:239], a[204:207], a[60:63]\n" \
"v_cndmask_b32_e64 v221, v215, 0, vcc\n" \
"v_cmp_lt_u32_e64 vcc, v217, v214\n" \
"v_mfma_f32_16x16x32_bf16 a[56:59], a[168:171], a[140:143], a[56:59]\n" \
"ds_read_b64_tr_b16 a[184:185], v38 offset:32768\n" \
"s_lshl_b32 s4, s4, 6\n" \
"v_mfma_f32_16x16x32_bf16 a[56:59], a[232:235], a[204:207], a[56:59]\n" \
"v_cndmask_b32_e32 v217, 0, v220, vcc\n" \
"v_or_b32_e64 v13, v221, v217\n" \
"v_mfma_f32_16x16x32_bf16 a[52:55], a[164:167], a[140:143], a[52:55]\n" \
"v_or_b32_e32 v217, 16, v219\n" \
"v_cmp_gt_u32_e32 vcc, v217, v214\n" \
"v_mfma_f32_16x16x32_bf16 a[52:55], a[228:231], a[204:207], a[52:55]\n" \
"ds_read_b64_tr_b16 a[186:187], v38 offset:33792\n" \
"v_cndmask_b32_e64 v221, v215, 0, vcc\n" \
"v_mfma_f32_16x16x32_bf16 a[48:51], a[160:163], a[140:143], a[48:51]\n" \
"v_cmp_lt_u32_e32 vcc, v217, v214\n" \
"s_nop 1\n" \
"v_cndmask_b32_e64 v217, 0, v220, vcc\n" \
"v_mfma_f32_16x16x32_bf16 a[48:51], a[224:227], a[204:207], a[48:51]\n" \
"v_or_b32_e32 v14, v221, v217\n" \
"v_or_b32_e32 v217, 18, v219\n" \
"v_mfma_f32_16x16x32_bf16 a[64:67], a[160:163], a[144:147], a[64:67]\n" \
"ds_read_b64_tr_b16 a[248:249], v38 offset:40960\n" \
"v_cmp_gt_u32_e64 vcc, v217, v214\n" \
"v_mfma_f32_16x16x32_bf16 a[64:67], a[224:227], a[208:211], a[64:67]\n" \
"s_nop 0\n" \
"v_cndmask_b32_e64 v215, v215, 0, vcc\n" \
"v_cmp_lt_u32_e32 vcc, v217, v214\n" \
"v_mfma_f32_16x16x32_bf16 a[68:71], a[164:167], a[144:147], a[68:71]\n" \
"s_nop 0\n" \
"v_cndmask_b32_e32 v214, 0, v220, vcc\n" \
"v_or_b32_e64 v15, v215, v214\n" \
"v_mfma_f32_16x16x32_bf16 a[68:71], a[228:231], a[208:211], a[68:71]\n" \
"ds_read_b64_tr_b16 a[250:251], v38 offset:41984\n" \
"v_mad_u64_u32 v[214:215], s[6:7], s5, 42, v[212:213]\n" \
"v_mfma_f32_16x16x32_bf16 a[72:75], a[168:171], a[144:147], a[72:75]\n" \
"s_movk_i32 s5, 0x1440\n" \
"v_cmp_gt_u32_e32 vcc, s5, v214\n" \
"v_mfma_f32_16x16x32_bf16 a[72:75], a[232:235], a[208:211], a[72:75]\n" \
"v_min_u32_e32 v217, 0xa43f, v214\n" \
"v_mov_b32_e64 v214, s24\n" \
"v_mfma_f32_16x16x32_bf16 a[76:79], a[172:175], a[144:147], a[76:79]\n" \
"v_cndmask_b32_e32 v215, v216, v213, vcc\n" \
"v_mov_b32_e32 v213, s22\n" \
"v_mfma_f32_16x16x32_bf16 a[76:79], a[236:239], a[208:211], a[76:79]\n" \
"ds_read_b64_tr_b16 a[188:189], v39 offset:32768\n" \
"s_movk_i32 s6, 0xffee\n" \
"v_mfma_f32_16x16x32_bf16 a[92:95], a[172:175], a[148:151], a[92:95]\n" \
"v_cndmask_b32_e32 v214, v214, v213, vcc\n" \
"v_add_u32_e32 v213, 0xffffebc0, v217\n" \
"v_mfma_f32_16x16x32_bf16 a[92:95], a[236:239], a[212:215], a[92:95]\n" \
"s_lshl_b32 s5, s2, 5\n" \
"v_cndmask_b32_e32 v213, v213, v217, vcc\n" \
"v_mfma_f32_16x16x32_bf16 a[88:91], a[168:171], a[148:151], a[88:91]\n" \
"ds_read_b64_tr_b16 a[190:191], v39 offset:33792\n" \
"v_lshlrev_b32_e64 v216, 7, v213\n" \
"v_mfma_f32_16x16x32_bf16 a[88:91], a[232:235], a[212:215], a[88:91]\n" \
"v_mul_u32_u24_e32 v213, 0xe39, v212\n" \
"v_mov_b32_e64 v217, 0\n" \
"v_mfma_f32_16x16x32_bf16 a[84:87], a[164:167], a[148:151], a[84:87]\n" \
"v_lshrrev_b32_e64 v213, 16, v213\n" \
"v_lshl_add_u64 v[16:17], v[214:215], 0, v[216:217]\n" \
"v_mfma_f32_16x16x32_bf16 a[84:87], a[228:231], a[212:215], a[84:87]\n" \
"ds_read_b64_tr_b16 a[252:253], v39 offset:40960\n" \
"v_mad_i32_i24 v214, v213, s6, v212\n" \
"v_mfma_f32_16x16x32_bf16 a[80:83], a[160:163], a[148:151], a[80:83]\n" \
"s_add_i32 s5, s5, 32\n" \
"v_lshlrev_b32_e32 v215, 4, v214\n" \
"v_mfma_f32_16x16x32_bf16 a[80:83], a[224:227], a[212:215], a[80:83]\n" \
"v_add_u32_e64 v216, s5, v215\n" \
"v_and_b32_e32 v216, 0xf0, v216\n" \
"v_mfma_f32_16x16x32_bf16 a[96:99], a[160:163], a[152:155], a[96:99]\n" \
"ds_read_b64_tr_b16 a[254:255], v39 offset:41984\n" \
"v_cmp_gt_u32_e64 vcc, 16, v214\n" \
"v_mfma_f32_16x16x32_bf16 a[96:99], a[224:227], a[216:219], a[96:99]\n" \
"s_movk_i32 s7, 0xf800\n" \
"v_cndmask_b32_e32 v214, v215, v216, vcc\n" \
"v_mfma_f32_16x16x32_bf16 a[100:103], a[164:167], a[152:155], a[100:103]\n" \
"v_or_b32_e64 v215, 64, v212\n" \
"v_mul_u32_u24_e32 v216, 0xe39, v215\n" \
"v_mfma_f32_16x16x32_bf16 a[100:103], a[228:231], a[216:219], a[100:103]\n" \
"v_lshrrev_b32_e64 v216, 16, v216\n" \
"v_mad_i32_i24 v215, v216, s6, v215\n" \
"v_mfma_f32_16x16x32_bf16 a[104:107], a[168:171], a[152:155], a[104:107]\n" \
"v_lshlrev_b32_e32 v217, 4, v215\n" \
"v_add_u32_e32 v220, s5, v217\n" \
"v_mfma_f32_16x16x32_bf16 a[104:107], a[232:235], a[216:219], a[104:107]\n" \
"v_and_b32_e32 v220, 0xf0, v220\n" \
"v_cmp_gt_u32_e64 vcc, 16, v215\n" \
"v_mfma_f32_16x16x32_bf16 a[108:111], a[172:175], a[152:155], a[108:111]\n" \
"s_nop 0\n" \
"v_cndmask_b32_e32 v215, v217, v220, vcc\n" \
"v_or_b32_e32 v217, 0x80, v212\n" \
"v_mfma_f32_16x16x32_bf16 a[108:111], a[236:239], a[216:219], a[108:111]\n" \
"v_mul_u32_u24_e32 v220, 0xe39, v217\n" \
"v_lshrrev_b32_e32 v220, 16, v220\n" \
"s_waitcnt lgkmcnt(15)\n" \
"v_mfma_f32_16x16x32_bf16 a[124:127], a[172:175], a[156:159], a[124:127]\n" \
"v_mad_i32_i24 v217, v220, s6, v217\n" \
"v_lshlrev_b32_e64 v221, 4, v217\n" \
"v_mfma_f32_16x16x32_bf16 a[124:127], a[236:239], a[220:223], a[124:127]\n" \
"v_add_u32_e64 v222, s5, v221\n" \
"v_and_b32_e32 v222, 0xf0, v222\n" \
"v_mfma_f32_16x16x32_bf16 a[120:123], a[168:171], a[156:159], a[120:123]\n" \
"v_cmp_gt_u32_e32 vcc, 16, v217\n" \
"s_nop 1\n" \
"v_cndmask_b32_e64 v217, v221, v222, vcc\n" \
"v_mfma_f32_16x16x32_bf16 a[120:123], a[232:235], a[220:223], a[120:123]\n" \
"v_or_b32_e32 v221, 0xc0, v212\n" \
"v_mul_u32_u24_e32 v222, 0xe39, v221\n" \
"v_mfma_f32_16x16x32_bf16 a[116:119], a[164:167], a[156:159], a[116:119]\n" \
"v_lshrrev_b32_e64 v222, 16, v222\n" \
"v_mad_i32_i24 v221, v222, s6, v221\n" \
"v_mfma_f32_16x16x32_bf16 a[116:119], a[228:231], a[220:223], a[116:119]\n" \
"v_lshlrev_b32_e32 v223, 4, v221\n" \
"v_add_u32_e32 v224, s5, v223\n" \
"v_mfma_f32_16x16x32_bf16 a[112:115], a[160:163], a[156:159], a[112:115]\n" \
"v_and_b32_e32 v224, 0xf0, v224\n" \
"v_cmp_gt_u32_e64 vcc, 16, v221\n" \
"v_mfma_f32_16x16x32_bf16 a[112:115], a[224:227], a[220:223], a[112:115]\n" \
"s_waitcnt vmcnt(20)\n" \
"v_readfirstlane_b32 s62, v180\n" \
"s_nop 3\n" \
"s_add_u32 s62, s62, 0x9E3779B1\n" \
"s_waitcnt vmcnt(2) lgkmcnt(0)\n" \
"s_barrier\n" \
"v_mfma_f32_16x16x32_bf16 v[52:55], a[176:179], a[128:131], v[52:55]\n" \
"s_add_u32 m0, s30, 0x8000\n" \
"s_movk_i32 s84, 0x180\n" \
"v_mfma_f32_16x16x32_bf16 v[52:55], a[240:243], a[192:195], v[52:55]\n" \
"buffer_load_dwordx4 v22, s[64:67], s84 offen offset:0 lds\n" \
"v_mfma_f32_16x16x32_bf16 v[56:59], a[180:183], a[128:131], v[56:59]\n" \
"ds_read_b64_tr_b16 a[160:161], v32 offset:0\n" \
"v_cndmask_b32_e64 v221, v223, v224, vcc\n" \
"v_mfma_f32_16x16x32_bf16 v[56:59], a[244:247], a[192:195], v[56:59]\n" \
"s_movk_i32 s84, 0x1d80\n" \
"v_mfma_f32_16x16x32_bf16 v[60:63], a[184:187], a[128:131], v[60:63]\n" \
"buffer_load_dwordx4 v23, s[64:67], s84 offen offset:1024 lds\n" \
"v_mfma_f32_16x16x32_bf16 v[60:63], a[248:251], a[192:195], v[60:63]\n" \
"ds_read_b64_tr_b16 a[162:163], v32 offset:1024\n" \
"v_or_b32_e32 v223, 0x100, v212\n" \
"v_mfma_f32_16x16x32_bf16 v[64:67], a[188:191], a[128:131], v[64:67]\n" \
"ds_read_b64_tr_b16 a[224:225], v32 offset:8192\n" \
"v_mul_u32_u24_e32 v224, 0xe39, v223\n" \
"v_mfma_f32_16x16x32_bf16 v[64:67], a[252:255], a[192:195], v[64:67]\n" \
"ds_read_b128 a[128:131], v28 offset:16384\n" \
"v_lshrrev_b32_e32 v224, 16, v224\n" \
"v_mfma_f32_16x16x32_bf16 v[80:83], a[188:191], a[132:135], v[80:83]\n" \
"ds_read_b128 a[192:195], v29 offset:16384\n" \
"v_mad_i32_i24 v223, v224, s6, v223\n" \
"v_mfma_f32_16x16x32_bf16 v[80:83], a[252:255], a[196:199], v[80:83]\n" \
"s_movk_i32 s84, 0x3980\n" \
"v_mfma_f32_16x16x32_bf16 v[76:79], a[184:187], a[132:135], v[76:79]\n" \
"buffer_load_dwordx4 v22, s[64:67], s84 offen offset:2048 lds\n" \
"v_mfma_f32_16x16x32_bf16 v[76:79], a[248:251], a[196:199], v[76:79]\n" \
"s_movk_i32 s84, 0x5580\n" \
"v_mfma_f32_16x16x32_bf16 v[72:75], a[180:183], a[132:135], v[72:75]\n" \
"buffer_load_dwordx4 v23, s[64:67], s84 offen offset:3072 lds\n" \
"v_mfma_f32_16x16x32_bf16 v[72:75], a[244:247], a[196:199], v[72:75]\n" \
"s_add_u32 m0, s31, 0x8000\n" \
"s_mov_b32 s84, 0x180000\n" \
"v_mfma_f32_16x16x32_bf16 v[68:71], a[176:179], a[132:135], v[68:71]\n" \
"buffer_load_dwordx4 v26, s[68:71], s84 offen offset:0 lds\n" \
"v_mfma_f32_16x16x32_bf16 v[68:71], a[240:243], a[196:199], v[68:71]\n" \
"ds_read_b128 a[132:135], v28 offset:18432\n" \
"v_lshlrev_b32_e64 v225, 4, v223\n" \
"v_mfma_f32_16x16x32_bf16 v[84:87], a[176:179], a[136:139], v[84:87]\n" \
"ds_read_b128 a[196:199], v29 offset:18432\n" \
"v_add_u32_e64 v226, s5, v225\n" \
"v_mfma_f32_16x16x32_bf16 v[84:87], a[240:243], a[200:203], v[84:87]\n" \
"s_addk_i32 s84, 0x7c00\n" \
"v_mfma_f32_16x16x32_bf16 v[88:91], a[180:183], a[136:139], v[88:91]\n" \
"buffer_load_dwordx4 v26, s[68:71], s84 offen offset:1024 lds\n" \
"v_mfma_f32_16x16x32_bf16 v[88:91], a[244:247], a[200:203], v[88:91]\n" \
"ds_read_b64_tr_b16 a[226:227], v32 offset:9216\n" \
"v_and_b32_e32 v226, 0xf0, v226\n" \
"v_mfma_f32_16x16x32_bf16 v[92:95], a[184:187], a[136:139], v[92:95]\n" \
"s_addk_i32 s84, 0x7c00\n" \
"v_mfma_f32_16x16x32_bf16 v[92:95], a[248:251], a[200:203], v[92:95]\n" \
"buffer_load_dwordx4 v27, s[68:71], s84 offen offset:2048 lds\n" \
"v_mfma_f32_16x16x32_bf16 v[96:99], a[188:191], a[136:139], v[96:99]\n" \
"ds_read_b64_tr_b16 a[164:165], v33 offset:0\n" \
"v_cmp_gt_u32_e64 vcc, 16, v223\n" \
"v_mfma_f32_16x16x32_bf16 v[96:99], a[252:255], a[200:203], v[96:99]\n" \
"ds_read_b128 a[136:139], v28 offset:20480\n" \
"v_cndmask_b32_e64 v223, v225, v226, vcc\n" \
"v_mfma_f32_16x16x32_bf16 v[112:115], a[188:191], a[140:143], v[112:115]\n" \
"ds_read_b128 a[200:203], v29 offset:20480\n" \
"v_or_b32_e32 v225, 0x140, v212\n" \
"v_mfma_f32_16x16x32_bf16 v[112:115], a[252:255], a[204:207], v[112:115]\n" \
"s_addk_i32 s84, 0x7c00\n" \
"s_nop 0\n" \
"v_mfma_f32_16x16x32_bf16 v[108:111], a[184:187], a[140:143], v[108:111]\n" \
"buffer_load_dwordx4 v27, s[68:71], s84 offen offset:3072 lds\n" \
"v_mfma_f32_16x16x32_bf16 v[108:111], a[248:251], a[204:207], v[108:111]\n" \
"s_add_u32 m0, s30, 0x9000\n" \
"s_mov_b32 s84, 0x8180\n" \
"v_mfma_f32_16x16x32_bf16 v[104:107], a[180:183], a[140:143], v[104:107]\n" \
"buffer_load_dwordx4 v22, s[64:67], s84 offen offset:0 lds\n" \
"v_mfma_f32_16x16x32_bf16 v[104:107], a[244:247], a[204:207], v[104:107]\n" \
"ds_read_b64_tr_b16 a[166:167], v33 offset:1024\n" \
"v_mul_u32_u24_e32 v226, 0xe39, v225\n" \
"v_mfma_f32_16x16x32_bf16 v[100:103], a[176:179], a[140:143], v[100:103]\n" \
"ds_read_b64_tr_b16 a[228:229], v33 offset:8192\n" \
"v_lshrrev_b32_e64 v226, 16, v226\n" \
"v_mfma_f32_16x16x32_bf16 v[100:103], a[240:243], a[204:207], v[100:103]\n" \
"ds_read_b128 a[140:143], v28 offset:22528\n" \
"v_mad_i32_i24 v225, v226, s6, v225\n" \
"v_mfma_f32_16x16x32_bf16 v[116:119], a[176:179], a[144:147], v[116:119]\n" \
"ds_read_b128 a[204:207], v29 offset:22528\n" \
"v_lshlrev_b32_e64 v227, 4, v225\n" \
"v_mfma_f32_16x16x32_bf16 v[116:119], a[240:243], a[208:211], v[116:119]\n" \
"s_addk_i32 s84, 0x1c00\n" \
"v_mfma_f32_16x16x32_bf16 v[120:123], a[180:183], a[144:147], v[120:123]\n" \
"buffer_load_dwordx4 v23, s[64:67], s84 offen offset:1024 lds\n" \
"v_mfma_f32_16x16x32_bf16 v[120:123], a[244:247], a[208:211], v[120:123]\n" \
"s_addk_i32 s84, 0x1c00\n" \
"v_mfma_f32_16x16x32_bf16 v[124:127], a[184:187], a[144:147], v[124:127]\n" \
"buffer_load_dwordx4 v22, s[64:67], s84 offen offset:2048 lds\n" \
"v_mfma_f32_16x16x32_bf16 v[124:127], a[248:251], a[208:211], v[124:127]\n" \
"s_addk_i32 s84, 0x1c00\n" \
"v_mfma_f32_16x16x32_bf16 v[128:131], a[188:191], a[144:147], v[128:131]\n" \
"buffer_load_dwordx4 v23, s[64:67], s84 offen offset:3072 lds\n" \
"v_mfma_f32_16x16x32_bf16 v[128:131], a[252:255], a[208:211], v[128:131]\n" \
"ds_read_b128 a[144:147], v28 offset:0\n" \
"v_add_u32_e32 v228, s5, v227\n" \
"v_mfma_f32_16x16x32_bf16 v[144:147], a[188:191], a[148:151], v[144:147]\n" \
"ds_read_b128 a[208:211], v29 offset:0\n" \
"v_and_b32_e32 v228, 0xf0, v228\n" \
"v_mfma_f32_16x16x32_bf16 v[144:147], a[252:255], a[212:215], v[144:147]\n" \
"s_add_u32 m0, s31, 0xc000\n" \
"s_mov_b32 s84, 0x180000\n" \
"v_mfma_f32_16x16x32_bf16 v[140:143], a[184:187], a[148:151], v[140:143]\n" \
"buffer_load_dwordx4 v26, s[72:75], s84 offen offset:0 lds\n" \
"v_mfma_f32_16x16x32_bf16 v[140:143], a[248:251], a[212:215], v[140:143]\n" \
"ds_read_b64_tr_b16 a[230:231], v33 offset:9216\n" \
"v_cmp_gt_u32_e64 vcc, 16, v225\n" \
"v_mfma_f32_16x16x32_bf16 v[136:139], a[180:183], a[148:151], v[136:139]\n" \
"s_addk_i32 s84, 0x7c00\n" \
"v_mfma_f32_16x16x32_bf16 v[136:139], a[244:247], a[212:215], v[136:139]\n" \
"buffer_load_dwordx4 v26, s[72:75], s84 offen offset:1024 lds\n" \
"v_mfma_f32_16x16x32_bf16 v[132:135], a[176:179], a[148:151], v[132:135]\n" \
"ds_read_b64_tr_b16 a[168:169], v34 offset:0\n" \
"v_cndmask_b32_e32 v225, v227, v228, vcc\n" \
"v_mfma_f32_16x16x32_bf16 v[132:135], a[240:243], a[212:215], v[132:135]\n" \
"ds_read_b128 a[148:151], v28 offset:2048\n" \
"v_or_b32_e32 v227, 0x180, v212\n" \
"v_mfma_f32_16x16x32_bf16 v[148:151], a[176:179], a[152:155], v[148:151]\n" \
"ds_read_b128 a[212:215], v29 offset:2048\n" \
"v_mul_u32_u24_e32 v228, 0xe39, v227\n" \
"v_mfma_f32_16x16x32_bf16 v[148:151], a[240:243], a[216:219], v[148:151]\n" \
"ds_read_b64_tr_b16 a[170:171], v34 offset:1024\n" \
"v_lshrrev_b32_e64 v228, 16, v228\n" \
"v_mfma_f32_16x16x32_bf16 v[152:155], a[180:183], a[152:155], v[152:155]\n" \
"ds_read_b64_tr_b16 a[232:233], v34 offset:8192\n" \
"v_mad_i32_i24 v227, v228, s6, v227\n" \
"v_mfma_f32_16x16x32_bf16 v[152:155], a[244:247], a[216:219], v[152:155]\n" \
"ds_read_b64_tr_b16 a[234:235], v34 offset:9216\n" \
"v_lshlrev_b32_e64 v229, 4, v227\n" \
"v_mfma_f32_16x16x32_bf16 v[156:159], a[184:187], a[152:155], v[156:159]\n" \
"ds_read_b64_tr_b16 a[172:173], v35 offset:0\n" \
"v_add_u32_e64 v230, s5, v229\n" \
"v_mfma_f32_16x16x32_bf16 v[156:159], a[248:251], a[216:219], v[156:159]\n" \
"ds_read_b64_tr_b16 a[174:175], v35 offset:1024\n" \
"v_and_b32_e32 v230, 0xf0, v230\n" \
"v_mfma_f32_16x16x32_bf16 v[160:163], a[188:191], a[152:155], v[160:163]\n" \
"ds_read_b64_tr_b16 a[236:237], v35 offset:8192\n" \
"v_cmp_gt_u32_e64 vcc, 16, v227\n" \
"v_mfma_f32_16x16x32_bf16 v[160:163], a[252:255], a[216:219], v[160:163]\n" \
"ds_read_b128 a[152:155], v28 offset:4096\n" \
"v_cndmask_b32_e64 v227, v229, v230, vcc\n" \
"v_mfma_f32_16x16x32_bf16 v[176:179], a[188:191], a[156:159], v[176:179]\n" \
"ds_read_b128 a[216:219], v29 offset:4096\n" \
"v_or_b32_e32 v229, 0x1c0, v212\n" \
"v_mfma_f32_16x16x32_bf16 v[176:179], a[252:255], a[220:223], v[176:179]\n" \
"ds_read_b64_tr_b16 a[238:239], v35 offset:9216\n" \
"v_mul_u32_u24_e32 v230, 0xe39, v229\n" \
"v_mfma_f32_16x16x32_bf16 v[172:175], a[184:187], a[156:159], v[172:175]\n" \
"v_lshrrev_b32_e64 v230, 16, v230\n" \
"v_mad_i32_i24 v229, v230, s6, v229\n" \
"v_mfma_f32_16x16x32_bf16 v[172:175], a[248:251], a[220:223], v[172:175]\n" \
"v_lshlrev_b32_e32 v231, 4, v229\n" \
"v_add_u32_e32 v232, s5, v231\n" \
"v_mfma_f32_16x16x32_bf16 v[168:171], a[180:183], a[156:159], v[168:171]\n" \
"v_and_b32_e32 v232, 0xf0, v232\n" \
"v_cmp_gt_u32_e64 vcc, 16, v229\n" \
"v_mfma_f32_16x16x32_bf16 v[168:171], a[244:247], a[220:223], v[168:171]\n" \
"s_nop 0\n" \
"v_cndmask_b32_e32 v229, v231, v232, vcc\n" \
"v_or_b32_e32 v231, 0x200, v212\n" \
"v_mfma_f32_16x16x32_bf16 v[164:167], a[176:179], a[156:159], v[164:167]\n" \
"v_mul_u32_u24_e32 v232, 0xe39, v231\n" \
"v_lshrrev_b32_e64 v232, 16, v232\n" \
"v_mfma_f32_16x16x32_bf16 v[164:167], a[240:243], a[220:223], v[164:167]\n" \
"ds_read_b128 a[156:159], v28 offset:6144\n" \
"ds_read_b128 a[220:223], v29 offset:6144\n" \
"s_waitcnt lgkmcnt(15)\n" \
"v_mfma_f32_16x16x32_bf16 a[0:3], a[160:163], a[128:131], a[0:3]\n" \
"s_addk_i32 s84, 0x7c00\n" \
"v_mfma_f32_16x16x32_bf16 a[0:3], a[224:227], a[192:195], a[0:3]\n" \
"buffer_load_dwordx4 v27, s[72:75], s84 offen offset:2048 lds\n" \
"v_mfma_f32_16x16x32_bf16 a[4:7], a[164:167], a[128:131], a[4:7]\n" \
"ds_read_b64_tr_b16 a[176:177], v36 offset:0\n" \
"v_mad_i32_i24 v231, v232, s6, v231\n" \
"s_waitcnt lgkmcnt(15)\n" \
"v_mfma_f32_16x16x32_bf16 a[4:7], a[228:231], a[192:195], a[4:7]\n" \
"s_addk_i32 s84, 0x7c00\n" \
"s_waitcnt lgkmcnt(11)\n" \
"v_mfma_f32_16x16x32_bf16 a[8:11], a[168:171], a[128:131], a[8:11]\n" \
"buffer_load_dwordx4 v27, s[72:75], s84 offen offset:3072 lds\n" \
"s_waitcnt lgkmcnt(9)\n" \
"v_mfma_f32_16x16x32_bf16 a[8:11], a[232:235], a[192:195], a[8:11]\n" \
"ds_read_b64_tr_b16 a[178:179], v36 offset:1024\n" \
"v_lshlrev_b32_e32 v233, 4, v231\n" \
"s_waitcnt lgkmcnt(8)\n" \
"v_mfma_f32_16x16x32_bf16 a[12:15], a[172:175], a[128:131], a[12:15]\n" \
"ds_read_b64_tr_b16 a[240:241], v36 offset:8192\n" \
"v_add_u32_e32 v234, s5, v233\n" \
"s_waitcnt lgkmcnt(5)\n" \
"v_mfma_f32_16x16x32_bf16 a[12:15], a[236:239], a[192:195], a[12:15]\n" \
"s_movk_i32 s5, 0x480\n" \
"v_mad_u32_u24 v0, v213, s5, v214\n" \
"v_mfma_f32_16x16x32_bf16 a[28:31], a[172:175], a[132:135], a[28:31]\n" \
"v_mul_u32_u24_e32 v213, 0x480, v216\n" \
"s_movk_i32 s6, 0xfc00\n" \
"v_mfma_f32_16x16x32_bf16 a[28:31], a[236:239], a[196:199], a[28:31]\n" \
"ds_read_b64_tr_b16 a[242:243], v36 offset:9216\n" \
"v_add3_u32 v1, v213, v215, s6\n" \
"v_mfma_f32_16x16x32_bf16 a[24:27], a[168:171], a[132:135], a[24:27]\n" \
"buffer_load_dwordx4 v[200:203], v192, s[76:79], s34 offen offset:0\n" \
"v_mfma_f32_16x16x32_bf16 a[24:27], a[232:235], a[196:199], a[24:27]\n" \
"v_mul_u32_u24_e32 v213, 0x480, v220\n" \
"v_add3_u32 v2, v213, v217, s7\n" \
"v_mfma_f32_16x16x32_bf16 a[20:23], a[164:167], a[132:135], a[20:23]\n" \
"v_mul_u32_u24_e32 v213, 0x480, v222\n" \
"v_add3_u32 v3, v213, v221, s40\n" \
"v_mfma_f32_16x16x32_bf16 a[20:23], a[228:231], a[196:199], a[20:23]\n" \
"ds_read_b64_tr_b16 a[180:181], v37 offset:0\n" \
"v_mul_u32_u24_e32 v213, 0x480, v226\n" \
"v_mfma_f32_16x16x32_bf16 a[16:19], a[160:163], a[132:135], a[16:19]\n" \
"v_add3_u32 v5, v213, v225, s6\n" \
"v_mul_u32_u24_e32 v213, 0x480, v228\n" \
"v_mfma_f32_16x16x32_bf16 a[16:19], a[224:227], a[196:199], a[16:19]\n" \
"v_add3_u32 v6, v213, v227, s7\n" \
"v_mul_u32_u24_e32 v213, 0x480, v230\n" \
"v_mfma_f32_16x16x32_bf16 a[32:35], a[160:163], a[136:139], a[32:35]\n" \
"buffer_load_dwordx4 v[204:207], v193, s[76:79], s34 offen offset:0\n" \
"v_mfma_f32_16x16x32_bf16 a[32:35], a[224:227], a[200:203], a[32:35]\n" \
"s_mov_b32 m0, s32\n" \
"ds_read_b64_tr_b16 a[182:183], v37 offset:1024\n" \
"v_mfma_f32_16x16x32_bf16 a[36:39], a[164:167], a[136:139], a[36:39]\n" \
"s_mov_b32 exec_lo, 0x0\n" \
"s_mov_b32 exec_hi, 0xff00\n" \
"global_load_lds_dword v[184:185], off\n" \
"s_mov_b64 exec, -1\n" \
"v_mfma_f32_16x16x32_bf16 a[36:39], a[228:231], a[200:203], a[36:39]\n" \
"ds_read_b64_tr_b16 a[244:245], v37 offset:8192\n" \
"v_add3_u32 v7, v213, v229, s40\n" \
"v_mfma_f32_16x16x32_bf16 a[40:43], a[168:171], a[136:139], a[40:43]\n" \
"s_lshl_b32 s7, s3, 3\n" \
"s_lshl_b32 s40, s3, 1\n" \
"v_mfma_f32_16x16x32_bf16 a[40:43], a[232:235], a[200:203], a[40:43]\n" \
"buffer_load_dword v208, v194, s[76:79], s34 offen offset:32\n" \
"v_mfma_f32_16x16x32_bf16 a[44:47], a[172:175], a[136:139], a[44:47]\n" \
"ds_read_b64_tr_b16 a[246:247], v37 offset:9216\n" \
"s_and_b32 s7, s7, 16\n" \
"v_mfma_f32_16x16x32_bf16 a[44:47], a[236:239], a[200:203], a[44:47]\n" \
"s_and_b32 s40, s40, 2\n" \
"s_and_b32 s6, s2, 7\n" \
"v_mfma_f32_16x16x32_bf16 a[60:63], a[172:175], a[140:143], a[60:63]\n" \
"v_bfe_u32 v213, v20, 7, 1\n" \
"s_or_b32 s7, s40, s7\n" \
"v_mfma_f32_16x16x32_bf16 a[60:63], a[236:239], a[204:207], a[60:63]\n" \
"v_and_b32_e32 v234, 0xf0, v234\n" \
"v_cmp_gt_u32_e32 vcc, 16, v231\n" \
"v_mfma_f32_16x16x32_bf16 a[56:59], a[168:171], a[140:143], a[56:59]\n" \
"buffer_load_dword v209, v194, s[76:79], s34 offen offset:1184\n" \
"v_mfma_f32_16x16x32_bf16 a[56:59], a[232:235], a[204:207], a[56:59]\n" \
"ds_read_b64_tr_b16 a[184:185], v38 offset:0\n" \
"v_or3_b32 v213, s7, v213, v219\n" \
"v_mfma_f32_16x16x32_bf16 a[52:55], a[164:167], a[140:143], a[52:55]\n" \
"v_and_b32_e32 v214, 32, v20\n" \
"s_lshl_b32 s6, s6, 5\n" \
"v_mfma_f32_16x16x32_bf16 a[52:55], a[228:231], a[204:207], a[52:55]\n" \
"ds_read_b64_tr_b16 a[186:187], v38 offset:1024\n" \
"v_cndmask_b32_e64 v231, v233, v234, vcc\n" \
"v_mfma_f32_16x16x32_bf16 a[48:51], a[160:163], a[140:143], a[48:51]\n" \
"v_mul_u32_u24_e64 v213, 18, v213\n" \
"v_mov_b32_e32 v215, 0x100\n" \
"v_mfma_f32_16x16x32_bf16 a[48:51], a[224:227], a[204:207], a[48:51]\n" \
"v_mov_b32_e32 v216, s6\n" \
"v_cmp_eq_u32_e32 vcc, 0, v214\n" \
"v_mfma_f32_16x16x32_bf16 a[64:67], a[160:163], a[144:147], a[64:67]\n" \
"buffer_load_dword v211, v194, s[76:79], s34 offen offset:2336\n" \
"v_mfma_f32_16x16x32_bf16 a[64:67], a[224:227], a[208:211], a[64:67]\n" \
"ds_read_b64_tr_b16 a[248:249], v38 offset:8192\n" \
"v_and_or_b32 v213, v218, 1, v213\n" \
"v_mfma_f32_16x16x32_bf16 a[68:71], a[164:167], a[144:147], a[68:71]\n" \
"v_lshlrev_b32_e32 v212, 3, v212\n" \
"v_cndmask_b32_e32 v214, v215, v216, vcc\n" \
"v_mfma_f32_16x16x32_bf16 a[68:71], a[228:231], a[208:211], a[68:71]\n" \
"ds_read_b64_tr_b16 a[250:251], v38 offset:9216\n" \
"v_mad_u32_u24 v213, v213, s5, v214\n" \
"v_mfma_f32_16x16x32_bf16 a[72:75], a[168:171], a[144:147], a[72:75]\n" \
"v_and_or_b32 v213, v20, 16, v213\n" \
"v_add_u32_e32 v9, 0x4800, v213\n" \
"v_mfma_f32_16x16x32_bf16 a[72:75], a[232:235], a[208:211], a[72:75]\n" \
"s_mov_b32 s76, s20\n" \
"s_and_b32 s77, s21, 0xffff\n" \
"v_mfma_f32_16x16x32_bf16 a[76:79], a[172:175], a[144:147], a[76:79]\n" \
"s_mov_b32 s78, 0x480\n" \
"v_mul_u32_u24_e32 v213, 0x120, v218\n" \
"v_mfma_f32_16x16x32_bf16 a[76:79], a[236:239], a[208:211], a[76:79]\n" \
"buffer_load_ushort v210, v195, s[76:79], 0 offen\n" \
"v_mfma_f32_16x16x32_bf16 a[92:95], a[172:175], a[148:151], a[92:95]\n" \
"ds_read_b64_tr_b16 a[188:189], v39 offset:0\n" \
"v_and_or_b32 v10, v212, 24, v213\n" \
"v_mfma_f32_16x16x32_bf16 a[92:95], a[236:239], a[212:215], a[92:95]\n" \
"v_lshrrev_b32_e32 v213, 2, v20\n" \
"v_and_or_b32 v213, v213, 60, s4\n" \
"v_mfma_f32_16x16x32_bf16 a[88:91], a[168:171], a[148:151], a[88:91]\n" \
"s_mov_b32 s76, s8\n" \
"s_nop 0\n" \
"s_and_b32 s77, s9, 0xffff\n" \
"v_mfma_f32_16x16x32_bf16 a[88:91], a[232:235], a[212:215], a[88:91]\n" \
"s_mov_b32 s78, 0x240000\n" \
"ds_read_b64_tr_b16 a[190:191], v39 offset:1024\n" \
"v_mfma_f32_16x16x32_bf16 a[84:87], a[164:167], a[148:151], a[84:87]\n" \
"v_lshl_or_b32 v212, s3, 9, v212\n" \
"v_mad_u32_u24 v4, v224, s5, v223\n" \
"v_mfma_f32_16x16x32_bf16 a[84:87], a[228:231], a[212:215], a[84:87]\n" \
"ds_read_b64_tr_b16 a[252:253], v39 offset:8192\n" \
"v_mad_u32_u24 v8, v232, s5, v231\n" \
"v_mfma_f32_16x16x32_bf16 a[80:83], a[160:163], a[148:151], a[80:83]\n" \
"v_or_b32_e32 v18, 0x27000, v213\n" \
"v_or_b32_e32 v19, 0x27800, v212\n" \
"v_mfma_f32_16x16x32_bf16 a[80:83], a[224:227], a[212:215], a[80:83]\n" \
"v_mov_b32_e32 v219, 0x1000c0c\n" \
"v_mov_b32_e32 v220, 0xc0c0100\n" \
"v_mfma_f32_16x16x32_bf16 a[96:99], a[160:163], a[152:155], a[96:99]\n" \
"ds_read_b64_tr_b16 a[254:255], v39 offset:9216\n" \
"v_bfe_u32 v218, v20, 4, 1\n" \
"v_mfma_f32_16x16x32_bf16 a[96:99], a[224:227], a[216:219], a[96:99]\n" \
"v_lshrrev_b32_e32 v213, 8, v20\n" \
"v_cmp_eq_u32_e32 vcc, 0, v218\n" \
"v_mfma_f32_16x16x32_bf16 a[100:103], a[164:167], a[152:155], a[100:103]\n" \
"s_lshr_b32 s4, s2, 5\n" \
"s_and_b32 s5, s3, 1\n" \
"v_mfma_f32_16x16x32_bf16 a[100:103], a[228:231], a[216:219], a[100:103]\n" \
"v_bfe_u32 v217, v20, 4, 4\n" \
"v_cndmask_b32_e64 v219, v219, v220, vcc\n" \
"v_mfma_f32_16x16x32_bf16 a[104:107], a[168:171], a[152:155], a[104:107]\n" \
"v_mov_b32_e32 v220, 0x3020c0c\n" \
"v_mov_b32_e32 v221, 0xc0c0302\n" \
"v_mfma_f32_16x16x32_bf16 a[104:107], a[232:235], a[216:219], a[104:107]\n" \
"v_lshl_or_b32 v218, v213, 2, v218\n" \
"s_xor_b32 s6, s4, 15\n" \
"v_mfma_f32_16x16x32_bf16 a[108:111], a[172:175], a[152:155], a[108:111]\n" \
"s_cmpk_lt_u32 s3, 2\n" \
"s_cselect_b32 s7, s6, s4\n" \
"v_mfma_f32_16x16x32_bf16 a[108:111], a[236:239], a[216:219], a[108:111]\n" \
"s_lshl_b32 s40, s7, 7\n" \
".fill (L_pb + 7904 - .) / 4, 4, 0xBF800000\n" \
"L_pp2:\n" \
"s_cbranch_execz L_pret\n" \
"s_lshl_b32 s41, s5, 6\n" \
"s_waitcnt lgkmcnt(15)\n" \
"v_mfma_f32_16x16x32_bf16 a[124:127], a[172:175], a[156:159], a[124:127]\n" \
"v_cndmask_b32_e32 v220, v220, v221, vcc\n" \
"v_mov_b32_e32 v221, 0xc0c0c0c\n" \
"v_mfma_f32_16x16x32_bf16 a[124:127], a[236:239], a[220:223], a[124:127]\n" \
"v_cmp_gt_u32_e32 vcc, v218, v217\n" \
"v_or_b32_e32 v218, 2, v218\n" \
"v_mfma_f32_16x16x32_bf16 a[120:123], a[168:171], a[156:159], a[120:123]\n" \
"v_lshrrev_b32_e32 v212, 7, v20\n" \
"s_or_b32 s41, s41, s40\n" \
"v_mfma_f32_16x16x32_bf16 a[120:123], a[232:235], a[220:223], a[120:123]\n" \
"v_cndmask_b32_e32 v40, v221, v219, vcc\n" \
"v_cndmask_b32_e32 v42, v219, v221, vcc\n" \
"v_mfma_f32_16x16x32_bf16 a[116:119], a[164:167], a[156:159], a[116:119]\n" \
"v_cmp_gt_u32_e32 vcc, v218, v217\n" \
"v_and_b32_e32 v218, 16, v20\n" \
"v_mfma_f32_16x16x32_bf16 a[116:119], a[228:231], a[220:223], a[116:119]\n" \
"s_movk_i32 s42, 0x3f80\n" \
"v_or_b32_e32 v214, s41, v212\n" \
"v_mfma_f32_16x16x32_bf16 a[112:115], a[160:163], a[156:159], a[112:115]\n" \
"v_and_b32_e32 v217, 0x320, v20\n" \
"s_movk_i32 s41, 0x100\n" \
"v_mfma_f32_16x16x32_bf16 a[112:115], a[224:227], a[220:223], a[112:115]\n" \
"s_waitcnt vmcnt(7) lgkmcnt(0)\n" \
"s_barrier\n" \
"v_mfma_f32_16x16x32_bf16 v[52:55], a[176:179], a[128:131], v[52:55]\n" \
"s_add_u32 m0, s30, 0x0\n" \
"s_movk_i32 s84, 0x200\n" \
"v_mfma_f32_16x16x32_bf16 v[52:55], a[240:243], a[192:195], v[52:55]\n" \
"buffer_load_dwordx4 v22, s[64:67], s84 offen offset:0 lds\n" \
"v_mfma_f32_16x16x32_bf16 v[56:59], a[180:183], a[128:131], v[56:59]\n" \
"ds_read_b64_tr_b16 a[160:161], v32 offset:32768\n" \
"v_lshlrev_b32_e64 v218, v218, s42\n" \
"v_mfma_f32_16x16x32_bf16 v[56:59], a[244:247], a[192:195], v[56:59]\n" \
"s_movk_i32 s84, 0x1e00\n" \
"v_mfma_f32_16x16x32_bf16 v[60:63], a[184:187], a[128:131], v[60:63]\n" \
"buffer_load_dwordx4 v23, s[64:67], s84 offen offset:1024 lds\n" \
"v_mfma_f32_16x16x32_bf16 v[60:63], a[248:251], a[192:195], v[60:63]\n" \
"ds_read_b64_tr_b16 a[162:163], v32 offset:33792\n" \
"v_mov_b32_e64 v219, 0\n" \
"v_mfma_f32_16x16x32_bf16 v[64:67], a[188:191], a[128:131], v[64:67]\n" \
"ds_read_b64_tr_b16 a[224:225], v32 offset:40960\n" \
"v_lshrrev_b32_e64 v215, 6, v20\n" \
"v_mfma_f32_16x16x32_bf16 v[64:67], a[252:255], a[192:195], v[64:67]\n" \
"ds_read_b128 a[128:131], v28 offset:49152\n" \
"v_cndmask_b32_e64 v41, v221, v220, vcc\n" \
"v_mfma_f32_16x16x32_bf16 v[80:83], a[188:191], a[132:135], v[80:83]\n" \
"ds_read_b128 a[192:195], v29 offset:49152\n" \
"v_cndmask_b32_e64 v43, v220, v221, vcc\n" \
"v_mfma_f32_16x16x32_bf16 v[80:83], a[252:255], a[196:199], v[80:83]\n" \
"s_movk_i32 s84, 0x3a00\n" \
"v_mfma_f32_16x16x32_bf16 v[76:79], a[184:187], a[132:135], v[76:79]\n" \
"buffer_load_dwordx4 v22, s[64:67], s84 offen offset:2048 lds\n" \
"v_mfma_f32_16x16x32_bf16 v[76:79], a[248:251], a[196:199], v[76:79]\n" \
"s_movk_i32 s84, 0x5600\n" \
"v_mfma_f32_16x16x32_bf16 v[72:75], a[180:183], a[132:135], v[72:75]\n" \
"buffer_load_dwordx4 v23, s[64:67], s84 offen offset:3072 lds\n" \
"v_mfma_f32_16x16x32_bf16 v[72:75], a[244:247], a[196:199], v[72:75]\n" \
"s_add_u32 m0, s31, 0x0\n" \
"s_mov_b32 s84, 0x200000\n" \
"v_mfma_f32_16x16x32_bf16 v[68:71], a[176:179], a[132:135], v[68:71]\n" \
"buffer_load_dwordx4 v26, s[68:71], s84 offen offset:0 lds\n" \
"v_mfma_f32_16x16x32_bf16 v[68:71], a[240:243], a[196:199], v[68:71]\n" \
"ds_read_b128 a[132:135], v28 offset:51200\n" \
"v_cmp_eq_u32_e32 vcc, s41, v217\n" \
"v_mfma_f32_16x16x32_bf16 v[84:87], a[176:179], a[136:139], v[84:87]\n" \
"ds_read_b128 a[196:199], v29 offset:51200\n" \
"v_and_b32_e32 v217, 0x330, v20\n" \
"v_mfma_f32_16x16x32_bf16 v[84:87], a[240:243], a[200:203], v[84:87]\n" \
"s_addk_i32 s84, 0x7c00\n" \
"v_mfma_f32_16x16x32_bf16 v[88:91], a[180:183], a[136:139], v[88:91]\n" \
"buffer_load_dwordx4 v26, s[68:71], s84 offen offset:1024 lds\n" \
"v_mfma_f32_16x16x32_bf16 v[88:91], a[244:247], a[200:203], v[88:91]\n" \
"ds_read_b64_tr_b16 a[226:227], v32 offset:41984\n" \
"v_bfe_u32 v216, v20, 6, 2\n" \
"v_mfma_f32_16x16x32_bf16 v[92:95], a[184:187], a[136:139], v[92:95]\n" \
"s_addk_i32 s84, 0x7c00\n" \
"v_mfma_f32_16x16x32_bf16 v[92:95], a[248:251], a[200:203], v[92:95]\n" \
"buffer_load_dwordx4 v27, s[68:71], s84 offen offset:2048 lds\n" \
"v_mfma_f32_16x16x32_bf16 v[96:99], a[188:191], a[136:139], v[96:99]\n" \
"ds_read_b64_tr_b16 a[164:165], v33 offset:32768\n" \
"v_cndmask_b32_e64 v46, v219, v218, vcc\n" \
"v_mfma_f32_16x16x32_bf16 v[96:99], a[252:255], a[200:203], v[96:99]\n" \
"ds_read_b128 a[136:139], v28 offset:53248\n" \
"v_mov_b32_e32 v218, 0x3f80\n" \
"v_mfma_f32_16x16x32_bf16 v[112:115], a[188:191], a[140:143], v[112:115]\n" \
"ds_read_b128 a[200:203], v29 offset:53248\n" \
"v_cmp_eq_u32_e64 vcc, 32, v217\n" \
"v_mfma_f32_16x16x32_bf16 v[112:115], a[252:255], a[204:207], v[112:115]\n" \
"s_addk_i32 s84, 0x7c00\n" \
"v_mfma_f32_16x16x32_bf16 v[108:111], a[184:187], a[140:143], v[108:111]\n" \
"buffer_load_dwordx4 v27, s[68:71], s84 offen offset:3072 lds\n" \
"v_mfma_f32_16x16x32_bf16 v[108:111], a[248:251], a[204:207], v[108:111]\n" \
"s_add_u32 m0, s30, 0x1000\n" \
"s_mov_b32 s84, 0x8200\n" \
"v_mfma_f32_16x16x32_bf16 v[104:107], a[180:183], a[140:143], v[104:107]\n" \
"buffer_load_dwordx4 v22, s[64:67], s84 offen offset:0 lds\n" \
"v_mfma_f32_16x16x32_bf16 v[104:107], a[244:247], a[204:207], v[104:107]\n" \
"ds_read_b64_tr_b16 a[166:167], v33 offset:33792\n" \
"s_add_i32 s41, s40, -16\n" \
"v_mfma_f32_16x16x32_bf16 v[100:103], a[176:179], a[140:143], v[100:103]\n" \
"ds_read_b64_tr_b16 a[228:229], v33 offset:40960\n" \
"v_lshrrev_b32_e64 v219, 1, v20\n" \
"v_mfma_f32_16x16x32_bf16 v[100:103], a[240:243], a[204:207], v[100:103]\n" \
"ds_read_b128 a[140:143], v28 offset:55296\n" \
"v_bfe_u32 v215, v215, 1, 1\n" \
"v_mfma_f32_16x16x32_bf16 v[116:119], a[176:179], a[144:147], v[116:119]\n" \
"ds_read_b128 a[204:207], v29 offset:55296\n" \
"v_cndmask_b32_e64 v47, 0, v218, vcc\n" \
"v_mfma_f32_16x16x32_bf16 v[116:119], a[240:243], a[208:211], v[116:119]\n" \
"s_addk_i32 s84, 0x1c00\n" \
"v_mfma_f32_16x16x32_bf16 v[120:123], a[180:183], a[144:147], v[120:123]\n" \
"buffer_load_dwordx4 v23, s[64:67], s84 offen offset:1024 lds\n" \
"v_mfma_f32_16x16x32_bf16 v[120:123], a[244:247], a[208:211], v[120:123]\n" \
"s_addk_i32 s84, 0x1c00\n" \
"v_mfma_f32_16x16x32_bf16 v[124:127], a[184:187], a[144:147], v[124:127]\n" \
"buffer_load_dwordx4 v22, s[64:67], s84 offen offset:2048 lds\n" \
"v_mfma_f32_16x16x32_bf16 v[124:127], a[248:251], a[208:211], v[124:127]\n" \
"s_addk_i32 s84, 0x1c00\n" \
"v_mfma_f32_16x16x32_bf16 v[128:131], a[188:191], a[144:147], v[128:131]\n" \
"buffer_load_dwordx4 v23, s[64:67], s84 offen offset:3072 lds\n" \
"v_mfma_f32_16x16x32_bf16 v[128:131], a[252:255], a[208:211], v[128:131]\n" \
"ds_read_b128 a[144:147], v28 offset:32768\n" \
"s_mov_b32 s42, 0\n" \
"v_mfma_f32_16x16x32_bf16 v[144:147], a[188:191], a[148:151], v[144:147]\n" \
"ds_read_b128 a[208:211], v29 offset:32768\n" \
"v_mfma_f32_16x16x32_bf16 v[144:147], a[252:255], a[212:215], v[144:147]\n" \
"s_add_u32 m0, s31, 0x4000\n" \
"s_mov_b32 s84, 0x200000\n" \
"v_mfma_f32_16x16x32_bf16 v[140:143], a[184:187], a[148:151], v[140:143]\n" \
"buffer_load_dwordx4 v26, s[72:75], s84 offen offset:0 lds\n" \
"v_mfma_f32_16x16x32_bf16 v[140:143], a[248:251], a[212:215], v[140:143]\n" \
"ds_read_b64_tr_b16 a[230:231], v33 offset:41984\n" \
"v_mfma_f32_16x16x32_bf16 v[136:139], a[180:183], a[148:151], v[136:139]\n" \
"s_addk_i32 s84, 0x7c00\n" \
"s_nop 0\n" \
"v_mfma_f32_16x16x32_bf16 v[136:139], a[244:247], a[212:215], v[136:139]\n" \
"buffer_load_dwordx4 v26, s[72:75], s84 offen offset:1024 lds\n" \
"v_mfma_f32_16x16x32_bf16 v[132:135], a[176:179], a[148:151], v[132:135]\n" \
"ds_read_b64_tr_b16 a[168:169], v34 offset:32768\n" \
"v_mfma_f32_16x16x32_bf16 v[132:135], a[240:243], a[212:215], v[132:135]\n" \
"ds_read_b128 a[148:151], v28 offset:34816\n" \
"v_mfma_f32_16x16x32_bf16 v[148:151], a[176:179], a[152:155], v[148:151]\n" \
"ds_read_b128 a[212:215], v29 offset:34816\n" \
"v_mfma_f32_16x16x32_bf16 v[148:151], a[240:243], a[216:219], v[148:151]\n" \
"ds_read_b64_tr_b16 a[170:171], v34 offset:33792\n" \
"v_mfma_f32_16x16x32_bf16 v[152:155], a[180:183], a[152:155], v[152:155]\n" \
"ds_read_b64_tr_b16 a[232:233], v34 offset:40960\n" \
"v_mfma_f32_16x16x32_bf16 v[152:155], a[244:247], a[216:219], v[152:155]\n" \
"ds_read_b64_tr_b16 a[234:235], v34 offset:41984\n" \
"v_mfma_f32_16x16x32_bf16 v[156:159], a[184:187], a[152:155], v[156:159]\n" \
"ds_read_b64_tr_b16 a[172:173], v35 offset:32768\n" \
"v_mfma_f32_16x16x32_bf16 v[156:159], a[248:251], a[216:219], v[156:159]\n" \
"ds_read_b64_tr_b16 a[174:175], v35 offset:33792\n" \
"v_mfma_f32_16x16x32_bf16 v[160:163], a[188:191], a[152:155], v[160:163]\n" \
"ds_read_b64_tr_b16 a[236:237], v35 offset:40960\n" \
"v_mfma_f32_16x16x32_bf16 v[160:163], a[252:255], a[216:219], v[160:163]\n" \
"ds_read_b128 a[152:155], v28 offset:36864\n" \
"v_mfma_f32_16x16x32_bf16 v[176:179], a[188:191], a[156:159], v[176:179]\n" \
"ds_read_b128 a[216:219], v29 offset:36864\n" \
"v_mfma_f32_16x16x32_bf16 v[176:179], a[252:255], a[220:223], v[176:179]\n" \
"ds_read_b64_tr_b16 a[238:239], v35 offset:41984\n" \
"v_mfma_f32_16x16x32_bf16 v[172:175], a[184:187], a[156:159], v[172:175]\n" \
"s_cmp_lg_u32 s40, 0\n" \
"s_cselect_b32 s41, s41, s42\n" \
"v_mfma_f32_16x16x32_bf16 v[172:175], a[248:251], a[220:223], v[172:175]\n" \
"v_lshl_or_b32 v217, s5, 3, v212\n" \
"v_lshlrev_b32_e64 v218, 10, v213\n" \
"v_mfma_f32_16x16x32_bf16 v[168:171], a[180:183], a[156:159], v[168:171]\n" \
"v_lshlrev_b32_e32 v216, 7, v216\n" \
"v_and_b32_e32 v219, 24, v219\n" \
"v_mfma_f32_16x16x32_bf16 v[168:171], a[244:247], a[220:223], v[168:171]\n" \
"v_and_or_b32 v212, v212, 2, v215\n" \
"v_add_u32_e64 v217, s41, v217\n" \
"v_mfma_f32_16x16x32_bf16 v[164:167], a[176:179], a[156:159], v[164:167]\n" \
"v_or3_b32 v216, v216, v218, v219\n" \
"v_lshlrev_b32_e64 v212, 5, v212\n" \
"v_mfma_f32_16x16x32_bf16 v[164:167], a[240:243], a[220:223], v[164:167]\n" \
"ds_read_b128 a[156:159], v28 offset:38912\n" \
"ds_read_b128 a[220:223], v29 offset:38912\n" \
"s_waitcnt lgkmcnt(15)\n" \
"v_mfma_f32_16x16x32_bf16 a[0:3], a[160:163], a[128:131], a[0:3]\n" \
"s_addk_i32 s84, 0x7c00\n" \
"v_mfma_f32_16x16x32_bf16 a[0:3], a[224:227], a[192:195], a[0:3]\n" \
"buffer_load_dwordx4 v27, s[72:75], s84 offen offset:2048 lds\n" \
"v_mfma_f32_16x16x32_bf16 a[4:7], a[164:167], a[128:131], a[4:7]\n" \
"ds_read_b64_tr_b16 a[176:177], v36 offset:32768\n" \
"s_mov_b32 s41, 0x14000\n" \
"s_waitcnt lgkmcnt(15)\n" \
"v_mfma_f32_16x16x32_bf16 a[4:7], a[228:231], a[192:195], a[4:7]\n" \
"s_addk_i32 s84, 0x7c00\n" \
"s_waitcnt lgkmcnt(11)\n" \
"v_mfma_f32_16x16x32_bf16 a[8:11], a[168:171], a[128:131], a[8:11]\n" \
"buffer_load_dwordx4 v27, s[72:75], s84 offen offset:3072 lds\n" \
"s_waitcnt lgkmcnt(9)\n" \
"v_mfma_f32_16x16x32_bf16 a[8:11], a[232:235], a[192:195], a[8:11]\n" \
"ds_read_b64_tr_b16 a[178:179], v36 offset:33792\n" \
"v_or3_b32 v48, v216, v212, s41\n" \
"s_waitcnt lgkmcnt(8)\n" \
"v_mfma_f32_16x16x32_bf16 a[12:15], a[172:175], a[128:131], a[12:15]\n" \
"ds_read_b64_tr_b16 a[240:241], v36 offset:40960\n" \
"s_lshl_b32 s41, s5, 2\n" \
"s_waitcnt lgkmcnt(5)\n" \
"v_mfma_f32_16x16x32_bf16 a[12:15], a[236:239], a[192:195], a[12:15]\n" \
"s_lshl_b32 s7, s7, 3\n" \
"v_or_b32_e64 v212, s41, v213\n" \
"v_mfma_f32_16x16x32_bf16 a[28:31], a[172:175], a[132:135], a[28:31]\n" \
"s_or_b32 s7, s7, s41\n" \
"s_mov_b32 s41, 0xff800000\n" \
"v_mfma_f32_16x16x32_bf16 a[28:31], a[236:239], a[196:199], a[28:31]\n" \
"ds_read_b64_tr_b16 a[242:243], v36 offset:41984\n" \
"v_mfma_f32_16x16x32_bf16 a[24:27], a[168:171], a[132:135], a[24:27]\n" \
"s_cmp_lg_u32 s40, 0\n" \
"s_cselect_b32 s47, s42, s41\n" \
"v_mfma_f32_16x16x32_bf16 a[24:27], a[232:235], a[196:199], a[24:27]\n" \
"s_mov_b32 s40, 0xc000\n" \
"s_mov_b32 s41, 0x8000\n" \
"v_mfma_f32_16x16x32_bf16 a[20:23], a[164:167], a[132:135], a[20:23]\n" \
"s_cmp_lg_u32 s4, 0\n" \
"s_cselect_b32 s51, s40, s41\n" \
"v_mfma_f32_16x16x32_bf16 a[20:23], a[228:231], a[196:199], a[20:23]\n" \
"ds_read_b64_tr_b16 a[180:181], v37 offset:32768\n" \
"s_movk_i32 s40, 0x4000\n" \
"v_mfma_f32_16x16x32_bf16 a[16:19], a[160:163], a[132:135], a[16:19]\n" \
"s_cmp_lg_u32 s4, 0\n" \
"s_cselect_b32 s53, s41, s40\n" \
"v_mfma_f32_16x16x32_bf16 a[16:19], a[224:227], a[196:199], a[16:19]\n" \
"s_lshl_b32 s40, s2, 2\n" \
"s_and_b32 s40, s40, 28\n" \
"v_mfma_f32_16x16x32_bf16 a[32:35], a[160:163], a[136:139], a[32:35]\n" \
"s_waitcnt vmcnt(16)\n" \
"v_or_b32_e32 v204, v210, v204\n" \
"v_or_b32_e64 v206, v210, v206\n" \
"v_mfma_f32_16x16x32_bf16 a[32:35], a[224:227], a[200:203], a[32:35]\n" \
"v_mov_b32_e64 v210, 0\n" \
"s_add_u32 s88, s35, 0x1000\n" \
"v_mfma_f32_16x16x32_bf16 a[36:39], a[164:167], a[136:139], a[36:39]\n" \
"ds_read_b64_tr_b16 a[182:183], v37 offset:33792\n" \
"s_bfe_u32 s45, s2, 0x20003\n" \
"v_mfma_f32_16x16x32_bf16 a[36:39], a[228:231], a[200:203], a[36:39]\n" \
"ds_read_b64_tr_b16 a[244:245], v37 offset:40960\n" \
"s_lshr_b32 s44, s3, 1\n" \
"v_mfma_f32_16x16x32_bf16 a[40:43], a[168:171], a[136:139], a[40:43]\n" \
"s_mov_b32 m0, s32\n" \
"s_or_b32 s46, s40, s45\n" \
"v_mfma_f32_16x16x32_bf16 a[40:43], a[232:235], a[200:203], a[40:43]\n" \
"s_mov_b32 exec_lo, 0xffffffff\n" \
"s_mov_b32 exec_hi, 0x0\n" \
"global_load_lds_dword v[188:189], off\n" \
"s_mov_b64 exec, -1\n" \
"v_mfma_f32_16x16x32_bf16 a[44:47], a[172:175], a[136:139], a[44:47]\n" \
"ds_read_b64_tr_b16 a[246:247], v37 offset:41984\n" \
"s_lshl_b32 s43, s44, 15\n" \
"v_mfma_f32_16x16x32_bf16 a[44:47], a[236:239], a[200:203], a[44:47]\n" \
"s_lshl_b32 s41, s5, 14\n" \
"s_lshl_b32 s48, s46, 20\n" \
"v_mfma_f32_16x16x32_bf16 a[60:63], a[172:175], a[140:143], a[60:63]\n" \
"ds_write_b128 v196, v[200:203]\n" \
"v_mfma_f32_16x16x32_bf16 a[60:63], a[236:239], a[204:207], a[60:63]\n" \
"s_or_b32 s41, s43, s41\n" \
"s_add_i32 s49, s48, 0x1000000\n" \
"v_mfma_f32_16x16x32_bf16 a[56:59], a[168:171], a[140:143], a[56:59]\n" \
"ds_read_b64_tr_b16 a[184:185], v38 offset:32768\n" \
"s_or_b32 s42, s49, s41\n" \
"v_mfma_f32_16x16x32_bf16 a[56:59], a[232:235], a[204:207], a[56:59]\n" \
"s_lshl_b32 s4, s4, 16\n" \
"s_or_b32 s41, s42, s4\n" \
"v_mfma_f32_16x16x32_bf16 a[52:55], a[164:167], a[140:143], a[52:55]\n" \
"s_lshl_b32 s4, s6, 16\n" \
"s_or_b32 s42, s42, s4\n" \
"v_mfma_f32_16x16x32_bf16 a[52:55], a[228:231], a[204:207], a[52:55]\n" \
"ds_read_b64_tr_b16 a[186:187], v38 offset:33792\n" \
"s_lshl_b32 s4, s5, 11\n" \
"v_mfma_f32_16x16x32_bf16 a[48:51], a[160:163], a[140:143], a[48:51]\n" \
"s_mul_i32 s5, s7, 0x24800\n" \
"s_lshl_b32 s6, s46, 9\n" \
"v_mfma_f32_16x16x32_bf16 a[48:51], a[224:227], a[204:207], a[48:51]\n" \
"s_lshl_b32 s7, s40, 6\n" \
"s_add_i32 s6, s5, s6\n" \
"v_mfma_f32_16x16x32_bf16 a[64:67], a[160:163], a[144:147], a[64:67]\n" \
"ds_write_b128 v197, v[204:207]\n" \
"v_mfma_f32_16x16x32_bf16 a[64:67], a[224:227], a[208:211], a[64:67]\n" \
"ds_read_b64_tr_b16 a[248:249], v38 offset:40960\n" \
"s_or_b32 s5, s5, s7\n" \
"v_mfma_f32_16x16x32_bf16 a[68:71], a[164:167], a[144:147], a[68:71]\n" \
"s_lshl_b32 s7, s45, 2\n" \
"s_or_b32 s5, s5, s7\n" \
"v_mfma_f32_16x16x32_bf16 a[68:71], a[228:231], a[208:211], a[68:71]\n" \
"ds_read_b64_tr_b16 a[250:251], v38 offset:41984\n" \
"s_lshl_b32 s7, s3, 12\n" \
"v_mfma_f32_16x16x32_bf16 a[72:75], a[168:171], a[144:147], a[72:75]\n" \
"s_or_b32 s50, s49, s7\n" \
"s_lshl_b32 s7, s3, 11\n" \
"v_mfma_f32_16x16x32_bf16 a[72:75], a[232:235], a[208:211], a[72:75]\n" \
"v_lshrrev_b32_e32 v215, 4, v20\n" \
"v_bfe_u32 v216, v20, 4, 3\n" \
"v_mfma_f32_16x16x32_bf16 a[76:79], a[172:175], a[144:147], a[76:79]\n" \
"s_add_i32 s52, s48, s7\n" \
"s_add_i32 s53, s53, s7\n" \
"v_mfma_f32_16x16x32_bf16 a[76:79], a[236:239], a[208:211], a[76:79]\n" \
"ds_read_b64_tr_b16 a[188:189], v39 offset:32768\n" \
"v_mul_u32_u24_e32 v214, 0x480, v214\n" \
"v_mfma_f32_16x16x32_bf16 a[92:95], a[172:175], a[148:151], a[92:95]\n" \
"ds_write_b128 v198, v[208:211]\n" \
"v_mfma_f32_16x16x32_bf16 a[92:95], a[236:239], a[212:215], a[92:95]\n" \
"v_bitop3_b32 v218, v213, v215, 7 bitop3:0x78\n" \
"v_bitop3_b32 v213, v213, v216, 4 bitop3:0x36\n" \
"v_mfma_f32_16x16x32_bf16 a[88:91], a[168:171], a[148:151], a[88:91]\n" \
"ds_read_b64_tr_b16 a[190:191], v39 offset:33792\n" \
"s_lshl_b32 s7, s3, 10\n" \
"v_mfma_f32_16x16x32_bf16 a[88:91], a[232:235], a[212:215], a[88:91]\n" \
"s_or_b32 s4, s43, s4\n" \
"v_lshl_or_b32 v25, v213, 4, v214\n" \
"v_mfma_f32_16x16x32_bf16 a[84:87], a[164:167], a[148:151], a[84:87]\n" \
"v_mul_u32_u24_e32 v213, 0x480, v217\n" \
"v_bitop3_b32 v212, v215, v212, 7 bitop3:0x6c\n" \
"v_mfma_f32_16x16x32_bf16 a[84:87], a[228:231], a[212:215], a[84:87]\n" \
"ds_read_b64_tr_b16 a[252:253], v39 offset:40960\n" \
"s_or_b32 s40, s7, 0x14000\n" \
"v_mfma_f32_16x16x32_bf16 a[80:83], a[160:163], a[148:151], a[80:83]\n" \
"s_lshl_b32 s43, s44, 13\n" \
"s_mulk_i32 s44, 0xe800\n" \
"v_mfma_f32_16x16x32_bf16 a[80:83], a[224:227], a[212:215], a[80:83]\n" \
"s_lshl_b32 s7, s3, 8\n" \
"s_nop 0\n" \
"v_lshl_or_b32 v24, v218, 4, v214\n" \
"v_mfma_f32_16x16x32_bf16 a[96:99], a[160:163], a[152:155], a[96:99]\n" \
"ds_read_b64_tr_b16 a[254:255], v39 offset:41984\n" \
"v_lshl_or_b32 v21, v212, 4, v213\n" \
"v_mfma_f32_16x16x32_bf16 a[96:99], a[224:227], a[216:219], a[96:99]\n" \
"s_add_i32 s44, s44, 0x17000\n" \
"s_or_b32 s45, s7, 0x27000\n" \
"v_mfma_f32_16x16x32_bf16 a[100:103], a[164:167], a[152:155], a[100:103]\n" \
"s_or_b32 s46, s4, 0x10000\n" \
"s_add_i32 s48, s6, 0x3000000\n" \
"v_mfma_f32_16x16x32_bf16 a[100:103], a[228:231], a[216:219], a[100:103]\n" \
"s_add_i32 s49, s5, 0x3004000\n" \
"s_add_i32 s52, s52, 0x1010000\n" \
"v_mfma_f32_16x16x32_bf16 a[104:107], a[168:171], a[152:155], a[104:107]\n" \
"v_mfma_f32_16x16x32_bf16 a[104:107], a[232:235], a[216:219], a[104:107]\n" \
"v_mfma_f32_16x16x32_bf16 a[108:111], a[172:175], a[152:155], a[108:111]\n" \
"v_mfma_f32_16x16x32_bf16 a[108:111], a[236:239], a[216:219], a[108:111]\n" \
"s_waitcnt lgkmcnt(15)\n" \
"v_mfma_f32_16x16x32_bf16 a[124:127], a[172:175], a[156:159], a[124:127]\n" \
"v_mfma_f32_16x16x32_bf16 a[124:127], a[236:239], a[220:223], a[124:127]\n" \
"v_mfma_f32_16x16x32_bf16 a[120:123], a[168:171], a[156:159], a[120:123]\n" \
"v_mfma_f32_16x16x32_bf16 a[120:123], a[232:235], a[220:223], a[120:123]\n" \
"v_mfma_f32_16x16x32_bf16 a[116:119], a[164:167], a[156:159], a[116:119]\n" \
"v_mfma_f32_16x16x32_bf16 a[116:119], a[228:231], a[220:223], a[116:119]\n" \
"v_mfma_f32_16x16x32_bf16 a[112:115], a[160:163], a[156:159], a[112:115]\n" \
"v_mfma_f32_16x16x32_bf16 a[112:115], a[224:227], a[220:223], a[112:115]\n" \
"s_waitcnt vmcnt(1) lgkmcnt(0)\n" \
"s_barrier\n" \
"v_mfma_f32_16x16x32_bf16 v[52:55], a[176:179], a[128:131], v[52:55]\n" \
"s_add_u32 m0, s30, 0x8000\n" \
"s_movk_i32 s84, 0x280\n" \
"v_mfma_f32_16x16x32_bf16 v[52:55], a[240:243], a[192:195], v[52:55]\n" \
"buffer_load_dwordx4 v22, s[64:67], s84 offen offset:0 lds\n" \
"v_mfma_f32_16x16x32_bf16 v[56:59], a[180:183], a[128:131], v[56:59]\n" \
"s_movk_i32 s84, 0x1e80\n" \
"v_mfma_f32_16x16x32_bf16 v[56:59], a[244:247], a[192:195], v[56:59]\n" \
"buffer_load_dwordx4 v23, s[64:67], s84 offen offset:1024 lds\n" \
"v_mfma_f32_16x16x32_bf16 v[60:63], a[184:187], a[128:131], v[60:63]\n" \
"ds_read_b64_tr_b16 a[160:161], v32 offset:0\n" \
"v_mfma_f32_16x16x32_bf16 v[60:63], a[248:251], a[192:195], v[60:63]\n" \
"s_movk_i32 s84, 0x3a80\n" \
"v_mfma_f32_16x16x32_bf16 v[64:67], a[188:191], a[128:131], v[64:67]\n" \
"buffer_load_dwordx4 v22, s[64:67], s84 offen offset:2048 lds\n" \
"v_mfma_f32_16x16x32_bf16 v[64:67], a[252:255], a[192:195], v[64:67]\n" \
"ds_read_b128 a[128:131], v28 offset:16384\n" \
"v_mfma_f32_16x16x32_bf16 v[80:83], a[188:191], a[132:135], v[80:83]\n" \
"ds_read_b128 a[192:195], v29 offset:16384\n" \
"v_mfma_f32_16x16x32_bf16 v[80:83], a[252:255], a[196:199], v[80:83]\n" \
"s_movk_i32 s84, 0x5680\n" \
"s_nop 0\n" \
"v_mfma_f32_16x16x32_bf16 v[76:79], a[184:187], a[132:135], v[76:79]\n" \
"buffer_load_dwordx4 v23, s[64:67], s84 offen offset:3072 lds\n" \
"v_mfma_f32_16x16x32_bf16 v[76:79], a[248:251], a[196:199], v[76:79]\n" \
"s_add_u32 m0, s31, 0x8000\n" \
"s_mov_b32 s84, 0x280000\n" \
"v_mfma_f32_16x16x32_bf16 v[72:75], a[180:183], a[132:135], v[72:75]\n" \
"buffer_load_dwordx4 v26, s[68:71], s84 offen offset:0 lds\n" \
"v_mfma_f32_16x16x32_bf16 v[72:75], a[244:247], a[196:199], v[72:75]\n" \
"ds_read_b64_tr_b16 a[162:163], v32 offset:1024\n" \
"v_mfma_f32_16x16x32_bf16 v[68:71], a[176:179], a[132:135], v[68:71]\n" \
"ds_read_b64_tr_b16 a[224:225], v32 offset:8192\n" \
"v_mfma_f32_16x16x32_bf16 v[68:71], a[240:243], a[196:199], v[68:71]\n" \
"ds_read_b128 a[132:135], v28 offset:18432\n" \
"v_mfma_f32_16x16x32_bf16 v[84:87], a[176:179], a[136:139], v[84:87]\n" \
"ds_read_b128 a[196:199], v29 offset:18432\n" \
"v_mfma_f32_16x16x32_bf16 v[84:87], a[240:243], a[200:203], v[84:87]\n" \
"s_addk_i32 s84, 0x7c00\n" \
"v_mfma_f32_16x16x32_bf16 v[88:91], a[180:183], a[136:139], v[88:91]\n" \
"buffer_load_dwordx4 v26, s[68:71], s84 offen offset:1024 lds\n" \
"v_mfma_f32_16x16x32_bf16 v[88:91], a[244:247], a[200:203], v[88:91]\n" \
"s_addk_i32 s84, 0x7c00\n" \
"v_mfma_f32_16x16x32_bf16 v[92:95], a[184:187], a[136:139], v[92:95]\n" \
"buffer_load_dwordx4 v27, s[68:71], s84 offen offset:2048 lds\n" \
"v_mfma_f32_16x16x32_bf16 v[92:95], a[248:251], a[200:203], v[92:95]\n" \
"s_addk_i32 s84, 0x7c00\n" \
"s_nop 0\n" \
"v_mfma_f32_16x16x32_bf16 v[96:99], a[188:191], a[136:139], v[96:99]\n" \
"buffer_load_dwordx4 v27, s[68:71], s84 offen offset:3072 lds\n" \
"v_mfma_f32_16x16x32_bf16 v[96:99], a[252:255], a[200:203], v[96:99]\n" \
"ds_read_b128 a[136:139], v28 offset:20480\n" \
"v_mfma_f32_16x16x32_bf16 v[112:115], a[188:191], a[140:143], v[112:115]\n" \
"ds_read_b128 a[200:203], v29 offset:20480\n" \
"v_mfma_f32_16x16x32_bf16 v[112:115], a[252:255], a[204:207], v[112:115]\n" \
"s_add_u32 m0, s30, 0x9000\n" \
"s_mov_b32 s84, 0x8280\n" \
"v_mfma_f32_16x16x32_bf16 v[108:111], a[184:187], a[140:143], v[108:111]\n" \
"buffer_load_dwordx4 v22, s[64:67], s84 offen offset:0 lds\n" \
"v_mfma_f32_16x16x32_bf16 v[108:111], a[248:251], a[204:207], v[108:111]\n" \
"s_addk_i32 s84, 0x1c00\n" \
"v_mfma_f32_16x16x32_bf16 v[104:107], a[180:183], a[140:143], v[104:107]\n" \
"buffer_load_dwordx4 v23, s[64:67], s84 offen offset:1024 lds\n" \
"v_mfma_f32_16x16x32_bf16 v[104:107], a[244:247], a[204:207], v[104:107]\n" \
"s_addk_i32 s84, 0x1c00\n" \
"v_mfma_f32_16x16x32_bf16 v[100:103], a[176:179], a[140:143], v[100:103]\n" \
"buffer_load_dwordx4 v22, s[64:67], s84 offen offset:2048 lds\n" \
"v_mfma_f32_16x16x32_bf16 v[100:103], a[240:243], a[204:207], v[100:103]\n" \
"ds_read_b128 a[140:143], v28 offset:22528\n" \
"v_mfma_f32_16x16x32_bf16 v[116:119], a[176:179], a[144:147], v[116:119]\n" \
"ds_read_b128 a[204:207], v29 offset:22528\n" \
"v_mfma_f32_16x16x32_bf16 v[116:119], a[240:243], a[208:211], v[116:119]\n" \
"s_addk_i32 s84, 0x1c00\n" \
"v_mfma_f32_16x16x32_bf16 v[120:123], a[180:183], a[144:147], v[120:123]\n" \
"buffer_load_dwordx4 v23, s[64:67], s84 offen offset:3072 lds\n" \
"v_mfma_f32_16x16x32_bf16 v[120:123], a[244:247], a[208:211], v[120:123]\n" \
"s_add_u32 m0, s31, 0xc000\n" \
"s_mov_b32 s84, 0x280000\n" \
"v_mfma_f32_16x16x32_bf16 v[124:127], a[184:187], a[144:147], v[124:127]\n" \
"buffer_load_dwordx4 v26, s[72:75], s84 offen offset:0 lds\n" \
"v_mfma_f32_16x16x32_bf16 v[124:127], a[248:251], a[208:211], v[124:127]\n" \
"s_addk_i32 s84, 0x7c00\n" \
"v_mfma_f32_16x16x32_bf16 v[128:131], a[188:191], a[144:147], v[128:131]\n" \
"buffer_load_dwordx4 v26, s[72:75], s84 offen offset:1024 lds\n" \
"v_mfma_f32_16x16x32_bf16 v[128:131], a[252:255], a[208:211], v[128:131]\n" \
"ds_read_b128 a[144:147], v28 offset:0\n" \
"v_mfma_f32_16x16x32_bf16 v[144:147], a[188:191], a[148:151], v[144:147]\n" \
"ds_read_b128 a[208:211], v29 offset:0\n" \
"v_mfma_f32_16x16x32_bf16 v[144:147], a[252:255], a[212:215], v[144:147]\n" \
"ds_read_b128 v[200:203], v199 offset:0\n" \
"v_mfma_f32_16x16x32_bf16 v[140:143], a[184:187], a[148:151], v[140:143]\n" \
"ds_read_b128 v[204:207], v199 offset:4096\n" \
"v_mfma_f32_16x16x32_bf16 v[140:143], a[248:251], a[212:215], v[140:143]\n" \
"ds_read_b64_tr_b16 a[226:227], v32 offset:9216\n" \
"v_mfma_f32_16x16x32_bf16 v[136:139], a[180:183], a[148:151], v[136:139]\n" \
"ds_read_b64_tr_b16 a[164:165], v33 offset:0\n" \
"v_mfma_f32_16x16x32_bf16 v[136:139], a[244:247], a[212:215], v[136:139]\n" \
"ds_read_b128 v[208:211], v199 offset:8192\n" \
"v_mfma_f32_16x16x32_bf16 v[132:135], a[176:179], a[148:151], v[132:135]\n" \
"ds_read_b64_tr_b16 a[166:167], v33 offset:1024\n" \
"v_mfma_f32_16x16x32_bf16 v[132:135], a[240:243], a[212:215], v[132:135]\n" \
"ds_read_b128 a[148:151], v28 offset:2048\n" \
"v_mfma_f32_16x16x32_bf16 v[148:151], a[176:179], a[152:155], v[148:151]\n" \
"ds_read_b128 a[212:215], v29 offset:2048\n" \
"v_mfma_f32_16x16x32_bf16 v[148:151], a[240:243], a[216:219], v[148:151]\n" \
"ds_read_b64_tr_b16 a[228:229], v33 offset:8192\n" \
"v_mfma_f32_16x16x32_bf16 v[152:155], a[180:183], a[152:155], v[152:155]\n" \
"ds_read_b64_tr_b16 a[230:231], v33 offset:9216\n" \
"v_mfma_f32_16x16x32_bf16 v[152:155], a[244:247], a[216:219], v[152:155]\n" \
"ds_read_b64_tr_b16 a[168:169], v34 offset:0\n" \
"v_mfma_f32_16x16x32_bf16 v[156:159], a[184:187], a[152:155], v[156:159]\n" \
"ds_read_b64_tr_b16 a[170:171], v34 offset:1024\n" \
"v_mfma_f32_16x16x32_bf16 v[156:159], a[248:251], a[216:219], v[156:159]\n" \
"ds_read_b64_tr_b16 a[232:233], v34 offset:8192\n" \
"v_mfma_f32_16x16x32_bf16 v[160:163], a[188:191], a[152:155], v[160:163]\n" \
"ds_read_b64_tr_b16 a[234:235], v34 offset:9216\n" \
"v_mfma_f32_16x16x32_bf16 v[160:163], a[252:255], a[216:219], v[160:163]\n" \
"ds_read_b128 a[152:155], v28 offset:4096\n" \
"v_mfma_f32_16x16x32_bf16 v[176:179], a[188:191], a[156:159], v[176:179]\n" \
"ds_read_b128 a[216:219], v29 offset:4096\n" \
"v_mfma_f32_16x16x32_bf16 v[176:179], a[252:255], a[220:223], v[176:179]\n" \
"ds_read_b64_tr_b16 a[172:173], v35 offset:0\n" \
"v_mfma_f32_16x16x32_bf16 v[172:175], a[184:187], a[156:159], v[172:175]\n" \
"ds_read_b64_tr_b16 a[174:175], v35 offset:1024\n" \
"v_mfma_f32_16x16x32_bf16 v[172:175], a[248:251], a[220:223], v[172:175]\n" \
"ds_read_b64_tr_b16 a[236:237], v35 offset:8192\n" \
"v_mfma_f32_16x16x32_bf16 v[168:171], a[180:183], a[156:159], v[168:171]\n" \
"ds_read_b64_tr_b16 a[238:239], v35 offset:9216\n" \
"v_mfma_f32_16x16x32_bf16 v[168:171], a[244:247], a[220:223], v[168:171]\n" \
"v_mfma_f32_16x16x32_bf16 v[164:167], a[176:179], a[156:159], v[164:167]\n" \
"v_mfma_f32_16x16x32_bf16 v[164:167], a[240:243], a[220:223], v[164:167]\n" \
"ds_read_b128 a[156:159], v28 offset:6144\n" \
"ds_read_b128 a[220:223], v29 offset:6144\n" \
"s_waitcnt lgkmcnt(15)\n" \
"v_mfma_f32_16x16x32_bf16 a[0:3], a[160:163], a[128:131], a[0:3]\n" \
"s_addk_i32 s84, 0x7c00\n" \
"v_mfma_f32_16x16x32_bf16 a[0:3], a[224:227], a[192:195], a[0:3]\n" \
"buffer_load_dwordx4 v27, s[72:75], s84 offen offset:2048 lds\n" \
"v_mfma_f32_16x16x32_bf16 a[4:7], a[164:167], a[128:131], a[4:7]\n" \
"ds_read_b64_tr_b16 a[176:177], v36 offset:0\n" \
"s_waitcnt lgkmcnt(13)\n" \
"v_mfma_f32_16x16x32_bf16 a[4:7], a[228:231], a[192:195], a[4:7]\n" \
"s_addk_i32 s84, 0x7c00\n" \
"s_waitcnt lgkmcnt(11)\n" \
"v_mfma_f32_16x16x32_bf16 a[8:11], a[168:171], a[128:131], a[8:11]\n" \
"buffer_load_dwordx4 v27, s[72:75], s84 offen offset:3072 lds\n" \
"s_waitcnt lgkmcnt(9)\n" \
"v_mfma_f32_16x16x32_bf16 a[8:11], a[232:235], a[192:195], a[8:11]\n" \
"ds_read_b64_tr_b16 a[178:179], v36 offset:1024\n" \
"s_waitcnt lgkmcnt(6)\n" \
"v_mfma_f32_16x16x32_bf16 a[12:15], a[172:175], a[128:131], a[12:15]\n" \
"ds_read_b64_tr_b16 a[240:241], v36 offset:8192\n" \
"s_waitcnt lgkmcnt(5)\n" \
"v_mfma_f32_16x16x32_bf16 a[12:15], a[236:239], a[192:195], a[12:15]\n" \
"v_mfma_f32_16x16x32_bf16 a[28:31], a[172:175], a[132:135], a[28:31]\n" \
"v_mfma_f32_16x16x32_bf16 a[28:31], a[236:239], a[196:199], a[28:31]\n" \
"ds_read_b64_tr_b16 a[242:243], v36 offset:9216\n" \
"v_mfma_f32_16x16x32_bf16 a[24:27], a[168:171], a[132:135], a[24:27]\n" \
"v_mfma_f32_16x16x32_bf16 a[24:27], a[232:235], a[196:199], a[24:27]\n" \
"v_mfma_f32_16x16x32_bf16 a[20:23], a[164:167], a[132:135], a[20:23]\n" \
"v_mfma_f32_16x16x32_bf16 a[20:23], a[228:231], a[196:199], a[20:23]\n" \
"ds_read_b64_tr_b16 a[180:181], v37 offset:0\n" \
"v_mfma_f32_16x16x32_bf16 a[16:19], a[160:163], a[132:135], a[16:19]\n" \
"v_mfma_f32_16x16x32_bf16 a[16:19], a[224:227], a[196:199], a[16:19]\n" \
"v_mfma_f32_16x16x32_bf16 a[32:35], a[160:163], a[136:139], a[32:35]\n" \
"ds_read_b64_tr_b16 a[182:183], v37 offset:1024\n" \
"v_mfma_f32_16x16x32_bf16 a[32:35], a[224:227], a[200:203], a[32:35]\n" \
"v_mfma_f32_16x16x32_bf16 a[36:39], a[164:167], a[136:139], a[36:39]\n" \
"v_mfma_f32_16x16x32_bf16 a[36:39], a[228:231], a[200:203], a[36:39]\n" \
"buffer_store_dwordx4 v[200:203], v20, s[80:83], s35 offen sc1\n" \
"v_mfma_f32_16x16x32_bf16 a[40:43], a[168:171], a[136:139], a[40:43]\n" \
"s_mov_b32 m0, s32\n" \
"ds_read_b64_tr_b16 a[244:245], v37 offset:8192\n" \
"v_mfma_f32_16x16x32_bf16 a[40:43], a[232:235], a[200:203], a[40:43]\n" \
"s_mov_b32 exec_lo, 0x0\n" \
"s_mov_b32 exec_hi, 0xffffffff\n" \
"global_load_lds_dword v[188:189], off\n" \
"s_mov_b64 exec, -1\n" \
"v_mfma_f32_16x16x32_bf16 a[44:47], a[172:175], a[136:139], a[44:47]\n" \
"ds_read_b64_tr_b16 a[246:247], v37 offset:9216\n" \
"v_mfma_f32_16x16x32_bf16 a[44:47], a[236:239], a[200:203], a[44:47]\n" \
"v_mfma_f32_16x16x32_bf16 a[60:63], a[172:175], a[140:143], a[60:63]\n" \
"v_mfma_f32_16x16x32_bf16 a[60:63], a[236:239], a[204:207], a[60:63]\n" \
"v_mfma_f32_16x16x32_bf16 a[56:59], a[168:171], a[140:143], a[56:59]\n" \
"ds_read_b64_tr_b16 a[184:185], v38 offset:0\n" \
"v_mfma_f32_16x16x32_bf16 a[56:59], a[232:235], a[204:207], a[56:59]\n" \
"v_mfma_f32_16x16x32_bf16 a[52:55], a[164:167], a[140:143], a[52:55]\n" \
"buffer_store_dwordx4 v[204:207], v20, s[80:83], s88 offen sc1\n" \
"v_mfma_f32_16x16x32_bf16 a[52:55], a[228:231], a[204:207], a[52:55]\n" \
"ds_read_b64_tr_b16 a[186:187], v38 offset:1024\n" \
"v_mfma_f32_16x16x32_bf16 a[48:51], a[160:163], a[140:143], a[48:51]\n" \
"v_mfma_f32_16x16x32_bf16 a[48:51], a[224:227], a[204:207], a[48:51]\n" \
"v_mfma_f32_16x16x32_bf16 a[64:67], a[160:163], a[144:147], a[64:67]\n" \
"ds_read_b64_tr_b16 a[248:249], v38 offset:8192\n" \
"v_mfma_f32_16x16x32_bf16 a[64:67], a[224:227], a[208:211], a[64:67]\n" \
"v_mfma_f32_16x16x32_bf16 a[68:71], a[164:167], a[144:147], a[68:71]\n" \
"v_mfma_f32_16x16x32_bf16 a[68:71], a[228:231], a[208:211], a[68:71]\n" \
"ds_read_b64_tr_b16 a[250:251], v38 offset:9216\n" \
"v_mfma_f32_16x16x32_bf16 a[72:75], a[168:171], a[144:147], a[72:75]\n" \
"v_mfma_f32_16x16x32_bf16 a[72:75], a[232:235], a[208:211], a[72:75]\n" \
"v_mfma_f32_16x16x32_bf16 a[76:79], a[172:175], a[144:147], a[76:79]\n" \
"buffer_store_dwordx4 v[208:211], v20, s[80:83], s36 offen sc1\n" \
"v_mfma_f32_16x16x32_bf16 a[76:79], a[236:239], a[208:211], a[76:79]\n" \
"ds_read_b64_tr_b16 a[188:189], v39 offset:0\n" \
"v_mfma_f32_16x16x32_bf16 a[92:95], a[172:175], a[148:151], a[92:95]\n" \
"v_mfma_f32_16x16x32_bf16 a[92:95], a[236:239], a[212:215], a[92:95]\n" \
"v_mfma_f32_16x16x32_bf16 a[88:91], a[168:171], a[148:151], a[88:91]\n" \
"ds_read_b64_tr_b16 a[190:191], v39 offset:1024\n" \
"v_mfma_f32_16x16x32_bf16 a[88:91], a[232:235], a[212:215], a[88:91]\n" \
"v_mfma_f32_16x16x32_bf16 a[84:87], a[164:167], a[148:151], a[84:87]\n" \
"v_mfma_f32_16x16x32_bf16 a[84:87], a[228:231], a[212:215], a[84:87]\n" \
"ds_read_b64_tr_b16 a[252:253], v39 offset:8192\n" \
"v_mfma_f32_16x16x32_bf16 a[80:83], a[160:163], a[148:151], a[80:83]\n" \
"v_mfma_f32_16x16x32_bf16 a[80:83], a[224:227], a[212:215], a[80:83]\n" \
"v_mfma_f32_16x16x32_bf16 a[96:99], a[160:163], a[152:155], a[96:99]\n" \
"ds_read_b64_tr_b16 a[254:255], v39 offset:9216\n" \
"v_mfma_f32_16x16x32_bf16 a[96:99], a[224:227], a[216:219], a[96:99]\n" \
"v_mfma_f32_16x16x32_bf16 a[100:103], a[164:167], a[152:155], a[100:103]\n" \
".fill (L_pb + 12000 - .) / 4, 4, 0xBF800000\n" \
"L_pp3:\n" \
"s_cbranch_execz L_pret\n" \
"v_mfma_f32_16x16x32_bf16 a[100:103], a[228:231], a[216:219], a[100:103]\n" \
"v_mfma_f32_16x16x32_bf16 a[104:107], a[168:171], a[152:155], a[104:107]\n" \
"v_mfma_f32_16x16x32_bf16 a[104:107], a[232:235], a[216:219], a[104:107]\n" \
"v_mfma_f32_16x16x32_bf16 a[108:111], a[172:175], a[152:155], a[108:111]\n" \
"v_mfma_f32_16x16x32_bf16 a[108:111], a[236:239], a[216:219], a[108:111]\n" \
"s_waitcnt lgkmcnt(15)\n" \
"v_mfma_f32_16x16x32_bf16 a[124:127], a[172:175], a[156:159], a[124:127]\n" \
"v_mfma_f32_16x16x32_bf16 a[124:127], a[236:239], a[220:223], a[124:127]\n" \
"v_mfma_f32_16x16x32_bf16 a[120:123], a[168:171], a[156:159], a[120:123]\n" \
"v_mfma_f32_16x16x32_bf16 a[120:123], a[232:235], a[220:223], a[120:123]\n" \
"v_mfma_f32_16x16x32_bf16 a[116:119], a[164:167], a[156:159], a[116:119]\n" \
"v_mfma_f32_16x16x32_bf16 a[116:119], a[228:231], a[220:223], a[116:119]\n" \
"v_mfma_f32_16x16x32_bf16 a[112:115], a[160:163], a[156:159], a[112:115]\n" \
"v_mfma_f32_16x16x32_bf16 a[112:115], a[224:227], a[220:223], a[112:115]\n" \
"s_waitcnt vmcnt(4) lgkmcnt(0)\n" \
"s_barrier\n" \
"v_mfma_f32_16x16x32_bf16 v[52:55], a[176:179], a[128:131], v[52:55]\n" \
"s_add_u32 m0, s30, 0x0\n" \
"s_movk_i32 s84, 0x300\n" \
"v_mfma_f32_16x16x32_bf16 v[52:55], a[240:243], a[192:195], v[52:55]\n" \
"buffer_load_dwordx4 v22, s[64:67], s84 offen offset:0 lds\n" \
"v_mfma_f32_16x16x32_bf16 v[56:59], a[180:183], a[128:131], v[56:59]\n" \
"ds_read_b64_tr_b16 a[160:161], v32 offset:32768\n" \
"v_mfma_f32_16x16x32_bf16 v[56:59], a[244:247], a[192:195], v[56:59]\n" \
"s_movk_i32 s84, 0x1f00\n" \
"s_nop 0\n" \
"v_mfma_f32_16x16x32_bf16 v[60:63], a[184:187], a[128:131], v[60:63]\n" \
"buffer_load_dwordx4 v23, s[64:67], s84 offen offset:1024 lds\n" \
"v_mfma_f32_16x16x32_bf16 v[60:63], a[248:251], a[192:195], v[60:63]\n" \
"ds_read_b64_tr_b16 a[162:163], v32 offset:33792\n" \
"v_mfma_f32_16x16x32_bf16 v[64:67], a[188:191], a[128:131], v[64:67]\n" \
"ds_read_b64_tr_b16 a[224:225], v32 offset:40960\n" \
"v_mfma_f32_16x16x32_bf16 v[64:67], a[252:255], a[192:195], v[64:67]\n" \
"ds_read_b128 a[128:131], v28 offset:49152\n" \
"v_mfma_f32_16x16x32_bf16 v[80:83], a[188:191], a[132:135], v[80:83]\n" \
"ds_read_b128 a[192:195], v29 offset:49152\n" \
"v_mfma_f32_16x16x32_bf16 v[80:83], a[252:255], a[196:199], v[80:83]\n" \
"s_movk_i32 s84, 0x3b00\n" \
"v_mfma_f32_16x16x32_bf16 v[76:79], a[184:187], a[132:135], v[76:79]\n" \
"buffer_load_dwordx4 v22, s[64:67], s84 offen offset:2048 lds\n" \
"v_mfma_f32_16x16x32_bf16 v[76:79], a[248:251], a[196:199], v[76:79]\n" \
"s_movk_i32 s84, 0x5700\n" \
"v_mfma_f32_16x16x32_bf16 v[72:75], a[180:183], a[132:135], v[72:75]\n" \
"buffer_load_dwordx4 v23, s[64:67], s84 offen offset:3072 lds\n" \
"v_mfma_f32_16x16x32_bf16 v[72:75], a[244:247], a[196:199], v[72:75]\n" \
"s_add_u32 m0, s31, 0x0\n" \
"s_mov_b32 s84, 0x300000\n" \
"v_mfma_f32_16x16x32_bf16 v[68:71], a[176:179], a[132:135], v[68:71]\n" \
"buffer_load_dwordx4 v26, s[68:71], s84 offen offset:0 lds\n" \
"v_mfma_f32_16x16x32_bf16 v[68:71], a[240:243], a[196:199], v[68:71]\n" \
"ds_read_b128 a[132:135], v28 offset:51200\n" \
"v_mfma_f32_16x16x32_bf16 v[84:87], a[176:179], a[136:139], v[84:87]\n" \
"ds_read_b128 a[196:199], v29 offset:51200\n" \
"v_mfma_f32_16x16x32_bf16 v[84:87], a[240:243], a[200:203], v[84:87]\n" \
"s_addk_i32 s84, 0x7c00\n" \
"v_mfma_f32_16x16x32_bf16 v[88:91], a[180:183], a[136:139], v[88:91]\n" \
"buffer_load_dwordx4 v26, s[68:71], s84 offen offset:1024 lds\n" \
"v_mfma_f32_16x16x32_bf16 v[88:91], a[244:247], a[200:203], v[88:91]\n" \
"ds_read_b64_tr_b16 a[226:227], v32 offset:41984\n" \
"v_mfma_f32_16x16x32_bf16 v[92:95], a[184:187], a[136:139], v[92:95]\n" \
"s_addk_i32 s84, 0x7c00\n" \
"v_mfma_f32_16x16x32_bf16 v[92:95], a[248:251], a[200:203], v[92:95]\n" \
"buffer_load_dwordx4 v27, s[68:71], s84 offen offset:2048 lds\n" \
"v_mfma_f32_16x16x32_bf16 v[96:99], a[188:191], a[136:139], v[96:99]\n" \
"ds_read_b64_tr_b16 a[164:165], v33 offset:32768\n" \
"v_mfma_f32_16x16x32_bf16 v[96:99], a[252:255], a[200:203], v[96:99]\n" \
"ds_read_b128 a[136:139], v28 offset:53248\n" \
"v_mfma_f32_16x16x32_bf16 v[112:115], a[188:191], a[140:143], v[112:115]\n" \
"ds_read_b128 a[200:203], v29 offset:53248\n" \
"v_mfma_f32_16x16x32_bf16 v[112:115], a[252:255], a[204:207], v[112:115]\n" \
"s_addk_i32 s84, 0x7c00\n" \
"v_mfma_f32_16x16x32_bf16 v[108:111], a[184:187], a[140:143], v[108:111]\n" \
"buffer_load_dwordx4 v27, s[68:71], s84 offen offset:3072 lds\n" \
"v_mfma_f32_16x16x32_bf16 v[108:111], a[248:251], a[204:207], v[108:111]\n" \
"s_add_u32 m0, s30, 0x1000\n" \
"s_mov_b32 s84, 0x8300\n" \
"v_mfma_f32_16x16x32_bf16 v[104:107], a[180:183], a[140:143], v[104:107]\n" \
"buffer_load_dwordx4 v22, s[64:67], s84 offen offset:0 lds\n" \
"v_mfma_f32_16x16x32_bf16 v[104:107], a[244:247], a[204:207], v[104:107]\n" \
"ds_read_b64_tr_b16 a[166:167], v33 offset:33792\n" \
"v_mfma_f32_16x16x32_bf16 v[100:103], a[176:179], a[140:143], v[100:103]\n" \
"ds_read_b64_tr_b16 a[228:229], v33 offset:40960\n" \
"v_mfma_f32_16x16x32_bf16 v[100:103], a[240:243], a[204:207], v[100:103]\n" \
"ds_read_b128 a[140:143], v28 offset:55296\n" \
"v_mfma_f32_16x16x32_bf16 v[116:119], a[176:179], a[144:147], v[116:119]\n" \
"ds_read_b128 a[204:207], v29 offset:55296\n" \
"v_mfma_f32_16x16x32_bf16 v[116:119], a[240:243], a[208:211], v[116:119]\n" \
"s_addk_i32 s84, 0x1c00\n" \
"v_mfma_f32_16x16x32_bf16 v[120:123], a[180:183], a[144:147], v[120:123]\n" \
"buffer_load_dwordx4 v23, s[64:67], s84 offen offset:1024 lds\n" \
"v_mfma_f32_16x16x32_bf16 v[120:123], a[244:247], a[208:211], v[120:123]\n" \
"s_addk_i32 s84, 0x1c00\n" \
"v_mfma_f32_16x16x32_bf16 v[124:127], a[184:187], a[144:147], v[124:127]\n" \
"buffer_load_dwordx4 v22, s[64:67], s84 offen offset:2048 lds\n" \
"v_mfma_f32_16x16x32_bf16 v[124:127], a[248:251], a[208:211], v[124:127]\n" \
"s_addk_i32 s84, 0x1c00\n" \
"s_nop 0\n" \
"v_mfma_f32_16x16x32_bf16 v[128:131], a[188:191], a[144:147], v[128:131]\n" \
"buffer_load_dwordx4 v23, s[64:67], s84 offen offset:3072 lds\n" \
"v_mfma_f32_16x16x32_bf16 v[128:131], a[252:255], a[208:211], v[128:131]\n" \
"ds_read_b128 a[144:147], v28 offset:32768\n" \
"v_mfma_f32_16x16x32_bf16 v[144:147], a[188:191], a[148:151], v[144:147]\n" \
"ds_read_b128 a[208:211], v29 offset:32768\n" \
"v_mfma_f32_16x16x32_bf16 v[144:147], a[252:255], a[212:215], v[144:147]\n" \
"s_add_u32 m0, s31, 0x4000\n" \
"s_mov_b32 s84, 0x300000\n" \
"v_mfma_f32_16x16x32_bf16 v[140:143], a[184:187], a[148:151], v[140:143]\n" \
"buffer_load_dwordx4 v26, s[72:75], s84 offen offset:0 lds\n" \
"v_mfma_f32_16x16x32_bf16 v[140:143], a[248:251], a[212:215], v[140:143]\n" \
"ds_read_b64_tr_b16 a[230:231], v33 offset:41984\n" \
"v_mfma_f32_16x16x32_bf16 v[136:139], a[180:183], a[148:151], v[136:139]\n" \
"s_addk_i32 s84, 0x7c00\n" \
"s_nop 0\n" \
"v_mfma_f32_16x16x32_bf16 v[136:139], a[244:247], a[212:215], v[136:139]\n" \
"buffer_load_dwordx4 v26, s[72:75], s84 offen offset:1024 lds\n" \
"v_mfma_f32_16x16x32_bf16 v[132:135], a[176:179], a[148:151], v[132:135]\n" \
"ds_read_b64_tr_b16 a[168:169], v34 offset:32768\n" \
"v_mfma_f32_16x16x32_bf16 v[132:135], a[240:243], a[212:215], v[132:135]\n" \
"ds_read_b128 a[148:151], v28 offset:34816\n" \
"v_mfma_f32_16x16x32_bf16 v[148:151], a[176:179], a[152:155], v[148:151]\n" \
"ds_read_b128 a[212:215], v29 offset:34816\n" \
"v_mfma_f32_16x16x32_bf16 v[148:151], a[240:243], a[216:219], v[148:151]\n" \
"ds_read_b64_tr_b16 a[170:171], v34 offset:33792\n" \
"v_mfma_f32_16x16x32_bf16 v[152:155], a[180:183], a[152:155], v[152:155]\n" \
"ds_read_b64_tr_b16 a[232:233], v34 offset:40960\n" \
"v_mfma_f32_16x16x32_bf16 v[152:155], a[244:247], a[216:219], v[152:155]\n" \
"ds_read_b64_tr_b16 a[234:235], v34 offset:41984\n" \
"v_mfma_f32_16x16x32_bf16 v[156:159], a[184:187], a[152:155], v[156:159]\n" \
"ds_read_b64_tr_b16 a[172:173], v35 offset:32768\n" \
"v_mfma_f32_16x16x32_bf16 v[156:159], a[248:251], a[216:219], v[156:159]\n" \
"ds_read_b64_tr_b16 a[174:175], v35 offset:33792\n" \
"v_mfma_f32_16x16x32_bf16 v[160:163], a[188:191], a[152:155], v[160:163]\n" \
"ds_read_b64_tr_b16 a[236:237], v35 offset:40960\n" \
"v_mfma_f32_16x16x32_bf16 v[160:163], a[252:255], a[216:219], v[160:163]\n" \
"ds_read_b128 a[152:155], v28 offset:36864\n" \
"v_mfma_f32_16x16x32_bf16 v[176:179], a[188:191], a[156:159], v[176:179]\n" \
"ds_read_b128 a[216:219], v29 offset:36864\n" \
"v_mfma_f32_16x16x32_bf16 v[176:179], a[252:255], a[220:223], v[176:179]\n" \
"ds_read_b64_tr_b16 a[238:239], v35 offset:41984\n" \
"v_mfma_f32_16x16x32_bf16 v[172:175], a[184:187], a[156:159], v[172:175]\n" \
"v_mfma_f32_16x16x32_bf16 v[172:175], a[248:251], a[220:223], v[172:175]\n" \
"v_mfma_f32_16x16x32_bf16 v[168:171], a[180:183], a[156:159], v[168:171]\n" \
"v_mfma_f32_16x16x32_bf16 v[168:171], a[244:247], a[220:223], v[168:171]\n" \
"v_mfma_f32_16x16x32_bf16 v[164:167], a[176:179], a[156:159], v[164:167]\n" \
"v_mfma_f32_16x16x32_bf16 v[164:167], a[240:243], a[220:223], v[164:167]\n" \
"ds_read_b128 a[156:159], v28 offset:38912\n" \
"ds_read_b128 a[220:223], v29 offset:38912\n" \
"s_waitcnt lgkmcnt(15)\n" \
"v_mfma_f32_16x16x32_bf16 a[0:3], a[160:163], a[128:131], a[0:3]\n" \
"s_addk_i32 s84, 0x7c00\n" \
"v_mfma_f32_16x16x32_bf16 a[0:3], a[224:227], a[192:195], a[0:3]\n" \
"buffer_load_dwordx4 v27, s[72:75], s84 offen offset:2048 lds\n" \
"v_mfma_f32_16x16x32_bf16 a[4:7], a[164:167], a[128:131], a[4:7]\n" \
"ds_read_b64_tr_b16 a[176:177], v36 offset:32768\n" \
"s_waitcnt lgkmcnt(15)\n" \
"v_mfma_f32_16x16x32_bf16 a[4:7], a[228:231], a[192:195], a[4:7]\n" \
"s_addk_i32 s84, 0x7c00\n" \
"s_waitcnt lgkmcnt(11)\n" \
"v_mfma_f32_16x16x32_bf16 a[8:11], a[168:171], a[128:131], a[8:11]\n" \
"buffer_load_dwordx4 v27, s[72:75], s84 offen offset:3072 lds\n" \
"s_waitcnt lgkmcnt(9)\n" \
"v_mfma_f32_16x16x32_bf16 a[8:11], a[232:235], a[192:195], a[8:11]\n" \
"ds_read_b64_tr_b16 a[178:179], v36 offset:33792\n" \
"s_waitcnt lgkmcnt(8)\n" \
"v_mfma_f32_16x16x32_bf16 a[12:15], a[172:175], a[128:131], a[12:15]\n" \
"ds_read_b64_tr_b16 a[240:241], v36 offset:40960\n" \
"s_waitcnt lgkmcnt(5)\n" \
"v_mfma_f32_16x16x32_bf16 a[12:15], a[236:239], a[192:195], a[12:15]\n" \
"v_mfma_f32_16x16x32_bf16 a[28:31], a[172:175], a[132:135], a[28:31]\n" \
"v_mfma_f32_16x16x32_bf16 a[28:31], a[236:239], a[196:199], a[28:31]\n" \
"ds_read_b64_tr_b16 a[242:243], v36 offset:41984\n" \
"v_mfma_f32_16x16x32_bf16 a[24:27], a[168:171], a[132:135], a[24:27]\n" \
"v_mfma_f32_16x16x32_bf16 a[24:27], a[232:235], a[196:199], a[24:27]\n" \
"v_mfma_f32_16x16x32_bf16 a[20:23], a[164:167], a[132:135], a[20:23]\n" \
"v_mfma_f32_16x16x32_bf16 a[20:23], a[228:231], a[196:199], a[20:23]\n" \
"ds_read_b64_tr_b16 a[180:181], v37 offset:32768\n" \
"v_mfma_f32_16x16x32_bf16 a[16:19], a[160:163], a[132:135], a[16:19]\n" \
"v_mfma_f32_16x16x32_bf16 a[16:19], a[224:227], a[196:199], a[16:19]\n" \
"v_mfma_f32_16x16x32_bf16 a[32:35], a[160:163], a[136:139], a[32:35]\n" \
"ds_read_b64_tr_b16 a[182:183], v37 offset:33792\n" \
"v_mfma_f32_16x16x32_bf16 a[32:35], a[224:227], a[200:203], a[32:35]\n" \
"v_mfma_f32_16x16x32_bf16 a[36:39], a[164:167], a[136:139], a[36:39]\n" \
"v_lshlrev_b32_e32 v45, 3, v20\n" \
"v_add_u32_e32 v45, s31, v45\n" \
"v_mfma_f32_16x16x32_bf16 a[36:39], a[228:231], a[200:203], a[36:39]\n" \
"s_getpc_b64 s[86:87]\n" \
"s_sub_u32 s86, s86, 0xf800\n" \
"s_subb_u32 s87, s87, 0\n" \
"s_mov_b32 m0, s32\n" \
"s_nop 0\n" \
"v_mfma_f32_16x16x32_bf16 a[40:43], a[168:171], a[136:139], a[40:43]\n" \
"global_load_lds_dword v45, s[86:87]\n" \
"v_mfma_f32_16x16x32_bf16 a[40:43], a[232:235], a[200:203], a[40:43]\n" \
"ds_read_b64_tr_b16 a[244:245], v37 offset:40960\n" \
"v_mfma_f32_16x16x32_bf16 a[44:47], a[172:175], a[136:139], a[44:47]\n" \
"ds_read_b64_tr_b16 a[246:247], v37 offset:41984\n" \
"v_mfma_f32_16x16x32_bf16 a[44:47], a[236:239], a[200:203], a[44:47]\n" \
"v_mfma_f32_16x16x32_bf16 a[60:63], a[172:175], a[140:143], a[60:63]\n" \
"v_mfma_f32_16x16x32_bf16 a[60:63], a[236:239], a[204:207], a[60:63]\n" \
"v_mfma_f32_16x16x32_bf16 a[56:59], a[168:171], a[140:143], a[56:59]\n" \
"ds_read_b64_tr_b16 a[184:185], v38 offset:32768\n" \
"v_mfma_f32_16x16x32_bf16 a[56:59], a[232:235], a[204:207], a[56:59]\n" \
"v_mfma_f32_16x16x32_bf16 a[52:55], a[164:167], a[140:143], a[52:55]\n" \
"v_mfma_f32_16x16x32_bf16 a[52:55], a[228:231], a[204:207], a[52:55]\n" \
"ds_read_b64_tr_b16 a[186:187], v38 offset:33792\n" \
"v_mfma_f32_16x16x32_bf16 a[48:51], a[160:163], a[140:143], a[48:51]\n" \
"v_mfma_f32_16x16x32_bf16 a[48:51], a[224:227], a[204:207], a[48:51]\n" \
"v_mfma_f32_16x16x32_bf16 a[64:67], a[160:163], a[144:147], a[64:67]\n" \
"ds_read_b64_tr_b16 a[248:249], v38 offset:40960\n" \
"v_mfma_f32_16x16x32_bf16 a[64:67], a[224:227], a[208:211], a[64:67]\n" \
"v_mfma_f32_16x16x32_bf16 a[68:71], a[164:167], a[144:147], a[68:71]\n" \
"v_mfma_f32_16x16x32_bf16 a[68:71], a[228:231], a[208:211], a[68:71]\n" \
"ds_read_b64_tr_b16 a[250:251], v38 offset:41984\n" \
"v_mfma_f32_16x16x32_bf16 a[72:75], a[168:171], a[144:147], a[72:75]\n" \
"v_mfma_f32_16x16x32_bf16 a[72:75], a[232:235], a[208:211], a[72:75]\n" \
"v_mfma_f32_16x16x32_bf16 a[76:79], a[172:175], a[144:147], a[76:79]\n" \
"v_mfma_f32_16x16x32_bf16 a[76:79], a[236:239], a[208:211], a[76:79]\n" \
"ds_read_b64_tr_b16 a[188:189], v39 offset:32768\n" \
"v_mfma_f32_16x16x32_bf16 a[92:95], a[172:175], a[148:151], a[92:95]\n" \
"v_mfma_f32_16x16x32_bf16 a[92:95], a[236:239], a[212:215], a[92:95]\n" \
"v_mfma_f32_16x16x32_bf16 a[88:91], a[168:171], a[148:151], a[88:91]\n" \
"ds_read_b64_tr_b16 a[190:191], v39 offset:33792\n" \
"v_mfma_f32_16x16x32_bf16 a[88:91], a[232:235], a[212:215], a[88:91]\n" \
"v_mfma_f32_16x16x32_bf16 a[84:87], a[164:167], a[148:151], a[84:87]\n" \
"v_mfma_f32_16x16x32_bf16 a[84:87], a[228:231], a[212:215], a[84:87]\n" \
"ds_read_b64_tr_b16 a[252:253], v39 offset:40960\n" \
"v_mfma_f32_16x16x32_bf16 a[80:83], a[160:163], a[148:151], a[80:83]\n" \
"v_mfma_f32_16x16x32_bf16 a[80:83], a[224:227], a[212:215], a[80:83]\n" \
"v_mfma_f32_16x16x32_bf16 a[96:99], a[160:163], a[152:155], a[96:99]\n" \
"ds_read_b64_tr_b16 a[254:255], v39 offset:41984\n" \
"v_mfma_f32_16x16x32_bf16 a[96:99], a[224:227], a[216:219], a[96:99]\n" \
"v_mfma_f32_16x16x32_bf16 a[100:103], a[164:167], a[152:155], a[100:103]\n" \
"v_mfma_f32_16x16x32_bf16 a[100:103], a[228:231], a[216:219], a[100:103]\n" \
"v_mfma_f32_16x16x32_bf16 a[104:107], a[168:171], a[152:155], a[104:107]\n" \
"v_mfma_f32_16x16x32_bf16 a[104:107], a[232:235], a[216:219], a[104:107]\n" \
"v_mfma_f32_16x16x32_bf16 a[108:111], a[172:175], a[152:155], a[108:111]\n" \
"v_mfma_f32_16x16x32_bf16 a[108:111], a[236:239], a[216:219], a[108:111]\n" \
"s_waitcnt lgkmcnt(15)\n" \
"v_mfma_f32_16x16x32_bf16 a[124:127], a[172:175], a[156:159], a[124:127]\n" \
"v_mfma_f32_16x16x32_bf16 a[124:127], a[236:239], a[220:223], a[124:127]\n" \
"v_mfma_f32_16x16x32_bf16 a[120:123], a[168:171], a[156:159], a[120:123]\n" \
"v_mfma_f32_16x16x32_bf16 a[120:123], a[232:235], a[220:223], a[120:123]\n" \
"v_mfma_f32_16x16x32_bf16 a[116:119], a[164:167], a[156:159], a[116:119]\n" \
"v_mfma_f32_16x16x32_bf16 a[116:119], a[228:231], a[220:223], a[116:119]\n" \
"v_mfma_f32_16x16x32_bf16 a[112:115], a[160:163], a[156:159], a[112:115]\n" \
"v_mfma_f32_16x16x32_bf16 a[112:115], a[224:227], a[220:223], a[112:115]\n" \
"s_waitcnt vmcnt(1) lgkmcnt(0)\n" \
"s_barrier\n" \
"v_mov_b32_e32 v45, s62\n" \
"s_mov_b64 exec, 1\n" \
"buffer_store_dword v45, off, s[80:83], s37 offset:128 sc1\n" \
"s_mov_b64 exec, -1\n" \
"v_mfma_f32_16x16x32_bf16 v[52:55], a[176:179], a[128:131], v[52:55]\n" \
"s_add_u32 m0, s30, 0x8000\n" \
"s_movk_i32 s84, 0x380\n" \
"v_mfma_f32_16x16x32_bf16 v[52:55], a[240:243], a[192:195], v[52:55]\n" \
"buffer_load_dwordx4 v22, s[64:67], s84 offen offset:0 lds\n" \
"v_mfma_f32_16x16x32_bf16 v[56:59], a[180:183], a[128:131], v[56:59]\n" \
"ds_read_b64_tr_b16 a[160:161], v32 offset:0\n" \
"v_mfma_f32_16x16x32_bf16 v[56:59], a[244:247], a[192:195], v[56:59]\n" \
"s_movk_i32 s84, 0x1f80\n" \
"v_mfma_f32_16x16x32_bf16 v[60:63], a[184:187], a[128:131], v[60:63]\n" \
"buffer_load_dwordx4 v23, s[64:67], s84 offen offset:1024 lds\n" \
"v_mfma_f32_16x16x32_bf16 v[60:63], a[248:251], a[192:195], v[60:63]\n" \
"ds_read_b64_tr_b16 a[162:163], v32 offset:1024\n" \
"v_mfma_f32_16x16x32_bf16 v[64:67], a[188:191], a[128:131], v[64:67]\n" \
"ds_read_b64_tr_b16 a[224:225], v32 offset:8192\n" \
"v_mfma_f32_16x16x32_bf16 v[64:67], a[252:255], a[192:195], v[64:67]\n" \
"ds_read_b128 a[128:131], v28 offset:16384\n" \
"v_mfma_f32_16x16x32_bf16 v[80:83], a[188:191], a[132:135], v[80:83]\n" \
"ds_read_b128 a[192:195], v29 offset:16384\n" \
"v_mfma_f32_16x16x32_bf16 v[80:83], a[252:255], a[196:199], v[80:83]\n" \
"s_movk_i32 s84, 0x3b80\n" \
"v_mfma_f32_16x16x32_bf16 v[76:79], a[184:187], a[132:135], v[76:79]\n" \
"buffer_load_dwordx4 v22, s[64:67], s84 offen offset:2048 lds\n" \
"v_mfma_f32_16x16x32_bf16 v[76:79], a[248:251], a[196:199], v[76:79]\n" \
"s_movk_i32 s84, 0x5780\n" \
"v_mfma_f32_16x16x32_bf16 v[72:75], a[180:183], a[132:135], v[72:75]\n" \
"buffer_load_dwordx4 v23, s[64:67], s84 offen offset:3072 lds\n" \
"v_mfma_f32_16x16x32_bf16 v[72:75], a[244:247], a[196:199], v[72:75]\n" \
"s_add_u32 m0, s31, 0x8000\n" \
"s_mov_b32 s84, 0x380000\n" \
"v_mfma_f32_16x16x32_bf16 v[68:71], a[176:179], a[132:135], v[68:71]\n" \
"buffer_load_dwordx4 v26, s[68:71], s84 offen offset:0 lds\n" \
"v_mfma_f32_16x16x32_bf16 v[68:71], a[240:243], a[196:199], v[68:71]\n" \
"ds_read_b128 a[132:135], v28 offset:18432\n" \
"v_mfma_f32_16x16x32_bf16 v[84:87], a[176:179], a[136:139], v[84:87]\n" \
"ds_read_b128 a[196:199], v29 offset:18432\n" \
"v_mfma_f32_16x16x32_bf16 v[84:87], a[240:243], a[200:203], v[84:87]\n" \
"s_addk_i32 s84, 0x7c00\n" \
"v_mfma_f32_16x16x32_bf16 v[88:91], a[180:183], a[136:139], v[88:91]\n" \
"buffer_load_dwordx4 v26, s[68:71], s84 offen offset:1024 lds\n" \
"v_mfma_f32_16x16x32_bf16 v[88:91], a[244:247], a[200:203], v[88:91]\n" \
"ds_read_b64_tr_b16 a[226:227], v32 offset:9216\n" \
"v_mfma_f32_16x16x32_bf16 v[92:95], a[184:187], a[136:139], v[92:95]\n" \
"s_addk_i32 s84, 0x7c00\n" \
"v_mfma_f32_16x16x32_bf16 v[92:95], a[248:251], a[200:203], v[92:95]\n" \
"buffer_load_dwordx4 v27, s[68:71], s84 offen offset:2048 lds\n" \
"v_mfma_f32_16x16x32_bf16 v[96:99], a[188:191], a[136:139], v[96:99]\n" \
"ds_read_b64_tr_b16 a[164:165], v33 offset:0\n" \
"v_mfma_f32_16x16x32_bf16 v[96:99], a[252:255], a[200:203], v[96:99]\n" \
"ds_read_b128 a[136:139], v28 offset:20480\n" \
"v_mfma_f32_16x16x32_bf16 v[112:115], a[188:191], a[140:143], v[112:115]\n" \
"ds_read_b128 a[200:203], v29 offset:20480\n" \
"v_mfma_f32_16x16x32_bf16 v[112:115], a[252:255], a[204:207], v[112:115]\n" \
"s_addk_i32 s84, 0x7c00\n" \
"s_nop 0\n" \
"v_mfma_f32_16x16x32_bf16 v[108:111], a[184:187], a[140:143], v[108:111]\n" \
"buffer_load_dwordx4 v27, s[68:71], s84 offen offset:3072 lds\n" \
"v_mfma_f32_16x16x32_bf16 v[108:111], a[248:251], a[204:207], v[108:111]\n" \
"s_add_u32 m0, s30, 0x9000\n" \
"s_mov_b32 s84, 0x8380\n" \
"v_mfma_f32_16x16x32_bf16 v[104:107], a[180:183], a[140:143], v[104:107]\n" \
"buffer_load_dwordx4 v22, s[64:67], s84 offen offset:0 lds\n" \
"v_mfma_f32_16x16x32_bf16 v[104:107], a[244:247], a[204:207], v[104:107]\n" \
"ds_read_b64_tr_b16 a[166:167], v33 offset:1024\n" \
"v_mfma_f32_16x16x32_bf16 v[100:103], a[176:179], a[140:143], v[100:103]\n" \
"ds_read_b64_tr_b16 a[228:229], v33 offset:8192\n" \
"v_mfma_f32_16x16x32_bf16 v[100:103], a[240:243], a[204:207], v[100:103]\n" \
"ds_read_b128 a[140:143], v28 offset:22528\n" \
"v_mfma_f32_16x16x32_bf16 v[116:119], a[176:179], a[144:147], v[116:119]\n" \
"ds_read_b128 a[204:207], v29 offset:22528\n" \
"v_mfma_f32_16x16x32_bf16 v[116:119], a[240:243], a[208:211], v[116:119]\n" \
"s_addk_i32 s84, 0x1c00\n" \
"v_mfma_f32_16x16x32_bf16 v[120:123], a[180:183], a[144:147], v[120:123]\n" \
"buffer_load_dwordx4 v23, s[64:67], s84 offen offset:1024 lds\n" \
"v_mfma_f32_16x16x32_bf16 v[120:123], a[244:247], a[208:211], v[120:123]\n" \
"s_addk_i32 s84, 0x1c00\n" \
"v_mfma_f32_16x16x32_bf16 v[124:127], a[184:187], a[144:147], v[124:127]\n" \
"buffer_load_dwordx4 v22, s[64:67], s84 offen offset:2048 lds\n" \
"v_mfma_f32_16x16x32_bf16 v[124:127], a[248:251], a[208:211], v[124:127]\n" \
"s_addk_i32 s84, 0x1c00\n" \
"s_nop 0\n" \
"v_mfma_f32_16x16x32_bf16 v[128:131], a[188:191], a[144:147], v[128:131]\n" \
"buffer_load_dwordx4 v23, s[64:67], s84 offen offset:3072 lds\n" \
"v_mfma_f32_16x16x32_bf16 v[128:131], a[252:255], a[208:211], v[128:131]\n" \
"ds_read_b128 a[144:147], v28 offset:0\n" \
"v_mfma_f32_16x16x32_bf16 v[144:147], a[188:191], a[148:151], v[144:147]\n" \
"ds_read_b128 a[208:211], v29 offset:0\n" \
"v_mfma_f32_16x16x32_bf16 v[144:147], a[252:255], a[212:215], v[144:147]\n" \
"s_add_u32 m0, s31, 0xc000\n" \
"s_mov_b32 s84, 0x380000\n" \
"v_mfma_f32_16x16x32_bf16 v[140:143], a[184:187], a[148:151], v[140:143]\n" \
"buffer_load_dwordx4 v26, s[72:75], s84 offen offset:0 lds\n" \
"v_mfma_f32_16x16x32_bf16 v[140:143], a[248:251], a[212:215], v[140:143]\n" \
"ds_read_b64_tr_b16 a[230:231], v33 offset:9216\n" \
"v_mfma_f32_16x16x32_bf16 v[136:139], a[180:183], a[148:151], v[136:139]\n" \
"s_addk_i32 s84, 0x7c00\n" \
"s_nop 0\n" \
"v_mfma_f32_16x16x32_bf16 v[136:139], a[244:247], a[212:215], v[136:139]\n" \
"buffer_load_dwordx4 v26, s[72:75], s84 offen offset:1024 lds\n" \
"v_mfma_f32_16x16x32_bf16 v[132:135], a[176:179], a[148:151], v[132:135]\n" \
"ds_read_b64_tr_b16 a[168:169], v34 offset:0\n" \
"v_mfma_f32_16x16x32_bf16 v[132:135], a[240:243], a[212:215], v[132:135]\n" \
"ds_read_b128 a[148:151], v28 offset:2048\n" \
"v_mfma_f32_16x16x32_bf16 v[148:151], a[176:179], a[152:155], v[148:151]\n" \
"ds_read_b128 a[212:215], v29 offset:2048\n" \
"v_mfma_f32_16x16x32_bf16 v[148:151], a[240:243], a[216:219], v[148:151]\n" \
"ds_read_b64_tr_b16 a[170:171], v34 offset:1024\n" \
"v_mfma_f32_16x16x32_bf16 v[152:155], a[180:183], a[152:155], v[152:155]\n" \
"ds_read_b64_tr_b16 a[232:233], v34 offset:8192\n" \
"v_mfma_f32_16x16x32_bf16 v[152:155], a[244:247], a[216:219], v[152:155]\n" \
"ds_read_b64_tr_b16 a[234:235], v34 offset:9216\n" \
"v_mfma_f32_16x16x32_bf16 v[156:159], a[184:187], a[152:155], v[156:159]\n" \
"ds_read_b64_tr_b16 a[172:173], v35 offset:0\n" \
"v_mfma_f32_16x16x32_bf16 v[156:159], a[248:251], a[216:219], v[156:159]\n" \
"ds_read_b64_tr_b16 a[174:175], v35 offset:1024\n" \
"v_mfma_f32_16x16x32_bf16 v[160:163], a[188:191], a[152:155], v[160:163]\n" \
"ds_read_b64_tr_b16 a[236:237], v35 offset:8192\n" \
"v_mfma_f32_16x16x32_bf16 v[160:163], a[252:255], a[216:219], v[160:163]\n" \
"ds_read_b128 a[152:155], v28 offset:4096\n" \
"v_mfma_f32_16x16x32_bf16 v[176:179], a[188:191], a[156:159], v[176:179]\n" \
"ds_read_b128 a[216:219], v29 offset:4096\n" \
"v_mfma_f32_16x16x32_bf16 v[176:179], a[252:255], a[220:223], v[176:179]\n" \
"ds_read_b64_tr_b16 a[238:239], v35 offset:9216\n" \
"v_mfma_f32_16x16x32_bf16 v[172:175], a[184:187], a[156:159], v[172:175]\n" \
"v_mfma_f32_16x16x32_bf16 v[172:175], a[248:251], a[220:223], v[172:175]\n" \
"v_mfma_f32_16x16x32_bf16 v[168:171], a[180:183], a[156:159], v[168:171]\n" \
"v_mfma_f32_16x16x32_bf16 v[168:171], a[244:247], a[220:223], v[168:171]\n" \
"v_mfma_f32_16x16x32_bf16 v[164:167], a[176:179], a[156:159], v[164:167]\n" \
"v_mfma_f32_16x16x32_bf16 v[164:167], a[240:243], a[220:223], v[164:167]\n" \
"ds_read_b128 a[156:159], v28 offset:6144\n" \
"ds_read_b128 a[220:223], v29 offset:6144\n" \
"s_waitcnt lgkmcnt(15)\n" \
"v_mfma_f32_16x16x32_bf16 a[0:3], a[160:163], a[128:131], a[0:3]\n" \
"s_addk_i32 s84, 0x7c00\n" \
"v_mfma_f32_16x16x32_bf16 a[0:3], a[224:227], a[192:195], a[0:3]\n" \
"buffer_load_dwordx4 v27, s[72:75], s84 offen offset:2048 lds\n" \
"v_mfma_f32_16x16x32_bf16 a[4:7], a[164:167], a[128:131], a[4:7]\n" \
"ds_read_b64_tr_b16 a[176:177], v36 offset:0\n" \
"s_waitcnt lgkmcnt(15)\n" \
"v_mfma_f32_16x16x32_bf16 a[4:7], a[228:231], a[192:195], a[4:7]\n" \
"s_addk_i32 s84, 0x7c00\n" \
"s_waitcnt lgkmcnt(11)\n" \
"v_mfma_f32_16x16x32_bf16 a[8:11], a[168:171], a[128:131], a[8:11]\n" \
"buffer_load_dwordx4 v27, s[72:75], s84 offen offset:3072 lds\n" \
"s_waitcnt lgkmcnt(9)\n" \
"v_mfma_f32_16x16x32_bf16 a[8:11], a[232:235], a[192:195], a[8:11]\n" \
"ds_read_b64_tr_b16 a[178:179], v36 offset:1024\n" \
"s_waitcnt lgkmcnt(8)\n" \
"v_mfma_f32_16x16x32_bf16 a[12:15], a[172:175], a[128:131], a[12:15]\n" \
"ds_read_b64_tr_b16 a[240:241], v36 offset:8192\n" \
"s_waitcnt lgkmcnt(5)\n" \
"v_mfma_f32_16x16x32_bf16 a[12:15], a[236:239], a[192:195], a[12:15]\n" \
"v_mfma_f32_16x16x32_bf16 a[28:31], a[172:175], a[132:135], a[28:31]\n" \
"v_mfma_f32_16x16x32_bf16 a[28:31], a[236:239], a[196:199], a[28:31]\n" \
"ds_read_b64_tr_b16 a[242:243], v36 offset:9216\n" \
"v_mfma_f32_16x16x32_bf16 a[24:27], a[168:171], a[132:135], a[24:27]\n" \
"v_mfma_f32_16x16x32_bf16 a[24:27], a[232:235], a[196:199], a[24:27]\n" \
"v_mfma_f32_16x16x32_bf16 a[20:23], a[164:167], a[132:135], a[20:23]\n" \
"v_mfma_f32_16x16x32_bf16 a[20:23], a[228:231], a[196:199], a[20:23]\n" \
"ds_read_b64_tr_b16 a[180:181], v37 offset:0\n" \
"v_mfma_f32_16x16x32_bf16 a[16:19], a[160:163], a[132:135], a[16:19]\n" \
"v_mfma_f32_16x16x32_bf16 a[16:19], a[224:227], a[196:199], a[16:19]\n" \
"v_mfma_f32_16x16x32_bf16 a[32:35], a[160:163], a[136:139], a[32:35]\n" \
"ds_read_b64_tr_b16 a[182:183], v37 offset:1024\n" \
"v_mfma_f32_16x16x32_bf16 a[32:35], a[224:227], a[200:203], a[32:35]\n" \
"v_mfma_f32_16x16x32_bf16 a[36:39], a[164:167], a[136:139], a[36:39]\n" \
"v_mfma_f32_16x16x32_bf16 a[36:39], a[228:231], a[200:203], a[36:39]\n" \
"ds_read_b64_tr_b16 a[244:245], v37 offset:8192\n" \
"v_mfma_f32_16x16x32_bf16 a[40:43], a[168:171], a[136:139], a[40:43]\n" \
"v_mfma_f32_16x16x32_bf16 a[40:43], a[232:235], a[200:203], a[40:43]\n" \
"v_mfma_f32_16x16x32_bf16 a[44:47], a[172:175], a[136:139], a[44:47]\n" \
"ds_read_b64_tr_b16 a[246:247], v37 offset:9216\n" \
"v_mfma_f32_16x16x32_bf16 a[44:47], a[236:239], a[200:203], a[44:47]\n" \
"v_mfma_f32_16x16x32_bf16 a[60:63], a[172:175], a[140:143], a[60:63]\n" \
"v_mfma_f32_16x16x32_bf16 a[60:63], a[236:239], a[204:207], a[60:63]\n" \
"v_mfma_f32_16x16x32_bf16 a[56:59], a[168:171], a[140:143], a[56:59]\n" \
"ds_read_b64_tr_b16 a[184:185], v38 offset:0\n" \
"v_mfma_f32_16x16x32_bf16 a[56:59], a[232:235], a[204:207], a[56:59]\n" \
"v_mfma_f32_16x16x32_bf16 a[52:55], a[164:167], a[140:143], a[52:55]\n" \
"v_mfma_f32_16x16x32_bf16 a[52:55], a[228:231], a[204:207], a[52:55]\n" \
"ds_read_b64_tr_b16 a[186:187], v38 offset:1024\n" \
"v_mfma_f32_16x16x32_bf16 a[48:51], a[160:163], a[140:143], a[48:51]\n" \
"v_mfma_f32_16x16x32_bf16 a[48:51], a[224:227], a[204:207], a[48:51]\n" \
"v_mfma_f32_16x16x32_bf16 a[64:67], a[160:163], a[144:147], a[64:67]\n" \
"v_bfe_u32 v44, v20, 2, 5\n" \
"ds_read_b64_tr_b16 a[248:249], v38 offset:8192\n" \
"v_mfma_f32_16x16x32_bf16 a[64:67], a[224:227], a[208:211], a[64:67]\n" \
"buffer_load_dword v44, v44, s[80:83], s38 offen offset:128 sc1\n" \
"v_mfma_f32_16x16x32_bf16 a[68:71], a[164:167], a[144:147], a[68:71]\n" \
"v_mfma_f32_16x16x32_bf16 a[68:71], a[228:231], a[208:211], a[68:71]\n" \
"ds_read_b64_tr_b16 a[250:251], v38 offset:9216\n" \
"v_mfma_f32_16x16x32_bf16 a[72:75], a[168:171], a[144:147], a[72:75]\n" \
"v_mfma_f32_16x16x32_bf16 a[72:75], a[232:235], a[208:211], a[72:75]\n" \
"v_mfma_f32_16x16x32_bf16 a[76:79], a[172:175], a[144:147], a[76:79]\n" \
"v_mfma_f32_16x16x32_bf16 a[76:79], a[236:239], a[208:211], a[76:79]\n" \
"ds_read_b64_tr_b16 a[188:189], v39 offset:0\n" \
"v_mfma_f32_16x16x32_bf16 a[92:95], a[172:175], a[148:151], a[92:95]\n" \
"v_mfma_f32_16x16x32_bf16 a[92:95], a[236:239], a[212:215], a[92:95]\n" \
"v_mfma_f32_16x16x32_bf16 a[88:91], a[168:171], a[148:151], a[88:91]\n" \
"ds_read_b64_tr_b16 a[190:191], v39 offset:1024\n" \
"v_mfma_f32_16x16x32_bf16 a[88:91], a[232:235], a[212:215], a[88:91]\n" \
"v_mfma_f32_16x16x32_bf16 a[84:87], a[164:167], a[148:151], a[84:87]\n" \
"v_mfma_f32_16x16x32_bf16 a[84:87], a[228:231], a[212:215], a[84:87]\n" \
"ds_read_b64_tr_b16 a[252:253], v39 offset:8192\n" \
"v_mfma_f32_16x16x32_bf16 a[80:83], a[160:163], a[148:151], a[80:83]\n" \
"v_mfma_f32_16x16x32_bf16 a[80:83], a[224:227], a[212:215], a[80:83]\n" \
"v_mfma_f32_16x16x32_bf16 a[96:99], a[160:163], a[152:155], a[96:99]\n" \
"ds_read_b64_tr_b16 a[254:255], v39 offset:9216\n" \
"v_mfma_f32_16x16x32_bf16 a[96:99], a[224:227], a[216:219], a[96:99]\n" \
"v_mfma_f32_16x16x32_bf16 a[100:103], a[164:167], a[152:155], a[100:103]\n" \
"v_mfma_f32_16x16x32_bf16 a[100:103], a[228:231], a[216:219], a[100:103]\n" \
"v_mfma_f32_16x16x32_bf16 a[104:107], a[168:171], a[152:155], a[104:107]\n" \
"v_mfma_f32_16x16x32_bf16 a[104:107], a[232:235], a[216:219], a[104:107]\n" \
"v_mfma_f32_16x16x32_bf16 a[108:111], a[172:175], a[152:155], a[108:111]\n" \
"v_mfma_f32_16x16x32_bf16 a[108:111], a[236:239], a[216:219], a[108:111]\n" \
"s_waitcnt lgkmcnt(15)\n" \
"v_mfma_f32_16x16x32_bf16 a[124:127], a[172:175], a[156:159], a[124:127]\n" \
"v_mfma_f32_16x16x32_bf16 a[124:127], a[236:239], a[220:223], a[124:127]\n" \
"v_mfma_f32_16x16x32_bf16 a[120:123], a[168:171], a[156:159], a[120:123]\n" \
"v_mfma_f32_16x16x32_bf16 a[120:123], a[232:235], a[220:223], a[120:123]\n" \
"v_mfma_f32_16x16x32_bf16 a[116:119], a[164:167], a[156:159], a[116:119]\n" \
"v_mfma_f32_16x16x32_bf16 a[116:119], a[228:231], a[220:223], a[116:119]\n" \
"v_mfma_f32_16x16x32_bf16 a[112:115], a[160:163], a[156:159], a[112:115]\n" \
"v_mfma_f32_16x16x32_bf16 a[112:115], a[224:227], a[220:223], a[112:115]\n" \
"s_waitcnt vmcnt(0) lgkmcnt(0)\n" \
"s_barrier\n" \
"v_cmp_ne_u32_e32 vcc, s62, v44\n" \
"s_nop 4\n" \
"s_cbranch_vccnz L_poll60_%=\n" \
"L_pollr60_%=:\n" \
"s_mov_b32 s68, s12\n" \
"s_and_b32 s69, s13, 0xffff\n" \
"s_mov_b32 s70, 0x480000\n" \
"s_mov_b32 s71, 0x20000\n" \
"v_mfma_f32_16x16x32_bf16 v[52:55], a[176:179], a[128:131], v[52:55]\n" \
"s_add_u32 m0, s40, 0x0\n" \
"s_add_u32 s85, s39, 0x0\n" \
"s_nop 0\n" \
"v_mfma_f32_16x16x32_bf16 v[52:55], a[240:243], a[192:195], v[52:55]\n" \
"buffer_load_dwordx4 v20, s[80:83], s85 offen sc1 lds\n" \
"v_mfma_f32_16x16x32_bf16 v[56:59], a[180:183], a[128:131], v[56:59]\n" \
"ds_read_b64_tr_b16 a[160:161], v32 offset:32768\n" \
"v_mfma_f32_16x16x32_bf16 v[56:59], a[244:247], a[192:195], v[56:59]\n" \
"s_add_u32 m0, s40, 0x1000\n" \
"s_add_u32 s85, s39, 0x1000\n" \
"v_mfma_f32_16x16x32_bf16 v[60:63], a[184:187], a[128:131], v[60:63]\n" \
"buffer_load_dwordx4 v20, s[80:83], s85 offen sc1 lds\n" \
"v_mfma_f32_16x16x32_bf16 v[60:63], a[248:251], a[192:195], v[60:63]\n" \
"ds_read_b64_tr_b16 a[162:163], v32 offset:33792\n" \
"v_mfma_f32_16x16x32_bf16 v[64:67], a[188:191], a[128:131], v[64:67]\n" \
"ds_read_b64_tr_b16 a[224:225], v32 offset:40960\n" \
"v_mfma_f32_16x16x32_bf16 v[64:67], a[252:255], a[192:195], v[64:67]\n" \
"ds_read_b128 a[128:131], v28 offset:49152\n" \
"v_mfma_f32_16x16x32_bf16 v[80:83], a[188:191], a[132:135], v[80:83]\n" \
"ds_read_b128 a[192:195], v29 offset:49152\n" \
"v_mfma_f32_16x16x32_bf16 v[80:83], a[252:255], a[196:199], v[80:83]\n" \
"s_add_u32 m0, s40, 0x3000\n" \
"s_movk_i32 s84, 0x0\n" \
"v_mfma_f32_16x16x32_bf16 v[76:79], a[184:187], a[132:135], v[76:79]\n" \
"buffer_load_dwordx4 v21, s[76:79], s84 offen offset:0 lds\n" \
"v_mfma_f32_16x16x32_bf16 v[76:79], a[248:251], a[196:199], v[76:79]\n" \
"s_add_u32 m0, s30, 0x0\n" \
"s_movk_i32 s84, 0x0\n" \
"s_nop 0\n" \
"v_mfma_f32_16x16x32_bf16 v[72:75], a[180:183], a[132:135], v[72:75]\n" \
"buffer_load_dwordx4 v24, s[76:79], s84 offen offset:0 lds\n" \
"v_mfma_f32_16x16x32_bf16 v[72:75], a[244:247], a[196:199], v[72:75]\n" \
"ds_read_b64_tr_b16 a[226:227], v32 offset:41984\n" \
"v_mfma_f32_16x16x32_bf16 v[68:71], a[176:179], a[132:135], v[68:71]\n" \
"ds_read_b64_tr_b16 a[164:165], v33 offset:32768\n" \
"v_mfma_f32_16x16x32_bf16 v[68:71], a[240:243], a[196:199], v[68:71]\n" \
"ds_read_b128 a[132:135], v28 offset:51200\n" \
"v_mfma_f32_16x16x32_bf16 v[84:87], a[176:179], a[136:139], v[84:87]\n" \
"ds_read_b128 a[196:199], v29 offset:51200\n" \
"v_mfma_f32_16x16x32_bf16 v[84:87], a[240:243], a[200:203], v[84:87]\n" \
"s_movk_i32 s84, 0x2000\n" \
"v_mfma_f32_16x16x32_bf16 v[88:91], a[180:183], a[136:139], v[88:91]\n" \
"buffer_load_dwordx4 v25, s[76:79], s84 offen offset:1024 lds\n" \
"v_mfma_f32_16x16x32_bf16 v[88:91], a[244:247], a[200:203], v[88:91]\n" \
"s_movk_i32 s84, 0x4000\n" \
"v_mfma_f32_16x16x32_bf16 v[92:95], a[184:187], a[136:139], v[92:95]\n" \
"buffer_load_dwordx4 v24, s[76:79], s84 offen offset:2048 lds\n" \
"v_mfma_f32_16x16x32_bf16 v[92:95], a[248:251], a[200:203], v[92:95]\n" \
"ds_read_b64_tr_b16 a[166:167], v33 offset:33792\n" \
"v_mfma_f32_16x16x32_bf16 v[96:99], a[188:191], a[136:139], v[96:99]\n" \
"ds_read_b64_tr_b16 a[228:229], v33 offset:40960\n" \
"v_mfma_f32_16x16x32_bf16 v[96:99], a[252:255], a[200:203], v[96:99]\n" \
"ds_read_b128 a[136:139], v28 offset:53248\n" \
"v_mfma_f32_16x16x32_bf16 v[112:115], a[188:191], a[140:143], v[112:115]\n" \
"ds_read_b128 a[200:203], v29 offset:53248\n" \
"v_mfma_f32_16x16x32_bf16 v[112:115], a[252:255], a[204:207], v[112:115]\n" \
"s_movk_i32 s84, 0x6000\n" \
"v_mfma_f32_16x16x32_bf16 v[108:111], a[184:187], a[140:143], v[108:111]\n" \
"buffer_load_dwordx4 v25, s[76:79], s84 offen offset:3072 lds\n" \
"v_mfma_f32_16x16x32_bf16 v[108:111], a[248:251], a[204:207], v[108:111]\n" \
"s_add_u32 m0, s31, 0x0\n" \
"s_movk_i32 s84, 0x0\n" \
"v_mfma_f32_16x16x32_bf16 v[104:107], a[180:183], a[140:143], v[104:107]\n" \
"buffer_load_dwordx4 v26, s[68:71], s84 offen offset:0 lds\n" \
"v_mfma_f32_16x16x32_bf16 v[104:107], a[244:247], a[204:207], v[104:107]\n" \
"s_movk_i32 s84, 0x7c00\n" \
".fill (L_pb + 16096 - .) / 4, 4, 0xBF800000\n" \
"L_pp4:\n" \
"s_cbranch_execz L_pret\n" \
"v_mfma_f32_16x16x32_bf16 v[100:103], a[176:179], a[140:143], v[100:103]\n" \
"buffer_load_dwordx4 v26, s[68:71], s84 offen offset:1024 lds\n" \
"v_mfma_f32_16x16x32_bf16 v[100:103], a[240:243], a[204:207], v[100:103]\n" \
"ds_read_b128 a[140:143], v28 offset:55296\n" \
"v_mfma_f32_16x16x32_bf16 v[116:119], a[176:179], a[144:147], v[116:119]\n" \
"ds_read_b128 a[204:207], v29 offset:55296\n" \
"v_mfma_f32_16x16x32_bf16 v[116:119], a[240:243], a[208:211], v[116:119]\n" \
"s_addk_i32 s84, 0x7c00\n" \
"v_mfma_f32_16x16x32_bf16 v[120:123], a[180:183], a[144:147], v[120:123]\n" \
"buffer_load_dwordx4 v27, s[68:71], s84 offen offset:2048 lds\n" \
"v_mfma_f32_16x16x32_bf16 v[120:123], a[244:247], a[208:211], v[120:123]\n" \
"ds_read_b64_tr_b16 a[230:231], v33 offset:41984\n" \
"v_mfma_f32_16x16x32_bf16 v[124:127], a[184:187], a[144:147], v[124:127]\n" \
"s_addk_i32 s84, 0x7c00\n" \
"s_nop 0\n" \
"v_mfma_f32_16x16x32_bf16 v[124:127], a[248:251], a[208:211], v[124:127]\n" \
"buffer_load_dwordx4 v27, s[68:71], s84 offen offset:3072 lds\n" \
"v_mfma_f32_16x16x32_bf16 v[128:131], a[188:191], a[144:147], v[128:131]\n" \
"ds_read_b64_tr_b16 a[168:169], v34 offset:32768\n" \
"v_mfma_f32_16x16x32_bf16 v[128:131], a[252:255], a[208:211], v[128:131]\n" \
"ds_read_b128 a[144:147], v28 offset:32768\n" \
"v_mfma_f32_16x16x32_bf16 v[144:147], a[188:191], a[148:151], v[144:147]\n" \
"ds_read_b128 a[208:211], v29 offset:32768\n" \
"v_mfma_f32_16x16x32_bf16 v[144:147], a[252:255], a[212:215], v[144:147]\n" \
"s_add_u32 m0, s30, 0x1000\n" \
"s_mov_b32 s84, 0x9000\n" \
"v_mfma_f32_16x16x32_bf16 v[140:143], a[184:187], a[148:151], v[140:143]\n" \
"buffer_load_dwordx4 v24, s[76:79], s84 offen offset:0 lds\n" \
"v_mfma_f32_16x16x32_bf16 v[140:143], a[248:251], a[212:215], v[140:143]\n" \
"ds_read_b64_tr_b16 a[170:171], v34 offset:33792\n" \
"v_mfma_f32_16x16x32_bf16 v[136:139], a[180:183], a[148:151], v[136:139]\n" \
"s_addk_i32 s84, 0x2000\n" \
"s_nop 0\n" \
"v_mfma_f32_16x16x32_bf16 v[136:139], a[244:247], a[212:215], v[136:139]\n" \
"buffer_load_dwordx4 v25, s[76:79], s84 offen offset:1024 lds\n" \
"v_mfma_f32_16x16x32_bf16 v[132:135], a[176:179], a[148:151], v[132:135]\n" \
"ds_read_b64_tr_b16 a[232:233], v34 offset:40960\n" \
"v_mfma_f32_16x16x32_bf16 v[132:135], a[240:243], a[212:215], v[132:135]\n" \
"ds_read_b128 a[148:151], v28 offset:34816\n" \
"v_mfma_f32_16x16x32_bf16 v[148:151], a[176:179], a[152:155], v[148:151]\n" \
"ds_read_b128 a[212:215], v29 offset:34816\n" \
"v_mfma_f32_16x16x32_bf16 v[148:151], a[240:243], a[216:219], v[148:151]\n" \
"ds_read_b64_tr_b16 a[234:235], v34 offset:41984\n" \
"v_mfma_f32_16x16x32_bf16 v[152:155], a[180:183], a[152:155], v[152:155]\n" \
"ds_read_b64_tr_b16 a[172:173], v35 offset:32768\n" \
"v_mfma_f32_16x16x32_bf16 v[152:155], a[244:247], a[216:219], v[152:155]\n" \
"ds_read_b64_tr_b16 a[174:175], v35 offset:33792\n" \
"v_mfma_f32_16x16x32_bf16 v[156:159], a[184:187], a[152:155], v[156:159]\n" \
"ds_read_b64_tr_b16 a[236:237], v35 offset:40960\n" \
"v_mfma_f32_16x16x32_bf16 v[156:159], a[248:251], a[216:219], v[156:159]\n" \
"ds_read_b64_tr_b16 a[238:239], v35 offset:41984\n" \
"v_mfma_f32_16x16x32_bf16 v[160:163], a[188:191], a[152:155], v[160:163]\n" \
"v_mfma_f32_16x16x32_bf16 v[160:163], a[252:255], a[216:219], v[160:163]\n" \
"ds_read_b128 a[152:155], v28 offset:36864\n" \
"v_mfma_f32_16x16x32_bf16 v[176:179], a[188:191], a[156:159], v[176:179]\n" \
"ds_read_b128 a[216:219], v29 offset:36864\n" \
"v_mfma_f32_16x16x32_bf16 v[176:179], a[252:255], a[220:223], v[176:179]\n" \
"v_mfma_f32_16x16x32_bf16 v[172:175], a[184:187], a[156:159], v[172:175]\n" \
"v_mfma_f32_16x16x32_bf16 v[172:175], a[248:251], a[220:223], v[172:175]\n" \
"v_mfma_f32_16x16x32_bf16 v[168:171], a[180:183], a[156:159], v[168:171]\n" \
"v_mfma_f32_16x16x32_bf16 v[168:171], a[244:247], a[220:223], v[168:171]\n" \
"v_mfma_f32_16x16x32_bf16 v[164:167], a[176:179], a[156:159], v[164:167]\n" \
"v_mfma_f32_16x16x32_bf16 v[164:167], a[240:243], a[220:223], v[164:167]\n" \
"ds_read_b128 a[156:159], v28 offset:38912\n" \
"ds_read_b128 a[220:223], v29 offset:38912\n" \
"s_waitcnt lgkmcnt(15)\n" \
"v_mfma_f32_16x16x32_bf16 a[0:3], a[160:163], a[128:131], a[0:3]\n" \
"s_addk_i32 s84, 0x2000\n" \
"v_mfma_f32_16x16x32_bf16 a[0:3], a[224:227], a[192:195], a[0:3]\n" \
"buffer_load_dwordx4 v24, s[76:79], s84 offen offset:2048 lds\n" \
"v_mfma_f32_16x16x32_bf16 a[4:7], a[164:167], a[128:131], a[4:7]\n" \
"ds_read_b64_tr_b16 a[176:177], v36 offset:32768\n" \
"v_mfma_f32_16x16x32_bf16 a[4:7], a[228:231], a[192:195], a[4:7]\n" \
"s_addk_i32 s84, 0x2000\n" \
"s_waitcnt lgkmcnt(13)\n" \
"v_mfma_f32_16x16x32_bf16 a[8:11], a[168:171], a[128:131], a[8:11]\n" \
"buffer_load_dwordx4 v25, s[76:79], s84 offen offset:3072 lds\n" \
"s_waitcnt lgkmcnt(9)\n" \
"v_mfma_f32_16x16x32_bf16 a[8:11], a[232:235], a[192:195], a[8:11]\n" \
"ds_read_b64_tr_b16 a[178:179], v36 offset:33792\n" \
"s_waitcnt lgkmcnt(8)\n" \
"v_mfma_f32_16x16x32_bf16 a[12:15], a[172:175], a[128:131], a[12:15]\n" \
"ds_read_b64_tr_b16 a[240:241], v36 offset:40960\n" \
"v_accvgpr_read_b32 v184, a0\n" \
"s_waitcnt lgkmcnt(7)\n" \
"s_nop 0\n" \
"v_mfma_f32_16x16x32_bf16 a[12:15], a[236:239], a[192:195], a[12:15]\n" \
"v_accvgpr_read_b32 v185, a1\n" \
"v_accvgpr_read_b32 v186, a2\n" \
"v_mfma_f32_16x16x32_bf16 a[28:31], a[172:175], a[132:135], a[28:31]\n" \
"v_accvgpr_read_b32 v187, a3\n" \
"v_accvgpr_read_b32 v180, a4\n" \
"v_mfma_f32_16x16x32_bf16 a[28:31], a[236:239], a[196:199], a[28:31]\n" \
"ds_read_b64_tr_b16 a[242:243], v36 offset:41984\n" \
"v_accvgpr_read_b32 v181, a5\n" \
"v_mfma_f32_16x16x32_bf16 a[24:27], a[168:171], a[132:135], a[24:27]\n" \
"v_accvgpr_read_b32 v182, a6\n" \
"v_accvgpr_read_b32 v183, a7\n" \
"v_mfma_f32_16x16x32_bf16 a[24:27], a[232:235], a[196:199], a[24:27]\n" \
"v_cvt_pk_bf16_f32 v184, v184, v185\n" \
"v_cvt_pk_bf16_f32 v185, v186, v187\n" \
"v_mfma_f32_16x16x32_bf16 a[20:23], a[164:167], a[132:135], a[20:23]\n" \
"v_cvt_pk_bf16_f32 v186, v180, v181\n" \
"v_cvt_pk_bf16_f32 v187, v182, v183\n" \
"v_mfma_f32_16x16x32_bf16 a[20:23], a[228:231], a[196:199], a[20:23]\n" \
"ds_read_b64_tr_b16 a[180:181], v37 offset:32768\n" \
"v_accvgpr_read_b32 v188, a8\n" \
"v_mfma_f32_16x16x32_bf16 a[16:19], a[160:163], a[132:135], a[16:19]\n" \
"v_accvgpr_read_b32 v189, a9\n" \
"v_accvgpr_read_b32 v190, a10\n" \
"v_mfma_f32_16x16x32_bf16 a[16:19], a[224:227], a[196:199], a[16:19]\n" \
"v_accvgpr_read_b32 v191, a11\n" \
"v_accvgpr_read_b32 v180, a12\n" \
"v_mfma_f32_16x16x32_bf16 a[32:35], a[160:163], a[136:139], a[32:35]\n" \
"ds_read_b64_tr_b16 a[182:183], v37 offset:33792\n" \
"v_accvgpr_read_b32 v181, a13\n" \
"v_mfma_f32_16x16x32_bf16 a[32:35], a[224:227], a[200:203], a[32:35]\n" \
"v_accvgpr_read_b32 v182, a14\n" \
"v_accvgpr_read_b32 v183, a15\n" \
"v_mfma_f32_16x16x32_bf16 a[36:39], a[164:167], a[136:139], a[36:39]\n" \
"v_lshlrev_b32_e32 v45, 3, v20\n" \
"v_add_u32_e32 v45, s31, v45\n" \
"v_mfma_f32_16x16x32_bf16 a[36:39], a[228:231], a[200:203], a[36:39]\n" \
"s_getpc_b64 s[86:87]\n" \
"s_sub_u32 s86, s86, 0xf800\n" \
"s_subb_u32 s87, s87, 0\n" \
"s_mov_b32 m0, s32\n" \
"s_nop 0\n" \
"v_mfma_f32_16x16x32_bf16 a[40:43], a[168:171], a[136:139], a[40:43]\n" \
"global_load_lds_dword v45, s[86:87]\n" \
"v_mfma_f32_16x16x32_bf16 a[40:43], a[232:235], a[200:203], a[40:43]\n" \
"ds_read_b64_tr_b16 a[244:245], v37 offset:40960\n" \
"v_cvt_pk_bf16_f32 v188, v188, v189\n" \
"v_mfma_f32_16x16x32_bf16 a[44:47], a[172:175], a[136:139], a[44:47]\n" \
"ds_read_b64_tr_b16 a[246:247], v37 offset:41984\n" \
"v_cvt_pk_bf16_f32 v189, v190, v191\n" \
"v_mfma_f32_16x16x32_bf16 a[44:47], a[236:239], a[200:203], a[44:47]\n" \
"v_cvt_pk_bf16_f32 v190, v180, v181\n" \
"v_cvt_pk_bf16_f32 v191, v182, v183\n" \
"v_mfma_f32_16x16x32_bf16 a[60:63], a[172:175], a[140:143], a[60:63]\n" \
"v_accvgpr_read_b32 v192, a24\n" \
"v_accvgpr_read_b32 v193, a25\n" \
"v_mfma_f32_16x16x32_bf16 a[60:63], a[236:239], a[204:207], a[60:63]\n" \
"v_accvgpr_read_b32 v194, a26\n" \
"v_accvgpr_read_b32 v195, a27\n" \
"v_mfma_f32_16x16x32_bf16 a[56:59], a[168:171], a[140:143], a[56:59]\n" \
"ds_read_b64_tr_b16 a[184:185], v38 offset:32768\n" \
"v_accvgpr_read_b32 v180, a28\n" \
"v_mfma_f32_16x16x32_bf16 a[56:59], a[232:235], a[204:207], a[56:59]\n" \
"v_accvgpr_read_b32 v181, a29\n" \
"v_accvgpr_read_b32 v182, a30\n" \
"v_mfma_f32_16x16x32_bf16 a[52:55], a[164:167], a[140:143], a[52:55]\n" \
"v_accvgpr_read_b32 v183, a31\n" \
"v_cvt_pk_bf16_f32 v192, v192, v193\n" \
"v_mfma_f32_16x16x32_bf16 a[52:55], a[228:231], a[204:207], a[52:55]\n" \
"ds_read_b64_tr_b16 a[186:187], v38 offset:33792\n" \
"v_cvt_pk_bf16_f32 v193, v194, v195\n" \
"v_mfma_f32_16x16x32_bf16 a[48:51], a[160:163], a[140:143], a[48:51]\n" \
"v_cvt_pk_bf16_f32 v194, v180, v181\n" \
"v_cvt_pk_bf16_f32 v195, v182, v183\n" \
"v_mfma_f32_16x16x32_bf16 a[48:51], a[224:227], a[204:207], a[48:51]\n" \
"v_accvgpr_read_b32 v196, a16\n" \
"v_accvgpr_read_b32 v197, a17\n" \
"v_mfma_f32_16x16x32_bf16 a[64:67], a[160:163], a[144:147], a[64:67]\n" \
"ds_read_b64_tr_b16 a[248:249], v38 offset:40960\n" \
"v_accvgpr_read_b32 v198, a18\n" \
"v_mfma_f32_16x16x32_bf16 a[64:67], a[224:227], a[208:211], a[64:67]\n" \
"v_accvgpr_read_b32 v199, a19\n" \
"v_accvgpr_read_b32 v180, a20\n" \
"v_mfma_f32_16x16x32_bf16 a[68:71], a[164:167], a[144:147], a[68:71]\n" \
"v_accvgpr_read_b32 v181, a21\n" \
"v_accvgpr_read_b32 v182, a22\n" \
"v_mfma_f32_16x16x32_bf16 a[68:71], a[228:231], a[208:211], a[68:71]\n" \
"ds_read_b64_tr_b16 a[250:251], v38 offset:41984\n" \
"v_accvgpr_read_b32 v183, a23\n" \
"v_mfma_f32_16x16x32_bf16 a[72:75], a[168:171], a[144:147], a[72:75]\n" \
"v_cvt_pk_bf16_f32 v196, v196, v197\n" \
"v_cvt_pk_bf16_f32 v197, v198, v199\n" \
"v_mfma_f32_16x16x32_bf16 a[72:75], a[232:235], a[208:211], a[72:75]\n" \
"v_cvt_pk_bf16_f32 v198, v180, v181\n" \
"v_cvt_pk_bf16_f32 v199, v182, v183\n" \
"v_mfma_f32_16x16x32_bf16 a[76:79], a[172:175], a[144:147], a[76:79]\n" \
"v_accvgpr_read_b32 v200, a32\n" \
"v_accvgpr_read_b32 v201, a33\n" \
"v_mfma_f32_16x16x32_bf16 a[76:79], a[236:239], a[208:211], a[76:79]\n" \
"ds_read_b64_tr_b16 a[188:189], v39 offset:32768\n" \
"v_accvgpr_read_b32 v202, a34\n" \
"v_mfma_f32_16x16x32_bf16 a[92:95], a[172:175], a[148:151], a[92:95]\n" \
"s_add_u32 s89, s41, 0x0\n" \
"s_nop 0\n" \
"buffer_store_dwordx4 v[184:187], v20, s[80:83], s89 offen offset:0 sc1\n" \
"v_mfma_f32_16x16x32_bf16 a[92:95], a[236:239], a[212:215], a[92:95]\n" \
"v_accvgpr_read_b32 v203, a35\n" \
"v_accvgpr_read_b32 v180, a36\n" \
"v_mfma_f32_16x16x32_bf16 a[88:91], a[168:171], a[148:151], a[88:91]\n" \
"ds_read_b64_tr_b16 a[190:191], v39 offset:33792\n" \
"v_accvgpr_read_b32 v181, a37\n" \
"v_mfma_f32_16x16x32_bf16 a[88:91], a[232:235], a[212:215], a[88:91]\n" \
"v_accvgpr_read_b32 v182, a38\n" \
"v_accvgpr_read_b32 v183, a39\n" \
"v_mfma_f32_16x16x32_bf16 a[84:87], a[164:167], a[148:151], a[84:87]\n" \
"v_cvt_pk_bf16_f32 v200, v200, v201\n" \
"v_cvt_pk_bf16_f32 v201, v202, v203\n" \
"v_mfma_f32_16x16x32_bf16 a[84:87], a[228:231], a[212:215], a[84:87]\n" \
"ds_read_b64_tr_b16 a[252:253], v39 offset:40960\n" \
"v_cvt_pk_bf16_f32 v202, v180, v181\n" \
"v_mfma_f32_16x16x32_bf16 a[80:83], a[160:163], a[148:151], a[80:83]\n" \
"s_add_u32 s88, s41, 0x0\n" \
"buffer_store_dwordx4 v[188:191], v20, s[80:83], s88 offen offset:1024 sc1\n" \
"v_mfma_f32_16x16x32_bf16 a[80:83], a[224:227], a[212:215], a[80:83]\n" \
"v_cvt_pk_bf16_f32 v203, v182, v183\n" \
"v_accvgpr_read_b32 v204, a40\n" \
"s_waitcnt lgkmcnt(15)\n" \
"v_mfma_f32_16x16x32_bf16 a[96:99], a[160:163], a[152:155], a[96:99]\n" \
"ds_read_b64_tr_b16 a[254:255], v39 offset:41984\n" \
"v_accvgpr_read_b32 v205, a41\n" \
"v_mfma_f32_16x16x32_bf16 a[96:99], a[224:227], a[216:219], a[96:99]\n" \
"v_accvgpr_read_b32 v206, a42\n" \
"v_accvgpr_read_b32 v207, a43\n" \
"v_mfma_f32_16x16x32_bf16 a[100:103], a[164:167], a[152:155], a[100:103]\n" \
"v_accvgpr_read_b32 v180, a44\n" \
"v_accvgpr_read_b32 v181, a45\n" \
"v_mfma_f32_16x16x32_bf16 a[100:103], a[228:231], a[216:219], a[100:103]\n" \
"v_accvgpr_read_b32 v182, a46\n" \
"v_accvgpr_read_b32 v183, a47\n" \
"v_mfma_f32_16x16x32_bf16 a[104:107], a[168:171], a[152:155], a[104:107]\n" \
"s_add_u32 s89, s41, 0x1000\n" \
"buffer_store_dwordx4 v[192:195], v20, s[80:83], s89 offen offset:1024 sc1\n" \
"v_mfma_f32_16x16x32_bf16 a[104:107], a[232:235], a[216:219], a[104:107]\n" \
"v_cvt_pk_bf16_f32 v204, v204, v205\n" \
"v_cvt_pk_bf16_f32 v205, v206, v207\n" \
"v_mfma_f32_16x16x32_bf16 a[108:111], a[172:175], a[152:155], a[108:111]\n" \
"v_cvt_pk_bf16_f32 v206, v180, v181\n" \
"v_cvt_pk_bf16_f32 v207, v182, v183\n" \
"v_mfma_f32_16x16x32_bf16 a[108:111], a[236:239], a[216:219], a[108:111]\n" \
"v_accvgpr_read_b32 v208, a56\n" \
"v_accvgpr_read_b32 v209, a57\n" \
"v_mfma_f32_16x16x32_bf16 a[124:127], a[172:175], a[156:159], a[124:127]\n" \
"v_accvgpr_read_b32 v210, a58\n" \
"v_accvgpr_read_b32 v211, a59\n" \
"v_mfma_f32_16x16x32_bf16 a[124:127], a[236:239], a[220:223], a[124:127]\n" \
"v_accvgpr_read_b32 v180, a60\n" \
"v_accvgpr_read_b32 v181, a61\n" \
"v_mfma_f32_16x16x32_bf16 a[120:123], a[168:171], a[156:159], a[120:123]\n" \
"s_add_u32 s88, s41, 0x1000\n" \
"buffer_store_dwordx4 v[196:199], v20, s[80:83], s88 offen offset:0 sc1\n" \
"v_mfma_f32_16x16x32_bf16 a[120:123], a[232:235], a[220:223], a[120:123]\n" \
"v_accvgpr_read_b32 v182, a62\n" \
"v_accvgpr_read_b32 v183, a63\n" \
"v_mfma_f32_16x16x32_bf16 a[116:119], a[164:167], a[156:159], a[116:119]\n" \
"v_cvt_pk_bf16_f32 v208, v208, v209\n" \
"v_cvt_pk_bf16_f32 v209, v210, v211\n" \
"v_mfma_f32_16x16x32_bf16 a[116:119], a[228:231], a[220:223], a[116:119]\n" \
"v_cvt_pk_bf16_f32 v210, v180, v181\n" \
"v_cvt_pk_bf16_f32 v211, v182, v183\n" \
"v_mfma_f32_16x16x32_bf16 a[112:115], a[160:163], a[156:159], a[112:115]\n" \
"v_accvgpr_read_b32 v212, a48\n" \
"v_accvgpr_read_b32 v213, a49\n" \
"v_mfma_f32_16x16x32_bf16 a[112:115], a[224:227], a[220:223], a[112:115]\n" \
"v_accvgpr_read_b32 v214, a50\n" \
"v_accvgpr_read_b32 v215, a51\n" \
"s_waitcnt vmcnt(5) lgkmcnt(0)\n" \
"s_barrier\n" \
"v_mov_b32_e32 v36, v32\n" \
"v_xor_b32_e32 v32, 0x0, v48\n" \
"v_mov_b32_e32 v37, v33\n" \
"v_xor_b32_e32 v33, 0x20, v48\n" \
"v_mov_b32_e32 v38, v34\n" \
"v_xor_b32_e32 v34, 0x40, v48\n" \
"v_mov_b32_e64 v39, v35\n" \
"v_xor_b32_e32 v35, 0x60, v48\n" \
"v_add_u32_e32 v22, s44, v28\n" \
"v_add_u32_e32 v28, s43, v28\n" \
"v_add_u32_e32 v23, s44, v29\n" \
"v_add_u32_e32 v29, s43, v29\n" \
"v_mfma_f32_16x16x32_bf16 v[52:55], a[176:179], a[128:131], v[52:55]\n" \
"s_add_u32 m0, s40, 0x8000\n" \
"s_add_u32 s85, s39, 0x2000\n" \
"v_mfma_f32_16x16x32_bf16 v[52:55], a[240:243], a[192:195], v[52:55]\n" \
"buffer_load_dwordx4 v20, s[80:83], s85 offen sc1 lds\n" \
"v_mfma_f32_16x16x32_bf16 v[56:59], a[180:183], a[128:131], v[56:59]\n" \
"ds_read_b64_tr_b16 a[160:161], v32 offset:0\n" \
"v_accvgpr_read_b32 v180, a52\n" \
"v_mfma_f32_16x16x32_bf16 v[56:59], a[244:247], a[192:195], v[56:59]\n" \
"s_add_u32 m0, s40, 0x9000\n" \
"s_add_u32 s85, s39, 0x3000\n" \
"v_mfma_f32_16x16x32_bf16 v[60:63], a[184:187], a[128:131], v[60:63]\n" \
"buffer_load_dwordx4 v20, s[80:83], s85 offen sc1 lds\n" \
"v_mfma_f32_16x16x32_bf16 v[60:63], a[248:251], a[192:195], v[60:63]\n" \
"ds_read_b64_tr_b16 a[162:163], v32 offset:512\n" \
"v_accvgpr_read_b32 v181, a53\n" \
"v_mfma_f32_16x16x32_bf16 v[64:67], a[188:191], a[128:131], v[64:67]\n" \
"ds_read_b64_tr_b16 a[224:225], v32 offset:4096\n" \
"v_accvgpr_read_b32 v182, a54\n" \
"v_mfma_f32_16x16x32_bf16 v[64:67], a[252:255], a[192:195], v[64:67]\n" \
"ds_read_b128 a[128:131], v28 offset:0\n" \
"v_accvgpr_read_b32 v183, a55\n" \
"v_mfma_f32_16x16x32_bf16 v[80:83], a[188:191], a[132:135], v[80:83]\n" \
"ds_read_b128 a[192:195], v29 offset:0\n" \
"v_cvt_pk_bf16_f32 v212, v212, v213\n" \
"v_mfma_f32_16x16x32_bf16 v[80:83], a[252:255], a[196:199], v[80:83]\n" \
"s_add_u32 m0, s40, 0xb000\n" \
"s_movk_i32 s84, 0x80\n" \
"v_mfma_f32_16x16x32_bf16 v[76:79], a[184:187], a[132:135], v[76:79]\n" \
"buffer_load_dwordx4 v21, s[76:79], s84 offen offset:0 lds\n" \
"v_mfma_f32_16x16x32_bf16 v[76:79], a[248:251], a[196:199], v[76:79]\n" \
"s_add_u32 m0, s30, 0x8000\n" \
"s_movk_i32 s84, 0x80\n" \
"v_mfma_f32_16x16x32_bf16 v[72:75], a[180:183], a[132:135], v[72:75]\n" \
"buffer_load_dwordx4 v24, s[76:79], s84 offen offset:0 lds\n" \
"v_mfma_f32_16x16x32_bf16 v[72:75], a[244:247], a[196:199], v[72:75]\n" \
"ds_read_b64_tr_b16 a[226:227], v32 offset:4608\n" \
"v_cvt_pk_bf16_f32 v213, v214, v215\n" \
"v_mfma_f32_16x16x32_bf16 v[68:71], a[176:179], a[132:135], v[68:71]\n" \
"ds_read_b64_tr_b16 a[164:165], v33 offset:0\n" \
"v_cvt_pk_bf16_f32 v214, v180, v181\n" \
"v_mfma_f32_16x16x32_bf16 v[68:71], a[240:243], a[196:199], v[68:71]\n" \
"ds_read_b128 a[132:135], v28 offset:2048\n" \
"v_cvt_pk_bf16_f32 v215, v182, v183\n" \
"v_mfma_f32_16x16x32_bf16 v[84:87], a[176:179], a[136:139], v[84:87]\n" \
"ds_read_b128 a[196:199], v29 offset:2048\n" \
"v_accvgpr_read_b32 v216, a64\n" \
"v_mfma_f32_16x16x32_bf16 v[84:87], a[240:243], a[200:203], v[84:87]\n" \
"s_movk_i32 s84, 0x2080\n" \
"v_mfma_f32_16x16x32_bf16 v[88:91], a[180:183], a[136:139], v[88:91]\n" \
"buffer_load_dwordx4 v25, s[76:79], s84 offen offset:1024 lds\n" \
"v_mfma_f32_16x16x32_bf16 v[88:91], a[244:247], a[200:203], v[88:91]\n" \
"s_movk_i32 s84, 0x4080\n" \
"v_mfma_f32_16x16x32_bf16 v[92:95], a[184:187], a[136:139], v[92:95]\n" \
"buffer_load_dwordx4 v24, s[76:79], s84 offen offset:2048 lds\n" \
"v_mfma_f32_16x16x32_bf16 v[92:95], a[248:251], a[200:203], v[92:95]\n" \
"ds_read_b64_tr_b16 a[166:167], v33 offset:512\n" \
"v_accvgpr_read_b32 v217, a65\n" \
"v_mfma_f32_16x16x32_bf16 v[96:99], a[188:191], a[136:139], v[96:99]\n" \
"ds_read_b64_tr_b16 a[228:229], v33 offset:4096\n" \
"v_accvgpr_read_b32 v218, a66\n" \
"v_mfma_f32_16x16x32_bf16 v[96:99], a[252:255], a[200:203], v[96:99]\n" \
"ds_read_b128 a[136:139], v28 offset:4096\n" \
"v_accvgpr_read_b32 v219, a67\n" \
"v_mfma_f32_16x16x32_bf16 v[112:115], a[188:191], a[140:143], v[112:115]\n" \
"ds_read_b128 a[200:203], v29 offset:4096\n" \
"v_accvgpr_read_b32 v180, a68\n" \
"v_mfma_f32_16x16x32_bf16 v[112:115], a[252:255], a[204:207], v[112:115]\n" \
"s_movk_i32 s84, 0x6080\n" \
"v_mfma_f32_16x16x32_bf16 v[108:111], a[184:187], a[140:143], v[108:111]\n" \
"buffer_load_dwordx4 v25, s[76:79], s84 offen offset:3072 lds\n" \
"v_mfma_f32_16x16x32_bf16 v[108:111], a[248:251], a[204:207], v[108:111]\n" \
"s_add_u32 m0, s31, 0x8000\n" \
"s_mov_b32 s84, 0x80000\n" \
"v_mfma_f32_16x16x32_bf16 v[104:107], a[180:183], a[140:143], v[104:107]\n" \
"buffer_load_dwordx4 v26, s[68:71], s84 offen offset:0 lds\n" \
"v_mfma_f32_16x16x32_bf16 v[104:107], a[244:247], a[204:207], v[104:107]\n" \
"s_addk_i32 s84, 0x7c00\n" \
"v_mfma_f32_16x16x32_bf16 v[100:103], a[176:179], a[140:143], v[100:103]\n" \
"buffer_load_dwordx4 v26, s[68:71], s84 offen offset:1024 lds\n" \
"v_mfma_f32_16x16x32_bf16 v[100:103], a[240:243], a[204:207], v[100:103]\n" \
"ds_read_b128 a[140:143], v28 offset:6144\n" \
"v_accvgpr_read_b32 v181, a69\n" \
"v_mfma_f32_16x16x32_bf16 v[116:119], a[176:179], a[144:147], v[116:119]\n" \
"ds_read_b128 a[204:207], v29 offset:6144\n" \
"v_accvgpr_read_b32 v182, a70\n" \
"v_mfma_f32_16x16x32_bf16 v[116:119], a[240:243], a[208:211], v[116:119]\n" \
"s_addk_i32 s84, 0x7c00\n" \
"v_mfma_f32_16x16x32_bf16 v[120:123], a[180:183], a[144:147], v[120:123]\n" \
"buffer_load_dwordx4 v27, s[68:71], s84 offen offset:2048 lds\n" \
"v_mfma_f32_16x16x32_bf16 v[120:123], a[244:247], a[208:211], v[120:123]\n" \
"ds_read_b64_tr_b16 a[230:231], v33 offset:4608\n" \
"v_accvgpr_read_b32 v183, a71\n" \
"v_mfma_f32_16x16x32_bf16 v[124:127], a[184:187], a[144:147], v[124:127]\n" \
"s_addk_i32 s84, 0x7c00\n" \
"v_mfma_f32_16x16x32_bf16 v[124:127], a[248:251], a[208:211], v[124:127]\n" \
"buffer_load_dwordx4 v27, s[68:71], s84 offen offset:3072 lds\n" \
"v_mfma_f32_16x16x32_bf16 v[128:131], a[188:191], a[144:147], v[128:131]\n" \
"ds_read_b64_tr_b16 a[172:173], v35 offset:0\n" \
"v_cvt_pk_bf16_f32 v216, v216, v217\n" \
"v_mfma_f32_16x16x32_bf16 v[128:131], a[252:255], a[208:211], v[128:131]\n" \
"ds_read_b128 a[144:147], v28 offset:8192\n" \
"v_cvt_pk_bf16_f32 v217, v218, v219\n" \
"v_mfma_f32_16x16x32_bf16 v[144:147], a[188:191], a[148:151], v[144:147]\n" \
"ds_read_b128 a[208:211], v29 offset:8192\n" \
"v_cvt_pk_bf16_f32 v218, v180, v181\n" \
"v_mfma_f32_16x16x32_bf16 v[144:147], a[252:255], a[212:215], v[144:147]\n" \
"s_add_u32 m0, s30, 0x9000\n" \
"s_mov_b32 s84, 0x9080\n" \
"v_mfma_f32_16x16x32_bf16 v[140:143], a[184:187], a[148:151], v[140:143]\n" \
"buffer_load_dwordx4 v24, s[76:79], s84 offen offset:0 lds\n" \
"v_mfma_f32_16x16x32_bf16 v[140:143], a[248:251], a[212:215], v[140:143]\n" \
"ds_read_b64_tr_b16 a[174:175], v35 offset:512\n" \
"v_cvt_pk_bf16_f32 v219, v182, v183\n" \
"v_mfma_f32_16x16x32_bf16 v[136:139], a[180:183], a[148:151], v[136:139]\n" \
"s_addk_i32 s84, 0x2000\n" \
"s_nop 0\n" \
"v_mfma_f32_16x16x32_bf16 v[136:139], a[244:247], a[212:215], v[136:139]\n" \
"buffer_load_dwordx4 v25, s[76:79], s84 offen offset:1024 lds\n" \
"v_mfma_f32_16x16x32_bf16 v[132:135], a[176:179], a[148:151], v[132:135]\n" \
"ds_read_b64_tr_b16 a[236:237], v35 offset:4096\n" \
"v_accvgpr_read_b32 v220, a80\n" \
"v_mfma_f32_16x16x32_bf16 v[132:135], a[240:243], a[212:215], v[132:135]\n" \
"ds_read_b128 a[148:151], v28 offset:10240\n" \
"v_accvgpr_read_b32 v221, a81\n" \
"v_mfma_f32_16x16x32_bf16 v[148:151], a[176:179], a[152:155], v[148:151]\n" \
"ds_read_b128 a[212:215], v29 offset:10240\n" \
"v_accvgpr_read_b32 v222, a82\n" \
"v_mfma_f32_16x16x32_bf16 v[148:151], a[240:243], a[216:219], v[148:151]\n" \
"ds_read_b64_tr_b16 a[238:239], v35 offset:4608\n" \
"v_accvgpr_read_b32 v223, a83\n" \
"v_mfma_f32_16x16x32_bf16 v[152:155], a[180:183], a[152:155], v[152:155]\n" \
"ds_read_b64_tr_b16 a[168:169], v34 offset:0\n" \
"v_accvgpr_read_b32 v180, a84\n" \
"v_mfma_f32_16x16x32_bf16 v[152:155], a[244:247], a[216:219], v[152:155]\n" \
"ds_read_b64_tr_b16 a[170:171], v34 offset:512\n" \
"v_accvgpr_read_b32 v181, a85\n" \
"v_mfma_f32_16x16x32_bf16 v[156:159], a[184:187], a[152:155], v[156:159]\n" \
"ds_read_b64_tr_b16 a[232:233], v34 offset:4096\n" \
"v_accvgpr_read_b32 v182, a86\n" \
"v_mfma_f32_16x16x32_bf16 v[156:159], a[248:251], a[216:219], v[156:159]\n" \
"ds_read_b64_tr_b16 a[234:235], v34 offset:4608\n" \
"v_accvgpr_read_b32 v183, a87\n" \
"v_mfma_f32_16x16x32_bf16 v[160:163], a[188:191], a[152:155], v[160:163]\n" \
"ds_read_b128 v[248:251], v22 offset:0\n" \
"v_cvt_pk_bf16_f32 v220, v220, v221\n" \
"v_mfma_f32_16x16x32_bf16 v[160:163], a[252:255], a[216:219], v[160:163]\n" \
"ds_read_b128 a[152:155], v28 offset:12288\n" \
"v_cvt_pk_bf16_f32 v221, v222, v223\n" \
"v_mfma_f32_16x16x32_bf16 v[176:179], a[188:191], a[156:159], v[176:179]\n" \
"ds_read_b128 a[216:219], v29 offset:12288\n" \
"v_cvt_pk_bf16_f32 v222, v180, v181\n" \
"v_mfma_f32_16x16x32_bf16 v[176:179], a[252:255], a[220:223], v[176:179]\n" \
"ds_read_b128 v[48:51], v23 offset:0\n" \
"v_cvt_pk_bf16_f32 v223, v182, v183\n" \
"v_mfma_f32_16x16x32_bf16 v[172:175], a[184:187], a[156:159], v[172:175]\n" \
"v_cvt_pk_bf16_f32 v224, v52, v53\n" \
"v_cvt_pk_bf16_f32 v225, v54, v55\n" \
"v_mfma_f32_16x16x32_bf16 v[172:175], a[248:251], a[220:223], v[172:175]\n" \
"v_cvt_pk_bf16_f32 v226, v56, v57\n" \
"v_cvt_pk_bf16_f32 v227, v58, v59\n" \
"v_mfma_f32_16x16x32_bf16 v[168:171], a[180:183], a[156:159], v[168:171]\n" \
"v_cvt_pk_bf16_f32 v228, v60, v61\n" \
"v_cvt_pk_bf16_f32 v229, v62, v63\n" \
"v_mfma_f32_16x16x32_bf16 v[168:171], a[244:247], a[220:223], v[168:171]\n" \
"v_cvt_pk_bf16_f32 v230, v64, v65\n" \
"v_cvt_pk_bf16_f32 v231, v66, v67\n" \
"v_mfma_f32_16x16x32_bf16 v[164:167], a[176:179], a[156:159], v[164:167]\n" \
"v_cvt_pk_bf16_f32 v232, v76, v77\n" \
"v_cvt_pk_bf16_f32 v233, v78, v79\n" \
"v_mfma_f32_16x16x32_bf16 v[164:167], a[240:243], a[220:223], v[164:167]\n" \
"ds_read_b128 a[156:159], v28 offset:14336\n" \
"ds_read_b128 a[220:223], v29 offset:14336\n" \
"s_waitcnt lgkmcnt(15)\n" \
"v_mfma_f32_16x16x32_bf16 a[0:3], a[160:163], a[128:131], 0\n" \
"s_addk_i32 s84, 0x2000\n" \
"v_cvt_pk_bf16_f32 v234, v80, v81\n" \
"v_mfma_f32_16x16x32_bf16 a[0:3], a[224:227], a[192:195], a[0:3]\n" \
"buffer_load_dwordx4 v24, s[76:79], s84 offen offset:2048 lds\n" \
"v_cvt_pk_bf16_f32 v235, v82, v83\n" \
"v_mfma_f32_16x16x32_bf16 a[4:7], a[164:167], a[128:131], 0\n" \
"ds_read_b64_tr_b16 a[176:177], v36 offset:0\n" \
"v_cvt_pk_bf16_f32 v236, v68, v69\n" \
"v_mfma_f32_16x16x32_bf16 a[4:7], a[228:231], a[192:195], a[4:7]\n" \
"s_addk_i32 s84, 0x2000\n" \
"v_cvt_pk_bf16_f32 v237, v70, v71\n" \
"s_waitcnt lgkmcnt(15)\n" \
"v_mfma_f32_16x16x32_bf16 a[12:15], a[172:175], a[128:131], 0\n" \
"buffer_load_dwordx4 v25, s[76:79], s84 offen offset:3072 lds\n" \
"v_cvt_pk_bf16_f32 v238, v72, v73\n" \
"s_waitcnt lgkmcnt(11)\n" \
"v_mfma_f32_16x16x32_bf16 a[12:15], a[236:239], a[192:195], a[12:15]\n" \
"ds_read_b64_tr_b16 a[178:179], v36 offset:1024\n" \
"v_cvt_pk_bf16_f32 v239, v74, v75\n" \
"s_waitcnt lgkmcnt(10)\n" \
"v_mfma_f32_16x16x32_bf16 a[8:11], a[128:131], a[168:171], 0\n" \
"ds_read_b64_tr_b16 a[240:241], v36 offset:8192\n" \
"v_cvt_pk_bf16_f32 v240, v84, v85\n" \
"s_waitcnt lgkmcnt(9)\n" \
"s_nop 0\n" \
"v_mfma_f32_16x16x32_bf16 a[8:11], a[192:195], a[232:235], a[8:11]\n" \
"ds_read_b64_tr_b16 a[242:243], v36 offset:9216\n" \
"v_cvt_pk_bf16_f32 v241, v86, v87\n" \
"v_mfma_f32_16x16x32_bf16 a[24:27], a[132:135], a[168:171], 0\n" \
"ds_read_b64_tr_b16 a[180:181], v37 offset:0\n" \
"v_cvt_pk_bf16_f32 v242, v88, v89\n" \
"v_mfma_f32_16x16x32_bf16 a[24:27], a[196:199], a[232:235], a[24:27]\n" \
"ds_read_b64_tr_b16 a[182:183], v37 offset:1024\n" \
"v_cvt_pk_bf16_f32 v243, v90, v91\n" \
"v_mfma_f32_16x16x32_bf16 a[28:31], a[172:175], a[132:135], 0\n" \
"ds_read_b64_tr_b16 a[244:245], v37 offset:8192\n" \
"v_cvt_pk_bf16_f32 v244, v92, v93\n" \
"v_mfma_f32_16x16x32_bf16 a[28:31], a[236:239], a[196:199], a[28:31]\n" \
"s_add_u32 s89, s41, 0x2000\n" \
"buffer_store_dwordx4 v[200:203], v20, s[80:83], s89 offen offset:0 sc1\n" \
"v_mfma_f32_16x16x32_bf16 a[20:23], a[164:167], a[132:135], 0\n" \
"ds_read_b64_tr_b16 a[246:247], v37 offset:9216\n" \
"v_cvt_pk_bf16_f32 v245, v94, v95\n" \
"v_mfma_f32_16x16x32_bf16 a[20:23], a[228:231], a[196:199], a[20:23]\n" \
"ds_read_b64_tr_b16 a[184:185], v38 offset:0\n" \
"v_cvt_pk_bf16_f32 v246, v96, v97\n" \
"v_mfma_f32_16x16x32_bf16 a[16:19], a[160:163], a[132:135], 0\n" \
"ds_read_b64_tr_b16 a[186:187], v38 offset:1024\n" \
"v_cvt_pk_bf16_f32 v247, v98, v99\n" \
"v_mfma_f32_16x16x32_bf16 a[16:19], a[224:227], a[196:199], a[16:19]\n" \
"v_cvt_pk_bf16_f32 v184, v108, v109\n" \
"v_cvt_pk_bf16_f32 v185, v110, v111\n" \
"v_cvt_pk_bf16_f32 v186, v112, v113\n" \
"v_mfma_f32_16x16x32_bf16 a[32:35], a[160:163], a[136:139], 0\n" \
"s_add_u32 s88, s41, 0x2000\n" \
"buffer_store_dwordx4 v[204:207], v20, s[80:83], s88 offen offset:1024 sc1\n" \
"v_mfma_f32_16x16x32_bf16 a[32:35], a[224:227], a[200:203], a[32:35]\n" \
"ds_read_b64_tr_b16 a[248:249], v38 offset:8192\n" \
"v_cvt_pk_bf16_f32 v187, v114, v115\n" \
"v_mfma_f32_16x16x32_bf16 a[36:39], a[164:167], a[136:139], 0\n" \
"ds_read_b64_tr_b16 a[250:251], v38 offset:9216\n" \
"v_cvt_pk_bf16_f32 v188, v100, v101\n" \
"v_mfma_f32_16x16x32_bf16 a[36:39], a[228:231], a[200:203], a[36:39]\n" \
"ds_read_b64_tr_b16 a[188:189], v39 offset:0\n" \
"v_cvt_pk_bf16_f32 v189, v102, v103\n" \
"v_mfma_f32_16x16x32_bf16 a[44:47], a[172:175], a[136:139], 0\n" \
"v_cvt_pk_bf16_f32 v190, v104, v105\n" \
"v_cvt_pk_bf16_f32 v191, v106, v107\n" \
"v_cvt_pk_bf16_f32 v192, v116, v117\n" \
"v_mfma_f32_16x16x32_bf16 a[44:47], a[236:239], a[200:203], a[44:47]\n" \
"ds_read_b64_tr_b16 a[190:191], v39 offset:1024\n" \
"v_cvt_pk_bf16_f32 v193, v118, v119\n" \
"v_mfma_f32_16x16x32_bf16 a[40:43], a[136:139], a[168:171], 0\n" \
"s_add_u32 s89, s41, 0x3000\n" \
"buffer_store_dwordx4 v[208:211], v20, s[80:83], s89 offen offset:1024 sc1\n" \
"v_mfma_f32_16x16x32_bf16 a[40:43], a[200:203], a[232:235], a[40:43]\n" \
"ds_read_b64_tr_b16 a[252:253], v39 offset:8192\n" \
"v_cvt_pk_bf16_f32 v194, v120, v121\n" \
"v_mfma_f32_16x16x32_bf16 a[56:59], a[140:143], a[168:171], 0\n" \
"ds_read_b64_tr_b16 a[254:255], v39 offset:9216\n" \
"v_cvt_pk_bf16_f32 v195, v122, v123\n" \
"v_mfma_f32_16x16x32_bf16 a[56:59], a[204:207], a[232:235], a[56:59]\n" \
"v_cvt_pk_bf16_f32 v196, v124, v125\n" \
"v_cvt_pk_bf16_f32 v197, v126, v127\n" \
"v_cvt_pk_bf16_f32 v198, v128, v129\n" \
"v_mfma_f32_16x16x32_bf16 a[60:63], a[172:175], a[140:143], 0\n" \
"v_cvt_pk_bf16_f32 v199, v130, v131\n" \
"v_cvt_pk_bf16_f32 v252, v140, v141\n" \
".fill (L_pb + 20192 - .) / 4, 4, 0xBF800000\n" \
"L_pp5:\n" \
"s_cbranch_execz L_pret\n" \
"s_nop 0\n" \
"v_cvt_pk_bf16_f32 v253, v142, v143\n" \
"v_mfma_f32_16x16x32_bf16 a[60:63], a[236:239], a[204:207], a[60:63]\n" \
"s_add_u32 s88, s41, 0x3000\n" \
"buffer_store_dwordx4 v[212:215], v20, s[80:83], s88 offen offset:0 sc1\n" \
"v_mfma_f32_16x16x32_bf16 a[52:55], a[164:167], a[140:143], 0\n" \
"v_cvt_pk_bf16_f32 v254, v144, v145\n" \
"v_cvt_pk_bf16_f32 v255, v146, v147\n" \
"v_accvgpr_write_b32 a84, v252\n" \
"v_mfma_f32_16x16x32_bf16 a[52:55], a[228:231], a[204:207], a[52:55]\n" \
"v_accvgpr_write_b32 a85, v253\n" \
"v_accvgpr_write_b32 a86, v254\n" \
"v_accvgpr_write_b32 a87, v255\n" \
"v_mfma_f32_16x16x32_bf16 a[48:51], a[160:163], a[140:143], 0\n" \
"v_cvt_pk_bf16_f32 v252, v132, v133\n" \
"v_cvt_pk_bf16_f32 v253, v134, v135\n" \
"v_cvt_pk_bf16_f32 v254, v136, v137\n" \
"v_mfma_f32_16x16x32_bf16 a[48:51], a[224:227], a[204:207], a[48:51]\n" \
"v_cvt_pk_bf16_f32 v255, v138, v139\n" \
"s_waitcnt lgkmcnt(15)\n" \
"v_mfma_f32_16x16x32_bf16 a[64:67], a[164:167], v[248:251], 0\n" \
"v_mfma_f32_16x16x32_bf16 a[64:67], a[228:231], v[48:51], a[64:67]\n" \
"v_mfma_f32_16x16x32_bf16 a[80:83], a[172:175], v[248:251], 0\n" \
"v_mfma_f32_16x16x32_bf16 a[80:83], a[236:239], v[48:51], a[80:83]\n" \
"v_mfma_f32_16x16x32_bf16 a[68:71], v[248:251], a[168:171], 0\n" \
"v_mfma_f32_16x16x32_bf16 a[68:71], v[48:51], a[232:235], a[68:71]\n" \
"s_waitcnt vmcnt(4) lgkmcnt(0)\n" \
"s_barrier\n" \
"v_mfma_f32_16x16x32_bf16 v[52:55], a[176:179], a[128:131], 0\n" \
"s_add_u32 m0, s40, 0x0\n" \
"s_add_u32 s85, s39, 0x4000\n" \
"v_cvt_pk_bf16_f32 v200, v148, v149\n" \
"v_mfma_f32_16x16x32_bf16 v[52:55], a[240:243], a[192:195], v[52:55]\n" \
"buffer_load_dwordx4 v20, s[80:83], s85 offen sc1 lds\n" \
"v_cvt_pk_bf16_f32 v201, v150, v151\n" \
"v_mfma_f32_16x16x32_bf16 v[56:59], a[180:183], a[128:131], 0\n" \
"ds_read_b64_tr_b16 a[160:161], v32 offset:32768\n" \
"v_cvt_pk_bf16_f32 v202, v152, v153\n" \
"v_mfma_f32_16x16x32_bf16 v[56:59], a[244:247], a[192:195], v[56:59]\n" \
"ds_read_b64_tr_b16 a[162:163], v32 offset:33280\n" \
"v_cvt_pk_bf16_f32 v203, v154, v155\n" \
"v_mfma_f32_16x16x32_bf16 v[60:63], a[184:187], a[128:131], 0\n" \
"s_add_u32 m0, s40, 0x1000\n" \
"s_add_u32 s85, s39, 0x5000\n" \
"v_cvt_pk_bf16_f32 v204, v156, v157\n" \
"v_mfma_f32_16x16x32_bf16 v[60:63], a[248:251], a[192:195], v[60:63]\n" \
"buffer_load_dwordx4 v20, s[80:83], s85 offen sc1 lds\n" \
"v_cvt_pk_bf16_f32 v205, v158, v159\n" \
"v_mfma_f32_16x16x32_bf16 v[64:67], a[188:191], a[128:131], 0\n" \
"ds_read_b64_tr_b16 a[224:225], v32 offset:36864\n" \
"v_cvt_pk_bf16_f32 v206, v160, v161\n" \
"v_mfma_f32_16x16x32_bf16 v[64:67], a[252:255], a[192:195], v[64:67]\n" \
"ds_read_b128 a[128:131], v28 offset:32768\n" \
"v_cvt_pk_bf16_f32 v207, v162, v163\n" \
"v_mfma_f32_16x16x32_bf16 v[80:83], a[188:191], a[132:135], 0\n" \
"ds_read_b128 a[192:195], v29 offset:32768\n" \
"v_cvt_pk_bf16_f32 v208, v172, v173\n" \
"v_mfma_f32_16x16x32_bf16 v[80:83], a[252:255], a[196:199], v[80:83]\n" \
"s_add_u32 m0, s40, 0x3000\n" \
"s_movk_i32 s84, 0x100\n" \
"v_cvt_pk_bf16_f32 v209, v174, v175\n" \
"v_mfma_f32_16x16x32_bf16 v[76:79], a[184:187], a[132:135], 0\n" \
"buffer_load_dwordx4 v21, s[76:79], s84 offen offset:0 lds\n" \
"v_cvt_pk_bf16_f32 v210, v176, v177\n" \
"v_mfma_f32_16x16x32_bf16 v[76:79], a[248:251], a[196:199], v[76:79]\n" \
"ds_read_b64_tr_b16 a[226:227], v32 offset:37376\n" \
"v_cvt_pk_bf16_f32 v211, v178, v179\n" \
"v_mfma_f32_16x16x32_bf16 v[72:75], a[180:183], a[132:135], 0\n" \
"s_add_u32 m0, s30, 0x0\n" \
"s_movk_i32 s84, 0x100\n" \
"s_nop 0\n" \
"v_cvt_pk_bf16_f32 v212, v164, v165\n" \
"v_mfma_f32_16x16x32_bf16 v[72:75], a[244:247], a[196:199], v[72:75]\n" \
"buffer_load_dwordx4 v24, s[76:79], s84 offen offset:0 lds\n" \
"v_cvt_pk_bf16_f32 v213, v166, v167\n" \
"v_mfma_f32_16x16x32_bf16 v[68:71], a[176:179], a[132:135], 0\n" \
"ds_read_b64_tr_b16 a[164:165], v33 offset:32768\n" \
"v_cvt_pk_bf16_f32 v214, v168, v169\n" \
"v_mfma_f32_16x16x32_bf16 v[68:71], a[240:243], a[196:199], v[68:71]\n" \
"ds_read_b128 a[132:135], v28 offset:34816\n" \
"v_cvt_pk_bf16_f32 v215, v170, v171\n" \
"v_mfma_f32_16x16x32_bf16 v[84:87], a[176:179], a[136:139], 0\n" \
"ds_read_b128 a[196:199], v29 offset:34816\n" \
"v_mfma_f32_16x16x32_bf16 v[84:87], a[240:243], a[200:203], v[84:87]\n" \
"s_movk_i32 s84, 0x2100\n" \
"v_mfma_f32_16x16x32_bf16 v[88:91], a[180:183], a[136:139], 0\n" \
"buffer_load_dwordx4 v25, s[76:79], s84 offen offset:1024 lds\n" \
"v_mfma_f32_16x16x32_bf16 v[88:91], a[244:247], a[200:203], v[88:91]\n" \
"ds_read_b64_tr_b16 a[166:167], v33 offset:33280\n" \
"v_mfma_f32_16x16x32_bf16 v[92:95], a[184:187], a[136:139], 0\n" \
"ds_read_b64_tr_b16 a[228:229], v33 offset:36864\n" \
"v_mfma_f32_16x16x32_bf16 v[92:95], a[248:251], a[200:203], v[92:95]\n" \
"s_movk_i32 s84, 0x4100\n" \
"v_mfma_f32_16x16x32_bf16 v[96:99], a[188:191], a[136:139], 0\n" \
"buffer_load_dwordx4 v24, s[76:79], s84 offen offset:2048 lds\n" \
"v_mfma_f32_16x16x32_bf16 v[96:99], a[252:255], a[200:203], v[96:99]\n" \
"ds_read_b128 a[136:139], v28 offset:36864\n" \
"v_mfma_f32_16x16x32_bf16 v[112:115], a[188:191], a[140:143], 0\n" \
"ds_read_b128 a[200:203], v29 offset:36864\n" \
"v_mfma_f32_16x16x32_bf16 v[112:115], a[252:255], a[204:207], v[112:115]\n" \
"s_movk_i32 s84, 0x6100\n" \
"s_nop 0\n" \
"v_mfma_f32_16x16x32_bf16 v[108:111], a[184:187], a[140:143], 0\n" \
"buffer_load_dwordx4 v25, s[76:79], s84 offen offset:3072 lds\n" \
"v_mfma_f32_16x16x32_bf16 v[108:111], a[248:251], a[204:207], v[108:111]\n" \
"ds_read_b64_tr_b16 a[230:231], v33 offset:37376\n" \
"v_mfma_f32_16x16x32_bf16 v[104:107], a[180:183], a[140:143], 0\n" \
"ds_read_b64_tr_b16 a[172:173], v35 offset:32768\n" \
"v_mfma_f32_16x16x32_bf16 v[104:107], a[244:247], a[204:207], v[104:107]\n" \
"ds_read_b64_tr_b16 a[174:175], v35 offset:33280\n" \
"v_mfma_f32_16x16x32_bf16 v[100:103], a[176:179], a[140:143], 0\n" \
"ds_read_b64_tr_b16 a[236:237], v35 offset:36864\n" \
"v_mfma_f32_16x16x32_bf16 v[100:103], a[240:243], a[204:207], v[100:103]\n" \
"ds_read_b128 a[140:143], v28 offset:38912\n" \
"v_mfma_f32_16x16x32_bf16 v[116:119], a[176:179], a[144:147], 0\n" \
"ds_read_b128 a[204:207], v29 offset:38912\n" \
"v_mfma_f32_16x16x32_bf16 v[116:119], a[240:243], a[208:211], v[116:119]\n" \
"s_add_u32 m0, s31, 0x0\n" \
"s_mov_b32 s84, 0x100000\n" \
"v_mfma_f32_16x16x32_bf16 v[120:123], a[180:183], a[144:147], 0\n" \
"buffer_load_dwordx4 v26, s[68:71], s84 offen offset:0 lds\n" \
"v_mfma_f32_16x16x32_bf16 v[120:123], a[244:247], a[208:211], v[120:123]\n" \
"s_addk_i32 s84, 0x7c00\n" \
"v_mfma_f32_16x16x32_bf16 v[124:127], a[184:187], a[144:147], 0\n" \
"buffer_load_dwordx4 v26, s[68:71], s84 offen offset:1024 lds\n" \
"v_mfma_f32_16x16x32_bf16 v[124:127], a[248:251], a[208:211], v[124:127]\n" \
"ds_read_b64_tr_b16 a[238:239], v35 offset:37376\n" \
"v_mfma_f32_16x16x32_bf16 v[128:131], a[188:191], a[144:147], 0\n" \
"ds_read_b64_tr_b16 a[168:169], v34 offset:32768\n" \
"v_mfma_f32_16x16x32_bf16 v[128:131], a[252:255], a[208:211], v[128:131]\n" \
"ds_read_b128 a[144:147], v28 offset:40960\n" \
"v_mfma_f32_16x16x32_bf16 v[144:147], a[188:191], a[148:151], 0\n" \
"ds_read_b128 a[208:211], v29 offset:40960\n" \
"v_mfma_f32_16x16x32_bf16 v[144:147], a[252:255], a[212:215], v[144:147]\n" \
"s_addk_i32 s84, 0x7c00\n" \
"v_mfma_f32_16x16x32_bf16 v[140:143], a[184:187], a[148:151], 0\n" \
"buffer_load_dwordx4 v27, s[68:71], s84 offen offset:2048 lds\n" \
"v_mfma_f32_16x16x32_bf16 v[140:143], a[248:251], a[212:215], v[140:143]\n" \
"s_addk_i32 s84, 0x7c00\n" \
"v_mfma_f32_16x16x32_bf16 v[136:139], a[180:183], a[148:151], 0\n" \
"buffer_load_dwordx4 v27, s[68:71], s84 offen offset:3072 lds\n" \
"v_mfma_f32_16x16x32_bf16 v[136:139], a[244:247], a[212:215], v[136:139]\n" \
"ds_read_b64_tr_b16 a[170:171], v34 offset:33280\n" \
"v_mfma_f32_16x16x32_bf16 v[132:135], a[176:179], a[148:151], 0\n" \
"ds_read_b64_tr_b16 a[232:233], v34 offset:36864\n" \
"v_mfma_f32_16x16x32_bf16 v[132:135], a[240:243], a[212:215], v[132:135]\n" \
"ds_read_b128 a[148:151], v28 offset:43008\n" \
"v_mfma_f32_16x16x32_bf16 v[148:151], a[176:179], a[152:155], 0\n" \
"ds_read_b128 a[212:215], v29 offset:43008\n" \
"v_mfma_f32_16x16x32_bf16 v[148:151], a[240:243], a[216:219], v[148:151]\n" \
"s_add_u32 m0, s30, 0x1000\n" \
"s_mov_b32 s84, 0x9100\n" \
"v_mfma_f32_16x16x32_bf16 v[152:155], a[180:183], a[152:155], 0\n" \
"buffer_load_dwordx4 v24, s[76:79], s84 offen offset:0 lds\n" \
"v_mfma_f32_16x16x32_bf16 v[152:155], a[244:247], a[216:219], v[152:155]\n" \
"s_addk_i32 s84, 0x2000\n" \
"s_nop 0\n" \
"v_mfma_f32_16x16x32_bf16 v[156:159], a[184:187], a[152:155], 0\n" \
"buffer_load_dwordx4 v25, s[76:79], s84 offen offset:1024 lds\n" \
"v_mfma_f32_16x16x32_bf16 v[156:159], a[248:251], a[216:219], v[156:159]\n" \
"ds_read_b64_tr_b16 a[234:235], v34 offset:37376\n" \
"v_mfma_f32_16x16x32_bf16 v[160:163], a[188:191], a[152:155], 0\n" \
"ds_read_b128 v[248:251], v22 offset:32768\n" \
"v_mfma_f32_16x16x32_bf16 v[160:163], a[252:255], a[216:219], v[160:163]\n" \
"ds_read_b128 a[152:155], v28 offset:45056\n" \
"v_mfma_f32_16x16x32_bf16 v[176:179], a[188:191], a[156:159], 0\n" \
"ds_read_b128 a[216:219], v29 offset:45056\n" \
"v_mfma_f32_16x16x32_bf16 v[176:179], a[252:255], a[220:223], v[176:179]\n" \
"ds_read_b128 v[48:51], v23 offset:32768\n" \
"v_mfma_f32_16x16x32_bf16 v[172:175], a[184:187], a[156:159], 0\n" \
"v_mfma_f32_16x16x32_bf16 v[172:175], a[248:251], a[220:223], v[172:175]\n" \
"v_mfma_f32_16x16x32_bf16 v[168:171], a[180:183], a[156:159], 0\n" \
"v_mfma_f32_16x16x32_bf16 v[168:171], a[244:247], a[220:223], v[168:171]\n" \
"v_mfma_f32_16x16x32_bf16 v[164:167], a[176:179], a[156:159], 0\n" \
"v_mfma_f32_16x16x32_bf16 v[164:167], a[240:243], a[220:223], v[164:167]\n" \
"ds_read_b128 a[156:159], v28 offset:47104\n" \
"ds_read_b128 a[220:223], v29 offset:47104\n" \
"s_waitcnt lgkmcnt(15)\n" \
"v_mfma_f32_16x16x32_bf16 a[0:3], a[160:163], a[128:131], a[0:3]\n" \
"s_addk_i32 s84, 0x2000\n" \
"v_mfma_f32_16x16x32_bf16 a[0:3], a[224:227], a[192:195], a[0:3]\n" \
"buffer_load_dwordx4 v24, s[76:79], s84 offen offset:2048 lds\n" \
"v_mfma_f32_16x16x32_bf16 a[4:7], a[164:167], a[128:131], a[4:7]\n" \
"ds_read_b64_tr_b16 a[176:177], v36 offset:32768\n" \
"v_mov_b32_e64 v11, 0\n" \
"v_mfma_f32_16x16x32_bf16 a[4:7], a[228:231], a[192:195], a[4:7]\n" \
"s_addk_i32 s84, 0x2000\n" \
"v_mfma_f32_16x16x32_bf16 a[12:15], a[172:175], a[128:131], a[12:15]\n" \
"buffer_load_dwordx4 v25, s[76:79], s84 offen offset:3072 lds\n" \
"s_waitcnt lgkmcnt(15)\n" \
"v_mfma_f32_16x16x32_bf16 a[12:15], a[236:239], a[192:195], a[12:15]\n" \
"ds_read_b64_tr_b16 a[178:179], v36 offset:33792\n" \
"s_and_b32 s6, s2, 7\n" \
"s_waitcnt lgkmcnt(12)\n" \
"v_mfma_f32_16x16x32_bf16 a[8:11], a[128:131], a[168:171], a[8:11]\n" \
"ds_read_b64_tr_b16 a[240:241], v36 offset:40960\n" \
"s_lshl_b32 s7, s3, 1\n" \
"s_waitcnt lgkmcnt(9)\n" \
"v_mfma_f32_16x16x32_bf16 a[8:11], a[192:195], a[232:235], a[8:11]\n" \
"ds_read_b64_tr_b16 a[242:243], v36 offset:41984\n" \
"s_lshr_b32 s4, s2, 5\n" \
"v_mfma_f32_16x16x32_bf16 a[24:27], a[132:135], a[168:171], a[24:27]\n" \
"ds_read_b64_tr_b16 a[180:181], v37 offset:32768\n" \
"s_lshl_b32 s5, s6, 2\n" \
"v_mfma_f32_16x16x32_bf16 a[24:27], a[196:199], a[232:235], a[24:27]\n" \
"ds_read_b64_tr_b16 a[182:183], v37 offset:33792\n" \
"s_bfe_u32 s8, s2, 0x20003\n" \
"v_mfma_f32_16x16x32_bf16 a[28:31], a[172:175], a[132:135], a[28:31]\n" \
"ds_read_b64_tr_b16 a[244:245], v37 offset:40960\n" \
"s_and_b32 s7, s7, 4\n" \
"v_mfma_f32_16x16x32_bf16 a[28:31], a[236:239], a[196:199], a[28:31]\n" \
"s_add_u32 s89, s41, 0x0\n" \
"buffer_store_dwordx4 v[224:227], v20, s[80:83], s89 offen offset:2048 sc1\n" \
"v_mfma_f32_16x16x32_bf16 a[20:23], a[164:167], a[132:135], a[20:23]\n" \
"ds_read_b64_tr_b16 a[246:247], v37 offset:41984\n" \
"s_and_b32 s9, s3, 1\n" \
"v_mfma_f32_16x16x32_bf16 a[20:23], a[228:231], a[196:199], a[20:23]\n" \
"ds_read_b64_tr_b16 a[184:185], v38 offset:32768\n" \
"s_or_b32 s13, s5, s8\n" \
"v_mfma_f32_16x16x32_bf16 a[16:19], a[160:163], a[132:135], a[16:19]\n" \
"ds_read_b64_tr_b16 a[186:187], v38 offset:33792\n" \
"s_or_b32 s9, s7, s9\n" \
"v_mfma_f32_16x16x32_bf16 a[16:19], a[224:227], a[196:199], a[16:19]\n" \
"s_mul_i32 s7, s4, 0xa00\n" \
"s_add_i32 s7, s7, 0x5bc1000\n" \
"v_mfma_f32_16x16x32_bf16 a[32:35], a[160:163], a[136:139], a[32:35]\n" \
"s_add_u32 s88, s41, 0x0\n" \
"buffer_store_dwordx4 v[228:231], v20, s[80:83], s88 offen offset:3072 sc1\n" \
"v_mfma_f32_16x16x32_bf16 a[32:35], a[224:227], a[200:203], a[32:35]\n" \
"ds_read_b64_tr_b16 a[248:249], v38 offset:40960\n" \
"s_lshl_b32 s10, s13, 2\n" \
"v_mfma_f32_16x16x32_bf16 a[36:39], a[164:167], a[136:139], a[36:39]\n" \
"ds_read_b64_tr_b16 a[250:251], v38 offset:41984\n" \
"s_or_b32 s10, s10, s7\n" \
"v_mfma_f32_16x16x32_bf16 a[36:39], a[228:231], a[200:203], a[36:39]\n" \
"ds_read_b64_tr_b16 a[188:189], v39 offset:32768\n" \
"v_writelane_b32 v11, s10, 0\n" \
"v_mfma_f32_16x16x32_bf16 a[44:47], a[172:175], a[136:139], a[44:47]\n" \
"s_lshl_b32 s14, s4, 3\n" \
"v_writelane_b32 v11, s7, 1\n" \
"v_mfma_f32_16x16x32_bf16 a[44:47], a[236:239], a[200:203], a[44:47]\n" \
"ds_read_b64_tr_b16 a[190:191], v39 offset:33792\n" \
"s_or_b32 s7, s14, s6\n" \
"v_mfma_f32_16x16x32_bf16 a[40:43], a[136:139], a[168:171], a[40:43]\n" \
"s_add_u32 s89, s41, 0x1000\n" \
"buffer_store_dwordx4 v[232:235], v20, s[80:83], s89 offen offset:3072 sc1\n" \
"v_mfma_f32_16x16x32_bf16 a[40:43], a[200:203], a[232:235], a[40:43]\n" \
"ds_read_b64_tr_b16 a[252:253], v39 offset:40960\n" \
"s_lshl_b32 s7, s7, 10\n" \
"v_mfma_f32_16x16x32_bf16 a[56:59], a[140:143], a[168:171], a[56:59]\n" \
"ds_read_b64_tr_b16 a[254:255], v39 offset:41984\n" \
"s_lshl_b32 s10, s3, 8\n" \
"v_mfma_f32_16x16x32_bf16 a[56:59], a[204:207], a[232:235], a[56:59]\n" \
"s_or_b32 s7, s7, s10\n" \
"s_or_b32 s10, s7, 0x5c00200\n" \
"v_mfma_f32_16x16x32_bf16 a[60:63], a[172:175], a[140:143], a[60:63]\n" \
"v_writelane_b32 v11, s10, 2\n" \
"s_lshl_b32 s10, s8, 2\n" \
"v_mfma_f32_16x16x32_bf16 a[60:63], a[236:239], a[204:207], a[60:63]\n" \
"s_add_u32 s88, s41, 0x1000\n" \
"buffer_store_dwordx4 v[236:239], v20, s[80:83], s88 offen offset:2048 sc1\n" \
"v_mfma_f32_16x16x32_bf16 a[52:55], a[164:167], a[140:143], a[52:55]\n" \
"s_or_b32 s11, s10, s7\n" \
"s_or_b32 s11, s11, 0x5c00000\n" \
"v_mfma_f32_16x16x32_bf16 a[52:55], a[228:231], a[204:207], a[52:55]\n" \
"v_writelane_b32 v11, s11, 3\n" \
"s_or_b32 s7, s7, 0x5c00080\n" \
"v_mfma_f32_16x16x32_bf16 a[48:51], a[160:163], a[140:143], a[48:51]\n" \
"v_writelane_b32 v11, s7, 4\n" \
"s_or_b32 s7, s7, s10\n" \
"v_mfma_f32_16x16x32_bf16 a[48:51], a[224:227], a[204:207], a[48:51]\n" \
"s_add_i32 s10, s3, s2\n" \
"v_writelane_b32 v11, s7, 5\n" \
"s_waitcnt lgkmcnt(15)\n" \
"v_mfma_f32_16x16x32_bf16 a[64:67], a[164:167], v[248:251], a[64:67]\n" \
"s_mov_b32 s7, 2.0\n" \
"s_mov_b32 s11, 0x5bc0000\n" \
"v_mfma_f32_16x16x32_bf16 a[64:67], a[228:231], v[48:51], a[64:67]\n" \
"s_cmp_lg_u32 s10, 0\n" \
"s_cselect_b32 s10, s7, s11\n" \
"v_mfma_f32_16x16x32_bf16 a[80:83], a[172:175], v[248:251], a[80:83]\n" \
"s_lshr_b32 s15, s2, 3\n" \
"v_writelane_b32 v11, s10, 53\n" \
"v_mfma_f32_16x16x32_bf16 a[80:83], a[236:239], v[48:51], a[80:83]\n" \
"s_lshl_b32 s10, s6, 5\n" \
"s_min_u32 s16, s3, 2\n" \
"v_mfma_f32_16x16x32_bf16 a[68:71], v[248:251], a[168:171], a[68:71]\n" \
"s_mul_i32 s12, s8, 0x120\n" \
"v_mfma_f32_16x16x32_bf16 a[68:71], v[48:51], a[232:235], a[68:71]\n" \
"s_waitcnt vmcnt(4) lgkmcnt(0)\n" \
"s_barrier\n" \
"v_mfma_f32_16x16x32_bf16 v[52:55], a[176:179], a[128:131], v[52:55]\n" \
"s_add_u32 m0, s40, 0x8000\n" \
"s_add_u32 s85, s39, 0x6000\n" \
"v_mfma_f32_16x16x32_bf16 v[52:55], a[240:243], a[192:195], v[52:55]\n" \
"buffer_load_dwordx4 v20, s[80:83], s85 offen sc1 lds\n" \
"v_mfma_f32_16x16x32_bf16 v[56:59], a[180:183], a[128:131], v[56:59]\n" \
"ds_read_b64_tr_b16 a[160:161], v32 offset:0\n" \
"v_mfma_f32_16x16x32_bf16 v[56:59], a[244:247], a[192:195], v[56:59]\n" \
"ds_read_b64_tr_b16 a[162:163], v32 offset:512\n" \
"v_mfma_f32_16x16x32_bf16 v[60:63], a[184:187], a[128:131], v[60:63]\n" \
"s_add_u32 m0, s40, 0x9000\n" \
"s_add_u32 s85, s39, 0x7000\n" \
"v_mfma_f32_16x16x32_bf16 v[60:63], a[248:251], a[192:195], v[60:63]\n" \
"buffer_load_dwordx4 v20, s[80:83], s85 offen sc1 lds\n" \
"v_mfma_f32_16x16x32_bf16 v[64:67], a[188:191], a[128:131], v[64:67]\n" \
"ds_read_b64_tr_b16 a[224:225], v32 offset:4096\n" \
"v_mfma_f32_16x16x32_bf16 v[64:67], a[252:255], a[192:195], v[64:67]\n" \
"ds_read_b128 a[128:131], v28 offset:0\n" \
"v_mfma_f32_16x16x32_bf16 v[80:83], a[188:191], a[132:135], v[80:83]\n" \
"ds_read_b128 a[192:195], v29 offset:0\n" \
"v_mfma_f32_16x16x32_bf16 v[80:83], a[252:255], a[196:199], v[80:83]\n" \
"s_add_u32 m0, s40, 0xb000\n" \
"s_movk_i32 s84, 0x180\n" \
"v_mfma_f32_16x16x32_bf16 v[76:79], a[184:187], a[132:135], v[76:79]\n" \
"buffer_load_dwordx4 v21, s[76:79], s84 offen offset:0 lds\n" \
"v_mfma_f32_16x16x32_bf16 v[76:79], a[248:251], a[196:199], v[76:79]\n" \
"ds_read_b64_tr_b16 a[226:227], v32 offset:4608\n" \
"v_mfma_f32_16x16x32_bf16 v[72:75], a[180:183], a[132:135], v[72:75]\n" \
"s_add_u32 m0, s30, 0x8000\n" \
"s_movk_i32 s84, 0x180\n" \
"v_mfma_f32_16x16x32_bf16 v[72:75], a[244:247], a[196:199], v[72:75]\n" \
"buffer_load_dwordx4 v24, s[76:79], s84 offen offset:0 lds\n" \
"v_mfma_f32_16x16x32_bf16 v[68:71], a[176:179], a[132:135], v[68:71]\n" \
"ds_read_b64_tr_b16 a[164:165], v33 offset:0\n" \
"v_mfma_f32_16x16x32_bf16 v[68:71], a[240:243], a[196:199], v[68:71]\n" \
"ds_read_b128 a[132:135], v28 offset:2048\n" \
"v_mfma_f32_16x16x32_bf16 v[84:87], a[176:179], a[136:139], v[84:87]\n" \
"ds_read_b128 a[196:199], v29 offset:2048\n" \
"v_mfma_f32_16x16x32_bf16 v[84:87], a[240:243], a[200:203], v[84:87]\n" \
"s_movk_i32 s84, 0x2180\n" \
"v_mfma_f32_16x16x32_bf16 v[88:91], a[180:183], a[136:139], v[88:91]\n" \
"buffer_load_dwordx4 v25, s[76:79], s84 offen offset:1024 lds\n" \
"v_mfma_f32_16x16x32_bf16 v[88:91], a[244:247], a[200:203], v[88:91]\n" \
"ds_read_b64_tr_b16 a[166:167], v33 offset:512\n" \
"v_mfma_f32_16x16x32_bf16 v[92:95], a[184:187], a[136:139], v[92:95]\n" \
"ds_read_b64_tr_b16 a[228:229], v33 offset:4096\n" \
"v_mfma_f32_16x16x32_bf16 v[92:95], a[248:251], a[200:203], v[92:95]\n" \
"s_movk_i32 s84, 0x4180\n" \
"v_mfma_f32_16x16x32_bf16 v[96:99], a[188:191], a[136:139], v[96:99]\n" \
"buffer_load_dwordx4 v24, s[76:79], s84 offen offset:2048 lds\n" \
"v_mfma_f32_16x16x32_bf16 v[96:99], a[252:255], a[200:203], v[96:99]\n" \
"ds_read_b128 a[136:139], v28 offset:4096\n" \
"v_mfma_f32_16x16x32_bf16 v[112:115], a[188:191], a[140:143], v[112:115]\n" \
"ds_read_b128 a[200:203], v29 offset:4096\n" \
"v_mfma_f32_16x16x32_bf16 v[112:115], a[252:255], a[204:207], v[112:115]\n" \
"s_movk_i32 s84, 0x6180\n" \
"s_nop 0\n" \
"v_mfma_f32_16x16x32_bf16 v[108:111], a[184:187], a[140:143], v[108:111]\n" \
"buffer_load_dwordx4 v25, s[76:79], s84 offen offset:3072 lds\n" \
"v_mfma_f32_16x16x32_bf16 v[108:111], a[248:251], a[204:207], v[108:111]\n" \
"ds_read_b64_tr_b16 a[230:231], v33 offset:4608\n" \
"v_mfma_f32_16x16x32_bf16 v[104:107], a[180:183], a[140:143], v[104:107]\n" \
"ds_read_b64_tr_b16 a[172:173], v35 offset:0\n" \
"v_mfma_f32_16x16x32_bf16 v[104:107], a[244:247], a[204:207], v[104:107]\n" \
"ds_read_b64_tr_b16 a[174:175], v35 offset:512\n" \
"v_mfma_f32_16x16x32_bf16 v[100:103], a[176:179], a[140:143], v[100:103]\n" \
"ds_read_b64_tr_b16 a[236:237], v35 offset:4096\n" \
"v_mfma_f32_16x16x32_bf16 v[100:103], a[240:243], a[204:207], v[100:103]\n" \
"ds_read_b128 a[140:143], v28 offset:6144\n" \
"v_mfma_f32_16x16x32_bf16 v[116:119], a[176:179], a[144:147], v[116:119]\n" \
"ds_read_b128 a[204:207], v29 offset:6144\n" \
"v_mfma_f32_16x16x32_bf16 v[116:119], a[240:243], a[208:211], v[116:119]\n" \
"s_add_u32 m0, s31, 0x8000\n" \
"s_mov_b32 s84, 0x180000\n" \
"v_mfma_f32_16x16x32_bf16 v[120:123], a[180:183], a[144:147], v[120:123]\n" \
"buffer_load_dwordx4 v26, s[68:71], s84 offen offset:0 lds\n" \
"v_mfma_f32_16x16x32_bf16 v[120:123], a[244:247], a[208:211], v[120:123]\n" \
"s_addk_i32 s84, 0x7c00\n" \
"s_nop 0\n" \
"v_mfma_f32_16x16x32_bf16 v[124:127], a[184:187], a[144:147], v[124:127]\n" \
"buffer_load_dwordx4 v26, s[68:71], s84 offen offset:1024 lds\n" \
"v_mfma_f32_16x16x32_bf16 v[124:127], a[248:251], a[208:211], v[124:127]\n" \
"ds_read_b64_tr_b16 a[238:239], v35 offset:4608\n" \
"v_mfma_f32_16x16x32_bf16 v[128:131], a[188:191], a[144:147], v[128:131]\n" \
"ds_read_b64_tr_b16 a[168:169], v34 offset:0\n" \
"v_mfma_f32_16x16x32_bf16 v[128:131], a[252:255], a[208:211], v[128:131]\n" \
"ds_read_b128 a[144:147], v28 offset:8192\n" \
"v_mfma_f32_16x16x32_bf16 v[144:147], a[188:191], a[148:151], v[144:147]\n" \
"ds_read_b128 a[208:211], v29 offset:8192\n" \
"v_mfma_f32_16x16x32_bf16 v[144:147], a[252:255], a[212:215], v[144:147]\n" \
"s_addk_i32 s84, 0x7c00\n" \
"v_mfma_f32_16x16x32_bf16 v[140:143], a[184:187], a[148:151], v[140:143]\n" \
"buffer_load_dwordx4 v27, s[68:71], s84 offen offset:2048 lds\n" \
"v_mfma_f32_16x16x32_bf16 v[140:143], a[248:251], a[212:215], v[140:143]\n" \
"s_addk_i32 s84, 0x7c00\n" \
"v_mfma_f32_16x16x32_bf16 v[136:139], a[180:183], a[148:151], v[136:139]\n" \
"buffer_load_dwordx4 v27, s[68:71], s84 offen offset:3072 lds\n" \
"v_mfma_f32_16x16x32_bf16 v[136:139], a[244:247], a[212:215], v[136:139]\n" \
"ds_read_b64_tr_b16 a[170:171], v34 offset:512\n" \
"v_mfma_f32_16x16x32_bf16 v[132:135], a[176:179], a[148:151], v[132:135]\n" \
"ds_read_b64_tr_b16 a[232:233], v34 offset:4096\n" \
"v_mfma_f32_16x16x32_bf16 v[132:135], a[240:243], a[212:215], v[132:135]\n" \
"ds_read_b128 a[148:151], v28 offset:10240\n" \
"v_mfma_f32_16x16x32_bf16 v[148:151], a[176:179], a[152:155], v[148:151]\n" \
"ds_read_b128 a[212:215], v29 offset:10240\n" \
"v_mfma_f32_16x16x32_bf16 v[148:151], a[240:243], a[216:219], v[148:151]\n" \
"s_add_u32 m0, s30, 0x9000\n" \
"s_mov_b32 s84, 0x9180\n" \
"v_mfma_f32_16x16x32_bf16 v[152:155], a[180:183], a[152:155], v[152:155]\n" \
"buffer_load_dwordx4 v24, s[76:79], s84 offen offset:0 lds\n" \
"v_mfma_f32_16x16x32_bf16 v[152:155], a[244:247], a[216:219], v[152:155]\n" \
"s_addk_i32 s84, 0x2000\n" \
"s_nop 0\n" \
"v_mfma_f32_16x16x32_bf16 v[156:159], a[184:187], a[152:155], v[156:159]\n" \
"buffer_load_dwordx4 v25, s[76:79], s84 offen offset:1024 lds\n" \
"v_mfma_f32_16x16x32_bf16 v[156:159], a[248:251], a[216:219], v[156:159]\n" \
"ds_read_b64_tr_b16 a[234:235], v34 offset:4608\n" \
"v_mfma_f32_16x16x32_bf16 v[160:163], a[188:191], a[152:155], v[160:163]\n" \
"ds_read_b128 v[248:251], v22 offset:0\n" \
"v_mfma_f32_16x16x32_bf16 v[160:163], a[252:255], a[216:219], v[160:163]\n" \
"ds_read_b128 a[152:155], v28 offset:12288\n" \
"v_mfma_f32_16x16x32_bf16 v[176:179], a[188:191], a[156:159], v[176:179]\n" \
"ds_read_b128 a[216:219], v29 offset:12288\n" \
"v_mfma_f32_16x16x32_bf16 v[176:179], a[252:255], a[220:223], v[176:179]\n" \
"ds_read_b128 v[48:51], v23 offset:0\n" \
"v_mfma_f32_16x16x32_bf16 v[172:175], a[184:187], a[156:159], v[172:175]\n" \
"s_add_u32 s11, s22, s12\n" \
"s_addc_u32 s17, s23, 0\n" \
"v_mfma_f32_16x16x32_bf16 v[172:175], a[248:251], a[220:223], v[172:175]\n" \
"s_add_u32 s18, s24, s12\n" \
"v_writelane_b32 v11, s11, 9\n" \
"s_addc_u32 s19, s25, 0\n" \
"v_mfma_f32_16x16x32_bf16 v[168:171], a[180:183], a[156:159], v[168:171]\n" \
"s_and_b32 s20, s17, 0xffff\n" \
"v_writelane_b32 v11, s20, 10\n" \
"v_mfma_f32_16x16x32_bf16 v[168:171], a[244:247], a[220:223], v[168:171]\n" \
"s_and_b32 s20, s19, 0xffff\n" \
"v_writelane_b32 v11, s18, 11\n" \
"v_mfma_f32_16x16x32_bf16 v[164:167], a[176:179], a[156:159], v[164:167]\n" \
"s_cmpk_lt_u32 s3, 3\n" \
"s_cselect_b32 s11, s11, s18\n" \
"v_mfma_f32_16x16x32_bf16 v[164:167], a[240:243], a[220:223], v[164:167]\n" \
"ds_read_b128 a[156:159], v28 offset:14336\n" \
"ds_read_b128 a[220:223], v29 offset:14336\n" \
"s_waitcnt lgkmcnt(15)\n" \
"v_mfma_f32_16x16x32_bf16 a[0:3], a[160:163], a[128:131], a[0:3]\n" \
"s_addk_i32 s84, 0x2000\n" \
"v_mfma_f32_16x16x32_bf16 a[0:3], a[224:227], a[192:195], a[0:3]\n" \
"buffer_load_dwordx4 v24, s[76:79], s84 offen offset:2048 lds\n" \
"v_mfma_f32_16x16x32_bf16 a[4:7], a[164:167], a[128:131], a[4:7]\n" \
"ds_read_b64_tr_b16 a[176:177], v36 offset:0\n" \
"s_movk_i32 s18, 0xfff\n" \
"v_mfma_f32_16x16x32_bf16 a[4:7], a[228:231], a[192:195], a[4:7]\n" \
"s_addk_i32 s84, 0x2000\n" \
"v_mfma_f32_16x16x32_bf16 a[12:15], a[172:175], a[128:131], a[12:15]\n" \
"buffer_load_dwordx4 v25, s[76:79], s84 offen offset:3072 lds\n" \
"s_waitcnt lgkmcnt(15)\n" \
"v_mfma_f32_16x16x32_bf16 a[12:15], a[236:239], a[192:195], a[12:15]\n" \
"ds_read_b64_tr_b16 a[178:179], v36 offset:1024\n" \
"v_writelane_b32 v11, s20, 12\n" \
"s_waitcnt lgkmcnt(12)\n" \
"v_mfma_f32_16x16x32_bf16 a[8:11], a[128:131], a[168:171], a[8:11]\n" \
"ds_read_b64_tr_b16 a[240:241], v36 offset:8192\n" \
"s_and_b32 s20, s29, 0xffff\n" \
"s_waitcnt lgkmcnt(9)\n" \
"s_nop 0\n" \
"v_mfma_f32_16x16x32_bf16 a[8:11], a[192:195], a[232:235], a[8:11]\n" \
"ds_read_b64_tr_b16 a[242:243], v36 offset:9216\n" \
"v_writelane_b32 v11, s28, 13\n" \
"v_mfma_f32_16x16x32_bf16 a[24:27], a[132:135], a[168:171], a[24:27]\n" \
"ds_read_b64_tr_b16 a[180:181], v37 offset:0\n" \
"v_writelane_b32 v11, s20, 14\n" \
"v_mfma_f32_16x16x32_bf16 a[24:27], a[196:199], a[232:235], a[24:27]\n" \
"ds_read_b64_tr_b16 a[182:183], v37 offset:1024\n" \
"v_writelane_b32 v11, s11, 15\n" \
"v_mfma_f32_16x16x32_bf16 a[28:31], a[172:175], a[132:135], a[28:31]\n" \
"ds_read_b64_tr_b16 a[244:245], v37 offset:8192\n" \
"v_mfma_f32_16x16x32_bf16 a[28:31], a[236:239], a[196:199], a[28:31]\n" \
"s_add_u32 s89, s41, 0x2000\n" \
"buffer_store_dwordx4 v[240:243], v20, s[80:83], s89 offen offset:2048 sc1\n" \
"v_mfma_f32_16x16x32_bf16 a[20:23], a[164:167], a[132:135], a[20:23]\n" \
"ds_read_b64_tr_b16 a[246:247], v37 offset:9216\n" \
"v_mfma_f32_16x16x32_bf16 a[20:23], a[228:231], a[196:199], a[20:23]\n" \
"ds_read_b64_tr_b16 a[184:185], v38 offset:0\n" \
"v_mfma_f32_16x16x32_bf16 a[16:19], a[160:163], a[132:135], a[16:19]\n" \
"ds_read_b64_tr_b16 a[186:187], v38 offset:1024\n" \
"v_mfma_f32_16x16x32_bf16 a[16:19], a[224:227], a[196:199], a[16:19]\n" \
"s_cmpk_lt_u32 s3, 3\n" \
"s_cselect_b32 s11, s17, s19\n" \
"v_mfma_f32_16x16x32_bf16 a[32:35], a[160:163], a[136:139], a[32:35]\n" \
"s_add_u32 s88, s41, 0x2000\n" \
"buffer_store_dwordx4 v[244:247], v20, s[80:83], s88 offen offset:3072 sc1\n" \
"v_mfma_f32_16x16x32_bf16 a[32:35], a[224:227], a[200:203], a[32:35]\n" \
"ds_read_b64_tr_b16 a[248:249], v38 offset:8192\n" \
"s_and_b32 s11, s11, 0xffff\n" \
"v_mfma_f32_16x16x32_bf16 a[36:39], a[164:167], a[136:139], a[36:39]\n" \
"ds_read_b64_tr_b16 a[250:251], v38 offset:9216\n" \
"v_writelane_b32 v11, s11, 16\n" \
"v_mfma_f32_16x16x32_bf16 a[36:39], a[228:231], a[200:203], a[36:39]\n" \
"ds_read_b64_tr_b16 a[188:189], v39 offset:0\n" \
"s_mul_i32 s11, s6, 0x48\n" \
"v_mfma_f32_16x16x32_bf16 a[44:47], a[172:175], a[136:139], a[44:47]\n" \
"s_add_i32 s17, s11, 0x47\n" \
"v_mfma_f32_16x16x32_bf16 a[44:47], a[236:239], a[200:203], a[44:47]\n" \
"ds_read_b64_tr_b16 a[190:191], v39 offset:1024\n" \
"v_mfma_f32_16x16x32_bf16 a[40:43], a[136:139], a[168:171], a[40:43]\n" \
"s_add_u32 s89, s41, 0x3000\n" \
"buffer_store_dwordx4 v[184:187], v20, s[80:83], s89 offen offset:3072 sc1\n" \
"v_mfma_f32_16x16x32_bf16 a[40:43], a[200:203], a[232:235], a[40:43]\n" \
"ds_read_b64_tr_b16 a[252:253], v39 offset:8192\n" \
"v_mfma_f32_16x16x32_bf16 a[56:59], a[140:143], a[168:171], a[56:59]\n" \
"ds_read_b64_tr_b16 a[254:255], v39 offset:9216\n" \
"v_mfma_f32_16x16x32_bf16 a[56:59], a[204:207], a[232:235], a[56:59]\n" \
"s_cmpk_lt_u32 s3, 3\n" \
"s_cselect_b32 s17, s17, s18\n" \
"v_mfma_f32_16x16x32_bf16 a[60:63], a[172:175], a[140:143], a[60:63]\n" \
"s_mulk_i32 s17, 0x480\n" \
"s_addk_i32 s17, 0x120\n" \
"v_mfma_f32_16x16x32_bf16 a[60:63], a[236:239], a[204:207], a[60:63]\n" \
"s_add_u32 s88, s41, 0x3000\n" \
"buffer_store_dwordx4 v[188:191], v20, s[80:83], s88 offen offset:2048 sc1\n" \
"v_mfma_f32_16x16x32_bf16 a[52:55], a[164:167], a[140:143], a[52:55]\n" \
"v_writelane_b32 v11, s17, 17\n" \
"s_lshl_b32 s17, s3, 5\n" \
"v_mfma_f32_16x16x32_bf16 a[52:55], a[228:231], a[204:207], a[52:55]\n" \
"s_add_i32 s11, s11, s17\n" \
"s_mul_i32 s17, s11, 0x480\n" \
"v_mfma_f32_16x16x32_bf16 a[48:51], a[160:163], a[140:143], a[48:51]\n" \
"s_mul_i32 s11, s13, 0x24000\n" \
"s_add_i32 s18, s11, 0x1b000\n" \
"v_mfma_f32_16x16x32_bf16 a[48:51], a[224:227], a[204:207], a[48:51]\n" \
"s_cmpk_lt_u32 s3, 3\n" \
"s_cselect_b32 s17, s17, s18\n" \
"s_waitcnt lgkmcnt(15)\n" \
"v_mfma_f32_16x16x32_bf16 a[64:67], a[164:167], v[248:251], a[64:67]\n" \
"s_mul_i32 s19, s3, 0x9000\n" \
"v_writelane_b32 v11, s17, 18\n" \
"v_mfma_f32_16x16x32_bf16 a[64:67], a[228:231], v[48:51], a[64:67]\n" \
"s_mul_i32 s17, s16, 0x2400\n" \
"s_mul_i32 s16, s16, 0x9000\n" \
"v_mfma_f32_16x16x32_bf16 a[80:83], a[172:175], v[248:251], a[80:83]\n" \
"s_bitset1_b32 s17, 17\n" \
"v_writelane_b32 v11, s17, 19\n" \
"v_mfma_f32_16x16x32_bf16 a[80:83], a[236:239], v[48:51], a[80:83]\n" \
"s_add_i32 s16, s11, s16\n" \
"v_writelane_b32 v11, s16, 25\n" \
"v_mfma_f32_16x16x32_bf16 a[68:71], v[248:251], a[168:171], a[68:71]\n" \
"s_mul_i32 s16, s3, 0x2400\n" \
"s_add_i32 s17, s16, 0x6000\n" \
"v_mfma_f32_16x16x32_bf16 a[68:71], v[48:51], a[232:235], a[68:71]\n" \
"s_waitcnt vmcnt(4) lgkmcnt(0)\n" \
"s_barrier\n" \
"v_mfma_f32_16x16x32_bf16 v[52:55], a[176:179], a[128:131], v[52:55]\n" \
"s_add_u32 m0, s40, 0x0\n" \
"s_add_u32 s85, s39, 0x8000\n" \
"v_mfma_f32_16x16x32_bf16 v[52:55], a[240:243], a[192:195], v[52:55]\n" \
"buffer_load_dwordx4 v20, s[80:83], s85 offen sc1 lds\n" \
"v_mfma_f32_16x16x32_bf16 v[56:59], a[180:183], a[128:131], v[56:59]\n" \
"ds_read_b64_tr_b16 a[160:161], v32 offset:32768\n" \
"v_writelane_b32 v11, s17, 20\n" \
"v_mfma_f32_16x16x32_bf16 v[56:59], a[244:247], a[192:195], v[56:59]\n" \
"ds_read_b64_tr_b16 a[162:163], v32 offset:33280\n" \
"s_or_b32 s17, s16, 0x10000\n" \
"v_mfma_f32_16x16x32_bf16 v[60:63], a[184:187], a[128:131], v[60:63]\n" \
"s_add_u32 m0, s40, 0x1000\n" \
"s_add_u32 s85, s39, 0x9000\n" \
"v_mfma_f32_16x16x32_bf16 v[60:63], a[248:251], a[192:195], v[60:63]\n" \
"buffer_load_dwordx4 v20, s[80:83], s85 offen sc1 lds\n" \
"v_mfma_f32_16x16x32_bf16 v[64:67], a[188:191], a[128:131], v[64:67]\n" \
".fill (L_pb + 24288 - .) / 4, 4, 0xBF800000\n" \
"L_pp6:\n" \
"s_cbranch_execz L_pret\n" \
"ds_read_b64_tr_b16 a[224:225], v32 offset:36864\n" \
"s_add_i32 s18, s16, 0x19000\n" \
"v_mfma_f32_16x16x32_bf16 v[64:67], a[252:255], a[192:195], v[64:67]\n" \
"ds_read_b128 a[128:131], v28 offset:32768\n" \
"v_writelane_b32 v11, s17, 21\n" \
"v_mfma_f32_16x16x32_bf16 v[80:83], a[188:191], a[132:135], v[80:83]\n" \
"ds_read_b128 a[192:195], v29 offset:32768\n" \
"s_mov_b32 s17, 0\n" \
"v_mfma_f32_16x16x32_bf16 v[80:83], a[252:255], a[196:199], v[80:83]\n" \
"s_add_u32 m0, s40, 0x3000\n" \
"s_movk_i32 s84, 0x200\n" \
"v_mfma_f32_16x16x32_bf16 v[76:79], a[184:187], a[132:135], v[76:79]\n" \
"buffer_load_dwordx4 v21, s[76:79], s84 offen offset:0 lds\n" \
"v_mfma_f32_16x16x32_bf16 v[76:79], a[248:251], a[196:199], v[76:79]\n" \
"ds_read_b64_tr_b16 a[226:227], v32 offset:37376\n" \
"v_mfma_f32_16x16x32_bf16 v[72:75], a[180:183], a[132:135], v[72:75]\n" \
"s_add_u32 m0, s30, 0x0\n" \
"s_movk_i32 s84, 0x200\n" \
"v_mfma_f32_16x16x32_bf16 v[72:75], a[244:247], a[196:199], v[72:75]\n" \
"buffer_load_dwordx4 v24, s[76:79], s84 offen offset:0 lds\n" \
"v_mfma_f32_16x16x32_bf16 v[68:71], a[176:179], a[132:135], v[68:71]\n" \
"ds_read_b64_tr_b16 a[164:165], v33 offset:32768\n" \
"v_mfma_f32_16x16x32_bf16 v[68:71], a[240:243], a[196:199], v[68:71]\n" \
"ds_read_b128 a[132:135], v28 offset:34816\n" \
"v_mfma_f32_16x16x32_bf16 v[84:87], a[176:179], a[136:139], v[84:87]\n" \
"ds_read_b128 a[196:199], v29 offset:34816\n" \
"v_mfma_f32_16x16x32_bf16 v[84:87], a[240:243], a[200:203], v[84:87]\n" \
"s_movk_i32 s84, 0x2200\n" \
"v_mfma_f32_16x16x32_bf16 v[88:91], a[180:183], a[136:139], v[88:91]\n" \
"buffer_load_dwordx4 v25, s[76:79], s84 offen offset:1024 lds\n" \
"v_mfma_f32_16x16x32_bf16 v[88:91], a[244:247], a[200:203], v[88:91]\n" \
"ds_read_b64_tr_b16 a[166:167], v33 offset:33280\n" \
"v_mfma_f32_16x16x32_bf16 v[92:95], a[184:187], a[136:139], v[92:95]\n" \
"ds_read_b64_tr_b16 a[228:229], v33 offset:36864\n" \
"v_mfma_f32_16x16x32_bf16 v[92:95], a[248:251], a[200:203], v[92:95]\n" \
"s_movk_i32 s84, 0x4200\n" \
"v_mfma_f32_16x16x32_bf16 v[96:99], a[188:191], a[136:139], v[96:99]\n" \
"buffer_load_dwordx4 v24, s[76:79], s84 offen offset:2048 lds\n" \
"v_mfma_f32_16x16x32_bf16 v[96:99], a[252:255], a[200:203], v[96:99]\n" \
"ds_read_b128 a[136:139], v28 offset:36864\n" \
"v_mfma_f32_16x16x32_bf16 v[112:115], a[188:191], a[140:143], v[112:115]\n" \
"ds_read_b128 a[200:203], v29 offset:36864\n" \
"v_mfma_f32_16x16x32_bf16 v[112:115], a[252:255], a[204:207], v[112:115]\n" \
"s_movk_i32 s84, 0x6200\n" \
"v_mfma_f32_16x16x32_bf16 v[108:111], a[184:187], a[140:143], v[108:111]\n" \
"buffer_load_dwordx4 v25, s[76:79], s84 offen offset:3072 lds\n" \
"v_mfma_f32_16x16x32_bf16 v[108:111], a[248:251], a[204:207], v[108:111]\n" \
"ds_read_b64_tr_b16 a[230:231], v33 offset:37376\n" \
"v_mfma_f32_16x16x32_bf16 v[104:107], a[180:183], a[140:143], v[104:107]\n" \
"ds_read_b64_tr_b16 a[172:173], v35 offset:32768\n" \
"v_mfma_f32_16x16x32_bf16 v[104:107], a[244:247], a[204:207], v[104:107]\n" \
"ds_read_b64_tr_b16 a[174:175], v35 offset:33280\n" \
"v_mfma_f32_16x16x32_bf16 v[100:103], a[176:179], a[140:143], v[100:103]\n" \
"ds_read_b64_tr_b16 a[236:237], v35 offset:36864\n" \
"v_mfma_f32_16x16x32_bf16 v[100:103], a[240:243], a[204:207], v[100:103]\n" \
"ds_read_b128 a[140:143], v28 offset:38912\n" \
"v_mfma_f32_16x16x32_bf16 v[116:119], a[176:179], a[144:147], v[116:119]\n" \
"ds_read_b128 a[204:207], v29 offset:38912\n" \
"v_mfma_f32_16x16x32_bf16 v[116:119], a[240:243], a[208:211], v[116:119]\n" \
"s_add_u32 m0, s31, 0x0\n" \
"s_mov_b32 s84, 0x200000\n" \
"v_mfma_f32_16x16x32_bf16 v[120:123], a[180:183], a[144:147], v[120:123]\n" \
"buffer_load_dwordx4 v26, s[68:71], s84 offen offset:0 lds\n" \
"v_mfma_f32_16x16x32_bf16 v[120:123], a[244:247], a[208:211], v[120:123]\n" \
"s_addk_i32 s84, 0x7c00\n" \
"v_mfma_f32_16x16x32_bf16 v[124:127], a[184:187], a[144:147], v[124:127]\n" \
"buffer_load_dwordx4 v26, s[68:71], s84 offen offset:1024 lds\n" \
"v_mfma_f32_16x16x32_bf16 v[124:127], a[248:251], a[208:211], v[124:127]\n" \
"ds_read_b64_tr_b16 a[238:239], v35 offset:37376\n" \
"v_mfma_f32_16x16x32_bf16 v[128:131], a[188:191], a[144:147], v[128:131]\n" \
"ds_read_b64_tr_b16 a[168:169], v34 offset:32768\n" \
"v_mfma_f32_16x16x32_bf16 v[128:131], a[252:255], a[208:211], v[128:131]\n" \
"ds_read_b128 a[144:147], v28 offset:40960\n" \
"v_mfma_f32_16x16x32_bf16 v[144:147], a[188:191], a[148:151], v[144:147]\n" \
"ds_read_b128 a[208:211], v29 offset:40960\n" \
"v_mfma_f32_16x16x32_bf16 v[144:147], a[252:255], a[212:215], v[144:147]\n" \
"s_addk_i32 s84, 0x7c00\n" \
"v_mfma_f32_16x16x32_bf16 v[140:143], a[184:187], a[148:151], v[140:143]\n" \
"buffer_load_dwordx4 v27, s[68:71], s84 offen offset:2048 lds\n" \
"v_mfma_f32_16x16x32_bf16 v[140:143], a[248:251], a[212:215], v[140:143]\n" \
"s_addk_i32 s84, 0x7c00\n" \
"v_mfma_f32_16x16x32_bf16 v[136:139], a[180:183], a[148:151], v[136:139]\n" \
"buffer_load_dwordx4 v27, s[68:71], s84 offen offset:3072 lds\n" \
"v_mfma_f32_16x16x32_bf16 v[136:139], a[244:247], a[212:215], v[136:139]\n" \
"ds_read_b64_tr_b16 a[170:171], v34 offset:33280\n" \
"v_mfma_f32_16x16x32_bf16 v[132:135], a[176:179], a[148:151], v[132:135]\n" \
"ds_read_b64_tr_b16 a[232:233], v34 offset:36864\n" \
"v_mfma_f32_16x16x32_bf16 v[132:135], a[240:243], a[212:215], v[132:135]\n" \
"ds_read_b128 a[148:151], v28 offset:43008\n" \
"v_mfma_f32_16x16x32_bf16 v[148:151], a[176:179], a[152:155], v[148:151]\n" \
"ds_read_b128 a[212:215], v29 offset:43008\n" \
"v_mfma_f32_16x16x32_bf16 v[148:151], a[240:243], a[216:219], v[148:151]\n" \
"s_add_u32 m0, s30, 0x1000\n" \
"s_mov_b32 s84, 0x9200\n" \
"v_mfma_f32_16x16x32_bf16 v[152:155], a[180:183], a[152:155], v[152:155]\n" \
"buffer_load_dwordx4 v24, s[76:79], s84 offen offset:0 lds\n" \
"v_mfma_f32_16x16x32_bf16 v[152:155], a[244:247], a[216:219], v[152:155]\n" \
"s_addk_i32 s84, 0x2000\n" \
"s_nop 0\n" \
"v_mfma_f32_16x16x32_bf16 v[156:159], a[184:187], a[152:155], v[156:159]\n" \
"buffer_load_dwordx4 v25, s[76:79], s84 offen offset:1024 lds\n" \
"v_mfma_f32_16x16x32_bf16 v[156:159], a[248:251], a[216:219], v[156:159]\n" \
"ds_read_b64_tr_b16 a[234:235], v34 offset:37376\n" \
"v_mfma_f32_16x16x32_bf16 v[160:163], a[188:191], a[152:155], v[160:163]\n" \
"ds_read_b128 v[248:251], v22 offset:32768\n" \
"v_mfma_f32_16x16x32_bf16 v[160:163], a[252:255], a[216:219], v[160:163]\n" \
"ds_read_b128 a[152:155], v28 offset:45056\n" \
"v_mfma_f32_16x16x32_bf16 v[176:179], a[188:191], a[156:159], v[176:179]\n" \
"ds_read_b128 a[216:219], v29 offset:45056\n" \
"v_mfma_f32_16x16x32_bf16 v[176:179], a[252:255], a[220:223], v[176:179]\n" \
"ds_read_b128 v[48:51], v23 offset:32768\n" \
"v_mfma_f32_16x16x32_bf16 v[172:175], a[184:187], a[156:159], v[172:175]\n" \
"s_cmpk_lt_u32 s3, 3\n" \
"s_cselect_b32 s18, s18, s17\n" \
"v_mfma_f32_16x16x32_bf16 v[172:175], a[248:251], a[220:223], v[172:175]\n" \
"s_bitset1_b32 s16, 17\n" \
"v_writelane_b32 v11, s18, 22\n" \
"v_mfma_f32_16x16x32_bf16 v[168:171], a[180:183], a[156:159], v[168:171]\n" \
"s_movk_i32 s18, 0x2400\n" \
"v_mfma_f32_16x16x32_bf16 v[168:171], a[244:247], a[220:223], v[168:171]\n" \
"s_cmpk_lt_u32 s3, 3\n" \
"s_cselect_b32 s16, s16, s18\n" \
"v_mfma_f32_16x16x32_bf16 v[164:167], a[176:179], a[156:159], v[164:167]\n" \
"s_xor_b32 s13, s13, 2\n" \
"v_writelane_b32 v11, s16, 23\n" \
"v_mfma_f32_16x16x32_bf16 v[164:167], a[240:243], a[220:223], v[164:167]\n" \
"ds_read_b128 a[156:159], v28 offset:47104\n" \
"ds_read_b128 a[220:223], v29 offset:47104\n" \
"s_waitcnt lgkmcnt(15)\n" \
"v_mfma_f32_16x16x32_bf16 a[0:3], a[160:163], a[128:131], a[0:3]\n" \
"s_addk_i32 s84, 0x2000\n" \
"v_mfma_f32_16x16x32_bf16 a[0:3], a[224:227], a[192:195], a[0:3]\n" \
"buffer_load_dwordx4 v24, s[76:79], s84 offen offset:2048 lds\n" \
"v_mfma_f32_16x16x32_bf16 a[4:7], a[164:167], a[128:131], a[4:7]\n" \
"ds_read_b64_tr_b16 a[176:177], v36 offset:32768\n" \
"s_lshl_b32 s16, s3, 10\n" \
"v_mfma_f32_16x16x32_bf16 a[4:7], a[228:231], a[192:195], a[4:7]\n" \
"s_addk_i32 s84, 0x2000\n" \
"v_mfma_f32_16x16x32_bf16 a[12:15], a[172:175], a[128:131], a[12:15]\n" \
"buffer_load_dwordx4 v25, s[76:79], s84 offen offset:3072 lds\n" \
"s_waitcnt lgkmcnt(15)\n" \
"v_mfma_f32_16x16x32_bf16 a[12:15], a[236:239], a[192:195], a[12:15]\n" \
"ds_read_b64_tr_b16 a[178:179], v36 offset:33792\n" \
"s_or_b32 s16, s16, 0xf000\n" \
"s_waitcnt lgkmcnt(12)\n" \
"v_mfma_f32_16x16x32_bf16 a[8:11], a[128:131], a[168:171], a[8:11]\n" \
"ds_read_b64_tr_b16 a[240:241], v36 offset:40960\n" \
"v_writelane_b32 v11, s16, 24\n" \
"s_waitcnt lgkmcnt(9)\n" \
"v_mfma_f32_16x16x32_bf16 a[8:11], a[192:195], a[232:235], a[8:11]\n" \
"ds_read_b64_tr_b16 a[242:243], v36 offset:41984\n" \
"s_add_i32 s16, s15, 1\n" \
"v_mfma_f32_16x16x32_bf16 a[24:27], a[132:135], a[168:171], a[24:27]\n" \
"ds_read_b64_tr_b16 a[180:181], v37 offset:32768\n" \
"s_and_b32 s16, s16, 3\n" \
"v_mfma_f32_16x16x32_bf16 a[24:27], a[196:199], a[232:235], a[24:27]\n" \
"ds_read_b64_tr_b16 a[182:183], v37 offset:33792\n" \
"s_or_b32 s16, s16, s5\n" \
"v_mfma_f32_16x16x32_bf16 a[28:31], a[172:175], a[132:135], a[28:31]\n" \
"ds_read_b64_tr_b16 a[244:245], v37 offset:40960\n" \
"s_mul_i32 s18, s16, 0x24000\n" \
"v_mfma_f32_16x16x32_bf16 a[28:31], a[236:239], a[196:199], a[28:31]\n" \
"s_add_u32 s89, s42, 0x0\n" \
"buffer_store_dwordx4 v[216:219], v20, s[80:83], s89 offen offset:0 sc1\n" \
"v_mfma_f32_16x16x32_bf16 a[20:23], a[164:167], a[132:135], a[20:23]\n" \
"ds_read_b64_tr_b16 a[246:247], v37 offset:41984\n" \
"s_add_i32 s18, s18, s19\n" \
"v_mfma_f32_16x16x32_bf16 a[20:23], a[228:231], a[196:199], a[20:23]\n" \
"ds_read_b64_tr_b16 a[184:185], v38 offset:32768\n" \
"s_add_i32 s15, s15, -1\n" \
"v_mfma_f32_16x16x32_bf16 a[16:19], a[160:163], a[132:135], a[16:19]\n" \
"ds_read_b64_tr_b16 a[186:187], v38 offset:33792\n" \
"v_writelane_b32 v11, s18, 26\n" \
"v_mfma_f32_16x16x32_bf16 a[16:19], a[224:227], a[196:199], a[16:19]\n" \
"s_mul_i32 s18, s13, 0x24000\n" \
"s_and_b32 s15, s15, 3\n" \
"v_mfma_f32_16x16x32_bf16 a[32:35], a[160:163], a[136:139], a[32:35]\n" \
"s_add_u32 s88, s42, 0x1000\n" \
"buffer_store_dwordx4 v[220:223], v20, s[80:83], s88 offen offset:0 sc1\n" \
"v_mfma_f32_16x16x32_bf16 a[32:35], a[224:227], a[200:203], a[32:35]\n" \
"ds_read_b64_tr_b16 a[248:249], v38 offset:40960\n" \
"s_add_i32 s18, s18, s19\n" \
"v_mfma_f32_16x16x32_bf16 a[36:39], a[164:167], a[136:139], a[36:39]\n" \
"ds_read_b64_tr_b16 a[250:251], v38 offset:41984\n" \
"s_or_b32 s15, s15, s5\n" \
"v_mfma_f32_16x16x32_bf16 a[36:39], a[228:231], a[200:203], a[36:39]\n" \
"ds_read_b64_tr_b16 a[188:189], v39 offset:32768\n" \
"v_writelane_b32 v11, s18, 27\n" \
"v_mfma_f32_16x16x32_bf16 a[44:47], a[172:175], a[136:139], a[44:47]\n" \
"s_mul_i32 s18, s15, 0x24000\n" \
"s_add_i32 s18, s18, s19\n" \
"v_mfma_f32_16x16x32_bf16 a[44:47], a[236:239], a[200:203], a[44:47]\n" \
"ds_read_b64_tr_b16 a[190:191], v39 offset:33792\n" \
"v_writelane_b32 v11, s18, 28\n" \
"v_mfma_f32_16x16x32_bf16 a[40:43], a[136:139], a[168:171], a[40:43]\n" \
"s_add_u32 s89, s42, 0x0\n" \
"buffer_store_dwordx4 v[192:195], v20, s[80:83], s89 offen offset:2048 sc1\n" \
"v_mfma_f32_16x16x32_bf16 a[40:43], a[200:203], a[232:235], a[40:43]\n" \
"ds_read_b64_tr_b16 a[252:253], v39 offset:40960\n" \
"s_or_b32 s18, s9, s14\n" \
"v_mfma_f32_16x16x32_bf16 a[56:59], a[140:143], a[168:171], a[56:59]\n" \
"ds_read_b64_tr_b16 a[254:255], v39 offset:41984\n" \
"s_mul_i32 s18, s18, 0x24800\n" \
"v_mfma_f32_16x16x32_bf16 a[56:59], a[204:207], a[232:235], a[56:59]\n" \
"s_lshl_b32 s16, s16, 12\n" \
"s_add_i32 s19, s18, 0x3004800\n" \
"v_mfma_f32_16x16x32_bf16 a[60:63], a[172:175], a[140:143], a[60:63]\n" \
"s_add_i32 s20, s19, s16\n" \
"v_writelane_b32 v11, s20, 29\n" \
"v_mfma_f32_16x16x32_bf16 a[60:63], a[236:239], a[204:207], a[60:63]\n" \
"s_add_u32 s88, s42, 0x0\n" \
"buffer_store_dwordx4 v[196:199], v20, s[80:83], s88 offen offset:3072 sc1\n" \
"v_mfma_f32_16x16x32_bf16 a[52:55], a[164:167], a[140:143], a[52:55]\n" \
"s_lshl_b32 s13, s13, 12\n" \
"s_lshl_b32 s15, s15, 12\n" \
"v_mfma_f32_16x16x32_bf16 a[52:55], a[228:231], a[204:207], a[52:55]\n" \
"s_sub_i32 s14, s9, s14\n" \
"s_add_i32 s20, s19, s13\n" \
"v_mfma_f32_16x16x32_bf16 a[48:51], a[160:163], a[140:143], a[48:51]\n" \
"v_writelane_b32 v11, s20, 30\n" \
"s_add_i32 s19, s19, s15\n" \
"v_mfma_f32_16x16x32_bf16 a[48:51], a[224:227], a[204:207], a[48:51]\n" \
"s_mul_i32 s14, s14, 0x24800\n" \
"v_writelane_b32 v11, s19, 31\n" \
"s_waitcnt lgkmcnt(15)\n" \
"v_mfma_f32_16x16x32_bf16 a[64:67], a[164:167], v[248:251], a[64:67]\n" \
"s_add_i32 s19, s14, 0x4120800\n" \
"s_add_i32 s16, s19, s16\n" \
"v_mfma_f32_16x16x32_bf16 a[64:67], a[228:231], v[48:51], a[64:67]\n" \
"v_writelane_b32 v11, s16, 32\n" \
"s_add_i32 s13, s19, s13\n" \
"v_mfma_f32_16x16x32_bf16 a[80:83], a[172:175], v[248:251], a[80:83]\n" \
"v_writelane_b32 v11, s13, 33\n" \
"s_add_i32 s19, s19, s15\n" \
"v_mfma_f32_16x16x32_bf16 a[80:83], a[236:239], v[48:51], a[80:83]\n" \
"s_or_b32 s13, s18, 0x3000000\n" \
"s_lshl_b32 s15, s6, 11\n" \
"v_mfma_f32_16x16x32_bf16 a[68:71], v[248:251], a[168:171], a[68:71]\n" \
"v_writelane_b32 v11, s19, 34\n" \
"s_add_i32 s16, s13, s15\n" \
"v_mfma_f32_16x16x32_bf16 a[68:71], v[48:51], a[232:235], a[68:71]\n" \
"s_waitcnt vmcnt(4) lgkmcnt(0)\n" \
"s_barrier\n" \
"v_mov_b32_e32 v45, s62\n" \
"s_mov_b64 exec, 1\n" \
"buffer_store_dword v45, off, s[80:83], s37 offset:0 sc1\n" \
"s_mov_b64 exec, -1\n" \
"s_nop 0\n" \
"v_mfma_f32_16x16x32_bf16 v[52:55], a[176:179], a[128:131], v[52:55]\n" \
"s_add_u32 m0, s40, 0x8000\n" \
"s_add_u32 s85, s39, 0xa000\n" \
"v_mfma_f32_16x16x32_bf16 v[52:55], a[240:243], a[192:195], v[52:55]\n" \
"buffer_load_dwordx4 v20, s[80:83], s85 offen sc1 lds\n" \
"v_mfma_f32_16x16x32_bf16 v[56:59], a[180:183], a[128:131], v[56:59]\n" \
"ds_read_b64_tr_b16 a[160:161], v32 offset:0\n" \
"v_accvgpr_read_b32 v220, a120\n" \
"v_mfma_f32_16x16x32_bf16 v[56:59], a[244:247], a[192:195], v[56:59]\n" \
"ds_read_b64_tr_b16 a[162:163], v32 offset:512\n" \
"v_accvgpr_read_b32 v221, a121\n" \
"v_mfma_f32_16x16x32_bf16 v[60:63], a[184:187], a[128:131], v[60:63]\n" \
"s_add_u32 m0, s40, 0x9000\n" \
"s_add_u32 s85, s39, 0xb000\n" \
"v_mfma_f32_16x16x32_bf16 v[60:63], a[248:251], a[192:195], v[60:63]\n" \
"buffer_load_dwordx4 v20, s[80:83], s85 offen sc1 lds\n" \
"v_mfma_f32_16x16x32_bf16 v[64:67], a[188:191], a[128:131], v[64:67]\n" \
"ds_read_b64_tr_b16 a[224:225], v32 offset:4096\n" \
"v_accvgpr_read_b32 v222, a122\n" \
"v_mfma_f32_16x16x32_bf16 v[64:67], a[252:255], a[192:195], v[64:67]\n" \
"ds_read_b128 a[128:131], v28 offset:0\n" \
"v_accvgpr_read_b32 v223, a123\n" \
"v_mfma_f32_16x16x32_bf16 v[80:83], a[188:191], a[132:135], v[80:83]\n" \
"ds_read_b128 a[192:195], v29 offset:0\n" \
"v_accvgpr_read_b32 v180, a124\n" \
"v_mfma_f32_16x16x32_bf16 v[80:83], a[252:255], a[196:199], v[80:83]\n" \
"s_add_u32 m0, s40, 0xb000\n" \
"s_movk_i32 s84, 0x280\n" \
"v_mfma_f32_16x16x32_bf16 v[76:79], a[184:187], a[132:135], v[76:79]\n" \
"buffer_load_dwordx4 v21, s[76:79], s84 offen offset:0 lds\n" \
"v_mfma_f32_16x16x32_bf16 v[76:79], a[248:251], a[196:199], v[76:79]\n" \
"ds_read_b64_tr_b16 a[226:227], v32 offset:4608\n" \
"v_accvgpr_read_b32 v181, a125\n" \
"v_mfma_f32_16x16x32_bf16 v[72:75], a[180:183], a[132:135], v[72:75]\n" \
"s_add_u32 m0, s30, 0x8000\n" \
"s_movk_i32 s84, 0x280\n" \
"v_mfma_f32_16x16x32_bf16 v[72:75], a[244:247], a[196:199], v[72:75]\n" \
"buffer_load_dwordx4 v24, s[76:79], s84 offen offset:0 lds\n" \
"v_mfma_f32_16x16x32_bf16 v[68:71], a[176:179], a[132:135], v[68:71]\n" \
"ds_read_b64_tr_b16 a[164:165], v33 offset:0\n" \
"v_accvgpr_read_b32 v182, a126\n" \
"v_mfma_f32_16x16x32_bf16 v[68:71], a[240:243], a[196:199], v[68:71]\n" \
"ds_read_b128 a[132:135], v28 offset:2048\n" \
"v_accvgpr_read_b32 v183, a127\n" \
"v_mfma_f32_16x16x32_bf16 v[84:87], a[176:179], a[136:139], v[84:87]\n" \
"ds_read_b128 a[196:199], v29 offset:2048\n" \
"v_cvt_pk_bf16_f32 v220, v220, v221\n" \
"v_mfma_f32_16x16x32_bf16 v[84:87], a[240:243], a[200:203], v[84:87]\n" \
"s_movk_i32 s84, 0x2280\n" \
"v_mfma_f32_16x16x32_bf16 v[88:91], a[180:183], a[136:139], v[88:91]\n" \
"buffer_load_dwordx4 v25, s[76:79], s84 offen offset:1024 lds\n" \
"v_mfma_f32_16x16x32_bf16 v[88:91], a[244:247], a[200:203], v[88:91]\n" \
"ds_read_b64_tr_b16 a[166:167], v33 offset:512\n" \
"v_cvt_pk_bf16_f32 v221, v222, v223\n" \
"v_mfma_f32_16x16x32_bf16 v[92:95], a[184:187], a[136:139], v[92:95]\n" \
"ds_read_b64_tr_b16 a[228:229], v33 offset:4096\n" \
"v_cvt_pk_bf16_f32 v222, v180, v181\n" \
"v_mfma_f32_16x16x32_bf16 v[92:95], a[248:251], a[200:203], v[92:95]\n" \
"s_movk_i32 s84, 0x4280\n" \
"v_mfma_f32_16x16x32_bf16 v[96:99], a[188:191], a[136:139], v[96:99]\n" \
"buffer_load_dwordx4 v24, s[76:79], s84 offen offset:2048 lds\n" \
"v_mfma_f32_16x16x32_bf16 v[96:99], a[252:255], a[200:203], v[96:99]\n" \
"ds_read_b128 a[136:139], v28 offset:4096\n" \
"v_cvt_pk_bf16_f32 v223, v182, v183\n" \
"v_mfma_f32_16x16x32_bf16 v[112:115], a[188:191], a[140:143], v[112:115]\n" \
"ds_read_b128 a[200:203], v29 offset:4096\n" \
"v_accvgpr_read_b32 v216, a112\n" \
"v_mfma_f32_16x16x32_bf16 v[112:115], a[252:255], a[204:207], v[112:115]\n" \
"s_movk_i32 s84, 0x6280\n" \
"s_nop 0\n" \
"v_mfma_f32_16x16x32_bf16 v[108:111], a[184:187], a[140:143], v[108:111]\n" \
"buffer_load_dwordx4 v25, s[76:79], s84 offen offset:3072 lds\n" \
"v_mfma_f32_16x16x32_bf16 v[108:111], a[248:251], a[204:207], v[108:111]\n" \
"ds_read_b64_tr_b16 a[230:231], v33 offset:4608\n" \
"v_accvgpr_read_b32 v217, a113\n" \
"v_mfma_f32_16x16x32_bf16 v[104:107], a[180:183], a[140:143], v[104:107]\n" \
"ds_read_b64_tr_b16 a[172:173], v35 offset:0\n" \
"v_accvgpr_read_b32 v218, a114\n" \
"v_mfma_f32_16x16x32_bf16 v[104:107], a[244:247], a[204:207], v[104:107]\n" \
"ds_read_b64_tr_b16 a[174:175], v35 offset:512\n" \
"v_accvgpr_read_b32 v219, a115\n" \
"v_mfma_f32_16x16x32_bf16 v[100:103], a[176:179], a[140:143], v[100:103]\n" \
"ds_read_b64_tr_b16 a[236:237], v35 offset:4096\n" \
"v_accvgpr_read_b32 v180, a116\n" \
"v_mfma_f32_16x16x32_bf16 v[100:103], a[240:243], a[204:207], v[100:103]\n" \
"ds_read_b128 a[140:143], v28 offset:6144\n" \
"v_accvgpr_read_b32 v181, a117\n" \
"v_mfma_f32_16x16x32_bf16 v[116:119], a[176:179], a[144:147], v[116:119]\n" \
"ds_read_b128 a[204:207], v29 offset:6144\n" \
"v_accvgpr_read_b32 v182, a118\n" \
"v_mfma_f32_16x16x32_bf16 v[116:119], a[240:243], a[208:211], v[116:119]\n" \
"s_add_u32 m0, s31, 0x8000\n" \
"s_mov_b32 s84, 0x280000\n" \
"v_mfma_f32_16x16x32_bf16 v[120:123], a[180:183], a[144:147], v[120:123]\n" \
"buffer_load_dwordx4 v26, s[68:71], s84 offen offset:0 lds\n" \
"v_mfma_f32_16x16x32_bf16 v[120:123], a[244:247], a[208:211], v[120:123]\n" \
"s_addk_i32 s84, 0x7c00\n" \
"s_nop 0\n" \
"v_mfma_f32_16x16x32_bf16 v[124:127], a[184:187], a[144:147], v[124:127]\n" \
"buffer_load_dwordx4 v26, s[68:71], s84 offen offset:1024 lds\n" \
"v_mfma_f32_16x16x32_bf16 v[124:127], a[248:251], a[208:211], v[124:127]\n" \
"ds_read_b64_tr_b16 a[238:239], v35 offset:4608\n" \
"v_accvgpr_read_b32 v183, a119\n" \
"v_mfma_f32_16x16x32_bf16 v[128:131], a[188:191], a[144:147], v[128:131]\n" \
"ds_read_b64_tr_b16 a[168:169], v34 offset:0\n" \
"v_cvt_pk_bf16_f32 v216, v216, v217\n" \
"v_mfma_f32_16x16x32_bf16 v[128:131], a[252:255], a[208:211], v[128:131]\n" \
"ds_read_b128 a[144:147], v28 offset:8192\n" \
"v_cvt_pk_bf16_f32 v217, v218, v219\n" \
"v_mfma_f32_16x16x32_bf16 v[144:147], a[188:191], a[148:151], v[144:147]\n" \
"ds_read_b128 a[208:211], v29 offset:8192\n" \
"v_cvt_pk_bf16_f32 v218, v180, v181\n" \
"v_mfma_f32_16x16x32_bf16 v[144:147], a[252:255], a[212:215], v[144:147]\n" \
"s_addk_i32 s84, 0x7c00\n" \
"v_mfma_f32_16x16x32_bf16 v[140:143], a[184:187], a[148:151], v[140:143]\n" \
"buffer_load_dwordx4 v27, s[68:71], s84 offen offset:2048 lds\n" \
"v_mfma_f32_16x16x32_bf16 v[140:143], a[248:251], a[212:215], v[140:143]\n" \
"s_addk_i32 s84, 0x7c00\n" \
"v_mfma_f32_16x16x32_bf16 v[136:139], a[180:183], a[148:151], v[136:139]\n" \
"buffer_load_dwordx4 v27, s[68:71], s84 offen offset:3072 lds\n" \
"v_mfma_f32_16x16x32_bf16 v[136:139], a[244:247], a[212:215], v[136:139]\n" \
"ds_read_b64_tr_b16 a[170:171], v34 offset:512\n" \
"v_cvt_pk_bf16_f32 v219, v182, v183\n" \
"v_mfma_f32_16x16x32_bf16 v[132:135], a[176:179], a[148:151], v[132:135]\n" \
"ds_read_b64_tr_b16 a[232:233], v34 offset:4096\n" \
"v_accvgpr_read_b32 v196, a104\n" \
"v_mfma_f32_16x16x32_bf16 v[132:135], a[240:243], a[212:215], v[132:135]\n" \
"ds_read_b128 a[148:151], v28 offset:10240\n" \
"v_accvgpr_read_b32 v197, a105\n" \
"v_mfma_f32_16x16x32_bf16 v[148:151], a[176:179], a[152:155], v[148:151]\n" \
"ds_read_b128 a[212:215], v29 offset:10240\n" \
"v_accvgpr_read_b32 v198, a106\n" \
"v_mfma_f32_16x16x32_bf16 v[148:151], a[240:243], a[216:219], v[148:151]\n" \
"s_add_u32 m0, s30, 0x9000\n" \
"s_mov_b32 s84, 0x9280\n" \
"v_mfma_f32_16x16x32_bf16 v[152:155], a[180:183], a[152:155], v[152:155]\n" \
"buffer_load_dwordx4 v24, s[76:79], s84 offen offset:0 lds\n" \
"v_mfma_f32_16x16x32_bf16 v[152:155], a[244:247], a[216:219], v[152:155]\n" \
"s_addk_i32 s84, 0x2000\n" \
"s_nop 0\n" \
"v_mfma_f32_16x16x32_bf16 v[156:159], a[184:187], a[152:155], v[156:159]\n" \
"buffer_load_dwordx4 v25, s[76:79], s84 offen offset:1024 lds\n" \
"v_mfma_f32_16x16x32_bf16 v[156:159], a[248:251], a[216:219], v[156:159]\n" \
"ds_read_b64_tr_b16 a[234:235], v34 offset:4608\n" \
"v_accvgpr_read_b32 v199, a107\n" \
"v_mfma_f32_16x16x32_bf16 v[160:163], a[188:191], a[152:155], v[160:163]\n" \
"ds_read_b128 v[248:251], v22 offset:0\n" \
"v_accvgpr_read_b32 v180, a108\n" \
"v_mfma_f32_16x16x32_bf16 v[160:163], a[252:255], a[216:219], v[160:163]\n" \
"ds_read_b128 a[152:155], v28 offset:12288\n" \
"v_accvgpr_read_b32 v181, a109\n" \
"v_mfma_f32_16x16x32_bf16 v[176:179], a[188:191], a[156:159], v[176:179]\n" \
"ds_read_b128 a[216:219], v29 offset:12288\n" \
"v_accvgpr_read_b32 v182, a110\n" \
"v_mfma_f32_16x16x32_bf16 v[176:179], a[252:255], a[220:223], v[176:179]\n" \
"ds_read_b128 v[48:51], v23 offset:0\n" \
"v_accvgpr_read_b32 v183, a111\n" \
"v_mfma_f32_16x16x32_bf16 v[172:175], a[184:187], a[156:159], v[172:175]\n" \
"v_cvt_pk_bf16_f32 v196, v196, v197\n" \
"v_cvt_pk_bf16_f32 v197, v198, v199\n" \
"v_mfma_f32_16x16x32_bf16 v[172:175], a[248:251], a[220:223], v[172:175]\n" \
"v_cvt_pk_bf16_f32 v198, v180, v181\n" \
"v_cvt_pk_bf16_f32 v199, v182, v183\n" \
"v_mfma_f32_16x16x32_bf16 v[168:171], a[180:183], a[156:159], v[168:171]\n" \
"v_accvgpr_read_b32 v192, a96\n" \
"v_accvgpr_read_b32 v193, a97\n" \
"v_mfma_f32_16x16x32_bf16 v[168:171], a[244:247], a[220:223], v[168:171]\n" \
"v_accvgpr_read_b32 v194, a98\n" \
"v_accvgpr_read_b32 v195, a99\n" \
"v_mfma_f32_16x16x32_bf16 v[164:167], a[176:179], a[156:159], v[164:167]\n" \
"v_accvgpr_read_b32 v180, a100\n" \
"v_accvgpr_read_b32 v181, a101\n" \
"v_mfma_f32_16x16x32_bf16 v[164:167], a[240:243], a[220:223], v[164:167]\n" \
"ds_read_b128 a[156:159], v28 offset:14336\n" \
"ds_read_b128 a[220:223], v29 offset:14336\n" \
"s_waitcnt lgkmcnt(15)\n" \
"v_mfma_f32_16x16x32_bf16 a[0:3], a[160:163], a[128:131], a[0:3]\n" \
"s_addk_i32 s84, 0x2000\n" \
"v_mfma_f32_16x16x32_bf16 a[0:3], a[224:227], a[192:195], a[0:3]\n" \
"buffer_load_dwordx4 v24, s[76:79], s84 offen offset:2048 lds\n" \
"v_mfma_f32_16x16x32_bf16 a[4:7], a[164:167], a[128:131], a[4:7]\n" \
"ds_read_b64_tr_b16 a[176:177], v36 offset:0\n" \
"v_accvgpr_read_b32 v182, a102\n" \
"v_mfma_f32_16x16x32_bf16 a[4:7], a[228:231], a[192:195], a[4:7]\n" \
"s_addk_i32 s84, 0x2000\n" \
"v_mfma_f32_16x16x32_bf16 a[12:15], a[172:175], a[128:131], a[12:15]\n" \
"buffer_load_dwordx4 v25, s[76:79], s84 offen offset:3072 lds\n" \
"s_waitcnt lgkmcnt(15)\n" \
"v_mfma_f32_16x16x32_bf16 a[12:15], a[236:239], a[192:195], a[12:15]\n" \
"ds_read_b64_tr_b16 a[178:179], v36 offset:1024\n" \
"v_accvgpr_read_b32 v183, a103\n" \
"s_waitcnt lgkmcnt(12)\n" \
"v_mfma_f32_16x16x32_bf16 a[8:11], a[128:131], a[168:171], a[8:11]\n" \
"ds_read_b64_tr_b16 a[240:241], v36 offset:8192\n" \
"v_cvt_pk_bf16_f32 v192, v192, v193\n" \
"s_waitcnt lgkmcnt(9)\n" \
"v_mfma_f32_16x16x32_bf16 a[8:11], a[192:195], a[232:235], a[8:11]\n" \
"ds_read_b64_tr_b16 a[242:243], v36 offset:9216\n" \
"v_cvt_pk_bf16_f32 v193, v194, v195\n" \
"v_mfma_f32_16x16x32_bf16 a[24:27], a[132:135], a[168:171], a[24:27]\n" \
"ds_read_b64_tr_b16 a[180:181], v37 offset:0\n" \
"v_cvt_pk_bf16_f32 v194, v180, v181\n" \
"v_mfma_f32_16x16x32_bf16 a[24:27], a[196:199], a[232:235], a[24:27]\n" \
"ds_read_b64_tr_b16 a[182:183], v37 offset:1024\n" \
"v_cvt_pk_bf16_f32 v195, v182, v183\n" \
"v_mfma_f32_16x16x32_bf16 a[28:31], a[172:175], a[132:135], a[28:31]\n" \
"ds_read_b64_tr_b16 a[244:245], v37 offset:8192\n" \
"v_accvgpr_read_b32 v188, a88\n" \
"v_mfma_f32_16x16x32_bf16 a[28:31], a[236:239], a[196:199], a[28:31]\n" \
"ds_read_b64_tr_b16 a[246:247], v37 offset:9216\n" \
"v_accvgpr_read_b32 v189, a89\n" \
"v_mfma_f32_16x16x32_bf16 a[20:23], a[164:167], a[132:135], a[20:23]\n" \
"v_accvgpr_read_b32 v190, a90\n" \
"v_accvgpr_read_b32 v191, a91\n" \
"v_mfma_f32_16x16x32_bf16 a[20:23], a[228:231], a[196:199], a[20:23]\n" \
"s_add_u32 s89, s42, 0x1000\n" \
"buffer_store_dwordx4 a[84:87], v20, s[80:83], s89 offen offset:3072 sc1\n" \
"v_mfma_f32_16x16x32_bf16 a[16:19], a[160:163], a[132:135], a[16:19]\n" \
"ds_read_b64_tr_b16 a[184:185], v38 offset:0\n" \
"v_accvgpr_read_b32 v180, a92\n" \
"v_mfma_f32_16x16x32_bf16 a[16:19], a[224:227], a[196:199], a[16:19]\n" \
"ds_read_b64_tr_b16 a[186:187], v38 offset:1024\n" \
"v_accvgpr_read_b32 v181, a93\n" \
"v_mfma_f32_16x16x32_bf16 a[32:35], a[160:163], a[136:139], a[32:35]\n" \
"ds_read_b64_tr_b16 a[248:249], v38 offset:8192\n" \
"v_accvgpr_read_b32 v182, a94\n" \
"v_mfma_f32_16x16x32_bf16 a[32:35], a[224:227], a[200:203], a[32:35]\n" \
"ds_read_b64_tr_b16 a[250:251], v38 offset:9216\n" \
"v_accvgpr_read_b32 v183, a95\n" \
"v_mfma_f32_16x16x32_bf16 a[36:39], a[164:167], a[136:139], a[36:39]\n" \
"v_cvt_pk_bf16_f32 v188, v188, v189\n" \
"v_cvt_pk_bf16_f32 v189, v190, v191\n" \
"v_mfma_f32_16x16x32_bf16 a[36:39], a[228:231], a[200:203], a[36:39]\n" \
"s_add_u32 s88, s42, 0x1000\n" \
"buffer_store_dwordx4 v[252:255], v20, s[80:83], s88 offen offset:2048 sc1\n" \
"v_mfma_f32_16x16x32_bf16 a[44:47], a[172:175], a[136:139], a[44:47]\n" \
"ds_read_b64_tr_b16 a[188:189], v39 offset:0\n" \
"v_cvt_pk_bf16_f32 v190, v180, v181\n" \
"v_mfma_f32_16x16x32_bf16 a[44:47], a[236:239], a[200:203], a[44:47]\n" \
"ds_read_b64_tr_b16 a[190:191], v39 offset:1024\n" \
"v_cvt_pk_bf16_f32 v191, v182, v183\n" \
"v_mfma_f32_16x16x32_bf16 a[40:43], a[136:139], a[168:171], a[40:43]\n" \
"ds_read_b64_tr_b16 a[252:253], v39 offset:8192\n" \
"v_accvgpr_read_b32 v184, a72\n" \
"v_mfma_f32_16x16x32_bf16 a[40:43], a[200:203], a[232:235], a[40:43]\n" \
"v_accvgpr_read_b32 v185, a73\n" \
"v_accvgpr_read_b32 v186, a74\n" \
"v_mfma_f32_16x16x32_bf16 a[56:59], a[140:143], a[168:171], a[56:59]\n" \
"ds_read_b64_tr_b16 a[254:255], v39 offset:9216\n" \
"v_accvgpr_read_b32 v187, a75\n" \
"v_mfma_f32_16x16x32_bf16 a[56:59], a[204:207], a[232:235], a[56:59]\n" \
"v_accvgpr_read_b32 v180, a76\n" \
"v_accvgpr_read_b32 v181, a77\n" \
"v_mfma_f32_16x16x32_bf16 a[60:63], a[172:175], a[140:143], a[60:63]\n" \
"s_add_u32 s89, s42, 0x2000\n" \
"buffer_store_dwordx4 v[200:203], v20, s[80:83], s89 offen offset:2048 sc1\n" \
"v_mfma_f32_16x16x32_bf16 a[60:63], a[236:239], a[204:207], a[60:63]\n" \
"v_accvgpr_read_b32 v182, a78\n" \
"v_accvgpr_read_b32 v183, a79\n" \
"v_mfma_f32_16x16x32_bf16 a[52:55], a[164:167], a[140:143], a[52:55]\n" \
"v_cvt_pk_bf16_f32 v184, v184, v185\n" \
"v_cvt_pk_bf16_f32 v185, v186, v187\n" \
"v_mfma_f32_16x16x32_bf16 a[52:55], a[228:231], a[204:207], a[52:55]\n" \
"v_cvt_pk_bf16_f32 v186, v180, v181\n" \
"v_cvt_pk_bf16_f32 v187, v182, v183\n" \
"v_mfma_f32_16x16x32_bf16 a[48:51], a[160:163], a[140:143], a[48:51]\n" \
"v_writelane_b32 v11, s16, 35\n" \
"s_add_i32 s16, s14, 0x411c000\n" \
"v_mfma_f32_16x16x32_bf16 a[48:51], a[224:227], a[204:207], a[48:51]\n" \
"s_add_i32 s19, s16, s15\n" \
"v_writelane_b32 v11, s19, 36\n" \
"s_waitcnt lgkmcnt(15)\n" \
"v_mfma_f32_16x16x32_bf16 a[64:67], a[164:167], v[248:251], a[64:67]\n" \
"s_add_i32 s18, s18, 0x3004000\n" \
"v_writelane_b32 v11, s18, 37\n" \
"v_mfma_f32_16x16x32_bf16 a[64:67], a[228:231], v[48:51], a[64:67]\n" \
"s_add_i32 s14, s14, 0x4120000\n" \
"v_writelane_b32 v11, s14, 38\n" \
"v_mfma_f32_16x16x32_bf16 a[80:83], a[172:175], v[248:251], a[80:83]\n" \
"s_lshl_b32 s14, s4, 2\n" \
"s_or_b32 s8, s14, s8\n" \
"v_mfma_f32_16x16x32_bf16 a[80:83], a[236:239], v[48:51], a[80:83]\n" \
"s_mul_i32 s8, s8, 0x90000\n" \
"s_lshl_b32 s14, s3, 14\n" \
"v_mfma_f32_16x16x32_bf16 a[68:71], v[248:251], a[168:171], a[68:71]\n" \
"s_add_i32 s8, s8, s14\n" \
"s_lshl_b32 s9, s9, 4\n" \
"v_mfma_f32_16x16x32_bf16 a[68:71], v[48:51], a[232:235], a[68:71]\n" \
"s_waitcnt vmcnt(3) lgkmcnt(0)\n" \
"s_barrier\n" \
"v_mfma_f32_16x16x32_bf16 v[52:55], a[176:179], a[128:131], v[52:55]\n" \
"s_add_u32 m0, s40, 0x0\n" \
"s_add_u32 s85, s39, 0xc000\n" \
"v_mfma_f32_16x16x32_bf16 v[52:55], a[240:243], a[192:195], v[52:55]\n" \
"buffer_load_dwordx4 v20, s[80:83], s85 offen sc1 lds\n" \
"v_mfma_f32_16x16x32_bf16 v[56:59], a[180:183], a[128:131], v[56:59]\n" \
"ds_read_b64_tr_b16 a[160:161], v32 offset:32768\n" \
"s_lshl_b32 s14, s4, 7\n" \
"v_mfma_f32_16x16x32_bf16 v[56:59], a[244:247], a[192:195], v[56:59]\n" \
"ds_read_b64_tr_b16 a[162:163], v32 offset:33280\n" \
"s_or_b32 s18, s9, s14\n" \
"v_mfma_f32_16x16x32_bf16 v[60:63], a[184:187], a[128:131], v[60:63]\n" \
"s_add_u32 m0, s40, 0x1000\n" \
"s_add_u32 s85, s39, 0xd000\n" \
"v_mfma_f32_16x16x32_bf16 v[60:63], a[248:251], a[192:195], v[60:63]\n" \
"buffer_load_dwordx4 v20, s[80:83], s85 offen sc1 lds\n" \
"v_mfma_f32_16x16x32_bf16 v[64:67], a[188:191], a[128:131], v[64:67]\n" \
"ds_read_b64_tr_b16 a[224:225], v32 offset:36864\n" \
".fill (L_pb + 28384 - .) / 4, 4, 0xBF800000\n" \
"L_pp7:\n" \
"s_cbranch_execz L_pret\n" \
"s_sub_i32 s9, s9, s14\n" \
"v_mfma_f32_16x16x32_bf16 v[64:67], a[252:255], a[192:195], v[64:67]\n" \
"ds_read_b128 a[128:131], v28 offset:32768\n" \
"s_mulk_i32 s18, 0x480\n" \
"v_mfma_f32_16x16x32_bf16 v[80:83], a[188:191], a[132:135], v[80:83]\n" \
"ds_read_b128 a[192:195], v29 offset:32768\n" \
"s_mulk_i32 s9, 0x480\n" \
"v_mfma_f32_16x16x32_bf16 v[80:83], a[252:255], a[196:199], v[80:83]\n" \
"s_add_u32 m0, s40, 0x3000\n" \
"s_movk_i32 s84, 0x300\n" \
"v_mfma_f32_16x16x32_bf16 v[76:79], a[184:187], a[132:135], v[76:79]\n" \
"buffer_load_dwordx4 v21, s[76:79], s84 offen offset:0 lds\n" \
"v_mfma_f32_16x16x32_bf16 v[76:79], a[248:251], a[196:199], v[76:79]\n" \
"ds_read_b64_tr_b16 a[226:227], v32 offset:37376\n" \
"s_or_b32 s14, s18, s12\n" \
"v_mfma_f32_16x16x32_bf16 v[72:75], a[180:183], a[132:135], v[72:75]\n" \
"s_add_u32 m0, s30, 0x0\n" \
"s_movk_i32 s84, 0x300\n" \
"v_mfma_f32_16x16x32_bf16 v[72:75], a[244:247], a[196:199], v[72:75]\n" \
"buffer_load_dwordx4 v24, s[76:79], s84 offen offset:0 lds\n" \
"v_mfma_f32_16x16x32_bf16 v[68:71], a[176:179], a[132:135], v[68:71]\n" \
"ds_read_b64_tr_b16 a[164:165], v33 offset:32768\n" \
"s_or_b32 s9, s9, s12\n" \
"v_mfma_f32_16x16x32_bf16 v[68:71], a[240:243], a[196:199], v[68:71]\n" \
"ds_read_b128 a[132:135], v28 offset:34816\n" \
"s_and_b32 s12, s2, 1\n" \
"v_mfma_f32_16x16x32_bf16 v[84:87], a[176:179], a[136:139], v[84:87]\n" \
"ds_read_b128 a[196:199], v29 offset:34816\n" \
"v_mfma_f32_16x16x32_bf16 v[84:87], a[240:243], a[200:203], v[84:87]\n" \
"s_movk_i32 s84, 0x2300\n" \
"v_mfma_f32_16x16x32_bf16 v[88:91], a[180:183], a[136:139], v[88:91]\n" \
"buffer_load_dwordx4 v25, s[76:79], s84 offen offset:1024 lds\n" \
"v_mfma_f32_16x16x32_bf16 v[88:91], a[244:247], a[200:203], v[88:91]\n" \
"ds_read_b64_tr_b16 a[166:167], v33 offset:33280\n" \
"v_mfma_f32_16x16x32_bf16 v[92:95], a[184:187], a[136:139], v[92:95]\n" \
"ds_read_b64_tr_b16 a[228:229], v33 offset:36864\n" \
"v_mfma_f32_16x16x32_bf16 v[92:95], a[248:251], a[200:203], v[92:95]\n" \
"s_movk_i32 s84, 0x4300\n" \
"v_mfma_f32_16x16x32_bf16 v[96:99], a[188:191], a[136:139], v[96:99]\n" \
"buffer_load_dwordx4 v24, s[76:79], s84 offen offset:2048 lds\n" \
"v_mfma_f32_16x16x32_bf16 v[96:99], a[252:255], a[200:203], v[96:99]\n" \
"ds_read_b128 a[136:139], v28 offset:36864\n" \
"v_mfma_f32_16x16x32_bf16 v[112:115], a[188:191], a[140:143], v[112:115]\n" \
"ds_read_b128 a[200:203], v29 offset:36864\n" \
"v_mfma_f32_16x16x32_bf16 v[112:115], a[252:255], a[204:207], v[112:115]\n" \
"s_movk_i32 s84, 0x6300\n" \
"s_nop 0\n" \
"v_mfma_f32_16x16x32_bf16 v[108:111], a[184:187], a[140:143], v[108:111]\n" \
"buffer_load_dwordx4 v25, s[76:79], s84 offen offset:3072 lds\n" \
"v_mfma_f32_16x16x32_bf16 v[108:111], a[248:251], a[204:207], v[108:111]\n" \
"ds_read_b64_tr_b16 a[230:231], v33 offset:37376\n" \
"v_mfma_f32_16x16x32_bf16 v[104:107], a[180:183], a[140:143], v[104:107]\n" \
"ds_read_b64_tr_b16 a[172:173], v35 offset:32768\n" \
"v_mfma_f32_16x16x32_bf16 v[104:107], a[244:247], a[204:207], v[104:107]\n" \
"ds_read_b64_tr_b16 a[174:175], v35 offset:33280\n" \
"v_mfma_f32_16x16x32_bf16 v[100:103], a[176:179], a[140:143], v[100:103]\n" \
"ds_read_b64_tr_b16 a[236:237], v35 offset:36864\n" \
"v_mfma_f32_16x16x32_bf16 v[100:103], a[240:243], a[204:207], v[100:103]\n" \
"ds_read_b128 a[140:143], v28 offset:38912\n" \
"v_mfma_f32_16x16x32_bf16 v[116:119], a[176:179], a[144:147], v[116:119]\n" \
"ds_read_b128 a[204:207], v29 offset:38912\n" \
"v_mfma_f32_16x16x32_bf16 v[116:119], a[240:243], a[208:211], v[116:119]\n" \
"s_add_u32 m0, s31, 0x0\n" \
"s_mov_b32 s84, 0x300000\n" \
"v_mfma_f32_16x16x32_bf16 v[120:123], a[180:183], a[144:147], v[120:123]\n" \
"buffer_load_dwordx4 v26, s[68:71], s84 offen offset:0 lds\n" \
"v_mfma_f32_16x16x32_bf16 v[120:123], a[244:247], a[208:211], v[120:123]\n" \
"s_addk_i32 s84, 0x7c00\n" \
"v_mfma_f32_16x16x32_bf16 v[124:127], a[184:187], a[144:147], v[124:127]\n" \
"buffer_load_dwordx4 v26, s[68:71], s84 offen offset:1024 lds\n" \
"v_mfma_f32_16x16x32_bf16 v[124:127], a[248:251], a[208:211], v[124:127]\n" \
"ds_read_b64_tr_b16 a[238:239], v35 offset:37376\n" \
"v_mfma_f32_16x16x32_bf16 v[128:131], a[188:191], a[144:147], v[128:131]\n" \
"ds_read_b64_tr_b16 a[168:169], v34 offset:32768\n" \
"v_mfma_f32_16x16x32_bf16 v[128:131], a[252:255], a[208:211], v[128:131]\n" \
"ds_read_b128 a[144:147], v28 offset:40960\n" \
"v_mfma_f32_16x16x32_bf16 v[144:147], a[188:191], a[148:151], v[144:147]\n" \
"ds_read_b128 a[208:211], v29 offset:40960\n" \
"v_mfma_f32_16x16x32_bf16 v[144:147], a[252:255], a[212:215], v[144:147]\n" \
"s_addk_i32 s84, 0x7c00\n" \
"v_mfma_f32_16x16x32_bf16 v[140:143], a[184:187], a[148:151], v[140:143]\n" \
"buffer_load_dwordx4 v27, s[68:71], s84 offen offset:2048 lds\n" \
"v_mfma_f32_16x16x32_bf16 v[140:143], a[248:251], a[212:215], v[140:143]\n" \
"s_addk_i32 s84, 0x7c00\n" \
"v_mfma_f32_16x16x32_bf16 v[136:139], a[180:183], a[148:151], v[136:139]\n" \
"buffer_load_dwordx4 v27, s[68:71], s84 offen offset:3072 lds\n" \
"v_mfma_f32_16x16x32_bf16 v[136:139], a[244:247], a[212:215], v[136:139]\n" \
"ds_read_b64_tr_b16 a[170:171], v34 offset:33280\n" \
"v_mfma_f32_16x16x32_bf16 v[132:135], a[176:179], a[148:151], v[132:135]\n" \
"ds_read_b64_tr_b16 a[232:233], v34 offset:36864\n" \
"v_mfma_f32_16x16x32_bf16 v[132:135], a[240:243], a[212:215], v[132:135]\n" \
"ds_read_b128 a[148:151], v28 offset:43008\n" \
"v_mfma_f32_16x16x32_bf16 v[148:151], a[176:179], a[152:155], v[148:151]\n" \
"ds_read_b128 a[212:215], v29 offset:43008\n" \
"v_mfma_f32_16x16x32_bf16 v[148:151], a[240:243], a[216:219], v[148:151]\n" \
"s_add_u32 m0, s30, 0x1000\n" \
"s_mov_b32 s84, 0x9300\n" \
"v_mfma_f32_16x16x32_bf16 v[152:155], a[180:183], a[152:155], v[152:155]\n" \
"buffer_load_dwordx4 v24, s[76:79], s84 offen offset:0 lds\n" \
"v_mfma_f32_16x16x32_bf16 v[152:155], a[244:247], a[216:219], v[152:155]\n" \
"s_addk_i32 s84, 0x2000\n" \
"s_nop 0\n" \
"v_mfma_f32_16x16x32_bf16 v[156:159], a[184:187], a[152:155], v[156:159]\n" \
"buffer_load_dwordx4 v25, s[76:79], s84 offen offset:1024 lds\n" \
"v_mfma_f32_16x16x32_bf16 v[156:159], a[248:251], a[216:219], v[156:159]\n" \
"ds_read_b64_tr_b16 a[234:235], v34 offset:37376\n" \
"v_mfma_f32_16x16x32_bf16 v[160:163], a[188:191], a[152:155], v[160:163]\n" \
"ds_read_b128 v[248:251], v22 offset:32768\n" \
"v_mfma_f32_16x16x32_bf16 v[160:163], a[252:255], a[216:219], v[160:163]\n" \
"ds_read_b128 a[152:155], v28 offset:45056\n" \
"v_mfma_f32_16x16x32_bf16 v[176:179], a[188:191], a[156:159], v[176:179]\n" \
"ds_read_b128 a[216:219], v29 offset:45056\n" \
"v_mfma_f32_16x16x32_bf16 v[176:179], a[252:255], a[220:223], v[176:179]\n" \
"ds_read_b128 v[48:51], v23 offset:32768\n" \
"v_mfma_f32_16x16x32_bf16 v[172:175], a[184:187], a[156:159], v[172:175]\n" \
"s_cmp_lg_u32 s12, 0\n" \
"s_cselect_b32 s13, s16, s13\n" \
"v_mfma_f32_16x16x32_bf16 v[172:175], a[248:251], a[220:223], v[172:175]\n" \
"s_bfe_u32 s16, s2, 0x20001\n" \
"s_addk_i32 s13, 0x4000\n" \
"v_mfma_f32_16x16x32_bf16 v[168:171], a[180:183], a[156:159], v[168:171]\n" \
"s_xor_b32 s16, s16, s3\n" \
"v_mfma_f32_16x16x32_bf16 v[168:171], a[244:247], a[220:223], v[168:171]\n" \
"s_cmp_lg_u32 s16, 0\n" \
"s_cselect_b32 s13, s7, s13\n" \
"v_mfma_f32_16x16x32_bf16 v[164:167], a[176:179], a[156:159], v[164:167]\n" \
"s_lshl_b32 s6, s6, 16\n" \
"v_writelane_b32 v11, s13, 39\n" \
"v_mfma_f32_16x16x32_bf16 v[164:167], a[240:243], a[220:223], v[164:167]\n" \
"ds_read_b128 a[156:159], v28 offset:47104\n" \
"ds_read_b128 a[220:223], v29 offset:47104\n" \
"s_waitcnt lgkmcnt(15)\n" \
"v_mfma_f32_16x16x32_bf16 a[0:3], a[160:163], a[128:131], a[0:3]\n" \
"s_addk_i32 s84, 0x2000\n" \
"v_mfma_f32_16x16x32_bf16 a[0:3], a[224:227], a[192:195], a[0:3]\n" \
"buffer_load_dwordx4 v24, s[76:79], s84 offen offset:2048 lds\n" \
"v_mfma_f32_16x16x32_bf16 a[4:7], a[164:167], a[128:131], a[4:7]\n" \
"ds_read_b64_tr_b16 a[176:177], v36 offset:32768\n" \
"s_add_i32 s13, s8, 0x4240000\n" \
"v_mfma_f32_16x16x32_bf16 a[4:7], a[228:231], a[192:195], a[4:7]\n" \
"s_addk_i32 s84, 0x2000\n" \
"v_mfma_f32_16x16x32_bf16 a[12:15], a[172:175], a[128:131], a[12:15]\n" \
"buffer_load_dwordx4 v25, s[76:79], s84 offen offset:3072 lds\n" \
"s_waitcnt lgkmcnt(15)\n" \
"v_mfma_f32_16x16x32_bf16 a[12:15], a[236:239], a[192:195], a[12:15]\n" \
"ds_read_b64_tr_b16 a[178:179], v36 offset:33792\n" \
"s_add_i32 s6, s13, s6\n" \
"s_waitcnt lgkmcnt(12)\n" \
"v_mfma_f32_16x16x32_bf16 a[8:11], a[128:131], a[168:171], a[8:11]\n" \
"ds_read_b64_tr_b16 a[240:241], v36 offset:40960\n" \
"v_writelane_b32 v11, s6, 40\n" \
"s_waitcnt lgkmcnt(9)\n" \
"v_mfma_f32_16x16x32_bf16 a[8:11], a[192:195], a[232:235], a[8:11]\n" \
"ds_read_b64_tr_b16 a[242:243], v36 offset:41984\n" \
"s_add_i32 s8, s8, 0x42c0000\n" \
"v_mfma_f32_16x16x32_bf16 a[24:27], a[132:135], a[168:171], a[24:27]\n" \
"ds_read_b64_tr_b16 a[180:181], v37 offset:32768\n" \
"s_lshl_b32 s6, s12, 10\n" \
"v_mfma_f32_16x16x32_bf16 a[24:27], a[196:199], a[232:235], a[24:27]\n" \
"ds_read_b64_tr_b16 a[182:183], v37 offset:33792\n" \
"s_or_b32 s6, s8, s6\n" \
"v_mfma_f32_16x16x32_bf16 a[28:31], a[172:175], a[132:135], a[28:31]\n" \
"ds_read_b64_tr_b16 a[244:245], v37 offset:40960\n" \
"v_mfma_f32_16x16x32_bf16 a[28:31], a[236:239], a[196:199], a[28:31]\n" \
"ds_read_b64_tr_b16 a[246:247], v37 offset:41984\n" \
"v_mfma_f32_16x16x32_bf16 a[20:23], a[164:167], a[132:135], a[20:23]\n" \
"s_cmp_lg_u32 s16, 0\n" \
"s_cselect_b32 s6, s7, s6\n" \
"v_mfma_f32_16x16x32_bf16 a[20:23], a[228:231], a[196:199], a[20:23]\n" \
"s_add_u32 s88, s42, 0x2000\n" \
"buffer_store_dwordx4 v[204:207], v20, s[80:83], s88 offen offset:3072 sc1\n" \
"v_mfma_f32_16x16x32_bf16 a[16:19], a[160:163], a[132:135], a[16:19]\n" \
"ds_read_b64_tr_b16 a[184:185], v38 offset:32768\n" \
"s_mulk_i32 s4, 0x4800\n" \
"v_mfma_f32_16x16x32_bf16 a[16:19], a[224:227], a[196:199], a[16:19]\n" \
"ds_read_b64_tr_b16 a[186:187], v38 offset:33792\n" \
"v_writelane_b32 v11, s6, 41\n" \
"v_mfma_f32_16x16x32_bf16 a[32:35], a[160:163], a[136:139], a[32:35]\n" \
"ds_read_b64_tr_b16 a[248:249], v38 offset:40960\n" \
"s_add_i32 s6, s14, s10\n" \
"v_mfma_f32_16x16x32_bf16 a[32:35], a[224:227], a[200:203], a[32:35]\n" \
"ds_read_b64_tr_b16 a[250:251], v38 offset:41984\n" \
"v_writelane_b32 v11, s6, 42\n" \
"v_mfma_f32_16x16x32_bf16 a[36:39], a[164:167], a[136:139], a[36:39]\n" \
"s_add_i32 s6, s9, 0x21c000\n" \
"s_add_i32 s9, s6, s10\n" \
"v_mfma_f32_16x16x32_bf16 a[36:39], a[228:231], a[200:203], a[36:39]\n" \
"s_add_u32 s89, s42, 0x3000\n" \
"buffer_store_dwordx4 v[208:211], v20, s[80:83], s89 offen offset:3072 sc1\n" \
"v_mfma_f32_16x16x32_bf16 a[44:47], a[172:175], a[136:139], a[44:47]\n" \
"ds_read_b64_tr_b16 a[188:189], v39 offset:32768\n" \
"v_mfma_f32_16x16x32_bf16 a[44:47], a[236:239], a[200:203], a[44:47]\n" \
"ds_read_b64_tr_b16 a[190:191], v39 offset:33792\n" \
"v_mfma_f32_16x16x32_bf16 a[40:43], a[136:139], a[168:171], a[40:43]\n" \
"ds_read_b64_tr_b16 a[252:253], v39 offset:40960\n" \
"v_mfma_f32_16x16x32_bf16 a[40:43], a[200:203], a[232:235], a[40:43]\n" \
"s_cmp_lg_u32 s12, 0\n" \
"s_cselect_b32 s6, s6, s14\n" \
"v_mfma_f32_16x16x32_bf16 a[56:59], a[140:143], a[168:171], a[56:59]\n" \
"ds_read_b64_tr_b16 a[254:255], v39 offset:41984\n" \
"s_addk_i32 s6, 0x100\n" \
"v_mfma_f32_16x16x32_bf16 a[56:59], a[204:207], a[232:235], a[56:59]\n" \
"v_writelane_b32 v11, s9, 43\n" \
"v_mfma_f32_16x16x32_bf16 a[60:63], a[172:175], a[140:143], a[60:63]\n" \
"s_add_u32 s88, s42, 0x3000\n" \
"buffer_store_dwordx4 v[212:215], v20, s[80:83], s88 offen offset:2048 sc1\n" \
"v_mfma_f32_16x16x32_bf16 a[60:63], a[236:239], a[204:207], a[60:63]\n" \
"s_cmp_lg_u32 s16, 0\n" \
"s_cselect_b32 s6, s7, s6\n" \
"v_mfma_f32_16x16x32_bf16 a[52:55], a[164:167], a[140:143], a[52:55]\n" \
"v_writelane_b32 v11, s6, 44\n" \
"s_or_b32 s6, s8, s15\n" \
"v_mfma_f32_16x16x32_bf16 a[52:55], a[228:231], a[204:207], a[52:55]\n" \
"v_writelane_b32 v11, s6, 52\n" \
"s_lshl_b32 s6, s3, 17\n" \
"v_mfma_f32_16x16x32_bf16 a[48:51], a[160:163], a[140:143], a[48:51]\n" \
"s_add_i32 s6, s13, s6\n" \
"s_or_b32 s5, s6, s5\n" \
"v_mfma_f32_16x16x32_bf16 a[48:51], a[224:227], a[204:207], a[48:51]\n" \
"s_or_b32 s5, s5, 0x3840\n" \
"v_writelane_b32 v11, s5, 6\n" \
"s_waitcnt lgkmcnt(15)\n" \
"v_mfma_f32_16x16x32_bf16 a[64:67], a[164:167], v[248:251], a[64:67]\n" \
"s_mov_b32 s5, 0xff0000\n" \
"v_mfma_f32_16x16x32_bf16 a[64:67], a[228:231], v[48:51], a[64:67]\n" \
"s_cmp_lg_u32 s16, 0\n" \
"s_cselect_b32 s5, s17, s5\n" \
"v_mfma_f32_16x16x32_bf16 a[80:83], a[172:175], v[248:251], a[80:83]\n" \
"v_writelane_b32 v11, s5, 7\n" \
"s_mul_i32 s5, s3, 0x1200\n" \
"v_mfma_f32_16x16x32_bf16 a[80:83], a[236:239], v[48:51], a[80:83]\n" \
"s_add_i32 s4, s5, s4\n" \
"s_add_i32 s4, s4, s11\n" \
"v_mfma_f32_16x16x32_bf16 a[68:71], v[248:251], a[168:171], a[68:71]\n" \
"v_writelane_b32 v11, s4, 8\n" \
"s_lshl_b32 s4, s2, 16\n" \
"v_mfma_f32_16x16x32_bf16 a[68:71], v[48:51], a[232:235], a[68:71]\n" \
"s_waitcnt vmcnt(3) lgkmcnt(0)\n" \
"s_barrier\n" \
"v_mfma_f32_16x16x32_bf16 v[52:55], a[176:179], a[128:131], v[52:55]\n" \
"s_add_u32 m0, s40, 0x8000\n" \
"s_add_u32 s85, s39, 0xe000\n" \
"v_mfma_f32_16x16x32_bf16 v[52:55], a[240:243], a[192:195], v[52:55]\n" \
"buffer_load_dwordx4 v20, s[80:83], s85 offen sc1 lds\n" \
"v_mfma_f32_16x16x32_bf16 v[56:59], a[180:183], a[128:131], v[56:59]\n" \
"ds_read_b64_tr_b16 a[160:161], v32 offset:0\n" \
"s_add_i32 s5, s4, 0x10000\n" \
"v_mfma_f32_16x16x32_bf16 v[56:59], a[244:247], a[192:195], v[56:59]\n" \
"ds_read_b64_tr_b16 a[162:163], v32 offset:512\n" \
"s_and_b32 s5, s5, 0x70000\n" \
"v_mfma_f32_16x16x32_bf16 v[60:63], a[184:187], a[128:131], v[60:63]\n" \
"s_add_u32 m0, s40, 0x9000\n" \
"s_add_u32 s85, s39, 0xf000\n" \
"v_mfma_f32_16x16x32_bf16 v[60:63], a[248:251], a[192:195], v[60:63]\n" \
"buffer_load_dwordx4 v20, s[80:83], s85 offen sc1 lds\n" \
"v_mfma_f32_16x16x32_bf16 v[64:67], a[188:191], a[128:131], v[64:67]\n" \
"ds_read_b64_tr_b16 a[224:225], v32 offset:4096\n" \
"s_add_i32 s5, s13, s5\n" \
"v_mfma_f32_16x16x32_bf16 v[64:67], a[252:255], a[192:195], v[64:67]\n" \
"ds_read_b128 a[128:131], v28 offset:0\n" \
"v_writelane_b32 v11, s5, 45\n" \
"v_mfma_f32_16x16x32_bf16 v[80:83], a[188:191], a[132:135], v[80:83]\n" \
"ds_read_b128 a[192:195], v29 offset:0\n" \
"s_add_i32 s5, s4, 0x20000\n" \
"v_mfma_f32_16x16x32_bf16 v[80:83], a[252:255], a[196:199], v[80:83]\n" \
"s_add_u32 m0, s40, 0xb000\n" \
"s_movk_i32 s84, 0x380\n" \
"v_mfma_f32_16x16x32_bf16 v[76:79], a[184:187], a[132:135], v[76:79]\n" \
"buffer_load_dwordx4 v21, s[76:79], s84 offen offset:0 lds\n" \
"v_mfma_f32_16x16x32_bf16 v[76:79], a[248:251], a[196:199], v[76:79]\n" \
"ds_read_b64_tr_b16 a[226:227], v32 offset:4608\n" \
"s_and_b32 s5, s5, 0x70000\n" \
"v_mfma_f32_16x16x32_bf16 v[72:75], a[180:183], a[132:135], v[72:75]\n" \
"s_add_u32 m0, s30, 0x8000\n" \
"s_movk_i32 s84, 0x380\n" \
"v_mfma_f32_16x16x32_bf16 v[72:75], a[244:247], a[196:199], v[72:75]\n" \
"buffer_load_dwordx4 v24, s[76:79], s84 offen offset:0 lds\n" \
"v_mfma_f32_16x16x32_bf16 v[68:71], a[176:179], a[132:135], v[68:71]\n" \
"ds_read_b64_tr_b16 a[164:165], v33 offset:0\n" \
"s_add_i32 s5, s13, s5\n" \
"v_mfma_f32_16x16x32_bf16 v[68:71], a[240:243], a[196:199], v[68:71]\n" \
"ds_read_b128 a[132:135], v28 offset:2048\n" \
"s_bitset1_b32 s5, 11\n" \
"v_mfma_f32_16x16x32_bf16 v[84:87], a[176:179], a[136:139], v[84:87]\n" \
"ds_read_b128 a[196:199], v29 offset:2048\n" \
"v_writelane_b32 v11, s5, 46\n" \
"v_mfma_f32_16x16x32_bf16 v[84:87], a[240:243], a[200:203], v[84:87]\n" \
"s_movk_i32 s84, 0x2380\n" \
"v_mfma_f32_16x16x32_bf16 v[88:91], a[180:183], a[136:139], v[88:91]\n" \
"buffer_load_dwordx4 v25, s[76:79], s84 offen offset:1024 lds\n" \
"v_mfma_f32_16x16x32_bf16 v[88:91], a[244:247], a[200:203], v[88:91]\n" \
"ds_read_b64_tr_b16 a[166:167], v33 offset:512\n" \
"s_add_i32 s5, s4, 0x30000\n" \
"v_mfma_f32_16x16x32_bf16 v[92:95], a[184:187], a[136:139], v[92:95]\n" \
"ds_read_b64_tr_b16 a[228:229], v33 offset:4096\n" \
"s_and_b32 s5, s5, 0x70000\n" \
"v_mfma_f32_16x16x32_bf16 v[92:95], a[248:251], a[200:203], v[92:95]\n" \
"s_movk_i32 s84, 0x4380\n" \
"v_mfma_f32_16x16x32_bf16 v[96:99], a[188:191], a[136:139], v[96:99]\n" \
"buffer_load_dwordx4 v24, s[76:79], s84 offen offset:2048 lds\n" \
"v_mfma_f32_16x16x32_bf16 v[96:99], a[252:255], a[200:203], v[96:99]\n" \
"ds_read_b128 a[136:139], v28 offset:4096\n" \
"s_add_i32 s5, s13, s5\n" \
"v_mfma_f32_16x16x32_bf16 v[112:115], a[188:191], a[140:143], v[112:115]\n" \
"ds_read_b128 a[200:203], v29 offset:4096\n" \
"s_bitset1_b32 s5, 12\n" \
"v_mfma_f32_16x16x32_bf16 v[112:115], a[252:255], a[204:207], v[112:115]\n" \
"s_movk_i32 s84, 0x6380\n" \
"v_mfma_f32_16x16x32_bf16 v[108:111], a[184:187], a[140:143], v[108:111]\n" \
"buffer_load_dwordx4 v25, s[76:79], s84 offen offset:3072 lds\n" \
"v_mfma_f32_16x16x32_bf16 v[108:111], a[248:251], a[204:207], v[108:111]\n" \
"ds_read_b64_tr_b16 a[230:231], v33 offset:4608\n" \
"v_writelane_b32 v11, s5, 47\n" \
"v_mfma_f32_16x16x32_bf16 v[104:107], a[180:183], a[140:143], v[104:107]\n" \
"ds_read_b64_tr_b16 a[172:173], v35 offset:0\n" \
"s_and_b32 s5, s4, 0x70000\n" \
"v_mfma_f32_16x16x32_bf16 v[104:107], a[244:247], a[204:207], v[104:107]\n" \
"ds_read_b64_tr_b16 a[174:175], v35 offset:512\n" \
"s_xor_b32 s5, s5, 0x40000\n" \
"v_mfma_f32_16x16x32_bf16 v[100:103], a[176:179], a[140:143], v[100:103]\n" \
"ds_read_b64_tr_b16 a[236:237], v35 offset:4096\n" \
"s_add_i32 s5, s13, s5\n" \
"s_nop 0\n" \
"v_mfma_f32_16x16x32_bf16 v[100:103], a[240:243], a[204:207], v[100:103]\n" \
"ds_read_b128 a[140:143], v28 offset:6144\n" \
"s_or_b32 s5, s5, 0x1800\n" \
"v_mfma_f32_16x16x32_bf16 v[116:119], a[176:179], a[144:147], v[116:119]\n" \
"ds_read_b128 a[204:207], v29 offset:6144\n" \
"v_writelane_b32 v11, s5, 48\n" \
"v_mfma_f32_16x16x32_bf16 v[116:119], a[240:243], a[208:211], v[116:119]\n" \
"s_add_u32 m0, s31, 0x8000\n" \
"s_mov_b32 s84, 0x380000\n" \
"v_mfma_f32_16x16x32_bf16 v[120:123], a[180:183], a[144:147], v[120:123]\n" \
"buffer_load_dwordx4 v26, s[68:71], s84 offen offset:0 lds\n" \
"v_mfma_f32_16x16x32_bf16 v[120:123], a[244:247], a[208:211], v[120:123]\n" \
"s_addk_i32 s84, 0x7c00\n" \
"v_mfma_f32_16x16x32_bf16 v[124:127], a[184:187], a[144:147], v[124:127]\n" \
"buffer_load_dwordx4 v26, s[68:71], s84 offen offset:1024 lds\n" \
"v_mfma_f32_16x16x32_bf16 v[124:127], a[248:251], a[208:211], v[124:127]\n" \
"ds_read_b64_tr_b16 a[238:239], v35 offset:4608\n" \
"s_add_i32 s5, s4, 0x50000\n" \
"v_mfma_f32_16x16x32_bf16 v[128:131], a[188:191], a[144:147], v[128:131]\n" \
"ds_read_b64_tr_b16 a[168:169], v34 offset:0\n" \
"s_and_b32 s5, s5, 0x70000\n" \
"v_mfma_f32_16x16x32_bf16 v[128:131], a[252:255], a[208:211], v[128:131]\n" \
"ds_read_b128 a[144:147], v28 offset:8192\n" \
"s_add_i32 s5, s13, s5\n" \
"v_mfma_f32_16x16x32_bf16 v[144:147], a[188:191], a[148:151], v[144:147]\n" \
"ds_read_b128 a[208:211], v29 offset:8192\n" \
"s_bitset1_b32 s5, 13\n" \
"v_mfma_f32_16x16x32_bf16 v[144:147], a[252:255], a[212:215], v[144:147]\n" \
"s_addk_i32 s84, 0x7c00\n" \
"v_mfma_f32_16x16x32_bf16 v[140:143], a[184:187], a[148:151], v[140:143]\n" \
"buffer_load_dwordx4 v27, s[68:71], s84 offen offset:2048 lds\n" \
"v_mfma_f32_16x16x32_bf16 v[140:143], a[248:251], a[212:215], v[140:143]\n" \
"s_addk_i32 s84, 0x7c00\n" \
"s_nop 0\n" \
"v_mfma_f32_16x16x32_bf16 v[136:139], a[180:183], a[148:151], v[136:139]\n" \
"buffer_load_dwordx4 v27, s[68:71], s84 offen offset:3072 lds\n" \
"v_mfma_f32_16x16x32_bf16 v[136:139], a[244:247], a[212:215], v[136:139]\n" \
"ds_read_b64_tr_b16 a[170:171], v34 offset:512\n" \
"v_writelane_b32 v11, s5, 49\n" \
"v_mfma_f32_16x16x32_bf16 v[132:135], a[176:179], a[148:151], v[132:135]\n" \
"ds_read_b64_tr_b16 a[232:233], v34 offset:4096\n" \
"s_add_i32 s5, s4, 0x60000\n" \
"v_mfma_f32_16x16x32_bf16 v[132:135], a[240:243], a[212:215], v[132:135]\n" \
"ds_read_b128 a[148:151], v28 offset:10240\n" \
"s_and_b32 s5, s5, 0x70000\n" \
"v_mfma_f32_16x16x32_bf16 v[148:151], a[176:179], a[152:155], v[148:151]\n" \
"ds_read_b128 a[212:215], v29 offset:10240\n" \
"s_add_i32 s4, s4, 0x70000\n" \
"v_mfma_f32_16x16x32_bf16 v[148:151], a[240:243], a[216:219], v[148:151]\n" \
"s_add_u32 m0, s30, 0x9000\n" \
"s_mov_b32 s84, 0x9380\n" \
"v_mfma_f32_16x16x32_bf16 v[152:155], a[180:183], a[152:155], v[152:155]\n" \
"buffer_load_dwordx4 v24, s[76:79], s84 offen offset:0 lds\n" \
"v_mfma_f32_16x16x32_bf16 v[152:155], a[244:247], a[216:219], v[152:155]\n" \
"s_addk_i32 s84, 0x2000\n" \
"v_mfma_f32_16x16x32_bf16 v[156:159], a[184:187], a[152:155], v[156:159]\n" \
"buffer_load_dwordx4 v25, s[76:79], s84 offen offset:1024 lds\n" \
"v_mfma_f32_16x16x32_bf16 v[156:159], a[248:251], a[216:219], v[156:159]\n" \
"ds_read_b64_tr_b16 a[234:235], v34 offset:4608\n" \
"s_add_i32 s5, s13, s5\n" \
"v_mfma_f32_16x16x32_bf16 v[160:163], a[188:191], a[152:155], v[160:163]\n" \
"ds_read_b128 v[248:251], v22 offset:0\n" \
"s_and_b32 s4, s4, 0x70000\n" \
"v_mfma_f32_16x16x32_bf16 v[160:163], a[252:255], a[216:219], v[160:163]\n" \
"ds_read_b128 a[152:155], v28 offset:12288\n" \
"s_or_b32 s5, s5, 0x2800\n" \
"v_mfma_f32_16x16x32_bf16 v[176:179], a[188:191], a[156:159], v[176:179]\n" \
"ds_read_b128 a[216:219], v29 offset:12288\n" \
"v_writelane_b32 v11, s5, 50\n" \
"v_mfma_f32_16x16x32_bf16 v[176:179], a[252:255], a[220:223], v[176:179]\n" \
"ds_read_b128 v[48:51], v23 offset:0\n" \
"s_add_i32 s13, s13, s4\n" \
"v_mfma_f32_16x16x32_bf16 v[172:175], a[184:187], a[156:159], v[172:175]\n" \
"s_or_b32 s4, s13, 0x3000\n" \
"v_writelane_b32 v11, s4, 51\n" \
"v_mfma_f32_16x16x32_bf16 v[172:175], a[248:251], a[220:223], v[172:175]\n" \
"s_movk_i32 s11, 0x4000\n" \
"s_lshl_b32 s4, s2, 2\n" \
"v_mfma_f32_16x16x32_bf16 v[168:171], a[180:183], a[156:159], v[168:171]\n" \
"s_and_b32 s4, s4, 28\n" \
"s_bfe_u32 s5, s2, 0x20003\n" \
"v_mfma_f32_16x16x32_bf16 v[168:171], a[244:247], a[220:223], v[168:171]\n" \
"s_or_b32 s4, s4, s5\n" \
"s_lshr_b32 s5, s2, 5\n" \
"v_mfma_f32_16x16x32_bf16 v[164:167], a[176:179], a[156:159], v[164:167]\n" \
"s_lshr_b32 s19, s3, 1\n" \
"s_lshl_b32 s18, s5, 1\n" \
"v_mfma_f32_16x16x32_bf16 v[164:167], a[240:243], a[220:223], v[164:167]\n" \
"ds_read_b128 a[156:159], v28 offset:14336\n" \
"ds_read_b128 a[220:223], v29 offset:14336\n" \
"s_waitcnt lgkmcnt(15)\n" \
"v_mfma_f32_16x16x32_bf16 a[0:3], a[160:163], a[128:131], a[0:3]\n" \
"s_addk_i32 s84, 0x2000\n" \
"v_mfma_f32_16x16x32_bf16 a[0:3], a[224:227], a[192:195], a[0:3]\n" \
"buffer_load_dwordx4 v24, s[76:79], s84 offen offset:2048 lds\n" \
"v_mfma_f32_16x16x32_bf16 a[4:7], a[164:167], a[128:131], a[4:7]\n" \
"ds_read_b64_tr_b16 a[176:177], v36 offset:0\n" \
"s_lshl_b32 s6, s4, 20\n" \
"v_mfma_f32_16x16x32_bf16 a[4:7], a[228:231], a[192:195], a[4:7]\n" \
"s_addk_i32 s84, 0x2000\n" \
"v_mfma_f32_16x16x32_bf16 a[12:15], a[172:175], a[128:131], a[12:15]\n" \
"buffer_load_dwordx4 v25, s[76:79], s84 offen offset:3072 lds\n" \
"s_waitcnt lgkmcnt(15)\n" \
"v_mfma_f32_16x16x32_bf16 a[12:15], a[236:239], a[192:195], a[12:15]\n" \
"ds_read_b64_tr_b16 a[178:179], v36 offset:1024\n" \
"s_lshl_b32 s7, s4, 8\n" \
"s_waitcnt lgkmcnt(12)\n" \
"v_mfma_f32_16x16x32_bf16 a[8:11], a[128:131], a[168:171], a[8:11]\n" \
"ds_read_b64_tr_b16 a[240:241], v36 offset:8192\n" \
"s_and_b32 s13, s3, 1\n" \
"s_waitcnt lgkmcnt(9)\n" \
"v_mfma_f32_16x16x32_bf16 a[8:11], a[192:195], a[232:235], a[8:11]\n" \
"ds_read_b64_tr_b16 a[242:243], v36 offset:9216\n" \
"s_add_i32 s14, s18, s19\n" \
"v_mfma_f32_16x16x32_bf16 a[24:27], a[132:135], a[168:171], a[24:27]\n" \
"ds_read_b64_tr_b16 a[180:181], v37 offset:0\n" \
"v_mfma_f32_16x16x32_bf16 a[24:27], a[196:199], a[232:235], a[24:27]\n" \
"ds_read_b64_tr_b16 a[182:183], v37 offset:1024\n" \
"v_mfma_f32_16x16x32_bf16 a[28:31], a[172:175], a[132:135], a[28:31]\n" \
"ds_read_b64_tr_b16 a[244:245], v37 offset:8192\n" \
"v_mfma_f32_16x16x32_bf16 a[28:31], a[236:239], a[196:199], a[28:31]\n" \
"ds_read_b64_tr_b16 a[246:247], v37 offset:9216\n" \
"v_mfma_f32_16x16x32_bf16 a[20:23], a[164:167], a[132:135], a[20:23]\n" \
"s_add_u32 s6, s26, s6\n" \
"s_addc_u32 s9, s27, 0\n" \
"v_mfma_f32_16x16x32_bf16 a[20:23], a[228:231], a[196:199], a[20:23]\n" \
"s_add_u32 s89, s42, 0x0\n" \
"s_nop 0\n" \
"buffer_store_dwordx4 v[184:187], v20, s[80:83], s89 offen offset:1024 sc1\n" \
"v_mfma_f32_16x16x32_bf16 a[16:19], a[160:163], a[132:135], a[16:19]\n" \
"ds_read_b64_tr_b16 a[184:185], v38 offset:0\n" \
"v_mfma_f32_16x16x32_bf16 a[16:19], a[224:227], a[196:199], a[16:19]\n" \
"ds_read_b64_tr_b16 a[186:187], v38 offset:1024\n" \
"v_mfma_f32_16x16x32_bf16 a[32:35], a[160:163], a[136:139], a[32:35]\n" \
"ds_read_b64_tr_b16 a[248:249], v38 offset:8192\n" \
"v_mfma_f32_16x16x32_bf16 a[32:35], a[224:227], a[200:203], a[32:35]\n" \
"ds_read_b64_tr_b16 a[250:251], v38 offset:9216\n" \
"v_mfma_f32_16x16x32_bf16 a[36:39], a[164:167], a[136:139], a[36:39]\n" \
"s_add_u32 s8, s6, 0x1000000\n" \
"s_addc_u32 s6, s9, 0\n" \
"v_mfma_f32_16x16x32_bf16 a[36:39], a[228:231], a[200:203], a[36:39]\n" \
"s_add_u32 s88, s42, 0x1000\n" \
"buffer_store_dwordx4 v[188:191], v20, s[80:83], s88 offen offset:1024 sc1\n" \
"v_mfma_f32_16x16x32_bf16 a[44:47], a[172:175], a[136:139], a[44:47]\n" \
"ds_read_b64_tr_b16 a[188:189], v39 offset:0\n" \
"s_and_b32 s9, s6, 0xffff\n" \
"v_mfma_f32_16x16x32_bf16 a[44:47], a[236:239], a[200:203], a[44:47]\n" \
"ds_read_b64_tr_b16 a[190:191], v39 offset:1024\n" \
"s_lshl_b32 s6, s5, 16\n" \
"v_mfma_f32_16x16x32_bf16 a[40:43], a[136:139], a[168:171], a[40:43]\n" \
"ds_read_b64_tr_b16 a[252:253], v39 offset:8192\n" \
"s_sub_i32 s10, 0x100000, s6\n" \
"v_mfma_f32_16x16x32_bf16 a[40:43], a[200:203], a[232:235], a[40:43]\n" \
"s_mov_b32 s6, 0x8000\n" \
"v_mfma_f32_16x16x32_bf16 a[56:59], a[140:143], a[168:171], a[56:59]\n" \
"ds_read_b64_tr_b16 a[254:255], v39 offset:9216\n" \
"v_mfma_f32_16x16x32_bf16 a[56:59], a[204:207], a[232:235], a[56:59]\n" \
"s_cmp_lg_u32 s5, 0\n" \
"s_cselect_b32 s17, s11, s6\n" \
"v_mfma_f32_16x16x32_bf16 a[60:63], a[172:175], a[140:143], a[60:63]\n" \
"s_add_u32 s89, s42, 0x2000\n" \
"buffer_store_dwordx4 v[192:195], v20, s[80:83], s89 offen offset:0 sc1\n" \
"v_mfma_f32_16x16x32_bf16 a[60:63], a[236:239], a[204:207], a[60:63]\n" \
"s_lshl_b32 s6, s3, 1\n" \
"s_lshl_b32 s5, s5, 3\n" \
"v_mfma_f32_16x16x32_bf16 a[52:55], a[164:167], a[140:143], a[52:55]\n" \
"s_and_b32 s6, s6, 4\n" \
"s_or_b32 s12, s5, s6\n" \
"v_mfma_f32_16x16x32_bf16 a[52:55], a[228:231], a[204:207], a[52:55]\n" \
"s_sub_i32 s5, s13, s5\n" \
"s_or_b32 s5, s5, s6\n" \
"v_mfma_f32_16x16x32_bf16 a[48:51], a[160:163], a[140:143], a[48:51]\n" \
"s_or_b32 s6, s12, s13\n" \
"s_lshl_b32 s4, s4, 2\n" \
"v_mfma_f32_16x16x32_bf16 a[48:51], a[224:227], a[204:207], a[48:51]\n" \
"s_mulk_i32 s6, 0x92\n" \
"s_add_i32 s4, s4, 18\n" \
"s_waitcnt lgkmcnt(15)\n" \
"v_mfma_f32_16x16x32_bf16 a[64:67], a[164:167], v[248:251], a[64:67]\n" \
"s_mulk_i32 s5, 0x92\n" \
"s_add_i32 s6, s4, s6\n" \
"v_mfma_f32_16x16x32_bf16 a[64:67], a[228:231], v[48:51], a[64:67]\n" \
"s_add_i32 s5, s5, s4\n" \
"s_lshl_b32 s4, s3, 11\n" \
"v_mfma_f32_16x16x32_bf16 a[80:83], a[172:175], v[248:251], a[80:83]\n" \
"s_lshl_b32 s15, s13, 12\n" \
"s_lshl_b32 s16, s19, 14\n" \
"v_mfma_f32_16x16x32_bf16 a[80:83], a[236:239], v[48:51], a[80:83]\n" \
"s_mul_i32 s19, s19, 0xe000\n" \
"s_mov_b32 s11, 0x20000\n" \
"v_mfma_f32_16x16x32_bf16 a[68:71], v[248:251], a[168:171], a[68:71]\n" \
"s_or_b32 s12, s4, 0xc000\n" \
"s_lshl_b32 s4, s3, 8\n" \
"v_mfma_f32_16x16x32_bf16 a[68:71], v[48:51], a[232:235], a[68:71]\n" \
"s_waitcnt vmcnt(3) lgkmcnt(0)\n" \
"s_barrier\n" \
"v_mfma_f32_16x16x32_bf16 v[52:55], a[176:179], a[128:131], v[52:55]\n" \
"s_add_u32 m0, s40, 0x0\n" \
"s_add_u32 s85, s39, 0x10000\n" \
"v_mfma_f32_16x16x32_bf16 v[52:55], a[240:243], a[192:195], v[52:55]\n" \
"buffer_load_dwordx4 v20, s[80:83], s85 offen sc1 lds\n" \
"v_mfma_f32_16x16x32_bf16 v[56:59], a[180:183], a[128:131], v[56:59]\n" \
"ds_read_b64_tr_b16 a[160:161], v32 offset:32768\n" \
"s_or_b32 s22, s15, s16\n" \
"v_mfma_f32_16x16x32_bf16 v[56:59], a[244:247], a[192:195], v[56:59]\n" \
"ds_read_b64_tr_b16 a[162:163], v32 offset:33280\n" \
"s_or_b32 s23, s19, s15\n" \
"v_mfma_f32_16x16x32_bf16 v[60:63], a[184:187], a[128:131], v[60:63]\n" \
"s_add_u32 m0, s40, 0x1000\n" \
"s_add_u32 s85, s39, 0x11000\n" \
"v_mfma_f32_16x16x32_bf16 v[60:63], a[248:251], a[192:195], v[60:63]\n" \
"buffer_load_dwordx4 v20, s[80:83], s85 offen sc1 lds\n" \
"v_mfma_f32_16x16x32_bf16 v[64:67], a[188:191], a[128:131], v[64:67]\n" \
"ds_read_b64_tr_b16 a[224:225], v32 offset:36864\n" \
"s_lshl_b32 s60, s6, 10\n" \
"v_mfma_f32_16x16x32_bf16 v[64:67], a[252:255], a[192:195], v[64:67]\n" \
"ds_read_b128 a[128:131], v28 offset:32768\n" \
"s_lshl_b32 s61, s5, 10\n" \
"v_mfma_f32_16x16x32_bf16 v[80:83], a[188:191], a[132:135], v[80:83]\n" \
"ds_read_b128 a[192:195], v29 offset:32768\n" \
"s_lshl_b32 s14, s14, 15\n" \
"v_mfma_f32_16x16x32_bf16 v[80:83], a[252:255], a[196:199], v[80:83]\n" \
"s_add_u32 m0, s40, 0x3000\n" \
"s_movk_i32 s84, 0x400\n" \
"v_mfma_f32_16x16x32_bf16 v[76:79], a[184:187], a[132:135], v[76:79]\n" \
"buffer_load_dwordx4 v21, s[76:79], s84 offen offset:0 lds\n" \
"v_mfma_f32_16x16x32_bf16 v[76:79], a[248:251], a[196:199], v[76:79]\n" \
"ds_read_b64_tr_b16 a[226:227], v32 offset:37376\n" \
"s_or_b32 s20, s4, 0x26c00\n" \
"v_mfma_f32_16x16x32_bf16 v[72:75], a[180:183], a[132:135], v[72:75]\n" \
"s_add_u32 m0, s30, 0x0\n" \
"s_movk_i32 s84, 0x400\n" \
"v_mfma_f32_16x16x32_bf16 v[72:75], a[244:247], a[196:199], v[72:75]\n" \
"buffer_load_dwordx4 v24, s[76:79], s84 offen offset:0 lds\n" \
"v_mfma_f32_16x16x32_bf16 v[68:71], a[176:179], a[132:135], v[68:71]\n" \
"ds_read_b64_tr_b16 a[164:165], v33 offset:32768\n" \
"s_or_b32 s13, s22, 0x8000\n" \
"v_mfma_f32_16x16x32_bf16 v[68:71], a[240:243], a[196:199], v[68:71]\n" \
"ds_read_b128 a[132:135], v28 offset:34816\n" \
"s_add_i32 s22, s22, 0x14000\n" \
"v_mfma_f32_16x16x32_bf16 v[84:87], a[176:179], a[136:139], v[84:87]\n" \
"ds_read_b128 a[196:199], v29 offset:34816\n" \
"s_bitset1_b32 s23, 16\n" \
"v_mfma_f32_16x16x32_bf16 v[84:87], a[240:243], a[200:203], v[84:87]\n" \
"s_movk_i32 s84, 0x2400\n" \
"v_mfma_f32_16x16x32_bf16 v[88:91], a[180:183], a[136:139], v[88:91]\n" \
"buffer_load_dwordx4 v25, s[76:79], s84 offen offset:1024 lds\n" \
"v_mfma_f32_16x16x32_bf16 v[88:91], a[244:247], a[200:203], v[88:91]\n" \
"ds_read_b64_tr_b16 a[166:167], v33 offset:33280\n" \
"s_or_b32 s24, s7, 0x5be8020\n" \
"v_mfma_f32_16x16x32_bf16 v[92:95], a[184:187], a[136:139], v[92:95]\n" \
"ds_read_b64_tr_b16 a[228:229], v33 offset:36864\n" \
"s_and_b32 s57, s27, 0xffff\n" \
"v_mfma_f32_16x16x32_bf16 v[92:95], a[248:251], a[200:203], v[92:95]\n" \
"s_movk_i32 s84, 0x4400\n" \
"v_mfma_f32_16x16x32_bf16 v[96:99], a[188:191], a[136:139], v[96:99]\n" \
".fill (L_pb + 32480 - .) / 4, 4, 0xBF800000\n" \
"L_pp8:\n" \
"s_cbranch_execz L_pret\n" \
"buffer_load_dwordx4 v24, s[76:79], s84 offen offset:2048 lds\n" \
"v_mfma_f32_16x16x32_bf16 v[96:99], a[252:255], a[200:203], v[96:99]\n" \
"ds_read_b128 a[136:139], v28 offset:36864\n" \
"s_mov_b32 s58, 0x6000000\n" \
"v_mfma_f32_16x16x32_bf16 v[112:115], a[188:191], a[140:143], v[112:115]\n" \
"ds_read_b128 a[200:203], v29 offset:36864\n" \
"s_mov_b32 s56, s26\n" \
"v_mfma_f32_16x16x32_bf16 v[112:115], a[252:255], a[204:207], v[112:115]\n" \
"s_movk_i32 s84, 0x6400\n" \
"v_mfma_f32_16x16x32_bf16 v[108:111], a[184:187], a[140:143], v[108:111]\n" \
"buffer_load_dwordx4 v25, s[76:79], s84 offen offset:3072 lds\n" \
"v_mfma_f32_16x16x32_bf16 v[108:111], a[248:251], a[204:207], v[108:111]\n" \
"ds_read_b64_tr_b16 a[230:231], v33 offset:37376\n" \
"s_mov_b32 s59, s11\n" \
"v_mfma_f32_16x16x32_bf16 v[104:107], a[180:183], a[140:143], v[104:107]\n" \
"ds_read_b64_tr_b16 a[172:173], v35 offset:32768\n" \
"s_add_i32 s60, s60, 0x3000000\n" \
"v_mfma_f32_16x16x32_bf16 v[104:107], a[244:247], a[204:207], v[104:107]\n" \
"ds_read_b64_tr_b16 a[174:175], v35 offset:33280\n" \
"s_add_i32 s61, s61, 0x411c000\n" \
"v_mfma_f32_16x16x32_bf16 v[100:103], a[176:179], a[140:143], v[100:103]\n" \
"ds_read_b64_tr_b16 a[236:237], v35 offset:36864\n" \
"s_sub_i32 s19, 14, s18\n" \
"v_mfma_f32_16x16x32_bf16 v[100:103], a[240:243], a[204:207], v[100:103]\n" \
"ds_read_b128 a[140:143], v28 offset:38912\n" \
"s_mov_b32 s21, 0x71800000\n" \
"v_mfma_f32_16x16x32_bf16 v[116:119], a[176:179], a[144:147], v[116:119]\n" \
"ds_read_b128 a[204:207], v29 offset:38912\n" \
"v_mfma_f32_16x16x32_bf16 v[116:119], a[240:243], a[208:211], v[116:119]\n" \
"s_add_u32 m0, s31, 0x0\n" \
"s_mov_b32 s84, 0x400000\n" \
"v_mfma_f32_16x16x32_bf16 v[120:123], a[180:183], a[144:147], v[120:123]\n" \
"buffer_load_dwordx4 v26, s[68:71], s84 offen offset:0 lds\n" \
"v_mfma_f32_16x16x32_bf16 v[120:123], a[244:247], a[208:211], v[120:123]\n" \
"s_addk_i32 s84, 0x7c00\n" \
"s_nop 0\n" \
"v_mfma_f32_16x16x32_bf16 v[124:127], a[184:187], a[144:147], v[124:127]\n" \
"buffer_load_dwordx4 v26, s[68:71], s84 offen offset:1024 lds\n" \
"v_mfma_f32_16x16x32_bf16 v[124:127], a[248:251], a[208:211], v[124:127]\n" \
"ds_read_b64_tr_b16 a[238:239], v35 offset:37376\n" \
"v_mfma_f32_16x16x32_bf16 v[128:131], a[188:191], a[144:147], v[128:131]\n" \
"ds_read_b64_tr_b16 a[168:169], v34 offset:32768\n" \
"v_mfma_f32_16x16x32_bf16 v[128:131], a[252:255], a[208:211], v[128:131]\n" \
"ds_read_b128 a[144:147], v28 offset:40960\n" \
"v_mfma_f32_16x16x32_bf16 v[144:147], a[188:191], a[148:151], v[144:147]\n" \
"ds_read_b128 a[208:211], v29 offset:40960\n" \
"v_mfma_f32_16x16x32_bf16 v[144:147], a[252:255], a[212:215], v[144:147]\n" \
"s_addk_i32 s84, 0x7c00\n" \
"v_mfma_f32_16x16x32_bf16 v[140:143], a[184:187], a[148:151], v[140:143]\n" \
"buffer_load_dwordx4 v27, s[68:71], s84 offen offset:2048 lds\n" \
"v_mfma_f32_16x16x32_bf16 v[140:143], a[248:251], a[212:215], v[140:143]\n" \
"s_addk_i32 s84, 0x7c00\n" \
"v_mfma_f32_16x16x32_bf16 v[136:139], a[180:183], a[148:151], v[136:139]\n" \
"buffer_load_dwordx4 v27, s[68:71], s84 offen offset:3072 lds\n" \
"v_mfma_f32_16x16x32_bf16 v[136:139], a[244:247], a[212:215], v[136:139]\n" \
"ds_read_b64_tr_b16 a[170:171], v34 offset:33280\n" \
"v_mfma_f32_16x16x32_bf16 v[132:135], a[176:179], a[148:151], v[132:135]\n" \
"ds_read_b64_tr_b16 a[232:233], v34 offset:36864\n" \
"v_mfma_f32_16x16x32_bf16 v[132:135], a[240:243], a[212:215], v[132:135]\n" \
"ds_read_b128 a[148:151], v28 offset:43008\n" \
"v_mfma_f32_16x16x32_bf16 v[148:151], a[176:179], a[152:155], v[148:151]\n" \
"ds_read_b128 a[212:215], v29 offset:43008\n" \
"v_mfma_f32_16x16x32_bf16 v[148:151], a[240:243], a[216:219], v[148:151]\n" \
"s_add_u32 m0, s30, 0x1000\n" \
"s_mov_b32 s84, 0x9400\n" \
"v_mfma_f32_16x16x32_bf16 v[152:155], a[180:183], a[152:155], v[152:155]\n" \
"buffer_load_dwordx4 v24, s[76:79], s84 offen offset:0 lds\n" \
"v_mfma_f32_16x16x32_bf16 v[152:155], a[244:247], a[216:219], v[152:155]\n" \
"s_addk_i32 s84, 0x2000\n" \
"s_nop 0\n" \
"v_mfma_f32_16x16x32_bf16 v[156:159], a[184:187], a[152:155], v[156:159]\n" \
"buffer_load_dwordx4 v25, s[76:79], s84 offen offset:1024 lds\n" \
"v_mfma_f32_16x16x32_bf16 v[156:159], a[248:251], a[216:219], v[156:159]\n" \
"ds_read_b64_tr_b16 a[234:235], v34 offset:37376\n" \
"v_mfma_f32_16x16x32_bf16 v[160:163], a[188:191], a[152:155], v[160:163]\n" \
"ds_read_b128 v[248:251], v22 offset:32768\n" \
"v_mfma_f32_16x16x32_bf16 v[160:163], a[252:255], a[216:219], v[160:163]\n" \
"ds_read_b128 a[152:155], v28 offset:45056\n" \
"v_mfma_f32_16x16x32_bf16 v[176:179], a[188:191], a[156:159], v[176:179]\n" \
"ds_read_b128 a[216:219], v29 offset:45056\n" \
"v_mfma_f32_16x16x32_bf16 v[176:179], a[252:255], a[220:223], v[176:179]\n" \
"ds_read_b128 v[48:51], v23 offset:32768\n" \
"v_mfma_f32_16x16x32_bf16 v[172:175], a[184:187], a[156:159], v[172:175]\n" \
"v_mfma_f32_16x16x32_bf16 v[172:175], a[248:251], a[220:223], v[172:175]\n" \
"v_mfma_f32_16x16x32_bf16 v[168:171], a[180:183], a[156:159], v[168:171]\n" \
"v_mfma_f32_16x16x32_bf16 v[168:171], a[244:247], a[220:223], v[168:171]\n" \
"v_mfma_f32_16x16x32_bf16 v[164:167], a[176:179], a[156:159], v[164:167]\n" \
"v_mfma_f32_16x16x32_bf16 v[164:167], a[240:243], a[220:223], v[164:167]\n" \
"ds_read_b128 a[156:159], v28 offset:47104\n" \
"ds_read_b128 a[220:223], v29 offset:47104\n" \
"s_waitcnt lgkmcnt(15)\n" \
"v_mfma_f32_16x16x32_bf16 a[0:3], a[160:163], a[128:131], a[0:3]\n" \
"s_addk_i32 s84, 0x2000\n" \
"v_mfma_f32_16x16x32_bf16 a[0:3], a[224:227], a[192:195], a[0:3]\n" \
"buffer_load_dwordx4 v24, s[76:79], s84 offen offset:2048 lds\n" \
"v_mfma_f32_16x16x32_bf16 a[4:7], a[164:167], a[128:131], a[4:7]\n" \
"ds_read_b64_tr_b16 a[176:177], v36 offset:32768\n" \
"v_mfma_f32_16x16x32_bf16 a[4:7], a[228:231], a[192:195], a[4:7]\n" \
"s_addk_i32 s84, 0x2000\n" \
"v_mfma_f32_16x16x32_bf16 a[12:15], a[172:175], a[128:131], a[12:15]\n" \
"buffer_load_dwordx4 v25, s[76:79], s84 offen offset:3072 lds\n" \
"s_waitcnt lgkmcnt(15)\n" \
"v_mfma_f32_16x16x32_bf16 a[12:15], a[236:239], a[192:195], a[12:15]\n" \
"ds_read_b64_tr_b16 a[178:179], v36 offset:33792\n" \
"s_waitcnt lgkmcnt(12)\n" \
"v_mfma_f32_16x16x32_bf16 a[8:11], a[128:131], a[168:171], a[8:11]\n" \
"ds_read_b64_tr_b16 a[240:241], v36 offset:40960\n" \
"s_waitcnt lgkmcnt(9)\n" \
"v_mfma_f32_16x16x32_bf16 a[8:11], a[192:195], a[232:235], a[8:11]\n" \
"ds_read_b64_tr_b16 a[242:243], v36 offset:41984\n" \
"v_mfma_f32_16x16x32_bf16 a[24:27], a[132:135], a[168:171], a[24:27]\n" \
"ds_read_b64_tr_b16 a[180:181], v37 offset:32768\n" \
"v_mfma_f32_16x16x32_bf16 a[24:27], a[196:199], a[232:235], a[24:27]\n" \
"ds_read_b64_tr_b16 a[182:183], v37 offset:33792\n" \
"v_mfma_f32_16x16x32_bf16 a[28:31], a[172:175], a[132:135], a[28:31]\n" \
"ds_read_b64_tr_b16 a[244:245], v37 offset:40960\n" \
"v_mfma_f32_16x16x32_bf16 a[28:31], a[236:239], a[196:199], a[28:31]\n" \
"ds_read_b64_tr_b16 a[246:247], v37 offset:41984\n" \
"v_mfma_f32_16x16x32_bf16 a[20:23], a[164:167], a[132:135], a[20:23]\n" \
"v_mfma_f32_16x16x32_bf16 a[20:23], a[228:231], a[196:199], a[20:23]\n" \
"s_add_u32 s88, s42, 0x2000\n" \
"buffer_store_dwordx4 v[196:199], v20, s[80:83], s88 offen offset:1024 sc1\n" \
"v_mfma_f32_16x16x32_bf16 a[16:19], a[160:163], a[132:135], a[16:19]\n" \
"ds_read_b64_tr_b16 a[184:185], v38 offset:32768\n" \
"v_mfma_f32_16x16x32_bf16 a[16:19], a[224:227], a[196:199], a[16:19]\n" \
"ds_read_b64_tr_b16 a[186:187], v38 offset:33792\n" \
"v_mfma_f32_16x16x32_bf16 a[32:35], a[160:163], a[136:139], a[32:35]\n" \
"ds_read_b64_tr_b16 a[248:249], v38 offset:40960\n" \
"v_mfma_f32_16x16x32_bf16 a[32:35], a[224:227], a[200:203], a[32:35]\n" \
"ds_read_b64_tr_b16 a[250:251], v38 offset:41984\n" \
"v_mfma_f32_16x16x32_bf16 a[36:39], a[164:167], a[136:139], a[36:39]\n" \
"v_lshlrev_b32_e32 v45, 3, v20\n" \
"v_add_u32_e32 v45, s30, v45\n" \
"v_mfma_f32_16x16x32_bf16 a[36:39], a[228:231], a[200:203], a[36:39]\n" \
"s_add_u32 s89, s42, 0x3000\n" \
"buffer_store_dwordx4 v[216:219], v20, s[80:83], s89 offen offset:0 sc1\n" \
"s_getpc_b64 s[86:87]\n" \
"L_ct0_%=:\n" \
"s_add_u32 s86, s86, L_ap0-L_ct0_%=\n" \
"s_addc_u32 s87, s87, 0\n" \
"s_add_u32 m0, s40, 0x2000\n" \
"v_mfma_f32_16x16x32_bf16 a[44:47], a[172:175], a[136:139], a[44:47]\n" \
"global_load_lds_dword v45, s[86:87]\n" \
"v_mfma_f32_16x16x32_bf16 a[44:47], a[236:239], a[200:203], a[44:47]\n" \
"ds_read_b64_tr_b16 a[188:189], v39 offset:32768\n" \
"v_mfma_f32_16x16x32_bf16 a[40:43], a[136:139], a[168:171], a[40:43]\n" \
"ds_read_b64_tr_b16 a[190:191], v39 offset:33792\n" \
"v_mfma_f32_16x16x32_bf16 a[40:43], a[200:203], a[232:235], a[40:43]\n" \
"ds_read_b64_tr_b16 a[252:253], v39 offset:40960\n" \
"v_mfma_f32_16x16x32_bf16 a[56:59], a[140:143], a[168:171], a[56:59]\n" \
"ds_read_b64_tr_b16 a[254:255], v39 offset:41984\n" \
"v_mfma_f32_16x16x32_bf16 a[56:59], a[204:207], a[232:235], a[56:59]\n" \
"v_mfma_f32_16x16x32_bf16 a[60:63], a[172:175], a[140:143], a[60:63]\n" \
"s_add_u32 s88, s42, 0x3000\n" \
"buffer_store_dwordx4 v[220:223], v20, s[80:83], s88 offen offset:1024 sc1\n" \
"v_mfma_f32_16x16x32_bf16 a[60:63], a[236:239], a[204:207], a[60:63]\n" \
"v_mfma_f32_16x16x32_bf16 a[52:55], a[164:167], a[140:143], a[52:55]\n" \
"v_mfma_f32_16x16x32_bf16 a[52:55], a[228:231], a[204:207], a[52:55]\n" \
"v_mfma_f32_16x16x32_bf16 a[48:51], a[160:163], a[140:143], a[48:51]\n" \
"v_mfma_f32_16x16x32_bf16 a[48:51], a[224:227], a[204:207], a[48:51]\n" \
"s_waitcnt lgkmcnt(15)\n" \
"v_mfma_f32_16x16x32_bf16 a[64:67], a[164:167], v[248:251], a[64:67]\n" \
"v_mfma_f32_16x16x32_bf16 a[64:67], a[228:231], v[48:51], a[64:67]\n" \
"v_mfma_f32_16x16x32_bf16 a[80:83], a[172:175], v[248:251], a[80:83]\n" \
"v_mfma_f32_16x16x32_bf16 a[80:83], a[236:239], v[48:51], a[80:83]\n" \
"v_mfma_f32_16x16x32_bf16 a[68:71], v[248:251], a[168:171], a[68:71]\n" \
"v_mfma_f32_16x16x32_bf16 a[68:71], v[48:51], a[232:235], a[68:71]\n" \
"s_waitcnt vmcnt(4) lgkmcnt(0)\n" \
"s_barrier\n" \
"s_nop 0\n" \
"v_mfma_f32_16x16x32_bf16 v[52:55], a[176:179], a[128:131], v[52:55]\n" \
"ds_read_b64_tr_b16 a[160:161], v32 offset:0\n" \
"v_accvgpr_read_b32 v184, a0\n" \
"v_mfma_f32_16x16x32_bf16 v[52:55], a[240:243], a[192:195], v[52:55]\n" \
"v_accvgpr_read_b32 v185, a1\n" \
"v_accvgpr_read_b32 v186, a2\n" \
"v_mfma_f32_16x16x32_bf16 v[56:59], a[180:183], a[128:131], v[56:59]\n" \
"ds_read_b64_tr_b16 a[162:163], v32 offset:512\n" \
"v_accvgpr_read_b32 v187, a3\n" \
"v_mfma_f32_16x16x32_bf16 v[56:59], a[244:247], a[192:195], v[56:59]\n" \
"v_accvgpr_read_b32 v188, a4\n" \
"v_accvgpr_read_b32 v189, a5\n" \
"v_mfma_f32_16x16x32_bf16 v[60:63], a[184:187], a[128:131], v[60:63]\n" \
"v_accvgpr_read_b32 v190, a6\n" \
"v_accvgpr_read_b32 v191, a7\n" \
"v_mfma_f32_16x16x32_bf16 v[60:63], a[248:251], a[192:195], v[60:63]\n" \
"ds_read_b64_tr_b16 a[224:225], v32 offset:4096\n" \
"v_accvgpr_read_b32 v196, a12\n" \
"v_mfma_f32_16x16x32_bf16 v[64:67], a[188:191], a[128:131], v[64:67]\n" \
"v_accvgpr_read_b32 v197, a13\n" \
"v_accvgpr_read_b32 v198, a14\n" \
"v_mfma_f32_16x16x32_bf16 v[64:67], a[252:255], a[192:195], v[64:67]\n" \
"ds_read_b128 a[128:131], v28 offset:0\n" \
"v_accvgpr_read_b32 v199, a15\n" \
"v_mfma_f32_16x16x32_bf16 v[80:83], a[188:191], a[132:135], v[80:83]\n" \
"ds_read_b128 a[192:195], v29 offset:0\n" \
"v_accvgpr_read_b32 v192, a8\n" \
"v_mfma_f32_16x16x32_bf16 v[80:83], a[252:255], a[196:199], v[80:83]\n" \
"ds_read_b64_tr_b16 a[226:227], v32 offset:4608\n" \
"v_accvgpr_read_b32 v193, a9\n" \
"v_mfma_f32_16x16x32_bf16 v[76:79], a[184:187], a[132:135], v[76:79]\n" \
"v_accvgpr_read_b32 v194, a10\n" \
"v_accvgpr_read_b32 v195, a11\n" \
"v_mfma_f32_16x16x32_bf16 v[76:79], a[248:251], a[196:199], v[76:79]\n" \
"ds_read_b64_tr_b16 a[164:165], v33 offset:0\n" \
"v_accvgpr_read_b32 v208, a24\n" \
"v_mfma_f32_16x16x32_bf16 v[72:75], a[180:183], a[132:135], v[72:75]\n" \
"v_accvgpr_read_b32 v209, a25\n" \
"v_accvgpr_read_b32 v210, a26\n" \
"v_mfma_f32_16x16x32_bf16 v[72:75], a[244:247], a[196:199], v[72:75]\n" \
"v_accvgpr_read_b32 v211, a27\n" \
"v_accvgpr_read_b32 v212, a28\n" \
"v_mfma_f32_16x16x32_bf16 v[68:71], a[176:179], a[132:135], v[68:71]\n" \
"ds_read_b64_tr_b16 a[166:167], v33 offset:512\n" \
"v_accvgpr_read_b32 v213, a29\n" \
"v_mfma_f32_16x16x32_bf16 v[68:71], a[240:243], a[196:199], v[68:71]\n" \
"ds_read_b128 a[132:135], v28 offset:2048\n" \
"v_accvgpr_read_b32 v214, a30\n" \
"v_mfma_f32_16x16x32_bf16 v[84:87], a[176:179], a[136:139], v[84:87]\n" \
"ds_read_b128 a[196:199], v29 offset:2048\n" \
"v_accvgpr_read_b32 v215, a31\n" \
"v_mfma_f32_16x16x32_bf16 v[84:87], a[240:243], a[200:203], v[84:87]\n" \
"ds_read_b64_tr_b16 a[228:229], v33 offset:4096\n" \
"v_accvgpr_read_b32 v204, a20\n" \
"v_mfma_f32_16x16x32_bf16 v[88:91], a[180:183], a[136:139], v[88:91]\n" \
"v_accvgpr_read_b32 v205, a21\n" \
"v_accvgpr_read_b32 v206, a22\n" \
"v_mfma_f32_16x16x32_bf16 v[88:91], a[244:247], a[200:203], v[88:91]\n" \
"v_accvgpr_read_b32 v207, a23\n" \
"v_accvgpr_read_b32 v200, a16\n" \
"v_mfma_f32_16x16x32_bf16 v[92:95], a[184:187], a[136:139], v[92:95]\n" \
"ds_read_b64_tr_b16 a[230:231], v33 offset:4608\n" \
"v_accvgpr_read_b32 v201, a17\n" \
"v_mfma_f32_16x16x32_bf16 v[92:95], a[248:251], a[200:203], v[92:95]\n" \
"v_accvgpr_read_b32 v202, a18\n" \
"v_accvgpr_read_b32 v203, a19\n" \
"v_mfma_f32_16x16x32_bf16 v[96:99], a[188:191], a[136:139], v[96:99]\n" \
"v_accvgpr_read_b32 v216, a32\n" \
"v_accvgpr_read_b32 v217, a33\n" \
"v_mfma_f32_16x16x32_bf16 v[96:99], a[252:255], a[200:203], v[96:99]\n" \
"ds_read_b128 a[136:139], v28 offset:4096\n" \
"v_accvgpr_read_b32 v218, a34\n" \
"v_mfma_f32_16x16x32_bf16 v[112:115], a[188:191], a[140:143], v[112:115]\n" \
"ds_read_b128 a[200:203], v29 offset:4096\n" \
"v_accvgpr_read_b32 v219, a35\n" \
"v_mfma_f32_16x16x32_bf16 v[112:115], a[252:255], a[204:207], v[112:115]\n" \
"ds_read_b64_tr_b16 a[172:173], v35 offset:0\n" \
"v_accvgpr_read_b32 v220, a36\n" \
"v_mfma_f32_16x16x32_bf16 v[108:111], a[184:187], a[140:143], v[108:111]\n" \
"ds_read_b64_tr_b16 a[174:175], v35 offset:512\n" \
"v_accvgpr_read_b32 v221, a37\n" \
"v_mfma_f32_16x16x32_bf16 v[108:111], a[248:251], a[204:207], v[108:111]\n" \
"v_accvgpr_read_b32 v222, a38\n" \
"v_accvgpr_read_b32 v223, a39\n" \
"v_mfma_f32_16x16x32_bf16 v[104:107], a[180:183], a[140:143], v[104:107]\n" \
"ds_read_b64_tr_b16 a[236:237], v35 offset:4096\n" \
"v_accvgpr_read_b32 v228, a44\n" \
"v_mfma_f32_16x16x32_bf16 v[104:107], a[244:247], a[204:207], v[104:107]\n" \
"v_accvgpr_read_b32 v229, a45\n" \
"v_accvgpr_read_b32 v230, a46\n" \
"v_mfma_f32_16x16x32_bf16 v[100:103], a[176:179], a[140:143], v[100:103]\n" \
"v_accvgpr_read_b32 v231, a47\n" \
"v_accvgpr_read_b32 v224, a40\n" \
"v_mfma_f32_16x16x32_bf16 v[100:103], a[240:243], a[204:207], v[100:103]\n" \
"ds_read_b128 a[140:143], v28 offset:6144\n" \
"v_accvgpr_read_b32 v225, a41\n" \
"v_mfma_f32_16x16x32_bf16 v[116:119], a[176:179], a[144:147], v[116:119]\n" \
"ds_read_b128 a[204:207], v29 offset:6144\n" \
"v_accvgpr_read_b32 v226, a42\n" \
"v_mfma_f32_16x16x32_bf16 v[116:119], a[240:243], a[208:211], v[116:119]\n" \
"ds_read_b64_tr_b16 a[238:239], v35 offset:4608\n" \
"v_accvgpr_read_b32 v227, a43\n" \
"v_mfma_f32_16x16x32_bf16 v[120:123], a[180:183], a[144:147], v[120:123]\n" \
"ds_read_b64_tr_b16 a[168:169], v34 offset:0\n" \
"v_accvgpr_read_b32 v240, a56\n" \
"v_mfma_f32_16x16x32_bf16 v[120:123], a[244:247], a[208:211], v[120:123]\n" \
"v_accvgpr_read_b32 v241, a57\n" \
"v_accvgpr_read_b32 v242, a58\n" \
"v_mfma_f32_16x16x32_bf16 v[124:127], a[184:187], a[144:147], v[124:127]\n" \
"v_accvgpr_read_b32 v243, a59\n" \
"v_accvgpr_read_b32 v244, a60\n" \
"v_mfma_f32_16x16x32_bf16 v[124:127], a[248:251], a[208:211], v[124:127]\n" \
"ds_read_b64_tr_b16 a[170:171], v34 offset:512\n" \
"v_accvgpr_read_b32 v245, a61\n" \
"v_mfma_f32_16x16x32_bf16 v[128:131], a[188:191], a[144:147], v[128:131]\n" \
"v_accvgpr_read_b32 v246, a62\n" \
"v_accvgpr_read_b32 v247, a63\n" \
"v_mfma_f32_16x16x32_bf16 v[128:131], a[252:255], a[208:211], v[128:131]\n" \
"ds_read_b128 a[144:147], v28 offset:8192\n" \
"v_accvgpr_read_b32 v236, a52\n" \
"v_mfma_f32_16x16x32_bf16 v[144:147], a[188:191], a[148:151], v[144:147]\n" \
"ds_read_b128 a[208:211], v29 offset:8192\n" \
"v_accvgpr_read_b32 v237, a53\n" \
"v_mfma_f32_16x16x32_bf16 v[144:147], a[252:255], a[212:215], v[144:147]\n" \
"ds_read_b64_tr_b16 a[232:233], v34 offset:4096\n" \
"v_accvgpr_read_b32 v238, a54\n" \
"v_mfma_f32_16x16x32_bf16 v[140:143], a[184:187], a[148:151], v[140:143]\n" \
"v_accvgpr_read_b32 v239, a55\n" \
"v_accvgpr_read_b32 v232, a48\n" \
"v_mfma_f32_16x16x32_bf16 v[140:143], a[248:251], a[212:215], v[140:143]\n" \
"ds_read_b64_tr_b16 a[234:235], v34 offset:4608\n" \
"v_accvgpr_read_b32 v233, a49\n" \
"v_mfma_f32_16x16x32_bf16 v[136:139], a[180:183], a[148:151], v[136:139]\n" \
"v_accvgpr_read_b32 v234, a50\n" \
"v_accvgpr_read_b32 v235, a51\n" \
"v_mfma_f32_16x16x32_bf16 v[136:139], a[244:247], a[212:215], v[136:139]\n" \
"v_accvgpr_read_b32 v180, a64\n" \
"v_accvgpr_read_b32 v181, a65\n" \
"v_mfma_f32_16x16x32_bf16 v[132:135], a[176:179], a[148:151], v[132:135]\n" \
"ds_read_b128 v[248:251], v22 offset:0\n" \
"v_accvgpr_read_b32 v182, a66\n" \
"v_mfma_f32_16x16x32_bf16 v[132:135], a[240:243], a[212:215], v[132:135]\n" \
"ds_read_b128 a[148:151], v28 offset:10240\n" \
"v_accvgpr_read_b32 v183, a67\n" \
"v_mfma_f32_16x16x32_bf16 v[148:151], a[176:179], a[152:155], v[148:151]\n" \
"ds_read_b128 a[212:215], v29 offset:10240\n" \
"v_accvgpr_read_b32 v252, a68\n" \
"v_mfma_f32_16x16x32_bf16 v[148:151], a[240:243], a[216:219], v[148:151]\n" \
"ds_read_b128 v[48:51], v23 offset:0\n" \
"v_accvgpr_read_b32 v253, a69\n" \
"v_mfma_f32_16x16x32_bf16 v[152:155], a[180:183], a[152:155], v[152:155]\n" \
"v_accvgpr_read_b32 v254, a70\n" \
"v_accvgpr_read_b32 v255, a71\n" \
"v_mfma_f32_16x16x32_bf16 v[152:155], a[244:247], a[216:219], v[152:155]\n" \
"v_lshrrev_b32_e64 v24, 9, v20\n" \
"v_and_b32_e32 v26, 0x1ff, v20\n" \
"v_mfma_f32_16x16x32_bf16 v[156:159], a[184:187], a[152:155], v[156:159]\n" \
"v_lshl_add_u32 v24, v24, 3, v26\n" \
"v_lshrrev_b32_e64 v25, 8, v20\n" \
"v_mfma_f32_16x16x32_bf16 v[156:159], a[248:251], a[216:219], v[156:159]\n" \
"v_mul_u32_u24_e32 v25, 0x24700, v25\n" \
"v_add_u32_e64 v25, v25, v20\n" \
"v_mfma_f32_16x16x32_bf16 v[160:163], a[188:191], a[152:155], v[160:163]\n" \
"v_lshrrev_b32_e32 v26, 2, v20\n" \
"v_add_u32_e32 v26, s45, v26\n" \
"v_mfma_f32_16x16x32_bf16 v[160:163], a[252:255], a[216:219], v[160:163]\n" \
"ds_read_b128 a[152:155], v28 offset:12288\n" \
"v_add_u32_e64 v27, s46, v20\n" \
"v_mfma_f32_16x16x32_bf16 v[176:179], a[188:191], a[156:159], v[176:179]\n" \
"ds_read_b128 a[216:219], v29 offset:12288\n" \
"v_add_u32_e64 v21, s43, v27\n" \
"v_mfma_f32_16x16x32_bf16 v[176:179], a[252:255], a[220:223], v[176:179]\n" \
"v_mfma_f32_16x16x32_bf16 v[172:175], a[184:187], a[156:159], v[172:175]\n" \
"v_mfma_f32_16x16x32_bf16 v[172:175], a[248:251], a[220:223], v[172:175]\n" \
"v_mfma_f32_16x16x32_bf16 v[168:171], a[180:183], a[156:159], v[168:171]\n" \
"v_mfma_f32_16x16x32_bf16 v[168:171], a[244:247], a[220:223], v[168:171]\n" \
"v_mfma_f32_16x16x32_bf16 v[164:167], a[176:179], a[156:159], v[164:167]\n" \
"v_mfma_f32_16x16x32_bf16 v[164:167], a[240:243], a[220:223], v[164:167]\n" \
"ds_read_b128 a[156:159], v28 offset:14336\n" \
"ds_read_b128 a[220:223], v29 offset:14336\n" \
"s_waitcnt lgkmcnt(15)\n" \
"s_nop 0\n" \
"v_mfma_f32_16x16x32_bf16 v[184:187], a[160:163], a[128:131], v[184:187]\n" \
"v_bfe_u32 v44, v20, 2, 5\n" \
"ds_read_b64_tr_b16 a[176:177], v36 offset:0\n" \
"v_mfma_f32_16x16x32_bf16 v[184:187], a[224:227], a[192:195], v[184:187]\n" \
"buffer_load_dword v44, v44, s[80:83], s38 offen offset:0 sc1\n" \
"v_mfma_f32_16x16x32_bf16 v[188:191], a[164:167], a[128:131], v[188:191]\n" \
"ds_read_b64_tr_b16 a[178:179], v36 offset:1024\n" \
"v_mfma_f32_16x16x32_bf16 v[188:191], a[228:231], a[192:195], v[188:191]\n" \
"ds_read_b64_tr_b16 a[240:241], v36 offset:8192\n" \
"v_mfma_f32_16x16x32_bf16 v[196:199], a[172:175], a[128:131], v[196:199]\n" \
"ds_read_b64_tr_b16 a[242:243], v36 offset:9216\n" \
"s_waitcnt lgkmcnt(15)\n" \
"v_mfma_f32_16x16x32_bf16 v[196:199], a[236:239], a[192:195], v[196:199]\n" \
"v_mfma_f32_16x16x32_bf16 v[192:195], a[128:131], a[168:171], v[192:195]\n" \
"ds_read_b64_tr_b16 a[180:181], v37 offset:0\n" \
"s_waitcnt lgkmcnt(13)\n" \
"v_mfma_f32_16x16x32_bf16 v[192:195], a[192:195], a[232:235], v[192:195]\n" \
"v_mfma_f32_16x16x32_bf16 v[208:211], a[132:135], a[168:171], v[208:211]\n" \
"ds_read_b64_tr_b16 a[182:183], v37 offset:1024\n" \
"v_mfma_f32_16x16x32_bf16 v[208:211], a[196:199], a[232:235], v[208:211]\n" \
"ds_read_b64_tr_b16 a[244:245], v37 offset:8192\n" \
"v_mfma_f32_16x16x32_bf16 v[212:215], a[172:175], a[132:135], v[212:215]\n" \
"v_mfma_f32_16x16x32_bf16 v[212:215], a[236:239], a[196:199], v[212:215]\n" \
"ds_read_b64_tr_b16 a[246:247], v37 offset:9216\n" \
"v_cvt_pk_bf16_f32 v188, v188, v189\n" \
"v_mfma_f32_16x16x32_bf16 v[204:207], a[164:167], a[132:135], v[204:207]\n" \
"v_cvt_pk_bf16_f32 v189, v190, v191\n" \
"v_cvt_pk_bf16_f32 v190, v198, v199\n" \
"v_mfma_f32_16x16x32_bf16 v[204:207], a[228:231], a[196:199], v[204:207]\n" \
"ds_read_b64_tr_b16 a[184:185], v38 offset:0\n" \
"v_mov_b32_e32 v191, 0x3f80\n" \
"v_mfma_f32_16x16x32_bf16 v[200:203], a[160:163], a[132:135], v[200:203]\n" \
"ds_read_b64_tr_b16 a[186:187], v38 offset:1024\n" \
"v_cvt_pk_bf16_f32 v184, v184, v185\n" \
"v_mfma_f32_16x16x32_bf16 v[200:203], a[224:227], a[196:199], v[200:203]\n" \
"v_cvt_pk_bf16_f32 v185, v186, v187\n" \
"v_cvt_pk_bf16_f32 v186, v196, v197\n" \
"v_mfma_f32_16x16x32_bf16 v[216:219], a[160:163], a[136:139], v[216:219]\n" \
"ds_read_b64_tr_b16 a[248:249], v38 offset:8192\n" \
"v_mov_b32_e64 v187, 0\n" \
"v_mfma_f32_16x16x32_bf16 v[216:219], a[224:227], a[200:203], v[216:219]\n" \
"ds_read_b64_tr_b16 a[250:251], v38 offset:9216\n" \
"v_mfma_f32_16x16x32_bf16 v[220:223], a[164:167], a[136:139], v[220:223]\n" \
"v_mfma_f32_16x16x32_bf16 v[220:223], a[228:231], a[200:203], v[220:223]\n" \
"ds_read_b64_tr_b16 a[188:189], v39 offset:0\n" \
"v_cvt_pk_bf16_f32 v204, v204, v205\n" \
"v_mfma_f32_16x16x32_bf16 v[228:231], a[172:175], a[136:139], v[228:231]\n" \
"v_cvt_pk_bf16_f32 v205, v206, v207\n" \
"v_cvt_pk_bf16_f32 v206, v214, v215\n" \
"v_mfma_f32_16x16x32_bf16 v[228:231], a[236:239], a[200:203], v[228:231]\n" \
"ds_read_b64_tr_b16 a[190:191], v39 offset:1024\n" \
"v_mov_b32_e32 v207, 0x3f80\n" \
"v_mfma_f32_16x16x32_bf16 v[224:227], a[136:139], a[168:171], v[224:227]\n" \
"ds_read_b64_tr_b16 a[252:253], v39 offset:8192\n" \
"v_cvt_pk_bf16_f32 v200, v200, v201\n" \
"v_mfma_f32_16x16x32_bf16 v[224:227], a[200:203], a[232:235], v[224:227]\n" \
"v_cvt_pk_bf16_f32 v201, v202, v203\n" \
"v_cvt_pk_bf16_f32 v202, v212, v213\n" \
"v_mfma_f32_16x16x32_bf16 v[240:243], a[140:143], a[168:171], v[240:243]\n" \
"ds_read_b64_tr_b16 a[254:255], v39 offset:9216\n" \
"v_mov_b32_e64 v203, 0\n" \
"v_mfma_f32_16x16x32_bf16 v[240:243], a[204:207], a[232:235], v[240:243]\n" \
"v_mfma_f32_16x16x32_bf16 v[244:247], a[172:175], a[140:143], v[244:247]\n" \
"v_mfma_f32_16x16x32_bf16 v[244:247], a[236:239], a[204:207], v[244:247]\n" \
"v_cvt_pk_bf16_f32 v220, v220, v221\n" \
"v_cvt_pk_bf16_f32 v221, v222, v223\n" \
"v_mfma_f32_16x16x32_bf16 v[236:239], a[164:167], a[140:143], v[236:239]\n" \
"v_cvt_pk_bf16_f32 v222, v230, v231\n" \
"v_mov_b32_e32 v223, 0x3f80\n" \
"v_mfma_f32_16x16x32_bf16 v[236:239], a[228:231], a[204:207], v[236:239]\n" \
"v_cvt_pk_bf16_f32 v216, v216, v217\n" \
"v_cvt_pk_bf16_f32 v217, v218, v219\n" \
"v_mfma_f32_16x16x32_bf16 v[232:235], a[160:163], a[140:143], v[232:235]\n" \
"v_cvt_pk_bf16_f32 v218, v228, v229\n" \
"v_mov_b32_e64 v219, 0\n" \
"v_mfma_f32_16x16x32_bf16 v[232:235], a[224:227], a[204:207], v[232:235]\n" \
"s_waitcnt lgkmcnt(15)\n" \
"s_nop 0\n" \
"v_mfma_f32_16x16x32_bf16 v[180:183], a[164:167], v[248:251], v[180:183]\n" \
"v_mfma_f32_16x16x32_bf16 v[180:183], a[228:231], v[48:51], v[180:183]\n" \
"v_mfma_f32_16x16x32_bf16 a[80:83], a[172:175], v[248:251], a[80:83]\n" \
"v_mfma_f32_16x16x32_bf16 a[80:83], a[236:239], v[48:51], a[80:83]\n" \
"v_cvt_pk_bf16_f32 v236, v236, v237\n" \
"v_cvt_pk_bf16_f32 v237, v238, v239\n" \
"v_mfma_f32_16x16x32_bf16 v[252:255], v[248:251], a[168:171], v[252:255]\n" \
"v_cvt_pk_bf16_f32 v238, v246, v247\n" \
"v_mov_b32_e32 v239, 0x3f80\n" \
"v_mfma_f32_16x16x32_bf16 v[252:255], v[48:51], a[232:235], v[252:255]\n" \
"v_cvt_pk_bf16_f32 v232, v232, v233\n" \
"v_cvt_pk_bf16_f32 v233, v234, v235\n" \
"s_waitcnt vmcnt(0) lgkmcnt(0)\n" \
"s_barrier\n" \
"v_mov_b32_e32 v45, s62\n" \
"s_mov_b64 exec, 1\n" \
"buffer_store_dword v45, off, s[80:83], s37 offset:32 sc1\n" \
"s_mov_b64 exec, -1\n" \
"v_cmp_ne_u32_e32 vcc, s62, v44\n" \
"s_nop 4\n" \
"s_cbranch_vccnz L_poll160_%=\n" \
"L_pollr160_%=:\n" \
"v_mfma_f32_16x16x32_bf16 v[52:55], a[176:179], a[128:131], v[52:55]\n" \
"s_mov_b32 s73, s50\n" \
"s_sub_u32 m0, s31, 0x10000\n" \
"v_mfma_f32_16x16x32_bf16 v[52:55], a[240:243], a[192:195], v[52:55]\n" \
"buffer_load_dwordx4 v20, s[80:83], s73 offen offset:0 sc1 lds\n" \
"v_mfma_f32_16x16x32_bf16 v[56:59], a[180:183], a[128:131], v[56:59]\n" \
"s_add_u32 s74, s50, 0x4000\n" \
"s_add_u32 s75, s50, 0x8000\n" \
"v_mfma_f32_16x16x32_bf16 v[56:59], a[244:247], a[192:195], v[56:59]\n" \
"s_mov_b32 s70, 0x3e0293ee\n" \
"s_mov_b32 s76, s52\n" \
"v_mfma_f32_16x16x32_bf16 v[60:63], a[184:187], a[128:131], v[60:63]\n" \
"v_mul_f32_e32 v52, s70, v52\n" \
"v_mul_f32_e32 v53, s70, v53\n" \
"v_mfma_f32_16x16x32_bf16 v[60:63], a[248:251], a[192:195], v[60:63]\n" \
"buffer_load_dwordx4 v20, s[80:83], s73 offen offset:1024 sc1 lds\n" \
"v_mfma_f32_16x16x32_bf16 v[64:67], a[188:191], a[128:131], v[64:67]\n" \
"v_mul_f32_e32 v54, s70, v54\n" \
"v_mul_f32_e32 v55, s70, v55\n" \
"v_mfma_f32_16x16x32_bf16 v[64:67], a[252:255], a[192:195], v[64:67]\n" \
"v_cvt_pk_bf16_f32 v52, v52, v53\n" \
"v_mul_f32_e64 v56, s70, v56\n" \
"v_mfma_f32_16x16x32_bf16 v[80:83], a[188:191], a[132:135], v[80:83]\n" \
"v_mul_f32_e32 v60, s70, v60\n" \
"v_mul_f32_e32 v61, s70, v61\n" \
"v_mfma_f32_16x16x32_bf16 v[80:83], a[252:255], a[196:199], v[80:83]\n" \
"buffer_load_dwordx4 v20, s[80:83], s73 offen offset:2048 sc1 lds\n" \
"v_mfma_f32_16x16x32_bf16 v[76:79], a[184:187], a[132:135], v[76:79]\n" \
"v_mul_f32_e32 v62, s70, v62\n" \
"v_mul_f32_e32 v63, s70, v63\n" \
"v_mfma_f32_16x16x32_bf16 v[76:79], a[248:251], a[196:199], v[76:79]\n" \
"v_cvt_pk_bf16_f32 v60, v60, v61\n" \
"v_mul_f32_e64 v57, s70, v57\n" \
"v_mfma_f32_16x16x32_bf16 v[72:75], a[180:183], a[132:135], v[72:75]\n" \
"v_mul_f32_e32 v58, s70, v58\n" \
"v_mul_f32_e32 v59, s70, v59\n" \
"v_mfma_f32_16x16x32_bf16 v[72:75], a[244:247], a[196:199], v[72:75]\n" \
"buffer_load_dwordx4 v20, s[80:83], s73 offen offset:3072 sc1 lds\n" \
"v_mfma_f32_16x16x32_bf16 v[68:71], a[176:179], a[132:135], v[68:71]\n" \
"s_sub_u32 m0, s31, s51\n" \
"v_cvt_pk_bf16_f32 v53, v54, v55\n" \
"v_mfma_f32_16x16x32_bf16 v[68:71], a[240:243], a[196:199], v[68:71]\n" \
"v_mul_f32_e32 v64, s70, v64\n" \
"v_mul_f32_e64 v65, s70, v65\n" \
"v_mfma_f32_16x16x32_bf16 v[84:87], a[176:179], a[136:139], v[84:87]\n" \
"v_mul_f32_e32 v66, s70, v66\n" \
"v_mul_f32_e32 v67, s70, v67\n" \
"v_mfma_f32_16x16x32_bf16 v[84:87], a[240:243], a[200:203], v[84:87]\n" \
"buffer_load_dwordx4 v20, s[80:83], s74 offen offset:0 sc1 lds\n" \
"v_mfma_f32_16x16x32_bf16 v[88:91], a[180:183], a[136:139], v[88:91]\n" \
"v_cvt_pk_bf16_f32 v61, v62, v63\n" \
"v_cvt_pk_bf16_f32 v54, v56, v57\n" \
"v_mfma_f32_16x16x32_bf16 v[88:91], a[244:247], a[200:203], v[88:91]\n" \
"v_cvt_pk_bf16_f32 v55, v58, v59\n" \
"v_cvt_pk_bf16_f32 v62, v64, v65\n" \
"v_mfma_f32_16x16x32_bf16 v[92:95], a[184:187], a[136:139], v[92:95]\n" \
"v_cvt_pk_bf16_f32 v63, v66, v67\n" \
"v_mul_f32_e64 v68, s70, v68\n" \
"v_mfma_f32_16x16x32_bf16 v[92:95], a[248:251], a[200:203], v[92:95]\n" \
"buffer_load_dwordx4 v20, s[80:83], s74 offen offset:1024 sc1 lds\n" \
"v_mfma_f32_16x16x32_bf16 v[96:99], a[188:191], a[136:139], v[96:99]\n" \
"ds_write_b128 v27, v[52:55] offset:0\n" \
"v_mfma_f32_16x16x32_bf16 v[96:99], a[252:255], a[200:203], v[96:99]\n" \
"v_mul_f32_e32 v69, s70, v69\n" \
"v_mul_f32_e32 v76, s70, v76\n" \
"v_mfma_f32_16x16x32_bf16 v[112:115], a[188:191], a[140:143], v[112:115]\n" \
"v_mul_f32_e32 v77, s70, v77\n" \
"v_mul_f32_e32 v70, s70, v70\n" \
"v_mfma_f32_16x16x32_bf16 v[112:115], a[252:255], a[204:207], v[112:115]\n" \
"buffer_load_dwordx4 v20, s[80:83], s74 offen offset:2048 sc1 lds\n" \
"v_mfma_f32_16x16x32_bf16 v[108:111], a[184:187], a[140:143], v[108:111]\n" \
"ds_write_b128 v27, v[60:63] offset:1024\n" \
"v_mfma_f32_16x16x32_bf16 v[108:111], a[248:251], a[204:207], v[108:111]\n" \
"v_mul_f32_e64 v71, s70, v71\n" \
"v_cvt_pk_bf16_f32 v68, v68, v69\n" \
"v_mfma_f32_16x16x32_bf16 v[104:107], a[180:183], a[140:143], v[104:107]\n" \
"v_mul_f32_e32 v78, s70, v78\n" \
"v_mul_f32_e32 v79, s70, v79\n" \
"v_mfma_f32_16x16x32_bf16 v[104:107], a[244:247], a[204:207], v[104:107]\n" \
"buffer_load_dwordx4 v20, s[80:83], s74 offen offset:3072 sc1 lds\n" \
"v_mfma_f32_16x16x32_bf16 v[100:103], a[176:179], a[140:143], v[100:103]\n" \
"s_sub_u32 m0, s31, 0x4000\n" \
"v_cvt_pk_bf16_f32 v76, v76, v77\n" \
"v_mfma_f32_16x16x32_bf16 v[100:103], a[240:243], a[204:207], v[100:103]\n" \
"v_mul_f32_e32 v72, s70, v72\n" \
"v_mul_f32_e32 v73, s70, v73\n" \
"v_mfma_f32_16x16x32_bf16 v[116:119], a[176:179], a[144:147], v[116:119]\n" \
"v_mul_f32_e32 v74, s70, v74\n" \
"v_mul_f32_e32 v75, s70, v75\n" \
"v_mfma_f32_16x16x32_bf16 v[116:119], a[240:243], a[208:211], v[116:119]\n" \
"buffer_load_dwordx4 v20, s[80:83], s75 offen offset:0 sc1 lds\n" \
"v_mfma_f32_16x16x32_bf16 v[120:123], a[180:183], a[144:147], v[120:123]\n" \
"v_cvt_pk_bf16_f32 v69, v70, v71\n" \
"v_mul_f32_e64 v80, s70, v80\n" \
"v_mfma_f32_16x16x32_bf16 v[120:123], a[244:247], a[208:211], v[120:123]\n" \
".fill (L_pb + 36576 - .) / 4, 4, 0xBF800000\n" \
"L_pp9:\n" \
"s_cbranch_execz L_pret\n" \
"v_mul_f32_e32 v81, s70, v81\n" \
"v_mul_f32_e64 v82, s70, v82\n" \
"v_mfma_f32_16x16x32_bf16 v[124:127], a[184:187], a[144:147], v[124:127]\n" \
"v_mul_f32_e64 v83, s70, v83\n" \
"v_cvt_pk_bf16_f32 v77, v78, v79\n" \
"v_mfma_f32_16x16x32_bf16 v[124:127], a[248:251], a[208:211], v[124:127]\n" \
"buffer_load_dwordx4 v20, s[80:83], s75 offen offset:1024 sc1 lds\n" \
"v_mfma_f32_16x16x32_bf16 v[128:131], a[188:191], a[144:147], v[128:131]\n" \
"v_cvt_pk_bf16_f32 v70, v72, v73\n" \
"v_cvt_pk_bf16_f32 v71, v74, v75\n" \
"v_mfma_f32_16x16x32_bf16 v[128:131], a[252:255], a[208:211], v[128:131]\n" \
"v_cvt_pk_bf16_f32 v78, v80, v81\n" \
"v_cvt_pk_bf16_f32 v79, v82, v83\n" \
"v_mfma_f32_16x16x32_bf16 v[144:147], a[188:191], a[148:151], v[144:147]\n" \
"ds_write_b128 v27, v[68:71] offset:4096\n" \
"v_mfma_f32_16x16x32_bf16 v[144:147], a[252:255], a[212:215], v[144:147]\n" \
"buffer_load_dwordx4 v20, s[80:83], s75 offen offset:2048 sc1 lds\n" \
"v_mfma_f32_16x16x32_bf16 v[140:143], a[184:187], a[148:151], v[140:143]\n" \
"v_cvt_pk_bf16_f32 v180, v180, v181\n" \
"v_accvgpr_read_b32 v50, a82\n" \
"v_mfma_f32_16x16x32_bf16 v[140:143], a[248:251], a[212:215], v[140:143]\n" \
"ds_write_b128 v27, v[76:79] offset:5120\n" \
"v_mfma_f32_16x16x32_bf16 v[136:139], a[180:183], a[148:151], v[136:139]\n" \
"v_accvgpr_read_b32 v51, a83\n" \
"v_cvt_pk_bf16_f32 v181, v182, v183\n" \
"v_mfma_f32_16x16x32_bf16 v[136:139], a[244:247], a[212:215], v[136:139]\n" \
"buffer_load_dwordx4 v20, s[80:83], s75 offen offset:3072 sc1 lds\n" \
"v_mfma_f32_16x16x32_bf16 v[132:135], a[176:179], a[148:151], v[132:135]\n" \
"s_mov_b32 m0, s53\n" \
"v_cvt_pk_bf16_f32 v182, v50, v51\n" \
"v_mfma_f32_16x16x32_bf16 v[132:135], a[240:243], a[212:215], v[132:135]\n" \
"v_mov_b32_e32 v183, 0x3f80\n" \
"v_cvt_pk_bf16_f32 v234, v244, v245\n" \
"v_mfma_f32_16x16x32_bf16 v[148:151], a[176:179], a[152:155], v[148:151]\n" \
"v_mov_b32_e32 v235, 0\n" \
"v_cvt_pk_bf16_f32 v54, v192, v193\n" \
"v_mfma_f32_16x16x32_bf16 v[148:151], a[240:243], a[216:219], v[148:151]\n" \
"buffer_load_dwordx4 v20, s[80:83], s76 offen offset:0 sc1 lds\n" \
"v_mfma_f32_16x16x32_bf16 v[152:155], a[180:183], a[152:155], v[152:155]\n" \
"v_cvt_pk_bf16_f32 v55, v194, v195\n" \
"v_cvt_pk_bf16_f32 v56, v208, v209\n" \
"v_mfma_f32_16x16x32_bf16 v[152:155], a[244:247], a[216:219], v[152:155]\n" \
"v_cvt_pk_bf16_f32 v57, v210, v211\n" \
"v_cvt_pk_bf16_f32 v58, v224, v225\n" \
"v_cvt_pk_bf16_f32 v59, v226, v227\n" \
"v_cvt_pk_bf16_f32 v60, v240, v241\n" \
"v_cvt_pk_bf16_f32 v61, v242, v243\n" \
"v_mfma_f32_16x16x32_bf16 v[32:35], v[180:183], v[184:187], 0\n" \
"buffer_load_dwordx4 v20, s[80:83], s76 offen offset:1024 sc1 lds\n" \
"v_mfma_f32_16x16x32_bf16 v[156:159], a[184:187], a[152:155], v[156:159]\n" \
"v_mfma_f32_16x16x32_bf16 v[192:195], v[188:191], v[184:187], 0\n" \
"v_mfma_f32_16x16x32_bf16 v[208:211], v[204:207], v[200:203], 0\n" \
"v_mfma_f32_16x16x32_bf16 v[36:39], v[188:191], v[200:203], 0\n" \
"v_mfma_f32_16x16x32_bf16 v[224:227], v[220:223], v[216:219], 0\n" \
"v_mfma_f32_16x16x32_bf16 v[248:251], v[204:207], v[216:219], 0\n" \
"v_mfma_f32_16x16x32_bf16 v[240:243], v[236:239], v[232:235], 0\n" \
"v_mfma_f32_16x16x32_bf16 v[64:67], v[220:223], v[232:235], 0\n" \
"s_mov_b32 s64, 0xaaaaaaaa\n" \
"s_mov_b32 s65, 0xaaaaaaaa\n" \
"v_mfma_f32_16x16x32_bf16 v[156:159], a[248:251], a[216:219], v[156:159]\n" \
"v_cndmask_b32_e64 v32, v32, v33, s[64:65]\n" \
"v_cndmask_b32_e64 v34, v34, v35, s[64:65]\n" \
"v_mfma_f32_16x16x32_bf16 v[160:163], a[188:191], a[152:155], v[160:163]\n" \
"s_mov_b32 s66, 0xfff0ffff\n" \
"s_mov_b32 s67, 0xf000ff00\n" \
"s_mov_b32 s68, 0xffc0fffc\n" \
"s_mov_b32 s69, 0xc000fc00\n" \
"v_cndmask_b32_e64 v192, v192, v193, s[64:65]\n" \
"v_add_f32_e64 v32, s47, v32\n" \
"v_cndmask_b32_e64 v194, v194, v195, s[64:65]\n" \
"v_add_f32_e64 v34, s47, v34\n" \
"v_cndmask_b32_e64 v208, v208, v209, s[64:65]\n" \
"v_cndmask_b32_e64 v36, v36, v37, s[64:65]\n" \
"v_cndmask_b32_e64 v210, v210, v211, s[64:65]\n" \
"v_cndmask_b32_e64 v38, v38, v39, s[64:65]\n" \
"v_cndmask_b32_e64 v224, v224, v225, s[64:65]\n" \
"v_cndmask_b32_e64 v248, v248, v249, s[64:65]\n" \
"v_cndmask_b32_e64 v226, v226, v227, s[64:65]\n" \
"v_cndmask_b32_e64 v250, v250, v251, s[64:65]\n" \
"v_cndmask_b32_e64 v240, v240, v241, s[64:65]\n" \
"v_cndmask_b32_e64 v64, v64, v65, s[64:65]\n" \
"v_cndmask_b32_e64 v242, v242, v243, s[64:65]\n" \
"v_cndmask_b32_e64 v66, v66, v67, s[64:65]\n" \
"v_cndmask_b32_e64 v192, v32, v192, s[66:67]\n" \
"v_cndmask_b32_e64 v194, v34, v194, s[68:69]\n" \
"v_cndmask_b32_e64 v208, v36, v208, s[66:67]\n" \
"v_cndmask_b32_e64 v210, v38, v210, s[68:69]\n" \
"v_cndmask_b32_e64 v224, v248, v224, s[66:67]\n" \
"v_cndmask_b32_e64 v226, v250, v226, s[68:69]\n" \
"v_cndmask_b32_e64 v240, v64, v240, s[66:67]\n" \
"v_cndmask_b32_e64 v242, v66, v242, s[68:69]\n" \
"v_mfma_f32_16x16x32_bf16 v[160:163], a[252:255], a[216:219], v[160:163]\n" \
"v_max_f32_e32 v193, v192, v194\n" \
"v_max_f32_e32 v209, v208, v210\n" \
"v_max_f32_e32 v225, v224, v226\n" \
"v_max_f32_e32 v241, v240, v242\n" \
"v_mul_f32_e32 v84, s70, v84\n" \
"v_permlane32_swap_b32_e32 v193, v225\n" \
"v_permlane32_swap_b32_e32 v209, v241\n" \
"v_mul_f32_e32 v85, s70, v85\n" \
"v_mul_f32_e32 v92, s70, v92\n" \
"v_mul_f32_e32 v93, s70, v93\n" \
"v_permlane32_swap_b32_e32 v196, v228\n" \
"v_permlane32_swap_b32_e32 v212, v244\n" \
"v_max_f32_e32 v193, v193, v225\n" \
"v_max_f32_e32 v209, v209, v241\n" \
"v_accvgpr_read_b32 v48, a80\n" \
"v_accvgpr_read_b32 v49, a81\n" \
"v_mul_f32_e32 v86, s70, v86\n" \
"v_mul_f32_e32 v87, s70, v87\n" \
"v_cvt_pk_bf16_f32 v84, v84, v85\n" \
"v_mul_f32_e32 v94, s70, v94\n" \
"v_mul_f32_e32 v95, s70, v95\n" \
"v_cvt_pk_bf16_f32 v92, v92, v93\n" \
"v_permlane16_swap_b32_e32 v228, v244\n" \
"v_permlane16_swap_b32_e32 v193, v209\n" \
"v_mfma_f32_16x16x32_bf16 v[176:179], a[188:191], a[156:159], v[176:179]\n" \
"s_mov_b32 s72, 0x3eae1a93\n" \
"v_mul_f32_e32 v88, s70, v88\n" \
"v_mul_f32_e32 v89, s70, v89\n" \
"v_mul_f32_e32 v90, s70, v90\n" \
"v_mul_f32_e32 v91, s70, v91\n" \
"v_cvt_pk_bf16_f32 v85, v86, v87\n" \
"v_mul_f32_e32 v96, s70, v96\n" \
"v_mul_f32_e32 v97, s70, v97\n" \
"v_mul_f32_e32 v98, s70, v98\n" \
"v_mul_f32_e32 v99, s70, v99\n" \
"v_cvt_pk_bf16_f32 v93, v94, v95\n" \
"v_mul_f32_e32 v45, 0xbfb8aa3b, v228\n" \
"v_mfma_f32_16x16x16_bf16 v[48:51], v[182:183], v[46:47], 0\n" \
"v_mfma_f32_16x16x16_bf16 v[196:199], v[190:191], v[46:47], 0\n" \
"v_mfma_f32_16x16x16_bf16 v[212:215], v[206:207], v[46:47], 0\n" \
"v_max_f32_e64 v193, v193, v209\n" \
"v_cvt_pk_bf16_f32 v86, v88, v89\n" \
"v_cvt_pk_bf16_f32 v87, v90, v91\n" \
"v_cvt_pk_bf16_f32 v94, v96, v97\n" \
"v_cvt_pk_bf16_f32 v95, v98, v99\n" \
"v_mfma_f32_16x16x16_bf16 v[228:231], v[222:223], v[46:47], 0\n" \
"v_mul_f32_e32 v193, s72, v193\n" \
"v_mov_b32_e32 v209, v193\n" \
"ds_write_b128 v27, v[84:87] offset:8192\n" \
"v_mfma_f32_16x16x16_bf16 v[244:247], v[238:239], v[46:47], 0\n" \
"v_permlane16_swap_b32_e32 v193, v209\n" \
"s_mov_b32 s71, 0x3e0293ee\n" \
"v_mfma_f32_16x16x32_bf16 v[176:179], a[252:255], a[220:223], v[176:179]\n" \
"v_mov_b32_e32 v225, v193\n" \
"v_mov_b32_e32 v241, v209\n" \
"v_mul_f32_e32 v100, s70, v100\n" \
"v_mul_f32_e64 v101, s70, v101\n" \
"ds_write_b128 v27, v[92:95] offset:9216\n" \
"v_mul_f32_e32 v108, s70, v108\n" \
"v_mul_f32_e32 v109, s70, v109\n" \
"v_pk_mul_f32 v[116:117], v[116:117], s[70:71]\n" \
"v_pk_mul_f32 v[124:125], v[124:125], s[70:71]\n" \
"v_pk_mul_f32 v[132:133], v[132:133], s[70:71]\n" \
"v_pk_mul_f32 v[140:141], v[140:141], s[70:71]\n" \
"v_pk_mul_f32 v[148:149], v[148:149], s[70:71]\n" \
"v_pk_mul_f32 v[156:157], v[156:157], s[70:71]\n" \
"v_permlane32_swap_b32_e32 v193, v225\n" \
"v_permlane32_swap_b32_e32 v209, v241\n" \
"v_mul_f32_e32 v102, s70, v102\n" \
"v_mul_f32_e32 v103, s70, v103\n" \
"v_cvt_pk_bf16_f32 v100, v100, v101\n" \
"v_mul_f32_e32 v110, s70, v110\n" \
"v_mul_f32_e32 v111, s70, v111\n" \
"v_cvt_pk_bf16_f32 v108, v108, v109\n" \
"v_pk_mul_f32 v[118:119], v[118:119], s[70:71]\n" \
"v_cvt_pk_bf16_f32 v116, v116, v117\n" \
"v_pk_mul_f32 v[126:127], v[126:127], s[70:71]\n" \
"v_cvt_pk_bf16_f32 v124, v124, v125\n" \
"v_pk_mul_f32 v[134:135], v[134:135], s[70:71]\n" \
"v_cvt_pk_bf16_f32 v132, v132, v133\n" \
"v_pk_mul_f32 v[142:143], v[142:143], s[70:71]\n" \
"v_cvt_pk_bf16_f32 v140, v140, v141\n" \
"v_pk_mul_f32 v[150:151], v[150:151], s[70:71]\n" \
"v_cvt_pk_bf16_f32 v148, v148, v149\n" \
"v_pk_mul_f32 v[158:159], v[158:159], s[70:71]\n" \
"v_cvt_pk_bf16_f32 v156, v156, v157\n" \
"v_fma_f32 v192, v192, s72, -v193\n" \
"v_fma_f32 v194, v194, s72, -v193\n" \
"v_fma_f32 v208, v208, s72, -v209\n" \
"v_fma_f32 v210, v210, s72, -v209\n" \
"v_mul_f32_e32 v104, s70, v104\n" \
"v_mul_f32_e32 v105, s70, v105\n" \
"v_mul_f32_e32 v106, s70, v106\n" \
"v_mul_f32_e32 v107, s70, v107\n" \
"v_cvt_pk_bf16_f32 v101, v102, v103\n" \
"v_mul_f32_e32 v112, s70, v112\n" \
"v_mul_f32_e32 v113, s70, v113\n" \
"v_mul_f32_e32 v114, s70, v114\n" \
"v_mul_f32_e32 v115, s70, v115\n" \
"v_cvt_pk_bf16_f32 v109, v110, v111\n" \
"v_pk_mul_f32 v[120:121], v[120:121], s[70:71]\n" \
"v_pk_mul_f32 v[122:123], v[122:123], s[70:71]\n" \
"v_cvt_pk_bf16_f32 v117, v118, v119\n" \
"v_pk_mul_f32 v[128:129], v[128:129], s[70:71]\n" \
"v_pk_mul_f32 v[130:131], v[130:131], s[70:71]\n" \
"v_cvt_pk_bf16_f32 v125, v126, v127\n" \
"v_pk_mul_f32 v[136:137], v[136:137], s[70:71]\n" \
"v_pk_mul_f32 v[138:139], v[138:139], s[70:71]\n" \
"v_cvt_pk_bf16_f32 v133, v134, v135\n" \
"v_pk_mul_f32 v[144:145], v[144:145], s[70:71]\n" \
"v_pk_mul_f32 v[146:147], v[146:147], s[70:71]\n" \
"v_cvt_pk_bf16_f32 v141, v142, v143\n" \
"v_pk_mul_f32 v[152:153], v[152:153], s[70:71]\n" \
"v_pk_mul_f32 v[154:155], v[154:155], s[70:71]\n" \
"v_cvt_pk_bf16_f32 v149, v150, v151\n" \
"v_pk_mul_f32 v[160:161], v[160:161], s[70:71]\n" \
"v_pk_mul_f32 v[162:163], v[162:163], s[70:71]\n" \
"v_cvt_pk_bf16_f32 v157, v158, v159\n" \
"v_pk_mul_f32 v[176:177], v[176:177], s[70:71]\n" \
"v_pk_mul_f32 v[178:179], v[178:179], s[70:71]\n" \
"v_exp_f32_e32 v192, v192\n" \
"v_exp_f32_e32 v194, v194\n" \
"v_exp_f32_e32 v208, v208\n" \
"v_exp_f32_e32 v210, v210\n" \
"v_fma_f32 v224, v224, s72, -v225\n" \
"v_fma_f32 v226, v226, s72, -v225\n" \
"v_mfma_f32_16x16x32_bf16 v[172:175], a[184:187], a[156:159], v[172:175]\n" \
"v_cvt_pk_bf16_f32 v102, v104, v105\n" \
"v_cvt_pk_bf16_f32 v103, v106, v107\n" \
"v_cvt_pk_bf16_f32 v110, v112, v113\n" \
"v_cvt_pk_bf16_f32 v111, v114, v115\n" \
"v_cvt_pk_bf16_f32 v118, v120, v121\n" \
"v_cvt_pk_bf16_f32 v119, v122, v123\n" \
"v_cvt_pk_bf16_f32 v126, v128, v129\n" \
"v_cvt_pk_bf16_f32 v127, v130, v131\n" \
"v_cvt_pk_bf16_f32 v134, v136, v137\n" \
"v_cvt_pk_bf16_f32 v135, v138, v139\n" \
"v_cvt_pk_bf16_f32 v142, v144, v145\n" \
"v_cvt_pk_bf16_f32 v143, v146, v147\n" \
"v_cvt_pk_bf16_f32 v150, v152, v153\n" \
"v_cvt_pk_bf16_f32 v151, v154, v155\n" \
"v_cvt_pk_bf16_f32 v158, v160, v161\n" \
"v_cvt_pk_bf16_f32 v159, v162, v163\n" \
"v_exp_f32_e32 v224, v224\n" \
"v_exp_f32_e32 v226, v226\n" \
"v_fma_f32 v240, v240, s72, -v241\n" \
"v_fma_f32 v242, v242, s72, -v241\n" \
"ds_write_b128 v27, v[100:103] offset:12288\n" \
"v_cvt_pk_bf16_f32 v192, v192, v194\n" \
"v_cvt_pk_bf16_f32 v208, v208, v210\n" \
"v_exp_f32_e32 v240, v240\n" \
"v_exp_f32_e32 v242, v242\n" \
"v_cvt_pk_bf16_f32 v68, v48, v49\n" \
"v_cvt_pk_bf16_f32 v69, v50, v51\n" \
"v_cvt_pk_bf16_f32 v70, v196, v197\n" \
"v_cvt_pk_bf16_f32 v71, v198, v199\n" \
"ds_write_b128 v27, v[108:111] offset:13312\n" \
"v_cvt_pk_bf16_f32 v72, v212, v213\n" \
"v_cvt_pk_bf16_f32 v73, v214, v215\n" \
"v_perm_b32 v184, v192, v192, v40\n" \
"v_perm_b32 v185, v192, v192, v41\n" \
"v_perm_b32 v186, v192, v192, v42\n" \
"v_perm_b32 v187, v192, v192, v43\n" \
"v_perm_b32 v200, v208, v208, v40\n" \
"v_perm_b32 v201, v208, v208, v41\n" \
"v_perm_b32 v202, v208, v208, v42\n" \
"v_perm_b32 v203, v208, v208, v43\n" \
"ds_write_b128 v21, v[116:119] offset:16384\n" \
"v_cvt_pk_bf16_f32 v224, v224, v226\n" \
"v_cvt_pk_bf16_f32 v74, v228, v229\n" \
"v_cvt_pk_bf16_f32 v75, v230, v231\n" \
"v_mfma_f32_16x16x32_bf16 v[32:35], v[68:71], v[184:187], 0\n" \
"v_mfma_f32_16x16x32_bf16 v[36:39], v[70:73], v[200:203], 0\n" \
"v_perm_b32 v216, v224, v224, v40\n" \
"v_perm_b32 v217, v224, v224, v41\n" \
"ds_write_b128 v21, v[124:127] offset:17408\n" \
"v_perm_b32 v218, v224, v224, v42\n" \
"v_perm_b32 v219, v224, v224, v43\n" \
"v_cvt_pk_bf16_f32 v240, v240, v242\n" \
"v_mfma_f32_16x16x32_bf16 v[172:175], a[248:251], a[220:223], v[172:175]\n" \
"v_cvt_pk_bf16_f32 v76, v244, v245\n" \
"v_cvt_pk_bf16_f32 v77, v246, v247\n" \
"v_mfma_f32_16x16x32_bf16 v[248:251], v[72:75], v[216:219], 0\n" \
"v_perm_b32 v232, v240, v240, v40\n" \
"v_perm_b32 v233, v240, v240, v41\n" \
"ds_write_b128 v21, v[132:135] offset:20480\n" \
"v_perm_b32 v234, v240, v240, v42\n" \
"v_perm_b32 v235, v240, v240, v43\n" \
"v_pk_mul_f32 v[172:173], v[172:173], s[70:71]\n" \
"v_pk_mul_f32 v[174:175], v[174:175], s[70:71]\n" \
"v_cvt_pk_bf16_f32 v172, v172, v173\n" \
"v_cvt_pk_bf16_f32 v173, v174, v175\n" \
"v_cvt_pk_bf16_f32 v174, v176, v177\n" \
"v_cvt_pk_bf16_f32 v175, v178, v179\n" \
"v_mfma_f32_16x16x32_bf16 v[64:67], v[74:77], v[232:235], 0\n" \
"ds_write_b128 v21, v[140:143] offset:21504\n" \
"v_mfma_f32_16x16x32_bf16 v[168:171], a[180:183], a[156:159], v[168:171]\n" \
"v_cvt_pk_bf16_f32 v52, v252, v253\n" \
"v_cvt_pk_bf16_f32 v53, v254, v255\n" \
"v_mfma_f32_16x16x32_bf16 v[168:171], a[244:247], a[220:223], v[168:171]\n" \
"v_exp_f32_e64 v45, v45\n" \
"v_mfma_f32_16x16x32_bf16 v[192:195], v[52:55], v[184:187], 0\n" \
"ds_write_b128 v21, v[148:151] offset:24576\n" \
"v_mfma_f32_16x16x32_bf16 v[208:211], v[54:57], v[200:203], 0\n" \
"v_mfma_f32_16x16x32_bf16 v[224:227], v[56:59], v[216:219], 0\n" \
"v_mfma_f32_16x16x32_bf16 v[240:243], v[58:61], v[232:235], 0\n" \
"ds_write_b128 v21, v[156:159] offset:25600\n" \
"v_mov_b32_dpp v34, v38 quad_perm:[0,1,2,3] row_mask:0x2 bank_mask:0xf\n" \
"v_pk_mul_f32 v[168:169], v[168:169], s[70:71]\n" \
"v_pk_mul_f32 v[170:171], v[170:171], s[70:71]\n" \
"v_mov_b32_dpp v34, v250 quad_perm:[0,1,2,3] row_mask:0x4 bank_mask:0xf\n" \
"v_add_f32_e64 v45, 1.0, v45\n" \
"v_mfma_f32_16x16x32_bf16 v[164:167], a[176:179], a[156:159], v[164:167]\n" \
"v_mov_b32_dpp v34, v66 quad_perm:[0,1,2,3] row_mask:0x8 bank_mask:0xf\n" \
"v_mov_b32_dpp v32, v36 quad_perm:[0,1,2,3] row_mask:0x2 bank_mask:0xf\n" \
"v_rcp_f32_e64 v45, v45\n" \
"ds_write_b128 v21, v[172:175] offset:29696\n" \
"v_mov_b32_dpp v33, v37 quad_perm:[0,1,2,3] row_mask:0x2 bank_mask:0xf\n" \
"v_rcp_f32_e64 v34, v34\n" \
"v_mov_b32_dpp v32, v248 quad_perm:[0,1,2,3] row_mask:0x4 bank_mask:0xf\n" \
"v_mov_b32_dpp v33, v249 quad_perm:[0,1,2,3] row_mask:0x4 bank_mask:0xf\n" \
"v_mul_f32_e64 v34, v45, v34\n" \
"v_mov_b32_dpp v32, v64 quad_perm:[0,1,2,3] row_mask:0x8 bank_mask:0xf\n" \
"v_mov_b32_dpp v33, v65 quad_perm:[0,1,2,3] row_mask:0x8 bank_mask:0xf\n" \
"v_mul_f32_e32 v32, v34, v32\n" \
"v_mul_f32_e32 v33, v34, v33\n" \
"v_mov_b32_e64 v38, v34\n" \
"v_mfma_f32_16x16x32_bf16 v[164:167], a[240:243], a[220:223], v[164:167]\n" \
"v_sub_f32_e32 v44, 1.0, v45\n" \
"v_permlane16_swap_b32_e32 v34, v38\n" \
"v_mov_b32_e32 v250, v34\n" \
"v_mov_b32_e32 v66, v38\n" \
"v_cvt_pk_bf16_f32 v32, v32, v33\n" \
"v_permlane32_swap_b32_e32 v34, v250\n" \
"v_permlane32_swap_b32_e32 v38, v66\n" \
"v_mul_f32_e64 v192, v34, v192\n" \
"v_pk_mul_f32 v[164:165], v[164:165], s[70:71]\n" \
"v_pk_mul_f32 v[166:167], v[166:167], s[70:71]\n" \
"v_cvt_pk_bf16_f32 v164, v164, v165\n" \
"v_cvt_pk_bf16_f32 v165, v166, v167\n" \
"v_cvt_pk_bf16_f32 v166, v168, v169\n" \
"v_cvt_pk_bf16_f32 v167, v170, v171\n" \
"ds_write_b128 v21, v[164:167] offset:28672\n" \
"v_mul_f32_e32 v193, v34, v193\n" \
"v_mul_f32_e32 v208, v38, v208\n" \
"v_mul_f32_e32 v209, v38, v209\n" \
"v_mul_f32_e32 v224, v250, v224\n" \
"v_mul_f32_e32 v225, v250, v225\n" \
"v_mul_f32_e32 v240, v66, v240\n" \
"v_mul_f32_e32 v241, v66, v241\n" \
"v_mul_f32_e32 v194, v34, v194\n" \
"v_mul_f32_e64 v195, v34, v195\n" \
"v_cvt_pk_bf16_f32 v192, v192, v193\n" \
"v_mul_f32_e32 v210, v38, v210\n" \
"v_mul_f32_e32 v211, v38, v211\n" \
"v_cvt_pk_bf16_f32 v208, v208, v209\n" \
"v_mul_f32_e32 v226, v250, v226\n" \
"v_mul_f32_e32 v227, v250, v227\n" \
"v_cvt_pk_bf16_f32 v224, v224, v225\n" \
"v_mul_f32_e32 v242, v66, v242\n" \
"v_mul_f32_e32 v243, v66, v243\n" \
"v_cvt_pk_bf16_f32 v240, v240, v241\n" \
"v_cvt_pk_bf16_f32 v193, v194, v195\n" \
"s_add_u32 s84, s48, 0x0\n" \
"s_nop 0\n" \
"v_cvt_pk_bf16_f32 v209, v210, v211\n" \
"s_add_u32 s85, s48, 0x24800\n" \
"v_cvt_pk_bf16_f32 v225, v226, v227\n" \
"s_add_u32 s86, s48, 0x49000\n" \
"v_cvt_pk_bf16_f32 v241, v242, v243\n" \
"s_add_u32 s87, s48, 0x6d800\n" \
"ds_write_b32 v26, v44\n" \
"buffer_store_dword v32, v25, s[80:83], s49 offen sc1\n" \
"buffer_store_dwordx2 v[192:193], v24, s[80:83], s84 offen sc1\n" \
"buffer_store_dwordx2 v[208:209], v24, s[80:83], s85 offen sc1\n" \
"buffer_store_dwordx2 v[224:225], v24, s[80:83], s86 offen sc1\n" \
"buffer_store_dwordx2 v[240:241], v24, s[80:83], s87 offen sc1\n" \
"s_branch L_end_%=\n" \
"L_poll60_%=:\n" \
"s_mov_b32 s84, 0\n" \
"L_pl60_%=:\n" \
"s_sleep 1\n" \
"v_bfe_u32 v44, v20, 2, 5\n" \
"buffer_load_dword v44, v44, s[80:83], s38 offen offset:128 sc1\n" \
"s_waitcnt vmcnt(0)\n" \
"v_cmp_ne_u32_e32 vcc, s62, v44\n" \
"s_add_u32 s84, s84, 1\n" \
"s_nop 3\n" \
"s_cbranch_vccz L_pollr60_%=\n" \
"s_cmp_gt_u32 s84, 0x400000\n" \
"s_cbranch_scc0 L_pl60_%=\n" \
"s_branch L_pollr60_%=\n" \
"L_poll160_%=:\n" \
"s_mov_b32 s84, 0\n" \
"L_pl160_%=:\n" \
"s_sleep 1\n" \
"v_bfe_u32 v44, v20, 2, 5\n" \
"buffer_load_dword v44, v44, s[80:83], s38 offen offset:0 sc1\n" \
"s_waitcnt vmcnt(0)\n" \
"v_cmp_ne_u32_e32 vcc, s62, v44\n" \
"s_add_u32 s84, s84, 1\n" \
"s_nop 3\n" \
"s_cbranch_vccz L_pollr160_%=\n" \
"s_cmp_gt_u32 s84, 0x400000\n" \
"s_cbranch_scc0 L_pl160_%=\n" \
"s_branch L_pollr160_%=\n" \
".fill (L_pb + 42440 - .) / 4, 4, 0xBF800000\n" \
"L_w1_%=:\n" \
"s_waitcnt vmcnt(18)\n" \
"s_barrier\n" \
"ds_read_b64_tr_b16 a[160:161], v32 offset:0\n" \
"ds_read_b64_tr_b16 a[162:163], v32 offset:1024\n" \
"ds_read_b128 a[128:131], v28 offset:16384\n" \
"ds_read_b64_tr_b16 a[224:225], v32 offset:8192\n" \
"ds_read_b64_tr_b16 a[226:227], v32 offset:9216\n" \
"ds_read_b128 a[192:195], v29 offset:16384\n" \
"ds_read_b128 a[132:135], v28 offset:18432\n" \
"ds_read_b128 a[196:199], v29 offset:18432\n" \
"ds_read_b64_tr_b16 a[164:165], v33 offset:0\n" \
"ds_read_b64_tr_b16 a[166:167], v33 offset:1024\n" \
"ds_read_b64_tr_b16 a[228:229], v33 offset:8192\n" \
"ds_read_b64_tr_b16 a[230:231], v33 offset:9216\n" \
"ds_read_b128 a[136:139], v28 offset:20480\n" \
"ds_read_b128 a[200:203], v29 offset:20480\n" \
"s_waitcnt lgkmcnt(11)\n" \
"v_mfma_f32_16x16x32_bf16 a[0:3], a[128:131], a[160:163], 0\n" \
"ds_read_b64_tr_b16 a[168:169], v34 offset:0\n" \
"v_mad_i32_i24 v217, v216, -6, v215\n" \
"s_waitcnt lgkmcnt(9)\n" \
"v_mfma_f32_16x16x32_bf16 a[0:3], a[192:195], a[224:227], a[0:3]\n" \
"ds_read_b64_tr_b16 a[170:171], v34 offset:1024\n" \
"v_add_u32_e32 v218, 0xfffffe50, v215\n" \
"s_waitcnt lgkmcnt(9)\n" \
"v_mfma_f32_16x16x32_bf16 a[16:19], a[132:135], a[160:163], 0\n" \
"ds_read_b64_tr_b16 a[232:233], v34 offset:8192\n" \
"s_mul_i32 s35, s6, 0x48\n" \
"s_waitcnt lgkmcnt(9)\n" \
"v_mfma_f32_16x16x32_bf16 a[16:19], a[196:199], a[224:227], a[16:19]\n" \
"ds_read_b64_tr_b16 a[234:235], v34 offset:9216\n" \
"v_lshrrev_b32_e32 v219, 1, v217\n" \
"s_waitcnt lgkmcnt(8)\n" \
"v_mfma_f32_16x16x32_bf16 a[20:23], a[132:135], a[164:167], 0\n" \
"ds_read_b128 a[140:143], v28 offset:22528\n" \
"v_add_u32_e32 v218, s35, v216\n" \
"s_waitcnt lgkmcnt(7)\n" \
"v_mfma_f32_16x16x32_bf16 a[20:23], a[196:199], a[228:231], a[20:23]\n" \
"ds_read_b128 a[204:207], v29 offset:22528\n" \
"v_mul_u32_u24_e32 v219, 0x480, v219\n" \
"v_mfma_f32_16x16x32_bf16 a[4:7], a[128:131], a[164:167], 0\n" \
"ds_read_b64_tr_b16 a[172:173], v35 offset:0\n" \
"v_and_b32_e64 v214, 16, v20\n" \
"v_mfma_f32_16x16x32_bf16 a[4:7], a[192:195], a[228:231], a[4:7]\n" \
"ds_read_b64_tr_b16 a[174:175], v35 offset:1024\n" \
"v_mad_u32_u24 v219, v218, s5, v219\n" \
"s_waitcnt lgkmcnt(9)\n" \
"v_mfma_f32_16x16x32_bf16 a[32:35], a[136:139], a[160:163], 0\n" \
"ds_read_b64_tr_b16 a[236:237], v35 offset:8192\n" \
"v_or_b32_e32 v192, v219, v214\n" \
"s_waitcnt lgkmcnt(9)\n" \
"v_mfma_f32_16x16x32_bf16 a[32:35], a[200:203], a[224:227], a[32:35]\n" \
"ds_read_b64_tr_b16 a[238:239], v35 offset:9216\n" \
"v_mov_b32_e32 v220, 2.0\n" \
"v_mfma_f32_16x16x32_bf16 a[36:39], a[136:139], a[164:167], 0\n" \
"ds_read_b128 a[144:147], v28 offset:0\n" \
"v_mov_b32_e64 v222, 7\n" \
"v_mfma_f32_16x16x32_bf16 a[36:39], a[200:203], a[228:231], a[36:39]\n" \
"ds_read_b128 a[208:211], v29 offset:0\n" \
"v_lshlrev_b32_e32 v220, 7, v218\n" \
"s_waitcnt lgkmcnt(10)\n" \
"v_mfma_f32_16x16x32_bf16 a[40:43], a[136:139], a[168:171], 0\n" \
"ds_read_b128 a[148:151], v28 offset:2048\n" \
"v_lshrrev_b32_e32 v218, 1, v218\n" \
"s_waitcnt lgkmcnt(9)\n" \
"v_mfma_f32_16x16x32_bf16 a[40:43], a[200:203], a[232:235], a[40:43]\n" \
"ds_read_b128 a[212:215], v29 offset:2048\n" \
"v_and_b32_e64 v218, 4, v218\n" \
"v_mfma_f32_16x16x32_bf16 a[24:27], a[132:135], a[168:171], 0\n" \
"ds_read_b128 a[152:155], v28 offset:4096\n" \
"v_and_b32_e64 v216, 2, v216\n" \
"v_mfma_f32_16x16x32_bf16 a[24:27], a[196:199], a[232:235], a[24:27]\n" \
"ds_read_b128 a[216:219], v29 offset:4096\n" \
"s_mul_i32 s7, s6, 0x2400\n" \
"v_mfma_f32_16x16x32_bf16 a[8:11], a[128:131], a[168:171], 0\n" \
"ds_read_b128 a[156:159], v28 offset:6144\n" \
"s_sub_i32 s34, 0x22000, s7\n" \
"v_mfma_f32_16x16x32_bf16 a[8:11], a[192:195], a[232:235], a[8:11]\n" \
"ds_read_b128 a[220:223], v29 offset:6144\n" \
"s_mov_b32 m0, s32\n" \
"s_waitcnt lgkmcnt(13)\n" \
"v_mfma_f32_16x16x32_bf16 a[48:51], a[140:143], a[160:163], 0\n" \
"s_mov_b32 exec_lo, 0xff000000\n" \
"s_mov_b32 exec_hi, 0x0\n" \
"global_load_lds_dword v[184:185], off\n" \
"s_mov_b64 exec, -1\n" \
"s_waitcnt lgkmcnt(12)\n" \
"v_mfma_f32_16x16x32_bf16 a[48:51], a[204:207], a[224:227], a[48:51]\n" \
"ds_read_b64_tr_b16 a[176:177], v36 offset:0\n" \
"v_bitop3_b32 v216, v217, v218, v216 bitop3:0x1e\n" \
"v_mfma_f32_16x16x32_bf16 a[52:55], a[140:143], a[164:167], 0\n" \
"ds_read_b64_tr_b16 a[178:179], v36 offset:1024\n" \
"v_or_b32_e32 v213, 0x24400, v20\n" \
"v_mfma_f32_16x16x32_bf16 a[52:55], a[204:207], a[228:231], a[52:55]\n" \
"s_mov_b32 m0, s32\n" \
"ds_read_b64_tr_b16 a[240:241], v36 offset:8192\n" \
"v_mfma_f32_16x16x32_bf16 a[56:59], a[140:143], a[168:171], 0\n" \
"s_mov_b32 exec_lo, 0xffff\n" \
"s_mov_b32 exec_hi, 0x0\n" \
"global_load_lds_dword v[186:187], off\n" \
"s_mov_b64 exec, -1\n" \
"v_mfma_f32_16x16x32_bf16 a[56:59], a[204:207], a[232:235], a[56:59]\n" \
"ds_read_b64_tr_b16 a[242:243], v36 offset:9216\n" \
"v_lshlrev_b32_e32 v216, 4, v216\n" \
"s_waitcnt lgkmcnt(14)\n" \
"v_mfma_f32_16x16x32_bf16 a[60:63], a[140:143], a[172:175], 0\n" \
"ds_read_b64_tr_b16 a[180:181], v37 offset:0\n" \
"v_add3_u32 v196, v220, s34, v216\n" \
"s_waitcnt lgkmcnt(13)\n" \
"v_mfma_f32_16x16x32_bf16 a[60:63], a[204:207], a[236:239], a[60:63]\n" \
"ds_read_b64_tr_b16 a[182:183], v37 offset:1024\n" \
"v_mov_b32_e32 v216, v213\n" \
"v_mfma_f32_16x16x32_bf16 a[44:47], a[136:139], a[172:175], 0\n" \
"ds_read_b64_tr_b16 a[244:245], v37 offset:8192\n" \
"s_movk_i32 s36, 0x1b0\n" \
"v_mfma_f32_16x16x32_bf16 a[44:47], a[200:203], a[236:239], a[44:47]\n" \
"ds_read_b64_tr_b16 a[246:247], v37 offset:9216\n" \
"v_or_b32_e32 v216, 0x100, v215\n" \
"v_mfma_f32_16x16x32_bf16 a[28:31], a[132:135], a[172:175], 0\n" \
"ds_read_b64_tr_b16 a[184:185], v38 offset:0\n" \
"v_mul_u32_u24_e32 v217, 0x2aab, v216\n" \
"v_mfma_f32_16x16x32_bf16 a[28:31], a[196:199], a[236:239], a[28:31]\n" \
"ds_read_b64_tr_b16 a[186:187], v38 offset:1024\n" \
"v_add_u32_e32 v215, 0xffffff50, v215\n" \
"v_mfma_f32_16x16x32_bf16 a[12:15], a[128:131], a[172:175], 0\n" \
"v_lshrrev_b32_e32 v217, 16, v217\n" \
"v_mov_b32_e64 v220, v215\n" \
"v_mfma_f32_16x16x32_bf16 a[12:15], a[192:195], a[236:239], a[12:15]\n" \
"v_mad_i32_i24 v218, v217, -6, v216\n" \
"v_cmp_gt_u32_e32 vcc, s36, v216\n" \
"s_waitcnt lgkmcnt(15)\n" \
"v_mfma_f32_16x16x32_bf16 a[76:79], a[144:147], a[172:175], 0\n" \
"ds_read_b64_tr_b16 a[248:249], v38 offset:8192\n" \
"v_mov_b32_e64 v221, 7\n" \
"v_mfma_f32_16x16x32_bf16 a[76:79], a[208:211], a[236:239], a[76:79]\n" \
"v_lshrrev_b32_e32 v222, 1, v218\n" \
"v_cndmask_b32_e32 v217, v220, v217, vcc\n" \
"v_mfma_f32_16x16x32_bf16 a[72:75], a[144:147], a[168:171], 0\n" \
"s_movk_i32 s4, 0x48\n" \
"v_add_u32_e32 v220, s35, v217\n" \
"v_mfma_f32_16x16x32_bf16 a[72:75], a[208:211], a[232:235], a[72:75]\n" \
"ds_read_b64_tr_b16 a[250:251], v38 offset:9216\n" \
"v_mul_u32_u24_e32 v222, 0x480, v222\n" \
"v_mfma_f32_16x16x32_bf16 a[68:71], a[144:147], a[164:167], 0\n" \
"v_mad_u32_u24 v222, v220, s5, v222\n" \
"v_cmp_gt_u32_e64 s[4:5], s4, v215\n" \
"v_mfma_f32_16x16x32_bf16 a[68:71], a[208:211], a[228:231], a[68:71]\n" \
"v_cndmask_b32_e32 v215, v221, v218, vcc\n" \
"v_lshrrev_b32_e32 v218, 1, v220\n" \
"v_mfma_f32_16x16x32_bf16 a[64:67], a[144:147], a[160:163], 0\n" \
"v_or_b32_e32 v214, v222, v214\n" \
"v_mov_b32_e32 v222, 2.0\n" \
"v_mfma_f32_16x16x32_bf16 a[64:67], a[208:211], a[224:227], a[64:67]\n" \
"ds_read_b64_tr_b16 a[188:189], v39 offset:0\n" \
"v_and_b32_e64 v218, 4, v218\n" \
"v_mfma_f32_16x16x32_bf16 a[80:83], a[148:151], a[160:163], 0\n" \
"v_and_b32_e64 v217, 2, v217\n" \
"v_bitop3_b32 v215, v215, v218, v217 bitop3:0x1e\n" \
"s_waitcnt lgkmcnt(15)\n" \
"v_mfma_f32_16x16x32_bf16 a[80:83], a[212:215], a[224:227], a[80:83]\n" \
"v_lshlrev_b32_e32 v223, 1, v220\n" \
"v_mov_b32_e64 v224, 2.0\n" \
"v_mfma_f32_16x16x32_bf16 a[84:87], a[148:151], a[164:167], 0\n" \
"ds_read_b64_tr_b16 a[190:191], v39 offset:1024\n" \
"v_cndmask_b32_e64 v193, v222, v214, vcc\n" \
"v_mfma_f32_16x16x32_bf16 a[84:87], a[212:215], a[228:231], a[84:87]\n" \
"v_lshlrev_b32_e32 v214, 7, v220\n" \
"v_lshlrev_b32_e32 v215, 4, v215\n" \
"v_mfma_f32_16x16x32_bf16 a[88:91], a[148:151], a[168:171], 0\n" \
"s_mul_i32 s39, s3, 18\n" \
"v_add3_u32 v214, v214, s34, v215\n" \
"v_mfma_f32_16x16x32_bf16 a[88:91], a[212:215], a[232:235], a[88:91]\n" \
"ds_read_b64_tr_b16 a[252:253], v39 offset:8192\n" \
"v_cndmask_b32_e64 v195, v224, v223, s[4:5]\n" \
"v_mfma_f32_16x16x32_bf16 a[92:95], a[148:151], a[172:175], 0\n" \
"s_movk_i32 s4, 0x1f8\n" \
"v_mov_b32_e64 v215, v213\n" \
"v_mfma_f32_16x16x32_bf16 a[92:95], a[212:215], a[236:239], a[92:95]\n" \
"s_add_i32 s35, s35, s39\n" \
"v_cmp_gt_u32_e32 vcc, s4, v216\n" \
"v_mfma_f32_16x16x32_bf16 a[108:111], a[152:155], a[172:175], 0\n" \
"ds_read_b64_tr_b16 a[254:255], v39 offset:9216\n" \
"v_add_u32_e64 v212, s35, v212\n" \
"v_mfma_f32_16x16x32_bf16 a[108:111], a[216:219], a[236:239], a[108:111]\n" \
"v_mov_b32_e32 v219, 2.0\n" \
"v_cndmask_b32_e32 v197, v215, v214, vcc\n" \
"v_mfma_f32_16x16x32_bf16 a[104:107], a[152:155], a[168:171], 0\n" \
"s_movk_i32 s35, 0x120\n" \
"v_mul_u32_u24_e32 v214, 0xd80, v212\n" \
"v_mfma_f32_16x16x32_bf16 a[104:107], a[216:219], a[232:235], a[104:107]\n" \
"v_cmp_gt_u32_e32 vcc, s35, v20\n" \
"v_lshrrev_b32_e64 v215, 1, v212\n" \
"v_mfma_f32_16x16x32_bf16 a[100:103], a[152:155], a[164:167], 0\n" \
"s_and_b32 s4, s2, 7\n" \
"v_cndmask_b32_e32 v194, v219, v214, vcc\n" \
"v_mfma_f32_16x16x32_bf16 a[100:103], a[216:219], a[228:231], a[100:103]\n" \
"v_lshlrev_b32_e32 v214, 7, v212\n" \
"v_lshrrev_b32_e32 v212, 2, v212\n" \
"v_mfma_f32_16x16x32_bf16 a[96:99], a[152:155], a[160:163], 0\n" \
"v_and_b32_e64 v212, 2, v212\n" \
"v_and_or_b32 v212, v215, 1, v212\n" \
"v_mfma_f32_16x16x32_bf16 a[96:99], a[216:219], a[224:227], a[96:99]\n" \
"s_lshl_b32 s5, s4, 2\n" \
"s_bfe_u32 s38, s2, 0x20003\n" \
"s_waitcnt lgkmcnt(15)\n" \
"v_mfma_f32_16x16x32_bf16 a[112:115], a[156:159], a[160:163], 0\n" \
"v_lshlrev_b32_e32 v212, 5, v212\n" \
"s_movk_i32 s35, 0x60\n" \
"v_mfma_f32_16x16x32_bf16 a[112:115], a[220:223], a[224:227], a[112:115]\n" \
"s_or_b32 s5, s5, s38\n" \
"v_bitop3_b32 v212, v212, s35, v214 bitop3:0x36\n" \
"v_mfma_f32_16x16x32_bf16 a[116:119], a[156:159], a[164:167], 0\n" \
"s_lshr_b32 s36, s2, 3\n" \
"v_add_u32_e64 v212, s34, v212\n" \
"v_mfma_f32_16x16x32_bf16 a[116:119], a[220:223], a[228:231], a[116:119]\n" \
"s_mul_i32 s34, s5, 0x12000\n" \
"s_or_b32 s40, s34, 0x6000000\n" \
"v_mfma_f32_16x16x32_bf16 a[120:123], a[156:159], a[168:171], 0\n" \
"s_mul_i32 s34, s36, 0x90000\n" \
"s_lshl_b32 s36, s3, 14\n" \
"v_mfma_f32_16x16x32_bf16 a[120:123], a[220:223], a[232:235], a[120:123]\n" \
"s_add_i32 s34, s34, s36\n" \
"s_lshl_b32 s38, s5, 8\n" \
"v_mfma_f32_16x16x32_bf16 a[124:127], a[156:159], a[172:175], 0\n" \
"s_add_i32 s41, s34, 0x4240000\n" \
"s_mul_i32 s34, s5, 36\n" \
"v_mfma_f32_16x16x32_bf16 a[124:127], a[220:223], a[236:239], a[124:127]\n" \
"s_waitcnt vmcnt(4) lgkmcnt(0)\n" \
"s_barrier\n" \
"v_mfma_f32_16x16x32_bf16 v[52:55], a[128:131], a[176:179], 0\n" \
"s_add_u32 m0, s30, 0x0\n" \
"s_movk_i32 s84, 0x100\n" \
"v_mfma_f32_16x16x32_bf16 v[52:55], a[192:195], a[240:243], v[52:55]\n" \
"buffer_load_dwordx4 v22, s[64:67], s84 offen offset:0 lds\n" \
"v_mfma_f32_16x16x32_bf16 v[56:59], a[128:131], a[180:183], 0\n" \
"ds_read_b64_tr_b16 a[160:161], v32 offset:32768\n" \
"s_add_i32 s5, s7, s40\n" \
"v_mfma_f32_16x16x32_bf16 v[56:59], a[192:195], a[244:247], v[56:59]\n" \
"s_movk_i32 s84, 0x1d00\n" \
"v_mfma_f32_16x16x32_bf16 v[60:63], a[128:131], a[184:187], 0\n" \
"buffer_load_dwordx4 v23, s[64:67], s84 offen offset:1024 lds\n" \
"v_mfma_f32_16x16x32_bf16 v[60:63], a[192:195], a[248:251], v[60:63]\n" \
"ds_read_b64_tr_b16 a[162:163], v32 offset:33792\n" \
"s_addk_i32 s5, 0x2000\n" \
"v_mfma_f32_16x16x32_bf16 v[64:67], a[128:131], a[188:191], 0\n" \
"ds_read_b64_tr_b16 a[224:225], v32 offset:40960\n" \
"s_mov_b32 s37, 2.0\n" \
"v_mfma_f32_16x16x32_bf16 v[64:67], a[192:195], a[252:255], v[64:67]\n" \
"ds_read_b128 a[128:131], v28 offset:49152\n" \
"s_or_b32 s38, s38, 0x5be8000\n" \
"v_mfma_f32_16x16x32_bf16 v[80:83], a[132:135], a[188:191], 0\n" \
"ds_read_b128 a[192:195], v29 offset:49152\n" \
"v_mfma_f32_16x16x32_bf16 v[80:83], a[196:199], a[252:255], v[80:83]\n" \
"s_movk_i32 s84, 0x3900\n" \
"v_mfma_f32_16x16x32_bf16 v[76:79], a[132:135], a[184:187], 0\n" \
"buffer_load_dwordx4 v22, s[64:67], s84 offen offset:2048 lds\n" \
"v_mfma_f32_16x16x32_bf16 v[76:79], a[196:199], a[248:251], v[76:79]\n" \
"s_movk_i32 s84, 0x5500\n" \
"v_mfma_f32_16x16x32_bf16 v[72:75], a[132:135], a[180:183], 0\n" \
"buffer_load_dwordx4 v23, s[64:67], s84 offen offset:3072 lds\n" \
"v_mfma_f32_16x16x32_bf16 v[72:75], a[196:199], a[244:247], v[72:75]\n" \
"s_add_u32 m0, s31, 0x0\n" \
"s_mov_b32 s84, 0x100000\n" \
"v_mfma_f32_16x16x32_bf16 v[68:71], a[132:135], a[176:179], 0\n" \
"buffer_load_dwordx4 v26, s[68:71], s84 offen offset:0 lds\n" \
"v_mfma_f32_16x16x32_bf16 v[68:71], a[196:199], a[240:243], v[68:71]\n" \
"ds_read_b128 a[132:135], v28 offset:51200\n" \
"v_mfma_f32_16x16x32_bf16 v[84:87], a[136:139], a[176:179], 0\n" \
"ds_read_b128 a[196:199], v29 offset:51200\n" \
"v_mfma_f32_16x16x32_bf16 v[84:87], a[200:203], a[240:243], v[84:87]\n" \
"s_addk_i32 s84, 0x7c00\n" \
"v_mfma_f32_16x16x32_bf16 v[88:91], a[136:139], a[180:183], 0\n" \
"buffer_load_dwordx4 v26, s[68:71], s84 offen offset:1024 lds\n" \
"v_mfma_f32_16x16x32_bf16 v[88:91], a[200:203], a[244:247], v[88:91]\n" \
"ds_read_b64_tr_b16 a[226:227], v32 offset:41984\n" \
"v_mfma_f32_16x16x32_bf16 v[92:95], a[136:139], a[184:187], 0\n" \
"s_addk_i32 s84, 0x7c00\n" \
"v_mfma_f32_16x16x32_bf16 v[92:95], a[200:203], a[248:251], v[92:95]\n" \
"buffer_load_dwordx4 v27, s[68:71], s84 offen offset:2048 lds\n" \
"v_mfma_f32_16x16x32_bf16 v[96:99], a[136:139], a[188:191], 0\n" \
"ds_read_b64_tr_b16 a[164:165], v33 offset:32768\n" \
"v_mfma_f32_16x16x32_bf16 v[96:99], a[200:203], a[252:255], v[96:99]\n" \
"ds_read_b128 a[136:139], v28 offset:53248\n" \
"v_mfma_f32_16x16x32_bf16 v[112:115], a[140:143], a[188:191], 0\n" \
"ds_read_b128 a[200:203], v29 offset:53248\n" \
"v_mfma_f32_16x16x32_bf16 v[112:115], a[204:207], a[252:255], v[112:115]\n" \
"s_addk_i32 s84, 0x7c00\n" \
"v_mfma_f32_16x16x32_bf16 v[108:111], a[140:143], a[184:187], 0\n" \
"buffer_load_dwordx4 v27, s[68:71], s84 offen offset:3072 lds\n" \
"v_mfma_f32_16x16x32_bf16 v[108:111], a[204:207], a[248:251], v[108:111]\n" \
"s_add_u32 m0, s30, 0x1000\n" \
"s_mov_b32 s84, 0x8100\n" \
"v_mfma_f32_16x16x32_bf16 v[104:107], a[140:143], a[180:183], 0\n" \
"buffer_load_dwordx4 v22, s[64:67], s84 offen offset:0 lds\n" \
"v_mfma_f32_16x16x32_bf16 v[104:107], a[204:207], a[244:247], v[104:107]\n" \
"ds_read_b64_tr_b16 a[166:167], v33 offset:33792\n" \
"v_mfma_f32_16x16x32_bf16 v[100:103], a[140:143], a[176:179], 0\n" \
"ds_read_b64_tr_b16 a[228:229], v33 offset:40960\n" \
"v_mfma_f32_16x16x32_bf16 v[100:103], a[204:207], a[240:243], v[100:103]\n" \
"ds_read_b128 a[140:143], v28 offset:55296\n" \
"v_mfma_f32_16x16x32_bf16 v[116:119], a[144:147], a[176:179], 0\n" \
"ds_read_b128 a[204:207], v29 offset:55296\n" \
"v_mfma_f32_16x16x32_bf16 v[116:119], a[208:211], a[240:243], v[116:119]\n" \
"s_addk_i32 s84, 0x1c00\n" \
"v_mfma_f32_16x16x32_bf16 v[120:123], a[144:147], a[180:183], 0\n" \
"buffer_load_dwordx4 v23, s[64:67], s84 offen offset:1024 lds\n" \
"v_mfma_f32_16x16x32_bf16 v[120:123], a[208:211], a[244:247], v[120:123]\n" \
"s_addk_i32 s84, 0x1c00\n" \
"v_mfma_f32_16x16x32_bf16 v[124:127], a[144:147], a[184:187], 0\n" \
"buffer_load_dwordx4 v22, s[64:67], s84 offen offset:2048 lds\n" \
"v_mfma_f32_16x16x32_bf16 v[124:127], a[208:211], a[248:251], v[124:127]\n" \
"s_addk_i32 s84, 0x1c00\n" \
"s_nop 0\n" \
"v_mfma_f32_16x16x32_bf16 v[128:131], a[144:147], a[188:191], 0\n" \
"buffer_load_dwordx4 v23, s[64:67], s84 offen offset:3072 lds\n" \
"v_mfma_f32_16x16x32_bf16 v[128:131], a[208:211], a[252:255], v[128:131]\n" \
"ds_read_b128 a[144:147], v28 offset:32768\n" \
"v_mfma_f32_16x16x32_bf16 v[144:147], a[148:151], a[188:191], 0\n" \
"ds_read_b128 a[208:211], v29 offset:32768\n" \
"v_mfma_f32_16x16x32_bf16 v[144:147], a[212:215], a[252:255], v[144:147]\n" \
"s_add_u32 m0, s31, 0x4000\n" \
"s_mov_b32 s84, 0x100000\n" \
"v_mfma_f32_16x16x32_bf16 v[140:143], a[148:151], a[184:187], 0\n" \
"buffer_load_dwordx4 v26, s[72:75], s84 offen offset:0 lds\n" \
"v_mfma_f32_16x16x32_bf16 v[140:143], a[212:215], a[248:251], v[140:143]\n" \
"ds_read_b64_tr_b16 a[230:231], v33 offset:41984\n" \
"v_mfma_f32_16x16x32_bf16 v[136:139], a[148:151], a[180:183], 0\n" \
"s_addk_i32 s84, 0x7c00\n" \
"s_nop 0\n" \
"v_mfma_f32_16x16x32_bf16 v[136:139], a[212:215], a[244:247], v[136:139]\n" \
"buffer_load_dwordx4 v26, s[72:75], s84 offen offset:1024 lds\n" \
"v_mfma_f32_16x16x32_bf16 v[132:135], a[148:151], a[176:179], 0\n" \
"ds_read_b64_tr_b16 a[168:169], v34 offset:32768\n" \
"v_mfma_f32_16x16x32_bf16 v[132:135], a[212:215], a[240:243], v[132:135]\n" \
"ds_read_b128 a[148:151], v28 offset:34816\n" \
".fill (L_pb + 44768 - .) / 4, 4, 0xBF800000\n" \
"L_pp11:\n" \
"s_cbranch_execz L_pret\n" \
"s_nop 0\n" \
"v_mfma_f32_16x16x32_bf16 v[148:151], a[152:155], a[176:179], 0\n" \
"ds_read_b128 a[212:215], v29 offset:34816\n" \
"v_mfma_f32_16x16x32_bf16 v[148:151], a[216:219], a[240:243], v[148:151]\n" \
"ds_read_b64_tr_b16 a[170:171], v34 offset:33792\n" \
"v_mfma_f32_16x16x32_bf16 v[152:155], a[152:155], a[180:183], 0\n" \
"ds_read_b64_tr_b16 a[232:233], v34 offset:40960\n" \
"v_mfma_f32_16x16x32_bf16 v[152:155], a[216:219], a[244:247], v[152:155]\n" \
"ds_read_b64_tr_b16 a[234:235], v34 offset:41984\n" \
"v_mfma_f32_16x16x32_bf16 v[156:159], a[152:155], a[184:187], 0\n" \
"ds_read_b64_tr_b16 a[172:173], v35 offset:32768\n" \
"v_mfma_f32_16x16x32_bf16 v[156:159], a[216:219], a[248:251], v[156:159]\n" \
"ds_read_b64_tr_b16 a[174:175], v35 offset:33792\n" \
"v_mfma_f32_16x16x32_bf16 v[160:163], a[152:155], a[188:191], 0\n" \
"ds_read_b64_tr_b16 a[236:237], v35 offset:40960\n" \
"v_mfma_f32_16x16x32_bf16 v[160:163], a[216:219], a[252:255], v[160:163]\n" \
"ds_read_b128 a[152:155], v28 offset:36864\n" \
"v_mfma_f32_16x16x32_bf16 v[176:179], a[156:159], a[188:191], 0\n" \
"ds_read_b128 a[216:219], v29 offset:36864\n" \
"v_mfma_f32_16x16x32_bf16 v[176:179], a[220:223], a[252:255], v[176:179]\n" \
"ds_read_b64_tr_b16 a[238:239], v35 offset:41984\n" \
"v_mfma_f32_16x16x32_bf16 v[172:175], a[156:159], a[184:187], 0\n" \
"s_cmp_lg_u32 s3, 0\n" \
"s_cselect_b32 s36, s37, s5\n" \
"v_mfma_f32_16x16x32_bf16 v[172:175], a[220:223], a[248:251], v[172:175]\n" \
"s_lshl_b32 s5, s6, 2\n" \
"s_or_b32 s5, s38, s5\n" \
"v_mfma_f32_16x16x32_bf16 v[168:171], a[156:159], a[180:183], 0\n" \
"s_lshl_b32 s35, s3, 10\n" \
"v_mfma_f32_16x16x32_bf16 v[168:171], a[220:223], a[244:247], v[168:171]\n" \
"s_cmpk_lt_u32 s3, 3\n" \
"s_cselect_b32 s37, s37, s5\n" \
"v_mfma_f32_16x16x32_bf16 v[164:167], a[156:159], a[176:179], 0\n" \
"s_lshl_b32 s5, s4, 16\n" \
"s_lshl_b32 s4, s4, 11\n" \
"v_mfma_f32_16x16x32_bf16 v[164:167], a[220:223], a[240:243], v[164:167]\n" \
"ds_read_b128 a[156:159], v28 offset:38912\n" \
"ds_read_b128 a[220:223], v29 offset:38912\n" \
"s_waitcnt lgkmcnt(15)\n" \
"v_mfma_f32_16x16x32_bf16 a[0:3], a[128:131], a[160:163], a[0:3]\n" \
"s_addk_i32 s84, 0x7c00\n" \
"v_mfma_f32_16x16x32_bf16 a[0:3], a[192:195], a[224:227], a[0:3]\n" \
"buffer_load_dwordx4 v27, s[72:75], s84 offen offset:2048 lds\n" \
"v_mfma_f32_16x16x32_bf16 a[4:7], a[128:131], a[164:167], a[4:7]\n" \
"ds_read_b64_tr_b16 a[176:177], v36 offset:32768\n" \
"v_cndmask_b32_e32 v198, v213, v212, vcc\n" \
"s_waitcnt lgkmcnt(15)\n" \
"v_mfma_f32_16x16x32_bf16 a[4:7], a[192:195], a[228:231], a[4:7]\n" \
"s_addk_i32 s84, 0x7c00\n" \
"s_waitcnt lgkmcnt(11)\n" \
"v_mfma_f32_16x16x32_bf16 a[8:11], a[128:131], a[168:171], a[8:11]\n" \
"buffer_load_dwordx4 v27, s[72:75], s84 offen offset:3072 lds\n" \
"s_waitcnt lgkmcnt(9)\n" \
"v_mfma_f32_16x16x32_bf16 a[8:11], a[192:195], a[232:235], a[8:11]\n" \
"ds_read_b64_tr_b16 a[178:179], v36 offset:33792\n" \
"v_or_b32_e32 v212, s35, v20\n" \
"s_waitcnt lgkmcnt(8)\n" \
"v_mfma_f32_16x16x32_bf16 a[12:15], a[128:131], a[172:175], a[12:15]\n" \
"ds_read_b64_tr_b16 a[240:241], v36 offset:40960\n" \
"s_or_b32 s39, s40, s35\n" \
"s_waitcnt lgkmcnt(5)\n" \
"v_mfma_f32_16x16x32_bf16 a[12:15], a[192:195], a[236:239], a[12:15]\n" \
"s_or_b32 s55, s41, s4\n" \
"v_or_b32_e32 v199, 0x22000, v212\n" \
"v_mfma_f32_16x16x32_bf16 a[28:31], a[132:135], a[172:175], a[28:31]\n" \
"s_add_i32 s35, s39, s7\n" \
"s_add_i32 s54, s41, s5\n" \
"v_mfma_f32_16x16x32_bf16 a[28:31], a[196:199], a[236:239], a[28:31]\n" \
"ds_read_b64_tr_b16 a[242:243], v36 offset:41984\n" \
"s_add_i32 s55, s55, 0x80000\n" \
"v_mfma_f32_16x16x32_bf16 a[24:27], a[132:135], a[168:171], a[24:27]\n" \
"v_mov_b32_e32 v215, 0xffff\n" \
"s_and_b32 s4, s3, 1\n" \
"v_mfma_f32_16x16x32_bf16 a[24:27], a[196:199], a[232:235], a[24:27]\n" \
"s_lshl_b32 s5, s4, 4\n" \
"v_lshrrev_b32_e32 v212, 4, v20\n" \
"v_mfma_f32_16x16x32_bf16 a[20:23], a[132:135], a[164:167], a[20:23]\n" \
"v_lshrrev_b32_e32 v218, 6, v20\n" \
"v_and_b32_e32 v219, 12, v218\n" \
"v_mfma_f32_16x16x32_bf16 a[20:23], a[196:199], a[228:231], a[20:23]\n" \
"ds_read_b64_tr_b16 a[180:181], v37 offset:32768\n" \
"v_and_or_b32 v214, v212, 15, s5\n" \
"v_mfma_f32_16x16x32_bf16 a[16:19], a[132:135], a[160:163], a[16:19]\n" \
"v_cmp_gt_u32_e64 vcc, v219, v214\n" \
"v_mov_b32_e32 v220, 0xffff0000\n" \
"v_mfma_f32_16x16x32_bf16 a[16:19], a[196:199], a[224:227], a[16:19]\n" \
"s_lshl_b32 s5, s2, 2\n" \
"v_cndmask_b32_e64 v217, v215, 0, vcc\n" \
"v_mfma_f32_16x16x32_bf16 a[32:35], a[136:139], a[160:163], a[32:35]\n" \
"s_mov_b32 m0, s32\n" \
"ds_read_b64_tr_b16 a[182:183], v37 offset:33792\n" \
"v_mfma_f32_16x16x32_bf16 a[32:35], a[200:203], a[224:227], a[32:35]\n" \
"s_mov_b32 exec_lo, 0x0\n" \
"s_mov_b32 exec_hi, 0xff\n" \
"global_load_lds_dword v[184:185], off\n" \
"s_mov_b64 exec, -1\n" \
"v_mfma_f32_16x16x32_bf16 a[36:39], a[136:139], a[164:167], a[36:39]\n" \
"v_cmp_lt_u32_e32 vcc, v219, v214\n" \
"v_mov_b32_e32 v213, s23\n" \
"v_mfma_f32_16x16x32_bf16 a[36:39], a[200:203], a[228:231], a[36:39]\n" \
"ds_read_b64_tr_b16 a[244:245], v37 offset:40960\n" \
"s_or_b32 s5, s5, s3\n" \
"v_mfma_f32_16x16x32_bf16 a[40:43], a[136:139], a[168:171], a[40:43]\n" \
"v_cndmask_b32_e32 v221, 0, v220, vcc\n" \
"v_or_b32_e64 v12, v217, v221\n" \
"v_mfma_f32_16x16x32_bf16 a[40:43], a[200:203], a[232:235], a[40:43]\n" \
"s_mov_b32 m0, s32\n" \
"v_or_b32_e32 v217, 2, v219\n" \
"v_mfma_f32_16x16x32_bf16 a[44:47], a[136:139], a[172:175], a[44:47]\n" \
"s_mov_b32 exec_lo, 0xffff0000\n" \
"s_mov_b32 exec_hi, 0x0\n" \
"global_load_lds_dword v[186:187], off\n" \
"s_mov_b64 exec, -1\n" \
"v_mfma_f32_16x16x32_bf16 a[44:47], a[200:203], a[236:239], a[44:47]\n" \
"ds_read_b64_tr_b16 a[246:247], v37 offset:41984\n" \
"v_cmp_gt_u32_e64 vcc, v217, v214\n" \
"v_mfma_f32_16x16x32_bf16 a[60:63], a[140:143], a[172:175], a[60:63]\n" \
"v_mov_b32_e32 v216, s25\n" \
"s_movk_i32 s40, 0xf400\n" \
"v_mfma_f32_16x16x32_bf16 a[60:63], a[204:207], a[236:239], a[60:63]\n" \
"v_cndmask_b32_e64 v221, v215, 0, vcc\n" \
"v_cmp_lt_u32_e64 vcc, v217, v214\n" \
"v_mfma_f32_16x16x32_bf16 a[56:59], a[140:143], a[168:171], a[56:59]\n" \
"ds_read_b64_tr_b16 a[184:185], v38 offset:32768\n" \
"s_lshl_b32 s4, s4, 6\n" \
"v_mfma_f32_16x16x32_bf16 a[56:59], a[204:207], a[232:235], a[56:59]\n" \
"v_cndmask_b32_e32 v217, 0, v220, vcc\n" \
"v_or_b32_e64 v13, v221, v217\n" \
"v_mfma_f32_16x16x32_bf16 a[52:55], a[140:143], a[164:167], a[52:55]\n" \
"v_or_b32_e32 v217, 16, v219\n" \
"v_cmp_gt_u32_e32 vcc, v217, v214\n" \
"v_mfma_f32_16x16x32_bf16 a[52:55], a[204:207], a[228:231], a[52:55]\n" \
"ds_read_b64_tr_b16 a[186:187], v38 offset:33792\n" \
"v_cndmask_b32_e64 v221, v215, 0, vcc\n" \
"v_mfma_f32_16x16x32_bf16 a[48:51], a[140:143], a[160:163], a[48:51]\n" \
"v_cmp_lt_u32_e32 vcc, v217, v214\n" \
"s_nop 1\n" \
"v_cndmask_b32_e64 v217, 0, v220, vcc\n" \
"v_mfma_f32_16x16x32_bf16 a[48:51], a[204:207], a[224:227], a[48:51]\n" \
"v_or_b32_e32 v14, v221, v217\n" \
"v_or_b32_e32 v217, 18, v219\n" \
"v_mfma_f32_16x16x32_bf16 a[64:67], a[144:147], a[160:163], a[64:67]\n" \
"ds_read_b64_tr_b16 a[248:249], v38 offset:40960\n" \
"v_cmp_gt_u32_e64 vcc, v217, v214\n" \
"v_mfma_f32_16x16x32_bf16 a[64:67], a[208:211], a[224:227], a[64:67]\n" \
"s_nop 0\n" \
"v_cndmask_b32_e64 v215, v215, 0, vcc\n" \
"v_cmp_lt_u32_e32 vcc, v217, v214\n" \
"v_mfma_f32_16x16x32_bf16 a[68:71], a[144:147], a[164:167], a[68:71]\n" \
"s_nop 0\n" \
"v_cndmask_b32_e32 v214, 0, v220, vcc\n" \
"v_or_b32_e64 v15, v215, v214\n" \
"v_mfma_f32_16x16x32_bf16 a[68:71], a[208:211], a[228:231], a[68:71]\n" \
"ds_read_b64_tr_b16 a[250:251], v38 offset:41984\n" \
"v_mad_u64_u32 v[214:215], s[6:7], s5, 42, v[212:213]\n" \
"v_mfma_f32_16x16x32_bf16 a[72:75], a[144:147], a[168:171], a[72:75]\n" \
"s_movk_i32 s5, 0x1440\n" \
"v_cmp_gt_u32_e32 vcc, s5, v214\n" \
"v_mfma_f32_16x16x32_bf16 a[72:75], a[208:211], a[232:235], a[72:75]\n" \
"v_min_u32_e32 v217, 0xa43f, v214\n" \
"v_mov_b32_e64 v214, s24\n" \
"v_mfma_f32_16x16x32_bf16 a[76:79], a[144:147], a[172:175], a[76:79]\n" \
"v_cndmask_b32_e32 v215, v216, v213, vcc\n" \
"v_mov_b32_e32 v213, s22\n" \
"v_mfma_f32_16x16x32_bf16 a[76:79], a[208:211], a[236:239], a[76:79]\n" \
"ds_read_b64_tr_b16 a[188:189], v39 offset:32768\n" \
"s_movk_i32 s6, 0xffee\n" \
"v_mfma_f32_16x16x32_bf16 a[92:95], a[148:151], a[172:175], a[92:95]\n" \
"v_cndmask_b32_e32 v214, v214, v213, vcc\n" \
"v_add_u32_e32 v213, 0xffffebc0, v217\n" \
"v_mfma_f32_16x16x32_bf16 a[92:95], a[212:215], a[236:239], a[92:95]\n" \
"s_lshl_b32 s5, s2, 5\n" \
"v_cndmask_b32_e32 v213, v213, v217, vcc\n" \
"v_mfma_f32_16x16x32_bf16 a[88:91], a[148:151], a[168:171], a[88:91]\n" \
"ds_read_b64_tr_b16 a[190:191], v39 offset:33792\n" \
"v_lshlrev_b32_e64 v216, 7, v213\n" \
"v_mfma_f32_16x16x32_bf16 a[88:91], a[212:215], a[232:235], a[88:91]\n" \
"v_mul_u32_u24_e32 v213, 0xe39, v212\n" \
"v_mov_b32_e64 v217, 0\n" \
"v_mfma_f32_16x16x32_bf16 a[84:87], a[148:151], a[164:167], a[84:87]\n" \
"v_lshrrev_b32_e64 v213, 16, v213\n" \
"v_lshl_add_u64 v[16:17], v[214:215], 0, v[216:217]\n" \
"v_mfma_f32_16x16x32_bf16 a[84:87], a[212:215], a[228:231], a[84:87]\n" \
"ds_read_b64_tr_b16 a[252:253], v39 offset:40960\n" \
"v_mad_i32_i24 v214, v213, s6, v212\n" \
"v_mfma_f32_16x16x32_bf16 a[80:83], a[148:151], a[160:163], a[80:83]\n" \
"s_add_i32 s5, s5, 32\n" \
"v_lshlrev_b32_e32 v215, 4, v214\n" \
"v_mfma_f32_16x16x32_bf16 a[80:83], a[212:215], a[224:227], a[80:83]\n" \
"v_add_u32_e64 v216, s5, v215\n" \
"v_and_b32_e32 v216, 0xf0, v216\n" \
"v_mfma_f32_16x16x32_bf16 a[96:99], a[152:155], a[160:163], a[96:99]\n" \
"ds_read_b64_tr_b16 a[254:255], v39 offset:41984\n" \
"v_cmp_gt_u32_e64 vcc, 16, v214\n" \
"v_mfma_f32_16x16x32_bf16 a[96:99], a[216:219], a[224:227], a[96:99]\n" \
"s_movk_i32 s7, 0xf800\n" \
"v_cndmask_b32_e32 v214, v215, v216, vcc\n" \
"v_mfma_f32_16x16x32_bf16 a[100:103], a[152:155], a[164:167], a[100:103]\n" \
"v_or_b32_e64 v215, 64, v212\n" \
"v_mul_u32_u24_e32 v216, 0xe39, v215\n" \
"v_mfma_f32_16x16x32_bf16 a[100:103], a[216:219], a[228:231], a[100:103]\n" \
"v_lshrrev_b32_e64 v216, 16, v216\n" \
"v_mad_i32_i24 v215, v216, s6, v215\n" \
"v_mfma_f32_16x16x32_bf16 a[104:107], a[152:155], a[168:171], a[104:107]\n" \
"v_lshlrev_b32_e32 v217, 4, v215\n" \
"v_add_u32_e32 v220, s5, v217\n" \
"v_mfma_f32_16x16x32_bf16 a[104:107], a[216:219], a[232:235], a[104:107]\n" \
"v_and_b32_e32 v220, 0xf0, v220\n" \
"v_cmp_gt_u32_e64 vcc, 16, v215\n" \
"v_mfma_f32_16x16x32_bf16 a[108:111], a[152:155], a[172:175], a[108:111]\n" \
"s_nop 0\n" \
"v_cndmask_b32_e32 v215, v217, v220, vcc\n" \
"v_or_b32_e32 v217, 0x80, v212\n" \
"v_mfma_f32_16x16x32_bf16 a[108:111], a[216:219], a[236:239], a[108:111]\n" \
"v_mul_u32_u24_e32 v220, 0xe39, v217\n" \
"v_lshrrev_b32_e32 v220, 16, v220\n" \
"s_waitcnt lgkmcnt(15)\n" \
"v_mfma_f32_16x16x32_bf16 a[124:127], a[156:159], a[172:175], a[124:127]\n" \
"v_mad_i32_i24 v217, v220, s6, v217\n" \
"v_lshlrev_b32_e64 v221, 4, v217\n" \
"v_mfma_f32_16x16x32_bf16 a[124:127], a[220:223], a[236:239], a[124:127]\n" \
"v_add_u32_e64 v222, s5, v221\n" \
"v_and_b32_e32 v222, 0xf0, v222\n" \
"v_mfma_f32_16x16x32_bf16 a[120:123], a[156:159], a[168:171], a[120:123]\n" \
"v_cmp_gt_u32_e32 vcc, 16, v217\n" \
"s_nop 1\n" \
"v_cndmask_b32_e64 v217, v221, v222, vcc\n" \
"v_mfma_f32_16x16x32_bf16 a[120:123], a[220:223], a[232:235], a[120:123]\n" \
"v_or_b32_e32 v221, 0xc0, v212\n" \
"v_mul_u32_u24_e32 v222, 0xe39, v221\n" \
"v_mfma_f32_16x16x32_bf16 a[116:119], a[156:159], a[164:167], a[116:119]\n" \
"v_lshrrev_b32_e64 v222, 16, v222\n" \
"v_mad_i32_i24 v221, v222, s6, v221\n" \
"v_mfma_f32_16x16x32_bf16 a[116:119], a[220:223], a[228:231], a[116:119]\n" \
"v_lshlrev_b32_e32 v223, 4, v221\n" \
"v_add_u32_e32 v224, s5, v223\n" \
"v_mfma_f32_16x16x32_bf16 a[112:115], a[156:159], a[160:163], a[112:115]\n" \
"v_and_b32_e32 v224, 0xf0, v224\n" \
"v_cmp_gt_u32_e64 vcc, 16, v221\n" \
"v_mfma_f32_16x16x32_bf16 a[112:115], a[220:223], a[224:227], a[112:115]\n" \
"s_waitcnt vmcnt(20)\n" \
"v_readfirstlane_b32 s62, v180\n" \
"s_nop 3\n" \
"s_add_u32 s62, s62, 0x9E3779B1\n" \
"s_waitcnt vmcnt(2) lgkmcnt(0)\n" \
"s_barrier\n" \
"v_mfma_f32_16x16x32_bf16 v[52:55], a[128:131], a[176:179], v[52:55]\n" \
"s_add_u32 m0, s30, 0x8000\n" \
"s_movk_i32 s84, 0x180\n" \
"v_mfma_f32_16x16x32_bf16 v[52:55], a[192:195], a[240:243], v[52:55]\n" \
"buffer_load_dwordx4 v22, s[64:67], s84 offen offset:0 lds\n" \
"v_mfma_f32_16x16x32_bf16 v[56:59], a[128:131], a[180:183], v[56:59]\n" \
"ds_read_b64_tr_b16 a[160:161], v32 offset:0\n" \
"v_cndmask_b32_e64 v221, v223, v224, vcc\n" \
"v_mfma_f32_16x16x32_bf16 v[56:59], a[192:195], a[244:247], v[56:59]\n" \
"s_movk_i32 s84, 0x1d80\n" \
"v_mfma_f32_16x16x32_bf16 v[60:63], a[128:131], a[184:187], v[60:63]\n" \
"buffer_load_dwordx4 v23, s[64:67], s84 offen offset:1024 lds\n" \
"v_mfma_f32_16x16x32_bf16 v[60:63], a[192:195], a[248:251], v[60:63]\n" \
"ds_read_b64_tr_b16 a[162:163], v32 offset:1024\n" \
"v_or_b32_e32 v223, 0x100, v212\n" \
"v_mfma_f32_16x16x32_bf16 v[64:67], a[128:131], a[188:191], v[64:67]\n" \
"ds_read_b64_tr_b16 a[224:225], v32 offset:8192\n" \
"v_mul_u32_u24_e32 v224, 0xe39, v223\n" \
"v_mfma_f32_16x16x32_bf16 v[64:67], a[192:195], a[252:255], v[64:67]\n" \
"ds_read_b128 a[128:131], v28 offset:16384\n" \
"v_lshrrev_b32_e32 v224, 16, v224\n" \
"v_mfma_f32_16x16x32_bf16 v[80:83], a[132:135], a[188:191], v[80:83]\n" \
"ds_read_b128 a[192:195], v29 offset:16384\n" \
"v_mad_i32_i24 v223, v224, s6, v223\n" \
"v_mfma_f32_16x16x32_bf16 v[80:83], a[196:199], a[252:255], v[80:83]\n" \
"s_movk_i32 s84, 0x3980\n" \
"v_mfma_f32_16x16x32_bf16 v[76:79], a[132:135], a[184:187], v[76:79]\n" \
"buffer_load_dwordx4 v22, s[64:67], s84 offen offset:2048 lds\n" \
"v_mfma_f32_16x16x32_bf16 v[76:79], a[196:199], a[248:251], v[76:79]\n" \
"s_movk_i32 s84, 0x5580\n" \
"v_mfma_f32_16x16x32_bf16 v[72:75], a[132:135], a[180:183], v[72:75]\n" \
"buffer_load_dwordx4 v23, s[64:67], s84 offen offset:3072 lds\n" \
"v_mfma_f32_16x16x32_bf16 v[72:75], a[196:199], a[244:247], v[72:75]\n" \
"s_add_u32 m0, s31, 0x8000\n" \
"s_mov_b32 s84, 0x180000\n" \
"v_mfma_f32_16x16x32_bf16 v[68:71], a[132:135], a[176:179], v[68:71]\n" \
"buffer_load_dwordx4 v26, s[68:71], s84 offen offset:0 lds\n" \
"v_mfma_f32_16x16x32_bf16 v[68:71], a[196:199], a[240:243], v[68:71]\n" \
"ds_read_b128 a[132:135], v28 offset:18432\n" \
"v_lshlrev_b32_e64 v225, 4, v223\n" \
"v_mfma_f32_16x16x32_bf16 v[84:87], a[136:139], a[176:179], v[84:87]\n" \
"ds_read_b128 a[196:199], v29 offset:18432\n" \
"v_add_u32_e64 v226, s5, v225\n" \
"v_mfma_f32_16x16x32_bf16 v[84:87], a[200:203], a[240:243], v[84:87]\n" \
"s_addk_i32 s84, 0x7c00\n" \
"v_mfma_f32_16x16x32_bf16 v[88:91], a[136:139], a[180:183], v[88:91]\n" \
"buffer_load_dwordx4 v26, s[68:71], s84 offen offset:1024 lds\n" \
"v_mfma_f32_16x16x32_bf16 v[88:91], a[200:203], a[244:247], v[88:91]\n" \
"ds_read_b64_tr_b16 a[226:227], v32 offset:9216\n" \
"v_and_b32_e32 v226, 0xf0, v226\n" \
"v_mfma_f32_16x16x32_bf16 v[92:95], a[136:139], a[184:187], v[92:95]\n" \
"s_addk_i32 s84, 0x7c00\n" \
"v_mfma_f32_16x16x32_bf16 v[92:95], a[200:203], a[248:251], v[92:95]\n" \
"buffer_load_dwordx4 v27, s[68:71], s84 offen offset:2048 lds\n" \
"v_mfma_f32_16x16x32_bf16 v[96:99], a[136:139], a[188:191], v[96:99]\n" \
"ds_read_b64_tr_b16 a[164:165], v33 offset:0\n" \
"v_cmp_gt_u32_e64 vcc, 16, v223\n" \
"v_mfma_f32_16x16x32_bf16 v[96:99], a[200:203], a[252:255], v[96:99]\n" \
"ds_read_b128 a[136:139], v28 offset:20480\n" \
"v_cndmask_b32_e64 v223, v225, v226, vcc\n" \
"v_mfma_f32_16x16x32_bf16 v[112:115], a[140:143], a[188:191], v[112:115]\n" \
"ds_read_b128 a[200:203], v29 offset:20480\n" \
"v_or_b32_e32 v225, 0x140, v212\n" \
"v_mfma_f32_16x16x32_bf16 v[112:115], a[204:207], a[252:255], v[112:115]\n" \
"s_addk_i32 s84, 0x7c00\n" \
"s_nop 0\n" \
"v_mfma_f32_16x16x32_bf16 v[108:111], a[140:143], a[184:187], v[108:111]\n" \
"buffer_load_dwordx4 v27, s[68:71], s84 offen offset:3072 lds\n" \
"v_mfma_f32_16x16x32_bf16 v[108:111], a[204:207], a[248:251], v[108:111]\n" \
"s_add_u32 m0, s30, 0x9000\n" \
"s_mov_b32 s84, 0x8180\n" \
"v_mfma_f32_16x16x32_bf16 v[104:107], a[140:143], a[180:183], v[104:107]\n" \
"buffer_load_dwordx4 v22, s[64:67], s84 offen offset:0 lds\n" \
"v_mfma_f32_16x16x32_bf16 v[104:107], a[204:207], a[244:247], v[104:107]\n" \
"ds_read_b64_tr_b16 a[166:167], v33 offset:1024\n" \
"v_mul_u32_u24_e32 v226, 0xe39, v225\n" \
"v_mfma_f32_16x16x32_bf16 v[100:103], a[140:143], a[176:179], v[100:103]\n" \
"ds_read_b64_tr_b16 a[228:229], v33 offset:8192\n" \
"v_lshrrev_b32_e64 v226, 16, v226\n" \
"v_mfma_f32_16x16x32_bf16 v[100:103], a[204:207], a[240:243], v[100:103]\n" \
"ds_read_b128 a[140:143], v28 offset:22528\n" \
"v_mad_i32_i24 v225, v226, s6, v225\n" \
"v_mfma_f32_16x16x32_bf16 v[116:119], a[144:147], a[176:179], v[116:119]\n" \
"ds_read_b128 a[204:207], v29 offset:22528\n" \
"v_lshlrev_b32_e64 v227, 4, v225\n" \
"v_mfma_f32_16x16x32_bf16 v[116:119], a[208:211], a[240:243], v[116:119]\n" \
"s_addk_i32 s84, 0x1c00\n" \
"v_mfma_f32_16x16x32_bf16 v[120:123], a[144:147], a[180:183], v[120:123]\n" \
"buffer_load_dwordx4 v23, s[64:67], s84 offen offset:1024 lds\n" \
"v_mfma_f32_16x16x32_bf16 v[120:123], a[208:211], a[244:247], v[120:123]\n" \
"s_addk_i32 s84, 0x1c00\n" \
"v_mfma_f32_16x16x32_bf16 v[124:127], a[144:147], a[184:187], v[124:127]\n" \
"buffer_load_dwordx4 v22, s[64:67], s84 offen offset:2048 lds\n" \
"v_mfma_f32_16x16x32_bf16 v[124:127], a[208:211], a[248:251], v[124:127]\n" \
"s_addk_i32 s84, 0x1c00\n" \
"v_mfma_f32_16x16x32_bf16 v[128:131], a[144:147], a[188:191], v[128:131]\n" \
"buffer_load_dwordx4 v23, s[64:67], s84 offen offset:3072 lds\n" \
"v_mfma_f32_16x16x32_bf16 v[128:131], a[208:211], a[252:255], v[128:131]\n" \
"ds_read_b128 a[144:147], v28 offset:0\n" \
"v_add_u32_e32 v228, s5, v227\n" \
"v_mfma_f32_16x16x32_bf16 v[144:147], a[148:151], a[188:191], v[144:147]\n" \
"ds_read_b128 a[208:211], v29 offset:0\n" \
"v_and_b32_e32 v228, 0xf0, v228\n" \
"v_mfma_f32_16x16x32_bf16 v[144:147], a[212:215], a[252:255], v[144:147]\n" \
"s_add_u32 m0, s31, 0xc000\n" \
"s_mov_b32 s84, 0x180000\n" \
"v_mfma_f32_16x16x32_bf16 v[140:143], a[148:151], a[184:187], v[140:143]\n" \
"buffer_load_dwordx4 v26, s[72:75], s84 offen offset:0 lds\n" \
"v_mfma_f32_16x16x32_bf16 v[140:143], a[212:215], a[248:251], v[140:143]\n" \
"ds_read_b64_tr_b16 a[230:231], v33 offset:9216\n" \
"v_cmp_gt_u32_e64 vcc, 16, v225\n" \
"v_mfma_f32_16x16x32_bf16 v[136:139], a[148:151], a[180:183], v[136:139]\n" \
"s_addk_i32 s84, 0x7c00\n" \
"v_mfma_f32_16x16x32_bf16 v[136:139], a[212:215], a[244:247], v[136:139]\n" \
"buffer_load_dwordx4 v26, s[72:75], s84 offen offset:1024 lds\n" \
"v_mfma_f32_16x16x32_bf16 v[132:135], a[148:151], a[176:179], v[132:135]\n" \
"ds_read_b64_tr_b16 a[168:169], v34 offset:0\n" \
"v_cndmask_b32_e32 v225, v227, v228, vcc\n" \
"v_mfma_f32_16x16x32_bf16 v[132:135], a[212:215], a[240:243], v[132:135]\n" \
"ds_read_b128 a[148:151], v28 offset:2048\n" \
"v_or_b32_e32 v227, 0x180, v212\n" \
"v_mfma_f32_16x16x32_bf16 v[148:151], a[152:155], a[176:179], v[148:151]\n" \
"ds_read_b128 a[212:215], v29 offset:2048\n" \
"v_mul_u32_u24_e32 v228, 0xe39, v227\n" \
"v_mfma_f32_16x16x32_bf16 v[148:151], a[216:219], a[240:243], v[148:151]\n" \
"ds_read_b64_tr_b16 a[170:171], v34 offset:1024\n" \
"v_lshrrev_b32_e64 v228, 16, v228\n" \
"v_mfma_f32_16x16x32_bf16 v[152:155], a[152:155], a[180:183], v[152:155]\n" \
"ds_read_b64_tr_b16 a[232:233], v34 offset:8192\n" \
"v_mad_i32_i24 v227, v228, s6, v227\n" \
"v_mfma_f32_16x16x32_bf16 v[152:155], a[216:219], a[244:247], v[152:155]\n" \
"ds_read_b64_tr_b16 a[234:235], v34 offset:9216\n" \
"v_lshlrev_b32_e64 v229, 4, v227\n" \
"v_mfma_f32_16x16x32_bf16 v[156:159], a[152:155], a[184:187], v[156:159]\n" \
"ds_read_b64_tr_b16 a[172:173], v35 offset:0\n" \
"v_add_u32_e64 v230, s5, v229\n" \
"v_mfma_f32_16x16x32_bf16 v[156:159], a[216:219], a[248:251], v[156:159]\n" \
"ds_read_b64_tr_b16 a[174:175], v35 offset:1024\n" \
"v_and_b32_e32 v230, 0xf0, v230\n" \
"v_mfma_f32_16x16x32_bf16 v[160:163], a[152:155], a[188:191], v[160:163]\n" \
"ds_read_b64_tr_b16 a[236:237], v35 offset:8192\n" \
"v_cmp_gt_u32_e64 vcc, 16, v227\n" \
"v_mfma_f32_16x16x32_bf16 v[160:163], a[216:219], a[252:255], v[160:163]\n" \
"ds_read_b128 a[152:155], v28 offset:4096\n" \
"v_cndmask_b32_e64 v227, v229, v230, vcc\n" \
"v_mfma_f32_16x16x32_bf16 v[176:179], a[156:159], a[188:191], v[176:179]\n" \
"ds_read_b128 a[216:219], v29 offset:4096\n" \
"v_or_b32_e32 v229, 0x1c0, v212\n" \
"v_mfma_f32_16x16x32_bf16 v[176:179], a[220:223], a[252:255], v[176:179]\n" \
"ds_read_b64_tr_b16 a[238:239], v35 offset:9216\n" \
"v_mul_u32_u24_e32 v230, 0xe39, v229\n" \
"v_mfma_f32_16x16x32_bf16 v[172:175], a[156:159], a[184:187], v[172:175]\n" \
"v_lshrrev_b32_e64 v230, 16, v230\n" \
"v_mad_i32_i24 v229, v230, s6, v229\n" \
"v_mfma_f32_16x16x32_bf16 v[172:175], a[220:223], a[248:251], v[172:175]\n" \
"v_lshlrev_b32_e32 v231, 4, v229\n" \
"v_add_u32_e32 v232, s5, v231\n" \
"v_mfma_f32_16x16x32_bf16 v[168:171], a[156:159], a[180:183], v[168:171]\n" \
"v_and_b32_e32 v232, 0xf0, v232\n" \
"v_cmp_gt_u32_e64 vcc, 16, v229\n" \
"v_mfma_f32_16x16x32_bf16 v[168:171], a[220:223], a[244:247], v[168:171]\n" \
"s_nop 0\n" \
"v_cndmask_b32_e32 v229, v231, v232, vcc\n" \
"v_or_b32_e32 v231, 0x200, v212\n" \
"v_mfma_f32_16x16x32_bf16 v[164:167], a[156:159], a[176:179], v[164:167]\n" \
"v_mul_u32_u24_e32 v232, 0xe39, v231\n" \
"v_lshrrev_b32_e64 v232, 16, v232\n" \
"v_mfma_f32_16x16x32_bf16 v[164:167], a[220:223], a[240:243], v[164:167]\n" \
"ds_read_b128 a[156:159], v28 offset:6144\n" \
"ds_read_b128 a[220:223], v29 offset:6144\n" \
"s_waitcnt lgkmcnt(15)\n" \
"v_mfma_f32_16x16x32_bf16 a[0:3], a[128:131], a[160:163], a[0:3]\n" \
"s_addk_i32 s84, 0x7c00\n" \
"v_mfma_f32_16x16x32_bf16 a[0:3], a[192:195], a[224:227], a[0:3]\n" \
"buffer_load_dwordx4 v27, s[72:75], s84 offen offset:2048 lds\n" \
"v_mfma_f32_16x16x32_bf16 a[4:7], a[128:131], a[164:167], a[4:7]\n" \
"ds_read_b64_tr_b16 a[176:177], v36 offset:0\n" \
"v_mad_i32_i24 v231, v232, s6, v231\n" \
"s_waitcnt lgkmcnt(15)\n" \
"v_mfma_f32_16x16x32_bf16 a[4:7], a[192:195], a[228:231], a[4:7]\n" \
"s_addk_i32 s84, 0x7c00\n" \
"s_waitcnt lgkmcnt(11)\n" \
"v_mfma_f32_16x16x32_bf16 a[8:11], a[128:131], a[168:171], a[8:11]\n" \
"buffer_load_dwordx4 v27, s[72:75], s84 offen offset:3072 lds\n" \
"s_waitcnt lgkmcnt(9)\n" \
"v_mfma_f32_16x16x32_bf16 a[8:11], a[192:195], a[232:235], a[8:11]\n" \
"ds_read_b64_tr_b16 a[178:179], v36 offset:1024\n" \
"v_lshlrev_b32_e32 v233, 4, v231\n" \
"s_waitcnt lgkmcnt(8)\n" \
"v_mfma_f32_16x16x32_bf16 a[12:15], a[128:131], a[172:175], a[12:15]\n" \
"ds_read_b64_tr_b16 a[240:241], v36 offset:8192\n" \
"v_add_u32_e32 v234, s5, v233\n" \
"s_waitcnt lgkmcnt(5)\n" \
"v_mfma_f32_16x16x32_bf16 a[12:15], a[192:195], a[236:239], a[12:15]\n" \
"s_movk_i32 s5, 0x480\n" \
"v_mad_u32_u24 v0, v213, s5, v214\n" \
"v_mfma_f32_16x16x32_bf16 a[28:31], a[132:135], a[172:175], a[28:31]\n" \
"v_mul_u32_u24_e32 v213, 0x480, v216\n" \
"s_movk_i32 s6, 0xfc00\n" \
"v_mfma_f32_16x16x32_bf16 a[28:31], a[196:199], a[236:239], a[28:31]\n" \
"ds_read_b64_tr_b16 a[242:243], v36 offset:9216\n" \
"v_add3_u32 v1, v213, v215, s6\n" \
"v_mfma_f32_16x16x32_bf16 a[24:27], a[132:135], a[168:171], a[24:27]\n" \
"buffer_load_dwordx4 v[200:203], v192, s[76:79], s34 offen offset:0\n" \
"v_mfma_f32_16x16x32_bf16 a[24:27], a[196:199], a[232:235], a[24:27]\n" \
"v_mul_u32_u24_e32 v213, 0x480, v220\n" \
"v_add3_u32 v2, v213, v217, s7\n" \
"v_mfma_f32_16x16x32_bf16 a[20:23], a[132:135], a[164:167], a[20:23]\n" \
"v_mul_u32_u24_e32 v213, 0x480, v222\n" \
"v_add3_u32 v3, v213, v221, s40\n" \
"v_mfma_f32_16x16x32_bf16 a[20:23], a[196:199], a[228:231], a[20:23]\n" \
"ds_read_b64_tr_b16 a[180:181], v37 offset:0\n" \
"v_mul_u32_u24_e32 v213, 0x480, v226\n" \
"v_mfma_f32_16x16x32_bf16 a[16:19], a[132:135], a[160:163], a[16:19]\n" \
"v_add3_u32 v5, v213, v225, s6\n" \
"v_mul_u32_u24_e32 v213, 0x480, v228\n" \
"v_mfma_f32_16x16x32_bf16 a[16:19], a[196:199], a[224:227], a[16:19]\n" \
"v_add3_u32 v6, v213, v227, s7\n" \
"v_mul_u32_u24_e32 v213, 0x480, v230\n" \
"v_mfma_f32_16x16x32_bf16 a[32:35], a[136:139], a[160:163], a[32:35]\n" \
"buffer_load_dwordx4 v[204:207], v193, s[76:79], s34 offen offset:0\n" \
"v_mfma_f32_16x16x32_bf16 a[32:35], a[200:203], a[224:227], a[32:35]\n" \
"s_mov_b32 m0, s32\n" \
"ds_read_b64_tr_b16 a[182:183], v37 offset:1024\n" \
"v_mfma_f32_16x16x32_bf16 a[36:39], a[136:139], a[164:167], a[36:39]\n" \
"s_mov_b32 exec_lo, 0x0\n" \
"s_mov_b32 exec_hi, 0xff00\n" \
"global_load_lds_dword v[184:185], off\n" \
"s_mov_b64 exec, -1\n" \
"v_mfma_f32_16x16x32_bf16 a[36:39], a[200:203], a[228:231], a[36:39]\n" \
"ds_read_b64_tr_b16 a[244:245], v37 offset:8192\n" \
"v_add3_u32 v7, v213, v229, s40\n" \
"v_mfma_f32_16x16x32_bf16 a[40:43], a[136:139], a[168:171], a[40:43]\n" \
"s_lshl_b32 s7, s3, 3\n" \
"s_lshl_b32 s40, s3, 1\n" \
"v_mfma_f32_16x16x32_bf16 a[40:43], a[200:203], a[232:235], a[40:43]\n" \
"buffer_load_dword v208, v194, s[76:79], s34 offen offset:32\n" \
"v_mfma_f32_16x16x32_bf16 a[44:47], a[136:139], a[172:175], a[44:47]\n" \
"ds_read_b64_tr_b16 a[246:247], v37 offset:9216\n" \
"s_and_b32 s7, s7, 16\n" \
"v_mfma_f32_16x16x32_bf16 a[44:47], a[200:203], a[236:239], a[44:47]\n" \
"s_and_b32 s40, s40, 2\n" \
"s_and_b32 s6, s2, 7\n" \
"v_mfma_f32_16x16x32_bf16 a[60:63], a[140:143], a[172:175], a[60:63]\n" \
"v_bfe_u32 v213, v20, 7, 1\n" \
"s_or_b32 s7, s40, s7\n" \
"v_mfma_f32_16x16x32_bf16 a[60:63], a[204:207], a[236:239], a[60:63]\n" \
"v_and_b32_e32 v234, 0xf0, v234\n" \
"v_cmp_gt_u32_e32 vcc, 16, v231\n" \
"v_mfma_f32_16x16x32_bf16 a[56:59], a[140:143], a[168:171], a[56:59]\n" \
"buffer_load_dword v209, v194, s[76:79], s34 offen offset:1184\n" \
"v_mfma_f32_16x16x32_bf16 a[56:59], a[204:207], a[232:235], a[56:59]\n" \
"ds_read_b64_tr_b16 a[184:185], v38 offset:0\n" \
"v_or3_b32 v213, s7, v213, v219\n" \
"v_mfma_f32_16x16x32_bf16 a[52:55], a[140:143], a[164:167], a[52:55]\n" \
"v_and_b32_e32 v214, 32, v20\n" \
"s_lshl_b32 s6, s6, 5\n" \
"v_mfma_f32_16x16x32_bf16 a[52:55], a[204:207], a[228:231], a[52:55]\n" \
"ds_read_b64_tr_b16 a[186:187], v38 offset:1024\n" \
"v_cndmask_b32_e64 v231, v233, v234, vcc\n" \
"v_mfma_f32_16x16x32_bf16 a[48:51], a[140:143], a[160:163], a[48:51]\n" \
"v_mul_u32_u24_e64 v213, 18, v213\n" \
"v_mov_b32_e32 v215, 0x100\n" \
"v_mfma_f32_16x16x32_bf16 a[48:51], a[204:207], a[224:227], a[48:51]\n" \
"v_mov_b32_e32 v216, s6\n" \
"v_cmp_eq_u32_e32 vcc, 0, v214\n" \
"v_mfma_f32_16x16x32_bf16 a[64:67], a[144:147], a[160:163], a[64:67]\n" \
"buffer_load_dword v211, v194, s[76:79], s34 offen offset:2336\n" \
"v_mfma_f32_16x16x32_bf16 a[64:67], a[208:211], a[224:227], a[64:67]\n" \
"ds_read_b64_tr_b16 a[248:249], v38 offset:8192\n" \
"v_and_or_b32 v213, v218, 1, v213\n" \
"v_mfma_f32_16x16x32_bf16 a[68:71], a[144:147], a[164:167], a[68:71]\n" \
"v_lshlrev_b32_e32 v212, 3, v212\n" \
"v_cndmask_b32_e32 v214, v215, v216, vcc\n" \
"v_mfma_f32_16x16x32_bf16 a[68:71], a[208:211], a[228:231], a[68:71]\n" \
"ds_read_b64_tr_b16 a[250:251], v38 offset:9216\n" \
"v_mad_u32_u24 v213, v213, s5, v214\n" \
"v_mfma_f32_16x16x32_bf16 a[72:75], a[144:147], a[168:171], a[72:75]\n" \
"v_and_or_b32 v213, v20, 16, v213\n" \
"v_add_u32_e32 v9, 0x4800, v213\n" \
"v_mfma_f32_16x16x32_bf16 a[72:75], a[208:211], a[232:235], a[72:75]\n" \
"s_mov_b32 s76, s20\n" \
"s_and_b32 s77, s21, 0xffff\n" \
"v_mfma_f32_16x16x32_bf16 a[76:79], a[144:147], a[172:175], a[76:79]\n" \
"s_mov_b32 s78, 0x480\n" \
"v_mul_u32_u24_e32 v213, 0x120, v218\n" \
"v_mfma_f32_16x16x32_bf16 a[76:79], a[208:211], a[236:239], a[76:79]\n" \
"buffer_load_ushort v210, v195, s[76:79], 0 offen\n" \
"v_mfma_f32_16x16x32_bf16 a[92:95], a[148:151], a[172:175], a[92:95]\n" \
"ds_read_b64_tr_b16 a[188:189], v39 offset:0\n" \
"v_and_or_b32 v10, v212, 24, v213\n" \
"v_mfma_f32_16x16x32_bf16 a[92:95], a[212:215], a[236:239], a[92:95]\n" \
"v_lshrrev_b32_e32 v213, 2, v20\n" \
"v_and_or_b32 v213, v213, 60, s4\n" \
"v_mfma_f32_16x16x32_bf16 a[88:91], a[148:151], a[168:171], a[88:91]\n" \
"s_mov_b32 s76, s8\n" \
"s_nop 0\n" \
"s_and_b32 s77, s9, 0xffff\n" \
"v_mfma_f32_16x16x32_bf16 a[88:91], a[212:215], a[232:235], a[88:91]\n" \
"s_mov_b32 s78, 0x240000\n" \
"ds_read_b64_tr_b16 a[190:191], v39 offset:1024\n" \
"v_mfma_f32_16x16x32_bf16 a[84:87], a[148:151], a[164:167], a[84:87]\n" \
"v_lshl_or_b32 v212, s3, 9, v212\n" \
"v_mad_u32_u24 v4, v224, s5, v223\n" \
"v_mfma_f32_16x16x32_bf16 a[84:87], a[212:215], a[228:231], a[84:87]\n" \
"ds_read_b64_tr_b16 a[252:253], v39 offset:8192\n" \
"v_mad_u32_u24 v8, v232, s5, v231\n" \
"v_mfma_f32_16x16x32_bf16 a[80:83], a[148:151], a[160:163], a[80:83]\n" \
"v_or_b32_e32 v18, 0x27000, v213\n" \
"v_or_b32_e32 v19, 0x27800, v212\n" \
"v_mfma_f32_16x16x32_bf16 a[80:83], a[212:215], a[224:227], a[80:83]\n" \
"v_mov_b32_e32 v219, 0x1000c0c\n" \
"v_mov_b32_e32 v220, 0xc0c0100\n" \
"v_mfma_f32_16x16x32_bf16 a[96:99], a[152:155], a[160:163], a[96:99]\n" \
"ds_read_b64_tr_b16 a[254:255], v39 offset:9216\n" \
"v_bfe_u32 v218, v20, 4, 1\n" \
"v_mfma_f32_16x16x32_bf16 a[96:99], a[216:219], a[224:227], a[96:99]\n" \
"v_lshrrev_b32_e32 v213, 8, v20\n" \
"v_cmp_eq_u32_e32 vcc, 0, v218\n" \
"v_mfma_f32_16x16x32_bf16 a[100:103], a[152:155], a[164:167], a[100:103]\n" \
"s_lshr_b32 s4, s2, 5\n" \
"s_and_b32 s5, s3, 1\n" \
"v_mfma_f32_16x16x32_bf16 a[100:103], a[216:219], a[228:231], a[100:103]\n" \
"v_bfe_u32 v217, v20, 4, 4\n" \
"v_cndmask_b32_e64 v219, v219, v220, vcc\n" \
"v_mfma_f32_16x16x32_bf16 a[104:107], a[152:155], a[168:171], a[104:107]\n" \
"v_mov_b32_e32 v220, 0x3020c0c\n" \
"v_mov_b32_e32 v221, 0xc0c0302\n" \
"v_mfma_f32_16x16x32_bf16 a[104:107], a[216:219], a[232:235], a[104:107]\n" \
"v_lshl_or_b32 v218, v213, 2, v218\n" \
"s_xor_b32 s6, s4, 15\n" \
"v_mfma_f32_16x16x32_bf16 a[108:111], a[152:155], a[172:175], a[108:111]\n" \
"s_cmpk_lt_u32 s3, 2\n" \
"s_cselect_b32 s7, s6, s4\n" \
"v_mfma_f32_16x16x32_bf16 a[108:111], a[216:219], a[236:239], a[108:111]\n" \
"s_lshl_b32 s40, s7, 7\n" \
".fill (L_pb + 48864 - .) / 4, 4, 0xBF800000\n" \
"L_pp12:\n" \
"s_cbranch_execz L_pret\n" \
"s_lshl_b32 s41, s5, 6\n" \
"s_waitcnt lgkmcnt(15)\n" \
"v_mfma_f32_16x16x32_bf16 a[124:127], a[156:159], a[172:175], a[124:127]\n" \
"v_cndmask_b32_e32 v220, v220, v221, vcc\n" \
"v_mov_b32_e32 v221, 0xc0c0c0c\n" \
"v_mfma_f32_16x16x32_bf16 a[124:127], a[220:223], a[236:239], a[124:127]\n" \
"v_cmp_gt_u32_e32 vcc, v218, v217\n" \
"v_or_b32_e32 v218, 2, v218\n" \
"v_mfma_f32_16x16x32_bf16 a[120:123], a[156:159], a[168:171], a[120:123]\n" \
"v_lshrrev_b32_e32 v212, 7, v20\n" \
"s_or_b32 s41, s41, s40\n" \
"v_mfma_f32_16x16x32_bf16 a[120:123], a[220:223], a[232:235], a[120:123]\n" \
"v_cndmask_b32_e32 v40, v221, v219, vcc\n" \
"v_cndmask_b32_e32 v42, v219, v221, vcc\n" \
"v_mfma_f32_16x16x32_bf16 a[116:119], a[156:159], a[164:167], a[116:119]\n" \
"v_cmp_gt_u32_e32 vcc, v218, v217\n" \
"v_and_b32_e32 v218, 16, v20\n" \
"v_mfma_f32_16x16x32_bf16 a[116:119], a[220:223], a[228:231], a[116:119]\n" \
"s_movk_i32 s42, 0x3f80\n" \
"v_or_b32_e32 v214, s41, v212\n" \
"v_mfma_f32_16x16x32_bf16 a[112:115], a[156:159], a[160:163], a[112:115]\n" \
"v_and_b32_e32 v217, 0x320, v20\n" \
"s_movk_i32 s41, 0x100\n" \
"v_mfma_f32_16x16x32_bf16 a[112:115], a[220:223], a[224:227], a[112:115]\n" \
"s_waitcnt vmcnt(7) lgkmcnt(0)\n" \
"s_barrier\n" \
"v_mfma_f32_16x16x32_bf16 v[52:55], a[128:131], a[176:179], v[52:55]\n" \
"s_add_u32 m0, s30, 0x0\n" \
"s_movk_i32 s84, 0x200\n" \
"v_mfma_f32_16x16x32_bf16 v[52:55], a[192:195], a[240:243], v[52:55]\n" \
"buffer_load_dwordx4 v22, s[64:67], s84 offen offset:0 lds\n" \
"v_mfma_f32_16x16x32_bf16 v[56:59], a[128:131], a[180:183], v[56:59]\n" \
"ds_read_b64_tr_b16 a[160:161], v32 offset:32768\n" \
"v_lshlrev_b32_e64 v218, v218, s42\n" \
"v_mfma_f32_16x16x32_bf16 v[56:59], a[192:195], a[244:247], v[56:59]\n" \
"s_movk_i32 s84, 0x1e00\n" \
"v_mfma_f32_16x16x32_bf16 v[60:63], a[128:131], a[184:187], v[60:63]\n" \
"buffer_load_dwordx4 v23, s[64:67], s84 offen offset:1024 lds\n" \
"v_mfma_f32_16x16x32_bf16 v[60:63], a[192:195], a[248:251], v[60:63]\n" \
"ds_read_b64_tr_b16 a[162:163], v32 offset:33792\n" \
"v_mov_b32_e64 v219, 0\n" \
"v_mfma_f32_16x16x32_bf16 v[64:67], a[128:131], a[188:191], v[64:67]\n" \
"ds_read_b64_tr_b16 a[224:225], v32 offset:40960\n" \
"v_lshrrev_b32_e64 v215, 6, v20\n" \
"v_mfma_f32_16x16x32_bf16 v[64:67], a[192:195], a[252:255], v[64:67]\n" \
"ds_read_b128 a[128:131], v28 offset:49152\n" \
"v_cndmask_b32_e64 v41, v221, v220, vcc\n" \
"v_mfma_f32_16x16x32_bf16 v[80:83], a[132:135], a[188:191], v[80:83]\n" \
"ds_read_b128 a[192:195], v29 offset:49152\n" \
"v_cndmask_b32_e64 v43, v220, v221, vcc\n" \
"v_mfma_f32_16x16x32_bf16 v[80:83], a[196:199], a[252:255], v[80:83]\n" \
"s_movk_i32 s84, 0x3a00\n" \
"v_mfma_f32_16x16x32_bf16 v[76:79], a[132:135], a[184:187], v[76:79]\n" \
"buffer_load_dwordx4 v22, s[64:67], s84 offen offset:2048 lds\n" \
"v_mfma_f32_16x16x32_bf16 v[76:79], a[196:199], a[248:251], v[76:79]\n" \
"s_movk_i32 s84, 0x5600\n" \
"v_mfma_f32_16x16x32_bf16 v[72:75], a[132:135], a[180:183], v[72:75]\n" \
"buffer_load_dwordx4 v23, s[64:67], s84 offen offset:3072 lds\n" \
"v_mfma_f32_16x16x32_bf16 v[72:75], a[196:199], a[244:247], v[72:75]\n" \
"s_add_u32 m0, s31, 0x0\n" \
"s_mov_b32 s84, 0x200000\n" \
"v_mfma_f32_16x16x32_bf16 v[68:71], a[132:135], a[176:179], v[68:71]\n" \
"buffer_load_dwordx4 v26, s[68:71], s84 offen offset:0 lds\n" \
"v_mfma_f32_16x16x32_bf16 v[68:71], a[196:199], a[240:243], v[68:71]\n" \
"ds_read_b128 a[132:135], v28 offset:51200\n" \
"v_cmp_eq_u32_e32 vcc, s41, v217\n" \
"v_mfma_f32_16x16x32_bf16 v[84:87], a[136:139], a[176:179], v[84:87]\n" \
"ds_read_b128 a[196:199], v29 offset:51200\n" \
"v_and_b32_e32 v217, 0x330, v20\n" \
"v_mfma_f32_16x16x32_bf16 v[84:87], a[200:203], a[240:243], v[84:87]\n" \
"s_addk_i32 s84, 0x7c00\n" \
"v_mfma_f32_16x16x32_bf16 v[88:91], a[136:139], a[180:183], v[88:91]\n" \
"buffer_load_dwordx4 v26, s[68:71], s84 offen offset:1024 lds\n" \
"v_mfma_f32_16x16x32_bf16 v[88:91], a[200:203], a[244:247], v[88:91]\n" \
"ds_read_b64_tr_b16 a[226:227], v32 offset:41984\n" \
"v_bfe_u32 v216, v20, 6, 2\n" \
"v_mfma_f32_16x16x32_bf16 v[92:95], a[136:139], a[184:187], v[92:95]\n" \
"s_addk_i32 s84, 0x7c00\n" \
"v_mfma_f32_16x16x32_bf16 v[92:95], a[200:203], a[248:251], v[92:95]\n" \
"buffer_load_dwordx4 v27, s[68:71], s84 offen offset:2048 lds\n" \
"v_mfma_f32_16x16x32_bf16 v[96:99], a[136:139], a[188:191], v[96:99]\n" \
"ds_read_b64_tr_b16 a[164:165], v33 offset:32768\n" \
"v_cndmask_b32_e64 v46, v219, v218, vcc\n" \
"v_mfma_f32_16x16x32_bf16 v[96:99], a[200:203], a[252:255], v[96:99]\n" \
"ds_read_b128 a[136:139], v28 offset:53248\n" \
"v_mov_b32_e32 v218, 0x3f80\n" \
"v_mfma_f32_16x16x32_bf16 v[112:115], a[140:143], a[188:191], v[112:115]\n" \
"ds_read_b128 a[200:203], v29 offset:53248\n" \
"v_cmp_eq_u32_e64 vcc, 32, v217\n" \
"v_mfma_f32_16x16x32_bf16 v[112:115], a[204:207], a[252:255], v[112:115]\n" \
"s_addk_i32 s84, 0x7c00\n" \
"v_mfma_f32_16x16x32_bf16 v[108:111], a[140:143], a[184:187], v[108:111]\n" \
"buffer_load_dwordx4 v27, s[68:71], s84 offen offset:3072 lds\n" \
"v_mfma_f32_16x16x32_bf16 v[108:111], a[204:207], a[248:251], v[108:111]\n" \
"s_add_u32 m0, s30, 0x1000\n" \
"s_mov_b32 s84, 0x8200\n" \
"v_mfma_f32_16x16x32_bf16 v[104:107], a[140:143], a[180:183], v[104:107]\n" \
"buffer_load_dwordx4 v22, s[64:67], s84 offen offset:0 lds\n" \
"v_mfma_f32_16x16x32_bf16 v[104:107], a[204:207], a[244:247], v[104:107]\n" \
"ds_read_b64_tr_b16 a[166:167], v33 offset:33792\n" \
"s_add_i32 s41, s40, -16\n" \
"v_mfma_f32_16x16x32_bf16 v[100:103], a[140:143], a[176:179], v[100:103]\n" \
"ds_read_b64_tr_b16 a[228:229], v33 offset:40960\n" \
"v_lshrrev_b32_e64 v219, 1, v20\n" \
"v_mfma_f32_16x16x32_bf16 v[100:103], a[204:207], a[240:243], v[100:103]\n" \
"ds_read_b128 a[140:143], v28 offset:55296\n" \
"v_bfe_u32 v215, v215, 1, 1\n" \
"v_mfma_f32_16x16x32_bf16 v[116:119], a[144:147], a[176:179], v[116:119]\n" \
"ds_read_b128 a[204:207], v29 offset:55296\n" \
"v_cndmask_b32_e64 v47, 0, v218, vcc\n" \
"v_mfma_f32_16x16x32_bf16 v[116:119], a[208:211], a[240:243], v[116:119]\n" \
"s_addk_i32 s84, 0x1c00\n" \
"v_mfma_f32_16x16x32_bf16 v[120:123], a[144:147], a[180:183], v[120:123]\n" \
"buffer_load_dwordx4 v23, s[64:67], s84 offen offset:1024 lds\n" \
"v_mfma_f32_16x16x32_bf16 v[120:123], a[208:211], a[244:247], v[120:123]\n" \
"s_addk_i32 s84, 0x1c00\n" \
"v_mfma_f32_16x16x32_bf16 v[124:127], a[144:147], a[184:187], v[124:127]\n" \
"buffer_load_dwordx4 v22, s[64:67], s84 offen offset:2048 lds\n" \
"v_mfma_f32_16x16x32_bf16 v[124:127], a[208:211], a[248:251], v[124:127]\n" \
"s_addk_i32 s84, 0x1c00\n" \
"v_mfma_f32_16x16x32_bf16 v[128:131], a[144:147], a[188:191], v[128:131]\n" \
"buffer_load_dwordx4 v23, s[64:67], s84 offen offset:3072 lds\n" \
"v_mfma_f32_16x16x32_bf16 v[128:131], a[208:211], a[252:255], v[128:131]\n" \
"ds_read_b128 a[144:147], v28 offset:32768\n" \
"s_mov_b32 s42, 0\n" \
"v_mfma_f32_16x16x32_bf16 v[144:147], a[148:151], a[188:191], v[144:147]\n" \
"ds_read_b128 a[208:211], v29 offset:32768\n" \
"v_mfma_f32_16x16x32_bf16 v[144:147], a[212:215], a[252:255], v[144:147]\n" \
"s_add_u32 m0, s31, 0x4000\n" \
"s_mov_b32 s84, 0x200000\n" \
"v_mfma_f32_16x16x32_bf16 v[140:143], a[148:151], a[184:187], v[140:143]\n" \
"buffer_load_dwordx4 v26, s[72:75], s84 offen offset:0 lds\n" \
"v_mfma_f32_16x16x32_bf16 v[140:143], a[212:215], a[248:251], v[140:143]\n" \
"ds_read_b64_tr_b16 a[230:231], v33 offset:41984\n" \
"v_mfma_f32_16x16x32_bf16 v[136:139], a[148:151], a[180:183], v[136:139]\n" \
"s_addk_i32 s84, 0x7c00\n" \
"s_nop 0\n" \
"v_mfma_f32_16x16x32_bf16 v[136:139], a[212:215], a[244:247], v[136:139]\n" \
"buffer_load_dwordx4 v26, s[72:75], s84 offen offset:1024 lds\n" \
"v_mfma_f32_16x16x32_bf16 v[132:135], a[148:151], a[176:179], v[132:135]\n" \
"ds_read_b64_tr_b16 a[168:169], v34 offset:32768\n" \
"v_mfma_f32_16x16x32_bf16 v[132:135], a[212:215], a[240:243], v[132:135]\n" \
"ds_read_b128 a[148:151], v28 offset:34816\n" \
"v_mfma_f32_16x16x32_bf16 v[148:151], a[152:155], a[176:179], v[148:151]\n" \
"ds_read_b128 a[212:215], v29 offset:34816\n" \
"v_mfma_f32_16x16x32_bf16 v[148:151], a[216:219], a[240:243], v[148:151]\n" \
"ds_read_b64_tr_b16 a[170:171], v34 offset:33792\n" \
"v_mfma_f32_16x16x32_bf16 v[152:155], a[152:155], a[180:183], v[152:155]\n" \
"ds_read_b64_tr_b16 a[232:233], v34 offset:40960\n" \
"v_mfma_f32_16x16x32_bf16 v[152:155], a[216:219], a[244:247], v[152:155]\n" \
"ds_read_b64_tr_b16 a[234:235], v34 offset:41984\n" \
"v_mfma_f32_16x16x32_bf16 v[156:159], a[152:155], a[184:187], v[156:159]\n" \
"ds_read_b64_tr_b16 a[172:173], v35 offset:32768\n" \
"v_mfma_f32_16x16x32_bf16 v[156:159], a[216:219], a[248:251], v[156:159]\n" \
"ds_read_b64_tr_b16 a[174:175], v35 offset:33792\n" \
"v_mfma_f32_16x16x32_bf16 v[160:163], a[152:155], a[188:191], v[160:163]\n" \
"ds_read_b64_tr_b16 a[236:237], v35 offset:40960\n" \
"v_mfma_f32_16x16x32_bf16 v[160:163], a[216:219], a[252:255], v[160:163]\n" \
"ds_read_b128 a[152:155], v28 offset:36864\n" \
"v_mfma_f32_16x16x32_bf16 v[176:179], a[156:159], a[188:191], v[176:179]\n" \
"ds_read_b128 a[216:219], v29 offset:36864\n" \
"v_mfma_f32_16x16x32_bf16 v[176:179], a[220:223], a[252:255], v[176:179]\n" \
"ds_read_b64_tr_b16 a[238:239], v35 offset:41984\n" \
"v_mfma_f32_16x16x32_bf16 v[172:175], a[156:159], a[184:187], v[172:175]\n" \
"s_cmp_lg_u32 s40, 0\n" \
"s_cselect_b32 s41, s41, s42\n" \
"v_mfma_f32_16x16x32_bf16 v[172:175], a[220:223], a[248:251], v[172:175]\n" \
"v_lshl_or_b32 v217, s5, 3, v212\n" \
"v_lshlrev_b32_e64 v218, 10, v213\n" \
"v_mfma_f32_16x16x32_bf16 v[168:171], a[156:159], a[180:183], v[168:171]\n" \
"v_lshlrev_b32_e32 v216, 7, v216\n" \
"v_and_b32_e32 v219, 24, v219\n" \
"v_mfma_f32_16x16x32_bf16 v[168:171], a[220:223], a[244:247], v[168:171]\n" \
"v_and_or_b32 v212, v212, 2, v215\n" \
"v_add_u32_e64 v217, s41, v217\n" \
"v_mfma_f32_16x16x32_bf16 v[164:167], a[156:159], a[176:179], v[164:167]\n" \
"v_or3_b32 v216, v216, v218, v219\n" \
"v_lshlrev_b32_e64 v212, 5, v212\n" \
"v_mfma_f32_16x16x32_bf16 v[164:167], a[220:223], a[240:243], v[164:167]\n" \
"ds_read_b128 a[156:159], v28 offset:38912\n" \
"ds_read_b128 a[220:223], v29 offset:38912\n" \
"s_waitcnt lgkmcnt(15)\n" \
"v_mfma_f32_16x16x32_bf16 a[0:3], a[128:131], a[160:163], a[0:3]\n" \
"s_addk_i32 s84, 0x7c00\n" \
"v_mfma_f32_16x16x32_bf16 a[0:3], a[192:195], a[224:227], a[0:3]\n" \
"buffer_load_dwordx4 v27, s[72:75], s84 offen offset:2048 lds\n" \
"v_mfma_f32_16x16x32_bf16 a[4:7], a[128:131], a[164:167], a[4:7]\n" \
"ds_read_b64_tr_b16 a[176:177], v36 offset:32768\n" \
"s_mov_b32 s41, 0x14000\n" \
"s_waitcnt lgkmcnt(15)\n" \
"v_mfma_f32_16x16x32_bf16 a[4:7], a[192:195], a[228:231], a[4:7]\n" \
"s_addk_i32 s84, 0x7c00\n" \
"s_waitcnt lgkmcnt(11)\n" \
"v_mfma_f32_16x16x32_bf16 a[8:11], a[128:131], a[168:171], a[8:11]\n" \
"buffer_load_dwordx4 v27, s[72:75], s84 offen offset:3072 lds\n" \
"s_waitcnt lgkmcnt(9)\n" \
"v_mfma_f32_16x16x32_bf16 a[8:11], a[192:195], a[232:235], a[8:11]\n" \
"ds_read_b64_tr_b16 a[178:179], v36 offset:33792\n" \
"v_or3_b32 v48, v216, v212, s41\n" \
"s_waitcnt lgkmcnt(8)\n" \
"v_mfma_f32_16x16x32_bf16 a[12:15], a[128:131], a[172:175], a[12:15]\n" \
"ds_read_b64_tr_b16 a[240:241], v36 offset:40960\n" \
"s_lshl_b32 s41, s5, 2\n" \
"s_waitcnt lgkmcnt(5)\n" \
"v_mfma_f32_16x16x32_bf16 a[12:15], a[192:195], a[236:239], a[12:15]\n" \
"s_lshl_b32 s7, s7, 3\n" \
"v_or_b32_e64 v212, s41, v213\n" \
"v_mfma_f32_16x16x32_bf16 a[28:31], a[132:135], a[172:175], a[28:31]\n" \
"s_or_b32 s7, s7, s41\n" \
"s_mov_b32 s41, 0xff800000\n" \
"v_mfma_f32_16x16x32_bf16 a[28:31], a[196:199], a[236:239], a[28:31]\n" \
"ds_read_b64_tr_b16 a[242:243], v36 offset:41984\n" \
"v_mfma_f32_16x16x32_bf16 a[24:27], a[132:135], a[168:171], a[24:27]\n" \
"s_cmp_lg_u32 s40, 0\n" \
"s_cselect_b32 s47, s42, s41\n" \
"v_mfma_f32_16x16x32_bf16 a[24:27], a[196:199], a[232:235], a[24:27]\n" \
"s_mov_b32 s40, 0xc000\n" \
"s_mov_b32 s41, 0x8000\n" \
"v_mfma_f32_16x16x32_bf16 a[20:23], a[132:135], a[164:167], a[20:23]\n" \
"s_cmp_lg_u32 s4, 0\n" \
"s_cselect_b32 s51, s40, s41\n" \
"v_mfma_f32_16x16x32_bf16 a[20:23], a[196:199], a[228:231], a[20:23]\n" \
"ds_read_b64_tr_b16 a[180:181], v37 offset:32768\n" \
"s_movk_i32 s40, 0x4000\n" \
"v_mfma_f32_16x16x32_bf16 a[16:19], a[132:135], a[160:163], a[16:19]\n" \
"s_cmp_lg_u32 s4, 0\n" \
"s_cselect_b32 s53, s41, s40\n" \
"v_mfma_f32_16x16x32_bf16 a[16:19], a[196:199], a[224:227], a[16:19]\n" \
"s_lshl_b32 s40, s2, 2\n" \
"s_and_b32 s40, s40, 28\n" \
"v_mfma_f32_16x16x32_bf16 a[32:35], a[136:139], a[160:163], a[32:35]\n" \
"s_waitcnt vmcnt(16)\n" \
"v_or_b32_e32 v204, v210, v204\n" \
"v_or_b32_e64 v206, v210, v206\n" \
"v_mfma_f32_16x16x32_bf16 a[32:35], a[200:203], a[224:227], a[32:35]\n" \
"v_mov_b32_e64 v210, 0\n" \
"s_add_u32 s88, s35, 0x1000\n" \
"v_mfma_f32_16x16x32_bf16 a[36:39], a[136:139], a[164:167], a[36:39]\n" \
"ds_read_b64_tr_b16 a[182:183], v37 offset:33792\n" \
"s_bfe_u32 s45, s2, 0x20003\n" \
"v_mfma_f32_16x16x32_bf16 a[36:39], a[200:203], a[228:231], a[36:39]\n" \
"ds_read_b64_tr_b16 a[244:245], v37 offset:40960\n" \
"s_lshr_b32 s44, s3, 1\n" \
"v_mfma_f32_16x16x32_bf16 a[40:43], a[136:139], a[168:171], a[40:43]\n" \
"s_mov_b32 m0, s32\n" \
"s_or_b32 s46, s40, s45\n" \
"v_mfma_f32_16x16x32_bf16 a[40:43], a[200:203], a[232:235], a[40:43]\n" \
"s_mov_b32 exec_lo, 0xffffffff\n" \
"s_mov_b32 exec_hi, 0x0\n" \
"global_load_lds_dword v[188:189], off\n" \
"s_mov_b64 exec, -1\n" \
"v_mfma_f32_16x16x32_bf16 a[44:47], a[136:139], a[172:175], a[44:47]\n" \
"ds_read_b64_tr_b16 a[246:247], v37 offset:41984\n" \
"s_lshl_b32 s43, s44, 15\n" \
"v_mfma_f32_16x16x32_bf16 a[44:47], a[200:203], a[236:239], a[44:47]\n" \
"s_lshl_b32 s41, s5, 14\n" \
"s_lshl_b32 s48, s46, 20\n" \
"v_mfma_f32_16x16x32_bf16 a[60:63], a[140:143], a[172:175], a[60:63]\n" \
"ds_write_b128 v196, v[200:203]\n" \
"v_mfma_f32_16x16x32_bf16 a[60:63], a[204:207], a[236:239], a[60:63]\n" \
"s_or_b32 s41, s43, s41\n" \
"s_add_i32 s49, s48, 0x1000000\n" \
"v_mfma_f32_16x16x32_bf16 a[56:59], a[140:143], a[168:171], a[56:59]\n" \
"ds_read_b64_tr_b16 a[184:185], v38 offset:32768\n" \
"s_or_b32 s42, s49, s41\n" \
"v_mfma_f32_16x16x32_bf16 a[56:59], a[204:207], a[232:235], a[56:59]\n" \
"s_lshl_b32 s4, s4, 16\n" \
"s_or_b32 s41, s42, s4\n" \
"v_mfma_f32_16x16x32_bf16 a[52:55], a[140:143], a[164:167], a[52:55]\n" \
"s_lshl_b32 s4, s6, 16\n" \
"s_or_b32 s42, s42, s4\n" \
"v_mfma_f32_16x16x32_bf16 a[52:55], a[204:207], a[228:231], a[52:55]\n" \
"ds_read_b64_tr_b16 a[186:187], v38 offset:33792\n" \
"s_lshl_b32 s4, s5, 11\n" \
"v_mfma_f32_16x16x32_bf16 a[48:51], a[140:143], a[160:163], a[48:51]\n" \
"s_mul_i32 s5, s7, 0x24800\n" \
"s_lshl_b32 s6, s46, 9\n" \
"v_mfma_f32_16x16x32_bf16 a[48:51], a[204:207], a[224:227], a[48:51]\n" \
"s_lshl_b32 s7, s40, 6\n" \
"s_add_i32 s6, s5, s6\n" \
"v_mfma_f32_16x16x32_bf16 a[64:67], a[144:147], a[160:163], a[64:67]\n" \
"ds_write_b128 v197, v[204:207]\n" \
"v_mfma_f32_16x16x32_bf16 a[64:67], a[208:211], a[224:227], a[64:67]\n" \
"ds_read_b64_tr_b16 a[248:249], v38 offset:40960\n" \
"s_or_b32 s5, s5, s7\n" \
"v_mfma_f32_16x16x32_bf16 a[68:71], a[144:147], a[164:167], a[68:71]\n" \
"s_lshl_b32 s7, s45, 2\n" \
"s_or_b32 s5, s5, s7\n" \
"v_mfma_f32_16x16x32_bf16 a[68:71], a[208:211], a[228:231], a[68:71]\n" \
"ds_read_b64_tr_b16 a[250:251], v38 offset:41984\n" \
"s_lshl_b32 s7, s3, 12\n" \
"v_mfma_f32_16x16x32_bf16 a[72:75], a[144:147], a[168:171], a[72:75]\n" \
"s_or_b32 s50, s49, s7\n" \
"s_lshl_b32 s7, s3, 11\n" \
"v_mfma_f32_16x16x32_bf16 a[72:75], a[208:211], a[232:235], a[72:75]\n" \
"v_lshrrev_b32_e32 v215, 4, v20\n" \
"v_bfe_u32 v216, v20, 4, 3\n" \
"v_mfma_f32_16x16x32_bf16 a[76:79], a[144:147], a[172:175], a[76:79]\n" \
"s_add_i32 s52, s48, s7\n" \
"s_add_i32 s53, s53, s7\n" \
"v_mfma_f32_16x16x32_bf16 a[76:79], a[208:211], a[236:239], a[76:79]\n" \
"ds_read_b64_tr_b16 a[188:189], v39 offset:32768\n" \
"v_mul_u32_u24_e32 v214, 0x480, v214\n" \
"v_mfma_f32_16x16x32_bf16 a[92:95], a[148:151], a[172:175], a[92:95]\n" \
"ds_write_b128 v198, v[208:211]\n" \
"v_mfma_f32_16x16x32_bf16 a[92:95], a[212:215], a[236:239], a[92:95]\n" \
"v_bitop3_b32 v218, v213, v215, 7 bitop3:0x78\n" \
"v_bitop3_b32 v213, v213, v216, 4 bitop3:0x36\n" \
"v_mfma_f32_16x16x32_bf16 a[88:91], a[148:151], a[168:171], a[88:91]\n" \
"ds_read_b64_tr_b16 a[190:191], v39 offset:33792\n" \
"s_lshl_b32 s7, s3, 10\n" \
"v_mfma_f32_16x16x32_bf16 a[88:91], a[212:215], a[232:235], a[88:91]\n" \
"s_or_b32 s4, s43, s4\n" \
"v_lshl_or_b32 v25, v213, 4, v214\n" \
"v_mfma_f32_16x16x32_bf16 a[84:87], a[148:151], a[164:167], a[84:87]\n" \
"v_mul_u32_u24_e32 v213, 0x480, v217\n" \
"v_bitop3_b32 v212, v215, v212, 7 bitop3:0x6c\n" \
"v_mfma_f32_16x16x32_bf16 a[84:87], a[212:215], a[228:231], a[84:87]\n" \
"ds_read_b64_tr_b16 a[252:253], v39 offset:40960\n" \
"s_or_b32 s40, s7, 0x14000\n" \
"v_mfma_f32_16x16x32_bf16 a[80:83], a[148:151], a[160:163], a[80:83]\n" \
"s_lshl_b32 s43, s44, 13\n" \
"s_mulk_i32 s44, 0xe800\n" \
"v_mfma_f32_16x16x32_bf16 a[80:83], a[212:215], a[224:227], a[80:83]\n" \
"s_lshl_b32 s7, s3, 8\n" \
"s_nop 0\n" \
"v_lshl_or_b32 v24, v218, 4, v214\n" \
"v_mfma_f32_16x16x32_bf16 a[96:99], a[152:155], a[160:163], a[96:99]\n" \
"ds_read_b64_tr_b16 a[254:255], v39 offset:41984\n" \
"v_lshl_or_b32 v21, v212, 4, v213\n" \
"v_mfma_f32_16x16x32_bf16 a[96:99], a[216:219], a[224:227], a[96:99]\n" \
"s_add_i32 s44, s44, 0x17000\n" \
"s_or_b32 s45, s7, 0x27000\n" \
"v_mfma_f32_16x16x32_bf16 a[100:103], a[152:155], a[164:167], a[100:103]\n" \
"s_or_b32 s46, s4, 0x10000\n" \
"s_add_i32 s48, s6, 0x3000000\n" \
"v_mfma_f32_16x16x32_bf16 a[100:103], a[216:219], a[228:231], a[100:103]\n" \
"s_add_i32 s49, s5, 0x3004000\n" \
"s_add_i32 s52, s52, 0x1010000\n" \
"v_mfma_f32_16x16x32_bf16 a[104:107], a[152:155], a[168:171], a[104:107]\n" \
"v_mfma_f32_16x16x32_bf16 a[104:107], a[216:219], a[232:235], a[104:107]\n" \
"v_mfma_f32_16x16x32_bf16 a[108:111], a[152:155], a[172:175], a[108:111]\n" \
"v_mfma_f32_16x16x32_bf16 a[108:111], a[216:219], a[236:239], a[108:111]\n" \
"s_waitcnt lgkmcnt(15)\n" \
"v_mfma_f32_16x16x32_bf16 a[124:127], a[156:159], a[172:175], a[124:127]\n" \
"v_mfma_f32_16x16x32_bf16 a[124:127], a[220:223], a[236:239], a[124:127]\n" \
"v_mfma_f32_16x16x32_bf16 a[120:123], a[156:159], a[168:171], a[120:123]\n" \
"v_mfma_f32_16x16x32_bf16 a[120:123], a[220:223], a[232:235], a[120:123]\n" \
"v_mfma_f32_16x16x32_bf16 a[116:119], a[156:159], a[164:167], a[116:119]\n" \
"v_mfma_f32_16x16x32_bf16 a[116:119], a[220:223], a[228:231], a[116:119]\n" \
"v_mfma_f32_16x16x32_bf16 a[112:115], a[156:159], a[160:163], a[112:115]\n" \
"v_mfma_f32_16x16x32_bf16 a[112:115], a[220:223], a[224:227], a[112:115]\n" \
"s_waitcnt vmcnt(1) lgkmcnt(0)\n" \
"s_barrier\n" \
"v_mfma_f32_16x16x32_bf16 v[52:55], a[128:131], a[176:179], v[52:55]\n" \
"s_add_u32 m0, s30, 0x8000\n" \
"s_movk_i32 s84, 0x280\n" \
"v_mfma_f32_16x16x32_bf16 v[52:55], a[192:195], a[240:243], v[52:55]\n" \
"buffer_load_dwordx4 v22, s[64:67], s84 offen offset:0 lds\n" \
"v_mfma_f32_16x16x32_bf16 v[56:59], a[128:131], a[180:183], v[56:59]\n" \
"s_movk_i32 s84, 0x1e80\n" \
"v_mfma_f32_16x16x32_bf16 v[56:59], a[192:195], a[244:247], v[56:59]\n" \
"buffer_load_dwordx4 v23, s[64:67], s84 offen offset:1024 lds\n" \
"v_mfma_f32_16x16x32_bf16 v[60:63], a[128:131], a[184:187], v[60:63]\n" \
"ds_read_b64_tr_b16 a[160:161], v32 offset:0\n" \
"v_mfma_f32_16x16x32_bf16 v[60:63], a[192:195], a[248:251], v[60:63]\n" \
"s_movk_i32 s84, 0x3a80\n" \
"v_mfma_f32_16x16x32_bf16 v[64:67], a[128:131], a[188:191], v[64:67]\n" \
"buffer_load_dwordx4 v22, s[64:67], s84 offen offset:2048 lds\n" \
"v_mfma_f32_16x16x32_bf16 v[64:67], a[192:195], a[252:255], v[64:67]\n" \
"ds_read_b128 a[128:131], v28 offset:16384\n" \
"v_mfma_f32_16x16x32_bf16 v[80:83], a[132:135], a[188:191], v[80:83]\n" \
"ds_read_b128 a[192:195], v29 offset:16384\n" \
"v_mfma_f32_16x16x32_bf16 v[80:83], a[196:199], a[252:255], v[80:83]\n" \
"s_movk_i32 s84, 0x5680\n" \
"s_nop 0\n" \
"v_mfma_f32_16x16x32_bf16 v[76:79], a[132:135], a[184:187], v[76:79]\n" \
"buffer_load_dwordx4 v23, s[64:67], s84 offen offset:3072 lds\n" \
"v_mfma_f32_16x16x32_bf16 v[76:79], a[196:199], a[248:251], v[76:79]\n" \
"s_add_u32 m0, s31, 0x8000\n" \
"s_mov_b32 s84, 0x280000\n" \
"v_mfma_f32_16x16x32_bf16 v[72:75], a[132:135], a[180:183], v[72:75]\n" \
"buffer_load_dwordx4 v26, s[68:71], s84 offen offset:0 lds\n" \
"v_mfma_f32_16x16x32_bf16 v[72:75], a[196:199], a[244:247], v[72:75]\n" \
"ds_read_b64_tr_b16 a[162:163], v32 offset:1024\n" \
"v_mfma_f32_16x16x32_bf16 v[68:71], a[132:135], a[176:179], v[68:71]\n" \
"ds_read_b64_tr_b16 a[224:225], v32 offset:8192\n" \
"v_mfma_f32_16x16x32_bf16 v[68:71], a[196:199], a[240:243], v[68:71]\n" \
"ds_read_b128 a[132:135], v28 offset:18432\n" \
"v_mfma_f32_16x16x32_bf16 v[84:87], a[136:139], a[176:179], v[84:87]\n" \
"ds_read_b128 a[196:199], v29 offset:18432\n" \
"v_mfma_f32_16x16x32_bf16 v[84:87], a[200:203], a[240:243], v[84:87]\n" \
"s_addk_i32 s84, 0x7c00\n" \
"v_mfma_f32_16x16x32_bf16 v[88:91], a[136:139], a[180:183], v[88:91]\n" \
"buffer_load_dwordx4 v26, s[68:71], s84 offen offset:1024 lds\n" \
"v_mfma_f32_16x16x32_bf16 v[88:91], a[200:203], a[244:247], v[88:91]\n" \
"s_addk_i32 s84, 0x7c00\n" \
"v_mfma_f32_16x16x32_bf16 v[92:95], a[136:139], a[184:187], v[92:95]\n" \
"buffer_load_dwordx4 v27, s[68:71], s84 offen offset:2048 lds\n" \
"v_mfma_f32_16x16x32_bf16 v[92:95], a[200:203], a[248:251], v[92:95]\n" \
"s_addk_i32 s84, 0x7c00\n" \
"s_nop 0\n" \
"v_mfma_f32_16x16x32_bf16 v[96:99], a[136:139], a[188:191], v[96:99]\n" \
"buffer_load_dwordx4 v27, s[68:71], s84 offen offset:3072 lds\n" \
"v_mfma_f32_16x16x32_bf16 v[96:99], a[200:203], a[252:255], v[96:99]\n" \
"ds_read_b128 a[136:139], v28 offset:20480\n" \
"v_mfma_f32_16x16x32_bf16 v[112:115], a[140:143], a[188:191], v[112:115]\n" \
"ds_read_b128 a[200:203], v29 offset:20480\n" \
"v_mfma_f32_16x16x32_bf16 v[112:115], a[204:207], a[252:255], v[112:115]\n" \
"s_add_u32 m0, s30, 0x9000\n" \
"s_mov_b32 s84, 0x8280\n" \
"v_mfma_f32_16x16x32_bf16 v[108:111], a[140:143], a[184:187], v[108:111]\n" \
"buffer_load_dwordx4 v22, s[64:67], s84 offen offset:0 lds\n" \
"v_mfma_f32_16x16x32_bf16 v[108:111], a[204:207], a[248:251], v[108:111]\n" \
"s_addk_i32 s84, 0x1c00\n" \
"v_mfma_f32_16x16x32_bf16 v[104:107], a[140:143], a[180:183], v[104:107]\n" \
"buffer_load_dwordx4 v23, s[64:67], s84 offen offset:1024 lds\n" \
"v_mfma_f32_16x16x32_bf16 v[104:107], a[204:207], a[244:247], v[104:107]\n" \
"s_addk_i32 s84, 0x1c00\n" \
"v_mfma_f32_16x16x32_bf16 v[100:103], a[140:143], a[176:179], v[100:103]\n" \
"buffer_load_dwordx4 v22, s[64:67], s84 offen offset:2048 lds\n" \
"v_mfma_f32_16x16x32_bf16 v[100:103], a[204:207], a[240:243], v[100:103]\n" \
"ds_read_b128 a[140:143], v28 offset:22528\n" \
"v_mfma_f32_16x16x32_bf16 v[116:119], a[144:147], a[176:179], v[116:119]\n" \
"ds_read_b128 a[204:207], v29 offset:22528\n" \
"v_mfma_f32_16x16x32_bf16 v[116:119], a[208:211], a[240:243], v[116:119]\n" \
"s_addk_i32 s84, 0x1c00\n" \
"v_mfma_f32_16x16x32_bf16 v[120:123], a[144:147], a[180:183], v[120:123]\n" \
"buffer_load_dwordx4 v23, s[64:67], s84 offen offset:3072 lds\n" \
"v_mfma_f32_16x16x32_bf16 v[120:123], a[208:211], a[244:247], v[120:123]\n" \
"s_add_u32 m0, s31, 0xc000\n" \
"s_mov_b32 s84, 0x280000\n" \
"v_mfma_f32_16x16x32_bf16 v[124:127], a[144:147], a[184:187], v[124:127]\n" \
"buffer_load_dwordx4 v26, s[72:75], s84 offen offset:0 lds\n" \
"v_mfma_f32_16x16x32_bf16 v[124:127], a[208:211], a[248:251], v[124:127]\n" \
"s_addk_i32 s84, 0x7c00\n" \
"v_mfma_f32_16x16x32_bf16 v[128:131], a[144:147], a[188:191], v[128:131]\n" \
"buffer_load_dwordx4 v26, s[72:75], s84 offen offset:1024 lds\n" \
"v_mfma_f32_16x16x32_bf16 v[128:131], a[208:211], a[252:255], v[128:131]\n" \
"ds_read_b128 a[144:147], v28 offset:0\n" \
"v_mfma_f32_16x16x32_bf16 v[144:147], a[148:151], a[188:191], v[144:147]\n" \
"ds_read_b128 a[208:211], v29 offset:0\n" \
"v_mfma_f32_16x16x32_bf16 v[144:147], a[212:215], a[252:255], v[144:147]\n" \
"ds_read_b128 v[200:203], v199 offset:0\n" \
"v_mfma_f32_16x16x32_bf16 v[140:143], a[148:151], a[184:187], v[140:143]\n" \
"ds_read_b128 v[204:207], v199 offset:4096\n" \
"v_mfma_f32_16x16x32_bf16 v[140:143], a[212:215], a[248:251], v[140:143]\n" \
"ds_read_b64_tr_b16 a[226:227], v32 offset:9216\n" \
"v_mfma_f32_16x16x32_bf16 v[136:139], a[148:151], a[180:183], v[136:139]\n" \
"ds_read_b64_tr_b16 a[164:165], v33 offset:0\n" \
"v_mfma_f32_16x16x32_bf16 v[136:139], a[212:215], a[244:247], v[136:139]\n" \
"ds_read_b128 v[208:211], v199 offset:8192\n" \
"v_mfma_f32_16x16x32_bf16 v[132:135], a[148:151], a[176:179], v[132:135]\n" \
"ds_read_b64_tr_b16 a[166:167], v33 offset:1024\n" \
"v_mfma_f32_16x16x32_bf16 v[132:135], a[212:215], a[240:243], v[132:135]\n" \
"ds_read_b128 a[148:151], v28 offset:2048\n" \
"v_mfma_f32_16x16x32_bf16 v[148:151], a[152:155], a[176:179], v[148:151]\n" \
"ds_read_b128 a[212:215], v29 offset:2048\n" \
"v_mfma_f32_16x16x32_bf16 v[148:151], a[216:219], a[240:243], v[148:151]\n" \
"ds_read_b64_tr_b16 a[228:229], v33 offset:8192\n" \
"v_mfma_f32_16x16x32_bf16 v[152:155], a[152:155], a[180:183], v[152:155]\n" \
"ds_read_b64_tr_b16 a[230:231], v33 offset:9216\n" \
"v_mfma_f32_16x16x32_bf16 v[152:155], a[216:219], a[244:247], v[152:155]\n" \
"ds_read_b64_tr_b16 a[168:169], v34 offset:0\n" \
"v_mfma_f32_16x16x32_bf16 v[156:159], a[152:155], a[184:187], v[156:159]\n" \
"ds_read_b64_tr_b16 a[170:171], v34 offset:1024\n" \
"v_mfma_f32_16x16x32_bf16 v[156:159], a[216:219], a[248:251], v[156:159]\n" \
"ds_read_b64_tr_b16 a[232:233], v34 offset:8192\n" \
"v_mfma_f32_16x16x32_bf16 v[160:163], a[152:155], a[188:191], v[160:163]\n" \
"ds_read_b64_tr_b16 a[234:235], v34 offset:9216\n" \
"v_mfma_f32_16x16x32_bf16 v[160:163], a[216:219], a[252:255], v[160:163]\n" \
"ds_read_b128 a[152:155], v28 offset:4096\n" \
"v_mfma_f32_16x16x32_bf16 v[176:179], a[156:159], a[188:191], v[176:179]\n" \
"ds_read_b128 a[216:219], v29 offset:4096\n" \
"v_mfma_f32_16x16x32_bf16 v[176:179], a[220:223], a[252:255], v[176:179]\n" \
"ds_read_b64_tr_b16 a[172:173], v35 offset:0\n" \
"v_mfma_f32_16x16x32_bf16 v[172:175], a[156:159], a[184:187], v[172:175]\n" \
"ds_read_b64_tr_b16 a[174:175], v35 offset:1024\n" \
"v_mfma_f32_16x16x32_bf16 v[172:175], a[220:223], a[248:251], v[172:175]\n" \
"ds_read_b64_tr_b16 a[236:237], v35 offset:8192\n" \
"v_mfma_f32_16x16x32_bf16 v[168:171], a[156:159], a[180:183], v[168:171]\n" \
"ds_read_b64_tr_b16 a[238:239], v35 offset:9216\n" \
"v_mfma_f32_16x16x32_bf16 v[168:171], a[220:223], a[244:247], v[168:171]\n" \
"v_mfma_f32_16x16x32_bf16 v[164:167], a[156:159], a[176:179], v[164:167]\n" \
"v_mfma_f32_16x16x32_bf16 v[164:167], a[220:223], a[240:243], v[164:167]\n" \
"ds_read_b128 a[156:159], v28 offset:6144\n" \
"ds_read_b128 a[220:223], v29 offset:6144\n" \
"s_waitcnt lgkmcnt(15)\n" \
"v_mfma_f32_16x16x32_bf16 a[0:3], a[128:131], a[160:163], a[0:3]\n" \
"s_addk_i32 s84, 0x7c00\n" \
"v_mfma_f32_16x16x32_bf16 a[0:3], a[192:195], a[224:227], a[0:3]\n" \
"buffer_load_dwordx4 v27, s[72:75], s84 offen offset:2048 lds\n" \
"v_mfma_f32_16x16x32_bf16 a[4:7], a[128:131], a[164:167], a[4:7]\n" \
"ds_read_b64_tr_b16 a[176:177], v36 offset:0\n" \
"s_waitcnt lgkmcnt(13)\n" \
"v_mfma_f32_16x16x32_bf16 a[4:7], a[192:195], a[228:231], a[4:7]\n" \
"s_addk_i32 s84, 0x7c00\n" \
"s_waitcnt lgkmcnt(11)\n" \
"v_mfma_f32_16x16x32_bf16 a[8:11], a[128:131], a[168:171], a[8:11]\n" \
"buffer_load_dwordx4 v27, s[72:75], s84 offen offset:3072 lds\n" \
"s_waitcnt lgkmcnt(9)\n" \
"v_mfma_f32_16x16x32_bf16 a[8:11], a[192:195], a[232:235], a[8:11]\n" \
"ds_read_b64_tr_b16 a[178:179], v36 offset:1024\n" \
"s_waitcnt lgkmcnt(6)\n" \
"v_mfma_f32_16x16x32_bf16 a[12:15], a[128:131], a[172:175], a[12:15]\n" \
"ds_read_b64_tr_b16 a[240:241], v36 offset:8192\n" \
"s_waitcnt lgkmcnt(5)\n" \
"v_mfma_f32_16x16x32_bf16 a[12:15], a[192:195], a[236:239], a[12:15]\n" \
"v_mfma_f32_16x16x32_bf16 a[28:31], a[132:135], a[172:175], a[28:31]\n" \
"v_mfma_f32_16x16x32_bf16 a[28:31], a[196:199], a[236:239], a[28:31]\n" \
"ds_read_b64_tr_b16 a[242:243], v36 offset:9216\n" \
"v_mfma_f32_16x16x32_bf16 a[24:27], a[132:135], a[168:171], a[24:27]\n" \
"v_mfma_f32_16x16x32_bf16 a[24:27], a[196:199], a[232:235], a[24:27]\n" \
"v_mfma_f32_16x16x32_bf16 a[20:23], a[132:135], a[164:167], a[20:23]\n" \
"v_mfma_f32_16x16x32_bf16 a[20:23], a[196:199], a[228:231], a[20:23]\n" \
"ds_read_b64_tr_b16 a[180:181], v37 offset:0\n" \
"v_mfma_f32_16x16x32_bf16 a[16:19], a[132:135], a[160:163], a[16:19]\n" \
"v_mfma_f32_16x16x32_bf16 a[16:19], a[196:199], a[224:227], a[16:19]\n" \
"v_mfma_f32_16x16x32_bf16 a[32:35], a[136:139], a[160:163], a[32:35]\n" \
"ds_read_b64_tr_b16 a[182:183], v37 offset:1024\n" \
"v_mfma_f32_16x16x32_bf16 a[32:35], a[200:203], a[224:227], a[32:35]\n" \
"v_mfma_f32_16x16x32_bf16 a[36:39], a[136:139], a[164:167], a[36:39]\n" \
"v_mfma_f32_16x16x32_bf16 a[36:39], a[200:203], a[228:231], a[36:39]\n" \
"buffer_store_dwordx4 v[200:203], v20, s[80:83], s35 offen sc1\n" \
"v_mfma_f32_16x16x32_bf16 a[40:43], a[136:139], a[168:171], a[40:43]\n" \
"s_mov_b32 m0, s32\n" \
"ds_read_b64_tr_b16 a[244:245], v37 offset:8192\n" \
"v_mfma_f32_16x16x32_bf16 a[40:43], a[200:203], a[232:235], a[40:43]\n" \
"s_mov_b32 exec_lo, 0x0\n" \
"s_mov_b32 exec_hi, 0xffffffff\n" \
"global_load_lds_dword v[188:189], off\n" \
"s_mov_b64 exec, -1\n" \
"v_mfma_f32_16x16x32_bf16 a[44:47], a[136:139], a[172:175], a[44:47]\n" \
"ds_read_b64_tr_b16 a[246:247], v37 offset:9216\n" \
"v_mfma_f32_16x16x32_bf16 a[44:47], a[200:203], a[236:239], a[44:47]\n" \
"v_mfma_f32_16x16x32_bf16 a[60:63], a[140:143], a[172:175], a[60:63]\n" \
"v_mfma_f32_16x16x32_bf16 a[60:63], a[204:207], a[236:239], a[60:63]\n" \
"v_mfma_f32_16x16x32_bf16 a[56:59], a[140:143], a[168:171], a[56:59]\n" \
"ds_read_b64_tr_b16 a[184:185], v38 offset:0\n" \
"v_mfma_f32_16x16x32_bf16 a[56:59], a[204:207], a[232:235], a[56:59]\n" \
"v_mfma_f32_16x16x32_bf16 a[52:55], a[140:143], a[164:167], a[52:55]\n" \
"buffer_store_dwordx4 v[204:207], v20, s[80:83], s88 offen sc1\n" \
"v_mfma_f32_16x16x32_bf16 a[52:55], a[204:207], a[228:231], a[52:55]\n" \
"ds_read_b64_tr_b16 a[186:187], v38 offset:1024\n" \
"v_mfma_f32_16x16x32_bf16 a[48:51], a[140:143], a[160:163], a[48:51]\n" \
"v_mfma_f32_16x16x32_bf16 a[48:51], a[204:207], a[224:227], a[48:51]\n" \
"v_mfma_f32_16x16x32_bf16 a[64:67], a[144:147], a[160:163], a[64:67]\n" \
"ds_read_b64_tr_b16 a[248:249], v38 offset:8192\n" \
"v_mfma_f32_16x16x32_bf16 a[64:67], a[208:211], a[224:227], a[64:67]\n" \
"v_mfma_f32_16x16x32_bf16 a[68:71], a[144:147], a[164:167], a[68:71]\n" \
"v_mfma_f32_16x16x32_bf16 a[68:71], a[208:211], a[228:231], a[68:71]\n" \
"ds_read_b64_tr_b16 a[250:251], v38 offset:9216\n" \
"v_mfma_f32_16x16x32_bf16 a[72:75], a[144:147], a[168:171], a[72:75]\n" \
"v_mfma_f32_16x16x32_bf16 a[72:75], a[208:211], a[232:235], a[72:75]\n" \
"v_mfma_f32_16x16x32_bf16 a[76:79], a[144:147], a[172:175], a[76:79]\n" \
"buffer_store_dwordx4 v[208:211], v20, s[80:83], s36 offen sc1\n" \
"v_mfma_f32_16x16x32_bf16 a[76:79], a[208:211], a[236:239], a[76:79]\n" \
"ds_read_b64_tr_b16 a[188:189], v39 offset:0\n" \
"v_mfma_f32_16x16x32_bf16 a[92:95], a[148:151], a[172:175], a[92:95]\n" \
"v_mfma_f32_16x16x32_bf16 a[92:95], a[212:215], a[236:239], a[92:95]\n" \
"v_mfma_f32_16x16x32_bf16 a[88:91], a[148:151], a[168:171], a[88:91]\n" \
"ds_read_b64_tr_b16 a[190:191], v39 offset:1024\n" \
"v_mfma_f32_16x16x32_bf16 a[88:91], a[212:215], a[232:235], a[88:91]\n" \
"v_mfma_f32_16x16x32_bf16 a[84:87], a[148:151], a[164:167], a[84:87]\n" \
"v_mfma_f32_16x16x32_bf16 a[84:87], a[212:215], a[228:231], a[84:87]\n" \
"ds_read_b64_tr_b16 a[252:253], v39 offset:8192\n" \
"v_mfma_f32_16x16x32_bf16 a[80:83], a[148:151], a[160:163], a[80:83]\n" \
"v_mfma_f32_16x16x32_bf16 a[80:83], a[212:215], a[224:227], a[80:83]\n" \
"v_mfma_f32_16x16x32_bf16 a[96:99], a[152:155], a[160:163], a[96:99]\n" \
"ds_read_b64_tr_b16 a[254:255], v39 offset:9216\n" \
"v_mfma_f32_16x16x32_bf16 a[96:99], a[216:219], a[224:227], a[96:99]\n" \
"v_mfma_f32_16x16x32_bf16 a[100:103], a[152:155], a[164:167], a[100:103]\n" \
".fill (L_pb + 52960 - .) / 4, 4, 0xBF800000\n" \
"L_pp13:\n" \
"s_cbranch_execz L_pret\n" \
"v_mfma_f32_16x16x32_bf16 a[100:103], a[216:219], a[228:231], a[100:103]\n" \
"v_mfma_f32_16x16x32_bf16 a[104:107], a[152:155], a[168:171], a[104:107]\n" \
"v_mfma_f32_16x16x32_bf16 a[104:107], a[216:219], a[232:235], a[104:107]\n" \
"v_mfma_f32_16x16x32_bf16 a[108:111], a[152:155], a[172:175], a[108:111]\n" \
"v_mfma_f32_16x16x32_bf16 a[108:111], a[216:219], a[236:239], a[108:111]\n" \
"s_waitcnt lgkmcnt(15)\n" \
"v_mfma_f32_16x16x32_bf16 a[124:127], a[156:159], a[172:175], a[124:127]\n" \
"v_mfma_f32_16x16x32_bf16 a[124:127], a[220:223], a[236:239], a[124:127]\n" \
"v_mfma_f32_16x16x32_bf16 a[120:123], a[156:159], a[168:171], a[120:123]\n" \
"v_mfma_f32_16x16x32_bf16 a[120:123], a[220:223], a[232:235], a[120:123]\n" \
"v_mfma_f32_16x16x32_bf16 a[116:119], a[156:159], a[164:167], a[116:119]\n" \
"v_mfma_f32_16x16x32_bf16 a[116:119], a[220:223], a[228:231], a[116:119]\n" \
"v_mfma_f32_16x16x32_bf16 a[112:115], a[156:159], a[160:163], a[112:115]\n" \
"v_mfma_f32_16x16x32_bf16 a[112:115], a[220:223], a[224:227], a[112:115]\n" \
"s_waitcnt vmcnt(4) lgkmcnt(0)\n" \
"s_barrier\n" \
"v_mfma_f32_16x16x32_bf16 v[52:55], a[128:131], a[176:179], v[52:55]\n" \
"s_add_u32 m0, s30, 0x0\n" \
"s_movk_i32 s84, 0x300\n" \
"v_mfma_f32_16x16x32_bf16 v[52:55], a[192:195], a[240:243], v[52:55]\n" \
"buffer_load_dwordx4 v22, s[64:67], s84 offen offset:0 lds\n" \
"v_mfma_f32_16x16x32_bf16 v[56:59], a[128:131], a[180:183], v[56:59]\n" \
"ds_read_b64_tr_b16 a[160:161], v32 offset:32768\n" \
"v_mfma_f32_16x16x32_bf16 v[56:59], a[192:195], a[244:247], v[56:59]\n" \
"s_movk_i32 s84, 0x1f00\n" \
"s_nop 0\n" \
"v_mfma_f32_16x16x32_bf16 v[60:63], a[128:131], a[184:187], v[60:63]\n" \
"buffer_load_dwordx4 v23, s[64:67], s84 offen offset:1024 lds\n" \
"v_mfma_f32_16x16x32_bf16 v[60:63], a[192:195], a[248:251], v[60:63]\n" \
"ds_read_b64_tr_b16 a[162:163], v32 offset:33792\n" \
"v_mfma_f32_16x16x32_bf16 v[64:67], a[128:131], a[188:191], v[64:67]\n" \
"ds_read_b64_tr_b16 a[224:225], v32 offset:40960\n" \
"v_mfma_f32_16x16x32_bf16 v[64:67], a[192:195], a[252:255], v[64:67]\n" \
"ds_read_b128 a[128:131], v28 offset:49152\n" \
"v_mfma_f32_16x16x32_bf16 v[80:83], a[132:135], a[188:191], v[80:83]\n" \
"ds_read_b128 a[192:195], v29 offset:49152\n" \
"v_mfma_f32_16x16x32_bf16 v[80:83], a[196:199], a[252:255], v[80:83]\n" \
"s_movk_i32 s84, 0x3b00\n" \
"v_mfma_f32_16x16x32_bf16 v[76:79], a[132:135], a[184:187], v[76:79]\n" \
"buffer_load_dwordx4 v22, s[64:67], s84 offen offset:2048 lds\n" \
"v_mfma_f32_16x16x32_bf16 v[76:79], a[196:199], a[248:251], v[76:79]\n" \
"s_movk_i32 s84, 0x5700\n" \
"v_mfma_f32_16x16x32_bf16 v[72:75], a[132:135], a[180:183], v[72:75]\n" \
"buffer_load_dwordx4 v23, s[64:67], s84 offen offset:3072 lds\n" \
"v_mfma_f32_16x16x32_bf16 v[72:75], a[196:199], a[244:247], v[72:75]\n" \
"s_add_u32 m0, s31, 0x0\n" \
"s_mov_b32 s84, 0x300000\n" \
"v_mfma_f32_16x16x32_bf16 v[68:71], a[132:135], a[176:179], v[68:71]\n" \
"buffer_load_dwordx4 v26, s[68:71], s84 offen offset:0 lds\n" \
"v_mfma_f32_16x16x32_bf16 v[68:71], a[196:199], a[240:243], v[68:71]\n" \
"ds_read_b128 a[132:135], v28 offset:51200\n" \
"v_mfma_f32_16x16x32_bf16 v[84:87], a[136:139], a[176:179], v[84:87]\n" \
"ds_read_b128 a[196:199], v29 offset:51200\n" \
"v_mfma_f32_16x16x32_bf16 v[84:87], a[200:203], a[240:243], v[84:87]\n" \
"s_addk_i32 s84, 0x7c00\n" \
"v_mfma_f32_16x16x32_bf16 v[88:91], a[136:139], a[180:183], v[88:91]\n" \
"buffer_load_dwordx4 v26, s[68:71], s84 offen offset:1024 lds\n" \
"v_mfma_f32_16x16x32_bf16 v[88:91], a[200:203], a[244:247], v[88:91]\n" \
"ds_read_b64_tr_b16 a[226:227], v32 offset:41984\n" \
"v_mfma_f32_16x16x32_bf16 v[92:95], a[136:139], a[184:187], v[92:95]\n" \
"s_addk_i32 s84, 0x7c00\n" \
"v_mfma_f32_16x16x32_bf16 v[92:95], a[200:203], a[248:251], v[92:95]\n" \
"buffer_load_dwordx4 v27, s[68:71], s84 offen offset:2048 lds\n" \
"v_mfma_f32_16x16x32_bf16 v[96:99], a[136:139], a[188:191], v[96:99]\n" \
"ds_read_b64_tr_b16 a[164:165], v33 offset:32768\n" \
"v_mfma_f32_16x16x32_bf16 v[96:99], a[200:203], a[252:255], v[96:99]\n" \
"ds_read_b128 a[136:139], v28 offset:53248\n" \
"v_mfma_f32_16x16x32_bf16 v[112:115], a[140:143], a[188:191], v[112:115]\n" \
"ds_read_b128 a[200:203], v29 offset:53248\n" \
"v_mfma_f32_16x16x32_bf16 v[112:115], a[204:207], a[252:255], v[112:115]\n" \
"s_addk_i32 s84, 0x7c00\n" \
"v_mfma_f32_16x16x32_bf16 v[108:111], a[140:143], a[184:187], v[108:111]\n" \
"buffer_load_dwordx4 v27, s[68:71], s84 offen offset:3072 lds\n" \
"v_mfma_f32_16x16x32_bf16 v[108:111], a[204:207], a[248:251], v[108:111]\n" \
"s_add_u32 m0, s30, 0x1000\n" \
"s_mov_b32 s84, 0x8300\n" \
"v_mfma_f32_16x16x32_bf16 v[104:107], a[140:143], a[180:183], v[104:107]\n" \
"buffer_load_dwordx4 v22, s[64:67], s84 offen offset:0 lds\n" \
"v_mfma_f32_16x16x32_bf16 v[104:107], a[204:207], a[244:247], v[104:107]\n" \
"ds_read_b64_tr_b16 a[166:167], v33 offset:33792\n" \
"v_mfma_f32_16x16x32_bf16 v[100:103], a[140:143], a[176:179], v[100:103]\n" \
"ds_read_b64_tr_b16 a[228:229], v33 offset:40960\n" \
"v_mfma_f32_16x16x32_bf16 v[100:103], a[204:207], a[240:243], v[100:103]\n" \
"ds_read_b128 a[140:143], v28 offset:55296\n" \
"v_mfma_f32_16x16x32_bf16 v[116:119], a[144:147], a[176:179], v[116:119]\n" \
"ds_read_b128 a[204:207], v29 offset:55296\n" \
"v_mfma_f32_16x16x32_bf16 v[116:119], a[208:211], a[240:243], v[116:119]\n" \
"s_addk_i32 s84, 0x1c00\n" \
"v_mfma_f32_16x16x32_bf16 v[120:123], a[144:147], a[180:183], v[120:123]\n" \
"buffer_load_dwordx4 v23, s[64:67], s84 offen offset:1024 lds\n" \
"v_mfma_f32_16x16x32_bf16 v[120:123], a[208:211], a[244:247], v[120:123]\n" \
"s_addk_i32 s84, 0x1c00\n" \
"v_mfma_f32_16x16x32_bf16 v[124:127], a[144:147], a[184:187], v[124:127]\n" \
"buffer_load_dwordx4 v22, s[64:67], s84 offen offset:2048 lds\n" \
"v_mfma_f32_16x16x32_bf16 v[124:127], a[208:211], a[248:251], v[124:127]\n" \
"s_addk_i32 s84, 0x1c00\n" \
"s_nop 0\n" \
"v_mfma_f32_16x16x32_bf16 v[128:131], a[144:147], a[188:191], v[128:131]\n" \
"buffer_load_dwordx4 v23, s[64:67], s84 offen offset:3072 lds\n" \
"v_mfma_f32_16x16x32_bf16 v[128:131], a[208:211], a[252:255], v[128:131]\n" \
"ds_read_b128 a[144:147], v28 offset:32768\n" \
"v_mfma_f32_16x16x32_bf16 v[144:147], a[148:151], a[188:191], v[144:147]\n" \
"ds_read_b128 a[208:211], v29 offset:32768\n" \
"v_mfma_f32_16x16x32_bf16 v[144:147], a[212:215], a[252:255], v[144:147]\n" \
"s_add_u32 m0, s31, 0x4000\n" \
"s_mov_b32 s84, 0x300000\n" \
"v_mfma_f32_16x16x32_bf16 v[140:143], a[148:151], a[184:187], v[140:143]\n" \
"buffer_load_dwordx4 v26, s[72:75], s84 offen offset:0 lds\n" \
"v_mfma_f32_16x16x32_bf16 v[140:143], a[212:215], a[248:251], v[140:143]\n" \
"ds_read_b64_tr_b16 a[230:231], v33 offset:41984\n" \
"v_mfma_f32_16x16x32_bf16 v[136:139], a[148:151], a[180:183], v[136:139]\n" \
"s_addk_i32 s84, 0x7c00\n" \
"s_nop 0\n" \
"v_mfma_f32_16x16x32_bf16 v[136:139], a[212:215], a[244:247], v[136:139]\n" \
"buffer_load_dwordx4 v26, s[72:75], s84 offen offset:1024 lds\n" \
"v_mfma_f32_16x16x32_bf16 v[132:135], a[148:151], a[176:179], v[132:135]\n" \
"ds_read_b64_tr_b16 a[168:169], v34 offset:32768\n" \
"v_mfma_f32_16x16x32_bf16 v[132:135], a[212:215], a[240:243], v[132:135]\n" \
"ds_read_b128 a[148:151], v28 offset:34816\n" \
"v_mfma_f32_16x16x32_bf16 v[148:151], a[152:155], a[176:179], v[148:151]\n" \
"ds_read_b128 a[212:215], v29 offset:34816\n" \
"v_mfma_f32_16x16x32_bf16 v[148:151], a[216:219], a[240:243], v[148:151]\n" \
"ds_read_b64_tr_b16 a[170:171], v34 offset:33792\n" \
"v_mfma_f32_16x16x32_bf16 v[152:155], a[152:155], a[180:183], v[152:155]\n" \
"ds_read_b64_tr_b16 a[232:233], v34 offset:40960\n" \
"v_mfma_f32_16x16x32_bf16 v[152:155], a[216:219], a[244:247], v[152:155]\n" \
"ds_read_b64_tr_b16 a[234:235], v34 offset:41984\n" \
"v_mfma_f32_16x16x32_bf16 v[156:159], a[152:155], a[184:187], v[156:159]\n" \
"ds_read_b64_tr_b16 a[172:173], v35 offset:32768\n" \
"v_mfma_f32_16x16x32_bf16 v[156:159], a[216:219], a[248:251], v[156:159]\n" \
"ds_read_b64_tr_b16 a[174:175], v35 offset:33792\n" \
"v_mfma_f32_16x16x32_bf16 v[160:163], a[152:155], a[188:191], v[160:163]\n" \
"ds_read_b64_tr_b16 a[236:237], v35 offset:40960\n" \
"v_mfma_f32_16x16x32_bf16 v[160:163], a[216:219], a[252:255], v[160:163]\n" \
"ds_read_b128 a[152:155], v28 offset:36864\n" \
"v_mfma_f32_16x16x32_bf16 v[176:179], a[156:159], a[188:191], v[176:179]\n" \
"ds_read_b128 a[216:219], v29 offset:36864\n" \
"v_mfma_f32_16x16x32_bf16 v[176:179], a[220:223], a[252:255], v[176:179]\n" \
"ds_read_b64_tr_b16 a[238:239], v35 offset:41984\n" \
"v_mfma_f32_16x16x32_bf16 v[172:175], a[156:159], a[184:187], v[172:175]\n" \
"v_mfma_f32_16x16x32_bf16 v[172:175], a[220:223], a[248:251], v[172:175]\n" \
"v_mfma_f32_16x16x32_bf16 v[168:171], a[156:159], a[180:183], v[168:171]\n" \
"v_mfma_f32_16x16x32_bf16 v[168:171], a[220:223], a[244:247], v[168:171]\n" \
"v_mfma_f32_16x16x32_bf16 v[164:167], a[156:159], a[176:179], v[164:167]\n" \
"v_mfma_f32_16x16x32_bf16 v[164:167], a[220:223], a[240:243], v[164:167]\n" \
"ds_read_b128 a[156:159], v28 offset:38912\n" \
"ds_read_b128 a[220:223], v29 offset:38912\n" \
"s_waitcnt lgkmcnt(15)\n" \
"v_mfma_f32_16x16x32_bf16 a[0:3], a[128:131], a[160:163], a[0:3]\n" \
"s_addk_i32 s84, 0x7c00\n" \
"v_mfma_f32_16x16x32_bf16 a[0:3], a[192:195], a[224:227], a[0:3]\n" \
"buffer_load_dwordx4 v27, s[72:75], s84 offen offset:2048 lds\n" \
"v_mfma_f32_16x16x32_bf16 a[4:7], a[128:131], a[164:167], a[4:7]\n" \
"ds_read_b64_tr_b16 a[176:177], v36 offset:32768\n" \
"s_waitcnt lgkmcnt(15)\n" \
"v_mfma_f32_16x16x32_bf16 a[4:7], a[192:195], a[228:231], a[4:7]\n" \
"s_addk_i32 s84, 0x7c00\n" \
"s_waitcnt lgkmcnt(11)\n" \
"v_mfma_f32_16x16x32_bf16 a[8:11], a[128:131], a[168:171], a[8:11]\n" \
"buffer_load_dwordx4 v27, s[72:75], s84 offen offset:3072 lds\n" \
"s_waitcnt lgkmcnt(9)\n" \
"v_mfma_f32_16x16x32_bf16 a[8:11], a[192:195], a[232:235], a[8:11]\n" \
"ds_read_b64_tr_b16 a[178:179], v36 offset:33792\n" \
"s_waitcnt lgkmcnt(8)\n" \
"v_mfma_f32_16x16x32_bf16 a[12:15], a[128:131], a[172:175], a[12:15]\n" \
"ds_read_b64_tr_b16 a[240:241], v36 offset:40960\n" \
"s_waitcnt lgkmcnt(5)\n" \
"v_mfma_f32_16x16x32_bf16 a[12:15], a[192:195], a[236:239], a[12:15]\n" \
"v_mfma_f32_16x16x32_bf16 a[28:31], a[132:135], a[172:175], a[28:31]\n" \
"v_mfma_f32_16x16x32_bf16 a[28:31], a[196:199], a[236:239], a[28:31]\n" \
"ds_read_b64_tr_b16 a[242:243], v36 offset:41984\n" \
"v_mfma_f32_16x16x32_bf16 a[24:27], a[132:135], a[168:171], a[24:27]\n" \
"v_mfma_f32_16x16x32_bf16 a[24:27], a[196:199], a[232:235], a[24:27]\n" \
"v_mfma_f32_16x16x32_bf16 a[20:23], a[132:135], a[164:167], a[20:23]\n" \
"v_mfma_f32_16x16x32_bf16 a[20:23], a[196:199], a[228:231], a[20:23]\n" \
"ds_read_b64_tr_b16 a[180:181], v37 offset:32768\n" \
"v_mfma_f32_16x16x32_bf16 a[16:19], a[132:135], a[160:163], a[16:19]\n" \
"v_mfma_f32_16x16x32_bf16 a[16:19], a[196:199], a[224:227], a[16:19]\n" \
"v_mfma_f32_16x16x32_bf16 a[32:35], a[136:139], a[160:163], a[32:35]\n" \
"ds_read_b64_tr_b16 a[182:183], v37 offset:33792\n" \
"v_mfma_f32_16x16x32_bf16 a[32:35], a[200:203], a[224:227], a[32:35]\n" \
"v_mfma_f32_16x16x32_bf16 a[36:39], a[136:139], a[164:167], a[36:39]\n" \
"v_lshlrev_b32_e32 v45, 3, v20\n" \
"v_add_u32_e32 v45, s31, v45\n" \
"v_mfma_f32_16x16x32_bf16 a[36:39], a[200:203], a[228:231], a[36:39]\n" \
"s_getpc_b64 s[86:87]\n" \
"s_sub_u32 s86, s86, 0x10800\n" \
"s_subb_u32 s87, s87, 0\n" \
"s_mov_b32 m0, s32\n" \
"s_nop 0\n" \
"v_mfma_f32_16x16x32_bf16 a[40:43], a[136:139], a[168:171], a[40:43]\n" \
"global_load_lds_dword v45, s[86:87]\n" \
"v_mfma_f32_16x16x32_bf16 a[40:43], a[200:203], a[232:235], a[40:43]\n" \
"ds_read_b64_tr_b16 a[244:245], v37 offset:40960\n" \
"v_mfma_f32_16x16x32_bf16 a[44:47], a[136:139], a[172:175], a[44:47]\n" \
"ds_read_b64_tr_b16 a[246:247], v37 offset:41984\n" \
"v_mfma_f32_16x16x32_bf16 a[44:47], a[200:203], a[236:239], a[44:47]\n" \
"v_mfma_f32_16x16x32_bf16 a[60:63], a[140:143], a[172:175], a[60:63]\n" \
"v_mfma_f32_16x16x32_bf16 a[60:63], a[204:207], a[236:239], a[60:63]\n" \
"v_mfma_f32_16x16x32_bf16 a[56:59], a[140:143], a[168:171], a[56:59]\n" \
"ds_read_b64_tr_b16 a[184:185], v38 offset:32768\n" \
"v_mfma_f32_16x16x32_bf16 a[56:59], a[204:207], a[232:235], a[56:59]\n" \
"v_mfma_f32_16x16x32_bf16 a[52:55], a[140:143], a[164:167], a[52:55]\n" \
"v_mfma_f32_16x16x32_bf16 a[52:55], a[204:207], a[228:231], a[52:55]\n" \
"ds_read_b64_tr_b16 a[186:187], v38 offset:33792\n" \
"v_mfma_f32_16x16x32_bf16 a[48:51], a[140:143], a[160:163], a[48:51]\n" \
"v_mfma_f32_16x16x32_bf16 a[48:51], a[204:207], a[224:227], a[48:51]\n" \
"v_mfma_f32_16x16x32_bf16 a[64:67], a[144:147], a[160:163], a[64:67]\n" \
"ds_read_b64_tr_b16 a[248:249], v38 offset:40960\n" \
"v_mfma_f32_16x16x32_bf16 a[64:67], a[208:211], a[224:227], a[64:67]\n" \
"v_mfma_f32_16x16x32_bf16 a[68:71], a[144:147], a[164:167], a[68:71]\n" \
"v_mfma_f32_16x16x32_bf16 a[68:71], a[208:211], a[228:231], a[68:71]\n" \
"ds_read_b64_tr_b16 a[250:251], v38 offset:41984\n" \
"v_mfma_f32_16x16x32_bf16 a[72:75], a[144:147], a[168:171], a[72:75]\n" \
"v_mfma_f32_16x16x32_bf16 a[72:75], a[208:211], a[232:235], a[72:75]\n" \
"v_mfma_f32_16x16x32_bf16 a[76:79], a[144:147], a[172:175], a[76:79]\n" \
"v_mfma_f32_16x16x32_bf16 a[76:79], a[208:211], a[236:239], a[76:79]\n" \
"ds_read_b64_tr_b16 a[188:189], v39 offset:32768\n" \
"v_mfma_f32_16x16x32_bf16 a[92:95], a[148:151], a[172:175], a[92:95]\n" \
"v_mfma_f32_16x16x32_bf16 a[92:95], a[212:215], a[236:239], a[92:95]\n" \
"v_mfma_f32_16x16x32_bf16 a[88:91], a[148:151], a[168:171], a[88:91]\n" \
"ds_read_b64_tr_b16 a[190:191], v39 offset:33792\n" \
"v_mfma_f32_16x16x32_bf16 a[88:91], a[212:215], a[232:235], a[88:91]\n" \
"v_mfma_f32_16x16x32_bf16 a[84:87], a[148:151], a[164:167], a[84:87]\n" \
"v_mfma_f32_16x16x32_bf16 a[84:87], a[212:215], a[228:231], a[84:87]\n" \
"ds_read_b64_tr_b16 a[252:253], v39 offset:40960\n" \
"v_mfma_f32_16x16x32_bf16 a[80:83], a[148:151], a[160:163], a[80:83]\n" \
"v_mfma_f32_16x16x32_bf16 a[80:83], a[212:215], a[224:227], a[80:83]\n" \
"v_mfma_f32_16x16x32_bf16 a[96:99], a[152:155], a[160:163], a[96:99]\n" \
"ds_read_b64_tr_b16 a[254:255], v39 offset:41984\n" \
"v_mfma_f32_16x16x32_bf16 a[96:99], a[216:219], a[224:227], a[96:99]\n" \
"v_mfma_f32_16x16x32_bf16 a[100:103], a[152:155], a[164:167], a[100:103]\n" \
"v_mfma_f32_16x16x32_bf16 a[100:103], a[216:219], a[228:231], a[100:103]\n" \
"v_mfma_f32_16x16x32_bf16 a[104:107], a[152:155], a[168:171], a[104:107]\n" \
"v_mfma_f32_16x16x32_bf16 a[104:107], a[216:219], a[232:235], a[104:107]\n" \
"v_mfma_f32_16x16x32_bf16 a[108:111], a[152:155], a[172:175], a[108:111]\n" \
"v_mfma_f32_16x16x32_bf16 a[108:111], a[216:219], a[236:239], a[108:111]\n" \
"s_waitcnt lgkmcnt(15)\n" \
"v_mfma_f32_16x16x32_bf16 a[124:127], a[156:159], a[172:175], a[124:127]\n" \
"v_mfma_f32_16x16x32_bf16 a[124:127], a[220:223], a[236:239], a[124:127]\n" \
"v_mfma_f32_16x16x32_bf16 a[120:123], a[156:159], a[168:171], a[120:123]\n" \
"v_mfma_f32_16x16x32_bf16 a[120:123], a[220:223], a[232:235], a[120:123]\n" \
"v_mfma_f32_16x16x32_bf16 a[116:119], a[156:159], a[164:167], a[116:119]\n" \
"v_mfma_f32_16x16x32_bf16 a[116:119], a[220:223], a[228:231], a[116:119]\n" \
"v_mfma_f32_16x16x32_bf16 a[112:115], a[156:159], a[160:163], a[112:115]\n" \
"v_mfma_f32_16x16x32_bf16 a[112:115], a[220:223], a[224:227], a[112:115]\n" \
"s_waitcnt vmcnt(1) lgkmcnt(0)\n" \
"s_barrier\n" \
"v_mov_b32_e32 v45, s62\n" \
"s_mov_b64 exec, 1\n" \
"buffer_store_dword v45, off, s[80:83], s37 offset:128 sc1\n" \
"s_mov_b64 exec, -1\n" \
"v_mfma_f32_16x16x32_bf16 v[52:55], a[128:131], a[176:179], v[52:55]\n" \
"s_add_u32 m0, s30, 0x8000\n" \
"s_movk_i32 s84, 0x380\n" \
"v_mfma_f32_16x16x32_bf16 v[52:55], a[192:195], a[240:243], v[52:55]\n" \
"buffer_load_dwordx4 v22, s[64:67], s84 offen offset:0 lds\n" \
"v_mfma_f32_16x16x32_bf16 v[56:59], a[128:131], a[180:183], v[56:59]\n" \
"ds_read_b64_tr_b16 a[160:161], v32 offset:0\n" \
"v_mfma_f32_16x16x32_bf16 v[56:59], a[192:195], a[244:247], v[56:59]\n" \
"s_movk_i32 s84, 0x1f80\n" \
"v_mfma_f32_16x16x32_bf16 v[60:63], a[128:131], a[184:187], v[60:63]\n" \
"buffer_load_dwordx4 v23, s[64:67], s84 offen offset:1024 lds\n" \
"v_mfma_f32_16x16x32_bf16 v[60:63], a[192:195], a[248:251], v[60:63]\n" \
"ds_read_b64_tr_b16 a[162:163], v32 offset:1024\n" \
"v_mfma_f32_16x16x32_bf16 v[64:67], a[128:131], a[188:191], v[64:67]\n" \
"ds_read_b64_tr_b16 a[224:225], v32 offset:8192\n" \
"v_mfma_f32_16x16x32_bf16 v[64:67], a[192:195], a[252:255], v[64:67]\n" \
"ds_read_b128 a[128:131], v28 offset:16384\n" \
"v_mfma_f32_16x16x32_bf16 v[80:83], a[132:135], a[188:191], v[80:83]\n" \
"ds_read_b128 a[192:195], v29 offset:16384\n" \
"v_mfma_f32_16x16x32_bf16 v[80:83], a[196:199], a[252:255], v[80:83]\n" \
"s_movk_i32 s84, 0x3b80\n" \
"v_mfma_f32_16x16x32_bf16 v[76:79], a[132:135], a[184:187], v[76:79]\n" \
"buffer_load_dwordx4 v22, s[64:67], s84 offen offset:2048 lds\n" \
"v_mfma_f32_16x16x32_bf16 v[76:79], a[196:199], a[248:251], v[76:79]\n" \
"s_movk_i32 s84, 0x5780\n" \
"v_mfma_f32_16x16x32_bf16 v[72:75], a[132:135], a[180:183], v[72:75]\n" \
"buffer_load_dwordx4 v23, s[64:67], s84 offen offset:3072 lds\n" \
"v_mfma_f32_16x16x32_bf16 v[72:75], a[196:199], a[244:247], v[72:75]\n" \
"s_add_u32 m0, s31, 0x8000\n" \
"s_mov_b32 s84, 0x380000\n" \
"v_mfma_f32_16x16x32_bf16 v[68:71], a[132:135], a[176:179], v[68:71]\n" \
"buffer_load_dwordx4 v26, s[68:71], s84 offen offset:0 lds\n" \
"v_mfma_f32_16x16x32_bf16 v[68:71], a[196:199], a[240:243], v[68:71]\n" \
"ds_read_b128 a[132:135], v28 offset:18432\n" \
"v_mfma_f32_16x16x32_bf16 v[84:87], a[136:139], a[176:179], v[84:87]\n" \
"ds_read_b128 a[196:199], v29 offset:18432\n" \
"v_mfma_f32_16x16x32_bf16 v[84:87], a[200:203], a[240:243], v[84:87]\n" \
"s_addk_i32 s84, 0x7c00\n" \
"v_mfma_f32_16x16x32_bf16 v[88:91], a[136:139], a[180:183], v[88:91]\n" \
"buffer_load_dwordx4 v26, s[68:71], s84 offen offset:1024 lds\n" \
"v_mfma_f32_16x16x32_bf16 v[88:91], a[200:203], a[244:247], v[88:91]\n" \
"ds_read_b64_tr_b16 a[226:227], v32 offset:9216\n" \
"v_mfma_f32_16x16x32_bf16 v[92:95], a[136:139], a[184:187], v[92:95]\n" \
"s_addk_i32 s84, 0x7c00\n" \
"v_mfma_f32_16x16x32_bf16 v[92:95], a[200:203], a[248:251], v[92:95]\n" \
"buffer_load_dwordx4 v27, s[68:71], s84 offen offset:2048 lds\n" \
"v_mfma_f32_16x16x32_bf16 v[96:99], a[136:139], a[188:191], v[96:99]\n" \
"ds_read_b64_tr_b16 a[164:165], v33 offset:0\n" \
"v_mfma_f32_16x16x32_bf16 v[96:99], a[200:203], a[252:255], v[96:99]\n" \
"ds_read_b128 a[136:139], v28 offset:20480\n" \
"v_mfma_f32_16x16x32_bf16 v[112:115], a[140:143], a[188:191], v[112:115]\n" \
"ds_read_b128 a[200:203], v29 offset:20480\n" \
"v_mfma_f32_16x16x32_bf16 v[112:115], a[204:207], a[252:255], v[112:115]\n" \
"s_addk_i32 s84, 0x7c00\n" \
"s_nop 0\n" \
"v_mfma_f32_16x16x32_bf16 v[108:111], a[140:143], a[184:187], v[108:111]\n" \
"buffer_load_dwordx4 v27, s[68:71], s84 offen offset:3072 lds\n" \
"v_mfma_f32_16x16x32_bf16 v[108:111], a[204:207], a[248:251], v[108:111]\n" \
"s_add_u32 m0, s30, 0x9000\n" \
"s_mov_b32 s84, 0x8380\n" \
"v_mfma_f32_16x16x32_bf16 v[104:107], a[140:143], a[180:183], v[104:107]\n" \
"buffer_load_dwordx4 v22, s[64:67], s84 offen offset:0 lds\n" \
"v_mfma_f32_16x16x32_bf16 v[104:107], a[204:207], a[244:247], v[104:107]\n" \
"ds_read_b64_tr_b16 a[166:167], v33 offset:1024\n" \
"v_mfma_f32_16x16x32_bf16 v[100:103], a[140:143], a[176:179], v[100:103]\n" \
"ds_read_b64_tr_b16 a[228:229], v33 offset:8192\n" \
"v_mfma_f32_16x16x32_bf16 v[100:103], a[204:207], a[240:243], v[100:103]\n" \
"ds_read_b128 a[140:143], v28 offset:22528\n" \
"v_mfma_f32_16x16x32_bf16 v[116:119], a[144:147], a[176:179], v[116:119]\n" \
"ds_read_b128 a[204:207], v29 offset:22528\n" \
"v_mfma_f32_16x16x32_bf16 v[116:119], a[208:211], a[240:243], v[116:119]\n" \
"s_addk_i32 s84, 0x1c00\n" \
"v_mfma_f32_16x16x32_bf16 v[120:123], a[144:147], a[180:183], v[120:123]\n" \
"buffer_load_dwordx4 v23, s[64:67], s84 offen offset:1024 lds\n" \
"v_mfma_f32_16x16x32_bf16 v[120:123], a[208:211], a[244:247], v[120:123]\n" \
"s_addk_i32 s84, 0x1c00\n" \
"v_mfma_f32_16x16x32_bf16 v[124:127], a[144:147], a[184:187], v[124:127]\n" \
"buffer_load_dwordx4 v22, s[64:67], s84 offen offset:2048 lds\n" \
"v_mfma_f32_16x16x32_bf16 v[124:127], a[208:211], a[248:251], v[124:127]\n" \
"s_addk_i32 s84, 0x1c00\n" \
"s_nop 0\n" \
"v_mfma_f32_16x16x32_bf16 v[128:131], a[144:147], a[188:191], v[128:131]\n" \
"buffer_load_dwordx4 v23, s[64:67], s84 offen offset:3072 lds\n" \
"v_mfma_f32_16x16x32_bf16 v[128:131], a[208:211], a[252:255], v[128:131]\n" \
"ds_read_b128 a[144:147], v28 offset:0\n" \
"v_mfma_f32_16x16x32_bf16 v[144:147], a[148:151], a[188:191], v[144:147]\n" \
"ds_read_b128 a[208:211], v29 offset:0\n" \
"v_mfma_f32_16x16x32_bf16 v[144:147], a[212:215], a[252:255], v[144:147]\n" \
"s_add_u32 m0, s31, 0xc000\n" \
"s_mov_b32 s84, 0x380000\n" \
"v_mfma_f32_16x16x32_bf16 v[140:143], a[148:151], a[184:187], v[140:143]\n" \
"buffer_load_dwordx4 v26, s[72:75], s84 offen offset:0 lds\n" \
"v_mfma_f32_16x16x32_bf16 v[140:143], a[212:215], a[248:251], v[140:143]\n" \
"ds_read_b64_tr_b16 a[230:231], v33 offset:9216\n" \
"v_mfma_f32_16x16x32_bf16 v[136:139], a[148:151], a[180:183], v[136:139]\n" \
"s_addk_i32 s84, 0x7c00\n" \
"s_nop 0\n" \
"v_mfma_f32_16x16x32_bf16 v[136:139], a[212:215], a[244:247], v[136:139]\n" \
"buffer_load_dwordx4 v26, s[72:75], s84 offen offset:1024 lds\n" \
"v_mfma_f32_16x16x32_bf16 v[132:135], a[148:151], a[176:179], v[132:135]\n" \
"ds_read_b64_tr_b16 a[168:169], v34 offset:0\n" \
"v_mfma_f32_16x16x32_bf16 v[132:135], a[212:215], a[240:243], v[132:135]\n" \
"ds_read_b128 a[148:151], v28 offset:2048\n" \
"v_mfma_f32_16x16x32_bf16 v[148:151], a[152:155], a[176:179], v[148:151]\n" \
"ds_read_b128 a[212:215], v29 offset:2048\n" \
"v_mfma_f32_16x16x32_bf16 v[148:151], a[216:219], a[240:243], v[148:151]\n" \
"ds_read_b64_tr_b16 a[170:171], v34 offset:1024\n" \
"v_mfma_f32_16x16x32_bf16 v[152:155], a[152:155], a[180:183], v[152:155]\n" \
"ds_read_b64_tr_b16 a[232:233], v34 offset:8192\n" \
"v_mfma_f32_16x16x32_bf16 v[152:155], a[216:219], a[244:247], v[152:155]\n" \
"ds_read_b64_tr_b16 a[234:235], v34 offset:9216\n" \
"v_mfma_f32_16x16x32_bf16 v[156:159], a[152:155], a[184:187], v[156:159]\n" \
"ds_read_b64_tr_b16 a[172:173], v35 offset:0\n" \
"v_mfma_f32_16x16x32_bf16 v[156:159], a[216:219], a[248:251], v[156:159]\n" \
"ds_read_b64_tr_b16 a[174:175], v35 offset:1024\n" \
"v_mfma_f32_16x16x32_bf16 v[160:163], a[152:155], a[188:191], v[160:163]\n" \
"ds_read_b64_tr_b16 a[236:237], v35 offset:8192\n" \
"v_mfma_f32_16x16x32_bf16 v[160:163], a[216:219], a[252:255], v[160:163]\n" \
"ds_read_b128 a[152:155], v28 offset:4096\n" \
"v_mfma_f32_16x16x32_bf16 v[176:179], a[156:159], a[188:191], v[176:179]\n" \
"ds_read_b128 a[216:219], v29 offset:4096\n" \
"v_mfma_f32_16x16x32_bf16 v[176:179], a[220:223], a[252:255], v[176:179]\n" \
"ds_read_b64_tr_b16 a[238:239], v35 offset:9216\n" \
"v_mfma_f32_16x16x32_bf16 v[172:175], a[156:159], a[184:187], v[172:175]\n" \
"v_mfma_f32_16x16x32_bf16 v[172:175], a[220:223], a[248:251], v[172:175]\n" \
"v_mfma_f32_16x16x32_bf16 v[168:171], a[156:159], a[180:183], v[168:171]\n" \
"v_mfma_f32_16x16x32_bf16 v[168:171], a[220:223], a[244:247], v[168:171]\n" \
"v_mfma_f32_16x16x32_bf16 v[164:167], a[156:159], a[176:179], v[164:167]\n" \
"v_mfma_f32_16x16x32_bf16 v[164:167], a[220:223], a[240:243], v[164:167]\n" \
"ds_read_b128 a[156:159], v28 offset:6144\n" \
"ds_read_b128 a[220:223], v29 offset:6144\n" \
"s_waitcnt lgkmcnt(15)\n" \
"v_mfma_f32_16x16x32_bf16 a[0:3], a[128:131], a[160:163], a[0:3]\n" \
"s_addk_i32 s84, 0x7c00\n" \
"v_mfma_f32_16x16x32_bf16 a[0:3], a[192:195], a[224:227], a[0:3]\n" \
"buffer_load_dwordx4 v27, s[72:75], s84 offen offset:2048 lds\n" \
"v_mfma_f32_16x16x32_bf16 a[4:7], a[128:131], a[164:167], a[4:7]\n" \
"ds_read_b64_tr_b16 a[176:177], v36 offset:0\n" \
"s_waitcnt lgkmcnt(15)\n" \
"v_mfma_f32_16x16x32_bf16 a[4:7], a[192:195], a[228:231], a[4:7]\n" \
"s_addk_i32 s84, 0x7c00\n" \
"s_waitcnt lgkmcnt(11)\n" \
"v_mfma_f32_16x16x32_bf16 a[8:11], a[128:131], a[168:171], a[8:11]\n" \
"buffer_load_dwordx4 v27, s[72:75], s84 offen offset:3072 lds\n" \
"s_waitcnt lgkmcnt(9)\n" \
"v_mfma_f32_16x16x32_bf16 a[8:11], a[192:195], a[232:235], a[8:11]\n" \
"ds_read_b64_tr_b16 a[178:179], v36 offset:1024\n" \
"s_waitcnt lgkmcnt(8)\n" \
"v_mfma_f32_16x16x32_bf16 a[12:15], a[128:131], a[172:175], a[12:15]\n" \
"ds_read_b64_tr_b16 a[240:241], v36 offset:8192\n" \
"s_waitcnt lgkmcnt(5)\n" \
"v_mfma_f32_16x16x32_bf16 a[12:15], a[192:195], a[236:239], a[12:15]\n" \
"v_mfma_f32_16x16x32_bf16 a[28:31], a[132:135], a[172:175], a[28:31]\n" \
"v_mfma_f32_16x16x32_bf16 a[28:31], a[196:199], a[236:239], a[28:31]\n" \
"ds_read_b64_tr_b16 a[242:243], v36 offset:9216\n" \
"v_mfma_f32_16x16x32_bf16 a[24:27], a[132:135], a[168:171], a[24:27]\n" \
"v_mfma_f32_16x16x32_bf16 a[24:27], a[196:199], a[232:235], a[24:27]\n" \
"v_mfma_f32_16x16x32_bf16 a[20:23], a[132:135], a[164:167], a[20:23]\n" \
"v_mfma_f32_16x16x32_bf16 a[20:23], a[196:199], a[228:231], a[20:23]\n" \
"ds_read_b64_tr_b16 a[180:181], v37 offset:0\n" \
"v_mfma_f32_16x16x32_bf16 a[16:19], a[132:135], a[160:163], a[16:19]\n" \
"v_mfma_f32_16x16x32_bf16 a[16:19], a[196:199], a[224:227], a[16:19]\n" \
"v_mfma_f32_16x16x32_bf16 a[32:35], a[136:139], a[160:163], a[32:35]\n" \
"ds_read_b64_tr_b16 a[182:183], v37 offset:1024\n" \
"v_mfma_f32_16x16x32_bf16 a[32:35], a[200:203], a[224:227], a[32:35]\n" \
"v_mfma_f32_16x16x32_bf16 a[36:39], a[136:139], a[164:167], a[36:39]\n" \
"v_mfma_f32_16x16x32_bf16 a[36:39], a[200:203], a[228:231], a[36:39]\n" \
"ds_read_b64_tr_b16 a[244:245], v37 offset:8192\n" \
"v_mfma_f32_16x16x32_bf16 a[40:43], a[136:139], a[168:171], a[40:43]\n" \
"v_mfma_f32_16x16x32_bf16 a[40:43], a[200:203], a[232:235], a[40:43]\n" \
"v_mfma_f32_16x16x32_bf16 a[44:47], a[136:139], a[172:175], a[44:47]\n" \
"ds_read_b64_tr_b16 a[246:247], v37 offset:9216\n" \
"v_mfma_f32_16x16x32_bf16 a[44:47], a[200:203], a[236:239], a[44:47]\n" \
"v_mfma_f32_16x16x32_bf16 a[60:63], a[140:143], a[172:175], a[60:63]\n" \
"v_mfma_f32_16x16x32_bf16 a[60:63], a[204:207], a[236:239], a[60:63]\n" \
"v_mfma_f32_16x16x32_bf16 a[56:59], a[140:143], a[168:171], a[56:59]\n" \
"ds_read_b64_tr_b16 a[184:185], v38 offset:0\n" \
"v_mfma_f32_16x16x32_bf16 a[56:59], a[204:207], a[232:235], a[56:59]\n" \
"v_mfma_f32_16x16x32_bf16 a[52:55], a[140:143], a[164:167], a[52:55]\n" \
"v_mfma_f32_16x16x32_bf16 a[52:55], a[204:207], a[228:231], a[52:55]\n" \
"ds_read_b64_tr_b16 a[186:187], v38 offset:1024\n" \
"v_mfma_f32_16x16x32_bf16 a[48:51], a[140:143], a[160:163], a[48:51]\n" \
"v_mfma_f32_16x16x32_bf16 a[48:51], a[204:207], a[224:227], a[48:51]\n" \
"v_mfma_f32_16x16x32_bf16 a[64:67], a[144:147], a[160:163], a[64:67]\n" \
"v_bfe_u32 v44, v20, 2, 5\n" \
"ds_read_b64_tr_b16 a[248:249], v38 offset:8192\n" \
"v_mfma_f32_16x16x32_bf16 a[64:67], a[208:211], a[224:227], a[64:67]\n" \
"buffer_load_dword v44, v44, s[80:83], s38 offen offset:128 sc1\n" \
"v_mfma_f32_16x16x32_bf16 a[68:71], a[144:147], a[164:167], a[68:71]\n" \
"v_mfma_f32_16x16x32_bf16 a[68:71], a[208:211], a[228:231], a[68:71]\n" \
"ds_read_b64_tr_b16 a[250:251], v38 offset:9216\n" \
"v_mfma_f32_16x16x32_bf16 a[72:75], a[144:147], a[168:171], a[72:75]\n" \
"v_mfma_f32_16x16x32_bf16 a[72:75], a[208:211], a[232:235], a[72:75]\n" \
"v_mfma_f32_16x16x32_bf16 a[76:79], a[144:147], a[172:175], a[76:79]\n" \
"v_mfma_f32_16x16x32_bf16 a[76:79], a[208:211], a[236:239], a[76:79]\n" \
"ds_read_b64_tr_b16 a[188:189], v39 offset:0\n" \
"v_mfma_f32_16x16x32_bf16 a[92:95], a[148:151], a[172:175], a[92:95]\n" \
"v_mfma_f32_16x16x32_bf16 a[92:95], a[212:215], a[236:239], a[92:95]\n" \
"v_mfma_f32_16x16x32_bf16 a[88:91], a[148:151], a[168:171], a[88:91]\n" \
"ds_read_b64_tr_b16 a[190:191], v39 offset:1024\n" \
"v_mfma_f32_16x16x32_bf16 a[88:91], a[212:215], a[232:235], a[88:91]\n" \
"v_mfma_f32_16x16x32_bf16 a[84:87], a[148:151], a[164:167], a[84:87]\n" \
"v_mfma_f32_16x16x32_bf16 a[84:87], a[212:215], a[228:231], a[84:87]\n" \
"ds_read_b64_tr_b16 a[252:253], v39 offset:8192\n" \
"v_mfma_f32_16x16x32_bf16 a[80:83], a[148:151], a[160:163], a[80:83]\n" \
"v_mfma_f32_16x16x32_bf16 a[80:83], a[212:215], a[224:227], a[80:83]\n" \
"v_mfma_f32_16x16x32_bf16 a[96:99], a[152:155], a[160:163], a[96:99]\n" \
"ds_read_b64_tr_b16 a[254:255], v39 offset:9216\n" \
"v_mfma_f32_16x16x32_bf16 a[96:99], a[216:219], a[224:227], a[96:99]\n" \
"v_mfma_f32_16x16x32_bf16 a[100:103], a[152:155], a[164:167], a[100:103]\n" \
"v_mfma_f32_16x16x32_bf16 a[100:103], a[216:219], a[228:231], a[100:103]\n" \
"v_mfma_f32_16x16x32_bf16 a[104:107], a[152:155], a[168:171], a[104:107]\n" \
"v_mfma_f32_16x16x32_bf16 a[104:107], a[216:219], a[232:235], a[104:107]\n" \
"v_mfma_f32_16x16x32_bf16 a[108:111], a[152:155], a[172:175], a[108:111]\n" \
"v_mfma_f32_16x16x32_bf16 a[108:111], a[216:219], a[236:239], a[108:111]\n" \
"s_waitcnt lgkmcnt(15)\n" \
"v_mfma_f32_16x16x32_bf16 a[124:127], a[156:159], a[172:175], a[124:127]\n" \
"v_mfma_f32_16x16x32_bf16 a[124:127], a[220:223], a[236:239], a[124:127]\n" \
"v_mfma_f32_16x16x32_bf16 a[120:123], a[156:159], a[168:171], a[120:123]\n" \
"v_mfma_f32_16x16x32_bf16 a[120:123], a[220:223], a[232:235], a[120:123]\n" \
"v_mfma_f32_16x16x32_bf16 a[116:119], a[156:159], a[164:167], a[116:119]\n" \
"v_mfma_f32_16x16x32_bf16 a[116:119], a[220:223], a[228:231], a[116:119]\n" \
"v_mfma_f32_16x16x32_bf16 a[112:115], a[156:159], a[160:163], a[112:115]\n" \
"v_mfma_f32_16x16x32_bf16 a[112:115], a[220:223], a[224:227], a[112:115]\n" \
"s_waitcnt vmcnt(0) lgkmcnt(0)\n" \
"s_barrier\n" \
"v_cmp_ne_u32_e32 vcc, s62, v44\n" \
"s_nop 4\n" \
"s_cbranch_vccnz L_poll61_%=\n" \
"L_pollr61_%=:\n" \
"s_mov_b32 s68, s12\n" \
"s_and_b32 s69, s13, 0xffff\n" \
"s_mov_b32 s70, 0x480000\n" \
"s_mov_b32 s71, 0x20000\n" \
"v_mfma_f32_16x16x32_bf16 v[52:55], a[128:131], a[176:179], v[52:55]\n" \
"s_add_u32 m0, s40, 0x0\n" \
"s_add_u32 s85, s39, 0x0\n" \
"s_nop 0\n" \
"v_mfma_f32_16x16x32_bf16 v[52:55], a[192:195], a[240:243], v[52:55]\n" \
"buffer_load_dwordx4 v20, s[80:83], s85 offen sc1 lds\n" \
"v_mfma_f32_16x16x32_bf16 v[56:59], a[128:131], a[180:183], v[56:59]\n" \
"ds_read_b64_tr_b16 a[160:161], v32 offset:32768\n" \
"v_mfma_f32_16x16x32_bf16 v[56:59], a[192:195], a[244:247], v[56:59]\n" \
"s_add_u32 m0, s40, 0x1000\n" \
"s_add_u32 s85, s39, 0x1000\n" \
"v_mfma_f32_16x16x32_bf16 v[60:63], a[128:131], a[184:187], v[60:63]\n" \
"buffer_load_dwordx4 v20, s[80:83], s85 offen sc1 lds\n" \
"v_mfma_f32_16x16x32_bf16 v[60:63], a[192:195], a[248:251], v[60:63]\n" \
"ds_read_b64_tr_b16 a[162:163], v32 offset:33792\n" \
"v_mfma_f32_16x16x32_bf16 v[64:67], a[128:131], a[188:191], v[64:67]\n" \
"ds_read_b64_tr_b16 a[224:225], v32 offset:40960\n" \
"v_mfma_f32_16x16x32_bf16 v[64:67], a[192:195], a[252:255], v[64:67]\n" \
"ds_read_b128 a[128:131], v28 offset:49152\n" \
"v_mfma_f32_16x16x32_bf16 v[80:83], a[132:135], a[188:191], v[80:83]\n" \
"ds_read_b128 a[192:195], v29 offset:49152\n" \
"v_mfma_f32_16x16x32_bf16 v[80:83], a[196:199], a[252:255], v[80:83]\n" \
"s_add_u32 m0, s40, 0x3000\n" \
"s_movk_i32 s84, 0x0\n" \
"v_mfma_f32_16x16x32_bf16 v[76:79], a[132:135], a[184:187], v[76:79]\n" \
"buffer_load_dwordx4 v21, s[76:79], s84 offen offset:0 lds\n" \
"v_mfma_f32_16x16x32_bf16 v[76:79], a[196:199], a[248:251], v[76:79]\n" \
"s_add_u32 m0, s30, 0x0\n" \
"s_movk_i32 s84, 0x0\n" \
"s_nop 0\n" \
"v_mfma_f32_16x16x32_bf16 v[72:75], a[132:135], a[180:183], v[72:75]\n" \
"buffer_load_dwordx4 v24, s[76:79], s84 offen offset:0 lds\n" \
"v_mfma_f32_16x16x32_bf16 v[72:75], a[196:199], a[244:247], v[72:75]\n" \
"ds_read_b64_tr_b16 a[226:227], v32 offset:41984\n" \
"v_mfma_f32_16x16x32_bf16 v[68:71], a[132:135], a[176:179], v[68:71]\n" \
"ds_read_b64_tr_b16 a[164:165], v33 offset:32768\n" \
"v_mfma_f32_16x16x32_bf16 v[68:71], a[196:199], a[240:243], v[68:71]\n" \
"ds_read_b128 a[132:135], v28 offset:51200\n" \
"v_mfma_f32_16x16x32_bf16 v[84:87], a[136:139], a[176:179], v[84:87]\n" \
"ds_read_b128 a[196:199], v29 offset:51200\n" \
"v_mfma_f32_16x16x32_bf16 v[84:87], a[200:203], a[240:243], v[84:87]\n" \
"s_movk_i32 s84, 0x2000\n" \
"v_mfma_f32_16x16x32_bf16 v[88:91], a[136:139], a[180:183], v[88:91]\n" \
"buffer_load_dwordx4 v25, s[76:79], s84 offen offset:1024 lds\n" \
"v_mfma_f32_16x16x32_bf16 v[88:91], a[200:203], a[244:247], v[88:91]\n" \
"s_movk_i32 s84, 0x4000\n" \
"v_mfma_f32_16x16x32_bf16 v[92:95], a[136:139], a[184:187], v[92:95]\n" \
"buffer_load_dwordx4 v24, s[76:79], s84 offen offset:2048 lds\n" \
"v_mfma_f32_16x16x32_bf16 v[92:95], a[200:203], a[248:251], v[92:95]\n" \
"ds_read_b64_tr_b16 a[166:167], v33 offset:33792\n" \
"v_mfma_f32_16x16x32_bf16 v[96:99], a[136:139], a[188:191], v[96:99]\n" \
"ds_read_b64_tr_b16 a[228:229], v33 offset:40960\n" \
"v_mfma_f32_16x16x32_bf16 v[96:99], a[200:203], a[252:255], v[96:99]\n" \
"ds_read_b128 a[136:139], v28 offset:53248\n" \
"v_mfma_f32_16x16x32_bf16 v[112:115], a[140:143], a[188:191], v[112:115]\n" \
"ds_read_b128 a[200:203], v29 offset:53248\n" \
"v_mfma_f32_16x16x32_bf16 v[112:115], a[204:207], a[252:255], v[112:115]\n" \
"s_movk_i32 s84, 0x6000\n" \
"v_mfma_f32_16x16x32_bf16 v[108:111], a[140:143], a[184:187], v[108:111]\n" \
"buffer_load_dwordx4 v25, s[76:79], s84 offen offset:3072 lds\n" \
"v_mfma_f32_16x16x32_bf16 v[108:111], a[204:207], a[248:251], v[108:111]\n" \
"s_add_u32 m0, s31, 0x0\n" \
"s_movk_i32 s84, 0x0\n" \
"v_mfma_f32_16x16x32_bf16 v[104:107], a[140:143], a[180:183], v[104:107]\n" \
"buffer_load_dwordx4 v26, s[68:71], s84 offen offset:0 lds\n" \
"v_mfma_f32_16x16x32_bf16 v[104:107], a[204:207], a[244:247], v[104:107]\n" \
"s_movk_i32 s84, 0x7c00\n" \
".fill (L_pb + 57056 - .) / 4, 4, 0xBF800000\n" \
"L_pp14:\n" \
"s_cbranch_execz L_pret\n" \
"v_mfma_f32_16x16x32_bf16 v[100:103], a[140:143], a[176:179], v[100:103]\n" \
"buffer_load_dwordx4 v26, s[68:71], s84 offen offset:1024 lds\n" \
"v_mfma_f32_16x16x32_bf16 v[100:103], a[204:207], a[240:243], v[100:103]\n" \
"ds_read_b128 a[140:143], v28 offset:55296\n" \
"v_mfma_f32_16x16x32_bf16 v[116:119], a[144:147], a[176:179], v[116:119]\n" \
"ds_read_b128 a[204:207], v29 offset:55296\n" \
"v_mfma_f32_16x16x32_bf16 v[116:119], a[208:211], a[240:243], v[116:119]\n" \
"s_addk_i32 s84, 0x7c00\n" \
"v_mfma_f32_16x16x32_bf16 v[120:123], a[144:147], a[180:183], v[120:123]\n" \
"buffer_load_dwordx4 v27, s[68:71], s84 offen offset:2048 lds\n" \
"v_mfma_f32_16x16x32_bf16 v[120:123], a[208:211], a[244:247], v[120:123]\n" \
"ds_read_b64_tr_b16 a[230:231], v33 offset:41984\n" \
"v_mfma_f32_16x16x32_bf16 v[124:127], a[144:147], a[184:187], v[124:127]\n" \
"s_addk_i32 s84, 0x7c00\n" \
"s_nop 0\n" \
"v_mfma_f32_16x16x32_bf16 v[124:127], a[208:211], a[248:251], v[124:127]\n" \
"buffer_load_dwordx4 v27, s[68:71], s84 offen offset:3072 lds\n" \
"v_mfma_f32_16x16x32_bf16 v[128:131], a[144:147], a[188:191], v[128:131]\n" \
"ds_read_b64_tr_b16 a[168:169], v34 offset:32768\n" \
"v_mfma_f32_16x16x32_bf16 v[128:131], a[208:211], a[252:255], v[128:131]\n" \
"ds_read_b128 a[144:147], v28 offset:32768\n" \
"v_mfma_f32_16x16x32_bf16 v[144:147], a[148:151], a[188:191], v[144:147]\n" \
"ds_read_b128 a[208:211], v29 offset:32768\n" \
"v_mfma_f32_16x16x32_bf16 v[144:147], a[212:215], a[252:255], v[144:147]\n" \
"s_add_u32 m0, s30, 0x1000\n" \
"s_mov_b32 s84, 0x9000\n" \
"v_mfma_f32_16x16x32_bf16 v[140:143], a[148:151], a[184:187], v[140:143]\n" \
"buffer_load_dwordx4 v24, s[76:79], s84 offen offset:0 lds\n" \
"v_mfma_f32_16x16x32_bf16 v[140:143], a[212:215], a[248:251], v[140:143]\n" \
"ds_read_b64_tr_b16 a[170:171], v34 offset:33792\n" \
"v_mfma_f32_16x16x32_bf16 v[136:139], a[148:151], a[180:183], v[136:139]\n" \
"s_addk_i32 s84, 0x2000\n" \
"s_nop 0\n" \
"v_mfma_f32_16x16x32_bf16 v[136:139], a[212:215], a[244:247], v[136:139]\n" \
"buffer_load_dwordx4 v25, s[76:79], s84 offen offset:1024 lds\n" \
"v_mfma_f32_16x16x32_bf16 v[132:135], a[148:151], a[176:179], v[132:135]\n" \
"ds_read_b64_tr_b16 a[232:233], v34 offset:40960\n" \
"v_mfma_f32_16x16x32_bf16 v[132:135], a[212:215], a[240:243], v[132:135]\n" \
"ds_read_b128 a[148:151], v28 offset:34816\n" \
"v_mfma_f32_16x16x32_bf16 v[148:151], a[152:155], a[176:179], v[148:151]\n" \
"ds_read_b128 a[212:215], v29 offset:34816\n" \
"v_mfma_f32_16x16x32_bf16 v[148:151], a[216:219], a[240:243], v[148:151]\n" \
"ds_read_b64_tr_b16 a[234:235], v34 offset:41984\n" \
"v_mfma_f32_16x16x32_bf16 v[152:155], a[152:155], a[180:183], v[152:155]\n" \
"ds_read_b64_tr_b16 a[172:173], v35 offset:32768\n" \
"v_mfma_f32_16x16x32_bf16 v[152:155], a[216:219], a[244:247], v[152:155]\n" \
"ds_read_b64_tr_b16 a[174:175], v35 offset:33792\n" \
"v_mfma_f32_16x16x32_bf16 v[156:159], a[152:155], a[184:187], v[156:159]\n" \
"ds_read_b64_tr_b16 a[236:237], v35 offset:40960\n" \
"v_mfma_f32_16x16x32_bf16 v[156:159], a[216:219], a[248:251], v[156:159]\n" \
"ds_read_b64_tr_b16 a[238:239], v35 offset:41984\n" \
"v_mfma_f32_16x16x32_bf16 v[160:163], a[152:155], a[188:191], v[160:163]\n" \
"v_mfma_f32_16x16x32_bf16 v[160:163], a[216:219], a[252:255], v[160:163]\n" \
"ds_read_b128 a[152:155], v28 offset:36864\n" \
"v_mfma_f32_16x16x32_bf16 v[176:179], a[156:159], a[188:191], v[176:179]\n" \
"ds_read_b128 a[216:219], v29 offset:36864\n" \
"v_mfma_f32_16x16x32_bf16 v[176:179], a[220:223], a[252:255], v[176:179]\n" \
"v_mfma_f32_16x16x32_bf16 v[172:175], a[156:159], a[184:187], v[172:175]\n" \
"v_mfma_f32_16x16x32_bf16 v[172:175], a[220:223], a[248:251], v[172:175]\n" \
"v_mfma_f32_16x16x32_bf16 v[168:171], a[156:159], a[180:183], v[168:171]\n" \
"v_mfma_f32_16x16x32_bf16 v[168:171], a[220:223], a[244:247], v[168:171]\n" \
"v_mfma_f32_16x16x32_bf16 v[164:167], a[156:159], a[176:179], v[164:167]\n" \
"v_mfma_f32_16x16x32_bf16 v[164:167], a[220:223], a[240:243], v[164:167]\n" \
"ds_read_b128 a[156:159], v28 offset:38912\n" \
"ds_read_b128 a[220:223], v29 offset:38912\n" \
"s_waitcnt lgkmcnt(15)\n" \
"v_mfma_f32_16x16x32_bf16 a[0:3], a[128:131], a[160:163], a[0:3]\n" \
"s_addk_i32 s84, 0x2000\n" \
"v_mfma_f32_16x16x32_bf16 a[0:3], a[192:195], a[224:227], a[0:3]\n" \
"buffer_load_dwordx4 v24, s[76:79], s84 offen offset:2048 lds\n" \
"v_mfma_f32_16x16x32_bf16 a[4:7], a[128:131], a[164:167], a[4:7]\n" \
"ds_read_b64_tr_b16 a[176:177], v36 offset:32768\n" \
"v_mfma_f32_16x16x32_bf16 a[4:7], a[192:195], a[228:231], a[4:7]\n" \
"s_addk_i32 s84, 0x2000\n" \
"s_waitcnt lgkmcnt(13)\n" \
"v_mfma_f32_16x16x32_bf16 a[8:11], a[128:131], a[168:171], a[8:11]\n" \
"buffer_load_dwordx4 v25, s[76:79], s84 offen offset:3072 lds\n" \
"s_waitcnt lgkmcnt(9)\n" \
"v_mfma_f32_16x16x32_bf16 a[8:11], a[192:195], a[232:235], a[8:11]\n" \
"ds_read_b64_tr_b16 a[178:179], v36 offset:33792\n" \
"s_waitcnt lgkmcnt(8)\n" \
"v_mfma_f32_16x16x32_bf16 a[12:15], a[128:131], a[172:175], a[12:15]\n" \
"ds_read_b64_tr_b16 a[240:241], v36 offset:40960\n" \
"s_waitcnt lgkmcnt(7)\n" \
"v_mfma_f32_16x16x32_bf16 a[12:15], a[192:195], a[236:239], a[12:15]\n" \
"v_mfma_f32_16x16x32_bf16 a[28:31], a[132:135], a[172:175], a[28:31]\n" \
"v_mfma_f32_16x16x32_bf16 a[28:31], a[196:199], a[236:239], a[28:31]\n" \
"ds_read_b64_tr_b16 a[242:243], v36 offset:41984\n" \
"v_mfma_f32_16x16x32_bf16 a[24:27], a[132:135], a[168:171], a[24:27]\n" \
"v_mfma_f32_16x16x32_bf16 a[24:27], a[196:199], a[232:235], a[24:27]\n" \
"v_mfma_f32_16x16x32_bf16 a[20:23], a[132:135], a[164:167], a[20:23]\n" \
"s_nop 1\n" \
"v_accvgpr_read_b32 v184, a12\n" \
"v_accvgpr_read_b32 v185, a13\n" \
"v_mfma_f32_16x16x32_bf16 a[20:23], a[196:199], a[228:231], a[20:23]\n" \
"ds_read_b64_tr_b16 a[180:181], v37 offset:32768\n" \
"v_accvgpr_read_b32 v186, a14\n" \
"v_mfma_f32_16x16x32_bf16 a[16:19], a[132:135], a[160:163], a[16:19]\n" \
"v_accvgpr_read_b32 v187, a15\n" \
"v_accvgpr_read_b32 v180, a28\n" \
"v_mfma_f32_16x16x32_bf16 a[16:19], a[196:199], a[224:227], a[16:19]\n" \
"v_accvgpr_read_b32 v181, a29\n" \
"v_accvgpr_read_b32 v182, a30\n" \
"v_mfma_f32_16x16x32_bf16 a[32:35], a[136:139], a[160:163], a[32:35]\n" \
"ds_read_b64_tr_b16 a[182:183], v37 offset:33792\n" \
"v_accvgpr_read_b32 v183, a31\n" \
"v_mfma_f32_16x16x32_bf16 a[32:35], a[200:203], a[224:227], a[32:35]\n" \
"v_cvt_pk_bf16_f32 v184, v184, v185\n" \
"v_cvt_pk_bf16_f32 v185, v186, v187\n" \
"v_mfma_f32_16x16x32_bf16 a[36:39], a[136:139], a[164:167], a[36:39]\n" \
"v_lshlrev_b32_e32 v45, 3, v20\n" \
"v_add_u32_e32 v45, s31, v45\n" \
"v_mfma_f32_16x16x32_bf16 a[36:39], a[200:203], a[228:231], a[36:39]\n" \
"s_getpc_b64 s[86:87]\n" \
"s_sub_u32 s86, s86, 0x10800\n" \
"s_subb_u32 s87, s87, 0\n" \
"s_mov_b32 m0, s32\n" \
"s_nop 0\n" \
"v_mfma_f32_16x16x32_bf16 a[40:43], a[136:139], a[168:171], a[40:43]\n" \
"global_load_lds_dword v45, s[86:87]\n" \
"v_mfma_f32_16x16x32_bf16 a[40:43], a[200:203], a[232:235], a[40:43]\n" \
"ds_read_b64_tr_b16 a[244:245], v37 offset:40960\n" \
"v_cvt_pk_bf16_f32 v186, v180, v181\n" \
"v_mfma_f32_16x16x32_bf16 a[44:47], a[136:139], a[172:175], a[44:47]\n" \
"ds_read_b64_tr_b16 a[246:247], v37 offset:41984\n" \
"v_cvt_pk_bf16_f32 v187, v182, v183\n" \
"v_mfma_f32_16x16x32_bf16 a[44:47], a[200:203], a[236:239], a[44:47]\n" \
"v_accvgpr_read_b32 v188, a8\n" \
"v_accvgpr_read_b32 v189, a9\n" \
"v_mfma_f32_16x16x32_bf16 a[60:63], a[140:143], a[172:175], a[60:63]\n" \
"v_accvgpr_read_b32 v190, a10\n" \
"v_accvgpr_read_b32 v191, a11\n" \
"v_mfma_f32_16x16x32_bf16 a[60:63], a[204:207], a[236:239], a[60:63]\n" \
"v_accvgpr_read_b32 v180, a24\n" \
"v_accvgpr_read_b32 v181, a25\n" \
"v_mfma_f32_16x16x32_bf16 a[56:59], a[140:143], a[168:171], a[56:59]\n" \
"ds_read_b64_tr_b16 a[184:185], v38 offset:32768\n" \
"v_accvgpr_read_b32 v182, a26\n" \
"v_mfma_f32_16x16x32_bf16 a[56:59], a[204:207], a[232:235], a[56:59]\n" \
"v_accvgpr_read_b32 v183, a27\n" \
"v_cvt_pk_bf16_f32 v188, v188, v189\n" \
"v_mfma_f32_16x16x32_bf16 a[52:55], a[140:143], a[164:167], a[52:55]\n" \
"v_cvt_pk_bf16_f32 v189, v190, v191\n" \
"v_cvt_pk_bf16_f32 v190, v180, v181\n" \
"v_mfma_f32_16x16x32_bf16 a[52:55], a[204:207], a[228:231], a[52:55]\n" \
"ds_read_b64_tr_b16 a[186:187], v38 offset:33792\n" \
"v_cvt_pk_bf16_f32 v191, v182, v183\n" \
"v_mfma_f32_16x16x32_bf16 a[48:51], a[140:143], a[160:163], a[48:51]\n" \
"v_accvgpr_read_b32 v192, a4\n" \
"v_accvgpr_read_b32 v193, a5\n" \
"v_mfma_f32_16x16x32_bf16 a[48:51], a[204:207], a[224:227], a[48:51]\n" \
"v_accvgpr_read_b32 v194, a6\n" \
"v_accvgpr_read_b32 v195, a7\n" \
"v_mfma_f32_16x16x32_bf16 a[64:67], a[144:147], a[160:163], a[64:67]\n" \
"ds_read_b64_tr_b16 a[248:249], v38 offset:40960\n" \
"v_accvgpr_read_b32 v180, a20\n" \
"v_mfma_f32_16x16x32_bf16 a[64:67], a[208:211], a[224:227], a[64:67]\n" \
"v_accvgpr_read_b32 v181, a21\n" \
"v_accvgpr_read_b32 v182, a22\n" \
"v_mfma_f32_16x16x32_bf16 a[68:71], a[144:147], a[164:167], a[68:71]\n" \
"v_accvgpr_read_b32 v183, a23\n" \
"v_cvt_pk_bf16_f32 v192, v192, v193\n" \
"v_mfma_f32_16x16x32_bf16 a[68:71], a[208:211], a[228:231], a[68:71]\n" \
"ds_read_b64_tr_b16 a[250:251], v38 offset:41984\n" \
"v_cvt_pk_bf16_f32 v193, v194, v195\n" \
"v_mfma_f32_16x16x32_bf16 a[72:75], a[144:147], a[168:171], a[72:75]\n" \
"v_cvt_pk_bf16_f32 v194, v180, v181\n" \
"v_cvt_pk_bf16_f32 v195, v182, v183\n" \
"v_mfma_f32_16x16x32_bf16 a[72:75], a[208:211], a[232:235], a[72:75]\n" \
"v_accvgpr_read_b32 v196, a0\n" \
"v_accvgpr_read_b32 v197, a1\n" \
"v_mfma_f32_16x16x32_bf16 a[76:79], a[144:147], a[172:175], a[76:79]\n" \
"v_accvgpr_read_b32 v198, a2\n" \
"v_accvgpr_read_b32 v199, a3\n" \
"v_mfma_f32_16x16x32_bf16 a[76:79], a[208:211], a[236:239], a[76:79]\n" \
"ds_read_b64_tr_b16 a[188:189], v39 offset:32768\n" \
"v_accvgpr_read_b32 v180, a16\n" \
"v_mfma_f32_16x16x32_bf16 a[92:95], a[148:151], a[172:175], a[92:95]\n" \
"s_add_u32 s89, s41, 0x0\n" \
"s_nop 0\n" \
"buffer_store_dwordx4 v[184:187], v20, s[80:83], s89 offen offset:3072 sc1\n" \
"v_mfma_f32_16x16x32_bf16 a[92:95], a[212:215], a[236:239], a[92:95]\n" \
"v_accvgpr_read_b32 v181, a17\n" \
"v_accvgpr_read_b32 v182, a18\n" \
"v_mfma_f32_16x16x32_bf16 a[88:91], a[148:151], a[168:171], a[88:91]\n" \
"ds_read_b64_tr_b16 a[190:191], v39 offset:33792\n" \
"v_accvgpr_read_b32 v183, a19\n" \
"v_mfma_f32_16x16x32_bf16 a[88:91], a[212:215], a[232:235], a[88:91]\n" \
"v_cvt_pk_bf16_f32 v196, v196, v197\n" \
"v_cvt_pk_bf16_f32 v197, v198, v199\n" \
"v_mfma_f32_16x16x32_bf16 a[84:87], a[148:151], a[164:167], a[84:87]\n" \
"v_cvt_pk_bf16_f32 v198, v180, v181\n" \
"v_cvt_pk_bf16_f32 v199, v182, v183\n" \
"v_mfma_f32_16x16x32_bf16 a[84:87], a[212:215], a[228:231], a[84:87]\n" \
"ds_read_b64_tr_b16 a[252:253], v39 offset:40960\n" \
"v_accvgpr_read_b32 v200, a44\n" \
"v_mfma_f32_16x16x32_bf16 a[80:83], a[148:151], a[160:163], a[80:83]\n" \
"s_add_u32 s88, s41, 0x0\n" \
"buffer_store_dwordx4 v[188:191], v20, s[80:83], s88 offen offset:2048 sc1\n" \
"v_mfma_f32_16x16x32_bf16 a[80:83], a[212:215], a[224:227], a[80:83]\n" \
"v_accvgpr_read_b32 v201, a45\n" \
"v_accvgpr_read_b32 v202, a46\n" \
"s_waitcnt lgkmcnt(15)\n" \
"v_mfma_f32_16x16x32_bf16 a[96:99], a[152:155], a[160:163], a[96:99]\n" \
"ds_read_b64_tr_b16 a[254:255], v39 offset:41984\n" \
"v_accvgpr_read_b32 v203, a47\n" \
"v_mfma_f32_16x16x32_bf16 a[96:99], a[216:219], a[224:227], a[96:99]\n" \
"v_accvgpr_read_b32 v180, a60\n" \
"v_accvgpr_read_b32 v181, a61\n" \
"v_mfma_f32_16x16x32_bf16 a[100:103], a[152:155], a[164:167], a[100:103]\n" \
"v_accvgpr_read_b32 v182, a62\n" \
"v_accvgpr_read_b32 v183, a63\n" \
"v_mfma_f32_16x16x32_bf16 a[100:103], a[216:219], a[228:231], a[100:103]\n" \
"v_cvt_pk_bf16_f32 v200, v200, v201\n" \
"v_cvt_pk_bf16_f32 v201, v202, v203\n" \
"v_mfma_f32_16x16x32_bf16 a[104:107], a[152:155], a[168:171], a[104:107]\n" \
"s_add_u32 s89, s41, 0x0\n" \
"s_nop 0\n" \
"buffer_store_dwordx4 v[192:195], v20, s[80:83], s89 offen offset:1024 sc1\n" \
"v_mfma_f32_16x16x32_bf16 a[104:107], a[216:219], a[232:235], a[104:107]\n" \
"v_cvt_pk_bf16_f32 v202, v180, v181\n" \
"v_cvt_pk_bf16_f32 v203, v182, v183\n" \
"v_mfma_f32_16x16x32_bf16 a[108:111], a[152:155], a[172:175], a[108:111]\n" \
"v_accvgpr_read_b32 v204, a40\n" \
"v_accvgpr_read_b32 v205, a41\n" \
"v_mfma_f32_16x16x32_bf16 a[108:111], a[216:219], a[236:239], a[108:111]\n" \
"v_accvgpr_read_b32 v206, a42\n" \
"v_accvgpr_read_b32 v207, a43\n" \
"v_mfma_f32_16x16x32_bf16 a[124:127], a[156:159], a[172:175], a[124:127]\n" \
"v_accvgpr_read_b32 v180, a56\n" \
"v_accvgpr_read_b32 v181, a57\n" \
"v_mfma_f32_16x16x32_bf16 a[124:127], a[220:223], a[236:239], a[124:127]\n" \
"v_accvgpr_read_b32 v182, a58\n" \
"v_accvgpr_read_b32 v183, a59\n" \
"v_mfma_f32_16x16x32_bf16 a[120:123], a[156:159], a[168:171], a[120:123]\n" \
"s_add_u32 s88, s41, 0x0\n" \
"s_nop 0\n" \
"buffer_store_dwordx4 v[196:199], v20, s[80:83], s88 offen offset:0 sc1\n" \
"v_mfma_f32_16x16x32_bf16 a[120:123], a[220:223], a[232:235], a[120:123]\n" \
"v_cvt_pk_bf16_f32 v204, v204, v205\n" \
"v_cvt_pk_bf16_f32 v205, v206, v207\n" \
"v_mfma_f32_16x16x32_bf16 a[116:119], a[156:159], a[164:167], a[116:119]\n" \
"v_cvt_pk_bf16_f32 v206, v180, v181\n" \
"v_cvt_pk_bf16_f32 v207, v182, v183\n" \
"v_mfma_f32_16x16x32_bf16 a[116:119], a[220:223], a[228:231], a[116:119]\n" \
"v_accvgpr_read_b32 v208, a36\n" \
"v_accvgpr_read_b32 v209, a37\n" \
"v_mfma_f32_16x16x32_bf16 a[112:115], a[156:159], a[160:163], a[112:115]\n" \
"v_accvgpr_read_b32 v210, a38\n" \
"v_accvgpr_read_b32 v211, a39\n" \
"v_mfma_f32_16x16x32_bf16 a[112:115], a[220:223], a[224:227], a[112:115]\n" \
"v_accvgpr_read_b32 v180, a52\n" \
"v_accvgpr_read_b32 v181, a53\n" \
"s_waitcnt vmcnt(5) lgkmcnt(0)\n" \
"s_barrier\n" \
"v_xor_b32_e32 v36, 0x4000, v36\n" \
"v_xor_b32_e64 v32, 0x0, v48\n" \
"v_xor_b32_e32 v37, 0x4000, v37\n" \
"v_xor_b32_e64 v33, 0x20, v48\n" \
"v_xor_b32_e32 v38, 0x4000, v38\n" \
"v_xor_b32_e64 v34, 0x40, v48\n" \
"v_xor_b32_e32 v39, 0x4000, v39\n" \
"v_xor_b32_e32 v35, 0x60, v48\n" \
"v_add_u32_e32 v28, s43, v28\n" \
"v_add_u32_e32 v29, s43, v29\n" \
"v_mfma_f32_16x16x32_bf16 v[116:119], a[144:147], a[176:179], v[116:119]\n" \
"s_add_u32 m0, s40, 0x8000\n" \
"s_add_u32 s85, s39, 0x2000\n" \
"v_mfma_f32_16x16x32_bf16 v[116:119], a[208:211], a[240:243], v[116:119]\n" \
"buffer_load_dwordx4 v20, s[80:83], s85 offen sc1 lds\n" \
"v_mfma_f32_16x16x32_bf16 v[120:123], a[144:147], a[180:183], v[120:123]\n" \
"ds_read_b64_tr_b16 a[160:161], v32 offset:0\n" \
"v_accvgpr_read_b32 v182, a54\n" \
"v_mfma_f32_16x16x32_bf16 v[120:123], a[208:211], a[244:247], v[120:123]\n" \
"s_add_u32 m0, s40, 0x9000\n" \
"s_add_u32 s85, s39, 0x3000\n" \
"v_mfma_f32_16x16x32_bf16 v[124:127], a[144:147], a[184:187], v[124:127]\n" \
"buffer_load_dwordx4 v20, s[80:83], s85 offen sc1 lds\n" \
"v_mfma_f32_16x16x32_bf16 v[124:127], a[208:211], a[248:251], v[124:127]\n" \
"ds_read_b64_tr_b16 a[162:163], v32 offset:512\n" \
"v_accvgpr_read_b32 v183, a55\n" \
"v_mfma_f32_16x16x32_bf16 v[128:131], a[144:147], a[188:191], v[128:131]\n" \
"ds_read_b64_tr_b16 a[224:225], v32 offset:4096\n" \
"v_cvt_pk_bf16_f32 v208, v208, v209\n" \
"v_mfma_f32_16x16x32_bf16 v[128:131], a[208:211], a[252:255], v[128:131]\n" \
"ds_read_b128 a[144:147], v28 offset:8192\n" \
"v_cvt_pk_bf16_f32 v209, v210, v211\n" \
"v_mfma_f32_16x16x32_bf16 v[144:147], a[148:151], a[188:191], v[144:147]\n" \
"ds_read_b128 a[208:211], v29 offset:8192\n" \
"v_cvt_pk_bf16_f32 v210, v180, v181\n" \
"v_mfma_f32_16x16x32_bf16 v[144:147], a[212:215], a[252:255], v[144:147]\n" \
"s_add_u32 m0, s40, 0xb000\n" \
"s_movk_i32 s84, 0x80\n" \
"v_mfma_f32_16x16x32_bf16 v[140:143], a[148:151], a[184:187], v[140:143]\n" \
"buffer_load_dwordx4 v21, s[76:79], s84 offen offset:0 lds\n" \
"v_mfma_f32_16x16x32_bf16 v[140:143], a[212:215], a[248:251], v[140:143]\n" \
"s_add_u32 m0, s30, 0x8000\n" \
"s_movk_i32 s84, 0x80\n" \
"v_mfma_f32_16x16x32_bf16 v[136:139], a[148:151], a[180:183], v[136:139]\n" \
"buffer_load_dwordx4 v24, s[76:79], s84 offen offset:0 lds\n" \
"v_mfma_f32_16x16x32_bf16 v[136:139], a[212:215], a[244:247], v[136:139]\n" \
"ds_read_b64_tr_b16 a[226:227], v32 offset:4608\n" \
"v_cvt_pk_bf16_f32 v211, v182, v183\n" \
"v_mfma_f32_16x16x32_bf16 v[132:135], a[148:151], a[176:179], v[132:135]\n" \
"ds_read_b64_tr_b16 a[164:165], v33 offset:0\n" \
"v_accvgpr_read_b32 v212, a32\n" \
"v_mfma_f32_16x16x32_bf16 v[132:135], a[212:215], a[240:243], v[132:135]\n" \
"ds_read_b128 a[148:151], v28 offset:10240\n" \
"v_accvgpr_read_b32 v213, a33\n" \
"v_mfma_f32_16x16x32_bf16 v[148:151], a[152:155], a[176:179], v[148:151]\n" \
"ds_read_b128 a[212:215], v29 offset:10240\n" \
"v_accvgpr_read_b32 v214, a34\n" \
"v_mfma_f32_16x16x32_bf16 v[148:151], a[216:219], a[240:243], v[148:151]\n" \
"s_movk_i32 s84, 0x2080\n" \
"v_mfma_f32_16x16x32_bf16 v[152:155], a[152:155], a[180:183], v[152:155]\n" \
"buffer_load_dwordx4 v25, s[76:79], s84 offen offset:1024 lds\n" \
"v_mfma_f32_16x16x32_bf16 v[152:155], a[216:219], a[244:247], v[152:155]\n" \
"s_movk_i32 s84, 0x4080\n" \
"v_mfma_f32_16x16x32_bf16 v[156:159], a[152:155], a[184:187], v[156:159]\n" \
"buffer_load_dwordx4 v24, s[76:79], s84 offen offset:2048 lds\n" \
"v_mfma_f32_16x16x32_bf16 v[156:159], a[216:219], a[248:251], v[156:159]\n" \
"ds_read_b64_tr_b16 a[166:167], v33 offset:512\n" \
"v_accvgpr_read_b32 v215, a35\n" \
"v_mfma_f32_16x16x32_bf16 v[160:163], a[152:155], a[188:191], v[160:163]\n" \
"ds_read_b64_tr_b16 a[228:229], v33 offset:4096\n" \
"v_accvgpr_read_b32 v180, a48\n" \
"v_mfma_f32_16x16x32_bf16 v[160:163], a[216:219], a[252:255], v[160:163]\n" \
"ds_read_b128 a[152:155], v28 offset:12288\n" \
"v_accvgpr_read_b32 v181, a49\n" \
"v_mfma_f32_16x16x32_bf16 v[176:179], a[156:159], a[188:191], v[176:179]\n" \
"ds_read_b128 a[216:219], v29 offset:12288\n" \
"v_accvgpr_read_b32 v182, a50\n" \
"v_mfma_f32_16x16x32_bf16 v[176:179], a[220:223], a[252:255], v[176:179]\n" \
"s_movk_i32 s84, 0x6080\n" \
"v_mfma_f32_16x16x32_bf16 v[172:175], a[156:159], a[184:187], v[172:175]\n" \
"buffer_load_dwordx4 v25, s[76:79], s84 offen offset:3072 lds\n" \
"v_mfma_f32_16x16x32_bf16 v[172:175], a[220:223], a[248:251], v[172:175]\n" \
"s_add_u32 m0, s31, 0x8000\n" \
"s_mov_b32 s84, 0x80000\n" \
"v_mfma_f32_16x16x32_bf16 v[168:171], a[156:159], a[180:183], v[168:171]\n" \
"buffer_load_dwordx4 v26, s[68:71], s84 offen offset:0 lds\n" \
"v_mfma_f32_16x16x32_bf16 v[168:171], a[220:223], a[244:247], v[168:171]\n" \
"s_addk_i32 s84, 0x7c00\n" \
"v_mfma_f32_16x16x32_bf16 v[164:167], a[156:159], a[176:179], v[164:167]\n" \
"buffer_load_dwordx4 v26, s[68:71], s84 offen offset:1024 lds\n" \
"v_mfma_f32_16x16x32_bf16 v[164:167], a[220:223], a[240:243], v[164:167]\n" \
"ds_read_b128 a[156:159], v28 offset:14336\n" \
"v_accvgpr_read_b32 v183, a51\n" \
"v_mfma_f32_16x16x32_bf16 v[84:87], a[136:139], a[176:179], v[84:87]\n" \
"ds_read_b128 a[220:223], v29 offset:14336\n" \
"v_cvt_pk_bf16_f32 v212, v212, v213\n" \
"v_mfma_f32_16x16x32_bf16 v[84:87], a[200:203], a[240:243], v[84:87]\n" \
"s_addk_i32 s84, 0x7c00\n" \
"v_mfma_f32_16x16x32_bf16 v[88:91], a[136:139], a[180:183], v[88:91]\n" \
"buffer_load_dwordx4 v27, s[68:71], s84 offen offset:2048 lds\n" \
"v_mfma_f32_16x16x32_bf16 v[88:91], a[200:203], a[244:247], v[88:91]\n" \
"ds_read_b64_tr_b16 a[230:231], v33 offset:4608\n" \
"v_cvt_pk_bf16_f32 v213, v214, v215\n" \
"v_mfma_f32_16x16x32_bf16 v[92:95], a[136:139], a[184:187], v[92:95]\n" \
"s_addk_i32 s84, 0x7c00\n" \
"v_mfma_f32_16x16x32_bf16 v[92:95], a[200:203], a[248:251], v[92:95]\n" \
"buffer_load_dwordx4 v27, s[68:71], s84 offen offset:3072 lds\n" \
"v_mfma_f32_16x16x32_bf16 v[96:99], a[136:139], a[188:191], v[96:99]\n" \
"ds_read_b64_tr_b16 a[172:173], v35 offset:0\n" \
"v_cvt_pk_bf16_f32 v214, v180, v181\n" \
"v_mfma_f32_16x16x32_bf16 v[96:99], a[200:203], a[252:255], v[96:99]\n" \
"ds_read_b128 a[136:139], v28 offset:4096\n" \
"v_cvt_pk_bf16_f32 v215, v182, v183\n" \
"v_mfma_f32_16x16x32_bf16 v[112:115], a[140:143], a[188:191], v[112:115]\n" \
"ds_read_b128 a[200:203], v29 offset:4096\n" \
"v_accvgpr_read_b32 v216, a68\n" \
"v_mfma_f32_16x16x32_bf16 v[112:115], a[204:207], a[252:255], v[112:115]\n" \
"s_add_u32 m0, s30, 0x9000\n" \
"s_mov_b32 s84, 0x9080\n" \
"v_mfma_f32_16x16x32_bf16 v[108:111], a[140:143], a[184:187], v[108:111]\n" \
"buffer_load_dwordx4 v24, s[76:79], s84 offen offset:0 lds\n" \
"v_mfma_f32_16x16x32_bf16 v[108:111], a[204:207], a[248:251], v[108:111]\n" \
"ds_read_b64_tr_b16 a[174:175], v35 offset:512\n" \
"v_accvgpr_read_b32 v217, a69\n" \
"v_mfma_f32_16x16x32_bf16 v[104:107], a[140:143], a[180:183], v[104:107]\n" \
"s_addk_i32 s84, 0x2000\n" \
"s_nop 0\n" \
"v_mfma_f32_16x16x32_bf16 v[104:107], a[204:207], a[244:247], v[104:107]\n" \
"buffer_load_dwordx4 v25, s[76:79], s84 offen offset:1024 lds\n" \
"v_mfma_f32_16x16x32_bf16 v[100:103], a[140:143], a[176:179], v[100:103]\n" \
"ds_read_b64_tr_b16 a[236:237], v35 offset:4096\n" \
"v_accvgpr_read_b32 v218, a70\n" \
"v_mfma_f32_16x16x32_bf16 v[100:103], a[204:207], a[240:243], v[100:103]\n" \
"ds_read_b128 a[140:143], v28 offset:6144\n" \
"v_accvgpr_read_b32 v219, a71\n" \
"v_mfma_f32_16x16x32_bf16 v[52:55], a[128:131], a[176:179], v[52:55]\n" \
"ds_read_b128 a[204:207], v29 offset:6144\n" \
"v_accvgpr_read_b32 v180, a84\n" \
"v_mfma_f32_16x16x32_bf16 v[52:55], a[192:195], a[240:243], v[52:55]\n" \
"ds_read_b64_tr_b16 a[238:239], v35 offset:4608\n" \
"v_accvgpr_read_b32 v181, a85\n" \
"v_mfma_f32_16x16x32_bf16 v[56:59], a[128:131], a[180:183], v[56:59]\n" \
"ds_read_b64_tr_b16 a[168:169], v34 offset:0\n" \
"v_accvgpr_read_b32 v182, a86\n" \
"v_mfma_f32_16x16x32_bf16 v[56:59], a[192:195], a[244:247], v[56:59]\n" \
"ds_read_b64_tr_b16 a[170:171], v34 offset:512\n" \
"v_accvgpr_read_b32 v183, a87\n" \
"v_mfma_f32_16x16x32_bf16 v[60:63], a[128:131], a[184:187], v[60:63]\n" \
"ds_read_b64_tr_b16 a[232:233], v34 offset:4096\n" \
"v_cvt_pk_bf16_f32 v216, v216, v217\n" \
"v_mfma_f32_16x16x32_bf16 v[60:63], a[192:195], a[248:251], v[60:63]\n" \
"ds_read_b64_tr_b16 a[234:235], v34 offset:4608\n" \
"v_cvt_pk_bf16_f32 v217, v218, v219\n" \
"v_mfma_f32_16x16x32_bf16 v[64:67], a[128:131], a[188:191], v[64:67]\n" \
"v_cvt_pk_bf16_f32 v218, v180, v181\n" \
"v_cvt_pk_bf16_f32 v219, v182, v183\n" \
"v_mfma_f32_16x16x32_bf16 v[64:67], a[192:195], a[252:255], v[64:67]\n" \
"ds_read_b128 a[128:131], v28 offset:0\n" \
"v_accvgpr_read_b32 v220, a64\n" \
"v_mfma_f32_16x16x32_bf16 v[80:83], a[132:135], a[188:191], v[80:83]\n" \
"ds_read_b128 a[192:195], v29 offset:0\n" \
"v_accvgpr_read_b32 v221, a65\n" \
"v_mfma_f32_16x16x32_bf16 v[80:83], a[196:199], a[252:255], v[80:83]\n" \
"v_accvgpr_read_b32 v222, a66\n" \
"v_accvgpr_read_b32 v223, a67\n" \
"v_mfma_f32_16x16x32_bf16 v[76:79], a[132:135], a[184:187], v[76:79]\n" \
"v_accvgpr_read_b32 v180, a80\n" \
"v_accvgpr_read_b32 v181, a81\n" \
"v_mfma_f32_16x16x32_bf16 v[76:79], a[196:199], a[248:251], v[76:79]\n" \
"v_accvgpr_read_b32 v182, a82\n" \
"v_accvgpr_read_b32 v183, a83\n" \
"v_mfma_f32_16x16x32_bf16 v[72:75], a[132:135], a[180:183], v[72:75]\n" \
"v_cvt_pk_bf16_f32 v220, v220, v221\n" \
"v_cvt_pk_bf16_f32 v221, v222, v223\n" \
"v_mfma_f32_16x16x32_bf16 v[72:75], a[196:199], a[244:247], v[72:75]\n" \
"v_cvt_pk_bf16_f32 v222, v180, v181\n" \
"v_cvt_pk_bf16_f32 v223, v182, v183\n" \
"v_mfma_f32_16x16x32_bf16 v[68:71], a[132:135], a[176:179], v[68:71]\n" \
"v_cvt_pk_bf16_f32 v224, v128, v129\n" \
"v_cvt_pk_bf16_f32 v225, v130, v131\n" \
"v_mfma_f32_16x16x32_bf16 v[68:71], a[196:199], a[240:243], v[68:71]\n" \
"ds_read_b128 a[132:135], v28 offset:2048\n" \
"ds_read_b128 a[196:199], v29 offset:2048\n" \
"s_waitcnt lgkmcnt(15)\n" \
"v_mfma_f32_16x16x32_bf16 a[0:3], a[160:163], a[144:147], 0\n" \
"s_addk_i32 s84, 0x2000\n" \
"v_cvt_pk_bf16_f32 v226, v144, v145\n" \
"v_mfma_f32_16x16x32_bf16 a[0:3], a[224:227], a[208:211], a[0:3]\n" \
"buffer_load_dwordx4 v24, s[76:79], s84 offen offset:2048 lds\n" \
"v_cvt_pk_bf16_f32 v227, v146, v147\n" \
"v_mfma_f32_16x16x32_bf16 a[4:7], a[164:167], a[144:147], 0\n" \
"ds_read_b64_tr_b16 a[176:177], v36 offset:0\n" \
"v_cvt_pk_bf16_f32 v228, v124, v125\n" \
"v_mfma_f32_16x16x32_bf16 a[4:7], a[228:231], a[208:211], a[4:7]\n" \
"s_addk_i32 s84, 0x2000\n" \
"v_cvt_pk_bf16_f32 v229, v126, v127\n" \
"s_waitcnt lgkmcnt(13)\n" \
"v_mfma_f32_16x16x32_bf16 a[12:15], a[172:175], a[144:147], 0\n" \
"buffer_load_dwordx4 v25, s[76:79], s84 offen offset:3072 lds\n" \
"v_cvt_pk_bf16_f32 v230, v140, v141\n" \
"s_waitcnt lgkmcnt(9)\n" \
"v_mfma_f32_16x16x32_bf16 a[12:15], a[236:239], a[208:211], a[12:15]\n" \
"ds_read_b64_tr_b16 a[178:179], v36 offset:1024\n" \
"v_cvt_pk_bf16_f32 v231, v142, v143\n" \
"s_waitcnt lgkmcnt(8)\n" \
"v_mfma_f32_16x16x32_bf16 a[8:11], a[144:147], a[168:171], 0\n" \
"ds_read_b64_tr_b16 a[240:241], v36 offset:8192\n" \
"v_cvt_pk_bf16_f32 v232, v120, v121\n" \
"s_waitcnt lgkmcnt(7)\n" \
"s_nop 0\n" \
"v_mfma_f32_16x16x32_bf16 a[8:11], a[208:211], a[232:235], a[8:11]\n" \
"ds_read_b64_tr_b16 a[242:243], v36 offset:9216\n" \
"v_cvt_pk_bf16_f32 v233, v122, v123\n" \
"v_mfma_f32_16x16x32_bf16 a[24:27], a[148:151], a[168:171], 0\n" \
"ds_read_b64_tr_b16 a[180:181], v37 offset:0\n" \
"v_cvt_pk_bf16_f32 v234, v136, v137\n" \
"v_mfma_f32_16x16x32_bf16 a[24:27], a[212:215], a[232:235], a[24:27]\n" \
"ds_read_b64_tr_b16 a[182:183], v37 offset:1024\n" \
"v_cvt_pk_bf16_f32 v235, v138, v139\n" \
"v_mfma_f32_16x16x32_bf16 a[28:31], a[172:175], a[148:151], 0\n" \
"ds_read_b64_tr_b16 a[244:245], v37 offset:8192\n" \
"v_cvt_pk_bf16_f32 v236, v116, v117\n" \
"v_mfma_f32_16x16x32_bf16 a[28:31], a[236:239], a[212:215], a[28:31]\n" \
"s_add_u32 s89, s41, 0x2000\n" \
"buffer_store_dwordx4 v[200:203], v20, s[80:83], s89 offen offset:3072 sc1\n" \
"v_mfma_f32_16x16x32_bf16 a[20:23], a[164:167], a[148:151], 0\n" \
"ds_read_b64_tr_b16 a[246:247], v37 offset:9216\n" \
"v_cvt_pk_bf16_f32 v237, v118, v119\n" \
"v_mfma_f32_16x16x32_bf16 a[20:23], a[228:231], a[212:215], a[20:23]\n" \
"ds_read_b64_tr_b16 a[184:185], v38 offset:0\n" \
"v_cvt_pk_bf16_f32 v238, v132, v133\n" \
"v_mfma_f32_16x16x32_bf16 a[16:19], a[160:163], a[148:151], 0\n" \
"ds_read_b64_tr_b16 a[186:187], v38 offset:1024\n" \
"v_cvt_pk_bf16_f32 v239, v134, v135\n" \
"v_mfma_f32_16x16x32_bf16 a[16:19], a[224:227], a[212:215], a[16:19]\n" \
"v_cvt_pk_bf16_f32 v240, v160, v161\n" \
"v_cvt_pk_bf16_f32 v241, v162, v163\n" \
"v_cvt_pk_bf16_f32 v242, v176, v177\n" \
"v_mfma_f32_16x16x32_bf16 a[32:35], a[160:163], a[152:155], 0\n" \
"s_add_u32 s88, s41, 0x2000\n" \
"buffer_store_dwordx4 v[204:207], v20, s[80:83], s88 offen offset:2048 sc1\n" \
"v_mfma_f32_16x16x32_bf16 a[32:35], a[224:227], a[216:219], a[32:35]\n" \
"ds_read_b64_tr_b16 a[248:249], v38 offset:8192\n" \
"v_cvt_pk_bf16_f32 v243, v178, v179\n" \
"v_mfma_f32_16x16x32_bf16 a[36:39], a[164:167], a[152:155], 0\n" \
"ds_read_b64_tr_b16 a[250:251], v38 offset:9216\n" \
"v_cvt_pk_bf16_f32 v244, v156, v157\n" \
"v_mfma_f32_16x16x32_bf16 a[36:39], a[228:231], a[216:219], a[36:39]\n" \
"ds_read_b64_tr_b16 a[188:189], v39 offset:0\n" \
"v_cvt_pk_bf16_f32 v245, v158, v159\n" \
"v_mfma_f32_16x16x32_bf16 a[44:47], a[172:175], a[152:155], 0\n" \
"v_cvt_pk_bf16_f32 v246, v172, v173\n" \
"v_cvt_pk_bf16_f32 v247, v174, v175\n" \
"v_cvt_pk_bf16_f32 v184, v152, v153\n" \
"v_mfma_f32_16x16x32_bf16 a[44:47], a[236:239], a[216:219], a[44:47]\n" \
"ds_read_b64_tr_b16 a[190:191], v39 offset:1024\n" \
"v_cvt_pk_bf16_f32 v185, v154, v155\n" \
"v_mfma_f32_16x16x32_bf16 a[40:43], a[152:155], a[168:171], 0\n" \
"s_add_u32 s89, s41, 0x2000\n" \
"buffer_store_dwordx4 v[208:211], v20, s[80:83], s89 offen offset:1024 sc1\n" \
"v_mfma_f32_16x16x32_bf16 a[40:43], a[216:219], a[232:235], a[40:43]\n" \
"ds_read_b64_tr_b16 a[252:253], v39 offset:8192\n" \
"v_cvt_pk_bf16_f32 v186, v168, v169\n" \
"v_mfma_f32_16x16x32_bf16 a[56:59], a[156:159], a[168:171], 0\n" \
"ds_read_b64_tr_b16 a[254:255], v39 offset:9216\n" \
"v_cvt_pk_bf16_f32 v187, v170, v171\n" \
"v_mfma_f32_16x16x32_bf16 a[56:59], a[220:223], a[232:235], a[56:59]\n" \
"v_cvt_pk_bf16_f32 v188, v148, v149\n" \
"v_cvt_pk_bf16_f32 v189, v150, v151\n" \
"v_cvt_pk_bf16_f32 v190, v164, v165\n" \
"v_mfma_f32_16x16x32_bf16 a[60:63], a[172:175], a[156:159], 0\n" \
"v_cvt_pk_bf16_f32 v191, v166, v167\n" \
"v_cvt_pk_bf16_f32 v192, v96, v97\n" \
"v_cvt_pk_bf16_f32 v193, v98, v99\n" \
"v_mfma_f32_16x16x32_bf16 a[60:63], a[236:239], a[220:223], a[60:63]\n" \
"s_add_u32 s88, s41, 0x2000\n" \
"buffer_store_dwordx4 v[212:215], v20, s[80:83], s88 offen offset:0 sc1\n" \
"v_mfma_f32_16x16x32_bf16 a[52:55], a[164:167], a[156:159], 0\n" \
"v_cvt_pk_bf16_f32 v194, v112, v113\n" \
"v_cvt_pk_bf16_f32 v195, v114, v115\n" \
"v_cvt_pk_bf16_f32 v196, v92, v93\n" \
".fill (L_pb + 61152 - .) / 4, 4, 0xBF800000\n" \
"L_pp15:\n" \
"s_cbranch_execz L_pret\n" \
"s_nop 0\n" \
"v_mfma_f32_16x16x32_bf16 a[52:55], a[228:231], a[220:223], a[52:55]\n" \
"v_cvt_pk_bf16_f32 v197, v94, v95\n" \
"v_cvt_pk_bf16_f32 v198, v108, v109\n" \
"v_cvt_pk_bf16_f32 v199, v110, v111\n" \
"v_mfma_f32_16x16x32_bf16 a[48:51], a[160:163], a[156:159], 0\n" \
"v_cvt_pk_bf16_f32 v252, v88, v89\n" \
"v_cvt_pk_bf16_f32 v253, v90, v91\n" \
"v_cvt_pk_bf16_f32 v254, v104, v105\n" \
"v_mfma_f32_16x16x32_bf16 a[48:51], a[224:227], a[220:223], a[48:51]\n" \
"v_cvt_pk_bf16_f32 v255, v106, v107\n" \
"v_accvgpr_write_b32 a84, v252\n" \
"v_accvgpr_write_b32 a85, v253\n" \
"v_mfma_f32_16x16x32_bf16 a[64:67], a[164:167], a[140:143], 0\n" \
"v_accvgpr_write_b32 a86, v254\n" \
"v_accvgpr_write_b32 a87, v255\n" \
"v_cvt_pk_bf16_f32 v252, v84, v85\n" \
"v_mfma_f32_16x16x32_bf16 a[64:67], a[228:231], a[204:207], a[64:67]\n" \
"v_cvt_pk_bf16_f32 v253, v86, v87\n" \
"v_cvt_pk_bf16_f32 v254, v100, v101\n" \
"v_cvt_pk_bf16_f32 v255, v102, v103\n" \
"v_mfma_f32_16x16x32_bf16 a[80:83], a[172:175], a[140:143], 0\n" \
"v_mfma_f32_16x16x32_bf16 a[80:83], a[236:239], a[204:207], a[80:83]\n" \
"v_mfma_f32_16x16x32_bf16 a[68:71], a[140:143], a[168:171], 0\n" \
"v_mfma_f32_16x16x32_bf16 a[68:71], a[204:207], a[232:235], a[68:71]\n" \
"s_waitcnt vmcnt(4) lgkmcnt(0)\n" \
"s_barrier\n" \
"v_mfma_f32_16x16x32_bf16 v[116:119], a[176:179], a[144:147], 0\n" \
"s_add_u32 m0, s40, 0x0\n" \
"s_nop 0\n" \
"s_add_u32 s85, s39, 0x4000\n" \
"v_cvt_pk_bf16_f32 v200, v64, v65\n" \
"v_mfma_f32_16x16x32_bf16 v[116:119], a[240:243], a[208:211], v[116:119]\n" \
"buffer_load_dwordx4 v20, s[80:83], s85 offen sc1 lds\n" \
"v_cvt_pk_bf16_f32 v201, v66, v67\n" \
"v_mfma_f32_16x16x32_bf16 v[120:123], a[180:183], a[144:147], 0\n" \
"ds_read_b64_tr_b16 a[160:161], v32 offset:32768\n" \
"v_cvt_pk_bf16_f32 v202, v80, v81\n" \
"v_mfma_f32_16x16x32_bf16 v[120:123], a[244:247], a[208:211], v[120:123]\n" \
"ds_read_b64_tr_b16 a[162:163], v32 offset:33280\n" \
"v_cvt_pk_bf16_f32 v203, v82, v83\n" \
"v_mfma_f32_16x16x32_bf16 v[124:127], a[184:187], a[144:147], 0\n" \
"s_add_u32 m0, s40, 0x1000\n" \
"s_add_u32 s85, s39, 0x5000\n" \
"v_cvt_pk_bf16_f32 v204, v60, v61\n" \
"v_mfma_f32_16x16x32_bf16 v[124:127], a[248:251], a[208:211], v[124:127]\n" \
"buffer_load_dwordx4 v20, s[80:83], s85 offen sc1 lds\n" \
"v_cvt_pk_bf16_f32 v205, v62, v63\n" \
"v_mfma_f32_16x16x32_bf16 v[128:131], a[188:191], a[144:147], 0\n" \
"ds_read_b64_tr_b16 a[224:225], v32 offset:36864\n" \
"v_cvt_pk_bf16_f32 v206, v76, v77\n" \
"v_mfma_f32_16x16x32_bf16 v[128:131], a[252:255], a[208:211], v[128:131]\n" \
"ds_read_b128 a[144:147], v28 offset:40960\n" \
"v_cvt_pk_bf16_f32 v207, v78, v79\n" \
"v_mfma_f32_16x16x32_bf16 v[144:147], a[188:191], a[148:151], 0\n" \
"ds_read_b128 a[208:211], v29 offset:40960\n" \
"v_cvt_pk_bf16_f32 v208, v56, v57\n" \
"v_mfma_f32_16x16x32_bf16 v[144:147], a[252:255], a[212:215], v[144:147]\n" \
"s_add_u32 m0, s40, 0x3000\n" \
"s_movk_i32 s84, 0x100\n" \
"v_cvt_pk_bf16_f32 v209, v58, v59\n" \
"v_mfma_f32_16x16x32_bf16 v[140:143], a[184:187], a[148:151], 0\n" \
"buffer_load_dwordx4 v21, s[76:79], s84 offen offset:0 lds\n" \
"v_cvt_pk_bf16_f32 v210, v72, v73\n" \
"v_mfma_f32_16x16x32_bf16 v[140:143], a[248:251], a[212:215], v[140:143]\n" \
"ds_read_b64_tr_b16 a[226:227], v32 offset:37376\n" \
"v_cvt_pk_bf16_f32 v211, v74, v75\n" \
"v_mfma_f32_16x16x32_bf16 v[136:139], a[180:183], a[148:151], 0\n" \
"s_add_u32 m0, s30, 0x0\n" \
"s_movk_i32 s84, 0x100\n" \
"s_nop 0\n" \
"v_cvt_pk_bf16_f32 v212, v52, v53\n" \
"v_mfma_f32_16x16x32_bf16 v[136:139], a[244:247], a[212:215], v[136:139]\n" \
"buffer_load_dwordx4 v24, s[76:79], s84 offen offset:0 lds\n" \
"v_cvt_pk_bf16_f32 v213, v54, v55\n" \
"v_mfma_f32_16x16x32_bf16 v[132:135], a[176:179], a[148:151], 0\n" \
"ds_read_b64_tr_b16 a[164:165], v33 offset:32768\n" \
"v_cvt_pk_bf16_f32 v214, v68, v69\n" \
"v_mfma_f32_16x16x32_bf16 v[132:135], a[240:243], a[212:215], v[132:135]\n" \
"ds_read_b128 a[148:151], v28 offset:43008\n" \
"v_cvt_pk_bf16_f32 v215, v70, v71\n" \
"v_mfma_f32_16x16x32_bf16 v[148:151], a[176:179], a[152:155], 0\n" \
"ds_read_b128 a[212:215], v29 offset:43008\n" \
"v_mfma_f32_16x16x32_bf16 v[148:151], a[240:243], a[216:219], v[148:151]\n" \
"s_movk_i32 s84, 0x2100\n" \
"v_mfma_f32_16x16x32_bf16 v[152:155], a[180:183], a[152:155], 0\n" \
"buffer_load_dwordx4 v25, s[76:79], s84 offen offset:1024 lds\n" \
"v_mfma_f32_16x16x32_bf16 v[152:155], a[244:247], a[216:219], v[152:155]\n" \
"ds_read_b64_tr_b16 a[166:167], v33 offset:33280\n" \
"v_mfma_f32_16x16x32_bf16 v[156:159], a[184:187], a[152:155], 0\n" \
"ds_read_b64_tr_b16 a[228:229], v33 offset:36864\n" \
"v_mfma_f32_16x16x32_bf16 v[156:159], a[248:251], a[216:219], v[156:159]\n" \
"s_movk_i32 s84, 0x4100\n" \
"v_mfma_f32_16x16x32_bf16 v[160:163], a[188:191], a[152:155], 0\n" \
"buffer_load_dwordx4 v24, s[76:79], s84 offen offset:2048 lds\n" \
"v_mfma_f32_16x16x32_bf16 v[160:163], a[252:255], a[216:219], v[160:163]\n" \
"ds_read_b128 a[152:155], v28 offset:45056\n" \
"v_mfma_f32_16x16x32_bf16 v[176:179], a[188:191], a[156:159], 0\n" \
"ds_read_b128 a[216:219], v29 offset:45056\n" \
"v_mfma_f32_16x16x32_bf16 v[176:179], a[252:255], a[220:223], v[176:179]\n" \
"s_movk_i32 s84, 0x6100\n" \
"s_nop 0\n" \
"v_mfma_f32_16x16x32_bf16 v[172:175], a[184:187], a[156:159], 0\n" \
"buffer_load_dwordx4 v25, s[76:79], s84 offen offset:3072 lds\n" \
"v_mfma_f32_16x16x32_bf16 v[172:175], a[248:251], a[220:223], v[172:175]\n" \
"ds_read_b64_tr_b16 a[230:231], v33 offset:37376\n" \
"v_mfma_f32_16x16x32_bf16 v[168:171], a[180:183], a[156:159], 0\n" \
"ds_read_b64_tr_b16 a[172:173], v35 offset:32768\n" \
"v_mfma_f32_16x16x32_bf16 v[168:171], a[244:247], a[220:223], v[168:171]\n" \
"ds_read_b64_tr_b16 a[174:175], v35 offset:33280\n" \
"v_mfma_f32_16x16x32_bf16 v[164:167], a[176:179], a[156:159], 0\n" \
"v_mfma_f32_16x16x32_bf16 v[164:167], a[240:243], a[220:223], v[164:167]\n" \
"ds_read_b128 a[156:159], v28 offset:47104\n" \
"v_mfma_f32_16x16x32_bf16 v[84:87], a[176:179], a[136:139], 0\n" \
"ds_read_b128 a[220:223], v29 offset:47104\n" \
"v_mfma_f32_16x16x32_bf16 v[84:87], a[240:243], a[200:203], v[84:87]\n" \
"s_add_u32 m0, s31, 0x0\n" \
"s_mov_b32 s84, 0x100000\n" \
"v_mfma_f32_16x16x32_bf16 v[88:91], a[180:183], a[136:139], 0\n" \
"buffer_load_dwordx4 v26, s[68:71], s84 offen offset:0 lds\n" \
"v_mfma_f32_16x16x32_bf16 v[88:91], a[244:247], a[200:203], v[88:91]\n" \
"s_addk_i32 s84, 0x7c00\n" \
"v_mfma_f32_16x16x32_bf16 v[92:95], a[184:187], a[136:139], 0\n" \
"buffer_load_dwordx4 v26, s[68:71], s84 offen offset:1024 lds\n" \
"v_mfma_f32_16x16x32_bf16 v[92:95], a[248:251], a[200:203], v[92:95]\n" \
"ds_read_b64_tr_b16 a[236:237], v35 offset:36864\n" \
"v_mfma_f32_16x16x32_bf16 v[96:99], a[188:191], a[136:139], 0\n" \
"ds_read_b64_tr_b16 a[238:239], v35 offset:37376\n" \
"v_mfma_f32_16x16x32_bf16 v[96:99], a[252:255], a[200:203], v[96:99]\n" \
"ds_read_b128 a[136:139], v28 offset:36864\n" \
"v_mfma_f32_16x16x32_bf16 v[112:115], a[188:191], a[140:143], 0\n" \
"ds_read_b128 a[200:203], v29 offset:36864\n" \
"v_mfma_f32_16x16x32_bf16 v[112:115], a[252:255], a[204:207], v[112:115]\n" \
"s_addk_i32 s84, 0x7c00\n" \
"v_mfma_f32_16x16x32_bf16 v[108:111], a[184:187], a[140:143], 0\n" \
"buffer_load_dwordx4 v27, s[68:71], s84 offen offset:2048 lds\n" \
"v_mfma_f32_16x16x32_bf16 v[108:111], a[248:251], a[204:207], v[108:111]\n" \
"s_addk_i32 s84, 0x7c00\n" \
"v_mfma_f32_16x16x32_bf16 v[104:107], a[180:183], a[140:143], 0\n" \
"buffer_load_dwordx4 v27, s[68:71], s84 offen offset:3072 lds\n" \
"v_mfma_f32_16x16x32_bf16 v[104:107], a[244:247], a[204:207], v[104:107]\n" \
"ds_read_b64_tr_b16 a[168:169], v34 offset:32768\n" \
"v_mfma_f32_16x16x32_bf16 v[100:103], a[176:179], a[140:143], 0\n" \
"ds_read_b64_tr_b16 a[170:171], v34 offset:33280\n" \
"v_mfma_f32_16x16x32_bf16 v[100:103], a[240:243], a[204:207], v[100:103]\n" \
"ds_read_b128 a[140:143], v28 offset:38912\n" \
"v_mfma_f32_16x16x32_bf16 v[52:55], a[176:179], a[128:131], 0\n" \
"ds_read_b128 a[204:207], v29 offset:38912\n" \
"v_mfma_f32_16x16x32_bf16 v[52:55], a[240:243], a[192:195], v[52:55]\n" \
"s_add_u32 m0, s30, 0x1000\n" \
"s_mov_b32 s84, 0x9100\n" \
"v_mfma_f32_16x16x32_bf16 v[56:59], a[180:183], a[128:131], 0\n" \
"buffer_load_dwordx4 v24, s[76:79], s84 offen offset:0 lds\n" \
"v_mfma_f32_16x16x32_bf16 v[56:59], a[244:247], a[192:195], v[56:59]\n" \
"s_addk_i32 s84, 0x2000\n" \
"s_nop 0\n" \
"v_mfma_f32_16x16x32_bf16 v[60:63], a[184:187], a[128:131], 0\n" \
"buffer_load_dwordx4 v25, s[76:79], s84 offen offset:1024 lds\n" \
"v_mfma_f32_16x16x32_bf16 v[60:63], a[248:251], a[192:195], v[60:63]\n" \
"ds_read_b64_tr_b16 a[232:233], v34 offset:36864\n" \
"v_mfma_f32_16x16x32_bf16 v[64:67], a[188:191], a[128:131], 0\n" \
"ds_read_b64_tr_b16 a[234:235], v34 offset:37376\n" \
"v_mfma_f32_16x16x32_bf16 v[64:67], a[252:255], a[192:195], v[64:67]\n" \
"ds_read_b128 a[128:131], v28 offset:32768\n" \
"v_mfma_f32_16x16x32_bf16 v[80:83], a[188:191], a[132:135], 0\n" \
"ds_read_b128 a[192:195], v29 offset:32768\n" \
"v_mfma_f32_16x16x32_bf16 v[80:83], a[252:255], a[196:199], v[80:83]\n" \
"v_mfma_f32_16x16x32_bf16 v[76:79], a[184:187], a[132:135], 0\n" \
"v_mfma_f32_16x16x32_bf16 v[76:79], a[248:251], a[196:199], v[76:79]\n" \
"v_mfma_f32_16x16x32_bf16 v[72:75], a[180:183], a[132:135], 0\n" \
"v_mfma_f32_16x16x32_bf16 v[72:75], a[244:247], a[196:199], v[72:75]\n" \
"v_mfma_f32_16x16x32_bf16 v[68:71], a[176:179], a[132:135], 0\n" \
"v_mfma_f32_16x16x32_bf16 v[68:71], a[240:243], a[196:199], v[68:71]\n" \
"ds_read_b128 a[132:135], v28 offset:34816\n" \
"ds_read_b128 a[196:199], v29 offset:34816\n" \
"s_waitcnt lgkmcnt(15)\n" \
"v_mfma_f32_16x16x32_bf16 a[0:3], a[160:163], a[144:147], a[0:3]\n" \
"s_addk_i32 s84, 0x2000\n" \
"v_mfma_f32_16x16x32_bf16 a[0:3], a[224:227], a[208:211], a[0:3]\n" \
"buffer_load_dwordx4 v24, s[76:79], s84 offen offset:2048 lds\n" \
"v_mfma_f32_16x16x32_bf16 a[4:7], a[164:167], a[144:147], a[4:7]\n" \
"ds_read_b64_tr_b16 a[176:177], v36 offset:32768\n" \
"v_mov_b32_e64 v11, 0\n" \
"v_mfma_f32_16x16x32_bf16 a[4:7], a[228:231], a[208:211], a[4:7]\n" \
"s_addk_i32 s84, 0x2000\n" \
"v_mfma_f32_16x16x32_bf16 a[12:15], a[172:175], a[144:147], a[12:15]\n" \
"buffer_load_dwordx4 v25, s[76:79], s84 offen offset:3072 lds\n" \
"s_waitcnt lgkmcnt(13)\n" \
"v_mfma_f32_16x16x32_bf16 a[12:15], a[236:239], a[208:211], a[12:15]\n" \
"ds_read_b64_tr_b16 a[178:179], v36 offset:33792\n" \
"s_and_b32 s6, s2, 7\n" \
"s_waitcnt lgkmcnt(10)\n" \
"v_mfma_f32_16x16x32_bf16 a[8:11], a[144:147], a[168:171], a[8:11]\n" \
"ds_read_b64_tr_b16 a[240:241], v36 offset:40960\n" \
"s_lshl_b32 s7, s3, 1\n" \
"s_waitcnt lgkmcnt(7)\n" \
"v_mfma_f32_16x16x32_bf16 a[8:11], a[208:211], a[232:235], a[8:11]\n" \
"ds_read_b64_tr_b16 a[242:243], v36 offset:41984\n" \
"s_lshr_b32 s4, s2, 5\n" \
"v_mfma_f32_16x16x32_bf16 a[24:27], a[148:151], a[168:171], a[24:27]\n" \
"ds_read_b64_tr_b16 a[180:181], v37 offset:32768\n" \
"s_lshl_b32 s5, s6, 2\n" \
"v_mfma_f32_16x16x32_bf16 a[24:27], a[212:215], a[232:235], a[24:27]\n" \
"ds_read_b64_tr_b16 a[182:183], v37 offset:33792\n" \
"s_bfe_u32 s8, s2, 0x20003\n" \
"v_mfma_f32_16x16x32_bf16 a[28:31], a[172:175], a[148:151], a[28:31]\n" \
"ds_read_b64_tr_b16 a[244:245], v37 offset:40960\n" \
"s_and_b32 s7, s7, 4\n" \
"v_mfma_f32_16x16x32_bf16 a[28:31], a[236:239], a[212:215], a[28:31]\n" \
"s_add_u32 s89, s41, 0x3000\n" \
"buffer_store_dwordx4 v[192:195], v20, s[80:83], s89 offen offset:3072 sc1\n" \
"v_mfma_f32_16x16x32_bf16 a[20:23], a[164:167], a[148:151], a[20:23]\n" \
"ds_read_b64_tr_b16 a[246:247], v37 offset:41984\n" \
"s_and_b32 s9, s3, 1\n" \
"v_mfma_f32_16x16x32_bf16 a[20:23], a[228:231], a[212:215], a[20:23]\n" \
"ds_read_b64_tr_b16 a[184:185], v38 offset:32768\n" \
"s_or_b32 s13, s5, s8\n" \
"v_mfma_f32_16x16x32_bf16 a[16:19], a[160:163], a[148:151], a[16:19]\n" \
"ds_read_b64_tr_b16 a[186:187], v38 offset:33792\n" \
"s_or_b32 s9, s7, s9\n" \
"v_mfma_f32_16x16x32_bf16 a[16:19], a[224:227], a[212:215], a[16:19]\n" \
"s_mul_i32 s7, s4, 0xa00\n" \
"s_add_i32 s7, s7, 0x5bc1000\n" \
"v_mfma_f32_16x16x32_bf16 a[32:35], a[160:163], a[152:155], a[32:35]\n" \
"s_add_u32 s88, s41, 0x3000\n" \
"buffer_store_dwordx4 v[196:199], v20, s[80:83], s88 offen offset:2048 sc1\n" \
"v_mfma_f32_16x16x32_bf16 a[32:35], a[224:227], a[216:219], a[32:35]\n" \
"ds_read_b64_tr_b16 a[248:249], v38 offset:40960\n" \
"s_lshl_b32 s10, s13, 2\n" \
"v_mfma_f32_16x16x32_bf16 a[36:39], a[164:167], a[152:155], a[36:39]\n" \
"ds_read_b64_tr_b16 a[250:251], v38 offset:41984\n" \
"s_or_b32 s10, s10, s7\n" \
"v_mfma_f32_16x16x32_bf16 a[36:39], a[228:231], a[216:219], a[36:39]\n" \
"ds_read_b64_tr_b16 a[188:189], v39 offset:32768\n" \
"v_writelane_b32 v11, s10, 0\n" \
"v_mfma_f32_16x16x32_bf16 a[44:47], a[172:175], a[152:155], a[44:47]\n" \
"s_lshl_b32 s14, s4, 3\n" \
"v_writelane_b32 v11, s7, 1\n" \
"v_mfma_f32_16x16x32_bf16 a[44:47], a[236:239], a[216:219], a[44:47]\n" \
"ds_read_b64_tr_b16 a[190:191], v39 offset:33792\n" \
"s_or_b32 s7, s14, s6\n" \
"v_mfma_f32_16x16x32_bf16 a[40:43], a[152:155], a[168:171], a[40:43]\n" \
"s_add_u32 s89, s41, 0x3000\n" \
"buffer_store_dwordx4 a[84:87], v20, s[80:83], s89 offen offset:1024 sc1\n" \
"v_mfma_f32_16x16x32_bf16 a[40:43], a[216:219], a[232:235], a[40:43]\n" \
"ds_read_b64_tr_b16 a[252:253], v39 offset:40960\n" \
"s_lshl_b32 s7, s7, 10\n" \
"v_mfma_f32_16x16x32_bf16 a[56:59], a[156:159], a[168:171], a[56:59]\n" \
"ds_read_b64_tr_b16 a[254:255], v39 offset:41984\n" \
"s_lshl_b32 s10, s3, 8\n" \
"v_mfma_f32_16x16x32_bf16 a[56:59], a[220:223], a[232:235], a[56:59]\n" \
"s_or_b32 s7, s7, s10\n" \
"s_or_b32 s10, s7, 0x5c00200\n" \
"v_mfma_f32_16x16x32_bf16 a[60:63], a[172:175], a[156:159], a[60:63]\n" \
"v_writelane_b32 v11, s10, 2\n" \
"s_lshl_b32 s10, s8, 2\n" \
"v_mfma_f32_16x16x32_bf16 a[60:63], a[236:239], a[220:223], a[60:63]\n" \
"s_add_u32 s88, s41, 0x3000\n" \
"buffer_store_dwordx4 v[252:255], v20, s[80:83], s88 offen offset:0 sc1\n" \
"v_mfma_f32_16x16x32_bf16 a[52:55], a[164:167], a[156:159], a[52:55]\n" \
"s_or_b32 s11, s10, s7\n" \
"s_or_b32 s11, s11, 0x5c00000\n" \
"v_mfma_f32_16x16x32_bf16 a[52:55], a[228:231], a[220:223], a[52:55]\n" \
"v_writelane_b32 v11, s11, 3\n" \
"s_or_b32 s7, s7, 0x5c00080\n" \
"v_mfma_f32_16x16x32_bf16 a[48:51], a[160:163], a[156:159], a[48:51]\n" \
"v_writelane_b32 v11, s7, 4\n" \
"s_or_b32 s7, s7, s10\n" \
"v_mfma_f32_16x16x32_bf16 a[48:51], a[224:227], a[220:223], a[48:51]\n" \
"s_add_i32 s10, s3, s2\n" \
"v_writelane_b32 v11, s7, 5\n" \
"v_mfma_f32_16x16x32_bf16 a[64:67], a[164:167], a[140:143], a[64:67]\n" \
"s_mov_b32 s7, 2.0\n" \
"s_mov_b32 s11, 0x5bc0000\n" \
"v_mfma_f32_16x16x32_bf16 a[64:67], a[228:231], a[204:207], a[64:67]\n" \
"s_cmp_lg_u32 s10, 0\n" \
"s_cselect_b32 s10, s7, s11\n" \
"v_mfma_f32_16x16x32_bf16 a[80:83], a[172:175], a[140:143], a[80:83]\n" \
"s_lshr_b32 s15, s2, 3\n" \
"v_writelane_b32 v11, s10, 53\n" \
"v_mfma_f32_16x16x32_bf16 a[80:83], a[236:239], a[204:207], a[80:83]\n" \
"s_lshl_b32 s10, s6, 5\n" \
"s_min_u32 s16, s3, 2\n" \
"v_mfma_f32_16x16x32_bf16 a[68:71], a[140:143], a[168:171], a[68:71]\n" \
"s_mul_i32 s12, s8, 0x120\n" \
"v_mfma_f32_16x16x32_bf16 a[68:71], a[204:207], a[232:235], a[68:71]\n" \
"s_waitcnt vmcnt(4) lgkmcnt(0)\n" \
"s_barrier\n" \
"s_nop 0\n" \
"v_mfma_f32_16x16x32_bf16 v[116:119], a[176:179], a[144:147], v[116:119]\n" \
"s_add_u32 m0, s40, 0x8000\n" \
"s_add_u32 s85, s39, 0x6000\n" \
"v_mfma_f32_16x16x32_bf16 v[116:119], a[240:243], a[208:211], v[116:119]\n" \
"buffer_load_dwordx4 v20, s[80:83], s85 offen sc1 lds\n" \
"v_mfma_f32_16x16x32_bf16 v[120:123], a[180:183], a[144:147], v[120:123]\n" \
"ds_read_b64_tr_b16 a[160:161], v32 offset:0\n" \
"v_mfma_f32_16x16x32_bf16 v[120:123], a[244:247], a[208:211], v[120:123]\n" \
"ds_read_b64_tr_b16 a[162:163], v32 offset:512\n" \
"v_mfma_f32_16x16x32_bf16 v[124:127], a[184:187], a[144:147], v[124:127]\n" \
"s_add_u32 m0, s40, 0x9000\n" \
"s_add_u32 s85, s39, 0x7000\n" \
"v_mfma_f32_16x16x32_bf16 v[124:127], a[248:251], a[208:211], v[124:127]\n" \
"buffer_load_dwordx4 v20, s[80:83], s85 offen sc1 lds\n" \
"v_mfma_f32_16x16x32_bf16 v[128:131], a[188:191], a[144:147], v[128:131]\n" \
"ds_read_b64_tr_b16 a[224:225], v32 offset:4096\n" \
"v_mfma_f32_16x16x32_bf16 v[128:131], a[252:255], a[208:211], v[128:131]\n" \
"ds_read_b128 a[144:147], v28 offset:8192\n" \
"v_mfma_f32_16x16x32_bf16 v[144:147], a[188:191], a[148:151], v[144:147]\n" \
"ds_read_b128 a[208:211], v29 offset:8192\n" \
"v_mfma_f32_16x16x32_bf16 v[144:147], a[252:255], a[212:215], v[144:147]\n" \
"s_add_u32 m0, s40, 0xb000\n" \
"s_movk_i32 s84, 0x180\n" \
"v_mfma_f32_16x16x32_bf16 v[140:143], a[184:187], a[148:151], v[140:143]\n" \
"buffer_load_dwordx4 v21, s[76:79], s84 offen offset:0 lds\n" \
"v_mfma_f32_16x16x32_bf16 v[140:143], a[248:251], a[212:215], v[140:143]\n" \
"ds_read_b64_tr_b16 a[226:227], v32 offset:4608\n" \
"v_mfma_f32_16x16x32_bf16 v[136:139], a[180:183], a[148:151], v[136:139]\n" \
"s_add_u32 m0, s30, 0x8000\n" \
"s_movk_i32 s84, 0x180\n" \
"v_mfma_f32_16x16x32_bf16 v[136:139], a[244:247], a[212:215], v[136:139]\n" \
"buffer_load_dwordx4 v24, s[76:79], s84 offen offset:0 lds\n" \
"v_mfma_f32_16x16x32_bf16 v[132:135], a[176:179], a[148:151], v[132:135]\n" \
"ds_read_b64_tr_b16 a[164:165], v33 offset:0\n" \
"v_mfma_f32_16x16x32_bf16 v[132:135], a[240:243], a[212:215], v[132:135]\n" \
"ds_read_b128 a[148:151], v28 offset:10240\n" \
"v_mfma_f32_16x16x32_bf16 v[148:151], a[176:179], a[152:155], v[148:151]\n" \
"ds_read_b128 a[212:215], v29 offset:10240\n" \
"v_mfma_f32_16x16x32_bf16 v[148:151], a[240:243], a[216:219], v[148:151]\n" \
"s_movk_i32 s84, 0x2180\n" \
"v_mfma_f32_16x16x32_bf16 v[152:155], a[180:183], a[152:155], v[152:155]\n" \
"buffer_load_dwordx4 v25, s[76:79], s84 offen offset:1024 lds\n" \
"v_mfma_f32_16x16x32_bf16 v[152:155], a[244:247], a[216:219], v[152:155]\n" \
"ds_read_b64_tr_b16 a[166:167], v33 offset:512\n" \
"v_mfma_f32_16x16x32_bf16 v[156:159], a[184:187], a[152:155], v[156:159]\n" \
"ds_read_b64_tr_b16 a[228:229], v33 offset:4096\n" \
"v_mfma_f32_16x16x32_bf16 v[156:159], a[248:251], a[216:219], v[156:159]\n" \
"s_movk_i32 s84, 0x4180\n" \
"v_mfma_f32_16x16x32_bf16 v[160:163], a[188:191], a[152:155], v[160:163]\n" \
"buffer_load_dwordx4 v24, s[76:79], s84 offen offset:2048 lds\n" \
"v_mfma_f32_16x16x32_bf16 v[160:163], a[252:255], a[216:219], v[160:163]\n" \
"ds_read_b128 a[152:155], v28 offset:12288\n" \
"v_mfma_f32_16x16x32_bf16 v[176:179], a[188:191], a[156:159], v[176:179]\n" \
"ds_read_b128 a[216:219], v29 offset:12288\n" \
"v_mfma_f32_16x16x32_bf16 v[176:179], a[252:255], a[220:223], v[176:179]\n" \
"s_movk_i32 s84, 0x6180\n" \
"v_mfma_f32_16x16x32_bf16 v[172:175], a[184:187], a[156:159], v[172:175]\n" \
"buffer_load_dwordx4 v25, s[76:79], s84 offen offset:3072 lds\n" \
"v_mfma_f32_16x16x32_bf16 v[172:175], a[248:251], a[220:223], v[172:175]\n" \
"ds_read_b64_tr_b16 a[230:231], v33 offset:4608\n" \
"v_mfma_f32_16x16x32_bf16 v[168:171], a[180:183], a[156:159], v[168:171]\n" \
"ds_read_b64_tr_b16 a[172:173], v35 offset:0\n" \
"v_mfma_f32_16x16x32_bf16 v[168:171], a[244:247], a[220:223], v[168:171]\n" \
"ds_read_b64_tr_b16 a[174:175], v35 offset:512\n" \
"v_mfma_f32_16x16x32_bf16 v[164:167], a[176:179], a[156:159], v[164:167]\n" \
"s_add_u32 s11, s22, s12\n" \
"s_addc_u32 s17, s23, 0\n" \
"v_mfma_f32_16x16x32_bf16 v[164:167], a[240:243], a[220:223], v[164:167]\n" \
"ds_read_b128 a[156:159], v28 offset:14336\n" \
"v_mfma_f32_16x16x32_bf16 v[84:87], a[176:179], a[136:139], v[84:87]\n" \
"ds_read_b128 a[220:223], v29 offset:14336\n" \
"v_mfma_f32_16x16x32_bf16 v[84:87], a[240:243], a[200:203], v[84:87]\n" \
"s_add_u32 m0, s31, 0x8000\n" \
"s_mov_b32 s84, 0x180000\n" \
"v_mfma_f32_16x16x32_bf16 v[88:91], a[180:183], a[136:139], v[88:91]\n" \
"buffer_load_dwordx4 v26, s[68:71], s84 offen offset:0 lds\n" \
"v_mfma_f32_16x16x32_bf16 v[88:91], a[244:247], a[200:203], v[88:91]\n" \
"s_addk_i32 s84, 0x7c00\n" \
"v_mfma_f32_16x16x32_bf16 v[92:95], a[184:187], a[136:139], v[92:95]\n" \
"buffer_load_dwordx4 v26, s[68:71], s84 offen offset:1024 lds\n" \
"v_mfma_f32_16x16x32_bf16 v[92:95], a[248:251], a[200:203], v[92:95]\n" \
"ds_read_b64_tr_b16 a[236:237], v35 offset:4096\n" \
"v_mfma_f32_16x16x32_bf16 v[96:99], a[188:191], a[136:139], v[96:99]\n" \
"ds_read_b64_tr_b16 a[238:239], v35 offset:4608\n" \
"v_mfma_f32_16x16x32_bf16 v[96:99], a[252:255], a[200:203], v[96:99]\n" \
"ds_read_b128 a[136:139], v28 offset:4096\n" \
"v_mfma_f32_16x16x32_bf16 v[112:115], a[188:191], a[140:143], v[112:115]\n" \
"ds_read_b128 a[200:203], v29 offset:4096\n" \
"v_mfma_f32_16x16x32_bf16 v[112:115], a[252:255], a[204:207], v[112:115]\n" \
"s_addk_i32 s84, 0x7c00\n" \
"v_mfma_f32_16x16x32_bf16 v[108:111], a[184:187], a[140:143], v[108:111]\n" \
"buffer_load_dwordx4 v27, s[68:71], s84 offen offset:2048 lds\n" \
"v_mfma_f32_16x16x32_bf16 v[108:111], a[248:251], a[204:207], v[108:111]\n" \
"s_addk_i32 s84, 0x7c00\n" \
"v_mfma_f32_16x16x32_bf16 v[104:107], a[180:183], a[140:143], v[104:107]\n" \
"buffer_load_dwordx4 v27, s[68:71], s84 offen offset:3072 lds\n" \
"v_mfma_f32_16x16x32_bf16 v[104:107], a[244:247], a[204:207], v[104:107]\n" \
"ds_read_b64_tr_b16 a[168:169], v34 offset:0\n" \
"v_mfma_f32_16x16x32_bf16 v[100:103], a[176:179], a[140:143], v[100:103]\n" \
"ds_read_b64_tr_b16 a[170:171], v34 offset:512\n" \
"v_mfma_f32_16x16x32_bf16 v[100:103], a[240:243], a[204:207], v[100:103]\n" \
"ds_read_b128 a[140:143], v28 offset:6144\n" \
"v_mfma_f32_16x16x32_bf16 v[52:55], a[176:179], a[128:131], v[52:55]\n" \
"ds_read_b128 a[204:207], v29 offset:6144\n" \
"v_mfma_f32_16x16x32_bf16 v[52:55], a[240:243], a[192:195], v[52:55]\n" \
"s_add_u32 m0, s30, 0x9000\n" \
"s_mov_b32 s84, 0x9180\n" \
"v_mfma_f32_16x16x32_bf16 v[56:59], a[180:183], a[128:131], v[56:59]\n" \
"buffer_load_dwordx4 v24, s[76:79], s84 offen offset:0 lds\n" \
"v_mfma_f32_16x16x32_bf16 v[56:59], a[244:247], a[192:195], v[56:59]\n" \
"s_addk_i32 s84, 0x2000\n" \
"s_nop 0\n" \
"v_mfma_f32_16x16x32_bf16 v[60:63], a[184:187], a[128:131], v[60:63]\n" \
"buffer_load_dwordx4 v25, s[76:79], s84 offen offset:1024 lds\n" \
"v_mfma_f32_16x16x32_bf16 v[60:63], a[248:251], a[192:195], v[60:63]\n" \
"ds_read_b64_tr_b16 a[232:233], v34 offset:4096\n" \
"v_mfma_f32_16x16x32_bf16 v[64:67], a[188:191], a[128:131], v[64:67]\n" \
"ds_read_b64_tr_b16 a[234:235], v34 offset:4608\n" \
"v_mfma_f32_16x16x32_bf16 v[64:67], a[252:255], a[192:195], v[64:67]\n" \
"ds_read_b128 a[128:131], v28 offset:0\n" \
"v_mfma_f32_16x16x32_bf16 v[80:83], a[188:191], a[132:135], v[80:83]\n" \
"ds_read_b128 a[192:195], v29 offset:0\n" \
"v_mfma_f32_16x16x32_bf16 v[80:83], a[252:255], a[196:199], v[80:83]\n" \
"s_add_u32 s18, s24, s12\n" \
"v_writelane_b32 v11, s11, 9\n" \
"s_addc_u32 s19, s25, 0\n" \
"v_mfma_f32_16x16x32_bf16 v[76:79], a[184:187], a[132:135], v[76:79]\n" \
"s_and_b32 s20, s17, 0xffff\n" \
"v_writelane_b32 v11, s20, 10\n" \
"v_mfma_f32_16x16x32_bf16 v[76:79], a[248:251], a[196:199], v[76:79]\n" \
"s_and_b32 s20, s19, 0xffff\n" \
"v_writelane_b32 v11, s18, 11\n" \
"v_mfma_f32_16x16x32_bf16 v[72:75], a[180:183], a[132:135], v[72:75]\n" \
"s_cmpk_lt_u32 s3, 3\n" \
"s_cselect_b32 s11, s11, s18\n" \
"v_mfma_f32_16x16x32_bf16 v[72:75], a[244:247], a[196:199], v[72:75]\n" \
"s_movk_i32 s18, 0xfff\n" \
"v_writelane_b32 v11, s20, 12\n" \
"v_mfma_f32_16x16x32_bf16 v[68:71], a[176:179], a[132:135], v[68:71]\n" \
"s_and_b32 s20, s29, 0xffff\n" \
"v_writelane_b32 v11, s28, 13\n" \
"v_mfma_f32_16x16x32_bf16 v[68:71], a[240:243], a[196:199], v[68:71]\n" \
"ds_read_b128 a[132:135], v28 offset:2048\n" \
"ds_read_b128 a[196:199], v29 offset:2048\n" \
"s_waitcnt lgkmcnt(15)\n" \
"v_mfma_f32_16x16x32_bf16 a[0:3], a[160:163], a[144:147], a[0:3]\n" \
"s_addk_i32 s84, 0x2000\n" \
"v_mfma_f32_16x16x32_bf16 a[0:3], a[224:227], a[208:211], a[0:3]\n" \
"buffer_load_dwordx4 v24, s[76:79], s84 offen offset:2048 lds\n" \
"v_mfma_f32_16x16x32_bf16 a[4:7], a[164:167], a[144:147], a[4:7]\n" \
"ds_read_b64_tr_b16 a[176:177], v36 offset:0\n" \
"v_writelane_b32 v11, s20, 14\n" \
"v_mfma_f32_16x16x32_bf16 a[4:7], a[228:231], a[208:211], a[4:7]\n" \
"s_addk_i32 s84, 0x2000\n" \
"v_mfma_f32_16x16x32_bf16 a[12:15], a[172:175], a[144:147], a[12:15]\n" \
"buffer_load_dwordx4 v25, s[76:79], s84 offen offset:3072 lds\n" \
"s_waitcnt lgkmcnt(13)\n" \
"v_mfma_f32_16x16x32_bf16 a[12:15], a[236:239], a[208:211], a[12:15]\n" \
"ds_read_b64_tr_b16 a[178:179], v36 offset:1024\n" \
"v_writelane_b32 v11, s11, 15\n" \
"s_waitcnt lgkmcnt(10)\n" \
"v_mfma_f32_16x16x32_bf16 a[8:11], a[144:147], a[168:171], a[8:11]\n" \
"ds_read_b64_tr_b16 a[240:241], v36 offset:8192\n" \
"s_waitcnt lgkmcnt(7)\n" \
"s_nop 0\n" \
"v_mfma_f32_16x16x32_bf16 a[8:11], a[208:211], a[232:235], a[8:11]\n" \
"ds_read_b64_tr_b16 a[242:243], v36 offset:9216\n" \
"v_mfma_f32_16x16x32_bf16 a[24:27], a[148:151], a[168:171], a[24:27]\n" \
"ds_read_b64_tr_b16 a[180:181], v37 offset:0\n" \
"v_mfma_f32_16x16x32_bf16 a[24:27], a[212:215], a[232:235], a[24:27]\n" \
"ds_read_b64_tr_b16 a[182:183], v37 offset:1024\n" \
"v_mfma_f32_16x16x32_bf16 a[28:31], a[172:175], a[148:151], a[28:31]\n" \
"ds_read_b64_tr_b16 a[244:245], v37 offset:8192\n" \
"v_mfma_f32_16x16x32_bf16 a[28:31], a[236:239], a[212:215], a[28:31]\n" \
"s_add_u32 s89, s41, 0x1000\n" \
"buffer_store_dwordx4 v[200:203], v20, s[80:83], s89 offen offset:3072 sc1\n" \
"v_mfma_f32_16x16x32_bf16 a[20:23], a[164:167], a[148:151], a[20:23]\n" \
"ds_read_b64_tr_b16 a[246:247], v37 offset:9216\n" \
"v_mfma_f32_16x16x32_bf16 a[20:23], a[228:231], a[212:215], a[20:23]\n" \
"ds_read_b64_tr_b16 a[184:185], v38 offset:0\n" \
"v_mfma_f32_16x16x32_bf16 a[16:19], a[160:163], a[148:151], a[16:19]\n" \
"ds_read_b64_tr_b16 a[186:187], v38 offset:1024\n" \
"v_mfma_f32_16x16x32_bf16 a[16:19], a[224:227], a[212:215], a[16:19]\n" \
"s_cmpk_lt_u32 s3, 3\n" \
"s_cselect_b32 s11, s17, s19\n" \
"v_mfma_f32_16x16x32_bf16 a[32:35], a[160:163], a[152:155], a[32:35]\n" \
"s_add_u32 s88, s41, 0x1000\n" \
"buffer_store_dwordx4 v[204:207], v20, s[80:83], s88 offen offset:2048 sc1\n" \
"v_mfma_f32_16x16x32_bf16 a[32:35], a[224:227], a[216:219], a[32:35]\n" \
"ds_read_b64_tr_b16 a[248:249], v38 offset:8192\n" \
"s_and_b32 s11, s11, 0xffff\n" \
"v_mfma_f32_16x16x32_bf16 a[36:39], a[164:167], a[152:155], a[36:39]\n" \
"ds_read_b64_tr_b16 a[250:251], v38 offset:9216\n" \
"v_writelane_b32 v11, s11, 16\n" \
"v_mfma_f32_16x16x32_bf16 a[36:39], a[228:231], a[216:219], a[36:39]\n" \
"ds_read_b64_tr_b16 a[188:189], v39 offset:0\n" \
"s_mul_i32 s11, s6, 0x48\n" \
"v_mfma_f32_16x16x32_bf16 a[44:47], a[172:175], a[152:155], a[44:47]\n" \
"s_add_i32 s17, s11, 0x47\n" \
"v_mfma_f32_16x16x32_bf16 a[44:47], a[236:239], a[216:219], a[44:47]\n" \
"ds_read_b64_tr_b16 a[190:191], v39 offset:1024\n" \
"v_mfma_f32_16x16x32_bf16 a[40:43], a[152:155], a[168:171], a[40:43]\n" \
"s_add_u32 s89, s41, 0x1000\n" \
"buffer_store_dwordx4 v[208:211], v20, s[80:83], s89 offen offset:1024 sc1\n" \
"v_mfma_f32_16x16x32_bf16 a[40:43], a[216:219], a[232:235], a[40:43]\n" \
"ds_read_b64_tr_b16 a[252:253], v39 offset:8192\n" \
"v_mfma_f32_16x16x32_bf16 a[56:59], a[156:159], a[168:171], a[56:59]\n" \
"ds_read_b64_tr_b16 a[254:255], v39 offset:9216\n" \
"v_mfma_f32_16x16x32_bf16 a[56:59], a[220:223], a[232:235], a[56:59]\n" \
"s_cmpk_lt_u32 s3, 3\n" \
"s_cselect_b32 s17, s17, s18\n" \
"v_mfma_f32_16x16x32_bf16 a[60:63], a[172:175], a[156:159], a[60:63]\n" \
"s_mulk_i32 s17, 0x480\n" \
"s_addk_i32 s17, 0x120\n" \
"v_mfma_f32_16x16x32_bf16 a[60:63], a[236:239], a[220:223], a[60:63]\n" \
"s_add_u32 s88, s41, 0x1000\n" \
"buffer_store_dwordx4 v[212:215], v20, s[80:83], s88 offen offset:0 sc1\n" \
"v_mfma_f32_16x16x32_bf16 a[52:55], a[164:167], a[156:159], a[52:55]\n" \
"v_writelane_b32 v11, s17, 17\n" \
"s_lshl_b32 s17, s3, 5\n" \
"v_mfma_f32_16x16x32_bf16 a[52:55], a[228:231], a[220:223], a[52:55]\n" \
"s_add_i32 s11, s11, s17\n" \
"s_mul_i32 s17, s11, 0x480\n" \
"v_mfma_f32_16x16x32_bf16 a[48:51], a[160:163], a[156:159], a[48:51]\n" \
"s_mul_i32 s11, s13, 0x24000\n" \
"s_add_i32 s18, s11, 0x1b000\n" \
"v_mfma_f32_16x16x32_bf16 a[48:51], a[224:227], a[220:223], a[48:51]\n" \
"s_cmpk_lt_u32 s3, 3\n" \
"s_cselect_b32 s17, s17, s18\n" \
"v_mfma_f32_16x16x32_bf16 a[64:67], a[164:167], a[140:143], a[64:67]\n" \
"s_mul_i32 s19, s3, 0x9000\n" \
"v_writelane_b32 v11, s17, 18\n" \
"v_mfma_f32_16x16x32_bf16 a[64:67], a[228:231], a[204:207], a[64:67]\n" \
"s_mul_i32 s17, s16, 0x2400\n" \
"s_mul_i32 s16, s16, 0x9000\n" \
"v_mfma_f32_16x16x32_bf16 a[80:83], a[172:175], a[140:143], a[80:83]\n" \
"s_bitset1_b32 s17, 17\n" \
"v_writelane_b32 v11, s17, 19\n" \
"v_mfma_f32_16x16x32_bf16 a[80:83], a[236:239], a[204:207], a[80:83]\n" \
"s_add_i32 s16, s11, s16\n" \
"v_writelane_b32 v11, s16, 25\n" \
"v_mfma_f32_16x16x32_bf16 a[68:71], a[140:143], a[168:171], a[68:71]\n" \
"s_mul_i32 s16, s3, 0x2400\n" \
"s_add_i32 s17, s16, 0x6000\n" \
"v_mfma_f32_16x16x32_bf16 a[68:71], a[204:207], a[232:235], a[68:71]\n" \
"s_waitcnt vmcnt(4) lgkmcnt(0)\n" \
"s_barrier\n" \
"v_mfma_f32_16x16x32_bf16 v[116:119], a[176:179], a[144:147], v[116:119]\n" \
"s_add_u32 m0, s40, 0x0\n" \
"s_nop 0\n" \
"s_add_u32 s85, s39, 0x8000\n" \
"v_mfma_f32_16x16x32_bf16 v[116:119], a[240:243], a[208:211], v[116:119]\n" \
"buffer_load_dwordx4 v20, s[80:83], s85 offen sc1 lds\n" \
"v_mfma_f32_16x16x32_bf16 v[120:123], a[180:183], a[144:147], v[120:123]\n" \
"ds_read_b64_tr_b16 a[160:161], v32 offset:32768\n" \
"v_writelane_b32 v11, s17, 20\n" \
"v_mfma_f32_16x16x32_bf16 v[120:123], a[244:247], a[208:211], v[120:123]\n" \
"ds_read_b64_tr_b16 a[162:163], v32 offset:33280\n" \
"s_or_b32 s17, s16, 0x10000\n" \
"v_mfma_f32_16x16x32_bf16 v[124:127], a[184:187], a[144:147], v[124:127]\n" \
"s_add_u32 m0, s40, 0x1000\n" \
"s_add_u32 s85, s39, 0x9000\n" \
"v_mfma_f32_16x16x32_bf16 v[124:127], a[248:251], a[208:211], v[124:127]\n" \
"buffer_load_dwordx4 v20, s[80:83], s85 offen sc1 lds\n" \
"v_mfma_f32_16x16x32_bf16 v[128:131], a[188:191], a[144:147], v[128:131]\n" \
"ds_read_b64_tr_b16 a[224:225], v32 offset:36864\n" \
"s_add_i32 s18, s16, 0x19000\n" \
"v_mfma_f32_16x16x32_bf16 v[128:131], a[252:255], a[208:211], v[128:131]\n" \
"ds_read_b128 a[144:147], v28 offset:40960\n" \
".fill (L_pb + 65248 - .) / 4, 4, 0xBF800000\n" \
"L_pp16:\n" \
"s_cbranch_execz L_pret\n" \
"v_writelane_b32 v11, s17, 21\n" \
"v_mfma_f32_16x16x32_bf16 v[144:147], a[188:191], a[148:151], v[144:147]\n" \
"ds_read_b128 a[208:211], v29 offset:40960\n" \
"s_mov_b32 s17, 0\n" \
"v_mfma_f32_16x16x32_bf16 v[144:147], a[252:255], a[212:215], v[144:147]\n" \
"s_add_u32 m0, s40, 0x3000\n" \
"s_movk_i32 s84, 0x200\n" \
"v_mfma_f32_16x16x32_bf16 v[140:143], a[184:187], a[148:151], v[140:143]\n" \
"buffer_load_dwordx4 v21, s[76:79], s84 offen offset:0 lds\n" \
"v_mfma_f32_16x16x32_bf16 v[140:143], a[248:251], a[212:215], v[140:143]\n" \
"ds_read_b64_tr_b16 a[226:227], v32 offset:37376\n" \
"v_mfma_f32_16x16x32_bf16 v[136:139], a[180:183], a[148:151], v[136:139]\n" \
"s_add_u32 m0, s30, 0x0\n" \
"s_movk_i32 s84, 0x200\n" \
"v_mfma_f32_16x16x32_bf16 v[136:139], a[244:247], a[212:215], v[136:139]\n" \
"buffer_load_dwordx4 v24, s[76:79], s84 offen offset:0 lds\n" \
"v_mfma_f32_16x16x32_bf16 v[132:135], a[176:179], a[148:151], v[132:135]\n" \
"ds_read_b64_tr_b16 a[164:165], v33 offset:32768\n" \
"v_mfma_f32_16x16x32_bf16 v[132:135], a[240:243], a[212:215], v[132:135]\n" \
"ds_read_b128 a[148:151], v28 offset:43008\n" \
"v_mfma_f32_16x16x32_bf16 v[148:151], a[176:179], a[152:155], v[148:151]\n" \
"ds_read_b128 a[212:215], v29 offset:43008\n" \
"v_mfma_f32_16x16x32_bf16 v[148:151], a[240:243], a[216:219], v[148:151]\n" \
"s_movk_i32 s84, 0x2200\n" \
"v_mfma_f32_16x16x32_bf16 v[152:155], a[180:183], a[152:155], v[152:155]\n" \
"buffer_load_dwordx4 v25, s[76:79], s84 offen offset:1024 lds\n" \
"v_mfma_f32_16x16x32_bf16 v[152:155], a[244:247], a[216:219], v[152:155]\n" \
"ds_read_b64_tr_b16 a[166:167], v33 offset:33280\n" \
"v_mfma_f32_16x16x32_bf16 v[156:159], a[184:187], a[152:155], v[156:159]\n" \
"ds_read_b64_tr_b16 a[228:229], v33 offset:36864\n" \
"v_mfma_f32_16x16x32_bf16 v[156:159], a[248:251], a[216:219], v[156:159]\n" \
"s_movk_i32 s84, 0x4200\n" \
"v_mfma_f32_16x16x32_bf16 v[160:163], a[188:191], a[152:155], v[160:163]\n" \
"buffer_load_dwordx4 v24, s[76:79], s84 offen offset:2048 lds\n" \
"v_mfma_f32_16x16x32_bf16 v[160:163], a[252:255], a[216:219], v[160:163]\n" \
"ds_read_b128 a[152:155], v28 offset:45056\n" \
"v_mfma_f32_16x16x32_bf16 v[176:179], a[188:191], a[156:159], v[176:179]\n" \
"ds_read_b128 a[216:219], v29 offset:45056\n" \
"v_mfma_f32_16x16x32_bf16 v[176:179], a[252:255], a[220:223], v[176:179]\n" \
"s_movk_i32 s84, 0x6200\n" \
"v_mfma_f32_16x16x32_bf16 v[172:175], a[184:187], a[156:159], v[172:175]\n" \
"buffer_load_dwordx4 v25, s[76:79], s84 offen offset:3072 lds\n" \
"v_mfma_f32_16x16x32_bf16 v[172:175], a[248:251], a[220:223], v[172:175]\n" \
"ds_read_b64_tr_b16 a[230:231], v33 offset:37376\n" \
"v_mfma_f32_16x16x32_bf16 v[168:171], a[180:183], a[156:159], v[168:171]\n" \
"ds_read_b64_tr_b16 a[172:173], v35 offset:32768\n" \
"v_mfma_f32_16x16x32_bf16 v[168:171], a[244:247], a[220:223], v[168:171]\n" \
"ds_read_b64_tr_b16 a[174:175], v35 offset:33280\n" \
"v_mfma_f32_16x16x32_bf16 v[164:167], a[176:179], a[156:159], v[164:167]\n" \
"s_cmpk_lt_u32 s3, 3\n" \
"s_cselect_b32 s18, s18, s17\n" \
"v_mfma_f32_16x16x32_bf16 v[164:167], a[240:243], a[220:223], v[164:167]\n" \
"ds_read_b128 a[156:159], v28 offset:47104\n" \
"s_bitset1_b32 s16, 17\n" \
"v_mfma_f32_16x16x32_bf16 v[84:87], a[176:179], a[136:139], v[84:87]\n" \
"ds_read_b128 a[220:223], v29 offset:47104\n" \
"v_writelane_b32 v11, s18, 22\n" \
"v_mfma_f32_16x16x32_bf16 v[84:87], a[240:243], a[200:203], v[84:87]\n" \
"s_add_u32 m0, s31, 0x0\n" \
"s_mov_b32 s84, 0x200000\n" \
"v_mfma_f32_16x16x32_bf16 v[88:91], a[180:183], a[136:139], v[88:91]\n" \
"buffer_load_dwordx4 v26, s[68:71], s84 offen offset:0 lds\n" \
"v_mfma_f32_16x16x32_bf16 v[88:91], a[244:247], a[200:203], v[88:91]\n" \
"s_addk_i32 s84, 0x7c00\n" \
"v_mfma_f32_16x16x32_bf16 v[92:95], a[184:187], a[136:139], v[92:95]\n" \
"buffer_load_dwordx4 v26, s[68:71], s84 offen offset:1024 lds\n" \
"v_mfma_f32_16x16x32_bf16 v[92:95], a[248:251], a[200:203], v[92:95]\n" \
"ds_read_b64_tr_b16 a[236:237], v35 offset:36864\n" \
"s_movk_i32 s18, 0x2400\n" \
"v_mfma_f32_16x16x32_bf16 v[96:99], a[188:191], a[136:139], v[96:99]\n" \
"ds_read_b64_tr_b16 a[238:239], v35 offset:37376\n" \
"v_mfma_f32_16x16x32_bf16 v[96:99], a[252:255], a[200:203], v[96:99]\n" \
"ds_read_b128 a[136:139], v28 offset:36864\n" \
"v_mfma_f32_16x16x32_bf16 v[112:115], a[188:191], a[140:143], v[112:115]\n" \
"ds_read_b128 a[200:203], v29 offset:36864\n" \
"v_mfma_f32_16x16x32_bf16 v[112:115], a[252:255], a[204:207], v[112:115]\n" \
"s_addk_i32 s84, 0x7c00\n" \
"v_mfma_f32_16x16x32_bf16 v[108:111], a[184:187], a[140:143], v[108:111]\n" \
"buffer_load_dwordx4 v27, s[68:71], s84 offen offset:2048 lds\n" \
"v_mfma_f32_16x16x32_bf16 v[108:111], a[248:251], a[204:207], v[108:111]\n" \
"s_addk_i32 s84, 0x7c00\n" \
"v_mfma_f32_16x16x32_bf16 v[104:107], a[180:183], a[140:143], v[104:107]\n" \
"buffer_load_dwordx4 v27, s[68:71], s84 offen offset:3072 lds\n" \
"v_mfma_f32_16x16x32_bf16 v[104:107], a[244:247], a[204:207], v[104:107]\n" \
"ds_read_b64_tr_b16 a[168:169], v34 offset:32768\n" \
"v_mfma_f32_16x16x32_bf16 v[100:103], a[176:179], a[140:143], v[100:103]\n" \
"ds_read_b64_tr_b16 a[170:171], v34 offset:33280\n" \
"v_mfma_f32_16x16x32_bf16 v[100:103], a[240:243], a[204:207], v[100:103]\n" \
"ds_read_b128 a[140:143], v28 offset:38912\n" \
"v_mfma_f32_16x16x32_bf16 v[52:55], a[176:179], a[128:131], v[52:55]\n" \
"ds_read_b128 a[204:207], v29 offset:38912\n" \
"v_mfma_f32_16x16x32_bf16 v[52:55], a[240:243], a[192:195], v[52:55]\n" \
"s_add_u32 m0, s30, 0x1000\n" \
"s_mov_b32 s84, 0x9200\n" \
"v_mfma_f32_16x16x32_bf16 v[56:59], a[180:183], a[128:131], v[56:59]\n" \
"buffer_load_dwordx4 v24, s[76:79], s84 offen offset:0 lds\n" \
"v_mfma_f32_16x16x32_bf16 v[56:59], a[244:247], a[192:195], v[56:59]\n" \
"s_addk_i32 s84, 0x2000\n" \
"s_nop 0\n" \
"v_mfma_f32_16x16x32_bf16 v[60:63], a[184:187], a[128:131], v[60:63]\n" \
"buffer_load_dwordx4 v25, s[76:79], s84 offen offset:1024 lds\n" \
"v_mfma_f32_16x16x32_bf16 v[60:63], a[248:251], a[192:195], v[60:63]\n" \
"ds_read_b64_tr_b16 a[232:233], v34 offset:36864\n" \
"v_mfma_f32_16x16x32_bf16 v[64:67], a[188:191], a[128:131], v[64:67]\n" \
"ds_read_b64_tr_b16 a[234:235], v34 offset:37376\n" \
"v_mfma_f32_16x16x32_bf16 v[64:67], a[252:255], a[192:195], v[64:67]\n" \
"ds_read_b128 a[128:131], v28 offset:32768\n" \
"v_mfma_f32_16x16x32_bf16 v[80:83], a[188:191], a[132:135], v[80:83]\n" \
"ds_read_b128 a[192:195], v29 offset:32768\n" \
"v_mfma_f32_16x16x32_bf16 v[80:83], a[252:255], a[196:199], v[80:83]\n" \
"s_cmpk_lt_u32 s3, 3\n" \
"s_cselect_b32 s16, s16, s18\n" \
"v_mfma_f32_16x16x32_bf16 v[76:79], a[184:187], a[132:135], v[76:79]\n" \
"s_xor_b32 s13, s13, 2\n" \
"v_writelane_b32 v11, s16, 23\n" \
"v_mfma_f32_16x16x32_bf16 v[76:79], a[248:251], a[196:199], v[76:79]\n" \
"s_lshl_b32 s16, s3, 10\n" \
"s_or_b32 s16, s16, 0xf000\n" \
"v_mfma_f32_16x16x32_bf16 v[72:75], a[180:183], a[132:135], v[72:75]\n" \
"v_writelane_b32 v11, s16, 24\n" \
"s_add_i32 s16, s15, 1\n" \
"v_mfma_f32_16x16x32_bf16 v[72:75], a[244:247], a[196:199], v[72:75]\n" \
"s_and_b32 s16, s16, 3\n" \
"s_or_b32 s16, s16, s5\n" \
"v_mfma_f32_16x16x32_bf16 v[68:71], a[176:179], a[132:135], v[68:71]\n" \
"s_mul_i32 s18, s16, 0x24000\n" \
"s_add_i32 s18, s18, s19\n" \
"v_mfma_f32_16x16x32_bf16 v[68:71], a[240:243], a[196:199], v[68:71]\n" \
"ds_read_b128 a[132:135], v28 offset:34816\n" \
"ds_read_b128 a[196:199], v29 offset:34816\n" \
"s_waitcnt lgkmcnt(15)\n" \
"v_mfma_f32_16x16x32_bf16 a[0:3], a[160:163], a[144:147], a[0:3]\n" \
"s_addk_i32 s84, 0x2000\n" \
"v_mfma_f32_16x16x32_bf16 a[0:3], a[224:227], a[208:211], a[0:3]\n" \
"buffer_load_dwordx4 v24, s[76:79], s84 offen offset:2048 lds\n" \
"v_mfma_f32_16x16x32_bf16 a[4:7], a[164:167], a[144:147], a[4:7]\n" \
"ds_read_b64_tr_b16 a[176:177], v36 offset:32768\n" \
"s_add_i32 s15, s15, -1\n" \
"v_mfma_f32_16x16x32_bf16 a[4:7], a[228:231], a[208:211], a[4:7]\n" \
"s_addk_i32 s84, 0x2000\n" \
"v_mfma_f32_16x16x32_bf16 a[12:15], a[172:175], a[144:147], a[12:15]\n" \
"buffer_load_dwordx4 v25, s[76:79], s84 offen offset:3072 lds\n" \
"s_waitcnt lgkmcnt(13)\n" \
"v_mfma_f32_16x16x32_bf16 a[12:15], a[236:239], a[208:211], a[12:15]\n" \
"ds_read_b64_tr_b16 a[178:179], v36 offset:33792\n" \
"v_writelane_b32 v11, s18, 26\n" \
"s_waitcnt lgkmcnt(10)\n" \
"v_mfma_f32_16x16x32_bf16 a[8:11], a[144:147], a[168:171], a[8:11]\n" \
"ds_read_b64_tr_b16 a[240:241], v36 offset:40960\n" \
"s_mul_i32 s18, s13, 0x24000\n" \
"s_waitcnt lgkmcnt(7)\n" \
"v_mfma_f32_16x16x32_bf16 a[8:11], a[208:211], a[232:235], a[8:11]\n" \
"ds_read_b64_tr_b16 a[242:243], v36 offset:41984\n" \
"s_and_b32 s15, s15, 3\n" \
"v_mfma_f32_16x16x32_bf16 a[24:27], a[148:151], a[168:171], a[24:27]\n" \
"ds_read_b64_tr_b16 a[180:181], v37 offset:32768\n" \
"s_add_i32 s18, s18, s19\n" \
"v_mfma_f32_16x16x32_bf16 a[24:27], a[212:215], a[232:235], a[24:27]\n" \
"ds_read_b64_tr_b16 a[182:183], v37 offset:33792\n" \
"s_or_b32 s15, s15, s5\n" \
"v_mfma_f32_16x16x32_bf16 a[28:31], a[172:175], a[148:151], a[28:31]\n" \
"ds_read_b64_tr_b16 a[244:245], v37 offset:40960\n" \
"v_writelane_b32 v11, s18, 27\n" \
"v_mfma_f32_16x16x32_bf16 a[28:31], a[236:239], a[212:215], a[28:31]\n" \
"s_add_u32 s89, s42, 0x0\n" \
"buffer_store_dwordx4 v[216:219], v20, s[80:83], s89 offen offset:1024 sc1\n" \
"v_mfma_f32_16x16x32_bf16 a[20:23], a[164:167], a[148:151], a[20:23]\n" \
"ds_read_b64_tr_b16 a[246:247], v37 offset:41984\n" \
"s_mul_i32 s18, s15, 0x24000\n" \
"v_mfma_f32_16x16x32_bf16 a[20:23], a[228:231], a[212:215], a[20:23]\n" \
"ds_read_b64_tr_b16 a[184:185], v38 offset:32768\n" \
"s_add_i32 s18, s18, s19\n" \
"v_mfma_f32_16x16x32_bf16 a[16:19], a[160:163], a[148:151], a[16:19]\n" \
"ds_read_b64_tr_b16 a[186:187], v38 offset:33792\n" \
"v_writelane_b32 v11, s18, 28\n" \
"v_mfma_f32_16x16x32_bf16 a[16:19], a[224:227], a[212:215], a[16:19]\n" \
"s_or_b32 s18, s9, s14\n" \
"s_mul_i32 s18, s18, 0x24800\n" \
"v_mfma_f32_16x16x32_bf16 a[32:35], a[160:163], a[152:155], a[32:35]\n" \
"s_add_u32 s88, s42, 0x0\n" \
"buffer_store_dwordx4 v[220:223], v20, s[80:83], s88 offen offset:0 sc1\n" \
"v_mfma_f32_16x16x32_bf16 a[32:35], a[224:227], a[216:219], a[32:35]\n" \
"ds_read_b64_tr_b16 a[248:249], v38 offset:40960\n" \
"s_lshl_b32 s16, s16, 12\n" \
"v_mfma_f32_16x16x32_bf16 a[36:39], a[164:167], a[152:155], a[36:39]\n" \
"ds_read_b64_tr_b16 a[250:251], v38 offset:41984\n" \
"s_add_i32 s19, s18, 0x3004800\n" \
"v_mfma_f32_16x16x32_bf16 a[36:39], a[228:231], a[216:219], a[36:39]\n" \
"ds_read_b64_tr_b16 a[188:189], v39 offset:32768\n" \
"s_add_i32 s20, s19, s16\n" \
"v_mfma_f32_16x16x32_bf16 a[44:47], a[172:175], a[152:155], a[44:47]\n" \
"v_writelane_b32 v11, s20, 29\n" \
"s_lshl_b32 s13, s13, 12\n" \
"v_mfma_f32_16x16x32_bf16 a[44:47], a[236:239], a[216:219], a[44:47]\n" \
"ds_read_b64_tr_b16 a[190:191], v39 offset:33792\n" \
"s_lshl_b32 s15, s15, 12\n" \
"v_mfma_f32_16x16x32_bf16 a[40:43], a[152:155], a[168:171], a[40:43]\n" \
"s_add_u32 s89, s42, 0x1000\n" \
"buffer_store_dwordx4 v[224:227], v20, s[80:83], s89 offen offset:3072 sc1\n" \
"v_mfma_f32_16x16x32_bf16 a[40:43], a[216:219], a[232:235], a[40:43]\n" \
"ds_read_b64_tr_b16 a[252:253], v39 offset:40960\n" \
"s_sub_i32 s14, s9, s14\n" \
"v_mfma_f32_16x16x32_bf16 a[56:59], a[156:159], a[168:171], a[56:59]\n" \
"ds_read_b64_tr_b16 a[254:255], v39 offset:41984\n" \
"s_add_i32 s20, s19, s13\n" \
"v_mfma_f32_16x16x32_bf16 a[56:59], a[220:223], a[232:235], a[56:59]\n" \
"v_writelane_b32 v11, s20, 30\n" \
"s_add_i32 s19, s19, s15\n" \
"v_mfma_f32_16x16x32_bf16 a[60:63], a[172:175], a[156:159], a[60:63]\n" \
"s_mul_i32 s14, s14, 0x24800\n" \
"v_writelane_b32 v11, s19, 31\n" \
"v_mfma_f32_16x16x32_bf16 a[60:63], a[236:239], a[220:223], a[60:63]\n" \
"s_add_u32 s88, s42, 0x1000\n" \
"buffer_store_dwordx4 v[228:231], v20, s[80:83], s88 offen offset:2048 sc1\n" \
"v_mfma_f32_16x16x32_bf16 a[52:55], a[164:167], a[156:159], a[52:55]\n" \
"s_add_i32 s19, s14, 0x4120800\n" \
"s_add_i32 s16, s19, s16\n" \
"v_mfma_f32_16x16x32_bf16 a[52:55], a[228:231], a[220:223], a[52:55]\n" \
"v_writelane_b32 v11, s16, 32\n" \
"s_add_i32 s13, s19, s13\n" \
"v_mfma_f32_16x16x32_bf16 a[48:51], a[160:163], a[156:159], a[48:51]\n" \
"v_writelane_b32 v11, s13, 33\n" \
"s_add_i32 s19, s19, s15\n" \
"v_mfma_f32_16x16x32_bf16 a[48:51], a[224:227], a[220:223], a[48:51]\n" \
"s_or_b32 s13, s18, 0x3000000\n" \
"s_lshl_b32 s15, s6, 11\n" \
"v_mfma_f32_16x16x32_bf16 a[64:67], a[164:167], a[140:143], a[64:67]\n" \
"v_writelane_b32 v11, s19, 34\n" \
"s_add_i32 s16, s13, s15\n" \
"v_mfma_f32_16x16x32_bf16 a[64:67], a[228:231], a[204:207], a[64:67]\n" \
"v_writelane_b32 v11, s16, 35\n" \
"s_add_i32 s16, s14, 0x411c000\n" \
"v_mfma_f32_16x16x32_bf16 a[80:83], a[172:175], a[140:143], a[80:83]\n" \
"s_add_i32 s19, s16, s15\n" \
"v_writelane_b32 v11, s19, 36\n" \
"v_mfma_f32_16x16x32_bf16 a[80:83], a[236:239], a[204:207], a[80:83]\n" \
"s_add_i32 s18, s18, 0x3004000\n" \
"v_writelane_b32 v11, s18, 37\n" \
"v_mfma_f32_16x16x32_bf16 a[68:71], a[140:143], a[168:171], a[68:71]\n" \
"s_add_i32 s14, s14, 0x4120000\n" \
"v_writelane_b32 v11, s14, 38\n" \
"v_mfma_f32_16x16x32_bf16 a[68:71], a[204:207], a[232:235], a[68:71]\n" \
"s_waitcnt vmcnt(4) lgkmcnt(0)\n" \
"s_barrier\n" \
"v_mov_b32_e32 v45, s62\n" \
"s_mov_b64 exec, 1\n" \
"buffer_store_dword v45, off, s[80:83], s37 offset:0 sc1\n" \
"s_mov_b64 exec, -1\n" \
"v_mfma_f32_16x16x32_bf16 v[116:119], a[176:179], a[144:147], v[116:119]\n" \
"s_add_u32 m0, s40, 0x8000\n" \
"s_add_u32 s85, s39, 0xa000\n" \
"v_mfma_f32_16x16x32_bf16 v[116:119], a[240:243], a[208:211], v[116:119]\n" \
"buffer_load_dwordx4 v20, s[80:83], s85 offen sc1 lds\n" \
"v_mfma_f32_16x16x32_bf16 v[120:123], a[180:183], a[144:147], v[120:123]\n" \
"ds_read_b64_tr_b16 a[160:161], v32 offset:0\n" \
"v_accvgpr_read_b32 v212, a108\n" \
"v_mfma_f32_16x16x32_bf16 v[120:123], a[244:247], a[208:211], v[120:123]\n" \
"ds_read_b64_tr_b16 a[162:163], v32 offset:512\n" \
"v_accvgpr_read_b32 v213, a109\n" \
"v_mfma_f32_16x16x32_bf16 v[124:127], a[184:187], a[144:147], v[124:127]\n" \
"s_add_u32 m0, s40, 0x9000\n" \
"s_add_u32 s85, s39, 0xb000\n" \
"v_mfma_f32_16x16x32_bf16 v[124:127], a[248:251], a[208:211], v[124:127]\n" \
"buffer_load_dwordx4 v20, s[80:83], s85 offen sc1 lds\n" \
"v_mfma_f32_16x16x32_bf16 v[128:131], a[188:191], a[144:147], v[128:131]\n" \
"ds_read_b64_tr_b16 a[224:225], v32 offset:4096\n" \
"v_accvgpr_read_b32 v214, a110\n" \
"v_mfma_f32_16x16x32_bf16 v[128:131], a[252:255], a[208:211], v[128:131]\n" \
"ds_read_b128 a[144:147], v28 offset:8192\n" \
"v_accvgpr_read_b32 v215, a111\n" \
"v_mfma_f32_16x16x32_bf16 v[144:147], a[188:191], a[148:151], v[144:147]\n" \
"ds_read_b128 a[208:211], v29 offset:8192\n" \
"v_accvgpr_read_b32 v180, a124\n" \
"v_mfma_f32_16x16x32_bf16 v[144:147], a[252:255], a[212:215], v[144:147]\n" \
"s_add_u32 m0, s40, 0xb000\n" \
"s_movk_i32 s84, 0x280\n" \
"v_mfma_f32_16x16x32_bf16 v[140:143], a[184:187], a[148:151], v[140:143]\n" \
"buffer_load_dwordx4 v21, s[76:79], s84 offen offset:0 lds\n" \
"v_mfma_f32_16x16x32_bf16 v[140:143], a[248:251], a[212:215], v[140:143]\n" \
"ds_read_b64_tr_b16 a[226:227], v32 offset:4608\n" \
"v_accvgpr_read_b32 v181, a125\n" \
"v_mfma_f32_16x16x32_bf16 v[136:139], a[180:183], a[148:151], v[136:139]\n" \
"s_add_u32 m0, s30, 0x8000\n" \
"s_movk_i32 s84, 0x280\n" \
"v_mfma_f32_16x16x32_bf16 v[136:139], a[244:247], a[212:215], v[136:139]\n" \
"buffer_load_dwordx4 v24, s[76:79], s84 offen offset:0 lds\n" \
"v_mfma_f32_16x16x32_bf16 v[132:135], a[176:179], a[148:151], v[132:135]\n" \
"ds_read_b64_tr_b16 a[164:165], v33 offset:0\n" \
"v_accvgpr_read_b32 v182, a126\n" \
"v_mfma_f32_16x16x32_bf16 v[132:135], a[240:243], a[212:215], v[132:135]\n" \
"ds_read_b128 a[148:151], v28 offset:10240\n" \
"v_accvgpr_read_b32 v183, a127\n" \
"v_mfma_f32_16x16x32_bf16 v[148:151], a[176:179], a[152:155], v[148:151]\n" \
"ds_read_b128 a[212:215], v29 offset:10240\n" \
"v_cvt_pk_bf16_f32 v212, v212, v213\n" \
"v_mfma_f32_16x16x32_bf16 v[148:151], a[240:243], a[216:219], v[148:151]\n" \
"s_movk_i32 s84, 0x2280\n" \
"v_mfma_f32_16x16x32_bf16 v[152:155], a[180:183], a[152:155], v[152:155]\n" \
"buffer_load_dwordx4 v25, s[76:79], s84 offen offset:1024 lds\n" \
"v_mfma_f32_16x16x32_bf16 v[152:155], a[244:247], a[216:219], v[152:155]\n" \
"ds_read_b64_tr_b16 a[166:167], v33 offset:512\n" \
"v_cvt_pk_bf16_f32 v213, v214, v215\n" \
"v_mfma_f32_16x16x32_bf16 v[156:159], a[184:187], a[152:155], v[156:159]\n" \
"ds_read_b64_tr_b16 a[228:229], v33 offset:4096\n" \
"v_cvt_pk_bf16_f32 v214, v180, v181\n" \
"v_mfma_f32_16x16x32_bf16 v[156:159], a[248:251], a[216:219], v[156:159]\n" \
"s_movk_i32 s84, 0x4280\n" \
"v_mfma_f32_16x16x32_bf16 v[160:163], a[188:191], a[152:155], v[160:163]\n" \
"buffer_load_dwordx4 v24, s[76:79], s84 offen offset:2048 lds\n" \
"v_mfma_f32_16x16x32_bf16 v[160:163], a[252:255], a[216:219], v[160:163]\n" \
"ds_read_b128 a[152:155], v28 offset:12288\n" \
"v_cvt_pk_bf16_f32 v215, v182, v183\n" \
"v_mfma_f32_16x16x32_bf16 v[176:179], a[188:191], a[156:159], v[176:179]\n" \
"ds_read_b128 a[216:219], v29 offset:12288\n" \
"v_accvgpr_read_b32 v208, a104\n" \
"v_mfma_f32_16x16x32_bf16 v[176:179], a[252:255], a[220:223], v[176:179]\n" \
"s_movk_i32 s84, 0x6280\n" \
"s_nop 0\n" \
"v_mfma_f32_16x16x32_bf16 v[172:175], a[184:187], a[156:159], v[172:175]\n" \
"buffer_load_dwordx4 v25, s[76:79], s84 offen offset:3072 lds\n" \
"v_mfma_f32_16x16x32_bf16 v[172:175], a[248:251], a[220:223], v[172:175]\n" \
"ds_read_b64_tr_b16 a[230:231], v33 offset:4608\n" \
"v_accvgpr_read_b32 v209, a105\n" \
"v_mfma_f32_16x16x32_bf16 v[168:171], a[180:183], a[156:159], v[168:171]\n" \
"ds_read_b64_tr_b16 a[172:173], v35 offset:0\n" \
"v_accvgpr_read_b32 v210, a106\n" \
"v_mfma_f32_16x16x32_bf16 v[168:171], a[244:247], a[220:223], v[168:171]\n" \
"ds_read_b64_tr_b16 a[174:175], v35 offset:512\n" \
"v_accvgpr_read_b32 v211, a107\n" \
"v_mfma_f32_16x16x32_bf16 v[164:167], a[176:179], a[156:159], v[164:167]\n" \
"v_accvgpr_read_b32 v180, a120\n" \
"v_accvgpr_read_b32 v181, a121\n" \
"v_mfma_f32_16x16x32_bf16 v[164:167], a[240:243], a[220:223], v[164:167]\n" \
"ds_read_b128 a[156:159], v28 offset:14336\n" \
"v_accvgpr_read_b32 v182, a122\n" \
"v_mfma_f32_16x16x32_bf16 v[84:87], a[176:179], a[136:139], v[84:87]\n" \
"ds_read_b128 a[220:223], v29 offset:14336\n" \
"v_accvgpr_read_b32 v183, a123\n" \
"v_mfma_f32_16x16x32_bf16 v[84:87], a[240:243], a[200:203], v[84:87]\n" \
"s_add_u32 m0, s31, 0x8000\n" \
"s_mov_b32 s84, 0x280000\n" \
"v_mfma_f32_16x16x32_bf16 v[88:91], a[180:183], a[136:139], v[88:91]\n" \
"buffer_load_dwordx4 v26, s[68:71], s84 offen offset:0 lds\n" \
"v_mfma_f32_16x16x32_bf16 v[88:91], a[244:247], a[200:203], v[88:91]\n" \
"s_addk_i32 s84, 0x7c00\n" \
"s_nop 0\n" \
"v_mfma_f32_16x16x32_bf16 v[92:95], a[184:187], a[136:139], v[92:95]\n" \
"buffer_load_dwordx4 v26, s[68:71], s84 offen offset:1024 lds\n" \
"v_mfma_f32_16x16x32_bf16 v[92:95], a[248:251], a[200:203], v[92:95]\n" \
"ds_read_b64_tr_b16 a[236:237], v35 offset:4096\n" \
"v_cvt_pk_bf16_f32 v208, v208, v209\n" \
"v_mfma_f32_16x16x32_bf16 v[96:99], a[188:191], a[136:139], v[96:99]\n" \
"ds_read_b64_tr_b16 a[238:239], v35 offset:4608\n" \
"v_cvt_pk_bf16_f32 v209, v210, v211\n" \
"v_mfma_f32_16x16x32_bf16 v[96:99], a[252:255], a[200:203], v[96:99]\n" \
"ds_read_b128 a[136:139], v28 offset:4096\n" \
"v_cvt_pk_bf16_f32 v210, v180, v181\n" \
"v_mfma_f32_16x16x32_bf16 v[112:115], a[188:191], a[140:143], v[112:115]\n" \
"ds_read_b128 a[200:203], v29 offset:4096\n" \
"v_cvt_pk_bf16_f32 v211, v182, v183\n" \
"v_mfma_f32_16x16x32_bf16 v[112:115], a[252:255], a[204:207], v[112:115]\n" \
"s_addk_i32 s84, 0x7c00\n" \
"v_mfma_f32_16x16x32_bf16 v[108:111], a[184:187], a[140:143], v[108:111]\n" \
"buffer_load_dwordx4 v27, s[68:71], s84 offen offset:2048 lds\n" \
"v_mfma_f32_16x16x32_bf16 v[108:111], a[248:251], a[204:207], v[108:111]\n" \
"s_addk_i32 s84, 0x7c00\n" \
"v_mfma_f32_16x16x32_bf16 v[104:107], a[180:183], a[140:143], v[104:107]\n" \
"buffer_load_dwordx4 v27, s[68:71], s84 offen offset:3072 lds\n" \
"v_mfma_f32_16x16x32_bf16 v[104:107], a[244:247], a[204:207], v[104:107]\n" \
"ds_read_b64_tr_b16 a[168:169], v34 offset:0\n" \
"v_accvgpr_read_b32 v204, a100\n" \
"v_mfma_f32_16x16x32_bf16 v[100:103], a[176:179], a[140:143], v[100:103]\n" \
"ds_read_b64_tr_b16 a[170:171], v34 offset:512\n" \
"v_accvgpr_read_b32 v205, a101\n" \
"v_mfma_f32_16x16x32_bf16 v[100:103], a[240:243], a[204:207], v[100:103]\n" \
"ds_read_b128 a[140:143], v28 offset:6144\n" \
"v_accvgpr_read_b32 v206, a102\n" \
"v_mfma_f32_16x16x32_bf16 v[52:55], a[176:179], a[128:131], v[52:55]\n" \
"ds_read_b128 a[204:207], v29 offset:6144\n" \
"v_accvgpr_read_b32 v207, a103\n" \
"v_mfma_f32_16x16x32_bf16 v[52:55], a[240:243], a[192:195], v[52:55]\n" \
"s_add_u32 m0, s30, 0x9000\n" \
"s_mov_b32 s84, 0x9280\n" \
"v_mfma_f32_16x16x32_bf16 v[56:59], a[180:183], a[128:131], v[56:59]\n" \
"buffer_load_dwordx4 v24, s[76:79], s84 offen offset:0 lds\n" \
"v_mfma_f32_16x16x32_bf16 v[56:59], a[244:247], a[192:195], v[56:59]\n" \
"s_addk_i32 s84, 0x2000\n" \
"s_nop 0\n" \
"v_mfma_f32_16x16x32_bf16 v[60:63], a[184:187], a[128:131], v[60:63]\n" \
"buffer_load_dwordx4 v25, s[76:79], s84 offen offset:1024 lds\n" \
"v_mfma_f32_16x16x32_bf16 v[60:63], a[248:251], a[192:195], v[60:63]\n" \
"ds_read_b64_tr_b16 a[232:233], v34 offset:4096\n" \
"v_accvgpr_read_b32 v180, a116\n" \
"v_mfma_f32_16x16x32_bf16 v[64:67], a[188:191], a[128:131], v[64:67]\n" \
"ds_read_b64_tr_b16 a[234:235], v34 offset:4608\n" \
"v_accvgpr_read_b32 v181, a117\n" \
"v_mfma_f32_16x16x32_bf16 v[64:67], a[252:255], a[192:195], v[64:67]\n" \
"ds_read_b128 a[128:131], v28 offset:0\n" \
"v_accvgpr_read_b32 v182, a118\n" \
"v_mfma_f32_16x16x32_bf16 v[80:83], a[188:191], a[132:135], v[80:83]\n" \
"ds_read_b128 a[192:195], v29 offset:0\n" \
"v_accvgpr_read_b32 v183, a119\n" \
"v_mfma_f32_16x16x32_bf16 v[80:83], a[252:255], a[196:199], v[80:83]\n" \
"v_cvt_pk_bf16_f32 v204, v204, v205\n" \
"v_cvt_pk_bf16_f32 v205, v206, v207\n" \
"v_mfma_f32_16x16x32_bf16 v[76:79], a[184:187], a[132:135], v[76:79]\n" \
"v_cvt_pk_bf16_f32 v206, v180, v181\n" \
"v_cvt_pk_bf16_f32 v207, v182, v183\n" \
"v_mfma_f32_16x16x32_bf16 v[76:79], a[248:251], a[196:199], v[76:79]\n" \
"v_accvgpr_read_b32 v200, a96\n" \
"v_accvgpr_read_b32 v201, a97\n" \
"v_mfma_f32_16x16x32_bf16 v[72:75], a[180:183], a[132:135], v[72:75]\n" \
"v_accvgpr_read_b32 v202, a98\n" \
"v_accvgpr_read_b32 v203, a99\n" \
"v_mfma_f32_16x16x32_bf16 v[72:75], a[244:247], a[196:199], v[72:75]\n" \
"v_accvgpr_read_b32 v180, a112\n" \
"v_accvgpr_read_b32 v181, a113\n" \
"v_mfma_f32_16x16x32_bf16 v[68:71], a[176:179], a[132:135], v[68:71]\n" \
"v_accvgpr_read_b32 v182, a114\n" \
"v_accvgpr_read_b32 v183, a115\n" \
"v_mfma_f32_16x16x32_bf16 v[68:71], a[240:243], a[196:199], v[68:71]\n" \
"ds_read_b128 a[132:135], v28 offset:2048\n" \
"ds_read_b128 a[196:199], v29 offset:2048\n" \
"s_waitcnt lgkmcnt(15)\n" \
"v_mfma_f32_16x16x32_bf16 a[0:3], a[160:163], a[144:147], a[0:3]\n" \
"s_addk_i32 s84, 0x2000\n" \
"v_mfma_f32_16x16x32_bf16 a[0:3], a[224:227], a[208:211], a[0:3]\n" \
"buffer_load_dwordx4 v24, s[76:79], s84 offen offset:2048 lds\n" \
"v_mfma_f32_16x16x32_bf16 a[4:7], a[164:167], a[144:147], a[4:7]\n" \
"ds_read_b64_tr_b16 a[176:177], v36 offset:0\n" \
"v_cvt_pk_bf16_f32 v200, v200, v201\n" \
"v_mfma_f32_16x16x32_bf16 a[4:7], a[228:231], a[208:211], a[4:7]\n" \
"s_addk_i32 s84, 0x2000\n" \
"v_mfma_f32_16x16x32_bf16 a[12:15], a[172:175], a[144:147], a[12:15]\n" \
"buffer_load_dwordx4 v25, s[76:79], s84 offen offset:3072 lds\n" \
"s_waitcnt lgkmcnt(13)\n" \
"v_mfma_f32_16x16x32_bf16 a[12:15], a[236:239], a[208:211], a[12:15]\n" \
"ds_read_b64_tr_b16 a[178:179], v36 offset:1024\n" \
"v_cvt_pk_bf16_f32 v201, v202, v203\n" \
"s_waitcnt lgkmcnt(10)\n" \
"v_mfma_f32_16x16x32_bf16 a[8:11], a[144:147], a[168:171], a[8:11]\n" \
"ds_read_b64_tr_b16 a[240:241], v36 offset:8192\n" \
"v_cvt_pk_bf16_f32 v202, v180, v181\n" \
"s_waitcnt lgkmcnt(7)\n" \
"v_mfma_f32_16x16x32_bf16 a[8:11], a[208:211], a[232:235], a[8:11]\n" \
"ds_read_b64_tr_b16 a[242:243], v36 offset:9216\n" \
"v_cvt_pk_bf16_f32 v203, v182, v183\n" \
"v_mfma_f32_16x16x32_bf16 a[24:27], a[148:151], a[168:171], a[24:27]\n" \
"ds_read_b64_tr_b16 a[180:181], v37 offset:0\n" \
"v_accvgpr_read_b32 v196, a76\n" \
"v_mfma_f32_16x16x32_bf16 a[24:27], a[212:215], a[232:235], a[24:27]\n" \
"ds_read_b64_tr_b16 a[182:183], v37 offset:1024\n" \
"v_accvgpr_read_b32 v197, a77\n" \
"v_mfma_f32_16x16x32_bf16 a[28:31], a[172:175], a[148:151], a[28:31]\n" \
"ds_read_b64_tr_b16 a[244:245], v37 offset:8192\n" \
"v_accvgpr_read_b32 v198, a78\n" \
"v_mfma_f32_16x16x32_bf16 a[28:31], a[236:239], a[212:215], a[28:31]\n" \
"ds_read_b64_tr_b16 a[246:247], v37 offset:9216\n" \
"v_accvgpr_read_b32 v199, a79\n" \
"v_mfma_f32_16x16x32_bf16 a[20:23], a[164:167], a[148:151], a[20:23]\n" \
"v_accvgpr_read_b32 v180, a92\n" \
"v_accvgpr_read_b32 v181, a93\n" \
"v_mfma_f32_16x16x32_bf16 a[20:23], a[228:231], a[212:215], a[20:23]\n" \
"s_add_u32 s89, s42, 0x1000\n" \
"buffer_store_dwordx4 v[232:235], v20, s[80:83], s89 offen offset:1024 sc1\n" \
"v_mfma_f32_16x16x32_bf16 a[16:19], a[160:163], a[148:151], a[16:19]\n" \
"ds_read_b64_tr_b16 a[184:185], v38 offset:0\n" \
"v_accvgpr_read_b32 v182, a94\n" \
"v_mfma_f32_16x16x32_bf16 a[16:19], a[224:227], a[212:215], a[16:19]\n" \
"ds_read_b64_tr_b16 a[186:187], v38 offset:1024\n" \
"v_accvgpr_read_b32 v183, a95\n" \
"v_mfma_f32_16x16x32_bf16 a[32:35], a[160:163], a[152:155], a[32:35]\n" \
"ds_read_b64_tr_b16 a[248:249], v38 offset:8192\n" \
"v_cvt_pk_bf16_f32 v196, v196, v197\n" \
"v_mfma_f32_16x16x32_bf16 a[32:35], a[224:227], a[216:219], a[32:35]\n" \
"ds_read_b64_tr_b16 a[250:251], v38 offset:9216\n" \
"v_cvt_pk_bf16_f32 v197, v198, v199\n" \
"v_mfma_f32_16x16x32_bf16 a[36:39], a[164:167], a[152:155], a[36:39]\n" \
"v_cvt_pk_bf16_f32 v198, v180, v181\n" \
"v_cvt_pk_bf16_f32 v199, v182, v183\n" \
"v_mfma_f32_16x16x32_bf16 a[36:39], a[228:231], a[216:219], a[36:39]\n" \
"s_add_u32 s88, s42, 0x1000\n" \
"buffer_store_dwordx4 v[236:239], v20, s[80:83], s88 offen offset:0 sc1\n" \
"v_mfma_f32_16x16x32_bf16 a[44:47], a[172:175], a[152:155], a[44:47]\n" \
"ds_read_b64_tr_b16 a[188:189], v39 offset:0\n" \
"v_accvgpr_read_b32 v192, a72\n" \
"v_mfma_f32_16x16x32_bf16 a[44:47], a[236:239], a[216:219], a[44:47]\n" \
"ds_read_b64_tr_b16 a[190:191], v39 offset:1024\n" \
"v_accvgpr_read_b32 v193, a73\n" \
"v_mfma_f32_16x16x32_bf16 a[40:43], a[152:155], a[168:171], a[40:43]\n" \
"ds_read_b64_tr_b16 a[252:253], v39 offset:8192\n" \
"v_accvgpr_read_b32 v194, a74\n" \
"v_mfma_f32_16x16x32_bf16 a[40:43], a[216:219], a[232:235], a[40:43]\n" \
"v_accvgpr_read_b32 v195, a75\n" \
"v_accvgpr_read_b32 v180, a88\n" \
"v_mfma_f32_16x16x32_bf16 a[56:59], a[156:159], a[168:171], a[56:59]\n" \
"ds_read_b64_tr_b16 a[254:255], v39 offset:9216\n" \
"v_accvgpr_read_b32 v181, a89\n" \
"v_mfma_f32_16x16x32_bf16 a[56:59], a[220:223], a[232:235], a[56:59]\n" \
"v_accvgpr_read_b32 v182, a90\n" \
"v_accvgpr_read_b32 v183, a91\n" \
"v_mfma_f32_16x16x32_bf16 a[60:63], a[172:175], a[156:159], a[60:63]\n" \
"s_add_u32 s89, s42, 0x3000\n" \
"buffer_store_dwordx4 v[240:243], v20, s[80:83], s89 offen offset:3072 sc1\n" \
"v_mfma_f32_16x16x32_bf16 a[60:63], a[236:239], a[220:223], a[60:63]\n" \
"v_cvt_pk_bf16_f32 v192, v192, v193\n" \
"v_cvt_pk_bf16_f32 v193, v194, v195\n" \
"v_mfma_f32_16x16x32_bf16 a[52:55], a[164:167], a[156:159], a[52:55]\n" \
"v_cvt_pk_bf16_f32 v194, v180, v181\n" \
"v_cvt_pk_bf16_f32 v195, v182, v183\n" \
"v_mfma_f32_16x16x32_bf16 a[52:55], a[228:231], a[220:223], a[52:55]\n" \
"s_lshl_b32 s14, s4, 2\n" \
"s_or_b32 s8, s14, s8\n" \
"v_mfma_f32_16x16x32_bf16 a[48:51], a[160:163], a[156:159], a[48:51]\n" \
"s_mul_i32 s8, s8, 0x90000\n" \
"s_lshl_b32 s14, s3, 14\n" \
"v_mfma_f32_16x16x32_bf16 a[48:51], a[224:227], a[220:223], a[48:51]\n" \
"s_add_i32 s8, s8, s14\n" \
"s_lshl_b32 s9, s9, 4\n" \
"v_mfma_f32_16x16x32_bf16 a[64:67], a[164:167], a[140:143], a[64:67]\n" \
"s_lshl_b32 s14, s4, 7\n" \
"s_or_b32 s18, s9, s14\n" \
"v_mfma_f32_16x16x32_bf16 a[64:67], a[228:231], a[204:207], a[64:67]\n" \
"s_sub_i32 s9, s9, s14\n" \
"s_mulk_i32 s18, 0x480\n" \
"v_mfma_f32_16x16x32_bf16 a[80:83], a[172:175], a[140:143], a[80:83]\n" \
"s_mulk_i32 s9, 0x480\n" \
"s_or_b32 s14, s18, s12\n" \
"v_mfma_f32_16x16x32_bf16 a[80:83], a[236:239], a[204:207], a[80:83]\n" \
"s_or_b32 s9, s9, s12\n" \
"s_and_b32 s12, s2, 1\n" \
"v_mfma_f32_16x16x32_bf16 a[68:71], a[140:143], a[168:171], a[68:71]\n" \
"s_cmp_lg_u32 s12, 0\n" \
"s_cselect_b32 s13, s16, s13\n" \
"v_mfma_f32_16x16x32_bf16 a[68:71], a[204:207], a[232:235], a[68:71]\n" \
"s_waitcnt vmcnt(3) lgkmcnt(0)\n" \
"s_barrier\n" \
"v_mfma_f32_16x16x32_bf16 v[116:119], a[176:179], a[144:147], v[116:119]\n" \
"s_add_u32 m0, s40, 0x0\n" \
"s_add_u32 s85, s39, 0xc000\n" \
"v_mfma_f32_16x16x32_bf16 v[116:119], a[240:243], a[208:211], v[116:119]\n" \
"buffer_load_dwordx4 v20, s[80:83], s85 offen sc1 lds\n" \
"v_mfma_f32_16x16x32_bf16 v[120:123], a[180:183], a[144:147], v[120:123]\n" \
"ds_read_b64_tr_b16 a[160:161], v32 offset:32768\n" \
"s_bfe_u32 s16, s2, 0x20001\n" \
"v_mfma_f32_16x16x32_bf16 v[120:123], a[244:247], a[208:211], v[120:123]\n" \
"ds_read_b64_tr_b16 a[162:163], v32 offset:33280\n" \
"s_addk_i32 s13, 0x4000\n" \
"v_mfma_f32_16x16x32_bf16 v[124:127], a[184:187], a[144:147], v[124:127]\n" \
"s_add_u32 m0, s40, 0x1000\n" \
"s_add_u32 s85, s39, 0xd000\n" \
"v_mfma_f32_16x16x32_bf16 v[124:127], a[248:251], a[208:211], v[124:127]\n" \
"buffer_load_dwordx4 v20, s[80:83], s85 offen sc1 lds\n" \
"v_mfma_f32_16x16x32_bf16 v[128:131], a[188:191], a[144:147], v[128:131]\n" \
"ds_read_b64_tr_b16 a[224:225], v32 offset:36864\n" \
"s_xor_b32 s16, s16, s3\n" \
"v_mfma_f32_16x16x32_bf16 v[128:131], a[252:255], a[208:211], v[128:131]\n" \
"ds_read_b128 a[144:147], v28 offset:40960\n" \
"v_mfma_f32_16x16x32_bf16 v[144:147], a[188:191], a[148:151], v[144:147]\n" \
".fill (L_pb + 69344 - .) / 4, 4, 0xBF800000\n" \
"L_pp17:\n" \
"s_cbranch_execz L_pret\n" \
"ds_read_b128 a[208:211], v29 offset:40960\n" \
"v_mfma_f32_16x16x32_bf16 v[144:147], a[252:255], a[212:215], v[144:147]\n" \
"s_add_u32 m0, s40, 0x3000\n" \
"s_movk_i32 s84, 0x300\n" \
"v_mfma_f32_16x16x32_bf16 v[140:143], a[184:187], a[148:151], v[140:143]\n" \
"buffer_load_dwordx4 v21, s[76:79], s84 offen offset:0 lds\n" \
"v_mfma_f32_16x16x32_bf16 v[140:143], a[248:251], a[212:215], v[140:143]\n" \
"ds_read_b64_tr_b16 a[226:227], v32 offset:37376\n" \
"v_mfma_f32_16x16x32_bf16 v[136:139], a[180:183], a[148:151], v[136:139]\n" \
"s_add_u32 m0, s30, 0x0\n" \
"s_movk_i32 s84, 0x300\n" \
"v_mfma_f32_16x16x32_bf16 v[136:139], a[244:247], a[212:215], v[136:139]\n" \
"buffer_load_dwordx4 v24, s[76:79], s84 offen offset:0 lds\n" \
"v_mfma_f32_16x16x32_bf16 v[132:135], a[176:179], a[148:151], v[132:135]\n" \
"ds_read_b64_tr_b16 a[164:165], v33 offset:32768\n" \
"v_mfma_f32_16x16x32_bf16 v[132:135], a[240:243], a[212:215], v[132:135]\n" \
"ds_read_b128 a[148:151], v28 offset:43008\n" \
"v_mfma_f32_16x16x32_bf16 v[148:151], a[176:179], a[152:155], v[148:151]\n" \
"ds_read_b128 a[212:215], v29 offset:43008\n" \
"v_mfma_f32_16x16x32_bf16 v[148:151], a[240:243], a[216:219], v[148:151]\n" \
"s_movk_i32 s84, 0x2300\n" \
"v_mfma_f32_16x16x32_bf16 v[152:155], a[180:183], a[152:155], v[152:155]\n" \
"buffer_load_dwordx4 v25, s[76:79], s84 offen offset:1024 lds\n" \
"v_mfma_f32_16x16x32_bf16 v[152:155], a[244:247], a[216:219], v[152:155]\n" \
"ds_read_b64_tr_b16 a[166:167], v33 offset:33280\n" \
"v_mfma_f32_16x16x32_bf16 v[156:159], a[184:187], a[152:155], v[156:159]\n" \
"ds_read_b64_tr_b16 a[228:229], v33 offset:36864\n" \
"v_mfma_f32_16x16x32_bf16 v[156:159], a[248:251], a[216:219], v[156:159]\n" \
"s_movk_i32 s84, 0x4300\n" \
"v_mfma_f32_16x16x32_bf16 v[160:163], a[188:191], a[152:155], v[160:163]\n" \
"buffer_load_dwordx4 v24, s[76:79], s84 offen offset:2048 lds\n" \
"v_mfma_f32_16x16x32_bf16 v[160:163], a[252:255], a[216:219], v[160:163]\n" \
"ds_read_b128 a[152:155], v28 offset:45056\n" \
"v_mfma_f32_16x16x32_bf16 v[176:179], a[188:191], a[156:159], v[176:179]\n" \
"ds_read_b128 a[216:219], v29 offset:45056\n" \
"v_mfma_f32_16x16x32_bf16 v[176:179], a[252:255], a[220:223], v[176:179]\n" \
"s_movk_i32 s84, 0x6300\n" \
"v_mfma_f32_16x16x32_bf16 v[172:175], a[184:187], a[156:159], v[172:175]\n" \
"buffer_load_dwordx4 v25, s[76:79], s84 offen offset:3072 lds\n" \
"v_mfma_f32_16x16x32_bf16 v[172:175], a[248:251], a[220:223], v[172:175]\n" \
"ds_read_b64_tr_b16 a[230:231], v33 offset:37376\n" \
"v_mfma_f32_16x16x32_bf16 v[168:171], a[180:183], a[156:159], v[168:171]\n" \
"ds_read_b64_tr_b16 a[172:173], v35 offset:32768\n" \
"v_mfma_f32_16x16x32_bf16 v[168:171], a[244:247], a[220:223], v[168:171]\n" \
"ds_read_b64_tr_b16 a[174:175], v35 offset:33280\n" \
"v_mfma_f32_16x16x32_bf16 v[164:167], a[176:179], a[156:159], v[164:167]\n" \
"s_cmp_lg_u32 s16, 0\n" \
"s_cselect_b32 s13, s7, s13\n" \
"v_mfma_f32_16x16x32_bf16 v[164:167], a[240:243], a[220:223], v[164:167]\n" \
"ds_read_b128 a[156:159], v28 offset:47104\n" \
"s_lshl_b32 s6, s6, 16\n" \
"v_mfma_f32_16x16x32_bf16 v[84:87], a[176:179], a[136:139], v[84:87]\n" \
"ds_read_b128 a[220:223], v29 offset:47104\n" \
"v_writelane_b32 v11, s13, 39\n" \
"v_mfma_f32_16x16x32_bf16 v[84:87], a[240:243], a[200:203], v[84:87]\n" \
"s_add_u32 m0, s31, 0x0\n" \
"s_mov_b32 s84, 0x300000\n" \
"v_mfma_f32_16x16x32_bf16 v[88:91], a[180:183], a[136:139], v[88:91]\n" \
"buffer_load_dwordx4 v26, s[68:71], s84 offen offset:0 lds\n" \
"v_mfma_f32_16x16x32_bf16 v[88:91], a[244:247], a[200:203], v[88:91]\n" \
"s_addk_i32 s84, 0x7c00\n" \
"v_mfma_f32_16x16x32_bf16 v[92:95], a[184:187], a[136:139], v[92:95]\n" \
"buffer_load_dwordx4 v26, s[68:71], s84 offen offset:1024 lds\n" \
"v_mfma_f32_16x16x32_bf16 v[92:95], a[248:251], a[200:203], v[92:95]\n" \
"ds_read_b64_tr_b16 a[236:237], v35 offset:36864\n" \
"s_add_i32 s13, s8, 0x4240000\n" \
"v_mfma_f32_16x16x32_bf16 v[96:99], a[188:191], a[136:139], v[96:99]\n" \
"ds_read_b64_tr_b16 a[238:239], v35 offset:37376\n" \
"s_add_i32 s6, s13, s6\n" \
"v_mfma_f32_16x16x32_bf16 v[96:99], a[252:255], a[200:203], v[96:99]\n" \
"ds_read_b128 a[136:139], v28 offset:36864\n" \
"v_writelane_b32 v11, s6, 40\n" \
"v_mfma_f32_16x16x32_bf16 v[112:115], a[188:191], a[140:143], v[112:115]\n" \
"ds_read_b128 a[200:203], v29 offset:36864\n" \
"s_add_i32 s8, s8, 0x42c0000\n" \
"v_mfma_f32_16x16x32_bf16 v[112:115], a[252:255], a[204:207], v[112:115]\n" \
"s_addk_i32 s84, 0x7c00\n" \
"v_mfma_f32_16x16x32_bf16 v[108:111], a[184:187], a[140:143], v[108:111]\n" \
"buffer_load_dwordx4 v27, s[68:71], s84 offen offset:2048 lds\n" \
"v_mfma_f32_16x16x32_bf16 v[108:111], a[248:251], a[204:207], v[108:111]\n" \
"s_addk_i32 s84, 0x7c00\n" \
"v_mfma_f32_16x16x32_bf16 v[104:107], a[180:183], a[140:143], v[104:107]\n" \
"buffer_load_dwordx4 v27, s[68:71], s84 offen offset:3072 lds\n" \
"v_mfma_f32_16x16x32_bf16 v[104:107], a[244:247], a[204:207], v[104:107]\n" \
"ds_read_b64_tr_b16 a[168:169], v34 offset:32768\n" \
"s_lshl_b32 s6, s12, 10\n" \
"v_mfma_f32_16x16x32_bf16 v[100:103], a[176:179], a[140:143], v[100:103]\n" \
"ds_read_b64_tr_b16 a[170:171], v34 offset:33280\n" \
"s_or_b32 s6, s8, s6\n" \
"v_mfma_f32_16x16x32_bf16 v[100:103], a[240:243], a[204:207], v[100:103]\n" \
"ds_read_b128 a[140:143], v28 offset:38912\n" \
"v_mfma_f32_16x16x32_bf16 v[52:55], a[176:179], a[128:131], v[52:55]\n" \
"ds_read_b128 a[204:207], v29 offset:38912\n" \
"v_mfma_f32_16x16x32_bf16 v[52:55], a[240:243], a[192:195], v[52:55]\n" \
"s_add_u32 m0, s30, 0x1000\n" \
"s_mov_b32 s84, 0x9300\n" \
"v_mfma_f32_16x16x32_bf16 v[56:59], a[180:183], a[128:131], v[56:59]\n" \
"buffer_load_dwordx4 v24, s[76:79], s84 offen offset:0 lds\n" \
"v_mfma_f32_16x16x32_bf16 v[56:59], a[244:247], a[192:195], v[56:59]\n" \
"s_addk_i32 s84, 0x2000\n" \
"v_mfma_f32_16x16x32_bf16 v[60:63], a[184:187], a[128:131], v[60:63]\n" \
"buffer_load_dwordx4 v25, s[76:79], s84 offen offset:1024 lds\n" \
"v_mfma_f32_16x16x32_bf16 v[60:63], a[248:251], a[192:195], v[60:63]\n" \
"ds_read_b64_tr_b16 a[232:233], v34 offset:36864\n" \
"v_mfma_f32_16x16x32_bf16 v[64:67], a[188:191], a[128:131], v[64:67]\n" \
"ds_read_b64_tr_b16 a[234:235], v34 offset:37376\n" \
"v_mfma_f32_16x16x32_bf16 v[64:67], a[252:255], a[192:195], v[64:67]\n" \
"ds_read_b128 a[128:131], v28 offset:32768\n" \
"v_mfma_f32_16x16x32_bf16 v[80:83], a[188:191], a[132:135], v[80:83]\n" \
"ds_read_b128 a[192:195], v29 offset:32768\n" \
"v_mfma_f32_16x16x32_bf16 v[80:83], a[252:255], a[196:199], v[80:83]\n" \
"s_cmp_lg_u32 s16, 0\n" \
"s_cselect_b32 s6, s7, s6\n" \
"v_mfma_f32_16x16x32_bf16 v[76:79], a[184:187], a[132:135], v[76:79]\n" \
"s_mulk_i32 s4, 0x4800\n" \
"v_writelane_b32 v11, s6, 41\n" \
"v_mfma_f32_16x16x32_bf16 v[76:79], a[248:251], a[196:199], v[76:79]\n" \
"s_add_i32 s6, s14, s10\n" \
"v_writelane_b32 v11, s6, 42\n" \
"v_mfma_f32_16x16x32_bf16 v[72:75], a[180:183], a[132:135], v[72:75]\n" \
"s_add_i32 s6, s9, 0x21c000\n" \
"s_add_i32 s9, s6, s10\n" \
"v_mfma_f32_16x16x32_bf16 v[72:75], a[244:247], a[196:199], v[72:75]\n" \
"s_cmp_lg_u32 s12, 0\n" \
"s_cselect_b32 s6, s6, s14\n" \
"v_mfma_f32_16x16x32_bf16 v[68:71], a[176:179], a[132:135], v[68:71]\n" \
"s_addk_i32 s6, 0x100\n" \
"v_writelane_b32 v11, s9, 43\n" \
"v_mfma_f32_16x16x32_bf16 v[68:71], a[240:243], a[196:199], v[68:71]\n" \
"ds_read_b128 a[132:135], v28 offset:34816\n" \
"ds_read_b128 a[196:199], v29 offset:34816\n" \
"s_waitcnt lgkmcnt(15)\n" \
"v_mfma_f32_16x16x32_bf16 a[0:3], a[160:163], a[144:147], a[0:3]\n" \
"s_addk_i32 s84, 0x2000\n" \
"v_mfma_f32_16x16x32_bf16 a[0:3], a[224:227], a[208:211], a[0:3]\n" \
"buffer_load_dwordx4 v24, s[76:79], s84 offen offset:2048 lds\n" \
"v_mfma_f32_16x16x32_bf16 a[4:7], a[164:167], a[144:147], a[4:7]\n" \
"ds_read_b64_tr_b16 a[176:177], v36 offset:32768\n" \
"v_mfma_f32_16x16x32_bf16 a[4:7], a[228:231], a[208:211], a[4:7]\n" \
"s_addk_i32 s84, 0x2000\n" \
"v_mfma_f32_16x16x32_bf16 a[12:15], a[172:175], a[144:147], a[12:15]\n" \
"buffer_load_dwordx4 v25, s[76:79], s84 offen offset:3072 lds\n" \
"s_waitcnt lgkmcnt(13)\n" \
"v_mfma_f32_16x16x32_bf16 a[12:15], a[236:239], a[208:211], a[12:15]\n" \
"ds_read_b64_tr_b16 a[178:179], v36 offset:33792\n" \
"s_waitcnt lgkmcnt(10)\n" \
"v_mfma_f32_16x16x32_bf16 a[8:11], a[144:147], a[168:171], a[8:11]\n" \
"ds_read_b64_tr_b16 a[240:241], v36 offset:40960\n" \
"s_waitcnt lgkmcnt(7)\n" \
"v_mfma_f32_16x16x32_bf16 a[8:11], a[208:211], a[232:235], a[8:11]\n" \
"ds_read_b64_tr_b16 a[242:243], v36 offset:41984\n" \
"v_mfma_f32_16x16x32_bf16 a[24:27], a[148:151], a[168:171], a[24:27]\n" \
"ds_read_b64_tr_b16 a[180:181], v37 offset:32768\n" \
"v_mfma_f32_16x16x32_bf16 a[24:27], a[212:215], a[232:235], a[24:27]\n" \
"ds_read_b64_tr_b16 a[182:183], v37 offset:33792\n" \
"v_mfma_f32_16x16x32_bf16 a[28:31], a[172:175], a[148:151], a[28:31]\n" \
"ds_read_b64_tr_b16 a[244:245], v37 offset:40960\n" \
"v_mfma_f32_16x16x32_bf16 a[28:31], a[236:239], a[212:215], a[28:31]\n" \
"ds_read_b64_tr_b16 a[246:247], v37 offset:41984\n" \
"v_mfma_f32_16x16x32_bf16 a[20:23], a[164:167], a[148:151], a[20:23]\n" \
"s_cmp_lg_u32 s16, 0\n" \
"s_cselect_b32 s6, s7, s6\n" \
"v_mfma_f32_16x16x32_bf16 a[20:23], a[228:231], a[212:215], a[20:23]\n" \
"s_add_u32 s88, s42, 0x3000\n" \
"buffer_store_dwordx4 v[244:247], v20, s[80:83], s88 offen offset:2048 sc1\n" \
"v_mfma_f32_16x16x32_bf16 a[16:19], a[160:163], a[148:151], a[16:19]\n" \
"ds_read_b64_tr_b16 a[184:185], v38 offset:32768\n" \
"v_writelane_b32 v11, s6, 44\n" \
"v_mfma_f32_16x16x32_bf16 a[16:19], a[224:227], a[212:215], a[16:19]\n" \
"ds_read_b64_tr_b16 a[186:187], v38 offset:33792\n" \
"s_or_b32 s6, s8, s15\n" \
"v_mfma_f32_16x16x32_bf16 a[32:35], a[160:163], a[152:155], a[32:35]\n" \
"ds_read_b64_tr_b16 a[248:249], v38 offset:40960\n" \
"v_writelane_b32 v11, s6, 52\n" \
"v_mfma_f32_16x16x32_bf16 a[32:35], a[224:227], a[216:219], a[32:35]\n" \
"ds_read_b64_tr_b16 a[250:251], v38 offset:41984\n" \
"s_lshl_b32 s6, s3, 17\n" \
"v_mfma_f32_16x16x32_bf16 a[36:39], a[164:167], a[152:155], a[36:39]\n" \
"s_add_i32 s6, s13, s6\n" \
"s_or_b32 s5, s6, s5\n" \
"v_mfma_f32_16x16x32_bf16 a[36:39], a[228:231], a[216:219], a[36:39]\n" \
"s_add_u32 s89, s42, 0x3000\n" \
"buffer_store_dwordx4 v[184:187], v20, s[80:83], s89 offen offset:1024 sc1\n" \
"v_mfma_f32_16x16x32_bf16 a[44:47], a[172:175], a[152:155], a[44:47]\n" \
"ds_read_b64_tr_b16 a[188:189], v39 offset:32768\n" \
"s_or_b32 s5, s5, 0x3840\n" \
"v_mfma_f32_16x16x32_bf16 a[44:47], a[236:239], a[216:219], a[44:47]\n" \
"ds_read_b64_tr_b16 a[190:191], v39 offset:33792\n" \
"v_writelane_b32 v11, s5, 6\n" \
"v_mfma_f32_16x16x32_bf16 a[40:43], a[152:155], a[168:171], a[40:43]\n" \
"ds_read_b64_tr_b16 a[252:253], v39 offset:40960\n" \
"s_mov_b32 s5, 0xff0000\n" \
"v_mfma_f32_16x16x32_bf16 a[40:43], a[216:219], a[232:235], a[40:43]\n" \
"s_cmp_lg_u32 s16, 0\n" \
"s_cselect_b32 s5, s17, s5\n" \
"v_mfma_f32_16x16x32_bf16 a[56:59], a[156:159], a[168:171], a[56:59]\n" \
"ds_read_b64_tr_b16 a[254:255], v39 offset:41984\n" \
"v_writelane_b32 v11, s5, 7\n" \
"v_mfma_f32_16x16x32_bf16 a[56:59], a[220:223], a[232:235], a[56:59]\n" \
"s_mul_i32 s5, s3, 0x1200\n" \
"s_add_i32 s4, s5, s4\n" \
"v_mfma_f32_16x16x32_bf16 a[60:63], a[172:175], a[156:159], a[60:63]\n" \
"s_add_u32 s88, s42, 0x3000\n" \
"buffer_store_dwordx4 v[188:191], v20, s[80:83], s88 offen offset:0 sc1\n" \
"v_mfma_f32_16x16x32_bf16 a[60:63], a[236:239], a[220:223], a[60:63]\n" \
"s_add_i32 s4, s4, s11\n" \
"v_writelane_b32 v11, s4, 8\n" \
"v_mfma_f32_16x16x32_bf16 a[52:55], a[164:167], a[156:159], a[52:55]\n" \
"s_lshl_b32 s4, s2, 16\n" \
"s_add_i32 s5, s4, 0x10000\n" \
"v_mfma_f32_16x16x32_bf16 a[52:55], a[228:231], a[220:223], a[52:55]\n" \
"s_and_b32 s5, s5, 0x70000\n" \
"s_add_i32 s5, s13, s5\n" \
"v_mfma_f32_16x16x32_bf16 a[48:51], a[160:163], a[156:159], a[48:51]\n" \
"v_writelane_b32 v11, s5, 45\n" \
"s_add_i32 s5, s4, 0x20000\n" \
"v_mfma_f32_16x16x32_bf16 a[48:51], a[224:227], a[220:223], a[48:51]\n" \
"s_and_b32 s5, s5, 0x70000\n" \
"s_add_i32 s5, s13, s5\n" \
"v_mfma_f32_16x16x32_bf16 a[64:67], a[164:167], a[140:143], a[64:67]\n" \
"s_bitset1_b32 s5, 11\n" \
"v_writelane_b32 v11, s5, 46\n" \
"v_mfma_f32_16x16x32_bf16 a[64:67], a[228:231], a[204:207], a[64:67]\n" \
"s_add_i32 s5, s4, 0x30000\n" \
"s_and_b32 s5, s5, 0x70000\n" \
"v_mfma_f32_16x16x32_bf16 a[80:83], a[172:175], a[140:143], a[80:83]\n" \
"s_add_i32 s5, s13, s5\n" \
"s_bitset1_b32 s5, 12\n" \
"v_mfma_f32_16x16x32_bf16 a[80:83], a[236:239], a[204:207], a[80:83]\n" \
"v_writelane_b32 v11, s5, 47\n" \
"s_and_b32 s5, s4, 0x70000\n" \
"v_mfma_f32_16x16x32_bf16 a[68:71], a[140:143], a[168:171], a[68:71]\n" \
"s_xor_b32 s5, s5, 0x40000\n" \
"s_add_i32 s5, s13, s5\n" \
"v_mfma_f32_16x16x32_bf16 a[68:71], a[204:207], a[232:235], a[68:71]\n" \
"s_waitcnt vmcnt(3) lgkmcnt(0)\n" \
"s_barrier\n" \
"s_nop 0\n" \
"v_mfma_f32_16x16x32_bf16 v[116:119], a[176:179], a[144:147], v[116:119]\n" \
"s_add_u32 m0, s40, 0x8000\n" \
"s_add_u32 s85, s39, 0xe000\n" \
"v_mfma_f32_16x16x32_bf16 v[116:119], a[240:243], a[208:211], v[116:119]\n" \
"buffer_load_dwordx4 v20, s[80:83], s85 offen sc1 lds\n" \
"v_mfma_f32_16x16x32_bf16 v[120:123], a[180:183], a[144:147], v[120:123]\n" \
"ds_read_b64_tr_b16 a[160:161], v32 offset:0\n" \
"s_or_b32 s5, s5, 0x1800\n" \
"v_mfma_f32_16x16x32_bf16 v[120:123], a[244:247], a[208:211], v[120:123]\n" \
"ds_read_b64_tr_b16 a[162:163], v32 offset:512\n" \
"v_writelane_b32 v11, s5, 48\n" \
"v_mfma_f32_16x16x32_bf16 v[124:127], a[184:187], a[144:147], v[124:127]\n" \
"s_add_u32 m0, s40, 0x9000\n" \
"s_add_u32 s85, s39, 0xf000\n" \
"v_mfma_f32_16x16x32_bf16 v[124:127], a[248:251], a[208:211], v[124:127]\n" \
"buffer_load_dwordx4 v20, s[80:83], s85 offen sc1 lds\n" \
"v_mfma_f32_16x16x32_bf16 v[128:131], a[188:191], a[144:147], v[128:131]\n" \
"ds_read_b64_tr_b16 a[224:225], v32 offset:4096\n" \
"s_add_i32 s5, s4, 0x50000\n" \
"v_mfma_f32_16x16x32_bf16 v[128:131], a[252:255], a[208:211], v[128:131]\n" \
"ds_read_b128 a[144:147], v28 offset:8192\n" \
"s_and_b32 s5, s5, 0x70000\n" \
"v_mfma_f32_16x16x32_bf16 v[144:147], a[188:191], a[148:151], v[144:147]\n" \
"ds_read_b128 a[208:211], v29 offset:8192\n" \
"s_add_i32 s5, s13, s5\n" \
"v_mfma_f32_16x16x32_bf16 v[144:147], a[252:255], a[212:215], v[144:147]\n" \
"s_add_u32 m0, s40, 0xb000\n" \
"s_movk_i32 s84, 0x380\n" \
"v_mfma_f32_16x16x32_bf16 v[140:143], a[184:187], a[148:151], v[140:143]\n" \
"buffer_load_dwordx4 v21, s[76:79], s84 offen offset:0 lds\n" \
"v_mfma_f32_16x16x32_bf16 v[140:143], a[248:251], a[212:215], v[140:143]\n" \
"ds_read_b64_tr_b16 a[226:227], v32 offset:4608\n" \
"s_bitset1_b32 s5, 13\n" \
"v_mfma_f32_16x16x32_bf16 v[136:139], a[180:183], a[148:151], v[136:139]\n" \
"s_add_u32 m0, s30, 0x8000\n" \
"s_movk_i32 s84, 0x380\n" \
"v_mfma_f32_16x16x32_bf16 v[136:139], a[244:247], a[212:215], v[136:139]\n" \
"buffer_load_dwordx4 v24, s[76:79], s84 offen offset:0 lds\n" \
"v_mfma_f32_16x16x32_bf16 v[132:135], a[176:179], a[148:151], v[132:135]\n" \
"ds_read_b64_tr_b16 a[164:165], v33 offset:0\n" \
"v_writelane_b32 v11, s5, 49\n" \
"v_mfma_f32_16x16x32_bf16 v[132:135], a[240:243], a[212:215], v[132:135]\n" \
"ds_read_b128 a[148:151], v28 offset:10240\n" \
"s_add_i32 s5, s4, 0x60000\n" \
"v_mfma_f32_16x16x32_bf16 v[148:151], a[176:179], a[152:155], v[148:151]\n" \
"ds_read_b128 a[212:215], v29 offset:10240\n" \
"s_and_b32 s5, s5, 0x70000\n" \
"v_mfma_f32_16x16x32_bf16 v[148:151], a[240:243], a[216:219], v[148:151]\n" \
"s_movk_i32 s84, 0x2380\n" \
"v_mfma_f32_16x16x32_bf16 v[152:155], a[180:183], a[152:155], v[152:155]\n" \
"buffer_load_dwordx4 v25, s[76:79], s84 offen offset:1024 lds\n" \
"v_mfma_f32_16x16x32_bf16 v[152:155], a[244:247], a[216:219], v[152:155]\n" \
"ds_read_b64_tr_b16 a[166:167], v33 offset:512\n" \
"s_add_i32 s4, s4, 0x70000\n" \
"v_mfma_f32_16x16x32_bf16 v[156:159], a[184:187], a[152:155], v[156:159]\n" \
"ds_read_b64_tr_b16 a[228:229], v33 offset:4096\n" \
"s_add_i32 s5, s13, s5\n" \
"v_mfma_f32_16x16x32_bf16 v[156:159], a[248:251], a[216:219], v[156:159]\n" \
"s_movk_i32 s84, 0x4380\n" \
"v_mfma_f32_16x16x32_bf16 v[160:163], a[188:191], a[152:155], v[160:163]\n" \
"buffer_load_dwordx4 v24, s[76:79], s84 offen offset:2048 lds\n" \
"v_mfma_f32_16x16x32_bf16 v[160:163], a[252:255], a[216:219], v[160:163]\n" \
"ds_read_b128 a[152:155], v28 offset:12288\n" \
"s_and_b32 s4, s4, 0x70000\n" \
"v_mfma_f32_16x16x32_bf16 v[176:179], a[188:191], a[156:159], v[176:179]\n" \
"ds_read_b128 a[216:219], v29 offset:12288\n" \
"s_or_b32 s5, s5, 0x2800\n" \
"v_mfma_f32_16x16x32_bf16 v[176:179], a[252:255], a[220:223], v[176:179]\n" \
"s_movk_i32 s84, 0x6380\n" \
"v_mfma_f32_16x16x32_bf16 v[172:175], a[184:187], a[156:159], v[172:175]\n" \
"buffer_load_dwordx4 v25, s[76:79], s84 offen offset:3072 lds\n" \
"v_mfma_f32_16x16x32_bf16 v[172:175], a[248:251], a[220:223], v[172:175]\n" \
"ds_read_b64_tr_b16 a[230:231], v33 offset:4608\n" \
"v_writelane_b32 v11, s5, 50\n" \
"v_mfma_f32_16x16x32_bf16 v[168:171], a[180:183], a[156:159], v[168:171]\n" \
"ds_read_b64_tr_b16 a[172:173], v35 offset:0\n" \
"s_add_i32 s13, s13, s4\n" \
"v_mfma_f32_16x16x32_bf16 v[168:171], a[244:247], a[220:223], v[168:171]\n" \
"ds_read_b64_tr_b16 a[174:175], v35 offset:512\n" \
"s_or_b32 s4, s13, 0x3000\n" \
"v_mfma_f32_16x16x32_bf16 v[164:167], a[176:179], a[156:159], v[164:167]\n" \
"v_writelane_b32 v11, s4, 51\n" \
"s_movk_i32 s11, 0x4000\n" \
"v_mfma_f32_16x16x32_bf16 v[164:167], a[240:243], a[220:223], v[164:167]\n" \
"ds_read_b128 a[156:159], v28 offset:14336\n" \
"s_lshl_b32 s4, s2, 2\n" \
"v_mfma_f32_16x16x32_bf16 v[84:87], a[176:179], a[136:139], v[84:87]\n" \
"ds_read_b128 a[220:223], v29 offset:14336\n" \
"s_and_b32 s4, s4, 28\n" \
"v_mfma_f32_16x16x32_bf16 v[84:87], a[240:243], a[200:203], v[84:87]\n" \
"s_add_u32 m0, s31, 0x8000\n" \
"s_mov_b32 s84, 0x380000\n" \
"v_mfma_f32_16x16x32_bf16 v[88:91], a[180:183], a[136:139], v[88:91]\n" \
"buffer_load_dwordx4 v26, s[68:71], s84 offen offset:0 lds\n" \
"v_mfma_f32_16x16x32_bf16 v[88:91], a[244:247], a[200:203], v[88:91]\n" \
"s_addk_i32 s84, 0x7c00\n" \
"v_mfma_f32_16x16x32_bf16 v[92:95], a[184:187], a[136:139], v[92:95]\n" \
"buffer_load_dwordx4 v26, s[68:71], s84 offen offset:1024 lds\n" \
"v_mfma_f32_16x16x32_bf16 v[92:95], a[248:251], a[200:203], v[92:95]\n" \
"ds_read_b64_tr_b16 a[236:237], v35 offset:4096\n" \
"s_bfe_u32 s5, s2, 0x20003\n" \
"v_mfma_f32_16x16x32_bf16 v[96:99], a[188:191], a[136:139], v[96:99]\n" \
"ds_read_b64_tr_b16 a[238:239], v35 offset:4608\n" \
"s_or_b32 s4, s4, s5\n" \
"v_mfma_f32_16x16x32_bf16 v[96:99], a[252:255], a[200:203], v[96:99]\n" \
"ds_read_b128 a[136:139], v28 offset:4096\n" \
"s_lshr_b32 s5, s2, 5\n" \
"v_mfma_f32_16x16x32_bf16 v[112:115], a[188:191], a[140:143], v[112:115]\n" \
"ds_read_b128 a[200:203], v29 offset:4096\n" \
"s_lshr_b32 s19, s3, 1\n" \
"v_mfma_f32_16x16x32_bf16 v[112:115], a[252:255], a[204:207], v[112:115]\n" \
"s_addk_i32 s84, 0x7c00\n" \
"v_mfma_f32_16x16x32_bf16 v[108:111], a[184:187], a[140:143], v[108:111]\n" \
"buffer_load_dwordx4 v27, s[68:71], s84 offen offset:2048 lds\n" \
"v_mfma_f32_16x16x32_bf16 v[108:111], a[248:251], a[204:207], v[108:111]\n" \
"s_addk_i32 s84, 0x7c00\n" \
"v_mfma_f32_16x16x32_bf16 v[104:107], a[180:183], a[140:143], v[104:107]\n" \
"buffer_load_dwordx4 v27, s[68:71], s84 offen offset:3072 lds\n" \
"v_mfma_f32_16x16x32_bf16 v[104:107], a[244:247], a[204:207], v[104:107]\n" \
"ds_read_b64_tr_b16 a[168:169], v34 offset:0\n" \
"s_lshl_b32 s18, s5, 1\n" \
"v_mfma_f32_16x16x32_bf16 v[100:103], a[176:179], a[140:143], v[100:103]\n" \
"ds_read_b64_tr_b16 a[170:171], v34 offset:512\n" \
"s_lshl_b32 s6, s4, 20\n" \
"v_mfma_f32_16x16x32_bf16 v[100:103], a[240:243], a[204:207], v[100:103]\n" \
"ds_read_b128 a[140:143], v28 offset:6144\n" \
"s_lshl_b32 s7, s4, 8\n" \
"v_mfma_f32_16x16x32_bf16 v[52:55], a[176:179], a[128:131], v[52:55]\n" \
"ds_read_b128 a[204:207], v29 offset:6144\n" \
"s_and_b32 s13, s3, 1\n" \
"v_mfma_f32_16x16x32_bf16 v[52:55], a[240:243], a[192:195], v[52:55]\n" \
"s_add_u32 m0, s30, 0x9000\n" \
"s_mov_b32 s84, 0x9380\n" \
"v_mfma_f32_16x16x32_bf16 v[56:59], a[180:183], a[128:131], v[56:59]\n" \
"buffer_load_dwordx4 v24, s[76:79], s84 offen offset:0 lds\n" \
"v_mfma_f32_16x16x32_bf16 v[56:59], a[244:247], a[192:195], v[56:59]\n" \
"s_addk_i32 s84, 0x2000\n" \
"v_mfma_f32_16x16x32_bf16 v[60:63], a[184:187], a[128:131], v[60:63]\n" \
"buffer_load_dwordx4 v25, s[76:79], s84 offen offset:1024 lds\n" \
"v_mfma_f32_16x16x32_bf16 v[60:63], a[248:251], a[192:195], v[60:63]\n" \
"ds_read_b64_tr_b16 a[232:233], v34 offset:4096\n" \
"s_add_i32 s14, s18, s19\n" \
"v_mfma_f32_16x16x32_bf16 v[64:67], a[188:191], a[128:131], v[64:67]\n" \
"ds_read_b64_tr_b16 a[234:235], v34 offset:4608\n" \
"v_mfma_f32_16x16x32_bf16 v[64:67], a[252:255], a[192:195], v[64:67]\n" \
"ds_read_b128 a[128:131], v28 offset:0\n" \
"v_mfma_f32_16x16x32_bf16 v[80:83], a[188:191], a[132:135], v[80:83]\n" \
"ds_read_b128 a[192:195], v29 offset:0\n" \
"v_mfma_f32_16x16x32_bf16 v[80:83], a[252:255], a[196:199], v[80:83]\n" \
"s_add_u32 s6, s26, s6\n" \
"s_addc_u32 s9, s27, 0\n" \
"v_mfma_f32_16x16x32_bf16 v[76:79], a[184:187], a[132:135], v[76:79]\n" \
"s_add_u32 s8, s6, 0x1000000\n" \
"s_addc_u32 s6, s9, 0\n" \
"v_mfma_f32_16x16x32_bf16 v[76:79], a[248:251], a[196:199], v[76:79]\n" \
"s_and_b32 s9, s6, 0xffff\n" \
"s_lshl_b32 s6, s5, 16\n" \
"v_mfma_f32_16x16x32_bf16 v[72:75], a[180:183], a[132:135], v[72:75]\n" \
"s_sub_i32 s10, 0x100000, s6\n" \
"s_mov_b32 s6, 0x8000\n" \
"v_mfma_f32_16x16x32_bf16 v[72:75], a[244:247], a[196:199], v[72:75]\n" \
"s_cmp_lg_u32 s5, 0\n" \
"s_cselect_b32 s17, s11, s6\n" \
"v_mfma_f32_16x16x32_bf16 v[68:71], a[176:179], a[132:135], v[68:71]\n" \
"s_lshl_b32 s6, s3, 1\n" \
"s_lshl_b32 s5, s5, 3\n" \
"v_mfma_f32_16x16x32_bf16 v[68:71], a[240:243], a[196:199], v[68:71]\n" \
"ds_read_b128 a[132:135], v28 offset:2048\n" \
"ds_read_b128 a[196:199], v29 offset:2048\n" \
"s_waitcnt lgkmcnt(15)\n" \
"v_mfma_f32_16x16x32_bf16 a[0:3], a[160:163], a[144:147], a[0:3]\n" \
"s_addk_i32 s84, 0x2000\n" \
"v_mfma_f32_16x16x32_bf16 a[0:3], a[224:227], a[208:211], a[0:3]\n" \
"buffer_load_dwordx4 v24, s[76:79], s84 offen offset:2048 lds\n" \
"v_mfma_f32_16x16x32_bf16 a[4:7], a[164:167], a[144:147], a[4:7]\n" \
"ds_read_b64_tr_b16 a[176:177], v36 offset:0\n" \
"s_and_b32 s6, s6, 4\n" \
"v_mfma_f32_16x16x32_bf16 a[4:7], a[228:231], a[208:211], a[4:7]\n" \
"s_addk_i32 s84, 0x2000\n" \
"v_mfma_f32_16x16x32_bf16 a[12:15], a[172:175], a[144:147], a[12:15]\n" \
"buffer_load_dwordx4 v25, s[76:79], s84 offen offset:3072 lds\n" \
"s_waitcnt lgkmcnt(13)\n" \
"v_mfma_f32_16x16x32_bf16 a[12:15], a[236:239], a[208:211], a[12:15]\n" \
"ds_read_b64_tr_b16 a[178:179], v36 offset:1024\n" \
"s_or_b32 s12, s5, s6\n" \
"s_waitcnt lgkmcnt(10)\n" \
"v_mfma_f32_16x16x32_bf16 a[8:11], a[144:147], a[168:171], a[8:11]\n" \
"ds_read_b64_tr_b16 a[240:241], v36 offset:8192\n" \
"s_sub_i32 s5, s13, s5\n" \
"s_waitcnt lgkmcnt(7)\n" \
"v_mfma_f32_16x16x32_bf16 a[8:11], a[208:211], a[232:235], a[8:11]\n" \
"ds_read_b64_tr_b16 a[242:243], v36 offset:9216\n" \
"s_or_b32 s5, s5, s6\n" \
"v_mfma_f32_16x16x32_bf16 a[24:27], a[148:151], a[168:171], a[24:27]\n" \
"ds_read_b64_tr_b16 a[180:181], v37 offset:0\n" \
"s_or_b32 s6, s12, s13\n" \
"v_mfma_f32_16x16x32_bf16 a[24:27], a[212:215], a[232:235], a[24:27]\n" \
"ds_read_b64_tr_b16 a[182:183], v37 offset:1024\n" \
"s_lshl_b32 s4, s4, 2\n" \
"v_mfma_f32_16x16x32_bf16 a[28:31], a[172:175], a[148:151], a[28:31]\n" \
"ds_read_b64_tr_b16 a[244:245], v37 offset:8192\n" \
"s_mulk_i32 s6, 0x92\n" \
"v_mfma_f32_16x16x32_bf16 a[28:31], a[236:239], a[212:215], a[28:31]\n" \
"ds_read_b64_tr_b16 a[246:247], v37 offset:9216\n" \
"s_add_i32 s4, s4, 18\n" \
"v_mfma_f32_16x16x32_bf16 a[20:23], a[164:167], a[148:151], a[20:23]\n" \
"s_mulk_i32 s5, 0x92\n" \
"s_add_i32 s6, s4, s6\n" \
"v_mfma_f32_16x16x32_bf16 a[20:23], a[228:231], a[212:215], a[20:23]\n" \
"s_add_u32 s89, s42, 0x0\n" \
"buffer_store_dwordx4 v[192:195], v20, s[80:83], s89 offen offset:2048 sc1\n" \
"v_mfma_f32_16x16x32_bf16 a[16:19], a[160:163], a[148:151], a[16:19]\n" \
"ds_read_b64_tr_b16 a[184:185], v38 offset:0\n" \
"s_add_i32 s5, s5, s4\n" \
"v_mfma_f32_16x16x32_bf16 a[16:19], a[224:227], a[212:215], a[16:19]\n" \
"ds_read_b64_tr_b16 a[186:187], v38 offset:1024\n" \
"s_lshl_b32 s4, s3, 11\n" \
"v_mfma_f32_16x16x32_bf16 a[32:35], a[160:163], a[152:155], a[32:35]\n" \
"ds_read_b64_tr_b16 a[248:249], v38 offset:8192\n" \
"s_lshl_b32 s15, s13, 12\n" \
"v_mfma_f32_16x16x32_bf16 a[32:35], a[224:227], a[216:219], a[32:35]\n" \
"ds_read_b64_tr_b16 a[250:251], v38 offset:9216\n" \
"s_lshl_b32 s16, s19, 14\n" \
"v_mfma_f32_16x16x32_bf16 a[36:39], a[164:167], a[152:155], a[36:39]\n" \
"s_mul_i32 s19, s19, 0xe000\n" \
"s_mov_b32 s11, 0x20000\n" \
"v_mfma_f32_16x16x32_bf16 a[36:39], a[228:231], a[216:219], a[36:39]\n" \
"s_add_u32 s88, s42, 0x0\n" \
"buffer_store_dwordx4 v[196:199], v20, s[80:83], s88 offen offset:3072 sc1\n" \
"v_mfma_f32_16x16x32_bf16 a[44:47], a[172:175], a[152:155], a[44:47]\n" \
"ds_read_b64_tr_b16 a[188:189], v39 offset:0\n" \
"s_or_b32 s12, s4, 0xc000\n" \
"v_mfma_f32_16x16x32_bf16 a[44:47], a[236:239], a[216:219], a[44:47]\n" \
"ds_read_b64_tr_b16 a[190:191], v39 offset:1024\n" \
"s_lshl_b32 s4, s3, 8\n" \
"v_mfma_f32_16x16x32_bf16 a[40:43], a[152:155], a[168:171], a[40:43]\n" \
"ds_read_b64_tr_b16 a[252:253], v39 offset:8192\n" \
"s_or_b32 s22, s15, s16\n" \
"v_mfma_f32_16x16x32_bf16 a[40:43], a[216:219], a[232:235], a[40:43]\n" \
"s_or_b32 s23, s19, s15\n" \
"s_lshl_b32 s60, s6, 10\n" \
"v_mfma_f32_16x16x32_bf16 a[56:59], a[156:159], a[168:171], a[56:59]\n" \
"ds_read_b64_tr_b16 a[254:255], v39 offset:9216\n" \
"s_lshl_b32 s61, s5, 10\n" \
"v_mfma_f32_16x16x32_bf16 a[56:59], a[220:223], a[232:235], a[56:59]\n" \
"s_lshl_b32 s14, s14, 15\n" \
"s_or_b32 s20, s4, 0x26c00\n" \
"v_mfma_f32_16x16x32_bf16 a[60:63], a[172:175], a[156:159], a[60:63]\n" \
"s_add_u32 s89, s42, 0x2000\n" \
"buffer_store_dwordx4 v[200:203], v20, s[80:83], s89 offen offset:0 sc1\n" \
"v_mfma_f32_16x16x32_bf16 a[60:63], a[236:239], a[220:223], a[60:63]\n" \
"s_or_b32 s13, s22, 0x8000\n" \
"s_add_i32 s22, s22, 0x14000\n" \
"v_mfma_f32_16x16x32_bf16 a[52:55], a[164:167], a[156:159], a[52:55]\n" \
"s_bitset1_b32 s23, 16\n" \
"s_or_b32 s24, s7, 0x5be8020\n" \
"v_mfma_f32_16x16x32_bf16 a[52:55], a[228:231], a[220:223], a[52:55]\n" \
"s_and_b32 s57, s27, 0xffff\n" \
"s_mov_b32 s58, 0x6000000\n" \
"v_mfma_f32_16x16x32_bf16 a[48:51], a[160:163], a[156:159], a[48:51]\n" \
"s_mov_b32 s56, s26\n" \
"s_mov_b32 s59, s11\n" \
"v_mfma_f32_16x16x32_bf16 a[48:51], a[224:227], a[220:223], a[48:51]\n" \
"s_add_i32 s60, s60, 0x3000000\n" \
"s_add_i32 s61, s61, 0x411c000\n" \
"v_mfma_f32_16x16x32_bf16 a[64:67], a[164:167], a[140:143], a[64:67]\n" \
"s_sub_i32 s19, 14, s18\n" \
"s_mov_b32 s21, 0x71800000\n" \
"v_mfma_f32_16x16x32_bf16 a[64:67], a[228:231], a[204:207], a[64:67]\n" \
"v_mfma_f32_16x16x32_bf16 a[80:83], a[172:175], a[140:143], a[80:83]\n" \
"v_mfma_f32_16x16x32_bf16 a[80:83], a[236:239], a[204:207], a[80:83]\n" \
"v_mfma_f32_16x16x32_bf16 a[68:71], a[140:143], a[168:171], a[68:71]\n" \
"v_mfma_f32_16x16x32_bf16 a[68:71], a[204:207], a[232:235], a[68:71]\n" \
"s_waitcnt vmcnt(3) lgkmcnt(0)\n" \
"s_barrier\n" \
"v_mfma_f32_16x16x32_bf16 v[116:119], a[176:179], a[144:147], v[116:119]\n" \
"s_add_u32 m0, s40, 0x0\n" \
"s_nop 0\n" \
"s_add_u32 s85, s39, 0x10000\n" \
"v_mfma_f32_16x16x32_bf16 v[116:119], a[240:243], a[208:211], v[116:119]\n" \
"buffer_load_dwordx4 v20, s[80:83], s85 offen sc1 lds\n" \
"v_mfma_f32_16x16x32_bf16 v[120:123], a[180:183], a[144:147], v[120:123]\n" \
"ds_read_b64_tr_b16 a[160:161], v32 offset:32768\n" \
"v_mfma_f32_16x16x32_bf16 v[120:123], a[244:247], a[208:211], v[120:123]\n" \
"ds_read_b64_tr_b16 a[162:163], v32 offset:33280\n" \
"v_mfma_f32_16x16x32_bf16 v[124:127], a[184:187], a[144:147], v[124:127]\n" \
"s_add_u32 m0, s40, 0x1000\n" \
"s_add_u32 s85, s39, 0x11000\n" \
"v_mfma_f32_16x16x32_bf16 v[124:127], a[248:251], a[208:211], v[124:127]\n" \
"buffer_load_dwordx4 v20, s[80:83], s85 offen sc1 lds\n" \
"v_mfma_f32_16x16x32_bf16 v[128:131], a[188:191], a[144:147], v[128:131]\n" \
"ds_read_b64_tr_b16 a[224:225], v32 offset:36864\n" \
"v_mfma_f32_16x16x32_bf16 v[128:131], a[252:255], a[208:211], v[128:131]\n" \
"ds_read_b128 a[144:147], v28 offset:40960\n" \
"v_mfma_f32_16x16x32_bf16 v[144:147], a[188:191], a[148:151], v[144:147]\n" \
"ds_read_b128 a[208:211], v29 offset:40960\n" \
"v_mfma_f32_16x16x32_bf16 v[144:147], a[252:255], a[212:215], v[144:147]\n" \
"s_add_u32 m0, s40, 0x3000\n" \
"s_movk_i32 s84, 0x400\n" \
"v_mfma_f32_16x16x32_bf16 v[140:143], a[184:187], a[148:151], v[140:143]\n" \
"buffer_load_dwordx4 v21, s[76:79], s84 offen offset:0 lds\n" \
"v_mfma_f32_16x16x32_bf16 v[140:143], a[248:251], a[212:215], v[140:143]\n" \
"ds_read_b64_tr_b16 a[226:227], v32 offset:37376\n" \
"v_mfma_f32_16x16x32_bf16 v[136:139], a[180:183], a[148:151], v[136:139]\n" \
"s_add_u32 m0, s30, 0x0\n" \
"s_movk_i32 s84, 0x400\n" \
"v_mfma_f32_16x16x32_bf16 v[136:139], a[244:247], a[212:215], v[136:139]\n" \
"buffer_load_dwordx4 v24, s[76:79], s84 offen offset:0 lds\n" \
"v_mfma_f32_16x16x32_bf16 v[132:135], a[176:179], a[148:151], v[132:135]\n" \
"ds_read_b64_tr_b16 a[164:165], v33 offset:32768\n" \
"v_mfma_f32_16x16x32_bf16 v[132:135], a[240:243], a[212:215], v[132:135]\n" \
"ds_read_b128 a[148:151], v28 offset:43008\n" \
"v_mfma_f32_16x16x32_bf16 v[148:151], a[176:179], a[152:155], v[148:151]\n" \
"ds_read_b128 a[212:215], v29 offset:43008\n" \
"v_mfma_f32_16x16x32_bf16 v[148:151], a[240:243], a[216:219], v[148:151]\n" \
"s_movk_i32 s84, 0x2400\n" \
"v_mfma_f32_16x16x32_bf16 v[152:155], a[180:183], a[152:155], v[152:155]\n" \
"buffer_load_dwordx4 v25, s[76:79], s84 offen offset:1024 lds\n" \
"v_mfma_f32_16x16x32_bf16 v[152:155], a[244:247], a[216:219], v[152:155]\n" \
"ds_read_b64_tr_b16 a[166:167], v33 offset:33280\n" \
"v_mfma_f32_16x16x32_bf16 v[156:159], a[184:187], a[152:155], v[156:159]\n" \
"ds_read_b64_tr_b16 a[228:229], v33 offset:36864\n" \
"v_mfma_f32_16x16x32_bf16 v[156:159], a[248:251], a[216:219], v[156:159]\n" \
"s_movk_i32 s84, 0x4400\n" \
"v_mfma_f32_16x16x32_bf16 v[160:163], a[188:191], a[152:155], v[160:163]\n" \
"buffer_load_dwordx4 v24, s[76:79], s84 offen offset:2048 lds\n" \
"v_mfma_f32_16x16x32_bf16 v[160:163], a[252:255], a[216:219], v[160:163]\n" \
"ds_read_b128 a[152:155], v28 offset:45056\n" \
"v_mfma_f32_16x16x32_bf16 v[176:179], a[188:191], a[156:159], v[176:179]\n" \
"ds_read_b128 a[216:219], v29 offset:45056\n" \
"v_mfma_f32_16x16x32_bf16 v[176:179], a[252:255], a[220:223], v[176:179]\n" \
"s_movk_i32 s84, 0x6400\n" \
"v_mfma_f32_16x16x32_bf16 v[172:175], a[184:187], a[156:159], v[172:175]\n" \
"buffer_load_dwordx4 v25, s[76:79], s84 offen offset:3072 lds\n" \
"v_mfma_f32_16x16x32_bf16 v[172:175], a[248:251], a[220:223], v[172:175]\n" \
"ds_read_b64_tr_b16 a[230:231], v33 offset:37376\n" \
".fill (L_pb + 73440 - .) / 4, 4, 0xBF800000\n" \
"L_pp18:\n" \
"s_cbranch_execz L_pret\n" \
"v_mfma_f32_16x16x32_bf16 v[168:171], a[180:183], a[156:159], v[168:171]\n" \
"ds_read_b64_tr_b16 a[172:173], v35 offset:32768\n" \
"v_mfma_f32_16x16x32_bf16 v[168:171], a[244:247], a[220:223], v[168:171]\n" \
"ds_read_b64_tr_b16 a[174:175], v35 offset:33280\n" \
"v_mfma_f32_16x16x32_bf16 v[164:167], a[176:179], a[156:159], v[164:167]\n" \
"v_mfma_f32_16x16x32_bf16 v[164:167], a[240:243], a[220:223], v[164:167]\n" \
"ds_read_b128 a[156:159], v28 offset:47104\n" \
"v_mfma_f32_16x16x32_bf16 v[84:87], a[176:179], a[136:139], v[84:87]\n" \
"ds_read_b128 a[220:223], v29 offset:47104\n" \
"v_mfma_f32_16x16x32_bf16 v[84:87], a[240:243], a[200:203], v[84:87]\n" \
"s_add_u32 m0, s31, 0x0\n" \
"s_mov_b32 s84, 0x400000\n" \
"v_mfma_f32_16x16x32_bf16 v[88:91], a[180:183], a[136:139], v[88:91]\n" \
"buffer_load_dwordx4 v26, s[68:71], s84 offen offset:0 lds\n" \
"v_mfma_f32_16x16x32_bf16 v[88:91], a[244:247], a[200:203], v[88:91]\n" \
"s_addk_i32 s84, 0x7c00\n" \
"s_nop 0\n" \
"v_mfma_f32_16x16x32_bf16 v[92:95], a[184:187], a[136:139], v[92:95]\n" \
"buffer_load_dwordx4 v26, s[68:71], s84 offen offset:1024 lds\n" \
"v_mfma_f32_16x16x32_bf16 v[92:95], a[248:251], a[200:203], v[92:95]\n" \
"ds_read_b64_tr_b16 a[236:237], v35 offset:36864\n" \
"v_mfma_f32_16x16x32_bf16 v[96:99], a[188:191], a[136:139], v[96:99]\n" \
"ds_read_b64_tr_b16 a[238:239], v35 offset:37376\n" \
"v_mfma_f32_16x16x32_bf16 v[96:99], a[252:255], a[200:203], v[96:99]\n" \
"ds_read_b128 a[136:139], v28 offset:36864\n" \
"v_mfma_f32_16x16x32_bf16 v[112:115], a[188:191], a[140:143], v[112:115]\n" \
"ds_read_b128 a[200:203], v29 offset:36864\n" \
"v_mfma_f32_16x16x32_bf16 v[112:115], a[252:255], a[204:207], v[112:115]\n" \
"s_addk_i32 s84, 0x7c00\n" \
"v_mfma_f32_16x16x32_bf16 v[108:111], a[184:187], a[140:143], v[108:111]\n" \
"buffer_load_dwordx4 v27, s[68:71], s84 offen offset:2048 lds\n" \
"v_mfma_f32_16x16x32_bf16 v[108:111], a[248:251], a[204:207], v[108:111]\n" \
"s_addk_i32 s84, 0x7c00\n" \
"v_mfma_f32_16x16x32_bf16 v[104:107], a[180:183], a[140:143], v[104:107]\n" \
"buffer_load_dwordx4 v27, s[68:71], s84 offen offset:3072 lds\n" \
"v_mfma_f32_16x16x32_bf16 v[104:107], a[244:247], a[204:207], v[104:107]\n" \
"ds_read_b64_tr_b16 a[168:169], v34 offset:32768\n" \
"v_mfma_f32_16x16x32_bf16 v[100:103], a[176:179], a[140:143], v[100:103]\n" \
"ds_read_b64_tr_b16 a[170:171], v34 offset:33280\n" \
"v_mfma_f32_16x16x32_bf16 v[100:103], a[240:243], a[204:207], v[100:103]\n" \
"ds_read_b128 a[140:143], v28 offset:38912\n" \
"v_mfma_f32_16x16x32_bf16 v[52:55], a[176:179], a[128:131], v[52:55]\n" \
"ds_read_b128 a[204:207], v29 offset:38912\n" \
"v_mfma_f32_16x16x32_bf16 v[52:55], a[240:243], a[192:195], v[52:55]\n" \
"s_add_u32 m0, s30, 0x1000\n" \
"s_mov_b32 s84, 0x9400\n" \
"v_mfma_f32_16x16x32_bf16 v[56:59], a[180:183], a[128:131], v[56:59]\n" \
"buffer_load_dwordx4 v24, s[76:79], s84 offen offset:0 lds\n" \
"v_mfma_f32_16x16x32_bf16 v[56:59], a[244:247], a[192:195], v[56:59]\n" \
"s_addk_i32 s84, 0x2000\n" \
"s_nop 0\n" \
"v_mfma_f32_16x16x32_bf16 v[60:63], a[184:187], a[128:131], v[60:63]\n" \
"buffer_load_dwordx4 v25, s[76:79], s84 offen offset:1024 lds\n" \
"v_mfma_f32_16x16x32_bf16 v[60:63], a[248:251], a[192:195], v[60:63]\n" \
"ds_read_b64_tr_b16 a[232:233], v34 offset:36864\n" \
"v_mfma_f32_16x16x32_bf16 v[64:67], a[188:191], a[128:131], v[64:67]\n" \
"ds_read_b64_tr_b16 a[234:235], v34 offset:37376\n" \
"v_mfma_f32_16x16x32_bf16 v[64:67], a[252:255], a[192:195], v[64:67]\n" \
"ds_read_b128 a[128:131], v28 offset:32768\n" \
"v_mfma_f32_16x16x32_bf16 v[80:83], a[188:191], a[132:135], v[80:83]\n" \
"ds_read_b128 a[192:195], v29 offset:32768\n" \
"v_mfma_f32_16x16x32_bf16 v[80:83], a[252:255], a[196:199], v[80:83]\n" \
"v_mfma_f32_16x16x32_bf16 v[76:79], a[184:187], a[132:135], v[76:79]\n" \
"v_mfma_f32_16x16x32_bf16 v[76:79], a[248:251], a[196:199], v[76:79]\n" \
"v_mfma_f32_16x16x32_bf16 v[72:75], a[180:183], a[132:135], v[72:75]\n" \
"v_mfma_f32_16x16x32_bf16 v[72:75], a[244:247], a[196:199], v[72:75]\n" \
"v_mfma_f32_16x16x32_bf16 v[68:71], a[176:179], a[132:135], v[68:71]\n" \
"v_mfma_f32_16x16x32_bf16 v[68:71], a[240:243], a[196:199], v[68:71]\n" \
"ds_read_b128 a[132:135], v28 offset:34816\n" \
"ds_read_b128 a[196:199], v29 offset:34816\n" \
"s_waitcnt lgkmcnt(15)\n" \
"v_mfma_f32_16x16x32_bf16 a[0:3], a[160:163], a[144:147], a[0:3]\n" \
"s_addk_i32 s84, 0x2000\n" \
"v_mfma_f32_16x16x32_bf16 a[0:3], a[224:227], a[208:211], a[0:3]\n" \
"buffer_load_dwordx4 v24, s[76:79], s84 offen offset:2048 lds\n" \
"v_mfma_f32_16x16x32_bf16 a[4:7], a[164:167], a[144:147], a[4:7]\n" \
"ds_read_b64_tr_b16 a[176:177], v36 offset:32768\n" \
"v_mfma_f32_16x16x32_bf16 a[4:7], a[228:231], a[208:211], a[4:7]\n" \
"s_addk_i32 s84, 0x2000\n" \
"v_mfma_f32_16x16x32_bf16 a[12:15], a[172:175], a[144:147], a[12:15]\n" \
"buffer_load_dwordx4 v25, s[76:79], s84 offen offset:3072 lds\n" \
"s_waitcnt lgkmcnt(13)\n" \
"v_mfma_f32_16x16x32_bf16 a[12:15], a[236:239], a[208:211], a[12:15]\n" \
"ds_read_b64_tr_b16 a[178:179], v36 offset:33792\n" \
"s_waitcnt lgkmcnt(10)\n" \
"v_mfma_f32_16x16x32_bf16 a[8:11], a[144:147], a[168:171], a[8:11]\n" \
"ds_read_b64_tr_b16 a[240:241], v36 offset:40960\n" \
"s_waitcnt lgkmcnt(7)\n" \
"v_mfma_f32_16x16x32_bf16 a[8:11], a[208:211], a[232:235], a[8:11]\n" \
"ds_read_b64_tr_b16 a[242:243], v36 offset:41984\n" \
"v_mfma_f32_16x16x32_bf16 a[24:27], a[148:151], a[168:171], a[24:27]\n" \
"ds_read_b64_tr_b16 a[180:181], v37 offset:32768\n" \
"v_mfma_f32_16x16x32_bf16 a[24:27], a[212:215], a[232:235], a[24:27]\n" \
"ds_read_b64_tr_b16 a[182:183], v37 offset:33792\n" \
"v_mfma_f32_16x16x32_bf16 a[28:31], a[172:175], a[148:151], a[28:31]\n" \
"ds_read_b64_tr_b16 a[244:245], v37 offset:40960\n" \
"v_mfma_f32_16x16x32_bf16 a[28:31], a[236:239], a[212:215], a[28:31]\n" \
"ds_read_b64_tr_b16 a[246:247], v37 offset:41984\n" \
"v_mfma_f32_16x16x32_bf16 a[20:23], a[164:167], a[148:151], a[20:23]\n" \
"v_mfma_f32_16x16x32_bf16 a[20:23], a[228:231], a[212:215], a[20:23]\n" \
"s_add_u32 s88, s42, 0x2000\n" \
"buffer_store_dwordx4 v[204:207], v20, s[80:83], s88 offen offset:1024 sc1\n" \
"v_mfma_f32_16x16x32_bf16 a[16:19], a[160:163], a[148:151], a[16:19]\n" \
"ds_read_b64_tr_b16 a[184:185], v38 offset:32768\n" \
"v_mfma_f32_16x16x32_bf16 a[16:19], a[224:227], a[212:215], a[16:19]\n" \
"ds_read_b64_tr_b16 a[186:187], v38 offset:33792\n" \
"v_mfma_f32_16x16x32_bf16 a[32:35], a[160:163], a[152:155], a[32:35]\n" \
"ds_read_b64_tr_b16 a[248:249], v38 offset:40960\n" \
"v_mfma_f32_16x16x32_bf16 a[32:35], a[224:227], a[216:219], a[32:35]\n" \
"ds_read_b64_tr_b16 a[250:251], v38 offset:41984\n" \
"v_mfma_f32_16x16x32_bf16 a[36:39], a[164:167], a[152:155], a[36:39]\n" \
"v_lshlrev_b32_e32 v45, 3, v20\n" \
"v_add_u32_e32 v45, s30, v45\n" \
"v_mfma_f32_16x16x32_bf16 a[36:39], a[228:231], a[216:219], a[36:39]\n" \
"s_add_u32 s89, s42, 0x2000\n" \
"buffer_store_dwordx4 v[208:211], v20, s[80:83], s89 offen offset:2048 sc1\n" \
"s_getpc_b64 s[86:87]\n" \
"L_ct1_%=:\n" \
"s_add_u32 s86, s86, L_ap0-L_ct1_%=\n" \
"s_addc_u32 s87, s87, 0\n" \
"s_add_u32 m0, s40, 0x2000\n" \
"v_mfma_f32_16x16x32_bf16 a[44:47], a[172:175], a[152:155], a[44:47]\n" \
"global_load_lds_dword v45, s[86:87]\n" \
"v_mfma_f32_16x16x32_bf16 a[44:47], a[236:239], a[216:219], a[44:47]\n" \
"ds_read_b64_tr_b16 a[188:189], v39 offset:32768\n" \
"v_mfma_f32_16x16x32_bf16 a[40:43], a[152:155], a[168:171], a[40:43]\n" \
"ds_read_b64_tr_b16 a[190:191], v39 offset:33792\n" \
"v_mfma_f32_16x16x32_bf16 a[40:43], a[216:219], a[232:235], a[40:43]\n" \
"ds_read_b64_tr_b16 a[252:253], v39 offset:40960\n" \
"v_mfma_f32_16x16x32_bf16 a[56:59], a[156:159], a[168:171], a[56:59]\n" \
"ds_read_b64_tr_b16 a[254:255], v39 offset:41984\n" \
"v_mfma_f32_16x16x32_bf16 a[56:59], a[220:223], a[232:235], a[56:59]\n" \
"v_mfma_f32_16x16x32_bf16 a[60:63], a[172:175], a[156:159], a[60:63]\n" \
"s_add_u32 s88, s42, 0x2000\n" \
"buffer_store_dwordx4 v[212:215], v20, s[80:83], s88 offen offset:3072 sc1\n" \
"v_mfma_f32_16x16x32_bf16 a[60:63], a[236:239], a[220:223], a[60:63]\n" \
"v_mfma_f32_16x16x32_bf16 a[52:55], a[164:167], a[156:159], a[52:55]\n" \
"v_mfma_f32_16x16x32_bf16 a[52:55], a[228:231], a[220:223], a[52:55]\n" \
"v_mfma_f32_16x16x32_bf16 a[48:51], a[160:163], a[156:159], a[48:51]\n" \
"v_mfma_f32_16x16x32_bf16 a[48:51], a[224:227], a[220:223], a[48:51]\n" \
"v_mfma_f32_16x16x32_bf16 a[64:67], a[164:167], a[140:143], a[64:67]\n" \
"v_mfma_f32_16x16x32_bf16 a[64:67], a[228:231], a[204:207], a[64:67]\n" \
"v_mfma_f32_16x16x32_bf16 a[80:83], a[172:175], a[140:143], a[80:83]\n" \
"v_mfma_f32_16x16x32_bf16 a[80:83], a[236:239], a[204:207], a[80:83]\n" \
"v_mfma_f32_16x16x32_bf16 a[68:71], a[140:143], a[168:171], a[68:71]\n" \
"v_mfma_f32_16x16x32_bf16 a[68:71], a[204:207], a[232:235], a[68:71]\n" \
"s_waitcnt vmcnt(4) lgkmcnt(0)\n" \
"s_barrier\n" \
"v_mfma_f32_16x16x32_bf16 v[116:119], a[176:179], a[144:147], v[116:119]\n" \
"ds_read_b64_tr_b16 a[160:161], v32 offset:0\n" \
"v_accvgpr_read_b32 v184, a0\n" \
"v_mfma_f32_16x16x32_bf16 v[116:119], a[240:243], a[208:211], v[116:119]\n" \
"v_accvgpr_read_b32 v185, a1\n" \
"v_accvgpr_read_b32 v186, a2\n" \
"v_mfma_f32_16x16x32_bf16 v[120:123], a[180:183], a[144:147], v[120:123]\n" \
"v_accvgpr_read_b32 v187, a3\n" \
"v_accvgpr_read_b32 v188, a4\n" \
"v_mfma_f32_16x16x32_bf16 v[120:123], a[244:247], a[208:211], v[120:123]\n" \
"ds_read_b64_tr_b16 a[162:163], v32 offset:512\n" \
"v_accvgpr_read_b32 v189, a5\n" \
"v_mfma_f32_16x16x32_bf16 v[124:127], a[184:187], a[144:147], v[124:127]\n" \
"v_accvgpr_read_b32 v190, a6\n" \
"v_accvgpr_read_b32 v191, a7\n" \
"v_mfma_f32_16x16x32_bf16 v[124:127], a[248:251], a[208:211], v[124:127]\n" \
"v_accvgpr_read_b32 v196, a12\n" \
"v_accvgpr_read_b32 v197, a13\n" \
"v_mfma_f32_16x16x32_bf16 v[128:131], a[188:191], a[144:147], v[128:131]\n" \
"ds_read_b64_tr_b16 a[224:225], v32 offset:4096\n" \
"v_accvgpr_read_b32 v198, a14\n" \
"v_mfma_f32_16x16x32_bf16 v[128:131], a[252:255], a[208:211], v[128:131]\n" \
"ds_read_b128 a[144:147], v28 offset:8192\n" \
"v_accvgpr_read_b32 v199, a15\n" \
"v_mfma_f32_16x16x32_bf16 v[144:147], a[188:191], a[148:151], v[144:147]\n" \
"ds_read_b128 a[208:211], v29 offset:8192\n" \
"v_accvgpr_read_b32 v192, a8\n" \
"v_mfma_f32_16x16x32_bf16 v[144:147], a[252:255], a[212:215], v[144:147]\n" \
"ds_read_b64_tr_b16 a[226:227], v32 offset:4608\n" \
"v_accvgpr_read_b32 v193, a9\n" \
"v_mfma_f32_16x16x32_bf16 v[140:143], a[184:187], a[148:151], v[140:143]\n" \
"v_accvgpr_read_b32 v194, a10\n" \
"v_accvgpr_read_b32 v195, a11\n" \
"v_mfma_f32_16x16x32_bf16 v[140:143], a[248:251], a[212:215], v[140:143]\n" \
"v_accvgpr_read_b32 v208, a24\n" \
"v_accvgpr_read_b32 v209, a25\n" \
"v_mfma_f32_16x16x32_bf16 v[136:139], a[180:183], a[148:151], v[136:139]\n" \
"v_accvgpr_read_b32 v210, a26\n" \
"v_accvgpr_read_b32 v211, a27\n" \
"v_mfma_f32_16x16x32_bf16 v[136:139], a[244:247], a[212:215], v[136:139]\n" \
"ds_read_b64_tr_b16 a[164:165], v33 offset:0\n" \
"v_accvgpr_read_b32 v212, a28\n" \
"v_mfma_f32_16x16x32_bf16 v[132:135], a[176:179], a[148:151], v[132:135]\n" \
"v_accvgpr_read_b32 v213, a29\n" \
"v_accvgpr_read_b32 v214, a30\n" \
"v_mfma_f32_16x16x32_bf16 v[132:135], a[240:243], a[212:215], v[132:135]\n" \
"ds_read_b128 a[148:151], v28 offset:10240\n" \
"v_accvgpr_read_b32 v215, a31\n" \
"v_mfma_f32_16x16x32_bf16 v[148:151], a[176:179], a[152:155], v[148:151]\n" \
"ds_read_b128 a[212:215], v29 offset:10240\n" \
"v_accvgpr_read_b32 v204, a20\n" \
"v_mfma_f32_16x16x32_bf16 v[148:151], a[240:243], a[216:219], v[148:151]\n" \
"ds_read_b64_tr_b16 a[166:167], v33 offset:512\n" \
"v_accvgpr_read_b32 v205, a21\n" \
"v_mfma_f32_16x16x32_bf16 v[152:155], a[180:183], a[152:155], v[152:155]\n" \
"v_accvgpr_read_b32 v206, a22\n" \
"v_accvgpr_read_b32 v207, a23\n" \
"v_mfma_f32_16x16x32_bf16 v[152:155], a[244:247], a[216:219], v[152:155]\n" \
"ds_read_b64_tr_b16 a[228:229], v33 offset:4096\n" \
"v_accvgpr_read_b32 v200, a16\n" \
"v_mfma_f32_16x16x32_bf16 v[156:159], a[184:187], a[152:155], v[156:159]\n" \
"v_accvgpr_read_b32 v201, a17\n" \
"v_accvgpr_read_b32 v202, a18\n" \
"v_mfma_f32_16x16x32_bf16 v[156:159], a[248:251], a[216:219], v[156:159]\n" \
"v_accvgpr_read_b32 v203, a19\n" \
"v_accvgpr_read_b32 v216, a32\n" \
"v_mfma_f32_16x16x32_bf16 v[160:163], a[188:191], a[152:155], v[160:163]\n" \
"ds_read_b64_tr_b16 a[230:231], v33 offset:4608\n" \
"v_accvgpr_read_b32 v217, a33\n" \
"v_mfma_f32_16x16x32_bf16 v[160:163], a[252:255], a[216:219], v[160:163]\n" \
"ds_read_b128 a[152:155], v28 offset:12288\n" \
"v_accvgpr_read_b32 v218, a34\n" \
"v_mfma_f32_16x16x32_bf16 v[176:179], a[188:191], a[156:159], v[176:179]\n" \
"ds_read_b128 a[216:219], v29 offset:12288\n" \
"v_accvgpr_read_b32 v219, a35\n" \
"v_mfma_f32_16x16x32_bf16 v[176:179], a[252:255], a[220:223], v[176:179]\n" \
"v_accvgpr_read_b32 v220, a36\n" \
"v_accvgpr_read_b32 v221, a37\n" \
"v_mfma_f32_16x16x32_bf16 v[172:175], a[184:187], a[156:159], v[172:175]\n" \
"ds_read_b64_tr_b16 a[172:173], v35 offset:0\n" \
"v_accvgpr_read_b32 v222, a38\n" \
"v_mfma_f32_16x16x32_bf16 v[172:175], a[248:251], a[220:223], v[172:175]\n" \
"v_accvgpr_read_b32 v223, a39\n" \
"v_accvgpr_read_b32 v228, a44\n" \
"v_mfma_f32_16x16x32_bf16 v[168:171], a[180:183], a[156:159], v[168:171]\n" \
"v_accvgpr_read_b32 v229, a45\n" \
"v_accvgpr_read_b32 v230, a46\n" \
"v_mfma_f32_16x16x32_bf16 v[168:171], a[244:247], a[220:223], v[168:171]\n" \
"ds_read_b64_tr_b16 a[174:175], v35 offset:512\n" \
"v_accvgpr_read_b32 v231, a47\n" \
"v_mfma_f32_16x16x32_bf16 v[164:167], a[176:179], a[156:159], v[164:167]\n" \
"v_accvgpr_read_b32 v224, a40\n" \
"v_accvgpr_read_b32 v225, a41\n" \
"v_mfma_f32_16x16x32_bf16 v[164:167], a[240:243], a[220:223], v[164:167]\n" \
"ds_read_b128 a[156:159], v28 offset:14336\n" \
"v_accvgpr_read_b32 v226, a42\n" \
"v_mfma_f32_16x16x32_bf16 v[84:87], a[176:179], a[136:139], v[84:87]\n" \
"ds_read_b128 a[220:223], v29 offset:14336\n" \
"v_accvgpr_read_b32 v227, a43\n" \
"v_mfma_f32_16x16x32_bf16 v[84:87], a[240:243], a[200:203], v[84:87]\n" \
"ds_read_b64_tr_b16 a[236:237], v35 offset:4096\n" \
"v_accvgpr_read_b32 v240, a56\n" \
"v_mfma_f32_16x16x32_bf16 v[88:91], a[180:183], a[136:139], v[88:91]\n" \
"v_accvgpr_read_b32 v241, a57\n" \
"v_accvgpr_read_b32 v242, a58\n" \
"v_mfma_f32_16x16x32_bf16 v[88:91], a[244:247], a[200:203], v[88:91]\n" \
"ds_read_b64_tr_b16 a[238:239], v35 offset:4608\n" \
"v_accvgpr_read_b32 v243, a59\n" \
"v_mfma_f32_16x16x32_bf16 v[92:95], a[184:187], a[136:139], v[92:95]\n" \
"v_accvgpr_read_b32 v244, a60\n" \
"v_accvgpr_read_b32 v245, a61\n" \
"v_mfma_f32_16x16x32_bf16 v[92:95], a[248:251], a[200:203], v[92:95]\n" \
"v_accvgpr_read_b32 v246, a62\n" \
"v_accvgpr_read_b32 v247, a63\n" \
"v_mfma_f32_16x16x32_bf16 v[96:99], a[188:191], a[136:139], v[96:99]\n" \
"v_accvgpr_read_b32 v236, a52\n" \
"v_accvgpr_read_b32 v237, a53\n" \
"v_mfma_f32_16x16x32_bf16 v[96:99], a[252:255], a[200:203], v[96:99]\n" \
"ds_read_b128 a[136:139], v28 offset:4096\n" \
"v_accvgpr_read_b32 v238, a54\n" \
"v_mfma_f32_16x16x32_bf16 v[112:115], a[188:191], a[140:143], v[112:115]\n" \
"ds_read_b128 a[200:203], v29 offset:4096\n" \
"v_accvgpr_read_b32 v239, a55\n" \
"v_mfma_f32_16x16x32_bf16 v[112:115], a[252:255], a[204:207], v[112:115]\n" \
"ds_read_b64_tr_b16 a[168:169], v34 offset:0\n" \
"v_accvgpr_read_b32 v232, a48\n" \
"v_mfma_f32_16x16x32_bf16 v[108:111], a[184:187], a[140:143], v[108:111]\n" \
"ds_read_b64_tr_b16 a[170:171], v34 offset:512\n" \
"v_accvgpr_read_b32 v233, a49\n" \
"v_mfma_f32_16x16x32_bf16 v[108:111], a[248:251], a[204:207], v[108:111]\n" \
"v_accvgpr_read_b32 v234, a50\n" \
"v_accvgpr_read_b32 v235, a51\n" \
"v_mfma_f32_16x16x32_bf16 v[104:107], a[180:183], a[140:143], v[104:107]\n" \
"v_accvgpr_read_b32 v180, a64\n" \
"v_accvgpr_read_b32 v181, a65\n" \
"v_mfma_f32_16x16x32_bf16 v[104:107], a[244:247], a[204:207], v[104:107]\n" \
"ds_read_b64_tr_b16 a[232:233], v34 offset:4096\n" \
"v_accvgpr_read_b32 v182, a66\n" \
"v_mfma_f32_16x16x32_bf16 v[100:103], a[176:179], a[140:143], v[100:103]\n" \
"v_accvgpr_read_b32 v183, a67\n" \
"v_accvgpr_read_b32 v48, a80\n" \
"v_mfma_f32_16x16x32_bf16 v[100:103], a[240:243], a[204:207], v[100:103]\n" \
"ds_read_b128 a[140:143], v28 offset:6144\n" \
"v_accvgpr_read_b32 v49, a81\n" \
"v_mfma_f32_16x16x32_bf16 v[52:55], a[176:179], a[128:131], v[52:55]\n" \
"ds_read_b128 a[204:207], v29 offset:6144\n" \
"v_accvgpr_read_b32 v50, a82\n" \
"v_mfma_f32_16x16x32_bf16 v[52:55], a[240:243], a[192:195], v[52:55]\n" \
"ds_read_b64_tr_b16 a[234:235], v34 offset:4608\n" \
"v_accvgpr_read_b32 v51, a83\n" \
"v_mfma_f32_16x16x32_bf16 v[56:59], a[180:183], a[128:131], v[56:59]\n" \
"v_accvgpr_read_b32 v252, a68\n" \
"v_accvgpr_read_b32 v253, a69\n" \
"v_mfma_f32_16x16x32_bf16 v[56:59], a[244:247], a[192:195], v[56:59]\n" \
"v_accvgpr_read_b32 v254, a70\n" \
"v_accvgpr_read_b32 v255, a71\n" \
"v_mfma_f32_16x16x32_bf16 v[60:63], a[184:187], a[128:131], v[60:63]\n" \
"v_lshrrev_b32_e64 v24, 9, v20\n" \
"v_and_b32_e32 v26, 0x1ff, v20\n" \
"v_mfma_f32_16x16x32_bf16 v[60:63], a[248:251], a[192:195], v[60:63]\n" \
"v_lshl_add_u32 v24, v24, 3, v26\n" \
"v_lshrrev_b32_e64 v25, 8, v20\n" \
"v_mfma_f32_16x16x32_bf16 v[64:67], a[188:191], a[128:131], v[64:67]\n" \
"v_mul_u32_u24_e32 v25, 0x24700, v25\n" \
"v_add_u32_e64 v25, v25, v20\n" \
"v_mfma_f32_16x16x32_bf16 v[64:67], a[252:255], a[192:195], v[64:67]\n" \
"ds_read_b128 a[128:131], v28 offset:0\n" \
"v_lshrrev_b32_e64 v26, 2, v20\n" \
"v_mfma_f32_16x16x32_bf16 v[80:83], a[188:191], a[132:135], v[80:83]\n" \
"ds_read_b128 a[192:195], v29 offset:0\n" \
"v_add_u32_e64 v26, s45, v26\n" \
"v_mfma_f32_16x16x32_bf16 v[80:83], a[252:255], a[196:199], v[80:83]\n" \
"v_add_u32_e32 v27, s46, v20\n" \
"v_add_u32_e32 v21, s43, v27\n" \
"v_mfma_f32_16x16x32_bf16 v[76:79], a[184:187], a[132:135], v[76:79]\n" \
"v_mfma_f32_16x16x32_bf16 v[76:79], a[248:251], a[196:199], v[76:79]\n" \
"v_mfma_f32_16x16x32_bf16 v[72:75], a[180:183], a[132:135], v[72:75]\n" \
"v_mfma_f32_16x16x32_bf16 v[72:75], a[244:247], a[196:199], v[72:75]\n" \
"v_mfma_f32_16x16x32_bf16 v[68:71], a[176:179], a[132:135], v[68:71]\n" \
"v_mfma_f32_16x16x32_bf16 v[68:71], a[240:243], a[196:199], v[68:71]\n" \
"ds_read_b128 a[132:135], v28 offset:2048\n" \
"ds_read_b128 a[196:199], v29 offset:2048\n" \
"s_waitcnt lgkmcnt(15)\n" \
"s_nop 0\n" \
"v_mfma_f32_16x16x32_bf16 v[184:187], a[160:163], a[144:147], v[184:187]\n" \
"v_bfe_u32 v44, v20, 2, 5\n" \
"ds_read_b64_tr_b16 a[176:177], v36 offset:0\n" \
"v_mfma_f32_16x16x32_bf16 v[184:187], a[224:227], a[208:211], v[184:187]\n" \
"buffer_load_dword v44, v44, s[80:83], s38 offen offset:0 sc1\n" \
"v_mfma_f32_16x16x32_bf16 v[188:191], a[164:167], a[144:147], v[188:191]\n" \
"ds_read_b64_tr_b16 a[178:179], v36 offset:1024\n" \
"v_mfma_f32_16x16x32_bf16 v[188:191], a[228:231], a[208:211], v[188:191]\n" \
"ds_read_b64_tr_b16 a[240:241], v36 offset:8192\n" \
"v_mfma_f32_16x16x32_bf16 v[196:199], a[172:175], a[144:147], v[196:199]\n" \
"ds_read_b64_tr_b16 a[242:243], v36 offset:9216\n" \
"s_waitcnt lgkmcnt(15)\n" \
"v_mfma_f32_16x16x32_bf16 v[196:199], a[236:239], a[208:211], v[196:199]\n" \
"s_waitcnt lgkmcnt(12)\n" \
"v_mfma_f32_16x16x32_bf16 v[192:195], a[144:147], a[168:171], v[192:195]\n" \
"ds_read_b64_tr_b16 a[180:181], v37 offset:0\n" \
"s_waitcnt lgkmcnt(9)\n" \
"s_nop 0\n" \
"v_mfma_f32_16x16x32_bf16 v[192:195], a[208:211], a[232:235], v[192:195]\n" \
"v_mfma_f32_16x16x32_bf16 v[208:211], a[148:151], a[168:171], v[208:211]\n" \
"ds_read_b64_tr_b16 a[182:183], v37 offset:1024\n" \
"v_mfma_f32_16x16x32_bf16 v[208:211], a[212:215], a[232:235], v[208:211]\n" \
"ds_read_b64_tr_b16 a[244:245], v37 offset:8192\n" \
"v_mfma_f32_16x16x32_bf16 v[212:215], a[172:175], a[148:151], v[212:215]\n" \
"v_mfma_f32_16x16x32_bf16 v[212:215], a[236:239], a[212:215], v[212:215]\n" \
"ds_read_b64_tr_b16 a[246:247], v37 offset:9216\n" \
"v_cvt_pk_bf16_f32 v188, v188, v189\n" \
"v_mfma_f32_16x16x32_bf16 v[204:207], a[164:167], a[148:151], v[204:207]\n" \
"v_cvt_pk_bf16_f32 v189, v190, v191\n" \
"v_cvt_pk_bf16_f32 v190, v198, v199\n" \
"v_mfma_f32_16x16x32_bf16 v[204:207], a[228:231], a[212:215], v[204:207]\n" \
"ds_read_b64_tr_b16 a[184:185], v38 offset:0\n" \
"v_mov_b32_e32 v191, 0x3f80\n" \
"v_mfma_f32_16x16x32_bf16 v[200:203], a[160:163], a[148:151], v[200:203]\n" \
"ds_read_b64_tr_b16 a[186:187], v38 offset:1024\n" \
"v_cvt_pk_bf16_f32 v184, v184, v185\n" \
"v_mfma_f32_16x16x32_bf16 v[200:203], a[224:227], a[212:215], v[200:203]\n" \
"v_cvt_pk_bf16_f32 v185, v186, v187\n" \
"v_cvt_pk_bf16_f32 v186, v196, v197\n" \
"v_mfma_f32_16x16x32_bf16 v[216:219], a[160:163], a[152:155], v[216:219]\n" \
"ds_read_b64_tr_b16 a[248:249], v38 offset:8192\n" \
"v_mov_b32_e64 v187, 0\n" \
"v_mfma_f32_16x16x32_bf16 v[216:219], a[224:227], a[216:219], v[216:219]\n" \
"ds_read_b64_tr_b16 a[250:251], v38 offset:9216\n" \
"v_mfma_f32_16x16x32_bf16 v[220:223], a[164:167], a[152:155], v[220:223]\n" \
"v_mfma_f32_16x16x32_bf16 v[220:223], a[228:231], a[216:219], v[220:223]\n" \
"ds_read_b64_tr_b16 a[188:189], v39 offset:0\n" \
"v_cvt_pk_bf16_f32 v204, v204, v205\n" \
"v_mfma_f32_16x16x32_bf16 v[228:231], a[172:175], a[152:155], v[228:231]\n" \
"v_cvt_pk_bf16_f32 v205, v206, v207\n" \
"v_cvt_pk_bf16_f32 v206, v214, v215\n" \
"v_mfma_f32_16x16x32_bf16 v[228:231], a[236:239], a[216:219], v[228:231]\n" \
"ds_read_b64_tr_b16 a[190:191], v39 offset:1024\n" \
"v_mov_b32_e32 v207, 0x3f80\n" \
"v_mfma_f32_16x16x32_bf16 v[224:227], a[152:155], a[168:171], v[224:227]\n" \
"ds_read_b64_tr_b16 a[252:253], v39 offset:8192\n" \
"v_cvt_pk_bf16_f32 v200, v200, v201\n" \
"v_mfma_f32_16x16x32_bf16 v[224:227], a[216:219], a[232:235], v[224:227]\n" \
"v_cvt_pk_bf16_f32 v201, v202, v203\n" \
"v_cvt_pk_bf16_f32 v202, v212, v213\n" \
"v_mfma_f32_16x16x32_bf16 v[240:243], a[156:159], a[168:171], v[240:243]\n" \
"ds_read_b64_tr_b16 a[254:255], v39 offset:9216\n" \
"v_mov_b32_e64 v203, 0\n" \
"v_mfma_f32_16x16x32_bf16 v[240:243], a[220:223], a[232:235], v[240:243]\n" \
"v_mfma_f32_16x16x32_bf16 v[244:247], a[172:175], a[156:159], v[244:247]\n" \
"v_mfma_f32_16x16x32_bf16 v[244:247], a[236:239], a[220:223], v[244:247]\n" \
"v_cvt_pk_bf16_f32 v220, v220, v221\n" \
"v_cvt_pk_bf16_f32 v221, v222, v223\n" \
"v_mfma_f32_16x16x32_bf16 v[236:239], a[164:167], a[156:159], v[236:239]\n" \
"v_cvt_pk_bf16_f32 v222, v230, v231\n" \
"v_mov_b32_e32 v223, 0x3f80\n" \
"v_mfma_f32_16x16x32_bf16 v[236:239], a[228:231], a[220:223], v[236:239]\n" \
"v_cvt_pk_bf16_f32 v216, v216, v217\n" \
"v_cvt_pk_bf16_f32 v217, v218, v219\n" \
"v_mfma_f32_16x16x32_bf16 v[232:235], a[160:163], a[156:159], v[232:235]\n" \
"v_cvt_pk_bf16_f32 v218, v228, v229\n" \
"v_mov_b32_e64 v219, 0\n" \
"v_mfma_f32_16x16x32_bf16 v[232:235], a[224:227], a[220:223], v[232:235]\n" \
"v_mfma_f32_16x16x32_bf16 v[180:183], a[164:167], a[140:143], v[180:183]\n" \
"v_mfma_f32_16x16x32_bf16 v[180:183], a[228:231], a[204:207], v[180:183]\n" \
"v_mfma_f32_16x16x32_bf16 v[48:51], a[172:175], a[140:143], v[48:51]\n" \
"v_mfma_f32_16x16x32_bf16 v[48:51], a[236:239], a[204:207], v[48:51]\n" \
"v_cvt_pk_bf16_f32 v236, v236, v237\n" \
"v_cvt_pk_bf16_f32 v237, v238, v239\n" \
"v_mfma_f32_16x16x32_bf16 v[252:255], a[140:143], a[168:171], v[252:255]\n" \
"v_cvt_pk_bf16_f32 v238, v246, v247\n" \
"v_mov_b32_e32 v239, 0x3f80\n" \
"v_mfma_f32_16x16x32_bf16 v[252:255], a[204:207], a[232:235], v[252:255]\n" \
"v_cvt_pk_bf16_f32 v232, v232, v233\n" \
"v_cvt_pk_bf16_f32 v233, v234, v235\n" \
"s_waitcnt vmcnt(0) lgkmcnt(0)\n" \
"s_barrier\n" \
"v_mov_b32_e32 v45, s62\n" \
"s_mov_b64 exec, 1\n" \
"buffer_store_dword v45, off, s[80:83], s37 offset:32 sc1\n" \
"s_mov_b64 exec, -1\n" \
"v_cmp_ne_u32_e32 vcc, s62, v44\n" \
"s_nop 4\n" \
"s_cbranch_vccnz L_poll161_%=\n" \
"L_pollr161_%=:\n" \
"v_mfma_f32_16x16x32_bf16 v[116:119], a[176:179], a[144:147], v[116:119]\n" \
"s_mov_b32 s73, s50\n" \
"s_sub_u32 m0, s31, 0x10000\n" \
"v_mfma_f32_16x16x32_bf16 v[116:119], a[240:243], a[208:211], v[116:119]\n" \
"buffer_load_dwordx4 v20, s[80:83], s73 offen offset:0 sc1 lds\n" \
"v_mfma_f32_16x16x32_bf16 v[120:123], a[180:183], a[144:147], v[120:123]\n" \
"s_add_u32 s74, s50, 0x4000\n" \
"s_add_u32 s75, s50, 0x8000\n" \
"v_mfma_f32_16x16x32_bf16 v[120:123], a[244:247], a[208:211], v[120:123]\n" \
"s_mov_b32 s70, 0x3e0293ee\n" \
"s_mov_b32 s76, s52\n" \
"v_mfma_f32_16x16x32_bf16 v[124:127], a[184:187], a[144:147], v[124:127]\n" \
"v_mul_f32_e32 v116, s70, v116\n" \
"v_mul_f32_e32 v117, s70, v117\n" \
"v_mfma_f32_16x16x32_bf16 v[124:127], a[248:251], a[208:211], v[124:127]\n" \
"buffer_load_dwordx4 v20, s[80:83], s73 offen offset:1024 sc1 lds\n" \
"v_mfma_f32_16x16x32_bf16 v[128:131], a[188:191], a[144:147], v[128:131]\n" \
"v_mul_f32_e32 v118, s70, v118\n" \
"v_mul_f32_e32 v119, s70, v119\n" \
"v_mfma_f32_16x16x32_bf16 v[128:131], a[252:255], a[208:211], v[128:131]\n" \
"v_cvt_pk_bf16_f32 v116, v116, v117\n" \
"v_mul_f32_e64 v120, s70, v120\n" \
"v_mfma_f32_16x16x32_bf16 v[144:147], a[188:191], a[148:151], v[144:147]\n" \
"v_mul_f32_e32 v124, s70, v124\n" \
"v_mul_f32_e32 v125, s70, v125\n" \
"v_mfma_f32_16x16x32_bf16 v[144:147], a[252:255], a[212:215], v[144:147]\n" \
"buffer_load_dwordx4 v20, s[80:83], s73 offen offset:2048 sc1 lds\n" \
"v_mfma_f32_16x16x32_bf16 v[140:143], a[184:187], a[148:151], v[140:143]\n" \
"v_mul_f32_e32 v126, s70, v126\n" \
"v_mul_f32_e32 v127, s70, v127\n" \
"v_mfma_f32_16x16x32_bf16 v[140:143], a[248:251], a[212:215], v[140:143]\n" \
"v_cvt_pk_bf16_f32 v124, v124, v125\n" \
"v_mul_f32_e64 v121, s70, v121\n" \
"v_mfma_f32_16x16x32_bf16 v[136:139], a[180:183], a[148:151], v[136:139]\n" \
"v_mul_f32_e32 v122, s70, v122\n" \
"v_mul_f32_e32 v123, s70, v123\n" \
"v_mfma_f32_16x16x32_bf16 v[136:139], a[244:247], a[212:215], v[136:139]\n" \
"buffer_load_dwordx4 v20, s[80:83], s73 offen offset:3072 sc1 lds\n" \
"v_mfma_f32_16x16x32_bf16 v[132:135], a[176:179], a[148:151], v[132:135]\n" \
"s_sub_u32 m0, s31, s51\n" \
"v_cvt_pk_bf16_f32 v117, v118, v119\n" \
"v_mfma_f32_16x16x32_bf16 v[132:135], a[240:243], a[212:215], v[132:135]\n" \
"v_mul_f32_e32 v128, s70, v128\n" \
"v_mul_f32_e64 v129, s70, v129\n" \
"v_mfma_f32_16x16x32_bf16 v[148:151], a[176:179], a[152:155], v[148:151]\n" \
"v_mul_f32_e32 v130, s70, v130\n" \
"v_mul_f32_e32 v131, s70, v131\n" \
"v_mfma_f32_16x16x32_bf16 v[148:151], a[240:243], a[216:219], v[148:151]\n" \
"buffer_load_dwordx4 v20, s[80:83], s74 offen offset:0 sc1 lds\n" \
"v_mfma_f32_16x16x32_bf16 v[152:155], a[180:183], a[152:155], v[152:155]\n" \
"v_cvt_pk_bf16_f32 v125, v126, v127\n" \
"v_cvt_pk_bf16_f32 v118, v120, v121\n" \
"v_mfma_f32_16x16x32_bf16 v[152:155], a[244:247], a[216:219], v[152:155]\n" \
"v_cvt_pk_bf16_f32 v119, v122, v123\n" \
"v_cvt_pk_bf16_f32 v126, v128, v129\n" \
"v_mfma_f32_16x16x32_bf16 v[156:159], a[184:187], a[152:155], v[156:159]\n" \
"v_cvt_pk_bf16_f32 v127, v130, v131\n" \
"v_mul_f32_e64 v132, s70, v132\n" \
"v_mfma_f32_16x16x32_bf16 v[156:159], a[248:251], a[216:219], v[156:159]\n" \
"buffer_load_dwordx4 v20, s[80:83], s74 offen offset:1024 sc1 lds\n" \
"v_mfma_f32_16x16x32_bf16 v[160:163], a[188:191], a[152:155], v[160:163]\n" \
"ds_write_b128 v21, v[116:119] offset:16384\n" \
"v_mfma_f32_16x16x32_bf16 v[160:163], a[252:255], a[216:219], v[160:163]\n" \
"v_mul_f32_e32 v133, s70, v133\n" \
"v_mul_f32_e32 v140, s70, v140\n" \
"v_mfma_f32_16x16x32_bf16 v[176:179], a[188:191], a[156:159], v[176:179]\n" \
"v_mul_f32_e32 v141, s70, v141\n" \
"v_mul_f32_e32 v134, s70, v134\n" \
"v_mfma_f32_16x16x32_bf16 v[176:179], a[252:255], a[220:223], v[176:179]\n" \
"buffer_load_dwordx4 v20, s[80:83], s74 offen offset:2048 sc1 lds\n" \
"v_mfma_f32_16x16x32_bf16 v[172:175], a[184:187], a[156:159], v[172:175]\n" \
"ds_write_b128 v21, v[124:127] offset:17408\n" \
"v_mfma_f32_16x16x32_bf16 v[172:175], a[248:251], a[220:223], v[172:175]\n" \
"v_mul_f32_e64 v135, s70, v135\n" \
"v_cvt_pk_bf16_f32 v132, v132, v133\n" \
"v_mfma_f32_16x16x32_bf16 v[168:171], a[180:183], a[156:159], v[168:171]\n" \
"v_mul_f32_e32 v142, s70, v142\n" \
"v_mul_f32_e32 v143, s70, v143\n" \
"v_mfma_f32_16x16x32_bf16 v[168:171], a[244:247], a[220:223], v[168:171]\n" \
"buffer_load_dwordx4 v20, s[80:83], s74 offen offset:3072 sc1 lds\n" \
"v_mfma_f32_16x16x32_bf16 v[164:167], a[176:179], a[156:159], v[164:167]\n" \
"s_sub_u32 m0, s31, 0x4000\n" \
"v_cvt_pk_bf16_f32 v140, v140, v141\n" \
"v_mfma_f32_16x16x32_bf16 v[164:167], a[240:243], a[220:223], v[164:167]\n" \
"v_mul_f32_e32 v136, s70, v136\n" \
"v_mul_f32_e32 v137, s70, v137\n" \
"v_mfma_f32_16x16x32_bf16 v[84:87], a[176:179], a[136:139], v[84:87]\n" \
"v_mul_f32_e32 v138, s70, v138\n" \
"v_mul_f32_e32 v139, s70, v139\n" \
"v_mfma_f32_16x16x32_bf16 v[84:87], a[240:243], a[200:203], v[84:87]\n" \
"buffer_load_dwordx4 v20, s[80:83], s75 offen offset:0 sc1 lds\n" \
"v_mfma_f32_16x16x32_bf16 v[88:91], a[180:183], a[136:139], v[88:91]\n" \
"v_cvt_pk_bf16_f32 v133, v134, v135\n" \
"v_mul_f32_e64 v144, s70, v144\n" \
"v_mfma_f32_16x16x32_bf16 v[88:91], a[244:247], a[200:203], v[88:91]\n" \
"v_mul_f32_e32 v145, s70, v145\n" \
"v_mul_f32_e32 v146, s70, v146\n" \
"v_mfma_f32_16x16x32_bf16 v[92:95], a[184:187], a[136:139], v[92:95]\n" \
"v_mul_f32_e64 v147, s70, v147\n" \
"v_cvt_pk_bf16_f32 v141, v142, v143\n" \
"v_mfma_f32_16x16x32_bf16 v[92:95], a[248:251], a[200:203], v[92:95]\n" \
"buffer_load_dwordx4 v20, s[80:83], s75 offen offset:1024 sc1 lds\n" \
"v_mfma_f32_16x16x32_bf16 v[96:99], a[188:191], a[136:139], v[96:99]\n" \
"v_cvt_pk_bf16_f32 v134, v136, v137\n" \
"v_cvt_pk_bf16_f32 v135, v138, v139\n" \
"v_mfma_f32_16x16x32_bf16 v[96:99], a[252:255], a[200:203], v[96:99]\n" \
"v_cvt_pk_bf16_f32 v142, v144, v145\n" \
"v_cvt_pk_bf16_f32 v143, v146, v147\n" \
"v_mfma_f32_16x16x32_bf16 v[112:115], a[188:191], a[140:143], v[112:115]\n" \
"ds_write_b128 v21, v[132:135] offset:20480\n" \
"v_mfma_f32_16x16x32_bf16 v[112:115], a[252:255], a[204:207], v[112:115]\n" \
"buffer_load_dwordx4 v20, s[80:83], s75 offen offset:2048 sc1 lds\n" \
"v_mfma_f32_16x16x32_bf16 v[108:111], a[184:187], a[140:143], v[108:111]\n" \
".fill (L_pb + 77536 - .) / 4, 4, 0xBF800000\n" \
"L_pp19:\n" \
"s_cbranch_execz L_pret\n" \
"v_cvt_pk_bf16_f32 v180, v180, v181\n" \
"v_cvt_pk_bf16_f32 v181, v182, v183\n" \
"v_mfma_f32_16x16x32_bf16 v[108:111], a[248:251], a[204:207], v[108:111]\n" \
"ds_write_b128 v21, v[140:143] offset:21504\n" \
"v_mfma_f32_16x16x32_bf16 v[104:107], a[180:183], a[140:143], v[104:107]\n" \
"v_cvt_pk_bf16_f32 v234, v244, v245\n" \
"v_mov_b32_e32 v235, 0\n" \
"v_mfma_f32_16x16x32_bf16 v[104:107], a[244:247], a[204:207], v[104:107]\n" \
"buffer_load_dwordx4 v20, s[80:83], s75 offen offset:3072 sc1 lds\n" \
"v_mfma_f32_16x16x32_bf16 v[100:103], a[176:179], a[140:143], v[100:103]\n" \
"s_mov_b32 m0, s53\n" \
"s_nop 0\n" \
"v_cvt_pk_bf16_f32 v182, v50, v51\n" \
"v_mfma_f32_16x16x32_bf16 v[100:103], a[240:243], a[204:207], v[100:103]\n" \
"v_mov_b32_e32 v183, 0x3f80\n" \
"v_cvt_pk_bf16_f32 v118, v192, v193\n" \
"v_mfma_f32_16x16x32_bf16 v[52:55], a[176:179], a[128:131], v[52:55]\n" \
"v_cvt_pk_bf16_f32 v119, v194, v195\n" \
"v_cvt_pk_bf16_f32 v120, v208, v209\n" \
"v_mfma_f32_16x16x32_bf16 v[52:55], a[240:243], a[192:195], v[52:55]\n" \
"buffer_load_dwordx4 v20, s[80:83], s76 offen offset:0 sc1 lds\n" \
"v_mfma_f32_16x16x32_bf16 v[56:59], a[180:183], a[128:131], v[56:59]\n" \
"v_cvt_pk_bf16_f32 v121, v210, v211\n" \
"v_cvt_pk_bf16_f32 v122, v224, v225\n" \
"v_mfma_f32_16x16x32_bf16 v[56:59], a[244:247], a[192:195], v[56:59]\n" \
"v_cvt_pk_bf16_f32 v123, v226, v227\n" \
"v_cvt_pk_bf16_f32 v124, v240, v241\n" \
"v_cvt_pk_bf16_f32 v125, v242, v243\n" \
"v_mfma_f32_16x16x32_bf16 v[60:63], a[184:187], a[128:131], v[60:63]\n" \
"v_mfma_f32_16x16x32_bf16 v[192:195], v[188:191], v[184:187], 0\n" \
"buffer_load_dwordx4 v20, s[80:83], s76 offen offset:1024 sc1 lds\n" \
"v_mfma_f32_16x16x32_bf16 v[32:35], v[180:183], v[184:187], 0\n" \
"v_mfma_f32_16x16x32_bf16 v[208:211], v[204:207], v[200:203], 0\n" \
"v_mfma_f32_16x16x32_bf16 v[36:39], v[188:191], v[200:203], 0\n" \
"v_mfma_f32_16x16x32_bf16 v[224:227], v[220:223], v[216:219], 0\n" \
"v_mfma_f32_16x16x32_bf16 v[248:251], v[204:207], v[216:219], 0\n" \
"v_mfma_f32_16x16x32_bf16 v[240:243], v[236:239], v[232:235], 0\n" \
"v_mfma_f32_16x16x32_bf16 v[128:131], v[220:223], v[232:235], 0\n" \
"s_mov_b32 s64, 0xaaaaaaaa\n" \
"s_mov_b32 s65, 0xaaaaaaaa\n" \
"v_mfma_f32_16x16x32_bf16 v[60:63], a[248:251], a[192:195], v[60:63]\n" \
"s_mov_b32 s66, 0xfff0ffff\n" \
"s_mov_b32 s67, 0xf000ff00\n" \
"v_mfma_f32_16x16x32_bf16 v[64:67], a[188:191], a[128:131], v[64:67]\n" \
"s_mov_b32 s68, 0xffc0fffc\n" \
"s_mov_b32 s69, 0xc000fc00\n" \
"v_cndmask_b32_e64 v192, v192, v193, s[64:65]\n" \
"v_cndmask_b32_e64 v32, v32, v33, s[64:65]\n" \
"v_cndmask_b32_e64 v194, v194, v195, s[64:65]\n" \
"v_cndmask_b32_e64 v34, v34, v35, s[64:65]\n" \
"v_cndmask_b32_e64 v208, v208, v209, s[64:65]\n" \
"v_cndmask_b32_e64 v36, v36, v37, s[64:65]\n" \
"v_cndmask_b32_e64 v210, v210, v211, s[64:65]\n" \
"v_cndmask_b32_e64 v38, v38, v39, s[64:65]\n" \
"v_cndmask_b32_e64 v224, v224, v225, s[64:65]\n" \
"v_cndmask_b32_e64 v248, v248, v249, s[64:65]\n" \
"v_cndmask_b32_e64 v226, v226, v227, s[64:65]\n" \
"v_cndmask_b32_e64 v250, v250, v251, s[64:65]\n" \
"v_cndmask_b32_e64 v240, v240, v241, s[64:65]\n" \
"v_cndmask_b32_e64 v128, v128, v129, s[64:65]\n" \
"v_cndmask_b32_e64 v242, v242, v243, s[64:65]\n" \
"v_cndmask_b32_e64 v130, v130, v131, s[64:65]\n" \
"v_cndmask_b32_e64 v192, v32, v192, s[66:67]\n" \
"v_cndmask_b32_e64 v194, v34, v194, s[68:69]\n" \
"v_cndmask_b32_e64 v208, v36, v208, s[66:67]\n" \
"v_cndmask_b32_e64 v210, v38, v210, s[68:69]\n" \
"v_cndmask_b32_e64 v224, v248, v224, s[66:67]\n" \
"v_cndmask_b32_e64 v226, v250, v226, s[68:69]\n" \
"v_cndmask_b32_e64 v240, v128, v240, s[66:67]\n" \
"v_cndmask_b32_e64 v242, v130, v242, s[68:69]\n" \
"v_mfma_f32_16x16x32_bf16 v[64:67], a[252:255], a[192:195], v[64:67]\n" \
"v_max_f32_e32 v193, v192, v194\n" \
"v_max_f32_e32 v209, v208, v210\n" \
"v_max_f32_e32 v225, v224, v226\n" \
"v_max_f32_e32 v241, v240, v242\n" \
"v_mul_f32_e32 v148, s70, v148\n" \
"v_permlane32_swap_b32_e32 v193, v225\n" \
"v_permlane32_swap_b32_e32 v209, v241\n" \
"v_mul_f32_e32 v149, s70, v149\n" \
"v_mul_f32_e32 v156, s70, v156\n" \
"v_mul_f32_e32 v157, s70, v157\n" \
"v_permlane32_swap_b32_e32 v196, v228\n" \
"v_permlane32_swap_b32_e32 v212, v244\n" \
"v_max_f32_e32 v193, v193, v225\n" \
"v_max_f32_e32 v209, v209, v241\n" \
"v_mul_f32_e32 v150, s70, v150\n" \
"v_mul_f32_e32 v151, s70, v151\n" \
"v_cvt_pk_bf16_f32 v148, v148, v149\n" \
"v_mul_f32_e32 v158, s70, v158\n" \
"v_mul_f32_e32 v159, s70, v159\n" \
"v_cvt_pk_bf16_f32 v156, v156, v157\n" \
"v_permlane16_swap_b32_e32 v228, v244\n" \
"v_permlane16_swap_b32_e32 v193, v209\n" \
"v_mfma_f32_16x16x32_bf16 v[80:83], a[188:191], a[132:135], v[80:83]\n" \
"s_mov_b32 s72, 0x3eae1a93\n" \
"v_mul_f32_e32 v152, s70, v152\n" \
"v_mul_f32_e32 v153, s70, v153\n" \
"v_mul_f32_e32 v154, s70, v154\n" \
"v_mul_f32_e32 v155, s70, v155\n" \
"v_cvt_pk_bf16_f32 v149, v150, v151\n" \
"v_mul_f32_e32 v160, s70, v160\n" \
"v_mul_f32_e32 v161, s70, v161\n" \
"v_mul_f32_e32 v162, s70, v162\n" \
"v_mul_f32_e32 v163, s70, v163\n" \
"v_cvt_pk_bf16_f32 v157, v158, v159\n" \
"v_mul_f32_e32 v45, 0xbfb8aa3b, v228\n" \
"v_mfma_f32_16x16x16_bf16 v[48:51], v[182:183], v[46:47], 0\n" \
"v_mfma_f32_16x16x16_bf16 v[196:199], v[190:191], v[46:47], 0\n" \
"v_mfma_f32_16x16x16_bf16 v[212:215], v[206:207], v[46:47], 0\n" \
"v_max_f32_e64 v193, v193, v209\n" \
"v_cvt_pk_bf16_f32 v150, v152, v153\n" \
"v_cvt_pk_bf16_f32 v151, v154, v155\n" \
"v_cvt_pk_bf16_f32 v158, v160, v161\n" \
"v_cvt_pk_bf16_f32 v159, v162, v163\n" \
"v_mfma_f32_16x16x16_bf16 v[228:231], v[222:223], v[46:47], 0\n" \
"v_mul_f32_e32 v193, s72, v193\n" \
"v_mov_b32_e32 v209, v193\n" \
"ds_write_b128 v21, v[148:151] offset:24576\n" \
"v_mfma_f32_16x16x16_bf16 v[244:247], v[238:239], v[46:47], 0\n" \
"v_permlane16_swap_b32_e32 v193, v209\n" \
"s_mov_b32 s71, 0x3e0293ee\n" \
"v_mfma_f32_16x16x32_bf16 v[80:83], a[252:255], a[196:199], v[80:83]\n" \
"v_mov_b32_e32 v225, v193\n" \
"v_mov_b32_e32 v241, v209\n" \
"v_mul_f32_e32 v164, s70, v164\n" \
"v_mul_f32_e64 v165, s70, v165\n" \
"ds_write_b128 v21, v[156:159] offset:25600\n" \
"v_mul_f32_e32 v172, s70, v172\n" \
"v_mul_f32_e32 v173, s70, v173\n" \
"v_pk_mul_f32 v[84:85], v[84:85], s[70:71]\n" \
"v_pk_mul_f32 v[92:93], v[92:93], s[70:71]\n" \
"v_pk_mul_f32 v[100:101], v[100:101], s[70:71]\n" \
"v_pk_mul_f32 v[108:109], v[108:109], s[70:71]\n" \
"v_pk_mul_f32 v[52:53], v[52:53], s[70:71]\n" \
"v_pk_mul_f32 v[60:61], v[60:61], s[70:71]\n" \
"v_permlane32_swap_b32_e32 v193, v225\n" \
"v_permlane32_swap_b32_e32 v209, v241\n" \
"v_mul_f32_e32 v166, s70, v166\n" \
"v_mul_f32_e32 v167, s70, v167\n" \
"v_cvt_pk_bf16_f32 v164, v164, v165\n" \
"v_mul_f32_e32 v174, s70, v174\n" \
"v_mul_f32_e32 v175, s70, v175\n" \
"v_cvt_pk_bf16_f32 v172, v172, v173\n" \
"v_pk_mul_f32 v[86:87], v[86:87], s[70:71]\n" \
"v_cvt_pk_bf16_f32 v84, v84, v85\n" \
"v_pk_mul_f32 v[94:95], v[94:95], s[70:71]\n" \
"v_cvt_pk_bf16_f32 v92, v92, v93\n" \
"v_pk_mul_f32 v[102:103], v[102:103], s[70:71]\n" \
"v_cvt_pk_bf16_f32 v100, v100, v101\n" \
"v_pk_mul_f32 v[110:111], v[110:111], s[70:71]\n" \
"v_cvt_pk_bf16_f32 v108, v108, v109\n" \
"v_pk_mul_f32 v[54:55], v[54:55], s[70:71]\n" \
"v_cvt_pk_bf16_f32 v52, v52, v53\n" \
"v_pk_mul_f32 v[62:63], v[62:63], s[70:71]\n" \
"v_cvt_pk_bf16_f32 v60, v60, v61\n" \
"v_fma_f32 v192, v192, s72, -v193\n" \
"v_fma_f32 v194, v194, s72, -v193\n" \
"v_fma_f32 v208, v208, s72, -v209\n" \
"v_fma_f32 v210, v210, s72, -v209\n" \
"v_mul_f32_e32 v168, s70, v168\n" \
"v_mul_f32_e32 v169, s70, v169\n" \
"v_mul_f32_e32 v170, s70, v170\n" \
"v_mul_f32_e32 v171, s70, v171\n" \
"v_cvt_pk_bf16_f32 v165, v166, v167\n" \
"v_mul_f32_e32 v176, s70, v176\n" \
"v_mul_f32_e32 v177, s70, v177\n" \
"v_mul_f32_e32 v178, s70, v178\n" \
"v_mul_f32_e32 v179, s70, v179\n" \
"v_cvt_pk_bf16_f32 v173, v174, v175\n" \
"v_pk_mul_f32 v[88:89], v[88:89], s[70:71]\n" \
"v_pk_mul_f32 v[90:91], v[90:91], s[70:71]\n" \
"v_cvt_pk_bf16_f32 v85, v86, v87\n" \
"v_pk_mul_f32 v[96:97], v[96:97], s[70:71]\n" \
"v_pk_mul_f32 v[98:99], v[98:99], s[70:71]\n" \
"v_cvt_pk_bf16_f32 v93, v94, v95\n" \
"v_pk_mul_f32 v[104:105], v[104:105], s[70:71]\n" \
"v_pk_mul_f32 v[106:107], v[106:107], s[70:71]\n" \
"v_cvt_pk_bf16_f32 v101, v102, v103\n" \
"v_pk_mul_f32 v[112:113], v[112:113], s[70:71]\n" \
"v_pk_mul_f32 v[114:115], v[114:115], s[70:71]\n" \
"v_cvt_pk_bf16_f32 v109, v110, v111\n" \
"v_pk_mul_f32 v[56:57], v[56:57], s[70:71]\n" \
"v_pk_mul_f32 v[58:59], v[58:59], s[70:71]\n" \
"v_cvt_pk_bf16_f32 v53, v54, v55\n" \
"v_pk_mul_f32 v[64:65], v[64:65], s[70:71]\n" \
"v_pk_mul_f32 v[66:67], v[66:67], s[70:71]\n" \
"v_cvt_pk_bf16_f32 v61, v62, v63\n" \
"v_pk_mul_f32 v[80:81], v[80:81], s[70:71]\n" \
"v_pk_mul_f32 v[82:83], v[82:83], s[70:71]\n" \
"v_exp_f32_e32 v192, v192\n" \
"v_exp_f32_e32 v194, v194\n" \
"v_exp_f32_e32 v208, v208\n" \
"v_exp_f32_e32 v210, v210\n" \
"v_fma_f32 v224, v224, s72, -v225\n" \
"v_fma_f32 v226, v226, s72, -v225\n" \
"v_mfma_f32_16x16x32_bf16 v[76:79], a[184:187], a[132:135], v[76:79]\n" \
"v_cvt_pk_bf16_f32 v166, v168, v169\n" \
"v_cvt_pk_bf16_f32 v167, v170, v171\n" \
"v_cvt_pk_bf16_f32 v174, v176, v177\n" \
"v_cvt_pk_bf16_f32 v175, v178, v179\n" \
"v_cvt_pk_bf16_f32 v86, v88, v89\n" \
"v_cvt_pk_bf16_f32 v87, v90, v91\n" \
"v_cvt_pk_bf16_f32 v94, v96, v97\n" \
"v_cvt_pk_bf16_f32 v95, v98, v99\n" \
"v_cvt_pk_bf16_f32 v102, v104, v105\n" \
"v_cvt_pk_bf16_f32 v103, v106, v107\n" \
"v_cvt_pk_bf16_f32 v110, v112, v113\n" \
"v_cvt_pk_bf16_f32 v111, v114, v115\n" \
"v_cvt_pk_bf16_f32 v54, v56, v57\n" \
"v_cvt_pk_bf16_f32 v55, v58, v59\n" \
"v_cvt_pk_bf16_f32 v62, v64, v65\n" \
"v_cvt_pk_bf16_f32 v63, v66, v67\n" \
"v_exp_f32_e32 v224, v224\n" \
"v_exp_f32_e32 v226, v226\n" \
"v_fma_f32 v240, v240, s72, -v241\n" \
"v_fma_f32 v242, v242, s72, -v241\n" \
"ds_write_b128 v21, v[164:167] offset:28672\n" \
"v_cvt_pk_bf16_f32 v192, v192, v194\n" \
"v_cvt_pk_bf16_f32 v208, v208, v210\n" \
"v_exp_f32_e32 v240, v240\n" \
"v_exp_f32_e32 v242, v242\n" \
"v_cvt_pk_bf16_f32 v132, v48, v49\n" \
"v_cvt_pk_bf16_f32 v133, v50, v51\n" \
"v_cvt_pk_bf16_f32 v134, v196, v197\n" \
"v_cvt_pk_bf16_f32 v135, v198, v199\n" \
"ds_write_b128 v21, v[172:175] offset:29696\n" \
"v_cvt_pk_bf16_f32 v136, v212, v213\n" \
"v_cvt_pk_bf16_f32 v137, v214, v215\n" \
"v_perm_b32 v184, v192, v192, v40\n" \
"v_perm_b32 v185, v192, v192, v41\n" \
"v_perm_b32 v186, v192, v192, v42\n" \
"v_perm_b32 v187, v192, v192, v43\n" \
"v_perm_b32 v200, v208, v208, v40\n" \
"v_perm_b32 v201, v208, v208, v41\n" \
"v_perm_b32 v202, v208, v208, v42\n" \
"v_perm_b32 v203, v208, v208, v43\n" \
"ds_write_b128 v27, v[84:87] offset:8192\n" \
"v_cvt_pk_bf16_f32 v224, v224, v226\n" \
"v_cvt_pk_bf16_f32 v138, v228, v229\n" \
"v_cvt_pk_bf16_f32 v139, v230, v231\n" \
"v_mfma_f32_16x16x32_bf16 v[32:35], v[132:135], v[184:187], 0\n" \
"v_mfma_f32_16x16x32_bf16 v[36:39], v[134:137], v[200:203], 0\n" \
"v_perm_b32 v216, v224, v224, v40\n" \
"v_perm_b32 v217, v224, v224, v41\n" \
"ds_write_b128 v27, v[92:95] offset:9216\n" \
"v_perm_b32 v218, v224, v224, v42\n" \
"v_perm_b32 v219, v224, v224, v43\n" \
"v_cvt_pk_bf16_f32 v240, v240, v242\n" \
"v_mfma_f32_16x16x32_bf16 v[76:79], a[248:251], a[196:199], v[76:79]\n" \
"v_cvt_pk_bf16_f32 v140, v244, v245\n" \
"v_cvt_pk_bf16_f32 v141, v246, v247\n" \
"v_mfma_f32_16x16x32_bf16 v[248:251], v[136:139], v[216:219], 0\n" \
"v_perm_b32 v232, v240, v240, v40\n" \
"v_perm_b32 v233, v240, v240, v41\n" \
"ds_write_b128 v27, v[100:103] offset:12288\n" \
"v_perm_b32 v234, v240, v240, v42\n" \
"v_perm_b32 v235, v240, v240, v43\n" \
"v_pk_mul_f32 v[76:77], v[76:77], s[70:71]\n" \
"v_pk_mul_f32 v[78:79], v[78:79], s[70:71]\n" \
"v_cvt_pk_bf16_f32 v76, v76, v77\n" \
"v_cvt_pk_bf16_f32 v77, v78, v79\n" \
"v_cvt_pk_bf16_f32 v78, v80, v81\n" \
"v_cvt_pk_bf16_f32 v79, v82, v83\n" \
"v_mfma_f32_16x16x32_bf16 v[128:131], v[138:141], v[232:235], 0\n" \
"ds_write_b128 v27, v[108:111] offset:13312\n" \
"v_mfma_f32_16x16x32_bf16 v[72:75], a[180:183], a[132:135], v[72:75]\n" \
"v_cvt_pk_bf16_f32 v116, v252, v253\n" \
"v_cvt_pk_bf16_f32 v117, v254, v255\n" \
"v_mfma_f32_16x16x32_bf16 v[72:75], a[244:247], a[196:199], v[72:75]\n" \
"v_exp_f32_e64 v45, v45\n" \
"v_mfma_f32_16x16x32_bf16 v[192:195], v[116:119], v[184:187], 0\n" \
"ds_write_b128 v27, v[52:55] offset:0\n" \
"v_mfma_f32_16x16x32_bf16 v[208:211], v[118:121], v[200:203], 0\n" \
"v_mfma_f32_16x16x32_bf16 v[224:227], v[120:123], v[216:219], 0\n" \
"v_mfma_f32_16x16x32_bf16 v[240:243], v[122:125], v[232:235], 0\n" \
"ds_write_b128 v27, v[60:63] offset:1024\n" \
"v_mov_b32_dpp v34, v38 quad_perm:[0,1,2,3] row_mask:0x2 bank_mask:0xf\n" \
"v_pk_mul_f32 v[72:73], v[72:73], s[70:71]\n" \
"v_pk_mul_f32 v[74:75], v[74:75], s[70:71]\n" \
"v_mov_b32_dpp v34, v250 quad_perm:[0,1,2,3] row_mask:0x4 bank_mask:0xf\n" \
"v_add_f32_e64 v45, 1.0, v45\n" \
"v_mfma_f32_16x16x32_bf16 v[68:71], a[176:179], a[132:135], v[68:71]\n" \
"v_mov_b32_dpp v34, v130 quad_perm:[0,1,2,3] row_mask:0x8 bank_mask:0xf\n" \
"v_mov_b32_dpp v32, v36 quad_perm:[0,1,2,3] row_mask:0x2 bank_mask:0xf\n" \
"v_rcp_f32_e64 v45, v45\n" \
"ds_write_b128 v27, v[76:79] offset:5120\n" \
"v_mov_b32_dpp v33, v37 quad_perm:[0,1,2,3] row_mask:0x2 bank_mask:0xf\n" \
"v_rcp_f32_e64 v34, v34\n" \
"v_mov_b32_dpp v32, v248 quad_perm:[0,1,2,3] row_mask:0x4 bank_mask:0xf\n" \
"v_mov_b32_dpp v33, v249 quad_perm:[0,1,2,3] row_mask:0x4 bank_mask:0xf\n" \
"v_mul_f32_e64 v34, v45, v34\n" \
"v_mov_b32_dpp v32, v128 quad_perm:[0,1,2,3] row_mask:0x8 bank_mask:0xf\n" \
"v_mov_b32_dpp v33, v129 quad_perm:[0,1,2,3] row_mask:0x8 bank_mask:0xf\n" \
"v_mul_f32_e32 v32, v34, v32\n" \
"v_mul_f32_e32 v33, v34, v33\n" \
"v_mov_b32_e64 v38, v34\n" \
"v_mfma_f32_16x16x32_bf16 v[68:71], a[240:243], a[196:199], v[68:71]\n" \
"v_sub_f32_e32 v44, 1.0, v45\n" \
"v_permlane16_swap_b32_e32 v34, v38\n" \
"v_mov_b32_e32 v250, v34\n" \
"v_mov_b32_e32 v130, v38\n" \
"v_cvt_pk_bf16_f32 v32, v32, v33\n" \
"v_permlane32_swap_b32_e32 v34, v250\n" \
"v_permlane32_swap_b32_e32 v38, v130\n" \
"v_mul_f32_e64 v192, v34, v192\n" \
"v_pk_mul_f32 v[68:69], v[68:69], s[70:71]\n" \
"v_pk_mul_f32 v[70:71], v[70:71], s[70:71]\n" \
"v_cvt_pk_bf16_f32 v68, v68, v69\n" \
"v_cvt_pk_bf16_f32 v69, v70, v71\n" \
"v_cvt_pk_bf16_f32 v70, v72, v73\n" \
"v_cvt_pk_bf16_f32 v71, v74, v75\n" \
"ds_write_b128 v27, v[68:71] offset:4096\n" \
"v_mul_f32_e32 v193, v34, v193\n" \
"v_mul_f32_e32 v208, v38, v208\n" \
"v_mul_f32_e32 v209, v38, v209\n" \
"v_mul_f32_e32 v224, v250, v224\n" \
"v_mul_f32_e32 v225, v250, v225\n" \
"v_mul_f32_e32 v240, v130, v240\n" \
"v_mul_f32_e32 v241, v130, v241\n" \
"v_mul_f32_e32 v194, v34, v194\n" \
"v_mul_f32_e64 v195, v34, v195\n" \
"v_cvt_pk_bf16_f32 v192, v192, v193\n" \
"v_mul_f32_e32 v210, v38, v210\n" \
"v_mul_f32_e32 v211, v38, v211\n" \
"v_cvt_pk_bf16_f32 v208, v208, v209\n" \
"v_mul_f32_e32 v226, v250, v226\n" \
"v_mul_f32_e32 v227, v250, v227\n" \
"v_cvt_pk_bf16_f32 v224, v224, v225\n" \
"v_mul_f32_e32 v242, v130, v242\n" \
"v_mul_f32_e32 v243, v130, v243\n" \
"v_cvt_pk_bf16_f32 v240, v240, v241\n" \
"v_cvt_pk_bf16_f32 v193, v194, v195\n" \
"s_add_u32 s84, s48, 0x0\n" \
"s_nop 0\n" \
"v_cvt_pk_bf16_f32 v209, v210, v211\n" \
"s_add_u32 s85, s48, 0x24800\n" \
"v_cvt_pk_bf16_f32 v225, v226, v227\n" \
"s_add_u32 s86, s48, 0x49000\n" \
"v_cvt_pk_bf16_f32 v241, v242, v243\n" \
"s_add_u32 s87, s48, 0x6d800\n" \
"ds_write_b32 v26, v44\n" \
"buffer_store_dword v32, v25, s[80:83], s49 offen sc1\n" \
"buffer_store_dwordx2 v[192:193], v24, s[80:83], s84 offen sc1\n" \
"buffer_store_dwordx2 v[208:209], v24, s[80:83], s85 offen sc1\n" \
"buffer_store_dwordx2 v[224:225], v24, s[80:83], s86 offen sc1\n" \
"buffer_store_dwordx2 v[240:241], v24, s[80:83], s87 offen sc1\n" \
"s_branch L_end_%=\n" \
"L_poll61_%=:\n" \
"s_mov_b32 s84, 0\n" \
"L_pl61_%=:\n" \
"s_sleep 1\n" \
"v_bfe_u32 v44, v20, 2, 5\n" \
"buffer_load_dword v44, v44, s[80:83], s38 offen offset:128 sc1\n" \
"s_waitcnt vmcnt(0)\n" \
"v_cmp_ne_u32_e32 vcc, s62, v44\n" \
"s_add_u32 s84, s84, 1\n" \
"s_nop 3\n" \
"s_cbranch_vccz L_pollr61_%=\n" \
"s_cmp_gt_u32 s84, 0x400000\n" \
"s_cbranch_scc0 L_pl61_%=\n" \
"s_branch L_pollr61_%=\n" \
"L_poll161_%=:\n" \
"s_mov_b32 s84, 0\n" \
"L_pl161_%=:\n" \
"s_sleep 1\n" \
"v_bfe_u32 v44, v20, 2, 5\n" \
"buffer_load_dword v44, v44, s[80:83], s38 offen offset:0 sc1\n" \
"s_waitcnt vmcnt(0)\n" \
"v_cmp_ne_u32_e32 vcc, s62, v44\n" \
"s_add_u32 s84, s84, 1\n" \
"s_nop 3\n" \
"s_cbranch_vccz L_pollr161_%=\n" \
"s_cmp_gt_u32 s84, 0x400000\n" \
"s_cbranch_scc0 L_pl161_%=\n" \
"s_branch L_pollr161_%=\n" \
"L_end_%=:\n"
#define PROJ_VCLOB "v21","v24","v25","v28","v29","v32","v33","v34","v35","v36","v37","v38","v39","v40","v41","v42","v43","v44","v45","v46","v47","v48","v49","v50","v51","v52","v53","v54","v55","v56","v57","v58","v59","v60","v61","v62","v63","v64","v65","v66","v67","v68","v69","v70","v71","v72","v73","v74","v75","v76","v77","v78","v79","v80","v81","v82","v83","v84","v85","v86","v87","v88","v89","v90","v91","v92","v93","v94","v95","v96","v97","v98","v99","v100","v101","v102","v103","v104","v105","v106","v107","v108","v109","v110","v111","v112","v113","v114","v115","v116","v117","v118","v119","v120","v121","v122","v123","v124","v125","v126","v127","v128","v129","v130","v131","v132","v133","v134","v135","v136","v137","v138","v139","v140","v141","v142","v143","v144","v145","v146","v147","v148","v149","v150","v151","v152","v153","v154","v155","v156","v157","v158","v159","v160","v161","v162","v163","v164","v165","v166","v167","v168","v169","v170","v171","v172","v173","v174","v175","v176","v177","v178","v179","v180","v181","v182","v183","v184","v185","v186","v187","v188","v189","v190","v192","v193","v194","v195","v196","v197","v198","v199","v200","v201","v202","v203","v204","v205","v206","v207","v208","v209","v210","v211","v212","v213","v214","v215","v216","v217","v218","v219","v220","v221","v222","v223","v224","v225","v226","v227","v228","v229","v230","v231","v232","v233","v234","v235","v236","v237","v238","v239","v240","v241","v242","v243","v244","v245","v246","v247","v248","v249","v250","v251","v252","v253","v254","v255"
#define PROJ_SCLOB "s4","s5","s6","s7","s32","s33","s34","s35","s36","s37","s38","s39","s40","s41","s42","s43","s44","s45","s46","s47","s48","s49","s50","s51","s52","s53","s54","s55","s63","s64","s65","s66","s67","s68","s69","s70","s71","s72","s73","s74","s75","s76","s77","s78","s79","s80","s81","s82","s83","s84","s85","s86","s87","s88","s89","s90","s91","s92","s93","s94","s95","s96","s97","s98","s99","s100","s101"

typedef const u16* const __attribute__((address_space(4)))* kaptr;
__device__ __forceinline__ void proj_phase(kaptr& ka, char*& ws, const unsigned wid, u32x4 (&vp)[5], unsigned& vl, u32x4 (&sa)[5], u32x2 s24, u32x2 s28, unsigned tdum, u32x4& srdA, u32x2& sd, unsigned& tag
    ) {
    const unsigned b = blockIdx.x, h = (b & 7) * 4 + ((b >> 3) & 3), mt = b >> 5;
    const unsigned rb = (wid < 2u ? 15u - mt : mt) * 128u;
    const unsigned lane = threadIdx.x & 63u, wc = wid & 1u;
    u32x2 vAc, vB;
#pragma unroll
    for (int par = 0; par < 2; ++par) {
        const unsigned lr = lane >> 3, cc = (lane & 7u) ^ (4u * par + (lr >> 1)), r0 = rb + wc * 64u + lr;
        vAc[par] = r0 * (DC * 2) + cc * 16u;
        const unsigned br = lane >> 4, p16 = lane & 15u, sc = (((p16 >> 1) ^ (br | (par << 2))) << 1) | (p16 & 1u);
        vB[par] = (16u * wid + br) * (NBIG * 2) + h * (HD * 2) + sc * 16u;
    }
    vl = lane * 16u;
    u32x2 s30 = {wid * 8192u, 65536u + wid * 4096u};
    asm volatile(PROJ_ASM
                 : "={s62}"(tag), "={s[56:59]}"(srdA), "={s[60:61]}"(sd), "+{s[8:11]}"(sa[0]), "+{s[12:15]}"(sa[1]), "+{s[16:19]}"(sa[2]), "+{s[20:23]}"(sa[3]), "+{s[24:25]}"(s24), "+{s[28:29]}"(s28), "+{s[30:31]}"(s30),
                   "={v[0:3]}"(vp[0]), "={v[4:7]}"(vp[1]), "={v[8:11]}"(vp[2]), "={v[12:15]}"(vp[3]), "={v[16:19]}"(vp[4]), "+{v[22:23]}"(vAc), "+{v[26:27]}"(vB), "+{v191}"(tdum), "+{s[26:27]}"(ws), "+{s[0:1]}"(ka)
                 : "{s2}"(b), "{s3}"(wid), "{v20}"(vl)
                 : "memory", "m0", "vcc", "scc", PROJ_VCLOB, PROJ_SCLOB, ACLOB_LO, ACLOB_HI);
    sa[4] = (u32x4){s24[0], 0u, 0u, 0u};
}

namespace att {
#define DI __device__ __forceinline__
typedef __bf16 bfx8 __attribute__((ext_vector_type(8)));
DI unsigned rfl(unsigned x) { return __builtin_amdgcn_readfirstlane(x); }
DI f32x4 mfma(u32x4 a, u32x4 b, f32x4 c) { return __builtin_amdgcn_mfma_f32_16x16x32_bf16(__builtin_bit_cast(bfx8, a), __builtin_bit_cast(bfx8, b), c, 0, 0, 0); }
typedef __bf16 bfx2 __attribute__((ext_vector_type(2)));
typedef float f32x2 __attribute__((ext_vector_type(2)));
DI unsigned cvtpk2(float lo, float hi) { f32x2 v = {lo, hi}; bfx2 b = __builtin_convertvector(v, bfx2); return __builtin_bit_cast(unsigned, b); }
DI void att_exact(const u16* __restrict__ x, const u16* __restrict__ wq, char* __restrict__ ws, unsigned h, unsigned r0, float g1) {
    const unsigned lane = lane_id(), g = lane >> 4, c = lane & 15u;
    f32x4 o[8];
#pragma unroll
    for (int n = 0; n < 8; ++n) o[n] = (f32x4){0.f, 0.f, 0.f, 0.f};
#pragma unroll 1
    for (unsigned kk = 0; kk < (unsigned)DM; kk += 32u) {
        const unsigned k = kk + 8u * g;
        const u32x4 xf = *reinterpret_cast<const u32x4*>(x + (r0 + c) * DM + k);
#pragma unroll
        for (int n = 0; n < 8; ++n) {
            const u16* w = wq + k * NBIG + h * HD + n * 16 + c; u32x4 wf;
#pragma unroll
            for (int e = 0; e < 4; ++e) wf[e] = (unsigned)w[2 * e * NBIG] | ((unsigned)w[(2 * e + 1) * NBIG] << 16);
            o[n] = mfma(wf, xf, o[n]);
        }
    }
    u32x4 q[4];
#pragma unroll
    for (int p = 0; p < 4; ++p) {
        const f32x4 a = o[2 * p] * 0.12751743082f, b = o[2 * p + 1] * 0.12751743082f;
        q[p] = (u32x4){cvtpk2(a[0], a[1]), cvtpk2(a[2], a[3]), cvtpk2(b[0], b[1]), cvtpk2(b[2], b[3])};
    }
#pragma unroll
    for (int n = 0; n < 8; ++n) o[n] = (f32x4){0.f, 0.f, 0.f, 0.f};
    const __amdgpu_buffer_rsrc_t srd = __builtin_amdgcn_make_buffer_rsrc((void*)(ws + WS_K + (size_t)h * 1048576), (short)0, 1048576, 0x00020000);
    float m = -3.0e38f, l = 0.f;
#pragma unroll 1
    for (unsigned k0 = 0; k0 <= r0 + 15u; k0 += 32u) {
        const unsigned tb = (k0 >> 6) * 32768u + (k0 & 32u) * 256u;
        f32x4 s[2]; float mx = m;
#pragma unroll
        for (int kt = 0; kt < 2; ++kt) {
            s[kt] = (f32x4){0.f, 0.f, 0.f, 0.f};
#pragma unroll
            for (int p = 0; p < 4; ++p) s[kt] = mfma(__builtin_amdgcn_raw_buffer_load_b128(srd, lane * 16u + (kt * 4 + p) * 1024u, tb, 16), q[p], s[kt]);
#pragma unroll
            for (int e = 0; e < 4; ++e) { if (k0 + 16u * kt + 4u * g + e > r0 + c) s[kt][e] = -3.0e38f; mx = fmaxf(mx, s[kt][e]); }
        }
        mx = fmaxf(mx, __shfl_xor(mx, 16)); mx = fmaxf(mx, __shfl_xor(mx, 32));
        const float al = __builtin_amdgcn_exp2f(m - mx); m = mx;
        u32x4 pf;
#pragma unroll
        for (int e = 0; e < 4; ++e) pf[e] = cvtpk2(__builtin_amdgcn_exp2f(s[e >> 1][2 * (e & 1)] - m), __builtin_amdgcn_exp2f(s[e >> 1][2 * (e & 1) + 1] - m));
        l *= al;
#pragma unroll
        for (int e = 0; e < 4; ++e) l += __uint_as_float(pf[e] << 16) + __uint_as_float(pf[e] & 0xffff0000u);
#pragma unroll
        for (int n = 0; n < 8; ++n) o[n] = mfma(__builtin_amdgcn_raw_buffer_load_b128(srd, lane * 16u + 16384u + n * 1024u, tb, 16), pf, o[n] * al);
    }
    l += __shfl_xor(l, 16); l += __shfl_xor(l, 32);
    const float f = g1 / l;
    const __amdgpu_buffer_rsrc_t srdA = __builtin_amdgcn_make_buffer_rsrc((void*)(ws + WS_AF), (short)0, 128 * KS_TOT * 1024, 0x00020000);
#pragma unroll
    for (int p = 0; p < 4; ++p) {
        const f32x4 a = o[2 * p] * f, b = o[2 * p + 1] * f;
        __builtin_amdgcn_raw_buffer_store_b128((u32x4){cvtpk2(a[0], a[1]), cvtpk2(a[2], a[3]), cvtpk2(b[0], b[1]), cvtpk2(b[2], b[3])}, srdA, lane * 16u + p * 1024u, ((r0 >> 4) * KS_TOT + 18u + 4u * h) * 1024u, 16);
    }
}
}

#define ATT_ASM \
"s_branch L_ago_%=\n" \
".p2align 12\n" \
"L_ab:\n" \
".fill (L_ab + 0 - .) / 4, 4, 0xBF800000\n" \
"L_ap0:\n" \
"s_setpc_b64 s[88:89]\n" \
"L_ago_%=:\n" \
"s_mov_b32 s101, 0\n" \
"s_waitcnt vmcnt(15) lgkmcnt(0)\n" \
"s_barrier\n" \
"v_accvgpr_write_b32 a216, 0\n" \
"v_accvgpr_write_b32 a217, 0\n" \
"v_accvgpr_write_b32 a218, 0\n" \
"v_accvgpr_write_b32 a219, 0\n" \
"v_bfe_u32 v61, v20, 2, 5\n" \
"s_mov_b32 s96, s24\n" \
"v_mfma_f32_32x32x16_bf16 v[128:143], a[216:219], a[216:219], 0\n" \
"buffer_load_dword v57, v61, s[56:59], s96 offen sc1\n" \
"v_and_b32_e32 v63, 0x300, v20\n" \
"ds_read_b128 v[96:99], v63 offset:0\n" \
"v_add_u32_e32 v58, s22, v20\n" \
"ds_read_b128 a[0:3], v58 offset:0\n" \
"v_mfma_f32_32x32x16_bf16 v[144:159], a[216:219], a[216:219], 0\n" \
"ds_read_b128 a[16:19], v58 offset:8192\n" \
"v_add_u32_e32 v59, s23, v20\n" \
"ds_read_b128 a[32:35], v59 offset:0\n" \
"ds_read_b128 a[48:51], v59 offset:8192\n" \
"ds_read_b128 v[100:103], v63 offset:1024\n" \
"ds_read_b128 a[4:7], v58 offset:1024\n" \
"v_mfma_f32_32x32x16_bf16 v[160:175], a[216:219], a[216:219], 0\n" \
"ds_read_b128 a[20:23], v58 offset:9216\n" \
"ds_read_b128 a[36:39], v59 offset:1024\n" \
"ds_read_b128 a[52:55], v59 offset:9216\n" \
"s_sub_u32 s67, s12, 0xc000\n" \
"ds_read_b128 v[104:107], v63 offset:2048\n" \
"s_mov_b32 s65, s12\n" \
"v_mfma_f32_32x32x16_bf16 v[176:191], a[216:219], a[216:219], 0\n" \
"v_add_u32_e32 v55, s67, v20\n" \
"v_add_u32_e32 v52, 0xc000, v55\n" \
"s_xor_b32 m0, s65, 0x10000\n" \
"ds_read_b128 a[8:11], v58 offset:2048\n" \
"buffer_load_dwordx4 v52, s[8:11], 0 offen offset:0 sc1 lds\n" \
"v_mfma_f32_32x32x16_bf16 v[192:207], a[216:219], a[216:219], 0\n" \
"ds_read_b128 a[24:27], v58 offset:10240\n" \
"ds_read_b128 a[40:43], v59 offset:2048\n" \
"ds_read_b128 a[56:59], v59 offset:10240\n" \
"ds_read_b128 v[108:111], v63 offset:3072\n" \
"buffer_load_dwordx4 v52, s[8:11], 0 offen offset:1024 sc1 lds\n" \
"v_mfma_f32_32x32x16_bf16 v[208:223], a[216:219], a[216:219], 0\n" \
"ds_read_b128 a[12:15], v58 offset:3072\n" \
"ds_read_b128 a[28:31], v58 offset:11264\n" \
"ds_read_b128 a[44:47], v59 offset:3072\n" \
"ds_read_b128 a[60:63], v59 offset:11264\n" \
"s_waitcnt lgkmcnt(15)\n" \
"v_xor_b32_e32 v96, 0x80008000, v96\n" \
"v_mfma_f32_32x32x16_bf16 v[224:239], a[216:219], a[216:219], 0\n" \
"v_xor_b32_e32 v97, 0x80008000, v97\n" \
"v_xor_b32_e32 v98, 0x80008000, v98\n" \
"v_xor_b32_e32 v99, 0x80008000, v99\n" \
"ds_read_b128 a[160:163], v20 offset:0\n" \
"ds_read_b128 a[164:167], v20 offset:1024\n" \
"s_waitcnt lgkmcnt(15)\n" \
"v_xor_b32_e32 v100, 0x80008000, v100\n" \
"v_mfma_f32_32x32x16_bf16 v[240:255], a[216:219], a[216:219], 0\n" \
"v_xor_b32_e32 v101, 0x80008000, v101\n" \
"v_xor_b32_e32 v102, 0x80008000, v102\n" \
"v_xor_b32_e32 v103, 0x80008000, v103\n" \
"ds_read_b128 a[168:171], v20 offset:2048\n" \
"ds_read_b128 a[172:175], v20 offset:3072\n" \
"ds_read_b128 a[188:191], v20 offset:7168\n" \
"v_mfma_f32_16x16x32_bf16 a[192:195], a[216:219], a[216:219], 0\n" \
"ds_read_b128 a[184:187], v20 offset:6144\n" \
"s_waitcnt lgkmcnt(15)\n" \
"v_xor_b32_e32 v104, 0x80008000, v104\n" \
"v_mfma_f32_16x16x32_bf16 a[196:199], a[216:219], a[216:219], 0\n" \
"v_xor_b32_e32 v105, 0x80008000, v105\n" \
"v_xor_b32_e32 v106, 0x80008000, v106\n" \
"v_mfma_f32_16x16x32_bf16 v[32:35], v[96:99], a[0:3], 0\n" \
"v_xor_b32_e32 v107, 0x80008000, v107\n" \
"ds_read_b128 a[180:183], v20 offset:5120\n" \
"v_mfma_f32_16x16x32_bf16 v[36:39], v[96:99], a[16:19], 0\n" \
"ds_read_b128 a[176:179], v20 offset:4096\n" \
"s_waitcnt lgkmcnt(12)\n" \
"v_xor_b32_e32 v108, 0x80008000, v108\n" \
"v_mfma_f32_16x16x32_bf16 v[40:43], v[96:99], a[32:35], 0\n" \
"v_xor_b32_e32 v109, 0x80008000, v109\n" \
"v_xor_b32_e32 v110, 0x80008000, v110\n" \
"v_mfma_f32_16x16x32_bf16 v[44:47], v[96:99], a[48:51], 0\n" \
"v_xor_b32_e32 v111, 0x80008000, v111\n" \
"v_add_u32_e32 v60, s15, v20\n" \
"v_mfma_f32_16x16x32_bf16 v[32:35], v[100:103], a[4:7], v[32:35]\n" \
"v_add_u32_e32 v60, 0x10000, v60\n" \
"ds_read_b128 a[128:131], v20 offset:8192\n" \
"v_mfma_f32_16x16x32_bf16 v[36:39], v[100:103], a[20:23], v[36:39]\n" \
"ds_read_b128 a[144:147], v20 offset:12288\n" \
"ds_read_b128 a[132:135], v20 offset:9216\n" \
"v_mfma_f32_16x16x32_bf16 v[40:43], v[100:103], a[36:39], v[40:43]\n" \
"ds_read_b128 a[148:151], v20 offset:13312\n" \
"ds_read_b128 a[136:139], v20 offset:10240\n" \
"v_mfma_f32_16x16x32_bf16 v[44:47], v[100:103], a[52:55], v[44:47]\n" \
"ds_read_b128 a[152:155], v20 offset:14336\n" \
"ds_read_b128 a[140:143], v20 offset:11264\n" \
"v_mfma_f32_16x16x32_bf16 v[32:35], v[104:107], a[8:11], v[32:35]\n" \
"ds_read_b128 a[156:159], v20 offset:15360\n" \
"ds_read_b128 a[64:67], v60 offset:16384\n" \
"v_mfma_f32_16x16x32_bf16 v[36:39], v[104:107], a[24:27], v[36:39]\n" \
"ds_read_b128 a[68:71], v60 offset:17408\n" \
"ds_read_b128 a[72:75], v60 offset:18432\n" \
"v_mfma_f32_16x16x32_bf16 v[40:43], v[104:107], a[40:43], v[40:43]\n" \
"ds_read_b128 a[76:79], v60 offset:19456\n" \
"ds_read_b128 a[80:83], v60 offset:24576\n" \
"v_mfma_f32_16x16x32_bf16 v[44:47], v[104:107], a[56:59], v[44:47]\n" \
"ds_read_b128 a[84:87], v60 offset:25600\n" \
"s_waitcnt lgkmcnt(15)\n" \
"v_mfma_f32_16x16x32_bf16 v[32:35], v[108:111], a[12:15], v[32:35]\n" \
"ds_read_b128 a[88:91], v60 offset:26624\n" \
"ds_read_b128 a[92:95], v60 offset:27648\n" \
"v_mfma_f32_16x16x32_bf16 v[36:39], v[108:111], a[28:31], v[36:39]\n" \
"ds_read_b128 a[96:99], v60 offset:0\n" \
"ds_read_b128 a[100:103], v60 offset:1024\n" \
"v_mfma_f32_16x16x32_bf16 v[40:43], v[108:111], a[44:47], v[40:43]\n" \
"ds_read_b128 a[104:107], v60 offset:2048\n" \
"ds_read_b128 a[108:111], v60 offset:3072\n" \
"v_mfma_f32_16x16x32_bf16 v[44:47], v[108:111], a[60:63], v[44:47]\n" \
"ds_read_b128 a[112:115], v60 offset:8192\n" \
"ds_read_b128 a[116:119], v60 offset:9216\n" \
"v_mfma_f32_16x16x32_bf16 v[64:67], a[160:163], a[0:3], v[32:35]\n" \
"ds_read_b128 a[120:123], v60 offset:10240\n" \
"ds_read_b128 a[124:127], v60 offset:11264\n" \
"v_mfma_f32_16x16x32_bf16 v[64:67], a[164:167], a[4:7], v[64:67]\n" \
"v_bfe_u32 v26, v20, 8, 2\n" \
"s_mov_b32 m0, s20\n" \
"v_mfma_f32_16x16x32_bf16 v[64:67], a[168:171], a[8:11], v[64:67]\n" \
"v_bfe_u32 v25, v20, 5, 3\n" \
"v_lshlrev_b32_e32 v26, 1, v26\n" \
"v_mfma_f32_16x16x32_bf16 v[64:67], a[172:175], a[12:15], v[64:67]\n" \
"global_load_lds_dword v[16:17], off\n" \
"v_mfma_f32_16x16x32_bf16 v[68:71], a[188:191], a[12:15], v[32:35]\n" \
"v_sub_u32_e32 v25, v25, v26\n" \
"v_and_b32_e32 v26, 16, v20\n" \
"v_mfma_f32_16x16x32_bf16 v[68:71], a[184:187], a[8:11], v[68:71]\n" \
"v_mov_b32_e32 v27, 0x3f80\n" \
"v_lshlrev_b32_e32 v26, v26, v27\n" \
"v_mfma_f32_16x16x32_bf16 v[68:71], a[180:183], a[4:7], v[68:71]\n" \
"v_mov_b32_e32 v54, 0x3f803f80\n" \
"s_waitcnt lgkmcnt(15)\n" \
"v_mfma_f32_16x16x32_bf16 v[68:71], a[176:179], a[0:3], v[68:71]\n" \
"s_mov_b32 s84, 0x33333333\n" \
"s_mov_b32 s85, 0xcccccccc\n" \
"v_mfma_f32_16x16x32_bf16 v[76:79], a[176:179], a[16:19], v[36:39]\n" \
"s_mov_b32 s86, 0x11111111\n" \
"s_mov_b32 s87, 0x11111111\n" \
"v_mfma_f32_16x16x32_bf16 v[76:79], a[180:183], a[20:23], v[76:79]\n" \
"s_mov_b32 s88, 0x44444444\n" \
"s_mov_b32 s89, 0x44444444\n" \
"v_mfma_f32_16x16x32_bf16 v[76:79], a[184:187], a[24:27], v[76:79]\n" \
"v_exp_f32_e32 v70, v70\n" \
"v_mfma_f32_16x16x32_bf16 v[76:79], a[188:191], a[28:31], v[76:79]\n" \
"v_exp_f32_e32 v71, v71\n" \
"v_mfma_f32_16x16x32_bf16 v[72:75], a[172:175], a[28:31], v[36:39]\n" \
"v_exp_f32_e32 v68, v68\n" \
"v_mfma_f32_16x16x32_bf16 v[72:75], a[168:171], a[24:27], v[72:75]\n" \
"v_exp_f32_e32 v69, v69\n" \
"v_mfma_f32_16x16x32_bf16 v[72:75], a[164:167], a[20:23], v[72:75]\n" \
"v_bfe_i32 v21, v20, 4, 1\n" \
"v_cvt_pk_bf16_f32 v71, v70, v71\n" \
"v_mfma_f32_16x16x32_bf16 v[72:75], a[160:163], a[16:19], v[72:75]\n" \
"v_exp_f32_e32 v66, v66\n" \
"v_mfma_f32_16x16x32_bf16 v[80:83], a[160:163], a[32:35], v[40:43]\n" \
"v_exp_f32_e32 v67, v67\n" \
"v_mfma_f32_16x16x32_bf16 v[80:83], a[164:167], a[36:39], v[80:83]\n" \
"v_exp_f32_e32 v64, v64\n" \
"v_mfma_f32_16x16x32_bf16 v[80:83], a[168:171], a[40:43], v[80:83]\n" \
"v_exp_f32_e32 v65, v65\n" \
"v_mfma_f32_16x16x32_bf16 v[80:83], a[172:175], a[44:47], v[80:83]\n" \
"v_exp_f32_e32 v72, v72\n" \
"v_mfma_f32_16x16x32_bf16 v[84:87], a[188:191], a[44:47], v[40:43]\n" \
"s_waitcnt vmcnt(3)\n" \
"v_cmp_ne_u32_e32 vcc, s62, v57\n" \
"v_mfma_f32_16x16x32_bf16 v[84:87], a[184:187], a[40:43], v[84:87]\n" \
"v_exp_f32_e32 v73, v73\n" \
"v_mfma_f32_16x16x32_bf16 v[84:87], a[180:183], a[36:39], v[84:87]\n" \
"v_exp_f32_e32 v74, v74\n" \
"v_mfma_f32_16x16x32_bf16 v[84:87], a[176:179], a[32:35], v[84:87]\n" \
"s_cbranch_vccnz L_skb_%=\n" \
"L_rkb_%=:\n" \
"v_cmp_eq_u32_e32 vcc, 0, v25\n" \
"v_mfma_f32_16x16x32_bf16 v[92:95], a[176:179], a[48:51], v[44:47]\n" \
"v_exp_f32_e32 v75, v75\n" \
"v_mfma_f32_16x16x32_bf16 v[92:95], a[180:183], a[52:55], v[92:95]\n" \
"v_cndmask_b32_e32 v28, 0, v26, vcc\n" \
"v_cvt_pk_bf16_f32 v72, v72, v73\n" \
"v_mfma_f32_16x16x32_bf16 v[92:95], a[184:187], a[56:59], v[92:95]\n" \
"v_exp_f32_e32 v76, v76\n" \
"v_mfma_f32_16x16x32_bf16 v[92:95], a[188:191], a[60:63], v[92:95]\n" \
"v_exp_f32_e32 v77, v77\n" \
"v_mfma_f32_16x16x32_bf16 v[88:91], a[172:175], a[60:63], v[44:47]\n" \
"v_exp_f32_e32 v78, v78\n" \
"v_mfma_f32_16x16x32_bf16 v[88:91], a[168:171], a[56:59], v[88:91]\n" \
"v_exp_f32_e32 v79, v79\n" \
"v_mfma_f32_16x16x32_bf16 v[88:91], a[164:167], a[52:55], v[88:91]\n" \
"v_cndmask_b32_e64 v22, 0, v54, s[84:85]\n" \
"v_cndmask_b32_e64 v23, 0, v54, s[86:87]\n" \
"v_mfma_f32_16x16x32_bf16 v[88:91], a[160:163], a[48:51], v[88:91]\n" \
"v_cndmask_b32_e64 v24, 0, v54, s[88:89]\n" \
"v_and_b32_e32 v21, 0xaaaaaaaa, v21\n" \
"s_waitcnt vmcnt(4) lgkmcnt(0)\n" \
"s_barrier\n" \
"v_cmp_eq_u32_e32 vcc, 1, v25\n" \
"v_cvt_pk_bf16_f32 v70, v68, v69\n" \
"v_cvt_pk_bf16_f32 v73, v74, v75\n" \
"v_mov_b32_e32 v62, 0\n" \
"v_accvgpr_write_b32 a208, 0\n" \
"v_accvgpr_write_b32 a209, 0\n" \
"v_accvgpr_write_b32 a210, 0\n" \
"v_accvgpr_write_b32 a211, 0\n" \
"v_accvgpr_write_b32 a200, v22\n" \
"v_accvgpr_write_b32 a201, v22\n" \
"v_accvgpr_write_b32 a202, v22\n" \
"v_accvgpr_write_b32 a203, v22\n" \
"v_accvgpr_write_b32 a204, v23\n" \
"v_accvgpr_write_b32 a205, v23\n" \
"v_accvgpr_write_b32 a206, v23\n" \
"v_accvgpr_write_b32 a207, v23\n" \
"v_accvgpr_write_b32 a212, v24\n" \
"v_accvgpr_write_b32 a213, v24\n" \
"v_accvgpr_write_b32 a214, v24\n" \
"v_accvgpr_write_b32 a215, v24\n" \
"v_or_b32_e32 v21, 0x44444444, v21\n" \
"s_mov_b32 s64, 0x8000\n" \
"v_add_u32_e32 v50, 0x6000, v55\n" \
"v_add_u32_e32 v51, 0xa000, v55\n" \
"v_add_u32_e32 v53, 0x10000, v55\n" \
"v_add_u32_e32 v48, 0xc000, v20\n" \
"v_add_u32_e32 v49, s17, v20\n" \
"v_cndmask_b32_e32 v29, 0, v26, vcc\n" \
"v_accvgpr_write_b32 a220, v96\n" \
"v_accvgpr_write_b32 a221, v97\n" \
"v_accvgpr_write_b32 a222, v98\n" \
"v_accvgpr_write_b32 a223, v99\n" \
"v_accvgpr_write_b32 a224, v100\n" \
"v_accvgpr_write_b32 a225, v101\n" \
"v_accvgpr_write_b32 a226, v102\n" \
"v_accvgpr_write_b32 a227, v103\n" \
"v_accvgpr_write_b32 a228, v104\n" \
"v_accvgpr_write_b32 a229, v105\n" \
"v_accvgpr_write_b32 a230, v106\n" \
"v_accvgpr_write_b32 a231, v107\n" \
"v_accvgpr_write_b32 a232, v108\n" \
"v_accvgpr_write_b32 a233, v109\n" \
"v_accvgpr_write_b32 a234, v110\n" \
"v_accvgpr_write_b32 a235, v111\n" \
"v_cvt_pk_bf16_f32 v69, v66, v67\n" \
"v_cvt_pk_bf16_f32 v68, v64, v65\n" \
"v_cvt_pk_bf16_f32 v74, v76, v77\n" \
"v_cvt_pk_bf16_f32 v75, v78, v79\n" \
"s_sub_u32 s66, s18, 1\n" \
"s_cbranch_scc1 L_tr_%=\n" \
".fill (L_ab + 1856 - .) / 4, 4, 0xBF800000\n" \
"L_1_%=:\n" \
"v_mfma_f32_16x16x32_bf16 v[96:99], a[128:131], a[0:3], v[32:35]\n" \
"s_xor_b32 m0, s65, 0x12000\n" \
"ds_read_b128 a[160:163], v49 offset:0\n" \
"v_mfma_f32_16x16x32_bf16 v[96:99], a[132:135], a[4:7], v[96:99]\n" \
"buffer_load_dwordx4 v50, s[8:11], s64 offen offset:0 sc1 lds\n" \
"v_mfma_f32_16x16x32_bf16 v[96:99], a[136:139], a[8:11], v[96:99]\n" \
"ds_read_b128 a[164:167], v49 offset:1024\n" \
"ds_read_b128 a[168:171], v49 offset:2048\n" \
"v_mfma_f32_16x16x32_bf16 v[96:99], a[140:143], a[12:15], v[96:99]\n" \
"ds_read_b128 a[172:175], v49 offset:3072\n" \
"ds_read_b128 a[176:179], v49 offset:4096\n" \
"v_mfma_f32_16x16x32_bf16 v[100:103], a[156:159], a[12:15], v[32:35]\n" \
"ds_read_b128 a[180:183], v49 offset:5120\n" \
"ds_read_b128 a[184:187], v49 offset:6144\n" \
"v_mfma_f32_16x16x32_bf16 v[100:103], a[152:155], a[8:11], v[100:103]\n" \
"v_exp_f32_e64 v86, v86\n" \
"v_mfma_f32_16x16x32_bf16 v[100:103], a[148:151], a[4:7], v[100:103]\n" \
"v_exp_f32_e64 v87, v87\n" \
"v_mfma_f32_16x16x32_bf16 v[100:103], a[144:147], a[0:3], v[100:103]\n" \
"ds_read_b128 a[188:191], v49 offset:7168\n" \
"v_cvt_pk_bf16_f32 v87, v86, v87\n" \
"v_mfma_f32_16x16x32_bf16 v[108:111], a[144:147], a[16:19], v[36:39]\n" \
"v_exp_f32_e64 v84, v84\n" \
"v_mfma_f32_16x16x32_bf16 v[108:111], a[148:151], a[20:23], v[108:111]\n" \
"v_exp_f32_e64 v85, v85\n" \
"v_mfma_f32_16x16x32_bf16 v[108:111], a[152:155], a[24:27], v[108:111]\n" \
"v_exp_f32_e64 v82, v82\n" \
"v_mfma_f32_16x16x32_bf16 v[108:111], a[156:159], a[28:31], v[108:111]\n" \
"v_exp_f32_e64 v83, v83\n" \
"v_mfma_f32_16x16x32_bf16 v[104:107], a[140:143], a[28:31], v[36:39]\n" \
"v_exp_f32_e64 v80, v80\n" \
"v_mfma_f32_16x16x32_bf16 v[104:107], a[136:139], a[24:27], v[104:107]\n" \
"v_exp_f32_e64 v81, v81\n" \
"v_mfma_f32_16x16x32_bf16 v[104:107], a[132:135], a[20:23], v[104:107]\n" \
"v_exp_f32_e64 v88, v88\n" \
"v_mfma_f32_16x16x32_bf16 v[104:107], a[128:131], a[16:19], v[104:107]\n" \
"buffer_load_dwordx4 v50, s[8:11], s64 offen offset:1024 sc1 lds\n" \
"v_mfma_f32_16x16x32_bf16 v[112:115], a[128:131], a[32:35], v[40:43]\n" \
"s_xor_b32 m0, s65, 0x6000\n" \
"v_cvt_pk_bf16_f32 v86, v84, v85\n" \
"v_mfma_f32_16x16x32_bf16 v[112:115], a[132:135], a[36:39], v[112:115]\n" \
"v_cvt_pk_bf16_f32 v85, v82, v83\n" \
"v_cvt_pk_bf16_f32 v84, v80, v81\n" \
"v_mfma_f32_16x16x32_bf16 v[112:115], a[136:139], a[40:43], v[112:115]\n" \
"v_exp_f32_e64 v89, v89\n" \
"v_mfma_f32_16x16x32_bf16 v[112:115], a[140:143], a[44:47], v[112:115]\n" \
"v_exp_f32_e64 v90, v90\n" \
"v_mfma_f32_16x16x32_bf16 v[116:119], a[156:159], a[44:47], v[40:43]\n" \
"v_exp_f32_e64 v91, v91\n" \
"v_mfma_f32_16x16x32_bf16 v[116:119], a[152:155], a[40:43], v[116:119]\n" \
"v_exp_f32_e64 v92, v92\n" \
"v_mfma_f32_16x16x32_bf16 v[116:119], a[148:151], a[36:39], v[116:119]\n" \
"v_exp_f32_e64 v93, v93\n" \
"v_mfma_f32_16x16x32_bf16 v[116:119], a[144:147], a[32:35], v[116:119]\n" \
"v_exp_f32_e64 v94, v94\n" \
"v_mfma_f32_16x16x32_bf16 v[124:127], a[144:147], a[48:51], v[44:47]\n" \
"v_cvt_pk_bf16_f32 v88, v88, v89\n" \
"v_cvt_pk_bf16_f32 v89, v90, v91\n" \
"v_mfma_f32_16x16x32_bf16 v[124:127], a[148:151], a[52:55], v[124:127]\n" \
"v_exp_f32_e64 v95, v95\n" \
"v_mfma_f32_16x16x32_bf16 v[124:127], a[152:155], a[56:59], v[124:127]\n" \
"v_cvt_pk_bf16_f32 v90, v92, v93\n" \
"v_cvt_pk_bf16_f32 v91, v94, v95\n" \
"v_mfma_f32_16x16x32_bf16 v[124:127], a[156:159], a[60:63], v[124:127]\n" \
"ds_read_b128 a[144:147], v48 offset:4096\n" \
"ds_read_b128 a[148:151], v48 offset:5120\n" \
"v_mfma_f32_16x16x32_bf16 v[120:123], a[140:143], a[60:63], v[44:47]\n" \
"ds_read_b128 a[152:155], v48 offset:6144\n" \
"ds_read_b128 a[140:143], v48 offset:3072\n" \
"v_mfma_f32_16x16x32_bf16 v[120:123], a[136:139], a[56:59], v[120:123]\n" \
"ds_read_b128 a[136:139], v48 offset:2048\n" \
"ds_read_b128 a[156:159], v48 offset:7168\n" \
"v_mfma_f32_16x16x32_bf16 v[120:123], a[132:135], a[52:55], v[120:123]\n" \
"buffer_load_dwordx4 v51, s[8:11], s64 offen offset:0 sc1 lds\n" \
"v_mfma_f32_16x16x32_bf16 v[120:123], a[128:131], a[48:51], v[120:123]\n" \
"ds_read_b128 a[128:131], v48 offset:0\n" \
"s_waitcnt lgkmcnt(7)\n" \
"v_mfma_f32_16x16x32_bf16 v[128:131], a[160:163], v[68:71], v[128:131]\n" \
"v_exp_f32_e32 v102, v102\n" \
"v_mfma_f32_16x16x32_bf16 v[160:163], a[160:163], v[72:75], v[160:163]\n" \
"v_exp_f32_e64 v103, v103\n" \
"v_mfma_f32_16x16x32_bf16 v[132:135], a[164:167], v[68:71], v[132:135]\n" \
"ds_read_b128 a[132:135], v48 offset:1024\n" \
"v_cvt_pk_bf16_f32 v103, v102, v103\n" \
"v_mfma_f32_16x16x32_bf16 v[164:167], a[164:167], v[72:75], v[164:167]\n" \
"v_exp_f32_e64 v100, v100\n" \
"v_mfma_f32_16x16x32_bf16 v[136:139], a[168:171], v[68:71], v[136:139]\n" \
"v_exp_f32_e64 v101, v101\n" \
"v_mfma_f32_16x16x32_bf16 v[168:171], a[168:171], v[72:75], v[168:171]\n" \
"v_exp_f32_e64 v98, v98\n" \
"v_mfma_f32_16x16x32_bf16 v[140:143], a[172:175], v[68:71], v[140:143]\n" \
"v_exp_f32_e64 v99, v99\n" \
"v_mfma_f32_16x16x32_bf16 v[172:175], a[172:175], v[72:75], v[172:175]\n" \
"v_exp_f32_e64 v96, v96\n" \
"v_mfma_f32_16x16x32_bf16 v[144:147], a[176:179], v[68:71], v[144:147]\n" \
"v_exp_f32_e64 v97, v97\n" \
"v_mfma_f32_16x16x32_bf16 v[176:179], a[176:179], v[72:75], v[176:179]\n" \
"v_exp_f32_e64 v104, v104\n" \
"v_mfma_f32_16x16x32_bf16 v[148:151], a[180:183], v[68:71], v[148:151]\n" \
"v_exp_f32_e64 v105, v105\n" \
"v_mfma_f32_16x16x32_bf16 v[180:183], a[180:183], v[72:75], v[180:183]\n" \
"v_cvt_pk_bf16_f32 v102, v100, v101\n" \
"v_cvt_pk_bf16_f32 v101, v98, v99\n" \
"v_mfma_f32_16x16x32_bf16 v[152:155], a[184:187], v[68:71], v[152:155]\n" \
"v_cvt_pk_bf16_f32 v100, v96, v97\n" \
"v_cvt_pk_bf16_f32 v104, v104, v105\n" \
"v_mfma_f32_16x16x32_bf16 v[184:187], a[184:187], v[72:75], v[184:187]\n" \
"buffer_load_dwordx4 v51, s[8:11], s64 offen offset:1024 sc1 lds\n" \
"v_mfma_f32_16x16x32_bf16 v[156:159], a[188:191], v[68:71], v[156:159]\n" \
"v_exp_f32_e64 v106, v106\n" \
"v_mfma_f32_16x16x32_bf16 v[188:191], a[188:191], v[72:75], v[188:191]\n" \
"v_exp_f32_e64 v107, v107\n" \
"v_smfmac_f32_16x16x64_bf16 a[192:195], a[200:203], v[68:75], v21\n" \
"v_exp_f32_e64 v108, v108\n" \
"v_mfma_f32_16x16x32_bf16 v[220:223], a[188:191], v[84:87], v[220:223]\n" \
"v_exp_f32_e64 v109, v109\n" \
"v_mfma_f32_16x16x32_bf16 v[252:255], a[188:191], v[88:91], v[252:255]\n" \
"v_exp_f32_e64 v110, v110\n" \
"v_mfma_f32_16x16x32_bf16 v[216:219], a[184:187], v[84:87], v[216:219]\n" \
"v_exp_f32_e64 v111, v111\n" \
"v_mfma_f32_16x16x32_bf16 v[248:251], a[184:187], v[88:91], v[248:251]\n" \
"v_exp_f32_e64 v118, v118\n" \
"v_mfma_f32_16x16x32_bf16 v[212:215], a[180:183], v[84:87], v[212:215]\n" \
"v_exp_f32_e64 v119, v119\n" \
"v_mfma_f32_16x16x32_bf16 v[244:247], a[180:183], v[88:91], v[244:247]\n" \
"v_cvt_pk_bf16_f32 v105, v106, v107\n" \
"v_cvt_pk_bf16_f32 v106, v108, v109\n" \
"v_mfma_f32_16x16x32_bf16 v[208:211], a[176:179], v[84:87], v[208:211]\n" \
"v_cvt_pk_bf16_f32 v107, v110, v111\n" \
"v_cvt_pk_bf16_f32 v119, v118, v119\n" \
"v_mfma_f32_16x16x32_bf16 v[240:243], a[176:179], v[88:91], v[240:243]\n" \
"v_exp_f32_e64 v116, v116\n" \
"v_mfma_f32_16x16x32_bf16 v[204:207], a[172:175], v[84:87], v[204:207]\n" \
"v_exp_f32_e64 v117, v117\n" \
"v_mfma_f32_16x16x32_bf16 v[236:239], a[172:175], v[88:91], v[236:239]\n" \
"v_exp_f32_e64 v114, v114\n" \
"v_mfma_f32_16x16x32_bf16 v[200:203], a[168:171], v[84:87], v[200:203]\n" \
"v_exp_f32_e64 v115, v115\n" \
"v_mfma_f32_16x16x32_bf16 v[232:235], a[168:171], v[88:91], v[232:235]\n" \
"v_exp_f32_e64 v112, v112\n" \
"v_mfma_f32_16x16x32_bf16 v[196:199], a[164:167], v[84:87], v[196:199]\n" \
"v_exp_f32_e64 v113, v113\n" \
"v_mfma_f32_16x16x32_bf16 v[228:231], a[164:167], v[88:91], v[228:231]\n" \
"v_exp_f32_e64 v120, v120\n" \
"v_mfma_f32_16x16x32_bf16 v[192:195], a[160:163], v[84:87], v[192:195]\n" \
"v_exp_f32_e64 v121, v121\n" \
"v_mfma_f32_16x16x32_bf16 v[224:227], a[160:163], v[88:91], v[224:227]\n" \
"v_cvt_pk_bf16_f32 v118, v116, v117\n" \
"v_cvt_pk_bf16_f32 v117, v114, v115\n" \
"v_smfmac_f32_16x16x64_bf16 a[196:199], a[200:203], v[84:91], v21\n" \
"v_cvt_pk_bf16_f32 v116, v112, v113\n" \
"v_cvt_pk_bf16_f32 v120, v120, v121\n" \
"s_waitcnt vmcnt(4) lgkmcnt(0)\n" \
"s_barrier\n" \
"v_mfma_f32_16x16x32_bf16 v[64:67], a[128:131], a[0:3], v[32:35]\n" \
"s_xor_b32 m0, s65, 0x14000\n" \
"ds_read_b128 a[160:163], v49 offset:8192\n" \
"v_mfma_f32_16x16x32_bf16 v[64:67], a[132:135], a[4:7], v[64:67]\n" \
"buffer_load_dwordx4 v52, s[8:11], s64 offen offset:0 sc1 lds\n" \
"v_mfma_f32_16x16x32_bf16 v[64:67], a[136:139], a[8:11], v[64:67]\n" \
"ds_read_b128 a[164:167], v49 offset:9216\n" \
"ds_read_b128 a[168:171], v49 offset:10240\n" \
"v_mfma_f32_16x16x32_bf16 v[64:67], a[140:143], a[12:15], v[64:67]\n" \
"ds_read_b128 a[172:175], v49 offset:11264\n" \
"ds_read_b128 a[176:179], v49 offset:12288\n" \
"v_mfma_f32_16x16x32_bf16 v[68:71], a[156:159], a[12:15], v[32:35]\n" \
"ds_read_b128 a[180:183], v49 offset:13312\n" \
"ds_read_b128 a[184:187], v49 offset:14336\n" \
"v_mfma_f32_16x16x32_bf16 v[68:71], a[152:155], a[8:11], v[68:71]\n" \
"v_exp_f32_e64 v122, v122\n" \
"v_mfma_f32_16x16x32_bf16 v[68:71], a[148:151], a[4:7], v[68:71]\n" \
"v_exp_f32_e64 v123, v123\n" \
"v_mfma_f32_16x16x32_bf16 v[68:71], a[144:147], a[0:3], v[68:71]\n" \
"ds_read_b128 a[188:191], v49 offset:15360\n" \
"v_cvt_pk_bf16_f32 v121, v122, v123\n" \
"v_mfma_f32_16x16x32_bf16 v[76:79], a[144:147], a[16:19], v[36:39]\n" \
"v_exp_f32_e64 v124, v124\n" \
"v_mfma_f32_16x16x32_bf16 v[76:79], a[148:151], a[20:23], v[76:79]\n" \
"v_exp_f32_e64 v125, v125\n" \
"v_mfma_f32_16x16x32_bf16 v[76:79], a[152:155], a[24:27], v[76:79]\n" \
"v_exp_f32_e64 v126, v126\n" \
"v_mfma_f32_16x16x32_bf16 v[76:79], a[156:159], a[28:31], v[76:79]\n" \
"v_exp_f32_e64 v127, v127\n" \
"v_mfma_f32_16x16x32_bf16 v[72:75], a[140:143], a[28:31], v[36:39]\n" \
"v_cvt_pk_bf16_f32 v122, v124, v125\n" \
"v_cvt_pk_bf16_f32 v123, v126, v127\n" \
"v_mfma_f32_16x16x32_bf16 v[72:75], a[136:139], a[24:27], v[72:75]\n" \
"v_exp_f32_e64 v70, v70\n" \
"v_mfma_f32_16x16x32_bf16 v[72:75], a[132:135], a[20:23], v[72:75]\n" \
"v_exp_f32_e64 v71, v71\n" \
"v_mfma_f32_16x16x32_bf16 v[72:75], a[128:131], a[16:19], v[72:75]\n" \
"buffer_load_dwordx4 v52, s[8:11], s64 offen offset:1024 sc1 lds\n" \
"v_mfma_f32_16x16x32_bf16 v[80:83], a[128:131], a[32:35], v[40:43]\n" \
"s_mov_b32 m0, s65\n" \
"v_cvt_pk_bf16_f32 v71, v70, v71\n" \
"v_mfma_f32_16x16x32_bf16 v[80:83], a[132:135], a[36:39], v[80:83]\n" \
"v_exp_f32_e32 v68, v68\n" \
"v_mfma_f32_16x16x32_bf16 v[80:83], a[136:139], a[40:43], v[80:83]\n" \
"v_exp_f32_e64 v69, v69\n" \
"v_mfma_f32_16x16x32_bf16 v[80:83], a[140:143], a[44:47], v[80:83]\n" \
"v_exp_f32_e64 v72, v72\n" \
"v_mfma_f32_16x16x32_bf16 v[84:87], a[156:159], a[44:47], v[40:43]\n" \
"v_exp_f32_e64 v73, v73\n" \
"v_mfma_f32_16x16x32_bf16 v[84:87], a[152:155], a[40:43], v[84:87]\n" \
"v_exp_f32_e64 v74, v74\n" \
"v_mfma_f32_16x16x32_bf16 v[84:87], a[148:151], a[36:39], v[84:87]\n" \
"v_exp_f32_e64 v75, v75\n" \
"v_mfma_f32_16x16x32_bf16 v[84:87], a[144:147], a[32:35], v[84:87]\n" \
"v_exp_f32_e64 v66, v66\n" \
"v_mfma_f32_16x16x32_bf16 v[92:95], a[144:147], a[48:51], v[44:47]\n" \
"ds_read_b128 a[144:147], v48 offset:12288\n" \
"v_cvt_pk_bf16_f32 v72, v72, v73\n" \
"v_mfma_f32_16x16x32_bf16 v[92:95], a[148:151], a[52:55], v[92:95]\n" \
"ds_read_b128 a[148:151], v48 offset:13312\n" \
"v_cvt_pk_bf16_f32 v70, v68, v69\n" \
"v_mfma_f32_16x16x32_bf16 v[92:95], a[152:155], a[56:59], v[92:95]\n" \
"ds_read_b128 a[152:155], v48 offset:14336\n" \
"v_cvt_pk_bf16_f32 v73, v74, v75\n" \
"v_mfma_f32_16x16x32_bf16 v[92:95], a[156:159], a[60:63], v[92:95]\n" \
"ds_read_b128 a[156:159], v48 offset:15360\n" \
"v_add_u32_e32 v49, 0x10000, v48\n" \
"v_mfma_f32_16x16x32_bf16 v[88:91], a[140:143], a[60:63], v[44:47]\n" \
"ds_read_b128 a[140:143], v48 offset:11264\n" \
"s_xor_b32 s65, s65, 0x4000\n" \
"v_mfma_f32_16x16x32_bf16 v[88:91], a[136:139], a[56:59], v[88:91]\n" \
"v_exp_f32_e64 v67, v67\n" \
"v_mfma_f32_16x16x32_bf16 v[88:91], a[132:135], a[52:55], v[88:91]\n" \
"buffer_load_dwordx4 v53, s[8:11], s64 offen offset:0 sc1 lds\n" \
"v_mfma_f32_16x16x32_bf16 v[88:91], a[128:131], a[48:51], v[88:91]\n" \
"ds_read_b128 a[128:131], v48 offset:8192\n" \
"s_waitcnt lgkmcnt(6)\n" \
"v_mfma_f32_16x16x32_bf16 v[128:131], a[160:163], v[100:103], v[128:131]\n" \
"ds_read_b128 a[132:135], v48 offset:9216\n" \
"ds_read_b128 a[136:139], v48 offset:10240\n" \
"v_mfma_f32_16x16x32_bf16 v[160:163], a[160:163], v[104:107], v[160:163]\n" \
"v_exp_f32_e32 v64, v64\n" \
"v_mfma_f32_16x16x32_bf16 v[132:135], a[164:167], v[100:103], v[132:135]\n" \
"v_exp_f32_e64 v65, v65\n" \
"v_mfma_f32_16x16x32_bf16 v[164:167], a[164:167], v[104:107], v[164:167]\n" \
"v_exp_f32_e64 v76, v76\n" \
"v_mfma_f32_16x16x32_bf16 v[136:139], a[168:171], v[100:103], v[136:139]\n" \
"v_exp_f32_e64 v77, v77\n" \
"v_mfma_f32_16x16x32_bf16 v[168:171], a[168:171], v[104:107], v[168:171]\n" \
"v_exp_f32_e64 v78, v78\n" \
"v_mfma_f32_16x16x32_bf16 v[140:143], a[172:175], v[100:103], v[140:143]\n" \
"v_exp_f32_e64 v79, v79\n" \
"v_mfma_f32_16x16x32_bf16 v[172:175], a[172:175], v[104:107], v[172:175]\n" \
"v_cvt_pk_bf16_f32 v69, v66, v67\n" \
"v_cvt_pk_bf16_f32 v68, v64, v65\n" \
"v_mfma_f32_16x16x32_bf16 v[144:147], a[176:179], v[100:103], v[144:147]\n" \
"v_cvt_pk_bf16_f32 v74, v76, v77\n" \
"v_cvt_pk_bf16_f32 v75, v78, v79\n" \
"v_mfma_f32_16x16x32_bf16 v[176:179], a[176:179], v[104:107], v[176:179]\n" \
"v_xor_b32_e32 v48, 0x4000, v48\n" \
"v_mfma_f32_16x16x32_bf16 v[148:151], a[180:183], v[100:103], v[148:151]\n" \
"v_mfma_f32_16x16x32_bf16 v[180:183], a[180:183], v[104:107], v[180:183]\n" \
"v_mfma_f32_16x16x32_bf16 v[152:155], a[184:187], v[100:103], v[152:155]\n" \
"v_mfma_f32_16x16x32_bf16 v[184:187], a[184:187], v[104:107], v[184:187]\n" \
"buffer_load_dwordx4 v53, s[8:11], s64 offen offset:1024 sc1 lds\n" \
"v_mfma_f32_16x16x32_bf16 v[156:159], a[188:191], v[100:103], v[156:159]\n" \
"s_add_u32 s64, s64, 0x8000\n" \
"s_sub_u32 s66, s66, 1\n" \
"s_nop 0\n" \
"v_mfma_f32_16x16x32_bf16 v[188:191], a[188:191], v[104:107], v[188:191]\n" \
"v_smfmac_f32_16x16x64_bf16 a[192:195], a[200:203], v[100:107], v21\n" \
"v_mfma_f32_16x16x32_bf16 v[220:223], a[188:191], v[116:119], v[220:223]\n" \
"v_mfma_f32_16x16x32_bf16 v[252:255], a[188:191], v[120:123], v[252:255]\n" \
"v_mfma_f32_16x16x32_bf16 v[216:219], a[184:187], v[116:119], v[216:219]\n" \
"v_mfma_f32_16x16x32_bf16 v[248:251], a[184:187], v[120:123], v[248:251]\n" \
"v_mfma_f32_16x16x32_bf16 v[212:215], a[180:183], v[116:119], v[212:215]\n" \
"v_mfma_f32_16x16x32_bf16 v[244:247], a[180:183], v[120:123], v[244:247]\n" \
"v_mfma_f32_16x16x32_bf16 v[208:211], a[176:179], v[116:119], v[208:211]\n" \
"v_mfma_f32_16x16x32_bf16 v[240:243], a[176:179], v[120:123], v[240:243]\n" \
"v_mfma_f32_16x16x32_bf16 v[204:207], a[172:175], v[116:119], v[204:207]\n" \
"v_mfma_f32_16x16x32_bf16 v[236:239], a[172:175], v[120:123], v[236:239]\n" \
"v_mfma_f32_16x16x32_bf16 v[200:203], a[168:171], v[116:119], v[200:203]\n" \
"v_mfma_f32_16x16x32_bf16 v[232:235], a[168:171], v[120:123], v[232:235]\n" \
"v_mfma_f32_16x16x32_bf16 v[196:199], a[164:167], v[116:119], v[196:199]\n" \
"v_mfma_f32_16x16x32_bf16 v[228:231], a[164:167], v[120:123], v[228:231]\n" \
"v_mfma_f32_16x16x32_bf16 v[192:195], a[160:163], v[116:119], v[192:195]\n" \
"v_mfma_f32_16x16x32_bf16 v[224:227], a[160:163], v[120:123], v[224:227]\n" \
"v_smfmac_f32_16x16x64_bf16 a[196:199], a[200:203], v[116:123], v21\n" \
"s_waitcnt vmcnt(4) lgkmcnt(0)\n" \
"s_barrier\n" \
"s_cbranch_scc0 L_1_%=\n" \
"L_tr_%=:\n" \
"s_cmp_lg_u32 s16, 0\n" \
"s_cbranch_scc1 L_sw_%=\n" \
".fill (L_ab + 4096 - .) / 4, 4, 0xBF800000\n" \
"L_ap1:\n" \
"s_cbranch_execz L_ap0\n" \
"v_mfma_f32_16x16x32_bf16 v[96:99], a[128:131], a[0:3], v[32:35]\n" \
"s_xor_b32 m0, s65, 0x12000\n" \
"s_mov_b32 s65, s13\n" \
"v_mfma_f32_16x16x32_bf16 v[96:99], a[132:135], a[4:7], v[96:99]\n" \
"buffer_load_dwordx4 v50, s[8:11], s64 offen offset:0 sc1 lds\n" \
"v_mfma_f32_16x16x32_bf16 v[96:99], a[136:139], a[8:11], v[96:99]\n" \
"v_add_u32_e32 v63, s15, v20\n" \
"s_add_u32 s67, s14, 40960\n" \
"v_mfma_f32_16x16x32_bf16 v[96:99], a[140:143], a[12:15], v[96:99]\n" \
"ds_read_b128 a[160:163], v49 offset:0\n" \
"ds_read_b128 a[164:167], v49 offset:1024\n" \
"v_mfma_f32_16x16x32_bf16 v[100:103], a[156:159], a[12:15], v[32:35]\n" \
"buffer_load_dwordx4 v50, s[8:11], s64 offen offset:1024 sc1 lds\n" \
"v_mfma_f32_16x16x32_bf16 v[100:103], a[152:155], a[8:11], v[100:103]\n" \
"v_add_u32_e32 v50, 0x6000, v63\n" \
"s_xor_b32 m0, s65, 0x8000\n" \
"v_mfma_f32_16x16x32_bf16 v[100:103], a[148:151], a[4:7], v[100:103]\n" \
"ds_read_b128 a[168:171], v49 offset:2048\n" \
"ds_read_b128 a[172:175], v49 offset:3072\n" \
"v_mfma_f32_16x16x32_bf16 v[100:103], a[144:147], a[0:3], v[100:103]\n" \
"ds_read_b128 a[176:179], v49 offset:4096\n" \
"ds_read_b128 a[180:183], v49 offset:5120\n" \
"v_mfma_f32_16x16x32_bf16 v[108:111], a[144:147], a[16:19], v[36:39]\n" \
"buffer_load_dwordx4 v50, s[8:11], s67 offen offset:0 sc1 lds\n" \
"v_mfma_f32_16x16x32_bf16 v[108:111], a[148:151], a[20:23], v[108:111]\n" \
"ds_read_b128 a[184:187], v49 offset:6144\n" \
"ds_read_b128 a[188:191], v49 offset:7168\n" \
"v_mfma_f32_16x16x32_bf16 v[108:111], a[152:155], a[24:27], v[108:111]\n" \
"s_mov_b32 s64, s14\n" \
"v_add_u32_e32 v51, 0x12000, v63\n" \
"v_mfma_f32_16x16x32_bf16 v[108:111], a[156:159], a[28:31], v[108:111]\n" \
"v_exp_f32_e32 v86, v86\n" \
"v_mfma_f32_16x16x32_bf16 v[104:107], a[140:143], a[28:31], v[36:39]\n" \
"buffer_load_dwordx4 v50, s[8:11], s67 offen offset:1024 sc1 lds\n" \
"v_mfma_f32_16x16x32_bf16 v[104:107], a[136:139], a[24:27], v[104:107]\n" \
"v_exp_f32_e32 v87, v87\n" \
"v_mfma_f32_16x16x32_bf16 v[104:107], a[132:135], a[20:23], v[104:107]\n" \
"v_exp_f32_e32 v84, v84\n" \
"v_mfma_f32_16x16x32_bf16 v[104:107], a[128:131], a[16:19], v[104:107]\n" \
"v_exp_f32_e32 v85, v85\n" \
"v_mfma_f32_16x16x32_bf16 v[112:115], a[128:131], a[32:35], v[40:43]\n" \
"v_cvt_pk_bf16_f32 v87, v86, v87\n" \
"v_cvt_pk_bf16_f32 v86, v84, v85\n" \
"v_mfma_f32_16x16x32_bf16 v[112:115], a[132:135], a[36:39], v[112:115]\n" \
"buffer_load_dwordx4 v50, s[8:11], s67 offen offset:2048 sc1 lds\n" \
"v_mfma_f32_16x16x32_bf16 v[112:115], a[136:139], a[40:43], v[112:115]\n" \
"v_exp_f32_e32 v82, v82\n" \
"v_mfma_f32_16x16x32_bf16 v[112:115], a[140:143], a[44:47], v[112:115]\n" \
"v_exp_f32_e32 v83, v83\n" \
"v_mfma_f32_16x16x32_bf16 v[116:119], a[156:159], a[44:47], v[40:43]\n" \
"v_exp_f32_e32 v80, v80\n" \
"v_mfma_f32_16x16x32_bf16 v[116:119], a[152:155], a[40:43], v[116:119]\n" \
"buffer_load_dwordx4 v50, s[8:11], s67 offen offset:3072 sc1 lds\n" \
"v_mfma_f32_16x16x32_bf16 v[116:119], a[148:151], a[36:39], v[116:119]\n" \
"s_xor_b32 m0, s65, 0xa000\n" \
"v_cvt_pk_bf16_f32 v85, v82, v83\n" \
"v_mfma_f32_16x16x32_bf16 v[116:119], a[144:147], a[32:35], v[116:119]\n" \
"v_exp_f32_e32 v81, v81\n" \
"v_mfma_f32_16x16x32_bf16 v[124:127], a[144:147], a[48:51], v[44:47]\n" \
"v_cvt_pk_bf16_f32 v84, v80, v81\n" \
"v_readlane_b32 s93, v11, 19\n" \
"v_mfma_f32_16x16x32_bf16 v[124:127], a[148:151], a[52:55], v[124:127]\n" \
"buffer_load_dwordx4 v51, s[8:11], s64 offen offset:0 sc1 lds\n" \
"v_mfma_f32_16x16x32_bf16 v[124:127], a[152:155], a[56:59], v[124:127]\n" \
"v_exp_f32_e32 v88, v88\n" \
"v_mfma_f32_16x16x32_bf16 v[124:127], a[156:159], a[60:63], v[124:127]\n" \
"v_exp_f32_e32 v89, v89\n" \
"v_mfma_f32_16x16x32_bf16 v[120:123], a[140:143], a[60:63], v[44:47]\n" \
"v_exp_f32_e32 v90, v90\n" \
"v_mfma_f32_16x16x32_bf16 v[120:123], a[136:139], a[56:59], v[120:123]\n" \
"v_exp_f32_e32 v91, v91\n" \
"v_mfma_f32_16x16x32_bf16 v[120:123], a[132:135], a[52:55], v[120:123]\n" \
"buffer_load_dwordx4 v51, s[8:11], s64 offen offset:1024 sc1 lds\n" \
"v_mfma_f32_16x16x32_bf16 v[120:123], a[128:131], a[48:51], v[120:123]\n" \
"v_cvt_pk_bf16_f32 v88, v88, v89\n" \
"s_waitcnt lgkmcnt(0)\n" \
"v_mfma_f32_16x16x32_bf16 v[128:131], a[160:163], v[68:71], v[128:131]\n" \
"v_exp_f32_e32 v92, v92\n" \
"v_mfma_f32_16x16x32_bf16 v[160:163], a[160:163], v[72:75], v[160:163]\n" \
"v_exp_f32_e32 v93, v93\n" \
"v_mfma_f32_16x16x32_bf16 v[132:135], a[164:167], v[68:71], v[132:135]\n" \
"buffer_load_dwordx4 v51, s[8:11], s64 offen offset:2048 sc1 lds\n" \
"v_mfma_f32_16x16x32_bf16 v[164:167], a[164:167], v[72:75], v[164:167]\n" \
"v_exp_f32_e32 v94, v94\n" \
"v_mfma_f32_16x16x32_bf16 v[136:139], a[168:171], v[68:71], v[136:139]\n" \
"v_exp_f32_e32 v95, v95\n" \
"v_mfma_f32_16x16x32_bf16 v[168:171], a[168:171], v[72:75], v[168:171]\n" \
"v_cvt_pk_bf16_f32 v89, v90, v91\n" \
"v_cvt_pk_bf16_f32 v90, v92, v93\n" \
"v_mfma_f32_16x16x32_bf16 v[140:143], a[172:175], v[68:71], v[140:143]\n" \
"buffer_load_dwordx4 v51, s[8:11], s64 offen offset:3072 sc1 lds\n" \
"v_mfma_f32_16x16x32_bf16 v[172:175], a[172:175], v[72:75], v[172:175]\n" \
"v_cvt_pk_bf16_f32 v91, v94, v95\n" \
"v_readlane_b32 s80, v11, 11\n" \
"v_mfma_f32_16x16x32_bf16 v[144:147], a[176:179], v[68:71], v[144:147]\n" \
"v_readlane_b32 s81, v11, 12\n" \
"s_mov_b32 s82, 0x47fca0\n" \
"v_mfma_f32_16x16x32_bf16 v[176:179], a[176:179], v[72:75], v[176:179]\n" \
"s_mov_b32 s83, 0x20000\n" \
"v_readlane_b32 s92, v11, 25\n" \
"v_mfma_f32_16x16x32_bf16 v[148:151], a[180:183], v[68:71], v[148:151]\n" \
"s_add_u32 m0, s93, 0x0\n" \
"ds_read_b128 a[128:131], v48 offset:0\n" \
"v_mfma_f32_16x16x32_bf16 v[180:183], a[180:183], v[72:75], v[180:183]\n" \
"ds_read_b128 a[144:147], v48 offset:4096\n" \
"ds_read_b128 a[132:135], v48 offset:1024\n" \
"v_mfma_f32_16x16x32_bf16 v[152:155], a[184:187], v[68:71], v[152:155]\n" \
"buffer_load_dwordx4 v0, s[80:83], s92 offen offset:0 lds\n" \
"v_mfma_f32_16x16x32_bf16 v[184:187], a[184:187], v[72:75], v[184:187]\n" \
"ds_read_b128 a[148:151], v48 offset:5120\n" \
"ds_read_b128 a[136:139], v48 offset:2048\n" \
"v_mfma_f32_16x16x32_bf16 v[156:159], a[188:191], v[68:71], v[156:159]\n" \
"ds_read_b128 a[152:155], v48 offset:6144\n" \
"ds_read_b128 a[140:143], v48 offset:3072\n" \
"v_mfma_f32_16x16x32_bf16 v[188:191], a[188:191], v[72:75], v[188:191]\n" \
"ds_read_b128 a[156:159], v48 offset:7168\n" \
"v_mov_b32_e32 v56, s62\n" \
"v_smfmac_f32_16x16x64_bf16 a[192:195], a[200:203], v[68:75], v21\n" \
"buffer_load_dwordx4 v1, s[80:83], s92 offen offset:1024 lds\n" \
"v_mfma_f32_16x16x32_bf16 v[220:223], a[188:191], v[84:87], v[220:223]\n" \
"v_readlane_b32 s68, v11, 0\n" \
"v_add_u32_e32 v52, 0x14000, v63\n" \
"v_mfma_f32_16x16x32_bf16 v[252:255], a[188:191], v[88:91], v[252:255]\n" \
"v_add_u32_e32 v53, 0x20000, v63\n" \
"s_mov_b32 s66, s19\n" \
"v_mfma_f32_16x16x32_bf16 v[216:219], a[184:187], v[84:87], v[216:219]\n" \
"buffer_load_dwordx4 v2, s[80:83], s92 offen offset:2048 lds\n" \
"v_mfma_f32_16x16x32_bf16 v[248:251], a[184:187], v[88:91], v[248:251]\n" \
"v_exp_f32_e32 v102, v102\n" \
"v_mfma_f32_16x16x32_bf16 v[212:215], a[180:183], v[84:87], v[212:215]\n" \
"v_exp_f32_e32 v103, v103\n" \
"v_mfma_f32_16x16x32_bf16 v[244:247], a[180:183], v[88:91], v[244:247]\n" \
"v_exp_f32_e32 v100, v100\n" \
"v_mfma_f32_16x16x32_bf16 v[208:211], a[176:179], v[84:87], v[208:211]\n" \
"buffer_load_dwordx4 v3, s[80:83], s92 offen offset:3072 lds\n" \
"v_mfma_f32_16x16x32_bf16 v[240:243], a[176:179], v[88:91], v[240:243]\n" \
"v_exp_f32_e32 v101, v101\n" \
"v_mfma_f32_16x16x32_bf16 v[204:207], a[172:175], v[84:87], v[204:207]\n" \
"v_exp_f32_e32 v98, v98\n" \
"v_mfma_f32_16x16x32_bf16 v[236:239], a[172:175], v[88:91], v[236:239]\n" \
"v_exp_f32_e32 v99, v99\n" \
"v_mfma_f32_16x16x32_bf16 v[200:203], a[168:171], v[84:87], v[200:203]\n" \
"v_exp_f32_e32 v96, v96\n" \
"v_mfma_f32_16x16x32_bf16 v[232:235], a[168:171], v[88:91], v[232:235]\n" \
"v_exp_f32_e32 v97, v97\n" \
"v_mfma_f32_16x16x32_bf16 v[196:199], a[164:167], v[84:87], v[196:199]\n" \
"v_exp_f32_e32 v104, v104\n" \
"v_mfma_f32_16x16x32_bf16 v[228:231], a[164:167], v[88:91], v[228:231]\n" \
"v_exp_f32_e32 v105, v105\n" \
"v_mfma_f32_16x16x32_bf16 v[192:195], a[160:163], v[84:87], v[192:195]\n" \
"v_cvt_pk_bf16_f32 v103, v102, v103\n" \
"v_cvt_pk_bf16_f32 v102, v100, v101\n" \
"v_mfma_f32_16x16x32_bf16 v[224:227], a[160:163], v[88:91], v[224:227]\n" \
"v_cvt_pk_bf16_f32 v101, v98, v99\n" \
"v_cvt_pk_bf16_f32 v100, v96, v97\n" \
"v_smfmac_f32_16x16x64_bf16 a[196:199], a[200:203], v[84:91], v21\n" \
"v_exp_f32_e32 v106, v106\n" \
"s_waitcnt vmcnt(14) lgkmcnt(0)\n" \
"s_barrier\n" \
"v_mfma_f32_16x16x32_bf16 v[64:67], a[128:131], a[0:3], v[32:35]\n" \
"s_xor_b32 m0, s65, 0x18000\n" \
"ds_read_b128 a[160:163], v49 offset:8192\n" \
"v_mfma_f32_16x16x32_bf16 v[64:67], a[132:135], a[4:7], v[64:67]\n" \
"buffer_load_dwordx4 v52, s[8:11], s64 offen offset:0 sc1 lds\n" \
"v_mfma_f32_16x16x32_bf16 v[64:67], a[136:139], a[8:11], v[64:67]\n" \
"ds_read_b128 a[164:167], v49 offset:9216\n" \
"ds_read_b128 a[168:171], v49 offset:10240\n" \
"v_mfma_f32_16x16x32_bf16 v[64:67], a[140:143], a[12:15], v[64:67]\n" \
"ds_read_b128 a[172:175], v49 offset:11264\n" \
"ds_read_b128 a[176:179], v49 offset:12288\n" \
"v_mfma_f32_16x16x32_bf16 v[68:71], a[156:159], a[12:15], v[32:35]\n" \
"buffer_load_dwordx4 v52, s[8:11], s64 offen offset:1024 sc1 lds\n" \
"s_mov_b64 exec, 1\n" \
"buffer_store_dword v56, off, s[56:59], s68 offset:0 sc1\n" \
"s_mov_b64 exec, -1\n" \
"v_mfma_f32_16x16x32_bf16 v[68:71], a[152:155], a[8:11], v[68:71]\n" \
"ds_read_b128 a[180:183], v49 offset:13312\n" \
"ds_read_b128 a[184:187], v49 offset:14336\n" \
"v_mfma_f32_16x16x32_bf16 v[68:71], a[148:151], a[4:7], v[68:71]\n" \
"v_exp_f32_e32 v107, v107\n" \
"v_mfma_f32_16x16x32_bf16 v[68:71], a[144:147], a[0:3], v[68:71]\n" \
"v_cvt_pk_bf16_f32 v104, v104, v105\n" \
"v_cvt_pk_bf16_f32 v105, v106, v107\n" \
"v_mfma_f32_16x16x32_bf16 v[76:79], a[144:147], a[16:19], v[36:39]\n" \
"v_exp_f32_e32 v108, v108\n" \
"v_mfma_f32_16x16x32_bf16 v[76:79], a[148:151], a[20:23], v[76:79]\n" \
"buffer_load_dwordx4 v52, s[8:11], s64 offen offset:2048 sc1 lds\n" \
"v_mfma_f32_16x16x32_bf16 v[76:79], a[152:155], a[24:27], v[76:79]\n" \
"v_exp_f32_e32 v109, v109\n" \
"v_mfma_f32_16x16x32_bf16 v[76:79], a[156:159], a[28:31], v[76:79]\n" \
"v_exp_f32_e32 v110, v110\n" \
"v_mfma_f32_16x16x32_bf16 v[72:75], a[140:143], a[28:31], v[36:39]\n" \
"v_exp_f32_e32 v111, v111\n" \
"v_mfma_f32_16x16x32_bf16 v[72:75], a[136:139], a[24:27], v[72:75]\n" \
"buffer_load_dwordx4 v52, s[8:11], s64 offen offset:3072 sc1 lds\n" \
"v_mfma_f32_16x16x32_bf16 v[72:75], a[132:135], a[20:23], v[72:75]\n" \
"s_mov_b32 m0, s65\n" \
"v_cvt_pk_bf16_f32 v106, v108, v109\n" \
"v_mfma_f32_16x16x32_bf16 v[72:75], a[128:131], a[16:19], v[72:75]\n" \
"v_cvt_pk_bf16_f32 v107, v110, v111\n" \
"ds_read_b128 a[188:191], v49 offset:15360\n" \
"v_mfma_f32_16x16x32_bf16 v[80:83], a[128:131], a[32:35], v[40:43]\n" \
"v_exp_f32_e32 v118, v118\n" \
"v_mfma_f32_16x16x32_bf16 v[80:83], a[132:135], a[36:39], v[80:83]\n" \
"v_exp_f32_e32 v119, v119\n" \
"v_mfma_f32_16x16x32_bf16 v[80:83], a[136:139], a[40:43], v[80:83]\n" \
"buffer_load_dwordx4 v53, s[8:11], s64 offen offset:0 sc1 lds\n" \
"v_mfma_f32_16x16x32_bf16 v[80:83], a[140:143], a[44:47], v[80:83]\n" \
"v_exp_f32_e32 v116, v116\n" \
"v_mfma_f32_16x16x32_bf16 v[84:87], a[156:159], a[44:47], v[40:43]\n" \
"v_exp_f32_e32 v117, v117\n" \
"v_mfma_f32_16x16x32_bf16 v[84:87], a[152:155], a[40:43], v[84:87]\n" \
"v_cvt_pk_bf16_f32 v119, v118, v119\n" \
"v_cvt_pk_bf16_f32 v118, v116, v117\n" \
"v_mfma_f32_16x16x32_bf16 v[84:87], a[148:151], a[36:39], v[84:87]\n" \
"v_exp_f32_e32 v114, v114\n" \
"v_mfma_f32_16x16x32_bf16 v[84:87], a[144:147], a[32:35], v[84:87]\n" \
"buffer_load_dwordx4 v53, s[8:11], s64 offen offset:1024 sc1 lds\n" \
"v_mfma_f32_16x16x32_bf16 v[92:95], a[144:147], a[48:51], v[44:47]\n" \
"v_exp_f32_e32 v115, v115\n" \
"v_mfma_f32_16x16x32_bf16 v[92:95], a[148:151], a[52:55], v[92:95]\n" \
"v_exp_f32_e32 v112, v112\n" \
"v_mfma_f32_16x16x32_bf16 v[92:95], a[152:155], a[56:59], v[92:95]\n" \
"v_exp_f32_e32 v113, v113\n" \
"v_mfma_f32_16x16x32_bf16 v[92:95], a[156:159], a[60:63], v[92:95]\n" \
"buffer_load_dwordx4 v53, s[8:11], s64 offen offset:2048 sc1 lds\n" \
"v_mfma_f32_16x16x32_bf16 v[88:91], a[140:143], a[60:63], v[44:47]\n" \
"v_cvt_pk_bf16_f32 v117, v114, v115\n" \
"v_cvt_pk_bf16_f32 v116, v112, v113\n" \
"v_mfma_f32_16x16x32_bf16 v[88:91], a[136:139], a[56:59], v[88:91]\n" \
"v_exp_f32_e32 v120, v120\n" \
"v_mfma_f32_16x16x32_bf16 v[88:91], a[132:135], a[52:55], v[88:91]\n" \
"v_exp_f32_e32 v121, v121\n" \
"v_mfma_f32_16x16x32_bf16 v[88:91], a[128:131], a[48:51], v[88:91]\n" \
"v_cvt_pk_bf16_f32 v120, v120, v121\n" \
"s_waitcnt lgkmcnt(0)\n" \
"v_mfma_f32_16x16x32_bf16 v[128:131], a[160:163], v[100:103], v[128:131]\n" \
"buffer_load_dwordx4 v53, s[8:11], s64 offen offset:3072 sc1 lds\n" \
"v_mfma_f32_16x16x32_bf16 v[160:163], a[160:163], v[104:107], v[160:163]\n" \
"s_add_u32 m0, s93, 0x1000\n" \
"ds_read_b128 a[128:131], v48 offset:8192\n" \
"v_mfma_f32_16x16x32_bf16 v[132:135], a[164:167], v[100:103], v[132:135]\n" \
"v_exp_f32_e32 v122, v122\n" \
"v_mfma_f32_16x16x32_bf16 v[164:167], a[164:167], v[104:107], v[164:167]\n" \
"v_exp_f32_e32 v123, v123\n" \
"v_mfma_f32_16x16x32_bf16 v[136:139], a[168:171], v[100:103], v[136:139]\n" \
"buffer_load_dwordx4 v4, s[80:83], s92 offen offset:0 lds\n" \
"v_mfma_f32_16x16x32_bf16 v[168:171], a[168:171], v[104:107], v[168:171]\n" \
"v_exp_f32_e32 v124, v124\n" \
"v_mfma_f32_16x16x32_bf16 v[140:143], a[172:175], v[100:103], v[140:143]\n" \
"v_exp_f32_e32 v125, v125\n" \
"v_mfma_f32_16x16x32_bf16 v[172:175], a[172:175], v[104:107], v[172:175]\n" \
"v_exp_f32_e32 v126, v126\n" \
"v_mfma_f32_16x16x32_bf16 v[144:147], a[176:179], v[100:103], v[144:147]\n" \
"v_exp_f32_e32 v127, v127\n" \
"v_mfma_f32_16x16x32_bf16 v[176:179], a[176:179], v[104:107], v[176:179]\n" \
"v_cvt_pk_bf16_f32 v121, v122, v123\n" \
"v_cvt_pk_bf16_f32 v122, v124, v125\n" \
"v_mfma_f32_16x16x32_bf16 v[148:151], a[180:183], v[100:103], v[148:151]\n" \
"buffer_load_dwordx4 v5, s[80:83], s92 offen offset:1024 lds\n" \
"v_mfma_f32_16x16x32_bf16 v[180:183], a[180:183], v[104:107], v[180:183]\n" \
"v_cvt_pk_bf16_f32 v123, v126, v127\n" \
"ds_read_b128 a[144:147], v48 offset:12288\n" \
"v_mfma_f32_16x16x32_bf16 v[152:155], a[184:187], v[100:103], v[152:155]\n" \
"ds_read_b128 a[132:135], v48 offset:9216\n" \
"ds_read_b128 a[148:151], v48 offset:13312\n" \
"v_mfma_f32_16x16x32_bf16 v[184:187], a[184:187], v[104:107], v[184:187]\n" \
"ds_read_b128 a[136:139], v48 offset:10240\n" \
"ds_read_b128 a[152:155], v48 offset:14336\n" \
"v_mfma_f32_16x16x32_bf16 v[156:159], a[188:191], v[100:103], v[156:159]\n" \
"ds_read_b128 a[140:143], v48 offset:11264\n" \
"ds_read_b128 a[156:159], v48 offset:15360\n" \
"v_mfma_f32_16x16x32_bf16 v[188:191], a[188:191], v[104:107], v[188:191]\n" \
"buffer_load_dwordx4 v6, s[80:83], s92 offen offset:2048 lds\n" \
"v_smfmac_f32_16x16x64_bf16 a[192:195], a[200:203], v[100:107], v21\n" \
"v_exp_f32_e32 v70, v70\n" \
"v_mfma_f32_16x16x32_bf16 v[220:223], a[188:191], v[116:119], v[220:223]\n" \
"v_exp_f32_e32 v71, v71\n" \
"v_mfma_f32_16x16x32_bf16 v[252:255], a[188:191], v[120:123], v[252:255]\n" \
"buffer_load_dwordx4 v7, s[80:83], s92 offen offset:3072 lds\n" \
"v_mfma_f32_16x16x32_bf16 v[216:219], a[184:187], v[116:119], v[216:219]\n" \
"s_add_u32 m0, s93, 0x2000\n" \
"v_cvt_pk_bf16_f32 v71, v70, v71\n" \
"v_mfma_f32_16x16x32_bf16 v[248:251], a[184:187], v[120:123], v[248:251]\n" \
"v_exp_f32_e32 v72, v72\n" \
"v_mfma_f32_16x16x32_bf16 v[212:215], a[180:183], v[116:119], v[212:215]\n" \
"v_exp_f32_e32 v73, v73\n" \
"v_mfma_f32_16x16x32_bf16 v[244:247], a[180:183], v[120:123], v[244:247]\n" \
"v_exp_f32_e32 v68, v68\n" \
"v_mfma_f32_16x16x32_bf16 v[208:211], a[176:179], v[116:119], v[208:211]\n" \
"buffer_load_dwordx4 v8, s[80:83], s92 offen offset:0 lds\n" \
"v_mfma_f32_16x16x32_bf16 v[240:243], a[176:179], v[120:123], v[240:243]\n" \
"v_exp_f32_e32 v69, v69\n" \
"v_mfma_f32_16x16x32_bf16 v[204:207], a[172:175], v[116:119], v[204:207]\n" \
"v_exp_f32_e32 v74, v74\n" \
"v_mfma_f32_16x16x32_bf16 v[236:239], a[172:175], v[120:123], v[236:239]\n" \
"v_exp_f32_e32 v75, v75\n" \
"v_mfma_f32_16x16x32_bf16 v[200:203], a[168:171], v[116:119], v[200:203]\n" \
"v_exp_f32_e32 v66, v66\n" \
"v_mfma_f32_16x16x32_bf16 v[232:235], a[168:171], v[120:123], v[232:235]\n" \
"v_exp_f32_e32 v67, v67\n" \
"v_mfma_f32_16x16x32_bf16 v[196:199], a[164:167], v[116:119], v[196:199]\n" \
"v_exp_f32_e32 v64, v64\n" \
"v_mfma_f32_16x16x32_bf16 v[228:231], a[164:167], v[120:123], v[228:231]\n" \
"v_exp_f32_e32 v65, v65\n" \
"v_mfma_f32_16x16x32_bf16 v[192:195], a[160:163], v[116:119], v[192:195]\n" \
"v_cvt_pk_bf16_f32 v72, v72, v73\n" \
"v_cvt_pk_bf16_f32 v70, v68, v69\n" \
"v_mfma_f32_16x16x32_bf16 v[224:227], a[160:163], v[120:123], v[224:227]\n" \
"v_exp_f32_e32 v76, v76\n" \
"v_smfmac_f32_16x16x64_bf16 a[196:199], a[200:203], v[116:123], v21\n" \
"v_exp_f32_e32 v77, v77\n" \
"s_waitcnt vmcnt(14) lgkmcnt(0)\n" \
"s_barrier\n" \
"v_exp_f32_e32 v78, v78\n" \
"v_exp_f32_e32 v79, v79\n" \
"v_cvt_pk_bf16_f32 v73, v74, v75\n" \
"v_add_u32_e32 v49, 0x10000, v48\n" \
"v_cvt_pk_bf16_f32 v69, v66, v67\n" \
"v_cvt_pk_bf16_f32 v68, v64, v65\n" \
"v_cvt_pk_bf16_f32 v74, v76, v77\n" \
"v_cvt_pk_bf16_f32 v75, v78, v79\n" \
"v_add_u32_e32 v48, s16, v20\n" \
"s_xor_b32 s65, s65, 0x8000\n" \
"s_add_u32 s64, s64, 0x10000\n" \
"s_getpc_b64 s[86:87]\n" \
"L_hpt_%=:\n" \
"s_add_u32 s86, s86, L_ap5-L_hpt_%=\n" \
"s_addc_u32 s87, s87, 0\n" \
"s_add_u32 s86, s86, s15\n" \
"s_addc_u32 s87, s87, 0\n" \
"s_mov_b64 exec, 0\n" \
"s_swappc_b64 s[88:89], s[86:87]\n" \
"s_mov_b64 exec, -1\n" \
"s_add_u32 s86, s86, 0x2000\n" \
"s_addc_u32 s87, s87, 0\n" \
"s_mov_b64 exec, 0\n" \
"s_swappc_b64 s[88:89], s[86:87]\n" \
"s_mov_b64 exec, -1\n" \
".fill (L_ab + 6464 - .) / 4, 4, 0xBF800000\n" \
"L_2_%=:\n" \
"v_mfma_f32_16x16x32_bf16 v[96:99], a[128:131], a[64:67], v[32:35]\n" \
"s_xor_b32 m0, s65, 0x12000\n" \
"ds_read_b128 a[160:163], v49 offset:0\n" \
"v_mfma_f32_16x16x32_bf16 v[96:99], a[132:135], a[68:71], v[96:99]\n" \
"buffer_load_dwordx4 v50, s[8:11], s64 offen offset:0 sc1 lds\n" \
"v_mfma_f32_16x16x32_bf16 v[96:99], a[136:139], a[72:75], v[96:99]\n" \
"ds_read_b128 a[164:167], v49 offset:1024\n" \
"ds_read_b128 a[168:171], v49 offset:2048\n" \
"v_mfma_f32_16x16x32_bf16 v[96:99], a[140:143], a[76:79], v[96:99]\n" \
"ds_read_b128 a[172:175], v49 offset:3072\n" \
"ds_read_b128 a[176:179], v49 offset:4096\n" \
"v_mfma_f32_16x16x32_bf16 v[100:103], a[156:159], a[76:79], v[32:35]\n" \
"ds_read_b128 a[180:183], v49 offset:5120\n" \
"ds_read_b128 a[184:187], v49 offset:6144\n" \
"v_mfma_f32_16x16x32_bf16 v[100:103], a[152:155], a[72:75], v[100:103]\n" \
"v_exp_f32_e64 v86, v86\n" \
"v_mfma_f32_16x16x32_bf16 v[100:103], a[148:151], a[68:71], v[100:103]\n" \
"v_exp_f32_e64 v87, v87\n" \
"v_mfma_f32_16x16x32_bf16 v[100:103], a[144:147], a[64:67], v[100:103]\n" \
"buffer_load_dwordx4 v50, s[8:11], s64 offen offset:1024 sc1 lds\n" \
"v_mfma_f32_16x16x32_bf16 v[108:111], a[144:147], a[80:83], v[36:39]\n" \
"ds_read_b128 a[188:191], v49 offset:7168\n" \
"v_cvt_pk_bf16_f32 v87, v86, v87\n" \
"v_mfma_f32_16x16x32_bf16 v[108:111], a[148:151], a[84:87], v[108:111]\n" \
"v_exp_f32_e64 v84, v84\n" \
"v_mfma_f32_16x16x32_bf16 v[108:111], a[152:155], a[88:91], v[108:111]\n" \
"v_exp_f32_e64 v85, v85\n" \
"v_mfma_f32_16x16x32_bf16 v[108:111], a[156:159], a[92:95], v[108:111]\n" \
"v_exp_f32_e64 v82, v82\n" \
"v_mfma_f32_16x16x32_bf16 v[104:107], a[140:143], a[92:95], v[36:39]\n" \
"v_exp_f32_e64 v83, v83\n" \
"v_mfma_f32_16x16x32_bf16 v[104:107], a[136:139], a[88:91], v[104:107]\n" \
"v_exp_f32_e64 v80, v80\n" \
"v_mfma_f32_16x16x32_bf16 v[104:107], a[132:135], a[84:87], v[104:107]\n" \
"v_exp_f32_e64 v81, v81\n" \
"v_mfma_f32_16x16x32_bf16 v[104:107], a[128:131], a[80:83], v[104:107]\n" \
"buffer_load_dwordx4 v50, s[8:11], s64 offen offset:2048 sc1 lds\n" \
"v_mfma_f32_16x16x32_bf16 v[112:115], a[128:131], a[96:99], v[40:43]\n" \
"v_exp_f32_e64 v88, v88\n" \
"v_mfma_f32_16x16x32_bf16 v[112:115], a[132:135], a[100:103], v[112:115]\n" \
"v_exp_f32_e64 v89, v89\n" \
"v_mfma_f32_16x16x32_bf16 v[112:115], a[136:139], a[104:107], v[112:115]\n" \
"v_cvt_pk_bf16_f32 v86, v84, v85\n" \
"v_cvt_pk_bf16_f32 v85, v82, v83\n" \
"v_mfma_f32_16x16x32_bf16 v[112:115], a[140:143], a[108:111], v[112:115]\n" \
"v_cvt_pk_bf16_f32 v84, v80, v81\n" \
"v_cvt_pk_bf16_f32 v88, v88, v89\n" \
"v_mfma_f32_16x16x32_bf16 v[116:119], a[156:159], a[108:111], v[40:43]\n" \
"v_exp_f32_e64 v90, v90\n" \
"v_mfma_f32_16x16x32_bf16 v[116:119], a[152:155], a[104:107], v[116:119]\n" \
"v_exp_f32_e64 v91, v91\n" \
"v_mfma_f32_16x16x32_bf16 v[116:119], a[148:151], a[100:103], v[116:119]\n" \
"buffer_load_dwordx4 v50, s[8:11], s64 offen offset:3072 sc1 lds\n" \
"v_mfma_f32_16x16x32_bf16 v[116:119], a[144:147], a[96:99], v[116:119]\n" \
"s_xor_b32 m0, s65, 0xa000\n" \
"v_cvt_pk_bf16_f32 v89, v90, v91\n" \
"v_mfma_f32_16x16x32_bf16 v[124:127], a[144:147], a[112:115], v[44:47]\n" \
"v_exp_f32_e64 v92, v92\n" \
"v_mfma_f32_16x16x32_bf16 v[124:127], a[148:151], a[116:119], v[124:127]\n" \
"v_exp_f32_e64 v93, v93\n" \
"v_mfma_f32_16x16x32_bf16 v[124:127], a[152:155], a[120:123], v[124:127]\n" \
"v_exp_f32_e64 v94, v94\n" \
"v_mfma_f32_16x16x32_bf16 v[124:127], a[156:159], a[124:127], v[124:127]\n" \
"v_exp_f32_e64 v95, v95\n" \
"v_mfma_f32_16x16x32_bf16 v[120:123], a[140:143], a[124:127], v[44:47]\n" \
"v_cvt_pk_bf16_f32 v90, v92, v93\n" \
"v_cvt_pk_bf16_f32 v91, v94, v95\n" \
"v_mfma_f32_16x16x32_bf16 v[120:123], a[136:139], a[120:123], v[120:123]\n" \
"ds_read_b128 a[144:147], v48 offset:4096\n" \
"ds_read_b128 a[148:151], v48 offset:5120\n" \
"v_mfma_f32_16x16x32_bf16 v[120:123], a[132:135], a[116:119], v[120:123]\n" \
"buffer_load_dwordx4 v51, s[8:11], s64 offen offset:0 sc1 lds\n" \
"v_mfma_f32_16x16x32_bf16 v[120:123], a[128:131], a[112:115], v[120:123]\n" \
"ds_read_b128 a[128:131], v48 offset:0\n" \
"s_waitcnt lgkmcnt(3)\n" \
"v_mfma_f32_16x16x32_bf16 v[128:131], a[160:163], v[68:71], v[128:131]\n" \
"ds_read_b128 a[132:135], v48 offset:1024\n" \
"ds_read_b128 a[136:139], v48 offset:2048\n" \
"v_mfma_f32_16x16x32_bf16 v[160:163], a[160:163], v[72:75], v[160:163]\n" \
"ds_read_b128 a[152:155], v48 offset:6144\n" \
"ds_read_b128 a[140:143], v48 offset:3072\n" \
"v_mfma_f32_16x16x32_bf16 v[132:135], a[164:167], v[68:71], v[132:135]\n" \
"v_exp_f32_e32 v102, v102\n" \
"v_mfma_f32_16x16x32_bf16 v[164:167], a[164:167], v[72:75], v[164:167]\n" \
"v_exp_f32_e64 v103, v103\n" \
"v_mfma_f32_16x16x32_bf16 v[136:139], a[168:171], v[68:71], v[136:139]\n" \
"ds_read_b128 a[156:159], v48 offset:7168\n" \
"v_cvt_pk_bf16_f32 v103, v102, v103\n" \
"v_mfma_f32_16x16x32_bf16 v[168:171], a[168:171], v[72:75], v[168:171]\n" \
"buffer_load_dwordx4 v51, s[8:11], s64 offen offset:1024 sc1 lds\n" \
"v_mfma_f32_16x16x32_bf16 v[140:143], a[172:175], v[68:71], v[140:143]\n" \
"v_exp_f32_e64 v100, v100\n" \
"v_mfma_f32_16x16x32_bf16 v[172:175], a[172:175], v[72:75], v[172:175]\n" \
"v_exp_f32_e64 v101, v101\n" \
"v_mfma_f32_16x16x32_bf16 v[144:147], a[176:179], v[68:71], v[144:147]\n" \
"v_exp_f32_e64 v98, v98\n" \
"v_mfma_f32_16x16x32_bf16 v[176:179], a[176:179], v[72:75], v[176:179]\n" \
"v_exp_f32_e64 v99, v99\n" \
"v_mfma_f32_16x16x32_bf16 v[148:151], a[180:183], v[68:71], v[148:151]\n" \
"v_exp_f32_e64 v96, v96\n" \
"v_mfma_f32_16x16x32_bf16 v[180:183], a[180:183], v[72:75], v[180:183]\n" \
"v_exp_f32_e64 v97, v97\n" \
"v_mfma_f32_16x16x32_bf16 v[152:155], a[184:187], v[68:71], v[152:155]\n" \
"v_exp_f32_e64 v104, v104\n" \
"v_mfma_f32_16x16x32_bf16 v[184:187], a[184:187], v[72:75], v[184:187]\n" \
"buffer_load_dwordx4 v51, s[8:11], s64 offen offset:2048 sc1 lds\n" \
"v_mfma_f32_16x16x32_bf16 v[156:159], a[188:191], v[68:71], v[156:159]\n" \
"v_exp_f32_e64 v105, v105\n" \
"v_mfma_f32_16x16x32_bf16 v[188:191], a[188:191], v[72:75], v[188:191]\n" \
"v_cvt_pk_bf16_f32 v102, v100, v101\n" \
"v_cvt_pk_bf16_f32 v101, v98, v99\n" \
"v_smfmac_f32_16x16x64_bf16 a[192:195], a[200:203], v[68:75], v21\n" \
"v_cvt_pk_bf16_f32 v100, v96, v97\n" \
"v_cvt_pk_bf16_f32 v104, v104, v105\n" \
"v_mfma_f32_16x16x32_bf16 v[220:223], a[188:191], v[84:87], v[220:223]\n" \
"v_exp_f32_e64 v106, v106\n" \
"v_mfma_f32_16x16x32_bf16 v[252:255], a[188:191], v[88:91], v[252:255]\n" \
"v_exp_f32_e64 v107, v107\n" \
"v_mfma_f32_16x16x32_bf16 v[216:219], a[184:187], v[84:87], v[216:219]\n" \
"v_exp_f32_e64 v108, v108\n" \
"v_mfma_f32_16x16x32_bf16 v[248:251], a[184:187], v[88:91], v[248:251]\n" \
"buffer_load_dwordx4 v51, s[8:11], s64 offen offset:3072 sc1 lds\n" \
"v_mfma_f32_16x16x32_bf16 v[212:215], a[180:183], v[84:87], v[212:215]\n" \
"v_exp_f32_e64 v109, v109\n" \
"v_mfma_f32_16x16x32_bf16 v[244:247], a[180:183], v[88:91], v[244:247]\n" \
"v_exp_f32_e64 v110, v110\n" \
"v_mfma_f32_16x16x32_bf16 v[208:211], a[176:179], v[84:87], v[208:211]\n" \
"v_exp_f32_e64 v111, v111\n" \
"v_mfma_f32_16x16x32_bf16 v[240:243], a[176:179], v[88:91], v[240:243]\n" \
"v_exp_f32_e64 v118, v118\n" \
"v_mfma_f32_16x16x32_bf16 v[204:207], a[172:175], v[84:87], v[204:207]\n" \
"v_exp_f32_e64 v119, v119\n" \
"v_mfma_f32_16x16x32_bf16 v[236:239], a[172:175], v[88:91], v[236:239]\n" \
"v_cvt_pk_bf16_f32 v105, v106, v107\n" \
"v_cvt_pk_bf16_f32 v106, v108, v109\n" \
"v_mfma_f32_16x16x32_bf16 v[200:203], a[168:171], v[84:87], v[200:203]\n" \
"v_cvt_pk_bf16_f32 v107, v110, v111\n" \
"v_cvt_pk_bf16_f32 v119, v118, v119\n" \
"v_mfma_f32_16x16x32_bf16 v[232:235], a[168:171], v[88:91], v[232:235]\n" \
"v_exp_f32_e64 v116, v116\n" \
"v_mfma_f32_16x16x32_bf16 v[196:199], a[164:167], v[84:87], v[196:199]\n" \
"v_exp_f32_e64 v117, v117\n" \
"v_mfma_f32_16x16x32_bf16 v[228:231], a[164:167], v[88:91], v[228:231]\n" \
"v_exp_f32_e64 v114, v114\n" \
"v_mfma_f32_16x16x32_bf16 v[192:195], a[160:163], v[84:87], v[192:195]\n" \
"v_exp_f32_e64 v115, v115\n" \
"v_mfma_f32_16x16x32_bf16 v[224:227], a[160:163], v[88:91], v[224:227]\n" \
"v_exp_f32_e64 v112, v112\n" \
"v_smfmac_f32_16x16x64_bf16 a[196:199], a[200:203], v[84:91], v21\n" \
"v_exp_f32_e32 v113, v113\n" \
"s_waitcnt vmcnt(8) lgkmcnt(0)\n" \
"s_barrier\n" \
"s_nop 0\n" \
"v_mfma_f32_16x16x32_bf16 v[64:67], a[128:131], a[64:67], v[32:35]\n" \
"s_xor_b32 m0, s65, 0x18000\n" \
"ds_read_b128 a[160:163], v49 offset:8192\n" \
"v_mfma_f32_16x16x32_bf16 v[64:67], a[132:135], a[68:71], v[64:67]\n" \
"buffer_load_dwordx4 v52, s[8:11], s64 offen offset:0 sc1 lds\n" \
"v_mfma_f32_16x16x32_bf16 v[64:67], a[136:139], a[72:75], v[64:67]\n" \
"ds_read_b128 a[164:167], v49 offset:9216\n" \
"ds_read_b128 a[168:171], v49 offset:10240\n" \
"v_mfma_f32_16x16x32_bf16 v[64:67], a[140:143], a[76:79], v[64:67]\n" \
"ds_read_b128 a[172:175], v49 offset:11264\n" \
"ds_read_b128 a[176:179], v49 offset:12288\n" \
"v_mfma_f32_16x16x32_bf16 v[68:71], a[156:159], a[76:79], v[32:35]\n" \
"ds_read_b128 a[180:183], v49 offset:13312\n" \
"ds_read_b128 a[184:187], v49 offset:14336\n" \
"v_mfma_f32_16x16x32_bf16 v[68:71], a[152:155], a[72:75], v[68:71]\n" \
"ds_read_b128 a[188:191], v49 offset:15360\n" \
"v_cvt_pk_bf16_f32 v118, v116, v117\n" \
"v_mfma_f32_16x16x32_bf16 v[68:71], a[148:151], a[68:71], v[68:71]\n" \
"v_cvt_pk_bf16_f32 v117, v114, v115\n" \
"v_cvt_pk_bf16_f32 v116, v112, v113\n" \
"v_mfma_f32_16x16x32_bf16 v[68:71], a[144:147], a[64:67], v[68:71]\n" \
"buffer_load_dwordx4 v52, s[8:11], s64 offen offset:1024 sc1 lds\n" \
"v_mfma_f32_16x16x32_bf16 v[76:79], a[144:147], a[80:83], v[36:39]\n" \
"v_exp_f32_e64 v120, v120\n" \
"v_mfma_f32_16x16x32_bf16 v[76:79], a[148:151], a[84:87], v[76:79]\n" \
"v_exp_f32_e64 v121, v121\n" \
"v_mfma_f32_16x16x32_bf16 v[76:79], a[152:155], a[88:91], v[76:79]\n" \
"v_exp_f32_e64 v122, v122\n" \
"v_mfma_f32_16x16x32_bf16 v[76:79], a[156:159], a[92:95], v[76:79]\n" \
"v_exp_f32_e64 v123, v123\n" \
"v_mfma_f32_16x16x32_bf16 v[72:75], a[140:143], a[92:95], v[36:39]\n" \
"v_cvt_pk_bf16_f32 v120, v120, v121\n" \
"v_cvt_pk_bf16_f32 v121, v122, v123\n" \
"v_mfma_f32_16x16x32_bf16 v[72:75], a[136:139], a[88:91], v[72:75]\n" \
"v_exp_f32_e64 v124, v124\n" \
"v_mfma_f32_16x16x32_bf16 v[72:75], a[132:135], a[84:87], v[72:75]\n" \
"v_exp_f32_e64 v125, v125\n" \
"v_mfma_f32_16x16x32_bf16 v[72:75], a[128:131], a[80:83], v[72:75]\n" \
"buffer_load_dwordx4 v52, s[8:11], s64 offen offset:2048 sc1 lds\n" \
"v_mfma_f32_16x16x32_bf16 v[80:83], a[128:131], a[96:99], v[40:43]\n" \
"v_exp_f32_e64 v126, v126\n" \
"v_mfma_f32_16x16x32_bf16 v[80:83], a[132:135], a[100:103], v[80:83]\n" \
"v_exp_f32_e64 v127, v127\n" \
"v_mfma_f32_16x16x32_bf16 v[80:83], a[136:139], a[104:107], v[80:83]\n" \
"v_cvt_pk_bf16_f32 v122, v124, v125\n" \
"v_cvt_pk_bf16_f32 v123, v126, v127\n" \
"v_mfma_f32_16x16x32_bf16 v[80:83], a[140:143], a[108:111], v[80:83]\n" \
"v_exp_f32_e64 v70, v70\n" \
"v_mfma_f32_16x16x32_bf16 v[84:87], a[156:159], a[108:111], v[40:43]\n" \
"v_exp_f32_e64 v71, v71\n" \
"v_mfma_f32_16x16x32_bf16 v[84:87], a[152:155], a[104:107], v[84:87]\n" \
"v_exp_f32_e64 v72, v72\n" \
"v_mfma_f32_16x16x32_bf16 v[84:87], a[148:151], a[100:103], v[84:87]\n" \
"buffer_load_dwordx4 v52, s[8:11], s64 offen offset:3072 sc1 lds\n" \
"v_mfma_f32_16x16x32_bf16 v[84:87], a[144:147], a[96:99], v[84:87]\n" \
"s_mov_b32 m0, s65\n" \
"v_cvt_pk_bf16_f32 v71, v70, v71\n" \
"v_mfma_f32_16x16x32_bf16 v[92:95], a[144:147], a[112:115], v[44:47]\n" \
"ds_read_b128 a[144:147], v48 offset:12288\n" \
"v_add_u32_e32 v49, 0x10000, v48\n" \
"v_mfma_f32_16x16x32_bf16 v[92:95], a[148:151], a[116:119], v[92:95]\n" \
"ds_read_b128 a[148:151], v48 offset:13312\n" \
"s_xor_b32 s65, s65, 0x8000\n" \
"v_mfma_f32_16x16x32_bf16 v[92:95], a[152:155], a[120:123], v[92:95]\n" \
"v_exp_f32_e32 v73, v73\n" \
"v_mfma_f32_16x16x32_bf16 v[92:95], a[156:159], a[124:127], v[92:95]\n" \
"ds_read_b128 a[152:155], v48 offset:14336\n" \
".fill (L_ab + 8192 - .) / 4, 4, 0xBF800000\n" \
"L_ap2:\n" \
"s_cbranch_execz L_ap0\n" \
"ds_read_b128 a[156:159], v48 offset:15360\n" \
"v_mfma_f32_16x16x32_bf16 v[88:91], a[140:143], a[124:127], v[44:47]\n" \
"ds_read_b128 a[140:143], v48 offset:11264\n" \
"v_cvt_pk_bf16_f32 v72, v72, v73\n" \
"v_mfma_f32_16x16x32_bf16 v[88:91], a[136:139], a[120:123], v[88:91]\n" \
"v_exp_f32_e32 v68, v68\n" \
"v_mfma_f32_16x16x32_bf16 v[88:91], a[132:135], a[116:119], v[88:91]\n" \
"buffer_load_dwordx4 v53, s[8:11], s64 offen offset:0 sc1 lds\n" \
"v_mfma_f32_16x16x32_bf16 v[88:91], a[128:131], a[112:115], v[88:91]\n" \
"ds_read_b128 a[128:131], v48 offset:8192\n" \
"s_waitcnt lgkmcnt(6)\n" \
"v_mfma_f32_16x16x32_bf16 v[128:131], a[160:163], v[100:103], v[128:131]\n" \
"ds_read_b128 a[132:135], v48 offset:9216\n" \
"ds_read_b128 a[136:139], v48 offset:10240\n" \
"v_mfma_f32_16x16x32_bf16 v[160:163], a[160:163], v[104:107], v[160:163]\n" \
"v_exp_f32_e32 v69, v69\n" \
"v_mfma_f32_16x16x32_bf16 v[132:135], a[164:167], v[100:103], v[132:135]\n" \
"v_exp_f32_e64 v74, v74\n" \
"v_mfma_f32_16x16x32_bf16 v[164:167], a[164:167], v[104:107], v[164:167]\n" \
"v_exp_f32_e64 v75, v75\n" \
"v_mfma_f32_16x16x32_bf16 v[136:139], a[168:171], v[100:103], v[136:139]\n" \
"v_exp_f32_e64 v66, v66\n" \
"v_mfma_f32_16x16x32_bf16 v[168:171], a[168:171], v[104:107], v[168:171]\n" \
"buffer_load_dwordx4 v53, s[8:11], s64 offen offset:1024 sc1 lds\n" \
"v_mfma_f32_16x16x32_bf16 v[140:143], a[172:175], v[100:103], v[140:143]\n" \
"v_exp_f32_e64 v67, v67\n" \
"v_mfma_f32_16x16x32_bf16 v[172:175], a[172:175], v[104:107], v[172:175]\n" \
"v_exp_f32_e64 v64, v64\n" \
"v_mfma_f32_16x16x32_bf16 v[144:147], a[176:179], v[100:103], v[144:147]\n" \
"v_exp_f32_e64 v65, v65\n" \
"v_mfma_f32_16x16x32_bf16 v[176:179], a[176:179], v[104:107], v[176:179]\n" \
"v_exp_f32_e64 v76, v76\n" \
"v_mfma_f32_16x16x32_bf16 v[148:151], a[180:183], v[100:103], v[148:151]\n" \
"v_exp_f32_e64 v77, v77\n" \
"v_mfma_f32_16x16x32_bf16 v[180:183], a[180:183], v[104:107], v[180:183]\n" \
"v_exp_f32_e64 v78, v78\n" \
"v_mfma_f32_16x16x32_bf16 v[152:155], a[184:187], v[100:103], v[152:155]\n" \
"v_exp_f32_e64 v79, v79\n" \
"v_mfma_f32_16x16x32_bf16 v[184:187], a[184:187], v[104:107], v[184:187]\n" \
"buffer_load_dwordx4 v53, s[8:11], s64 offen offset:2048 sc1 lds\n" \
"v_mfma_f32_16x16x32_bf16 v[156:159], a[188:191], v[100:103], v[156:159]\n" \
"v_cvt_pk_bf16_f32 v70, v68, v69\n" \
"v_cvt_pk_bf16_f32 v73, v74, v75\n" \
"v_mfma_f32_16x16x32_bf16 v[188:191], a[188:191], v[104:107], v[188:191]\n" \
"v_cvt_pk_bf16_f32 v69, v66, v67\n" \
"v_cvt_pk_bf16_f32 v68, v64, v65\n" \
"v_smfmac_f32_16x16x64_bf16 a[192:195], a[200:203], v[100:107], v21\n" \
"v_cvt_pk_bf16_f32 v74, v76, v77\n" \
"v_cvt_pk_bf16_f32 v75, v78, v79\n" \
"v_mfma_f32_16x16x32_bf16 v[220:223], a[188:191], v[116:119], v[220:223]\n" \
"v_xor_b32_e32 v48, 0x8000, v48\n" \
"v_mfma_f32_16x16x32_bf16 v[252:255], a[188:191], v[120:123], v[252:255]\n" \
"v_mfma_f32_16x16x32_bf16 v[216:219], a[184:187], v[116:119], v[216:219]\n" \
"v_mfma_f32_16x16x32_bf16 v[248:251], a[184:187], v[120:123], v[248:251]\n" \
"buffer_load_dwordx4 v53, s[8:11], s64 offen offset:3072 sc1 lds\n" \
"v_mfma_f32_16x16x32_bf16 v[212:215], a[180:183], v[116:119], v[212:215]\n" \
"s_add_u32 s64, s64, 0x10000\n" \
"s_sub_u32 s66, s66, 1\n" \
"s_nop 0\n" \
"v_mfma_f32_16x16x32_bf16 v[244:247], a[180:183], v[120:123], v[244:247]\n" \
"v_mfma_f32_16x16x32_bf16 v[208:211], a[176:179], v[116:119], v[208:211]\n" \
"v_mfma_f32_16x16x32_bf16 v[240:243], a[176:179], v[120:123], v[240:243]\n" \
"v_mfma_f32_16x16x32_bf16 v[204:207], a[172:175], v[116:119], v[204:207]\n" \
"v_mfma_f32_16x16x32_bf16 v[236:239], a[172:175], v[120:123], v[236:239]\n" \
"v_mfma_f32_16x16x32_bf16 v[200:203], a[168:171], v[116:119], v[200:203]\n" \
"v_mfma_f32_16x16x32_bf16 v[232:235], a[168:171], v[120:123], v[232:235]\n" \
"v_mfma_f32_16x16x32_bf16 v[196:199], a[164:167], v[116:119], v[196:199]\n" \
"v_mfma_f32_16x16x32_bf16 v[228:231], a[164:167], v[120:123], v[228:231]\n" \
"v_mfma_f32_16x16x32_bf16 v[192:195], a[160:163], v[116:119], v[192:195]\n" \
"v_mfma_f32_16x16x32_bf16 v[224:227], a[160:163], v[120:123], v[224:227]\n" \
"v_smfmac_f32_16x16x64_bf16 a[196:199], a[200:203], v[116:123], v21\n" \
"s_waitcnt vmcnt(8) lgkmcnt(0)\n" \
"s_barrier\n" \
"s_cbranch_scc0 L_2_%=\n" \
"s_cmp_lg_u32 s16, 0\n" \
"s_cbranch_scc1 L_f1_%=\n" \
"v_bfe_u32 v58, v20, 2, 7\n" \
"v_readlane_b32 s96, v11, 1\n" \
"ds_read_b128 a[160:163], v49 offset:0\n" \
"ds_read_b128 a[164:167], v49 offset:1024\n" \
"ds_read_b128 a[168:171], v49 offset:2048\n" \
"v_mfma_f32_16x16x32_bf16 v[120:123], a[128:131], a[112:115], v[44:47]\n" \
"v_readlane_b32 s93, v11, 20\n" \
"ds_read_b128 a[172:175], v49 offset:3072\n" \
"buffer_load_dword v55, v58, s[56:59], s96 offen sc1\n" \
"v_readlane_b32 s84, v11, 15\n" \
"v_readlane_b32 s85, v11, 16\n" \
"v_readlane_b32 s86, v11, 17\n" \
"s_mov_b32 s87, 0x20000\n" \
"v_readlane_b32 s92, v11, 18\n" \
"s_add_u32 m0, s93, 0x0\n" \
"v_mfma_f32_16x16x32_bf16 v[120:123], a[132:135], a[116:119], v[120:123]\n" \
"ds_read_b128 a[176:179], v49 offset:4096\n" \
"v_add_u32_e32 v54, 0x49000, v20\n" \
"v_mfma_f32_16x16x32_bf16 v[120:123], a[136:139], a[120:123], v[120:123]\n" \
"v_readlane_b32 s97, v11, 35\n" \
"v_readlane_b32 s98, v11, 36\n" \
"v_mfma_f32_16x16x32_bf16 v[120:123], a[140:143], a[124:127], v[120:123]\n" \
"ds_read_b128 a[180:183], v49 offset:5120\n" \
"ds_read_b128 a[184:187], v49 offset:6144\n" \
"v_mfma_f32_16x16x32_bf16 v[124:127], a[156:159], a[124:127], v[44:47]\n" \
"ds_read_b128 a[188:191], v49 offset:7168\n" \
"v_add_u32_e32 v50, s15, v20\n" \
"buffer_load_dwordx4 v0, s[84:87], s92 offen offset:0 lds\n" \
"v_mfma_f32_16x16x32_bf16 v[124:127], a[152:155], a[120:123], v[124:127]\n" \
"v_exp_f32_e32 v86, v86\n" \
"v_mfma_f32_16x16x32_bf16 v[124:127], a[148:151], a[116:119], v[124:127]\n" \
"v_exp_f32_e32 v87, v87\n" \
"v_mfma_f32_16x16x32_bf16 v[124:127], a[144:147], a[112:115], v[124:127]\n" \
"v_cvt_pk_bf16_f32 v87, v86, v87\n" \
"s_waitcnt lgkmcnt(7)\n" \
"v_mfma_f32_16x16x32_bf16 v[128:131], a[160:163], v[68:71], v[128:131]\n" \
"v_exp_f32_e32 v84, v84\n" \
"v_mfma_f32_16x16x32_bf16 v[160:163], a[160:163], v[72:75], v[160:163]\n" \
"v_and_b32_e32 v87, v15, v87\n" \
"s_waitcnt lgkmcnt(6)\n" \
"v_mfma_f32_16x16x32_bf16 v[132:135], a[164:167], v[68:71], v[132:135]\n" \
"buffer_load_dwordx4 v1, s[84:87], s92 offen offset:1024 lds\n" \
"v_mfma_f32_16x16x32_bf16 v[164:167], a[164:167], v[72:75], v[164:167]\n" \
"v_add_u32_e32 v51, 0x10000, v50\n" \
"s_waitcnt lgkmcnt(5)\n" \
"v_mfma_f32_16x16x32_bf16 v[136:139], a[168:171], v[68:71], v[136:139]\n" \
"v_exp_f32_e32 v85, v85\n" \
"v_mfma_f32_16x16x32_bf16 v[168:171], a[168:171], v[72:75], v[168:171]\n" \
"v_cvt_pk_bf16_f32 v86, v84, v85\n" \
"s_waitcnt lgkmcnt(4)\n" \
"v_mfma_f32_16x16x32_bf16 v[140:143], a[172:175], v[68:71], v[140:143]\n" \
"v_exp_f32_e32 v82, v82\n" \
"v_mfma_f32_16x16x32_bf16 v[172:175], a[172:175], v[72:75], v[172:175]\n" \
"v_and_b32_e32 v86, v14, v86\n" \
"s_waitcnt lgkmcnt(3)\n" \
"v_mfma_f32_16x16x32_bf16 v[144:147], a[176:179], v[68:71], v[144:147]\n" \
"buffer_load_dwordx4 v2, s[84:87], s92 offen offset:2048 lds\n" \
"v_mfma_f32_16x16x32_bf16 v[176:179], a[176:179], v[72:75], v[176:179]\n" \
"v_cvt_pk_bf16_f32 v128, v128, v129\n" \
"s_waitcnt lgkmcnt(1)\n" \
"v_mfma_f32_16x16x32_bf16 v[148:151], a[180:183], v[68:71], v[148:151]\n" \
"v_exp_f32_e32 v83, v83\n" \
"v_mfma_f32_16x16x32_bf16 v[180:183], a[180:183], v[72:75], v[180:183]\n" \
"v_exp_f32_e32 v80, v80\n" \
"v_mfma_f32_16x16x32_bf16 v[152:155], a[184:187], v[68:71], v[152:155]\n" \
"v_exp_f32_e32 v81, v81\n" \
"v_mfma_f32_16x16x32_bf16 v[184:187], a[184:187], v[72:75], v[184:187]\n" \
"v_cvt_pk_bf16_f32 v85, v82, v83\n" \
"s_waitcnt lgkmcnt(0)\n" \
"v_mfma_f32_16x16x32_bf16 v[156:159], a[188:191], v[68:71], v[156:159]\n" \
"buffer_load_dwordx4 v3, s[84:87], s92 offen offset:3072 lds\n" \
"s_add_u32 m0, s93, 0x1000\n" \
"v_mfma_f32_16x16x32_bf16 v[188:191], a[188:191], v[72:75], v[188:191]\n" \
"v_cvt_pk_bf16_f32 v84, v80, v81\n" \
"v_and_b32_e32 v84, v12, v84\n" \
"v_smfmac_f32_16x16x64_bf16 a[192:195], a[200:203], v[68:75], v21\n" \
"v_and_b32_e32 v85, v13, v85\n" \
"v_exp_f32_e32 v88, v88\n" \
"v_exp_f32_e32 v89, v89\n" \
"v_mfma_f32_16x16x32_bf16 v[220:223], a[188:191], v[84:87], v[220:223]\n" \
"v_exp_f32_e32 v90, v90\n" \
"v_exp_f32_e32 v91, v91\n" \
"v_cvt_pk_bf16_f32 v88, v88, v89\n" \
"v_exp_f32_e32 v92, v92\n" \
"buffer_load_dwordx4 v4, s[84:87], s92 offen offset:0 lds\n" \
"v_exp_f32_e32 v93, v93\n" \
"v_exp_f32_e32 v94, v94\n" \
"v_exp_f32_e32 v95, v95\n" \
"v_cvt_pk_bf16_f32 v160, v160, v161\n" \
"v_cvt_pk_bf16_f32 v89, v90, v91\n" \
"v_cvt_pk_bf16_f32 v129, v130, v131\n" \
"v_cvt_pk_bf16_f32 v161, v162, v163\n" \
"v_cvt_pk_bf16_f32 v90, v92, v93\n" \
"v_cvt_pk_bf16_f32 v91, v94, v95\n" \
"v_cvt_pk_bf16_f32 v130, v132, v133\n" \
"v_cvt_pk_bf16_f32 v131, v134, v135\n" \
"v_cvt_pk_bf16_f32 v162, v164, v165\n" \
"v_cvt_pk_bf16_f32 v163, v166, v167\n" \
"v_mfma_f32_16x16x32_bf16 v[252:255], a[188:191], v[88:91], v[252:255]\n" \
"v_cvt_pk_bf16_f32 v132, v136, v137\n" \
"v_cvt_pk_bf16_f32 v133, v138, v139\n" \
"v_cvt_pk_bf16_f32 v134, v140, v141\n" \
"buffer_load_dwordx4 v5, s[84:87], s92 offen offset:1024 lds\n" \
"v_cvt_pk_bf16_f32 v135, v142, v143\n" \
"v_cvt_pk_bf16_f32 v164, v168, v169\n" \
"v_cvt_pk_bf16_f32 v165, v170, v171\n" \
"v_cvt_pk_bf16_f32 v166, v172, v173\n" \
"v_cvt_pk_bf16_f32 v167, v174, v175\n" \
"v_cvt_pk_bf16_f32 v136, v144, v145\n" \
"v_cvt_pk_bf16_f32 v137, v146, v147\n" \
"v_cvt_pk_bf16_f32 v138, v148, v149\n" \
"v_cvt_pk_bf16_f32 v139, v150, v151\n" \
"v_cvt_pk_bf16_f32 v140, v152, v153\n" \
"v_cvt_pk_bf16_f32 v141, v154, v155\n" \
"v_cvt_pk_bf16_f32 v142, v156, v157\n" \
"v_cvt_pk_bf16_f32 v143, v158, v159\n" \
"v_cvt_pk_bf16_f32 v168, v176, v177\n" \
"v_cvt_pk_bf16_f32 v169, v178, v179\n" \
"v_cvt_pk_bf16_f32 v170, v180, v181\n" \
"v_cvt_pk_bf16_f32 v171, v182, v183\n" \
"v_cvt_pk_bf16_f32 v172, v184, v185\n" \
"v_cvt_pk_bf16_f32 v173, v186, v187\n" \
"v_cvt_pk_bf16_f32 v174, v188, v189\n" \
"v_cvt_pk_bf16_f32 v175, v190, v191\n" \
"buffer_load_dwordx4 v6, s[84:87], s92 offen offset:2048 lds\n" \
"s_waitcnt vmcnt(8)\n" \
"ds_write_b128 v50, v[128:131] offset:0\n" \
"v_mfma_f32_16x16x32_bf16 v[216:219], a[184:187], v[84:87], v[216:219]\n" \
"ds_read_b32 v60, v18 offset:0\n" \
"ds_read_b32 v61, v18 offset:128\n" \
"v_mfma_f32_16x16x32_bf16 v[248:251], a[184:187], v[88:91], v[248:251]\n" \
"ds_write_b128 v50, v[132:135] offset:1024\n" \
"s_waitcnt vmcnt(7)\n" \
"v_cmp_ne_u32_e32 vcc, s62, v55\n" \
"v_mfma_f32_16x16x32_bf16 v[212:215], a[180:183], v[84:87], v[212:215]\n" \
"ds_read_b128 a[0:3], v49 offset:8192\n" \
"ds_read_b128 a[4:7], v49 offset:9216\n" \
"ds_write_b128 v50, v[136:139] offset:2048\n" \
"v_mfma_f32_16x16x32_bf16 v[244:247], a[180:183], v[88:91], v[244:247]\n" \
"buffer_load_dwordx4 v7, s[84:87], s92 offen offset:3072 lds\n" \
"s_add_u32 m0, s93, 0x2000\n" \
"s_cbranch_vccnz L_so0_%=\n" \
"L_ro0_%=:\n" \
"ds_write_b128 v50, v[140:143] offset:3072\n" \
"ds_read_b128 a[8:11], v49 offset:10240\n" \
"ds_read_b128 a[12:15], v49 offset:11264\n" \
"ds_read_b128 a[16:19], v49 offset:12288\n" \
"ds_read_b128 a[20:23], v49 offset:13312\n" \
"ds_read_b128 a[24:27], v49 offset:14336\n" \
"ds_read_b128 a[28:31], v49 offset:15360\n" \
"ds_write_b128 v51, v[160:163] offset:32768\n" \
"v_mfma_f32_16x16x32_bf16 v[208:211], a[176:179], v[84:87], v[208:211]\n" \
"v_accvgpr_read_b32 v40, a192\n" \
"v_accvgpr_read_b32 v22, a193\n" \
"v_mfma_f32_16x16x32_bf16 v[240:243], a[176:179], v[88:91], v[240:243]\n" \
"ds_write_b128 v51, v[164:167] offset:33792\n" \
"v_mfma_f32_16x16x32_bf16 v[204:207], a[172:175], v[84:87], v[204:207]\n" \
"buffer_load_dwordx4 v8, s[84:87], s92 offen offset:0 lds\n" \
"v_mfma_f32_16x16x32_bf16 v[236:239], a[172:175], v[88:91], v[236:239]\n" \
"ds_write_b128 v51, v[168:171] offset:34816\n" \
"v_mfma_f32_16x16x32_bf16 v[200:203], a[168:171], v[84:87], v[200:203]\n" \
"v_accvgpr_read_b32 v41, a194\n" \
"v_accvgpr_read_b32 v23, a195\n" \
"v_mfma_f32_16x16x32_bf16 v[232:235], a[168:171], v[88:91], v[232:235]\n" \
"ds_write_b128 v51, v[172:175] offset:35840\n" \
"v_mfma_f32_16x16x32_bf16 v[196:199], a[164:167], v[84:87], v[196:199]\n" \
"v_add_f32_e32 v40, v40, v22\n" \
"v_add_f32_e32 v41, v41, v23\n" \
"v_mfma_f32_16x16x32_bf16 v[228:231], a[164:167], v[88:91], v[228:231]\n" \
"ds_write_b64 v19, v[40:41]\n" \
"v_lshl_add_u32 v52, s15, 2, v20\n" \
"v_mfma_f32_16x16x32_bf16 v[192:195], a[160:163], v[84:87], v[192:195]\n" \
"buffer_load_dwordx4 a[220:223], v20, s[56:59], s60 offen offset:0 sc1\n" \
"v_mfma_f32_16x16x32_bf16 v[224:227], a[160:163], v[88:91], v[224:227]\n" \
"v_add_u32_e32 v52, 0x1a000, v52\n" \
"v_xor_b32_e32 v63, 0x400, v19\n" \
"v_smfmac_f32_16x16x64_bf16 a[196:199], a[200:203], v[84:91], v21\n" \
"v_exp_f32_e32 v120, v120\n" \
"v_exp_f32_e32 v121, v121\n" \
"v_exp_f32_e32 v122, v122\n" \
"v_exp_f32_e32 v123, v123\n" \
"v_cvt_pk_bf16_f32 v120, v120, v121\n" \
"v_exp_f32_e32 v124, v124\n" \
"v_exp_f32_e32 v125, v125\n" \
"v_exp_f32_e32 v126, v126\n" \
"buffer_load_dwordx4 a[236:239], v54, s[56:59], s60 offen offset:0 sc1\n" \
"v_exp_f32_e32 v127, v127\n" \
"v_cvt_pk_bf16_f32 v121, v122, v123\n" \
"v_cvt_pk_bf16_f32 v122, v124, v125\n" \
"v_cvt_pk_bf16_f32 v123, v126, v127\n" \
"v_and_b32_e32 v120, v12, v120\n" \
"v_and_b32_e32 v121, v13, v121\n" \
"v_and_b32_e32 v122, v14, v122\n" \
"v_and_b32_e32 v123, v15, v123\n" \
"s_waitcnt lgkmcnt(0)\n" \
"s_barrier\n" \
"ds_read_b128 v[144:147], v52 offset:0\n" \
"ds_read_b128 v[148:151], v52 offset:1024\n" \
"ds_read_b128 v[152:155], v52 offset:2048\n" \
"ds_read_b128 v[156:159], v52 offset:3072\n" \
"buffer_load_dwordx4 a[224:227], v20, s[56:59], s60 offen offset:1024 sc1\n" \
"ds_read_b128 v[176:179], v52 offset:4096\n" \
"ds_read_b128 v[180:183], v52 offset:5120\n" \
"ds_read_b128 v[184:187], v52 offset:6144\n" \
"ds_read_b128 v[188:191], v52 offset:7168\n" \
"v_mfma_f32_16x16x32_bf16 a[196:199], a[212:215], v[120:123], a[196:199]\n" \
"ds_read_b64 v[42:43], v63\n" \
"v_mov_b32_e32 v130, v28\n" \
"v_mfma_f32_16x16x32_bf16 v[224:227], a[0:3], v[120:123], v[224:227]\n" \
"v_mov_b32_e32 v131, v29\n" \
"v_mov_b32_e32 v132, 0\n" \
"v_mfma_f32_16x16x32_bf16 v[228:231], a[4:7], v[120:123], v[228:231]\n" \
"v_mov_b32_e32 v133, 0\n" \
"v_mov_b32_e32 v128, 0\n" \
"v_mfma_f32_16x16x32_bf16 v[232:235], a[8:11], v[120:123], v[232:235]\n" \
"v_mov_b32_e32 v129, 0\n" \
"v_accvgpr_read_b32 v56, a196\n" \
"v_mfma_f32_16x16x32_bf16 v[236:239], a[12:15], v[120:123], v[236:239]\n" \
"buffer_load_dwordx4 a[240:243], v54, s[56:59], s60 offen offset:1024 sc1\n" \
"v_mfma_f32_16x16x32_bf16 v[240:243], a[16:19], v[120:123], v[240:243]\n" \
"v_accvgpr_read_b32 v24, a197\n" \
"v_add_f32_e32 v56, v56, v24\n" \
"v_mfma_f32_16x16x32_bf16 v[244:247], a[20:23], v[120:123], v[244:247]\n" \
"v_accvgpr_read_b32 v57, a198\n" \
"v_accvgpr_read_b32 v25, a199\n" \
"v_mfma_f32_16x16x32_bf16 v[248:251], a[24:27], v[120:123], v[248:251]\n" \
"v_add_f32_e32 v57, v57, v25\n" \
"v_mfma_f32_16x16x32_bf16 v[252:255], a[28:31], v[120:123], v[252:255]\n" \
"s_waitcnt lgkmcnt(1)\n" \
"s_barrier\n" \
"buffer_load_dwordx4 a[228:231], v20, s[56:59], s60 offen offset:2048 sc1\n" \
"v_mfma_f32_16x16x32_bf16 v[192:195], v[130:133], v[144:147], v[192:195]\n" \
"s_waitcnt lgkmcnt(0)\n" \
"v_add_f32_e32 v56, v56, v42\n" \
"v_mfma_f32_16x16x32_bf16 v[196:199], v[128:131], v[144:147], v[196:199]\n" \
"v_max_f32_e32 v62, v62, v56\n" \
"v_add_f32_e32 v57, v57, v43\n" \
"v_mfma_f32_16x16x32_bf16 v[200:203], v[130:133], v[148:151], v[200:203]\n" \
"v_rcp_f32_e32 v56, v56\n" \
"v_mfma_f32_16x16x32_bf16 v[204:207], v[128:131], v[148:151], v[204:207]\n" \
"v_mul_f32_e32 v56, v56, v60\n" \
"v_mul_f32_e32 v192, v56, v192\n" \
"v_mfma_f32_16x16x32_bf16 v[208:211], v[130:133], v[152:155], v[208:211]\n" \
"v_mul_f32_e32 v193, v56, v193\n" \
"v_mul_f32_e32 v194, v56, v194\n" \
"v_mfma_f32_16x16x32_bf16 v[212:215], v[128:131], v[152:155], v[212:215]\n" \
"buffer_load_dwordx4 a[244:247], v54, s[56:59], s60 offen offset:2048 sc1\n" \
"v_mfma_f32_16x16x32_bf16 v[216:219], v[130:133], v[156:159], v[216:219]\n" \
"v_mul_f32_e32 v195, v56, v195\n" \
"v_mul_f32_e32 v196, v56, v196\n" \
"v_mfma_f32_16x16x32_bf16 v[220:223], v[128:131], v[156:159], v[220:223]\n" \
"v_mul_f32_e32 v197, v56, v197\n" \
"v_mul_f32_e32 v198, v56, v198\n" \
"v_mfma_f32_16x16x32_bf16 v[224:227], v[130:133], v[176:179], v[224:227]\n" \
"v_mul_f32_e32 v199, v56, v199\n" \
"v_cvt_pk_bf16_f32 v64, v192, v193\n" \
"v_mfma_f32_16x16x32_bf16 v[228:231], v[128:131], v[176:179], v[228:231]\n" \
"v_cvt_pk_bf16_f32 v65, v194, v195\n" \
"v_cvt_pk_bf16_f32 v66, v196, v197\n" \
"v_mfma_f32_16x16x32_bf16 v[232:235], v[130:133], v[180:183], v[232:235]\n" \
"v_cvt_pk_bf16_f32 v67, v198, v199\n" \
"v_mul_f32_e32 v200, v56, v200\n" \
"v_mfma_f32_16x16x32_bf16 v[236:239], v[128:131], v[180:183], v[236:239]\n" \
"buffer_load_dwordx4 a[232:235], v20, s[56:59], s60 offen offset:3072 sc1\n" \
"v_mfma_f32_16x16x32_bf16 v[240:243], v[130:133], v[184:187], v[240:243]\n" \
"v_mfma_f32_16x16x32_bf16 a[208:211], a[216:219], v[64:67], a[208:211]\n" \
"v_mul_f32_e32 v201, v56, v201\n" \
"v_mul_f32_e32 v202, v56, v202\n" \
"v_mul_f32_e32 v203, v56, v203\n" \
"v_mul_f32_e32 v204, v56, v204\n" \
"v_mul_f32_e32 v205, v56, v205\n" \
"v_mul_f32_e32 v206, v56, v206\n" \
"v_mul_f32_e32 v207, v56, v207\n" \
"v_cvt_pk_bf16_f32 v68, v200, v201\n" \
"v_cvt_pk_bf16_f32 v69, v202, v203\n" \
"v_cvt_pk_bf16_f32 v70, v204, v205\n" \
"v_cvt_pk_bf16_f32 v71, v206, v207\n" \
"v_mul_f32_e32 v208, v56, v208\n" \
"v_mul_f32_e32 v209, v56, v209\n" \
"v_mfma_f32_16x16x32_bf16 a[208:211], a[216:219], v[68:71], a[208:211]\n" \
"buffer_load_dwordx4 a[248:251], v54, s[56:59], s60 offen offset:3072 sc1\n" \
"v_mul_f32_e32 v210, v56, v210\n" \
"v_mul_f32_e32 v211, v56, v211\n" \
"v_mul_f32_e32 v212, v56, v212\n" \
"v_mul_f32_e32 v213, v56, v213\n" \
"v_mul_f32_e32 v214, v56, v214\n" \
"v_mul_f32_e32 v215, v56, v215\n" \
"v_max_f32_e32 v62, v62, v57\n" \
"v_cvt_pk_bf16_f32 v72, v208, v209\n" \
"v_cvt_pk_bf16_f32 v73, v210, v211\n" \
"v_cvt_pk_bf16_f32 v74, v212, v213\n" \
"v_cvt_pk_bf16_f32 v75, v214, v215\n" \
"v_rcp_f32_e32 v57, v57\n" \
"v_mfma_f32_16x16x32_bf16 v[244:247], v[128:131], v[184:187], v[244:247]\n" \
"v_mfma_f32_16x16x32_bf16 a[208:211], a[216:219], v[72:75], a[208:211]\n" \
"v_mul_f32_e32 v216, v56, v216\n" \
"v_mul_f32_e32 v217, v56, v217\n" \
"buffer_load_dwordx4 a[96:99], v20, s[56:59], s97 offen offset:0 sc1\n" \
"v_mul_f32_e32 v218, v56, v218\n" \
"v_mul_f32_e32 v219, v56, v219\n" \
"v_mul_f32_e32 v220, v56, v220\n" \
"v_mul_f32_e32 v221, v56, v221\n" \
"v_mul_f32_e32 v222, v56, v222\n" \
"v_mul_f32_e32 v223, v56, v223\n" \
"v_cvt_pk_bf16_f32 v76, v216, v217\n" \
"v_cvt_pk_bf16_f32 v77, v218, v219\n" \
"v_cvt_pk_bf16_f32 v78, v220, v221\n" \
"v_cvt_pk_bf16_f32 v79, v222, v223\n" \
"v_mul_f32_e32 v57, v57, v61\n" \
"v_mul_f32_e32 v224, v57, v224\n" \
"v_mfma_f32_16x16x32_bf16 a[208:211], a[216:219], v[76:79], a[208:211]\n" \
"v_mul_f32_e32 v225, v57, v225\n" \
"v_mul_f32_e32 v226, v57, v226\n" \
"v_mul_f32_e32 v227, v57, v227\n" \
"v_mul_f32_e32 v228, v57, v228\n" \
"v_mul_f32_e32 v229, v57, v229\n" \
"v_mul_f32_e32 v230, v57, v230\n" \
"v_mul_f32_e32 v231, v57, v231\n" \
"buffer_load_dwordx4 a[104:107], v54, s[56:59], s97 offen offset:0 sc1\n" \
"v_cvt_pk_bf16_f32 v80, v224, v225\n" \
"v_cvt_pk_bf16_f32 v81, v226, v227\n" \
"v_cvt_pk_bf16_f32 v82, v228, v229\n" \
"v_cvt_pk_bf16_f32 v83, v230, v231\n" \
"v_mfma_f32_16x16x32_bf16 v[248:251], v[130:133], v[188:191], v[248:251]\n" \
"v_mul_f32_e32 v232, v57, v232\n" \
"v_mul_f32_e32 v233, v57, v233\n" \
"v_mfma_f32_16x16x32_bf16 a[208:211], a[216:219], v[80:83], a[208:211]\n" \
"v_mul_f32_e32 v234, v57, v234\n" \
"v_mul_f32_e32 v235, v57, v235\n" \
"v_mul_f32_e32 v236, v57, v236\n" \
"v_mul_f32_e32 v237, v57, v237\n" \
"v_mul_f32_e32 v238, v57, v238\n" \
"v_mul_f32_e32 v239, v57, v239\n" \
"v_cvt_pk_bf16_f32 v84, v232, v233\n" \
"v_cvt_pk_bf16_f32 v85, v234, v235\n" \
"v_cvt_pk_bf16_f32 v86, v236, v237\n" \
"v_cvt_pk_bf16_f32 v87, v238, v239\n" \
"v_mul_f32_e32 v240, v57, v240\n" \
"buffer_load_dwordx4 a[112:115], v20, s[56:59], s98 offen offset:0 sc1\n" \
"v_mfma_f32_16x16x32_bf16 a[208:211], a[216:219], v[84:87], a[208:211]\n" \
"v_mul_f32_e32 v241, v57, v241\n" \
"v_mul_f32_e32 v242, v57, v242\n" \
"v_mul_f32_e32 v243, v57, v243\n" \
"v_mul_f32_e32 v244, v57, v244\n" \
"v_mul_f32_e32 v245, v57, v245\n" \
"v_mul_f32_e32 v246, v57, v246\n" \
"v_mul_f32_e32 v247, v57, v247\n" \
"v_cvt_pk_bf16_f32 v96, v240, v241\n" \
"v_cvt_pk_bf16_f32 v97, v242, v243\n" \
"v_cvt_pk_bf16_f32 v98, v244, v245\n" \
"v_cvt_pk_bf16_f32 v99, v246, v247\n" \
"v_mfma_f32_16x16x32_bf16 v[252:255], v[128:131], v[188:191], v[252:255]\n" \
"v_mul_f32_e32 v248, v57, v248\n" \
"v_mfma_f32_16x16x32_bf16 a[208:211], a[216:219], v[96:99], a[208:211]\n" \
"v_mul_f32_e32 v249, v57, v249\n" \
"v_mul_f32_e32 v250, v57, v250\n" \
"buffer_load_dwordx4 a[120:123], v54, s[56:59], s98 offen offset:0 sc1\n" \
"v_mul_f32_e32 v251, v57, v251\n" \
"v_cvt_pk_bf16_f32 v100, v248, v249\n" \
"v_cvt_pk_bf16_f32 v101, v250, v251\n" \
"v_mul_f32_e32 v252, v57, v252\n" \
"v_mul_f32_e32 v253, v57, v253\n" \
"v_mul_f32_e32 v254, v57, v254\n" \
"v_mul_f32_e32 v255, v57, v255\n" \
"v_cvt_pk_bf16_f32 v102, v252, v253\n" \
"v_cvt_pk_bf16_f32 v103, v254, v255\n" \
"v_cmp_lt_f32_e64 s[72:73], s21, v62\n" \
"buffer_load_dwordx4 a[100:103], v20, s[56:59], s97 offen offset:1024 sc1\n" \
"v_mfma_f32_16x16x32_bf16 a[208:211], a[216:219], v[100:103], a[208:211]\n" \
"buffer_load_dwordx4 a[108:111], v54, s[56:59], s97 offen offset:1024 sc1\n" \
"buffer_load_dwordx4 a[116:119], v20, s[56:59], s98 offen offset:1024 sc1\n" \
"buffer_load_dwordx4 a[124:127], v54, s[56:59], s98 offen offset:1024 sc1\n" \
"s_nop 4\n" \
"v_accvgpr_read_b32 v63, a208\n" \
"v_cmp_u_f32_e32 vcc, v63, v63\n" \
"s_nop 0\n" \
"s_or_b64 vcc, s[72:73], vcc\n" \
"s_cbranch_scc0 L_out_%=\n" \
"s_branch L_end_%=\n" \
"L_skb_%=:\n" \
"s_sleep 1\n" \
"buffer_load_dword v57, v61, s[56:59], s96 offen sc1\n" \
"s_add_u32 s101, s101, 1\n" \
"s_waitcnt vmcnt(0)\n" \
"v_cmp_ne_u32_e32 vcc, s62, v57\n" \
"s_cmp_gt_u32 s101, 0x100000\n" \
"s_cbranch_scc1 L_rkb_%=\n" \
"s_nop 2\n" \
"s_cbranch_vccnz L_skb_%=\n" \
"s_branch L_rkb_%=\n" \
"L_so0_%=:\n" \
"s_sleep 1\n" \
"buffer_load_dword v55, v58, s[56:59], s96 offen sc1\n" \
"s_add_u32 s101, s101, 1\n" \
"s_waitcnt vmcnt(0)\n" \
"v_cmp_ne_u32_e32 vcc, s62, v55\n" \
"s_cmp_gt_u32 s101, 0x100000\n" \
"s_cbranch_scc1 L_ro0_%=\n" \
"s_nop 2\n" \
"s_cbranch_vccnz L_so0_%=\n" \
"s_branch L_ro0_%=\n" \
"L_so1_%=:\n" \
"s_sleep 1\n" \
"buffer_load_dword v57, v60, s[56:59], s96 offen sc1\n" \
"s_add_u32 s101, s101, 1\n" \
"s_waitcnt vmcnt(0)\n" \
"v_cmp_ne_u32_e32 vcc, s62, v57\n" \
"s_cmp_gt_u32 s101, 0x100000\n" \
"s_cbranch_scc1 L_ro1_%=\n" \
"s_nop 2\n" \
"s_cbranch_vccnz L_so1_%=\n" \
"s_branch L_ro1_%=\n" \
"L_sa_%=:\n" \
"s_sleep 1\n" \
"buffer_load_dword v26, v38, s[56:59], s64 offen sc1\n" \
"s_add_u32 s101, s101, 1\n" \
"s_waitcnt vmcnt(0)\n" \
"v_cmp_ne_u32_e32 vcc, s62, v26\n" \
"s_cmp_gt_u32 s101, 0x100000\n" \
"s_cbranch_scc1 L_ra_%=\n" \
"s_nop 2\n" \
"s_cbranch_vccnz L_sa_%=\n" \
"s_branch L_ra_%=\n" \
"L_sb_%=:\n" \
"s_sleep 1\n" \
"buffer_load_dword v47, v38, s[56:59], s73 offen sc1\n" \
"s_add_u32 s101, s101, 1\n" \
"s_waitcnt vmcnt(0)\n" \
"v_cmp_ne_u32_e32 vcc, s62, v47\n" \
"s_cmp_gt_u32 s101, 0x100000\n" \
"s_cbranch_scc1 L_rb_%=\n" \
"s_nop 2\n" \
"s_cbranch_vccnz L_sb_%=\n" \
"s_branch L_rb_%=\n" \
"L_sx0_%=:\n" \
"s_sleep 1\n" \
"buffer_load_dword v37, v40, s[56:59], s74 offen sc1\n" \
"s_add_u32 s101, s101, 1\n" \
"s_waitcnt vmcnt(0)\n" \
"v_cmp_eq_u32_e64 s[98:99], s62, v37\n" \
"s_cmp_gt_u32 s101, 0x100000\n" \
"s_cbranch_scc1 L_rx0_%=\n" \
"s_or_b32 s75, 0x7f, s100\n" \
"s_andn2_b32 s75, s75, s98\n" \
"s_cbranch_scc1 L_sx0_%=\n" \
"s_branch L_rx0_%=\n" \
"L_sx1_%=:\n" \
"s_sleep 1\n" \
"buffer_load_dword v37, v40, s[56:59], s74 offen sc1\n" \
"s_add_u32 s101, s101, 1\n" \
"s_waitcnt vmcnt(0)\n" \
"v_cmp_eq_u32_e64 s[98:99], s62, v37\n" \
"s_cmp_gt_u32 s101, 0x100000\n" \
"s_cbranch_scc1 L_rx1_%=\n" \
"s_andn2_b32 s75, 0x100, s98\n" \
"s_cbranch_scc1 L_sx1_%=\n" \
"s_branch L_rx1_%=\n" \
"L_sx2_%=:\n" \
"s_sleep 1\n" \
"buffer_load_dword v37, v40, s[56:59], s74 offen sc1\n" \
"s_add_u32 s101, s101, 1\n" \
"s_waitcnt vmcnt(0)\n" \
"v_cmp_eq_u32_e64 s[98:99], s62, v37\n" \
"s_cmp_gt_u32 s101, 0x100000\n" \
"s_cbranch_scc1 L_rx2_%=\n" \
"s_andn2_b32 s75, 0x200, s98\n" \
"s_cbranch_scc1 L_sx2_%=\n" \
"s_branch L_rx2_%=\n" \
"L_sx3_%=:\n" \
"s_sleep 1\n" \
"buffer_load_dword v37, v40, s[56:59], s74 offen sc1\n" \
"s_add_u32 s101, s101, 1\n" \
"s_waitcnt vmcnt(0)\n" \
"v_cmp_eq_u32_e64 s[98:99], s62, v37\n" \
"s_cmp_gt_u32 s101, 0x100000\n" \
"s_cbranch_scc1 L_rx3_%=\n" \
"s_andn2_b32 s75, 0x400, s98\n" \
"s_cbranch_scc1 L_sx3_%=\n" \
"s_branch L_rx3_%=\n" \
"L_sx4_%=:\n" \
"s_sleep 1\n" \
"buffer_load_dword v37, v40, s[56:59], s74 offen sc1\n" \
"s_add_u32 s101, s101, 1\n" \
"s_waitcnt vmcnt(0)\n" \
"v_cmp_eq_u32_e64 s[98:99], s62, v37\n" \
"s_cmp_gt_u32 s101, 0x100000\n" \
"s_cbranch_scc1 L_rx4_%=\n" \
"s_andn2_b32 s75, 0x800, s98\n" \
"s_cbranch_scc1 L_sx4_%=\n" \
"s_branch L_rx4_%=\n" \
"L_sx5_%=:\n" \
"s_sleep 1\n" \
"buffer_load_dword v37, v40, s[56:59], s74 offen sc1\n" \
"s_add_u32 s101, s101, 1\n" \
"s_waitcnt vmcnt(0)\n" \
"v_cmp_eq_u32_e64 s[98:99], s62, v37\n" \
"s_cmp_gt_u32 s101, 0x100000\n" \
"s_cbranch_scc1 L_rx5_%=\n" \
"s_andn2_b32 s75, 0x1000, s98\n" \
"s_cbranch_scc1 L_sx5_%=\n" \
"s_branch L_rx5_%=\n" \
"L_sx6_%=:\n" \
"s_sleep 1\n" \
"buffer_load_dword v37, v40, s[56:59], s74 offen sc1\n" \
"s_add_u32 s101, s101, 1\n" \
"s_waitcnt vmcnt(0)\n" \
"v_cmp_eq_u32_e64 s[98:99], s62, v37\n" \
"s_cmp_gt_u32 s101, 0x100000\n" \
"s_cbranch_scc1 L_rx6_%=\n" \
"s_andn2_b32 s75, 0x2000, s98\n" \
"s_cbranch_scc1 L_sx6_%=\n" \
"s_branch L_rx6_%=\n" \
"L_sx7_%=:\n" \
"s_sleep 1\n" \
"buffer_load_dword v37, v40, s[56:59], s74 offen sc1\n" \
"s_add_u32 s101, s101, 1\n" \
"s_waitcnt vmcnt(0)\n" \
"v_cmp_eq_u32_e64 s[98:99], s62, v37\n" \
"s_cmp_gt_u32 s101, 0x100000\n" \
"s_cbranch_scc1 L_rx7_%=\n" \
"s_andn2_b32 s75, 0x4000, s98\n" \
"s_cbranch_scc1 L_sx7_%=\n" \
"s_branch L_rx7_%=\n" \
".fill (L_ab + 12288 - .) / 4, 4, 0xBF800000\n" \
".fill (L_ab + 12288 - .) / 4, 4, 0xBF800000\n" \
"L_ap3:\n" \
"s_setpc_b64 s[88:89]\n" \
"L_sw_%=:\n" \
"v_mfma_f32_16x16x32_bf16 v[104:107], a[128:131], a[16:19], v[36:39]\n" \
"s_add_u32 s68, s60, 0xfff6e000\n" \
"s_add_u32 s69, s60, 0xfffb7000\n" \
"s_add_u32 s70, s60, 0x0\n" \
"s_add_u32 s71, s60, 0x49000\n" \
"v_mfma_f32_16x16x32_bf16 v[104:107], a[132:135], a[20:23], v[104:107]\n" \
"s_xor_b32 m0, s65, 0x12000\n" \
"ds_read_b128 a[160:163], v49 offset:0\n" \
"v_mfma_f32_16x16x32_bf16 v[104:107], a[136:139], a[24:27], v[104:107]\n" \
"buffer_load_dwordx4 v50, s[8:11], s64 offen offset:0 sc1 lds\n" \
"v_mfma_f32_16x16x32_bf16 v[104:107], a[140:143], a[28:31], v[104:107]\n" \
"ds_read_b128 a[164:167], v49 offset:1024\n" \
"ds_read_b128 a[168:171], v49 offset:2048\n" \
"v_mfma_f32_16x16x32_bf16 v[108:111], a[156:159], a[28:31], v[36:39]\n" \
"ds_read_b128 a[172:175], v49 offset:3072\n" \
"ds_read_b128 a[176:179], v49 offset:4096\n" \
"v_mfma_f32_16x16x32_bf16 v[108:111], a[152:155], a[24:27], v[108:111]\n" \
"buffer_load_dwordx4 v50, s[8:11], s64 offen offset:1024 sc1 lds\n" \
"v_mfma_f32_16x16x32_bf16 v[108:111], a[148:151], a[20:23], v[108:111]\n" \
"s_mov_b32 s65, s13\n" \
"v_add_u32_e32 v63, s15, v20\n" \
"v_mfma_f32_16x16x32_bf16 v[108:111], a[144:147], a[16:19], v[108:111]\n" \
"s_add_u32 s67, s14, 40960\n" \
"v_add_u32_e32 v50, 0x6000, v63\n" \
"v_mfma_f32_16x16x32_bf16 v[116:119], a[144:147], a[32:35], v[40:43]\n" \
"s_xor_b32 m0, s65, 0x8000\n" \
"ds_read_b128 a[180:183], v49 offset:5120\n" \
"v_mfma_f32_16x16x32_bf16 v[116:119], a[148:151], a[36:39], v[116:119]\n" \
"ds_read_b128 a[184:187], v49 offset:6144\n" \
"v_and_b32_e32 v68, v12, v68\n" \
"v_mfma_f32_16x16x32_bf16 v[116:119], a[152:155], a[40:43], v[116:119]\n" \
"v_and_b32_e32 v69, v13, v69\n" \
"v_and_b32_e32 v70, v14, v70\n" \
"v_mfma_f32_16x16x32_bf16 v[116:119], a[156:159], a[44:47], v[116:119]\n" \
"buffer_load_dwordx4 v50, s[8:11], s67 offen offset:0 sc1 lds\n" \
"v_mfma_f32_16x16x32_bf16 v[112:115], a[140:143], a[44:47], v[40:43]\n" \
"v_and_b32_e32 v71, v15, v71\n" \
"ds_read_b128 a[188:191], v49 offset:7168\n" \
"v_mfma_f32_16x16x32_bf16 v[112:115], a[136:139], a[40:43], v[112:115]\n" \
"v_exp_f32_e32 v86, v86\n" \
"v_mfma_f32_16x16x32_bf16 v[112:115], a[132:135], a[36:39], v[112:115]\n" \
"v_exp_f32_e32 v87, v87\n" \
"v_mfma_f32_16x16x32_bf16 v[112:115], a[128:131], a[32:35], v[112:115]\n" \
"v_exp_f32_e32 v84, v84\n" \
"v_mfma_f32_16x16x32_bf16 v[120:123], a[128:131], a[48:51], v[44:47]\n" \
"buffer_load_dwordx4 v50, s[8:11], s67 offen offset:1024 sc1 lds\n" \
"v_mfma_f32_16x16x32_bf16 v[120:123], a[132:135], a[52:55], v[120:123]\n" \
"v_exp_f32_e32 v85, v85\n" \
"v_mfma_f32_16x16x32_bf16 v[120:123], a[136:139], a[56:59], v[120:123]\n" \
"v_cvt_pk_bf16_f32 v87, v86, v87\n" \
"v_cvt_pk_bf16_f32 v86, v84, v85\n" \
"v_mfma_f32_16x16x32_bf16 v[120:123], a[140:143], a[60:63], v[120:123]\n" \
"v_exp_f32_e32 v82, v82\n" \
"v_mfma_f32_16x16x32_bf16 v[124:127], a[156:159], a[60:63], v[44:47]\n" \
"v_exp_f32_e32 v83, v83\n" \
"v_mfma_f32_16x16x32_bf16 v[124:127], a[152:155], a[56:59], v[124:127]\n" \
"v_exp_f32_e32 v80, v80\n" \
"v_mfma_f32_16x16x32_bf16 v[124:127], a[148:151], a[52:55], v[124:127]\n" \
"buffer_load_dwordx4 v50, s[8:11], s67 offen offset:2048 sc1 lds\n" \
"v_mfma_f32_16x16x32_bf16 v[124:127], a[144:147], a[48:51], v[124:127]\n" \
"v_cvt_pk_bf16_f32 v85, v82, v83\n" \
"s_waitcnt lgkmcnt(1)\n" \
"v_mfma_f32_16x16x32_bf16 v[128:131], a[160:163], v[68:71], v[128:131]\n" \
"v_exp_f32_e32 v81, v81\n" \
"v_mfma_f32_16x16x32_bf16 v[160:163], a[160:163], v[72:75], v[160:163]\n" \
"v_cvt_pk_bf16_f32 v84, v80, v81\n" \
"s_mov_b32 s64, s14\n" \
"v_mfma_f32_16x16x32_bf16 v[132:135], a[164:167], v[68:71], v[132:135]\n" \
"v_exp_f32_e32 v88, v88\n" \
"v_mfma_f32_16x16x32_bf16 v[164:167], a[164:167], v[72:75], v[164:167]\n" \
"v_exp_f32_e32 v89, v89\n" \
"v_mfma_f32_16x16x32_bf16 v[136:139], a[168:171], v[68:71], v[136:139]\n" \
"buffer_load_dwordx4 v50, s[8:11], s67 offen offset:3072 sc1 lds\n" \
"v_mfma_f32_16x16x32_bf16 v[168:171], a[168:171], v[72:75], v[168:171]\n" \
"v_exp_f32_e32 v90, v90\n" \
"v_mfma_f32_16x16x32_bf16 v[140:143], a[172:175], v[68:71], v[140:143]\n" \
"v_exp_f32_e32 v91, v91\n" \
"v_mfma_f32_16x16x32_bf16 v[172:175], a[172:175], v[72:75], v[172:175]\n" \
"v_cvt_pk_bf16_f32 v88, v88, v89\n" \
"v_cvt_pk_bf16_f32 v89, v90, v91\n" \
"v_mfma_f32_16x16x32_bf16 v[144:147], a[176:179], v[68:71], v[144:147]\n" \
"v_exp_f32_e32 v92, v92\n" \
"v_mfma_f32_16x16x32_bf16 v[176:179], a[176:179], v[72:75], v[176:179]\n" \
"v_exp_f32_e32 v93, v93\n" \
"v_mfma_f32_16x16x32_bf16 v[148:151], a[180:183], v[68:71], v[148:151]\n" \
"v_exp_f32_e32 v94, v94\n" \
"v_mfma_f32_16x16x32_bf16 v[180:183], a[180:183], v[72:75], v[180:183]\n" \
"v_exp_f32_e32 v95, v95\n" \
"v_mfma_f32_16x16x32_bf16 v[152:155], a[184:187], v[68:71], v[152:155]\n" \
"v_cvt_pk_bf16_f32 v90, v92, v93\n" \
"v_cvt_pk_bf16_f32 v91, v94, v95\n" \
"v_mfma_f32_16x16x32_bf16 v[184:187], a[184:187], v[72:75], v[184:187]\n" \
"v_add_u32_e32 v51, 0x12000, v63\n" \
"s_waitcnt lgkmcnt(0)\n" \
"v_mfma_f32_16x16x32_bf16 v[156:159], a[188:191], v[68:71], v[156:159]\n" \
"s_xor_b32 m0, s65, 0xa000\n" \
"ds_read_b32 v58, v18 offset:512\n" \
"v_mfma_f32_16x16x32_bf16 v[188:191], a[188:191], v[72:75], v[188:191]\n" \
"buffer_load_dwordx4 v51, s[8:11], s64 offen offset:0 sc1 lds\n" \
"v_smfmac_f32_16x16x64_bf16 a[192:195], a[200:203], v[68:75], v21\n" \
"buffer_load_dwordx4 v51, s[8:11], s64 offen offset:1024 sc1 lds\n" \
"v_mfma_f32_16x16x32_bf16 v[220:223], a[188:191], v[84:87], v[220:223]\n" \
"ds_read_b32 v59, v18 offset:640\n" \
"ds_read_b32 v60, v18 offset:768\n" \
"v_mfma_f32_16x16x32_bf16 v[252:255], a[188:191], v[88:91], v[252:255]\n" \
"ds_read_b32 v61, v18 offset:896\n" \
"ds_read_b128 a[128:131], v48 offset:0\n" \
"v_mfma_f32_16x16x32_bf16 v[216:219], a[184:187], v[84:87], v[216:219]\n" \
"ds_read_b128 a[144:147], v48 offset:4096\n" \
"ds_read_b128 a[132:135], v48 offset:1024\n" \
"v_mfma_f32_16x16x32_bf16 v[248:251], a[184:187], v[88:91], v[248:251]\n" \
"ds_read_b128 a[148:151], v48 offset:5120\n" \
"ds_read_b128 a[136:139], v48 offset:2048\n" \
"v_mfma_f32_16x16x32_bf16 v[212:215], a[180:183], v[84:87], v[212:215]\n" \
"ds_read_b128 a[152:155], v48 offset:6144\n" \
"ds_read_b128 a[140:143], v48 offset:3072\n" \
"v_mfma_f32_16x16x32_bf16 v[244:247], a[180:183], v[88:91], v[244:247]\n" \
"ds_read_b128 a[156:159], v48 offset:7168\n" \
"v_add_u32_e32 v52, 0x14000, v63\n" \
"v_mfma_f32_16x16x32_bf16 v[208:211], a[176:179], v[84:87], v[208:211]\n" \
"buffer_load_dwordx4 v51, s[8:11], s64 offen offset:2048 sc1 lds\n" \
"v_mfma_f32_16x16x32_bf16 v[240:243], a[176:179], v[88:91], v[240:243]\n" \
"v_exp_f32_e32 v104, v104\n" \
"v_mfma_f32_16x16x32_bf16 v[204:207], a[172:175], v[84:87], v[204:207]\n" \
"v_exp_f32_e32 v105, v105\n" \
"v_mfma_f32_16x16x32_bf16 v[236:239], a[172:175], v[88:91], v[236:239]\n" \
"buffer_load_dwordx4 v51, s[8:11], s64 offen offset:3072 sc1 lds\n" \
"v_mfma_f32_16x16x32_bf16 v[200:203], a[168:171], v[84:87], v[200:203]\n" \
"v_exp_f32_e32 v106, v106\n" \
"v_mfma_f32_16x16x32_bf16 v[232:235], a[168:171], v[88:91], v[232:235]\n" \
"v_exp_f32_e32 v107, v107\n" \
"v_mfma_f32_16x16x32_bf16 v[196:199], a[164:167], v[84:87], v[196:199]\n" \
"v_cvt_pk_bf16_f32 v104, v104, v105\n" \
"v_cvt_pk_bf16_f32 v105, v106, v107\n" \
"v_mfma_f32_16x16x32_bf16 v[228:231], a[164:167], v[88:91], v[228:231]\n" \
"v_exp_f32_e32 v108, v108\n" \
"v_mfma_f32_16x16x32_bf16 v[192:195], a[160:163], v[84:87], v[192:195]\n" \
"v_exp_f32_e32 v109, v109\n" \
"v_mfma_f32_16x16x32_bf16 v[224:227], a[160:163], v[88:91], v[224:227]\n" \
"v_exp_f32_e32 v110, v110\n" \
"v_smfmac_f32_16x16x64_bf16 a[196:199], a[200:203], v[84:91], v21\n" \
"v_exp_f32_e32 v111, v111\n" \
"v_cvt_pk_bf16_f32 v106, v108, v109\n" \
"v_cvt_pk_bf16_f32 v107, v110, v111\n" \
"v_and_b32_e32 v104, v12, v104\n" \
"v_and_b32_e32 v105, v13, v105\n" \
"v_and_b32_e32 v106, v14, v106\n" \
"v_and_b32_e32 v107, v15, v107\n" \
"v_accvgpr_read_b32 v54, a192\n" \
"v_accvgpr_read_b32 v22, a193\n" \
"v_add_u32_e32 v53, 0x20000, v63\n" \
"v_exp_f32_e32 v118, v118\n" \
"v_exp_f32_e32 v119, v119\n" \
"v_exp_f32_e32 v116, v116\n" \
"v_exp_f32_e32 v117, v117\n" \
"v_cvt_pk_bf16_f32 v119, v118, v119\n" \
"v_exp_f32_e32 v114, v114\n" \
"v_exp_f32_e32 v115, v115\n" \
"v_exp_f32_e32 v112, v112\n" \
"v_exp_f32_e32 v113, v113\n" \
"v_cvt_pk_bf16_f32 v118, v116, v117\n" \
"v_cvt_pk_bf16_f32 v117, v114, v115\n" \
"v_cvt_pk_bf16_f32 v116, v112, v113\n" \
"v_readlane_b32 s93, v11, 19\n" \
"s_waitcnt vmcnt(10) lgkmcnt(0)\n" \
"s_barrier\n" \
"v_mfma_f32_16x16x32_bf16 v[80:83], a[128:131], a[32:35], v[40:43]\n" \
"ds_read_b128 a[160:163], v49 offset:8192\n" \
"ds_read_b128 a[164:167], v49 offset:9216\n" \
"v_mfma_f32_16x16x32_bf16 v[80:83], a[132:135], a[36:39], v[80:83]\n" \
"ds_read_b128 a[168:171], v49 offset:10240\n" \
"s_xor_b32 m0, s65, 0x18000\n" \
"v_mfma_f32_16x16x32_bf16 v[80:83], a[136:139], a[40:43], v[80:83]\n" \
"buffer_load_dwordx4 v52, s[8:11], s64 offen offset:0 sc1 lds\n" \
"v_mfma_f32_16x16x32_bf16 v[80:83], a[140:143], a[44:47], v[80:83]\n" \
"ds_read_b128 a[172:175], v49 offset:11264\n" \
"ds_read_b128 a[176:179], v49 offset:12288\n" \
"v_mfma_f32_16x16x32_bf16 v[84:87], a[156:159], a[44:47], v[40:43]\n" \
"ds_read_b128 a[180:183], v49 offset:13312\n" \
"ds_read_b128 a[184:187], v49 offset:14336\n" \
"v_mfma_f32_16x16x32_bf16 v[84:87], a[152:155], a[40:43], v[84:87]\n" \
"buffer_load_dwordx4 v52, s[8:11], s64 offen offset:1024 sc1 lds\n" \
"v_mfma_f32_16x16x32_bf16 v[84:87], a[148:151], a[36:39], v[84:87]\n" \
"ds_read_b128 a[188:191], v49 offset:15360\n" \
"v_readlane_b32 s80, v11, 11\n" \
"v_mfma_f32_16x16x32_bf16 v[84:87], a[144:147], a[32:35], v[84:87]\n" \
"buffer_load_dwordx4 v52, s[8:11], s64 offen offset:2048 sc1 lds\n" \
"v_mfma_f32_16x16x32_bf16 v[92:95], a[144:147], a[48:51], v[44:47]\n" \
"v_exp_f32_e32 v120, v120\n" \
"v_mfma_f32_16x16x32_bf16 v[92:95], a[148:151], a[52:55], v[92:95]\n" \
"v_exp_f32_e32 v121, v121\n" \
"v_mfma_f32_16x16x32_bf16 v[92:95], a[152:155], a[56:59], v[92:95]\n" \
"buffer_load_dwordx4 v52, s[8:11], s64 offen offset:3072 sc1 lds\n" \
"v_mfma_f32_16x16x32_bf16 v[92:95], a[156:159], a[60:63], v[92:95]\n" \
"s_mov_b32 m0, s65\n" \
"v_cvt_pk_bf16_f32 v120, v120, v121\n" \
"v_mfma_f32_16x16x32_bf16 v[88:91], a[140:143], a[60:63], v[44:47]\n" \
"v_exp_f32_e32 v122, v122\n" \
"v_mfma_f32_16x16x32_bf16 v[88:91], a[136:139], a[56:59], v[88:91]\n" \
"buffer_load_dwordx4 v53, s[8:11], s64 offen offset:0 sc1 lds\n" \
"v_mfma_f32_16x16x32_bf16 v[88:91], a[132:135], a[52:55], v[88:91]\n" \
"v_exp_f32_e32 v123, v123\n" \
"v_mfma_f32_16x16x32_bf16 v[88:91], a[128:131], a[48:51], v[88:91]\n" \
"v_exp_f32_e32 v124, v124\n" \
"v_mfma_f32_16x16x32_bf16 a[192:195], a[212:215], v[104:107], a[192:195]\n" \
"v_cvt_pk_bf16_f32 v121, v122, v123\n" \
"s_waitcnt lgkmcnt(5)\n" \
"v_mfma_f32_16x16x32_bf16 v[160:163], a[160:163], v[104:107], v[160:163]\n" \
"buffer_load_dwordx4 v53, s[8:11], s64 offen offset:1024 sc1 lds\n" \
"v_mfma_f32_16x16x32_bf16 v[164:167], a[164:167], v[104:107], v[164:167]\n" \
"v_exp_f32_e32 v125, v125\n" \
"v_mfma_f32_16x16x32_bf16 v[168:171], a[168:171], v[104:107], v[168:171]\n" \
"v_cvt_pk_bf16_f32 v122, v124, v125\n" \
"s_waitcnt lgkmcnt(1)\n" \
"v_mfma_f32_16x16x32_bf16 v[172:175], a[172:175], v[104:107], v[172:175]\n" \
"v_exp_f32_e32 v126, v126\n" \
"v_mfma_f32_16x16x32_bf16 v[176:179], a[176:179], v[104:107], v[176:179]\n" \
"v_exp_f32_e32 v127, v127\n" \
"v_mfma_f32_16x16x32_bf16 v[180:183], a[180:183], v[104:107], v[180:183]\n" \
"v_cvt_pk_bf16_f32 v123, v126, v127\n" \
"v_readlane_b32 s81, v11, 12\n" \
"v_mfma_f32_16x16x32_bf16 v[184:187], a[184:187], v[104:107], v[184:187]\n" \
"s_mov_b32 s82, 0x47fca0\n" \
"s_waitcnt lgkmcnt(0)\n" \
"v_mfma_f32_16x16x32_bf16 v[188:191], a[188:191], v[104:107], v[188:191]\n" \
"buffer_load_dwordx4 v53, s[8:11], s64 offen offset:2048 sc1 lds\n" \
"v_mfma_f32_16x16x32_bf16 v[220:223], a[188:191], v[116:119], v[220:223]\n" \
"buffer_load_dwordx4 v53, s[8:11], s64 offen offset:3072 sc1 lds\n" \
"v_mfma_f32_16x16x32_bf16 v[252:255], a[188:191], v[120:123], v[252:255]\n" \
"s_mov_b32 s83, 0x20000\n" \
"v_readlane_b32 s92, v11, 25\n" \
"v_mfma_f32_16x16x32_bf16 v[216:219], a[184:187], v[116:119], v[216:219]\n" \
"s_add_u32 m0, s93, 0x0\n" \
"ds_read_b128 a[128:131], v48 offset:8192\n" \
"v_mfma_f32_16x16x32_bf16 v[248:251], a[184:187], v[120:123], v[248:251]\n" \
"ds_read_b128 a[144:147], v48 offset:12288\n" \
"ds_read_b128 a[132:135], v48 offset:9216\n" \
"v_mfma_f32_16x16x32_bf16 v[212:215], a[180:183], v[116:119], v[212:215]\n" \
"buffer_load_dwordx4 v0, s[80:83], s92 offen offset:0 lds\n" \
"v_mfma_f32_16x16x32_bf16 v[244:247], a[180:183], v[120:123], v[244:247]\n" \
"buffer_load_dwordx4 v1, s[80:83], s92 offen offset:1024 lds\n" \
"v_mfma_f32_16x16x32_bf16 v[208:211], a[176:179], v[116:119], v[208:211]\n" \
"ds_read_b128 a[148:151], v48 offset:13312\n" \
"ds_read_b128 a[136:139], v48 offset:10240\n" \
"v_mfma_f32_16x16x32_bf16 v[240:243], a[176:179], v[120:123], v[240:243]\n" \
"ds_read_b128 a[152:155], v48 offset:14336\n" \
"ds_read_b128 a[140:143], v48 offset:11264\n" \
"v_mfma_f32_16x16x32_bf16 v[204:207], a[172:175], v[116:119], v[204:207]\n" \
"buffer_load_dwordx4 v2, s[80:83], s92 offen offset:2048 lds\n" \
"v_mfma_f32_16x16x32_bf16 v[236:239], a[172:175], v[120:123], v[236:239]\n" \
"ds_read_b128 a[156:159], v48 offset:15360\n" \
"s_xor_b32 s65, s65, 0x8000\n" \
"v_mfma_f32_16x16x32_bf16 v[200:203], a[168:171], v[116:119], v[200:203]\n" \
"s_add_u32 s64, s64, 0x10000\n" \
"v_add_u32_e32 v49, 0x10000, v48\n" \
"v_mfma_f32_16x16x32_bf16 v[232:235], a[168:171], v[120:123], v[232:235]\n" \
"buffer_load_dwordx4 v3, s[80:83], s92 offen offset:3072 lds\n" \
"v_mfma_f32_16x16x32_bf16 v[196:199], a[164:167], v[116:119], v[196:199]\n" \
"v_exp_f32_e32 v86, v86\n" \
"v_mfma_f32_16x16x32_bf16 v[228:231], a[164:167], v[120:123], v[228:231]\n" \
"v_exp_f32_e32 v87, v87\n" \
"v_mfma_f32_16x16x32_bf16 v[192:195], a[160:163], v[116:119], v[192:195]\n" \
"v_exp_f32_e32 v84, v84\n" \
"v_mfma_f32_16x16x32_bf16 v[224:227], a[160:163], v[120:123], v[224:227]\n" \
"v_exp_f32_e32 v85, v85\n" \
"v_smfmac_f32_16x16x64_bf16 a[196:199], a[200:203], v[116:123], v21\n" \
"v_exp_f32_e32 v88, v88\n" \
"v_exp_f32_e32 v89, v89\n" \
"v_cvt_pk_bf16_f32 v87, v86, v87\n" \
"v_exp_f32_e32 v82, v82\n" \
"v_exp_f32_e32 v83, v83\n" \
"v_exp_f32_e32 v80, v80\n" \
"v_exp_f32_e32 v81, v81\n" \
"v_exp_f32_e32 v90, v90\n" \
"v_exp_f32_e32 v91, v91\n" \
"v_cvt_pk_bf16_f32 v86, v84, v85\n" \
"v_cvt_pk_bf16_f32 v88, v88, v89\n" \
"v_exp_f32_e32 v92, v92\n" \
"v_exp_f32_e32 v93, v93\n" \
"v_exp_f32_e32 v94, v94\n" \
"v_exp_f32_e32 v95, v95\n" \
"v_cvt_pk_bf16_f32 v85, v82, v83\n" \
"v_cvt_pk_bf16_f32 v84, v80, v81\n" \
"v_cvt_pk_bf16_f32 v89, v90, v91\n" \
"v_and_b32_e32 v84, v12, v84\n" \
"v_and_b32_e32 v85, v13, v85\n" \
"v_and_b32_e32 v86, v14, v86\n" \
"v_and_b32_e32 v87, v15, v87\n" \
"v_cvt_pk_bf16_f32 v90, v92, v93\n" \
"v_cvt_pk_bf16_f32 v91, v94, v95\n" \
"v_add_u32_e32 v48, s16, v20\n" \
"v_add_f32_e32 v54, v54, v22\n" \
"v_accvgpr_read_b32 v55, a194\n" \
"v_accvgpr_read_b32 v23, a195\n" \
"v_mfma_f32_16x16x32_bf16 a[192:195], a[216:219], a[216:219], 0\n" \
"v_max_f32_e32 v62, v62, v54\n" \
"v_add_f32_e32 v55, v55, v23\n" \
"v_max_f32_e32 v62, v62, v55\n" \
"v_rcp_f32_e32 v54, v54\n" \
"v_rcp_f32_e32 v55, v55\n" \
"v_mul_f32_e32 v54, v54, v58\n" \
"v_mul_f32_e32 v55, v55, v59\n" \
"v_mul_f32_e32 v128, v54, v128\n" \
"v_mul_f32_e32 v129, v54, v129\n" \
"v_mul_f32_e32 v136, v54, v136\n" \
"v_mul_f32_e32 v137, v54, v137\n" \
"v_mul_f32_e32 v144, v54, v144\n" \
"v_mul_f32_e32 v145, v54, v145\n" \
"v_mul_f32_e32 v152, v54, v152\n" \
"v_mul_f32_e32 v153, v54, v153\n" \
"v_mul_f32_e32 v160, v55, v160\n" \
"v_mul_f32_e32 v161, v55, v161\n" \
"v_mul_f32_e32 v168, v55, v168\n" \
"v_mul_f32_e32 v169, v55, v169\n" \
"v_mul_f32_e32 v176, v55, v176\n" \
"v_mul_f32_e32 v177, v55, v177\n" \
"v_mul_f32_e32 v184, v55, v184\n" \
"v_mul_f32_e32 v185, v55, v185\n" \
"v_cvt_pk_bf16_f32 v128, v128, v129\n" \
"v_mul_f32_e32 v130, v54, v130\n" \
"v_mul_f32_e32 v131, v54, v131\n" \
"v_cvt_pk_bf16_f32 v136, v136, v137\n" \
"v_mul_f32_e32 v138, v54, v138\n" \
"v_mul_f32_e32 v139, v54, v139\n" \
"v_cvt_pk_bf16_f32 v144, v144, v145\n" \
"v_mul_f32_e32 v146, v54, v146\n" \
"v_mul_f32_e32 v147, v54, v147\n" \
"v_cvt_pk_bf16_f32 v152, v152, v153\n" \
"v_mul_f32_e32 v154, v54, v154\n" \
"v_mul_f32_e32 v155, v54, v155\n" \
"v_cvt_pk_bf16_f32 v160, v160, v161\n" \
"v_mul_f32_e32 v162, v55, v162\n" \
"v_mul_f32_e32 v163, v55, v163\n" \
"v_cvt_pk_bf16_f32 v168, v168, v169\n" \
"v_mul_f32_e32 v170, v55, v170\n" \
"v_mul_f32_e32 v171, v55, v171\n" \
"v_cvt_pk_bf16_f32 v176, v176, v177\n" \
"v_mul_f32_e32 v178, v55, v178\n" \
"v_mul_f32_e32 v179, v55, v179\n" \
"v_cvt_pk_bf16_f32 v184, v184, v185\n" \
"v_mul_f32_e32 v186, v55, v186\n" \
"v_mul_f32_e32 v187, v55, v187\n" \
"v_cvt_pk_bf16_f32 v129, v130, v131\n" \
"v_mul_f32_e32 v132, v54, v132\n" \
"v_mul_f32_e32 v133, v54, v133\n" \
"v_mul_f32_e32 v134, v54, v134\n" \
"v_mul_f32_e32 v135, v54, v135\n" \
"v_cvt_pk_bf16_f32 v137, v138, v139\n" \
"v_mul_f32_e32 v140, v54, v140\n" \
"s_waitcnt vmcnt(12) lgkmcnt(0)\n" \
"s_barrier\n" \
"v_mfma_f32_16x16x32_bf16 v[120:123], a[128:131], a[48:51], v[44:47]\n" \
"s_xor_b32 m0, s65, 0x12000\n" \
"ds_read_b128 a[188:191], v49 offset:7168\n" \
"v_mfma_f32_16x16x32_bf16 v[120:123], a[132:135], a[52:55], v[120:123]\n" \
"buffer_load_dwordx4 v50, s[8:11], s64 offen offset:0 sc1 lds\n" \
"v_mfma_f32_16x16x32_bf16 v[120:123], a[136:139], a[56:59], v[120:123]\n" \
"ds_read_b128 a[184:187], v49 offset:6144\n" \
"ds_read_b128 a[180:183], v49 offset:5120\n" \
"v_mfma_f32_16x16x32_bf16 v[120:123], a[140:143], a[60:63], v[120:123]\n" \
"buffer_load_dwordx4 v50, s[8:11], s64 offen offset:1024 sc1 lds\n" \
"v_mfma_f32_16x16x32_bf16 v[124:127], a[156:159], a[60:63], v[44:47]\n" \
"ds_read_b128 a[176:179], v49 offset:4096\n" \
"ds_read_b128 a[172:175], v49 offset:3072\n" \
"v_mfma_f32_16x16x32_bf16 v[124:127], a[152:155], a[56:59], v[124:127]\n" \
"ds_read_b128 a[168:171], v49 offset:2048\n" \
"ds_read_b128 a[164:167], v49 offset:1024\n" \
"v_mfma_f32_16x16x32_bf16 v[124:127], a[148:151], a[52:55], v[124:127]\n" \
"ds_read_b128 a[160:163], v49 offset:0\n" \
"ds_read_b128 a[0:3], v49 offset:8192\n" \
"v_mfma_f32_16x16x32_bf16 v[124:127], a[144:147], a[48:51], v[124:127]\n" \
"buffer_load_dwordx4 v50, s[8:11], s64 offen offset:2048 sc1 lds\n" \
"v_mfma_f32_16x16x32_bf16 v[32:35], a[220:223], a[64:67], 0\n" \
"ds_read_b128 a[4:7], v49 offset:9216\n" \
"ds_read_b128 a[8:11], v49 offset:10240\n" \
"v_mfma_f32_16x16x32_bf16 v[36:39], a[220:223], a[80:83], 0\n" \
"ds_read_b128 a[12:15], v49 offset:11264\n" \
"ds_read_b128 a[16:19], v49 offset:12288\n" \
"v_mfma_f32_16x16x32_bf16 v[40:43], a[220:223], a[96:99], 0\n" \
"ds_read_b128 a[20:23], v49 offset:13312\n" \
"ds_read_b128 a[24:27], v49 offset:14336\n" \
"v_mfma_f32_16x16x32_bf16 v[44:47], a[220:223], a[112:115], 0\n" \
"buffer_load_dwordx4 v50, s[8:11], s64 offen offset:3072 sc1 lds\n" \
"v_mfma_f32_16x16x32_bf16 v[32:35], a[224:227], a[68:71], v[32:35]\n" \
"s_xor_b32 m0, s65, 0xa000\n" \
"ds_read_b128 a[28:31], v49 offset:15360\n" \
"v_mfma_f32_16x16x32_bf16 v[36:39], a[224:227], a[84:87], v[36:39]\n" \
"v_exp_f32_e32 v120, v120\n" \
"v_mfma_f32_16x16x32_bf16 v[40:43], a[224:227], a[100:103], v[40:43]\n" \
"buffer_load_dwordx4 v51, s[8:11], s64 offen offset:0 sc1 lds\n" \
"v_mfma_f32_16x16x32_bf16 v[44:47], a[224:227], a[116:119], v[44:47]\n" \
"v_exp_f32_e32 v121, v121\n" \
"v_mfma_f32_16x16x32_bf16 v[32:35], a[228:231], a[72:75], v[32:35]\n" \
"v_exp_f32_e32 v122, v122\n" \
"v_mfma_f32_16x16x32_bf16 v[36:39], a[228:231], a[88:91], v[36:39]\n" \
"v_exp_f32_e32 v123, v123\n" \
"v_mfma_f32_16x16x32_bf16 v[40:43], a[228:231], a[104:107], v[40:43]\n" \
"buffer_load_dwordx4 v51, s[8:11], s64 offen offset:1024 sc1 lds\n" \
"v_mfma_f32_16x16x32_bf16 v[44:47], a[228:231], a[120:123], v[44:47]\n" \
"v_cvt_pk_bf16_f32 v120, v120, v121\n" \
"v_cvt_pk_bf16_f32 v121, v122, v123\n" \
"v_mfma_f32_16x16x32_bf16 v[32:35], a[232:235], a[76:79], v[32:35]\n" \
"v_exp_f32_e32 v124, v124\n" \
"v_mfma_f32_16x16x32_bf16 v[36:39], a[232:235], a[92:95], v[36:39]\n" \
"v_exp_f32_e32 v125, v125\n" \
"v_mfma_f32_16x16x32_bf16 v[40:43], a[232:235], a[108:111], v[40:43]\n" \
"buffer_load_dwordx4 v51, s[8:11], s64 offen offset:2048 sc1 lds\n" \
"v_mfma_f32_16x16x32_bf16 v[44:47], a[232:235], a[124:127], v[44:47]\n" \
"v_exp_f32_e32 v126, v126\n" \
"v_smfmac_f32_16x16x64_bf16 a[196:199], a[200:203], v[84:91], v21\n" \
"v_cvt_pk_bf16_f32 v122, v124, v125\n" \
"s_waitcnt lgkmcnt(1)\n" \
"v_mfma_f32_16x16x32_bf16 v[220:223], a[188:191], v[84:87], v[220:223]\n" \
"buffer_load_dwordx4 v51, s[8:11], s64 offen offset:3072 sc1 lds\n" \
"v_mfma_f32_16x16x32_bf16 v[252:255], a[188:191], v[88:91], v[252:255]\n" \
"s_add_u32 m0, s93, 0x1000\n" \
"v_and_b32_e32 v120, v12, v120\n" \
"v_mfma_f32_16x16x32_bf16 v[216:219], a[184:187], v[84:87], v[216:219]\n" \
"v_exp_f32_e32 v127, v127\n" \
"v_mfma_f32_16x16x32_bf16 v[248:251], a[184:187], v[88:91], v[248:251]\n" \
"v_cvt_pk_bf16_f32 v123, v126, v127\n" \
"v_and_b32_e32 v121, v13, v121\n" \
"v_mfma_f32_16x16x32_bf16 v[212:215], a[180:183], v[84:87], v[212:215]\n" \
"buffer_load_dwordx4 v4, s[80:83], s92 offen offset:0 lds\n" \
"v_mfma_f32_16x16x32_bf16 v[244:247], a[180:183], v[88:91], v[244:247]\n" \
"v_and_b32_e32 v122, v14, v122\n" \
"v_and_b32_e32 v123, v15, v123\n" \
"v_mfma_f32_16x16x32_bf16 v[208:211], a[176:179], v[84:87], v[208:211]\n" \
"v_accvgpr_read_b32 v56, a196\n" \
"v_accvgpr_read_b32 v24, a197\n" \
"v_mfma_f32_16x16x32_bf16 v[240:243], a[176:179], v[88:91], v[240:243]\n" \
"ds_read_b128 a[128:131], v48 offset:0\n" \
"ds_read_b128 a[144:147], v48 offset:4096\n" \
"v_mfma_f32_16x16x32_bf16 v[204:207], a[172:175], v[84:87], v[204:207]\n" \
"buffer_load_dwordx4 v5, s[80:83], s92 offen offset:1024 lds\n" \
"v_mfma_f32_16x16x32_bf16 v[236:239], a[172:175], v[88:91], v[236:239]\n" \
"ds_read_b128 a[132:135], v48 offset:1024\n" \
"ds_read_b128 a[148:151], v48 offset:5120\n" \
"v_mfma_f32_16x16x32_bf16 v[200:203], a[168:171], v[84:87], v[200:203]\n" \
"ds_read_b128 a[136:139], v48 offset:2048\n" \
"ds_read_b128 a[152:155], v48 offset:6144\n" \
"v_mfma_f32_16x16x32_bf16 v[232:235], a[168:171], v[88:91], v[232:235]\n" \
"buffer_load_dwordx4 v6, s[80:83], s92 offen offset:2048 lds\n" \
"v_mfma_f32_16x16x32_bf16 v[196:199], a[164:167], v[84:87], v[196:199]\n" \
"ds_read_b128 a[140:143], v48 offset:3072\n" \
"ds_read_b128 a[156:159], v48 offset:7168\n" \
"v_mfma_f32_16x16x32_bf16 v[228:231], a[164:167], v[88:91], v[228:231]\n" \
"v_mul_f32_e32 v141, v54, v141\n" \
"v_mul_f32_e32 v142, v54, v142\n" \
"v_mfma_f32_16x16x32_bf16 v[192:195], a[160:163], v[84:87], v[192:195]\n" \
"v_mul_f32_e32 v143, v54, v143\n" \
"v_cvt_pk_bf16_f32 v145, v146, v147\n" \
"v_mfma_f32_16x16x32_bf16 v[224:227], a[160:163], v[88:91], v[224:227]\n" \
"buffer_load_dwordx4 v7, s[80:83], s92 offen offset:3072 lds\n" \
"v_mfma_f32_16x16x32_bf16 a[196:199], a[212:215], v[120:123], a[196:199]\n" \
"v_mul_f32_e32 v148, v54, v148\n" \
"v_mul_f32_e32 v149, v54, v149\n" \
"v_mfma_f32_16x16x32_bf16 v[224:227], a[0:3], v[120:123], v[224:227]\n" \
"v_mul_f32_e32 v150, v54, v150\n" \
"v_mul_f32_e32 v151, v54, v151\n" \
"v_mfma_f32_16x16x32_bf16 v[228:231], a[4:7], v[120:123], v[228:231]\n" \
"v_cvt_pk_bf16_f32 v153, v154, v155\n" \
"v_mul_f32_e32 v156, v54, v156\n" \
"v_mfma_f32_16x16x32_bf16 v[232:235], a[8:11], v[120:123], v[232:235]\n" \
"v_mul_f32_e32 v157, v54, v157\n" \
"v_mul_f32_e32 v158, v54, v158\n" \
"v_mfma_f32_16x16x32_bf16 v[236:239], a[12:15], v[120:123], v[236:239]\n" \
"v_mul_f32_e32 v159, v54, v159\n" \
"v_cvt_pk_bf16_f32 v161, v162, v163\n" \
"v_mfma_f32_16x16x32_bf16 v[240:243], a[16:19], v[120:123], v[240:243]\n" \
"v_mul_f32_e32 v164, v55, v164\n" \
"v_mul_f32_e32 v165, v55, v165\n" \
"v_mfma_f32_16x16x32_bf16 v[244:247], a[20:23], v[120:123], v[244:247]\n" \
"v_mul_f32_e32 v166, v55, v166\n" \
"v_mul_f32_e32 v167, v55, v167\n" \
"v_mfma_f32_16x16x32_bf16 v[248:251], a[24:27], v[120:123], v[248:251]\n" \
"v_cvt_pk_bf16_f32 v169, v170, v171\n" \
"s_waitcnt lgkmcnt(4)\n" \
"v_mfma_f32_16x16x32_bf16 v[252:255], a[28:31], v[120:123], v[252:255]\n" \
"v_mul_f32_e32 v172, v55, v172\n" \
"v_mul_f32_e32 v173, v55, v173\n" \
"v_mul_f32_e32 v174, v55, v174\n" \
"v_mul_f32_e32 v175, v55, v175\n" \
"v_cvt_pk_bf16_f32 v177, v178, v179\n" \
"v_mul_f32_e32 v180, v55, v180\n" \
"v_mul_f32_e32 v181, v55, v181\n" \
"v_mul_f32_e32 v182, v55, v182\n" \
"v_mul_f32_e32 v183, v55, v183\n" \
"v_cvt_pk_bf16_f32 v185, v186, v187\n" \
"v_mul_f32_e32 v188, v55, v188\n" \
"v_mul_f32_e32 v189, v55, v189\n" \
"v_mul_f32_e32 v190, v55, v190\n" \
"v_mul_f32_e32 v191, v55, v191\n" \
"v_cvt_pk_bf16_f32 v130, v132, v133\n" \
"v_cvt_pk_bf16_f32 v131, v134, v135\n" \
"buffer_store_dwordx4 v[128:131], v20, s[56:59], s68 offen offset:0 sc1\n" \
"v_cvt_pk_bf16_f32 v138, v140, v141\n" \
"v_cvt_pk_bf16_f32 v139, v142, v143\n" \
"buffer_store_dwordx4 v[136:139], v20, s[56:59], s68 offen offset:1024 sc1\n" \
"v_cvt_pk_bf16_f32 v146, v148, v149\n" \
"v_cvt_pk_bf16_f32 v147, v150, v151\n" \
"buffer_store_dwordx4 v[144:147], v20, s[56:59], s68 offen offset:2048 sc1\n" \
"v_cvt_pk_bf16_f32 v154, v156, v157\n" \
"v_cvt_pk_bf16_f32 v155, v158, v159\n" \
"buffer_store_dwordx4 v[152:155], v20, s[56:59], s68 offen offset:3072 sc1\n" \
"v_cvt_pk_bf16_f32 v162, v164, v165\n" \
"v_cvt_pk_bf16_f32 v163, v166, v167\n" \
"buffer_store_dwordx4 v[160:163], v20, s[56:59], s69 offen offset:0 sc1\n" \
"v_cvt_pk_bf16_f32 v170, v172, v173\n" \
"v_cvt_pk_bf16_f32 v171, v174, v175\n" \
"buffer_store_dwordx4 v[168:171], v20, s[56:59], s69 offen offset:1024 sc1\n" \
"v_cvt_pk_bf16_f32 v178, v180, v181\n" \
"v_cvt_pk_bf16_f32 v179, v182, v183\n" \
"buffer_store_dwordx4 v[176:179], v20, s[56:59], s69 offen offset:2048 sc1\n" \
"v_cvt_pk_bf16_f32 v186, v188, v189\n" \
"v_cvt_pk_bf16_f32 v187, v190, v191\n" \
"buffer_store_dwordx4 v[184:187], v20, s[56:59], s69 offen offset:3072 sc1\n" \
"s_add_u32 m0, s93, 0x2000\n" \
"v_mfma_f32_16x16x32_bf16 a[208:211], a[216:219], v[128:131], a[208:211]\n" \
"buffer_load_dwordx4 v8, s[80:83], s92 offen offset:0 lds\n" \
"v_mfma_f32_16x16x32_bf16 a[208:211], a[216:219], v[136:139], a[208:211]\n" \
"v_mfma_f32_32x32x16_bf16 v[128:143], a[216:219], a[216:219], 0\n" \
"v_mfma_f32_16x16x32_bf16 a[208:211], a[216:219], v[144:147], a[208:211]\n" \
"v_mfma_f32_16x16x32_bf16 a[208:211], a[216:219], v[152:155], a[208:211]\n" \
"v_mfma_f32_32x32x16_bf16 v[144:159], a[216:219], a[216:219], 0\n" \
"v_add_f32_e32 v56, v56, v24\n" \
"v_mfma_f32_16x16x32_bf16 a[208:211], a[216:219], v[160:163], a[208:211]\n" \
"v_max_f32_e32 v62, v62, v56\n" \
"v_accvgpr_read_b32 v57, a198\n" \
"s_waitcnt vmcnt(21) lgkmcnt(0)\n" \
"s_barrier\n" \
"v_mfma_f32_16x16x32_bf16 v[64:67], a[128:131], a[64:67], v[32:35]\n" \
"s_xor_b32 m0, s65, 0x18000\n" \
"v_accvgpr_read_b32 v25, a199\n" \
"v_mfma_f32_16x16x32_bf16 a[196:199], a[216:219], a[216:219], 0\n" \
"buffer_load_dwordx4 v52, s[8:11], s64 offen offset:0 sc1 lds\n" \
"v_mfma_f32_16x16x32_bf16 v[64:67], a[132:135], a[68:71], v[64:67]\n" \
"v_rcp_f32_e32 v56, v56\n" \
"v_mfma_f32_16x16x32_bf16 v[64:67], a[136:139], a[72:75], v[64:67]\n" \
"v_mfma_f32_16x16x32_bf16 a[208:211], a[216:219], v[168:171], a[208:211]\n" \
"v_mfma_f32_32x32x16_bf16 v[160:175], a[216:219], a[216:219], 0\n" \
"v_mul_f32_e32 v56, v56, v60\n" \
"v_mfma_f32_16x16x32_bf16 a[208:211], a[216:219], v[176:179], a[208:211]\n" \
"v_mul_f32_e32 v192, v56, v192\n" \
"v_mul_f32_e32 v193, v56, v193\n" \
"v_mfma_f32_16x16x32_bf16 v[64:67], a[140:143], a[76:79], v[64:67]\n" \
"buffer_load_dwordx4 v52, s[8:11], s64 offen offset:1024 sc1 lds\n" \
"v_mfma_f32_16x16x32_bf16 v[68:71], a[156:159], a[76:79], v[32:35]\n" \
"v_cvt_pk_bf16_f32 v192, v192, v193\n" \
"v_mul_f32_e32 v194, v56, v194\n" \
"v_mfma_f32_16x16x32_bf16 v[68:71], a[152:155], a[72:75], v[68:71]\n" \
"v_mul_f32_e32 v195, v56, v195\n" \
"v_cvt_pk_bf16_f32 v193, v194, v195\n" \
"v_mfma_f32_16x16x32_bf16 v[68:71], a[148:151], a[68:71], v[68:71]\n" \
"v_mfma_f32_16x16x32_bf16 a[208:211], a[216:219], v[184:187], a[208:211]\n" \
"v_mfma_f32_32x32x16_bf16 v[176:191], a[216:219], a[216:219], 0\n" \
"v_mul_f32_e32 v196, v56, v196\n" \
"v_mul_f32_e32 v197, v56, v197\n" \
"v_mul_f32_e32 v198, v56, v198\n" \
"v_mul_f32_e32 v199, v56, v199\n" \
"v_mul_f32_e32 v200, v56, v200\n" \
"v_mul_f32_e32 v201, v56, v201\n" \
"v_mfma_f32_16x16x32_bf16 v[68:71], a[144:147], a[64:67], v[68:71]\n" \
"buffer_load_dwordx4 v52, s[8:11], s64 offen offset:2048 sc1 lds\n" \
"v_mfma_f32_16x16x32_bf16 v[76:79], a[144:147], a[80:83], v[36:39]\n" \
"v_cvt_pk_bf16_f32 v194, v196, v197\n" \
"v_cvt_pk_bf16_f32 v195, v198, v199\n" \
"buffer_store_dwordx4 v[192:195], v20, s[56:59], s70 offen offset:0 sc1\n" \
"v_mfma_f32_16x16x32_bf16 v[76:79], a[148:151], a[84:87], v[76:79]\n" \
"v_cvt_pk_bf16_f32 v200, v200, v201\n" \
"v_mul_f32_e32 v202, v56, v202\n" \
"v_mfma_f32_16x16x32_bf16 v[76:79], a[152:155], a[88:91], v[76:79]\n" \
"v_mul_f32_e32 v203, v56, v203\n" \
"v_add_f32_e32 v57, v57, v25\n" \
"v_mfma_f32_16x16x32_bf16 v[76:79], a[156:159], a[92:95], v[76:79]\n" \
"buffer_load_dwordx4 v52, s[8:11], s64 offen offset:3072 sc1 lds\n" \
"v_mfma_f32_16x16x32_bf16 v[72:75], a[140:143], a[92:95], v[36:39]\n" \
"s_mov_b32 m0, s65\n" \
"v_cvt_pk_bf16_f32 v201, v202, v203\n" \
"v_mfma_f32_16x16x32_bf16 v[72:75], a[136:139], a[88:91], v[72:75]\n" \
"v_mfma_f32_16x16x32_bf16 a[208:211], a[216:219], v[192:195], a[208:211]\n" \
"v_mul_f32_e32 v204, v56, v204\n" \
"v_mul_f32_e32 v205, v56, v205\n" \
"v_mfma_f32_16x16x32_bf16 v[72:75], a[132:135], a[84:87], v[72:75]\n" \
"v_mul_f32_e32 v206, v56, v206\n" \
"v_mul_f32_e32 v207, v56, v207\n" \
"v_mfma_f32_16x16x32_bf16 v[72:75], a[128:131], a[80:83], v[72:75]\n" \
"buffer_load_dwordx4 v53, s[8:11], s64 offen offset:0 sc1 lds\n" \
"v_mfma_f32_16x16x32_bf16 v[80:83], a[128:131], a[96:99], v[40:43]\n" \
"v_mul_f32_e32 v208, v56, v208\n" \
"v_mul_f32_e32 v209, v56, v209\n" \
"v_mfma_f32_16x16x32_bf16 v[80:83], a[132:135], a[100:103], v[80:83]\n" \
"v_max_f32_e32 v62, v62, v57\n" \
"v_cvt_pk_bf16_f32 v202, v204, v205\n" \
"v_mfma_f32_16x16x32_bf16 v[80:83], a[136:139], a[104:107], v[80:83]\n" \
"buffer_load_dwordx4 v53, s[8:11], s64 offen offset:1024 sc1 lds\n" \
"v_mfma_f32_16x16x32_bf16 v[80:83], a[140:143], a[108:111], v[80:83]\n" \
"v_cvt_pk_bf16_f32 v203, v206, v207\n" \
"v_cvt_pk_bf16_f32 v208, v208, v209\n" \
"buffer_store_dwordx4 v[200:203], v20, s[56:59], s70 offen offset:1024 sc1\n" \
"v_mfma_f32_16x16x32_bf16 v[84:87], a[156:159], a[108:111], v[40:43]\n" \
"v_mul_f32_e32 v210, v56, v210\n" \
"v_mul_f32_e32 v211, v56, v211\n" \
"v_mfma_f32_16x16x32_bf16 v[84:87], a[152:155], a[104:107], v[84:87]\n" \
"v_rcp_f32_e32 v57, v57\n" \
"v_mfma_f32_16x16x32_bf16 v[84:87], a[148:151], a[100:103], v[84:87]\n" \
"buffer_load_dwordx4 v53, s[8:11], s64 offen offset:2048 sc1 lds\n" \
"v_mfma_f32_16x16x32_bf16 v[84:87], a[144:147], a[96:99], v[84:87]\n" \
"v_mfma_f32_16x16x32_bf16 a[208:211], a[216:219], v[200:203], a[208:211]\n" \
"v_mfma_f32_32x32x16_bf16 v[192:207], a[216:219], a[216:219], 0\n" \
"v_cvt_pk_bf16_f32 v209, v210, v211\n" \
"v_mul_f32_e32 v212, v56, v212\n" \
".fill (L_ab + 16384 - .) / 4, 4, 0xBF800000\n" \
"L_ap4:\n" \
"s_cbranch_execz L_ap0\n" \
"v_mul_f32_e32 v213, v56, v213\n" \
"v_mul_f32_e32 v214, v56, v214\n" \
"v_mul_f32_e32 v215, v56, v215\n" \
"v_mul_f32_e32 v216, v56, v216\n" \
"v_mfma_f32_16x16x32_bf16 v[92:95], a[144:147], a[112:115], v[44:47]\n" \
"ds_read_b128 a[144:147], v48 offset:12288\n" \
"v_mul_f32_e32 v217, v56, v217\n" \
"v_mfma_f32_16x16x32_bf16 v[92:95], a[148:151], a[116:119], v[92:95]\n" \
"ds_read_b128 a[148:151], v48 offset:13312\n" \
"v_cvt_pk_bf16_f32 v210, v212, v213\n" \
"v_mfma_f32_16x16x32_bf16 v[92:95], a[152:155], a[120:123], v[92:95]\n" \
"ds_read_b128 a[152:155], v48 offset:14336\n" \
"v_cvt_pk_bf16_f32 v211, v214, v215\n" \
"buffer_store_dwordx4 v[208:211], v20, s[56:59], s70 offen offset:2048 sc1\n" \
"v_mfma_f32_16x16x32_bf16 v[92:95], a[156:159], a[124:127], v[92:95]\n" \
"ds_read_b128 a[156:159], v48 offset:15360\n" \
"v_cvt_pk_bf16_f32 v216, v216, v217\n" \
"v_mfma_f32_16x16x32_bf16 v[88:91], a[140:143], a[124:127], v[44:47]\n" \
"ds_read_b128 a[140:143], v48 offset:11264\n" \
"v_mul_f32_e32 v218, v56, v218\n" \
"v_mfma_f32_16x16x32_bf16 v[88:91], a[136:139], a[120:123], v[88:91]\n" \
"ds_read_b128 a[136:139], v48 offset:10240\n" \
"v_mul_f32_e32 v219, v56, v219\n" \
"v_mfma_f32_16x16x32_bf16 v[88:91], a[132:135], a[116:119], v[88:91]\n" \
"ds_read_b128 a[132:135], v48 offset:9216\n" \
"v_mul_f32_e32 v57, v57, v61\n" \
"v_mfma_f32_16x16x32_bf16 v[88:91], a[128:131], a[112:115], v[88:91]\n" \
"ds_read_b128 a[128:131], v48 offset:8192\n" \
"v_mfma_f32_16x16x32_bf16 a[208:211], a[216:219], v[208:211], a[208:211]\n" \
"v_cvt_pk_bf16_f32 v217, v218, v219\n" \
"v_mul_f32_e32 v220, v56, v220\n" \
"v_mul_f32_e32 v221, v56, v221\n" \
"v_mul_f32_e32 v222, v56, v222\n" \
"v_mul_f32_e32 v223, v56, v223\n" \
"v_mul_f32_e32 v224, v57, v224\n" \
"v_mul_f32_e32 v225, v57, v225\n" \
"v_cvt_pk_bf16_f32 v218, v220, v221\n" \
"v_cvt_pk_bf16_f32 v219, v222, v223\n" \
"buffer_store_dwordx4 v[216:219], v20, s[56:59], s70 offen offset:3072 sc1\n" \
"v_cvt_pk_bf16_f32 v224, v224, v225\n" \
"v_mul_f32_e32 v226, v57, v226\n" \
"v_mul_f32_e32 v227, v57, v227\n" \
"v_mfma_f32_16x16x32_bf16 a[208:211], a[216:219], v[216:219], a[208:211]\n" \
"v_mfma_f32_32x32x16_bf16 v[208:223], a[216:219], a[216:219], 0\n" \
"v_cvt_pk_bf16_f32 v225, v226, v227\n" \
"v_mul_f32_e32 v228, v57, v228\n" \
"v_mul_f32_e32 v229, v57, v229\n" \
"v_mul_f32_e32 v230, v57, v230\n" \
"v_mul_f32_e32 v231, v57, v231\n" \
"v_mul_f32_e32 v232, v57, v232\n" \
"v_mul_f32_e32 v233, v57, v233\n" \
"v_cvt_pk_bf16_f32 v226, v228, v229\n" \
"v_cvt_pk_bf16_f32 v227, v230, v231\n" \
"buffer_store_dwordx4 v[224:227], v20, s[56:59], s71 offen offset:0 sc1\n" \
"v_cvt_pk_bf16_f32 v232, v232, v233\n" \
"v_mul_f32_e32 v234, v57, v234\n" \
"v_mul_f32_e32 v235, v57, v235\n" \
"v_mfma_f32_16x16x32_bf16 a[208:211], a[216:219], v[224:227], a[208:211]\n" \
"v_cvt_pk_bf16_f32 v233, v234, v235\n" \
"v_mul_f32_e32 v236, v57, v236\n" \
"v_mul_f32_e32 v237, v57, v237\n" \
"v_mul_f32_e32 v238, v57, v238\n" \
"v_mul_f32_e32 v239, v57, v239\n" \
"v_mul_f32_e32 v240, v57, v240\n" \
"v_mul_f32_e32 v241, v57, v241\n" \
"v_cvt_pk_bf16_f32 v234, v236, v237\n" \
"v_cvt_pk_bf16_f32 v235, v238, v239\n" \
"buffer_store_dwordx4 v[232:235], v20, s[56:59], s71 offen offset:1024 sc1\n" \
"v_cvt_pk_bf16_f32 v240, v240, v241\n" \
"v_mul_f32_e32 v242, v57, v242\n" \
"v_mul_f32_e32 v243, v57, v243\n" \
"v_mfma_f32_16x16x32_bf16 a[208:211], a[216:219], v[232:235], a[208:211]\n" \
"v_mfma_f32_32x32x16_bf16 v[224:239], a[216:219], a[216:219], 0\n" \
"v_cvt_pk_bf16_f32 v241, v242, v243\n" \
"v_mul_f32_e32 v244, v57, v244\n" \
"v_mul_f32_e32 v245, v57, v245\n" \
"v_mul_f32_e32 v246, v57, v246\n" \
"v_mul_f32_e32 v247, v57, v247\n" \
"v_mul_f32_e32 v248, v57, v248\n" \
"v_mul_f32_e32 v249, v57, v249\n" \
"v_cvt_pk_bf16_f32 v242, v244, v245\n" \
"v_cvt_pk_bf16_f32 v243, v246, v247\n" \
"buffer_store_dwordx4 v[240:243], v20, s[56:59], s71 offen offset:2048 sc1\n" \
"v_cvt_pk_bf16_f32 v248, v248, v249\n" \
"v_mul_f32_e32 v250, v57, v250\n" \
"v_mul_f32_e32 v251, v57, v251\n" \
"v_mfma_f32_16x16x32_bf16 a[208:211], a[216:219], v[240:243], a[208:211]\n" \
"v_cvt_pk_bf16_f32 v249, v250, v251\n" \
"v_mul_f32_e32 v252, v57, v252\n" \
"v_mul_f32_e32 v253, v57, v253\n" \
"v_mul_f32_e32 v254, v57, v254\n" \
"v_mul_f32_e32 v255, v57, v255\n" \
"v_cvt_pk_bf16_f32 v250, v252, v253\n" \
"v_cvt_pk_bf16_f32 v251, v254, v255\n" \
"buffer_store_dwordx4 v[248:251], v20, s[56:59], s71 offen offset:3072 sc1\n" \
"buffer_load_dwordx4 v53, s[8:11], s64 offen offset:3072 sc1 lds\n" \
"s_waitcnt vmcnt(16) lgkmcnt(0)\n" \
"s_barrier\n" \
"v_mfma_f32_16x16x32_bf16 a[208:211], a[216:219], v[248:251], a[208:211]\n" \
"v_mfma_f32_32x32x16_bf16 v[240:255], a[216:219], a[216:219], 0\n" \
"v_exp_f32_e32 v70, v70\n" \
"v_exp_f32_e32 v71, v71\n" \
"v_exp_f32_e32 v72, v72\n" \
"v_exp_f32_e32 v73, v73\n" \
"s_add_u32 s64, s64, 0x10000\n" \
"v_cmp_lt_f32_e64 s[72:73], s21, v62\n" \
"v_readlane_b32 s95, v11, 3\n" \
"v_accvgpr_read_b32 v63, a208\n" \
"v_cmp_u_f32_e32 vcc, v63, v63\n" \
"v_exp_f32_e32 v68, v68\n" \
"v_exp_f32_e32 v69, v69\n" \
"v_exp_f32_e32 v74, v74\n" \
"v_exp_f32_e32 v75, v75\n" \
"s_or_b64 vcc, s[72:73], vcc\n" \
"s_cselect_b32 s95, 0x40000000, s95\n" \
"s_cselect_b32 s94, 0x7f800000, 0\n" \
"v_cvt_pk_bf16_f32 v71, v70, v71\n" \
"v_exp_f32_e32 v66, v66\n" \
"v_exp_f32_e32 v67, v67\n" \
"v_exp_f32_e32 v64, v64\n" \
"v_exp_f32_e32 v65, v65\n" \
"v_cvt_pk_bf16_f32 v72, v72, v73\n" \
"v_exp_f32_e32 v76, v76\n" \
"v_exp_f32_e32 v77, v77\n" \
"v_exp_f32_e32 v78, v78\n" \
"v_exp_f32_e32 v79, v79\n" \
"v_cvt_pk_bf16_f32 v70, v68, v69\n" \
"v_cvt_pk_bf16_f32 v73, v74, v75\n" \
"v_add_u32_e32 v49, 0x10000, v48\n" \
"s_xor_b32 s65, s65, 0x8000\n" \
"v_cvt_pk_bf16_f32 v69, v66, v67\n" \
"v_cvt_pk_bf16_f32 v68, v64, v65\n" \
"v_cvt_pk_bf16_f32 v74, v76, v77\n" \
"v_cvt_pk_bf16_f32 v75, v78, v79\n" \
"v_xor_b32_e32 v48, 0x8000, v48\n" \
"s_sub_u32 s66, s19, 1\n" \
"s_cbranch_scc0 L_2_%=\n" \
"L_f1_%=:\n" \
"v_bfe_u32 v60, v20, 2, 7\n" \
"v_readlane_b32 s96, v11, 1\n" \
"ds_read_b128 a[160:163], v49 offset:0\n" \
"ds_read_b128 a[164:167], v49 offset:1024\n" \
"ds_read_b128 a[168:171], v49 offset:2048\n" \
"v_mfma_f32_16x16x32_bf16 v[104:107], a[128:131], a[80:83], v[36:39]\n" \
"v_cvt_pk_bf16_f32 v192, v192, v193\n" \
"v_cvt_pk_bf16_f32 v224, v224, v225\n" \
"buffer_load_dword v57, v60, s[56:59], s96 offen sc1\n" \
"v_readlane_b32 s93, v11, 20\n" \
"v_cvt_pk_bf16_f32 v193, v194, v195\n" \
"v_cvt_pk_bf16_f32 v225, v226, v227\n" \
"ds_read_b128 a[172:175], v49 offset:3072\n" \
"v_readlane_b32 s84, v11, 15\n" \
"v_readlane_b32 s85, v11, 16\n" \
"v_readlane_b32 s86, v11, 17\n" \
"s_mov_b32 s87, 0x20000\n" \
"v_readlane_b32 s92, v11, 18\n" \
"s_add_u32 m0, s93, 0x0\n" \
"v_cvt_pk_bf16_f32 v194, v196, v197\n" \
"v_cvt_pk_bf16_f32 v195, v198, v199\n" \
"v_cvt_pk_bf16_f32 v226, v228, v229\n" \
"v_cvt_pk_bf16_f32 v227, v230, v231\n" \
"v_mfma_f32_16x16x32_bf16 v[104:107], a[132:135], a[84:87], v[104:107]\n" \
"v_lshl_add_u32 v50, s15, 2, v20\n" \
"v_cvt_pk_bf16_f32 v196, v200, v201\n" \
"v_cvt_pk_bf16_f32 v197, v202, v203\n" \
"v_cvt_pk_bf16_f32 v198, v204, v205\n" \
"v_cvt_pk_bf16_f32 v199, v206, v207\n" \
"buffer_load_dwordx4 v0, s[84:87], s92 offen offset:0 lds\n" \
"v_cvt_pk_bf16_f32 v228, v232, v233\n" \
"v_cvt_pk_bf16_f32 v229, v234, v235\n" \
"v_cvt_pk_bf16_f32 v230, v236, v237\n" \
"v_cvt_pk_bf16_f32 v231, v238, v239\n" \
"v_add_u32_e32 v50, 0x1a000, v50\n" \
"v_mov_b32_e32 v61, s62\n" \
"v_cvt_pk_bf16_f32 v200, v208, v209\n" \
"v_cvt_pk_bf16_f32 v201, v210, v211\n" \
"v_cvt_pk_bf16_f32 v202, v212, v213\n" \
"v_cvt_pk_bf16_f32 v203, v214, v215\n" \
"v_cvt_pk_bf16_f32 v204, v216, v217\n" \
"v_cvt_pk_bf16_f32 v205, v218, v219\n" \
"v_cvt_pk_bf16_f32 v206, v220, v221\n" \
"v_cvt_pk_bf16_f32 v207, v222, v223\n" \
"v_cvt_pk_bf16_f32 v232, v240, v241\n" \
"v_cvt_pk_bf16_f32 v233, v242, v243\n" \
"v_cvt_pk_bf16_f32 v234, v244, v245\n" \
"v_cvt_pk_bf16_f32 v235, v246, v247\n" \
"v_cvt_pk_bf16_f32 v236, v248, v249\n" \
"v_cvt_pk_bf16_f32 v237, v250, v251\n" \
"v_cvt_pk_bf16_f32 v238, v252, v253\n" \
"buffer_load_dwordx4 v1, s[84:87], s92 offen offset:1024 lds\n" \
"v_cvt_pk_bf16_f32 v239, v254, v255\n" \
"ds_write_b128 v50, v[192:195] offset:0\n" \
"v_mfma_f32_16x16x32_bf16 v[104:107], a[136:139], a[88:91], v[104:107]\n" \
"ds_read_b128 a[176:179], v49 offset:4096\n" \
"v_add_u32_e32 v56, 0x49000, v20\n" \
"v_mfma_f32_16x16x32_bf16 v[104:107], a[140:143], a[92:95], v[104:107]\n" \
"ds_write_b128 v50, v[196:199] offset:1024\n" \
"v_mfma_f32_16x16x32_bf16 v[108:111], a[156:159], a[92:95], v[36:39]\n" \
"v_readlane_b32 s97, v11, 35\n" \
"v_readlane_b32 s98, v11, 36\n" \
"ds_read_b128 a[180:183], v49 offset:5120\n" \
"v_mfma_f32_16x16x32_bf16 v[108:111], a[152:155], a[88:91], v[108:111]\n" \
"ds_write_b128 v50, v[200:203] offset:2048\n" \
"v_mfma_f32_16x16x32_bf16 v[108:111], a[148:151], a[84:87], v[108:111]\n" \
"buffer_load_dwordx4 v2, s[84:87], s92 offen offset:2048 lds\n" \
"ds_read_b128 a[184:187], v49 offset:6144\n" \
"v_mfma_f32_16x16x32_bf16 v[108:111], a[144:147], a[80:83], v[108:111]\n" \
"ds_write_b128 v50, v[204:207] offset:3072\n" \
"v_and_b32_e32 v68, v12, v68\n" \
"v_and_b32_e32 v69, v13, v69\n" \
"v_and_b32_e32 v70, v14, v70\n" \
"v_and_b32_e32 v71, v15, v71\n" \
"ds_read_b128 a[188:191], v49 offset:7168\n" \
"ds_read_b128 a[0:3], v20 offset:8192\n" \
"ds_write_b128 v50, v[224:227] offset:4096\n" \
"s_waitcnt lgkmcnt(10)\n" \
"v_mfma_f32_16x16x32_bf16 v[128:131], a[160:163], v[68:71], v[128:131]\n" \
"ds_read_b128 a[4:7], v20 offset:9216\n" \
"ds_read_b128 a[8:11], v20 offset:10240\n" \
"v_mfma_f32_16x16x32_bf16 v[160:163], a[160:163], v[72:75], v[160:163]\n" \
"ds_write_b128 v50, v[228:231] offset:5120\n" \
"buffer_load_dwordx4 v3, s[84:87], s92 offen offset:3072 lds\n" \
"s_add_u32 m0, s93, 0x1000\n" \
"v_mfma_f32_16x16x32_bf16 v[132:135], a[164:167], v[68:71], v[132:135]\n" \
"ds_read_b128 a[12:15], v20 offset:11264\n" \
"ds_read_b128 a[28:31], v20 offset:15360\n" \
"ds_write_b128 v50, v[232:235] offset:6144\n" \
"v_mfma_f32_16x16x32_bf16 v[164:167], a[164:167], v[72:75], v[164:167]\n" \
"ds_read_b128 a[24:27], v20 offset:14336\n" \
"ds_read_b128 a[20:23], v20 offset:13312\n" \
"v_mfma_f32_16x16x32_bf16 v[136:139], a[168:171], v[68:71], v[136:139]\n" \
"ds_write_b128 v50, v[236:239] offset:7168\n" \
"v_mfma_f32_16x16x32_bf16 v[168:171], a[168:171], v[72:75], v[168:171]\n" \
"ds_read_b128 a[16:19], v20 offset:12288\n" \
"v_xor_b32_e32 v48, 0x4000, v49\n" \
"v_mfma_f32_16x16x32_bf16 v[140:143], a[172:175], v[68:71], v[140:143]\n" \
"buffer_load_dwordx4 v4, s[84:87], s92 offen offset:0 lds\n" \
"v_mfma_f32_16x16x32_bf16 v[172:175], a[172:175], v[72:75], v[172:175]\n" \
"ds_read_b32 v58, v18 offset:256\n" \
"s_waitcnt lgkmcnt(15)\n" \
"v_mfma_f32_16x16x32_bf16 v[144:147], a[176:179], v[68:71], v[144:147]\n" \
"ds_read_b32 v59, v18 offset:384\n" \
"ds_read_b128 a[32:35], v48 offset:8192\n" \
"v_mfma_f32_16x16x32_bf16 v[176:179], a[176:179], v[72:75], v[176:179]\n" \
"ds_read_b128 a[36:39], v48 offset:9216\n" \
"ds_read_b128 a[40:43], v48 offset:10240\n" \
"v_mfma_f32_16x16x32_bf16 v[148:151], a[180:183], v[68:71], v[148:151]\n" \
"ds_read_b128 a[44:47], v48 offset:11264\n" \
"ds_read_b128 a[48:51], v48 offset:12288\n" \
"v_mfma_f32_16x16x32_bf16 v[180:183], a[180:183], v[72:75], v[180:183]\n" \
"ds_read_b128 a[52:55], v48 offset:13312\n" \
"ds_read_b128 a[56:59], v48 offset:14336\n" \
"v_mfma_f32_16x16x32_bf16 v[152:155], a[184:187], v[68:71], v[152:155]\n" \
"buffer_load_dwordx4 v5, s[84:87], s92 offen offset:1024 lds\n" \
"v_mfma_f32_16x16x32_bf16 v[184:187], a[184:187], v[72:75], v[184:187]\n" \
"ds_read_b128 a[60:63], v48 offset:15360\n" \
"s_waitcnt lgkmcnt(15)\n" \
"v_mfma_f32_16x16x32_bf16 v[156:159], a[188:191], v[68:71], v[156:159]\n" \
"ds_read_b128 a[128:131], v49 offset:8192\n" \
"ds_read_b128 a[132:135], v49 offset:9216\n" \
"v_mfma_f32_16x16x32_bf16 v[188:191], a[188:191], v[72:75], v[188:191]\n" \
"ds_read_b128 a[136:139], v49 offset:10240\n" \
"ds_read_b128 a[140:143], v49 offset:11264\n" \
"v_smfmac_f32_16x16x64_bf16 a[192:195], a[200:203], v[68:75], v21\n" \
"ds_read_b128 a[144:147], v49 offset:12288\n" \
"ds_read_b128 a[148:151], v49 offset:13312\n" \
"v_mfma_f32_16x16x32_bf16 v[112:115], a[0:3], a[64:67], v[32:35]\n" \
"ds_read_b128 a[152:155], v49 offset:14336\n" \
"ds_read_b128 a[156:159], v49 offset:15360\n" \
"v_mfma_f32_16x16x32_bf16 v[112:115], a[4:7], a[68:71], v[112:115]\n" \
"buffer_load_dwordx4 v6, s[84:87], s92 offen offset:2048 lds\n" \
"v_mfma_f32_16x16x32_bf16 v[112:115], a[8:11], a[72:75], v[112:115]\n" \
"v_accvgpr_read_b32 v40, a196\n" \
"v_accvgpr_read_b32 v24, a197\n" \
"v_mfma_f32_16x16x32_bf16 v[112:115], a[12:15], a[76:79], v[112:115]\n" \
"v_accvgpr_read_b32 v41, a198\n" \
"v_accvgpr_read_b32 v25, a199\n" \
"v_mfma_f32_16x16x32_bf16 v[116:119], a[28:31], a[76:79], v[32:35]\n" \
"s_waitcnt vmcnt(7)\n" \
"v_cmp_ne_u32_e32 vcc, s62, v57\n" \
"s_mov_b64 exec, 1\n" \
"buffer_store_dword v61, off, s[56:59], s95 offset:0 sc1\n" \
"s_mov_b64 exec, -1\n" \
"s_waitcnt lgkmcnt(15)\n" \
"v_mfma_f32_16x16x32_bf16 v[116:119], a[24:27], a[72:75], v[116:119]\n" \
"s_cbranch_vccnz L_so1_%=\n" \
"L_ro1_%=:\n" \
"v_add_f32_e32 v40, v40, v24\n" \
"v_mfma_f32_16x16x32_bf16 v[116:119], a[20:23], a[68:71], v[116:119]\n" \
"v_add_f32_e32 v41, v41, v25\n" \
"v_add_f32_e32 v40, s94, v40\n" \
"v_mfma_f32_16x16x32_bf16 v[116:119], a[16:19], a[64:67], v[116:119]\n" \
"v_add_f32_e32 v41, s94, v41\n" \
"ds_write_b64 v19, v[40:41]\n" \
"v_mfma_f32_16x16x32_bf16 v[124:127], a[16:19], a[80:83], v[36:39]\n" \
"v_add_u32_e32 v52, s15, v20\n" \
"v_add_u32_e32 v51, 0x10000, v52\n" \
"v_mfma_f32_16x16x32_bf16 v[124:127], a[20:23], a[84:87], v[124:127]\n" \
"v_exp_f32_e32 v104, v104\n" \
"buffer_load_dwordx4 v7, s[84:87], s92 offen offset:3072 lds\n" \
"s_add_u32 m0, s93, 0x2000\n" \
"v_mfma_f32_16x16x32_bf16 v[124:127], a[24:27], a[88:91], v[124:127]\n" \
"v_exp_f32_e32 v105, v105\n" \
"v_mfma_f32_16x16x32_bf16 v[124:127], a[28:31], a[92:95], v[124:127]\n" \
"v_exp_f32_e32 v106, v106\n" \
"v_mfma_f32_16x16x32_bf16 v[120:123], a[12:15], a[92:95], v[36:39]\n" \
"v_exp_f32_e32 v107, v107\n" \
"v_mfma_f32_16x16x32_bf16 v[120:123], a[8:11], a[88:91], v[120:123]\n" \
"v_cvt_pk_bf16_f32 v104, v104, v105\n" \
"v_cvt_pk_bf16_f32 v105, v106, v107\n" \
"v_mfma_f32_16x16x32_bf16 v[120:123], a[4:7], a[84:87], v[120:123]\n" \
"v_exp_f32_e32 v108, v108\n" \
"buffer_load_dwordx4 v8, s[84:87], s92 offen offset:0 lds\n" \
"v_mfma_f32_16x16x32_bf16 v[120:123], a[0:3], a[80:83], v[120:123]\n" \
"v_exp_f32_e32 v109, v109\n" \
"v_exp_f32_e32 v110, v110\n" \
"v_exp_f32_e32 v111, v111\n" \
"s_waitcnt lgkmcnt(0)\n" \
"s_barrier\n" \
"ds_read_b128 v[208:211], v52 offset:0\n" \
"ds_read_b128 v[212:215], v52 offset:1024\n" \
"ds_read_b128 v[216:219], v52 offset:2048\n" \
"ds_read_b128 v[220:223], v52 offset:3072\n" \
"ds_read_b128 v[240:243], v51 offset:32768\n" \
"ds_read_b128 v[244:247], v51 offset:33792\n" \
"ds_read_b128 v[248:251], v51 offset:34816\n" \
"ds_read_b128 v[252:255], v51 offset:35840\n" \
"buffer_load_dwordx4 a[220:223], v20, s[56:59], s60 offen offset:0 sc1\n" \
"v_cvt_pk_bf16_f32 v106, v108, v109\n" \
"v_cvt_pk_bf16_f32 v107, v110, v111\n" \
"v_and_b32_e32 v104, v12, v104\n" \
"v_and_b32_e32 v105, v13, v105\n" \
"v_and_b32_e32 v106, v14, v106\n" \
"v_and_b32_e32 v107, v15, v107\n" \
"v_xor_b32_e32 v63, 0x400, v19\n" \
"ds_read_b64 v[42:43], v63\n" \
"v_mfma_f32_16x16x32_bf16 a[192:195], a[212:215], v[104:107], a[192:195]\n" \
"v_mov_b32_e32 v194, v28\n" \
"v_mov_b32_e32 v195, v29\n" \
"v_mfma_f32_16x16x32_bf16 v[160:163], a[128:131], v[104:107], v[160:163]\n" \
"v_mov_b32_e32 v196, 0\n" \
"v_mov_b32_e32 v197, 0\n" \
"v_mfma_f32_16x16x32_bf16 v[164:167], a[132:135], v[104:107], v[164:167]\n" \
"v_mov_b32_e32 v192, 0\n" \
"v_mov_b32_e32 v193, 0\n" \
"v_mfma_f32_16x16x32_bf16 v[168:171], a[136:139], v[104:107], v[168:171]\n" \
"buffer_load_dwordx4 a[236:239], v56, s[56:59], s60 offen offset:0 sc1\n" \
"v_mfma_f32_16x16x32_bf16 v[172:175], a[140:143], v[104:107], v[172:175]\n" \
"v_exp_f32_e32 v118, v118\n" \
"v_mfma_f32_16x16x32_bf16 v[176:179], a[144:147], v[104:107], v[176:179]\n" \
"v_exp_f32_e32 v119, v119\n" \
"v_mfma_f32_16x16x32_bf16 v[180:183], a[148:151], v[104:107], v[180:183]\n" \
"v_exp_f32_e32 v120, v120\n" \
"v_mfma_f32_16x16x32_bf16 v[184:187], a[152:155], v[104:107], v[184:187]\n" \
"v_exp_f32_e32 v121, v121\n" \
"v_mfma_f32_16x16x32_bf16 v[188:191], a[156:159], v[104:107], v[188:191]\n" \
"v_exp_f32_e32 v116, v116\n" \
"s_waitcnt lgkmcnt(1)\n" \
"s_barrier\n" \
"buffer_load_dwordx4 a[224:227], v20, s[56:59], s60 offen offset:1024 sc1\n" \
"v_mfma_f32_16x16x32_bf16 v[128:131], v[194:197], v[208:211], v[128:131]\n" \
"v_exp_f32_e32 v117, v117\n" \
"v_mfma_f32_16x16x32_bf16 v[132:135], v[192:195], v[208:211], v[132:135]\n" \
"v_exp_f32_e32 v122, v122\n" \
"v_mfma_f32_16x16x32_bf16 v[136:139], v[194:197], v[212:215], v[136:139]\n" \
"v_exp_f32_e32 v123, v123\n" \
"v_mfma_f32_16x16x32_bf16 v[140:143], v[192:195], v[212:215], v[140:143]\n" \
"v_cvt_pk_bf16_f32 v119, v118, v119\n" \
"v_cvt_pk_bf16_f32 v120, v120, v121\n" \
"v_mfma_f32_16x16x32_bf16 v[144:147], v[194:197], v[216:219], v[144:147]\n" \
"v_exp_f32_e32 v114, v114\n" \
"v_mfma_f32_16x16x32_bf16 v[148:151], v[192:195], v[216:219], v[148:151]\n" \
"buffer_load_dwordx4 a[240:243], v56, s[56:59], s60 offen offset:1024 sc1\n" \
"v_mfma_f32_16x16x32_bf16 v[152:155], v[194:197], v[220:223], v[152:155]\n" \
"v_exp_f32_e32 v115, v115\n" \
"v_mfma_f32_16x16x32_bf16 v[156:159], v[192:195], v[220:223], v[156:159]\n" \
"v_exp_f32_e32 v112, v112\n" \
"v_mfma_f32_16x16x32_bf16 v[160:163], v[194:197], v[240:243], v[160:163]\n" \
"v_exp_f32_e32 v113, v113\n" \
"v_mfma_f32_16x16x32_bf16 v[164:167], v[192:195], v[240:243], v[164:167]\n" \
"v_exp_f32_e32 v124, v124\n" \
"v_mfma_f32_16x16x32_bf16 v[168:171], v[194:197], v[244:247], v[168:171]\n" \
"v_exp_f32_e32 v125, v125\n" \
"v_mfma_f32_16x16x32_bf16 v[172:175], v[192:195], v[244:247], v[172:175]\n" \
"buffer_load_dwordx4 a[228:231], v20, s[56:59], s60 offen offset:2048 sc1\n" \
"v_mfma_f32_16x16x32_bf16 v[176:179], v[194:197], v[248:251], v[176:179]\n" \
"v_exp_f32_e32 v126, v126\n" \
"v_mfma_f32_16x16x32_bf16 v[180:183], v[192:195], v[248:251], v[180:183]\n" \
"v_exp_f32_e32 v127, v127\n" \
"v_mfma_f32_16x16x32_bf16 v[184:187], v[194:197], v[252:255], v[184:187]\n" \
"v_cvt_pk_bf16_f32 v118, v116, v117\n" \
"v_cvt_pk_bf16_f32 v121, v122, v123\n" \
"v_mfma_f32_16x16x32_bf16 v[188:191], v[192:195], v[252:255], v[188:191]\n" \
"v_cvt_pk_bf16_f32 v117, v114, v115\n" \
"v_cvt_pk_bf16_f32 v116, v112, v113\n" \
"v_cvt_pk_bf16_f32 v122, v124, v125\n" \
"v_cvt_pk_bf16_f32 v123, v126, v127\n" \
"s_nop 1\n" \
"v_smfmac_f32_16x16x64_bf16 a[192:195], a[200:203], v[116:123], v21\n" \
"buffer_load_dwordx4 a[244:247], v56, s[56:59], s60 offen offset:2048 sc1\n" \
"v_mfma_f32_16x16x32_bf16 v[128:131], a[32:35], v[116:119], v[128:131]\n" \
"v_mfma_f32_16x16x32_bf16 v[160:163], a[32:35], v[120:123], v[160:163]\n" \
"v_mfma_f32_16x16x32_bf16 v[132:135], a[36:39], v[116:119], v[132:135]\n" \
"v_mfma_f32_16x16x32_bf16 v[164:167], a[36:39], v[120:123], v[164:167]\n" \
"v_mfma_f32_16x16x32_bf16 v[136:139], a[40:43], v[116:119], v[136:139]\n" \
"v_mfma_f32_16x16x32_bf16 v[168:171], a[40:43], v[120:123], v[168:171]\n" \
"buffer_load_dwordx4 a[232:235], v20, s[56:59], s60 offen offset:3072 sc1\n" \
"v_mfma_f32_16x16x32_bf16 v[140:143], a[44:47], v[116:119], v[140:143]\n" \
"v_accvgpr_read_b32 v54, a192\n" \
"v_accvgpr_read_b32 v22, a193\n" \
"v_mfma_f32_16x16x32_bf16 v[172:175], a[44:47], v[120:123], v[172:175]\n" \
"v_add_f32_e32 v54, v54, v22\n" \
"s_waitcnt lgkmcnt(0)\n" \
"v_add_f32_e32 v54, v54, v42\n" \
"v_mfma_f32_16x16x32_bf16 v[144:147], a[48:51], v[116:119], v[144:147]\n" \
"v_max_f32_e32 v62, v62, v54\n" \
"v_accvgpr_read_b32 v55, a194\n" \
"v_mfma_f32_16x16x32_bf16 v[176:179], a[48:51], v[120:123], v[176:179]\n" \
"v_rcp_f32_e32 v54, v54\n" \
"v_mfma_f32_16x16x32_bf16 v[148:151], a[52:55], v[116:119], v[148:151]\n" \
"v_mul_f32_e32 v54, v54, v58\n" \
"v_mul_f32_e32 v128, v54, v128\n" \
"buffer_load_dwordx4 a[248:251], v56, s[56:59], s60 offen offset:3072 sc1\n" \
"v_mfma_f32_16x16x32_bf16 v[180:183], a[52:55], v[120:123], v[180:183]\n" \
"v_mul_f32_e32 v129, v54, v129\n" \
"v_mul_f32_e32 v130, v54, v130\n" \
"v_mul_f32_e32 v131, v54, v131\n" \
"v_mul_f32_e32 v132, v54, v132\n" \
"v_mul_f32_e32 v133, v54, v133\n" \
"v_mul_f32_e32 v134, v54, v134\n" \
"v_mul_f32_e32 v135, v54, v135\n" \
"v_cvt_pk_bf16_f32 v64, v128, v129\n" \
"v_cvt_pk_bf16_f32 v65, v130, v131\n" \
"v_cvt_pk_bf16_f32 v66, v132, v133\n" \
"v_cvt_pk_bf16_f32 v67, v134, v135\n" \
"v_accvgpr_read_b32 v23, a195\n" \
"v_mfma_f32_16x16x32_bf16 v[152:155], a[56:59], v[116:119], v[152:155]\n" \
"v_mfma_f32_16x16x32_bf16 a[208:211], a[216:219], v[64:67], a[208:211]\n" \
"v_mul_f32_e32 v136, v54, v136\n" \
"v_mul_f32_e32 v137, v54, v137\n" \
"buffer_load_dwordx4 a[96:99], v20, s[56:59], s97 offen offset:0 sc1\n" \
"v_mul_f32_e32 v138, v54, v138\n" \
"v_mul_f32_e32 v139, v54, v139\n" \
"v_mul_f32_e32 v140, v54, v140\n" \
"v_mul_f32_e32 v141, v54, v141\n" \
"v_mul_f32_e32 v142, v54, v142\n" \
"v_mul_f32_e32 v143, v54, v143\n" \
"v_add_f32_e32 v55, v55, v23\n" \
"v_cvt_pk_bf16_f32 v68, v136, v137\n" \
"v_cvt_pk_bf16_f32 v69, v138, v139\n" \
"v_cvt_pk_bf16_f32 v70, v140, v141\n" \
"v_cvt_pk_bf16_f32 v71, v142, v143\n" \
"v_add_f32_e32 v55, v55, v43\n" \
"v_mul_f32_e32 v144, v54, v144\n" \
"v_mfma_f32_16x16x32_bf16 a[208:211], a[216:219], v[68:71], a[208:211]\n" \
"v_mul_f32_e32 v145, v54, v145\n" \
"v_mul_f32_e32 v146, v54, v146\n" \
"v_mul_f32_e32 v147, v54, v147\n" \
"v_mul_f32_e32 v148, v54, v148\n" \
"v_mul_f32_e32 v149, v54, v149\n" \
"v_mul_f32_e32 v150, v54, v150\n" \
"buffer_load_dwordx4 a[104:107], v56, s[56:59], s97 offen offset:0 sc1\n" \
"v_mul_f32_e32 v151, v54, v151\n" \
"v_max_f32_e32 v62, v62, v55\n" \
"v_cvt_pk_bf16_f32 v72, v144, v145\n" \
"v_cvt_pk_bf16_f32 v73, v146, v147\n" \
"v_cvt_pk_bf16_f32 v74, v148, v149\n" \
"v_cvt_pk_bf16_f32 v75, v150, v151\n" \
"v_rcp_f32_e32 v55, v55\n" \
"v_mfma_f32_16x16x32_bf16 v[184:187], a[56:59], v[120:123], v[184:187]\n" \
"v_mfma_f32_16x16x32_bf16 a[208:211], a[216:219], v[72:75], a[208:211]\n" \
"v_mul_f32_e32 v152, v54, v152\n" \
"v_mul_f32_e32 v153, v54, v153\n" \
"v_mul_f32_e32 v154, v54, v154\n" \
"v_mul_f32_e32 v155, v54, v155\n" \
"v_cvt_pk_bf16_f32 v76, v152, v153\n" \
"v_cvt_pk_bf16_f32 v77, v154, v155\n" \
"v_mul_f32_e32 v55, v55, v59\n" \
"buffer_load_dwordx4 a[112:115], v20, s[56:59], s98 offen offset:0 sc1\n" \
"v_mul_f32_e32 v160, v55, v160\n" \
"v_mul_f32_e32 v161, v55, v161\n" \
"v_mul_f32_e32 v162, v55, v162\n" \
"v_mul_f32_e32 v163, v55, v163\n" \
"v_mul_f32_e32 v164, v55, v164\n" \
"v_mul_f32_e32 v165, v55, v165\n" \
"v_mul_f32_e32 v166, v55, v166\n" \
"v_mul_f32_e32 v167, v55, v167\n" \
"v_cvt_pk_bf16_f32 v80, v160, v161\n" \
"v_cvt_pk_bf16_f32 v81, v162, v163\n" \
"v_cvt_pk_bf16_f32 v82, v164, v165\n" \
"v_cvt_pk_bf16_f32 v83, v166, v167\n" \
"v_mfma_f32_16x16x32_bf16 v[156:159], a[60:63], v[116:119], v[156:159]\n" \
"v_mul_f32_e32 v168, v55, v168\n" \
"v_mul_f32_e32 v169, v55, v169\n" \
"v_mul_f32_e32 v170, v55, v170\n" \
"v_mul_f32_e32 v171, v55, v171\n" \
"v_mul_f32_e32 v172, v55, v172\n" \
"v_mul_f32_e32 v173, v55, v173\n" \
"v_mul_f32_e32 v174, v55, v174\n" \
"buffer_load_dwordx4 a[120:123], v56, s[56:59], s98 offen offset:0 sc1\n" \
"v_mul_f32_e32 v156, v54, v156\n" \
"v_mul_f32_e32 v157, v54, v157\n" \
"v_mul_f32_e32 v158, v54, v158\n" \
"v_mul_f32_e32 v159, v54, v159\n" \
"v_cvt_pk_bf16_f32 v78, v156, v157\n" \
"v_cvt_pk_bf16_f32 v79, v158, v159\n" \
"v_mul_f32_e32 v175, v55, v175\n" \
"v_cvt_pk_bf16_f32 v84, v168, v169\n" \
"v_mfma_f32_16x16x32_bf16 a[208:211], a[216:219], v[76:79], a[208:211]\n" \
"v_mfma_f32_16x16x32_bf16 a[208:211], a[216:219], v[80:83], a[208:211]\n" \
"v_cvt_pk_bf16_f32 v85, v170, v171\n" \
"v_cvt_pk_bf16_f32 v86, v172, v173\n" \
"v_cvt_pk_bf16_f32 v87, v174, v175\n" \
"v_mul_f32_e32 v176, v55, v176\n" \
"v_mul_f32_e32 v177, v55, v177\n" \
"v_mfma_f32_16x16x32_bf16 a[208:211], a[216:219], v[84:87], a[208:211]\n" \
"buffer_load_dwordx4 a[100:103], v20, s[56:59], s97 offen offset:1024 sc1\n" \
"v_mul_f32_e32 v178, v55, v178\n" \
"v_mul_f32_e32 v179, v55, v179\n" \
"v_mul_f32_e32 v180, v55, v180\n" \
"v_mul_f32_e32 v181, v55, v181\n" \
"v_mul_f32_e32 v182, v55, v182\n" \
"v_mul_f32_e32 v183, v55, v183\n" \
"v_cvt_pk_bf16_f32 v96, v176, v177\n" \
"v_cvt_pk_bf16_f32 v97, v178, v179\n" \
"v_cvt_pk_bf16_f32 v98, v180, v181\n" \
"v_cvt_pk_bf16_f32 v99, v182, v183\n" \
"v_mfma_f32_16x16x32_bf16 v[188:191], a[60:63], v[120:123], v[188:191]\n" \
"v_mul_f32_e32 v184, v55, v184\n" \
"v_mfma_f32_16x16x32_bf16 a[208:211], a[216:219], v[96:99], a[208:211]\n" \
"v_mul_f32_e32 v185, v55, v185\n" \
"v_mul_f32_e32 v186, v55, v186\n" \
"v_mul_f32_e32 v187, v55, v187\n" \
"v_cvt_pk_bf16_f32 v100, v184, v185\n" \
"v_cvt_pk_bf16_f32 v101, v186, v187\n" \
"buffer_load_dwordx4 a[108:111], v56, s[56:59], s97 offen offset:1024 sc1\n" \
"v_mul_f32_e32 v188, v55, v188\n" \
"v_mul_f32_e32 v189, v55, v189\n" \
"v_mul_f32_e32 v190, v55, v190\n" \
"v_mul_f32_e32 v191, v55, v191\n" \
"v_cvt_pk_bf16_f32 v102, v188, v189\n" \
"v_cvt_pk_bf16_f32 v103, v190, v191\n" \
"v_cmp_lt_f32_e64 s[72:73], s21, v62\n" \
"buffer_load_dwordx4 a[116:119], v20, s[56:59], s98 offen offset:1024 sc1\n" \
"v_mfma_f32_16x16x32_bf16 a[208:211], a[216:219], v[100:103], a[208:211]\n" \
"buffer_load_dwordx4 a[124:127], v56, s[56:59], s98 offen offset:1024 sc1\n" \
"s_nop 6\n" \
"v_accvgpr_read_b32 v63, a208\n" \
"v_cmp_u_f32_e32 vcc, v63, v63\n" \
"s_nop 0\n" \
"s_or_b64 vcc, s[72:73], vcc\n" \
"s_cbranch_scc0 L_out_%=\n" \
"s_branch L_end_%=\n" \
"L_att_stub:\n" \
"s_mov_b32 s101, 0\n" \
"v_add_u32_e32 v32, 0x49000, v20\n" \
"v_readlane_b32 s96, v11, 35\n" \
"v_readlane_b32 s97, v11, 36\n" \
"buffer_load_dwordx4 a[220:223], v20, s[56:59], s60 offen offset:0 sc1\n" \
"buffer_load_dwordx4 a[236:239], v32, s[56:59], s60 offen offset:0 sc1\n" \
"buffer_load_dwordx4 a[224:227], v20, s[56:59], s60 offen offset:1024 sc1\n" \
"buffer_load_dwordx4 a[240:243], v32, s[56:59], s60 offen offset:1024 sc1\n" \
"buffer_load_dwordx4 a[228:231], v20, s[56:59], s60 offen offset:2048 sc1\n" \
"buffer_load_dwordx4 a[244:247], v32, s[56:59], s60 offen offset:2048 sc1\n" \
"buffer_load_dwordx4 a[232:235], v20, s[56:59], s60 offen offset:3072 sc1\n" \
"buffer_load_dwordx4 a[248:251], v32, s[56:59], s60 offen offset:3072 sc1\n" \
"buffer_load_dwordx4 a[96:99], v20, s[56:59], s96 offen offset:0 sc1\n" \
"buffer_load_dwordx4 a[104:107], v32, s[56:59], s96 offen offset:0 sc1\n" \
"buffer_load_dwordx4 a[112:115], v20, s[56:59], s97 offen offset:0 sc1\n" \
"buffer_load_dwordx4 a[120:123], v32, s[56:59], s97 offen offset:0 sc1\n" \
"buffer_load_dwordx4 a[100:103], v20, s[56:59], s96 offen offset:1024 sc1\n" \
"buffer_load_dwordx4 a[108:111], v32, s[56:59], s96 offen offset:1024 sc1\n" \
"buffer_load_dwordx4 a[116:119], v20, s[56:59], s97 offen offset:1024 sc1\n" \
"buffer_load_dwordx4 a[124:127], v32, s[56:59], s97 offen offset:1024 sc1\n" \
"buffer_load_dwordx4 v[64:67], v20, s[56:59], s61 offen offset:0 sc1\n" \
"buffer_load_dwordx4 v[68:71], v20, s[56:59], s61 offen offset:1024 sc1\n" \
"buffer_load_dwordx4 v[72:75], v20, s[56:59], s61 offen offset:2048 sc1\n" \
"buffer_load_dwordx4 v[76:79], v20, s[56:59], s61 offen offset:3072 sc1\n" \
"buffer_load_dwordx4 v[80:83], v32, s[56:59], s61 offen offset:0 sc1\n" \
"buffer_load_dwordx4 v[84:87], v32, s[56:59], s61 offen offset:1024 sc1\n" \
"buffer_load_dwordx4 v[96:99], v32, s[56:59], s61 offen offset:2048 sc1\n" \
"buffer_load_dwordx4 v[100:103], v32, s[56:59], s61 offen offset:3072 sc1\n" \
"v_not_b32_e32 v26, s62\n" \
"s_waitcnt vmcnt(0)\n" \
"s_branch L_out_%=\n" \
".fill (L_ab + 20480 - .) / 4, 4, 0xBF800000\n" \
".fill (L_ab + 20480 - .) / 4, 4, 0xBF800000\n" \
"L_ap5:\n" \
"s_setpc_b64 s[88:89]\n" \
"L_out_%=:\n" \
"v_bfe_u32 v38, v20, 2, 4\n" \
"v_readlane_b32 s64, v11, 2\n" \
"v_add_u32_e32 v32, 0x49000, v20\n" \
"v_add_u32_e32 v34, 0x20000, v10\n" \
"ds_read_b64_tr_b16 a[0:1], v34 offset:0\n" \
"ds_read_b64_tr_b16 a[2:3], v34 offset:4608\n" \
"ds_read_b64_tr_b16 a[4:5], v34 offset:32\n" \
"buffer_load_dword v26, v38, s[56:59], s64 offen sc1\n" \
"ds_read_b64_tr_b16 a[6:7], v34 offset:4640\n" \
"ds_read_b64_tr_b16 a[8:9], v34 offset:64\n" \
"ds_read_b64_tr_b16 a[10:11], v34 offset:4672\n" \
"ds_read_b64_tr_b16 a[12:13], v34 offset:96\n" \
"buffer_store_dwordx4 v[64:67], v20, s[56:59], s61 offen offset:0 sc1\n" \
"ds_read_b64_tr_b16 a[14:15], v34 offset:4704\n" \
"v_mov_b32_e32 v39, s62\n" \
"v_readlane_b32 s72, v11, 5\n" \
"v_readlane_b32 s73, v11, 4\n" \
"buffer_store_dwordx4 v[68:71], v20, s[56:59], s61 offen offset:1024 sc1\n" \
"ds_read_b64_tr_b16 a[16:17], v34 offset:128\n" \
"ds_read_b64_tr_b16 a[18:19], v34 offset:4736\n" \
"ds_read_b64_tr_b16 a[20:21], v34 offset:160\n" \
"ds_read_b64_tr_b16 a[22:23], v34 offset:4768\n" \
"buffer_store_dwordx4 v[72:75], v20, s[56:59], s61 offen offset:2048 sc1\n" \
"ds_read_b64_tr_b16 a[24:25], v34 offset:192\n" \
"ds_read_b64_tr_b16 a[26:27], v34 offset:4800\n" \
"ds_read_b64_tr_b16 a[28:29], v34 offset:224\n" \
"ds_read_b64_tr_b16 a[30:31], v34 offset:4832\n" \
"buffer_store_dwordx4 v[76:79], v20, s[56:59], s61 offen offset:3072 sc1\n" \
"ds_read_b64_tr_b16 a[192:193], v34 offset:256\n" \
"ds_read_b64_tr_b16 a[194:195], v34 offset:4864\n" \
"ds_read_b64_tr_b16 a[196:197], v34 offset:9216\n" \
"ds_read_b64_tr_b16 a[198:199], v34 offset:13824\n" \
"buffer_store_dwordx4 v[80:83], v32, s[56:59], s61 offen offset:0 sc1\n" \
"ds_read_b64_tr_b16 a[200:201], v34 offset:9248\n" \
"ds_read_b64_tr_b16 a[202:203], v34 offset:13856\n" \
"ds_read_b64_tr_b16 a[204:205], v34 offset:9280\n" \
"ds_read_b64_tr_b16 a[206:207], v34 offset:13888\n" \
"buffer_store_dwordx4 v[84:87], v32, s[56:59], s61 offen offset:1024 sc1\n" \
"ds_read_b64_tr_b16 a[208:209], v34 offset:9312\n" \
"s_waitcnt vmcnt(15) lgkmcnt(15)\n" \
"v_mfma_f32_16x16x32_bf16 v[112:115], a[0:3], a[220:223], 0\n" \
"buffer_store_dwordx4 v[96:99], v32, s[56:59], s61 offen offset:2048 sc1\n" \
"v_mfma_f32_16x16x32_bf16 v[116:119], a[0:3], a[236:239], 0\n" \
"ds_read_b64_tr_b16 a[210:211], v34 offset:13920\n" \
"ds_read_b64_tr_b16 a[212:213], v34 offset:9344\n" \
"buffer_store_dwordx4 v[100:103], v32, s[56:59], s61 offen offset:3072 sc1\n" \
"v_mfma_f32_16x16x32_bf16 v[184:187], a[0:3], v[64:67], 0\n" \
"ds_read_b64_tr_b16 a[214:215], v34 offset:13952\n" \
"ds_read_b64_tr_b16 a[216:217], v34 offset:9376\n" \
"v_mfma_f32_16x16x32_bf16 v[188:191], a[0:3], v[80:83], 0\n" \
"ds_read_b64_tr_b16 a[218:219], v34 offset:13984\n" \
"ds_read_b64_tr_b16 a[0:1], v34 offset:9408\n" \
"v_mfma_f32_16x16x32_bf16 v[120:123], a[4:7], a[220:223], 0\n" \
"ds_read_b64_tr_b16 a[2:3], v34 offset:14016\n" \
"v_readlane_b32 s66, v11, 21\n" \
"v_mfma_f32_16x16x32_bf16 v[124:127], a[4:7], a[236:239], 0\n" \
"v_readlane_b32 s80, v11, 11\n" \
"v_readlane_b32 s81, v11, 12\n" \
"v_mfma_f32_16x16x32_bf16 v[192:195], a[4:7], v[64:67], 0\n" \
"s_mov_b32 s82, 0x47fca0\n" \
"s_mov_b32 s83, 0x20000\n" \
"v_mfma_f32_16x16x32_bf16 v[196:199], a[4:7], v[80:83], 0\n" \
"ds_read_b64_tr_b16 a[4:5], v34 offset:9440\n" \
"ds_read_b64_tr_b16 a[6:7], v34 offset:14048\n" \
"v_mfma_f32_16x16x32_bf16 v[128:131], a[8:11], a[220:223], 0\n" \
"v_readlane_b32 s65, v11, 26\n" \
"s_add_u32 m0, s66, 0x0\n" \
"v_mfma_f32_16x16x32_bf16 v[132:135], a[8:11], a[236:239], 0\n" \
"v_readlane_b32 s67, v11, 29\n" \
"v_lshrrev_b32_e32 v44, 9, v20\n" \
"v_mfma_f32_16x16x32_bf16 v[200:203], a[8:11], v[64:67], 0\n" \
"buffer_load_dwordx4 v0, s[80:83], s65 offen offset:0 lds\n" \
"v_mfma_f32_16x16x32_bf16 v[204:207], a[8:11], v[80:83], 0\n" \
"ds_read_b64_tr_b16 a[8:9], v34 offset:9472\n" \
"ds_read_b64_tr_b16 a[10:11], v34 offset:14080\n" \
"v_mfma_f32_16x16x32_bf16 v[136:139], a[12:15], a[220:223], 0\n" \
"v_mul_u32_u24_e32 v44, 0xb40, v44\n" \
"v_add_u32_e32 v35, v44, v10\n" \
"v_mfma_f32_16x16x32_bf16 v[140:143], a[12:15], a[236:239], 0\n" \
"v_readlane_b32 s69, v11, 22\n" \
"v_readlane_b32 s68, v11, 27\n" \
"v_mfma_f32_16x16x32_bf16 v[208:211], a[12:15], v[64:67], 0\n" \
"v_readlane_b32 s94, v11, 23\n" \
"v_readlane_b32 s93, v11, 28\n" \
"v_mfma_f32_16x16x32_bf16 v[212:215], a[12:15], v[80:83], 0\n" \
"ds_read_b64_tr_b16 a[12:13], v34 offset:18432\n" \
"ds_read_b64_tr_b16 a[14:15], v34 offset:23040\n" \
"v_mfma_f32_16x16x32_bf16 v[144:147], a[16:19], a[220:223], 0\n" \
"buffer_load_dwordx4 v1, s[80:83], s65 offen offset:1024 lds\n" \
"v_mfma_f32_16x16x32_bf16 v[148:151], a[16:19], a[236:239], 0\n" \
"v_readlane_b32 s96, v11, 24\n" \
"v_readlane_b32 s84, v11, 9\n" \
"v_mfma_f32_16x16x32_bf16 v[216:219], a[16:19], v[64:67], 0\n" \
"v_readlane_b32 s85, v11, 10\n" \
"s_mov_b32 s86, 0xa1ca0\n" \
"v_mfma_f32_16x16x32_bf16 v[220:223], a[16:19], v[80:83], 0\n" \
"ds_read_b64_tr_b16 a[16:17], v34 offset:18464\n" \
"s_waitcnt lgkmcnt(7)\n" \
"v_mfma_f32_16x16x32_bf16 v[152:155], a[20:23], a[220:223], 0\n" \
"ds_read_b64_tr_b16 a[18:19], v34 offset:23072\n" \
"s_mov_b32 s87, 0x20000\n" \
"v_mfma_f32_16x16x32_bf16 v[156:159], a[20:23], a[236:239], 0\n" \
"buffer_load_dwordx4 v2, s[80:83], s65 offen offset:2048 lds\n" \
"v_mfma_f32_16x16x32_bf16 v[224:227], a[20:23], v[64:67], 0\n" \
"v_readlane_b32 s70, v11, 30\n" \
"v_add_u32_e32 v33, 0x10000, v10\n" \
"v_mfma_f32_16x16x32_bf16 v[228:231], a[20:23], v[80:83], 0\n" \
"ds_read_b64_tr_b16 a[20:21], v34 offset:18496\n" \
"ds_read_b64_tr_b16 a[22:23], v34 offset:23104\n" \
"v_mfma_f32_16x16x32_bf16 v[160:163], a[24:27], a[220:223], 0\n" \
"v_readlane_b32 s71, v11, 31\n" \
"v_readlane_b32 s92, v11, 33\n" \
"v_mfma_f32_16x16x32_bf16 v[164:167], a[24:27], a[236:239], 0\n" \
"v_readlane_b32 s95, v11, 34\n" \
"v_lshlrev_b32_e32 v41, 12, v20\n" \
"v_mfma_f32_16x16x32_bf16 v[232:235], a[24:27], v[64:67], 0\n" \
"buffer_load_dwordx4 v3, s[80:83], s65 offen offset:3072 lds\n" \
"v_mfma_f32_16x16x32_bf16 v[236:239], a[24:27], v[80:83], 0\n" \
"ds_read_b64_tr_b16 a[24:25], v34 offset:18528\n" \
"ds_read_b64_tr_b16 a[26:27], v34 offset:23136\n" \
"v_mfma_f32_16x16x32_bf16 v[168:171], a[28:31], a[220:223], 0\n" \
"s_add_u32 m0, s66, 0x1000\n" \
"v_readlane_b32 s97, v11, 37\n" \
"v_mfma_f32_16x16x32_bf16 v[172:175], a[28:31], a[236:239], 0\n" \
"v_readlane_b32 s63, v11, 40\n" \
"v_bfe_u32 v40, v20, 2, 7\n" \
"v_mfma_f32_16x16x32_bf16 v[240:243], a[28:31], v[64:67], 0\n" \
"s_add_u32 s74, s63, 0x3800\n" \
"v_readlane_b32 s100, v11, 7\n" \
"v_mfma_f32_16x16x32_bf16 v[244:247], a[28:31], v[80:83], 0\n" \
"ds_read_b64_tr_b16 a[28:29], v34 offset:18560\n" \
"ds_read_b64_tr_b16 a[30:31], v34 offset:23168\n" \
"v_mfma_f32_16x16x32_bf16 v[176:179], a[192:195], a[220:223], 0\n" \
"buffer_load_dwordx4 v4, s[80:83], s65 offen offset:0 lds\n" \
"v_mfma_f32_16x16x32_bf16 v[180:183], a[192:195], a[236:239], 0\n" \
"v_bfe_u32 v44, v20, 4, 4\n" \
"v_bfe_u32 v45, v20, 8, 2\n" \
"v_mfma_f32_16x16x32_bf16 v[248:251], a[192:195], v[64:67], 0\n" \
"v_mul_u32_u24_e32 v42, 0x480, v44\n" \
"v_lshl_add_u32 v42, v45, 3, v42\n" \
"v_mfma_f32_16x16x32_bf16 v[252:255], a[192:195], v[80:83], 0\n" \
"ds_read_b64_tr_b16 a[192:193], v34 offset:18592\n" \
"ds_read_b64_tr_b16 a[194:195], v34 offset:23200\n" \
"v_mfma_f32_16x16x32_bf16 v[112:115], a[196:199], a[224:227], v[112:115]\n" \
"v_lshrrev_b32_e32 v44, 1, v44\n" \
"v_lshlrev_b32_e32 v45, 1, v45\n" \
"v_mfma_f32_16x16x32_bf16 v[116:119], a[196:199], a[240:243], v[116:119]\n" \
"buffer_load_dwordx4 v5, s[80:83], s65 offen offset:1024 lds\n" \
"v_mfma_f32_16x16x32_bf16 v[184:187], a[196:199], v[68:71], v[184:187]\n" \
"v_sub_u32_e32 v44, v44, v45\n" \
"v_and_b32_e32 v45, 16, v20\n" \
"v_mfma_f32_16x16x32_bf16 v[188:191], a[196:199], v[84:87], v[188:191]\n" \
"ds_read_b64_tr_b16 a[196:197], v34 offset:18624\n" \
"ds_read_b64_tr_b16 a[198:199], v34 offset:23232\n" \
"v_mfma_f32_16x16x32_bf16 v[120:123], a[200:203], a[224:227], v[120:123]\n" \
"v_mov_b32_e32 v46, 0x3f80\n" \
"v_lshlrev_b32_e32 v45, v45, v46\n" \
"v_mfma_f32_16x16x32_bf16 v[124:127], a[200:203], a[240:243], v[124:127]\n" \
"v_mov_b32_e32 v52, 0\n" \
"v_mov_b32_e32 v53, 0\n" \
"v_mfma_f32_16x16x32_bf16 v[192:195], a[200:203], v[68:71], v[192:195]\n" \
"buffer_load_dwordx4 v6, s[80:83], s65 offen offset:2048 lds\n" \
"v_mfma_f32_16x16x32_bf16 v[196:199], a[200:203], v[84:87], v[196:199]\n" \
"ds_read_b64_tr_b16 a[200:201], v34 offset:18656\n" \
"ds_read_b64_tr_b16 a[202:203], v34 offset:23264\n" \
"v_mfma_f32_16x16x32_bf16 v[128:131], a[204:207], a[224:227], v[128:131]\n" \
"s_waitcnt vmcnt(15)\n" \
"v_cmp_ne_u32_e32 vcc, s62, v26\n" \
"v_mfma_f32_16x16x32_bf16 v[132:135], a[204:207], a[240:243], v[132:135]\n" \
"v_mov_b32_e32 v48, 0\n" \
"v_mov_b32_e32 v49, 0\n" \
"v_mfma_f32_16x16x32_bf16 v[200:203], a[204:207], v[68:71], v[200:203]\n" \
"v_add_u32_e32 v43, 0x9000, v42\n" \
"s_cbranch_vccnz L_sa_%=\n" \
"L_ra_%=:\n" \
"v_mfma_f32_16x16x32_bf16 v[204:207], a[204:207], v[84:87], v[204:207]\n" \
"ds_read_b64_tr_b16 a[204:205], v34 offset:18688\n" \
"ds_read_b64_tr_b16 a[206:207], v34 offset:23296\n" \
"v_mfma_f32_16x16x32_bf16 v[136:139], a[208:211], a[224:227], v[136:139]\n" \
"buffer_load_dwordx4 v7, s[80:83], s65 offen offset:3072 lds\n" \
"v_mfma_f32_16x16x32_bf16 v[140:143], a[208:211], a[240:243], v[140:143]\n" \
"s_add_u32 m0, s66, 0x2000\n" \
"v_readlane_b32 s66, v11, 32\n" \
"v_mfma_f32_16x16x32_bf16 v[208:211], a[208:211], v[68:71], v[208:211]\n" \
"v_readlane_b32 s64, v11, 38\n" \
"v_readlane_b32 s88, v11, 13\n" \
"v_mfma_f32_16x16x32_bf16 v[212:215], a[208:211], v[84:87], v[212:215]\n" \
"v_readlane_b32 s89, v11, 14\n" \
"s_mov_b32 s90, 0x240000\n" \
"v_mfma_f32_16x16x32_bf16 v[144:147], a[212:215], a[224:227], v[144:147]\n" \
"s_mov_b32 s91, 0x20000\n" \
"v_mov_b32_e32 v46, s62\n" \
"v_mfma_f32_16x16x32_bf16 v[148:151], a[212:215], a[240:243], v[148:151]\n" \
"buffer_load_dwordx4 v8, s[80:83], s65 offen offset:0 lds\n" \
"v_mfma_f32_16x16x32_bf16 v[216:219], a[212:215], v[68:71], v[216:219]\n" \
"s_add_u32 m0, s69, 0x0\n" \
"v_readlane_b32 s65, v11, 39\n" \
"v_mfma_f32_16x16x32_bf16 v[220:223], a[212:215], v[84:87], v[220:223]\n" \
"v_mfma_f32_16x16x32_bf16 v[152:155], a[216:219], a[224:227], v[152:155]\n" \
"v_mfma_f32_16x16x32_bf16 v[156:159], a[216:219], a[240:243], v[156:159]\n" \
"v_mfma_f32_16x16x32_bf16 v[224:227], a[216:219], v[68:71], v[224:227]\n" \
"buffer_load_dwordx4 a[64:67], v20, s[56:59], s67 offen offset:0 sc1\n" \
"v_mfma_f32_16x16x32_bf16 v[228:231], a[216:219], v[84:87], v[228:231]\n" \
"v_mfma_f32_16x16x32_bf16 v[160:163], a[0:3], a[224:227], v[160:163]\n" \
"v_mfma_f32_16x16x32_bf16 v[164:167], a[0:3], a[240:243], v[164:167]\n" \
"v_mfma_f32_16x16x32_bf16 v[232:235], a[0:3], v[68:71], v[232:235]\n" \
"v_mfma_f32_16x16x32_bf16 v[236:239], a[0:3], v[84:87], v[236:239]\n" \
"s_waitcnt lgkmcnt(0)\n" \
"v_mfma_f32_16x16x32_bf16 v[168:171], a[4:7], a[224:227], v[168:171]\n" \
"buffer_load_dwordx4 a[80:83], v32, s[56:59], s67 offen offset:0 sc1\n" \
"v_mfma_f32_16x16x32_bf16 v[172:175], a[4:7], a[240:243], v[172:175]\n" \
"v_mfma_f32_16x16x32_bf16 v[240:243], a[4:7], v[68:71], v[240:243]\n" \
"v_mfma_f32_16x16x32_bf16 v[244:247], a[4:7], v[84:87], v[244:247]\n" \
"v_mfma_f32_16x16x32_bf16 v[176:179], a[8:11], a[224:227], v[176:179]\n" \
"v_mfma_f32_16x16x32_bf16 v[180:183], a[8:11], a[240:243], v[180:183]\n" \
"buffer_load_dwordx4 a[68:71], v20, s[56:59], s67 offen offset:1024 sc1\n" \
"v_mfma_f32_16x16x32_bf16 v[248:251], a[8:11], v[68:71], v[248:251]\n" \
"v_mfma_f32_16x16x32_bf16 v[252:255], a[8:11], v[84:87], v[252:255]\n" \
"v_mfma_f32_16x16x32_bf16 v[112:115], a[12:15], a[228:231], v[112:115]\n" \
"v_mfma_f32_16x16x32_bf16 v[116:119], a[12:15], a[244:247], v[116:119]\n" \
"v_mfma_f32_16x16x32_bf16 v[184:187], a[12:15], v[72:75], v[184:187]\n" \
"buffer_load_dwordx4 a[84:87], v32, s[56:59], s67 offen offset:1024 sc1\n" \
"v_mfma_f32_16x16x32_bf16 v[188:191], a[12:15], v[96:99], v[188:191]\n" \
"v_mfma_f32_16x16x32_bf16 v[120:123], a[16:19], a[228:231], v[120:123]\n" \
"v_mfma_f32_16x16x32_bf16 v[124:127], a[16:19], a[244:247], v[124:127]\n" \
"v_mfma_f32_16x16x32_bf16 v[192:195], a[16:19], v[72:75], v[192:195]\n" \
"v_mfma_f32_16x16x32_bf16 v[196:199], a[16:19], v[96:99], v[196:199]\n" \
"buffer_load_dwordx4 a[72:75], v20, s[56:59], s67 offen offset:2048 sc1\n" \
"v_mfma_f32_16x16x32_bf16 v[128:131], a[20:23], a[228:231], v[128:131]\n" \
"v_mfma_f32_16x16x32_bf16 v[132:135], a[20:23], a[244:247], v[132:135]\n" \
"v_mfma_f32_16x16x32_bf16 v[200:203], a[20:23], v[72:75], v[200:203]\n" \
"v_mfma_f32_16x16x32_bf16 v[204:207], a[20:23], v[96:99], v[204:207]\n" \
"s_barrier\n" \
"v_mfma_f32_16x16x32_bf16 v[136:139], a[24:27], a[228:231], v[136:139]\n" \
"ds_read_b64_tr_b16 a[208:209], v10 offset:52224\n" \
"ds_read_b64_tr_b16 a[210:211], v10 offset:56832\n" \
"v_mfma_f32_16x16x32_bf16 v[140:143], a[24:27], a[244:247], v[140:143]\n" \
"ds_read_b64_tr_b16 a[212:213], v10 offset:52256\n" \
"ds_read_b64_tr_b16 a[214:215], v10 offset:56864\n" \
"v_mfma_f32_16x16x32_bf16 v[208:211], a[24:27], v[72:75], v[208:211]\n" \
"ds_read_b64_tr_b16 a[216:217], v10 offset:52288\n" \
"ds_read_b64_tr_b16 a[218:219], v10 offset:56896\n" \
"v_mfma_f32_16x16x32_bf16 v[212:215], a[24:27], v[96:99], v[212:215]\n" \
"ds_read_b64_tr_b16 a[0:1], v10 offset:52320\n" \
"ds_read_b64_tr_b16 a[2:3], v10 offset:56928\n" \
"v_mfma_f32_16x16x32_bf16 v[144:147], a[28:31], a[228:231], v[144:147]\n" \
"ds_read_b64_tr_b16 a[4:5], v10 offset:52352\n" \
"ds_read_b64_tr_b16 a[6:7], v10 offset:56960\n" \
"v_mfma_f32_16x16x32_bf16 v[148:151], a[28:31], a[244:247], v[148:151]\n" \
"ds_read_b64_tr_b16 a[8:9], v10 offset:52384\n" \
"ds_read_b64_tr_b16 a[10:11], v10 offset:56992\n" \
"v_mfma_f32_16x16x32_bf16 v[216:219], a[28:31], v[72:75], v[216:219]\n" \
"ds_read_b64_tr_b16 a[12:13], v10 offset:52416\n" \
"ds_read_b64_tr_b16 a[14:15], v10 offset:57024\n" \
"v_mfma_f32_16x16x32_bf16 v[220:223], a[28:31], v[96:99], v[220:223]\n" \
"ds_read_b64_tr_b16 a[16:17], v10 offset:52448\n" \
"ds_read_b64_tr_b16 a[18:19], v10 offset:57056\n" \
"v_mfma_f32_16x16x32_bf16 v[152:155], a[192:195], a[228:231], v[152:155]\n" \
"ds_read_b64_tr_b16 a[20:21], v10 offset:52480\n" \
"ds_read_b64_tr_b16 a[22:23], v10 offset:57088\n" \
"v_mfma_f32_16x16x32_bf16 v[156:159], a[192:195], a[244:247], v[156:159]\n" \
"ds_read_b64_tr_b16 a[24:25], v35 offset:24576\n" \
"ds_read_b64_tr_b16 a[26:27], v35 offset:26880\n" \
"v_mfma_f32_16x16x32_bf16 v[224:227], a[192:195], v[72:75], v[224:227]\n" \
"ds_read_b64_tr_b16 a[28:29], v35 offset:24608\n" \
"ds_read_b64_tr_b16 a[30:31], v35 offset:26912\n" \
"v_mfma_f32_16x16x32_bf16 v[228:231], a[192:195], v[96:99], v[228:231]\n" \
"ds_read_b64_tr_b16 a[192:193], v35 offset:24640\n" \
"ds_read_b64_tr_b16 a[194:195], v35 offset:26944\n" \
"v_mfma_f32_16x16x32_bf16 v[160:163], a[196:199], a[228:231], v[160:163]\n" \
"buffer_load_dwordx4 a[88:91], v32, s[56:59], s67 offen offset:2048 sc1\n" \
"v_mfma_f32_16x16x32_bf16 v[164:167], a[196:199], a[244:247], v[164:167]\n" \
"v_mfma_f32_16x16x32_bf16 v[232:235], a[196:199], v[72:75], v[232:235]\n" \
"v_mfma_f32_16x16x32_bf16 v[236:239], a[196:199], v[96:99], v[236:239]\n" \
"ds_read_b64_tr_b16 a[196:197], v35 offset:24672\n" \
"ds_read_b64_tr_b16 a[198:199], v35 offset:26976\n" \
"v_mfma_f32_16x16x32_bf16 v[168:171], a[200:203], a[228:231], v[168:171]\n" \
"v_mfma_f32_16x16x32_bf16 v[172:175], a[200:203], a[244:247], v[172:175]\n" \
"buffer_load_dwordx4 v0, s[80:83], s68 offen offset:0 lds\n" \
"v_mfma_f32_16x16x32_bf16 v[240:243], a[200:203], v[72:75], v[240:243]\n" \
"v_mfma_f32_16x16x32_bf16 v[244:247], a[200:203], v[96:99], v[244:247]\n" \
"ds_read_b64_tr_b16 a[200:201], v35 offset:24704\n" \
"ds_read_b64_tr_b16 a[202:203], v35 offset:27008\n" \
"v_mfma_f32_16x16x32_bf16 v[176:179], a[204:207], a[228:231], v[176:179]\n" \
"v_mfma_f32_16x16x32_bf16 v[180:183], a[204:207], a[244:247], v[180:183]\n" \
"v_mfma_f32_16x16x32_bf16 v[248:251], a[204:207], v[72:75], v[248:251]\n" \
"buffer_load_dwordx4 v1, s[80:83], s68 offen offset:1024 lds\n" \
"v_mfma_f32_16x16x32_bf16 v[252:255], a[204:207], v[96:99], v[252:255]\n" \
"ds_read_b64_tr_b16 a[204:205], v35 offset:24736\n" \
"s_waitcnt lgkmcnt(9)\n" \
"v_mfma_f32_16x16x32_bf16 v[112:115], a[208:211], a[232:235], v[112:115]\n" \
"ds_read_b64_tr_b16 a[206:207], v35 offset:27040\n" \
"v_mfma_f32_16x16x32_bf16 v[116:119], a[208:211], a[248:251], v[116:119]\n" \
"v_mfma_f32_16x16x32_bf16 v[184:187], a[208:211], v[76:79], v[184:187]\n" \
"v_mfma_f32_16x16x32_bf16 v[188:191], a[208:211], v[100:103], v[188:191]\n" \
"ds_read_b64_tr_b16 a[208:209], v35 offset:24768\n" \
"ds_read_b64_tr_b16 a[210:211], v35 offset:27072\n" \
"v_mfma_f32_16x16x32_bf16 v[120:123], a[212:215], a[232:235], v[120:123]\n" \
"buffer_load_dwordx4 v2, s[80:83], s68 offen offset:2048 lds\n" \
"v_mfma_f32_16x16x32_bf16 v[124:127], a[212:215], a[248:251], v[124:127]\n" \
"v_mfma_f32_16x16x32_bf16 v[192:195], a[212:215], v[76:79], v[192:195]\n" \
"s_waitcnt vmcnt(18)\n" \
"s_mov_b64 exec, 1\n" \
"buffer_store_dword v39, off, s[56:59], s72 offset:0 sc1\n" \
"s_mov_b64 exec, -1\n" \
"v_mfma_f32_16x16x32_bf16 v[196:199], a[212:215], v[100:103], v[196:199]\n" \
"ds_read_b64_tr_b16 a[212:213], v35 offset:24800\n" \
"ds_read_b64_tr_b16 a[214:215], v35 offset:27104\n" \
"v_mfma_f32_16x16x32_bf16 v[128:131], a[216:219], a[232:235], v[128:131]\n" \
"v_readlane_b32 s72, v11, 50\n" \
"v_mfma_f32_16x16x32_bf16 v[132:135], a[216:219], a[248:251], v[132:135]\n" \
"v_mfma_f32_16x16x32_bf16 v[200:203], a[216:219], v[76:79], v[200:203]\n" \
"buffer_load_dwordx4 v3, s[80:83], s68 offen offset:3072 lds\n" \
"v_mfma_f32_16x16x32_bf16 v[204:207], a[216:219], v[100:103], v[204:207]\n" \
"ds_read_b64_tr_b16 a[216:217], v35 offset:24832\n" \
"ds_read_b64_tr_b16 a[218:219], v35 offset:27136\n" \
"v_mfma_f32_16x16x32_bf16 v[136:139], a[0:3], a[232:235], v[136:139]\n" \
"s_add_u32 m0, s69, 0x1000\n" \
"v_mfma_f32_16x16x32_bf16 v[140:143], a[0:3], a[248:251], v[140:143]\n" \
"v_mfma_f32_16x16x32_bf16 v[208:211], a[0:3], v[76:79], v[208:211]\n" \
"v_mfma_f32_16x16x32_bf16 v[212:215], a[0:3], v[100:103], v[212:215]\n" \
"ds_read_b64_tr_b16 a[0:1], v35 offset:34944\n" \
"ds_read_b64_tr_b16 a[2:3], v35 offset:37248\n" \
"v_mfma_f32_16x16x32_bf16 v[144:147], a[4:7], a[232:235], v[144:147]\n" \
"buffer_load_dwordx4 v4, s[80:83], s68 offen offset:0 lds\n" \
"v_mfma_f32_16x16x32_bf16 v[148:151], a[4:7], a[248:251], v[148:151]\n" \
"v_mfma_f32_16x16x32_bf16 v[216:219], a[4:7], v[76:79], v[216:219]\n" \
"v_mfma_f32_16x16x32_bf16 v[220:223], a[4:7], v[100:103], v[220:223]\n" \
"ds_read_b64_tr_b16 a[4:5], v35 offset:34976\n" \
"ds_read_b64_tr_b16 a[6:7], v35 offset:37280\n" \
"v_mfma_f32_16x16x32_bf16 v[152:155], a[8:11], a[232:235], v[152:155]\n" \
"v_mfma_f32_16x16x32_bf16 v[156:159], a[8:11], a[248:251], v[156:159]\n" \
"buffer_load_dwordx4 v5, s[80:83], s68 offen offset:1024 lds\n" \
"v_mfma_f32_16x16x32_bf16 v[224:227], a[8:11], v[76:79], v[224:227]\n" \
"v_mfma_f32_16x16x32_bf16 v[228:231], a[8:11], v[100:103], v[228:231]\n" \
"ds_read_b64_tr_b16 a[8:9], v35 offset:35008\n" \
"ds_read_b64_tr_b16 a[10:11], v35 offset:37312\n" \
"v_mfma_f32_16x16x32_bf16 v[160:163], a[12:15], a[232:235], v[160:163]\n" \
"v_mfma_f32_16x16x32_bf16 v[164:167], a[12:15], a[248:251], v[164:167]\n" \
"v_mfma_f32_16x16x32_bf16 v[232:235], a[12:15], v[76:79], v[232:235]\n" \
"buffer_load_dwordx4 v6, s[80:83], s68 offen offset:2048 lds\n" \
"v_mfma_f32_16x16x32_bf16 v[236:239], a[12:15], v[100:103], v[236:239]\n" \
"ds_read_b64_tr_b16 a[12:13], v35 offset:35040\n" \
"ds_read_b64_tr_b16 a[14:15], v35 offset:37344\n" \
"v_mfma_f32_16x16x32_bf16 v[168:171], a[16:19], a[232:235], v[168:171]\n" \
"v_mfma_f32_16x16x32_bf16 v[172:175], a[16:19], a[248:251], v[172:175]\n" \
"v_mfma_f32_16x16x32_bf16 v[240:243], a[16:19], v[76:79], v[240:243]\n" \
"v_mfma_f32_16x16x32_bf16 v[244:247], a[16:19], v[100:103], v[244:247]\n" \
"buffer_load_dwordx4 v7, s[80:83], s68 offen offset:3072 lds\n" \
"v_mfma_f32_16x16x32_bf16 v[176:179], a[20:23], a[232:235], v[176:179]\n" \
"s_add_u32 m0, s69, 0x2000\n" \
"ds_read_b64_tr_b16 a[16:17], v35 offset:35072\n" \
"v_mfma_f32_16x16x32_bf16 v[180:183], a[20:23], a[248:251], v[180:183]\n" \
"ds_read_b64_tr_b16 a[18:19], v35 offset:37376\n" \
"v_readlane_b32 s69, v11, 47\n" \
"v_mfma_f32_16x16x32_bf16 v[248:251], a[20:23], v[76:79], v[248:251]\n" \
"v_mfma_f32_16x16x32_bf16 v[252:255], a[20:23], v[100:103], v[252:255]\n" \
"ds_read_b64_tr_b16 a[20:21], v35 offset:35104\n" \
"ds_read_b64_tr_b16 a[22:23], v35 offset:37408\n" \
"v_mfma_f32_16x16x32_bf16 v[112:115], a[24:27], a[96:99], v[112:115]\n" \
"buffer_load_dwordx4 v8, s[80:83], s68 offen offset:0 lds\n" \
"v_mfma_f32_16x16x32_bf16 v[116:119], a[24:27], a[104:107], v[116:119]\n" \
"s_add_u32 m0, s94, 0x0\n" \
"v_readlane_b32 s68, v11, 46\n" \
"v_mfma_f32_16x16x32_bf16 v[184:187], a[24:27], a[112:115], v[184:187]\n" \
"v_mfma_f32_16x16x32_bf16 v[188:191], a[24:27], a[120:123], v[188:191]\n" \
"ds_read_b64_tr_b16 a[24:25], v35 offset:35136\n" \
"s_waitcnt lgkmcnt(7)\n" \
"v_mfma_f32_16x16x32_bf16 v[120:123], a[28:31], a[96:99], v[120:123]\n" \
"ds_read_b64_tr_b16 a[26:27], v35 offset:37440\n" \
"buffer_load_dword v47, v38, s[56:59], s73 offen sc1\n" \
"v_mfma_f32_16x16x32_bf16 v[124:127], a[28:31], a[104:107], v[124:127]\n" \
"v_mfma_f32_16x16x32_bf16 v[192:195], a[28:31], a[112:115], v[192:195]\n" \
"v_mfma_f32_16x16x32_bf16 v[196:199], a[28:31], a[120:123], v[196:199]\n" \
"ds_read_b64_tr_b16 a[28:29], v35 offset:35168\n" \
"ds_read_b64_tr_b16 a[30:31], v35 offset:37472\n" \
"v_mfma_f32_16x16x32_bf16 v[128:131], a[192:195], a[96:99], v[128:131]\n" \
"v_mfma_f32_16x16x32_bf16 v[132:135], a[192:195], a[104:107], v[132:135]\n" \
"buffer_load_dwordx4 v0, s[80:83], s93 offen offset:0 lds\n" \
"v_mfma_f32_16x16x32_bf16 v[200:203], a[192:195], a[112:115], v[200:203]\n" \
"v_mfma_f32_16x16x32_bf16 v[204:207], a[192:195], a[120:123], v[204:207]\n" \
"ds_read_b64_tr_b16 a[192:193], v35 offset:35200\n" \
"ds_read_b64_tr_b16 a[194:195], v35 offset:37504\n" \
"v_mfma_f32_16x16x32_bf16 v[136:139], a[196:199], a[96:99], v[136:139]\n" \
"v_mfma_f32_16x16x32_bf16 v[140:143], a[196:199], a[104:107], v[140:143]\n" \
"v_mfma_f32_16x16x32_bf16 v[208:211], a[196:199], a[112:115], v[208:211]\n" \
"buffer_load_dwordx4 v1, s[80:83], s93 offen offset:1024 lds\n" \
"v_mfma_f32_16x16x32_bf16 v[212:215], a[196:199], a[120:123], v[212:215]\n" \
"v_mfma_f32_16x16x32_bf16 v[144:147], a[200:203], a[96:99], v[144:147]\n" \
"v_mfma_f32_16x16x32_bf16 v[148:151], a[200:203], a[104:107], v[148:151]\n" \
"v_mfma_f32_16x16x32_bf16 v[216:219], a[200:203], a[112:115], v[216:219]\n" \
"v_mfma_f32_16x16x32_bf16 v[220:223], a[200:203], a[120:123], v[220:223]\n" \
"buffer_load_dwordx4 v2, s[80:83], s93 offen offset:2048 lds\n" \
"v_mfma_f32_16x16x32_bf16 v[152:155], a[204:207], a[96:99], v[152:155]\n" \
"v_mfma_f32_16x16x32_bf16 v[156:159], a[204:207], a[104:107], v[156:159]\n" \
"v_mfma_f32_16x16x32_bf16 v[224:227], a[204:207], a[112:115], v[224:227]\n" \
"v_mfma_f32_16x16x32_bf16 v[228:231], a[204:207], a[120:123], v[228:231]\n" \
"v_mfma_f32_16x16x32_bf16 v[160:163], a[208:211], a[96:99], v[160:163]\n" \
"buffer_load_dwordx4 v3, s[80:83], s93 offen offset:3072 lds\n" \
"v_mfma_f32_16x16x32_bf16 v[164:167], a[208:211], a[104:107], v[164:167]\n" \
"s_add_u32 m0, s94, 0x1000\n" \
"v_mfma_f32_16x16x32_bf16 v[232:235], a[208:211], a[112:115], v[232:235]\n" \
"v_mfma_f32_16x16x32_bf16 v[236:239], a[208:211], a[120:123], v[236:239]\n" \
"v_mfma_f32_16x16x32_bf16 v[168:171], a[212:215], a[96:99], v[168:171]\n" \
"v_mfma_f32_16x16x32_bf16 v[172:175], a[212:215], a[104:107], v[172:175]\n" \
"buffer_load_dwordx4 v4, s[80:83], s93 offen offset:0 lds\n" \
"v_mfma_f32_16x16x32_bf16 v[240:243], a[212:215], a[112:115], v[240:243]\n" \
"v_mfma_f32_16x16x32_bf16 v[244:247], a[212:215], a[120:123], v[244:247]\n" \
"v_mfma_f32_16x16x32_bf16 v[176:179], a[216:219], a[96:99], v[176:179]\n" \
"v_mfma_f32_16x16x32_bf16 v[180:183], a[216:219], a[104:107], v[180:183]\n" \
"v_mfma_f32_16x16x32_bf16 v[248:251], a[216:219], a[112:115], v[248:251]\n" \
"buffer_load_dwordx4 v5, s[80:83], s93 offen offset:1024 lds\n" \
"v_mfma_f32_16x16x32_bf16 v[252:255], a[216:219], a[120:123], v[252:255]\n" \
"v_mfma_f32_16x16x32_bf16 v[112:115], a[0:3], a[100:103], v[112:115]\n" \
"v_mfma_f32_16x16x32_bf16 v[116:119], a[0:3], a[108:111], v[116:119]\n" \
"v_mfma_f32_16x16x32_bf16 v[184:187], a[0:3], a[116:119], v[184:187]\n" \
"v_mfma_f32_16x16x32_bf16 v[188:191], a[0:3], a[124:127], v[188:191]\n" \
"buffer_load_dwordx4 v6, s[80:83], s93 offen offset:2048 lds\n" \
"v_mfma_f32_16x16x32_bf16 v[120:123], a[4:7], a[100:103], v[120:123]\n" \
"v_mfma_f32_16x16x32_bf16 v[124:127], a[4:7], a[108:111], v[124:127]\n" \
"v_mfma_f32_16x16x32_bf16 v[192:195], a[4:7], a[116:119], v[192:195]\n" \
"v_mfma_f32_16x16x32_bf16 v[196:199], a[4:7], a[124:127], v[196:199]\n" \
"v_mfma_f32_16x16x32_bf16 v[128:131], a[8:11], a[100:103], v[128:131]\n" \
"buffer_load_dwordx4 v7, s[80:83], s93 offen offset:3072 lds\n" \
"s_add_u32 m0, s94, 0x2000\n" \
"v_mfma_f32_16x16x32_bf16 v[132:135], a[8:11], a[108:111], v[132:135]\n" \
"v_readlane_b32 s94, v11, 45\n" \
"v_mfma_f32_16x16x32_bf16 v[200:203], a[8:11], a[116:119], v[200:203]\n" \
"v_mfma_f32_16x16x32_bf16 v[204:207], a[8:11], a[124:127], v[204:207]\n" \
"s_waitcnt lgkmcnt(0)\n" \
"v_mfma_f32_16x16x32_bf16 v[136:139], a[12:15], a[100:103], v[136:139]\n" \
"buffer_load_dwordx4 v8, s[80:83], s93 offen offset:0 lds\n" \
"s_mov_b32 m0, s96\n" \
"v_mfma_f32_16x16x32_bf16 v[140:143], a[12:15], a[108:111], v[140:143]\n" \
"v_readlane_b32 s96, v11, 47\n" \
"v_readlane_b32 s93, v11, 6\n" \
"v_mfma_f32_16x16x32_bf16 v[208:211], a[12:15], a[116:119], v[208:211]\n" \
"v_mfma_f32_16x16x32_bf16 v[212:215], a[12:15], a[124:127], v[212:215]\n" \
"s_waitcnt vmcnt(23)\n" \
"s_barrier\n" \
"buffer_load_dwordx4 v9, s[84:87], 0 offen lds\n" \
"v_mfma_f32_16x16x32_bf16 v[144:147], a[16:19], a[100:103], v[144:147]\n" \
"ds_read_b64_tr_b16 a[196:197], v33 offset:0\n" \
"ds_read_b64_tr_b16 a[198:199], v33 offset:4608\n" \
"v_mfma_f32_16x16x32_bf16 v[148:151], a[16:19], a[108:111], v[148:151]\n" \
"ds_read_b64_tr_b16 a[200:201], v33 offset:32\n" \
"ds_read_b64_tr_b16 a[202:203], v33 offset:4640\n" \
"v_mfma_f32_16x16x32_bf16 v[216:219], a[16:19], a[116:119], v[216:219]\n" \
"ds_read_b64_tr_b16 a[204:205], v33 offset:64\n" \
"ds_read_b64_tr_b16 a[206:207], v33 offset:4672\n" \
"v_mfma_f32_16x16x32_bf16 v[220:223], a[16:19], a[124:127], v[220:223]\n" \
"ds_read_b64_tr_b16 a[208:209], v33 offset:96\n" \
"ds_read_b64_tr_b16 a[210:211], v33 offset:4704\n" \
"buffer_load_dwordx4 a[76:79], v20, s[56:59], s67 offen offset:3072 sc1\n" \
"v_mfma_f32_16x16x32_bf16 v[152:155], a[20:23], a[100:103], v[152:155]\n" \
"ds_read_b64_tr_b16 a[212:213], v33 offset:128\n" \
"ds_read_b64_tr_b16 a[214:215], v33 offset:4736\n" \
"v_mfma_f32_16x16x32_bf16 v[156:159], a[20:23], a[108:111], v[156:159]\n" \
"ds_read_b64_tr_b16 a[216:217], v33 offset:160\n" \
"ds_read_b64_tr_b16 a[218:219], v33 offset:4768\n" \
"v_mfma_f32_16x16x32_bf16 v[224:227], a[20:23], a[116:119], v[224:227]\n" \
"ds_read_b64_tr_b16 a[0:1], v33 offset:192\n" \
"ds_read_b64_tr_b16 a[2:3], v33 offset:4800\n" \
"v_mfma_f32_16x16x32_bf16 v[228:231], a[20:23], a[124:127], v[228:231]\n" \
"ds_read_b64_tr_b16 a[4:5], v33 offset:224\n" \
"ds_read_b64_tr_b16 a[6:7], v33 offset:4832\n" \
"buffer_load_dwordx4 a[92:95], v32, s[56:59], s67 offen offset:3072 sc1\n" \
"v_mfma_f32_16x16x32_bf16 v[160:163], a[24:27], a[100:103], v[160:163]\n" \
"v_readlane_b32 s67, v11, 45\n" \
"ds_read_b64_tr_b16 a[8:9], v33 offset:256\n" \
"v_mfma_f32_16x16x32_bf16 v[164:167], a[24:27], a[108:111], v[164:167]\n" \
"ds_read_b64_tr_b16 a[10:11], v33 offset:4864\n" \
"ds_read_b64_tr_b16 a[12:13], v33 offset:9216\n" \
"v_mfma_f32_16x16x32_bf16 v[232:235], a[24:27], a[116:119], v[232:235]\n" \
"ds_read_b64_tr_b16 a[14:15], v33 offset:13824\n" \
"ds_read_b64_tr_b16 a[16:17], v33 offset:9248\n" \
"v_mfma_f32_16x16x32_bf16 v[236:239], a[24:27], a[124:127], v[236:239]\n" \
"ds_read_b64_tr_b16 a[18:19], v33 offset:13856\n" \
"ds_read_b64_tr_b16 a[20:21], v33 offset:9280\n" \
"buffer_load_dwordx4 a[128:131], v20, s[56:59], s70 offen offset:0 sc1\n" \
"v_mfma_f32_16x16x32_bf16 v[168:171], a[28:31], a[100:103], v[168:171]\n" \
"ds_read_b64_tr_b16 a[22:23], v33 offset:13888\n" \
"ds_read_b64_tr_b16 a[24:25], v33 offset:9312\n" \
"v_mfma_f32_16x16x32_bf16 v[172:175], a[28:31], a[108:111], v[172:175]\n" \
"ds_read_b64_tr_b16 a[26:27], v33 offset:13920\n" \
"s_waitcnt vmcnt(13)\n" \
"v_cmp_ne_u32_e32 vcc, s62, v47\n" \
"v_mfma_f32_16x16x32_bf16 v[240:243], a[28:31], a[116:119], v[240:243]\n" \
"v_mfma_f32_16x16x32_bf16 v[244:247], a[28:31], a[124:127], v[244:247]\n" \
"buffer_load_dwordx4 a[144:147], v32, s[56:59], s70 offen offset:0 sc1\n" \
"v_mfma_f32_16x16x32_bf16 v[176:179], a[192:195], a[100:103], v[176:179]\n" \
"ds_read_b64_tr_b16 a[28:29], v33 offset:9344\n" \
"ds_read_b64_tr_b16 a[30:31], v33 offset:13952\n" \
"v_mfma_f32_16x16x32_bf16 v[180:183], a[192:195], a[108:111], v[180:183]\n" \
"s_cbranch_vccnz L_sb_%=\n" \
"L_rb_%=:\n" \
"v_readlane_b32 s73, v11, 51\n" \
"v_mfma_f32_16x16x32_bf16 v[248:251], a[192:195], a[116:119], v[248:251]\n" \
"v_cmp_eq_u32_e32 vcc, 0, v44\n" \
"v_mfma_f32_16x16x32_bf16 v[252:255], a[192:195], a[124:127], v[252:255]\n" \
"ds_read_b64_tr_b16 a[192:193], v33 offset:9376\n" \
"buffer_load_dwordx4 a[132:135], v20, s[56:59], s70 offen offset:1024 sc1\n" \
"s_waitcnt lgkmcnt(14)\n" \
"v_mfma_f32_16x16x32_bf16 v[112:115], a[196:199], a[64:67], v[112:115]\n" \
"ds_read_b64_tr_b16 a[194:195], v33 offset:13984\n" \
"v_cndmask_b32_e32 v50, 0, v45, vcc\n" \
"v_mfma_f32_16x16x32_bf16 v[116:119], a[196:199], a[80:83], v[116:119]\n" \
"ds_read_b64_tr_b16 a[196:197], v33 offset:9408\n" \
"ds_read_b64_tr_b16 a[198:199], v33 offset:14016\n" \
"v_mfma_f32_16x16x32_bf16 v[120:123], a[200:203], a[64:67], v[120:123]\n" \
"v_cmp_eq_u32_e32 vcc, 1, v44\n" \
"v_and_b32_e32 v44, 0x3c0, v20\n" \
"v_mfma_f32_16x16x32_bf16 v[124:127], a[200:203], a[80:83], v[124:127]\n" \
"buffer_load_dwordx4 a[148:151], v32, s[56:59], s70 offen offset:1024 sc1\n" \
"v_mfma_f32_16x16x32_bf16 v[128:131], a[204:207], a[64:67], v[128:131]\n" \
"ds_read_b64_tr_b16 a[200:201], v33 offset:9440\n" \
"ds_read_b64_tr_b16 a[202:203], v33 offset:14048\n" \
"v_mfma_f32_16x16x32_bf16 v[132:135], a[204:207], a[80:83], v[132:135]\n" \
"ds_read_b64_tr_b16 a[204:205], v33 offset:9472\n" \
"ds_read_b64_tr_b16 a[206:207], v33 offset:14080\n" \
"v_mfma_f32_16x16x32_bf16 v[136:139], a[208:211], a[64:67], v[136:139]\n" \
"v_cndmask_b32_e32 v51, 0, v45, vcc\n" \
"v_bfe_u32 v45, v20, 1, 5\n" \
"v_mfma_f32_16x16x32_bf16 v[140:143], a[208:211], a[80:83], v[140:143]\n" \
"ds_read_b64_tr_b16 a[208:209], v33 offset:18432\n" \
"ds_read_b64_tr_b16 a[210:211], v33 offset:23040\n" \
"buffer_load_dwordx4 a[136:139], v20, s[56:59], s70 offen offset:2048 sc1\n" \
"v_mfma_f32_16x16x32_bf16 v[144:147], a[212:215], a[64:67], v[144:147]\n" \
"v_and_b32_e32 v45, 24, v45\n" \
"v_add_u32_e32 v45, 0xf000, v45\n" \
"v_mfma_f32_16x16x32_bf16 v[148:151], a[212:215], a[80:83], v[148:151]\n" \
"ds_read_b64_tr_b16 a[212:213], v33 offset:18464\n" \
"ds_read_b64_tr_b16 a[214:215], v33 offset:23072\n" \
"v_mfma_f32_16x16x32_bf16 v[152:155], a[216:219], a[64:67], v[152:155]\n" \
"v_add_u32_e32 v36, v44, v45\n" \
".fill (L_ab + 24576 - .) / 4, 4, 0xBF800000\n" \
"L_ap6:\n" \
"s_cbranch_execz L_ap0\n" \
"v_mfma_f32_16x16x32_bf16 v[156:159], a[216:219], a[80:83], v[156:159]\n" \
"ds_read_b64_tr_b16 a[216:217], v33 offset:18496\n" \
"ds_read_b64_tr_b16 a[218:219], v33 offset:23104\n" \
"buffer_load_dwordx4 a[152:155], v32, s[56:59], s70 offen offset:2048 sc1\n" \
"v_mfma_f32_16x16x32_bf16 v[160:163], a[0:3], a[64:67], v[160:163]\n" \
"v_mfma_f32_16x16x32_bf16 v[164:167], a[0:3], a[80:83], v[164:167]\n" \
"ds_read_b64_tr_b16 a[0:1], v33 offset:18528\n" \
"s_waitcnt lgkmcnt(13)\n" \
"v_mfma_f32_16x16x32_bf16 v[168:171], a[4:7], a[64:67], v[168:171]\n" \
"ds_read_b64_tr_b16 a[2:3], v33 offset:23136\n" \
"v_mfma_f32_16x16x32_bf16 v[172:175], a[4:7], a[80:83], v[172:175]\n" \
"ds_read_b64_tr_b16 a[4:5], v33 offset:18560\n" \
"ds_read_b64_tr_b16 a[6:7], v33 offset:23168\n" \
"buffer_load_dwordx4 a[140:143], v20, s[56:59], s70 offen offset:3072 sc1\n" \
"v_mfma_f32_16x16x32_bf16 v[176:179], a[8:11], a[64:67], v[176:179]\n" \
"v_mfma_f32_16x16x32_bf16 v[180:183], a[8:11], a[80:83], v[180:183]\n" \
"ds_read_b64_tr_b16 a[8:9], v33 offset:18592\n" \
"ds_read_b64_tr_b16 a[10:11], v33 offset:23200\n" \
"v_mfma_f32_16x16x32_bf16 v[112:115], a[12:15], a[68:71], v[112:115]\n" \
"v_mfma_f32_16x16x32_bf16 v[116:119], a[12:15], a[84:87], v[116:119]\n" \
"ds_read_b64_tr_b16 a[12:13], v33 offset:18624\n" \
"ds_read_b64_tr_b16 a[14:15], v33 offset:23232\n" \
"buffer_load_dwordx4 a[156:159], v32, s[56:59], s70 offen offset:3072 sc1\n" \
"v_mfma_f32_16x16x32_bf16 v[120:123], a[16:19], a[68:71], v[120:123]\n" \
"v_readlane_b32 s70, v11, 48\n" \
"v_mfma_f32_16x16x32_bf16 v[124:127], a[16:19], a[84:87], v[124:127]\n" \
"ds_read_b64_tr_b16 a[16:17], v33 offset:18656\n" \
"ds_read_b64_tr_b16 a[18:19], v33 offset:23264\n" \
"v_mfma_f32_16x16x32_bf16 v[128:131], a[20:23], a[68:71], v[128:131]\n" \
"v_mfma_f32_16x16x32_bf16 v[132:135], a[20:23], a[84:87], v[132:135]\n" \
"ds_read_b64_tr_b16 a[20:21], v33 offset:18688\n" \
"ds_read_b64_tr_b16 a[22:23], v33 offset:23296\n" \
"buffer_load_dwordx4 a[32:35], v20, s[56:59], s71 offen offset:0 sc1\n" \
"v_mfma_f32_16x16x32_bf16 v[136:139], a[24:27], a[68:71], v[136:139]\n" \
"v_mfma_f32_16x16x32_bf16 v[140:143], a[24:27], a[84:87], v[140:143]\n" \
"ds_read_b64_tr_b16 a[24:25], v33 offset:27648\n" \
"ds_read_b64_tr_b16 a[26:27], v33 offset:32256\n" \
"v_mfma_f32_16x16x32_bf16 v[144:147], a[28:31], a[68:71], v[144:147]\n" \
"v_mfma_f32_16x16x32_bf16 v[148:151], a[28:31], a[84:87], v[148:151]\n" \
"ds_read_b64_tr_b16 a[28:29], v33 offset:27680\n" \
"ds_read_b64_tr_b16 a[30:31], v33 offset:32288\n" \
"buffer_load_dwordx4 a[48:51], v32, s[56:59], s71 offen offset:0 sc1\n" \
"v_mfma_f32_16x16x32_bf16 v[152:155], a[192:195], a[68:71], v[152:155]\n" \
"v_mfma_f32_16x16x32_bf16 v[156:159], a[192:195], a[84:87], v[156:159]\n" \
"ds_read_b64_tr_b16 a[192:193], v33 offset:27712\n" \
"s_waitcnt lgkmcnt(13)\n" \
"v_mfma_f32_16x16x32_bf16 v[160:163], a[196:199], a[68:71], v[160:163]\n" \
"ds_read_b64_tr_b16 a[194:195], v33 offset:32320\n" \
"v_mfma_f32_16x16x32_bf16 v[164:167], a[196:199], a[84:87], v[164:167]\n" \
"ds_read_b64_tr_b16 a[196:197], v33 offset:27744\n" \
"ds_read_b64_tr_b16 a[198:199], v33 offset:32352\n" \
"buffer_load_dwordx4 a[36:39], v20, s[56:59], s71 offen offset:1024 sc1\n" \
"v_mfma_f32_16x16x32_bf16 v[168:171], a[200:203], a[68:71], v[168:171]\n" \
"v_mfma_f32_16x16x32_bf16 v[172:175], a[200:203], a[84:87], v[172:175]\n" \
"ds_read_b64_tr_b16 a[200:201], v33 offset:27776\n" \
"ds_read_b64_tr_b16 a[202:203], v33 offset:32384\n" \
"v_mfma_f32_16x16x32_bf16 v[176:179], a[204:207], a[68:71], v[176:179]\n" \
"v_mfma_f32_16x16x32_bf16 v[180:183], a[204:207], a[84:87], v[180:183]\n" \
"ds_read_b64_tr_b16 a[204:205], v33 offset:27808\n" \
"ds_read_b64_tr_b16 a[206:207], v33 offset:32416\n" \
"buffer_load_dwordx4 a[52:55], v32, s[56:59], s71 offen offset:1024 sc1\n" \
"v_mfma_f32_16x16x32_bf16 v[112:115], a[208:211], a[72:75], v[112:115]\n" \
"v_mfma_f32_16x16x32_bf16 v[116:119], a[208:211], a[88:91], v[116:119]\n" \
"ds_read_b64_tr_b16 a[208:209], v33 offset:27840\n" \
"ds_read_b64_tr_b16 a[210:211], v33 offset:32448\n" \
"v_mfma_f32_16x16x32_bf16 v[120:123], a[212:215], a[72:75], v[120:123]\n" \
"v_mfma_f32_16x16x32_bf16 v[124:127], a[212:215], a[88:91], v[124:127]\n" \
"ds_read_b64_tr_b16 a[212:213], v33 offset:27872\n" \
"ds_read_b64_tr_b16 a[214:215], v33 offset:32480\n" \
"buffer_load_dwordx4 a[40:43], v20, s[56:59], s71 offen offset:2048 sc1\n" \
"v_mfma_f32_16x16x32_bf16 v[128:131], a[216:219], a[72:75], v[128:131]\n" \
"v_mfma_f32_16x16x32_bf16 v[132:135], a[216:219], a[88:91], v[132:135]\n" \
"ds_read_b64_tr_b16 a[216:217], v33 offset:27904\n" \
"ds_read_b64_tr_b16 a[218:219], v33 offset:32512\n" \
"v_mfma_f32_16x16x32_bf16 v[136:139], a[0:3], a[72:75], v[136:139]\n" \
"v_mfma_f32_16x16x32_bf16 v[140:143], a[0:3], a[88:91], v[140:143]\n" \
"v_mfma_f32_16x16x32_bf16 v[144:147], a[4:7], a[72:75], v[144:147]\n" \
"buffer_load_dwordx4 a[56:59], v32, s[56:59], s71 offen offset:2048 sc1\n" \
"v_mfma_f32_16x16x32_bf16 v[148:151], a[4:7], a[88:91], v[148:151]\n" \
"s_waitcnt lgkmcnt(10)\n" \
"v_mfma_f32_16x16x32_bf16 v[152:155], a[8:11], a[72:75], v[152:155]\n" \
"v_mfma_f32_16x16x32_bf16 v[156:159], a[8:11], a[88:91], v[156:159]\n" \
"v_mfma_f32_16x16x32_bf16 v[160:163], a[12:15], a[72:75], v[160:163]\n" \
"v_mfma_f32_16x16x32_bf16 v[164:167], a[12:15], a[88:91], v[164:167]\n" \
"buffer_load_dwordx4 a[44:47], v20, s[56:59], s71 offen offset:3072 sc1\n" \
"v_mfma_f32_16x16x32_bf16 v[168:171], a[16:19], a[72:75], v[168:171]\n" \
"v_mfma_f32_16x16x32_bf16 v[172:175], a[16:19], a[88:91], v[172:175]\n" \
"v_mfma_f32_16x16x32_bf16 v[176:179], a[20:23], a[72:75], v[176:179]\n" \
"v_mfma_f32_16x16x32_bf16 v[180:183], a[20:23], a[88:91], v[180:183]\n" \
"buffer_load_dwordx4 a[60:63], v32, s[56:59], s71 offen offset:3072 sc1\n" \
"v_readlane_b32 s71, v11, 49\n" \
"buffer_load_dwordx4 a[96:99], v20, s[56:59], s66 offen offset:0 sc1\n" \
"buffer_load_dwordx4 a[112:115], v32, s[56:59], s66 offen offset:0 sc1\n" \
"s_waitcnt vmcnt(20)\n" \
"s_barrier\n" \
"ds_read_b64_tr_b16 a[0:1], v33 offset:36864\n" \
"ds_read_b64_tr_b16 a[2:3], v33 offset:41472\n" \
"ds_read_b64_tr_b16 a[4:5], v33 offset:46080\n" \
"ds_read_b64_tr_b16 a[6:7], v33 offset:50688\n" \
"ds_read_b64_tr_b16 a[8:9], v33 offset:55296\n" \
"ds_read_b64_tr_b16 a[10:11], v33 offset:59904\n" \
"ds_read_b64_tr_b16 a[12:13], v10 offset:0\n" \
"ds_read_b64_tr_b16 a[14:15], v10 offset:4608\n" \
"ds_read_b64_tr_b16 a[16:17], v34 offset:0\n" \
"ds_read_b64_tr_b16 a[18:19], v34 offset:4608\n" \
"ds_read_b64_tr_b16 a[20:21], v34 offset:9216\n" \
"ds_read_b64_tr_b16 a[22:23], v34 offset:13824\n" \
"buffer_load_dwordx4 a[100:103], v20, s[56:59], s66 offen offset:1024 sc1\n" \
"s_waitcnt vmcnt(20)\n" \
"v_mfma_f32_16x16x32_bf16 v[112:115], a[24:27], a[76:79], v[112:115]\n" \
"s_waitcnt vmcnt(19)\n" \
"v_mfma_f32_16x16x32_bf16 v[116:119], a[24:27], a[92:95], v[116:119]\n" \
"ds_read_b64_tr_b16 a[24:25], v34 offset:18432\n" \
"ds_read_b64_tr_b16 a[26:27], v34 offset:23040\n" \
"v_mfma_f32_16x16x32_bf16 v[120:123], a[28:31], a[76:79], v[120:123]\n" \
"buffer_load_dwordx4 a[116:119], v32, s[56:59], s66 offen offset:1024 sc1\n" \
"v_mfma_f32_16x16x32_bf16 v[124:127], a[28:31], a[92:95], v[124:127]\n" \
"ds_read_b64_tr_b16 a[28:29], v10 offset:9216\n" \
"ds_read_b64_tr_b16 a[30:31], v10 offset:13824\n" \
"v_mfma_f32_16x16x32_bf16 v[128:131], a[192:195], a[76:79], v[128:131]\n" \
"v_mfma_f32_16x16x32_bf16 v[132:135], a[192:195], a[92:95], v[132:135]\n" \
"ds_read_b64_tr_b16 a[192:193], v33 offset:36896\n" \
"ds_read_b64_tr_b16 a[194:195], v33 offset:41504\n" \
"v_mfma_f32_16x16x32_bf16 v[136:139], a[196:199], a[76:79], v[136:139]\n" \
"v_mfma_f32_16x16x32_bf16 v[140:143], a[196:199], a[92:95], v[140:143]\n" \
"ds_read_b64_tr_b16 a[196:197], v33 offset:46112\n" \
"s_waitcnt lgkmcnt(15)\n" \
"v_mfma_f32_16x16x32_bf16 v[144:147], a[200:203], a[76:79], v[144:147]\n" \
"buffer_load_dwordx4 a[104:107], v20, s[56:59], s66 offen offset:2048 sc1\n" \
"v_mfma_f32_16x16x32_bf16 v[148:151], a[200:203], a[92:95], v[148:151]\n" \
"ds_read_b64_tr_b16 a[198:199], v33 offset:50720\n" \
"ds_read_b64_tr_b16 a[200:201], v33 offset:55328\n" \
"v_mfma_f32_16x16x32_bf16 v[152:155], a[204:207], a[76:79], v[152:155]\n" \
"ds_read_b64_tr_b16 a[202:203], v33 offset:59936\n" \
"v_mfma_f32_16x16x32_bf16 v[156:159], a[204:207], a[92:95], v[156:159]\n" \
"ds_read_b64_tr_b16 a[204:205], v10 offset:32\n" \
"ds_read_b64_tr_b16 a[206:207], v10 offset:4640\n" \
"v_mfma_f32_16x16x32_bf16 v[160:163], a[208:211], a[76:79], v[160:163]\n" \
"v_mfma_f32_16x16x32_bf16 v[164:167], a[208:211], a[92:95], v[164:167]\n" \
"ds_read_b64_tr_b16 a[208:209], v34 offset:32\n" \
"ds_read_b64_tr_b16 a[210:211], v34 offset:4640\n" \
"v_mfma_f32_16x16x32_bf16 v[168:171], a[212:215], a[76:79], v[168:171]\n" \
"buffer_load_dwordx4 a[120:123], v32, s[56:59], s66 offen offset:2048 sc1\n" \
"v_mfma_f32_16x16x32_bf16 v[172:175], a[212:215], a[92:95], v[172:175]\n" \
"ds_read_b64_tr_b16 a[212:213], v34 offset:9248\n" \
"ds_read_b64_tr_b16 a[214:215], v34 offset:13856\n" \
"v_mfma_f32_16x16x32_bf16 v[176:179], a[216:219], a[76:79], v[176:179]\n" \
"v_mfma_f32_16x16x32_bf16 v[180:183], a[216:219], a[92:95], v[180:183]\n" \
"ds_read_b64_tr_b16 a[216:217], v34 offset:18464\n" \
"s_waitcnt vmcnt(21)\n" \
"v_mfma_f32_16x16x32_bf16 v[112:115], a[0:3], a[128:131], v[112:115]\n" \
"ds_read_b64_tr_b16 a[218:219], v34 offset:23072\n" \
"s_waitcnt vmcnt(20)\n" \
"v_mfma_f32_16x16x32_bf16 v[116:119], a[0:3], a[144:147], v[116:119]\n" \
"ds_read_b64_tr_b16 a[0:1], v10 offset:9248\n" \
"s_waitcnt vmcnt(19)\n" \
"v_mfma_f32_16x16x32_bf16 v[112:115], a[4:7], a[132:135], v[112:115]\n" \
"ds_read_b64_tr_b16 a[2:3], v10 offset:13856\n" \
"s_waitcnt vmcnt(18)\n" \
"v_mfma_f32_16x16x32_bf16 v[116:119], a[4:7], a[148:151], v[116:119]\n" \
"ds_read_b64_tr_b16 a[4:5], v33 offset:36928\n" \
"ds_read_b64_tr_b16 a[6:7], v33 offset:41536\n" \
"buffer_load_dwordx4 a[108:111], v20, s[56:59], s66 offen offset:3072 sc1\n" \
"s_waitcnt vmcnt(18) lgkmcnt(15)\n" \
"v_mfma_f32_16x16x32_bf16 v[112:115], a[8:11], a[136:139], v[112:115]\n" \
"buffer_load_dwordx4 a[124:127], v32, s[56:59], s66 offen offset:3072 sc1\n" \
"s_waitcnt vmcnt(18)\n" \
"v_mfma_f32_16x16x32_bf16 v[116:119], a[8:11], a[152:155], v[116:119]\n" \
"ds_read_b64_tr_b16 a[8:9], v33 offset:46144\n" \
"ds_read_b64_tr_b16 a[10:11], v33 offset:50752\n" \
"buffer_load_dwordx4 a[160:163], v20, s[56:59], s92 offen offset:0 sc1\n" \
"s_waitcnt vmcnt(18)\n" \
"v_mfma_f32_16x16x32_bf16 v[112:115], a[12:15], a[140:143], v[112:115]\n" \
"buffer_load_dwordx4 a[176:179], v32, s[56:59], s92 offen offset:0 sc1\n" \
"s_waitcnt vmcnt(18)\n" \
"v_mfma_f32_16x16x32_bf16 v[116:119], a[12:15], a[156:159], v[116:119]\n" \
"ds_read_b64_tr_b16 a[12:13], v33 offset:55360\n" \
"ds_read_b64_tr_b16 a[14:15], v33 offset:59968\n" \
"buffer_load_dwordx4 a[164:167], v20, s[56:59], s92 offen offset:1024 sc1\n" \
"s_waitcnt vmcnt(18)\n" \
"v_mfma_f32_16x16x32_bf16 v[112:115], a[16:19], a[32:35], v[112:115]\n" \
"buffer_load_dwordx4 a[180:183], v32, s[56:59], s92 offen offset:1024 sc1\n" \
"s_waitcnt vmcnt(18)\n" \
"v_mfma_f32_16x16x32_bf16 v[116:119], a[16:19], a[48:51], v[116:119]\n" \
"ds_read_b64_tr_b16 a[16:17], v10 offset:64\n" \
"ds_read_b64_tr_b16 a[18:19], v10 offset:4672\n" \
"buffer_load_dwordx4 a[168:171], v20, s[56:59], s92 offen offset:2048 sc1\n" \
"s_waitcnt vmcnt(18)\n" \
"v_mfma_f32_16x16x32_bf16 v[112:115], a[20:23], a[36:39], v[112:115]\n" \
"buffer_load_dwordx4 a[184:187], v32, s[56:59], s92 offen offset:2048 sc1\n" \
"s_waitcnt vmcnt(18)\n" \
"v_mfma_f32_16x16x32_bf16 v[116:119], a[20:23], a[52:55], v[116:119]\n" \
"ds_read_b64_tr_b16 a[20:21], v34 offset:64\n" \
"ds_read_b64_tr_b16 a[22:23], v34 offset:4672\n" \
"buffer_load_dwordx4 a[172:175], v20, s[56:59], s92 offen offset:3072 sc1\n" \
"s_waitcnt vmcnt(18)\n" \
"v_mfma_f32_16x16x32_bf16 v[112:115], a[24:27], a[40:43], v[112:115]\n" \
"buffer_load_dwordx4 a[188:191], v32, s[56:59], s92 offen offset:3072 sc1\n" \
"v_readlane_b32 s92, v11, 52\n" \
"s_waitcnt vmcnt(18)\n" \
"v_mfma_f32_16x16x32_bf16 v[116:119], a[24:27], a[56:59], v[116:119]\n" \
"ds_read_b64_tr_b16 a[24:25], v34 offset:9280\n" \
"ds_read_b64_tr_b16 a[26:27], v34 offset:13888\n" \
"buffer_load_dwordx4 a[220:223], v20, s[56:59], s95 offen offset:0 sc1\n" \
"s_waitcnt vmcnt(18)\n" \
"v_mfma_f32_16x16x32_bf16 v[112:115], a[28:31], a[44:47], v[112:115]\n" \
"buffer_load_dwordx4 a[236:239], v32, s[56:59], s95 offen offset:0 sc1\n" \
"s_waitcnt vmcnt(18)\n" \
"v_mfma_f32_16x16x32_bf16 v[116:119], a[28:31], a[60:63], v[116:119]\n" \
"ds_read_b64_tr_b16 a[28:29], v34 offset:18496\n" \
"ds_read_b64_tr_b16 a[30:31], v34 offset:23104\n" \
"v_mfma_f32_16x16x32_bf16 v[120:123], a[192:195], a[128:131], v[120:123]\n" \
"buffer_load_dwordx4 a[224:227], v20, s[56:59], s95 offen offset:1024 sc1\n" \
"v_mfma_f32_16x16x32_bf16 v[124:127], a[192:195], a[144:147], v[124:127]\n" \
"v_cvt_pk_bf16_f32 v112, v112, v113\n" \
"v_cvt_pk_bf16_f32 v113, v114, v115\n" \
"s_waitcnt lgkmcnt(4)\n" \
"v_mfma_f32_16x16x32_bf16 v[120:123], a[196:199], a[132:135], v[120:123]\n" \
"v_cvt_pk_bf16_f32 v114, v116, v117\n" \
"v_cvt_pk_bf16_f32 v115, v118, v119\n" \
"buffer_store_dwordx4 v[112:115], v20, s[56:59], s67 offen offset:0 sc1\n" \
"s_add_u32 s67, s67, 0x3800\n" \
"v_mfma_f32_16x16x32_bf16 v[124:127], a[196:199], a[148:151], v[124:127]\n" \
"ds_read_b64_tr_b16 a[192:193], v10 offset:9280\n" \
"ds_read_b64_tr_b16 a[194:195], v10 offset:13888\n" \
"v_mfma_f32_16x16x32_bf16 v[120:123], a[200:203], a[136:139], v[120:123]\n" \
"ds_read_b64_tr_b16 a[196:197], v33 offset:36960\n" \
"ds_read_b64_tr_b16 a[198:199], v33 offset:41568\n" \
"v_mfma_f32_16x16x32_bf16 v[124:127], a[200:203], a[152:155], v[124:127]\n" \
"ds_read_b64_tr_b16 a[200:201], v33 offset:46176\n" \
"ds_read_b64_tr_b16 a[202:203], v33 offset:50784\n" \
"v_mfma_f32_16x16x32_bf16 v[120:123], a[204:207], a[140:143], v[120:123]\n" \
"buffer_load_dwordx4 a[240:243], v32, s[56:59], s95 offen offset:1024 sc1\n" \
"v_mfma_f32_16x16x32_bf16 v[124:127], a[204:207], a[156:159], v[124:127]\n" \
"ds_read_b64_tr_b16 a[204:205], v33 offset:55392\n" \
"ds_read_b64_tr_b16 a[206:207], v33 offset:60000\n" \
"v_mfma_f32_16x16x32_bf16 v[120:123], a[208:211], a[32:35], v[120:123]\n" \
"v_mfma_f32_16x16x32_bf16 v[124:127], a[208:211], a[48:51], v[124:127]\n" \
"ds_read_b64_tr_b16 a[208:209], v10 offset:96\n" \
"ds_read_b64_tr_b16 a[210:211], v10 offset:4704\n" \
"v_mfma_f32_16x16x32_bf16 v[120:123], a[212:215], a[36:39], v[120:123]\n" \
"v_mfma_f32_16x16x32_bf16 v[124:127], a[212:215], a[52:55], v[124:127]\n" \
"ds_read_b64_tr_b16 a[212:213], v34 offset:96\n" \
"ds_read_b64_tr_b16 a[214:215], v34 offset:4704\n" \
"v_mfma_f32_16x16x32_bf16 v[120:123], a[216:219], a[40:43], v[120:123]\n" \
"buffer_load_dwordx4 a[228:231], v20, s[56:59], s95 offen offset:2048 sc1\n" \
"v_mfma_f32_16x16x32_bf16 v[124:127], a[216:219], a[56:59], v[124:127]\n" \
"ds_read_b64_tr_b16 a[216:217], v34 offset:9312\n" \
"ds_read_b64_tr_b16 a[218:219], v34 offset:13920\n" \
"v_mfma_f32_16x16x32_bf16 v[120:123], a[0:3], a[44:47], v[120:123]\n" \
"v_mfma_f32_16x16x32_bf16 v[124:127], a[0:3], a[60:63], v[124:127]\n" \
"ds_read_b64_tr_b16 a[0:1], v34 offset:18528\n" \
"ds_read_b64_tr_b16 a[2:3], v34 offset:23136\n" \
"v_mfma_f32_16x16x32_bf16 v[128:131], a[4:7], a[128:131], v[128:131]\n" \
"v_mfma_f32_16x16x32_bf16 v[132:135], a[4:7], a[144:147], v[132:135]\n" \
"ds_read_b64_tr_b16 a[4:5], v10 offset:9312\n" \
"ds_read_b64_tr_b16 a[6:7], v10 offset:13920\n" \
"v_mfma_f32_16x16x32_bf16 v[128:131], a[8:11], a[132:135], v[128:131]\n" \
"v_cvt_pk_bf16_f32 v120, v120, v121\n" \
"v_cvt_pk_bf16_f32 v121, v122, v123\n" \
"v_cvt_pk_bf16_f32 v122, v124, v125\n" \
"v_cvt_pk_bf16_f32 v123, v126, v127\n" \
"buffer_store_dwordx4 v[120:123], v20, s[56:59], s68 offen offset:0 sc1\n" \
"s_add_u32 s68, s68, 0x3000\n" \
"v_mfma_f32_16x16x32_bf16 v[132:135], a[8:11], a[148:151], v[132:135]\n" \
"ds_read_b64_tr_b16 a[8:9], v33 offset:36992\n" \
"ds_read_b64_tr_b16 a[10:11], v33 offset:41600\n" \
"v_mfma_f32_16x16x32_bf16 v[128:131], a[12:15], a[136:139], v[128:131]\n" \
"v_mfma_f32_16x16x32_bf16 v[132:135], a[12:15], a[152:155], v[132:135]\n" \
"ds_read_b64_tr_b16 a[12:13], v33 offset:46208\n" \
"ds_read_b64_tr_b16 a[14:15], v33 offset:50816\n" \
"v_mfma_f32_16x16x32_bf16 v[128:131], a[16:19], a[140:143], v[128:131]\n" \
"buffer_load_dwordx4 a[244:247], v32, s[56:59], s95 offen offset:2048 sc1\n" \
"v_mfma_f32_16x16x32_bf16 v[132:135], a[16:19], a[156:159], v[132:135]\n" \
"ds_read_b64_tr_b16 a[16:17], v33 offset:55424\n" \
"ds_read_b64_tr_b16 a[18:19], v33 offset:60032\n" \
"v_mfma_f32_16x16x32_bf16 v[128:131], a[20:23], a[32:35], v[128:131]\n" \
"v_mfma_f32_16x16x32_bf16 v[132:135], a[20:23], a[48:51], v[132:135]\n" \
"ds_read_b64_tr_b16 a[20:21], v10 offset:128\n" \
"s_waitcnt lgkmcnt(13)\n" \
"v_mfma_f32_16x16x32_bf16 v[128:131], a[24:27], a[36:39], v[128:131]\n" \
"ds_read_b64_tr_b16 a[22:23], v10 offset:4736\n" \
"v_mfma_f32_16x16x32_bf16 v[132:135], a[24:27], a[52:55], v[132:135]\n" \
"buffer_load_dwordx4 a[232:235], v20, s[56:59], s95 offen offset:3072 sc1\n" \
"v_mfma_f32_16x16x32_bf16 v[128:131], a[28:31], a[40:43], v[128:131]\n" \
"ds_read_b64_tr_b16 a[24:25], v34 offset:128\n" \
"ds_read_b64_tr_b16 a[26:27], v34 offset:4736\n" \
"v_mfma_f32_16x16x32_bf16 v[132:135], a[28:31], a[56:59], v[132:135]\n" \
"ds_read_b64_tr_b16 a[28:29], v34 offset:9344\n" \
"ds_read_b64_tr_b16 a[30:31], v34 offset:13952\n" \
"v_mfma_f32_16x16x32_bf16 v[128:131], a[192:195], a[44:47], v[128:131]\n" \
"v_mfma_f32_16x16x32_bf16 v[132:135], a[192:195], a[60:63], v[132:135]\n" \
"ds_read_b64_tr_b16 a[192:193], v34 offset:18560\n" \
"ds_read_b64_tr_b16 a[194:195], v34 offset:23168\n" \
"v_mfma_f32_16x16x32_bf16 v[136:139], a[196:199], a[128:131], v[136:139]\n" \
"buffer_load_dwordx4 a[248:251], v32, s[56:59], s95 offen offset:3072 sc1\n" \
"v_mfma_f32_16x16x32_bf16 v[140:143], a[196:199], a[144:147], v[140:143]\n" \
"ds_read_b64_tr_b16 a[196:197], v10 offset:9344\n" \
"ds_read_b64_tr_b16 a[198:199], v10 offset:13952\n" \
"v_cvt_pk_bf16_f32 v128, v128, v129\n" \
"v_cvt_pk_bf16_f32 v129, v130, v131\n" \
"v_cvt_pk_bf16_f32 v130, v132, v133\n" \
"v_cvt_pk_bf16_f32 v131, v134, v135\n" \
"buffer_store_dwordx4 v[128:131], v20, s[56:59], s69 offen offset:0 sc1\n" \
"s_add_u32 s69, s69, 0x2800\n" \
"v_mfma_f32_16x16x32_bf16 v[136:139], a[200:203], a[132:135], v[136:139]\n" \
"v_readlane_b32 s95, v11, 46\n" \
"v_mfma_f32_16x16x32_bf16 v[140:143], a[200:203], a[148:151], v[140:143]\n" \
"ds_read_b64_tr_b16 a[200:201], v33 offset:37024\n" \
"ds_read_b64_tr_b16 a[202:203], v33 offset:41632\n" \
"v_mfma_f32_16x16x32_bf16 v[136:139], a[204:207], a[136:139], v[136:139]\n" \
"v_mfma_f32_16x16x32_bf16 v[140:143], a[204:207], a[152:155], v[140:143]\n" \
"ds_read_b64_tr_b16 a[204:205], v33 offset:46240\n" \
"ds_read_b64_tr_b16 a[206:207], v33 offset:50848\n" \
"v_mfma_f32_16x16x32_bf16 v[136:139], a[208:211], a[140:143], v[136:139]\n" \
"buffer_load_dwordx4 v[64:67], v20, s[56:59], s97 offen offset:0 sc1\n" \
"v_mfma_f32_16x16x32_bf16 v[140:143], a[208:211], a[156:159], v[140:143]\n" \
"ds_read_b64_tr_b16 a[208:209], v33 offset:55456\n" \
"ds_read_b64_tr_b16 a[210:211], v33 offset:60064\n" \
"v_mfma_f32_16x16x32_bf16 v[136:139], a[212:215], a[32:35], v[136:139]\n" \
"v_mfma_f32_16x16x32_bf16 v[140:143], a[212:215], a[48:51], v[140:143]\n" \
"ds_read_b64_tr_b16 a[212:213], v10 offset:160\n" \
"s_waitcnt lgkmcnt(12)\n" \
"v_mfma_f32_16x16x32_bf16 v[136:139], a[216:219], a[36:39], v[136:139]\n" \
"ds_read_b64_tr_b16 a[214:215], v10 offset:4768\n" \
"v_mfma_f32_16x16x32_bf16 v[140:143], a[216:219], a[52:55], v[140:143]\n" \
"ds_read_b64_tr_b16 a[216:217], v34 offset:160\n" \
"ds_read_b64_tr_b16 a[218:219], v34 offset:4768\n" \
"v_mfma_f32_16x16x32_bf16 v[136:139], a[0:3], a[40:43], v[136:139]\n" \
"buffer_load_dwordx4 v[68:71], v20, s[56:59], s97 offen offset:1024 sc1\n" \
"v_mfma_f32_16x16x32_bf16 v[140:143], a[0:3], a[56:59], v[140:143]\n" \
"ds_read_b64_tr_b16 a[0:1], v34 offset:9376\n" \
"ds_read_b64_tr_b16 a[2:3], v34 offset:13984\n" \
"v_mfma_f32_16x16x32_bf16 v[136:139], a[4:7], a[44:47], v[136:139]\n" \
"v_mfma_f32_16x16x32_bf16 v[140:143], a[4:7], a[60:63], v[140:143]\n" \
"ds_read_b64_tr_b16 a[4:5], v34 offset:18592\n" \
"ds_read_b64_tr_b16 a[6:7], v34 offset:23200\n" \
"v_mfma_f32_16x16x32_bf16 v[144:147], a[8:11], a[128:131], v[144:147]\n" \
"v_mfma_f32_16x16x32_bf16 v[148:151], a[8:11], a[144:147], v[148:151]\n" \
"ds_read_b64_tr_b16 a[8:9], v10 offset:9376\n" \
"ds_read_b64_tr_b16 a[10:11], v10 offset:13984\n" \
"v_mfma_f32_16x16x32_bf16 v[144:147], a[12:15], a[132:135], v[144:147]\n" \
"v_cvt_pk_bf16_f32 v136, v136, v137\n" \
"v_cvt_pk_bf16_f32 v137, v138, v139\n" \
"v_cvt_pk_bf16_f32 v138, v140, v141\n" \
"v_cvt_pk_bf16_f32 v139, v142, v143\n" \
"buffer_store_dwordx4 v[136:139], v20, s[56:59], s70 offen offset:0 sc1\n" \
"s_add_u32 s70, s70, 0x2000\n" \
"v_mfma_f32_16x16x32_bf16 v[148:151], a[12:15], a[148:151], v[148:151]\n" \
"ds_read_b64_tr_b16 a[12:13], v33 offset:37056\n" \
"ds_read_b64_tr_b16 a[14:15], v33 offset:41664\n" \
"v_mfma_f32_16x16x32_bf16 v[144:147], a[16:19], a[136:139], v[144:147]\n" \
"v_mfma_f32_16x16x32_bf16 v[148:151], a[16:19], a[152:155], v[148:151]\n" \
"ds_read_b64_tr_b16 a[16:17], v33 offset:46272\n" \
"ds_read_b64_tr_b16 a[18:19], v33 offset:50880\n" \
"v_mfma_f32_16x16x32_bf16 v[144:147], a[20:23], a[140:143], v[144:147]\n" \
"buffer_load_dwordx4 v[72:75], v32, s[56:59], s97 offen offset:0 sc1\n" \
"v_mfma_f32_16x16x32_bf16 v[148:151], a[20:23], a[156:159], v[148:151]\n" \
"ds_read_b64_tr_b16 a[20:21], v33 offset:55488\n" \
"ds_read_b64_tr_b16 a[22:23], v33 offset:60096\n" \
"v_mfma_f32_16x16x32_bf16 v[144:147], a[24:27], a[32:35], v[144:147]\n" \
"v_mfma_f32_16x16x32_bf16 v[148:151], a[24:27], a[48:51], v[148:151]\n" \
"ds_read_b64_tr_b16 a[24:25], v10 offset:192\n" \
"s_waitcnt lgkmcnt(13)\n" \
"v_mfma_f32_16x16x32_bf16 v[144:147], a[28:31], a[36:39], v[144:147]\n" \
"ds_read_b64_tr_b16 a[26:27], v10 offset:4800\n" \
"v_mfma_f32_16x16x32_bf16 v[148:151], a[28:31], a[52:55], v[148:151]\n" \
"ds_read_b64_tr_b16 a[28:29], v34 offset:192\n" \
"ds_read_b64_tr_b16 a[30:31], v34 offset:4800\n" \
"v_mfma_f32_16x16x32_bf16 v[144:147], a[192:195], a[40:43], v[144:147]\n" \
"buffer_load_dwordx4 v[76:79], v32, s[56:59], s97 offen offset:1024 sc1\n" \
"v_mfma_f32_16x16x32_bf16 v[148:151], a[192:195], a[56:59], v[148:151]\n" \
"ds_read_b64_tr_b16 a[192:193], v34 offset:9408\n" \
"ds_read_b64_tr_b16 a[194:195], v34 offset:14016\n" \
"v_mfma_f32_16x16x32_bf16 v[144:147], a[196:199], a[44:47], v[144:147]\n" \
"v_readlane_b32 s97, v11, 48\n" \
"v_mfma_f32_16x16x32_bf16 v[148:151], a[196:199], a[60:63], v[148:151]\n" \
"ds_read_b64_tr_b16 a[196:197], v34 offset:18624\n" \
"ds_read_b64_tr_b16 a[198:199], v34 offset:23232\n" \
"v_mfma_f32_16x16x32_bf16 v[152:155], a[200:203], a[128:131], v[152:155]\n" \
"v_mfma_f32_16x16x32_bf16 v[156:159], a[200:203], a[144:147], v[156:159]\n" \
"ds_read_b64_tr_b16 a[200:201], v10 offset:9408\n" \
"ds_read_b64_tr_b16 a[202:203], v10 offset:14016\n" \
"v_cvt_pk_bf16_f32 v144, v144, v145\n" \
"v_cvt_pk_bf16_f32 v145, v146, v147\n" \
"v_cvt_pk_bf16_f32 v146, v148, v149\n" \
"v_cvt_pk_bf16_f32 v147, v150, v151\n" \
"buffer_store_dwordx4 v[144:147], v20, s[56:59], s71 offen offset:0 sc1\n" \
"s_add_u32 s71, s71, 0x1800\n" \
"v_mfma_f32_16x16x32_bf16 v[152:155], a[204:207], a[132:135], v[152:155]\n" \
"v_mfma_f32_16x16x32_bf16 v[156:159], a[204:207], a[148:151], v[156:159]\n" \
"ds_read_b64_tr_b16 a[204:205], v33 offset:256\n" \
"ds_read_b64_tr_b16 a[206:207], v33 offset:4864\n" \
"v_mfma_f32_16x16x32_bf16 v[152:155], a[208:211], a[136:139], v[152:155]\n" \
"v_mfma_f32_16x16x32_bf16 v[156:159], a[208:211], a[152:155], v[156:159]\n" \
"ds_read_b64_tr_b16 a[208:209], v33 offset:9472\n" \
"ds_read_b64_tr_b16 a[210:211], v33 offset:14080\n" \
"v_mfma_f32_16x16x32_bf16 v[152:155], a[212:215], a[140:143], v[152:155]\n" \
"buffer_load_dwordx4 v[80:83], v20, s[56:59], s64 offen offset:0 sc1\n" \
"v_mfma_f32_16x16x32_bf16 v[156:159], a[212:215], a[156:159], v[156:159]\n" \
"ds_read_b64_tr_b16 a[212:213], v33 offset:18688\n" \
"ds_read_b64_tr_b16 a[214:215], v33 offset:23296\n" \
"v_mfma_f32_16x16x32_bf16 v[152:155], a[216:219], a[32:35], v[152:155]\n" \
"v_mfma_f32_16x16x32_bf16 v[156:159], a[216:219], a[48:51], v[156:159]\n" \
"ds_read_b64_tr_b16 a[216:217], v33 offset:27904\n" \
"s_waitcnt lgkmcnt(12)\n" \
"v_mfma_f32_16x16x32_bf16 v[152:155], a[0:3], a[36:39], v[152:155]\n" \
"ds_read_b64_tr_b16 a[218:219], v33 offset:32512\n" \
"v_mfma_f32_16x16x32_bf16 v[156:159], a[0:3], a[52:55], v[156:159]\n" \
"ds_read_b64_tr_b16 a[0:1], v33 offset:37120\n" \
"ds_read_b64_tr_b16 a[2:3], v33 offset:41728\n" \
"v_mfma_f32_16x16x32_bf16 v[152:155], a[4:7], a[40:43], v[152:155]\n" \
"buffer_load_dwordx4 v[84:87], v20, s[56:59], s64 offen offset:1024 sc1\n" \
"v_mfma_f32_16x16x32_bf16 v[156:159], a[4:7], a[56:59], v[156:159]\n" \
"ds_read_b64_tr_b16 a[4:5], v33 offset:46336\n" \
"ds_read_b64_tr_b16 a[6:7], v33 offset:50944\n" \
"v_mfma_f32_16x16x32_bf16 v[152:155], a[8:11], a[44:47], v[152:155]\n" \
"v_mfma_f32_16x16x32_bf16 v[156:159], a[8:11], a[60:63], v[156:159]\n" \
"ds_read_b64_tr_b16 a[8:9], v33 offset:55552\n" \
"ds_read_b64_tr_b16 a[10:11], v33 offset:60160\n" \
"v_mfma_f32_16x16x32_bf16 v[160:163], a[12:15], a[128:131], v[160:163]\n" \
"v_mfma_f32_16x16x32_bf16 v[164:167], a[12:15], a[144:147], v[164:167]\n" \
"ds_read_b64_tr_b16 a[12:13], v10 offset:256\n" \
"ds_read_b64_tr_b16 a[14:15], v10 offset:4864\n" \
"v_mfma_f32_16x16x32_bf16 v[160:163], a[16:19], a[132:135], v[160:163]\n" \
"v_cvt_pk_bf16_f32 v152, v152, v153\n" \
"v_cvt_pk_bf16_f32 v153, v154, v155\n" \
"v_cvt_pk_bf16_f32 v154, v156, v157\n" \
"v_cvt_pk_bf16_f32 v155, v158, v159\n" \
"buffer_store_dwordx4 v[152:155], v20, s[56:59], s72 offen offset:0 sc1\n" \
"s_add_u32 s72, s72, 0x1000\n" \
"v_mfma_f32_16x16x32_bf16 v[164:167], a[16:19], a[148:151], v[164:167]\n" \
"ds_read_b64_tr_b16 a[16:17], v34 offset:256\n" \
"ds_read_b64_tr_b16 a[18:19], v34 offset:4864\n" \
"v_mfma_f32_16x16x32_bf16 v[160:163], a[20:23], a[136:139], v[160:163]\n" \
"v_mfma_f32_16x16x32_bf16 v[164:167], a[20:23], a[152:155], v[164:167]\n" \
"ds_read_b64_tr_b16 a[20:21], v34 offset:9472\n" \
"ds_read_b64_tr_b16 a[22:23], v34 offset:14080\n" \
"v_mfma_f32_16x16x32_bf16 v[160:163], a[24:27], a[140:143], v[160:163]\n" \
"buffer_load_dwordx4 v[88:91], v32, s[56:59], s64 offen offset:0 sc1\n" \
"v_mfma_f32_16x16x32_bf16 v[164:167], a[24:27], a[156:159], v[164:167]\n" \
"ds_read_b64_tr_b16 a[24:25], v34 offset:18688\n" \
"ds_read_b64_tr_b16 a[26:27], v34 offset:23296\n" \
"v_mfma_f32_16x16x32_bf16 v[160:163], a[28:31], a[32:35], v[160:163]\n" \
"v_mfma_f32_16x16x32_bf16 v[164:167], a[28:31], a[48:51], v[164:167]\n" \
"ds_read_b64_tr_b16 a[28:29], v10 offset:9472\n" \
"s_waitcnt vmcnt(17)\n" \
"s_mov_b64 exec, 1\n" \
"buffer_store_dword v39, off, s[56:59], s67 offset:0 sc1\n" \
"s_mov_b64 exec, -1\n" \
"s_waitcnt lgkmcnt(11)\n" \
"v_mfma_f32_16x16x32_bf16 v[160:163], a[192:195], a[36:39], v[160:163]\n" \
"ds_read_b64_tr_b16 a[30:31], v10 offset:14080\n" \
"v_mfma_f32_16x16x32_bf16 v[164:167], a[192:195], a[52:55], v[164:167]\n" \
"ds_read_b64_tr_b16 a[192:193], v33 offset:0\n" \
"ds_read_b64_tr_b16 a[194:195], v33 offset:4608\n" \
"v_mfma_f32_16x16x32_bf16 v[160:163], a[196:199], a[40:43], v[160:163]\n" \
"v_mfma_f32_16x16x32_bf16 v[164:167], a[196:199], a[56:59], v[164:167]\n" \
"ds_read_b64_tr_b16 a[196:197], v33 offset:9216\n" \
"ds_read_b64_tr_b16 a[198:199], v33 offset:13824\n" \
"v_mfma_f32_16x16x32_bf16 v[160:163], a[200:203], a[44:47], v[160:163]\n" \
"buffer_load_dwordx4 v[92:95], v32, s[56:59], s64 offen offset:1024 sc1\n" \
"v_mfma_f32_16x16x32_bf16 v[164:167], a[200:203], a[60:63], v[164:167]\n" \
"ds_read_b64_tr_b16 a[200:201], v33 offset:18432\n" \
"ds_read_b64_tr_b16 a[202:203], v33 offset:23040\n" \
"v_mfma_f32_16x16x32_bf16 v[248:251], a[204:207], a[96:99], v[248:251]\n" \
"v_readlane_b32 s64, v11, 49\n" \
"v_mfma_f32_16x16x32_bf16 v[252:255], a[204:207], a[112:115], v[252:255]\n" \
"ds_read_b64_tr_b16 a[204:205], v33 offset:27648\n" \
"v_cvt_pk_bf16_f32 v160, v160, v161\n" \
"v_cvt_pk_bf16_f32 v161, v162, v163\n" \
"v_cvt_pk_bf16_f32 v162, v164, v165\n" \
"v_cvt_pk_bf16_f32 v163, v166, v167\n" \
"buffer_store_dwordx4 v[160:163], v20, s[56:59], s73 offen offset:0 sc1\n" \
"s_add_u32 s73, s73, 0x800\n" \
"v_mfma_f32_16x16x32_bf16 v[248:251], a[208:211], a[100:103], v[248:251]\n" \
"ds_read_b64_tr_b16 a[206:207], v33 offset:32256\n" \
"v_mfma_f32_16x16x32_bf16 v[252:255], a[208:211], a[116:119], v[252:255]\n" \
"ds_read_b64_tr_b16 a[208:209], v33 offset:36864\n" \
"ds_read_b64_tr_b16 a[210:211], v33 offset:41472\n" \
"v_mfma_f32_16x16x32_bf16 v[248:251], a[212:215], a[104:107], v[248:251]\n" \
"v_mfma_f32_16x16x32_bf16 v[252:255], a[212:215], a[120:123], v[252:255]\n" \
"ds_read_b64_tr_b16 a[212:213], v33 offset:46080\n" \
"ds_read_b64_tr_b16 a[214:215], v33 offset:50688\n" \
"v_mfma_f32_16x16x32_bf16 v[248:251], a[216:219], a[108:111], v[248:251]\n" \
"buffer_load_dwordx4 v[96:99], v20, s[56:59], s65 offen offset:0 sc1\n" \
"v_mfma_f32_16x16x32_bf16 v[252:255], a[216:219], a[124:127], v[252:255]\n" \
"ds_read_b64_tr_b16 a[216:217], v33 offset:55296\n" \
"ds_read_b64_tr_b16 a[218:219], v33 offset:59904\n" \
"s_waitcnt vmcnt(18)\n" \
"s_mov_b64 exec, 1\n" \
"buffer_store_dword v39, off, s[56:59], s68 offset:4 sc1\n" \
"s_mov_b64 exec, -1\n" \
"v_mfma_f32_16x16x32_bf16 v[176:179], a[0:3], a[128:131], v[176:179]\n" \
"v_mfma_f32_16x16x32_bf16 v[180:183], a[0:3], a[144:147], v[180:183]\n" \
"v_mfma_f32_16x16x32_bf16 v[248:251], a[0:3], a[160:163], v[248:251]\n" \
"v_mfma_f32_16x16x32_bf16 v[252:255], a[0:3], a[176:179], v[252:255]\n" \
"ds_read_b64_tr_b16 a[0:1], v10 offset:0\n" \
"ds_read_b64_tr_b16 a[2:3], v10 offset:4608\n" \
"v_mfma_f32_16x16x32_bf16 v[176:179], a[4:7], a[132:135], v[176:179]\n" \
"buffer_load_dwordx4 v[104:107], v32, s[56:59], s65 offen offset:0 sc1\n" \
"v_mfma_f32_16x16x32_bf16 v[180:183], a[4:7], a[148:151], v[180:183]\n" \
"v_mfma_f32_16x16x32_bf16 v[248:251], a[4:7], a[164:167], v[248:251]\n" \
"v_mfma_f32_16x16x32_bf16 v[252:255], a[4:7], a[180:183], v[252:255]\n" \
"ds_read_b64_tr_b16 a[4:5], v34 offset:0\n" \
"s_waitcnt lgkmcnt(9)\n" \
"v_mfma_f32_16x16x32_bf16 v[176:179], a[8:11], a[136:139], v[176:179]\n" \
"ds_read_b64_tr_b16 a[6:7], v34 offset:4608\n" \
"v_mfma_f32_16x16x32_bf16 v[180:183], a[8:11], a[152:155], v[180:183]\n" \
"buffer_load_dwordx4 v[100:103], v20, s[56:59], s65 offen offset:1024 sc1\n" \
"v_mfma_f32_16x16x32_bf16 v[248:251], a[8:11], a[168:171], v[248:251]\n" \
"v_mfma_f32_16x16x32_bf16 v[252:255], a[8:11], a[184:187], v[252:255]\n" \
"ds_read_b64_tr_b16 a[8:9], v34 offset:9216\n" \
"ds_read_b64_tr_b16 a[10:11], v34 offset:13824\n" \
"v_mfma_f32_16x16x32_bf16 v[176:179], a[12:15], a[140:143], v[176:179]\n" \
"v_mfma_f32_16x16x32_bf16 v[180:183], a[12:15], a[156:159], v[180:183]\n" \
"v_mfma_f32_16x16x32_bf16 v[248:251], a[12:15], a[172:175], v[248:251]\n" \
"buffer_load_dwordx4 v[108:111], v32, s[56:59], s65 offen offset:1024 sc1\n" \
"v_mfma_f32_16x16x32_bf16 v[252:255], a[12:15], a[188:191], v[252:255]\n" \
"ds_read_b64_tr_b16 a[12:13], v34 offset:18432\n" \
"ds_read_b64_tr_b16 a[14:15], v34 offset:23040\n" \
"s_waitcnt vmcnt(18)\n" \
"s_mov_b64 exec, 1\n" \
"buffer_store_dword v39, off, s[56:59], s69 offset:8 sc1\n" \
"s_mov_b64 exec, -1\n" \
"v_mfma_f32_16x16x32_bf16 v[176:179], a[16:19], a[32:35], v[176:179]\n" \
"v_mfma_f32_16x16x32_bf16 v[180:183], a[16:19], a[48:51], v[180:183]\n" \
"v_mfma_f32_16x16x32_bf16 v[248:251], a[16:19], a[220:223], v[248:251]\n" \
"v_mfma_f32_16x16x32_bf16 v[252:255], a[16:19], a[236:239], v[252:255]\n" \
"ds_read_b64_tr_b16 a[16:17], v10 offset:9216\n" \
"ds_read_b64_tr_b16 a[18:19], v10 offset:13824\n" \
".fill (L_ab + 28672 - .) / 4, 4, 0xBF800000\n" \
"L_ap7:\n" \
"s_cbranch_execz L_ap0\n" \
"v_mfma_f32_16x16x32_bf16 v[176:179], a[20:23], a[36:39], v[176:179]\n" \
"v_mfma_f32_16x16x32_bf16 v[180:183], a[20:23], a[52:55], v[180:183]\n" \
"v_mfma_f32_16x16x32_bf16 v[248:251], a[20:23], a[224:227], v[248:251]\n" \
"v_mfma_f32_16x16x32_bf16 v[252:255], a[20:23], a[240:243], v[252:255]\n" \
"ds_read_b64_tr_b16 a[20:21], v33 offset:32\n" \
"ds_read_b64_tr_b16 a[22:23], v33 offset:4640\n" \
"v_mfma_f32_16x16x32_bf16 v[176:179], a[24:27], a[40:43], v[176:179]\n" \
"v_mfma_f32_16x16x32_bf16 v[180:183], a[24:27], a[56:59], v[180:183]\n" \
"v_mfma_f32_16x16x32_bf16 v[248:251], a[24:27], a[228:231], v[248:251]\n" \
"v_mfma_f32_16x16x32_bf16 v[252:255], a[24:27], a[244:247], v[252:255]\n" \
"ds_read_b64_tr_b16 a[24:25], v33 offset:9248\n" \
"ds_read_b64_tr_b16 a[26:27], v33 offset:13856\n" \
"v_mfma_f32_16x16x32_bf16 v[176:179], a[28:31], a[44:47], v[176:179]\n" \
"v_mfma_f32_16x16x32_bf16 v[180:183], a[28:31], a[60:63], v[180:183]\n" \
"v_mfma_f32_16x16x32_bf16 v[248:251], a[28:31], a[232:235], v[248:251]\n" \
"v_mfma_f32_16x16x32_bf16 v[252:255], a[28:31], a[248:251], v[252:255]\n" \
"ds_read_b64_tr_b16 a[28:29], v33 offset:18464\n" \
"ds_read_b64_tr_b16 a[30:31], v33 offset:23072\n" \
"v_mfma_f32_16x16x32_bf16 v[184:187], a[192:195], a[96:99], v[184:187]\n" \
"s_waitcnt vmcnt(16)\n" \
"s_mov_b64 exec, 1\n" \
"buffer_store_dword v39, off, s[56:59], s70 offset:12 sc1\n" \
"s_mov_b64 exec, -1\n" \
"v_cvt_pk_bf16_f32 v176, v176, v177\n" \
"v_cvt_pk_bf16_f32 v248, v248, v249\n" \
"v_cvt_pk_bf16_f32 v177, v178, v179\n" \
"v_cvt_pk_bf16_f32 v249, v250, v251\n" \
"v_cvt_pk_bf16_f32 v178, v180, v181\n" \
"v_cvt_pk_bf16_f32 v179, v182, v183\n" \
"v_cvt_pk_bf16_f32 v250, v252, v253\n" \
"v_cvt_pk_bf16_f32 v251, v254, v255\n" \
"buffer_store_dwordx4 v[176:179], v20, s[56:59], s92 offen offset:0 sc1\n" \
"v_mfma_f32_16x16x32_bf16 v[188:191], a[192:195], a[112:115], v[188:191]\n" \
"ds_read_b64_tr_b16 a[192:193], v33 offset:27680\n" \
"ds_read_b64_tr_b16 a[194:195], v33 offset:32288\n" \
"v_mfma_f32_16x16x32_bf16 v[184:187], a[196:199], a[100:103], v[184:187]\n" \
"v_mfma_f32_16x16x32_bf16 v[188:191], a[196:199], a[116:119], v[188:191]\n" \
"buffer_store_dwordx4 v[248:251], v20, s[56:59], s92 offen offset:1024 sc1\n" \
"v_mfma_f32_16x16x32_bf16 v[184:187], a[200:203], a[104:107], v[184:187]\n" \
"ds_read_b64_tr_b16 a[196:197], v33 offset:36896\n" \
"ds_read_b64_tr_b16 a[198:199], v33 offset:41504\n" \
"v_mfma_f32_16x16x32_bf16 v[188:191], a[200:203], a[120:123], v[188:191]\n" \
"ds_read_b64_tr_b16 a[200:201], v33 offset:46112\n" \
"ds_read_b64_tr_b16 a[202:203], v33 offset:50720\n" \
"v_mfma_f32_16x16x32_bf16 v[184:187], a[204:207], a[108:111], v[184:187]\n" \
"v_readlane_b32 s92, v11, 51\n" \
"v_mfma_f32_16x16x32_bf16 v[188:191], a[204:207], a[124:127], v[188:191]\n" \
"ds_read_b64_tr_b16 a[204:205], v33 offset:55328\n" \
"s_waitcnt lgkmcnt(9)\n" \
"v_mfma_f32_16x16x32_bf16 v[184:187], a[208:211], a[160:163], v[184:187]\n" \
"ds_read_b64_tr_b16 a[206:207], v33 offset:59936\n" \
"v_mfma_f32_16x16x32_bf16 v[188:191], a[208:211], a[176:179], v[188:191]\n" \
"ds_read_b64_tr_b16 a[208:209], v10 offset:32\n" \
"ds_read_b64_tr_b16 a[210:211], v10 offset:4640\n" \
"v_mfma_f32_16x16x32_bf16 v[184:187], a[212:215], a[164:167], v[184:187]\n" \
"v_mfma_f32_16x16x32_bf16 v[188:191], a[212:215], a[180:183], v[188:191]\n" \
"ds_read_b64_tr_b16 a[212:213], v34 offset:32\n" \
"ds_read_b64_tr_b16 a[214:215], v34 offset:4640\n" \
"v_mfma_f32_16x16x32_bf16 v[184:187], a[216:219], a[168:171], v[184:187]\n" \
"v_mfma_f32_16x16x32_bf16 v[188:191], a[216:219], a[184:187], v[188:191]\n" \
"ds_read_b64_tr_b16 a[216:217], v34 offset:9248\n" \
"ds_read_b64_tr_b16 a[218:219], v34 offset:13856\n" \
"s_waitcnt vmcnt(16)\n" \
"s_mov_b64 exec, 1\n" \
"buffer_store_dword v39, off, s[56:59], s71 offset:16 sc1\n" \
"s_mov_b64 exec, -1\n" \
"v_mfma_f32_16x16x32_bf16 v[184:187], a[0:3], a[172:175], v[184:187]\n" \
"v_mfma_f32_16x16x32_bf16 v[188:191], a[0:3], a[188:191], v[188:191]\n" \
"ds_read_b64_tr_b16 a[0:1], v34 offset:18464\n" \
"ds_read_b64_tr_b16 a[2:3], v34 offset:23072\n" \
"v_mfma_f32_16x16x32_bf16 v[184:187], a[4:7], a[220:223], v[184:187]\n" \
"v_mfma_f32_16x16x32_bf16 v[188:191], a[4:7], a[236:239], v[188:191]\n" \
"ds_read_b64_tr_b16 a[4:5], v10 offset:9248\n" \
"ds_read_b64_tr_b16 a[6:7], v10 offset:13856\n" \
"v_mfma_f32_16x16x32_bf16 v[184:187], a[8:11], a[224:227], v[184:187]\n" \
"v_mfma_f32_16x16x32_bf16 v[188:191], a[8:11], a[240:243], v[188:191]\n" \
"ds_read_b64_tr_b16 a[8:9], v33 offset:64\n" \
"ds_read_b64_tr_b16 a[10:11], v33 offset:4672\n" \
"v_mfma_f32_16x16x32_bf16 v[184:187], a[12:15], a[228:231], v[184:187]\n" \
"v_mfma_f32_16x16x32_bf16 v[188:191], a[12:15], a[244:247], v[188:191]\n" \
"ds_read_b64_tr_b16 a[12:13], v33 offset:9280\n" \
"ds_read_b64_tr_b16 a[14:15], v33 offset:13888\n" \
"v_mfma_f32_16x16x32_bf16 v[184:187], a[16:19], a[232:235], v[184:187]\n" \
"v_mfma_f32_16x16x32_bf16 v[188:191], a[16:19], a[248:251], v[188:191]\n" \
"ds_read_b64_tr_b16 a[16:17], v33 offset:18496\n" \
"ds_read_b64_tr_b16 a[18:19], v33 offset:23104\n" \
"v_mfma_f32_16x16x32_bf16 v[192:195], a[20:23], a[96:99], v[192:195]\n" \
"v_mfma_f32_16x16x32_bf16 v[196:199], a[20:23], a[112:115], v[196:199]\n" \
"ds_read_b64_tr_b16 a[20:21], v33 offset:27712\n" \
"ds_read_b64_tr_b16 a[22:23], v33 offset:32320\n" \
"v_mfma_f32_16x16x32_bf16 v[192:195], a[24:27], a[100:103], v[192:195]\n" \
"v_cvt_pk_bf16_f32 v184, v184, v185\n" \
"v_cvt_pk_bf16_f32 v185, v186, v187\n" \
"v_cvt_pk_bf16_f32 v186, v188, v189\n" \
"v_cvt_pk_bf16_f32 v187, v190, v191\n" \
"buffer_store_dwordx4 v[184:187], v20, s[56:59], s94 offen offset:1024 sc1\n" \
"s_add_u32 s94, s94, 0x3800\n" \
"v_mfma_f32_16x16x32_bf16 v[196:199], a[24:27], a[116:119], v[196:199]\n" \
"ds_read_b64_tr_b16 a[24:25], v33 offset:36928\n" \
"s_waitcnt lgkmcnt(13)\n" \
"v_mfma_f32_16x16x32_bf16 v[192:195], a[28:31], a[104:107], v[192:195]\n" \
"ds_read_b64_tr_b16 a[26:27], v33 offset:41536\n" \
"v_mfma_f32_16x16x32_bf16 v[196:199], a[28:31], a[120:123], v[196:199]\n" \
"ds_read_b64_tr_b16 a[28:29], v33 offset:46144\n" \
"ds_read_b64_tr_b16 a[30:31], v33 offset:50752\n" \
"s_waitcnt vmcnt(15)\n" \
"s_mov_b64 exec, 1\n" \
"buffer_store_dword v39, off, s[56:59], s72 offset:20 sc1\n" \
"s_mov_b64 exec, -1\n" \
"v_mfma_f32_16x16x32_bf16 v[192:195], a[192:195], a[108:111], v[192:195]\n" \
"v_readlane_b32 s72, v11, 41\n" \
"v_mfma_f32_16x16x32_bf16 v[196:199], a[192:195], a[124:127], v[196:199]\n" \
"ds_read_b64_tr_b16 a[192:193], v33 offset:55360\n" \
"ds_read_b64_tr_b16 a[194:195], v33 offset:59968\n" \
"v_mfma_f32_16x16x32_bf16 v[192:195], a[196:199], a[160:163], v[192:195]\n" \
"v_mfma_f32_16x16x32_bf16 v[196:199], a[196:199], a[176:179], v[196:199]\n" \
"ds_read_b64_tr_b16 a[196:197], v10 offset:64\n" \
"ds_read_b64_tr_b16 a[198:199], v10 offset:4672\n" \
"v_mfma_f32_16x16x32_bf16 v[192:195], a[200:203], a[164:167], v[192:195]\n" \
"v_mfma_f32_16x16x32_bf16 v[196:199], a[200:203], a[180:183], v[196:199]\n" \
"ds_read_b64_tr_b16 a[200:201], v34 offset:64\n" \
"ds_read_b64_tr_b16 a[202:203], v34 offset:4672\n" \
"v_mfma_f32_16x16x32_bf16 v[192:195], a[204:207], a[168:171], v[192:195]\n" \
"v_mfma_f32_16x16x32_bf16 v[196:199], a[204:207], a[184:187], v[196:199]\n" \
"ds_read_b64_tr_b16 a[204:205], v34 offset:9280\n" \
"ds_read_b64_tr_b16 a[206:207], v34 offset:13888\n" \
"v_mfma_f32_16x16x32_bf16 v[192:195], a[208:211], a[172:175], v[192:195]\n" \
"v_mfma_f32_16x16x32_bf16 v[196:199], a[208:211], a[188:191], v[196:199]\n" \
"ds_read_b64_tr_b16 a[208:209], v34 offset:18496\n" \
"ds_read_b64_tr_b16 a[210:211], v34 offset:23104\n" \
"v_mfma_f32_16x16x32_bf16 v[192:195], a[212:215], a[220:223], v[192:195]\n" \
"v_mfma_f32_16x16x32_bf16 v[196:199], a[212:215], a[236:239], v[196:199]\n" \
"ds_read_b64_tr_b16 a[212:213], v10 offset:9280\n" \
"ds_read_b64_tr_b16 a[214:215], v10 offset:13888\n" \
"v_mfma_f32_16x16x32_bf16 v[192:195], a[216:219], a[224:227], v[192:195]\n" \
"v_mfma_f32_16x16x32_bf16 v[196:199], a[216:219], a[240:243], v[196:199]\n" \
"ds_read_b64_tr_b16 a[216:217], v33 offset:96\n" \
"s_waitcnt lgkmcnt(13)\n" \
"v_mfma_f32_16x16x32_bf16 v[192:195], a[0:3], a[228:231], v[192:195]\n" \
"ds_read_b64_tr_b16 a[218:219], v33 offset:4704\n" \
"v_mfma_f32_16x16x32_bf16 v[196:199], a[0:3], a[244:247], v[196:199]\n" \
"ds_read_b64_tr_b16 a[0:1], v33 offset:9312\n" \
"ds_read_b64_tr_b16 a[2:3], v33 offset:13920\n" \
"s_waitcnt vmcnt(12)\n" \
"s_mov_b64 exec, 1\n" \
"buffer_store_dword v39, off, s[56:59], s73 offset:24 sc1\n" \
"s_mov_b64 exec, -1\n" \
"v_mfma_f32_16x16x32_bf16 v[192:195], a[4:7], a[232:235], v[192:195]\n" \
"v_readlane_b32 s73, v11, 50\n" \
"v_mfma_f32_16x16x32_bf16 v[196:199], a[4:7], a[248:251], v[196:199]\n" \
"ds_read_b64_tr_b16 a[4:5], v33 offset:18528\n" \
"ds_read_b64_tr_b16 a[6:7], v33 offset:23136\n" \
"v_mfma_f32_16x16x32_bf16 v[200:203], a[8:11], a[96:99], v[200:203]\n" \
"v_mfma_f32_16x16x32_bf16 v[204:207], a[8:11], a[112:115], v[204:207]\n" \
"ds_read_b64_tr_b16 a[8:9], v33 offset:27744\n" \
"ds_read_b64_tr_b16 a[10:11], v33 offset:32352\n" \
"v_cvt_pk_bf16_f32 v192, v192, v193\n" \
"v_cvt_pk_bf16_f32 v193, v194, v195\n" \
"v_cvt_pk_bf16_f32 v194, v196, v197\n" \
"v_cvt_pk_bf16_f32 v195, v198, v199\n" \
"buffer_store_dwordx4 v[192:195], v20, s[56:59], s95 offen offset:1024 sc1\n" \
"s_add_u32 s95, s95, 0x3000\n" \
"v_mfma_f32_16x16x32_bf16 v[200:203], a[12:15], a[100:103], v[200:203]\n" \
"v_mfma_f32_16x16x32_bf16 v[204:207], a[12:15], a[116:119], v[204:207]\n" \
"ds_read_b64_tr_b16 a[12:13], v33 offset:36960\n" \
"ds_read_b64_tr_b16 a[14:15], v33 offset:41568\n" \
"v_mfma_f32_16x16x32_bf16 v[200:203], a[16:19], a[104:107], v[200:203]\n" \
"v_mfma_f32_16x16x32_bf16 v[204:207], a[16:19], a[120:123], v[204:207]\n" \
"ds_read_b64_tr_b16 a[16:17], v33 offset:46176\n" \
"ds_read_b64_tr_b16 a[18:19], v33 offset:50784\n" \
"v_mfma_f32_16x16x32_bf16 v[200:203], a[20:23], a[108:111], v[200:203]\n" \
"v_mfma_f32_16x16x32_bf16 v[204:207], a[20:23], a[124:127], v[204:207]\n" \
"ds_read_b64_tr_b16 a[20:21], v33 offset:55392\n" \
"ds_read_b64_tr_b16 a[22:23], v33 offset:60000\n" \
"v_mfma_f32_16x16x32_bf16 v[200:203], a[24:27], a[160:163], v[200:203]\n" \
"v_mfma_f32_16x16x32_bf16 v[204:207], a[24:27], a[176:179], v[204:207]\n" \
"ds_read_b64_tr_b16 a[24:25], v10 offset:96\n" \
"ds_read_b64_tr_b16 a[26:27], v10 offset:4704\n" \
"v_mfma_f32_16x16x32_bf16 v[200:203], a[28:31], a[164:167], v[200:203]\n" \
"v_mfma_f32_16x16x32_bf16 v[204:207], a[28:31], a[180:183], v[204:207]\n" \
"ds_read_b64_tr_b16 a[28:29], v34 offset:96\n" \
"s_waitcnt lgkmcnt(12)\n" \
"v_mfma_f32_16x16x32_bf16 v[200:203], a[192:195], a[168:171], v[200:203]\n" \
"ds_read_b64_tr_b16 a[30:31], v34 offset:4704\n" \
"v_mfma_f32_16x16x32_bf16 v[204:207], a[192:195], a[184:187], v[204:207]\n" \
"ds_read_b64_tr_b16 a[192:193], v34 offset:9312\n" \
"ds_read_b64_tr_b16 a[194:195], v34 offset:13920\n" \
"v_mfma_f32_16x16x32_bf16 v[200:203], a[196:199], a[172:175], v[200:203]\n" \
"v_mfma_f32_16x16x32_bf16 v[204:207], a[196:199], a[188:191], v[204:207]\n" \
"ds_read_b64_tr_b16 a[196:197], v34 offset:18528\n" \
"ds_read_b64_tr_b16 a[198:199], v34 offset:23136\n" \
"v_mfma_f32_16x16x32_bf16 v[200:203], a[200:203], a[220:223], v[200:203]\n" \
"v_mfma_f32_16x16x32_bf16 v[204:207], a[200:203], a[236:239], v[204:207]\n" \
"ds_read_b64_tr_b16 a[200:201], v10 offset:9312\n" \
"ds_read_b64_tr_b16 a[202:203], v10 offset:13920\n" \
"v_mfma_f32_16x16x32_bf16 v[200:203], a[204:207], a[224:227], v[200:203]\n" \
"v_mfma_f32_16x16x32_bf16 v[204:207], a[204:207], a[240:243], v[204:207]\n" \
"ds_read_b64_tr_b16 a[204:205], v33 offset:128\n" \
"ds_read_b64_tr_b16 a[206:207], v33 offset:4736\n" \
"v_mfma_f32_16x16x32_bf16 v[200:203], a[208:211], a[228:231], v[200:203]\n" \
"v_mfma_f32_16x16x32_bf16 v[204:207], a[208:211], a[244:247], v[204:207]\n" \
"ds_read_b64_tr_b16 a[208:209], v33 offset:9344\n" \
"ds_read_b64_tr_b16 a[210:211], v33 offset:13952\n" \
"v_mfma_f32_16x16x32_bf16 v[200:203], a[212:215], a[232:235], v[200:203]\n" \
"v_mfma_f32_16x16x32_bf16 v[204:207], a[212:215], a[248:251], v[204:207]\n" \
"ds_read_b64_tr_b16 a[212:213], v33 offset:18560\n" \
"ds_read_b64_tr_b16 a[214:215], v33 offset:23168\n" \
"v_mfma_f32_16x16x32_bf16 v[208:211], a[216:219], a[96:99], v[208:211]\n" \
"v_mfma_f32_16x16x32_bf16 v[212:215], a[216:219], a[112:115], v[212:215]\n" \
"ds_read_b64_tr_b16 a[216:217], v33 offset:27776\n" \
"ds_read_b64_tr_b16 a[218:219], v33 offset:32384\n" \
"v_mfma_f32_16x16x32_bf16 v[208:211], a[0:3], a[100:103], v[208:211]\n" \
"v_cvt_pk_bf16_f32 v200, v200, v201\n" \
"v_cvt_pk_bf16_f32 v201, v202, v203\n" \
"v_cvt_pk_bf16_f32 v202, v204, v205\n" \
"v_cvt_pk_bf16_f32 v203, v206, v207\n" \
"buffer_store_dwordx4 v[200:203], v20, s[56:59], s96 offen offset:1024 sc1\n" \
"s_add_u32 s96, s96, 0x2800\n" \
"v_mfma_f32_16x16x32_bf16 v[212:215], a[0:3], a[116:119], v[212:215]\n" \
"ds_read_b64_tr_b16 a[0:1], v33 offset:36992\n" \
"s_waitcnt lgkmcnt(13)\n" \
"v_mfma_f32_16x16x32_bf16 v[208:211], a[4:7], a[104:107], v[208:211]\n" \
"ds_read_b64_tr_b16 a[2:3], v33 offset:41600\n" \
"v_mfma_f32_16x16x32_bf16 v[212:215], a[4:7], a[120:123], v[212:215]\n" \
"ds_read_b64_tr_b16 a[4:5], v33 offset:46208\n" \
"ds_read_b64_tr_b16 a[6:7], v33 offset:50816\n" \
"v_mfma_f32_16x16x32_bf16 v[208:211], a[8:11], a[108:111], v[208:211]\n" \
"v_mfma_f32_16x16x32_bf16 v[212:215], a[8:11], a[124:127], v[212:215]\n" \
"ds_read_b64_tr_b16 a[8:9], v33 offset:55424\n" \
"ds_read_b64_tr_b16 a[10:11], v33 offset:60032\n" \
"v_mfma_f32_16x16x32_bf16 v[208:211], a[12:15], a[160:163], v[208:211]\n" \
"v_mfma_f32_16x16x32_bf16 v[212:215], a[12:15], a[176:179], v[212:215]\n" \
"ds_read_b64_tr_b16 a[12:13], v10 offset:128\n" \
"ds_read_b64_tr_b16 a[14:15], v10 offset:4736\n" \
"v_mfma_f32_16x16x32_bf16 v[208:211], a[16:19], a[164:167], v[208:211]\n" \
"v_mfma_f32_16x16x32_bf16 v[212:215], a[16:19], a[180:183], v[212:215]\n" \
"ds_read_b64_tr_b16 a[16:17], v34 offset:128\n" \
"ds_read_b64_tr_b16 a[18:19], v34 offset:4736\n" \
"v_mfma_f32_16x16x32_bf16 v[208:211], a[20:23], a[168:171], v[208:211]\n" \
"v_mfma_f32_16x16x32_bf16 v[212:215], a[20:23], a[184:187], v[212:215]\n" \
"ds_read_b64_tr_b16 a[20:21], v34 offset:9344\n" \
"ds_read_b64_tr_b16 a[22:23], v34 offset:13952\n" \
"v_mfma_f32_16x16x32_bf16 v[208:211], a[24:27], a[172:175], v[208:211]\n" \
"v_mfma_f32_16x16x32_bf16 v[212:215], a[24:27], a[188:191], v[212:215]\n" \
"ds_read_b64_tr_b16 a[24:25], v34 offset:18560\n" \
"ds_read_b64_tr_b16 a[26:27], v34 offset:23168\n" \
"v_mfma_f32_16x16x32_bf16 v[208:211], a[28:31], a[220:223], v[208:211]\n" \
"v_mfma_f32_16x16x32_bf16 v[212:215], a[28:31], a[236:239], v[212:215]\n" \
"ds_read_b64_tr_b16 a[28:29], v10 offset:9344\n" \
"ds_read_b64_tr_b16 a[30:31], v10 offset:13952\n" \
"s_waitcnt vmcnt(6)\n" \
"s_mov_b64 exec, 3\n" \
"buffer_store_dword v39, v41, s[56:59], s93 offen sc1\n" \
"s_mov_b64 exec, -1\n" \
"v_mfma_f32_16x16x32_bf16 v[208:211], a[192:195], a[224:227], v[208:211]\n" \
"v_mfma_f32_16x16x32_bf16 v[212:215], a[192:195], a[240:243], v[212:215]\n" \
"ds_read_b64_tr_b16 a[192:193], v33 offset:160\n" \
"s_waitcnt lgkmcnt(13)\n" \
"v_mfma_f32_16x16x32_bf16 v[208:211], a[196:199], a[228:231], v[208:211]\n" \
"ds_read_b64_tr_b16 a[194:195], v33 offset:4768\n" \
"v_mfma_f32_16x16x32_bf16 v[212:215], a[196:199], a[244:247], v[212:215]\n" \
"ds_read_b64_tr_b16 a[196:197], v33 offset:9376\n" \
"ds_read_b64_tr_b16 a[198:199], v33 offset:13984\n" \
"v_mfma_f32_16x16x32_bf16 v[208:211], a[200:203], a[232:235], v[208:211]\n" \
"v_mfma_f32_16x16x32_bf16 v[212:215], a[200:203], a[248:251], v[212:215]\n" \
"ds_read_b64_tr_b16 a[200:201], v33 offset:18592\n" \
"ds_read_b64_tr_b16 a[202:203], v33 offset:23200\n" \
"v_mfma_f32_16x16x32_bf16 v[216:219], a[204:207], a[96:99], v[216:219]\n" \
"v_mfma_f32_16x16x32_bf16 v[220:223], a[204:207], a[112:115], v[220:223]\n" \
"ds_read_b64_tr_b16 a[204:205], v33 offset:27808\n" \
"ds_read_b64_tr_b16 a[206:207], v33 offset:32416\n" \
"v_mfma_f32_16x16x32_bf16 v[216:219], a[208:211], a[100:103], v[216:219]\n" \
"v_cvt_pk_bf16_f32 v208, v208, v209\n" \
"v_cvt_pk_bf16_f32 v209, v210, v211\n" \
"v_cvt_pk_bf16_f32 v210, v212, v213\n" \
"v_cvt_pk_bf16_f32 v211, v214, v215\n" \
"buffer_store_dwordx4 v[208:211], v20, s[56:59], s97 offen offset:1024 sc1\n" \
"s_add_u32 s97, s97, 0x2000\n" \
"v_mfma_f32_16x16x32_bf16 v[220:223], a[208:211], a[116:119], v[220:223]\n" \
"ds_read_b64_tr_b16 a[208:209], v33 offset:37024\n" \
"ds_read_b64_tr_b16 a[210:211], v33 offset:41632\n" \
"v_mfma_f32_16x16x32_bf16 v[216:219], a[212:215], a[104:107], v[216:219]\n" \
"v_mfma_f32_16x16x32_bf16 v[220:223], a[212:215], a[120:123], v[220:223]\n" \
"ds_read_b64_tr_b16 a[212:213], v33 offset:46240\n" \
"ds_read_b64_tr_b16 a[214:215], v33 offset:50848\n" \
"v_mfma_f32_16x16x32_bf16 v[216:219], a[216:219], a[108:111], v[216:219]\n" \
"v_mfma_f32_16x16x32_bf16 v[220:223], a[216:219], a[124:127], v[220:223]\n" \
"ds_read_b64_tr_b16 a[216:217], v33 offset:55456\n" \
"ds_read_b64_tr_b16 a[218:219], v33 offset:60064\n" \
"v_mfma_f32_16x16x32_bf16 v[216:219], a[0:3], a[160:163], v[216:219]\n" \
"v_mfma_f32_16x16x32_bf16 v[220:223], a[0:3], a[176:179], v[220:223]\n" \
"ds_read_b64_tr_b16 a[0:1], v10 offset:160\n" \
"ds_read_b64_tr_b16 a[2:3], v10 offset:4768\n" \
"v_mfma_f32_16x16x32_bf16 v[216:219], a[4:7], a[164:167], v[216:219]\n" \
"v_mfma_f32_16x16x32_bf16 v[220:223], a[4:7], a[180:183], v[220:223]\n" \
"ds_read_b64_tr_b16 a[4:5], v34 offset:160\n" \
"s_waitcnt lgkmcnt(13)\n" \
"v_mfma_f32_16x16x32_bf16 v[216:219], a[8:11], a[168:171], v[216:219]\n" \
"ds_read_b64_tr_b16 a[6:7], v34 offset:4768\n" \
"v_mfma_f32_16x16x32_bf16 v[220:223], a[8:11], a[184:187], v[220:223]\n" \
"ds_read_b64_tr_b16 a[8:9], v34 offset:9376\n" \
"ds_read_b64_tr_b16 a[10:11], v34 offset:13984\n" \
"v_mfma_f32_16x16x32_bf16 v[216:219], a[12:15], a[172:175], v[216:219]\n" \
"v_mfma_f32_16x16x32_bf16 v[220:223], a[12:15], a[188:191], v[220:223]\n" \
"ds_read_b64_tr_b16 a[12:13], v34 offset:18592\n" \
"ds_read_b64_tr_b16 a[14:15], v34 offset:23200\n" \
"v_mfma_f32_16x16x32_bf16 v[216:219], a[16:19], a[220:223], v[216:219]\n" \
"s_waitcnt vmcnt(6)\n" \
"s_mov_b64 exec, 1\n" \
"buffer_store_dword v39, off, s[56:59], s94 offset:32 sc1\n" \
"s_mov_b64 exec, -1\n" \
"v_mfma_f32_16x16x32_bf16 v[220:223], a[16:19], a[236:239], v[220:223]\n" \
"ds_read_b64_tr_b16 a[16:17], v10 offset:9376\n" \
"ds_read_b64_tr_b16 a[18:19], v10 offset:13984\n" \
"v_mfma_f32_16x16x32_bf16 v[216:219], a[20:23], a[224:227], v[216:219]\n" \
"v_mfma_f32_16x16x32_bf16 v[220:223], a[20:23], a[240:243], v[220:223]\n" \
"ds_read_b64_tr_b16 a[20:21], v33 offset:192\n" \
"ds_read_b64_tr_b16 a[22:23], v33 offset:4800\n" \
"v_mfma_f32_16x16x32_bf16 v[216:219], a[24:27], a[228:231], v[216:219]\n" \
"v_mfma_f32_16x16x32_bf16 v[220:223], a[24:27], a[244:247], v[220:223]\n" \
"ds_read_b64_tr_b16 a[24:25], v33 offset:9408\n" \
"ds_read_b64_tr_b16 a[26:27], v33 offset:14016\n" \
"v_mfma_f32_16x16x32_bf16 v[216:219], a[28:31], a[232:235], v[216:219]\n" \
"v_mfma_f32_16x16x32_bf16 v[220:223], a[28:31], a[248:251], v[220:223]\n" \
"buffer_load_dword v37, v40, s[56:59], s74 offen sc1\n" \
"v_mfma_f32_16x16x32_bf16 v[224:227], a[192:195], a[96:99], v[224:227]\n" \
"ds_read_b64_tr_b16 a[28:29], v33 offset:18624\n" \
"ds_read_b64_tr_b16 a[30:31], v33 offset:23232\n" \
"v_mfma_f32_16x16x32_bf16 v[228:231], a[192:195], a[112:115], v[228:231]\n" \
"ds_read_b64_tr_b16 a[192:193], v33 offset:27840\n" \
"ds_read_b64_tr_b16 a[194:195], v33 offset:32448\n" \
"v_cvt_pk_bf16_f32 v216, v216, v217\n" \
"v_cvt_pk_bf16_f32 v217, v218, v219\n" \
"v_cvt_pk_bf16_f32 v218, v220, v221\n" \
"v_cvt_pk_bf16_f32 v219, v222, v223\n" \
"buffer_store_dwordx4 v[216:219], v20, s[56:59], s64 offen offset:1024 sc1\n" \
"s_add_u32 s64, s64, 0x1800\n" \
"v_mfma_f32_16x16x32_bf16 v[224:227], a[196:199], a[100:103], v[224:227]\n" \
"v_mfma_f32_16x16x32_bf16 v[228:231], a[196:199], a[116:119], v[228:231]\n" \
"ds_read_b64_tr_b16 a[196:197], v33 offset:37056\n" \
"s_waitcnt lgkmcnt(11)\n" \
"v_mfma_f32_16x16x32_bf16 v[224:227], a[200:203], a[104:107], v[224:227]\n" \
"ds_read_b64_tr_b16 a[198:199], v33 offset:41664\n" \
"v_mfma_f32_16x16x32_bf16 v[228:231], a[200:203], a[120:123], v[228:231]\n" \
"ds_read_b64_tr_b16 a[200:201], v33 offset:46272\n" \
"ds_read_b64_tr_b16 a[202:203], v33 offset:50880\n" \
"v_mfma_f32_16x16x32_bf16 v[224:227], a[204:207], a[108:111], v[224:227]\n" \
"v_mfma_f32_16x16x32_bf16 v[228:231], a[204:207], a[124:127], v[228:231]\n" \
"ds_read_b64_tr_b16 a[204:205], v33 offset:55488\n" \
"ds_read_b64_tr_b16 a[206:207], v33 offset:60096\n" \
"v_mfma_f32_16x16x32_bf16 v[224:227], a[208:211], a[160:163], v[224:227]\n" \
"v_mfma_f32_16x16x32_bf16 v[228:231], a[208:211], a[176:179], v[228:231]\n" \
"ds_read_b64_tr_b16 a[208:209], v10 offset:192\n" \
"ds_read_b64_tr_b16 a[210:211], v10 offset:4800\n" \
"v_mfma_f32_16x16x32_bf16 v[224:227], a[212:215], a[164:167], v[224:227]\n" \
"v_mfma_f32_16x16x32_bf16 v[228:231], a[212:215], a[180:183], v[228:231]\n" \
"ds_read_b64_tr_b16 a[212:213], v34 offset:192\n" \
"ds_read_b64_tr_b16 a[214:215], v34 offset:4800\n" \
"v_mfma_f32_16x16x32_bf16 v[224:227], a[216:219], a[168:171], v[224:227]\n" \
"v_mfma_f32_16x16x32_bf16 v[228:231], a[216:219], a[184:187], v[228:231]\n" \
"ds_read_b64_tr_b16 a[216:217], v34 offset:9408\n" \
"ds_read_b64_tr_b16 a[218:219], v34 offset:14016\n" \
"v_mfma_f32_16x16x32_bf16 v[224:227], a[0:3], a[172:175], v[224:227]\n" \
"v_mfma_f32_16x16x32_bf16 v[228:231], a[0:3], a[188:191], v[228:231]\n" \
"ds_read_b64_tr_b16 a[0:1], v34 offset:18624\n" \
"ds_read_b64_tr_b16 a[2:3], v34 offset:23232\n" \
"v_mfma_f32_16x16x32_bf16 v[224:227], a[4:7], a[220:223], v[224:227]\n" \
"v_mfma_f32_16x16x32_bf16 v[228:231], a[4:7], a[236:239], v[228:231]\n" \
"ds_read_b64_tr_b16 a[4:5], v10 offset:9408\n" \
"ds_read_b64_tr_b16 a[6:7], v10 offset:14016\n" \
"s_waitcnt vmcnt(6)\n" \
"s_mov_b64 exec, 1\n" \
"buffer_store_dword v39, off, s[56:59], s95 offset:36 sc1\n" \
"s_mov_b64 exec, -1\n" \
"v_mfma_f32_16x16x32_bf16 v[224:227], a[8:11], a[224:227], v[224:227]\n" \
"v_mfma_f32_16x16x32_bf16 v[228:231], a[8:11], a[240:243], v[228:231]\n" \
"ds_read_b64_tr_b16 a[8:9], v33 offset:224\n" \
"ds_read_b64_tr_b16 a[10:11], v33 offset:4832\n" \
"v_mfma_f32_16x16x32_bf16 v[224:227], a[12:15], a[228:231], v[224:227]\n" \
"v_mfma_f32_16x16x32_bf16 v[228:231], a[12:15], a[244:247], v[228:231]\n" \
"ds_read_b64_tr_b16 a[12:13], v33 offset:9440\n" \
"s_waitcnt lgkmcnt(13)\n" \
"v_mfma_f32_16x16x32_bf16 v[224:227], a[16:19], a[232:235], v[224:227]\n" \
"ds_read_b64_tr_b16 a[14:15], v33 offset:14048\n" \
"v_mfma_f32_16x16x32_bf16 v[228:231], a[16:19], a[248:251], v[228:231]\n" \
"ds_read_b64_tr_b16 a[16:17], v33 offset:18656\n" \
"ds_read_b64_tr_b16 a[18:19], v33 offset:23264\n" \
"v_mfma_f32_16x16x32_bf16 v[232:235], a[20:23], a[96:99], v[232:235]\n" \
"v_mfma_f32_16x16x32_bf16 v[236:239], a[20:23], a[112:115], v[236:239]\n" \
"ds_read_b64_tr_b16 a[20:21], v33 offset:27872\n" \
"ds_read_b64_tr_b16 a[22:23], v33 offset:32480\n" \
"v_cvt_pk_bf16_f32 v224, v224, v225\n" \
"v_cvt_pk_bf16_f32 v225, v226, v227\n" \
"v_cvt_pk_bf16_f32 v226, v228, v229\n" \
"v_cvt_pk_bf16_f32 v227, v230, v231\n" \
"buffer_store_dwordx4 v[224:227], v20, s[56:59], s73 offen offset:1024 sc1\n" \
"v_mfma_f32_16x16x32_bf16 v[232:235], a[24:27], a[100:103], v[232:235]\n" \
"v_mfma_f32_16x16x32_bf16 v[236:239], a[24:27], a[116:119], v[236:239]\n" \
"ds_read_b64_tr_b16 a[24:25], v33 offset:37088\n" \
"ds_read_b64_tr_b16 a[26:27], v33 offset:41696\n" \
"v_mfma_f32_16x16x32_bf16 v[232:235], a[28:31], a[104:107], v[232:235]\n" \
"v_mfma_f32_16x16x32_bf16 v[236:239], a[28:31], a[120:123], v[236:239]\n" \
"ds_read_b64_tr_b16 a[28:29], v33 offset:46304\n" \
"ds_read_b64_tr_b16 a[30:31], v33 offset:50912\n" \
"v_mfma_f32_16x16x32_bf16 v[232:235], a[192:195], a[108:111], v[232:235]\n" \
"v_mfma_f32_16x16x32_bf16 v[236:239], a[192:195], a[124:127], v[236:239]\n" \
"ds_read_b64_tr_b16 a[192:193], v33 offset:55520\n" \
"ds_read_b64_tr_b16 a[194:195], v33 offset:60128\n" \
"v_mfma_f32_16x16x32_bf16 v[232:235], a[196:199], a[160:163], v[232:235]\n" \
"v_mfma_f32_16x16x32_bf16 v[236:239], a[196:199], a[176:179], v[236:239]\n" \
"ds_read_b64_tr_b16 a[196:197], v10 offset:224\n" \
"ds_read_b64_tr_b16 a[198:199], v10 offset:4832\n" \
"v_mfma_f32_16x16x32_bf16 v[232:235], a[200:203], a[164:167], v[232:235]\n" \
"v_mfma_f32_16x16x32_bf16 v[236:239], a[200:203], a[180:183], v[236:239]\n" \
"ds_read_b64_tr_b16 a[200:201], v34 offset:224\n" \
"ds_read_b64_tr_b16 a[202:203], v34 offset:4832\n" \
"v_mfma_f32_16x16x32_bf16 v[232:235], a[204:207], a[168:171], v[232:235]\n" \
"v_mfma_f32_16x16x32_bf16 v[236:239], a[204:207], a[184:187], v[236:239]\n" \
"ds_read_b64_tr_b16 a[204:205], v34 offset:9440\n" \
"s_waitcnt lgkmcnt(13)\n" \
"v_mfma_f32_16x16x32_bf16 v[232:235], a[208:211], a[172:175], v[232:235]\n" \
"ds_read_b64_tr_b16 a[206:207], v34 offset:14048\n" \
"v_mfma_f32_16x16x32_bf16 v[236:239], a[208:211], a[188:191], v[236:239]\n" \
"ds_read_b64_tr_b16 a[208:209], v34 offset:18656\n" \
"ds_read_b64_tr_b16 a[210:211], v34 offset:23264\n" \
"v_mfma_f32_16x16x32_bf16 v[232:235], a[212:215], a[220:223], v[232:235]\n" \
"v_mfma_f32_16x16x32_bf16 v[236:239], a[212:215], a[236:239], v[236:239]\n" \
"ds_read_b64_tr_b16 a[212:213], v10 offset:9440\n" \
"ds_read_b64_tr_b16 a[214:215], v10 offset:14048\n" \
"s_waitcnt vmcnt(7)\n" \
"s_mov_b64 exec, 1\n" \
"buffer_store_dword v39, off, s[56:59], s96 offset:40 sc1\n" \
"s_mov_b64 exec, -1\n" \
"v_mfma_f32_16x16x32_bf16 v[232:235], a[216:219], a[224:227], v[232:235]\n" \
"v_mfma_f32_16x16x32_bf16 v[236:239], a[216:219], a[240:243], v[236:239]\n" \
"ds_read_b64_tr_b16 a[216:217], v36 offset:0\n" \
"ds_read_b64_tr_b16 a[218:219], v36 offset:1024\n" \
"v_mfma_f32_16x16x32_bf16 v[232:235], a[0:3], a[228:231], v[232:235]\n" \
"v_mfma_f32_16x16x32_bf16 v[236:239], a[0:3], a[244:247], v[236:239]\n" \
"ds_read_b64_tr_b16 a[0:1], v36 offset:2048\n" \
"ds_read_b64_tr_b16 a[2:3], v36 offset:3072\n" \
"v_mfma_f32_16x16x32_bf16 v[232:235], a[4:7], a[232:235], v[232:235]\n" \
"v_mfma_f32_16x16x32_bf16 v[236:239], a[4:7], a[248:251], v[236:239]\n" \
"ds_read_b64_tr_b16 a[4:5], v36 offset:32\n" \
"ds_read_b64_tr_b16 a[6:7], v36 offset:1056\n" \
"v_mfma_f32_16x16x32_bf16 v[240:243], a[8:11], a[96:99], v[240:243]\n" \
"v_mfma_f32_16x16x32_bf16 v[244:247], a[8:11], a[112:115], v[244:247]\n" \
"ds_read_b64_tr_b16 a[8:9], v36 offset:2080\n" \
"ds_read_b64_tr_b16 a[10:11], v36 offset:3104\n" \
"v_mfma_f32_16x16x32_bf16 v[240:243], a[12:15], a[100:103], v[240:243]\n" \
"v_cvt_pk_bf16_f32 v232, v232, v233\n" \
"v_cvt_pk_bf16_f32 v233, v234, v235\n" \
"v_cvt_pk_bf16_f32 v234, v236, v237\n" \
"v_cvt_pk_bf16_f32 v235, v238, v239\n" \
"buffer_store_dwordx4 v[232:235], v20, s[56:59], s92 offen offset:1024 sc1\n" \
"s_waitcnt vmcnt(5)\n" \
"v_cmp_eq_u32_e64 s[98:99], s62, v37\n" \
"s_mov_b64 exec, 1\n" \
"buffer_store_dword v39, off, s[56:59], s97 offset:44 sc1\n" \
"s_mov_b64 exec, -1\n" \
"v_mfma_f32_16x16x32_bf16 v[244:247], a[12:15], a[116:119], v[244:247]\n" \
"s_or_b32 s75, 0x7f, s100\n" \
"s_andn2_b32 s75, s75, s98\n" \
"s_cbranch_scc1 L_sx0_%=\n" \
"L_rx0_%=:\n" \
"buffer_load_dword v37, v40, s[56:59], s74 offen sc1\n" \
"s_add_u32 s65, s63, 0x0\n" \
"s_add_u32 s66, s63, 0x800\n" \
"s_add_u32 s67, s63, 0x1000\n" \
"s_add_u32 s68, s63, 0x1800\n" \
"s_add_u32 s69, s63, 0x2000\n" \
"s_add_u32 s70, s63, 0x2800\n" \
"s_add_u32 s71, s63, 0x3000\n" \
"s_add_u32 s73, s73, 0x1000\n" \
"s_add_u32 s92, s92, 0x800\n" \
"v_mfma_f32_16x16x32_bf16 v[240:243], a[16:19], a[104:107], v[240:243]\n" \
"v_mfma_f32_16x16x32_bf16 v[244:247], a[16:19], a[120:123], v[244:247]\n" \
"buffer_load_dwordx4 v[116:119], v20, s[56:59], s65 offen offset:0 sc1\n" \
"s_waitcnt lgkmcnt(5)\n" \
"v_mfma_f32_16x16x32_bf16 v[240:243], a[20:23], a[108:111], v[240:243]\n" \
"v_mfma_f32_16x16x32_bf16 v[244:247], a[20:23], a[124:127], v[244:247]\n" \
"v_mfma_f32_16x16x32_bf16 v[168:171], a[24:27], a[128:131], v[168:171]\n" \
"v_mfma_f32_16x16x32_bf16 v[172:175], a[24:27], a[144:147], v[172:175]\n" \
"buffer_load_dwordx4 v[124:127], v20, s[56:59], s66 offen offset:0 sc1\n" \
"v_mfma_f32_16x16x32_bf16 v[240:243], a[24:27], a[160:163], v[240:243]\n" \
"v_readlane_b32 s66, v11, 53\n" \
"v_mfma_f32_16x16x32_bf16 v[244:247], a[24:27], a[176:179], v[244:247]\n" \
"v_mfma_f32_16x16x32_bf16 v[168:171], a[28:31], a[132:135], v[168:171]\n" \
"v_mfma_f32_16x16x32_bf16 v[172:175], a[28:31], a[148:151], v[172:175]\n" \
"buffer_load_dwordx4 v[132:135], v20, s[56:59], s67 offen offset:0 sc1\n" \
"v_mfma_f32_16x16x32_bf16 v[240:243], a[28:31], a[164:167], v[240:243]\n" \
"v_readlane_b32 s67, v11, 44\n" \
"v_mfma_f32_16x16x32_bf16 v[244:247], a[28:31], a[180:183], v[244:247]\n" \
"v_mfma_f32_16x16x32_bf16 v[168:171], a[192:195], a[136:139], v[168:171]\n" \
"v_mfma_f32_16x16x32_bf16 v[172:175], a[192:195], a[152:155], v[172:175]\n" \
"buffer_load_dwordx4 v[140:143], v20, s[56:59], s68 offen offset:0 sc1\n" \
"v_mfma_f32_16x16x32_bf16 v[240:243], a[192:195], a[168:171], v[240:243]\n" \
"v_readlane_b32 s68, v11, 42\n" \
"v_mfma_f32_16x16x32_bf16 v[244:247], a[192:195], a[184:187], v[244:247]\n" \
"v_mfma_f32_16x16x32_bf16 v[168:171], a[196:199], a[140:143], v[168:171]\n" \
"v_mfma_f32_16x16x32_bf16 v[172:175], a[196:199], a[156:159], v[172:175]\n" \
"buffer_load_dwordx4 v[148:151], v20, s[56:59], s69 offen offset:0 sc1\n" \
"v_mfma_f32_16x16x32_bf16 v[240:243], a[196:199], a[172:175], v[240:243]\n" \
"v_readlane_b32 s69, v11, 43\n" \
"v_mfma_f32_16x16x32_bf16 v[244:247], a[196:199], a[188:191], v[244:247]\n" \
"v_mfma_f32_16x16x32_bf16 v[168:171], a[200:203], a[32:35], v[168:171]\n" \
"v_mfma_f32_16x16x32_bf16 v[172:175], a[200:203], a[48:51], v[172:175]\n" \
"buffer_load_dwordx4 v[156:159], v20, s[56:59], s70 offen offset:0 sc1\n" \
"v_mfma_f32_16x16x32_bf16 v[240:243], a[200:203], a[220:223], v[240:243]\n" \
"v_mfma_f32_16x16x32_bf16 v[244:247], a[200:203], a[236:239], v[244:247]\n" \
"v_mfma_f32_16x16x32_bf16 v[168:171], a[204:207], a[36:39], v[168:171]\n" \
"v_mfma_f32_16x16x32_bf16 v[172:175], a[204:207], a[52:55], v[172:175]\n" \
"buffer_load_dwordx4 v[164:167], v20, s[56:59], s71 offen offset:0 sc1\n" \
"v_mfma_f32_16x16x32_bf16 v[240:243], a[204:207], a[224:227], v[240:243]\n" \
"v_mfma_f32_16x16x32_bf16 v[244:247], a[204:207], a[240:243], v[244:247]\n" \
"v_mfma_f32_16x16x32_bf16 v[168:171], a[208:211], a[40:43], v[168:171]\n" \
"v_mfma_f32_16x16x32_bf16 v[172:175], a[208:211], a[56:59], v[172:175]\n" \
"buffer_load_dwordx4 v[112:115], v20, s[56:59], s72 offen offset:0 sc1\n" \
"v_mfma_f32_16x16x32_bf16 v[240:243], a[208:211], a[228:231], v[240:243]\n" \
"v_mfma_f32_16x16x32_bf16 v[244:247], a[208:211], a[244:247], v[244:247]\n" \
"v_mfma_f32_16x16x32_bf16 v[168:171], a[212:215], a[44:47], v[168:171]\n" \
"v_mfma_f32_16x16x32_bf16 v[172:175], a[212:215], a[60:63], v[172:175]\n" \
"buffer_load_dwordx4 v[184:187], v20, s[56:59], s72 offen offset:2048 sc1\n" \
"s_waitcnt vmcnt(15)\n" \
"s_mov_b64 exec, 1\n" \
"buffer_store_dword v39, off, s[56:59], s64 offset:48 sc1\n" \
"s_mov_b64 exec, -1\n" \
"s_add_u32 s72, s72, 0x1000\n" \
"v_mfma_f32_16x16x32_bf16 v[240:243], a[212:215], a[232:235], v[240:243]\n" \
"v_mfma_f32_16x16x32_bf16 v[244:247], a[212:215], a[248:251], v[244:247]\n" \
"v_mfma_f32_16x16x32_bf16 v[168:171], a[216:219], v[64:67], v[168:171]\n" \
"v_mfma_f32_16x16x32_bf16 v[172:175], a[216:219], v[72:75], v[172:175]\n" \
"buffer_load_dwordx4 v[120:123], v20, s[56:59], s72 offen offset:0 sc1\n" \
"v_mfma_f32_16x16x32_bf16 v[240:243], a[216:219], v[80:83], v[240:243]\n" \
".fill (L_ab + 32768 - .) / 4, 4, 0xBF800000\n" \
"L_ap8:\n" \
"s_cbranch_execz L_ap0\n" \
"v_mfma_f32_16x16x32_bf16 v[244:247], a[216:219], v[88:91], v[244:247]\n" \
"s_waitcnt lgkmcnt(0)\n" \
"v_mfma_f32_16x16x32_bf16 v[168:171], a[0:3], v[68:71], v[168:171]\n" \
"v_mfma_f32_16x16x32_bf16 v[172:175], a[0:3], v[76:79], v[172:175]\n" \
"buffer_load_dwordx4 v[192:195], v20, s[56:59], s72 offen offset:2048 sc1\n" \
"s_add_u32 s72, s72, 0x1000\n" \
"v_mfma_f32_16x16x32_bf16 v[240:243], a[0:3], v[84:87], v[240:243]\n" \
"v_mfma_f32_16x16x32_bf16 v[244:247], a[0:3], v[92:95], v[244:247]\n" \
"v_mfma_f32_16x16x32_bf16 v[56:59], a[4:7], v[96:99], 0\n" \
"v_mfma_f32_16x16x32_bf16 v[60:63], a[4:7], v[104:107], 0\n" \
"buffer_load_dwordx4 v[128:131], v20, s[56:59], s72 offen offset:0 sc1\n" \
"v_mfma_f32_16x16x32_bf16 v[56:59], a[8:11], v[100:103], v[56:59]\n" \
"v_mfma_f32_16x16x32_bf16 v[60:63], a[8:11], v[108:111], v[60:63]\n" \
"buffer_load_dwordx4 v[200:203], v20, s[56:59], s72 offen offset:2048 sc1\n" \
"s_add_u32 s72, s72, 0x1000\n" \
"buffer_load_dwordx4 v[176:179], v20, s[56:59], s72 offen offset:0 sc1\n" \
"buffer_load_dwordx4 v[248:251], v20, s[56:59], s72 offen offset:2048 sc1\n" \
"s_waitcnt vmcnt(15)\n" \
"v_mfma_f32_16x16x32_bf16 v[168:171], v[50:53], v[116:119], v[168:171]\n" \
"s_mov_b64 exec, 1\n" \
"buffer_store_dword v39, off, s[56:59], s73 offset:52 sc1\n" \
"s_mov_b64 exec, -1\n" \
"s_mov_b64 exec, 1\n" \
"buffer_store_dword v39, off, s[56:59], s92 offset:56 sc1\n" \
"s_mov_b64 exec, -1\n" \
"v_cmp_eq_u32_e64 s[98:99], s62, v37\n" \
"v_mfma_f32_16x16x32_bf16 v[172:175], v[48:51], v[116:119], v[172:175]\n" \
"s_andn2_b32 s75, 0x100, s98\n" \
"s_cbranch_scc1 L_sx1_%=\n" \
"L_rx1_%=:\n" \
"s_add_u32 s93, s63, 0x0\n" \
"s_waitcnt vmcnt(16)\n" \
"v_mfma_f32_16x16x32_bf16 v[168:171], v[50:53], v[124:127], v[168:171]\n" \
"v_mfma_f32_16x16x32_bf16 v[172:175], v[48:51], v[124:127], v[172:175]\n" \
"buffer_load_dwordx4 v[188:191], v20, s[56:59], s93 offen offset:1024 sc1\n" \
"s_andn2_b32 s75, 0x200, s98\n" \
"s_cbranch_scc1 L_sx2_%=\n" \
"L_rx2_%=:\n" \
"s_add_u32 s94, s63, 0x800\n" \
"s_waitcnt vmcnt(16)\n" \
"v_mfma_f32_16x16x32_bf16 v[168:171], v[50:53], v[132:135], v[168:171]\n" \
"v_mfma_f32_16x16x32_bf16 v[172:175], v[48:51], v[132:135], v[172:175]\n" \
"buffer_load_dwordx4 v[196:199], v20, s[56:59], s94 offen offset:1024 sc1\n" \
"s_andn2_b32 s75, 0x400, s98\n" \
"s_cbranch_scc1 L_sx3_%=\n" \
"L_rx3_%=:\n" \
"s_add_u32 s95, s63, 0x1000\n" \
"s_waitcnt vmcnt(16)\n" \
"v_mfma_f32_16x16x32_bf16 v[168:171], v[50:53], v[140:143], v[168:171]\n" \
"v_mfma_f32_16x16x32_bf16 v[172:175], v[48:51], v[140:143], v[172:175]\n" \
"buffer_load_dwordx4 v[204:207], v20, s[56:59], s95 offen offset:1024 sc1\n" \
"s_andn2_b32 s75, 0x800, s98\n" \
"s_cbranch_scc1 L_sx4_%=\n" \
"L_rx4_%=:\n" \
"s_add_u32 s96, s63, 0x1800\n" \
"s_waitcnt vmcnt(16)\n" \
"v_mfma_f32_16x16x32_bf16 v[168:171], v[50:53], v[148:151], v[168:171]\n" \
"v_mfma_f32_16x16x32_bf16 v[172:175], v[48:51], v[148:151], v[172:175]\n" \
"buffer_load_dwordx4 v[212:215], v20, s[56:59], s96 offen offset:1024 sc1\n" \
"s_andn2_b32 s75, 0x1000, s98\n" \
"s_cbranch_scc1 L_sx5_%=\n" \
"L_rx5_%=:\n" \
"s_add_u32 s97, s63, 0x2000\n" \
"s_waitcnt vmcnt(16)\n" \
"v_mfma_f32_16x16x32_bf16 v[168:171], v[50:53], v[156:159], v[168:171]\n" \
"v_mfma_f32_16x16x32_bf16 v[172:175], v[48:51], v[156:159], v[172:175]\n" \
"buffer_load_dwordx4 v[220:223], v20, s[56:59], s97 offen offset:1024 sc1\n" \
"s_andn2_b32 s75, 0x2000, s98\n" \
"s_cbranch_scc1 L_sx6_%=\n" \
"L_rx6_%=:\n" \
"s_add_u32 s64, s63, 0x2800\n" \
"s_waitcnt vmcnt(16)\n" \
"v_mfma_f32_16x16x32_bf16 v[168:171], v[50:53], v[164:167], v[168:171]\n" \
"v_mfma_f32_16x16x32_bf16 v[172:175], v[48:51], v[164:167], v[172:175]\n" \
"buffer_load_dwordx4 v[228:231], v20, s[56:59], s64 offen offset:1024 sc1\n" \
"s_andn2_b32 s75, 0x4000, s98\n" \
"s_cbranch_scc1 L_sx7_%=\n" \
"L_rx7_%=:\n" \
"s_add_u32 s65, s63, 0x3000\n" \
"s_waitcnt vmcnt(16)\n" \
"v_mfma_f32_16x16x32_bf16 v[56:59], v[50:53], v[112:115], v[56:59]\n" \
"v_mfma_f32_16x16x32_bf16 v[60:63], v[48:51], v[112:115], v[60:63]\n" \
"buffer_load_dwordx4 v[236:239], v20, s[56:59], s65 offen offset:1024 sc1\n" \
"v_cvt_pk_bf16_f32 v168, v168, v169\n" \
"v_cvt_pk_bf16_f32 v172, v172, v173\n" \
"v_cvt_pk_bf16_f32 v169, v170, v171\n" \
"v_cvt_pk_bf16_f32 v173, v174, v175\n" \
"buffer_store_dwordx2 v[168:169], v42, s[88:91], s68 offen sc1\n" \
"buffer_store_dwordx2 v[172:173], v43, s[88:91], s68 offen sc1\n" \
"s_waitcnt vmcnt(18)\n" \
"v_mfma_f32_16x16x32_bf16 v[56:59], v[50:53], v[184:187], v[56:59]\n" \
"v_mfma_f32_16x16x32_bf16 v[60:63], v[48:51], v[184:187], v[60:63]\n" \
"s_waitcnt vmcnt(16)\n" \
"v_mfma_f32_16x16x32_bf16 v[56:59], v[50:53], v[120:123], v[56:59]\n" \
"v_mfma_f32_16x16x32_bf16 v[60:63], v[48:51], v[120:123], v[60:63]\n" \
"s_waitcnt vmcnt(15)\n" \
"v_mfma_f32_16x16x32_bf16 v[56:59], v[50:53], v[192:195], v[56:59]\n" \
"v_mfma_f32_16x16x32_bf16 v[60:63], v[48:51], v[192:195], v[60:63]\n" \
"s_waitcnt vmcnt(14)\n" \
"v_mfma_f32_16x16x32_bf16 v[56:59], v[50:53], v[128:131], v[56:59]\n" \
"v_mfma_f32_16x16x32_bf16 v[60:63], v[48:51], v[128:131], v[60:63]\n" \
"s_waitcnt vmcnt(13)\n" \
"v_mfma_f32_16x16x32_bf16 v[56:59], v[50:53], v[200:203], v[56:59]\n" \
"v_mfma_f32_16x16x32_bf16 v[60:63], v[48:51], v[200:203], v[60:63]\n" \
"s_waitcnt vmcnt(12)\n" \
"v_mfma_f32_16x16x32_bf16 v[56:59], v[50:53], v[176:179], v[56:59]\n" \
"v_mfma_f32_16x16x32_bf16 v[60:63], v[48:51], v[176:179], v[60:63]\n" \
"s_waitcnt vmcnt(11)\n" \
"v_mfma_f32_16x16x32_bf16 v[56:59], v[50:53], v[248:251], v[56:59]\n" \
"v_mfma_f32_16x16x32_bf16 v[60:63], v[48:51], v[248:251], v[60:63]\n" \
"s_mov_b64 exec, 1\n" \
"buffer_store_dword v46, off, s[56:59], s66 offset:0 sc1\n" \
"s_mov_b64 exec, -1\n" \
"s_waitcnt vmcnt(9)\n" \
"v_mfma_f32_16x16x32_bf16 v[240:243], v[50:53], v[188:191], v[240:243]\n" \
"v_mfma_f32_16x16x32_bf16 v[244:247], v[48:51], v[188:191], v[244:247]\n" \
"s_waitcnt vmcnt(8)\n" \
"v_mfma_f32_16x16x32_bf16 v[240:243], v[50:53], v[196:199], v[240:243]\n" \
"v_cvt_pk_bf16_f32 v56, v56, v57\n" \
"v_cvt_pk_bf16_f32 v60, v60, v61\n" \
"v_mfma_f32_16x16x32_bf16 v[244:247], v[48:51], v[196:199], v[244:247]\n" \
"v_cvt_pk_bf16_f32 v57, v58, v59\n" \
"v_cvt_pk_bf16_f32 v61, v62, v63\n" \
"buffer_store_dwordx2 v[56:57], v42, s[88:91], s67 offen sc1\n" \
"buffer_store_dwordx2 v[60:61], v43, s[88:91], s67 offen sc1\n" \
"s_waitcnt vmcnt(9)\n" \
"v_mfma_f32_16x16x32_bf16 v[240:243], v[50:53], v[204:207], v[240:243]\n" \
"v_mfma_f32_16x16x32_bf16 v[244:247], v[48:51], v[204:207], v[244:247]\n" \
"s_waitcnt vmcnt(8)\n" \
"v_mfma_f32_16x16x32_bf16 v[240:243], v[50:53], v[212:215], v[240:243]\n" \
"v_mfma_f32_16x16x32_bf16 v[244:247], v[48:51], v[212:215], v[244:247]\n" \
"s_waitcnt vmcnt(7)\n" \
"v_mfma_f32_16x16x32_bf16 v[240:243], v[50:53], v[220:223], v[240:243]\n" \
"v_mfma_f32_16x16x32_bf16 v[244:247], v[48:51], v[220:223], v[244:247]\n" \
"s_waitcnt vmcnt(6)\n" \
"v_mfma_f32_16x16x32_bf16 v[240:243], v[50:53], v[228:231], v[240:243]\n" \
"v_mfma_f32_16x16x32_bf16 v[244:247], v[48:51], v[228:231], v[244:247]\n" \
"s_waitcnt vmcnt(5)\n" \
"v_mfma_f32_16x16x32_bf16 v[240:243], v[50:53], v[236:239], v[240:243]\n" \
"v_mfma_f32_16x16x32_bf16 v[244:247], v[48:51], v[236:239], v[244:247]\n" \
"s_nop 6\n" \
"v_cvt_pk_bf16_f32 v240, v240, v241\n" \
"v_cvt_pk_bf16_f32 v244, v244, v245\n" \
"v_cvt_pk_bf16_f32 v241, v242, v243\n" \
"v_cvt_pk_bf16_f32 v245, v246, v247\n" \
"buffer_store_dwordx2 v[240:241], v42, s[88:91], s69 offen sc1\n" \
"buffer_store_dwordx2 v[244:245], v43, s[88:91], s69 offen sc1\n"
#define ATT_VCLOB "v21","v22","v23","v24","v25","v26","v27","v28","v29","v32","v33","v34","v35","v36","v37","v38","v39","v40","v41","v42","v43","v44","v45","v46","v47","v48","v49","v50","v51","v52","v53","v54","v55","v56","v57","v58","v59","v60","v61","v62","v63","v64","v65","v66","v67","v68","v69","v70","v71","v72","v73","v74","v75","v76","v77","v78","v79","v80","v81","v82","v83","v84","v85","v86","v87","v88","v89","v90","v91","v92","v93","v94","v95","v96","v97","v98","v99","v100","v101","v102","v103","v104","v105","v106","v107","v108","v109","v110","v111","v112","v113","v114","v115","v116","v117","v118","v119","v120","v121","v122","v123","v124","v125","v126","v127","v128","v129","v130","v131","v132","v133","v134","v135","v136","v137","v138","v139","v140","v141","v142","v143","v144","v145","v146","v147","v148","v149","v150","v151","v152","v153","v154","v155","v156","v157","v158","v159","v160","v161","v162","v163","v164","v165","v166","v167","v168","v169","v170","v171","v172","v173","v174","v175","v176","v177","v178","v179","v180","v181","v182","v183","v184","v185","v186","v187","v188","v189","v190","v191","v192","v193","v194","v195","v196","v197","v198","v199","v200","v201","v202","v203","v204","v205","v206","v207","v208","v209","v210","v211","v212","v213","v214","v215","v216","v217","v218","v219","v220","v221","v222","v223","v224","v225","v226","v227","v228","v229","v230","v231","v232","v233","v234","v235","v236","v237","v238","v239","v240","v241","v242","v243","v244","v245","v246","v247","v248","v249","v250","v251","v252","v253","v254","v255"
#define ATT_SCLOB "s63","s64","s65","s66","s67","s68","s69","s70","s71","s72","s73","s74","s75","s76","s77","s78","s79","s80","s81","s82","s83","s84","s85","s86","s87","s88","s89","s90","s91","s92","s93","s95","s96","s97","s98","s99","s100","s101"
#define ATT_WTCH 0
enum { T_fo, T_fj, T_fa, T_fas, T_fb, T_fbs, T_f8, T_mk8, T_wt, T_wol, T_woh, T_wbl, T_wbh, T_outl, T_outh, T_r1l, T_r1h, T_r1n, T_r1s, T_m0, T_m1, T_mw, T_m3, T_m4, T_ml, T_w0, T_w1, T_w2, T_w3, T_A1, T_A2, T_A3, T_B1, T_B2, T_B3, T_Ao, T_Bo, T_Al, T_Bl, T_l8, T_xr, T_x8r, T_oA, T_oB, T_o8, T_x0, T_x1, T_x2, T_x3, T_x4, T_x5, T_x6, T_x8, T_ep };

__device__ __forceinline__ void out_consts(u32x4 (&vp)[3], const u16* wo, const u16* wob, const u16* out, const unsigned b, const unsigned w, const unsigned lane) {
    const unsigned sp = b & 7u, nt = (b >> 3) & 3u, j = b >> 5, head = 4u * sp + nt, OOR = 0x40000000u;
    unsigned vw[9];
#pragma unroll
    for (unsigned i = 0; i < 9u; ++i) {
        const unsigned q = 64u * i + lane, row = __umul24(q, 3641u) >> 16, c = q - __umul24(row, 18u);
        vw[i] = __umul24(row, DM * 2) + (c < 16u ? (c * 16u + sp * 32u + 32u) & 255u : c * 16u) - (i & 3u) * 1024u;
    }
    const unsigned row = 16u * w + (lane >> 2), r9 = __umul24(16u * (row >> 5) + (row & 12u) + 2u * ((row >> 4) & 1u) + ((row >> 1) & 1u), 18u) + 16u + (row & 1u);
    unsigned vwl = __umul24(r9, DM * 2) + ((lane & 2u) ? 256u : sp * 32u) + (lane & 1u) * 16u, vrd = __umul24(lane >> 2, 288u) + (lane & 3u) * 8u;
    unsigned tab = 0;
#define TB(n, v) asm("v_writelane_b32 %0, %1, %2" : "+v"(tab) : "s"((unsigned)(v)), "n"((int)T_##n))
    const unsigned FJ = (unsigned)WS_F + (F_J + 640u * j) * 4u, mt = 2u * (w & 2u) + (w & 1u), w2 = w < 2u ? w : 2u, hw = head * 147456u;
    const unsigned FR = (unsigned)WS_F + F_R * 4u + (8u * j + sp) * 1024u;
    TB(fo, FJ + 4u * head); TB(fj, FJ); TB(fa, FR + (w | 2u) * 256u); TB(fas, FR + w * 256u + nt * 4u);
    TB(fb, FR + 128u + w * 256u); TB(fbs, FR + 128u + w * 256u + nt * 4u);
    TB(ep, ssel(b + w, OOR, (unsigned)WS_F + F_EPOCH * 4u));
    const u16* pwo = wo + nt * 144u; const u16* pwb = wob + nt * 144u;
    TB(wol, PLO(pwo)); TB(woh, PHI(pwo) & 0xffffu); TB(wbl, PLO(pwb)); TB(wbh, PHI(pwb) & 0xffffu); TB(outl, PLO(out)); TB(outh, PHI(out) & 0xffffu); TB(r1l, sslt<3>(w, PLO(pwo), PLO(pwb))); TB(r1h, sslt<3>(w, PHI(pwo), PHI(pwb)) & 0xffffu);
    TB(r1n, sslt<3>(w, 72u * sp + 71u, 4095u) * 1152u + 288u); TB(r1s, sslt<3>(w, (72u * sp + 32u * w) * 1152u, hw + 110592u));
    TB(m0, 131072u + 9216u * w2); TB(w0, hw + 36864u * w2); TB(m1, 24576u + 9216u * w); TB(mw, 65536u + 9216u * w); TB(m3, sslt<3>(w, 102400u + 9216u * w, 0u)); TB(m4, sslt<3>(w, 131072u + 9216u * w, 9216u)); TB(ml, 61440u + 1024u * w);
    const unsigned tA = (unsigned)WS_AF + (8u * j + mt) * (KS_TOT * 1024u), tB = (unsigned)WS_AF + (120u - 8u * j + mt) * (KS_TOT * 1024u);
#define HK(k) (4u * sp + ((nt + k) & 3u))
    TB(w1, HK(1u) * 147456u + w * 36864u); TB(w2, HK(2u) * 147456u + w * 36864u); TB(w3, HK(3u) * 147456u + w * 36864u);
    TB(A1, tA + 18432u + HK(1u) * 4096u); TB(A2, tA + 18432u + HK(2u) * 4096u); TB(A3, tA + 18432u + HK(3u) * 4096u);
    TB(B1, tB + 18432u + HK(1u) * 4096u); TB(B2, tB + 18432u + HK(2u) * 4096u); TB(B3, tB + 18432u + HK(3u) * 4096u);
    TB(Ao, tA + sp * 2048u); TB(Bo, tB + sp * 2048u); TB(Al, tA + 16384u); TB(Bl, tB + 16384u);
    const unsigned nred = w ^ (sp >> 1), blk = sp & 1u;
    const unsigned xg = (unsigned)WS_X + (4u * j + nt) * 589824u + w * 16384u, oA = (128u * j + 16u * mt) * 1152u + nt * 288u, oB = (1920u - 128u * j + 16u * mt) * 1152u + nt * 288u;
    TB(l8, ssel(nred, OOR, ssel(blk, tB, tA) + 16384u)); TB(xr, xg + sp * 65536u); TB(x8r, ssel(nred, OOR, xg + 524288u + (sp & 1u) * 1024u));
    TB(oA, oA + sp * 32u); TB(oB, oB + sp * 32u); TB(o8, ssel(nred, OOR, ssel(blk, oB, oA) + 256u)); TB(x8, xg + 524288u + sp * 2048u);
    TB(f8, xg + w * 131072u + 14400u + sp * 4u); TB(mk8, ssel(nred, 0u, 0xff0000u)); TB(wt, hw + j * 18432u + w * 4608u);
#define XI(i) TB(x##i, xg + ((sp + 1u + i) & 7u) * 65536u + i * 2048u)
    XI(0); XI(1); XI(2); XI(3); XI(4); XI(5); XI(6);
#undef XI
#undef HK
#undef TB
    vp[0] = (u32x4){vw[0], vw[1], vw[2], vw[3]}; vp[1] = (u32x4){vw[4], vw[5], vw[6], vw[7]}; vp[2] = (u32x4){vw[8], vwl, vrd, tab};
}
__global__ __launch_bounds__(256, 1) __attribute__((aligned(4096))) void fused_kernel(const u16* __restrict__ x, const u16* __restrict__ ckv, const u16* __restrict__ wq, const u16* __restrict__ wuk,
                                                       const u16* __restrict__ wuv, const u16* __restrict__ wqkv, const u16* __restrict__ gw,
                                                       const u16* __restrict__ wo, const u16* __restrict__ wob, char* __restrict__ ws_, u16* __restrict__ out) {
    using namespace att;
    extern __shared__ __attribute__((aligned(16))) char lds[];
    u32x4 sa[5]; u32x2 s24, s28; char* ws; unsigned tdum; kaptr ka = (kaptr)__builtin_amdgcn_kernarg_segment_ptr();
    asm volatile("s_load_dwordx8 s[8:15], %[ka], 0x0\n\ts_load_dwordx8 s[16:23], %[ka], 0x20\n\ts_getpc_b64 s[4:5]\n\tv_lshlrev_b32_e32 v191, 7, %[t]\n\ts_bfe_u32 s6, %[b], 0x20003\n\t"
                 "v_lshl_add_u32 v191, s6, 15, v191\n\tv_min_u32_e32 v191, %[e], v191\n\tglobal_load_dword v191, v191, s[4:5]\n\ts_load_dwordx4 s[24:27], %[ka], 0x40\n\ts_load_dwordx2 s[28:29], %[ka], 0x50"
                 : "={s[8:11]}"(sa[0]), "={s[12:15]}"(sa[1]), "={s[16:19]}"(sa[2]), "={s[20:23]}"(sa[3]), "={s[24:25]}"(s24), "={s[26:27]}"(ws), "={s[28:29]}"(s28), "={v191}"(tdum), [ka] "+{s[0:1]}"(ka)
                 : [t] "v"(threadIdx.x), [b] "s"(blockIdx.x), [e] "i"(CT_END) : "s4", "s5", "s6", "scc");
    SBAR();
    const unsigned wid = rfl(threadIdx.x >> 6);
    unsigned tag, vl;
    const int b = blockIdx.x;
    u32x4 vp[5], srdA; u32x2 sd;
    proj_phase(ka, ws, wid, vp, vl, sa, s24, s28, tdum, srdA, sd, tag
    );
    unsigned* const flags = (unsigned*)(ws + WS_F);
    RTS(2);
#define AST(L) do {} while (0)
#define ATT_TAIL "s_endpgm\n"
    unsigned abad;
    asm volatile(ATT_ASM ATT_TAIL "L_end_%=:\n s_nop 1"
                 : "={s94}"(abad)
                 : "{v[0:3]}"(vp[0]), "{v[4:7]}"(vp[1]), "{v[8:11]}"(vp[2]), "{v[12:15]}"(vp[3]), "{v[16:19]}"(vp[4]), "{v20}"(vl), "{s[56:59]}"(srdA), "{s[60:61]}"(sd), "{s62}"(tag),
                   "{s[8:11]}"(sa[0]), "{s[12:15]}"(sa[1]), "{s[16:19]}"(sa[2]), "{s[20:23]}"(sa[3]), "{s24}"(sa[4][0]), "{s3}"(wid)
                 : "memory", "m0", "vcc", "scc", ATT_VCLOB, ATT_SCLOB, ACLOB_LO, ACLOB_HI);
    {
        unsigned b2 = b, w2 = wid;
        asm volatile("s_waitcnt vmcnt(0)" : "+s"(ka), "+s"(b2), "+s"(w2), "+s"(ws) :: "memory");
        const unsigned j2 = b2 >> 5, p2 = w2 & 1u, h2 = (b2 & 7u) * 4u + ((b2 >> 3) & 3u);
#pragma unroll 1
        for (unsigned i = w2 & 2u; i < ((w2 & 2u) ? (abad ? 8u : 4u) : 2u); ++i)
            att_exact(ka[0], ka[2], ws, h2, (i < 4u ? 1920u - 128u * j2 : 128u * j2 - 128u) + 32u * i + 16u * p2, *reinterpret_cast<const float*>(lds + LDS_G + (32u * i + 16u * p2 + (lane_id() & 15u)) * 4u));
        asm volatile("s_waitcnt vmcnt(0)" ::: "memory");
        unsigned* fa = (unsigned*)(ws + WS_F) + F_R + (8u * j2 + (b2 & 7u)) * 256u + (2u + p2) * 64u + ((b2 >> 3) & 3u);
        if (w2 & 2u) st_flag(fa, tag);
        wait_flags(fa, ld_flag(fa), tag);
        const unsigned l2 = lane_id(); u32x4 c[3]; out_consts(c, ka[7], ka[8], ka[10], b2, w2, l2);
        asm volatile("s_waitcnt vmcnt(0) lgkmcnt(0)\n\ts_nop 7\n\ts_branch L_att_stub" :: "{v[0:3]}"(c[0]), "{v[4:7]}"(c[1]), "{v[8:11]}"(c[2]), "{v20}"(l2 * 16u), "{s[56:59]}"(SRDA), "{s60}"(SD(8u * j2, h2, w2)),
                     "{s61}"(SD(120u - 8u * j2, h2, w2)), "{s62}"(tag) : "memory");
    }
    RTS(5);
}

extern "C" void kernel_launch(void* const* d_in, const int* in_sizes, int n_in, void* d_out, int out_size, void* d_ws, size_t ws_size,
                              hipStream_t stream) {
    (void)hipFuncSetAttribute((const void*)fused_kernel, hipFuncAttributeMaxDynamicSharedMemorySize, att::LDS_BYTES);
    fused_kernel<<<dim3(256), 256, att::LDS_BYTES, stream>>>((const u16*)d_in[0], (const u16*)d_in[4], (const u16*)d_in[5], (const u16*)d_in[6], (const u16*)d_in[7], (const u16*)d_in[1],
                                                             (const u16*)d_in[9], (const u16*)d_in[2], (const u16*)d_in[8], (char*)d_ws, (u16*)d_out);
}
```

```cpp
#include <hip/hip_runtime.h>
#include <hip/hip_bf16.h>
#include <stdint.h>
#include <utility>

typedef short bf16x8 __attribute__((ext_vector_type(8)));
typedef short s16x4 __attribute__((ext_vector_type(4)));
typedef float f32x16 __attribute__((ext_vector_type(16)));
typedef float f32x4 __attribute__((ext_vector_type(4)));
typedef unsigned u32x4 __attribute__((ext_vector_type(4)));
typedef unsigned u32x2 __attribute__((ext_vector_type(2)));
typedef unsigned short u16;

constexpr int S = 2048, DM = 576, NH = 32, HD = 128, DC = 512, NQKV = 1728, DHA = 18;
constexpr int NBIG = NH * HD;
constexpr int KS_TOT = 146;

#define SBAR() __builtin_amdgcn_sched_barrier(0)
constexpr size_t WS_Q = 0, WS_K = WS_Q + (size_t)S * NBIG * 2, WS_V = WS_K + (size_t)S * NBIG * 2, WS_AF = WS_V + (size_t)S * NBIG * 2, WS_X = WS_AF + (size_t)128 * KS_TOT * 1024,
                 WS_P = WS_X + (size_t)64 * 16 * 9216, WS_F = WS_P + (size_t)128 * (4 * 32 * 256 + 1024) * 4;
constexpr size_t WS_L = (size_t)96 << 20;
static_assert(WS_F == 96206848, "workspace map is baked into the generated asm");
constexpr int F_EPOCH = 0, F_J = 1024, F_ST = 8192, F_KV = 40960, F_R = 65536;
constexpr unsigned TAG_STEP = 0x9E3779B1u;
__device__ __forceinline__ unsigned ld_flag(const unsigned* p) { return __hip_atomic_load(p, __ATOMIC_RELAXED, __HIP_MEMORY_SCOPE_AGENT); }
__device__ __forceinline__ void st_flag(unsigned* p, unsigned v) { __hip_atomic_store(p, v, __ATOMIC_RELAXED, __HIP_MEMORY_SCOPE_AGENT); }
__device__ __forceinline__ unsigned ld_flag_async(const unsigned* p) { unsigned v; asm volatile("global_load_dword %0, %1, off sc1" : "=v"(v) : "v"(p) : "memory"); return v; }
#define WAITV(v, N) asm volatile("s_waitcnt vmcnt(" #N ")" : "+v"(v) :: "memory")
__device__ __forceinline__ void wait_flags(const unsigned* p, unsigned v, unsigned tag) {
    if (__builtin_expect(__builtin_amdgcn_ballot_w64(v != tag) != 0ull, 0)) {
        unsigned spins = 0;
        do { __builtin_amdgcn_s_sleep(1); } while (__builtin_amdgcn_ballot_w64(ld_flag(p) != tag) != 0ull && ++spins < (1u << 22));
    }
    asm volatile("" ::: "memory");
}

__device__ __forceinline__ unsigned cvtpk(float lo, float hi) {
    unsigned r; asm volatile("v_cvt_pk_bf16_f32 %0, %1, %2" : "=v"(r) : "v"(lo), "v"(hi)); return r;
}
__device__ __forceinline__ float bf2f(u16 v) { return __uint_as_float(((unsigned)v) << 16); }
__device__ __forceinline__ int crow(int r, int hi) { return (r & 3) + 8 * (r >> 2) + 4 * hi; }

typedef __attribute__((address_space(3))) s16x4* lds_s16x4_p;
__device__ __forceinline__ s16x4 trrd(unsigned addr) { return __builtin_amdgcn_ds_read_tr16_b64_v4i16((lds_s16x4_p)addr); }

__device__ __forceinline__ unsigned lane_id() { unsigned l; asm volatile("v_mbcnt_lo_u32_b32 %0, -1, 0\n\tv_mbcnt_hi_u32_b32 %0, -1, %0" : "=v"(l)); return l; }
#define RTS(slot) do {} while (0)
#define CT_END 0x1f300
#define ACLOB_LO "a0","a1","a2","a3","a4","a5","a6","a7","a8","a9","a10","a11","a12","a13","a14","a15","a16","a17","a18","a19","a20","a21","a22","a23","a24","a25","a26","a27","a28","a29","a30","a31","a32","a33","a34","a35","a36","a37","a38","a39","a40","a41","a42","a43","a44","a45","a46","a47","a48","a49","a50","a51","a52","a53","a54","a55","a56","a57","a58","a59","a60","a61","a62","a63","a64","a65","a66","a67","a68","a69","a70","a71","a72","a73","a74","a75","a76","a77","a78","a79","a80","a81","a82","a83","a84","a85","a86","a87","a88","a89","a90","a91","a92","a93","a94","a95","a96","a97","a98","a99","a100","a101","a102","a103","a104","a105","a106","a107","a108","a109","a110","a111","a112","a113","a114","a115","a116","a117","a118","a119","a120","a121","a122","a123","a124","a125","a126","a127"
#define ACLOB_HI "a128","a129","a130","a131","a132","a133","a134","a135","a136","a137","a138","a139","a140","a141","a142","a143","a144","a145","a146","a147","a148","a149","a150","a151","a152","a153","a154","a155","a156","a157","a158","a159","a160","a161","a162","a163","a164","a165","a166","a167","a168","a169","a170","a171","a172","a173","a174","a175","a176","a177","a178","a179","a180","a181","a182","a183","a184","a185","a186","a187","a188","a189","a190","a191","a192","a193","a194","a195","a196","a197","a198","a199","a200","a201","a202","a203","a204","a205","a206","a207","a208","a209","a210","a211","a212","a213","a214","a215","a216","a217","a218","a219","a220","a221","a222","a223","a224","a225","a226","a227","a228","a229","a230","a231","a232","a233","a234","a235","a236","a237","a238","a239","a240","a241","a242","a243","a244","a245","a246","a247","a248","a249","a250","a251","a252","a253","a254","a255"
#define PLO(p) ((unsigned)(size_t)(p))
#define PHI(p) ((unsigned)((size_t)(p) >> 32))
__device__ __forceinline__ unsigned vsel(bool c, unsigned a, unsigned b) { asm("" : "+v"(a), "+v"(b)); return c ? a : b; }
__device__ __forceinline__ unsigned ssel(unsigned c, unsigned a, unsigned b) { unsigned r; asm("s_cmp_lg_u32 %1, 0\n\ts_cselect_b32 %0, %2, %3" : "=s"(r) : "s"(c), "s"(a), "s"(b) : "scc"); return r; }
template <int N> __device__ __forceinline__ unsigned sslt(unsigned c, unsigned a, unsigned b) { unsigned r; asm("s_cmpk_lt_u32 %1, %4\n\ts_cselect_b32 %0, %2, %3" : "=s"(r) : "s"(c), "s"(a), "s"(b), "n"(N) : "scc"); return r; }
namespace att {
constexpr int LDS_BYTES = 163840;
constexpr unsigned LDS_G = 159744u, LDS_L = 161792u;
}
#define SD(J8, H, W) ((unsigned)WS_AF + (((J8) + 2u * ((W) & 2u) + ((W) & 1u)) * KS_TOT + 18u + 4u * (H)) * 1024u)
#define SRDA (u32x4){PLO(ws), PHI(ws) & 0xffffu, (unsigned)WS_L, 0x00020000u}
#define PROJ_ASM \
".p2align 3\n" \
"L_pb:\n" \
"s_waitcnt lgkmcnt(0)\n" \
"s_mov_b32 s64, s10\n" \
"s_and_b32 s65, s11, 0xffff\n" \
"s_mov_b32 s66, 0x200000\n" \
"s_mov_b32 s67, 0x20000\n" \
"s_mov_b32 s68, s14\n" \
"s_and_b32 s69, s15, 0xffff\n" \
"s_mov_b32 s70, 0x400000\n" \
"s_mov_b32 s71, 0x20000\n" \
"s_mov_b32 s72, s16\n" \
"s_and_b32 s73, s17, 0xffff\n" \
"s_mov_b32 s74, 0x400000\n" \
"s_mov_b32 s75, 0x20000\n" \
"s_add_u32 m0, s30, 0x0\n" \
"s_movk_i32 s84, 0x0\n" \
"buffer_load_dwordx4 v22, s[64:67], s84 offen offset:0 lds\n" \
"s_movk_i32 s84, 0x1c00\n" \
"buffer_load_dwordx4 v23, s[64:67], s84 offen offset:1024 lds\n" \
"s_movk_i32 s84, 0x3800\n" \
"buffer_load_dwordx4 v22, s[64:67], s84 offen offset:2048 lds\n" \
"s_movk_i32 s84, 0x5400\n" \
"buffer_load_dwordx4 v23, s[64:67], s84 offen offset:3072 lds\n" \
"s_add_u32 m0, s31, 0x0\n" \
"s_movk_i32 s84, 0x0\n" \
"buffer_load_dwordx4 v26, s[68:71], s84 offen offset:0 lds\n" \
"s_movk_i32 s84, 0x7c00\n" \
"buffer_load_dwordx4 v26, s[68:71], s84 offen offset:1024 lds\n" \
"s_addk_i32 s84, 0x7c00\n" \
"buffer_load_dwordx4 v27, s[68:71], s84 offen offset:2048 lds\n" \
"s_addk_i32 s84, 0x7c00\n" \
"buffer_load_dwordx4 v27, s[68:71], s84 offen offset:3072 lds\n" \
"s_movk_i32 s7, 0x300\n" \
"s_lshr_b32 s4, s2, 1\n" \
"s_and_b32 s4, s4, 60\n" \
"s_or_b32 s4, s4, s3\n" \
"v_lshrrev_b32_e32 v212, 4, v20\n" \
"v_min_u32_e32 v212, 59, v212\n" \
"s_cmpk_lt_u32 s2, 0x80\n" \
"s_cselect_b32 s6, s14, s16\n" \
"v_subrev_u32_e32 v214, 48, v212\n" \
"v_mov_b32_e32 v212, s10\n" \
"v_mov_b32_e32 v215, s6\n" \
"s_cmpk_lt_u32 s2, 0x80\n" \
"s_cselect_b32 s5, s15, s17\n" \
"v_cmp_gt_u32_e32 vcc, s7, v20\n" \
"v_mov_b32_e64 v213, s11\n" \
"v_bfe_u32 v216, v20, 4, 3\n" \
"v_cndmask_b32_e32 v212, v212, v215, vcc\n" \
"v_mov_b32_e32 v215, s5\n" \
"s_lshl_b32 s5, s3, 4\n" \
"v_cndmask_b32_e32 v213, v213, v215, vcc\n" \
"v_lshlrev_b32_e64 v215, 5, v20\n" \
"v_and_b32_e32 v215, 0x7000, v215\n" \
"v_lshl_add_u32 v215, s4, 6, v215\n" \
"s_lshl_b32 s4, s2, 3\n" \
"s_and_b32 s4, s4, 56\n" \
"v_or3_b32 v215, v215, s4, v216\n" \
"s_lshl_b32 s4, s2, 6\n" \
"s_or_b32 s6, s5, s4\n" \
"v_cmp_gt_u32_e64 s[4:5], 6, v214\n" \
"v_add_u32_e32 v215, 0x2000, v215\n" \
"s_nop 0\n" \
"v_cndmask_b32_e64 v216, 4, 2, s[4:5]\n" \
"v_add3_u32 v214, s6, v214, v216\n" \
"s_lshl_b32 s4, s3, 9\n" \
"v_cndmask_b32_e32 v214, v214, v215, vcc\n" \
"v_lshlrev_b32_e32 v214, 7, v214\n" \
"v_mov_b32_e64 v215, 0\n" \
"v_lshl_add_u64 v[184:185], v[212:213], 0, v[214:215]\n" \
"s_or_b32 s32, s4, 0x25000\n" \
"s_add_u32 m0, s30, 0x1000\n" \
"s_mov_b32 s84, 0x8000\n" \
"buffer_load_dwordx4 v22, s[64:67], s84 offen offset:0 lds\n" \
"s_and_b32 s33, s3, 1\n" \
"v_lshrrev_b32_e32 v212, 4, v20\n" \
"v_lshrrev_b32_e64 v214, 8, v20\n" \
"v_bfe_u32 v212, v212, 1, 3\n" \
"v_lshlrev_b32_e32 v215, 2, v20\n" \
"s_addk_i32 s84, 0x1c00\n" \
"buffer_load_dwordx4 v23, s[64:67], s84 offen offset:1024 lds\n" \
"v_lshrrev_b32_e64 v216, 1, v20\n" \
"v_bfe_u32 v213, v20, 4, 4\n" \
"s_lshl_b32 s4, s3, 5\n" \
"v_xor_b32_e32 v212, v212, v214\n" \
"v_lshlrev_b32_e32 v214, 11, v214\n" \
"s_addk_i32 s84, 0x1c00\n" \
"buffer_load_dwordx4 v22, s[64:67], s84 offen offset:2048 lds\n" \
"v_and_b32_e32 v215, 0x300, v215\n" \
"v_and_b32_e64 v217, 24, v216\n" \
"v_and_or_b32 v213, s4, 64, v213\n" \
"v_or3_b32 v214, v215, v214, v217\n" \
"s_lshl_b32 s4, s33, 14\n" \
"s_addk_i32 s84, 0x1c00\n" \
"buffer_load_dwordx4 v23, s[64:67], s84 offen offset:3072 lds\n" \
"v_and_b32_e32 v215, 0xe0, v216\n" \
"v_lshlrev_b32_e32 v213, 7, v213\n" \
"v_lshlrev_b32_e32 v212, 4, v212\n" \
"v_or3_b32 v214, v214, s4, v215\n" \
"v_or_b32_e64 v28, v212, v213\n" \
"s_add_u32 m0, s31, 0x4000\n" \
"s_movk_i32 s84, 0x0\n" \
"buffer_load_dwordx4 v26, s[72:75], s84 offen offset:0 lds\n" \
"v_add_u32_e32 v32, 0x10000, v214\n" \
"v_bitop3_b32 v29, v212, 64, v213 bitop3:0x36\n" \
"v_xor_b32_e32 v33, 0x20, v32\n" \
"v_xor_b32_e64 v34, 0x40, v32\n" \
"v_xor_b32_e32 v35, 0x60, v32\n" \
"s_movk_i32 s84, 0x7c00\n" \
"buffer_load_dwordx4 v26, s[72:75], s84 offen offset:1024 lds\n" \
"v_xor_b32_e32 v36, 0x80, v32\n" \
"v_xor_b32_e32 v37, 0xa0, v32\n" \
"v_xor_b32_e32 v38, 0xc0, v32\n" \
"v_xor_b32_e32 v39, 0xe0, v32\n" \
"s_mov_b32 s80, s26\n" \
"s_addk_i32 s84, 0x7c00\n" \
"buffer_load_dwordx4 v27, s[72:75], s84 offen offset:2048 lds\n" \
"s_and_b32 s81, s27, 0xffff\n" \
"s_mov_b32 s82, 0x10000000\n" \
"s_mov_b32 s83, 0x20000\n" \
"s_mov_b32 s76, s18\n" \
"s_and_b32 s77, s19, 0xffff\n" \
"s_addk_i32 s84, 0x7c00\n" \
"buffer_load_dwordx4 v27, s[72:75], s84 offen offset:3072 lds\n" \
"s_mov_b32 s78, 0x1e6000\n" \
"s_mov_b32 s79, 0x20000\n" \
"s_branch L_pgo_%=\n" \
"L_pret:\n" \
"s_setpc_b64 s[88:89]\n" \
"L_pgo_%=:\n" \
"s_mul_i32 s88, s33, 0xa000\n" \
"s_lshl_b32 s89, s3, 11\n" \
"s_and_b32 s89, s89, 0x1000\n" \
"s_add_u32 s88, s88, s89\n" \
"s_getpc_b64 s[86:87]\n" \
"L_hpa_%=:\n" \
"s_add_u32 s86, s86, L_pp1-L_hpa_%=\n" \
"s_addc_u32 s87, s87, 0\n" \
"s_add_u32 s86, s86, s88\n" \
"s_addc_u32 s87, s87, 0\n" \
"s_add_u32 m0, s30, 0x8000\n" \
"s_movk_i32 s84, 0x80\n" \
"buffer_load_dwordx4 v22, s[64:67], s84 offen offset:0 lds\n" \
"s_lshr_b32 s4, s2, 1\n" \
"s_and_b32 s4, s4, 0x7c\n" \
"v_bfe_u32 v214, v20, 4, 5\n" \
"s_movk_i32 s84, 0x1c80\n" \
"buffer_load_dwordx4 v23, s[64:67], s84 offen offset:1024 lds\n" \
"s_or_b32 s7, s4, s3\n" \
"v_min_u32_e32 v214, 26, v214\n" \
"v_mad_u64_u32 v[214:215], s[4:5], s7, 27, v[214:215]\n" \
"s_movk_i32 s84, 0x3880\n" \
"buffer_load_dwordx4 v22, s[64:67], s84 offen offset:2048 lds\n" \
"v_mul_u32_u24_e32 v215, 0xaaab, v214\n" \
"v_lshrrev_b32_e32 v215, 18, v215\n" \
"s_mov_b32 s4, 0x1fffffa\n" \
"s_movk_i32 s84, 0x5480\n" \
"buffer_load_dwordx4 v23, s[64:67], s84 offen offset:3072 lds\n" \
"v_mad_u64_u32 v[216:217], s[4:5], v215, s4, v[214:215]\n" \
"s_and_b32 s6, s2, 7\n" \
"v_mul_u32_u24_e32 v215, 27, v215\n" \
"s_mov_b64 exec, 0\n" \
"s_swappc_b64 s[88:89], s[86:87]\n" \
"s_mov_b64 exec, -1\n" \
"s_add_u32 s86, s86, 0x2000\n" \
"s_addc_u32 s87, s87, 0\n" \
"s_add_u32 m0, s31, 0x8000\n" \
"s_mov_b32 s84, 0x80000\n" \
"buffer_load_dwordx4 v26, s[68:71], s84 offen offset:0 lds\n" \
"v_lshrrev_b32_e32 v216, 1, v216\n" \
"v_and_b32_e32 v214, 1, v214\n" \
"v_mov_b32_e32 v213, s13\n" \
"s_addk_i32 s84, 0x7c00\n" \
"buffer_load_dwordx4 v26, s[68:71], s84 offen offset:1024 lds\n" \
"v_lshrrev_b32_e32 v212, 4, v20\n" \
"v_mul_u32_u24_e64 v216, 9, v216\n" \
"v_add3_u32 v214, v214, s6, v215\n" \
"s_mov_b64 exec, 0\n" \
"s_swappc_b64 s[88:89], s[86:87]\n" \
"s_mov_b64 exec, -1\n" \
"s_add_u32 s86, s86, 0x2000\n" \
"s_addc_u32 s87, s87, 0\n" \
"s_addk_i32 s84, 0x7c00\n" \
"buffer_load_dwordx4 v27, s[68:71], s84 offen offset:2048 lds\n" \
"v_add_lshl_u32 v214, v214, v216, 7\n" \
"v_mad_u64_u32 v[216:217], s[4:5], s7, 36, v[212:213]\n" \
"s_movk_i32 s4, 0x240\n" \
"s_addk_i32 s84, 0x7c00\n" \
"buffer_load_dwordx4 v27, s[68:71], s84 offen offset:3072 lds\n" \
"v_mov_b32_e32 v219, s9\n" \
"v_mov_b32_e32 v215, 0\n" \
"v_cmp_gt_u32_e32 vcc, s4, v20\n" \
"s_mov_b64 exec, 0\n" \
"s_swappc_b64 s[88:89], s[86:87]\n" \
"s_mov_b64 exec, -1\n" \
"s_add_u32 s86, s86, 0x2000\n" \
"s_addc_u32 s87, s87, 0\n" \
"s_add_u32 m0, s30, 0x9000\n" \
"s_mov_b32 s84, 0x8080\n" \
"buffer_load_dwordx4 v22, s[64:67], s84 offen offset:0 lds\n" \
"s_lshl_b32 s4, s2, 2\n" \
"v_lshl_add_u64 v[186:187], s[18:19], 0, v[214:215]\n" \
"v_mov_b32_e32 v214, s12\n" \
"s_addk_i32 s84, 0x1c00\n" \
"buffer_load_dwordx4 v23, s[64:67], s84 offen offset:1024 lds\n" \
"v_mov_b32_e32 v217, s8\n" \
"s_or_b32 s4, s4, s3\n" \
"v_cndmask_b32_e32 v219, v219, v213, vcc\n" \
"s_addk_i32 s84, 0x1c00\n" \
"buffer_load_dwordx4 v22, s[64:67], s84 offen offset:2048 lds\n" \
"v_and_b32_e32 v213, 0x3ff8, v216\n" \
"s_mul_i32 s4, s4, 18\n" \
"v_cndmask_b32_e32 v218, v217, v214, vcc\n" \
"s_mov_b64 exec, 0\n" \
"s_swappc_b64 s[88:89], s[86:87]\n" \
"s_mov_b64 exec, -1\n" \
"s_andn2_b32 s85, 2, s3\n" \
"s_lshl_b32 s85, s85, 12\n" \
"s_add_u32 s86, s86, s85\n" \
"s_addc_u32 s87, s87, 0\n" \
"s_addk_i32 s84, 0x1c00\n" \
"buffer_load_dwordx4 v23, s[64:67], s84 offen offset:3072 lds\n" \
"v_or_b32_e32 v213, s6, v213\n" \
"v_and_b32_e32 v214, 7, v216\n" \
"v_min_u32_e32 v212, 53, v212\n" \
"s_add_u32 m0, s31, 0xc000\n" \
"s_mov_b32 s84, 0x80000\n" \
"buffer_load_dwordx4 v26, s[72:75], s84 offen offset:0 lds\n" \
"s_sub_i32 s4, s4, 36\n" \
"v_lshl_or_b32 v213, v213, 3, v214\n" \
"v_add_u32_e32 v212, s4, v212\n" \
"s_mov_b64 exec, 0\n" \
"s_swappc_b64 s[88:89], s[86:87]\n" \
"s_mov_b64 exec, -1\n" \
"s_addk_i32 s84, 0x7c00\n" \
"buffer_load_dwordx4 v26, s[72:75], s84 offen offset:1024 lds\n" \
"v_cndmask_b32_e32 v212, v212, v213, vcc\n" \
"v_lshlrev_b32_e32 v214, 7, v212\n" \
"v_lshl_add_u64 v[188:189], v[218:219], 0, v[214:215]\n" \
"s_addk_i32 s84, 0x7c00\n" \
"buffer_load_dwordx4 v27, s[72:75], s84 offen offset:2048 lds\n" \
"s_movk_i32 s5, 0xd80\n" \
"s_lshr_b32 s6, s2, 5\n" \
"v_lshrrev_b32_e32 v212, 4, v20\n" \
"s_addk_i32 s84, 0x7c00\n" \
"buffer_load_dwordx4 v27, s[72:75], s84 offen offset:3072 lds\n" \
"v_lshl_or_b32 v215, s3, 6, v212\n" \
"v_mul_u32_u24_e32 v216, 0x2aab, v215\n" \
"v_lshrrev_b32_e32 v216, 16, v216\n" \
"s_mov_b32 m0, s32\n" \
"s_mov_b32 exec_lo, 0xffffff\n" \
"s_mov_b32 exec_hi, 0xfff0000\n" \
"global_load_lds_dword v[184:185], off\n" \
"s_mov_b64 exec, -1\n" \
"s_mov_b32 s84, 0x5bc0000\n" \
"buffer_load_dword v180, off, s[80:83], s84 sc1\n" \
"s_cmp_lg_u32 s33, 0\n" \
"s_cbranch_scc1 L_w1_%=\n" \
"L_w0_%=:\n" \
"s_waitcnt vmcnt(18)\n" \
"s_barrier\n" \
"ds_read_b64_tr_b16 a[160:161], v32 offset:0\n" \
"ds_read_b64_tr_b16 a[162:163], v32 offset:1024\n" \
"ds_read_b128 a[128:131], v28 offset:16384\n" \
"ds_read_b64_tr_b16 a[224:225], v32 offset:8192\n" \
"ds_read_b64_tr_b16 a[226:227], v32 offset:9216\n" \
"ds_read_b128 a[192:195], v29 offset:16384\n" \
"ds_read_b128 a[132:135], v28 offset:18432\n" \
"ds_read_b128 a[196:199], v29 offset:18432\n" \
"ds_read_b64_tr_b16 a[164:165], v33 offset:0\n" \
"ds_read_b64_tr_b16 a[166:167], v33 offset:1024\n" \
"ds_read_b64_tr_b16 a[228:229], v33 offset:8192\n" \
"ds_read_b64_tr_b16 a[230:231], v33 offset:9216\n" \
"ds_read_b128 a[136:139], v28 offset:20480\n" \
"ds_read_b128 a[200:203], v29 offset:20480\n" \
"s_waitcnt lgkmcnt(11)\n" \
"v_mfma_f32_16x16x32_bf16 a[0:3], a[160:163], a[128:131], 0\n" \
"ds_read_b64_tr_b16 a[168:169], v34 offset:0\n" \
"v_mad_i32_i24 v217, v216, -6, v215\n" \
"s_waitcnt lgkmcnt(9)\n" \
"v_mfma_f32_16x16x32_bf16 a[0:3], a[224:227], a[192:195], a[0:3]\n" \
"ds_read_b64_tr_b16 a[170:171], v34 offset:1024\n" \
"v_add_u32_e32 v218, 0xfffffe50, v215\n" \
"s_waitcnt lgkmcnt(9)\n" \
"v_mfma_f32_16x16x32_bf16 a[16:19], a[160:163], a[132:135], 0\n" \
"ds_read_b64_tr_b16 a[232:233], v34 offset:8192\n" \
"s_mul_i32 s35, s6, 0x48\n" \
"s_waitcnt lgkmcnt(9)\n" \
"v_mfma_f32_16x16x32_bf16 a[16:19], a[224:227], a[196:199], a[16:19]\n" \
"ds_read_b64_tr_b16 a[234:235], v34 offset:9216\n" \
"v_lshrrev_b32_e32 v219, 1, v217\n" \
"s_waitcnt lgkmcnt(8)\n" \
"v_mfma_f32_16x16x32_bf16 a[20:23], a[164:167], a[132:135], 0\n" \
"ds_read_b128 a[140:143], v28 offset:22528\n" \
"v_add_u32_e32 v218, s35, v216\n" \
"s_waitcnt lgkmcnt(7)\n" \
"v_mfma_f32_16x16x32_bf16 a[20:23], a[228:231], a[196:199], a[20:23]\n" \
"ds_read_b128 a[204:207], v29 offset:22528\n" \
"v_mul_u32_u24_e32 v219, 0x480, v219\n" \
"v_mfma_f32_16x16x32_bf16 a[4:7], a[164:167], a[128:131], 0\n" \
"ds_read_b64_tr_b16 a[172:173], v35 offset:0\n" \
"v_and_b32_e64 v214, 16, v20\n" \
"v_mfma_f32_16x16x32_bf16 a[4:7], a[228:231], a[192:195], a[4:7]\n" \
"ds_read_b64_tr_b16 a[174:175], v35 offset:1024\n" \
"v_mad_u32_u24 v219, v218, s5, v219\n" \
"s_waitcnt lgkmcnt(9)\n" \
"v_mfma_f32_16x16x32_bf16 a[32:35], a[160:163], a[136:139], 0\n" \
"ds_read_b64_tr_b16 a[236:237], v35 offset:8192\n" \
"v_or_b32_e32 v192, v219, v214\n" \
"s_waitcnt lgkmcnt(9)\n" \
"v_mfma_f32_16x16x32_bf16 a[32:35], a[224:227], a[200:203], a[32:35]\n" \
"ds_read_b64_tr_b16 a[238:239], v35 offset:9216\n" \
"v_mov_b32_e32 v220, 2.0\n" \
"v_mfma_f32_16x16x32_bf16 a[36:39], a[164:167], a[136:139], 0\n" \
"ds_read_b128 a[144:147], v28 offset:0\n" \
"v_mov_b32_e64 v222, 7\n" \
"v_mfma_f32_16x16x32_bf16 a[36:39], a[228:231], a[200:203], a[36:39]\n" \
"ds_read_b128 a[208:211], v29 offset:0\n" \
"v_lshlrev_b32_e32 v220, 7, v218\n" \
"s_waitcnt lgkmcnt(10)\n" \
"v_mfma_f32_16x16x32_bf16 a[40:43], a[168:171], a[136:139], 0\n" \
"ds_read_b128 a[148:151], v28 offset:2048\n" \
"v_lshrrev_b32_e32 v218, 1, v218\n" \
"s_waitcnt lgkmcnt(9)\n" \
"v_mfma_f32_16x16x32_bf16 a[40:43], a[232:235], a[200:203], a[40:43]\n" \
"ds_read_b128 a[212:215], v29 offset:2048\n" \
"v_and_b32_e64 v218, 4, v218\n" \
"v_mfma_f32_16x16x32_bf16 a[24:27], a[168:171], a[132:135], 0\n" \
"ds_read_b128 a[152:155], v28 offset:4096\n" \
"v_and_b32_e64 v216, 2, v216\n" \
"v_mfma_f32_16x16x32_bf16 a[24:27], a[232:235], a[196:199], a[24:27]\n" \
"ds_read_b128 a[216:219], v29 offset:4096\n" \
"s_mul_i32 s7, s6, 0x2400\n" \
"v_mfma_f32_16x16x32_bf16 a[8:11], a[168:171], a[128:131], 0\n" \
"ds_read_b128 a[156:159], v28 offset:6144\n" \
"s_sub_i32 s34, 0x22000, s7\n" \
"v_mfma_f32_16x16x32_bf16 a[8:11], a[232:235], a[192:195], a[8:11]\n" \
"ds_read_b128 a[220:223], v29 offset:6144\n" \
"s_mov_b32 m0, s32\n" \
"s_waitcnt lgkmcnt(13)\n" \
"v_mfma_f32_16x16x32_bf16 a[48:51], a[160:163], a[140:143], 0\n" \
"s_mov_b32 exec_lo, 0xff000000\n" \
"s_mov_b32 exec_hi, 0x0\n" \
"global_load_lds_dword v[184:185], off\n" \
"s_mov_b64 exec, -1\n" \
"s_waitcnt lgkmcnt(12)\n" \
"v_mfma_f32_16x16x32_bf16 a[48:51], a[224:227], a[204:207], a[48:51]\n" \
"ds_read_b64_tr_b16 a[176:177], v36 offset:0\n" \
"v_bitop3_b32 v216, v217, v218, v216 bitop3:0x1e\n" \
"v_mfma_f32_16x16x32_bf16 a[52:55], a[164:167], a[140:143], 0\n" \
"ds_read_b64_tr_b16 a[178:179], v36 offset:1024\n" \
"v_or_b32_e32 v213, 0x24400, v20\n" \
"v_mfma_f32_16x16x32_bf16 a[52:55], a[228:231], a[204:207], a[52:55]\n" \
"s_mov_b32 m0, s32\n" \
"ds_read_b64_tr_b16 a[240:241], v36 offset:8192\n" \
"v_mfma_f32_16x16x32_bf16 a[56:59], a[168:171], a[140:143], 0\n" \
"s_mov_b32 exec_lo, 0xffff\n" \
"s_mov_b32 exec_hi, 0x0\n" \
"global_load_lds_dword v[186:187], off\n" \
"s_mov_b64 exec, -1\n" \
"v_mfma_f32_16x16x32_bf16 a[56:59], a[232:235], a[204:207], a[56:59]\n" \
"ds_read_b64_tr_b16 a[242:243], v36 offset:9216\n" \
"v_lshlrev_b32_e32 v216, 4, v216\n" \
"s_waitcnt lgkmcnt(14)\n" \
"v_mfma_f32_16x16x32_bf16 a[60:63], a[172:175], a[140:143], 0\n" \
"ds_read_b64_tr_b16 a[180:181], v37 offset:0\n" \
"v_add3_u32 v196, v220, s34, v216\n" \
"s_waitcnt lgkmcnt(13)\n" \
"v_mfma_f32_16x16x32_bf16 a[60:63], a[236:239], a[204:207], a[60:63]\n" \
"ds_read_b64_tr_b16 a[182:183], v37 offset:1024\n" \
"v_mov_b32_e32 v216, v213\n" \
"v_mfma_f32_16x16x32_bf16 a[44:47], a[172:175], a[136:139], 0\n" \
"ds_read_b64_tr_b16 a[244:245], v37 offset:8192\n" \
"s_movk_i32 s36, 0x1b0\n" \
"v_mfma_f32_16x16x32_bf16 a[44:47], a[236:239], a[200:203], a[44:47]\n" \
"ds_read_b64_tr_b16 a[246:247], v37 offset:9216\n" \
"v_or_b32_e32 v216, 0x100, v215\n" \
"v_mfma_f32_16x16x32_bf16 a[28:31], a[172:175], a[132:135], 0\n" \
"ds_read_b64_tr_b16 a[184:185], v38 offset:0\n" \
"v_mul_u32_u24_e32 v217, 0x2aab, v216\n" \
"v_mfma_f32_16x16x32_bf16 a[28:31], a[236:239], a[196:199], a[28:31]\n" \
"ds_read_b64_tr_b16 a[186:187], v38 offset:1024\n" \
"v_add_u32_e32 v215, 0xffffff50, v215\n" \
"v_mfma_f32_16x16x32_bf16 a[12:15], a[172:175], a[128:131], 0\n" \
"v_lshrrev_b32_e32 v217, 16, v217\n" \
"v_mov_b32_e64 v220, v215\n" \
"v_mfma_f32_16x16x32_bf16 a[12:15], a[236:239], a[192:195], a[12:15]\n" \
"v_mad_i32_i24 v218, v217, -6, v216\n" \
"v_cmp_gt_u32_e32 vcc, s36, v216\n" \
"s_waitcnt lgkmcnt(15)\n" \
"v_mfma_f32_16x16x32_bf16 a[76:79], a[172:175], a[144:147], 0\n" \
"ds_read_b64_tr_b16 a[248:249], v38 offset:8192\n" \
"v_mov_b32_e64 v221, 7\n" \
"v_mfma_f32_16x16x32_bf16 a[76:79], a[236:239], a[208:211], a[76:79]\n" \
"v_lshrrev_b32_e32 v222, 1, v218\n" \
"v_cndmask_b32_e32 v217, v220, v217, vcc\n" \
"v_mfma_f32_16x16x32_bf16 a[72:75], a[168:171], a[144:147], 0\n" \
"s_movk_i32 s4, 0x48\n" \
"v_add_u32_e32 v220, s35, v217\n" \
"v_mfma_f32_16x16x32_bf16 a[72:75], a[232:235], a[208:211], a[72:75]\n" \
"ds_read_b64_tr_b16 a[250:251], v38 offset:9216\n" \
"v_mul_u32_u24_e32 v222, 0x480, v222\n" \
"v_mfma_f32_16x16x32_bf16 a[68:71], a[164:167], a[144:147], 0\n" \
"v_mad_u32_u24 v222, v220, s5, v222\n" \
"v_cmp_gt_u32_e64 s[4:5], s4, v215\n" \
"v_mfma_f32_16x16x32_bf16 a[68:71], a[228:231], a[208:211], a[68:71]\n" \
"v_cndmask_b32_e32 v215, v221, v218, vcc\n" \
"v_lshrrev_b32_e32 v218, 1, v220\n" \
"v_mfma_f32_16x16x32_bf16 a[64:67], a[160:163], a[144:147], 0\n" \
"v_or_b32_e32 v214, v222, v214\n" \
"v_mov_b32_e32 v222, 2.0\n" \
"v_mfma_f32_16x16x32_bf16 a[64:67], a[224:227], a[208:211], a[64:67]\n" \
"ds_read_b64_tr_b16 a[188:189], v39 offset:0\n" \
"v_and_b32_e64 v218, 4, v218\n" \
"v_mfma_f32_16x16x32_bf16 a[80:83], a[160:163], a[148:151], 0\n" \
"v_and_b32_e64 v217, 2, v217\n" \
"v_bitop3_b32 v215, v215, v218, v217 bitop3:0x1e\n" \
"s_waitcnt lgkmcnt(15)\n" \
"v_mfma_f32_16x16x32_bf16 a[80:83], a[224:227], a[212:215], a[80:83]\n" \
"v_lshlrev_b32_e32 v223, 1, v220\n" \
"v_mov_b32_e64 v224, 2.0\n" \
"v_mfma_f32_16x16x32_bf16 a[84:87], a[164:167], a[148:151], 0\n" \
"ds_read_b64_tr_b16 a[190:191], v39 offset:1024\n" \
"v_cndmask_b32_e64 v193, v222, v214, vcc\n" \
"v_mfma_f32_16x16x32_bf16 a[84:87], a[228:231], a[212:215], a[84:87]\n" \
"v_lshlrev_b32_e32 v214, 7, v220\n" \
"v_lshlrev_b32_e32 v215, 4, v215\n" \
"v_mfma_f32_16x16x32_bf16 a[88:91], a[168:171], a[148:151], 0\n" \
"s_mul_i32 s39, s3, 18\n" \
"v_add3_u32 v214, v214, s34, v215\n" \
"v_mfma_f32_16x16x32_bf16 a[88:91], a[232:235], a[212:215], a[88:91]\n" \
"ds_read_b64_tr_b16 a[252:253], v39 offset:8192\n" \
"v_cndmask_b32_e64 v195, v224, v223, s[4:5]\n" \
"v_mfma_f32_16x16x32_bf16 a[92:95], a[172:175], a[148:151], 0\n" \
"s_movk_i32 s4, 0x1f8\n" \
"v_mov_b32_e64 v215, v213\n" \
"v_mfma_f32_16x16x32_bf16 a[92:95], a[236:239], a[212:215], a[92:95]\n" \
"s_add_i32 s35, s35, s39\n" \
"v_cmp_gt_u32_e32 vcc, s4, v216\n" \
"v_mfma_f32_16x16x32_bf16 a[108:111], a[172:175], a[152:155], 0\n" \
"ds_read_b64_tr_b16 a[254:255], v39 offset:9216\n" \
"v_add_u32_e64 v212, s35, v212\n" \
"v_mfma_f32_16x16x32_bf16 a[108:111], a[236:239], a[216:219], a[108:111]\n" \
"v_mov_b32_e32 v219, 2.0\n" \
"v_cndmask_b32_e32 v197, v215, v214, vcc\n" \
"v_mfma_f32_16x16x32_bf16 a[104:107], a[168:171], a[152:155], 0\n" \
"s_movk_i32 s35, 0x120\n" \
"v_mul_u32_u24_e32 v214, 0xd80, v212\n" \
"v_mfma_f32_16x16x32_bf16 a[104:107], a[232:235], a[216:219], a[104:107]\n" \
"v_cmp_gt_u32_e32 vcc, s35, v20\n" \
"v_lshrrev_b32_e64 v215, 1, v212\n" \
"v_mfma_f32_16x16x32_bf16 a[100:103], a[164:167], a[152:155], 0\n" \
"s_and_b32 s4, s2, 7\n" \
"v_cndmask_b32_e32 v194, v219, v214, vcc\n" \
"v_mfma_f32_16x16x32_bf16 a[100:103], a[228:231], a[216:219], a[100:103]\n" \
"v_lshlrev_b32_e32 v214, 7, v212\n" \
"v_lshrrev_b32_e32 v212, 2, v212\n" \
"v_mfma_f32_16x16x32_bf16 a[96:99], a[160:163], a[152:155], 0\n" \
"v_and_b32_e64 v212, 2, v212\n" \
"v_and_or_b32 v212, v215, 1, v212\n" \
"v_mfma_f32_16x16x32_bf16 a[96:99], a[224:227], a[216:219], a[96:99]\n" \
"s_lshl_b32 s5, s4, 2\n" \
"s_bfe_u32 s38, s2, 0x20003\n" \
"s_waitcnt lgkmcnt(15)\n" \
"v_mfma_f32_16x16x32_bf16 a[112:115], a[160:163], a[156:159], 0\n" \
"v_lshlrev_b32_e32 v212, 5, v212\n" \
"s_movk_i32 s35, 0x60\n" \
"v_mfma_f32_16x16x32_bf16 a[112:115], a[224:227], a[220:223], a[112:115]\n" \
"s_or_b32 s5, s5, s38\n" \
"v_bitop3_b32 v212, v212, s35, v214 bitop3:0x36\n" \
"v_mfma_f32_16x16x32_bf16 a[116:119], a[164:167], a[156:159], 0\n" \
"s_lshr_b32 s36, s2, 3\n" \
"v_add_u32_e64 v212, s34, v212\n" \
"v_mfma_f32_16x16x32_bf16 a[116:119], a[228:231], a[220:223], a[116:119]\n" \
"s_mul_i32 s34, s5, 0x12000\n" \
"s_or_b32 s40, s34, 0x6000000\n" \
"v_mfma_f32_16x16x32_bf16 a[120:123], a[168:171], a[156:159], 0\n" \
"s_mul_i32 s34, s36, 0x90000\n" \
"s_lshl_b32 s36, s3, 14\n" \
"v_mfma_f32_16x16x32_bf16 a[120:123], a[232:235], a[220:223], a[120:123]\n" \
"s_add_i32 s34, s34, s36\n" \
"s_lshl_b32 s38, s5, 8\n" \
"v_mfma_f32_16x16x32_bf16 a[124:127], a[172:175], a[156:159], 0\n" \
"s_add_i32 s41, s34, 0x4240000\n" \
"s_mul_i32 s34, s5, 36\n" \
"v_mfma_f32_16x16x32_bf16 a[124:127], a[236:239], a[220:223], a[124:127]\n" \
"s_waitcnt vmcnt(4) lgkmcnt(0)\n" \
"s_barrier\n" \
"v_mfma_f32_16x16x32_bf16 v[52:55], a[176:179], a[128:131], 0\n" \
"s_add_u32 m0, s30, 0x0\n" \
"s_movk_i32 s84, 0x100\n" \
"v_mfma_f32_16x16x32_bf16 v[52:55], a[240:243], a[192:195], v[52:55]\n" \
"buffer_load_dwordx4 v22, s[64:67], s84 offen offset:0 lds\n" \
"v_mfma_f32_16x16x32_bf16 v[56:59], a[180:183], a[128:131], 0\n" \
"ds_read_b64_tr_b16 a[160:161], v32 offset:32768\n" \
"s_add_i32 s5, s7, s40\n" \
"v_mfma_f32_16x16x32_bf16 v[56:59], a[244:247], a[192:195], v[56:59]\n" \
"s_movk_i32 s84, 0x1d00\n" \
"v_mfma_f32_16x16x32_bf16 v[60:63], a[184:187], a[128:131], 0\n" \
"buffer_load_dwordx4 v23, s[64:67], s84 offen offset:1024 lds\n" \
"v_mfma_f32_16x16x32_bf16 v[60:63], a[248:251], a[192:195], v[60:63]\n" \
"ds_read_b64_tr_b16 a[162:163], v32 offset:33792\n" \
"s_addk_i32 s5, 0x2000\n" \
"v_mfma_f32_16x16x32_bf16 v[64:67], a[188:191], a[128:131], 0\n" \
"ds_read_b64_tr_b16 a[224:225], v32 offset:40960\n" \
"s_mov_b32 s37, 2.0\n" \
"v_mfma_f32_16x16x32_bf16 v[64:67], a[252:255], a[192:195], v[64:67]\n" \
"ds_read_b128 a[128:131], v28 offset:49152\n" \
"s_or_b32 s38, s38, 0x5be8000\n" \
"v_mfma_f32_16x16x32_bf16 v[80:83], a[188:191], a[132:135], 0\n" \
"ds_read_b128 a[192:195], v29 offset:49152\n" \
"v_mfma_f32_16x16x32_bf16 v[80:83], a[252:255], a[196:199], v[80:83]\n" \
"s_movk_i32 s84, 0x3900\n" \
"v_mfma_f32_16x16x32_bf16 v[76:79], a[184:187], a[132:135], 0\n" \
"buffer_load_dwordx4 v22, s[64:67], s84 offen offset:2048 lds\n" \
"v_mfma_f32_16x16x32_bf16 v[76:79], a[248:251], a[196:199], v[76:79]\n" \
"s_movk_i32 s84, 0x5500\n" \
"v_mfma_f32_16x16x32_bf16 v[72:75], a[180:183], a[132:135], 0\n" \
"buffer_load_dwordx4 v23, s[64:67], s84 offen offset:3072 lds\n" \
"v_mfma_f32_16x16x32_bf16 v[72:75], a[244:247], a[196:199], v[72:75]\n" \
"s_add_u32 m0, s31, 0x0\n" \
"s_mov_b32 s84, 0x100000\n" \
"v_mfma_f32_16x16x32_bf16 v[68:71], a[176:179], a[132:135], 0\n" \
"buffer_load_dwordx4 v26, s[68:71], s84 offen offset:0 lds\n" \
"v_mfma_f32_16x16x32_bf16 v[68:71], a[240:243], a[196:199], v[68:71]\n" \
"ds_read_b128 a[132:135], v28 offset:51200\n" \
"v_mfma_f32_16x16x32_bf16 v[84:87], a[176:179], a[136:139], 0\n" \
"ds_read_b128 a[196:199], v29 offset:51200\n" \
"v_mfma_f32_16x16x32_bf16 v[84:87], a[240:243], a[200:203], v[84:87]\n" \
"s_addk_i32 s84, 0x7c00\n" \
"v_mfma_f32_16x16x32_bf16 v[88:91], a[180:183], a[136:139], 0\n" \
"buffer_load_dwordx4 v26, s[68:71], s84 offen offset:1024 lds\n" \
"v_mfma_f32_16x16x32_bf16 v[88:91], a[244:247], a[200:203], v[88:91]\n" \
"ds_read_b64_tr_b16 a[226:227], v32 offset:41984\n" \
"v_mfma_f32_16x16x32_bf16 v[92:95], a[184:187], a[136:139], 0\n" \
"s_addk_i32 s84, 0x7c00\n" \
"v_mfma_f32_16x16x32_bf16 v[92:95], a[248:251], a[200:203], v[92:95]\n" \
"buffer_load_dwordx4 v27, s[68:71], s84 offen offset:2048 lds\n" \
"v_mfma_f32_16x16x32_bf16 v[96:99], a[188:191], a[136:139], 0\n" \
"ds_read_b64_tr_b16 a[164:165], v33 offset:32768\n" \
"v_mfma_f32_16x16x32_bf16 v[96:99], a[252:255], a[200:203], v[96:99]\n" \
"ds_read_b128 a[136:139], v28 offset:53248\n" \
"v_mfma_f32_16x16x32_bf16 v[112:115], a[188:191], a[140:143], 0\n" \
"ds_read_b128 a[200:203], v29 offset:53248\n" \
"v_mfma_f32_16x16x32_bf16 v[112:115], a[252:255], a[204:207], v[112:115]\n" \
"s_addk_i32 s84, 0x7c00\n" \
"v_mfma_f32_16x16x32_bf16 v[108:111], a[184:187], a[140:143], 0\n" \
"buffer_load_dwordx4 v27, s[68:71], s84 offen offset:3072 lds\n" \
"v_mfma_f32_16x16x32_bf16 v[108:111], a[248:251], a[204:207], v[108:111]\n" \
"s_add_u32 m0, s30, 0x1000\n" \
"s_mov_b32 s84, 0x8100\n" \
"v_mfma_f32_16x16x32_bf16 v[104:107], a[180:183], a[140:143], 0\n" \
"buffer_load_dwordx4 v22, s[64:67], s84 offen offset:0 lds\n" \
"v_mfma_f32_16x16x32_bf16 v[104:107], a[244:247], a[204:207], v[104:107]\n" \
"ds_read_b64_tr_b16 a[166:167], v33 offset:33792\n" \
"v_mfma_f32_16x16x32_bf16 v[100:103], a[176:179], a[140:143], 0\n" \
"ds_read_b64_tr_b16 a[228:229], v33 offset:40960\n" \
"v_mfma_f32_16x16x32_bf16 v[100:103], a[240:243], a[204:207], v[100:103]\n" \
"ds_read_b128 a[140:143], v28 offset:55296\n" \
"v_mfma_f32_16x16x32_bf16 v[116:119], a[176:179], a[144:147], 0\n" \
"ds_read_b128 a[204:207], v29 offset:55296\n" \
"v_mfma_f32_16x16x32_bf16 v[116:119], a[240:243], a[208:211], v[116:119]\n" \
"s_addk_i32 s84, 0x1c00\n" \
"v_mfma_f32_16x16x32_bf16 v[120:123], a[180:183], a[144:147], 0\n" \
"buffer_load_dwordx4 v23, s[64:67], s84 offen offset:1024 lds\n" \
"v_mfma_f32_16x16x32_bf16 v[120:123], a[244:247], a[208:211], v[120:123]\n" \
"s_addk_i32 s84, 0x1c00\n" \
"v_mfma_f32_16x16x32_bf16 v[124:127], a[184:187], a[144:147], 0\n" \
"buffer_load_dwordx4 v22, s[64:67], s84 offen offset:2048 lds\n" \
"v_mfma_f32_16x16x32_bf16 v[124:127], a[248:251], a[208:211], v[124:127]\n" \
"s_addk_i32 s84, 0x1c00\n" \
"s_nop 0\n" \
"v_mfma_f32_16x16x32_bf16 v[128:131], a[188:191], a[144:147], 0\n" \
"buffer_load_dwordx4 v23, s[64:67], s84 offen offset:3072 lds\n" \
"v_mfma_f32_16x16x32_bf16 v[128:131], a[252:255], a[208:211], v[128:131]\n" \
"ds_read_b128 a[144:147], v28 offset:32768\n" \
"v_mfma_f32_16x16x32_bf16 v[144:147], a[188:191], a[148:151], 0\n" \
"ds_read_b128 a[208:211], v29 offset:32768\n" \
"v_mfma_f32_16x16x32_bf16 v[144:147], a[252:255], a[212:215], v[144:147]\n" \
"s_add_u32 m0, s31, 0x4000\n" \
"s_mov_b32 s84, 0x100000\n" \
"v_mfma_f32_16x16x32_bf16 v[140:143], a[184:187], a[148:151], 0\n" \
"buffer_load_dwordx4 v26, s[72:75], s84 offen offset:0 lds\n" \
"v_mfma_f32_16x16x32_bf16 v[140:143], a[248:251], a[212:215], v[140:143]\n" \
"ds_read_b64_tr_b16 a[230:231], v33 offset:41984\n" \
"v_mfma_f32_16x16x32_bf16 v[136:139], a[180:183], a[148:151], 0\n" \
"s_addk_i32 s84, 0x7c00\n" \
"s_nop 0\n" \
"v_mfma_f32_16x16x32_bf16 v[136:139], a[244:247], a[212:215], v[136:139]\n" \
"buffer_load_dwordx4 v26, s[72:75], s84 offen offset:1024 lds\n" \
"v_mfma_f32_16x16x32_bf16 v[132:135], a[176:179], a[148:151], 0\n" \
"ds_read_b64_tr_b16 a[168:169], v34 offset:32768\n" \
"v_mfma_f32_16x16x32_bf16 v[132:135], a[240:243], a[212:215], v[132:135]\n" \
"ds_read_b128 a[148:151], v28 offset:34816\n" \
".fill (L_pb + 3808 - .) / 4, 4, 0xBF800000\n" \
"L_pp1:\n" \
"s_cbranch_execz L_pret\n" \
"s_nop 0\n" \
"v_mfma_f32_16x16x32_bf16 v[148:151], a[176:179], a[152:155], 0\n" \
"ds_read_b128 a[212:215], v29 offset:34816\n" \
"v_mfma_f32_16x16x32_bf16 v[148:151], a[240:243], a[216:219], v[148:151]\n" \
"ds_read_b64_tr_b16 a[170:171], v34 offset:33792\n" \
"v_mfma_f32_16x16x32_bf16 v[152:155], a[180:183], a[152:155], 0\n" \
"ds_read_b64_tr_b16 a[232:233], v34 offset:40960\n" \
"v_mfma_f32_16x16x32_bf16 v[152:155], a[244:247], a[216:219], v[152:155]\n" \
"ds_read_b64_tr_b16 a[234:235], v34 offset:41984\n" \
"v_mfma_f32_16x16x32_bf16 v[156:159], a[184:187], a[152:155], 0\n" \
"ds_read_b64_tr_b16 a[172:173], v35 offset:32768\n" \
"v_mfma_f32_16x16x32_bf16 v[156:159], a[248:251], a[216:219], v[156:159]\n" \
"ds_read_b64_tr_b16 a[174:175], v35 offset:33792\n" \
"v_mfma_f32_16x16x32_bf16 v[160:163], a[188:191], a[152:155], 0\n" \
"ds_read_b64_tr_b16 a[236:237], v35 offset:40960\n" \
"v_mfma_f32_16x16x32_bf16 v[160:163], a[252:255], a[216:219], v[160:163]\n" \
"ds_read_b128 a[152:155], v28 offset:36864\n" \
"v_mfma_f32_16x16x32_bf16 v[176:179], a[188:191], a[156:159], 0\n" \
"ds_read_b128 a[216:219], v29 offset:36864\n" \
"v_mfma_f32_16x16x32_bf16 v[176:179], a[252:255], a[220:223], v[176:179]\n" \
"ds_read_b64_tr_b16 a[238:239], v35 offset:41984\n" \
"v_mfma_f32_16x16x32_bf16 v[172:175], a[184:187], a[156:159], 0\n" \
"s_cmp_lg_u32 s3, 0\n" \
"s_cselect_b32 s36, s37, s5\n" \
"v_mfma_f32_16x16x32_bf16 v[172:175], a[248:251], a[220:223], v[172:175]\n" \
"s_lshl_b32 s5, s6, 2\n" \
"s_or_b32 s5, s38, s5\n" \
"v_mfma_f32_16x16x32_bf16 v[168:171], a[180:183], a[156:159], 0\n" \
"s_lshl_b32 s35, s3, 10\n" \
"v_mfma_f32_16x16x32_bf16 v[168:171], a[244:247], a[220:223], v[168:171]\n" \
"s_cmpk_lt_u32 s3, 3\n" \
"s_cselect_b32 s37, s37, s5\n" \
"v_mfma_f32_16x16x32_bf16 v[164:167], a[176:179], a[156:159], 0\n" \
"s_lshl_b32 s5, s4, 16\n" \
"s_lshl_b32 s4, s4, 11\n" \
"v_mfma_f32_16x16x32_bf16 v[164:167], a[240:243], a[220:223], v[164:167]\n" \
"ds_read_b128 a[156:159], v28 offset:38912\n" \
"ds_read_b128 a[220:223], v29 offset:38912\n" \
"s_waitcnt lgkmcnt(15)\n" \
"v_mfma_f32_16x16x32_bf16 a[0:3], a[160:163], a[128:131], a[0:3]\n" \
"s_addk_i32 s84, 0x7c00\n" \
"v_mfma_f32_16x16x32_bf16 a[0:3], a[224:227], a[192:195], a[0:3]\n" \
"buffer_load_dwordx4 v27, s[72:75], s84 offen offset:2048 lds\n" \
"v_mfma_f32_16x16x32_bf16 a[4:7], a[164:167], a[128:131], a[4:7]\n" \
"ds_read_b64_tr_b16 a[176:177], v36 offset:32768\n" \
"v_cndmask_b32_e32 v198, v213, v212, vcc\n" \
"s_waitcnt lgkmcnt(15)\n" \
"v_mfma_f32_16x16x32_bf16 a[4:7], a[228:231], a[192:195], a[4:7]\n" \
"s_addk_i32 s84, 0x7c00\n" \
"s_waitcnt lgkmcnt(11)\n" \
"v_mfma_f32_16x16x32_bf16 a[8:11], a[168:171], a[128:131], a[8:11]\n" \
"buffer_load_dwordx4 v27, s[72:75], s84 offen offset:3072 lds\n" \
"s_waitcnt lgkmcnt(9)\n" \
"v_mfma_f32_16x16x32_bf16 a[8:11], a[232:235], a[192:195], a[8:11]\n" \
"ds_read_b64_tr_b16 a[178:179], v36 offset:33792\n" \
"v_or_b32_e32 v212, s35, v20\n" \
"s_waitcnt lgkmcnt(8)\n" \
"v_mfma_f32_16x16x32_bf16 a[12:15], a[172:175], a[128:131], a[12:15]\n" \
"ds_read_b64_tr_b16 a[240:241], v36 offset:40960\n" \
"s_or_b32 s39, s40, s35\n" \
"s_waitcnt lgkmcnt(5)\n" \
"v_mfma_f32_16x16x32_bf16 a[12:15], a[236:239], a[192:195], a[12:15]\n" \
"s_or_b32 s55, s41, s4\n" \
"v_or_b32_e32 v199, 0x22000, v212\n" \
"v_mfma_f32_16x16x32_bf16 a[28:31], a[172:175], a[132:135], a[28:31]\n" \
"s_add_i32 s35, s39, s7\n" \
"s_add_i32 s54, s41, s5\n" \
"v_mfma_f32_16x16x32_bf16 a[28:31], a[236:239], a[196:199], a[28:31]\n" \
"ds_read_b64_tr_b16 a[242:243], v36 offset:41984\n" \
"s_add_i32 s55, s55, 0x80000\n" \
"v_mfma_f32_16x16x32_bf16 a[24:27], a[168:171], a[132:135], a[24:27]\n" \
"v_mov_b32_e32 v215, 0xffff\n" \
"s_and_b32 s4, s3, 1\n" \
"v_mfma_f32_16x16x32_bf16 a[24:27], a[232:235], a[196:199], a[24:27]\n" \
"s_lshl_b32 s5, s4, 4\n" \
"v_lshrrev_b32_e32 v212, 4, v20\n" \
"v_mfma_f32_16x16x32_bf16 a[20:23], a[164:167], a[132:135], a[20:23]\n" \
"v_lshrrev_b32_e32 v218, 6, v20\n" \
"v_and_b32_e32 v219, 12, v218\n" \
"v_mfma_f32_16x16x32_bf16 a[20:23], a[228:231], a[196:199], a[20:23]\n" \
"ds_read_b64_tr_b16 a[180:181], v37 offset:32768\n" \
"v_and_or_b32 v214, v212, 15, s5\n" \
"v_mfma_f32_16x16x32_bf16 a[16:19], a[160:163], a[132:135], a[16:19]\n" \
"v_cmp_gt_u32_e64 vcc, v219, v214\n" \
"v_mov_b32_e32 v220, 0xffff0000\n" \
"v_mfma_f32_16x16x32_bf16 a[16:19], a[224:227], a[196:199], a[16:19]\n" \
"s_lshl_b32 s5, s2, 2\n" \
"v_cndmask_b32_e64 v217, v215, 0, vcc\n" \
"v_mfma_f32_16x16x32_bf16 a[32:35], a[160:163], a[136:139], a[32:35]\n" \
"s_mov_b32 m0, s32\n" \
"ds_read_b64_tr_b16 a[182:183], v37 offset:33792\n" \
"v_mfma_f32_16x16x32_bf16 a[32:35], a[224:227], a[200:203], a[32:35]\n" \
"s_mov_b32 exec_lo, 0x0\n" \
"s_mov_b32 exec_hi, 0xff\n" \
"global_load_lds_dword v[184:185], off\n" \
"s_mov_b64 exec, -1\n" \
"v_mfma_f32_16x16x32_bf16 a[36:39], a[164:167], a[136:139], a[36:39]\n" \
"v_cmp_lt_u32_e32 vcc, v219, v214\n" \
"v_mov_b32_e32 v213, s23\n" \
"v_mfma_f32_16x16x32_bf16 a[36:39], a[228:231], a[200:203], a[36:39]\n" \
"ds_read_b64_tr_b16 a[244:245], v37 offset:40960\n" \
"s_or_b32 s5, s5, s3\n" \
"v_mfma_f32_16x16x32_bf16 a[40:43], a[168:171], a[136:139], a[40:43]\n" \
"v_cndmask_b32_e32 v221, 0, v220, vcc\n" \
"v_or_b32_e64 v12, v217, v221\n" \
"v_mfma_f32_16x16x32_bf16 a[40:43], a[232:235], a[200:203], a[40:43]\n" \
"s_mov_b32 m0, s32\n" \
"v_or_b32_e32 v217, 2, v219\n" \
"v_mfma_f32_16x16x32_bf16 a[44:47], a[172:175], a[136:139], a[44:47]\n" \
"s_mov_b32 exec_lo, 0xffff0000\n" \
"s_mov_b32 exec_hi, 0x0\n" \
"global_load_lds_dword v[186:187], off\n" \
"s_mov_b64 exec, -1\n" \
"v_mfma_f32_16x16x32_bf16 a[44:47], a[236:239], a[200:203], a[44:47]\n" \
"ds_read_b64_tr_b16 a[246:247], v37 offset:41984\n" \
"v_cmp_gt_u32_e64 vcc, v217, v214\n" \
"v_mfma_f32_16x16x32_bf16 a[60:63], a[172:175], a[140:143], a[60:63]\n" \
"v_mov_b32_e32 v216, s25\n" \
"s_movk_i32 s40, 0xf400\n" \
"v_mfma_f32_16x16x32_bf16 a[60:63], a[236:239], a[204:207], a[60:63]\n" \
"v_cndmask_b32_e64 v221, v215, 0, vcc\n" \
"v_cmp_lt_u32_e64 vcc, v217, v214\n" \
"v_mfma_f32_16x16x32_bf16 a[56:59], a[168:171], a[140:143], a[56:59]\n" \
"ds_read_b64_tr_b16 a[184:185], v38 offset:32768\n" \
"s_lshl_b32 s4, s4, 6\n" \
"v_mfma_f32_16x16x32_bf16 a[56:59], a[232:235], a[204:207], a[56:59]\n" \
"v_cndmask_b32_e32 v217, 0, v220, vcc\n" \
"v_or_b32_e64 v13, v221, v217\n" \
"v_mfma_f32_16x16x32_bf16 a[52:55], a[164:167], a[140:143], a[52:55]\n" \
"v_or_b32_e32 v217, 16, v219\n" \
"v_cmp_gt_u32_e32 vcc, v217, v214\n" \
"v_mfma_f32_16x16x32_bf16 a[52:55], a[228:231], a[204:207], a[52:55]\n" \
"ds_read_b64_tr_b16 a[186:187], v38 offset:33792\n" \
"v_cndmask_b32_e64 v221, v215, 0, vcc\n" \
"v_mfma_f32_16x16x32_bf16 a[48:51], a[160:163], a[140:143], a[48:51]\n" \
"v_cmp_lt_u32_e32 vcc, v217, v214\n" \
"s_nop 1\n" \
"v_cndmask_b32_e64 v217, 0, v220, vcc\n" \
"v_mfma_f32_16x16x32_bf16 a[48:51], a[224:227], a[204:207], a[48:51]\n" \
"v_or_b32_e32 v14, v221, v217\n" \
"v_or_b32_e32 v217, 18, v219\n" \
"v_mfma_f32_16x16x32_bf16 a[64:67], a[160:163], a[144:147], a[64:67]\n" \
"ds_read_b64_tr_b16 a[248:249], v38 offset:40960\n" \
"v_cmp_gt_u32_e64 vcc, v217, v214\n" \
"v_mfma_f32_16x16x32_bf16 a[64:67], a[224:227], a[208:211], a[64:67]\n" \
"s_nop 0\n" \
"v_cndmask_b32_e64 v215, v215, 0, vcc\n" \
"v_cmp_lt_u32_e32 vcc, v217, v214\n" \
"v_mfma_f32_16x16x32_bf16 a[68:71], a[164:167], a[144:147], a[68:71]\n" \
"s_nop 0\n" \
"v_cndmask_b32_e32 v214, 0, v220, vcc\n" \
"v_or_b32_e64 v15, v215, v214\n" \
"v_mfma_f32_16x16x32_bf16 a[68:71], a[228:231], a[208:211], a[68:71]\n" \
"ds_read_b64_tr_b16 a[250:251], v38 offset:41984\n" \
"v_mad_u64_u32 v[214:215], s[6:7], s5, 42, v[212:213]\n" \
"v_mfma_f32_16x16x32_bf16 a[72:75], a[168:171], a[144:147], a[72:75]\n" \
"s_movk_i32 s5, 0x1440\n" \
"v_cmp_gt_u32_e32 vcc, s5, v214\n" \
"v_mfma_f32_16x16x32_bf16 a[72:75], a[232:235], a[208:211], a[72:75]\n" \
"v_min_u32_e32 v217, 0xa43f, v214\n" \
"v_mov_b32_e64 v214, s24\n" \
"v_mfma_f32_16x16x32_bf16 a[76:79], a[172:175], a[144:147], a[76:79]\n" \
"v_cndmask_b32_e32 v215, v216, v213, vcc\n" \
"v_mov_b32_e32 v213, s22\n" \
"v_mfma_f32_16x16x32_bf16 a[76:79], a[236:239], a[208:211], a[76:79]\n" \
"ds_read_b64_tr_b16 a[188:189], v39 offset:32768\n" \
"s_movk_i32 s6, 0xffee\n" \
"v_mfma_f32_16x16x32_bf16 a[92:95], a[172:175], a[148:151], a[92:95]\n" \
"v_cndmask_b32_e32 v214, v214, v213, vcc\n" \
"v_add_u32_e32 v213, 0xffffebc0, v217\n" \
"v_mfma_f32_16x16x32_bf16 a[92:95], a[236:239], a[212:215], a[92:95]\n" \
"s_lshl_b32 s5, s2, 5\n" \
"v_cndmask_b32_e32 v213, v213, v217, vcc\n" \
"v_mfma_f32_16x16x32_bf16 a[88:91], a[168:171], a[148:151], a[88:91]\n" \
"ds_read_b64_tr_b16 a[190:191], v39 offset:33792\n" \
"v_lshlrev_b32_e64 v216, 7, v213\n" \
"v_mfma_f32_16x16x32_bf16 a[88:91], a[232:235], a[212:215], a[88:91]\n" \
"v_mul_u32_u24_e32 v213, 0xe39, v212\n" \
"v_mov_b32_e64 v217, 0\n" \
"v_mfma_f32_16x16x32_bf16 a[84:87], a[164:167], a[148:151], a[84:87]\n" \
"v_lshrrev_b32_e64 v213, 16, v213\n" \
"v_lshl_add_u64 v[16:17], v[214:215], 0, v[216:217]\n" \
"v_mfma_f32_16x16x32_bf16 a[84:87], a[228:231], a[212:215], a[84:87]\n" \
"ds_read_b64_tr_b16 a[252:253], v39 offset:40960\n" \
"v_mad_i32_i24 v214, v213, s6, v212\n" \
"v_mfma_f32_16x16x32_bf16 a[80:83], a[160:163], a[148:151], a[80:83]\n" \
"s_add_i32 s5, s5, 32\n" \
"v_lshlrev_b32_e32 v215, 4, v214\n" \
"v_mfma_f32_16x16x32_bf16 a[80:83], a[224:227], a[212:215], a[80:83]\n" \
"v_add_u32_e64 v216, s5, v215\n" \
"v_and_b32_e32 v216, 0xf0, v216\n" \
"v_mfma_f32_16x16x32_bf16 a[96:99], a[160:163], a[152:155], a[96:99]\n" \
"ds_read_b64_tr_b16 a[254:255], v39 offset:41984\n" \
"v_cmp_gt_u32_e64 vcc, 16, v214\n" \
"v_mfma_f32_16x16x32_bf16 a[96:99], a[224:227], a[216:219], a[96:99]\n" \
"s_movk_i32 s7, 0xf800\n" \
"v_cndmask_b32_e32 v214, v215, v216, vcc\n" \
"v_mfma_f32_16x16x32_bf16 a[100:103], a[164:167], a[152:155], a[100:103]\n" \
"v_or_b32_e64 v215, 64, v212\n" \
"v_mul_u32_u24_e32 v216, 0xe39, v215\n" \
"v_mfma_f32_16x16x32_bf16 a[100:103], a[228:231], a[216:219], a[100:103]\n" \
"v_lshrrev_b32_e64 v216, 16, v216\n" \
"v_mad_i32_i24 v215, v216, s6, v215\n" \
"v_mfma_f32_16x16x32_bf16 a[104:107], a[168:171], a[152:155], a[104:107]\n" \
"v_lshlrev_b32_e32 v217, 4, v215\n" \
"v_add_u32_e32 v220, s5, v217\n" \
"v_mfma_f32_16x16x32_bf16 a[104:107], a[232:235], a[216:219], a[104:107]\n" \
"v_and_b32_e32 v220, 0xf0, v220\n" \
"v_cmp_gt_u32_e64 vcc, 16, v215\n" \
"v_mfma_f32_16x16x32_bf16 a[108:111], a[172:175], a[152:155], a[108:111]\n" \
"s_nop 0\n" \
"v_cndmask_b32_e32 v215, v217, v220, vcc\n" \
"v_or_b32_e32 v217, 0x80, v212\n" \
"v_mfma_f32_16x16x32_bf16 a[108:111], a[236:239], a[216:219], a[108:111]\n" \
"v_mul_u32_u24_e32 v220, 0xe39, v217\n" \
"v_lshrrev_b32_e32 v220, 16, v220\n" \
"s_waitcnt lgkmcnt(15)\n" \
"v_mfma_f32_16x16x32_bf16 a[124:127], a[172:175], a[156:159], a[124:127]\n" \
"v_mad_i32_i24 v217, v220, s6, v217\n" \
"v_lshlrev_b32_e64 v221, 4, v217\n" \
"v_mfma_f32_16x16x32_bf16 a[124:127], a[236:239], a[220:223], a[124:127]\n" \
"v_add_u32_e64 v222, s5, v221\n" \
"v_and_b32_e32 v222, 0xf0, v222\n" \
"v_mfma_f32_16x16x32_bf16 a[120:123], a[168:171], a[156:159], a[120:123]\n" \
"v_cmp_gt_u32_e32 vcc, 16, v217\n" \
"s_nop 1\n" \
"v_cndmask_b32_e64 v217, v221, v222, vcc\n" \
"v_mfma_f32_16x16x32_bf16 a[120:123], a[232:235], a[220:223], a[120:123]\n" \
"v_or_b32_e32 v221, 0xc0, v212\n" \
"v_mul_u32_u24_e32 v222, 0xe39, v221\n" \
"v_mfma_f32_16x16x32_bf16 a[116:119], a[164:167], a[156:159], a[116:119]\n" \
"v_lshrrev_b32_e64 v222, 16, v222\n" \
"v_mad_i32_i24 v221, v222, s6, v221\n" \
"v_mfma_f32_16x16x32_bf16 a[116:119], a[228:231], a[220:223], a[116:119]\n" \
"v_lshlrev_b32_e32 v223, 4, v221\n" \
"v_add_u32_e32 v224, s5, v223\n" \
"v_mfma_f32_16x16x32_bf16 a[112:115], a[160:163], a[156:159], a[112:115]\n" \
"v_and_b32_e32 v224, 0xf0, v224\n" \
"v_cmp_gt_u32_e64 vcc, 16, v221\n" \
"v_mfma_f32_16x16x32_bf16 a[112:115], a[224:227], a[220:223], a[112:115]\n" \
"s_waitcnt vmcnt(20)\n" \
"v_readfirstlane_b32 s62, v180\n" \
"s_nop 3\n" \
"s_add_u32 s62, s62, 0x9E3779B1\n" \
"s_waitcnt vmcnt(2) lgkmcnt(0)\n" \
"s_barrier\n" \
"v_mfma_f32_16x16x32_bf16 v[52:55], a[176:179], a[128:131], v[52:55]\n" \
"s_add_u32 m0, s30, 0x8000\n" \
"s_movk_i32 s84, 0x180\n" \
"v_mfma_f32_16x16x32_bf16 v[52:55], a[240:243], a[192:195], v[52:55]\n" \
"buffer_load_dwordx4 v22, s[64:67], s84 offen offset:0 lds\n" \
"v_mfma_f32_16x16x32_bf16 v[56:59], a[180:183], a[128:131], v[56:59]\n" \
"ds_read_b64_tr_b16 a[160:161], v32 offset:0\n" \
"v_cndmask_b32_e64 v221, v223, v224, vcc\n" \
"v_mfma_f32_16x16x32_bf16 v[56:59], a[244:247], a[192:195], v[56:59]\n" \
"s_movk_i32 s84, 0x1d80\n" \
"v_mfma_f32_16x16x32_bf16 v[60:63], a[184:187], a[128:131], v[60:63]\n" \
"buffer_load_dwordx4 v23, s[64:67], s84 offen offset:1024 lds\n" \
"v_mfma_f32_16x16x32_bf16 v[60:63], a[248:251], a[192:195], v[60:63]\n" \
"ds_read_b64_tr_b16 a[162:163], v32 offset:1024\n" \
"v_or_b32_e32 v223, 0x100, v212\n" \
"v_mfma_f32_16x16x32_bf16 v[64:67], a[188:191], a[128:131], v[64:67]\n" \
"ds_read_b64_tr_b16 a[224:225], v32 offset:8192\n" \
"v_mul_u32_u24_e32 v224, 0xe39, v223\n" \
"v_mfma_f32_16x16x32_bf16 v[64:67], a[252:255], a[192:195], v[64:67]\n" \
"ds_read_b128 a[128:131], v28 offset:16384\n" \
"v_lshrrev_b32_e32 v224, 16, v224\n" \
"v_mfma_f32_16x16x32_bf16 v[80:83], a[188:191], a[132:135], v[80:83]\n" \
"ds_read_b128 a[192:195], v29 offset:16384\n" \
"v_mad_i32_i24 v223, v224, s6, v223\n" \
"v_mfma_f32_16x16x32_bf16 v[80:83], a[252:255], a[196:199], v[80:83]\n" \
"s_movk_i32 s84, 0x3980\n" \
"v_mfma_f32_16x16x32_bf16 v[76:79], a[184:187], a[132:135], v[76:79]\n" \
"buffer_load_dwordx4 v22, s[64:67], s84 offen offset:2048 lds\n" \
"v_mfma_f32_16x16x32_bf16 v[76:79], a[248:251], a[196:199], v[76:79]\n" \
"s_movk_i32 s84, 0x5580\n" \
"v_mfma_f32_16x16x32_bf16 v[72:75], a[180:183], a[132:135], v[72:75]\n" \
"buffer_load_dwordx4 v23, s[64:67], s84 offen offset:3072 lds\n" \
"v_mfma_f32_16x16x32_bf16 v[72:75], a[244:247], a[196:199], v[72:75]\n" \
"s_add_u32 m0, s31, 0x8000\n" \
"s_mov_b32 s84, 0x180000\n" \
"v_mfma_f32_16x16x32_bf16 v[68:71], a[176:179], a[132:135], v[68:71]\n" \
"buffer_load_dwordx4 v26, s[68:71], s84 offen offset:0 lds\n" \
"v_mfma_f32_16x16x32_bf16 v[68:71], a[240:243], a[196:199], v[68:71]\n" \
"ds_read_b128 a[132:135], v28 offset:18432\n" \
"v_lshlrev_b32_e64 v225, 4, v223\n" \
"v_mfma_f32_16x16x32_bf16 v[84:87], a[176:179], a[136:139], v[84:87]\n" \
"ds_read_b128 a[196:199], v29 offset:18432\n" \
"v_add_u32_e64 v226, s5, v225\n" \
"v_mfma_f32_16x16x32_bf16 v[84:87], a[240:243], a[200:203], v[84:87]\n" \
"s_addk_i32 s84, 0x7c00\n" \
"v_mfma_f32_16x16x32_bf16 v[88:91], a[180:183], a[136:139], v[88:91]\n" \
"buffer_load_dwordx4 v26, s[68:71], s84 offen offset:1024 lds\n" \
"v_mfma_f32_16x16x32_bf16 v[88:91], a[244:247], a[200:203], v[88:91]\n" \
"ds_read_b64_tr_b16 a[226:227], v32 offset:9216\n" \
"v_and_b32_e32 v226, 0xf0, v226\n" \
"v_mfma_f32_16x16x32_bf16 v[92:95], a[184:187], a[136:139], v[92:95]\n" \
"s_addk_i32 s84, 0x7c00\n" \
"v_mfma_f32_16x16x32_bf16 v[92:95], a[248:251], a[200:203], v[92:95]\n" \
"buffer_load_dwordx4 v27, s[68:71], s84 offen offset:2048 lds\n" \
"v_mfma_f32_16x16x32_bf16 v[96:99], a[188:191], a[136:139], v[96:99]\n" \
"ds_read_b64_tr_b16 a[164:165], v33 offset:0\n" \
"v_cmp_gt_u32_e64 vcc, 16, v223\n" \
"v_mfma_f32_16x16x32_bf16 v[96:99], a[252:255], a[200:203], v[96:99]\n" \
"ds_read_b128 a[136:139], v28 offset:20480\n" \
"v_cndmask_b32_e64 v223, v225, v226, vcc\n" \
"v_mfma_f32_16x16x32_bf16 v[112:115], a[188:191], a[140:143], v[112:115]\n" \
"ds_read_b128 a[200:203], v29 offset:20480\n" \
"v_or_b32_e32 v225, 0x140, v212\n" \
"v_mfma_f32_16x16x32_bf16 v[112:115], a[252:255], a[204:207], v[112:115]\n" \
"s_addk_i32 s84, 0x7c00\n" \
"s_nop 0\n" \
"v_mfma_f32_16x16x32_bf16 v[108:111], a[184:187], a[140:143], v[108:111]\n" \
"buffer_load_dwordx4 v27, s[68:71], s84 offen offset:3072 lds\n" \
"v_mfma_f32_16x16x32_bf16 v[108:111], a[248:251], a[204:207], v[108:111]\n" \
"s_add_u32 m0, s30, 0x9000\n" \
"s_mov_b32 s84, 0x8180\n" \
"v_mfma_f32_16x16x32_bf16 v[104:107], a[180:183], a[140:143], v[104:107]\n" \
"buffer_load_dwordx4 v22, s[64:67], s84 offen offset:0 lds\n" \
"v_mfma_f32_16x16x32_bf16 v[104:107], a[244:247], a[204:207], v[104:107]\n" \
"ds_read_b64_tr_b16 a[166:167], v33 offset:1024\n" \
"v_mul_u32_u24_e32 v226, 0xe39, v225\n" \
"v_mfma_f32_16x16x32_bf16 v[100:103], a[176:179], a[140:143], v[100:103]\n" \
"ds_read_b64_tr_b16 a[228:229], v33 offset:8192\n" \
"v_lshrrev_b32_e64 v226, 16, v226\n" \
"v_mfma_f32_16x16x32_bf16 v[100:103], a[240:243], a[204:207], v[100:103]\n" \
"ds_read_b128 a[140:143], v28 offset:22528\n" \
"v_mad_i32_i24 v225, v226, s6, v225\n" \
"v_mfma_f32_16x16x32_bf16 v[116:119], a[176:179], a[144:147], v[116:119]\n" \
"ds_read_b128 a[204:207], v29 offset:22528\n" \
"v_lshlrev_b32_e64 v227, 4, v225\n" \
"v_mfma_f32_16x16x32_bf16 v[116:119], a[240:243], a[208:211], v[116:119]\n" \
"s_addk_i32 s84, 0x1c00\n" \
"v_mfma_f32_16x16x32_bf16 v[120:123], a[180:183], a[144:147], v[120:123]\n" \
"buffer_load_dwordx4 v23, s[64:67], s84 offen offset:1024 lds\n" \
"v_mfma_f32_16x16x32_bf16 v[120:123], a[244:247], a[208:211], v[120:123]\n" \
"s_addk_i32 s84, 0x1c00\n" \
"v_mfma_f32_16x16x32_bf16 v[124:127], a[184:187], a[144:147], v[124:127]\n" \
"buffer_load_dwordx4 v22, s[64:67], s84 offen offset:2048 lds\n" \
"v_mfma_f32_16x16x32_bf16 v[124:127], a[248:251], a[208:211], v[124:127]\n" \
"s_addk_i32 s84, 0x1c00\n" \
"v_mfma_f32_16x16x32_bf16 v[128:131], a[188:191], a[144:147], v[128:131]\n" \
"buffer_load_dwordx4 v23, s[64:67], s84 offen offset:3072 lds\n" \
"v_mfma_f32_16x16x32_bf16 v[128:131], a[252:255], a[208:211], v[128:131]\n" \
"ds_read_b128 a[144:147], v28 offset:0\n" \
"v_add_u32_e32 v228, s5, v227\n" \
"v_mfma_f32_16x16x32_bf16 v[144:147], a[188:191], a[148:151], v[144:147]\n" \
"ds_read_b128 a[208:211], v29 offset:0\n" \
"v_and_b32_e32 v228, 0xf0, v228\n" \
"v_mfma_f32_16x16x32_bf16 v[144:147], a[252:255], a[212:215], v[144:147]\n" \
"s_add_u32 m0, s31, 0xc000\n" \
"s_mov_b32 s84, 0x180000\n" \
"v_mfma_f32_16x16x32_bf16 v[140:143], a[184:187], a[148:151], v[140:143]\n" \
"buffer_load_dwordx4 v26, s[72:75], s84 offen offset:0 lds\n" \
"v_mfma_f32_16x16x32_bf16 v[140:143], a[248:251], a[212:215], v[140:143]\n" \
"ds_read_b64_tr_b16 a[230:231], v33 offset:9216\n" \
"v_cmp_gt_u32_e64 vcc, 16, v225\n" \
"v_mfma_f32_16x16x32_bf16 v[136:139], a[180:183], a[148:151], v[136:139]\n" \
"s_addk_i32 s84, 0x7c00\n" \
"v_mfma_f32_16x16x32_bf16 v[136:139], a[244:247], a[212:215], v[136:139]\n" \
"buffer_load_dwordx4 v26, s[72:75], s84 offen offset:1024 lds\n" \
"v_mfma_f32_16x16x32_bf16 v[132:135], a[176:179], a[148:151], v[132:135]\n" \
"ds_read_b64_tr_b16 a[168:169], v34 offset:0\n" \
"v_cndmask_b32_e32 v225, v227, v228, vcc\n" \
"v_mfma_f32_16x16x32_bf16 v[132:135], a[240:243], a[212:215], v[132:135]\n" \
"ds_read_b128 a[148:151], v28 offset:2048\n" \
"v_or_b32_e32 v227, 0x180, v212\n" \
"v_mfma_f32_16x16x32_bf16 v[148:151], a[176:179], a[152:155], v[148:151]\n" \
"ds_read_b128 a[212:215], v29 offset:2048\n" \
"v_mul_u32_u24_e32 v228, 0xe39, v227\n" \
"v_mfma_f32_16x16x32_bf16 v[148:151], a[240:243], a[216:219], v[148:151]\n" \
"ds_read_b64_tr_b16 a[170:171], v34 offset:1024\n" \
"v_lshrrev_b32_e64 v228, 16, v228\n" \
"v_mfma_f32_16x16x32_bf16 v[152:155], a[180:183], a[152:155], v[152:155]\n" \
"ds_read_b64_tr_b16 a[232:233], v34 offset:8192\n" \
"v_mad_i32_i24 v227, v228, s6, v227\n" \
"v_mfma_f32_16x16x32_bf16 v[152:155], a[244:247], a[216:219], v[152:155]\n" \
"ds_read_b64_tr_b16 a[234:235], v34 offset:9216\n" \
"v_lshlrev_b32_e64 v229, 4, v227\n" \
"v_mfma_f32_16x16x32_bf16 v[156:159], a[184:187], a[152:155], v[156:159]\n" \
"ds_read_b64_tr_b16 a[172:173], v35 offset:0\n" \
"v_add_u32_e64 v230, s5, v229\n" \
"v_mfma_f32_16x16x32_bf16 v[156:159], a[248:251], a[216:219], v[156:159]\n" \
"ds_read_b64_tr_b16 a[174:175], v35 offset:1024\n" \
"v_and_b32_e32 v230, 0xf0, v230\n" \
"v_mfma_f32_16x16x32_bf16 v[160:163], a[188:191], a[152:155], v[160:163]\n" \
"ds_read_b64_tr_b16 a[236:237], v35 offset:8192\n" \
"v_cmp_gt_u32_e64 vcc, 16, v227\n" \
"v_mfma_f32_16x16x32_bf16 v[160:163], a[252:255], a[216:219], v[160:163]\n" \
"ds_read_b128 a[152:155], v28 offset:4096\n" \
"v_cndmask_b32_e64 v227, v229, v230, vcc\n" \
"v_mfma_f32_16x16x32_bf16 v[176:179], a[188:191], a[156:159], v[176:179]\n" \
"ds_read_b128 a[216:219], v29 offset:4096\n" \
"v_or_b32_e32 v229, 0x1c0, v212\n" \
"v_mfma_f32_16x16x32_bf16 v[176:179], a[252:255], a[220:223], v[176:179]\n" \
"ds_read_b64_tr_b16 a[238:239], v35 offset:9216\n" \
"v_mul_u32_u24_e32 v230, 0xe39, v229\n" \
"v_mfma_f32_16x16x32_bf16 v[172:175], a[184:187], a[156:159], v[172:175]\n" \
"v_lshrrev_b32_e64 v230, 16, v230\n" \
"v_mad_i32_i24 v229, v230, s6, v229\n" \
"v_mfma_f32_16x16x32_bf16 v[172:175], a[248:251], a[220:223], v[172:175]\n" \
"v_lshlrev_b32_e32 v231, 4, v229\n" \
"v_add_u32_e32 v232, s5, v231\n" \
"v_mfma_f32_16x16x32_bf16 v[168:171], a[180:183], a[156:159], v[168:171]\n" \
"v_and_b32_e32 v232, 0xf0, v232\n" \
"v_cmp_gt_u32_e64 vcc, 16, v229\n" \
"v_mfma_f32_16x16x32_bf16 v[168:171], a[244:247], a[220:223], v[168:171]\n" \
"s_nop 0\n" \
"v_cndmask_b32_e32 v229, v231, v232, vcc\n" \
"v_or_b32_e32 v231, 0x200, v212\n" \
"v_mfma_f32_16x16x32_bf16 v[164:167], a[176:179], a[156:159], v[164:167]\n" \
"v_mul_u32_u24_e32 v232, 0xe39, v231\n" \
"v_lshrrev_b32_e64 v232, 16, v232\n" \
"v_mfma_f32_16x16x32_bf16 v[164:167], a[240:243], a[220:223], v[164:167]\n" \
"ds_read_b128 a[156:159], v28 offset:6144\n" \
"ds_read_b128 a[220:223], v29 offset:6144\n" \
"s_waitcnt lgkmcnt(15)\n" \
"v_mfma_f32_16x16x32_bf16 a[0:3], a[160:163], a[128:131], a[0:3]\n" \
"s_addk_i32 s84, 0x7c00\n" \
"v_mfma_f32_16x16x32_bf16 a[0:3], a[224:227], a[192:195], a[0:3]\n" \
"buffer_load_dwordx4 v27, s[72:75], s84 offen offset:2048 lds\n" \
"v_mfma_f32_16x16x32_bf16 a[4:7], a[164:167], a[128:131], a[4:7]\n" \
"ds_read_b64_tr_b16 a[176:177], v36 offset:0\n" \
"v_mad_i32_i24 v231, v232, s6, v231\n" \
"s_waitcnt lgkmcnt(15)\n" \
"v_mfma_f32_16x16x32_bf16 a[4:7], a[228:231], a[192:195], a[4:7]\n" \
"s_addk_i32 s84, 0x7c00\n" \
"s_waitcnt lgkmcnt(11)\n" \
"v_mfma_f32_16x16x32_bf16 a[8:11], a[168:171], a[128:131], a[8:11]\n" \
"buffer_load_dwordx4 v27, s[72:75], s84 offen offset:3072 lds\n" \
"s_waitcnt lgkmcnt(9)\n" \
"v_mfma_f32_16x16x32_bf16 a[8:11], a[232:235], a[192:195], a[8:11]\n" \
"ds_read_b64_tr_b16 a[178:179], v36 offset:1024\n" \
"v_lshlrev_b32_e32 v233, 4, v231\n" \
"s_waitcnt lgkmcnt(8)\n" \
"v_mfma_f32_16x16x32_bf16 a[12:15], a[172:175], a[128:131], a[12:15]\n" \
"ds_read_b64_tr_b16 a[240:241], v36 offset:8192\n" \
"v_add_u32_e32 v234, s5, v233\n" \
"s_waitcnt lgkmcnt(5)\n" \
"v_mfma_f32_16x16x32_bf16 a[12:15], a[236:239], a[192:195], a[12:15]\n" \
"s_movk_i32 s5, 0x480\n" \
"v_mad_u32_u24 v0, v213, s5, v214\n" \
"v_mfma_f32_16x16x32_bf16 a[28:31], a[172:175], a[132:135], a[28:31]\n" \
"v_mul_u32_u24_e32 v213, 0x480, v216\n" \
"s_movk_i32 s6, 0xfc00\n" \
"v_mfma_f32_16x16x32_bf16 a[28:31], a[236:239], a[196:199], a[28:31]\n" \
"ds_read_b64_tr_b16 a[242:243], v36 offset:9216\n" \
"v_add3_u32 v1, v213, v215, s6\n" \
"v_mfma_f32_16x16x32_bf16 a[24:27], a[168:171], a[132:135], a[24:27]\n" \
"buffer_load_dwordx4 v[200:203], v192, s[76:79], s34 offen offset:0\n" \
"v_mfma_f32_16x16x32_bf16 a[24:27], a[232:235], a[196:199], a[24:27]\n" \
"v_mul_u32_u24_e32 v213, 0x480, v220\n" \
"v_add3_u32 v2, v213, v217, s7\n" \
"v_mfma_f32_16x16x32_bf16 a[20:23], a[164:167], a[132:135], a[20:23]\n" \
"v_mul_u32_u24_e32 v213, 0x480, v222\n" \
"v_add3_u32 v3, v213, v221, s40\n" \
"v_mfma_f32_16x16x32_bf16 a[20:23], a[228:231], a[196:199], a[20:23]\n" \
"ds_read_b64_tr_b16 a[180:181], v37 offset:0\n" \
"v_mul_u32_u24_e32 v213, 0x480, v226\n" \
"v_mfma_f32_16x16x32_bf16 a[16:19], a[160:163], a[132:135], a[16:19]\n" \
"v_add3_u32 v5, v213, v225, s6\n" \
"v_mul_u32_u24_e32 v213, 0x480, v228\n" \
"v_mfma_f32_16x16x32_bf16 a[16:19], a[224:227], a[196:199], a[16:19]\n" \
"v_add3_u32 v6, v213, v227, s7\n" \
"v_mul_u32_u24_e32 v213, 0x480, v230\n" \
"v_mfma_f32_16x16x32_bf16 a[32:35], a[160:163], a[136:139], a[32:35]\n" \
"buffer_load_dwordx4 v[204:207], v193, s[76:79], s34 offen offset:0\n" \
"v_mfma_f32_16x16x32_bf16 a[32:35], a[224:227], a[200:203], a[32:35]\n" \
"s_mov_b32 m0, s32\n" \
"ds_read_b64_tr_b16 a[182:183], v37 offset:1024\n" \
"v_mfma_f32_16x16x32_bf16 a[36:39], a[164:167], a[136:139], a[36:39]\n" \
"s_mov_b32 exec_lo, 0x0\n" \
"s_mov_b32 exec_hi, 0xff00\n" \
"global_load_lds_dword v[184:185], off\n" \
"s_mov_b64 exec, -1\n" \
"v_mfma_f32_16x16x32_bf16 a[36:39], a[228:231], a[200:203], a[36:39]\n" \
"ds_read_b64_tr_b16 a[244:245], v37 offset:8192\n" \
"v_add3_u32 v7, v213, v229, s40\n" \
"v_mfma_f32_16x16x32_bf16 a[40:43], a[168:171], a[136:139], a[40:43]\n" \
"s_lshl_b32 s7, s3, 3\n" \
"s_lshl_b32 s40, s3, 1\n" \
"v_mfma_f32_16x16x32_bf16 a[40:43], a[232:235], a[200:203], a[40:43]\n" \
"buffer_load_dword v208, v194, s[76:79], s34 offen offset:32\n" \
"v_mfma_f32_16x16x32_bf16 a[44:47], a[172:175], a[136:139], a[44:47]\n" \
"ds_read_b64_tr_b16 a[246:247], v37 offset:9216\n" \
"s_and_b32 s7, s7, 16\n" \
"v_mfma_f32_16x16x32_bf16 a[44:47], a[236:239], a[200:203], a[44:47]\n" \
"s_and_b32 s40, s40, 2\n" \
"s_and_b32 s6, s2, 7\n" \
"v_mfma_f32_16x16x32_bf16 a[60:63], a[172:175], a[140:143], a[60:63]\n" \
"v_bfe_u32 v213, v20, 7, 1\n" \
"s_or_b32 s7, s40, s7\n" \
"v_mfma_f32_16x16x32_bf16 a[60:63], a[236:239], a[204:207], a[60:63]\n" \
"v_and_b32_e32 v234, 0xf0, v234\n" \
"v_cmp_gt_u32_e32 vcc, 16, v231\n" \
"v_mfma_f32_16x16x32_bf16 a[56:59], a[168:171], a[140:143], a[56:59]\n" \
"buffer_load_dword v209, v194, s[76:79], s34 offen offset:1184\n" \
"v_mfma_f32_16x16x32_bf16 a[56:59], a[232:235], a[204:207], a[56:59]\n" \
"ds_read_b64_tr_b16 a[184:185], v38 offset:0\n" \
"v_or3_b32 v213, s7, v213, v219\n" \
"v_mfma_f32_16x16x32_bf16 a[52:55], a[164:167], a[140:143], a[52:55]\n" \
"v_and_b32_e32 v214, 32, v20\n" \
"s_lshl_b32 s6, s6, 5\n" \
"v_mfma_f32_16x16x32_bf16 a[52:55], a[228:231], a[204:207], a[52:55]\n" \
"ds_read_b64_tr_b16 a[186:187], v38 offset:1024\n" \
"v_cndmask_b32_e64 v231, v233, v234, vcc\n" \
"v_mfma_f32_16x16x32_bf16 a[48:51], a[160:163], a[140:143], a[48:51]\n" \
"v_mul_u32_u24_e64 v213, 18, v213\n" \
"v_mov_b32_e32 v215, 0x100\n" \
"v_mfma_f32_16x16x32_bf16 a[48:51], a[224:227], a[204:207], a[48:51]\n" \
"v_mov_b32_e32 v216, s6\n" \
"v_cmp_eq_u32_e32 vcc, 0, v214\n" \
"v_mfma_f32_16x16x32_bf16 a[64:67], a[160:163], a[144:147], a[64:67]\n" \
"buffer_load_dword v211, v194, s[76:79], s34 offen offset:2336\n" \
"v_mfma_f32_16x16x32_bf16 a[64:67], a[224:227], a[208:211], a[64:67]\n" \
"ds_read_b64_tr_b16 a[248:249], v38 offset:8192\n" \
"v_and_or_b32 v213, v218, 1, v213\n" \
"v_mfma_f32_16x16x32_bf16 a[68:71], a[164:167], a[144:147], a[68:71]\n" \
"v_lshlrev_b32_e32 v212, 3, v212\n" \
"v_cndmask_b32_e32 v214, v215, v216, vcc\n" \
"v_mfma_f32_16x16x32_bf16 a[68:71], a[228:231], a[208:211], a[68:71]\n" \
"ds_read_b64_tr_b16 a[250:251], v38 offset:9216\n" \
"v_mad_u32_u24 v213, v213, s5, v214\n" \
"v_mfma_f32_16x16x32_bf16 a[72:75], a[168:171], a[144:147], a[72:75]\n" \
"v_and_or_b32 v213, v20, 16, v213\n" \
"v_add_u32_e32 v9, 0x4800, v213\n" \
"v_mfma_f32_16x16x32_bf16 a[72:75], a[232:235], a[208:211], a[72:75]\n" \
"s_mov_b32 s76, s20\n" \
"s_and_b32 s77, s21, 0xffff\n" \
"v_mfma_f32_16x16x32_bf16 a[76:79], a[172:175], a[144:147], a[76:79]\n" \
"s_mov_b32 s78, 0x480\n" \
"v_mul_u32_u24_e32 v213, 0x120, v218\n" \
"v_mfma_f32_16x16x32_bf16 a[76:79], a[236:239], a[208:211], a[76:79]\n" \
"buffer_load_ushort v210, v195, s[76:79], 0 offen\n" \
"v_mfma_f32_16x16x32_bf16 a[92:95], a[172:175], a[148:151], a[92:95]\n" \
"ds_read_b64_tr_b16 a[188:189], v39 offset:0\n" \
"v_and_or_b32 v10, v212, 24, v213\n" \
"v_mfma_f32_16x16x32_bf16 a[92:95], a[236:239], a[212:215], a[92:95]\n" \
"v_lshrrev_b32_e32 v213, 2, v20\n" \
"v_and_or_b32 v213, v213, 60, s4\n" \
"v_mfma_f32_16x16x32_bf16 a[88:91], a[168:171], a[148:151], a[88:91]\n" \
"s_mov_b32 s76, s8\n" \
"s_nop 0\n" \
"s_and_b32 s77, s9, 0xffff\n" \
"v_mfma_f32_16x16x32_bf16 a[88:91], a[232:235], a[212:215], a[88:91]\n" \
"s_mov_b32 s78, 0x240000\n" \
"ds_read_b64_tr_b16 a[190:191], v39 offset:1024\n" \
"v_mfma_f32_16x16x32_bf16 a[84:87], a[164:167], a[148:151], a[84:87]\n" \
"v_lshl_or_b32 v212, s3, 9, v212\n" \
"v_mad_u32_u24 v4, v224, s5, v223\n" \
"v_mfma_f32_16x16x32_bf16 a[84:87], a[228:231], a[212:215], a[84:87]\n" \
"ds_read_b64_tr_b16 a[252:253], v39 offset:8192\n" \
"v_mad_u32_u24 v8, v232, s5, v231\n" \
"v_mfma_f32_16x16x32_bf16 a[80:83], a[160:163], a[148:151], a[80:83]\n" \
"v_or_b32_e32 v18, 0x27000, v213\n" \
"v_or_b32_e32 v19, 0x27800, v212\n" \
"v_mfma_f32_16x16x32_bf16 a[80:83], a[224:227], a[212:215], a[80:83]\n" \
"v_mov_b32_e32 v219, 0x1000c0c\n" \
"v_mov_b32_e32 v220, 0xc0c0100\n" \
"v_mfma_f32_16x16x32_bf16 a[96:99], a[160:163], a[152:155], a[96:99]\n" \
"ds_read_b64_tr_b16 a[254:255], v39 offset:9216\n" \
"v_bfe_u32 v218, v20, 4, 1\n" \
"v_mfma_f32_16x16x32_bf16 a[96:99], a[224:227], a[216:219], a[96:99]\n" \
"v_lshrrev_b32_e32 v213, 8, v20\n" \
"v_cmp_eq_u32_e32 vcc, 0, v218\n" \
"v_mfma_f32_16x16x32_bf16 a[100:103], a[164:167], a[152:155], a[100:103]\n" \
"s_lshr_b32 s4, s2, 5\n" \
"s_and_b32 s5, s3, 1\n" \
"v_mfma_f32_16x16x32_bf16 a[100:103], a[228:231], a[216:219], a[100:103]\n" \
"v_bfe_u32 v217, v20, 4, 4\n" \
"v_cndmask_b32_e64 v219, v219, v220, vcc\n" \
"v_mfma_f32_16x16x32_bf16 a[104:107], a[168:171], a[152:155], a[104:107]\n" \
"v_mov_b32_e32 v220, 0x3020c0c\n" \
"v_mov_b32_e32 v221, 0xc0c0302\n" \
"v_mfma_f32_16x16x32_bf16 a[104:107], a[232:235], a[216:219], a[104:107]\n" \
"v_lshl_or_b32 v218, v213, 2, v218\n" \
"s_xor_b32 s6, s4, 15\n" \
"v_mfma_f32_16x16x32_bf16 a[108:111], a[172:175], a[152:155], a[108:111]\n" \
"s_cmpk_lt_u32 s3, 2\n" \
"s_cselect_b32 s7, s6, s4\n" \
"v_mfma_f32_16x16x32_bf16 a[108:111], a[236:239], a[216:219], a[108:111]\n" \
"s_lshl_b32 s40, s7, 7\n" \
".fill (L_pb + 7904 - .) / 4, 4, 0xBF800000\n" \
"L_pp2:\n" \
"s_cbranch_execz L_pret\n" \
"s_lshl_b32 s41, s5, 6\n" \
"s_waitcnt lgkmcnt(15)\n" \
"v_mfma_f32_16x16x32_bf16 a[124:127], a[172:175], a[156:159], a[124:127]\n" \
"v_cndmask_b32_e32 v220, v220, v221, vcc\n" \
"v_mov_b32_e32 v221, 0xc0c0c0c\n" \
"v_mfma_f32_16x16x32_bf16 a[124:127], a[236:239], a[220:223], a[124:127]\n" \
"v_cmp_gt_u32_e32 vcc, v218, v217\n" \
"v_or_b32_e32 v218, 2, v218\n" \
"v_mfma_f32_16x16x32_bf16 a[120:123], a[168:171], a[156:159], a[120:123]\n" \
"v_lshrrev_b32_e32 v212, 7, v20\n" \
"s_or_b32 s41, s41, s40\n" \
"v_mfma_f32_16x16x32_bf16 a[120:123], a[232:235], a[220:223], a[120:123]\n" \
"v_cndmask_b32_e32 v40, v221, v219, vcc\n" \
"v_cndmask_b32_e32 v42, v219, v221, vcc\n" \
"v_mfma_f32_16x16x32_bf16 a[116:119], a[164:167], a[156:159], a[116:119]\n" \
"v_cmp_gt_u32_e32 vcc, v218, v217\n" \
"v_and_b32_e32 v218, 16, v20\n" \
"v_mfma_f32_16x16x32_bf16 a[116:119], a[228:231], a[220:223], a[116:119]\n" \
"s_movk_i32 s42, 0x3f80\n" \
"v_or_b32_e32 v214, s41, v212\n" \
"v_mfma_f32_16x16x32_bf16 a[112:115], a[160:163], a[156:159], a[112:115]\n" \
"v_and_b32_e32 v217, 0x320, v20\n" \
"s_movk_i32 s41, 0x100\n" \
"v_mfma_f32_16x16x32_bf16 a[112:115], a[224:227], a[220:223], a[112:115]\n" \
"s_waitcnt vmcnt(7) lgkmcnt(0)\n" \
"s_barrier\n" \
"v_mfma_f32_16x16x32_bf16 v[52:55], a[176:179], a[128:131], v[52:55]\n" \
"s_add_u32 m0, s30, 0x0\n" \
"s_movk_i32 s84, 0x200\n" \
"v_mfma_f32_16x16x32_bf16 v[52:55], a[240:243], a[192:195], v[52:55]\n" \
"buffer_load_dwordx4 v22, s[64:67], s84 offen offset:0 lds\n" \
"v_mfma_f32_16x16x32_bf16 v[56:59], a[180:183], a[128:131], v[56:59]\n" \
"ds_read_b64_tr_b16 a[160:161], v32 offset:32768\n" \
"v_lshlrev_b32_e64 v218, v218, s42\n" \
"v_mfma_f32_16x16x32_bf16 v[56:59], a[244:247], a[192:195], v[56:59]\n" \
"s_movk_i32 s84, 0x1e00\n" \
"v_mfma_f32_16x16x32_bf16 v[60:63], a[184:187], a[128:131], v[60:63]\n" \
"buffer_load_dwordx4 v23, s[64:67], s84 offen offset:1024 lds\n" \
"v_mfma_f32_16x16x32_bf16 v[60:63], a[248:251], a[192:195], v[60:63]\n" \
"ds_read_b64_tr_b16 a[162:163], v32 offset:33792\n" \
"v_mov_b32_e64 v219, 0\n" \
"v_mfma_f32_16x16x32_bf16 v[64:67], a[188:191], a[128:131], v[64:67]\n" \
"ds_read_b64_tr_b16 a[224:225], v32 offset:40960\n" \
"v_lshrrev_b32_e64 v215, 6, v20\n" \
"v_mfma_f32_16x16x32_bf16 v[64:67], a[252:255], a[192:195], v[64:67]\n" \
"ds_read_b128 a[128:131], v28 offset:49152\n" \
"v_cndmask_b32_e64 v41, v221, v220, vcc\n" \
"v_mfma_f32_16x16x32_bf16 v[80:83], a[188:191], a[132:135], v[80:83]\n" \
"ds_read_b128 a[192:195], v29 offset:49152\n" \
"v_cndmask_b32_e64 v43, v220, v221, vcc\n" \
"v_mfma_f32_16x16x32_bf16 v[80:83], a[252:255], a[196:199], v[80:83]\n" \
"s_movk_i32 s84, 0x3a00\n" \
"v_mfma_f32_16x16x32_bf16 v[76:79], a[184:187], a[132:135], v[76:79]\n" \
"buffer_load_dwordx4 v22, s[64:67], s84 offen offset:2048 lds\n" \
"v_mfma_f32_16x16x32_bf16 v[76:79], a[248:251], a[196:199], v[76:79]\n" \
"s_movk_i32 s84, 0x5600\n" \
"v_mfma_f32_16x16x32_bf16 v[72:75], a[180:183], a[132:135], v[72:75]\n" \
"buffer_load_dwordx4 v23, s[64:67], s84 offen offset:3072 lds\n" \
"v_mfma_f32_16x16x32_bf16 v[72:75], a[244:247], a[196:199], v[72:75]\n" \
"s_add_u32 m0, s31, 0x0\n" \
"s_mov_b32 s84, 0x200000\n" \
"v_mfma_f32_16x16x32_bf16 v[68:71], a[176:179], a[132:135], v[68:71]\n" \
"buffer_load_dwordx4 v26, s[68:71], s84 offen offset:0 lds\n" \
"v_mfma_f32_16x16x32_bf16 v[68:71], a[240:243], a[196:199], v[68:71]\n" \
"ds_read_b128 a[132:135], v28 offset:51200\n" \
"v_cmp_eq_u32_e32 vcc, s41, v217\n" \
"v_mfma_f32_16x16x32_bf16 v[84:87], a[176:179], a[136:139], v[84:87]\n" \
"ds_read_b128 a[196:199], v29 offset:51200\n" \
"v_and_b32_e32 v217, 0x330, v20\n" \
"v_mfma_f32_16x16x32_bf16 v[84:87], a[240:243], a[200:203], v[84:87]\n" \
"s_addk_i32 s84, 0x7c00\n" \
"v_mfma_f32_16x16x32_bf16 v[88:91], a[180:183], a[136:139], v[88:91]\n" \
"buffer_load_dwordx4 v26, s[68:71], s84 offen offset:1024 lds\n" \
"v_mfma_f32_16x16x32_bf16 v[88:91], a[244:247], a[200:203], v[88:91]\n" \
"ds_read_b64_tr_b16 a[226:227], v32 offset:41984\n" \
"v_bfe_u32 v216, v20, 6, 2\n" \
"v_mfma_f32_16x16x32_bf16 v[92:95], a[184:187], a[136:139], v[92:95]\n" \
"s_addk_i32 s84, 0x7c00\n" \
"v_mfma_f32_16x16x32_bf16 v[92:95], a[248:251], a[200:203], v[92:95]\n" \
"buffer_load_dwordx4 v27, s[68:71], s84 offen offset:2048 lds\n" \
"v_mfma_f32_16x16x32_bf16 v[96:99], a[188:191], a[136:139], v[96:99]\n" \
"ds_read_b64_tr_b16 a[164:165], v33 offset:32768\n" \
"v_cndmask_b32_e64 v46, v219, v218, vcc\n" \
"v_mfma_f32_16x16x32_bf16 v[96:99], a[252:255], a[200:203], v[96:99]\n" \
"ds_read_b128 a[136:139], v28 offset:53248\n" \
"v_mov_b32_e32 v218, 0x3f80\n" \
"v_mfma_f32_16x16x32_bf16 v[112:115], a[188:191], a[140:143], v[112:115]\n" \
"ds_read_b128 a[200:203], v29 offset:53248\n" \
"v_cmp_eq_u32_e64 vcc, 32, v217\n" \
"v_mfma_f32_16x16x32_bf16 v[112:115], a[252:255], a[204:207], v[112:115]\n" \
"s_addk_i32 s84, 0x7c00\n" \
"v_mfma_f32_16x16x32_bf16 v[108:111], a[184:187], a[140:143], v[108:111]\n" \
"buffer_load_dwordx4 v27, s[68:71], s84 offen offset:3072 lds\n" \
"v_mfma_f32_16x16x32_bf16 v[108:111], a[248:251], a[204:207], v[108:111]\n" \
"s_add_u32 m0, s30, 0x1000\n" \
"s_mov_b32 s84, 0x8200\n" \
"v_mfma_f32_16x16x32_bf16 v[104:107], a[180:183], a[140:143], v[104:107]\n" \
"buffer_load_dwordx4 v22, s[64:67], s84 offen offset:0 lds\n" \
"v_mfma_f32_16x16x32_bf16 v[104:107], a[244:247], a[204:207], v[104:107]\n" \
"ds_read_b64_tr_b16 a[166:167], v33 offset:33792\n" \
"s_add_i32 s41, s40, -16\n" \
"v_mfma_f32_16x16x32_bf16 v[100:103], a[176:179], a[140:143], v[100:103]\n" \
"ds_read_b64_tr_b16 a[228:229], v33 offset:40960\n" \
"v_lshrrev_b32_e64 v219, 1, v20\n" \
"v_mfma_f32_16x16x32_bf16 v[100:103], a[240:243], a[204:207], v[100:103]\n" \
"ds_read_b128 a[140:143], v28 offset:55296\n" \
"v_bfe_u32 v215, v215, 1, 1\n" \
"v_mfma_f32_16x16x32_bf16 v[116:119], a[176:179], a[144:147], v[116:119]\n" \
"ds_read_b128 a[204:207], v29 offset:55296\n" \
"v_cndmask_b32_e64 v47, 0, v218, vcc\n" \
"v_mfma_f32_16x16x32_bf16 v[116:119], a[240:243], a[208:211], v[116:119]\n" \
"s_addk_i32 s84, 0x1c00\n" \
"v_mfma_f32_16x16x32_bf16 v[120:123], a[180:183], a[144:147], v[120:123]\n" \
"buffer_load_dwordx4 v23, s[64:67], s84 offen offset:1024 lds\n" \
"v_mfma_f32_16x16x32_bf16 v[120:123], a[244:247], a[208:211], v[120:123]\n" \
"s_addk_i32 s84, 0x1c00\n" \
"v_mfma_f32_16x16x32_bf16 v[124:127], a[184:187], a[144:147], v[124:127]\n" \
"buffer_load_dwordx4 v22, s[64:67], s84 offen offset:2048 lds\n" \
"v_mfma_f32_16x16x32_bf16 v[124:127], a[248:251], a[208:211], v[124:127]\n" \
"s_addk_i32 s84, 0x1c00\n" \
"v_mfma_f32_16x16x32_bf16 v[128:131], a[188:191], a[144:147], v[128:131]\n" \
"buffer_load_dwordx4 v23, s[64:67], s84 offen offset:3072 lds\n" \
"v_mfma_f32_16x16x32_bf16 v[128:131], a[252:255], a[208:211], v[128:131]\n" \
"ds_read_b128 a[144:147], v28 offset:32768\n" \
"s_mov_b32 s42, 0\n" \
"v_mfma_f32_16x16x32_bf16 v[144:147], a[188:191], a[148:151], v[144:147]\n" \
"ds_read_b128 a[208:211], v29 offset:32768\n" \
"v_mfma_f32_16x16x32_bf16 v[144:147], a[252:255], a[212:215], v[144:147]\n" \
"s_add_u32 m0, s31, 0x4000\n" \
"s_mov_b32 s84, 0x200000\n" \
"v_mfma_f32_16x16x32_bf16 v[140:143], a[184:187], a[148:151], v[140:143]\n" \
"buffer_load_dwordx4 v26, s[72:75], s84 offen offset:0 lds\n" \
"v_mfma_f32_16x16x32_bf16 v[140:143], a[248:251], a[212:215], v[140:143]\n" \
"ds_read_b64_tr_b16 a[230:231], v33 offset:41984\n" \
"v_mfma_f32_16x16x32_bf16 v[136:139], a[180:183], a[148:151], v[136:139]\n" \
"s_addk_i32 s84, 0x7c00\n" \
"s_nop 0\n" \
"v_mfma_f32_16x16x32_bf16 v[136:139], a[244:247], a[212:215], v[136:139]\n" \
"buffer_load_dwordx4 v26, s[72:75], s84 offen offset:1024 lds\n" \
"v_mfma_f32_16x16x32_bf16 v[132:135], a[176:179], a[148:151], v[132:135]\n" \
"ds_read_b64_tr_b16 a[168:169], v34 offset:32768\n" \
"v_mfma_f32_16x16x32_bf16 v[132:135], a[240:243], a[212:215], v[132:135]\n" \
"ds_read_b128 a[148:151], v28 offset:34816\n" \
"v_mfma_f32_16x16x32_bf16 v[148:151], a[176:179], a[152:155], v[148:151]\n" \
"ds_read_b128 a[212:215], v29 offset:34816\n" \
"v_mfma_f32_16x16x32_bf16 v[148:151], a[240:243], a[216:219], v[148:151]\n" \
"ds_read_b64_tr_b16 a[170:171], v34 offset:33792\n" \
"v_mfma_f32_16x16x32_bf16 v[152:155], a[180:183], a[152:155], v[152:155]\n" \
"ds_read_b64_tr_b16 a[232:233], v34 offset:40960\n" \
"v_mfma_f32_16x16x32_bf16 v[152:155], a[244:247], a[216:219], v[152:155]\n" \
"ds_read_b64_tr_b16 a[234:235], v34 offset:41984\n" \
"v_mfma_f32_16x16x32_bf16 v[156:159], a[184:187], a[152:155], v[156:159]\n" \
"ds_read_b64_tr_b16 a[172:173], v35 offset:32768\n" \
"v_mfma_f32_16x16x32_bf16 v[156:159], a[248:251], a[216:219], v[156:159]\n" \
"ds_read_b64_tr_b16 a[174:175], v35 offset:33792\n" \
"v_mfma_f32_16x16x32_bf16 v[160:163], a[188:191], a[152:155], v[160:163]\n" \
"ds_read_b64_tr_b16 a[236:237], v35 offset:40960\n" \
"v_mfma_f32_16x16x32_bf16 v[160:163], a[252:255], a[216:219], v[160:163]\n" \
"ds_read_b128 a[152:155], v28 offset:36864\n" \
"v_mfma_f32_16x16x32_bf16 v[176:179], a[188:191], a[156:159], v[176:179]\n" \
"ds_read_b128 a[216:219], v29 offset:36864\n" \
"v_mfma_f32_16x16x32_bf16 v[176:179], a[252:255], a[220:223], v[176:179]\n" \
"ds_read_b64_tr_b16 a[238:239], v35 offset:41984\n" \
"v_mfma_f32_16x16x32_bf16 v[172:175], a[184:187], a[156:159], v[172:175]\n" \
"s_cmp_lg_u32 s40, 0\n" \
"s_cselect_b32 s41, s41, s42\n" \
"v_mfma_f32_16x16x32_bf16 v[172:175], a[248:251], a[220:223], v[172:175]\n" \
"v_lshl_or_b32 v217, s5, 3, v212\n" \
"v_lshlrev_b32_e64 v218, 10, v213\n" \
"v_mfma_f32_16x16x32_bf16 v[168:171], a[180:183], a[156:159], v[168:171]\n" \
"v_lshlrev_b32_e32 v216, 7, v216\n" \
"v_and_b32_e32 v219, 24, v219\n" \
"v_mfma_f32_16x16x32_bf16 v[168:171], a[244:247], a[220:223], v[168:171]\n" \
"v_and_or_b32 v212, v212, 2, v215\n" \
"v_add_u32_e64 v217, s41, v217\n" \
"v_mfma_f32_16x16x32_bf16 v[164:167], a[176:179], a[156:159], v[164:167]\n" \
"v_or3_b32 v216, v216, v218, v219\n" \
"v_lshlrev_b32_e64 v212, 5, v212\n" \
"v_mfma_f32_16x16x32_bf16 v[164:167], a[240:243], a[220:223], v[164:167]\n" \
"ds_read_b128 a[156:159], v28 offset:38912\n" \
"ds_read_b128 a[220:223], v29 offset:38912\n" \
"s_waitcnt lgkmcnt(15)\n" \
"v_mfma_f32_16x16x32_bf16 a[0:3], a[160:163], a[128:131], a[0:3]\n" \
"s_addk_i32 s84, 0x7c00\n" \
"v_mfma_f32_16x16x32_bf16 a[0:3], a[224:227], a[192:195], a[0:3]\n" \
"buffer_load_dwordx4 v27, s[72:75], s84 offen offset:2048 lds\n" \
"v_mfma_f32_16x16x32_bf16 a[4:7], a[164:167], a[128:131], a[4:7]\n" \
"ds_read_b64_tr_b16 a[176:177], v36 offset:32768\n" \
"s_mov_b32 s41, 0x14000\n" \
"s_waitcnt lgkmcnt(15)\n" \
"v_mfma_f32_16x16x32_bf16 a[4:7], a[228:231], a[192:195], a[4:7]\n" \
"s_addk_i32 s84, 0x7c00\n" \
"s_waitcnt lgkmcnt(11)\n" \
"v_mfma_f32_16x16x32_bf16 a[8:11], a[168:171], a[128:131], a[8:11]\n" \
"buffer_load_dwordx4 v27, s[72:75], s84 offen offset:3072 lds\n" \
"s_waitcnt lgkmcnt(9)\n" \
"v_mfma_f32_16x16x32_bf16 a[8:11], a[232:235], a[192:195], a[8:11]\n" \
"ds_read_b64_tr_b16 a[178:179], v36 offset:33792\n" \
"v_or3_b32 v48, v216, v212, s41\n" \
"s_waitcnt lgkmcnt(8)\n" \
"v_mfma_f32_16x16x32_bf16 a[12:15], a[172:175], a[128:131], a[12:15]\n" \
"ds_read_b64_tr_b16 a[240:241], v36 offset:40960\n" \
"s_lshl_b32 s41, s5, 2\n" \
"s_waitcnt lgkmcnt(5)\n" \
"v_mfma_f32_16x16x32_bf16 a[12:15], a[236:239], a[192:195], a[12:15]\n" \
"s_lshl_b32 s7, s7, 3\n" \
"v_or_b32_e64 v212, s41, v213\n" \
"v_mfma_f32_16x16x32_bf16 a[28:31], a[172:175], a[132:135], a[28:31]\n" \
"s_or_b32 s7, s7, s41\n" \
"s_mov_b32 s41, 0xff800000\n" \
"v_mfma_f32_16x16x32_bf16 a[28:31], a[236:239], a[196:199], a[28:31]\n" \
"ds_read_b64_tr_b16 a[242:243], v36 offset:41984\n" \
"v_mfma_f32_16x16x32_bf16 a[24:27], a[168:171], a[132:135], a[24:27]\n" \
"s_cmp_lg_u32 s40, 0\n" \
"s_cselect_b32 s47, s42, s41\n" \
"v_mfma_f32_16x16x32_bf16 a[24:27], a[232:235], a[196:199], a[24:27]\n" \
"s_mov_b32 s40, 0xc000\n" \
"s_mov_b32 s41, 0x8000\n" \
"v_mfma_f32_16x16x32_bf16 a[20:23], a[164:167], a[132:135], a[20:23]\n" \
"s_cmp_lg_u32 s4, 0\n" \
"s_cselect_b32 s51, s40, s41\n" \
"v_mfma_f32_16x16x32_bf16 a[20:23], a[228:231], a[196:199], a[20:23]\n" \
"ds_read_b64_tr_b16 a[180:181], v37 offset:32768\n" \
"s_movk_i32 s40, 0x4000\n" \
"v_mfma_f32_16x16x32_bf16 a[16:19], a[160:163], a[132:135], a[16:19]\n" \
"s_cmp_lg_u32 s4, 0\n" \
"s_cselect_b32 s53, s41, s40\n" \
"v_mfma_f32_16x16x32_bf16 a[16:19], a[224:227], a[196:199], a[16:19]\n" \
"s_lshl_b32 s40, s2, 2\n" \
"s_and_b32 s40, s40, 28\n" \
"v_mfma_f32_16x16x32_bf16 a[32:35], a[160:163], a[136:139], a[32:35]\n" \
"s_waitcnt vmcnt(16)\n" \
"v_or_b32_e32 v204, v210, v204\n" \
"v_or_b32_e64 v206, v210, v206\n" \
"v_mfma_f32_16x16x32_bf16 a[32:35], a[224:227], a[200:203], a[32:35]\n" \
"v_mov_b32_e64 v210, 0\n" \
"s_add_u32 s88, s35, 0x1000\n" \
"v_mfma_f32_16x16x32_bf16 a[36:39], a[164:167], a[136:139], a[36:39]\n" \
"ds_read_b64_tr_b16 a[182:183], v37 offset:33792\n" \
"s_bfe_u32 s45, s2, 0x20003\n" \
"v_mfma_f32_16x16x32_bf16 a[36:39], a[228:231], a[200:203], a[36:39]\n" \
"ds_read_b64_tr_b16 a[244:245], v37 offset:40960\n" \
"s_lshr_b32 s44, s3, 1\n" \
"v_mfma_f32_16x16x32_bf16 a[40:43], a[168:171], a[136:139], a[40:43]\n" \
"s_mov_b32 m0, s32\n" \
"s_or_b32 s46, s40, s45\n" \
"v_mfma_f32_16x16x32_bf16 a[40:43], a[232:235], a[200:203], a[40:43]\n" \
"s_mov_b32 exec_lo, 0xffffffff\n" \
"s_mov_b32 exec_hi, 0x0\n" \
"global_load_lds_dword v[188:189], off\n" \
"s_mov_b64 exec, -1\n" \
"v_mfma_f32_16x16x32_bf16 a[44:47], a[172:175], a[136:139], a[44:47]\n" \
"ds_read_b64_tr_b16 a[246:247], v37 offset:41984\n" \
"s_lshl_b32 s43, s44, 15\n" \
"v_mfma_f32_16x16x32_bf16 a[44:47], a[236:239], a[200:203], a[44:47]\n" \
"s_lshl_b32 s41, s5, 14\n" \
"s_lshl_b32 s48, s46, 20\n" \
"v_mfma_f32_16x16x32_bf16 a[60:63], a[172:175], a[140:143], a[60:63]\n" \
"ds_write_b128 v196, v[200:203]\n" \
"v_mfma_f32_16x16x32_bf16 a[60:63], a[236:239], a[204:207], a[60:63]\n" \
"s_or_b32 s41, s43, s41\n" \
"s_add_i32 s49, s48, 0x1000000\n" \
"v_mfma_f32_16x16x32_bf16 a[56:59], a[168:171], a[140:143], a[56:59]\n" \
"ds_read_b64_tr_b16 a[184:185], v38 offset:32768\n" \
"s_or_b32 s42, s49, s41\n" \
"v_mfma_f32_16x16x32_bf16 a[56:59], a[232:235], a[204:207], a[56:59]\n" \
"s_lshl_b32 s4, s4, 16\n" \
"s_or_b32 s41, s42, s4\n" \
"v_mfma_f32_16x16x32_bf16 a[52:55], a[164:167], a[140:143], a[52:55]\n" \
"s_lshl_b32 s4, s6, 16\n" \
"s_or_b32 s42, s42, s4\n" \
"v_mfma_f32_16x16x32_bf16 a[52:55], a[228:231], a[204:207], a[52:55]\n" \
"ds_read_b64_tr_b16 a[186:187], v38 offset:33792\n" \
"s_lshl_b32 s4, s5, 11\n" \
"v_mfma_f32_16x16x32_bf16 a[48:51], a[160:163], a[140:143], a[48:51]\n" \
"s_mul_i32 s5, s7, 0x24800\n" \
"s_lshl_b32 s6, s46, 9\n" \
"v_mfma_f32_16x16x32_bf16 a[48:51], a[224:227], a[204:207], a[48:51]\n" \
"s_lshl_b32 s7, s40, 6\n" \
"s_add_i32 s6, s5, s6\n" \
"v_mfma_f32_16x16x32_bf16 a[64:67], a[160:163], a[144:147], a[64:67]\n" \
"ds_write_b128 v197, v[204:207]\n" \
"v_mfma_f32_16x16x32_bf16 a[64:67], a[224:227], a[208:211], a[64:67]\n" \
"ds_read_b64_tr_b16 a[248:249], v38 offset:40960\n" \
"s_or_b32 s5, s5, s7\n" \
"v_mfma_f32_16x16x32_bf16 a[68:71], a[164:167], a[144:147], a[68:71]\n" \
"s_lshl_b32 s7, s45, 2\n" \
"s_or_b32 s5, s5, s7\n" \
"v_mfma_f32_16x16x32_bf16 a[68:71], a[228:231], a[208:211], a[68:71]\n" \
"ds_read_b64_tr_b16 a[250:251], v38 offset:41984\n" \
"s_lshl_b32 s7, s3, 12\n" \
"v_mfma_f32_16x16x32_bf16 a[72:75], a[168:171], a[144:147], a[72:75]\n" \
"s_or_b32 s50, s49, s7\n" \
"s_lshl_b32 s7, s3, 11\n" \
"v_mfma_f32_16x16x32_bf16 a[72:75], a[232:235], a[208:211], a[72:75]\n" \
"v_lshrrev_b32_e32 v215, 4, v20\n" \
"v_bfe_u32 v216, v20, 4, 3\n" \
"v_mfma_f32_16x16x32_bf16 a[76:79], a[172:175], a[144:147], a[76:79]\n" \
"s_add_i32 s52, s48, s7\n" \
"s_add_i32 s53, s53, s7\n" \
"v_mfma_f32_16x16x32_bf16 a[76:79], a[236:239], a[208:211], a[76:79]\n" \
"ds_read_b64_tr_b16 a[188:189], v39 offset:32768\n" \
"v_mul_u32_u24_e32 v214, 0x480, v214\n" \
"v_mfma_f32_16x16x32_bf16 a[92:95], a[172:175], a[148:151], a[92:95]\n" \
"ds_write_b128 v198, v[208:211]\n" \
"v_mfma_f32_16x16x32_bf16 a[92:95], a[236:239], a[212:215], a[92:95]\n" \
"v_bitop3_b32 v218, v213, v215, 7 bitop3:0x78\n" \
"v_bitop3_b32 v213, v213, v216, 4 bitop3:0x36\n" \
"v_mfma_f32_16x16x32_bf16 a[88:91], a[168:171], a[148:151], a[88:91]\n" \
"ds_read_b64_tr_b16 a[190:191], v39 offset:33792\n" \
"s_lshl_b32 s7, s3, 10\n" \
"v_mfma_f32_16x16x32_bf16 a[88:91], a[232:235], a[212:215], a[88:91]\n" \
"s_or_b32 s4, s43, s4\n" \
"v_lshl_or_b32 v25, v213, 4, v214\n" \
"v_mfma_f32_16x16x32_bf16 a[84:87], a[164:167], a[148:151], a[84:87]\n" \
"v_mul_u32_u24_e32 v213, 0x480, v217\n" \
"v_bitop3_b32 v212, v215, v212, 7 bitop3:0x6c\n" \
"v_mfma_f32_16x16x32_bf16 a[84:87], a[228:231], a[212:215], a[84:87]\n" \
"ds_read_b64_tr_b16 a[252:253], v39 offset:40960\n" \
"s_or_b32 s40, s7, 0x14000\n" \
"v_mfma_f32_16x16x32_bf16 a[80:83], a[160:163], a[148:151], a[80:83]\n" \
"s_lshl_b32 s43, s44, 13\n" \
"s_mulk_i32 s44, 0xe800\n" \
"v_mfma_f32_16x16x32_bf16 a[80:83], a[224:227], a[212:215], a[80:83]\n" \
"s_lshl_b32 s7, s3, 8\n" \
"s_nop 0\n" \
"v_lshl_or_b32 v24, v218, 4, v214\n" \
"v_mfma_f32_16x16x32_bf16 a[96:99], a[160:163], a[152:155], a[96:99]\n" \
"ds_read_b64_tr_b16 a[254:255], v39 offset:41984\n" \
"v_lshl_or_b32 v21, v212, 4, v213\n" \
"v_mfma_f32_16x16x32_bf16 a[96:99], a[224:227], a[216:219], a[96:99]\n" \
"s_add_i32 s44, s44, 0x17000\n" \
"s_or_b32 s45, s7, 0x27000\n" \
"v_mfma_f32_16x16x32_bf16 a[100:103], a[164:167], a[152:155], a[100:103]\n" \
"s_or_b32 s46, s4, 0x10000\n" \
"s_add_i32 s48, s6, 0x3000000\n" \
"v_mfma_f32_16x16x32_bf16 a[100:103], a[228:231], a[216:219], a[100:103]\n" \
"s_add_i32 s49, s5, 0x3004000\n" \
"s_add_i32 s52, s52, 0x1010000\n" \
"v_mfma_f32_16x16x32_bf16 a[104:107], a[168:171], a[152:155], a[104:107]\n" \
"v_mfma_f32_16x16x32_bf16 a[104:107], a[232:235], a[216:219], a[104:107]\n" \
"v_mfma_f32_16x16x32_bf16 a[108:111], a[172:175], a[152:155], a[108:111]\n" \
"v_mfma_f32_16x16x32_bf16 a[108:111], a[236:239], a[216:219], a[108:111]\n" \
"s_waitcnt lgkmcnt(15)\n" \
"v_mfma_f32_16x16x32_bf16 a[124:127], a[172:175], a[156:159], a[124:127]\n" \
"v_mfma_f32_16x16x32_bf16 a[124:127], a[236:239], a[220:223], a[124:127]\n" \
"v_mfma_f32_16x16x32_bf16 a[120:123], a[168:171], a[156:159], a[120:123]\n" \
"v_mfma_f32_16x16x32_bf16 a[120:123], a[232:235], a[220:223], a[120:123]\n" \
"v_mfma_f32_16x16x32_bf16 a[116:119], a[164:167], a[156:159], a[116:119]\n" \
"v_mfma_f32_16x16x32_bf16 a[116:119], a[228:231], a[220:223], a[116:119]\n" \
"v_mfma_f32_16x16x32_bf16 a[112:115], a[160:163], a[156:159], a[112:115]\n" \
"v_mfma_f32_16x16x32_bf16 a[112:115], a[224:227], a[220:223], a[112:115]\n" \
"s_waitcnt vmcnt(1) lgkmcnt(0)\n" \
"s_barrier\n" \
"v_mfma_f32_16x16x32_bf16 v[52:55], a[176:179], a[128:131], v[52:55]\n" \
"s_add_u32 m0, s30, 0x8000\n" \
"s_movk_i32 s84, 0x280\n" \
"v_mfma_f32_16x16x32_bf16 v[52:55], a[240:243], a[192:195], v[52:55]\n" \
"buffer_load_dwordx4 v22, s[64:67], s84 offen offset:0 lds\n" \
"v_mfma_f32_16x16x32_bf16 v[56:59], a[180:183], a[128:131], v[56:59]\n" \
"s_movk_i32 s84, 0x1e80\n" \
"v_mfma_f32_16x16x32_bf16 v[56:59], a[244:247], a[192:195], v[56:59]\n" \
"buffer_load_dwordx4 v23, s[64:67], s84 offen offset:1024 lds\n" \
"v_mfma_f32_16x16x32_bf16 v[60:63], a[184:187], a[128:131], v[60:63]\n" \
"ds_read_b64_tr_b16 a[160:161], v32 offset:0\n" \
"v_mfma_f32_16x16x32_bf16 v[60:63], a[248:251], a[192:195], v[60:63]\n" \
"s_movk_i32 s84, 0x3a80\n" \
"v_mfma_f32_16x16x32_bf16 v[64:67], a[188:191], a[128:131], v[64:67]\n" \
"buffer_load_dwordx4 v22, s[64:67], s84 offen offset:2048 lds\n" \
"v_mfma_f32_16x16x32_bf16 v[64:67], a[252:255], a[192:195], v[64:67]\n" \
"ds_read_b128 a[128:131], v28 offset:16384\n" \
"v_mfma_f32_16x16x32_bf16 v[80:83], a[188:191], a[132:135], v[80:83]\n" \
"ds_read_b128 a[192:195], v29 offset:16384\n" \
"v_mfma_f32_16x16x32_bf16 v[80:83], a[252:255], a[196:199], v[80:83]\n" \
"s_movk_i32 s84, 0x5680\n" \
"s_nop 0\n" \
"v_mfma_f32_16x16x32_bf16 v[76:79], a[184:187], a[132:135], v[76:79]\n" \
"buffer_load_dwordx4 v23, s[64:67], s84 offen offset:3072 lds\n" \
"v_mfma_f32_16x16x32_bf16 v[76:79], a[248:251], a[196:199], v[76:79]\n" \
"s_add_u32 m0, s31, 0x8000\n" \
"s_mov_b32 s84, 0x280000\n" \
"v_mfma_f32_16x16x32_bf16 v[72:75], a[180:183], a[132:135], v[72:75]\n" \
"buffer_load_dwordx4 v26, s[68:71], s84 offen offset:0 lds\n" \
"v_mfma_f32_16x16x32_bf16 v[72:75], a[244:247], a[196:199], v[72:75]\n" \
"ds_read_b64_tr_b16 a[162:163], v32 offset:1024\n" \
"v_mfma_f32_16x16x32_bf16 v[68:71], a[176:179], a[132:135], v[68:71]\n" \
"ds_read_b64_tr_b16 a[224:225], v32 offset:8192\n" \
"v_mfma_f32_16x16x32_bf16 v[68:71], a[240:243], a[196:199], v[68:71]\n" \
"ds_read_b128 a[132:135], v28 offset:18432\n" \
"v_mfma_f32_16x16x32_bf16 v[84:87], a[176:179], a[136:139], v[84:87]\n" \
"ds_read_b128 a[196:199], v29 offset:18432\n" \
"v_mfma_f32_16x16x32_bf16 v[84:87], a[240:243], a[200:203], v[84:87]\n" \
"s_addk_i32 s84, 0x7c00\n" \
"v_mfma_f32_16x16x32_bf16 v[88:91], a[180:183], a[136:139], v[88:91]\n" \
"buffer_load_dwordx4 v26, s[68:71], s84 offen offset:1024 lds\n" \
"v_mfma_f32_16x16x32_bf16 v[88:91], a[244:247], a[200:203], v[88:91]\n" \
"s_addk_i32 s84, 0x7c00\n" \
"v_mfma_f32_16x16x32_bf16 v[92:95], a[184:187], a[136:139], v[92:95]\n" \
"buffer_load_dwordx4 v27, s[68:71], s84 offen offset:2048 lds\n" \
"v_mfma_f32_16x16x32_bf16 v[92:95], a[248:251], a[200:203], v[92:95]\n" \
"s_addk_i32 s84, 0x7c00\n" \
"s_nop 0\n" \
"v_mfma_f32_16x16x32_bf16 v[96:99], a[188:191], a[136:139], v[96:99]\n" \
"buffer_load_dwordx4 v27, s[68:71], s84 offen offset:3072 lds\n" \
"v_mfma_f32_16x16x32_bf16 v[96:99], a[252:255], a[200:203], v[96:99]\n" \
"ds_read_b128 a[136:139], v28 offset:20480\n" \
"v_mfma_f32_16x16x32_bf16 v[112:115], a[188:191], a[140:143], v[112:115]\n" \
"ds_read_b128 a[200:203], v29 offset:20480\n" \
"v_mfma_f32_16x16x32_bf16 v[112:115], a[252:255], a[204:207], v[112:115]\n" \
"s_add_u32 m0, s30, 0x9000\n" \
"s_mov_b32 s84, 0x8280\n" \
"v_mfma_f32_16x16x32_bf16 v[108:111], a[184:187], a[140:143], v[108:111]\n" \
"buffer_load_dwordx4 v22, s[64:67], s84 offen offset:0 lds\n" \
"v_mfma_f32_16x16x32_bf16 v[108:111], a[248:251], a[204:207], v[108:111]\n" \
"s_addk_i32 s84, 0x1c00\n" \
"v_mfma_f32_16x16x32_bf16 v[104:107], a[180:183], a[140:143], v[104:107]\n" \
"buffer_load_dwordx4 v23, s[64:67], s84 offen offset:1024 lds\n" \
"v_mfma_f32_16x16x32_bf16 v[104:107], a[244:247], a[204:207], v[104:107]\n" \
"s_addk_i32 s84, 0x1c00\n" \
"v_mfma_f32_16x16x32_bf16 v[100:103], a[176:179], a[140:143], v[100:103]\n" \
"buffer_load_dwordx4 v22, s[64:67], s84 offen offset:2048 lds\n" \
"v_mfma_f32_16x16x32_bf16 v[100:103], a[240:243], a[204:207], v[100:103]\n" \
"ds_read_b128 a[140:143], v28 offset:22528\n" \
"v_mfma_f32_16x16x32_bf16 v[116:119], a[176:179], a[144:147], v[116:119]\n" \
"ds_read_b128 a[204:207], v29 offset:22528\n" \
"v_mfma_f32_16x16x32_bf16 v[116:119], a[240:243], a[208:211], v[116:119]\n" \
"s_addk_i32 s84, 0x1c00\n" \
"v_mfma_f32_16x16x32_bf16 v[120:123], a[180:183], a[144:147], v[120:123]\n" \
"buffer_load_dwordx4 v23, s[64:67], s84 offen offset:3072 lds\n" \
"v_mfma_f32_16x16x32_bf16 v[120:123], a[244:247], a[208:211], v[120:123]\n" \
"s_add_u32 m0, s31, 0xc000\n" \
"s_mov_b32 s84, 0x280000\n" \
"v_mfma_f32_16x16x32_bf16 v[124:127], a[184:187], a[144:147], v[124:127]\n" \
"buffer_load_dwordx4 v26, s[72:75], s84 offen offset:0 lds\n" \
"v_mfma_f32_16x16x32_bf16 v[124:127], a[248:251], a[208:211], v[124:127]\n" \
"s_addk_i32 s84, 0x7c00\n" \
"v_mfma_f32_16x16x32_bf16 v[128:131], a[188:191], a[144:147], v[128:131]\n" \
"buffer_load_dwordx4 v26, s[72:75], s84 offen offset:1024 lds\n" \
"v_mfma_f32_16x16x32_bf16 v[128:131], a[252:255], a[208:211], v[128:131]\n" \
"ds_read_b128 a[144:147], v28 offset:0\n" \
"v_mfma_f32_16x16x32_bf16 v[144:147], a[188:191], a[148:151], v[144:147]\n" \
"ds_read_b128 a[208:211], v29 offset:0\n" \
"v_mfma_f32_16x16x32_bf16 v[144:147], a[252:255], a[212:215], v[144:147]\n" \
"ds_read_b128 v[200:203], v199 offset:0\n" \
"v_mfma_f32_16x16x32_bf16 v[140:143], a[184:187], a[148:151], v[140:143]\n" \
"ds_read_b128 v[204:207], v199 offset:4096\n" \
"v_mfma_f32_16x16x32_bf16 v[140:143], a[248:251], a[212:215], v[140:143]\n" \
"ds_read_b64_tr_b16 a[226:227], v32 offset:9216\n" \
"v_mfma_f32_16x16x32_bf16 v[136:139], a[180:183], a[148:151], v[136:139]\n" \
"ds_read_b64_tr_b16 a[164:165], v33 offset:0\n" \
"v_mfma_f32_16x16x32_bf16 v[136:139], a[244:247], a[212:215], v[136:139]\n" \
"ds_read_b128 v[208:211], v199 offset:8192\n" \
"v_mfma_f32_16x16x32_bf16 v[132:135], a[176:179], a[148:151], v[132:135]\n" \
"ds_read_b64_tr_b16 a[166:167], v33 offset:1024\n" \
"v_mfma_f32_16x16x32_bf16 v[132:135], a[240:243], a[212:215], v[132:135]\n" \
"ds_read_b128 a[148:151], v28 offset:2048\n" \
"v_mfma_f32_16x16x32_bf16 v[148:151], a[176:179], a[152:155], v[148:151]\n" \
"ds_read_b128 a[212:215], v29 offset:2048\n" \
"v_mfma_f32_16x16x32_bf16 v[148:151], a[240:243], a[216:219], v[148:151]\n" \
"ds_read_b64_tr_b16 a[228:229], v33 offset:8192\n" \
"v_mfma_f32_16x16x32_bf16 v[152:155], a[180:183], a[152:155], v[152:155]\n" \
"ds_read_b64_tr_b16 a[230:231], v33 offset:9216\n" \
"v_mfma_f32_16x16x32_bf16 v[152:155], a[244:247], a[216:219], v[152:155]\n" \
"ds_read_b64_tr_b16 a[168:169], v34 offset:0\n" \
"v_mfma_f32_16x16x32_bf16 v[156:159], a[184:187], a[152:155], v[156:159]\n" \
"ds_read_b64_tr_b16 a[170:171], v34 offset:1024\n" \
"v_mfma_f32_16x16x32_bf16 v[156:159], a[248:251], a[216:219], v[156:159]\n" \
"ds_read_b64_tr_b16 a[232:233], v34 offset:8192\n" \
"v_mfma_f32_16x16x32_bf16 v[160:163], a[188:191], a[152:155], v[160:163]\n" \
"ds_read_b64_tr_b16 a[234:235], v34 offset:9216\n" \
"v_mfma_f32_16x16x32_bf16 v[160:163], a[252:255], a[216:219], v[160:163]\n" \
"ds_read_b128 a[152:155], v28 offset:4096\n" \
"v_mfma_f32_16x16x32_bf16 v[176:179], a[188:191], a[156:159], v[176:179]\n" \
"ds_read_b128 a[216:219], v29 offset:4096\n" \
"v_mfma_f32_16x16x32_bf16 v[176:179], a[252:255], a[220:223], v[176:179]\n" \
"ds_read_b64_tr_b16 a[172:173], v35 offset:0\n" \
"v_mfma_f32_16x16x32_bf16 v[172:175], a[184:187], a[156:159], v[172:175]\n" \
"ds_read_b64_tr_b16 a[174:175], v35 offset:1024\n" \
"v_mfma_f32_16x16x32_bf16 v[172:175], a[248:251], a[220:223], v[172:175]\n" \
"ds_read_b64_tr_b16 a[236:237], v35 offset:8192\n" \
"v_mfma_f32_16x16x32_bf16 v[168:171], a[180:183], a[156:159], v[168:171]\n" \
"ds_read_b64_tr_b16 a[238:239], v35 offset:9216\n" \
"v_mfma_f32_16x16x32_bf16 v[168:171], a[244:247], a[220:223], v[168:171]\n" \
"v_mfma_f32_16x16x32_bf16 v[164:167], a[176:179], a[156:159], v[164:167]\n" \
"v_mfma_f32_16x16x32_bf16 v[164:167], a[240:243], a[220:223], v[164:167]\n" \
"ds_read_b128 a[156:159], v28 offset:6144\n" \
"ds_read_b128 a[220:223], v29 offset:6144\n" \
"s_waitcnt lgkmcnt(15)\n" \
"v_mfma_f32_16x16x32_bf16 a[0:3], a[160:163], a[128:131], a[0:3]\n" \
"s_addk_i32 s84, 0x7c00\n" \
"v_mfma_f32_16x16x32_bf16 a[0:3], a[224:227], a[192:195], a[0:3]\n" \
"buffer_load_dwordx4 v27, s[72:75], s84 offen offset:2048 lds\n" \
"v_mfma_f32_16x16x32_bf16 a[4:7], a[164:167], a[128:131], a[4:7]\n" \
"ds_read_b64_tr_b16 a[176:177], v36 offset:0\n" \
"s_waitcnt lgkmcnt(13)\n" \
"v_mfma_f32_16x16x32_bf16 a[4:7], a[228:231], a[192:195], a[4:7]\n" \
"s_addk_i32 s84, 0x7c00\n" \
"s_waitcnt lgkmcnt(11)\n" \
"v_mfma_f32_16x16x32_bf16 a[8:11], a[168:171], a[128:131], a[8:11]\n" \
"buffer_load_dwordx4 v27, s[72:75], s84 offen offset:3072 lds\n" \
"s_waitcnt lgkmcnt(9)\n" \
"v_mfma_f32_16x16x32_bf16 a[8:11], a[232:235], a[192:195], a[8:11]\n" \
"ds_read_b64_tr_b16 a[178:179], v36 offset:1024\n" \
"s_waitcnt lgkmcnt(6)\n" \
"v_mfma_f32_16x16x32_bf16 a[12:15], a[172:175], a[128:131], a[12:15]\n" \
"ds_read_b64_tr_b16 a[240:241], v36 offset:8192\n" \
"s_waitcnt lgkmcnt(5)\n" \
"v_mfma_f32_16x16x32_bf16 a[12:15], a[236:239], a[192:195], a[12:15]\n" \
"v_mfma_f32_16x16x32_bf16 a[28:31], a[172:175], a[132:135], a[28:31]\n" \
"v_mfma_f32_16x16x32_bf16 a[28:31], a[236:239], a[196:199], a[28:31]\n" \
"ds_read_b64_tr_b16 a[242:243], v36 offset:9216\n" \
"v_mfma_f32_16x16x32_bf16 a[24:27], a[168:171], a[132:135], a[24:27]\n" \
"v_mfma_f32_16x16x32_bf16 a[24:27], a[232:235], a[196:199], a[24:27]\n" \
"v_mfma_f32_16x16x32_bf16 a[20:23], a[164:167], a[132:135], a[20:23]\n" \
"v_mfma_f32_16x16x32_bf16 a[20:23], a[228:231], a[196:199], a[20:23]\n" \
"ds_read_b64_tr_b16 a[180:181], v37 offset:0\n" \
"v_mfma_f32_16x16x32_bf16 a[16:19], a[160:163], a[132:135], a[16:19]\n" \
"v_mfma_f32_16x16x32_bf16 a[16:19], a[224:227], a[196:199], a[16:19]\n" \
"v_mfma_f32_16x16x32_bf16 a[32:35], a[160:163], a[136:139], a[32:35]\n" \
"ds_read_b64_tr_b16 a[182:183], v37 offset:1024\n" \
"v_mfma_f32_16x16x32_bf16 a[32:35], a[224:227], a[200:203], a[32:35]\n" \
"v_mfma_f32_16x16x32_bf16 a[36:39], a[164:167], a[136:139], a[36:39]\n" \
"v_mfma_f32_16x16x32_bf16 a[36:39], a[228:231], a[200:203], a[36:39]\n" \
"buffer_store_dwordx4 v[200:203], v20, s[80:83], s35 offen sc1\n" \
"v_mfma_f32_16x16x32_bf16 a[40:43], a[168:171], a[136:139], a[40:43]\n" \
"s_mov_b32 m0, s32\n" \
"ds_read_b64_tr_b16 a[244:245], v37 offset:8192\n" \
"v_mfma_f32_16x16x32_bf16 a[40:43], a[232:235], a[200:203], a[40:43]\n" \
"s_mov_b32 exec_lo, 0x0\n" \
"s_mov_b32 exec_hi, 0xffffffff\n" \
"global_load_lds_dword v[188:189], off\n" \
"s_mov_b64 exec, -1\n" \
"v_mfma_f32_16x16x32_bf16 a[44:47], a[172:175], a[136:139], a[44:47]\n" \
"ds_read_b64_tr_b16 a[246:247], v37 offset:9216\n" \
"v_mfma_f32_16x16x32_bf16 a[44:47], a[236:239], a[200:203], a[44:47]\n" \
"v_mfma_f32_16x16x32_bf16 a[60:63], a[172:175], a[140:143], a[60:63]\n" \
"v_mfma_f32_16x16x32_bf16 a[60:63], a[236:239], a[204:207], a[60:63]\n" \
"v_mfma_f32_16x16x32_bf16 a[56:59], a[168:171], a[140:143], a[56:59]\n" \
"ds_read_b64_tr_b16 a[184:185], v38 offset:0\n" \
"v_mfma_f32_16x16x32_bf16 a[56:59], a[232:235], a[204:207], a[56:59]\n" \
"v_mfma_f32_16x16x32_bf16 a[52:55], a[164:167], a[140:143], a[52:55]\n" \
"buffer_store_dwordx4 v[204:207], v20, s[80:83], s88 offen sc1\n" \
"v_mfma_f32_16x16x32_bf16 a[52:55], a[228:231], a[204:207], a[52:55]\n" \
"ds_read_b64_tr_b16 a[186:187], v38 offset:1024\n" \
"v_mfma_f32_16x16x32_bf16 a[48:51], a[160:163], a[140:143], a[48:51]\n" \
"v_mfma_f32_16x16x32_bf16 a[48:51], a[224:227], a[204:207], a[48:51]\n" \
"v_mfma_f32_16x16x32_bf16 a[64:67], a[160:163], a[144:147], a[64:67]\n" \
"ds_read_b64_tr_b16 a[248:249], v38 offset:8192\n" \
"v_mfma_f32_16x16x32_bf16 a[64:67], a[224:227], a[208:211], a[64:67]\n" \
"v_mfma_f32_16x16x32_bf16 a[68:71], a[164:167], a[144:147], a[68:71]\n" \
"v_mfma_f32_16x16x32_bf16 a[68:71], a[228:231], a[208:211], a[68:71]\n" \
"ds_read_b64_tr_b16 a[250:251], v38 offset:9216\n" \
"v_mfma_f32_16x16x32_bf16 a[72:75], a[168:171], a[144:147], a[72:75]\n" \
"v_mfma_f32_16x16x32_bf16 a[72:75], a[232:235], a[208:211], a[72:75]\n" \
"v_mfma_f32_16x16x32_bf16 a[76:79], a[172:175], a[144:147], a[76:79]\n" \
"buffer_store_dwordx4 v[208:211], v20, s[80:83], s36 offen sc1\n" \
"v_mfma_f32_16x16x32_bf16 a[76:79], a[236:239], a[208:211], a[76:79]\n" \
"ds_read_b64_tr_b16 a[188:189], v39 offset:0\n" \
"v_mfma_f32_16x16x32_bf16 a[92:95], a[172:175], a[148:151], a[92:95]\n" \
"v_mfma_f32_16x16x32_bf16 a[92:95], a[236:239], a[212:215], a[92:95]\n" \
"v_mfma_f32_16x16x32_bf16 a[88:91], a[168:171], a[148:151], a[88:91]\n" \
"ds_read_b64_tr_b16 a[190:191], v39 offset:1024\n" \
"v_mfma_f32_16x16x32_bf16 a[88:91], a[232:235], a[212:215], a[88:91]\n" \
"v_mfma_f32_16x16x32_bf16 a[84:87], a[164:167], a[148:151], a[84:87]\n" \
"v_mfma_f32_16x16x32_bf16 a[84:87], a[228:231], a[212:215], a[84:87]\n" \
"ds_read_b64_tr_b16 a[252:253], v39 offset:8192\n" \
"v_mfma_f32_16x16x32_bf16 a[80:83], a[160:163], a[148:151], a[80:83]\n" \
"v_mfma_f32_16x16x32_bf16 a[80:83], a[224:227], a[212:215], a[80:83]\n" \
"v_mfma_f32_16x16x32_bf16 a[96:99], a[160:163], a[152:155], a[96:99]\n" \
"ds_read_b64_tr_b16 a[254:255], v39 offset:9216\n" \
"v_mfma_f32_16x16x32_bf16 a[96:99], a[224:227], a[216:219], a[96:99]\n" \
"v_mfma_f32_16x16x32_bf16 a[100:103], a[164:167], a[152:155], a[100:103]\n" \
".fill (L_pb + 12000 - .) / 4, 4, 0xBF800000\n" \
"L_pp3:\n" \
"s_cbranch_execz L_pret\n" \
"v_mfma_f32_16x16x32_bf16 a[100:103], a[228:231], a[216:219], a[100:103]\n" \
"v_mfma_f32_16x16x32_bf16 a[104:107], a[168:171], a[152:155], a[104:107]\n" \
"v_mfma_f32_16x16x32_bf16 a[104:107], a[232:235], a[216:219], a[104:107]\n" \
"v_mfma_f32_16x16x32_bf16 a[108:111], a[172:175], a[152:155], a[108:111]\n" \
"v_mfma_f32_16x16x32_bf16 a[108:111], a[236:239], a[216:219], a[108:111]\n" \
"s_waitcnt lgkmcnt(15)\n" \
"v_mfma_f32_16x16x32_bf16 a[124:127], a[172:175], a[156:159], a[124:127]\n" \
"v_mfma_f32_16x16x32_bf16 a[124:127], a[236:239], a[220:223], a[124:127]\n" \
"v_mfma_f32_16x16x32_bf16 a[120:123], a[168:171], a[156:159], a[120:123]\n" \
"v_mfma_f32_16x16x32_bf16 a[120:123], a[232:235], a[220:223], a[120:123]\n" \
"v_mfma_f32_16x16x32_bf16 a[116:119], a[164:167], a[156:159], a[116:119]\n" \
"v_mfma_f32_16x16x32_bf16 a[116:119], a[228:231], a[220:223], a[116:119]\n" \
"v_mfma_f32_16x16x32_bf16 a[112:115], a[160:163], a[156:159], a[112:115]\n" \
"v_mfma_f32_16x16x32_bf16 a[112:115], a[224:227], a[220:223], a[112:115]\n" \
"s_waitcnt vmcnt(4) lgkmcnt(0)\n" \
"s_barrier\n" \
"v_mfma_f32_16x16x32_bf16 v[52:55], a[176:179], a[128:131], v[52:55]\n" \
"s_add_u32 m0, s30, 0x0\n" \
"s_movk_i32 s84, 0x300\n" \
"v_mfma_f32_16x16x32_bf16 v[52:55], a[240:243], a[192:195], v[52:55]\n" \
"buffer_load_dwordx4 v22, s[64:67], s84 offen offset:0 lds\n" \
"v_mfma_f32_16x16x32_bf16 v[56:59], a[180:183], a[128:131], v[56:59]\n" \
"ds_read_b64_tr_b16 a[160:161], v32 offset:32768\n" \
"v_mfma_f32_16x16x32_bf16 v[56:59], a[244:247], a[192:195], v[56:59]\n" \
"s_movk_i32 s84, 0x1f00\n" \
"s_nop 0\n" \
"v_mfma_f32_16x16x32_bf16 v[60:63], a[184:187], a[128:131], v[60:63]\n" \
"buffer_load_dwordx4 v23, s[64:67], s84 offen offset:1024 lds\n" \
"v_mfma_f32_16x16x32_bf16 v[60:63], a[248:251], a[192:195], v[60:63]\n" \
"ds_read_b64_tr_b16 a[162:163], v32 offset:33792\n" \
"v_mfma_f32_16x16x32_bf16 v[64:67], a[188:191], a[128:131], v[64:67]\n" \
"ds_read_b64_tr_b16 a[224:225], v32 offset:40960\n" \
"v_mfma_f32_16x16x32_bf16 v[64:67], a[252:255], a[192:195], v[64:67]\n" \
"ds_read_b128 a[128:131], v28 offset:49152\n" \
"v_mfma_f32_16x16x32_bf16 v[80:83], a[188:191], a[132:135], v[80:83]\n" \
"ds_read_b128 a[192:195], v29 offset:49152\n" \
"v_mfma_f32_16x16x32_bf16 v[80:83], a[252:255], a[196:199], v[80:83]\n" \
"s_movk_i32 s84, 0x3b00\n" \
"v_mfma_f32_16x16x32_bf16 v[76:79], a[184:187], a[132:135], v[76:79]\n" \
"buffer_load_dwordx4 v22, s[64:67], s84 offen offset:2048 lds\n" \
"v_mfma_f32_16x16x32_bf16 v[76:79], a[248:251], a[196:199], v[76:79]\n" \
"s_movk_i32 s84, 0x5700\n" \
"v_mfma_f32_16x16x32_bf16 v[72:75], a[180:183], a[132:135], v[72:75]\n" \
"buffer_load_dwordx4 v23, s[64:67], s84 offen offset:3072 lds\n" \
"v_mfma_f32_16x16x32_bf16 v[72:75], a[244:247], a[196:199], v[72:75]\n" \
"s_add_u32 m0, s31, 0x0\n" \
"s_mov_b32 s84, 0x300000\n" \
"v_mfma_f32_16x16x32_bf16 v[68:71], a[176:179], a[132:135], v[68:71]\n" \
"buffer_load_dwordx4 v26, s[68:71], s84 offen offset:0 lds\n" \
"v_mfma_f32_16x16x32_bf16 v[68:71], a[240:243], a[196:199], v[68:71]\n" \
"ds_read_b128 a[132:135], v28 offset:51200\n" \
"v_mfma_f32_16x16x32_bf16 v[84:87], a[176:179], a[136:139], v[84:87]\n" \
"ds_read_b128 a[196:199], v29 offset:51200\n" \
"v_mfma_f32_16x16x32_bf16 v[84:87], a[240:243], a[200:203], v[84:87]\n" \
"s_addk_i32 s84, 0x7c00\n" \
"v_mfma_f32_16x16x32_bf16 v[88:91], a[180:183], a[136:139], v[88:91]\n" \
"buffer_load_dwordx4 v26, s[68:71], s84 offen offset:1024 lds\n" \
"v_mfma_f32_16x16x32_bf16 v[88:91], a[244:247], a[200:203], v[88:91]\n" \
"ds_read_b64_tr_b16 a[226:227], v32 offset:41984\n" \
"v_mfma_f32_16x16x32_bf16 v[92:95], a[184:187], a[136:139], v[92:95]\n" \
"s_addk_i32 s84, 0x7c00\n" \
"v_mfma_f32_16x16x32_bf16 v[92:95], a[248:251], a[200:203], v[92:95]\n" \
"buffer_load_dwordx4 v27, s[68:71], s84 offen offset:2048 lds\n" \
"v_mfma_f32_16x16x32_bf16 v[96:99], a[188:191], a[136:139], v[96:99]\n" \
"ds_read_b64_tr_b16 a[164:165], v33 offset:32768\n" \
"v_mfma_f32_16x16x32_bf16 v[96:99], a[252:255], a[200:203], v[96:99]\n" \
"ds_read_b128 a[136:139], v28 offset:53248\n" \
"v_mfma_f32_16x16x32_bf16 v[112:115], a[188:191], a[140:143], v[112:115]\n" \
"ds_read_b128 a[200:203], v29 offset:53248\n" \
"v_mfma_f32_16x16x32_bf16 v[112:115], a[252:255], a[204:207], v[112:115]\n" \
"s_addk_i32 s84, 0x7c00\n" \
"v_mfma_f32_16x16x32_bf16 v[108:111], a[184:187], a[140:143], v[108:111]\n" \
"buffer_load_dwordx4 v27, s[68:71], s84 offen offset:3072 lds\n" \
"v_mfma_f32_16x16x32_bf16 v[108:111], a[248:251], a[204:207], v[108:111]\n" \
"s_add_u32 m0, s30, 0x1000\n" \
"s_mov_b32 s84, 0x8300\n" \
"v_mfma_f32_16x16x32_bf16 v[104:107], a[180:183], a[140:143], v[104:107]\n" \
"buffer_load_dwordx4 v22, s[64:67], s84 offen offset:0 lds\n" \
"v_mfma_f32_16x16x32_bf16 v[104:107], a[244:247], a[204:207], v[104:107]\n" \
"ds_read_b64_tr_b16 a[166:167], v33 offset:33792\n" \
"v_mfma_f32_16x16x32_bf16 v[100:103], a[176:179], a[140:143], v[100:103]\n" \
"ds_read_b64_tr_b16 a[228:229], v33 offset:40960\n" \
"v_mfma_f32_16x16x32_bf16 v[100:103], a[240:243], a[204:207], v[100:103]\n" \
"ds_read_b128 a[140:143], v28 offset:55296\n" \
"v_mfma_f32_16x16x32_bf16 v[116:119], a[176:179], a[144:147], v[116:119]\n" \
"ds_read_b128 a[204:207], v29 offset:55296\n" \
"v_mfma_f32_16x16x32_bf16 v[116:119], a[240:243], a[208:211], v[116:119]\n" \
"s_addk_i32 s84, 0x1c00\n" \
"v_mfma_f32_16x16x32_bf16 v[120:123], a[180:183], a[144:147], v[120:123]\n" \
"buffer_load_dwordx4 v23, s[64:67], s84 offen offset:1024 lds\n" \
"v_mfma_f32_16x16x32_bf16 v[120:123], a[244:247], a[208:211], v[120:123]\n" \
"s_addk_i32 s84, 0x1c00\n" \
"v_mfma_f32_16x16x32_bf16 v[124:127], a[184:187], a[144:147], v[124:127]\n" \
"buffer_load_dwordx4 v22, s[64:67], s84 offen offset:2048 lds\n" \
"v_mfma_f32_16x16x32_bf16 v[124:127], a[248:251], a[208:211], v[124:127]\n" \
"s_addk_i32 s84, 0x1c00\n" \
"s_nop 0\n" \
"v_mfma_f32_16x16x32_bf16 v[128:131], a[188:191], a[144:147], v[128:131]\n" \
"buffer_load_dwordx4 v23, s[64:67], s84 offen offset:3072 lds\n" \
"v_mfma_f32_16x16x32_bf16 v[128:131], a[252:255], a[208:211], v[128:131]\n" \
"ds_read_b128 a[144:147], v28 offset:32768\n" \
"v_mfma_f32_16x16x32_bf16 v[144:147], a[188:191], a[148:151], v[144:147]\n" \
"ds_read_b128 a[208:211], v29 offset:32768\n" \
"v_mfma_f32_16x16x32_bf16 v[144:147], a[252:255], a[212:215], v[144:147]\n" \
"s_add_u32 m0, s31, 0x4000\n" \
"s_mov_b32 s84, 0x300000\n" \
"v_mfma_f32_16x16x32_bf16 v[140:143], a[184:187], a[148:151], v[140:143]\n" \
"buffer_load_dwordx4 v26, s[72:75], s84 offen offset:0 lds\n" \
"v_mfma_f32_16x16x32_bf16 v[140:143], a[248:251], a[212:215], v[140:143]\n" \
"ds_read_b64_tr_b16 a[230:231], v33 offset:41984\n" \
"v_mfma_f32_16x16x32_bf16 v[136:139], a[180:183], a[148:151], v[136:139]\n" \
"s_addk_i32 s84, 0x7c00\n" \
"s_nop 0\n" \
"v_mfma_f32_16x16x32_bf16 v[136:139], a[244:247], a[212:215], v[136:139]\n" \
"buffer_load_dwordx4 v26, s[72:75], s84 offen offset:1024 lds\n" \
"v_mfma_f32_16x16x32_bf16 v[132:135], a[176:179], a[148:151], v[132:135]\n" \
"ds_read_b64_tr_b16 a[168:169], v34 offset:32768\n" \
"v_mfma_f32_16x16x32_bf16 v[132:135], a[240:243], a[212:215], v[132:135]\n" \
"ds_read_b128 a[148:151], v28 offset:34816\n" \
"v_mfma_f32_16x16x32_bf16 v[148:151], a[176:179], a[152:155], v[148:151]\n" \
"ds_read_b128 a[212:215], v29 offset:34816\n" \
"v_mfma_f32_16x16x32_bf16 v[148:151], a[240:243], a[216:219], v[148:151]\n" \
"ds_read_b64_tr_b16 a[170:171], v34 offset:33792\n" \
"v_mfma_f32_16x16x32_bf16 v[152:155], a[180:183], a[152:155], v[152:155]\n" \
"ds_read_b64_tr_b16 a[232:233], v34 offset:40960\n" \
"v_mfma_f32_16x16x32_bf16 v[152:155], a[244:247], a[216:219], v[152:155]\n" \
"ds_read_b64_tr_b16 a[234:235], v34 offset:41984\n" \
"v_mfma_f32_16x16x32_bf16 v[156:159], a[184:187], a[152:155], v[156:159]\n" \
"ds_read_b64_tr_b16 a[172:173], v35 offset:32768\n" \
"v_mfma_f32_16x16x32_bf16 v[156:159], a[248:251], a[216:219], v[156:159]\n" \
"ds_read_b64_tr_b16 a[174:175], v35 offset:33792\n" \
"v_mfma_f32_16x16x32_bf16 v[160:163], a[188:191], a[152:155], v[160:163]\n" \
"ds_read_b64_tr_b16 a[236:237], v35 offset:40960\n" \
"v_mfma_f32_16x16x32_bf16 v[160:163], a[252:255], a[216:219], v[160:163]\n" \
"ds_read_b128 a[152:155], v28 offset:36864\n" \
"v_mfma_f32_16x16x32_bf16 v[176:179], a[188:191], a[156:159], v[176:179]\n" \
"ds_read_b128 a[216:219], v29 offset:36864\n" \
"v_mfma_f32_16x16x32_bf16 v[176:179], a[252:255], a[220:223], v[176:179]\n" \
"ds_read_b64_tr_b16 a[238:239], v35 offset:41984\n" \
"v_mfma_f32_16x16x32_bf16 v[172:175], a[184:187], a[156:159], v[172:175]\n" \
"v_mfma_f32_16x16x32_bf16 v[172:175], a[248:251], a[220:223], v[172:175]\n" \
"v_mfma_f32_16x16x32_bf16 v[168:171], a[180:183], a[156:159], v[168:171]\n" \
"v_mfma_f32_16x16x32_bf16 v[168:171], a[244:247], a[220:223], v[168:171]\n" \
"v_mfma_f32_16x16x32_bf16 v[164:167], a[176:179], a[156:159], v[164:167]\n" \
"v_mfma_f32_16x16x32_bf16 v[164:167], a[240:243], a[220:223], v[164:167]\n" \
"ds_read_b128 a[156:159], v28 offset:38912\n" \
"ds_read_b128 a[220:223], v29 offset:38912\n" \
"s_waitcnt lgkmcnt(15)\n" \
"v_mfma_f32_16x16x32_bf16 a[0:3], a[160:163], a[128:131], a[0:3]\n" \
"s_addk_i32 s84, 0x7c00\n" \
"v_mfma_f32_16x16x32_bf16 a[0:3], a[224:227], a[192:195], a[0:3]\n" \
"buffer_load_dwordx4 v27, s[72:75], s84 offen offset:2048 lds\n" \
"v_mfma_f32_16x16x32_bf16 a[4:7], a[164:167], a[128:131], a[4:7]\n" \
"ds_read_b64_tr_b16 a[176:177], v36 offset:32768\n" \
"s_waitcnt lgkmcnt(15)\n" \
"v_mfma_f32_16x16x32_bf16 a[4:7], a[228:231], a[192:195], a[4:7]\n" \
"s_addk_i32 s84, 0x7c00\n" \
"s_waitcnt lgkmcnt(11)\n" \
"v_mfma_f32_16x16x32_bf16 a[8:11], a[168:171], a[128:131], a[8:11]\n" \
"buffer_load_dwordx4 v27, s[72:75], s84 offen offset:3072 lds\n" \
"s_waitcnt lgkmcnt(9)\n" \
"v_mfma_f32_16x16x32_bf16 a[8:11], a[232:235], a[192:195], a[8:11]\n" \
"ds_read_b64_tr_b16 a[178:179], v36 offset:33792\n" \
"s_waitcnt lgkmcnt(8)\n" \
"v_mfma_f32_16x16x32_bf16 a[12:15], a[172:175], a[128:131], a[12:15]\n" \
"ds_read_b64_tr_b16 a[240:241], v36 offset:40960\n" \
"s_waitcnt lgkmcnt(5)\n" \
"v_mfma_f32_16x16x32_bf16 a[12:15], a[236:239], a[192:195], a[12:15]\n" \
"v_mfma_f32_16x16x32_bf16 a[28:31], a[172:175], a[132:135], a[28:31]\n" \
"v_mfma_f32_16x16x32_bf16 a[28:31], a[236:239], a[196:199], a[28:31]\n" \
"ds_read_b64_tr_b16 a[242:243], v36 offset:41984\n" \
"v_mfma_f32_16x16x32_bf16 a[24:27], a[168:171], a[132:135], a[24:27]\n" \
"v_mfma_f32_16x16x32_bf16 a[24:27], a[232:235], a[196:199], a[24:27]\n" \
"v_mfma_f32_16x16x32_bf16 a[20:23], a[164:167], a[132:135], a[20:23]\n" \
"v_mfma_f32_16x16x32_bf16 a[20:23], a[228:231], a[196:199], a[20:23]\n" \
"ds_read_b64_tr_b16 a[180:181], v37 offset:32768\n" \
"v_mfma_f32_16x16x32_bf16 a[16:19], a[160:163], a[132:135], a[16:19]\n" \
"v_mfma_f32_16x16x32_bf16 a[16:19], a[224:227], a[196:199], a[16:19]\n" \
"v_mfma_f32_16x16x32_bf16 a[32:35], a[160:163], a[136:139], a[32:35]\n" \
"ds_read_b64_tr_b16 a[182:183], v37 offset:33792\n" \
"v_mfma_f32_16x16x32_bf16 a[32:35], a[224:227], a[200:203], a[32:35]\n" \
"v_mfma_f32_16x16x32_bf16 a[36:39], a[164:167], a[136:139], a[36:39]\n" \
"v_lshlrev_b32_e32 v45, 3, v20\n" \
"v_add_u32_e32 v45, s31, v45\n" \
"v_mfma_f32_16x16x32_bf16 a[36:39], a[228:231], a[200:203], a[36:39]\n" \
"s_getpc_b64 s[86:87]\n" \
"s_sub_u32 s86, s86, 0xf800\n" \
"s_subb_u32 s87, s87, 0\n" \
"s_mov_b32 m0, s32\n" \
"s_nop 0\n" \
"v_mfma_f32_16x16x32_bf16 a[40:43], a[168:171], a[136:139], a[40:43]\n" \
"global_load_lds_dword v45, s[86:87]\n" \
"v_mfma_f32_16x16x32_bf16 a[40:43], a[232:235], a[200:203], a[40:43]\n" \
"ds_read_b64_tr_b16 a[244:245], v37 offset:40960\n" \
"v_mfma_f32_16x16x32_bf16 a[44:47], a[172:175], a[136:139], a[44:47]\n" \
"ds_read_b64_tr_b16 a[246:247], v37 offset:41984\n" \
"v_mfma_f32_16x16x32_bf16 a[44:47], a[236:239], a[200:203], a[44:47]\n" \
"v_mfma_f32_16x16x32_bf16 a[60:63], a[172:175], a[140:143], a[60:63]\n" \
"v_mfma_f32_16x16x32_bf16 a[60:63], a[236:239], a[204:207], a[60:63]\n" \
"v_mfma_f32_16x16x32_bf16 a[56:59], a[168:171], a[140:143], a[56:59]\n" \
"ds_read_b64_tr_b16 a[184:185], v38 offset:32768\n" \
"v_mfma_f32_16x16x32_bf16 a[56:59], a[232:235], a[204:207], a[56:59]\n" \
"v_mfma_f32_16x16x32_bf16 a[52:55], a[164:167], a[140:143], a[52:55]\n" \
"v_mfma_f32_16x16x32_bf16 a[52:55], a[228:231], a[204:207], a[52:55]\n" \
"ds_read_b64_tr_b16 a[186:187], v38 offset:33792\n" \
"v_mfma_f32_16x16x32_bf16 a[48:51], a[160:163], a[140:143], a[48:51]\n" \
"v_mfma_f32_16x16x32_bf16 a[48:51], a[224:227], a[204:207], a[48:51]\n" \
"v_mfma_f32_16x16x32_bf16 a[64:67], a[160:163], a[144:147], a[64:67]\n" \
"ds_read_b64_tr_b16 a[248:249], v38 offset:40960\n" \
"v_mfma_f32_16x16x32_bf16 a[64:67], a[224:227], a[208:211], a[64:67]\n" \
"v_mfma_f32_16x16x32_bf16 a[68:71], a[164:167], a[144:147], a[68:71]\n" \
"v_mfma_f32_16x16x32_bf16 a[68:71], a[228:231], a[208:211], a[68:71]\n" \
"ds_read_b64_tr_b16 a[250:251], v38 offset:41984\n" \
"v_mfma_f32_16x16x32_bf16 a[72:75], a[168:171], a[144:147], a[72:75]\n" \
"v_mfma_f32_16x16x32_bf16 a[72:75], a[232:235], a[208:211], a[72:75]\n" \
"v_mfma_f32_16x16x32_bf16 a[76:79], a[172:175], a[144:147], a[76:79]\n" \
"v_mfma_f32_16x16x32_bf16 a[76:79], a[236:239], a[208:211], a[76:79]\n" \
"ds_read_b64_tr_b16 a[188:189], v39 offset:32768\n" \
"v_mfma_f32_16x16x32_bf16 a[92:95], a[172:175], a[148:151], a[92:95]\n" \
"v_mfma_f32_16x16x32_bf16 a[92:95], a[236:239], a[212:215], a[92:95]\n" \
"v_mfma_f32_16x16x32_bf16 a[88:91], a[168:171], a[148:151], a[88:91]\n" \
"ds_read_b64_tr_b16 a[190:191], v39 offset:33792\n" \
"v_mfma_f32_16x16x32_bf16 a[88:91], a[232:235], a[212:215], a[88:91]\n" \
"v_mfma_f32_16x16x32_bf16 a[84:87], a[164:167], a[148:151], a[84:87]\n" \
"v_mfma_f32_16x16x32_bf16 a[84:87], a[228:231], a[212:215], a[84:87]\n" \
"ds_read_b64_tr_b16 a[252:253], v39 offset:40960\n" \
"v_mfma_f32_16x16x32_bf16 a[80:83], a[160:163], a[148:151], a[80:83]\n" \
"v_mfma_f32_16x16x32_bf16 a[80:83], a[224:227], a[212:215], a[80:83]\n" \
"v_mfma_f32_16x16x32_bf16 a[96:99], a[160:163], a[152:155], a[96:99]\n" \
"ds_read_b64_tr_b16 a[254:255], v39 offset:41984\n" \
"v_mfma_f32_16x16x32_bf16 a[96:99], a[224:227], a[216:219], a[96:99]\n" \
"v_mfma_f32_16x16x32_bf16 a[100:103], a[164:167], a[152:155], a[100:103]\n" \
"v_mfma_f32_16x16x32_bf16 a[100:103], a[228:231], a[216:219], a[100:103]\n" \
"v_mfma_f32_16x16x32_bf16 a[104:107], a[168:171], a[152:155], a[104:107]\n" \
"v_mfma_f32_16x16x32_bf16 a[104:107], a[232:235], a[216:219], a[104:107]\n" \
"v_mfma_f32_16x16x32_bf16 a[108:111], a[172:175], a[152:155], a[108:111]\n" \
"v_mfma_f32_16x16x32_bf16 a[108:111], a[236:239], a[216:219], a[108:111]\n" \
"s_waitcnt lgkmcnt(15)\n" \
"v_mfma_f32_16x16x32_bf16 a[124:127], a[172:175], a[156:159], a[124:127]\n" \
"v_mfma_f32_16x16x32_bf16 a[124:127], a[236:239], a[220:223], a[124:127]\n" \
"v_mfma_f32_16x16x32_bf16 a[120:123], a[168:171], a[156:159], a[120:123]\n" \
"v_mfma_f32_16x16x32_bf16 a[120:123], a[232:235], a[220:223], a[120:123]\n" \
"v_mfma_f32_16x16x32_bf16 a[116:119], a[164:167], a[156:159], a[116:119]\n" \
"v_mfma_f32_16x16x32_bf16 a[116:119], a[228:231], a[220:223], a[116:119]\n" \
"v_mfma_f32_16x16x32_bf16 a[112:115], a[160:163], a[156:159], a[112:115]\n" \
"v_mfma_f32_16x16x32_bf16 a[112:115], a[224:227], a[220:223], a[112:115]\n" \
"s_waitcnt vmcnt(1) lgkmcnt(0)\n" \
"s_barrier\n" \
"v_mov_b32_e32 v45, s62\n" \
"s_mov_b64 exec, 1\n" \
"buffer_store_dword v45, off, s[80:83], s37 offset:128 sc1\n" \
"s_mov_b64 exec, -1\n" \
"v_mfma_f32_16x16x32_bf16 v[52:55], a[176:179], a[128:131], v[52:55]\n" \
"s_add_u32 m0, s30, 0x8000\n" \
"s_movk_i32 s84, 0x380\n" \
"v_mfma_f32_16x16x32_bf16 v[52:55], a[240:243], a[192:195], v[52:55]\n" \
"buffer_load_dwordx4 v22, s[64:67], s84 offen offset:0 lds\n" \
"v_mfma_f32_16x16x32_bf16 v[56:59], a[180:183], a[128:131], v[56:59]\n" \
"ds_read_b64_tr_b16 a[160:161], v32 offset:0\n" \
"v_mfma_f32_16x16x32_bf16 v[56:59], a[244:247], a[192:195], v[56:59]\n" \
"s_movk_i32 s84, 0x1f80\n" \
"v_mfma_f32_16x16x32_bf16 v[60:63], a[184:187], a[128:131], v[60:63]\n" \
"buffer_load_dwordx4 v23, s[64:67], s84 offen offset:1024 lds\n" \
"v_mfma_f32_16x16x32_bf16 v[60:63], a[248:251], a[192:195], v[60:63]\n" \
"ds_read_b64_tr_b16 a[162:163], v32 offset:1024\n" \
"v_mfma_f32_16x16x32_bf16 v[64:67], a[188:191], a[128:131], v[64:67]\n" \
"ds_read_b64_tr_b16 a[224:225], v32 offset:8192\n" \
"v_mfma_f32_16x16x32_bf16 v[64:67], a[252:255], a[192:195], v[64:67]\n" \
"ds_read_b128 a[128:131], v28 offset:16384\n" \
"v_mfma_f32_16x16x32_bf16 v[80:83], a[188:191], a[132:135], v[80:83]\n" \
"ds_read_b128 a[192:195], v29 offset:16384\n" \
"v_mfma_f32_16x16x32_bf16 v[80:83], a[252:255], a[196:199], v[80:83]\n" \
"s_movk_i32 s84, 0x3b80\n" \
"v_mfma_f32_16x16x32_bf16 v[76:79], a[184:187], a[132:135], v[76:79]\n" \
"buffer_load_dwordx4 v22, s[64:67], s84 offen offset:2048 lds\n" \
"v_mfma_f32_16x16x32_bf16 v[76:79], a[248:251], a[196:199], v[76:79]\n" \
"s_movk_i32 s84, 0x5780\n" \
"v_mfma_f32_16x16x32_bf16 v[72:75], a[180:183], a[132:135], v[72:75]\n" \
"buffer_load_dwordx4 v23, s[64:67], s84 offen offset:3072 lds\n" \
"v_mfma_f32_16x16x32_bf16 v[72:75], a[244:247], a[196:199], v[72:75]\n" \
"s_add_u32 m0, s31, 0x8000\n" \
"s_mov_b32 s84, 0x380000\n" \
"v_mfma_f32_16x16x32_bf16 v[68:71], a[176:179], a[132:135], v[68:71]\n" \
"buffer_load_dwordx4 v26, s[68:71], s84 offen offset:0 lds\n" \
"v_mfma_f32_16x16x32_bf16 v[68:71], a[240:243], a[196:199], v[68:71]\n" \
"ds_read_b128 a[132:135], v28 offset:18432\n" \
"v_mfma_f32_16x16x32_bf16 v[84:87], a[176:179], a[136:139], v[84:87]\n" \
"ds_read_b128 a[196:199], v29 offset:18432\n" \
"v_mfma_f32_16x16x32_bf16 v[84:87], a[240:243], a[200:203], v[84:87]\n" \
"s_addk_i32 s84, 0x7c00\n" \
"v_mfma_f32_16x16x32_bf16 v[88:91], a[180:183], a[136:139], v[88:91]\n" \
"buffer_load_dwordx4 v26, s[68:71], s84 offen offset:1024 lds\n" \
"v_mfma_f32_16x16x32_bf16 v[88:91], a[244:247], a[200:203], v[88:91]\n" \
"ds_read_b64_tr_b16 a[226:227], v32 offset:9216\n" \
"v_mfma_f32_16x16x32_bf16 v[92:95], a[184:187], a[136:139], v[92:95]\n" \
"s_addk_i32 s84, 0x7c00\n" \
"v_mfma_f32_16x16x32_bf16 v[92:95], a[248:251], a[200:203], v[92:95]\n" \
"buffer_load_dwordx4 v27, s[68:71], s84 offen offset:2048 lds\n" \
"v_mfma_f32_16x16x32_bf16 v[96:99], a[188:191], a[136:139], v[96:99]\n" \
"ds_read_b64_tr_b16 a[164:165], v33 offset:0\n" \
"v_mfma_f32_16x16x32_bf16 v[96:99], a[252:255], a[200:203], v[96:99]\n" \
"ds_read_b128 a[136:139], v28 offset:20480\n" \
"v_mfma_f32_16x16x32_bf16 v[112:115], a[188:191], a[140:143], v[112:115]\n" \
"ds_read_b128 a[200:203], v29 offset:20480\n" \
"v_mfma_f32_16x16x32_bf16 v[112:115], a[252:255], a[204:207], v[112:115]\n" \
"s_addk_i32 s84, 0x7c00\n" \
"s_nop 0\n" \
"v_mfma_f32_16x16x32_bf16 v[108:111], a[184:187], a[140:143], v[108:111]\n" \
"buffer_load_dwordx4 v27, s[68:71], s84 offen offset:3072 lds\n" \
"v_mfma_f32_16x16x32_bf16 v[108:111], a[248:251], a[204:207], v[108:111]\n" \
"s_add_u32 m0, s30, 0x9000\n" \
"s_mov_b32 s84, 0x8380\n" \
"v_mfma_f32_16x16x32_bf16 v[104:107], a[180:183], a[140:143], v[104:107]\n" \
"buffer_load_dwordx4 v22, s[64:67], s84 offen offset:0 lds\n" \
"v_mfma_f32_16x16x32_bf16 v[104:107], a[244:247], a[204:207], v[104:107]\n" \
"ds_read_b64_tr_b16 a[166:167], v33 offset:1024\n" \
"v_mfma_f32_16x16x32_bf16 v[100:103], a[176:179], a[140:143], v[100:103]\n" \
"ds_read_b64_tr_b16 a[228:229], v33 offset:8192\n" \
"v_mfma_f32_16x16x32_bf16 v[100:103], a[240:243], a[204:207], v[100:103]\n" \
"ds_read_b128 a[140:143], v28 offset:22528\n" \
"v_mfma_f32_16x16x32_bf16 v[116:119], a[176:179], a[144:147], v[116:119]\n" \
"ds_read_b128 a[204:207], v29 offset:22528\n" \
"v_mfma_f32_16x16x32_bf16 v[116:119], a[240:243], a[208:211], v[116:119]\n" \
"s_addk_i32 s84, 0x1c00\n" \
"v_mfma_f32_16x16x32_bf16 v[120:123], a[180:183], a[144:147], v[120:123]\n" \
"buffer_load_dwordx4 v23, s[64:67], s84 offen offset:1024 lds\n" \
"v_mfma_f32_16x16x32_bf16 v[120:123], a[244:247], a[208:211], v[120:123]\n" \
"s_addk_i32 s84, 0x1c00\n" \
"v_mfma_f32_16x16x32_bf16 v[124:127], a[184:187], a[144:147], v[124:127]\n" \
"buffer_load_dwordx4 v22, s[64:67], s84 offen offset:2048 lds\n" \
"v_mfma_f32_16x16x32_bf16 v[124:127], a[248:251], a[208:211], v[124:127]\n" \
"s_addk_i32 s84, 0x1c00\n" \
"s_nop 0\n" \
"v_mfma_f32_16x16x32_bf16 v[128:131], a[188:191], a[144:147], v[128:131]\n" \
"buffer_load_dwordx4 v23, s[64:67], s84 offen offset:3072 lds\n" \
"v_mfma_f32_16x16x32_bf16 v[128:131], a[252:255], a[208:211], v[128:131]\n" \
"ds_read_b128 a[144:147], v28 offset:0\n" \
"v_mfma_f32_16x16x32_bf16 v[144:147], a[188:191], a[148:151], v[144:147]\n" \
"ds_read_b128 a[208:211], v29 offset:0\n" \
"v_mfma_f32_16x16x32_bf16 v[144:147], a[252:255], a[212:215], v[144:147]\n" \
"s_add_u32 m0, s31, 0xc000\n" \
"s_mov_b32 s84, 0x380000\n" \
"v_mfma_f32_16x16x32_bf16 v[140:143], a[184:187], a[148:151], v[140:143]\n" \
"buffer_load_dwordx4 v26, s[72:75], s84 offen offset:0 lds\n" \
"v_mfma_f32_16x16x32_bf16 v[140:143], a[248:251], a[212:215], v[140:143]\n" \
"ds_read_b64_tr_b16 a[230:231], v33 offset:9216\n" \
"v_mfma_f32_16x16x32_bf16 v[136:139], a[180:183], a[148:151], v[136:139]\n" \
"s_addk_i32 s84, 0x7c00\n" \
"s_nop 0\n" \
"v_mfma_f32_16x16x32_bf16 v[136:139], a[244:247], a[212:215], v[136:139]\n" \
"buffer_load_dwordx4 v26, s[72:75], s84 offen offset:1024 lds\n" \
"v_mfma_f32_16x16x32_bf16 v[132:135], a[176:179], a[148:151], v[132:135]\n" \
"ds_read_b64_tr_b16 a[168:169], v34 offset:0\n" \
"v_mfma_f32_16x16x32_bf16 v[132:135], a[240:243], a[212:215], v[132:135]\n" \
"ds_read_b128 a[148:151], v28 offset:2048\n" \
"v_mfma_f32_16x16x32_bf16 v[148:151], a[176:179], a[152:155], v[148:151]\n" \
"ds_read_b128 a[212:215], v29 offset:2048\n" \
"v_mfma_f32_16x16x32_bf16 v[148:151], a[240:243], a[216:219], v[148:151]\n" \
"ds_read_b64_tr_b16 a[170:171], v34 offset:1024\n" \
"v_mfma_f32_16x16x32_bf16 v[152:155], a[180:183], a[152:155], v[152:155]\n" \
"ds_read_b64_tr_b16 a[232:233], v34 offset:8192\n" \
"v_mfma_f32_16x16x32_bf16 v[152:155], a[244:247], a[216:219], v[152:155]\n" \
"ds_read_b64_tr_b16 a[234:235], v34 offset:9216\n" \
"v_mfma_f32_16x16x32_bf16 v[156:159], a[184:187], a[152:155], v[156:159]\n" \
"ds_read_b64_tr_b16 a[172:173], v35 offset:0\n" \
"v_mfma_f32_16x16x32_bf16 v[156:159], a[248:251], a[216:219], v[156:159]\n" \
"ds_read_b64_tr_b16 a[174:175], v35 offset:1024\n" \
"v_mfma_f32_16x16x32_bf16 v[160:163], a[188:191], a[152:155], v[160:163]\n" \
"ds_read_b64_tr_b16 a[236:237], v35 offset:8192\n" \
"v_mfma_f32_16x16x32_bf16 v[160:163], a[252:255], a[216:219], v[160:163]\n" \
"ds_read_b128 a[152:155], v28 offset:4096\n" \
"v_mfma_f32_16x16x32_bf16 v[176:179], a[188:191], a[156:159], v[176:179]\n" \
"ds_read_b128 a[216:219], v29 offset:4096\n" \
"v_mfma_f32_16x16x32_bf16 v[176:179], a[252:255], a[220:223], v[176:179]\n" \
"ds_read_b64_tr_b16 a[238:239], v35 offset:9216\n" \
"v_mfma_f32_16x16x32_bf16 v[172:175], a[184:187], a[156:159], v[172:175]\n" \
"v_mfma_f32_16x16x32_bf16 v[172:175], a[248:251], a[220:223], v[172:175]\n" \
"v_mfma_f32_16x16x32_bf16 v[168:171], a[180:183], a[156:159], v[168:171]\n" \
"v_mfma_f32_16x16x32_bf16 v[168:171], a[244:247], a[220:223], v[168:171]\n" \
"v_mfma_f32_16x16x32_bf16 v[164:167], a[176:179], a[156:159], v[164:167]\n" \
"v_mfma_f32_16x16x32_bf16 v[164:167], a[240:243], a[220:223], v[164:167]\n" \
"ds_read_b128 a[156:159], v28 offset:6144\n" \
"ds_read_b128 a[220:223], v29 offset:6144\n" \
"s_waitcnt lgkmcnt(15)\n" \
"v_mfma_f32_16x16x32_bf16 a[0:3], a[160:163], a[128:131], a[0:3]\n" \
"s_addk_i32 s84, 0x7c00\n" \
"v_mfma_f32_16x16x32_bf16 a[0:3], a[224:227], a[192:195], a[0:3]\n" \
"buffer_load_dwordx4 v27, s[72:75], s84 offen offset:2048 lds\n" \
"v_mfma_f32_16x16x32_bf16 a[4:7], a[164:167], a[128:131], a[4:7]\n" \
"ds_read_b64_tr_b16 a[176:177], v36 offset:0\n" \
"s_waitcnt lgkmcnt(15)\n" \
"v_mfma_f32_16x16x32_bf16 a[4:7], a[228:231], a[192:195], a[4:7]\n" \
"s_addk_i32 s84, 0x7c00\n" \
"s_waitcnt lgkmcnt(11)\n" \
"v_mfma_f32_16x16x32_bf16 a[8:11], a[168:171], a[128:131], a[8:11]\n" \
"buffer_load_dwordx4 v27, s[72:75], s84 offen offset:3072 lds\n" \
"s_waitcnt lgkmcnt(9)\n" \
"v_mfma_f32_16x16x32_bf16 a[8:11], a[232:235], a[192:195], a[8:11]\n" \
"ds_read_b64_tr_b16 a[178:179], v36 offset:1024\n" \
"s_waitcnt lgkmcnt(8)\n" \
"v_mfma_f32_16x16x32_bf16 a[12:15], a[172:175], a[128:131], a[12:15]\n" \
"ds_read_b64_tr_b16 a[240:241], v36 offset:8192\n" \
"s_waitcnt lgkmcnt(5)\n" \
"v_mfma_f32_16x16x32_bf16 a[12:15], a[236:239], a[192:195], a[12:15]\n" \
"v_mfma_f32_16x16x32_bf16 a[28:31], a[172:175], a[132:135], a[28:31]\n" \
"v_mfma_f32_16x16x32_bf16 a[28:31], a[236:239], a[196:199], a[28:31]\n" \
"ds_read_b64_tr_b16 a[242:243], v36 offset:9216\n" \
"v_mfma_f32_16x16x32_bf16 a[24:27], a[168:171], a[132:135], a[24:27]\n" \
"v_mfma_f32_16x16x32_bf16 a[24:27], a[232:235], a[196:199], a[24:27]\n" \
"v_mfma_f32_16x16x32_bf16 a[20:23], a[164:167], a[132:135], a[20:23]\n" \
"v_mfma_f32_16x16x32_bf16 a[20:23], a[228:231], a[196:199], a[20:23]\n" \
"ds_read_b64_tr_b16 a[180:181], v37 offset:0\n" \
"v_mfma_f32_16x16x32_bf16 a[16:19], a[160:163], a[132:135], a[16:19]\n" \
"v_mfma_f32_16x16x32_bf16 a[16:19], a[224:227], a[196:199], a[16:19]\n" \
"v_mfma_f32_16x16x32_bf16 a[32:35], a[160:163], a[136:139], a[32:35]\n" \
"ds_read_b64_tr_b16 a[182:183], v37 offset:1024\n" \
"v_mfma_f32_16x16x32_bf16 a[32:35], a[224:227], a[200:203], a[32:35]\n" \
"v_mfma_f32_16x16x32_bf16 a[36:39], a[164:167], a[136:139], a[36:39]\n" \
"v_mfma_f32_16x16x32_bf16 a[36:39], a[228:231], a[200:203], a[36:39]\n" \
"ds_read_b64_tr_b16 a[244:245], v37 offset:8192\n" \
"v_mfma_f32_16x16x32_bf16 a[40:43], a[168:171], a[136:139], a[40:43]\n" \
"v_mfma_f32_16x16x32_bf16 a[40:43], a[232:235], a[200:203], a[40:43]\n" \
"v_mfma_f32_16x16x32_bf16 a[44:47], a[172:175], a[136:139], a[44:47]\n" \
"ds_read_b64_tr_b16 a[246:247], v37 offset:9216\n" \
"v_mfma_f32_16x16x32_bf16 a[44:47], a[236:239], a[200:203], a[44:47]\n" \
"v_mfma_f32_16x16x32_bf16 a[60:63], a[172:175], a[140:143], a[60:63]\n" \
"v_mfma_f32_16x16x32_bf16 a[60:63], a[236:239], a[204:207], a[60:63]\n" \
"v_mfma_f32_16x16x32_bf16 a[56:59], a[168:171], a[140:143], a[56:59]\n" \
"ds_read_b64_tr_b16 a[184:185], v38 offset:0\n" \
"v_mfma_f32_16x16x32_bf16 a[56:59], a[232:235], a[204:207], a[56:59]\n" \
"v_mfma_f32_16x16x32_bf16 a[52:55], a[164:167], a[140:143], a[52:55]\n" \
"v_mfma_f32_16x16x32_bf16 a[52:55], a[228:231], a[204:207], a[52:55]\n" \
"ds_read_b64_tr_b16 a[186:187], v38 offset:1024\n" \
"v_mfma_f32_16x16x32_bf16 a[48:51], a[160:163], a[140:143], a[48:51]\n" \
"v_mfma_f32_16x16x32_bf16 a[48:51], a[224:227], a[204:207], a[48:51]\n" \
"v_mfma_f32_16x16x32_bf16 a[64:67], a[160:163], a[144:147], a[64:67]\n" \
"v_bfe_u32 v44, v20, 2, 5\n" \
"ds_read_b64_tr_b16 a[248:249], v38 offset:8192\n" \
"v_mfma_f32_16x16x32_bf16 a[64:67], a[224:227], a[208:211], a[64:67]\n" \
"buffer_load_dword v44, v44, s[80:83], s38 offen offset:128 sc1\n" \
"v_mfma_f32_16x16x32_bf16 a[68:71], a[164:167], a[144:147], a[68:71]\n" \
"v_mfma_f32_16x16x32_bf16 a[68:71], a[228:231], a[208:211], a[68:71]\n" \
"ds_read_b64_tr_b16 a[250:251], v38 offset:9216\n" \
"v_mfma_f32_16x16x32_bf16 a[72:75], a[168:171], a[144:147], a[72:75]\n" \
"v_mfma_f32_16x16x32_bf16 a[72:75], a[232:235], a[208:211], a[72:75]\n" \
"v_mfma_f32_16x16x32_bf16 a[76:79], a[172:175], a[144:147], a[76:79]\n" \
"v_mfma_f32_16x16x32_bf16 a[76:79], a[236:239], a[208:211], a[76:79]\n" \
"ds_read_b64_tr_b16 a[188:189], v39 offset:0\n" \
"v_mfma_f32_16x16x32_bf16 a[92:95], a[172:175], a[148:151], a[92:95]\n" \
"v_mfma_f32_16x16x32_bf16 a[92:95], a[236:239], a[212:215], a[92:95]\n" \
"v_mfma_f32_16x16x32_bf16 a[88:91], a[168:171], a[148:151], a[88:91]\n" \
"ds_read_b64_tr_b16 a[190:191], v39 offset:1024\n" \
"v_mfma_f32_16x16x32_bf16 a[88:91], a[232:235], a[212:215], a[88:91]\n" \
"v_mfma_f32_16x16x32_bf16 a[84:87], a[164:167], a[148:151], a[84:87]\n" \
"v_mfma_f32_16x16x32_bf16 a[84:87], a[228:231], a[212:215], a[84:87]\n" \
"ds_read_b64_tr_b16 a[252:253], v39 offset:8192\n" \
"v_mfma_f32_16x16x32_bf16 a[80:83], a[160:163], a[148:151], a[80:83]\n" \
"v_mfma_f32_16x16x32_bf16 a[80:83], a[224:227], a[212:215], a[80:83]\n" \
"v_mfma_f32_16x16x32_bf16 a[96:99], a[160:163], a[152:155], a[96:99]\n" \
"ds_read_b64_tr_b16 a[254:255], v39 offset:9216\n" \
"v_mfma_f32_16x16x32_bf16 a[96:99], a[224:227], a[216:219], a[96:99]\n" \
"v_mfma_f32_16x16x32_bf16 a[100:103], a[164:167], a[152:155], a[100:103]\n" \
"v_mfma_f32_16x16x32_bf16 a[100:103], a[228:231], a[216:219], a[100:103]\n" \
"v_mfma_f32_16x16x32_bf16 a[104:107], a[168:171], a[152:155], a[104:107]\n" \
"v_mfma_f32_16x16x32_bf16 a[104:107], a[232:235], a[216:219], a[104:107]\n" \
"v_mfma_f32_16x16x32_bf16 a[108:111], a[172:175], a[152:155], a[108:111]\n" \
"v_mfma_f32_16x16x32_bf16 a[108:111], a[236:239], a[216:219], a[108:111]\n" \
"s_waitcnt lgkmcnt(15)\n" \
"v_mfma_f32_16x16x32_bf16 a[124:127], a[172:175], a[156:159], a[124:127]\n" \
"v_mfma_f32_16x16x32_bf16 a[124:127], a[236:239], a[220:223], a[124:127]\n" \
"v_mfma_f32_16x16x32_bf16 a[120:123], a[168:171], a[156:159], a[120:123]\n" \
"v_mfma_f32_16x16x32_bf16 a[120:123], a[232:235], a[220:223], a[120:123]\n" \
"v_mfma_f32_16x16x32_bf16 a[116:119], a[164:167], a[156:159], a[116:119]\n" \
"v_mfma_f32_16x16x32_bf16 a[116:119], a[228:231], a[220:223], a[116:119]\n" \
"v_mfma_f32_16x16x32_bf16 a[112:115], a[160:163], a[156:159], a[112:115]\n" \
"v_mfma_f32_16x16x32_bf16 a[112:115], a[224:227], a[220:223], a[112:115]\n" \
"s_waitcnt vmcnt(0) lgkmcnt(0)\n" \
"s_barrier\n" \
"v_cmp_ne_u32_e32 vcc, s62, v44\n" \
"s_nop 4\n" \
"s_cbranch_vccnz L_poll60_%=\n" \
"L_pollr60_%=:\n" \
"s_mov_b32 s68, s12\n" \
"s_and_b32 s69, s13, 0xffff\n" \
"s_mov_b32 s70, 0x480000\n" \
"s_mov_b32 s71, 0x20000\n" \
"v_mfma_f32_16x16x32_bf16 v[52:55], a[176:179], a[128:131], v[52:55]\n" \
"s_add_u32 m0, s40, 0x0\n" \
"s_add_u32 s85, s39, 0x0\n" \
"s_nop 0\n" \
"v_mfma_f32_16x16x32_bf16 v[52:55], a[240:243], a[192:195], v[52:55]\n" \
"buffer_load_dwordx4 v20, s[80:83], s85 offen sc1 lds\n" \
"v_mfma_f32_16x16x32_bf16 v[56:59], a[180:183], a[128:131], v[56:59]\n" \
"ds_read_b64_tr_b16 a[160:161], v32 offset:32768\n" \
"v_mfma_f32_16x16x32_bf16 v[56:59], a[244:247], a[192:195], v[56:59]\n" \
"s_add_u32 m0, s40, 0x1000\n" \
"s_add_u32 s85, s39, 0x1000\n" \
"v_mfma_f32_16x16x32_bf16 v[60:63], a[184:187], a[128:131], v[60:63]\n" \
"buffer_load_dwordx4 v20, s[80:83], s85 offen sc1 lds\n" \
"v_mfma_f32_16x16x32_bf16 v[60:63], a[248:251], a[192:195], v[60:63]\n" \
"ds_read_b64_tr_b16 a[162:163], v32 offset:33792\n" \
"v_mfma_f32_16x16x32_bf16 v[64:67], a[188:191], a[128:131], v[64:67]\n" \
"ds_read_b64_tr_b16 a[224:225], v32 offset:40960\n" \
"v_mfma_f32_16x16x32_bf16 v[64:67], a[252:255], a[192:195], v[64:67]\n" \
"ds_read_b128 a[128:131], v28 offset:49152\n" \
"v_mfma_f32_16x16x32_bf16 v[80:83], a[188:191], a[132:135], v[80:83]\n" \
"ds_read_b128 a[192:195], v29 offset:49152\n" \
"v_mfma_f32_16x16x32_bf16 v[80:83], a[252:255], a[196:199], v[80:83]\n" \
"s_add_u32 m0, s40, 0x3000\n" \
"s_movk_i32 s84, 0x0\n" \
"v_mfma_f32_16x16x32_bf16 v[76:79], a[184:187], a[132:135], v[76:79]\n" \
"buffer_load_dwordx4 v21, s[76:79], s84 offen offset:0 lds\n" \
"v_mfma_f32_16x16x32_bf16 v[76:79], a[248:251], a[196:199], v[76:79]\n" \
"s_add_u32 m0, s30, 0x0\n" \
"s_movk_i32 s84, 0x0\n" \
"s_nop 0\n" \
"v_mfma_f32_16x16x32_bf16 v[72:75], a[180:183], a[132:135], v[72:75]\n" \
"buffer_load_dwordx4 v24, s[76:79], s84 offen offset:0 lds\n" \
"v_mfma_f32_16x16x32_bf16 v[72:75], a[244:247], a[196:199], v[72:75]\n" \
"ds_read_b64_tr_b16 a[226:227], v32 offset:41984\n" \
"v_mfma_f32_16x16x32_bf16 v[68:71], a[176:179], a[132:135], v[68:71]\n" \
"ds_read_b64_tr_b16 a[164:165], v33 offset:32768\n" \
"v_mfma_f32_16x16x32_bf16 v[68:71], a[240:243], a[196:199], v[68:71]\n" \
"ds_read_b128 a[132:135], v28 offset:51200\n" \
"v_mfma_f32_16x16x32_bf16 v[84:87], a[176:179], a[136:139], v[84:87]\n" \
"ds_read_b128 a[196:199], v29 offset:51200\n" \
"v_mfma_f32_16x16x32_bf16 v[84:87], a[240:243], a[200:203], v[84:87]\n" \
"s_movk_i32 s84, 0x2000\n" \
"v_mfma_f32_16x16x32_bf16 v[88:91], a[180:183], a[136:139], v[88:91]\n" \
"buffer_load_dwordx4 v25, s[76:79], s84 offen offset:1024 lds\n" \
"v_mfma_f32_16x16x32_bf16 v[88:91], a[244:247], a[200:203], v[88:91]\n" \
"s_movk_i32 s84, 0x4000\n" \
"v_mfma_f32_16x16x32_bf16 v[92:95], a[184:187], a[136:139], v[92:95]\n" \
"buffer_load_dwordx4 v24, s[76:79], s84 offen offset:2048 lds\n" \
"v_mfma_f32_16x16x32_bf16 v[92:95], a[248:251], a[200:203], v[92:95]\n" \
"ds_read_b64_tr_b16 a[166:167], v33 offset:33792\n" \
"v_mfma_f32_16x16x32_bf16 v[96:99], a[188:191], a[136:139], v[96:99]\n" \
"ds_read_b64_tr_b16 a[228:229], v33 offset:40960\n" \
"v_mfma_f32_16x16x32_bf16 v[96:99], a[252:255], a[200:203], v[96:99]\n" \
"ds_read_b128 a[136:139], v28 offset:53248\n" \
"v_mfma_f32_16x16x32_bf16 v[112:115], a[188:191], a[140:143], v[112:115]\n" \
"ds_read_b128 a[200:203], v29 offset:53248\n" \
"v_mfma_f32_16x16x32_bf16 v[112:115], a[252:255], a[204:207], v[112:115]\n" \
"s_movk_i32 s84, 0x6000\n" \
"v_mfma_f32_16x16x32_bf16 v[108:111], a[184:187], a[140:143], v[108:111]\n" \
"buffer_load_dwordx4 v25, s[76:79], s84 offen offset:3072 lds\n" \
"v_mfma_f32_16x16x32_bf16 v[108:111], a[248:251], a[204:207], v[108:111]\n" \
"s_add_u32 m0, s31, 0x0\n" \
"s_movk_i32 s84, 0x0\n" \
"v_mfma_f32_16x16x32_bf16 v[104:107], a[180:183], a[140:143], v[104:107]\n" \
"buffer_load_dwordx4 v26, s[68:71], s84 offen offset:0 lds\n" \
"v_mfma_f32_16x16x32_bf16 v[104:107], a[244:247], a[204:207], v[104:107]\n" \
"s_movk_i32 s84, 0x7c00\n" \
".fill (L_pb + 16096 - .) / 4, 4, 0xBF800000\n" \
"L_pp4:\n" \
"s_cbranch_execz L_pret\n" \
"v_mfma_f32_16x16x32_bf16 v[100:103], a[176:179], a[140:143], v[100:103]\n" \
"buffer_load_dwordx4 v26, s[68:71], s84 offen offset:1024 lds\n" \
"v_mfma_f32_16x16x32_bf16 v[100:103], a[240:243], a[204:207], v[100:103]\n" \
"ds_read_b128 a[140:143], v28 offset:55296\n" \
"v_mfma_f32_16x16x32_bf16 v[116:119], a[176:179], a[144:147], v[116:119]\n" \
"ds_read_b128 a[204:207], v29 offset:55296\n" \
"v_mfma_f32_16x16x32_bf16 v[116:119], a[240:243], a[208:211], v[116:119]\n" \
"s_addk_i32 s84, 0x7c00\n" \
"v_mfma_f32_16x16x32_bf16 v[120:123], a[180:183], a[144:147], v[120:123]\n" \
"buffer_load_dwordx4 v27, s[68:71], s84 offen offset:2048 lds\n" \
"v_mfma_f32_16x16x32_bf16 v[120:123], a[244:247], a[208:211], v[120:123]\n" \
"ds_read_b64_tr_b16 a[230:231], v33 offset:41984\n" \
"v_mfma_f32_16x16x32_bf16 v[124:127], a[184:187], a[144:147], v[124:127]\n" \
"s_addk_i32 s84, 0x7c00\n" \
"s_nop 0\n" \
"v_mfma_f32_16x16x32_bf16 v[124:127], a[248:251], a[208:211], v[124:127]\n" \
"buffer_load_dwordx4 v27, s[68:71], s84 offen offset:3072 lds\n" \
"v_mfma_f32_16x16x32_bf16 v[128:131], a[188:191], a[144:147], v[128:131]\n" \
"ds_read_b64_tr_b16 a[168:169], v34 offset:32768\n" \
"v_mfma_f32_16x16x32_bf16 v[128:131], a[252:255], a[208:211], v[128:131]\n" \
"ds_read_b128 a[144:147], v28 offset:32768\n" \
"v_mfma_f32_16x16x32_bf16 v[144:147], a[188:191], a[148:151], v[144:147]\n" \
"ds_read_b128 a[208:211], v29 offset:32768\n" \
"v_mfma_f32_16x16x32_bf16 v[144:147], a[252:255], a[212:215], v[144:147]\n" \
"s_add_u32 m0, s30, 0x1000\n" \
"s_mov_b32 s84, 0x9000\n" \
"v_mfma_f32_16x16x32_bf16 v[140:143], a[184:187], a[148:151], v[140:143]\n" \
"buffer_load_dwordx4 v24, s[76:79], s84 offen offset:0 lds\n" \
"v_mfma_f32_16x16x32_bf16 v[140:143], a[248:251], a[212:215], v[140:143]\n" \
"ds_read_b64_tr_b16 a[170:171], v34 offset:33792\n" \
"v_mfma_f32_16x16x32_bf16 v[136:139], a[180:183], a[148:151], v[136:139]\n" \
"s_addk_i32 s84, 0x2000\n" \
"s_nop 0\n" \
"v_mfma_f32_16x16x32_bf16 v[136:139], a[244:247], a[212:215], v[136:139]\n" \
"buffer_load_dwordx4 v25, s[76:79], s84 offen offset:1024 lds\n" \
"v_mfma_f32_16x16x32_bf16 v[132:135], a[176:179], a[148:151], v[132:135]\n" \
"ds_read_b64_tr_b16 a[232:233], v34 offset:40960\n" \
"v_mfma_f32_16x16x32_bf16 v[132:135], a[240:243], a[212:215], v[132:135]\n" \
"ds_read_b128 a[148:151], v28 offset:34816\n" \
"v_mfma_f32_16x16x32_bf16 v[148:151], a[176:179], a[152:155], v[148:151]\n" \
"ds_read_b128 a[212:215], v29 offset:34816\n" \
"v_mfma_f32_16x16x32_bf16 v[148:151], a[240:243], a[216:219], v[148:151]\n" \
"ds_read_b64_tr_b16 a[234:235], v34 offset:41984\n" \
"v_mfma_f32_16x16x32_bf16 v[152:155], a[180:183], a[152:155], v[152:155]\n" \
"ds_read_b64_tr_b16 a[172:173], v35 offset:32768\n" \
"v_mfma_f32_16x16x32_bf16 v[152:155], a[244:247], a[216:219], v[152:155]\n" \
"ds_read_b64_tr_b16 a[174:175], v35 offset:33792\n" \
"v_mfma_f32_16x16x32_bf16 v[156:159], a[184:187], a[152:155], v[156:159]\n" \
"ds_read_b64_tr_b16 a[236:237], v35 offset:40960\n" \
"v_mfma_f32_16x16x32_bf16 v[156:159], a[248:251], a[216:219], v[156:159]\n" \
"ds_read_b64_tr_b16 a[238:239], v35 offset:41984\n" \
"v_mfma_f32_16x16x32_bf16 v[160:163], a[188:191], a[152:155], v[160:163]\n" \
"v_mfma_f32_16x16x32_bf16 v[160:163], a[252:255], a[216:219], v[160:163]\n" \
"ds_read_b128 a[152:155], v28 offset:36864\n" \
"v_mfma_f32_16x16x32_bf16 v[176:179], a[188:191], a[156:159], v[176:179]\n" \
"ds_read_b128 a[216:219], v29 offset:36864\n" \
"v_mfma_f32_16x16x32_bf16 v[176:179], a[252:255], a[220:223], v[176:179]\n" \
"v_mfma_f32_16x16x32_bf16 v[172:175], a[184:187], a[156:159], v[172:175]\n" \
"v_mfma_f32_16x16x32_bf16 v[172:175], a[248:251], a[220:223], v[172:175]\n" \
"v_mfma_f32_16x16x32_bf16 v[168:171], a[180:183], a[156:159], v[168:171]\n" \
"v_mfma_f32_16x16x32_bf16 v[168:171], a[244:247], a[220:223], v[168:171]\n" \
"v_mfma_f32_16x16x32_bf16 v[164:167], a[176:179], a[156:159], v[164:167]\n" \
"v_mfma_f32_16x16x32_bf16 v[164:167], a[240:243], a[220:223], v[164:167]\n" \
"ds_read_b128 a[156:159], v28 offset:38912\n" \
"ds_read_b128 a[220:223], v29 offset:38912\n" \
"s_waitcnt lgkmcnt(15)\n" \
"v_mfma_f32_16x16x32_bf16 a[0:3], a[160:163], a[128:131], a[0:3]\n" \
"s_addk_i32 s84, 0x2000\n" \
"v_mfma_f32_16x16x32_bf16 a[0:3], a[224:227], a[192:195], a[0:3]\n" \
"buffer_load_dwordx4 v24, s[76:79], s84 offen offset:2048 lds\n" \
"v_mfma_f32_16x16x32_bf16 a[4:7], a[164:167], a[128:131], a[4:7]\n" \
"ds_read_b64_tr_b16 a[176:177], v36 offset:32768\n" \
"v_mfma_f32_16x16x32_bf16 a[4:7], a[228:231], a[192:195], a[4:7]\n" \
"s_addk_i32 s84, 0x2000\n" \
"s_waitcnt lgkmcnt(13)\n" \
"v_mfma_f32_16x16x32_bf16 a[8:11], a[168:171], a[128:131], a[8:11]\n" \
"buffer_load_dwordx4 v25, s[76:79], s84 offen offset:3072 lds\n" \
"s_waitcnt lgkmcnt(9)\n" \
"v_mfma_f32_16x16x32_bf16 a[8:11], a[232:235], a[192:195], a[8:11]\n" \
"ds_read_b64_tr_b16 a[178:179], v36 offset:33792\n" \
"s_waitcnt lgkmcnt(8)\n" \
"v_mfma_f32_16x16x32_bf16 a[12:15], a[172:175], a[128:131], a[12:15]\n" \
"ds_read_b64_tr_b16 a[240:241], v36 offset:40960\n" \
"v_accvgpr_read_b32 v184, a0\n" \
"s_waitcnt lgkmcnt(7)\n" \
"s_nop 0\n" \
"v_mfma_f32_16x16x32_bf16 a[12:15], a[236:239], a[192:195], a[12:15]\n" \
"v_accvgpr_read_b32 v185, a1\n" \
"v_accvgpr_read_b32 v186, a2\n" \
"v_mfma_f32_16x16x32_bf16 a[28:31], a[172:175], a[132:135], a[28:31]\n" \
"v_accvgpr_read_b32 v187, a3\n" \
"v_accvgpr_read_b32 v180, a4\n" \
"v_mfma_f32_16x16x32_bf16 a[28:31], a[236:239], a[196:199], a[28:31]\n" \
"ds_read_b64_tr_b16 a[242:243], v36 offset:41984\n" \
"v_accvgpr_read_b32 v181, a5\n" \
"v_mfma_f32_16x16x32_bf16 a[24:27], a[168:171], a[132:135], a[24:27]\n" \
"v_accvgpr_read_b32 v182, a6\n" \
"v_accvgpr_read_b32 v183, a7\n" \
"v_mfma_f32_16x16x32_bf16 a[24:27], a[232:235], a[196:199], a[24:27]\n" \
"v_cvt_pk_bf16_f32 v184, v184, v185\n" \
"v_cvt_pk_bf16_f32 v185, v186, v187\n" \
"v_mfma_f32_16x16x32_bf16 a[20:23], a[164:167], a[132:135], a[20:23]\n" \
"v_cvt_pk_bf16_f32 v186, v180, v181\n" \
"v_cvt_pk_bf16_f32 v187, v182, v183\n" \
"v_mfma_f32_16x16x32_bf16 a[20:23], a[228:231], a[196:199], a[20:23]\n" \
"ds_read_b64_tr_b16 a[180:181], v37 offset:32768\n" \
"v_accvgpr_read_b32 v188, a8\n" \
"v_mfma_f32_16x16x32_bf16 a[16:19], a[160:163], a[132:135], a[16:19]\n" \
"v_accvgpr_read_b32 v189, a9\n" \
"v_accvgpr_read_b32 v190, a10\n" \
"v_mfma_f32_16x16x32_bf16 a[16:19], a[224:227], a[196:199], a[16:19]\n" \
"v_accvgpr_read_b32 v191, a11\n" \
"v_accvgpr_read_b32 v180, a12\n" \
"v_mfma_f32_16x16x32_bf16 a[32:35], a[160:163], a[136:139], a[32:35]\n" \
"ds_read_b64_tr_b16 a[182:183], v37 offset:33792\n" \
"v_accvgpr_read_b32 v181, a13\n" \
"v_mfma_f32_16x16x32_bf16 a[32:35], a[224:227], a[200:203], a[32:35]\n" \
"v_accvgpr_read_b32 v182, a14\n" \
"v_accvgpr_read_b32 v183, a15\n" \
"v_mfma_f32_16x16x32_bf16 a[36:39], a[164:167], a[136:139], a[36:39]\n" \
"v_lshlrev_b32_e32 v45, 3, v20\n" \
"v_add_u32_e32 v45, s31, v45\n" \
"v_mfma_f32_16x16x32_bf16 a[36:39], a[228:231], a[200:203], a[36:39]\n" \
"s_getpc_b64 s[86:87]\n" \
"s_sub_u32 s86, s86, 0xf800\n" \
"s_subb_u32 s87, s87, 0\n" \
"s_mov_b32 m0, s32\n" \
"s_nop 0\n" \
"v_mfma_f32_16x16x32_bf16 a[40:43], a[168:171], a[136:139], a[40:43]\n" \
"global_load_lds_dword v45, s[86:87]\n" \
"v_mfma_f32_16x16x32_bf16 a[40:43], a[232:235], a[200:203], a[40:43]\n" \
"ds_read_b64_tr_b16 a[244:245], v37 offset:40960\n" \
"v_cvt_pk_bf16_f32 v188, v188, v189\n" \
"v_mfma_f32_16x16x32_bf16 a[44:47], a[172:175], a[136:139], a[44:47]\n" \
"ds_read_b64_tr_b16 a[246:247], v37 offset:41984\n" \
"v_cvt_pk_bf16_f32 v189, v190, v191\n" \
"v_mfma_f32_16x16x32_bf16 a[44:47], a[236:239], a[200:203], a[44:47]\n" \
"v_cvt_pk_bf16_f32 v190, v180, v181\n" \
"v_cvt_pk_bf16_f32 v191, v182, v183\n" \
"v_mfma_f32_16x16x32_bf16 a[60:63], a[172:175], a[140:143], a[60:63]\n" \
"v_accvgpr_read_b32 v192, a24\n" \
"v_accvgpr_read_b32 v193, a25\n" \
"v_mfma_f32_16x16x32_bf16 a[60:63], a[236:239], a[204:207], a[60:63]\n" \
"v_accvgpr_read_b32 v194, a26\n" \
"v_accvgpr_read_b32 v195, a27\n" \
"v_mfma_f32_16x16x32_bf16 a[56:59], a[168:171], a[140:143], a[56:59]\n" \
"ds_read_b64_tr_b16 a[184:185], v38 offset:32768\n" \
"v_accvgpr_read_b32 v180, a28\n" \
"v_mfma_f32_16x16x32_bf16 a[56:59], a[232:235], a[204:207], a[56:59]\n" \
"v_accvgpr_read_b32 v181, a29\n" \
"v_accvgpr_read_b32 v182, a30\n" \
"v_mfma_f32_16x16x32_bf16 a[52:55], a[164:167], a[140:143], a[52:55]\n" \
"v_accvgpr_read_b32 v183, a31\n" \
"v_cvt_pk_bf16_f32 v192, v192, v193\n" \
"v_mfma_f32_16x16x32_bf16 a[52:55], a[228:231], a[204:207], a[52:55]\n" \
"ds_read_b64_tr_b16 a[186:187], v38 offset:33792\n" \
"v_cvt_pk_bf16_f32 v193, v194, v195\n" \
"v_mfma_f32_16x16x32_bf16 a[48:51], a[160:163], a[140:143], a[48:51]\n" \
"v_cvt_pk_bf16_f32 v194, v180, v181\n" \
"v_cvt_pk_bf16_f32 v195, v182, v183\n" \
"v_mfma_f32_16x16x32_bf16 a[48:51], a[224:227], a[204:207], a[48:51]\n" \
"v_accvgpr_read_b32 v196, a16\n" \
"v_accvgpr_read_b32 v197, a17\n" \
"v_mfma_f32_16x16x32_bf16 a[64:67], a[160:163], a[144:147], a[64:67]\n" \
"ds_read_b64_tr_b16 a[248:249], v38 offset:40960\n" \
"v_accvgpr_read_b32 v198, a18\n" \
"v_mfma_f32_16x16x32_bf16 a[64:67], a[224:227], a[208:211], a[64:67]\n" \
"v_accvgpr_read_b32 v199, a19\n" \
"v_accvgpr_read_b32 v180, a20\n" \
"v_mfma_f32_16x16x32_bf16 a[68:71], a[164:167], a[144:147], a[68:71]\n" \
"v_accvgpr_read_b32 v181, a21\n" \
"v_accvgpr_read_b32 v182, a22\n" \
"v_mfma_f32_16x16x32_bf16 a[68:71], a[228:231], a[208:211], a[68:71]\n" \
"ds_read_b64_tr_b16 a[250:251], v38 offset:41984\n" \
"v_accvgpr_read_b32 v183, a23\n" \
"v_mfma_f32_16x16x32_bf16 a[72:75], a[168:171], a[144:147], a[72:75]\n" \
"v_cvt_pk_bf16_f32 v196, v196, v197\n" \
"v_cvt_pk_bf16_f32 v197, v198, v199\n" \
"v_mfma_f32_16x16x32_bf16 a[72:75], a[232:235], a[208:211], a[72:75]\n" \
"v_cvt_pk_bf16_f32 v198, v180, v181\n" \
"v_cvt_pk_bf16_f32 v199, v182, v183\n" \
"v_mfma_f32_16x16x32_bf16 a[76:79], a[172:175], a[144:147], a[76:79]\n" \
"v_accvgpr_read_b32 v200, a32\n" \
"v_accvgpr_read_b32 v201, a33\n" \
"v_mfma_f32_16x16x32_bf16 a[76:79], a[236:239], a[208:211], a[76:79]\n" \
"ds_read_b64_tr_b16 a[188:189], v39 offset:32768\n" \
"v_accvgpr_read_b32 v202, a34\n" \
"v_mfma_f32_16x16x32_bf16 a[92:95], a[172:175], a[148:151], a[92:95]\n" \
"s_add_u32 s89, s41, 0x0\n" \
"s_nop 0\n" \
"buffer_store_dwordx4 v[184:187], v20, s[80:83], s89 offen offset:0 sc1\n" \
"v_mfma_f32_16x16x32_bf16 a[92:95], a[236:239], a[212:215], a[92:95]\n" \
"v_accvgpr_read_b32 v203, a35\n" \
"v_accvgpr_read_b32 v180, a36\n" \
"v_mfma_f32_16x16x32_bf16 a[88:91], a[168:171], a[148:151], a[88:91]\n" \
"ds_read_b64_tr_b16 a[190:191], v39 offset:33792\n" \
"v_accvgpr_read_b32 v181, a37\n" \
"v_mfma_f32_16x16x32_bf16 a[88:91], a[232:235], a[212:215], a[88:91]\n" \
"v_accvgpr_read_b32 v182, a38\n" \
"v_accvgpr_read_b32 v183, a39\n" \
"v_mfma_f32_16x16x32_bf16 a[84:87], a[164:167], a[148:151], a[84:87]\n" \
"v_cvt_pk_bf16_f32 v200, v200, v201\n" \
"v_cvt_pk_bf16_f32 v201, v202, v203\n" \
"v_mfma_f32_16x16x32_bf16 a[84:87], a[228:231], a[212:215], a[84:87]\n" \
"ds_read_b64_tr_b16 a[252:253], v39 offset:40960\n" \
"v_cvt_pk_bf16_f32 v202, v180, v181\n" \
"v_mfma_f32_16x16x32_bf16 a[80:83], a[160:163], a[148:151], a[80:83]\n" \
"s_add_u32 s88, s41, 0x0\n" \
"buffer_store_dwordx4 v[188:191], v20, s[80:83], s88 offen offset:1024 sc1\n" \
"v_mfma_f32_16x16x32_bf16 a[80:83], a[224:227], a[212:215], a[80:83]\n" \
"v_cvt_pk_bf16_f32 v203, v182, v183\n" \
"v_accvgpr_read_b32 v204, a40\n" \
"s_waitcnt lgkmcnt(15)\n" \
"v_mfma_f32_16x16x32_bf16 a[96:99], a[160:163], a[152:155], a[96:99]\n" \
"ds_read_b64_tr_b16 a[254:255], v39 offset:41984\n" \
"v_accvgpr_read_b32 v205, a41\n" \
"v_mfma_f32_16x16x32_bf16 a[96:99], a[224:227], a[216:219], a[96:99]\n" \
"v_accvgpr_read_b32 v206, a42\n" \
"v_accvgpr_read_b32 v207, a43\n" \
"v_mfma_f32_16x16x32_bf16 a[100:103], a[164:167], a[152:155], a[100:103]\n" \
"v_accvgpr_read_b32 v180, a44\n" \
"v_accvgpr_read_b32 v181, a45\n" \
"v_mfma_f32_16x16x32_bf16 a[100:103], a[228:231], a[216:219], a[100:103]\n" \
"v_accvgpr_read_b32 v182, a46\n" \
"v_accvgpr_read_b32 v183, a47\n" \
"v_mfma_f32_16x16x32_bf16 a[104:107], a[168:171], a[152:155], a[104:107]\n" \
"s_add_u32 s89, s41, 0x1000\n" \
"buffer_store_dwordx4 v[192:195], v20, s[80:83], s89 offen offset:1024 sc1\n" \
"v_mfma_f32_16x16x32_bf16 a[104:107], a[232:235], a[216:219], a[104:107]\n" \
"v_cvt_pk_bf16_f32 v204, v204, v205\n" \
"v_cvt_pk_bf16_f32 v205, v206, v207\n" \
"v_mfma_f32_16x16x32_bf16 a[108:111], a[172:175], a[152:155], a[108:111]\n" \
"v_cvt_pk_bf16_f32 v206, v180, v181\n" \
"v_cvt_pk_bf16_f32 v207, v182, v183\n" \
"v_mfma_f32_16x16x32_bf16 a[108:111], a[236:239], a[216:219], a[108:111]\n" \
"v_accvgpr_read_b32 v208, a56\n" \
"v_accvgpr_read_b32 v209, a57\n" \
"v_mfma_f32_16x16x32_bf16 a[124:127], a[172:175], a[156:159], a[124:127]\n" \
"v_accvgpr_read_b32 v210, a58\n" \
"v_accvgpr_read_b32 v211, a59\n" \
"v_mfma_f32_16x16x32_bf16 a[124:127], a[236:239], a[220:223], a[124:127]\n" \
"v_accvgpr_read_b32 v180, a60\n" \
"v_accvgpr_read_b32 v181, a61\n" \
"v_mfma_f32_16x16x32_bf16 a[120:123], a[168:171], a[156:159], a[120:123]\n" \
"s_add_u32 s88, s41, 0x1000\n" \
"buffer_store_dwordx4 v[196:199], v20, s[80:83], s88 offen offset:0 sc1\n" \
"v_mfma_f32_16x16x32_bf16 a[120:123], a[232:235], a[220:223], a[120:123]\n" \
"v_accvgpr_read_b32 v182, a62\n" \
"v_accvgpr_read_b32 v183, a63\n" \
"v_mfma_f32_16x16x32_bf16 a[116:119], a[164:167], a[156:159], a[116:119]\n" \
"v_cvt_pk_bf16_f32 v208, v208, v209\n" \
"v_cvt_pk_bf16_f32 v209, v210, v211\n" \
"v_mfma_f32_16x16x32_bf16 a[116:119], a[228:231], a[220:223], a[116:119]\n" \
"v_cvt_pk_bf16_f32 v210, v180, v181\n" \
"v_cvt_pk_bf16_f32 v211, v182, v183\n" \
"v_mfma_f32_16x16x32_bf16 a[112:115], a[160:163], a[156:159], a[112:115]\n" \
"v_accvgpr_read_b32 v212, a48\n" \
"v_accvgpr_read_b32 v213, a49\n" \
"v_mfma_f32_16x16x32_bf16 a[112:115], a[224:227], a[220:223], a[112:115]\n" \
"v_accvgpr_read_b32 v214, a50\n" \
"v_accvgpr_read_b32 v215, a51\n" \
"s_waitcnt vmcnt(5) lgkmcnt(0)\n" \
"s_barrier\n" \
"v_mov_b32_e32 v36, v32\n" \
"v_xor_b32_e32 v32, 0x0, v48\n" \
"v_mov_b32_e32 v37, v33\n" \
"v_xor_b32_e32 v33, 0x20, v48\n" \
"v_mov_b32_e32 v38, v34\n" \
"v_xor_b32_e32 v34, 0x40, v48\n" \
"v_mov_b32_e64 v39, v35\n" \
"v_xor_b32_e32 v35, 0x60, v48\n" \
"v_add_u32_e32 v22, s44, v28\n" \
"v_add_u32_e32 v28, s43, v28\n" \
"v_add_u32_e32 v23, s44, v29\n" \
"v_add_u32_e32 v29, s43, v29\n" \
"v_mfma_f32_16x16x32_bf16 v[52:55], a[176:179], a[128:131], v[52:55]\n" \
"s_add_u32 m0, s40, 0x8000\n" \
"s_add_u32 s85, s39, 0x2000\n" \
"v_mfma_f32_16x16x32_bf16 v[52:55], a[240:243], a[192:195], v[52:55]\n" \
"buffer_load_dwordx4 v20, s[80:83], s85 offen sc1 lds\n" \
"v_mfma_f32_16x16x32_bf16 v[56:59], a[180:183], a[128:131], v[56:59]\n" \
"ds_read_b64_tr_b16 a[160:161], v32 offset:0\n" \
"v_accvgpr_read_b32 v180, a52\n" \
"v_mfma_f32_16x16x32_bf16 v[56:59], a[244:247], a[192:195], v[56:59]\n" \
"s_add_u32 m0, s40, 0x9000\n" \
"s_add_u32 s85, s39, 0x3000\n" \
"v_mfma_f32_16x16x32_bf16 v[60:63], a[184:187], a[128:131], v[60:63]\n" \
"buffer_load_dwordx4 v20, s[80:83], s85 offen sc1 lds\n" \
"v_mfma_f32_16x16x32_bf16 v[60:63], a[248:251], a[192:195], v[60:63]\n" \
"ds_read_b64_tr_b16 a[162:163], v32 offset:512\n" \
"v_accvgpr_read_b32 v181, a53\n" \
"v_mfma_f32_16x16x32_bf16 v[64:67], a[188:191], a[128:131], v[64:67]\n" \
"ds_read_b64_tr_b16 a[224:225], v32 offset:4096\n" \
"v_accvgpr_read_b32 v182, a54\n" \
"v_mfma_f32_16x16x32_bf16 v[64:67], a[252:255], a[192:195], v[64:67]\n" \
"ds_read_b128 a[128:131], v28 offset:0\n" \
"v_accvgpr_read_b32 v183, a55\n" \
"v_mfma_f32_16x16x32_bf16 v[80:83], a[188:191], a[132:135], v[80:83]\n" \
"ds_read_b128 a[192:195], v29 offset:0\n" \
"v_cvt_pk_bf16_f32 v212, v212, v213\n" \
"v_mfma_f32_16x16x32_bf16 v[80:83], a[252:255], a[196:199], v[80:83]\n" \
"s_add_u32 m0, s40, 0xb000\n" \
"s_movk_i32 s84, 0x80\n" \
"v_mfma_f32_16x16x32_bf16 v[76:79], a[184:187], a[132:135], v[76:79]\n" \
"buffer_load_dwordx4 v21, s[76:79], s84 offen offset:0 lds\n" \
"v_mfma_f32_16x16x32_bf16 v[76:79], a[248:251], a[196:199], v[76:79]\n" \
"s_add_u32 m0, s30, 0x8000\n" \
"s_movk_i32 s84, 0x80\n" \
"v_mfma_f32_16x16x32_bf16 v[72:75], a[180:183], a[132:135], v[72:75]\n" \
"buffer_load_dwordx4 v24, s[76:79], s84 offen offset:0 lds\n" \
"v_mfma_f32_16x16x32_bf16 v[72:75], a[244:247], a[196:199], v[72:75]\n" \
"ds_read_b64_tr_b16 a[226:227], v32 offset:4608\n" \
"v_cvt_pk_bf16_f32 v213, v214, v215\n" \
"v_mfma_f32_16x16x32_bf16 v[68:71], a[176:179], a[132:135], v[68:71]\n" \
"ds_read_b64_tr_b16 a[164:165], v33 offset:0\n" \
"v_cvt_pk_bf16_f32 v214, v180, v181\n" \
"v_mfma_f32_16x16x32_bf16 v[68:71], a[240:243], a[196:199], v[68:71]\n" \
"ds_read_b128 a[132:135], v28 offset:2048\n" \
"v_cvt_pk_bf16_f32 v215, v182, v183\n" \
"v_mfma_f32_16x16x32_bf16 v[84:87], a[176:179], a[136:139], v[84:87]\n" \
"ds_read_b128 a[196:199], v29 offset:2048\n" \
"v_accvgpr_read_b32 v216, a64\n" \
"v_mfma_f32_16x16x32_bf16 v[84:87], a[240:243], a[200:203], v[84:87]\n" \
"s_movk_i32 s84, 0x2080\n" \
"v_mfma_f32_16x16x32_bf16 v[88:91], a[180:183], a[136:139], v[88:91]\n" \
"buffer_load_dwordx4 v25, s[76:79], s84 offen offset:1024 lds\n" \
"v_mfma_f32_16x16x32_bf16 v[88:91], a[244:247], a[200:203], v[88:91]\n" \
"s_movk_i32 s84, 0x4080\n" \
"v_mfma_f32_16x16x32_bf16 v[92:95], a[184:187], a[136:139], v[92:95]\n" \
"buffer_load_dwordx4 v24, s[76:79], s84 offen offset:2048 lds\n" \
"v_mfma_f32_16x16x32_bf16 v[92:95], a[248:251], a[200:203], v[92:95]\n" \
"ds_read_b64_tr_b16 a[166:167], v33 offset:512\n" \
"v_accvgpr_read_b32 v217, a65\n" \
"v_mfma_f32_16x16x32_bf16 v[96:99], a[188:191], a[136:139], v[96:99]\n" \
"ds_read_b64_tr_b16 a[228:229], v33 offset:4096\n" \
"v_accvgpr_read_b32 v218, a66\n" \
"v_mfma_f32_16x16x32_bf16 v[96:99], a[252:255], a[200:203], v[96:99]\n" \
"ds_read_b128 a[136:139], v28 offset:4096\n" \
"v_accvgpr_read_b32 v219, a67\n" \
"v_mfma_f32_16x16x32_bf16 v[112:115], a[188:191], a[140:143], v[112:115]\n" \
"ds_read_b128 a[200:203], v29 offset:4096\n" \
"v_accvgpr_read_b32 v180, a68\n" \
"v_mfma_f32_16x16x32_bf16 v[112:115], a[252:255], a[204:207], v[112:115]\n" \
"s_movk_i32 s84, 0x6080\n" \
"v_mfma_f32_16x16x32_bf16 v[108:111], a[184:187], a[140:143], v[108:111]\n" \
"buffer_load_dwordx4 v25, s[76:79], s84 offen offset:3072 lds\n" \
"v_mfma_f32_16x16x32_bf16 v[108:111], a[248:251], a[204:207], v[108:111]\n" \
"s_add_u32 m0, s31, 0x8000\n" \
"s_mov_b32 s84, 0x80000\n" \
"v_mfma_f32_16x16x32_bf16 v[104:107], a[180:183], a[140:143], v[104:107]\n" \
"buffer_load_dwordx4 v26, s[68:71], s84 offen offset:0 lds\n" \
"v_mfma_f32_16x16x32_bf16 v[104:107], a[244:247], a[204:207], v[104:107]\n" \
"s_addk_i32 s84, 0x7c00\n" \
"v_mfma_f32_16x16x32_bf16 v[100:103], a[176:179], a[140:143], v[100:103]\n" \
"buffer_load_dwordx4 v26, s[68:71], s84 offen offset:1024 lds\n" \
"v_mfma_f32_16x16x32_bf16 v[100:103], a[240:243], a[204:207], v[100:103]\n" \
"ds_read_b128 a[140:143], v28 offset:6144\n" \
"v_accvgpr_read_b32 v181, a69\n" \
"v_mfma_f32_16x16x32_bf16 v[116:119], a[176:179], a[144:147], v[116:119]\n" \
"ds_read_b128 a[204:207], v29 offset:6144\n" \
"v_accvgpr_read_b32 v182, a70\n" \
"v_mfma_f32_16x16x32_bf16 v[116:119], a[240:243], a[208:211], v[116:119]\n" \
"s_addk_i32 s84, 0x7c00\n" \
"v_mfma_f32_16x16x32_bf16 v[120:123], a[180:183], a[144:147], v[120:123]\n" \
"buffer_load_dwordx4 v27, s[68:71], s84 offen offset:2048 lds\n" \
"v_mfma_f32_16x16x32_bf16 v[120:123], a[244:247], a[208:211], v[120:123]\n" \
"ds_read_b64_tr_b16 a[230:231], v33 offset:4608\n" \
"v_accvgpr_read_b32 v183, a71\n" \
"v_mfma_f32_16x16x32_bf16 v[124:127], a[184:187], a[144:147], v[124:127]\n" \
"s_addk_i32 s84, 0x7c00\n" \
"v_mfma_f32_16x16x32_bf16 v[124:127], a[248:251], a[208:211], v[124:127]\n" \
"buffer_load_dwordx4 v27, s[68:71], s84 offen offset:3072 lds\n" \
"v_mfma_f32_16x16x32_bf16 v[128:131], a[188:191], a[144:147], v[128:131]\n" \
"ds_read_b64_tr_b16 a[172:173], v35 offset:0\n" \
"v_cvt_pk_bf16_f32 v216, v216, v217\n" \
"v_mfma_f32_16x16x32_bf16 v[128:131], a[252:255], a[208:211], v[128:131]\n" \
"ds_read_b128 a[144:147], v28 offset:8192\n" \
"v_cvt_pk_bf16_f32 v217, v218, v219\n" \
"v_mfma_f32_16x16x32_bf16 v[144:147], a[188:191], a[148:151], v[144:147]\n" \
"ds_read_b128 a[208:211], v29 offset:8192\n" \
"v_cvt_pk_bf16_f32 v218, v180, v181\n" \
"v_mfma_f32_16x16x32_bf16 v[144:147], a[252:255], a[212:215], v[144:147]\n" \
"s_add_u32 m0, s30, 0x9000\n" \
"s_mov_b32 s84, 0x9080\n" \
"v_mfma_f32_16x16x32_bf16 v[140:143], a[184:187], a[148:151], v[140:143]\n" \
"buffer_load_dwordx4 v24, s[76:79], s84 offen offset:0 lds\n" \
"v_mfma_f32_16x16x32_bf16 v[140:143], a[248:251], a[212:215], v[140:143]\n" \
"ds_read_b64_tr_b16 a[174:175], v35 offset:512\n" \
"v_cvt_pk_bf16_f32 v219, v182, v183\n" \
"v_mfma_f32_16x16x32_bf16 v[136:139], a[180:183], a[148:151], v[136:139]\n" \
"s_addk_i32 s84, 0x2000\n" \
"s_nop 0\n" \
"v_mfma_f32_16x16x32_bf16 v[136:139], a[244:247], a[212:215], v[136:139]\n" \
"buffer_load_dwordx4 v25, s[76:79], s84 offen offset:1024 lds\n" \
"v_mfma_f32_16x16x32_bf16 v[132:135], a[176:179], a[148:151], v[132:135]\n" \
"ds_read_b64_tr_b16 a[236:237], v35 offset:4096\n" \
"v_accvgpr_read_b32 v220, a80\n" \
"v_mfma_f32_16x16x32_bf16 v[132:135], a[240:243], a[212:215], v[132:135]\n" \
"ds_read_b128 a[148:151], v28 offset:10240\n" \
"v_accvgpr_read_b32 v221, a81\n" \
"v_mfma_f32_16x16x32_bf16 v[148:151], a[176:179], a[152:155], v[148:151]\n" \
"ds_read_b128 a[212:215], v29 offset:10240\n" \
"v_accvgpr_read_b32 v222, a82\n" \
"v_mfma_f32_16x16x32_bf16 v[148:151], a[240:243], a[216:219], v[148:151]\n" \
"ds_read_b64_tr_b16 a[238:239], v35 offset:4608\n" \
"v_accvgpr_read_b32 v223, a83\n" \
"v_mfma_f32_16x16x32_bf16 v[152:155], a[180:183], a[152:155], v[152:155]\n" \
"ds_read_b64_tr_b16 a[168:169], v34 offset:0\n" \
"v_accvgpr_read_b32 v180, a84\n" \
"v_mfma_f32_16x16x32_bf16 v[152:155], a[244:247], a[216:219], v[152:155]\n" \
"ds_read_b64_tr_b16 a[170:171], v34 offset:512\n" \
"v_accvgpr_read_b32 v181, a85\n" \
"v_mfma_f32_16x16x32_bf16 v[156:159], a[184:187], a[152:155], v[156:159]\n" \
"ds_read_b64_tr_b16 a[232:233], v34 offset:4096\n" \
"v_accvgpr_read_b32 v182, a86\n" \
"v_mfma_f32_16x16x32_bf16 v[156:159], a[248:251], a[216:219], v[156:159]\n" \
"ds_read_b64_tr_b16 a[234:235], v34 offset:4608\n" \
"v_accvgpr_read_b32 v183, a87\n" \
"v_mfma_f32_16x16x32_bf16 v[160:163], a[188:191], a[152:155], v[160:163]\n" \
"ds_read_b128 v[248:251], v22 offset:0\n" \
"v_cvt_pk_bf16_f32 v220, v220, v221\n" \
"v_mfma_f32_16x16x32_bf16 v[160:163], a[252:255], a[216:219], v[160:163]\n" \
"ds_read_b128 a[152:155], v28 offset:12288\n" \
"v_cvt_pk_bf16_f32 v221, v222, v223\n" \
"v_mfma_f32_16x16x32_bf16 v[176:179], a[188:191], a[156:159], v[176:179]\n" \
"ds_read_b128 a[216:219], v29 offset:12288\n" \
"v_cvt_pk_bf16_f32 v222, v180, v181\n" \
"v_mfma_f32_16x16x32_bf16 v[176:179], a[252:255], a[220:223], v[176:179]\n" \
"ds_read_b128 v[48:51], v23 offset:0\n" \
"v_cvt_pk_bf16_f32 v223, v182, v183\n" \
"v_mfma_f32_16x16x32_bf16 v[172:175], a[184:187], a[156:159], v[172:175]\n" \
"v_cvt_pk_bf16_f32 v224, v52, v53\n" \
"v_cvt_pk_bf16_f32 v225, v54, v55\n" \
"v_mfma_f32_16x16x32_bf16 v[172:175], a[248:251], a[220:223], v[172:175]\n" \
"v_cvt_pk_bf16_f32 v226, v56, v57\n" \
"v_cvt_pk_bf16_f32 v227, v58, v59\n" \
"v_mfma_f32_16x16x32_bf16 v[168:171], a[180:183], a[156:159], v[168:171]\n" \
"v_cvt_pk_bf16_f32 v228, v60, v61\n" \
"v_cvt_pk_bf16_f32 v229, v62, v63\n" \
"v_mfma_f32_16x16x32_bf16 v[168:171], a[244:247], a[220:223], v[168:171]\n" \
"v_cvt_pk_bf16_f32 v230, v64, v65\n" \
"v_cvt_pk_bf16_f32 v231, v66, v67\n" \
"v_mfma_f32_16x16x32_bf16 v[164:167], a[176:179], a[156:159], v[164:167]\n" \
"v_cvt_pk_bf16_f32 v232, v76, v77\n" \
"v_cvt_pk_bf16_f32 v233, v78, v79\n" \
"v_mfma_f32_16x16x32_bf16 v[164:167], a[240:243], a[220:223], v[164:167]\n" \
"ds_read_b128 a[156:159], v28 offset:14336\n" \
"ds_read_b128 a[220:223], v29 offset:14336\n" \
"s_waitcnt lgkmcnt(15)\n" \
"v_mfma_f32_16x16x32_bf16 a[0:3], a[160:163], a[128:131], 0\n" \
"s_addk_i32 s84, 0x2000\n" \
"v_cvt_pk_bf16_f32 v234, v80, v81\n" \
"v_mfma_f32_16x16x32_bf16 a[0:3], a[224:227], a[192:195], a[0:3]\n" \
"buffer_load_dwordx4 v24, s[76:79], s84 offen offset:2048 lds\n" \
"v_cvt_pk_bf16_f32 v235, v82, v83\n" \
"v_mfma_f32_16x16x32_bf16 a[4:7], a[164:167], a[128:131], 0\n" \
"ds_read_b64_tr_b16 a[176:177], v36 offset:0\n" \
"v_cvt_pk_bf16_f32 v236, v68, v69\n" \
"v_mfma_f32_16x16x32_bf16 a[4:7], a[228:231], a[192:195], a[4:7]\n" \
"s_addk_i32 s84, 0x2000\n" \
"v_cvt_pk_bf16_f32 v237, v70, v71\n" \
"s_waitcnt lgkmcnt(15)\n" \
"v_mfma_f32_16x16x32_bf16 a[12:15], a[172:175], a[128:131], 0\n" \
"buffer_load_dwordx4 v25, s[76:79], s84 offen offset:3072 lds\n" \
"v_cvt_pk_bf16_f32 v238, v72, v73\n" \
"s_waitcnt lgkmcnt(11)\n" \
"v_mfma_f32_16x16x32_bf16 a[12:15], a[236:239], a[192:195], a[12:15]\n" \
"ds_read_b64_tr_b16 a[178:179], v36 offset:1024\n" \
"v_cvt_pk_bf16_f32 v239, v74, v75\n" \
"s_waitcnt lgkmcnt(10)\n" \
"v_mfma_f32_16x16x32_bf16 a[8:11], a[128:131], a[168:171], 0\n" \
"ds_read_b64_tr_b16 a[240:241], v36 offset:8192\n" \
"v_cvt_pk_bf16_f32 v240, v84, v85\n" \
"s_waitcnt lgkmcnt(9)\n" \
"s_nop 0\n" \
"v_mfma_f32_16x16x32_bf16 a[8:11], a[192:195], a[232:235], a[8:11]\n" \
"ds_read_b64_tr_b16 a[242:243], v36 offset:9216\n" \
"v_cvt_pk_bf16_f32 v241, v86, v87\n" \
"v_mfma_f32_16x16x32_bf16 a[24:27], a[132:135], a[168:171], 0\n" \
"ds_read_b64_tr_b16 a[180:181], v37 offset:0\n" \
"v_cvt_pk_bf16_f32 v242, v88, v89\n" \
"v_mfma_f32_16x16x32_bf16 a[24:27], a[196:199], a[232:235], a[24:27]\n" \
"ds_read_b64_tr_b16 a[182:183], v37 offset:1024\n" \
"v_cvt_pk_bf16_f32 v243, v90, v91\n" \
"v_mfma_f32_16x16x32_bf16 a[28:31], a[172:175], a[132:135], 0\n" \
"ds_read_b64_tr_b16 a[244:245], v37 offset:8192\n" \
"v_cvt_pk_bf16_f32 v244, v92, v93\n" \
"v_mfma_f32_16x16x32_bf16 a[28:31], a[236:239], a[196:199], a[28:31]\n" \
"s_add_u32 s89, s41, 0x2000\n" \
"buffer_store_dwordx4 v[200:203], v20, s[80:83], s89 offen offset:0 sc1\n" \
"v_mfma_f32_16x16x32_bf16 a[20:23], a[164:167], a[132:135], 0\n" \
"ds_read_b64_tr_b16 a[246:247], v37 offset:9216\n" \
"v_cvt_pk_bf16_f32 v245, v94, v95\n" \
"v_mfma_f32_16x16x32_bf16 a[20:23], a[228:231], a[196:199], a[20:23]\n" \
"ds_read_b64_tr_b16 a[184:185], v38 offset:0\n" \
"v_cvt_pk_bf16_f32 v246, v96, v97\n" \
"v_mfma_f32_16x16x32_bf16 a[16:19], a[160:163], a[132:135], 0\n" \
"ds_read_b64_tr_b16 a[186:187], v38 offset:1024\n" \
"v_cvt_pk_bf16_f32 v247, v98, v99\n" \
"v_mfma_f32_16x16x32_bf16 a[16:19], a[224:227], a[196:199], a[16:19]\n" \
"v_cvt_pk_bf16_f32 v184, v108, v109\n" \
"v_cvt_pk_bf16_f32 v185, v110, v111\n" \
"v_cvt_pk_bf16_f32 v186, v112, v113\n" \
"v_mfma_f32_16x16x32_bf16 a[32:35], a[160:163], a[136:139], 0\n" \
"s_add_u32 s88, s41, 0x2000\n" \
"buffer_store_dwordx4 v[204:207], v20, s[80:83], s88 offen offset:1024 sc1\n" \
"v_mfma_f32_16x16x32_bf16 a[32:35], a[224:227], a[200:203], a[32:35]\n" \
"ds_read_b64_tr_b16 a[248:249], v38 offset:8192\n" \
"v_cvt_pk_bf16_f32 v187, v114, v115\n" \
"v_mfma_f32_16x16x32_bf16 a[36:39], a[164:167], a[136:139], 0\n" \
"ds_read_b64_tr_b16 a[250:251], v38 offset:9216\n" \
"v_cvt_pk_bf16_f32 v188, v100, v101\n" \
"v_mfma_f32_16x16x32_bf16 a[36:39], a[228:231], a[200:203], a[36:39]\n" \
"ds_read_b64_tr_b16 a[188:189], v39 offset:0\n" \
"v_cvt_pk_bf16_f32 v189, v102, v103\n" \
"v_mfma_f32_16x16x32_bf16 a[44:47], a[172:175], a[136:139], 0\n" \
"v_cvt_pk_bf16_f32 v190, v104, v105\n" \
"v_cvt_pk_bf16_f32 v191, v106, v107\n" \
"v_cvt_pk_bf16_f32 v192, v116, v117\n" \
"v_mfma_f32_16x16x32_bf16 a[44:47], a[236:239], a[200:203], a[44:47]\n" \
"ds_read_b64_tr_b16 a[190:191], v39 offset:1024\n" \
"v_cvt_pk_bf16_f32 v193, v118, v119\n" \
"v_mfma_f32_16x16x32_bf16 a[40:43], a[136:139], a[168:171], 0\n" \
"s_add_u32 s89, s41, 0x3000\n" \
"buffer_store_dwordx4 v[208:211], v20, s[80:83], s89 offen offset:1024 sc1\n" \
"v_mfma_f32_16x16x32_bf16 a[40:43], a[200:203], a[232:235], a[40:43]\n" \
"ds_read_b64_tr_b16 a[252:253], v39 offset:8192\n" \
"v_cvt_pk_bf16_f32 v194, v120, v121\n" \
"v_mfma_f32_16x16x32_bf16 a[56:59], a[140:143], a[168:171], 0\n" \
"ds_read_b64_tr_b16 a[254:255], v39 offset:9216\n" \
"v_cvt_pk_bf16_f32 v195, v122, v123\n" \
"v_mfma_f32_16x16x32_bf16 a[56:59], a[204:207], a[232:235], a[56:59]\n" \
"v_cvt_pk_bf16_f32 v196, v124, v125\n" \
"v_cvt_pk_bf16_f32 v197, v126, v127\n" \
"v_cvt_pk_bf16_f32 v198, v128, v129\n" \
"v_mfma_f32_16x16x32_bf16 a[60:63], a[172:175], a[140:143], 0\n" \
"v_cvt_pk_bf16_f32 v199, v130, v131\n" \
"v_cvt_pk_bf16_f32 v252, v140, v141\n" \
".fill (L_pb + 20192 - .) / 4, 4, 0xBF800000\n" \
"L_pp5:\n" \
"s_cbranch_execz L_pret\n" \
"s_nop 0\n" \
"v_cvt_pk_bf16_f32 v253, v142, v143\n" \
"v_mfma_f32_16x16x32_bf16 a[60:63], a[236:239], a[204:207], a[60:63]\n" \
"s_add_u32 s88, s41, 0x3000\n" \
"buffer_store_dwordx4 v[212:215], v20, s[80:83], s88 offen offset:0 sc1\n" \
"v_mfma_f32_16x16x32_bf16 a[52:55], a[164:167], a[140:143], 0\n" \
"v_cvt_pk_bf16_f32 v254, v144, v145\n" \
"v_cvt_pk_bf16_f32 v255, v146, v147\n" \
"v_accvgpr_write_b32 a84, v252\n" \
"v_mfma_f32_16x16x32_bf16 a[52:55], a[228:231], a[204:207], a[52:55]\n" \
"v_accvgpr_write_b32 a85, v253\n" \
"v_accvgpr_write_b32 a86, v254\n" \
"v_accvgpr_write_b32 a87, v255\n" \
"v_mfma_f32_16x16x32_bf16 a[48:51], a[160:163], a[140:143], 0\n" \
"v_cvt_pk_bf16_f32 v252, v132, v133\n" \
"v_cvt_pk_bf16_f32 v253, v134, v135\n" \
"v_cvt_pk_bf16_f32 v254, v136, v137\n" \
"v_mfma_f32_16x16x32_bf16 a[48:51], a[224:227], a[204:207], a[48:51]\n" \
"v_cvt_pk_bf16_f32 v255, v138, v139\n" \
"s_waitcnt lgkmcnt(15)\n" \
"v_mfma_f32_16x16x32_bf16 a[64:67], a[164:167], v[248:251], 0\n" \
"v_mfma_f32_16x16x32_bf16 a[64:67], a[228:231], v[48:51], a[64:67]\n" \
"v_mfma_f32_16x16x32_bf16 a[80:83], a[172:175], v[248:251], 0\n" \
"v_mfma_f32_16x16x32_bf16 a[80:83], a[236:239], v[48:51], a[80:83]\n" \
"v_mfma_f32_16x16x32_bf16 a[68:71], v[248:251], a[168:171], 0\n" \
"v_mfma_f32_16x16x32_bf16 a[68:71], v[48:51], a[232:235], a[68:71]\n" \
"s_waitcnt vmcnt(4) lgkmcnt(0)\n" \
"s_barrier\n" \
"v_mfma_f32_16x16x32_bf16 v[52:55], a[176:179], a[128:131], 0\n" \
"s_add_u32 m0, s40, 0x0\n" \
"s_add_u32 s85, s39, 0x4000\n" \
"v_cvt_pk_bf16_f32 v200, v148, v149\n" \
"v_mfma_f32_16x16x32_bf16 v[52:55], a[240:243], a[192:195], v[52:55]\n" \
"buffer_load_dwordx4 v20, s[80:83], s85 offen sc1 lds\n" \
"v_cvt_pk_bf16_f32 v201, v150, v151\n" \
"v_mfma_f32_16x16x32_bf16 v[56:59], a[180:183], a[128:131], 0\n" \
"ds_read_b64_tr_b16 a[160:161], v32 offset:32768\n" \
"v_cvt_pk_bf16_f32 v202, v152, v153\n" \
"v_mfma_f32_16x16x32_bf16 v[56:59], a[244:247], a[192:195], v[56:59]\n" \
"ds_read_b64_tr_b16 a[162:163], v32 offset:33280\n" \
"v_cvt_pk_bf16_f32 v203, v154, v155\n" \
"v_mfma_f32_16x16x32_bf16 v[60:63], a[184:187], a[128:131], 0\n" \
"s_add_u32 m0, s40, 0x1000\n" \
"s_add_u32 s85, s39, 0x5000\n" \
"v_cvt_pk_bf16_f32 v204, v156, v157\n" \
"v_mfma_f32_16x16x32_bf16 v[60:63], a[248:251], a[192:195], v[60:63]\n" \
"buffer_load_dwordx4 v20, s[80:83], s85 offen sc1 lds\n" \
"v_cvt_pk_bf16_f32 v205, v158, v159\n" \
"v_mfma_f32_16x16x32_bf16 v[64:67], a[188:191], a[128:131], 0\n" \
"ds_read_b64_tr_b16 a[224:225], v32 offset:36864\n" \
"v_cvt_pk_bf16_f32 v206, v160, v161\n" \
"v_mfma_f32_16x16x32_bf16 v[64:67], a[252:255], a[192:195], v[64:67]\n" \
"ds_read_b128 a[128:131], v28 offset:32768\n" \
"v_cvt_pk_bf16_f32 v207, v162, v163\n" \
"v_mfma_f32_16x16x32_bf16 v[80:83], a[188:191], a[132:135], 0\n" \
"ds_read_b128 a[192:195], v29 offset:32768\n" \
"v_cvt_pk_bf16_f32 v208, v172, v173\n" \
"v_mfma_f32_16x16x32_bf16 v[80:83], a[252:255], a[196:199], v[80:83]\n" \
"s_add_u32 m0, s40, 0x3000\n" \
"s_movk_i32 s84, 0x100\n" \
"v_cvt_pk_bf16_f32 v209, v174, v175\n" \
"v_mfma_f32_16x16x32_bf16 v[76:79], a[184:187], a[132:135], 0\n" \
"buffer_load_dwordx4 v21, s[76:79], s84 offen offset:0 lds\n" \
"v_cvt_pk_bf16_f32 v210, v176, v177\n" \
"v_mfma_f32_16x16x32_bf16 v[76:79], a[248:251], a[196:199], v[76:79]\n" \
"ds_read_b64_tr_b16 a[226:227], v32 offset:37376\n" \
"v_cvt_pk_bf16_f32 v211, v178, v179\n" \
"v_mfma_f32_16x16x32_bf16 v[72:75], a[180:183], a[132:135], 0\n" \
"s_add_u32 m0, s30, 0x0\n" \
"s_movk_i32 s84, 0x100\n" \
"s_nop 0\n" \
"v_cvt_pk_bf16_f32 v212, v164, v165\n" \
"v_mfma_f32_16x16x32_bf16 v[72:75], a[244:247], a[196:199], v[72:75]\n" \
"buffer_load_dwordx4 v24, s[76:79], s84 offen offset:0 lds\n" \
"v_cvt_pk_bf16_f32 v213, v166, v167\n" \
"v_mfma_f32_16x16x32_bf16 v[68:71], a[176:179], a[132:135], 0\n" \
"ds_read_b64_tr_b16 a[164:165], v33 offset:32768\n" \
"v_cvt_pk_bf16_f32 v214, v168, v169\n" \
"v_mfma_f32_16x16x32_bf16 v[68:71], a[240:243], a[196:199], v[68:71]\n" \
"ds_read_b128 a[132:135], v28 offset:34816\n" \
"v_cvt_pk_bf16_f32 v215, v170, v171\n" \
"v_mfma_f32_16x16x32_bf16 v[84:87], a[176:179], a[136:139], 0\n" \
"ds_read_b128 a[196:199], v29 offset:34816\n" \
"v_mfma_f32_16x16x32_bf16 v[84:87], a[240:243], a[200:203], v[84:87]\n" \
"s_movk_i32 s84, 0x2100\n" \
"v_mfma_f32_16x16x32_bf16 v[88:91], a[180:183], a[136:139], 0\n" \
"buffer_load_dwordx4 v25, s[76:79], s84 offen offset:1024 lds\n" \
"v_mfma_f32_16x16x32_bf16 v[88:91], a[244:247], a[200:203], v[88:91]\n" \
"ds_read_b64_tr_b16 a[166:167], v33 offset:33280\n" \
"v_mfma_f32_16x16x32_bf16 v[92:95], a[184:187], a[136:139], 0\n" \
"ds_read_b64_tr_b16 a[228:229], v33 offset:36864\n" \
"v_mfma_f32_16x16x32_bf16 v[92:95], a[248:251], a[200:203], v[92:95]\n" \
"s_movk_i32 s84, 0x4100\n" \
"v_mfma_f32_16x16x32_bf16 v[96:99], a[188:191], a[136:139], 0\n" \
"buffer_load_dwordx4 v24, s[76:79], s84 offen offset:2048 lds\n" \
"v_mfma_f32_16x16x32_bf16 v[96:99], a[252:255], a[200:203], v[96:99]\n" \
"ds_read_b128 a[136:139], v28 offset:36864\n" \
"v_mfma_f32_16x16x32_bf16 v[112:115], a[188:191], a[140:143], 0\n" \
"ds_read_b128 a[200:203], v29 offset:36864\n" \
"v_mfma_f32_16x16x32_bf16 v[112:115], a[252:255], a[204:207], v[112:115]\n" \
"s_movk_i32 s84, 0x6100\n" \
"s_nop 0\n" \
"v_mfma_f32_16x16x32_bf16 v[108:111], a[184:187], a[140:143], 0\n" \
"buffer_load_dwordx4 v25, s[76:79], s84 offen offset:3072 lds\n" \
"v_mfma_f32_16x16x32_bf16 v[108:111], a[248:251], a[204:207], v[108:111]\n" \
"ds_read_b64_tr_b16 a[230:231], v33 offset:37376\n" \
"v_mfma_f32_16x16x32_bf16 v[104:107], a[180:183], a[140:143], 0\n" \
"ds_read_b64_tr_b16 a[172:173], v35 offset:32768\n" \
"v_mfma_f32_16x16x32_bf16 v[104:107], a[244:247], a[204:207], v[104:107]\n" \
"ds_read_b64_tr_b16 a[174:175], v35 offset:33280\n" \
"v_mfma_f32_16x16x32_bf16 v[100:103], a[176:179], a[140:143], 0\n" \
"ds_read_b64_tr_b16 a[236:237], v35 offset:36864\n" \
"v_mfma_f32_16x16x32_bf16 v[100:103], a[240:243], a[204:207], v[100:103]\n" \
"ds_read_b128 a[140:143], v28 offset:38912\n" \
"v_mfma_f32_16x16x32_bf16 v[116:119], a[176:179], a[144:147], 0\n" \
"ds_read_b128 a[204:207], v29 offset:38912\n" \
"v_mfma_f32_16x16x32_bf16 v[116:119], a[240:243], a[208:211], v[116:119]\n" \
"s_add_u32 m0, s31, 0x0\n" \
"s_mov_b32 s84, 0x100000\n" \
"v_mfma_f32_16x16x32_bf16 v[120:123], a[180:183], a[144:147], 0\n" \
"buffer_load_dwordx4 v26, s[68:71], s84 offen offset:0 lds\n" \
"v_mfma_f32_16x16x32_bf16 v[120:123], a[244:247], a[208:211], v[120:123]\n" \
"s_addk_i32 s84, 0x7c00\n" \
"v_mfma_f32_16x16x32_bf16 v[124:127], a[184:187], a[144:147], 0\n" \
"buffer_load_dwordx4 v26, s[68:71], s84 offen offset:1024 lds\n" \
"v_mfma_f32_16x16x32_bf16 v[124:127], a[248:251], a[208:211], v[124:127]\n" \
"ds_read_b64_tr_b16 a[238:239], v35 offset:37376\n" \
"v_mfma_f32_16x16x32_bf16 v[128:131], a[188:191], a[144:147], 0\n" \
"ds_read_b64_tr_b16 a[168:169], v34 offset:32768\n" \
"v_mfma_f32_16x16x32_bf16 v[128:131], a[252:255], a[208:211], v[128:131]\n" \
"ds_read_b128 a[144:147], v28 offset:40960\n" \
"v_mfma_f32_16x16x32_bf16 v[144:147], a[188:191], a[148:151], 0\n" \
"ds_read_b128 a[208:211], v29 offset:40960\n" \
"v_mfma_f32_16x16x32_bf16 v[144:147], a[252:255], a[212:215], v[144:147]\n" \
"s_addk_i32 s84, 0x7c00\n" \
"v_mfma_f32_16x16x32_bf16 v[140:143], a[184:187], a[148:151], 0\n" \
"buffer_load_dwordx4 v27, s[68:71], s84 offen offset:2048 lds\n" \
"v_mfma_f32_16x16x32_bf16 v[140:143], a[248:251], a[212:215], v[140:143]\n" \
"s_addk_i32 s84, 0x7c00\n" \
"v_mfma_f32_16x16x32_bf16 v[136:139], a[180:183], a[148:151], 0\n" \
"buffer_load_dwordx4 v27, s[68:71], s84 offen offset:3072 lds\n" \
"v_mfma_f32_16x16x32_bf16 v[136:139], a[244:247], a[212:215], v[136:139]\n" \
"ds_read_b64_tr_b16 a[170:171], v34 offset:33280\n" \
"v_mfma_f32_16x16x32_bf16 v[132:135], a[176:179], a[148:151], 0\n" \
"ds_read_b64_tr_b16 a[232:233], v34 offset:36864\n" \
"v_mfma_f32_16x16x32_bf16 v[132:135], a[240:243], a[212:215], v[132:135]\n" \
"ds_read_b128 a[148:151], v28 offset:43008\n" \
"v_mfma_f32_16x16x32_bf16 v[148:151], a[176:179], a[152:155], 0\n" \
"ds_read_b128 a[212:215], v29 offset:43008\n" \
"v_mfma_f32_16x16x32_bf16 v[148:151], a[240:243], a[216:219], v[148:151]\n" \
"s_add_u32 m0, s30, 0x1000\n" \
"s_mov_b32 s84, 0x9100\n" \
"v_mfma_f32_16x16x32_bf16 v[152:155], a[180:183], a[152:155], 0\n" \
"buffer_load_dwordx4 v24, s[76:79], s84 offen offset:0 lds\n" \
"v_mfma_f32_16x16x32_bf16 v[152:155], a[244:247], a[216:219], v[152:155]\n" \
"s_addk_i32 s84, 0x2000\n" \
"s_nop 0\n" \
"v_mfma_f32_16x16x32_bf16 v[156:159], a[184:187], a[152:155], 0\n" \
"buffer_load_dwordx4 v25, s[76:79], s84 offen offset:1024 lds\n" \
"v_mfma_f32_16x16x32_bf16 v[156:159], a[248:251], a[216:219], v[156:159]\n" \
"ds_read_b64_tr_b16 a[234:235], v34 offset:37376\n" \
"v_mfma_f32_16x16x32_bf16 v[160:163], a[188:191], a[152:155], 0\n" \
"ds_read_b128 v[248:251], v22 offset:32768\n" \
"v_mfma_f32_16x16x32_bf16 v[160:163], a[252:255], a[216:219], v[160:163]\n" \
"ds_read_b128 a[152:155], v28 offset:45056\n" \
"v_mfma_f32_16x16x32_bf16 v[176:179], a[188:191], a[156:159], 0\n" \
"ds_read_b128 a[216:219], v29 offset:45056\n" \
"v_mfma_f32_16x16x32_bf16 v[176:179], a[252:255], a[220:223], v[176:179]\n" \
"ds_read_b128 v[48:51], v23 offset:32768\n" \
"v_mfma_f32_16x16x32_bf16 v[172:175], a[184:187], a[156:159], 0\n" \
"v_mfma_f32_16x16x32_bf16 v[172:175], a[248:251], a[220:223], v[172:175]\n" \
"v_mfma_f32_16x16x32_bf16 v[168:171], a[180:183], a[156:159], 0\n" \
"v_mfma_f32_16x16x32_bf16 v[168:171], a[244:247], a[220:223], v[168:171]\n" \
"v_mfma_f32_16x16x32_bf16 v[164:167], a[176:179], a[156:159], 0\n" \
"v_mfma_f32_16x16x32_bf16 v[164:167], a[240:243], a[220:223], v[164:167]\n" \
"ds_read_b128 a[156:159], v28 offset:47104\n" \
"ds_read_b128 a[220:223], v29 offset:47104\n" \
"s_waitcnt lgkmcnt(15)\n" \
"v_mfma_f32_16x16x32_bf16 a[0:3], a[160:163], a[128:131], a[0:3]\n" \
"s_addk_i32 s84, 0x2000\n" \
"v_mfma_f32_16x16x32_bf16 a[0:3], a[224:227], a[192:195], a[0:3]\n" \
"buffer_load_dwordx4 v24, s[76:79], s84 offen offset:2048 lds\n" \
"v_mfma_f32_16x16x32_bf16 a[4:7], a[164:167], a[128:131], a[4:7]\n" \
"ds_read_b64_tr_b16 a[176:177], v36 offset:32768\n" \
"v_mov_b32_e64 v11, 0\n" \
"v_mfma_f32_16x16x32_bf16 a[4:7], a[228:231], a[192:195], a[4:7]\n" \
"s_addk_i32 s84, 0x2000\n" \
"v_mfma_f32_16x16x32_bf16 a[12:15], a[172:175], a[128:131], a[12:15]\n" \
"buffer_load_dwordx4 v25, s[76:79], s84 offen offset:3072 lds\n" \
"s_waitcnt lgkmcnt(15)\n" \
"v_mfma_f32_16x16x32_bf16 a[12:15], a[236:239], a[192:195], a[12:15]\n" \
"ds_read_b64_tr_b16 a[178:179], v36 offset:33792\n" \
"s_and_b32 s6, s2, 7\n" \
"s_waitcnt lgkmcnt(12)\n" \
"v_mfma_f32_16x16x32_bf16 a[8:11], a[128:131], a[168:171], a[8:11]\n" \
"ds_read_b64_tr_b16 a[240:241], v36 offset:40960\n" \
"s_lshl_b32 s7, s3, 1\n" \
"s_waitcnt lgkmcnt(9)\n" \
"v_mfma_f32_16x16x32_bf16 a[8:11], a[192:195], a[232:235], a[8:11]\n" \
"ds_read_b64_tr_b16 a[242:243], v36 offset:41984\n" \
"s_lshr_b32 s4, s2, 5\n" \
"v_mfma_f32_16x16x32_bf16 a[24:27], a[132:135], a[168:171], a[24:27]\n" \
"ds_read_b64_tr_b16 a[180:181], v37 offset:32768\n" \
"s_lshl_b32 s5, s6, 2\n" \
"v_mfma_f32_16x16x32_bf16 a[24:27], a[196:199], a[232:235], a[24:27]\n" \
"ds_read_b64_tr_b16 a[182:183], v37 offset:33792\n" \
"s_bfe_u32 s8, s2, 0x20003\n" \
"v_mfma_f32_16x16x32_bf16 a[28:31], a[172:175], a[132:135], a[28:31]\n" \
"ds_read_b64_tr_b16 a[244:245], v37 offset:40960\n" \
"s_and_b32 s7, s7, 4\n" \
"v_mfma_f32_16x16x32_bf16 a[28:31], a[236:239], a[196:199], a[28:31]\n" \
"s_add_u32 s89, s41, 0x0\n" \
"buffer_store_dwordx4 v[224:227], v20, s[80:83], s89 offen offset:2048 sc1\n" \
"v_mfma_f32_16x16x32_bf16 a[20:23], a[164:167], a[132:135], a[20:23]\n" \
"ds_read_b64_tr_b16 a[246:247], v37 offset:41984\n" \
"s_and_b32 s9, s3, 1\n" \
"v_mfma_f32_16x16x32_bf16 a[20:23], a[228:231], a[196:199], a[20:23]\n" \
"ds_read_b64_tr_b16 a[184:185], v38 offset:32768\n" \
"s_or_b32 s13, s5, s8\n" \
"v_mfma_f32_16x16x32_bf16 a[16:19], a[160:163], a[132:135], a[16:19]\n" \
"ds_read_b64_tr_b16 a[186:187], v38 offset:33792\n" \
"s_or_b32 s9, s7, s9\n" \
"v_mfma_f32_16x16x32_bf16 a[16:19], a[224:227], a[196:199], a[16:19]\n" \
"s_mul_i32 s7, s4, 0xa00\n" \
"s_add_i32 s7, s7, 0x5bc1000\n" \
"v_mfma_f32_16x16x32_bf16 a[32:35], a[160:163], a[136:139], a[32:35]\n" \
"s_add_u32 s88, s41, 0x0\n" \
"buffer_store_dwordx4 v[228:231], v20, s[80:83], s88 offen offset:3072 sc1\n" \
"v_mfma_f32_16x16x32_bf16 a[32:35], a[224:227], a[200:203], a[32:35]\n" \
"ds_read_b64_tr_b16 a[248:249], v38 offset:40960\n" \
"s_lshl_b32 s10, s13, 2\n" \
"v_mfma_f32_16x16x32_bf16 a[36:39], a[164:167], a[136:139], a[36:39]\n" \
"ds_read_b64_tr_b16 a[250:251], v38 offset:41984\n" \
"s_or_b32 s10, s10, s7\n" \
"v_mfma_f32_16x16x32_bf16 a[36:39], a[228:231], a[200:203], a[36:39]\n" \
"ds_read_b64_tr_b16 a[188:189], v39 offset:32768\n" \
"v_writelane_b32 v11, s10, 0\n" \
"v_mfma_f32_16x16x32_bf16 a[44:47], a[172:175], a[136:139], a[44:47]\n" \
"s_lshl_b32 s14, s4, 3\n" \
"v_writelane_b32 v11, s7, 1\n" \
"v_mfma_f32_16x16x32_bf16 a[44:47], a[236:239], a[200:203], a[44:47]\n" \
"ds_read_b64_tr_b16 a[190:191], v39 offset:33792\n" \
"s_or_b32 s7, s14, s6\n" \
"v_mfma_f32_16x16x32_bf16 a[40:43], a[136:139], a[168:171], a[40:43]\n" \
"s_add_u32 s89, s41, 0x1000\n" \
"buffer_store_dwordx4 v[232:235], v20, s[80:83], s89 offen offset:3072 sc1\n" \
"v_mfma_f32_16x16x32_bf16 a[40:43], a[200:203], a[232:235], a[40:43]\n" \
"ds_read_b64_tr_b16 a[252:253], v39 offset:40960\n" \
"s_lshl_b32 s7, s7, 10\n" \
"v_mfma_f32_16x16x32_bf16 a[56:59], a[140:143], a[168:171], a[56:59]\n" \
"ds_read_b64_tr_b16 a[254:255], v39 offset:41984\n" \
"s_lshl_b32 s10, s3, 8\n" \
"v_mfma_f32_16x16x32_bf16 a[56:59], a[204:207], a[232:235], a[56:59]\n" \
"s_or_b32 s7, s7, s10\n" \
"s_or_b32 s10, s7, 0x5c00200\n" \
"v_mfma_f32_16x16x32_bf16 a[60:63], a[172:175], a[140:143], a[60:63]\n" \
"v_writelane_b32 v11, s10, 2\n" \
"s_lshl_b32 s10, s8, 2\n" \
"v_mfma_f32_16x16x32_bf16 a[60:63], a[236:239], a[204:207], a[60:63]\n" \
"s_add_u32 s88, s41, 0x1000\n" \
"buffer_store_dwordx4 v[236:239], v20, s[80:83], s88 offen offset:2048 sc1\n" \
"v_mfma_f32_16x16x32_bf16 a[52:55], a[164:167], a[140:143], a[52:55]\n" \
"s_or_b32 s11, s10, s7\n" \
"s_or_b32 s11, s11, 0x5c00000\n" \
"v_mfma_f32_16x16x32_bf16 a[52:55], a[228:231], a[204:207], a[52:55]\n" \
"v_writelane_b32 v11, s11, 3\n" \
"s_or_b32 s7, s7, 0x5c00080\n" \
"v_mfma_f32_16x16x32_bf16 a[48:51], a[160:163], a[140:143], a[48:51]\n" \
"v_writelane_b32 v11, s7, 4\n" \
"s_or_b32 s7, s7, s10\n" \
"v_mfma_f32_16x16x32_bf16 a[48:51], a[224:227], a[204:207], a[48:51]\n" \
"s_add_i32 s10, s3, s2\n" \
"v_writelane_b32 v11, s7, 5\n" \
"s_waitcnt lgkmcnt(15)\n" \
"v_mfma_f32_16x16x32_bf16 a[64:67], a[164:167], v[248:251], a[64:67]\n" \
"s_mov_b32 s7, 2.0\n" \
"s_mov_b32 s11, 0x5bc0000\n" \
"v_mfma_f32_16x16x32_bf16 a[64:67], a[228:231], v[48:51], a[64:67]\n" \
"s_cmp_lg_u32 s10, 0\n" \
"s_cselect_b32 s10, s7, s11\n" \
"v_mfma_f32_16x16x32_bf16 a[80:83], a[172:175], v[248:251], a[80:83]\n" \
"s_lshr_b32 s15, s2, 3\n" \
"v_writelane_b32 v11, s10, 54\n" \
"v_mfma_f32_16x16x32_bf16 a[80:83], a[236:239], v[48:51], a[80:83]\n" \
"s_lshl_b32 s10, s6, 5\n" \
"s_min_u32 s16, s3, 2\n" \
"v_mfma_f32_16x16x32_bf16 a[68:71], v[248:251], a[168:171], a[68:71]\n" \
"s_mul_i32 s12, s8, 0x120\n" \
"v_mfma_f32_16x16x32_bf16 a[68:71], v[48:51], a[232:235], a[68:71]\n" \
"s_waitcnt vmcnt(4) lgkmcnt(0)\n" \
"s_barrier\n" \
"v_mfma_f32_16x16x32_bf16 v[52:55], a[176:179], a[128:131], v[52:55]\n" \
"s_add_u32 m0, s40, 0x8000\n" \
"s_add_u32 s85, s39, 0x6000\n" \
"v_mfma_f32_16x16x32_bf16 v[52:55], a[240:243], a[192:195], v[52:55]\n" \
"buffer_load_dwordx4 v20, s[80:83], s85 offen sc1 lds\n" \
"v_mfma_f32_16x16x32_bf16 v[56:59], a[180:183], a[128:131], v[56:59]\n" \
"ds_read_b64_tr_b16 a[160:161], v32 offset:0\n" \
"v_mfma_f32_16x16x32_bf16 v[56:59], a[244:247], a[192:195], v[56:59]\n" \
"ds_read_b64_tr_b16 a[162:163], v32 offset:512\n" \
"v_mfma_f32_16x16x32_bf16 v[60:63], a[184:187], a[128:131], v[60:63]\n" \
"s_add_u32 m0, s40, 0x9000\n" \
"s_add_u32 s85, s39, 0x7000\n" \
"v_mfma_f32_16x16x32_bf16 v[60:63], a[248:251], a[192:195], v[60:63]\n" \
"buffer_load_dwordx4 v20, s[80:83], s85 offen sc1 lds\n" \
"v_mfma_f32_16x16x32_bf16 v[64:67], a[188:191], a[128:131], v[64:67]\n" \
"ds_read_b64_tr_b16 a[224:225], v32 offset:4096\n" \
"v_mfma_f32_16x16x32_bf16 v[64:67], a[252:255], a[192:195], v[64:67]\n" \
"ds_read_b128 a[128:131], v28 offset:0\n" \
"v_mfma_f32_16x16x32_bf16 v[80:83], a[188:191], a[132:135], v[80:83]\n" \
"ds_read_b128 a[192:195], v29 offset:0\n" \
"v_mfma_f32_16x16x32_bf16 v[80:83], a[252:255], a[196:199], v[80:83]\n" \
"s_add_u32 m0, s40, 0xb000\n" \
"s_movk_i32 s84, 0x180\n" \
"v_mfma_f32_16x16x32_bf16 v[76:79], a[184:187], a[132:135], v[76:79]\n" \
"buffer_load_dwordx4 v21, s[76:79], s84 offen offset:0 lds\n" \
"v_mfma_f32_16x16x32_bf16 v[76:79], a[248:251], a[196:199], v[76:79]\n" \
"ds_read_b64_tr_b16 a[226:227], v32 offset:4608\n" \
"v_mfma_f32_16x16x32_bf16 v[72:75], a[180:183], a[132:135], v[72:75]\n" \
"s_add_u32 m0, s30, 0x8000\n" \
"s_movk_i32 s84, 0x180\n" \
"v_mfma_f32_16x16x32_bf16 v[72:75], a[244:247], a[196:199], v[72:75]\n" \
"buffer_load_dwordx4 v24, s[76:79], s84 offen offset:0 lds\n" \
"v_mfma_f32_16x16x32_bf16 v[68:71], a[176:179], a[132:135], v[68:71]\n" \
"ds_read_b64_tr_b16 a[164:165], v33 offset:0\n" \
"v_mfma_f32_16x16x32_bf16 v[68:71], a[240:243], a[196:199], v[68:71]\n" \
"ds_read_b128 a[132:135], v28 offset:2048\n" \
"v_mfma_f32_16x16x32_bf16 v[84:87], a[176:179], a[136:139], v[84:87]\n" \
"ds_read_b128 a[196:199], v29 offset:2048\n" \
"v_mfma_f32_16x16x32_bf16 v[84:87], a[240:243], a[200:203], v[84:87]\n" \
"s_movk_i32 s84, 0x2180\n" \
"v_mfma_f32_16x16x32_bf16 v[88:91], a[180:183], a[136:139], v[88:91]\n" \
"buffer_load_dwordx4 v25, s[76:79], s84 offen offset:1024 lds\n" \
"v_mfma_f32_16x16x32_bf16 v[88:91], a[244:247], a[200:203], v[88:91]\n" \
"ds_read_b64_tr_b16 a[166:167], v33 offset:512\n" \
"v_mfma_f32_16x16x32_bf16 v[92:95], a[184:187], a[136:139], v[92:95]\n" \
"ds_read_b64_tr_b16 a[228:229], v33 offset:4096\n" \
"v_mfma_f32_16x16x32_bf16 v[92:95], a[248:251], a[200:203], v[92:95]\n" \
"s_movk_i32 s84, 0x4180\n" \
"v_mfma_f32_16x16x32_bf16 v[96:99], a[188:191], a[136:139], v[96:99]\n" \
"buffer_load_dwordx4 v24, s[76:79], s84 offen offset:2048 lds\n" \
"v_mfma_f32_16x16x32_bf16 v[96:99], a[252:255], a[200:203], v[96:99]\n" \
"ds_read_b128 a[136:139], v28 offset:4096\n" \
"v_mfma_f32_16x16x32_bf16 v[112:115], a[188:191], a[140:143], v[112:115]\n" \
"ds_read_b128 a[200:203], v29 offset:4096\n" \
"v_mfma_f32_16x16x32_bf16 v[112:115], a[252:255], a[204:207], v[112:115]\n" \
"s_movk_i32 s84, 0x6180\n" \
"s_nop 0\n" \
"v_mfma_f32_16x16x32_bf16 v[108:111], a[184:187], a[140:143], v[108:111]\n" \
"buffer_load_dwordx4 v25, s[76:79], s84 offen offset:3072 lds\n" \
"v_mfma_f32_16x16x32_bf16 v[108:111], a[248:251], a[204:207], v[108:111]\n" \
"ds_read_b64_tr_b16 a[230:231], v33 offset:4608\n" \
"v_mfma_f32_16x16x32_bf16 v[104:107], a[180:183], a[140:143], v[104:107]\n" \
"ds_read_b64_tr_b16 a[172:173], v35 offset:0\n" \
"v_mfma_f32_16x16x32_bf16 v[104:107], a[244:247], a[204:207], v[104:107]\n" \
"ds_read_b64_tr_b16 a[174:175], v35 offset:512\n" \
"v_mfma_f32_16x16x32_bf16 v[100:103], a[176:179], a[140:143], v[100:103]\n" \
"ds_read_b64_tr_b16 a[236:237], v35 offset:4096\n" \
"v_mfma_f32_16x16x32_bf16 v[100:103], a[240:243], a[204:207], v[100:103]\n" \
"ds_read_b128 a[140:143], v28 offset:6144\n" \
"v_mfma_f32_16x16x32_bf16 v[116:119], a[176:179], a[144:147], v[116:119]\n" \
"ds_read_b128 a[204:207], v29 offset:6144\n" \
"v_mfma_f32_16x16x32_bf16 v[116:119], a[240:243], a[208:211], v[116:119]\n" \
"s_add_u32 m0, s31, 0x8000\n" \
"s_mov_b32 s84, 0x180000\n" \
"v_mfma_f32_16x16x32_bf16 v[120:123], a[180:183], a[144:147], v[120:123]\n" \
"buffer_load_dwordx4 v26, s[68:71], s84 offen offset:0 lds\n" \
"v_mfma_f32_16x16x32_bf16 v[120:123], a[244:247], a[208:211], v[120:123]\n" \
"s_addk_i32 s84, 0x7c00\n" \
"s_nop 0\n" \
"v_mfma_f32_16x16x32_bf16 v[124:127], a[184:187], a[144:147], v[124:127]\n" \
"buffer_load_dwordx4 v26, s[68:71], s84 offen offset:1024 lds\n" \
"v_mfma_f32_16x16x32_bf16 v[124:127], a[248:251], a[208:211], v[124:127]\n" \
"ds_read_b64_tr_b16 a[238:239], v35 offset:4608\n" \
"v_mfma_f32_16x16x32_bf16 v[128:131], a[188:191], a[144:147], v[128:131]\n" \
"ds_read_b64_tr_b16 a[168:169], v34 offset:0\n" \
"v_mfma_f32_16x16x32_bf16 v[128:131], a[252:255], a[208:211], v[128:131]\n" \
"ds_read_b128 a[144:147], v28 offset:8192\n" \
"v_mfma_f32_16x16x32_bf16 v[144:147], a[188:191], a[148:151], v[144:147]\n" \
"ds_read_b128 a[208:211], v29 offset:8192\n" \
"v_mfma_f32_16x16x32_bf16 v[144:147], a[252:255], a[212:215], v[144:147]\n" \
"s_addk_i32 s84, 0x7c00\n" \
"v_mfma_f32_16x16x32_bf16 v[140:143], a[184:187], a[148:151], v[140:143]\n" \
"buffer_load_dwordx4 v27, s[68:71], s84 offen offset:2048 lds\n" \
"v_mfma_f32_16x16x32_bf16 v[140:143], a[248:251], a[212:215], v[140:143]\n" \
"s_addk_i32 s84, 0x7c00\n" \
"v_mfma_f32_16x16x32_bf16 v[136:139], a[180:183], a[148:151], v[136:139]\n" \
"buffer_load_dwordx4 v27, s[68:71], s84 offen offset:3072 lds\n" \
"v_mfma_f32_16x16x32_bf16 v[136:139], a[244:247], a[212:215], v[136:139]\n" \
"ds_read_b64_tr_b16 a[170:171], v34 offset:512\n" \
"v_mfma_f32_16x16x32_bf16 v[132:135], a[176:179], a[148:151], v[132:135]\n" \
"ds_read_b64_tr_b16 a[232:233], v34 offset:4096\n" \
"v_mfma_f32_16x16x32_bf16 v[132:135], a[240:243], a[212:215], v[132:135]\n" \
"ds_read_b128 a[148:151], v28 offset:10240\n" \
"v_mfma_f32_16x16x32_bf16 v[148:151], a[176:179], a[152:155], v[148:151]\n" \
"ds_read_b128 a[212:215], v29 offset:10240\n" \
"v_mfma_f32_16x16x32_bf16 v[148:151], a[240:243], a[216:219], v[148:151]\n" \
"s_add_u32 m0, s30, 0x9000\n" \
"s_mov_b32 s84, 0x9180\n" \
"v_mfma_f32_16x16x32_bf16 v[152:155], a[180:183], a[152:155], v[152:155]\n" \
"buffer_load_dwordx4 v24, s[76:79], s84 offen offset:0 lds\n" \
"v_mfma_f32_16x16x32_bf16 v[152:155], a[244:247], a[216:219], v[152:155]\n" \
"s_addk_i32 s84, 0x2000\n" \
"s_nop 0\n" \
"v_mfma_f32_16x16x32_bf16 v[156:159], a[184:187], a[152:155], v[156:159]\n" \
"buffer_load_dwordx4 v25, s[76:79], s84 offen offset:1024 lds\n" \
"v_mfma_f32_16x16x32_bf16 v[156:159], a[248:251], a[216:219], v[156:159]\n" \
"ds_read_b64_tr_b16 a[234:235], v34 offset:4608\n" \
"v_mfma_f32_16x16x32_bf16 v[160:163], a[188:191], a[152:155], v[160:163]\n" \
"ds_read_b128 v[248:251], v22 offset:0\n" \
"v_mfma_f32_16x16x32_bf16 v[160:163], a[252:255], a[216:219], v[160:163]\n" \
"ds_read_b128 a[152:155], v28 offset:12288\n" \
"v_mfma_f32_16x16x32_bf16 v[176:179], a[188:191], a[156:159], v[176:179]\n" \
"ds_read_b128 a[216:219], v29 offset:12288\n" \
"v_mfma_f32_16x16x32_bf16 v[176:179], a[252:255], a[220:223], v[176:179]\n" \
"ds_read_b128 v[48:51], v23 offset:0\n" \
"v_mfma_f32_16x16x32_bf16 v[172:175], a[184:187], a[156:159], v[172:175]\n" \
"s_add_u32 s11, s22, s12\n" \
"s_addc_u32 s17, s23, 0\n" \
"v_mfma_f32_16x16x32_bf16 v[172:175], a[248:251], a[220:223], v[172:175]\n" \
"s_add_u32 s18, s24, s12\n" \
"v_writelane_b32 v11, s11, 9\n" \
"s_addc_u32 s19, s25, 0\n" \
"v_mfma_f32_16x16x32_bf16 v[168:171], a[180:183], a[156:159], v[168:171]\n" \
"s_and_b32 s20, s17, 0xffff\n" \
"v_writelane_b32 v11, s20, 10\n" \
"v_mfma_f32_16x16x32_bf16 v[168:171], a[244:247], a[220:223], v[168:171]\n" \
"s_and_b32 s20, s19, 0xffff\n" \
"v_writelane_b32 v11, s18, 11\n" \
"v_mfma_f32_16x16x32_bf16 v[164:167], a[176:179], a[156:159], v[164:167]\n" \
"s_cmpk_lt_u32 s3, 3\n" \
"s_cselect_b32 s11, s11, s18\n" \
"v_mfma_f32_16x16x32_bf16 v[164:167], a[240:243], a[220:223], v[164:167]\n" \
"ds_read_b128 a[156:159], v28 offset:14336\n" \
"ds_read_b128 a[220:223], v29 offset:14336\n" \
"s_waitcnt lgkmcnt(15)\n" \
"v_mfma_f32_16x16x32_bf16 a[0:3], a[160:163], a[128:131], a[0:3]\n" \
"s_addk_i32 s84, 0x2000\n" \
"v_mfma_f32_16x16x32_bf16 a[0:3], a[224:227], a[192:195], a[0:3]\n" \
"buffer_load_dwordx4 v24, s[76:79], s84 offen offset:2048 lds\n" \
"v_mfma_f32_16x16x32_bf16 a[4:7], a[164:167], a[128:131], a[4:7]\n" \
"ds_read_b64_tr_b16 a[176:177], v36 offset:0\n" \
"s_movk_i32 s18, 0xfff\n" \
"v_mfma_f32_16x16x32_bf16 a[4:7], a[228:231], a[192:195], a[4:7]\n" \
"s_addk_i32 s84, 0x2000\n" \
"v_mfma_f32_16x16x32_bf16 a[12:15], a[172:175], a[128:131], a[12:15]\n" \
"buffer_load_dwordx4 v25, s[76:79], s84 offen offset:3072 lds\n" \
"s_waitcnt lgkmcnt(15)\n" \
"v_mfma_f32_16x16x32_bf16 a[12:15], a[236:239], a[192:195], a[12:15]\n" \
"ds_read_b64_tr_b16 a[178:179], v36 offset:1024\n" \
"v_writelane_b32 v11, s20, 12\n" \
"s_waitcnt lgkmcnt(12)\n" \
"v_mfma_f32_16x16x32_bf16 a[8:11], a[128:131], a[168:171], a[8:11]\n" \
"ds_read_b64_tr_b16 a[240:241], v36 offset:8192\n" \
"s_and_b32 s20, s29, 0xffff\n" \
"s_waitcnt lgkmcnt(9)\n" \
"s_nop 0\n" \
"v_mfma_f32_16x16x32_bf16 a[8:11], a[192:195], a[232:235], a[8:11]\n" \
"ds_read_b64_tr_b16 a[242:243], v36 offset:9216\n" \
"v_writelane_b32 v11, s28, 13\n" \
"v_mfma_f32_16x16x32_bf16 a[24:27], a[132:135], a[168:171], a[24:27]\n" \
"ds_read_b64_tr_b16 a[180:181], v37 offset:0\n" \
"v_writelane_b32 v11, s20, 14\n" \
"v_mfma_f32_16x16x32_bf16 a[24:27], a[196:199], a[232:235], a[24:27]\n" \
"ds_read_b64_tr_b16 a[182:183], v37 offset:1024\n" \
"v_writelane_b32 v11, s11, 15\n" \
"v_mfma_f32_16x16x32_bf16 a[28:31], a[172:175], a[132:135], a[28:31]\n" \
"ds_read_b64_tr_b16 a[244:245], v37 offset:8192\n" \
"v_mfma_f32_16x16x32_bf16 a[28:31], a[236:239], a[196:199], a[28:31]\n" \
"s_add_u32 s89, s41, 0x2000\n" \
"buffer_store_dwordx4 v[240:243], v20, s[80:83], s89 offen offset:2048 sc1\n" \
"v_mfma_f32_16x16x32_bf16 a[20:23], a[164:167], a[132:135], a[20:23]\n" \
"ds_read_b64_tr_b16 a[246:247], v37 offset:9216\n" \
"v_mfma_f32_16x16x32_bf16 a[20:23], a[228:231], a[196:199], a[20:23]\n" \
"ds_read_b64_tr_b16 a[184:185], v38 offset:0\n" \
"v_mfma_f32_16x16x32_bf16 a[16:19], a[160:163], a[132:135], a[16:19]\n" \
"ds_read_b64_tr_b16 a[186:187], v38 offset:1024\n" \
"v_mfma_f32_16x16x32_bf16 a[16:19], a[224:227], a[196:199], a[16:19]\n" \
"s_cmpk_lt_u32 s3, 3\n" \
"s_cselect_b32 s11, s17, s19\n" \
"v_mfma_f32_16x16x32_bf16 a[32:35], a[160:163], a[136:139], a[32:35]\n" \
"s_add_u32 s88, s41, 0x2000\n" \
"buffer_store_dwordx4 v[244:247], v20, s[80:83], s88 offen offset:3072 sc1\n" \
"v_mfma_f32_16x16x32_bf16 a[32:35], a[224:227], a[200:203], a[32:35]\n" \
"ds_read_b64_tr_b16 a[248:249], v38 offset:8192\n" \
"s_and_b32 s11, s11, 0xffff\n" \
"v_mfma_f32_16x16x32_bf16 a[36:39], a[164:167], a[136:139], a[36:39]\n" \
"ds_read_b64_tr_b16 a[250:251], v38 offset:9216\n" \
"v_writelane_b32 v11, s11, 16\n" \
"v_mfma_f32_16x16x32_bf16 a[36:39], a[228:231], a[200:203], a[36:39]\n" \
"ds_read_b64_tr_b16 a[188:189], v39 offset:0\n" \
"s_mul_i32 s11, s6, 0x48\n" \
"v_mfma_f32_16x16x32_bf16 a[44:47], a[172:175], a[136:139], a[44:47]\n" \
"s_add_i32 s17, s11, 0x47\n" \
"v_mfma_f32_16x16x32_bf16 a[44:47], a[236:239], a[200:203], a[44:47]\n" \
"ds_read_b64_tr_b16 a[190:191], v39 offset:1024\n" \
"v_mfma_f32_16x16x32_bf16 a[40:43], a[136:139], a[168:171], a[40:43]\n" \
"s_add_u32 s89, s41, 0x3000\n" \
"buffer_store_dwordx4 v[184:187], v20, s[80:83], s89 offen offset:3072 sc1\n" \
"v_mfma_f32_16x16x32_bf16 a[40:43], a[200:203], a[232:235], a[40:43]\n" \
"ds_read_b64_tr_b16 a[252:253], v39 offset:8192\n" \
"v_mfma_f32_16x16x32_bf16 a[56:59], a[140:143], a[168:171], a[56:59]\n" \
"ds_read_b64_tr_b16 a[254:255], v39 offset:9216\n" \
"v_mfma_f32_16x16x32_bf16 a[56:59], a[204:207], a[232:235], a[56:59]\n" \
"s_cmpk_lt_u32 s3, 3\n" \
"s_cselect_b32 s17, s17, s18\n" \
"v_mfma_f32_16x16x32_bf16 a[60:63], a[172:175], a[140:143], a[60:63]\n" \
"s_mulk_i32 s17, 0x480\n" \
"s_addk_i32 s17, 0x120\n" \
"v_mfma_f32_16x16x32_bf16 a[60:63], a[236:239], a[204:207], a[60:63]\n" \
"s_add_u32 s88, s41, 0x3000\n" \
"buffer_store_dwordx4 v[188:191], v20, s[80:83], s88 offen offset:2048 sc1\n" \
"v_mfma_f32_16x16x32_bf16 a[52:55], a[164:167], a[140:143], a[52:55]\n" \
"v_writelane_b32 v11, s17, 17\n" \
"s_lshl_b32 s17, s3, 5\n" \
"v_mfma_f32_16x16x32_bf16 a[52:55], a[228:231], a[204:207], a[52:55]\n" \
"s_add_i32 s11, s11, s17\n" \
"s_mul_i32 s17, s11, 0x480\n" \
"v_mfma_f32_16x16x32_bf16 a[48:51], a[160:163], a[140:143], a[48:51]\n" \
"s_mul_i32 s11, s13, 0x24000\n" \
"s_add_i32 s18, s11, 0x1b000\n" \
"v_mfma_f32_16x16x32_bf16 a[48:51], a[224:227], a[204:207], a[48:51]\n" \
"s_cmpk_lt_u32 s3, 3\n" \
"s_cselect_b32 s17, s17, s18\n" \
"s_waitcnt lgkmcnt(15)\n" \
"v_mfma_f32_16x16x32_bf16 a[64:67], a[164:167], v[248:251], a[64:67]\n" \
"s_mov_b32 s18, 0x8000\n" \
"v_writelane_b32 v11, s17, 18\n" \
"v_mfma_f32_16x16x32_bf16 a[64:67], a[228:231], v[48:51], a[64:67]\n" \
"s_mul_i32 s17, s16, 0x2400\n" \
"s_mul_i32 s16, s16, 0x9000\n" \
"v_mfma_f32_16x16x32_bf16 a[80:83], a[172:175], v[248:251], a[80:83]\n" \
"s_bitset1_b32 s17, 17\n" \
"v_writelane_b32 v11, s17, 19\n" \
"v_mfma_f32_16x16x32_bf16 a[80:83], a[236:239], v[48:51], a[80:83]\n" \
"s_add_i32 s16, s11, s16\n" \
"v_writelane_b32 v11, s16, 26\n" \
"v_mfma_f32_16x16x32_bf16 a[68:71], v[248:251], a[168:171], a[68:71]\n" \
"s_mul_i32 s16, s3, 0x2400\n" \
"s_add_i32 s17, s16, 0xa400\n" \
"v_mfma_f32_16x16x32_bf16 a[68:71], v[48:51], a[232:235], a[68:71]\n" \
"s_waitcnt vmcnt(4) lgkmcnt(0)\n" \
"s_barrier\n" \
"v_mfma_f32_16x16x32_bf16 v[52:55], a[176:179], a[128:131], v[52:55]\n" \
"s_add_u32 m0, s40, 0x0\n" \
"s_add_u32 s85, s39, 0x8000\n" \
"v_mfma_f32_16x16x32_bf16 v[52:55], a[240:243], a[192:195], v[52:55]\n" \
"buffer_load_dwordx4 v20, s[80:83], s85 offen sc1 lds\n" \
"v_mfma_f32_16x16x32_bf16 v[56:59], a[180:183], a[128:131], v[56:59]\n" \
"ds_read_b64_tr_b16 a[160:161], v32 offset:32768\n" \
"v_mfma_f32_16x16x32_bf16 v[56:59], a[244:247], a[192:195], v[56:59]\n" \
"ds_read_b64_tr_b16 a[162:163], v32 offset:33280\n" \
"v_mfma_f32_16x16x32_bf16 v[60:63], a[184:187], a[128:131], v[60:63]\n" \
"s_add_u32 m0, s40, 0x1000\n" \
"s_add_u32 s85, s39, 0x9000\n" \
"v_mfma_f32_16x16x32_bf16 v[60:63], a[248:251], a[192:195], v[60:63]\n" \
"buffer_load_dwordx4 v20, s[80:83], s85 offen sc1 lds\n" \
"v_mfma_f32_16x16x32_bf16 v[64:67], a[188:191], a[128:131], v[64:67]\n" \
"ds_read_b64_tr_b16 a[224:225], v32 offset:36864\n" \
"v_mfma_f32_16x16x32_bf16 v[64:67], a[252:255], a[192:195], v[64:67]\n" \
".fill (L_pb + 24288 - .) / 4, 4, 0xBF800000\n" \
"L_pp6:\n" \
"s_cbranch_execz L_pret\n" \
"ds_read_b128 a[128:131], v28 offset:32768\n" \
"v_mfma_f32_16x16x32_bf16 v[80:83], a[188:191], a[132:135], v[80:83]\n" \
"ds_read_b128 a[192:195], v29 offset:32768\n" \
"v_mfma_f32_16x16x32_bf16 v[80:83], a[252:255], a[196:199], v[80:83]\n" \
"s_add_u32 m0, s40, 0x3000\n" \
"s_movk_i32 s84, 0x200\n" \
"v_mfma_f32_16x16x32_bf16 v[76:79], a[184:187], a[132:135], v[76:79]\n" \
"buffer_load_dwordx4 v21, s[76:79], s84 offen offset:0 lds\n" \
"v_mfma_f32_16x16x32_bf16 v[76:79], a[248:251], a[196:199], v[76:79]\n" \
"ds_read_b64_tr_b16 a[226:227], v32 offset:37376\n" \
"v_mfma_f32_16x16x32_bf16 v[72:75], a[180:183], a[132:135], v[72:75]\n" \
"s_add_u32 m0, s30, 0x0\n" \
"s_movk_i32 s84, 0x200\n" \
"v_mfma_f32_16x16x32_bf16 v[72:75], a[244:247], a[196:199], v[72:75]\n" \
"buffer_load_dwordx4 v24, s[76:79], s84 offen offset:0 lds\n" \
"v_mfma_f32_16x16x32_bf16 v[68:71], a[176:179], a[132:135], v[68:71]\n" \
"ds_read_b64_tr_b16 a[164:165], v33 offset:32768\n" \
"v_mfma_f32_16x16x32_bf16 v[68:71], a[240:243], a[196:199], v[68:71]\n" \
"ds_read_b128 a[132:135], v28 offset:34816\n" \
"v_mfma_f32_16x16x32_bf16 v[84:87], a[176:179], a[136:139], v[84:87]\n" \
"ds_read_b128 a[196:199], v29 offset:34816\n" \
"v_mfma_f32_16x16x32_bf16 v[84:87], a[240:243], a[200:203], v[84:87]\n" \
"s_movk_i32 s84, 0x2200\n" \
"v_mfma_f32_16x16x32_bf16 v[88:91], a[180:183], a[136:139], v[88:91]\n" \
"buffer_load_dwordx4 v25, s[76:79], s84 offen offset:1024 lds\n" \
"v_mfma_f32_16x16x32_bf16 v[88:91], a[244:247], a[200:203], v[88:91]\n" \
"ds_read_b64_tr_b16 a[166:167], v33 offset:33280\n" \
"v_mfma_f32_16x16x32_bf16 v[92:95], a[184:187], a[136:139], v[92:95]\n" \
"ds_read_b64_tr_b16 a[228:229], v33 offset:36864\n" \
"v_mfma_f32_16x16x32_bf16 v[92:95], a[248:251], a[200:203], v[92:95]\n" \
"s_movk_i32 s84, 0x4200\n" \
"v_mfma_f32_16x16x32_bf16 v[96:99], a[188:191], a[136:139], v[96:99]\n" \
"buffer_load_dwordx4 v24, s[76:79], s84 offen offset:2048 lds\n" \
"v_mfma_f32_16x16x32_bf16 v[96:99], a[252:255], a[200:203], v[96:99]\n" \
"ds_read_b128 a[136:139], v28 offset:36864\n" \
"v_mfma_f32_16x16x32_bf16 v[112:115], a[188:191], a[140:143], v[112:115]\n" \
"ds_read_b128 a[200:203], v29 offset:36864\n" \
"v_mfma_f32_16x16x32_bf16 v[112:115], a[252:255], a[204:207], v[112:115]\n" \
"s_movk_i32 s84, 0x6200\n" \
"s_nop 0\n" \
"v_mfma_f32_16x16x32_bf16 v[108:111], a[184:187], a[140:143], v[108:111]\n" \
"buffer_load_dwordx4 v25, s[76:79], s84 offen offset:3072 lds\n" \
"v_mfma_f32_16x16x32_bf16 v[108:111], a[248:251], a[204:207], v[108:111]\n" \
"ds_read_b64_tr_b16 a[230:231], v33 offset:37376\n" \
"v_mfma_f32_16x16x32_bf16 v[104:107], a[180:183], a[140:143], v[104:107]\n" \
"ds_read_b64_tr_b16 a[172:173], v35 offset:32768\n" \
"v_mfma_f32_16x16x32_bf16 v[104:107], a[244:247], a[204:207], v[104:107]\n" \
"ds_read_b64_tr_b16 a[174:175], v35 offset:33280\n" \
"v_mfma_f32_16x16x32_bf16 v[100:103], a[176:179], a[140:143], v[100:103]\n" \
"ds_read_b64_tr_b16 a[236:237], v35 offset:36864\n" \
"v_mfma_f32_16x16x32_bf16 v[100:103], a[240:243], a[204:207], v[100:103]\n" \
"ds_read_b128 a[140:143], v28 offset:38912\n" \
"v_mfma_f32_16x16x32_bf16 v[116:119], a[176:179], a[144:147], v[116:119]\n" \
"ds_read_b128 a[204:207], v29 offset:38912\n" \
"v_mfma_f32_16x16x32_bf16 v[116:119], a[240:243], a[208:211], v[116:119]\n" \
"s_add_u32 m0, s31, 0x0\n" \
"s_mov_b32 s84, 0x200000\n" \
"v_mfma_f32_16x16x32_bf16 v[120:123], a[180:183], a[144:147], v[120:123]\n" \
"buffer_load_dwordx4 v26, s[68:71], s84 offen offset:0 lds\n" \
"v_mfma_f32_16x16x32_bf16 v[120:123], a[244:247], a[208:211], v[120:123]\n" \
"s_addk_i32 s84, 0x7c00\n" \
"v_mfma_f32_16x16x32_bf16 v[124:127], a[184:187], a[144:147], v[124:127]\n" \
"buffer_load_dwordx4 v26, s[68:71], s84 offen offset:1024 lds\n" \
"v_mfma_f32_16x16x32_bf16 v[124:127], a[248:251], a[208:211], v[124:127]\n" \
"ds_read_b64_tr_b16 a[238:239], v35 offset:37376\n" \
"v_mfma_f32_16x16x32_bf16 v[128:131], a[188:191], a[144:147], v[128:131]\n" \
"ds_read_b64_tr_b16 a[168:169], v34 offset:32768\n" \
"v_mfma_f32_16x16x32_bf16 v[128:131], a[252:255], a[208:211], v[128:131]\n" \
"ds_read_b128 a[144:147], v28 offset:40960\n" \
"v_mfma_f32_16x16x32_bf16 v[144:147], a[188:191], a[148:151], v[144:147]\n" \
"ds_read_b128 a[208:211], v29 offset:40960\n" \
"v_mfma_f32_16x16x32_bf16 v[144:147], a[252:255], a[212:215], v[144:147]\n" \
"s_addk_i32 s84, 0x7c00\n" \
"v_mfma_f32_16x16x32_bf16 v[140:143], a[184:187], a[148:151], v[140:143]\n" \
"buffer_load_dwordx4 v27, s[68:71], s84 offen offset:2048 lds\n" \
"v_mfma_f32_16x16x32_bf16 v[140:143], a[248:251], a[212:215], v[140:143]\n" \
"s_addk_i32 s84, 0x7c00\n" \
"v_mfma_f32_16x16x32_bf16 v[136:139], a[180:183], a[148:151], v[136:139]\n" \
"buffer_load_dwordx4 v27, s[68:71], s84 offen offset:3072 lds\n" \
"v_mfma_f32_16x16x32_bf16 v[136:139], a[244:247], a[212:215], v[136:139]\n" \
"ds_read_b64_tr_b16 a[170:171], v34 offset:33280\n" \
"v_mfma_f32_16x16x32_bf16 v[132:135], a[176:179], a[148:151], v[132:135]\n" \
"ds_read_b64_tr_b16 a[232:233], v34 offset:36864\n" \
"v_mfma_f32_16x16x32_bf16 v[132:135], a[240:243], a[212:215], v[132:135]\n" \
"ds_read_b128 a[148:151], v28 offset:43008\n" \
"v_mfma_f32_16x16x32_bf16 v[148:151], a[176:179], a[152:155], v[148:151]\n" \
"ds_read_b128 a[212:215], v29 offset:43008\n" \
"v_mfma_f32_16x16x32_bf16 v[148:151], a[240:243], a[216:219], v[148:151]\n" \
"s_add_u32 m0, s30, 0x1000\n" \
"s_mov_b32 s84, 0x9200\n" \
"v_mfma_f32_16x16x32_bf16 v[152:155], a[180:183], a[152:155], v[152:155]\n" \
"buffer_load_dwordx4 v24, s[76:79], s84 offen offset:0 lds\n" \
"v_mfma_f32_16x16x32_bf16 v[152:155], a[244:247], a[216:219], v[152:155]\n" \
"s_addk_i32 s84, 0x2000\n" \
"s_nop 0\n" \
"v_mfma_f32_16x16x32_bf16 v[156:159], a[184:187], a[152:155], v[156:159]\n" \
"buffer_load_dwordx4 v25, s[76:79], s84 offen offset:1024 lds\n" \
"v_mfma_f32_16x16x32_bf16 v[156:159], a[248:251], a[216:219], v[156:159]\n" \
"ds_read_b64_tr_b16 a[234:235], v34 offset:37376\n" \
"v_mfma_f32_16x16x32_bf16 v[160:163], a[188:191], a[152:155], v[160:163]\n" \
"ds_read_b128 v[248:251], v22 offset:32768\n" \
"v_mfma_f32_16x16x32_bf16 v[160:163], a[252:255], a[216:219], v[160:163]\n" \
"ds_read_b128 a[152:155], v28 offset:45056\n" \
"v_mfma_f32_16x16x32_bf16 v[176:179], a[188:191], a[156:159], v[176:179]\n" \
"ds_read_b128 a[216:219], v29 offset:45056\n" \
"v_mfma_f32_16x16x32_bf16 v[176:179], a[252:255], a[220:223], v[176:179]\n" \
"ds_read_b128 v[48:51], v23 offset:32768\n" \
"v_mfma_f32_16x16x32_bf16 v[172:175], a[184:187], a[156:159], v[172:175]\n" \
"s_cmpk_lt_u32 s3, 3\n" \
"s_cselect_b32 s17, s17, s18\n" \
"v_mfma_f32_16x16x32_bf16 v[172:175], a[248:251], a[220:223], v[172:175]\n" \
"s_mov_b32 s18, 0x1b000\n" \
"s_xor_b32 s19, s3, 2\n" \
"v_mfma_f32_16x16x32_bf16 v[168:171], a[180:183], a[156:159], v[168:171]\n" \
"s_cmp_lg_u32 s19, 0\n" \
"s_cselect_b32 s18, s17, s18\n" \
"v_mfma_f32_16x16x32_bf16 v[168:171], a[244:247], a[220:223], v[168:171]\n" \
"v_writelane_b32 v11, s17, 20\n" \
"s_or_b32 s17, s16, 0x10000\n" \
"v_mfma_f32_16x16x32_bf16 v[164:167], a[176:179], a[156:159], v[164:167]\n" \
"v_writelane_b32 v11, s18, 21\n" \
"s_add_i32 s18, s16, 0x19000\n" \
"v_mfma_f32_16x16x32_bf16 v[164:167], a[240:243], a[220:223], v[164:167]\n" \
"ds_read_b128 a[156:159], v28 offset:47104\n" \
"ds_read_b128 a[220:223], v29 offset:47104\n" \
"s_waitcnt lgkmcnt(15)\n" \
"v_mfma_f32_16x16x32_bf16 a[0:3], a[160:163], a[128:131], a[0:3]\n" \
"s_addk_i32 s84, 0x2000\n" \
"v_mfma_f32_16x16x32_bf16 a[0:3], a[224:227], a[192:195], a[0:3]\n" \
"buffer_load_dwordx4 v24, s[76:79], s84 offen offset:2048 lds\n" \
"v_mfma_f32_16x16x32_bf16 a[4:7], a[164:167], a[128:131], a[4:7]\n" \
"ds_read_b64_tr_b16 a[176:177], v36 offset:32768\n" \
"v_writelane_b32 v11, s17, 22\n" \
"v_mfma_f32_16x16x32_bf16 a[4:7], a[228:231], a[192:195], a[4:7]\n" \
"s_addk_i32 s84, 0x2000\n" \
"v_mfma_f32_16x16x32_bf16 a[12:15], a[172:175], a[128:131], a[12:15]\n" \
"buffer_load_dwordx4 v25, s[76:79], s84 offen offset:3072 lds\n" \
"s_waitcnt lgkmcnt(15)\n" \
"v_mfma_f32_16x16x32_bf16 a[12:15], a[236:239], a[192:195], a[12:15]\n" \
"ds_read_b64_tr_b16 a[178:179], v36 offset:33792\n" \
"s_mov_b32 s17, 0\n" \
"s_waitcnt lgkmcnt(12)\n" \
"v_mfma_f32_16x16x32_bf16 a[8:11], a[128:131], a[168:171], a[8:11]\n" \
"ds_read_b64_tr_b16 a[240:241], v36 offset:40960\n" \
"s_waitcnt lgkmcnt(9)\n" \
"v_mfma_f32_16x16x32_bf16 a[8:11], a[192:195], a[232:235], a[8:11]\n" \
"ds_read_b64_tr_b16 a[242:243], v36 offset:41984\n" \
"v_mfma_f32_16x16x32_bf16 a[24:27], a[132:135], a[168:171], a[24:27]\n" \
"ds_read_b64_tr_b16 a[180:181], v37 offset:32768\n" \
"v_mfma_f32_16x16x32_bf16 a[24:27], a[196:199], a[232:235], a[24:27]\n" \
"ds_read_b64_tr_b16 a[182:183], v37 offset:33792\n" \
"v_mfma_f32_16x16x32_bf16 a[28:31], a[172:175], a[132:135], a[28:31]\n" \
"ds_read_b64_tr_b16 a[244:245], v37 offset:40960\n" \
"v_mfma_f32_16x16x32_bf16 a[28:31], a[236:239], a[196:199], a[28:31]\n" \
"s_add_u32 s89, s42, 0x0\n" \
"buffer_store_dwordx4 v[216:219], v20, s[80:83], s89 offen offset:0 sc1\n" \
"v_mfma_f32_16x16x32_bf16 a[20:23], a[164:167], a[132:135], a[20:23]\n" \
"ds_read_b64_tr_b16 a[246:247], v37 offset:41984\n" \
"v_mfma_f32_16x16x32_bf16 a[20:23], a[228:231], a[196:199], a[20:23]\n" \
"ds_read_b64_tr_b16 a[184:185], v38 offset:32768\n" \
"v_mfma_f32_16x16x32_bf16 a[16:19], a[160:163], a[132:135], a[16:19]\n" \
"ds_read_b64_tr_b16 a[186:187], v38 offset:33792\n" \
"v_mfma_f32_16x16x32_bf16 a[16:19], a[224:227], a[196:199], a[16:19]\n" \
"s_cmpk_lt_u32 s3, 3\n" \
"s_cselect_b32 s18, s18, s17\n" \
"v_mfma_f32_16x16x32_bf16 a[32:35], a[160:163], a[136:139], a[32:35]\n" \
"s_add_u32 s88, s42, 0x1000\n" \
"buffer_store_dwordx4 v[220:223], v20, s[80:83], s88 offen offset:0 sc1\n" \
"v_mfma_f32_16x16x32_bf16 a[32:35], a[224:227], a[200:203], a[32:35]\n" \
"ds_read_b64_tr_b16 a[248:249], v38 offset:40960\n" \
"s_bitset1_b32 s16, 17\n" \
"v_mfma_f32_16x16x32_bf16 a[36:39], a[164:167], a[136:139], a[36:39]\n" \
"ds_read_b64_tr_b16 a[250:251], v38 offset:41984\n" \
"v_writelane_b32 v11, s18, 23\n" \
"v_mfma_f32_16x16x32_bf16 a[36:39], a[228:231], a[200:203], a[36:39]\n" \
"ds_read_b64_tr_b16 a[188:189], v39 offset:32768\n" \
"s_movk_i32 s18, 0x2400\n" \
"v_mfma_f32_16x16x32_bf16 a[44:47], a[172:175], a[136:139], a[44:47]\n" \
"s_cmpk_lt_u32 s3, 3\n" \
"s_cselect_b32 s16, s16, s18\n" \
"v_mfma_f32_16x16x32_bf16 a[44:47], a[236:239], a[200:203], a[44:47]\n" \
"ds_read_b64_tr_b16 a[190:191], v39 offset:33792\n" \
"s_mul_i32 s19, s3, 0x9000\n" \
"v_mfma_f32_16x16x32_bf16 a[40:43], a[136:139], a[168:171], a[40:43]\n" \
"s_add_u32 s89, s42, 0x0\n" \
"buffer_store_dwordx4 v[192:195], v20, s[80:83], s89 offen offset:2048 sc1\n" \
"v_mfma_f32_16x16x32_bf16 a[40:43], a[200:203], a[232:235], a[40:43]\n" \
"ds_read_b64_tr_b16 a[252:253], v39 offset:40960\n" \
"v_writelane_b32 v11, s16, 24\n" \
"v_mfma_f32_16x16x32_bf16 a[56:59], a[140:143], a[168:171], a[56:59]\n" \
"ds_read_b64_tr_b16 a[254:255], v39 offset:41984\n" \
"s_lshl_b32 s16, s3, 10\n" \
"v_mfma_f32_16x16x32_bf16 a[56:59], a[204:207], a[232:235], a[56:59]\n" \
"s_addk_i32 s16, 0x4800\n" \
"v_writelane_b32 v11, s16, 25\n" \
"v_mfma_f32_16x16x32_bf16 a[60:63], a[172:175], a[140:143], a[60:63]\n" \
"s_add_i32 s16, s15, 1\n" \
"s_and_b32 s16, s16, 3\n" \
"v_mfma_f32_16x16x32_bf16 a[60:63], a[236:239], a[204:207], a[60:63]\n" \
"s_add_u32 s88, s42, 0x0\n" \
"buffer_store_dwordx4 v[196:199], v20, s[80:83], s88 offen offset:3072 sc1\n" \
"v_mfma_f32_16x16x32_bf16 a[52:55], a[164:167], a[140:143], a[52:55]\n" \
"s_or_b32 s16, s16, s5\n" \
"s_mul_i32 s18, s16, 0x24000\n" \
"v_mfma_f32_16x16x32_bf16 a[52:55], a[228:231], a[204:207], a[52:55]\n" \
"s_add_i32 s18, s18, s19\n" \
"s_xor_b32 s13, s13, 2\n" \
"v_mfma_f32_16x16x32_bf16 a[48:51], a[160:163], a[140:143], a[48:51]\n" \
"s_add_i32 s15, s15, -1\n" \
"v_writelane_b32 v11, s18, 27\n" \
"v_mfma_f32_16x16x32_bf16 a[48:51], a[224:227], a[204:207], a[48:51]\n" \
"s_mul_i32 s18, s13, 0x24000\n" \
"s_and_b32 s15, s15, 3\n" \
"s_waitcnt lgkmcnt(15)\n" \
"v_mfma_f32_16x16x32_bf16 a[64:67], a[164:167], v[248:251], a[64:67]\n" \
"s_add_i32 s18, s18, s19\n" \
"s_or_b32 s15, s15, s5\n" \
"v_mfma_f32_16x16x32_bf16 a[64:67], a[228:231], v[48:51], a[64:67]\n" \
"v_writelane_b32 v11, s18, 28\n" \
"s_mul_i32 s18, s15, 0x24000\n" \
"v_mfma_f32_16x16x32_bf16 a[80:83], a[172:175], v[248:251], a[80:83]\n" \
"s_add_i32 s18, s18, s19\n" \
"v_writelane_b32 v11, s18, 29\n" \
"v_mfma_f32_16x16x32_bf16 a[80:83], a[236:239], v[48:51], a[80:83]\n" \
"s_or_b32 s18, s9, s14\n" \
"s_mul_i32 s18, s18, 0x24800\n" \
"v_mfma_f32_16x16x32_bf16 a[68:71], v[248:251], a[168:171], a[68:71]\n" \
"s_lshl_b32 s16, s16, 12\n" \
"s_add_i32 s19, s18, 0x3004800\n" \
"v_mfma_f32_16x16x32_bf16 a[68:71], v[48:51], a[232:235], a[68:71]\n" \
"s_waitcnt vmcnt(4) lgkmcnt(0)\n" \
"s_barrier\n" \
"v_mov_b32_e32 v45, s62\n" \
"s_mov_b64 exec, 1\n" \
"buffer_store_dword v45, off, s[80:83], s37 offset:0 sc1\n" \
"s_mov_b64 exec, -1\n" \
"s_nop 0\n" \
"v_mfma_f32_16x16x32_bf16 v[52:55], a[176:179], a[128:131], v[52:55]\n" \
"s_add_u32 m0, s40, 0x8000\n" \
"s_add_u32 s85, s39, 0xa000\n" \
"v_mfma_f32_16x16x32_bf16 v[52:55], a[240:243], a[192:195], v[52:55]\n" \
"buffer_load_dwordx4 v20, s[80:83], s85 offen sc1 lds\n" \
"v_mfma_f32_16x16x32_bf16 v[56:59], a[180:183], a[128:131], v[56:59]\n" \
"ds_read_b64_tr_b16 a[160:161], v32 offset:0\n" \
"v_accvgpr_read_b32 v220, a120\n" \
"v_mfma_f32_16x16x32_bf16 v[56:59], a[244:247], a[192:195], v[56:59]\n" \
"ds_read_b64_tr_b16 a[162:163], v32 offset:512\n" \
"v_accvgpr_read_b32 v221, a121\n" \
"v_mfma_f32_16x16x32_bf16 v[60:63], a[184:187], a[128:131], v[60:63]\n" \
"s_add_u32 m0, s40, 0x9000\n" \
"s_add_u32 s85, s39, 0xb000\n" \
"v_mfma_f32_16x16x32_bf16 v[60:63], a[248:251], a[192:195], v[60:63]\n" \
"buffer_load_dwordx4 v20, s[80:83], s85 offen sc1 lds\n" \
"v_mfma_f32_16x16x32_bf16 v[64:67], a[188:191], a[128:131], v[64:67]\n" \
"ds_read_b64_tr_b16 a[224:225], v32 offset:4096\n" \
"v_accvgpr_read_b32 v222, a122\n" \
"v_mfma_f32_16x16x32_bf16 v[64:67], a[252:255], a[192:195], v[64:67]\n" \
"ds_read_b128 a[128:131], v28 offset:0\n" \
"v_accvgpr_read_b32 v223, a123\n" \
"v_mfma_f32_16x16x32_bf16 v[80:83], a[188:191], a[132:135], v[80:83]\n" \
"ds_read_b128 a[192:195], v29 offset:0\n" \
"v_accvgpr_read_b32 v180, a124\n" \
"v_mfma_f32_16x16x32_bf16 v[80:83], a[252:255], a[196:199], v[80:83]\n" \
"s_add_u32 m0, s40, 0xb000\n" \
"s_movk_i32 s84, 0x280\n" \
"v_mfma_f32_16x16x32_bf16 v[76:79], a[184:187], a[132:135], v[76:79]\n" \
"buffer_load_dwordx4 v21, s[76:79], s84 offen offset:0 lds\n" \
"v_mfma_f32_16x16x32_bf16 v[76:79], a[248:251], a[196:199], v[76:79]\n" \
"ds_read_b64_tr_b16 a[226:227], v32 offset:4608\n" \
"v_accvgpr_read_b32 v181, a125\n" \
"v_mfma_f32_16x16x32_bf16 v[72:75], a[180:183], a[132:135], v[72:75]\n" \
"s_add_u32 m0, s30, 0x8000\n" \
"s_movk_i32 s84, 0x280\n" \
"v_mfma_f32_16x16x32_bf16 v[72:75], a[244:247], a[196:199], v[72:75]\n" \
"buffer_load_dwordx4 v24, s[76:79], s84 offen offset:0 lds\n" \
"v_mfma_f32_16x16x32_bf16 v[68:71], a[176:179], a[132:135], v[68:71]\n" \
"ds_read_b64_tr_b16 a[164:165], v33 offset:0\n" \
"v_accvgpr_read_b32 v182, a126\n" \
"v_mfma_f32_16x16x32_bf16 v[68:71], a[240:243], a[196:199], v[68:71]\n" \
"ds_read_b128 a[132:135], v28 offset:2048\n" \
"v_accvgpr_read_b32 v183, a127\n" \
"v_mfma_f32_16x16x32_bf16 v[84:87], a[176:179], a[136:139], v[84:87]\n" \
"ds_read_b128 a[196:199], v29 offset:2048\n" \
"v_cvt_pk_bf16_f32 v220, v220, v221\n" \
"v_mfma_f32_16x16x32_bf16 v[84:87], a[240:243], a[200:203], v[84:87]\n" \
"s_movk_i32 s84, 0x2280\n" \
"v_mfma_f32_16x16x32_bf16 v[88:91], a[180:183], a[136:139], v[88:91]\n" \
"buffer_load_dwordx4 v25, s[76:79], s84 offen offset:1024 lds\n" \
"v_mfma_f32_16x16x32_bf16 v[88:91], a[244:247], a[200:203], v[88:91]\n" \
"ds_read_b64_tr_b16 a[166:167], v33 offset:512\n" \
"v_cvt_pk_bf16_f32 v221, v222, v223\n" \
"v_mfma_f32_16x16x32_bf16 v[92:95], a[184:187], a[136:139], v[92:95]\n" \
"ds_read_b64_tr_b16 a[228:229], v33 offset:4096\n" \
"v_cvt_pk_bf16_f32 v222, v180, v181\n" \
"v_mfma_f32_16x16x32_bf16 v[92:95], a[248:251], a[200:203], v[92:95]\n" \
"s_movk_i32 s84, 0x4280\n" \
"v_mfma_f32_16x16x32_bf16 v[96:99], a[188:191], a[136:139], v[96:99]\n" \
"buffer_load_dwordx4 v24, s[76:79], s84 offen offset:2048 lds\n" \
"v_mfma_f32_16x16x32_bf16 v[96:99], a[252:255], a[200:203], v[96:99]\n" \
"ds_read_b128 a[136:139], v28 offset:4096\n" \
"v_cvt_pk_bf16_f32 v223, v182, v183\n" \
"v_mfma_f32_16x16x32_bf16 v[112:115], a[188:191], a[140:143], v[112:115]\n" \
"ds_read_b128 a[200:203], v29 offset:4096\n" \
"v_accvgpr_read_b32 v216, a112\n" \
"v_mfma_f32_16x16x32_bf16 v[112:115], a[252:255], a[204:207], v[112:115]\n" \
"s_movk_i32 s84, 0x6280\n" \
"s_nop 0\n" \
"v_mfma_f32_16x16x32_bf16 v[108:111], a[184:187], a[140:143], v[108:111]\n" \
"buffer_load_dwordx4 v25, s[76:79], s84 offen offset:3072 lds\n" \
"v_mfma_f32_16x16x32_bf16 v[108:111], a[248:251], a[204:207], v[108:111]\n" \
"ds_read_b64_tr_b16 a[230:231], v33 offset:4608\n" \
"v_accvgpr_read_b32 v217, a113\n" \
"v_mfma_f32_16x16x32_bf16 v[104:107], a[180:183], a[140:143], v[104:107]\n" \
"ds_read_b64_tr_b16 a[172:173], v35 offset:0\n" \
"v_accvgpr_read_b32 v218, a114\n" \
"v_mfma_f32_16x16x32_bf16 v[104:107], a[244:247], a[204:207], v[104:107]\n" \
"ds_read_b64_tr_b16 a[174:175], v35 offset:512\n" \
"v_accvgpr_read_b32 v219, a115\n" \
"v_mfma_f32_16x16x32_bf16 v[100:103], a[176:179], a[140:143], v[100:103]\n" \
"ds_read_b64_tr_b16 a[236:237], v35 offset:4096\n" \
"v_accvgpr_read_b32 v180, a116\n" \
"v_mfma_f32_16x16x32_bf16 v[100:103], a[240:243], a[204:207], v[100:103]\n" \
"ds_read_b128 a[140:143], v28 offset:6144\n" \
"v_accvgpr_read_b32 v181, a117\n" \
"v_mfma_f32_16x16x32_bf16 v[116:119], a[176:179], a[144:147], v[116:119]\n" \
"ds_read_b128 a[204:207], v29 offset:6144\n" \
"v_accvgpr_read_b32 v182, a118\n" \
"v_mfma_f32_16x16x32_bf16 v[116:119], a[240:243], a[208:211], v[116:119]\n" \
"s_add_u32 m0, s31, 0x8000\n" \
"s_mov_b32 s84, 0x280000\n" \
"v_mfma_f32_16x16x32_bf16 v[120:123], a[180:183], a[144:147], v[120:123]\n" \
"buffer_load_dwordx4 v26, s[68:71], s84 offen offset:0 lds\n" \
"v_mfma_f32_16x16x32_bf16 v[120:123], a[244:247], a[208:211], v[120:123]\n" \
"s_addk_i32 s84, 0x7c00\n" \
"s_nop 0\n" \
"v_mfma_f32_16x16x32_bf16 v[124:127], a[184:187], a[144:147], v[124:127]\n" \
"buffer_load_dwordx4 v26, s[68:71], s84 offen offset:1024 lds\n" \
"v_mfma_f32_16x16x32_bf16 v[124:127], a[248:251], a[208:211], v[124:127]\n" \
"ds_read_b64_tr_b16 a[238:239], v35 offset:4608\n" \
"v_accvgpr_read_b32 v183, a119\n" \
"v_mfma_f32_16x16x32_bf16 v[128:131], a[188:191], a[144:147], v[128:131]\n" \
"ds_read_b64_tr_b16 a[168:169], v34 offset:0\n" \
"v_cvt_pk_bf16_f32 v216, v216, v217\n" \
"v_mfma_f32_16x16x32_bf16 v[128:131], a[252:255], a[208:211], v[128:131]\n" \
"ds_read_b128 a[144:147], v28 offset:8192\n" \
"v_cvt_pk_bf16_f32 v217, v218, v219\n" \
"v_mfma_f32_16x16x32_bf16 v[144:147], a[188:191], a[148:151], v[144:147]\n" \
"ds_read_b128 a[208:211], v29 offset:8192\n" \
"v_cvt_pk_bf16_f32 v218, v180, v181\n" \
"v_mfma_f32_16x16x32_bf16 v[144:147], a[252:255], a[212:215], v[144:147]\n" \
"s_addk_i32 s84, 0x7c00\n" \
"v_mfma_f32_16x16x32_bf16 v[140:143], a[184:187], a[148:151], v[140:143]\n" \
"buffer_load_dwordx4 v27, s[68:71], s84 offen offset:2048 lds\n" \
"v_mfma_f32_16x16x32_bf16 v[140:143], a[248:251], a[212:215], v[140:143]\n" \
"s_addk_i32 s84, 0x7c00\n" \
"v_mfma_f32_16x16x32_bf16 v[136:139], a[180:183], a[148:151], v[136:139]\n" \
"buffer_load_dwordx4 v27, s[68:71], s84 offen offset:3072 lds\n" \
"v_mfma_f32_16x16x32_bf16 v[136:139], a[244:247], a[212:215], v[136:139]\n" \
"ds_read_b64_tr_b16 a[170:171], v34 offset:512\n" \
"v_cvt_pk_bf16_f32 v219, v182, v183\n" \
"v_mfma_f32_16x16x32_bf16 v[132:135], a[176:179], a[148:151], v[132:135]\n" \
"ds_read_b64_tr_b16 a[232:233], v34 offset:4096\n" \
"v_accvgpr_read_b32 v196, a104\n" \
"v_mfma_f32_16x16x32_bf16 v[132:135], a[240:243], a[212:215], v[132:135]\n" \
"ds_read_b128 a[148:151], v28 offset:10240\n" \
"v_accvgpr_read_b32 v197, a105\n" \
"v_mfma_f32_16x16x32_bf16 v[148:151], a[176:179], a[152:155], v[148:151]\n" \
"ds_read_b128 a[212:215], v29 offset:10240\n" \
"v_accvgpr_read_b32 v198, a106\n" \
"v_mfma_f32_16x16x32_bf16 v[148:151], a[240:243], a[216:219], v[148:151]\n" \
"s_add_u32 m0, s30, 0x9000\n" \
"s_mov_b32 s84, 0x9280\n" \
"v_mfma_f32_16x16x32_bf16 v[152:155], a[180:183], a[152:155], v[152:155]\n" \
"buffer_load_dwordx4 v24, s[76:79], s84 offen offset:0 lds\n" \
"v_mfma_f32_16x16x32_bf16 v[152:155], a[244:247], a[216:219], v[152:155]\n" \
"s_addk_i32 s84, 0x2000\n" \
"s_nop 0\n" \
"v_mfma_f32_16x16x32_bf16 v[156:159], a[184:187], a[152:155], v[156:159]\n" \
"buffer_load_dwordx4 v25, s[76:79], s84 offen offset:1024 lds\n" \
"v_mfma_f32_16x16x32_bf16 v[156:159], a[248:251], a[216:219], v[156:159]\n" \
"ds_read_b64_tr_b16 a[234:235], v34 offset:4608\n" \
"v_accvgpr_read_b32 v199, a107\n" \
"v_mfma_f32_16x16x32_bf16 v[160:163], a[188:191], a[152:155], v[160:163]\n" \
"ds_read_b128 v[248:251], v22 offset:0\n" \
"v_accvgpr_read_b32 v180, a108\n" \
"v_mfma_f32_16x16x32_bf16 v[160:163], a[252:255], a[216:219], v[160:163]\n" \
"ds_read_b128 a[152:155], v28 offset:12288\n" \
"v_accvgpr_read_b32 v181, a109\n" \
"v_mfma_f32_16x16x32_bf16 v[176:179], a[188:191], a[156:159], v[176:179]\n" \
"ds_read_b128 a[216:219], v29 offset:12288\n" \
"v_accvgpr_read_b32 v182, a110\n" \
"v_mfma_f32_16x16x32_bf16 v[176:179], a[252:255], a[220:223], v[176:179]\n" \
"ds_read_b128 v[48:51], v23 offset:0\n" \
"v_accvgpr_read_b32 v183, a111\n" \
"v_mfma_f32_16x16x32_bf16 v[172:175], a[184:187], a[156:159], v[172:175]\n" \
"v_cvt_pk_bf16_f32 v196, v196, v197\n" \
"v_cvt_pk_bf16_f32 v197, v198, v199\n" \
"v_mfma_f32_16x16x32_bf16 v[172:175], a[248:251], a[220:223], v[172:175]\n" \
"v_cvt_pk_bf16_f32 v198, v180, v181\n" \
"v_cvt_pk_bf16_f32 v199, v182, v183\n" \
"v_mfma_f32_16x16x32_bf16 v[168:171], a[180:183], a[156:159], v[168:171]\n" \
"v_accvgpr_read_b32 v192, a96\n" \
"v_accvgpr_read_b32 v193, a97\n" \
"v_mfma_f32_16x16x32_bf16 v[168:171], a[244:247], a[220:223], v[168:171]\n" \
"v_accvgpr_read_b32 v194, a98\n" \
"v_accvgpr_read_b32 v195, a99\n" \
"v_mfma_f32_16x16x32_bf16 v[164:167], a[176:179], a[156:159], v[164:167]\n" \
"v_accvgpr_read_b32 v180, a100\n" \
"v_accvgpr_read_b32 v181, a101\n" \
"v_mfma_f32_16x16x32_bf16 v[164:167], a[240:243], a[220:223], v[164:167]\n" \
"ds_read_b128 a[156:159], v28 offset:14336\n" \
"ds_read_b128 a[220:223], v29 offset:14336\n" \
"s_waitcnt lgkmcnt(15)\n" \
"v_mfma_f32_16x16x32_bf16 a[0:3], a[160:163], a[128:131], a[0:3]\n" \
"s_addk_i32 s84, 0x2000\n" \
"v_mfma_f32_16x16x32_bf16 a[0:3], a[224:227], a[192:195], a[0:3]\n" \
"buffer_load_dwordx4 v24, s[76:79], s84 offen offset:2048 lds\n" \
"v_mfma_f32_16x16x32_bf16 a[4:7], a[164:167], a[128:131], a[4:7]\n" \
"ds_read_b64_tr_b16 a[176:177], v36 offset:0\n" \
"v_accvgpr_read_b32 v182, a102\n" \
"v_mfma_f32_16x16x32_bf16 a[4:7], a[228:231], a[192:195], a[4:7]\n" \
"s_addk_i32 s84, 0x2000\n" \
"v_mfma_f32_16x16x32_bf16 a[12:15], a[172:175], a[128:131], a[12:15]\n" \
"buffer_load_dwordx4 v25, s[76:79], s84 offen offset:3072 lds\n" \
"s_waitcnt lgkmcnt(15)\n" \
"v_mfma_f32_16x16x32_bf16 a[12:15], a[236:239], a[192:195], a[12:15]\n" \
"ds_read_b64_tr_b16 a[178:179], v36 offset:1024\n" \
"v_accvgpr_read_b32 v183, a103\n" \
"s_waitcnt lgkmcnt(12)\n" \
"v_mfma_f32_16x16x32_bf16 a[8:11], a[128:131], a[168:171], a[8:11]\n" \
"ds_read_b64_tr_b16 a[240:241], v36 offset:8192\n" \
"v_cvt_pk_bf16_f32 v192, v192, v193\n" \
"s_waitcnt lgkmcnt(9)\n" \
"v_mfma_f32_16x16x32_bf16 a[8:11], a[192:195], a[232:235], a[8:11]\n" \
"ds_read_b64_tr_b16 a[242:243], v36 offset:9216\n" \
"v_cvt_pk_bf16_f32 v193, v194, v195\n" \
"v_mfma_f32_16x16x32_bf16 a[24:27], a[132:135], a[168:171], a[24:27]\n" \
"ds_read_b64_tr_b16 a[180:181], v37 offset:0\n" \
"v_cvt_pk_bf16_f32 v194, v180, v181\n" \
"v_mfma_f32_16x16x32_bf16 a[24:27], a[196:199], a[232:235], a[24:27]\n" \
"ds_read_b64_tr_b16 a[182:183], v37 offset:1024\n" \
"v_cvt_pk_bf16_f32 v195, v182, v183\n" \
"v_mfma_f32_16x16x32_bf16 a[28:31], a[172:175], a[132:135], a[28:31]\n" \
"ds_read_b64_tr_b16 a[244:245], v37 offset:8192\n" \
"v_accvgpr_read_b32 v188, a88\n" \
"v_mfma_f32_16x16x32_bf16 a[28:31], a[236:239], a[196:199], a[28:31]\n" \
"ds_read_b64_tr_b16 a[246:247], v37 offset:9216\n" \
"v_accvgpr_read_b32 v189, a89\n" \
"v_mfma_f32_16x16x32_bf16 a[20:23], a[164:167], a[132:135], a[20:23]\n" \
"v_accvgpr_read_b32 v190, a90\n" \
"v_accvgpr_read_b32 v191, a91\n" \
"v_mfma_f32_16x16x32_bf16 a[20:23], a[228:231], a[196:199], a[20:23]\n" \
"s_add_u32 s89, s42, 0x1000\n" \
"buffer_store_dwordx4 a[84:87], v20, s[80:83], s89 offen offset:3072 sc1\n" \
"v_mfma_f32_16x16x32_bf16 a[16:19], a[160:163], a[132:135], a[16:19]\n" \
"ds_read_b64_tr_b16 a[184:185], v38 offset:0\n" \
"v_accvgpr_read_b32 v180, a92\n" \
"v_mfma_f32_16x16x32_bf16 a[16:19], a[224:227], a[196:199], a[16:19]\n" \
"ds_read_b64_tr_b16 a[186:187], v38 offset:1024\n" \
"v_accvgpr_read_b32 v181, a93\n" \
"v_mfma_f32_16x16x32_bf16 a[32:35], a[160:163], a[136:139], a[32:35]\n" \
"ds_read_b64_tr_b16 a[248:249], v38 offset:8192\n" \
"v_accvgpr_read_b32 v182, a94\n" \
"v_mfma_f32_16x16x32_bf16 a[32:35], a[224:227], a[200:203], a[32:35]\n" \
"ds_read_b64_tr_b16 a[250:251], v38 offset:9216\n" \
"v_accvgpr_read_b32 v183, a95\n" \
"v_mfma_f32_16x16x32_bf16 a[36:39], a[164:167], a[136:139], a[36:39]\n" \
"v_cvt_pk_bf16_f32 v188, v188, v189\n" \
"v_cvt_pk_bf16_f32 v189, v190, v191\n" \
"v_mfma_f32_16x16x32_bf16 a[36:39], a[228:231], a[200:203], a[36:39]\n" \
"s_add_u32 s88, s42, 0x1000\n" \
"buffer_store_dwordx4 v[252:255], v20, s[80:83], s88 offen offset:2048 sc1\n" \
"v_mfma_f32_16x16x32_bf16 a[44:47], a[172:175], a[136:139], a[44:47]\n" \
"ds_read_b64_tr_b16 a[188:189], v39 offset:0\n" \
"v_cvt_pk_bf16_f32 v190, v180, v181\n" \
"v_mfma_f32_16x16x32_bf16 a[44:47], a[236:239], a[200:203], a[44:47]\n" \
"ds_read_b64_tr_b16 a[190:191], v39 offset:1024\n" \
"v_cvt_pk_bf16_f32 v191, v182, v183\n" \
"v_mfma_f32_16x16x32_bf16 a[40:43], a[136:139], a[168:171], a[40:43]\n" \
"ds_read_b64_tr_b16 a[252:253], v39 offset:8192\n" \
"v_accvgpr_read_b32 v184, a72\n" \
"v_mfma_f32_16x16x32_bf16 a[40:43], a[200:203], a[232:235], a[40:43]\n" \
"v_accvgpr_read_b32 v185, a73\n" \
"v_accvgpr_read_b32 v186, a74\n" \
"v_mfma_f32_16x16x32_bf16 a[56:59], a[140:143], a[168:171], a[56:59]\n" \
"ds_read_b64_tr_b16 a[254:255], v39 offset:9216\n" \
"v_accvgpr_read_b32 v187, a75\n" \
"v_mfma_f32_16x16x32_bf16 a[56:59], a[204:207], a[232:235], a[56:59]\n" \
"v_accvgpr_read_b32 v180, a76\n" \
"v_accvgpr_read_b32 v181, a77\n" \
"v_mfma_f32_16x16x32_bf16 a[60:63], a[172:175], a[140:143], a[60:63]\n" \
"s_add_u32 s89, s42, 0x2000\n" \
"buffer_store_dwordx4 v[200:203], v20, s[80:83], s89 offen offset:2048 sc1\n" \
"v_mfma_f32_16x16x32_bf16 a[60:63], a[236:239], a[204:207], a[60:63]\n" \
"v_accvgpr_read_b32 v182, a78\n" \
"v_accvgpr_read_b32 v183, a79\n" \
"v_mfma_f32_16x16x32_bf16 a[52:55], a[164:167], a[140:143], a[52:55]\n" \
"v_cvt_pk_bf16_f32 v184, v184, v185\n" \
"v_cvt_pk_bf16_f32 v185, v186, v187\n" \
"v_mfma_f32_16x16x32_bf16 a[52:55], a[228:231], a[204:207], a[52:55]\n" \
"v_cvt_pk_bf16_f32 v186, v180, v181\n" \
"v_cvt_pk_bf16_f32 v187, v182, v183\n" \
"v_mfma_f32_16x16x32_bf16 a[48:51], a[160:163], a[140:143], a[48:51]\n" \
"s_add_i32 s20, s19, s16\n" \
"v_writelane_b32 v11, s20, 30\n" \
"v_mfma_f32_16x16x32_bf16 a[48:51], a[224:227], a[204:207], a[48:51]\n" \
"s_lshl_b32 s13, s13, 12\n" \
"s_lshl_b32 s15, s15, 12\n" \
"s_waitcnt lgkmcnt(15)\n" \
"v_mfma_f32_16x16x32_bf16 a[64:67], a[164:167], v[248:251], a[64:67]\n" \
"s_sub_i32 s14, s9, s14\n" \
"s_add_i32 s20, s19, s13\n" \
"v_mfma_f32_16x16x32_bf16 a[64:67], a[228:231], v[48:51], a[64:67]\n" \
"v_writelane_b32 v11, s20, 31\n" \
"s_add_i32 s19, s19, s15\n" \
"v_mfma_f32_16x16x32_bf16 a[80:83], a[172:175], v[248:251], a[80:83]\n" \
"s_mul_i32 s14, s14, 0x24800\n" \
"v_writelane_b32 v11, s19, 32\n" \
"v_mfma_f32_16x16x32_bf16 a[80:83], a[236:239], v[48:51], a[80:83]\n" \
"s_add_i32 s19, s14, 0x4120800\n" \
"s_add_i32 s16, s19, s16\n" \
"v_mfma_f32_16x16x32_bf16 a[68:71], v[248:251], a[168:171], a[68:71]\n" \
"v_writelane_b32 v11, s16, 33\n" \
"s_add_i32 s13, s19, s13\n" \
"v_mfma_f32_16x16x32_bf16 a[68:71], v[48:51], a[232:235], a[68:71]\n" \
"s_waitcnt vmcnt(3) lgkmcnt(0)\n" \
"s_barrier\n" \
"v_mfma_f32_16x16x32_bf16 v[52:55], a[176:179], a[128:131], v[52:55]\n" \
"s_add_u32 m0, s40, 0x0\n" \
"s_add_u32 s85, s39, 0xc000\n" \
"v_mfma_f32_16x16x32_bf16 v[52:55], a[240:243], a[192:195], v[52:55]\n" \
"buffer_load_dwordx4 v20, s[80:83], s85 offen sc1 lds\n" \
"v_mfma_f32_16x16x32_bf16 v[56:59], a[180:183], a[128:131], v[56:59]\n" \
"ds_read_b64_tr_b16 a[160:161], v32 offset:32768\n" \
"v_writelane_b32 v11, s13, 34\n" \
"v_mfma_f32_16x16x32_bf16 v[56:59], a[244:247], a[192:195], v[56:59]\n" \
"ds_read_b64_tr_b16 a[162:163], v32 offset:33280\n" \
"s_add_i32 s19, s19, s15\n" \
"v_mfma_f32_16x16x32_bf16 v[60:63], a[184:187], a[128:131], v[60:63]\n" \
"s_add_u32 m0, s40, 0x1000\n" \
"s_add_u32 s85, s39, 0xd000\n" \
"v_mfma_f32_16x16x32_bf16 v[60:63], a[248:251], a[192:195], v[60:63]\n" \
"buffer_load_dwordx4 v20, s[80:83], s85 offen sc1 lds\n" \
"v_mfma_f32_16x16x32_bf16 v[64:67], a[188:191], a[128:131], v[64:67]\n" \
"ds_read_b64_tr_b16 a[224:225], v32 offset:36864\n" \
"s_or_b32 s13, s18, 0x3000000\n" \
"v_mfma_f32_16x16x32_bf16 v[64:67], a[252:255], a[192:195], v[64:67]\n" \
"ds_read_b128 a[128:131], v28 offset:32768\n" \
"s_lshl_b32 s15, s6, 11\n" \
"v_mfma_f32_16x16x32_bf16 v[80:83], a[188:191], a[132:135], v[80:83]\n" \
"ds_read_b128 a[192:195], v29 offset:32768\n" \
"v_writelane_b32 v11, s19, 35\n" \
"v_mfma_f32_16x16x32_bf16 v[80:83], a[252:255], a[196:199], v[80:83]\n" \
"s_add_u32 m0, s40, 0x3000\n" \
"s_movk_i32 s84, 0x300\n" \
".fill (L_pb + 28384 - .) / 4, 4, 0xBF800000\n" \
"L_pp7:\n" \
"s_cbranch_execz L_pret\n" \
"v_mfma_f32_16x16x32_bf16 v[76:79], a[184:187], a[132:135], v[76:79]\n" \
"buffer_load_dwordx4 v21, s[76:79], s84 offen offset:0 lds\n" \
"v_mfma_f32_16x16x32_bf16 v[76:79], a[248:251], a[196:199], v[76:79]\n" \
"ds_read_b64_tr_b16 a[226:227], v32 offset:37376\n" \
"s_add_i32 s16, s13, s15\n" \
"v_mfma_f32_16x16x32_bf16 v[72:75], a[180:183], a[132:135], v[72:75]\n" \
"s_add_u32 m0, s30, 0x0\n" \
"s_movk_i32 s84, 0x300\n" \
"v_mfma_f32_16x16x32_bf16 v[72:75], a[244:247], a[196:199], v[72:75]\n" \
"buffer_load_dwordx4 v24, s[76:79], s84 offen offset:0 lds\n" \
"v_mfma_f32_16x16x32_bf16 v[68:71], a[176:179], a[132:135], v[68:71]\n" \
"ds_read_b64_tr_b16 a[164:165], v33 offset:32768\n" \
"v_writelane_b32 v11, s16, 36\n" \
"v_mfma_f32_16x16x32_bf16 v[68:71], a[240:243], a[196:199], v[68:71]\n" \
"ds_read_b128 a[132:135], v28 offset:34816\n" \
"s_add_i32 s16, s14, 0x411c000\n" \
"v_mfma_f32_16x16x32_bf16 v[84:87], a[176:179], a[136:139], v[84:87]\n" \
"ds_read_b128 a[196:199], v29 offset:34816\n" \
"s_add_i32 s19, s16, s15\n" \
"v_mfma_f32_16x16x32_bf16 v[84:87], a[240:243], a[200:203], v[84:87]\n" \
"s_movk_i32 s84, 0x2300\n" \
"v_mfma_f32_16x16x32_bf16 v[88:91], a[180:183], a[136:139], v[88:91]\n" \
"buffer_load_dwordx4 v25, s[76:79], s84 offen offset:1024 lds\n" \
"v_mfma_f32_16x16x32_bf16 v[88:91], a[244:247], a[200:203], v[88:91]\n" \
"ds_read_b64_tr_b16 a[166:167], v33 offset:33280\n" \
"v_writelane_b32 v11, s19, 37\n" \
"v_mfma_f32_16x16x32_bf16 v[92:95], a[184:187], a[136:139], v[92:95]\n" \
"ds_read_b64_tr_b16 a[228:229], v33 offset:36864\n" \
"s_add_i32 s18, s18, 0x3004000\n" \
"v_mfma_f32_16x16x32_bf16 v[92:95], a[248:251], a[200:203], v[92:95]\n" \
"s_movk_i32 s84, 0x4300\n" \
"v_mfma_f32_16x16x32_bf16 v[96:99], a[188:191], a[136:139], v[96:99]\n" \
"buffer_load_dwordx4 v24, s[76:79], s84 offen offset:2048 lds\n" \
"v_mfma_f32_16x16x32_bf16 v[96:99], a[252:255], a[200:203], v[96:99]\n" \
"ds_read_b128 a[136:139], v28 offset:36864\n" \
"v_writelane_b32 v11, s18, 38\n" \
"v_mfma_f32_16x16x32_bf16 v[112:115], a[188:191], a[140:143], v[112:115]\n" \
"ds_read_b128 a[200:203], v29 offset:36864\n" \
"s_add_i32 s14, s14, 0x4120000\n" \
"v_mfma_f32_16x16x32_bf16 v[112:115], a[252:255], a[204:207], v[112:115]\n" \
"s_movk_i32 s84, 0x6300\n" \
"v_mfma_f32_16x16x32_bf16 v[108:111], a[184:187], a[140:143], v[108:111]\n" \
"buffer_load_dwordx4 v25, s[76:79], s84 offen offset:3072 lds\n" \
"v_mfma_f32_16x16x32_bf16 v[108:111], a[248:251], a[204:207], v[108:111]\n" \
"ds_read_b64_tr_b16 a[230:231], v33 offset:37376\n" \
"v_writelane_b32 v11, s14, 39\n" \
"v_mfma_f32_16x16x32_bf16 v[104:107], a[180:183], a[140:143], v[104:107]\n" \
"ds_read_b64_tr_b16 a[172:173], v35 offset:32768\n" \
"s_lshl_b32 s14, s4, 2\n" \
"v_mfma_f32_16x16x32_bf16 v[104:107], a[244:247], a[204:207], v[104:107]\n" \
"ds_read_b64_tr_b16 a[174:175], v35 offset:33280\n" \
"s_or_b32 s8, s14, s8\n" \
"v_mfma_f32_16x16x32_bf16 v[100:103], a[176:179], a[140:143], v[100:103]\n" \
"ds_read_b64_tr_b16 a[236:237], v35 offset:36864\n" \
"s_mul_i32 s8, s8, 0x90000\n" \
"v_mfma_f32_16x16x32_bf16 v[100:103], a[240:243], a[204:207], v[100:103]\n" \
"ds_read_b128 a[140:143], v28 offset:38912\n" \
"s_lshl_b32 s14, s3, 14\n" \
"v_mfma_f32_16x16x32_bf16 v[116:119], a[176:179], a[144:147], v[116:119]\n" \
"ds_read_b128 a[204:207], v29 offset:38912\n" \
"s_add_i32 s8, s8, s14\n" \
"v_mfma_f32_16x16x32_bf16 v[116:119], a[240:243], a[208:211], v[116:119]\n" \
"s_add_u32 m0, s31, 0x0\n" \
"s_mov_b32 s84, 0x300000\n" \
"v_mfma_f32_16x16x32_bf16 v[120:123], a[180:183], a[144:147], v[120:123]\n" \
"buffer_load_dwordx4 v26, s[68:71], s84 offen offset:0 lds\n" \
"v_mfma_f32_16x16x32_bf16 v[120:123], a[244:247], a[208:211], v[120:123]\n" \
"s_addk_i32 s84, 0x7c00\n" \
"v_mfma_f32_16x16x32_bf16 v[124:127], a[184:187], a[144:147], v[124:127]\n" \
"buffer_load_dwordx4 v26, s[68:71], s84 offen offset:1024 lds\n" \
"v_mfma_f32_16x16x32_bf16 v[124:127], a[248:251], a[208:211], v[124:127]\n" \
"ds_read_b64_tr_b16 a[238:239], v35 offset:37376\n" \
"s_lshl_b32 s9, s9, 4\n" \
"v_mfma_f32_16x16x32_bf16 v[128:131], a[188:191], a[144:147], v[128:131]\n" \
"ds_read_b64_tr_b16 a[168:169], v34 offset:32768\n" \
"s_lshl_b32 s14, s4, 7\n" \
"v_mfma_f32_16x16x32_bf16 v[128:131], a[252:255], a[208:211], v[128:131]\n" \
"ds_read_b128 a[144:147], v28 offset:40960\n" \
"s_or_b32 s18, s9, s14\n" \
"v_mfma_f32_16x16x32_bf16 v[144:147], a[188:191], a[148:151], v[144:147]\n" \
"ds_read_b128 a[208:211], v29 offset:40960\n" \
"s_sub_i32 s9, s9, s14\n" \
"v_mfma_f32_16x16x32_bf16 v[144:147], a[252:255], a[212:215], v[144:147]\n" \
"s_addk_i32 s84, 0x7c00\n" \
"v_mfma_f32_16x16x32_bf16 v[140:143], a[184:187], a[148:151], v[140:143]\n" \
"buffer_load_dwordx4 v27, s[68:71], s84 offen offset:2048 lds\n" \
"v_mfma_f32_16x16x32_bf16 v[140:143], a[248:251], a[212:215], v[140:143]\n" \
"s_addk_i32 s84, 0x7c00\n" \
"v_mfma_f32_16x16x32_bf16 v[136:139], a[180:183], a[148:151], v[136:139]\n" \
"buffer_load_dwordx4 v27, s[68:71], s84 offen offset:3072 lds\n" \
"v_mfma_f32_16x16x32_bf16 v[136:139], a[244:247], a[212:215], v[136:139]\n" \
"ds_read_b64_tr_b16 a[170:171], v34 offset:33280\n" \
"s_mulk_i32 s18, 0x480\n" \
"v_mfma_f32_16x16x32_bf16 v[132:135], a[176:179], a[148:151], v[132:135]\n" \
"ds_read_b64_tr_b16 a[232:233], v34 offset:36864\n" \
"s_mulk_i32 s9, 0x480\n" \
"v_mfma_f32_16x16x32_bf16 v[132:135], a[240:243], a[212:215], v[132:135]\n" \
"ds_read_b128 a[148:151], v28 offset:43008\n" \
"s_or_b32 s14, s18, s12\n" \
"v_mfma_f32_16x16x32_bf16 v[148:151], a[176:179], a[152:155], v[148:151]\n" \
"ds_read_b128 a[212:215], v29 offset:43008\n" \
"s_or_b32 s9, s9, s12\n" \
"v_mfma_f32_16x16x32_bf16 v[148:151], a[240:243], a[216:219], v[148:151]\n" \
"s_add_u32 m0, s30, 0x1000\n" \
"s_mov_b32 s84, 0x9300\n" \
"v_mfma_f32_16x16x32_bf16 v[152:155], a[180:183], a[152:155], v[152:155]\n" \
"buffer_load_dwordx4 v24, s[76:79], s84 offen offset:0 lds\n" \
"v_mfma_f32_16x16x32_bf16 v[152:155], a[244:247], a[216:219], v[152:155]\n" \
"s_addk_i32 s84, 0x2000\n" \
"v_mfma_f32_16x16x32_bf16 v[156:159], a[184:187], a[152:155], v[156:159]\n" \
"buffer_load_dwordx4 v25, s[76:79], s84 offen offset:1024 lds\n" \
"v_mfma_f32_16x16x32_bf16 v[156:159], a[248:251], a[216:219], v[156:159]\n" \
"ds_read_b64_tr_b16 a[234:235], v34 offset:37376\n" \
"s_and_b32 s12, s2, 1\n" \
"v_mfma_f32_16x16x32_bf16 v[160:163], a[188:191], a[152:155], v[160:163]\n" \
"ds_read_b128 v[248:251], v22 offset:32768\n" \
"v_mfma_f32_16x16x32_bf16 v[160:163], a[252:255], a[216:219], v[160:163]\n" \
"ds_read_b128 a[152:155], v28 offset:45056\n" \
"v_mfma_f32_16x16x32_bf16 v[176:179], a[188:191], a[156:159], v[176:179]\n" \
"ds_read_b128 a[216:219], v29 offset:45056\n" \
"v_mfma_f32_16x16x32_bf16 v[176:179], a[252:255], a[220:223], v[176:179]\n" \
"ds_read_b128 v[48:51], v23 offset:32768\n" \
"v_mfma_f32_16x16x32_bf16 v[172:175], a[184:187], a[156:159], v[172:175]\n" \
"s_cmp_lg_u32 s12, 0\n" \
"s_cselect_b32 s13, s16, s13\n" \
"v_mfma_f32_16x16x32_bf16 v[172:175], a[248:251], a[220:223], v[172:175]\n" \
"s_bfe_u32 s16, s2, 0x20001\n" \
"s_addk_i32 s13, 0x4000\n" \
"v_mfma_f32_16x16x32_bf16 v[168:171], a[180:183], a[156:159], v[168:171]\n" \
"s_xor_b32 s16, s16, s3\n" \
"v_mfma_f32_16x16x32_bf16 v[168:171], a[244:247], a[220:223], v[168:171]\n" \
"s_cmp_lg_u32 s16, 0\n" \
"s_cselect_b32 s13, s7, s13\n" \
"v_mfma_f32_16x16x32_bf16 v[164:167], a[176:179], a[156:159], v[164:167]\n" \
"s_lshl_b32 s6, s6, 16\n" \
"v_writelane_b32 v11, s13, 40\n" \
"v_mfma_f32_16x16x32_bf16 v[164:167], a[240:243], a[220:223], v[164:167]\n" \
"ds_read_b128 a[156:159], v28 offset:47104\n" \
"ds_read_b128 a[220:223], v29 offset:47104\n" \
"s_waitcnt lgkmcnt(15)\n" \
"v_mfma_f32_16x16x32_bf16 a[0:3], a[160:163], a[128:131], a[0:3]\n" \
"s_addk_i32 s84, 0x2000\n" \
"v_mfma_f32_16x16x32_bf16 a[0:3], a[224:227], a[192:195], a[0:3]\n" \
"buffer_load_dwordx4 v24, s[76:79], s84 offen offset:2048 lds\n" \
"v_mfma_f32_16x16x32_bf16 a[4:7], a[164:167], a[128:131], a[4:7]\n" \
"ds_read_b64_tr_b16 a[176:177], v36 offset:32768\n" \
"s_add_i32 s13, s8, 0x4240000\n" \
"v_mfma_f32_16x16x32_bf16 a[4:7], a[228:231], a[192:195], a[4:7]\n" \
"s_addk_i32 s84, 0x2000\n" \
"v_mfma_f32_16x16x32_bf16 a[12:15], a[172:175], a[128:131], a[12:15]\n" \
"buffer_load_dwordx4 v25, s[76:79], s84 offen offset:3072 lds\n" \
"s_waitcnt lgkmcnt(15)\n" \
"v_mfma_f32_16x16x32_bf16 a[12:15], a[236:239], a[192:195], a[12:15]\n" \
"ds_read_b64_tr_b16 a[178:179], v36 offset:33792\n" \
"s_add_i32 s6, s13, s6\n" \
"s_waitcnt lgkmcnt(12)\n" \
"v_mfma_f32_16x16x32_bf16 a[8:11], a[128:131], a[168:171], a[8:11]\n" \
"ds_read_b64_tr_b16 a[240:241], v36 offset:40960\n" \
"v_writelane_b32 v11, s6, 41\n" \
"s_waitcnt lgkmcnt(9)\n" \
"v_mfma_f32_16x16x32_bf16 a[8:11], a[192:195], a[232:235], a[8:11]\n" \
"ds_read_b64_tr_b16 a[242:243], v36 offset:41984\n" \
"s_add_i32 s8, s8, 0x42c0000\n" \
"v_mfma_f32_16x16x32_bf16 a[24:27], a[132:135], a[168:171], a[24:27]\n" \
"ds_read_b64_tr_b16 a[180:181], v37 offset:32768\n" \
"s_lshl_b32 s6, s12, 10\n" \
"v_mfma_f32_16x16x32_bf16 a[24:27], a[196:199], a[232:235], a[24:27]\n" \
"ds_read_b64_tr_b16 a[182:183], v37 offset:33792\n" \
"s_or_b32 s6, s8, s6\n" \
"v_mfma_f32_16x16x32_bf16 a[28:31], a[172:175], a[132:135], a[28:31]\n" \
"ds_read_b64_tr_b16 a[244:245], v37 offset:40960\n" \
"v_mfma_f32_16x16x32_bf16 a[28:31], a[236:239], a[196:199], a[28:31]\n" \
"ds_read_b64_tr_b16 a[246:247], v37 offset:41984\n" \
"v_mfma_f32_16x16x32_bf16 a[20:23], a[164:167], a[132:135], a[20:23]\n" \
"s_cmp_lg_u32 s16, 0\n" \
"s_cselect_b32 s6, s7, s6\n" \
"v_mfma_f32_16x16x32_bf16 a[20:23], a[228:231], a[196:199], a[20:23]\n" \
"s_add_u32 s88, s42, 0x2000\n" \
"buffer_store_dwordx4 v[204:207], v20, s[80:83], s88 offen offset:3072 sc1\n" \
"v_mfma_f32_16x16x32_bf16 a[16:19], a[160:163], a[132:135], a[16:19]\n" \
"ds_read_b64_tr_b16 a[184:185], v38 offset:32768\n" \
"s_mulk_i32 s4, 0x4800\n" \
"v_mfma_f32_16x16x32_bf16 a[16:19], a[224:227], a[196:199], a[16:19]\n" \
"ds_read_b64_tr_b16 a[186:187], v38 offset:33792\n" \
"v_writelane_b32 v11, s6, 42\n" \
"v_mfma_f32_16x16x32_bf16 a[32:35], a[160:163], a[136:139], a[32:35]\n" \
"ds_read_b64_tr_b16 a[248:249], v38 offset:40960\n" \
"s_add_i32 s6, s14, s10\n" \
"v_mfma_f32_16x16x32_bf16 a[32:35], a[224:227], a[200:203], a[32:35]\n" \
"ds_read_b64_tr_b16 a[250:251], v38 offset:41984\n" \
"v_writelane_b32 v11, s6, 43\n" \
"v_mfma_f32_16x16x32_bf16 a[36:39], a[164:167], a[136:139], a[36:39]\n" \
"s_add_i32 s6, s9, 0x21c000\n" \
"s_add_i32 s9, s6, s10\n" \
"v_mfma_f32_16x16x32_bf16 a[36:39], a[228:231], a[200:203], a[36:39]\n" \
"s_add_u32 s89, s42, 0x3000\n" \
"buffer_store_dwordx4 v[208:211], v20, s[80:83], s89 offen offset:3072 sc1\n" \
"v_mfma_f32_16x16x32_bf16 a[44:47], a[172:175], a[136:139], a[44:47]\n" \
"ds_read_b64_tr_b16 a[188:189], v39 offset:32768\n" \
"v_mfma_f32_16x16x32_bf16 a[44:47], a[236:239], a[200:203], a[44:47]\n" \
"ds_read_b64_tr_b16 a[190:191], v39 offset:33792\n" \
"v_mfma_f32_16x16x32_bf16 a[40:43], a[136:139], a[168:171], a[40:43]\n" \
"ds_read_b64_tr_b16 a[252:253], v39 offset:40960\n" \
"v_mfma_f32_16x16x32_bf16 a[40:43], a[200:203], a[232:235], a[40:43]\n" \
"s_cmp_lg_u32 s12, 0\n" \
"s_cselect_b32 s6, s6, s14\n" \
"v_mfma_f32_16x16x32_bf16 a[56:59], a[140:143], a[168:171], a[56:59]\n" \
"ds_read_b64_tr_b16 a[254:255], v39 offset:41984\n" \
"s_addk_i32 s6, 0x100\n" \
"v_mfma_f32_16x16x32_bf16 a[56:59], a[204:207], a[232:235], a[56:59]\n" \
"v_writelane_b32 v11, s9, 44\n" \
"v_mfma_f32_16x16x32_bf16 a[60:63], a[172:175], a[140:143], a[60:63]\n" \
"s_add_u32 s88, s42, 0x3000\n" \
"buffer_store_dwordx4 v[212:215], v20, s[80:83], s88 offen offset:2048 sc1\n" \
"v_mfma_f32_16x16x32_bf16 a[60:63], a[236:239], a[204:207], a[60:63]\n" \
"s_cmp_lg_u32 s16, 0\n" \
"s_cselect_b32 s6, s7, s6\n" \
"v_mfma_f32_16x16x32_bf16 a[52:55], a[164:167], a[140:143], a[52:55]\n" \
"v_writelane_b32 v11, s6, 45\n" \
"s_or_b32 s6, s8, s15\n" \
"v_mfma_f32_16x16x32_bf16 a[52:55], a[228:231], a[204:207], a[52:55]\n" \
"v_writelane_b32 v11, s6, 53\n" \
"s_lshl_b32 s6, s3, 17\n" \
"v_mfma_f32_16x16x32_bf16 a[48:51], a[160:163], a[140:143], a[48:51]\n" \
"s_add_i32 s6, s13, s6\n" \
"s_or_b32 s5, s6, s5\n" \
"v_mfma_f32_16x16x32_bf16 a[48:51], a[224:227], a[204:207], a[48:51]\n" \
"s_or_b32 s5, s5, 0x3840\n" \
"v_writelane_b32 v11, s5, 6\n" \
"s_waitcnt lgkmcnt(15)\n" \
"v_mfma_f32_16x16x32_bf16 a[64:67], a[164:167], v[248:251], a[64:67]\n" \
"s_mov_b32 s5, 0xff0000\n" \
"v_mfma_f32_16x16x32_bf16 a[64:67], a[228:231], v[48:51], a[64:67]\n" \
"s_cmp_lg_u32 s16, 0\n" \
"s_cselect_b32 s5, s17, s5\n" \
"v_mfma_f32_16x16x32_bf16 a[80:83], a[172:175], v[248:251], a[80:83]\n" \
"v_writelane_b32 v11, s5, 7\n" \
"s_mul_i32 s5, s3, 0x1200\n" \
"v_mfma_f32_16x16x32_bf16 a[80:83], a[236:239], v[48:51], a[80:83]\n" \
"s_add_i32 s4, s5, s4\n" \
"s_add_i32 s4, s4, s11\n" \
"v_mfma_f32_16x16x32_bf16 a[68:71], v[248:251], a[168:171], a[68:71]\n" \
"v_writelane_b32 v11, s4, 8\n" \
"s_lshl_b32 s4, s2, 16\n" \
"v_mfma_f32_16x16x32_bf16 a[68:71], v[48:51], a[232:235], a[68:71]\n" \
"s_waitcnt vmcnt(3) lgkmcnt(0)\n" \
"s_barrier\n" \
"v_mfma_f32_16x16x32_bf16 v[52:55], a[176:179], a[128:131], v[52:55]\n" \
"s_add_u32 m0, s40, 0x8000\n" \
"s_add_u32 s85, s39, 0xe000\n" \
"v_mfma_f32_16x16x32_bf16 v[52:55], a[240:243], a[192:195], v[52:55]\n" \
"buffer_load_dwordx4 v20, s[80:83], s85 offen sc1 lds\n" \
"v_mfma_f32_16x16x32_bf16 v[56:59], a[180:183], a[128:131], v[56:59]\n" \
"ds_read_b64_tr_b16 a[160:161], v32 offset:0\n" \
"s_add_i32 s5, s4, 0x10000\n" \
"v_mfma_f32_16x16x32_bf16 v[56:59], a[244:247], a[192:195], v[56:59]\n" \
"ds_read_b64_tr_b16 a[162:163], v32 offset:512\n" \
"s_and_b32 s5, s5, 0x70000\n" \
"v_mfma_f32_16x16x32_bf16 v[60:63], a[184:187], a[128:131], v[60:63]\n" \
"s_add_u32 m0, s40, 0x9000\n" \
"s_add_u32 s85, s39, 0xf000\n" \
"v_mfma_f32_16x16x32_bf16 v[60:63], a[248:251], a[192:195], v[60:63]\n" \
"buffer_load_dwordx4 v20, s[80:83], s85 offen sc1 lds\n" \
"v_mfma_f32_16x16x32_bf16 v[64:67], a[188:191], a[128:131], v[64:67]\n" \
"ds_read_b64_tr_b16 a[224:225], v32 offset:4096\n" \
"s_add_i32 s5, s13, s5\n" \
"v_mfma_f32_16x16x32_bf16 v[64:67], a[252:255], a[192:195], v[64:67]\n" \
"ds_read_b128 a[128:131], v28 offset:0\n" \
"v_writelane_b32 v11, s5, 46\n" \
"v_mfma_f32_16x16x32_bf16 v[80:83], a[188:191], a[132:135], v[80:83]\n" \
"ds_read_b128 a[192:195], v29 offset:0\n" \
"s_add_i32 s5, s4, 0x20000\n" \
"v_mfma_f32_16x16x32_bf16 v[80:83], a[252:255], a[196:199], v[80:83]\n" \
"s_add_u32 m0, s40, 0xb000\n" \
"s_movk_i32 s84, 0x380\n" \
"v_mfma_f32_16x16x32_bf16 v[76:79], a[184:187], a[132:135], v[76:79]\n" \
"buffer_load_dwordx4 v21, s[76:79], s84 offen offset:0 lds\n" \
"v_mfma_f32_16x16x32_bf16 v[76:79], a[248:251], a[196:199], v[76:79]\n" \
"ds_read_b64_tr_b16 a[226:227], v32 offset:4608\n" \
"s_and_b32 s5, s5, 0x70000\n" \
"v_mfma_f32_16x16x32_bf16 v[72:75], a[180:183], a[132:135], v[72:75]\n" \
"s_add_u32 m0, s30, 0x8000\n" \
"s_movk_i32 s84, 0x380\n" \
"v_mfma_f32_16x16x32_bf16 v[72:75], a[244:247], a[196:199], v[72:75]\n" \
"buffer_load_dwordx4 v24, s[76:79], s84 offen offset:0 lds\n" \
"v_mfma_f32_16x16x32_bf16 v[68:71], a[176:179], a[132:135], v[68:71]\n" \
"ds_read_b64_tr_b16 a[164:165], v33 offset:0\n" \
"s_add_i32 s5, s13, s5\n" \
"v_mfma_f32_16x16x32_bf16 v[68:71], a[240:243], a[196:199], v[68:71]\n" \
"ds_read_b128 a[132:135], v28 offset:2048\n" \
"s_bitset1_b32 s5, 11\n" \
"v_mfma_f32_16x16x32_bf16 v[84:87], a[176:179], a[136:139], v[84:87]\n" \
"ds_read_b128 a[196:199], v29 offset:2048\n" \
"v_writelane_b32 v11, s5, 47\n" \
"v_mfma_f32_16x16x32_bf16 v[84:87], a[240:243], a[200:203], v[84:87]\n" \
"s_movk_i32 s84, 0x2380\n" \
"v_mfma_f32_16x16x32_bf16 v[88:91], a[180:183], a[136:139], v[88:91]\n" \
"buffer_load_dwordx4 v25, s[76:79], s84 offen offset:1024 lds\n" \
"v_mfma_f32_16x16x32_bf16 v[88:91], a[244:247], a[200:203], v[88:91]\n" \
"ds_read_b64_tr_b16 a[166:167], v33 offset:512\n" \
"s_add_i32 s5, s4, 0x30000\n" \
"v_mfma_f32_16x16x32_bf16 v[92:95], a[184:187], a[136:139], v[92:95]\n" \
"ds_read_b64_tr_b16 a[228:229], v33 offset:4096\n" \
"s_and_b32 s5, s5, 0x70000\n" \
"v_mfma_f32_16x16x32_bf16 v[92:95], a[248:251], a[200:203], v[92:95]\n" \
"s_movk_i32 s84, 0x4380\n" \
"v_mfma_f32_16x16x32_bf16 v[96:99], a[188:191], a[136:139], v[96:99]\n" \
"buffer_load_dwordx4 v24, s[76:79], s84 offen offset:2048 lds\n" \
"v_mfma_f32_16x16x32_bf16 v[96:99], a[252:255], a[200:203], v[96:99]\n" \
"ds_read_b128 a[136:139], v28 offset:4096\n" \
"s_add_i32 s5, s13, s5\n" \
"v_mfma_f32_16x16x32_bf16 v[112:115], a[188:191], a[140:143], v[112:115]\n" \
"ds_read_b128 a[200:203], v29 offset:4096\n" \
"s_bitset1_b32 s5, 12\n" \
"v_mfma_f32_16x16x32_bf16 v[112:115], a[252:255], a[204:207], v[112:115]\n" \
"s_movk_i32 s84, 0x6380\n" \
"v_mfma_f32_16x16x32_bf16 v[108:111], a[184:187], a[140:143], v[108:111]\n" \
"buffer_load_dwordx4 v25, s[76:79], s84 offen offset:3072 lds\n" \
"v_mfma_f32_16x16x32_bf16 v[108:111], a[248:251], a[204:207], v[108:111]\n" \
"ds_read_b64_tr_b16 a[230:231], v33 offset:4608\n" \
"v_writelane_b32 v11, s5, 48\n" \
"v_mfma_f32_16x16x32_bf16 v[104:107], a[180:183], a[140:143], v[104:107]\n" \
"ds_read_b64_tr_b16 a[172:173], v35 offset:0\n" \
"s_and_b32 s5, s4, 0x70000\n" \
"v_mfma_f32_16x16x32_bf16 v[104:107], a[244:247], a[204:207], v[104:107]\n" \
"ds_read_b64_tr_b16 a[174:175], v35 offset:512\n" \
"s_xor_b32 s5, s5, 0x40000\n" \
"v_mfma_f32_16x16x32_bf16 v[100:103], a[176:179], a[140:143], v[100:103]\n" \
"ds_read_b64_tr_b16 a[236:237], v35 offset:4096\n" \
"s_add_i32 s5, s13, s5\n" \
"s_nop 0\n" \
"v_mfma_f32_16x16x32_bf16 v[100:103], a[240:243], a[204:207], v[100:103]\n" \
"ds_read_b128 a[140:143], v28 offset:6144\n" \
"s_or_b32 s5, s5, 0x1800\n" \
"v_mfma_f32_16x16x32_bf16 v[116:119], a[176:179], a[144:147], v[116:119]\n" \
"ds_read_b128 a[204:207], v29 offset:6144\n" \
"v_writelane_b32 v11, s5, 49\n" \
"v_mfma_f32_16x16x32_bf16 v[116:119], a[240:243], a[208:211], v[116:119]\n" \
"s_add_u32 m0, s31, 0x8000\n" \
"s_mov_b32 s84, 0x380000\n" \
"v_mfma_f32_16x16x32_bf16 v[120:123], a[180:183], a[144:147], v[120:123]\n" \
"buffer_load_dwordx4 v26, s[68:71], s84 offen offset:0 lds\n" \
"v_mfma_f32_16x16x32_bf16 v[120:123], a[244:247], a[208:211], v[120:123]\n" \
"s_addk_i32 s84, 0x7c00\n" \
"v_mfma_f32_16x16x32_bf16 v[124:127], a[184:187], a[144:147], v[124:127]\n" \
"buffer_load_dwordx4 v26, s[68:71], s84 offen offset:1024 lds\n" \
"v_mfma_f32_16x16x32_bf16 v[124:127], a[248:251], a[208:211], v[124:127]\n" \
"ds_read_b64_tr_b16 a[238:239], v35 offset:4608\n" \
"s_add_i32 s5, s4, 0x50000\n" \
"v_mfma_f32_16x16x32_bf16 v[128:131], a[188:191], a[144:147], v[128:131]\n" \
"ds_read_b64_tr_b16 a[168:169], v34 offset:0\n" \
"s_and_b32 s5, s5, 0x70000\n" \
"v_mfma_f32_16x16x32_bf16 v[128:131], a[252:255], a[208:211], v[128:131]\n" \
"ds_read_b128 a[144:147], v28 offset:8192\n" \
"s_add_i32 s5, s13, s5\n" \
"v_mfma_f32_16x16x32_bf16 v[144:147], a[188:191], a[148:151], v[144:147]\n" \
"ds_read_b128 a[208:211], v29 offset:8192\n" \
"s_bitset1_b32 s5, 13\n" \
"v_mfma_f32_16x16x32_bf16 v[144:147], a[252:255], a[212:215], v[144:147]\n" \
"s_addk_i32 s84, 0x7c00\n" \
"v_mfma_f32_16x16x32_bf16 v[140:143], a[184:187], a[148:151], v[140:143]\n" \
"buffer_load_dwordx4 v27, s[68:71], s84 offen offset:2048 lds\n" \
"v_mfma_f32_16x16x32_bf16 v[140:143], a[248:251], a[212:215], v[140:143]\n" \
"s_addk_i32 s84, 0x7c00\n" \
"s_nop 0\n" \
"v_mfma_f32_16x16x32_bf16 v[136:139], a[180:183], a[148:151], v[136:139]\n" \
"buffer_load_dwordx4 v27, s[68:71], s84 offen offset:3072 lds\n" \
"v_mfma_f32_16x16x32_bf16 v[136:139], a[244:247], a[212:215], v[136:139]\n" \
"ds_read_b64_tr_b16 a[170:171], v34 offset:512\n" \
"v_writelane_b32 v11, s5, 50\n" \
"v_mfma_f32_16x16x32_bf16 v[132:135], a[176:179], a[148:151], v[132:135]\n" \
"ds_read_b64_tr_b16 a[232:233], v34 offset:4096\n" \
"s_add_i32 s5, s4, 0x60000\n" \
"v_mfma_f32_16x16x32_bf16 v[132:135], a[240:243], a[212:215], v[132:135]\n" \
"ds_read_b128 a[148:151], v28 offset:10240\n" \
"s_and_b32 s5, s5, 0x70000\n" \
"v_mfma_f32_16x16x32_bf16 v[148:151], a[176:179], a[152:155], v[148:151]\n" \
"ds_read_b128 a[212:215], v29 offset:10240\n" \
"s_add_i32 s4, s4, 0x70000\n" \
"v_mfma_f32_16x16x32_bf16 v[148:151], a[240:243], a[216:219], v[148:151]\n" \
"s_add_u32 m0, s30, 0x9000\n" \
"s_mov_b32 s84, 0x9380\n" \
"v_mfma_f32_16x16x32_bf16 v[152:155], a[180:183], a[152:155], v[152:155]\n" \
"buffer_load_dwordx4 v24, s[76:79], s84 offen offset:0 lds\n" \
"v_mfma_f32_16x16x32_bf16 v[152:155], a[244:247], a[216:219], v[152:155]\n" \
"s_addk_i32 s84, 0x2000\n" \
"v_mfma_f32_16x16x32_bf16 v[156:159], a[184:187], a[152:155], v[156:159]\n" \
"buffer_load_dwordx4 v25, s[76:79], s84 offen offset:1024 lds\n" \
"v_mfma_f32_16x16x32_bf16 v[156:159], a[248:251], a[216:219], v[156:159]\n" \
"ds_read_b64_tr_b16 a[234:235], v34 offset:4608\n" \
"s_add_i32 s5, s13, s5\n" \
"v_mfma_f32_16x16x32_bf16 v[160:163], a[188:191], a[152:155], v[160:163]\n" \
"ds_read_b128 v[248:251], v22 offset:0\n" \
"s_and_b32 s4, s4, 0x70000\n" \
"v_mfma_f32_16x16x32_bf16 v[160:163], a[252:255], a[216:219], v[160:163]\n" \
"ds_read_b128 a[152:155], v28 offset:12288\n" \
"s_or_b32 s5, s5, 0x2800\n" \
"v_mfma_f32_16x16x32_bf16 v[176:179], a[188:191], a[156:159], v[176:179]\n" \
"ds_read_b128 a[216:219], v29 offset:12288\n" \
"v_writelane_b32 v11, s5, 51\n" \
"v_mfma_f32_16x16x32_bf16 v[176:179], a[252:255], a[220:223], v[176:179]\n" \
"ds_read_b128 v[48:51], v23 offset:0\n" \
"s_add_i32 s13, s13, s4\n" \
"v_mfma_f32_16x16x32_bf16 v[172:175], a[184:187], a[156:159], v[172:175]\n" \
"s_or_b32 s4, s13, 0x3000\n" \
"v_writelane_b32 v11, s4, 52\n" \
"v_mfma_f32_16x16x32_bf16 v[172:175], a[248:251], a[220:223], v[172:175]\n" \
"s_movk_i32 s11, 0x4000\n" \
"s_lshl_b32 s4, s2, 2\n" \
"v_mfma_f32_16x16x32_bf16 v[168:171], a[180:183], a[156:159], v[168:171]\n" \
"s_and_b32 s4, s4, 28\n" \
"s_bfe_u32 s5, s2, 0x20003\n" \
"v_mfma_f32_16x16x32_bf16 v[168:171], a[244:247], a[220:223], v[168:171]\n" \
"s_or_b32 s4, s4, s5\n" \
"s_lshr_b32 s5, s2, 5\n" \
"v_mfma_f32_16x16x32_bf16 v[164:167], a[176:179], a[156:159], v[164:167]\n" \
"s_lshr_b32 s19, s3, 1\n" \
"s_lshl_b32 s18, s5, 1\n" \
"v_mfma_f32_16x16x32_bf16 v[164:167], a[240:243], a[220:223], v[164:167]\n" \
"ds_read_b128 a[156:159], v28 offset:14336\n" \
"ds_read_b128 a[220:223], v29 offset:14336\n" \
"s_waitcnt lgkmcnt(15)\n" \
"v_mfma_f32_16x16x32_bf16 a[0:3], a[160:163], a[128:131], a[0:3]\n" \
"s_addk_i32 s84, 0x2000\n" \
"v_mfma_f32_16x16x32_bf16 a[0:3], a[224:227], a[192:195], a[0:3]\n" \
"buffer_load_dwordx4 v24, s[76:79], s84 offen offset:2048 lds\n" \
"v_mfma_f32_16x16x32_bf16 a[4:7], a[164:167], a[128:131], a[4:7]\n" \
"ds_read_b64_tr_b16 a[176:177], v36 offset:0\n" \
"s_lshl_b32 s6, s4, 20\n" \
"v_mfma_f32_16x16x32_bf16 a[4:7], a[228:231], a[192:195], a[4:7]\n" \
"s_addk_i32 s84, 0x2000\n" \
"v_mfma_f32_16x16x32_bf16 a[12:15], a[172:175], a[128:131], a[12:15]\n" \
"buffer_load_dwordx4 v25, s[76:79], s84 offen offset:3072 lds\n" \
"s_waitcnt lgkmcnt(15)\n" \
"v_mfma_f32_16x16x32_bf16 a[12:15], a[236:239], a[192:195], a[12:15]\n" \
"ds_read_b64_tr_b16 a[178:179], v36 offset:1024\n" \
"s_lshl_b32 s7, s4, 8\n" \
"s_waitcnt lgkmcnt(12)\n" \
"v_mfma_f32_16x16x32_bf16 a[8:11], a[128:131], a[168:171], a[8:11]\n" \
"ds_read_b64_tr_b16 a[240:241], v36 offset:8192\n" \
"s_and_b32 s13, s3, 1\n" \
"s_waitcnt lgkmcnt(9)\n" \
"v_mfma_f32_16x16x32_bf16 a[8:11], a[192:195], a[232:235], a[8:11]\n" \
"ds_read_b64_tr_b16 a[242:243], v36 offset:9216\n" \
"s_add_i32 s14, s18, s19\n" \
"v_mfma_f32_16x16x32_bf16 a[24:27], a[132:135], a[168:171], a[24:27]\n" \
"ds_read_b64_tr_b16 a[180:181], v37 offset:0\n" \
"v_mfma_f32_16x16x32_bf16 a[24:27], a[196:199], a[232:235], a[24:27]\n" \
"ds_read_b64_tr_b16 a[182:183], v37 offset:1024\n" \
"v_mfma_f32_16x16x32_bf16 a[28:31], a[172:175], a[132:135], a[28:31]\n" \
"ds_read_b64_tr_b16 a[244:245], v37 offset:8192\n" \
"v_mfma_f32_16x16x32_bf16 a[28:31], a[236:239], a[196:199], a[28:31]\n" \
"ds_read_b64_tr_b16 a[246:247], v37 offset:9216\n" \
"v_mfma_f32_16x16x32_bf16 a[20:23], a[164:167], a[132:135], a[20:23]\n" \
"s_add_u32 s6, s26, s6\n" \
"s_addc_u32 s9, s27, 0\n" \
"v_mfma_f32_16x16x32_bf16 a[20:23], a[228:231], a[196:199], a[20:23]\n" \
"s_add_u32 s89, s42, 0x0\n" \
"s_nop 0\n" \
"buffer_store_dwordx4 v[184:187], v20, s[80:83], s89 offen offset:1024 sc1\n" \
"v_mfma_f32_16x16x32_bf16 a[16:19], a[160:163], a[132:135], a[16:19]\n" \
"ds_read_b64_tr_b16 a[184:185], v38 offset:0\n" \
"v_mfma_f32_16x16x32_bf16 a[16:19], a[224:227], a[196:199], a[16:19]\n" \
"ds_read_b64_tr_b16 a[186:187], v38 offset:1024\n" \
"v_mfma_f32_16x16x32_bf16 a[32:35], a[160:163], a[136:139], a[32:35]\n" \
"ds_read_b64_tr_b16 a[248:249], v38 offset:8192\n" \
"v_mfma_f32_16x16x32_bf16 a[32:35], a[224:227], a[200:203], a[32:35]\n" \
"ds_read_b64_tr_b16 a[250:251], v38 offset:9216\n" \
"v_mfma_f32_16x16x32_bf16 a[36:39], a[164:167], a[136:139], a[36:39]\n" \
"s_add_u32 s8, s6, 0x1000000\n" \
"s_addc_u32 s6, s9, 0\n" \
"v_mfma_f32_16x16x32_bf16 a[36:39], a[228:231], a[200:203], a[36:39]\n" \
"s_add_u32 s88, s42, 0x1000\n" \
"buffer_store_dwordx4 v[188:191], v20, s[80:83], s88 offen offset:1024 sc1\n" \
"v_mfma_f32_16x16x32_bf16 a[44:47], a[172:175], a[136:139], a[44:47]\n" \
"ds_read_b64_tr_b16 a[188:189], v39 offset:0\n" \
"s_and_b32 s9, s6, 0xffff\n" \
"v_mfma_f32_16x16x32_bf16 a[44:47], a[236:239], a[200:203], a[44:47]\n" \
"ds_read_b64_tr_b16 a[190:191], v39 offset:1024\n" \
"s_lshl_b32 s6, s5, 16\n" \
"v_mfma_f32_16x16x32_bf16 a[40:43], a[136:139], a[168:171], a[40:43]\n" \
"ds_read_b64_tr_b16 a[252:253], v39 offset:8192\n" \
"s_sub_i32 s10, 0x100000, s6\n" \
"v_mfma_f32_16x16x32_bf16 a[40:43], a[200:203], a[232:235], a[40:43]\n" \
"s_mov_b32 s6, 0x8000\n" \
"v_mfma_f32_16x16x32_bf16 a[56:59], a[140:143], a[168:171], a[56:59]\n" \
"ds_read_b64_tr_b16 a[254:255], v39 offset:9216\n" \
"v_mfma_f32_16x16x32_bf16 a[56:59], a[204:207], a[232:235], a[56:59]\n" \
"s_cmp_lg_u32 s5, 0\n" \
"s_cselect_b32 s17, s11, s6\n" \
"v_mfma_f32_16x16x32_bf16 a[60:63], a[172:175], a[140:143], a[60:63]\n" \
"s_add_u32 s89, s42, 0x2000\n" \
"buffer_store_dwordx4 v[192:195], v20, s[80:83], s89 offen offset:0 sc1\n" \
"v_mfma_f32_16x16x32_bf16 a[60:63], a[236:239], a[204:207], a[60:63]\n" \
"s_lshl_b32 s6, s3, 1\n" \
"s_lshl_b32 s5, s5, 3\n" \
"v_mfma_f32_16x16x32_bf16 a[52:55], a[164:167], a[140:143], a[52:55]\n" \
"s_and_b32 s6, s6, 4\n" \
"s_or_b32 s12, s5, s6\n" \
"v_mfma_f32_16x16x32_bf16 a[52:55], a[228:231], a[204:207], a[52:55]\n" \
"s_sub_i32 s5, s13, s5\n" \
"s_or_b32 s5, s5, s6\n" \
"v_mfma_f32_16x16x32_bf16 a[48:51], a[160:163], a[140:143], a[48:51]\n" \
"s_or_b32 s6, s12, s13\n" \
"s_lshl_b32 s4, s4, 2\n" \
"v_mfma_f32_16x16x32_bf16 a[48:51], a[224:227], a[204:207], a[48:51]\n" \
"s_mulk_i32 s6, 0x92\n" \
"s_add_i32 s4, s4, 18\n" \
"s_waitcnt lgkmcnt(15)\n" \
"v_mfma_f32_16x16x32_bf16 a[64:67], a[164:167], v[248:251], a[64:67]\n" \
"s_mulk_i32 s5, 0x92\n" \
"s_add_i32 s6, s4, s6\n" \
"v_mfma_f32_16x16x32_bf16 a[64:67], a[228:231], v[48:51], a[64:67]\n" \
"s_add_i32 s5, s5, s4\n" \
"s_lshl_b32 s4, s3, 11\n" \
"v_mfma_f32_16x16x32_bf16 a[80:83], a[172:175], v[248:251], a[80:83]\n" \
"s_lshl_b32 s15, s13, 12\n" \
"s_lshl_b32 s16, s19, 14\n" \
"v_mfma_f32_16x16x32_bf16 a[80:83], a[236:239], v[48:51], a[80:83]\n" \
"s_mul_i32 s19, s19, 0xe000\n" \
"s_mov_b32 s11, 0x20000\n" \
"v_mfma_f32_16x16x32_bf16 a[68:71], v[248:251], a[168:171], a[68:71]\n" \
"s_or_b32 s12, s4, 0xc000\n" \
"s_lshl_b32 s4, s3, 8\n" \
"v_mfma_f32_16x16x32_bf16 a[68:71], v[48:51], a[232:235], a[68:71]\n" \
"s_waitcnt vmcnt(3) lgkmcnt(0)\n" \
"s_barrier\n" \
"v_mfma_f32_16x16x32_bf16 v[52:55], a[176:179], a[128:131], v[52:55]\n" \
"s_add_u32 m0, s40, 0x0\n" \
"s_add_u32 s85, s39, 0x10000\n" \
"v_mfma_f32_16x16x32_bf16 v[52:55], a[240:243], a[192:195], v[52:55]\n" \
"buffer_load_dwordx4 v20, s[80:83], s85 offen sc1 lds\n" \
"v_mfma_f32_16x16x32_bf16 v[56:59], a[180:183], a[128:131], v[56:59]\n" \
"ds_read_b64_tr_b16 a[160:161], v32 offset:32768\n" \
"s_or_b32 s22, s15, s16\n" \
"v_mfma_f32_16x16x32_bf16 v[56:59], a[244:247], a[192:195], v[56:59]\n" \
"ds_read_b64_tr_b16 a[162:163], v32 offset:33280\n" \
"s_or_b32 s23, s19, s15\n" \
"v_mfma_f32_16x16x32_bf16 v[60:63], a[184:187], a[128:131], v[60:63]\n" \
"s_add_u32 m0, s40, 0x1000\n" \
"s_add_u32 s85, s39, 0x11000\n" \
"v_mfma_f32_16x16x32_bf16 v[60:63], a[248:251], a[192:195], v[60:63]\n" \
"buffer_load_dwordx4 v20, s[80:83], s85 offen sc1 lds\n" \
"v_mfma_f32_16x16x32_bf16 v[64:67], a[188:191], a[128:131], v[64:67]\n" \
"ds_read_b64_tr_b16 a[224:225], v32 offset:36864\n" \
"s_lshl_b32 s60, s6, 10\n" \
"v_mfma_f32_16x16x32_bf16 v[64:67], a[252:255], a[192:195], v[64:67]\n" \
"ds_read_b128 a[128:131], v28 offset:32768\n" \
"s_lshl_b32 s61, s5, 10\n" \
"v_mfma_f32_16x16x32_bf16 v[80:83], a[188:191], a[132:135], v[80:83]\n" \
"ds_read_b128 a[192:195], v29 offset:32768\n" \
"s_lshl_b32 s14, s14, 15\n" \
"v_mfma_f32_16x16x32_bf16 v[80:83], a[252:255], a[196:199], v[80:83]\n" \
"s_add_u32 m0, s40, 0x3000\n" \
"s_movk_i32 s84, 0x400\n" \
"v_mfma_f32_16x16x32_bf16 v[76:79], a[184:187], a[132:135], v[76:79]\n" \
"buffer_load_dwordx4 v21, s[76:79], s84 offen offset:0 lds\n" \
"v_mfma_f32_16x16x32_bf16 v[76:79], a[248:251], a[196:199], v[76:79]\n" \
"ds_read_b64_tr_b16 a[226:227], v32 offset:37376\n" \
"s_or_b32 s20, s4, 0x26c00\n" \
"v_mfma_f32_16x16x32_bf16 v[72:75], a[180:183], a[132:135], v[72:75]\n" \
"s_add_u32 m0, s30, 0x0\n" \
"s_movk_i32 s84, 0x400\n" \
"v_mfma_f32_16x16x32_bf16 v[72:75], a[244:247], a[196:199], v[72:75]\n" \
"buffer_load_dwordx4 v24, s[76:79], s84 offen offset:0 lds\n" \
"v_mfma_f32_16x16x32_bf16 v[68:71], a[176:179], a[132:135], v[68:71]\n" \
"ds_read_b64_tr_b16 a[164:165], v33 offset:32768\n" \
"s_or_b32 s13, s22, 0x8000\n" \
"v_mfma_f32_16x16x32_bf16 v[68:71], a[240:243], a[196:199], v[68:71]\n" \
"ds_read_b128 a[132:135], v28 offset:34816\n" \
"s_add_i32 s22, s22, 0x14000\n" \
"v_mfma_f32_16x16x32_bf16 v[84:87], a[176:179], a[136:139], v[84:87]\n" \
"ds_read_b128 a[196:199], v29 offset:34816\n" \
"s_bitset1_b32 s23, 16\n" \
"v_mfma_f32_16x16x32_bf16 v[84:87], a[240:243], a[200:203], v[84:87]\n" \
"s_movk_i32 s84, 0x2400\n" \
"v_mfma_f32_16x16x32_bf16 v[88:91], a[180:183], a[136:139], v[88:91]\n" \
"buffer_load_dwordx4 v25, s[76:79], s84 offen offset:1024 lds\n" \
"v_mfma_f32_16x16x32_bf16 v[88:91], a[244:247], a[200:203], v[88:91]\n" \
"ds_read_b64_tr_b16 a[166:167], v33 offset:33280\n" \
"s_or_b32 s24, s7, 0x5be8020\n" \
".fill (L_pb + 32480 - .) / 4, 4, 0xBF800000\n" \
"L_pp8:\n" \
"s_cbranch_execz L_pret\n" \
"v_mfma_f32_16x16x32_bf16 v[92:95], a[184:187], a[136:139], v[92:95]\n" \
"ds_read_b64_tr_b16 a[228:229], v33 offset:36864\n" \
"s_and_b32 s57, s27, 0xffff\n" \
"v_mfma_f32_16x16x32_bf16 v[92:95], a[248:251], a[200:203], v[92:95]\n" \
"s_movk_i32 s84, 0x4400\n" \
"v_mfma_f32_16x16x32_bf16 v[96:99], a[188:191], a[136:139], v[96:99]\n" \
"buffer_load_dwordx4 v24, s[76:79], s84 offen offset:2048 lds\n" \
"v_mfma_f32_16x16x32_bf16 v[96:99], a[252:255], a[200:203], v[96:99]\n" \
"ds_read_b128 a[136:139], v28 offset:36864\n" \
"s_mov_b32 s58, 0x6000000\n" \
"v_mfma_f32_16x16x32_bf16 v[112:115], a[188:191], a[140:143], v[112:115]\n" \
"ds_read_b128 a[200:203], v29 offset:36864\n" \
"s_mov_b32 s56, s26\n" \
"v_mfma_f32_16x16x32_bf16 v[112:115], a[252:255], a[204:207], v[112:115]\n" \
"s_movk_i32 s84, 0x6400\n" \
"v_mfma_f32_16x16x32_bf16 v[108:111], a[184:187], a[140:143], v[108:111]\n" \
"buffer_load_dwordx4 v25, s[76:79], s84 offen offset:3072 lds\n" \
"v_mfma_f32_16x16x32_bf16 v[108:111], a[248:251], a[204:207], v[108:111]\n" \
"ds_read_b64_tr_b16 a[230:231], v33 offset:37376\n" \
"s_mov_b32 s59, s11\n" \
"v_mfma_f32_16x16x32_bf16 v[104:107], a[180:183], a[140:143], v[104:107]\n" \
"ds_read_b64_tr_b16 a[172:173], v35 offset:32768\n" \
"s_add_i32 s60, s60, 0x3000000\n" \
"v_mfma_f32_16x16x32_bf16 v[104:107], a[244:247], a[204:207], v[104:107]\n" \
"ds_read_b64_tr_b16 a[174:175], v35 offset:33280\n" \
"s_add_i32 s61, s61, 0x411c000\n" \
"v_mfma_f32_16x16x32_bf16 v[100:103], a[176:179], a[140:143], v[100:103]\n" \
"ds_read_b64_tr_b16 a[236:237], v35 offset:36864\n" \
"s_sub_i32 s19, 14, s18\n" \
"v_mfma_f32_16x16x32_bf16 v[100:103], a[240:243], a[204:207], v[100:103]\n" \
"ds_read_b128 a[140:143], v28 offset:38912\n" \
"s_mov_b32 s21, 0x71800000\n" \
"v_mfma_f32_16x16x32_bf16 v[116:119], a[176:179], a[144:147], v[116:119]\n" \
"ds_read_b128 a[204:207], v29 offset:38912\n" \
"s_mov_b32 s4, L_gsw-L_gtr\n" \
"v_mfma_f32_16x16x32_bf16 v[116:119], a[240:243], a[208:211], v[116:119]\n" \
"s_add_u32 m0, s31, 0x0\n" \
"s_mov_b32 s84, 0x400000\n" \
"v_mfma_f32_16x16x32_bf16 v[120:123], a[180:183], a[144:147], v[120:123]\n" \
"buffer_load_dwordx4 v26, s[68:71], s84 offen offset:0 lds\n" \
"v_mfma_f32_16x16x32_bf16 v[120:123], a[244:247], a[208:211], v[120:123]\n" \
"s_addk_i32 s84, 0x7c00\n" \
"v_mfma_f32_16x16x32_bf16 v[124:127], a[184:187], a[144:147], v[124:127]\n" \
"buffer_load_dwordx4 v26, s[68:71], s84 offen offset:1024 lds\n" \
"v_mfma_f32_16x16x32_bf16 v[124:127], a[248:251], a[208:211], v[124:127]\n" \
"ds_read_b64_tr_b16 a[238:239], v35 offset:37376\n" \
"v_mfma_f32_16x16x32_bf16 v[128:131], a[188:191], a[144:147], v[128:131]\n" \
"ds_read_b64_tr_b16 a[168:169], v34 offset:32768\n" \
"v_mfma_f32_16x16x32_bf16 v[128:131], a[252:255], a[208:211], v[128:131]\n" \
"ds_read_b128 a[144:147], v28 offset:40960\n" \
"v_mfma_f32_16x16x32_bf16 v[144:147], a[188:191], a[148:151], v[144:147]\n" \
"ds_read_b128 a[208:211], v29 offset:40960\n" \
"v_mfma_f32_16x16x32_bf16 v[144:147], a[252:255], a[212:215], v[144:147]\n" \
"s_addk_i32 s84, 0x7c00\n" \
"v_mfma_f32_16x16x32_bf16 v[140:143], a[184:187], a[148:151], v[140:143]\n" \
"buffer_load_dwordx4 v27, s[68:71], s84 offen offset:2048 lds\n" \
"v_mfma_f32_16x16x32_bf16 v[140:143], a[248:251], a[212:215], v[140:143]\n" \
"s_addk_i32 s84, 0x7c00\n" \
"v_mfma_f32_16x16x32_bf16 v[136:139], a[180:183], a[148:151], v[136:139]\n" \
"buffer_load_dwordx4 v27, s[68:71], s84 offen offset:3072 lds\n" \
"v_mfma_f32_16x16x32_bf16 v[136:139], a[244:247], a[212:215], v[136:139]\n" \
"ds_read_b64_tr_b16 a[170:171], v34 offset:33280\n" \
"v_mfma_f32_16x16x32_bf16 v[132:135], a[176:179], a[148:151], v[132:135]\n" \
"ds_read_b64_tr_b16 a[232:233], v34 offset:36864\n" \
"v_mfma_f32_16x16x32_bf16 v[132:135], a[240:243], a[212:215], v[132:135]\n" \
"ds_read_b128 a[148:151], v28 offset:43008\n" \
"v_mfma_f32_16x16x32_bf16 v[148:151], a[176:179], a[152:155], v[148:151]\n" \
"ds_read_b128 a[212:215], v29 offset:43008\n" \
"v_mfma_f32_16x16x32_bf16 v[148:151], a[240:243], a[216:219], v[148:151]\n" \
"s_add_u32 m0, s30, 0x1000\n" \
"s_mov_b32 s84, 0x9400\n" \
"v_mfma_f32_16x16x32_bf16 v[152:155], a[180:183], a[152:155], v[152:155]\n" \
"buffer_load_dwordx4 v24, s[76:79], s84 offen offset:0 lds\n" \
"v_mfma_f32_16x16x32_bf16 v[152:155], a[244:247], a[216:219], v[152:155]\n" \
"s_addk_i32 s84, 0x2000\n" \
"s_nop 0\n" \
"v_mfma_f32_16x16x32_bf16 v[156:159], a[184:187], a[152:155], v[156:159]\n" \
"buffer_load_dwordx4 v25, s[76:79], s84 offen offset:1024 lds\n" \
"v_mfma_f32_16x16x32_bf16 v[156:159], a[248:251], a[216:219], v[156:159]\n" \
"ds_read_b64_tr_b16 a[234:235], v34 offset:37376\n" \
"v_mfma_f32_16x16x32_bf16 v[160:163], a[188:191], a[152:155], v[160:163]\n" \
"ds_read_b128 v[248:251], v22 offset:32768\n" \
"v_mfma_f32_16x16x32_bf16 v[160:163], a[252:255], a[216:219], v[160:163]\n" \
"ds_read_b128 a[152:155], v28 offset:45056\n" \
"v_mfma_f32_16x16x32_bf16 v[176:179], a[188:191], a[156:159], v[176:179]\n" \
"ds_read_b128 a[216:219], v29 offset:45056\n" \
"v_mfma_f32_16x16x32_bf16 v[176:179], a[252:255], a[220:223], v[176:179]\n" \
"ds_read_b128 v[48:51], v23 offset:32768\n" \
"v_mfma_f32_16x16x32_bf16 v[172:175], a[184:187], a[156:159], v[172:175]\n" \
"s_cmp_eq_u32 s19, 0\n" \
"s_cselect_b32 s4, L_gs7-L_gtr, s4\n" \
"v_mfma_f32_16x16x32_bf16 v[172:175], a[248:251], a[220:223], v[172:175]\n" \
"s_cmp_eq_u32 s16, 0\n" \
"s_cselect_b32 s4, 0, s4\n" \
"v_mfma_f32_16x16x32_bf16 v[168:171], a[180:183], a[156:159], v[168:171]\n" \
"s_getpc_b64 s[28:29]\n" \
"L_gp0_%=:\n" \
"s_add_u32 s28, s28, L_gtr-L_gp0_%=\n" \
"s_addc_u32 s29, s29, 0\n" \
"v_mfma_f32_16x16x32_bf16 v[168:171], a[244:247], a[220:223], v[168:171]\n" \
"s_add_u32 s28, s28, s4\n" \
"s_addc_u32 s29, s29, 0\n" \
"v_mfma_f32_16x16x32_bf16 v[164:167], a[176:179], a[156:159], v[164:167]\n" \
"v_mfma_f32_16x16x32_bf16 v[164:167], a[240:243], a[220:223], v[164:167]\n" \
"ds_read_b128 a[156:159], v28 offset:47104\n" \
"ds_read_b128 a[220:223], v29 offset:47104\n" \
"s_waitcnt lgkmcnt(15)\n" \
"v_mfma_f32_16x16x32_bf16 a[0:3], a[160:163], a[128:131], a[0:3]\n" \
"s_add_u32 m0, s30, 0x1000\n" \
"s_mov_b32 s84, 0xd400\n" \
"v_mfma_f32_16x16x32_bf16 a[0:3], a[224:227], a[192:195], a[0:3]\n" \
"buffer_load_dwordx4 v24, s[76:79], s84 offen offset:2048 lds\n" \
"v_mfma_f32_16x16x32_bf16 a[4:7], a[164:167], a[128:131], a[4:7]\n" \
"ds_read_b64_tr_b16 a[176:177], v36 offset:32768\n" \
"v_mfma_f32_16x16x32_bf16 a[4:7], a[228:231], a[192:195], a[4:7]\n" \
"s_addk_i32 s84, 0x2000\n" \
"v_mfma_f32_16x16x32_bf16 a[12:15], a[172:175], a[128:131], a[12:15]\n" \
"buffer_load_dwordx4 v25, s[76:79], s84 offen offset:3072 lds\n" \
"s_waitcnt lgkmcnt(15)\n" \
"v_mfma_f32_16x16x32_bf16 a[12:15], a[236:239], a[192:195], a[12:15]\n" \
"ds_read_b64_tr_b16 a[178:179], v36 offset:33792\n" \
"s_waitcnt lgkmcnt(12)\n" \
"v_mfma_f32_16x16x32_bf16 a[8:11], a[128:131], a[168:171], a[8:11]\n" \
"ds_read_b64_tr_b16 a[240:241], v36 offset:40960\n" \
"s_waitcnt lgkmcnt(9)\n" \
"v_mfma_f32_16x16x32_bf16 a[8:11], a[192:195], a[232:235], a[8:11]\n" \
"ds_read_b64_tr_b16 a[242:243], v36 offset:41984\n" \
"v_mfma_f32_16x16x32_bf16 a[24:27], a[132:135], a[168:171], a[24:27]\n" \
"ds_read_b64_tr_b16 a[180:181], v37 offset:32768\n" \
"v_mfma_f32_16x16x32_bf16 a[24:27], a[196:199], a[232:235], a[24:27]\n" \
"ds_read_b64_tr_b16 a[182:183], v37 offset:33792\n" \
"v_mfma_f32_16x16x32_bf16 a[28:31], a[172:175], a[132:135], a[28:31]\n" \
"ds_read_b64_tr_b16 a[244:245], v37 offset:40960\n" \
"v_mfma_f32_16x16x32_bf16 a[28:31], a[236:239], a[196:199], a[28:31]\n" \
"ds_read_b64_tr_b16 a[246:247], v37 offset:41984\n" \
"v_mfma_f32_16x16x32_bf16 a[20:23], a[164:167], a[132:135], a[20:23]\n" \
"v_mfma_f32_16x16x32_bf16 a[20:23], a[228:231], a[196:199], a[20:23]\n" \
"s_add_u32 s88, s42, 0x2000\n" \
"buffer_store_dwordx4 v[196:199], v20, s[80:83], s88 offen offset:1024 sc1\n" \
"v_mfma_f32_16x16x32_bf16 a[16:19], a[160:163], a[132:135], a[16:19]\n" \
"ds_read_b64_tr_b16 a[184:185], v38 offset:32768\n" \
"v_mfma_f32_16x16x32_bf16 a[16:19], a[224:227], a[196:199], a[16:19]\n" \
"ds_read_b64_tr_b16 a[186:187], v38 offset:33792\n" \
"v_mfma_f32_16x16x32_bf16 a[32:35], a[160:163], a[136:139], a[32:35]\n" \
"ds_read_b64_tr_b16 a[248:249], v38 offset:40960\n" \
"v_mfma_f32_16x16x32_bf16 a[32:35], a[224:227], a[200:203], a[32:35]\n" \
"ds_read_b64_tr_b16 a[250:251], v38 offset:41984\n" \
"v_mfma_f32_16x16x32_bf16 a[36:39], a[164:167], a[136:139], a[36:39]\n" \
"v_lshlrev_b32_e32 v45, 3, v20\n" \
"v_add_u32_e32 v45, s30, v45\n" \
"v_mfma_f32_16x16x32_bf16 a[36:39], a[228:231], a[200:203], a[36:39]\n" \
"s_add_u32 s89, s42, 0x3000\n" \
"buffer_store_dwordx4 v[216:219], v20, s[80:83], s89 offen offset:0 sc1\n" \
"s_getpc_b64 s[86:87]\n" \
"L_ct0_%=:\n" \
"s_add_u32 s86, s86, L_ap0-L_ct0_%=\n" \
"s_addc_u32 s87, s87, 0\n" \
"s_add_u32 m0, s40, 0x2000\n" \
"v_mfma_f32_16x16x32_bf16 a[44:47], a[172:175], a[136:139], a[44:47]\n" \
"global_load_lds_dword v45, s[86:87]\n" \
"v_mfma_f32_16x16x32_bf16 a[44:47], a[236:239], a[200:203], a[44:47]\n" \
"ds_read_b64_tr_b16 a[188:189], v39 offset:32768\n" \
"v_mfma_f32_16x16x32_bf16 a[40:43], a[136:139], a[168:171], a[40:43]\n" \
"ds_read_b64_tr_b16 a[190:191], v39 offset:33792\n" \
"v_mfma_f32_16x16x32_bf16 a[40:43], a[200:203], a[232:235], a[40:43]\n" \
"ds_read_b64_tr_b16 a[252:253], v39 offset:40960\n" \
"v_mfma_f32_16x16x32_bf16 a[56:59], a[140:143], a[168:171], a[56:59]\n" \
"ds_read_b64_tr_b16 a[254:255], v39 offset:41984\n" \
"v_mfma_f32_16x16x32_bf16 a[56:59], a[204:207], a[232:235], a[56:59]\n" \
"v_mfma_f32_16x16x32_bf16 a[60:63], a[172:175], a[140:143], a[60:63]\n" \
"s_add_u32 s88, s42, 0x3000\n" \
"buffer_store_dwordx4 v[220:223], v20, s[80:83], s88 offen offset:1024 sc1\n" \
"v_mfma_f32_16x16x32_bf16 a[60:63], a[236:239], a[204:207], a[60:63]\n" \
"v_mfma_f32_16x16x32_bf16 a[52:55], a[164:167], a[140:143], a[52:55]\n" \
"v_mfma_f32_16x16x32_bf16 a[52:55], a[228:231], a[204:207], a[52:55]\n" \
"v_mfma_f32_16x16x32_bf16 a[48:51], a[160:163], a[140:143], a[48:51]\n" \
"v_mfma_f32_16x16x32_bf16 a[48:51], a[224:227], a[204:207], a[48:51]\n" \
"s_waitcnt lgkmcnt(15)\n" \
"v_mfma_f32_16x16x32_bf16 a[64:67], a[164:167], v[248:251], a[64:67]\n" \
"v_mfma_f32_16x16x32_bf16 a[64:67], a[228:231], v[48:51], a[64:67]\n" \
"v_mfma_f32_16x16x32_bf16 a[80:83], a[172:175], v[248:251], a[80:83]\n" \
"v_mfma_f32_16x16x32_bf16 a[80:83], a[236:239], v[48:51], a[80:83]\n" \
"v_mfma_f32_16x16x32_bf16 a[68:71], v[248:251], a[168:171], a[68:71]\n" \
"v_mfma_f32_16x16x32_bf16 a[68:71], v[48:51], a[232:235], a[68:71]\n" \
"s_waitcnt vmcnt(4) lgkmcnt(0)\n" \
"s_barrier\n" \
"s_nop 0\n" \
"v_mfma_f32_16x16x32_bf16 v[52:55], a[176:179], a[128:131], v[52:55]\n" \
"ds_read_b64_tr_b16 a[160:161], v32 offset:0\n" \
"v_accvgpr_read_b32 v184, a0\n" \
"v_mfma_f32_16x16x32_bf16 v[52:55], a[240:243], a[192:195], v[52:55]\n" \
"v_accvgpr_read_b32 v185, a1\n" \
"v_accvgpr_read_b32 v186, a2\n" \
"v_mfma_f32_16x16x32_bf16 v[56:59], a[180:183], a[128:131], v[56:59]\n" \
"ds_read_b64_tr_b16 a[162:163], v32 offset:512\n" \
"v_accvgpr_read_b32 v187, a3\n" \
"v_mfma_f32_16x16x32_bf16 v[56:59], a[244:247], a[192:195], v[56:59]\n" \
"v_accvgpr_read_b32 v188, a4\n" \
"v_accvgpr_read_b32 v189, a5\n" \
"v_mfma_f32_16x16x32_bf16 v[60:63], a[184:187], a[128:131], v[60:63]\n" \
"v_accvgpr_read_b32 v190, a6\n" \
"v_accvgpr_read_b32 v191, a7\n" \
"v_mfma_f32_16x16x32_bf16 v[60:63], a[248:251], a[192:195], v[60:63]\n" \
"ds_read_b64_tr_b16 a[224:225], v32 offset:4096\n" \
"v_accvgpr_read_b32 v196, a12\n" \
"v_mfma_f32_16x16x32_bf16 v[64:67], a[188:191], a[128:131], v[64:67]\n" \
"v_accvgpr_read_b32 v197, a13\n" \
"v_accvgpr_read_b32 v198, a14\n" \
"v_mfma_f32_16x16x32_bf16 v[64:67], a[252:255], a[192:195], v[64:67]\n" \
"ds_read_b128 a[128:131], v28 offset:0\n" \
"v_accvgpr_read_b32 v199, a15\n" \
"v_mfma_f32_16x16x32_bf16 v[80:83], a[188:191], a[132:135], v[80:83]\n" \
"ds_read_b128 a[192:195], v29 offset:0\n" \
"v_accvgpr_read_b32 v192, a8\n" \
"v_mfma_f32_16x16x32_bf16 v[80:83], a[252:255], a[196:199], v[80:83]\n" \
"ds_read_b64_tr_b16 a[226:227], v32 offset:4608\n" \
"v_accvgpr_read_b32 v193, a9\n" \
"v_mfma_f32_16x16x32_bf16 v[76:79], a[184:187], a[132:135], v[76:79]\n" \
"v_accvgpr_read_b32 v194, a10\n" \
"v_accvgpr_read_b32 v195, a11\n" \
"v_mfma_f32_16x16x32_bf16 v[76:79], a[248:251], a[196:199], v[76:79]\n" \
"ds_read_b64_tr_b16 a[164:165], v33 offset:0\n" \
"v_accvgpr_read_b32 v208, a24\n" \
"v_mfma_f32_16x16x32_bf16 v[72:75], a[180:183], a[132:135], v[72:75]\n" \
"v_accvgpr_read_b32 v209, a25\n" \
"v_accvgpr_read_b32 v210, a26\n" \
"v_mfma_f32_16x16x32_bf16 v[72:75], a[244:247], a[196:199], v[72:75]\n" \
"v_accvgpr_read_b32 v211, a27\n" \
"v_accvgpr_read_b32 v212, a28\n" \
"v_mfma_f32_16x16x32_bf16 v[68:71], a[176:179], a[132:135], v[68:71]\n" \
"ds_read_b64_tr_b16 a[166:167], v33 offset:512\n" \
"v_accvgpr_read_b32 v213, a29\n" \
"v_mfma_f32_16x16x32_bf16 v[68:71], a[240:243], a[196:199], v[68:71]\n" \
"ds_read_b128 a[132:135], v28 offset:2048\n" \
"v_accvgpr_read_b32 v214, a30\n" \
"v_mfma_f32_16x16x32_bf16 v[84:87], a[176:179], a[136:139], v[84:87]\n" \
"ds_read_b128 a[196:199], v29 offset:2048\n" \
"v_accvgpr_read_b32 v215, a31\n" \
"v_mfma_f32_16x16x32_bf16 v[84:87], a[240:243], a[200:203], v[84:87]\n" \
"ds_read_b64_tr_b16 a[228:229], v33 offset:4096\n" \
"v_accvgpr_read_b32 v204, a20\n" \
"v_mfma_f32_16x16x32_bf16 v[88:91], a[180:183], a[136:139], v[88:91]\n" \
"v_accvgpr_read_b32 v205, a21\n" \
"v_accvgpr_read_b32 v206, a22\n" \
"v_mfma_f32_16x16x32_bf16 v[88:91], a[244:247], a[200:203], v[88:91]\n" \
"v_accvgpr_read_b32 v207, a23\n" \
"v_accvgpr_read_b32 v200, a16\n" \
"v_mfma_f32_16x16x32_bf16 v[92:95], a[184:187], a[136:139], v[92:95]\n" \
"ds_read_b64_tr_b16 a[230:231], v33 offset:4608\n" \
"v_accvgpr_read_b32 v201, a17\n" \
"v_mfma_f32_16x16x32_bf16 v[92:95], a[248:251], a[200:203], v[92:95]\n" \
"v_accvgpr_read_b32 v202, a18\n" \
"v_accvgpr_read_b32 v203, a19\n" \
"v_mfma_f32_16x16x32_bf16 v[96:99], a[188:191], a[136:139], v[96:99]\n" \
"v_accvgpr_read_b32 v216, a32\n" \
"v_accvgpr_read_b32 v217, a33\n" \
"v_mfma_f32_16x16x32_bf16 v[96:99], a[252:255], a[200:203], v[96:99]\n" \
"ds_read_b128 a[136:139], v28 offset:4096\n" \
"v_accvgpr_read_b32 v218, a34\n" \
"v_mfma_f32_16x16x32_bf16 v[112:115], a[188:191], a[140:143], v[112:115]\n" \
"ds_read_b128 a[200:203], v29 offset:4096\n" \
"v_accvgpr_read_b32 v219, a35\n" \
"v_mfma_f32_16x16x32_bf16 v[112:115], a[252:255], a[204:207], v[112:115]\n" \
"ds_read_b64_tr_b16 a[172:173], v35 offset:0\n" \
"v_accvgpr_read_b32 v220, a36\n" \
"v_mfma_f32_16x16x32_bf16 v[108:111], a[184:187], a[140:143], v[108:111]\n" \
"ds_read_b64_tr_b16 a[174:175], v35 offset:512\n" \
"v_accvgpr_read_b32 v221, a37\n" \
"v_mfma_f32_16x16x32_bf16 v[108:111], a[248:251], a[204:207], v[108:111]\n" \
"v_accvgpr_read_b32 v222, a38\n" \
"v_accvgpr_read_b32 v223, a39\n" \
"v_mfma_f32_16x16x32_bf16 v[104:107], a[180:183], a[140:143], v[104:107]\n" \
"ds_read_b64_tr_b16 a[236:237], v35 offset:4096\n" \
"v_accvgpr_read_b32 v228, a44\n" \
"v_mfma_f32_16x16x32_bf16 v[104:107], a[244:247], a[204:207], v[104:107]\n" \
"v_accvgpr_read_b32 v229, a45\n" \
"v_accvgpr_read_b32 v230, a46\n" \
"v_mfma_f32_16x16x32_bf16 v[100:103], a[176:179], a[140:143], v[100:103]\n" \
"v_accvgpr_read_b32 v231, a47\n" \
"v_accvgpr_read_b32 v224, a40\n" \
"v_mfma_f32_16x16x32_bf16 v[100:103], a[240:243], a[204:207], v[100:103]\n" \
"ds_read_b128 a[140:143], v28 offset:6144\n" \
"v_accvgpr_read_b32 v225, a41\n" \
"v_mfma_f32_16x16x32_bf16 v[116:119], a[176:179], a[144:147], v[116:119]\n" \
"ds_read_b128 a[204:207], v29 offset:6144\n" \
"v_accvgpr_read_b32 v226, a42\n" \
"v_mfma_f32_16x16x32_bf16 v[116:119], a[240:243], a[208:211], v[116:119]\n" \
"ds_read_b64_tr_b16 a[238:239], v35 offset:4608\n" \
"v_accvgpr_read_b32 v227, a43\n" \
"v_mfma_f32_16x16x32_bf16 v[120:123], a[180:183], a[144:147], v[120:123]\n" \
"ds_read_b64_tr_b16 a[168:169], v34 offset:0\n" \
"v_accvgpr_read_b32 v240, a56\n" \
"v_mfma_f32_16x16x32_bf16 v[120:123], a[244:247], a[208:211], v[120:123]\n" \
"v_accvgpr_read_b32 v241, a57\n" \
"v_accvgpr_read_b32 v242, a58\n" \
"v_mfma_f32_16x16x32_bf16 v[124:127], a[184:187], a[144:147], v[124:127]\n" \
"v_accvgpr_read_b32 v243, a59\n" \
"v_accvgpr_read_b32 v244, a60\n" \
"v_mfma_f32_16x16x32_bf16 v[124:127], a[248:251], a[208:211], v[124:127]\n" \
"ds_read_b64_tr_b16 a[170:171], v34 offset:512\n" \
"v_accvgpr_read_b32 v245, a61\n" \
"v_mfma_f32_16x16x32_bf16 v[128:131], a[188:191], a[144:147], v[128:131]\n" \
"v_accvgpr_read_b32 v246, a62\n" \
"v_accvgpr_read_b32 v247, a63\n" \
"v_mfma_f32_16x16x32_bf16 v[128:131], a[252:255], a[208:211], v[128:131]\n" \
"ds_read_b128 a[144:147], v28 offset:8192\n" \
"v_accvgpr_read_b32 v236, a52\n" \
"v_mfma_f32_16x16x32_bf16 v[144:147], a[188:191], a[148:151], v[144:147]\n" \
"ds_read_b128 a[208:211], v29 offset:8192\n" \
"v_accvgpr_read_b32 v237, a53\n" \
"v_mfma_f32_16x16x32_bf16 v[144:147], a[252:255], a[212:215], v[144:147]\n" \
"ds_read_b64_tr_b16 a[232:233], v34 offset:4096\n" \
"v_accvgpr_read_b32 v238, a54\n" \
"v_mfma_f32_16x16x32_bf16 v[140:143], a[184:187], a[148:151], v[140:143]\n" \
"v_accvgpr_read_b32 v239, a55\n" \
"v_accvgpr_read_b32 v232, a48\n" \
"v_mfma_f32_16x16x32_bf16 v[140:143], a[248:251], a[212:215], v[140:143]\n" \
"ds_read_b64_tr_b16 a[234:235], v34 offset:4608\n" \
"v_accvgpr_read_b32 v233, a49\n" \
"v_mfma_f32_16x16x32_bf16 v[136:139], a[180:183], a[148:151], v[136:139]\n" \
"v_accvgpr_read_b32 v234, a50\n" \
"v_accvgpr_read_b32 v235, a51\n" \
"v_mfma_f32_16x16x32_bf16 v[136:139], a[244:247], a[212:215], v[136:139]\n" \
"v_accvgpr_read_b32 v180, a64\n" \
"v_accvgpr_read_b32 v181, a65\n" \
"v_mfma_f32_16x16x32_bf16 v[132:135], a[176:179], a[148:151], v[132:135]\n" \
"ds_read_b128 v[248:251], v22 offset:0\n" \
"v_accvgpr_read_b32 v182, a66\n" \
"v_mfma_f32_16x16x32_bf16 v[132:135], a[240:243], a[212:215], v[132:135]\n" \
"ds_read_b128 a[148:151], v28 offset:10240\n" \
"v_accvgpr_read_b32 v183, a67\n" \
"v_mfma_f32_16x16x32_bf16 v[148:151], a[176:179], a[152:155], v[148:151]\n" \
"ds_read_b128 a[212:215], v29 offset:10240\n" \
"v_accvgpr_read_b32 v252, a68\n" \
"v_mfma_f32_16x16x32_bf16 v[148:151], a[240:243], a[216:219], v[148:151]\n" \
"ds_read_b128 v[48:51], v23 offset:0\n" \
"v_accvgpr_read_b32 v253, a69\n" \
"v_mfma_f32_16x16x32_bf16 v[152:155], a[180:183], a[152:155], v[152:155]\n" \
"v_accvgpr_read_b32 v254, a70\n" \
"v_accvgpr_read_b32 v255, a71\n" \
"v_mfma_f32_16x16x32_bf16 v[152:155], a[244:247], a[216:219], v[152:155]\n" \
"v_lshrrev_b32_e64 v24, 9, v20\n" \
"v_and_b32_e32 v26, 0x1ff, v20\n" \
"v_mfma_f32_16x16x32_bf16 v[156:159], a[184:187], a[152:155], v[156:159]\n" \
"v_lshl_add_u32 v24, v24, 3, v26\n" \
"v_lshrrev_b32_e64 v25, 8, v20\n" \
"v_mfma_f32_16x16x32_bf16 v[156:159], a[248:251], a[216:219], v[156:159]\n" \
"v_mul_u32_u24_e32 v25, 0x24700, v25\n" \
"v_add_u32_e64 v25, v25, v20\n" \
"v_mfma_f32_16x16x32_bf16 v[160:163], a[188:191], a[152:155], v[160:163]\n" \
"v_lshrrev_b32_e32 v26, 2, v20\n" \
"v_add_u32_e32 v26, s45, v26\n" \
"v_mfma_f32_16x16x32_bf16 v[160:163], a[252:255], a[216:219], v[160:163]\n" \
"ds_read_b128 a[152:155], v28 offset:12288\n" \
"v_add_u32_e64 v27, s46, v20\n" \
"v_mfma_f32_16x16x32_bf16 v[176:179], a[188:191], a[156:159], v[176:179]\n" \
"ds_read_b128 a[216:219], v29 offset:12288\n" \
"v_add_u32_e64 v21, s43, v27\n" \
"v_mfma_f32_16x16x32_bf16 v[176:179], a[252:255], a[220:223], v[176:179]\n" \
"v_mfma_f32_16x16x32_bf16 v[172:175], a[184:187], a[156:159], v[172:175]\n" \
"v_mfma_f32_16x16x32_bf16 v[172:175], a[248:251], a[220:223], v[172:175]\n" \
"v_mfma_f32_16x16x32_bf16 v[168:171], a[180:183], a[156:159], v[168:171]\n" \
"v_mfma_f32_16x16x32_bf16 v[168:171], a[244:247], a[220:223], v[168:171]\n" \
"v_mfma_f32_16x16x32_bf16 v[164:167], a[176:179], a[156:159], v[164:167]\n" \
"v_mfma_f32_16x16x32_bf16 v[164:167], a[240:243], a[220:223], v[164:167]\n" \
"ds_read_b128 a[156:159], v28 offset:14336\n" \
"ds_read_b128 a[220:223], v29 offset:14336\n" \
"s_waitcnt lgkmcnt(15)\n" \
"s_nop 0\n" \
"v_mfma_f32_16x16x32_bf16 v[184:187], a[160:163], a[128:131], v[184:187]\n" \
"v_bfe_u32 v44, v20, 2, 5\n" \
"ds_read_b64_tr_b16 a[176:177], v36 offset:0\n" \
"v_mfma_f32_16x16x32_bf16 v[184:187], a[224:227], a[192:195], v[184:187]\n" \
"buffer_load_dword v44, v44, s[80:83], s38 offen offset:0 sc1\n" \
"v_mfma_f32_16x16x32_bf16 v[188:191], a[164:167], a[128:131], v[188:191]\n" \
"ds_read_b64_tr_b16 a[178:179], v36 offset:1024\n" \
"v_mfma_f32_16x16x32_bf16 v[188:191], a[228:231], a[192:195], v[188:191]\n" \
"ds_read_b64_tr_b16 a[240:241], v36 offset:8192\n" \
"v_mfma_f32_16x16x32_bf16 v[196:199], a[172:175], a[128:131], v[196:199]\n" \
"ds_read_b64_tr_b16 a[242:243], v36 offset:9216\n" \
"s_waitcnt lgkmcnt(15)\n" \
"v_mfma_f32_16x16x32_bf16 v[196:199], a[236:239], a[192:195], v[196:199]\n" \
"v_mfma_f32_16x16x32_bf16 v[192:195], a[128:131], a[168:171], v[192:195]\n" \
"ds_read_b64_tr_b16 a[180:181], v37 offset:0\n" \
"s_waitcnt lgkmcnt(13)\n" \
"v_mfma_f32_16x16x32_bf16 v[192:195], a[192:195], a[232:235], v[192:195]\n" \
"v_mfma_f32_16x16x32_bf16 v[208:211], a[132:135], a[168:171], v[208:211]\n" \
"ds_read_b64_tr_b16 a[182:183], v37 offset:1024\n" \
"v_mfma_f32_16x16x32_bf16 v[208:211], a[196:199], a[232:235], v[208:211]\n" \
"ds_read_b64_tr_b16 a[244:245], v37 offset:8192\n" \
"v_mfma_f32_16x16x32_bf16 v[212:215], a[172:175], a[132:135], v[212:215]\n" \
"v_mfma_f32_16x16x32_bf16 v[212:215], a[236:239], a[196:199], v[212:215]\n" \
"ds_read_b64_tr_b16 a[246:247], v37 offset:9216\n" \
"v_cvt_pk_bf16_f32 v188, v188, v189\n" \
"v_mfma_f32_16x16x32_bf16 v[204:207], a[164:167], a[132:135], v[204:207]\n" \
"v_cvt_pk_bf16_f32 v189, v190, v191\n" \
"v_cvt_pk_bf16_f32 v190, v198, v199\n" \
"v_mfma_f32_16x16x32_bf16 v[204:207], a[228:231], a[196:199], v[204:207]\n" \
"ds_read_b64_tr_b16 a[184:185], v38 offset:0\n" \
"v_mov_b32_e32 v191, 0x3f80\n" \
"v_mfma_f32_16x16x32_bf16 v[200:203], a[160:163], a[132:135], v[200:203]\n" \
"ds_read_b64_tr_b16 a[186:187], v38 offset:1024\n" \
"v_cvt_pk_bf16_f32 v184, v184, v185\n" \
"v_mfma_f32_16x16x32_bf16 v[200:203], a[224:227], a[196:199], v[200:203]\n" \
"v_cvt_pk_bf16_f32 v185, v186, v187\n" \
"v_cvt_pk_bf16_f32 v186, v196, v197\n" \
"v_mfma_f32_16x16x32_bf16 v[216:219], a[160:163], a[136:139], v[216:219]\n" \
"ds_read_b64_tr_b16 a[248:249], v38 offset:8192\n" \
"v_mov_b32_e64 v187, 0\n" \
"v_mfma_f32_16x16x32_bf16 v[216:219], a[224:227], a[200:203], v[216:219]\n" \
"ds_read_b64_tr_b16 a[250:251], v38 offset:9216\n" \
"v_mfma_f32_16x16x32_bf16 v[220:223], a[164:167], a[136:139], v[220:223]\n" \
"v_mfma_f32_16x16x32_bf16 v[220:223], a[228:231], a[200:203], v[220:223]\n" \
"ds_read_b64_tr_b16 a[188:189], v39 offset:0\n" \
"v_cvt_pk_bf16_f32 v204, v204, v205\n" \
"v_mfma_f32_16x16x32_bf16 v[228:231], a[172:175], a[136:139], v[228:231]\n" \
"v_cvt_pk_bf16_f32 v205, v206, v207\n" \
"v_cvt_pk_bf16_f32 v206, v214, v215\n" \
"v_mfma_f32_16x16x32_bf16 v[228:231], a[236:239], a[200:203], v[228:231]\n" \
"ds_read_b64_tr_b16 a[190:191], v39 offset:1024\n" \
"v_mov_b32_e32 v207, 0x3f80\n" \
"v_mfma_f32_16x16x32_bf16 v[224:227], a[136:139], a[168:171], v[224:227]\n" \
"ds_read_b64_tr_b16 a[252:253], v39 offset:8192\n" \
"v_cvt_pk_bf16_f32 v200, v200, v201\n" \
"v_mfma_f32_16x16x32_bf16 v[224:227], a[200:203], a[232:235], v[224:227]\n" \
"v_cvt_pk_bf16_f32 v201, v202, v203\n" \
"v_cvt_pk_bf16_f32 v202, v212, v213\n" \
"v_mfma_f32_16x16x32_bf16 v[240:243], a[140:143], a[168:171], v[240:243]\n" \
"ds_read_b64_tr_b16 a[254:255], v39 offset:9216\n" \
"v_mov_b32_e64 v203, 0\n" \
"v_mfma_f32_16x16x32_bf16 v[240:243], a[204:207], a[232:235], v[240:243]\n" \
"v_mfma_f32_16x16x32_bf16 v[244:247], a[172:175], a[140:143], v[244:247]\n" \
"v_mfma_f32_16x16x32_bf16 v[244:247], a[236:239], a[204:207], v[244:247]\n" \
"v_cvt_pk_bf16_f32 v220, v220, v221\n" \
"v_cvt_pk_bf16_f32 v221, v222, v223\n" \
"v_mfma_f32_16x16x32_bf16 v[236:239], a[164:167], a[140:143], v[236:239]\n" \
"v_cvt_pk_bf16_f32 v222, v230, v231\n" \
"v_mov_b32_e32 v223, 0x3f80\n" \
"v_mfma_f32_16x16x32_bf16 v[236:239], a[228:231], a[204:207], v[236:239]\n" \
"v_cvt_pk_bf16_f32 v216, v216, v217\n" \
"v_cvt_pk_bf16_f32 v217, v218, v219\n" \
"v_mfma_f32_16x16x32_bf16 v[232:235], a[160:163], a[140:143], v[232:235]\n" \
"v_cvt_pk_bf16_f32 v218, v228, v229\n" \
"v_mov_b32_e64 v219, 0\n" \
"v_mfma_f32_16x16x32_bf16 v[232:235], a[224:227], a[204:207], v[232:235]\n" \
"s_waitcnt lgkmcnt(15)\n" \
"s_nop 0\n" \
"v_mfma_f32_16x16x32_bf16 v[180:183], a[164:167], v[248:251], v[180:183]\n" \
"v_mfma_f32_16x16x32_bf16 v[180:183], a[228:231], v[48:51], v[180:183]\n" \
"v_mfma_f32_16x16x32_bf16 a[80:83], a[172:175], v[248:251], a[80:83]\n" \
"v_mfma_f32_16x16x32_bf16 a[80:83], a[236:239], v[48:51], a[80:83]\n" \
"v_cvt_pk_bf16_f32 v236, v236, v237\n" \
"v_cvt_pk_bf16_f32 v237, v238, v239\n" \
"v_mfma_f32_16x16x32_bf16 v[252:255], v[248:251], a[168:171], v[252:255]\n" \
"v_cvt_pk_bf16_f32 v238, v246, v247\n" \
"v_mov_b32_e32 v239, 0x3f80\n" \
"v_mfma_f32_16x16x32_bf16 v[252:255], v[48:51], a[232:235], v[252:255]\n" \
"v_cvt_pk_bf16_f32 v232, v232, v233\n" \
"v_cvt_pk_bf16_f32 v233, v234, v235\n" \
"s_waitcnt vmcnt(0) lgkmcnt(0)\n" \
"s_barrier\n" \
"v_mov_b32_e32 v45, s62\n" \
"s_mov_b64 exec, 1\n" \
"buffer_store_dword v45, off, s[80:83], s37 offset:32 sc1\n" \
"s_mov_b64 exec, -1\n" \
"v_cmp_ne_u32_e32 vcc, s62, v44\n" \
"s_nop 4\n" \
"s_cbranch_vccnz L_poll160_%=\n" \
"L_pollr160_%=:\n" \
"v_mfma_f32_16x16x32_bf16 v[52:55], a[176:179], a[128:131], v[52:55]\n" \
"s_mov_b32 s73, s50\n" \
"s_sub_u32 m0, s31, 0x10000\n" \
"v_mfma_f32_16x16x32_bf16 v[52:55], a[240:243], a[192:195], v[52:55]\n" \
"buffer_load_dwordx4 v20, s[80:83], s73 offen offset:0 sc1 lds\n" \
"v_mfma_f32_16x16x32_bf16 v[56:59], a[180:183], a[128:131], v[56:59]\n" \
"s_add_u32 s74, s50, 0x4000\n" \
"s_add_u32 s75, s50, 0x8000\n" \
"v_mfma_f32_16x16x32_bf16 v[56:59], a[244:247], a[192:195], v[56:59]\n" \
"s_mov_b32 s70, 0x3e0293ee\n" \
"s_mov_b32 s76, s52\n" \
"v_mfma_f32_16x16x32_bf16 v[60:63], a[184:187], a[128:131], v[60:63]\n" \
"v_mul_f32_e32 v52, s70, v52\n" \
"v_mul_f32_e32 v53, s70, v53\n" \
"v_mfma_f32_16x16x32_bf16 v[60:63], a[248:251], a[192:195], v[60:63]\n" \
"buffer_load_dwordx4 v20, s[80:83], s73 offen offset:1024 sc1 lds\n" \
"v_mfma_f32_16x16x32_bf16 v[64:67], a[188:191], a[128:131], v[64:67]\n" \
"v_mul_f32_e32 v54, s70, v54\n" \
"v_mul_f32_e32 v55, s70, v55\n" \
"v_mfma_f32_16x16x32_bf16 v[64:67], a[252:255], a[192:195], v[64:67]\n" \
"v_cvt_pk_bf16_f32 v52, v52, v53\n" \
"v_mul_f32_e64 v56, s70, v56\n" \
"v_mfma_f32_16x16x32_bf16 v[80:83], a[188:191], a[132:135], v[80:83]\n" \
"v_mul_f32_e32 v60, s70, v60\n" \
"v_mul_f32_e32 v61, s70, v61\n" \
"v_mfma_f32_16x16x32_bf16 v[80:83], a[252:255], a[196:199], v[80:83]\n" \
"buffer_load_dwordx4 v20, s[80:83], s73 offen offset:2048 sc1 lds\n" \
"v_mfma_f32_16x16x32_bf16 v[76:79], a[184:187], a[132:135], v[76:79]\n" \
"v_mul_f32_e32 v62, s70, v62\n" \
"v_mul_f32_e32 v63, s70, v63\n" \
"v_mfma_f32_16x16x32_bf16 v[76:79], a[248:251], a[196:199], v[76:79]\n" \
"v_cvt_pk_bf16_f32 v60, v60, v61\n" \
"v_mul_f32_e64 v57, s70, v57\n" \
"v_mfma_f32_16x16x32_bf16 v[72:75], a[180:183], a[132:135], v[72:75]\n" \
"v_mul_f32_e32 v58, s70, v58\n" \
"v_mul_f32_e32 v59, s70, v59\n" \
"v_mfma_f32_16x16x32_bf16 v[72:75], a[244:247], a[196:199], v[72:75]\n" \
"buffer_load_dwordx4 v20, s[80:83], s73 offen offset:3072 sc1 lds\n" \
"v_mfma_f32_16x16x32_bf16 v[68:71], a[176:179], a[132:135], v[68:71]\n" \
"s_sub_u32 m0, s31, s51\n" \
"v_cvt_pk_bf16_f32 v53, v54, v55\n" \
"v_mfma_f32_16x16x32_bf16 v[68:71], a[240:243], a[196:199], v[68:71]\n" \
"v_mul_f32_e32 v64, s70, v64\n" \
"v_mul_f32_e64 v65, s70, v65\n" \
"v_mfma_f32_16x16x32_bf16 v[84:87], a[176:179], a[136:139], v[84:87]\n" \
"v_mul_f32_e32 v66, s70, v66\n" \
"v_mul_f32_e32 v67, s70, v67\n" \
"v_mfma_f32_16x16x32_bf16 v[84:87], a[240:243], a[200:203], v[84:87]\n" \
"buffer_load_dwordx4 v20, s[80:83], s74 offen offset:0 sc1 lds\n" \
"v_mfma_f32_16x16x32_bf16 v[88:91], a[180:183], a[136:139], v[88:91]\n" \
"v_cvt_pk_bf16_f32 v61, v62, v63\n" \
"v_cvt_pk_bf16_f32 v54, v56, v57\n" \
"v_mfma_f32_16x16x32_bf16 v[88:91], a[244:247], a[200:203], v[88:91]\n" \
"v_cvt_pk_bf16_f32 v55, v58, v59\n" \
"v_cvt_pk_bf16_f32 v62, v64, v65\n" \
"v_mfma_f32_16x16x32_bf16 v[92:95], a[184:187], a[136:139], v[92:95]\n" \
"v_cvt_pk_bf16_f32 v63, v66, v67\n" \
"v_mul_f32_e64 v68, s70, v68\n" \
"v_mfma_f32_16x16x32_bf16 v[92:95], a[248:251], a[200:203], v[92:95]\n" \
"buffer_load_dwordx4 v20, s[80:83], s74 offen offset:1024 sc1 lds\n" \
"v_mfma_f32_16x16x32_bf16 v[96:99], a[188:191], a[136:139], v[96:99]\n" \
"ds_write_b128 v27, v[52:55] offset:0\n" \
"v_mfma_f32_16x16x32_bf16 v[96:99], a[252:255], a[200:203], v[96:99]\n" \
"v_mul_f32_e32 v69, s70, v69\n" \
"v_mul_f32_e32 v76, s70, v76\n" \
"v_mfma_f32_16x16x32_bf16 v[112:115], a[188:191], a[140:143], v[112:115]\n" \
"v_mul_f32_e32 v77, s70, v77\n" \
"v_mul_f32_e32 v70, s70, v70\n" \
"v_mfma_f32_16x16x32_bf16 v[112:115], a[252:255], a[204:207], v[112:115]\n" \
"buffer_load_dwordx4 v20, s[80:83], s74 offen offset:2048 sc1 lds\n" \
"v_mfma_f32_16x16x32_bf16 v[108:111], a[184:187], a[140:143], v[108:111]\n" \
"ds_write_b128 v27, v[60:63] offset:1024\n" \
"v_mfma_f32_16x16x32_bf16 v[108:111], a[248:251], a[204:207], v[108:111]\n" \
"v_mul_f32_e64 v71, s70, v71\n" \
"v_cvt_pk_bf16_f32 v68, v68, v69\n" \
"v_mfma_f32_16x16x32_bf16 v[104:107], a[180:183], a[140:143], v[104:107]\n" \
"v_mul_f32_e32 v78, s70, v78\n" \
"v_mul_f32_e32 v79, s70, v79\n" \
"v_mfma_f32_16x16x32_bf16 v[104:107], a[244:247], a[204:207], v[104:107]\n" \
"buffer_load_dwordx4 v20, s[80:83], s74 offen offset:3072 sc1 lds\n" \
".fill (L_pb + 36576 - .) / 4, 4, 0xBF800000\n" \
"L_pp9:\n" \
"s_cbranch_execz L_pret\n" \
"v_mfma_f32_16x16x32_bf16 v[100:103], a[176:179], a[140:143], v[100:103]\n" \
"s_sub_u32 m0, s31, 0x4000\n" \
"v_cvt_pk_bf16_f32 v76, v76, v77\n" \
"v_mfma_f32_16x16x32_bf16 v[100:103], a[240:243], a[204:207], v[100:103]\n" \
"v_mul_f32_e32 v72, s70, v72\n" \
"v_mul_f32_e64 v73, s70, v73\n" \
"v_mfma_f32_16x16x32_bf16 v[116:119], a[176:179], a[144:147], v[116:119]\n" \
"v_mul_f32_e32 v74, s70, v74\n" \
"v_mul_f32_e32 v75, s70, v75\n" \
"v_mfma_f32_16x16x32_bf16 v[116:119], a[240:243], a[208:211], v[116:119]\n" \
"buffer_load_dwordx4 v20, s[80:83], s75 offen offset:0 sc1 lds\n" \
"v_mfma_f32_16x16x32_bf16 v[120:123], a[180:183], a[144:147], v[120:123]\n" \
"v_cvt_pk_bf16_f32 v69, v70, v71\n" \
"v_mul_f32_e64 v80, s70, v80\n" \
"v_mfma_f32_16x16x32_bf16 v[120:123], a[244:247], a[208:211], v[120:123]\n" \
"v_mul_f32_e32 v81, s70, v81\n" \
"v_mul_f32_e32 v82, s70, v82\n" \
"v_mfma_f32_16x16x32_bf16 v[124:127], a[184:187], a[144:147], v[124:127]\n" \
"v_mul_f32_e64 v83, s70, v83\n" \
"v_cvt_pk_bf16_f32 v77, v78, v79\n" \
"v_mfma_f32_16x16x32_bf16 v[124:127], a[248:251], a[208:211], v[124:127]\n" \
"buffer_load_dwordx4 v20, s[80:83], s75 offen offset:1024 sc1 lds\n" \
"v_mfma_f32_16x16x32_bf16 v[128:131], a[188:191], a[144:147], v[128:131]\n" \
"v_cvt_pk_bf16_f32 v70, v72, v73\n" \
"v_cvt_pk_bf16_f32 v71, v74, v75\n" \
"v_mfma_f32_16x16x32_bf16 v[128:131], a[252:255], a[208:211], v[128:131]\n" \
"v_cvt_pk_bf16_f32 v78, v80, v81\n" \
"v_cvt_pk_bf16_f32 v79, v82, v83\n" \
"v_mfma_f32_16x16x32_bf16 v[144:147], a[188:191], a[148:151], v[144:147]\n" \
"ds_write_b128 v27, v[68:71] offset:4096\n" \
"v_mfma_f32_16x16x32_bf16 v[144:147], a[252:255], a[212:215], v[144:147]\n" \
"buffer_load_dwordx4 v20, s[80:83], s75 offen offset:2048 sc1 lds\n" \
"v_mfma_f32_16x16x32_bf16 v[140:143], a[184:187], a[148:151], v[140:143]\n" \
"v_cvt_pk_bf16_f32 v180, v180, v181\n" \
"v_accvgpr_read_b32 v50, a82\n" \
"v_mfma_f32_16x16x32_bf16 v[140:143], a[248:251], a[212:215], v[140:143]\n" \
"ds_write_b128 v27, v[76:79] offset:5120\n" \
"v_mfma_f32_16x16x32_bf16 v[136:139], a[180:183], a[148:151], v[136:139]\n" \
"v_accvgpr_read_b32 v51, a83\n" \
"v_cvt_pk_bf16_f32 v181, v182, v183\n" \
"v_mfma_f32_16x16x32_bf16 v[136:139], a[244:247], a[212:215], v[136:139]\n" \
"buffer_load_dwordx4 v20, s[80:83], s75 offen offset:3072 sc1 lds\n" \
"v_mfma_f32_16x16x32_bf16 v[132:135], a[176:179], a[148:151], v[132:135]\n" \
"s_mov_b32 m0, s53\n" \
"v_cvt_pk_bf16_f32 v182, v50, v51\n" \
"v_mfma_f32_16x16x32_bf16 v[132:135], a[240:243], a[212:215], v[132:135]\n" \
"v_mov_b32_e32 v183, 0x3f80\n" \
"v_cvt_pk_bf16_f32 v234, v244, v245\n" \
"v_mfma_f32_16x16x32_bf16 v[148:151], a[176:179], a[152:155], v[148:151]\n" \
"v_mov_b32_e32 v235, 0\n" \
"v_cvt_pk_bf16_f32 v54, v192, v193\n" \
"v_mfma_f32_16x16x32_bf16 v[148:151], a[240:243], a[216:219], v[148:151]\n" \
"buffer_load_dwordx4 v20, s[80:83], s76 offen offset:0 sc1 lds\n" \
"v_mfma_f32_16x16x32_bf16 v[152:155], a[180:183], a[152:155], v[152:155]\n" \
"v_cvt_pk_bf16_f32 v55, v194, v195\n" \
"v_cvt_pk_bf16_f32 v56, v208, v209\n" \
"v_mfma_f32_16x16x32_bf16 v[152:155], a[244:247], a[216:219], v[152:155]\n" \
"v_cvt_pk_bf16_f32 v57, v210, v211\n" \
"v_cvt_pk_bf16_f32 v58, v224, v225\n" \
"v_cvt_pk_bf16_f32 v59, v226, v227\n" \
"v_cvt_pk_bf16_f32 v60, v240, v241\n" \
"v_cvt_pk_bf16_f32 v61, v242, v243\n" \
"v_mfma_f32_16x16x32_bf16 v[32:35], v[180:183], v[184:187], 0\n" \
"buffer_load_dwordx4 v20, s[80:83], s76 offen offset:1024 sc1 lds\n" \
"v_mfma_f32_16x16x32_bf16 v[156:159], a[184:187], a[152:155], v[156:159]\n" \
"v_mfma_f32_16x16x32_bf16 v[192:195], v[188:191], v[184:187], 0\n" \
"v_mfma_f32_16x16x32_bf16 v[208:211], v[204:207], v[200:203], 0\n" \
"v_mfma_f32_16x16x32_bf16 v[36:39], v[188:191], v[200:203], 0\n" \
"v_mfma_f32_16x16x32_bf16 v[224:227], v[220:223], v[216:219], 0\n" \
"v_mfma_f32_16x16x32_bf16 v[248:251], v[204:207], v[216:219], 0\n" \
"v_mfma_f32_16x16x32_bf16 v[240:243], v[236:239], v[232:235], 0\n" \
"v_mfma_f32_16x16x32_bf16 v[64:67], v[220:223], v[232:235], 0\n" \
"s_mov_b32 s64, 0xaaaaaaaa\n" \
"s_mov_b32 s65, 0xaaaaaaaa\n" \
"v_mfma_f32_16x16x32_bf16 v[156:159], a[248:251], a[216:219], v[156:159]\n" \
"v_cndmask_b32_e64 v32, v32, v33, s[64:65]\n" \
"v_cndmask_b32_e64 v34, v34, v35, s[64:65]\n" \
"v_mfma_f32_16x16x32_bf16 v[160:163], a[188:191], a[152:155], v[160:163]\n" \
"s_mov_b32 s66, 0xfff0ffff\n" \
"s_mov_b32 s67, 0xf000ff00\n" \
"s_mov_b32 s68, 0xffc0fffc\n" \
"s_mov_b32 s69, 0xc000fc00\n" \
"v_cndmask_b32_e64 v192, v192, v193, s[64:65]\n" \
"v_add_f32_e64 v32, s47, v32\n" \
"v_cndmask_b32_e64 v194, v194, v195, s[64:65]\n" \
"v_add_f32_e64 v34, s47, v34\n" \
"v_cndmask_b32_e64 v208, v208, v209, s[64:65]\n" \
"v_cndmask_b32_e64 v36, v36, v37, s[64:65]\n" \
"v_cndmask_b32_e64 v210, v210, v211, s[64:65]\n" \
"v_cndmask_b32_e64 v38, v38, v39, s[64:65]\n" \
"v_cndmask_b32_e64 v224, v224, v225, s[64:65]\n" \
"v_cndmask_b32_e64 v248, v248, v249, s[64:65]\n" \
"v_cndmask_b32_e64 v226, v226, v227, s[64:65]\n" \
"v_cndmask_b32_e64 v250, v250, v251, s[64:65]\n" \
"v_cndmask_b32_e64 v240, v240, v241, s[64:65]\n" \
"v_cndmask_b32_e64 v64, v64, v65, s[64:65]\n" \
"v_cndmask_b32_e64 v242, v242, v243, s[64:65]\n" \
"v_cndmask_b32_e64 v66, v66, v67, s[64:65]\n" \
"v_cndmask_b32_e64 v192, v32, v192, s[66:67]\n" \
"v_cndmask_b32_e64 v194, v34, v194, s[68:69]\n" \
"v_cndmask_b32_e64 v208, v36, v208, s[66:67]\n" \
"v_cndmask_b32_e64 v210, v38, v210, s[68:69]\n" \
"v_cndmask_b32_e64 v224, v248, v224, s[66:67]\n" \
"v_cndmask_b32_e64 v226, v250, v226, s[68:69]\n" \
"v_cndmask_b32_e64 v240, v64, v240, s[66:67]\n" \
"v_cndmask_b32_e64 v242, v66, v242, s[68:69]\n" \
"v_mfma_f32_16x16x32_bf16 v[160:163], a[252:255], a[216:219], v[160:163]\n" \
"v_max_f32_e32 v193, v192, v194\n" \
"v_max_f32_e32 v209, v208, v210\n" \
"v_max_f32_e32 v225, v224, v226\n" \
"v_max_f32_e32 v241, v240, v242\n" \
"v_mul_f32_e32 v84, s70, v84\n" \
"v_permlane32_swap_b32_e32 v193, v225\n" \
"v_permlane32_swap_b32_e32 v209, v241\n" \
"v_mul_f32_e32 v85, s70, v85\n" \
"v_mul_f32_e32 v92, s70, v92\n" \
"v_mul_f32_e32 v93, s70, v93\n" \
"v_permlane32_swap_b32_e32 v196, v228\n" \
"v_permlane32_swap_b32_e32 v212, v244\n" \
"v_max_f32_e32 v193, v193, v225\n" \
"v_max_f32_e32 v209, v209, v241\n" \
"v_accvgpr_read_b32 v48, a80\n" \
"v_accvgpr_read_b32 v49, a81\n" \
"v_mul_f32_e32 v86, s70, v86\n" \
"v_mul_f32_e32 v87, s70, v87\n" \
"v_cvt_pk_bf16_f32 v84, v84, v85\n" \
"v_mul_f32_e32 v94, s70, v94\n" \
"v_mul_f32_e32 v95, s70, v95\n" \
"v_cvt_pk_bf16_f32 v92, v92, v93\n" \
"v_permlane16_swap_b32_e32 v228, v244\n" \
"v_permlane16_swap_b32_e32 v193, v209\n" \
"v_mfma_f32_16x16x32_bf16 v[176:179], a[188:191], a[156:159], v[176:179]\n" \
"s_mov_b32 s72, 0x3eae1a93\n" \
"v_mul_f32_e32 v88, s70, v88\n" \
"v_mul_f32_e32 v89, s70, v89\n" \
"v_mul_f32_e32 v90, s70, v90\n" \
"v_mul_f32_e32 v91, s70, v91\n" \
"v_cvt_pk_bf16_f32 v85, v86, v87\n" \
"v_mul_f32_e32 v96, s70, v96\n" \
"v_mul_f32_e32 v97, s70, v97\n" \
"v_mul_f32_e32 v98, s70, v98\n" \
"v_mul_f32_e32 v99, s70, v99\n" \
"v_cvt_pk_bf16_f32 v93, v94, v95\n" \
"v_mul_f32_e32 v45, 0xbfb8aa3b, v228\n" \
"v_mfma_f32_16x16x16_bf16 v[48:51], v[182:183], v[46:47], 0\n" \
"v_mfma_f32_16x16x16_bf16 v[196:199], v[190:191], v[46:47], 0\n" \
"v_mfma_f32_16x16x16_bf16 v[212:215], v[206:207], v[46:47], 0\n" \
"v_max_f32_e64 v193, v193, v209\n" \
"v_cvt_pk_bf16_f32 v86, v88, v89\n" \
"v_cvt_pk_bf16_f32 v87, v90, v91\n" \
"v_cvt_pk_bf16_f32 v94, v96, v97\n" \
"v_cvt_pk_bf16_f32 v95, v98, v99\n" \
"v_mfma_f32_16x16x16_bf16 v[228:231], v[222:223], v[46:47], 0\n" \
"v_mul_f32_e32 v193, s72, v193\n" \
"v_mov_b32_e32 v209, v193\n" \
"ds_write_b128 v27, v[84:87] offset:8192\n" \
"v_mfma_f32_16x16x16_bf16 v[244:247], v[238:239], v[46:47], 0\n" \
"v_permlane16_swap_b32_e32 v193, v209\n" \
"s_mov_b32 s71, 0x3e0293ee\n" \
"v_mfma_f32_16x16x32_bf16 v[176:179], a[252:255], a[220:223], v[176:179]\n" \
"v_mov_b32_e32 v225, v193\n" \
"v_mov_b32_e32 v241, v209\n" \
"v_mul_f32_e32 v100, s70, v100\n" \
"v_mul_f32_e64 v101, s70, v101\n" \
"ds_write_b128 v27, v[92:95] offset:9216\n" \
"v_mul_f32_e32 v108, s70, v108\n" \
"v_mul_f32_e32 v109, s70, v109\n" \
"v_pk_mul_f32 v[116:117], v[116:117], s[70:71]\n" \
"v_pk_mul_f32 v[124:125], v[124:125], s[70:71]\n" \
"v_pk_mul_f32 v[132:133], v[132:133], s[70:71]\n" \
"v_pk_mul_f32 v[140:141], v[140:141], s[70:71]\n" \
"v_pk_mul_f32 v[148:149], v[148:149], s[70:71]\n" \
"v_pk_mul_f32 v[156:157], v[156:157], s[70:71]\n" \
"v_permlane32_swap_b32_e32 v193, v225\n" \
"v_permlane32_swap_b32_e32 v209, v241\n" \
"v_mul_f32_e32 v102, s70, v102\n" \
"v_mul_f32_e32 v103, s70, v103\n" \
"v_cvt_pk_bf16_f32 v100, v100, v101\n" \
"v_mul_f32_e32 v110, s70, v110\n" \
"v_mul_f32_e32 v111, s70, v111\n" \
"v_cvt_pk_bf16_f32 v108, v108, v109\n" \
"v_pk_mul_f32 v[118:119], v[118:119], s[70:71]\n" \
"v_cvt_pk_bf16_f32 v116, v116, v117\n" \
"v_pk_mul_f32 v[126:127], v[126:127], s[70:71]\n" \
"v_cvt_pk_bf16_f32 v124, v124, v125\n" \
"v_pk_mul_f32 v[134:135], v[134:135], s[70:71]\n" \
"v_cvt_pk_bf16_f32 v132, v132, v133\n" \
"v_pk_mul_f32 v[142:143], v[142:143], s[70:71]\n" \
"v_cvt_pk_bf16_f32 v140, v140, v141\n" \
"v_pk_mul_f32 v[150:151], v[150:151], s[70:71]\n" \
"v_cvt_pk_bf16_f32 v148, v148, v149\n" \
"v_pk_mul_f32 v[158:159], v[158:159], s[70:71]\n" \
"v_cvt_pk_bf16_f32 v156, v156, v157\n" \
"v_fma_f32 v192, v192, s72, -v193\n" \
"v_fma_f32 v194, v194, s72, -v193\n" \
"v_fma_f32 v208, v208, s72, -v209\n" \
"v_fma_f32 v210, v210, s72, -v209\n" \
"v_mul_f32_e32 v104, s70, v104\n" \
"v_mul_f32_e32 v105, s70, v105\n" \
"v_mul_f32_e32 v106, s70, v106\n" \
"v_mul_f32_e32 v107, s70, v107\n" \
"v_cvt_pk_bf16_f32 v101, v102, v103\n" \
"v_mul_f32_e32 v112, s70, v112\n" \
"v_mul_f32_e32 v113, s70, v113\n" \
"v_mul_f32_e32 v114, s70, v114\n" \
"v_mul_f32_e32 v115, s70, v115\n" \
"v_cvt_pk_bf16_f32 v109, v110, v111\n" \
"v_pk_mul_f32 v[120:121], v[120:121], s[70:71]\n" \
"v_pk_mul_f32 v[122:123], v[122:123], s[70:71]\n" \
"v_cvt_pk_bf16_f32 v117, v118, v119\n" \
"v_pk_mul_f32 v[128:129], v[128:129], s[70:71]\n" \
"v_pk_mul_f32 v[130:131], v[130:131], s[70:71]\n" \
"v_cvt_pk_bf16_f32 v125, v126, v127\n" \
"v_pk_mul_f32 v[136:137], v[136:137], s[70:71]\n" \
"v_pk_mul_f32 v[138:139], v[138:139], s[70:71]\n" \
"v_cvt_pk_bf16_f32 v133, v134, v135\n" \
"v_pk_mul_f32 v[144:145], v[144:145], s[70:71]\n" \
"v_pk_mul_f32 v[146:147], v[146:147], s[70:71]\n" \
"v_cvt_pk_bf16_f32 v141, v142, v143\n" \
"v_pk_mul_f32 v[152:153], v[152:153], s[70:71]\n" \
"v_pk_mul_f32 v[154:155], v[154:155], s[70:71]\n" \
"v_cvt_pk_bf16_f32 v149, v150, v151\n" \
"v_pk_mul_f32 v[160:161], v[160:161], s[70:71]\n" \
"v_pk_mul_f32 v[162:163], v[162:163], s[70:71]\n" \
"v_cvt_pk_bf16_f32 v157, v158, v159\n" \
"v_pk_mul_f32 v[176:177], v[176:177], s[70:71]\n" \
"v_pk_mul_f32 v[178:179], v[178:179], s[70:71]\n" \
"v_exp_f32_e32 v192, v192\n" \
"v_exp_f32_e32 v194, v194\n" \
"v_exp_f32_e32 v208, v208\n" \
"v_exp_f32_e32 v210, v210\n" \
"v_fma_f32 v224, v224, s72, -v225\n" \
"v_fma_f32 v226, v226, s72, -v225\n" \
"v_mfma_f32_16x16x32_bf16 v[172:175], a[184:187], a[156:159], v[172:175]\n" \
"v_cvt_pk_bf16_f32 v102, v104, v105\n" \
"v_cvt_pk_bf16_f32 v103, v106, v107\n" \
"v_cvt_pk_bf16_f32 v110, v112, v113\n" \
"v_cvt_pk_bf16_f32 v111, v114, v115\n" \
"v_cvt_pk_bf16_f32 v118, v120, v121\n" \
"v_cvt_pk_bf16_f32 v119, v122, v123\n" \
"v_cvt_pk_bf16_f32 v126, v128, v129\n" \
"v_cvt_pk_bf16_f32 v127, v130, v131\n" \
"v_cvt_pk_bf16_f32 v134, v136, v137\n" \
"v_cvt_pk_bf16_f32 v135, v138, v139\n" \
"v_cvt_pk_bf16_f32 v142, v144, v145\n" \
"v_cvt_pk_bf16_f32 v143, v146, v147\n" \
"v_cvt_pk_bf16_f32 v150, v152, v153\n" \
"v_cvt_pk_bf16_f32 v151, v154, v155\n" \
"v_cvt_pk_bf16_f32 v158, v160, v161\n" \
"v_cvt_pk_bf16_f32 v159, v162, v163\n" \
"v_exp_f32_e32 v224, v224\n" \
"v_exp_f32_e32 v226, v226\n" \
"v_fma_f32 v240, v240, s72, -v241\n" \
"v_fma_f32 v242, v242, s72, -v241\n" \
"ds_write_b128 v27, v[100:103] offset:12288\n" \
"v_cvt_pk_bf16_f32 v192, v192, v194\n" \
"v_cvt_pk_bf16_f32 v208, v208, v210\n" \
"v_exp_f32_e32 v240, v240\n" \
"v_exp_f32_e32 v242, v242\n" \
"v_cvt_pk_bf16_f32 v68, v48, v49\n" \
"v_cvt_pk_bf16_f32 v69, v50, v51\n" \
"v_cvt_pk_bf16_f32 v70, v196, v197\n" \
"v_cvt_pk_bf16_f32 v71, v198, v199\n" \
"ds_write_b128 v27, v[108:111] offset:13312\n" \
"v_cvt_pk_bf16_f32 v72, v212, v213\n" \
"v_cvt_pk_bf16_f32 v73, v214, v215\n" \
"v_perm_b32 v184, v192, v192, v40\n" \
"v_perm_b32 v185, v192, v192, v41\n" \
"v_perm_b32 v186, v192, v192, v42\n" \
"v_perm_b32 v187, v192, v192, v43\n" \
"v_perm_b32 v200, v208, v208, v40\n" \
"v_perm_b32 v201, v208, v208, v41\n" \
"v_perm_b32 v202, v208, v208, v42\n" \
"v_perm_b32 v203, v208, v208, v43\n" \
"ds_write_b128 v21, v[116:119] offset:16384\n" \
"v_cvt_pk_bf16_f32 v224, v224, v226\n" \
"v_cvt_pk_bf16_f32 v74, v228, v229\n" \
"v_cvt_pk_bf16_f32 v75, v230, v231\n" \
"v_mfma_f32_16x16x32_bf16 v[32:35], v[68:71], v[184:187], 0\n" \
"v_mfma_f32_16x16x32_bf16 v[36:39], v[70:73], v[200:203], 0\n" \
"v_perm_b32 v216, v224, v224, v40\n" \
"v_perm_b32 v217, v224, v224, v41\n" \
"ds_write_b128 v21, v[124:127] offset:17408\n" \
"v_perm_b32 v218, v224, v224, v42\n" \
"v_perm_b32 v219, v224, v224, v43\n" \
"v_cvt_pk_bf16_f32 v240, v240, v242\n" \
"v_mfma_f32_16x16x32_bf16 v[172:175], a[248:251], a[220:223], v[172:175]\n" \
"v_cvt_pk_bf16_f32 v76, v244, v245\n" \
"v_cvt_pk_bf16_f32 v77, v246, v247\n" \
"v_mfma_f32_16x16x32_bf16 v[248:251], v[72:75], v[216:219], 0\n" \
"v_perm_b32 v232, v240, v240, v40\n" \
"v_perm_b32 v233, v240, v240, v41\n" \
"ds_write_b128 v21, v[132:135] offset:20480\n" \
"v_perm_b32 v234, v240, v240, v42\n" \
"v_perm_b32 v235, v240, v240, v43\n" \
"v_pk_mul_f32 v[172:173], v[172:173], s[70:71]\n" \
"v_pk_mul_f32 v[174:175], v[174:175], s[70:71]\n" \
"v_cvt_pk_bf16_f32 v172, v172, v173\n" \
"v_cvt_pk_bf16_f32 v173, v174, v175\n" \
"v_cvt_pk_bf16_f32 v174, v176, v177\n" \
"v_cvt_pk_bf16_f32 v175, v178, v179\n" \
"v_mfma_f32_16x16x32_bf16 v[64:67], v[74:77], v[232:235], 0\n" \
"ds_write_b128 v21, v[140:143] offset:21504\n" \
"v_mfma_f32_16x16x32_bf16 v[168:171], a[180:183], a[156:159], v[168:171]\n" \
"v_cvt_pk_bf16_f32 v52, v252, v253\n" \
"v_cvt_pk_bf16_f32 v53, v254, v255\n" \
"v_mfma_f32_16x16x32_bf16 v[168:171], a[244:247], a[220:223], v[168:171]\n" \
"v_exp_f32_e64 v45, v45\n" \
"v_mfma_f32_16x16x32_bf16 v[192:195], v[52:55], v[184:187], 0\n" \
"ds_write_b128 v21, v[148:151] offset:24576\n" \
"v_mfma_f32_16x16x32_bf16 v[208:211], v[54:57], v[200:203], 0\n" \
"v_mfma_f32_16x16x32_bf16 v[224:227], v[56:59], v[216:219], 0\n" \
"v_mfma_f32_16x16x32_bf16 v[240:243], v[58:61], v[232:235], 0\n" \
"ds_write_b128 v21, v[156:159] offset:25600\n" \
"v_mov_b32_dpp v34, v38 quad_perm:[0,1,2,3] row_mask:0x2 bank_mask:0xf\n" \
"v_pk_mul_f32 v[168:169], v[168:169], s[70:71]\n" \
"v_pk_mul_f32 v[170:171], v[170:171], s[70:71]\n" \
"v_mov_b32_dpp v34, v250 quad_perm:[0,1,2,3] row_mask:0x4 bank_mask:0xf\n" \
"v_add_f32_e64 v45, 1.0, v45\n" \
"v_mfma_f32_16x16x32_bf16 v[164:167], a[176:179], a[156:159], v[164:167]\n" \
"v_mov_b32_dpp v34, v66 quad_perm:[0,1,2,3] row_mask:0x8 bank_mask:0xf\n" \
"v_mov_b32_dpp v32, v36 quad_perm:[0,1,2,3] row_mask:0x2 bank_mask:0xf\n" \
"v_rcp_f32_e64 v45, v45\n" \
"ds_write_b128 v21, v[172:175] offset:29696\n" \
"v_mov_b32_dpp v33, v37 quad_perm:[0,1,2,3] row_mask:0x2 bank_mask:0xf\n" \
"v_rcp_f32_e64 v34, v34\n" \
"v_mov_b32_dpp v32, v248 quad_perm:[0,1,2,3] row_mask:0x4 bank_mask:0xf\n" \
"v_mov_b32_dpp v33, v249 quad_perm:[0,1,2,3] row_mask:0x4 bank_mask:0xf\n" \
"v_mul_f32_e64 v34, v45, v34\n" \
"v_mov_b32_dpp v32, v64 quad_perm:[0,1,2,3] row_mask:0x8 bank_mask:0xf\n" \
"v_mov_b32_dpp v33, v65 quad_perm:[0,1,2,3] row_mask:0x8 bank_mask:0xf\n" \
"v_mul_f32_e32 v32, v34, v32\n" \
"v_mul_f32_e32 v33, v34, v33\n" \
"v_mov_b32_e64 v38, v34\n" \
"v_mfma_f32_16x16x32_bf16 v[164:167], a[240:243], a[220:223], v[164:167]\n" \
"v_sub_f32_e32 v44, 1.0, v45\n" \
"v_permlane16_swap_b32_e32 v34, v38\n" \
"v_mov_b32_e32 v250, v34\n" \
"v_mov_b32_e32 v66, v38\n" \
"v_cvt_pk_bf16_f32 v32, v32, v33\n" \
"v_permlane32_swap_b32_e32 v34, v250\n" \
"v_permlane32_swap_b32_e32 v38, v66\n" \
"v_mul_f32_e64 v192, v34, v192\n" \
"v_pk_mul_f32 v[164:165], v[164:165], s[70:71]\n" \
"v_pk_mul_f32 v[166:167], v[166:167], s[70:71]\n" \
"v_cvt_pk_bf16_f32 v164, v164, v165\n" \
"v_cvt_pk_bf16_f32 v165, v166, v167\n" \
"v_cvt_pk_bf16_f32 v166, v168, v169\n" \
"v_cvt_pk_bf16_f32 v167, v170, v171\n" \
"ds_write_b128 v21, v[164:167] offset:28672\n" \
"v_mul_f32_e32 v193, v34, v193\n" \
"v_mul_f32_e32 v208, v38, v208\n" \
"v_mul_f32_e32 v209, v38, v209\n" \
"v_mul_f32_e32 v224, v250, v224\n" \
"v_mul_f32_e32 v225, v250, v225\n" \
"v_mul_f32_e32 v240, v66, v240\n" \
"v_mul_f32_e32 v241, v66, v241\n" \
"v_mul_f32_e32 v194, v34, v194\n" \
"v_mul_f32_e64 v195, v34, v195\n" \
"v_cvt_pk_bf16_f32 v192, v192, v193\n" \
"v_mul_f32_e32 v210, v38, v210\n" \
"v_mul_f32_e32 v211, v38, v211\n" \
"v_cvt_pk_bf16_f32 v208, v208, v209\n" \
"v_mul_f32_e32 v226, v250, v226\n" \
"v_mul_f32_e32 v227, v250, v227\n" \
"v_cvt_pk_bf16_f32 v224, v224, v225\n" \
"v_mul_f32_e32 v242, v66, v242\n" \
"v_mul_f32_e32 v243, v66, v243\n" \
"v_cvt_pk_bf16_f32 v240, v240, v241\n" \
"v_cvt_pk_bf16_f32 v193, v194, v195\n" \
"s_add_u32 s84, s48, 0x0\n" \
"s_nop 0\n" \
"v_cvt_pk_bf16_f32 v209, v210, v211\n" \
"s_add_u32 s85, s48, 0x24800\n" \
"v_cvt_pk_bf16_f32 v225, v226, v227\n" \
"s_add_u32 s86, s48, 0x49000\n" \
"v_cvt_pk_bf16_f32 v241, v242, v243\n" \
"s_add_u32 s87, s48, 0x6d800\n" \
"ds_write_b32 v26, v44\n" \
"buffer_store_dword v32, v25, s[80:83], s49 offen sc1\n" \
"buffer_store_dwordx2 v[192:193], v24, s[80:83], s84 offen sc1\n" \
"buffer_store_dwordx2 v[208:209], v24, s[80:83], s85 offen sc1\n" \
"buffer_store_dwordx2 v[224:225], v24, s[80:83], s86 offen sc1\n" \
"buffer_store_dwordx2 v[240:241], v24, s[80:83], s87 offen sc1\n" \
"s_branch L_end_%=\n" \
"L_poll60_%=:\n" \
"s_mov_b32 s84, 0\n" \
"L_pl60_%=:\n" \
"s_sleep 1\n" \
"v_bfe_u32 v44, v20, 2, 5\n" \
"buffer_load_dword v44, v44, s[80:83], s38 offen offset:128 sc1\n" \
"s_waitcnt vmcnt(0)\n" \
"v_cmp_ne_u32_e32 vcc, s62, v44\n" \
"s_add_u32 s84, s84, 1\n" \
"s_nop 3\n" \
"s_cbranch_vccz L_pollr60_%=\n" \
"s_cmp_gt_u32 s84, 0x400000\n" \
"s_cbranch_scc0 L_pl60_%=\n" \
"s_branch L_pollr60_%=\n" \
"L_poll160_%=:\n" \
"s_mov_b32 s84, 0\n" \
"L_pl160_%=:\n" \
"s_sleep 1\n" \
"v_bfe_u32 v44, v20, 2, 5\n" \
"buffer_load_dword v44, v44, s[80:83], s38 offen offset:0 sc1\n" \
"s_waitcnt vmcnt(0)\n" \
"v_cmp_ne_u32_e32 vcc, s62, v44\n" \
"s_add_u32 s84, s84, 1\n" \
"s_nop 3\n" \
"s_cbranch_vccz L_pollr160_%=\n" \
"s_cmp_gt_u32 s84, 0x400000\n" \
"s_cbranch_scc0 L_pl160_%=\n" \
"s_branch L_pollr160_%=\n" \
".fill (L_pb + 42440 - .) / 4, 4, 0xBF800000\n" \
"L_w1_%=:\n" \
"s_waitcnt vmcnt(18)\n" \
"s_barrier\n" \
"ds_read_b64_tr_b16 a[160:161], v32 offset:0\n" \
"ds_read_b64_tr_b16 a[162:163], v32 offset:1024\n" \
"ds_read_b128 a[128:131], v28 offset:16384\n" \
"ds_read_b64_tr_b16 a[224:225], v32 offset:8192\n" \
"ds_read_b64_tr_b16 a[226:227], v32 offset:9216\n" \
"ds_read_b128 a[192:195], v29 offset:16384\n" \
"ds_read_b128 a[132:135], v28 offset:18432\n" \
"ds_read_b128 a[196:199], v29 offset:18432\n" \
"ds_read_b64_tr_b16 a[164:165], v33 offset:0\n" \
"ds_read_b64_tr_b16 a[166:167], v33 offset:1024\n" \
"ds_read_b64_tr_b16 a[228:229], v33 offset:8192\n" \
"ds_read_b64_tr_b16 a[230:231], v33 offset:9216\n" \
"ds_read_b128 a[136:139], v28 offset:20480\n" \
"ds_read_b128 a[200:203], v29 offset:20480\n" \
"s_waitcnt lgkmcnt(11)\n" \
"v_mfma_f32_16x16x32_bf16 a[0:3], a[128:131], a[160:163], 0\n" \
"ds_read_b64_tr_b16 a[168:169], v34 offset:0\n" \
"v_mad_i32_i24 v217, v216, -6, v215\n" \
"s_waitcnt lgkmcnt(9)\n" \
"v_mfma_f32_16x16x32_bf16 a[0:3], a[192:195], a[224:227], a[0:3]\n" \
"ds_read_b64_tr_b16 a[170:171], v34 offset:1024\n" \
"v_add_u32_e32 v218, 0xfffffe50, v215\n" \
"s_waitcnt lgkmcnt(9)\n" \
"v_mfma_f32_16x16x32_bf16 a[16:19], a[132:135], a[160:163], 0\n" \
"ds_read_b64_tr_b16 a[232:233], v34 offset:8192\n" \
"s_mul_i32 s35, s6, 0x48\n" \
"s_waitcnt lgkmcnt(9)\n" \
"v_mfma_f32_16x16x32_bf16 a[16:19], a[196:199], a[224:227], a[16:19]\n" \
"ds_read_b64_tr_b16 a[234:235], v34 offset:9216\n" \
"v_lshrrev_b32_e32 v219, 1, v217\n" \
"s_waitcnt lgkmcnt(8)\n" \
"v_mfma_f32_16x16x32_bf16 a[20:23], a[132:135], a[164:167], 0\n" \
"ds_read_b128 a[140:143], v28 offset:22528\n" \
"v_add_u32_e32 v218, s35, v216\n" \
"s_waitcnt lgkmcnt(7)\n" \
"v_mfma_f32_16x16x32_bf16 a[20:23], a[196:199], a[228:231], a[20:23]\n" \
"ds_read_b128 a[204:207], v29 offset:22528\n" \
"v_mul_u32_u24_e32 v219, 0x480, v219\n" \
"v_mfma_f32_16x16x32_bf16 a[4:7], a[128:131], a[164:167], 0\n" \
"ds_read_b64_tr_b16 a[172:173], v35 offset:0\n" \
"v_and_b32_e64 v214, 16, v20\n" \
"v_mfma_f32_16x16x32_bf16 a[4:7], a[192:195], a[228:231], a[4:7]\n" \
"ds_read_b64_tr_b16 a[174:175], v35 offset:1024\n" \
"v_mad_u32_u24 v219, v218, s5, v219\n" \
"s_waitcnt lgkmcnt(9)\n" \
"v_mfma_f32_16x16x32_bf16 a[32:35], a[136:139], a[160:163], 0\n" \
"ds_read_b64_tr_b16 a[236:237], v35 offset:8192\n" \
"v_or_b32_e32 v192, v219, v214\n" \
"s_waitcnt lgkmcnt(9)\n" \
"v_mfma_f32_16x16x32_bf16 a[32:35], a[200:203], a[224:227], a[32:35]\n" \
"ds_read_b64_tr_b16 a[238:239], v35 offset:9216\n" \
"v_mov_b32_e32 v220, 2.0\n" \
"v_mfma_f32_16x16x32_bf16 a[36:39], a[136:139], a[164:167], 0\n" \
"ds_read_b128 a[144:147], v28 offset:0\n" \
"v_mov_b32_e64 v222, 7\n" \
"v_mfma_f32_16x16x32_bf16 a[36:39], a[200:203], a[228:231], a[36:39]\n" \
"ds_read_b128 a[208:211], v29 offset:0\n" \
"v_lshlrev_b32_e32 v220, 7, v218\n" \
"s_waitcnt lgkmcnt(10)\n" \
"v_mfma_f32_16x16x32_bf16 a[40:43], a[136:139], a[168:171], 0\n" \
"ds_read_b128 a[148:151], v28 offset:2048\n" \
"v_lshrrev_b32_e32 v218, 1, v218\n" \
"s_waitcnt lgkmcnt(9)\n" \
"v_mfma_f32_16x16x32_bf16 a[40:43], a[200:203], a[232:235], a[40:43]\n" \
"ds_read_b128 a[212:215], v29 offset:2048\n" \
"v_and_b32_e64 v218, 4, v218\n" \
"v_mfma_f32_16x16x32_bf16 a[24:27], a[132:135], a[168:171], 0\n" \
"ds_read_b128 a[152:155], v28 offset:4096\n" \
"v_and_b32_e64 v216, 2, v216\n" \
"v_mfma_f32_16x16x32_bf16 a[24:27], a[196:199], a[232:235], a[24:27]\n" \
"ds_read_b128 a[216:219], v29 offset:4096\n" \
"s_mul_i32 s7, s6, 0x2400\n" \
"v_mfma_f32_16x16x32_bf16 a[8:11], a[128:131], a[168:171], 0\n" \
"ds_read_b128 a[156:159], v28 offset:6144\n" \
"s_sub_i32 s34, 0x22000, s7\n" \
"v_mfma_f32_16x16x32_bf16 a[8:11], a[192:195], a[232:235], a[8:11]\n" \
"ds_read_b128 a[220:223], v29 offset:6144\n" \
"s_mov_b32 m0, s32\n" \
"s_waitcnt lgkmcnt(13)\n" \
"v_mfma_f32_16x16x32_bf16 a[48:51], a[140:143], a[160:163], 0\n" \
"s_mov_b32 exec_lo, 0xff000000\n" \
"s_mov_b32 exec_hi, 0x0\n" \
"global_load_lds_dword v[184:185], off\n" \
"s_mov_b64 exec, -1\n" \
"s_waitcnt lgkmcnt(12)\n" \
"v_mfma_f32_16x16x32_bf16 a[48:51], a[204:207], a[224:227], a[48:51]\n" \
"ds_read_b64_tr_b16 a[176:177], v36 offset:0\n" \
"v_bitop3_b32 v216, v217, v218, v216 bitop3:0x1e\n" \
"v_mfma_f32_16x16x32_bf16 a[52:55], a[140:143], a[164:167], 0\n" \
"ds_read_b64_tr_b16 a[178:179], v36 offset:1024\n" \
"v_or_b32_e32 v213, 0x24400, v20\n" \
"v_mfma_f32_16x16x32_bf16 a[52:55], a[204:207], a[228:231], a[52:55]\n" \
"s_mov_b32 m0, s32\n" \
"ds_read_b64_tr_b16 a[240:241], v36 offset:8192\n" \
"v_mfma_f32_16x16x32_bf16 a[56:59], a[140:143], a[168:171], 0\n" \
"s_mov_b32 exec_lo, 0xffff\n" \
"s_mov_b32 exec_hi, 0x0\n" \
"global_load_lds_dword v[186:187], off\n" \
"s_mov_b64 exec, -1\n" \
"v_mfma_f32_16x16x32_bf16 a[56:59], a[204:207], a[232:235], a[56:59]\n" \
"ds_read_b64_tr_b16 a[242:243], v36 offset:9216\n" \
"v_lshlrev_b32_e32 v216, 4, v216\n" \
"s_waitcnt lgkmcnt(14)\n" \
"v_mfma_f32_16x16x32_bf16 a[60:63], a[140:143], a[172:175], 0\n" \
"ds_read_b64_tr_b16 a[180:181], v37 offset:0\n" \
"v_add3_u32 v196, v220, s34, v216\n" \
"s_waitcnt lgkmcnt(13)\n" \
"v_mfma_f32_16x16x32_bf16 a[60:63], a[204:207], a[236:239], a[60:63]\n" \
"ds_read_b64_tr_b16 a[182:183], v37 offset:1024\n" \
"v_mov_b32_e32 v216, v213\n" \
"v_mfma_f32_16x16x32_bf16 a[44:47], a[136:139], a[172:175], 0\n" \
"ds_read_b64_tr_b16 a[244:245], v37 offset:8192\n" \
"s_movk_i32 s36, 0x1b0\n" \
"v_mfma_f32_16x16x32_bf16 a[44:47], a[200:203], a[236:239], a[44:47]\n" \
"ds_read_b64_tr_b16 a[246:247], v37 offset:9216\n" \
"v_or_b32_e32 v216, 0x100, v215\n" \
"v_mfma_f32_16x16x32_bf16 a[28:31], a[132:135], a[172:175], 0\n" \
"ds_read_b64_tr_b16 a[184:185], v38 offset:0\n" \
"v_mul_u32_u24_e32 v217, 0x2aab, v216\n" \
"v_mfma_f32_16x16x32_bf16 a[28:31], a[196:199], a[236:239], a[28:31]\n" \
"ds_read_b64_tr_b16 a[186:187], v38 offset:1024\n" \
"v_add_u32_e32 v215, 0xffffff50, v215\n" \
"v_mfma_f32_16x16x32_bf16 a[12:15], a[128:131], a[172:175], 0\n" \
"v_lshrrev_b32_e32 v217, 16, v217\n" \
"v_mov_b32_e64 v220, v215\n" \
"v_mfma_f32_16x16x32_bf16 a[12:15], a[192:195], a[236:239], a[12:15]\n" \
"v_mad_i32_i24 v218, v217, -6, v216\n" \
"v_cmp_gt_u32_e32 vcc, s36, v216\n" \
"s_waitcnt lgkmcnt(15)\n" \
"v_mfma_f32_16x16x32_bf16 a[76:79], a[144:147], a[172:175], 0\n" \
"ds_read_b64_tr_b16 a[248:249], v38 offset:8192\n" \
"v_mov_b32_e64 v221, 7\n" \
"v_mfma_f32_16x16x32_bf16 a[76:79], a[208:211], a[236:239], a[76:79]\n" \
"v_lshrrev_b32_e32 v222, 1, v218\n" \
"v_cndmask_b32_e32 v217, v220, v217, vcc\n" \
"v_mfma_f32_16x16x32_bf16 a[72:75], a[144:147], a[168:171], 0\n" \
"s_movk_i32 s4, 0x48\n" \
"v_add_u32_e32 v220, s35, v217\n" \
"v_mfma_f32_16x16x32_bf16 a[72:75], a[208:211], a[232:235], a[72:75]\n" \
"ds_read_b64_tr_b16 a[250:251], v38 offset:9216\n" \
"v_mul_u32_u24_e32 v222, 0x480, v222\n" \
"v_mfma_f32_16x16x32_bf16 a[68:71], a[144:147], a[164:167], 0\n" \
"v_mad_u32_u24 v222, v220, s5, v222\n" \
"v_cmp_gt_u32_e64 s[4:5], s4, v215\n" \
"v_mfma_f32_16x16x32_bf16 a[68:71], a[208:211], a[228:231], a[68:71]\n" \
"v_cndmask_b32_e32 v215, v221, v218, vcc\n" \
"v_lshrrev_b32_e32 v218, 1, v220\n" \
"v_mfma_f32_16x16x32_bf16 a[64:67], a[144:147], a[160:163], 0\n" \
"v_or_b32_e32 v214, v222, v214\n" \
"v_mov_b32_e32 v222, 2.0\n" \
"v_mfma_f32_16x16x32_bf16 a[64:67], a[208:211], a[224:227], a[64:67]\n" \
"ds_read_b64_tr_b16 a[188:189], v39 offset:0\n" \
"v_and_b32_e64 v218, 4, v218\n" \
"v_mfma_f32_16x16x32_bf16 a[80:83], a[148:151], a[160:163], 0\n" \
"v_and_b32_e64 v217, 2, v217\n" \
"v_bitop3_b32 v215, v215, v218, v217 bitop3:0x1e\n" \
"s_waitcnt lgkmcnt(15)\n" \
"v_mfma_f32_16x16x32_bf16 a[80:83], a[212:215], a[224:227], a[80:83]\n" \
"v_lshlrev_b32_e32 v223, 1, v220\n" \
"v_mov_b32_e64 v224, 2.0\n" \
"v_mfma_f32_16x16x32_bf16 a[84:87], a[148:151], a[164:167], 0\n" \
"ds_read_b64_tr_b16 a[190:191], v39 offset:1024\n" \
"v_cndmask_b32_e64 v193, v222, v214, vcc\n" \
"v_mfma_f32_16x16x32_bf16 a[84:87], a[212:215], a[228:231], a[84:87]\n" \
"v_lshlrev_b32_e32 v214, 7, v220\n" \
"v_lshlrev_b32_e32 v215, 4, v215\n" \
"v_mfma_f32_16x16x32_bf16 a[88:91], a[148:151], a[168:171], 0\n" \
"s_mul_i32 s39, s3, 18\n" \
"v_add3_u32 v214, v214, s34, v215\n" \
"v_mfma_f32_16x16x32_bf16 a[88:91], a[212:215], a[232:235], a[88:91]\n" \
"ds_read_b64_tr_b16 a[252:253], v39 offset:8192\n" \
"v_cndmask_b32_e64 v195, v224, v223, s[4:5]\n" \
"v_mfma_f32_16x16x32_bf16 a[92:95], a[148:151], a[172:175], 0\n" \
"s_movk_i32 s4, 0x1f8\n" \
"v_mov_b32_e64 v215, v213\n" \
"v_mfma_f32_16x16x32_bf16 a[92:95], a[212:215], a[236:239], a[92:95]\n" \
"s_add_i32 s35, s35, s39\n" \
"v_cmp_gt_u32_e32 vcc, s4, v216\n" \
"v_mfma_f32_16x16x32_bf16 a[108:111], a[152:155], a[172:175], 0\n" \
"ds_read_b64_tr_b16 a[254:255], v39 offset:9216\n" \
"v_add_u32_e64 v212, s35, v212\n" \
"v_mfma_f32_16x16x32_bf16 a[108:111], a[216:219], a[236:239], a[108:111]\n" \
"v_mov_b32_e32 v219, 2.0\n" \
"v_cndmask_b32_e32 v197, v215, v214, vcc\n" \
"v_mfma_f32_16x16x32_bf16 a[104:107], a[152:155], a[168:171], 0\n" \
"s_movk_i32 s35, 0x120\n" \
"v_mul_u32_u24_e32 v214, 0xd80, v212\n" \
"v_mfma_f32_16x16x32_bf16 a[104:107], a[216:219], a[232:235], a[104:107]\n" \
"v_cmp_gt_u32_e32 vcc, s35, v20\n" \
"v_lshrrev_b32_e64 v215, 1, v212\n" \
"v_mfma_f32_16x16x32_bf16 a[100:103], a[152:155], a[164:167], 0\n" \
"s_and_b32 s4, s2, 7\n" \
"v_cndmask_b32_e32 v194, v219, v214, vcc\n" \
"v_mfma_f32_16x16x32_bf16 a[100:103], a[216:219], a[228:231], a[100:103]\n" \
"v_lshlrev_b32_e32 v214, 7, v212\n" \
"v_lshrrev_b32_e32 v212, 2, v212\n" \
"v_mfma_f32_16x16x32_bf16 a[96:99], a[152:155], a[160:163], 0\n" \
"v_and_b32_e64 v212, 2, v212\n" \
"v_and_or_b32 v212, v215, 1, v212\n" \
"v_mfma_f32_16x16x32_bf16 a[96:99], a[216:219], a[224:227], a[96:99]\n" \
"s_lshl_b32 s5, s4, 2\n" \
"s_bfe_u32 s38, s2, 0x20003\n" \
"s_waitcnt lgkmcnt(15)\n" \
"v_mfma_f32_16x16x32_bf16 a[112:115], a[156:159], a[160:163], 0\n" \
"v_lshlrev_b32_e32 v212, 5, v212\n" \
"s_movk_i32 s35, 0x60\n" \
"v_mfma_f32_16x16x32_bf16 a[112:115], a[220:223], a[224:227], a[112:115]\n" \
"s_or_b32 s5, s5, s38\n" \
"v_bitop3_b32 v212, v212, s35, v214 bitop3:0x36\n" \
"v_mfma_f32_16x16x32_bf16 a[116:119], a[156:159], a[164:167], 0\n" \
"s_lshr_b32 s36, s2, 3\n" \
"v_add_u32_e64 v212, s34, v212\n" \
"v_mfma_f32_16x16x32_bf16 a[116:119], a[220:223], a[228:231], a[116:119]\n" \
"s_mul_i32 s34, s5, 0x12000\n" \
"s_or_b32 s40, s34, 0x6000000\n" \
"v_mfma_f32_16x16x32_bf16 a[120:123], a[156:159], a[168:171], 0\n" \
"s_mul_i32 s34, s36, 0x90000\n" \
"s_lshl_b32 s36, s3, 14\n" \
"v_mfma_f32_16x16x32_bf16 a[120:123], a[220:223], a[232:235], a[120:123]\n" \
"s_add_i32 s34, s34, s36\n" \
"s_lshl_b32 s38, s5, 8\n" \
"v_mfma_f32_16x16x32_bf16 a[124:127], a[156:159], a[172:175], 0\n" \
"s_add_i32 s41, s34, 0x4240000\n" \
"s_mul_i32 s34, s5, 36\n" \
"v_mfma_f32_16x16x32_bf16 a[124:127], a[220:223], a[236:239], a[124:127]\n" \
"s_waitcnt vmcnt(4) lgkmcnt(0)\n" \
"s_barrier\n" \
"v_mfma_f32_16x16x32_bf16 v[52:55], a[128:131], a[176:179], 0\n" \
"s_add_u32 m0, s30, 0x0\n" \
"s_movk_i32 s84, 0x100\n" \
"v_mfma_f32_16x16x32_bf16 v[52:55], a[192:195], a[240:243], v[52:55]\n" \
"buffer_load_dwordx4 v22, s[64:67], s84 offen offset:0 lds\n" \
"v_mfma_f32_16x16x32_bf16 v[56:59], a[128:131], a[180:183], 0\n" \
"ds_read_b64_tr_b16 a[160:161], v32 offset:32768\n" \
"s_add_i32 s5, s7, s40\n" \
"v_mfma_f32_16x16x32_bf16 v[56:59], a[192:195], a[244:247], v[56:59]\n" \
"s_movk_i32 s84, 0x1d00\n" \
"v_mfma_f32_16x16x32_bf16 v[60:63], a[128:131], a[184:187], 0\n" \
"buffer_load_dwordx4 v23, s[64:67], s84 offen offset:1024 lds\n" \
"v_mfma_f32_16x16x32_bf16 v[60:63], a[192:195], a[248:251], v[60:63]\n" \
"ds_read_b64_tr_b16 a[162:163], v32 offset:33792\n" \
"s_addk_i32 s5, 0x2000\n" \
"v_mfma_f32_16x16x32_bf16 v[64:67], a[128:131], a[188:191], 0\n" \
"ds_read_b64_tr_b16 a[224:225], v32 offset:40960\n" \
"s_mov_b32 s37, 2.0\n" \
"v_mfma_f32_16x16x32_bf16 v[64:67], a[192:195], a[252:255], v[64:67]\n" \
"ds_read_b128 a[128:131], v28 offset:49152\n" \
"s_or_b32 s38, s38, 0x5be8000\n" \
"v_mfma_f32_16x16x32_bf16 v[80:83], a[132:135], a[188:191], 0\n" \
"ds_read_b128 a[192:195], v29 offset:49152\n" \
"v_mfma_f32_16x16x32_bf16 v[80:83], a[196:199], a[252:255], v[80:83]\n" \
"s_movk_i32 s84, 0x3900\n" \
"v_mfma_f32_16x16x32_bf16 v[76:79], a[132:135], a[184:187], 0\n" \
"buffer_load_dwordx4 v22, s[64:67], s84 offen offset:2048 lds\n" \
"v_mfma_f32_16x16x32_bf16 v[76:79], a[196:199], a[248:251], v[76:79]\n" \
"s_movk_i32 s84, 0x5500\n" \
"v_mfma_f32_16x16x32_bf16 v[72:75], a[132:135], a[180:183], 0\n" \
"buffer_load_dwordx4 v23, s[64:67], s84 offen offset:3072 lds\n" \
"v_mfma_f32_16x16x32_bf16 v[72:75], a[196:199], a[244:247], v[72:75]\n" \
"s_add_u32 m0, s31, 0x0\n" \
"s_mov_b32 s84, 0x100000\n" \
"v_mfma_f32_16x16x32_bf16 v[68:71], a[132:135], a[176:179], 0\n" \
"buffer_load_dwordx4 v26, s[68:71], s84 offen offset:0 lds\n" \
"v_mfma_f32_16x16x32_bf16 v[68:71], a[196:199], a[240:243], v[68:71]\n" \
"ds_read_b128 a[132:135], v28 offset:51200\n" \
"v_mfma_f32_16x16x32_bf16 v[84:87], a[136:139], a[176:179], 0\n" \
"ds_read_b128 a[196:199], v29 offset:51200\n" \
"v_mfma_f32_16x16x32_bf16 v[84:87], a[200:203], a[240:243], v[84:87]\n" \
"s_addk_i32 s84, 0x7c00\n" \
"v_mfma_f32_16x16x32_bf16 v[88:91], a[136:139], a[180:183], 0\n" \
"buffer_load_dwordx4 v26, s[68:71], s84 offen offset:1024 lds\n" \
"v_mfma_f32_16x16x32_bf16 v[88:91], a[200:203], a[244:247], v[88:91]\n" \
"ds_read_b64_tr_b16 a[226:227], v32 offset:41984\n" \
"v_mfma_f32_16x16x32_bf16 v[92:95], a[136:139], a[184:187], 0\n" \
"s_addk_i32 s84, 0x7c00\n" \
"v_mfma_f32_16x16x32_bf16 v[92:95], a[200:203], a[248:251], v[92:95]\n" \
"buffer_load_dwordx4 v27, s[68:71], s84 offen offset:2048 lds\n" \
"v_mfma_f32_16x16x32_bf16 v[96:99], a[136:139], a[188:191], 0\n" \
"ds_read_b64_tr_b16 a[164:165], v33 offset:32768\n" \
"v_mfma_f32_16x16x32_bf16 v[96:99], a[200:203], a[252:255], v[96:99]\n" \
"ds_read_b128 a[136:139], v28 offset:53248\n" \
"v_mfma_f32_16x16x32_bf16 v[112:115], a[140:143], a[188:191], 0\n" \
"ds_read_b128 a[200:203], v29 offset:53248\n" \
"v_mfma_f32_16x16x32_bf16 v[112:115], a[204:207], a[252:255], v[112:115]\n" \
"s_addk_i32 s84, 0x7c00\n" \
"v_mfma_f32_16x16x32_bf16 v[108:111], a[140:143], a[184:187], 0\n" \
"buffer_load_dwordx4 v27, s[68:71], s84 offen offset:3072 lds\n" \
"v_mfma_f32_16x16x32_bf16 v[108:111], a[204:207], a[248:251], v[108:111]\n" \
"s_add_u32 m0, s30, 0x1000\n" \
"s_mov_b32 s84, 0x8100\n" \
"v_mfma_f32_16x16x32_bf16 v[104:107], a[140:143], a[180:183], 0\n" \
"buffer_load_dwordx4 v22, s[64:67], s84 offen offset:0 lds\n" \
"v_mfma_f32_16x16x32_bf16 v[104:107], a[204:207], a[244:247], v[104:107]\n" \
"ds_read_b64_tr_b16 a[166:167], v33 offset:33792\n" \
"v_mfma_f32_16x16x32_bf16 v[100:103], a[140:143], a[176:179], 0\n" \
"ds_read_b64_tr_b16 a[228:229], v33 offset:40960\n" \
"v_mfma_f32_16x16x32_bf16 v[100:103], a[204:207], a[240:243], v[100:103]\n" \
"ds_read_b128 a[140:143], v28 offset:55296\n" \
"v_mfma_f32_16x16x32_bf16 v[116:119], a[144:147], a[176:179], 0\n" \
"ds_read_b128 a[204:207], v29 offset:55296\n" \
"v_mfma_f32_16x16x32_bf16 v[116:119], a[208:211], a[240:243], v[116:119]\n" \
"s_addk_i32 s84, 0x1c00\n" \
"v_mfma_f32_16x16x32_bf16 v[120:123], a[144:147], a[180:183], 0\n" \
"buffer_load_dwordx4 v23, s[64:67], s84 offen offset:1024 lds\n" \
"v_mfma_f32_16x16x32_bf16 v[120:123], a[208:211], a[244:247], v[120:123]\n" \
"s_addk_i32 s84, 0x1c00\n" \
"v_mfma_f32_16x16x32_bf16 v[124:127], a[144:147], a[184:187], 0\n" \
"buffer_load_dwordx4 v22, s[64:67], s84 offen offset:2048 lds\n" \
"v_mfma_f32_16x16x32_bf16 v[124:127], a[208:211], a[248:251], v[124:127]\n" \
"s_addk_i32 s84, 0x1c00\n" \
"s_nop 0\n" \
"v_mfma_f32_16x16x32_bf16 v[128:131], a[144:147], a[188:191], 0\n" \
"buffer_load_dwordx4 v23, s[64:67], s84 offen offset:3072 lds\n" \
"v_mfma_f32_16x16x32_bf16 v[128:131], a[208:211], a[252:255], v[128:131]\n" \
"ds_read_b128 a[144:147], v28 offset:32768\n" \
"v_mfma_f32_16x16x32_bf16 v[144:147], a[148:151], a[188:191], 0\n" \
"ds_read_b128 a[208:211], v29 offset:32768\n" \
"v_mfma_f32_16x16x32_bf16 v[144:147], a[212:215], a[252:255], v[144:147]\n" \
"s_add_u32 m0, s31, 0x4000\n" \
"s_mov_b32 s84, 0x100000\n" \
"v_mfma_f32_16x16x32_bf16 v[140:143], a[148:151], a[184:187], 0\n" \
"buffer_load_dwordx4 v26, s[72:75], s84 offen offset:0 lds\n" \
"v_mfma_f32_16x16x32_bf16 v[140:143], a[212:215], a[248:251], v[140:143]\n" \
"ds_read_b64_tr_b16 a[230:231], v33 offset:41984\n" \
"v_mfma_f32_16x16x32_bf16 v[136:139], a[148:151], a[180:183], 0\n" \
"s_addk_i32 s84, 0x7c00\n" \
"s_nop 0\n" \
"v_mfma_f32_16x16x32_bf16 v[136:139], a[212:215], a[244:247], v[136:139]\n" \
"buffer_load_dwordx4 v26, s[72:75], s84 offen offset:1024 lds\n" \
"v_mfma_f32_16x16x32_bf16 v[132:135], a[148:151], a[176:179], 0\n" \
"ds_read_b64_tr_b16 a[168:169], v34 offset:32768\n" \
"v_mfma_f32_16x16x32_bf16 v[132:135], a[212:215], a[240:243], v[132:135]\n" \
"ds_read_b128 a[148:151], v28 offset:34816\n" \
".fill (L_pb + 44768 - .) / 4, 4, 0xBF800000\n" \
"L_pp11:\n" \
"s_cbranch_execz L_pret\n" \
"s_nop 0\n" \
"v_mfma_f32_16x16x32_bf16 v[148:151], a[152:155], a[176:179], 0\n" \
"ds_read_b128 a[212:215], v29 offset:34816\n" \
"v_mfma_f32_16x16x32_bf16 v[148:151], a[216:219], a[240:243], v[148:151]\n" \
"ds_read_b64_tr_b16 a[170:171], v34 offset:33792\n" \
"v_mfma_f32_16x16x32_bf16 v[152:155], a[152:155], a[180:183], 0\n" \
"ds_read_b64_tr_b16 a[232:233], v34 offset:40960\n" \
"v_mfma_f32_16x16x32_bf16 v[152:155], a[216:219], a[244:247], v[152:155]\n" \
"ds_read_b64_tr_b16 a[234:235], v34 offset:41984\n" \
"v_mfma_f32_16x16x32_bf16 v[156:159], a[152:155], a[184:187], 0\n" \
"ds_read_b64_tr_b16 a[172:173], v35 offset:32768\n" \
"v_mfma_f32_16x16x32_bf16 v[156:159], a[216:219], a[248:251], v[156:159]\n" \
"ds_read_b64_tr_b16 a[174:175], v35 offset:33792\n" \
"v_mfma_f32_16x16x32_bf16 v[160:163], a[152:155], a[188:191], 0\n" \
"ds_read_b64_tr_b16 a[236:237], v35 offset:40960\n" \
"v_mfma_f32_16x16x32_bf16 v[160:163], a[216:219], a[252:255], v[160:163]\n" \
"ds_read_b128 a[152:155], v28 offset:36864\n" \
"v_mfma_f32_16x16x32_bf16 v[176:179], a[156:159], a[188:191], 0\n" \
"ds_read_b128 a[216:219], v29 offset:36864\n" \
"v_mfma_f32_16x16x32_bf16 v[176:179], a[220:223], a[252:255], v[176:179]\n" \
"ds_read_b64_tr_b16 a[238:239], v35 offset:41984\n" \
"v_mfma_f32_16x16x32_bf16 v[172:175], a[156:159], a[184:187], 0\n" \
"s_cmp_lg_u32 s3, 0\n" \
"s_cselect_b32 s36, s37, s5\n" \
"v_mfma_f32_16x16x32_bf16 v[172:175], a[220:223], a[248:251], v[172:175]\n" \
"s_lshl_b32 s5, s6, 2\n" \
"s_or_b32 s5, s38, s5\n" \
"v_mfma_f32_16x16x32_bf16 v[168:171], a[156:159], a[180:183], 0\n" \
"s_lshl_b32 s35, s3, 10\n" \
"v_mfma_f32_16x16x32_bf16 v[168:171], a[220:223], a[244:247], v[168:171]\n" \
"s_cmpk_lt_u32 s3, 3\n" \
"s_cselect_b32 s37, s37, s5\n" \
"v_mfma_f32_16x16x32_bf16 v[164:167], a[156:159], a[176:179], 0\n" \
"s_lshl_b32 s5, s4, 16\n" \
"s_lshl_b32 s4, s4, 11\n" \
"v_mfma_f32_16x16x32_bf16 v[164:167], a[220:223], a[240:243], v[164:167]\n" \
"ds_read_b128 a[156:159], v28 offset:38912\n" \
"ds_read_b128 a[220:223], v29 offset:38912\n" \
"s_waitcnt lgkmcnt(15)\n" \
"v_mfma_f32_16x16x32_bf16 a[0:3], a[128:131], a[160:163], a[0:3]\n" \
"s_addk_i32 s84, 0x7c00\n" \
"v_mfma_f32_16x16x32_bf16 a[0:3], a[192:195], a[224:227], a[0:3]\n" \
"buffer_load_dwordx4 v27, s[72:75], s84 offen offset:2048 lds\n" \
"v_mfma_f32_16x16x32_bf16 a[4:7], a[128:131], a[164:167], a[4:7]\n" \
"ds_read_b64_tr_b16 a[176:177], v36 offset:32768\n" \
"v_cndmask_b32_e32 v198, v213, v212, vcc\n" \
"s_waitcnt lgkmcnt(15)\n" \
"v_mfma_f32_16x16x32_bf16 a[4:7], a[192:195], a[228:231], a[4:7]\n" \
"s_addk_i32 s84, 0x7c00\n" \
"s_waitcnt lgkmcnt(11)\n" \
"v_mfma_f32_16x16x32_bf16 a[8:11], a[128:131], a[168:171], a[8:11]\n" \
"buffer_load_dwordx4 v27, s[72:75], s84 offen offset:3072 lds\n" \
"s_waitcnt lgkmcnt(9)\n" \
"v_mfma_f32_16x16x32_bf16 a[8:11], a[192:195], a[232:235], a[8:11]\n" \
"ds_read_b64_tr_b16 a[178:179], v36 offset:33792\n" \
"v_or_b32_e32 v212, s35, v20\n" \
"s_waitcnt lgkmcnt(8)\n" \
"v_mfma_f32_16x16x32_bf16 a[12:15], a[128:131], a[172:175], a[12:15]\n" \
"ds_read_b64_tr_b16 a[240:241], v36 offset:40960\n" \
"s_or_b32 s39, s40, s35\n" \
"s_waitcnt lgkmcnt(5)\n" \
"v_mfma_f32_16x16x32_bf16 a[12:15], a[192:195], a[236:239], a[12:15]\n" \
"s_or_b32 s55, s41, s4\n" \
"v_or_b32_e32 v199, 0x22000, v212\n" \
"v_mfma_f32_16x16x32_bf16 a[28:31], a[132:135], a[172:175], a[28:31]\n" \
"s_add_i32 s35, s39, s7\n" \
"s_add_i32 s54, s41, s5\n" \
"v_mfma_f32_16x16x32_bf16 a[28:31], a[196:199], a[236:239], a[28:31]\n" \
"ds_read_b64_tr_b16 a[242:243], v36 offset:41984\n" \
"s_add_i32 s55, s55, 0x80000\n" \
"v_mfma_f32_16x16x32_bf16 a[24:27], a[132:135], a[168:171], a[24:27]\n" \
"v_mov_b32_e32 v215, 0xffff\n" \
"s_and_b32 s4, s3, 1\n" \
"v_mfma_f32_16x16x32_bf16 a[24:27], a[196:199], a[232:235], a[24:27]\n" \
"s_lshl_b32 s5, s4, 4\n" \
"v_lshrrev_b32_e32 v212, 4, v20\n" \
"v_mfma_f32_16x16x32_bf16 a[20:23], a[132:135], a[164:167], a[20:23]\n" \
"v_lshrrev_b32_e32 v218, 6, v20\n" \
"v_and_b32_e32 v219, 12, v218\n" \
"v_mfma_f32_16x16x32_bf16 a[20:23], a[196:199], a[228:231], a[20:23]\n" \
"ds_read_b64_tr_b16 a[180:181], v37 offset:32768\n" \
"v_and_or_b32 v214, v212, 15, s5\n" \
"v_mfma_f32_16x16x32_bf16 a[16:19], a[132:135], a[160:163], a[16:19]\n" \
"v_cmp_gt_u32_e64 vcc, v219, v214\n" \
"v_mov_b32_e32 v220, 0xffff0000\n" \
"v_mfma_f32_16x16x32_bf16 a[16:19], a[196:199], a[224:227], a[16:19]\n" \
"s_lshl_b32 s5, s2, 2\n" \
"v_cndmask_b32_e64 v217, v215, 0, vcc\n" \
"v_mfma_f32_16x16x32_bf16 a[32:35], a[136:139], a[160:163], a[32:35]\n" \
"s_mov_b32 m0, s32\n" \
"ds_read_b64_tr_b16 a[182:183], v37 offset:33792\n" \
"v_mfma_f32_16x16x32_bf16 a[32:35], a[200:203], a[224:227], a[32:35]\n" \
"s_mov_b32 exec_lo, 0x0\n" \
"s_mov_b32 exec_hi, 0xff\n" \
"global_load_lds_dword v[184:185], off\n" \
"s_mov_b64 exec, -1\n" \
"v_mfma_f32_16x16x32_bf16 a[36:39], a[136:139], a[164:167], a[36:39]\n" \
"v_cmp_lt_u32_e32 vcc, v219, v214\n" \
"v_mov_b32_e32 v213, s23\n" \
"v_mfma_f32_16x16x32_bf16 a[36:39], a[200:203], a[228:231], a[36:39]\n" \
"ds_read_b64_tr_b16 a[244:245], v37 offset:40960\n" \
"s_or_b32 s5, s5, s3\n" \
"v_mfma_f32_16x16x32_bf16 a[40:43], a[136:139], a[168:171], a[40:43]\n" \
"v_cndmask_b32_e32 v221, 0, v220, vcc\n" \
"v_or_b32_e64 v12, v217, v221\n" \
"v_mfma_f32_16x16x32_bf16 a[40:43], a[200:203], a[232:235], a[40:43]\n" \
"s_mov_b32 m0, s32\n" \
"v_or_b32_e32 v217, 2, v219\n" \
"v_mfma_f32_16x16x32_bf16 a[44:47], a[136:139], a[172:175], a[44:47]\n" \
"s_mov_b32 exec_lo, 0xffff0000\n" \
"s_mov_b32 exec_hi, 0x0\n" \
"global_load_lds_dword v[186:187], off\n" \
"s_mov_b64 exec, -1\n" \
"v_mfma_f32_16x16x32_bf16 a[44:47], a[200:203], a[236:239], a[44:47]\n" \
"ds_read_b64_tr_b16 a[246:247], v37 offset:41984\n" \
"v_cmp_gt_u32_e64 vcc, v217, v214\n" \
"v_mfma_f32_16x16x32_bf16 a[60:63], a[140:143], a[172:175], a[60:63]\n" \
"v_mov_b32_e32 v216, s25\n" \
"s_movk_i32 s40, 0xf400\n" \
"v_mfma_f32_16x16x32_bf16 a[60:63], a[204:207], a[236:239], a[60:63]\n" \
"v_cndmask_b32_e64 v221, v215, 0, vcc\n" \
"v_cmp_lt_u32_e64 vcc, v217, v214\n" \
"v_mfma_f32_16x16x32_bf16 a[56:59], a[140:143], a[168:171], a[56:59]\n" \
"ds_read_b64_tr_b16 a[184:185], v38 offset:32768\n" \
"s_lshl_b32 s4, s4, 6\n" \
"v_mfma_f32_16x16x32_bf16 a[56:59], a[204:207], a[232:235], a[56:59]\n" \
"v_cndmask_b32_e32 v217, 0, v220, vcc\n" \
"v_or_b32_e64 v13, v221, v217\n" \
"v_mfma_f32_16x16x32_bf16 a[52:55], a[140:143], a[164:167], a[52:55]\n" \
"v_or_b32_e32 v217, 16, v219\n" \
"v_cmp_gt_u32_e32 vcc, v217, v214\n" \
"v_mfma_f32_16x16x32_bf16 a[52:55], a[204:207], a[228:231], a[52:55]\n" \
"ds_read_b64_tr_b16 a[186:187], v38 offset:33792\n" \
"v_cndmask_b32_e64 v221, v215, 0, vcc\n" \
"v_mfma_f32_16x16x32_bf16 a[48:51], a[140:143], a[160:163], a[48:51]\n" \
"v_cmp_lt_u32_e32 vcc, v217, v214\n" \
"s_nop 1\n" \
"v_cndmask_b32_e64 v217, 0, v220, vcc\n" \
"v_mfma_f32_16x16x32_bf16 a[48:51], a[204:207], a[224:227], a[48:51]\n" \
"v_or_b32_e32 v14, v221, v217\n" \
"v_or_b32_e32 v217, 18, v219\n" \
"v_mfma_f32_16x16x32_bf16 a[64:67], a[144:147], a[160:163], a[64:67]\n" \
"ds_read_b64_tr_b16 a[248:249], v38 offset:40960\n" \
"v_cmp_gt_u32_e64 vcc, v217, v214\n" \
"v_mfma_f32_16x16x32_bf16 a[64:67], a[208:211], a[224:227], a[64:67]\n" \
"s_nop 0\n" \
"v_cndmask_b32_e64 v215, v215, 0, vcc\n" \
"v_cmp_lt_u32_e32 vcc, v217, v214\n" \
"v_mfma_f32_16x16x32_bf16 a[68:71], a[144:147], a[164:167], a[68:71]\n" \
"s_nop 0\n" \
"v_cndmask_b32_e32 v214, 0, v220, vcc\n" \
"v_or_b32_e64 v15, v215, v214\n" \
"v_mfma_f32_16x16x32_bf16 a[68:71], a[208:211], a[228:231], a[68:71]\n" \
"ds_read_b64_tr_b16 a[250:251], v38 offset:41984\n" \
"v_mad_u64_u32 v[214:215], s[6:7], s5, 42, v[212:213]\n" \
"v_mfma_f32_16x16x32_bf16 a[72:75], a[144:147], a[168:171], a[72:75]\n" \
"s_movk_i32 s5, 0x1440\n" \
"v_cmp_gt_u32_e32 vcc, s5, v214\n" \
"v_mfma_f32_16x16x32_bf16 a[72:75], a[208:211], a[232:235], a[72:75]\n" \
"v_min_u32_e32 v217, 0xa43f, v214\n" \
"v_mov_b32_e64 v214, s24\n" \
"v_mfma_f32_16x16x32_bf16 a[76:79], a[144:147], a[172:175], a[76:79]\n" \
"v_cndmask_b32_e32 v215, v216, v213, vcc\n" \
"v_mov_b32_e32 v213, s22\n" \
"v_mfma_f32_16x16x32_bf16 a[76:79], a[208:211], a[236:239], a[76:79]\n" \
"ds_read_b64_tr_b16 a[188:189], v39 offset:32768\n" \
"s_movk_i32 s6, 0xffee\n" \
"v_mfma_f32_16x16x32_bf16 a[92:95], a[148:151], a[172:175], a[92:95]\n" \
"v_cndmask_b32_e32 v214, v214, v213, vcc\n" \
"v_add_u32_e32 v213, 0xffffebc0, v217\n" \
"v_mfma_f32_16x16x32_bf16 a[92:95], a[212:215], a[236:239], a[92:95]\n" \
"s_lshl_b32 s5, s2, 5\n" \
"v_cndmask_b32_e32 v213, v213, v217, vcc\n" \
"v_mfma_f32_16x16x32_bf16 a[88:91], a[148:151], a[168:171], a[88:91]\n" \
"ds_read_b64_tr_b16 a[190:191], v39 offset:33792\n" \
"v_lshlrev_b32_e64 v216, 7, v213\n" \
"v_mfma_f32_16x16x32_bf16 a[88:91], a[212:215], a[232:235], a[88:91]\n" \
"v_mul_u32_u24_e32 v213, 0xe39, v212\n" \
"v_mov_b32_e64 v217, 0\n" \
"v_mfma_f32_16x16x32_bf16 a[84:87], a[148:151], a[164:167], a[84:87]\n" \
"v_lshrrev_b32_e64 v213, 16, v213\n" \
"v_lshl_add_u64 v[16:17], v[214:215], 0, v[216:217]\n" \
"v_mfma_f32_16x16x32_bf16 a[84:87], a[212:215], a[228:231], a[84:87]\n" \
"ds_read_b64_tr_b16 a[252:253], v39 offset:40960\n" \
"v_mad_i32_i24 v214, v213, s6, v212\n" \
"v_mfma_f32_16x16x32_bf16 a[80:83], a[148:151], a[160:163], a[80:83]\n" \
"s_add_i32 s5, s5, 32\n" \
"v_lshlrev_b32_e32 v215, 4, v214\n" \
"v_mfma_f32_16x16x32_bf16 a[80:83], a[212:215], a[224:227], a[80:83]\n" \
"v_add_u32_e64 v216, s5, v215\n" \
"v_and_b32_e32 v216, 0xf0, v216\n" \
"v_mfma_f32_16x16x32_bf16 a[96:99], a[152:155], a[160:163], a[96:99]\n" \
"ds_read_b64_tr_b16 a[254:255], v39 offset:41984\n" \
"v_cmp_gt_u32_e64 vcc, 16, v214\n" \
"v_mfma_f32_16x16x32_bf16 a[96:99], a[216:219], a[224:227], a[96:99]\n" \
"s_movk_i32 s7, 0xf800\n" \
"v_cndmask_b32_e32 v214, v215, v216, vcc\n" \
"v_mfma_f32_16x16x32_bf16 a[100:103], a[152:155], a[164:167], a[100:103]\n" \
"v_or_b32_e64 v215, 64, v212\n" \
"v_mul_u32_u24_e32 v216, 0xe39, v215\n" \
"v_mfma_f32_16x16x32_bf16 a[100:103], a[216:219], a[228:231], a[100:103]\n" \
"v_lshrrev_b32_e64 v216, 16, v216\n" \
"v_mad_i32_i24 v215, v216, s6, v215\n" \
"v_mfma_f32_16x16x32_bf16 a[104:107], a[152:155], a[168:171], a[104:107]\n" \
"v_lshlrev_b32_e32 v217, 4, v215\n" \
"v_add_u32_e32 v220, s5, v217\n" \
"v_mfma_f32_16x16x32_bf16 a[104:107], a[216:219], a[232:235], a[104:107]\n" \
"v_and_b32_e32 v220, 0xf0, v220\n" \
"v_cmp_gt_u32_e64 vcc, 16, v215\n" \
"v_mfma_f32_16x16x32_bf16 a[108:111], a[152:155], a[172:175], a[108:111]\n" \
"s_nop 0\n" \
"v_cndmask_b32_e32 v215, v217, v220, vcc\n" \
"v_or_b32_e32 v217, 0x80, v212\n" \
"v_mfma_f32_16x16x32_bf16 a[108:111], a[216:219], a[236:239], a[108:111]\n" \
"v_mul_u32_u24_e32 v220, 0xe39, v217\n" \
"v_lshrrev_b32_e32 v220, 16, v220\n" \
"s_waitcnt lgkmcnt(15)\n" \
"v_mfma_f32_16x16x32_bf16 a[124:127], a[156:159], a[172:175], a[124:127]\n" \
"v_mad_i32_i24 v217, v220, s6, v217\n" \
"v_lshlrev_b32_e64 v221, 4, v217\n" \
"v_mfma_f32_16x16x32_bf16 a[124:127], a[220:223], a[236:239], a[124:127]\n" \
"v_add_u32_e64 v222, s5, v221\n" \
"v_and_b32_e32 v222, 0xf0, v222\n" \
"v_mfma_f32_16x16x32_bf16 a[120:123], a[156:159], a[168:171], a[120:123]\n" \
"v_cmp_gt_u32_e32 vcc, 16, v217\n" \
"s_nop 1\n" \
"v_cndmask_b32_e64 v217, v221, v222, vcc\n" \
"v_mfma_f32_16x16x32_bf16 a[120:123], a[220:223], a[232:235], a[120:123]\n" \
"v_or_b32_e32 v221, 0xc0, v212\n" \
"v_mul_u32_u24_e32 v222, 0xe39, v221\n" \
"v_mfma_f32_16x16x32_bf16 a[116:119], a[156:159], a[164:167], a[116:119]\n" \
"v_lshrrev_b32_e64 v222, 16, v222\n" \
"v_mad_i32_i24 v221, v222, s6, v221\n" \
"v_mfma_f32_16x16x32_bf16 a[116:119], a[220:223], a[228:231], a[116:119]\n" \
"v_lshlrev_b32_e32 v223, 4, v221\n" \
"v_add_u32_e32 v224, s5, v223\n" \
"v_mfma_f32_16x16x32_bf16 a[112:115], a[156:159], a[160:163], a[112:115]\n" \
"v_and_b32_e32 v224, 0xf0, v224\n" \
"v_cmp_gt_u32_e64 vcc, 16, v221\n" \
"v_mfma_f32_16x16x32_bf16 a[112:115], a[220:223], a[224:227], a[112:115]\n" \
"s_waitcnt vmcnt(20)\n" \
"v_readfirstlane_b32 s62, v180\n" \
"s_nop 3\n" \
"s_add_u32 s62, s62, 0x9E3779B1\n" \
"s_waitcnt vmcnt(2) lgkmcnt(0)\n" \
"s_barrier\n" \
"v_mfma_f32_16x16x32_bf16 v[52:55], a[128:131], a[176:179], v[52:55]\n" \
"s_add_u32 m0, s30, 0x8000\n" \
"s_movk_i32 s84, 0x180\n" \
"v_mfma_f32_16x16x32_bf16 v[52:55], a[192:195], a[240:243], v[52:55]\n" \
"buffer_load_dwordx4 v22, s[64:67], s84 offen offset:0 lds\n" \
"v_mfma_f32_16x16x32_bf16 v[56:59], a[128:131], a[180:183], v[56:59]\n" \
"ds_read_b64_tr_b16 a[160:161], v32 offset:0\n" \
"v_cndmask_b32_e64 v221, v223, v224, vcc\n" \
"v_mfma_f32_16x16x32_bf16 v[56:59], a[192:195], a[244:247], v[56:59]\n" \
"s_movk_i32 s84, 0x1d80\n" \
"v_mfma_f32_16x16x32_bf16 v[60:63], a[128:131], a[184:187], v[60:63]\n" \
"buffer_load_dwordx4 v23, s[64:67], s84 offen offset:1024 lds\n" \
"v_mfma_f32_16x16x32_bf16 v[60:63], a[192:195], a[248:251], v[60:63]\n" \
"ds_read_b64_tr_b16 a[162:163], v32 offset:1024\n" \
"v_or_b32_e32 v223, 0x100, v212\n" \
"v_mfma_f32_16x16x32_bf16 v[64:67], a[128:131], a[188:191], v[64:67]\n" \
"ds_read_b64_tr_b16 a[224:225], v32 offset:8192\n" \
"v_mul_u32_u24_e32 v224, 0xe39, v223\n" \
"v_mfma_f32_16x16x32_bf16 v[64:67], a[192:195], a[252:255], v[64:67]\n" \
"ds_read_b128 a[128:131], v28 offset:16384\n" \
"v_lshrrev_b32_e32 v224, 16, v224\n" \
"v_mfma_f32_16x16x32_bf16 v[80:83], a[132:135], a[188:191], v[80:83]\n" \
"ds_read_b128 a[192:195], v29 offset:16384\n" \
"v_mad_i32_i24 v223, v224, s6, v223\n" \
"v_mfma_f32_16x16x32_bf16 v[80:83], a[196:199], a[252:255], v[80:83]\n" \
"s_movk_i32 s84, 0x3980\n" \
"v_mfma_f32_16x16x32_bf16 v[76:79], a[132:135], a[184:187], v[76:79]\n" \
"buffer_load_dwordx4 v22, s[64:67], s84 offen offset:2048 lds\n" \
"v_mfma_f32_16x16x32_bf16 v[76:79], a[196:199], a[248:251], v[76:79]\n" \
"s_movk_i32 s84, 0x5580\n" \
"v_mfma_f32_16x16x32_bf16 v[72:75], a[132:135], a[180:183], v[72:75]\n" \
"buffer_load_dwordx4 v23, s[64:67], s84 offen offset:3072 lds\n" \
"v_mfma_f32_16x16x32_bf16 v[72:75], a[196:199], a[244:247], v[72:75]\n" \
"s_add_u32 m0, s31, 0x8000\n" \
"s_mov_b32 s84, 0x180000\n" \
"v_mfma_f32_16x16x32_bf16 v[68:71], a[132:135], a[176:179], v[68:71]\n" \
"buffer_load_dwordx4 v26, s[68:71], s84 offen offset:0 lds\n" \
"v_mfma_f32_16x16x32_bf16 v[68:71], a[196:199], a[240:243], v[68:71]\n" \
"ds_read_b128 a[132:135], v28 offset:18432\n" \
"v_lshlrev_b32_e64 v225, 4, v223\n" \
"v_mfma_f32_16x16x32_bf16 v[84:87], a[136:139], a[176:179], v[84:87]\n" \
"ds_read_b128 a[196:199], v29 offset:18432\n" \
"v_add_u32_e64 v226, s5, v225\n" \
"v_mfma_f32_16x16x32_bf16 v[84:87], a[200:203], a[240:243], v[84:87]\n" \
"s_addk_i32 s84, 0x7c00\n" \
"v_mfma_f32_16x16x32_bf16 v[88:91], a[136:139], a[180:183], v[88:91]\n" \
"buffer_load_dwordx4 v26, s[68:71], s84 offen offset:1024 lds\n" \
"v_mfma_f32_16x16x32_bf16 v[88:91], a[200:203], a[244:247], v[88:91]\n" \
"ds_read_b64_tr_b16 a[226:227], v32 offset:9216\n" \
"v_and_b32_e32 v226, 0xf0, v226\n" \
"v_mfma_f32_16x16x32_bf16 v[92:95], a[136:139], a[184:187], v[92:95]\n" \
"s_addk_i32 s84, 0x7c00\n" \
"v_mfma_f32_16x16x32_bf16 v[92:95], a[200:203], a[248:251], v[92:95]\n" \
"buffer_load_dwordx4 v27, s[68:71], s84 offen offset:2048 lds\n" \
"v_mfma_f32_16x16x32_bf16 v[96:99], a[136:139], a[188:191], v[96:99]\n" \
"ds_read_b64_tr_b16 a[164:165], v33 offset:0\n" \
"v_cmp_gt_u32_e64 vcc, 16, v223\n" \
"v_mfma_f32_16x16x32_bf16 v[96:99], a[200:203], a[252:255], v[96:99]\n" \
"ds_read_b128 a[136:139], v28 offset:20480\n" \
"v_cndmask_b32_e64 v223, v225, v226, vcc\n" \
"v_mfma_f32_16x16x32_bf16 v[112:115], a[140:143], a[188:191], v[112:115]\n" \
"ds_read_b128 a[200:203], v29 offset:20480\n" \
"v_or_b32_e32 v225, 0x140, v212\n" \
"v_mfma_f32_16x16x32_bf16 v[112:115], a[204:207], a[252:255], v[112:115]\n" \
"s_addk_i32 s84, 0x7c00\n" \
"s_nop 0\n" \
"v_mfma_f32_16x16x32_bf16 v[108:111], a[140:143], a[184:187], v[108:111]\n" \
"buffer_load_dwordx4 v27, s[68:71], s84 offen offset:3072 lds\n" \
"v_mfma_f32_16x16x32_bf16 v[108:111], a[204:207], a[248:251], v[108:111]\n" \
"s_add_u32 m0, s30, 0x9000\n" \
"s_mov_b32 s84, 0x8180\n" \
"v_mfma_f32_16x16x32_bf16 v[104:107], a[140:143], a[180:183], v[104:107]\n" \
"buffer_load_dwordx4 v22, s[64:67], s84 offen offset:0 lds\n" \
"v_mfma_f32_16x16x32_bf16 v[104:107], a[204:207], a[244:247], v[104:107]\n" \
"ds_read_b64_tr_b16 a[166:167], v33 offset:1024\n" \
"v_mul_u32_u24_e32 v226, 0xe39, v225\n" \
"v_mfma_f32_16x16x32_bf16 v[100:103], a[140:143], a[176:179], v[100:103]\n" \
"ds_read_b64_tr_b16 a[228:229], v33 offset:8192\n" \
"v_lshrrev_b32_e64 v226, 16, v226\n" \
"v_mfma_f32_16x16x32_bf16 v[100:103], a[204:207], a[240:243], v[100:103]\n" \
"ds_read_b128 a[140:143], v28 offset:22528\n" \
"v_mad_i32_i24 v225, v226, s6, v225\n" \
"v_mfma_f32_16x16x32_bf16 v[116:119], a[144:147], a[176:179], v[116:119]\n" \
"ds_read_b128 a[204:207], v29 offset:22528\n" \
"v_lshlrev_b32_e64 v227, 4, v225\n" \
"v_mfma_f32_16x16x32_bf16 v[116:119], a[208:211], a[240:243], v[116:119]\n" \
"s_addk_i32 s84, 0x1c00\n" \
"v_mfma_f32_16x16x32_bf16 v[120:123], a[144:147], a[180:183], v[120:123]\n" \
"buffer_load_dwordx4 v23, s[64:67], s84 offen offset:1024 lds\n" \
"v_mfma_f32_16x16x32_bf16 v[120:123], a[208:211], a[244:247], v[120:123]\n" \
"s_addk_i32 s84, 0x1c00\n" \
"v_mfma_f32_16x16x32_bf16 v[124:127], a[144:147], a[184:187], v[124:127]\n" \
"buffer_load_dwordx4 v22, s[64:67], s84 offen offset:2048 lds\n" \
"v_mfma_f32_16x16x32_bf16 v[124:127], a[208:211], a[248:251], v[124:127]\n" \
"s_addk_i32 s84, 0x1c00\n" \
"v_mfma_f32_16x16x32_bf16 v[128:131], a[144:147], a[188:191], v[128:131]\n" \
"buffer_load_dwordx4 v23, s[64:67], s84 offen offset:3072 lds\n" \
"v_mfma_f32_16x16x32_bf16 v[128:131], a[208:211], a[252:255], v[128:131]\n" \
"ds_read_b128 a[144:147], v28 offset:0\n" \
"v_add_u32_e32 v228, s5, v227\n" \
"v_mfma_f32_16x16x32_bf16 v[144:147], a[148:151], a[188:191], v[144:147]\n" \
"ds_read_b128 a[208:211], v29 offset:0\n" \
"v_and_b32_e32 v228, 0xf0, v228\n" \
"v_mfma_f32_16x16x32_bf16 v[144:147], a[212:215], a[252:255], v[144:147]\n" \
"s_add_u32 m0, s31, 0xc000\n" \
"s_mov_b32 s84, 0x180000\n" \
"v_mfma_f32_16x16x32_bf16 v[140:143], a[148:151], a[184:187], v[140:143]\n" \
"buffer_load_dwordx4 v26, s[72:75], s84 offen offset:0 lds\n" \
"v_mfma_f32_16x16x32_bf16 v[140:143], a[212:215], a[248:251], v[140:143]\n" \
"ds_read_b64_tr_b16 a[230:231], v33 offset:9216\n" \
"v_cmp_gt_u32_e64 vcc, 16, v225\n" \
"v_mfma_f32_16x16x32_bf16 v[136:139], a[148:151], a[180:183], v[136:139]\n" \
"s_addk_i32 s84, 0x7c00\n" \
"v_mfma_f32_16x16x32_bf16 v[136:139], a[212:215], a[244:247], v[136:139]\n" \
"buffer_load_dwordx4 v26, s[72:75], s84 offen offset:1024 lds\n" \
"v_mfma_f32_16x16x32_bf16 v[132:135], a[148:151], a[176:179], v[132:135]\n" \
"ds_read_b64_tr_b16 a[168:169], v34 offset:0\n" \
"v_cndmask_b32_e32 v225, v227, v228, vcc\n" \
"v_mfma_f32_16x16x32_bf16 v[132:135], a[212:215], a[240:243], v[132:135]\n" \
"ds_read_b128 a[148:151], v28 offset:2048\n" \
"v_or_b32_e32 v227, 0x180, v212\n" \
"v_mfma_f32_16x16x32_bf16 v[148:151], a[152:155], a[176:179], v[148:151]\n" \
"ds_read_b128 a[212:215], v29 offset:2048\n" \
"v_mul_u32_u24_e32 v228, 0xe39, v227\n" \
"v_mfma_f32_16x16x32_bf16 v[148:151], a[216:219], a[240:243], v[148:151]\n" \
"ds_read_b64_tr_b16 a[170:171], v34 offset:1024\n" \
"v_lshrrev_b32_e64 v228, 16, v228\n" \
"v_mfma_f32_16x16x32_bf16 v[152:155], a[152:155], a[180:183], v[152:155]\n" \
"ds_read_b64_tr_b16 a[232:233], v34 offset:8192\n" \
"v_mad_i32_i24 v227, v228, s6, v227\n" \
"v_mfma_f32_16x16x32_bf16 v[152:155], a[216:219], a[244:247], v[152:155]\n" \
"ds_read_b64_tr_b16 a[234:235], v34 offset:9216\n" \
"v_lshlrev_b32_e64 v229, 4, v227\n" \
"v_mfma_f32_16x16x32_bf16 v[156:159], a[152:155], a[184:187], v[156:159]\n" \
"ds_read_b64_tr_b16 a[172:173], v35 offset:0\n" \
"v_add_u32_e64 v230, s5, v229\n" \
"v_mfma_f32_16x16x32_bf16 v[156:159], a[216:219], a[248:251], v[156:159]\n" \
"ds_read_b64_tr_b16 a[174:175], v35 offset:1024\n" \
"v_and_b32_e32 v230, 0xf0, v230\n" \
"v_mfma_f32_16x16x32_bf16 v[160:163], a[152:155], a[188:191], v[160:163]\n" \
"ds_read_b64_tr_b16 a[236:237], v35 offset:8192\n" \
"v_cmp_gt_u32_e64 vcc, 16, v227\n" \
"v_mfma_f32_16x16x32_bf16 v[160:163], a[216:219], a[252:255], v[160:163]\n" \
"ds_read_b128 a[152:155], v28 offset:4096\n" \
"v_cndmask_b32_e64 v227, v229, v230, vcc\n" \
"v_mfma_f32_16x16x32_bf16 v[176:179], a[156:159], a[188:191], v[176:179]\n" \
"ds_read_b128 a[216:219], v29 offset:4096\n" \
"v_or_b32_e32 v229, 0x1c0, v212\n" \
"v_mfma_f32_16x16x32_bf16 v[176:179], a[220:223], a[252:255], v[176:179]\n" \
"ds_read_b64_tr_b16 a[238:239], v35 offset:9216\n" \
"v_mul_u32_u24_e32 v230, 0xe39, v229\n" \
"v_mfma_f32_16x16x32_bf16 v[172:175], a[156:159], a[184:187], v[172:175]\n" \
"v_lshrrev_b32_e64 v230, 16, v230\n" \
"v_mad_i32_i24 v229, v230, s6, v229\n" \
"v_mfma_f32_16x16x32_bf16 v[172:175], a[220:223], a[248:251], v[172:175]\n" \
"v_lshlrev_b32_e32 v231, 4, v229\n" \
"v_add_u32_e32 v232, s5, v231\n" \
"v_mfma_f32_16x16x32_bf16 v[168:171], a[156:159], a[180:183], v[168:171]\n" \
"v_and_b32_e32 v232, 0xf0, v232\n" \
"v_cmp_gt_u32_e64 vcc, 16, v229\n" \
"v_mfma_f32_16x16x32_bf16 v[168:171], a[220:223], a[244:247], v[168:171]\n" \
"s_nop 0\n" \
"v_cndmask_b32_e32 v229, v231, v232, vcc\n" \
"v_or_b32_e32 v231, 0x200, v212\n" \
"v_mfma_f32_16x16x32_bf16 v[164:167], a[156:159], a[176:179], v[164:167]\n" \
"v_mul_u32_u24_e32 v232, 0xe39, v231\n" \
"v_lshrrev_b32_e64 v232, 16, v232\n" \
"v_mfma_f32_16x16x32_bf16 v[164:167], a[220:223], a[240:243], v[164:167]\n" \
"ds_read_b128 a[156:159], v28 offset:6144\n" \
"ds_read_b128 a[220:223], v29 offset:6144\n" \
"s_waitcnt lgkmcnt(15)\n" \
"v_mfma_f32_16x16x32_bf16 a[0:3], a[128:131], a[160:163], a[0:3]\n" \
"s_addk_i32 s84, 0x7c00\n" \
"v_mfma_f32_16x16x32_bf16 a[0:3], a[192:195], a[224:227], a[0:3]\n" \
"buffer_load_dwordx4 v27, s[72:75], s84 offen offset:2048 lds\n" \
"v_mfma_f32_16x16x32_bf16 a[4:7], a[128:131], a[164:167], a[4:7]\n" \
"ds_read_b64_tr_b16 a[176:177], v36 offset:0\n" \
"v_mad_i32_i24 v231, v232, s6, v231\n" \
"s_waitcnt lgkmcnt(15)\n" \
"v_mfma_f32_16x16x32_bf16 a[4:7], a[192:195], a[228:231], a[4:7]\n" \
"s_addk_i32 s84, 0x7c00\n" \
"s_waitcnt lgkmcnt(11)\n" \
"v_mfma_f32_16x16x32_bf16 a[8:11], a[128:131], a[168:171], a[8:11]\n" \
"buffer_load_dwordx4 v27, s[72:75], s84 offen offset:3072 lds\n" \
"s_waitcnt lgkmcnt(9)\n" \
"v_mfma_f32_16x16x32_bf16 a[8:11], a[192:195], a[232:235], a[8:11]\n" \
"ds_read_b64_tr_b16 a[178:179], v36 offset:1024\n" \
"v_lshlrev_b32_e32 v233, 4, v231\n" \
"s_waitcnt lgkmcnt(8)\n" \
"v_mfma_f32_16x16x32_bf16 a[12:15], a[128:131], a[172:175], a[12:15]\n" \
"ds_read_b64_tr_b16 a[240:241], v36 offset:8192\n" \
"v_add_u32_e32 v234, s5, v233\n" \
"s_waitcnt lgkmcnt(5)\n" \
"v_mfma_f32_16x16x32_bf16 a[12:15], a[192:195], a[236:239], a[12:15]\n" \
"s_movk_i32 s5, 0x480\n" \
"v_mad_u32_u24 v0, v213, s5, v214\n" \
"v_mfma_f32_16x16x32_bf16 a[28:31], a[132:135], a[172:175], a[28:31]\n" \
"v_mul_u32_u24_e32 v213, 0x480, v216\n" \
"s_movk_i32 s6, 0xfc00\n" \
"v_mfma_f32_16x16x32_bf16 a[28:31], a[196:199], a[236:239], a[28:31]\n" \
"ds_read_b64_tr_b16 a[242:243], v36 offset:9216\n" \
"v_add3_u32 v1, v213, v215, s6\n" \
"v_mfma_f32_16x16x32_bf16 a[24:27], a[132:135], a[168:171], a[24:27]\n" \
"buffer_load_dwordx4 v[200:203], v192, s[76:79], s34 offen offset:0\n" \
"v_mfma_f32_16x16x32_bf16 a[24:27], a[196:199], a[232:235], a[24:27]\n" \
"v_mul_u32_u24_e32 v213, 0x480, v220\n" \
"v_add3_u32 v2, v213, v217, s7\n" \
"v_mfma_f32_16x16x32_bf16 a[20:23], a[132:135], a[164:167], a[20:23]\n" \
"v_mul_u32_u24_e32 v213, 0x480, v222\n" \
"v_add3_u32 v3, v213, v221, s40\n" \
"v_mfma_f32_16x16x32_bf16 a[20:23], a[196:199], a[228:231], a[20:23]\n" \
"ds_read_b64_tr_b16 a[180:181], v37 offset:0\n" \
"v_mul_u32_u24_e32 v213, 0x480, v226\n" \
"v_mfma_f32_16x16x32_bf16 a[16:19], a[132:135], a[160:163], a[16:19]\n" \
"v_add3_u32 v5, v213, v225, s6\n" \
"v_mul_u32_u24_e32 v213, 0x480, v228\n" \
"v_mfma_f32_16x16x32_bf16 a[16:19], a[196:199], a[224:227], a[16:19]\n" \
"v_add3_u32 v6, v213, v227, s7\n" \
"v_mul_u32_u24_e32 v213, 0x480, v230\n" \
"v_mfma_f32_16x16x32_bf16 a[32:35], a[136:139], a[160:163], a[32:35]\n" \
"buffer_load_dwordx4 v[204:207], v193, s[76:79], s34 offen offset:0\n" \
"v_mfma_f32_16x16x32_bf16 a[32:35], a[200:203], a[224:227], a[32:35]\n" \
"s_mov_b32 m0, s32\n" \
"ds_read_b64_tr_b16 a[182:183], v37 offset:1024\n" \
"v_mfma_f32_16x16x32_bf16 a[36:39], a[136:139], a[164:167], a[36:39]\n" \
"s_mov_b32 exec_lo, 0x0\n" \
"s_mov_b32 exec_hi, 0xff00\n" \
"global_load_lds_dword v[184:185], off\n" \
"s_mov_b64 exec, -1\n" \
"v_mfma_f32_16x16x32_bf16 a[36:39], a[200:203], a[228:231], a[36:39]\n" \
"ds_read_b64_tr_b16 a[244:245], v37 offset:8192\n" \
"v_add3_u32 v7, v213, v229, s40\n" \
"v_mfma_f32_16x16x32_bf16 a[40:43], a[136:139], a[168:171], a[40:43]\n" \
"s_lshl_b32 s7, s3, 3\n" \
"s_lshl_b32 s40, s3, 1\n" \
"v_mfma_f32_16x16x32_bf16 a[40:43], a[200:203], a[232:235], a[40:43]\n" \
"buffer_load_dword v208, v194, s[76:79], s34 offen offset:32\n" \
"v_mfma_f32_16x16x32_bf16 a[44:47], a[136:139], a[172:175], a[44:47]\n" \
"ds_read_b64_tr_b16 a[246:247], v37 offset:9216\n" \
"s_and_b32 s7, s7, 16\n" \
"v_mfma_f32_16x16x32_bf16 a[44:47], a[200:203], a[236:239], a[44:47]\n" \
"s_and_b32 s40, s40, 2\n" \
"s_and_b32 s6, s2, 7\n" \
"v_mfma_f32_16x16x32_bf16 a[60:63], a[140:143], a[172:175], a[60:63]\n" \
"v_bfe_u32 v213, v20, 7, 1\n" \
"s_or_b32 s7, s40, s7\n" \
"v_mfma_f32_16x16x32_bf16 a[60:63], a[204:207], a[236:239], a[60:63]\n" \
"v_and_b32_e32 v234, 0xf0, v234\n" \
"v_cmp_gt_u32_e32 vcc, 16, v231\n" \
"v_mfma_f32_16x16x32_bf16 a[56:59], a[140:143], a[168:171], a[56:59]\n" \
"buffer_load_dword v209, v194, s[76:79], s34 offen offset:1184\n" \
"v_mfma_f32_16x16x32_bf16 a[56:59], a[204:207], a[232:235], a[56:59]\n" \
"ds_read_b64_tr_b16 a[184:185], v38 offset:0\n" \
"v_or3_b32 v213, s7, v213, v219\n" \
"v_mfma_f32_16x16x32_bf16 a[52:55], a[140:143], a[164:167], a[52:55]\n" \
"v_and_b32_e32 v214, 32, v20\n" \
"s_lshl_b32 s6, s6, 5\n" \
"v_mfma_f32_16x16x32_bf16 a[52:55], a[204:207], a[228:231], a[52:55]\n" \
"ds_read_b64_tr_b16 a[186:187], v38 offset:1024\n" \
"v_cndmask_b32_e64 v231, v233, v234, vcc\n" \
"v_mfma_f32_16x16x32_bf16 a[48:51], a[140:143], a[160:163], a[48:51]\n" \
"v_mul_u32_u24_e64 v213, 18, v213\n" \
"v_mov_b32_e32 v215, 0x100\n" \
"v_mfma_f32_16x16x32_bf16 a[48:51], a[204:207], a[224:227], a[48:51]\n" \
"v_mov_b32_e32 v216, s6\n" \
"v_cmp_eq_u32_e32 vcc, 0, v214\n" \
"v_mfma_f32_16x16x32_bf16 a[64:67], a[144:147], a[160:163], a[64:67]\n" \
"buffer_load_dword v211, v194, s[76:79], s34 offen offset:2336\n" \
"v_mfma_f32_16x16x32_bf16 a[64:67], a[208:211], a[224:227], a[64:67]\n" \
"ds_read_b64_tr_b16 a[248:249], v38 offset:8192\n" \
"v_and_or_b32 v213, v218, 1, v213\n" \
"v_mfma_f32_16x16x32_bf16 a[68:71], a[144:147], a[164:167], a[68:71]\n" \
"v_lshlrev_b32_e32 v212, 3, v212\n" \
"v_cndmask_b32_e32 v214, v215, v216, vcc\n" \
"v_mfma_f32_16x16x32_bf16 a[68:71], a[208:211], a[228:231], a[68:71]\n" \
"ds_read_b64_tr_b16 a[250:251], v38 offset:9216\n" \
"v_mad_u32_u24 v213, v213, s5, v214\n" \
"v_mfma_f32_16x16x32_bf16 a[72:75], a[144:147], a[168:171], a[72:75]\n" \
"v_and_or_b32 v213, v20, 16, v213\n" \
"v_add_u32_e32 v9, 0x4800, v213\n" \
"v_mfma_f32_16x16x32_bf16 a[72:75], a[208:211], a[232:235], a[72:75]\n" \
"s_mov_b32 s76, s20\n" \
"s_and_b32 s77, s21, 0xffff\n" \
"v_mfma_f32_16x16x32_bf16 a[76:79], a[144:147], a[172:175], a[76:79]\n" \
"s_mov_b32 s78, 0x480\n" \
"v_mul_u32_u24_e32 v213, 0x120, v218\n" \
"v_mfma_f32_16x16x32_bf16 a[76:79], a[208:211], a[236:239], a[76:79]\n" \
"buffer_load_ushort v210, v195, s[76:79], 0 offen\n" \
"v_mfma_f32_16x16x32_bf16 a[92:95], a[148:151], a[172:175], a[92:95]\n" \
"ds_read_b64_tr_b16 a[188:189], v39 offset:0\n" \
"v_and_or_b32 v10, v212, 24, v213\n" \
"v_mfma_f32_16x16x32_bf16 a[92:95], a[212:215], a[236:239], a[92:95]\n" \
"v_lshrrev_b32_e32 v213, 2, v20\n" \
"v_and_or_b32 v213, v213, 60, s4\n" \
"v_mfma_f32_16x16x32_bf16 a[88:91], a[148:151], a[168:171], a[88:91]\n" \
"s_mov_b32 s76, s8\n" \
"s_nop 0\n" \
"s_and_b32 s77, s9, 0xffff\n" \
"v_mfma_f32_16x16x32_bf16 a[88:91], a[212:215], a[232:235], a[88:91]\n" \
"s_mov_b32 s78, 0x240000\n" \
"ds_read_b64_tr_b16 a[190:191], v39 offset:1024\n" \
"v_mfma_f32_16x16x32_bf16 a[84:87], a[148:151], a[164:167], a[84:87]\n" \
"v_lshl_or_b32 v212, s3, 9, v212\n" \
"v_mad_u32_u24 v4, v224, s5, v223\n" \
"v_mfma_f32_16x16x32_bf16 a[84:87], a[212:215], a[228:231], a[84:87]\n" \
"ds_read_b64_tr_b16 a[252:253], v39 offset:8192\n" \
"v_mad_u32_u24 v8, v232, s5, v231\n" \
"v_mfma_f32_16x16x32_bf16 a[80:83], a[148:151], a[160:163], a[80:83]\n" \
"v_or_b32_e32 v18, 0x27000, v213\n" \
"v_or_b32_e32 v19, 0x27800, v212\n" \
"v_mfma_f32_16x16x32_bf16 a[80:83], a[212:215], a[224:227], a[80:83]\n" \
"v_mov_b32_e32 v219, 0x1000c0c\n" \
"v_mov_b32_e32 v220, 0xc0c0100\n" \
"v_mfma_f32_16x16x32_bf16 a[96:99], a[152:155], a[160:163], a[96:99]\n" \
"ds_read_b64_tr_b16 a[254:255], v39 offset:9216\n" \
"v_bfe_u32 v218, v20, 4, 1\n" \
"v_mfma_f32_16x16x32_bf16 a[96:99], a[216:219], a[224:227], a[96:99]\n" \
"v_lshrrev_b32_e32 v213, 8, v20\n" \
"v_cmp_eq_u32_e32 vcc, 0, v218\n" \
"v_mfma_f32_16x16x32_bf16 a[100:103], a[152:155], a[164:167], a[100:103]\n" \
"s_lshr_b32 s4, s2, 5\n" \
"s_and_b32 s5, s3, 1\n" \
"v_mfma_f32_16x16x32_bf16 a[100:103], a[216:219], a[228:231], a[100:103]\n" \
"v_bfe_u32 v217, v20, 4, 4\n" \
"v_cndmask_b32_e64 v219, v219, v220, vcc\n" \
"v_mfma_f32_16x16x32_bf16 a[104:107], a[152:155], a[168:171], a[104:107]\n" \
"v_mov_b32_e32 v220, 0x3020c0c\n" \
"v_mov_b32_e32 v221, 0xc0c0302\n" \
"v_mfma_f32_16x16x32_bf16 a[104:107], a[216:219], a[232:235], a[104:107]\n" \
"v_lshl_or_b32 v218, v213, 2, v218\n" \
"s_xor_b32 s6, s4, 15\n" \
"v_mfma_f32_16x16x32_bf16 a[108:111], a[152:155], a[172:175], a[108:111]\n" \
"s_cmpk_lt_u32 s3, 2\n" \
"s_cselect_b32 s7, s6, s4\n" \
"v_mfma_f32_16x16x32_bf16 a[108:111], a[216:219], a[236:239], a[108:111]\n" \
"s_lshl_b32 s40, s7, 7\n" \
".fill (L_pb + 48864 - .) / 4, 4, 0xBF800000\n" \
"L_pp12:\n" \
"s_cbranch_execz L_pret\n" \
"s_lshl_b32 s41, s5, 6\n" \
"s_waitcnt lgkmcnt(15)\n" \
"v_mfma_f32_16x16x32_bf16 a[124:127], a[156:159], a[172:175], a[124:127]\n" \
"v_cndmask_b32_e32 v220, v220, v221, vcc\n" \
"v_mov_b32_e32 v221, 0xc0c0c0c\n" \
"v_mfma_f32_16x16x32_bf16 a[124:127], a[220:223], a[236:239], a[124:127]\n" \
"v_cmp_gt_u32_e32 vcc, v218, v217\n" \
"v_or_b32_e32 v218, 2, v218\n" \
"v_mfma_f32_16x16x32_bf16 a[120:123], a[156:159], a[168:171], a[120:123]\n" \
"v_lshrrev_b32_e32 v212, 7, v20\n" \
"s_or_b32 s41, s41, s40\n" \
"v_mfma_f32_16x16x32_bf16 a[120:123], a[220:223], a[232:235], a[120:123]\n" \
"v_cndmask_b32_e32 v40, v221, v219, vcc\n" \
"v_cndmask_b32_e32 v42, v219, v221, vcc\n" \
"v_mfma_f32_16x16x32_bf16 a[116:119], a[156:159], a[164:167], a[116:119]\n" \
"v_cmp_gt_u32_e32 vcc, v218, v217\n" \
"v_and_b32_e32 v218, 16, v20\n" \
"v_mfma_f32_16x16x32_bf16 a[116:119], a[220:223], a[228:231], a[116:119]\n" \
"s_movk_i32 s42, 0x3f80\n" \
"v_or_b32_e32 v214, s41, v212\n" \
"v_mfma_f32_16x16x32_bf16 a[112:115], a[156:159], a[160:163], a[112:115]\n" \
"v_and_b32_e32 v217, 0x320, v20\n" \
"s_movk_i32 s41, 0x100\n" \
"v_mfma_f32_16x16x32_bf16 a[112:115], a[220:223], a[224:227], a[112:115]\n" \
"s_waitcnt vmcnt(7) lgkmcnt(0)\n" \
"s_barrier\n" \
"v_mfma_f32_16x16x32_bf16 v[52:55], a[128:131], a[176:179], v[52:55]\n" \
"s_add_u32 m0, s30, 0x0\n" \
"s_movk_i32 s84, 0x200\n" \
"v_mfma_f32_16x16x32_bf16 v[52:55], a[192:195], a[240:243], v[52:55]\n" \
"buffer_load_dwordx4 v22, s[64:67], s84 offen offset:0 lds\n" \
"v_mfma_f32_16x16x32_bf16 v[56:59], a[128:131], a[180:183], v[56:59]\n" \
"ds_read_b64_tr_b16 a[160:161], v32 offset:32768\n" \
"v_lshlrev_b32_e64 v218, v218, s42\n" \
"v_mfma_f32_16x16x32_bf16 v[56:59], a[192:195], a[244:247], v[56:59]\n" \
"s_movk_i32 s84, 0x1e00\n" \
"v_mfma_f32_16x16x32_bf16 v[60:63], a[128:131], a[184:187], v[60:63]\n" \
"buffer_load_dwordx4 v23, s[64:67], s84 offen offset:1024 lds\n" \
"v_mfma_f32_16x16x32_bf16 v[60:63], a[192:195], a[248:251], v[60:63]\n" \
"ds_read_b64_tr_b16 a[162:163], v32 offset:33792\n" \
"v_mov_b32_e64 v219, 0\n" \
"v_mfma_f32_16x16x32_bf16 v[64:67], a[128:131], a[188:191], v[64:67]\n" \
"ds_read_b64_tr_b16 a[224:225], v32 offset:40960\n" \
"v_lshrrev_b32_e64 v215, 6, v20\n" \
"v_mfma_f32_16x16x32_bf16 v[64:67], a[192:195], a[252:255], v[64:67]\n" \
"ds_read_b128 a[128:131], v28 offset:49152\n" \
"v_cndmask_b32_e64 v41, v221, v220, vcc\n" \
"v_mfma_f32_16x16x32_bf16 v[80:83], a[132:135], a[188:191], v[80:83]\n" \
"ds_read_b128 a[192:195], v29 offset:49152\n" \
"v_cndmask_b32_e64 v43, v220, v221, vcc\n" \
"v_mfma_f32_16x16x32_bf16 v[80:83], a[196:199], a[252:255], v[80:83]\n" \
"s_movk_i32 s84, 0x3a00\n" \
"v_mfma_f32_16x16x32_bf16 v[76:79], a[132:135], a[184:187], v[76:79]\n" \
"buffer_load_dwordx4 v22, s[64:67], s84 offen offset:2048 lds\n" \
"v_mfma_f32_16x16x32_bf16 v[76:79], a[196:199], a[248:251], v[76:79]\n" \
"s_movk_i32 s84, 0x5600\n" \
"v_mfma_f32_16x16x32_bf16 v[72:75], a[132:135], a[180:183], v[72:75]\n" \
"buffer_load_dwordx4 v23, s[64:67], s84 offen offset:3072 lds\n" \
"v_mfma_f32_16x16x32_bf16 v[72:75], a[196:199], a[244:247], v[72:75]\n" \
"s_add_u32 m0, s31, 0x0\n" \
"s_mov_b32 s84, 0x200000\n" \
"v_mfma_f32_16x16x32_bf16 v[68:71], a[132:135], a[176:179], v[68:71]\n" \
"buffer_load_dwordx4 v26, s[68:71], s84 offen offset:0 lds\n" \
"v_mfma_f32_16x16x32_bf16 v[68:71], a[196:199], a[240:243], v[68:71]\n" \
"ds_read_b128 a[132:135], v28 offset:51200\n" \
"v_cmp_eq_u32_e32 vcc, s41, v217\n" \
"v_mfma_f32_16x16x32_bf16 v[84:87], a[136:139], a[176:179], v[84:87]\n" \
"ds_read_b128 a[196:199], v29 offset:51200\n" \
"v_and_b32_e32 v217, 0x330, v20\n" \
"v_mfma_f32_16x16x32_bf16 v[84:87], a[200:203], a[240:243], v[84:87]\n" \
"s_addk_i32 s84, 0x7c00\n" \
"v_mfma_f32_16x16x32_bf16 v[88:91], a[136:139], a[180:183], v[88:91]\n" \
"buffer_load_dwordx4 v26, s[68:71], s84 offen offset:1024 lds\n" \
"v_mfma_f32_16x16x32_bf16 v[88:91], a[200:203], a[244:247], v[88:91]\n" \
"ds_read_b64_tr_b16 a[226:227], v32 offset:41984\n" \
"v_bfe_u32 v216, v20, 6, 2\n" \
"v_mfma_f32_16x16x32_bf16 v[92:95], a[136:139], a[184:187], v[92:95]\n" \
"s_addk_i32 s84, 0x7c00\n" \
"v_mfma_f32_16x16x32_bf16 v[92:95], a[200:203], a[248:251], v[92:95]\n" \
"buffer_load_dwordx4 v27, s[68:71], s84 offen offset:2048 lds\n" \
"v_mfma_f32_16x16x32_bf16 v[96:99], a[136:139], a[188:191], v[96:99]\n" \
"ds_read_b64_tr_b16 a[164:165], v33 offset:32768\n" \
"v_cndmask_b32_e64 v46, v219, v218, vcc\n" \
"v_mfma_f32_16x16x32_bf16 v[96:99], a[200:203], a[252:255], v[96:99]\n" \
"ds_read_b128 a[136:139], v28 offset:53248\n" \
"v_mov_b32_e32 v218, 0x3f80\n" \
"v_mfma_f32_16x16x32_bf16 v[112:115], a[140:143], a[188:191], v[112:115]\n" \
"ds_read_b128 a[200:203], v29 offset:53248\n" \
"v_cmp_eq_u32_e64 vcc, 32, v217\n" \
"v_mfma_f32_16x16x32_bf16 v[112:115], a[204:207], a[252:255], v[112:115]\n" \
"s_addk_i32 s84, 0x7c00\n" \
"v_mfma_f32_16x16x32_bf16 v[108:111], a[140:143], a[184:187], v[108:111]\n" \
"buffer_load_dwordx4 v27, s[68:71], s84 offen offset:3072 lds\n" \
"v_mfma_f32_16x16x32_bf16 v[108:111], a[204:207], a[248:251], v[108:111]\n" \
"s_add_u32 m0, s30, 0x1000\n" \
"s_mov_b32 s84, 0x8200\n" \
"v_mfma_f32_16x16x32_bf16 v[104:107], a[140:143], a[180:183], v[104:107]\n" \
"buffer_load_dwordx4 v22, s[64:67], s84 offen offset:0 lds\n" \
"v_mfma_f32_16x16x32_bf16 v[104:107], a[204:207], a[244:247], v[104:107]\n" \
"ds_read_b64_tr_b16 a[166:167], v33 offset:33792\n" \
"s_add_i32 s41, s40, -16\n" \
"v_mfma_f32_16x16x32_bf16 v[100:103], a[140:143], a[176:179], v[100:103]\n" \
"ds_read_b64_tr_b16 a[228:229], v33 offset:40960\n" \
"v_lshrrev_b32_e64 v219, 1, v20\n" \
"v_mfma_f32_16x16x32_bf16 v[100:103], a[204:207], a[240:243], v[100:103]\n" \
"ds_read_b128 a[140:143], v28 offset:55296\n" \
"v_bfe_u32 v215, v215, 1, 1\n" \
"v_mfma_f32_16x16x32_bf16 v[116:119], a[144:147], a[176:179], v[116:119]\n" \
"ds_read_b128 a[204:207], v29 offset:55296\n" \
"v_cndmask_b32_e64 v47, 0, v218, vcc\n" \
"v_mfma_f32_16x16x32_bf16 v[116:119], a[208:211], a[240:243], v[116:119]\n" \
"s_addk_i32 s84, 0x1c00\n" \
"v_mfma_f32_16x16x32_bf16 v[120:123], a[144:147], a[180:183], v[120:123]\n" \
"buffer_load_dwordx4 v23, s[64:67], s84 offen offset:1024 lds\n" \
"v_mfma_f32_16x16x32_bf16 v[120:123], a[208:211], a[244:247], v[120:123]\n" \
"s_addk_i32 s84, 0x1c00\n" \
"v_mfma_f32_16x16x32_bf16 v[124:127], a[144:147], a[184:187], v[124:127]\n" \
"buffer_load_dwordx4 v22, s[64:67], s84 offen offset:2048 lds\n" \
"v_mfma_f32_16x16x32_bf16 v[124:127], a[208:211], a[248:251], v[124:127]\n" \
"s_addk_i32 s84, 0x1c00\n" \
"v_mfma_f32_16x16x32_bf16 v[128:131], a[144:147], a[188:191], v[128:131]\n" \
"buffer_load_dwordx4 v23, s[64:67], s84 offen offset:3072 lds\n" \
"v_mfma_f32_16x16x32_bf16 v[128:131], a[208:211], a[252:255], v[128:131]\n" \
"ds_read_b128 a[144:147], v28 offset:32768\n" \
"s_mov_b32 s42, 0\n" \
"v_mfma_f32_16x16x32_bf16 v[144:147], a[148:151], a[188:191], v[144:147]\n" \
"ds_read_b128 a[208:211], v29 offset:32768\n" \
"v_mfma_f32_16x16x32_bf16 v[144:147], a[212:215], a[252:255], v[144:147]\n" \
"s_add_u32 m0, s31, 0x4000\n" \
"s_mov_b32 s84, 0x200000\n" \
"v_mfma_f32_16x16x32_bf16 v[140:143], a[148:151], a[184:187], v[140:143]\n" \
"buffer_load_dwordx4 v26, s[72:75], s84 offen offset:0 lds\n" \
"v_mfma_f32_16x16x32_bf16 v[140:143], a[212:215], a[248:251], v[140:143]\n" \
"ds_read_b64_tr_b16 a[230:231], v33 offset:41984\n" \
"v_mfma_f32_16x16x32_bf16 v[136:139], a[148:151], a[180:183], v[136:139]\n" \
"s_addk_i32 s84, 0x7c00\n" \
"s_nop 0\n" \
"v_mfma_f32_16x16x32_bf16 v[136:139], a[212:215], a[244:247], v[136:139]\n" \
"buffer_load_dwordx4 v26, s[72:75], s84 offen offset:1024 lds\n" \
"v_mfma_f32_16x16x32_bf16 v[132:135], a[148:151], a[176:179], v[132:135]\n" \
"ds_read_b64_tr_b16 a[168:169], v34 offset:32768\n" \
"v_mfma_f32_16x16x32_bf16 v[132:135], a[212:215], a[240:243], v[132:135]\n" \
"ds_read_b128 a[148:151], v28 offset:34816\n" \
"v_mfma_f32_16x16x32_bf16 v[148:151], a[152:155], a[176:179], v[148:151]\n" \
"ds_read_b128 a[212:215], v29 offset:34816\n" \
"v_mfma_f32_16x16x32_bf16 v[148:151], a[216:219], a[240:243], v[148:151]\n" \
"ds_read_b64_tr_b16 a[170:171], v34 offset:33792\n" \
"v_mfma_f32_16x16x32_bf16 v[152:155], a[152:155], a[180:183], v[152:155]\n" \
"ds_read_b64_tr_b16 a[232:233], v34 offset:40960\n" \
"v_mfma_f32_16x16x32_bf16 v[152:155], a[216:219], a[244:247], v[152:155]\n" \
"ds_read_b64_tr_b16 a[234:235], v34 offset:41984\n" \
"v_mfma_f32_16x16x32_bf16 v[156:159], a[152:155], a[184:187], v[156:159]\n" \
"ds_read_b64_tr_b16 a[172:173], v35 offset:32768\n" \
"v_mfma_f32_16x16x32_bf16 v[156:159], a[216:219], a[248:251], v[156:159]\n" \
"ds_read_b64_tr_b16 a[174:175], v35 offset:33792\n" \
"v_mfma_f32_16x16x32_bf16 v[160:163], a[152:155], a[188:191], v[160:163]\n" \
"ds_read_b64_tr_b16 a[236:237], v35 offset:40960\n" \
"v_mfma_f32_16x16x32_bf16 v[160:163], a[216:219], a[252:255], v[160:163]\n" \
"ds_read_b128 a[152:155], v28 offset:36864\n" \
"v_mfma_f32_16x16x32_bf16 v[176:179], a[156:159], a[188:191], v[176:179]\n" \
"ds_read_b128 a[216:219], v29 offset:36864\n" \
"v_mfma_f32_16x16x32_bf16 v[176:179], a[220:223], a[252:255], v[176:179]\n" \
"ds_read_b64_tr_b16 a[238:239], v35 offset:41984\n" \
"v_mfma_f32_16x16x32_bf16 v[172:175], a[156:159], a[184:187], v[172:175]\n" \
"s_cmp_lg_u32 s40, 0\n" \
"s_cselect_b32 s41, s41, s42\n" \
"v_mfma_f32_16x16x32_bf16 v[172:175], a[220:223], a[248:251], v[172:175]\n" \
"v_lshl_or_b32 v217, s5, 3, v212\n" \
"v_lshlrev_b32_e64 v218, 10, v213\n" \
"v_mfma_f32_16x16x32_bf16 v[168:171], a[156:159], a[180:183], v[168:171]\n" \
"v_lshlrev_b32_e32 v216, 7, v216\n" \
"v_and_b32_e32 v219, 24, v219\n" \
"v_mfma_f32_16x16x32_bf16 v[168:171], a[220:223], a[244:247], v[168:171]\n" \
"v_and_or_b32 v212, v212, 2, v215\n" \
"v_add_u32_e64 v217, s41, v217\n" \
"v_mfma_f32_16x16x32_bf16 v[164:167], a[156:159], a[176:179], v[164:167]\n" \
"v_or3_b32 v216, v216, v218, v219\n" \
"v_lshlrev_b32_e64 v212, 5, v212\n" \
"v_mfma_f32_16x16x32_bf16 v[164:167], a[220:223], a[240:243], v[164:167]\n" \
"ds_read_b128 a[156:159], v28 offset:38912\n" \
"ds_read_b128 a[220:223], v29 offset:38912\n" \
"s_waitcnt lgkmcnt(15)\n" \
"v_mfma_f32_16x16x32_bf16 a[0:3], a[128:131], a[160:163], a[0:3]\n" \
"s_addk_i32 s84, 0x7c00\n" \
"v_mfma_f32_16x16x32_bf16 a[0:3], a[192:195], a[224:227], a[0:3]\n" \
"buffer_load_dwordx4 v27, s[72:75], s84 offen offset:2048 lds\n" \
"v_mfma_f32_16x16x32_bf16 a[4:7], a[128:131], a[164:167], a[4:7]\n" \
"ds_read_b64_tr_b16 a[176:177], v36 offset:32768\n" \
"s_mov_b32 s41, 0x14000\n" \
"s_waitcnt lgkmcnt(15)\n" \
"v_mfma_f32_16x16x32_bf16 a[4:7], a[192:195], a[228:231], a[4:7]\n" \
"s_addk_i32 s84, 0x7c00\n" \
"s_waitcnt lgkmcnt(11)\n" \
"v_mfma_f32_16x16x32_bf16 a[8:11], a[128:131], a[168:171], a[8:11]\n" \
"buffer_load_dwordx4 v27, s[72:75], s84 offen offset:3072 lds\n" \
"s_waitcnt lgkmcnt(9)\n" \
"v_mfma_f32_16x16x32_bf16 a[8:11], a[192:195], a[232:235], a[8:11]\n" \
"ds_read_b64_tr_b16 a[178:179], v36 offset:33792\n" \
"v_or3_b32 v48, v216, v212, s41\n" \
"s_waitcnt lgkmcnt(8)\n" \
"v_mfma_f32_16x16x32_bf16 a[12:15], a[128:131], a[172:175], a[12:15]\n" \
"ds_read_b64_tr_b16 a[240:241], v36 offset:40960\n" \
"s_lshl_b32 s41, s5, 2\n" \
"s_waitcnt lgkmcnt(5)\n" \
"v_mfma_f32_16x16x32_bf16 a[12:15], a[192:195], a[236:239], a[12:15]\n" \
"s_lshl_b32 s7, s7, 3\n" \
"v_or_b32_e64 v212, s41, v213\n" \
"v_mfma_f32_16x16x32_bf16 a[28:31], a[132:135], a[172:175], a[28:31]\n" \
"s_or_b32 s7, s7, s41\n" \
"s_mov_b32 s41, 0xff800000\n" \
"v_mfma_f32_16x16x32_bf16 a[28:31], a[196:199], a[236:239], a[28:31]\n" \
"ds_read_b64_tr_b16 a[242:243], v36 offset:41984\n" \
"v_mfma_f32_16x16x32_bf16 a[24:27], a[132:135], a[168:171], a[24:27]\n" \
"s_cmp_lg_u32 s40, 0\n" \
"s_cselect_b32 s47, s42, s41\n" \
"v_mfma_f32_16x16x32_bf16 a[24:27], a[196:199], a[232:235], a[24:27]\n" \
"s_mov_b32 s40, 0xc000\n" \
"s_mov_b32 s41, 0x8000\n" \
"v_mfma_f32_16x16x32_bf16 a[20:23], a[132:135], a[164:167], a[20:23]\n" \
"s_cmp_lg_u32 s4, 0\n" \
"s_cselect_b32 s51, s40, s41\n" \
"v_mfma_f32_16x16x32_bf16 a[20:23], a[196:199], a[228:231], a[20:23]\n" \
"ds_read_b64_tr_b16 a[180:181], v37 offset:32768\n" \
"s_movk_i32 s40, 0x4000\n" \
"v_mfma_f32_16x16x32_bf16 a[16:19], a[132:135], a[160:163], a[16:19]\n" \
"s_cmp_lg_u32 s4, 0\n" \
"s_cselect_b32 s53, s41, s40\n" \
"v_mfma_f32_16x16x32_bf16 a[16:19], a[196:199], a[224:227], a[16:19]\n" \
"s_lshl_b32 s40, s2, 2\n" \
"s_and_b32 s40, s40, 28\n" \
"v_mfma_f32_16x16x32_bf16 a[32:35], a[136:139], a[160:163], a[32:35]\n" \
"s_waitcnt vmcnt(16)\n" \
"v_or_b32_e32 v204, v210, v204\n" \
"v_or_b32_e64 v206, v210, v206\n" \
"v_mfma_f32_16x16x32_bf16 a[32:35], a[200:203], a[224:227], a[32:35]\n" \
"v_mov_b32_e64 v210, 0\n" \
"s_add_u32 s88, s35, 0x1000\n" \
"v_mfma_f32_16x16x32_bf16 a[36:39], a[136:139], a[164:167], a[36:39]\n" \
"ds_read_b64_tr_b16 a[182:183], v37 offset:33792\n" \
"s_bfe_u32 s45, s2, 0x20003\n" \
"v_mfma_f32_16x16x32_bf16 a[36:39], a[200:203], a[228:231], a[36:39]\n" \
"ds_read_b64_tr_b16 a[244:245], v37 offset:40960\n" \
"s_lshr_b32 s44, s3, 1\n" \
"v_mfma_f32_16x16x32_bf16 a[40:43], a[136:139], a[168:171], a[40:43]\n" \
"s_mov_b32 m0, s32\n" \
"s_or_b32 s46, s40, s45\n" \
"v_mfma_f32_16x16x32_bf16 a[40:43], a[200:203], a[232:235], a[40:43]\n" \
"s_mov_b32 exec_lo, 0xffffffff\n" \
"s_mov_b32 exec_hi, 0x0\n" \
"global_load_lds_dword v[188:189], off\n" \
"s_mov_b64 exec, -1\n" \
"v_mfma_f32_16x16x32_bf16 a[44:47], a[136:139], a[172:175], a[44:47]\n" \
"ds_read_b64_tr_b16 a[246:247], v37 offset:41984\n" \
"s_lshl_b32 s43, s44, 15\n" \
"v_mfma_f32_16x16x32_bf16 a[44:47], a[200:203], a[236:239], a[44:47]\n" \
"s_lshl_b32 s41, s5, 14\n" \
"s_lshl_b32 s48, s46, 20\n" \
"v_mfma_f32_16x16x32_bf16 a[60:63], a[140:143], a[172:175], a[60:63]\n" \
"ds_write_b128 v196, v[200:203]\n" \
"v_mfma_f32_16x16x32_bf16 a[60:63], a[204:207], a[236:239], a[60:63]\n" \
"s_or_b32 s41, s43, s41\n" \
"s_add_i32 s49, s48, 0x1000000\n" \
"v_mfma_f32_16x16x32_bf16 a[56:59], a[140:143], a[168:171], a[56:59]\n" \
"ds_read_b64_tr_b16 a[184:185], v38 offset:32768\n" \
"s_or_b32 s42, s49, s41\n" \
"v_mfma_f32_16x16x32_bf16 a[56:59], a[204:207], a[232:235], a[56:59]\n" \
"s_lshl_b32 s4, s4, 16\n" \
"s_or_b32 s41, s42, s4\n" \
"v_mfma_f32_16x16x32_bf16 a[52:55], a[140:143], a[164:167], a[52:55]\n" \
"s_lshl_b32 s4, s6, 16\n" \
"s_or_b32 s42, s42, s4\n" \
"v_mfma_f32_16x16x32_bf16 a[52:55], a[204:207], a[228:231], a[52:55]\n" \
"ds_read_b64_tr_b16 a[186:187], v38 offset:33792\n" \
"s_lshl_b32 s4, s5, 11\n" \
"v_mfma_f32_16x16x32_bf16 a[48:51], a[140:143], a[160:163], a[48:51]\n" \
"s_mul_i32 s5, s7, 0x24800\n" \
"s_lshl_b32 s6, s46, 9\n" \
"v_mfma_f32_16x16x32_bf16 a[48:51], a[204:207], a[224:227], a[48:51]\n" \
"s_lshl_b32 s7, s40, 6\n" \
"s_add_i32 s6, s5, s6\n" \
"v_mfma_f32_16x16x32_bf16 a[64:67], a[144:147], a[160:163], a[64:67]\n" \
"ds_write_b128 v197, v[204:207]\n" \
"v_mfma_f32_16x16x32_bf16 a[64:67], a[208:211], a[224:227], a[64:67]\n" \
"ds_read_b64_tr_b16 a[248:249], v38 offset:40960\n" \
"s_or_b32 s5, s5, s7\n" \
"v_mfma_f32_16x16x32_bf16 a[68:71], a[144:147], a[164:167], a[68:71]\n" \
"s_lshl_b32 s7, s45, 2\n" \
"s_or_b32 s5, s5, s7\n" \
"v_mfma_f32_16x16x32_bf16 a[68:71], a[208:211], a[228:231], a[68:71]\n" \
"ds_read_b64_tr_b16 a[250:251], v38 offset:41984\n" \
"s_lshl_b32 s7, s3, 12\n" \
"v_mfma_f32_16x16x32_bf16 a[72:75], a[144:147], a[168:171], a[72:75]\n" \
"s_or_b32 s50, s49, s7\n" \
"s_lshl_b32 s7, s3, 11\n" \
"v_mfma_f32_16x16x32_bf16 a[72:75], a[208:211], a[232:235], a[72:75]\n" \
"v_lshrrev_b32_e32 v215, 4, v20\n" \
"v_bfe_u32 v216, v20, 4, 3\n" \
"v_mfma_f32_16x16x32_bf16 a[76:79], a[144:147], a[172:175], a[76:79]\n" \
"s_add_i32 s52, s48, s7\n" \
"s_add_i32 s53, s53, s7\n" \
"v_mfma_f32_16x16x32_bf16 a[76:79], a[208:211], a[236:239], a[76:79]\n" \
"ds_read_b64_tr_b16 a[188:189], v39 offset:32768\n" \
"v_mul_u32_u24_e32 v214, 0x480, v214\n" \
"v_mfma_f32_16x16x32_bf16 a[92:95], a[148:151], a[172:175], a[92:95]\n" \
"ds_write_b128 v198, v[208:211]\n" \
"v_mfma_f32_16x16x32_bf16 a[92:95], a[212:215], a[236:239], a[92:95]\n" \
"v_bitop3_b32 v218, v213, v215, 7 bitop3:0x78\n" \
"v_bitop3_b32 v213, v213, v216, 4 bitop3:0x36\n" \
"v_mfma_f32_16x16x32_bf16 a[88:91], a[148:151], a[168:171], a[88:91]\n" \
"ds_read_b64_tr_b16 a[190:191], v39 offset:33792\n" \
"s_lshl_b32 s7, s3, 10\n" \
"v_mfma_f32_16x16x32_bf16 a[88:91], a[212:215], a[232:235], a[88:91]\n" \
"s_or_b32 s4, s43, s4\n" \
"v_lshl_or_b32 v25, v213, 4, v214\n" \
"v_mfma_f32_16x16x32_bf16 a[84:87], a[148:151], a[164:167], a[84:87]\n" \
"v_mul_u32_u24_e32 v213, 0x480, v217\n" \
"v_bitop3_b32 v212, v215, v212, 7 bitop3:0x6c\n" \
"v_mfma_f32_16x16x32_bf16 a[84:87], a[212:215], a[228:231], a[84:87]\n" \
"ds_read_b64_tr_b16 a[252:253], v39 offset:40960\n" \
"s_or_b32 s40, s7, 0x14000\n" \
"v_mfma_f32_16x16x32_bf16 a[80:83], a[148:151], a[160:163], a[80:83]\n" \
"s_lshl_b32 s43, s44, 13\n" \
"s_mulk_i32 s44, 0xe800\n" \
"v_mfma_f32_16x16x32_bf16 a[80:83], a[212:215], a[224:227], a[80:83]\n" \
"s_lshl_b32 s7, s3, 8\n" \
"s_nop 0\n" \
"v_lshl_or_b32 v24, v218, 4, v214\n" \
"v_mfma_f32_16x16x32_bf16 a[96:99], a[152:155], a[160:163], a[96:99]\n" \
"ds_read_b64_tr_b16 a[254:255], v39 offset:41984\n" \
"v_lshl_or_b32 v21, v212, 4, v213\n" \
"v_mfma_f32_16x16x32_bf16 a[96:99], a[216:219], a[224:227], a[96:99]\n" \
"s_add_i32 s44, s44, 0x17000\n" \
"s_or_b32 s45, s7, 0x27000\n" \
"v_mfma_f32_16x16x32_bf16 a[100:103], a[152:155], a[164:167], a[100:103]\n" \
"s_or_b32 s46, s4, 0x10000\n" \
"s_add_i32 s48, s6, 0x3000000\n" \
"v_mfma_f32_16x16x32_bf16 a[100:103], a[216:219], a[228:231], a[100:103]\n" \
"s_add_i32 s49, s5, 0x3004000\n" \
"s_add_i32 s52, s52, 0x1010000\n" \
"v_mfma_f32_16x16x32_bf16 a[104:107], a[152:155], a[168:171], a[104:107]\n" \
"v_mfma_f32_16x16x32_bf16 a[104:107], a[216:219], a[232:235], a[104:107]\n" \
"v_mfma_f32_16x16x32_bf16 a[108:111], a[152:155], a[172:175], a[108:111]\n" \
"v_mfma_f32_16x16x32_bf16 a[108:111], a[216:219], a[236:239], a[108:111]\n" \
"s_waitcnt lgkmcnt(15)\n" \
"v_mfma_f32_16x16x32_bf16 a[124:127], a[156:159], a[172:175], a[124:127]\n" \
"v_mfma_f32_16x16x32_bf16 a[124:127], a[220:223], a[236:239], a[124:127]\n" \
"v_mfma_f32_16x16x32_bf16 a[120:123], a[156:159], a[168:171], a[120:123]\n" \
"v_mfma_f32_16x16x32_bf16 a[120:123], a[220:223], a[232:235], a[120:123]\n" \
"v_mfma_f32_16x16x32_bf16 a[116:119], a[156:159], a[164:167], a[116:119]\n" \
"v_mfma_f32_16x16x32_bf16 a[116:119], a[220:223], a[228:231], a[116:119]\n" \
"v_mfma_f32_16x16x32_bf16 a[112:115], a[156:159], a[160:163], a[112:115]\n" \
"v_mfma_f32_16x16x32_bf16 a[112:115], a[220:223], a[224:227], a[112:115]\n" \
"s_waitcnt vmcnt(1) lgkmcnt(0)\n" \
"s_barrier\n" \
"v_mfma_f32_16x16x32_bf16 v[52:55], a[128:131], a[176:179], v[52:55]\n" \
"s_add_u32 m0, s30, 0x8000\n" \
"s_movk_i32 s84, 0x280\n" \
"v_mfma_f32_16x16x32_bf16 v[52:55], a[192:195], a[240:243], v[52:55]\n" \
"buffer_load_dwordx4 v22, s[64:67], s84 offen offset:0 lds\n" \
"v_mfma_f32_16x16x32_bf16 v[56:59], a[128:131], a[180:183], v[56:59]\n" \
"s_movk_i32 s84, 0x1e80\n" \
"v_mfma_f32_16x16x32_bf16 v[56:59], a[192:195], a[244:247], v[56:59]\n" \
"buffer_load_dwordx4 v23, s[64:67], s84 offen offset:1024 lds\n" \
"v_mfma_f32_16x16x32_bf16 v[60:63], a[128:131], a[184:187], v[60:63]\n" \
"ds_read_b64_tr_b16 a[160:161], v32 offset:0\n" \
"v_mfma_f32_16x16x32_bf16 v[60:63], a[192:195], a[248:251], v[60:63]\n" \
"s_movk_i32 s84, 0x3a80\n" \
"v_mfma_f32_16x16x32_bf16 v[64:67], a[128:131], a[188:191], v[64:67]\n" \
"buffer_load_dwordx4 v22, s[64:67], s84 offen offset:2048 lds\n" \
"v_mfma_f32_16x16x32_bf16 v[64:67], a[192:195], a[252:255], v[64:67]\n" \
"ds_read_b128 a[128:131], v28 offset:16384\n" \
"v_mfma_f32_16x16x32_bf16 v[80:83], a[132:135], a[188:191], v[80:83]\n" \
"ds_read_b128 a[192:195], v29 offset:16384\n" \
"v_mfma_f32_16x16x32_bf16 v[80:83], a[196:199], a[252:255], v[80:83]\n" \
"s_movk_i32 s84, 0x5680\n" \
"s_nop 0\n" \
"v_mfma_f32_16x16x32_bf16 v[76:79], a[132:135], a[184:187], v[76:79]\n" \
"buffer_load_dwordx4 v23, s[64:67], s84 offen offset:3072 lds\n" \
"v_mfma_f32_16x16x32_bf16 v[76:79], a[196:199], a[248:251], v[76:79]\n" \
"s_add_u32 m0, s31, 0x8000\n" \
"s_mov_b32 s84, 0x280000\n" \
"v_mfma_f32_16x16x32_bf16 v[72:75], a[132:135], a[180:183], v[72:75]\n" \
"buffer_load_dwordx4 v26, s[68:71], s84 offen offset:0 lds\n" \
"v_mfma_f32_16x16x32_bf16 v[72:75], a[196:199], a[244:247], v[72:75]\n" \
"ds_read_b64_tr_b16 a[162:163], v32 offset:1024\n" \
"v_mfma_f32_16x16x32_bf16 v[68:71], a[132:135], a[176:179], v[68:71]\n" \
"ds_read_b64_tr_b16 a[224:225], v32 offset:8192\n" \
"v_mfma_f32_16x16x32_bf16 v[68:71], a[196:199], a[240:243], v[68:71]\n" \
"ds_read_b128 a[132:135], v28 offset:18432\n" \
"v_mfma_f32_16x16x32_bf16 v[84:87], a[136:139], a[176:179], v[84:87]\n" \
"ds_read_b128 a[196:199], v29 offset:18432\n" \
"v_mfma_f32_16x16x32_bf16 v[84:87], a[200:203], a[240:243], v[84:87]\n" \
"s_addk_i32 s84, 0x7c00\n" \
"v_mfma_f32_16x16x32_bf16 v[88:91], a[136:139], a[180:183], v[88:91]\n" \
"buffer_load_dwordx4 v26, s[68:71], s84 offen offset:1024 lds\n" \
"v_mfma_f32_16x16x32_bf16 v[88:91], a[200:203], a[244:247], v[88:91]\n" \
"s_addk_i32 s84, 0x7c00\n" \
"v_mfma_f32_16x16x32_bf16 v[92:95], a[136:139], a[184:187], v[92:95]\n" \
"buffer_load_dwordx4 v27, s[68:71], s84 offen offset:2048 lds\n" \
"v_mfma_f32_16x16x32_bf16 v[92:95], a[200:203], a[248:251], v[92:95]\n" \
"s_addk_i32 s84, 0x7c00\n" \
"s_nop 0\n" \
"v_mfma_f32_16x16x32_bf16 v[96:99], a[136:139], a[188:191], v[96:99]\n" \
"buffer_load_dwordx4 v27, s[68:71], s84 offen offset:3072 lds\n" \
"v_mfma_f32_16x16x32_bf16 v[96:99], a[200:203], a[252:255], v[96:99]\n" \
"ds_read_b128 a[136:139], v28 offset:20480\n" \
"v_mfma_f32_16x16x32_bf16 v[112:115], a[140:143], a[188:191], v[112:115]\n" \
"ds_read_b128 a[200:203], v29 offset:20480\n" \
"v_mfma_f32_16x16x32_bf16 v[112:115], a[204:207], a[252:255], v[112:115]\n" \
"s_add_u32 m0, s30, 0x9000\n" \
"s_mov_b32 s84, 0x8280\n" \
"v_mfma_f32_16x16x32_bf16 v[108:111], a[140:143], a[184:187], v[108:111]\n" \
"buffer_load_dwordx4 v22, s[64:67], s84 offen offset:0 lds\n" \
"v_mfma_f32_16x16x32_bf16 v[108:111], a[204:207], a[248:251], v[108:111]\n" \
"s_addk_i32 s84, 0x1c00\n" \
"v_mfma_f32_16x16x32_bf16 v[104:107], a[140:143], a[180:183], v[104:107]\n" \
"buffer_load_dwordx4 v23, s[64:67], s84 offen offset:1024 lds\n" \
"v_mfma_f32_16x16x32_bf16 v[104:107], a[204:207], a[244:247], v[104:107]\n" \
"s_addk_i32 s84, 0x1c00\n" \
"v_mfma_f32_16x16x32_bf16 v[100:103], a[140:143], a[176:179], v[100:103]\n" \
"buffer_load_dwordx4 v22, s[64:67], s84 offen offset:2048 lds\n" \
"v_mfma_f32_16x16x32_bf16 v[100:103], a[204:207], a[240:243], v[100:103]\n" \
"ds_read_b128 a[140:143], v28 offset:22528\n" \
"v_mfma_f32_16x16x32_bf16 v[116:119], a[144:147], a[176:179], v[116:119]\n" \
"ds_read_b128 a[204:207], v29 offset:22528\n" \
"v_mfma_f32_16x16x32_bf16 v[116:119], a[208:211], a[240:243], v[116:119]\n" \
"s_addk_i32 s84, 0x1c00\n" \
"v_mfma_f32_16x16x32_bf16 v[120:123], a[144:147], a[180:183], v[120:123]\n" \
"buffer_load_dwordx4 v23, s[64:67], s84 offen offset:3072 lds\n" \
"v_mfma_f32_16x16x32_bf16 v[120:123], a[208:211], a[244:247], v[120:123]\n" \
"s_add_u32 m0, s31, 0xc000\n" \
"s_mov_b32 s84, 0x280000\n" \
"v_mfma_f32_16x16x32_bf16 v[124:127], a[144:147], a[184:187], v[124:127]\n" \
"buffer_load_dwordx4 v26, s[72:75], s84 offen offset:0 lds\n" \
"v_mfma_f32_16x16x32_bf16 v[124:127], a[208:211], a[248:251], v[124:127]\n" \
"s_addk_i32 s84, 0x7c00\n" \
"v_mfma_f32_16x16x32_bf16 v[128:131], a[144:147], a[188:191], v[128:131]\n" \
"buffer_load_dwordx4 v26, s[72:75], s84 offen offset:1024 lds\n" \
"v_mfma_f32_16x16x32_bf16 v[128:131], a[208:211], a[252:255], v[128:131]\n" \
"ds_read_b128 a[144:147], v28 offset:0\n" \
"v_mfma_f32_16x16x32_bf16 v[144:147], a[148:151], a[188:191], v[144:147]\n" \
"ds_read_b128 a[208:211], v29 offset:0\n" \
"v_mfma_f32_16x16x32_bf16 v[144:147], a[212:215], a[252:255], v[144:147]\n" \
"ds_read_b128 v[200:203], v199 offset:0\n" \
"v_mfma_f32_16x16x32_bf16 v[140:143], a[148:151], a[184:187], v[140:143]\n" \
"ds_read_b128 v[204:207], v199 offset:4096\n" \
"v_mfma_f32_16x16x32_bf16 v[140:143], a[212:215], a[248:251], v[140:143]\n" \
"ds_read_b64_tr_b16 a[226:227], v32 offset:9216\n" \
"v_mfma_f32_16x16x32_bf16 v[136:139], a[148:151], a[180:183], v[136:139]\n" \
"ds_read_b64_tr_b16 a[164:165], v33 offset:0\n" \
"v_mfma_f32_16x16x32_bf16 v[136:139], a[212:215], a[244:247], v[136:139]\n" \
"ds_read_b128 v[208:211], v199 offset:8192\n" \
"v_mfma_f32_16x16x32_bf16 v[132:135], a[148:151], a[176:179], v[132:135]\n" \
"ds_read_b64_tr_b16 a[166:167], v33 offset:1024\n" \
"v_mfma_f32_16x16x32_bf16 v[132:135], a[212:215], a[240:243], v[132:135]\n" \
"ds_read_b128 a[148:151], v28 offset:2048\n" \
"v_mfma_f32_16x16x32_bf16 v[148:151], a[152:155], a[176:179], v[148:151]\n" \
"ds_read_b128 a[212:215], v29 offset:2048\n" \
"v_mfma_f32_16x16x32_bf16 v[148:151], a[216:219], a[240:243], v[148:151]\n" \
"ds_read_b64_tr_b16 a[228:229], v33 offset:8192\n" \
"v_mfma_f32_16x16x32_bf16 v[152:155], a[152:155], a[180:183], v[152:155]\n" \
"ds_read_b64_tr_b16 a[230:231], v33 offset:9216\n" \
"v_mfma_f32_16x16x32_bf16 v[152:155], a[216:219], a[244:247], v[152:155]\n" \
"ds_read_b64_tr_b16 a[168:169], v34 offset:0\n" \
"v_mfma_f32_16x16x32_bf16 v[156:159], a[152:155], a[184:187], v[156:159]\n" \
"ds_read_b64_tr_b16 a[170:171], v34 offset:1024\n" \
"v_mfma_f32_16x16x32_bf16 v[156:159], a[216:219], a[248:251], v[156:159]\n" \
"ds_read_b64_tr_b16 a[232:233], v34 offset:8192\n" \
"v_mfma_f32_16x16x32_bf16 v[160:163], a[152:155], a[188:191], v[160:163]\n" \
"ds_read_b64_tr_b16 a[234:235], v34 offset:9216\n" \
"v_mfma_f32_16x16x32_bf16 v[160:163], a[216:219], a[252:255], v[160:163]\n" \
"ds_read_b128 a[152:155], v28 offset:4096\n" \
"v_mfma_f32_16x16x32_bf16 v[176:179], a[156:159], a[188:191], v[176:179]\n" \
"ds_read_b128 a[216:219], v29 offset:4096\n" \
"v_mfma_f32_16x16x32_bf16 v[176:179], a[220:223], a[252:255], v[176:179]\n" \
"ds_read_b64_tr_b16 a[172:173], v35 offset:0\n" \
"v_mfma_f32_16x16x32_bf16 v[172:175], a[156:159], a[184:187], v[172:175]\n" \
"ds_read_b64_tr_b16 a[174:175], v35 offset:1024\n" \
"v_mfma_f32_16x16x32_bf16 v[172:175], a[220:223], a[248:251], v[172:175]\n" \
"ds_read_b64_tr_b16 a[236:237], v35 offset:8192\n" \
"v_mfma_f32_16x16x32_bf16 v[168:171], a[156:159], a[180:183], v[168:171]\n" \
"ds_read_b64_tr_b16 a[238:239], v35 offset:9216\n" \
"v_mfma_f32_16x16x32_bf16 v[168:171], a[220:223], a[244:247], v[168:171]\n" \
"v_mfma_f32_16x16x32_bf16 v[164:167], a[156:159], a[176:179], v[164:167]\n" \
"v_mfma_f32_16x16x32_bf16 v[164:167], a[220:223], a[240:243], v[164:167]\n" \
"ds_read_b128 a[156:159], v28 offset:6144\n" \
"ds_read_b128 a[220:223], v29 offset:6144\n" \
"s_waitcnt lgkmcnt(15)\n" \
"v_mfma_f32_16x16x32_bf16 a[0:3], a[128:131], a[160:163], a[0:3]\n" \
"s_addk_i32 s84, 0x7c00\n" \
"v_mfma_f32_16x16x32_bf16 a[0:3], a[192:195], a[224:227], a[0:3]\n" \
"buffer_load_dwordx4 v27, s[72:75], s84 offen offset:2048 lds\n" \
"v_mfma_f32_16x16x32_bf16 a[4:7], a[128:131], a[164:167], a[4:7]\n" \
"ds_read_b64_tr_b16 a[176:177], v36 offset:0\n" \
"s_waitcnt lgkmcnt(13)\n" \
"v_mfma_f32_16x16x32_bf16 a[4:7], a[192:195], a[228:231], a[4:7]\n" \
"s_addk_i32 s84, 0x7c00\n" \
"s_waitcnt lgkmcnt(11)\n" \
"v_mfma_f32_16x16x32_bf16 a[8:11], a[128:131], a[168:171], a[8:11]\n" \
"buffer_load_dwordx4 v27, s[72:75], s84 offen offset:3072 lds\n" \
"s_waitcnt lgkmcnt(9)\n" \
"v_mfma_f32_16x16x32_bf16 a[8:11], a[192:195], a[232:235], a[8:11]\n" \
"ds_read_b64_tr_b16 a[178:179], v36 offset:1024\n" \
"s_waitcnt lgkmcnt(6)\n" \
"v_mfma_f32_16x16x32_bf16 a[12:15], a[128:131], a[172:175], a[12:15]\n" \
"ds_read_b64_tr_b16 a[240:241], v36 offset:8192\n" \
"s_waitcnt lgkmcnt(5)\n" \
"v_mfma_f32_16x16x32_bf16 a[12:15], a[192:195], a[236:239], a[12:15]\n" \
"v_mfma_f32_16x16x32_bf16 a[28:31], a[132:135], a[172:175], a[28:31]\n" \
"v_mfma_f32_16x16x32_bf16 a[28:31], a[196:199], a[236:239], a[28:31]\n" \
"ds_read_b64_tr_b16 a[242:243], v36 offset:9216\n" \
"v_mfma_f32_16x16x32_bf16 a[24:27], a[132:135], a[168:171], a[24:27]\n" \
"v_mfma_f32_16x16x32_bf16 a[24:27], a[196:199], a[232:235], a[24:27]\n" \
"v_mfma_f32_16x16x32_bf16 a[20:23], a[132:135], a[164:167], a[20:23]\n" \
"v_mfma_f32_16x16x32_bf16 a[20:23], a[196:199], a[228:231], a[20:23]\n" \
"ds_read_b64_tr_b16 a[180:181], v37 offset:0\n" \
"v_mfma_f32_16x16x32_bf16 a[16:19], a[132:135], a[160:163], a[16:19]\n" \
"v_mfma_f32_16x16x32_bf16 a[16:19], a[196:199], a[224:227], a[16:19]\n" \
"v_mfma_f32_16x16x32_bf16 a[32:35], a[136:139], a[160:163], a[32:35]\n" \
"ds_read_b64_tr_b16 a[182:183], v37 offset:1024\n" \
"v_mfma_f32_16x16x32_bf16 a[32:35], a[200:203], a[224:227], a[32:35]\n" \
"v_mfma_f32_16x16x32_bf16 a[36:39], a[136:139], a[164:167], a[36:39]\n" \
"v_mfma_f32_16x16x32_bf16 a[36:39], a[200:203], a[228:231], a[36:39]\n" \
"buffer_store_dwordx4 v[200:203], v20, s[80:83], s35 offen sc1\n" \
"v_mfma_f32_16x16x32_bf16 a[40:43], a[136:139], a[168:171], a[40:43]\n" \
"s_mov_b32 m0, s32\n" \
"ds_read_b64_tr_b16 a[244:245], v37 offset:8192\n" \
"v_mfma_f32_16x16x32_bf16 a[40:43], a[200:203], a[232:235], a[40:43]\n" \
"s_mov_b32 exec_lo, 0x0\n" \
"s_mov_b32 exec_hi, 0xffffffff\n" \
"global_load_lds_dword v[188:189], off\n" \
"s_mov_b64 exec, -1\n" \
"v_mfma_f32_16x16x32_bf16 a[44:47], a[136:139], a[172:175], a[44:47]\n" \
"ds_read_b64_tr_b16 a[246:247], v37 offset:9216\n" \
"v_mfma_f32_16x16x32_bf16 a[44:47], a[200:203], a[236:239], a[44:47]\n" \
"v_mfma_f32_16x16x32_bf16 a[60:63], a[140:143], a[172:175], a[60:63]\n" \
"v_mfma_f32_16x16x32_bf16 a[60:63], a[204:207], a[236:239], a[60:63]\n" \
"v_mfma_f32_16x16x32_bf16 a[56:59], a[140:143], a[168:171], a[56:59]\n" \
"ds_read_b64_tr_b16 a[184:185], v38 offset:0\n" \
"v_mfma_f32_16x16x32_bf16 a[56:59], a[204:207], a[232:235], a[56:59]\n" \
"v_mfma_f32_16x16x32_bf16 a[52:55], a[140:143], a[164:167], a[52:55]\n" \
"buffer_store_dwordx4 v[204:207], v20, s[80:83], s88 offen sc1\n" \
"v_mfma_f32_16x16x32_bf16 a[52:55], a[204:207], a[228:231], a[52:55]\n" \
"ds_read_b64_tr_b16 a[186:187], v38 offset:1024\n" \
"v_mfma_f32_16x16x32_bf16 a[48:51], a[140:143], a[160:163], a[48:51]\n" \
"v_mfma_f32_16x16x32_bf16 a[48:51], a[204:207], a[224:227], a[48:51]\n" \
"v_mfma_f32_16x16x32_bf16 a[64:67], a[144:147], a[160:163], a[64:67]\n" \
"ds_read_b64_tr_b16 a[248:249], v38 offset:8192\n" \
"v_mfma_f32_16x16x32_bf16 a[64:67], a[208:211], a[224:227], a[64:67]\n" \
"v_mfma_f32_16x16x32_bf16 a[68:71], a[144:147], a[164:167], a[68:71]\n" \
"v_mfma_f32_16x16x32_bf16 a[68:71], a[208:211], a[228:231], a[68:71]\n" \
"ds_read_b64_tr_b16 a[250:251], v38 offset:9216\n" \
"v_mfma_f32_16x16x32_bf16 a[72:75], a[144:147], a[168:171], a[72:75]\n" \
"v_mfma_f32_16x16x32_bf16 a[72:75], a[208:211], a[232:235], a[72:75]\n" \
"v_mfma_f32_16x16x32_bf16 a[76:79], a[144:147], a[172:175], a[76:79]\n" \
"buffer_store_dwordx4 v[208:211], v20, s[80:83], s36 offen sc1\n" \
"v_mfma_f32_16x16x32_bf16 a[76:79], a[208:211], a[236:239], a[76:79]\n" \
"ds_read_b64_tr_b16 a[188:189], v39 offset:0\n" \
"v_mfma_f32_16x16x32_bf16 a[92:95], a[148:151], a[172:175], a[92:95]\n" \
"v_mfma_f32_16x16x32_bf16 a[92:95], a[212:215], a[236:239], a[92:95]\n" \
"v_mfma_f32_16x16x32_bf16 a[88:91], a[148:151], a[168:171], a[88:91]\n" \
"ds_read_b64_tr_b16 a[190:191], v39 offset:1024\n" \
"v_mfma_f32_16x16x32_bf16 a[88:91], a[212:215], a[232:235], a[88:91]\n" \
"v_mfma_f32_16x16x32_bf16 a[84:87], a[148:151], a[164:167], a[84:87]\n" \
"v_mfma_f32_16x16x32_bf16 a[84:87], a[212:215], a[228:231], a[84:87]\n" \
"ds_read_b64_tr_b16 a[252:253], v39 offset:8192\n" \
"v_mfma_f32_16x16x32_bf16 a[80:83], a[148:151], a[160:163], a[80:83]\n" \
"v_mfma_f32_16x16x32_bf16 a[80:83], a[212:215], a[224:227], a[80:83]\n" \
"v_mfma_f32_16x16x32_bf16 a[96:99], a[152:155], a[160:163], a[96:99]\n" \
"ds_read_b64_tr_b16 a[254:255], v39 offset:9216\n" \
"v_mfma_f32_16x16x32_bf16 a[96:99], a[216:219], a[224:227], a[96:99]\n" \
"v_mfma_f32_16x16x32_bf16 a[100:103], a[152:155], a[164:167], a[100:103]\n" \
".fill (L_pb + 52960 - .) / 4, 4, 0xBF800000\n" \
"L_pp13:\n" \
"s_cbranch_execz L_pret\n" \
"v_mfma_f32_16x16x32_bf16 a[100:103], a[216:219], a[228:231], a[100:103]\n" \
"v_mfma_f32_16x16x32_bf16 a[104:107], a[152:155], a[168:171], a[104:107]\n" \
"v_mfma_f32_16x16x32_bf16 a[104:107], a[216:219], a[232:235], a[104:107]\n" \
"v_mfma_f32_16x16x32_bf16 a[108:111], a[152:155], a[172:175], a[108:111]\n" \
"v_mfma_f32_16x16x32_bf16 a[108:111], a[216:219], a[236:239], a[108:111]\n" \
"s_waitcnt lgkmcnt(15)\n" \
"v_mfma_f32_16x16x32_bf16 a[124:127], a[156:159], a[172:175], a[124:127]\n" \
"v_mfma_f32_16x16x32_bf16 a[124:127], a[220:223], a[236:239], a[124:127]\n" \
"v_mfma_f32_16x16x32_bf16 a[120:123], a[156:159], a[168:171], a[120:123]\n" \
"v_mfma_f32_16x16x32_bf16 a[120:123], a[220:223], a[232:235], a[120:123]\n" \
"v_mfma_f32_16x16x32_bf16 a[116:119], a[156:159], a[164:167], a[116:119]\n" \
"v_mfma_f32_16x16x32_bf16 a[116:119], a[220:223], a[228:231], a[116:119]\n" \
"v_mfma_f32_16x16x32_bf16 a[112:115], a[156:159], a[160:163], a[112:115]\n" \
"v_mfma_f32_16x16x32_bf16 a[112:115], a[220:223], a[224:227], a[112:115]\n" \
"s_waitcnt vmcnt(4) lgkmcnt(0)\n" \
"s_barrier\n" \
"v_mfma_f32_16x16x32_bf16 v[52:55], a[128:131], a[176:179], v[52:55]\n" \
"s_add_u32 m0, s30, 0x0\n" \
"s_movk_i32 s84, 0x300\n" \
"v_mfma_f32_16x16x32_bf16 v[52:55], a[192:195], a[240:243], v[52:55]\n" \
"buffer_load_dwordx4 v22, s[64:67], s84 offen offset:0 lds\n" \
"v_mfma_f32_16x16x32_bf16 v[56:59], a[128:131], a[180:183], v[56:59]\n" \
"ds_read_b64_tr_b16 a[160:161], v32 offset:32768\n" \
"v_mfma_f32_16x16x32_bf16 v[56:59], a[192:195], a[244:247], v[56:59]\n" \
"s_movk_i32 s84, 0x1f00\n" \
"s_nop 0\n" \
"v_mfma_f32_16x16x32_bf16 v[60:63], a[128:131], a[184:187], v[60:63]\n" \
"buffer_load_dwordx4 v23, s[64:67], s84 offen offset:1024 lds\n" \
"v_mfma_f32_16x16x32_bf16 v[60:63], a[192:195], a[248:251], v[60:63]\n" \
"ds_read_b64_tr_b16 a[162:163], v32 offset:33792\n" \
"v_mfma_f32_16x16x32_bf16 v[64:67], a[128:131], a[188:191], v[64:67]\n" \
"ds_read_b64_tr_b16 a[224:225], v32 offset:40960\n" \
"v_mfma_f32_16x16x32_bf16 v[64:67], a[192:195], a[252:255], v[64:67]\n" \
"ds_read_b128 a[128:131], v28 offset:49152\n" \
"v_mfma_f32_16x16x32_bf16 v[80:83], a[132:135], a[188:191], v[80:83]\n" \
"ds_read_b128 a[192:195], v29 offset:49152\n" \
"v_mfma_f32_16x16x32_bf16 v[80:83], a[196:199], a[252:255], v[80:83]\n" \
"s_movk_i32 s84, 0x3b00\n" \
"v_mfma_f32_16x16x32_bf16 v[76:79], a[132:135], a[184:187], v[76:79]\n" \
"buffer_load_dwordx4 v22, s[64:67], s84 offen offset:2048 lds\n" \
"v_mfma_f32_16x16x32_bf16 v[76:79], a[196:199], a[248:251], v[76:79]\n" \
"s_movk_i32 s84, 0x5700\n" \
"v_mfma_f32_16x16x32_bf16 v[72:75], a[132:135], a[180:183], v[72:75]\n" \
"buffer_load_dwordx4 v23, s[64:67], s84 offen offset:3072 lds\n" \
"v_mfma_f32_16x16x32_bf16 v[72:75], a[196:199], a[244:247], v[72:75]\n" \
"s_add_u32 m0, s31, 0x0\n" \
"s_mov_b32 s84, 0x300000\n" \
"v_mfma_f32_16x16x32_bf16 v[68:71], a[132:135], a[176:179], v[68:71]\n" \
"buffer_load_dwordx4 v26, s[68:71], s84 offen offset:0 lds\n" \
"v_mfma_f32_16x16x32_bf16 v[68:71], a[196:199], a[240:243], v[68:71]\n" \
"ds_read_b128 a[132:135], v28 offset:51200\n" \
"v_mfma_f32_16x16x32_bf16 v[84:87], a[136:139], a[176:179], v[84:87]\n" \
"ds_read_b128 a[196:199], v29 offset:51200\n" \
"v_mfma_f32_16x16x32_bf16 v[84:87], a[200:203], a[240:243], v[84:87]\n" \
"s_addk_i32 s84, 0x7c00\n" \
"v_mfma_f32_16x16x32_bf16 v[88:91], a[136:139], a[180:183], v[88:91]\n" \
"buffer_load_dwordx4 v26, s[68:71], s84 offen offset:1024 lds\n" \
"v_mfma_f32_16x16x32_bf16 v[88:91], a[200:203], a[244:247], v[88:91]\n" \
"ds_read_b64_tr_b16 a[226:227], v32 offset:41984\n" \
"v_mfma_f32_16x16x32_bf16 v[92:95], a[136:139], a[184:187], v[92:95]\n" \
"s_addk_i32 s84, 0x7c00\n" \
"v_mfma_f32_16x16x32_bf16 v[92:95], a[200:203], a[248:251], v[92:95]\n" \
"buffer_load_dwordx4 v27, s[68:71], s84 offen offset:2048 lds\n" \
"v_mfma_f32_16x16x32_bf16 v[96:99], a[136:139], a[188:191], v[96:99]\n" \
"ds_read_b64_tr_b16 a[164:165], v33 offset:32768\n" \
"v_mfma_f32_16x16x32_bf16 v[96:99], a[200:203], a[252:255], v[96:99]\n" \
"ds_read_b128 a[136:139], v28 offset:53248\n" \
"v_mfma_f32_16x16x32_bf16 v[112:115], a[140:143], a[188:191], v[112:115]\n" \
"ds_read_b128 a[200:203], v29 offset:53248\n" \
"v_mfma_f32_16x16x32_bf16 v[112:115], a[204:207], a[252:255], v[112:115]\n" \
"s_addk_i32 s84, 0x7c00\n" \
"v_mfma_f32_16x16x32_bf16 v[108:111], a[140:143], a[184:187], v[108:111]\n" \
"buffer_load_dwordx4 v27, s[68:71], s84 offen offset:3072 lds\n" \
"v_mfma_f32_16x16x32_bf16 v[108:111], a[204:207], a[248:251], v[108:111]\n" \
"s_add_u32 m0, s30, 0x1000\n" \
"s_mov_b32 s84, 0x8300\n" \
"v_mfma_f32_16x16x32_bf16 v[104:107], a[140:143], a[180:183], v[104:107]\n" \
"buffer_load_dwordx4 v22, s[64:67], s84 offen offset:0 lds\n" \
"v_mfma_f32_16x16x32_bf16 v[104:107], a[204:207], a[244:247], v[104:107]\n" \
"ds_read_b64_tr_b16 a[166:167], v33 offset:33792\n" \
"v_mfma_f32_16x16x32_bf16 v[100:103], a[140:143], a[176:179], v[100:103]\n" \
"ds_read_b64_tr_b16 a[228:229], v33 offset:40960\n" \
"v_mfma_f32_16x16x32_bf16 v[100:103], a[204:207], a[240:243], v[100:103]\n" \
"ds_read_b128 a[140:143], v28 offset:55296\n" \
"v_mfma_f32_16x16x32_bf16 v[116:119], a[144:147], a[176:179], v[116:119]\n" \
"ds_read_b128 a[204:207], v29 offset:55296\n" \
"v_mfma_f32_16x16x32_bf16 v[116:119], a[208:211], a[240:243], v[116:119]\n" \
"s_addk_i32 s84, 0x1c00\n" \
"v_mfma_f32_16x16x32_bf16 v[120:123], a[144:147], a[180:183], v[120:123]\n" \
"buffer_load_dwordx4 v23, s[64:67], s84 offen offset:1024 lds\n" \
"v_mfma_f32_16x16x32_bf16 v[120:123], a[208:211], a[244:247], v[120:123]\n" \
"s_addk_i32 s84, 0x1c00\n" \
"v_mfma_f32_16x16x32_bf16 v[124:127], a[144:147], a[184:187], v[124:127]\n" \
"buffer_load_dwordx4 v22, s[64:67], s84 offen offset:2048 lds\n" \
"v_mfma_f32_16x16x32_bf16 v[124:127], a[208:211], a[248:251], v[124:127]\n" \
"s_addk_i32 s84, 0x1c00\n" \
"s_nop 0\n" \
"v_mfma_f32_16x16x32_bf16 v[128:131], a[144:147], a[188:191], v[128:131]\n" \
"buffer_load_dwordx4 v23, s[64:67], s84 offen offset:3072 lds\n" \
"v_mfma_f32_16x16x32_bf16 v[128:131], a[208:211], a[252:255], v[128:131]\n" \
"ds_read_b128 a[144:147], v28 offset:32768\n" \
"v_mfma_f32_16x16x32_bf16 v[144:147], a[148:151], a[188:191], v[144:147]\n" \
"ds_read_b128 a[208:211], v29 offset:32768\n" \
"v_mfma_f32_16x16x32_bf16 v[144:147], a[212:215], a[252:255], v[144:147]\n" \
"s_add_u32 m0, s31, 0x4000\n" \
"s_mov_b32 s84, 0x300000\n" \
"v_mfma_f32_16x16x32_bf16 v[140:143], a[148:151], a[184:187], v[140:143]\n" \
"buffer_load_dwordx4 v26, s[72:75], s84 offen offset:0 lds\n" \
"v_mfma_f32_16x16x32_bf16 v[140:143], a[212:215], a[248:251], v[140:143]\n" \
"ds_read_b64_tr_b16 a[230:231], v33 offset:41984\n" \
"v_mfma_f32_16x16x32_bf16 v[136:139], a[148:151], a[180:183], v[136:139]\n" \
"s_addk_i32 s84, 0x7c00\n" \
"s_nop 0\n" \
"v_mfma_f32_16x16x32_bf16 v[136:139], a[212:215], a[244:247], v[136:139]\n" \
"buffer_load_dwordx4 v26, s[72:75], s84 offen offset:1024 lds\n" \
"v_mfma_f32_16x16x32_bf16 v[132:135], a[148:151], a[176:179], v[132:135]\n" \
"ds_read_b64_tr_b16 a[168:169], v34 offset:32768\n" \
"v_mfma_f32_16x16x32_bf16 v[132:135], a[212:215], a[240:243], v[132:135]\n" \
"ds_read_b128 a[148:151], v28 offset:34816\n" \
"v_mfma_f32_16x16x32_bf16 v[148:151], a[152:155], a[176:179], v[148:151]\n" \
"ds_read_b128 a[212:215], v29 offset:34816\n" \
"v_mfma_f32_16x16x32_bf16 v[148:151], a[216:219], a[240:243], v[148:151]\n" \
"ds_read_b64_tr_b16 a[170:171], v34 offset:33792\n" \
"v_mfma_f32_16x16x32_bf16 v[152:155], a[152:155], a[180:183], v[152:155]\n" \
"ds_read_b64_tr_b16 a[232:233], v34 offset:40960\n" \
"v_mfma_f32_16x16x32_bf16 v[152:155], a[216:219], a[244:247], v[152:155]\n" \
"ds_read_b64_tr_b16 a[234:235], v34 offset:41984\n" \
"v_mfma_f32_16x16x32_bf16 v[156:159], a[152:155], a[184:187], v[156:159]\n" \
"ds_read_b64_tr_b16 a[172:173], v35 offset:32768\n" \
"v_mfma_f32_16x16x32_bf16 v[156:159], a[216:219], a[248:251], v[156:159]\n" \
"ds_read_b64_tr_b16 a[174:175], v35 offset:33792\n" \
"v_mfma_f32_16x16x32_bf16 v[160:163], a[152:155], a[188:191], v[160:163]\n" \
"ds_read_b64_tr_b16 a[236:237], v35 offset:40960\n" \
"v_mfma_f32_16x16x32_bf16 v[160:163], a[216:219], a[252:255], v[160:163]\n" \
"ds_read_b128 a[152:155], v28 offset:36864\n" \
"v_mfma_f32_16x16x32_bf16 v[176:179], a[156:159], a[188:191], v[176:179]\n" \
"ds_read_b128 a[216:219], v29 offset:36864\n" \
"v_mfma_f32_16x16x32_bf16 v[176:179], a[220:223], a[252:255], v[176:179]\n" \
"ds_read_b64_tr_b16 a[238:239], v35 offset:41984\n" \
"v_mfma_f32_16x16x32_bf16 v[172:175], a[156:159], a[184:187], v[172:175]\n" \
"v_mfma_f32_16x16x32_bf16 v[172:175], a[220:223], a[248:251], v[172:175]\n" \
"v_mfma_f32_16x16x32_bf16 v[168:171], a[156:159], a[180:183], v[168:171]\n" \
"v_mfma_f32_16x16x32_bf16 v[168:171], a[220:223], a[244:247], v[168:171]\n" \
"v_mfma_f32_16x16x32_bf16 v[164:167], a[156:159], a[176:179], v[164:167]\n" \
"v_mfma_f32_16x16x32_bf16 v[164:167], a[220:223], a[240:243], v[164:167]\n" \
"ds_read_b128 a[156:159], v28 offset:38912\n" \
"ds_read_b128 a[220:223], v29 offset:38912\n" \
"s_waitcnt lgkmcnt(15)\n" \
"v_mfma_f32_16x16x32_bf16 a[0:3], a[128:131], a[160:163], a[0:3]\n" \
"s_addk_i32 s84, 0x7c00\n" \
"v_mfma_f32_16x16x32_bf16 a[0:3], a[192:195], a[224:227], a[0:3]\n" \
"buffer_load_dwordx4 v27, s[72:75], s84 offen offset:2048 lds\n" \
"v_mfma_f32_16x16x32_bf16 a[4:7], a[128:131], a[164:167], a[4:7]\n" \
"ds_read_b64_tr_b16 a[176:177], v36 offset:32768\n" \
"s_waitcnt lgkmcnt(15)\n" \
"v_mfma_f32_16x16x32_bf16 a[4:7], a[192:195], a[228:231], a[4:7]\n" \
"s_addk_i32 s84, 0x7c00\n" \
"s_waitcnt lgkmcnt(11)\n" \
"v_mfma_f32_16x16x32_bf16 a[8:11], a[128:131], a[168:171], a[8:11]\n" \
"buffer_load_dwordx4 v27, s[72:75], s84 offen offset:3072 lds\n" \
"s_waitcnt lgkmcnt(9)\n" \
"v_mfma_f32_16x16x32_bf16 a[8:11], a[192:195], a[232:235], a[8:11]\n" \
"ds_read_b64_tr_b16 a[178:179], v36 offset:33792\n" \
"s_waitcnt lgkmcnt(8)\n" \
"v_mfma_f32_16x16x32_bf16 a[12:15], a[128:131], a[172:175], a[12:15]\n" \
"ds_read_b64_tr_b16 a[240:241], v36 offset:40960\n" \
"s_waitcnt lgkmcnt(5)\n" \
"v_mfma_f32_16x16x32_bf16 a[12:15], a[192:195], a[236:239], a[12:15]\n" \
"v_mfma_f32_16x16x32_bf16 a[28:31], a[132:135], a[172:175], a[28:31]\n" \
"v_mfma_f32_16x16x32_bf16 a[28:31], a[196:199], a[236:239], a[28:31]\n" \
"ds_read_b64_tr_b16 a[242:243], v36 offset:41984\n" \
"v_mfma_f32_16x16x32_bf16 a[24:27], a[132:135], a[168:171], a[24:27]\n" \
"v_mfma_f32_16x16x32_bf16 a[24:27], a[196:199], a[232:235], a[24:27]\n" \
"v_mfma_f32_16x16x32_bf16 a[20:23], a[132:135], a[164:167], a[20:23]\n" \
"v_mfma_f32_16x16x32_bf16 a[20:23], a[196:199], a[228:231], a[20:23]\n" \
"ds_read_b64_tr_b16 a[180:181], v37 offset:32768\n" \
"v_mfma_f32_16x16x32_bf16 a[16:19], a[132:135], a[160:163], a[16:19]\n" \
"v_mfma_f32_16x16x32_bf16 a[16:19], a[196:199], a[224:227], a[16:19]\n" \
"v_mfma_f32_16x16x32_bf16 a[32:35], a[136:139], a[160:163], a[32:35]\n" \
"ds_read_b64_tr_b16 a[182:183], v37 offset:33792\n" \
"v_mfma_f32_16x16x32_bf16 a[32:35], a[200:203], a[224:227], a[32:35]\n" \
"v_mfma_f32_16x16x32_bf16 a[36:39], a[136:139], a[164:167], a[36:39]\n" \
"v_lshlrev_b32_e32 v45, 3, v20\n" \
"v_add_u32_e32 v45, s31, v45\n" \
"v_mfma_f32_16x16x32_bf16 a[36:39], a[200:203], a[228:231], a[36:39]\n" \
"s_getpc_b64 s[86:87]\n" \
"s_sub_u32 s86, s86, 0x10800\n" \
"s_subb_u32 s87, s87, 0\n" \
"s_mov_b32 m0, s32\n" \
"s_nop 0\n" \
"v_mfma_f32_16x16x32_bf16 a[40:43], a[136:139], a[168:171], a[40:43]\n" \
"global_load_lds_dword v45, s[86:87]\n" \
"v_mfma_f32_16x16x32_bf16 a[40:43], a[200:203], a[232:235], a[40:43]\n" \
"ds_read_b64_tr_b16 a[244:245], v37 offset:40960\n" \
"v_mfma_f32_16x16x32_bf16 a[44:47], a[136:139], a[172:175], a[44:47]\n" \
"ds_read_b64_tr_b16 a[246:247], v37 offset:41984\n" \
"v_mfma_f32_16x16x32_bf16 a[44:47], a[200:203], a[236:239], a[44:47]\n" \
"v_mfma_f32_16x16x32_bf16 a[60:63], a[140:143], a[172:175], a[60:63]\n" \
"v_mfma_f32_16x16x32_bf16 a[60:63], a[204:207], a[236:239], a[60:63]\n" \
"v_mfma_f32_16x16x32_bf16 a[56:59], a[140:143], a[168:171], a[56:59]\n" \
"ds_read_b64_tr_b16 a[184:185], v38 offset:32768\n" \
"v_mfma_f32_16x16x32_bf16 a[56:59], a[204:207], a[232:235], a[56:59]\n" \
"v_mfma_f32_16x16x32_bf16 a[52:55], a[140:143], a[164:167], a[52:55]\n" \
"v_mfma_f32_16x16x32_bf16 a[52:55], a[204:207], a[228:231], a[52:55]\n" \
"ds_read_b64_tr_b16 a[186:187], v38 offset:33792\n" \
"v_mfma_f32_16x16x32_bf16 a[48:51], a[140:143], a[160:163], a[48:51]\n" \
"v_mfma_f32_16x16x32_bf16 a[48:51], a[204:207], a[224:227], a[48:51]\n" \
"v_mfma_f32_16x16x32_bf16 a[64:67], a[144:147], a[160:163], a[64:67]\n" \
"ds_read_b64_tr_b16 a[248:249], v38 offset:40960\n" \
"v_mfma_f32_16x16x32_bf16 a[64:67], a[208:211], a[224:227], a[64:67]\n" \
"v_mfma_f32_16x16x32_bf16 a[68:71], a[144:147], a[164:167], a[68:71]\n" \
"v_mfma_f32_16x16x32_bf16 a[68:71], a[208:211], a[228:231], a[68:71]\n" \
"ds_read_b64_tr_b16 a[250:251], v38 offset:41984\n" \
"v_mfma_f32_16x16x32_bf16 a[72:75], a[144:147], a[168:171], a[72:75]\n" \
"v_mfma_f32_16x16x32_bf16 a[72:75], a[208:211], a[232:235], a[72:75]\n" \
"v_mfma_f32_16x16x32_bf16 a[76:79], a[144:147], a[172:175], a[76:79]\n" \
"v_mfma_f32_16x16x32_bf16 a[76:79], a[208:211], a[236:239], a[76:79]\n" \
"ds_read_b64_tr_b16 a[188:189], v39 offset:32768\n" \
"v_mfma_f32_16x16x32_bf16 a[92:95], a[148:151], a[172:175], a[92:95]\n" \
"v_mfma_f32_16x16x32_bf16 a[92:95], a[212:215], a[236:239], a[92:95]\n" \
"v_mfma_f32_16x16x32_bf16 a[88:91], a[148:151], a[168:171], a[88:91]\n" \
"ds_read_b64_tr_b16 a[190:191], v39 offset:33792\n" \
"v_mfma_f32_16x16x32_bf16 a[88:91], a[212:215], a[232:235], a[88:91]\n" \
"v_mfma_f32_16x16x32_bf16 a[84:87], a[148:151], a[164:167], a[84:87]\n" \
"v_mfma_f32_16x16x32_bf16 a[84:87], a[212:215], a[228:231], a[84:87]\n" \
"ds_read_b64_tr_b16 a[252:253], v39 offset:40960\n" \
"v_mfma_f32_16x16x32_bf16 a[80:83], a[148:151], a[160:163], a[80:83]\n" \
"v_mfma_f32_16x16x32_bf16 a[80:83], a[212:215], a[224:227], a[80:83]\n" \
"v_mfma_f32_16x16x32_bf16 a[96:99], a[152:155], a[160:163], a[96:99]\n" \
"ds_read_b64_tr_b16 a[254:255], v39 offset:41984\n" \
"v_mfma_f32_16x16x32_bf16 a[96:99], a[216:219], a[224:227], a[96:99]\n" \
"v_mfma_f32_16x16x32_bf16 a[100:103], a[152:155], a[164:167], a[100:103]\n" \
"v_mfma_f32_16x16x32_bf16 a[100:103], a[216:219], a[228:231], a[100:103]\n" \
"v_mfma_f32_16x16x32_bf16 a[104:107], a[152:155], a[168:171], a[104:107]\n" \
"v_mfma_f32_16x16x32_bf16 a[104:107], a[216:219], a[232:235], a[104:107]\n" \
"v_mfma_f32_16x16x32_bf16 a[108:111], a[152:155], a[172:175], a[108:111]\n" \
"v_mfma_f32_16x16x32_bf16 a[108:111], a[216:219], a[236:239], a[108:111]\n" \
"s_waitcnt lgkmcnt(15)\n" \
"v_mfma_f32_16x16x32_bf16 a[124:127], a[156:159], a[172:175], a[124:127]\n" \
"v_mfma_f32_16x16x32_bf16 a[124:127], a[220:223], a[236:239], a[124:127]\n" \
"v_mfma_f32_16x16x32_bf16 a[120:123], a[156:159], a[168:171], a[120:123]\n" \
"v_mfma_f32_16x16x32_bf16 a[120:123], a[220:223], a[232:235], a[120:123]\n" \
"v_mfma_f32_16x16x32_bf16 a[116:119], a[156:159], a[164:167], a[116:119]\n" \
"v_mfma_f32_16x16x32_bf16 a[116:119], a[220:223], a[228:231], a[116:119]\n" \
"v_mfma_f32_16x16x32_bf16 a[112:115], a[156:159], a[160:163], a[112:115]\n" \
"v_mfma_f32_16x16x32_bf16 a[112:115], a[220:223], a[224:227], a[112:115]\n" \
"s_waitcnt vmcnt(1) lgkmcnt(0)\n" \
"s_barrier\n" \
"v_mov_b32_e32 v45, s62\n" \
"s_mov_b64 exec, 1\n" \
"buffer_store_dword v45, off, s[80:83], s37 offset:128 sc1\n" \
"s_mov_b64 exec, -1\n" \
"v_mfma_f32_16x16x32_bf16 v[52:55], a[128:131], a[176:179], v[52:55]\n" \
"s_add_u32 m0, s30, 0x8000\n" \
"s_movk_i32 s84, 0x380\n" \
"v_mfma_f32_16x16x32_bf16 v[52:55], a[192:195], a[240:243], v[52:55]\n" \
"buffer_load_dwordx4 v22, s[64:67], s84 offen offset:0 lds\n" \
"v_mfma_f32_16x16x32_bf16 v[56:59], a[128:131], a[180:183], v[56:59]\n" \
"ds_read_b64_tr_b16 a[160:161], v32 offset:0\n" \
"v_mfma_f32_16x16x32_bf16 v[56:59], a[192:195], a[244:247], v[56:59]\n" \
"s_movk_i32 s84, 0x1f80\n" \
"v_mfma_f32_16x16x32_bf16 v[60:63], a[128:131], a[184:187], v[60:63]\n" \
"buffer_load_dwordx4 v23, s[64:67], s84 offen offset:1024 lds\n" \
"v_mfma_f32_16x16x32_bf16 v[60:63], a[192:195], a[248:251], v[60:63]\n" \
"ds_read_b64_tr_b16 a[162:163], v32 offset:1024\n" \
"v_mfma_f32_16x16x32_bf16 v[64:67], a[128:131], a[188:191], v[64:67]\n" \
"ds_read_b64_tr_b16 a[224:225], v32 offset:8192\n" \
"v_mfma_f32_16x16x32_bf16 v[64:67], a[192:195], a[252:255], v[64:67]\n" \
"ds_read_b128 a[128:131], v28 offset:16384\n" \
"v_mfma_f32_16x16x32_bf16 v[80:83], a[132:135], a[188:191], v[80:83]\n" \
"ds_read_b128 a[192:195], v29 offset:16384\n" \
"v_mfma_f32_16x16x32_bf16 v[80:83], a[196:199], a[252:255], v[80:83]\n" \
"s_movk_i32 s84, 0x3b80\n" \
"v_mfma_f32_16x16x32_bf16 v[76:79], a[132:135], a[184:187], v[76:79]\n" \
"buffer_load_dwordx4 v22, s[64:67], s84 offen offset:2048 lds\n" \
"v_mfma_f32_16x16x32_bf16 v[76:79], a[196:199], a[248:251], v[76:79]\n" \
"s_movk_i32 s84, 0x5780\n" \
"v_mfma_f32_16x16x32_bf16 v[72:75], a[132:135], a[180:183], v[72:75]\n" \
"buffer_load_dwordx4 v23, s[64:67], s84 offen offset:3072 lds\n" \
"v_mfma_f32_16x16x32_bf16 v[72:75], a[196:199], a[244:247], v[72:75]\n" \
"s_add_u32 m0, s31, 0x8000\n" \
"s_mov_b32 s84, 0x380000\n" \
"v_mfma_f32_16x16x32_bf16 v[68:71], a[132:135], a[176:179], v[68:71]\n" \
"buffer_load_dwordx4 v26, s[68:71], s84 offen offset:0 lds\n" \
"v_mfma_f32_16x16x32_bf16 v[68:71], a[196:199], a[240:243], v[68:71]\n" \
"ds_read_b128 a[132:135], v28 offset:18432\n" \
"v_mfma_f32_16x16x32_bf16 v[84:87], a[136:139], a[176:179], v[84:87]\n" \
"ds_read_b128 a[196:199], v29 offset:18432\n" \
"v_mfma_f32_16x16x32_bf16 v[84:87], a[200:203], a[240:243], v[84:87]\n" \
"s_addk_i32 s84, 0x7c00\n" \
"v_mfma_f32_16x16x32_bf16 v[88:91], a[136:139], a[180:183], v[88:91]\n" \
"buffer_load_dwordx4 v26, s[68:71], s84 offen offset:1024 lds\n" \
"v_mfma_f32_16x16x32_bf16 v[88:91], a[200:203], a[244:247], v[88:91]\n" \
"ds_read_b64_tr_b16 a[226:227], v32 offset:9216\n" \
"v_mfma_f32_16x16x32_bf16 v[92:95], a[136:139], a[184:187], v[92:95]\n" \
"s_addk_i32 s84, 0x7c00\n" \
"v_mfma_f32_16x16x32_bf16 v[92:95], a[200:203], a[248:251], v[92:95]\n" \
"buffer_load_dwordx4 v27, s[68:71], s84 offen offset:2048 lds\n" \
"v_mfma_f32_16x16x32_bf16 v[96:99], a[136:139], a[188:191], v[96:99]\n" \
"ds_read_b64_tr_b16 a[164:165], v33 offset:0\n" \
"v_mfma_f32_16x16x32_bf16 v[96:99], a[200:203], a[252:255], v[96:99]\n" \
"ds_read_b128 a[136:139], v28 offset:20480\n" \
"v_mfma_f32_16x16x32_bf16 v[112:115], a[140:143], a[188:191], v[112:115]\n" \
"ds_read_b128 a[200:203], v29 offset:20480\n" \
"v_mfma_f32_16x16x32_bf16 v[112:115], a[204:207], a[252:255], v[112:115]\n" \
"s_addk_i32 s84, 0x7c00\n" \
"s_nop 0\n" \
"v_mfma_f32_16x16x32_bf16 v[108:111], a[140:143], a[184:187], v[108:111]\n" \
"buffer_load_dwordx4 v27, s[68:71], s84 offen offset:3072 lds\n" \
"v_mfma_f32_16x16x32_bf16 v[108:111], a[204:207], a[248:251], v[108:111]\n" \
"s_add_u32 m0, s30, 0x9000\n" \
"s_mov_b32 s84, 0x8380\n" \
"v_mfma_f32_16x16x32_bf16 v[104:107], a[140:143], a[180:183], v[104:107]\n" \
"buffer_load_dwordx4 v22, s[64:67], s84 offen offset:0 lds\n" \
"v_mfma_f32_16x16x32_bf16 v[104:107], a[204:207], a[244:247], v[104:107]\n" \
"ds_read_b64_tr_b16 a[166:167], v33 offset:1024\n" \
"v_mfma_f32_16x16x32_bf16 v[100:103], a[140:143], a[176:179], v[100:103]\n" \
"ds_read_b64_tr_b16 a[228:229], v33 offset:8192\n" \
"v_mfma_f32_16x16x32_bf16 v[100:103], a[204:207], a[240:243], v[100:103]\n" \
"ds_read_b128 a[140:143], v28 offset:22528\n" \
"v_mfma_f32_16x16x32_bf16 v[116:119], a[144:147], a[176:179], v[116:119]\n" \
"ds_read_b128 a[204:207], v29 offset:22528\n" \
"v_mfma_f32_16x16x32_bf16 v[116:119], a[208:211], a[240:243], v[116:119]\n" \
"s_addk_i32 s84, 0x1c00\n" \
"v_mfma_f32_16x16x32_bf16 v[120:123], a[144:147], a[180:183], v[120:123]\n" \
"buffer_load_dwordx4 v23, s[64:67], s84 offen offset:1024 lds\n" \
"v_mfma_f32_16x16x32_bf16 v[120:123], a[208:211], a[244:247], v[120:123]\n" \
"s_addk_i32 s84, 0x1c00\n" \
"v_mfma_f32_16x16x32_bf16 v[124:127], a[144:147], a[184:187], v[124:127]\n" \
"buffer_load_dwordx4 v22, s[64:67], s84 offen offset:2048 lds\n" \
"v_mfma_f32_16x16x32_bf16 v[124:127], a[208:211], a[248:251], v[124:127]\n" \
"s_addk_i32 s84, 0x1c00\n" \
"s_nop 0\n" \
"v_mfma_f32_16x16x32_bf16 v[128:131], a[144:147], a[188:191], v[128:131]\n" \
"buffer_load_dwordx4 v23, s[64:67], s84 offen offset:3072 lds\n" \
"v_mfma_f32_16x16x32_bf16 v[128:131], a[208:211], a[252:255], v[128:131]\n" \
"ds_read_b128 a[144:147], v28 offset:0\n" \
"v_mfma_f32_16x16x32_bf16 v[144:147], a[148:151], a[188:191], v[144:147]\n" \
"ds_read_b128 a[208:211], v29 offset:0\n" \
"v_mfma_f32_16x16x32_bf16 v[144:147], a[212:215], a[252:255], v[144:147]\n" \
"s_add_u32 m0, s31, 0xc000\n" \
"s_mov_b32 s84, 0x380000\n" \
"v_mfma_f32_16x16x32_bf16 v[140:143], a[148:151], a[184:187], v[140:143]\n" \
"buffer_load_dwordx4 v26, s[72:75], s84 offen offset:0 lds\n" \
"v_mfma_f32_16x16x32_bf16 v[140:143], a[212:215], a[248:251], v[140:143]\n" \
"ds_read_b64_tr_b16 a[230:231], v33 offset:9216\n" \
"v_mfma_f32_16x16x32_bf16 v[136:139], a[148:151], a[180:183], v[136:139]\n" \
"s_addk_i32 s84, 0x7c00\n" \
"s_nop 0\n" \
"v_mfma_f32_16x16x32_bf16 v[136:139], a[212:215], a[244:247], v[136:139]\n" \
"buffer_load_dwordx4 v26, s[72:75], s84 offen offset:1024 lds\n" \
"v_mfma_f32_16x16x32_bf16 v[132:135], a[148:151], a[176:179], v[132:135]\n" \
"ds_read_b64_tr_b16 a[168:169], v34 offset:0\n" \
"v_mfma_f32_16x16x32_bf16 v[132:135], a[212:215], a[240:243], v[132:135]\n" \
"ds_read_b128 a[148:151], v28 offset:2048\n" \
"v_mfma_f32_16x16x32_bf16 v[148:151], a[152:155], a[176:179], v[148:151]\n" \
"ds_read_b128 a[212:215], v29 offset:2048\n" \
"v_mfma_f32_16x16x32_bf16 v[148:151], a[216:219], a[240:243], v[148:151]\n" \
"ds_read_b64_tr_b16 a[170:171], v34 offset:1024\n" \
"v_mfma_f32_16x16x32_bf16 v[152:155], a[152:155], a[180:183], v[152:155]\n" \
"ds_read_b64_tr_b16 a[232:233], v34 offset:8192\n" \
"v_mfma_f32_16x16x32_bf16 v[152:155], a[216:219], a[244:247], v[152:155]\n" \
"ds_read_b64_tr_b16 a[234:235], v34 offset:9216\n" \
"v_mfma_f32_16x16x32_bf16 v[156:159], a[152:155], a[184:187], v[156:159]\n" \
"ds_read_b64_tr_b16 a[172:173], v35 offset:0\n" \
"v_mfma_f32_16x16x32_bf16 v[156:159], a[216:219], a[248:251], v[156:159]\n" \
"ds_read_b64_tr_b16 a[174:175], v35 offset:1024\n" \
"v_mfma_f32_16x16x32_bf16 v[160:163], a[152:155], a[188:191], v[160:163]\n" \
"ds_read_b64_tr_b16 a[236:237], v35 offset:8192\n" \
"v_mfma_f32_16x16x32_bf16 v[160:163], a[216:219], a[252:255], v[160:163]\n" \
"ds_read_b128 a[152:155], v28 offset:4096\n" \
"v_mfma_f32_16x16x32_bf16 v[176:179], a[156:159], a[188:191], v[176:179]\n" \
"ds_read_b128 a[216:219], v29 offset:4096\n" \
"v_mfma_f32_16x16x32_bf16 v[176:179], a[220:223], a[252:255], v[176:179]\n" \
"ds_read_b64_tr_b16 a[238:239], v35 offset:9216\n" \
"v_mfma_f32_16x16x32_bf16 v[172:175], a[156:159], a[184:187], v[172:175]\n" \
"v_mfma_f32_16x16x32_bf16 v[172:175], a[220:223], a[248:251], v[172:175]\n" \
"v_mfma_f32_16x16x32_bf16 v[168:171], a[156:159], a[180:183], v[168:171]\n" \
"v_mfma_f32_16x16x32_bf16 v[168:171], a[220:223], a[244:247], v[168:171]\n" \
"v_mfma_f32_16x16x32_bf16 v[164:167], a[156:159], a[176:179], v[164:167]\n" \
"v_mfma_f32_16x16x32_bf16 v[164:167], a[220:223], a[240:243], v[164:167]\n" \
"ds_read_b128 a[156:159], v28 offset:6144\n" \
"ds_read_b128 a[220:223], v29 offset:6144\n" \
"s_waitcnt lgkmcnt(15)\n" \
"v_mfma_f32_16x16x32_bf16 a[0:3], a[128:131], a[160:163], a[0:3]\n" \
"s_addk_i32 s84, 0x7c00\n" \
"v_mfma_f32_16x16x32_bf16 a[0:3], a[192:195], a[224:227], a[0:3]\n" \
"buffer_load_dwordx4 v27, s[72:75], s84 offen offset:2048 lds\n" \
"v_mfma_f32_16x16x32_bf16 a[4:7], a[128:131], a[164:167], a[4:7]\n" \
"ds_read_b64_tr_b16 a[176:177], v36 offset:0\n" \
"s_waitcnt lgkmcnt(15)\n" \
"v_mfma_f32_16x16x32_bf16 a[4:7], a[192:195], a[228:231], a[4:7]\n" \
"s_addk_i32 s84, 0x7c00\n" \
"s_waitcnt lgkmcnt(11)\n" \
"v_mfma_f32_16x16x32_bf16 a[8:11], a[128:131], a[168:171], a[8:11]\n" \
"buffer_load_dwordx4 v27, s[72:75], s84 offen offset:3072 lds\n" \
"s_waitcnt lgkmcnt(9)\n" \
"v_mfma_f32_16x16x32_bf16 a[8:11], a[192:195], a[232:235], a[8:11]\n" \
"ds_read_b64_tr_b16 a[178:179], v36 offset:1024\n" \
"s_waitcnt lgkmcnt(8)\n" \
"v_mfma_f32_16x16x32_bf16 a[12:15], a[128:131], a[172:175], a[12:15]\n" \
"ds_read_b64_tr_b16 a[240:241], v36 offset:8192\n" \
"s_waitcnt lgkmcnt(5)\n" \
"v_mfma_f32_16x16x32_bf16 a[12:15], a[192:195], a[236:239], a[12:15]\n" \
"v_mfma_f32_16x16x32_bf16 a[28:31], a[132:135], a[172:175], a[28:31]\n" \
"v_mfma_f32_16x16x32_bf16 a[28:31], a[196:199], a[236:239], a[28:31]\n" \
"ds_read_b64_tr_b16 a[242:243], v36 offset:9216\n" \
"v_mfma_f32_16x16x32_bf16 a[24:27], a[132:135], a[168:171], a[24:27]\n" \
"v_mfma_f32_16x16x32_bf16 a[24:27], a[196:199], a[232:235], a[24:27]\n" \
"v_mfma_f32_16x16x32_bf16 a[20:23], a[132:135], a[164:167], a[20:23]\n" \
"v_mfma_f32_16x16x32_bf16 a[20:23], a[196:199], a[228:231], a[20:23]\n" \
"ds_read_b64_tr_b16 a[180:181], v37 offset:0\n" \
"v_mfma_f32_16x16x32_bf16 a[16:19], a[132:135], a[160:163], a[16:19]\n" \
"v_mfma_f32_16x16x32_bf16 a[16:19], a[196:199], a[224:227], a[16:19]\n" \
"v_mfma_f32_16x16x32_bf16 a[32:35], a[136:139], a[160:163], a[32:35]\n" \
"ds_read_b64_tr_b16 a[182:183], v37 offset:1024\n" \
"v_mfma_f32_16x16x32_bf16 a[32:35], a[200:203], a[224:227], a[32:35]\n" \
"v_mfma_f32_16x16x32_bf16 a[36:39], a[136:139], a[164:167], a[36:39]\n" \
"v_mfma_f32_16x16x32_bf16 a[36:39], a[200:203], a[228:231], a[36:39]\n" \
"ds_read_b64_tr_b16 a[244:245], v37 offset:8192\n" \
"v_mfma_f32_16x16x32_bf16 a[40:43], a[136:139], a[168:171], a[40:43]\n" \
"v_mfma_f32_16x16x32_bf16 a[40:43], a[200:203], a[232:235], a[40:43]\n" \
"v_mfma_f32_16x16x32_bf16 a[44:47], a[136:139], a[172:175], a[44:47]\n" \
"ds_read_b64_tr_b16 a[246:247], v37 offset:9216\n" \
"v_mfma_f32_16x16x32_bf16 a[44:47], a[200:203], a[236:239], a[44:47]\n" \
"v_mfma_f32_16x16x32_bf16 a[60:63], a[140:143], a[172:175], a[60:63]\n" \
"v_mfma_f32_16x16x32_bf16 a[60:63], a[204:207], a[236:239], a[60:63]\n" \
"v_mfma_f32_16x16x32_bf16 a[56:59], a[140:143], a[168:171], a[56:59]\n" \
"ds_read_b64_tr_b16 a[184:185], v38 offset:0\n" \
"v_mfma_f32_16x16x32_bf16 a[56:59], a[204:207], a[232:235], a[56:59]\n" \
"v_mfma_f32_16x16x32_bf16 a[52:55], a[140:143], a[164:167], a[52:55]\n" \
"v_mfma_f32_16x16x32_bf16 a[52:55], a[204:207], a[228:231], a[52:55]\n" \
"ds_read_b64_tr_b16 a[186:187], v38 offset:1024\n" \
"v_mfma_f32_16x16x32_bf16 a[48:51], a[140:143], a[160:163], a[48:51]\n" \
"v_mfma_f32_16x16x32_bf16 a[48:51], a[204:207], a[224:227], a[48:51]\n" \
"v_mfma_f32_16x16x32_bf16 a[64:67], a[144:147], a[160:163], a[64:67]\n" \
"v_bfe_u32 v44, v20, 2, 5\n" \
"ds_read_b64_tr_b16 a[248:249], v38 offset:8192\n" \
"v_mfma_f32_16x16x32_bf16 a[64:67], a[208:211], a[224:227], a[64:67]\n" \
"buffer_load_dword v44, v44, s[80:83], s38 offen offset:128 sc1\n" \
"v_mfma_f32_16x16x32_bf16 a[68:71], a[144:147], a[164:167], a[68:71]\n" \
"v_mfma_f32_16x16x32_bf16 a[68:71], a[208:211], a[228:231], a[68:71]\n" \
"ds_read_b64_tr_b16 a[250:251], v38 offset:9216\n" \
"v_mfma_f32_16x16x32_bf16 a[72:75], a[144:147], a[168:171], a[72:75]\n" \
"v_mfma_f32_16x16x32_bf16 a[72:75], a[208:211], a[232:235], a[72:75]\n" \
"v_mfma_f32_16x16x32_bf16 a[76:79], a[144:147], a[172:175], a[76:79]\n" \
"v_mfma_f32_16x16x32_bf16 a[76:79], a[208:211], a[236:239], a[76:79]\n" \
"ds_read_b64_tr_b16 a[188:189], v39 offset:0\n" \
"v_mfma_f32_16x16x32_bf16 a[92:95], a[148:151], a[172:175], a[92:95]\n" \
"v_mfma_f32_16x16x32_bf16 a[92:95], a[212:215], a[236:239], a[92:95]\n" \
"v_mfma_f32_16x16x32_bf16 a[88:91], a[148:151], a[168:171], a[88:91]\n" \
"ds_read_b64_tr_b16 a[190:191], v39 offset:1024\n" \
"v_mfma_f32_16x16x32_bf16 a[88:91], a[212:215], a[232:235], a[88:91]\n" \
"v_mfma_f32_16x16x32_bf16 a[84:87], a[148:151], a[164:167], a[84:87]\n" \
"v_mfma_f32_16x16x32_bf16 a[84:87], a[212:215], a[228:231], a[84:87]\n" \
"ds_read_b64_tr_b16 a[252:253], v39 offset:8192\n" \
"v_mfma_f32_16x16x32_bf16 a[80:83], a[148:151], a[160:163], a[80:83]\n" \
"v_mfma_f32_16x16x32_bf16 a[80:83], a[212:215], a[224:227], a[80:83]\n" \
"v_mfma_f32_16x16x32_bf16 a[96:99], a[152:155], a[160:163], a[96:99]\n" \
"ds_read_b64_tr_b16 a[254:255], v39 offset:9216\n" \
"v_mfma_f32_16x16x32_bf16 a[96:99], a[216:219], a[224:227], a[96:99]\n" \
"v_mfma_f32_16x16x32_bf16 a[100:103], a[152:155], a[164:167], a[100:103]\n" \
"v_mfma_f32_16x16x32_bf16 a[100:103], a[216:219], a[228:231], a[100:103]\n" \
"v_mfma_f32_16x16x32_bf16 a[104:107], a[152:155], a[168:171], a[104:107]\n" \
"v_mfma_f32_16x16x32_bf16 a[104:107], a[216:219], a[232:235], a[104:107]\n" \
"v_mfma_f32_16x16x32_bf16 a[108:111], a[152:155], a[172:175], a[108:111]\n" \
"v_mfma_f32_16x16x32_bf16 a[108:111], a[216:219], a[236:239], a[108:111]\n" \
"s_waitcnt lgkmcnt(15)\n" \
"v_mfma_f32_16x16x32_bf16 a[124:127], a[156:159], a[172:175], a[124:127]\n" \
"v_mfma_f32_16x16x32_bf16 a[124:127], a[220:223], a[236:239], a[124:127]\n" \
"v_mfma_f32_16x16x32_bf16 a[120:123], a[156:159], a[168:171], a[120:123]\n" \
"v_mfma_f32_16x16x32_bf16 a[120:123], a[220:223], a[232:235], a[120:123]\n" \
"v_mfma_f32_16x16x32_bf16 a[116:119], a[156:159], a[164:167], a[116:119]\n" \
"v_mfma_f32_16x16x32_bf16 a[116:119], a[220:223], a[228:231], a[116:119]\n" \
"v_mfma_f32_16x16x32_bf16 a[112:115], a[156:159], a[160:163], a[112:115]\n" \
"v_mfma_f32_16x16x32_bf16 a[112:115], a[220:223], a[224:227], a[112:115]\n" \
"s_waitcnt vmcnt(0) lgkmcnt(0)\n" \
"s_barrier\n" \
"v_cmp_ne_u32_e32 vcc, s62, v44\n" \
"s_nop 4\n" \
"s_cbranch_vccnz L_poll61_%=\n" \
"L_pollr61_%=:\n" \
"s_mov_b32 s68, s12\n" \
"s_and_b32 s69, s13, 0xffff\n" \
"s_mov_b32 s70, 0x480000\n" \
"s_mov_b32 s71, 0x20000\n" \
"v_mfma_f32_16x16x32_bf16 v[52:55], a[128:131], a[176:179], v[52:55]\n" \
"s_add_u32 m0, s40, 0x0\n" \
"s_add_u32 s85, s39, 0x0\n" \
"s_nop 0\n" \
"v_mfma_f32_16x16x32_bf16 v[52:55], a[192:195], a[240:243], v[52:55]\n" \
"buffer_load_dwordx4 v20, s[80:83], s85 offen sc1 lds\n" \
"v_mfma_f32_16x16x32_bf16 v[56:59], a[128:131], a[180:183], v[56:59]\n" \
"ds_read_b64_tr_b16 a[160:161], v32 offset:32768\n" \
"v_mfma_f32_16x16x32_bf16 v[56:59], a[192:195], a[244:247], v[56:59]\n" \
"s_add_u32 m0, s40, 0x1000\n" \
"s_add_u32 s85, s39, 0x1000\n" \
"v_mfma_f32_16x16x32_bf16 v[60:63], a[128:131], a[184:187], v[60:63]\n" \
"buffer_load_dwordx4 v20, s[80:83], s85 offen sc1 lds\n" \
"v_mfma_f32_16x16x32_bf16 v[60:63], a[192:195], a[248:251], v[60:63]\n" \
"ds_read_b64_tr_b16 a[162:163], v32 offset:33792\n" \
"v_mfma_f32_16x16x32_bf16 v[64:67], a[128:131], a[188:191], v[64:67]\n" \
"ds_read_b64_tr_b16 a[224:225], v32 offset:40960\n" \
"v_mfma_f32_16x16x32_bf16 v[64:67], a[192:195], a[252:255], v[64:67]\n" \
"ds_read_b128 a[128:131], v28 offset:49152\n" \
"v_mfma_f32_16x16x32_bf16 v[80:83], a[132:135], a[188:191], v[80:83]\n" \
"ds_read_b128 a[192:195], v29 offset:49152\n" \
"v_mfma_f32_16x16x32_bf16 v[80:83], a[196:199], a[252:255], v[80:83]\n" \
"s_add_u32 m0, s40, 0x3000\n" \
"s_movk_i32 s84, 0x0\n" \
"v_mfma_f32_16x16x32_bf16 v[76:79], a[132:135], a[184:187], v[76:79]\n" \
"buffer_load_dwordx4 v21, s[76:79], s84 offen offset:0 lds\n" \
"v_mfma_f32_16x16x32_bf16 v[76:79], a[196:199], a[248:251], v[76:79]\n" \
"s_add_u32 m0, s30, 0x0\n" \
"s_movk_i32 s84, 0x0\n" \
"s_nop 0\n" \
"v_mfma_f32_16x16x32_bf16 v[72:75], a[132:135], a[180:183], v[72:75]\n" \
"buffer_load_dwordx4 v24, s[76:79], s84 offen offset:0 lds\n" \
"v_mfma_f32_16x16x32_bf16 v[72:75], a[196:199], a[244:247], v[72:75]\n" \
"ds_read_b64_tr_b16 a[226:227], v32 offset:41984\n" \
"v_mfma_f32_16x16x32_bf16 v[68:71], a[132:135], a[176:179], v[68:71]\n" \
"ds_read_b64_tr_b16 a[164:165], v33 offset:32768\n" \
"v_mfma_f32_16x16x32_bf16 v[68:71], a[196:199], a[240:243], v[68:71]\n" \
"ds_read_b128 a[132:135], v28 offset:51200\n" \
"v_mfma_f32_16x16x32_bf16 v[84:87], a[136:139], a[176:179], v[84:87]\n" \
"ds_read_b128 a[196:199], v29 offset:51200\n" \
"v_mfma_f32_16x16x32_bf16 v[84:87], a[200:203], a[240:243], v[84:87]\n" \
"s_movk_i32 s84, 0x2000\n" \
"v_mfma_f32_16x16x32_bf16 v[88:91], a[136:139], a[180:183], v[88:91]\n" \
"buffer_load_dwordx4 v25, s[76:79], s84 offen offset:1024 lds\n" \
"v_mfma_f32_16x16x32_bf16 v[88:91], a[200:203], a[244:247], v[88:91]\n" \
"s_movk_i32 s84, 0x4000\n" \
"v_mfma_f32_16x16x32_bf16 v[92:95], a[136:139], a[184:187], v[92:95]\n" \
"buffer_load_dwordx4 v24, s[76:79], s84 offen offset:2048 lds\n" \
"v_mfma_f32_16x16x32_bf16 v[92:95], a[200:203], a[248:251], v[92:95]\n" \
"ds_read_b64_tr_b16 a[166:167], v33 offset:33792\n" \
"v_mfma_f32_16x16x32_bf16 v[96:99], a[136:139], a[188:191], v[96:99]\n" \
"ds_read_b64_tr_b16 a[228:229], v33 offset:40960\n" \
"v_mfma_f32_16x16x32_bf16 v[96:99], a[200:203], a[252:255], v[96:99]\n" \
"ds_read_b128 a[136:139], v28 offset:53248\n" \
"v_mfma_f32_16x16x32_bf16 v[112:115], a[140:143], a[188:191], v[112:115]\n" \
"ds_read_b128 a[200:203], v29 offset:53248\n" \
"v_mfma_f32_16x16x32_bf16 v[112:115], a[204:207], a[252:255], v[112:115]\n" \
"s_movk_i32 s84, 0x6000\n" \
"v_mfma_f32_16x16x32_bf16 v[108:111], a[140:143], a[184:187], v[108:111]\n" \
"buffer_load_dwordx4 v25, s[76:79], s84 offen offset:3072 lds\n" \
"v_mfma_f32_16x16x32_bf16 v[108:111], a[204:207], a[248:251], v[108:111]\n" \
"s_add_u32 m0, s31, 0x0\n" \
"s_movk_i32 s84, 0x0\n" \
"v_mfma_f32_16x16x32_bf16 v[104:107], a[140:143], a[180:183], v[104:107]\n" \
"buffer_load_dwordx4 v26, s[68:71], s84 offen offset:0 lds\n" \
"v_mfma_f32_16x16x32_bf16 v[104:107], a[204:207], a[244:247], v[104:107]\n" \
"s_movk_i32 s84, 0x7c00\n" \
".fill (L_pb + 57056 - .) / 4, 4, 0xBF800000\n" \
"L_pp14:\n" \
"s_cbranch_execz L_pret\n" \
"v_mfma_f32_16x16x32_bf16 v[100:103], a[140:143], a[176:179], v[100:103]\n" \
"buffer_load_dwordx4 v26, s[68:71], s84 offen offset:1024 lds\n" \
"v_mfma_f32_16x16x32_bf16 v[100:103], a[204:207], a[240:243], v[100:103]\n" \
"ds_read_b128 a[140:143], v28 offset:55296\n" \
"v_mfma_f32_16x16x32_bf16 v[116:119], a[144:147], a[176:179], v[116:119]\n" \
"ds_read_b128 a[204:207], v29 offset:55296\n" \
"v_mfma_f32_16x16x32_bf16 v[116:119], a[208:211], a[240:243], v[116:119]\n" \
"s_addk_i32 s84, 0x7c00\n" \
"v_mfma_f32_16x16x32_bf16 v[120:123], a[144:147], a[180:183], v[120:123]\n" \
"buffer_load_dwordx4 v27, s[68:71], s84 offen offset:2048 lds\n" \
"v_mfma_f32_16x16x32_bf16 v[120:123], a[208:211], a[244:247], v[120:123]\n" \
"ds_read_b64_tr_b16 a[230:231], v33 offset:41984\n" \
"v_mfma_f32_16x16x32_bf16 v[124:127], a[144:147], a[184:187], v[124:127]\n" \
"s_addk_i32 s84, 0x7c00\n" \
"s_nop 0\n" \
"v_mfma_f32_16x16x32_bf16 v[124:127], a[208:211], a[248:251], v[124:127]\n" \
"buffer_load_dwordx4 v27, s[68:71], s84 offen offset:3072 lds\n" \
"v_mfma_f32_16x16x32_bf16 v[128:131], a[144:147], a[188:191], v[128:131]\n" \
"ds_read_b64_tr_b16 a[168:169], v34 offset:32768\n" \
"v_mfma_f32_16x16x32_bf16 v[128:131], a[208:211], a[252:255], v[128:131]\n" \
"ds_read_b128 a[144:147], v28 offset:32768\n" \
"v_mfma_f32_16x16x32_bf16 v[144:147], a[148:151], a[188:191], v[144:147]\n" \
"ds_read_b128 a[208:211], v29 offset:32768\n" \
"v_mfma_f32_16x16x32_bf16 v[144:147], a[212:215], a[252:255], v[144:147]\n" \
"s_add_u32 m0, s30, 0x1000\n" \
"s_mov_b32 s84, 0x9000\n" \
"v_mfma_f32_16x16x32_bf16 v[140:143], a[148:151], a[184:187], v[140:143]\n" \
"buffer_load_dwordx4 v24, s[76:79], s84 offen offset:0 lds\n" \
"v_mfma_f32_16x16x32_bf16 v[140:143], a[212:215], a[248:251], v[140:143]\n" \
"ds_read_b64_tr_b16 a[170:171], v34 offset:33792\n" \
"v_mfma_f32_16x16x32_bf16 v[136:139], a[148:151], a[180:183], v[136:139]\n" \
"s_addk_i32 s84, 0x2000\n" \
"s_nop 0\n" \
"v_mfma_f32_16x16x32_bf16 v[136:139], a[212:215], a[244:247], v[136:139]\n" \
"buffer_load_dwordx4 v25, s[76:79], s84 offen offset:1024 lds\n" \
"v_mfma_f32_16x16x32_bf16 v[132:135], a[148:151], a[176:179], v[132:135]\n" \
"ds_read_b64_tr_b16 a[232:233], v34 offset:40960\n" \
"v_mfma_f32_16x16x32_bf16 v[132:135], a[212:215], a[240:243], v[132:135]\n" \
"ds_read_b128 a[148:151], v28 offset:34816\n" \
"v_mfma_f32_16x16x32_bf16 v[148:151], a[152:155], a[176:179], v[148:151]\n" \
"ds_read_b128 a[212:215], v29 offset:34816\n" \
"v_mfma_f32_16x16x32_bf16 v[148:151], a[216:219], a[240:243], v[148:151]\n" \
"ds_read_b64_tr_b16 a[234:235], v34 offset:41984\n" \
"v_mfma_f32_16x16x32_bf16 v[152:155], a[152:155], a[180:183], v[152:155]\n" \
"ds_read_b64_tr_b16 a[172:173], v35 offset:32768\n" \
"v_mfma_f32_16x16x32_bf16 v[152:155], a[216:219], a[244:247], v[152:155]\n" \
"ds_read_b64_tr_b16 a[174:175], v35 offset:33792\n" \
"v_mfma_f32_16x16x32_bf16 v[156:159], a[152:155], a[184:187], v[156:159]\n" \
"ds_read_b64_tr_b16 a[236:237], v35 offset:40960\n" \
"v_mfma_f32_16x16x32_bf16 v[156:159], a[216:219], a[248:251], v[156:159]\n" \
"ds_read_b64_tr_b16 a[238:239], v35 offset:41984\n" \
"v_mfma_f32_16x16x32_bf16 v[160:163], a[152:155], a[188:191], v[160:163]\n" \
"v_mfma_f32_16x16x32_bf16 v[160:163], a[216:219], a[252:255], v[160:163]\n" \
"ds_read_b128 a[152:155], v28 offset:36864\n" \
"v_mfma_f32_16x16x32_bf16 v[176:179], a[156:159], a[188:191], v[176:179]\n" \
"ds_read_b128 a[216:219], v29 offset:36864\n" \
"v_mfma_f32_16x16x32_bf16 v[176:179], a[220:223], a[252:255], v[176:179]\n" \
"v_mfma_f32_16x16x32_bf16 v[172:175], a[156:159], a[184:187], v[172:175]\n" \
"v_mfma_f32_16x16x32_bf16 v[172:175], a[220:223], a[248:251], v[172:175]\n" \
"v_mfma_f32_16x16x32_bf16 v[168:171], a[156:159], a[180:183], v[168:171]\n" \
"v_mfma_f32_16x16x32_bf16 v[168:171], a[220:223], a[244:247], v[168:171]\n" \
"v_mfma_f32_16x16x32_bf16 v[164:167], a[156:159], a[176:179], v[164:167]\n" \
"v_mfma_f32_16x16x32_bf16 v[164:167], a[220:223], a[240:243], v[164:167]\n" \
"ds_read_b128 a[156:159], v28 offset:38912\n" \
"ds_read_b128 a[220:223], v29 offset:38912\n" \
"s_waitcnt lgkmcnt(15)\n" \
"v_mfma_f32_16x16x32_bf16 a[0:3], a[128:131], a[160:163], a[0:3]\n" \
"s_addk_i32 s84, 0x2000\n" \
"v_mfma_f32_16x16x32_bf16 a[0:3], a[192:195], a[224:227], a[0:3]\n" \
"buffer_load_dwordx4 v24, s[76:79], s84 offen offset:2048 lds\n" \
"v_mfma_f32_16x16x32_bf16 a[4:7], a[128:131], a[164:167], a[4:7]\n" \
"ds_read_b64_tr_b16 a[176:177], v36 offset:32768\n" \
"v_mfma_f32_16x16x32_bf16 a[4:7], a[192:195], a[228:231], a[4:7]\n" \
"s_addk_i32 s84, 0x2000\n" \
"s_waitcnt lgkmcnt(13)\n" \
"v_mfma_f32_16x16x32_bf16 a[8:11], a[128:131], a[168:171], a[8:11]\n" \
"buffer_load_dwordx4 v25, s[76:79], s84 offen offset:3072 lds\n" \
"s_waitcnt lgkmcnt(9)\n" \
"v_mfma_f32_16x16x32_bf16 a[8:11], a[192:195], a[232:235], a[8:11]\n" \
"ds_read_b64_tr_b16 a[178:179], v36 offset:33792\n" \
"s_waitcnt lgkmcnt(8)\n" \
"v_mfma_f32_16x16x32_bf16 a[12:15], a[128:131], a[172:175], a[12:15]\n" \
"ds_read_b64_tr_b16 a[240:241], v36 offset:40960\n" \
"s_waitcnt lgkmcnt(7)\n" \
"v_mfma_f32_16x16x32_bf16 a[12:15], a[192:195], a[236:239], a[12:15]\n" \
"v_mfma_f32_16x16x32_bf16 a[28:31], a[132:135], a[172:175], a[28:31]\n" \
"v_mfma_f32_16x16x32_bf16 a[28:31], a[196:199], a[236:239], a[28:31]\n" \
"ds_read_b64_tr_b16 a[242:243], v36 offset:41984\n" \
"v_mfma_f32_16x16x32_bf16 a[24:27], a[132:135], a[168:171], a[24:27]\n" \
"v_mfma_f32_16x16x32_bf16 a[24:27], a[196:199], a[232:235], a[24:27]\n" \
"v_mfma_f32_16x16x32_bf16 a[20:23], a[132:135], a[164:167], a[20:23]\n" \
"s_nop 1\n" \
"v_accvgpr_read_b32 v184, a12\n" \
"v_accvgpr_read_b32 v185, a13\n" \
"v_mfma_f32_16x16x32_bf16 a[20:23], a[196:199], a[228:231], a[20:23]\n" \
"ds_read_b64_tr_b16 a[180:181], v37 offset:32768\n" \
"v_accvgpr_read_b32 v186, a14\n" \
"v_mfma_f32_16x16x32_bf16 a[16:19], a[132:135], a[160:163], a[16:19]\n" \
"v_accvgpr_read_b32 v187, a15\n" \
"v_accvgpr_read_b32 v180, a28\n" \
"v_mfma_f32_16x16x32_bf16 a[16:19], a[196:199], a[224:227], a[16:19]\n" \
"v_accvgpr_read_b32 v181, a29\n" \
"v_accvgpr_read_b32 v182, a30\n" \
"v_mfma_f32_16x16x32_bf16 a[32:35], a[136:139], a[160:163], a[32:35]\n" \
"ds_read_b64_tr_b16 a[182:183], v37 offset:33792\n" \
"v_accvgpr_read_b32 v183, a31\n" \
"v_mfma_f32_16x16x32_bf16 a[32:35], a[200:203], a[224:227], a[32:35]\n" \
"v_cvt_pk_bf16_f32 v184, v184, v185\n" \
"v_cvt_pk_bf16_f32 v185, v186, v187\n" \
"v_mfma_f32_16x16x32_bf16 a[36:39], a[136:139], a[164:167], a[36:39]\n" \
"v_lshlrev_b32_e32 v45, 3, v20\n" \
"v_add_u32_e32 v45, s31, v45\n" \
"v_mfma_f32_16x16x32_bf16 a[36:39], a[200:203], a[228:231], a[36:39]\n" \
"s_getpc_b64 s[86:87]\n" \
"s_sub_u32 s86, s86, 0x10800\n" \
"s_subb_u32 s87, s87, 0\n" \
"s_mov_b32 m0, s32\n" \
"s_nop 0\n" \
"v_mfma_f32_16x16x32_bf16 a[40:43], a[136:139], a[168:171], a[40:43]\n" \
"global_load_lds_dword v45, s[86:87]\n" \
"v_mfma_f32_16x16x32_bf16 a[40:43], a[200:203], a[232:235], a[40:43]\n" \
"ds_read_b64_tr_b16 a[244:245], v37 offset:40960\n" \
"v_cvt_pk_bf16_f32 v186, v180, v181\n" \
"v_mfma_f32_16x16x32_bf16 a[44:47], a[136:139], a[172:175], a[44:47]\n" \
"ds_read_b64_tr_b16 a[246:247], v37 offset:41984\n" \
"v_cvt_pk_bf16_f32 v187, v182, v183\n" \
"v_mfma_f32_16x16x32_bf16 a[44:47], a[200:203], a[236:239], a[44:47]\n" \
"v_accvgpr_read_b32 v188, a8\n" \
"v_accvgpr_read_b32 v189, a9\n" \
"v_mfma_f32_16x16x32_bf16 a[60:63], a[140:143], a[172:175], a[60:63]\n" \
"v_accvgpr_read_b32 v190, a10\n" \
"v_accvgpr_read_b32 v191, a11\n" \
"v_mfma_f32_16x16x32_bf16 a[60:63], a[204:207], a[236:239], a[60:63]\n" \
"v_accvgpr_read_b32 v180, a24\n" \
"v_accvgpr_read_b32 v181, a25\n" \
"v_mfma_f32_16x16x32_bf16 a[56:59], a[140:143], a[168:171], a[56:59]\n" \
"ds_read_b64_tr_b16 a[184:185], v38 offset:32768\n" \
"v_accvgpr_read_b32 v182, a26\n" \
"v_mfma_f32_16x16x32_bf16 a[56:59], a[204:207], a[232:235], a[56:59]\n" \
"v_accvgpr_read_b32 v183, a27\n" \
"v_cvt_pk_bf16_f32 v188, v188, v189\n" \
"v_mfma_f32_16x16x32_bf16 a[52:55], a[140:143], a[164:167], a[52:55]\n" \
"v_cvt_pk_bf16_f32 v189, v190, v191\n" \
"v_cvt_pk_bf16_f32 v190, v180, v181\n" \
"v_mfma_f32_16x16x32_bf16 a[52:55], a[204:207], a[228:231], a[52:55]\n" \
"ds_read_b64_tr_b16 a[186:187], v38 offset:33792\n" \
"v_cvt_pk_bf16_f32 v191, v182, v183\n" \
"v_mfma_f32_16x16x32_bf16 a[48:51], a[140:143], a[160:163], a[48:51]\n" \
"v_accvgpr_read_b32 v192, a4\n" \
"v_accvgpr_read_b32 v193, a5\n" \
"v_mfma_f32_16x16x32_bf16 a[48:51], a[204:207], a[224:227], a[48:51]\n" \
"v_accvgpr_read_b32 v194, a6\n" \
"v_accvgpr_read_b32 v195, a7\n" \
"v_mfma_f32_16x16x32_bf16 a[64:67], a[144:147], a[160:163], a[64:67]\n" \
"ds_read_b64_tr_b16 a[248:249], v38 offset:40960\n" \
"v_accvgpr_read_b32 v180, a20\n" \
"v_mfma_f32_16x16x32_bf16 a[64:67], a[208:211], a[224:227], a[64:67]\n" \
"v_accvgpr_read_b32 v181, a21\n" \
"v_accvgpr_read_b32 v182, a22\n" \
"v_mfma_f32_16x16x32_bf16 a[68:71], a[144:147], a[164:167], a[68:71]\n" \
"v_accvgpr_read_b32 v183, a23\n" \
"v_cvt_pk_bf16_f32 v192, v192, v193\n" \
"v_mfma_f32_16x16x32_bf16 a[68:71], a[208:211], a[228:231], a[68:71]\n" \
"ds_read_b64_tr_b16 a[250:251], v38 offset:41984\n" \
"v_cvt_pk_bf16_f32 v193, v194, v195\n" \
"v_mfma_f32_16x16x32_bf16 a[72:75], a[144:147], a[168:171], a[72:75]\n" \
"v_cvt_pk_bf16_f32 v194, v180, v181\n" \
"v_cvt_pk_bf16_f32 v195, v182, v183\n" \
"v_mfma_f32_16x16x32_bf16 a[72:75], a[208:211], a[232:235], a[72:75]\n" \
"v_accvgpr_read_b32 v196, a0\n" \
"v_accvgpr_read_b32 v197, a1\n" \
"v_mfma_f32_16x16x32_bf16 a[76:79], a[144:147], a[172:175], a[76:79]\n" \
"v_accvgpr_read_b32 v198, a2\n" \
"v_accvgpr_read_b32 v199, a3\n" \
"v_mfma_f32_16x16x32_bf16 a[76:79], a[208:211], a[236:239], a[76:79]\n" \
"ds_read_b64_tr_b16 a[188:189], v39 offset:32768\n" \
"v_accvgpr_read_b32 v180, a16\n" \
"v_mfma_f32_16x16x32_bf16 a[92:95], a[148:151], a[172:175], a[92:95]\n" \
"s_add_u32 s89, s41, 0x0\n" \
"s_nop 0\n" \
"buffer_store_dwordx4 v[184:187], v20, s[80:83], s89 offen offset:3072 sc1\n" \
"v_mfma_f32_16x16x32_bf16 a[92:95], a[212:215], a[236:239], a[92:95]\n" \
"v_accvgpr_read_b32 v181, a17\n" \
"v_accvgpr_read_b32 v182, a18\n" \
"v_mfma_f32_16x16x32_bf16 a[88:91], a[148:151], a[168:171], a[88:91]\n" \
"ds_read_b64_tr_b16 a[190:191], v39 offset:33792\n" \
"v_accvgpr_read_b32 v183, a19\n" \
"v_mfma_f32_16x16x32_bf16 a[88:91], a[212:215], a[232:235], a[88:91]\n" \
"v_cvt_pk_bf16_f32 v196, v196, v197\n" \
"v_cvt_pk_bf16_f32 v197, v198, v199\n" \
"v_mfma_f32_16x16x32_bf16 a[84:87], a[148:151], a[164:167], a[84:87]\n" \
"v_cvt_pk_bf16_f32 v198, v180, v181\n" \
"v_cvt_pk_bf16_f32 v199, v182, v183\n" \
"v_mfma_f32_16x16x32_bf16 a[84:87], a[212:215], a[228:231], a[84:87]\n" \
"ds_read_b64_tr_b16 a[252:253], v39 offset:40960\n" \
"v_accvgpr_read_b32 v200, a44\n" \
"v_mfma_f32_16x16x32_bf16 a[80:83], a[148:151], a[160:163], a[80:83]\n" \
"s_add_u32 s88, s41, 0x0\n" \
"buffer_store_dwordx4 v[188:191], v20, s[80:83], s88 offen offset:2048 sc1\n" \
"v_mfma_f32_16x16x32_bf16 a[80:83], a[212:215], a[224:227], a[80:83]\n" \
"v_accvgpr_read_b32 v201, a45\n" \
"v_accvgpr_read_b32 v202, a46\n" \
"s_waitcnt lgkmcnt(15)\n" \
"v_mfma_f32_16x16x32_bf16 a[96:99], a[152:155], a[160:163], a[96:99]\n" \
"ds_read_b64_tr_b16 a[254:255], v39 offset:41984\n" \
"v_accvgpr_read_b32 v203, a47\n" \
"v_mfma_f32_16x16x32_bf16 a[96:99], a[216:219], a[224:227], a[96:99]\n" \
"v_accvgpr_read_b32 v180, a60\n" \
"v_accvgpr_read_b32 v181, a61\n" \
"v_mfma_f32_16x16x32_bf16 a[100:103], a[152:155], a[164:167], a[100:103]\n" \
"v_accvgpr_read_b32 v182, a62\n" \
"v_accvgpr_read_b32 v183, a63\n" \
"v_mfma_f32_16x16x32_bf16 a[100:103], a[216:219], a[228:231], a[100:103]\n" \
"v_cvt_pk_bf16_f32 v200, v200, v201\n" \
"v_cvt_pk_bf16_f32 v201, v202, v203\n" \
"v_mfma_f32_16x16x32_bf16 a[104:107], a[152:155], a[168:171], a[104:107]\n" \
"s_add_u32 s89, s41, 0x0\n" \
"s_nop 0\n" \
"buffer_store_dwordx4 v[192:195], v20, s[80:83], s89 offen offset:1024 sc1\n" \
"v_mfma_f32_16x16x32_bf16 a[104:107], a[216:219], a[232:235], a[104:107]\n" \
"v_cvt_pk_bf16_f32 v202, v180, v181\n" \
"v_cvt_pk_bf16_f32 v203, v182, v183\n" \
"v_mfma_f32_16x16x32_bf16 a[108:111], a[152:155], a[172:175], a[108:111]\n" \
"v_accvgpr_read_b32 v204, a40\n" \
"v_accvgpr_read_b32 v205, a41\n" \
"v_mfma_f32_16x16x32_bf16 a[108:111], a[216:219], a[236:239], a[108:111]\n" \
"v_accvgpr_read_b32 v206, a42\n" \
"v_accvgpr_read_b32 v207, a43\n" \
"v_mfma_f32_16x16x32_bf16 a[124:127], a[156:159], a[172:175], a[124:127]\n" \
"v_accvgpr_read_b32 v180, a56\n" \
"v_accvgpr_read_b32 v181, a57\n" \
"v_mfma_f32_16x16x32_bf16 a[124:127], a[220:223], a[236:239], a[124:127]\n" \
"v_accvgpr_read_b32 v182, a58\n" \
"v_accvgpr_read_b32 v183, a59\n" \
"v_mfma_f32_16x16x32_bf16 a[120:123], a[156:159], a[168:171], a[120:123]\n" \
"s_add_u32 s88, s41, 0x0\n" \
"s_nop 0\n" \
"buffer_store_dwordx4 v[196:199], v20, s[80:83], s88 offen offset:0 sc1\n" \
"v_mfma_f32_16x16x32_bf16 a[120:123], a[220:223], a[232:235], a[120:123]\n" \
"v_cvt_pk_bf16_f32 v204, v204, v205\n" \
"v_cvt_pk_bf16_f32 v205, v206, v207\n" \
"v_mfma_f32_16x16x32_bf16 a[116:119], a[156:159], a[164:167], a[116:119]\n" \
"v_cvt_pk_bf16_f32 v206, v180, v181\n" \
"v_cvt_pk_bf16_f32 v207, v182, v183\n" \
"v_mfma_f32_16x16x32_bf16 a[116:119], a[220:223], a[228:231], a[116:119]\n" \
"v_accvgpr_read_b32 v208, a36\n" \
"v_accvgpr_read_b32 v209, a37\n" \
"v_mfma_f32_16x16x32_bf16 a[112:115], a[156:159], a[160:163], a[112:115]\n" \
"v_accvgpr_read_b32 v210, a38\n" \
"v_accvgpr_read_b32 v211, a39\n" \
"v_mfma_f32_16x16x32_bf16 a[112:115], a[220:223], a[224:227], a[112:115]\n" \
"v_accvgpr_read_b32 v180, a52\n" \
"v_accvgpr_read_b32 v181, a53\n" \
"s_waitcnt vmcnt(5) lgkmcnt(0)\n" \
"s_barrier\n" \
"v_xor_b32_e32 v36, 0x4000, v36\n" \
"v_xor_b32_e64 v32, 0x0, v48\n" \
"v_xor_b32_e32 v37, 0x4000, v37\n" \
"v_xor_b32_e64 v33, 0x20, v48\n" \
"v_xor_b32_e32 v38, 0x4000, v38\n" \
"v_xor_b32_e64 v34, 0x40, v48\n" \
"v_xor_b32_e32 v39, 0x4000, v39\n" \
"v_xor_b32_e32 v35, 0x60, v48\n" \
"v_add_u32_e32 v28, s43, v28\n" \
"v_add_u32_e32 v29, s43, v29\n" \
"v_mfma_f32_16x16x32_bf16 v[116:119], a[144:147], a[176:179], v[116:119]\n" \
"s_add_u32 m0, s40, 0x8000\n" \
"s_add_u32 s85, s39, 0x2000\n" \
"v_mfma_f32_16x16x32_bf16 v[116:119], a[208:211], a[240:243], v[116:119]\n" \
"buffer_load_dwordx4 v20, s[80:83], s85 offen sc1 lds\n" \
"v_mfma_f32_16x16x32_bf16 v[120:123], a[144:147], a[180:183], v[120:123]\n" \
"ds_read_b64_tr_b16 a[160:161], v32 offset:0\n" \
"v_accvgpr_read_b32 v182, a54\n" \
"v_mfma_f32_16x16x32_bf16 v[120:123], a[208:211], a[244:247], v[120:123]\n" \
"s_add_u32 m0, s40, 0x9000\n" \
"s_add_u32 s85, s39, 0x3000\n" \
"v_mfma_f32_16x16x32_bf16 v[124:127], a[144:147], a[184:187], v[124:127]\n" \
"buffer_load_dwordx4 v20, s[80:83], s85 offen sc1 lds\n" \
"v_mfma_f32_16x16x32_bf16 v[124:127], a[208:211], a[248:251], v[124:127]\n" \
"ds_read_b64_tr_b16 a[162:163], v32 offset:512\n" \
"v_accvgpr_read_b32 v183, a55\n" \
"v_mfma_f32_16x16x32_bf16 v[128:131], a[144:147], a[188:191], v[128:131]\n" \
"ds_read_b64_tr_b16 a[224:225], v32 offset:4096\n" \
"v_cvt_pk_bf16_f32 v208, v208, v209\n" \
"v_mfma_f32_16x16x32_bf16 v[128:131], a[208:211], a[252:255], v[128:131]\n" \
"ds_read_b128 a[144:147], v28 offset:8192\n" \
"v_cvt_pk_bf16_f32 v209, v210, v211\n" \
"v_mfma_f32_16x16x32_bf16 v[144:147], a[148:151], a[188:191], v[144:147]\n" \
"ds_read_b128 a[208:211], v29 offset:8192\n" \
"v_cvt_pk_bf16_f32 v210, v180, v181\n" \
"v_mfma_f32_16x16x32_bf16 v[144:147], a[212:215], a[252:255], v[144:147]\n" \
"s_add_u32 m0, s40, 0xb000\n" \
"s_movk_i32 s84, 0x80\n" \
"v_mfma_f32_16x16x32_bf16 v[140:143], a[148:151], a[184:187], v[140:143]\n" \
"buffer_load_dwordx4 v21, s[76:79], s84 offen offset:0 lds\n" \
"v_mfma_f32_16x16x32_bf16 v[140:143], a[212:215], a[248:251], v[140:143]\n" \
"s_add_u32 m0, s30, 0x8000\n" \
"s_movk_i32 s84, 0x80\n" \
"v_mfma_f32_16x16x32_bf16 v[136:139], a[148:151], a[180:183], v[136:139]\n" \
"buffer_load_dwordx4 v24, s[76:79], s84 offen offset:0 lds\n" \
"v_mfma_f32_16x16x32_bf16 v[136:139], a[212:215], a[244:247], v[136:139]\n" \
"ds_read_b64_tr_b16 a[226:227], v32 offset:4608\n" \
"v_cvt_pk_bf16_f32 v211, v182, v183\n" \
"v_mfma_f32_16x16x32_bf16 v[132:135], a[148:151], a[176:179], v[132:135]\n" \
"ds_read_b64_tr_b16 a[164:165], v33 offset:0\n" \
"v_accvgpr_read_b32 v212, a32\n" \
"v_mfma_f32_16x16x32_bf16 v[132:135], a[212:215], a[240:243], v[132:135]\n" \
"ds_read_b128 a[148:151], v28 offset:10240\n" \
"v_accvgpr_read_b32 v213, a33\n" \
"v_mfma_f32_16x16x32_bf16 v[148:151], a[152:155], a[176:179], v[148:151]\n" \
"ds_read_b128 a[212:215], v29 offset:10240\n" \
"v_accvgpr_read_b32 v214, a34\n" \
"v_mfma_f32_16x16x32_bf16 v[148:151], a[216:219], a[240:243], v[148:151]\n" \
"s_movk_i32 s84, 0x2080\n" \
"v_mfma_f32_16x16x32_bf16 v[152:155], a[152:155], a[180:183], v[152:155]\n" \
"buffer_load_dwordx4 v25, s[76:79], s84 offen offset:1024 lds\n" \
"v_mfma_f32_16x16x32_bf16 v[152:155], a[216:219], a[244:247], v[152:155]\n" \
"s_movk_i32 s84, 0x4080\n" \
"v_mfma_f32_16x16x32_bf16 v[156:159], a[152:155], a[184:187], v[156:159]\n" \
"buffer_load_dwordx4 v24, s[76:79], s84 offen offset:2048 lds\n" \
"v_mfma_f32_16x16x32_bf16 v[156:159], a[216:219], a[248:251], v[156:159]\n" \
"ds_read_b64_tr_b16 a[166:167], v33 offset:512\n" \
"v_accvgpr_read_b32 v215, a35\n" \
"v_mfma_f32_16x16x32_bf16 v[160:163], a[152:155], a[188:191], v[160:163]\n" \
"ds_read_b64_tr_b16 a[228:229], v33 offset:4096\n" \
"v_accvgpr_read_b32 v180, a48\n" \
"v_mfma_f32_16x16x32_bf16 v[160:163], a[216:219], a[252:255], v[160:163]\n" \
"ds_read_b128 a[152:155], v28 offset:12288\n" \
"v_accvgpr_read_b32 v181, a49\n" \
"v_mfma_f32_16x16x32_bf16 v[176:179], a[156:159], a[188:191], v[176:179]\n" \
"ds_read_b128 a[216:219], v29 offset:12288\n" \
"v_accvgpr_read_b32 v182, a50\n" \
"v_mfma_f32_16x16x32_bf16 v[176:179], a[220:223], a[252:255], v[176:179]\n" \
"s_movk_i32 s84, 0x6080\n" \
"v_mfma_f32_16x16x32_bf16 v[172:175], a[156:159], a[184:187], v[172:175]\n" \
"buffer_load_dwordx4 v25, s[76:79], s84 offen offset:3072 lds\n" \
"v_mfma_f32_16x16x32_bf16 v[172:175], a[220:223], a[248:251], v[172:175]\n" \
"s_add_u32 m0, s31, 0x8000\n" \
"s_mov_b32 s84, 0x80000\n" \
"v_mfma_f32_16x16x32_bf16 v[168:171], a[156:159], a[180:183], v[168:171]\n" \
"buffer_load_dwordx4 v26, s[68:71], s84 offen offset:0 lds\n" \
"v_mfma_f32_16x16x32_bf16 v[168:171], a[220:223], a[244:247], v[168:171]\n" \
"s_addk_i32 s84, 0x7c00\n" \
"v_mfma_f32_16x16x32_bf16 v[164:167], a[156:159], a[176:179], v[164:167]\n" \
"buffer_load_dwordx4 v26, s[68:71], s84 offen offset:1024 lds\n" \
"v_mfma_f32_16x16x32_bf16 v[164:167], a[220:223], a[240:243], v[164:167]\n" \
"ds_read_b128 a[156:159], v28 offset:14336\n" \
"v_accvgpr_read_b32 v183, a51\n" \
"v_mfma_f32_16x16x32_bf16 v[84:87], a[136:139], a[176:179], v[84:87]\n" \
"ds_read_b128 a[220:223], v29 offset:14336\n" \
"v_cvt_pk_bf16_f32 v212, v212, v213\n" \
"v_mfma_f32_16x16x32_bf16 v[84:87], a[200:203], a[240:243], v[84:87]\n" \
"s_addk_i32 s84, 0x7c00\n" \
"v_mfma_f32_16x16x32_bf16 v[88:91], a[136:139], a[180:183], v[88:91]\n" \
"buffer_load_dwordx4 v27, s[68:71], s84 offen offset:2048 lds\n" \
"v_mfma_f32_16x16x32_bf16 v[88:91], a[200:203], a[244:247], v[88:91]\n" \
"ds_read_b64_tr_b16 a[230:231], v33 offset:4608\n" \
"v_cvt_pk_bf16_f32 v213, v214, v215\n" \
"v_mfma_f32_16x16x32_bf16 v[92:95], a[136:139], a[184:187], v[92:95]\n" \
"s_addk_i32 s84, 0x7c00\n" \
"v_mfma_f32_16x16x32_bf16 v[92:95], a[200:203], a[248:251], v[92:95]\n" \
"buffer_load_dwordx4 v27, s[68:71], s84 offen offset:3072 lds\n" \
"v_mfma_f32_16x16x32_bf16 v[96:99], a[136:139], a[188:191], v[96:99]\n" \
"ds_read_b64_tr_b16 a[172:173], v35 offset:0\n" \
"v_cvt_pk_bf16_f32 v214, v180, v181\n" \
"v_mfma_f32_16x16x32_bf16 v[96:99], a[200:203], a[252:255], v[96:99]\n" \
"ds_read_b128 a[136:139], v28 offset:4096\n" \
"v_cvt_pk_bf16_f32 v215, v182, v183\n" \
"v_mfma_f32_16x16x32_bf16 v[112:115], a[140:143], a[188:191], v[112:115]\n" \
"ds_read_b128 a[200:203], v29 offset:4096\n" \
"v_accvgpr_read_b32 v216, a68\n" \
"v_mfma_f32_16x16x32_bf16 v[112:115], a[204:207], a[252:255], v[112:115]\n" \
"s_add_u32 m0, s30, 0x9000\n" \
"s_mov_b32 s84, 0x9080\n" \
"v_mfma_f32_16x16x32_bf16 v[108:111], a[140:143], a[184:187], v[108:111]\n" \
"buffer_load_dwordx4 v24, s[76:79], s84 offen offset:0 lds\n" \
"v_mfma_f32_16x16x32_bf16 v[108:111], a[204:207], a[248:251], v[108:111]\n" \
"ds_read_b64_tr_b16 a[174:175], v35 offset:512\n" \
"v_accvgpr_read_b32 v217, a69\n" \
"v_mfma_f32_16x16x32_bf16 v[104:107], a[140:143], a[180:183], v[104:107]\n" \
"s_addk_i32 s84, 0x2000\n" \
"s_nop 0\n" \
"v_mfma_f32_16x16x32_bf16 v[104:107], a[204:207], a[244:247], v[104:107]\n" \
"buffer_load_dwordx4 v25, s[76:79], s84 offen offset:1024 lds\n" \
"v_mfma_f32_16x16x32_bf16 v[100:103], a[140:143], a[176:179], v[100:103]\n" \
"ds_read_b64_tr_b16 a[236:237], v35 offset:4096\n" \
"v_accvgpr_read_b32 v218, a70\n" \
"v_mfma_f32_16x16x32_bf16 v[100:103], a[204:207], a[240:243], v[100:103]\n" \
"ds_read_b128 a[140:143], v28 offset:6144\n" \
"v_accvgpr_read_b32 v219, a71\n" \
"v_mfma_f32_16x16x32_bf16 v[52:55], a[128:131], a[176:179], v[52:55]\n" \
"ds_read_b128 a[204:207], v29 offset:6144\n" \
"v_accvgpr_read_b32 v180, a84\n" \
"v_mfma_f32_16x16x32_bf16 v[52:55], a[192:195], a[240:243], v[52:55]\n" \
"ds_read_b64_tr_b16 a[238:239], v35 offset:4608\n" \
"v_accvgpr_read_b32 v181, a85\n" \
"v_mfma_f32_16x16x32_bf16 v[56:59], a[128:131], a[180:183], v[56:59]\n" \
"ds_read_b64_tr_b16 a[168:169], v34 offset:0\n" \
"v_accvgpr_read_b32 v182, a86\n" \
"v_mfma_f32_16x16x32_bf16 v[56:59], a[192:195], a[244:247], v[56:59]\n" \
"ds_read_b64_tr_b16 a[170:171], v34 offset:512\n" \
"v_accvgpr_read_b32 v183, a87\n" \
"v_mfma_f32_16x16x32_bf16 v[60:63], a[128:131], a[184:187], v[60:63]\n" \
"ds_read_b64_tr_b16 a[232:233], v34 offset:4096\n" \
"v_cvt_pk_bf16_f32 v216, v216, v217\n" \
"v_mfma_f32_16x16x32_bf16 v[60:63], a[192:195], a[248:251], v[60:63]\n" \
"ds_read_b64_tr_b16 a[234:235], v34 offset:4608\n" \
"v_cvt_pk_bf16_f32 v217, v218, v219\n" \
"v_mfma_f32_16x16x32_bf16 v[64:67], a[128:131], a[188:191], v[64:67]\n" \
"v_cvt_pk_bf16_f32 v218, v180, v181\n" \
"v_cvt_pk_bf16_f32 v219, v182, v183\n" \
"v_mfma_f32_16x16x32_bf16 v[64:67], a[192:195], a[252:255], v[64:67]\n" \
"ds_read_b128 a[128:131], v28 offset:0\n" \
"v_accvgpr_read_b32 v220, a64\n" \
"v_mfma_f32_16x16x32_bf16 v[80:83], a[132:135], a[188:191], v[80:83]\n" \
"ds_read_b128 a[192:195], v29 offset:0\n" \
"v_accvgpr_read_b32 v221, a65\n" \
"v_mfma_f32_16x16x32_bf16 v[80:83], a[196:199], a[252:255], v[80:83]\n" \
"v_accvgpr_read_b32 v222, a66\n" \
"v_accvgpr_read_b32 v223, a67\n" \
"v_mfma_f32_16x16x32_bf16 v[76:79], a[132:135], a[184:187], v[76:79]\n" \
"v_accvgpr_read_b32 v180, a80\n" \
"v_accvgpr_read_b32 v181, a81\n" \
"v_mfma_f32_16x16x32_bf16 v[76:79], a[196:199], a[248:251], v[76:79]\n" \
"v_accvgpr_read_b32 v182, a82\n" \
"v_accvgpr_read_b32 v183, a83\n" \
"v_mfma_f32_16x16x32_bf16 v[72:75], a[132:135], a[180:183], v[72:75]\n" \
"v_cvt_pk_bf16_f32 v220, v220, v221\n" \
"v_cvt_pk_bf16_f32 v221, v222, v223\n" \
"v_mfma_f32_16x16x32_bf16 v[72:75], a[196:199], a[244:247], v[72:75]\n" \
"v_cvt_pk_bf16_f32 v222, v180, v181\n" \
"v_cvt_pk_bf16_f32 v223, v182, v183\n" \
"v_mfma_f32_16x16x32_bf16 v[68:71], a[132:135], a[176:179], v[68:71]\n" \
"v_cvt_pk_bf16_f32 v224, v128, v129\n" \
"v_cvt_pk_bf16_f32 v225, v130, v131\n" \
"v_mfma_f32_16x16x32_bf16 v[68:71], a[196:199], a[240:243], v[68:71]\n" \
"ds_read_b128 a[132:135], v28 offset:2048\n" \
"ds_read_b128 a[196:199], v29 offset:2048\n" \
"s_waitcnt lgkmcnt(15)\n" \
"v_mfma_f32_16x16x32_bf16 a[0:3], a[160:163], a[144:147], 0\n" \
"s_addk_i32 s84, 0x2000\n" \
"v_cvt_pk_bf16_f32 v226, v144, v145\n" \
"v_mfma_f32_16x16x32_bf16 a[0:3], a[224:227], a[208:211], a[0:3]\n" \
"buffer_load_dwordx4 v24, s[76:79], s84 offen offset:2048 lds\n" \
"v_cvt_pk_bf16_f32 v227, v146, v147\n" \
"v_mfma_f32_16x16x32_bf16 a[4:7], a[164:167], a[144:147], 0\n" \
"ds_read_b64_tr_b16 a[176:177], v36 offset:0\n" \
"v_cvt_pk_bf16_f32 v228, v124, v125\n" \
"v_mfma_f32_16x16x32_bf16 a[4:7], a[228:231], a[208:211], a[4:7]\n" \
"s_addk_i32 s84, 0x2000\n" \
"v_cvt_pk_bf16_f32 v229, v126, v127\n" \
"s_waitcnt lgkmcnt(13)\n" \
"v_mfma_f32_16x16x32_bf16 a[12:15], a[172:175], a[144:147], 0\n" \
"buffer_load_dwordx4 v25, s[76:79], s84 offen offset:3072 lds\n" \
"v_cvt_pk_bf16_f32 v230, v140, v141\n" \
"s_waitcnt lgkmcnt(9)\n" \
"v_mfma_f32_16x16x32_bf16 a[12:15], a[236:239], a[208:211], a[12:15]\n" \
"ds_read_b64_tr_b16 a[178:179], v36 offset:1024\n" \
"v_cvt_pk_bf16_f32 v231, v142, v143\n" \
"s_waitcnt lgkmcnt(8)\n" \
"v_mfma_f32_16x16x32_bf16 a[8:11], a[144:147], a[168:171], 0\n" \
"ds_read_b64_tr_b16 a[240:241], v36 offset:8192\n" \
"v_cvt_pk_bf16_f32 v232, v120, v121\n" \
"s_waitcnt lgkmcnt(7)\n" \
"s_nop 0\n" \
"v_mfma_f32_16x16x32_bf16 a[8:11], a[208:211], a[232:235], a[8:11]\n" \
"ds_read_b64_tr_b16 a[242:243], v36 offset:9216\n" \
"v_cvt_pk_bf16_f32 v233, v122, v123\n" \
"v_mfma_f32_16x16x32_bf16 a[24:27], a[148:151], a[168:171], 0\n" \
"ds_read_b64_tr_b16 a[180:181], v37 offset:0\n" \
"v_cvt_pk_bf16_f32 v234, v136, v137\n" \
"v_mfma_f32_16x16x32_bf16 a[24:27], a[212:215], a[232:235], a[24:27]\n" \
"ds_read_b64_tr_b16 a[182:183], v37 offset:1024\n" \
"v_cvt_pk_bf16_f32 v235, v138, v139\n" \
"v_mfma_f32_16x16x32_bf16 a[28:31], a[172:175], a[148:151], 0\n" \
"ds_read_b64_tr_b16 a[244:245], v37 offset:8192\n" \
"v_cvt_pk_bf16_f32 v236, v116, v117\n" \
"v_mfma_f32_16x16x32_bf16 a[28:31], a[236:239], a[212:215], a[28:31]\n" \
"s_add_u32 s89, s41, 0x2000\n" \
"buffer_store_dwordx4 v[200:203], v20, s[80:83], s89 offen offset:3072 sc1\n" \
"v_mfma_f32_16x16x32_bf16 a[20:23], a[164:167], a[148:151], 0\n" \
"ds_read_b64_tr_b16 a[246:247], v37 offset:9216\n" \
"v_cvt_pk_bf16_f32 v237, v118, v119\n" \
"v_mfma_f32_16x16x32_bf16 a[20:23], a[228:231], a[212:215], a[20:23]\n" \
"ds_read_b64_tr_b16 a[184:185], v38 offset:0\n" \
"v_cvt_pk_bf16_f32 v238, v132, v133\n" \
"v_mfma_f32_16x16x32_bf16 a[16:19], a[160:163], a[148:151], 0\n" \
"ds_read_b64_tr_b16 a[186:187], v38 offset:1024\n" \
"v_cvt_pk_bf16_f32 v239, v134, v135\n" \
"v_mfma_f32_16x16x32_bf16 a[16:19], a[224:227], a[212:215], a[16:19]\n" \
"v_cvt_pk_bf16_f32 v240, v160, v161\n" \
"v_cvt_pk_bf16_f32 v241, v162, v163\n" \
"v_cvt_pk_bf16_f32 v242, v176, v177\n" \
"v_mfma_f32_16x16x32_bf16 a[32:35], a[160:163], a[152:155], 0\n" \
"s_add_u32 s88, s41, 0x2000\n" \
"buffer_store_dwordx4 v[204:207], v20, s[80:83], s88 offen offset:2048 sc1\n" \
"v_mfma_f32_16x16x32_bf16 a[32:35], a[224:227], a[216:219], a[32:35]\n" \
"ds_read_b64_tr_b16 a[248:249], v38 offset:8192\n" \
"v_cvt_pk_bf16_f32 v243, v178, v179\n" \
"v_mfma_f32_16x16x32_bf16 a[36:39], a[164:167], a[152:155], 0\n" \
"ds_read_b64_tr_b16 a[250:251], v38 offset:9216\n" \
"v_cvt_pk_bf16_f32 v244, v156, v157\n" \
"v_mfma_f32_16x16x32_bf16 a[36:39], a[228:231], a[216:219], a[36:39]\n" \
"ds_read_b64_tr_b16 a[188:189], v39 offset:0\n" \
"v_cvt_pk_bf16_f32 v245, v158, v159\n" \
"v_mfma_f32_16x16x32_bf16 a[44:47], a[172:175], a[152:155], 0\n" \
"v_cvt_pk_bf16_f32 v246, v172, v173\n" \
"v_cvt_pk_bf16_f32 v247, v174, v175\n" \
"v_cvt_pk_bf16_f32 v184, v152, v153\n" \
"v_mfma_f32_16x16x32_bf16 a[44:47], a[236:239], a[216:219], a[44:47]\n" \
"ds_read_b64_tr_b16 a[190:191], v39 offset:1024\n" \
"v_cvt_pk_bf16_f32 v185, v154, v155\n" \
"v_mfma_f32_16x16x32_bf16 a[40:43], a[152:155], a[168:171], 0\n" \
"s_add_u32 s89, s41, 0x2000\n" \
"buffer_store_dwordx4 v[208:211], v20, s[80:83], s89 offen offset:1024 sc1\n" \
"v_mfma_f32_16x16x32_bf16 a[40:43], a[216:219], a[232:235], a[40:43]\n" \
"ds_read_b64_tr_b16 a[252:253], v39 offset:8192\n" \
"v_cvt_pk_bf16_f32 v186, v168, v169\n" \
"v_mfma_f32_16x16x32_bf16 a[56:59], a[156:159], a[168:171], 0\n" \
"ds_read_b64_tr_b16 a[254:255], v39 offset:9216\n" \
"v_cvt_pk_bf16_f32 v187, v170, v171\n" \
"v_mfma_f32_16x16x32_bf16 a[56:59], a[220:223], a[232:235], a[56:59]\n" \
"v_cvt_pk_bf16_f32 v188, v148, v149\n" \
"v_cvt_pk_bf16_f32 v189, v150, v151\n" \
"v_cvt_pk_bf16_f32 v190, v164, v165\n" \
"v_mfma_f32_16x16x32_bf16 a[60:63], a[172:175], a[156:159], 0\n" \
"v_cvt_pk_bf16_f32 v191, v166, v167\n" \
"v_cvt_pk_bf16_f32 v192, v96, v97\n" \
"v_cvt_pk_bf16_f32 v193, v98, v99\n" \
"v_mfma_f32_16x16x32_bf16 a[60:63], a[236:239], a[220:223], a[60:63]\n" \
"s_add_u32 s88, s41, 0x2000\n" \
"buffer_store_dwordx4 v[212:215], v20, s[80:83], s88 offen offset:0 sc1\n" \
"v_mfma_f32_16x16x32_bf16 a[52:55], a[164:167], a[156:159], 0\n" \
"v_cvt_pk_bf16_f32 v194, v112, v113\n" \
"v_cvt_pk_bf16_f32 v195, v114, v115\n" \
"v_cvt_pk_bf16_f32 v196, v92, v93\n" \
".fill (L_pb + 61152 - .) / 4, 4, 0xBF800000\n" \
"L_pp15:\n" \
"s_cbranch_execz L_pret\n" \
"s_nop 0\n" \
"v_mfma_f32_16x16x32_bf16 a[52:55], a[228:231], a[220:223], a[52:55]\n" \
"v_cvt_pk_bf16_f32 v197, v94, v95\n" \
"v_cvt_pk_bf16_f32 v198, v108, v109\n" \
"v_cvt_pk_bf16_f32 v199, v110, v111\n" \
"v_mfma_f32_16x16x32_bf16 a[48:51], a[160:163], a[156:159], 0\n" \
"v_cvt_pk_bf16_f32 v252, v88, v89\n" \
"v_cvt_pk_bf16_f32 v253, v90, v91\n" \
"v_cvt_pk_bf16_f32 v254, v104, v105\n" \
"v_mfma_f32_16x16x32_bf16 a[48:51], a[224:227], a[220:223], a[48:51]\n" \
"v_cvt_pk_bf16_f32 v255, v106, v107\n" \
"v_accvgpr_write_b32 a84, v252\n" \
"v_accvgpr_write_b32 a85, v253\n" \
"v_mfma_f32_16x16x32_bf16 a[64:67], a[164:167], a[140:143], 0\n" \
"v_accvgpr_write_b32 a86, v254\n" \
"v_accvgpr_write_b32 a87, v255\n" \
"v_cvt_pk_bf16_f32 v252, v84, v85\n" \
"v_mfma_f32_16x16x32_bf16 a[64:67], a[228:231], a[204:207], a[64:67]\n" \
"v_cvt_pk_bf16_f32 v253, v86, v87\n" \
"v_cvt_pk_bf16_f32 v254, v100, v101\n" \
"v_cvt_pk_bf16_f32 v255, v102, v103\n" \
"v_mfma_f32_16x16x32_bf16 a[80:83], a[172:175], a[140:143], 0\n" \
"v_mfma_f32_16x16x32_bf16 a[80:83], a[236:239], a[204:207], a[80:83]\n" \
"v_mfma_f32_16x16x32_bf16 a[68:71], a[140:143], a[168:171], 0\n" \
"v_mfma_f32_16x16x32_bf16 a[68:71], a[204:207], a[232:235], a[68:71]\n" \
"s_waitcnt vmcnt(4) lgkmcnt(0)\n" \
"s_barrier\n" \
"v_mfma_f32_16x16x32_bf16 v[116:119], a[176:179], a[144:147], 0\n" \
"s_add_u32 m0, s40, 0x0\n" \
"s_nop 0\n" \
"s_add_u32 s85, s39, 0x4000\n" \
"v_cvt_pk_bf16_f32 v200, v64, v65\n" \
"v_mfma_f32_16x16x32_bf16 v[116:119], a[240:243], a[208:211], v[116:119]\n" \
"buffer_load_dwordx4 v20, s[80:83], s85 offen sc1 lds\n" \
"v_cvt_pk_bf16_f32 v201, v66, v67\n" \
"v_mfma_f32_16x16x32_bf16 v[120:123], a[180:183], a[144:147], 0\n" \
"ds_read_b64_tr_b16 a[160:161], v32 offset:32768\n" \
"v_cvt_pk_bf16_f32 v202, v80, v81\n" \
"v_mfma_f32_16x16x32_bf16 v[120:123], a[244:247], a[208:211], v[120:123]\n" \
"ds_read_b64_tr_b16 a[162:163], v32 offset:33280\n" \
"v_cvt_pk_bf16_f32 v203, v82, v83\n" \
"v_mfma_f32_16x16x32_bf16 v[124:127], a[184:187], a[144:147], 0\n" \
"s_add_u32 m0, s40, 0x1000\n" \
"s_add_u32 s85, s39, 0x5000\n" \
"v_cvt_pk_bf16_f32 v204, v60, v61\n" \
"v_mfma_f32_16x16x32_bf16 v[124:127], a[248:251], a[208:211], v[124:127]\n" \
"buffer_load_dwordx4 v20, s[80:83], s85 offen sc1 lds\n" \
"v_cvt_pk_bf16_f32 v205, v62, v63\n" \
"v_mfma_f32_16x16x32_bf16 v[128:131], a[188:191], a[144:147], 0\n" \
"ds_read_b64_tr_b16 a[224:225], v32 offset:36864\n" \
"v_cvt_pk_bf16_f32 v206, v76, v77\n" \
"v_mfma_f32_16x16x32_bf16 v[128:131], a[252:255], a[208:211], v[128:131]\n" \
"ds_read_b128 a[144:147], v28 offset:40960\n" \
"v_cvt_pk_bf16_f32 v207, v78, v79\n" \
"v_mfma_f32_16x16x32_bf16 v[144:147], a[188:191], a[148:151], 0\n" \
"ds_read_b128 a[208:211], v29 offset:40960\n" \
"v_cvt_pk_bf16_f32 v208, v56, v57\n" \
"v_mfma_f32_16x16x32_bf16 v[144:147], a[252:255], a[212:215], v[144:147]\n" \
"s_add_u32 m0, s40, 0x3000\n" \
"s_movk_i32 s84, 0x100\n" \
"v_cvt_pk_bf16_f32 v209, v58, v59\n" \
"v_mfma_f32_16x16x32_bf16 v[140:143], a[184:187], a[148:151], 0\n" \
"buffer_load_dwordx4 v21, s[76:79], s84 offen offset:0 lds\n" \
"v_cvt_pk_bf16_f32 v210, v72, v73\n" \
"v_mfma_f32_16x16x32_bf16 v[140:143], a[248:251], a[212:215], v[140:143]\n" \
"ds_read_b64_tr_b16 a[226:227], v32 offset:37376\n" \
"v_cvt_pk_bf16_f32 v211, v74, v75\n" \
"v_mfma_f32_16x16x32_bf16 v[136:139], a[180:183], a[148:151], 0\n" \
"s_add_u32 m0, s30, 0x0\n" \
"s_movk_i32 s84, 0x100\n" \
"s_nop 0\n" \
"v_cvt_pk_bf16_f32 v212, v52, v53\n" \
"v_mfma_f32_16x16x32_bf16 v[136:139], a[244:247], a[212:215], v[136:139]\n" \
"buffer_load_dwordx4 v24, s[76:79], s84 offen offset:0 lds\n" \
"v_cvt_pk_bf16_f32 v213, v54, v55\n" \
"v_mfma_f32_16x16x32_bf16 v[132:135], a[176:179], a[148:151], 0\n" \
"ds_read_b64_tr_b16 a[164:165], v33 offset:32768\n" \
"v_cvt_pk_bf16_f32 v214, v68, v69\n" \
"v_mfma_f32_16x16x32_bf16 v[132:135], a[240:243], a[212:215], v[132:135]\n" \
"ds_read_b128 a[148:151], v28 offset:43008\n" \
"v_cvt_pk_bf16_f32 v215, v70, v71\n" \
"v_mfma_f32_16x16x32_bf16 v[148:151], a[176:179], a[152:155], 0\n" \
"ds_read_b128 a[212:215], v29 offset:43008\n" \
"v_mfma_f32_16x16x32_bf16 v[148:151], a[240:243], a[216:219], v[148:151]\n" \
"s_movk_i32 s84, 0x2100\n" \
"v_mfma_f32_16x16x32_bf16 v[152:155], a[180:183], a[152:155], 0\n" \
"buffer_load_dwordx4 v25, s[76:79], s84 offen offset:1024 lds\n" \
"v_mfma_f32_16x16x32_bf16 v[152:155], a[244:247], a[216:219], v[152:155]\n" \
"ds_read_b64_tr_b16 a[166:167], v33 offset:33280\n" \
"v_mfma_f32_16x16x32_bf16 v[156:159], a[184:187], a[152:155], 0\n" \
"ds_read_b64_tr_b16 a[228:229], v33 offset:36864\n" \
"v_mfma_f32_16x16x32_bf16 v[156:159], a[248:251], a[216:219], v[156:159]\n" \
"s_movk_i32 s84, 0x4100\n" \
"v_mfma_f32_16x16x32_bf16 v[160:163], a[188:191], a[152:155], 0\n" \
"buffer_load_dwordx4 v24, s[76:79], s84 offen offset:2048 lds\n" \
"v_mfma_f32_16x16x32_bf16 v[160:163], a[252:255], a[216:219], v[160:163]\n" \
"ds_read_b128 a[152:155], v28 offset:45056\n" \
"v_mfma_f32_16x16x32_bf16 v[176:179], a[188:191], a[156:159], 0\n" \
"ds_read_b128 a[216:219], v29 offset:45056\n" \
"v_mfma_f32_16x16x32_bf16 v[176:179], a[252:255], a[220:223], v[176:179]\n" \
"s_movk_i32 s84, 0x6100\n" \
"s_nop 0\n" \
"v_mfma_f32_16x16x32_bf16 v[172:175], a[184:187], a[156:159], 0\n" \
"buffer_load_dwordx4 v25, s[76:79], s84 offen offset:3072 lds\n" \
"v_mfma_f32_16x16x32_bf16 v[172:175], a[248:251], a[220:223], v[172:175]\n" \
"ds_read_b64_tr_b16 a[230:231], v33 offset:37376\n" \
"v_mfma_f32_16x16x32_bf16 v[168:171], a[180:183], a[156:159], 0\n" \
"ds_read_b64_tr_b16 a[172:173], v35 offset:32768\n" \
"v_mfma_f32_16x16x32_bf16 v[168:171], a[244:247], a[220:223], v[168:171]\n" \
"ds_read_b64_tr_b16 a[174:175], v35 offset:33280\n" \
"v_mfma_f32_16x16x32_bf16 v[164:167], a[176:179], a[156:159], 0\n" \
"v_mfma_f32_16x16x32_bf16 v[164:167], a[240:243], a[220:223], v[164:167]\n" \
"ds_read_b128 a[156:159], v28 offset:47104\n" \
"v_mfma_f32_16x16x32_bf16 v[84:87], a[176:179], a[136:139], 0\n" \
"ds_read_b128 a[220:223], v29 offset:47104\n" \
"v_mfma_f32_16x16x32_bf16 v[84:87], a[240:243], a[200:203], v[84:87]\n" \
"s_add_u32 m0, s31, 0x0\n" \
"s_mov_b32 s84, 0x100000\n" \
"v_mfma_f32_16x16x32_bf16 v[88:91], a[180:183], a[136:139], 0\n" \
"buffer_load_dwordx4 v26, s[68:71], s84 offen offset:0 lds\n" \
"v_mfma_f32_16x16x32_bf16 v[88:91], a[244:247], a[200:203], v[88:91]\n" \
"s_addk_i32 s84, 0x7c00\n" \
"v_mfma_f32_16x16x32_bf16 v[92:95], a[184:187], a[136:139], 0\n" \
"buffer_load_dwordx4 v26, s[68:71], s84 offen offset:1024 lds\n" \
"v_mfma_f32_16x16x32_bf16 v[92:95], a[248:251], a[200:203], v[92:95]\n" \
"ds_read_b64_tr_b16 a[236:237], v35 offset:36864\n" \
"v_mfma_f32_16x16x32_bf16 v[96:99], a[188:191], a[136:139], 0\n" \
"ds_read_b64_tr_b16 a[238:239], v35 offset:37376\n" \
"v_mfma_f32_16x16x32_bf16 v[96:99], a[252:255], a[200:203], v[96:99]\n" \
"ds_read_b128 a[136:139], v28 offset:36864\n" \
"v_mfma_f32_16x16x32_bf16 v[112:115], a[188:191], a[140:143], 0\n" \
"ds_read_b128 a[200:203], v29 offset:36864\n" \
"v_mfma_f32_16x16x32_bf16 v[112:115], a[252:255], a[204:207], v[112:115]\n" \
"s_addk_i32 s84, 0x7c00\n" \
"v_mfma_f32_16x16x32_bf16 v[108:111], a[184:187], a[140:143], 0\n" \
"buffer_load_dwordx4 v27, s[68:71], s84 offen offset:2048 lds\n" \
"v_mfma_f32_16x16x32_bf16 v[108:111], a[248:251], a[204:207], v[108:111]\n" \
"s_addk_i32 s84, 0x7c00\n" \
"v_mfma_f32_16x16x32_bf16 v[104:107], a[180:183], a[140:143], 0\n" \
"buffer_load_dwordx4 v27, s[68:71], s84 offen offset:3072 lds\n" \
"v_mfma_f32_16x16x32_bf16 v[104:107], a[244:247], a[204:207], v[104:107]\n" \
"ds_read_b64_tr_b16 a[168:169], v34 offset:32768\n" \
"v_mfma_f32_16x16x32_bf16 v[100:103], a[176:179], a[140:143], 0\n" \
"ds_read_b64_tr_b16 a[170:171], v34 offset:33280\n" \
"v_mfma_f32_16x16x32_bf16 v[100:103], a[240:243], a[204:207], v[100:103]\n" \
"ds_read_b128 a[140:143], v28 offset:38912\n" \
"v_mfma_f32_16x16x32_bf16 v[52:55], a[176:179], a[128:131], 0\n" \
"ds_read_b128 a[204:207], v29 offset:38912\n" \
"v_mfma_f32_16x16x32_bf16 v[52:55], a[240:243], a[192:195], v[52:55]\n" \
"s_add_u32 m0, s30, 0x1000\n" \
"s_mov_b32 s84, 0x9100\n" \
"v_mfma_f32_16x16x32_bf16 v[56:59], a[180:183], a[128:131], 0\n" \
"buffer_load_dwordx4 v24, s[76:79], s84 offen offset:0 lds\n" \
"v_mfma_f32_16x16x32_bf16 v[56:59], a[244:247], a[192:195], v[56:59]\n" \
"s_addk_i32 s84, 0x2000\n" \
"s_nop 0\n" \
"v_mfma_f32_16x16x32_bf16 v[60:63], a[184:187], a[128:131], 0\n" \
"buffer_load_dwordx4 v25, s[76:79], s84 offen offset:1024 lds\n" \
"v_mfma_f32_16x16x32_bf16 v[60:63], a[248:251], a[192:195], v[60:63]\n" \
"ds_read_b64_tr_b16 a[232:233], v34 offset:36864\n" \
"v_mfma_f32_16x16x32_bf16 v[64:67], a[188:191], a[128:131], 0\n" \
"ds_read_b64_tr_b16 a[234:235], v34 offset:37376\n" \
"v_mfma_f32_16x16x32_bf16 v[64:67], a[252:255], a[192:195], v[64:67]\n" \
"ds_read_b128 a[128:131], v28 offset:32768\n" \
"v_mfma_f32_16x16x32_bf16 v[80:83], a[188:191], a[132:135], 0\n" \
"ds_read_b128 a[192:195], v29 offset:32768\n" \
"v_mfma_f32_16x16x32_bf16 v[80:83], a[252:255], a[196:199], v[80:83]\n" \
"v_mfma_f32_16x16x32_bf16 v[76:79], a[184:187], a[132:135], 0\n" \
"v_mfma_f32_16x16x32_bf16 v[76:79], a[248:251], a[196:199], v[76:79]\n" \
"v_mfma_f32_16x16x32_bf16 v[72:75], a[180:183], a[132:135], 0\n" \
"v_mfma_f32_16x16x32_bf16 v[72:75], a[244:247], a[196:199], v[72:75]\n" \
"v_mfma_f32_16x16x32_bf16 v[68:71], a[176:179], a[132:135], 0\n" \
"v_mfma_f32_16x16x32_bf16 v[68:71], a[240:243], a[196:199], v[68:71]\n" \
"ds_read_b128 a[132:135], v28 offset:34816\n" \
"ds_read_b128 a[196:199], v29 offset:34816\n" \
"s_waitcnt lgkmcnt(15)\n" \
"v_mfma_f32_16x16x32_bf16 a[0:3], a[160:163], a[144:147], a[0:3]\n" \
"s_addk_i32 s84, 0x2000\n" \
"v_mfma_f32_16x16x32_bf16 a[0:3], a[224:227], a[208:211], a[0:3]\n" \
"buffer_load_dwordx4 v24, s[76:79], s84 offen offset:2048 lds\n" \
"v_mfma_f32_16x16x32_bf16 a[4:7], a[164:167], a[144:147], a[4:7]\n" \
"ds_read_b64_tr_b16 a[176:177], v36 offset:32768\n" \
"v_mov_b32_e64 v11, 0\n" \
"v_mfma_f32_16x16x32_bf16 a[4:7], a[228:231], a[208:211], a[4:7]\n" \
"s_addk_i32 s84, 0x2000\n" \
"v_mfma_f32_16x16x32_bf16 a[12:15], a[172:175], a[144:147], a[12:15]\n" \
"buffer_load_dwordx4 v25, s[76:79], s84 offen offset:3072 lds\n" \
"s_waitcnt lgkmcnt(13)\n" \
"v_mfma_f32_16x16x32_bf16 a[12:15], a[236:239], a[208:211], a[12:15]\n" \
"ds_read_b64_tr_b16 a[178:179], v36 offset:33792\n" \
"s_and_b32 s6, s2, 7\n" \
"s_waitcnt lgkmcnt(10)\n" \
"v_mfma_f32_16x16x32_bf16 a[8:11], a[144:147], a[168:171], a[8:11]\n" \
"ds_read_b64_tr_b16 a[240:241], v36 offset:40960\n" \
"s_lshl_b32 s7, s3, 1\n" \
"s_waitcnt lgkmcnt(7)\n" \
"v_mfma_f32_16x16x32_bf16 a[8:11], a[208:211], a[232:235], a[8:11]\n" \
"ds_read_b64_tr_b16 a[242:243], v36 offset:41984\n" \
"s_lshr_b32 s4, s2, 5\n" \
"v_mfma_f32_16x16x32_bf16 a[24:27], a[148:151], a[168:171], a[24:27]\n" \
"ds_read_b64_tr_b16 a[180:181], v37 offset:32768\n" \
"s_lshl_b32 s5, s6, 2\n" \
"v_mfma_f32_16x16x32_bf16 a[24:27], a[212:215], a[232:235], a[24:27]\n" \
"ds_read_b64_tr_b16 a[182:183], v37 offset:33792\n" \
"s_bfe_u32 s8, s2, 0x20003\n" \
"v_mfma_f32_16x16x32_bf16 a[28:31], a[172:175], a[148:151], a[28:31]\n" \
"ds_read_b64_tr_b16 a[244:245], v37 offset:40960\n" \
"s_and_b32 s7, s7, 4\n" \
"v_mfma_f32_16x16x32_bf16 a[28:31], a[236:239], a[212:215], a[28:31]\n" \
"s_add_u32 s89, s41, 0x3000\n" \
"buffer_store_dwordx4 v[192:195], v20, s[80:83], s89 offen offset:3072 sc1\n" \
"v_mfma_f32_16x16x32_bf16 a[20:23], a[164:167], a[148:151], a[20:23]\n" \
"ds_read_b64_tr_b16 a[246:247], v37 offset:41984\n" \
"s_and_b32 s9, s3, 1\n" \
"v_mfma_f32_16x16x32_bf16 a[20:23], a[228:231], a[212:215], a[20:23]\n" \
"ds_read_b64_tr_b16 a[184:185], v38 offset:32768\n" \
"s_or_b32 s13, s5, s8\n" \
"v_mfma_f32_16x16x32_bf16 a[16:19], a[160:163], a[148:151], a[16:19]\n" \
"ds_read_b64_tr_b16 a[186:187], v38 offset:33792\n" \
"s_or_b32 s9, s7, s9\n" \
"v_mfma_f32_16x16x32_bf16 a[16:19], a[224:227], a[212:215], a[16:19]\n" \
"s_mul_i32 s7, s4, 0xa00\n" \
"s_add_i32 s7, s7, 0x5bc1000\n" \
"v_mfma_f32_16x16x32_bf16 a[32:35], a[160:163], a[152:155], a[32:35]\n" \
"s_add_u32 s88, s41, 0x3000\n" \
"buffer_store_dwordx4 v[196:199], v20, s[80:83], s88 offen offset:2048 sc1\n" \
"v_mfma_f32_16x16x32_bf16 a[32:35], a[224:227], a[216:219], a[32:35]\n" \
"ds_read_b64_tr_b16 a[248:249], v38 offset:40960\n" \
"s_lshl_b32 s10, s13, 2\n" \
"v_mfma_f32_16x16x32_bf16 a[36:39], a[164:167], a[152:155], a[36:39]\n" \
"ds_read_b64_tr_b16 a[250:251], v38 offset:41984\n" \
"s_or_b32 s10, s10, s7\n" \
"v_mfma_f32_16x16x32_bf16 a[36:39], a[228:231], a[216:219], a[36:39]\n" \
"ds_read_b64_tr_b16 a[188:189], v39 offset:32768\n" \
"v_writelane_b32 v11, s10, 0\n" \
"v_mfma_f32_16x16x32_bf16 a[44:47], a[172:175], a[152:155], a[44:47]\n" \
"s_lshl_b32 s14, s4, 3\n" \
"v_writelane_b32 v11, s7, 1\n" \
"v_mfma_f32_16x16x32_bf16 a[44:47], a[236:239], a[216:219], a[44:47]\n" \
"ds_read_b64_tr_b16 a[190:191], v39 offset:33792\n" \
"s_or_b32 s7, s14, s6\n" \
"v_mfma_f32_16x16x32_bf16 a[40:43], a[152:155], a[168:171], a[40:43]\n" \
"s_add_u32 s89, s41, 0x3000\n" \
"buffer_store_dwordx4 a[84:87], v20, s[80:83], s89 offen offset:1024 sc1\n" \
"v_mfma_f32_16x16x32_bf16 a[40:43], a[216:219], a[232:235], a[40:43]\n" \
"ds_read_b64_tr_b16 a[252:253], v39 offset:40960\n" \
"s_lshl_b32 s7, s7, 10\n" \
"v_mfma_f32_16x16x32_bf16 a[56:59], a[156:159], a[168:171], a[56:59]\n" \
"ds_read_b64_tr_b16 a[254:255], v39 offset:41984\n" \
"s_lshl_b32 s10, s3, 8\n" \
"v_mfma_f32_16x16x32_bf16 a[56:59], a[220:223], a[232:235], a[56:59]\n" \
"s_or_b32 s7, s7, s10\n" \
"s_or_b32 s10, s7, 0x5c00200\n" \
"v_mfma_f32_16x16x32_bf16 a[60:63], a[172:175], a[156:159], a[60:63]\n" \
"v_writelane_b32 v11, s10, 2\n" \
"s_lshl_b32 s10, s8, 2\n" \
"v_mfma_f32_16x16x32_bf16 a[60:63], a[236:239], a[220:223], a[60:63]\n" \
"s_add_u32 s88, s41, 0x3000\n" \
"buffer_store_dwordx4 v[252:255], v20, s[80:83], s88 offen offset:0 sc1\n" \
"v_mfma_f32_16x16x32_bf16 a[52:55], a[164:167], a[156:159], a[52:55]\n" \
"s_or_b32 s11, s10, s7\n" \
"s_or_b32 s11, s11, 0x5c00000\n" \
"v_mfma_f32_16x16x32_bf16 a[52:55], a[228:231], a[220:223], a[52:55]\n" \
"v_writelane_b32 v11, s11, 3\n" \
"s_or_b32 s7, s7, 0x5c00080\n" \
"v_mfma_f32_16x16x32_bf16 a[48:51], a[160:163], a[156:159], a[48:51]\n" \
"v_writelane_b32 v11, s7, 4\n" \
"s_or_b32 s7, s7, s10\n" \
"v_mfma_f32_16x16x32_bf16 a[48:51], a[224:227], a[220:223], a[48:51]\n" \
"s_add_i32 s10, s3, s2\n" \
"v_writelane_b32 v11, s7, 5\n" \
"v_mfma_f32_16x16x32_bf16 a[64:67], a[164:167], a[140:143], a[64:67]\n" \
"s_mov_b32 s7, 2.0\n" \
"s_mov_b32 s11, 0x5bc0000\n" \
"v_mfma_f32_16x16x32_bf16 a[64:67], a[228:231], a[204:207], a[64:67]\n" \
"s_cmp_lg_u32 s10, 0\n" \
"s_cselect_b32 s10, s7, s11\n" \
"v_mfma_f32_16x16x32_bf16 a[80:83], a[172:175], a[140:143], a[80:83]\n" \
"s_lshr_b32 s15, s2, 3\n" \
"v_writelane_b32 v11, s10, 54\n" \
"v_mfma_f32_16x16x32_bf16 a[80:83], a[236:239], a[204:207], a[80:83]\n" \
"s_lshl_b32 s10, s6, 5\n" \
"s_min_u32 s16, s3, 2\n" \
"v_mfma_f32_16x16x32_bf16 a[68:71], a[140:143], a[168:171], a[68:71]\n" \
"s_mul_i32 s12, s8, 0x120\n" \
"v_mfma_f32_16x16x32_bf16 a[68:71], a[204:207], a[232:235], a[68:71]\n" \
"s_waitcnt vmcnt(4) lgkmcnt(0)\n" \
"s_barrier\n" \
"s_nop 0\n" \
"v_mfma_f32_16x16x32_bf16 v[116:119], a[176:179], a[144:147], v[116:119]\n" \
"s_add_u32 m0, s40, 0x8000\n" \
"s_add_u32 s85, s39, 0x6000\n" \
"v_mfma_f32_16x16x32_bf16 v[116:119], a[240:243], a[208:211], v[116:119]\n" \
"buffer_load_dwordx4 v20, s[80:83], s85 offen sc1 lds\n" \
"v_mfma_f32_16x16x32_bf16 v[120:123], a[180:183], a[144:147], v[120:123]\n" \
"ds_read_b64_tr_b16 a[160:161], v32 offset:0\n" \
"v_mfma_f32_16x16x32_bf16 v[120:123], a[244:247], a[208:211], v[120:123]\n" \
"ds_read_b64_tr_b16 a[162:163], v32 offset:512\n" \
"v_mfma_f32_16x16x32_bf16 v[124:127], a[184:187], a[144:147], v[124:127]\n" \
"s_add_u32 m0, s40, 0x9000\n" \
"s_add_u32 s85, s39, 0x7000\n" \
"v_mfma_f32_16x16x32_bf16 v[124:127], a[248:251], a[208:211], v[124:127]\n" \
"buffer_load_dwordx4 v20, s[80:83], s85 offen sc1 lds\n" \
"v_mfma_f32_16x16x32_bf16 v[128:131], a[188:191], a[144:147], v[128:131]\n" \
"ds_read_b64_tr_b16 a[224:225], v32 offset:4096\n" \
"v_mfma_f32_16x16x32_bf16 v[128:131], a[252:255], a[208:211], v[128:131]\n" \
"ds_read_b128 a[144:147], v28 offset:8192\n" \
"v_mfma_f32_16x16x32_bf16 v[144:147], a[188:191], a[148:151], v[144:147]\n" \
"ds_read_b128 a[208:211], v29 offset:8192\n" \
"v_mfma_f32_16x16x32_bf16 v[144:147], a[252:255], a[212:215], v[144:147]\n" \
"s_add_u32 m0, s40, 0xb000\n" \
"s_movk_i32 s84, 0x180\n" \
"v_mfma_f32_16x16x32_bf16 v[140:143], a[184:187], a[148:151], v[140:143]\n" \
"buffer_load_dwordx4 v21, s[76:79], s84 offen offset:0 lds\n" \
"v_mfma_f32_16x16x32_bf16 v[140:143], a[248:251], a[212:215], v[140:143]\n" \
"ds_read_b64_tr_b16 a[226:227], v32 offset:4608\n" \
"v_mfma_f32_16x16x32_bf16 v[136:139], a[180:183], a[148:151], v[136:139]\n" \
"s_add_u32 m0, s30, 0x8000\n" \
"s_movk_i32 s84, 0x180\n" \
"v_mfma_f32_16x16x32_bf16 v[136:139], a[244:247], a[212:215], v[136:139]\n" \
"buffer_load_dwordx4 v24, s[76:79], s84 offen offset:0 lds\n" \
"v_mfma_f32_16x16x32_bf16 v[132:135], a[176:179], a[148:151], v[132:135]\n" \
"ds_read_b64_tr_b16 a[164:165], v33 offset:0\n" \
"v_mfma_f32_16x16x32_bf16 v[132:135], a[240:243], a[212:215], v[132:135]\n" \
"ds_read_b128 a[148:151], v28 offset:10240\n" \
"v_mfma_f32_16x16x32_bf16 v[148:151], a[176:179], a[152:155], v[148:151]\n" \
"ds_read_b128 a[212:215], v29 offset:10240\n" \
"v_mfma_f32_16x16x32_bf16 v[148:151], a[240:243], a[216:219], v[148:151]\n" \
"s_movk_i32 s84, 0x2180\n" \
"v_mfma_f32_16x16x32_bf16 v[152:155], a[180:183], a[152:155], v[152:155]\n" \
"buffer_load_dwordx4 v25, s[76:79], s84 offen offset:1024 lds\n" \
"v_mfma_f32_16x16x32_bf16 v[152:155], a[244:247], a[216:219], v[152:155]\n" \
"ds_read_b64_tr_b16 a[166:167], v33 offset:512\n" \
"v_mfma_f32_16x16x32_bf16 v[156:159], a[184:187], a[152:155], v[156:159]\n" \
"ds_read_b64_tr_b16 a[228:229], v33 offset:4096\n" \
"v_mfma_f32_16x16x32_bf16 v[156:159], a[248:251], a[216:219], v[156:159]\n" \
"s_movk_i32 s84, 0x4180\n" \
"v_mfma_f32_16x16x32_bf16 v[160:163], a[188:191], a[152:155], v[160:163]\n" \
"buffer_load_dwordx4 v24, s[76:79], s84 offen offset:2048 lds\n" \
"v_mfma_f32_16x16x32_bf16 v[160:163], a[252:255], a[216:219], v[160:163]\n" \
"ds_read_b128 a[152:155], v28 offset:12288\n" \
"v_mfma_f32_16x16x32_bf16 v[176:179], a[188:191], a[156:159], v[176:179]\n" \
"ds_read_b128 a[216:219], v29 offset:12288\n" \
"v_mfma_f32_16x16x32_bf16 v[176:179], a[252:255], a[220:223], v[176:179]\n" \
"s_movk_i32 s84, 0x6180\n" \
"v_mfma_f32_16x16x32_bf16 v[172:175], a[184:187], a[156:159], v[172:175]\n" \
"buffer_load_dwordx4 v25, s[76:79], s84 offen offset:3072 lds\n" \
"v_mfma_f32_16x16x32_bf16 v[172:175], a[248:251], a[220:223], v[172:175]\n" \
"ds_read_b64_tr_b16 a[230:231], v33 offset:4608\n" \
"v_mfma_f32_16x16x32_bf16 v[168:171], a[180:183], a[156:159], v[168:171]\n" \
"ds_read_b64_tr_b16 a[172:173], v35 offset:0\n" \
"v_mfma_f32_16x16x32_bf16 v[168:171], a[244:247], a[220:223], v[168:171]\n" \
"ds_read_b64_tr_b16 a[174:175], v35 offset:512\n" \
"v_mfma_f32_16x16x32_bf16 v[164:167], a[176:179], a[156:159], v[164:167]\n" \
"s_add_u32 s11, s22, s12\n" \
"s_addc_u32 s17, s23, 0\n" \
"v_mfma_f32_16x16x32_bf16 v[164:167], a[240:243], a[220:223], v[164:167]\n" \
"ds_read_b128 a[156:159], v28 offset:14336\n" \
"v_mfma_f32_16x16x32_bf16 v[84:87], a[176:179], a[136:139], v[84:87]\n" \
"ds_read_b128 a[220:223], v29 offset:14336\n" \
"v_mfma_f32_16x16x32_bf16 v[84:87], a[240:243], a[200:203], v[84:87]\n" \
"s_add_u32 m0, s31, 0x8000\n" \
"s_mov_b32 s84, 0x180000\n" \
"v_mfma_f32_16x16x32_bf16 v[88:91], a[180:183], a[136:139], v[88:91]\n" \
"buffer_load_dwordx4 v26, s[68:71], s84 offen offset:0 lds\n" \
"v_mfma_f32_16x16x32_bf16 v[88:91], a[244:247], a[200:203], v[88:91]\n" \
"s_addk_i32 s84, 0x7c00\n" \
"v_mfma_f32_16x16x32_bf16 v[92:95], a[184:187], a[136:139], v[92:95]\n" \
"buffer_load_dwordx4 v26, s[68:71], s84 offen offset:1024 lds\n" \
"v_mfma_f32_16x16x32_bf16 v[92:95], a[248:251], a[200:203], v[92:95]\n" \
"ds_read_b64_tr_b16 a[236:237], v35 offset:4096\n" \
"v_mfma_f32_16x16x32_bf16 v[96:99], a[188:191], a[136:139], v[96:99]\n" \
"ds_read_b64_tr_b16 a[238:239], v35 offset:4608\n" \
"v_mfma_f32_16x16x32_bf16 v[96:99], a[252:255], a[200:203], v[96:99]\n" \
"ds_read_b128 a[136:139], v28 offset:4096\n" \
"v_mfma_f32_16x16x32_bf16 v[112:115], a[188:191], a[140:143], v[112:115]\n" \
"ds_read_b128 a[200:203], v29 offset:4096\n" \
"v_mfma_f32_16x16x32_bf16 v[112:115], a[252:255], a[204:207], v[112:115]\n" \
"s_addk_i32 s84, 0x7c00\n" \
"v_mfma_f32_16x16x32_bf16 v[108:111], a[184:187], a[140:143], v[108:111]\n" \
"buffer_load_dwordx4 v27, s[68:71], s84 offen offset:2048 lds\n" \
"v_mfma_f32_16x16x32_bf16 v[108:111], a[248:251], a[204:207], v[108:111]\n" \
"s_addk_i32 s84, 0x7c00\n" \
"v_mfma_f32_16x16x32_bf16 v[104:107], a[180:183], a[140:143], v[104:107]\n" \
"buffer_load_dwordx4 v27, s[68:71], s84 offen offset:3072 lds\n" \
"v_mfma_f32_16x16x32_bf16 v[104:107], a[244:247], a[204:207], v[104:107]\n" \
"ds_read_b64_tr_b16 a[168:169], v34 offset:0\n" \
"v_mfma_f32_16x16x32_bf16 v[100:103], a[176:179], a[140:143], v[100:103]\n" \
"ds_read_b64_tr_b16 a[170:171], v34 offset:512\n" \
"v_mfma_f32_16x16x32_bf16 v[100:103], a[240:243], a[204:207], v[100:103]\n" \
"ds_read_b128 a[140:143], v28 offset:6144\n" \
"v_mfma_f32_16x16x32_bf16 v[52:55], a[176:179], a[128:131], v[52:55]\n" \
"ds_read_b128 a[204:207], v29 offset:6144\n" \
"v_mfma_f32_16x16x32_bf16 v[52:55], a[240:243], a[192:195], v[52:55]\n" \
"s_add_u32 m0, s30, 0x9000\n" \
"s_mov_b32 s84, 0x9180\n" \
"v_mfma_f32_16x16x32_bf16 v[56:59], a[180:183], a[128:131], v[56:59]\n" \
"buffer_load_dwordx4 v24, s[76:79], s84 offen offset:0 lds\n" \
"v_mfma_f32_16x16x32_bf16 v[56:59], a[244:247], a[192:195], v[56:59]\n" \
"s_addk_i32 s84, 0x2000\n" \
"s_nop 0\n" \
"v_mfma_f32_16x16x32_bf16 v[60:63], a[184:187], a[128:131], v[60:63]\n" \
"buffer_load_dwordx4 v25, s[76:79], s84 offen offset:1024 lds\n" \
"v_mfma_f32_16x16x32_bf16 v[60:63], a[248:251], a[192:195], v[60:63]\n" \
"ds_read_b64_tr_b16 a[232:233], v34 offset:4096\n" \
"v_mfma_f32_16x16x32_bf16 v[64:67], a[188:191], a[128:131], v[64:67]\n" \
"ds_read_b64_tr_b16 a[234:235], v34 offset:4608\n" \
"v_mfma_f32_16x16x32_bf16 v[64:67], a[252:255], a[192:195], v[64:67]\n" \
"ds_read_b128 a[128:131], v28 offset:0\n" \
"v_mfma_f32_16x16x32_bf16 v[80:83], a[188:191], a[132:135], v[80:83]\n" \
"ds_read_b128 a[192:195], v29 offset:0\n" \
"v_mfma_f32_16x16x32_bf16 v[80:83], a[252:255], a[196:199], v[80:83]\n" \
"s_add_u32 s18, s24, s12\n" \
"v_writelane_b32 v11, s11, 9\n" \
"s_addc_u32 s19, s25, 0\n" \
"v_mfma_f32_16x16x32_bf16 v[76:79], a[184:187], a[132:135], v[76:79]\n" \
"s_and_b32 s20, s17, 0xffff\n" \
"v_writelane_b32 v11, s20, 10\n" \
"v_mfma_f32_16x16x32_bf16 v[76:79], a[248:251], a[196:199], v[76:79]\n" \
"s_and_b32 s20, s19, 0xffff\n" \
"v_writelane_b32 v11, s18, 11\n" \
"v_mfma_f32_16x16x32_bf16 v[72:75], a[180:183], a[132:135], v[72:75]\n" \
"s_cmpk_lt_u32 s3, 3\n" \
"s_cselect_b32 s11, s11, s18\n" \
"v_mfma_f32_16x16x32_bf16 v[72:75], a[244:247], a[196:199], v[72:75]\n" \
"s_movk_i32 s18, 0xfff\n" \
"v_writelane_b32 v11, s20, 12\n" \
"v_mfma_f32_16x16x32_bf16 v[68:71], a[176:179], a[132:135], v[68:71]\n" \
"s_and_b32 s20, s29, 0xffff\n" \
"v_writelane_b32 v11, s28, 13\n" \
"v_mfma_f32_16x16x32_bf16 v[68:71], a[240:243], a[196:199], v[68:71]\n" \
"ds_read_b128 a[132:135], v28 offset:2048\n" \
"ds_read_b128 a[196:199], v29 offset:2048\n" \
"s_waitcnt lgkmcnt(15)\n" \
"v_mfma_f32_16x16x32_bf16 a[0:3], a[160:163], a[144:147], a[0:3]\n" \
"s_addk_i32 s84, 0x2000\n" \
"v_mfma_f32_16x16x32_bf16 a[0:3], a[224:227], a[208:211], a[0:3]\n" \
"buffer_load_dwordx4 v24, s[76:79], s84 offen offset:2048 lds\n" \
"v_mfma_f32_16x16x32_bf16 a[4:7], a[164:167], a[144:147], a[4:7]\n" \
"ds_read_b64_tr_b16 a[176:177], v36 offset:0\n" \
"v_writelane_b32 v11, s20, 14\n" \
"v_mfma_f32_16x16x32_bf16 a[4:7], a[228:231], a[208:211], a[4:7]\n" \
"s_addk_i32 s84, 0x2000\n" \
"v_mfma_f32_16x16x32_bf16 a[12:15], a[172:175], a[144:147], a[12:15]\n" \
"buffer_load_dwordx4 v25, s[76:79], s84 offen offset:3072 lds\n" \
"s_waitcnt lgkmcnt(13)\n" \
"v_mfma_f32_16x16x32_bf16 a[12:15], a[236:239], a[208:211], a[12:15]\n" \
"ds_read_b64_tr_b16 a[178:179], v36 offset:1024\n" \
"v_writelane_b32 v11, s11, 15\n" \
"s_waitcnt lgkmcnt(10)\n" \
"v_mfma_f32_16x16x32_bf16 a[8:11], a[144:147], a[168:171], a[8:11]\n" \
"ds_read_b64_tr_b16 a[240:241], v36 offset:8192\n" \
"s_waitcnt lgkmcnt(7)\n" \
"s_nop 0\n" \
"v_mfma_f32_16x16x32_bf16 a[8:11], a[208:211], a[232:235], a[8:11]\n" \
"ds_read_b64_tr_b16 a[242:243], v36 offset:9216\n" \
"v_mfma_f32_16x16x32_bf16 a[24:27], a[148:151], a[168:171], a[24:27]\n" \
"ds_read_b64_tr_b16 a[180:181], v37 offset:0\n" \
"v_mfma_f32_16x16x32_bf16 a[24:27], a[212:215], a[232:235], a[24:27]\n" \
"ds_read_b64_tr_b16 a[182:183], v37 offset:1024\n" \
"v_mfma_f32_16x16x32_bf16 a[28:31], a[172:175], a[148:151], a[28:31]\n" \
"ds_read_b64_tr_b16 a[244:245], v37 offset:8192\n" \
"v_mfma_f32_16x16x32_bf16 a[28:31], a[236:239], a[212:215], a[28:31]\n" \
"s_add_u32 s89, s41, 0x1000\n" \
"buffer_store_dwordx4 v[200:203], v20, s[80:83], s89 offen offset:3072 sc1\n" \
"v_mfma_f32_16x16x32_bf16 a[20:23], a[164:167], a[148:151], a[20:23]\n" \
"ds_read_b64_tr_b16 a[246:247], v37 offset:9216\n" \
"v_mfma_f32_16x16x32_bf16 a[20:23], a[228:231], a[212:215], a[20:23]\n" \
"ds_read_b64_tr_b16 a[184:185], v38 offset:0\n" \
"v_mfma_f32_16x16x32_bf16 a[16:19], a[160:163], a[148:151], a[16:19]\n" \
"ds_read_b64_tr_b16 a[186:187], v38 offset:1024\n" \
"v_mfma_f32_16x16x32_bf16 a[16:19], a[224:227], a[212:215], a[16:19]\n" \
"s_cmpk_lt_u32 s3, 3\n" \
"s_cselect_b32 s11, s17, s19\n" \
"v_mfma_f32_16x16x32_bf16 a[32:35], a[160:163], a[152:155], a[32:35]\n" \
"s_add_u32 s88, s41, 0x1000\n" \
"buffer_store_dwordx4 v[204:207], v20, s[80:83], s88 offen offset:2048 sc1\n" \
"v_mfma_f32_16x16x32_bf16 a[32:35], a[224:227], a[216:219], a[32:35]\n" \
"ds_read_b64_tr_b16 a[248:249], v38 offset:8192\n" \
"s_and_b32 s11, s11, 0xffff\n" \
"v_mfma_f32_16x16x32_bf16 a[36:39], a[164:167], a[152:155], a[36:39]\n" \
"ds_read_b64_tr_b16 a[250:251], v38 offset:9216\n" \
"v_writelane_b32 v11, s11, 16\n" \
"v_mfma_f32_16x16x32_bf16 a[36:39], a[228:231], a[216:219], a[36:39]\n" \
"ds_read_b64_tr_b16 a[188:189], v39 offset:0\n" \
"s_mul_i32 s11, s6, 0x48\n" \
"v_mfma_f32_16x16x32_bf16 a[44:47], a[172:175], a[152:155], a[44:47]\n" \
"s_add_i32 s17, s11, 0x47\n" \
"v_mfma_f32_16x16x32_bf16 a[44:47], a[236:239], a[216:219], a[44:47]\n" \
"ds_read_b64_tr_b16 a[190:191], v39 offset:1024\n" \
"v_mfma_f32_16x16x32_bf16 a[40:43], a[152:155], a[168:171], a[40:43]\n" \
"s_add_u32 s89, s41, 0x1000\n" \
"buffer_store_dwordx4 v[208:211], v20, s[80:83], s89 offen offset:1024 sc1\n" \
"v_mfma_f32_16x16x32_bf16 a[40:43], a[216:219], a[232:235], a[40:43]\n" \
"ds_read_b64_tr_b16 a[252:253], v39 offset:8192\n" \
"v_mfma_f32_16x16x32_bf16 a[56:59], a[156:159], a[168:171], a[56:59]\n" \
"ds_read_b64_tr_b16 a[254:255], v39 offset:9216\n" \
"v_mfma_f32_16x16x32_bf16 a[56:59], a[220:223], a[232:235], a[56:59]\n" \
"s_cmpk_lt_u32 s3, 3\n" \
"s_cselect_b32 s17, s17, s18\n" \
"v_mfma_f32_16x16x32_bf16 a[60:63], a[172:175], a[156:159], a[60:63]\n" \
"s_mulk_i32 s17, 0x480\n" \
"s_addk_i32 s17, 0x120\n" \
"v_mfma_f32_16x16x32_bf16 a[60:63], a[236:239], a[220:223], a[60:63]\n" \
"s_add_u32 s88, s41, 0x1000\n" \
"buffer_store_dwordx4 v[212:215], v20, s[80:83], s88 offen offset:0 sc1\n" \
"v_mfma_f32_16x16x32_bf16 a[52:55], a[164:167], a[156:159], a[52:55]\n" \
"v_writelane_b32 v11, s17, 17\n" \
"s_lshl_b32 s17, s3, 5\n" \
"v_mfma_f32_16x16x32_bf16 a[52:55], a[228:231], a[220:223], a[52:55]\n" \
"s_add_i32 s11, s11, s17\n" \
"s_mul_i32 s17, s11, 0x480\n" \
"v_mfma_f32_16x16x32_bf16 a[48:51], a[160:163], a[156:159], a[48:51]\n" \
"s_mul_i32 s11, s13, 0x24000\n" \
"s_add_i32 s18, s11, 0x1b000\n" \
"v_mfma_f32_16x16x32_bf16 a[48:51], a[224:227], a[220:223], a[48:51]\n" \
"s_cmpk_lt_u32 s3, 3\n" \
"s_cselect_b32 s17, s17, s18\n" \
"v_mfma_f32_16x16x32_bf16 a[64:67], a[164:167], a[140:143], a[64:67]\n" \
"s_mov_b32 s18, 0x8000\n" \
"v_writelane_b32 v11, s17, 18\n" \
"v_mfma_f32_16x16x32_bf16 a[64:67], a[228:231], a[204:207], a[64:67]\n" \
"s_mul_i32 s17, s16, 0x2400\n" \
"s_mul_i32 s16, s16, 0x9000\n" \
"v_mfma_f32_16x16x32_bf16 a[80:83], a[172:175], a[140:143], a[80:83]\n" \
"s_bitset1_b32 s17, 17\n" \
"v_writelane_b32 v11, s17, 19\n" \
"v_mfma_f32_16x16x32_bf16 a[80:83], a[236:239], a[204:207], a[80:83]\n" \
"s_add_i32 s16, s11, s16\n" \
"v_writelane_b32 v11, s16, 26\n" \
"v_mfma_f32_16x16x32_bf16 a[68:71], a[140:143], a[168:171], a[68:71]\n" \
"s_mul_i32 s16, s3, 0x2400\n" \
"s_add_i32 s17, s16, 0xa400\n" \
"v_mfma_f32_16x16x32_bf16 a[68:71], a[204:207], a[232:235], a[68:71]\n" \
"s_waitcnt vmcnt(4) lgkmcnt(0)\n" \
"s_barrier\n" \
"v_mfma_f32_16x16x32_bf16 v[116:119], a[176:179], a[144:147], v[116:119]\n" \
"s_add_u32 m0, s40, 0x0\n" \
"s_nop 0\n" \
"s_add_u32 s85, s39, 0x8000\n" \
"v_mfma_f32_16x16x32_bf16 v[116:119], a[240:243], a[208:211], v[116:119]\n" \
"buffer_load_dwordx4 v20, s[80:83], s85 offen sc1 lds\n" \
"v_mfma_f32_16x16x32_bf16 v[120:123], a[180:183], a[144:147], v[120:123]\n" \
"ds_read_b64_tr_b16 a[160:161], v32 offset:32768\n" \
"v_mfma_f32_16x16x32_bf16 v[120:123], a[244:247], a[208:211], v[120:123]\n" \
"ds_read_b64_tr_b16 a[162:163], v32 offset:33280\n" \
"v_mfma_f32_16x16x32_bf16 v[124:127], a[184:187], a[144:147], v[124:127]\n" \
"s_add_u32 m0, s40, 0x1000\n" \
"s_add_u32 s85, s39, 0x9000\n" \
"v_mfma_f32_16x16x32_bf16 v[124:127], a[248:251], a[208:211], v[124:127]\n" \
"buffer_load_dwordx4 v20, s[80:83], s85 offen sc1 lds\n" \
"v_mfma_f32_16x16x32_bf16 v[128:131], a[188:191], a[144:147], v[128:131]\n" \
"ds_read_b64_tr_b16 a[224:225], v32 offset:36864\n" \
"v_mfma_f32_16x16x32_bf16 v[128:131], a[252:255], a[208:211], v[128:131]\n" \
"ds_read_b128 a[144:147], v28 offset:40960\n" \
"v_mfma_f32_16x16x32_bf16 v[144:147], a[188:191], a[148:151], v[144:147]\n" \
"ds_read_b128 a[208:211], v29 offset:40960\n" \
"v_mfma_f32_16x16x32_bf16 v[144:147], a[252:255], a[212:215], v[144:147]\n" \
".fill (L_pb + 65248 - .) / 4, 4, 0xBF800000\n" \
"L_pp16:\n" \
"s_cbranch_execz L_pret\n" \
"s_add_u32 m0, s40, 0x3000\n" \
"s_movk_i32 s84, 0x200\n" \
"v_mfma_f32_16x16x32_bf16 v[140:143], a[184:187], a[148:151], v[140:143]\n" \
"buffer_load_dwordx4 v21, s[76:79], s84 offen offset:0 lds\n" \
"v_mfma_f32_16x16x32_bf16 v[140:143], a[248:251], a[212:215], v[140:143]\n" \
"ds_read_b64_tr_b16 a[226:227], v32 offset:37376\n" \
"v_mfma_f32_16x16x32_bf16 v[136:139], a[180:183], a[148:151], v[136:139]\n" \
"s_add_u32 m0, s30, 0x0\n" \
"s_movk_i32 s84, 0x200\n" \
"v_mfma_f32_16x16x32_bf16 v[136:139], a[244:247], a[212:215], v[136:139]\n" \
"buffer_load_dwordx4 v24, s[76:79], s84 offen offset:0 lds\n" \
"v_mfma_f32_16x16x32_bf16 v[132:135], a[176:179], a[148:151], v[132:135]\n" \
"ds_read_b64_tr_b16 a[164:165], v33 offset:32768\n" \
"v_mfma_f32_16x16x32_bf16 v[132:135], a[240:243], a[212:215], v[132:135]\n" \
"ds_read_b128 a[148:151], v28 offset:43008\n" \
"v_mfma_f32_16x16x32_bf16 v[148:151], a[176:179], a[152:155], v[148:151]\n" \
"ds_read_b128 a[212:215], v29 offset:43008\n" \
"v_mfma_f32_16x16x32_bf16 v[148:151], a[240:243], a[216:219], v[148:151]\n" \
"s_movk_i32 s84, 0x2200\n" \
"v_mfma_f32_16x16x32_bf16 v[152:155], a[180:183], a[152:155], v[152:155]\n" \
"buffer_load_dwordx4 v25, s[76:79], s84 offen offset:1024 lds\n" \
"v_mfma_f32_16x16x32_bf16 v[152:155], a[244:247], a[216:219], v[152:155]\n" \
"ds_read_b64_tr_b16 a[166:167], v33 offset:33280\n" \
"v_mfma_f32_16x16x32_bf16 v[156:159], a[184:187], a[152:155], v[156:159]\n" \
"ds_read_b64_tr_b16 a[228:229], v33 offset:36864\n" \
"v_mfma_f32_16x16x32_bf16 v[156:159], a[248:251], a[216:219], v[156:159]\n" \
"s_movk_i32 s84, 0x4200\n" \
"v_mfma_f32_16x16x32_bf16 v[160:163], a[188:191], a[152:155], v[160:163]\n" \
"buffer_load_dwordx4 v24, s[76:79], s84 offen offset:2048 lds\n" \
"v_mfma_f32_16x16x32_bf16 v[160:163], a[252:255], a[216:219], v[160:163]\n" \
"ds_read_b128 a[152:155], v28 offset:45056\n" \
"v_mfma_f32_16x16x32_bf16 v[176:179], a[188:191], a[156:159], v[176:179]\n" \
"ds_read_b128 a[216:219], v29 offset:45056\n" \
"v_mfma_f32_16x16x32_bf16 v[176:179], a[252:255], a[220:223], v[176:179]\n" \
"s_movk_i32 s84, 0x6200\n" \
"v_mfma_f32_16x16x32_bf16 v[172:175], a[184:187], a[156:159], v[172:175]\n" \
"buffer_load_dwordx4 v25, s[76:79], s84 offen offset:3072 lds\n" \
"v_mfma_f32_16x16x32_bf16 v[172:175], a[248:251], a[220:223], v[172:175]\n" \
"ds_read_b64_tr_b16 a[230:231], v33 offset:37376\n" \
"v_mfma_f32_16x16x32_bf16 v[168:171], a[180:183], a[156:159], v[168:171]\n" \
"ds_read_b64_tr_b16 a[172:173], v35 offset:32768\n" \
"v_mfma_f32_16x16x32_bf16 v[168:171], a[244:247], a[220:223], v[168:171]\n" \
"ds_read_b64_tr_b16 a[174:175], v35 offset:33280\n" \
"v_mfma_f32_16x16x32_bf16 v[164:167], a[176:179], a[156:159], v[164:167]\n" \
"s_cmpk_lt_u32 s3, 3\n" \
"s_cselect_b32 s17, s17, s18\n" \
"v_mfma_f32_16x16x32_bf16 v[164:167], a[240:243], a[220:223], v[164:167]\n" \
"ds_read_b128 a[156:159], v28 offset:47104\n" \
"s_mov_b32 s18, 0x1b000\n" \
"v_mfma_f32_16x16x32_bf16 v[84:87], a[176:179], a[136:139], v[84:87]\n" \
"ds_read_b128 a[220:223], v29 offset:47104\n" \
"s_xor_b32 s19, s3, 2\n" \
"v_mfma_f32_16x16x32_bf16 v[84:87], a[240:243], a[200:203], v[84:87]\n" \
"s_add_u32 m0, s31, 0x0\n" \
"s_mov_b32 s84, 0x200000\n" \
"v_mfma_f32_16x16x32_bf16 v[88:91], a[180:183], a[136:139], v[88:91]\n" \
"buffer_load_dwordx4 v26, s[68:71], s84 offen offset:0 lds\n" \
"v_mfma_f32_16x16x32_bf16 v[88:91], a[244:247], a[200:203], v[88:91]\n" \
"s_addk_i32 s84, 0x7c00\n" \
"v_mfma_f32_16x16x32_bf16 v[92:95], a[184:187], a[136:139], v[92:95]\n" \
"buffer_load_dwordx4 v26, s[68:71], s84 offen offset:1024 lds\n" \
"v_mfma_f32_16x16x32_bf16 v[92:95], a[248:251], a[200:203], v[92:95]\n" \
"ds_read_b64_tr_b16 a[236:237], v35 offset:36864\n" \
"v_mfma_f32_16x16x32_bf16 v[96:99], a[188:191], a[136:139], v[96:99]\n" \
"ds_read_b64_tr_b16 a[238:239], v35 offset:37376\n" \
"v_mfma_f32_16x16x32_bf16 v[96:99], a[252:255], a[200:203], v[96:99]\n" \
"ds_read_b128 a[136:139], v28 offset:36864\n" \
"v_mfma_f32_16x16x32_bf16 v[112:115], a[188:191], a[140:143], v[112:115]\n" \
"ds_read_b128 a[200:203], v29 offset:36864\n" \
"v_mfma_f32_16x16x32_bf16 v[112:115], a[252:255], a[204:207], v[112:115]\n" \
"s_addk_i32 s84, 0x7c00\n" \
"v_mfma_f32_16x16x32_bf16 v[108:111], a[184:187], a[140:143], v[108:111]\n" \
"buffer_load_dwordx4 v27, s[68:71], s84 offen offset:2048 lds\n" \
"v_mfma_f32_16x16x32_bf16 v[108:111], a[248:251], a[204:207], v[108:111]\n" \
"s_addk_i32 s84, 0x7c00\n" \
"v_mfma_f32_16x16x32_bf16 v[104:107], a[180:183], a[140:143], v[104:107]\n" \
"buffer_load_dwordx4 v27, s[68:71], s84 offen offset:3072 lds\n" \
"v_mfma_f32_16x16x32_bf16 v[104:107], a[244:247], a[204:207], v[104:107]\n" \
"ds_read_b64_tr_b16 a[168:169], v34 offset:32768\n" \
"v_mfma_f32_16x16x32_bf16 v[100:103], a[176:179], a[140:143], v[100:103]\n" \
"ds_read_b64_tr_b16 a[170:171], v34 offset:33280\n" \
"v_mfma_f32_16x16x32_bf16 v[100:103], a[240:243], a[204:207], v[100:103]\n" \
"ds_read_b128 a[140:143], v28 offset:38912\n" \
"v_mfma_f32_16x16x32_bf16 v[52:55], a[176:179], a[128:131], v[52:55]\n" \
"ds_read_b128 a[204:207], v29 offset:38912\n" \
"v_mfma_f32_16x16x32_bf16 v[52:55], a[240:243], a[192:195], v[52:55]\n" \
"s_add_u32 m0, s30, 0x1000\n" \
"s_mov_b32 s84, 0x9200\n" \
"v_mfma_f32_16x16x32_bf16 v[56:59], a[180:183], a[128:131], v[56:59]\n" \
"buffer_load_dwordx4 v24, s[76:79], s84 offen offset:0 lds\n" \
"v_mfma_f32_16x16x32_bf16 v[56:59], a[244:247], a[192:195], v[56:59]\n" \
"s_addk_i32 s84, 0x2000\n" \
"s_nop 0\n" \
"v_mfma_f32_16x16x32_bf16 v[60:63], a[184:187], a[128:131], v[60:63]\n" \
"buffer_load_dwordx4 v25, s[76:79], s84 offen offset:1024 lds\n" \
"v_mfma_f32_16x16x32_bf16 v[60:63], a[248:251], a[192:195], v[60:63]\n" \
"ds_read_b64_tr_b16 a[232:233], v34 offset:36864\n" \
"v_mfma_f32_16x16x32_bf16 v[64:67], a[188:191], a[128:131], v[64:67]\n" \
"ds_read_b64_tr_b16 a[234:235], v34 offset:37376\n" \
"v_mfma_f32_16x16x32_bf16 v[64:67], a[252:255], a[192:195], v[64:67]\n" \
"ds_read_b128 a[128:131], v28 offset:32768\n" \
"v_mfma_f32_16x16x32_bf16 v[80:83], a[188:191], a[132:135], v[80:83]\n" \
"ds_read_b128 a[192:195], v29 offset:32768\n" \
"v_mfma_f32_16x16x32_bf16 v[80:83], a[252:255], a[196:199], v[80:83]\n" \
"s_cmp_lg_u32 s19, 0\n" \
"s_cselect_b32 s18, s17, s18\n" \
"v_mfma_f32_16x16x32_bf16 v[76:79], a[184:187], a[132:135], v[76:79]\n" \
"v_writelane_b32 v11, s17, 20\n" \
"s_or_b32 s17, s16, 0x10000\n" \
"v_mfma_f32_16x16x32_bf16 v[76:79], a[248:251], a[196:199], v[76:79]\n" \
"v_writelane_b32 v11, s18, 21\n" \
"s_add_i32 s18, s16, 0x19000\n" \
"v_mfma_f32_16x16x32_bf16 v[72:75], a[180:183], a[132:135], v[72:75]\n" \
"v_writelane_b32 v11, s17, 22\n" \
"s_mov_b32 s17, 0\n" \
"v_mfma_f32_16x16x32_bf16 v[72:75], a[244:247], a[196:199], v[72:75]\n" \
"s_cmpk_lt_u32 s3, 3\n" \
"s_cselect_b32 s18, s18, s17\n" \
"v_mfma_f32_16x16x32_bf16 v[68:71], a[176:179], a[132:135], v[68:71]\n" \
"s_bitset1_b32 s16, 17\n" \
"v_writelane_b32 v11, s18, 23\n" \
"v_mfma_f32_16x16x32_bf16 v[68:71], a[240:243], a[196:199], v[68:71]\n" \
"ds_read_b128 a[132:135], v28 offset:34816\n" \
"ds_read_b128 a[196:199], v29 offset:34816\n" \
"s_waitcnt lgkmcnt(15)\n" \
"v_mfma_f32_16x16x32_bf16 a[0:3], a[160:163], a[144:147], a[0:3]\n" \
"s_addk_i32 s84, 0x2000\n" \
"v_mfma_f32_16x16x32_bf16 a[0:3], a[224:227], a[208:211], a[0:3]\n" \
"buffer_load_dwordx4 v24, s[76:79], s84 offen offset:2048 lds\n" \
"v_mfma_f32_16x16x32_bf16 a[4:7], a[164:167], a[144:147], a[4:7]\n" \
"ds_read_b64_tr_b16 a[176:177], v36 offset:32768\n" \
"s_movk_i32 s18, 0x2400\n" \
"v_mfma_f32_16x16x32_bf16 a[4:7], a[228:231], a[208:211], a[4:7]\n" \
"s_addk_i32 s84, 0x2000\n" \
"v_mfma_f32_16x16x32_bf16 a[12:15], a[172:175], a[144:147], a[12:15]\n" \
"buffer_load_dwordx4 v25, s[76:79], s84 offen offset:3072 lds\n" \
"s_waitcnt lgkmcnt(13)\n" \
"v_mfma_f32_16x16x32_bf16 a[12:15], a[236:239], a[208:211], a[12:15]\n" \
"ds_read_b64_tr_b16 a[178:179], v36 offset:33792\n" \
"s_waitcnt lgkmcnt(10)\n" \
"v_mfma_f32_16x16x32_bf16 a[8:11], a[144:147], a[168:171], a[8:11]\n" \
"ds_read_b64_tr_b16 a[240:241], v36 offset:40960\n" \
"s_waitcnt lgkmcnt(7)\n" \
"v_mfma_f32_16x16x32_bf16 a[8:11], a[208:211], a[232:235], a[8:11]\n" \
"ds_read_b64_tr_b16 a[242:243], v36 offset:41984\n" \
"v_mfma_f32_16x16x32_bf16 a[24:27], a[148:151], a[168:171], a[24:27]\n" \
"ds_read_b64_tr_b16 a[180:181], v37 offset:32768\n" \
"v_mfma_f32_16x16x32_bf16 a[24:27], a[212:215], a[232:235], a[24:27]\n" \
"ds_read_b64_tr_b16 a[182:183], v37 offset:33792\n" \
"v_mfma_f32_16x16x32_bf16 a[28:31], a[172:175], a[148:151], a[28:31]\n" \
"ds_read_b64_tr_b16 a[244:245], v37 offset:40960\n" \
"v_mfma_f32_16x16x32_bf16 a[28:31], a[236:239], a[212:215], a[28:31]\n" \
"s_add_u32 s89, s42, 0x0\n" \
"buffer_store_dwordx4 v[216:219], v20, s[80:83], s89 offen offset:1024 sc1\n" \
"v_mfma_f32_16x16x32_bf16 a[20:23], a[164:167], a[148:151], a[20:23]\n" \
"ds_read_b64_tr_b16 a[246:247], v37 offset:41984\n" \
"v_mfma_f32_16x16x32_bf16 a[20:23], a[228:231], a[212:215], a[20:23]\n" \
"ds_read_b64_tr_b16 a[184:185], v38 offset:32768\n" \
"v_mfma_f32_16x16x32_bf16 a[16:19], a[160:163], a[148:151], a[16:19]\n" \
"ds_read_b64_tr_b16 a[186:187], v38 offset:33792\n" \
"v_mfma_f32_16x16x32_bf16 a[16:19], a[224:227], a[212:215], a[16:19]\n" \
"s_cmpk_lt_u32 s3, 3\n" \
"s_cselect_b32 s16, s16, s18\n" \
"v_mfma_f32_16x16x32_bf16 a[32:35], a[160:163], a[152:155], a[32:35]\n" \
"s_add_u32 s88, s42, 0x0\n" \
"buffer_store_dwordx4 v[220:223], v20, s[80:83], s88 offen offset:0 sc1\n" \
"v_mfma_f32_16x16x32_bf16 a[32:35], a[224:227], a[216:219], a[32:35]\n" \
"ds_read_b64_tr_b16 a[248:249], v38 offset:40960\n" \
"s_mul_i32 s19, s3, 0x9000\n" \
"v_mfma_f32_16x16x32_bf16 a[36:39], a[164:167], a[152:155], a[36:39]\n" \
"ds_read_b64_tr_b16 a[250:251], v38 offset:41984\n" \
"v_writelane_b32 v11, s16, 24\n" \
"v_mfma_f32_16x16x32_bf16 a[36:39], a[228:231], a[216:219], a[36:39]\n" \
"ds_read_b64_tr_b16 a[188:189], v39 offset:32768\n" \
"s_lshl_b32 s16, s3, 10\n" \
"v_mfma_f32_16x16x32_bf16 a[44:47], a[172:175], a[152:155], a[44:47]\n" \
"s_addk_i32 s16, 0x4800\n" \
"v_writelane_b32 v11, s16, 25\n" \
"v_mfma_f32_16x16x32_bf16 a[44:47], a[236:239], a[216:219], a[44:47]\n" \
"ds_read_b64_tr_b16 a[190:191], v39 offset:33792\n" \
"s_add_i32 s16, s15, 1\n" \
"v_mfma_f32_16x16x32_bf16 a[40:43], a[152:155], a[168:171], a[40:43]\n" \
"s_add_u32 s89, s42, 0x1000\n" \
"buffer_store_dwordx4 v[224:227], v20, s[80:83], s89 offen offset:3072 sc1\n" \
"v_mfma_f32_16x16x32_bf16 a[40:43], a[216:219], a[232:235], a[40:43]\n" \
"ds_read_b64_tr_b16 a[252:253], v39 offset:40960\n" \
"s_and_b32 s16, s16, 3\n" \
"v_mfma_f32_16x16x32_bf16 a[56:59], a[156:159], a[168:171], a[56:59]\n" \
"ds_read_b64_tr_b16 a[254:255], v39 offset:41984\n" \
"s_or_b32 s16, s16, s5\n" \
"v_mfma_f32_16x16x32_bf16 a[56:59], a[220:223], a[232:235], a[56:59]\n" \
"s_mul_i32 s18, s16, 0x24000\n" \
"s_add_i32 s18, s18, s19\n" \
"v_mfma_f32_16x16x32_bf16 a[60:63], a[172:175], a[156:159], a[60:63]\n" \
"s_xor_b32 s13, s13, 2\n" \
"s_add_i32 s15, s15, -1\n" \
"v_mfma_f32_16x16x32_bf16 a[60:63], a[236:239], a[220:223], a[60:63]\n" \
"s_add_u32 s88, s42, 0x1000\n" \
"buffer_store_dwordx4 v[228:231], v20, s[80:83], s88 offen offset:2048 sc1\n" \
"v_mfma_f32_16x16x32_bf16 a[52:55], a[164:167], a[156:159], a[52:55]\n" \
"v_writelane_b32 v11, s18, 27\n" \
"s_mul_i32 s18, s13, 0x24000\n" \
"v_mfma_f32_16x16x32_bf16 a[52:55], a[228:231], a[220:223], a[52:55]\n" \
"s_and_b32 s15, s15, 3\n" \
"s_add_i32 s18, s18, s19\n" \
"v_mfma_f32_16x16x32_bf16 a[48:51], a[160:163], a[156:159], a[48:51]\n" \
"s_or_b32 s15, s15, s5\n" \
"v_writelane_b32 v11, s18, 28\n" \
"v_mfma_f32_16x16x32_bf16 a[48:51], a[224:227], a[220:223], a[48:51]\n" \
"s_mul_i32 s18, s15, 0x24000\n" \
"s_add_i32 s18, s18, s19\n" \
"v_mfma_f32_16x16x32_bf16 a[64:67], a[164:167], a[140:143], a[64:67]\n" \
"v_writelane_b32 v11, s18, 29\n" \
"s_or_b32 s18, s9, s14\n" \
"v_mfma_f32_16x16x32_bf16 a[64:67], a[228:231], a[204:207], a[64:67]\n" \
"s_mul_i32 s18, s18, 0x24800\n" \
"s_lshl_b32 s16, s16, 12\n" \
"v_mfma_f32_16x16x32_bf16 a[80:83], a[172:175], a[140:143], a[80:83]\n" \
"s_add_i32 s19, s18, 0x3004800\n" \
"s_add_i32 s20, s19, s16\n" \
"v_mfma_f32_16x16x32_bf16 a[80:83], a[236:239], a[204:207], a[80:83]\n" \
"v_writelane_b32 v11, s20, 30\n" \
"s_lshl_b32 s13, s13, 12\n" \
"v_mfma_f32_16x16x32_bf16 a[68:71], a[140:143], a[168:171], a[68:71]\n" \
"s_lshl_b32 s15, s15, 12\n" \
"s_sub_i32 s14, s9, s14\n" \
"v_mfma_f32_16x16x32_bf16 a[68:71], a[204:207], a[232:235], a[68:71]\n" \
"s_waitcnt vmcnt(4) lgkmcnt(0)\n" \
"s_barrier\n" \
"v_mov_b32_e32 v45, s62\n" \
"s_mov_b64 exec, 1\n" \
"buffer_store_dword v45, off, s[80:83], s37 offset:0 sc1\n" \
"s_mov_b64 exec, -1\n" \
"v_mfma_f32_16x16x32_bf16 v[116:119], a[176:179], a[144:147], v[116:119]\n" \
"s_add_u32 m0, s40, 0x8000\n" \
"s_add_u32 s85, s39, 0xa000\n" \
"v_mfma_f32_16x16x32_bf16 v[116:119], a[240:243], a[208:211], v[116:119]\n" \
"buffer_load_dwordx4 v20, s[80:83], s85 offen sc1 lds\n" \
"v_mfma_f32_16x16x32_bf16 v[120:123], a[180:183], a[144:147], v[120:123]\n" \
"ds_read_b64_tr_b16 a[160:161], v32 offset:0\n" \
"v_accvgpr_read_b32 v212, a108\n" \
"v_mfma_f32_16x16x32_bf16 v[120:123], a[244:247], a[208:211], v[120:123]\n" \
"ds_read_b64_tr_b16 a[162:163], v32 offset:512\n" \
"v_accvgpr_read_b32 v213, a109\n" \
"v_mfma_f32_16x16x32_bf16 v[124:127], a[184:187], a[144:147], v[124:127]\n" \
"s_add_u32 m0, s40, 0x9000\n" \
"s_add_u32 s85, s39, 0xb000\n" \
"v_mfma_f32_16x16x32_bf16 v[124:127], a[248:251], a[208:211], v[124:127]\n" \
"buffer_load_dwordx4 v20, s[80:83], s85 offen sc1 lds\n" \
"v_mfma_f32_16x16x32_bf16 v[128:131], a[188:191], a[144:147], v[128:131]\n" \
"ds_read_b64_tr_b16 a[224:225], v32 offset:4096\n" \
"v_accvgpr_read_b32 v214, a110\n" \
"v_mfma_f32_16x16x32_bf16 v[128:131], a[252:255], a[208:211], v[128:131]\n" \
"ds_read_b128 a[144:147], v28 offset:8192\n" \
"v_accvgpr_read_b32 v215, a111\n" \
"v_mfma_f32_16x16x32_bf16 v[144:147], a[188:191], a[148:151], v[144:147]\n" \
"ds_read_b128 a[208:211], v29 offset:8192\n" \
"v_accvgpr_read_b32 v180, a124\n" \
"v_mfma_f32_16x16x32_bf16 v[144:147], a[252:255], a[212:215], v[144:147]\n" \
"s_add_u32 m0, s40, 0xb000\n" \
"s_movk_i32 s84, 0x280\n" \
"v_mfma_f32_16x16x32_bf16 v[140:143], a[184:187], a[148:151], v[140:143]\n" \
"buffer_load_dwordx4 v21, s[76:79], s84 offen offset:0 lds\n" \
"v_mfma_f32_16x16x32_bf16 v[140:143], a[248:251], a[212:215], v[140:143]\n" \
"ds_read_b64_tr_b16 a[226:227], v32 offset:4608\n" \
"v_accvgpr_read_b32 v181, a125\n" \
"v_mfma_f32_16x16x32_bf16 v[136:139], a[180:183], a[148:151], v[136:139]\n" \
"s_add_u32 m0, s30, 0x8000\n" \
"s_movk_i32 s84, 0x280\n" \
"v_mfma_f32_16x16x32_bf16 v[136:139], a[244:247], a[212:215], v[136:139]\n" \
"buffer_load_dwordx4 v24, s[76:79], s84 offen offset:0 lds\n" \
"v_mfma_f32_16x16x32_bf16 v[132:135], a[176:179], a[148:151], v[132:135]\n" \
"ds_read_b64_tr_b16 a[164:165], v33 offset:0\n" \
"v_accvgpr_read_b32 v182, a126\n" \
"v_mfma_f32_16x16x32_bf16 v[132:135], a[240:243], a[212:215], v[132:135]\n" \
"ds_read_b128 a[148:151], v28 offset:10240\n" \
"v_accvgpr_read_b32 v183, a127\n" \
"v_mfma_f32_16x16x32_bf16 v[148:151], a[176:179], a[152:155], v[148:151]\n" \
"ds_read_b128 a[212:215], v29 offset:10240\n" \
"v_cvt_pk_bf16_f32 v212, v212, v213\n" \
"v_mfma_f32_16x16x32_bf16 v[148:151], a[240:243], a[216:219], v[148:151]\n" \
"s_movk_i32 s84, 0x2280\n" \
"v_mfma_f32_16x16x32_bf16 v[152:155], a[180:183], a[152:155], v[152:155]\n" \
"buffer_load_dwordx4 v25, s[76:79], s84 offen offset:1024 lds\n" \
"v_mfma_f32_16x16x32_bf16 v[152:155], a[244:247], a[216:219], v[152:155]\n" \
"ds_read_b64_tr_b16 a[166:167], v33 offset:512\n" \
"v_cvt_pk_bf16_f32 v213, v214, v215\n" \
"v_mfma_f32_16x16x32_bf16 v[156:159], a[184:187], a[152:155], v[156:159]\n" \
"ds_read_b64_tr_b16 a[228:229], v33 offset:4096\n" \
"v_cvt_pk_bf16_f32 v214, v180, v181\n" \
"v_mfma_f32_16x16x32_bf16 v[156:159], a[248:251], a[216:219], v[156:159]\n" \
"s_movk_i32 s84, 0x4280\n" \
"v_mfma_f32_16x16x32_bf16 v[160:163], a[188:191], a[152:155], v[160:163]\n" \
"buffer_load_dwordx4 v24, s[76:79], s84 offen offset:2048 lds\n" \
"v_mfma_f32_16x16x32_bf16 v[160:163], a[252:255], a[216:219], v[160:163]\n" \
"ds_read_b128 a[152:155], v28 offset:12288\n" \
"v_cvt_pk_bf16_f32 v215, v182, v183\n" \
"v_mfma_f32_16x16x32_bf16 v[176:179], a[188:191], a[156:159], v[176:179]\n" \
"ds_read_b128 a[216:219], v29 offset:12288\n" \
"v_accvgpr_read_b32 v208, a104\n" \
"v_mfma_f32_16x16x32_bf16 v[176:179], a[252:255], a[220:223], v[176:179]\n" \
"s_movk_i32 s84, 0x6280\n" \
"s_nop 0\n" \
"v_mfma_f32_16x16x32_bf16 v[172:175], a[184:187], a[156:159], v[172:175]\n" \
"buffer_load_dwordx4 v25, s[76:79], s84 offen offset:3072 lds\n" \
"v_mfma_f32_16x16x32_bf16 v[172:175], a[248:251], a[220:223], v[172:175]\n" \
"ds_read_b64_tr_b16 a[230:231], v33 offset:4608\n" \
"v_accvgpr_read_b32 v209, a105\n" \
"v_mfma_f32_16x16x32_bf16 v[168:171], a[180:183], a[156:159], v[168:171]\n" \
"ds_read_b64_tr_b16 a[172:173], v35 offset:0\n" \
"v_accvgpr_read_b32 v210, a106\n" \
"v_mfma_f32_16x16x32_bf16 v[168:171], a[244:247], a[220:223], v[168:171]\n" \
"ds_read_b64_tr_b16 a[174:175], v35 offset:512\n" \
"v_accvgpr_read_b32 v211, a107\n" \
"v_mfma_f32_16x16x32_bf16 v[164:167], a[176:179], a[156:159], v[164:167]\n" \
"v_accvgpr_read_b32 v180, a120\n" \
"v_accvgpr_read_b32 v181, a121\n" \
"v_mfma_f32_16x16x32_bf16 v[164:167], a[240:243], a[220:223], v[164:167]\n" \
"ds_read_b128 a[156:159], v28 offset:14336\n" \
"v_accvgpr_read_b32 v182, a122\n" \
"v_mfma_f32_16x16x32_bf16 v[84:87], a[176:179], a[136:139], v[84:87]\n" \
"ds_read_b128 a[220:223], v29 offset:14336\n" \
"v_accvgpr_read_b32 v183, a123\n" \
"v_mfma_f32_16x16x32_bf16 v[84:87], a[240:243], a[200:203], v[84:87]\n" \
"s_add_u32 m0, s31, 0x8000\n" \
"s_mov_b32 s84, 0x280000\n" \
"v_mfma_f32_16x16x32_bf16 v[88:91], a[180:183], a[136:139], v[88:91]\n" \
"buffer_load_dwordx4 v26, s[68:71], s84 offen offset:0 lds\n" \
"v_mfma_f32_16x16x32_bf16 v[88:91], a[244:247], a[200:203], v[88:91]\n" \
"s_addk_i32 s84, 0x7c00\n" \
"s_nop 0\n" \
"v_mfma_f32_16x16x32_bf16 v[92:95], a[184:187], a[136:139], v[92:95]\n" \
"buffer_load_dwordx4 v26, s[68:71], s84 offen offset:1024 lds\n" \
"v_mfma_f32_16x16x32_bf16 v[92:95], a[248:251], a[200:203], v[92:95]\n" \
"ds_read_b64_tr_b16 a[236:237], v35 offset:4096\n" \
"v_cvt_pk_bf16_f32 v208, v208, v209\n" \
"v_mfma_f32_16x16x32_bf16 v[96:99], a[188:191], a[136:139], v[96:99]\n" \
"ds_read_b64_tr_b16 a[238:239], v35 offset:4608\n" \
"v_cvt_pk_bf16_f32 v209, v210, v211\n" \
"v_mfma_f32_16x16x32_bf16 v[96:99], a[252:255], a[200:203], v[96:99]\n" \
"ds_read_b128 a[136:139], v28 offset:4096\n" \
"v_cvt_pk_bf16_f32 v210, v180, v181\n" \
"v_mfma_f32_16x16x32_bf16 v[112:115], a[188:191], a[140:143], v[112:115]\n" \
"ds_read_b128 a[200:203], v29 offset:4096\n" \
"v_cvt_pk_bf16_f32 v211, v182, v183\n" \
"v_mfma_f32_16x16x32_bf16 v[112:115], a[252:255], a[204:207], v[112:115]\n" \
"s_addk_i32 s84, 0x7c00\n" \
"v_mfma_f32_16x16x32_bf16 v[108:111], a[184:187], a[140:143], v[108:111]\n" \
"buffer_load_dwordx4 v27, s[68:71], s84 offen offset:2048 lds\n" \
"v_mfma_f32_16x16x32_bf16 v[108:111], a[248:251], a[204:207], v[108:111]\n" \
"s_addk_i32 s84, 0x7c00\n" \
"v_mfma_f32_16x16x32_bf16 v[104:107], a[180:183], a[140:143], v[104:107]\n" \
"buffer_load_dwordx4 v27, s[68:71], s84 offen offset:3072 lds\n" \
"v_mfma_f32_16x16x32_bf16 v[104:107], a[244:247], a[204:207], v[104:107]\n" \
"ds_read_b64_tr_b16 a[168:169], v34 offset:0\n" \
"v_accvgpr_read_b32 v204, a100\n" \
"v_mfma_f32_16x16x32_bf16 v[100:103], a[176:179], a[140:143], v[100:103]\n" \
"ds_read_b64_tr_b16 a[170:171], v34 offset:512\n" \
"v_accvgpr_read_b32 v205, a101\n" \
"v_mfma_f32_16x16x32_bf16 v[100:103], a[240:243], a[204:207], v[100:103]\n" \
"ds_read_b128 a[140:143], v28 offset:6144\n" \
"v_accvgpr_read_b32 v206, a102\n" \
"v_mfma_f32_16x16x32_bf16 v[52:55], a[176:179], a[128:131], v[52:55]\n" \
"ds_read_b128 a[204:207], v29 offset:6144\n" \
"v_accvgpr_read_b32 v207, a103\n" \
"v_mfma_f32_16x16x32_bf16 v[52:55], a[240:243], a[192:195], v[52:55]\n" \
"s_add_u32 m0, s30, 0x9000\n" \
"s_mov_b32 s84, 0x9280\n" \
"v_mfma_f32_16x16x32_bf16 v[56:59], a[180:183], a[128:131], v[56:59]\n" \
"buffer_load_dwordx4 v24, s[76:79], s84 offen offset:0 lds\n" \
"v_mfma_f32_16x16x32_bf16 v[56:59], a[244:247], a[192:195], v[56:59]\n" \
"s_addk_i32 s84, 0x2000\n" \
"s_nop 0\n" \
"v_mfma_f32_16x16x32_bf16 v[60:63], a[184:187], a[128:131], v[60:63]\n" \
"buffer_load_dwordx4 v25, s[76:79], s84 offen offset:1024 lds\n" \
"v_mfma_f32_16x16x32_bf16 v[60:63], a[248:251], a[192:195], v[60:63]\n" \
"ds_read_b64_tr_b16 a[232:233], v34 offset:4096\n" \
"v_accvgpr_read_b32 v180, a116\n" \
"v_mfma_f32_16x16x32_bf16 v[64:67], a[188:191], a[128:131], v[64:67]\n" \
"ds_read_b64_tr_b16 a[234:235], v34 offset:4608\n" \
"v_accvgpr_read_b32 v181, a117\n" \
"v_mfma_f32_16x16x32_bf16 v[64:67], a[252:255], a[192:195], v[64:67]\n" \
"ds_read_b128 a[128:131], v28 offset:0\n" \
"v_accvgpr_read_b32 v182, a118\n" \
"v_mfma_f32_16x16x32_bf16 v[80:83], a[188:191], a[132:135], v[80:83]\n" \
"ds_read_b128 a[192:195], v29 offset:0\n" \
"v_accvgpr_read_b32 v183, a119\n" \
"v_mfma_f32_16x16x32_bf16 v[80:83], a[252:255], a[196:199], v[80:83]\n" \
"v_cvt_pk_bf16_f32 v204, v204, v205\n" \
"v_cvt_pk_bf16_f32 v205, v206, v207\n" \
"v_mfma_f32_16x16x32_bf16 v[76:79], a[184:187], a[132:135], v[76:79]\n" \
"v_cvt_pk_bf16_f32 v206, v180, v181\n" \
"v_cvt_pk_bf16_f32 v207, v182, v183\n" \
"v_mfma_f32_16x16x32_bf16 v[76:79], a[248:251], a[196:199], v[76:79]\n" \
"v_accvgpr_read_b32 v200, a96\n" \
"v_accvgpr_read_b32 v201, a97\n" \
"v_mfma_f32_16x16x32_bf16 v[72:75], a[180:183], a[132:135], v[72:75]\n" \
"v_accvgpr_read_b32 v202, a98\n" \
"v_accvgpr_read_b32 v203, a99\n" \
"v_mfma_f32_16x16x32_bf16 v[72:75], a[244:247], a[196:199], v[72:75]\n" \
"v_accvgpr_read_b32 v180, a112\n" \
"v_accvgpr_read_b32 v181, a113\n" \
"v_mfma_f32_16x16x32_bf16 v[68:71], a[176:179], a[132:135], v[68:71]\n" \
"v_accvgpr_read_b32 v182, a114\n" \
"v_accvgpr_read_b32 v183, a115\n" \
"v_mfma_f32_16x16x32_bf16 v[68:71], a[240:243], a[196:199], v[68:71]\n" \
"ds_read_b128 a[132:135], v28 offset:2048\n" \
"ds_read_b128 a[196:199], v29 offset:2048\n" \
"s_waitcnt lgkmcnt(15)\n" \
"v_mfma_f32_16x16x32_bf16 a[0:3], a[160:163], a[144:147], a[0:3]\n" \
"s_addk_i32 s84, 0x2000\n" \
"v_mfma_f32_16x16x32_bf16 a[0:3], a[224:227], a[208:211], a[0:3]\n" \
"buffer_load_dwordx4 v24, s[76:79], s84 offen offset:2048 lds\n" \
"v_mfma_f32_16x16x32_bf16 a[4:7], a[164:167], a[144:147], a[4:7]\n" \
"ds_read_b64_tr_b16 a[176:177], v36 offset:0\n" \
"v_cvt_pk_bf16_f32 v200, v200, v201\n" \
"v_mfma_f32_16x16x32_bf16 a[4:7], a[228:231], a[208:211], a[4:7]\n" \
"s_addk_i32 s84, 0x2000\n" \
"v_mfma_f32_16x16x32_bf16 a[12:15], a[172:175], a[144:147], a[12:15]\n" \
"buffer_load_dwordx4 v25, s[76:79], s84 offen offset:3072 lds\n" \
"s_waitcnt lgkmcnt(13)\n" \
"v_mfma_f32_16x16x32_bf16 a[12:15], a[236:239], a[208:211], a[12:15]\n" \
"ds_read_b64_tr_b16 a[178:179], v36 offset:1024\n" \
"v_cvt_pk_bf16_f32 v201, v202, v203\n" \
"s_waitcnt lgkmcnt(10)\n" \
"v_mfma_f32_16x16x32_bf16 a[8:11], a[144:147], a[168:171], a[8:11]\n" \
"ds_read_b64_tr_b16 a[240:241], v36 offset:8192\n" \
"v_cvt_pk_bf16_f32 v202, v180, v181\n" \
"s_waitcnt lgkmcnt(7)\n" \
"v_mfma_f32_16x16x32_bf16 a[8:11], a[208:211], a[232:235], a[8:11]\n" \
"ds_read_b64_tr_b16 a[242:243], v36 offset:9216\n" \
"v_cvt_pk_bf16_f32 v203, v182, v183\n" \
"v_mfma_f32_16x16x32_bf16 a[24:27], a[148:151], a[168:171], a[24:27]\n" \
"ds_read_b64_tr_b16 a[180:181], v37 offset:0\n" \
"v_accvgpr_read_b32 v196, a76\n" \
"v_mfma_f32_16x16x32_bf16 a[24:27], a[212:215], a[232:235], a[24:27]\n" \
"ds_read_b64_tr_b16 a[182:183], v37 offset:1024\n" \
"v_accvgpr_read_b32 v197, a77\n" \
"v_mfma_f32_16x16x32_bf16 a[28:31], a[172:175], a[148:151], a[28:31]\n" \
"ds_read_b64_tr_b16 a[244:245], v37 offset:8192\n" \
"v_accvgpr_read_b32 v198, a78\n" \
"v_mfma_f32_16x16x32_bf16 a[28:31], a[236:239], a[212:215], a[28:31]\n" \
"ds_read_b64_tr_b16 a[246:247], v37 offset:9216\n" \
"v_accvgpr_read_b32 v199, a79\n" \
"v_mfma_f32_16x16x32_bf16 a[20:23], a[164:167], a[148:151], a[20:23]\n" \
"v_accvgpr_read_b32 v180, a92\n" \
"v_accvgpr_read_b32 v181, a93\n" \
"v_mfma_f32_16x16x32_bf16 a[20:23], a[228:231], a[212:215], a[20:23]\n" \
"s_add_u32 s89, s42, 0x1000\n" \
"buffer_store_dwordx4 v[232:235], v20, s[80:83], s89 offen offset:1024 sc1\n" \
"v_mfma_f32_16x16x32_bf16 a[16:19], a[160:163], a[148:151], a[16:19]\n" \
"ds_read_b64_tr_b16 a[184:185], v38 offset:0\n" \
"v_accvgpr_read_b32 v182, a94\n" \
"v_mfma_f32_16x16x32_bf16 a[16:19], a[224:227], a[212:215], a[16:19]\n" \
"ds_read_b64_tr_b16 a[186:187], v38 offset:1024\n" \
"v_accvgpr_read_b32 v183, a95\n" \
"v_mfma_f32_16x16x32_bf16 a[32:35], a[160:163], a[152:155], a[32:35]\n" \
"ds_read_b64_tr_b16 a[248:249], v38 offset:8192\n" \
"v_cvt_pk_bf16_f32 v196, v196, v197\n" \
"v_mfma_f32_16x16x32_bf16 a[32:35], a[224:227], a[216:219], a[32:35]\n" \
"ds_read_b64_tr_b16 a[250:251], v38 offset:9216\n" \
"v_cvt_pk_bf16_f32 v197, v198, v199\n" \
"v_mfma_f32_16x16x32_bf16 a[36:39], a[164:167], a[152:155], a[36:39]\n" \
"v_cvt_pk_bf16_f32 v198, v180, v181\n" \
"v_cvt_pk_bf16_f32 v199, v182, v183\n" \
"v_mfma_f32_16x16x32_bf16 a[36:39], a[228:231], a[216:219], a[36:39]\n" \
"s_add_u32 s88, s42, 0x1000\n" \
"buffer_store_dwordx4 v[236:239], v20, s[80:83], s88 offen offset:0 sc1\n" \
"v_mfma_f32_16x16x32_bf16 a[44:47], a[172:175], a[152:155], a[44:47]\n" \
"ds_read_b64_tr_b16 a[188:189], v39 offset:0\n" \
"v_accvgpr_read_b32 v192, a72\n" \
"v_mfma_f32_16x16x32_bf16 a[44:47], a[236:239], a[216:219], a[44:47]\n" \
"ds_read_b64_tr_b16 a[190:191], v39 offset:1024\n" \
"v_accvgpr_read_b32 v193, a73\n" \
"v_mfma_f32_16x16x32_bf16 a[40:43], a[152:155], a[168:171], a[40:43]\n" \
"ds_read_b64_tr_b16 a[252:253], v39 offset:8192\n" \
"v_accvgpr_read_b32 v194, a74\n" \
"v_mfma_f32_16x16x32_bf16 a[40:43], a[216:219], a[232:235], a[40:43]\n" \
"v_accvgpr_read_b32 v195, a75\n" \
"v_accvgpr_read_b32 v180, a88\n" \
"v_mfma_f32_16x16x32_bf16 a[56:59], a[156:159], a[168:171], a[56:59]\n" \
"ds_read_b64_tr_b16 a[254:255], v39 offset:9216\n" \
"v_accvgpr_read_b32 v181, a89\n" \
"v_mfma_f32_16x16x32_bf16 a[56:59], a[220:223], a[232:235], a[56:59]\n" \
"v_accvgpr_read_b32 v182, a90\n" \
"v_accvgpr_read_b32 v183, a91\n" \
"v_mfma_f32_16x16x32_bf16 a[60:63], a[172:175], a[156:159], a[60:63]\n" \
"s_add_u32 s89, s42, 0x3000\n" \
"buffer_store_dwordx4 v[240:243], v20, s[80:83], s89 offen offset:3072 sc1\n" \
"v_mfma_f32_16x16x32_bf16 a[60:63], a[236:239], a[220:223], a[60:63]\n" \
"v_cvt_pk_bf16_f32 v192, v192, v193\n" \
"v_cvt_pk_bf16_f32 v193, v194, v195\n" \
"v_mfma_f32_16x16x32_bf16 a[52:55], a[164:167], a[156:159], a[52:55]\n" \
"v_cvt_pk_bf16_f32 v194, v180, v181\n" \
"v_cvt_pk_bf16_f32 v195, v182, v183\n" \
"v_mfma_f32_16x16x32_bf16 a[52:55], a[228:231], a[220:223], a[52:55]\n" \
"s_add_i32 s20, s19, s13\n" \
"v_writelane_b32 v11, s20, 31\n" \
"v_mfma_f32_16x16x32_bf16 a[48:51], a[160:163], a[156:159], a[48:51]\n" \
"s_add_i32 s19, s19, s15\n" \
"s_mul_i32 s14, s14, 0x24800\n" \
"v_mfma_f32_16x16x32_bf16 a[48:51], a[224:227], a[220:223], a[48:51]\n" \
"v_writelane_b32 v11, s19, 32\n" \
"s_add_i32 s19, s14, 0x4120800\n" \
"v_mfma_f32_16x16x32_bf16 a[64:67], a[164:167], a[140:143], a[64:67]\n" \
"s_add_i32 s16, s19, s16\n" \
"v_writelane_b32 v11, s16, 33\n" \
"v_mfma_f32_16x16x32_bf16 a[64:67], a[228:231], a[204:207], a[64:67]\n" \
"s_add_i32 s13, s19, s13\n" \
"v_writelane_b32 v11, s13, 34\n" \
"v_mfma_f32_16x16x32_bf16 a[80:83], a[172:175], a[140:143], a[80:83]\n" \
"s_add_i32 s19, s19, s15\n" \
"s_or_b32 s13, s18, 0x3000000\n" \
"v_mfma_f32_16x16x32_bf16 a[80:83], a[236:239], a[204:207], a[80:83]\n" \
"s_lshl_b32 s15, s6, 11\n" \
"v_writelane_b32 v11, s19, 35\n" \
"v_mfma_f32_16x16x32_bf16 a[68:71], a[140:143], a[168:171], a[68:71]\n" \
"s_add_i32 s16, s13, s15\n" \
"v_writelane_b32 v11, s16, 36\n" \
"v_mfma_f32_16x16x32_bf16 a[68:71], a[204:207], a[232:235], a[68:71]\n" \
"s_waitcnt vmcnt(3) lgkmcnt(0)\n" \
"s_barrier\n" \
"v_mfma_f32_16x16x32_bf16 v[116:119], a[176:179], a[144:147], v[116:119]\n" \
"s_add_u32 m0, s40, 0x0\n" \
"s_add_u32 s85, s39, 0xc000\n" \
"v_mfma_f32_16x16x32_bf16 v[116:119], a[240:243], a[208:211], v[116:119]\n" \
"buffer_load_dwordx4 v20, s[80:83], s85 offen sc1 lds\n" \
"v_mfma_f32_16x16x32_bf16 v[120:123], a[180:183], a[144:147], v[120:123]\n" \
"ds_read_b64_tr_b16 a[160:161], v32 offset:32768\n" \
"s_add_i32 s16, s14, 0x411c000\n" \
"v_mfma_f32_16x16x32_bf16 v[120:123], a[244:247], a[208:211], v[120:123]\n" \
"ds_read_b64_tr_b16 a[162:163], v32 offset:33280\n" \
"s_add_i32 s19, s16, s15\n" \
"s_nop 0\n" \
"v_mfma_f32_16x16x32_bf16 v[124:127], a[184:187], a[144:147], v[124:127]\n" \
"s_add_u32 m0, s40, 0x1000\n" \
"s_add_u32 s85, s39, 0xd000\n" \
"v_mfma_f32_16x16x32_bf16 v[124:127], a[248:251], a[208:211], v[124:127]\n" \
"buffer_load_dwordx4 v20, s[80:83], s85 offen sc1 lds\n" \
"v_mfma_f32_16x16x32_bf16 v[128:131], a[188:191], a[144:147], v[128:131]\n" \
"ds_read_b64_tr_b16 a[224:225], v32 offset:36864\n" \
"v_writelane_b32 v11, s19, 37\n" \
"v_mfma_f32_16x16x32_bf16 v[128:131], a[252:255], a[208:211], v[128:131]\n" \
"ds_read_b128 a[144:147], v28 offset:40960\n" \
"s_add_i32 s18, s18, 0x3004000\n" \
"v_mfma_f32_16x16x32_bf16 v[144:147], a[188:191], a[148:151], v[144:147]\n" \
"ds_read_b128 a[208:211], v29 offset:40960\n" \
"v_writelane_b32 v11, s18, 38\n" \
"v_mfma_f32_16x16x32_bf16 v[144:147], a[252:255], a[212:215], v[144:147]\n" \
"s_add_u32 m0, s40, 0x3000\n" \
"s_movk_i32 s84, 0x300\n" \
"v_mfma_f32_16x16x32_bf16 v[140:143], a[184:187], a[148:151], v[140:143]\n" \
"buffer_load_dwordx4 v21, s[76:79], s84 offen offset:0 lds\n" \
"v_mfma_f32_16x16x32_bf16 v[140:143], a[248:251], a[212:215], v[140:143]\n" \
".fill (L_pb + 69344 - .) / 4, 4, 0xBF800000\n" \
"L_pp17:\n" \
"s_cbranch_execz L_pret\n" \
"ds_read_b64_tr_b16 a[226:227], v32 offset:37376\n" \
"s_add_i32 s14, s14, 0x4120000\n" \
"v_mfma_f32_16x16x32_bf16 v[136:139], a[180:183], a[148:151], v[136:139]\n" \
"s_add_u32 m0, s30, 0x0\n" \
"s_movk_i32 s84, 0x300\n" \
"v_mfma_f32_16x16x32_bf16 v[136:139], a[244:247], a[212:215], v[136:139]\n" \
"buffer_load_dwordx4 v24, s[76:79], s84 offen offset:0 lds\n" \
"v_mfma_f32_16x16x32_bf16 v[132:135], a[176:179], a[148:151], v[132:135]\n" \
"ds_read_b64_tr_b16 a[164:165], v33 offset:32768\n" \
"v_writelane_b32 v11, s14, 39\n" \
"v_mfma_f32_16x16x32_bf16 v[132:135], a[240:243], a[212:215], v[132:135]\n" \
"ds_read_b128 a[148:151], v28 offset:43008\n" \
"s_lshl_b32 s14, s4, 2\n" \
"v_mfma_f32_16x16x32_bf16 v[148:151], a[176:179], a[152:155], v[148:151]\n" \
"ds_read_b128 a[212:215], v29 offset:43008\n" \
"s_or_b32 s8, s14, s8\n" \
"v_mfma_f32_16x16x32_bf16 v[148:151], a[240:243], a[216:219], v[148:151]\n" \
"s_movk_i32 s84, 0x2300\n" \
"v_mfma_f32_16x16x32_bf16 v[152:155], a[180:183], a[152:155], v[152:155]\n" \
"buffer_load_dwordx4 v25, s[76:79], s84 offen offset:1024 lds\n" \
"v_mfma_f32_16x16x32_bf16 v[152:155], a[244:247], a[216:219], v[152:155]\n" \
"ds_read_b64_tr_b16 a[166:167], v33 offset:33280\n" \
"s_mul_i32 s8, s8, 0x90000\n" \
"v_mfma_f32_16x16x32_bf16 v[156:159], a[184:187], a[152:155], v[156:159]\n" \
"ds_read_b64_tr_b16 a[228:229], v33 offset:36864\n" \
"s_lshl_b32 s14, s3, 14\n" \
"v_mfma_f32_16x16x32_bf16 v[156:159], a[248:251], a[216:219], v[156:159]\n" \
"s_movk_i32 s84, 0x4300\n" \
"v_mfma_f32_16x16x32_bf16 v[160:163], a[188:191], a[152:155], v[160:163]\n" \
"buffer_load_dwordx4 v24, s[76:79], s84 offen offset:2048 lds\n" \
"v_mfma_f32_16x16x32_bf16 v[160:163], a[252:255], a[216:219], v[160:163]\n" \
"ds_read_b128 a[152:155], v28 offset:45056\n" \
"s_add_i32 s8, s8, s14\n" \
"v_mfma_f32_16x16x32_bf16 v[176:179], a[188:191], a[156:159], v[176:179]\n" \
"ds_read_b128 a[216:219], v29 offset:45056\n" \
"s_lshl_b32 s9, s9, 4\n" \
"v_mfma_f32_16x16x32_bf16 v[176:179], a[252:255], a[220:223], v[176:179]\n" \
"s_movk_i32 s84, 0x6300\n" \
"v_mfma_f32_16x16x32_bf16 v[172:175], a[184:187], a[156:159], v[172:175]\n" \
"buffer_load_dwordx4 v25, s[76:79], s84 offen offset:3072 lds\n" \
"v_mfma_f32_16x16x32_bf16 v[172:175], a[248:251], a[220:223], v[172:175]\n" \
"ds_read_b64_tr_b16 a[230:231], v33 offset:37376\n" \
"s_lshl_b32 s14, s4, 7\n" \
"v_mfma_f32_16x16x32_bf16 v[168:171], a[180:183], a[156:159], v[168:171]\n" \
"ds_read_b64_tr_b16 a[172:173], v35 offset:32768\n" \
"s_or_b32 s18, s9, s14\n" \
"v_mfma_f32_16x16x32_bf16 v[168:171], a[244:247], a[220:223], v[168:171]\n" \
"ds_read_b64_tr_b16 a[174:175], v35 offset:33280\n" \
"s_sub_i32 s9, s9, s14\n" \
"v_mfma_f32_16x16x32_bf16 v[164:167], a[176:179], a[156:159], v[164:167]\n" \
"s_mulk_i32 s18, 0x480\n" \
"s_mulk_i32 s9, 0x480\n" \
"v_mfma_f32_16x16x32_bf16 v[164:167], a[240:243], a[220:223], v[164:167]\n" \
"ds_read_b128 a[156:159], v28 offset:47104\n" \
"s_or_b32 s14, s18, s12\n" \
"v_mfma_f32_16x16x32_bf16 v[84:87], a[176:179], a[136:139], v[84:87]\n" \
"ds_read_b128 a[220:223], v29 offset:47104\n" \
"s_or_b32 s9, s9, s12\n" \
"v_mfma_f32_16x16x32_bf16 v[84:87], a[240:243], a[200:203], v[84:87]\n" \
"s_add_u32 m0, s31, 0x0\n" \
"s_mov_b32 s84, 0x300000\n" \
"v_mfma_f32_16x16x32_bf16 v[88:91], a[180:183], a[136:139], v[88:91]\n" \
"buffer_load_dwordx4 v26, s[68:71], s84 offen offset:0 lds\n" \
"v_mfma_f32_16x16x32_bf16 v[88:91], a[244:247], a[200:203], v[88:91]\n" \
"s_addk_i32 s84, 0x7c00\n" \
"v_mfma_f32_16x16x32_bf16 v[92:95], a[184:187], a[136:139], v[92:95]\n" \
"buffer_load_dwordx4 v26, s[68:71], s84 offen offset:1024 lds\n" \
"v_mfma_f32_16x16x32_bf16 v[92:95], a[248:251], a[200:203], v[92:95]\n" \
"ds_read_b64_tr_b16 a[236:237], v35 offset:36864\n" \
"s_and_b32 s12, s2, 1\n" \
"v_mfma_f32_16x16x32_bf16 v[96:99], a[188:191], a[136:139], v[96:99]\n" \
"ds_read_b64_tr_b16 a[238:239], v35 offset:37376\n" \
"v_mfma_f32_16x16x32_bf16 v[96:99], a[252:255], a[200:203], v[96:99]\n" \
"ds_read_b128 a[136:139], v28 offset:36864\n" \
"v_mfma_f32_16x16x32_bf16 v[112:115], a[188:191], a[140:143], v[112:115]\n" \
"ds_read_b128 a[200:203], v29 offset:36864\n" \
"v_mfma_f32_16x16x32_bf16 v[112:115], a[252:255], a[204:207], v[112:115]\n" \
"s_addk_i32 s84, 0x7c00\n" \
"v_mfma_f32_16x16x32_bf16 v[108:111], a[184:187], a[140:143], v[108:111]\n" \
"buffer_load_dwordx4 v27, s[68:71], s84 offen offset:2048 lds\n" \
"v_mfma_f32_16x16x32_bf16 v[108:111], a[248:251], a[204:207], v[108:111]\n" \
"s_addk_i32 s84, 0x7c00\n" \
"s_nop 0\n" \
"v_mfma_f32_16x16x32_bf16 v[104:107], a[180:183], a[140:143], v[104:107]\n" \
"buffer_load_dwordx4 v27, s[68:71], s84 offen offset:3072 lds\n" \
"v_mfma_f32_16x16x32_bf16 v[104:107], a[244:247], a[204:207], v[104:107]\n" \
"ds_read_b64_tr_b16 a[168:169], v34 offset:32768\n" \
"v_mfma_f32_16x16x32_bf16 v[100:103], a[176:179], a[140:143], v[100:103]\n" \
"ds_read_b64_tr_b16 a[170:171], v34 offset:33280\n" \
"v_mfma_f32_16x16x32_bf16 v[100:103], a[240:243], a[204:207], v[100:103]\n" \
"ds_read_b128 a[140:143], v28 offset:38912\n" \
"v_mfma_f32_16x16x32_bf16 v[52:55], a[176:179], a[128:131], v[52:55]\n" \
"ds_read_b128 a[204:207], v29 offset:38912\n" \
"v_mfma_f32_16x16x32_bf16 v[52:55], a[240:243], a[192:195], v[52:55]\n" \
"s_add_u32 m0, s30, 0x1000\n" \
"s_mov_b32 s84, 0x9300\n" \
"v_mfma_f32_16x16x32_bf16 v[56:59], a[180:183], a[128:131], v[56:59]\n" \
"buffer_load_dwordx4 v24, s[76:79], s84 offen offset:0 lds\n" \
"v_mfma_f32_16x16x32_bf16 v[56:59], a[244:247], a[192:195], v[56:59]\n" \
"s_addk_i32 s84, 0x2000\n" \
"s_nop 0\n" \
"v_mfma_f32_16x16x32_bf16 v[60:63], a[184:187], a[128:131], v[60:63]\n" \
"buffer_load_dwordx4 v25, s[76:79], s84 offen offset:1024 lds\n" \
"v_mfma_f32_16x16x32_bf16 v[60:63], a[248:251], a[192:195], v[60:63]\n" \
"ds_read_b64_tr_b16 a[232:233], v34 offset:36864\n" \
"v_mfma_f32_16x16x32_bf16 v[64:67], a[188:191], a[128:131], v[64:67]\n" \
"ds_read_b64_tr_b16 a[234:235], v34 offset:37376\n" \
"v_mfma_f32_16x16x32_bf16 v[64:67], a[252:255], a[192:195], v[64:67]\n" \
"ds_read_b128 a[128:131], v28 offset:32768\n" \
"v_mfma_f32_16x16x32_bf16 v[80:83], a[188:191], a[132:135], v[80:83]\n" \
"ds_read_b128 a[192:195], v29 offset:32768\n" \
"v_mfma_f32_16x16x32_bf16 v[80:83], a[252:255], a[196:199], v[80:83]\n" \
"s_cmp_lg_u32 s12, 0\n" \
"s_cselect_b32 s13, s16, s13\n" \
"v_mfma_f32_16x16x32_bf16 v[76:79], a[184:187], a[132:135], v[76:79]\n" \
"s_bfe_u32 s16, s2, 0x20001\n" \
"s_addk_i32 s13, 0x4000\n" \
"v_mfma_f32_16x16x32_bf16 v[76:79], a[248:251], a[196:199], v[76:79]\n" \
"s_xor_b32 s16, s16, s3\n" \
"v_mfma_f32_16x16x32_bf16 v[72:75], a[180:183], a[132:135], v[72:75]\n" \
"s_cmp_lg_u32 s16, 0\n" \
"s_cselect_b32 s13, s7, s13\n" \
"v_mfma_f32_16x16x32_bf16 v[72:75], a[244:247], a[196:199], v[72:75]\n" \
"s_lshl_b32 s6, s6, 16\n" \
"v_writelane_b32 v11, s13, 40\n" \
"v_mfma_f32_16x16x32_bf16 v[68:71], a[176:179], a[132:135], v[68:71]\n" \
"s_add_i32 s13, s8, 0x4240000\n" \
"s_add_i32 s6, s13, s6\n" \
"v_mfma_f32_16x16x32_bf16 v[68:71], a[240:243], a[196:199], v[68:71]\n" \
"ds_read_b128 a[132:135], v28 offset:34816\n" \
"ds_read_b128 a[196:199], v29 offset:34816\n" \
"s_waitcnt lgkmcnt(15)\n" \
"v_mfma_f32_16x16x32_bf16 a[0:3], a[160:163], a[144:147], a[0:3]\n" \
"s_addk_i32 s84, 0x2000\n" \
"v_mfma_f32_16x16x32_bf16 a[0:3], a[224:227], a[208:211], a[0:3]\n" \
"buffer_load_dwordx4 v24, s[76:79], s84 offen offset:2048 lds\n" \
"v_mfma_f32_16x16x32_bf16 a[4:7], a[164:167], a[144:147], a[4:7]\n" \
"ds_read_b64_tr_b16 a[176:177], v36 offset:32768\n" \
"v_writelane_b32 v11, s6, 41\n" \
"v_mfma_f32_16x16x32_bf16 a[4:7], a[228:231], a[208:211], a[4:7]\n" \
"s_addk_i32 s84, 0x2000\n" \
"v_mfma_f32_16x16x32_bf16 a[12:15], a[172:175], a[144:147], a[12:15]\n" \
"buffer_load_dwordx4 v25, s[76:79], s84 offen offset:3072 lds\n" \
"s_waitcnt lgkmcnt(13)\n" \
"v_mfma_f32_16x16x32_bf16 a[12:15], a[236:239], a[208:211], a[12:15]\n" \
"ds_read_b64_tr_b16 a[178:179], v36 offset:33792\n" \
"s_add_i32 s8, s8, 0x42c0000\n" \
"s_waitcnt lgkmcnt(10)\n" \
"v_mfma_f32_16x16x32_bf16 a[8:11], a[144:147], a[168:171], a[8:11]\n" \
"ds_read_b64_tr_b16 a[240:241], v36 offset:40960\n" \
"s_lshl_b32 s6, s12, 10\n" \
"s_waitcnt lgkmcnt(7)\n" \
"v_mfma_f32_16x16x32_bf16 a[8:11], a[208:211], a[232:235], a[8:11]\n" \
"ds_read_b64_tr_b16 a[242:243], v36 offset:41984\n" \
"s_or_b32 s6, s8, s6\n" \
"v_mfma_f32_16x16x32_bf16 a[24:27], a[148:151], a[168:171], a[24:27]\n" \
"ds_read_b64_tr_b16 a[180:181], v37 offset:32768\n" \
"v_mfma_f32_16x16x32_bf16 a[24:27], a[212:215], a[232:235], a[24:27]\n" \
"ds_read_b64_tr_b16 a[182:183], v37 offset:33792\n" \
"v_mfma_f32_16x16x32_bf16 a[28:31], a[172:175], a[148:151], a[28:31]\n" \
"ds_read_b64_tr_b16 a[244:245], v37 offset:40960\n" \
"v_mfma_f32_16x16x32_bf16 a[28:31], a[236:239], a[212:215], a[28:31]\n" \
"ds_read_b64_tr_b16 a[246:247], v37 offset:41984\n" \
"v_mfma_f32_16x16x32_bf16 a[20:23], a[164:167], a[148:151], a[20:23]\n" \
"s_cmp_lg_u32 s16, 0\n" \
"s_cselect_b32 s6, s7, s6\n" \
"v_mfma_f32_16x16x32_bf16 a[20:23], a[228:231], a[212:215], a[20:23]\n" \
"s_add_u32 s88, s42, 0x3000\n" \
"buffer_store_dwordx4 v[244:247], v20, s[80:83], s88 offen offset:2048 sc1\n" \
"v_mfma_f32_16x16x32_bf16 a[16:19], a[160:163], a[148:151], a[16:19]\n" \
"ds_read_b64_tr_b16 a[184:185], v38 offset:32768\n" \
"s_mulk_i32 s4, 0x4800\n" \
"v_mfma_f32_16x16x32_bf16 a[16:19], a[224:227], a[212:215], a[16:19]\n" \
"ds_read_b64_tr_b16 a[186:187], v38 offset:33792\n" \
"v_writelane_b32 v11, s6, 42\n" \
"v_mfma_f32_16x16x32_bf16 a[32:35], a[160:163], a[152:155], a[32:35]\n" \
"ds_read_b64_tr_b16 a[248:249], v38 offset:40960\n" \
"s_add_i32 s6, s14, s10\n" \
"v_mfma_f32_16x16x32_bf16 a[32:35], a[224:227], a[216:219], a[32:35]\n" \
"ds_read_b64_tr_b16 a[250:251], v38 offset:41984\n" \
"v_writelane_b32 v11, s6, 43\n" \
"v_mfma_f32_16x16x32_bf16 a[36:39], a[164:167], a[152:155], a[36:39]\n" \
"s_add_i32 s6, s9, 0x21c000\n" \
"s_add_i32 s9, s6, s10\n" \
"v_mfma_f32_16x16x32_bf16 a[36:39], a[228:231], a[216:219], a[36:39]\n" \
"s_add_u32 s89, s42, 0x3000\n" \
"buffer_store_dwordx4 v[184:187], v20, s[80:83], s89 offen offset:1024 sc1\n" \
"v_mfma_f32_16x16x32_bf16 a[44:47], a[172:175], a[152:155], a[44:47]\n" \
"ds_read_b64_tr_b16 a[188:189], v39 offset:32768\n" \
"v_mfma_f32_16x16x32_bf16 a[44:47], a[236:239], a[216:219], a[44:47]\n" \
"ds_read_b64_tr_b16 a[190:191], v39 offset:33792\n" \
"v_mfma_f32_16x16x32_bf16 a[40:43], a[152:155], a[168:171], a[40:43]\n" \
"ds_read_b64_tr_b16 a[252:253], v39 offset:40960\n" \
"v_mfma_f32_16x16x32_bf16 a[40:43], a[216:219], a[232:235], a[40:43]\n" \
"s_cmp_lg_u32 s12, 0\n" \
"s_cselect_b32 s6, s6, s14\n" \
"v_mfma_f32_16x16x32_bf16 a[56:59], a[156:159], a[168:171], a[56:59]\n" \
"ds_read_b64_tr_b16 a[254:255], v39 offset:41984\n" \
"s_addk_i32 s6, 0x100\n" \
"v_mfma_f32_16x16x32_bf16 a[56:59], a[220:223], a[232:235], a[56:59]\n" \
"v_writelane_b32 v11, s9, 44\n" \
"v_mfma_f32_16x16x32_bf16 a[60:63], a[172:175], a[156:159], a[60:63]\n" \
"s_add_u32 s88, s42, 0x3000\n" \
"buffer_store_dwordx4 v[188:191], v20, s[80:83], s88 offen offset:0 sc1\n" \
"v_mfma_f32_16x16x32_bf16 a[60:63], a[236:239], a[220:223], a[60:63]\n" \
"s_cmp_lg_u32 s16, 0\n" \
"s_cselect_b32 s6, s7, s6\n" \
"v_mfma_f32_16x16x32_bf16 a[52:55], a[164:167], a[156:159], a[52:55]\n" \
"v_writelane_b32 v11, s6, 45\n" \
"s_or_b32 s6, s8, s15\n" \
"v_mfma_f32_16x16x32_bf16 a[52:55], a[228:231], a[220:223], a[52:55]\n" \
"v_writelane_b32 v11, s6, 53\n" \
"s_lshl_b32 s6, s3, 17\n" \
"v_mfma_f32_16x16x32_bf16 a[48:51], a[160:163], a[156:159], a[48:51]\n" \
"s_add_i32 s6, s13, s6\n" \
"s_or_b32 s5, s6, s5\n" \
"v_mfma_f32_16x16x32_bf16 a[48:51], a[224:227], a[220:223], a[48:51]\n" \
"s_or_b32 s5, s5, 0x3840\n" \
"v_writelane_b32 v11, s5, 6\n" \
"v_mfma_f32_16x16x32_bf16 a[64:67], a[164:167], a[140:143], a[64:67]\n" \
"s_mov_b32 s5, 0xff0000\n" \
"v_mfma_f32_16x16x32_bf16 a[64:67], a[228:231], a[204:207], a[64:67]\n" \
"s_cmp_lg_u32 s16, 0\n" \
"s_cselect_b32 s5, s17, s5\n" \
"v_mfma_f32_16x16x32_bf16 a[80:83], a[172:175], a[140:143], a[80:83]\n" \
"v_writelane_b32 v11, s5, 7\n" \
"s_mul_i32 s5, s3, 0x1200\n" \
"v_mfma_f32_16x16x32_bf16 a[80:83], a[236:239], a[204:207], a[80:83]\n" \
"s_add_i32 s4, s5, s4\n" \
"s_add_i32 s4, s4, s11\n" \
"v_mfma_f32_16x16x32_bf16 a[68:71], a[140:143], a[168:171], a[68:71]\n" \
"v_writelane_b32 v11, s4, 8\n" \
"s_lshl_b32 s4, s2, 16\n" \
"v_mfma_f32_16x16x32_bf16 a[68:71], a[204:207], a[232:235], a[68:71]\n" \
"s_waitcnt vmcnt(3) lgkmcnt(0)\n" \
"s_barrier\n" \
"s_nop 0\n" \
"v_mfma_f32_16x16x32_bf16 v[116:119], a[176:179], a[144:147], v[116:119]\n" \
"s_add_u32 m0, s40, 0x8000\n" \
"s_add_u32 s85, s39, 0xe000\n" \
"v_mfma_f32_16x16x32_bf16 v[116:119], a[240:243], a[208:211], v[116:119]\n" \
"buffer_load_dwordx4 v20, s[80:83], s85 offen sc1 lds\n" \
"v_mfma_f32_16x16x32_bf16 v[120:123], a[180:183], a[144:147], v[120:123]\n" \
"ds_read_b64_tr_b16 a[160:161], v32 offset:0\n" \
"s_add_i32 s5, s4, 0x10000\n" \
"v_mfma_f32_16x16x32_bf16 v[120:123], a[244:247], a[208:211], v[120:123]\n" \
"ds_read_b64_tr_b16 a[162:163], v32 offset:512\n" \
"s_and_b32 s5, s5, 0x70000\n" \
"v_mfma_f32_16x16x32_bf16 v[124:127], a[184:187], a[144:147], v[124:127]\n" \
"s_add_u32 m0, s40, 0x9000\n" \
"s_add_u32 s85, s39, 0xf000\n" \
"v_mfma_f32_16x16x32_bf16 v[124:127], a[248:251], a[208:211], v[124:127]\n" \
"buffer_load_dwordx4 v20, s[80:83], s85 offen sc1 lds\n" \
"v_mfma_f32_16x16x32_bf16 v[128:131], a[188:191], a[144:147], v[128:131]\n" \
"ds_read_b64_tr_b16 a[224:225], v32 offset:4096\n" \
"s_add_i32 s5, s13, s5\n" \
"v_mfma_f32_16x16x32_bf16 v[128:131], a[252:255], a[208:211], v[128:131]\n" \
"ds_read_b128 a[144:147], v28 offset:8192\n" \
"v_writelane_b32 v11, s5, 46\n" \
"v_mfma_f32_16x16x32_bf16 v[144:147], a[188:191], a[148:151], v[144:147]\n" \
"ds_read_b128 a[208:211], v29 offset:8192\n" \
"s_add_i32 s5, s4, 0x20000\n" \
"v_mfma_f32_16x16x32_bf16 v[144:147], a[252:255], a[212:215], v[144:147]\n" \
"s_add_u32 m0, s40, 0xb000\n" \
"s_movk_i32 s84, 0x380\n" \
"v_mfma_f32_16x16x32_bf16 v[140:143], a[184:187], a[148:151], v[140:143]\n" \
"buffer_load_dwordx4 v21, s[76:79], s84 offen offset:0 lds\n" \
"v_mfma_f32_16x16x32_bf16 v[140:143], a[248:251], a[212:215], v[140:143]\n" \
"ds_read_b64_tr_b16 a[226:227], v32 offset:4608\n" \
"s_and_b32 s5, s5, 0x70000\n" \
"v_mfma_f32_16x16x32_bf16 v[136:139], a[180:183], a[148:151], v[136:139]\n" \
"s_add_u32 m0, s30, 0x8000\n" \
"s_movk_i32 s84, 0x380\n" \
"v_mfma_f32_16x16x32_bf16 v[136:139], a[244:247], a[212:215], v[136:139]\n" \
"buffer_load_dwordx4 v24, s[76:79], s84 offen offset:0 lds\n" \
"v_mfma_f32_16x16x32_bf16 v[132:135], a[176:179], a[148:151], v[132:135]\n" \
"ds_read_b64_tr_b16 a[164:165], v33 offset:0\n" \
"s_add_i32 s5, s13, s5\n" \
"v_mfma_f32_16x16x32_bf16 v[132:135], a[240:243], a[212:215], v[132:135]\n" \
"ds_read_b128 a[148:151], v28 offset:10240\n" \
"s_bitset1_b32 s5, 11\n" \
"v_mfma_f32_16x16x32_bf16 v[148:151], a[176:179], a[152:155], v[148:151]\n" \
"ds_read_b128 a[212:215], v29 offset:10240\n" \
"v_writelane_b32 v11, s5, 47\n" \
"v_mfma_f32_16x16x32_bf16 v[148:151], a[240:243], a[216:219], v[148:151]\n" \
"s_movk_i32 s84, 0x2380\n" \
"v_mfma_f32_16x16x32_bf16 v[152:155], a[180:183], a[152:155], v[152:155]\n" \
"buffer_load_dwordx4 v25, s[76:79], s84 offen offset:1024 lds\n" \
"v_mfma_f32_16x16x32_bf16 v[152:155], a[244:247], a[216:219], v[152:155]\n" \
"ds_read_b64_tr_b16 a[166:167], v33 offset:512\n" \
"s_add_i32 s5, s4, 0x30000\n" \
"v_mfma_f32_16x16x32_bf16 v[156:159], a[184:187], a[152:155], v[156:159]\n" \
"ds_read_b64_tr_b16 a[228:229], v33 offset:4096\n" \
"s_and_b32 s5, s5, 0x70000\n" \
"v_mfma_f32_16x16x32_bf16 v[156:159], a[248:251], a[216:219], v[156:159]\n" \
"s_movk_i32 s84, 0x4380\n" \
"v_mfma_f32_16x16x32_bf16 v[160:163], a[188:191], a[152:155], v[160:163]\n" \
"buffer_load_dwordx4 v24, s[76:79], s84 offen offset:2048 lds\n" \
"v_mfma_f32_16x16x32_bf16 v[160:163], a[252:255], a[216:219], v[160:163]\n" \
"ds_read_b128 a[152:155], v28 offset:12288\n" \
"s_add_i32 s5, s13, s5\n" \
"v_mfma_f32_16x16x32_bf16 v[176:179], a[188:191], a[156:159], v[176:179]\n" \
"ds_read_b128 a[216:219], v29 offset:12288\n" \
"s_bitset1_b32 s5, 12\n" \
"v_mfma_f32_16x16x32_bf16 v[176:179], a[252:255], a[220:223], v[176:179]\n" \
"s_movk_i32 s84, 0x6380\n" \
"v_mfma_f32_16x16x32_bf16 v[172:175], a[184:187], a[156:159], v[172:175]\n" \
"buffer_load_dwordx4 v25, s[76:79], s84 offen offset:3072 lds\n" \
"v_mfma_f32_16x16x32_bf16 v[172:175], a[248:251], a[220:223], v[172:175]\n" \
"ds_read_b64_tr_b16 a[230:231], v33 offset:4608\n" \
"v_writelane_b32 v11, s5, 48\n" \
"v_mfma_f32_16x16x32_bf16 v[168:171], a[180:183], a[156:159], v[168:171]\n" \
"ds_read_b64_tr_b16 a[172:173], v35 offset:0\n" \
"s_and_b32 s5, s4, 0x70000\n" \
"v_mfma_f32_16x16x32_bf16 v[168:171], a[244:247], a[220:223], v[168:171]\n" \
"ds_read_b64_tr_b16 a[174:175], v35 offset:512\n" \
"s_xor_b32 s5, s5, 0x40000\n" \
"v_mfma_f32_16x16x32_bf16 v[164:167], a[176:179], a[156:159], v[164:167]\n" \
"s_add_i32 s5, s13, s5\n" \
"s_nop 0\n" \
"s_or_b32 s5, s5, 0x1800\n" \
"v_mfma_f32_16x16x32_bf16 v[164:167], a[240:243], a[220:223], v[164:167]\n" \
"ds_read_b128 a[156:159], v28 offset:14336\n" \
"v_writelane_b32 v11, s5, 49\n" \
"v_mfma_f32_16x16x32_bf16 v[84:87], a[176:179], a[136:139], v[84:87]\n" \
"ds_read_b128 a[220:223], v29 offset:14336\n" \
"s_add_i32 s5, s4, 0x50000\n" \
"v_mfma_f32_16x16x32_bf16 v[84:87], a[240:243], a[200:203], v[84:87]\n" \
"s_add_u32 m0, s31, 0x8000\n" \
"s_mov_b32 s84, 0x380000\n" \
"v_mfma_f32_16x16x32_bf16 v[88:91], a[180:183], a[136:139], v[88:91]\n" \
"buffer_load_dwordx4 v26, s[68:71], s84 offen offset:0 lds\n" \
"v_mfma_f32_16x16x32_bf16 v[88:91], a[244:247], a[200:203], v[88:91]\n" \
"s_addk_i32 s84, 0x7c00\n" \
"v_mfma_f32_16x16x32_bf16 v[92:95], a[184:187], a[136:139], v[92:95]\n" \
"buffer_load_dwordx4 v26, s[68:71], s84 offen offset:1024 lds\n" \
"v_mfma_f32_16x16x32_bf16 v[92:95], a[248:251], a[200:203], v[92:95]\n" \
"ds_read_b64_tr_b16 a[236:237], v35 offset:4096\n" \
"s_and_b32 s5, s5, 0x70000\n" \
"v_mfma_f32_16x16x32_bf16 v[96:99], a[188:191], a[136:139], v[96:99]\n" \
"ds_read_b64_tr_b16 a[238:239], v35 offset:4608\n" \
"s_add_i32 s5, s13, s5\n" \
"v_mfma_f32_16x16x32_bf16 v[96:99], a[252:255], a[200:203], v[96:99]\n" \
"ds_read_b128 a[136:139], v28 offset:4096\n" \
"s_bitset1_b32 s5, 13\n" \
"v_mfma_f32_16x16x32_bf16 v[112:115], a[188:191], a[140:143], v[112:115]\n" \
"ds_read_b128 a[200:203], v29 offset:4096\n" \
"v_writelane_b32 v11, s5, 50\n" \
"v_mfma_f32_16x16x32_bf16 v[112:115], a[252:255], a[204:207], v[112:115]\n" \
"s_addk_i32 s84, 0x7c00\n" \
"v_mfma_f32_16x16x32_bf16 v[108:111], a[184:187], a[140:143], v[108:111]\n" \
"buffer_load_dwordx4 v27, s[68:71], s84 offen offset:2048 lds\n" \
"v_mfma_f32_16x16x32_bf16 v[108:111], a[248:251], a[204:207], v[108:111]\n" \
"s_addk_i32 s84, 0x7c00\n" \
"s_nop 0\n" \
"v_mfma_f32_16x16x32_bf16 v[104:107], a[180:183], a[140:143], v[104:107]\n" \
"buffer_load_dwordx4 v27, s[68:71], s84 offen offset:3072 lds\n" \
"v_mfma_f32_16x16x32_bf16 v[104:107], a[244:247], a[204:207], v[104:107]\n" \
"ds_read_b64_tr_b16 a[168:169], v34 offset:0\n" \
"s_add_i32 s5, s4, 0x60000\n" \
"v_mfma_f32_16x16x32_bf16 v[100:103], a[176:179], a[140:143], v[100:103]\n" \
"ds_read_b64_tr_b16 a[170:171], v34 offset:512\n" \
"s_and_b32 s5, s5, 0x70000\n" \
"v_mfma_f32_16x16x32_bf16 v[100:103], a[240:243], a[204:207], v[100:103]\n" \
"ds_read_b128 a[140:143], v28 offset:6144\n" \
"s_add_i32 s4, s4, 0x70000\n" \
"v_mfma_f32_16x16x32_bf16 v[52:55], a[176:179], a[128:131], v[52:55]\n" \
"ds_read_b128 a[204:207], v29 offset:6144\n" \
"s_add_i32 s5, s13, s5\n" \
"v_mfma_f32_16x16x32_bf16 v[52:55], a[240:243], a[192:195], v[52:55]\n" \
"s_add_u32 m0, s30, 0x9000\n" \
"s_mov_b32 s84, 0x9380\n" \
"v_mfma_f32_16x16x32_bf16 v[56:59], a[180:183], a[128:131], v[56:59]\n" \
"buffer_load_dwordx4 v24, s[76:79], s84 offen offset:0 lds\n" \
"v_mfma_f32_16x16x32_bf16 v[56:59], a[244:247], a[192:195], v[56:59]\n" \
"s_addk_i32 s84, 0x2000\n" \
"v_mfma_f32_16x16x32_bf16 v[60:63], a[184:187], a[128:131], v[60:63]\n" \
"buffer_load_dwordx4 v25, s[76:79], s84 offen offset:1024 lds\n" \
"v_mfma_f32_16x16x32_bf16 v[60:63], a[248:251], a[192:195], v[60:63]\n" \
"ds_read_b64_tr_b16 a[232:233], v34 offset:4096\n" \
"s_and_b32 s4, s4, 0x70000\n" \
"v_mfma_f32_16x16x32_bf16 v[64:67], a[188:191], a[128:131], v[64:67]\n" \
"ds_read_b64_tr_b16 a[234:235], v34 offset:4608\n" \
"s_or_b32 s5, s5, 0x2800\n" \
"v_mfma_f32_16x16x32_bf16 v[64:67], a[252:255], a[192:195], v[64:67]\n" \
"ds_read_b128 a[128:131], v28 offset:0\n" \
"v_writelane_b32 v11, s5, 51\n" \
"v_mfma_f32_16x16x32_bf16 v[80:83], a[188:191], a[132:135], v[80:83]\n" \
"ds_read_b128 a[192:195], v29 offset:0\n" \
"s_add_i32 s13, s13, s4\n" \
"v_mfma_f32_16x16x32_bf16 v[80:83], a[252:255], a[196:199], v[80:83]\n" \
"s_or_b32 s4, s13, 0x3000\n" \
"v_writelane_b32 v11, s4, 52\n" \
"v_mfma_f32_16x16x32_bf16 v[76:79], a[184:187], a[132:135], v[76:79]\n" \
"s_movk_i32 s11, 0x4000\n" \
"s_lshl_b32 s4, s2, 2\n" \
"v_mfma_f32_16x16x32_bf16 v[76:79], a[248:251], a[196:199], v[76:79]\n" \
"s_and_b32 s4, s4, 28\n" \
"s_bfe_u32 s5, s2, 0x20003\n" \
"v_mfma_f32_16x16x32_bf16 v[72:75], a[180:183], a[132:135], v[72:75]\n" \
"s_or_b32 s4, s4, s5\n" \
"s_lshr_b32 s5, s2, 5\n" \
"v_mfma_f32_16x16x32_bf16 v[72:75], a[244:247], a[196:199], v[72:75]\n" \
"s_lshr_b32 s19, s3, 1\n" \
"s_lshl_b32 s18, s5, 1\n" \
"v_mfma_f32_16x16x32_bf16 v[68:71], a[176:179], a[132:135], v[68:71]\n" \
"s_lshl_b32 s6, s4, 20\n" \
"s_lshl_b32 s7, s4, 8\n" \
"v_mfma_f32_16x16x32_bf16 v[68:71], a[240:243], a[196:199], v[68:71]\n" \
"ds_read_b128 a[132:135], v28 offset:2048\n" \
"ds_read_b128 a[196:199], v29 offset:2048\n" \
"s_waitcnt lgkmcnt(15)\n" \
"v_mfma_f32_16x16x32_bf16 a[0:3], a[160:163], a[144:147], a[0:3]\n" \
"s_addk_i32 s84, 0x2000\n" \
"v_mfma_f32_16x16x32_bf16 a[0:3], a[224:227], a[208:211], a[0:3]\n" \
"buffer_load_dwordx4 v24, s[76:79], s84 offen offset:2048 lds\n" \
"v_mfma_f32_16x16x32_bf16 a[4:7], a[164:167], a[144:147], a[4:7]\n" \
"ds_read_b64_tr_b16 a[176:177], v36 offset:0\n" \
"s_and_b32 s13, s3, 1\n" \
"v_mfma_f32_16x16x32_bf16 a[4:7], a[228:231], a[208:211], a[4:7]\n" \
"s_addk_i32 s84, 0x2000\n" \
"v_mfma_f32_16x16x32_bf16 a[12:15], a[172:175], a[144:147], a[12:15]\n" \
"buffer_load_dwordx4 v25, s[76:79], s84 offen offset:3072 lds\n" \
"s_waitcnt lgkmcnt(13)\n" \
"v_mfma_f32_16x16x32_bf16 a[12:15], a[236:239], a[208:211], a[12:15]\n" \
"ds_read_b64_tr_b16 a[178:179], v36 offset:1024\n" \
"s_add_i32 s14, s18, s19\n" \
"s_waitcnt lgkmcnt(10)\n" \
"v_mfma_f32_16x16x32_bf16 a[8:11], a[144:147], a[168:171], a[8:11]\n" \
"ds_read_b64_tr_b16 a[240:241], v36 offset:8192\n" \
"s_waitcnt lgkmcnt(7)\n" \
"v_mfma_f32_16x16x32_bf16 a[8:11], a[208:211], a[232:235], a[8:11]\n" \
"ds_read_b64_tr_b16 a[242:243], v36 offset:9216\n" \
"v_mfma_f32_16x16x32_bf16 a[24:27], a[148:151], a[168:171], a[24:27]\n" \
"ds_read_b64_tr_b16 a[180:181], v37 offset:0\n" \
"v_mfma_f32_16x16x32_bf16 a[24:27], a[212:215], a[232:235], a[24:27]\n" \
"ds_read_b64_tr_b16 a[182:183], v37 offset:1024\n" \
"v_mfma_f32_16x16x32_bf16 a[28:31], a[172:175], a[148:151], a[28:31]\n" \
"ds_read_b64_tr_b16 a[244:245], v37 offset:8192\n" \
"v_mfma_f32_16x16x32_bf16 a[28:31], a[236:239], a[212:215], a[28:31]\n" \
"ds_read_b64_tr_b16 a[246:247], v37 offset:9216\n" \
"v_mfma_f32_16x16x32_bf16 a[20:23], a[164:167], a[148:151], a[20:23]\n" \
"s_add_u32 s6, s26, s6\n" \
"s_addc_u32 s9, s27, 0\n" \
"v_mfma_f32_16x16x32_bf16 a[20:23], a[228:231], a[212:215], a[20:23]\n" \
"s_add_u32 s89, s42, 0x0\n" \
"s_nop 0\n" \
"buffer_store_dwordx4 v[192:195], v20, s[80:83], s89 offen offset:2048 sc1\n" \
"v_mfma_f32_16x16x32_bf16 a[16:19], a[160:163], a[148:151], a[16:19]\n" \
"ds_read_b64_tr_b16 a[184:185], v38 offset:0\n" \
"v_mfma_f32_16x16x32_bf16 a[16:19], a[224:227], a[212:215], a[16:19]\n" \
"ds_read_b64_tr_b16 a[186:187], v38 offset:1024\n" \
"v_mfma_f32_16x16x32_bf16 a[32:35], a[160:163], a[152:155], a[32:35]\n" \
"ds_read_b64_tr_b16 a[248:249], v38 offset:8192\n" \
"v_mfma_f32_16x16x32_bf16 a[32:35], a[224:227], a[216:219], a[32:35]\n" \
"ds_read_b64_tr_b16 a[250:251], v38 offset:9216\n" \
"v_mfma_f32_16x16x32_bf16 a[36:39], a[164:167], a[152:155], a[36:39]\n" \
"s_add_u32 s8, s6, 0x1000000\n" \
"s_addc_u32 s6, s9, 0\n" \
"v_mfma_f32_16x16x32_bf16 a[36:39], a[228:231], a[216:219], a[36:39]\n" \
"s_add_u32 s88, s42, 0x0\n" \
"buffer_store_dwordx4 v[196:199], v20, s[80:83], s88 offen offset:3072 sc1\n" \
"v_mfma_f32_16x16x32_bf16 a[44:47], a[172:175], a[152:155], a[44:47]\n" \
"ds_read_b64_tr_b16 a[188:189], v39 offset:0\n" \
"s_and_b32 s9, s6, 0xffff\n" \
"v_mfma_f32_16x16x32_bf16 a[44:47], a[236:239], a[216:219], a[44:47]\n" \
"ds_read_b64_tr_b16 a[190:191], v39 offset:1024\n" \
"s_lshl_b32 s6, s5, 16\n" \
"v_mfma_f32_16x16x32_bf16 a[40:43], a[152:155], a[168:171], a[40:43]\n" \
"ds_read_b64_tr_b16 a[252:253], v39 offset:8192\n" \
"s_sub_i32 s10, 0x100000, s6\n" \
"v_mfma_f32_16x16x32_bf16 a[40:43], a[216:219], a[232:235], a[40:43]\n" \
"s_mov_b32 s6, 0x8000\n" \
"v_mfma_f32_16x16x32_bf16 a[56:59], a[156:159], a[168:171], a[56:59]\n" \
"ds_read_b64_tr_b16 a[254:255], v39 offset:9216\n" \
"v_mfma_f32_16x16x32_bf16 a[56:59], a[220:223], a[232:235], a[56:59]\n" \
"s_cmp_lg_u32 s5, 0\n" \
"s_cselect_b32 s17, s11, s6\n" \
"v_mfma_f32_16x16x32_bf16 a[60:63], a[172:175], a[156:159], a[60:63]\n" \
"s_add_u32 s89, s42, 0x2000\n" \
"buffer_store_dwordx4 v[200:203], v20, s[80:83], s89 offen offset:0 sc1\n" \
"v_mfma_f32_16x16x32_bf16 a[60:63], a[236:239], a[220:223], a[60:63]\n" \
"s_lshl_b32 s6, s3, 1\n" \
"s_lshl_b32 s5, s5, 3\n" \
"v_mfma_f32_16x16x32_bf16 a[52:55], a[164:167], a[156:159], a[52:55]\n" \
"s_and_b32 s6, s6, 4\n" \
"s_or_b32 s12, s5, s6\n" \
"v_mfma_f32_16x16x32_bf16 a[52:55], a[228:231], a[220:223], a[52:55]\n" \
"s_sub_i32 s5, s13, s5\n" \
"s_or_b32 s5, s5, s6\n" \
"v_mfma_f32_16x16x32_bf16 a[48:51], a[160:163], a[156:159], a[48:51]\n" \
"s_or_b32 s6, s12, s13\n" \
"s_lshl_b32 s4, s4, 2\n" \
"v_mfma_f32_16x16x32_bf16 a[48:51], a[224:227], a[220:223], a[48:51]\n" \
"s_mulk_i32 s6, 0x92\n" \
"s_add_i32 s4, s4, 18\n" \
"v_mfma_f32_16x16x32_bf16 a[64:67], a[164:167], a[140:143], a[64:67]\n" \
"s_mulk_i32 s5, 0x92\n" \
"s_add_i32 s6, s4, s6\n" \
"v_mfma_f32_16x16x32_bf16 a[64:67], a[228:231], a[204:207], a[64:67]\n" \
"s_add_i32 s5, s5, s4\n" \
"s_lshl_b32 s4, s3, 11\n" \
"v_mfma_f32_16x16x32_bf16 a[80:83], a[172:175], a[140:143], a[80:83]\n" \
"s_lshl_b32 s15, s13, 12\n" \
"s_lshl_b32 s16, s19, 14\n" \
"v_mfma_f32_16x16x32_bf16 a[80:83], a[236:239], a[204:207], a[80:83]\n" \
"s_mul_i32 s19, s19, 0xe000\n" \
"s_mov_b32 s11, 0x20000\n" \
"v_mfma_f32_16x16x32_bf16 a[68:71], a[140:143], a[168:171], a[68:71]\n" \
"s_or_b32 s12, s4, 0xc000\n" \
"s_lshl_b32 s4, s3, 8\n" \
"v_mfma_f32_16x16x32_bf16 a[68:71], a[204:207], a[232:235], a[68:71]\n" \
"s_waitcnt vmcnt(3) lgkmcnt(0)\n" \
"s_barrier\n" \
"v_mfma_f32_16x16x32_bf16 v[116:119], a[176:179], a[144:147], v[116:119]\n" \
"s_add_u32 m0, s40, 0x0\n" \
"s_add_u32 s85, s39, 0x10000\n" \
"v_mfma_f32_16x16x32_bf16 v[116:119], a[240:243], a[208:211], v[116:119]\n" \
"buffer_load_dwordx4 v20, s[80:83], s85 offen sc1 lds\n" \
"v_mfma_f32_16x16x32_bf16 v[120:123], a[180:183], a[144:147], v[120:123]\n" \
"ds_read_b64_tr_b16 a[160:161], v32 offset:32768\n" \
"s_or_b32 s22, s15, s16\n" \
"v_mfma_f32_16x16x32_bf16 v[120:123], a[244:247], a[208:211], v[120:123]\n" \
"ds_read_b64_tr_b16 a[162:163], v32 offset:33280\n" \
"s_or_b32 s23, s19, s15\n" \
"v_mfma_f32_16x16x32_bf16 v[124:127], a[184:187], a[144:147], v[124:127]\n" \
"s_add_u32 m0, s40, 0x1000\n" \
"s_add_u32 s85, s39, 0x11000\n" \
"v_mfma_f32_16x16x32_bf16 v[124:127], a[248:251], a[208:211], v[124:127]\n" \
"buffer_load_dwordx4 v20, s[80:83], s85 offen sc1 lds\n" \
"v_mfma_f32_16x16x32_bf16 v[128:131], a[188:191], a[144:147], v[128:131]\n" \
"ds_read_b64_tr_b16 a[224:225], v32 offset:36864\n" \
"s_lshl_b32 s60, s6, 10\n" \
"v_mfma_f32_16x16x32_bf16 v[128:131], a[252:255], a[208:211], v[128:131]\n" \
"ds_read_b128 a[144:147], v28 offset:40960\n" \
"s_lshl_b32 s61, s5, 10\n" \
"v_mfma_f32_16x16x32_bf16 v[144:147], a[188:191], a[148:151], v[144:147]\n" \
"ds_read_b128 a[208:211], v29 offset:40960\n" \
"s_lshl_b32 s14, s14, 15\n" \
"v_mfma_f32_16x16x32_bf16 v[144:147], a[252:255], a[212:215], v[144:147]\n" \
"s_add_u32 m0, s40, 0x3000\n" \
"s_movk_i32 s84, 0x400\n" \
"v_mfma_f32_16x16x32_bf16 v[140:143], a[184:187], a[148:151], v[140:143]\n" \
"buffer_load_dwordx4 v21, s[76:79], s84 offen offset:0 lds\n" \
"v_mfma_f32_16x16x32_bf16 v[140:143], a[248:251], a[212:215], v[140:143]\n" \
"ds_read_b64_tr_b16 a[226:227], v32 offset:37376\n" \
"s_or_b32 s20, s4, 0x26c00\n" \
"v_mfma_f32_16x16x32_bf16 v[136:139], a[180:183], a[148:151], v[136:139]\n" \
"s_add_u32 m0, s30, 0x0\n" \
"s_movk_i32 s84, 0x400\n" \
"v_mfma_f32_16x16x32_bf16 v[136:139], a[244:247], a[212:215], v[136:139]\n" \
"buffer_load_dwordx4 v24, s[76:79], s84 offen offset:0 lds\n" \
"v_mfma_f32_16x16x32_bf16 v[132:135], a[176:179], a[148:151], v[132:135]\n" \
"ds_read_b64_tr_b16 a[164:165], v33 offset:32768\n" \
"s_or_b32 s13, s22, 0x8000\n" \
"v_mfma_f32_16x16x32_bf16 v[132:135], a[240:243], a[212:215], v[132:135]\n" \
"ds_read_b128 a[148:151], v28 offset:43008\n" \
"s_add_i32 s22, s22, 0x14000\n" \
"v_mfma_f32_16x16x32_bf16 v[148:151], a[176:179], a[152:155], v[148:151]\n" \
"ds_read_b128 a[212:215], v29 offset:43008\n" \
"s_bitset1_b32 s23, 16\n" \
"v_mfma_f32_16x16x32_bf16 v[148:151], a[240:243], a[216:219], v[148:151]\n" \
"s_movk_i32 s84, 0x2400\n" \
"v_mfma_f32_16x16x32_bf16 v[152:155], a[180:183], a[152:155], v[152:155]\n" \
"buffer_load_dwordx4 v25, s[76:79], s84 offen offset:1024 lds\n" \
"v_mfma_f32_16x16x32_bf16 v[152:155], a[244:247], a[216:219], v[152:155]\n" \
"ds_read_b64_tr_b16 a[166:167], v33 offset:33280\n" \
"s_or_b32 s24, s7, 0x5be8020\n" \
"v_mfma_f32_16x16x32_bf16 v[156:159], a[184:187], a[152:155], v[156:159]\n" \
"ds_read_b64_tr_b16 a[228:229], v33 offset:36864\n" \
"s_and_b32 s57, s27, 0xffff\n" \
"v_mfma_f32_16x16x32_bf16 v[156:159], a[248:251], a[216:219], v[156:159]\n" \
"s_movk_i32 s84, 0x4400\n" \
"v_mfma_f32_16x16x32_bf16 v[160:163], a[188:191], a[152:155], v[160:163]\n" \
"buffer_load_dwordx4 v24, s[76:79], s84 offen offset:2048 lds\n" \
"v_mfma_f32_16x16x32_bf16 v[160:163], a[252:255], a[216:219], v[160:163]\n" \
"ds_read_b128 a[152:155], v28 offset:45056\n" \
"s_mov_b32 s58, 0x6000000\n" \
"v_mfma_f32_16x16x32_bf16 v[176:179], a[188:191], a[156:159], v[176:179]\n" \
"ds_read_b128 a[216:219], v29 offset:45056\n" \
"s_mov_b32 s56, s26\n" \
"v_mfma_f32_16x16x32_bf16 v[176:179], a[252:255], a[220:223], v[176:179]\n" \
"s_movk_i32 s84, 0x6400\n" \
".fill (L_pb + 73440 - .) / 4, 4, 0xBF800000\n" \
"L_pp18:\n" \
"s_cbranch_execz L_pret\n" \
"v_mfma_f32_16x16x32_bf16 v[172:175], a[184:187], a[156:159], v[172:175]\n" \
"buffer_load_dwordx4 v25, s[76:79], s84 offen offset:3072 lds\n" \
"v_mfma_f32_16x16x32_bf16 v[172:175], a[248:251], a[220:223], v[172:175]\n" \
"ds_read_b64_tr_b16 a[230:231], v33 offset:37376\n" \
"s_mov_b32 s59, s11\n" \
"v_mfma_f32_16x16x32_bf16 v[168:171], a[180:183], a[156:159], v[168:171]\n" \
"ds_read_b64_tr_b16 a[172:173], v35 offset:32768\n" \
"s_add_i32 s60, s60, 0x3000000\n" \
"v_mfma_f32_16x16x32_bf16 v[168:171], a[244:247], a[220:223], v[168:171]\n" \
"ds_read_b64_tr_b16 a[174:175], v35 offset:33280\n" \
"s_add_i32 s61, s61, 0x411c000\n" \
"v_mfma_f32_16x16x32_bf16 v[164:167], a[176:179], a[156:159], v[164:167]\n" \
"s_sub_i32 s19, 14, s18\n" \
"s_mov_b32 s21, 0x71800000\n" \
"v_mfma_f32_16x16x32_bf16 v[164:167], a[240:243], a[220:223], v[164:167]\n" \
"ds_read_b128 a[156:159], v28 offset:47104\n" \
"s_mov_b32 s4, L_gsw-L_gtr\n" \
"v_mfma_f32_16x16x32_bf16 v[84:87], a[176:179], a[136:139], v[84:87]\n" \
"ds_read_b128 a[220:223], v29 offset:47104\n" \
"v_mfma_f32_16x16x32_bf16 v[84:87], a[240:243], a[200:203], v[84:87]\n" \
"s_add_u32 m0, s31, 0x0\n" \
"s_mov_b32 s84, 0x400000\n" \
"v_mfma_f32_16x16x32_bf16 v[88:91], a[180:183], a[136:139], v[88:91]\n" \
"buffer_load_dwordx4 v26, s[68:71], s84 offen offset:0 lds\n" \
"v_mfma_f32_16x16x32_bf16 v[88:91], a[244:247], a[200:203], v[88:91]\n" \
"s_addk_i32 s84, 0x7c00\n" \
"s_nop 0\n" \
"v_mfma_f32_16x16x32_bf16 v[92:95], a[184:187], a[136:139], v[92:95]\n" \
"buffer_load_dwordx4 v26, s[68:71], s84 offen offset:1024 lds\n" \
"v_mfma_f32_16x16x32_bf16 v[92:95], a[248:251], a[200:203], v[92:95]\n" \
"ds_read_b64_tr_b16 a[236:237], v35 offset:36864\n" \
"v_mfma_f32_16x16x32_bf16 v[96:99], a[188:191], a[136:139], v[96:99]\n" \
"ds_read_b64_tr_b16 a[238:239], v35 offset:37376\n" \
"v_mfma_f32_16x16x32_bf16 v[96:99], a[252:255], a[200:203], v[96:99]\n" \
"ds_read_b128 a[136:139], v28 offset:36864\n" \
"v_mfma_f32_16x16x32_bf16 v[112:115], a[188:191], a[140:143], v[112:115]\n" \
"ds_read_b128 a[200:203], v29 offset:36864\n" \
"v_mfma_f32_16x16x32_bf16 v[112:115], a[252:255], a[204:207], v[112:115]\n" \
"s_addk_i32 s84, 0x7c00\n" \
"v_mfma_f32_16x16x32_bf16 v[108:111], a[184:187], a[140:143], v[108:111]\n" \
"buffer_load_dwordx4 v27, s[68:71], s84 offen offset:2048 lds\n" \
"v_mfma_f32_16x16x32_bf16 v[108:111], a[248:251], a[204:207], v[108:111]\n" \
"s_addk_i32 s84, 0x7c00\n" \
"v_mfma_f32_16x16x32_bf16 v[104:107], a[180:183], a[140:143], v[104:107]\n" \
"buffer_load_dwordx4 v27, s[68:71], s84 offen offset:3072 lds\n" \
"v_mfma_f32_16x16x32_bf16 v[104:107], a[244:247], a[204:207], v[104:107]\n" \
"ds_read_b64_tr_b16 a[168:169], v34 offset:32768\n" \
"v_mfma_f32_16x16x32_bf16 v[100:103], a[176:179], a[140:143], v[100:103]\n" \
"ds_read_b64_tr_b16 a[170:171], v34 offset:33280\n" \
"v_mfma_f32_16x16x32_bf16 v[100:103], a[240:243], a[204:207], v[100:103]\n" \
"ds_read_b128 a[140:143], v28 offset:38912\n" \
"v_mfma_f32_16x16x32_bf16 v[52:55], a[176:179], a[128:131], v[52:55]\n" \
"ds_read_b128 a[204:207], v29 offset:38912\n" \
"v_mfma_f32_16x16x32_bf16 v[52:55], a[240:243], a[192:195], v[52:55]\n" \
"s_add_u32 m0, s30, 0x1000\n" \
"s_mov_b32 s84, 0x9400\n" \
"v_mfma_f32_16x16x32_bf16 v[56:59], a[180:183], a[128:131], v[56:59]\n" \
"buffer_load_dwordx4 v24, s[76:79], s84 offen offset:0 lds\n" \
"v_mfma_f32_16x16x32_bf16 v[56:59], a[244:247], a[192:195], v[56:59]\n" \
"s_addk_i32 s84, 0x2000\n" \
"s_nop 0\n" \
"v_mfma_f32_16x16x32_bf16 v[60:63], a[184:187], a[128:131], v[60:63]\n" \
"buffer_load_dwordx4 v25, s[76:79], s84 offen offset:1024 lds\n" \
"v_mfma_f32_16x16x32_bf16 v[60:63], a[248:251], a[192:195], v[60:63]\n" \
"ds_read_b64_tr_b16 a[232:233], v34 offset:36864\n" \
"v_mfma_f32_16x16x32_bf16 v[64:67], a[188:191], a[128:131], v[64:67]\n" \
"ds_read_b64_tr_b16 a[234:235], v34 offset:37376\n" \
"v_mfma_f32_16x16x32_bf16 v[64:67], a[252:255], a[192:195], v[64:67]\n" \
"ds_read_b128 a[128:131], v28 offset:32768\n" \
"v_mfma_f32_16x16x32_bf16 v[80:83], a[188:191], a[132:135], v[80:83]\n" \
"ds_read_b128 a[192:195], v29 offset:32768\n" \
"v_mfma_f32_16x16x32_bf16 v[80:83], a[252:255], a[196:199], v[80:83]\n" \
"s_cmp_eq_u32 s19, 0\n" \
"s_cselect_b32 s4, L_gs7-L_gtr, s4\n" \
"v_mfma_f32_16x16x32_bf16 v[76:79], a[184:187], a[132:135], v[76:79]\n" \
"s_cmp_eq_u32 s16, 0\n" \
"s_cselect_b32 s4, 0, s4\n" \
"v_mfma_f32_16x16x32_bf16 v[76:79], a[248:251], a[196:199], v[76:79]\n" \
"s_getpc_b64 s[28:29]\n" \
"L_gp1_%=:\n" \
"s_add_u32 s28, s28, L_gtr-L_gp1_%=\n" \
"s_addc_u32 s29, s29, 0\n" \
"v_mfma_f32_16x16x32_bf16 v[72:75], a[180:183], a[132:135], v[72:75]\n" \
"s_add_u32 s28, s28, s4\n" \
"s_addc_u32 s29, s29, 0\n" \
"v_mfma_f32_16x16x32_bf16 v[72:75], a[244:247], a[196:199], v[72:75]\n" \
"v_mfma_f32_16x16x32_bf16 v[68:71], a[176:179], a[132:135], v[68:71]\n" \
"v_mfma_f32_16x16x32_bf16 v[68:71], a[240:243], a[196:199], v[68:71]\n" \
"ds_read_b128 a[132:135], v28 offset:34816\n" \
"ds_read_b128 a[196:199], v29 offset:34816\n" \
"s_waitcnt lgkmcnt(15)\n" \
"v_mfma_f32_16x16x32_bf16 a[0:3], a[160:163], a[144:147], a[0:3]\n" \
"s_add_u32 m0, s30, 0x1000\n" \
"s_mov_b32 s84, 0xd400\n" \
"v_mfma_f32_16x16x32_bf16 a[0:3], a[224:227], a[208:211], a[0:3]\n" \
"buffer_load_dwordx4 v24, s[76:79], s84 offen offset:2048 lds\n" \
"v_mfma_f32_16x16x32_bf16 a[4:7], a[164:167], a[144:147], a[4:7]\n" \
"ds_read_b64_tr_b16 a[176:177], v36 offset:32768\n" \
"v_mfma_f32_16x16x32_bf16 a[4:7], a[228:231], a[208:211], a[4:7]\n" \
"s_addk_i32 s84, 0x2000\n" \
"v_mfma_f32_16x16x32_bf16 a[12:15], a[172:175], a[144:147], a[12:15]\n" \
"buffer_load_dwordx4 v25, s[76:79], s84 offen offset:3072 lds\n" \
"s_waitcnt lgkmcnt(13)\n" \
"v_mfma_f32_16x16x32_bf16 a[12:15], a[236:239], a[208:211], a[12:15]\n" \
"ds_read_b64_tr_b16 a[178:179], v36 offset:33792\n" \
"s_waitcnt lgkmcnt(10)\n" \
"v_mfma_f32_16x16x32_bf16 a[8:11], a[144:147], a[168:171], a[8:11]\n" \
"ds_read_b64_tr_b16 a[240:241], v36 offset:40960\n" \
"s_waitcnt lgkmcnt(7)\n" \
"v_mfma_f32_16x16x32_bf16 a[8:11], a[208:211], a[232:235], a[8:11]\n" \
"ds_read_b64_tr_b16 a[242:243], v36 offset:41984\n" \
"v_mfma_f32_16x16x32_bf16 a[24:27], a[148:151], a[168:171], a[24:27]\n" \
"ds_read_b64_tr_b16 a[180:181], v37 offset:32768\n" \
"v_mfma_f32_16x16x32_bf16 a[24:27], a[212:215], a[232:235], a[24:27]\n" \
"ds_read_b64_tr_b16 a[182:183], v37 offset:33792\n" \
"v_mfma_f32_16x16x32_bf16 a[28:31], a[172:175], a[148:151], a[28:31]\n" \
"ds_read_b64_tr_b16 a[244:245], v37 offset:40960\n" \
"v_mfma_f32_16x16x32_bf16 a[28:31], a[236:239], a[212:215], a[28:31]\n" \
"ds_read_b64_tr_b16 a[246:247], v37 offset:41984\n" \
"v_mfma_f32_16x16x32_bf16 a[20:23], a[164:167], a[148:151], a[20:23]\n" \
"v_mfma_f32_16x16x32_bf16 a[20:23], a[228:231], a[212:215], a[20:23]\n" \
"s_add_u32 s88, s42, 0x2000\n" \
"buffer_store_dwordx4 v[204:207], v20, s[80:83], s88 offen offset:1024 sc1\n" \
"v_mfma_f32_16x16x32_bf16 a[16:19], a[160:163], a[148:151], a[16:19]\n" \
"ds_read_b64_tr_b16 a[184:185], v38 offset:32768\n" \
"v_mfma_f32_16x16x32_bf16 a[16:19], a[224:227], a[212:215], a[16:19]\n" \
"ds_read_b64_tr_b16 a[186:187], v38 offset:33792\n" \
"v_mfma_f32_16x16x32_bf16 a[32:35], a[160:163], a[152:155], a[32:35]\n" \
"ds_read_b64_tr_b16 a[248:249], v38 offset:40960\n" \
"v_mfma_f32_16x16x32_bf16 a[32:35], a[224:227], a[216:219], a[32:35]\n" \
"ds_read_b64_tr_b16 a[250:251], v38 offset:41984\n" \
"v_mfma_f32_16x16x32_bf16 a[36:39], a[164:167], a[152:155], a[36:39]\n" \
"v_lshlrev_b32_e32 v45, 3, v20\n" \
"v_add_u32_e32 v45, s30, v45\n" \
"v_mfma_f32_16x16x32_bf16 a[36:39], a[228:231], a[216:219], a[36:39]\n" \
"s_add_u32 s89, s42, 0x2000\n" \
"buffer_store_dwordx4 v[208:211], v20, s[80:83], s89 offen offset:2048 sc1\n" \
"s_getpc_b64 s[86:87]\n" \
"L_ct1_%=:\n" \
"s_add_u32 s86, s86, L_ap0-L_ct1_%=\n" \
"s_addc_u32 s87, s87, 0\n" \
"s_add_u32 m0, s40, 0x2000\n" \
"v_mfma_f32_16x16x32_bf16 a[44:47], a[172:175], a[152:155], a[44:47]\n" \
"global_load_lds_dword v45, s[86:87]\n" \
"v_mfma_f32_16x16x32_bf16 a[44:47], a[236:239], a[216:219], a[44:47]\n" \
"ds_read_b64_tr_b16 a[188:189], v39 offset:32768\n" \
"v_mfma_f32_16x16x32_bf16 a[40:43], a[152:155], a[168:171], a[40:43]\n" \
"ds_read_b64_tr_b16 a[190:191], v39 offset:33792\n" \
"v_mfma_f32_16x16x32_bf16 a[40:43], a[216:219], a[232:235], a[40:43]\n" \
"ds_read_b64_tr_b16 a[252:253], v39 offset:40960\n" \
"v_mfma_f32_16x16x32_bf16 a[56:59], a[156:159], a[168:171], a[56:59]\n" \
"ds_read_b64_tr_b16 a[254:255], v39 offset:41984\n" \
"v_mfma_f32_16x16x32_bf16 a[56:59], a[220:223], a[232:235], a[56:59]\n" \
"v_mfma_f32_16x16x32_bf16 a[60:63], a[172:175], a[156:159], a[60:63]\n" \
"s_add_u32 s88, s42, 0x2000\n" \
"buffer_store_dwordx4 v[212:215], v20, s[80:83], s88 offen offset:3072 sc1\n" \
"v_mfma_f32_16x16x32_bf16 a[60:63], a[236:239], a[220:223], a[60:63]\n" \
"v_mfma_f32_16x16x32_bf16 a[52:55], a[164:167], a[156:159], a[52:55]\n" \
"v_mfma_f32_16x16x32_bf16 a[52:55], a[228:231], a[220:223], a[52:55]\n" \
"v_mfma_f32_16x16x32_bf16 a[48:51], a[160:163], a[156:159], a[48:51]\n" \
"v_mfma_f32_16x16x32_bf16 a[48:51], a[224:227], a[220:223], a[48:51]\n" \
"v_mfma_f32_16x16x32_bf16 a[64:67], a[164:167], a[140:143], a[64:67]\n" \
"v_mfma_f32_16x16x32_bf16 a[64:67], a[228:231], a[204:207], a[64:67]\n" \
"v_mfma_f32_16x16x32_bf16 a[80:83], a[172:175], a[140:143], a[80:83]\n" \
"v_mfma_f32_16x16x32_bf16 a[80:83], a[236:239], a[204:207], a[80:83]\n" \
"v_mfma_f32_16x16x32_bf16 a[68:71], a[140:143], a[168:171], a[68:71]\n" \
"v_mfma_f32_16x16x32_bf16 a[68:71], a[204:207], a[232:235], a[68:71]\n" \
"s_waitcnt vmcnt(4) lgkmcnt(0)\n" \
"s_barrier\n" \
"v_mfma_f32_16x16x32_bf16 v[116:119], a[176:179], a[144:147], v[116:119]\n" \
"ds_read_b64_tr_b16 a[160:161], v32 offset:0\n" \
"v_accvgpr_read_b32 v184, a0\n" \
"v_mfma_f32_16x16x32_bf16 v[116:119], a[240:243], a[208:211], v[116:119]\n" \
"v_accvgpr_read_b32 v185, a1\n" \
"v_accvgpr_read_b32 v186, a2\n" \
"v_mfma_f32_16x16x32_bf16 v[120:123], a[180:183], a[144:147], v[120:123]\n" \
"v_accvgpr_read_b32 v187, a3\n" \
"v_accvgpr_read_b32 v188, a4\n" \
"v_mfma_f32_16x16x32_bf16 v[120:123], a[244:247], a[208:211], v[120:123]\n" \
"ds_read_b64_tr_b16 a[162:163], v32 offset:512\n" \
"v_accvgpr_read_b32 v189, a5\n" \
"v_mfma_f32_16x16x32_bf16 v[124:127], a[184:187], a[144:147], v[124:127]\n" \
"v_accvgpr_read_b32 v190, a6\n" \
"v_accvgpr_read_b32 v191, a7\n" \
"v_mfma_f32_16x16x32_bf16 v[124:127], a[248:251], a[208:211], v[124:127]\n" \
"v_accvgpr_read_b32 v196, a12\n" \
"v_accvgpr_read_b32 v197, a13\n" \
"v_mfma_f32_16x16x32_bf16 v[128:131], a[188:191], a[144:147], v[128:131]\n" \
"ds_read_b64_tr_b16 a[224:225], v32 offset:4096\n" \
"v_accvgpr_read_b32 v198, a14\n" \
"v_mfma_f32_16x16x32_bf16 v[128:131], a[252:255], a[208:211], v[128:131]\n" \
"ds_read_b128 a[144:147], v28 offset:8192\n" \
"v_accvgpr_read_b32 v199, a15\n" \
"v_mfma_f32_16x16x32_bf16 v[144:147], a[188:191], a[148:151], v[144:147]\n" \
"ds_read_b128 a[208:211], v29 offset:8192\n" \
"v_accvgpr_read_b32 v192, a8\n" \
"v_mfma_f32_16x16x32_bf16 v[144:147], a[252:255], a[212:215], v[144:147]\n" \
"ds_read_b64_tr_b16 a[226:227], v32 offset:4608\n" \
"v_accvgpr_read_b32 v193, a9\n" \
"v_mfma_f32_16x16x32_bf16 v[140:143], a[184:187], a[148:151], v[140:143]\n" \
"v_accvgpr_read_b32 v194, a10\n" \
"v_accvgpr_read_b32 v195, a11\n" \
"v_mfma_f32_16x16x32_bf16 v[140:143], a[248:251], a[212:215], v[140:143]\n" \
"v_accvgpr_read_b32 v208, a24\n" \
"v_accvgpr_read_b32 v209, a25\n" \
"v_mfma_f32_16x16x32_bf16 v[136:139], a[180:183], a[148:151], v[136:139]\n" \
"v_accvgpr_read_b32 v210, a26\n" \
"v_accvgpr_read_b32 v211, a27\n" \
"v_mfma_f32_16x16x32_bf16 v[136:139], a[244:247], a[212:215], v[136:139]\n" \
"ds_read_b64_tr_b16 a[164:165], v33 offset:0\n" \
"v_accvgpr_read_b32 v212, a28\n" \
"v_mfma_f32_16x16x32_bf16 v[132:135], a[176:179], a[148:151], v[132:135]\n" \
"v_accvgpr_read_b32 v213, a29\n" \
"v_accvgpr_read_b32 v214, a30\n" \
"v_mfma_f32_16x16x32_bf16 v[132:135], a[240:243], a[212:215], v[132:135]\n" \
"ds_read_b128 a[148:151], v28 offset:10240\n" \
"v_accvgpr_read_b32 v215, a31\n" \
"v_mfma_f32_16x16x32_bf16 v[148:151], a[176:179], a[152:155], v[148:151]\n" \
"ds_read_b128 a[212:215], v29 offset:10240\n" \
"v_accvgpr_read_b32 v204, a20\n" \
"v_mfma_f32_16x16x32_bf16 v[148:151], a[240:243], a[216:219], v[148:151]\n" \
"ds_read_b64_tr_b16 a[166:167], v33 offset:512\n" \
"v_accvgpr_read_b32 v205, a21\n" \
"v_mfma_f32_16x16x32_bf16 v[152:155], a[180:183], a[152:155], v[152:155]\n" \
"v_accvgpr_read_b32 v206, a22\n" \
"v_accvgpr_read_b32 v207, a23\n" \
"v_mfma_f32_16x16x32_bf16 v[152:155], a[244:247], a[216:219], v[152:155]\n" \
"ds_read_b64_tr_b16 a[228:229], v33 offset:4096\n" \
"v_accvgpr_read_b32 v200, a16\n" \
"v_mfma_f32_16x16x32_bf16 v[156:159], a[184:187], a[152:155], v[156:159]\n" \
"v_accvgpr_read_b32 v201, a17\n" \
"v_accvgpr_read_b32 v202, a18\n" \
"v_mfma_f32_16x16x32_bf16 v[156:159], a[248:251], a[216:219], v[156:159]\n" \
"v_accvgpr_read_b32 v203, a19\n" \
"v_accvgpr_read_b32 v216, a32\n" \
"v_mfma_f32_16x16x32_bf16 v[160:163], a[188:191], a[152:155], v[160:163]\n" \
"ds_read_b64_tr_b16 a[230:231], v33 offset:4608\n" \
"v_accvgpr_read_b32 v217, a33\n" \
"v_mfma_f32_16x16x32_bf16 v[160:163], a[252:255], a[216:219], v[160:163]\n" \
"ds_read_b128 a[152:155], v28 offset:12288\n" \
"v_accvgpr_read_b32 v218, a34\n" \
"v_mfma_f32_16x16x32_bf16 v[176:179], a[188:191], a[156:159], v[176:179]\n" \
"ds_read_b128 a[216:219], v29 offset:12288\n" \
"v_accvgpr_read_b32 v219, a35\n" \
"v_mfma_f32_16x16x32_bf16 v[176:179], a[252:255], a[220:223], v[176:179]\n" \
"v_accvgpr_read_b32 v220, a36\n" \
"v_accvgpr_read_b32 v221, a37\n" \
"v_mfma_f32_16x16x32_bf16 v[172:175], a[184:187], a[156:159], v[172:175]\n" \
"ds_read_b64_tr_b16 a[172:173], v35 offset:0\n" \
"v_accvgpr_read_b32 v222, a38\n" \
"v_mfma_f32_16x16x32_bf16 v[172:175], a[248:251], a[220:223], v[172:175]\n" \
"v_accvgpr_read_b32 v223, a39\n" \
"v_accvgpr_read_b32 v228, a44\n" \
"v_mfma_f32_16x16x32_bf16 v[168:171], a[180:183], a[156:159], v[168:171]\n" \
"v_accvgpr_read_b32 v229, a45\n" \
"v_accvgpr_read_b32 v230, a46\n" \
"v_mfma_f32_16x16x32_bf16 v[168:171], a[244:247], a[220:223], v[168:171]\n" \
"ds_read_b64_tr_b16 a[174:175], v35 offset:512\n" \
"v_accvgpr_read_b32 v231, a47\n" \
"v_mfma_f32_16x16x32_bf16 v[164:167], a[176:179], a[156:159], v[164:167]\n" \
"v_accvgpr_read_b32 v224, a40\n" \
"v_accvgpr_read_b32 v225, a41\n" \
"v_mfma_f32_16x16x32_bf16 v[164:167], a[240:243], a[220:223], v[164:167]\n" \
"ds_read_b128 a[156:159], v28 offset:14336\n" \
"v_accvgpr_read_b32 v226, a42\n" \
"v_mfma_f32_16x16x32_bf16 v[84:87], a[176:179], a[136:139], v[84:87]\n" \
"ds_read_b128 a[220:223], v29 offset:14336\n" \
"v_accvgpr_read_b32 v227, a43\n" \
"v_mfma_f32_16x16x32_bf16 v[84:87], a[240:243], a[200:203], v[84:87]\n" \
"ds_read_b64_tr_b16 a[236:237], v35 offset:4096\n" \
"v_accvgpr_read_b32 v240, a56\n" \
"v_mfma_f32_16x16x32_bf16 v[88:91], a[180:183], a[136:139], v[88:91]\n" \
"v_accvgpr_read_b32 v241, a57\n" \
"v_accvgpr_read_b32 v242, a58\n" \
"v_mfma_f32_16x16x32_bf16 v[88:91], a[244:247], a[200:203], v[88:91]\n" \
"ds_read_b64_tr_b16 a[238:239], v35 offset:4608\n" \
"v_accvgpr_read_b32 v243, a59\n" \
"v_mfma_f32_16x16x32_bf16 v[92:95], a[184:187], a[136:139], v[92:95]\n" \
"v_accvgpr_read_b32 v244, a60\n" \
"v_accvgpr_read_b32 v245, a61\n" \
"v_mfma_f32_16x16x32_bf16 v[92:95], a[248:251], a[200:203], v[92:95]\n" \
"v_accvgpr_read_b32 v246, a62\n" \
"v_accvgpr_read_b32 v247, a63\n" \
"v_mfma_f32_16x16x32_bf16 v[96:99], a[188:191], a[136:139], v[96:99]\n" \
"v_accvgpr_read_b32 v236, a52\n" \
"v_accvgpr_read_b32 v237, a53\n" \
"v_mfma_f32_16x16x32_bf16 v[96:99], a[252:255], a[200:203], v[96:99]\n" \
"ds_read_b128 a[136:139], v28 offset:4096\n" \
"v_accvgpr_read_b32 v238, a54\n" \
"v_mfma_f32_16x16x32_bf16 v[112:115], a[188:191], a[140:143], v[112:115]\n" \
"ds_read_b128 a[200:203], v29 offset:4096\n" \
"v_accvgpr_read_b32 v239, a55\n" \
"v_mfma_f32_16x16x32_bf16 v[112:115], a[252:255], a[204:207], v[112:115]\n" \
"ds_read_b64_tr_b16 a[168:169], v34 offset:0\n" \
"v_accvgpr_read_b32 v232, a48\n" \
"v_mfma_f32_16x16x32_bf16 v[108:111], a[184:187], a[140:143], v[108:111]\n" \
"ds_read_b64_tr_b16 a[170:171], v34 offset:512\n" \
"v_accvgpr_read_b32 v233, a49\n" \
"v_mfma_f32_16x16x32_bf16 v[108:111], a[248:251], a[204:207], v[108:111]\n" \
"v_accvgpr_read_b32 v234, a50\n" \
"v_accvgpr_read_b32 v235, a51\n" \
"v_mfma_f32_16x16x32_bf16 v[104:107], a[180:183], a[140:143], v[104:107]\n" \
"v_accvgpr_read_b32 v180, a64\n" \
"v_accvgpr_read_b32 v181, a65\n" \
"v_mfma_f32_16x16x32_bf16 v[104:107], a[244:247], a[204:207], v[104:107]\n" \
"ds_read_b64_tr_b16 a[232:233], v34 offset:4096\n" \
"v_accvgpr_read_b32 v182, a66\n" \
"v_mfma_f32_16x16x32_bf16 v[100:103], a[176:179], a[140:143], v[100:103]\n" \
"v_accvgpr_read_b32 v183, a67\n" \
"v_accvgpr_read_b32 v48, a80\n" \
"v_mfma_f32_16x16x32_bf16 v[100:103], a[240:243], a[204:207], v[100:103]\n" \
"ds_read_b128 a[140:143], v28 offset:6144\n" \
"v_accvgpr_read_b32 v49, a81\n" \
"v_mfma_f32_16x16x32_bf16 v[52:55], a[176:179], a[128:131], v[52:55]\n" \
"ds_read_b128 a[204:207], v29 offset:6144\n" \
"v_accvgpr_read_b32 v50, a82\n" \
"v_mfma_f32_16x16x32_bf16 v[52:55], a[240:243], a[192:195], v[52:55]\n" \
"ds_read_b64_tr_b16 a[234:235], v34 offset:4608\n" \
"v_accvgpr_read_b32 v51, a83\n" \
"v_mfma_f32_16x16x32_bf16 v[56:59], a[180:183], a[128:131], v[56:59]\n" \
"v_accvgpr_read_b32 v252, a68\n" \
"v_accvgpr_read_b32 v253, a69\n" \
"v_mfma_f32_16x16x32_bf16 v[56:59], a[244:247], a[192:195], v[56:59]\n" \
"v_accvgpr_read_b32 v254, a70\n" \
"v_accvgpr_read_b32 v255, a71\n" \
"v_mfma_f32_16x16x32_bf16 v[60:63], a[184:187], a[128:131], v[60:63]\n" \
"v_lshrrev_b32_e64 v24, 9, v20\n" \
"v_and_b32_e32 v26, 0x1ff, v20\n" \
"v_mfma_f32_16x16x32_bf16 v[60:63], a[248:251], a[192:195], v[60:63]\n" \
"v_lshl_add_u32 v24, v24, 3, v26\n" \
"v_lshrrev_b32_e64 v25, 8, v20\n" \
"v_mfma_f32_16x16x32_bf16 v[64:67], a[188:191], a[128:131], v[64:67]\n" \
"v_mul_u32_u24_e32 v25, 0x24700, v25\n" \
"v_add_u32_e64 v25, v25, v20\n" \
"v_mfma_f32_16x16x32_bf16 v[64:67], a[252:255], a[192:195], v[64:67]\n" \
"ds_read_b128 a[128:131], v28 offset:0\n" \
"v_lshrrev_b32_e64 v26, 2, v20\n" \
"v_mfma_f32_16x16x32_bf16 v[80:83], a[188:191], a[132:135], v[80:83]\n" \
"ds_read_b128 a[192:195], v29 offset:0\n" \
"v_add_u32_e64 v26, s45, v26\n" \
"v_mfma_f32_16x16x32_bf16 v[80:83], a[252:255], a[196:199], v[80:83]\n" \
"v_add_u32_e32 v27, s46, v20\n" \
"v_add_u32_e32 v21, s43, v27\n" \
"v_mfma_f32_16x16x32_bf16 v[76:79], a[184:187], a[132:135], v[76:79]\n" \
"v_mfma_f32_16x16x32_bf16 v[76:79], a[248:251], a[196:199], v[76:79]\n" \
"v_mfma_f32_16x16x32_bf16 v[72:75], a[180:183], a[132:135], v[72:75]\n" \
"v_mfma_f32_16x16x32_bf16 v[72:75], a[244:247], a[196:199], v[72:75]\n" \
"v_mfma_f32_16x16x32_bf16 v[68:71], a[176:179], a[132:135], v[68:71]\n" \
"v_mfma_f32_16x16x32_bf16 v[68:71], a[240:243], a[196:199], v[68:71]\n" \
"ds_read_b128 a[132:135], v28 offset:2048\n" \
"ds_read_b128 a[196:199], v29 offset:2048\n" \
"s_waitcnt lgkmcnt(15)\n" \
"s_nop 0\n" \
"v_mfma_f32_16x16x32_bf16 v[184:187], a[160:163], a[144:147], v[184:187]\n" \
"v_bfe_u32 v44, v20, 2, 5\n" \
"ds_read_b64_tr_b16 a[176:177], v36 offset:0\n" \
"v_mfma_f32_16x16x32_bf16 v[184:187], a[224:227], a[208:211], v[184:187]\n" \
"buffer_load_dword v44, v44, s[80:83], s38 offen offset:0 sc1\n" \
"v_mfma_f32_16x16x32_bf16 v[188:191], a[164:167], a[144:147], v[188:191]\n" \
"ds_read_b64_tr_b16 a[178:179], v36 offset:1024\n" \
"v_mfma_f32_16x16x32_bf16 v[188:191], a[228:231], a[208:211], v[188:191]\n" \
"ds_read_b64_tr_b16 a[240:241], v36 offset:8192\n" \
"v_mfma_f32_16x16x32_bf16 v[196:199], a[172:175], a[144:147], v[196:199]\n" \
"ds_read_b64_tr_b16 a[242:243], v36 offset:9216\n" \
"s_waitcnt lgkmcnt(15)\n" \
"v_mfma_f32_16x16x32_bf16 v[196:199], a[236:239], a[208:211], v[196:199]\n" \
"s_waitcnt lgkmcnt(12)\n" \
"v_mfma_f32_16x16x32_bf16 v[192:195], a[144:147], a[168:171], v[192:195]\n" \
"ds_read_b64_tr_b16 a[180:181], v37 offset:0\n" \
"s_waitcnt lgkmcnt(9)\n" \
"s_nop 0\n" \
"v_mfma_f32_16x16x32_bf16 v[192:195], a[208:211], a[232:235], v[192:195]\n" \
"v_mfma_f32_16x16x32_bf16 v[208:211], a[148:151], a[168:171], v[208:211]\n" \
"ds_read_b64_tr_b16 a[182:183], v37 offset:1024\n" \
"v_mfma_f32_16x16x32_bf16 v[208:211], a[212:215], a[232:235], v[208:211]\n" \
"ds_read_b64_tr_b16 a[244:245], v37 offset:8192\n" \
"v_mfma_f32_16x16x32_bf16 v[212:215], a[172:175], a[148:151], v[212:215]\n" \
"v_mfma_f32_16x16x32_bf16 v[212:215], a[236:239], a[212:215], v[212:215]\n" \
"ds_read_b64_tr_b16 a[246:247], v37 offset:9216\n" \
"v_cvt_pk_bf16_f32 v188, v188, v189\n" \
"v_mfma_f32_16x16x32_bf16 v[204:207], a[164:167], a[148:151], v[204:207]\n" \
"v_cvt_pk_bf16_f32 v189, v190, v191\n" \
"v_cvt_pk_bf16_f32 v190, v198, v199\n" \
"v_mfma_f32_16x16x32_bf16 v[204:207], a[228:231], a[212:215], v[204:207]\n" \
"ds_read_b64_tr_b16 a[184:185], v38 offset:0\n" \
"v_mov_b32_e32 v191, 0x3f80\n" \
"v_mfma_f32_16x16x32_bf16 v[200:203], a[160:163], a[148:151], v[200:203]\n" \
"ds_read_b64_tr_b16 a[186:187], v38 offset:1024\n" \
"v_cvt_pk_bf16_f32 v184, v184, v185\n" \
"v_mfma_f32_16x16x32_bf16 v[200:203], a[224:227], a[212:215], v[200:203]\n" \
"v_cvt_pk_bf16_f32 v185, v186, v187\n" \
"v_cvt_pk_bf16_f32 v186, v196, v197\n" \
"v_mfma_f32_16x16x32_bf16 v[216:219], a[160:163], a[152:155], v[216:219]\n" \
"ds_read_b64_tr_b16 a[248:249], v38 offset:8192\n" \
"v_mov_b32_e64 v187, 0\n" \
"v_mfma_f32_16x16x32_bf16 v[216:219], a[224:227], a[216:219], v[216:219]\n" \
"ds_read_b64_tr_b16 a[250:251], v38 offset:9216\n" \
"v_mfma_f32_16x16x32_bf16 v[220:223], a[164:167], a[152:155], v[220:223]\n" \
"v_mfma_f32_16x16x32_bf16 v[220:223], a[228:231], a[216:219], v[220:223]\n" \
"ds_read_b64_tr_b16 a[188:189], v39 offset:0\n" \
"v_cvt_pk_bf16_f32 v204, v204, v205\n" \
"v_mfma_f32_16x16x32_bf16 v[228:231], a[172:175], a[152:155], v[228:231]\n" \
"v_cvt_pk_bf16_f32 v205, v206, v207\n" \
"v_cvt_pk_bf16_f32 v206, v214, v215\n" \
"v_mfma_f32_16x16x32_bf16 v[228:231], a[236:239], a[216:219], v[228:231]\n" \
"ds_read_b64_tr_b16 a[190:191], v39 offset:1024\n" \
"v_mov_b32_e32 v207, 0x3f80\n" \
"v_mfma_f32_16x16x32_bf16 v[224:227], a[152:155], a[168:171], v[224:227]\n" \
"ds_read_b64_tr_b16 a[252:253], v39 offset:8192\n" \
"v_cvt_pk_bf16_f32 v200, v200, v201\n" \
"v_mfma_f32_16x16x32_bf16 v[224:227], a[216:219], a[232:235], v[224:227]\n" \
"v_cvt_pk_bf16_f32 v201, v202, v203\n" \
"v_cvt_pk_bf16_f32 v202, v212, v213\n" \
"v_mfma_f32_16x16x32_bf16 v[240:243], a[156:159], a[168:171], v[240:243]\n" \
"ds_read_b64_tr_b16 a[254:255], v39 offset:9216\n" \
"v_mov_b32_e64 v203, 0\n" \
"v_mfma_f32_16x16x32_bf16 v[240:243], a[220:223], a[232:235], v[240:243]\n" \
"v_mfma_f32_16x16x32_bf16 v[244:247], a[172:175], a[156:159], v[244:247]\n" \
"v_mfma_f32_16x16x32_bf16 v[244:247], a[236:239], a[220:223], v[244:247]\n" \
"v_cvt_pk_bf16_f32 v220, v220, v221\n" \
"v_cvt_pk_bf16_f32 v221, v222, v223\n" \
"v_mfma_f32_16x16x32_bf16 v[236:239], a[164:167], a[156:159], v[236:239]\n" \
"v_cvt_pk_bf16_f32 v222, v230, v231\n" \
"v_mov_b32_e32 v223, 0x3f80\n" \
"v_mfma_f32_16x16x32_bf16 v[236:239], a[228:231], a[220:223], v[236:239]\n" \
"v_cvt_pk_bf16_f32 v216, v216, v217\n" \
"v_cvt_pk_bf16_f32 v217, v218, v219\n" \
"v_mfma_f32_16x16x32_bf16 v[232:235], a[160:163], a[156:159], v[232:235]\n" \
"v_cvt_pk_bf16_f32 v218, v228, v229\n" \
"v_mov_b32_e64 v219, 0\n" \
"v_mfma_f32_16x16x32_bf16 v[232:235], a[224:227], a[220:223], v[232:235]\n" \
"v_mfma_f32_16x16x32_bf16 v[180:183], a[164:167], a[140:143], v[180:183]\n" \
"v_mfma_f32_16x16x32_bf16 v[180:183], a[228:231], a[204:207], v[180:183]\n" \
"v_mfma_f32_16x16x32_bf16 v[48:51], a[172:175], a[140:143], v[48:51]\n" \
"v_mfma_f32_16x16x32_bf16 v[48:51], a[236:239], a[204:207], v[48:51]\n" \
"v_cvt_pk_bf16_f32 v236, v236, v237\n" \
"v_cvt_pk_bf16_f32 v237, v238, v239\n" \
"v_mfma_f32_16x16x32_bf16 v[252:255], a[140:143], a[168:171], v[252:255]\n" \
"v_cvt_pk_bf16_f32 v238, v246, v247\n" \
"v_mov_b32_e32 v239, 0x3f80\n" \
"v_mfma_f32_16x16x32_bf16 v[252:255], a[204:207], a[232:235], v[252:255]\n" \
"v_cvt_pk_bf16_f32 v232, v232, v233\n" \
"v_cvt_pk_bf16_f32 v233, v234, v235\n" \
"s_waitcnt vmcnt(0) lgkmcnt(0)\n" \
"s_barrier\n" \
"v_mov_b32_e32 v45, s62\n" \
"s_mov_b64 exec, 1\n" \
"buffer_store_dword v45, off, s[80:83], s37 offset:32 sc1\n" \
"s_mov_b64 exec, -1\n" \
"v_cmp_ne_u32_e32 vcc, s62, v44\n" \
"s_nop 4\n" \
"s_cbranch_vccnz L_poll161_%=\n" \
"L_pollr161_%=:\n" \
"v_mfma_f32_16x16x32_bf16 v[116:119], a[176:179], a[144:147], v[116:119]\n" \
"s_mov_b32 s73, s50\n" \
"s_sub_u32 m0, s31, 0x10000\n" \
"v_mfma_f32_16x16x32_bf16 v[116:119], a[240:243], a[208:211], v[116:119]\n" \
"buffer_load_dwordx4 v20, s[80:83], s73 offen offset:0 sc1 lds\n" \
"v_mfma_f32_16x16x32_bf16 v[120:123], a[180:183], a[144:147], v[120:123]\n" \
"s_add_u32 s74, s50, 0x4000\n" \
"s_add_u32 s75, s50, 0x8000\n" \
"v_mfma_f32_16x16x32_bf16 v[120:123], a[244:247], a[208:211], v[120:123]\n" \
"s_mov_b32 s70, 0x3e0293ee\n" \
"s_mov_b32 s76, s52\n" \
"v_mfma_f32_16x16x32_bf16 v[124:127], a[184:187], a[144:147], v[124:127]\n" \
"v_mul_f32_e32 v116, s70, v116\n" \
"v_mul_f32_e32 v117, s70, v117\n" \
"v_mfma_f32_16x16x32_bf16 v[124:127], a[248:251], a[208:211], v[124:127]\n" \
"buffer_load_dwordx4 v20, s[80:83], s73 offen offset:1024 sc1 lds\n" \
"v_mfma_f32_16x16x32_bf16 v[128:131], a[188:191], a[144:147], v[128:131]\n" \
"v_mul_f32_e32 v118, s70, v118\n" \
"v_mul_f32_e32 v119, s70, v119\n" \
"v_mfma_f32_16x16x32_bf16 v[128:131], a[252:255], a[208:211], v[128:131]\n" \
"v_cvt_pk_bf16_f32 v116, v116, v117\n" \
"v_mul_f32_e64 v120, s70, v120\n" \
"v_mfma_f32_16x16x32_bf16 v[144:147], a[188:191], a[148:151], v[144:147]\n" \
"v_mul_f32_e32 v124, s70, v124\n" \
"v_mul_f32_e32 v125, s70, v125\n" \
"v_mfma_f32_16x16x32_bf16 v[144:147], a[252:255], a[212:215], v[144:147]\n" \
"buffer_load_dwordx4 v20, s[80:83], s73 offen offset:2048 sc1 lds\n" \
"v_mfma_f32_16x16x32_bf16 v[140:143], a[184:187], a[148:151], v[140:143]\n" \
"v_mul_f32_e32 v126, s70, v126\n" \
"v_mul_f32_e32 v127, s70, v127\n" \
"v_mfma_f32_16x16x32_bf16 v[140:143], a[248:251], a[212:215], v[140:143]\n" \
"v_cvt_pk_bf16_f32 v124, v124, v125\n" \
"v_mul_f32_e64 v121, s70, v121\n" \
"v_mfma_f32_16x16x32_bf16 v[136:139], a[180:183], a[148:151], v[136:139]\n" \
"v_mul_f32_e32 v122, s70, v122\n" \
"v_mul_f32_e32 v123, s70, v123\n" \
"v_mfma_f32_16x16x32_bf16 v[136:139], a[244:247], a[212:215], v[136:139]\n" \
"buffer_load_dwordx4 v20, s[80:83], s73 offen offset:3072 sc1 lds\n" \
"v_mfma_f32_16x16x32_bf16 v[132:135], a[176:179], a[148:151], v[132:135]\n" \
"s_sub_u32 m0, s31, s51\n" \
"v_cvt_pk_bf16_f32 v117, v118, v119\n" \
"v_mfma_f32_16x16x32_bf16 v[132:135], a[240:243], a[212:215], v[132:135]\n" \
"v_mul_f32_e32 v128, s70, v128\n" \
"v_mul_f32_e64 v129, s70, v129\n" \
"v_mfma_f32_16x16x32_bf16 v[148:151], a[176:179], a[152:155], v[148:151]\n" \
"v_mul_f32_e32 v130, s70, v130\n" \
"v_mul_f32_e32 v131, s70, v131\n" \
"v_mfma_f32_16x16x32_bf16 v[148:151], a[240:243], a[216:219], v[148:151]\n" \
"buffer_load_dwordx4 v20, s[80:83], s74 offen offset:0 sc1 lds\n" \
"v_mfma_f32_16x16x32_bf16 v[152:155], a[180:183], a[152:155], v[152:155]\n" \
"v_cvt_pk_bf16_f32 v125, v126, v127\n" \
"v_cvt_pk_bf16_f32 v118, v120, v121\n" \
"v_mfma_f32_16x16x32_bf16 v[152:155], a[244:247], a[216:219], v[152:155]\n" \
"v_cvt_pk_bf16_f32 v119, v122, v123\n" \
"v_cvt_pk_bf16_f32 v126, v128, v129\n" \
"v_mfma_f32_16x16x32_bf16 v[156:159], a[184:187], a[152:155], v[156:159]\n" \
"v_cvt_pk_bf16_f32 v127, v130, v131\n" \
"v_mul_f32_e64 v132, s70, v132\n" \
"v_mfma_f32_16x16x32_bf16 v[156:159], a[248:251], a[216:219], v[156:159]\n" \
"buffer_load_dwordx4 v20, s[80:83], s74 offen offset:1024 sc1 lds\n" \
"v_mfma_f32_16x16x32_bf16 v[160:163], a[188:191], a[152:155], v[160:163]\n" \
"ds_write_b128 v21, v[116:119] offset:16384\n" \
"v_mfma_f32_16x16x32_bf16 v[160:163], a[252:255], a[216:219], v[160:163]\n" \
"v_mul_f32_e32 v133, s70, v133\n" \
"v_mul_f32_e32 v140, s70, v140\n" \
"v_mfma_f32_16x16x32_bf16 v[176:179], a[188:191], a[156:159], v[176:179]\n" \
"v_mul_f32_e32 v141, s70, v141\n" \
"v_mul_f32_e32 v134, s70, v134\n" \
"v_mfma_f32_16x16x32_bf16 v[176:179], a[252:255], a[220:223], v[176:179]\n" \
"buffer_load_dwordx4 v20, s[80:83], s74 offen offset:2048 sc1 lds\n" \
"v_mfma_f32_16x16x32_bf16 v[172:175], a[184:187], a[156:159], v[172:175]\n" \
"ds_write_b128 v21, v[124:127] offset:17408\n" \
"v_mfma_f32_16x16x32_bf16 v[172:175], a[248:251], a[220:223], v[172:175]\n" \
"v_mul_f32_e64 v135, s70, v135\n" \
"v_cvt_pk_bf16_f32 v132, v132, v133\n" \
"v_mfma_f32_16x16x32_bf16 v[168:171], a[180:183], a[156:159], v[168:171]\n" \
"v_mul_f32_e32 v142, s70, v142\n" \
"v_mul_f32_e32 v143, s70, v143\n" \
"v_mfma_f32_16x16x32_bf16 v[168:171], a[244:247], a[220:223], v[168:171]\n" \
"buffer_load_dwordx4 v20, s[80:83], s74 offen offset:3072 sc1 lds\n" \
"v_mfma_f32_16x16x32_bf16 v[164:167], a[176:179], a[156:159], v[164:167]\n" \
"s_sub_u32 m0, s31, 0x4000\n" \
"v_cvt_pk_bf16_f32 v140, v140, v141\n" \
"v_mfma_f32_16x16x32_bf16 v[164:167], a[240:243], a[220:223], v[164:167]\n" \
"v_mul_f32_e32 v136, s70, v136\n" \
"v_mul_f32_e32 v137, s70, v137\n" \
"v_mfma_f32_16x16x32_bf16 v[84:87], a[176:179], a[136:139], v[84:87]\n" \
"v_mul_f32_e32 v138, s70, v138\n" \
"v_mul_f32_e32 v139, s70, v139\n" \
"v_mfma_f32_16x16x32_bf16 v[84:87], a[240:243], a[200:203], v[84:87]\n" \
"buffer_load_dwordx4 v20, s[80:83], s75 offen offset:0 sc1 lds\n" \
"v_mfma_f32_16x16x32_bf16 v[88:91], a[180:183], a[136:139], v[88:91]\n" \
"v_cvt_pk_bf16_f32 v133, v134, v135\n" \
"v_mul_f32_e64 v144, s70, v144\n" \
"v_mfma_f32_16x16x32_bf16 v[88:91], a[244:247], a[200:203], v[88:91]\n" \
"v_mul_f32_e32 v145, s70, v145\n" \
"v_mul_f32_e32 v146, s70, v146\n" \
".fill (L_pb + 77536 - .) / 4, 4, 0xBF800000\n" \
"L_pp19:\n" \
"s_cbranch_execz L_pret\n" \
"v_mfma_f32_16x16x32_bf16 v[92:95], a[184:187], a[136:139], v[92:95]\n" \
"v_mul_f32_e32 v147, s70, v147\n" \
"v_cvt_pk_bf16_f32 v141, v142, v143\n" \
"v_mfma_f32_16x16x32_bf16 v[92:95], a[248:251], a[200:203], v[92:95]\n" \
"buffer_load_dwordx4 v20, s[80:83], s75 offen offset:1024 sc1 lds\n" \
"v_mfma_f32_16x16x32_bf16 v[96:99], a[188:191], a[136:139], v[96:99]\n" \
"v_cvt_pk_bf16_f32 v134, v136, v137\n" \
"v_cvt_pk_bf16_f32 v135, v138, v139\n" \
"v_mfma_f32_16x16x32_bf16 v[96:99], a[252:255], a[200:203], v[96:99]\n" \
"v_cvt_pk_bf16_f32 v142, v144, v145\n" \
"v_cvt_pk_bf16_f32 v143, v146, v147\n" \
"v_mfma_f32_16x16x32_bf16 v[112:115], a[188:191], a[140:143], v[112:115]\n" \
"ds_write_b128 v21, v[132:135] offset:20480\n" \
"v_mfma_f32_16x16x32_bf16 v[112:115], a[252:255], a[204:207], v[112:115]\n" \
"buffer_load_dwordx4 v20, s[80:83], s75 offen offset:2048 sc1 lds\n" \
"v_mfma_f32_16x16x32_bf16 v[108:111], a[184:187], a[140:143], v[108:111]\n" \
"v_cvt_pk_bf16_f32 v180, v180, v181\n" \
"v_cvt_pk_bf16_f32 v181, v182, v183\n" \
"v_mfma_f32_16x16x32_bf16 v[108:111], a[248:251], a[204:207], v[108:111]\n" \
"ds_write_b128 v21, v[140:143] offset:21504\n" \
"v_mfma_f32_16x16x32_bf16 v[104:107], a[180:183], a[140:143], v[104:107]\n" \
"v_cvt_pk_bf16_f32 v234, v244, v245\n" \
"v_mov_b32_e64 v235, 0\n" \
"v_mfma_f32_16x16x32_bf16 v[104:107], a[244:247], a[204:207], v[104:107]\n" \
"buffer_load_dwordx4 v20, s[80:83], s75 offen offset:3072 sc1 lds\n" \
"v_mfma_f32_16x16x32_bf16 v[100:103], a[176:179], a[140:143], v[100:103]\n" \
"s_mov_b32 m0, s53\n" \
"s_nop 0\n" \
"v_cvt_pk_bf16_f32 v182, v50, v51\n" \
"v_mfma_f32_16x16x32_bf16 v[100:103], a[240:243], a[204:207], v[100:103]\n" \
"v_mov_b32_e32 v183, 0x3f80\n" \
"v_cvt_pk_bf16_f32 v118, v192, v193\n" \
"v_mfma_f32_16x16x32_bf16 v[52:55], a[176:179], a[128:131], v[52:55]\n" \
"v_cvt_pk_bf16_f32 v119, v194, v195\n" \
"v_cvt_pk_bf16_f32 v120, v208, v209\n" \
"v_mfma_f32_16x16x32_bf16 v[52:55], a[240:243], a[192:195], v[52:55]\n" \
"buffer_load_dwordx4 v20, s[80:83], s76 offen offset:0 sc1 lds\n" \
"v_mfma_f32_16x16x32_bf16 v[56:59], a[180:183], a[128:131], v[56:59]\n" \
"v_cvt_pk_bf16_f32 v121, v210, v211\n" \
"v_cvt_pk_bf16_f32 v122, v224, v225\n" \
"v_mfma_f32_16x16x32_bf16 v[56:59], a[244:247], a[192:195], v[56:59]\n" \
"v_cvt_pk_bf16_f32 v123, v226, v227\n" \
"v_cvt_pk_bf16_f32 v124, v240, v241\n" \
"v_cvt_pk_bf16_f32 v125, v242, v243\n" \
"v_mfma_f32_16x16x32_bf16 v[60:63], a[184:187], a[128:131], v[60:63]\n" \
"v_mfma_f32_16x16x32_bf16 v[192:195], v[188:191], v[184:187], 0\n" \
"buffer_load_dwordx4 v20, s[80:83], s76 offen offset:1024 sc1 lds\n" \
"v_mfma_f32_16x16x32_bf16 v[32:35], v[180:183], v[184:187], 0\n" \
"v_mfma_f32_16x16x32_bf16 v[208:211], v[204:207], v[200:203], 0\n" \
"v_mfma_f32_16x16x32_bf16 v[36:39], v[188:191], v[200:203], 0\n" \
"v_mfma_f32_16x16x32_bf16 v[224:227], v[220:223], v[216:219], 0\n" \
"v_mfma_f32_16x16x32_bf16 v[248:251], v[204:207], v[216:219], 0\n" \
"v_mfma_f32_16x16x32_bf16 v[240:243], v[236:239], v[232:235], 0\n" \
"v_mfma_f32_16x16x32_bf16 v[128:131], v[220:223], v[232:235], 0\n" \
"s_mov_b32 s64, 0xaaaaaaaa\n" \
"s_mov_b32 s65, 0xaaaaaaaa\n" \
"v_mfma_f32_16x16x32_bf16 v[60:63], a[248:251], a[192:195], v[60:63]\n" \
"s_mov_b32 s66, 0xfff0ffff\n" \
"s_mov_b32 s67, 0xf000ff00\n" \
"v_mfma_f32_16x16x32_bf16 v[64:67], a[188:191], a[128:131], v[64:67]\n" \
"s_mov_b32 s68, 0xffc0fffc\n" \
"s_mov_b32 s69, 0xc000fc00\n" \
"v_cndmask_b32_e64 v192, v192, v193, s[64:65]\n" \
"v_cndmask_b32_e64 v32, v32, v33, s[64:65]\n" \
"v_cndmask_b32_e64 v194, v194, v195, s[64:65]\n" \
"v_cndmask_b32_e64 v34, v34, v35, s[64:65]\n" \
"v_cndmask_b32_e64 v208, v208, v209, s[64:65]\n" \
"v_cndmask_b32_e64 v36, v36, v37, s[64:65]\n" \
"v_cndmask_b32_e64 v210, v210, v211, s[64:65]\n" \
"v_cndmask_b32_e64 v38, v38, v39, s[64:65]\n" \
"v_cndmask_b32_e64 v224, v224, v225, s[64:65]\n" \
"v_cndmask_b32_e64 v248, v248, v249, s[64:65]\n" \
"v_cndmask_b32_e64 v226, v226, v227, s[64:65]\n" \
"v_cndmask_b32_e64 v250, v250, v251, s[64:65]\n" \
"v_cndmask_b32_e64 v240, v240, v241, s[64:65]\n" \
"v_cndmask_b32_e64 v128, v128, v129, s[64:65]\n" \
"v_cndmask_b32_e64 v242, v242, v243, s[64:65]\n" \
"v_cndmask_b32_e64 v130, v130, v131, s[64:65]\n" \
"v_cndmask_b32_e64 v192, v32, v192, s[66:67]\n" \
"v_cndmask_b32_e64 v194, v34, v194, s[68:69]\n" \
"v_cndmask_b32_e64 v208, v36, v208, s[66:67]\n" \
"v_cndmask_b32_e64 v210, v38, v210, s[68:69]\n" \
"v_cndmask_b32_e64 v224, v248, v224, s[66:67]\n" \
"v_cndmask_b32_e64 v226, v250, v226, s[68:69]\n" \
"v_cndmask_b32_e64 v240, v128, v240, s[66:67]\n" \
"v_cndmask_b32_e64 v242, v130, v242, s[68:69]\n" \
"v_mfma_f32_16x16x32_bf16 v[64:67], a[252:255], a[192:195], v[64:67]\n" \
"v_max_f32_e32 v193, v192, v194\n" \
"v_max_f32_e32 v209, v208, v210\n" \
"v_max_f32_e32 v225, v224, v226\n" \
"v_max_f32_e32 v241, v240, v242\n" \
"v_mul_f32_e32 v148, s70, v148\n" \
"v_permlane32_swap_b32_e32 v193, v225\n" \
"v_permlane32_swap_b32_e32 v209, v241\n" \
"v_mul_f32_e32 v149, s70, v149\n" \
"v_mul_f32_e32 v156, s70, v156\n" \
"v_mul_f32_e32 v157, s70, v157\n" \
"v_permlane32_swap_b32_e32 v196, v228\n" \
"v_permlane32_swap_b32_e32 v212, v244\n" \
"v_max_f32_e32 v193, v193, v225\n" \
"v_max_f32_e32 v209, v209, v241\n" \
"v_mul_f32_e32 v150, s70, v150\n" \
"v_mul_f32_e32 v151, s70, v151\n" \
"v_cvt_pk_bf16_f32 v148, v148, v149\n" \
"v_mul_f32_e32 v158, s70, v158\n" \
"v_mul_f32_e32 v159, s70, v159\n" \
"v_cvt_pk_bf16_f32 v156, v156, v157\n" \
"v_permlane16_swap_b32_e32 v228, v244\n" \
"v_permlane16_swap_b32_e32 v193, v209\n" \
"v_mfma_f32_16x16x32_bf16 v[80:83], a[188:191], a[132:135], v[80:83]\n" \
"s_mov_b32 s72, 0x3eae1a93\n" \
"v_mul_f32_e32 v152, s70, v152\n" \
"v_mul_f32_e32 v153, s70, v153\n" \
"v_mul_f32_e32 v154, s70, v154\n" \
"v_mul_f32_e32 v155, s70, v155\n" \
"v_cvt_pk_bf16_f32 v149, v150, v151\n" \
"v_mul_f32_e32 v160, s70, v160\n" \
"v_mul_f32_e32 v161, s70, v161\n" \
"v_mul_f32_e32 v162, s70, v162\n" \
"v_mul_f32_e32 v163, s70, v163\n" \
"v_cvt_pk_bf16_f32 v157, v158, v159\n" \
"v_mul_f32_e32 v45, 0xbfb8aa3b, v228\n" \
"v_mfma_f32_16x16x16_bf16 v[48:51], v[182:183], v[46:47], 0\n" \
"v_mfma_f32_16x16x16_bf16 v[196:199], v[190:191], v[46:47], 0\n" \
"v_mfma_f32_16x16x16_bf16 v[212:215], v[206:207], v[46:47], 0\n" \
"v_max_f32_e64 v193, v193, v209\n" \
"v_cvt_pk_bf16_f32 v150, v152, v153\n" \
"v_cvt_pk_bf16_f32 v151, v154, v155\n" \
"v_cvt_pk_bf16_f32 v158, v160, v161\n" \
"v_cvt_pk_bf16_f32 v159, v162, v163\n" \
"v_mfma_f32_16x16x16_bf16 v[228:231], v[222:223], v[46:47], 0\n" \
"v_mul_f32_e32 v193, s72, v193\n" \
"v_mov_b32_e32 v209, v193\n" \
"ds_write_b128 v21, v[148:151] offset:24576\n" \
"v_mfma_f32_16x16x16_bf16 v[244:247], v[238:239], v[46:47], 0\n" \
"v_permlane16_swap_b32_e32 v193, v209\n" \
"s_mov_b32 s71, 0x3e0293ee\n" \
"v_mfma_f32_16x16x32_bf16 v[80:83], a[252:255], a[196:199], v[80:83]\n" \
"v_mov_b32_e32 v225, v193\n" \
"v_mov_b32_e32 v241, v209\n" \
"v_mul_f32_e32 v164, s70, v164\n" \
"v_mul_f32_e64 v165, s70, v165\n" \
"ds_write_b128 v21, v[156:159] offset:25600\n" \
"v_mul_f32_e32 v172, s70, v172\n" \
"v_mul_f32_e32 v173, s70, v173\n" \
"v_pk_mul_f32 v[84:85], v[84:85], s[70:71]\n" \
"v_pk_mul_f32 v[92:93], v[92:93], s[70:71]\n" \
"v_pk_mul_f32 v[100:101], v[100:101], s[70:71]\n" \
"v_pk_mul_f32 v[108:109], v[108:109], s[70:71]\n" \
"v_pk_mul_f32 v[52:53], v[52:53], s[70:71]\n" \
"v_pk_mul_f32 v[60:61], v[60:61], s[70:71]\n" \
"v_permlane32_swap_b32_e32 v193, v225\n" \
"v_permlane32_swap_b32_e32 v209, v241\n" \
"v_mul_f32_e32 v166, s70, v166\n" \
"v_mul_f32_e32 v167, s70, v167\n" \
"v_cvt_pk_bf16_f32 v164, v164, v165\n" \
"v_mul_f32_e32 v174, s70, v174\n" \
"v_mul_f32_e32 v175, s70, v175\n" \
"v_cvt_pk_bf16_f32 v172, v172, v173\n" \
"v_pk_mul_f32 v[86:87], v[86:87], s[70:71]\n" \
"v_cvt_pk_bf16_f32 v84, v84, v85\n" \
"v_pk_mul_f32 v[94:95], v[94:95], s[70:71]\n" \
"v_cvt_pk_bf16_f32 v92, v92, v93\n" \
"v_pk_mul_f32 v[102:103], v[102:103], s[70:71]\n" \
"v_cvt_pk_bf16_f32 v100, v100, v101\n" \
"v_pk_mul_f32 v[110:111], v[110:111], s[70:71]\n" \
"v_cvt_pk_bf16_f32 v108, v108, v109\n" \
"v_pk_mul_f32 v[54:55], v[54:55], s[70:71]\n" \
"v_cvt_pk_bf16_f32 v52, v52, v53\n" \
"v_pk_mul_f32 v[62:63], v[62:63], s[70:71]\n" \
"v_cvt_pk_bf16_f32 v60, v60, v61\n" \
"v_fma_f32 v192, v192, s72, -v193\n" \
"v_fma_f32 v194, v194, s72, -v193\n" \
"v_fma_f32 v208, v208, s72, -v209\n" \
"v_fma_f32 v210, v210, s72, -v209\n" \
"v_mul_f32_e32 v168, s70, v168\n" \
"v_mul_f32_e32 v169, s70, v169\n" \
"v_mul_f32_e32 v170, s70, v170\n" \
"v_mul_f32_e32 v171, s70, v171\n" \
"v_cvt_pk_bf16_f32 v165, v166, v167\n" \
"v_mul_f32_e32 v176, s70, v176\n" \
"v_mul_f32_e32 v177, s70, v177\n" \
"v_mul_f32_e32 v178, s70, v178\n" \
"v_mul_f32_e32 v179, s70, v179\n" \
"v_cvt_pk_bf16_f32 v173, v174, v175\n" \
"v_pk_mul_f32 v[88:89], v[88:89], s[70:71]\n" \
"v_pk_mul_f32 v[90:91], v[90:91], s[70:71]\n" \
"v_cvt_pk_bf16_f32 v85, v86, v87\n" \
"v_pk_mul_f32 v[96:97], v[96:97], s[70:71]\n" \
"v_pk_mul_f32 v[98:99], v[98:99], s[70:71]\n" \
"v_cvt_pk_bf16_f32 v93, v94, v95\n" \
"v_pk_mul_f32 v[104:105], v[104:105], s[70:71]\n" \
"v_pk_mul_f32 v[106:107], v[106:107], s[70:71]\n" \
"v_cvt_pk_bf16_f32 v101, v102, v103\n" \
"v_pk_mul_f32 v[112:113], v[112:113], s[70:71]\n" \
"v_pk_mul_f32 v[114:115], v[114:115], s[70:71]\n" \
"v_cvt_pk_bf16_f32 v109, v110, v111\n" \
"v_pk_mul_f32 v[56:57], v[56:57], s[70:71]\n" \
"v_pk_mul_f32 v[58:59], v[58:59], s[70:71]\n" \
"v_cvt_pk_bf16_f32 v53, v54, v55\n" \
"v_pk_mul_f32 v[64:65], v[64:65], s[70:71]\n" \
"v_pk_mul_f32 v[66:67], v[66:67], s[70:71]\n" \
"v_cvt_pk_bf16_f32 v61, v62, v63\n" \
"v_pk_mul_f32 v[80:81], v[80:81], s[70:71]\n" \
"v_pk_mul_f32 v[82:83], v[82:83], s[70:71]\n" \
"v_exp_f32_e32 v192, v192\n" \
"v_exp_f32_e32 v194, v194\n" \
"v_exp_f32_e32 v208, v208\n" \
"v_exp_f32_e32 v210, v210\n" \
"v_fma_f32 v224, v224, s72, -v225\n" \
"v_fma_f32 v226, v226, s72, -v225\n" \
"v_mfma_f32_16x16x32_bf16 v[76:79], a[184:187], a[132:135], v[76:79]\n" \
"v_cvt_pk_bf16_f32 v166, v168, v169\n" \
"v_cvt_pk_bf16_f32 v167, v170, v171\n" \
"v_cvt_pk_bf16_f32 v174, v176, v177\n" \
"v_cvt_pk_bf16_f32 v175, v178, v179\n" \
"v_cvt_pk_bf16_f32 v86, v88, v89\n" \
"v_cvt_pk_bf16_f32 v87, v90, v91\n" \
"v_cvt_pk_bf16_f32 v94, v96, v97\n" \
"v_cvt_pk_bf16_f32 v95, v98, v99\n" \
"v_cvt_pk_bf16_f32 v102, v104, v105\n" \
"v_cvt_pk_bf16_f32 v103, v106, v107\n" \
"v_cvt_pk_bf16_f32 v110, v112, v113\n" \
"v_cvt_pk_bf16_f32 v111, v114, v115\n" \
"v_cvt_pk_bf16_f32 v54, v56, v57\n" \
"v_cvt_pk_bf16_f32 v55, v58, v59\n" \
"v_cvt_pk_bf16_f32 v62, v64, v65\n" \
"v_cvt_pk_bf16_f32 v63, v66, v67\n" \
"v_exp_f32_e32 v224, v224\n" \
"v_exp_f32_e32 v226, v226\n" \
"v_fma_f32 v240, v240, s72, -v241\n" \
"v_fma_f32 v242, v242, s72, -v241\n" \
"ds_write_b128 v21, v[164:167] offset:28672\n" \
"v_cvt_pk_bf16_f32 v192, v192, v194\n" \
"v_cvt_pk_bf16_f32 v208, v208, v210\n" \
"v_exp_f32_e32 v240, v240\n" \
"v_exp_f32_e32 v242, v242\n" \
"v_cvt_pk_bf16_f32 v132, v48, v49\n" \
"v_cvt_pk_bf16_f32 v133, v50, v51\n" \
"v_cvt_pk_bf16_f32 v134, v196, v197\n" \
"v_cvt_pk_bf16_f32 v135, v198, v199\n" \
"ds_write_b128 v21, v[172:175] offset:29696\n" \
"v_cvt_pk_bf16_f32 v136, v212, v213\n" \
"v_cvt_pk_bf16_f32 v137, v214, v215\n" \
"v_perm_b32 v184, v192, v192, v40\n" \
"v_perm_b32 v185, v192, v192, v41\n" \
"v_perm_b32 v186, v192, v192, v42\n" \
"v_perm_b32 v187, v192, v192, v43\n" \
"v_perm_b32 v200, v208, v208, v40\n" \
"v_perm_b32 v201, v208, v208, v41\n" \
"v_perm_b32 v202, v208, v208, v42\n" \
"v_perm_b32 v203, v208, v208, v43\n" \
"ds_write_b128 v27, v[84:87] offset:8192\n" \
"v_cvt_pk_bf16_f32 v224, v224, v226\n" \
"v_cvt_pk_bf16_f32 v138, v228, v229\n" \
"v_cvt_pk_bf16_f32 v139, v230, v231\n" \
"v_mfma_f32_16x16x32_bf16 v[32:35], v[132:135], v[184:187], 0\n" \
"v_mfma_f32_16x16x32_bf16 v[36:39], v[134:137], v[200:203], 0\n" \
"v_perm_b32 v216, v224, v224, v40\n" \
"v_perm_b32 v217, v224, v224, v41\n" \
"ds_write_b128 v27, v[92:95] offset:9216\n" \
"v_perm_b32 v218, v224, v224, v42\n" \
"v_perm_b32 v219, v224, v224, v43\n" \
"v_cvt_pk_bf16_f32 v240, v240, v242\n" \
"v_mfma_f32_16x16x32_bf16 v[76:79], a[248:251], a[196:199], v[76:79]\n" \
"v_cvt_pk_bf16_f32 v140, v244, v245\n" \
"v_cvt_pk_bf16_f32 v141, v246, v247\n" \
"v_mfma_f32_16x16x32_bf16 v[248:251], v[136:139], v[216:219], 0\n" \
"v_perm_b32 v232, v240, v240, v40\n" \
"v_perm_b32 v233, v240, v240, v41\n" \
"ds_write_b128 v27, v[100:103] offset:12288\n" \
"v_perm_b32 v234, v240, v240, v42\n" \
"v_perm_b32 v235, v240, v240, v43\n" \
"v_pk_mul_f32 v[76:77], v[76:77], s[70:71]\n" \
"v_pk_mul_f32 v[78:79], v[78:79], s[70:71]\n" \
"v_cvt_pk_bf16_f32 v76, v76, v77\n" \
"v_cvt_pk_bf16_f32 v77, v78, v79\n" \
"v_cvt_pk_bf16_f32 v78, v80, v81\n" \
"v_cvt_pk_bf16_f32 v79, v82, v83\n" \
"v_mfma_f32_16x16x32_bf16 v[128:131], v[138:141], v[232:235], 0\n" \
"ds_write_b128 v27, v[108:111] offset:13312\n" \
"v_mfma_f32_16x16x32_bf16 v[72:75], a[180:183], a[132:135], v[72:75]\n" \
"v_cvt_pk_bf16_f32 v116, v252, v253\n" \
"v_cvt_pk_bf16_f32 v117, v254, v255\n" \
"v_mfma_f32_16x16x32_bf16 v[72:75], a[244:247], a[196:199], v[72:75]\n" \
"v_exp_f32_e64 v45, v45\n" \
"v_mfma_f32_16x16x32_bf16 v[192:195], v[116:119], v[184:187], 0\n" \
"ds_write_b128 v27, v[52:55] offset:0\n" \
"v_mfma_f32_16x16x32_bf16 v[208:211], v[118:121], v[200:203], 0\n" \
"v_mfma_f32_16x16x32_bf16 v[224:227], v[120:123], v[216:219], 0\n" \
"v_mfma_f32_16x16x32_bf16 v[240:243], v[122:125], v[232:235], 0\n" \
"ds_write_b128 v27, v[60:63] offset:1024\n" \
"v_mov_b32_dpp v34, v38 quad_perm:[0,1,2,3] row_mask:0x2 bank_mask:0xf\n" \
"v_pk_mul_f32 v[72:73], v[72:73], s[70:71]\n" \
"v_pk_mul_f32 v[74:75], v[74:75], s[70:71]\n" \
"v_mov_b32_dpp v34, v250 quad_perm:[0,1,2,3] row_mask:0x4 bank_mask:0xf\n" \
"v_add_f32_e64 v45, 1.0, v45\n" \
"v_mfma_f32_16x16x32_bf16 v[68:71], a[176:179], a[132:135], v[68:71]\n" \
"v_mov_b32_dpp v34, v130 quad_perm:[0,1,2,3] row_mask:0x8 bank_mask:0xf\n" \
"v_mov_b32_dpp v32, v36 quad_perm:[0,1,2,3] row_mask:0x2 bank_mask:0xf\n" \
"v_rcp_f32_e64 v45, v45\n" \
"ds_write_b128 v27, v[76:79] offset:5120\n" \
"v_mov_b32_dpp v33, v37 quad_perm:[0,1,2,3] row_mask:0x2 bank_mask:0xf\n" \
"v_rcp_f32_e64 v34, v34\n" \
"v_mov_b32_dpp v32, v248 quad_perm:[0,1,2,3] row_mask:0x4 bank_mask:0xf\n" \
"v_mov_b32_dpp v33, v249 quad_perm:[0,1,2,3] row_mask:0x4 bank_mask:0xf\n" \
"v_mul_f32_e64 v34, v45, v34\n" \
"v_mov_b32_dpp v32, v128 quad_perm:[0,1,2,3] row_mask:0x8 bank_mask:0xf\n" \
"v_mov_b32_dpp v33, v129 quad_perm:[0,1,2,3] row_mask:0x8 bank_mask:0xf\n" \
"v_mul_f32_e32 v32, v34, v32\n" \
"v_mul_f32_e32 v33, v34, v33\n" \
"v_mov_b32_e64 v38, v34\n" \
"v_mfma_f32_16x16x32_bf16 v[68:71], a[240:243], a[196:199], v[68:71]\n" \
"v_sub_f32_e32 v44, 1.0, v45\n" \
"v_permlane16_swap_b32_e32 v34, v38\n" \
"v_mov_b32_e32 v250, v34\n" \
"v_mov_b32_e32 v130, v38\n" \
"v_cvt_pk_bf16_f32 v32, v32, v33\n" \
"v_permlane32_swap_b32_e32 v34, v250\n" \
"v_permlane32_swap_b32_e32 v38, v130\n" \
"v_mul_f32_e64 v192, v34, v192\n" \
"v_pk_mul_f32 v[68:69], v[68:69], s[70:71]\n" \
"v_pk_mul_f32 v[70:71], v[70:71], s[70:71]\n" \
"v_cvt_pk_bf16_f32 v68, v68, v69\n" \
"v_cvt_pk_bf16_f32 v69, v70, v71\n" \
"v_cvt_pk_bf16_f32 v70, v72, v73\n" \
"v_cvt_pk_bf16_f32 v71, v74, v75\n" \
"ds_write_b128 v27, v[68:71] offset:4096\n" \
"v_mul_f32_e32 v193, v34, v193\n" \
"v_mul_f32_e32 v208, v38, v208\n" \
"v_mul_f32_e32 v209, v38, v209\n" \
"v_mul_f32_e32 v224, v250, v224\n" \
"v_mul_f32_e32 v225, v250, v225\n" \
"v_mul_f32_e32 v240, v130, v240\n" \
"v_mul_f32_e32 v241, v130, v241\n" \
"v_mul_f32_e32 v194, v34, v194\n" \
"v_mul_f32_e64 v195, v34, v195\n" \
"v_cvt_pk_bf16_f32 v192, v192, v193\n" \
"v_mul_f32_e32 v210, v38, v210\n" \
"v_mul_f32_e32 v211, v38, v211\n" \
"v_cvt_pk_bf16_f32 v208, v208, v209\n" \
"v_mul_f32_e32 v226, v250, v226\n" \
"v_mul_f32_e32 v227, v250, v227\n" \
"v_cvt_pk_bf16_f32 v224, v224, v225\n" \
"v_mul_f32_e32 v242, v130, v242\n" \
"v_mul_f32_e32 v243, v130, v243\n" \
"v_cvt_pk_bf16_f32 v240, v240, v241\n" \
"v_cvt_pk_bf16_f32 v193, v194, v195\n" \
"s_add_u32 s84, s48, 0x0\n" \
"s_nop 0\n" \
"v_cvt_pk_bf16_f32 v209, v210, v211\n" \
"s_add_u32 s85, s48, 0x24800\n" \
"v_cvt_pk_bf16_f32 v225, v226, v227\n" \
"s_add_u32 s86, s48, 0x49000\n" \
"v_cvt_pk_bf16_f32 v241, v242, v243\n" \
"s_add_u32 s87, s48, 0x6d800\n" \
"ds_write_b32 v26, v44\n" \
"buffer_store_dword v32, v25, s[80:83], s49 offen sc1\n" \
"buffer_store_dwordx2 v[192:193], v24, s[80:83], s84 offen sc1\n" \
"buffer_store_dwordx2 v[208:209], v24, s[80:83], s85 offen sc1\n" \
"buffer_store_dwordx2 v[224:225], v24, s[80:83], s86 offen sc1\n" \
"buffer_store_dwordx2 v[240:241], v24, s[80:83], s87 offen sc1\n" \
"s_branch L_end_%=\n" \
"L_poll61_%=:\n" \
"s_mov_b32 s84, 0\n" \
"L_pl61_%=:\n" \
"s_sleep 1\n" \
"v_bfe_u32 v44, v20, 2, 5\n" \
"buffer_load_dword v44, v44, s[80:83], s38 offen offset:128 sc1\n" \
"s_waitcnt vmcnt(0)\n" \
"v_cmp_ne_u32_e32 vcc, s62, v44\n" \
"s_add_u32 s84, s84, 1\n" \
"s_nop 3\n" \
"s_cbranch_vccz L_pollr61_%=\n" \
"s_cmp_gt_u32 s84, 0x400000\n" \
"s_cbranch_scc0 L_pl61_%=\n" \
"s_branch L_pollr61_%=\n" \
"L_poll161_%=:\n" \
"s_mov_b32 s84, 0\n" \
"L_pl161_%=:\n" \
"s_sleep 1\n" \
"v_bfe_u32 v44, v20, 2, 5\n" \
"buffer_load_dword v44, v44, s[80:83], s38 offen offset:0 sc1\n" \
"s_waitcnt vmcnt(0)\n" \
"v_cmp_ne_u32_e32 vcc, s62, v44\n" \
"s_add_u32 s84, s84, 1\n" \
"s_nop 3\n" \
"s_cbranch_vccz L_pollr161_%=\n" \
"s_cmp_gt_u32 s84, 0x400000\n" \
"s_cbranch_scc0 L_pl161_%=\n" \
"s_branch L_pollr161_%=\n" \
"L_end_%=:\n"
#define PROJ_VCLOB "v21","v24","v25","v28","v29","v32","v33","v34","v35","v36","v37","v38","v39","v40","v41","v42","v43","v44","v45","v46","v47","v48","v49","v50","v51","v52","v53","v54","v55","v56","v57","v58","v59","v60","v61","v62","v63","v64","v65","v66","v67","v68","v69","v70","v71","v72","v73","v74","v75","v76","v77","v78","v79","v80","v81","v82","v83","v84","v85","v86","v87","v88","v89","v90","v91","v92","v93","v94","v95","v96","v97","v98","v99","v100","v101","v102","v103","v104","v105","v106","v107","v108","v109","v110","v111","v112","v113","v114","v115","v116","v117","v118","v119","v120","v121","v122","v123","v124","v125","v126","v127","v128","v129","v130","v131","v132","v133","v134","v135","v136","v137","v138","v139","v140","v141","v142","v143","v144","v145","v146","v147","v148","v149","v150","v151","v152","v153","v154","v155","v156","v157","v158","v159","v160","v161","v162","v163","v164","v165","v166","v167","v168","v169","v170","v171","v172","v173","v174","v175","v176","v177","v178","v179","v180","v181","v182","v183","v184","v185","v186","v187","v188","v189","v190","v192","v193","v194","v195","v196","v197","v198","v199","v200","v201","v202","v203","v204","v205","v206","v207","v208","v209","v210","v211","v212","v213","v214","v215","v216","v217","v218","v219","v220","v221","v222","v223","v224","v225","v226","v227","v228","v229","v230","v231","v232","v233","v234","v235","v236","v237","v238","v239","v240","v241","v242","v243","v244","v245","v246","v247","v248","v249","v250","v251","v252","v253","v254","v255"
#define PROJ_SCLOB "s4","s5","s6","s7","s32","s33","s34","s35","s36","s37","s38","s39","s40","s41","s42","s43","s44","s45","s46","s47","s48","s49","s50","s51","s52","s53","s54","s55","s63","s64","s65","s66","s67","s68","s69","s70","s71","s72","s73","s74","s75","s76","s77","s78","s79","s80","s81","s82","s83","s84","s85","s86","s87","s88","s89","s90","s91","s92","s93","s94","s95","s96","s97","s98","s99","s100","s101"

typedef const u16* const __attribute__((address_space(4)))* kaptr;
__device__ __forceinline__ void proj_phase(kaptr& ka, char*& ws, const unsigned wid, u32x4 (&vp)[5], unsigned& vl, u32x4 (&sa)[5], u32x2 s24, u32x2& s28, unsigned tdum, u32x4& srdA, u32x2& sd, unsigned& tag
    ) {
    const unsigned b = blockIdx.x, h = (b & 7) * 4 + ((b >> 3) & 3), mt = b >> 5;
    const unsigned rb = (wid < 2u ? 15u - mt : mt) * 128u;
    const unsigned lane = threadIdx.x & 63u, wc = wid & 1u;
    u32x2 vAc, vB;
#pragma unroll
    for (int par = 0; par < 2; ++par) {
        const unsigned lr = lane >> 3, cc = (lane & 7u) ^ (4u * par + (lr >> 1)), r0 = rb + wc * 64u + lr;
        vAc[par] = r0 * (DC * 2) + cc * 16u;
        const unsigned br = lane >> 4, p16 = lane & 15u, sc = (((p16 >> 1) ^ (br | (par << 2))) << 1) | (p16 & 1u);
        vB[par] = (16u * wid + br) * (NBIG * 2) + h * (HD * 2) + sc * 16u;
    }
    vl = lane * 16u;
    u32x2 s30 = {wid * 8192u, 65536u + wid * 4096u};
    asm volatile(PROJ_ASM
                 : "={s62}"(tag), "={s[56:59]}"(srdA), "={s[60:61]}"(sd), "+{s[8:11]}"(sa[0]), "+{s[12:15]}"(sa[1]), "+{s[16:19]}"(sa[2]), "+{s[20:23]}"(sa[3]), "+{s[24:25]}"(s24), "+{s[28:29]}"(s28), "+{s[30:31]}"(s30),
                   "={v[0:3]}"(vp[0]), "={v[4:7]}"(vp[1]), "={v[8:11]}"(vp[2]), "={v[12:15]}"(vp[3]), "={v[16:19]}"(vp[4]), "+{v[22:23]}"(vAc), "+{v[26:27]}"(vB), "+{v191}"(tdum), "+{s[26:27]}"(ws), "+{s[0:1]}"(ka)
                 : "{s2}"(b), "{s3}"(wid), "{v20}"(vl)
                 : "memory", "m0", "vcc", "scc", PROJ_VCLOB, PROJ_SCLOB, ACLOB_LO, ACLOB_HI);
    sa[4] = (u32x4){s24[0], 0u, 0u, 0u};
}

namespace att {
#define DI __device__ __forceinline__
typedef __bf16 bfx8 __attribute__((ext_vector_type(8)));
DI unsigned rfl(unsigned x) { return __builtin_amdgcn_readfirstlane(x); }
DI f32x4 mfma(u32x4 a, u32x4 b, f32x4 c) { return __builtin_amdgcn_mfma_f32_16x16x32_bf16(__builtin_bit_cast(bfx8, a), __builtin_bit_cast(bfx8, b), c, 0, 0, 0); }
typedef __bf16 bfx2 __attribute__((ext_vector_type(2)));
typedef float f32x2 __attribute__((ext_vector_type(2)));
DI unsigned cvtpk2(float lo, float hi) { f32x2 v = {lo, hi}; bfx2 b = __builtin_convertvector(v, bfx2); return __builtin_bit_cast(unsigned, b); }
DI void att_exact(const u16* __restrict__ x, const u16* __restrict__ wq, char* __restrict__ ws, unsigned h, unsigned r0, float g1) {
    const unsigned lane = lane_id(), g = lane >> 4, c = lane & 15u;
    f32x4 o[8];
#pragma unroll
    for (int n = 0; n < 8; ++n) o[n] = (f32x4){0.f, 0.f, 0.f, 0.f};
#pragma unroll 1
    for (unsigned kk = 0; kk < (unsigned)DM; kk += 32u) {
        const unsigned k = kk + 8u * g;
        const u32x4 xf = *reinterpret_cast<const u32x4*>(x + (r0 + c) * DM + k);
#pragma unroll
        for (int n = 0; n < 8; ++n) {
            const u16* w = wq + k * NBIG + h * HD + n * 16 + c; u32x4 wf;
#pragma unroll
            for (int e = 0; e < 4; ++e) wf[e] = (unsigned)w[2 * e * NBIG] | ((unsigned)w[(2 * e + 1) * NBIG] << 16);
            o[n] = mfma(wf, xf, o[n]);
        }
    }
    u32x4 q[4];
#pragma unroll
    for (int p = 0; p < 4; ++p) {
        const f32x4 a = o[2 * p] * 0.12751743082f, b = o[2 * p + 1] * 0.12751743082f;
        q[p] = (u32x4){cvtpk2(a[0], a[1]), cvtpk2(a[2], a[3]), cvtpk2(b[0], b[1]), cvtpk2(b[2], b[3])};
    }
#pragma unroll
    for (int n = 0; n < 8; ++n) o[n] = (f32x4){0.f, 0.f, 0.f, 0.f};
    const __amdgpu_buffer_rsrc_t srd = __builtin_amdgcn_make_buffer_rsrc((void*)(ws + WS_K + (size_t)h * 1048576), (short)0, 1048576, 0x00020000);
    float m = -3.0e38f, l = 0.f;
#pragma unroll 1
    for (unsigned k0 = 0; k0 <= r0 + 15u; k0 += 32u) {
        const unsigned tb = (k0 >> 6) * 32768u + (k0 & 32u) * 256u;
        f32x4 s[2]; float mx = m;
#pragma unroll
        for (int kt = 0; kt < 2; ++kt) {
            s[kt] = (f32x4){0.f, 0.f, 0.f, 0.f};
#pragma unroll
            for (int p = 0; p < 4; ++p) s[kt] = mfma(__builtin_amdgcn_raw_buffer_load_b128(srd, lane * 16u + (kt * 4 + p) * 1024u, tb, 16), q[p], s[kt]);
#pragma unroll
            for (int e = 0; e < 4; ++e) { if (k0 + 16u * kt + 4u * g + e > r0 + c) s[kt][e] = -3.0e38f; mx = fmaxf(mx, s[kt][e]); }
        }
        mx = fmaxf(mx, __shfl_xor(mx, 16)); mx = fmaxf(mx, __shfl_xor(mx, 32));
        const float al = __builtin_amdgcn_exp2f(m - mx); m = mx;
        u32x4 pf;
#pragma unroll
        for (int e = 0; e < 4; ++e) pf[e] = cvtpk2(__builtin_amdgcn_exp2f(s[e >> 1][2 * (e & 1)] - m), __builtin_amdgcn_exp2f(s[e >> 1][2 * (e & 1) + 1] - m));
        l *= al;
#pragma unroll
        for (int e = 0; e < 4; ++e) l += __uint_as_float(pf[e] << 16) + __uint_as_float(pf[e] & 0xffff0000u);
#pragma unroll
        for (int n = 0; n < 8; ++n) o[n] = mfma(__builtin_amdgcn_raw_buffer_load_b128(srd, lane * 16u + 16384u + n * 1024u, tb, 16), pf, o[n] * al);
    }
    l += __shfl_xor(l, 16); l += __shfl_xor(l, 32);
    const float f = g1 / l;
    const __amdgpu_buffer_rsrc_t srdA = __builtin_amdgcn_make_buffer_rsrc((void*)(ws + WS_AF), (short)0, 128 * KS_TOT * 1024, 0x00020000);
#pragma unroll
    for (int p = 0; p < 4; ++p) {
        const f32x4 a = o[2 * p] * f, b = o[2 * p + 1] * f;
        __builtin_amdgcn_raw_buffer_store_b128((u32x4){cvtpk2(a[0], a[1]), cvtpk2(a[2], a[3]), cvtpk2(b[0], b[1]), cvtpk2(b[2], b[3])}, srdA, lane * 16u + p * 1024u, ((r0 >> 4) * KS_TOT + 18u + 4u * h) * 1024u, 16);
    }
}
}

#define ATT_ASM \
"s_branch L_ago_%=\n" \
".p2align 12\n" \
"L_ab:\n" \
".fill (L_ab + 0 - .) / 4, 4, 0xBF800000\n" \
"L_ap0:\n" \
"s_setpc_b64 s[88:89]\n" \
"L_ago_%=:\n" \
"s_mov_b32 s101, 0\n" \
"s_waitcnt vmcnt(15) lgkmcnt(0)\n" \
"s_barrier\n" \
"v_accvgpr_write_b32 a216, 0\n" \
"v_accvgpr_write_b32 a217, 0\n" \
"v_accvgpr_write_b32 a218, 0\n" \
"v_accvgpr_write_b32 a219, 0\n" \
"v_bfe_u32 v61, v20, 2, 5\n" \
"s_mov_b32 s96, s24\n" \
"v_mfma_f32_32x32x16_bf16 v[128:143], a[216:219], a[216:219], 0\n" \
"buffer_load_dword v57, v61, s[56:59], s96 offen sc1\n" \
"v_and_b32_e32 v63, 0x300, v20\n" \
"ds_read_b128 v[96:99], v63 offset:0\n" \
"v_add_u32_e32 v58, s22, v20\n" \
"ds_read_b128 a[0:3], v58 offset:0\n" \
"v_mfma_f32_32x32x16_bf16 v[144:159], a[216:219], a[216:219], 0\n" \
"ds_read_b128 a[16:19], v58 offset:8192\n" \
"v_add_u32_e32 v59, s23, v20\n" \
"ds_read_b128 a[32:35], v59 offset:0\n" \
"ds_read_b128 a[48:51], v59 offset:8192\n" \
"ds_read_b128 v[100:103], v63 offset:1024\n" \
"ds_read_b128 a[4:7], v58 offset:1024\n" \
"v_mfma_f32_32x32x16_bf16 v[160:175], a[216:219], a[216:219], 0\n" \
"ds_read_b128 a[20:23], v58 offset:9216\n" \
"ds_read_b128 a[36:39], v59 offset:1024\n" \
"ds_read_b128 a[52:55], v59 offset:9216\n" \
"s_sub_u32 s67, s12, 0xc000\n" \
"ds_read_b128 v[104:107], v63 offset:2048\n" \
"s_mov_b32 s65, s12\n" \
"v_mfma_f32_32x32x16_bf16 v[176:191], a[216:219], a[216:219], 0\n" \
"v_add_u32_e32 v55, s67, v20\n" \
"v_add_u32_e32 v52, 0xc000, v55\n" \
"s_xor_b32 m0, s65, 0x10000\n" \
"ds_read_b128 a[8:11], v58 offset:2048\n" \
"buffer_load_dwordx4 v52, s[8:11], 0 offen offset:0 sc1 lds\n" \
"v_mfma_f32_32x32x16_bf16 v[192:207], a[216:219], a[216:219], 0\n" \
"ds_read_b128 a[24:27], v58 offset:10240\n" \
"ds_read_b128 a[40:43], v59 offset:2048\n" \
"ds_read_b128 a[56:59], v59 offset:10240\n" \
"ds_read_b128 v[108:111], v63 offset:3072\n" \
"buffer_load_dwordx4 v52, s[8:11], 0 offen offset:1024 sc1 lds\n" \
"v_mfma_f32_32x32x16_bf16 v[208:223], a[216:219], a[216:219], 0\n" \
"ds_read_b128 a[12:15], v58 offset:3072\n" \
"ds_read_b128 a[28:31], v58 offset:11264\n" \
"ds_read_b128 a[44:47], v59 offset:3072\n" \
"ds_read_b128 a[60:63], v59 offset:11264\n" \
"s_waitcnt lgkmcnt(15)\n" \
"v_xor_b32_e32 v96, 0x80008000, v96\n" \
"v_mfma_f32_32x32x16_bf16 v[224:239], a[216:219], a[216:219], 0\n" \
"v_xor_b32_e32 v97, 0x80008000, v97\n" \
"v_xor_b32_e32 v98, 0x80008000, v98\n" \
"v_xor_b32_e32 v99, 0x80008000, v99\n" \
"ds_read_b128 a[160:163], v20 offset:0\n" \
"ds_read_b128 a[164:167], v20 offset:1024\n" \
"s_waitcnt lgkmcnt(15)\n" \
"v_xor_b32_e32 v100, 0x80008000, v100\n" \
"v_mfma_f32_32x32x16_bf16 v[240:255], a[216:219], a[216:219], 0\n" \
"v_xor_b32_e32 v101, 0x80008000, v101\n" \
"v_xor_b32_e32 v102, 0x80008000, v102\n" \
"v_xor_b32_e32 v103, 0x80008000, v103\n" \
"ds_read_b128 a[168:171], v20 offset:2048\n" \
"ds_read_b128 a[172:175], v20 offset:3072\n" \
"ds_read_b128 a[188:191], v20 offset:7168\n" \
"v_mfma_f32_16x16x32_bf16 a[192:195], a[216:219], a[216:219], 0\n" \
"ds_read_b128 a[184:187], v20 offset:6144\n" \
"s_waitcnt lgkmcnt(15)\n" \
"v_xor_b32_e32 v104, 0x80008000, v104\n" \
"v_mfma_f32_16x16x32_bf16 a[196:199], a[216:219], a[216:219], 0\n" \
"v_xor_b32_e32 v105, 0x80008000, v105\n" \
"v_xor_b32_e32 v106, 0x80008000, v106\n" \
"v_mfma_f32_16x16x32_bf16 v[32:35], v[96:99], a[0:3], 0\n" \
"v_xor_b32_e32 v107, 0x80008000, v107\n" \
"ds_read_b128 a[180:183], v20 offset:5120\n" \
"v_mfma_f32_16x16x32_bf16 v[36:39], v[96:99], a[16:19], 0\n" \
"ds_read_b128 a[176:179], v20 offset:4096\n" \
"s_waitcnt lgkmcnt(12)\n" \
"v_xor_b32_e32 v108, 0x80008000, v108\n" \
"v_mfma_f32_16x16x32_bf16 v[40:43], v[96:99], a[32:35], 0\n" \
"v_xor_b32_e32 v109, 0x80008000, v109\n" \
"v_xor_b32_e32 v110, 0x80008000, v110\n" \
"v_mfma_f32_16x16x32_bf16 v[44:47], v[96:99], a[48:51], 0\n" \
"v_xor_b32_e32 v111, 0x80008000, v111\n" \
"v_add_u32_e32 v60, s15, v20\n" \
"v_mfma_f32_16x16x32_bf16 v[32:35], v[100:103], a[4:7], v[32:35]\n" \
"v_add_u32_e32 v60, 0x10000, v60\n" \
"ds_read_b128 a[128:131], v20 offset:8192\n" \
"v_mfma_f32_16x16x32_bf16 v[36:39], v[100:103], a[20:23], v[36:39]\n" \
"ds_read_b128 a[144:147], v20 offset:12288\n" \
"ds_read_b128 a[132:135], v20 offset:9216\n" \
"v_mfma_f32_16x16x32_bf16 v[40:43], v[100:103], a[36:39], v[40:43]\n" \
"ds_read_b128 a[148:151], v20 offset:13312\n" \
"ds_read_b128 a[136:139], v20 offset:10240\n" \
"v_mfma_f32_16x16x32_bf16 v[44:47], v[100:103], a[52:55], v[44:47]\n" \
"ds_read_b128 a[152:155], v20 offset:14336\n" \
"ds_read_b128 a[140:143], v20 offset:11264\n" \
"v_mfma_f32_16x16x32_bf16 v[32:35], v[104:107], a[8:11], v[32:35]\n" \
"ds_read_b128 a[156:159], v20 offset:15360\n" \
"ds_read_b128 a[64:67], v60 offset:16384\n" \
"v_mfma_f32_16x16x32_bf16 v[36:39], v[104:107], a[24:27], v[36:39]\n" \
"ds_read_b128 a[68:71], v60 offset:17408\n" \
"ds_read_b128 a[72:75], v60 offset:18432\n" \
"v_mfma_f32_16x16x32_bf16 v[40:43], v[104:107], a[40:43], v[40:43]\n" \
"ds_read_b128 a[76:79], v60 offset:19456\n" \
"ds_read_b128 a[80:83], v60 offset:24576\n" \
"v_mfma_f32_16x16x32_bf16 v[44:47], v[104:107], a[56:59], v[44:47]\n" \
"ds_read_b128 a[84:87], v60 offset:25600\n" \
"s_waitcnt lgkmcnt(15)\n" \
"v_mfma_f32_16x16x32_bf16 v[32:35], v[108:111], a[12:15], v[32:35]\n" \
"ds_read_b128 a[88:91], v60 offset:26624\n" \
"ds_read_b128 a[92:95], v60 offset:27648\n" \
"v_mfma_f32_16x16x32_bf16 v[36:39], v[108:111], a[28:31], v[36:39]\n" \
"ds_read_b128 a[96:99], v60 offset:0\n" \
"ds_read_b128 a[100:103], v60 offset:1024\n" \
"v_mfma_f32_16x16x32_bf16 v[40:43], v[108:111], a[44:47], v[40:43]\n" \
"ds_read_b128 a[104:107], v60 offset:2048\n" \
"ds_read_b128 a[108:111], v60 offset:3072\n" \
"v_mfma_f32_16x16x32_bf16 v[44:47], v[108:111], a[60:63], v[44:47]\n" \
"ds_read_b128 a[112:115], v60 offset:8192\n" \
"ds_read_b128 a[116:119], v60 offset:9216\n" \
"v_mfma_f32_16x16x32_bf16 v[64:67], a[160:163], a[0:3], v[32:35]\n" \
"ds_read_b128 a[120:123], v60 offset:10240\n" \
"ds_read_b128 a[124:127], v60 offset:11264\n" \
"v_mfma_f32_16x16x32_bf16 v[64:67], a[164:167], a[4:7], v[64:67]\n" \
"v_bfe_u32 v26, v20, 8, 2\n" \
"s_mov_b32 m0, s20\n" \
"v_mfma_f32_16x16x32_bf16 v[64:67], a[168:171], a[8:11], v[64:67]\n" \
"v_bfe_u32 v25, v20, 5, 3\n" \
"v_lshlrev_b32_e32 v26, 1, v26\n" \
"v_mfma_f32_16x16x32_bf16 v[64:67], a[172:175], a[12:15], v[64:67]\n" \
"global_load_lds_dword v[16:17], off\n" \
"v_mfma_f32_16x16x32_bf16 v[68:71], a[188:191], a[12:15], v[32:35]\n" \
"v_sub_u32_e32 v25, v25, v26\n" \
"v_and_b32_e32 v26, 16, v20\n" \
"v_mfma_f32_16x16x32_bf16 v[68:71], a[184:187], a[8:11], v[68:71]\n" \
"v_mov_b32_e32 v27, 0x3f80\n" \
"v_lshlrev_b32_e32 v26, v26, v27\n" \
"v_mfma_f32_16x16x32_bf16 v[68:71], a[180:183], a[4:7], v[68:71]\n" \
"v_mov_b32_e32 v54, 0x3f803f80\n" \
"s_waitcnt lgkmcnt(15)\n" \
"v_mfma_f32_16x16x32_bf16 v[68:71], a[176:179], a[0:3], v[68:71]\n" \
"s_mov_b32 s84, 0x33333333\n" \
"s_mov_b32 s85, 0xcccccccc\n" \
"v_mfma_f32_16x16x32_bf16 v[76:79], a[176:179], a[16:19], v[36:39]\n" \
"s_mov_b32 s86, 0x11111111\n" \
"s_mov_b32 s87, 0x11111111\n" \
"v_mfma_f32_16x16x32_bf16 v[76:79], a[180:183], a[20:23], v[76:79]\n" \
"s_mov_b32 s88, 0x44444444\n" \
"s_mov_b32 s89, 0x44444444\n" \
"v_mfma_f32_16x16x32_bf16 v[76:79], a[184:187], a[24:27], v[76:79]\n" \
"v_exp_f32_e32 v70, v70\n" \
"v_mfma_f32_16x16x32_bf16 v[76:79], a[188:191], a[28:31], v[76:79]\n" \
"v_exp_f32_e32 v71, v71\n" \
"v_mfma_f32_16x16x32_bf16 v[72:75], a[172:175], a[28:31], v[36:39]\n" \
"v_exp_f32_e32 v68, v68\n" \
"v_mfma_f32_16x16x32_bf16 v[72:75], a[168:171], a[24:27], v[72:75]\n" \
"v_exp_f32_e32 v69, v69\n" \
"v_mfma_f32_16x16x32_bf16 v[72:75], a[164:167], a[20:23], v[72:75]\n" \
"v_bfe_i32 v21, v20, 4, 1\n" \
"v_cvt_pk_bf16_f32 v71, v70, v71\n" \
"v_mfma_f32_16x16x32_bf16 v[72:75], a[160:163], a[16:19], v[72:75]\n" \
"v_exp_f32_e32 v66, v66\n" \
"v_mfma_f32_16x16x32_bf16 v[80:83], a[160:163], a[32:35], v[40:43]\n" \
"v_exp_f32_e32 v67, v67\n" \
"v_mfma_f32_16x16x32_bf16 v[80:83], a[164:167], a[36:39], v[80:83]\n" \
"v_exp_f32_e32 v64, v64\n" \
"v_mfma_f32_16x16x32_bf16 v[80:83], a[168:171], a[40:43], v[80:83]\n" \
"v_exp_f32_e32 v65, v65\n" \
"v_mfma_f32_16x16x32_bf16 v[80:83], a[172:175], a[44:47], v[80:83]\n" \
"v_exp_f32_e32 v72, v72\n" \
"v_mfma_f32_16x16x32_bf16 v[84:87], a[188:191], a[44:47], v[40:43]\n" \
"s_waitcnt vmcnt(3)\n" \
"v_cmp_ne_u32_e32 vcc, s62, v57\n" \
"v_mfma_f32_16x16x32_bf16 v[84:87], a[184:187], a[40:43], v[84:87]\n" \
"v_exp_f32_e32 v73, v73\n" \
"v_mfma_f32_16x16x32_bf16 v[84:87], a[180:183], a[36:39], v[84:87]\n" \
"v_exp_f32_e32 v74, v74\n" \
"v_mfma_f32_16x16x32_bf16 v[84:87], a[176:179], a[32:35], v[84:87]\n" \
"s_cbranch_vccnz L_skb_%=\n" \
"L_rkb_%=:\n" \
"v_cmp_eq_u32_e32 vcc, 0, v25\n" \
"v_mfma_f32_16x16x32_bf16 v[92:95], a[176:179], a[48:51], v[44:47]\n" \
"v_exp_f32_e32 v75, v75\n" \
"v_mfma_f32_16x16x32_bf16 v[92:95], a[180:183], a[52:55], v[92:95]\n" \
"v_cndmask_b32_e32 v28, 0, v26, vcc\n" \
"v_cvt_pk_bf16_f32 v72, v72, v73\n" \
"v_mfma_f32_16x16x32_bf16 v[92:95], a[184:187], a[56:59], v[92:95]\n" \
"v_exp_f32_e32 v76, v76\n" \
"v_mfma_f32_16x16x32_bf16 v[92:95], a[188:191], a[60:63], v[92:95]\n" \
"v_exp_f32_e32 v77, v77\n" \
"v_mfma_f32_16x16x32_bf16 v[88:91], a[172:175], a[60:63], v[44:47]\n" \
"v_exp_f32_e32 v78, v78\n" \
"v_mfma_f32_16x16x32_bf16 v[88:91], a[168:171], a[56:59], v[88:91]\n" \
"v_exp_f32_e32 v79, v79\n" \
"v_mfma_f32_16x16x32_bf16 v[88:91], a[164:167], a[52:55], v[88:91]\n" \
"v_cndmask_b32_e64 v22, 0, v54, s[84:85]\n" \
"v_cndmask_b32_e64 v23, 0, v54, s[86:87]\n" \
"v_mfma_f32_16x16x32_bf16 v[88:91], a[160:163], a[48:51], v[88:91]\n" \
"v_cndmask_b32_e64 v24, 0, v54, s[88:89]\n" \
"v_and_b32_e32 v21, 0xaaaaaaaa, v21\n" \
"s_waitcnt vmcnt(4) lgkmcnt(0)\n" \
"s_barrier\n" \
"v_cmp_eq_u32_e32 vcc, 1, v25\n" \
"v_cvt_pk_bf16_f32 v70, v68, v69\n" \
"v_cvt_pk_bf16_f32 v73, v74, v75\n" \
"v_mov_b32_e32 v62, 0\n" \
"v_accvgpr_write_b32 a208, 0\n" \
"v_accvgpr_write_b32 a209, 0\n" \
"v_accvgpr_write_b32 a210, 0\n" \
"v_accvgpr_write_b32 a211, 0\n" \
"v_accvgpr_write_b32 a200, v22\n" \
"v_accvgpr_write_b32 a201, v22\n" \
"v_accvgpr_write_b32 a202, v22\n" \
"v_accvgpr_write_b32 a203, v22\n" \
"v_accvgpr_write_b32 a204, v23\n" \
"v_accvgpr_write_b32 a205, v23\n" \
"v_accvgpr_write_b32 a206, v23\n" \
"v_accvgpr_write_b32 a207, v23\n" \
"v_accvgpr_write_b32 a212, v24\n" \
"v_accvgpr_write_b32 a213, v24\n" \
"v_accvgpr_write_b32 a214, v24\n" \
"v_accvgpr_write_b32 a215, v24\n" \
"v_or_b32_e32 v21, 0x44444444, v21\n" \
"s_mov_b32 s64, 0x8000\n" \
"v_add_u32_e32 v50, 0x6000, v55\n" \
"v_add_u32_e32 v51, 0xa000, v55\n" \
"v_add_u32_e32 v53, 0x10000, v55\n" \
"v_add_u32_e32 v48, 0xc000, v20\n" \
"v_add_u32_e32 v49, s17, v20\n" \
"v_cndmask_b32_e32 v29, 0, v26, vcc\n" \
"v_accvgpr_write_b32 a220, v96\n" \
"v_accvgpr_write_b32 a221, v97\n" \
"v_accvgpr_write_b32 a222, v98\n" \
"v_accvgpr_write_b32 a223, v99\n" \
"v_accvgpr_write_b32 a224, v100\n" \
"v_accvgpr_write_b32 a225, v101\n" \
"v_accvgpr_write_b32 a226, v102\n" \
"v_accvgpr_write_b32 a227, v103\n" \
"v_accvgpr_write_b32 a228, v104\n" \
"v_accvgpr_write_b32 a229, v105\n" \
"v_accvgpr_write_b32 a230, v106\n" \
"v_accvgpr_write_b32 a231, v107\n" \
"v_accvgpr_write_b32 a232, v108\n" \
"v_accvgpr_write_b32 a233, v109\n" \
"v_accvgpr_write_b32 a234, v110\n" \
"v_accvgpr_write_b32 a235, v111\n" \
"v_cvt_pk_bf16_f32 v69, v66, v67\n" \
"v_cvt_pk_bf16_f32 v68, v64, v65\n" \
"v_cvt_pk_bf16_f32 v74, v76, v77\n" \
"v_cvt_pk_bf16_f32 v75, v78, v79\n" \
"s_sub_u32 s66, s18, 1\n" \
"s_cbranch_scc1 L_tr_%=\n" \
".fill (L_ab + 1856 - .) / 4, 4, 0xBF800000\n" \
"L_1_%=:\n" \
"v_mfma_f32_16x16x32_bf16 v[96:99], a[128:131], a[0:3], v[32:35]\n" \
"s_xor_b32 m0, s65, 0x12000\n" \
"ds_read_b128 a[160:163], v49 offset:0\n" \
"v_mfma_f32_16x16x32_bf16 v[96:99], a[132:135], a[4:7], v[96:99]\n" \
"buffer_load_dwordx4 v50, s[8:11], s64 offen offset:0 sc1 lds\n" \
"v_mfma_f32_16x16x32_bf16 v[96:99], a[136:139], a[8:11], v[96:99]\n" \
"ds_read_b128 a[164:167], v49 offset:1024\n" \
"ds_read_b128 a[168:171], v49 offset:2048\n" \
"v_mfma_f32_16x16x32_bf16 v[96:99], a[140:143], a[12:15], v[96:99]\n" \
"ds_read_b128 a[172:175], v49 offset:3072\n" \
"ds_read_b128 a[176:179], v49 offset:4096\n" \
"v_mfma_f32_16x16x32_bf16 v[100:103], a[156:159], a[12:15], v[32:35]\n" \
"ds_read_b128 a[180:183], v49 offset:5120\n" \
"ds_read_b128 a[184:187], v49 offset:6144\n" \
"v_mfma_f32_16x16x32_bf16 v[100:103], a[152:155], a[8:11], v[100:103]\n" \
"v_exp_f32_e64 v86, v86\n" \
"v_mfma_f32_16x16x32_bf16 v[100:103], a[148:151], a[4:7], v[100:103]\n" \
"v_exp_f32_e64 v87, v87\n" \
"v_mfma_f32_16x16x32_bf16 v[100:103], a[144:147], a[0:3], v[100:103]\n" \
"ds_read_b128 a[188:191], v49 offset:7168\n" \
"v_cvt_pk_bf16_f32 v87, v86, v87\n" \
"v_mfma_f32_16x16x32_bf16 v[108:111], a[144:147], a[16:19], v[36:39]\n" \
"v_exp_f32_e64 v84, v84\n" \
"v_mfma_f32_16x16x32_bf16 v[108:111], a[148:151], a[20:23], v[108:111]\n" \
"v_exp_f32_e64 v85, v85\n" \
"v_mfma_f32_16x16x32_bf16 v[108:111], a[152:155], a[24:27], v[108:111]\n" \
"v_exp_f32_e64 v82, v82\n" \
"v_mfma_f32_16x16x32_bf16 v[108:111], a[156:159], a[28:31], v[108:111]\n" \
"v_exp_f32_e64 v83, v83\n" \
"v_mfma_f32_16x16x32_bf16 v[104:107], a[140:143], a[28:31], v[36:39]\n" \
"v_exp_f32_e64 v80, v80\n" \
"v_mfma_f32_16x16x32_bf16 v[104:107], a[136:139], a[24:27], v[104:107]\n" \
"v_exp_f32_e64 v81, v81\n" \
"v_mfma_f32_16x16x32_bf16 v[104:107], a[132:135], a[20:23], v[104:107]\n" \
"v_exp_f32_e64 v88, v88\n" \
"v_mfma_f32_16x16x32_bf16 v[104:107], a[128:131], a[16:19], v[104:107]\n" \
"buffer_load_dwordx4 v50, s[8:11], s64 offen offset:1024 sc1 lds\n" \
"v_mfma_f32_16x16x32_bf16 v[112:115], a[128:131], a[32:35], v[40:43]\n" \
"s_xor_b32 m0, s65, 0x6000\n" \
"v_cvt_pk_bf16_f32 v86, v84, v85\n" \
"v_mfma_f32_16x16x32_bf16 v[112:115], a[132:135], a[36:39], v[112:115]\n" \
"v_cvt_pk_bf16_f32 v85, v82, v83\n" \
"v_cvt_pk_bf16_f32 v84, v80, v81\n" \
"v_mfma_f32_16x16x32_bf16 v[112:115], a[136:139], a[40:43], v[112:115]\n" \
"v_exp_f32_e64 v89, v89\n" \
"v_mfma_f32_16x16x32_bf16 v[112:115], a[140:143], a[44:47], v[112:115]\n" \
"v_exp_f32_e64 v90, v90\n" \
"v_mfma_f32_16x16x32_bf16 v[116:119], a[156:159], a[44:47], v[40:43]\n" \
"v_exp_f32_e64 v91, v91\n" \
"v_mfma_f32_16x16x32_bf16 v[116:119], a[152:155], a[40:43], v[116:119]\n" \
"v_exp_f32_e64 v92, v92\n" \
"v_mfma_f32_16x16x32_bf16 v[116:119], a[148:151], a[36:39], v[116:119]\n" \
"v_exp_f32_e64 v93, v93\n" \
"v_mfma_f32_16x16x32_bf16 v[116:119], a[144:147], a[32:35], v[116:119]\n" \
"v_exp_f32_e64 v94, v94\n" \
"v_mfma_f32_16x16x32_bf16 v[124:127], a[144:147], a[48:51], v[44:47]\n" \
"v_cvt_pk_bf16_f32 v88, v88, v89\n" \
"v_cvt_pk_bf16_f32 v89, v90, v91\n" \
"v_mfma_f32_16x16x32_bf16 v[124:127], a[148:151], a[52:55], v[124:127]\n" \
"v_exp_f32_e64 v95, v95\n" \
"v_mfma_f32_16x16x32_bf16 v[124:127], a[152:155], a[56:59], v[124:127]\n" \
"v_cvt_pk_bf16_f32 v90, v92, v93\n" \
"v_cvt_pk_bf16_f32 v91, v94, v95\n" \
"v_mfma_f32_16x16x32_bf16 v[124:127], a[156:159], a[60:63], v[124:127]\n" \
"ds_read_b128 a[144:147], v48 offset:4096\n" \
"ds_read_b128 a[148:151], v48 offset:5120\n" \
"v_mfma_f32_16x16x32_bf16 v[120:123], a[140:143], a[60:63], v[44:47]\n" \
"ds_read_b128 a[152:155], v48 offset:6144\n" \
"ds_read_b128 a[140:143], v48 offset:3072\n" \
"v_mfma_f32_16x16x32_bf16 v[120:123], a[136:139], a[56:59], v[120:123]\n" \
"ds_read_b128 a[136:139], v48 offset:2048\n" \
"ds_read_b128 a[156:159], v48 offset:7168\n" \
"v_mfma_f32_16x16x32_bf16 v[120:123], a[132:135], a[52:55], v[120:123]\n" \
"buffer_load_dwordx4 v51, s[8:11], s64 offen offset:0 sc1 lds\n" \
"v_mfma_f32_16x16x32_bf16 v[120:123], a[128:131], a[48:51], v[120:123]\n" \
"ds_read_b128 a[128:131], v48 offset:0\n" \
"s_waitcnt lgkmcnt(7)\n" \
"v_mfma_f32_16x16x32_bf16 v[128:131], a[160:163], v[68:71], v[128:131]\n" \
"v_exp_f32_e32 v102, v102\n" \
"v_mfma_f32_16x16x32_bf16 v[160:163], a[160:163], v[72:75], v[160:163]\n" \
"v_exp_f32_e64 v103, v103\n" \
"v_mfma_f32_16x16x32_bf16 v[132:135], a[164:167], v[68:71], v[132:135]\n" \
"ds_read_b128 a[132:135], v48 offset:1024\n" \
"v_cvt_pk_bf16_f32 v103, v102, v103\n" \
"v_mfma_f32_16x16x32_bf16 v[164:167], a[164:167], v[72:75], v[164:167]\n" \
"v_exp_f32_e64 v100, v100\n" \
"v_mfma_f32_16x16x32_bf16 v[136:139], a[168:171], v[68:71], v[136:139]\n" \
"v_exp_f32_e64 v101, v101\n" \
"v_mfma_f32_16x16x32_bf16 v[168:171], a[168:171], v[72:75], v[168:171]\n" \
"v_exp_f32_e64 v98, v98\n" \
"v_mfma_f32_16x16x32_bf16 v[140:143], a[172:175], v[68:71], v[140:143]\n" \
"v_exp_f32_e64 v99, v99\n" \
"v_mfma_f32_16x16x32_bf16 v[172:175], a[172:175], v[72:75], v[172:175]\n" \
"v_exp_f32_e64 v96, v96\n" \
"v_mfma_f32_16x16x32_bf16 v[144:147], a[176:179], v[68:71], v[144:147]\n" \
"v_exp_f32_e64 v97, v97\n" \
"v_mfma_f32_16x16x32_bf16 v[176:179], a[176:179], v[72:75], v[176:179]\n" \
"v_exp_f32_e64 v104, v104\n" \
"v_mfma_f32_16x16x32_bf16 v[148:151], a[180:183], v[68:71], v[148:151]\n" \
"v_exp_f32_e64 v105, v105\n" \
"v_mfma_f32_16x16x32_bf16 v[180:183], a[180:183], v[72:75], v[180:183]\n" \
"v_cvt_pk_bf16_f32 v102, v100, v101\n" \
"v_cvt_pk_bf16_f32 v101, v98, v99\n" \
"v_mfma_f32_16x16x32_bf16 v[152:155], a[184:187], v[68:71], v[152:155]\n" \
"v_cvt_pk_bf16_f32 v100, v96, v97\n" \
"v_cvt_pk_bf16_f32 v104, v104, v105\n" \
"v_mfma_f32_16x16x32_bf16 v[184:187], a[184:187], v[72:75], v[184:187]\n" \
"buffer_load_dwordx4 v51, s[8:11], s64 offen offset:1024 sc1 lds\n" \
"v_mfma_f32_16x16x32_bf16 v[156:159], a[188:191], v[68:71], v[156:159]\n" \
"v_exp_f32_e64 v106, v106\n" \
"v_mfma_f32_16x16x32_bf16 v[188:191], a[188:191], v[72:75], v[188:191]\n" \
"v_exp_f32_e64 v107, v107\n" \
"v_smfmac_f32_16x16x64_bf16 a[192:195], a[200:203], v[68:75], v21\n" \
"v_exp_f32_e64 v108, v108\n" \
"v_mfma_f32_16x16x32_bf16 v[220:223], a[188:191], v[84:87], v[220:223]\n" \
"v_exp_f32_e64 v109, v109\n" \
"v_mfma_f32_16x16x32_bf16 v[252:255], a[188:191], v[88:91], v[252:255]\n" \
"v_exp_f32_e64 v110, v110\n" \
"v_mfma_f32_16x16x32_bf16 v[216:219], a[184:187], v[84:87], v[216:219]\n" \
"v_exp_f32_e64 v111, v111\n" \
"v_mfma_f32_16x16x32_bf16 v[248:251], a[184:187], v[88:91], v[248:251]\n" \
"v_exp_f32_e64 v118, v118\n" \
"v_mfma_f32_16x16x32_bf16 v[212:215], a[180:183], v[84:87], v[212:215]\n" \
"v_exp_f32_e64 v119, v119\n" \
"v_mfma_f32_16x16x32_bf16 v[244:247], a[180:183], v[88:91], v[244:247]\n" \
"v_cvt_pk_bf16_f32 v105, v106, v107\n" \
"v_cvt_pk_bf16_f32 v106, v108, v109\n" \
"v_mfma_f32_16x16x32_bf16 v[208:211], a[176:179], v[84:87], v[208:211]\n" \
"v_cvt_pk_bf16_f32 v107, v110, v111\n" \
"v_cvt_pk_bf16_f32 v119, v118, v119\n" \
"v_mfma_f32_16x16x32_bf16 v[240:243], a[176:179], v[88:91], v[240:243]\n" \
"v_exp_f32_e64 v116, v116\n" \
"v_mfma_f32_16x16x32_bf16 v[204:207], a[172:175], v[84:87], v[204:207]\n" \
"v_exp_f32_e64 v117, v117\n" \
"v_mfma_f32_16x16x32_bf16 v[236:239], a[172:175], v[88:91], v[236:239]\n" \
"v_exp_f32_e64 v114, v114\n" \
"v_mfma_f32_16x16x32_bf16 v[200:203], a[168:171], v[84:87], v[200:203]\n" \
"v_exp_f32_e64 v115, v115\n" \
"v_mfma_f32_16x16x32_bf16 v[232:235], a[168:171], v[88:91], v[232:235]\n" \
"v_exp_f32_e64 v112, v112\n" \
"v_mfma_f32_16x16x32_bf16 v[196:199], a[164:167], v[84:87], v[196:199]\n" \
"v_exp_f32_e64 v113, v113\n" \
"v_mfma_f32_16x16x32_bf16 v[228:231], a[164:167], v[88:91], v[228:231]\n" \
"v_exp_f32_e64 v120, v120\n" \
"v_mfma_f32_16x16x32_bf16 v[192:195], a[160:163], v[84:87], v[192:195]\n" \
"v_exp_f32_e64 v121, v121\n" \
"v_mfma_f32_16x16x32_bf16 v[224:227], a[160:163], v[88:91], v[224:227]\n" \
"v_cvt_pk_bf16_f32 v118, v116, v117\n" \
"v_cvt_pk_bf16_f32 v117, v114, v115\n" \
"v_smfmac_f32_16x16x64_bf16 a[196:199], a[200:203], v[84:91], v21\n" \
"v_cvt_pk_bf16_f32 v116, v112, v113\n" \
"v_cvt_pk_bf16_f32 v120, v120, v121\n" \
"s_waitcnt vmcnt(4) lgkmcnt(0)\n" \
"s_barrier\n" \
"v_mfma_f32_16x16x32_bf16 v[64:67], a[128:131], a[0:3], v[32:35]\n" \
"s_xor_b32 m0, s65, 0x14000\n" \
"ds_read_b128 a[160:163], v49 offset:8192\n" \
"v_mfma_f32_16x16x32_bf16 v[64:67], a[132:135], a[4:7], v[64:67]\n" \
"buffer_load_dwordx4 v52, s[8:11], s64 offen offset:0 sc1 lds\n" \
"v_mfma_f32_16x16x32_bf16 v[64:67], a[136:139], a[8:11], v[64:67]\n" \
"ds_read_b128 a[164:167], v49 offset:9216\n" \
"ds_read_b128 a[168:171], v49 offset:10240\n" \
"v_mfma_f32_16x16x32_bf16 v[64:67], a[140:143], a[12:15], v[64:67]\n" \
"ds_read_b128 a[172:175], v49 offset:11264\n" \
"ds_read_b128 a[176:179], v49 offset:12288\n" \
"v_mfma_f32_16x16x32_bf16 v[68:71], a[156:159], a[12:15], v[32:35]\n" \
"ds_read_b128 a[180:183], v49 offset:13312\n" \
"ds_read_b128 a[184:187], v49 offset:14336\n" \
"v_mfma_f32_16x16x32_bf16 v[68:71], a[152:155], a[8:11], v[68:71]\n" \
"v_exp_f32_e64 v122, v122\n" \
"v_mfma_f32_16x16x32_bf16 v[68:71], a[148:151], a[4:7], v[68:71]\n" \
"v_exp_f32_e64 v123, v123\n" \
"v_mfma_f32_16x16x32_bf16 v[68:71], a[144:147], a[0:3], v[68:71]\n" \
"ds_read_b128 a[188:191], v49 offset:15360\n" \
"v_cvt_pk_bf16_f32 v121, v122, v123\n" \
"v_mfma_f32_16x16x32_bf16 v[76:79], a[144:147], a[16:19], v[36:39]\n" \
"v_exp_f32_e64 v124, v124\n" \
"v_mfma_f32_16x16x32_bf16 v[76:79], a[148:151], a[20:23], v[76:79]\n" \
"v_exp_f32_e64 v125, v125\n" \
"v_mfma_f32_16x16x32_bf16 v[76:79], a[152:155], a[24:27], v[76:79]\n" \
"v_exp_f32_e64 v126, v126\n" \
"v_mfma_f32_16x16x32_bf16 v[76:79], a[156:159], a[28:31], v[76:79]\n" \
"v_exp_f32_e64 v127, v127\n" \
"v_mfma_f32_16x16x32_bf16 v[72:75], a[140:143], a[28:31], v[36:39]\n" \
"v_cvt_pk_bf16_f32 v122, v124, v125\n" \
"v_cvt_pk_bf16_f32 v123, v126, v127\n" \
"v_mfma_f32_16x16x32_bf16 v[72:75], a[136:139], a[24:27], v[72:75]\n" \
"v_exp_f32_e64 v70, v70\n" \
"v_mfma_f32_16x16x32_bf16 v[72:75], a[132:135], a[20:23], v[72:75]\n" \
"v_exp_f32_e64 v71, v71\n" \
"v_mfma_f32_16x16x32_bf16 v[72:75], a[128:131], a[16:19], v[72:75]\n" \
"buffer_load_dwordx4 v52, s[8:11], s64 offen offset:1024 sc1 lds\n" \
"v_mfma_f32_16x16x32_bf16 v[80:83], a[128:131], a[32:35], v[40:43]\n" \
"s_mov_b32 m0, s65\n" \
"v_cvt_pk_bf16_f32 v71, v70, v71\n" \
"v_mfma_f32_16x16x32_bf16 v[80:83], a[132:135], a[36:39], v[80:83]\n" \
"v_exp_f32_e32 v68, v68\n" \
"v_mfma_f32_16x16x32_bf16 v[80:83], a[136:139], a[40:43], v[80:83]\n" \
"v_exp_f32_e64 v69, v69\n" \
"v_mfma_f32_16x16x32_bf16 v[80:83], a[140:143], a[44:47], v[80:83]\n" \
"v_exp_f32_e64 v72, v72\n" \
"v_mfma_f32_16x16x32_bf16 v[84:87], a[156:159], a[44:47], v[40:43]\n" \
"v_exp_f32_e64 v73, v73\n" \
"v_mfma_f32_16x16x32_bf16 v[84:87], a[152:155], a[40:43], v[84:87]\n" \
"v_exp_f32_e64 v74, v74\n" \
"v_mfma_f32_16x16x32_bf16 v[84:87], a[148:151], a[36:39], v[84:87]\n" \
"v_exp_f32_e64 v75, v75\n" \
"v_mfma_f32_16x16x32_bf16 v[84:87], a[144:147], a[32:35], v[84:87]\n" \
"v_exp_f32_e64 v66, v66\n" \
"v_mfma_f32_16x16x32_bf16 v[92:95], a[144:147], a[48:51], v[44:47]\n" \
"ds_read_b128 a[144:147], v48 offset:12288\n" \
"v_cvt_pk_bf16_f32 v72, v72, v73\n" \
"v_mfma_f32_16x16x32_bf16 v[92:95], a[148:151], a[52:55], v[92:95]\n" \
"ds_read_b128 a[148:151], v48 offset:13312\n" \
"v_cvt_pk_bf16_f32 v70, v68, v69\n" \
"v_mfma_f32_16x16x32_bf16 v[92:95], a[152:155], a[56:59], v[92:95]\n" \
"ds_read_b128 a[152:155], v48 offset:14336\n" \
"v_cvt_pk_bf16_f32 v73, v74, v75\n" \
"v_mfma_f32_16x16x32_bf16 v[92:95], a[156:159], a[60:63], v[92:95]\n" \
"ds_read_b128 a[156:159], v48 offset:15360\n" \
"v_add_u32_e32 v49, 0x10000, v48\n" \
"v_mfma_f32_16x16x32_bf16 v[88:91], a[140:143], a[60:63], v[44:47]\n" \
"ds_read_b128 a[140:143], v48 offset:11264\n" \
"s_xor_b32 s65, s65, 0x4000\n" \
"v_mfma_f32_16x16x32_bf16 v[88:91], a[136:139], a[56:59], v[88:91]\n" \
"v_exp_f32_e64 v67, v67\n" \
"v_mfma_f32_16x16x32_bf16 v[88:91], a[132:135], a[52:55], v[88:91]\n" \
"buffer_load_dwordx4 v53, s[8:11], s64 offen offset:0 sc1 lds\n" \
"v_mfma_f32_16x16x32_bf16 v[88:91], a[128:131], a[48:51], v[88:91]\n" \
"ds_read_b128 a[128:131], v48 offset:8192\n" \
"s_waitcnt lgkmcnt(6)\n" \
"v_mfma_f32_16x16x32_bf16 v[128:131], a[160:163], v[100:103], v[128:131]\n" \
"ds_read_b128 a[132:135], v48 offset:9216\n" \
"ds_read_b128 a[136:139], v48 offset:10240\n" \
"v_mfma_f32_16x16x32_bf16 v[160:163], a[160:163], v[104:107], v[160:163]\n" \
"v_exp_f32_e32 v64, v64\n" \
"v_mfma_f32_16x16x32_bf16 v[132:135], a[164:167], v[100:103], v[132:135]\n" \
"v_exp_f32_e64 v65, v65\n" \
"v_mfma_f32_16x16x32_bf16 v[164:167], a[164:167], v[104:107], v[164:167]\n" \
"v_exp_f32_e64 v76, v76\n" \
"v_mfma_f32_16x16x32_bf16 v[136:139], a[168:171], v[100:103], v[136:139]\n" \
"v_exp_f32_e64 v77, v77\n" \
"v_mfma_f32_16x16x32_bf16 v[168:171], a[168:171], v[104:107], v[168:171]\n" \
"v_exp_f32_e64 v78, v78\n" \
"v_mfma_f32_16x16x32_bf16 v[140:143], a[172:175], v[100:103], v[140:143]\n" \
"v_exp_f32_e64 v79, v79\n" \
"v_mfma_f32_16x16x32_bf16 v[172:175], a[172:175], v[104:107], v[172:175]\n" \
"v_cvt_pk_bf16_f32 v69, v66, v67\n" \
"v_cvt_pk_bf16_f32 v68, v64, v65\n" \
"v_mfma_f32_16x16x32_bf16 v[144:147], a[176:179], v[100:103], v[144:147]\n" \
"v_cvt_pk_bf16_f32 v74, v76, v77\n" \
"v_cvt_pk_bf16_f32 v75, v78, v79\n" \
"v_mfma_f32_16x16x32_bf16 v[176:179], a[176:179], v[104:107], v[176:179]\n" \
"v_xor_b32_e32 v48, 0x4000, v48\n" \
"v_mfma_f32_16x16x32_bf16 v[148:151], a[180:183], v[100:103], v[148:151]\n" \
"v_mfma_f32_16x16x32_bf16 v[180:183], a[180:183], v[104:107], v[180:183]\n" \
"v_mfma_f32_16x16x32_bf16 v[152:155], a[184:187], v[100:103], v[152:155]\n" \
"v_mfma_f32_16x16x32_bf16 v[184:187], a[184:187], v[104:107], v[184:187]\n" \
"buffer_load_dwordx4 v53, s[8:11], s64 offen offset:1024 sc1 lds\n" \
"v_mfma_f32_16x16x32_bf16 v[156:159], a[188:191], v[100:103], v[156:159]\n" \
"s_add_u32 s64, s64, 0x8000\n" \
"s_sub_u32 s66, s66, 1\n" \
"s_nop 0\n" \
"v_mfma_f32_16x16x32_bf16 v[188:191], a[188:191], v[104:107], v[188:191]\n" \
"v_smfmac_f32_16x16x64_bf16 a[192:195], a[200:203], v[100:107], v21\n" \
"v_mfma_f32_16x16x32_bf16 v[220:223], a[188:191], v[116:119], v[220:223]\n" \
"v_mfma_f32_16x16x32_bf16 v[252:255], a[188:191], v[120:123], v[252:255]\n" \
"v_mfma_f32_16x16x32_bf16 v[216:219], a[184:187], v[116:119], v[216:219]\n" \
"v_mfma_f32_16x16x32_bf16 v[248:251], a[184:187], v[120:123], v[248:251]\n" \
"v_mfma_f32_16x16x32_bf16 v[212:215], a[180:183], v[116:119], v[212:215]\n" \
"v_mfma_f32_16x16x32_bf16 v[244:247], a[180:183], v[120:123], v[244:247]\n" \
"v_mfma_f32_16x16x32_bf16 v[208:211], a[176:179], v[116:119], v[208:211]\n" \
"v_mfma_f32_16x16x32_bf16 v[240:243], a[176:179], v[120:123], v[240:243]\n" \
"v_mfma_f32_16x16x32_bf16 v[204:207], a[172:175], v[116:119], v[204:207]\n" \
"v_mfma_f32_16x16x32_bf16 v[236:239], a[172:175], v[120:123], v[236:239]\n" \
"v_mfma_f32_16x16x32_bf16 v[200:203], a[168:171], v[116:119], v[200:203]\n" \
"v_mfma_f32_16x16x32_bf16 v[232:235], a[168:171], v[120:123], v[232:235]\n" \
"v_mfma_f32_16x16x32_bf16 v[196:199], a[164:167], v[116:119], v[196:199]\n" \
"v_mfma_f32_16x16x32_bf16 v[228:231], a[164:167], v[120:123], v[228:231]\n" \
"v_mfma_f32_16x16x32_bf16 v[192:195], a[160:163], v[116:119], v[192:195]\n" \
"v_mfma_f32_16x16x32_bf16 v[224:227], a[160:163], v[120:123], v[224:227]\n" \
"v_smfmac_f32_16x16x64_bf16 a[196:199], a[200:203], v[116:123], v21\n" \
"s_waitcnt vmcnt(4) lgkmcnt(0)\n" \
"s_barrier\n" \
"s_cbranch_scc0 L_1_%=\n" \
"L_tr_%=:\n" \
"s_setpc_b64 s[28:29]\n" \
".fill (L_ab + 4096 - .) / 4, 4, 0xBF800000\n" \
".fill (L_ab + 4096 - .) / 4, 4, 0xBF800000\n" \
"L_ap1:\n" \
"s_setpc_b64 s[88:89]\n" \
"L_gtr:\n" \
"v_mfma_f32_16x16x32_bf16 v[96:99], a[128:131], a[0:3], v[32:35]\n" \
"v_mov_b32_e32 v56, s62\n" \
"v_readlane_b32 s68, v11, 0\n" \
"v_mfma_f32_16x16x32_bf16 v[96:99], a[132:135], a[4:7], v[96:99]\n" \
"s_xor_b32 m0, s65, 0x12000\n" \
"s_mov_b32 s65, s13\n" \
"v_mfma_f32_16x16x32_bf16 v[96:99], a[136:139], a[8:11], v[96:99]\n" \
"buffer_load_dwordx4 v50, s[8:11], s64 offen offset:0 sc1 lds\n" \
"v_mfma_f32_16x16x32_bf16 v[96:99], a[140:143], a[12:15], v[96:99]\n" \
"v_add_u32_e32 v63, s15, v20\n" \
"s_add_u32 s67, s14, 40960\n" \
"v_mfma_f32_16x16x32_bf16 v[100:103], a[156:159], a[12:15], v[32:35]\n" \
"buffer_load_dwordx4 v50, s[8:11], s64 offen offset:1024 sc1 lds\n" \
"s_mov_b64 exec, 1\n" \
"buffer_store_dword v56, off, s[56:59], s68 offset:0 sc1\n" \
"s_mov_b64 exec, -1\n" \
"v_mfma_f32_16x16x32_bf16 v[100:103], a[152:155], a[8:11], v[100:103]\n" \
"v_add_u32_e32 v50, 0x6000, v63\n" \
"s_xor_b32 m0, s65, 0x8000\n" \
"v_mfma_f32_16x16x32_bf16 v[100:103], a[148:151], a[4:7], v[100:103]\n" \
"ds_read_b128 a[160:163], v49 offset:0\n" \
"ds_read_b128 a[164:167], v49 offset:1024\n" \
"v_mfma_f32_16x16x32_bf16 v[100:103], a[144:147], a[0:3], v[100:103]\n" \
"ds_read_b128 a[168:171], v49 offset:2048\n" \
"ds_read_b128 a[172:175], v49 offset:3072\n" \
"v_mfma_f32_16x16x32_bf16 v[108:111], a[144:147], a[16:19], v[36:39]\n" \
"buffer_load_dwordx4 v50, s[8:11], s67 offen offset:0 sc1 lds\n" \
"v_mfma_f32_16x16x32_bf16 v[108:111], a[148:151], a[20:23], v[108:111]\n" \
"ds_read_b128 a[176:179], v49 offset:4096\n" \
"ds_read_b128 a[180:183], v49 offset:5120\n" \
"v_mfma_f32_16x16x32_bf16 v[108:111], a[152:155], a[24:27], v[108:111]\n" \
"ds_read_b128 a[184:187], v49 offset:6144\n" \
"ds_read_b128 a[188:191], v49 offset:7168\n" \
"v_mfma_f32_16x16x32_bf16 v[108:111], a[156:159], a[28:31], v[108:111]\n" \
"s_mov_b32 s64, s14\n" \
"v_add_u32_e32 v51, 0x12000, v63\n" \
"v_mfma_f32_16x16x32_bf16 v[104:107], a[140:143], a[28:31], v[36:39]\n" \
"buffer_load_dwordx4 v50, s[8:11], s67 offen offset:1024 sc1 lds\n" \
"v_mfma_f32_16x16x32_bf16 v[104:107], a[136:139], a[24:27], v[104:107]\n" \
"v_exp_f32_e32 v86, v86\n" \
"v_mfma_f32_16x16x32_bf16 v[104:107], a[132:135], a[20:23], v[104:107]\n" \
"v_exp_f32_e32 v87, v87\n" \
"v_mfma_f32_16x16x32_bf16 v[104:107], a[128:131], a[16:19], v[104:107]\n" \
"v_exp_f32_e32 v84, v84\n" \
"v_mfma_f32_16x16x32_bf16 v[112:115], a[128:131], a[32:35], v[40:43]\n" \
"v_exp_f32_e32 v85, v85\n" \
"v_mfma_f32_16x16x32_bf16 v[112:115], a[132:135], a[36:39], v[112:115]\n" \
"buffer_load_dwordx4 v50, s[8:11], s67 offen offset:2048 sc1 lds\n" \
"v_mfma_f32_16x16x32_bf16 v[112:115], a[136:139], a[40:43], v[112:115]\n" \
"v_cvt_pk_bf16_f32 v87, v86, v87\n" \
"v_cvt_pk_bf16_f32 v86, v84, v85\n" \
"v_mfma_f32_16x16x32_bf16 v[112:115], a[140:143], a[44:47], v[112:115]\n" \
"v_exp_f32_e32 v82, v82\n" \
"v_mfma_f32_16x16x32_bf16 v[116:119], a[156:159], a[44:47], v[40:43]\n" \
"v_exp_f32_e32 v83, v83\n" \
"v_mfma_f32_16x16x32_bf16 v[116:119], a[152:155], a[40:43], v[116:119]\n" \
"buffer_load_dwordx4 v50, s[8:11], s67 offen offset:3072 sc1 lds\n" \
"v_mfma_f32_16x16x32_bf16 v[116:119], a[148:151], a[36:39], v[116:119]\n" \
"s_xor_b32 m0, s65, 0xa000\n" \
"v_cvt_pk_bf16_f32 v85, v82, v83\n" \
"v_mfma_f32_16x16x32_bf16 v[116:119], a[144:147], a[32:35], v[116:119]\n" \
"v_exp_f32_e32 v80, v80\n" \
"v_mfma_f32_16x16x32_bf16 v[124:127], a[144:147], a[48:51], v[44:47]\n" \
"v_exp_f32_e32 v81, v81\n" \
"v_mfma_f32_16x16x32_bf16 v[124:127], a[148:151], a[52:55], v[124:127]\n" \
"buffer_load_dwordx4 v51, s[8:11], s64 offen offset:0 sc1 lds\n" \
"v_mfma_f32_16x16x32_bf16 v[124:127], a[152:155], a[56:59], v[124:127]\n" \
"v_cvt_pk_bf16_f32 v84, v80, v81\n" \
"v_readlane_b32 s93, v11, 19\n" \
"v_mfma_f32_16x16x32_bf16 v[124:127], a[156:159], a[60:63], v[124:127]\n" \
"v_exp_f32_e32 v88, v88\n" \
"v_mfma_f32_16x16x32_bf16 v[120:123], a[140:143], a[60:63], v[44:47]\n" \
"v_exp_f32_e32 v89, v89\n" \
"v_mfma_f32_16x16x32_bf16 v[120:123], a[136:139], a[56:59], v[120:123]\n" \
"v_exp_f32_e32 v90, v90\n" \
"v_mfma_f32_16x16x32_bf16 v[120:123], a[132:135], a[52:55], v[120:123]\n" \
"buffer_load_dwordx4 v51, s[8:11], s64 offen offset:1024 sc1 lds\n" \
"v_mfma_f32_16x16x32_bf16 v[120:123], a[128:131], a[48:51], v[120:123]\n" \
"v_cvt_pk_bf16_f32 v88, v88, v89\n" \
"s_waitcnt lgkmcnt(0)\n" \
"v_mfma_f32_16x16x32_bf16 v[128:131], a[160:163], v[68:71], v[128:131]\n" \
"v_exp_f32_e32 v91, v91\n" \
"v_mfma_f32_16x16x32_bf16 v[160:163], a[160:163], v[72:75], v[160:163]\n" \
"v_exp_f32_e32 v92, v92\n" \
"v_mfma_f32_16x16x32_bf16 v[132:135], a[164:167], v[68:71], v[132:135]\n" \
"buffer_load_dwordx4 v51, s[8:11], s64 offen offset:2048 sc1 lds\n" \
"v_mfma_f32_16x16x32_bf16 v[164:167], a[164:167], v[72:75], v[164:167]\n" \
"v_exp_f32_e32 v93, v93\n" \
"v_mfma_f32_16x16x32_bf16 v[136:139], a[168:171], v[68:71], v[136:139]\n" \
"v_exp_f32_e32 v94, v94\n" \
"v_mfma_f32_16x16x32_bf16 v[168:171], a[168:171], v[72:75], v[168:171]\n" \
"v_exp_f32_e32 v95, v95\n" \
"v_mfma_f32_16x16x32_bf16 v[140:143], a[172:175], v[68:71], v[140:143]\n" \
"buffer_load_dwordx4 v51, s[8:11], s64 offen offset:3072 sc1 lds\n" \
"v_mfma_f32_16x16x32_bf16 v[172:175], a[172:175], v[72:75], v[172:175]\n" \
"v_cvt_pk_bf16_f32 v89, v90, v91\n" \
"v_cvt_pk_bf16_f32 v90, v92, v93\n" \
"v_mfma_f32_16x16x32_bf16 v[144:147], a[176:179], v[68:71], v[144:147]\n" \
"v_cvt_pk_bf16_f32 v91, v94, v95\n" \
"v_readlane_b32 s80, v11, 11\n" \
"v_mfma_f32_16x16x32_bf16 v[176:179], a[176:179], v[72:75], v[176:179]\n" \
"v_readlane_b32 s81, v11, 12\n" \
"s_mov_b32 s82, 0x47fca0\n" \
"v_mfma_f32_16x16x32_bf16 v[148:151], a[180:183], v[68:71], v[148:151]\n" \
"s_mov_b32 s83, 0x20000\n" \
"v_readlane_b32 s92, v11, 26\n" \
"v_mfma_f32_16x16x32_bf16 v[180:183], a[180:183], v[72:75], v[180:183]\n" \
"s_add_u32 m0, s93, 0x0\n" \
"ds_read_b128 a[128:131], v48 offset:0\n" \
"v_mfma_f32_16x16x32_bf16 v[152:155], a[184:187], v[68:71], v[152:155]\n" \
"ds_read_b128 a[144:147], v48 offset:4096\n" \
"ds_read_b128 a[132:135], v48 offset:1024\n" \
"v_mfma_f32_16x16x32_bf16 v[184:187], a[184:187], v[72:75], v[184:187]\n" \
"buffer_load_dwordx4 v0, s[80:83], s92 offen offset:0 lds\n" \
"v_mfma_f32_16x16x32_bf16 v[156:159], a[188:191], v[68:71], v[156:159]\n" \
"ds_read_b128 a[148:151], v48 offset:5120\n" \
"ds_read_b128 a[136:139], v48 offset:2048\n" \
"v_mfma_f32_16x16x32_bf16 v[188:191], a[188:191], v[72:75], v[188:191]\n" \
"ds_read_b128 a[152:155], v48 offset:6144\n" \
"ds_read_b128 a[140:143], v48 offset:3072\n" \
"v_smfmac_f32_16x16x64_bf16 a[192:195], a[200:203], v[68:75], v21\n" \
"ds_read_b128 a[156:159], v48 offset:7168\n" \
"v_add_u32_e32 v52, 0x14000, v63\n" \
"v_mfma_f32_16x16x32_bf16 v[220:223], a[188:191], v[84:87], v[220:223]\n" \
"buffer_load_dwordx4 v1, s[80:83], s92 offen offset:1024 lds\n" \
"v_mfma_f32_16x16x32_bf16 v[252:255], a[188:191], v[88:91], v[252:255]\n" \
"v_exp_f32_e32 v102, v102\n" \
"v_mfma_f32_16x16x32_bf16 v[216:219], a[184:187], v[84:87], v[216:219]\n" \
"buffer_load_dwordx4 v2, s[80:83], s92 offen offset:2048 lds\n" \
"v_mfma_f32_16x16x32_bf16 v[248:251], a[184:187], v[88:91], v[248:251]\n" \
"v_exp_f32_e32 v103, v103\n" \
"v_mfma_f32_16x16x32_bf16 v[212:215], a[180:183], v[84:87], v[212:215]\n" \
"v_add_u32_e32 v53, 0x20000, v63\n" \
"v_cvt_pk_bf16_f32 v103, v102, v103\n" \
"v_mfma_f32_16x16x32_bf16 v[244:247], a[180:183], v[88:91], v[244:247]\n" \
"v_exp_f32_e32 v100, v100\n" \
"v_mfma_f32_16x16x32_bf16 v[208:211], a[176:179], v[84:87], v[208:211]\n" \
"buffer_load_dwordx4 v3, s[80:83], s92 offen offset:3072 lds\n" \
"v_mfma_f32_16x16x32_bf16 v[240:243], a[176:179], v[88:91], v[240:243]\n" \
"v_exp_f32_e32 v101, v101\n" \
"v_mfma_f32_16x16x32_bf16 v[204:207], a[172:175], v[84:87], v[204:207]\n" \
"v_exp_f32_e32 v98, v98\n" \
"v_mfma_f32_16x16x32_bf16 v[236:239], a[172:175], v[88:91], v[236:239]\n" \
"v_exp_f32_e32 v99, v99\n" \
"v_mfma_f32_16x16x32_bf16 v[200:203], a[168:171], v[84:87], v[200:203]\n" \
"v_exp_f32_e32 v96, v96\n" \
"v_mfma_f32_16x16x32_bf16 v[232:235], a[168:171], v[88:91], v[232:235]\n" \
"v_exp_f32_e32 v97, v97\n" \
"v_mfma_f32_16x16x32_bf16 v[196:199], a[164:167], v[84:87], v[196:199]\n" \
"v_exp_f32_e32 v104, v104\n" \
"v_mfma_f32_16x16x32_bf16 v[228:231], a[164:167], v[88:91], v[228:231]\n" \
"v_exp_f32_e32 v105, v105\n" \
"v_mfma_f32_16x16x32_bf16 v[192:195], a[160:163], v[84:87], v[192:195]\n" \
"v_cvt_pk_bf16_f32 v102, v100, v101\n" \
"v_cvt_pk_bf16_f32 v101, v98, v99\n" \
"v_mfma_f32_16x16x32_bf16 v[224:227], a[160:163], v[88:91], v[224:227]\n" \
"v_cvt_pk_bf16_f32 v100, v96, v97\n" \
"v_cvt_pk_bf16_f32 v104, v104, v105\n" \
"v_smfmac_f32_16x16x64_bf16 a[196:199], a[200:203], v[84:91], v21\n" \
"v_exp_f32_e32 v106, v106\n" \
"s_waitcnt vmcnt(15) lgkmcnt(0)\n" \
"s_barrier\n" \
"v_mfma_f32_16x16x32_bf16 v[64:67], a[128:131], a[0:3], v[32:35]\n" \
"s_xor_b32 m0, s65, 0x18000\n" \
"ds_read_b128 a[160:163], v49 offset:8192\n" \
"v_mfma_f32_16x16x32_bf16 v[64:67], a[132:135], a[4:7], v[64:67]\n" \
"buffer_load_dwordx4 v52, s[8:11], s64 offen offset:0 sc1 lds\n" \
"v_mfma_f32_16x16x32_bf16 v[64:67], a[136:139], a[8:11], v[64:67]\n" \
"ds_read_b128 a[164:167], v49 offset:9216\n" \
"ds_read_b128 a[168:171], v49 offset:10240\n" \
"v_mfma_f32_16x16x32_bf16 v[64:67], a[140:143], a[12:15], v[64:67]\n" \
"ds_read_b128 a[172:175], v49 offset:11264\n" \
"ds_read_b128 a[176:179], v49 offset:12288\n" \
"v_mfma_f32_16x16x32_bf16 v[68:71], a[156:159], a[12:15], v[32:35]\n" \
"buffer_load_dwordx4 v52, s[8:11], s64 offen offset:1024 sc1 lds\n" \
"v_mfma_f32_16x16x32_bf16 v[68:71], a[152:155], a[8:11], v[68:71]\n" \
"ds_read_b128 a[180:183], v49 offset:13312\n" \
"ds_read_b128 a[184:187], v49 offset:14336\n" \
"v_mfma_f32_16x16x32_bf16 v[68:71], a[148:151], a[4:7], v[68:71]\n" \
"v_exp_f32_e32 v107, v107\n" \
"v_mfma_f32_16x16x32_bf16 v[68:71], a[144:147], a[0:3], v[68:71]\n" \
"v_exp_f32_e32 v108, v108\n" \
"v_mfma_f32_16x16x32_bf16 v[76:79], a[144:147], a[16:19], v[36:39]\n" \
"v_exp_f32_e32 v109, v109\n" \
"v_mfma_f32_16x16x32_bf16 v[76:79], a[148:151], a[20:23], v[76:79]\n" \
"buffer_load_dwordx4 v52, s[8:11], s64 offen offset:2048 sc1 lds\n" \
"v_mfma_f32_16x16x32_bf16 v[76:79], a[152:155], a[24:27], v[76:79]\n" \
"v_exp_f32_e32 v110, v110\n" \
"v_mfma_f32_16x16x32_bf16 v[76:79], a[156:159], a[28:31], v[76:79]\n" \
"v_exp_f32_e32 v111, v111\n" \
"v_mfma_f32_16x16x32_bf16 v[72:75], a[140:143], a[28:31], v[36:39]\n" \
"v_cvt_pk_bf16_f32 v105, v106, v107\n" \
"v_cvt_pk_bf16_f32 v106, v108, v109\n" \
"v_mfma_f32_16x16x32_bf16 v[72:75], a[136:139], a[24:27], v[72:75]\n" \
"buffer_load_dwordx4 v52, s[8:11], s64 offen offset:3072 sc1 lds\n" \
"v_mfma_f32_16x16x32_bf16 v[72:75], a[132:135], a[20:23], v[72:75]\n" \
"s_mov_b32 m0, s65\n" \
"v_cvt_pk_bf16_f32 v107, v110, v111\n" \
"v_mfma_f32_16x16x32_bf16 v[72:75], a[128:131], a[16:19], v[72:75]\n" \
"ds_read_b128 a[188:191], v49 offset:15360\n" \
"v_add_u32_e32 v49, 0x10000, v48\n" \
"v_mfma_f32_16x16x32_bf16 v[80:83], a[128:131], a[32:35], v[40:43]\n" \
"v_exp_f32_e32 v118, v118\n" \
"v_mfma_f32_16x16x32_bf16 v[80:83], a[132:135], a[36:39], v[80:83]\n" \
"v_exp_f32_e32 v119, v119\n" \
"v_mfma_f32_16x16x32_bf16 v[80:83], a[136:139], a[40:43], v[80:83]\n" \
"buffer_load_dwordx4 v53, s[8:11], s64 offen offset:0 sc1 lds\n" \
"v_mfma_f32_16x16x32_bf16 v[80:83], a[140:143], a[44:47], v[80:83]\n" \
"v_exp_f32_e32 v116, v116\n" \
"v_mfma_f32_16x16x32_bf16 v[84:87], a[156:159], a[44:47], v[40:43]\n" \
"v_exp_f32_e32 v117, v117\n" \
"v_mfma_f32_16x16x32_bf16 v[84:87], a[152:155], a[40:43], v[84:87]\n" \
"v_cvt_pk_bf16_f32 v119, v118, v119\n" \
"v_cvt_pk_bf16_f32 v118, v116, v117\n" \
"v_mfma_f32_16x16x32_bf16 v[84:87], a[148:151], a[36:39], v[84:87]\n" \
"v_exp_f32_e32 v114, v114\n" \
"v_mfma_f32_16x16x32_bf16 v[84:87], a[144:147], a[32:35], v[84:87]\n" \
"buffer_load_dwordx4 v53, s[8:11], s64 offen offset:1024 sc1 lds\n" \
"v_mfma_f32_16x16x32_bf16 v[92:95], a[144:147], a[48:51], v[44:47]\n" \
"v_exp_f32_e32 v115, v115\n" \
"v_mfma_f32_16x16x32_bf16 v[92:95], a[148:151], a[52:55], v[92:95]\n" \
"v_exp_f32_e32 v112, v112\n" \
"v_mfma_f32_16x16x32_bf16 v[92:95], a[152:155], a[56:59], v[92:95]\n" \
"v_exp_f32_e32 v113, v113\n" \
"v_mfma_f32_16x16x32_bf16 v[92:95], a[156:159], a[60:63], v[92:95]\n" \
"buffer_load_dwordx4 v53, s[8:11], s64 offen offset:2048 sc1 lds\n" \
"v_mfma_f32_16x16x32_bf16 v[88:91], a[140:143], a[60:63], v[44:47]\n" \
"v_cvt_pk_bf16_f32 v117, v114, v115\n" \
"v_cvt_pk_bf16_f32 v116, v112, v113\n" \
"v_mfma_f32_16x16x32_bf16 v[88:91], a[136:139], a[56:59], v[88:91]\n" \
"v_exp_f32_e32 v120, v120\n" \
"v_mfma_f32_16x16x32_bf16 v[88:91], a[132:135], a[52:55], v[88:91]\n" \
"v_exp_f32_e32 v121, v121\n" \
"v_mfma_f32_16x16x32_bf16 v[88:91], a[128:131], a[48:51], v[88:91]\n" \
"v_cvt_pk_bf16_f32 v120, v120, v121\n" \
"s_waitcnt lgkmcnt(0)\n" \
"v_mfma_f32_16x16x32_bf16 v[128:131], a[160:163], v[100:103], v[128:131]\n" \
"buffer_load_dwordx4 v53, s[8:11], s64 offen offset:3072 sc1 lds\n" \
"v_mfma_f32_16x16x32_bf16 v[160:163], a[160:163], v[104:107], v[160:163]\n" \
"s_add_u32 m0, s93, 0x1000\n" \
"ds_read_b128 a[128:131], v48 offset:8192\n" \
"v_mfma_f32_16x16x32_bf16 v[132:135], a[164:167], v[100:103], v[132:135]\n" \
"v_exp_f32_e32 v122, v122\n" \
"v_mfma_f32_16x16x32_bf16 v[164:167], a[164:167], v[104:107], v[164:167]\n" \
"v_exp_f32_e32 v123, v123\n" \
"v_mfma_f32_16x16x32_bf16 v[136:139], a[168:171], v[100:103], v[136:139]\n" \
"buffer_load_dwordx4 v4, s[80:83], s92 offen offset:0 lds\n" \
"v_mfma_f32_16x16x32_bf16 v[168:171], a[168:171], v[104:107], v[168:171]\n" \
"v_exp_f32_e32 v124, v124\n" \
"v_mfma_f32_16x16x32_bf16 v[140:143], a[172:175], v[100:103], v[140:143]\n" \
"v_exp_f32_e32 v125, v125\n" \
"v_mfma_f32_16x16x32_bf16 v[172:175], a[172:175], v[104:107], v[172:175]\n" \
"v_exp_f32_e32 v126, v126\n" \
"v_mfma_f32_16x16x32_bf16 v[144:147], a[176:179], v[100:103], v[144:147]\n" \
"v_exp_f32_e32 v127, v127\n" \
"v_mfma_f32_16x16x32_bf16 v[176:179], a[176:179], v[104:107], v[176:179]\n" \
"v_cvt_pk_bf16_f32 v121, v122, v123\n" \
"v_cvt_pk_bf16_f32 v122, v124, v125\n" \
"v_mfma_f32_16x16x32_bf16 v[148:151], a[180:183], v[100:103], v[148:151]\n" \
"buffer_load_dwordx4 v5, s[80:83], s92 offen offset:1024 lds\n" \
"v_mfma_f32_16x16x32_bf16 v[180:183], a[180:183], v[104:107], v[180:183]\n" \
"v_cvt_pk_bf16_f32 v123, v126, v127\n" \
"ds_read_b128 a[144:147], v48 offset:12288\n" \
"v_mfma_f32_16x16x32_bf16 v[152:155], a[184:187], v[100:103], v[152:155]\n" \
"ds_read_b128 a[132:135], v48 offset:9216\n" \
"ds_read_b128 a[148:151], v48 offset:13312\n" \
"v_mfma_f32_16x16x32_bf16 v[184:187], a[184:187], v[104:107], v[184:187]\n" \
"ds_read_b128 a[136:139], v48 offset:10240\n" \
"ds_read_b128 a[152:155], v48 offset:14336\n" \
"v_mfma_f32_16x16x32_bf16 v[156:159], a[188:191], v[100:103], v[156:159]\n" \
"ds_read_b128 a[140:143], v48 offset:11264\n" \
"ds_read_b128 a[156:159], v48 offset:15360\n" \
"v_mfma_f32_16x16x32_bf16 v[188:191], a[188:191], v[104:107], v[188:191]\n" \
"buffer_load_dwordx4 v6, s[80:83], s92 offen offset:2048 lds\n" \
"v_smfmac_f32_16x16x64_bf16 a[192:195], a[200:203], v[100:107], v21\n" \
"v_exp_f32_e32 v70, v70\n" \
"v_mfma_f32_16x16x32_bf16 v[220:223], a[188:191], v[116:119], v[220:223]\n" \
"v_exp_f32_e32 v71, v71\n" \
"v_mfma_f32_16x16x32_bf16 v[252:255], a[188:191], v[120:123], v[252:255]\n" \
"buffer_load_dwordx4 v7, s[80:83], s92 offen offset:3072 lds\n" \
"v_mfma_f32_16x16x32_bf16 v[216:219], a[184:187], v[116:119], v[216:219]\n" \
"s_add_u32 m0, s93, 0x2000\n" \
"v_cvt_pk_bf16_f32 v71, v70, v71\n" \
"v_mfma_f32_16x16x32_bf16 v[248:251], a[184:187], v[120:123], v[248:251]\n" \
"v_exp_f32_e32 v72, v72\n" \
"v_mfma_f32_16x16x32_bf16 v[212:215], a[180:183], v[116:119], v[212:215]\n" \
"v_exp_f32_e32 v73, v73\n" \
"v_mfma_f32_16x16x32_bf16 v[244:247], a[180:183], v[120:123], v[244:247]\n" \
"v_exp_f32_e32 v68, v68\n" \
"v_mfma_f32_16x16x32_bf16 v[208:211], a[176:179], v[116:119], v[208:211]\n" \
"buffer_load_dwordx4 v8, s[80:83], s92 offen offset:0 lds\n" \
"v_mfma_f32_16x16x32_bf16 v[240:243], a[176:179], v[120:123], v[240:243]\n" \
"v_exp_f32_e32 v69, v69\n" \
"v_mfma_f32_16x16x32_bf16 v[204:207], a[172:175], v[116:119], v[204:207]\n" \
"v_exp_f32_e32 v74, v74\n" \
"v_mfma_f32_16x16x32_bf16 v[236:239], a[172:175], v[120:123], v[236:239]\n" \
"v_exp_f32_e32 v75, v75\n" \
"v_mfma_f32_16x16x32_bf16 v[200:203], a[168:171], v[116:119], v[200:203]\n" \
"v_exp_f32_e32 v66, v66\n" \
"v_mfma_f32_16x16x32_bf16 v[232:235], a[168:171], v[120:123], v[232:235]\n" \
"v_exp_f32_e32 v67, v67\n" \
"v_mfma_f32_16x16x32_bf16 v[196:199], a[164:167], v[116:119], v[196:199]\n" \
"v_exp_f32_e32 v64, v64\n" \
"v_mfma_f32_16x16x32_bf16 v[228:231], a[164:167], v[120:123], v[228:231]\n" \
"v_exp_f32_e32 v65, v65\n" \
"v_mfma_f32_16x16x32_bf16 v[192:195], a[160:163], v[116:119], v[192:195]\n" \
"v_cvt_pk_bf16_f32 v72, v72, v73\n" \
"v_cvt_pk_bf16_f32 v70, v68, v69\n" \
"v_mfma_f32_16x16x32_bf16 v[224:227], a[160:163], v[120:123], v[224:227]\n" \
"v_exp_f32_e32 v76, v76\n" \
"v_smfmac_f32_16x16x64_bf16 a[196:199], a[200:203], v[116:123], v21\n" \
"v_exp_f32_e32 v77, v77\n" \
"s_waitcnt vmcnt(13) lgkmcnt(0)\n" \
"s_barrier\n" \
"v_exp_f32_e32 v78, v78\n" \
"v_exp_f32_e32 v79, v79\n" \
"v_cvt_pk_bf16_f32 v73, v74, v75\n" \
"v_cvt_pk_bf16_f32 v69, v66, v67\n" \
"v_cvt_pk_bf16_f32 v68, v64, v65\n" \
"v_cvt_pk_bf16_f32 v74, v76, v77\n" \
"v_cvt_pk_bf16_f32 v75, v78, v79\n" \
"v_add_u32_e32 v48, s16, v20\n" \
"s_xor_b32 s65, s65, 0x8000\n" \
"s_add_u32 s64, s64, 0x10000\n" \
"s_getpc_b64 s[86:87]\n" \
"L_hpt_%=:\n" \
"s_add_u32 s86, s86, L_ap8-L_hpt_%=\n" \
"s_addc_u32 s87, s87, 0\n" \
"s_add_u32 s86, s86, s15\n" \
"s_addc_u32 s87, s87, 0\n" \
"s_mov_b64 exec, 0\n" \
"s_swappc_b64 s[88:89], s[86:87]\n" \
"s_mov_b64 exec, -1\n" \
"s_sub_u32 s66, s19, 1\n" \
"s_cbranch_scc1 L_la_%=\n" \
".fill (L_ab + 6464 - .) / 4, 4, 0xBF800000\n" \
"L_2_%=:\n" \
"v_mfma_f32_16x16x32_bf16 v[96:99], a[128:131], a[64:67], v[32:35]\n" \
"s_xor_b32 m0, s65, 0x12000\n" \
"ds_read_b128 a[160:163], v49 offset:0\n" \
"v_mfma_f32_16x16x32_bf16 v[96:99], a[132:135], a[68:71], v[96:99]\n" \
"buffer_load_dwordx4 v50, s[8:11], s64 offen offset:0 sc1 lds\n" \
"v_mfma_f32_16x16x32_bf16 v[96:99], a[136:139], a[72:75], v[96:99]\n" \
"ds_read_b128 a[164:167], v49 offset:1024\n" \
"ds_read_b128 a[168:171], v49 offset:2048\n" \
"v_mfma_f32_16x16x32_bf16 v[96:99], a[140:143], a[76:79], v[96:99]\n" \
"ds_read_b128 a[172:175], v49 offset:3072\n" \
"ds_read_b128 a[176:179], v49 offset:4096\n" \
"v_mfma_f32_16x16x32_bf16 v[100:103], a[156:159], a[76:79], v[32:35]\n" \
"ds_read_b128 a[180:183], v49 offset:5120\n" \
"ds_read_b128 a[184:187], v49 offset:6144\n" \
"v_mfma_f32_16x16x32_bf16 v[100:103], a[152:155], a[72:75], v[100:103]\n" \
"v_exp_f32_e64 v86, v86\n" \
"v_mfma_f32_16x16x32_bf16 v[100:103], a[148:151], a[68:71], v[100:103]\n" \
"v_exp_f32_e64 v87, v87\n" \
"v_mfma_f32_16x16x32_bf16 v[100:103], a[144:147], a[64:67], v[100:103]\n" \
"buffer_load_dwordx4 v50, s[8:11], s64 offen offset:1024 sc1 lds\n" \
"v_mfma_f32_16x16x32_bf16 v[108:111], a[144:147], a[80:83], v[36:39]\n" \
"ds_read_b128 a[188:191], v49 offset:7168\n" \
"v_cvt_pk_bf16_f32 v87, v86, v87\n" \
"v_mfma_f32_16x16x32_bf16 v[108:111], a[148:151], a[84:87], v[108:111]\n" \
"v_exp_f32_e64 v84, v84\n" \
"v_mfma_f32_16x16x32_bf16 v[108:111], a[152:155], a[88:91], v[108:111]\n" \
"v_exp_f32_e64 v85, v85\n" \
"v_mfma_f32_16x16x32_bf16 v[108:111], a[156:159], a[92:95], v[108:111]\n" \
"v_exp_f32_e64 v82, v82\n" \
"v_mfma_f32_16x16x32_bf16 v[104:107], a[140:143], a[92:95], v[36:39]\n" \
"v_exp_f32_e64 v83, v83\n" \
"v_mfma_f32_16x16x32_bf16 v[104:107], a[136:139], a[88:91], v[104:107]\n" \
"v_exp_f32_e64 v80, v80\n" \
"v_mfma_f32_16x16x32_bf16 v[104:107], a[132:135], a[84:87], v[104:107]\n" \
"v_exp_f32_e64 v81, v81\n" \
"v_mfma_f32_16x16x32_bf16 v[104:107], a[128:131], a[80:83], v[104:107]\n" \
"buffer_load_dwordx4 v50, s[8:11], s64 offen offset:2048 sc1 lds\n" \
"v_mfma_f32_16x16x32_bf16 v[112:115], a[128:131], a[96:99], v[40:43]\n" \
"v_exp_f32_e64 v88, v88\n" \
"v_mfma_f32_16x16x32_bf16 v[112:115], a[132:135], a[100:103], v[112:115]\n" \
"v_exp_f32_e64 v89, v89\n" \
"v_mfma_f32_16x16x32_bf16 v[112:115], a[136:139], a[104:107], v[112:115]\n" \
"v_cvt_pk_bf16_f32 v86, v84, v85\n" \
"v_cvt_pk_bf16_f32 v85, v82, v83\n" \
"v_mfma_f32_16x16x32_bf16 v[112:115], a[140:143], a[108:111], v[112:115]\n" \
"v_cvt_pk_bf16_f32 v84, v80, v81\n" \
"v_cvt_pk_bf16_f32 v88, v88, v89\n" \
"v_mfma_f32_16x16x32_bf16 v[116:119], a[156:159], a[108:111], v[40:43]\n" \
"v_exp_f32_e64 v90, v90\n" \
"v_mfma_f32_16x16x32_bf16 v[116:119], a[152:155], a[104:107], v[116:119]\n" \
"v_exp_f32_e64 v91, v91\n" \
"v_mfma_f32_16x16x32_bf16 v[116:119], a[148:151], a[100:103], v[116:119]\n" \
"buffer_load_dwordx4 v50, s[8:11], s64 offen offset:3072 sc1 lds\n" \
"v_mfma_f32_16x16x32_bf16 v[116:119], a[144:147], a[96:99], v[116:119]\n" \
"s_xor_b32 m0, s65, 0xa000\n" \
"v_cvt_pk_bf16_f32 v89, v90, v91\n" \
"v_mfma_f32_16x16x32_bf16 v[124:127], a[144:147], a[112:115], v[44:47]\n" \
"v_exp_f32_e64 v92, v92\n" \
"v_mfma_f32_16x16x32_bf16 v[124:127], a[148:151], a[116:119], v[124:127]\n" \
"v_exp_f32_e64 v93, v93\n" \
"v_mfma_f32_16x16x32_bf16 v[124:127], a[152:155], a[120:123], v[124:127]\n" \
"v_exp_f32_e64 v94, v94\n" \
"v_mfma_f32_16x16x32_bf16 v[124:127], a[156:159], a[124:127], v[124:127]\n" \
"v_exp_f32_e64 v95, v95\n" \
"v_mfma_f32_16x16x32_bf16 v[120:123], a[140:143], a[124:127], v[44:47]\n" \
"v_cvt_pk_bf16_f32 v90, v92, v93\n" \
"v_cvt_pk_bf16_f32 v91, v94, v95\n" \
"v_mfma_f32_16x16x32_bf16 v[120:123], a[136:139], a[120:123], v[120:123]\n" \
"ds_read_b128 a[144:147], v48 offset:4096\n" \
"ds_read_b128 a[148:151], v48 offset:5120\n" \
"v_mfma_f32_16x16x32_bf16 v[120:123], a[132:135], a[116:119], v[120:123]\n" \
"buffer_load_dwordx4 v51, s[8:11], s64 offen offset:0 sc1 lds\n" \
"v_mfma_f32_16x16x32_bf16 v[120:123], a[128:131], a[112:115], v[120:123]\n" \
"ds_read_b128 a[128:131], v48 offset:0\n" \
"s_waitcnt lgkmcnt(3)\n" \
"v_mfma_f32_16x16x32_bf16 v[128:131], a[160:163], v[68:71], v[128:131]\n" \
"ds_read_b128 a[132:135], v48 offset:1024\n" \
"ds_read_b128 a[136:139], v48 offset:2048\n" \
"v_mfma_f32_16x16x32_bf16 v[160:163], a[160:163], v[72:75], v[160:163]\n" \
"ds_read_b128 a[152:155], v48 offset:6144\n" \
"ds_read_b128 a[140:143], v48 offset:3072\n" \
"v_mfma_f32_16x16x32_bf16 v[132:135], a[164:167], v[68:71], v[132:135]\n" \
"v_exp_f32_e32 v102, v102\n" \
"v_mfma_f32_16x16x32_bf16 v[164:167], a[164:167], v[72:75], v[164:167]\n" \
"v_exp_f32_e64 v103, v103\n" \
"v_mfma_f32_16x16x32_bf16 v[136:139], a[168:171], v[68:71], v[136:139]\n" \
"ds_read_b128 a[156:159], v48 offset:7168\n" \
"v_cvt_pk_bf16_f32 v103, v102, v103\n" \
"v_mfma_f32_16x16x32_bf16 v[168:171], a[168:171], v[72:75], v[168:171]\n" \
"buffer_load_dwordx4 v51, s[8:11], s64 offen offset:1024 sc1 lds\n" \
"v_mfma_f32_16x16x32_bf16 v[140:143], a[172:175], v[68:71], v[140:143]\n" \
"v_exp_f32_e64 v100, v100\n" \
"v_mfma_f32_16x16x32_bf16 v[172:175], a[172:175], v[72:75], v[172:175]\n" \
"v_exp_f32_e64 v101, v101\n" \
"v_mfma_f32_16x16x32_bf16 v[144:147], a[176:179], v[68:71], v[144:147]\n" \
"v_exp_f32_e64 v98, v98\n" \
"v_mfma_f32_16x16x32_bf16 v[176:179], a[176:179], v[72:75], v[176:179]\n" \
"v_exp_f32_e64 v99, v99\n" \
"v_mfma_f32_16x16x32_bf16 v[148:151], a[180:183], v[68:71], v[148:151]\n" \
"v_exp_f32_e64 v96, v96\n" \
"v_mfma_f32_16x16x32_bf16 v[180:183], a[180:183], v[72:75], v[180:183]\n" \
"v_exp_f32_e64 v97, v97\n" \
"v_mfma_f32_16x16x32_bf16 v[152:155], a[184:187], v[68:71], v[152:155]\n" \
"v_exp_f32_e64 v104, v104\n" \
"v_mfma_f32_16x16x32_bf16 v[184:187], a[184:187], v[72:75], v[184:187]\n" \
"buffer_load_dwordx4 v51, s[8:11], s64 offen offset:2048 sc1 lds\n" \
"v_mfma_f32_16x16x32_bf16 v[156:159], a[188:191], v[68:71], v[156:159]\n" \
"v_exp_f32_e64 v105, v105\n" \
"v_mfma_f32_16x16x32_bf16 v[188:191], a[188:191], v[72:75], v[188:191]\n" \
"v_cvt_pk_bf16_f32 v102, v100, v101\n" \
"v_cvt_pk_bf16_f32 v101, v98, v99\n" \
"v_smfmac_f32_16x16x64_bf16 a[192:195], a[200:203], v[68:75], v21\n" \
"v_cvt_pk_bf16_f32 v100, v96, v97\n" \
"v_cvt_pk_bf16_f32 v104, v104, v105\n" \
"v_mfma_f32_16x16x32_bf16 v[220:223], a[188:191], v[84:87], v[220:223]\n" \
"v_exp_f32_e64 v106, v106\n" \
"v_mfma_f32_16x16x32_bf16 v[252:255], a[188:191], v[88:91], v[252:255]\n" \
"v_exp_f32_e64 v107, v107\n" \
"v_mfma_f32_16x16x32_bf16 v[216:219], a[184:187], v[84:87], v[216:219]\n" \
"v_exp_f32_e64 v108, v108\n" \
"v_mfma_f32_16x16x32_bf16 v[248:251], a[184:187], v[88:91], v[248:251]\n" \
"buffer_load_dwordx4 v51, s[8:11], s64 offen offset:3072 sc1 lds\n" \
"v_mfma_f32_16x16x32_bf16 v[212:215], a[180:183], v[84:87], v[212:215]\n" \
"v_exp_f32_e64 v109, v109\n" \
"v_mfma_f32_16x16x32_bf16 v[244:247], a[180:183], v[88:91], v[244:247]\n" \
"v_exp_f32_e64 v110, v110\n" \
"v_mfma_f32_16x16x32_bf16 v[208:211], a[176:179], v[84:87], v[208:211]\n" \
"v_exp_f32_e64 v111, v111\n" \
"v_mfma_f32_16x16x32_bf16 v[240:243], a[176:179], v[88:91], v[240:243]\n" \
"v_exp_f32_e64 v118, v118\n" \
"v_mfma_f32_16x16x32_bf16 v[204:207], a[172:175], v[84:87], v[204:207]\n" \
"v_exp_f32_e64 v119, v119\n" \
"v_mfma_f32_16x16x32_bf16 v[236:239], a[172:175], v[88:91], v[236:239]\n" \
"v_cvt_pk_bf16_f32 v105, v106, v107\n" \
"v_cvt_pk_bf16_f32 v106, v108, v109\n" \
"v_mfma_f32_16x16x32_bf16 v[200:203], a[168:171], v[84:87], v[200:203]\n" \
"v_cvt_pk_bf16_f32 v107, v110, v111\n" \
"v_cvt_pk_bf16_f32 v119, v118, v119\n" \
"v_mfma_f32_16x16x32_bf16 v[232:235], a[168:171], v[88:91], v[232:235]\n" \
"v_exp_f32_e64 v116, v116\n" \
"v_mfma_f32_16x16x32_bf16 v[196:199], a[164:167], v[84:87], v[196:199]\n" \
"v_exp_f32_e64 v117, v117\n" \
"v_mfma_f32_16x16x32_bf16 v[228:231], a[164:167], v[88:91], v[228:231]\n" \
"v_exp_f32_e64 v114, v114\n" \
"v_mfma_f32_16x16x32_bf16 v[192:195], a[160:163], v[84:87], v[192:195]\n" \
"v_exp_f32_e64 v115, v115\n" \
"v_mfma_f32_16x16x32_bf16 v[224:227], a[160:163], v[88:91], v[224:227]\n" \
"v_exp_f32_e64 v112, v112\n" \
"v_smfmac_f32_16x16x64_bf16 a[196:199], a[200:203], v[84:91], v21\n" \
"v_exp_f32_e32 v113, v113\n" \
"s_waitcnt vmcnt(8) lgkmcnt(0)\n" \
"s_barrier\n" \
"s_nop 0\n" \
"v_mfma_f32_16x16x32_bf16 v[64:67], a[128:131], a[64:67], v[32:35]\n" \
"s_xor_b32 m0, s65, 0x18000\n" \
"ds_read_b128 a[160:163], v49 offset:8192\n" \
"v_mfma_f32_16x16x32_bf16 v[64:67], a[132:135], a[68:71], v[64:67]\n" \
"buffer_load_dwordx4 v52, s[8:11], s64 offen offset:0 sc1 lds\n" \
"v_mfma_f32_16x16x32_bf16 v[64:67], a[136:139], a[72:75], v[64:67]\n" \
"ds_read_b128 a[164:167], v49 offset:9216\n" \
"ds_read_b128 a[168:171], v49 offset:10240\n" \
"v_mfma_f32_16x16x32_bf16 v[64:67], a[140:143], a[76:79], v[64:67]\n" \
"ds_read_b128 a[172:175], v49 offset:11264\n" \
"ds_read_b128 a[176:179], v49 offset:12288\n" \
"v_mfma_f32_16x16x32_bf16 v[68:71], a[156:159], a[76:79], v[32:35]\n" \
"ds_read_b128 a[180:183], v49 offset:13312\n" \
"ds_read_b128 a[184:187], v49 offset:14336\n" \
"v_mfma_f32_16x16x32_bf16 v[68:71], a[152:155], a[72:75], v[68:71]\n" \
"ds_read_b128 a[188:191], v49 offset:15360\n" \
"v_cvt_pk_bf16_f32 v118, v116, v117\n" \
"v_mfma_f32_16x16x32_bf16 v[68:71], a[148:151], a[68:71], v[68:71]\n" \
"v_cvt_pk_bf16_f32 v117, v114, v115\n" \
"v_cvt_pk_bf16_f32 v116, v112, v113\n" \
"v_mfma_f32_16x16x32_bf16 v[68:71], a[144:147], a[64:67], v[68:71]\n" \
"buffer_load_dwordx4 v52, s[8:11], s64 offen offset:1024 sc1 lds\n" \
"v_mfma_f32_16x16x32_bf16 v[76:79], a[144:147], a[80:83], v[36:39]\n" \
"v_exp_f32_e64 v120, v120\n" \
"v_mfma_f32_16x16x32_bf16 v[76:79], a[148:151], a[84:87], v[76:79]\n" \
"v_exp_f32_e64 v121, v121\n" \
"v_mfma_f32_16x16x32_bf16 v[76:79], a[152:155], a[88:91], v[76:79]\n" \
"v_exp_f32_e64 v122, v122\n" \
"v_mfma_f32_16x16x32_bf16 v[76:79], a[156:159], a[92:95], v[76:79]\n" \
"v_exp_f32_e64 v123, v123\n" \
"v_mfma_f32_16x16x32_bf16 v[72:75], a[140:143], a[92:95], v[36:39]\n" \
"v_cvt_pk_bf16_f32 v120, v120, v121\n" \
"v_cvt_pk_bf16_f32 v121, v122, v123\n" \
"v_mfma_f32_16x16x32_bf16 v[72:75], a[136:139], a[88:91], v[72:75]\n" \
"v_exp_f32_e64 v124, v124\n" \
"v_mfma_f32_16x16x32_bf16 v[72:75], a[132:135], a[84:87], v[72:75]\n" \
"v_exp_f32_e64 v125, v125\n" \
"v_mfma_f32_16x16x32_bf16 v[72:75], a[128:131], a[80:83], v[72:75]\n" \
"buffer_load_dwordx4 v52, s[8:11], s64 offen offset:2048 sc1 lds\n" \
"v_mfma_f32_16x16x32_bf16 v[80:83], a[128:131], a[96:99], v[40:43]\n" \
"v_exp_f32_e64 v126, v126\n" \
"v_mfma_f32_16x16x32_bf16 v[80:83], a[132:135], a[100:103], v[80:83]\n" \
"v_exp_f32_e64 v127, v127\n" \
"v_mfma_f32_16x16x32_bf16 v[80:83], a[136:139], a[104:107], v[80:83]\n" \
"v_cvt_pk_bf16_f32 v122, v124, v125\n" \
"v_cvt_pk_bf16_f32 v123, v126, v127\n" \
"v_mfma_f32_16x16x32_bf16 v[80:83], a[140:143], a[108:111], v[80:83]\n" \
"v_exp_f32_e64 v70, v70\n" \
"v_mfma_f32_16x16x32_bf16 v[84:87], a[156:159], a[108:111], v[40:43]\n" \
"v_exp_f32_e64 v71, v71\n" \
"v_mfma_f32_16x16x32_bf16 v[84:87], a[152:155], a[104:107], v[84:87]\n" \
"v_exp_f32_e64 v72, v72\n" \
"v_mfma_f32_16x16x32_bf16 v[84:87], a[148:151], a[100:103], v[84:87]\n" \
"buffer_load_dwordx4 v52, s[8:11], s64 offen offset:3072 sc1 lds\n" \
"v_mfma_f32_16x16x32_bf16 v[84:87], a[144:147], a[96:99], v[84:87]\n" \
"s_mov_b32 m0, s65\n" \
"v_cvt_pk_bf16_f32 v71, v70, v71\n" \
"v_mfma_f32_16x16x32_bf16 v[92:95], a[144:147], a[112:115], v[44:47]\n" \
"ds_read_b128 a[144:147], v48 offset:12288\n" \
"v_add_u32_e32 v49, 0x10000, v48\n" \
"v_mfma_f32_16x16x32_bf16 v[92:95], a[148:151], a[116:119], v[92:95]\n" \
"ds_read_b128 a[148:151], v48 offset:13312\n" \
"s_xor_b32 s65, s65, 0x8000\n" \
"v_mfma_f32_16x16x32_bf16 v[92:95], a[152:155], a[120:123], v[92:95]\n" \
"v_exp_f32_e32 v73, v73\n" \
"v_mfma_f32_16x16x32_bf16 v[92:95], a[156:159], a[124:127], v[92:95]\n" \
"ds_read_b128 a[152:155], v48 offset:14336\n" \
".fill (L_ab + 8192 - .) / 4, 4, 0xBF800000\n" \
"L_ap2:\n" \
"s_cbranch_execz L_ap0\n" \
"ds_read_b128 a[156:159], v48 offset:15360\n" \
"v_mfma_f32_16x16x32_bf16 v[88:91], a[140:143], a[124:127], v[44:47]\n" \
"ds_read_b128 a[140:143], v48 offset:11264\n" \
"v_cvt_pk_bf16_f32 v72, v72, v73\n" \
"v_mfma_f32_16x16x32_bf16 v[88:91], a[136:139], a[120:123], v[88:91]\n" \
"v_exp_f32_e32 v68, v68\n" \
"v_mfma_f32_16x16x32_bf16 v[88:91], a[132:135], a[116:119], v[88:91]\n" \
"buffer_load_dwordx4 v53, s[8:11], s64 offen offset:0 sc1 lds\n" \
"v_mfma_f32_16x16x32_bf16 v[88:91], a[128:131], a[112:115], v[88:91]\n" \
"ds_read_b128 a[128:131], v48 offset:8192\n" \
"s_waitcnt lgkmcnt(6)\n" \
"v_mfma_f32_16x16x32_bf16 v[128:131], a[160:163], v[100:103], v[128:131]\n" \
"ds_read_b128 a[132:135], v48 offset:9216\n" \
"ds_read_b128 a[136:139], v48 offset:10240\n" \
"v_mfma_f32_16x16x32_bf16 v[160:163], a[160:163], v[104:107], v[160:163]\n" \
"v_exp_f32_e32 v69, v69\n" \
"v_mfma_f32_16x16x32_bf16 v[132:135], a[164:167], v[100:103], v[132:135]\n" \
"v_exp_f32_e64 v74, v74\n" \
"v_mfma_f32_16x16x32_bf16 v[164:167], a[164:167], v[104:107], v[164:167]\n" \
"v_exp_f32_e64 v75, v75\n" \
"v_mfma_f32_16x16x32_bf16 v[136:139], a[168:171], v[100:103], v[136:139]\n" \
"v_exp_f32_e64 v66, v66\n" \
"v_mfma_f32_16x16x32_bf16 v[168:171], a[168:171], v[104:107], v[168:171]\n" \
"buffer_load_dwordx4 v53, s[8:11], s64 offen offset:1024 sc1 lds\n" \
"v_mfma_f32_16x16x32_bf16 v[140:143], a[172:175], v[100:103], v[140:143]\n" \
"v_exp_f32_e64 v67, v67\n" \
"v_mfma_f32_16x16x32_bf16 v[172:175], a[172:175], v[104:107], v[172:175]\n" \
"v_exp_f32_e64 v64, v64\n" \
"v_mfma_f32_16x16x32_bf16 v[144:147], a[176:179], v[100:103], v[144:147]\n" \
"v_exp_f32_e64 v65, v65\n" \
"v_mfma_f32_16x16x32_bf16 v[176:179], a[176:179], v[104:107], v[176:179]\n" \
"v_exp_f32_e64 v76, v76\n" \
"v_mfma_f32_16x16x32_bf16 v[148:151], a[180:183], v[100:103], v[148:151]\n" \
"v_exp_f32_e64 v77, v77\n" \
"v_mfma_f32_16x16x32_bf16 v[180:183], a[180:183], v[104:107], v[180:183]\n" \
"v_exp_f32_e64 v78, v78\n" \
"v_mfma_f32_16x16x32_bf16 v[152:155], a[184:187], v[100:103], v[152:155]\n" \
"v_exp_f32_e64 v79, v79\n" \
"v_mfma_f32_16x16x32_bf16 v[184:187], a[184:187], v[104:107], v[184:187]\n" \
"buffer_load_dwordx4 v53, s[8:11], s64 offen offset:2048 sc1 lds\n" \
"v_mfma_f32_16x16x32_bf16 v[156:159], a[188:191], v[100:103], v[156:159]\n" \
"v_cvt_pk_bf16_f32 v70, v68, v69\n" \
"v_cvt_pk_bf16_f32 v73, v74, v75\n" \
"v_mfma_f32_16x16x32_bf16 v[188:191], a[188:191], v[104:107], v[188:191]\n" \
"v_cvt_pk_bf16_f32 v69, v66, v67\n" \
"v_cvt_pk_bf16_f32 v68, v64, v65\n" \
"v_smfmac_f32_16x16x64_bf16 a[192:195], a[200:203], v[100:107], v21\n" \
"v_cvt_pk_bf16_f32 v74, v76, v77\n" \
"v_cvt_pk_bf16_f32 v75, v78, v79\n" \
"v_mfma_f32_16x16x32_bf16 v[220:223], a[188:191], v[116:119], v[220:223]\n" \
"v_xor_b32_e32 v48, 0x8000, v48\n" \
"v_mfma_f32_16x16x32_bf16 v[252:255], a[188:191], v[120:123], v[252:255]\n" \
"v_mfma_f32_16x16x32_bf16 v[216:219], a[184:187], v[116:119], v[216:219]\n" \
"v_mfma_f32_16x16x32_bf16 v[248:251], a[184:187], v[120:123], v[248:251]\n" \
"buffer_load_dwordx4 v53, s[8:11], s64 offen offset:3072 sc1 lds\n" \
"v_mfma_f32_16x16x32_bf16 v[212:215], a[180:183], v[116:119], v[212:215]\n" \
"s_add_u32 s64, s64, 0x10000\n" \
"s_sub_u32 s66, s66, 1\n" \
"s_nop 0\n" \
"v_mfma_f32_16x16x32_bf16 v[244:247], a[180:183], v[120:123], v[244:247]\n" \
"v_mfma_f32_16x16x32_bf16 v[208:211], a[176:179], v[116:119], v[208:211]\n" \
"v_mfma_f32_16x16x32_bf16 v[240:243], a[176:179], v[120:123], v[240:243]\n" \
"v_mfma_f32_16x16x32_bf16 v[204:207], a[172:175], v[116:119], v[204:207]\n" \
"v_mfma_f32_16x16x32_bf16 v[236:239], a[172:175], v[120:123], v[236:239]\n" \
"v_mfma_f32_16x16x32_bf16 v[200:203], a[168:171], v[116:119], v[200:203]\n" \
"v_mfma_f32_16x16x32_bf16 v[232:235], a[168:171], v[120:123], v[232:235]\n" \
"v_mfma_f32_16x16x32_bf16 v[196:199], a[164:167], v[116:119], v[196:199]\n" \
"v_mfma_f32_16x16x32_bf16 v[228:231], a[164:167], v[120:123], v[228:231]\n" \
"v_mfma_f32_16x16x32_bf16 v[192:195], a[160:163], v[116:119], v[192:195]\n" \
"v_mfma_f32_16x16x32_bf16 v[224:227], a[160:163], v[120:123], v[224:227]\n" \
"v_smfmac_f32_16x16x64_bf16 a[196:199], a[200:203], v[116:123], v21\n" \
"s_waitcnt vmcnt(8) lgkmcnt(0)\n" \
"s_barrier\n" \
"s_cbranch_scc0 L_2_%=\n" \
"s_nop 0\n" \
"L_la_%=:\n" \
"v_mfma_f32_16x16x32_bf16 v[96:99], a[128:131], a[64:67], v[32:35]\n" \
"v_bfe_u32 v27, v20, 2, 7\n" \
"v_readlane_b32 s96, v11, 1\n" \
"v_mfma_f32_16x16x32_bf16 v[96:99], a[132:135], a[68:71], v[96:99]\n" \
"s_xor_b32 m0, s65, 0x12000\n" \
"ds_read_b128 a[160:163], v49 offset:0\n" \
"v_mfma_f32_16x16x32_bf16 v[96:99], a[136:139], a[72:75], v[96:99]\n" \
"ds_read_b128 a[164:167], v49 offset:1024\n" \
"ds_read_b128 a[168:171], v49 offset:2048\n" \
"buffer_load_dword v26, v27, s[56:59], s96 offen sc1\n" \
"v_mfma_f32_16x16x32_bf16 v[96:99], a[140:143], a[76:79], v[96:99]\n" \
"buffer_load_dwordx4 v50, s[8:11], s64 offen offset:0 sc1 lds\n" \
"v_mfma_f32_16x16x32_bf16 v[100:103], a[156:159], a[76:79], v[32:35]\n" \
"ds_read_b128 a[172:175], v49 offset:3072\n" \
"ds_read_b128 a[176:179], v49 offset:4096\n" \
"v_mfma_f32_16x16x32_bf16 v[100:103], a[152:155], a[72:75], v[100:103]\n" \
"ds_read_b128 a[180:183], v49 offset:5120\n" \
"ds_read_b128 a[184:187], v49 offset:6144\n" \
"v_mfma_f32_16x16x32_bf16 v[100:103], a[148:151], a[68:71], v[100:103]\n" \
"ds_read_b128 a[188:191], v49 offset:7168\n" \
"v_readlane_b32 s97, v11, 36\n" \
"v_mfma_f32_16x16x32_bf16 v[100:103], a[144:147], a[64:67], v[100:103]\n" \
"v_exp_f32_e64 v86, v86\n" \
"v_mfma_f32_16x16x32_bf16 v[108:111], a[144:147], a[80:83], v[36:39]\n" \
"v_exp_f32_e64 v87, v87\n" \
"v_mfma_f32_16x16x32_bf16 v[108:111], a[148:151], a[84:87], v[108:111]\n" \
"v_exp_f32_e64 v84, v84\n" \
"v_mfma_f32_16x16x32_bf16 v[108:111], a[152:155], a[88:91], v[108:111]\n" \
"buffer_load_dwordx4 v50, s[8:11], s64 offen offset:1024 sc1 lds\n" \
"v_mfma_f32_16x16x32_bf16 v[108:111], a[156:159], a[92:95], v[108:111]\n" \
"v_exp_f32_e64 v85, v85\n" \
"v_mfma_f32_16x16x32_bf16 v[104:107], a[140:143], a[92:95], v[36:39]\n" \
"v_cvt_pk_bf16_f32 v87, v86, v87\n" \
"v_cvt_pk_bf16_f32 v86, v84, v85\n" \
"v_mfma_f32_16x16x32_bf16 v[104:107], a[136:139], a[88:91], v[104:107]\n" \
"v_exp_f32_e64 v82, v82\n" \
"v_mfma_f32_16x16x32_bf16 v[104:107], a[132:135], a[84:87], v[104:107]\n" \
"v_exp_f32_e64 v83, v83\n" \
"v_mfma_f32_16x16x32_bf16 v[104:107], a[128:131], a[80:83], v[104:107]\n" \
"v_exp_f32_e64 v80, v80\n" \
"v_mfma_f32_16x16x32_bf16 v[112:115], a[128:131], a[96:99], v[40:43]\n" \
"v_exp_f32_e64 v81, v81\n" \
"v_mfma_f32_16x16x32_bf16 v[112:115], a[132:135], a[100:103], v[112:115]\n" \
"v_cvt_pk_bf16_f32 v85, v82, v83\n" \
"v_cvt_pk_bf16_f32 v84, v80, v81\n" \
"v_mfma_f32_16x16x32_bf16 v[112:115], a[136:139], a[104:107], v[112:115]\n" \
"buffer_load_dwordx4 v50, s[8:11], s64 offen offset:2048 sc1 lds\n" \
"v_mfma_f32_16x16x32_bf16 v[112:115], a[140:143], a[108:111], v[112:115]\n" \
"v_exp_f32_e64 v88, v88\n" \
"v_mfma_f32_16x16x32_bf16 v[116:119], a[156:159], a[108:111], v[40:43]\n" \
"v_exp_f32_e64 v89, v89\n" \
"v_mfma_f32_16x16x32_bf16 v[116:119], a[152:155], a[104:107], v[116:119]\n" \
"v_exp_f32_e64 v90, v90\n" \
"v_mfma_f32_16x16x32_bf16 v[116:119], a[148:151], a[100:103], v[116:119]\n" \
"v_exp_f32_e64 v91, v91\n" \
"v_mfma_f32_16x16x32_bf16 v[116:119], a[144:147], a[96:99], v[116:119]\n" \
"v_cvt_pk_bf16_f32 v88, v88, v89\n" \
"v_cvt_pk_bf16_f32 v89, v90, v91\n" \
"v_mfma_f32_16x16x32_bf16 v[124:127], a[144:147], a[112:115], v[44:47]\n" \
"v_exp_f32_e64 v92, v92\n" \
"v_mfma_f32_16x16x32_bf16 v[124:127], a[148:151], a[116:119], v[124:127]\n" \
"buffer_load_dwordx4 v50, s[8:11], s64 offen offset:3072 sc1 lds\n" \
"v_mfma_f32_16x16x32_bf16 v[124:127], a[152:155], a[120:123], v[124:127]\n" \
"v_exp_f32_e64 v93, v93\n" \
"v_mfma_f32_16x16x32_bf16 v[124:127], a[156:159], a[124:127], v[124:127]\n" \
"v_exp_f32_e64 v94, v94\n" \
"v_mfma_f32_16x16x32_bf16 v[120:123], a[140:143], a[124:127], v[44:47]\n" \
"v_exp_f32_e64 v95, v95\n" \
"v_mfma_f32_16x16x32_bf16 v[120:123], a[136:139], a[120:123], v[120:123]\n" \
"v_cvt_pk_bf16_f32 v90, v92, v93\n" \
"v_cvt_pk_bf16_f32 v91, v94, v95\n" \
"v_mfma_f32_16x16x32_bf16 v[120:123], a[132:135], a[116:119], v[120:123]\n" \
"ds_read_b128 a[144:147], v48 offset:4096\n" \
"ds_read_b128 a[132:135], v48 offset:1024\n" \
"v_mfma_f32_16x16x32_bf16 v[120:123], a[128:131], a[112:115], v[120:123]\n" \
"ds_read_b128 a[128:131], v48 offset:0\n" \
"s_waitcnt lgkmcnt(3)\n" \
"s_nop 0\n" \
"v_mfma_f32_16x16x32_bf16 v[128:131], a[160:163], v[68:71], v[128:131]\n" \
"ds_read_b128 a[148:151], v48 offset:5120\n" \
"ds_read_b128 a[136:139], v48 offset:2048\n" \
"v_mfma_f32_16x16x32_bf16 v[160:163], a[160:163], v[72:75], v[160:163]\n" \
"ds_read_b128 a[152:155], v48 offset:6144\n" \
"ds_read_b128 a[140:143], v48 offset:3072\n" \
"v_mfma_f32_16x16x32_bf16 v[132:135], a[164:167], v[68:71], v[132:135]\n" \
"ds_read_b128 a[156:159], v48 offset:7168\n" \
"v_add_u32_e32 v17, 0x49000, v20\n" \
"v_mfma_f32_16x16x32_bf16 v[164:167], a[164:167], v[72:75], v[164:167]\n" \
"v_readlane_b32 s98, v11, 37\n" \
"v_readlane_b32 s93, v11, 20\n" \
"v_mfma_f32_16x16x32_bf16 v[136:139], a[168:171], v[68:71], v[136:139]\n" \
"v_readlane_b32 s84, v11, 15\n" \
"v_readlane_b32 s85, v11, 16\n" \
"v_mfma_f32_16x16x32_bf16 v[168:171], a[168:171], v[72:75], v[168:171]\n" \
"v_readlane_b32 s86, v11, 17\n" \
"s_mov_b32 s87, 0x20000\n" \
"v_mfma_f32_16x16x32_bf16 v[140:143], a[172:175], v[68:71], v[140:143]\n" \
"v_readlane_b32 s92, v11, 18\n" \
"v_readlane_b32 s91, v11, 21\n" \
"v_mfma_f32_16x16x32_bf16 v[172:175], a[172:175], v[72:75], v[172:175]\n" \
"v_exp_f32_e64 v102, v102\n" \
"v_mfma_f32_16x16x32_bf16 v[144:147], a[176:179], v[68:71], v[144:147]\n" \
"v_exp_f32_e64 v103, v103\n" \
"v_mfma_f32_16x16x32_bf16 v[176:179], a[176:179], v[72:75], v[176:179]\n" \
"v_exp_f32_e64 v100, v100\n" \
"v_mfma_f32_16x16x32_bf16 v[148:151], a[180:183], v[68:71], v[148:151]\n" \
"v_exp_f32_e64 v101, v101\n" \
"v_mfma_f32_16x16x32_bf16 v[180:183], a[180:183], v[72:75], v[180:183]\n" \
"v_cvt_pk_bf16_f32 v103, v102, v103\n" \
"v_cvt_pk_bf16_f32 v102, v100, v101\n" \
"v_mfma_f32_16x16x32_bf16 v[152:155], a[184:187], v[68:71], v[152:155]\n" \
"v_exp_f32_e64 v98, v98\n" \
"v_mfma_f32_16x16x32_bf16 v[184:187], a[184:187], v[72:75], v[184:187]\n" \
"v_exp_f32_e64 v99, v99\n" \
"v_mfma_f32_16x16x32_bf16 v[156:159], a[188:191], v[68:71], v[156:159]\n" \
"v_exp_f32_e64 v96, v96\n" \
"v_mfma_f32_16x16x32_bf16 v[188:191], a[188:191], v[72:75], v[188:191]\n" \
"s_waitcnt vmcnt(4)\n" \
"v_cmp_ne_u32_e32 vcc, s62, v26\n" \
"v_smfmac_f32_16x16x64_bf16 a[192:195], a[200:203], v[68:75], v21\n" \
"v_exp_f32_e64 v97, v97\n" \
"v_mfma_f32_16x16x32_bf16 v[220:223], a[188:191], v[84:87], v[220:223]\n" \
"v_cvt_pk_bf16_f32 v101, v98, v99\n" \
"v_cvt_pk_bf16_f32 v100, v96, v97\n" \
"v_mfma_f32_16x16x32_bf16 v[252:255], a[188:191], v[88:91], v[252:255]\n" \
"s_cbranch_vccnz L_sol_%=\n" \
"L_rol_%=:\n" \
"s_xor_b32 s65, s65, 0x8000\n" \
"buffer_load_dwordx4 a[220:223], v20, s[56:59], s97 offen offset:0 sc1\n" \
"v_mfma_f32_16x16x32_bf16 v[216:219], a[184:187], v[84:87], v[216:219]\n" \
"v_exp_f32_e32 v104, v104\n" \
"v_mfma_f32_16x16x32_bf16 v[248:251], a[184:187], v[88:91], v[248:251]\n" \
"v_exp_f32_e64 v105, v105\n" \
"v_mfma_f32_16x16x32_bf16 v[212:215], a[180:183], v[84:87], v[212:215]\n" \
"v_exp_f32_e64 v106, v106\n" \
"v_mfma_f32_16x16x32_bf16 v[244:247], a[180:183], v[88:91], v[244:247]\n" \
"v_exp_f32_e64 v107, v107\n" \
"v_mfma_f32_16x16x32_bf16 v[208:211], a[176:179], v[84:87], v[208:211]\n" \
"v_cvt_pk_bf16_f32 v104, v104, v105\n" \
"v_cvt_pk_bf16_f32 v105, v106, v107\n" \
"v_mfma_f32_16x16x32_bf16 v[240:243], a[176:179], v[88:91], v[240:243]\n" \
"buffer_load_dwordx4 a[228:231], v17, s[56:59], s97 offen offset:0 sc1\n" \
"v_mfma_f32_16x16x32_bf16 v[204:207], a[172:175], v[84:87], v[204:207]\n" \
"v_exp_f32_e64 v108, v108\n" \
"v_mfma_f32_16x16x32_bf16 v[236:239], a[172:175], v[88:91], v[236:239]\n" \
"v_exp_f32_e64 v109, v109\n" \
"v_mfma_f32_16x16x32_bf16 v[200:203], a[168:171], v[84:87], v[200:203]\n" \
"v_exp_f32_e64 v110, v110\n" \
"v_mfma_f32_16x16x32_bf16 v[232:235], a[168:171], v[88:91], v[232:235]\n" \
"v_exp_f32_e64 v111, v111\n" \
"v_mfma_f32_16x16x32_bf16 v[196:199], a[164:167], v[84:87], v[196:199]\n" \
"v_cvt_pk_bf16_f32 v106, v108, v109\n" \
"v_cvt_pk_bf16_f32 v107, v110, v111\n" \
"v_mfma_f32_16x16x32_bf16 v[228:231], a[164:167], v[88:91], v[228:231]\n" \
"v_exp_f32_e64 v118, v118\n" \
"v_mfma_f32_16x16x32_bf16 v[192:195], a[160:163], v[84:87], v[192:195]\n" \
"v_exp_f32_e64 v119, v119\n" \
"v_mfma_f32_16x16x32_bf16 v[224:227], a[160:163], v[88:91], v[224:227]\n" \
"v_exp_f32_e64 v116, v116\n" \
"v_smfmac_f32_16x16x64_bf16 a[196:199], a[200:203], v[84:91], v21\n" \
"buffer_load_dwordx4 a[236:239], v20, s[56:59], s98 offen offset:0 sc1\n" \
"s_waitcnt vmcnt(8) lgkmcnt(0)\n" \
"s_barrier\n" \
"v_mfma_f32_16x16x32_bf16 v[64:67], a[128:131], a[64:67], v[32:35]\n" \
"s_add_u32 m0, s93, 0x0\n" \
"s_nop 0\n" \
"ds_read_b128 a[160:163], v49 offset:8192\n" \
"v_mfma_f32_16x16x32_bf16 v[64:67], a[132:135], a[68:71], v[64:67]\n" \
"buffer_load_dwordx4 v0, s[84:87], s92 offen offset:0 lds\n" \
"v_mfma_f32_16x16x32_bf16 v[64:67], a[136:139], a[72:75], v[64:67]\n" \
"ds_read_b128 a[164:167], v49 offset:9216\n" \
"ds_read_b128 a[168:171], v49 offset:10240\n" \
"v_mfma_f32_16x16x32_bf16 v[64:67], a[140:143], a[76:79], v[64:67]\n" \
"ds_read_b128 a[172:175], v49 offset:11264\n" \
"ds_read_b128 a[176:179], v49 offset:12288\n" \
"v_mfma_f32_16x16x32_bf16 v[68:71], a[156:159], a[76:79], v[32:35]\n" \
"ds_read_b128 a[180:183], v49 offset:13312\n" \
"ds_read_b128 a[184:187], v49 offset:14336\n" \
"v_mfma_f32_16x16x32_bf16 v[68:71], a[152:155], a[72:75], v[68:71]\n" \
"ds_read_b128 a[188:191], v49 offset:15360\n" \
"v_cvt_pk_bf16_f32 v119, v118, v119\n" \
"v_mfma_f32_16x16x32_bf16 v[68:71], a[148:151], a[68:71], v[68:71]\n" \
"buffer_load_dwordx4 v1, s[84:87], s92 offen offset:1024 lds\n" \
"v_mfma_f32_16x16x32_bf16 v[68:71], a[144:147], a[64:67], v[68:71]\n" \
"buffer_load_dwordx4 a[244:247], v17, s[56:59], s98 offen offset:0 sc1\n" \
"v_mfma_f32_16x16x32_bf16 v[76:79], a[144:147], a[80:83], v[36:39]\n" \
"v_exp_f32_e64 v117, v117\n" \
"v_mfma_f32_16x16x32_bf16 v[76:79], a[148:151], a[84:87], v[76:79]\n" \
"v_exp_f32_e64 v114, v114\n" \
"v_mfma_f32_16x16x32_bf16 v[76:79], a[152:155], a[88:91], v[76:79]\n" \
"v_exp_f32_e64 v115, v115\n" \
"v_mfma_f32_16x16x32_bf16 v[76:79], a[156:159], a[92:95], v[76:79]\n" \
"v_exp_f32_e64 v112, v112\n" \
"v_mfma_f32_16x16x32_bf16 v[72:75], a[140:143], a[92:95], v[36:39]\n" \
"v_exp_f32_e64 v113, v113\n" \
"v_mfma_f32_16x16x32_bf16 v[72:75], a[136:139], a[88:91], v[72:75]\n" \
"buffer_load_dwordx4 v2, s[84:87], s92 offen offset:2048 lds\n" \
"v_mfma_f32_16x16x32_bf16 v[72:75], a[132:135], a[84:87], v[72:75]\n" \
"v_cvt_pk_bf16_f32 v118, v116, v117\n" \
"v_cvt_pk_bf16_f32 v117, v114, v115\n" \
"v_mfma_f32_16x16x32_bf16 v[72:75], a[128:131], a[80:83], v[72:75]\n" \
"v_cvt_pk_bf16_f32 v116, v112, v113\n" \
"v_add_u32_e32 v49, 0x10000, v48\n" \
"v_mfma_f32_16x16x32_bf16 v[80:83], a[128:131], a[96:99], v[40:43]\n" \
"buffer_load_dwordx4 a[224:227], v20, s[56:59], s97 offen offset:1024 sc1\n" \
"v_mfma_f32_16x16x32_bf16 v[80:83], a[132:135], a[100:103], v[80:83]\n" \
"v_exp_f32_e64 v120, v120\n" \
"v_mfma_f32_16x16x32_bf16 v[80:83], a[136:139], a[104:107], v[80:83]\n" \
"v_exp_f32_e64 v121, v121\n" \
"v_mfma_f32_16x16x32_bf16 v[80:83], a[140:143], a[108:111], v[80:83]\n" \
"v_exp_f32_e64 v122, v122\n" \
"v_mfma_f32_16x16x32_bf16 v[84:87], a[156:159], a[108:111], v[40:43]\n" \
"buffer_load_dwordx4 v3, s[84:87], s92 offen offset:3072 lds\n" \
"v_mfma_f32_16x16x32_bf16 v[84:87], a[152:155], a[104:107], v[84:87]\n" \
"s_add_u32 m0, s91, 0x1000\n" \
"v_cvt_pk_bf16_f32 v120, v120, v121\n" \
"v_mfma_f32_16x16x32_bf16 v[84:87], a[148:151], a[100:103], v[84:87]\n" \
"v_exp_f32_e64 v123, v123\n" \
"v_mfma_f32_16x16x32_bf16 v[84:87], a[144:147], a[96:99], v[84:87]\n" \
"v_exp_f32_e64 v124, v124\n" \
"v_mfma_f32_16x16x32_bf16 v[92:95], a[144:147], a[112:115], v[44:47]\n" \
"v_exp_f32_e64 v125, v125\n" \
"v_mfma_f32_16x16x32_bf16 v[92:95], a[148:151], a[116:119], v[92:95]\n" \
"buffer_load_dwordx4 a[232:235], v17, s[56:59], s97 offen offset:1024 sc1\n" \
"v_mfma_f32_16x16x32_bf16 v[92:95], a[152:155], a[120:123], v[92:95]\n" \
"buffer_load_dwordx4 v4, s[84:87], s92 offen offset:0 lds\n" \
"v_mfma_f32_16x16x32_bf16 v[92:95], a[156:159], a[124:127], v[92:95]\n" \
"v_exp_f32_e64 v126, v126\n" \
"v_mfma_f32_16x16x32_bf16 v[88:91], a[140:143], a[124:127], v[44:47]\n" \
"v_exp_f32_e64 v127, v127\n" \
"v_mfma_f32_16x16x32_bf16 v[88:91], a[136:139], a[120:123], v[88:91]\n" \
"v_cvt_pk_bf16_f32 v121, v122, v123\n" \
"v_cvt_pk_bf16_f32 v122, v124, v125\n" \
"v_mfma_f32_16x16x32_bf16 v[88:91], a[132:135], a[116:119], v[88:91]\n" \
"v_cvt_pk_bf16_f32 v123, v126, v127\n" \
"ds_read_b128 a[144:147], v48 offset:12288\n" \
"v_mfma_f32_16x16x32_bf16 v[88:91], a[128:131], a[112:115], v[88:91]\n" \
"ds_read_b128 a[128:131], v48 offset:8192\n" \
"s_waitcnt lgkmcnt(2)\n" \
"s_nop 0\n" \
"v_mfma_f32_16x16x32_bf16 v[128:131], a[160:163], v[100:103], v[128:131]\n" \
"ds_read_b128 a[132:135], v48 offset:9216\n" \
"ds_read_b128 a[148:151], v48 offset:13312\n" \
"v_mfma_f32_16x16x32_bf16 v[160:163], a[160:163], v[104:107], v[160:163]\n" \
"buffer_load_dwordx4 v5, s[84:87], s92 offen offset:1024 lds\n" \
"v_mfma_f32_16x16x32_bf16 v[132:135], a[164:167], v[100:103], v[132:135]\n" \
"buffer_load_dwordx4 a[240:243], v20, s[56:59], s98 offen offset:1024 sc1\n" \
"v_mfma_f32_16x16x32_bf16 v[164:167], a[164:167], v[104:107], v[164:167]\n" \
"ds_read_b128 a[136:139], v48 offset:10240\n" \
"ds_read_b128 a[152:155], v48 offset:14336\n" \
"v_mfma_f32_16x16x32_bf16 v[136:139], a[168:171], v[100:103], v[136:139]\n" \
"ds_read_b128 a[140:143], v48 offset:11264\n" \
"ds_read_b128 a[156:159], v48 offset:15360\n" \
"v_mfma_f32_16x16x32_bf16 v[168:171], a[168:171], v[104:107], v[168:171]\n" \
"v_exp_f32_e64 v70, v70\n" \
"v_mfma_f32_16x16x32_bf16 v[140:143], a[172:175], v[100:103], v[140:143]\n" \
"v_exp_f32_e64 v71, v71\n" \
"v_mfma_f32_16x16x32_bf16 v[172:175], a[172:175], v[104:107], v[172:175]\n" \
"v_exp_f32_e64 v72, v72\n" \
"v_mfma_f32_16x16x32_bf16 v[144:147], a[176:179], v[100:103], v[144:147]\n" \
"buffer_load_dwordx4 v6, s[84:87], s92 offen offset:2048 lds\n" \
"v_mfma_f32_16x16x32_bf16 v[176:179], a[176:179], v[104:107], v[176:179]\n" \
"v_exp_f32_e64 v73, v73\n" \
"v_mfma_f32_16x16x32_bf16 v[148:151], a[180:183], v[100:103], v[148:151]\n" \
"v_exp_f32_e64 v68, v68\n" \
"v_mfma_f32_16x16x32_bf16 v[180:183], a[180:183], v[104:107], v[180:183]\n" \
"buffer_load_dwordx4 a[248:251], v17, s[56:59], s98 offen offset:1024 sc1\n" \
"v_mfma_f32_16x16x32_bf16 v[152:155], a[184:187], v[100:103], v[152:155]\n" \
"v_exp_f32_e64 v69, v69\n" \
"v_mfma_f32_16x16x32_bf16 v[184:187], a[184:187], v[104:107], v[184:187]\n" \
"v_exp_f32_e64 v74, v74\n" \
"v_mfma_f32_16x16x32_bf16 v[156:159], a[188:191], v[100:103], v[156:159]\n" \
"buffer_load_dwordx4 v7, s[84:87], s92 offen offset:3072 lds\n" \
"v_mfma_f32_16x16x32_bf16 v[188:191], a[188:191], v[104:107], v[188:191]\n" \
"s_add_u32 m0, s91, 0x2000\n" \
"v_cvt_pk_bf16_f32 v71, v70, v71\n" \
"v_smfmac_f32_16x16x64_bf16 a[192:195], a[200:203], v[100:107], v21\n" \
"v_exp_f32_e64 v75, v75\n" \
"v_mfma_f32_16x16x32_bf16 v[220:223], a[188:191], v[116:119], v[220:223]\n" \
"v_exp_f32_e64 v66, v66\n" \
"v_mfma_f32_16x16x32_bf16 v[252:255], a[188:191], v[120:123], v[252:255]\n" \
"v_exp_f32_e64 v67, v67\n" \
"v_mfma_f32_16x16x32_bf16 v[216:219], a[184:187], v[116:119], v[216:219]\n" \
"v_exp_f32_e64 v64, v64\n" \
"v_mfma_f32_16x16x32_bf16 v[248:251], a[184:187], v[120:123], v[248:251]\n" \
"v_exp_f32_e64 v65, v65\n" \
"v_mfma_f32_16x16x32_bf16 v[212:215], a[180:183], v[116:119], v[212:215]\n" \
"buffer_load_dwordx4 v8, s[84:87], s92 offen offset:0 lds\n" \
"v_mfma_f32_16x16x32_bf16 v[244:247], a[180:183], v[120:123], v[244:247]\n" \
"v_cvt_pk_bf16_f32 v72, v72, v73\n" \
"v_cvt_pk_bf16_f32 v70, v68, v69\n" \
"v_mfma_f32_16x16x32_bf16 v[208:211], a[176:179], v[116:119], v[208:211]\n" \
"v_exp_f32_e64 v76, v76\n" \
"v_mfma_f32_16x16x32_bf16 v[240:243], a[176:179], v[120:123], v[240:243]\n" \
"v_exp_f32_e64 v77, v77\n" \
"v_mfma_f32_16x16x32_bf16 v[204:207], a[172:175], v[116:119], v[204:207]\n" \
"v_exp_f32_e64 v78, v78\n" \
"v_mfma_f32_16x16x32_bf16 v[236:239], a[172:175], v[120:123], v[236:239]\n" \
"v_exp_f32_e64 v79, v79\n" \
"v_mfma_f32_16x16x32_bf16 v[200:203], a[168:171], v[116:119], v[200:203]\n" \
"v_cvt_pk_bf16_f32 v73, v74, v75\n" \
"v_cvt_pk_bf16_f32 v69, v66, v67\n" \
"v_mfma_f32_16x16x32_bf16 v[232:235], a[168:171], v[120:123], v[232:235]\n" \
"v_cvt_pk_bf16_f32 v68, v64, v65\n" \
"v_cvt_pk_bf16_f32 v74, v76, v77\n" \
"v_mfma_f32_16x16x32_bf16 v[196:199], a[164:167], v[116:119], v[196:199]\n" \
"v_cvt_pk_bf16_f32 v75, v78, v79\n" \
"v_xor_b32_e32 v48, 0x8000, v48\n" \
"v_mfma_f32_16x16x32_bf16 v[228:231], a[164:167], v[120:123], v[228:231]\n" \
"s_add_u32 s64, s64, 0x10000\n" \
"v_mfma_f32_16x16x32_bf16 v[192:195], a[160:163], v[116:119], v[192:195]\n" \
"v_mfma_f32_16x16x32_bf16 v[224:227], a[160:163], v[120:123], v[224:227]\n" \
"v_smfmac_f32_16x16x64_bf16 a[196:199], a[200:203], v[116:123], v21\n" \
"s_waitcnt vmcnt(14) lgkmcnt(0)\n" \
"s_barrier\n" \
"s_cmp_lg_u32 s16, 0\n" \
"s_cbranch_scc1 L_f1_%=\n" \
"ds_read_b128 a[160:163], v49 offset:0\n" \
"ds_read_b128 a[164:167], v49 offset:1024\n" \
"ds_read_b128 a[168:171], v49 offset:2048\n" \
"v_mfma_f32_16x16x32_bf16 v[120:123], a[128:131], a[112:115], v[44:47]\n" \
"ds_read_b128 a[172:175], v49 offset:3072\n" \
"ds_read_b128 a[176:179], v49 offset:4096\n" \
"v_mfma_f32_16x16x32_bf16 v[120:123], a[132:135], a[116:119], v[120:123]\n" \
"v_add_u32_e32 v54, 0x49000, v20\n" \
"ds_read_b128 a[180:183], v49 offset:5120\n" \
"v_mfma_f32_16x16x32_bf16 v[120:123], a[136:139], a[120:123], v[120:123]\n" \
"buffer_load_dwordx4 a[96:99], v20, s[56:59], s60 offen offset:0 sc1\n" \
"v_mfma_f32_16x16x32_bf16 v[120:123], a[140:143], a[124:127], v[120:123]\n" \
"ds_read_b128 a[184:187], v49 offset:6144\n" \
"ds_read_b128 a[188:191], v49 offset:7168\n" \
"v_mfma_f32_16x16x32_bf16 v[124:127], a[156:159], a[124:127], v[44:47]\n" \
"v_exp_f32_e32 v86, v86\n" \
"v_mfma_f32_16x16x32_bf16 v[124:127], a[152:155], a[120:123], v[124:127]\n" \
"v_exp_f32_e32 v87, v87\n" \
"v_mfma_f32_16x16x32_bf16 v[124:127], a[148:151], a[116:119], v[124:127]\n" \
"v_exp_f32_e32 v84, v84\n" \
"v_mfma_f32_16x16x32_bf16 v[124:127], a[144:147], a[112:115], v[124:127]\n" \
"v_exp_f32_e32 v85, v85\n" \
"v_cvt_pk_bf16_f32 v87, v86, v87\n" \
"buffer_load_dwordx4 a[112:115], v54, s[56:59], s60 offen offset:0 sc1\n" \
"v_exp_f32_e32 v82, v82\n" \
"v_exp_f32_e32 v83, v83\n" \
"s_waitcnt lgkmcnt(7)\n" \
"v_mfma_f32_16x16x32_bf16 v[128:131], a[160:163], v[68:71], v[128:131]\n" \
"v_exp_f32_e32 v80, v80\n" \
"v_mfma_f32_16x16x32_bf16 v[160:163], a[160:163], v[72:75], v[160:163]\n" \
"v_cvt_pk_bf16_f32 v86, v84, v85\n" \
"s_waitcnt lgkmcnt(6)\n" \
"v_mfma_f32_16x16x32_bf16 v[132:135], a[164:167], v[68:71], v[132:135]\n" \
"v_exp_f32_e32 v81, v81\n" \
"v_mfma_f32_16x16x32_bf16 v[164:167], a[164:167], v[72:75], v[164:167]\n" \
"v_cvt_pk_bf16_f32 v85, v82, v83\n" \
"s_waitcnt lgkmcnt(5)\n" \
"v_mfma_f32_16x16x32_bf16 v[136:139], a[168:171], v[68:71], v[136:139]\n" \
"buffer_load_dwordx4 a[100:103], v20, s[56:59], s60 offen offset:1024 sc1\n" \
"v_mfma_f32_16x16x32_bf16 v[168:171], a[168:171], v[72:75], v[168:171]\n" \
"v_cvt_pk_bf16_f32 v84, v80, v81\n" \
"s_waitcnt lgkmcnt(2)\n" \
"v_mfma_f32_16x16x32_bf16 v[140:143], a[172:175], v[68:71], v[140:143]\n" \
"v_and_b32_e32 v84, v12, v84\n" \
"v_and_b32_e32 v85, v13, v85\n" \
"v_mfma_f32_16x16x32_bf16 v[172:175], a[172:175], v[72:75], v[172:175]\n" \
"v_and_b32_e32 v86, v14, v86\n" \
"v_and_b32_e32 v87, v15, v87\n" \
"v_mfma_f32_16x16x32_bf16 v[144:147], a[176:179], v[68:71], v[144:147]\n" \
"v_exp_f32_e32 v88, v88\n" \
"v_mfma_f32_16x16x32_bf16 v[176:179], a[176:179], v[72:75], v[176:179]\n" \
"v_exp_f32_e32 v89, v89\n" \
"v_mfma_f32_16x16x32_bf16 v[148:151], a[180:183], v[68:71], v[148:151]\n" \
"buffer_load_dwordx4 a[116:119], v54, s[56:59], s60 offen offset:1024 sc1\n" \
"v_mfma_f32_16x16x32_bf16 v[180:183], a[180:183], v[72:75], v[180:183]\n" \
"v_cvt_pk_bf16_f32 v88, v88, v89\n" \
"s_waitcnt lgkmcnt(0)\n" \
"v_mfma_f32_16x16x32_bf16 v[152:155], a[184:187], v[68:71], v[152:155]\n" \
"v_exp_f32_e32 v90, v90\n" \
"v_mfma_f32_16x16x32_bf16 v[184:187], a[184:187], v[72:75], v[184:187]\n" \
"v_exp_f32_e32 v91, v91\n" \
"v_mfma_f32_16x16x32_bf16 v[156:159], a[188:191], v[68:71], v[156:159]\n" \
"v_exp_f32_e32 v92, v92\n" \
"v_mfma_f32_16x16x32_bf16 v[188:191], a[188:191], v[72:75], v[188:191]\n" \
"v_exp_f32_e32 v93, v93\n" \
"v_smfmac_f32_16x16x64_bf16 a[192:195], a[200:203], v[68:75], v21\n" \
"buffer_load_dwordx4 a[104:107], v20, s[56:59], s60 offen offset:2048 sc1\n" \
"v_mfma_f32_16x16x32_bf16 v[220:223], a[188:191], v[84:87], v[220:223]\n" \
"v_exp_f32_e32 v94, v94\n" \
"v_exp_f32_e32 v95, v95\n" \
"v_cvt_pk_bf16_f32 v128, v128, v129\n" \
"v_cvt_pk_bf16_f32 v160, v160, v161\n" \
"v_cvt_pk_bf16_f32 v89, v90, v91\n" \
"v_cvt_pk_bf16_f32 v129, v130, v131\n" \
"v_cvt_pk_bf16_f32 v161, v162, v163\n" \
"v_cvt_pk_bf16_f32 v90, v92, v93\n" \
"v_cvt_pk_bf16_f32 v91, v94, v95\n" \
"v_cvt_pk_bf16_f32 v130, v132, v133\n" \
"v_cvt_pk_bf16_f32 v131, v134, v135\n" \
"v_cvt_pk_bf16_f32 v162, v164, v165\n" \
"v_cvt_pk_bf16_f32 v163, v166, v167\n" \
"v_mfma_f32_16x16x32_bf16 v[252:255], a[188:191], v[88:91], v[252:255]\n" \
"v_add_u32_e32 v50, s15, v20\n" \
"v_cvt_pk_bf16_f32 v132, v136, v137\n" \
"buffer_load_dwordx4 a[120:123], v54, s[56:59], s60 offen offset:2048 sc1\n" \
"v_cvt_pk_bf16_f32 v133, v138, v139\n" \
"v_cvt_pk_bf16_f32 v134, v140, v141\n" \
"v_cvt_pk_bf16_f32 v135, v142, v143\n" \
"v_cvt_pk_bf16_f32 v164, v168, v169\n" \
"v_cvt_pk_bf16_f32 v165, v170, v171\n" \
"v_cvt_pk_bf16_f32 v166, v172, v173\n" \
"v_cvt_pk_bf16_f32 v167, v174, v175\n" \
"v_add_u32_e32 v51, 0x10000, v50\n" \
"v_cvt_pk_bf16_f32 v136, v144, v145\n" \
"v_cvt_pk_bf16_f32 v137, v146, v147\n" \
"v_cvt_pk_bf16_f32 v138, v148, v149\n" \
"v_cvt_pk_bf16_f32 v139, v150, v151\n" \
"v_cvt_pk_bf16_f32 v140, v152, v153\n" \
"v_cvt_pk_bf16_f32 v141, v154, v155\n" \
"v_cvt_pk_bf16_f32 v142, v156, v157\n" \
"v_cvt_pk_bf16_f32 v143, v158, v159\n" \
"v_cvt_pk_bf16_f32 v168, v176, v177\n" \
"v_cvt_pk_bf16_f32 v169, v178, v179\n" \
"v_cvt_pk_bf16_f32 v170, v180, v181\n" \
"v_cvt_pk_bf16_f32 v171, v182, v183\n" \
"v_cvt_pk_bf16_f32 v172, v184, v185\n" \
"buffer_load_dwordx4 a[108:111], v20, s[56:59], s60 offen offset:3072 sc1\n" \
"v_cvt_pk_bf16_f32 v173, v186, v187\n" \
"v_cvt_pk_bf16_f32 v174, v188, v189\n" \
"v_cvt_pk_bf16_f32 v175, v190, v191\n" \
"ds_write_b128 v50, v[128:131] offset:0\n" \
"v_mfma_f32_16x16x32_bf16 v[216:219], a[184:187], v[84:87], v[216:219]\n" \
"ds_read_b32 v60, v18 offset:0\n" \
"ds_read_b32 v61, v18 offset:128\n" \
"v_mfma_f32_16x16x32_bf16 v[248:251], a[184:187], v[88:91], v[248:251]\n" \
"ds_write_b128 v50, v[132:135] offset:1024\n" \
"v_mfma_f32_16x16x32_bf16 v[212:215], a[180:183], v[84:87], v[212:215]\n" \
"ds_read_b128 a[0:3], v49 offset:8192\n" \
"ds_read_b128 a[4:7], v49 offset:9216\n" \
"v_mfma_f32_16x16x32_bf16 v[244:247], a[180:183], v[88:91], v[244:247]\n" \
"ds_write_b128 v50, v[136:139] offset:2048\n" \
"buffer_load_dwordx4 a[124:127], v54, s[56:59], s60 offen offset:3072 sc1\n" \
"ds_read_b128 a[8:11], v49 offset:10240\n" \
"ds_read_b128 a[12:15], v49 offset:11264\n" \
"ds_read_b128 a[16:19], v49 offset:12288\n" \
"ds_read_b128 a[20:23], v49 offset:13312\n" \
"ds_write_b128 v50, v[140:143] offset:3072\n" \
"ds_read_b128 a[24:27], v49 offset:14336\n" \
"ds_read_b128 a[28:31], v49 offset:15360\n" \
"v_mfma_f32_16x16x32_bf16 v[208:211], a[176:179], v[84:87], v[208:211]\n" \
"v_accvgpr_read_b32 v40, a192\n" \
"v_accvgpr_read_b32 v22, a193\n" \
"ds_write_b128 v51, v[160:163] offset:32768\n" \
"v_mfma_f32_16x16x32_bf16 v[240:243], a[176:179], v[88:91], v[240:243]\n" \
"v_accvgpr_read_b32 v41, a194\n" \
"v_accvgpr_read_b32 v23, a195\n" \
"v_mfma_f32_16x16x32_bf16 v[204:207], a[172:175], v[84:87], v[204:207]\n" \
"ds_write_b128 v51, v[164:167] offset:33792\n" \
"v_mfma_f32_16x16x32_bf16 v[236:239], a[172:175], v[88:91], v[236:239]\n" \
"v_add_f32_e32 v40, v40, v22\n" \
"v_add_f32_e32 v41, v41, v23\n" \
"v_mfma_f32_16x16x32_bf16 v[200:203], a[168:171], v[84:87], v[200:203]\n" \
"ds_write_b128 v51, v[168:171] offset:34816\n" \
"v_mfma_f32_16x16x32_bf16 v[232:235], a[168:171], v[88:91], v[232:235]\n" \
"ds_write_b64 v19, v[40:41]\n" \
"v_lshl_add_u32 v52, s15, 2, v20\n" \
"v_mfma_f32_16x16x32_bf16 v[196:199], a[164:167], v[84:87], v[196:199]\n" \
".fill (L_ab + 12288 - .) / 4, 4, 0xBF800000\n" \
"L_ap3:\n" \
"s_cbranch_execz L_ap0\n" \
"ds_write_b128 v51, v[172:175] offset:35840\n" \
"v_mfma_f32_16x16x32_bf16 v[228:231], a[164:167], v[88:91], v[228:231]\n" \
"v_add_u32_e32 v52, 0x1a000, v52\n" \
"v_xor_b32_e32 v63, 0x400, v19\n" \
"v_mfma_f32_16x16x32_bf16 v[192:195], a[160:163], v[84:87], v[192:195]\n" \
"v_exp_f32_e32 v120, v120\n" \
"v_mfma_f32_16x16x32_bf16 v[224:227], a[160:163], v[88:91], v[224:227]\n" \
"v_exp_f32_e32 v121, v121\n" \
"v_smfmac_f32_16x16x64_bf16 a[196:199], a[200:203], v[84:91], v21\n" \
"v_exp_f32_e32 v122, v122\n" \
"v_exp_f32_e32 v123, v123\n" \
"v_cvt_pk_bf16_f32 v120, v120, v121\n" \
"v_exp_f32_e32 v124, v124\n" \
"v_exp_f32_e32 v125, v125\n" \
"v_exp_f32_e32 v126, v126\n" \
"v_exp_f32_e32 v127, v127\n" \
"v_cvt_pk_bf16_f32 v121, v122, v123\n" \
"v_cvt_pk_bf16_f32 v122, v124, v125\n" \
"v_cvt_pk_bf16_f32 v123, v126, v127\n" \
"v_and_b32_e32 v120, v12, v120\n" \
"v_and_b32_e32 v121, v13, v121\n" \
"v_and_b32_e32 v122, v14, v122\n" \
"v_and_b32_e32 v123, v15, v123\n" \
"v_readlane_b32 s93, v11, 22\n" \
"v_readlane_b32 s92, v11, 27\n" \
"s_add_u32 m0, s93, 0x0\n" \
"s_waitcnt lgkmcnt(0)\n" \
"s_barrier\n" \
"ds_read_b128 v[144:147], v52 offset:0\n" \
"ds_read_b128 v[148:151], v52 offset:1024\n" \
"ds_read_b128 v[152:155], v52 offset:2048\n" \
"ds_read_b128 v[156:159], v52 offset:3072\n" \
"ds_read_b128 v[176:179], v52 offset:4096\n" \
"ds_read_b128 v[180:183], v52 offset:5120\n" \
"ds_read_b128 v[184:187], v52 offset:6144\n" \
"ds_read_b128 v[188:191], v52 offset:7168\n" \
"v_mfma_f32_16x16x32_bf16 a[196:199], a[212:215], v[120:123], a[196:199]\n" \
"ds_read_b64 v[42:43], v63\n" \
"v_mov_b32_e32 v130, v28\n" \
"v_mfma_f32_16x16x32_bf16 v[224:227], a[0:3], v[120:123], v[224:227]\n" \
"v_mov_b32_e32 v131, v29\n" \
"v_mov_b32_e32 v132, 0\n" \
"v_mfma_f32_16x16x32_bf16 v[228:231], a[4:7], v[120:123], v[228:231]\n" \
"v_mov_b32_e32 v133, 0\n" \
"v_mov_b32_e32 v128, 0\n" \
"v_mfma_f32_16x16x32_bf16 v[232:235], a[8:11], v[120:123], v[232:235]\n" \
"v_mov_b32_e32 v129, 0\n" \
"v_accvgpr_read_b32 v56, a196\n" \
"v_mfma_f32_16x16x32_bf16 v[236:239], a[12:15], v[120:123], v[236:239]\n" \
"v_accvgpr_read_b32 v24, a197\n" \
"v_add_f32_e32 v56, v56, v24\n" \
"v_mfma_f32_16x16x32_bf16 v[240:243], a[16:19], v[120:123], v[240:243]\n" \
"v_accvgpr_read_b32 v57, a198\n" \
"v_accvgpr_read_b32 v25, a199\n" \
"v_mfma_f32_16x16x32_bf16 v[244:247], a[20:23], v[120:123], v[244:247]\n" \
"v_add_f32_e32 v57, v57, v25\n" \
"v_mfma_f32_16x16x32_bf16 v[248:251], a[24:27], v[120:123], v[248:251]\n" \
"v_mfma_f32_16x16x32_bf16 v[252:255], a[28:31], v[120:123], v[252:255]\n" \
"s_waitcnt vmcnt(8) lgkmcnt(1)\n" \
"s_barrier\n" \
"v_mfma_f32_16x16x32_bf16 v[192:195], v[130:133], v[144:147], v[192:195]\n" \
"buffer_load_dwordx4 v0, s[80:83], s92 offen offset:0 lds\n" \
"v_mfma_f32_16x16x32_bf16 v[196:199], v[128:131], v[144:147], v[196:199]\n" \
"s_waitcnt lgkmcnt(0)\n" \
"v_add_f32_e32 v56, v56, v42\n" \
"v_mfma_f32_16x16x32_bf16 v[200:203], v[130:133], v[148:151], v[200:203]\n" \
"v_max_f32_e32 v62, v62, v56\n" \
"v_add_f32_e32 v57, v57, v43\n" \
"v_mfma_f32_16x16x32_bf16 v[204:207], v[128:131], v[148:151], v[204:207]\n" \
"v_rcp_f32_e32 v56, v56\n" \
"v_mfma_f32_16x16x32_bf16 v[208:211], v[130:133], v[152:155], v[208:211]\n" \
"v_mul_f32_e32 v56, v56, v60\n" \
"v_mul_f32_e32 v192, v56, v192\n" \
"v_mfma_f32_16x16x32_bf16 v[212:215], v[128:131], v[152:155], v[212:215]\n" \
"v_mul_f32_e32 v193, v56, v193\n" \
"v_mul_f32_e32 v194, v56, v194\n" \
"v_mfma_f32_16x16x32_bf16 v[216:219], v[130:133], v[156:159], v[216:219]\n" \
"buffer_load_dwordx4 v1, s[80:83], s92 offen offset:1024 lds\n" \
"v_mfma_f32_16x16x32_bf16 v[220:223], v[128:131], v[156:159], v[220:223]\n" \
"v_mul_f32_e32 v195, v56, v195\n" \
"v_mul_f32_e32 v196, v56, v196\n" \
"v_mfma_f32_16x16x32_bf16 v[224:227], v[130:133], v[176:179], v[224:227]\n" \
"v_mul_f32_e32 v197, v56, v197\n" \
"v_mul_f32_e32 v198, v56, v198\n" \
"v_mfma_f32_16x16x32_bf16 v[228:231], v[128:131], v[176:179], v[228:231]\n" \
"v_mul_f32_e32 v199, v56, v199\n" \
"v_cvt_pk_bf16_f32 v64, v192, v193\n" \
"v_mfma_f32_16x16x32_bf16 v[232:235], v[130:133], v[180:183], v[232:235]\n" \
"v_cvt_pk_bf16_f32 v65, v194, v195\n" \
"v_cvt_pk_bf16_f32 v66, v196, v197\n" \
"v_mfma_f32_16x16x32_bf16 v[236:239], v[128:131], v[180:183], v[236:239]\n" \
"v_cvt_pk_bf16_f32 v67, v198, v199\n" \
"v_mul_f32_e32 v200, v56, v200\n" \
"v_mfma_f32_16x16x32_bf16 v[240:243], v[130:133], v[184:187], v[240:243]\n" \
"buffer_load_dwordx4 v2, s[80:83], s92 offen offset:2048 lds\n" \
"v_mfma_f32_16x16x32_bf16 a[208:211], a[216:219], v[64:67], a[208:211]\n" \
"v_mul_f32_e32 v201, v56, v201\n" \
"v_mul_f32_e32 v202, v56, v202\n" \
"v_mul_f32_e32 v203, v56, v203\n" \
"v_mul_f32_e32 v204, v56, v204\n" \
"v_mul_f32_e32 v205, v56, v205\n" \
"v_mul_f32_e32 v206, v56, v206\n" \
"v_mul_f32_e32 v207, v56, v207\n" \
"v_cvt_pk_bf16_f32 v68, v200, v201\n" \
"v_cvt_pk_bf16_f32 v69, v202, v203\n" \
"v_cvt_pk_bf16_f32 v70, v204, v205\n" \
"v_cvt_pk_bf16_f32 v71, v206, v207\n" \
"v_mul_f32_e32 v208, v56, v208\n" \
"v_mul_f32_e32 v209, v56, v209\n" \
"v_mfma_f32_16x16x32_bf16 a[208:211], a[216:219], v[68:71], a[208:211]\n" \
"v_mul_f32_e32 v210, v56, v210\n" \
"v_mul_f32_e32 v211, v56, v211\n" \
"v_mul_f32_e32 v212, v56, v212\n" \
"v_mul_f32_e32 v213, v56, v213\n" \
"buffer_load_dwordx4 v3, s[80:83], s92 offen offset:3072 lds\n" \
"s_add_u32 m0, s93, 0x1000\n" \
"v_mul_f32_e32 v214, v56, v214\n" \
"v_mul_f32_e32 v215, v56, v215\n" \
"v_max_f32_e32 v62, v62, v57\n" \
"v_cvt_pk_bf16_f32 v72, v208, v209\n" \
"v_cvt_pk_bf16_f32 v73, v210, v211\n" \
"v_cvt_pk_bf16_f32 v74, v212, v213\n" \
"v_cvt_pk_bf16_f32 v75, v214, v215\n" \
"v_rcp_f32_e32 v57, v57\n" \
"v_mfma_f32_16x16x32_bf16 v[244:247], v[128:131], v[184:187], v[244:247]\n" \
"v_mfma_f32_16x16x32_bf16 a[208:211], a[216:219], v[72:75], a[208:211]\n" \
"v_mul_f32_e32 v216, v56, v216\n" \
"v_mul_f32_e32 v217, v56, v217\n" \
"v_mul_f32_e32 v218, v56, v218\n" \
"v_mul_f32_e32 v219, v56, v219\n" \
"v_mul_f32_e32 v220, v56, v220\n" \
"buffer_load_dwordx4 v4, s[80:83], s92 offen offset:0 lds\n" \
"v_mul_f32_e32 v221, v56, v221\n" \
"v_mul_f32_e32 v222, v56, v222\n" \
"v_mul_f32_e32 v223, v56, v223\n" \
"v_cvt_pk_bf16_f32 v76, v216, v217\n" \
"v_cvt_pk_bf16_f32 v77, v218, v219\n" \
"v_cvt_pk_bf16_f32 v78, v220, v221\n" \
"v_cvt_pk_bf16_f32 v79, v222, v223\n" \
"v_mul_f32_e32 v57, v57, v61\n" \
"v_mul_f32_e32 v224, v57, v224\n" \
"v_mfma_f32_16x16x32_bf16 a[208:211], a[216:219], v[76:79], a[208:211]\n" \
"v_mul_f32_e32 v225, v57, v225\n" \
"v_mul_f32_e32 v226, v57, v226\n" \
"v_mul_f32_e32 v227, v57, v227\n" \
"v_mul_f32_e32 v228, v57, v228\n" \
"v_mul_f32_e32 v229, v57, v229\n" \
"v_mul_f32_e32 v230, v57, v230\n" \
"v_mul_f32_e32 v231, v57, v231\n" \
"v_cvt_pk_bf16_f32 v80, v224, v225\n" \
"v_cvt_pk_bf16_f32 v81, v226, v227\n" \
"v_cvt_pk_bf16_f32 v82, v228, v229\n" \
"buffer_load_dwordx4 v5, s[80:83], s92 offen offset:1024 lds\n" \
"v_cvt_pk_bf16_f32 v83, v230, v231\n" \
"v_mfma_f32_16x16x32_bf16 v[248:251], v[130:133], v[188:191], v[248:251]\n" \
"v_mul_f32_e32 v232, v57, v232\n" \
"v_mul_f32_e32 v233, v57, v233\n" \
"v_mfma_f32_16x16x32_bf16 a[208:211], a[216:219], v[80:83], a[208:211]\n" \
"v_mul_f32_e32 v234, v57, v234\n" \
"v_mul_f32_e32 v235, v57, v235\n" \
"v_mul_f32_e32 v236, v57, v236\n" \
"v_mul_f32_e32 v237, v57, v237\n" \
"v_mul_f32_e32 v238, v57, v238\n" \
"v_mul_f32_e32 v239, v57, v239\n" \
"v_cvt_pk_bf16_f32 v84, v232, v233\n" \
"v_cvt_pk_bf16_f32 v85, v234, v235\n" \
"v_cvt_pk_bf16_f32 v86, v236, v237\n" \
"v_cvt_pk_bf16_f32 v87, v238, v239\n" \
"v_mul_f32_e32 v240, v57, v240\n" \
"v_mul_f32_e32 v241, v57, v241\n" \
"v_mfma_f32_16x16x32_bf16 a[208:211], a[216:219], v[84:87], a[208:211]\n" \
"buffer_load_dwordx4 v6, s[80:83], s92 offen offset:2048 lds\n" \
"v_mul_f32_e32 v242, v57, v242\n" \
"v_mul_f32_e32 v243, v57, v243\n" \
"v_mul_f32_e32 v244, v57, v244\n" \
"v_mul_f32_e32 v245, v57, v245\n" \
"v_mul_f32_e32 v246, v57, v246\n" \
"v_mul_f32_e32 v247, v57, v247\n" \
"v_cvt_pk_bf16_f32 v96, v240, v241\n" \
"v_cvt_pk_bf16_f32 v97, v242, v243\n" \
"v_cvt_pk_bf16_f32 v98, v244, v245\n" \
"v_cvt_pk_bf16_f32 v99, v246, v247\n" \
"v_mfma_f32_16x16x32_bf16 v[252:255], v[128:131], v[188:191], v[252:255]\n" \
"v_mul_f32_e32 v248, v57, v248\n" \
"v_mfma_f32_16x16x32_bf16 a[208:211], a[216:219], v[96:99], a[208:211]\n" \
"v_mul_f32_e32 v249, v57, v249\n" \
"v_mul_f32_e32 v250, v57, v250\n" \
"v_mul_f32_e32 v251, v57, v251\n" \
"v_cvt_pk_bf16_f32 v100, v248, v249\n" \
"v_cvt_pk_bf16_f32 v101, v250, v251\n" \
"buffer_load_dwordx4 v7, s[80:83], s92 offen offset:3072 lds\n" \
"s_add_u32 m0, s93, 0x2000\n" \
"v_mul_f32_e32 v252, v57, v252\n" \
"v_mul_f32_e32 v253, v57, v253\n" \
"v_mul_f32_e32 v254, v57, v254\n" \
"v_mul_f32_e32 v255, v57, v255\n" \
"v_cvt_pk_bf16_f32 v102, v252, v253\n" \
"v_cvt_pk_bf16_f32 v103, v254, v255\n" \
"v_cmp_lt_f32_e64 s[72:73], s21, v62\n" \
"buffer_load_dwordx4 v8, s[80:83], s92 offen offset:0 lds\n" \
"v_mfma_f32_16x16x32_bf16 a[208:211], a[216:219], v[100:103], a[208:211]\n" \
"s_nop 7\n" \
"v_accvgpr_read_b32 v63, a208\n" \
"v_cmp_u_f32_e32 vcc, v63, v63\n" \
"s_nop 0\n" \
"s_or_b64 vcc, s[72:73], vcc\n" \
"s_cbranch_scc0 L_out_%=\n" \
"s_branch L_end_%=\n" \
"L_skb_%=:\n" \
"s_sleep 1\n" \
"buffer_load_dword v57, v61, s[56:59], s96 offen sc1\n" \
"s_add_u32 s101, s101, 1\n" \
"s_waitcnt vmcnt(0)\n" \
"v_cmp_ne_u32_e32 vcc, s62, v57\n" \
"s_cmp_gt_u32 s101, 0x100000\n" \
"s_cbranch_scc1 L_rkb_%=\n" \
"s_nop 2\n" \
"s_cbranch_vccnz L_skb_%=\n" \
"s_branch L_rkb_%=\n" \
"L_sol_%=:\n" \
"s_sleep 1\n" \
"buffer_load_dword v26, v27, s[56:59], s96 offen sc1\n" \
"s_add_u32 s101, s101, 1\n" \
"s_waitcnt vmcnt(0)\n" \
"v_cmp_ne_u32_e32 vcc, s62, v26\n" \
"s_cmp_gt_u32 s101, 0x100000\n" \
"s_cbranch_scc1 L_rol_%=\n" \
"s_nop 2\n" \
"s_cbranch_vccnz L_sol_%=\n" \
"s_branch L_rol_%=\n" \
"L_sos_%=:\n" \
"s_sleep 1\n" \
"buffer_load_dword v26, v27, s[56:59], s96 offen sc1\n" \
"s_add_u32 s101, s101, 1\n" \
"s_waitcnt vmcnt(0)\n" \
"v_cmp_ne_u32_e32 vcc, s62, v26\n" \
"s_cmp_gt_u32 s101, 0x100000\n" \
"s_cbranch_scc1 L_ros_%=\n" \
"s_nop 2\n" \
"s_cbranch_vccnz L_sos_%=\n" \
"s_branch L_ros_%=\n" \
"L_sa_%=:\n" \
"s_sleep 1\n" \
"buffer_load_dword v26, v38, s[56:59], s64 offen sc1\n" \
"s_add_u32 s101, s101, 1\n" \
"s_waitcnt vmcnt(0)\n" \
"v_cmp_ne_u32_e32 vcc, s62, v26\n" \
"s_cmp_gt_u32 s101, 0x100000\n" \
"s_cbranch_scc1 L_ra_%=\n" \
"s_nop 2\n" \
"s_cbranch_vccnz L_sa_%=\n" \
"s_branch L_ra_%=\n" \
"L_sb_%=:\n" \
"s_sleep 1\n" \
"buffer_load_dword v47, v38, s[56:59], s71 offen sc1\n" \
"s_add_u32 s101, s101, 1\n" \
"s_waitcnt vmcnt(0)\n" \
"v_cmp_ne_u32_e32 vcc, s62, v47\n" \
"s_cmp_gt_u32 s101, 0x100000\n" \
"s_cbranch_scc1 L_rb_%=\n" \
"s_nop 2\n" \
"s_cbranch_vccnz L_sb_%=\n" \
"s_branch L_rb_%=\n" \
"L_sx0_%=:\n" \
"s_sleep 1\n" \
"buffer_load_dword v37, v40, s[56:59], s74 offen sc1\n" \
"s_add_u32 s101, s101, 1\n" \
"s_waitcnt vmcnt(0)\n" \
"v_cmp_eq_u32_e64 s[98:99], s62, v37\n" \
"s_cmp_gt_u32 s101, 0x100000\n" \
"s_cbranch_scc1 L_rx0_%=\n" \
"s_or_b32 s75, 0x7f, s100\n" \
"s_andn2_b32 s75, s75, s98\n" \
"s_cbranch_scc1 L_sx0_%=\n" \
"s_branch L_rx0_%=\n" \
"L_sx1_%=:\n" \
"s_sleep 1\n" \
"buffer_load_dword v37, v40, s[56:59], s74 offen sc1\n" \
"s_add_u32 s101, s101, 1\n" \
"s_waitcnt vmcnt(0)\n" \
"v_cmp_eq_u32_e64 s[98:99], s62, v37\n" \
"s_cmp_gt_u32 s101, 0x100000\n" \
"s_cbranch_scc1 L_rx1_%=\n" \
"s_andn2_b32 s75, 0x100, s98\n" \
"s_cbranch_scc1 L_sx1_%=\n" \
"s_branch L_rx1_%=\n" \
"L_sx2_%=:\n" \
"s_sleep 1\n" \
"buffer_load_dword v37, v40, s[56:59], s74 offen sc1\n" \
"s_add_u32 s101, s101, 1\n" \
"s_waitcnt vmcnt(0)\n" \
"v_cmp_eq_u32_e64 s[98:99], s62, v37\n" \
"s_cmp_gt_u32 s101, 0x100000\n" \
"s_cbranch_scc1 L_rx2_%=\n" \
"s_andn2_b32 s75, 0x200, s98\n" \
"s_cbranch_scc1 L_sx2_%=\n" \
"s_branch L_rx2_%=\n" \
"L_sx3_%=:\n" \
"s_sleep 1\n" \
"buffer_load_dword v37, v40, s[56:59], s74 offen sc1\n" \
"s_add_u32 s101, s101, 1\n" \
"s_waitcnt vmcnt(0)\n" \
"v_cmp_eq_u32_e64 s[98:99], s62, v37\n" \
"s_cmp_gt_u32 s101, 0x100000\n" \
"s_cbranch_scc1 L_rx3_%=\n" \
"s_andn2_b32 s75, 0x400, s98\n" \
"s_cbranch_scc1 L_sx3_%=\n" \
"s_branch L_rx3_%=\n" \
"L_sx4_%=:\n" \
"s_sleep 1\n" \
"buffer_load_dword v37, v40, s[56:59], s74 offen sc1\n" \
"s_add_u32 s101, s101, 1\n" \
"s_waitcnt vmcnt(0)\n" \
"v_cmp_eq_u32_e64 s[98:99], s62, v37\n" \
"s_cmp_gt_u32 s101, 0x100000\n" \
"s_cbranch_scc1 L_rx4_%=\n" \
"s_andn2_b32 s75, 0x800, s98\n" \
"s_cbranch_scc1 L_sx4_%=\n" \
"s_branch L_rx4_%=\n" \
"L_sx5_%=:\n" \
"s_sleep 1\n" \
"buffer_load_dword v37, v40, s[56:59], s74 offen sc1\n" \
"s_add_u32 s101, s101, 1\n" \
"s_waitcnt vmcnt(0)\n" \
"v_cmp_eq_u32_e64 s[98:99], s62, v37\n" \
"s_cmp_gt_u32 s101, 0x100000\n" \
"s_cbranch_scc1 L_rx5_%=\n" \
"s_andn2_b32 s75, 0x1000, s98\n" \
"s_cbranch_scc1 L_sx5_%=\n" \
"s_branch L_rx5_%=\n" \
"L_sx6_%=:\n" \
"s_sleep 1\n" \
"buffer_load_dword v37, v40, s[56:59], s74 offen sc1\n" \
"s_add_u32 s101, s101, 1\n" \
"s_waitcnt vmcnt(0)\n" \
"v_cmp_eq_u32_e64 s[98:99], s62, v37\n" \
"s_cmp_gt_u32 s101, 0x100000\n" \
"s_cbranch_scc1 L_rx6_%=\n" \
"s_andn2_b32 s75, 0x2000, s98\n" \
"s_cbranch_scc1 L_sx6_%=\n" \
"s_branch L_rx6_%=\n" \
"L_sx7_%=:\n" \
"s_sleep 1\n" \
"buffer_load_dword v37, v40, s[56:59], s74 offen sc1\n" \
"s_add_u32 s101, s101, 1\n" \
"s_waitcnt vmcnt(0)\n" \
"v_cmp_eq_u32_e64 s[98:99], s62, v37\n" \
"s_cmp_gt_u32 s101, 0x100000\n" \
"s_cbranch_scc1 L_rx7_%=\n" \
"s_andn2_b32 s75, 0x4000, s98\n" \
"s_cbranch_scc1 L_sx7_%=\n" \
"s_branch L_rx7_%=\n" \
"L_att_stub:\n" \
"s_mov_b32 s101, 0\n" \
"v_add_u32_e32 v32, 0x49000, v20\n" \
"v_readlane_b32 s96, v11, 36\n" \
"v_readlane_b32 s97, v11, 37\n" \
"buffer_load_dwordx4 a[96:99], v20, s[56:59], s60 offen offset:0 sc1\n" \
"buffer_load_dwordx4 a[112:115], v32, s[56:59], s60 offen offset:0 sc1\n" \
"buffer_load_dwordx4 a[100:103], v20, s[56:59], s60 offen offset:1024 sc1\n" \
"buffer_load_dwordx4 a[116:119], v32, s[56:59], s60 offen offset:1024 sc1\n" \
"buffer_load_dwordx4 a[104:107], v20, s[56:59], s60 offen offset:2048 sc1\n" \
"buffer_load_dwordx4 a[120:123], v32, s[56:59], s60 offen offset:2048 sc1\n" \
"buffer_load_dwordx4 a[108:111], v20, s[56:59], s60 offen offset:3072 sc1\n" \
"buffer_load_dwordx4 a[124:127], v32, s[56:59], s60 offen offset:3072 sc1\n" \
"buffer_load_dwordx4 a[220:223], v20, s[56:59], s96 offen offset:0 sc1\n" \
"buffer_load_dwordx4 a[228:231], v32, s[56:59], s96 offen offset:0 sc1\n" \
"buffer_load_dwordx4 a[236:239], v20, s[56:59], s97 offen offset:0 sc1\n" \
"buffer_load_dwordx4 a[244:247], v32, s[56:59], s97 offen offset:0 sc1\n" \
"buffer_load_dwordx4 a[224:227], v20, s[56:59], s96 offen offset:1024 sc1\n" \
"buffer_load_dwordx4 a[232:235], v32, s[56:59], s96 offen offset:1024 sc1\n" \
"buffer_load_dwordx4 a[240:243], v20, s[56:59], s97 offen offset:1024 sc1\n" \
"buffer_load_dwordx4 a[248:251], v32, s[56:59], s97 offen offset:1024 sc1\n" \
"buffer_load_dwordx4 v[64:67], v20, s[56:59], s61 offen offset:0 sc1\n" \
"buffer_load_dwordx4 v[68:71], v20, s[56:59], s61 offen offset:1024 sc1\n" \
"buffer_load_dwordx4 v[72:75], v20, s[56:59], s61 offen offset:2048 sc1\n" \
"buffer_load_dwordx4 v[76:79], v20, s[56:59], s61 offen offset:3072 sc1\n" \
"buffer_load_dwordx4 v[80:83], v32, s[56:59], s61 offen offset:0 sc1\n" \
"buffer_load_dwordx4 v[84:87], v32, s[56:59], s61 offen offset:1024 sc1\n" \
"buffer_load_dwordx4 v[96:99], v32, s[56:59], s61 offen offset:2048 sc1\n" \
"buffer_load_dwordx4 v[100:103], v32, s[56:59], s61 offen offset:3072 sc1\n" \
"v_not_b32_e32 v26, s62\n" \
"s_waitcnt vmcnt(0)\n" \
"s_branch L_out_%=\n" \
".fill (L_ab + 16384 - .) / 4, 4, 0xBF800000\n" \
".fill (L_ab + 16384 - .) / 4, 4, 0xBF800000\n" \
"L_ap4:\n" \
"s_setpc_b64 s[88:89]\n" \
"L_gsw:\n" \
"v_mfma_f32_16x16x32_bf16 v[104:107], a[128:131], a[16:19], v[36:39]\n" \
"s_add_u32 s68, s60, 0xfff6e000\n" \
"s_add_u32 s69, s60, 0xfffb7000\n" \
"s_add_u32 s70, s60, 0x0\n" \
"s_add_u32 s71, s60, 0x49000\n" \
"v_mfma_f32_16x16x32_bf16 v[104:107], a[132:135], a[20:23], v[104:107]\n" \
"s_xor_b32 m0, s65, 0x12000\n" \
"ds_read_b128 a[160:163], v49 offset:0\n" \
"v_mfma_f32_16x16x32_bf16 v[104:107], a[136:139], a[24:27], v[104:107]\n" \
"buffer_load_dwordx4 v50, s[8:11], s64 offen offset:0 sc1 lds\n" \
"v_mfma_f32_16x16x32_bf16 v[104:107], a[140:143], a[28:31], v[104:107]\n" \
"ds_read_b128 a[164:167], v49 offset:1024\n" \
"ds_read_b128 a[168:171], v49 offset:2048\n" \
"v_mfma_f32_16x16x32_bf16 v[108:111], a[156:159], a[28:31], v[36:39]\n" \
"ds_read_b128 a[172:175], v49 offset:3072\n" \
"ds_read_b128 a[176:179], v49 offset:4096\n" \
"v_mfma_f32_16x16x32_bf16 v[108:111], a[152:155], a[24:27], v[108:111]\n" \
"buffer_load_dwordx4 v50, s[8:11], s64 offen offset:1024 sc1 lds\n" \
"v_mfma_f32_16x16x32_bf16 v[108:111], a[148:151], a[20:23], v[108:111]\n" \
"s_mov_b32 s65, s13\n" \
"v_add_u32_e32 v63, s15, v20\n" \
"v_mfma_f32_16x16x32_bf16 v[108:111], a[144:147], a[16:19], v[108:111]\n" \
"s_add_u32 s67, s14, 40960\n" \
"v_add_u32_e32 v50, 0x6000, v63\n" \
"v_mfma_f32_16x16x32_bf16 v[116:119], a[144:147], a[32:35], v[40:43]\n" \
"s_xor_b32 m0, s65, 0x8000\n" \
"ds_read_b128 a[180:183], v49 offset:5120\n" \
"v_mfma_f32_16x16x32_bf16 v[116:119], a[148:151], a[36:39], v[116:119]\n" \
"ds_read_b128 a[184:187], v49 offset:6144\n" \
"v_and_b32_e32 v68, v12, v68\n" \
"v_mfma_f32_16x16x32_bf16 v[116:119], a[152:155], a[40:43], v[116:119]\n" \
"v_and_b32_e32 v69, v13, v69\n" \
"v_and_b32_e32 v70, v14, v70\n" \
"v_mfma_f32_16x16x32_bf16 v[116:119], a[156:159], a[44:47], v[116:119]\n" \
"buffer_load_dwordx4 v50, s[8:11], s67 offen offset:0 sc1 lds\n" \
"v_mfma_f32_16x16x32_bf16 v[112:115], a[140:143], a[44:47], v[40:43]\n" \
"v_and_b32_e32 v71, v15, v71\n" \
"ds_read_b128 a[188:191], v49 offset:7168\n" \
"v_mfma_f32_16x16x32_bf16 v[112:115], a[136:139], a[40:43], v[112:115]\n" \
"v_exp_f32_e32 v86, v86\n" \
"v_mfma_f32_16x16x32_bf16 v[112:115], a[132:135], a[36:39], v[112:115]\n" \
"v_exp_f32_e32 v87, v87\n" \
"v_mfma_f32_16x16x32_bf16 v[112:115], a[128:131], a[32:35], v[112:115]\n" \
"v_exp_f32_e32 v84, v84\n" \
"v_mfma_f32_16x16x32_bf16 v[120:123], a[128:131], a[48:51], v[44:47]\n" \
"buffer_load_dwordx4 v50, s[8:11], s67 offen offset:1024 sc1 lds\n" \
"v_mfma_f32_16x16x32_bf16 v[120:123], a[132:135], a[52:55], v[120:123]\n" \
"v_exp_f32_e32 v85, v85\n" \
"v_mfma_f32_16x16x32_bf16 v[120:123], a[136:139], a[56:59], v[120:123]\n" \
"v_cvt_pk_bf16_f32 v87, v86, v87\n" \
"v_cvt_pk_bf16_f32 v86, v84, v85\n" \
"v_mfma_f32_16x16x32_bf16 v[120:123], a[140:143], a[60:63], v[120:123]\n" \
"v_exp_f32_e32 v82, v82\n" \
"v_mfma_f32_16x16x32_bf16 v[124:127], a[156:159], a[60:63], v[44:47]\n" \
"v_exp_f32_e32 v83, v83\n" \
"v_mfma_f32_16x16x32_bf16 v[124:127], a[152:155], a[56:59], v[124:127]\n" \
"v_exp_f32_e32 v80, v80\n" \
"v_mfma_f32_16x16x32_bf16 v[124:127], a[148:151], a[52:55], v[124:127]\n" \
"buffer_load_dwordx4 v50, s[8:11], s67 offen offset:2048 sc1 lds\n" \
"v_mfma_f32_16x16x32_bf16 v[124:127], a[144:147], a[48:51], v[124:127]\n" \
"v_cvt_pk_bf16_f32 v85, v82, v83\n" \
"s_waitcnt lgkmcnt(1)\n" \
"v_mfma_f32_16x16x32_bf16 v[128:131], a[160:163], v[68:71], v[128:131]\n" \
"v_exp_f32_e32 v81, v81\n" \
"v_mfma_f32_16x16x32_bf16 v[160:163], a[160:163], v[72:75], v[160:163]\n" \
"v_cvt_pk_bf16_f32 v84, v80, v81\n" \
"s_mov_b32 s64, s14\n" \
"v_mfma_f32_16x16x32_bf16 v[132:135], a[164:167], v[68:71], v[132:135]\n" \
"v_exp_f32_e32 v88, v88\n" \
"v_mfma_f32_16x16x32_bf16 v[164:167], a[164:167], v[72:75], v[164:167]\n" \
"v_exp_f32_e32 v89, v89\n" \
"v_mfma_f32_16x16x32_bf16 v[136:139], a[168:171], v[68:71], v[136:139]\n" \
"buffer_load_dwordx4 v50, s[8:11], s67 offen offset:3072 sc1 lds\n" \
"v_mfma_f32_16x16x32_bf16 v[168:171], a[168:171], v[72:75], v[168:171]\n" \
"v_exp_f32_e32 v90, v90\n" \
"v_mfma_f32_16x16x32_bf16 v[140:143], a[172:175], v[68:71], v[140:143]\n" \
"v_exp_f32_e32 v91, v91\n" \
"v_mfma_f32_16x16x32_bf16 v[172:175], a[172:175], v[72:75], v[172:175]\n" \
"v_cvt_pk_bf16_f32 v88, v88, v89\n" \
"v_cvt_pk_bf16_f32 v89, v90, v91\n" \
"v_mfma_f32_16x16x32_bf16 v[144:147], a[176:179], v[68:71], v[144:147]\n" \
"v_exp_f32_e32 v92, v92\n" \
"v_mfma_f32_16x16x32_bf16 v[176:179], a[176:179], v[72:75], v[176:179]\n" \
"v_exp_f32_e32 v93, v93\n" \
"v_mfma_f32_16x16x32_bf16 v[148:151], a[180:183], v[68:71], v[148:151]\n" \
"v_exp_f32_e32 v94, v94\n" \
"v_mfma_f32_16x16x32_bf16 v[180:183], a[180:183], v[72:75], v[180:183]\n" \
"v_exp_f32_e32 v95, v95\n" \
"v_mfma_f32_16x16x32_bf16 v[152:155], a[184:187], v[68:71], v[152:155]\n" \
"v_cvt_pk_bf16_f32 v90, v92, v93\n" \
"v_cvt_pk_bf16_f32 v91, v94, v95\n" \
"v_mfma_f32_16x16x32_bf16 v[184:187], a[184:187], v[72:75], v[184:187]\n" \
"v_add_u32_e32 v51, 0x12000, v63\n" \
"s_waitcnt lgkmcnt(0)\n" \
"v_mfma_f32_16x16x32_bf16 v[156:159], a[188:191], v[68:71], v[156:159]\n" \
"s_xor_b32 m0, s65, 0xa000\n" \
"ds_read_b32 v58, v18 offset:512\n" \
"v_mfma_f32_16x16x32_bf16 v[188:191], a[188:191], v[72:75], v[188:191]\n" \
"buffer_load_dwordx4 v51, s[8:11], s64 offen offset:0 sc1 lds\n" \
"v_smfmac_f32_16x16x64_bf16 a[192:195], a[200:203], v[68:75], v21\n" \
"buffer_load_dwordx4 v51, s[8:11], s64 offen offset:1024 sc1 lds\n" \
"v_mfma_f32_16x16x32_bf16 v[220:223], a[188:191], v[84:87], v[220:223]\n" \
"ds_read_b32 v59, v18 offset:640\n" \
"ds_read_b32 v60, v18 offset:768\n" \
"v_mfma_f32_16x16x32_bf16 v[252:255], a[188:191], v[88:91], v[252:255]\n" \
"ds_read_b32 v61, v18 offset:896\n" \
"ds_read_b128 a[128:131], v48 offset:0\n" \
"v_mfma_f32_16x16x32_bf16 v[216:219], a[184:187], v[84:87], v[216:219]\n" \
"ds_read_b128 a[144:147], v48 offset:4096\n" \
"ds_read_b128 a[132:135], v48 offset:1024\n" \
"v_mfma_f32_16x16x32_bf16 v[248:251], a[184:187], v[88:91], v[248:251]\n" \
"ds_read_b128 a[148:151], v48 offset:5120\n" \
"ds_read_b128 a[136:139], v48 offset:2048\n" \
"v_mfma_f32_16x16x32_bf16 v[212:215], a[180:183], v[84:87], v[212:215]\n" \
"ds_read_b128 a[152:155], v48 offset:6144\n" \
"ds_read_b128 a[140:143], v48 offset:3072\n" \
"v_mfma_f32_16x16x32_bf16 v[244:247], a[180:183], v[88:91], v[244:247]\n" \
"ds_read_b128 a[156:159], v48 offset:7168\n" \
"v_add_u32_e32 v52, 0x14000, v63\n" \
"v_mfma_f32_16x16x32_bf16 v[208:211], a[176:179], v[84:87], v[208:211]\n" \
"buffer_load_dwordx4 v51, s[8:11], s64 offen offset:2048 sc1 lds\n" \
"v_mfma_f32_16x16x32_bf16 v[240:243], a[176:179], v[88:91], v[240:243]\n" \
"v_exp_f32_e32 v104, v104\n" \
"v_mfma_f32_16x16x32_bf16 v[204:207], a[172:175], v[84:87], v[204:207]\n" \
"v_exp_f32_e32 v105, v105\n" \
"v_mfma_f32_16x16x32_bf16 v[236:239], a[172:175], v[88:91], v[236:239]\n" \
"buffer_load_dwordx4 v51, s[8:11], s64 offen offset:3072 sc1 lds\n" \
"v_mfma_f32_16x16x32_bf16 v[200:203], a[168:171], v[84:87], v[200:203]\n" \
"v_exp_f32_e32 v106, v106\n" \
"v_mfma_f32_16x16x32_bf16 v[232:235], a[168:171], v[88:91], v[232:235]\n" \
"v_exp_f32_e32 v107, v107\n" \
"v_mfma_f32_16x16x32_bf16 v[196:199], a[164:167], v[84:87], v[196:199]\n" \
"v_cvt_pk_bf16_f32 v104, v104, v105\n" \
"v_cvt_pk_bf16_f32 v105, v106, v107\n" \
"v_mfma_f32_16x16x32_bf16 v[228:231], a[164:167], v[88:91], v[228:231]\n" \
"v_exp_f32_e32 v108, v108\n" \
"v_mfma_f32_16x16x32_bf16 v[192:195], a[160:163], v[84:87], v[192:195]\n" \
"v_exp_f32_e32 v109, v109\n" \
"v_mfma_f32_16x16x32_bf16 v[224:227], a[160:163], v[88:91], v[224:227]\n" \
"v_exp_f32_e32 v110, v110\n" \
"v_smfmac_f32_16x16x64_bf16 a[196:199], a[200:203], v[84:91], v21\n" \
"v_exp_f32_e32 v111, v111\n" \
"v_cvt_pk_bf16_f32 v106, v108, v109\n" \
"v_cvt_pk_bf16_f32 v107, v110, v111\n" \
"v_and_b32_e32 v104, v12, v104\n" \
"v_and_b32_e32 v105, v13, v105\n" \
"v_and_b32_e32 v106, v14, v106\n" \
"v_and_b32_e32 v107, v15, v107\n" \
"v_accvgpr_read_b32 v54, a192\n" \
"v_accvgpr_read_b32 v22, a193\n" \
"v_add_u32_e32 v53, 0x20000, v63\n" \
"v_exp_f32_e32 v118, v118\n" \
"v_exp_f32_e32 v119, v119\n" \
"v_exp_f32_e32 v116, v116\n" \
"v_exp_f32_e32 v117, v117\n" \
"v_cvt_pk_bf16_f32 v119, v118, v119\n" \
"v_exp_f32_e32 v114, v114\n" \
"v_exp_f32_e32 v115, v115\n" \
"v_exp_f32_e32 v112, v112\n" \
"v_exp_f32_e32 v113, v113\n" \
"v_cvt_pk_bf16_f32 v118, v116, v117\n" \
"v_cvt_pk_bf16_f32 v117, v114, v115\n" \
"v_cvt_pk_bf16_f32 v116, v112, v113\n" \
"v_readlane_b32 s93, v11, 19\n" \
"s_waitcnt vmcnt(10) lgkmcnt(0)\n" \
"s_barrier\n" \
"v_mfma_f32_16x16x32_bf16 v[80:83], a[128:131], a[32:35], v[40:43]\n" \
"ds_read_b128 a[160:163], v49 offset:8192\n" \
"ds_read_b128 a[164:167], v49 offset:9216\n" \
"v_mfma_f32_16x16x32_bf16 v[80:83], a[132:135], a[36:39], v[80:83]\n" \
"ds_read_b128 a[168:171], v49 offset:10240\n" \
"s_xor_b32 m0, s65, 0x18000\n" \
"v_mfma_f32_16x16x32_bf16 v[80:83], a[136:139], a[40:43], v[80:83]\n" \
"buffer_load_dwordx4 v52, s[8:11], s64 offen offset:0 sc1 lds\n" \
"v_mfma_f32_16x16x32_bf16 v[80:83], a[140:143], a[44:47], v[80:83]\n" \
"ds_read_b128 a[172:175], v49 offset:11264\n" \
"ds_read_b128 a[176:179], v49 offset:12288\n" \
"v_mfma_f32_16x16x32_bf16 v[84:87], a[156:159], a[44:47], v[40:43]\n" \
"ds_read_b128 a[180:183], v49 offset:13312\n" \
"ds_read_b128 a[184:187], v49 offset:14336\n" \
"v_mfma_f32_16x16x32_bf16 v[84:87], a[152:155], a[40:43], v[84:87]\n" \
"buffer_load_dwordx4 v52, s[8:11], s64 offen offset:1024 sc1 lds\n" \
"v_mfma_f32_16x16x32_bf16 v[84:87], a[148:151], a[36:39], v[84:87]\n" \
"ds_read_b128 a[188:191], v49 offset:15360\n" \
"v_readlane_b32 s80, v11, 11\n" \
"v_mfma_f32_16x16x32_bf16 v[84:87], a[144:147], a[32:35], v[84:87]\n" \
"buffer_load_dwordx4 v52, s[8:11], s64 offen offset:2048 sc1 lds\n" \
"v_mfma_f32_16x16x32_bf16 v[92:95], a[144:147], a[48:51], v[44:47]\n" \
"v_exp_f32_e32 v120, v120\n" \
"v_mfma_f32_16x16x32_bf16 v[92:95], a[148:151], a[52:55], v[92:95]\n" \
"v_exp_f32_e32 v121, v121\n" \
"v_mfma_f32_16x16x32_bf16 v[92:95], a[152:155], a[56:59], v[92:95]\n" \
"buffer_load_dwordx4 v52, s[8:11], s64 offen offset:3072 sc1 lds\n" \
"v_mfma_f32_16x16x32_bf16 v[92:95], a[156:159], a[60:63], v[92:95]\n" \
"s_mov_b32 m0, s65\n" \
"v_cvt_pk_bf16_f32 v120, v120, v121\n" \
"v_mfma_f32_16x16x32_bf16 v[88:91], a[140:143], a[60:63], v[44:47]\n" \
"v_exp_f32_e32 v122, v122\n" \
"v_mfma_f32_16x16x32_bf16 v[88:91], a[136:139], a[56:59], v[88:91]\n" \
"buffer_load_dwordx4 v53, s[8:11], s64 offen offset:0 sc1 lds\n" \
"v_mfma_f32_16x16x32_bf16 v[88:91], a[132:135], a[52:55], v[88:91]\n" \
"v_exp_f32_e32 v123, v123\n" \
"v_mfma_f32_16x16x32_bf16 v[88:91], a[128:131], a[48:51], v[88:91]\n" \
"v_exp_f32_e32 v124, v124\n" \
"v_mfma_f32_16x16x32_bf16 a[192:195], a[212:215], v[104:107], a[192:195]\n" \
"v_cvt_pk_bf16_f32 v121, v122, v123\n" \
"s_waitcnt lgkmcnt(5)\n" \
"v_mfma_f32_16x16x32_bf16 v[160:163], a[160:163], v[104:107], v[160:163]\n" \
"buffer_load_dwordx4 v53, s[8:11], s64 offen offset:1024 sc1 lds\n" \
"v_mfma_f32_16x16x32_bf16 v[164:167], a[164:167], v[104:107], v[164:167]\n" \
"v_exp_f32_e32 v125, v125\n" \
"v_mfma_f32_16x16x32_bf16 v[168:171], a[168:171], v[104:107], v[168:171]\n" \
"v_cvt_pk_bf16_f32 v122, v124, v125\n" \
"s_waitcnt lgkmcnt(1)\n" \
"v_mfma_f32_16x16x32_bf16 v[172:175], a[172:175], v[104:107], v[172:175]\n" \
"v_exp_f32_e32 v126, v126\n" \
"v_mfma_f32_16x16x32_bf16 v[176:179], a[176:179], v[104:107], v[176:179]\n" \
"v_exp_f32_e32 v127, v127\n" \
"v_mfma_f32_16x16x32_bf16 v[180:183], a[180:183], v[104:107], v[180:183]\n" \
"v_cvt_pk_bf16_f32 v123, v126, v127\n" \
"v_readlane_b32 s81, v11, 12\n" \
"v_mfma_f32_16x16x32_bf16 v[184:187], a[184:187], v[104:107], v[184:187]\n" \
"s_mov_b32 s82, 0x47fca0\n" \
"s_waitcnt lgkmcnt(0)\n" \
"v_mfma_f32_16x16x32_bf16 v[188:191], a[188:191], v[104:107], v[188:191]\n" \
"buffer_load_dwordx4 v53, s[8:11], s64 offen offset:2048 sc1 lds\n" \
"v_mfma_f32_16x16x32_bf16 v[220:223], a[188:191], v[116:119], v[220:223]\n" \
"buffer_load_dwordx4 v53, s[8:11], s64 offen offset:3072 sc1 lds\n" \
"v_mfma_f32_16x16x32_bf16 v[252:255], a[188:191], v[120:123], v[252:255]\n" \
"s_mov_b32 s83, 0x20000\n" \
"v_readlane_b32 s92, v11, 26\n" \
"v_mfma_f32_16x16x32_bf16 v[216:219], a[184:187], v[116:119], v[216:219]\n" \
"s_add_u32 m0, s93, 0x0\n" \
"ds_read_b128 a[128:131], v48 offset:8192\n" \
"v_mfma_f32_16x16x32_bf16 v[248:251], a[184:187], v[120:123], v[248:251]\n" \
"ds_read_b128 a[144:147], v48 offset:12288\n" \
"ds_read_b128 a[132:135], v48 offset:9216\n" \
"v_mfma_f32_16x16x32_bf16 v[212:215], a[180:183], v[116:119], v[212:215]\n" \
"buffer_load_dwordx4 v0, s[80:83], s92 offen offset:0 lds\n" \
"v_mfma_f32_16x16x32_bf16 v[244:247], a[180:183], v[120:123], v[244:247]\n" \
"buffer_load_dwordx4 v1, s[80:83], s92 offen offset:1024 lds\n" \
"v_mfma_f32_16x16x32_bf16 v[208:211], a[176:179], v[116:119], v[208:211]\n" \
"ds_read_b128 a[148:151], v48 offset:13312\n" \
"ds_read_b128 a[136:139], v48 offset:10240\n" \
"v_mfma_f32_16x16x32_bf16 v[240:243], a[176:179], v[120:123], v[240:243]\n" \
"ds_read_b128 a[152:155], v48 offset:14336\n" \
"ds_read_b128 a[140:143], v48 offset:11264\n" \
"v_mfma_f32_16x16x32_bf16 v[204:207], a[172:175], v[116:119], v[204:207]\n" \
"buffer_load_dwordx4 v2, s[80:83], s92 offen offset:2048 lds\n" \
"v_mfma_f32_16x16x32_bf16 v[236:239], a[172:175], v[120:123], v[236:239]\n" \
"ds_read_b128 a[156:159], v48 offset:15360\n" \
"s_xor_b32 s65, s65, 0x8000\n" \
"v_mfma_f32_16x16x32_bf16 v[200:203], a[168:171], v[116:119], v[200:203]\n" \
"s_add_u32 s64, s64, 0x10000\n" \
"v_add_u32_e32 v49, 0x10000, v48\n" \
"v_mfma_f32_16x16x32_bf16 v[232:235], a[168:171], v[120:123], v[232:235]\n" \
"buffer_load_dwordx4 v3, s[80:83], s92 offen offset:3072 lds\n" \
"v_mfma_f32_16x16x32_bf16 v[196:199], a[164:167], v[116:119], v[196:199]\n" \
"v_exp_f32_e32 v86, v86\n" \
"v_mfma_f32_16x16x32_bf16 v[228:231], a[164:167], v[120:123], v[228:231]\n" \
"v_exp_f32_e32 v87, v87\n" \
"v_mfma_f32_16x16x32_bf16 v[192:195], a[160:163], v[116:119], v[192:195]\n" \
"v_exp_f32_e32 v84, v84\n" \
"v_mfma_f32_16x16x32_bf16 v[224:227], a[160:163], v[120:123], v[224:227]\n" \
"v_exp_f32_e32 v85, v85\n" \
"v_smfmac_f32_16x16x64_bf16 a[196:199], a[200:203], v[116:123], v21\n" \
"v_exp_f32_e32 v88, v88\n" \
"v_exp_f32_e32 v89, v89\n" \
"v_cvt_pk_bf16_f32 v87, v86, v87\n" \
"v_exp_f32_e32 v82, v82\n" \
"v_exp_f32_e32 v83, v83\n" \
"v_exp_f32_e32 v80, v80\n" \
"v_exp_f32_e32 v81, v81\n" \
"v_exp_f32_e32 v90, v90\n" \
"v_exp_f32_e32 v91, v91\n" \
"v_cvt_pk_bf16_f32 v86, v84, v85\n" \
"v_cvt_pk_bf16_f32 v88, v88, v89\n" \
"v_exp_f32_e32 v92, v92\n" \
"v_exp_f32_e32 v93, v93\n" \
"v_exp_f32_e32 v94, v94\n" \
"v_exp_f32_e32 v95, v95\n" \
"v_cvt_pk_bf16_f32 v85, v82, v83\n" \
"v_cvt_pk_bf16_f32 v84, v80, v81\n" \
"v_cvt_pk_bf16_f32 v89, v90, v91\n" \
"v_and_b32_e32 v84, v12, v84\n" \
"v_and_b32_e32 v85, v13, v85\n" \
"v_and_b32_e32 v86, v14, v86\n" \
"v_and_b32_e32 v87, v15, v87\n" \
"v_cvt_pk_bf16_f32 v90, v92, v93\n" \
"v_cvt_pk_bf16_f32 v91, v94, v95\n" \
"v_add_u32_e32 v48, s16, v20\n" \
"v_add_f32_e32 v54, v54, v22\n" \
"v_accvgpr_read_b32 v55, a194\n" \
"v_accvgpr_read_b32 v23, a195\n" \
"v_mfma_f32_16x16x32_bf16 a[192:195], a[216:219], a[216:219], 0\n" \
"v_max_f32_e32 v62, v62, v54\n" \
"v_add_f32_e32 v55, v55, v23\n" \
"v_max_f32_e32 v62, v62, v55\n" \
"v_rcp_f32_e32 v54, v54\n" \
"v_rcp_f32_e32 v55, v55\n" \
"v_mul_f32_e32 v54, v54, v58\n" \
"v_mul_f32_e32 v55, v55, v59\n" \
"v_mul_f32_e32 v128, v54, v128\n" \
"v_mul_f32_e32 v129, v54, v129\n" \
"v_mul_f32_e32 v136, v54, v136\n" \
"v_mul_f32_e32 v137, v54, v137\n" \
"v_mul_f32_e32 v144, v54, v144\n" \
"v_mul_f32_e32 v145, v54, v145\n" \
"v_mul_f32_e32 v152, v54, v152\n" \
"v_mul_f32_e32 v153, v54, v153\n" \
"v_mul_f32_e32 v160, v55, v160\n" \
"v_mul_f32_e32 v161, v55, v161\n" \
"v_mul_f32_e32 v168, v55, v168\n" \
"v_mul_f32_e32 v169, v55, v169\n" \
"v_mul_f32_e32 v176, v55, v176\n" \
"v_mul_f32_e32 v177, v55, v177\n" \
"v_mul_f32_e32 v184, v55, v184\n" \
"v_mul_f32_e32 v185, v55, v185\n" \
"v_cvt_pk_bf16_f32 v128, v128, v129\n" \
"v_mul_f32_e32 v130, v54, v130\n" \
"v_mul_f32_e32 v131, v54, v131\n" \
"v_cvt_pk_bf16_f32 v136, v136, v137\n" \
"v_mul_f32_e32 v138, v54, v138\n" \
"v_mul_f32_e32 v139, v54, v139\n" \
"v_cvt_pk_bf16_f32 v144, v144, v145\n" \
"v_mul_f32_e32 v146, v54, v146\n" \
"v_mul_f32_e32 v147, v54, v147\n" \
"v_cvt_pk_bf16_f32 v152, v152, v153\n" \
"v_mul_f32_e32 v154, v54, v154\n" \
"v_mul_f32_e32 v155, v54, v155\n" \
"v_cvt_pk_bf16_f32 v160, v160, v161\n" \
"v_mul_f32_e32 v162, v55, v162\n" \
"v_mul_f32_e32 v163, v55, v163\n" \
"v_cvt_pk_bf16_f32 v168, v168, v169\n" \
"v_mul_f32_e32 v170, v55, v170\n" \
"v_mul_f32_e32 v171, v55, v171\n" \
"v_cvt_pk_bf16_f32 v176, v176, v177\n" \
"v_mul_f32_e32 v178, v55, v178\n" \
"v_mul_f32_e32 v179, v55, v179\n" \
"v_cvt_pk_bf16_f32 v184, v184, v185\n" \
"v_mul_f32_e32 v186, v55, v186\n" \
"v_mul_f32_e32 v187, v55, v187\n" \
"v_cvt_pk_bf16_f32 v129, v130, v131\n" \
"v_mul_f32_e32 v132, v54, v132\n" \
"v_mul_f32_e32 v133, v54, v133\n" \
"v_mul_f32_e32 v134, v54, v134\n" \
"v_mul_f32_e32 v135, v54, v135\n" \
"v_cvt_pk_bf16_f32 v137, v138, v139\n" \
"v_mul_f32_e32 v140, v54, v140\n" \
"s_waitcnt vmcnt(12) lgkmcnt(0)\n" \
"s_barrier\n" \
"v_mfma_f32_16x16x32_bf16 v[120:123], a[128:131], a[48:51], v[44:47]\n" \
"s_xor_b32 m0, s65, 0x12000\n" \
"ds_read_b128 a[188:191], v49 offset:7168\n" \
"v_mfma_f32_16x16x32_bf16 v[120:123], a[132:135], a[52:55], v[120:123]\n" \
"buffer_load_dwordx4 v50, s[8:11], s64 offen offset:0 sc1 lds\n" \
"v_mfma_f32_16x16x32_bf16 v[120:123], a[136:139], a[56:59], v[120:123]\n" \
"ds_read_b128 a[184:187], v49 offset:6144\n" \
"ds_read_b128 a[180:183], v49 offset:5120\n" \
"v_mfma_f32_16x16x32_bf16 v[120:123], a[140:143], a[60:63], v[120:123]\n" \
"buffer_load_dwordx4 v50, s[8:11], s64 offen offset:1024 sc1 lds\n" \
"v_mfma_f32_16x16x32_bf16 v[124:127], a[156:159], a[60:63], v[44:47]\n" \
"ds_read_b128 a[176:179], v49 offset:4096\n" \
"ds_read_b128 a[172:175], v49 offset:3072\n" \
"v_mfma_f32_16x16x32_bf16 v[124:127], a[152:155], a[56:59], v[124:127]\n" \
"ds_read_b128 a[168:171], v49 offset:2048\n" \
"ds_read_b128 a[164:167], v49 offset:1024\n" \
"v_mfma_f32_16x16x32_bf16 v[124:127], a[148:151], a[52:55], v[124:127]\n" \
"ds_read_b128 a[160:163], v49 offset:0\n" \
"ds_read_b128 a[0:3], v49 offset:8192\n" \
"v_mfma_f32_16x16x32_bf16 v[124:127], a[144:147], a[48:51], v[124:127]\n" \
"buffer_load_dwordx4 v50, s[8:11], s64 offen offset:2048 sc1 lds\n" \
"v_mfma_f32_16x16x32_bf16 v[32:35], a[220:223], a[64:67], 0\n" \
"ds_read_b128 a[4:7], v49 offset:9216\n" \
"ds_read_b128 a[8:11], v49 offset:10240\n" \
"v_mfma_f32_16x16x32_bf16 v[36:39], a[220:223], a[80:83], 0\n" \
"ds_read_b128 a[12:15], v49 offset:11264\n" \
"ds_read_b128 a[16:19], v49 offset:12288\n" \
"v_mfma_f32_16x16x32_bf16 v[40:43], a[220:223], a[96:99], 0\n" \
"ds_read_b128 a[20:23], v49 offset:13312\n" \
"ds_read_b128 a[24:27], v49 offset:14336\n" \
"v_mfma_f32_16x16x32_bf16 v[44:47], a[220:223], a[112:115], 0\n" \
"buffer_load_dwordx4 v50, s[8:11], s64 offen offset:3072 sc1 lds\n" \
"v_mfma_f32_16x16x32_bf16 v[32:35], a[224:227], a[68:71], v[32:35]\n" \
"s_xor_b32 m0, s65, 0xa000\n" \
"ds_read_b128 a[28:31], v49 offset:15360\n" \
"v_mfma_f32_16x16x32_bf16 v[36:39], a[224:227], a[84:87], v[36:39]\n" \
"v_exp_f32_e32 v120, v120\n" \
"v_mfma_f32_16x16x32_bf16 v[40:43], a[224:227], a[100:103], v[40:43]\n" \
"buffer_load_dwordx4 v51, s[8:11], s64 offen offset:0 sc1 lds\n" \
"v_mfma_f32_16x16x32_bf16 v[44:47], a[224:227], a[116:119], v[44:47]\n" \
"v_exp_f32_e32 v121, v121\n" \
"v_mfma_f32_16x16x32_bf16 v[32:35], a[228:231], a[72:75], v[32:35]\n" \
"v_exp_f32_e32 v122, v122\n" \
"v_mfma_f32_16x16x32_bf16 v[36:39], a[228:231], a[88:91], v[36:39]\n" \
"v_exp_f32_e32 v123, v123\n" \
"v_mfma_f32_16x16x32_bf16 v[40:43], a[228:231], a[104:107], v[40:43]\n" \
"buffer_load_dwordx4 v51, s[8:11], s64 offen offset:1024 sc1 lds\n" \
"v_mfma_f32_16x16x32_bf16 v[44:47], a[228:231], a[120:123], v[44:47]\n" \
"v_cvt_pk_bf16_f32 v120, v120, v121\n" \
"v_cvt_pk_bf16_f32 v121, v122, v123\n" \
"v_mfma_f32_16x16x32_bf16 v[32:35], a[232:235], a[76:79], v[32:35]\n" \
"v_exp_f32_e32 v124, v124\n" \
"v_mfma_f32_16x16x32_bf16 v[36:39], a[232:235], a[92:95], v[36:39]\n" \
"v_exp_f32_e32 v125, v125\n" \
"v_mfma_f32_16x16x32_bf16 v[40:43], a[232:235], a[108:111], v[40:43]\n" \
"buffer_load_dwordx4 v51, s[8:11], s64 offen offset:2048 sc1 lds\n" \
"v_mfma_f32_16x16x32_bf16 v[44:47], a[232:235], a[124:127], v[44:47]\n" \
"v_exp_f32_e32 v126, v126\n" \
"v_smfmac_f32_16x16x64_bf16 a[196:199], a[200:203], v[84:91], v21\n" \
"v_cvt_pk_bf16_f32 v122, v124, v125\n" \
"s_waitcnt lgkmcnt(1)\n" \
"v_mfma_f32_16x16x32_bf16 v[220:223], a[188:191], v[84:87], v[220:223]\n" \
"buffer_load_dwordx4 v51, s[8:11], s64 offen offset:3072 sc1 lds\n" \
"v_mfma_f32_16x16x32_bf16 v[252:255], a[188:191], v[88:91], v[252:255]\n" \
"s_add_u32 m0, s93, 0x1000\n" \
"v_and_b32_e32 v120, v12, v120\n" \
"v_mfma_f32_16x16x32_bf16 v[216:219], a[184:187], v[84:87], v[216:219]\n" \
"v_exp_f32_e32 v127, v127\n" \
"v_mfma_f32_16x16x32_bf16 v[248:251], a[184:187], v[88:91], v[248:251]\n" \
"v_cvt_pk_bf16_f32 v123, v126, v127\n" \
"v_and_b32_e32 v121, v13, v121\n" \
"v_mfma_f32_16x16x32_bf16 v[212:215], a[180:183], v[84:87], v[212:215]\n" \
"buffer_load_dwordx4 v4, s[80:83], s92 offen offset:0 lds\n" \
"v_mfma_f32_16x16x32_bf16 v[244:247], a[180:183], v[88:91], v[244:247]\n" \
"v_and_b32_e32 v122, v14, v122\n" \
"v_and_b32_e32 v123, v15, v123\n" \
"v_mfma_f32_16x16x32_bf16 v[208:211], a[176:179], v[84:87], v[208:211]\n" \
"v_accvgpr_read_b32 v56, a196\n" \
"v_accvgpr_read_b32 v24, a197\n" \
"v_mfma_f32_16x16x32_bf16 v[240:243], a[176:179], v[88:91], v[240:243]\n" \
"ds_read_b128 a[128:131], v48 offset:0\n" \
"ds_read_b128 a[144:147], v48 offset:4096\n" \
"v_mfma_f32_16x16x32_bf16 v[204:207], a[172:175], v[84:87], v[204:207]\n" \
"buffer_load_dwordx4 v5, s[80:83], s92 offen offset:1024 lds\n" \
"v_mfma_f32_16x16x32_bf16 v[236:239], a[172:175], v[88:91], v[236:239]\n" \
"ds_read_b128 a[132:135], v48 offset:1024\n" \
"ds_read_b128 a[148:151], v48 offset:5120\n" \
"v_mfma_f32_16x16x32_bf16 v[200:203], a[168:171], v[84:87], v[200:203]\n" \
"ds_read_b128 a[136:139], v48 offset:2048\n" \
"ds_read_b128 a[152:155], v48 offset:6144\n" \
"v_mfma_f32_16x16x32_bf16 v[232:235], a[168:171], v[88:91], v[232:235]\n" \
"buffer_load_dwordx4 v6, s[80:83], s92 offen offset:2048 lds\n" \
"v_mfma_f32_16x16x32_bf16 v[196:199], a[164:167], v[84:87], v[196:199]\n" \
"ds_read_b128 a[140:143], v48 offset:3072\n" \
"ds_read_b128 a[156:159], v48 offset:7168\n" \
"v_mfma_f32_16x16x32_bf16 v[228:231], a[164:167], v[88:91], v[228:231]\n" \
"v_mul_f32_e32 v141, v54, v141\n" \
"v_mul_f32_e32 v142, v54, v142\n" \
"v_mfma_f32_16x16x32_bf16 v[192:195], a[160:163], v[84:87], v[192:195]\n" \
"v_mul_f32_e32 v143, v54, v143\n" \
"v_cvt_pk_bf16_f32 v145, v146, v147\n" \
"v_mfma_f32_16x16x32_bf16 v[224:227], a[160:163], v[88:91], v[224:227]\n" \
"buffer_load_dwordx4 v7, s[80:83], s92 offen offset:3072 lds\n" \
"v_mfma_f32_16x16x32_bf16 a[196:199], a[212:215], v[120:123], a[196:199]\n" \
"v_mul_f32_e32 v148, v54, v148\n" \
"v_mul_f32_e32 v149, v54, v149\n" \
"v_mfma_f32_16x16x32_bf16 v[224:227], a[0:3], v[120:123], v[224:227]\n" \
"v_mul_f32_e32 v150, v54, v150\n" \
"v_mul_f32_e32 v151, v54, v151\n" \
"v_mfma_f32_16x16x32_bf16 v[228:231], a[4:7], v[120:123], v[228:231]\n" \
"v_cvt_pk_bf16_f32 v153, v154, v155\n" \
"v_mul_f32_e32 v156, v54, v156\n" \
"v_mfma_f32_16x16x32_bf16 v[232:235], a[8:11], v[120:123], v[232:235]\n" \
"v_mul_f32_e32 v157, v54, v157\n" \
"v_mul_f32_e32 v158, v54, v158\n" \
"v_mfma_f32_16x16x32_bf16 v[236:239], a[12:15], v[120:123], v[236:239]\n" \
"v_mul_f32_e32 v159, v54, v159\n" \
"v_cvt_pk_bf16_f32 v161, v162, v163\n" \
"v_mfma_f32_16x16x32_bf16 v[240:243], a[16:19], v[120:123], v[240:243]\n" \
"v_mul_f32_e32 v164, v55, v164\n" \
"v_mul_f32_e32 v165, v55, v165\n" \
"v_mfma_f32_16x16x32_bf16 v[244:247], a[20:23], v[120:123], v[244:247]\n" \
"v_mul_f32_e32 v166, v55, v166\n" \
"v_mul_f32_e32 v167, v55, v167\n" \
"v_mfma_f32_16x16x32_bf16 v[248:251], a[24:27], v[120:123], v[248:251]\n" \
"v_cvt_pk_bf16_f32 v169, v170, v171\n" \
"s_waitcnt lgkmcnt(4)\n" \
"v_mfma_f32_16x16x32_bf16 v[252:255], a[28:31], v[120:123], v[252:255]\n" \
"v_mul_f32_e32 v172, v55, v172\n" \
"v_mul_f32_e32 v173, v55, v173\n" \
"v_mul_f32_e32 v174, v55, v174\n" \
"v_mul_f32_e32 v175, v55, v175\n" \
"v_cvt_pk_bf16_f32 v177, v178, v179\n" \
"v_mul_f32_e32 v180, v55, v180\n" \
"v_mul_f32_e32 v181, v55, v181\n" \
"v_mul_f32_e32 v182, v55, v182\n" \
"v_mul_f32_e32 v183, v55, v183\n" \
"v_cvt_pk_bf16_f32 v185, v186, v187\n" \
"v_mul_f32_e32 v188, v55, v188\n" \
"v_mul_f32_e32 v189, v55, v189\n" \
"v_mul_f32_e32 v190, v55, v190\n" \
"v_mul_f32_e32 v191, v55, v191\n" \
"v_cvt_pk_bf16_f32 v130, v132, v133\n" \
"v_cvt_pk_bf16_f32 v131, v134, v135\n" \
"buffer_store_dwordx4 v[128:131], v20, s[56:59], s68 offen offset:0 sc1\n" \
"v_cvt_pk_bf16_f32 v138, v140, v141\n" \
"v_cvt_pk_bf16_f32 v139, v142, v143\n" \
"buffer_store_dwordx4 v[136:139], v20, s[56:59], s68 offen offset:1024 sc1\n" \
"v_cvt_pk_bf16_f32 v146, v148, v149\n" \
"v_cvt_pk_bf16_f32 v147, v150, v151\n" \
"buffer_store_dwordx4 v[144:147], v20, s[56:59], s68 offen offset:2048 sc1\n" \
"v_cvt_pk_bf16_f32 v154, v156, v157\n" \
"v_cvt_pk_bf16_f32 v155, v158, v159\n" \
"buffer_store_dwordx4 v[152:155], v20, s[56:59], s68 offen offset:3072 sc1\n" \
"v_cvt_pk_bf16_f32 v162, v164, v165\n" \
"v_cvt_pk_bf16_f32 v163, v166, v167\n" \
"buffer_store_dwordx4 v[160:163], v20, s[56:59], s69 offen offset:0 sc1\n" \
"v_cvt_pk_bf16_f32 v170, v172, v173\n" \
"v_cvt_pk_bf16_f32 v171, v174, v175\n" \
"buffer_store_dwordx4 v[168:171], v20, s[56:59], s69 offen offset:1024 sc1\n" \
"v_cvt_pk_bf16_f32 v178, v180, v181\n" \
"v_cvt_pk_bf16_f32 v179, v182, v183\n" \
"buffer_store_dwordx4 v[176:179], v20, s[56:59], s69 offen offset:2048 sc1\n" \
"v_cvt_pk_bf16_f32 v186, v188, v189\n" \
"v_cvt_pk_bf16_f32 v187, v190, v191\n" \
"buffer_store_dwordx4 v[184:187], v20, s[56:59], s69 offen offset:3072 sc1\n" \
"s_add_u32 m0, s93, 0x2000\n" \
"v_mfma_f32_16x16x32_bf16 a[208:211], a[216:219], v[128:131], a[208:211]\n" \
"buffer_load_dwordx4 v8, s[80:83], s92 offen offset:0 lds\n" \
"v_mfma_f32_16x16x32_bf16 a[208:211], a[216:219], v[136:139], a[208:211]\n" \
"v_mfma_f32_32x32x16_bf16 v[128:143], a[216:219], a[216:219], 0\n" \
"v_mfma_f32_16x16x32_bf16 a[208:211], a[216:219], v[144:147], a[208:211]\n" \
"v_mfma_f32_16x16x32_bf16 a[208:211], a[216:219], v[152:155], a[208:211]\n" \
"v_mfma_f32_32x32x16_bf16 v[144:159], a[216:219], a[216:219], 0\n" \
"v_add_f32_e32 v56, v56, v24\n" \
"v_mfma_f32_16x16x32_bf16 a[208:211], a[216:219], v[160:163], a[208:211]\n" \
"v_max_f32_e32 v62, v62, v56\n" \
"v_accvgpr_read_b32 v57, a198\n" \
"s_waitcnt vmcnt(21) lgkmcnt(0)\n" \
"s_barrier\n" \
"v_mfma_f32_16x16x32_bf16 v[64:67], a[128:131], a[64:67], v[32:35]\n" \
"s_xor_b32 m0, s65, 0x18000\n" \
"v_accvgpr_read_b32 v25, a199\n" \
"v_mfma_f32_16x16x32_bf16 a[196:199], a[216:219], a[216:219], 0\n" \
"buffer_load_dwordx4 v52, s[8:11], s64 offen offset:0 sc1 lds\n" \
"v_mfma_f32_16x16x32_bf16 v[64:67], a[132:135], a[68:71], v[64:67]\n" \
"v_rcp_f32_e32 v56, v56\n" \
"v_mfma_f32_16x16x32_bf16 v[64:67], a[136:139], a[72:75], v[64:67]\n" \
"v_mfma_f32_16x16x32_bf16 a[208:211], a[216:219], v[168:171], a[208:211]\n" \
"v_mfma_f32_32x32x16_bf16 v[160:175], a[216:219], a[216:219], 0\n" \
"v_mul_f32_e32 v56, v56, v60\n" \
"v_mfma_f32_16x16x32_bf16 a[208:211], a[216:219], v[176:179], a[208:211]\n" \
"v_mul_f32_e32 v192, v56, v192\n" \
"v_mul_f32_e32 v193, v56, v193\n" \
"v_mfma_f32_16x16x32_bf16 v[64:67], a[140:143], a[76:79], v[64:67]\n" \
"buffer_load_dwordx4 v52, s[8:11], s64 offen offset:1024 sc1 lds\n" \
"v_mfma_f32_16x16x32_bf16 v[68:71], a[156:159], a[76:79], v[32:35]\n" \
"v_cvt_pk_bf16_f32 v192, v192, v193\n" \
"v_mul_f32_e32 v194, v56, v194\n" \
"v_mfma_f32_16x16x32_bf16 v[68:71], a[152:155], a[72:75], v[68:71]\n" \
"v_mul_f32_e32 v195, v56, v195\n" \
"v_cvt_pk_bf16_f32 v193, v194, v195\n" \
"v_mfma_f32_16x16x32_bf16 v[68:71], a[148:151], a[68:71], v[68:71]\n" \
"v_mfma_f32_16x16x32_bf16 a[208:211], a[216:219], v[184:187], a[208:211]\n" \
"v_mfma_f32_32x32x16_bf16 v[176:191], a[216:219], a[216:219], 0\n" \
"v_mul_f32_e32 v196, v56, v196\n" \
"v_mul_f32_e32 v197, v56, v197\n" \
"v_mul_f32_e32 v198, v56, v198\n" \
"v_mul_f32_e32 v199, v56, v199\n" \
"v_mul_f32_e32 v200, v56, v200\n" \
"v_mul_f32_e32 v201, v56, v201\n" \
"v_mfma_f32_16x16x32_bf16 v[68:71], a[144:147], a[64:67], v[68:71]\n" \
"buffer_load_dwordx4 v52, s[8:11], s64 offen offset:2048 sc1 lds\n" \
"v_mfma_f32_16x16x32_bf16 v[76:79], a[144:147], a[80:83], v[36:39]\n" \
"v_cvt_pk_bf16_f32 v194, v196, v197\n" \
"v_cvt_pk_bf16_f32 v195, v198, v199\n" \
"buffer_store_dwordx4 v[192:195], v20, s[56:59], s70 offen offset:0 sc1\n" \
"v_mfma_f32_16x16x32_bf16 v[76:79], a[148:151], a[84:87], v[76:79]\n" \
"v_cvt_pk_bf16_f32 v200, v200, v201\n" \
"v_mul_f32_e32 v202, v56, v202\n" \
"v_mfma_f32_16x16x32_bf16 v[76:79], a[152:155], a[88:91], v[76:79]\n" \
"v_mul_f32_e32 v203, v56, v203\n" \
"v_add_f32_e32 v57, v57, v25\n" \
"v_mfma_f32_16x16x32_bf16 v[76:79], a[156:159], a[92:95], v[76:79]\n" \
"buffer_load_dwordx4 v52, s[8:11], s64 offen offset:3072 sc1 lds\n" \
"v_mfma_f32_16x16x32_bf16 v[72:75], a[140:143], a[92:95], v[36:39]\n" \
"s_mov_b32 m0, s65\n" \
"v_cvt_pk_bf16_f32 v201, v202, v203\n" \
"v_mfma_f32_16x16x32_bf16 v[72:75], a[136:139], a[88:91], v[72:75]\n" \
"v_mfma_f32_16x16x32_bf16 a[208:211], a[216:219], v[192:195], a[208:211]\n" \
"v_mul_f32_e32 v204, v56, v204\n" \
"v_mul_f32_e32 v205, v56, v205\n" \
"v_mfma_f32_16x16x32_bf16 v[72:75], a[132:135], a[84:87], v[72:75]\n" \
"v_mul_f32_e32 v206, v56, v206\n" \
"v_mul_f32_e32 v207, v56, v207\n" \
"v_mfma_f32_16x16x32_bf16 v[72:75], a[128:131], a[80:83], v[72:75]\n" \
"buffer_load_dwordx4 v53, s[8:11], s64 offen offset:0 sc1 lds\n" \
"v_mfma_f32_16x16x32_bf16 v[80:83], a[128:131], a[96:99], v[40:43]\n" \
"v_mul_f32_e32 v208, v56, v208\n" \
"v_mul_f32_e32 v209, v56, v209\n" \
"v_mfma_f32_16x16x32_bf16 v[80:83], a[132:135], a[100:103], v[80:83]\n" \
"v_max_f32_e32 v62, v62, v57\n" \
"v_cvt_pk_bf16_f32 v202, v204, v205\n" \
"v_mfma_f32_16x16x32_bf16 v[80:83], a[136:139], a[104:107], v[80:83]\n" \
"buffer_load_dwordx4 v53, s[8:11], s64 offen offset:1024 sc1 lds\n" \
"v_mfma_f32_16x16x32_bf16 v[80:83], a[140:143], a[108:111], v[80:83]\n" \
"v_cvt_pk_bf16_f32 v203, v206, v207\n" \
"v_cvt_pk_bf16_f32 v208, v208, v209\n" \
"buffer_store_dwordx4 v[200:203], v20, s[56:59], s70 offen offset:1024 sc1\n" \
"v_mfma_f32_16x16x32_bf16 v[84:87], a[156:159], a[108:111], v[40:43]\n" \
"v_mul_f32_e32 v210, v56, v210\n" \
"v_mul_f32_e32 v211, v56, v211\n" \
"v_mfma_f32_16x16x32_bf16 v[84:87], a[152:155], a[104:107], v[84:87]\n" \
"v_rcp_f32_e32 v57, v57\n" \
"v_mfma_f32_16x16x32_bf16 v[84:87], a[148:151], a[100:103], v[84:87]\n" \
"buffer_load_dwordx4 v53, s[8:11], s64 offen offset:2048 sc1 lds\n" \
"v_mfma_f32_16x16x32_bf16 v[84:87], a[144:147], a[96:99], v[84:87]\n" \
"v_mfma_f32_16x16x32_bf16 a[208:211], a[216:219], v[200:203], a[208:211]\n" \
"v_mfma_f32_32x32x16_bf16 v[192:207], a[216:219], a[216:219], 0\n" \
"v_cvt_pk_bf16_f32 v209, v210, v211\n" \
"v_mul_f32_e32 v212, v56, v212\n" \
".fill (L_ab + 20480 - .) / 4, 4, 0xBF800000\n" \
"L_ap5:\n" \
"s_cbranch_execz L_ap0\n" \
"v_mul_f32_e32 v213, v56, v213\n" \
"v_mul_f32_e32 v214, v56, v214\n" \
"v_mul_f32_e32 v215, v56, v215\n" \
"v_mul_f32_e32 v216, v56, v216\n" \
"v_mfma_f32_16x16x32_bf16 v[92:95], a[144:147], a[112:115], v[44:47]\n" \
"ds_read_b128 a[144:147], v48 offset:12288\n" \
"v_mul_f32_e32 v217, v56, v217\n" \
"v_mfma_f32_16x16x32_bf16 v[92:95], a[148:151], a[116:119], v[92:95]\n" \
"ds_read_b128 a[148:151], v48 offset:13312\n" \
"v_cvt_pk_bf16_f32 v210, v212, v213\n" \
"v_mfma_f32_16x16x32_bf16 v[92:95], a[152:155], a[120:123], v[92:95]\n" \
"ds_read_b128 a[152:155], v48 offset:14336\n" \
"v_cvt_pk_bf16_f32 v211, v214, v215\n" \
"buffer_store_dwordx4 v[208:211], v20, s[56:59], s70 offen offset:2048 sc1\n" \
"v_mfma_f32_16x16x32_bf16 v[92:95], a[156:159], a[124:127], v[92:95]\n" \
"ds_read_b128 a[156:159], v48 offset:15360\n" \
"v_cvt_pk_bf16_f32 v216, v216, v217\n" \
"v_mfma_f32_16x16x32_bf16 v[88:91], a[140:143], a[124:127], v[44:47]\n" \
"ds_read_b128 a[140:143], v48 offset:11264\n" \
"v_mul_f32_e32 v218, v56, v218\n" \
"v_mfma_f32_16x16x32_bf16 v[88:91], a[136:139], a[120:123], v[88:91]\n" \
"ds_read_b128 a[136:139], v48 offset:10240\n" \
"v_mul_f32_e32 v219, v56, v219\n" \
"v_mfma_f32_16x16x32_bf16 v[88:91], a[132:135], a[116:119], v[88:91]\n" \
"ds_read_b128 a[132:135], v48 offset:9216\n" \
"v_mul_f32_e32 v57, v57, v61\n" \
"v_mfma_f32_16x16x32_bf16 v[88:91], a[128:131], a[112:115], v[88:91]\n" \
"ds_read_b128 a[128:131], v48 offset:8192\n" \
"v_mfma_f32_16x16x32_bf16 a[208:211], a[216:219], v[208:211], a[208:211]\n" \
"v_cvt_pk_bf16_f32 v217, v218, v219\n" \
"v_mul_f32_e32 v220, v56, v220\n" \
"v_mul_f32_e32 v221, v56, v221\n" \
"v_mul_f32_e32 v222, v56, v222\n" \
"v_mul_f32_e32 v223, v56, v223\n" \
"v_mul_f32_e32 v224, v57, v224\n" \
"v_mul_f32_e32 v225, v57, v225\n" \
"v_cvt_pk_bf16_f32 v218, v220, v221\n" \
"v_cvt_pk_bf16_f32 v219, v222, v223\n" \
"buffer_store_dwordx4 v[216:219], v20, s[56:59], s70 offen offset:3072 sc1\n" \
"v_cvt_pk_bf16_f32 v224, v224, v225\n" \
"v_mul_f32_e32 v226, v57, v226\n" \
"v_mul_f32_e32 v227, v57, v227\n" \
"v_mfma_f32_16x16x32_bf16 a[208:211], a[216:219], v[216:219], a[208:211]\n" \
"v_mfma_f32_32x32x16_bf16 v[208:223], a[216:219], a[216:219], 0\n" \
"v_cvt_pk_bf16_f32 v225, v226, v227\n" \
"v_mul_f32_e32 v228, v57, v228\n" \
"v_mul_f32_e32 v229, v57, v229\n" \
"v_mul_f32_e32 v230, v57, v230\n" \
"v_mul_f32_e32 v231, v57, v231\n" \
"v_mul_f32_e32 v232, v57, v232\n" \
"v_mul_f32_e32 v233, v57, v233\n" \
"v_cvt_pk_bf16_f32 v226, v228, v229\n" \
"v_cvt_pk_bf16_f32 v227, v230, v231\n" \
"buffer_store_dwordx4 v[224:227], v20, s[56:59], s71 offen offset:0 sc1\n" \
"v_cvt_pk_bf16_f32 v232, v232, v233\n" \
"v_mul_f32_e32 v234, v57, v234\n" \
"v_mul_f32_e32 v235, v57, v235\n" \
"v_mfma_f32_16x16x32_bf16 a[208:211], a[216:219], v[224:227], a[208:211]\n" \
"v_cvt_pk_bf16_f32 v233, v234, v235\n" \
"v_mul_f32_e32 v236, v57, v236\n" \
"v_mul_f32_e32 v237, v57, v237\n" \
"v_mul_f32_e32 v238, v57, v238\n" \
"v_mul_f32_e32 v239, v57, v239\n" \
"v_mul_f32_e32 v240, v57, v240\n" \
"v_mul_f32_e32 v241, v57, v241\n" \
"v_cvt_pk_bf16_f32 v234, v236, v237\n" \
"v_cvt_pk_bf16_f32 v235, v238, v239\n" \
"buffer_store_dwordx4 v[232:235], v20, s[56:59], s71 offen offset:1024 sc1\n" \
"v_cvt_pk_bf16_f32 v240, v240, v241\n" \
"v_mul_f32_e32 v242, v57, v242\n" \
"v_mul_f32_e32 v243, v57, v243\n" \
"v_mfma_f32_16x16x32_bf16 a[208:211], a[216:219], v[232:235], a[208:211]\n" \
"v_mfma_f32_32x32x16_bf16 v[224:239], a[216:219], a[216:219], 0\n" \
"v_cvt_pk_bf16_f32 v241, v242, v243\n" \
"v_mul_f32_e32 v244, v57, v244\n" \
"v_mul_f32_e32 v245, v57, v245\n" \
"v_mul_f32_e32 v246, v57, v246\n" \
"v_mul_f32_e32 v247, v57, v247\n" \
"v_mul_f32_e32 v248, v57, v248\n" \
"v_mul_f32_e32 v249, v57, v249\n" \
"v_cvt_pk_bf16_f32 v242, v244, v245\n" \
"v_cvt_pk_bf16_f32 v243, v246, v247\n" \
"buffer_store_dwordx4 v[240:243], v20, s[56:59], s71 offen offset:2048 sc1\n" \
"v_cvt_pk_bf16_f32 v248, v248, v249\n" \
"v_mul_f32_e32 v250, v57, v250\n" \
"v_mul_f32_e32 v251, v57, v251\n" \
"v_mfma_f32_16x16x32_bf16 a[208:211], a[216:219], v[240:243], a[208:211]\n" \
"v_cvt_pk_bf16_f32 v249, v250, v251\n" \
"v_mul_f32_e32 v252, v57, v252\n" \
"v_mul_f32_e32 v253, v57, v253\n" \
"v_mul_f32_e32 v254, v57, v254\n" \
"v_mul_f32_e32 v255, v57, v255\n" \
"v_cvt_pk_bf16_f32 v250, v252, v253\n" \
"v_cvt_pk_bf16_f32 v251, v254, v255\n" \
"buffer_store_dwordx4 v[248:251], v20, s[56:59], s71 offen offset:3072 sc1\n" \
"buffer_load_dwordx4 v53, s[8:11], s64 offen offset:3072 sc1 lds\n" \
"s_waitcnt vmcnt(16) lgkmcnt(0)\n" \
"s_barrier\n" \
"v_mfma_f32_16x16x32_bf16 a[208:211], a[216:219], v[248:251], a[208:211]\n" \
"v_mfma_f32_32x32x16_bf16 v[240:255], a[216:219], a[216:219], 0\n" \
"v_exp_f32_e32 v70, v70\n" \
"v_exp_f32_e32 v71, v71\n" \
"v_exp_f32_e32 v72, v72\n" \
"v_exp_f32_e32 v73, v73\n" \
"s_add_u32 s64, s64, 0x10000\n" \
"v_cmp_lt_f32_e64 s[72:73], s21, v62\n" \
"v_readlane_b32 s95, v11, 3\n" \
"v_accvgpr_read_b32 v63, a208\n" \
"v_cmp_u_f32_e32 vcc, v63, v63\n" \
"v_exp_f32_e32 v68, v68\n" \
"v_exp_f32_e32 v69, v69\n" \
"v_exp_f32_e32 v74, v74\n" \
"v_exp_f32_e32 v75, v75\n" \
"s_or_b64 vcc, s[72:73], vcc\n" \
"s_cselect_b32 s95, 0x40000000, s95\n" \
"s_cselect_b32 s94, 0x7f800000, 0\n" \
"v_cvt_pk_bf16_f32 v71, v70, v71\n" \
"v_exp_f32_e32 v66, v66\n" \
"v_exp_f32_e32 v67, v67\n" \
"v_exp_f32_e32 v64, v64\n" \
"v_exp_f32_e32 v65, v65\n" \
"v_cvt_pk_bf16_f32 v72, v72, v73\n" \
"v_exp_f32_e32 v76, v76\n" \
"v_exp_f32_e32 v77, v77\n" \
"v_exp_f32_e32 v78, v78\n" \
"v_exp_f32_e32 v79, v79\n" \
"v_cvt_pk_bf16_f32 v70, v68, v69\n" \
"v_cvt_pk_bf16_f32 v73, v74, v75\n" \
"v_add_u32_e32 v49, 0x10000, v48\n" \
"s_xor_b32 s65, s65, 0x8000\n" \
"v_cvt_pk_bf16_f32 v69, v66, v67\n" \
"v_cvt_pk_bf16_f32 v68, v64, v65\n" \
"v_cvt_pk_bf16_f32 v74, v76, v77\n" \
"v_cvt_pk_bf16_f32 v75, v78, v79\n" \
"v_xor_b32_e32 v48, 0x8000, v48\n" \
"s_sub_u32 s66, s19, 2\n" \
"s_branch L_2_%=\n" \
"L_f1_%=:\n" \
"ds_read_b128 a[160:163], v49 offset:0\n" \
"ds_read_b128 a[164:167], v49 offset:1024\n" \
"ds_read_b128 a[168:171], v49 offset:2048\n" \
"v_mfma_f32_16x16x32_bf16 v[104:107], a[128:131], a[80:83], v[36:39]\n" \
"v_cvt_pk_bf16_f32 v192, v192, v193\n" \
"v_cvt_pk_bf16_f32 v224, v224, v225\n" \
"v_cvt_pk_bf16_f32 v193, v194, v195\n" \
"v_cvt_pk_bf16_f32 v225, v226, v227\n" \
"ds_read_b128 a[172:175], v49 offset:3072\n" \
"v_cvt_pk_bf16_f32 v194, v196, v197\n" \
"v_cvt_pk_bf16_f32 v195, v198, v199\n" \
"v_cvt_pk_bf16_f32 v226, v228, v229\n" \
"v_cvt_pk_bf16_f32 v227, v230, v231\n" \
"v_mfma_f32_16x16x32_bf16 v[104:107], a[132:135], a[84:87], v[104:107]\n" \
"v_lshl_add_u32 v50, s15, 2, v20\n" \
"v_cvt_pk_bf16_f32 v196, v200, v201\n" \
"v_cvt_pk_bf16_f32 v197, v202, v203\n" \
"v_cvt_pk_bf16_f32 v198, v204, v205\n" \
"v_cvt_pk_bf16_f32 v199, v206, v207\n" \
"v_cvt_pk_bf16_f32 v228, v232, v233\n" \
"v_cvt_pk_bf16_f32 v229, v234, v235\n" \
"v_cvt_pk_bf16_f32 v230, v236, v237\n" \
"v_cvt_pk_bf16_f32 v231, v238, v239\n" \
"v_add_u32_e32 v50, 0x1a000, v50\n" \
"v_mov_b32_e32 v61, s62\n" \
"v_cvt_pk_bf16_f32 v200, v208, v209\n" \
"v_cvt_pk_bf16_f32 v201, v210, v211\n" \
"v_cvt_pk_bf16_f32 v202, v212, v213\n" \
"v_cvt_pk_bf16_f32 v203, v214, v215\n" \
"v_cvt_pk_bf16_f32 v204, v216, v217\n" \
"v_cvt_pk_bf16_f32 v205, v218, v219\n" \
"v_cvt_pk_bf16_f32 v206, v220, v221\n" \
"v_cvt_pk_bf16_f32 v207, v222, v223\n" \
"v_cvt_pk_bf16_f32 v232, v240, v241\n" \
"v_cvt_pk_bf16_f32 v233, v242, v243\n" \
"v_cvt_pk_bf16_f32 v234, v244, v245\n" \
"v_cvt_pk_bf16_f32 v235, v246, v247\n" \
"v_cvt_pk_bf16_f32 v236, v248, v249\n" \
"v_cvt_pk_bf16_f32 v237, v250, v251\n" \
"v_cvt_pk_bf16_f32 v238, v252, v253\n" \
"v_cvt_pk_bf16_f32 v239, v254, v255\n" \
"ds_write_b128 v50, v[192:195] offset:0\n" \
"v_mfma_f32_16x16x32_bf16 v[104:107], a[136:139], a[88:91], v[104:107]\n" \
"ds_read_b128 a[176:179], v49 offset:4096\n" \
"v_add_u32_e32 v56, 0x49000, v20\n" \
"v_mfma_f32_16x16x32_bf16 v[104:107], a[140:143], a[92:95], v[104:107]\n" \
"ds_write_b128 v50, v[196:199] offset:1024\n" \
"v_mfma_f32_16x16x32_bf16 v[108:111], a[156:159], a[92:95], v[36:39]\n" \
"ds_read_b128 a[180:183], v49 offset:5120\n" \
"ds_read_b128 a[184:187], v49 offset:6144\n" \
"v_mfma_f32_16x16x32_bf16 v[108:111], a[152:155], a[88:91], v[108:111]\n" \
"ds_write_b128 v50, v[200:203] offset:2048\n" \
"v_mfma_f32_16x16x32_bf16 v[108:111], a[148:151], a[84:87], v[108:111]\n" \
"v_and_b32_e32 v68, v12, v68\n" \
"v_and_b32_e32 v69, v13, v69\n" \
"v_mfma_f32_16x16x32_bf16 v[108:111], a[144:147], a[80:83], v[108:111]\n" \
"ds_write_b128 v50, v[204:207] offset:3072\n" \
"v_and_b32_e32 v70, v14, v70\n" \
"v_and_b32_e32 v71, v15, v71\n" \
"ds_read_b128 a[188:191], v49 offset:7168\n" \
"ds_read_b128 a[0:3], v20 offset:8192\n" \
"s_waitcnt lgkmcnt(9)\n" \
"v_mfma_f32_16x16x32_bf16 v[128:131], a[160:163], v[68:71], v[128:131]\n" \
"ds_write_b128 v50, v[224:227] offset:4096\n" \
"v_mfma_f32_16x16x32_bf16 v[160:163], a[160:163], v[72:75], v[160:163]\n" \
"ds_read_b128 a[4:7], v20 offset:9216\n" \
"ds_read_b128 a[8:11], v20 offset:10240\n" \
"v_mfma_f32_16x16x32_bf16 v[132:135], a[164:167], v[68:71], v[132:135]\n" \
"ds_write_b128 v50, v[228:231] offset:5120\n" \
"v_mfma_f32_16x16x32_bf16 v[164:167], a[164:167], v[72:75], v[164:167]\n" \
"ds_read_b128 a[12:15], v20 offset:11264\n" \
"ds_read_b128 a[28:31], v20 offset:15360\n" \
"v_mfma_f32_16x16x32_bf16 v[136:139], a[168:171], v[68:71], v[136:139]\n" \
"ds_write_b128 v50, v[232:235] offset:6144\n" \
"v_mfma_f32_16x16x32_bf16 v[168:171], a[168:171], v[72:75], v[168:171]\n" \
"ds_read_b128 a[24:27], v20 offset:14336\n" \
"ds_read_b128 a[20:23], v20 offset:13312\n" \
"v_mfma_f32_16x16x32_bf16 v[140:143], a[172:175], v[68:71], v[140:143]\n" \
"ds_write_b128 v50, v[236:239] offset:7168\n" \
"v_mfma_f32_16x16x32_bf16 v[172:175], a[172:175], v[72:75], v[172:175]\n" \
"ds_read_b128 a[16:19], v20 offset:12288\n" \
"s_waitcnt lgkmcnt(15)\n" \
"v_mfma_f32_16x16x32_bf16 v[144:147], a[176:179], v[68:71], v[144:147]\n" \
"v_xor_b32_e32 v48, 0x4000, v49\n" \
"ds_read_b32 v58, v18 offset:256\n" \
"v_mfma_f32_16x16x32_bf16 v[176:179], a[176:179], v[72:75], v[176:179]\n" \
"ds_read_b32 v59, v18 offset:384\n" \
"ds_read_b128 a[32:35], v48 offset:8192\n" \
"v_mfma_f32_16x16x32_bf16 v[148:151], a[180:183], v[68:71], v[148:151]\n" \
"ds_read_b128 a[36:39], v48 offset:9216\n" \
"ds_read_b128 a[40:43], v48 offset:10240\n" \
"v_mfma_f32_16x16x32_bf16 v[180:183], a[180:183], v[72:75], v[180:183]\n" \
"ds_read_b128 a[44:47], v48 offset:11264\n" \
"ds_read_b128 a[48:51], v48 offset:12288\n" \
"v_mfma_f32_16x16x32_bf16 v[152:155], a[184:187], v[68:71], v[152:155]\n" \
"ds_read_b128 a[52:55], v48 offset:13312\n" \
"ds_read_b128 a[56:59], v48 offset:14336\n" \
"v_mfma_f32_16x16x32_bf16 v[184:187], a[184:187], v[72:75], v[184:187]\n" \
"ds_read_b128 a[60:63], v48 offset:15360\n" \
"s_waitcnt lgkmcnt(15)\n" \
"v_mfma_f32_16x16x32_bf16 v[156:159], a[188:191], v[68:71], v[156:159]\n" \
"ds_read_b128 a[128:131], v49 offset:8192\n" \
"ds_read_b128 a[132:135], v49 offset:9216\n" \
"v_mfma_f32_16x16x32_bf16 v[188:191], a[188:191], v[72:75], v[188:191]\n" \
"ds_read_b128 a[136:139], v49 offset:10240\n" \
"ds_read_b128 a[140:143], v49 offset:11264\n" \
"v_smfmac_f32_16x16x64_bf16 a[192:195], a[200:203], v[68:75], v21\n" \
"ds_read_b128 a[144:147], v49 offset:12288\n" \
"ds_read_b128 a[148:151], v49 offset:13312\n" \
"v_mfma_f32_16x16x32_bf16 v[112:115], a[0:3], a[64:67], v[32:35]\n" \
"ds_read_b128 a[152:155], v49 offset:14336\n" \
"ds_read_b128 a[156:159], v49 offset:15360\n" \
"v_mfma_f32_16x16x32_bf16 v[112:115], a[4:7], a[68:71], v[112:115]\n" \
"v_accvgpr_read_b32 v40, a196\n" \
"v_accvgpr_read_b32 v24, a197\n" \
"v_mfma_f32_16x16x32_bf16 v[112:115], a[8:11], a[72:75], v[112:115]\n" \
"v_accvgpr_read_b32 v41, a198\n" \
"v_accvgpr_read_b32 v25, a199\n" \
"v_mfma_f32_16x16x32_bf16 v[112:115], a[12:15], a[76:79], v[112:115]\n" \
"v_add_f32_e32 v40, v40, v24\n" \
"v_add_f32_e32 v41, v41, v25\n" \
"v_mfma_f32_16x16x32_bf16 v[116:119], a[28:31], a[76:79], v[32:35]\n" \
"v_add_f32_e32 v40, s94, v40\n" \
"s_waitcnt lgkmcnt(15)\n" \
"v_mfma_f32_16x16x32_bf16 v[116:119], a[24:27], a[72:75], v[116:119]\n" \
"v_add_f32_e32 v41, s94, v41\n" \
"ds_write_b64 v19, v[40:41]\n" \
"v_mfma_f32_16x16x32_bf16 v[116:119], a[20:23], a[68:71], v[116:119]\n" \
"v_add_u32_e32 v52, s15, v20\n" \
"v_add_u32_e32 v51, 0x10000, v52\n" \
"v_mfma_f32_16x16x32_bf16 v[116:119], a[16:19], a[64:67], v[116:119]\n" \
"v_exp_f32_e32 v104, v104\n" \
"v_mfma_f32_16x16x32_bf16 v[124:127], a[16:19], a[80:83], v[36:39]\n" \
"v_exp_f32_e32 v105, v105\n" \
"v_mfma_f32_16x16x32_bf16 v[124:127], a[20:23], a[84:87], v[124:127]\n" \
"v_exp_f32_e32 v106, v106\n" \
"v_mfma_f32_16x16x32_bf16 v[124:127], a[24:27], a[88:91], v[124:127]\n" \
"v_exp_f32_e32 v107, v107\n" \
"v_mfma_f32_16x16x32_bf16 v[124:127], a[28:31], a[92:95], v[124:127]\n" \
"v_cvt_pk_bf16_f32 v104, v104, v105\n" \
"v_cvt_pk_bf16_f32 v105, v106, v107\n" \
"v_mfma_f32_16x16x32_bf16 v[120:123], a[12:15], a[92:95], v[36:39]\n" \
"v_exp_f32_e32 v108, v108\n" \
"v_mfma_f32_16x16x32_bf16 v[120:123], a[8:11], a[88:91], v[120:123]\n" \
"v_exp_f32_e32 v109, v109\n" \
"v_mfma_f32_16x16x32_bf16 v[120:123], a[4:7], a[84:87], v[120:123]\n" \
"v_exp_f32_e32 v110, v110\n" \
"v_mfma_f32_16x16x32_bf16 v[120:123], a[0:3], a[80:83], v[120:123]\n" \
"v_exp_f32_e32 v111, v111\n" \
"s_waitcnt lgkmcnt(0)\n" \
"s_barrier\n" \
"ds_read_b128 v[208:211], v52 offset:0\n" \
"ds_read_b128 v[212:215], v52 offset:1024\n" \
"ds_read_b128 v[216:219], v52 offset:2048\n" \
"ds_read_b128 v[220:223], v52 offset:3072\n" \
"ds_read_b128 v[240:243], v51 offset:32768\n" \
"ds_read_b128 v[244:247], v51 offset:33792\n" \
"ds_read_b128 v[248:251], v51 offset:34816\n" \
"ds_read_b128 v[252:255], v51 offset:35840\n" \
"v_cvt_pk_bf16_f32 v106, v108, v109\n" \
"v_cvt_pk_bf16_f32 v107, v110, v111\n" \
"v_and_b32_e32 v104, v12, v104\n" \
"v_and_b32_e32 v105, v13, v105\n" \
"v_and_b32_e32 v106, v14, v106\n" \
"s_waitcnt vmcnt(0)\n" \
"s_mov_b64 exec, 1\n" \
"buffer_store_dword v61, off, s[56:59], s95 offset:0 sc1\n" \
"s_mov_b64 exec, -1\n" \
"v_and_b32_e32 v107, v15, v107\n" \
"v_xor_b32_e32 v63, 0x400, v19\n" \
"ds_read_b64 v[42:43], v63\n" \
"v_mfma_f32_16x16x32_bf16 a[192:195], a[212:215], v[104:107], a[192:195]\n" \
"v_mov_b32_e32 v194, v28\n" \
"v_mov_b32_e32 v195, v29\n" \
"v_mfma_f32_16x16x32_bf16 v[160:163], a[128:131], v[104:107], v[160:163]\n" \
"v_mov_b32_e32 v196, 0\n" \
"v_mov_b32_e32 v197, 0\n" \
"v_mfma_f32_16x16x32_bf16 v[164:167], a[132:135], v[104:107], v[164:167]\n" \
"v_mov_b32_e32 v192, 0\n" \
"v_mov_b32_e32 v193, 0\n" \
"v_mfma_f32_16x16x32_bf16 v[168:171], a[136:139], v[104:107], v[168:171]\n" \
"v_readlane_b32 s93, v11, 22\n" \
"v_readlane_b32 s92, v11, 27\n" \
"v_mfma_f32_16x16x32_bf16 v[172:175], a[140:143], v[104:107], v[172:175]\n" \
"buffer_load_dwordx4 a[96:99], v20, s[56:59], s60 offen offset:0 sc1\n" \
"v_mfma_f32_16x16x32_bf16 v[176:179], a[144:147], v[104:107], v[176:179]\n" \
"v_exp_f32_e32 v118, v118\n" \
"v_mfma_f32_16x16x32_bf16 v[180:183], a[148:151], v[104:107], v[180:183]\n" \
"v_exp_f32_e32 v119, v119\n" \
"v_mfma_f32_16x16x32_bf16 v[184:187], a[152:155], v[104:107], v[184:187]\n" \
"s_add_u32 m0, s93, 0x0\n" \
"v_cvt_pk_bf16_f32 v119, v118, v119\n" \
"v_mfma_f32_16x16x32_bf16 v[188:191], a[156:159], v[104:107], v[188:191]\n" \
"v_exp_f32_e32 v120, v120\n" \
"s_waitcnt lgkmcnt(1)\n" \
"s_barrier\n" \
"buffer_load_dwordx4 a[112:115], v56, s[56:59], s60 offen offset:0 sc1\n" \
"v_mfma_f32_16x16x32_bf16 v[128:131], v[194:197], v[208:211], v[128:131]\n" \
"v_exp_f32_e32 v121, v121\n" \
"v_mfma_f32_16x16x32_bf16 v[132:135], v[192:195], v[208:211], v[132:135]\n" \
"v_exp_f32_e32 v116, v116\n" \
"v_mfma_f32_16x16x32_bf16 v[136:139], v[194:197], v[212:215], v[136:139]\n" \
"v_exp_f32_e32 v117, v117\n" \
"v_mfma_f32_16x16x32_bf16 v[140:143], v[192:195], v[212:215], v[140:143]\n" \
"v_exp_f32_e32 v122, v122\n" \
"v_mfma_f32_16x16x32_bf16 v[144:147], v[194:197], v[216:219], v[144:147]\n" \
"v_exp_f32_e32 v123, v123\n" \
"v_mfma_f32_16x16x32_bf16 v[148:151], v[192:195], v[216:219], v[148:151]\n" \
"buffer_load_dwordx4 a[100:103], v20, s[56:59], s60 offen offset:1024 sc1\n" \
"v_mfma_f32_16x16x32_bf16 v[152:155], v[194:197], v[220:223], v[152:155]\n" \
"v_exp_f32_e32 v114, v114\n" \
"v_mfma_f32_16x16x32_bf16 v[156:159], v[192:195], v[220:223], v[156:159]\n" \
"v_exp_f32_e32 v115, v115\n" \
"v_mfma_f32_16x16x32_bf16 v[160:163], v[194:197], v[240:243], v[160:163]\n" \
"v_exp_f32_e32 v112, v112\n" \
"v_mfma_f32_16x16x32_bf16 v[164:167], v[192:195], v[240:243], v[164:167]\n" \
"v_exp_f32_e32 v113, v113\n" \
"v_mfma_f32_16x16x32_bf16 v[168:171], v[194:197], v[244:247], v[168:171]\n" \
"v_cvt_pk_bf16_f32 v120, v120, v121\n" \
"v_cvt_pk_bf16_f32 v118, v116, v117\n" \
"v_mfma_f32_16x16x32_bf16 v[172:175], v[192:195], v[244:247], v[172:175]\n" \
"buffer_load_dwordx4 a[116:119], v56, s[56:59], s60 offen offset:1024 sc1\n" \
"v_mfma_f32_16x16x32_bf16 v[176:179], v[194:197], v[248:251], v[176:179]\n" \
"v_exp_f32_e32 v124, v124\n" \
"v_mfma_f32_16x16x32_bf16 v[180:183], v[192:195], v[248:251], v[180:183]\n" \
"v_exp_f32_e32 v125, v125\n" \
"v_mfma_f32_16x16x32_bf16 v[184:187], v[194:197], v[252:255], v[184:187]\n" \
"v_exp_f32_e32 v126, v126\n" \
"v_mfma_f32_16x16x32_bf16 v[188:191], v[192:195], v[252:255], v[188:191]\n" \
"v_exp_f32_e32 v127, v127\n" \
"v_cvt_pk_bf16_f32 v121, v122, v123\n" \
"v_cvt_pk_bf16_f32 v117, v114, v115\n" \
"v_cvt_pk_bf16_f32 v116, v112, v113\n" \
"v_cvt_pk_bf16_f32 v122, v124, v125\n" \
"v_cvt_pk_bf16_f32 v123, v126, v127\n" \
"buffer_load_dwordx4 a[104:107], v20, s[56:59], s60 offen offset:2048 sc1\n" \
"s_nop 0\n" \
"v_smfmac_f32_16x16x64_bf16 a[192:195], a[200:203], v[116:123], v21\n" \
"v_mfma_f32_16x16x32_bf16 v[128:131], a[32:35], v[116:119], v[128:131]\n" \
"v_mfma_f32_16x16x32_bf16 v[160:163], a[32:35], v[120:123], v[160:163]\n" \
"v_mfma_f32_16x16x32_bf16 v[132:135], a[36:39], v[116:119], v[132:135]\n" \
"v_mfma_f32_16x16x32_bf16 v[164:167], a[36:39], v[120:123], v[164:167]\n" \
"v_mfma_f32_16x16x32_bf16 v[136:139], a[40:43], v[116:119], v[136:139]\n" \
"buffer_load_dwordx4 a[120:123], v56, s[56:59], s60 offen offset:2048 sc1\n" \
"v_mfma_f32_16x16x32_bf16 v[168:171], a[40:43], v[120:123], v[168:171]\n" \
"v_mfma_f32_16x16x32_bf16 v[140:143], a[44:47], v[116:119], v[140:143]\n" \
"v_accvgpr_read_b32 v54, a192\n" \
"v_accvgpr_read_b32 v22, a193\n" \
"v_mfma_f32_16x16x32_bf16 v[172:175], a[44:47], v[120:123], v[172:175]\n" \
"v_add_f32_e32 v54, v54, v22\n" \
"s_waitcnt lgkmcnt(0)\n" \
"v_add_f32_e32 v54, v54, v42\n" \
"v_mfma_f32_16x16x32_bf16 v[144:147], a[48:51], v[116:119], v[144:147]\n" \
"v_max_f32_e32 v62, v62, v54\n" \
"v_accvgpr_read_b32 v55, a194\n" \
"v_mfma_f32_16x16x32_bf16 v[176:179], a[48:51], v[120:123], v[176:179]\n" \
"v_rcp_f32_e32 v54, v54\n" \
"buffer_load_dwordx4 a[108:111], v20, s[56:59], s60 offen offset:3072 sc1\n" \
"v_mfma_f32_16x16x32_bf16 v[148:151], a[52:55], v[116:119], v[148:151]\n" \
"v_mul_f32_e32 v54, v54, v58\n" \
"v_mul_f32_e32 v128, v54, v128\n" \
"v_mfma_f32_16x16x32_bf16 v[180:183], a[52:55], v[120:123], v[180:183]\n" \
"v_mul_f32_e32 v129, v54, v129\n" \
"v_mul_f32_e32 v130, v54, v130\n" \
"v_mul_f32_e32 v131, v54, v131\n" \
"v_mul_f32_e32 v132, v54, v132\n" \
"v_mul_f32_e32 v133, v54, v133\n" \
"v_mul_f32_e32 v134, v54, v134\n" \
"v_mul_f32_e32 v135, v54, v135\n" \
"v_cvt_pk_bf16_f32 v64, v128, v129\n" \
"v_cvt_pk_bf16_f32 v65, v130, v131\n" \
"v_cvt_pk_bf16_f32 v66, v132, v133\n" \
"v_cvt_pk_bf16_f32 v67, v134, v135\n" \
"v_accvgpr_read_b32 v23, a195\n" \
"v_mfma_f32_16x16x32_bf16 v[152:155], a[56:59], v[116:119], v[152:155]\n" \
"v_mfma_f32_16x16x32_bf16 a[208:211], a[216:219], v[64:67], a[208:211]\n" \
"buffer_load_dwordx4 a[124:127], v56, s[56:59], s60 offen offset:3072 sc1\n" \
"v_mul_f32_e32 v136, v54, v136\n" \
"v_mul_f32_e32 v137, v54, v137\n" \
"v_mul_f32_e32 v138, v54, v138\n" \
"v_mul_f32_e32 v139, v54, v139\n" \
"v_mul_f32_e32 v140, v54, v140\n" \
"v_mul_f32_e32 v141, v54, v141\n" \
"v_mul_f32_e32 v142, v54, v142\n" \
"v_mul_f32_e32 v143, v54, v143\n" \
"v_add_f32_e32 v55, v55, v23\n" \
"v_cvt_pk_bf16_f32 v68, v136, v137\n" \
"v_cvt_pk_bf16_f32 v69, v138, v139\n" \
"v_cvt_pk_bf16_f32 v70, v140, v141\n" \
"v_cvt_pk_bf16_f32 v71, v142, v143\n" \
"v_add_f32_e32 v55, v55, v43\n" \
"v_mul_f32_e32 v144, v54, v144\n" \
"v_mfma_f32_16x16x32_bf16 a[208:211], a[216:219], v[68:71], a[208:211]\n" \
"v_mul_f32_e32 v145, v54, v145\n" \
"v_mul_f32_e32 v146, v54, v146\n" \
"v_mul_f32_e32 v147, v54, v147\n" \
"v_mul_f32_e32 v148, v54, v148\n" \
"buffer_load_dwordx4 v0, s[80:83], s92 offen offset:0 lds\n" \
"v_mul_f32_e32 v149, v54, v149\n" \
"v_mul_f32_e32 v150, v54, v150\n" \
"v_mul_f32_e32 v151, v54, v151\n" \
"v_max_f32_e32 v62, v62, v55\n" \
"v_cvt_pk_bf16_f32 v72, v144, v145\n" \
"v_cvt_pk_bf16_f32 v73, v146, v147\n" \
"v_cvt_pk_bf16_f32 v74, v148, v149\n" \
"v_cvt_pk_bf16_f32 v75, v150, v151\n" \
"v_rcp_f32_e32 v55, v55\n" \
"v_mfma_f32_16x16x32_bf16 v[184:187], a[56:59], v[120:123], v[184:187]\n" \
"v_mfma_f32_16x16x32_bf16 a[208:211], a[216:219], v[72:75], a[208:211]\n" \
"v_mul_f32_e32 v152, v54, v152\n" \
"v_mul_f32_e32 v153, v54, v153\n" \
"v_mul_f32_e32 v154, v54, v154\n" \
"v_mul_f32_e32 v155, v54, v155\n" \
"v_cvt_pk_bf16_f32 v76, v152, v153\n" \
"buffer_load_dwordx4 v1, s[80:83], s92 offen offset:1024 lds\n" \
"v_cvt_pk_bf16_f32 v77, v154, v155\n" \
"v_mul_f32_e32 v55, v55, v59\n" \
"v_mul_f32_e32 v160, v55, v160\n" \
"v_mul_f32_e32 v161, v55, v161\n" \
"v_mul_f32_e32 v162, v55, v162\n" \
"v_mul_f32_e32 v163, v55, v163\n" \
"v_mul_f32_e32 v164, v55, v164\n" \
"v_mul_f32_e32 v165, v55, v165\n" \
"v_mul_f32_e32 v166, v55, v166\n" \
"v_mul_f32_e32 v167, v55, v167\n" \
"v_cvt_pk_bf16_f32 v80, v160, v161\n" \
"v_cvt_pk_bf16_f32 v81, v162, v163\n" \
"v_cvt_pk_bf16_f32 v82, v164, v165\n" \
"v_cvt_pk_bf16_f32 v83, v166, v167\n" \
"v_mfma_f32_16x16x32_bf16 v[156:159], a[60:63], v[116:119], v[156:159]\n" \
"v_mul_f32_e32 v168, v55, v168\n" \
"v_mul_f32_e32 v169, v55, v169\n" \
"v_mul_f32_e32 v170, v55, v170\n" \
"v_mul_f32_e32 v171, v55, v171\n" \
"v_mul_f32_e32 v172, v55, v172\n" \
"buffer_load_dwordx4 v2, s[80:83], s92 offen offset:2048 lds\n" \
"v_mul_f32_e32 v173, v55, v173\n" \
"v_mul_f32_e32 v174, v55, v174\n" \
"v_mul_f32_e32 v156, v54, v156\n" \
"v_mul_f32_e32 v157, v54, v157\n" \
"v_mul_f32_e32 v158, v54, v158\n" \
"v_mul_f32_e32 v159, v54, v159\n" \
"v_cvt_pk_bf16_f32 v78, v156, v157\n" \
"v_cvt_pk_bf16_f32 v79, v158, v159\n" \
"v_mul_f32_e32 v175, v55, v175\n" \
"v_cvt_pk_bf16_f32 v84, v168, v169\n" \
"v_mfma_f32_16x16x32_bf16 a[208:211], a[216:219], v[76:79], a[208:211]\n" \
"v_mfma_f32_16x16x32_bf16 a[208:211], a[216:219], v[80:83], a[208:211]\n" \
"v_cvt_pk_bf16_f32 v85, v170, v171\n" \
"v_cvt_pk_bf16_f32 v86, v172, v173\n" \
"v_cvt_pk_bf16_f32 v87, v174, v175\n" \
"v_mul_f32_e32 v176, v55, v176\n" \
"v_mul_f32_e32 v177, v55, v177\n" \
"buffer_load_dwordx4 v3, s[80:83], s92 offen offset:3072 lds\n" \
"s_add_u32 m0, s93, 0x1000\n" \
"v_mfma_f32_16x16x32_bf16 a[208:211], a[216:219], v[84:87], a[208:211]\n" \
"v_mul_f32_e32 v178, v55, v178\n" \
"v_mul_f32_e32 v179, v55, v179\n" \
"v_mul_f32_e32 v180, v55, v180\n" \
"v_mul_f32_e32 v181, v55, v181\n" \
"v_mul_f32_e32 v182, v55, v182\n" \
"v_mul_f32_e32 v183, v55, v183\n" \
"v_cvt_pk_bf16_f32 v96, v176, v177\n" \
"v_cvt_pk_bf16_f32 v97, v178, v179\n" \
"v_cvt_pk_bf16_f32 v98, v180, v181\n" \
"v_cvt_pk_bf16_f32 v99, v182, v183\n" \
"v_mfma_f32_16x16x32_bf16 v[188:191], a[60:63], v[120:123], v[188:191]\n" \
"v_mul_f32_e32 v184, v55, v184\n" \
"v_mfma_f32_16x16x32_bf16 a[208:211], a[216:219], v[96:99], a[208:211]\n" \
"v_mul_f32_e32 v185, v55, v185\n" \
"v_mul_f32_e32 v186, v55, v186\n" \
"buffer_load_dwordx4 v4, s[80:83], s92 offen offset:0 lds\n" \
"v_mul_f32_e32 v187, v55, v187\n" \
"v_cvt_pk_bf16_f32 v100, v184, v185\n" \
"v_cvt_pk_bf16_f32 v101, v186, v187\n" \
"v_mul_f32_e32 v188, v55, v188\n" \
"v_mul_f32_e32 v189, v55, v189\n" \
"v_mul_f32_e32 v190, v55, v190\n" \
"v_mul_f32_e32 v191, v55, v191\n" \
"v_cvt_pk_bf16_f32 v102, v188, v189\n" \
"v_cvt_pk_bf16_f32 v103, v190, v191\n" \
"v_cmp_lt_f32_e64 s[72:73], s21, v62\n" \
"buffer_load_dwordx4 v5, s[80:83], s92 offen offset:1024 lds\n" \
"v_mfma_f32_16x16x32_bf16 a[208:211], a[216:219], v[100:103], a[208:211]\n" \
"buffer_load_dwordx4 v6, s[80:83], s92 offen offset:2048 lds\n" \
"buffer_load_dwordx4 v7, s[80:83], s92 offen offset:3072 lds\n" \
"s_add_u32 m0, s93, 0x2000\n" \
"s_nop 0\n" \
"buffer_load_dwordx4 v8, s[80:83], s92 offen offset:0 lds\n" \
"s_nop 2\n" \
"v_accvgpr_read_b32 v63, a208\n" \
"v_cmp_u_f32_e32 vcc, v63, v63\n" \
"s_nop 0\n" \
"s_or_b64 vcc, s[72:73], vcc\n" \
"s_cbranch_scc0 L_out_%=\n" \
"s_branch L_end_%=\n" \
".fill (L_ab + 24576 - .) / 4, 4, 0xBF800000\n" \
".fill (L_ab + 24576 - .) / 4, 4, 0xBF800000\n" \
"L_ap6:\n" \
"s_setpc_b64 s[88:89]\n" \
"L_gs7:\n" \
"v_mfma_f32_16x16x32_bf16 v[104:107], a[128:131], a[16:19], v[36:39]\n" \
"s_add_u32 s68, s60, 0xfff6e000\n" \
"s_add_u32 s69, s60, 0xfffb7000\n" \
"s_add_u32 s70, s60, 0x0\n" \
"s_add_u32 s71, s60, 0x49000\n" \
"v_mfma_f32_16x16x32_bf16 v[104:107], a[132:135], a[20:23], v[104:107]\n" \
"s_xor_b32 m0, s65, 0x12000\n" \
"ds_read_b128 a[160:163], v49 offset:0\n" \
"v_mfma_f32_16x16x32_bf16 v[104:107], a[136:139], a[24:27], v[104:107]\n" \
"buffer_load_dwordx4 v50, s[8:11], s64 offen offset:0 sc1 lds\n" \
"v_mfma_f32_16x16x32_bf16 v[104:107], a[140:143], a[28:31], v[104:107]\n" \
"ds_read_b128 a[164:167], v49 offset:1024\n" \
"ds_read_b128 a[168:171], v49 offset:2048\n" \
"v_mfma_f32_16x16x32_bf16 v[108:111], a[156:159], a[28:31], v[36:39]\n" \
"ds_read_b128 a[172:175], v49 offset:3072\n" \
"ds_read_b128 a[176:179], v49 offset:4096\n" \
"v_mfma_f32_16x16x32_bf16 v[108:111], a[152:155], a[24:27], v[108:111]\n" \
"s_mov_b32 s65, s13\n" \
"v_add_u32_e32 v63, s15, v20\n" \
"v_mfma_f32_16x16x32_bf16 v[108:111], a[148:151], a[20:23], v[108:111]\n" \
"buffer_load_dwordx4 v50, s[8:11], s64 offen offset:1024 sc1 lds\n" \
"v_mfma_f32_16x16x32_bf16 v[108:111], a[144:147], a[16:19], v[108:111]\n" \
"s_add_u32 s67, s14, 40960\n" \
"v_add_u32_e32 v50, 0x6000, v63\n" \
"v_mfma_f32_16x16x32_bf16 v[116:119], a[144:147], a[32:35], v[40:43]\n" \
"s_xor_b32 m0, s65, 0x8000\n" \
"ds_read_b128 a[180:183], v49 offset:5120\n" \
"v_mfma_f32_16x16x32_bf16 v[116:119], a[148:151], a[36:39], v[116:119]\n" \
"ds_read_b128 a[184:187], v49 offset:6144\n" \
"v_and_b32_e32 v68, v12, v68\n" \
"v_mfma_f32_16x16x32_bf16 v[116:119], a[152:155], a[40:43], v[116:119]\n" \
"v_and_b32_e32 v69, v13, v69\n" \
"v_and_b32_e32 v70, v14, v70\n" \
"v_mfma_f32_16x16x32_bf16 v[116:119], a[156:159], a[44:47], v[116:119]\n" \
"buffer_load_dwordx4 v50, s[8:11], s67 offen offset:0 sc1 lds\n" \
"v_mfma_f32_16x16x32_bf16 v[112:115], a[140:143], a[44:47], v[40:43]\n" \
"v_and_b32_e32 v71, v15, v71\n" \
"ds_read_b128 a[188:191], v49 offset:7168\n" \
"v_mfma_f32_16x16x32_bf16 v[112:115], a[136:139], a[40:43], v[112:115]\n" \
"v_exp_f32_e32 v86, v86\n" \
"v_mfma_f32_16x16x32_bf16 v[112:115], a[132:135], a[36:39], v[112:115]\n" \
"v_exp_f32_e32 v87, v87\n" \
"v_mfma_f32_16x16x32_bf16 v[112:115], a[128:131], a[32:35], v[112:115]\n" \
"v_exp_f32_e32 v84, v84\n" \
"v_mfma_f32_16x16x32_bf16 v[120:123], a[128:131], a[48:51], v[44:47]\n" \
"buffer_load_dwordx4 v50, s[8:11], s67 offen offset:1024 sc1 lds\n" \
"v_mfma_f32_16x16x32_bf16 v[120:123], a[132:135], a[52:55], v[120:123]\n" \
"v_exp_f32_e32 v85, v85\n" \
"v_mfma_f32_16x16x32_bf16 v[120:123], a[136:139], a[56:59], v[120:123]\n" \
"v_cvt_pk_bf16_f32 v87, v86, v87\n" \
"v_cvt_pk_bf16_f32 v86, v84, v85\n" \
"v_mfma_f32_16x16x32_bf16 v[120:123], a[140:143], a[60:63], v[120:123]\n" \
"v_exp_f32_e32 v82, v82\n" \
"v_mfma_f32_16x16x32_bf16 v[124:127], a[156:159], a[60:63], v[44:47]\n" \
"v_exp_f32_e32 v83, v83\n" \
"v_mfma_f32_16x16x32_bf16 v[124:127], a[152:155], a[56:59], v[124:127]\n" \
"v_exp_f32_e32 v80, v80\n" \
"v_mfma_f32_16x16x32_bf16 v[124:127], a[148:151], a[52:55], v[124:127]\n" \
"buffer_load_dwordx4 v50, s[8:11], s67 offen offset:2048 sc1 lds\n" \
"v_mfma_f32_16x16x32_bf16 v[124:127], a[144:147], a[48:51], v[124:127]\n" \
"v_cvt_pk_bf16_f32 v85, v82, v83\n" \
"s_waitcnt lgkmcnt(1)\n" \
"v_mfma_f32_16x16x32_bf16 v[128:131], a[160:163], v[68:71], v[128:131]\n" \
"v_exp_f32_e32 v81, v81\n" \
"v_mfma_f32_16x16x32_bf16 v[160:163], a[160:163], v[72:75], v[160:163]\n" \
"v_cvt_pk_bf16_f32 v84, v80, v81\n" \
"s_mov_b32 s64, s14\n" \
"v_mfma_f32_16x16x32_bf16 v[132:135], a[164:167], v[68:71], v[132:135]\n" \
"v_exp_f32_e32 v88, v88\n" \
"v_mfma_f32_16x16x32_bf16 v[164:167], a[164:167], v[72:75], v[164:167]\n" \
"v_exp_f32_e32 v89, v89\n" \
"v_mfma_f32_16x16x32_bf16 v[136:139], a[168:171], v[68:71], v[136:139]\n" \
"buffer_load_dwordx4 v50, s[8:11], s67 offen offset:3072 sc1 lds\n" \
"v_mfma_f32_16x16x32_bf16 v[168:171], a[168:171], v[72:75], v[168:171]\n" \
"v_exp_f32_e32 v90, v90\n" \
"v_mfma_f32_16x16x32_bf16 v[140:143], a[172:175], v[68:71], v[140:143]\n" \
"v_exp_f32_e32 v91, v91\n" \
"v_mfma_f32_16x16x32_bf16 v[172:175], a[172:175], v[72:75], v[172:175]\n" \
"v_cvt_pk_bf16_f32 v88, v88, v89\n" \
"v_cvt_pk_bf16_f32 v89, v90, v91\n" \
"v_mfma_f32_16x16x32_bf16 v[144:147], a[176:179], v[68:71], v[144:147]\n" \
"v_exp_f32_e32 v92, v92\n" \
"v_mfma_f32_16x16x32_bf16 v[176:179], a[176:179], v[72:75], v[176:179]\n" \
"v_exp_f32_e32 v93, v93\n" \
"v_mfma_f32_16x16x32_bf16 v[148:151], a[180:183], v[68:71], v[148:151]\n" \
"v_exp_f32_e32 v94, v94\n" \
"v_mfma_f32_16x16x32_bf16 v[180:183], a[180:183], v[72:75], v[180:183]\n" \
"v_exp_f32_e32 v95, v95\n" \
"v_mfma_f32_16x16x32_bf16 v[152:155], a[184:187], v[68:71], v[152:155]\n" \
"v_cvt_pk_bf16_f32 v90, v92, v93\n" \
"v_cvt_pk_bf16_f32 v91, v94, v95\n" \
"v_mfma_f32_16x16x32_bf16 v[184:187], a[184:187], v[72:75], v[184:187]\n" \
"v_add_u32_e32 v51, 0x12000, v63\n" \
"s_waitcnt lgkmcnt(0)\n" \
"v_mfma_f32_16x16x32_bf16 v[156:159], a[188:191], v[68:71], v[156:159]\n" \
"s_xor_b32 m0, s65, 0xa000\n" \
"ds_read_b32 v58, v18 offset:512\n" \
"v_mfma_f32_16x16x32_bf16 v[188:191], a[188:191], v[72:75], v[188:191]\n" \
"buffer_load_dwordx4 v51, s[8:11], s64 offen offset:0 sc1 lds\n" \
"v_smfmac_f32_16x16x64_bf16 a[192:195], a[200:203], v[68:75], v21\n" \
"ds_read_b32 v59, v18 offset:640\n" \
"ds_read_b32 v60, v18 offset:768\n" \
"v_mfma_f32_16x16x32_bf16 v[220:223], a[188:191], v[84:87], v[220:223]\n" \
"ds_read_b32 v61, v18 offset:896\n" \
"ds_read_b128 a[128:131], v48 offset:0\n" \
"v_mfma_f32_16x16x32_bf16 v[252:255], a[188:191], v[88:91], v[252:255]\n" \
"ds_read_b128 a[144:147], v48 offset:4096\n" \
"ds_read_b128 a[132:135], v48 offset:1024\n" \
"v_mfma_f32_16x16x32_bf16 v[216:219], a[184:187], v[84:87], v[216:219]\n" \
"buffer_load_dwordx4 v51, s[8:11], s64 offen offset:1024 sc1 lds\n" \
"v_mfma_f32_16x16x32_bf16 v[248:251], a[184:187], v[88:91], v[248:251]\n" \
"ds_read_b128 a[148:151], v48 offset:5120\n" \
"ds_read_b128 a[136:139], v48 offset:2048\n" \
"v_mfma_f32_16x16x32_bf16 v[212:215], a[180:183], v[84:87], v[212:215]\n" \
"ds_read_b128 a[152:155], v48 offset:6144\n" \
"ds_read_b128 a[140:143], v48 offset:3072\n" \
"v_mfma_f32_16x16x32_bf16 v[244:247], a[180:183], v[88:91], v[244:247]\n" \
"ds_read_b128 a[156:159], v48 offset:7168\n" \
"v_add_u32_e32 v52, 0x14000, v63\n" \
"v_mfma_f32_16x16x32_bf16 v[208:211], a[176:179], v[84:87], v[208:211]\n" \
"buffer_load_dwordx4 v51, s[8:11], s64 offen offset:2048 sc1 lds\n" \
"v_mfma_f32_16x16x32_bf16 v[240:243], a[176:179], v[88:91], v[240:243]\n" \
"v_exp_f32_e32 v104, v104\n" \
"v_mfma_f32_16x16x32_bf16 v[204:207], a[172:175], v[84:87], v[204:207]\n" \
"v_exp_f32_e32 v105, v105\n" \
"v_mfma_f32_16x16x32_bf16 v[236:239], a[172:175], v[88:91], v[236:239]\n" \
"v_exp_f32_e32 v106, v106\n" \
"v_mfma_f32_16x16x32_bf16 v[200:203], a[168:171], v[84:87], v[200:203]\n" \
"buffer_load_dwordx4 v51, s[8:11], s64 offen offset:3072 sc1 lds\n" \
"v_mfma_f32_16x16x32_bf16 v[232:235], a[168:171], v[88:91], v[232:235]\n" \
"v_exp_f32_e32 v107, v107\n" \
"v_mfma_f32_16x16x32_bf16 v[196:199], a[164:167], v[84:87], v[196:199]\n" \
"v_cvt_pk_bf16_f32 v104, v104, v105\n" \
"v_cvt_pk_bf16_f32 v105, v106, v107\n" \
"v_mfma_f32_16x16x32_bf16 v[228:231], a[164:167], v[88:91], v[228:231]\n" \
"v_exp_f32_e32 v108, v108\n" \
"v_mfma_f32_16x16x32_bf16 v[192:195], a[160:163], v[84:87], v[192:195]\n" \
"v_exp_f32_e32 v109, v109\n" \
"v_mfma_f32_16x16x32_bf16 v[224:227], a[160:163], v[88:91], v[224:227]\n" \
"v_exp_f32_e32 v110, v110\n" \
"v_smfmac_f32_16x16x64_bf16 a[196:199], a[200:203], v[84:91], v21\n" \
"v_exp_f32_e32 v111, v111\n" \
"v_cvt_pk_bf16_f32 v106, v108, v109\n" \
"v_cvt_pk_bf16_f32 v107, v110, v111\n" \
"v_and_b32_e32 v104, v12, v104\n" \
"v_and_b32_e32 v105, v13, v105\n" \
"v_and_b32_e32 v106, v14, v106\n" \
"v_and_b32_e32 v107, v15, v107\n" \
"v_accvgpr_read_b32 v54, a192\n" \
"v_accvgpr_read_b32 v22, a193\n" \
"v_readlane_b32 s93, v11, 19\n" \
"v_readlane_b32 s80, v11, 11\n" \
"v_readlane_b32 s81, v11, 12\n" \
"s_mov_b32 s82, 0x47fca0\n" \
"s_mov_b32 s83, 0x20000\n" \
"v_readlane_b32 s92, v11, 26\n" \
"v_exp_f32_e32 v118, v118\n" \
"v_exp_f32_e32 v119, v119\n" \
"v_exp_f32_e32 v116, v116\n" \
"v_exp_f32_e32 v117, v117\n" \
"v_cvt_pk_bf16_f32 v119, v118, v119\n" \
"v_exp_f32_e32 v114, v114\n" \
"v_exp_f32_e32 v115, v115\n" \
"v_exp_f32_e32 v112, v112\n" \
"v_cvt_pk_bf16_f32 v118, v116, v117\n" \
"s_waitcnt vmcnt(10) lgkmcnt(0)\n" \
"s_barrier\n" \
"v_mfma_f32_16x16x32_bf16 v[80:83], a[128:131], a[32:35], v[40:43]\n" \
"ds_read_b128 a[160:163], v49 offset:8192\n" \
"ds_read_b128 a[164:167], v49 offset:9216\n" \
"v_mfma_f32_16x16x32_bf16 v[80:83], a[132:135], a[36:39], v[80:83]\n" \
"ds_read_b128 a[168:171], v49 offset:10240\n" \
"s_xor_b32 m0, s65, 0x18000\n" \
"v_mfma_f32_16x16x32_bf16 v[80:83], a[136:139], a[40:43], v[80:83]\n" \
"buffer_load_dwordx4 v52, s[8:11], s64 offen offset:0 sc1 lds\n" \
"v_mfma_f32_16x16x32_bf16 v[80:83], a[140:143], a[44:47], v[80:83]\n" \
"ds_read_b128 a[172:175], v49 offset:11264\n" \
"ds_read_b128 a[176:179], v49 offset:12288\n" \
"v_mfma_f32_16x16x32_bf16 v[84:87], a[156:159], a[44:47], v[40:43]\n" \
"ds_read_b128 a[180:183], v49 offset:13312\n" \
"ds_read_b128 a[184:187], v49 offset:14336\n" \
"v_mfma_f32_16x16x32_bf16 v[84:87], a[152:155], a[40:43], v[84:87]\n" \
"ds_read_b128 a[188:191], v49 offset:15360\n" \
"v_cvt_pk_bf16_f32 v117, v114, v115\n" \
"v_mfma_f32_16x16x32_bf16 v[84:87], a[148:151], a[36:39], v[84:87]\n" \
"buffer_load_dwordx4 v52, s[8:11], s64 offen offset:1024 sc1 lds\n" \
"v_mfma_f32_16x16x32_bf16 v[84:87], a[144:147], a[32:35], v[84:87]\n" \
"v_exp_f32_e32 v113, v113\n" \
"v_mfma_f32_16x16x32_bf16 v[92:95], a[144:147], a[48:51], v[44:47]\n" \
"v_cvt_pk_bf16_f32 v116, v112, v113\n" \
"ds_read_b128 a[144:147], v48 offset:12288\n" \
"v_mfma_f32_16x16x32_bf16 v[92:95], a[148:151], a[52:55], v[92:95]\n" \
"v_exp_f32_e32 v120, v120\n" \
"v_mfma_f32_16x16x32_bf16 v[92:95], a[152:155], a[56:59], v[92:95]\n" \
"buffer_load_dwordx4 v52, s[8:11], s64 offen offset:2048 sc1 lds\n" \
"v_mfma_f32_16x16x32_bf16 v[92:95], a[156:159], a[60:63], v[92:95]\n" \
"v_exp_f32_e32 v121, v121\n" \
"v_mfma_f32_16x16x32_bf16 v[88:91], a[140:143], a[60:63], v[44:47]\n" \
"v_exp_f32_e32 v122, v122\n" \
"v_mfma_f32_16x16x32_bf16 v[88:91], a[136:139], a[56:59], v[88:91]\n" \
"v_exp_f32_e32 v123, v123\n" \
"v_mfma_f32_16x16x32_bf16 v[88:91], a[132:135], a[52:55], v[88:91]\n" \
"buffer_load_dwordx4 v52, s[8:11], s64 offen offset:3072 sc1 lds\n" \
"v_mfma_f32_16x16x32_bf16 v[88:91], a[128:131], a[48:51], v[88:91]\n" \
"s_add_u32 m0, s93, 0x0\n" \
"v_cvt_pk_bf16_f32 v120, v120, v121\n" \
"v_mfma_f32_16x16x32_bf16 a[192:195], a[212:215], v[104:107], a[192:195]\n" \
"v_cvt_pk_bf16_f32 v121, v122, v123\n" \
"s_waitcnt lgkmcnt(6)\n" \
"v_mfma_f32_16x16x32_bf16 v[160:163], a[160:163], v[104:107], v[160:163]\n" \
"v_exp_f32_e32 v124, v124\n" \
"v_mfma_f32_16x16x32_bf16 v[164:167], a[164:167], v[104:107], v[164:167]\n" \
"buffer_load_dwordx4 v0, s[80:83], s92 offen offset:0 lds\n" \
"v_mfma_f32_16x16x32_bf16 v[168:171], a[168:171], v[104:107], v[168:171]\n" \
"ds_read_b128 a[128:131], v48 offset:8192\n" \
"s_waitcnt lgkmcnt(2)\n" \
"v_mfma_f32_16x16x32_bf16 v[172:175], a[172:175], v[104:107], v[172:175]\n" \
"v_exp_f32_e32 v125, v125\n" \
"v_mfma_f32_16x16x32_bf16 v[176:179], a[176:179], v[104:107], v[176:179]\n" \
"v_exp_f32_e32 v126, v126\n" \
"buffer_load_dwordx4 v1, s[80:83], s92 offen offset:1024 lds\n" \
"v_mfma_f32_16x16x32_bf16 v[180:183], a[180:183], v[104:107], v[180:183]\n" \
"v_exp_f32_e32 v127, v127\n" \
"v_mfma_f32_16x16x32_bf16 v[184:187], a[184:187], v[104:107], v[184:187]\n" \
"v_cvt_pk_bf16_f32 v122, v124, v125\n" \
"v_cvt_pk_bf16_f32 v123, v126, v127\n" \
"v_mfma_f32_16x16x32_bf16 v[188:191], a[188:191], v[104:107], v[188:191]\n" \
"ds_read_b128 a[132:135], v48 offset:9216\n" \
"ds_read_b128 a[148:151], v48 offset:13312\n" \
"buffer_load_dwordx4 v2, s[80:83], s92 offen offset:2048 lds\n" \
"v_mfma_f32_16x16x32_bf16 v[220:223], a[188:191], v[116:119], v[220:223]\n" \
"ds_read_b128 a[136:139], v48 offset:10240\n" \
"ds_read_b128 a[152:155], v48 offset:14336\n" \
"v_mfma_f32_16x16x32_bf16 v[252:255], a[188:191], v[120:123], v[252:255]\n" \
"ds_read_b128 a[140:143], v48 offset:11264\n" \
"ds_read_b128 a[156:159], v48 offset:15360\n" \
"v_mfma_f32_16x16x32_bf16 v[216:219], a[184:187], v[116:119], v[216:219]\n" \
"v_bfe_u32 v27, v20, 2, 7\n" \
"v_readlane_b32 s96, v11, 1\n" \
"buffer_load_dwordx4 v3, s[80:83], s92 offen offset:3072 lds\n" \
"v_mfma_f32_16x16x32_bf16 v[248:251], a[184:187], v[120:123], v[248:251]\n" \
"s_add_u32 m0, s93, 0x1000\n" \
"v_add_f32_e32 v54, v54, v22\n" \
"v_mfma_f32_16x16x32_bf16 v[212:215], a[180:183], v[116:119], v[212:215]\n" \
"v_max_f32_e32 v62, v62, v54\n" \
"v_add_u32_e32 v49, 0x10000, v48\n" \
"v_mfma_f32_16x16x32_bf16 v[244:247], a[180:183], v[120:123], v[244:247]\n" \
"v_rcp_f32_e32 v54, v54\n" \
"buffer_load_dwordx4 v4, s[80:83], s92 offen offset:0 lds\n" \
"v_mfma_f32_16x16x32_bf16 v[208:211], a[176:179], v[116:119], v[208:211]\n" \
"v_mul_f32_e32 v54, v54, v58\n" \
"v_mul_f32_e32 v128, v54, v128\n" \
"v_mfma_f32_16x16x32_bf16 v[240:243], a[176:179], v[120:123], v[240:243]\n" \
"v_mul_f32_e32 v129, v54, v129\n" \
"v_mul_f32_e32 v136, v54, v136\n" \
"v_mfma_f32_16x16x32_bf16 v[204:207], a[172:175], v[116:119], v[204:207]\n" \
"v_mul_f32_e32 v137, v54, v137\n" \
"v_mul_f32_e32 v144, v54, v144\n" \
"buffer_load_dwordx4 v5, s[80:83], s92 offen offset:1024 lds\n" \
"v_mfma_f32_16x16x32_bf16 v[236:239], a[172:175], v[120:123], v[236:239]\n" \
"v_mul_f32_e32 v145, v54, v145\n" \
"v_mul_f32_e32 v152, v54, v152\n" \
"v_mfma_f32_16x16x32_bf16 v[200:203], a[168:171], v[116:119], v[200:203]\n" \
"v_mul_f32_e32 v153, v54, v153\n" \
"v_cvt_pk_bf16_f32 v128, v128, v129\n" \
"v_mfma_f32_16x16x32_bf16 v[232:235], a[168:171], v[120:123], v[232:235]\n" \
"v_mul_f32_e32 v130, v54, v130\n" \
"v_mul_f32_e32 v131, v54, v131\n" \
"buffer_load_dwordx4 v6, s[80:83], s92 offen offset:2048 lds\n" \
"v_mfma_f32_16x16x32_bf16 v[196:199], a[164:167], v[116:119], v[196:199]\n" \
"v_cvt_pk_bf16_f32 v136, v136, v137\n" \
"v_mul_f32_e32 v138, v54, v138\n" \
"v_mfma_f32_16x16x32_bf16 v[228:231], a[164:167], v[120:123], v[228:231]\n" \
"v_mul_f32_e32 v139, v54, v139\n" \
"v_cvt_pk_bf16_f32 v144, v144, v145\n" \
"v_mfma_f32_16x16x32_bf16 v[192:195], a[160:163], v[116:119], v[192:195]\n" \
"v_mul_f32_e32 v146, v54, v146\n" \
"v_mul_f32_e32 v147, v54, v147\n" \
"buffer_load_dwordx4 v7, s[80:83], s92 offen offset:3072 lds\n" \
"v_mfma_f32_16x16x32_bf16 v[224:227], a[160:163], v[120:123], v[224:227]\n" \
"v_cvt_pk_bf16_f32 v152, v152, v153\n" \
"v_mul_f32_e32 v154, v54, v154\n" \
"v_smfmac_f32_16x16x64_bf16 a[196:199], a[200:203], v[116:123], v21\n" \
"v_mul_f32_e32 v155, v54, v155\n" \
"v_cvt_pk_bf16_f32 v129, v130, v131\n" \
"v_mul_f32_e32 v132, v54, v132\n" \
"v_mul_f32_e32 v133, v54, v133\n" \
"v_mul_f32_e32 v134, v54, v134\n" \
"v_mul_f32_e32 v135, v54, v135\n" \
"v_cvt_pk_bf16_f32 v137, v138, v139\n" \
"v_mul_f32_e32 v140, v54, v140\n" \
"v_mul_f32_e32 v141, v54, v141\n" \
"v_mul_f32_e32 v142, v54, v142\n" \
"v_mul_f32_e32 v143, v54, v143\n" \
"v_cvt_pk_bf16_f32 v145, v146, v147\n" \
"v_mul_f32_e32 v148, v54, v148\n" \
"v_mul_f32_e32 v149, v54, v149\n" \
"v_mul_f32_e32 v150, v54, v150\n" \
"v_mul_f32_e32 v151, v54, v151\n" \
"v_cvt_pk_bf16_f32 v153, v154, v155\n" \
"v_mul_f32_e32 v156, v54, v156\n" \
"v_mul_f32_e32 v157, v54, v157\n" \
"v_mul_f32_e32 v158, v54, v158\n" \
"v_mul_f32_e32 v159, v54, v159\n" \
"v_cvt_pk_bf16_f32 v130, v132, v133\n" \
"v_cvt_pk_bf16_f32 v131, v134, v135\n" \
"buffer_store_dwordx4 v[128:131], v20, s[56:59], s68 offen offset:0 sc1\n" \
"v_cvt_pk_bf16_f32 v138, v140, v141\n" \
"v_cvt_pk_bf16_f32 v139, v142, v143\n" \
"v_cvt_pk_bf16_f32 v146, v148, v149\n" \
"v_cvt_pk_bf16_f32 v147, v150, v151\n" \
"v_cvt_pk_bf16_f32 v154, v156, v157\n" \
"v_cvt_pk_bf16_f32 v155, v158, v159\n" \
"s_add_u32 m0, s93, 0x2000\n" \
"s_xor_b32 s65, s65, 0x8000\n" \
"s_add_u32 s64, s64, 0x10000\n" \
"v_exp_f32_e32 v86, v86\n" \
"buffer_store_dwordx4 v[136:139], v20, s[56:59], s68 offen offset:1024 sc1\n" \
"v_exp_f32_e32 v87, v87\n" \
"v_exp_f32_e32 v84, v84\n" \
"v_exp_f32_e32 v85, v85\n" \
"v_exp_f32_e32 v88, v88\n" \
"v_exp_f32_e32 v89, v89\n" \
"v_cvt_pk_bf16_f32 v87, v86, v87\n" \
"buffer_store_dwordx4 v[144:147], v20, s[56:59], s68 offen offset:2048 sc1\n" \
"v_exp_f32_e32 v82, v82\n" \
"v_exp_f32_e32 v83, v83\n" \
"v_exp_f32_e32 v80, v80\n" \
"v_exp_f32_e32 v81, v81\n" \
"v_exp_f32_e32 v90, v90\n" \
"v_exp_f32_e32 v91, v91\n" \
"buffer_store_dwordx4 v[152:155], v20, s[56:59], s68 offen offset:3072 sc1\n" \
"v_cvt_pk_bf16_f32 v86, v84, v85\n" \
"v_cvt_pk_bf16_f32 v88, v88, v89\n" \
"v_exp_f32_e32 v92, v92\n" \
"v_exp_f32_e32 v93, v93\n" \
"v_exp_f32_e32 v94, v94\n" \
"v_exp_f32_e32 v95, v95\n" \
"v_cvt_pk_bf16_f32 v85, v82, v83\n" \
"buffer_load_dwordx4 v8, s[80:83], s92 offen offset:0 lds\n" \
"v_cvt_pk_bf16_f32 v84, v80, v81\n" \
"v_cvt_pk_bf16_f32 v89, v90, v91\n" \
"v_and_b32_e32 v84, v12, v84\n" \
"v_and_b32_e32 v85, v13, v85\n" \
"v_and_b32_e32 v86, v14, v86\n" \
"v_and_b32_e32 v87, v15, v87\n" \
"s_waitcnt vmcnt(17) lgkmcnt(0)\n" \
"s_barrier\n" \
"v_mfma_f32_16x16x32_bf16 v[120:123], a[128:131], a[48:51], v[44:47]\n" \
"buffer_load_dword v26, v27, s[56:59], s96 offen sc1\n" \
"v_mfma_f32_16x16x32_bf16 v[120:123], a[132:135], a[52:55], v[120:123]\n" \
"ds_read_b128 a[188:191], v49 offset:7168\n" \
"ds_read_b128 a[184:187], v49 offset:6144\n" \
"v_mfma_f32_16x16x32_bf16 v[120:123], a[136:139], a[56:59], v[120:123]\n" \
"s_xor_b32 m0, s65, 0x12000\n" \
"ds_read_b128 a[180:183], v49 offset:5120\n" \
"v_mfma_f32_16x16x32_bf16 v[120:123], a[140:143], a[60:63], v[120:123]\n" \
"ds_read_b128 a[176:179], v49 offset:4096\n" \
"ds_read_b128 a[172:175], v49 offset:3072\n" \
"v_mfma_f32_16x16x32_bf16 v[124:127], a[156:159], a[60:63], v[44:47]\n" \
"buffer_load_dwordx4 v50, s[8:11], s64 offen offset:0 sc1 lds\n" \
"v_mfma_f32_16x16x32_bf16 v[124:127], a[152:155], a[56:59], v[124:127]\n" \
"ds_read_b128 a[168:171], v49 offset:2048\n" \
"v_cvt_pk_bf16_f32 v90, v92, v93\n" \
"v_mfma_f32_16x16x32_bf16 v[124:127], a[148:151], a[52:55], v[124:127]\n" \
"v_cvt_pk_bf16_f32 v91, v94, v95\n" \
"ds_read_b128 a[164:167], v49 offset:1024\n" \
"v_mfma_f32_16x16x32_bf16 v[124:127], a[144:147], a[48:51], v[124:127]\n" \
"ds_read_b128 a[160:163], v49 offset:0\n" \
"ds_read_b128 a[0:3], v49 offset:8192\n" \
"v_mfma_f32_16x16x32_bf16 v[32:35], a[220:223], a[64:67], 0\n" \
"buffer_load_dwordx4 v50, s[8:11], s64 offen offset:1024 sc1 lds\n" \
"v_mfma_f32_16x16x32_bf16 v[36:39], a[220:223], a[80:83], 0\n" \
"ds_read_b128 a[4:7], v49 offset:9216\n" \
"ds_read_b128 a[8:11], v49 offset:10240\n" \
"v_mfma_f32_16x16x32_bf16 v[32:35], a[224:227], a[68:71], v[32:35]\n" \
"ds_read_b128 a[12:15], v49 offset:11264\n" \
"ds_read_b128 a[16:19], v49 offset:12288\n" \
"v_mfma_f32_16x16x32_bf16 v[36:39], a[224:227], a[84:87], v[36:39]\n" \
"ds_read_b128 a[20:23], v49 offset:13312\n" \
"ds_read_b128 a[24:27], v49 offset:14336\n" \
"v_mfma_f32_16x16x32_bf16 v[32:35], a[228:231], a[72:75], v[32:35]\n" \
"buffer_load_dwordx4 v50, s[8:11], s64 offen offset:2048 sc1 lds\n" \
"v_mfma_f32_16x16x32_bf16 v[36:39], a[228:231], a[88:91], v[36:39]\n" \
"ds_read_b128 a[28:31], v49 offset:15360\n" \
"v_readlane_b32 s97, v11, 36\n" \
"v_mfma_f32_16x16x32_bf16 v[32:35], a[232:235], a[76:79], v[32:35]\n" \
"v_exp_f32_e32 v120, v120\n" \
"v_mfma_f32_16x16x32_bf16 v[36:39], a[232:235], a[92:95], v[36:39]\n" \
"v_exp_f32_e32 v121, v121\n" \
"v_smfmac_f32_16x16x64_bf16 a[196:199], a[200:203], v[84:91], v21\n" \
"v_cvt_pk_bf16_f32 v120, v120, v121\n" \
"s_waitcnt lgkmcnt(13)\n" \
"v_mfma_f32_16x16x32_bf16 v[220:223], a[188:191], v[84:87], v[220:223]\n" \
"buffer_load_dwordx4 v50, s[8:11], s64 offen offset:3072 sc1 lds\n" \
"v_mfma_f32_16x16x32_bf16 v[252:255], a[188:191], v[88:91], v[252:255]\n" \
"v_exp_f32_e32 v122, v122\n" \
"v_mfma_f32_16x16x32_bf16 v[216:219], a[184:187], v[84:87], v[216:219]\n" \
"v_exp_f32_e32 v123, v123\n" \
"v_mfma_f32_16x16x32_bf16 v[248:251], a[184:187], v[88:91], v[248:251]\n" \
"v_exp_f32_e32 v124, v124\n" \
"v_mfma_f32_16x16x32_bf16 v[212:215], a[180:183], v[84:87], v[212:215]\n" \
"v_exp_f32_e32 v125, v125\n" \
"v_mfma_f32_16x16x32_bf16 v[244:247], a[180:183], v[88:91], v[244:247]\n" \
"v_cvt_pk_bf16_f32 v121, v122, v123\n" \
"s_waitcnt lgkmcnt(7)\n" \
"v_mfma_f32_16x16x32_bf16 v[208:211], a[176:179], v[84:87], v[208:211]\n" \
"v_exp_f32_e32 v126, v126\n" \
"v_mfma_f32_16x16x32_bf16 v[240:243], a[176:179], v[88:91], v[240:243]\n" \
"v_exp_f32_e32 v127, v127\n" \
"v_mfma_f32_16x16x32_bf16 v[204:207], a[172:175], v[84:87], v[204:207]\n" \
"v_cvt_pk_bf16_f32 v122, v124, v125\n" \
"v_cvt_pk_bf16_f32 v123, v126, v127\n" \
"v_mfma_f32_16x16x32_bf16 v[236:239], a[172:175], v[88:91], v[236:239]\n" \
"v_and_b32_e32 v120, v12, v120\n" \
"v_and_b32_e32 v121, v13, v121\n" \
"v_mfma_f32_16x16x32_bf16 v[200:203], a[168:171], v[84:87], v[200:203]\n" \
"v_and_b32_e32 v122, v14, v122\n" \
"v_and_b32_e32 v123, v15, v123\n" \
"v_mfma_f32_16x16x32_bf16 v[232:235], a[168:171], v[88:91], v[232:235]\n" \
"v_accvgpr_read_b32 v56, a196\n" \
"v_accvgpr_read_b32 v24, a197\n" \
"v_mfma_f32_16x16x32_bf16 v[196:199], a[164:167], v[84:87], v[196:199]\n" \
"v_add_u32_e32 v17, 0x49000, v20\n" \
"v_readlane_b32 s98, v11, 37\n" \
"v_mfma_f32_16x16x32_bf16 v[228:231], a[164:167], v[88:91], v[228:231]\n" \
"v_add_u32_e32 v48, s16, v20\n" \
"ds_read_b128 a[128:131], v48 offset:0\n" \
"v_mfma_f32_16x16x32_bf16 v[192:195], a[160:163], v[84:87], v[192:195]\n" \
"ds_read_b128 a[144:147], v48 offset:4096\n" \
"ds_read_b128 a[132:135], v48 offset:1024\n" \
"v_mfma_f32_16x16x32_bf16 v[224:227], a[160:163], v[88:91], v[224:227]\n" \
"ds_read_b128 a[148:151], v48 offset:5120\n" \
"ds_read_b128 a[136:139], v48 offset:2048\n" \
"v_mfma_f32_16x16x32_bf16 a[196:199], a[212:215], v[120:123], a[196:199]\n" \
"ds_read_b128 a[152:155], v48 offset:6144\n" \
"ds_read_b128 a[140:143], v48 offset:3072\n" \
"v_mfma_f32_16x16x32_bf16 v[224:227], a[0:3], v[120:123], v[224:227]\n" \
"ds_read_b128 a[156:159], v48 offset:7168\n" \
"s_waitcnt lgkmcnt(8)\n" \
"v_mfma_f32_16x16x32_bf16 v[228:231], a[4:7], v[120:123], v[228:231]\n" \
"v_accvgpr_read_b32 v55, a194\n" \
"v_accvgpr_read_b32 v23, a195\n" \
"v_mfma_f32_16x16x32_bf16 a[192:195], a[216:219], a[216:219], 0\n" \
"v_add_f32_e32 v55, v55, v23\n" \
"v_max_f32_e32 v62, v62, v55\n" \
"v_mfma_f32_16x16x32_bf16 v[232:235], a[8:11], v[120:123], v[232:235]\n" \
"v_rcp_f32_e32 v55, v55\n" \
"v_mfma_f32_16x16x32_bf16 v[236:239], a[12:15], v[120:123], v[236:239]\n" \
"v_mul_f32_e32 v55, v55, v59\n" \
"v_mul_f32_e32 v160, v55, v160\n" \
"v_mfma_f32_16x16x32_bf16 v[240:243], a[16:19], v[120:123], v[240:243]\n" \
"v_mul_f32_e32 v161, v55, v161\n" \
"v_mul_f32_e32 v168, v55, v168\n" \
"v_mfma_f32_16x16x32_bf16 v[244:247], a[20:23], v[120:123], v[244:247]\n" \
"v_mul_f32_e32 v169, v55, v169\n" \
"v_mul_f32_e32 v176, v55, v176\n" \
"v_mfma_f32_16x16x32_bf16 v[248:251], a[24:27], v[120:123], v[248:251]\n" \
"v_mul_f32_e32 v177, v55, v177\n" \
"v_mul_f32_e32 v184, v55, v184\n" \
"v_mfma_f32_16x16x32_bf16 v[252:255], a[28:31], v[120:123], v[252:255]\n" \
"v_mul_f32_e32 v185, v55, v185\n" \
"v_cvt_pk_bf16_f32 v160, v160, v161\n" \
"v_mul_f32_e32 v162, v55, v162\n" \
"v_mul_f32_e32 v163, v55, v163\n" \
"v_cvt_pk_bf16_f32 v168, v168, v169\n" \
"v_mul_f32_e32 v170, v55, v170\n" \
"v_mul_f32_e32 v171, v55, v171\n" \
"v_cvt_pk_bf16_f32 v176, v176, v177\n" \
"v_mul_f32_e32 v178, v55, v178\n" \
"s_waitcnt vmcnt(4)\n" \
"v_cmp_ne_u32_e32 vcc, s62, v26\n" \
"v_mul_f32_e32 v179, v55, v179\n" \
"v_cvt_pk_bf16_f32 v184, v184, v185\n" \
"v_mul_f32_e32 v186, v55, v186\n" \
"v_mul_f32_e32 v187, v55, v187\n" \
"v_cvt_pk_bf16_f32 v161, v162, v163\n" \
"s_cbranch_vccnz L_sos_%=\n" \
"L_ros_%=:\n" \
"buffer_load_dwordx4 a[220:223], v20, s[56:59], s97 offen offset:0 sc1\n" \
"v_mul_f32_e32 v164, v55, v164\n" \
"v_mul_f32_e32 v165, v55, v165\n" \
"v_mul_f32_e32 v166, v55, v166\n" \
"v_mul_f32_e32 v167, v55, v167\n" \
"v_cvt_pk_bf16_f32 v169, v170, v171\n" \
"v_mul_f32_e32 v172, v55, v172\n" \
"v_mul_f32_e32 v173, v55, v173\n" \
"v_mul_f32_e32 v174, v55, v174\n" \
"v_mul_f32_e32 v175, v55, v175\n" \
"v_cvt_pk_bf16_f32 v177, v178, v179\n" \
"v_mul_f32_e32 v180, v55, v180\n" \
"buffer_load_dwordx4 a[228:231], v17, s[56:59], s97 offen offset:0 sc1\n" \
"v_mul_f32_e32 v181, v55, v181\n" \
"v_mul_f32_e32 v182, v55, v182\n" \
"v_mul_f32_e32 v183, v55, v183\n" \
"v_cvt_pk_bf16_f32 v185, v186, v187\n" \
"v_mul_f32_e32 v188, v55, v188\n" \
"v_mul_f32_e32 v189, v55, v189\n" \
"v_mul_f32_e32 v190, v55, v190\n" \
"v_mul_f32_e32 v191, v55, v191\n" \
"v_cvt_pk_bf16_f32 v162, v164, v165\n" \
"v_cvt_pk_bf16_f32 v163, v166, v167\n" \
"v_cvt_pk_bf16_f32 v170, v172, v173\n" \
"buffer_store_dwordx4 v[160:163], v20, s[56:59], s69 offen offset:0 sc1\n" \
"v_cvt_pk_bf16_f32 v171, v174, v175\n" \
"v_cvt_pk_bf16_f32 v178, v180, v181\n" \
"v_cvt_pk_bf16_f32 v179, v182, v183\n" \
"v_cvt_pk_bf16_f32 v186, v188, v189\n" \
"v_cvt_pk_bf16_f32 v187, v190, v191\n" \
"v_readlane_b32 s93, v11, 20\n" \
"v_readlane_b32 s84, v11, 15\n" \
"v_readlane_b32 s85, v11, 16\n" \
"v_readlane_b32 s86, v11, 17\n" \
"s_mov_b32 s87, 0x20000\n" \
"v_readlane_b32 s92, v11, 18\n" \
"buffer_store_dwordx4 v[168:171], v20, s[56:59], s69 offen offset:1024 sc1\n" \
"v_readlane_b32 s91, v11, 21\n" \
"v_mfma_f32_16x16x32_bf16 a[208:211], a[216:219], v[128:131], a[208:211]\n" \
"v_mfma_f32_16x16x32_bf16 a[208:211], a[216:219], v[136:139], a[208:211]\n" \
"v_mfma_f32_32x32x16_bf16 v[128:143], a[216:219], a[216:219], 0\n" \
"buffer_store_dwordx4 v[176:179], v20, s[56:59], s69 offen offset:2048 sc1\n" \
"v_mfma_f32_16x16x32_bf16 a[208:211], a[216:219], v[144:147], a[208:211]\n" \
"v_mfma_f32_16x16x32_bf16 a[208:211], a[216:219], v[152:155], a[208:211]\n" \
"v_mfma_f32_32x32x16_bf16 v[144:159], a[216:219], a[216:219], 0\n" \
"buffer_store_dwordx4 v[184:187], v20, s[56:59], s69 offen offset:3072 sc1\n" \
"v_add_f32_e32 v56, v56, v24\n" \
"v_mfma_f32_16x16x32_bf16 a[208:211], a[216:219], v[160:163], a[208:211]\n" \
"v_max_f32_e32 v62, v62, v56\n" \
"v_rcp_f32_e32 v56, v56\n" \
"v_mfma_f32_16x16x32_bf16 a[208:211], a[216:219], v[168:171], a[208:211]\n" \
"v_mfma_f32_32x32x16_bf16 v[160:175], a[216:219], a[216:219], 0\n" \
"buffer_load_dwordx4 a[236:239], v20, s[56:59], s98 offen offset:0 sc1\n" \
"v_mul_f32_e32 v56, v56, v60\n" \
"v_mfma_f32_16x16x32_bf16 a[208:211], a[216:219], v[176:179], a[208:211]\n" \
"v_mul_f32_e32 v192, v56, v192\n" \
"v_mul_f32_e32 v193, v56, v193\n" \
"v_cvt_pk_bf16_f32 v192, v192, v193\n" \
"v_mul_f32_e32 v194, v56, v194\n" \
"v_mul_f32_e32 v195, v56, v195\n" \
"buffer_load_dwordx4 a[244:247], v17, s[56:59], s98 offen offset:0 sc1\n" \
"v_mfma_f32_16x16x32_bf16 a[208:211], a[216:219], v[184:187], a[208:211]\n" \
"v_mfma_f32_32x32x16_bf16 v[176:191], a[216:219], a[216:219], 0\n" \
"v_cvt_pk_bf16_f32 v193, v194, v195\n" \
"v_mul_f32_e32 v196, v56, v196\n" \
"v_mul_f32_e32 v197, v56, v197\n" \
"v_mul_f32_e32 v198, v56, v198\n" \
"v_mul_f32_e32 v199, v56, v199\n" \
"buffer_load_dwordx4 a[224:227], v20, s[56:59], s97 offen offset:1024 sc1\n" \
"v_mul_f32_e32 v200, v56, v200\n" \
"v_mul_f32_e32 v201, v56, v201\n" \
"v_accvgpr_read_b32 v57, a198\n" \
"v_accvgpr_read_b32 v25, a199\n" \
"v_mfma_f32_16x16x32_bf16 a[196:199], a[216:219], a[216:219], 0\n" \
"v_cvt_pk_bf16_f32 v194, v196, v197\n" \
"v_cvt_pk_bf16_f32 v195, v198, v199\n" \
"v_cvt_pk_bf16_f32 v200, v200, v201\n" \
"v_mul_f32_e32 v202, v56, v202\n" \
"v_mul_f32_e32 v203, v56, v203\n" \
"buffer_store_dwordx4 v[192:195], v20, s[56:59], s70 offen offset:0 sc1\n" \
"v_add_f32_e32 v57, v57, v25\n" \
"v_mfma_f32_16x16x32_bf16 a[208:211], a[216:219], v[192:195], a[208:211]\n" \
"v_cvt_pk_bf16_f32 v201, v202, v203\n" \
"v_mul_f32_e32 v204, v56, v204\n" \
"v_mul_f32_e32 v205, v56, v205\n" \
"v_mul_f32_e32 v206, v56, v206\n" \
"v_mul_f32_e32 v207, v56, v207\n" \
"v_mul_f32_e32 v208, v56, v208\n" \
"v_mul_f32_e32 v209, v56, v209\n" \
"v_max_f32_e32 v62, v62, v57\n" \
"buffer_load_dwordx4 a[232:235], v17, s[56:59], s97 offen offset:1024 sc1\n" \
"s_waitcnt vmcnt(16) lgkmcnt(0)\n" \
"s_barrier\n" \
"v_mfma_f32_16x16x32_bf16 v[64:67], a[128:131], a[64:67], v[32:35]\n" \
"s_add_u32 m0, s93, 0x0\n" \
"v_cvt_pk_bf16_f32 v202, v204, v205\n" \
"v_mfma_f32_16x16x32_bf16 v[64:67], a[132:135], a[68:71], v[64:67]\n" \
"buffer_load_dwordx4 a[240:243], v20, s[56:59], s98 offen offset:1024 sc1\n" \
"v_mfma_f32_16x16x32_bf16 v[64:67], a[136:139], a[72:75], v[64:67]\n" \
"v_cvt_pk_bf16_f32 v203, v206, v207\n" \
"v_cvt_pk_bf16_f32 v208, v208, v209\n" \
"v_mfma_f32_16x16x32_bf16 v[64:67], a[140:143], a[76:79], v[64:67]\n" \
"v_mul_f32_e32 v210, v56, v210\n" \
"v_mul_f32_e32 v211, v56, v211\n" \
"v_mfma_f32_16x16x32_bf16 v[68:71], a[156:159], a[76:79], v[32:35]\n" \
"v_rcp_f32_e32 v57, v57\n" \
"buffer_store_dwordx4 v[200:203], v20, s[56:59], s70 offen offset:1024 sc1\n" \
"v_mfma_f32_16x16x32_bf16 v[68:71], a[152:155], a[72:75], v[68:71]\n" \
"v_mfma_f32_16x16x32_bf16 a[208:211], a[216:219], v[200:203], a[208:211]\n" \
"v_mfma_f32_32x32x16_bf16 v[192:207], a[216:219], a[216:219], 0\n" \
"v_cvt_pk_bf16_f32 v209, v210, v211\n" \
"buffer_load_dwordx4 a[248:251], v17, s[56:59], s98 offen offset:1024 sc1\n" \
"v_mul_f32_e32 v212, v56, v212\n" \
"v_mul_f32_e32 v213, v56, v213\n" \
"v_mul_f32_e32 v214, v56, v214\n" \
"v_mfma_f32_16x16x32_bf16 v[68:71], a[148:151], a[68:71], v[68:71]\n" \
"v_mul_f32_e32 v215, v56, v215\n" \
"v_mul_f32_e32 v216, v56, v216\n" \
"v_mfma_f32_16x16x32_bf16 v[68:71], a[144:147], a[64:67], v[68:71]\n" \
"v_mul_f32_e32 v217, v56, v217\n" \
"v_cvt_pk_bf16_f32 v210, v212, v213\n" \
"buffer_load_dwordx4 v0, s[84:87], s92 offen offset:0 lds\n" \
"v_mfma_f32_16x16x32_bf16 v[76:79], a[144:147], a[80:83], v[36:39]\n" \
"ds_read_b128 a[144:147], v48 offset:12288\n" \
"v_cvt_pk_bf16_f32 v211, v214, v215\n" \
"v_mfma_f32_16x16x32_bf16 v[76:79], a[148:151], a[84:87], v[76:79]\n" \
"ds_read_b128 a[148:151], v48 offset:13312\n" \
"v_cvt_pk_bf16_f32 v216, v216, v217\n" \
"v_mfma_f32_16x16x32_bf16 v[76:79], a[152:155], a[88:91], v[76:79]\n" \
"ds_read_b128 a[152:155], v48 offset:14336\n" \
"v_mul_f32_e32 v218, v56, v218\n" \
"buffer_store_dwordx4 v[208:211], v20, s[56:59], s70 offen offset:2048 sc1\n" \
"v_mfma_f32_16x16x32_bf16 v[76:79], a[156:159], a[92:95], v[76:79]\n" \
"ds_read_b128 a[156:159], v48 offset:15360\n" \
"v_mul_f32_e32 v219, v56, v219\n" \
"v_mfma_f32_16x16x32_bf16 v[72:75], a[140:143], a[92:95], v[36:39]\n" \
"ds_read_b128 a[140:143], v48 offset:11264\n" \
"v_mul_f32_e32 v57, v57, v61\n" \
"v_mfma_f32_16x16x32_bf16 v[72:75], a[136:139], a[88:91], v[72:75]\n" \
".fill (L_ab + 28672 - .) / 4, 4, 0xBF800000\n" \
"L_ap7:\n" \
"s_cbranch_execz L_ap0\n" \
"ds_read_b128 a[136:139], v48 offset:10240\n" \
"v_cvt_pk_bf16_f32 v217, v218, v219\n" \
"buffer_load_dwordx4 v1, s[84:87], s92 offen offset:1024 lds\n" \
"v_mfma_f32_16x16x32_bf16 v[72:75], a[132:135], a[84:87], v[72:75]\n" \
"ds_read_b128 a[132:135], v48 offset:9216\n" \
"v_mul_f32_e32 v220, v56, v220\n" \
"v_mfma_f32_16x16x32_bf16 v[72:75], a[128:131], a[80:83], v[72:75]\n" \
"ds_read_b128 a[128:131], v48 offset:8192\n" \
"v_mfma_f32_16x16x32_bf16 a[208:211], a[216:219], v[208:211], a[208:211]\n" \
"v_mul_f32_e32 v221, v56, v221\n" \
"buffer_load_dwordx4 v2, s[84:87], s92 offen offset:2048 lds\n" \
"v_mul_f32_e32 v222, v56, v222\n" \
"v_mul_f32_e32 v223, v56, v223\n" \
"v_mul_f32_e32 v224, v57, v224\n" \
"v_mul_f32_e32 v225, v57, v225\n" \
"v_cvt_pk_bf16_f32 v218, v220, v221\n" \
"v_cvt_pk_bf16_f32 v219, v222, v223\n" \
"v_cvt_pk_bf16_f32 v224, v224, v225\n" \
"v_mul_f32_e32 v226, v57, v226\n" \
"v_mul_f32_e32 v227, v57, v227\n" \
"v_mfma_f32_16x16x32_bf16 a[208:211], a[216:219], v[216:219], a[208:211]\n" \
"buffer_store_dwordx4 v[216:219], v20, s[56:59], s70 offen offset:3072 sc1\n" \
"v_cvt_pk_bf16_f32 v225, v226, v227\n" \
"v_mul_f32_e32 v228, v57, v228\n" \
"v_mfma_f32_32x32x16_bf16 v[208:223], a[216:219], a[216:219], 0\n" \
"v_mul_f32_e32 v229, v57, v229\n" \
"v_mul_f32_e32 v230, v57, v230\n" \
"v_mul_f32_e32 v231, v57, v231\n" \
"v_mul_f32_e32 v232, v57, v232\n" \
"v_mul_f32_e32 v233, v57, v233\n" \
"v_cvt_pk_bf16_f32 v226, v228, v229\n" \
"v_cvt_pk_bf16_f32 v227, v230, v231\n" \
"buffer_store_dwordx4 v[224:227], v20, s[56:59], s71 offen offset:0 sc1\n" \
"v_cvt_pk_bf16_f32 v232, v232, v233\n" \
"v_mul_f32_e32 v234, v57, v234\n" \
"v_mul_f32_e32 v235, v57, v235\n" \
"v_mfma_f32_16x16x32_bf16 a[208:211], a[216:219], v[224:227], a[208:211]\n" \
"v_cvt_pk_bf16_f32 v233, v234, v235\n" \
"v_mul_f32_e32 v236, v57, v236\n" \
"v_mul_f32_e32 v237, v57, v237\n" \
"v_mul_f32_e32 v238, v57, v238\n" \
"v_mul_f32_e32 v239, v57, v239\n" \
"v_mul_f32_e32 v240, v57, v240\n" \
"buffer_load_dwordx4 v3, s[84:87], s92 offen offset:3072 lds\n" \
"s_add_u32 m0, s91, 0x1000\n" \
"v_mul_f32_e32 v241, v57, v241\n" \
"v_cvt_pk_bf16_f32 v234, v236, v237\n" \
"v_cvt_pk_bf16_f32 v235, v238, v239\n" \
"v_cvt_pk_bf16_f32 v240, v240, v241\n" \
"v_mul_f32_e32 v242, v57, v242\n" \
"v_mul_f32_e32 v243, v57, v243\n" \
"v_mfma_f32_16x16x32_bf16 a[208:211], a[216:219], v[232:235], a[208:211]\n" \
"v_cvt_pk_bf16_f32 v241, v242, v243\n" \
"v_mul_f32_e32 v244, v57, v244\n" \
"buffer_store_dwordx4 v[232:235], v20, s[56:59], s71 offen offset:1024 sc1\n" \
"v_mul_f32_e32 v245, v57, v245\n" \
"v_mul_f32_e32 v246, v57, v246\n" \
"v_mfma_f32_32x32x16_bf16 v[224:239], a[216:219], a[216:219], 0\n" \
"v_mul_f32_e32 v247, v57, v247\n" \
"v_mul_f32_e32 v248, v57, v248\n" \
"v_mul_f32_e32 v249, v57, v249\n" \
"v_cvt_pk_bf16_f32 v242, v244, v245\n" \
"v_cvt_pk_bf16_f32 v243, v246, v247\n" \
"v_cvt_pk_bf16_f32 v248, v248, v249\n" \
"v_mul_f32_e32 v250, v57, v250\n" \
"buffer_store_dwordx4 v[240:243], v20, s[56:59], s71 offen offset:2048 sc1\n" \
"v_mul_f32_e32 v251, v57, v251\n" \
"v_mfma_f32_16x16x32_bf16 a[208:211], a[216:219], v[240:243], a[208:211]\n" \
"v_cvt_pk_bf16_f32 v249, v250, v251\n" \
"v_mul_f32_e32 v252, v57, v252\n" \
"v_mul_f32_e32 v253, v57, v253\n" \
"v_mul_f32_e32 v254, v57, v254\n" \
"v_mul_f32_e32 v255, v57, v255\n" \
"v_cvt_pk_bf16_f32 v250, v252, v253\n" \
"v_cvt_pk_bf16_f32 v251, v254, v255\n" \
"v_add_u32_e32 v53, 0x20000, v63\n" \
"buffer_store_dwordx4 v[248:251], v20, s[56:59], s71 offen offset:3072 sc1\n" \
"v_mfma_f32_16x16x32_bf16 a[208:211], a[216:219], v[248:251], a[208:211]\n" \
"v_exp_f32_e32 v70, v70\n" \
"v_mfma_f32_32x32x16_bf16 v[240:255], a[216:219], a[216:219], 0\n" \
"v_exp_f32_e32 v71, v71\n" \
"v_exp_f32_e32 v72, v72\n" \
"v_exp_f32_e32 v73, v73\n" \
"buffer_load_dwordx4 v4, s[84:87], s92 offen offset:0 lds\n" \
"s_add_u32 s64, s64, 0x10000\n" \
"v_cmp_lt_f32_e64 s[72:73], s21, v62\n" \
"v_accvgpr_read_b32 v63, a208\n" \
"v_cmp_u_f32_e32 vcc, v63, v63\n" \
"v_readlane_b32 s95, v11, 3\n" \
"v_exp_f32_e32 v68, v68\n" \
"v_exp_f32_e32 v69, v69\n" \
"v_exp_f32_e32 v74, v74\n" \
"buffer_load_dwordx4 v5, s[84:87], s92 offen offset:1024 lds\n" \
"v_exp_f32_e32 v75, v75\n" \
"s_or_b64 vcc, s[72:73], vcc\n" \
"s_cselect_b32 s95, 0x40000000, s95\n" \
"s_cselect_b32 s94, 0x7f800000, 0\n" \
"v_cvt_pk_bf16_f32 v71, v70, v71\n" \
"v_exp_f32_e32 v66, v66\n" \
"v_exp_f32_e32 v67, v67\n" \
"v_exp_f32_e32 v64, v64\n" \
"buffer_load_dwordx4 v6, s[84:87], s92 offen offset:2048 lds\n" \
"v_exp_f32_e32 v65, v65\n" \
"v_cvt_pk_bf16_f32 v72, v72, v73\n" \
"v_exp_f32_e32 v76, v76\n" \
"v_exp_f32_e32 v77, v77\n" \
"v_exp_f32_e32 v78, v78\n" \
"v_exp_f32_e32 v79, v79\n" \
"buffer_load_dwordx4 v7, s[84:87], s92 offen offset:3072 lds\n" \
"s_add_u32 m0, s91, 0x2000\n" \
"v_cvt_pk_bf16_f32 v70, v68, v69\n" \
"v_cvt_pk_bf16_f32 v73, v74, v75\n" \
"v_add_u32_e32 v49, 0x10000, v48\n" \
"s_xor_b32 s65, s65, 0x8000\n" \
"v_cvt_pk_bf16_f32 v69, v66, v67\n" \
"v_cvt_pk_bf16_f32 v68, v64, v65\n" \
"v_cvt_pk_bf16_f32 v74, v76, v77\n" \
"v_cvt_pk_bf16_f32 v75, v78, v79\n" \
"v_xor_b32_e32 v48, 0x8000, v48\n" \
"s_sub_u32 s66, s19, 2\n" \
"buffer_load_dwordx4 v8, s[84:87], s92 offen offset:0 lds\n" \
"s_waitcnt vmcnt(18) lgkmcnt(0)\n" \
"s_barrier\n" \
"s_branch L_f1_%=\n" \
".fill (L_ab + 32768 - .) / 4, 4, 0xBF800000\n" \
".fill (L_ab + 32768 - .) / 4, 4, 0xBF800000\n" \
"L_ap8:\n" \
"s_setpc_b64 s[88:89]\n" \
"L_out_%=:\n" \
"v_bfe_u32 v38, v20, 2, 4\n" \
"v_readlane_b32 s64, v11, 2\n" \
"v_add_u32_e32 v32, 0x49000, v20\n" \
"v_add_u32_e32 v34, 0x20000, v10\n" \
"ds_read_b64_tr_b16 a[0:1], v34 offset:0\n" \
"ds_read_b64_tr_b16 a[2:3], v34 offset:4608\n" \
"ds_read_b64_tr_b16 a[4:5], v34 offset:32\n" \
"buffer_load_dword v26, v38, s[56:59], s64 offen sc1\n" \
"ds_read_b64_tr_b16 a[6:7], v34 offset:4640\n" \
"ds_read_b64_tr_b16 a[8:9], v34 offset:64\n" \
"ds_read_b64_tr_b16 a[10:11], v34 offset:4672\n" \
"ds_read_b64_tr_b16 a[12:13], v34 offset:96\n" \
"buffer_store_dwordx4 v[64:67], v20, s[56:59], s61 offen offset:0 sc1\n" \
"ds_read_b64_tr_b16 a[14:15], v34 offset:4704\n" \
"v_mov_b32_e32 v39, s62\n" \
"v_readlane_b32 s70, v11, 5\n" \
"v_readlane_b32 s71, v11, 4\n" \
"buffer_store_dwordx4 v[68:71], v20, s[56:59], s61 offen offset:1024 sc1\n" \
"ds_read_b64_tr_b16 a[16:17], v34 offset:128\n" \
"ds_read_b64_tr_b16 a[18:19], v34 offset:4736\n" \
"ds_read_b64_tr_b16 a[20:21], v34 offset:160\n" \
"ds_read_b64_tr_b16 a[22:23], v34 offset:4768\n" \
"buffer_store_dwordx4 v[72:75], v20, s[56:59], s61 offen offset:2048 sc1\n" \
"ds_read_b64_tr_b16 a[24:25], v34 offset:192\n" \
"ds_read_b64_tr_b16 a[26:27], v34 offset:4800\n" \
"ds_read_b64_tr_b16 a[28:29], v34 offset:224\n" \
"ds_read_b64_tr_b16 a[30:31], v34 offset:4832\n" \
"buffer_store_dwordx4 v[76:79], v20, s[56:59], s61 offen offset:3072 sc1\n" \
"ds_read_b64_tr_b16 a[192:193], v34 offset:256\n" \
"ds_read_b64_tr_b16 a[194:195], v34 offset:4864\n" \
"ds_read_b64_tr_b16 a[196:197], v34 offset:9216\n" \
"ds_read_b64_tr_b16 a[198:199], v34 offset:13824\n" \
"buffer_store_dwordx4 v[80:83], v32, s[56:59], s61 offen offset:0 sc1\n" \
"ds_read_b64_tr_b16 a[200:201], v34 offset:9248\n" \
"ds_read_b64_tr_b16 a[202:203], v34 offset:13856\n" \
"ds_read_b64_tr_b16 a[204:205], v34 offset:9280\n" \
"ds_read_b64_tr_b16 a[206:207], v34 offset:13888\n" \
"buffer_store_dwordx4 v[84:87], v32, s[56:59], s61 offen offset:1024 sc1\n" \
"ds_read_b64_tr_b16 a[208:209], v34 offset:9312\n" \
"s_waitcnt vmcnt(16) lgkmcnt(15)\n" \
"v_mfma_f32_16x16x32_bf16 v[112:115], a[0:3], a[96:99], 0\n" \
"buffer_store_dwordx4 v[96:99], v32, s[56:59], s61 offen offset:2048 sc1\n" \
"v_mfma_f32_16x16x32_bf16 v[116:119], a[0:3], a[112:115], 0\n" \
"ds_read_b64_tr_b16 a[210:211], v34 offset:13920\n" \
"ds_read_b64_tr_b16 a[212:213], v34 offset:9344\n" \
"buffer_store_dwordx4 v[100:103], v32, s[56:59], s61 offen offset:3072 sc1\n" \
"v_mfma_f32_16x16x32_bf16 v[184:187], a[0:3], v[64:67], 0\n" \
"ds_read_b64_tr_b16 a[214:215], v34 offset:13952\n" \
"ds_read_b64_tr_b16 a[216:217], v34 offset:9376\n" \
"v_mfma_f32_16x16x32_bf16 v[188:191], a[0:3], v[80:83], 0\n" \
"ds_read_b64_tr_b16 a[218:219], v34 offset:13984\n" \
"ds_read_b64_tr_b16 a[0:1], v34 offset:9408\n" \
"v_mfma_f32_16x16x32_bf16 v[120:123], a[4:7], a[96:99], 0\n" \
"ds_read_b64_tr_b16 a[2:3], v34 offset:14016\n" \
"v_readlane_b32 s65, v11, 30\n" \
"v_mfma_f32_16x16x32_bf16 v[124:127], a[4:7], a[112:115], 0\n" \
"v_lshrrev_b32_e32 v44, 9, v20\n" \
"v_mul_u32_u24_e32 v44, 0xb40, v44\n" \
"v_mfma_f32_16x16x32_bf16 v[192:195], a[4:7], v[64:67], 0\n" \
"v_add_u32_e32 v35, v44, v10\n" \
"v_readlane_b32 s67, v11, 23\n" \
"v_mfma_f32_16x16x32_bf16 v[196:199], a[4:7], v[80:83], 0\n" \
"ds_read_b64_tr_b16 a[4:5], v34 offset:9440\n" \
"ds_read_b64_tr_b16 a[6:7], v34 offset:14048\n" \
"v_mfma_f32_16x16x32_bf16 v[128:131], a[8:11], a[96:99], 0\n" \
"v_readlane_b32 s80, v11, 11\n" \
"v_readlane_b32 s81, v11, 12\n" \
"v_mfma_f32_16x16x32_bf16 v[132:135], a[8:11], a[112:115], 0\n" \
"s_mov_b32 s82, 0x47fca0\n" \
"s_mov_b32 s83, 0x20000\n" \
"v_mfma_f32_16x16x32_bf16 v[200:203], a[8:11], v[64:67], 0\n" \
"v_readlane_b32 s66, v11, 28\n" \
"s_add_u32 m0, s67, 0x0\n" \
"v_mfma_f32_16x16x32_bf16 v[204:207], a[8:11], v[80:83], 0\n" \
"ds_read_b64_tr_b16 a[8:9], v34 offset:9472\n" \
"ds_read_b64_tr_b16 a[10:11], v34 offset:14080\n" \
"v_mfma_f32_16x16x32_bf16 v[136:139], a[12:15], a[96:99], 0\n" \
"buffer_load_dwordx4 v0, s[80:83], s66 offen offset:0 lds\n" \
"v_mfma_f32_16x16x32_bf16 v[140:143], a[12:15], a[112:115], 0\n" \
"v_readlane_b32 s92, v11, 24\n" \
"v_readlane_b32 s73, v11, 29\n" \
"v_mfma_f32_16x16x32_bf16 v[208:211], a[12:15], v[64:67], 0\n" \
"v_readlane_b32 s94, v11, 25\n" \
"v_readlane_b32 s84, v11, 9\n" \
"v_mfma_f32_16x16x32_bf16 v[212:215], a[12:15], v[80:83], 0\n" \
"ds_read_b64_tr_b16 a[12:13], v34 offset:18432\n" \
"ds_read_b64_tr_b16 a[14:15], v34 offset:23040\n" \
"v_mfma_f32_16x16x32_bf16 v[144:147], a[16:19], a[96:99], 0\n" \
"v_readlane_b32 s85, v11, 10\n" \
"s_mov_b32 s86, 0xa1ca0\n" \
"v_mfma_f32_16x16x32_bf16 v[148:151], a[16:19], a[112:115], 0\n" \
"buffer_load_dwordx4 v1, s[80:83], s66 offen offset:1024 lds\n" \
"v_mfma_f32_16x16x32_bf16 v[216:219], a[16:19], v[64:67], 0\n" \
"s_mov_b32 s87, 0x20000\n" \
"v_readlane_b32 s68, v11, 31\n" \
"v_mfma_f32_16x16x32_bf16 v[220:223], a[16:19], v[80:83], 0\n" \
"ds_read_b64_tr_b16 a[16:17], v34 offset:18464\n" \
"s_waitcnt lgkmcnt(7)\n" \
"v_mfma_f32_16x16x32_bf16 v[152:155], a[20:23], a[96:99], 0\n" \
"ds_read_b64_tr_b16 a[18:19], v34 offset:23072\n" \
"v_add_u32_e32 v33, 0x10000, v10\n" \
"v_mfma_f32_16x16x32_bf16 v[156:159], a[20:23], a[112:115], 0\n" \
"v_readlane_b32 s69, v11, 32\n" \
"v_readlane_b32 s72, v11, 34\n" \
"v_mfma_f32_16x16x32_bf16 v[224:227], a[20:23], v[64:67], 0\n" \
"buffer_load_dwordx4 v2, s[80:83], s66 offen offset:2048 lds\n" \
"v_mfma_f32_16x16x32_bf16 v[228:231], a[20:23], v[80:83], 0\n" \
"ds_read_b64_tr_b16 a[20:21], v34 offset:18496\n" \
"ds_read_b64_tr_b16 a[22:23], v34 offset:23104\n" \
"v_mfma_f32_16x16x32_bf16 v[160:163], a[24:27], a[96:99], 0\n" \
"v_readlane_b32 s93, v11, 35\n" \
"v_lshlrev_b32_e32 v41, 12, v20\n" \
"v_mfma_f32_16x16x32_bf16 v[164:167], a[24:27], a[112:115], 0\n" \
"v_readlane_b32 s95, v11, 38\n" \
"v_readlane_b32 s96, v11, 39\n" \
"v_mfma_f32_16x16x32_bf16 v[232:235], a[24:27], v[64:67], 0\n" \
"v_readlane_b32 s63, v11, 41\n" \
"v_bfe_u32 v40, v20, 2, 7\n" \
"v_mfma_f32_16x16x32_bf16 v[236:239], a[24:27], v[80:83], 0\n" \
"ds_read_b64_tr_b16 a[24:25], v34 offset:18528\n" \
"ds_read_b64_tr_b16 a[26:27], v34 offset:23136\n" \
"v_mfma_f32_16x16x32_bf16 v[168:171], a[28:31], a[96:99], 0\n" \
"buffer_load_dwordx4 v3, s[80:83], s66 offen offset:3072 lds\n" \
"v_mfma_f32_16x16x32_bf16 v[172:175], a[28:31], a[112:115], 0\n" \
"s_add_u32 m0, s67, 0x1000\n" \
"s_add_u32 s74, s63, 0x3800\n" \
"v_mfma_f32_16x16x32_bf16 v[240:243], a[28:31], v[64:67], 0\n" \
"v_readlane_b32 s100, v11, 7\n" \
"v_readlane_b32 s97, v11, 40\n" \
"v_mfma_f32_16x16x32_bf16 v[244:247], a[28:31], v[80:83], 0\n" \
"ds_read_b64_tr_b16 a[28:29], v34 offset:18560\n" \
"ds_read_b64_tr_b16 a[30:31], v34 offset:23168\n" \
"v_mfma_f32_16x16x32_bf16 v[176:179], a[192:195], a[96:99], 0\n" \
"v_bfe_u32 v44, v20, 4, 4\n" \
"v_bfe_u32 v45, v20, 8, 2\n" \
"v_mfma_f32_16x16x32_bf16 v[180:183], a[192:195], a[112:115], 0\n" \
"buffer_load_dwordx4 v4, s[80:83], s66 offen offset:0 lds\n" \
"v_mfma_f32_16x16x32_bf16 v[248:251], a[192:195], v[64:67], 0\n" \
"v_mul_u32_u24_e32 v42, 0x480, v44\n" \
"v_lshl_add_u32 v42, v45, 3, v42\n" \
"v_mfma_f32_16x16x32_bf16 v[252:255], a[192:195], v[80:83], 0\n" \
"ds_read_b64_tr_b16 a[192:193], v34 offset:18592\n" \
"ds_read_b64_tr_b16 a[194:195], v34 offset:23200\n" \
"v_mfma_f32_16x16x32_bf16 v[112:115], a[196:199], a[100:103], v[112:115]\n" \
"v_lshrrev_b32_e32 v44, 1, v44\n" \
"v_lshlrev_b32_e32 v45, 1, v45\n" \
"v_mfma_f32_16x16x32_bf16 v[116:119], a[196:199], a[116:119], v[116:119]\n" \
"v_sub_u32_e32 v44, v44, v45\n" \
"v_and_b32_e32 v45, 16, v20\n" \
"v_mfma_f32_16x16x32_bf16 v[184:187], a[196:199], v[68:71], v[184:187]\n" \
"buffer_load_dwordx4 v5, s[80:83], s66 offen offset:1024 lds\n" \
"v_mfma_f32_16x16x32_bf16 v[188:191], a[196:199], v[84:87], v[188:191]\n" \
"ds_read_b64_tr_b16 a[196:197], v34 offset:18624\n" \
"ds_read_b64_tr_b16 a[198:199], v34 offset:23232\n" \
"v_mfma_f32_16x16x32_bf16 v[120:123], a[200:203], a[100:103], v[120:123]\n" \
"v_mov_b32_e32 v46, 0x3f80\n" \
"v_lshlrev_b32_e32 v45, v45, v46\n" \
"v_mfma_f32_16x16x32_bf16 v[124:127], a[200:203], a[116:119], v[124:127]\n" \
"v_mov_b32_e32 v52, 0\n" \
"v_mov_b32_e32 v53, 0\n" \
"v_mfma_f32_16x16x32_bf16 v[192:195], a[200:203], v[68:71], v[192:195]\n" \
"s_waitcnt vmcnt(14)\n" \
"v_cmp_ne_u32_e32 vcc, s62, v26\n" \
"v_mfma_f32_16x16x32_bf16 v[196:199], a[200:203], v[84:87], v[196:199]\n" \
"ds_read_b64_tr_b16 a[200:201], v34 offset:18656\n" \
"ds_read_b64_tr_b16 a[202:203], v34 offset:23264\n" \
"v_mfma_f32_16x16x32_bf16 v[128:131], a[204:207], a[100:103], v[128:131]\n" \
"buffer_load_dwordx4 v6, s[80:83], s66 offen offset:2048 lds\n" \
"v_mfma_f32_16x16x32_bf16 v[132:135], a[204:207], a[116:119], v[132:135]\n" \
"s_cbranch_vccnz L_sa_%=\n" \
"L_ra_%=:\n" \
"v_readlane_b32 s64, v11, 33\n" \
"v_mfma_f32_16x16x32_bf16 v[200:203], a[204:207], v[68:71], v[200:203]\n" \
"v_mov_b32_e32 v48, 0\n" \
"v_mov_b32_e32 v49, 0\n" \
"v_mfma_f32_16x16x32_bf16 v[204:207], a[204:207], v[84:87], v[204:207]\n" \
"ds_read_b64_tr_b16 a[204:205], v34 offset:18688\n" \
"ds_read_b64_tr_b16 a[206:207], v34 offset:23296\n" \
"v_mfma_f32_16x16x32_bf16 v[136:139], a[208:211], a[100:103], v[136:139]\n" \
"v_add_u32_e32 v43, 0x9000, v42\n" \
"v_readlane_b32 s88, v11, 13\n" \
"v_mfma_f32_16x16x32_bf16 v[140:143], a[208:211], a[116:119], v[140:143]\n" \
"buffer_load_dwordx4 a[64:67], v20, s[56:59], s65 offen offset:0 sc1\n" \
"v_mfma_f32_16x16x32_bf16 v[208:211], a[208:211], v[68:71], v[208:211]\n" \
"v_readlane_b32 s89, v11, 14\n" \
"s_mov_b32 s90, 0x240000\n" \
"v_mfma_f32_16x16x32_bf16 v[212:215], a[208:211], v[84:87], v[212:215]\n" \
"s_mov_b32 s91, 0x20000\n" \
"v_mov_b32_e32 v46, s62\n" \
"v_mfma_f32_16x16x32_bf16 v[144:147], a[212:215], a[100:103], v[144:147]\n" \
"v_mfma_f32_16x16x32_bf16 v[148:151], a[212:215], a[116:119], v[148:151]\n" \
"v_mfma_f32_16x16x32_bf16 v[216:219], a[212:215], v[68:71], v[216:219]\n" \
"buffer_load_dwordx4 a[80:83], v32, s[56:59], s65 offen offset:0 sc1\n" \
"v_mfma_f32_16x16x32_bf16 v[220:223], a[212:215], v[84:87], v[220:223]\n" \
"v_mfma_f32_16x16x32_bf16 v[152:155], a[216:219], a[100:103], v[152:155]\n" \
"v_mfma_f32_16x16x32_bf16 v[156:159], a[216:219], a[116:119], v[156:159]\n" \
"v_mfma_f32_16x16x32_bf16 v[224:227], a[216:219], v[68:71], v[224:227]\n" \
"v_mfma_f32_16x16x32_bf16 v[228:231], a[216:219], v[84:87], v[228:231]\n" \
"buffer_load_dwordx4 a[68:71], v20, s[56:59], s65 offen offset:1024 sc1\n" \
"v_mfma_f32_16x16x32_bf16 v[160:163], a[0:3], a[100:103], v[160:163]\n" \
"v_mfma_f32_16x16x32_bf16 v[164:167], a[0:3], a[116:119], v[164:167]\n" \
"v_mfma_f32_16x16x32_bf16 v[232:235], a[0:3], v[68:71], v[232:235]\n" \
"v_mfma_f32_16x16x32_bf16 v[236:239], a[0:3], v[84:87], v[236:239]\n" \
"s_waitcnt lgkmcnt(0)\n" \
"v_mfma_f32_16x16x32_bf16 v[168:171], a[4:7], a[100:103], v[168:171]\n" \
"buffer_load_dwordx4 a[84:87], v32, s[56:59], s65 offen offset:1024 sc1\n" \
"v_mfma_f32_16x16x32_bf16 v[172:175], a[4:7], a[116:119], v[172:175]\n" \
"v_mfma_f32_16x16x32_bf16 v[240:243], a[4:7], v[68:71], v[240:243]\n" \
"v_mfma_f32_16x16x32_bf16 v[244:247], a[4:7], v[84:87], v[244:247]\n" \
"v_mfma_f32_16x16x32_bf16 v[176:179], a[8:11], a[100:103], v[176:179]\n" \
"v_mfma_f32_16x16x32_bf16 v[180:183], a[8:11], a[116:119], v[180:183]\n" \
"buffer_load_dwordx4 a[72:75], v20, s[56:59], s65 offen offset:2048 sc1\n" \
"v_mfma_f32_16x16x32_bf16 v[248:251], a[8:11], v[68:71], v[248:251]\n" \
"v_mfma_f32_16x16x32_bf16 v[252:255], a[8:11], v[84:87], v[252:255]\n" \
"v_mfma_f32_16x16x32_bf16 v[112:115], a[12:15], a[104:107], v[112:115]\n" \
"v_mfma_f32_16x16x32_bf16 v[116:119], a[12:15], a[120:123], v[116:119]\n" \
"v_mfma_f32_16x16x32_bf16 v[184:187], a[12:15], v[72:75], v[184:187]\n" \
"buffer_load_dwordx4 a[88:91], v32, s[56:59], s65 offen offset:2048 sc1\n" \
"v_mfma_f32_16x16x32_bf16 v[188:191], a[12:15], v[96:99], v[188:191]\n" \
"v_mfma_f32_16x16x32_bf16 v[120:123], a[16:19], a[104:107], v[120:123]\n" \
"v_mfma_f32_16x16x32_bf16 v[124:127], a[16:19], a[120:123], v[124:127]\n" \
"v_mfma_f32_16x16x32_bf16 v[192:195], a[16:19], v[72:75], v[192:195]\n" \
"v_mfma_f32_16x16x32_bf16 v[196:199], a[16:19], v[96:99], v[196:199]\n" \
"buffer_load_dwordx4 v7, s[80:83], s66 offen offset:3072 lds\n" \
"v_mfma_f32_16x16x32_bf16 v[128:131], a[20:23], a[104:107], v[128:131]\n" \
"s_add_u32 m0, s67, 0x2000\n" \
"v_readlane_b32 s67, v11, 48\n" \
"v_mfma_f32_16x16x32_bf16 v[132:135], a[20:23], a[120:123], v[132:135]\n" \
"v_mfma_f32_16x16x32_bf16 v[200:203], a[20:23], v[72:75], v[200:203]\n" \
"v_mfma_f32_16x16x32_bf16 v[204:207], a[20:23], v[96:99], v[204:207]\n" \
"s_barrier\n" \
"v_mfma_f32_16x16x32_bf16 v[136:139], a[24:27], a[104:107], v[136:139]\n" \
"ds_read_b64_tr_b16 a[208:209], v10 offset:32768\n" \
"ds_read_b64_tr_b16 a[210:211], v10 offset:37376\n" \
"v_mfma_f32_16x16x32_bf16 v[140:143], a[24:27], a[120:123], v[140:143]\n" \
"ds_read_b64_tr_b16 a[212:213], v10 offset:32800\n" \
"ds_read_b64_tr_b16 a[214:215], v10 offset:37408\n" \
"v_mfma_f32_16x16x32_bf16 v[208:211], a[24:27], v[72:75], v[208:211]\n" \
"ds_read_b64_tr_b16 a[216:217], v10 offset:32832\n" \
"ds_read_b64_tr_b16 a[218:219], v10 offset:37440\n" \
"v_mfma_f32_16x16x32_bf16 v[212:215], a[24:27], v[96:99], v[212:215]\n" \
"ds_read_b64_tr_b16 a[0:1], v10 offset:32864\n" \
"ds_read_b64_tr_b16 a[2:3], v10 offset:37472\n" \
"v_mfma_f32_16x16x32_bf16 v[144:147], a[28:31], a[104:107], v[144:147]\n" \
"ds_read_b64_tr_b16 a[4:5], v10 offset:32896\n" \
"ds_read_b64_tr_b16 a[6:7], v10 offset:37504\n" \
"v_mfma_f32_16x16x32_bf16 v[148:151], a[28:31], a[120:123], v[148:151]\n" \
"ds_read_b64_tr_b16 a[8:9], v10 offset:32928\n" \
"ds_read_b64_tr_b16 a[10:11], v10 offset:37536\n" \
"v_mfma_f32_16x16x32_bf16 v[216:219], a[28:31], v[72:75], v[216:219]\n" \
"ds_read_b64_tr_b16 a[12:13], v10 offset:32960\n" \
"ds_read_b64_tr_b16 a[14:15], v10 offset:37568\n" \
"v_mfma_f32_16x16x32_bf16 v[220:223], a[28:31], v[96:99], v[220:223]\n" \
"ds_read_b64_tr_b16 a[16:17], v10 offset:32992\n" \
"ds_read_b64_tr_b16 a[18:19], v10 offset:37600\n" \
"v_mfma_f32_16x16x32_bf16 v[152:155], a[192:195], a[104:107], v[152:155]\n" \
"ds_read_b64_tr_b16 a[20:21], v10 offset:33024\n" \
"ds_read_b64_tr_b16 a[22:23], v10 offset:37632\n" \
"v_mfma_f32_16x16x32_bf16 v[156:159], a[192:195], a[120:123], v[156:159]\n" \
"ds_read_b64_tr_b16 a[24:25], v35 offset:41984\n" \
"ds_read_b64_tr_b16 a[26:27], v35 offset:44288\n" \
"v_mfma_f32_16x16x32_bf16 v[224:227], a[192:195], v[72:75], v[224:227]\n" \
"ds_read_b64_tr_b16 a[28:29], v35 offset:42016\n" \
"ds_read_b64_tr_b16 a[30:31], v35 offset:44320\n" \
"v_mfma_f32_16x16x32_bf16 v[228:231], a[192:195], v[96:99], v[228:231]\n" \
"ds_read_b64_tr_b16 a[192:193], v35 offset:42048\n" \
"ds_read_b64_tr_b16 a[194:195], v35 offset:44352\n" \
"v_mfma_f32_16x16x32_bf16 v[160:163], a[196:199], a[104:107], v[160:163]\n" \
"buffer_load_dwordx4 v8, s[80:83], s66 offen offset:0 lds\n" \
"v_mfma_f32_16x16x32_bf16 v[164:167], a[196:199], a[120:123], v[164:167]\n" \
"s_add_u32 m0, s92, 0x0\n" \
"v_readlane_b32 s66, v11, 47\n" \
"v_mfma_f32_16x16x32_bf16 v[232:235], a[196:199], v[72:75], v[232:235]\n" \
"v_mfma_f32_16x16x32_bf16 v[236:239], a[196:199], v[96:99], v[236:239]\n" \
"ds_read_b64_tr_b16 a[196:197], v35 offset:42080\n" \
"ds_read_b64_tr_b16 a[198:199], v35 offset:44384\n" \
"v_mfma_f32_16x16x32_bf16 v[168:171], a[200:203], a[104:107], v[168:171]\n" \
"v_mfma_f32_16x16x32_bf16 v[172:175], a[200:203], a[120:123], v[172:175]\n" \
"buffer_load_dwordx4 v0, s[80:83], s73 offen offset:0 lds\n" \
"v_mfma_f32_16x16x32_bf16 v[240:243], a[200:203], v[72:75], v[240:243]\n" \
"v_mfma_f32_16x16x32_bf16 v[244:247], a[200:203], v[96:99], v[244:247]\n" \
"ds_read_b64_tr_b16 a[200:201], v35 offset:42112\n" \
"ds_read_b64_tr_b16 a[202:203], v35 offset:44416\n" \
"v_mfma_f32_16x16x32_bf16 v[176:179], a[204:207], a[104:107], v[176:179]\n" \
"v_mfma_f32_16x16x32_bf16 v[180:183], a[204:207], a[120:123], v[180:183]\n" \
"v_mfma_f32_16x16x32_bf16 v[248:251], a[204:207], v[72:75], v[248:251]\n" \
"buffer_load_dwordx4 v1, s[80:83], s73 offen offset:1024 lds\n" \
"v_mfma_f32_16x16x32_bf16 v[252:255], a[204:207], v[96:99], v[252:255]\n" \
"ds_read_b64_tr_b16 a[204:205], v35 offset:42144\n" \
"s_waitcnt lgkmcnt(9)\n" \
"v_mfma_f32_16x16x32_bf16 v[112:115], a[208:211], a[108:111], v[112:115]\n" \
"ds_read_b64_tr_b16 a[206:207], v35 offset:44448\n" \
"v_mfma_f32_16x16x32_bf16 v[116:119], a[208:211], a[124:127], v[116:119]\n" \
"v_mfma_f32_16x16x32_bf16 v[184:187], a[208:211], v[76:79], v[184:187]\n" \
"v_mfma_f32_16x16x32_bf16 v[188:191], a[208:211], v[100:103], v[188:191]\n" \
"ds_read_b64_tr_b16 a[208:209], v35 offset:42176\n" \
"ds_read_b64_tr_b16 a[210:211], v35 offset:44480\n" \
"v_mfma_f32_16x16x32_bf16 v[120:123], a[212:215], a[108:111], v[120:123]\n" \
"buffer_load_dwordx4 v2, s[80:83], s73 offen offset:2048 lds\n" \
"v_mfma_f32_16x16x32_bf16 v[124:127], a[212:215], a[124:127], v[124:127]\n" \
"v_mfma_f32_16x16x32_bf16 v[192:195], a[212:215], v[76:79], v[192:195]\n" \
"s_waitcnt vmcnt(18)\n" \
"s_mov_b64 exec, 1\n" \
"buffer_store_dword v39, off, s[56:59], s70 offset:0 sc1\n" \
"s_mov_b64 exec, -1\n" \
"v_mfma_f32_16x16x32_bf16 v[196:199], a[212:215], v[100:103], v[196:199]\n" \
"ds_read_b64_tr_b16 a[212:213], v35 offset:42208\n" \
"ds_read_b64_tr_b16 a[214:215], v35 offset:44512\n" \
"v_mfma_f32_16x16x32_bf16 v[128:131], a[216:219], a[108:111], v[128:131]\n" \
"v_readlane_b32 s70, v11, 51\n" \
"v_mfma_f32_16x16x32_bf16 v[132:135], a[216:219], a[124:127], v[132:135]\n" \
"v_mfma_f32_16x16x32_bf16 v[200:203], a[216:219], v[76:79], v[200:203]\n" \
"buffer_load_dwordx4 v3, s[80:83], s73 offen offset:3072 lds\n" \
"v_mfma_f32_16x16x32_bf16 v[204:207], a[216:219], v[100:103], v[204:207]\n" \
"ds_read_b64_tr_b16 a[216:217], v35 offset:42240\n" \
"ds_read_b64_tr_b16 a[218:219], v35 offset:44544\n" \
"v_mfma_f32_16x16x32_bf16 v[136:139], a[0:3], a[108:111], v[136:139]\n" \
"s_add_u32 m0, s92, 0x1000\n" \
"v_mfma_f32_16x16x32_bf16 v[140:143], a[0:3], a[124:127], v[140:143]\n" \
"v_mfma_f32_16x16x32_bf16 v[208:211], a[0:3], v[76:79], v[208:211]\n" \
"v_mfma_f32_16x16x32_bf16 v[212:215], a[0:3], v[100:103], v[212:215]\n" \
"ds_read_b64_tr_b16 a[0:1], v35 offset:52352\n" \
"ds_read_b64_tr_b16 a[2:3], v35 offset:54656\n" \
"v_mfma_f32_16x16x32_bf16 v[144:147], a[4:7], a[108:111], v[144:147]\n" \
"buffer_load_dwordx4 v4, s[80:83], s73 offen offset:0 lds\n" \
"v_mfma_f32_16x16x32_bf16 v[148:151], a[4:7], a[124:127], v[148:151]\n" \
"v_mfma_f32_16x16x32_bf16 v[216:219], a[4:7], v[76:79], v[216:219]\n" \
"v_mfma_f32_16x16x32_bf16 v[220:223], a[4:7], v[100:103], v[220:223]\n" \
"ds_read_b64_tr_b16 a[4:5], v35 offset:52384\n" \
"ds_read_b64_tr_b16 a[6:7], v35 offset:54688\n" \
"v_mfma_f32_16x16x32_bf16 v[152:155], a[8:11], a[108:111], v[152:155]\n" \
"v_mfma_f32_16x16x32_bf16 v[156:159], a[8:11], a[124:127], v[156:159]\n" \
"buffer_load_dwordx4 v5, s[80:83], s73 offen offset:1024 lds\n" \
"v_mfma_f32_16x16x32_bf16 v[224:227], a[8:11], v[76:79], v[224:227]\n" \
"v_mfma_f32_16x16x32_bf16 v[228:231], a[8:11], v[100:103], v[228:231]\n" \
"ds_read_b64_tr_b16 a[8:9], v35 offset:52416\n" \
"ds_read_b64_tr_b16 a[10:11], v35 offset:54720\n" \
"v_mfma_f32_16x16x32_bf16 v[160:163], a[12:15], a[108:111], v[160:163]\n" \
"v_mfma_f32_16x16x32_bf16 v[164:167], a[12:15], a[124:127], v[164:167]\n" \
"v_mfma_f32_16x16x32_bf16 v[232:235], a[12:15], v[76:79], v[232:235]\n" \
"buffer_load_dwordx4 v6, s[80:83], s73 offen offset:2048 lds\n" \
"v_mfma_f32_16x16x32_bf16 v[236:239], a[12:15], v[100:103], v[236:239]\n" \
"ds_read_b64_tr_b16 a[12:13], v35 offset:52448\n" \
"ds_read_b64_tr_b16 a[14:15], v35 offset:54752\n" \
"v_mfma_f32_16x16x32_bf16 v[168:171], a[16:19], a[108:111], v[168:171]\n" \
"v_mfma_f32_16x16x32_bf16 v[172:175], a[16:19], a[124:127], v[172:175]\n" \
"v_mfma_f32_16x16x32_bf16 v[240:243], a[16:19], v[76:79], v[240:243]\n" \
"v_mfma_f32_16x16x32_bf16 v[244:247], a[16:19], v[100:103], v[244:247]\n" \
"buffer_load_dwordx4 v7, s[80:83], s73 offen offset:3072 lds\n" \
"v_mfma_f32_16x16x32_bf16 v[176:179], a[20:23], a[108:111], v[176:179]\n" \
"s_add_u32 m0, s92, 0x2000\n" \
"ds_read_b64_tr_b16 a[16:17], v35 offset:52480\n" \
"v_mfma_f32_16x16x32_bf16 v[180:183], a[20:23], a[124:127], v[180:183]\n" \
"ds_read_b64_tr_b16 a[18:19], v35 offset:54784\n" \
"v_readlane_b32 s92, v11, 46\n" \
"v_mfma_f32_16x16x32_bf16 v[248:251], a[20:23], v[76:79], v[248:251]\n" \
"v_mfma_f32_16x16x32_bf16 v[252:255], a[20:23], v[100:103], v[252:255]\n" \
"ds_read_b64_tr_b16 a[20:21], v35 offset:52512\n" \
"ds_read_b64_tr_b16 a[22:23], v35 offset:54816\n" \
"v_mfma_f32_16x16x32_bf16 v[112:115], a[24:27], a[220:223], v[112:115]\n" \
"buffer_load_dwordx4 v8, s[80:83], s73 offen offset:0 lds\n" \
"v_mfma_f32_16x16x32_bf16 v[116:119], a[24:27], a[228:231], v[116:119]\n" \
"s_mov_b32 m0, s94\n" \
"v_readlane_b32 s94, v11, 48\n" \
"v_mfma_f32_16x16x32_bf16 v[184:187], a[24:27], a[236:239], v[184:187]\n" \
"v_readlane_b32 s73, v11, 6\n" \
"v_mfma_f32_16x16x32_bf16 v[188:191], a[24:27], a[244:247], v[188:191]\n" \
"ds_read_b64_tr_b16 a[24:25], v35 offset:52544\n" \
"s_waitcnt lgkmcnt(7)\n" \
"v_mfma_f32_16x16x32_bf16 v[120:123], a[28:31], a[220:223], v[120:123]\n" \
"ds_read_b64_tr_b16 a[26:27], v35 offset:54848\n" \
"buffer_load_dword v47, v38, s[56:59], s71 offen sc1\n" \
"v_mfma_f32_16x16x32_bf16 v[124:127], a[28:31], a[228:231], v[124:127]\n" \
"v_mfma_f32_16x16x32_bf16 v[192:195], a[28:31], a[236:239], v[192:195]\n" \
"v_mfma_f32_16x16x32_bf16 v[196:199], a[28:31], a[244:247], v[196:199]\n" \
"ds_read_b64_tr_b16 a[28:29], v35 offset:52576\n" \
"ds_read_b64_tr_b16 a[30:31], v35 offset:54880\n" \
"v_mfma_f32_16x16x32_bf16 v[128:131], a[192:195], a[220:223], v[128:131]\n" \
"v_mfma_f32_16x16x32_bf16 v[132:135], a[192:195], a[228:231], v[132:135]\n" \
"buffer_load_dwordx4 v9, s[84:87], 0 offen lds\n" \
"v_mfma_f32_16x16x32_bf16 v[200:203], a[192:195], a[236:239], v[200:203]\n" \
"v_mfma_f32_16x16x32_bf16 v[204:207], a[192:195], a[244:247], v[204:207]\n" \
"ds_read_b64_tr_b16 a[192:193], v35 offset:52608\n" \
"ds_read_b64_tr_b16 a[194:195], v35 offset:54912\n" \
"v_mfma_f32_16x16x32_bf16 v[136:139], a[196:199], a[220:223], v[136:139]\n" \
"v_mfma_f32_16x16x32_bf16 v[140:143], a[196:199], a[228:231], v[140:143]\n" \
"v_mfma_f32_16x16x32_bf16 v[208:211], a[196:199], a[236:239], v[208:211]\n" \
"buffer_load_dwordx4 a[76:79], v20, s[56:59], s65 offen offset:3072 sc1\n" \
"v_mfma_f32_16x16x32_bf16 v[212:215], a[196:199], a[244:247], v[212:215]\n" \
"v_mfma_f32_16x16x32_bf16 v[144:147], a[200:203], a[220:223], v[144:147]\n" \
"v_mfma_f32_16x16x32_bf16 v[148:151], a[200:203], a[228:231], v[148:151]\n" \
"v_mfma_f32_16x16x32_bf16 v[216:219], a[200:203], a[236:239], v[216:219]\n" \
"v_mfma_f32_16x16x32_bf16 v[220:223], a[200:203], a[244:247], v[220:223]\n" \
"buffer_load_dwordx4 a[92:95], v32, s[56:59], s65 offen offset:3072 sc1\n" \
"v_mfma_f32_16x16x32_bf16 v[152:155], a[204:207], a[220:223], v[152:155]\n" \
"v_readlane_b32 s65, v11, 46\n" \
"v_mfma_f32_16x16x32_bf16 v[156:159], a[204:207], a[228:231], v[156:159]\n" \
"v_mfma_f32_16x16x32_bf16 v[224:227], a[204:207], a[236:239], v[224:227]\n" \
"v_mfma_f32_16x16x32_bf16 v[228:231], a[204:207], a[244:247], v[228:231]\n" \
"v_mfma_f32_16x16x32_bf16 v[160:163], a[208:211], a[220:223], v[160:163]\n" \
"buffer_load_dwordx4 a[128:131], v20, s[56:59], s68 offen offset:0 sc1\n" \
"v_mfma_f32_16x16x32_bf16 v[164:167], a[208:211], a[228:231], v[164:167]\n" \
"v_mfma_f32_16x16x32_bf16 v[232:235], a[208:211], a[236:239], v[232:235]\n" \
"v_mfma_f32_16x16x32_bf16 v[236:239], a[208:211], a[244:247], v[236:239]\n" \
"v_mfma_f32_16x16x32_bf16 v[168:171], a[212:215], a[220:223], v[168:171]\n" \
"v_mfma_f32_16x16x32_bf16 v[172:175], a[212:215], a[228:231], v[172:175]\n" \
"buffer_load_dwordx4 a[144:147], v32, s[56:59], s68 offen offset:0 sc1\n" \
"v_mfma_f32_16x16x32_bf16 v[240:243], a[212:215], a[236:239], v[240:243]\n" \
"v_mfma_f32_16x16x32_bf16 v[244:247], a[212:215], a[244:247], v[244:247]\n" \
"v_mfma_f32_16x16x32_bf16 v[176:179], a[216:219], a[220:223], v[176:179]\n" \
"v_mfma_f32_16x16x32_bf16 v[180:183], a[216:219], a[228:231], v[180:183]\n" \
"v_mfma_f32_16x16x32_bf16 v[248:251], a[216:219], a[236:239], v[248:251]\n" \
"buffer_load_dwordx4 a[132:135], v20, s[56:59], s68 offen offset:1024 sc1\n" \
"v_mfma_f32_16x16x32_bf16 v[252:255], a[216:219], a[244:247], v[252:255]\n" \
"v_mfma_f32_16x16x32_bf16 v[112:115], a[0:3], a[224:227], v[112:115]\n" \
"v_mfma_f32_16x16x32_bf16 v[116:119], a[0:3], a[232:235], v[116:119]\n" \
"v_mfma_f32_16x16x32_bf16 v[184:187], a[0:3], a[240:243], v[184:187]\n" \
"v_mfma_f32_16x16x32_bf16 v[188:191], a[0:3], a[248:251], v[188:191]\n" \
"buffer_load_dwordx4 a[148:151], v32, s[56:59], s68 offen offset:1024 sc1\n" \
"v_mfma_f32_16x16x32_bf16 v[120:123], a[4:7], a[224:227], v[120:123]\n" \
"v_mfma_f32_16x16x32_bf16 v[124:127], a[4:7], a[232:235], v[124:127]\n" \
"v_mfma_f32_16x16x32_bf16 v[192:195], a[4:7], a[240:243], v[192:195]\n" \
"v_mfma_f32_16x16x32_bf16 v[196:199], a[4:7], a[248:251], v[196:199]\n" \
"v_mfma_f32_16x16x32_bf16 v[128:131], a[8:11], a[224:227], v[128:131]\n" \
"buffer_load_dwordx4 a[136:139], v20, s[56:59], s68 offen offset:2048 sc1\n" \
"v_mfma_f32_16x16x32_bf16 v[132:135], a[8:11], a[232:235], v[132:135]\n" \
"v_mfma_f32_16x16x32_bf16 v[200:203], a[8:11], a[240:243], v[200:203]\n" \
"v_mfma_f32_16x16x32_bf16 v[204:207], a[8:11], a[248:251], v[204:207]\n" \
"s_waitcnt lgkmcnt(0)\n" \
"v_mfma_f32_16x16x32_bf16 v[136:139], a[12:15], a[224:227], v[136:139]\n" \
"v_mfma_f32_16x16x32_bf16 v[140:143], a[12:15], a[232:235], v[140:143]\n" \
"buffer_load_dwordx4 a[152:155], v32, s[56:59], s68 offen offset:2048 sc1\n" \
"v_mfma_f32_16x16x32_bf16 v[208:211], a[12:15], a[240:243], v[208:211]\n" \
"v_mfma_f32_16x16x32_bf16 v[212:215], a[12:15], a[248:251], v[212:215]\n" \
"s_barrier\n" \
"v_mfma_f32_16x16x32_bf16 v[144:147], a[16:19], a[224:227], v[144:147]\n" \
"ds_read_b64_tr_b16 a[196:197], v33 offset:0\n" \
"ds_read_b64_tr_b16 a[198:199], v33 offset:4608\n" \
"v_mfma_f32_16x16x32_bf16 v[148:151], a[16:19], a[232:235], v[148:151]\n" \
"buffer_load_dwordx4 a[140:143], v20, s[56:59], s68 offen offset:3072 sc1\n" \
"v_mfma_f32_16x16x32_bf16 v[216:219], a[16:19], a[240:243], v[216:219]\n" \
"ds_read_b64_tr_b16 a[200:201], v33 offset:32\n" \
"ds_read_b64_tr_b16 a[202:203], v33 offset:4640\n" \
"v_mfma_f32_16x16x32_bf16 v[220:223], a[16:19], a[248:251], v[220:223]\n" \
"ds_read_b64_tr_b16 a[204:205], v33 offset:64\n" \
"ds_read_b64_tr_b16 a[206:207], v33 offset:4672\n" \
"v_mfma_f32_16x16x32_bf16 v[152:155], a[20:23], a[224:227], v[152:155]\n" \
"ds_read_b64_tr_b16 a[208:209], v33 offset:96\n" \
"ds_read_b64_tr_b16 a[210:211], v33 offset:4704\n" \
"v_mfma_f32_16x16x32_bf16 v[156:159], a[20:23], a[232:235], v[156:159]\n" \
"ds_read_b64_tr_b16 a[212:213], v33 offset:128\n" \
"ds_read_b64_tr_b16 a[214:215], v33 offset:4736\n" \
"v_mfma_f32_16x16x32_bf16 v[224:227], a[20:23], a[240:243], v[224:227]\n" \
"buffer_load_dwordx4 a[156:159], v32, s[56:59], s68 offen offset:3072 sc1\n" \
"v_mfma_f32_16x16x32_bf16 v[228:231], a[20:23], a[248:251], v[228:231]\n" \
"ds_read_b64_tr_b16 a[216:217], v33 offset:160\n" \
"ds_read_b64_tr_b16 a[218:219], v33 offset:4768\n" \
"v_mfma_f32_16x16x32_bf16 v[160:163], a[24:27], a[224:227], v[160:163]\n" \
"ds_read_b64_tr_b16 a[0:1], v33 offset:192\n" \
"ds_read_b64_tr_b16 a[2:3], v33 offset:4800\n" \
"v_mfma_f32_16x16x32_bf16 v[164:167], a[24:27], a[232:235], v[164:167]\n" \
"ds_read_b64_tr_b16 a[4:5], v33 offset:224\n" \
"ds_read_b64_tr_b16 a[6:7], v33 offset:4832\n" \
"v_mfma_f32_16x16x32_bf16 v[232:235], a[24:27], a[240:243], v[232:235]\n" \
"ds_read_b64_tr_b16 a[8:9], v33 offset:256\n" \
"ds_read_b64_tr_b16 a[10:11], v33 offset:4864\n" \
"buffer_load_dwordx4 a[32:35], v20, s[56:59], s69 offen offset:0 sc1\n" \
"v_mfma_f32_16x16x32_bf16 v[236:239], a[24:27], a[248:251], v[236:239]\n" \
"ds_read_b64_tr_b16 a[12:13], v33 offset:9216\n" \
"ds_read_b64_tr_b16 a[14:15], v33 offset:13824\n" \
"v_mfma_f32_16x16x32_bf16 v[168:171], a[28:31], a[224:227], v[168:171]\n" \
"ds_read_b64_tr_b16 a[16:17], v33 offset:9248\n" \
"ds_read_b64_tr_b16 a[18:19], v33 offset:13856\n" \
"v_mfma_f32_16x16x32_bf16 v[172:175], a[28:31], a[232:235], v[172:175]\n" \
"ds_read_b64_tr_b16 a[20:21], v33 offset:9280\n" \
"ds_read_b64_tr_b16 a[22:23], v33 offset:13888\n" \
"v_mfma_f32_16x16x32_bf16 v[240:243], a[28:31], a[240:243], v[240:243]\n" \
"ds_read_b64_tr_b16 a[24:25], v33 offset:9312\n" \
"ds_read_b64_tr_b16 a[26:27], v33 offset:13920\n" \
"buffer_load_dwordx4 a[48:51], v32, s[56:59], s69 offen offset:0 sc1\n" \
"v_mfma_f32_16x16x32_bf16 v[244:247], a[28:31], a[248:251], v[244:247]\n" \
"ds_read_b64_tr_b16 a[28:29], v33 offset:9344\n" \
"ds_read_b64_tr_b16 a[30:31], v33 offset:13952\n" \
"v_mfma_f32_16x16x32_bf16 v[176:179], a[192:195], a[224:227], v[176:179]\n" \
"v_readlane_b32 s68, v11, 49\n" \
"s_waitcnt vmcnt(13)\n" \
"v_cmp_ne_u32_e32 vcc, s62, v47\n" \
"v_mfma_f32_16x16x32_bf16 v[180:183], a[192:195], a[232:235], v[180:183]\n" \
"v_mfma_f32_16x16x32_bf16 v[248:251], a[192:195], a[240:243], v[248:251]\n" \
"buffer_load_dwordx4 a[36:39], v20, s[56:59], s69 offen offset:1024 sc1\n" \
"v_mfma_f32_16x16x32_bf16 v[252:255], a[192:195], a[248:251], v[252:255]\n" \
"ds_read_b64_tr_b16 a[192:193], v33 offset:9376\n" \
"s_waitcnt lgkmcnt(15)\n" \
"v_mfma_f32_16x16x32_bf16 v[112:115], a[196:199], a[64:67], v[112:115]\n" \
"ds_read_b64_tr_b16 a[194:195], v33 offset:13984\n" \
"s_cbranch_vccnz L_sb_%=\n" \
"L_rb_%=:\n" \
"v_mfma_f32_16x16x32_bf16 v[116:119], a[196:199], a[80:83], v[116:119]\n" \
"ds_read_b64_tr_b16 a[196:197], v33 offset:9408\n" \
"ds_read_b64_tr_b16 a[198:199], v33 offset:14016\n" \
"v_mfma_f32_16x16x32_bf16 v[120:123], a[200:203], a[64:67], v[120:123]\n" \
"v_readlane_b32 s71, v11, 52\n" \
"v_cmp_eq_u32_e32 vcc, 0, v44\n" \
"buffer_load_dwordx4 a[52:55], v32, s[56:59], s69 offen offset:1024 sc1\n" \
"v_mfma_f32_16x16x32_bf16 v[124:127], a[200:203], a[80:83], v[124:127]\n" \
"ds_read_b64_tr_b16 a[200:201], v33 offset:9440\n" \
"ds_read_b64_tr_b16 a[202:203], v33 offset:14048\n" \
"v_mfma_f32_16x16x32_bf16 v[128:131], a[204:207], a[64:67], v[128:131]\n" \
"v_cndmask_b32_e32 v50, 0, v45, vcc\n" \
"v_cmp_eq_u32_e32 vcc, 1, v44\n" \
"v_mfma_f32_16x16x32_bf16 v[132:135], a[204:207], a[80:83], v[132:135]\n" \
"ds_read_b64_tr_b16 a[204:205], v33 offset:9472\n" \
"ds_read_b64_tr_b16 a[206:207], v33 offset:14080\n" \
"v_mfma_f32_16x16x32_bf16 v[136:139], a[208:211], a[64:67], v[136:139]\n" \
"v_cndmask_b32_e32 v51, 0, v45, vcc\n" \
"v_bfe_u32 v45, v20, 1, 5\n" \
"buffer_load_dwordx4 a[40:43], v20, s[56:59], s69 offen offset:2048 sc1\n" \
"v_mfma_f32_16x16x32_bf16 v[140:143], a[208:211], a[80:83], v[140:143]\n" \
"ds_read_b64_tr_b16 a[208:209], v33 offset:18432\n" \
"ds_read_b64_tr_b16 a[210:211], v33 offset:23040\n" \
"v_mfma_f32_16x16x32_bf16 v[144:147], a[212:215], a[64:67], v[144:147]\n" \
"v_and_b32_e32 v45, 24, v45\n" \
"v_and_b32_e32 v44, 0x3c0, v20\n" \
"v_mfma_f32_16x16x32_bf16 v[148:151], a[212:215], a[80:83], v[148:151]\n" \
"ds_read_b64_tr_b16 a[212:213], v33 offset:18464\n" \
"ds_read_b64_tr_b16 a[214:215], v33 offset:23072\n" \
"v_mfma_f32_16x16x32_bf16 v[152:155], a[216:219], a[64:67], v[152:155]\n" \
"v_add_u32_e32 v45, 0x4800, v45\n" \
"v_add_u32_e32 v36, v44, v45\n" \
"buffer_load_dwordx4 a[56:59], v32, s[56:59], s69 offen offset:2048 sc1\n" \
"v_mfma_f32_16x16x32_bf16 v[156:159], a[216:219], a[80:83], v[156:159]\n" \
"ds_read_b64_tr_b16 a[216:217], v33 offset:18496\n" \
"ds_read_b64_tr_b16 a[218:219], v33 offset:23104\n" \
"v_mfma_f32_16x16x32_bf16 v[160:163], a[0:3], a[64:67], v[160:163]\n" \
".fill (L_ab + 36864 - .) / 4, 4, 0xBF800000\n" \
"L_ap9:\n" \
"s_cbranch_execz L_ap0\n" \
"v_mfma_f32_16x16x32_bf16 v[164:167], a[0:3], a[80:83], v[164:167]\n" \
"ds_read_b64_tr_b16 a[0:1], v33 offset:18528\n" \
"s_waitcnt lgkmcnt(13)\n" \
"v_mfma_f32_16x16x32_bf16 v[168:171], a[4:7], a[64:67], v[168:171]\n" \
"ds_read_b64_tr_b16 a[2:3], v33 offset:23136\n" \
"buffer_load_dwordx4 a[44:47], v20, s[56:59], s69 offen offset:3072 sc1\n" \
"v_mfma_f32_16x16x32_bf16 v[172:175], a[4:7], a[80:83], v[172:175]\n" \
"ds_read_b64_tr_b16 a[4:5], v33 offset:18560\n" \
"ds_read_b64_tr_b16 a[6:7], v33 offset:23168\n" \
"v_mfma_f32_16x16x32_bf16 v[176:179], a[8:11], a[64:67], v[176:179]\n" \
"v_mfma_f32_16x16x32_bf16 v[180:183], a[8:11], a[80:83], v[180:183]\n" \
"ds_read_b64_tr_b16 a[8:9], v33 offset:18592\n" \
"ds_read_b64_tr_b16 a[10:11], v33 offset:23200\n" \
"v_mfma_f32_16x16x32_bf16 v[112:115], a[12:15], a[68:71], v[112:115]\n" \
"v_mfma_f32_16x16x32_bf16 v[116:119], a[12:15], a[84:87], v[116:119]\n" \
"buffer_load_dwordx4 a[60:63], v32, s[56:59], s69 offen offset:3072 sc1\n" \
"v_mfma_f32_16x16x32_bf16 v[120:123], a[16:19], a[68:71], v[120:123]\n" \
"ds_read_b64_tr_b16 a[12:13], v33 offset:18624\n" \
"ds_read_b64_tr_b16 a[14:15], v33 offset:23232\n" \
"v_mfma_f32_16x16x32_bf16 v[124:127], a[16:19], a[84:87], v[124:127]\n" \
"ds_read_b64_tr_b16 a[16:17], v33 offset:18656\n" \
"ds_read_b64_tr_b16 a[18:19], v33 offset:23264\n" \
"v_mfma_f32_16x16x32_bf16 v[128:131], a[20:23], a[68:71], v[128:131]\n" \
"v_readlane_b32 s69, v11, 50\n" \
"v_mfma_f32_16x16x32_bf16 v[132:135], a[20:23], a[84:87], v[132:135]\n" \
"ds_read_b64_tr_b16 a[20:21], v33 offset:18688\n" \
"ds_read_b64_tr_b16 a[22:23], v33 offset:23296\n" \
"v_mfma_f32_16x16x32_bf16 v[136:139], a[24:27], a[68:71], v[136:139]\n" \
"buffer_load_dwordx4 a[220:223], v20, s[56:59], s64 offen offset:0 sc1\n" \
"v_mfma_f32_16x16x32_bf16 v[140:143], a[24:27], a[84:87], v[140:143]\n" \
"ds_read_b64_tr_b16 a[24:25], v33 offset:27648\n" \
"ds_read_b64_tr_b16 a[26:27], v33 offset:32256\n" \
"v_mfma_f32_16x16x32_bf16 v[144:147], a[28:31], a[68:71], v[144:147]\n" \
"v_mfma_f32_16x16x32_bf16 v[148:151], a[28:31], a[84:87], v[148:151]\n" \
"ds_read_b64_tr_b16 a[28:29], v33 offset:27680\n" \
"ds_read_b64_tr_b16 a[30:31], v33 offset:32288\n" \
"v_mfma_f32_16x16x32_bf16 v[152:155], a[192:195], a[68:71], v[152:155]\n" \
"v_mfma_f32_16x16x32_bf16 v[156:159], a[192:195], a[84:87], v[156:159]\n" \
"ds_read_b64_tr_b16 a[192:193], v33 offset:27712\n" \
"s_waitcnt lgkmcnt(13)\n" \
"v_mfma_f32_16x16x32_bf16 v[160:163], a[196:199], a[68:71], v[160:163]\n" \
"buffer_load_dwordx4 a[236:239], v32, s[56:59], s64 offen offset:0 sc1\n" \
"v_mfma_f32_16x16x32_bf16 v[164:167], a[196:199], a[84:87], v[164:167]\n" \
"ds_read_b64_tr_b16 a[194:195], v33 offset:32320\n" \
"ds_read_b64_tr_b16 a[196:197], v33 offset:27744\n" \
"v_mfma_f32_16x16x32_bf16 v[168:171], a[200:203], a[68:71], v[168:171]\n" \
"ds_read_b64_tr_b16 a[198:199], v33 offset:32352\n" \
"v_mfma_f32_16x16x32_bf16 v[172:175], a[200:203], a[84:87], v[172:175]\n" \
"ds_read_b64_tr_b16 a[200:201], v33 offset:27776\n" \
"ds_read_b64_tr_b16 a[202:203], v33 offset:32384\n" \
"v_mfma_f32_16x16x32_bf16 v[176:179], a[204:207], a[68:71], v[176:179]\n" \
"v_mfma_f32_16x16x32_bf16 v[180:183], a[204:207], a[84:87], v[180:183]\n" \
"ds_read_b64_tr_b16 a[204:205], v33 offset:27808\n" \
"ds_read_b64_tr_b16 a[206:207], v33 offset:32416\n" \
"v_mfma_f32_16x16x32_bf16 v[112:115], a[208:211], a[72:75], v[112:115]\n" \
"buffer_load_dwordx4 a[224:227], v20, s[56:59], s64 offen offset:1024 sc1\n" \
"v_mfma_f32_16x16x32_bf16 v[116:119], a[208:211], a[88:91], v[116:119]\n" \
"ds_read_b64_tr_b16 a[208:209], v33 offset:27840\n" \
"ds_read_b64_tr_b16 a[210:211], v33 offset:32448\n" \
"v_mfma_f32_16x16x32_bf16 v[120:123], a[212:215], a[72:75], v[120:123]\n" \
"v_mfma_f32_16x16x32_bf16 v[124:127], a[212:215], a[88:91], v[124:127]\n" \
"ds_read_b64_tr_b16 a[212:213], v33 offset:27872\n" \
"ds_read_b64_tr_b16 a[214:215], v33 offset:32480\n" \
"v_mfma_f32_16x16x32_bf16 v[128:131], a[216:219], a[72:75], v[128:131]\n" \
"v_mfma_f32_16x16x32_bf16 v[132:135], a[216:219], a[88:91], v[132:135]\n" \
"ds_read_b64_tr_b16 a[216:217], v33 offset:27904\n" \
"ds_read_b64_tr_b16 a[218:219], v33 offset:32512\n" \
"v_mfma_f32_16x16x32_bf16 v[136:139], a[0:3], a[72:75], v[136:139]\n" \
"buffer_load_dwordx4 a[240:243], v32, s[56:59], s64 offen offset:1024 sc1\n" \
"v_mfma_f32_16x16x32_bf16 v[140:143], a[0:3], a[88:91], v[140:143]\n" \
"v_mfma_f32_16x16x32_bf16 v[144:147], a[4:7], a[72:75], v[144:147]\n" \
"v_mfma_f32_16x16x32_bf16 v[148:151], a[4:7], a[88:91], v[148:151]\n" \
"s_waitcnt lgkmcnt(12)\n" \
"v_mfma_f32_16x16x32_bf16 v[152:155], a[8:11], a[72:75], v[152:155]\n" \
"v_mfma_f32_16x16x32_bf16 v[156:159], a[8:11], a[88:91], v[156:159]\n" \
"buffer_load_dwordx4 a[228:231], v20, s[56:59], s64 offen offset:2048 sc1\n" \
"v_mfma_f32_16x16x32_bf16 v[160:163], a[12:15], a[72:75], v[160:163]\n" \
"v_mfma_f32_16x16x32_bf16 v[164:167], a[12:15], a[88:91], v[164:167]\n" \
"v_mfma_f32_16x16x32_bf16 v[168:171], a[16:19], a[72:75], v[168:171]\n" \
"v_mfma_f32_16x16x32_bf16 v[172:175], a[16:19], a[88:91], v[172:175]\n" \
"v_mfma_f32_16x16x32_bf16 v[176:179], a[20:23], a[72:75], v[176:179]\n" \
"buffer_load_dwordx4 a[244:247], v32, s[56:59], s64 offen offset:2048 sc1\n" \
"v_mfma_f32_16x16x32_bf16 v[180:183], a[20:23], a[88:91], v[180:183]\n" \
"s_waitcnt vmcnt(24)\n" \
"s_barrier\n" \
"s_waitcnt vmcnt(23)\n" \
"v_mfma_f32_16x16x32_bf16 v[112:115], a[24:27], a[76:79], v[112:115]\n" \
"ds_read_b64_tr_b16 a[0:1], v33 offset:36864\n" \
"s_waitcnt vmcnt(22)\n" \
"v_mfma_f32_16x16x32_bf16 v[116:119], a[24:27], a[92:95], v[116:119]\n" \
"ds_read_b64_tr_b16 a[2:3], v33 offset:41472\n" \
"ds_read_b64_tr_b16 a[4:5], v33 offset:46080\n" \
"v_mfma_f32_16x16x32_bf16 v[120:123], a[28:31], a[76:79], v[120:123]\n" \
"ds_read_b64_tr_b16 a[6:7], v33 offset:50688\n" \
"ds_read_b64_tr_b16 a[8:9], v33 offset:55296\n" \
"v_mfma_f32_16x16x32_bf16 v[124:127], a[28:31], a[92:95], v[124:127]\n" \
"ds_read_b64_tr_b16 a[10:11], v33 offset:59904\n" \
"ds_read_b64_tr_b16 a[12:13], v10 offset:0\n" \
"v_mfma_f32_16x16x32_bf16 v[128:131], a[192:195], a[76:79], v[128:131]\n" \
"ds_read_b64_tr_b16 a[14:15], v10 offset:4608\n" \
"ds_read_b64_tr_b16 a[16:17], v34 offset:0\n" \
"v_mfma_f32_16x16x32_bf16 v[132:135], a[192:195], a[92:95], v[132:135]\n" \
"ds_read_b64_tr_b16 a[18:19], v34 offset:4608\n" \
"s_waitcnt lgkmcnt(10)\n" \
"v_mfma_f32_16x16x32_bf16 v[136:139], a[196:199], a[76:79], v[136:139]\n" \
"ds_read_b64_tr_b16 a[20:21], v34 offset:9216\n" \
"ds_read_b64_tr_b16 a[22:23], v34 offset:13824\n" \
"v_mfma_f32_16x16x32_bf16 v[140:143], a[196:199], a[92:95], v[140:143]\n" \
"ds_read_b64_tr_b16 a[24:25], v34 offset:18432\n" \
"ds_read_b64_tr_b16 a[26:27], v34 offset:23040\n" \
"v_mfma_f32_16x16x32_bf16 v[144:147], a[200:203], a[76:79], v[144:147]\n" \
"ds_read_b64_tr_b16 a[28:29], v10 offset:9216\n" \
"ds_read_b64_tr_b16 a[30:31], v10 offset:13824\n" \
"v_mfma_f32_16x16x32_bf16 v[148:151], a[200:203], a[92:95], v[148:151]\n" \
"ds_read_b64_tr_b16 a[192:193], v33 offset:36896\n" \
"ds_read_b64_tr_b16 a[194:195], v33 offset:41504\n" \
"v_mfma_f32_16x16x32_bf16 v[152:155], a[204:207], a[76:79], v[152:155]\n" \
"ds_read_b64_tr_b16 a[196:197], v33 offset:46112\n" \
"ds_read_b64_tr_b16 a[198:199], v33 offset:50720\n" \
"v_mfma_f32_16x16x32_bf16 v[156:159], a[204:207], a[92:95], v[156:159]\n" \
"ds_read_b64_tr_b16 a[200:201], v33 offset:55328\n" \
"ds_read_b64_tr_b16 a[202:203], v33 offset:59936\n" \
"v_mfma_f32_16x16x32_bf16 v[160:163], a[208:211], a[76:79], v[160:163]\n" \
"ds_read_b64_tr_b16 a[204:205], v10 offset:32\n" \
"ds_read_b64_tr_b16 a[206:207], v10 offset:4640\n" \
"v_mfma_f32_16x16x32_bf16 v[164:167], a[208:211], a[92:95], v[164:167]\n" \
"ds_read_b64_tr_b16 a[208:209], v34 offset:32\n" \
"ds_read_b64_tr_b16 a[210:211], v34 offset:4640\n" \
"v_mfma_f32_16x16x32_bf16 v[168:171], a[212:215], a[76:79], v[168:171]\n" \
"buffer_load_dwordx4 a[232:235], v20, s[56:59], s64 offen offset:3072 sc1\n" \
"v_mfma_f32_16x16x32_bf16 v[172:175], a[212:215], a[92:95], v[172:175]\n" \
"ds_read_b64_tr_b16 a[212:213], v34 offset:9248\n" \
"ds_read_b64_tr_b16 a[214:215], v34 offset:13856\n" \
"v_mfma_f32_16x16x32_bf16 v[176:179], a[216:219], a[76:79], v[176:179]\n" \
"v_mfma_f32_16x16x32_bf16 v[180:183], a[216:219], a[92:95], v[180:183]\n" \
"ds_read_b64_tr_b16 a[216:217], v34 offset:18464\n" \
"s_waitcnt vmcnt(22) lgkmcnt(15)\n" \
"v_mfma_f32_16x16x32_bf16 v[112:115], a[0:3], a[128:131], v[112:115]\n" \
"ds_read_b64_tr_b16 a[218:219], v34 offset:23072\n" \
"s_waitcnt vmcnt(21)\n" \
"v_mfma_f32_16x16x32_bf16 v[116:119], a[0:3], a[144:147], v[116:119]\n" \
"ds_read_b64_tr_b16 a[0:1], v10 offset:9248\n" \
"s_waitcnt vmcnt(20)\n" \
"v_mfma_f32_16x16x32_bf16 v[112:115], a[4:7], a[132:135], v[112:115]\n" \
"ds_read_b64_tr_b16 a[2:3], v10 offset:13856\n" \
"s_waitcnt vmcnt(19)\n" \
"v_mfma_f32_16x16x32_bf16 v[116:119], a[4:7], a[148:151], v[116:119]\n" \
"ds_read_b64_tr_b16 a[4:5], v33 offset:36928\n" \
"s_waitcnt vmcnt(18)\n" \
"v_mfma_f32_16x16x32_bf16 v[112:115], a[8:11], a[136:139], v[112:115]\n" \
"ds_read_b64_tr_b16 a[6:7], v33 offset:41536\n" \
"s_waitcnt vmcnt(17)\n" \
"v_mfma_f32_16x16x32_bf16 v[116:119], a[8:11], a[152:155], v[116:119]\n" \
"ds_read_b64_tr_b16 a[8:9], v33 offset:46144\n" \
"s_waitcnt vmcnt(16)\n" \
"v_mfma_f32_16x16x32_bf16 v[112:115], a[12:15], a[140:143], v[112:115]\n" \
"ds_read_b64_tr_b16 a[10:11], v33 offset:50752\n" \
"s_waitcnt vmcnt(15)\n" \
"v_mfma_f32_16x16x32_bf16 v[116:119], a[12:15], a[156:159], v[116:119]\n" \
"ds_read_b64_tr_b16 a[12:13], v33 offset:55360\n" \
"ds_read_b64_tr_b16 a[14:15], v33 offset:59968\n" \
"buffer_load_dwordx4 a[248:251], v32, s[56:59], s64 offen offset:3072 sc1\n" \
"s_waitcnt vmcnt(15)\n" \
"v_mfma_f32_16x16x32_bf16 v[112:115], a[16:19], a[32:35], v[112:115]\n" \
"buffer_load_dwordx4 a[160:163], v20, s[56:59], s72 offen offset:0 sc1\n" \
"s_waitcnt vmcnt(15)\n" \
"v_mfma_f32_16x16x32_bf16 v[116:119], a[16:19], a[48:51], v[116:119]\n" \
"ds_read_b64_tr_b16 a[16:17], v10 offset:64\n" \
"ds_read_b64_tr_b16 a[18:19], v10 offset:4672\n" \
"buffer_load_dwordx4 a[176:179], v32, s[56:59], s72 offen offset:0 sc1\n" \
"s_waitcnt vmcnt(15)\n" \
"v_mfma_f32_16x16x32_bf16 v[112:115], a[20:23], a[36:39], v[112:115]\n" \
"buffer_load_dwordx4 a[164:167], v20, s[56:59], s72 offen offset:1024 sc1\n" \
"s_waitcnt vmcnt(15)\n" \
"v_mfma_f32_16x16x32_bf16 v[116:119], a[20:23], a[52:55], v[116:119]\n" \
"ds_read_b64_tr_b16 a[20:21], v34 offset:64\n" \
"ds_read_b64_tr_b16 a[22:23], v34 offset:4672\n" \
"buffer_load_dwordx4 a[180:183], v32, s[56:59], s72 offen offset:1024 sc1\n" \
"s_waitcnt vmcnt(15)\n" \
"v_mfma_f32_16x16x32_bf16 v[112:115], a[24:27], a[40:43], v[112:115]\n" \
"buffer_load_dwordx4 a[168:171], v20, s[56:59], s72 offen offset:2048 sc1\n" \
"s_waitcnt vmcnt(15)\n" \
"v_mfma_f32_16x16x32_bf16 v[116:119], a[24:27], a[56:59], v[116:119]\n" \
"ds_read_b64_tr_b16 a[24:25], v34 offset:9280\n" \
"ds_read_b64_tr_b16 a[26:27], v34 offset:13888\n" \
"buffer_load_dwordx4 a[184:187], v32, s[56:59], s72 offen offset:2048 sc1\n" \
"s_waitcnt vmcnt(15) lgkmcnt(4)\n" \
"v_mfma_f32_16x16x32_bf16 v[112:115], a[28:31], a[44:47], v[112:115]\n" \
"buffer_load_dwordx4 a[172:175], v20, s[56:59], s72 offen offset:3072 sc1\n" \
"s_waitcnt vmcnt(15)\n" \
"v_mfma_f32_16x16x32_bf16 v[116:119], a[28:31], a[60:63], v[116:119]\n" \
"ds_read_b64_tr_b16 a[28:29], v34 offset:18496\n" \
"ds_read_b64_tr_b16 a[30:31], v34 offset:23104\n" \
"v_mfma_f32_16x16x32_bf16 v[120:123], a[192:195], a[128:131], v[120:123]\n" \
"buffer_load_dwordx4 a[188:191], v32, s[56:59], s72 offen offset:3072 sc1\n" \
"v_mfma_f32_16x16x32_bf16 v[124:127], a[192:195], a[144:147], v[124:127]\n" \
"v_cvt_pk_bf16_f32 v112, v112, v113\n" \
"v_cvt_pk_bf16_f32 v113, v114, v115\n" \
"v_mfma_f32_16x16x32_bf16 v[120:123], a[196:199], a[132:135], v[120:123]\n" \
"v_cvt_pk_bf16_f32 v114, v116, v117\n" \
"v_cvt_pk_bf16_f32 v115, v118, v119\n" \
"buffer_store_dwordx4 v[112:115], v20, s[56:59], s65 offen offset:0 sc1\n" \
"s_add_u32 s65, s65, 0x3800\n" \
"v_mfma_f32_16x16x32_bf16 v[124:127], a[196:199], a[148:151], v[124:127]\n" \
"ds_read_b64_tr_b16 a[192:193], v10 offset:9280\n" \
"ds_read_b64_tr_b16 a[194:195], v10 offset:13888\n" \
"v_mfma_f32_16x16x32_bf16 v[120:123], a[200:203], a[136:139], v[120:123]\n" \
"ds_read_b64_tr_b16 a[196:197], v33 offset:36960\n" \
"ds_read_b64_tr_b16 a[198:199], v33 offset:41568\n" \
"v_mfma_f32_16x16x32_bf16 v[124:127], a[200:203], a[152:155], v[124:127]\n" \
"ds_read_b64_tr_b16 a[200:201], v33 offset:46176\n" \
"ds_read_b64_tr_b16 a[202:203], v33 offset:50784\n" \
"v_mfma_f32_16x16x32_bf16 v[120:123], a[204:207], a[140:143], v[120:123]\n" \
"buffer_load_dwordx4 a[96:99], v20, s[56:59], s93 offen offset:0 sc1\n" \
"v_mfma_f32_16x16x32_bf16 v[124:127], a[204:207], a[156:159], v[124:127]\n" \
"ds_read_b64_tr_b16 a[204:205], v33 offset:55392\n" \
"ds_read_b64_tr_b16 a[206:207], v33 offset:60000\n" \
"v_mfma_f32_16x16x32_bf16 v[120:123], a[208:211], a[32:35], v[120:123]\n" \
"v_readlane_b32 s72, v11, 53\n" \
"v_mfma_f32_16x16x32_bf16 v[124:127], a[208:211], a[48:51], v[124:127]\n" \
"ds_read_b64_tr_b16 a[208:209], v10 offset:96\n" \
"ds_read_b64_tr_b16 a[210:211], v10 offset:4704\n" \
"v_mfma_f32_16x16x32_bf16 v[120:123], a[212:215], a[36:39], v[120:123]\n" \
"v_mfma_f32_16x16x32_bf16 v[124:127], a[212:215], a[52:55], v[124:127]\n" \
"ds_read_b64_tr_b16 a[212:213], v34 offset:96\n" \
"ds_read_b64_tr_b16 a[214:215], v34 offset:4704\n" \
"v_mfma_f32_16x16x32_bf16 v[120:123], a[216:219], a[40:43], v[120:123]\n" \
"buffer_load_dwordx4 a[112:115], v32, s[56:59], s93 offen offset:0 sc1\n" \
"v_mfma_f32_16x16x32_bf16 v[124:127], a[216:219], a[56:59], v[124:127]\n" \
"ds_read_b64_tr_b16 a[216:217], v34 offset:9312\n" \
"ds_read_b64_tr_b16 a[218:219], v34 offset:13920\n" \
"v_mfma_f32_16x16x32_bf16 v[120:123], a[0:3], a[44:47], v[120:123]\n" \
"v_mfma_f32_16x16x32_bf16 v[124:127], a[0:3], a[60:63], v[124:127]\n" \
"ds_read_b64_tr_b16 a[0:1], v34 offset:18528\n" \
"ds_read_b64_tr_b16 a[2:3], v34 offset:23136\n" \
"v_mfma_f32_16x16x32_bf16 v[128:131], a[4:7], a[128:131], v[128:131]\n" \
"v_mfma_f32_16x16x32_bf16 v[132:135], a[4:7], a[144:147], v[132:135]\n" \
"buffer_load_dwordx4 a[100:103], v20, s[56:59], s93 offen offset:1024 sc1\n" \
"v_mfma_f32_16x16x32_bf16 v[128:131], a[8:11], a[132:135], v[128:131]\n" \
"ds_read_b64_tr_b16 a[4:5], v10 offset:9312\n" \
"v_cvt_pk_bf16_f32 v120, v120, v121\n" \
"v_cvt_pk_bf16_f32 v121, v122, v123\n" \
"v_cvt_pk_bf16_f32 v122, v124, v125\n" \
"v_cvt_pk_bf16_f32 v123, v126, v127\n" \
"v_mfma_f32_16x16x32_bf16 v[132:135], a[8:11], a[148:151], v[132:135]\n" \
"buffer_store_dwordx4 v[120:123], v20, s[56:59], s66 offen offset:0 sc1\n" \
"s_add_u32 s66, s66, 0x3000\n" \
"v_mfma_f32_16x16x32_bf16 v[128:131], a[12:15], a[136:139], v[128:131]\n" \
"ds_read_b64_tr_b16 a[6:7], v10 offset:13920\n" \
"ds_read_b64_tr_b16 a[8:9], v33 offset:36992\n" \
"v_mfma_f32_16x16x32_bf16 v[132:135], a[12:15], a[152:155], v[132:135]\n" \
"ds_read_b64_tr_b16 a[10:11], v33 offset:41600\n" \
"ds_read_b64_tr_b16 a[12:13], v33 offset:46208\n" \
"v_mfma_f32_16x16x32_bf16 v[128:131], a[16:19], a[140:143], v[128:131]\n" \
"ds_read_b64_tr_b16 a[14:15], v33 offset:50816\n" \
"v_mfma_f32_16x16x32_bf16 v[132:135], a[16:19], a[156:159], v[132:135]\n" \
"ds_read_b64_tr_b16 a[16:17], v33 offset:55424\n" \
"s_waitcnt lgkmcnt(13)\n" \
"v_mfma_f32_16x16x32_bf16 v[128:131], a[20:23], a[32:35], v[128:131]\n" \
"buffer_load_dwordx4 a[116:119], v32, s[56:59], s93 offen offset:1024 sc1\n" \
"v_mfma_f32_16x16x32_bf16 v[132:135], a[20:23], a[48:51], v[132:135]\n" \
"ds_read_b64_tr_b16 a[18:19], v33 offset:60032\n" \
"ds_read_b64_tr_b16 a[20:21], v10 offset:128\n" \
"v_mfma_f32_16x16x32_bf16 v[128:131], a[24:27], a[36:39], v[128:131]\n" \
"ds_read_b64_tr_b16 a[22:23], v10 offset:4736\n" \
"v_mfma_f32_16x16x32_bf16 v[132:135], a[24:27], a[52:55], v[132:135]\n" \
"ds_read_b64_tr_b16 a[24:25], v34 offset:128\n" \
"ds_read_b64_tr_b16 a[26:27], v34 offset:4736\n" \
"v_mfma_f32_16x16x32_bf16 v[128:131], a[28:31], a[40:43], v[128:131]\n" \
"v_mfma_f32_16x16x32_bf16 v[132:135], a[28:31], a[56:59], v[132:135]\n" \
"buffer_load_dwordx4 a[104:107], v20, s[56:59], s93 offen offset:2048 sc1\n" \
"v_mfma_f32_16x16x32_bf16 v[128:131], a[192:195], a[44:47], v[128:131]\n" \
"ds_read_b64_tr_b16 a[28:29], v34 offset:9344\n" \
"ds_read_b64_tr_b16 a[30:31], v34 offset:13952\n" \
"v_mfma_f32_16x16x32_bf16 v[132:135], a[192:195], a[60:63], v[132:135]\n" \
"ds_read_b64_tr_b16 a[192:193], v34 offset:18560\n" \
"ds_read_b64_tr_b16 a[194:195], v34 offset:23168\n" \
"v_mfma_f32_16x16x32_bf16 v[136:139], a[196:199], a[128:131], v[136:139]\n" \
"v_mfma_f32_16x16x32_bf16 v[140:143], a[196:199], a[144:147], v[140:143]\n" \
"ds_read_b64_tr_b16 a[196:197], v10 offset:9344\n" \
"v_cvt_pk_bf16_f32 v128, v128, v129\n" \
"v_cvt_pk_bf16_f32 v129, v130, v131\n" \
"v_mfma_f32_16x16x32_bf16 v[136:139], a[200:203], a[132:135], v[136:139]\n" \
"v_cvt_pk_bf16_f32 v130, v132, v133\n" \
"v_cvt_pk_bf16_f32 v131, v134, v135\n" \
"buffer_store_dwordx4 v[128:131], v20, s[56:59], s67 offen offset:0 sc1\n" \
"s_add_u32 s67, s67, 0x2800\n" \
"v_mfma_f32_16x16x32_bf16 v[140:143], a[200:203], a[148:151], v[140:143]\n" \
"ds_read_b64_tr_b16 a[198:199], v10 offset:13952\n" \
"ds_read_b64_tr_b16 a[200:201], v33 offset:37024\n" \
"v_mfma_f32_16x16x32_bf16 v[136:139], a[204:207], a[136:139], v[136:139]\n" \
"ds_read_b64_tr_b16 a[202:203], v33 offset:41632\n" \
"v_mfma_f32_16x16x32_bf16 v[140:143], a[204:207], a[152:155], v[140:143]\n" \
"ds_read_b64_tr_b16 a[204:205], v33 offset:46240\n" \
"ds_read_b64_tr_b16 a[206:207], v33 offset:50848\n" \
"v_mfma_f32_16x16x32_bf16 v[136:139], a[208:211], a[140:143], v[136:139]\n" \
"buffer_load_dwordx4 a[120:123], v32, s[56:59], s93 offen offset:2048 sc1\n" \
"v_mfma_f32_16x16x32_bf16 v[140:143], a[208:211], a[156:159], v[140:143]\n" \
"ds_read_b64_tr_b16 a[208:209], v33 offset:55456\n" \
"s_waitcnt lgkmcnt(13)\n" \
"v_mfma_f32_16x16x32_bf16 v[136:139], a[212:215], a[32:35], v[136:139]\n" \
"ds_read_b64_tr_b16 a[210:211], v33 offset:60064\n" \
"v_mfma_f32_16x16x32_bf16 v[140:143], a[212:215], a[48:51], v[140:143]\n" \
"ds_read_b64_tr_b16 a[212:213], v10 offset:160\n" \
"ds_read_b64_tr_b16 a[214:215], v10 offset:4768\n" \
"v_mfma_f32_16x16x32_bf16 v[136:139], a[216:219], a[36:39], v[136:139]\n" \
"v_mfma_f32_16x16x32_bf16 v[140:143], a[216:219], a[52:55], v[140:143]\n" \
"buffer_load_dwordx4 a[108:111], v20, s[56:59], s93 offen offset:3072 sc1\n" \
"v_mfma_f32_16x16x32_bf16 v[136:139], a[0:3], a[40:43], v[136:139]\n" \
"ds_read_b64_tr_b16 a[216:217], v34 offset:160\n" \
"ds_read_b64_tr_b16 a[218:219], v34 offset:4768\n" \
"v_mfma_f32_16x16x32_bf16 v[140:143], a[0:3], a[56:59], v[140:143]\n" \
"ds_read_b64_tr_b16 a[0:1], v34 offset:9376\n" \
"ds_read_b64_tr_b16 a[2:3], v34 offset:13984\n" \
"v_mfma_f32_16x16x32_bf16 v[136:139], a[4:7], a[44:47], v[136:139]\n" \
"v_mfma_f32_16x16x32_bf16 v[140:143], a[4:7], a[60:63], v[140:143]\n" \
"ds_read_b64_tr_b16 a[4:5], v34 offset:18592\n" \
"ds_read_b64_tr_b16 a[6:7], v34 offset:23200\n" \
"buffer_load_dwordx4 a[124:127], v32, s[56:59], s93 offen offset:3072 sc1\n" \
"v_mfma_f32_16x16x32_bf16 v[144:147], a[8:11], a[128:131], v[144:147]\n" \
"v_readlane_b32 s93, v11, 47\n" \
"v_mfma_f32_16x16x32_bf16 v[148:151], a[8:11], a[144:147], v[148:151]\n" \
"ds_read_b64_tr_b16 a[8:9], v10 offset:9376\n" \
"v_cvt_pk_bf16_f32 v136, v136, v137\n" \
"v_cvt_pk_bf16_f32 v137, v138, v139\n" \
"v_cvt_pk_bf16_f32 v138, v140, v141\n" \
"v_cvt_pk_bf16_f32 v139, v142, v143\n" \
"buffer_store_dwordx4 v[136:139], v20, s[56:59], s68 offen offset:0 sc1\n" \
"s_add_u32 s68, s68, 0x2000\n" \
"v_mfma_f32_16x16x32_bf16 v[144:147], a[12:15], a[132:135], v[144:147]\n" \
"ds_read_b64_tr_b16 a[10:11], v10 offset:13984\n" \
"v_mfma_f32_16x16x32_bf16 v[148:151], a[12:15], a[148:151], v[148:151]\n" \
"ds_read_b64_tr_b16 a[12:13], v33 offset:37056\n" \
"ds_read_b64_tr_b16 a[14:15], v33 offset:41664\n" \
"v_mfma_f32_16x16x32_bf16 v[144:147], a[16:19], a[136:139], v[144:147]\n" \
"v_mfma_f32_16x16x32_bf16 v[148:151], a[16:19], a[152:155], v[148:151]\n" \
"ds_read_b64_tr_b16 a[16:17], v33 offset:46272\n" \
"ds_read_b64_tr_b16 a[18:19], v33 offset:50880\n" \
"v_mfma_f32_16x16x32_bf16 v[144:147], a[20:23], a[140:143], v[144:147]\n" \
"buffer_load_dwordx4 v[64:67], v20, s[56:59], s95 offen offset:0 sc1\n" \
"v_mfma_f32_16x16x32_bf16 v[148:151], a[20:23], a[156:159], v[148:151]\n" \
"ds_read_b64_tr_b16 a[20:21], v33 offset:55488\n" \
"s_waitcnt lgkmcnt(13)\n" \
"v_mfma_f32_16x16x32_bf16 v[144:147], a[24:27], a[32:35], v[144:147]\n" \
"ds_read_b64_tr_b16 a[22:23], v33 offset:60096\n" \
"v_mfma_f32_16x16x32_bf16 v[148:151], a[24:27], a[48:51], v[148:151]\n" \
"ds_read_b64_tr_b16 a[24:25], v10 offset:192\n" \
"ds_read_b64_tr_b16 a[26:27], v10 offset:4800\n" \
"v_mfma_f32_16x16x32_bf16 v[144:147], a[28:31], a[36:39], v[144:147]\n" \
"v_mfma_f32_16x16x32_bf16 v[148:151], a[28:31], a[52:55], v[148:151]\n" \
"ds_read_b64_tr_b16 a[28:29], v34 offset:192\n" \
"ds_read_b64_tr_b16 a[30:31], v34 offset:4800\n" \
"v_mfma_f32_16x16x32_bf16 v[144:147], a[192:195], a[40:43], v[144:147]\n" \
"buffer_load_dwordx4 v[68:71], v20, s[56:59], s95 offen offset:1024 sc1\n" \
"v_mfma_f32_16x16x32_bf16 v[148:151], a[192:195], a[56:59], v[148:151]\n" \
"ds_read_b64_tr_b16 a[192:193], v34 offset:9408\n" \
"ds_read_b64_tr_b16 a[194:195], v34 offset:14016\n" \
"v_mfma_f32_16x16x32_bf16 v[144:147], a[196:199], a[44:47], v[144:147]\n" \
"v_mfma_f32_16x16x32_bf16 v[148:151], a[196:199], a[60:63], v[148:151]\n" \
"ds_read_b64_tr_b16 a[196:197], v34 offset:18624\n" \
"ds_read_b64_tr_b16 a[198:199], v34 offset:23232\n" \
"v_mfma_f32_16x16x32_bf16 v[152:155], a[200:203], a[128:131], v[152:155]\n" \
"v_mfma_f32_16x16x32_bf16 v[156:159], a[200:203], a[144:147], v[156:159]\n" \
"ds_read_b64_tr_b16 a[200:201], v10 offset:9408\n" \
"ds_read_b64_tr_b16 a[202:203], v10 offset:14016\n" \
"v_mfma_f32_16x16x32_bf16 v[152:155], a[204:207], a[132:135], v[152:155]\n" \
"v_cvt_pk_bf16_f32 v144, v144, v145\n" \
"v_cvt_pk_bf16_f32 v145, v146, v147\n" \
"v_cvt_pk_bf16_f32 v146, v148, v149\n" \
"v_cvt_pk_bf16_f32 v147, v150, v151\n" \
"buffer_store_dwordx4 v[144:147], v20, s[56:59], s69 offen offset:0 sc1\n" \
"s_add_u32 s69, s69, 0x1800\n" \
"v_mfma_f32_16x16x32_bf16 v[156:159], a[204:207], a[148:151], v[156:159]\n" \
"ds_read_b64_tr_b16 a[204:205], v33 offset:256\n" \
"ds_read_b64_tr_b16 a[206:207], v33 offset:4864\n" \
"v_mfma_f32_16x16x32_bf16 v[152:155], a[208:211], a[136:139], v[152:155]\n" \
"v_mfma_f32_16x16x32_bf16 v[156:159], a[208:211], a[152:155], v[156:159]\n" \
"ds_read_b64_tr_b16 a[208:209], v33 offset:9472\n" \
"ds_read_b64_tr_b16 a[210:211], v33 offset:14080\n" \
"v_mfma_f32_16x16x32_bf16 v[152:155], a[212:215], a[140:143], v[152:155]\n" \
"buffer_load_dwordx4 v[72:75], v32, s[56:59], s95 offen offset:0 sc1\n" \
"v_mfma_f32_16x16x32_bf16 v[156:159], a[212:215], a[156:159], v[156:159]\n" \
"ds_read_b64_tr_b16 a[212:213], v33 offset:18688\n" \
"s_waitcnt lgkmcnt(13)\n" \
"v_mfma_f32_16x16x32_bf16 v[152:155], a[216:219], a[32:35], v[152:155]\n" \
"ds_read_b64_tr_b16 a[214:215], v33 offset:23296\n" \
"v_mfma_f32_16x16x32_bf16 v[156:159], a[216:219], a[48:51], v[156:159]\n" \
"ds_read_b64_tr_b16 a[216:217], v33 offset:27904\n" \
"ds_read_b64_tr_b16 a[218:219], v33 offset:32512\n" \
"v_mfma_f32_16x16x32_bf16 v[152:155], a[0:3], a[36:39], v[152:155]\n" \
"v_mfma_f32_16x16x32_bf16 v[156:159], a[0:3], a[52:55], v[156:159]\n" \
"ds_read_b64_tr_b16 a[0:1], v33 offset:37120\n" \
"ds_read_b64_tr_b16 a[2:3], v33 offset:41728\n" \
"v_mfma_f32_16x16x32_bf16 v[152:155], a[4:7], a[40:43], v[152:155]\n" \
"buffer_load_dwordx4 v[76:79], v32, s[56:59], s95 offen offset:1024 sc1\n" \
"v_mfma_f32_16x16x32_bf16 v[156:159], a[4:7], a[56:59], v[156:159]\n" \
"ds_read_b64_tr_b16 a[4:5], v33 offset:46336\n" \
"ds_read_b64_tr_b16 a[6:7], v33 offset:50944\n" \
"v_mfma_f32_16x16x32_bf16 v[152:155], a[8:11], a[44:47], v[152:155]\n" \
"v_readlane_b32 s95, v11, 49\n" \
"v_mfma_f32_16x16x32_bf16 v[156:159], a[8:11], a[60:63], v[156:159]\n" \
"ds_read_b64_tr_b16 a[8:9], v33 offset:55552\n" \
"ds_read_b64_tr_b16 a[10:11], v33 offset:60160\n" \
"v_mfma_f32_16x16x32_bf16 v[160:163], a[12:15], a[128:131], v[160:163]\n" \
"v_mfma_f32_16x16x32_bf16 v[164:167], a[12:15], a[144:147], v[164:167]\n" \
"ds_read_b64_tr_b16 a[12:13], v10 offset:256\n" \
"ds_read_b64_tr_b16 a[14:15], v10 offset:4864\n" \
"v_cvt_pk_bf16_f32 v152, v152, v153\n" \
"v_cvt_pk_bf16_f32 v153, v154, v155\n" \
"v_cvt_pk_bf16_f32 v154, v156, v157\n" \
"v_cvt_pk_bf16_f32 v155, v158, v159\n" \
"buffer_store_dwordx4 v[152:155], v20, s[56:59], s70 offen offset:0 sc1\n" \
"s_add_u32 s70, s70, 0x1000\n" \
"v_mfma_f32_16x16x32_bf16 v[160:163], a[16:19], a[132:135], v[160:163]\n" \
"v_mfma_f32_16x16x32_bf16 v[164:167], a[16:19], a[148:151], v[164:167]\n" \
"ds_read_b64_tr_b16 a[16:17], v34 offset:256\n" \
"ds_read_b64_tr_b16 a[18:19], v34 offset:4864\n" \
"v_mfma_f32_16x16x32_bf16 v[160:163], a[20:23], a[136:139], v[160:163]\n" \
"v_mfma_f32_16x16x32_bf16 v[164:167], a[20:23], a[152:155], v[164:167]\n" \
"ds_read_b64_tr_b16 a[20:21], v34 offset:9472\n" \
"ds_read_b64_tr_b16 a[22:23], v34 offset:14080\n" \
"v_mfma_f32_16x16x32_bf16 v[160:163], a[24:27], a[140:143], v[160:163]\n" \
"buffer_load_dwordx4 v[80:83], v20, s[56:59], s96 offen offset:0 sc1\n" \
"v_mfma_f32_16x16x32_bf16 v[164:167], a[24:27], a[156:159], v[164:167]\n" \
"ds_read_b64_tr_b16 a[24:25], v34 offset:18688\n" \
"s_waitcnt lgkmcnt(12)\n" \
"v_mfma_f32_16x16x32_bf16 v[160:163], a[28:31], a[32:35], v[160:163]\n" \
"ds_read_b64_tr_b16 a[26:27], v34 offset:23296\n" \
"v_mfma_f32_16x16x32_bf16 v[164:167], a[28:31], a[48:51], v[164:167]\n" \
"ds_read_b64_tr_b16 a[28:29], v10 offset:9472\n" \
"ds_read_b64_tr_b16 a[30:31], v10 offset:14080\n" \
"s_waitcnt vmcnt(18)\n" \
"s_mov_b64 exec, 1\n" \
"buffer_store_dword v39, off, s[56:59], s65 offset:0 sc1\n" \
"s_mov_b64 exec, -1\n" \
"v_mfma_f32_16x16x32_bf16 v[160:163], a[192:195], a[36:39], v[160:163]\n" \
"v_mfma_f32_16x16x32_bf16 v[164:167], a[192:195], a[52:55], v[164:167]\n" \
"ds_read_b64_tr_b16 a[192:193], v33 offset:0\n" \
"ds_read_b64_tr_b16 a[194:195], v33 offset:4608\n" \
"v_mfma_f32_16x16x32_bf16 v[160:163], a[196:199], a[40:43], v[160:163]\n" \
"v_mfma_f32_16x16x32_bf16 v[164:167], a[196:199], a[56:59], v[164:167]\n" \
"ds_read_b64_tr_b16 a[196:197], v33 offset:9216\n" \
"ds_read_b64_tr_b16 a[198:199], v33 offset:13824\n" \
"v_mfma_f32_16x16x32_bf16 v[160:163], a[200:203], a[44:47], v[160:163]\n" \
"buffer_load_dwordx4 v[84:87], v20, s[56:59], s96 offen offset:1024 sc1\n" \
"v_mfma_f32_16x16x32_bf16 v[164:167], a[200:203], a[60:63], v[164:167]\n" \
"ds_read_b64_tr_b16 a[200:201], v33 offset:18432\n" \
"ds_read_b64_tr_b16 a[202:203], v33 offset:23040\n" \
"v_mfma_f32_16x16x32_bf16 v[248:251], a[204:207], a[220:223], v[248:251]\n" \
"v_mfma_f32_16x16x32_bf16 v[252:255], a[204:207], a[236:239], v[252:255]\n" \
"ds_read_b64_tr_b16 a[204:205], v33 offset:27648\n" \
"ds_read_b64_tr_b16 a[206:207], v33 offset:32256\n" \
"v_cvt_pk_bf16_f32 v160, v160, v161\n" \
"v_cvt_pk_bf16_f32 v161, v162, v163\n" \
"v_cvt_pk_bf16_f32 v162, v164, v165\n" \
"v_cvt_pk_bf16_f32 v163, v166, v167\n" \
"buffer_store_dwordx4 v[160:163], v20, s[56:59], s71 offen offset:0 sc1\n" \
"s_add_u32 s71, s71, 0x800\n" \
"v_mfma_f32_16x16x32_bf16 v[248:251], a[208:211], a[224:227], v[248:251]\n" \
"v_mfma_f32_16x16x32_bf16 v[252:255], a[208:211], a[240:243], v[252:255]\n" \
"ds_read_b64_tr_b16 a[208:209], v33 offset:36864\n" \
"ds_read_b64_tr_b16 a[210:211], v33 offset:41472\n" \
"v_mfma_f32_16x16x32_bf16 v[248:251], a[212:215], a[228:231], v[248:251]\n" \
"v_mfma_f32_16x16x32_bf16 v[252:255], a[212:215], a[244:247], v[252:255]\n" \
"ds_read_b64_tr_b16 a[212:213], v33 offset:46080\n" \
"ds_read_b64_tr_b16 a[214:215], v33 offset:50688\n" \
"v_mfma_f32_16x16x32_bf16 v[248:251], a[216:219], a[232:235], v[248:251]\n" \
"buffer_load_dwordx4 v[88:91], v32, s[56:59], s96 offen offset:0 sc1\n" \
"v_mfma_f32_16x16x32_bf16 v[252:255], a[216:219], a[248:251], v[252:255]\n" \
"ds_read_b64_tr_b16 a[216:217], v33 offset:55296\n" \
"s_waitcnt lgkmcnt(12)\n" \
"v_mfma_f32_16x16x32_bf16 v[176:179], a[0:3], a[128:131], v[176:179]\n" \
"ds_read_b64_tr_b16 a[218:219], v33 offset:59904\n" \
"s_waitcnt vmcnt(18)\n" \
"s_mov_b64 exec, 1\n" \
"buffer_store_dword v39, off, s[56:59], s66 offset:4 sc1\n" \
"s_mov_b64 exec, -1\n" \
"v_mfma_f32_16x16x32_bf16 v[180:183], a[0:3], a[144:147], v[180:183]\n" \
"v_mfma_f32_16x16x32_bf16 v[248:251], a[0:3], a[160:163], v[248:251]\n" \
"v_mfma_f32_16x16x32_bf16 v[252:255], a[0:3], a[176:179], v[252:255]\n" \
"ds_read_b64_tr_b16 a[0:1], v10 offset:0\n" \
"ds_read_b64_tr_b16 a[2:3], v10 offset:4608\n" \
"v_mfma_f32_16x16x32_bf16 v[176:179], a[4:7], a[132:135], v[176:179]\n" \
"buffer_load_dwordx4 v[92:95], v32, s[56:59], s96 offen offset:1024 sc1\n" \
"v_mfma_f32_16x16x32_bf16 v[180:183], a[4:7], a[148:151], v[180:183]\n" \
"v_readlane_b32 s96, v11, 50\n" \
"v_mfma_f32_16x16x32_bf16 v[248:251], a[4:7], a[164:167], v[248:251]\n" \
"v_mfma_f32_16x16x32_bf16 v[252:255], a[4:7], a[180:183], v[252:255]\n" \
"ds_read_b64_tr_b16 a[4:5], v34 offset:0\n" \
"ds_read_b64_tr_b16 a[6:7], v34 offset:4608\n" \
"v_mfma_f32_16x16x32_bf16 v[176:179], a[8:11], a[136:139], v[176:179]\n" \
"v_mfma_f32_16x16x32_bf16 v[180:183], a[8:11], a[152:155], v[180:183]\n" \
"buffer_load_dwordx4 v[96:99], v20, s[56:59], s97 offen offset:0 sc1\n" \
"v_mfma_f32_16x16x32_bf16 v[248:251], a[8:11], a[168:171], v[248:251]\n" \
"v_mfma_f32_16x16x32_bf16 v[252:255], a[8:11], a[184:187], v[252:255]\n" \
"ds_read_b64_tr_b16 a[8:9], v34 offset:9216\n" \
"ds_read_b64_tr_b16 a[10:11], v34 offset:13824\n" \
"v_mfma_f32_16x16x32_bf16 v[176:179], a[12:15], a[140:143], v[176:179]\n" \
"v_mfma_f32_16x16x32_bf16 v[180:183], a[12:15], a[156:159], v[180:183]\n" \
"v_mfma_f32_16x16x32_bf16 v[248:251], a[12:15], a[172:175], v[248:251]\n" \
"buffer_load_dwordx4 v[104:107], v32, s[56:59], s97 offen offset:0 sc1\n" \
"v_mfma_f32_16x16x32_bf16 v[252:255], a[12:15], a[188:191], v[252:255]\n" \
"ds_read_b64_tr_b16 a[12:13], v34 offset:18432\n" \
"ds_read_b64_tr_b16 a[14:15], v34 offset:23040\n" \
"s_waitcnt vmcnt(19)\n" \
"s_mov_b64 exec, 1\n" \
"buffer_store_dword v39, off, s[56:59], s67 offset:8 sc1\n" \
"s_mov_b64 exec, -1\n" \
"v_mfma_f32_16x16x32_bf16 v[176:179], a[16:19], a[32:35], v[176:179]\n" \
"v_mfma_f32_16x16x32_bf16 v[180:183], a[16:19], a[48:51], v[180:183]\n" \
"v_mfma_f32_16x16x32_bf16 v[248:251], a[16:19], a[96:99], v[248:251]\n" \
"v_mfma_f32_16x16x32_bf16 v[252:255], a[16:19], a[112:115], v[252:255]\n" \
"ds_read_b64_tr_b16 a[16:17], v10 offset:9216\n" \
"ds_read_b64_tr_b16 a[18:19], v10 offset:13824\n" \
"v_mfma_f32_16x16x32_bf16 v[176:179], a[20:23], a[36:39], v[176:179]\n" \
"buffer_load_dwordx4 v[100:103], v20, s[56:59], s97 offen offset:1024 sc1\n" \
"v_mfma_f32_16x16x32_bf16 v[180:183], a[20:23], a[52:55], v[180:183]\n" \
"v_mfma_f32_16x16x32_bf16 v[248:251], a[20:23], a[100:103], v[248:251]\n" \
"v_mfma_f32_16x16x32_bf16 v[252:255], a[20:23], a[116:119], v[252:255]\n" \
"ds_read_b64_tr_b16 a[20:21], v33 offset:32\n" \
"ds_read_b64_tr_b16 a[22:23], v33 offset:4640\n" \
"v_mfma_f32_16x16x32_bf16 v[176:179], a[24:27], a[40:43], v[176:179]\n" \
"v_mfma_f32_16x16x32_bf16 v[180:183], a[24:27], a[56:59], v[180:183]\n" \
"buffer_load_dwordx4 v[108:111], v32, s[56:59], s97 offen offset:1024 sc1\n" \
"v_mfma_f32_16x16x32_bf16 v[248:251], a[24:27], a[104:107], v[248:251]\n" \
"s_waitcnt vmcnt(21)\n" \
"v_mfma_f32_16x16x32_bf16 v[252:255], a[24:27], a[120:123], v[252:255]\n" \
"ds_read_b64_tr_b16 a[24:25], v33 offset:9248\n" \
"ds_read_b64_tr_b16 a[26:27], v33 offset:13856\n" \
"v_mfma_f32_16x16x32_bf16 v[176:179], a[28:31], a[44:47], v[176:179]\n" \
"v_mfma_f32_16x16x32_bf16 v[180:183], a[28:31], a[60:63], v[180:183]\n" \
"s_waitcnt vmcnt(20)\n" \
".fill (L_ab + 40960 - .) / 4, 4, 0xBF800000\n" \
"L_ap10:\n" \
"s_cbranch_execz L_ap0\n" \
"v_mfma_f32_16x16x32_bf16 v[248:251], a[28:31], a[108:111], v[248:251]\n" \
"s_waitcnt vmcnt(19)\n" \
"v_mfma_f32_16x16x32_bf16 v[252:255], a[28:31], a[124:127], v[252:255]\n" \
"ds_read_b64_tr_b16 a[28:29], v33 offset:18464\n" \
"s_waitcnt vmcnt(18)\n" \
"s_mov_b64 exec, 1\n" \
"buffer_store_dword v39, off, s[56:59], s68 offset:12 sc1\n" \
"s_mov_b64 exec, -1\n" \
"v_cvt_pk_bf16_f32 v176, v176, v177\n" \
"v_cvt_pk_bf16_f32 v248, v248, v249\n" \
"v_cvt_pk_bf16_f32 v177, v178, v179\n" \
"v_cvt_pk_bf16_f32 v249, v250, v251\n" \
"v_cvt_pk_bf16_f32 v178, v180, v181\n" \
"v_cvt_pk_bf16_f32 v179, v182, v183\n" \
"v_cvt_pk_bf16_f32 v250, v252, v253\n" \
"v_cvt_pk_bf16_f32 v251, v254, v255\n" \
"buffer_store_dwordx4 v[176:179], v20, s[56:59], s72 offen offset:0 sc1\n" \
"s_waitcnt lgkmcnt(5)\n" \
"v_mfma_f32_16x16x32_bf16 v[184:187], a[192:195], a[220:223], v[184:187]\n" \
"ds_read_b64_tr_b16 a[30:31], v33 offset:23072\n" \
"v_mfma_f32_16x16x32_bf16 v[188:191], a[192:195], a[236:239], v[188:191]\n" \
"ds_read_b64_tr_b16 a[192:193], v33 offset:27680\n" \
"ds_read_b64_tr_b16 a[194:195], v33 offset:32288\n" \
"buffer_store_dwordx4 v[248:251], v20, s[56:59], s72 offen offset:1024 sc1\n" \
"v_mfma_f32_16x16x32_bf16 v[184:187], a[196:199], a[224:227], v[184:187]\n" \
"v_readlane_b32 s72, v11, 52\n" \
"v_mfma_f32_16x16x32_bf16 v[188:191], a[196:199], a[240:243], v[188:191]\n" \
"ds_read_b64_tr_b16 a[196:197], v33 offset:36896\n" \
"ds_read_b64_tr_b16 a[198:199], v33 offset:41504\n" \
"v_mfma_f32_16x16x32_bf16 v[184:187], a[200:203], a[228:231], v[184:187]\n" \
"v_mfma_f32_16x16x32_bf16 v[188:191], a[200:203], a[244:247], v[188:191]\n" \
"ds_read_b64_tr_b16 a[200:201], v33 offset:46112\n" \
"ds_read_b64_tr_b16 a[202:203], v33 offset:50720\n" \
"v_mfma_f32_16x16x32_bf16 v[184:187], a[204:207], a[232:235], v[184:187]\n" \
"v_mfma_f32_16x16x32_bf16 v[188:191], a[204:207], a[248:251], v[188:191]\n" \
"ds_read_b64_tr_b16 a[204:205], v33 offset:55328\n" \
"ds_read_b64_tr_b16 a[206:207], v33 offset:59936\n" \
"v_mfma_f32_16x16x32_bf16 v[184:187], a[208:211], a[160:163], v[184:187]\n" \
"v_mfma_f32_16x16x32_bf16 v[188:191], a[208:211], a[176:179], v[188:191]\n" \
"ds_read_b64_tr_b16 a[208:209], v10 offset:32\n" \
"ds_read_b64_tr_b16 a[210:211], v10 offset:4640\n" \
"v_mfma_f32_16x16x32_bf16 v[184:187], a[212:215], a[164:167], v[184:187]\n" \
"v_mfma_f32_16x16x32_bf16 v[188:191], a[212:215], a[180:183], v[188:191]\n" \
"ds_read_b64_tr_b16 a[212:213], v34 offset:32\n" \
"ds_read_b64_tr_b16 a[214:215], v34 offset:4640\n" \
"v_mfma_f32_16x16x32_bf16 v[184:187], a[216:219], a[168:171], v[184:187]\n" \
"v_mfma_f32_16x16x32_bf16 v[188:191], a[216:219], a[184:187], v[188:191]\n" \
"ds_read_b64_tr_b16 a[216:217], v34 offset:9248\n" \
"ds_read_b64_tr_b16 a[218:219], v34 offset:13856\n" \
"s_waitcnt vmcnt(18)\n" \
"s_mov_b64 exec, 1\n" \
"buffer_store_dword v39, off, s[56:59], s69 offset:16 sc1\n" \
"s_mov_b64 exec, -1\n" \
"v_mfma_f32_16x16x32_bf16 v[184:187], a[0:3], a[172:175], v[184:187]\n" \
"v_mfma_f32_16x16x32_bf16 v[188:191], a[0:3], a[188:191], v[188:191]\n" \
"ds_read_b64_tr_b16 a[0:1], v34 offset:18464\n" \
"ds_read_b64_tr_b16 a[2:3], v34 offset:23072\n" \
"v_mfma_f32_16x16x32_bf16 v[184:187], a[4:7], a[96:99], v[184:187]\n" \
"v_mfma_f32_16x16x32_bf16 v[188:191], a[4:7], a[112:115], v[188:191]\n" \
"ds_read_b64_tr_b16 a[4:5], v10 offset:9248\n" \
"ds_read_b64_tr_b16 a[6:7], v10 offset:13856\n" \
"v_mfma_f32_16x16x32_bf16 v[184:187], a[8:11], a[100:103], v[184:187]\n" \
"v_mfma_f32_16x16x32_bf16 v[188:191], a[8:11], a[116:119], v[188:191]\n" \
"ds_read_b64_tr_b16 a[8:9], v33 offset:64\n" \
"ds_read_b64_tr_b16 a[10:11], v33 offset:4672\n" \
"v_mfma_f32_16x16x32_bf16 v[184:187], a[12:15], a[104:107], v[184:187]\n" \
"v_mfma_f32_16x16x32_bf16 v[188:191], a[12:15], a[120:123], v[188:191]\n" \
"ds_read_b64_tr_b16 a[12:13], v33 offset:9280\n" \
"ds_read_b64_tr_b16 a[14:15], v33 offset:13888\n" \
"v_mfma_f32_16x16x32_bf16 v[184:187], a[16:19], a[108:111], v[184:187]\n" \
"v_mfma_f32_16x16x32_bf16 v[188:191], a[16:19], a[124:127], v[188:191]\n" \
"ds_read_b64_tr_b16 a[16:17], v33 offset:18496\n" \
"s_waitcnt lgkmcnt(13)\n" \
"v_mfma_f32_16x16x32_bf16 v[192:195], a[20:23], a[220:223], v[192:195]\n" \
"ds_read_b64_tr_b16 a[18:19], v33 offset:23104\n" \
"v_mfma_f32_16x16x32_bf16 v[196:199], a[20:23], a[236:239], v[196:199]\n" \
"ds_read_b64_tr_b16 a[20:21], v33 offset:27712\n" \
"ds_read_b64_tr_b16 a[22:23], v33 offset:32320\n" \
"v_cvt_pk_bf16_f32 v184, v184, v185\n" \
"v_cvt_pk_bf16_f32 v185, v186, v187\n" \
"v_cvt_pk_bf16_f32 v186, v188, v189\n" \
"v_cvt_pk_bf16_f32 v187, v190, v191\n" \
"buffer_store_dwordx4 v[184:187], v20, s[56:59], s92 offen offset:1024 sc1\n" \
"s_add_u32 s92, s92, 0x3800\n" \
"v_mfma_f32_16x16x32_bf16 v[192:195], a[24:27], a[224:227], v[192:195]\n" \
"v_mfma_f32_16x16x32_bf16 v[196:199], a[24:27], a[240:243], v[196:199]\n" \
"ds_read_b64_tr_b16 a[24:25], v33 offset:36928\n" \
"ds_read_b64_tr_b16 a[26:27], v33 offset:41536\n" \
"s_waitcnt vmcnt(17)\n" \
"s_mov_b64 exec, 1\n" \
"buffer_store_dword v39, off, s[56:59], s70 offset:20 sc1\n" \
"s_mov_b64 exec, -1\n" \
"v_mfma_f32_16x16x32_bf16 v[192:195], a[28:31], a[228:231], v[192:195]\n" \
"v_readlane_b32 s70, v11, 42\n" \
"v_mfma_f32_16x16x32_bf16 v[196:199], a[28:31], a[244:247], v[196:199]\n" \
"ds_read_b64_tr_b16 a[28:29], v33 offset:46144\n" \
"ds_read_b64_tr_b16 a[30:31], v33 offset:50752\n" \
"v_mfma_f32_16x16x32_bf16 v[192:195], a[192:195], a[232:235], v[192:195]\n" \
"v_mfma_f32_16x16x32_bf16 v[196:199], a[192:195], a[248:251], v[196:199]\n" \
"ds_read_b64_tr_b16 a[192:193], v33 offset:55360\n" \
"ds_read_b64_tr_b16 a[194:195], v33 offset:59968\n" \
"v_mfma_f32_16x16x32_bf16 v[192:195], a[196:199], a[160:163], v[192:195]\n" \
"v_mfma_f32_16x16x32_bf16 v[196:199], a[196:199], a[176:179], v[196:199]\n" \
"ds_read_b64_tr_b16 a[196:197], v10 offset:64\n" \
"ds_read_b64_tr_b16 a[198:199], v10 offset:4672\n" \
"v_mfma_f32_16x16x32_bf16 v[192:195], a[200:203], a[164:167], v[192:195]\n" \
"v_mfma_f32_16x16x32_bf16 v[196:199], a[200:203], a[180:183], v[196:199]\n" \
"ds_read_b64_tr_b16 a[200:201], v34 offset:64\n" \
"ds_read_b64_tr_b16 a[202:203], v34 offset:4672\n" \
"v_mfma_f32_16x16x32_bf16 v[192:195], a[204:207], a[168:171], v[192:195]\n" \
"v_mfma_f32_16x16x32_bf16 v[196:199], a[204:207], a[184:187], v[196:199]\n" \
"ds_read_b64_tr_b16 a[204:205], v34 offset:9280\n" \
"ds_read_b64_tr_b16 a[206:207], v34 offset:13888\n" \
"v_mfma_f32_16x16x32_bf16 v[192:195], a[208:211], a[172:175], v[192:195]\n" \
"v_mfma_f32_16x16x32_bf16 v[196:199], a[208:211], a[188:191], v[196:199]\n" \
"ds_read_b64_tr_b16 a[208:209], v34 offset:18496\n" \
"s_waitcnt lgkmcnt(13)\n" \
"v_mfma_f32_16x16x32_bf16 v[192:195], a[212:215], a[96:99], v[192:195]\n" \
"ds_read_b64_tr_b16 a[210:211], v34 offset:23104\n" \
"v_mfma_f32_16x16x32_bf16 v[196:199], a[212:215], a[112:115], v[196:199]\n" \
"ds_read_b64_tr_b16 a[212:213], v10 offset:9280\n" \
"ds_read_b64_tr_b16 a[214:215], v10 offset:13888\n" \
"v_mfma_f32_16x16x32_bf16 v[192:195], a[216:219], a[100:103], v[192:195]\n" \
"v_mfma_f32_16x16x32_bf16 v[196:199], a[216:219], a[116:119], v[196:199]\n" \
"ds_read_b64_tr_b16 a[216:217], v33 offset:96\n" \
"ds_read_b64_tr_b16 a[218:219], v33 offset:4704\n" \
"v_mfma_f32_16x16x32_bf16 v[192:195], a[0:3], a[104:107], v[192:195]\n" \
"v_mfma_f32_16x16x32_bf16 v[196:199], a[0:3], a[120:123], v[196:199]\n" \
"ds_read_b64_tr_b16 a[0:1], v33 offset:9312\n" \
"ds_read_b64_tr_b16 a[2:3], v33 offset:13920\n" \
"s_waitcnt vmcnt(14)\n" \
"s_mov_b64 exec, 1\n" \
"buffer_store_dword v39, off, s[56:59], s71 offset:24 sc1\n" \
"s_mov_b64 exec, -1\n" \
"v_mfma_f32_16x16x32_bf16 v[192:195], a[4:7], a[108:111], v[192:195]\n" \
"v_readlane_b32 s71, v11, 51\n" \
"v_mfma_f32_16x16x32_bf16 v[196:199], a[4:7], a[124:127], v[196:199]\n" \
"ds_read_b64_tr_b16 a[4:5], v33 offset:18528\n" \
"ds_read_b64_tr_b16 a[6:7], v33 offset:23136\n" \
"v_mfma_f32_16x16x32_bf16 v[200:203], a[8:11], a[220:223], v[200:203]\n" \
"v_mfma_f32_16x16x32_bf16 v[204:207], a[8:11], a[236:239], v[204:207]\n" \
"ds_read_b64_tr_b16 a[8:9], v33 offset:27744\n" \
"ds_read_b64_tr_b16 a[10:11], v33 offset:32352\n" \
"v_cvt_pk_bf16_f32 v192, v192, v193\n" \
"v_cvt_pk_bf16_f32 v193, v194, v195\n" \
"v_cvt_pk_bf16_f32 v194, v196, v197\n" \
"v_cvt_pk_bf16_f32 v195, v198, v199\n" \
"buffer_store_dwordx4 v[192:195], v20, s[56:59], s93 offen offset:1024 sc1\n" \
"s_add_u32 s93, s93, 0x3000\n" \
"v_mfma_f32_16x16x32_bf16 v[200:203], a[12:15], a[224:227], v[200:203]\n" \
"v_mfma_f32_16x16x32_bf16 v[204:207], a[12:15], a[240:243], v[204:207]\n" \
"ds_read_b64_tr_b16 a[12:13], v33 offset:36960\n" \
"ds_read_b64_tr_b16 a[14:15], v33 offset:41568\n" \
"v_mfma_f32_16x16x32_bf16 v[200:203], a[16:19], a[228:231], v[200:203]\n" \
"v_mfma_f32_16x16x32_bf16 v[204:207], a[16:19], a[244:247], v[204:207]\n" \
"ds_read_b64_tr_b16 a[16:17], v33 offset:46176\n" \
"ds_read_b64_tr_b16 a[18:19], v33 offset:50784\n" \
"v_mfma_f32_16x16x32_bf16 v[200:203], a[20:23], a[232:235], v[200:203]\n" \
"v_mfma_f32_16x16x32_bf16 v[204:207], a[20:23], a[248:251], v[204:207]\n" \
"ds_read_b64_tr_b16 a[20:21], v33 offset:55392\n" \
"s_waitcnt lgkmcnt(11)\n" \
"v_mfma_f32_16x16x32_bf16 v[200:203], a[24:27], a[160:163], v[200:203]\n" \
"ds_read_b64_tr_b16 a[22:23], v33 offset:60000\n" \
"v_mfma_f32_16x16x32_bf16 v[204:207], a[24:27], a[176:179], v[204:207]\n" \
"ds_read_b64_tr_b16 a[24:25], v10 offset:96\n" \
"ds_read_b64_tr_b16 a[26:27], v10 offset:4704\n" \
"v_mfma_f32_16x16x32_bf16 v[200:203], a[28:31], a[164:167], v[200:203]\n" \
"v_mfma_f32_16x16x32_bf16 v[204:207], a[28:31], a[180:183], v[204:207]\n" \
"ds_read_b64_tr_b16 a[28:29], v34 offset:96\n" \
"ds_read_b64_tr_b16 a[30:31], v34 offset:4704\n" \
"v_mfma_f32_16x16x32_bf16 v[200:203], a[192:195], a[168:171], v[200:203]\n" \
"v_mfma_f32_16x16x32_bf16 v[204:207], a[192:195], a[184:187], v[204:207]\n" \
"ds_read_b64_tr_b16 a[192:193], v34 offset:9312\n" \
"ds_read_b64_tr_b16 a[194:195], v34 offset:13920\n" \
"v_mfma_f32_16x16x32_bf16 v[200:203], a[196:199], a[172:175], v[200:203]\n" \
"v_mfma_f32_16x16x32_bf16 v[204:207], a[196:199], a[188:191], v[204:207]\n" \
"ds_read_b64_tr_b16 a[196:197], v34 offset:18528\n" \
"ds_read_b64_tr_b16 a[198:199], v34 offset:23136\n" \
"v_mfma_f32_16x16x32_bf16 v[200:203], a[200:203], a[96:99], v[200:203]\n" \
"v_mfma_f32_16x16x32_bf16 v[204:207], a[200:203], a[112:115], v[204:207]\n" \
"ds_read_b64_tr_b16 a[200:201], v10 offset:9312\n" \
"ds_read_b64_tr_b16 a[202:203], v10 offset:13920\n" \
"v_mfma_f32_16x16x32_bf16 v[200:203], a[204:207], a[100:103], v[200:203]\n" \
"v_mfma_f32_16x16x32_bf16 v[204:207], a[204:207], a[116:119], v[204:207]\n" \
"ds_read_b64_tr_b16 a[204:205], v33 offset:128\n" \
"ds_read_b64_tr_b16 a[206:207], v33 offset:4736\n" \
"v_mfma_f32_16x16x32_bf16 v[200:203], a[208:211], a[104:107], v[200:203]\n" \
"v_mfma_f32_16x16x32_bf16 v[204:207], a[208:211], a[120:123], v[204:207]\n" \
"ds_read_b64_tr_b16 a[208:209], v33 offset:9344\n" \
"ds_read_b64_tr_b16 a[210:211], v33 offset:13952\n" \
"v_mfma_f32_16x16x32_bf16 v[200:203], a[212:215], a[108:111], v[200:203]\n" \
"v_mfma_f32_16x16x32_bf16 v[204:207], a[212:215], a[124:127], v[204:207]\n" \
"ds_read_b64_tr_b16 a[212:213], v33 offset:18560\n" \
"ds_read_b64_tr_b16 a[214:215], v33 offset:23168\n" \
"v_mfma_f32_16x16x32_bf16 v[208:211], a[216:219], a[220:223], v[208:211]\n" \
"v_mfma_f32_16x16x32_bf16 v[212:215], a[216:219], a[236:239], v[212:215]\n" \
"ds_read_b64_tr_b16 a[216:217], v33 offset:27776\n" \
"s_waitcnt lgkmcnt(13)\n" \
"v_mfma_f32_16x16x32_bf16 v[208:211], a[0:3], a[224:227], v[208:211]\n" \
"v_cvt_pk_bf16_f32 v200, v200, v201\n" \
"v_cvt_pk_bf16_f32 v201, v202, v203\n" \
"v_cvt_pk_bf16_f32 v202, v204, v205\n" \
"v_cvt_pk_bf16_f32 v203, v206, v207\n" \
"buffer_store_dwordx4 v[200:203], v20, s[56:59], s94 offen offset:1024 sc1\n" \
"s_add_u32 s94, s94, 0x2800\n" \
"v_mfma_f32_16x16x32_bf16 v[212:215], a[0:3], a[240:243], v[212:215]\n" \
"ds_read_b64_tr_b16 a[218:219], v33 offset:32384\n" \
"ds_read_b64_tr_b16 a[0:1], v33 offset:36992\n" \
"v_mfma_f32_16x16x32_bf16 v[208:211], a[4:7], a[228:231], v[208:211]\n" \
"ds_read_b64_tr_b16 a[2:3], v33 offset:41600\n" \
"v_mfma_f32_16x16x32_bf16 v[212:215], a[4:7], a[244:247], v[212:215]\n" \
"ds_read_b64_tr_b16 a[4:5], v33 offset:46208\n" \
"ds_read_b64_tr_b16 a[6:7], v33 offset:50816\n" \
"v_mfma_f32_16x16x32_bf16 v[208:211], a[8:11], a[232:235], v[208:211]\n" \
"v_mfma_f32_16x16x32_bf16 v[212:215], a[8:11], a[248:251], v[212:215]\n" \
"ds_read_b64_tr_b16 a[8:9], v33 offset:55424\n" \
"ds_read_b64_tr_b16 a[10:11], v33 offset:60032\n" \
"v_mfma_f32_16x16x32_bf16 v[208:211], a[12:15], a[160:163], v[208:211]\n" \
"v_mfma_f32_16x16x32_bf16 v[212:215], a[12:15], a[176:179], v[212:215]\n" \
"ds_read_b64_tr_b16 a[12:13], v10 offset:128\n" \
"ds_read_b64_tr_b16 a[14:15], v10 offset:4736\n" \
"v_mfma_f32_16x16x32_bf16 v[208:211], a[16:19], a[164:167], v[208:211]\n" \
"v_mfma_f32_16x16x32_bf16 v[212:215], a[16:19], a[180:183], v[212:215]\n" \
"ds_read_b64_tr_b16 a[16:17], v34 offset:128\n" \
"ds_read_b64_tr_b16 a[18:19], v34 offset:4736\n" \
"v_mfma_f32_16x16x32_bf16 v[208:211], a[20:23], a[168:171], v[208:211]\n" \
"v_mfma_f32_16x16x32_bf16 v[212:215], a[20:23], a[184:187], v[212:215]\n" \
"ds_read_b64_tr_b16 a[20:21], v34 offset:9344\n" \
"ds_read_b64_tr_b16 a[22:23], v34 offset:13952\n" \
"v_mfma_f32_16x16x32_bf16 v[208:211], a[24:27], a[172:175], v[208:211]\n" \
"v_mfma_f32_16x16x32_bf16 v[212:215], a[24:27], a[188:191], v[212:215]\n" \
"ds_read_b64_tr_b16 a[24:25], v34 offset:18560\n" \
"ds_read_b64_tr_b16 a[26:27], v34 offset:23168\n" \
"s_waitcnt vmcnt(6)\n" \
"s_mov_b64 exec, 3\n" \
"buffer_store_dword v39, v41, s[56:59], s73 offen sc1\n" \
"s_mov_b64 exec, -1\n" \
"v_mfma_f32_16x16x32_bf16 v[208:211], a[28:31], a[96:99], v[208:211]\n" \
"v_mfma_f32_16x16x32_bf16 v[212:215], a[28:31], a[112:115], v[212:215]\n" \
"ds_read_b64_tr_b16 a[28:29], v10 offset:9344\n" \
"s_waitcnt lgkmcnt(13)\n" \
"v_mfma_f32_16x16x32_bf16 v[208:211], a[192:195], a[100:103], v[208:211]\n" \
"ds_read_b64_tr_b16 a[30:31], v10 offset:13952\n" \
"v_mfma_f32_16x16x32_bf16 v[212:215], a[192:195], a[116:119], v[212:215]\n" \
"ds_read_b64_tr_b16 a[192:193], v33 offset:160\n" \
"ds_read_b64_tr_b16 a[194:195], v33 offset:4768\n" \
"v_mfma_f32_16x16x32_bf16 v[208:211], a[196:199], a[104:107], v[208:211]\n" \
"v_mfma_f32_16x16x32_bf16 v[212:215], a[196:199], a[120:123], v[212:215]\n" \
"ds_read_b64_tr_b16 a[196:197], v33 offset:9376\n" \
"ds_read_b64_tr_b16 a[198:199], v33 offset:13984\n" \
"v_mfma_f32_16x16x32_bf16 v[208:211], a[200:203], a[108:111], v[208:211]\n" \
"v_mfma_f32_16x16x32_bf16 v[212:215], a[200:203], a[124:127], v[212:215]\n" \
"ds_read_b64_tr_b16 a[200:201], v33 offset:18592\n" \
"ds_read_b64_tr_b16 a[202:203], v33 offset:23200\n" \
"v_mfma_f32_16x16x32_bf16 v[216:219], a[204:207], a[220:223], v[216:219]\n" \
"v_mfma_f32_16x16x32_bf16 v[220:223], a[204:207], a[236:239], v[220:223]\n" \
"ds_read_b64_tr_b16 a[204:205], v33 offset:27808\n" \
"ds_read_b64_tr_b16 a[206:207], v33 offset:32416\n" \
"v_mfma_f32_16x16x32_bf16 v[216:219], a[208:211], a[224:227], v[216:219]\n" \
"v_cvt_pk_bf16_f32 v208, v208, v209\n" \
"v_cvt_pk_bf16_f32 v209, v210, v211\n" \
"v_cvt_pk_bf16_f32 v210, v212, v213\n" \
"v_cvt_pk_bf16_f32 v211, v214, v215\n" \
"buffer_store_dwordx4 v[208:211], v20, s[56:59], s95 offen offset:1024 sc1\n" \
"s_add_u32 s95, s95, 0x2000\n" \
"v_mfma_f32_16x16x32_bf16 v[220:223], a[208:211], a[240:243], v[220:223]\n" \
"ds_read_b64_tr_b16 a[208:209], v33 offset:37024\n" \
"ds_read_b64_tr_b16 a[210:211], v33 offset:41632\n" \
"v_mfma_f32_16x16x32_bf16 v[216:219], a[212:215], a[228:231], v[216:219]\n" \
"v_mfma_f32_16x16x32_bf16 v[220:223], a[212:215], a[244:247], v[220:223]\n" \
"ds_read_b64_tr_b16 a[212:213], v33 offset:46240\n" \
"ds_read_b64_tr_b16 a[214:215], v33 offset:50848\n" \
"v_mfma_f32_16x16x32_bf16 v[216:219], a[216:219], a[232:235], v[216:219]\n" \
"v_mfma_f32_16x16x32_bf16 v[220:223], a[216:219], a[248:251], v[220:223]\n" \
"ds_read_b64_tr_b16 a[216:217], v33 offset:55456\n" \
"ds_read_b64_tr_b16 a[218:219], v33 offset:60064\n" \
"v_mfma_f32_16x16x32_bf16 v[216:219], a[0:3], a[160:163], v[216:219]\n" \
"v_mfma_f32_16x16x32_bf16 v[220:223], a[0:3], a[176:179], v[220:223]\n" \
"ds_read_b64_tr_b16 a[0:1], v10 offset:160\n" \
"s_waitcnt lgkmcnt(13)\n" \
"v_mfma_f32_16x16x32_bf16 v[216:219], a[4:7], a[164:167], v[216:219]\n" \
"ds_read_b64_tr_b16 a[2:3], v10 offset:4768\n" \
"v_mfma_f32_16x16x32_bf16 v[220:223], a[4:7], a[180:183], v[220:223]\n" \
"ds_read_b64_tr_b16 a[4:5], v34 offset:160\n" \
"ds_read_b64_tr_b16 a[6:7], v34 offset:4768\n" \
"v_mfma_f32_16x16x32_bf16 v[216:219], a[8:11], a[168:171], v[216:219]\n" \
"v_mfma_f32_16x16x32_bf16 v[220:223], a[8:11], a[184:187], v[220:223]\n" \
"ds_read_b64_tr_b16 a[8:9], v34 offset:9376\n" \
"ds_read_b64_tr_b16 a[10:11], v34 offset:13984\n" \
"v_mfma_f32_16x16x32_bf16 v[216:219], a[12:15], a[172:175], v[216:219]\n" \
"v_mfma_f32_16x16x32_bf16 v[220:223], a[12:15], a[188:191], v[220:223]\n" \
"ds_read_b64_tr_b16 a[12:13], v34 offset:18592\n" \
"ds_read_b64_tr_b16 a[14:15], v34 offset:23200\n" \
"s_waitcnt vmcnt(6)\n" \
"s_mov_b64 exec, 1\n" \
"buffer_store_dword v39, off, s[56:59], s92 offset:32 sc1\n" \
"s_mov_b64 exec, -1\n" \
"v_mfma_f32_16x16x32_bf16 v[216:219], a[16:19], a[96:99], v[216:219]\n" \
"v_mfma_f32_16x16x32_bf16 v[220:223], a[16:19], a[112:115], v[220:223]\n" \
"ds_read_b64_tr_b16 a[16:17], v10 offset:9376\n" \
"ds_read_b64_tr_b16 a[18:19], v10 offset:13984\n" \
"v_mfma_f32_16x16x32_bf16 v[216:219], a[20:23], a[100:103], v[216:219]\n" \
"v_mfma_f32_16x16x32_bf16 v[220:223], a[20:23], a[116:119], v[220:223]\n" \
"ds_read_b64_tr_b16 a[20:21], v33 offset:192\n" \
"ds_read_b64_tr_b16 a[22:23], v33 offset:4800\n" \
"v_mfma_f32_16x16x32_bf16 v[216:219], a[24:27], a[104:107], v[216:219]\n" \
"v_mfma_f32_16x16x32_bf16 v[220:223], a[24:27], a[120:123], v[220:223]\n" \
"ds_read_b64_tr_b16 a[24:25], v33 offset:9408\n" \
"ds_read_b64_tr_b16 a[26:27], v33 offset:14016\n" \
"v_mfma_f32_16x16x32_bf16 v[216:219], a[28:31], a[108:111], v[216:219]\n" \
"v_mfma_f32_16x16x32_bf16 v[220:223], a[28:31], a[124:127], v[220:223]\n" \
"buffer_load_dword v37, v40, s[56:59], s74 offen sc1\n" \
"v_mfma_f32_16x16x32_bf16 v[224:227], a[192:195], a[220:223], v[224:227]\n" \
"ds_read_b64_tr_b16 a[28:29], v33 offset:18624\n" \
"ds_read_b64_tr_b16 a[30:31], v33 offset:23232\n" \
"v_mfma_f32_16x16x32_bf16 v[228:231], a[192:195], a[236:239], v[228:231]\n" \
"ds_read_b64_tr_b16 a[192:193], v33 offset:27840\n" \
"s_waitcnt lgkmcnt(13)\n" \
"v_mfma_f32_16x16x32_bf16 v[224:227], a[196:199], a[224:227], v[224:227]\n" \
"v_cvt_pk_bf16_f32 v216, v216, v217\n" \
"v_cvt_pk_bf16_f32 v217, v218, v219\n" \
"v_cvt_pk_bf16_f32 v218, v220, v221\n" \
"v_cvt_pk_bf16_f32 v219, v222, v223\n" \
"buffer_store_dwordx4 v[216:219], v20, s[56:59], s96 offen offset:1024 sc1\n" \
"s_add_u32 s96, s96, 0x1800\n" \
"v_mfma_f32_16x16x32_bf16 v[228:231], a[196:199], a[240:243], v[228:231]\n" \
"ds_read_b64_tr_b16 a[194:195], v33 offset:32448\n" \
"ds_read_b64_tr_b16 a[196:197], v33 offset:37056\n" \
"v_mfma_f32_16x16x32_bf16 v[224:227], a[200:203], a[228:231], v[224:227]\n" \
"ds_read_b64_tr_b16 a[198:199], v33 offset:41664\n" \
"v_mfma_f32_16x16x32_bf16 v[228:231], a[200:203], a[244:247], v[228:231]\n" \
"ds_read_b64_tr_b16 a[200:201], v33 offset:46272\n" \
"ds_read_b64_tr_b16 a[202:203], v33 offset:50880\n" \
"v_mfma_f32_16x16x32_bf16 v[224:227], a[204:207], a[232:235], v[224:227]\n" \
"v_mfma_f32_16x16x32_bf16 v[228:231], a[204:207], a[248:251], v[228:231]\n" \
"ds_read_b64_tr_b16 a[204:205], v33 offset:55488\n" \
"ds_read_b64_tr_b16 a[206:207], v33 offset:60096\n" \
"v_mfma_f32_16x16x32_bf16 v[224:227], a[208:211], a[160:163], v[224:227]\n" \
"v_mfma_f32_16x16x32_bf16 v[228:231], a[208:211], a[176:179], v[228:231]\n" \
"ds_read_b64_tr_b16 a[208:209], v10 offset:192\n" \
"ds_read_b64_tr_b16 a[210:211], v10 offset:4800\n" \
"v_mfma_f32_16x16x32_bf16 v[224:227], a[212:215], a[164:167], v[224:227]\n" \
"v_mfma_f32_16x16x32_bf16 v[228:231], a[212:215], a[180:183], v[228:231]\n" \
"ds_read_b64_tr_b16 a[212:213], v34 offset:192\n" \
"ds_read_b64_tr_b16 a[214:215], v34 offset:4800\n" \
"v_mfma_f32_16x16x32_bf16 v[224:227], a[216:219], a[168:171], v[224:227]\n" \
"v_mfma_f32_16x16x32_bf16 v[228:231], a[216:219], a[184:187], v[228:231]\n" \
"ds_read_b64_tr_b16 a[216:217], v34 offset:9408\n" \
"ds_read_b64_tr_b16 a[218:219], v34 offset:14016\n" \
"v_mfma_f32_16x16x32_bf16 v[224:227], a[0:3], a[172:175], v[224:227]\n" \
"v_mfma_f32_16x16x32_bf16 v[228:231], a[0:3], a[188:191], v[228:231]\n" \
"ds_read_b64_tr_b16 a[0:1], v34 offset:18624\n" \
"ds_read_b64_tr_b16 a[2:3], v34 offset:23232\n" \
"v_mfma_f32_16x16x32_bf16 v[224:227], a[4:7], a[96:99], v[224:227]\n" \
"v_mfma_f32_16x16x32_bf16 v[228:231], a[4:7], a[112:115], v[228:231]\n" \
"ds_read_b64_tr_b16 a[4:5], v10 offset:9408\n" \
"s_waitcnt vmcnt(6)\n" \
"s_mov_b64 exec, 1\n" \
"buffer_store_dword v39, off, s[56:59], s93 offset:36 sc1\n" \
"s_mov_b64 exec, -1\n" \
"s_waitcnt lgkmcnt(13)\n" \
"v_mfma_f32_16x16x32_bf16 v[224:227], a[8:11], a[100:103], v[224:227]\n" \
"ds_read_b64_tr_b16 a[6:7], v10 offset:14016\n" \
"v_mfma_f32_16x16x32_bf16 v[228:231], a[8:11], a[116:119], v[228:231]\n" \
"ds_read_b64_tr_b16 a[8:9], v33 offset:224\n" \
"ds_read_b64_tr_b16 a[10:11], v33 offset:4832\n" \
"v_mfma_f32_16x16x32_bf16 v[224:227], a[12:15], a[104:107], v[224:227]\n" \
"v_mfma_f32_16x16x32_bf16 v[228:231], a[12:15], a[120:123], v[228:231]\n" \
"ds_read_b64_tr_b16 a[12:13], v33 offset:9440\n" \
"ds_read_b64_tr_b16 a[14:15], v33 offset:14048\n" \
"v_mfma_f32_16x16x32_bf16 v[224:227], a[16:19], a[108:111], v[224:227]\n" \
"v_mfma_f32_16x16x32_bf16 v[228:231], a[16:19], a[124:127], v[228:231]\n" \
"ds_read_b64_tr_b16 a[16:17], v33 offset:18656\n" \
"ds_read_b64_tr_b16 a[18:19], v33 offset:23264\n" \
"v_mfma_f32_16x16x32_bf16 v[232:235], a[20:23], a[220:223], v[232:235]\n" \
"v_mfma_f32_16x16x32_bf16 v[236:239], a[20:23], a[236:239], v[236:239]\n" \
"ds_read_b64_tr_b16 a[20:21], v33 offset:27872\n" \
"ds_read_b64_tr_b16 a[22:23], v33 offset:32480\n" \
"v_mfma_f32_16x16x32_bf16 v[232:235], a[24:27], a[224:227], v[232:235]\n" \
"v_cvt_pk_bf16_f32 v224, v224, v225\n" \
"v_cvt_pk_bf16_f32 v225, v226, v227\n" \
"v_cvt_pk_bf16_f32 v226, v228, v229\n" \
"v_cvt_pk_bf16_f32 v227, v230, v231\n" \
"buffer_store_dwordx4 v[224:227], v20, s[56:59], s71 offen offset:1024 sc1\n" \
"v_mfma_f32_16x16x32_bf16 v[236:239], a[24:27], a[240:243], v[236:239]\n" \
"ds_read_b64_tr_b16 a[24:25], v33 offset:37088\n" \
"ds_read_b64_tr_b16 a[26:27], v33 offset:41696\n" \
"v_mfma_f32_16x16x32_bf16 v[232:235], a[28:31], a[228:231], v[232:235]\n" \
"v_mfma_f32_16x16x32_bf16 v[236:239], a[28:31], a[244:247], v[236:239]\n" \
"ds_read_b64_tr_b16 a[28:29], v33 offset:46304\n" \
"ds_read_b64_tr_b16 a[30:31], v33 offset:50912\n" \
"v_mfma_f32_16x16x32_bf16 v[232:235], a[192:195], a[232:235], v[232:235]\n" \
"v_mfma_f32_16x16x32_bf16 v[236:239], a[192:195], a[248:251], v[236:239]\n" \
"ds_read_b64_tr_b16 a[192:193], v33 offset:55520\n" \
"ds_read_b64_tr_b16 a[194:195], v33 offset:60128\n" \
"v_mfma_f32_16x16x32_bf16 v[232:235], a[196:199], a[160:163], v[232:235]\n" \
"v_mfma_f32_16x16x32_bf16 v[236:239], a[196:199], a[176:179], v[236:239]\n" \
"ds_read_b64_tr_b16 a[196:197], v10 offset:224\n" \
"s_waitcnt lgkmcnt(13)\n" \
"v_mfma_f32_16x16x32_bf16 v[232:235], a[200:203], a[164:167], v[232:235]\n" \
"ds_read_b64_tr_b16 a[198:199], v10 offset:4832\n" \
"v_mfma_f32_16x16x32_bf16 v[236:239], a[200:203], a[180:183], v[236:239]\n" \
"ds_read_b64_tr_b16 a[200:201], v34 offset:224\n" \
"ds_read_b64_tr_b16 a[202:203], v34 offset:4832\n" \
"v_mfma_f32_16x16x32_bf16 v[232:235], a[204:207], a[168:171], v[232:235]\n" \
"v_mfma_f32_16x16x32_bf16 v[236:239], a[204:207], a[184:187], v[236:239]\n" \
"ds_read_b64_tr_b16 a[204:205], v34 offset:9440\n" \
"ds_read_b64_tr_b16 a[206:207], v34 offset:14048\n" \
"v_mfma_f32_16x16x32_bf16 v[232:235], a[208:211], a[172:175], v[232:235]\n" \
"v_mfma_f32_16x16x32_bf16 v[236:239], a[208:211], a[188:191], v[236:239]\n" \
"ds_read_b64_tr_b16 a[208:209], v34 offset:18656\n" \
"ds_read_b64_tr_b16 a[210:211], v34 offset:23264\n" \
"v_mfma_f32_16x16x32_bf16 v[232:235], a[212:215], a[96:99], v[232:235]\n" \
"v_mfma_f32_16x16x32_bf16 v[236:239], a[212:215], a[112:115], v[236:239]\n" \
"ds_read_b64_tr_b16 a[212:213], v10 offset:9440\n" \
"ds_read_b64_tr_b16 a[214:215], v10 offset:14048\n" \
"s_waitcnt vmcnt(7)\n" \
"s_mov_b64 exec, 1\n" \
"buffer_store_dword v39, off, s[56:59], s94 offset:40 sc1\n" \
"s_mov_b64 exec, -1\n" \
"v_mfma_f32_16x16x32_bf16 v[232:235], a[216:219], a[100:103], v[232:235]\n" \
"v_mfma_f32_16x16x32_bf16 v[236:239], a[216:219], a[116:119], v[236:239]\n" \
"ds_read_b64_tr_b16 a[216:217], v36 offset:0\n" \
"ds_read_b64_tr_b16 a[218:219], v36 offset:1024\n" \
"v_mfma_f32_16x16x32_bf16 v[232:235], a[0:3], a[104:107], v[232:235]\n" \
"v_mfma_f32_16x16x32_bf16 v[236:239], a[0:3], a[120:123], v[236:239]\n" \
"ds_read_b64_tr_b16 a[0:1], v36 offset:2048\n" \
"ds_read_b64_tr_b16 a[2:3], v36 offset:3072\n" \
"v_mfma_f32_16x16x32_bf16 v[232:235], a[4:7], a[108:111], v[232:235]\n" \
"v_mfma_f32_16x16x32_bf16 v[236:239], a[4:7], a[124:127], v[236:239]\n" \
"ds_read_b64_tr_b16 a[4:5], v36 offset:32\n" \
"ds_read_b64_tr_b16 a[6:7], v36 offset:1056\n" \
"v_mfma_f32_16x16x32_bf16 v[240:243], a[8:11], a[220:223], v[240:243]\n" \
"v_mfma_f32_16x16x32_bf16 v[244:247], a[8:11], a[236:239], v[244:247]\n" \
"ds_read_b64_tr_b16 a[8:9], v36 offset:2080\n" \
"s_waitcnt lgkmcnt(13)\n" \
"v_mfma_f32_16x16x32_bf16 v[240:243], a[12:15], a[224:227], v[240:243]\n" \
"v_cvt_pk_bf16_f32 v232, v232, v233\n" \
"v_cvt_pk_bf16_f32 v233, v234, v235\n" \
"v_cvt_pk_bf16_f32 v234, v236, v237\n" \
"v_cvt_pk_bf16_f32 v235, v238, v239\n" \
"buffer_store_dwordx4 v[232:235], v20, s[56:59], s72 offen offset:1024 sc1\n" \
"s_waitcnt vmcnt(5)\n" \
"v_cmp_eq_u32_e64 s[98:99], s62, v37\n" \
"s_mov_b64 exec, 1\n" \
"buffer_store_dword v39, off, s[56:59], s95 offset:44 sc1\n" \
"s_mov_b64 exec, -1\n" \
"v_mfma_f32_16x16x32_bf16 v[244:247], a[12:15], a[240:243], v[244:247]\n" \
"ds_read_b64_tr_b16 a[10:11], v36 offset:3104\n" \
"s_or_b32 s75, 0x7f, s100\n" \
"s_andn2_b32 s75, s75, s98\n" \
"s_cbranch_scc1 L_sx0_%=\n" \
"L_rx0_%=:\n" \
"buffer_load_dword v37, v40, s[56:59], s74 offen sc1\n" \
"s_add_u32 s97, s63, 0x0\n" \
"s_add_u32 s64, s63, 0x800\n" \
"s_add_u32 s65, s63, 0x1000\n" \
"s_add_u32 s66, s63, 0x1800\n" \
"s_add_u32 s67, s63, 0x2000\n" \
"s_add_u32 s68, s63, 0x2800\n" \
"s_add_u32 s69, s63, 0x3000\n" \
"s_add_u32 s71, s71, 0x1000\n" \
"s_add_u32 s72, s72, 0x800\n" \
"v_mfma_f32_16x16x32_bf16 v[240:243], a[16:19], a[228:231], v[240:243]\n" \
"v_mfma_f32_16x16x32_bf16 v[244:247], a[16:19], a[244:247], v[244:247]\n" \
"buffer_load_dwordx4 v[116:119], v20, s[56:59], s97 offen offset:0 sc1\n" \
"v_mfma_f32_16x16x32_bf16 v[240:243], a[20:23], a[232:235], v[240:243]\n" \
"v_mfma_f32_16x16x32_bf16 v[244:247], a[20:23], a[248:251], v[244:247]\n" \
"v_mfma_f32_16x16x32_bf16 v[168:171], a[24:27], a[128:131], v[168:171]\n" \
"v_mfma_f32_16x16x32_bf16 v[172:175], a[24:27], a[144:147], v[172:175]\n" \
"buffer_load_dwordx4 v[124:127], v20, s[56:59], s64 offen offset:0 sc1\n" \
"v_mfma_f32_16x16x32_bf16 v[240:243], a[24:27], a[160:163], v[240:243]\n" \
"v_readlane_b32 s64, v11, 54\n" \
"v_mfma_f32_16x16x32_bf16 v[244:247], a[24:27], a[176:179], v[244:247]\n" \
"v_mfma_f32_16x16x32_bf16 v[168:171], a[28:31], a[132:135], v[168:171]\n" \
"v_mfma_f32_16x16x32_bf16 v[172:175], a[28:31], a[148:151], v[172:175]\n" \
"buffer_load_dwordx4 v[132:135], v20, s[56:59], s65 offen offset:0 sc1\n" \
"v_mfma_f32_16x16x32_bf16 v[240:243], a[28:31], a[164:167], v[240:243]\n" \
"v_readlane_b32 s65, v11, 45\n" \
"v_mfma_f32_16x16x32_bf16 v[244:247], a[28:31], a[180:183], v[244:247]\n" \
"v_mfma_f32_16x16x32_bf16 v[168:171], a[192:195], a[136:139], v[168:171]\n" \
"v_mfma_f32_16x16x32_bf16 v[172:175], a[192:195], a[152:155], v[172:175]\n" \
"buffer_load_dwordx4 v[140:143], v20, s[56:59], s66 offen offset:0 sc1\n" \
"v_mfma_f32_16x16x32_bf16 v[240:243], a[192:195], a[168:171], v[240:243]\n" \
"v_readlane_b32 s66, v11, 43\n" \
"v_mfma_f32_16x16x32_bf16 v[244:247], a[192:195], a[184:187], v[244:247]\n" \
"v_mfma_f32_16x16x32_bf16 v[168:171], a[196:199], a[140:143], v[168:171]\n" \
"v_mfma_f32_16x16x32_bf16 v[172:175], a[196:199], a[156:159], v[172:175]\n" \
"buffer_load_dwordx4 v[148:151], v20, s[56:59], s67 offen offset:0 sc1\n" \
"v_mfma_f32_16x16x32_bf16 v[240:243], a[196:199], a[172:175], v[240:243]\n" \
"v_readlane_b32 s67, v11, 44\n" \
"v_mfma_f32_16x16x32_bf16 v[244:247], a[196:199], a[188:191], v[244:247]\n" \
"v_mfma_f32_16x16x32_bf16 v[168:171], a[200:203], a[32:35], v[168:171]\n" \
"v_mfma_f32_16x16x32_bf16 v[172:175], a[200:203], a[48:51], v[172:175]\n" \
"buffer_load_dwordx4 v[156:159], v20, s[56:59], s68 offen offset:0 sc1\n" \
"v_mfma_f32_16x16x32_bf16 v[240:243], a[200:203], a[96:99], v[240:243]\n" \
"v_mfma_f32_16x16x32_bf16 v[244:247], a[200:203], a[112:115], v[244:247]\n" \
"s_waitcnt lgkmcnt(0)\n" \
"v_mfma_f32_16x16x32_bf16 v[168:171], a[204:207], a[36:39], v[168:171]\n" \
"v_mfma_f32_16x16x32_bf16 v[172:175], a[204:207], a[52:55], v[172:175]\n" \
"buffer_load_dwordx4 v[164:167], v20, s[56:59], s69 offen offset:0 sc1\n" \
"v_mfma_f32_16x16x32_bf16 v[240:243], a[204:207], a[100:103], v[240:243]\n" \
"v_mfma_f32_16x16x32_bf16 v[244:247], a[204:207], a[116:119], v[244:247]\n" \
"v_mfma_f32_16x16x32_bf16 v[168:171], a[208:211], a[40:43], v[168:171]\n" \
"v_mfma_f32_16x16x32_bf16 v[172:175], a[208:211], a[56:59], v[172:175]\n" \
"buffer_load_dwordx4 v[112:115], v20, s[56:59], s70 offen offset:0 sc1\n" \
"v_mfma_f32_16x16x32_bf16 v[240:243], a[208:211], a[104:107], v[240:243]\n" \
"v_mfma_f32_16x16x32_bf16 v[244:247], a[208:211], a[120:123], v[244:247]\n" \
"v_mfma_f32_16x16x32_bf16 v[168:171], a[212:215], a[44:47], v[168:171]\n" \
"v_mfma_f32_16x16x32_bf16 v[172:175], a[212:215], a[60:63], v[172:175]\n" \
"buffer_load_dwordx4 v[184:187], v20, s[56:59], s70 offen offset:2048 sc1\n" \
"s_add_u32 s70, s70, 0x1000\n" \
"v_mfma_f32_16x16x32_bf16 v[240:243], a[212:215], a[108:111], v[240:243]\n" \
"v_mfma_f32_16x16x32_bf16 v[244:247], a[212:215], a[124:127], v[244:247]\n" \
"s_waitcnt vmcnt(15)\n" \
"s_mov_b64 exec, 1\n" \
"buffer_store_dword v39, off, s[56:59], s96 offset:48 sc1\n" \
"s_mov_b64 exec, -1\n" \
"v_mfma_f32_16x16x32_bf16 v[168:171], a[216:219], v[64:67], v[168:171]\n" \
"v_mfma_f32_16x16x32_bf16 v[172:175], a[216:219], v[72:75], v[172:175]\n" \
"v_mfma_f32_16x16x32_bf16 v[240:243], a[216:219], v[80:83], v[240:243]\n" \
"v_mfma_f32_16x16x32_bf16 v[244:247], a[216:219], v[88:91], v[244:247]\n" \
"buffer_load_dwordx4 v[120:123], v20, s[56:59], s70 offen offset:0 sc1\n" \
"v_mfma_f32_16x16x32_bf16 v[168:171], a[0:3], v[68:71], v[168:171]\n" \
"v_mfma_f32_16x16x32_bf16 v[172:175], a[0:3], v[76:79], v[172:175]\n" \
"v_mfma_f32_16x16x32_bf16 v[240:243], a[0:3], v[84:87], v[240:243]\n" \
"v_mfma_f32_16x16x32_bf16 v[244:247], a[0:3], v[92:95], v[244:247]\n" \
"buffer_load_dwordx4 v[192:195], v20, s[56:59], s70 offen offset:2048 sc1\n" \
"s_add_u32 s70, s70, 0x1000\n" \
"v_mfma_f32_16x16x32_bf16 v[56:59], a[4:7], v[96:99], 0\n" \
"v_mfma_f32_16x16x32_bf16 v[60:63], a[4:7], v[104:107], 0\n" \
"v_mfma_f32_16x16x32_bf16 v[56:59], a[8:11], v[100:103], v[56:59]\n" \
"v_mfma_f32_16x16x32_bf16 v[60:63], a[8:11], v[108:111], v[60:63]\n" \
"buffer_load_dwordx4 v[128:131], v20, s[56:59], s70 offen offset:0 sc1\n" \
"buffer_load_dwordx4 v[200:203], v20, s[56:59], s70 offen offset:2048 sc1\n" \
".fill (L_ab + 45056 - .) / 4, 4, 0xBF800000\n" \
"L_ap11:\n" \
"s_cbranch_execz L_ap0\n" \
"s_add_u32 s70, s70, 0x1000\n" \
"buffer_load_dwordx4 v[176:179], v20, s[56:59], s70 offen offset:0 sc1\n" \
"buffer_load_dwordx4 v[248:251], v20, s[56:59], s70 offen offset:2048 sc1\n" \
"s_waitcnt vmcnt(15)\n" \
"v_mfma_f32_16x16x32_bf16 v[168:171], v[50:53], v[116:119], v[168:171]\n" \
"s_mov_b64 exec, 1\n" \
"buffer_store_dword v39, off, s[56:59], s71 offset:52 sc1\n" \
"s_mov_b64 exec, -1\n" \
"s_mov_b64 exec, 1\n" \
"buffer_store_dword v39, off, s[56:59], s72 offset:56 sc1\n" \
"s_mov_b64 exec, -1\n" \
"v_cmp_eq_u32_e64 s[98:99], s62, v37\n" \
"v_mfma_f32_16x16x32_bf16 v[172:175], v[48:51], v[116:119], v[172:175]\n" \
"s_andn2_b32 s75, 0x100, s98\n" \
"s_cbranch_scc1 L_sx1_%=\n" \
"L_rx1_%=:\n" \
"s_add_u32 s73, s63, 0x0\n" \
"s_waitcnt vmcnt(16)\n" \
"v_mfma_f32_16x16x32_bf16 v[168:171], v[50:53], v[124:127], v[168:171]\n" \
"v_mfma_f32_16x16x32_bf16 v[172:175], v[48:51], v[124:127], v[172:175]\n" \
"buffer_load_dwordx4 v[188:191], v20, s[56:59], s73 offen offset:1024 sc1\n" \
"s_andn2_b32 s75, 0x200, s98\n" \
"s_cbranch_scc1 L_sx2_%=\n" \
"L_rx2_%=:\n" \
"s_add_u32 s92, s63, 0x800\n" \
"s_waitcnt vmcnt(16)\n" \
"v_mfma_f32_16x16x32_bf16 v[168:171], v[50:53], v[132:135], v[168:171]\n" \
"v_mfma_f32_16x16x32_bf16 v[172:175], v[48:51], v[132:135], v[172:175]\n" \
"buffer_load_dwordx4 v[196:199], v20, s[56:59], s92 offen offset:1024 sc1\n" \
"s_andn2_b32 s75, 0x400, s98\n" \
"s_cbranch_scc1 L_sx3_%=\n" \
"L_rx3_%=:\n" \
"s_add_u32 s93, s63, 0x1000\n" \
"s_waitcnt vmcnt(16)\n" \
"v_mfma_f32_16x16x32_bf16 v[168:171], v[50:53], v[140:143], v[168:171]\n" \
"v_mfma_f32_16x16x32_bf16 v[172:175], v[48:51], v[140:143], v[172:175]\n" \
"buffer_load_dwordx4 v[204:207], v20, s[56:59], s93 offen offset:1024 sc1\n" \
"s_andn2_b32 s75, 0x800, s98\n" \
"s_cbranch_scc1 L_sx4_%=\n" \
"L_rx4_%=:\n" \
"s_add_u32 s94, s63, 0x1800\n" \
"s_waitcnt vmcnt(16)\n" \
"v_mfma_f32_16x16x32_bf16 v[168:171], v[50:53], v[148:151], v[168:171]\n" \
"v_mfma_f32_16x16x32_bf16 v[172:175], v[48:51], v[148:151], v[172:175]\n" \
"buffer_load_dwordx4 v[212:215], v20, s[56:59], s94 offen offset:1024 sc1\n" \
"s_andn2_b32 s75, 0x1000, s98\n" \
"s_cbranch_scc1 L_sx5_%=\n" \
"L_rx5_%=:\n" \
"s_add_u32 s95, s63, 0x2000\n" \
"s_waitcnt vmcnt(16)\n" \
"v_mfma_f32_16x16x32_bf16 v[168:171], v[50:53], v[156:159], v[168:171]\n" \
"v_mfma_f32_16x16x32_bf16 v[172:175], v[48:51], v[156:159], v[172:175]\n" \
"buffer_load_dwordx4 v[220:223], v20, s[56:59], s95 offen offset:1024 sc1\n" \
"s_andn2_b32 s75, 0x2000, s98\n" \
"s_cbranch_scc1 L_sx6_%=\n" \
"L_rx6_%=:\n" \
"s_add_u32 s96, s63, 0x2800\n" \
"s_waitcnt vmcnt(16)\n" \
"v_mfma_f32_16x16x32_bf16 v[168:171], v[50:53], v[164:167], v[168:171]\n" \
"v_mfma_f32_16x16x32_bf16 v[172:175], v[48:51], v[164:167], v[172:175]\n" \
"buffer_load_dwordx4 v[228:231], v20, s[56:59], s96 offen offset:1024 sc1\n" \
"s_andn2_b32 s75, 0x4000, s98\n" \
"s_cbranch_scc1 L_sx7_%=\n" \
"L_rx7_%=:\n" \
"s_add_u32 s97, s63, 0x3000\n" \
"s_waitcnt vmcnt(16)\n" \
"v_mfma_f32_16x16x32_bf16 v[56:59], v[50:53], v[112:115], v[56:59]\n" \
"v_mfma_f32_16x16x32_bf16 v[60:63], v[48:51], v[112:115], v[60:63]\n" \
"buffer_load_dwordx4 v[236:239], v20, s[56:59], s97 offen offset:1024 sc1\n" \
"v_cvt_pk_bf16_f32 v168, v168, v169\n" \
"v_cvt_pk_bf16_f32 v172, v172, v173\n" \
"v_cvt_pk_bf16_f32 v169, v170, v171\n" \
"v_cvt_pk_bf16_f32 v173, v174, v175\n" \
"buffer_store_dwordx2 v[168:169], v42, s[88:91], s66 offen sc1\n" \
"buffer_store_dwordx2 v[172:173], v43, s[88:91], s66 offen sc1\n" \
"s_waitcnt vmcnt(18)\n" \
"v_mfma_f32_16x16x32_bf16 v[56:59], v[50:53], v[184:187], v[56:59]\n" \
"v_mfma_f32_16x16x32_bf16 v[60:63], v[48:51], v[184:187], v[60:63]\n" \
"s_waitcnt vmcnt(16)\n" \
"v_mfma_f32_16x16x32_bf16 v[56:59], v[50:53], v[120:123], v[56:59]\n" \
"v_mfma_f32_16x16x32_bf16 v[60:63], v[48:51], v[120:123], v[60:63]\n" \
"s_waitcnt vmcnt(15)\n" \
"v_mfma_f32_16x16x32_bf16 v[56:59], v[50:53], v[192:195], v[56:59]\n" \
"v_mfma_f32_16x16x32_bf16 v[60:63], v[48:51], v[192:195], v[60:63]\n" \
"s_waitcnt vmcnt(14)\n" \
"v_mfma_f32_16x16x32_bf16 v[56:59], v[50:53], v[128:131], v[56:59]\n" \
"v_mfma_f32_16x16x32_bf16 v[60:63], v[48:51], v[128:131], v[60:63]\n" \
"s_waitcnt vmcnt(13)\n" \
"v_mfma_f32_16x16x32_bf16 v[56:59], v[50:53], v[200:203], v[56:59]\n" \
"v_mfma_f32_16x16x32_bf16 v[60:63], v[48:51], v[200:203], v[60:63]\n" \
"s_waitcnt vmcnt(12)\n" \
"v_mfma_f32_16x16x32_bf16 v[56:59], v[50:53], v[176:179], v[56:59]\n" \
"v_mfma_f32_16x16x32_bf16 v[60:63], v[48:51], v[176:179], v[60:63]\n" \
"s_waitcnt vmcnt(11)\n" \
"v_mfma_f32_16x16x32_bf16 v[56:59], v[50:53], v[248:251], v[56:59]\n" \
"v_mfma_f32_16x16x32_bf16 v[60:63], v[48:51], v[248:251], v[60:63]\n" \
"s_mov_b64 exec, 1\n" \
"buffer_store_dword v46, off, s[56:59], s64 offset:0 sc1\n" \
"s_mov_b64 exec, -1\n" \
"s_waitcnt vmcnt(9)\n" \
"v_mfma_f32_16x16x32_bf16 v[240:243], v[50:53], v[188:191], v[240:243]\n" \
"v_mfma_f32_16x16x32_bf16 v[244:247], v[48:51], v[188:191], v[244:247]\n" \
"s_waitcnt vmcnt(8)\n" \
"v_mfma_f32_16x16x32_bf16 v[240:243], v[50:53], v[196:199], v[240:243]\n" \
"v_cvt_pk_bf16_f32 v56, v56, v57\n" \
"v_cvt_pk_bf16_f32 v60, v60, v61\n" \
"v_mfma_f32_16x16x32_bf16 v[244:247], v[48:51], v[196:199], v[244:247]\n" \
"v_cvt_pk_bf16_f32 v57, v58, v59\n" \
"v_cvt_pk_bf16_f32 v61, v62, v63\n" \
"buffer_store_dwordx2 v[56:57], v42, s[88:91], s65 offen sc1\n" \
"buffer_store_dwordx2 v[60:61], v43, s[88:91], s65 offen sc1\n" \
"s_waitcnt vmcnt(9)\n" \
"v_mfma_f32_16x16x32_bf16 v[240:243], v[50:53], v[204:207], v[240:243]\n" \
"v_mfma_f32_16x16x32_bf16 v[244:247], v[48:51], v[204:207], v[244:247]\n" \
"s_waitcnt vmcnt(8)\n" \
"v_mfma_f32_16x16x32_bf16 v[240:243], v[50:53], v[212:215], v[240:243]\n" \
"v_mfma_f32_16x16x32_bf16 v[244:247], v[48:51], v[212:215], v[244:247]\n" \
"s_waitcnt vmcnt(7)\n" \
"v_mfma_f32_16x16x32_bf16 v[240:243], v[50:53], v[220:223], v[240:243]\n" \
"v_mfma_f32_16x16x32_bf16 v[244:247], v[48:51], v[220:223], v[244:247]\n" \
"s_waitcnt vmcnt(6)\n" \
"v_mfma_f32_16x16x32_bf16 v[240:243], v[50:53], v[228:231], v[240:243]\n" \
"v_mfma_f32_16x16x32_bf16 v[244:247], v[48:51], v[228:231], v[244:247]\n" \
"s_waitcnt vmcnt(5)\n" \
"v_mfma_f32_16x16x32_bf16 v[240:243], v[50:53], v[236:239], v[240:243]\n" \
"v_mfma_f32_16x16x32_bf16 v[244:247], v[48:51], v[236:239], v[244:247]\n" \
"s_nop 6\n" \
"v_cvt_pk_bf16_f32 v240, v240, v241\n" \
"v_cvt_pk_bf16_f32 v244, v244, v245\n" \
"v_cvt_pk_bf16_f32 v241, v242, v243\n" \
"v_cvt_pk_bf16_f32 v245, v246, v247\n" \
"buffer_store_dwordx2 v[240:241], v42, s[88:91], s67 offen sc1\n" \
"buffer_store_dwordx2 v[244:245], v43, s[88:91], s67 offen sc1\n"
#define ATT_VCLOB "v21","v22","v23","v24","v25","v26","v27","v28","v29","v32","v33","v34","v35","v36","v37","v38","v39","v40","v41","v42","v43","v44","v45","v46","v47","v48","v49","v50","v51","v52","v53","v54","v55","v56","v57","v58","v59","v60","v61","v62","v63","v64","v65","v66","v67","v68","v69","v70","v71","v72","v73","v74","v75","v76","v77","v78","v79","v80","v81","v82","v83","v84","v85","v86","v87","v88","v89","v90","v91","v92","v93","v94","v95","v96","v97","v98","v99","v100","v101","v102","v103","v104","v105","v106","v107","v108","v109","v110","v111","v112","v113","v114","v115","v116","v117","v118","v119","v120","v121","v122","v123","v124","v125","v126","v127","v128","v129","v130","v131","v132","v133","v134","v135","v136","v137","v138","v139","v140","v141","v142","v143","v144","v145","v146","v147","v148","v149","v150","v151","v152","v153","v154","v155","v156","v157","v158","v159","v160","v161","v162","v163","v164","v165","v166","v167","v168","v169","v170","v171","v172","v173","v174","v175","v176","v177","v178","v179","v180","v181","v182","v183","v184","v185","v186","v187","v188","v189","v190","v191","v192","v193","v194","v195","v196","v197","v198","v199","v200","v201","v202","v203","v204","v205","v206","v207","v208","v209","v210","v211","v212","v213","v214","v215","v216","v217","v218","v219","v220","v221","v222","v223","v224","v225","v226","v227","v228","v229","v230","v231","v232","v233","v234","v235","v236","v237","v238","v239","v240","v241","v242","v243","v244","v245","v246","v247","v248","v249","v250","v251","v252","v253","v254","v255"
#define ATT_SCLOB "s63","s64","s65","s66","s67","s68","s69","s70","s71","s72","s73","s74","s75","s76","s77","s78","s79","s80","s81","s82","s83","s84","s85","s86","s87","s88","s89","s90","s91","s92","s93","s95","s96","s97","s98","s99","s100","s101"
#define ATT_WTCH 0
#define ATT_PEEL 1
enum { T_fo, T_fj, T_fa, T_fas, T_fb, T_fbs, T_f8, T_mk8, T_wt, T_wol, T_woh, T_wbl, T_wbh, T_outl, T_outh, T_r1l, T_r1h, T_r1n, T_r1s, T_m0, T_m1, T_m1b, T_mw, T_m3, T_m4, T_ml, T_w0, T_w1, T_w2, T_w3, T_A1, T_A2, T_A3, T_B1, T_B2, T_B3, T_Ao, T_Bo, T_Al, T_Bl, T_l8, T_xr, T_x8r, T_oA, T_oB, T_o8, T_x0, T_x1, T_x2, T_x3, T_x4, T_x5, T_x6, T_x8, T_ep };

__device__ __forceinline__ void out_consts(u32x4 (&vp)[3], const u16* wo, const u16* wob, const u16* out, const unsigned b, const unsigned w, const unsigned lane) {
    const unsigned sp = b & 7u, nt = (b >> 3) & 3u, j = b >> 5, head = 4u * sp + nt, OOR = 0x40000000u;
    unsigned vw[9];
#pragma unroll
    for (unsigned i = 0; i < 9u; ++i) {
        const unsigned q = 64u * i + lane, row = __umul24(q, 3641u) >> 16, c = q - __umul24(row, 18u);
        vw[i] = __umul24(row, DM * 2) + (c < 16u ? (c * 16u + sp * 32u + 32u) & 255u : c * 16u) - (i & 3u) * 1024u;
    }
    const unsigned row = 16u * w + (lane >> 2), r9 = __umul24(16u * (row >> 5) + (row & 12u) + 2u * ((row >> 4) & 1u) + ((row >> 1) & 1u), 18u) + 16u + (row & 1u);
    unsigned vwl = __umul24(r9, DM * 2) + ((lane & 2u) ? 256u : sp * 32u) + (lane & 1u) * 16u, vrd = __umul24(lane >> 2, 288u) + (lane & 3u) * 8u;
    unsigned tab = 0;
#define TB(n, v) asm("v_writelane_b32 %0, %1, %2" : "+v"(tab) : "s"((unsigned)(v)), "n"((int)T_##n))
    const unsigned FJ = (unsigned)WS_F + (F_J + 640u * j) * 4u, mt = 2u * (w & 2u) + (w & 1u), w2 = w < 2u ? w : 2u, hw = head * 147456u;
    const unsigned FR = (unsigned)WS_F + F_R * 4u + (8u * j + sp) * 1024u;
    TB(fo, FJ + 4u * head); TB(fj, FJ); TB(fa, FR + (w | 2u) * 256u); TB(fas, FR + w * 256u + nt * 4u);
    TB(fb, FR + 128u + w * 256u); TB(fbs, FR + 128u + w * 256u + nt * 4u);
    TB(ep, ssel(b + w, OOR, (unsigned)WS_F + F_EPOCH * 4u));
    const u16* pwo = wo + nt * 144u; const u16* pwb = wob + nt * 144u;
    TB(wol, PLO(pwo)); TB(woh, PHI(pwo) & 0xffffu); TB(wbl, PLO(pwb)); TB(wbh, PHI(pwb) & 0xffffu); TB(outl, PLO(out)); TB(outh, PHI(out) & 0xffffu); TB(r1l, sslt<3>(w, PLO(pwo), PLO(pwb))); TB(r1h, sslt<3>(w, PHI(pwo), PHI(pwb)) & 0xffffu);
    TB(r1n, sslt<3>(w, 72u * sp + 71u, 4095u) * 1152u + 288u); TB(r1s, sslt<3>(w, (72u * sp + 32u * w) * 1152u, hw + 110592u));
    TB(m0, 131072u + 9216u * w2); TB(w0, hw + 36864u * w2); TB(m1, ATT_PEEL ? sslt<3>(w, 41984u + 9216u * w, 32768u) : 24576u + 9216u * w); TB(m1b, ssel(w ^ 2u, sslt<3>(w, 41984u + 9216u * w, 32768u), 114688u - 4096u)); TB(mw, 65536u + 9216u * w); TB(m3, sslt<3>(w, 102400u + 9216u * w, 0u)); TB(m4, sslt<3>(w, 131072u + 9216u * w, 9216u)); TB(ml, (ATT_PEEL ? 18432u : 61440u) + 1024u * w);
    const unsigned tA = (unsigned)WS_AF + (8u * j + mt) * (KS_TOT * 1024u), tB = (unsigned)WS_AF + (120u - 8u * j + mt) * (KS_TOT * 1024u);
#define HK(k) (4u * sp + ((nt + k) & 3u))
    TB(w1, HK(1u) * 147456u + w * 36864u); TB(w2, HK(2u) * 147456u + w * 36864u); TB(w3, HK(3u) * 147456u + w * 36864u);
    TB(A1, tA + 18432u + HK(1u) * 4096u); TB(A2, tA + 18432u + HK(2u) * 4096u); TB(A3, tA + 18432u + HK(3u) * 4096u);
    TB(B1, tB + 18432u + HK(1u) * 4096u); TB(B2, tB + 18432u + HK(2u) * 4096u); TB(B3, tB + 18432u + HK(3u) * 4096u);
    TB(Ao, tA + sp * 2048u); TB(Bo, tB + sp * 2048u); TB(Al, tA + 16384u); TB(Bl, tB + 16384u);
    const unsigned nred = w ^ (sp >> 1), blk = sp & 1u;
    const unsigned xg = (unsigned)WS_X + (4u * j + nt) * 589824u + w * 16384u, oA = (128u * j + 16u * mt) * 1152u + nt * 288u, oB = (1920u - 128u * j + 16u * mt) * 1152u + nt * 288u;
    TB(l8, ssel(nred, OOR, ssel(blk, tB, tA) + 16384u)); TB(xr, xg + sp * 65536u); TB(x8r, ssel(nred, OOR, xg + 524288u + (sp & 1u) * 1024u));
    TB(oA, oA + sp * 32u); TB(oB, oB + sp * 32u); TB(o8, ssel(nred, OOR, ssel(blk, oB, oA) + 256u)); TB(x8, xg + 524288u + sp * 2048u);
    TB(f8, xg + w * 131072u + 14400u + sp * 4u); TB(mk8, ssel(nred, 0u, 0xff0000u)); TB(wt, hw + j * 18432u + w * 4608u);
#define XI(i) TB(x##i, xg + ((sp + 1u + i) & 7u) * 65536u + i * 2048u)
    XI(0); XI(1); XI(2); XI(3); XI(4); XI(5); XI(6);
#undef XI
#undef HK
#undef TB
    vp[0] = (u32x4){vw[0], vw[1], vw[2], vw[3]}; vp[1] = (u32x4){vw[4], vw[5], vw[6], vw[7]}; vp[2] = (u32x4){vw[8], vwl, vrd, tab};
}
__global__ __launch_bounds__(256, 1) __attribute__((aligned(4096))) void fused_kernel(const u16* __restrict__ x, const u16* __restrict__ ckv, const u16* __restrict__ wq, const u16* __restrict__ wuk,
                                                       const u16* __restrict__ wuv, const u16* __restrict__ wqkv, const u16* __restrict__ gw,
                                                       const u16* __restrict__ wo, const u16* __restrict__ wob, char* __restrict__ ws_, u16* __restrict__ out) {
    using namespace att;
    extern __shared__ __attribute__((aligned(16))) char lds[];
    u32x4 sa[5]; u32x2 s24, s28; char* ws; unsigned tdum; kaptr ka = (kaptr)__builtin_amdgcn_kernarg_segment_ptr();
    asm volatile("s_load_dwordx8 s[8:15], %[ka], 0x0\n\ts_load_dwordx8 s[16:23], %[ka], 0x20\n\ts_getpc_b64 s[4:5]\n\tv_lshlrev_b32_e32 v191, 7, %[t]\n\ts_bfe_u32 s6, %[b], 0x20003\n\t"
                 "v_lshl_add_u32 v191, s6, 15, v191\n\tv_min_u32_e32 v191, %[e], v191\n\tglobal_load_dword v191, v191, s[4:5]\n\ts_load_dwordx4 s[24:27], %[ka], 0x40\n\ts_load_dwordx2 s[28:29], %[ka], 0x50"
                 : "={s[8:11]}"(sa[0]), "={s[12:15]}"(sa[1]), "={s[16:19]}"(sa[2]), "={s[20:23]}"(sa[3]), "={s[24:25]}"(s24), "={s[26:27]}"(ws), "={s[28:29]}"(s28), "={v191}"(tdum), [ka] "+{s[0:1]}"(ka)
                 : [t] "v"(threadIdx.x), [b] "s"(blockIdx.x), [e] "i"(CT_END) : "s4", "s5", "s6", "scc");
    SBAR();
    const unsigned wid = rfl(threadIdx.x >> 6);
    unsigned tag, vl;
    const int b = blockIdx.x;
    u32x4 vp[5], srdA; u32x2 sd;
    proj_phase(ka, ws, wid, vp, vl, sa, s24, s28, tdum, srdA, sd, tag
    );
    unsigned* const flags = (unsigned*)(ws + WS_F);
    RTS(2);
#define AST(L) do {} while (0)
#define ATT_TAIL "s_endpgm\n"
    unsigned abad;
    asm volatile(ATT_ASM ATT_TAIL "L_end_%=:\n s_nop 1"
                 : "={s94}"(abad)
                 : "{v[0:3]}"(vp[0]), "{v[4:7]}"(vp[1]), "{v[8:11]}"(vp[2]), "{v[12:15]}"(vp[3]), "{v[16:19]}"(vp[4]), "{v20}"(vl), "{s[56:59]}"(srdA), "{s[60:61]}"(sd), "{s62}"(tag),
                   "{s[8:11]}"(sa[0]), "{s[12:15]}"(sa[1]), "{s[16:19]}"(sa[2]), "{s[20:23]}"(sa[3]), "{s24}"(sa[4][0]), "{s3}"(wid), "{s[28:29]}"(s28)
                 : "memory", "m0", "vcc", "scc", ATT_VCLOB, ATT_SCLOB, ACLOB_LO, ACLOB_HI);
    {
        unsigned b2 = b, w2 = wid;
        asm volatile("s_waitcnt vmcnt(0)" : "+s"(ka), "+s"(b2), "+s"(w2), "+s"(ws) :: "memory");
        const unsigned j2 = b2 >> 5, p2 = w2 & 1u, h2 = (b2 & 7u) * 4u + ((b2 >> 3) & 3u);
#pragma unroll 1
        for (unsigned i = w2 & 2u; i < ((w2 & 2u) ? (abad ? 8u : 4u) : 2u); ++i)
            att_exact(ka[0], ka[2], ws, h2, (i < 4u ? 1920u - 128u * j2 : 128u * j2 - 128u) + 32u * i + 16u * p2, *reinterpret_cast<const float*>(lds + LDS_G + (32u * i + 16u * p2 + (lane_id() & 15u)) * 4u));
        asm volatile("s_waitcnt vmcnt(0)" ::: "memory");
        unsigned* fa = (unsigned*)(ws + WS_F) + F_R + (8u * j2 + (b2 & 7u)) * 256u + (2u + p2) * 64u + ((b2 >> 3) & 3u);
        if (w2 & 2u) st_flag(fa, tag);
        wait_flags(fa, ld_flag(fa), tag);
        const unsigned l2 = lane_id(); u32x4 c[3]; out_consts(c, ka[7], ka[8], ka[10], b2, w2, l2);
        asm volatile("s_waitcnt vmcnt(0) lgkmcnt(0)\n\ts_nop 7\n\ts_branch L_att_stub" :: "{v[0:3]}"(c[0]), "{v[4:7]}"(c[1]), "{v[8:11]}"(c[2]), "{v20}"(l2 * 16u), "{s[56:59]}"(SRDA), "{s60}"(SD(8u * j2, h2, w2)),
                     "{s61}"(SD(120u - 8u * j2, h2, w2)), "{s62}"(tag) : "memory");
    }
    RTS(5);
}

extern "C" void kernel_launch(void* const* d_in, const int* in_sizes, int n_in, void* d_out, int out_size, void* d_ws, size_t ws_size,
                              hipStream_t stream) {
    (void)hipFuncSetAttribute((const void*)fused_kernel, hipFuncAttributeMaxDynamicSharedMemorySize, att::LDS_BYTES);
    fused_kernel<<<dim3(256), 256, att::LDS_BYTES, stream>>>((const u16*)d_in[0], (const u16*)d_in[4], (const u16*)d_in[5], (const u16*)d_in[6], (const u16*)d_in[7], (const u16*)d_in[1],
                                                             (const u16*)d_in[9], (const u16*)d_in[2], (const u16*)d_in[8], (char*)d_ws, (u16*)d_out);
}
```

```cpp
#include <hip/hip_runtime.h>
#include <hip/hip_bf16.h>
#include <stdint.h>
#include <utility>

typedef short bf16x8 __attribute__((ext_vector_type(8)));
typedef short s16x4 __attribute__((ext_vector_type(4)));
typedef float f32x16 __attribute__((ext_vector_type(16)));
typedef float f32x4 __attribute__((ext_vector_type(4)));
typedef unsigned u32x4 __attribute__((ext_vector_type(4)));
typedef unsigned u32x2 __attribute__((ext_vector_type(2)));
typedef unsigned short u16;

constexpr int S = 2048, DM = 576, NH = 32, HD = 128, DC = 512, NQKV = 1728, DHA = 18;
constexpr int NBIG = NH * HD;
constexpr int KS_TOT = 146;

#define SBAR() __builtin_amdgcn_sched_barrier(0)
constexpr size_t WS_Q = 0, WS_K = WS_Q + (size_t)S * NBIG * 2, WS_V = WS_K + (size_t)S * NBIG * 2, WS_AF = WS_V + (size_t)S * NBIG * 2, WS_X = WS_AF + (size_t)128 * KS_TOT * 1024,
                 WS_P = WS_X + (size_t)64 * 16 * 9216, WS_F = WS_P + (size_t)128 * (4 * 32 * 256 + 1024) * 4;
constexpr size_t WS_L = (size_t)96 << 20;
static_assert(WS_F == 96206848, "workspace map is baked into the generated asm");
constexpr int F_EPOCH = 0, F_J = 1024, F_ST = 8192, F_KV = 40960, F_R = 65536;
constexpr unsigned TAG_STEP = 0x9E3779B1u;
__device__ __forceinline__ unsigned ld_flag(const unsigned* p) { return __hip_atomic_load(p, __ATOMIC_RELAXED, __HIP_MEMORY_SCOPE_AGENT); }
__device__ __forceinline__ void st_flag(unsigned* p, unsigned v) { __hip_atomic_store(p, v, __ATOMIC_RELAXED, __HIP_MEMORY_SCOPE_AGENT); }
__device__ __forceinline__ unsigned ld_flag_async(const unsigned* p) { unsigned v; asm volatile("global_load_dword %0, %1, off sc1" : "=v"(v) : "v"(p) : "memory"); return v; }
#define WAITV(v, N) asm volatile("s_waitcnt vmcnt(" #N ")" : "+v"(v) :: "memory")
__device__ __forceinline__ void wait_flags(const unsigned* p, unsigned v, unsigned tag) {
    if (__builtin_expect(__builtin_amdgcn_ballot_w64(v != tag) != 0ull, 0)) {
        unsigned spins = 0;
        do { __builtin_amdgcn_s_sleep(1); } while (__builtin_amdgcn_ballot_w64(ld_flag(p) != tag) != 0ull && ++spins < (1u << 22));
    }
    asm volatile("" ::: "memory");
}

__device__ __forceinline__ unsigned cvtpk(float lo, float hi) {
    unsigned r; asm volatile("v_cvt_pk_bf16_f32 %0, %1, %2" : "=v"(r) : "v"(lo), "v"(hi)); return r;
}
__device__ __forceinline__ float bf2f(u16 v) { return __uint_as_float(((unsigned)v) << 16); }
__device__ __forceinline__ int crow(int r, int hi) { return (r & 3) + 8 * (r >> 2) + 4 * hi; }

typedef __attribute__((address_space(3))) s16x4* lds_s16x4_p;
__device__ __forceinline__ s16x4 trrd(unsigned addr) { return __builtin_amdgcn_ds_read_tr16_b64_v4i16((lds_s16x4_p)addr); }

__device__ __forceinline__ unsigned lane_id() { unsigned l; asm volatile("v_mbcnt_lo_u32_b32 %0, -1, 0\n\tv_mbcnt_hi_u32_b32 %0, -1, %0" : "=v"(l)); return l; }
#define RTS(slot) do {} while (0)
#define CT_END 0x1f300
#define ACLOB_LO "a0","a1","a2","a3","a4","a5","a6","a7","a8","a9","a10","a11","a12","a13","a14","a15","a16","a17","a18","a19","a20","a21","a22","a23","a24","a25","a26","a27","a28","a29","a30","a31","a32","a33","a34","a35","a36","a37","a38","a39","a40","a41","a42","a43","a44","a45","a46","a47","a48","a49","a50","a51","a52","a53","a54","a55","a56","a57","a58","a59","a60","a61","a62","a63","a64","a65","a66","a67","a68","a69","a70","a71","a72","a73","a74","a75","a76","a77","a78","a79","a80","a81","a82","a83","a84","a85","a86","a87","a88","a89","a90","a91","a92","a93","a94","a95","a96","a97","a98","a99","a100","a101","a102","a103","a104","a105","a106","a107","a108","a109","a110","a111","a112","a113","a114","a115","a116","a117","a118","a119","a120","a121","a122","a123","a124","a125","a126","a127"
#define ACLOB_HI "a128","a129","a130","a131","a132","a133","a134","a135","a136","a137","a138","a139","a140","a141","a142","a143","a144","a145","a146","a147","a148","a149","a150","a151","a152","a153","a154","a155","a156","a157","a158","a159","a160","a161","a162","a163","a164","a165","a166","a167","a168","a169","a170","a171","a172","a173","a174","a175","a176","a177","a178","a179","a180","a181","a182","a183","a184","a185","a186","a187","a188","a189","a190","a191","a192","a193","a194","a195","a196","a197","a198","a199","a200","a201","a202","a203","a204","a205","a206","a207","a208","a209","a210","a211","a212","a213","a214","a215","a216","a217","a218","a219","a220","a221","a222","a223","a224","a225","a226","a227","a228","a229","a230","a231","a232","a233","a234","a235","a236","a237","a238","a239","a240","a241","a242","a243","a244","a245","a246","a247","a248","a249","a250","a251","a252","a253","a254","a255"
#define PLO(p) ((unsigned)(size_t)(p))
#define PHI(p) ((unsigned)((size_t)(p) >> 32))
__device__ __forceinline__ unsigned vsel(bool c, unsigned a, unsigned b) { asm("" : "+v"(a), "+v"(b)); return c ? a : b; }
__device__ __forceinline__ unsigned ssel(unsigned c, unsigned a, unsigned b) { unsigned r; asm("s_cmp_lg_u32 %1, 0\n\ts_cselect_b32 %0, %2, %3" : "=s"(r) : "s"(c), "s"(a), "s"(b) : "scc"); return r; }
template <int N> __device__ __forceinline__ unsigned sslt(unsigned c, unsigned a, unsigned b) { unsigned r; asm("s_cmpk_lt_u32 %1, %4\n\ts_cselect_b32 %0, %2, %3" : "=s"(r) : "s"(c), "s"(a), "s"(b), "n"(N) : "scc"); return r; }
namespace att {
constexpr int LDS_BYTES = 163840;
constexpr unsigned LDS_G = 159744u, LDS_L = 161792u;
}
#define SD(J8, H, W) ((unsigned)WS_AF + (((J8) + 2u * ((W) & 2u) + ((W) & 1u)) * KS_TOT + 18u + 4u * (H)) * 1024u)
#define SRDA (u32x4){PLO(ws), PHI(ws) & 0xffffu, (unsigned)WS_L, 0x00020000u}
#define PROJ_ASM \
".p2align 3\n" \
"L_pb:\n" \
"s_waitcnt lgkmcnt(0)\n" \
"s_mov_b32 s64, s10\n" \
"s_and_b32 s65, s11, 0xffff\n" \
"s_mov_b32 s66, 0x200000\n" \
"s_mov_b32 s67, 0x20000\n" \
"s_mov_b32 s68, s14\n" \
"s_and_b32 s69, s15, 0xffff\n" \
"s_mov_b32 s70, 0x400000\n" \
"s_mov_b32 s71, 0x20000\n" \
"s_mov_b32 s72, s16\n" \
"s_and_b32 s73, s17, 0xffff\n" \
"s_mov_b32 s74, 0x400000\n" \
"s_mov_b32 s75, 0x20000\n" \
"s_add_u32 m0, s30, 0x0\n" \
"s_movk_i32 s84, 0x0\n" \
"buffer_load_dwordx4 v22, s[64:67], s84 offen offset:0 lds\n" \
"s_movk_i32 s84, 0x1c00\n" \
"buffer_load_dwordx4 v23, s[64:67], s84 offen offset:1024 lds\n" \
"s_movk_i32 s84, 0x3800\n" \
"buffer_load_dwordx4 v22, s[64:67], s84 offen offset:2048 lds\n" \
"s_movk_i32 s84, 0x5400\n" \
"buffer_load_dwordx4 v23, s[64:67], s84 offen offset:3072 lds\n" \
"s_add_u32 m0, s31, 0x0\n" \
"s_movk_i32 s84, 0x0\n" \
"buffer_load_dwordx4 v26, s[68:71], s84 offen offset:0 lds\n" \
"s_movk_i32 s84, 0x7c00\n" \
"buffer_load_dwordx4 v26, s[68:71], s84 offen offset:1024 lds\n" \
"s_addk_i32 s84, 0x7c00\n" \
"buffer_load_dwordx4 v27, s[68:71], s84 offen offset:2048 lds\n" \
"s_addk_i32 s84, 0x7c00\n" \
"buffer_load_dwordx4 v27, s[68:71], s84 offen offset:3072 lds\n" \
"s_movk_i32 s7, 0x300\n" \
"s_lshr_b32 s4, s2, 1\n" \
"s_and_b32 s4, s4, 60\n" \
"s_or_b32 s4, s4, s3\n" \
"v_lshrrev_b32_e32 v212, 4, v20\n" \
"v_min_u32_e32 v212, 59, v212\n" \
"s_cmpk_lt_u32 s2, 0x80\n" \
"s_cselect_b32 s6, s14, s16\n" \
"v_subrev_u32_e32 v214, 48, v212\n" \
"v_mov_b32_e32 v212, s10\n" \
"v_mov_b32_e32 v215, s6\n" \
"s_cmpk_lt_u32 s2, 0x80\n" \
"s_cselect_b32 s5, s15, s17\n" \
"v_cmp_gt_u32_e32 vcc, s7, v20\n" \
"v_mov_b32_e64 v213, s11\n" \
"v_bfe_u32 v216, v20, 4, 3\n" \
"v_cndmask_b32_e32 v212, v212, v215, vcc\n" \
"v_mov_b32_e32 v215, s5\n" \
"s_lshl_b32 s5, s3, 4\n" \
"v_cndmask_b32_e32 v213, v213, v215, vcc\n" \
"v_lshlrev_b32_e64 v215, 5, v20\n" \
"v_and_b32_e32 v215, 0x7000, v215\n" \
"v_lshl_add_u32 v215, s4, 6, v215\n" \
"s_lshl_b32 s4, s2, 3\n" \
"s_and_b32 s4, s4, 56\n" \
"v_or3_b32 v215, v215, s4, v216\n" \
"s_lshl_b32 s4, s2, 6\n" \
"s_or_b32 s6, s5, s4\n" \
"v_cmp_gt_u32_e64 s[4:5], 6, v214\n" \
"v_add_u32_e32 v215, 0x2000, v215\n" \
"s_nop 0\n" \
"v_cndmask_b32_e64 v216, 4, 2, s[4:5]\n" \
"v_add3_u32 v214, s6, v214, v216\n" \
"s_lshl_b32 s4, s3, 9\n" \
"v_cndmask_b32_e32 v214, v214, v215, vcc\n" \
"v_lshlrev_b32_e32 v214, 7, v214\n" \
"v_mov_b32_e64 v215, 0\n" \
"v_lshl_add_u64 v[184:185], v[212:213], 0, v[214:215]\n" \
"s_or_b32 s32, s4, 0x25000\n" \
"s_add_u32 m0, s30, 0x1000\n" \
"s_mov_b32 s84, 0x8000\n" \
"buffer_load_dwordx4 v22, s[64:67], s84 offen offset:0 lds\n" \
"s_and_b32 s33, s3, 1\n" \
"v_lshrrev_b32_e32 v212, 4, v20\n" \
"v_lshrrev_b32_e64 v214, 8, v20\n" \
"v_bfe_u32 v212, v212, 1, 3\n" \
"v_lshlrev_b32_e32 v215, 2, v20\n" \
"s_addk_i32 s84, 0x1c00\n" \
"buffer_load_dwordx4 v23, s[64:67], s84 offen offset:1024 lds\n" \
"v_lshrrev_b32_e64 v216, 1, v20\n" \
"v_bfe_u32 v213, v20, 4, 4\n" \
"s_lshl_b32 s4, s3, 5\n" \
"v_xor_b32_e32 v212, v212, v214\n" \
"v_lshlrev_b32_e32 v214, 11, v214\n" \
"s_addk_i32 s84, 0x1c00\n" \
"buffer_load_dwordx4 v22, s[64:67], s84 offen offset:2048 lds\n" \
"v_and_b32_e32 v215, 0x300, v215\n" \
"v_and_b32_e64 v217, 24, v216\n" \
"v_and_or_b32 v213, s4, 64, v213\n" \
"v_or3_b32 v214, v215, v214, v217\n" \
"s_lshl_b32 s4, s33, 14\n" \
"s_addk_i32 s84, 0x1c00\n" \
"buffer_load_dwordx4 v23, s[64:67], s84 offen offset:3072 lds\n" \
"v_and_b32_e32 v215, 0xe0, v216\n" \
"v_lshlrev_b32_e32 v213, 7, v213\n" \
"v_lshlrev_b32_e32 v212, 4, v212\n" \
"v_or3_b32 v214, v214, s4, v215\n" \
"v_or_b32_e64 v28, v212, v213\n" \
"s_add_u32 m0, s31, 0x4000\n" \
"s_movk_i32 s84, 0x0\n" \
"buffer_load_dwordx4 v26, s[72:75], s84 offen offset:0 lds\n" \
"v_add_u32_e32 v32, 0x10000, v214\n" \
"v_bitop3_b32 v29, v212, 64, v213 bitop3:0x36\n" \
"v_xor_b32_e32 v33, 0x20, v32\n" \
"v_xor_b32_e64 v34, 0x40, v32\n" \
"v_xor_b32_e32 v35, 0x60, v32\n" \
"s_movk_i32 s84, 0x7c00\n" \
"buffer_load_dwordx4 v26, s[72:75], s84 offen offset:1024 lds\n" \
"v_xor_b32_e32 v36, 0x80, v32\n" \
"v_xor_b32_e32 v37, 0xa0, v32\n" \
"v_xor_b32_e32 v38, 0xc0, v32\n" \
"v_xor_b32_e32 v39, 0xe0, v32\n" \
"s_mov_b32 s80, s26\n" \
"s_addk_i32 s84, 0x7c00\n" \
"buffer_load_dwordx4 v27, s[72:75], s84 offen offset:2048 lds\n" \
"s_and_b32 s81, s27, 0xffff\n" \
"s_mov_b32 s82, 0x10000000\n" \
"s_mov_b32 s83, 0x20000\n" \
"s_mov_b32 s76, s18\n" \
"s_and_b32 s77, s19, 0xffff\n" \
"s_addk_i32 s84, 0x7c00\n" \
"buffer_load_dwordx4 v27, s[72:75], s84 offen offset:3072 lds\n" \
"s_mov_b32 s78, 0x1e6000\n" \
"s_mov_b32 s79, 0x20000\n" \
"s_branch L_pgo_%=\n" \
"L_pret:\n" \
"s_setpc_b64 s[88:89]\n" \
"L_pgo_%=:\n" \
"s_mul_i32 s88, s33, 0xa000\n" \
"s_lshl_b32 s89, s3, 11\n" \
"s_and_b32 s89, s89, 0x1000\n" \
"s_add_u32 s88, s88, s89\n" \
"s_getpc_b64 s[86:87]\n" \
"L_hpa_%=:\n" \
"s_add_u32 s86, s86, L_pp1-L_hpa_%=\n" \
"s_addc_u32 s87, s87, 0\n" \
"s_add_u32 s86, s86, s88\n" \
"s_addc_u32 s87, s87, 0\n" \
"s_add_u32 m0, s30, 0x8000\n" \
"s_movk_i32 s84, 0x80\n" \
"buffer_load_dwordx4 v22, s[64:67], s84 offen offset:0 lds\n" \
"s_lshr_b32 s4, s2, 1\n" \
"s_and_b32 s4, s4, 0x7c\n" \
"v_bfe_u32 v214, v20, 4, 5\n" \
"s_movk_i32 s84, 0x1c80\n" \
"buffer_load_dwordx4 v23, s[64:67], s84 offen offset:1024 lds\n" \
"s_or_b32 s7, s4, s3\n" \
"v_min_u32_e32 v214, 26, v214\n" \
"v_mad_u64_u32 v[214:215], s[4:5], s7, 27, v[214:215]\n" \
"s_movk_i32 s84, 0x3880\n" \
"buffer_load_dwordx4 v22, s[64:67], s84 offen offset:2048 lds\n" \
"v_mul_u32_u24_e32 v215, 0xaaab, v214\n" \
"v_lshrrev_b32_e32 v215, 18, v215\n" \
"s_mov_b32 s4, 0x1fffffa\n" \
"s_movk_i32 s84, 0x5480\n" \
"buffer_load_dwordx4 v23, s[64:67], s84 offen offset:3072 lds\n" \
"v_mad_u64_u32 v[216:217], s[4:5], v215, s4, v[214:215]\n" \
"s_and_b32 s6, s2, 7\n" \
"v_mul_u32_u24_e32 v215, 27, v215\n" \
"s_mov_b64 exec, 0\n" \
"s_swappc_b64 s[88:89], s[86:87]\n" \
"s_mov_b64 exec, -1\n" \
"s_add_u32 s86, s86, 0x2000\n" \
"s_addc_u32 s87, s87, 0\n" \
"s_add_u32 m0, s31, 0x8000\n" \
"s_mov_b32 s84, 0x80000\n" \
"buffer_load_dwordx4 v26, s[68:71], s84 offen offset:0 lds\n" \
"v_lshrrev_b32_e32 v216, 1, v216\n" \
"v_and_b32_e32 v214, 1, v214\n" \
"v_mov_b32_e32 v213, s13\n" \
"s_addk_i32 s84, 0x7c00\n" \
"buffer_load_dwordx4 v26, s[68:71], s84 offen offset:1024 lds\n" \
"v_lshrrev_b32_e32 v212, 4, v20\n" \
"v_mul_u32_u24_e64 v216, 9, v216\n" \
"v_add3_u32 v214, v214, s6, v215\n" \
"s_mov_b64 exec, 0\n" \
"s_swappc_b64 s[88:89], s[86:87]\n" \
"s_mov_b64 exec, -1\n" \
"s_add_u32 s86, s86, 0x2000\n" \
"s_addc_u32 s87, s87, 0\n" \
"s_addk_i32 s84, 0x7c00\n" \
"buffer_load_dwordx4 v27, s[68:71], s84 offen offset:2048 lds\n" \
"v_add_lshl_u32 v214, v214, v216, 7\n" \
"v_mad_u64_u32 v[216:217], s[4:5], s7, 36, v[212:213]\n" \
"s_movk_i32 s4, 0x240\n" \
"s_addk_i32 s84, 0x7c00\n" \
"buffer_load_dwordx4 v27, s[68:71], s84 offen offset:3072 lds\n" \
"v_mov_b32_e32 v219, s9\n" \
"v_mov_b32_e32 v215, 0\n" \
"v_cmp_gt_u32_e32 vcc, s4, v20\n" \
"s_mov_b64 exec, 0\n" \
"s_swappc_b64 s[88:89], s[86:87]\n" \
"s_mov_b64 exec, -1\n" \
"s_add_u32 s86, s86, 0x2000\n" \
"s_addc_u32 s87, s87, 0\n" \
"s_add_u32 m0, s30, 0x9000\n" \
"s_mov_b32 s84, 0x8080\n" \
"buffer_load_dwordx4 v22, s[64:67], s84 offen offset:0 lds\n" \
"s_lshl_b32 s4, s2, 2\n" \
"v_lshl_add_u64 v[186:187], s[18:19], 0, v[214:215]\n" \
"v_mov_b32_e32 v214, s12\n" \
"s_addk_i32 s84, 0x1c00\n" \
"buffer_load_dwordx4 v23, s[64:67], s84 offen offset:1024 lds\n" \
"v_mov_b32_e32 v217, s8\n" \
"s_or_b32 s4, s4, s3\n" \
"v_cndmask_b32_e32 v219, v219, v213, vcc\n" \
"s_addk_i32 s84, 0x1c00\n" \
"buffer_load_dwordx4 v22, s[64:67], s84 offen offset:2048 lds\n" \
"v_and_b32_e32 v213, 0x3ff8, v216\n" \
"s_mul_i32 s4, s4, 18\n" \
"v_cndmask_b32_e32 v218, v217, v214, vcc\n" \
"s_mov_b64 exec, 0\n" \
"s_swappc_b64 s[88:89], s[86:87]\n" \
"s_mov_b64 exec, -1\n" \
"s_andn2_b32 s85, 2, s3\n" \
"s_lshl_b32 s85, s85, 12\n" \
"s_add_u32 s86, s86, s85\n" \
"s_addc_u32 s87, s87, 0\n" \
"s_addk_i32 s84, 0x1c00\n" \
"buffer_load_dwordx4 v23, s[64:67], s84 offen offset:3072 lds\n" \
"v_or_b32_e32 v213, s6, v213\n" \
"v_and_b32_e32 v214, 7, v216\n" \
"v_min_u32_e32 v212, 53, v212\n" \
"s_add_u32 m0, s31, 0xc000\n" \
"s_mov_b32 s84, 0x80000\n" \
"buffer_load_dwordx4 v26, s[72:75], s84 offen offset:0 lds\n" \
"s_sub_i32 s4, s4, 36\n" \
"v_lshl_or_b32 v213, v213, 3, v214\n" \
"v_add_u32_e32 v212, s4, v212\n" \
"s_mov_b64 exec, 0\n" \
"s_swappc_b64 s[88:89], s[86:87]\n" \
"s_mov_b64 exec, -1\n" \
"s_addk_i32 s84, 0x7c00\n" \
"buffer_load_dwordx4 v26, s[72:75], s84 offen offset:1024 lds\n" \
"v_cndmask_b32_e32 v212, v212, v213, vcc\n" \
"v_lshlrev_b32_e32 v214, 7, v212\n" \
"v_lshl_add_u64 v[188:189], v[218:219], 0, v[214:215]\n" \
"s_addk_i32 s84, 0x7c00\n" \
"buffer_load_dwordx4 v27, s[72:75], s84 offen offset:2048 lds\n" \
"s_movk_i32 s5, 0xd80\n" \
"s_lshr_b32 s6, s2, 5\n" \
"v_lshrrev_b32_e32 v212, 4, v20\n" \
"s_addk_i32 s84, 0x7c00\n" \
"buffer_load_dwordx4 v27, s[72:75], s84 offen offset:3072 lds\n" \
"v_lshl_or_b32 v215, s3, 6, v212\n" \
"v_mul_u32_u24_e32 v216, 0x2aab, v215\n" \
"v_lshrrev_b32_e32 v216, 16, v216\n" \
"s_mov_b32 m0, s32\n" \
"s_mov_b32 exec_lo, 0xffffff\n" \
"s_mov_b32 exec_hi, 0xfff0000\n" \
"global_load_lds_dword v[184:185], off\n" \
"s_mov_b64 exec, -1\n" \
"s_mov_b32 s84, 0x5bc0000\n" \
"buffer_load_dword v180, off, s[80:83], s84 sc1\n" \
"s_cmp_lg_u32 s33, 0\n" \
"s_cbranch_scc1 L_w1_%=\n" \
"L_w0_%=:\n" \
"s_waitcnt vmcnt(18)\n" \
"s_barrier\n" \
"ds_read_b64_tr_b16 a[160:161], v32 offset:0\n" \
"ds_read_b64_tr_b16 a[162:163], v32 offset:1024\n" \
"ds_read_b128 a[128:131], v28 offset:16384\n" \
"ds_read_b64_tr_b16 a[224:225], v32 offset:8192\n" \
"ds_read_b64_tr_b16 a[226:227], v32 offset:9216\n" \
"ds_read_b128 a[192:195], v29 offset:16384\n" \
"ds_read_b128 a[132:135], v28 offset:18432\n" \
"ds_read_b128 a[196:199], v29 offset:18432\n" \
"ds_read_b64_tr_b16 a[164:165], v33 offset:0\n" \
"ds_read_b64_tr_b16 a[166:167], v33 offset:1024\n" \
"ds_read_b64_tr_b16 a[228:229], v33 offset:8192\n" \
"ds_read_b64_tr_b16 a[230:231], v33 offset:9216\n" \
"ds_read_b128 a[136:139], v28 offset:20480\n" \
"ds_read_b128 a[200:203], v29 offset:20480\n" \
"s_waitcnt lgkmcnt(11)\n" \
"v_mfma_f32_16x16x32_bf16 a[0:3], a[160:163], a[128:131], 0\n" \
"ds_read_b64_tr_b16 a[168:169], v34 offset:0\n" \
"v_mad_i32_i24 v217, v216, -6, v215\n" \
"s_waitcnt lgkmcnt(9)\n" \
"v_mfma_f32_16x16x32_bf16 a[0:3], a[224:227], a[192:195], a[0:3]\n" \
"ds_read_b64_tr_b16 a[170:171], v34 offset:1024\n" \
"v_add_u32_e32 v218, 0xfffffe50, v215\n" \
"s_waitcnt lgkmcnt(9)\n" \
"v_mfma_f32_16x16x32_bf16 a[16:19], a[160:163], a[132:135], 0\n" \
"ds_read_b64_tr_b16 a[232:233], v34 offset:8192\n" \
"s_mul_i32 s35, s6, 0x48\n" \
"s_waitcnt lgkmcnt(9)\n" \
"v_mfma_f32_16x16x32_bf16 a[16:19], a[224:227], a[196:199], a[16:19]\n" \
"ds_read_b64_tr_b16 a[234:235], v34 offset:9216\n" \
"v_lshrrev_b32_e32 v219, 1, v217\n" \
"s_waitcnt lgkmcnt(8)\n" \
"v_mfma_f32_16x16x32_bf16 a[20:23], a[164:167], a[132:135], 0\n" \
"ds_read_b128 a[140:143], v28 offset:22528\n" \
"v_add_u32_e32 v218, s35, v216\n" \
"s_waitcnt lgkmcnt(7)\n" \
"v_mfma_f32_16x16x32_bf16 a[20:23], a[228:231], a[196:199], a[20:23]\n" \
"ds_read_b128 a[204:207], v29 offset:22528\n" \
"v_mul_u32_u24_e32 v219, 0x480, v219\n" \
"v_mfma_f32_16x16x32_bf16 a[4:7], a[164:167], a[128:131], 0\n" \
"ds_read_b64_tr_b16 a[172:173], v35 offset:0\n" \
"v_and_b32_e64 v214, 16, v20\n" \
"v_mfma_f32_16x16x32_bf16 a[4:7], a[228:231], a[192:195], a[4:7]\n" \
"ds_read_b64_tr_b16 a[174:175], v35 offset:1024\n" \
"v_mad_u32_u24 v219, v218, s5, v219\n" \
"s_waitcnt lgkmcnt(9)\n" \
"v_mfma_f32_16x16x32_bf16 a[32:35], a[160:163], a[136:139], 0\n" \
"ds_read_b64_tr_b16 a[236:237], v35 offset:8192\n" \
"v_or_b32_e32 v192, v219, v214\n" \
"s_waitcnt lgkmcnt(9)\n" \
"v_mfma_f32_16x16x32_bf16 a[32:35], a[224:227], a[200:203], a[32:35]\n" \
"ds_read_b64_tr_b16 a[238:239], v35 offset:9216\n" \
"v_mov_b32_e32 v220, 2.0\n" \
"v_mfma_f32_16x16x32_bf16 a[36:39], a[164:167], a[136:139], 0\n" \
"ds_read_b128 a[144:147], v28 offset:0\n" \
"v_mov_b32_e64 v222, 7\n" \
"v_mfma_f32_16x16x32_bf16 a[36:39], a[228:231], a[200:203], a[36:39]\n" \
"ds_read_b128 a[208:211], v29 offset:0\n" \
"v_lshlrev_b32_e32 v220, 7, v218\n" \
"s_waitcnt lgkmcnt(10)\n" \
"v_mfma_f32_16x16x32_bf16 a[40:43], a[168:171], a[136:139], 0\n" \
"ds_read_b128 a[148:151], v28 offset:2048\n" \
"v_lshrrev_b32_e32 v218, 1, v218\n" \
"s_waitcnt lgkmcnt(9)\n" \
"v_mfma_f32_16x16x32_bf16 a[40:43], a[232:235], a[200:203], a[40:43]\n" \
"ds_read_b128 a[212:215], v29 offset:2048\n" \
"v_and_b32_e64 v218, 4, v218\n" \
"v_mfma_f32_16x16x32_bf16 a[24:27], a[168:171], a[132:135], 0\n" \
"ds_read_b128 a[152:155], v28 offset:4096\n" \
"v_and_b32_e64 v216, 2, v216\n" \
"v_mfma_f32_16x16x32_bf16 a[24:27], a[232:235], a[196:199], a[24:27]\n" \
"ds_read_b128 a[216:219], v29 offset:4096\n" \
"s_mul_i32 s7, s6, 0x2400\n" \
"v_mfma_f32_16x16x32_bf16 a[8:11], a[168:171], a[128:131], 0\n" \
"ds_read_b128 a[156:159], v28 offset:6144\n" \
"s_sub_i32 s34, 0x22000, s7\n" \
"v_mfma_f32_16x16x32_bf16 a[8:11], a[232:235], a[192:195], a[8:11]\n" \
"ds_read_b128 a[220:223], v29 offset:6144\n" \
"s_mov_b32 m0, s32\n" \
"s_waitcnt lgkmcnt(13)\n" \
"v_mfma_f32_16x16x32_bf16 a[48:51], a[160:163], a[140:143], 0\n" \
"s_mov_b32 exec_lo, 0xff000000\n" \
"s_mov_b32 exec_hi, 0x0\n" \
"global_load_lds_dword v[184:185], off\n" \
"s_mov_b64 exec, -1\n" \
"s_waitcnt lgkmcnt(12)\n" \
"v_mfma_f32_16x16x32_bf16 a[48:51], a[224:227], a[204:207], a[48:51]\n" \
"ds_read_b64_tr_b16 a[176:177], v36 offset:0\n" \
"v_bitop3_b32 v216, v217, v218, v216 bitop3:0x1e\n" \
"v_mfma_f32_16x16x32_bf16 a[52:55], a[164:167], a[140:143], 0\n" \
"ds_read_b64_tr_b16 a[178:179], v36 offset:1024\n" \
"v_or_b32_e32 v213, 0x24400, v20\n" \
"v_mfma_f32_16x16x32_bf16 a[52:55], a[228:231], a[204:207], a[52:55]\n" \
"s_mov_b32 m0, s32\n" \
"ds_read_b64_tr_b16 a[240:241], v36 offset:8192\n" \
"v_mfma_f32_16x16x32_bf16 a[56:59], a[168:171], a[140:143], 0\n" \
"s_mov_b32 exec_lo, 0xffff\n" \
"s_mov_b32 exec_hi, 0x0\n" \
"global_load_lds_dword v[186:187], off\n" \
"s_mov_b64 exec, -1\n" \
"v_mfma_f32_16x16x32_bf16 a[56:59], a[232:235], a[204:207], a[56:59]\n" \
"ds_read_b64_tr_b16 a[242:243], v36 offset:9216\n" \
"v_lshlrev_b32_e32 v216, 4, v216\n" \
"s_waitcnt lgkmcnt(14)\n" \
"v_mfma_f32_16x16x32_bf16 a[60:63], a[172:175], a[140:143], 0\n" \
"ds_read_b64_tr_b16 a[180:181], v37 offset:0\n" \
"v_add3_u32 v196, v220, s34, v216\n" \
"s_waitcnt lgkmcnt(13)\n" \
"v_mfma_f32_16x16x32_bf16 a[60:63], a[236:239], a[204:207], a[60:63]\n" \
"ds_read_b64_tr_b16 a[182:183], v37 offset:1024\n" \
"v_mov_b32_e32 v216, v213\n" \
"v_mfma_f32_16x16x32_bf16 a[44:47], a[172:175], a[136:139], 0\n" \
"ds_read_b64_tr_b16 a[244:245], v37 offset:8192\n" \
"s_movk_i32 s36, 0x1b0\n" \
"v_mfma_f32_16x16x32_bf16 a[44:47], a[236:239], a[200:203], a[44:47]\n" \
"ds_read_b64_tr_b16 a[246:247], v37 offset:9216\n" \
"v_or_b32_e32 v216, 0x100, v215\n" \
"v_mfma_f32_16x16x32_bf16 a[28:31], a[172:175], a[132:135], 0\n" \
"ds_read_b64_tr_b16 a[184:185], v38 offset:0\n" \
"v_mul_u32_u24_e32 v217, 0x2aab, v216\n" \
"v_mfma_f32_16x16x32_bf16 a[28:31], a[236:239], a[196:199], a[28:31]\n" \
"ds_read_b64_tr_b16 a[186:187], v38 offset:1024\n" \
"v_add_u32_e32 v215, 0xffffff50, v215\n" \
"v_mfma_f32_16x16x32_bf16 a[12:15], a[172:175], a[128:131], 0\n" \
"v_lshrrev_b32_e32 v217, 16, v217\n" \
"v_mov_b32_e64 v220, v215\n" \
"v_mfma_f32_16x16x32_bf16 a[12:15], a[236:239], a[192:195], a[12:15]\n" \
"v_mad_i32_i24 v218, v217, -6, v216\n" \
"v_cmp_gt_u32_e32 vcc, s36, v216\n" \
"s_waitcnt lgkmcnt(15)\n" \
"v_mfma_f32_16x16x32_bf16 a[76:79], a[172:175], a[144:147], 0\n" \
"ds_read_b64_tr_b16 a[248:249], v38 offset:8192\n" \
"v_mov_b32_e64 v221, 7\n" \
"v_mfma_f32_16x16x32_bf16 a[76:79], a[236:239], a[208:211], a[76:79]\n" \
"v_lshrrev_b32_e32 v222, 1, v218\n" \
"v_cndmask_b32_e32 v217, v220, v217, vcc\n" \
"v_mfma_f32_16x16x32_bf16 a[72:75], a[168:171], a[144:147], 0\n" \
"s_movk_i32 s4, 0x48\n" \
"v_add_u32_e32 v220, s35, v217\n" \
"v_mfma_f32_16x16x32_bf16 a[72:75], a[232:235], a[208:211], a[72:75]\n" \
"ds_read_b64_tr_b16 a[250:251], v38 offset:9216\n" \
"v_mul_u32_u24_e32 v222, 0x480, v222\n" \
"v_mfma_f32_16x16x32_bf16 a[68:71], a[164:167], a[144:147], 0\n" \
"v_mad_u32_u24 v222, v220, s5, v222\n" \
"v_cmp_gt_u32_e64 s[4:5], s4, v215\n" \
"v_mfma_f32_16x16x32_bf16 a[68:71], a[228:231], a[208:211], a[68:71]\n" \
"v_cndmask_b32_e32 v215, v221, v218, vcc\n" \
"v_lshrrev_b32_e32 v218, 1, v220\n" \
"v_mfma_f32_16x16x32_bf16 a[64:67], a[160:163], a[144:147], 0\n" \
"v_or_b32_e32 v214, v222, v214\n" \
"v_mov_b32_e32 v222, 2.0\n" \
"v_mfma_f32_16x16x32_bf16 a[64:67], a[224:227], a[208:211], a[64:67]\n" \
"ds_read_b64_tr_b16 a[188:189], v39 offset:0\n" \
"v_and_b32_e64 v218, 4, v218\n" \
"v_mfma_f32_16x16x32_bf16 a[80:83], a[160:163], a[148:151], 0\n" \
"v_and_b32_e64 v217, 2, v217\n" \
"v_bitop3_b32 v215, v215, v218, v217 bitop3:0x1e\n" \
"s_waitcnt lgkmcnt(15)\n" \
"v_mfma_f32_16x16x32_bf16 a[80:83], a[224:227], a[212:215], a[80:83]\n" \
"v_lshlrev_b32_e32 v223, 1, v220\n" \
"v_mov_b32_e64 v224, 2.0\n" \
"v_mfma_f32_16x16x32_bf16 a[84:87], a[164:167], a[148:151], 0\n" \
"ds_read_b64_tr_b16 a[190:191], v39 offset:1024\n" \
"v_cndmask_b32_e64 v193, v222, v214, vcc\n" \
"v_mfma_f32_16x16x32_bf16 a[84:87], a[228:231], a[212:215], a[84:87]\n" \
"v_lshlrev_b32_e32 v214, 7, v220\n" \
"v_lshlrev_b32_e32 v215, 4, v215\n" \
"v_mfma_f32_16x16x32_bf16 a[88:91], a[168:171], a[148:151], 0\n" \
"s_mul_i32 s39, s3, 18\n" \
"v_add3_u32 v214, v214, s34, v215\n" \
"v_mfma_f32_16x16x32_bf16 a[88:91], a[232:235], a[212:215], a[88:91]\n" \
"ds_read_b64_tr_b16 a[252:253], v39 offset:8192\n" \
"v_cndmask_b32_e64 v195, v224, v223, s[4:5]\n" \
"v_mfma_f32_16x16x32_bf16 a[92:95], a[172:175], a[148:151], 0\n" \
"s_movk_i32 s4, 0x1f8\n" \
"v_mov_b32_e64 v215, v213\n" \
"v_mfma_f32_16x16x32_bf16 a[92:95], a[236:239], a[212:215], a[92:95]\n" \
"s_add_i32 s35, s35, s39\n" \
"v_cmp_gt_u32_e32 vcc, s4, v216\n" \
"v_mfma_f32_16x16x32_bf16 a[108:111], a[172:175], a[152:155], 0\n" \
"ds_read_b64_tr_b16 a[254:255], v39 offset:9216\n" \
"v_add_u32_e64 v212, s35, v212\n" \
"v_mfma_f32_16x16x32_bf16 a[108:111], a[236:239], a[216:219], a[108:111]\n" \
"v_mov_b32_e32 v219, 2.0\n" \
"v_cndmask_b32_e32 v197, v215, v214, vcc\n" \
"v_mfma_f32_16x16x32_bf16 a[104:107], a[168:171], a[152:155], 0\n" \
"s_movk_i32 s35, 0x120\n" \
"v_mul_u32_u24_e32 v214, 0xd80, v212\n" \
"v_mfma_f32_16x16x32_bf16 a[104:107], a[232:235], a[216:219], a[104:107]\n" \
"v_cmp_gt_u32_e32 vcc, s35, v20\n" \
"v_lshrrev_b32_e64 v215, 1, v212\n" \
"v_mfma_f32_16x16x32_bf16 a[100:103], a[164:167], a[152:155], 0\n" \
"s_and_b32 s4, s2, 7\n" \
"v_cndmask_b32_e32 v194, v219, v214, vcc\n" \
"v_mfma_f32_16x16x32_bf16 a[100:103], a[228:231], a[216:219], a[100:103]\n" \
"v_lshlrev_b32_e32 v214, 7, v212\n" \
"v_lshrrev_b32_e32 v212, 2, v212\n" \
"v_mfma_f32_16x16x32_bf16 a[96:99], a[160:163], a[152:155], 0\n" \
"v_and_b32_e64 v212, 2, v212\n" \
"v_and_or_b32 v212, v215, 1, v212\n" \
"v_mfma_f32_16x16x32_bf16 a[96:99], a[224:227], a[216:219], a[96:99]\n" \
"s_lshl_b32 s5, s4, 2\n" \
"s_bfe_u32 s38, s2, 0x20003\n" \
"s_waitcnt lgkmcnt(15)\n" \
"v_mfma_f32_16x16x32_bf16 a[112:115], a[160:163], a[156:159], 0\n" \
"v_lshlrev_b32_e32 v212, 5, v212\n" \
"s_movk_i32 s35, 0x60\n" \
"v_mfma_f32_16x16x32_bf16 a[112:115], a[224:227], a[220:223], a[112:115]\n" \
"s_or_b32 s5, s5, s38\n" \
"v_bitop3_b32 v212, v212, s35, v214 bitop3:0x36\n" \
"v_mfma_f32_16x16x32_bf16 a[116:119], a[164:167], a[156:159], 0\n" \
"s_lshr_b32 s36, s2, 3\n" \
"v_add_u32_e64 v212, s34, v212\n" \
"v_mfma_f32_16x16x32_bf16 a[116:119], a[228:231], a[220:223], a[116:119]\n" \
"s_mul_i32 s34, s5, 0x12000\n" \
"s_or_b32 s40, s34, 0x6000000\n" \
"v_mfma_f32_16x16x32_bf16 a[120:123], a[168:171], a[156:159], 0\n" \
"s_mul_i32 s34, s36, 0x90000\n" \
"s_lshl_b32 s36, s3, 14\n" \
"v_mfma_f32_16x16x32_bf16 a[120:123], a[232:235], a[220:223], a[120:123]\n" \
"s_add_i32 s34, s34, s36\n" \
"s_lshl_b32 s38, s5, 8\n" \
"v_mfma_f32_16x16x32_bf16 a[124:127], a[172:175], a[156:159], 0\n" \
"s_add_i32 s41, s34, 0x4240000\n" \
"s_mul_i32 s34, s5, 36\n" \
"v_mfma_f32_16x16x32_bf16 a[124:127], a[236:239], a[220:223], a[124:127]\n" \
"s_waitcnt vmcnt(4) lgkmcnt(0)\n" \
"s_barrier\n" \
"v_mfma_f32_16x16x32_bf16 v[52:55], a[176:179], a[128:131], 0\n" \
"s_add_u32 m0, s30, 0x0\n" \
"s_movk_i32 s84, 0x100\n" \
"v_mfma_f32_16x16x32_bf16 v[52:55], a[240:243], a[192:195], v[52:55]\n" \
"buffer_load_dwordx4 v22, s[64:67], s84 offen offset:0 lds\n" \
"v_mfma_f32_16x16x32_bf16 v[56:59], a[180:183], a[128:131], 0\n" \
"ds_read_b64_tr_b16 a[160:161], v32 offset:32768\n" \
"s_add_i32 s5, s7, s40\n" \
"v_mfma_f32_16x16x32_bf16 v[56:59], a[244:247], a[192:195], v[56:59]\n" \
"s_movk_i32 s84, 0x1d00\n" \
"v_mfma_f32_16x16x32_bf16 v[60:63], a[184:187], a[128:131], 0\n" \
"buffer_load_dwordx4 v23, s[64:67], s84 offen offset:1024 lds\n" \
"v_mfma_f32_16x16x32_bf16 v[60:63], a[248:251], a[192:195], v[60:63]\n" \
"ds_read_b64_tr_b16 a[162:163], v32 offset:33792\n" \
"s_addk_i32 s5, 0x2000\n" \
"v_mfma_f32_16x16x32_bf16 v[64:67], a[188:191], a[128:131], 0\n" \
"ds_read_b64_tr_b16 a[224:225], v32 offset:40960\n" \
"s_mov_b32 s37, 2.0\n" \
"v_mfma_f32_16x16x32_bf16 v[64:67], a[252:255], a[192:195], v[64:67]\n" \
"ds_read_b128 a[128:131], v28 offset:49152\n" \
"s_or_b32 s38, s38, 0x5be8000\n" \
"v_mfma_f32_16x16x32_bf16 v[80:83], a[188:191], a[132:135], 0\n" \
"ds_read_b128 a[192:195], v29 offset:49152\n" \
"v_mfma_f32_16x16x32_bf16 v[80:83], a[252:255], a[196:199], v[80:83]\n" \
"s_movk_i32 s84, 0x3900\n" \
"v_mfma_f32_16x16x32_bf16 v[76:79], a[184:187], a[132:135], 0\n" \
"buffer_load_dwordx4 v22, s[64:67], s84 offen offset:2048 lds\n" \
"v_mfma_f32_16x16x32_bf16 v[76:79], a[248:251], a[196:199], v[76:79]\n" \
"s_movk_i32 s84, 0x5500\n" \
"v_mfma_f32_16x16x32_bf16 v[72:75], a[180:183], a[132:135], 0\n" \
"buffer_load_dwordx4 v23, s[64:67], s84 offen offset:3072 lds\n" \
"v_mfma_f32_16x16x32_bf16 v[72:75], a[244:247], a[196:199], v[72:75]\n" \
"s_add_u32 m0, s31, 0x0\n" \
"s_mov_b32 s84, 0x100000\n" \
"v_mfma_f32_16x16x32_bf16 v[68:71], a[176:179], a[132:135], 0\n" \
"buffer_load_dwordx4 v26, s[68:71], s84 offen offset:0 lds\n" \
"v_mfma_f32_16x16x32_bf16 v[68:71], a[240:243], a[196:199], v[68:71]\n" \
"ds_read_b128 a[132:135], v28 offset:51200\n" \
"v_mfma_f32_16x16x32_bf16 v[84:87], a[176:179], a[136:139], 0\n" \
"ds_read_b128 a[196:199], v29 offset:51200\n" \
"v_mfma_f32_16x16x32_bf16 v[84:87], a[240:243], a[200:203], v[84:87]\n" \
"s_addk_i32 s84, 0x7c00\n" \
"v_mfma_f32_16x16x32_bf16 v[88:91], a[180:183], a[136:139], 0\n" \
"buffer_load_dwordx4 v26, s[68:71], s84 offen offset:1024 lds\n" \
"v_mfma_f32_16x16x32_bf16 v[88:91], a[244:247], a[200:203], v[88:91]\n" \
"ds_read_b64_tr_b16 a[226:227], v32 offset:41984\n" \
"v_mfma_f32_16x16x32_bf16 v[92:95], a[184:187], a[136:139], 0\n" \
"s_addk_i32 s84, 0x7c00\n" \
"v_mfma_f32_16x16x32_bf16 v[92:95], a[248:251], a[200:203], v[92:95]\n" \
"buffer_load_dwordx4 v27, s[68:71], s84 offen offset:2048 lds\n" \
"v_mfma_f32_16x16x32_bf16 v[96:99], a[188:191], a[136:139], 0\n" \
"ds_read_b64_tr_b16 a[164:165], v33 offset:32768\n" \
"v_mfma_f32_16x16x32_bf16 v[96:99], a[252:255], a[200:203], v[96:99]\n" \
"ds_read_b128 a[136:139], v28 offset:53248\n" \
"v_mfma_f32_16x16x32_bf16 v[112:115], a[188:191], a[140:143], 0\n" \
"ds_read_b128 a[200:203], v29 offset:53248\n" \
"v_mfma_f32_16x16x32_bf16 v[112:115], a[252:255], a[204:207], v[112:115]\n" \
"s_addk_i32 s84, 0x7c00\n" \
"v_mfma_f32_16x16x32_bf16 v[108:111], a[184:187], a[140:143], 0\n" \
"buffer_load_dwordx4 v27, s[68:71], s84 offen offset:3072 lds\n" \
"v_mfma_f32_16x16x32_bf16 v[108:111], a[248:251], a[204:207], v[108:111]\n" \
"s_add_u32 m0, s30, 0x1000\n" \
"s_mov_b32 s84, 0x8100\n" \
"v_mfma_f32_16x16x32_bf16 v[104:107], a[180:183], a[140:143], 0\n" \
"buffer_load_dwordx4 v22, s[64:67], s84 offen offset:0 lds\n" \
"v_mfma_f32_16x16x32_bf16 v[104:107], a[244:247], a[204:207], v[104:107]\n" \
"ds_read_b64_tr_b16 a[166:167], v33 offset:33792\n" \
"v_mfma_f32_16x16x32_bf16 v[100:103], a[176:179], a[140:143], 0\n" \
"ds_read_b64_tr_b16 a[228:229], v33 offset:40960\n" \
"v_mfma_f32_16x16x32_bf16 v[100:103], a[240:243], a[204:207], v[100:103]\n" \
"ds_read_b128 a[140:143], v28 offset:55296\n" \
"v_mfma_f32_16x16x32_bf16 v[116:119], a[176:179], a[144:147], 0\n" \
"ds_read_b128 a[204:207], v29 offset:55296\n" \
"v_mfma_f32_16x16x32_bf16 v[116:119], a[240:243], a[208:211], v[116:119]\n" \
"s_addk_i32 s84, 0x1c00\n" \
"v_mfma_f32_16x16x32_bf16 v[120:123], a[180:183], a[144:147], 0\n" \
"buffer_load_dwordx4 v23, s[64:67], s84 offen offset:1024 lds\n" \
"v_mfma_f32_16x16x32_bf16 v[120:123], a[244:247], a[208:211], v[120:123]\n" \
"s_addk_i32 s84, 0x1c00\n" \
"v_mfma_f32_16x16x32_bf16 v[124:127], a[184:187], a[144:147], 0\n" \
"buffer_load_dwordx4 v22, s[64:67], s84 offen offset:2048 lds\n" \
"v_mfma_f32_16x16x32_bf16 v[124:127], a[248:251], a[208:211], v[124:127]\n" \
"s_addk_i32 s84, 0x1c00\n" \
"s_nop 0\n" \
"v_mfma_f32_16x16x32_bf16 v[128:131], a[188:191], a[144:147], 0\n" \
"buffer_load_dwordx4 v23, s[64:67], s84 offen offset:3072 lds\n" \
"v_mfma_f32_16x16x32_bf16 v[128:131], a[252:255], a[208:211], v[128:131]\n" \
"ds_read_b128 a[144:147], v28 offset:32768\n" \
"v_mfma_f32_16x16x32_bf16 v[144:147], a[188:191], a[148:151], 0\n" \
"ds_read_b128 a[208:211], v29 offset:32768\n" \
"v_mfma_f32_16x16x32_bf16 v[144:147], a[252:255], a[212:215], v[144:147]\n" \
"s_add_u32 m0, s31, 0x4000\n" \
"s_mov_b32 s84, 0x100000\n" \
"v_mfma_f32_16x16x32_bf16 v[140:143], a[184:187], a[148:151], 0\n" \
"buffer_load_dwordx4 v26, s[72:75], s84 offen offset:0 lds\n" \
"v_mfma_f32_16x16x32_bf16 v[140:143], a[248:251], a[212:215], v[140:143]\n" \
"ds_read_b64_tr_b16 a[230:231], v33 offset:41984\n" \
"v_mfma_f32_16x16x32_bf16 v[136:139], a[180:183], a[148:151], 0\n" \
"s_addk_i32 s84, 0x7c00\n" \
"s_nop 0\n" \
"v_mfma_f32_16x16x32_bf16 v[136:139], a[244:247], a[212:215], v[136:139]\n" \
"buffer_load_dwordx4 v26, s[72:75], s84 offen offset:1024 lds\n" \
"v_mfma_f32_16x16x32_bf16 v[132:135], a[176:179], a[148:151], 0\n" \
"ds_read_b64_tr_b16 a[168:169], v34 offset:32768\n" \
"v_mfma_f32_16x16x32_bf16 v[132:135], a[240:243], a[212:215], v[132:135]\n" \
"ds_read_b128 a[148:151], v28 offset:34816\n" \
".fill (L_pb + 3808 - .) / 4, 4, 0xBF800000\n" \
"L_pp1:\n" \
"s_cbranch_execz L_pret\n" \
"s_nop 0\n" \
"v_mfma_f32_16x16x32_bf16 v[148:151], a[176:179], a[152:155], 0\n" \
"ds_read_b128 a[212:215], v29 offset:34816\n" \
"v_mfma_f32_16x16x32_bf16 v[148:151], a[240:243], a[216:219], v[148:151]\n" \
"ds_read_b64_tr_b16 a[170:171], v34 offset:33792\n" \
"v_mfma_f32_16x16x32_bf16 v[152:155], a[180:183], a[152:155], 0\n" \
"ds_read_b64_tr_b16 a[232:233], v34 offset:40960\n" \
"v_mfma_f32_16x16x32_bf16 v[152:155], a[244:247], a[216:219], v[152:155]\n" \
"ds_read_b64_tr_b16 a[234:235], v34 offset:41984\n" \
"v_mfma_f32_16x16x32_bf16 v[156:159], a[184:187], a[152:155], 0\n" \
"ds_read_b64_tr_b16 a[172:173], v35 offset:32768\n" \
"v_mfma_f32_16x16x32_bf16 v[156:159], a[248:251], a[216:219], v[156:159]\n" \
"ds_read_b64_tr_b16 a[174:175], v35 offset:33792\n" \
"v_mfma_f32_16x16x32_bf16 v[160:163], a[188:191], a[152:155], 0\n" \
"ds_read_b64_tr_b16 a[236:237], v35 offset:40960\n" \
"v_mfma_f32_16x16x32_bf16 v[160:163], a[252:255], a[216:219], v[160:163]\n" \
"ds_read_b128 a[152:155], v28 offset:36864\n" \
"v_mfma_f32_16x16x32_bf16 v[176:179], a[188:191], a[156:159], 0\n" \
"ds_read_b128 a[216:219], v29 offset:36864\n" \
"v_mfma_f32_16x16x32_bf16 v[176:179], a[252:255], a[220:223], v[176:179]\n" \
"ds_read_b64_tr_b16 a[238:239], v35 offset:41984\n" \
"v_mfma_f32_16x16x32_bf16 v[172:175], a[184:187], a[156:159], 0\n" \
"s_cmp_lg_u32 s3, 0\n" \
"s_cselect_b32 s36, s37, s5\n" \
"v_mfma_f32_16x16x32_bf16 v[172:175], a[248:251], a[220:223], v[172:175]\n" \
"s_lshl_b32 s5, s6, 2\n" \
"s_or_b32 s5, s38, s5\n" \
"v_mfma_f32_16x16x32_bf16 v[168:171], a[180:183], a[156:159], 0\n" \
"s_lshl_b32 s35, s3, 10\n" \
"v_mfma_f32_16x16x32_bf16 v[168:171], a[244:247], a[220:223], v[168:171]\n" \
"s_cmpk_lt_u32 s3, 3\n" \
"s_cselect_b32 s37, s37, s5\n" \
"v_mfma_f32_16x16x32_bf16 v[164:167], a[176:179], a[156:159], 0\n" \
"s_lshl_b32 s5, s4, 16\n" \
"s_lshl_b32 s4, s4, 11\n" \
"v_mfma_f32_16x16x32_bf16 v[164:167], a[240:243], a[220:223], v[164:167]\n" \
"ds_read_b128 a[156:159], v28 offset:38912\n" \
"ds_read_b128 a[220:223], v29 offset:38912\n" \
"s_waitcnt lgkmcnt(15)\n" \
"v_mfma_f32_16x16x32_bf16 a[0:3], a[160:163], a[128:131], a[0:3]\n" \
"s_addk_i32 s84, 0x7c00\n" \
"v_mfma_f32_16x16x32_bf16 a[0:3], a[224:227], a[192:195], a[0:3]\n" \
"buffer_load_dwordx4 v27, s[72:75], s84 offen offset:2048 lds\n" \
"v_mfma_f32_16x16x32_bf16 a[4:7], a[164:167], a[128:131], a[4:7]\n" \
"ds_read_b64_tr_b16 a[176:177], v36 offset:32768\n" \
"v_cndmask_b32_e32 v198, v213, v212, vcc\n" \
"s_waitcnt lgkmcnt(15)\n" \
"v_mfma_f32_16x16x32_bf16 a[4:7], a[228:231], a[192:195], a[4:7]\n" \
"s_addk_i32 s84, 0x7c00\n" \
"s_waitcnt lgkmcnt(11)\n" \
"v_mfma_f32_16x16x32_bf16 a[8:11], a[168:171], a[128:131], a[8:11]\n" \
"buffer_load_dwordx4 v27, s[72:75], s84 offen offset:3072 lds\n" \
"s_waitcnt lgkmcnt(9)\n" \
"v_mfma_f32_16x16x32_bf16 a[8:11], a[232:235], a[192:195], a[8:11]\n" \
"ds_read_b64_tr_b16 a[178:179], v36 offset:33792\n" \
"v_or_b32_e32 v212, s35, v20\n" \
"s_waitcnt lgkmcnt(8)\n" \
"v_mfma_f32_16x16x32_bf16 a[12:15], a[172:175], a[128:131], a[12:15]\n" \
"ds_read_b64_tr_b16 a[240:241], v36 offset:40960\n" \
"s_or_b32 s39, s40, s35\n" \
"s_waitcnt lgkmcnt(5)\n" \
"v_mfma_f32_16x16x32_bf16 a[12:15], a[236:239], a[192:195], a[12:15]\n" \
"s_or_b32 s55, s41, s4\n" \
"v_or_b32_e32 v199, 0x22000, v212\n" \
"v_mfma_f32_16x16x32_bf16 a[28:31], a[172:175], a[132:135], a[28:31]\n" \
"s_add_i32 s35, s39, s7\n" \
"s_add_i32 s54, s41, s5\n" \
"v_mfma_f32_16x16x32_bf16 a[28:31], a[236:239], a[196:199], a[28:31]\n" \
"ds_read_b64_tr_b16 a[242:243], v36 offset:41984\n" \
"s_add_i32 s55, s55, 0x80000\n" \
"v_mfma_f32_16x16x32_bf16 a[24:27], a[168:171], a[132:135], a[24:27]\n" \
"v_mov_b32_e32 v215, 0xffff\n" \
"s_and_b32 s4, s3, 1\n" \
"v_mfma_f32_16x16x32_bf16 a[24:27], a[232:235], a[196:199], a[24:27]\n" \
"s_lshl_b32 s5, s4, 4\n" \
"v_lshrrev_b32_e32 v212, 4, v20\n" \
"v_mfma_f32_16x16x32_bf16 a[20:23], a[164:167], a[132:135], a[20:23]\n" \
"v_lshrrev_b32_e32 v218, 6, v20\n" \
"v_and_b32_e32 v219, 12, v218\n" \
"v_mfma_f32_16x16x32_bf16 a[20:23], a[228:231], a[196:199], a[20:23]\n" \
"ds_read_b64_tr_b16 a[180:181], v37 offset:32768\n" \
"v_and_or_b32 v214, v212, 15, s5\n" \
"v_mfma_f32_16x16x32_bf16 a[16:19], a[160:163], a[132:135], a[16:19]\n" \
"v_cmp_gt_u32_e64 vcc, v219, v214\n" \
"v_mov_b32_e32 v220, 0xffff0000\n" \
"v_mfma_f32_16x16x32_bf16 a[16:19], a[224:227], a[196:199], a[16:19]\n" \
"s_lshl_b32 s5, s2, 2\n" \
"v_cndmask_b32_e64 v217, v215, 0, vcc\n" \
"v_mfma_f32_16x16x32_bf16 a[32:35], a[160:163], a[136:139], a[32:35]\n" \
"s_mov_b32 m0, s32\n" \
"ds_read_b64_tr_b16 a[182:183], v37 offset:33792\n" \
"v_mfma_f32_16x16x32_bf16 a[32:35], a[224:227], a[200:203], a[32:35]\n" \
"s_mov_b32 exec_lo, 0x0\n" \
"s_mov_b32 exec_hi, 0xff\n" \
"global_load_lds_dword v[184:185], off\n" \
"s_mov_b64 exec, -1\n" \
"v_mfma_f32_16x16x32_bf16 a[36:39], a[164:167], a[136:139], a[36:39]\n" \
"v_cmp_lt_u32_e32 vcc, v219, v214\n" \
"v_mov_b32_e32 v213, s23\n" \
"v_mfma_f32_16x16x32_bf16 a[36:39], a[228:231], a[200:203], a[36:39]\n" \
"ds_read_b64_tr_b16 a[244:245], v37 offset:40960\n" \
"s_or_b32 s5, s5, s3\n" \
"v_mfma_f32_16x16x32_bf16 a[40:43], a[168:171], a[136:139], a[40:43]\n" \
"v_cndmask_b32_e32 v221, 0, v220, vcc\n" \
"v_or_b32_e64 v12, v217, v221\n" \
"v_mfma_f32_16x16x32_bf16 a[40:43], a[232:235], a[200:203], a[40:43]\n" \
"s_mov_b32 m0, s32\n" \
"v_or_b32_e32 v217, 2, v219\n" \
"v_mfma_f32_16x16x32_bf16 a[44:47], a[172:175], a[136:139], a[44:47]\n" \
"s_mov_b32 exec_lo, 0xffff0000\n" \
"s_mov_b32 exec_hi, 0x0\n" \
"global_load_lds_dword v[186:187], off\n" \
"s_mov_b64 exec, -1\n" \
"v_mfma_f32_16x16x32_bf16 a[44:47], a[236:239], a[200:203], a[44:47]\n" \
"ds_read_b64_tr_b16 a[246:247], v37 offset:41984\n" \
"v_cmp_gt_u32_e64 vcc, v217, v214\n" \
"v_mfma_f32_16x16x32_bf16 a[60:63], a[172:175], a[140:143], a[60:63]\n" \
"v_mov_b32_e32 v216, s25\n" \
"s_movk_i32 s40, 0xf400\n" \
"v_mfma_f32_16x16x32_bf16 a[60:63], a[236:239], a[204:207], a[60:63]\n" \
"v_cndmask_b32_e64 v221, v215, 0, vcc\n" \
"v_cmp_lt_u32_e64 vcc, v217, v214\n" \
"v_mfma_f32_16x16x32_bf16 a[56:59], a[168:171], a[140:143], a[56:59]\n" \
"ds_read_b64_tr_b16 a[184:185], v38 offset:32768\n" \
"s_lshl_b32 s4, s4, 6\n" \
"v_mfma_f32_16x16x32_bf16 a[56:59], a[232:235], a[204:207], a[56:59]\n" \
"v_cndmask_b32_e32 v217, 0, v220, vcc\n" \
"v_or_b32_e64 v13, v221, v217\n" \
"v_mfma_f32_16x16x32_bf16 a[52:55], a[164:167], a[140:143], a[52:55]\n" \
"v_or_b32_e32 v217, 16, v219\n" \
"v_cmp_gt_u32_e32 vcc, v217, v214\n" \
"v_mfma_f32_16x16x32_bf16 a[52:55], a[228:231], a[204:207], a[52:55]\n" \
"ds_read_b64_tr_b16 a[186:187], v38 offset:33792\n" \
"v_cndmask_b32_e64 v221, v215, 0, vcc\n" \
"v_mfma_f32_16x16x32_bf16 a[48:51], a[160:163], a[140:143], a[48:51]\n" \
"v_cmp_lt_u32_e32 vcc, v217, v214\n" \
"s_nop 1\n" \
"v_cndmask_b32_e64 v217, 0, v220, vcc\n" \
"v_mfma_f32_16x16x32_bf16 a[48:51], a[224:227], a[204:207], a[48:51]\n" \
"v_or_b32_e32 v14, v221, v217\n" \
"v_or_b32_e32 v217, 18, v219\n" \
"v_mfma_f32_16x16x32_bf16 a[64:67], a[160:163], a[144:147], a[64:67]\n" \
"ds_read_b64_tr_b16 a[248:249], v38 offset:40960\n" \
"v_cmp_gt_u32_e64 vcc, v217, v214\n" \
"v_mfma_f32_16x16x32_bf16 a[64:67], a[224:227], a[208:211], a[64:67]\n" \
"s_nop 0\n" \
"v_cndmask_b32_e64 v215, v215, 0, vcc\n" \
"v_cmp_lt_u32_e32 vcc, v217, v214\n" \
"v_mfma_f32_16x16x32_bf16 a[68:71], a[164:167], a[144:147], a[68:71]\n" \
"s_nop 0\n" \
"v_cndmask_b32_e32 v214, 0, v220, vcc\n" \
"v_or_b32_e64 v15, v215, v214\n" \
"v_mfma_f32_16x16x32_bf16 a[68:71], a[228:231], a[208:211], a[68:71]\n" \
"ds_read_b64_tr_b16 a[250:251], v38 offset:41984\n" \
"v_mad_u64_u32 v[214:215], s[6:7], s5, 42, v[212:213]\n" \
"v_mfma_f32_16x16x32_bf16 a[72:75], a[168:171], a[144:147], a[72:75]\n" \
"s_movk_i32 s5, 0x1440\n" \
"v_cmp_gt_u32_e32 vcc, s5, v214\n" \
"v_mfma_f32_16x16x32_bf16 a[72:75], a[232:235], a[208:211], a[72:75]\n" \
"v_min_u32_e32 v217, 0xa43f, v214\n" \
"v_mov_b32_e64 v214, s24\n" \
"v_mfma_f32_16x16x32_bf16 a[76:79], a[172:175], a[144:147], a[76:79]\n" \
"v_cndmask_b32_e32 v215, v216, v213, vcc\n" \
"v_mov_b32_e32 v213, s22\n" \
"v_mfma_f32_16x16x32_bf16 a[76:79], a[236:239], a[208:211], a[76:79]\n" \
"ds_read_b64_tr_b16 a[188:189], v39 offset:32768\n" \
"s_movk_i32 s6, 0xffee\n" \
"v_mfma_f32_16x16x32_bf16 a[92:95], a[172:175], a[148:151], a[92:95]\n" \
"v_cndmask_b32_e32 v214, v214, v213, vcc\n" \
"v_add_u32_e32 v213, 0xffffebc0, v217\n" \
"v_mfma_f32_16x16x32_bf16 a[92:95], a[236:239], a[212:215], a[92:95]\n" \
"s_lshl_b32 s5, s2, 5\n" \
"v_cndmask_b32_e32 v213, v213, v217, vcc\n" \
"v_mfma_f32_16x16x32_bf16 a[88:91], a[168:171], a[148:151], a[88:91]\n" \
"ds_read_b64_tr_b16 a[190:191], v39 offset:33792\n" \
"v_lshlrev_b32_e64 v216, 7, v213\n" \
"v_mfma_f32_16x16x32_bf16 a[88:91], a[232:235], a[212:215], a[88:91]\n" \
"v_mul_u32_u24_e32 v213, 0xe39, v212\n" \
"v_mov_b32_e64 v217, 0\n" \
"v_mfma_f32_16x16x32_bf16 a[84:87], a[164:167], a[148:151], a[84:87]\n" \
"v_lshrrev_b32_e64 v213, 16, v213\n" \
"v_lshl_add_u64 v[16:17], v[214:215], 0, v[216:217]\n" \
"v_mfma_f32_16x16x32_bf16 a[84:87], a[228:231], a[212:215], a[84:87]\n" \
"ds_read_b64_tr_b16 a[252:253], v39 offset:40960\n" \
"v_mad_i32_i24 v214, v213, s6, v212\n" \
"v_mfma_f32_16x16x32_bf16 a[80:83], a[160:163], a[148:151], a[80:83]\n" \
"s_add_i32 s5, s5, 32\n" \
"v_lshlrev_b32_e32 v215, 4, v214\n" \
"v_mfma_f32_16x16x32_bf16 a[80:83], a[224:227], a[212:215], a[80:83]\n" \
"v_add_u32_e64 v216, s5, v215\n" \
"v_and_b32_e32 v216, 0xf0, v216\n" \
"v_mfma_f32_16x16x32_bf16 a[96:99], a[160:163], a[152:155], a[96:99]\n" \
"ds_read_b64_tr_b16 a[254:255], v39 offset:41984\n" \
"v_cmp_gt_u32_e64 vcc, 16, v214\n" \
"v_mfma_f32_16x16x32_bf16 a[96:99], a[224:227], a[216:219], a[96:99]\n" \
"s_movk_i32 s7, 0xf800\n" \
"v_cndmask_b32_e32 v214, v215, v216, vcc\n" \
"v_mfma_f32_16x16x32_bf16 a[100:103], a[164:167], a[152:155], a[100:103]\n" \
"v_or_b32_e64 v215, 64, v212\n" \
"v_mul_u32_u24_e32 v216, 0xe39, v215\n" \
"v_mfma_f32_16x16x32_bf16 a[100:103], a[228:231], a[216:219], a[100:103]\n" \
"v_lshrrev_b32_e64 v216, 16, v216\n" \
"v_mad_i32_i24 v215, v216, s6, v215\n" \
"v_mfma_f32_16x16x32_bf16 a[104:107], a[168:171], a[152:155], a[104:107]\n" \
"v_lshlrev_b32_e32 v217, 4, v215\n" \
"v_add_u32_e32 v220, s5, v217\n" \
"v_mfma_f32_16x16x32_bf16 a[104:107], a[232:235], a[216:219], a[104:107]\n" \
"v_and_b32_e32 v220, 0xf0, v220\n" \
"v_cmp_gt_u32_e64 vcc, 16, v215\n" \
"v_mfma_f32_16x16x32_bf16 a[108:111], a[172:175], a[152:155], a[108:111]\n" \
"s_nop 0\n" \
"v_cndmask_b32_e32 v215, v217, v220, vcc\n" \
"v_or_b32_e32 v217, 0x80, v212\n" \
"v_mfma_f32_16x16x32_bf16 a[108:111], a[236:239], a[216:219], a[108:111]\n" \
"v_mul_u32_u24_e32 v220, 0xe39, v217\n" \
"v_lshrrev_b32_e32 v220, 16, v220\n" \
"s_waitcnt lgkmcnt(15)\n" \
"v_mfma_f32_16x16x32_bf16 a[124:127], a[172:175], a[156:159], a[124:127]\n" \
"v_mad_i32_i24 v217, v220, s6, v217\n" \
"v_lshlrev_b32_e64 v221, 4, v217\n" \
"v_mfma_f32_16x16x32_bf16 a[124:127], a[236:239], a[220:223], a[124:127]\n" \
"v_add_u32_e64 v222, s5, v221\n" \
"v_and_b32_e32 v222, 0xf0, v222\n" \
"v_mfma_f32_16x16x32_bf16 a[120:123], a[168:171], a[156:159], a[120:123]\n" \
"v_cmp_gt_u32_e32 vcc, 16, v217\n" \
"s_nop 1\n" \
"v_cndmask_b32_e64 v217, v221, v222, vcc\n" \
"v_mfma_f32_16x16x32_bf16 a[120:123], a[232:235], a[220:223], a[120:123]\n" \
"v_or_b32_e32 v221, 0xc0, v212\n" \
"v_mul_u32_u24_e32 v222, 0xe39, v221\n" \
"v_mfma_f32_16x16x32_bf16 a[116:119], a[164:167], a[156:159], a[116:119]\n" \
"v_lshrrev_b32_e64 v222, 16, v222\n" \
"v_mad_i32_i24 v221, v222, s6, v221\n" \
"v_mfma_f32_16x16x32_bf16 a[116:119], a[228:231], a[220:223], a[116:119]\n" \
"v_lshlrev_b32_e32 v223, 4, v221\n" \
"v_add_u32_e32 v224, s5, v223\n" \
"v_mfma_f32_16x16x32_bf16 a[112:115], a[160:163], a[156:159], a[112:115]\n" \
"v_and_b32_e32 v224, 0xf0, v224\n" \
"v_cmp_gt_u32_e64 vcc, 16, v221\n" \
"v_mfma_f32_16x16x32_bf16 a[112:115], a[224:227], a[220:223], a[112:115]\n" \
"s_waitcnt vmcnt(20)\n" \
"v_readfirstlane_b32 s62, v180\n" \
"s_nop 3\n" \
"s_add_u32 s62, s62, 0x9E3779B1\n" \
"s_waitcnt vmcnt(2) lgkmcnt(0)\n" \
"s_barrier\n" \
"v_mfma_f32_16x16x32_bf16 v[52:55], a[176:179], a[128:131], v[52:55]\n" \
"s_add_u32 m0, s30, 0x8000\n" \
"s_movk_i32 s84, 0x180\n" \
"v_mfma_f32_16x16x32_bf16 v[52:55], a[240:243], a[192:195], v[52:55]\n" \
"buffer_load_dwordx4 v22, s[64:67], s84 offen offset:0 lds\n" \
"v_mfma_f32_16x16x32_bf16 v[56:59], a[180:183], a[128:131], v[56:59]\n" \
"ds_read_b64_tr_b16 a[160:161], v32 offset:0\n" \
"v_cndmask_b32_e64 v221, v223, v224, vcc\n" \
"v_mfma_f32_16x16x32_bf16 v[56:59], a[244:247], a[192:195], v[56:59]\n" \
"s_movk_i32 s84, 0x1d80\n" \
"v_mfma_f32_16x16x32_bf16 v[60:63], a[184:187], a[128:131], v[60:63]\n" \
"buffer_load_dwordx4 v23, s[64:67], s84 offen offset:1024 lds\n" \
"v_mfma_f32_16x16x32_bf16 v[60:63], a[248:251], a[192:195], v[60:63]\n" \
"ds_read_b64_tr_b16 a[162:163], v32 offset:1024\n" \
"v_or_b32_e32 v223, 0x100, v212\n" \
"v_mfma_f32_16x16x32_bf16 v[64:67], a[188:191], a[128:131], v[64:67]\n" \
"ds_read_b64_tr_b16 a[224:225], v32 offset:8192\n" \
"v_mul_u32_u24_e32 v224, 0xe39, v223\n" \
"v_mfma_f32_16x16x32_bf16 v[64:67], a[252:255], a[192:195], v[64:67]\n" \
"ds_read_b128 a[128:131], v28 offset:16384\n" \
"v_lshrrev_b32_e32 v224, 16, v224\n" \
"v_mfma_f32_16x16x32_bf16 v[80:83], a[188:191], a[132:135], v[80:83]\n" \
"ds_read_b128 a[192:195], v29 offset:16384\n" \
"v_mad_i32_i24 v223, v224, s6, v223\n" \
"v_mfma_f32_16x16x32_bf16 v[80:83], a[252:255], a[196:199], v[80:83]\n" \
"s_movk_i32 s84, 0x3980\n" \
"v_mfma_f32_16x16x32_bf16 v[76:79], a[184:187], a[132:135], v[76:79]\n" \
"buffer_load_dwordx4 v22, s[64:67], s84 offen offset:2048 lds\n" \
"v_mfma_f32_16x16x32_bf16 v[76:79], a[248:251], a[196:199], v[76:79]\n" \
"s_movk_i32 s84, 0x5580\n" \
"v_mfma_f32_16x16x32_bf16 v[72:75], a[180:183], a[132:135], v[72:75]\n" \
"buffer_load_dwordx4 v23, s[64:67], s84 offen offset:3072 lds\n" \
"v_mfma_f32_16x16x32_bf16 v[72:75], a[244:247], a[196:199], v[72:75]\n" \
"s_add_u32 m0, s31, 0x8000\n" \
"s_mov_b32 s84, 0x180000\n" \
"v_mfma_f32_16x16x32_bf16 v[68:71], a[176:179], a[132:135], v[68:71]\n" \
"buffer_load_dwordx4 v26, s[68:71], s84 offen offset:0 lds\n" \
"v_mfma_f32_16x16x32_bf16 v[68:71], a[240:243], a[196:199], v[68:71]\n" \
"ds_read_b128 a[132:135], v28 offset:18432\n" \
"v_lshlrev_b32_e64 v225, 4, v223\n" \
"v_mfma_f32_16x16x32_bf16 v[84:87], a[176:179], a[136:139], v[84:87]\n" \
"ds_read_b128 a[196:199], v29 offset:18432\n" \
"v_add_u32_e64 v226, s5, v225\n" \
"v_mfma_f32_16x16x32_bf16 v[84:87], a[240:243], a[200:203], v[84:87]\n" \
"s_addk_i32 s84, 0x7c00\n" \
"v_mfma_f32_16x16x32_bf16 v[88:91], a[180:183], a[136:139], v[88:91]\n" \
"buffer_load_dwordx4 v26, s[68:71], s84 offen offset:1024 lds\n" \
"v_mfma_f32_16x16x32_bf16 v[88:91], a[244:247], a[200:203], v[88:91]\n" \
"ds_read_b64_tr_b16 a[226:227], v32 offset:9216\n" \
"v_and_b32_e32 v226, 0xf0, v226\n" \
"v_mfma_f32_16x16x32_bf16 v[92:95], a[184:187], a[136:139], v[92:95]\n" \
"s_addk_i32 s84, 0x7c00\n" \
"v_mfma_f32_16x16x32_bf16 v[92:95], a[248:251], a[200:203], v[92:95]\n" \
"buffer_load_dwordx4 v27, s[68:71], s84 offen offset:2048 lds\n" \
"v_mfma_f32_16x16x32_bf16 v[96:99], a[188:191], a[136:139], v[96:99]\n" \
"ds_read_b64_tr_b16 a[164:165], v33 offset:0\n" \
"v_cmp_gt_u32_e64 vcc, 16, v223\n" \
"v_mfma_f32_16x16x32_bf16 v[96:99], a[252:255], a[200:203], v[96:99]\n" \
"ds_read_b128 a[136:139], v28 offset:20480\n" \
"v_cndmask_b32_e64 v223, v225, v226, vcc\n" \
"v_mfma_f32_16x16x32_bf16 v[112:115], a[188:191], a[140:143], v[112:115]\n" \
"ds_read_b128 a[200:203], v29 offset:20480\n" \
"v_or_b32_e32 v225, 0x140, v212\n" \
"v_mfma_f32_16x16x32_bf16 v[112:115], a[252:255], a[204:207], v[112:115]\n" \
"s_addk_i32 s84, 0x7c00\n" \
"s_nop 0\n" \
"v_mfma_f32_16x16x32_bf16 v[108:111], a[184:187], a[140:143], v[108:111]\n" \
"buffer_load_dwordx4 v27, s[68:71], s84 offen offset:3072 lds\n" \
"v_mfma_f32_16x16x32_bf16 v[108:111], a[248:251], a[204:207], v[108:111]\n" \
"s_add_u32 m0, s30, 0x9000\n" \
"s_mov_b32 s84, 0x8180\n" \
"v_mfma_f32_16x16x32_bf16 v[104:107], a[180:183], a[140:143], v[104:107]\n" \
"buffer_load_dwordx4 v22, s[64:67], s84 offen offset:0 lds\n" \
"v_mfma_f32_16x16x32_bf16 v[104:107], a[244:247], a[204:207], v[104:107]\n" \
"ds_read_b64_tr_b16 a[166:167], v33 offset:1024\n" \
"v_mul_u32_u24_e32 v226, 0xe39, v225\n" \
"v_mfma_f32_16x16x32_bf16 v[100:103], a[176:179], a[140:143], v[100:103]\n" \
"ds_read_b64_tr_b16 a[228:229], v33 offset:8192\n" \
"v_lshrrev_b32_e64 v226, 16, v226\n" \
"v_mfma_f32_16x16x32_bf16 v[100:103], a[240:243], a[204:207], v[100:103]\n" \
"ds_read_b128 a[140:143], v28 offset:22528\n" \
"v_mad_i32_i24 v225, v226, s6, v225\n" \
"v_mfma_f32_16x16x32_bf16 v[116:119], a[176:179], a[144:147], v[116:119]\n" \
"ds_read_b128 a[204:207], v29 offset:22528\n" \
"v_lshlrev_b32_e64 v227, 4, v225\n" \
"v_mfma_f32_16x16x32_bf16 v[116:119], a[240:243], a[208:211], v[116:119]\n" \
"s_addk_i32 s84, 0x1c00\n" \
"v_mfma_f32_16x16x32_bf16 v[120:123], a[180:183], a[144:147], v[120:123]\n" \
"buffer_load_dwordx4 v23, s[64:67], s84 offen offset:1024 lds\n" \
"v_mfma_f32_16x16x32_bf16 v[120:123], a[244:247], a[208:211], v[120:123]\n" \
"s_addk_i32 s84, 0x1c00\n" \
"v_mfma_f32_16x16x32_bf16 v[124:127], a[184:187], a[144:147], v[124:127]\n" \
"buffer_load_dwordx4 v22, s[64:67], s84 offen offset:2048 lds\n" \
"v_mfma_f32_16x16x32_bf16 v[124:127], a[248:251], a[208:211], v[124:127]\n" \
"s_addk_i32 s84, 0x1c00\n" \
"v_mfma_f32_16x16x32_bf16 v[128:131], a[188:191], a[144:147], v[128:131]\n" \
"buffer_load_dwordx4 v23, s[64:67], s84 offen offset:3072 lds\n" \
"v_mfma_f32_16x16x32_bf16 v[128:131], a[252:255], a[208:211], v[128:131]\n" \
"ds_read_b128 a[144:147], v28 offset:0\n" \
"v_add_u32_e32 v228, s5, v227\n" \
"v_mfma_f32_16x16x32_bf16 v[144:147], a[188:191], a[148:151], v[144:147]\n" \
"ds_read_b128 a[208:211], v29 offset:0\n" \
"v_and_b32_e32 v228, 0xf0, v228\n" \
"v_mfma_f32_16x16x32_bf16 v[144:147], a[252:255], a[212:215], v[144:147]\n" \
"s_add_u32 m0, s31, 0xc000\n" \
"s_mov_b32 s84, 0x180000\n" \
"v_mfma_f32_16x16x32_bf16 v[140:143], a[184:187], a[148:151], v[140:143]\n" \
"buffer_load_dwordx4 v26, s[72:75], s84 offen offset:0 lds\n" \
"v_mfma_f32_16x16x32_bf16 v[140:143], a[248:251], a[212:215], v[140:143]\n" \
"ds_read_b64_tr_b16 a[230:231], v33 offset:9216\n" \
"v_cmp_gt_u32_e64 vcc, 16, v225\n" \
"v_mfma_f32_16x16x32_bf16 v[136:139], a[180:183], a[148:151], v[136:139]\n" \
"s_addk_i32 s84, 0x7c00\n" \
"v_mfma_f32_16x16x32_bf16 v[136:139], a[244:247], a[212:215], v[136:139]\n" \
"buffer_load_dwordx4 v26, s[72:75], s84 offen offset:1024 lds\n" \
"v_mfma_f32_16x16x32_bf16 v[132:135], a[176:179], a[148:151], v[132:135]\n" \
"ds_read_b64_tr_b16 a[168:169], v34 offset:0\n" \
"v_cndmask_b32_e32 v225, v227, v228, vcc\n" \
"v_mfma_f32_16x16x32_bf16 v[132:135], a[240:243], a[212:215], v[132:135]\n" \
"ds_read_b128 a[148:151], v28 offset:2048\n" \
"v_or_b32_e32 v227, 0x180, v212\n" \
"v_mfma_f32_16x16x32_bf16 v[148:151], a[176:179], a[152:155], v[148:151]\n" \
"ds_read_b128 a[212:215], v29 offset:2048\n" \
"v_mul_u32_u24_e32 v228, 0xe39, v227\n" \
"v_mfma_f32_16x16x32_bf16 v[148:151], a[240:243], a[216:219], v[148:151]\n" \
"ds_read_b64_tr_b16 a[170:171], v34 offset:1024\n" \
"v_lshrrev_b32_e64 v228, 16, v228\n" \
"v_mfma_f32_16x16x32_bf16 v[152:155], a[180:183], a[152:155], v[152:155]\n" \
"ds_read_b64_tr_b16 a[232:233], v34 offset:8192\n" \
"v_mad_i32_i24 v227, v228, s6, v227\n" \
"v_mfma_f32_16x16x32_bf16 v[152:155], a[244:247], a[216:219], v[152:155]\n" \
"ds_read_b64_tr_b16 a[234:235], v34 offset:9216\n" \
"v_lshlrev_b32_e64 v229, 4, v227\n" \
"v_mfma_f32_16x16x32_bf16 v[156:159], a[184:187], a[152:155], v[156:159]\n" \
"ds_read_b64_tr_b16 a[172:173], v35 offset:0\n" \
"v_add_u32_e64 v230, s5, v229\n" \
"v_mfma_f32_16x16x32_bf16 v[156:159], a[248:251], a[216:219], v[156:159]\n" \
"ds_read_b64_tr_b16 a[174:175], v35 offset:1024\n" \
"v_and_b32_e32 v230, 0xf0, v230\n" \
"v_mfma_f32_16x16x32_bf16 v[160:163], a[188:191], a[152:155], v[160:163]\n" \
"ds_read_b64_tr_b16 a[236:237], v35 offset:8192\n" \
"v_cmp_gt_u32_e64 vcc, 16, v227\n" \
"v_mfma_f32_16x16x32_bf16 v[160:163], a[252:255], a[216:219], v[160:163]\n" \
"ds_read_b128 a[152:155], v28 offset:4096\n" \
"v_cndmask_b32_e64 v227, v229, v230, vcc\n" \
"v_mfma_f32_16x16x32_bf16 v[176:179], a[188:191], a[156:159], v[176:179]\n" \
"ds_read_b128 a[216:219], v29 offset:4096\n" \
"v_or_b32_e32 v229, 0x1c0, v212\n" \
"v_mfma_f32_16x16x32_bf16 v[176:179], a[252:255], a[220:223], v[176:179]\n" \
"ds_read_b64_tr_b16 a[238:239], v35 offset:9216\n" \
"v_mul_u32_u24_e32 v230, 0xe39, v229\n" \
"v_mfma_f32_16x16x32_bf16 v[172:175], a[184:187], a[156:159], v[172:175]\n" \
"v_lshrrev_b32_e64 v230, 16, v230\n" \
"v_mad_i32_i24 v229, v230, s6, v229\n" \
"v_mfma_f32_16x16x32_bf16 v[172:175], a[248:251], a[220:223], v[172:175]\n" \
"v_lshlrev_b32_e32 v231, 4, v229\n" \
"v_add_u32_e32 v232, s5, v231\n" \
"v_mfma_f32_16x16x32_bf16 v[168:171], a[180:183], a[156:159], v[168:171]\n" \
"v_and_b32_e32 v232, 0xf0, v232\n" \
"v_cmp_gt_u32_e64 vcc, 16, v229\n" \
"v_mfma_f32_16x16x32_bf16 v[168:171], a[244:247], a[220:223], v[168:171]\n" \
"s_nop 0\n" \
"v_cndmask_b32_e32 v229, v231, v232, vcc\n" \
"v_or_b32_e32 v231, 0x200, v212\n" \
"v_mfma_f32_16x16x32_bf16 v[164:167], a[176:179], a[156:159], v[164:167]\n" \
"v_mul_u32_u24_e32 v232, 0xe39, v231\n" \
"v_lshrrev_b32_e64 v232, 16, v232\n" \
"v_mfma_f32_16x16x32_bf16 v[164:167], a[240:243], a[220:223], v[164:167]\n" \
"ds_read_b128 a[156:159], v28 offset:6144\n" \
"ds_read_b128 a[220:223], v29 offset:6144\n" \
"s_waitcnt lgkmcnt(15)\n" \
"v_mfma_f32_16x16x32_bf16 a[0:3], a[160:163], a[128:131], a[0:3]\n" \
"s_addk_i32 s84, 0x7c00\n" \
"v_mfma_f32_16x16x32_bf16 a[0:3], a[224:227], a[192:195], a[0:3]\n" \
"buffer_load_dwordx4 v27, s[72:75], s84 offen offset:2048 lds\n" \
"v_mfma_f32_16x16x32_bf16 a[4:7], a[164:167], a[128:131], a[4:7]\n" \
"ds_read_b64_tr_b16 a[176:177], v36 offset:0\n" \
"v_mad_i32_i24 v231, v232, s6, v231\n" \
"s_waitcnt lgkmcnt(15)\n" \
"v_mfma_f32_16x16x32_bf16 a[4:7], a[228:231], a[192:195], a[4:7]\n" \
"s_addk_i32 s84, 0x7c00\n" \
"s_waitcnt lgkmcnt(11)\n" \
"v_mfma_f32_16x16x32_bf16 a[8:11], a[168:171], a[128:131], a[8:11]\n" \
"buffer_load_dwordx4 v27, s[72:75], s84 offen offset:3072 lds\n" \
"s_waitcnt lgkmcnt(9)\n" \
"v_mfma_f32_16x16x32_bf16 a[8:11], a[232:235], a[192:195], a[8:11]\n" \
"ds_read_b64_tr_b16 a[178:179], v36 offset:1024\n" \
"v_lshlrev_b32_e32 v233, 4, v231\n" \
"s_waitcnt lgkmcnt(8)\n" \
"v_mfma_f32_16x16x32_bf16 a[12:15], a[172:175], a[128:131], a[12:15]\n" \
"ds_read_b64_tr_b16 a[240:241], v36 offset:8192\n" \
"v_add_u32_e32 v234, s5, v233\n" \
"s_waitcnt lgkmcnt(5)\n" \
"v_mfma_f32_16x16x32_bf16 a[12:15], a[236:239], a[192:195], a[12:15]\n" \
"s_movk_i32 s5, 0x480\n" \
"v_mad_u32_u24 v0, v213, s5, v214\n" \
"v_mfma_f32_16x16x32_bf16 a[28:31], a[172:175], a[132:135], a[28:31]\n" \
"v_mul_u32_u24_e32 v213, 0x480, v216\n" \
"s_movk_i32 s6, 0xfc00\n" \
"v_mfma_f32_16x16x32_bf16 a[28:31], a[236:239], a[196:199], a[28:31]\n" \
"ds_read_b64_tr_b16 a[242:243], v36 offset:9216\n" \
"v_add3_u32 v1, v213, v215, s6\n" \
"v_mfma_f32_16x16x32_bf16 a[24:27], a[168:171], a[132:135], a[24:27]\n" \
"buffer_load_dwordx4 v[200:203], v192, s[76:79], s34 offen offset:0\n" \
"v_mfma_f32_16x16x32_bf16 a[24:27], a[232:235], a[196:199], a[24:27]\n" \
"v_mul_u32_u24_e32 v213, 0x480, v220\n" \
"v_add3_u32 v2, v213, v217, s7\n" \
"v_mfma_f32_16x16x32_bf16 a[20:23], a[164:167], a[132:135], a[20:23]\n" \
"v_mul_u32_u24_e32 v213, 0x480, v222\n" \
"v_add3_u32 v3, v213, v221, s40\n" \
"v_mfma_f32_16x16x32_bf16 a[20:23], a[228:231], a[196:199], a[20:23]\n" \
"ds_read_b64_tr_b16 a[180:181], v37 offset:0\n" \
"v_mul_u32_u24_e32 v213, 0x480, v226\n" \
"v_mfma_f32_16x16x32_bf16 a[16:19], a[160:163], a[132:135], a[16:19]\n" \
"v_add3_u32 v5, v213, v225, s6\n" \
"v_mul_u32_u24_e32 v213, 0x480, v228\n" \
"v_mfma_f32_16x16x32_bf16 a[16:19], a[224:227], a[196:199], a[16:19]\n" \
"v_add3_u32 v6, v213, v227, s7\n" \
"v_mul_u32_u24_e32 v213, 0x480, v230\n" \
"v_mfma_f32_16x16x32_bf16 a[32:35], a[160:163], a[136:139], a[32:35]\n" \
"buffer_load_dwordx4 v[204:207], v193, s[76:79], s34 offen offset:0\n" \
"v_mfma_f32_16x16x32_bf16 a[32:35], a[224:227], a[200:203], a[32:35]\n" \
"s_mov_b32 m0, s32\n" \
"ds_read_b64_tr_b16 a[182:183], v37 offset:1024\n" \
"v_mfma_f32_16x16x32_bf16 a[36:39], a[164:167], a[136:139], a[36:39]\n" \
"s_mov_b32 exec_lo, 0x0\n" \
"s_mov_b32 exec_hi, 0xff00\n" \
"global_load_lds_dword v[184:185], off\n" \
"s_mov_b64 exec, -1\n" \
"v_mfma_f32_16x16x32_bf16 a[36:39], a[228:231], a[200:203], a[36:39]\n" \
"ds_read_b64_tr_b16 a[244:245], v37 offset:8192\n" \
"v_add3_u32 v7, v213, v229, s40\n" \
"v_mfma_f32_16x16x32_bf16 a[40:43], a[168:171], a[136:139], a[40:43]\n" \
"s_lshl_b32 s7, s3, 3\n" \
"s_lshl_b32 s40, s3, 1\n" \
"v_mfma_f32_16x16x32_bf16 a[40:43], a[232:235], a[200:203], a[40:43]\n" \
"buffer_load_dword v208, v194, s[76:79], s34 offen offset:32\n" \
"v_mfma_f32_16x16x32_bf16 a[44:47], a[172:175], a[136:139], a[44:47]\n" \
"ds_read_b64_tr_b16 a[246:247], v37 offset:9216\n" \
"s_and_b32 s7, s7, 16\n" \
"v_mfma_f32_16x16x32_bf16 a[44:47], a[236:239], a[200:203], a[44:47]\n" \
"s_and_b32 s40, s40, 2\n" \
"s_and_b32 s6, s2, 7\n" \
"v_mfma_f32_16x16x32_bf16 a[60:63], a[172:175], a[140:143], a[60:63]\n" \
"v_bfe_u32 v213, v20, 7, 1\n" \
"s_or_b32 s7, s40, s7\n" \
"v_mfma_f32_16x16x32_bf16 a[60:63], a[236:239], a[204:207], a[60:63]\n" \
"v_and_b32_e32 v234, 0xf0, v234\n" \
"v_cmp_gt_u32_e32 vcc, 16, v231\n" \
"v_mfma_f32_16x16x32_bf16 a[56:59], a[168:171], a[140:143], a[56:59]\n" \
"buffer_load_dword v209, v194, s[76:79], s34 offen offset:1184\n" \
"v_mfma_f32_16x16x32_bf16 a[56:59], a[232:235], a[204:207], a[56:59]\n" \
"ds_read_b64_tr_b16 a[184:185], v38 offset:0\n" \
"v_or3_b32 v213, s7, v213, v219\n" \
"v_mfma_f32_16x16x32_bf16 a[52:55], a[164:167], a[140:143], a[52:55]\n" \
"v_and_b32_e32 v214, 32, v20\n" \
"s_lshl_b32 s6, s6, 5\n" \
"v_mfma_f32_16x16x32_bf16 a[52:55], a[228:231], a[204:207], a[52:55]\n" \
"ds_read_b64_tr_b16 a[186:187], v38 offset:1024\n" \
"v_cndmask_b32_e64 v231, v233, v234, vcc\n" \
"v_mfma_f32_16x16x32_bf16 a[48:51], a[160:163], a[140:143], a[48:51]\n" \
"v_mul_u32_u24_e64 v213, 18, v213\n" \
"v_mov_b32_e32 v215, 0x100\n" \
"v_mfma_f32_16x16x32_bf16 a[48:51], a[224:227], a[204:207], a[48:51]\n" \
"v_mov_b32_e32 v216, s6\n" \
"v_cmp_eq_u32_e32 vcc, 0, v214\n" \
"v_mfma_f32_16x16x32_bf16 a[64:67], a[160:163], a[144:147], a[64:67]\n" \
"buffer_load_dword v211, v194, s[76:79], s34 offen offset:2336\n" \
"v_mfma_f32_16x16x32_bf16 a[64:67], a[224:227], a[208:211], a[64:67]\n" \
"ds_read_b64_tr_b16 a[248:249], v38 offset:8192\n" \
"v_and_or_b32 v213, v218, 1, v213\n" \
"v_mfma_f32_16x16x32_bf16 a[68:71], a[164:167], a[144:147], a[68:71]\n" \
"v_lshlrev_b32_e32 v212, 3, v212\n" \
"v_cndmask_b32_e32 v214, v215, v216, vcc\n" \
"v_mfma_f32_16x16x32_bf16 a[68:71], a[228:231], a[208:211], a[68:71]\n" \
"ds_read_b64_tr_b16 a[250:251], v38 offset:9216\n" \
"v_mad_u32_u24 v213, v213, s5, v214\n" \
"v_mfma_f32_16x16x32_bf16 a[72:75], a[168:171], a[144:147], a[72:75]\n" \
"v_and_or_b32 v213, v20, 16, v213\n" \
"v_add_u32_e32 v9, 0x4800, v213\n" \
"v_mfma_f32_16x16x32_bf16 a[72:75], a[232:235], a[208:211], a[72:75]\n" \
"s_mov_b32 s76, s20\n" \
"s_and_b32 s77, s21, 0xffff\n" \
"v_mfma_f32_16x16x32_bf16 a[76:79], a[172:175], a[144:147], a[76:79]\n" \
"s_mov_b32 s78, 0x480\n" \
"v_mul_u32_u24_e32 v213, 0x120, v218\n" \
"v_mfma_f32_16x16x32_bf16 a[76:79], a[236:239], a[208:211], a[76:79]\n" \
"buffer_load_ushort v210, v195, s[76:79], 0 offen\n" \
"v_mfma_f32_16x16x32_bf16 a[92:95], a[172:175], a[148:151], a[92:95]\n" \
"ds_read_b64_tr_b16 a[188:189], v39 offset:0\n" \
"v_and_or_b32 v10, v212, 24, v213\n" \
"v_mfma_f32_16x16x32_bf16 a[92:95], a[236:239], a[212:215], a[92:95]\n" \
"v_lshrrev_b32_e32 v213, 2, v20\n" \
"v_and_or_b32 v213, v213, 60, s4\n" \
"v_mfma_f32_16x16x32_bf16 a[88:91], a[168:171], a[148:151], a[88:91]\n" \
"s_mov_b32 s76, s8\n" \
"s_nop 0\n" \
"s_and_b32 s77, s9, 0xffff\n" \
"v_mfma_f32_16x16x32_bf16 a[88:91], a[232:235], a[212:215], a[88:91]\n" \
"s_mov_b32 s78, 0x240000\n" \
"ds_read_b64_tr_b16 a[190:191], v39 offset:1024\n" \
"v_mfma_f32_16x16x32_bf16 a[84:87], a[164:167], a[148:151], a[84:87]\n" \
"v_lshl_or_b32 v212, s3, 9, v212\n" \
"v_mad_u32_u24 v4, v224, s5, v223\n" \
"v_mfma_f32_16x16x32_bf16 a[84:87], a[228:231], a[212:215], a[84:87]\n" \
"ds_read_b64_tr_b16 a[252:253], v39 offset:8192\n" \
"v_mad_u32_u24 v8, v232, s5, v231\n" \
"v_mfma_f32_16x16x32_bf16 a[80:83], a[160:163], a[148:151], a[80:83]\n" \
"v_or_b32_e32 v18, 0x27000, v213\n" \
"v_or_b32_e32 v19, 0x27800, v212\n" \
"v_mfma_f32_16x16x32_bf16 a[80:83], a[224:227], a[212:215], a[80:83]\n" \
"v_mov_b32_e32 v219, 0x1000c0c\n" \
"v_mov_b32_e32 v220, 0xc0c0100\n" \
"v_mfma_f32_16x16x32_bf16 a[96:99], a[160:163], a[152:155], a[96:99]\n" \
"ds_read_b64_tr_b16 a[254:255], v39 offset:9216\n" \
"v_bfe_u32 v218, v20, 4, 1\n" \
"v_mfma_f32_16x16x32_bf16 a[96:99], a[224:227], a[216:219], a[96:99]\n" \
"v_lshrrev_b32_e32 v213, 8, v20\n" \
"v_cmp_eq_u32_e32 vcc, 0, v218\n" \
"v_mfma_f32_16x16x32_bf16 a[100:103], a[164:167], a[152:155], a[100:103]\n" \
"s_lshr_b32 s4, s2, 5\n" \
"s_and_b32 s5, s3, 1\n" \
"v_mfma_f32_16x16x32_bf16 a[100:103], a[228:231], a[216:219], a[100:103]\n" \
"v_bfe_u32 v217, v20, 4, 4\n" \
"v_cndmask_b32_e64 v219, v219, v220, vcc\n" \
"v_mfma_f32_16x16x32_bf16 a[104:107], a[168:171], a[152:155], a[104:107]\n" \
"v_mov_b32_e32 v220, 0x3020c0c\n" \
"v_mov_b32_e32 v221, 0xc0c0302\n" \
"v_mfma_f32_16x16x32_bf16 a[104:107], a[232:235], a[216:219], a[104:107]\n" \
"v_lshl_or_b32 v218, v213, 2, v218\n" \
"s_xor_b32 s6, s4, 15\n" \
"v_mfma_f32_16x16x32_bf16 a[108:111], a[172:175], a[152:155], a[108:111]\n" \
"s_cmpk_lt_u32 s3, 2\n" \
"s_cselect_b32 s7, s6, s4\n" \
"v_mfma_f32_16x16x32_bf16 a[108:111], a[236:239], a[216:219], a[108:111]\n" \
"s_lshl_b32 s40, s7, 7\n" \
".fill (L_pb + 7904 - .) / 4, 4, 0xBF800000\n" \
"L_pp2:\n" \
"s_cbranch_execz L_pret\n" \
"s_lshl_b32 s41, s5, 6\n" \
"s_waitcnt lgkmcnt(15)\n" \
"v_mfma_f32_16x16x32_bf16 a[124:127], a[172:175], a[156:159], a[124:127]\n" \
"v_cndmask_b32_e32 v220, v220, v221, vcc\n" \
"v_mov_b32_e32 v221, 0xc0c0c0c\n" \
"v_mfma_f32_16x16x32_bf16 a[124:127], a[236:239], a[220:223], a[124:127]\n" \
"v_cmp_gt_u32_e32 vcc, v218, v217\n" \
"v_or_b32_e32 v218, 2, v218\n" \
"v_mfma_f32_16x16x32_bf16 a[120:123], a[168:171], a[156:159], a[120:123]\n" \
"v_lshrrev_b32_e32 v212, 7, v20\n" \
"s_or_b32 s41, s41, s40\n" \
"v_mfma_f32_16x16x32_bf16 a[120:123], a[232:235], a[220:223], a[120:123]\n" \
"v_cndmask_b32_e32 v40, v221, v219, vcc\n" \
"v_cndmask_b32_e32 v42, v219, v221, vcc\n" \
"v_mfma_f32_16x16x32_bf16 a[116:119], a[164:167], a[156:159], a[116:119]\n" \
"v_cmp_gt_u32_e32 vcc, v218, v217\n" \
"v_and_b32_e32 v218, 16, v20\n" \
"v_mfma_f32_16x16x32_bf16 a[116:119], a[228:231], a[220:223], a[116:119]\n" \
"s_movk_i32 s42, 0x3f80\n" \
"v_or_b32_e32 v214, s41, v212\n" \
"v_mfma_f32_16x16x32_bf16 a[112:115], a[160:163], a[156:159], a[112:115]\n" \
"v_and_b32_e32 v217, 0x320, v20\n" \
"s_movk_i32 s41, 0x100\n" \
"v_mfma_f32_16x16x32_bf16 a[112:115], a[224:227], a[220:223], a[112:115]\n" \
"s_waitcnt vmcnt(7) lgkmcnt(0)\n" \
"s_barrier\n" \
"v_mfma_f32_16x16x32_bf16 v[52:55], a[176:179], a[128:131], v[52:55]\n" \
"s_add_u32 m0, s30, 0x0\n" \
"s_movk_i32 s84, 0x200\n" \
"v_mfma_f32_16x16x32_bf16 v[52:55], a[240:243], a[192:195], v[52:55]\n" \
"buffer_load_dwordx4 v22, s[64:67], s84 offen offset:0 lds\n" \
"v_mfma_f32_16x16x32_bf16 v[56:59], a[180:183], a[128:131], v[56:59]\n" \
"ds_read_b64_tr_b16 a[160:161], v32 offset:32768\n" \
"v_lshlrev_b32_e64 v218, v218, s42\n" \
"v_mfma_f32_16x16x32_bf16 v[56:59], a[244:247], a[192:195], v[56:59]\n" \
"s_movk_i32 s84, 0x1e00\n" \
"v_mfma_f32_16x16x32_bf16 v[60:63], a[184:187], a[128:131], v[60:63]\n" \
"buffer_load_dwordx4 v23, s[64:67], s84 offen offset:1024 lds\n" \
"v_mfma_f32_16x16x32_bf16 v[60:63], a[248:251], a[192:195], v[60:63]\n" \
"ds_read_b64_tr_b16 a[162:163], v32 offset:33792\n" \
"v_mov_b32_e64 v219, 0\n" \
"v_mfma_f32_16x16x32_bf16 v[64:67], a[188:191], a[128:131], v[64:67]\n" \
"ds_read_b64_tr_b16 a[224:225], v32 offset:40960\n" \
"v_lshrrev_b32_e64 v215, 6, v20\n" \
"v_mfma_f32_16x16x32_bf16 v[64:67], a[252:255], a[192:195], v[64:67]\n" \
"ds_read_b128 a[128:131], v28 offset:49152\n" \
"v_cndmask_b32_e64 v41, v221, v220, vcc\n" \
"v_mfma_f32_16x16x32_bf16 v[80:83], a[188:191], a[132:135], v[80:83]\n" \
"ds_read_b128 a[192:195], v29 offset:49152\n" \
"v_cndmask_b32_e64 v43, v220, v221, vcc\n" \
"v_mfma_f32_16x16x32_bf16 v[80:83], a[252:255], a[196:199], v[80:83]\n" \
"s_movk_i32 s84, 0x3a00\n" \
"v_mfma_f32_16x16x32_bf16 v[76:79], a[184:187], a[132:135], v[76:79]\n" \
"buffer_load_dwordx4 v22, s[64:67], s84 offen offset:2048 lds\n" \
"v_mfma_f32_16x16x32_bf16 v[76:79], a[248:251], a[196:199], v[76:79]\n" \
"s_movk_i32 s84, 0x5600\n" \
"v_mfma_f32_16x16x32_bf16 v[72:75], a[180:183], a[132:135], v[72:75]\n" \
"buffer_load_dwordx4 v23, s[64:67], s84 offen offset:3072 lds\n" \
"v_mfma_f32_16x16x32_bf16 v[72:75], a[244:247], a[196:199], v[72:75]\n" \
"s_add_u32 m0, s31, 0x0\n" \
"s_mov_b32 s84, 0x200000\n" \
"v_mfma_f32_16x16x32_bf16 v[68:71], a[176:179], a[132:135], v[68:71]\n" \
"buffer_load_dwordx4 v26, s[68:71], s84 offen offset:0 lds\n" \
"v_mfma_f32_16x16x32_bf16 v[68:71], a[240:243], a[196:199], v[68:71]\n" \
"ds_read_b128 a[132:135], v28 offset:51200\n" \
"v_cmp_eq_u32_e32 vcc, s41, v217\n" \
"v_mfma_f32_16x16x32_bf16 v[84:87], a[176:179], a[136:139], v[84:87]\n" \
"ds_read_b128 a[196:199], v29 offset:51200\n" \
"v_and_b32_e32 v217, 0x330, v20\n" \
"v_mfma_f32_16x16x32_bf16 v[84:87], a[240:243], a[200:203], v[84:87]\n" \
"s_addk_i32 s84, 0x7c00\n" \
"v_mfma_f32_16x16x32_bf16 v[88:91], a[180:183], a[136:139], v[88:91]\n" \
"buffer_load_dwordx4 v26, s[68:71], s84 offen offset:1024 lds\n" \
"v_mfma_f32_16x16x32_bf16 v[88:91], a[244:247], a[200:203], v[88:91]\n" \
"ds_read_b64_tr_b16 a[226:227], v32 offset:41984\n" \
"v_bfe_u32 v216, v20, 6, 2\n" \
"v_mfma_f32_16x16x32_bf16 v[92:95], a[184:187], a[136:139], v[92:95]\n" \
"s_addk_i32 s84, 0x7c00\n" \
"v_mfma_f32_16x16x32_bf16 v[92:95], a[248:251], a[200:203], v[92:95]\n" \
"buffer_load_dwordx4 v27, s[68:71], s84 offen offset:2048 lds\n" \
"v_mfma_f32_16x16x32_bf16 v[96:99], a[188:191], a[136:139], v[96:99]\n" \
"ds_read_b64_tr_b16 a[164:165], v33 offset:32768\n" \
"v_cndmask_b32_e64 v46, v219, v218, vcc\n" \
"v_mfma_f32_16x16x32_bf16 v[96:99], a[252:255], a[200:203], v[96:99]\n" \
"ds_read_b128 a[136:139], v28 offset:53248\n" \
"v_mov_b32_e32 v218, 0x3f80\n" \
"v_mfma_f32_16x16x32_bf16 v[112:115], a[188:191], a[140:143], v[112:115]\n" \
"ds_read_b128 a[200:203], v29 offset:53248\n" \
"v_cmp_eq_u32_e64 vcc, 32, v217\n" \
"v_mfma_f32_16x16x32_bf16 v[112:115], a[252:255], a[204:207], v[112:115]\n" \
"s_addk_i32 s84, 0x7c00\n" \
"v_mfma_f32_16x16x32_bf16 v[108:111], a[184:187], a[140:143], v[108:111]\n" \
"buffer_load_dwordx4 v27, s[68:71], s84 offen offset:3072 lds\n" \
"v_mfma_f32_16x16x32_bf16 v[108:111], a[248:251], a[204:207], v[108:111]\n" \
"s_add_u32 m0, s30, 0x1000\n" \
"s_mov_b32 s84, 0x8200\n" \
"v_mfma_f32_16x16x32_bf16 v[104:107], a[180:183], a[140:143], v[104:107]\n" \
"buffer_load_dwordx4 v22, s[64:67], s84 offen offset:0 lds\n" \
"v_mfma_f32_16x16x32_bf16 v[104:107], a[244:247], a[204:207], v[104:107]\n" \
"ds_read_b64_tr_b16 a[166:167], v33 offset:33792\n" \
"s_add_i32 s41, s40, -16\n" \
"v_mfma_f32_16x16x32_bf16 v[100:103], a[176:179], a[140:143], v[100:103]\n" \
"ds_read_b64_tr_b16 a[228:229], v33 offset:40960\n" \
"v_lshrrev_b32_e64 v219, 1, v20\n" \
"v_mfma_f32_16x16x32_bf16 v[100:103], a[240:243], a[204:207], v[100:103]\n" \
"ds_read_b128 a[140:143], v28 offset:55296\n" \
"v_bfe_u32 v215, v215, 1, 1\n" \
"v_mfma_f32_16x16x32_bf16 v[116:119], a[176:179], a[144:147], v[116:119]\n" \
"ds_read_b128 a[204:207], v29 offset:55296\n" \
"v_cndmask_b32_e64 v47, 0, v218, vcc\n" \
"v_mfma_f32_16x16x32_bf16 v[116:119], a[240:243], a[208:211], v[116:119]\n" \
"s_addk_i32 s84, 0x1c00\n" \
"v_mfma_f32_16x16x32_bf16 v[120:123], a[180:183], a[144:147], v[120:123]\n" \
"buffer_load_dwordx4 v23, s[64:67], s84 offen offset:1024 lds\n" \
"v_mfma_f32_16x16x32_bf16 v[120:123], a[244:247], a[208:211], v[120:123]\n" \
"s_addk_i32 s84, 0x1c00\n" \
"v_mfma_f32_16x16x32_bf16 v[124:127], a[184:187], a[144:147], v[124:127]\n" \
"buffer_load_dwordx4 v22, s[64:67], s84 offen offset:2048 lds\n" \
"v_mfma_f32_16x16x32_bf16 v[124:127], a[248:251], a[208:211], v[124:127]\n" \
"s_addk_i32 s84, 0x1c00\n" \
"v_mfma_f32_16x16x32_bf16 v[128:131], a[188:191], a[144:147], v[128:131]\n" \
"buffer_load_dwordx4 v23, s[64:67], s84 offen offset:3072 lds\n" \
"v_mfma_f32_16x16x32_bf16 v[128:131], a[252:255], a[208:211], v[128:131]\n" \
"ds_read_b128 a[144:147], v28 offset:32768\n" \
"s_mov_b32 s42, 0\n" \
"v_mfma_f32_16x16x32_bf16 v[144:147], a[188:191], a[148:151], v[144:147]\n" \
"ds_read_b128 a[208:211], v29 offset:32768\n" \
"v_mfma_f32_16x16x32_bf16 v[144:147], a[252:255], a[212:215], v[144:147]\n" \
"s_add_u32 m0, s31, 0x4000\n" \
"s_mov_b32 s84, 0x200000\n" \
"v_mfma_f32_16x16x32_bf16 v[140:143], a[184:187], a[148:151], v[140:143]\n" \
"buffer_load_dwordx4 v26, s[72:75], s84 offen offset:0 lds\n" \
"v_mfma_f32_16x16x32_bf16 v[140:143], a[248:251], a[212:215], v[140:143]\n" \
"ds_read_b64_tr_b16 a[230:231], v33 offset:41984\n" \
"v_mfma_f32_16x16x32_bf16 v[136:139], a[180:183], a[148:151], v[136:139]\n" \
"s_addk_i32 s84, 0x7c00\n" \
"s_nop 0\n" \
"v_mfma_f32_16x16x32_bf16 v[136:139], a[244:247], a[212:215], v[136:139]\n" \
"buffer_load_dwordx4 v26, s[72:75], s84 offen offset:1024 lds\n" \
"v_mfma_f32_16x16x32_bf16 v[132:135], a[176:179], a[148:151], v[132:135]\n" \
"ds_read_b64_tr_b16 a[168:169], v34 offset:32768\n" \
"v_mfma_f32_16x16x32_bf16 v[132:135], a[240:243], a[212:215], v[132:135]\n" \
"ds_read_b128 a[148:151], v28 offset:34816\n" \
"v_mfma_f32_16x16x32_bf16 v[148:151], a[176:179], a[152:155], v[148:151]\n" \
"ds_read_b128 a[212:215], v29 offset:34816\n" \
"v_mfma_f32_16x16x32_bf16 v[148:151], a[240:243], a[216:219], v[148:151]\n" \
"ds_read_b64_tr_b16 a[170:171], v34 offset:33792\n" \
"v_mfma_f32_16x16x32_bf16 v[152:155], a[180:183], a[152:155], v[152:155]\n" \
"ds_read_b64_tr_b16 a[232:233], v34 offset:40960\n" \
"v_mfma_f32_16x16x32_bf16 v[152:155], a[244:247], a[216:219], v[152:155]\n" \
"ds_read_b64_tr_b16 a[234:235], v34 offset:41984\n" \
"v_mfma_f32_16x16x32_bf16 v[156:159], a[184:187], a[152:155], v[156:159]\n" \
"ds_read_b64_tr_b16 a[172:173], v35 offset:32768\n" \
"v_mfma_f32_16x16x32_bf16 v[156:159], a[248:251], a[216:219], v[156:159]\n" \
"ds_read_b64_tr_b16 a[174:175], v35 offset:33792\n" \
"v_mfma_f32_16x16x32_bf16 v[160:163], a[188:191], a[152:155], v[160:163]\n" \
"ds_read_b64_tr_b16 a[236:237], v35 offset:40960\n" \
"v_mfma_f32_16x16x32_bf16 v[160:163], a[252:255], a[216:219], v[160:163]\n" \
"ds_read_b128 a[152:155], v28 offset:36864\n" \
"v_mfma_f32_16x16x32_bf16 v[176:179], a[188:191], a[156:159], v[176:179]\n" \
"ds_read_b128 a[216:219], v29 offset:36864\n" \
"v_mfma_f32_16x16x32_bf16 v[176:179], a[252:255], a[220:223], v[176:179]\n" \
"ds_read_b64_tr_b16 a[238:239], v35 offset:41984\n" \
"v_mfma_f32_16x16x32_bf16 v[172:175], a[184:187], a[156:159], v[172:175]\n" \
"s_cmp_lg_u32 s40, 0\n" \
"s_cselect_b32 s41, s41, s42\n" \
"v_mfma_f32_16x16x32_bf16 v[172:175], a[248:251], a[220:223], v[172:175]\n" \
"v_lshl_or_b32 v217, s5, 3, v212\n" \
"v_lshlrev_b32_e64 v218, 10, v213\n" \
"v_mfma_f32_16x16x32_bf16 v[168:171], a[180:183], a[156:159], v[168:171]\n" \
"v_lshlrev_b32_e32 v216, 7, v216\n" \
"v_and_b32_e32 v219, 24, v219\n" \
"v_mfma_f32_16x16x32_bf16 v[168:171], a[244:247], a[220:223], v[168:171]\n" \
"v_and_or_b32 v212, v212, 2, v215\n" \
"v_add_u32_e64 v217, s41, v217\n" \
"v_mfma_f32_16x16x32_bf16 v[164:167], a[176:179], a[156:159], v[164:167]\n" \
"v_or3_b32 v216, v216, v218, v219\n" \
"v_lshlrev_b32_e64 v212, 5, v212\n" \
"v_mfma_f32_16x16x32_bf16 v[164:167], a[240:243], a[220:223], v[164:167]\n" \
"ds_read_b128 a[156:159], v28 offset:38912\n" \
"ds_read_b128 a[220:223], v29 offset:38912\n" \
"s_waitcnt lgkmcnt(15)\n" \
"v_mfma_f32_16x16x32_bf16 a[0:3], a[160:163], a[128:131], a[0:3]\n" \
"s_addk_i32 s84, 0x7c00\n" \
"v_mfma_f32_16x16x32_bf16 a[0:3], a[224:227], a[192:195], a[0:3]\n" \
"buffer_load_dwordx4 v27, s[72:75], s84 offen offset:2048 lds\n" \
"v_mfma_f32_16x16x32_bf16 a[4:7], a[164:167], a[128:131], a[4:7]\n" \
"ds_read_b64_tr_b16 a[176:177], v36 offset:32768\n" \
"s_mov_b32 s41, 0x14000\n" \
"s_waitcnt lgkmcnt(15)\n" \
"v_mfma_f32_16x16x32_bf16 a[4:7], a[228:231], a[192:195], a[4:7]\n" \
"s_addk_i32 s84, 0x7c00\n" \
"s_waitcnt lgkmcnt(11)\n" \
"v_mfma_f32_16x16x32_bf16 a[8:11], a[168:171], a[128:131], a[8:11]\n" \
"buffer_load_dwordx4 v27, s[72:75], s84 offen offset:3072 lds\n" \
"s_waitcnt lgkmcnt(9)\n" \
"v_mfma_f32_16x16x32_bf16 a[8:11], a[232:235], a[192:195], a[8:11]\n" \
"ds_read_b64_tr_b16 a[178:179], v36 offset:33792\n" \
"v_or3_b32 v48, v216, v212, s41\n" \
"s_waitcnt lgkmcnt(8)\n" \
"v_mfma_f32_16x16x32_bf16 a[12:15], a[172:175], a[128:131], a[12:15]\n" \
"ds_read_b64_tr_b16 a[240:241], v36 offset:40960\n" \
"s_lshl_b32 s41, s5, 2\n" \
"s_waitcnt lgkmcnt(5)\n" \
"v_mfma_f32_16x16x32_bf16 a[12:15], a[236:239], a[192:195], a[12:15]\n" \
"s_lshl_b32 s7, s7, 3\n" \
"v_or_b32_e64 v212, s41, v213\n" \
"v_mfma_f32_16x16x32_bf16 a[28:31], a[172:175], a[132:135], a[28:31]\n" \
"s_or_b32 s7, s7, s41\n" \
"s_mov_b32 s41, 0xff800000\n" \
"v_mfma_f32_16x16x32_bf16 a[28:31], a[236:239], a[196:199], a[28:31]\n" \
"ds_read_b64_tr_b16 a[242:243], v36 offset:41984\n" \
"v_mfma_f32_16x16x32_bf16 a[24:27], a[168:171], a[132:135], a[24:27]\n" \
"s_cmp_lg_u32 s40, 0\n" \
"s_cselect_b32 s47, s42, s41\n" \
"v_mfma_f32_16x16x32_bf16 a[24:27], a[232:235], a[196:199], a[24:27]\n" \
"s_mov_b32 s40, 0xc000\n" \
"s_mov_b32 s41, 0x8000\n" \
"v_mfma_f32_16x16x32_bf16 a[20:23], a[164:167], a[132:135], a[20:23]\n" \
"s_cmp_lg_u32 s4, 0\n" \
"s_cselect_b32 s51, s40, s41\n" \
"v_mfma_f32_16x16x32_bf16 a[20:23], a[228:231], a[196:199], a[20:23]\n" \
"ds_read_b64_tr_b16 a[180:181], v37 offset:32768\n" \
"s_movk_i32 s40, 0x4000\n" \
"v_mfma_f32_16x16x32_bf16 a[16:19], a[160:163], a[132:135], a[16:19]\n" \
"s_cmp_lg_u32 s4, 0\n" \
"s_cselect_b32 s53, s41, s40\n" \
"v_mfma_f32_16x16x32_bf16 a[16:19], a[224:227], a[196:199], a[16:19]\n" \
"s_lshl_b32 s40, s2, 2\n" \
"s_and_b32 s40, s40, 28\n" \
"v_mfma_f32_16x16x32_bf16 a[32:35], a[160:163], a[136:139], a[32:35]\n" \
"s_waitcnt vmcnt(16)\n" \
"v_or_b32_e32 v204, v210, v204\n" \
"v_or_b32_e64 v206, v210, v206\n" \
"v_mfma_f32_16x16x32_bf16 a[32:35], a[224:227], a[200:203], a[32:35]\n" \
"v_mov_b32_e64 v210, 0\n" \
"s_add_u32 s88, s35, 0x1000\n" \
"v_mfma_f32_16x16x32_bf16 a[36:39], a[164:167], a[136:139], a[36:39]\n" \
"ds_read_b64_tr_b16 a[182:183], v37 offset:33792\n" \
"s_bfe_u32 s45, s2, 0x20003\n" \
"v_mfma_f32_16x16x32_bf16 a[36:39], a[228:231], a[200:203], a[36:39]\n" \
"ds_read_b64_tr_b16 a[244:245], v37 offset:40960\n" \
"s_lshr_b32 s44, s3, 1\n" \
"v_mfma_f32_16x16x32_bf16 a[40:43], a[168:171], a[136:139], a[40:43]\n" \
"s_mov_b32 m0, s32\n" \
"s_or_b32 s46, s40, s45\n" \
"v_mfma_f32_16x16x32_bf16 a[40:43], a[232:235], a[200:203], a[40:43]\n" \
"s_mov_b32 exec_lo, 0xffffffff\n" \
"s_mov_b32 exec_hi, 0x0\n" \
"global_load_lds_dword v[188:189], off\n" \
"s_mov_b64 exec, -1\n" \
"v_mfma_f32_16x16x32_bf16 a[44:47], a[172:175], a[136:139], a[44:47]\n" \
"ds_read_b64_tr_b16 a[246:247], v37 offset:41984\n" \
"s_lshl_b32 s43, s44, 15\n" \
"v_mfma_f32_16x16x32_bf16 a[44:47], a[236:239], a[200:203], a[44:47]\n" \
"s_lshl_b32 s41, s5, 14\n" \
"s_lshl_b32 s48, s46, 20\n" \
"v_mfma_f32_16x16x32_bf16 a[60:63], a[172:175], a[140:143], a[60:63]\n" \
"ds_write_b128 v196, v[200:203]\n" \
"v_mfma_f32_16x16x32_bf16 a[60:63], a[236:239], a[204:207], a[60:63]\n" \
"s_or_b32 s41, s43, s41\n" \
"s_add_i32 s49, s48, 0x1000000\n" \
"v_mfma_f32_16x16x32_bf16 a[56:59], a[168:171], a[140:143], a[56:59]\n" \
"ds_read_b64_tr_b16 a[184:185], v38 offset:32768\n" \
"s_or_b32 s42, s49, s41\n" \
"v_mfma_f32_16x16x32_bf16 a[56:59], a[232:235], a[204:207], a[56:59]\n" \
"s_lshl_b32 s4, s4, 16\n" \
"s_or_b32 s41, s42, s4\n" \
"v_mfma_f32_16x16x32_bf16 a[52:55], a[164:167], a[140:143], a[52:55]\n" \
"s_lshl_b32 s4, s6, 16\n" \
"s_or_b32 s42, s42, s4\n" \
"v_mfma_f32_16x16x32_bf16 a[52:55], a[228:231], a[204:207], a[52:55]\n" \
"ds_read_b64_tr_b16 a[186:187], v38 offset:33792\n" \
"s_lshl_b32 s4, s5, 11\n" \
"v_mfma_f32_16x16x32_bf16 a[48:51], a[160:163], a[140:143], a[48:51]\n" \
"s_mul_i32 s5, s7, 0x24800\n" \
"s_lshl_b32 s6, s46, 9\n" \
"v_mfma_f32_16x16x32_bf16 a[48:51], a[224:227], a[204:207], a[48:51]\n" \
"s_lshl_b32 s7, s40, 6\n" \
"s_add_i32 s6, s5, s6\n" \
"v_mfma_f32_16x16x32_bf16 a[64:67], a[160:163], a[144:147], a[64:67]\n" \
"ds_write_b128 v197, v[204:207]\n" \
"v_mfma_f32_16x16x32_bf16 a[64:67], a[224:227], a[208:211], a[64:67]\n" \
"ds_read_b64_tr_b16 a[248:249], v38 offset:40960\n" \
"s_or_b32 s5, s5, s7\n" \
"v_mfma_f32_16x16x32_bf16 a[68:71], a[164:167], a[144:147], a[68:71]\n" \
"s_lshl_b32 s7, s45, 2\n" \
"s_or_b32 s5, s5, s7\n" \
"v_mfma_f32_16x16x32_bf16 a[68:71], a[228:231], a[208:211], a[68:71]\n" \
"ds_read_b64_tr_b16 a[250:251], v38 offset:41984\n" \
"s_lshl_b32 s7, s3, 12\n" \
"v_mfma_f32_16x16x32_bf16 a[72:75], a[168:171], a[144:147], a[72:75]\n" \
"s_or_b32 s50, s49, s7\n" \
"s_lshl_b32 s7, s3, 11\n" \
"v_mfma_f32_16x16x32_bf16 a[72:75], a[232:235], a[208:211], a[72:75]\n" \
"v_lshrrev_b32_e32 v215, 4, v20\n" \
"v_bfe_u32 v216, v20, 4, 3\n" \
"v_mfma_f32_16x16x32_bf16 a[76:79], a[172:175], a[144:147], a[76:79]\n" \
"s_add_i32 s52, s48, s7\n" \
"s_add_i32 s53, s53, s7\n" \
"v_mfma_f32_16x16x32_bf16 a[76:79], a[236:239], a[208:211], a[76:79]\n" \
"ds_read_b64_tr_b16 a[188:189], v39 offset:32768\n" \
"v_mul_u32_u24_e32 v214, 0x480, v214\n" \
"v_mfma_f32_16x16x32_bf16 a[92:95], a[172:175], a[148:151], a[92:95]\n" \
"ds_write_b128 v198, v[208:211]\n" \
"v_mfma_f32_16x16x32_bf16 a[92:95], a[236:239], a[212:215], a[92:95]\n" \
"v_bitop3_b32 v218, v213, v215, 7 bitop3:0x78\n" \
"v_bitop3_b32 v213, v213, v216, 4 bitop3:0x36\n" \
"v_mfma_f32_16x16x32_bf16 a[88:91], a[168:171], a[148:151], a[88:91]\n" \
"ds_read_b64_tr_b16 a[190:191], v39 offset:33792\n" \
"s_lshl_b32 s7, s3, 10\n" \
"v_mfma_f32_16x16x32_bf16 a[88:91], a[232:235], a[212:215], a[88:91]\n" \
"s_or_b32 s4, s43, s4\n" \
"v_lshl_or_b32 v25, v213, 4, v214\n" \
"v_mfma_f32_16x16x32_bf16 a[84:87], a[164:167], a[148:151], a[84:87]\n" \
"v_mul_u32_u24_e32 v213, 0x480, v217\n" \
"v_bitop3_b32 v212, v215, v212, 7 bitop3:0x6c\n" \
"v_mfma_f32_16x16x32_bf16 a[84:87], a[228:231], a[212:215], a[84:87]\n" \
"ds_read_b64_tr_b16 a[252:253], v39 offset:40960\n" \
"s_or_b32 s40, s7, 0x14000\n" \
"v_mfma_f32_16x16x32_bf16 a[80:83], a[160:163], a[148:151], a[80:83]\n" \
"s_lshl_b32 s43, s44, 13\n" \
"s_mulk_i32 s44, 0xe800\n" \
"v_mfma_f32_16x16x32_bf16 a[80:83], a[224:227], a[212:215], a[80:83]\n" \
"s_lshl_b32 s7, s3, 8\n" \
"s_nop 0\n" \
"v_lshl_or_b32 v24, v218, 4, v214\n" \
"v_mfma_f32_16x16x32_bf16 a[96:99], a[160:163], a[152:155], a[96:99]\n" \
"ds_read_b64_tr_b16 a[254:255], v39 offset:41984\n" \
"v_lshl_or_b32 v21, v212, 4, v213\n" \
"v_mfma_f32_16x16x32_bf16 a[96:99], a[224:227], a[216:219], a[96:99]\n" \
"s_add_i32 s44, s44, 0x17000\n" \
"s_or_b32 s45, s7, 0x27000\n" \
"v_mfma_f32_16x16x32_bf16 a[100:103], a[164:167], a[152:155], a[100:103]\n" \
"s_or_b32 s46, s4, 0x10000\n" \
"s_add_i32 s48, s6, 0x3000000\n" \
"v_mfma_f32_16x16x32_bf16 a[100:103], a[228:231], a[216:219], a[100:103]\n" \
"s_add_i32 s49, s5, 0x3004000\n" \
"s_add_i32 s52, s52, 0x1010000\n" \
"v_mfma_f32_16x16x32_bf16 a[104:107], a[168:171], a[152:155], a[104:107]\n" \
"v_mfma_f32_16x16x32_bf16 a[104:107], a[232:235], a[216:219], a[104:107]\n" \
"v_mfma_f32_16x16x32_bf16 a[108:111], a[172:175], a[152:155], a[108:111]\n" \
"v_mfma_f32_16x16x32_bf16 a[108:111], a[236:239], a[216:219], a[108:111]\n" \
"s_waitcnt lgkmcnt(15)\n" \
"v_mfma_f32_16x16x32_bf16 a[124:127], a[172:175], a[156:159], a[124:127]\n" \
"v_mfma_f32_16x16x32_bf16 a[124:127], a[236:239], a[220:223], a[124:127]\n" \
"v_mfma_f32_16x16x32_bf16 a[120:123], a[168:171], a[156:159], a[120:123]\n" \
"v_mfma_f32_16x16x32_bf16 a[120:123], a[232:235], a[220:223], a[120:123]\n" \
"v_mfma_f32_16x16x32_bf16 a[116:119], a[164:167], a[156:159], a[116:119]\n" \
"v_mfma_f32_16x16x32_bf16 a[116:119], a[228:231], a[220:223], a[116:119]\n" \
"v_mfma_f32_16x16x32_bf16 a[112:115], a[160:163], a[156:159], a[112:115]\n" \
"v_mfma_f32_16x16x32_bf16 a[112:115], a[224:227], a[220:223], a[112:115]\n" \
"s_waitcnt vmcnt(1) lgkmcnt(0)\n" \
"s_barrier\n" \
"v_mfma_f32_16x16x32_bf16 v[52:55], a[176:179], a[128:131], v[52:55]\n" \
"s_add_u32 m0, s30, 0x8000\n" \
"s_movk_i32 s84, 0x280\n" \
"v_mfma_f32_16x16x32_bf16 v[52:55], a[240:243], a[192:195], v[52:55]\n" \
"buffer_load_dwordx4 v22, s[64:67], s84 offen offset:0 lds\n" \
"v_mfma_f32_16x16x32_bf16 v[56:59], a[180:183], a[128:131], v[56:59]\n" \
"s_movk_i32 s84, 0x1e80\n" \
"v_mfma_f32_16x16x32_bf16 v[56:59], a[244:247], a[192:195], v[56:59]\n" \
"buffer_load_dwordx4 v23, s[64:67], s84 offen offset:1024 lds\n" \
"v_mfma_f32_16x16x32_bf16 v[60:63], a[184:187], a[128:131], v[60:63]\n" \
"ds_read_b64_tr_b16 a[160:161], v32 offset:0\n" \
"v_mfma_f32_16x16x32_bf16 v[60:63], a[248:251], a[192:195], v[60:63]\n" \
"s_movk_i32 s84, 0x3a80\n" \
"v_mfma_f32_16x16x32_bf16 v[64:67], a[188:191], a[128:131], v[64:67]\n" \
"buffer_load_dwordx4 v22, s[64:67], s84 offen offset:2048 lds\n" \
"v_mfma_f32_16x16x32_bf16 v[64:67], a[252:255], a[192:195], v[64:67]\n" \
"ds_read_b128 a[128:131], v28 offset:16384\n" \
"v_mfma_f32_16x16x32_bf16 v[80:83], a[188:191], a[132:135], v[80:83]\n" \
"ds_read_b128 a[192:195], v29 offset:16384\n" \
"v_mfma_f32_16x16x32_bf16 v[80:83], a[252:255], a[196:199], v[80:83]\n" \
"s_movk_i32 s84, 0x5680\n" \
"s_nop 0\n" \
"v_mfma_f32_16x16x32_bf16 v[76:79], a[184:187], a[132:135], v[76:79]\n" \
"buffer_load_dwordx4 v23, s[64:67], s84 offen offset:3072 lds\n" \
"v_mfma_f32_16x16x32_bf16 v[76:79], a[248:251], a[196:199], v[76:79]\n" \
"s_add_u32 m0, s31, 0x8000\n" \
"s_mov_b32 s84, 0x280000\n" \
"v_mfma_f32_16x16x32_bf16 v[72:75], a[180:183], a[132:135], v[72:75]\n" \
"buffer_load_dwordx4 v26, s[68:71], s84 offen offset:0 lds\n" \
"v_mfma_f32_16x16x32_bf16 v[72:75], a[244:247], a[196:199], v[72:75]\n" \
"ds_read_b64_tr_b16 a[162:163], v32 offset:1024\n" \
"v_mfma_f32_16x16x32_bf16 v[68:71], a[176:179], a[132:135], v[68:71]\n" \
"ds_read_b64_tr_b16 a[224:225], v32 offset:8192\n" \
"v_mfma_f32_16x16x32_bf16 v[68:71], a[240:243], a[196:199], v[68:71]\n" \
"ds_read_b128 a[132:135], v28 offset:18432\n" \
"v_mfma_f32_16x16x32_bf16 v[84:87], a[176:179], a[136:139], v[84:87]\n" \
"ds_read_b128 a[196:199], v29 offset:18432\n" \
"v_mfma_f32_16x16x32_bf16 v[84:87], a[240:243], a[200:203], v[84:87]\n" \
"s_addk_i32 s84, 0x7c00\n" \
"v_mfma_f32_16x16x32_bf16 v[88:91], a[180:183], a[136:139], v[88:91]\n" \
"buffer_load_dwordx4 v26, s[68:71], s84 offen offset:1024 lds\n" \
"v_mfma_f32_16x16x32_bf16 v[88:91], a[244:247], a[200:203], v[88:91]\n" \
"s_addk_i32 s84, 0x7c00\n" \
"v_mfma_f32_16x16x32_bf16 v[92:95], a[184:187], a[136:139], v[92:95]\n" \
"buffer_load_dwordx4 v27, s[68:71], s84 offen offset:2048 lds\n" \
"v_mfma_f32_16x16x32_bf16 v[92:95], a[248:251], a[200:203], v[92:95]\n" \
"s_addk_i32 s84, 0x7c00\n" \
"s_nop 0\n" \
"v_mfma_f32_16x16x32_bf16 v[96:99], a[188:191], a[136:139], v[96:99]\n" \
"buffer_load_dwordx4 v27, s[68:71], s84 offen offset:3072 lds\n" \
"v_mfma_f32_16x16x32_bf16 v[96:99], a[252:255], a[200:203], v[96:99]\n" \
"ds_read_b128 a[136:139], v28 offset:20480\n" \
"v_mfma_f32_16x16x32_bf16 v[112:115], a[188:191], a[140:143], v[112:115]\n" \
"ds_read_b128 a[200:203], v29 offset:20480\n" \
"v_mfma_f32_16x16x32_bf16 v[112:115], a[252:255], a[204:207], v[112:115]\n" \
"s_add_u32 m0, s30, 0x9000\n" \
"s_mov_b32 s84, 0x8280\n" \
"v_mfma_f32_16x16x32_bf16 v[108:111], a[184:187], a[140:143], v[108:111]\n" \
"buffer_load_dwordx4 v22, s[64:67], s84 offen offset:0 lds\n" \
"v_mfma_f32_16x16x32_bf16 v[108:111], a[248:251], a[204:207], v[108:111]\n" \
"s_addk_i32 s84, 0x1c00\n" \
"v_mfma_f32_16x16x32_bf16 v[104:107], a[180:183], a[140:143], v[104:107]\n" \
"buffer_load_dwordx4 v23, s[64:67], s84 offen offset:1024 lds\n" \
"v_mfma_f32_16x16x32_bf16 v[104:107], a[244:247], a[204:207], v[104:107]\n" \
"s_addk_i32 s84, 0x1c00\n" \
"v_mfma_f32_16x16x32_bf16 v[100:103], a[176:179], a[140:143], v[100:103]\n" \
"buffer_load_dwordx4 v22, s[64:67], s84 offen offset:2048 lds\n" \
"v_mfma_f32_16x16x32_bf16 v[100:103], a[240:243], a[204:207], v[100:103]\n" \
"ds_read_b128 a[140:143], v28 offset:22528\n" \
"v_mfma_f32_16x16x32_bf16 v[116:119], a[176:179], a[144:147], v[116:119]\n" \
"ds_read_b128 a[204:207], v29 offset:22528\n" \
"v_mfma_f32_16x16x32_bf16 v[116:119], a[240:243], a[208:211], v[116:119]\n" \
"s_addk_i32 s84, 0x1c00\n" \
"v_mfma_f32_16x16x32_bf16 v[120:123], a[180:183], a[144:147], v[120:123]\n" \
"buffer_load_dwordx4 v23, s[64:67], s84 offen offset:3072 lds\n" \
"v_mfma_f32_16x16x32_bf16 v[120:123], a[244:247], a[208:211], v[120:123]\n" \
"s_add_u32 m0, s31, 0xc000\n" \
"s_mov_b32 s84, 0x280000\n" \
"v_mfma_f32_16x16x32_bf16 v[124:127], a[184:187], a[144:147], v[124:127]\n" \
"buffer_load_dwordx4 v26, s[72:75], s84 offen offset:0 lds\n" \
"v_mfma_f32_16x16x32_bf16 v[124:127], a[248:251], a[208:211], v[124:127]\n" \
"s_addk_i32 s84, 0x7c00\n" \
"v_mfma_f32_16x16x32_bf16 v[128:131], a[188:191], a[144:147], v[128:131]\n" \
"buffer_load_dwordx4 v26, s[72:75], s84 offen offset:1024 lds\n" \
"v_mfma_f32_16x16x32_bf16 v[128:131], a[252:255], a[208:211], v[128:131]\n" \
"ds_read_b128 a[144:147], v28 offset:0\n" \
"v_mfma_f32_16x16x32_bf16 v[144:147], a[188:191], a[148:151], v[144:147]\n" \
"ds_read_b128 a[208:211], v29 offset:0\n" \
"v_mfma_f32_16x16x32_bf16 v[144:147], a[252:255], a[212:215], v[144:147]\n" \
"ds_read_b128 v[200:203], v199 offset:0\n" \
"v_mfma_f32_16x16x32_bf16 v[140:143], a[184:187], a[148:151], v[140:143]\n" \
"ds_read_b128 v[204:207], v199 offset:4096\n" \
"v_mfma_f32_16x16x32_bf16 v[140:143], a[248:251], a[212:215], v[140:143]\n" \
"ds_read_b64_tr_b16 a[226:227], v32 offset:9216\n" \
"v_mfma_f32_16x16x32_bf16 v[136:139], a[180:183], a[148:151], v[136:139]\n" \
"ds_read_b64_tr_b16 a[164:165], v33 offset:0\n" \
"v_mfma_f32_16x16x32_bf16 v[136:139], a[244:247], a[212:215], v[136:139]\n" \
"ds_read_b128 v[208:211], v199 offset:8192\n" \
"v_mfma_f32_16x16x32_bf16 v[132:135], a[176:179], a[148:151], v[132:135]\n" \
"ds_read_b64_tr_b16 a[166:167], v33 offset:1024\n" \
"v_mfma_f32_16x16x32_bf16 v[132:135], a[240:243], a[212:215], v[132:135]\n" \
"ds_read_b128 a[148:151], v28 offset:2048\n" \
"v_mfma_f32_16x16x32_bf16 v[148:151], a[176:179], a[152:155], v[148:151]\n" \
"ds_read_b128 a[212:215], v29 offset:2048\n" \
"v_mfma_f32_16x16x32_bf16 v[148:151], a[240:243], a[216:219], v[148:151]\n" \
"ds_read_b64_tr_b16 a[228:229], v33 offset:8192\n" \
"v_mfma_f32_16x16x32_bf16 v[152:155], a[180:183], a[152:155], v[152:155]\n" \
"ds_read_b64_tr_b16 a[230:231], v33 offset:9216\n" \
"v_mfma_f32_16x16x32_bf16 v[152:155], a[244:247], a[216:219], v[152:155]\n" \
"ds_read_b64_tr_b16 a[168:169], v34 offset:0\n" \
"v_mfma_f32_16x16x32_bf16 v[156:159], a[184:187], a[152:155], v[156:159]\n" \
"ds_read_b64_tr_b16 a[170:171], v34 offset:1024\n" \
"v_mfma_f32_16x16x32_bf16 v[156:159], a[248:251], a[216:219], v[156:159]\n" \
"ds_read_b64_tr_b16 a[232:233], v34 offset:8192\n" \
"v_mfma_f32_16x16x32_bf16 v[160:163], a[188:191], a[152:155], v[160:163]\n" \
"ds_read_b64_tr_b16 a[234:235], v34 offset:9216\n" \
"v_mfma_f32_16x16x32_bf16 v[160:163], a[252:255], a[216:219], v[160:163]\n" \
"ds_read_b128 a[152:155], v28 offset:4096\n" \
"v_mfma_f32_16x16x32_bf16 v[176:179], a[188:191], a[156:159], v[176:179]\n" \
"ds_read_b128 a[216:219], v29 offset:4096\n" \
"v_mfma_f32_16x16x32_bf16 v[176:179], a[252:255], a[220:223], v[176:179]\n" \
"ds_read_b64_tr_b16 a[172:173], v35 offset:0\n" \
"v_mfma_f32_16x16x32_bf16 v[172:175], a[184:187], a[156:159], v[172:175]\n" \
"ds_read_b64_tr_b16 a[174:175], v35 offset:1024\n" \
"v_mfma_f32_16x16x32_bf16 v[172:175], a[248:251], a[220:223], v[172:175]\n" \
"ds_read_b64_tr_b16 a[236:237], v35 offset:8192\n" \
"v_mfma_f32_16x16x32_bf16 v[168:171], a[180:183], a[156:159], v[168:171]\n" \
"ds_read_b64_tr_b16 a[238:239], v35 offset:9216\n" \
"v_mfma_f32_16x16x32_bf16 v[168:171], a[244:247], a[220:223], v[168:171]\n" \
"v_mfma_f32_16x16x32_bf16 v[164:167], a[176:179], a[156:159], v[164:167]\n" \
"v_mfma_f32_16x16x32_bf16 v[164:167], a[240:243], a[220:223], v[164:167]\n" \
"ds_read_b128 a[156:159], v28 offset:6144\n" \
"ds_read_b128 a[220:223], v29 offset:6144\n" \
"s_waitcnt lgkmcnt(15)\n" \
"v_mfma_f32_16x16x32_bf16 a[0:3], a[160:163], a[128:131], a[0:3]\n" \
"s_addk_i32 s84, 0x7c00\n" \
"v_mfma_f32_16x16x32_bf16 a[0:3], a[224:227], a[192:195], a[0:3]\n" \
"buffer_load_dwordx4 v27, s[72:75], s84 offen offset:2048 lds\n" \
"v_mfma_f32_16x16x32_bf16 a[4:7], a[164:167], a[128:131], a[4:7]\n" \
"ds_read_b64_tr_b16 a[176:177], v36 offset:0\n" \
"s_waitcnt lgkmcnt(13)\n" \
"v_mfma_f32_16x16x32_bf16 a[4:7], a[228:231], a[192:195], a[4:7]\n" \
"s_addk_i32 s84, 0x7c00\n" \
"s_waitcnt lgkmcnt(11)\n" \
"v_mfma_f32_16x16x32_bf16 a[8:11], a[168:171], a[128:131], a[8:11]\n" \
"buffer_load_dwordx4 v27, s[72:75], s84 offen offset:3072 lds\n" \
"s_waitcnt lgkmcnt(9)\n" \
"v_mfma_f32_16x16x32_bf16 a[8:11], a[232:235], a[192:195], a[8:11]\n" \
"ds_read_b64_tr_b16 a[178:179], v36 offset:1024\n" \
"s_waitcnt lgkmcnt(6)\n" \
"v_mfma_f32_16x16x32_bf16 a[12:15], a[172:175], a[128:131], a[12:15]\n" \
"ds_read_b64_tr_b16 a[240:241], v36 offset:8192\n" \
"s_waitcnt lgkmcnt(5)\n" \
"v_mfma_f32_16x16x32_bf16 a[12:15], a[236:239], a[192:195], a[12:15]\n" \
"v_mfma_f32_16x16x32_bf16 a[28:31], a[172:175], a[132:135], a[28:31]\n" \
"v_mfma_f32_16x16x32_bf16 a[28:31], a[236:239], a[196:199], a[28:31]\n" \
"ds_read_b64_tr_b16 a[242:243], v36 offset:9216\n" \
"v_mfma_f32_16x16x32_bf16 a[24:27], a[168:171], a[132:135], a[24:27]\n" \
"v_mfma_f32_16x16x32_bf16 a[24:27], a[232:235], a[196:199], a[24:27]\n" \
"v_mfma_f32_16x16x32_bf16 a[20:23], a[164:167], a[132:135], a[20:23]\n" \
"v_mfma_f32_16x16x32_bf16 a[20:23], a[228:231], a[196:199], a[20:23]\n" \
"ds_read_b64_tr_b16 a[180:181], v37 offset:0\n" \
"v_mfma_f32_16x16x32_bf16 a[16:19], a[160:163], a[132:135], a[16:19]\n" \
"v_mfma_f32_16x16x32_bf16 a[16:19], a[224:227], a[196:199], a[16:19]\n" \
"v_mfma_f32_16x16x32_bf16 a[32:35], a[160:163], a[136:139], a[32:35]\n" \
"ds_read_b64_tr_b16 a[182:183], v37 offset:1024\n" \
"v_mfma_f32_16x16x32_bf16 a[32:35], a[224:227], a[200:203], a[32:35]\n" \
"v_mfma_f32_16x16x32_bf16 a[36:39], a[164:167], a[136:139], a[36:39]\n" \
"v_mfma_f32_16x16x32_bf16 a[36:39], a[228:231], a[200:203], a[36:39]\n" \
"buffer_store_dwordx4 v[200:203], v20, s[80:83], s35 offen sc1\n" \
"v_mfma_f32_16x16x32_bf16 a[40:43], a[168:171], a[136:139], a[40:43]\n" \
"s_mov_b32 m0, s32\n" \
"ds_read_b64_tr_b16 a[244:245], v37 offset:8192\n" \
"v_mfma_f32_16x16x32_bf16 a[40:43], a[232:235], a[200:203], a[40:43]\n" \
"s_mov_b32 exec_lo, 0x0\n" \
"s_mov_b32 exec_hi, 0xffffffff\n" \
"global_load_lds_dword v[188:189], off\n" \
"s_mov_b64 exec, -1\n" \
"v_mfma_f32_16x16x32_bf16 a[44:47], a[172:175], a[136:139], a[44:47]\n" \
"ds_read_b64_tr_b16 a[246:247], v37 offset:9216\n" \
"v_mfma_f32_16x16x32_bf16 a[44:47], a[236:239], a[200:203], a[44:47]\n" \
"v_mfma_f32_16x16x32_bf16 a[60:63], a[172:175], a[140:143], a[60:63]\n" \
"v_mfma_f32_16x16x32_bf16 a[60:63], a[236:239], a[204:207], a[60:63]\n" \
"v_mfma_f32_16x16x32_bf16 a[56:59], a[168:171], a[140:143], a[56:59]\n" \
"ds_read_b64_tr_b16 a[184:185], v38 offset:0\n" \
"v_mfma_f32_16x16x32_bf16 a[56:59], a[232:235], a[204:207], a[56:59]\n" \
"v_mfma_f32_16x16x32_bf16 a[52:55], a[164:167], a[140:143], a[52:55]\n" \
"buffer_store_dwordx4 v[204:207], v20, s[80:83], s88 offen sc1\n" \
"v_mfma_f32_16x16x32_bf16 a[52:55], a[228:231], a[204:207], a[52:55]\n" \
"ds_read_b64_tr_b16 a[186:187], v38 offset:1024\n" \
"v_mfma_f32_16x16x32_bf16 a[48:51], a[160:163], a[140:143], a[48:51]\n" \
"v_mfma_f32_16x16x32_bf16 a[48:51], a[224:227], a[204:207], a[48:51]\n" \
"v_mfma_f32_16x16x32_bf16 a[64:67], a[160:163], a[144:147], a[64:67]\n" \
"ds_read_b64_tr_b16 a[248:249], v38 offset:8192\n" \
"v_mfma_f32_16x16x32_bf16 a[64:67], a[224:227], a[208:211], a[64:67]\n" \
"v_mfma_f32_16x16x32_bf16 a[68:71], a[164:167], a[144:147], a[68:71]\n" \
"v_mfma_f32_16x16x32_bf16 a[68:71], a[228:231], a[208:211], a[68:71]\n" \
"ds_read_b64_tr_b16 a[250:251], v38 offset:9216\n" \
"v_mfma_f32_16x16x32_bf16 a[72:75], a[168:171], a[144:147], a[72:75]\n" \
"v_mfma_f32_16x16x32_bf16 a[72:75], a[232:235], a[208:211], a[72:75]\n" \
"v_mfma_f32_16x16x32_bf16 a[76:79], a[172:175], a[144:147], a[76:79]\n" \
"buffer_store_dwordx4 v[208:211], v20, s[80:83], s36 offen sc1\n" \
"v_mfma_f32_16x16x32_bf16 a[76:79], a[236:239], a[208:211], a[76:79]\n" \
"ds_read_b64_tr_b16 a[188:189], v39 offset:0\n" \
"v_mfma_f32_16x16x32_bf16 a[92:95], a[172:175], a[148:151], a[92:95]\n" \
"v_mfma_f32_16x16x32_bf16 a[92:95], a[236:239], a[212:215], a[92:95]\n" \
"v_mfma_f32_16x16x32_bf16 a[88:91], a[168:171], a[148:151], a[88:91]\n" \
"ds_read_b64_tr_b16 a[190:191], v39 offset:1024\n" \
"v_mfma_f32_16x16x32_bf16 a[88:91], a[232:235], a[212:215], a[88:91]\n" \
"v_mfma_f32_16x16x32_bf16 a[84:87], a[164:167], a[148:151], a[84:87]\n" \
"v_mfma_f32_16x16x32_bf16 a[84:87], a[228:231], a[212:215], a[84:87]\n" \
"ds_read_b64_tr_b16 a[252:253], v39 offset:8192\n" \
"v_mfma_f32_16x16x32_bf16 a[80:83], a[160:163], a[148:151], a[80:83]\n" \
"v_mfma_f32_16x16x32_bf16 a[80:83], a[224:227], a[212:215], a[80:83]\n" \
"v_mfma_f32_16x16x32_bf16 a[96:99], a[160:163], a[152:155], a[96:99]\n" \
"ds_read_b64_tr_b16 a[254:255], v39 offset:9216\n" \
"v_mfma_f32_16x16x32_bf16 a[96:99], a[224:227], a[216:219], a[96:99]\n" \
"v_mfma_f32_16x16x32_bf16 a[100:103], a[164:167], a[152:155], a[100:103]\n" \
".fill (L_pb + 12000 - .) / 4, 4, 0xBF800000\n" \
"L_pp3:\n" \
"s_cbranch_execz L_pret\n" \
"v_mfma_f32_16x16x32_bf16 a[100:103], a[228:231], a[216:219], a[100:103]\n" \
"v_mfma_f32_16x16x32_bf16 a[104:107], a[168:171], a[152:155], a[104:107]\n" \
"v_mfma_f32_16x16x32_bf16 a[104:107], a[232:235], a[216:219], a[104:107]\n" \
"v_mfma_f32_16x16x32_bf16 a[108:111], a[172:175], a[152:155], a[108:111]\n" \
"v_mfma_f32_16x16x32_bf16 a[108:111], a[236:239], a[216:219], a[108:111]\n" \
"s_waitcnt lgkmcnt(15)\n" \
"v_mfma_f32_16x16x32_bf16 a[124:127], a[172:175], a[156:159], a[124:127]\n" \
"v_mfma_f32_16x16x32_bf16 a[124:127], a[236:239], a[220:223], a[124:127]\n" \
"v_mfma_f32_16x16x32_bf16 a[120:123], a[168:171], a[156:159], a[120:123]\n" \
"v_mfma_f32_16x16x32_bf16 a[120:123], a[232:235], a[220:223], a[120:123]\n" \
"v_mfma_f32_16x16x32_bf16 a[116:119], a[164:167], a[156:159], a[116:119]\n" \
"v_mfma_f32_16x16x32_bf16 a[116:119], a[228:231], a[220:223], a[116:119]\n" \
"v_mfma_f32_16x16x32_bf16 a[112:115], a[160:163], a[156:159], a[112:115]\n" \
"v_mfma_f32_16x16x32_bf16 a[112:115], a[224:227], a[220:223], a[112:115]\n" \
"s_waitcnt vmcnt(4) lgkmcnt(0)\n" \
"s_barrier\n" \
"v_mfma_f32_16x16x32_bf16 v[52:55], a[176:179], a[128:131], v[52:55]\n" \
"s_add_u32 m0, s30, 0x0\n" \
"s_movk_i32 s84, 0x300\n" \
"v_mfma_f32_16x16x32_bf16 v[52:55], a[240:243], a[192:195], v[52:55]\n" \
"buffer_load_dwordx4 v22, s[64:67], s84 offen offset:0 lds\n" \
"v_mfma_f32_16x16x32_bf16 v[56:59], a[180:183], a[128:131], v[56:59]\n" \
"ds_read_b64_tr_b16 a[160:161], v32 offset:32768\n" \
"v_mfma_f32_16x16x32_bf16 v[56:59], a[244:247], a[192:195], v[56:59]\n" \
"s_movk_i32 s84, 0x1f00\n" \
"s_nop 0\n" \
"v_mfma_f32_16x16x32_bf16 v[60:63], a[184:187], a[128:131], v[60:63]\n" \
"buffer_load_dwordx4 v23, s[64:67], s84 offen offset:1024 lds\n" \
"v_mfma_f32_16x16x32_bf16 v[60:63], a[248:251], a[192:195], v[60:63]\n" \
"ds_read_b64_tr_b16 a[162:163], v32 offset:33792\n" \
"v_mfma_f32_16x16x32_bf16 v[64:67], a[188:191], a[128:131], v[64:67]\n" \
"ds_read_b64_tr_b16 a[224:225], v32 offset:40960\n" \
"v_mfma_f32_16x16x32_bf16 v[64:67], a[252:255], a[192:195], v[64:67]\n" \
"ds_read_b128 a[128:131], v28 offset:49152\n" \
"v_mfma_f32_16x16x32_bf16 v[80:83], a[188:191], a[132:135], v[80:83]\n" \
"ds_read_b128 a[192:195], v29 offset:49152\n" \
"v_mfma_f32_16x16x32_bf16 v[80:83], a[252:255], a[196:199], v[80:83]\n" \
"s_movk_i32 s84, 0x3b00\n" \
"v_mfma_f32_16x16x32_bf16 v[76:79], a[184:187], a[132:135], v[76:79]\n" \
"buffer_load_dwordx4 v22, s[64:67], s84 offen offset:2048 lds\n" \
"v_mfma_f32_16x16x32_bf16 v[76:79], a[248:251], a[196:199], v[76:79]\n" \
"s_movk_i32 s84, 0x5700\n" \
"v_mfma_f32_16x16x32_bf16 v[72:75], a[180:183], a[132:135], v[72:75]\n" \
"buffer_load_dwordx4 v23, s[64:67], s84 offen offset:3072 lds\n" \
"v_mfma_f32_16x16x32_bf16 v[72:75], a[244:247], a[196:199], v[72:75]\n" \
"s_add_u32 m0, s31, 0x0\n" \
"s_mov_b32 s84, 0x300000\n" \
"v_mfma_f32_16x16x32_bf16 v[68:71], a[176:179], a[132:135], v[68:71]\n" \
"buffer_load_dwordx4 v26, s[68:71], s84 offen offset:0 lds\n" \
"v_mfma_f32_16x16x32_bf16 v[68:71], a[240:243], a[196:199], v[68:71]\n" \
"ds_read_b128 a[132:135], v28 offset:51200\n" \
"v_mfma_f32_16x16x32_bf16 v[84:87], a[176:179], a[136:139], v[84:87]\n" \
"ds_read_b128 a[196:199], v29 offset:51200\n" \
"v_mfma_f32_16x16x32_bf16 v[84:87], a[240:243], a[200:203], v[84:87]\n" \
"s_addk_i32 s84, 0x7c00\n" \
"v_mfma_f32_16x16x32_bf16 v[88:91], a[180:183], a[136:139], v[88:91]\n" \
"buffer_load_dwordx4 v26, s[68:71], s84 offen offset:1024 lds\n" \
"v_mfma_f32_16x16x32_bf16 v[88:91], a[244:247], a[200:203], v[88:91]\n" \
"ds_read_b64_tr_b16 a[226:227], v32 offset:41984\n" \
"v_mfma_f32_16x16x32_bf16 v[92:95], a[184:187], a[136:139], v[92:95]\n" \
"s_addk_i32 s84, 0x7c00\n" \
"v_mfma_f32_16x16x32_bf16 v[92:95], a[248:251], a[200:203], v[92:95]\n" \
"buffer_load_dwordx4 v27, s[68:71], s84 offen offset:2048 lds\n" \
"v_mfma_f32_16x16x32_bf16 v[96:99], a[188:191], a[136:139], v[96:99]\n" \
"ds_read_b64_tr_b16 a[164:165], v33 offset:32768\n" \
"v_mfma_f32_16x16x32_bf16 v[96:99], a[252:255], a[200:203], v[96:99]\n" \
"ds_read_b128 a[136:139], v28 offset:53248\n" \
"v_mfma_f32_16x16x32_bf16 v[112:115], a[188:191], a[140:143], v[112:115]\n" \
"ds_read_b128 a[200:203], v29 offset:53248\n" \
"v_mfma_f32_16x16x32_bf16 v[112:115], a[252:255], a[204:207], v[112:115]\n" \
"s_addk_i32 s84, 0x7c00\n" \
"v_mfma_f32_16x16x32_bf16 v[108:111], a[184:187], a[140:143], v[108:111]\n" \
"buffer_load_dwordx4 v27, s[68:71], s84 offen offset:3072 lds\n" \
"v_mfma_f32_16x16x32_bf16 v[108:111], a[248:251], a[204:207], v[108:111]\n" \
"s_add_u32 m0, s30, 0x1000\n" \
"s_mov_b32 s84, 0x8300\n" \
"v_mfma_f32_16x16x32_bf16 v[104:107], a[180:183], a[140:143], v[104:107]\n" \
"buffer_load_dwordx4 v22, s[64:67], s84 offen offset:0 lds\n" \
"v_mfma_f32_16x16x32_bf16 v[104:107], a[244:247], a[204:207], v[104:107]\n" \
"ds_read_b64_tr_b16 a[166:167], v33 offset:33792\n" \
"v_mfma_f32_16x16x32_bf16 v[100:103], a[176:179], a[140:143], v[100:103]\n" \
"ds_read_b64_tr_b16 a[228:229], v33 offset:40960\n" \
"v_mfma_f32_16x16x32_bf16 v[100:103], a[240:243], a[204:207], v[100:103]\n" \
"ds_read_b128 a[140:143], v28 offset:55296\n" \
"v_mfma_f32_16x16x32_bf16 v[116:119], a[176:179], a[144:147], v[116:119]\n" \
"ds_read_b128 a[204:207], v29 offset:55296\n" \
"v_mfma_f32_16x16x32_bf16 v[116:119], a[240:243], a[208:211], v[116:119]\n" \
"s_addk_i32 s84, 0x1c00\n" \
"v_mfma_f32_16x16x32_bf16 v[120:123], a[180:183], a[144:147], v[120:123]\n" \
"buffer_load_dwordx4 v23, s[64:67], s84 offen offset:1024 lds\n" \
"v_mfma_f32_16x16x32_bf16 v[120:123], a[244:247], a[208:211], v[120:123]\n" \
"s_addk_i32 s84, 0x1c00\n" \
"v_mfma_f32_16x16x32_bf16 v[124:127], a[184:187], a[144:147], v[124:127]\n" \
"buffer_load_dwordx4 v22, s[64:67], s84 offen offset:2048 lds\n" \
"v_mfma_f32_16x16x32_bf16 v[124:127], a[248:251], a[208:211], v[124:127]\n" \
"s_addk_i32 s84, 0x1c00\n" \
"s_nop 0\n" \
"v_mfma_f32_16x16x32_bf16 v[128:131], a[188:191], a[144:147], v[128:131]\n" \
"buffer_load_dwordx4 v23, s[64:67], s84 offen offset:3072 lds\n" \
"v_mfma_f32_16x16x32_bf16 v[128:131], a[252:255], a[208:211], v[128:131]\n" \
"ds_read_b128 a[144:147], v28 offset:32768\n" \
"v_mfma_f32_16x16x32_bf16 v[144:147], a[188:191], a[148:151], v[144:147]\n" \
"ds_read_b128 a[208:211], v29 offset:32768\n" \
"v_mfma_f32_16x16x32_bf16 v[144:147], a[252:255], a[212:215], v[144:147]\n" \
"s_add_u32 m0, s31, 0x4000\n" \
"s_mov_b32 s84, 0x300000\n" \
"v_mfma_f32_16x16x32_bf16 v[140:143], a[184:187], a[148:151], v[140:143]\n" \
"buffer_load_dwordx4 v26, s[72:75], s84 offen offset:0 lds\n" \
"v_mfma_f32_16x16x32_bf16 v[140:143], a[248:251], a[212:215], v[140:143]\n" \
"ds_read_b64_tr_b16 a[230:231], v33 offset:41984\n" \
"v_mfma_f32_16x16x32_bf16 v[136:139], a[180:183], a[148:151], v[136:139]\n" \
"s_addk_i32 s84, 0x7c00\n" \
"s_nop 0\n" \
"v_mfma_f32_16x16x32_bf16 v[136:139], a[244:247], a[212:215], v[136:139]\n" \
"buffer_load_dwordx4 v26, s[72:75], s84 offen offset:1024 lds\n" \
"v_mfma_f32_16x16x32_bf16 v[132:135], a[176:179], a[148:151], v[132:135]\n" \
"ds_read_b64_tr_b16 a[168:169], v34 offset:32768\n" \
"v_mfma_f32_16x16x32_bf16 v[132:135], a[240:243], a[212:215], v[132:135]\n" \
"ds_read_b128 a[148:151], v28 offset:34816\n" \
"v_mfma_f32_16x16x32_bf16 v[148:151], a[176:179], a[152:155], v[148:151]\n" \
"ds_read_b128 a[212:215], v29 offset:34816\n" \
"v_mfma_f32_16x16x32_bf16 v[148:151], a[240:243], a[216:219], v[148:151]\n" \
"ds_read_b64_tr_b16 a[170:171], v34 offset:33792\n" \
"v_mfma_f32_16x16x32_bf16 v[152:155], a[180:183], a[152:155], v[152:155]\n" \
"ds_read_b64_tr_b16 a[232:233], v34 offset:40960\n" \
"v_mfma_f32_16x16x32_bf16 v[152:155], a[244:247], a[216:219], v[152:155]\n" \
"ds_read_b64_tr_b16 a[234:235], v34 offset:41984\n" \
"v_mfma_f32_16x16x32_bf16 v[156:159], a[184:187], a[152:155], v[156:159]\n" \
"ds_read_b64_tr_b16 a[172:173], v35 offset:32768\n" \
"v_mfma_f32_16x16x32_bf16 v[156:159], a[248:251], a[216:219], v[156:159]\n" \
"ds_read_b64_tr_b16 a[174:175], v35 offset:33792\n" \
"v_mfma_f32_16x16x32_bf16 v[160:163], a[188:191], a[152:155], v[160:163]\n" \
"ds_read_b64_tr_b16 a[236:237], v35 offset:40960\n" \
"v_mfma_f32_16x16x32_bf16 v[160:163], a[252:255], a[216:219], v[160:163]\n" \
"ds_read_b128 a[152:155], v28 offset:36864\n" \
"v_mfma_f32_16x16x32_bf16 v[176:179], a[188:191], a[156:159], v[176:179]\n" \
"ds_read_b128 a[216:219], v29 offset:36864\n" \
"v_mfma_f32_16x16x32_bf16 v[176:179], a[252:255], a[220:223], v[176:179]\n" \
"ds_read_b64_tr_b16 a[238:239], v35 offset:41984\n" \
"v_mfma_f32_16x16x32_bf16 v[172:175], a[184:187], a[156:159], v[172:175]\n" \
"v_mfma_f32_16x16x32_bf16 v[172:175], a[248:251], a[220:223], v[172:175]\n" \
"v_mfma_f32_16x16x32_bf16 v[168:171], a[180:183], a[156:159], v[168:171]\n" \
"v_mfma_f32_16x16x32_bf16 v[168:171], a[244:247], a[220:223], v[168:171]\n" \
"v_mfma_f32_16x16x32_bf16 v[164:167], a[176:179], a[156:159], v[164:167]\n" \
"v_mfma_f32_16x16x32_bf16 v[164:167], a[240:243], a[220:223], v[164:167]\n" \
"ds_read_b128 a[156:159], v28 offset:38912\n" \
"ds_read_b128 a[220:223], v29 offset:38912\n" \
"s_waitcnt lgkmcnt(15)\n" \
"v_mfma_f32_16x16x32_bf16 a[0:3], a[160:163], a[128:131], a[0:3]\n" \
"s_addk_i32 s84, 0x7c00\n" \
"v_mfma_f32_16x16x32_bf16 a[0:3], a[224:227], a[192:195], a[0:3]\n" \
"buffer_load_dwordx4 v27, s[72:75], s84 offen offset:2048 lds\n" \
"v_mfma_f32_16x16x32_bf16 a[4:7], a[164:167], a[128:131], a[4:7]\n" \
"ds_read_b64_tr_b16 a[176:177], v36 offset:32768\n" \
"s_waitcnt lgkmcnt(15)\n" \
"v_mfma_f32_16x16x32_bf16 a[4:7], a[228:231], a[192:195], a[4:7]\n" \
"s_addk_i32 s84, 0x7c00\n" \
"s_waitcnt lgkmcnt(11)\n" \
"v_mfma_f32_16x16x32_bf16 a[8:11], a[168:171], a[128:131], a[8:11]\n" \
"buffer_load_dwordx4 v27, s[72:75], s84 offen offset:3072 lds\n" \
"s_waitcnt lgkmcnt(9)\n" \
"v_mfma_f32_16x16x32_bf16 a[8:11], a[232:235], a[192:195], a[8:11]\n" \
"ds_read_b64_tr_b16 a[178:179], v36 offset:33792\n" \
"s_waitcnt lgkmcnt(8)\n" \
"v_mfma_f32_16x16x32_bf16 a[12:15], a[172:175], a[128:131], a[12:15]\n" \
"ds_read_b64_tr_b16 a[240:241], v36 offset:40960\n" \
"s_waitcnt lgkmcnt(5)\n" \
"v_mfma_f32_16x16x32_bf16 a[12:15], a[236:239], a[192:195], a[12:15]\n" \
"v_mfma_f32_16x16x32_bf16 a[28:31], a[172:175], a[132:135], a[28:31]\n" \
"v_mfma_f32_16x16x32_bf16 a[28:31], a[236:239], a[196:199], a[28:31]\n" \
"ds_read_b64_tr_b16 a[242:243], v36 offset:41984\n" \
"v_mfma_f32_16x16x32_bf16 a[24:27], a[168:171], a[132:135], a[24:27]\n" \
"v_mfma_f32_16x16x32_bf16 a[24:27], a[232:235], a[196:199], a[24:27]\n" \
"v_mfma_f32_16x16x32_bf16 a[20:23], a[164:167], a[132:135], a[20:23]\n" \
"v_mfma_f32_16x16x32_bf16 a[20:23], a[228:231], a[196:199], a[20:23]\n" \
"ds_read_b64_tr_b16 a[180:181], v37 offset:32768\n" \
"v_mfma_f32_16x16x32_bf16 a[16:19], a[160:163], a[132:135], a[16:19]\n" \
"v_mfma_f32_16x16x32_bf16 a[16:19], a[224:227], a[196:199], a[16:19]\n" \
"v_mfma_f32_16x16x32_bf16 a[32:35], a[160:163], a[136:139], a[32:35]\n" \
"ds_read_b64_tr_b16 a[182:183], v37 offset:33792\n" \
"v_mfma_f32_16x16x32_bf16 a[32:35], a[224:227], a[200:203], a[32:35]\n" \
"v_mfma_f32_16x16x32_bf16 a[36:39], a[164:167], a[136:139], a[36:39]\n" \
"v_lshlrev_b32_e32 v45, 3, v20\n" \
"v_add_u32_e32 v45, s31, v45\n" \
"v_mfma_f32_16x16x32_bf16 a[36:39], a[228:231], a[200:203], a[36:39]\n" \
"s_getpc_b64 s[86:87]\n" \
"s_sub_u32 s86, s86, 0xf800\n" \
"s_subb_u32 s87, s87, 0\n" \
"s_mov_b32 m0, s32\n" \
"s_nop 0\n" \
"v_mfma_f32_16x16x32_bf16 a[40:43], a[168:171], a[136:139], a[40:43]\n" \
"global_load_lds_dword v45, s[86:87]\n" \
"v_mfma_f32_16x16x32_bf16 a[40:43], a[232:235], a[200:203], a[40:43]\n" \
"ds_read_b64_tr_b16 a[244:245], v37 offset:40960\n" \
"v_mfma_f32_16x16x32_bf16 a[44:47], a[172:175], a[136:139], a[44:47]\n" \
"ds_read_b64_tr_b16 a[246:247], v37 offset:41984\n" \
"v_mfma_f32_16x16x32_bf16 a[44:47], a[236:239], a[200:203], a[44:47]\n" \
"v_mfma_f32_16x16x32_bf16 a[60:63], a[172:175], a[140:143], a[60:63]\n" \
"v_mfma_f32_16x16x32_bf16 a[60:63], a[236:239], a[204:207], a[60:63]\n" \
"v_mfma_f32_16x16x32_bf16 a[56:59], a[168:171], a[140:143], a[56:59]\n" \
"ds_read_b64_tr_b16 a[184:185], v38 offset:32768\n" \
"v_mfma_f32_16x16x32_bf16 a[56:59], a[232:235], a[204:207], a[56:59]\n" \
"v_mfma_f32_16x16x32_bf16 a[52:55], a[164:167], a[140:143], a[52:55]\n" \
"v_mfma_f32_16x16x32_bf16 a[52:55], a[228:231], a[204:207], a[52:55]\n" \
"ds_read_b64_tr_b16 a[186:187], v38 offset:33792\n" \
"v_mfma_f32_16x16x32_bf16 a[48:51], a[160:163], a[140:143], a[48:51]\n" \
"v_mfma_f32_16x16x32_bf16 a[48:51], a[224:227], a[204:207], a[48:51]\n" \
"v_mfma_f32_16x16x32_bf16 a[64:67], a[160:163], a[144:147], a[64:67]\n" \
"ds_read_b64_tr_b16 a[248:249], v38 offset:40960\n" \
"v_mfma_f32_16x16x32_bf16 a[64:67], a[224:227], a[208:211], a[64:67]\n" \
"v_mfma_f32_16x16x32_bf16 a[68:71], a[164:167], a[144:147], a[68:71]\n" \
"v_mfma_f32_16x16x32_bf16 a[68:71], a[228:231], a[208:211], a[68:71]\n" \
"ds_read_b64_tr_b16 a[250:251], v38 offset:41984\n" \
"v_mfma_f32_16x16x32_bf16 a[72:75], a[168:171], a[144:147], a[72:75]\n" \
"v_mfma_f32_16x16x32_bf16 a[72:75], a[232:235], a[208:211], a[72:75]\n" \
"v_mfma_f32_16x16x32_bf16 a[76:79], a[172:175], a[144:147], a[76:79]\n" \
"v_mfma_f32_16x16x32_bf16 a[76:79], a[236:239], a[208:211], a[76:79]\n" \
"ds_read_b64_tr_b16 a[188:189], v39 offset:32768\n" \
"v_mfma_f32_16x16x32_bf16 a[92:95], a[172:175], a[148:151], a[92:95]\n" \
"v_mfma_f32_16x16x32_bf16 a[92:95], a[236:239], a[212:215], a[92:95]\n" \
"v_mfma_f32_16x16x32_bf16 a[88:91], a[168:171], a[148:151], a[88:91]\n" \
"ds_read_b64_tr_b16 a[190:191], v39 offset:33792\n" \
"v_mfma_f32_16x16x32_bf16 a[88:91], a[232:235], a[212:215], a[88:91]\n" \
"v_mfma_f32_16x16x32_bf16 a[84:87], a[164:167], a[148:151], a[84:87]\n" \
"v_mfma_f32_16x16x32_bf16 a[84:87], a[228:231], a[212:215], a[84:87]\n" \
"ds_read_b64_tr_b16 a[252:253], v39 offset:40960\n" \
"v_mfma_f32_16x16x32_bf16 a[80:83], a[160:163], a[148:151], a[80:83]\n" \
"v_mfma_f32_16x16x32_bf16 a[80:83], a[224:227], a[212:215], a[80:83]\n" \
"v_mfma_f32_16x16x32_bf16 a[96:99], a[160:163], a[152:155], a[96:99]\n" \
"ds_read_b64_tr_b16 a[254:255], v39 offset:41984\n" \
"v_mfma_f32_16x16x32_bf16 a[96:99], a[224:227], a[216:219], a[96:99]\n" \
"v_mfma_f32_16x16x32_bf16 a[100:103], a[164:167], a[152:155], a[100:103]\n" \
"v_mfma_f32_16x16x32_bf16 a[100:103], a[228:231], a[216:219], a[100:103]\n" \
"v_mfma_f32_16x16x32_bf16 a[104:107], a[168:171], a[152:155], a[104:107]\n" \
"v_mfma_f32_16x16x32_bf16 a[104:107], a[232:235], a[216:219], a[104:107]\n" \
"v_mfma_f32_16x16x32_bf16 a[108:111], a[172:175], a[152:155], a[108:111]\n" \
"v_mfma_f32_16x16x32_bf16 a[108:111], a[236:239], a[216:219], a[108:111]\n" \
"s_waitcnt lgkmcnt(15)\n" \
"v_mfma_f32_16x16x32_bf16 a[124:127], a[172:175], a[156:159], a[124:127]\n" \
"v_mfma_f32_16x16x32_bf16 a[124:127], a[236:239], a[220:223], a[124:127]\n" \
"v_mfma_f32_16x16x32_bf16 a[120:123], a[168:171], a[156:159], a[120:123]\n" \
"v_mfma_f32_16x16x32_bf16 a[120:123], a[232:235], a[220:223], a[120:123]\n" \
"v_mfma_f32_16x16x32_bf16 a[116:119], a[164:167], a[156:159], a[116:119]\n" \
"v_mfma_f32_16x16x32_bf16 a[116:119], a[228:231], a[220:223], a[116:119]\n" \
"v_mfma_f32_16x16x32_bf16 a[112:115], a[160:163], a[156:159], a[112:115]\n" \
"v_mfma_f32_16x16x32_bf16 a[112:115], a[224:227], a[220:223], a[112:115]\n" \
"s_waitcnt vmcnt(1) lgkmcnt(0)\n" \
"s_barrier\n" \
"v_mov_b32_e32 v45, s62\n" \
"s_mov_b64 exec, 1\n" \
"buffer_store_dword v45, off, s[80:83], s37 offset:128 sc1\n" \
"s_mov_b64 exec, -1\n" \
"v_mfma_f32_16x16x32_bf16 v[52:55], a[176:179], a[128:131], v[52:55]\n" \
"s_add_u32 m0, s30, 0x8000\n" \
"s_movk_i32 s84, 0x380\n" \
"v_mfma_f32_16x16x32_bf16 v[52:55], a[240:243], a[192:195], v[52:55]\n" \
"buffer_load_dwordx4 v22, s[64:67], s84 offen offset:0 lds\n" \
"v_mfma_f32_16x16x32_bf16 v[56:59], a[180:183], a[128:131], v[56:59]\n" \
"ds_read_b64_tr_b16 a[160:161], v32 offset:0\n" \
"v_mfma_f32_16x16x32_bf16 v[56:59], a[244:247], a[192:195], v[56:59]\n" \
"s_movk_i32 s84, 0x1f80\n" \
"v_mfma_f32_16x16x32_bf16 v[60:63], a[184:187], a[128:131], v[60:63]\n" \
"buffer_load_dwordx4 v23, s[64:67], s84 offen offset:1024 lds\n" \
"v_mfma_f32_16x16x32_bf16 v[60:63], a[248:251], a[192:195], v[60:63]\n" \
"ds_read_b64_tr_b16 a[162:163], v32 offset:1024\n" \
"v_mfma_f32_16x16x32_bf16 v[64:67], a[188:191], a[128:131], v[64:67]\n" \
"ds_read_b64_tr_b16 a[224:225], v32 offset:8192\n" \
"v_mfma_f32_16x16x32_bf16 v[64:67], a[252:255], a[192:195], v[64:67]\n" \
"ds_read_b128 a[128:131], v28 offset:16384\n" \
"v_mfma_f32_16x16x32_bf16 v[80:83], a[188:191], a[132:135], v[80:83]\n" \
"ds_read_b128 a[192:195], v29 offset:16384\n" \
"v_mfma_f32_16x16x32_bf16 v[80:83], a[252:255], a[196:199], v[80:83]\n" \
"s_movk_i32 s84, 0x3b80\n" \
"v_mfma_f32_16x16x32_bf16 v[76:79], a[184:187], a[132:135], v[76:79]\n" \
"buffer_load_dwordx4 v22, s[64:67], s84 offen offset:2048 lds\n" \
"v_mfma_f32_16x16x32_bf16 v[76:79], a[248:251], a[196:199], v[76:79]\n" \
"s_movk_i32 s84, 0x5780\n" \
"v_mfma_f32_16x16x32_bf16 v[72:75], a[180:183], a[132:135], v[72:75]\n" \
"buffer_load_dwordx4 v23, s[64:67], s84 offen offset:3072 lds\n" \
"v_mfma_f32_16x16x32_bf16 v[72:75], a[244:247], a[196:199], v[72:75]\n" \
"s_add_u32 m0, s31, 0x8000\n" \
"s_mov_b32 s84, 0x380000\n" \
"v_mfma_f32_16x16x32_bf16 v[68:71], a[176:179], a[132:135], v[68:71]\n" \
"buffer_load_dwordx4 v26, s[68:71], s84 offen offset:0 lds\n" \
"v_mfma_f32_16x16x32_bf16 v[68:71], a[240:243], a[196:199], v[68:71]\n" \
"ds_read_b128 a[132:135], v28 offset:18432\n" \
"v_mfma_f32_16x16x32_bf16 v[84:87], a[176:179], a[136:139], v[84:87]\n" \
"ds_read_b128 a[196:199], v29 offset:18432\n" \
"v_mfma_f32_16x16x32_bf16 v[84:87], a[240:243], a[200:203], v[84:87]\n" \
"s_addk_i32 s84, 0x7c00\n" \
"v_mfma_f32_16x16x32_bf16 v[88:91], a[180:183], a[136:139], v[88:91]\n" \
"buffer_load_dwordx4 v26, s[68:71], s84 offen offset:1024 lds\n" \
"v_mfma_f32_16x16x32_bf16 v[88:91], a[244:247], a[200:203], v[88:91]\n" \
"ds_read_b64_tr_b16 a[226:227], v32 offset:9216\n" \
"v_mfma_f32_16x16x32_bf16 v[92:95], a[184:187], a[136:139], v[92:95]\n" \
"s_addk_i32 s84, 0x7c00\n" \
"v_mfma_f32_16x16x32_bf16 v[92:95], a[248:251], a[200:203], v[92:95]\n" \
"buffer_load_dwordx4 v27, s[68:71], s84 offen offset:2048 lds\n" \
"v_mfma_f32_16x16x32_bf16 v[96:99], a[188:191], a[136:139], v[96:99]\n" \
"ds_read_b64_tr_b16 a[164:165], v33 offset:0\n" \
"v_mfma_f32_16x16x32_bf16 v[96:99], a[252:255], a[200:203], v[96:99]\n" \
"ds_read_b128 a[136:139], v28 offset:20480\n" \
"v_mfma_f32_16x16x32_bf16 v[112:115], a[188:191], a[140:143], v[112:115]\n" \
"ds_read_b128 a[200:203], v29 offset:20480\n" \
"v_mfma_f32_16x16x32_bf16 v[112:115], a[252:255], a[204:207], v[112:115]\n" \
"s_addk_i32 s84, 0x7c00\n" \
"s_nop 0\n" \
"v_mfma_f32_16x16x32_bf16 v[108:111], a[184:187], a[140:143], v[108:111]\n" \
"buffer_load_dwordx4 v27, s[68:71], s84 offen offset:3072 lds\n" \
"v_mfma_f32_16x16x32_bf16 v[108:111], a[248:251], a[204:207], v[108:111]\n" \
"s_add_u32 m0, s30, 0x9000\n" \
"s_mov_b32 s84, 0x8380\n" \
"v_mfma_f32_16x16x32_bf16 v[104:107], a[180:183], a[140:143], v[104:107]\n" \
"buffer_load_dwordx4 v22, s[64:67], s84 offen offset:0 lds\n" \
"v_mfma_f32_16x16x32_bf16 v[104:107], a[244:247], a[204:207], v[104:107]\n" \
"ds_read_b64_tr_b16 a[166:167], v33 offset:1024\n" \
"v_mfma_f32_16x16x32_bf16 v[100:103], a[176:179], a[140:143], v[100:103]\n" \
"ds_read_b64_tr_b16 a[228:229], v33 offset:8192\n" \
"v_mfma_f32_16x16x32_bf16 v[100:103], a[240:243], a[204:207], v[100:103]\n" \
"ds_read_b128 a[140:143], v28 offset:22528\n" \
"v_mfma_f32_16x16x32_bf16 v[116:119], a[176:179], a[144:147], v[116:119]\n" \
"ds_read_b128 a[204:207], v29 offset:22528\n" \
"v_mfma_f32_16x16x32_bf16 v[116:119], a[240:243], a[208:211], v[116:119]\n" \
"s_addk_i32 s84, 0x1c00\n" \
"v_mfma_f32_16x16x32_bf16 v[120:123], a[180:183], a[144:147], v[120:123]\n" \
"buffer_load_dwordx4 v23, s[64:67], s84 offen offset:1024 lds\n" \
"v_mfma_f32_16x16x32_bf16 v[120:123], a[244:247], a[208:211], v[120:123]\n" \
"s_addk_i32 s84, 0x1c00\n" \
"v_mfma_f32_16x16x32_bf16 v[124:127], a[184:187], a[144:147], v[124:127]\n" \
"buffer_load_dwordx4 v22, s[64:67], s84 offen offset:2048 lds\n" \
"v_mfma_f32_16x16x32_bf16 v[124:127], a[248:251], a[208:211], v[124:127]\n" \
"s_addk_i32 s84, 0x1c00\n" \
"s_nop 0\n" \
"v_mfma_f32_16x16x32_bf16 v[128:131], a[188:191], a[144:147], v[128:131]\n" \
"buffer_load_dwordx4 v23, s[64:67], s84 offen offset:3072 lds\n" \
"v_mfma_f32_16x16x32_bf16 v[128:131], a[252:255], a[208:211], v[128:131]\n" \
"ds_read_b128 a[144:147], v28 offset:0\n" \
"v_mfma_f32_16x16x32_bf16 v[144:147], a[188:191], a[148:151], v[144:147]\n" \
"ds_read_b128 a[208:211], v29 offset:0\n" \
"v_mfma_f32_16x16x32_bf16 v[144:147], a[252:255], a[212:215], v[144:147]\n" \
"s_add_u32 m0, s31, 0xc000\n" \
"s_mov_b32 s84, 0x380000\n" \
"v_mfma_f32_16x16x32_bf16 v[140:143], a[184:187], a[148:151], v[140:143]\n" \
"buffer_load_dwordx4 v26, s[72:75], s84 offen offset:0 lds\n" \
"v_mfma_f32_16x16x32_bf16 v[140:143], a[248:251], a[212:215], v[140:143]\n" \
"ds_read_b64_tr_b16 a[230:231], v33 offset:9216\n" \
"v_mfma_f32_16x16x32_bf16 v[136:139], a[180:183], a[148:151], v[136:139]\n" \
"s_addk_i32 s84, 0x7c00\n" \
"s_nop 0\n" \
"v_mfma_f32_16x16x32_bf16 v[136:139], a[244:247], a[212:215], v[136:139]\n" \
"buffer_load_dwordx4 v26, s[72:75], s84 offen offset:1024 lds\n" \
"v_mfma_f32_16x16x32_bf16 v[132:135], a[176:179], a[148:151], v[132:135]\n" \
"ds_read_b64_tr_b16 a[168:169], v34 offset:0\n" \
"v_mfma_f32_16x16x32_bf16 v[132:135], a[240:243], a[212:215], v[132:135]\n" \
"ds_read_b128 a[148:151], v28 offset:2048\n" \
"v_mfma_f32_16x16x32_bf16 v[148:151], a[176:179], a[152:155], v[148:151]\n" \
"ds_read_b128 a[212:215], v29 offset:2048\n" \
"v_mfma_f32_16x16x32_bf16 v[148:151], a[240:243], a[216:219], v[148:151]\n" \
"ds_read_b64_tr_b16 a[170:171], v34 offset:1024\n" \
"v_mfma_f32_16x16x32_bf16 v[152:155], a[180:183], a[152:155], v[152:155]\n" \
"ds_read_b64_tr_b16 a[232:233], v34 offset:8192\n" \
"v_mfma_f32_16x16x32_bf16 v[152:155], a[244:247], a[216:219], v[152:155]\n" \
"ds_read_b64_tr_b16 a[234:235], v34 offset:9216\n" \
"v_mfma_f32_16x16x32_bf16 v[156:159], a[184:187], a[152:155], v[156:159]\n" \
"ds_read_b64_tr_b16 a[172:173], v35 offset:0\n" \
"v_mfma_f32_16x16x32_bf16 v[156:159], a[248:251], a[216:219], v[156:159]\n" \
"ds_read_b64_tr_b16 a[174:175], v35 offset:1024\n" \
"v_mfma_f32_16x16x32_bf16 v[160:163], a[188:191], a[152:155], v[160:163]\n" \
"ds_read_b64_tr_b16 a[236:237], v35 offset:8192\n" \
"v_mfma_f32_16x16x32_bf16 v[160:163], a[252:255], a[216:219], v[160:163]\n" \
"ds_read_b128 a[152:155], v28 offset:4096\n" \
"v_mfma_f32_16x16x32_bf16 v[176:179], a[188:191], a[156:159], v[176:179]\n" \
"ds_read_b128 a[216:219], v29 offset:4096\n" \
"v_mfma_f32_16x16x32_bf16 v[176:179], a[252:255], a[220:223], v[176:179]\n" \
"ds_read_b64_tr_b16 a[238:239], v35 offset:9216\n" \
"v_mfma_f32_16x16x32_bf16 v[172:175], a[184:187], a[156:159], v[172:175]\n" \
"v_mfma_f32_16x16x32_bf16 v[172:175], a[248:251], a[220:223], v[172:175]\n" \
"v_mfma_f32_16x16x32_bf16 v[168:171], a[180:183], a[156:159], v[168:171]\n" \
"v_mfma_f32_16x16x32_bf16 v[168:171], a[244:247], a[220:223], v[168:171]\n" \
"v_mfma_f32_16x16x32_bf16 v[164:167], a[176:179], a[156:159], v[164:167]\n" \
"v_mfma_f32_16x16x32_bf16 v[164:167], a[240:243], a[220:223], v[164:167]\n" \
"ds_read_b128 a[156:159], v28 offset:6144\n" \
"ds_read_b128 a[220:223], v29 offset:6144\n" \
"s_waitcnt lgkmcnt(15)\n" \
"v_mfma_f32_16x16x32_bf16 a[0:3], a[160:163], a[128:131], a[0:3]\n" \
"s_addk_i32 s84, 0x7c00\n" \
"v_mfma_f32_16x16x32_bf16 a[0:3], a[224:227], a[192:195], a[0:3]\n" \
"buffer_load_dwordx4 v27, s[72:75], s84 offen offset:2048 lds\n" \
"v_mfma_f32_16x16x32_bf16 a[4:7], a[164:167], a[128:131], a[4:7]\n" \
"ds_read_b64_tr_b16 a[176:177], v36 offset:0\n" \
"s_waitcnt lgkmcnt(15)\n" \
"v_mfma_f32_16x16x32_bf16 a[4:7], a[228:231], a[192:195], a[4:7]\n" \
"s_addk_i32 s84, 0x7c00\n" \
"s_waitcnt lgkmcnt(11)\n" \
"v_mfma_f32_16x16x32_bf16 a[8:11], a[168:171], a[128:131], a[8:11]\n" \
"buffer_load_dwordx4 v27, s[72:75], s84 offen offset:3072 lds\n" \
"s_waitcnt lgkmcnt(9)\n" \
"v_mfma_f32_16x16x32_bf16 a[8:11], a[232:235], a[192:195], a[8:11]\n" \
"ds_read_b64_tr_b16 a[178:179], v36 offset:1024\n" \
"s_waitcnt lgkmcnt(8)\n" \
"v_mfma_f32_16x16x32_bf16 a[12:15], a[172:175], a[128:131], a[12:15]\n" \
"ds_read_b64_tr_b16 a[240:241], v36 offset:8192\n" \
"s_waitcnt lgkmcnt(5)\n" \
"v_mfma_f32_16x16x32_bf16 a[12:15], a[236:239], a[192:195], a[12:15]\n" \
"v_mfma_f32_16x16x32_bf16 a[28:31], a[172:175], a[132:135], a[28:31]\n" \
"v_mfma_f32_16x16x32_bf16 a[28:31], a[236:239], a[196:199], a[28:31]\n" \
"ds_read_b64_tr_b16 a[242:243], v36 offset:9216\n" \
"v_mfma_f32_16x16x32_bf16 a[24:27], a[168:171], a[132:135], a[24:27]\n" \
"v_mfma_f32_16x16x32_bf16 a[24:27], a[232:235], a[196:199], a[24:27]\n" \
"v_mfma_f32_16x16x32_bf16 a[20:23], a[164:167], a[132:135], a[20:23]\n" \
"v_mfma_f32_16x16x32_bf16 a[20:23], a[228:231], a[196:199], a[20:23]\n" \
"ds_read_b64_tr_b16 a[180:181], v37 offset:0\n" \
"v_mfma_f32_16x16x32_bf16 a[16:19], a[160:163], a[132:135], a[16:19]\n" \
"v_mfma_f32_16x16x32_bf16 a[16:19], a[224:227], a[196:199], a[16:19]\n" \
"v_mfma_f32_16x16x32_bf16 a[32:35], a[160:163], a[136:139], a[32:35]\n" \
"ds_read_b64_tr_b16 a[182:183], v37 offset:1024\n" \
"v_mfma_f32_16x16x32_bf16 a[32:35], a[224:227], a[200:203], a[32:35]\n" \
"v_mfma_f32_16x16x32_bf16 a[36:39], a[164:167], a[136:139], a[36:39]\n" \
"v_mfma_f32_16x16x32_bf16 a[36:39], a[228:231], a[200:203], a[36:39]\n" \
"ds_read_b64_tr_b16 a[244:245], v37 offset:8192\n" \
"v_mfma_f32_16x16x32_bf16 a[40:43], a[168:171], a[136:139], a[40:43]\n" \
"v_mfma_f32_16x16x32_bf16 a[40:43], a[232:235], a[200:203], a[40:43]\n" \
"v_mfma_f32_16x16x32_bf16 a[44:47], a[172:175], a[136:139], a[44:47]\n" \
"ds_read_b64_tr_b16 a[246:247], v37 offset:9216\n" \
"v_mfma_f32_16x16x32_bf16 a[44:47], a[236:239], a[200:203], a[44:47]\n" \
"v_mfma_f32_16x16x32_bf16 a[60:63], a[172:175], a[140:143], a[60:63]\n" \
"v_mfma_f32_16x16x32_bf16 a[60:63], a[236:239], a[204:207], a[60:63]\n" \
"v_mfma_f32_16x16x32_bf16 a[56:59], a[168:171], a[140:143], a[56:59]\n" \
"ds_read_b64_tr_b16 a[184:185], v38 offset:0\n" \
"v_mfma_f32_16x16x32_bf16 a[56:59], a[232:235], a[204:207], a[56:59]\n" \
"v_mfma_f32_16x16x32_bf16 a[52:55], a[164:167], a[140:143], a[52:55]\n" \
"v_mfma_f32_16x16x32_bf16 a[52:55], a[228:231], a[204:207], a[52:55]\n" \
"ds_read_b64_tr_b16 a[186:187], v38 offset:1024\n" \
"v_mfma_f32_16x16x32_bf16 a[48:51], a[160:163], a[140:143], a[48:51]\n" \
"v_mfma_f32_16x16x32_bf16 a[48:51], a[224:227], a[204:207], a[48:51]\n" \
"v_mfma_f32_16x16x32_bf16 a[64:67], a[160:163], a[144:147], a[64:67]\n" \
"v_bfe_u32 v44, v20, 2, 5\n" \
"ds_read_b64_tr_b16 a[248:249], v38 offset:8192\n" \
"v_mfma_f32_16x16x32_bf16 a[64:67], a[224:227], a[208:211], a[64:67]\n" \
"buffer_load_dword v44, v44, s[80:83], s38 offen offset:128 sc1\n" \
"v_mfma_f32_16x16x32_bf16 a[68:71], a[164:167], a[144:147], a[68:71]\n" \
"v_mfma_f32_16x16x32_bf16 a[68:71], a[228:231], a[208:211], a[68:71]\n" \
"ds_read_b64_tr_b16 a[250:251], v38 offset:9216\n" \
"v_mfma_f32_16x16x32_bf16 a[72:75], a[168:171], a[144:147], a[72:75]\n" \
"v_mfma_f32_16x16x32_bf16 a[72:75], a[232:235], a[208:211], a[72:75]\n" \
"v_mfma_f32_16x16x32_bf16 a[76:79], a[172:175], a[144:147], a[76:79]\n" \
"v_mfma_f32_16x16x32_bf16 a[76:79], a[236:239], a[208:211], a[76:79]\n" \
"ds_read_b64_tr_b16 a[188:189], v39 offset:0\n" \
"v_mfma_f32_16x16x32_bf16 a[92:95], a[172:175], a[148:151], a[92:95]\n" \
"v_mfma_f32_16x16x32_bf16 a[92:95], a[236:239], a[212:215], a[92:95]\n" \
"v_mfma_f32_16x16x32_bf16 a[88:91], a[168:171], a[148:151], a[88:91]\n" \
"ds_read_b64_tr_b16 a[190:191], v39 offset:1024\n" \
"v_mfma_f32_16x16x32_bf16 a[88:91], a[232:235], a[212:215], a[88:91]\n" \
"v_mfma_f32_16x16x32_bf16 a[84:87], a[164:167], a[148:151], a[84:87]\n" \
"v_mfma_f32_16x16x32_bf16 a[84:87], a[228:231], a[212:215], a[84:87]\n" \
"ds_read_b64_tr_b16 a[252:253], v39 offset:8192\n" \
"v_mfma_f32_16x16x32_bf16 a[80:83], a[160:163], a[148:151], a[80:83]\n" \
"v_mfma_f32_16x16x32_bf16 a[80:83], a[224:227], a[212:215], a[80:83]\n" \
"v_mfma_f32_16x16x32_bf16 a[96:99], a[160:163], a[152:155], a[96:99]\n" \
"ds_read_b64_tr_b16 a[254:255], v39 offset:9216\n" \
"v_mfma_f32_16x16x32_bf16 a[96:99], a[224:227], a[216:219], a[96:99]\n" \
"v_mfma_f32_16x16x32_bf16 a[100:103], a[164:167], a[152:155], a[100:103]\n" \
"v_mfma_f32_16x16x32_bf16 a[100:103], a[228:231], a[216:219], a[100:103]\n" \
"v_mfma_f32_16x16x32_bf16 a[104:107], a[168:171], a[152:155], a[104:107]\n" \
"v_mfma_f32_16x16x32_bf16 a[104:107], a[232:235], a[216:219], a[104:107]\n" \
"v_mfma_f32_16x16x32_bf16 a[108:111], a[172:175], a[152:155], a[108:111]\n" \
"v_mfma_f32_16x16x32_bf16 a[108:111], a[236:239], a[216:219], a[108:111]\n" \
"s_waitcnt lgkmcnt(15)\n" \
"v_mfma_f32_16x16x32_bf16 a[124:127], a[172:175], a[156:159], a[124:127]\n" \
"v_mfma_f32_16x16x32_bf16 a[124:127], a[236:239], a[220:223], a[124:127]\n" \
"v_mfma_f32_16x16x32_bf16 a[120:123], a[168:171], a[156:159], a[120:123]\n" \
"v_mfma_f32_16x16x32_bf16 a[120:123], a[232:235], a[220:223], a[120:123]\n" \
"v_mfma_f32_16x16x32_bf16 a[116:119], a[164:167], a[156:159], a[116:119]\n" \
"v_mfma_f32_16x16x32_bf16 a[116:119], a[228:231], a[220:223], a[116:119]\n" \
"v_mfma_f32_16x16x32_bf16 a[112:115], a[160:163], a[156:159], a[112:115]\n" \
"v_mfma_f32_16x16x32_bf16 a[112:115], a[224:227], a[220:223], a[112:115]\n" \
"s_waitcnt vmcnt(0) lgkmcnt(0)\n" \
"s_barrier\n" \
"v_cmp_ne_u32_e32 vcc, s62, v44\n" \
"s_nop 4\n" \
"s_cbranch_vccnz L_poll60_%=\n" \
"L_pollr60_%=:\n" \
"s_mov_b32 s68, s12\n" \
"s_and_b32 s69, s13, 0xffff\n" \
"s_mov_b32 s70, 0x480000\n" \
"s_mov_b32 s71, 0x20000\n" \
"v_mfma_f32_16x16x32_bf16 v[52:55], a[176:179], a[128:131], v[52:55]\n" \
"s_add_u32 m0, s40, 0x0\n" \
"s_add_u32 s85, s39, 0x0\n" \
"s_nop 0\n" \
"v_mfma_f32_16x16x32_bf16 v[52:55], a[240:243], a[192:195], v[52:55]\n" \
"buffer_load_dwordx4 v20, s[80:83], s85 offen sc1 lds\n" \
"v_mfma_f32_16x16x32_bf16 v[56:59], a[180:183], a[128:131], v[56:59]\n" \
"ds_read_b64_tr_b16 a[160:161], v32 offset:32768\n" \
"v_mfma_f32_16x16x32_bf16 v[56:59], a[244:247], a[192:195], v[56:59]\n" \
"s_add_u32 m0, s40, 0x1000\n" \
"s_add_u32 s85, s39, 0x1000\n" \
"v_mfma_f32_16x16x32_bf16 v[60:63], a[184:187], a[128:131], v[60:63]\n" \
"buffer_load_dwordx4 v20, s[80:83], s85 offen sc1 lds\n" \
"v_mfma_f32_16x16x32_bf16 v[60:63], a[248:251], a[192:195], v[60:63]\n" \
"ds_read_b64_tr_b16 a[162:163], v32 offset:33792\n" \
"v_mfma_f32_16x16x32_bf16 v[64:67], a[188:191], a[128:131], v[64:67]\n" \
"ds_read_b64_tr_b16 a[224:225], v32 offset:40960\n" \
"v_mfma_f32_16x16x32_bf16 v[64:67], a[252:255], a[192:195], v[64:67]\n" \
"ds_read_b128 a[128:131], v28 offset:49152\n" \
"v_mfma_f32_16x16x32_bf16 v[80:83], a[188:191], a[132:135], v[80:83]\n" \
"ds_read_b128 a[192:195], v29 offset:49152\n" \
"v_mfma_f32_16x16x32_bf16 v[80:83], a[252:255], a[196:199], v[80:83]\n" \
"s_add_u32 m0, s40, 0x3000\n" \
"s_movk_i32 s84, 0x0\n" \
"v_mfma_f32_16x16x32_bf16 v[76:79], a[184:187], a[132:135], v[76:79]\n" \
"buffer_load_dwordx4 v21, s[76:79], s84 offen offset:0 lds\n" \
"v_mfma_f32_16x16x32_bf16 v[76:79], a[248:251], a[196:199], v[76:79]\n" \
"s_add_u32 m0, s30, 0x0\n" \
"s_movk_i32 s84, 0x0\n" \
"s_nop 0\n" \
"v_mfma_f32_16x16x32_bf16 v[72:75], a[180:183], a[132:135], v[72:75]\n" \
"buffer_load_dwordx4 v24, s[76:79], s84 offen offset:0 lds\n" \
"v_mfma_f32_16x16x32_bf16 v[72:75], a[244:247], a[196:199], v[72:75]\n" \
"ds_read_b64_tr_b16 a[226:227], v32 offset:41984\n" \
"v_mfma_f32_16x16x32_bf16 v[68:71], a[176:179], a[132:135], v[68:71]\n" \
"ds_read_b64_tr_b16 a[164:165], v33 offset:32768\n" \
"v_mfma_f32_16x16x32_bf16 v[68:71], a[240:243], a[196:199], v[68:71]\n" \
"ds_read_b128 a[132:135], v28 offset:51200\n" \
"v_mfma_f32_16x16x32_bf16 v[84:87], a[176:179], a[136:139], v[84:87]\n" \
"ds_read_b128 a[196:199], v29 offset:51200\n" \
"v_mfma_f32_16x16x32_bf16 v[84:87], a[240:243], a[200:203], v[84:87]\n" \
"s_movk_i32 s84, 0x2000\n" \
"v_mfma_f32_16x16x32_bf16 v[88:91], a[180:183], a[136:139], v[88:91]\n" \
"buffer_load_dwordx4 v25, s[76:79], s84 offen offset:1024 lds\n" \
"v_mfma_f32_16x16x32_bf16 v[88:91], a[244:247], a[200:203], v[88:91]\n" \
"s_movk_i32 s84, 0x4000\n" \
"v_mfma_f32_16x16x32_bf16 v[92:95], a[184:187], a[136:139], v[92:95]\n" \
"buffer_load_dwordx4 v24, s[76:79], s84 offen offset:2048 lds\n" \
"v_mfma_f32_16x16x32_bf16 v[92:95], a[248:251], a[200:203], v[92:95]\n" \
"ds_read_b64_tr_b16 a[166:167], v33 offset:33792\n" \
"v_mfma_f32_16x16x32_bf16 v[96:99], a[188:191], a[136:139], v[96:99]\n" \
"ds_read_b64_tr_b16 a[228:229], v33 offset:40960\n" \
"v_mfma_f32_16x16x32_bf16 v[96:99], a[252:255], a[200:203], v[96:99]\n" \
"ds_read_b128 a[136:139], v28 offset:53248\n" \
"v_mfma_f32_16x16x32_bf16 v[112:115], a[188:191], a[140:143], v[112:115]\n" \
"ds_read_b128 a[200:203], v29 offset:53248\n" \
"v_mfma_f32_16x16x32_bf16 v[112:115], a[252:255], a[204:207], v[112:115]\n" \
"s_movk_i32 s84, 0x6000\n" \
"v_mfma_f32_16x16x32_bf16 v[108:111], a[184:187], a[140:143], v[108:111]\n" \
"buffer_load_dwordx4 v25, s[76:79], s84 offen offset:3072 lds\n" \
"v_mfma_f32_16x16x32_bf16 v[108:111], a[248:251], a[204:207], v[108:111]\n" \
"s_add_u32 m0, s31, 0x0\n" \
"s_movk_i32 s84, 0x0\n" \
"v_mfma_f32_16x16x32_bf16 v[104:107], a[180:183], a[140:143], v[104:107]\n" \
"buffer_load_dwordx4 v26, s[68:71], s84 offen offset:0 lds\n" \
"v_mfma_f32_16x16x32_bf16 v[104:107], a[244:247], a[204:207], v[104:107]\n" \
"s_movk_i32 s84, 0x7c00\n" \
".fill (L_pb + 16096 - .) / 4, 4, 0xBF800000\n" \
"L_pp4:\n" \
"s_cbranch_execz L_pret\n" \
"v_mfma_f32_16x16x32_bf16 v[100:103], a[176:179], a[140:143], v[100:103]\n" \
"buffer_load_dwordx4 v26, s[68:71], s84 offen offset:1024 lds\n" \
"v_mfma_f32_16x16x32_bf16 v[100:103], a[240:243], a[204:207], v[100:103]\n" \
"ds_read_b128 a[140:143], v28 offset:55296\n" \
"v_mfma_f32_16x16x32_bf16 v[116:119], a[176:179], a[144:147], v[116:119]\n" \
"ds_read_b128 a[204:207], v29 offset:55296\n" \
"v_mfma_f32_16x16x32_bf16 v[116:119], a[240:243], a[208:211], v[116:119]\n" \
"s_addk_i32 s84, 0x7c00\n" \
"v_mfma_f32_16x16x32_bf16 v[120:123], a[180:183], a[144:147], v[120:123]\n" \
"buffer_load_dwordx4 v27, s[68:71], s84 offen offset:2048 lds\n" \
"v_mfma_f32_16x16x32_bf16 v[120:123], a[244:247], a[208:211], v[120:123]\n" \
"ds_read_b64_tr_b16 a[230:231], v33 offset:41984\n" \
"v_mfma_f32_16x16x32_bf16 v[124:127], a[184:187], a[144:147], v[124:127]\n" \
"s_addk_i32 s84, 0x7c00\n" \
"s_nop 0\n" \
"v_mfma_f32_16x16x32_bf16 v[124:127], a[248:251], a[208:211], v[124:127]\n" \
"buffer_load_dwordx4 v27, s[68:71], s84 offen offset:3072 lds\n" \
"v_mfma_f32_16x16x32_bf16 v[128:131], a[188:191], a[144:147], v[128:131]\n" \
"ds_read_b64_tr_b16 a[168:169], v34 offset:32768\n" \
"v_mfma_f32_16x16x32_bf16 v[128:131], a[252:255], a[208:211], v[128:131]\n" \
"ds_read_b128 a[144:147], v28 offset:32768\n" \
"v_mfma_f32_16x16x32_bf16 v[144:147], a[188:191], a[148:151], v[144:147]\n" \
"ds_read_b128 a[208:211], v29 offset:32768\n" \
"v_mfma_f32_16x16x32_bf16 v[144:147], a[252:255], a[212:215], v[144:147]\n" \
"s_add_u32 m0, s30, 0x1000\n" \
"s_mov_b32 s84, 0x9000\n" \
"v_mfma_f32_16x16x32_bf16 v[140:143], a[184:187], a[148:151], v[140:143]\n" \
"buffer_load_dwordx4 v24, s[76:79], s84 offen offset:0 lds\n" \
"v_mfma_f32_16x16x32_bf16 v[140:143], a[248:251], a[212:215], v[140:143]\n" \
"ds_read_b64_tr_b16 a[170:171], v34 offset:33792\n" \
"v_mfma_f32_16x16x32_bf16 v[136:139], a[180:183], a[148:151], v[136:139]\n" \
"s_addk_i32 s84, 0x2000\n" \
"s_nop 0\n" \
"v_mfma_f32_16x16x32_bf16 v[136:139], a[244:247], a[212:215], v[136:139]\n" \
"buffer_load_dwordx4 v25, s[76:79], s84 offen offset:1024 lds\n" \
"v_mfma_f32_16x16x32_bf16 v[132:135], a[176:179], a[148:151], v[132:135]\n" \
"ds_read_b64_tr_b16 a[232:233], v34 offset:40960\n" \
"v_mfma_f32_16x16x32_bf16 v[132:135], a[240:243], a[212:215], v[132:135]\n" \
"ds_read_b128 a[148:151], v28 offset:34816\n" \
"v_mfma_f32_16x16x32_bf16 v[148:151], a[176:179], a[152:155], v[148:151]\n" \
"ds_read_b128 a[212:215], v29 offset:34816\n" \
"v_mfma_f32_16x16x32_bf16 v[148:151], a[240:243], a[216:219], v[148:151]\n" \
"ds_read_b64_tr_b16 a[234:235], v34 offset:41984\n" \
"v_mfma_f32_16x16x32_bf16 v[152:155], a[180:183], a[152:155], v[152:155]\n" \
"ds_read_b64_tr_b16 a[172:173], v35 offset:32768\n" \
"v_mfma_f32_16x16x32_bf16 v[152:155], a[244:247], a[216:219], v[152:155]\n" \
"ds_read_b64_tr_b16 a[174:175], v35 offset:33792\n" \
"v_mfma_f32_16x16x32_bf16 v[156:159], a[184:187], a[152:155], v[156:159]\n" \
"ds_read_b64_tr_b16 a[236:237], v35 offset:40960\n" \
"v_mfma_f32_16x16x32_bf16 v[156:159], a[248:251], a[216:219], v[156:159]\n" \
"ds_read_b64_tr_b16 a[238:239], v35 offset:41984\n" \
"v_mfma_f32_16x16x32_bf16 v[160:163], a[188:191], a[152:155], v[160:163]\n" \
"v_mfma_f32_16x16x32_bf16 v[160:163], a[252:255], a[216:219], v[160:163]\n" \
"ds_read_b128 a[152:155], v28 offset:36864\n" \
"v_mfma_f32_16x16x32_bf16 v[176:179], a[188:191], a[156:159], v[176:179]\n" \
"ds_read_b128 a[216:219], v29 offset:36864\n" \
"v_mfma_f32_16x16x32_bf16 v[176:179], a[252:255], a[220:223], v[176:179]\n" \
"v_mfma_f32_16x16x32_bf16 v[172:175], a[184:187], a[156:159], v[172:175]\n" \
"v_mfma_f32_16x16x32_bf16 v[172:175], a[248:251], a[220:223], v[172:175]\n" \
"v_mfma_f32_16x16x32_bf16 v[168:171], a[180:183], a[156:159], v[168:171]\n" \
"v_mfma_f32_16x16x32_bf16 v[168:171], a[244:247], a[220:223], v[168:171]\n" \
"v_mfma_f32_16x16x32_bf16 v[164:167], a[176:179], a[156:159], v[164:167]\n" \
"v_mfma_f32_16x16x32_bf16 v[164:167], a[240:243], a[220:223], v[164:167]\n" \
"ds_read_b128 a[156:159], v28 offset:38912\n" \
"ds_read_b128 a[220:223], v29 offset:38912\n" \
"s_waitcnt lgkmcnt(15)\n" \
"v_mfma_f32_16x16x32_bf16 a[0:3], a[160:163], a[128:131], a[0:3]\n" \
"s_addk_i32 s84, 0x2000\n" \
"v_mfma_f32_16x16x32_bf16 a[0:3], a[224:227], a[192:195], a[0:3]\n" \
"buffer_load_dwordx4 v24, s[76:79], s84 offen offset:2048 lds\n" \
"v_mfma_f32_16x16x32_bf16 a[4:7], a[164:167], a[128:131], a[4:7]\n" \
"ds_read_b64_tr_b16 a[176:177], v36 offset:32768\n" \
"v_mfma_f32_16x16x32_bf16 a[4:7], a[228:231], a[192:195], a[4:7]\n" \
"s_addk_i32 s84, 0x2000\n" \
"s_waitcnt lgkmcnt(13)\n" \
"v_mfma_f32_16x16x32_bf16 a[8:11], a[168:171], a[128:131], a[8:11]\n" \
"buffer_load_dwordx4 v25, s[76:79], s84 offen offset:3072 lds\n" \
"s_waitcnt lgkmcnt(9)\n" \
"v_mfma_f32_16x16x32_bf16 a[8:11], a[232:235], a[192:195], a[8:11]\n" \
"ds_read_b64_tr_b16 a[178:179], v36 offset:33792\n" \
"s_waitcnt lgkmcnt(8)\n" \
"v_mfma_f32_16x16x32_bf16 a[12:15], a[172:175], a[128:131], a[12:15]\n" \
"ds_read_b64_tr_b16 a[240:241], v36 offset:40960\n" \
"v_accvgpr_read_b32 v184, a0\n" \
"s_waitcnt lgkmcnt(7)\n" \
"s_nop 0\n" \
"v_mfma_f32_16x16x32_bf16 a[12:15], a[236:239], a[192:195], a[12:15]\n" \
"v_accvgpr_read_b32 v185, a1\n" \
"v_accvgpr_read_b32 v186, a2\n" \
"v_mfma_f32_16x16x32_bf16 a[28:31], a[172:175], a[132:135], a[28:31]\n" \
"v_accvgpr_read_b32 v187, a3\n" \
"v_accvgpr_read_b32 v180, a4\n" \
"v_mfma_f32_16x16x32_bf16 a[28:31], a[236:239], a[196:199], a[28:31]\n" \
"ds_read_b64_tr_b16 a[242:243], v36 offset:41984\n" \
"v_accvgpr_read_b32 v181, a5\n" \
"v_mfma_f32_16x16x32_bf16 a[24:27], a[168:171], a[132:135], a[24:27]\n" \
"v_accvgpr_read_b32 v182, a6\n" \
"v_accvgpr_read_b32 v183, a7\n" \
"v_mfma_f32_16x16x32_bf16 a[24:27], a[232:235], a[196:199], a[24:27]\n" \
"v_cvt_pk_bf16_f32 v184, v184, v185\n" \
"v_cvt_pk_bf16_f32 v185, v186, v187\n" \
"v_mfma_f32_16x16x32_bf16 a[20:23], a[164:167], a[132:135], a[20:23]\n" \
"v_cvt_pk_bf16_f32 v186, v180, v181\n" \
"v_cvt_pk_bf16_f32 v187, v182, v183\n" \
"v_mfma_f32_16x16x32_bf16 a[20:23], a[228:231], a[196:199], a[20:23]\n" \
"ds_read_b64_tr_b16 a[180:181], v37 offset:32768\n" \
"v_accvgpr_read_b32 v188, a8\n" \
"v_mfma_f32_16x16x32_bf16 a[16:19], a[160:163], a[132:135], a[16:19]\n" \
"v_accvgpr_read_b32 v189, a9\n" \
"v_accvgpr_read_b32 v190, a10\n" \
"v_mfma_f32_16x16x32_bf16 a[16:19], a[224:227], a[196:199], a[16:19]\n" \
"v_accvgpr_read_b32 v191, a11\n" \
"v_accvgpr_read_b32 v180, a12\n" \
"v_mfma_f32_16x16x32_bf16 a[32:35], a[160:163], a[136:139], a[32:35]\n" \
"ds_read_b64_tr_b16 a[182:183], v37 offset:33792\n" \
"v_accvgpr_read_b32 v181, a13\n" \
"v_mfma_f32_16x16x32_bf16 a[32:35], a[224:227], a[200:203], a[32:35]\n" \
"v_accvgpr_read_b32 v182, a14\n" \
"v_accvgpr_read_b32 v183, a15\n" \
"v_mfma_f32_16x16x32_bf16 a[36:39], a[164:167], a[136:139], a[36:39]\n" \
"v_lshlrev_b32_e32 v45, 3, v20\n" \
"v_add_u32_e32 v45, s31, v45\n" \
"v_mfma_f32_16x16x32_bf16 a[36:39], a[228:231], a[200:203], a[36:39]\n" \
"s_getpc_b64 s[86:87]\n" \
"s_sub_u32 s86, s86, 0xf800\n" \
"s_subb_u32 s87, s87, 0\n" \
"s_mov_b32 m0, s32\n" \
"s_nop 0\n" \
"v_mfma_f32_16x16x32_bf16 a[40:43], a[168:171], a[136:139], a[40:43]\n" \
"global_load_lds_dword v45, s[86:87]\n" \
"v_mfma_f32_16x16x32_bf16 a[40:43], a[232:235], a[200:203], a[40:43]\n" \
"ds_read_b64_tr_b16 a[244:245], v37 offset:40960\n" \
"v_cvt_pk_bf16_f32 v188, v188, v189\n" \
"v_mfma_f32_16x16x32_bf16 a[44:47], a[172:175], a[136:139], a[44:47]\n" \
"ds_read_b64_tr_b16 a[246:247], v37 offset:41984\n" \
"v_cvt_pk_bf16_f32 v189, v190, v191\n" \
"v_mfma_f32_16x16x32_bf16 a[44:47], a[236:239], a[200:203], a[44:47]\n" \
"v_cvt_pk_bf16_f32 v190, v180, v181\n" \
"v_cvt_pk_bf16_f32 v191, v182, v183\n" \
"v_mfma_f32_16x16x32_bf16 a[60:63], a[172:175], a[140:143], a[60:63]\n" \
"v_accvgpr_read_b32 v192, a24\n" \
"v_accvgpr_read_b32 v193, a25\n" \
"v_mfma_f32_16x16x32_bf16 a[60:63], a[236:239], a[204:207], a[60:63]\n" \
"v_accvgpr_read_b32 v194, a26\n" \
"v_accvgpr_read_b32 v195, a27\n" \
"v_mfma_f32_16x16x32_bf16 a[56:59], a[168:171], a[140:143], a[56:59]\n" \
"ds_read_b64_tr_b16 a[184:185], v38 offset:32768\n" \
"v_accvgpr_read_b32 v180, a28\n" \
"v_mfma_f32_16x16x32_bf16 a[56:59], a[232:235], a[204:207], a[56:59]\n" \
"v_accvgpr_read_b32 v181, a29\n" \
"v_accvgpr_read_b32 v182, a30\n" \
"v_mfma_f32_16x16x32_bf16 a[52:55], a[164:167], a[140:143], a[52:55]\n" \
"v_accvgpr_read_b32 v183, a31\n" \
"v_cvt_pk_bf16_f32 v192, v192, v193\n" \
"v_mfma_f32_16x16x32_bf16 a[52:55], a[228:231], a[204:207], a[52:55]\n" \
"ds_read_b64_tr_b16 a[186:187], v38 offset:33792\n" \
"v_cvt_pk_bf16_f32 v193, v194, v195\n" \
"v_mfma_f32_16x16x32_bf16 a[48:51], a[160:163], a[140:143], a[48:51]\n" \
"v_cvt_pk_bf16_f32 v194, v180, v181\n" \
"v_cvt_pk_bf16_f32 v195, v182, v183\n" \
"v_mfma_f32_16x16x32_bf16 a[48:51], a[224:227], a[204:207], a[48:51]\n" \
"v_accvgpr_read_b32 v196, a16\n" \
"v_accvgpr_read_b32 v197, a17\n" \
"v_mfma_f32_16x16x32_bf16 a[64:67], a[160:163], a[144:147], a[64:67]\n" \
"ds_read_b64_tr_b16 a[248:249], v38 offset:40960\n" \
"v_accvgpr_read_b32 v198, a18\n" \
"v_mfma_f32_16x16x32_bf16 a[64:67], a[224:227], a[208:211], a[64:67]\n" \
"v_accvgpr_read_b32 v199, a19\n" \
"v_accvgpr_read_b32 v180, a20\n" \
"v_mfma_f32_16x16x32_bf16 a[68:71], a[164:167], a[144:147], a[68:71]\n" \
"v_accvgpr_read_b32 v181, a21\n" \
"v_accvgpr_read_b32 v182, a22\n" \
"v_mfma_f32_16x16x32_bf16 a[68:71], a[228:231], a[208:211], a[68:71]\n" \
"ds_read_b64_tr_b16 a[250:251], v38 offset:41984\n" \
"v_accvgpr_read_b32 v183, a23\n" \
"v_mfma_f32_16x16x32_bf16 a[72:75], a[168:171], a[144:147], a[72:75]\n" \
"v_cvt_pk_bf16_f32 v196, v196, v197\n" \
"v_cvt_pk_bf16_f32 v197, v198, v199\n" \
"v_mfma_f32_16x16x32_bf16 a[72:75], a[232:235], a[208:211], a[72:75]\n" \
"v_cvt_pk_bf16_f32 v198, v180, v181\n" \
"v_cvt_pk_bf16_f32 v199, v182, v183\n" \
"v_mfma_f32_16x16x32_bf16 a[76:79], a[172:175], a[144:147], a[76:79]\n" \
"v_accvgpr_read_b32 v200, a32\n" \
"v_accvgpr_read_b32 v201, a33\n" \
"v_mfma_f32_16x16x32_bf16 a[76:79], a[236:239], a[208:211], a[76:79]\n" \
"ds_read_b64_tr_b16 a[188:189], v39 offset:32768\n" \
"v_accvgpr_read_b32 v202, a34\n" \
"v_mfma_f32_16x16x32_bf16 a[92:95], a[172:175], a[148:151], a[92:95]\n" \
"s_add_u32 s89, s41, 0x0\n" \
"s_nop 0\n" \
"buffer_store_dwordx4 v[184:187], v20, s[80:83], s89 offen offset:0 sc1\n" \
"v_mfma_f32_16x16x32_bf16 a[92:95], a[236:239], a[212:215], a[92:95]\n" \
"v_accvgpr_read_b32 v203, a35\n" \
"v_accvgpr_read_b32 v180, a36\n" \
"v_mfma_f32_16x16x32_bf16 a[88:91], a[168:171], a[148:151], a[88:91]\n" \
"ds_read_b64_tr_b16 a[190:191], v39 offset:33792\n" \
"v_accvgpr_read_b32 v181, a37\n" \
"v_mfma_f32_16x16x32_bf16 a[88:91], a[232:235], a[212:215], a[88:91]\n" \
"v_accvgpr_read_b32 v182, a38\n" \
"v_accvgpr_read_b32 v183, a39\n" \
"v_mfma_f32_16x16x32_bf16 a[84:87], a[164:167], a[148:151], a[84:87]\n" \
"v_cvt_pk_bf16_f32 v200, v200, v201\n" \
"v_cvt_pk_bf16_f32 v201, v202, v203\n" \
"v_mfma_f32_16x16x32_bf16 a[84:87], a[228:231], a[212:215], a[84:87]\n" \
"ds_read_b64_tr_b16 a[252:253], v39 offset:40960\n" \
"v_cvt_pk_bf16_f32 v202, v180, v181\n" \
"v_mfma_f32_16x16x32_bf16 a[80:83], a[160:163], a[148:151], a[80:83]\n" \
"s_add_u32 s88, s41, 0x0\n" \
"buffer_store_dwordx4 v[188:191], v20, s[80:83], s88 offen offset:1024 sc1\n" \
"v_mfma_f32_16x16x32_bf16 a[80:83], a[224:227], a[212:215], a[80:83]\n" \
"v_cvt_pk_bf16_f32 v203, v182, v183\n" \
"v_accvgpr_read_b32 v204, a40\n" \
"s_waitcnt lgkmcnt(15)\n" \
"v_mfma_f32_16x16x32_bf16 a[96:99], a[160:163], a[152:155], a[96:99]\n" \
"ds_read_b64_tr_b16 a[254:255], v39 offset:41984\n" \
"v_accvgpr_read_b32 v205, a41\n" \
"v_mfma_f32_16x16x32_bf16 a[96:99], a[224:227], a[216:219], a[96:99]\n" \
"v_accvgpr_read_b32 v206, a42\n" \
"v_accvgpr_read_b32 v207, a43\n" \
"v_mfma_f32_16x16x32_bf16 a[100:103], a[164:167], a[152:155], a[100:103]\n" \
"v_accvgpr_read_b32 v180, a44\n" \
"v_accvgpr_read_b32 v181, a45\n" \
"v_mfma_f32_16x16x32_bf16 a[100:103], a[228:231], a[216:219], a[100:103]\n" \
"v_accvgpr_read_b32 v182, a46\n" \
"v_accvgpr_read_b32 v183, a47\n" \
"v_mfma_f32_16x16x32_bf16 a[104:107], a[168:171], a[152:155], a[104:107]\n" \
"s_add_u32 s89, s41, 0x1000\n" \
"buffer_store_dwordx4 v[192:195], v20, s[80:83], s89 offen offset:1024 sc1\n" \
"v_mfma_f32_16x16x32_bf16 a[104:107], a[232:235], a[216:219], a[104:107]\n" \
"v_cvt_pk_bf16_f32 v204, v204, v205\n" \
"v_cvt_pk_bf16_f32 v205, v206, v207\n" \
"v_mfma_f32_16x16x32_bf16 a[108:111], a[172:175], a[152:155], a[108:111]\n" \
"v_cvt_pk_bf16_f32 v206, v180, v181\n" \
"v_cvt_pk_bf16_f32 v207, v182, v183\n" \
"v_mfma_f32_16x16x32_bf16 a[108:111], a[236:239], a[216:219], a[108:111]\n" \
"v_accvgpr_read_b32 v208, a56\n" \
"v_accvgpr_read_b32 v209, a57\n" \
"v_mfma_f32_16x16x32_bf16 a[124:127], a[172:175], a[156:159], a[124:127]\n" \
"v_accvgpr_read_b32 v210, a58\n" \
"v_accvgpr_read_b32 v211, a59\n" \
"v_mfma_f32_16x16x32_bf16 a[124:127], a[236:239], a[220:223], a[124:127]\n" \
"v_accvgpr_read_b32 v180, a60\n" \
"v_accvgpr_read_b32 v181, a61\n" \
"v_mfma_f32_16x16x32_bf16 a[120:123], a[168:171], a[156:159], a[120:123]\n" \
"s_add_u32 s88, s41, 0x1000\n" \
"buffer_store_dwordx4 v[196:199], v20, s[80:83], s88 offen offset:0 sc1\n" \
"v_mfma_f32_16x16x32_bf16 a[120:123], a[232:235], a[220:223], a[120:123]\n" \
"v_accvgpr_read_b32 v182, a62\n" \
"v_accvgpr_read_b32 v183, a63\n" \
"v_mfma_f32_16x16x32_bf16 a[116:119], a[164:167], a[156:159], a[116:119]\n" \
"v_cvt_pk_bf16_f32 v208, v208, v209\n" \
"v_cvt_pk_bf16_f32 v209, v210, v211\n" \
"v_mfma_f32_16x16x32_bf16 a[116:119], a[228:231], a[220:223], a[116:119]\n" \
"v_cvt_pk_bf16_f32 v210, v180, v181\n" \
"v_cvt_pk_bf16_f32 v211, v182, v183\n" \
"v_mfma_f32_16x16x32_bf16 a[112:115], a[160:163], a[156:159], a[112:115]\n" \
"v_accvgpr_read_b32 v212, a48\n" \
"v_accvgpr_read_b32 v213, a49\n" \
"v_mfma_f32_16x16x32_bf16 a[112:115], a[224:227], a[220:223], a[112:115]\n" \
"v_accvgpr_read_b32 v214, a50\n" \
"v_accvgpr_read_b32 v215, a51\n" \
"s_waitcnt vmcnt(5) lgkmcnt(0)\n" \
"s_barrier\n" \
"v_mov_b32_e32 v36, v32\n" \
"v_xor_b32_e32 v32, 0x0, v48\n" \
"v_mov_b32_e32 v37, v33\n" \
"v_xor_b32_e32 v33, 0x20, v48\n" \
"v_mov_b32_e32 v38, v34\n" \
"v_xor_b32_e32 v34, 0x40, v48\n" \
"v_mov_b32_e64 v39, v35\n" \
"v_xor_b32_e32 v35, 0x60, v48\n" \
"v_add_u32_e32 v22, s44, v28\n" \
"v_add_u32_e32 v28, s43, v28\n" \
"v_add_u32_e32 v23, s44, v29\n" \
"v_add_u32_e32 v29, s43, v29\n" \
"v_mfma_f32_16x16x32_bf16 v[52:55], a[176:179], a[128:131], v[52:55]\n" \
"s_add_u32 m0, s40, 0x8000\n" \
"s_add_u32 s85, s39, 0x2000\n" \
"v_mfma_f32_16x16x32_bf16 v[52:55], a[240:243], a[192:195], v[52:55]\n" \
"buffer_load_dwordx4 v20, s[80:83], s85 offen sc1 lds\n" \
"v_mfma_f32_16x16x32_bf16 v[56:59], a[180:183], a[128:131], v[56:59]\n" \
"ds_read_b64_tr_b16 a[160:161], v32 offset:0\n" \
"v_accvgpr_read_b32 v180, a52\n" \
"v_mfma_f32_16x16x32_bf16 v[56:59], a[244:247], a[192:195], v[56:59]\n" \
"s_add_u32 m0, s40, 0x9000\n" \
"s_add_u32 s85, s39, 0x3000\n" \
"v_mfma_f32_16x16x32_bf16 v[60:63], a[184:187], a[128:131], v[60:63]\n" \
"buffer_load_dwordx4 v20, s[80:83], s85 offen sc1 lds\n" \
"v_mfma_f32_16x16x32_bf16 v[60:63], a[248:251], a[192:195], v[60:63]\n" \
"ds_read_b64_tr_b16 a[162:163], v32 offset:512\n" \
"v_accvgpr_read_b32 v181, a53\n" \
"v_mfma_f32_16x16x32_bf16 v[64:67], a[188:191], a[128:131], v[64:67]\n" \
"ds_read_b64_tr_b16 a[224:225], v32 offset:4096\n" \
"v_accvgpr_read_b32 v182, a54\n" \
"v_mfma_f32_16x16x32_bf16 v[64:67], a[252:255], a[192:195], v[64:67]\n" \
"ds_read_b128 a[128:131], v28 offset:0\n" \
"v_accvgpr_read_b32 v183, a55\n" \
"v_mfma_f32_16x16x32_bf16 v[80:83], a[188:191], a[132:135], v[80:83]\n" \
"ds_read_b128 a[192:195], v29 offset:0\n" \
"v_cvt_pk_bf16_f32 v212, v212, v213\n" \
"v_mfma_f32_16x16x32_bf16 v[80:83], a[252:255], a[196:199], v[80:83]\n" \
"s_add_u32 m0, s40, 0xb000\n" \
"s_movk_i32 s84, 0x80\n" \
"v_mfma_f32_16x16x32_bf16 v[76:79], a[184:187], a[132:135], v[76:79]\n" \
"buffer_load_dwordx4 v21, s[76:79], s84 offen offset:0 lds\n" \
"v_mfma_f32_16x16x32_bf16 v[76:79], a[248:251], a[196:199], v[76:79]\n" \
"s_add_u32 m0, s30, 0x8000\n" \
"s_movk_i32 s84, 0x80\n" \
"v_mfma_f32_16x16x32_bf16 v[72:75], a[180:183], a[132:135], v[72:75]\n" \
"buffer_load_dwordx4 v24, s[76:79], s84 offen offset:0 lds\n" \
"v_mfma_f32_16x16x32_bf16 v[72:75], a[244:247], a[196:199], v[72:75]\n" \
"ds_read_b64_tr_b16 a[226:227], v32 offset:4608\n" \
"v_cvt_pk_bf16_f32 v213, v214, v215\n" \
"v_mfma_f32_16x16x32_bf16 v[68:71], a[176:179], a[132:135], v[68:71]\n" \
"ds_read_b64_tr_b16 a[164:165], v33 offset:0\n" \
"v_cvt_pk_bf16_f32 v214, v180, v181\n" \
"v_mfma_f32_16x16x32_bf16 v[68:71], a[240:243], a[196:199], v[68:71]\n" \
"ds_read_b128 a[132:135], v28 offset:2048\n" \
"v_cvt_pk_bf16_f32 v215, v182, v183\n" \
"v_mfma_f32_16x16x32_bf16 v[84:87], a[176:179], a[136:139], v[84:87]\n" \
"ds_read_b128 a[196:199], v29 offset:2048\n" \
"v_accvgpr_read_b32 v216, a64\n" \
"v_mfma_f32_16x16x32_bf16 v[84:87], a[240:243], a[200:203], v[84:87]\n" \
"s_movk_i32 s84, 0x2080\n" \
"v_mfma_f32_16x16x32_bf16 v[88:91], a[180:183], a[136:139], v[88:91]\n" \
"buffer_load_dwordx4 v25, s[76:79], s84 offen offset:1024 lds\n" \
"v_mfma_f32_16x16x32_bf16 v[88:91], a[244:247], a[200:203], v[88:91]\n" \
"s_movk_i32 s84, 0x4080\n" \
"v_mfma_f32_16x16x32_bf16 v[92:95], a[184:187], a[136:139], v[92:95]\n" \
"buffer_load_dwordx4 v24, s[76:79], s84 offen offset:2048 lds\n" \
"v_mfma_f32_16x16x32_bf16 v[92:95], a[248:251], a[200:203], v[92:95]\n" \
"ds_read_b64_tr_b16 a[166:167], v33 offset:512\n" \
"v_accvgpr_read_b32 v217, a65\n" \
"v_mfma_f32_16x16x32_bf16 v[96:99], a[188:191], a[136:139], v[96:99]\n" \
"ds_read_b64_tr_b16 a[228:229], v33 offset:4096\n" \
"v_accvgpr_read_b32 v218, a66\n" \
"v_mfma_f32_16x16x32_bf16 v[96:99], a[252:255], a[200:203], v[96:99]\n" \
"ds_read_b128 a[136:139], v28 offset:4096\n" \
"v_accvgpr_read_b32 v219, a67\n" \
"v_mfma_f32_16x16x32_bf16 v[112:115], a[188:191], a[140:143], v[112:115]\n" \
"ds_read_b128 a[200:203], v29 offset:4096\n" \
"v_accvgpr_read_b32 v180, a68\n" \
"v_mfma_f32_16x16x32_bf16 v[112:115], a[252:255], a[204:207], v[112:115]\n" \
"s_movk_i32 s84, 0x6080\n" \
"v_mfma_f32_16x16x32_bf16 v[108:111], a[184:187], a[140:143], v[108:111]\n" \
"buffer_load_dwordx4 v25, s[76:79], s84 offen offset:3072 lds\n" \
"v_mfma_f32_16x16x32_bf16 v[108:111], a[248:251], a[204:207], v[108:111]\n" \
"s_add_u32 m0, s31, 0x8000\n" \
"s_mov_b32 s84, 0x80000\n" \
"v_mfma_f32_16x16x32_bf16 v[104:107], a[180:183], a[140:143], v[104:107]\n" \
"buffer_load_dwordx4 v26, s[68:71], s84 offen offset:0 lds\n" \
"v_mfma_f32_16x16x32_bf16 v[104:107], a[244:247], a[204:207], v[104:107]\n" \
"s_addk_i32 s84, 0x7c00\n" \
"v_mfma_f32_16x16x32_bf16 v[100:103], a[176:179], a[140:143], v[100:103]\n" \
"buffer_load_dwordx4 v26, s[68:71], s84 offen offset:1024 lds\n" \
"v_mfma_f32_16x16x32_bf16 v[100:103], a[240:243], a[204:207], v[100:103]\n" \
"ds_read_b128 a[140:143], v28 offset:6144\n" \
"v_accvgpr_read_b32 v181, a69\n" \
"v_mfma_f32_16x16x32_bf16 v[116:119], a[176:179], a[144:147], v[116:119]\n" \
"ds_read_b128 a[204:207], v29 offset:6144\n" \
"v_accvgpr_read_b32 v182, a70\n" \
"v_mfma_f32_16x16x32_bf16 v[116:119], a[240:243], a[208:211], v[116:119]\n" \
"s_addk_i32 s84, 0x7c00\n" \
"v_mfma_f32_16x16x32_bf16 v[120:123], a[180:183], a[144:147], v[120:123]\n" \
"buffer_load_dwordx4 v27, s[68:71], s84 offen offset:2048 lds\n" \
"v_mfma_f32_16x16x32_bf16 v[120:123], a[244:247], a[208:211], v[120:123]\n" \
"ds_read_b64_tr_b16 a[230:231], v33 offset:4608\n" \
"v_accvgpr_read_b32 v183, a71\n" \
"v_mfma_f32_16x16x32_bf16 v[124:127], a[184:187], a[144:147], v[124:127]\n" \
"s_addk_i32 s84, 0x7c00\n" \
"v_mfma_f32_16x16x32_bf16 v[124:127], a[248:251], a[208:211], v[124:127]\n" \
"buffer_load_dwordx4 v27, s[68:71], s84 offen offset:3072 lds\n" \
"v_mfma_f32_16x16x32_bf16 v[128:131], a[188:191], a[144:147], v[128:131]\n" \
"ds_read_b64_tr_b16 a[172:173], v35 offset:0\n" \
"v_cvt_pk_bf16_f32 v216, v216, v217\n" \
"v_mfma_f32_16x16x32_bf16 v[128:131], a[252:255], a[208:211], v[128:131]\n" \
"ds_read_b128 a[144:147], v28 offset:8192\n" \
"v_cvt_pk_bf16_f32 v217, v218, v219\n" \
"v_mfma_f32_16x16x32_bf16 v[144:147], a[188:191], a[148:151], v[144:147]\n" \
"ds_read_b128 a[208:211], v29 offset:8192\n" \
"v_cvt_pk_bf16_f32 v218, v180, v181\n" \
"v_mfma_f32_16x16x32_bf16 v[144:147], a[252:255], a[212:215], v[144:147]\n" \
"s_add_u32 m0, s30, 0x9000\n" \
"s_mov_b32 s84, 0x9080\n" \
"v_mfma_f32_16x16x32_bf16 v[140:143], a[184:187], a[148:151], v[140:143]\n" \
"buffer_load_dwordx4 v24, s[76:79], s84 offen offset:0 lds\n" \
"v_mfma_f32_16x16x32_bf16 v[140:143], a[248:251], a[212:215], v[140:143]\n" \
"ds_read_b64_tr_b16 a[174:175], v35 offset:512\n" \
"v_cvt_pk_bf16_f32 v219, v182, v183\n" \
"v_mfma_f32_16x16x32_bf16 v[136:139], a[180:183], a[148:151], v[136:139]\n" \
"s_addk_i32 s84, 0x2000\n" \
"s_nop 0\n" \
"v_mfma_f32_16x16x32_bf16 v[136:139], a[244:247], a[212:215], v[136:139]\n" \
"buffer_load_dwordx4 v25, s[76:79], s84 offen offset:1024 lds\n" \
"v_mfma_f32_16x16x32_bf16 v[132:135], a[176:179], a[148:151], v[132:135]\n" \
"ds_read_b64_tr_b16 a[236:237], v35 offset:4096\n" \
"v_accvgpr_read_b32 v220, a80\n" \
"v_mfma_f32_16x16x32_bf16 v[132:135], a[240:243], a[212:215], v[132:135]\n" \
"ds_read_b128 a[148:151], v28 offset:10240\n" \
"v_accvgpr_read_b32 v221, a81\n" \
"v_mfma_f32_16x16x32_bf16 v[148:151], a[176:179], a[152:155], v[148:151]\n" \
"ds_read_b128 a[212:215], v29 offset:10240\n" \
"v_accvgpr_read_b32 v222, a82\n" \
"v_mfma_f32_16x16x32_bf16 v[148:151], a[240:243], a[216:219], v[148:151]\n" \
"ds_read_b64_tr_b16 a[238:239], v35 offset:4608\n" \
"v_accvgpr_read_b32 v223, a83\n" \
"v_mfma_f32_16x16x32_bf16 v[152:155], a[180:183], a[152:155], v[152:155]\n" \
"ds_read_b64_tr_b16 a[168:169], v34 offset:0\n" \
"v_accvgpr_read_b32 v180, a84\n" \
"v_mfma_f32_16x16x32_bf16 v[152:155], a[244:247], a[216:219], v[152:155]\n" \
"ds_read_b64_tr_b16 a[170:171], v34 offset:512\n" \
"v_accvgpr_read_b32 v181, a85\n" \
"v_mfma_f32_16x16x32_bf16 v[156:159], a[184:187], a[152:155], v[156:159]\n" \
"ds_read_b64_tr_b16 a[232:233], v34 offset:4096\n" \
"v_accvgpr_read_b32 v182, a86\n" \
"v_mfma_f32_16x16x32_bf16 v[156:159], a[248:251], a[216:219], v[156:159]\n" \
"ds_read_b64_tr_b16 a[234:235], v34 offset:4608\n" \
"v_accvgpr_read_b32 v183, a87\n" \
"v_mfma_f32_16x16x32_bf16 v[160:163], a[188:191], a[152:155], v[160:163]\n" \
"ds_read_b128 v[248:251], v22 offset:0\n" \
"v_cvt_pk_bf16_f32 v220, v220, v221\n" \
"v_mfma_f32_16x16x32_bf16 v[160:163], a[252:255], a[216:219], v[160:163]\n" \
"ds_read_b128 a[152:155], v28 offset:12288\n" \
"v_cvt_pk_bf16_f32 v221, v222, v223\n" \
"v_mfma_f32_16x16x32_bf16 v[176:179], a[188:191], a[156:159], v[176:179]\n" \
"ds_read_b128 a[216:219], v29 offset:12288\n" \
"v_cvt_pk_bf16_f32 v222, v180, v181\n" \
"v_mfma_f32_16x16x32_bf16 v[176:179], a[252:255], a[220:223], v[176:179]\n" \
"ds_read_b128 v[48:51], v23 offset:0\n" \
"v_cvt_pk_bf16_f32 v223, v182, v183\n" \
"v_mfma_f32_16x16x32_bf16 v[172:175], a[184:187], a[156:159], v[172:175]\n" \
"v_cvt_pk_bf16_f32 v224, v52, v53\n" \
"v_cvt_pk_bf16_f32 v225, v54, v55\n" \
"v_mfma_f32_16x16x32_bf16 v[172:175], a[248:251], a[220:223], v[172:175]\n" \
"v_cvt_pk_bf16_f32 v226, v56, v57\n" \
"v_cvt_pk_bf16_f32 v227, v58, v59\n" \
"v_mfma_f32_16x16x32_bf16 v[168:171], a[180:183], a[156:159], v[168:171]\n" \
"v_cvt_pk_bf16_f32 v228, v60, v61\n" \
"v_cvt_pk_bf16_f32 v229, v62, v63\n" \
"v_mfma_f32_16x16x32_bf16 v[168:171], a[244:247], a[220:223], v[168:171]\n" \
"v_cvt_pk_bf16_f32 v230, v64, v65\n" \
"v_cvt_pk_bf16_f32 v231, v66, v67\n" \
"v_mfma_f32_16x16x32_bf16 v[164:167], a[176:179], a[156:159], v[164:167]\n" \
"v_cvt_pk_bf16_f32 v232, v76, v77\n" \
"v_cvt_pk_bf16_f32 v233, v78, v79\n" \
"v_mfma_f32_16x16x32_bf16 v[164:167], a[240:243], a[220:223], v[164:167]\n" \
"ds_read_b128 a[156:159], v28 offset:14336\n" \
"ds_read_b128 a[220:223], v29 offset:14336\n" \
"s_waitcnt lgkmcnt(15)\n" \
"v_mfma_f32_16x16x32_bf16 a[0:3], a[160:163], a[128:131], 0\n" \
"s_addk_i32 s84, 0x2000\n" \
"v_cvt_pk_bf16_f32 v234, v80, v81\n" \
"v_mfma_f32_16x16x32_bf16 a[0:3], a[224:227], a[192:195], a[0:3]\n" \
"buffer_load_dwordx4 v24, s[76:79], s84 offen offset:2048 lds\n" \
"v_cvt_pk_bf16_f32 v235, v82, v83\n" \
"v_mfma_f32_16x16x32_bf16 a[4:7], a[164:167], a[128:131], 0\n" \
"ds_read_b64_tr_b16 a[176:177], v36 offset:0\n" \
"v_cvt_pk_bf16_f32 v236, v68, v69\n" \
"v_mfma_f32_16x16x32_bf16 a[4:7], a[228:231], a[192:195], a[4:7]\n" \
"s_addk_i32 s84, 0x2000\n" \
"v_cvt_pk_bf16_f32 v237, v70, v71\n" \
"s_waitcnt lgkmcnt(15)\n" \
"v_mfma_f32_16x16x32_bf16 a[12:15], a[172:175], a[128:131], 0\n" \
"buffer_load_dwordx4 v25, s[76:79], s84 offen offset:3072 lds\n" \
"v_cvt_pk_bf16_f32 v238, v72, v73\n" \
"s_waitcnt lgkmcnt(11)\n" \
"v_mfma_f32_16x16x32_bf16 a[12:15], a[236:239], a[192:195], a[12:15]\n" \
"ds_read_b64_tr_b16 a[178:179], v36 offset:1024\n" \
"v_cvt_pk_bf16_f32 v239, v74, v75\n" \
"s_waitcnt lgkmcnt(10)\n" \
"v_mfma_f32_16x16x32_bf16 a[8:11], a[128:131], a[168:171], 0\n" \
"ds_read_b64_tr_b16 a[240:241], v36 offset:8192\n" \
"v_cvt_pk_bf16_f32 v240, v84, v85\n" \
"s_waitcnt lgkmcnt(9)\n" \
"s_nop 0\n" \
"v_mfma_f32_16x16x32_bf16 a[8:11], a[192:195], a[232:235], a[8:11]\n" \
"ds_read_b64_tr_b16 a[242:243], v36 offset:9216\n" \
"v_cvt_pk_bf16_f32 v241, v86, v87\n" \
"v_mfma_f32_16x16x32_bf16 a[24:27], a[132:135], a[168:171], 0\n" \
"ds_read_b64_tr_b16 a[180:181], v37 offset:0\n" \
"v_cvt_pk_bf16_f32 v242, v88, v89\n" \
"v_mfma_f32_16x16x32_bf16 a[24:27], a[196:199], a[232:235], a[24:27]\n" \
"ds_read_b64_tr_b16 a[182:183], v37 offset:1024\n" \
"v_cvt_pk_bf16_f32 v243, v90, v91\n" \
"v_mfma_f32_16x16x32_bf16 a[28:31], a[172:175], a[132:135], 0\n" \
"ds_read_b64_tr_b16 a[244:245], v37 offset:8192\n" \
"v_cvt_pk_bf16_f32 v244, v92, v93\n" \
"v_mfma_f32_16x16x32_bf16 a[28:31], a[236:239], a[196:199], a[28:31]\n" \
"s_add_u32 s89, s41, 0x2000\n" \
"buffer_store_dwordx4 v[200:203], v20, s[80:83], s89 offen offset:0 sc1\n" \
"v_mfma_f32_16x16x32_bf16 a[20:23], a[164:167], a[132:135], 0\n" \
"ds_read_b64_tr_b16 a[246:247], v37 offset:9216\n" \
"v_cvt_pk_bf16_f32 v245, v94, v95\n" \
"v_mfma_f32_16x16x32_bf16 a[20:23], a[228:231], a[196:199], a[20:23]\n" \
"ds_read_b64_tr_b16 a[184:185], v38 offset:0\n" \
"v_cvt_pk_bf16_f32 v246, v96, v97\n" \
"v_mfma_f32_16x16x32_bf16 a[16:19], a[160:163], a[132:135], 0\n" \
"ds_read_b64_tr_b16 a[186:187], v38 offset:1024\n" \
"v_cvt_pk_bf16_f32 v247, v98, v99\n" \
"v_mfma_f32_16x16x32_bf16 a[16:19], a[224:227], a[196:199], a[16:19]\n" \
"v_cvt_pk_bf16_f32 v184, v108, v109\n" \
"v_cvt_pk_bf16_f32 v185, v110, v111\n" \
"v_cvt_pk_bf16_f32 v186, v112, v113\n" \
"v_mfma_f32_16x16x32_bf16 a[32:35], a[160:163], a[136:139], 0\n" \
"s_add_u32 s88, s41, 0x2000\n" \
"buffer_store_dwordx4 v[204:207], v20, s[80:83], s88 offen offset:1024 sc1\n" \
"v_mfma_f32_16x16x32_bf16 a[32:35], a[224:227], a[200:203], a[32:35]\n" \
"ds_read_b64_tr_b16 a[248:249], v38 offset:8192\n" \
"v_cvt_pk_bf16_f32 v187, v114, v115\n" \
"v_mfma_f32_16x16x32_bf16 a[36:39], a[164:167], a[136:139], 0\n" \
"ds_read_b64_tr_b16 a[250:251], v38 offset:9216\n" \
"v_cvt_pk_bf16_f32 v188, v100, v101\n" \
"v_mfma_f32_16x16x32_bf16 a[36:39], a[228:231], a[200:203], a[36:39]\n" \
"ds_read_b64_tr_b16 a[188:189], v39 offset:0\n" \
"v_cvt_pk_bf16_f32 v189, v102, v103\n" \
"v_mfma_f32_16x16x32_bf16 a[44:47], a[172:175], a[136:139], 0\n" \
"v_cvt_pk_bf16_f32 v190, v104, v105\n" \
"v_cvt_pk_bf16_f32 v191, v106, v107\n" \
"v_cvt_pk_bf16_f32 v192, v116, v117\n" \
"v_mfma_f32_16x16x32_bf16 a[44:47], a[236:239], a[200:203], a[44:47]\n" \
"ds_read_b64_tr_b16 a[190:191], v39 offset:1024\n" \
"v_cvt_pk_bf16_f32 v193, v118, v119\n" \
"v_mfma_f32_16x16x32_bf16 a[40:43], a[136:139], a[168:171], 0\n" \
"s_add_u32 s89, s41, 0x3000\n" \
"buffer_store_dwordx4 v[208:211], v20, s[80:83], s89 offen offset:1024 sc1\n" \
"v_mfma_f32_16x16x32_bf16 a[40:43], a[200:203], a[232:235], a[40:43]\n" \
"ds_read_b64_tr_b16 a[252:253], v39 offset:8192\n" \
"v_cvt_pk_bf16_f32 v194, v120, v121\n" \
"v_mfma_f32_16x16x32_bf16 a[56:59], a[140:143], a[168:171], 0\n" \
"ds_read_b64_tr_b16 a[254:255], v39 offset:9216\n" \
"v_cvt_pk_bf16_f32 v195, v122, v123\n" \
"v_mfma_f32_16x16x32_bf16 a[56:59], a[204:207], a[232:235], a[56:59]\n" \
"v_cvt_pk_bf16_f32 v196, v124, v125\n" \
"v_cvt_pk_bf16_f32 v197, v126, v127\n" \
"v_cvt_pk_bf16_f32 v198, v128, v129\n" \
"v_mfma_f32_16x16x32_bf16 a[60:63], a[172:175], a[140:143], 0\n" \
"v_cvt_pk_bf16_f32 v199, v130, v131\n" \
"v_cvt_pk_bf16_f32 v252, v140, v141\n" \
".fill (L_pb + 20192 - .) / 4, 4, 0xBF800000\n" \
"L_pp5:\n" \
"s_cbranch_execz L_pret\n" \
"s_nop 0\n" \
"v_cvt_pk_bf16_f32 v253, v142, v143\n" \
"v_mfma_f32_16x16x32_bf16 a[60:63], a[236:239], a[204:207], a[60:63]\n" \
"s_add_u32 s88, s41, 0x3000\n" \
"buffer_store_dwordx4 v[212:215], v20, s[80:83], s88 offen offset:0 sc1\n" \
"v_mfma_f32_16x16x32_bf16 a[52:55], a[164:167], a[140:143], 0\n" \
"v_cvt_pk_bf16_f32 v254, v144, v145\n" \
"v_cvt_pk_bf16_f32 v255, v146, v147\n" \
"v_accvgpr_write_b32 a84, v252\n" \
"v_mfma_f32_16x16x32_bf16 a[52:55], a[228:231], a[204:207], a[52:55]\n" \
"v_accvgpr_write_b32 a85, v253\n" \
"v_accvgpr_write_b32 a86, v254\n" \
"v_accvgpr_write_b32 a87, v255\n" \
"v_mfma_f32_16x16x32_bf16 a[48:51], a[160:163], a[140:143], 0\n" \
"v_cvt_pk_bf16_f32 v252, v132, v133\n" \
"v_cvt_pk_bf16_f32 v253, v134, v135\n" \
"v_cvt_pk_bf16_f32 v254, v136, v137\n" \
"v_mfma_f32_16x16x32_bf16 a[48:51], a[224:227], a[204:207], a[48:51]\n" \
"v_cvt_pk_bf16_f32 v255, v138, v139\n" \
"s_waitcnt lgkmcnt(15)\n" \
"v_mfma_f32_16x16x32_bf16 a[64:67], a[164:167], v[248:251], 0\n" \
"v_mfma_f32_16x16x32_bf16 a[64:67], a[228:231], v[48:51], a[64:67]\n" \
"v_mfma_f32_16x16x32_bf16 a[80:83], a[172:175], v[248:251], 0\n" \
"v_mfma_f32_16x16x32_bf16 a[80:83], a[236:239], v[48:51], a[80:83]\n" \
"v_mfma_f32_16x16x32_bf16 a[68:71], v[248:251], a[168:171], 0\n" \
"v_mfma_f32_16x16x32_bf16 a[68:71], v[48:51], a[232:235], a[68:71]\n" \
"s_waitcnt vmcnt(4) lgkmcnt(0)\n" \
"s_barrier\n" \
"v_mfma_f32_16x16x32_bf16 v[52:55], a[176:179], a[128:131], 0\n" \
"s_add_u32 m0, s40, 0x0\n" \
"s_add_u32 s85, s39, 0x4000\n" \
"v_cvt_pk_bf16_f32 v200, v148, v149\n" \
"v_mfma_f32_16x16x32_bf16 v[52:55], a[240:243], a[192:195], v[52:55]\n" \
"buffer_load_dwordx4 v20, s[80:83], s85 offen sc1 lds\n" \
"v_cvt_pk_bf16_f32 v201, v150, v151\n" \
"v_mfma_f32_16x16x32_bf16 v[56:59], a[180:183], a[128:131], 0\n" \
"ds_read_b64_tr_b16 a[160:161], v32 offset:32768\n" \
"v_cvt_pk_bf16_f32 v202, v152, v153\n" \
"v_mfma_f32_16x16x32_bf16 v[56:59], a[244:247], a[192:195], v[56:59]\n" \
"ds_read_b64_tr_b16 a[162:163], v32 offset:33280\n" \
"v_cvt_pk_bf16_f32 v203, v154, v155\n" \
"v_mfma_f32_16x16x32_bf16 v[60:63], a[184:187], a[128:131], 0\n" \
"s_add_u32 m0, s40, 0x1000\n" \
"s_add_u32 s85, s39, 0x5000\n" \
"v_cvt_pk_bf16_f32 v204, v156, v157\n" \
"v_mfma_f32_16x16x32_bf16 v[60:63], a[248:251], a[192:195], v[60:63]\n" \
"buffer_load_dwordx4 v20, s[80:83], s85 offen sc1 lds\n" \
"v_cvt_pk_bf16_f32 v205, v158, v159\n" \
"v_mfma_f32_16x16x32_bf16 v[64:67], a[188:191], a[128:131], 0\n" \
"ds_read_b64_tr_b16 a[224:225], v32 offset:36864\n" \
"v_cvt_pk_bf16_f32 v206, v160, v161\n" \
"v_mfma_f32_16x16x32_bf16 v[64:67], a[252:255], a[192:195], v[64:67]\n" \
"ds_read_b128 a[128:131], v28 offset:32768\n" \
"v_cvt_pk_bf16_f32 v207, v162, v163\n" \
"v_mfma_f32_16x16x32_bf16 v[80:83], a[188:191], a[132:135], 0\n" \
"ds_read_b128 a[192:195], v29 offset:32768\n" \
"v_cvt_pk_bf16_f32 v208, v172, v173\n" \
"v_mfma_f32_16x16x32_bf16 v[80:83], a[252:255], a[196:199], v[80:83]\n" \
"s_add_u32 m0, s40, 0x3000\n" \
"s_movk_i32 s84, 0x100\n" \
"v_cvt_pk_bf16_f32 v209, v174, v175\n" \
"v_mfma_f32_16x16x32_bf16 v[76:79], a[184:187], a[132:135], 0\n" \
"buffer_load_dwordx4 v21, s[76:79], s84 offen offset:0 lds\n" \
"v_cvt_pk_bf16_f32 v210, v176, v177\n" \
"v_mfma_f32_16x16x32_bf16 v[76:79], a[248:251], a[196:199], v[76:79]\n" \
"ds_read_b64_tr_b16 a[226:227], v32 offset:37376\n" \
"v_cvt_pk_bf16_f32 v211, v178, v179\n" \
"v_mfma_f32_16x16x32_bf16 v[72:75], a[180:183], a[132:135], 0\n" \
"s_add_u32 m0, s30, 0x0\n" \
"s_movk_i32 s84, 0x100\n" \
"s_nop 0\n" \
"v_cvt_pk_bf16_f32 v212, v164, v165\n" \
"v_mfma_f32_16x16x32_bf16 v[72:75], a[244:247], a[196:199], v[72:75]\n" \
"buffer_load_dwordx4 v24, s[76:79], s84 offen offset:0 lds\n" \
"v_cvt_pk_bf16_f32 v213, v166, v167\n" \
"v_mfma_f32_16x16x32_bf16 v[68:71], a[176:179], a[132:135], 0\n" \
"ds_read_b64_tr_b16 a[164:165], v33 offset:32768\n" \
"v_cvt_pk_bf16_f32 v214, v168, v169\n" \
"v_mfma_f32_16x16x32_bf16 v[68:71], a[240:243], a[196:199], v[68:71]\n" \
"ds_read_b128 a[132:135], v28 offset:34816\n" \
"v_cvt_pk_bf16_f32 v215, v170, v171\n" \
"v_mfma_f32_16x16x32_bf16 v[84:87], a[176:179], a[136:139], 0\n" \
"ds_read_b128 a[196:199], v29 offset:34816\n" \
"v_mfma_f32_16x16x32_bf16 v[84:87], a[240:243], a[200:203], v[84:87]\n" \
"s_movk_i32 s84, 0x2100\n" \
"v_mfma_f32_16x16x32_bf16 v[88:91], a[180:183], a[136:139], 0\n" \
"buffer_load_dwordx4 v25, s[76:79], s84 offen offset:1024 lds\n" \
"v_mfma_f32_16x16x32_bf16 v[88:91], a[244:247], a[200:203], v[88:91]\n" \
"ds_read_b64_tr_b16 a[166:167], v33 offset:33280\n" \
"v_mfma_f32_16x16x32_bf16 v[92:95], a[184:187], a[136:139], 0\n" \
"ds_read_b64_tr_b16 a[228:229], v33 offset:36864\n" \
"v_mfma_f32_16x16x32_bf16 v[92:95], a[248:251], a[200:203], v[92:95]\n" \
"s_movk_i32 s84, 0x4100\n" \
"v_mfma_f32_16x16x32_bf16 v[96:99], a[188:191], a[136:139], 0\n" \
"buffer_load_dwordx4 v24, s[76:79], s84 offen offset:2048 lds\n" \
"v_mfma_f32_16x16x32_bf16 v[96:99], a[252:255], a[200:203], v[96:99]\n" \
"ds_read_b128 a[136:139], v28 offset:36864\n" \
"v_mfma_f32_16x16x32_bf16 v[112:115], a[188:191], a[140:143], 0\n" \
"ds_read_b128 a[200:203], v29 offset:36864\n" \
"v_mfma_f32_16x16x32_bf16 v[112:115], a[252:255], a[204:207], v[112:115]\n" \
"s_movk_i32 s84, 0x6100\n" \
"s_nop 0\n" \
"v_mfma_f32_16x16x32_bf16 v[108:111], a[184:187], a[140:143], 0\n" \
"buffer_load_dwordx4 v25, s[76:79], s84 offen offset:3072 lds\n" \
"v_mfma_f32_16x16x32_bf16 v[108:111], a[248:251], a[204:207], v[108:111]\n" \
"ds_read_b64_tr_b16 a[230:231], v33 offset:37376\n" \
"v_mfma_f32_16x16x32_bf16 v[104:107], a[180:183], a[140:143], 0\n" \
"ds_read_b64_tr_b16 a[172:173], v35 offset:32768\n" \
"v_mfma_f32_16x16x32_bf16 v[104:107], a[244:247], a[204:207], v[104:107]\n" \
"ds_read_b64_tr_b16 a[174:175], v35 offset:33280\n" \
"v_mfma_f32_16x16x32_bf16 v[100:103], a[176:179], a[140:143], 0\n" \
"ds_read_b64_tr_b16 a[236:237], v35 offset:36864\n" \
"v_mfma_f32_16x16x32_bf16 v[100:103], a[240:243], a[204:207], v[100:103]\n" \
"ds_read_b128 a[140:143], v28 offset:38912\n" \
"v_mfma_f32_16x16x32_bf16 v[116:119], a[176:179], a[144:147], 0\n" \
"ds_read_b128 a[204:207], v29 offset:38912\n" \
"v_mfma_f32_16x16x32_bf16 v[116:119], a[240:243], a[208:211], v[116:119]\n" \
"s_add_u32 m0, s31, 0x0\n" \
"s_mov_b32 s84, 0x100000\n" \
"v_mfma_f32_16x16x32_bf16 v[120:123], a[180:183], a[144:147], 0\n" \
"buffer_load_dwordx4 v26, s[68:71], s84 offen offset:0 lds\n" \
"v_mfma_f32_16x16x32_bf16 v[120:123], a[244:247], a[208:211], v[120:123]\n" \
"s_addk_i32 s84, 0x7c00\n" \
"v_mfma_f32_16x16x32_bf16 v[124:127], a[184:187], a[144:147], 0\n" \
"buffer_load_dwordx4 v26, s[68:71], s84 offen offset:1024 lds\n" \
"v_mfma_f32_16x16x32_bf16 v[124:127], a[248:251], a[208:211], v[124:127]\n" \
"ds_read_b64_tr_b16 a[238:239], v35 offset:37376\n" \
"v_mfma_f32_16x16x32_bf16 v[128:131], a[188:191], a[144:147], 0\n" \
"ds_read_b64_tr_b16 a[168:169], v34 offset:32768\n" \
"v_mfma_f32_16x16x32_bf16 v[128:131], a[252:255], a[208:211], v[128:131]\n" \
"ds_read_b128 a[144:147], v28 offset:40960\n" \
"v_mfma_f32_16x16x32_bf16 v[144:147], a[188:191], a[148:151], 0\n" \
"ds_read_b128 a[208:211], v29 offset:40960\n" \
"v_mfma_f32_16x16x32_bf16 v[144:147], a[252:255], a[212:215], v[144:147]\n" \
"s_addk_i32 s84, 0x7c00\n" \
"v_mfma_f32_16x16x32_bf16 v[140:143], a[184:187], a[148:151], 0\n" \
"buffer_load_dwordx4 v27, s[68:71], s84 offen offset:2048 lds\n" \
"v_mfma_f32_16x16x32_bf16 v[140:143], a[248:251], a[212:215], v[140:143]\n" \
"s_addk_i32 s84, 0x7c00\n" \
"v_mfma_f32_16x16x32_bf16 v[136:139], a[180:183], a[148:151], 0\n" \
"buffer_load_dwordx4 v27, s[68:71], s84 offen offset:3072 lds\n" \
"v_mfma_f32_16x16x32_bf16 v[136:139], a[244:247], a[212:215], v[136:139]\n" \
"ds_read_b64_tr_b16 a[170:171], v34 offset:33280\n" \
"v_mfma_f32_16x16x32_bf16 v[132:135], a[176:179], a[148:151], 0\n" \
"ds_read_b64_tr_b16 a[232:233], v34 offset:36864\n" \
"v_mfma_f32_16x16x32_bf16 v[132:135], a[240:243], a[212:215], v[132:135]\n" \
"ds_read_b128 a[148:151], v28 offset:43008\n" \
"v_mfma_f32_16x16x32_bf16 v[148:151], a[176:179], a[152:155], 0\n" \
"ds_read_b128 a[212:215], v29 offset:43008\n" \
"v_mfma_f32_16x16x32_bf16 v[148:151], a[240:243], a[216:219], v[148:151]\n" \
"s_add_u32 m0, s30, 0x1000\n" \
"s_mov_b32 s84, 0x9100\n" \
"v_mfma_f32_16x16x32_bf16 v[152:155], a[180:183], a[152:155], 0\n" \
"buffer_load_dwordx4 v24, s[76:79], s84 offen offset:0 lds\n" \
"v_mfma_f32_16x16x32_bf16 v[152:155], a[244:247], a[216:219], v[152:155]\n" \
"s_addk_i32 s84, 0x2000\n" \
"s_nop 0\n" \
"v_mfma_f32_16x16x32_bf16 v[156:159], a[184:187], a[152:155], 0\n" \
"buffer_load_dwordx4 v25, s[76:79], s84 offen offset:1024 lds\n" \
"v_mfma_f32_16x16x32_bf16 v[156:159], a[248:251], a[216:219], v[156:159]\n" \
"ds_read_b64_tr_b16 a[234:235], v34 offset:37376\n" \
"v_mfma_f32_16x16x32_bf16 v[160:163], a[188:191], a[152:155], 0\n" \
"ds_read_b128 v[248:251], v22 offset:32768\n" \
"v_mfma_f32_16x16x32_bf16 v[160:163], a[252:255], a[216:219], v[160:163]\n" \
"ds_read_b128 a[152:155], v28 offset:45056\n" \
"v_mfma_f32_16x16x32_bf16 v[176:179], a[188:191], a[156:159], 0\n" \
"ds_read_b128 a[216:219], v29 offset:45056\n" \
"v_mfma_f32_16x16x32_bf16 v[176:179], a[252:255], a[220:223], v[176:179]\n" \
"ds_read_b128 v[48:51], v23 offset:32768\n" \
"v_mfma_f32_16x16x32_bf16 v[172:175], a[184:187], a[156:159], 0\n" \
"v_mfma_f32_16x16x32_bf16 v[172:175], a[248:251], a[220:223], v[172:175]\n" \
"v_mfma_f32_16x16x32_bf16 v[168:171], a[180:183], a[156:159], 0\n" \
"v_mfma_f32_16x16x32_bf16 v[168:171], a[244:247], a[220:223], v[168:171]\n" \
"v_mfma_f32_16x16x32_bf16 v[164:167], a[176:179], a[156:159], 0\n" \
"v_mfma_f32_16x16x32_bf16 v[164:167], a[240:243], a[220:223], v[164:167]\n" \
"ds_read_b128 a[156:159], v28 offset:47104\n" \
"ds_read_b128 a[220:223], v29 offset:47104\n" \
"s_waitcnt lgkmcnt(15)\n" \
"v_mfma_f32_16x16x32_bf16 a[0:3], a[160:163], a[128:131], a[0:3]\n" \
"s_addk_i32 s84, 0x2000\n" \
"v_mfma_f32_16x16x32_bf16 a[0:3], a[224:227], a[192:195], a[0:3]\n" \
"buffer_load_dwordx4 v24, s[76:79], s84 offen offset:2048 lds\n" \
"v_mfma_f32_16x16x32_bf16 a[4:7], a[164:167], a[128:131], a[4:7]\n" \
"ds_read_b64_tr_b16 a[176:177], v36 offset:32768\n" \
"v_mov_b32_e64 v11, 0\n" \
"v_mfma_f32_16x16x32_bf16 a[4:7], a[228:231], a[192:195], a[4:7]\n" \
"s_addk_i32 s84, 0x2000\n" \
"v_mfma_f32_16x16x32_bf16 a[12:15], a[172:175], a[128:131], a[12:15]\n" \
"buffer_load_dwordx4 v25, s[76:79], s84 offen offset:3072 lds\n" \
"s_waitcnt lgkmcnt(15)\n" \
"v_mfma_f32_16x16x32_bf16 a[12:15], a[236:239], a[192:195], a[12:15]\n" \
"ds_read_b64_tr_b16 a[178:179], v36 offset:33792\n" \
"s_and_b32 s6, s2, 7\n" \
"s_waitcnt lgkmcnt(12)\n" \
"v_mfma_f32_16x16x32_bf16 a[8:11], a[128:131], a[168:171], a[8:11]\n" \
"ds_read_b64_tr_b16 a[240:241], v36 offset:40960\n" \
"s_lshl_b32 s7, s3, 1\n" \
"s_waitcnt lgkmcnt(9)\n" \
"v_mfma_f32_16x16x32_bf16 a[8:11], a[192:195], a[232:235], a[8:11]\n" \
"ds_read_b64_tr_b16 a[242:243], v36 offset:41984\n" \
"s_lshr_b32 s4, s2, 5\n" \
"v_mfma_f32_16x16x32_bf16 a[24:27], a[132:135], a[168:171], a[24:27]\n" \
"ds_read_b64_tr_b16 a[180:181], v37 offset:32768\n" \
"s_lshl_b32 s5, s6, 2\n" \
"v_mfma_f32_16x16x32_bf16 a[24:27], a[196:199], a[232:235], a[24:27]\n" \
"ds_read_b64_tr_b16 a[182:183], v37 offset:33792\n" \
"s_bfe_u32 s8, s2, 0x20003\n" \
"v_mfma_f32_16x16x32_bf16 a[28:31], a[172:175], a[132:135], a[28:31]\n" \
"ds_read_b64_tr_b16 a[244:245], v37 offset:40960\n" \
"s_and_b32 s7, s7, 4\n" \
"v_mfma_f32_16x16x32_bf16 a[28:31], a[236:239], a[196:199], a[28:31]\n" \
"s_add_u32 s89, s41, 0x0\n" \
"buffer_store_dwordx4 v[224:227], v20, s[80:83], s89 offen offset:2048 sc1\n" \
"v_mfma_f32_16x16x32_bf16 a[20:23], a[164:167], a[132:135], a[20:23]\n" \
"ds_read_b64_tr_b16 a[246:247], v37 offset:41984\n" \
"s_and_b32 s9, s3, 1\n" \
"v_mfma_f32_16x16x32_bf16 a[20:23], a[228:231], a[196:199], a[20:23]\n" \
"ds_read_b64_tr_b16 a[184:185], v38 offset:32768\n" \
"s_or_b32 s13, s5, s8\n" \
"v_mfma_f32_16x16x32_bf16 a[16:19], a[160:163], a[132:135], a[16:19]\n" \
"ds_read_b64_tr_b16 a[186:187], v38 offset:33792\n" \
"s_or_b32 s9, s7, s9\n" \
"v_mfma_f32_16x16x32_bf16 a[16:19], a[224:227], a[196:199], a[16:19]\n" \
"s_mul_i32 s7, s4, 0xa00\n" \
"s_add_i32 s7, s7, 0x5bc1000\n" \
"v_mfma_f32_16x16x32_bf16 a[32:35], a[160:163], a[136:139], a[32:35]\n" \
"s_add_u32 s88, s41, 0x0\n" \
"buffer_store_dwordx4 v[228:231], v20, s[80:83], s88 offen offset:3072 sc1\n" \
"v_mfma_f32_16x16x32_bf16 a[32:35], a[224:227], a[200:203], a[32:35]\n" \
"ds_read_b64_tr_b16 a[248:249], v38 offset:40960\n" \
"s_lshl_b32 s10, s13, 2\n" \
"v_mfma_f32_16x16x32_bf16 a[36:39], a[164:167], a[136:139], a[36:39]\n" \
"ds_read_b64_tr_b16 a[250:251], v38 offset:41984\n" \
"s_or_b32 s10, s10, s7\n" \
"v_mfma_f32_16x16x32_bf16 a[36:39], a[228:231], a[200:203], a[36:39]\n" \
"ds_read_b64_tr_b16 a[188:189], v39 offset:32768\n" \
"v_writelane_b32 v11, s10, 0\n" \
"v_mfma_f32_16x16x32_bf16 a[44:47], a[172:175], a[136:139], a[44:47]\n" \
"s_lshl_b32 s14, s4, 3\n" \
"v_writelane_b32 v11, s7, 1\n" \
"v_mfma_f32_16x16x32_bf16 a[44:47], a[236:239], a[200:203], a[44:47]\n" \
"ds_read_b64_tr_b16 a[190:191], v39 offset:33792\n" \
"s_or_b32 s7, s14, s6\n" \
"v_mfma_f32_16x16x32_bf16 a[40:43], a[136:139], a[168:171], a[40:43]\n" \
"s_add_u32 s89, s41, 0x1000\n" \
"buffer_store_dwordx4 v[232:235], v20, s[80:83], s89 offen offset:3072 sc1\n" \
"v_mfma_f32_16x16x32_bf16 a[40:43], a[200:203], a[232:235], a[40:43]\n" \
"ds_read_b64_tr_b16 a[252:253], v39 offset:40960\n" \
"s_lshl_b32 s7, s7, 10\n" \
"v_mfma_f32_16x16x32_bf16 a[56:59], a[140:143], a[168:171], a[56:59]\n" \
"ds_read_b64_tr_b16 a[254:255], v39 offset:41984\n" \
"s_lshl_b32 s10, s3, 8\n" \
"v_mfma_f32_16x16x32_bf16 a[56:59], a[204:207], a[232:235], a[56:59]\n" \
"s_or_b32 s7, s7, s10\n" \
"s_or_b32 s10, s7, 0x5c00200\n" \
"v_mfma_f32_16x16x32_bf16 a[60:63], a[172:175], a[140:143], a[60:63]\n" \
"v_writelane_b32 v11, s10, 2\n" \
"s_lshl_b32 s10, s8, 2\n" \
"v_mfma_f32_16x16x32_bf16 a[60:63], a[236:239], a[204:207], a[60:63]\n" \
"s_add_u32 s88, s41, 0x1000\n" \
"buffer_store_dwordx4 v[236:239], v20, s[80:83], s88 offen offset:2048 sc1\n" \
"v_mfma_f32_16x16x32_bf16 a[52:55], a[164:167], a[140:143], a[52:55]\n" \
"s_or_b32 s11, s10, s7\n" \
"s_or_b32 s11, s11, 0x5c00000\n" \
"v_mfma_f32_16x16x32_bf16 a[52:55], a[228:231], a[204:207], a[52:55]\n" \
"v_writelane_b32 v11, s11, 3\n" \
"s_or_b32 s7, s7, 0x5c00080\n" \
"v_mfma_f32_16x16x32_bf16 a[48:51], a[160:163], a[140:143], a[48:51]\n" \
"v_writelane_b32 v11, s7, 4\n" \
"s_or_b32 s7, s7, s10\n" \
"v_mfma_f32_16x16x32_bf16 a[48:51], a[224:227], a[204:207], a[48:51]\n" \
"s_add_i32 s10, s3, s2\n" \
"v_writelane_b32 v11, s7, 5\n" \
"s_waitcnt lgkmcnt(15)\n" \
"v_mfma_f32_16x16x32_bf16 a[64:67], a[164:167], v[248:251], a[64:67]\n" \
"s_mov_b32 s7, 2.0\n" \
"s_mov_b32 s11, 0x5bc0000\n" \
"v_mfma_f32_16x16x32_bf16 a[64:67], a[228:231], v[48:51], a[64:67]\n" \
"s_cmp_lg_u32 s10, 0\n" \
"s_cselect_b32 s10, s7, s11\n" \
"v_mfma_f32_16x16x32_bf16 a[80:83], a[172:175], v[248:251], a[80:83]\n" \
"s_lshr_b32 s15, s2, 3\n" \
"v_writelane_b32 v11, s10, 54\n" \
"v_mfma_f32_16x16x32_bf16 a[80:83], a[236:239], v[48:51], a[80:83]\n" \
"s_lshl_b32 s10, s6, 5\n" \
"s_min_u32 s16, s3, 2\n" \
"v_mfma_f32_16x16x32_bf16 a[68:71], v[248:251], a[168:171], a[68:71]\n" \
"s_mul_i32 s12, s8, 0x120\n" \
"v_mfma_f32_16x16x32_bf16 a[68:71], v[48:51], a[232:235], a[68:71]\n" \
"s_waitcnt vmcnt(4) lgkmcnt(0)\n" \
"s_barrier\n" \
"v_mfma_f32_16x16x32_bf16 v[52:55], a[176:179], a[128:131], v[52:55]\n" \
"s_add_u32 m0, s40, 0x8000\n" \
"s_add_u32 s85, s39, 0x6000\n" \
"v_mfma_f32_16x16x32_bf16 v[52:55], a[240:243], a[192:195], v[52:55]\n" \
"buffer_load_dwordx4 v20, s[80:83], s85 offen sc1 lds\n" \
"v_mfma_f32_16x16x32_bf16 v[56:59], a[180:183], a[128:131], v[56:59]\n" \
"ds_read_b64_tr_b16 a[160:161], v32 offset:0\n" \
"v_mfma_f32_16x16x32_bf16 v[56:59], a[244:247], a[192:195], v[56:59]\n" \
"ds_read_b64_tr_b16 a[162:163], v32 offset:512\n" \
"v_mfma_f32_16x16x32_bf16 v[60:63], a[184:187], a[128:131], v[60:63]\n" \
"s_add_u32 m0, s40, 0x9000\n" \
"s_add_u32 s85, s39, 0x7000\n" \
"v_mfma_f32_16x16x32_bf16 v[60:63], a[248:251], a[192:195], v[60:63]\n" \
"buffer_load_dwordx4 v20, s[80:83], s85 offen sc1 lds\n" \
"v_mfma_f32_16x16x32_bf16 v[64:67], a[188:191], a[128:131], v[64:67]\n" \
"ds_read_b64_tr_b16 a[224:225], v32 offset:4096\n" \
"v_mfma_f32_16x16x32_bf16 v[64:67], a[252:255], a[192:195], v[64:67]\n" \
"ds_read_b128 a[128:131], v28 offset:0\n" \
"v_mfma_f32_16x16x32_bf16 v[80:83], a[188:191], a[132:135], v[80:83]\n" \
"ds_read_b128 a[192:195], v29 offset:0\n" \
"v_mfma_f32_16x16x32_bf16 v[80:83], a[252:255], a[196:199], v[80:83]\n" \
"s_add_u32 m0, s40, 0xb000\n" \
"s_movk_i32 s84, 0x180\n" \
"v_mfma_f32_16x16x32_bf16 v[76:79], a[184:187], a[132:135], v[76:79]\n" \
"buffer_load_dwordx4 v21, s[76:79], s84 offen offset:0 lds\n" \
"v_mfma_f32_16x16x32_bf16 v[76:79], a[248:251], a[196:199], v[76:79]\n" \
"ds_read_b64_tr_b16 a[226:227], v32 offset:4608\n" \
"v_mfma_f32_16x16x32_bf16 v[72:75], a[180:183], a[132:135], v[72:75]\n" \
"s_add_u32 m0, s30, 0x8000\n" \
"s_movk_i32 s84, 0x180\n" \
"v_mfma_f32_16x16x32_bf16 v[72:75], a[244:247], a[196:199], v[72:75]\n" \
"buffer_load_dwordx4 v24, s[76:79], s84 offen offset:0 lds\n" \
"v_mfma_f32_16x16x32_bf16 v[68:71], a[176:179], a[132:135], v[68:71]\n" \
"ds_read_b64_tr_b16 a[164:165], v33 offset:0\n" \
"v_mfma_f32_16x16x32_bf16 v[68:71], a[240:243], a[196:199], v[68:71]\n" \
"ds_read_b128 a[132:135], v28 offset:2048\n" \
"v_mfma_f32_16x16x32_bf16 v[84:87], a[176:179], a[136:139], v[84:87]\n" \
"ds_read_b128 a[196:199], v29 offset:2048\n" \
"v_mfma_f32_16x16x32_bf16 v[84:87], a[240:243], a[200:203], v[84:87]\n" \
"s_movk_i32 s84, 0x2180\n" \
"v_mfma_f32_16x16x32_bf16 v[88:91], a[180:183], a[136:139], v[88:91]\n" \
"buffer_load_dwordx4 v25, s[76:79], s84 offen offset:1024 lds\n" \
"v_mfma_f32_16x16x32_bf16 v[88:91], a[244:247], a[200:203], v[88:91]\n" \
"ds_read_b64_tr_b16 a[166:167], v33 offset:512\n" \
"v_mfma_f32_16x16x32_bf16 v[92:95], a[184:187], a[136:139], v[92:95]\n" \
"ds_read_b64_tr_b16 a[228:229], v33 offset:4096\n" \
"v_mfma_f32_16x16x32_bf16 v[92:95], a[248:251], a[200:203], v[92:95]\n" \
"s_movk_i32 s84, 0x4180\n" \
"v_mfma_f32_16x16x32_bf16 v[96:99], a[188:191], a[136:139], v[96:99]\n" \
"buffer_load_dwordx4 v24, s[76:79], s84 offen offset:2048 lds\n" \
"v_mfma_f32_16x16x32_bf16 v[96:99], a[252:255], a[200:203], v[96:99]\n" \
"ds_read_b128 a[136:139], v28 offset:4096\n" \
"v_mfma_f32_16x16x32_bf16 v[112:115], a[188:191], a[140:143], v[112:115]\n" \
"ds_read_b128 a[200:203], v29 offset:4096\n" \
"v_mfma_f32_16x16x32_bf16 v[112:115], a[252:255], a[204:207], v[112:115]\n" \
"s_movk_i32 s84, 0x6180\n" \
"s_nop 0\n" \
"v_mfma_f32_16x16x32_bf16 v[108:111], a[184:187], a[140:143], v[108:111]\n" \
"buffer_load_dwordx4 v25, s[76:79], s84 offen offset:3072 lds\n" \
"v_mfma_f32_16x16x32_bf16 v[108:111], a[248:251], a[204:207], v[108:111]\n" \
"ds_read_b64_tr_b16 a[230:231], v33 offset:4608\n" \
"v_mfma_f32_16x16x32_bf16 v[104:107], a[180:183], a[140:143], v[104:107]\n" \
"ds_read_b64_tr_b16 a[172:173], v35 offset:0\n" \
"v_mfma_f32_16x16x32_bf16 v[104:107], a[244:247], a[204:207], v[104:107]\n" \
"ds_read_b64_tr_b16 a[174:175], v35 offset:512\n" \
"v_mfma_f32_16x16x32_bf16 v[100:103], a[176:179], a[140:143], v[100:103]\n" \
"ds_read_b64_tr_b16 a[236:237], v35 offset:4096\n" \
"v_mfma_f32_16x16x32_bf16 v[100:103], a[240:243], a[204:207], v[100:103]\n" \
"ds_read_b128 a[140:143], v28 offset:6144\n" \
"v_mfma_f32_16x16x32_bf16 v[116:119], a[176:179], a[144:147], v[116:119]\n" \
"ds_read_b128 a[204:207], v29 offset:6144\n" \
"v_mfma_f32_16x16x32_bf16 v[116:119], a[240:243], a[208:211], v[116:119]\n" \
"s_add_u32 m0, s31, 0x8000\n" \
"s_mov_b32 s84, 0x180000\n" \
"v_mfma_f32_16x16x32_bf16 v[120:123], a[180:183], a[144:147], v[120:123]\n" \
"buffer_load_dwordx4 v26, s[68:71], s84 offen offset:0 lds\n" \
"v_mfma_f32_16x16x32_bf16 v[120:123], a[244:247], a[208:211], v[120:123]\n" \
"s_addk_i32 s84, 0x7c00\n" \
"s_nop 0\n" \
"v_mfma_f32_16x16x32_bf16 v[124:127], a[184:187], a[144:147], v[124:127]\n" \
"buffer_load_dwordx4 v26, s[68:71], s84 offen offset:1024 lds\n" \
"v_mfma_f32_16x16x32_bf16 v[124:127], a[248:251], a[208:211], v[124:127]\n" \
"ds_read_b64_tr_b16 a[238:239], v35 offset:4608\n" \
"v_mfma_f32_16x16x32_bf16 v[128:131], a[188:191], a[144:147], v[128:131]\n" \
"ds_read_b64_tr_b16 a[168:169], v34 offset:0\n" \
"v_mfma_f32_16x16x32_bf16 v[128:131], a[252:255], a[208:211], v[128:131]\n" \
"ds_read_b128 a[144:147], v28 offset:8192\n" \
"v_mfma_f32_16x16x32_bf16 v[144:147], a[188:191], a[148:151], v[144:147]\n" \
"ds_read_b128 a[208:211], v29 offset:8192\n" \
"v_mfma_f32_16x16x32_bf16 v[144:147], a[252:255], a[212:215], v[144:147]\n" \
"s_addk_i32 s84, 0x7c00\n" \
"v_mfma_f32_16x16x32_bf16 v[140:143], a[184:187], a[148:151], v[140:143]\n" \
"buffer_load_dwordx4 v27, s[68:71], s84 offen offset:2048 lds\n" \
"v_mfma_f32_16x16x32_bf16 v[140:143], a[248:251], a[212:215], v[140:143]\n" \
"s_addk_i32 s84, 0x7c00\n" \
"v_mfma_f32_16x16x32_bf16 v[136:139], a[180:183], a[148:151], v[136:139]\n" \
"buffer_load_dwordx4 v27, s[68:71], s84 offen offset:3072 lds\n" \
"v_mfma_f32_16x16x32_bf16 v[136:139], a[244:247], a[212:215], v[136:139]\n" \
"ds_read_b64_tr_b16 a[170:171], v34 offset:512\n" \
"v_mfma_f32_16x16x32_bf16 v[132:135], a[176:179], a[148:151], v[132:135]\n" \
"ds_read_b64_tr_b16 a[232:233], v34 offset:4096\n" \
"v_mfma_f32_16x16x32_bf16 v[132:135], a[240:243], a[212:215], v[132:135]\n" \
"ds_read_b128 a[148:151], v28 offset:10240\n" \
"v_mfma_f32_16x16x32_bf16 v[148:151], a[176:179], a[152:155], v[148:151]\n" \
"ds_read_b128 a[212:215], v29 offset:10240\n" \
"v_mfma_f32_16x16x32_bf16 v[148:151], a[240:243], a[216:219], v[148:151]\n" \
"s_add_u32 m0, s30, 0x9000\n" \
"s_mov_b32 s84, 0x9180\n" \
"v_mfma_f32_16x16x32_bf16 v[152:155], a[180:183], a[152:155], v[152:155]\n" \
"buffer_load_dwordx4 v24, s[76:79], s84 offen offset:0 lds\n" \
"v_mfma_f32_16x16x32_bf16 v[152:155], a[244:247], a[216:219], v[152:155]\n" \
"s_addk_i32 s84, 0x2000\n" \
"s_nop 0\n" \
"v_mfma_f32_16x16x32_bf16 v[156:159], a[184:187], a[152:155], v[156:159]\n" \
"buffer_load_dwordx4 v25, s[76:79], s84 offen offset:1024 lds\n" \
"v_mfma_f32_16x16x32_bf16 v[156:159], a[248:251], a[216:219], v[156:159]\n" \
"ds_read_b64_tr_b16 a[234:235], v34 offset:4608\n" \
"v_mfma_f32_16x16x32_bf16 v[160:163], a[188:191], a[152:155], v[160:163]\n" \
"ds_read_b128 v[248:251], v22 offset:0\n" \
"v_mfma_f32_16x16x32_bf16 v[160:163], a[252:255], a[216:219], v[160:163]\n" \
"ds_read_b128 a[152:155], v28 offset:12288\n" \
"v_mfma_f32_16x16x32_bf16 v[176:179], a[188:191], a[156:159], v[176:179]\n" \
"ds_read_b128 a[216:219], v29 offset:12288\n" \
"v_mfma_f32_16x16x32_bf16 v[176:179], a[252:255], a[220:223], v[176:179]\n" \
"ds_read_b128 v[48:51], v23 offset:0\n" \
"v_mfma_f32_16x16x32_bf16 v[172:175], a[184:187], a[156:159], v[172:175]\n" \
"s_add_u32 s11, s22, s12\n" \
"s_addc_u32 s17, s23, 0\n" \
"v_mfma_f32_16x16x32_bf16 v[172:175], a[248:251], a[220:223], v[172:175]\n" \
"s_add_u32 s18, s24, s12\n" \
"v_writelane_b32 v11, s11, 9\n" \
"s_addc_u32 s19, s25, 0\n" \
"v_mfma_f32_16x16x32_bf16 v[168:171], a[180:183], a[156:159], v[168:171]\n" \
"s_and_b32 s20, s17, 0xffff\n" \
"v_writelane_b32 v11, s20, 10\n" \
"v_mfma_f32_16x16x32_bf16 v[168:171], a[244:247], a[220:223], v[168:171]\n" \
"s_and_b32 s20, s19, 0xffff\n" \
"v_writelane_b32 v11, s18, 11\n" \
"v_mfma_f32_16x16x32_bf16 v[164:167], a[176:179], a[156:159], v[164:167]\n" \
"s_cmpk_lt_u32 s3, 3\n" \
"s_cselect_b32 s11, s11, s18\n" \
"v_mfma_f32_16x16x32_bf16 v[164:167], a[240:243], a[220:223], v[164:167]\n" \
"ds_read_b128 a[156:159], v28 offset:14336\n" \
"ds_read_b128 a[220:223], v29 offset:14336\n" \
"s_waitcnt lgkmcnt(15)\n" \
"v_mfma_f32_16x16x32_bf16 a[0:3], a[160:163], a[128:131], a[0:3]\n" \
"s_addk_i32 s84, 0x2000\n" \
"v_mfma_f32_16x16x32_bf16 a[0:3], a[224:227], a[192:195], a[0:3]\n" \
"buffer_load_dwordx4 v24, s[76:79], s84 offen offset:2048 lds\n" \
"v_mfma_f32_16x16x32_bf16 a[4:7], a[164:167], a[128:131], a[4:7]\n" \
"ds_read_b64_tr_b16 a[176:177], v36 offset:0\n" \
"s_movk_i32 s18, 0xfff\n" \
"v_mfma_f32_16x16x32_bf16 a[4:7], a[228:231], a[192:195], a[4:7]\n" \
"s_addk_i32 s84, 0x2000\n" \
"v_mfma_f32_16x16x32_bf16 a[12:15], a[172:175], a[128:131], a[12:15]\n" \
"buffer_load_dwordx4 v25, s[76:79], s84 offen offset:3072 lds\n" \
"s_waitcnt lgkmcnt(15)\n" \
"v_mfma_f32_16x16x32_bf16 a[12:15], a[236:239], a[192:195], a[12:15]\n" \
"ds_read_b64_tr_b16 a[178:179], v36 offset:1024\n" \
"v_writelane_b32 v11, s20, 12\n" \
"s_waitcnt lgkmcnt(12)\n" \
"v_mfma_f32_16x16x32_bf16 a[8:11], a[128:131], a[168:171], a[8:11]\n" \
"ds_read_b64_tr_b16 a[240:241], v36 offset:8192\n" \
"s_and_b32 s20, s29, 0xffff\n" \
"s_waitcnt lgkmcnt(9)\n" \
"s_nop 0\n" \
"v_mfma_f32_16x16x32_bf16 a[8:11], a[192:195], a[232:235], a[8:11]\n" \
"ds_read_b64_tr_b16 a[242:243], v36 offset:9216\n" \
"v_writelane_b32 v11, s28, 13\n" \
"v_mfma_f32_16x16x32_bf16 a[24:27], a[132:135], a[168:171], a[24:27]\n" \
"ds_read_b64_tr_b16 a[180:181], v37 offset:0\n" \
"v_writelane_b32 v11, s20, 14\n" \
"v_mfma_f32_16x16x32_bf16 a[24:27], a[196:199], a[232:235], a[24:27]\n" \
"ds_read_b64_tr_b16 a[182:183], v37 offset:1024\n" \
"v_writelane_b32 v11, s11, 15\n" \
"v_mfma_f32_16x16x32_bf16 a[28:31], a[172:175], a[132:135], a[28:31]\n" \
"ds_read_b64_tr_b16 a[244:245], v37 offset:8192\n" \
"v_mfma_f32_16x16x32_bf16 a[28:31], a[236:239], a[196:199], a[28:31]\n" \
"s_add_u32 s89, s41, 0x2000\n" \
"buffer_store_dwordx4 v[240:243], v20, s[80:83], s89 offen offset:2048 sc1\n" \
"v_mfma_f32_16x16x32_bf16 a[20:23], a[164:167], a[132:135], a[20:23]\n" \
"ds_read_b64_tr_b16 a[246:247], v37 offset:9216\n" \
"v_mfma_f32_16x16x32_bf16 a[20:23], a[228:231], a[196:199], a[20:23]\n" \
"ds_read_b64_tr_b16 a[184:185], v38 offset:0\n" \
"v_mfma_f32_16x16x32_bf16 a[16:19], a[160:163], a[132:135], a[16:19]\n" \
"ds_read_b64_tr_b16 a[186:187], v38 offset:1024\n" \
"v_mfma_f32_16x16x32_bf16 a[16:19], a[224:227], a[196:199], a[16:19]\n" \
"s_cmpk_lt_u32 s3, 3\n" \
"s_cselect_b32 s11, s17, s19\n" \
"v_mfma_f32_16x16x32_bf16 a[32:35], a[160:163], a[136:139], a[32:35]\n" \
"s_add_u32 s88, s41, 0x2000\n" \
"buffer_store_dwordx4 v[244:247], v20, s[80:83], s88 offen offset:3072 sc1\n" \
"v_mfma_f32_16x16x32_bf16 a[32:35], a[224:227], a[200:203], a[32:35]\n" \
"ds_read_b64_tr_b16 a[248:249], v38 offset:8192\n" \
"s_and_b32 s11, s11, 0xffff\n" \
"v_mfma_f32_16x16x32_bf16 a[36:39], a[164:167], a[136:139], a[36:39]\n" \
"ds_read_b64_tr_b16 a[250:251], v38 offset:9216\n" \
"v_writelane_b32 v11, s11, 16\n" \
"v_mfma_f32_16x16x32_bf16 a[36:39], a[228:231], a[200:203], a[36:39]\n" \
"ds_read_b64_tr_b16 a[188:189], v39 offset:0\n" \
"s_mul_i32 s11, s6, 0x48\n" \
"v_mfma_f32_16x16x32_bf16 a[44:47], a[172:175], a[136:139], a[44:47]\n" \
"s_add_i32 s17, s11, 0x47\n" \
"v_mfma_f32_16x16x32_bf16 a[44:47], a[236:239], a[200:203], a[44:47]\n" \
"ds_read_b64_tr_b16 a[190:191], v39 offset:1024\n" \
"v_mfma_f32_16x16x32_bf16 a[40:43], a[136:139], a[168:171], a[40:43]\n" \
"s_add_u32 s89, s41, 0x3000\n" \
"buffer_store_dwordx4 v[184:187], v20, s[80:83], s89 offen offset:3072 sc1\n" \
"v_mfma_f32_16x16x32_bf16 a[40:43], a[200:203], a[232:235], a[40:43]\n" \
"ds_read_b64_tr_b16 a[252:253], v39 offset:8192\n" \
"v_mfma_f32_16x16x32_bf16 a[56:59], a[140:143], a[168:171], a[56:59]\n" \
"ds_read_b64_tr_b16 a[254:255], v39 offset:9216\n" \
"v_mfma_f32_16x16x32_bf16 a[56:59], a[204:207], a[232:235], a[56:59]\n" \
"s_cmpk_lt_u32 s3, 3\n" \
"s_cselect_b32 s17, s17, s18\n" \
"v_mfma_f32_16x16x32_bf16 a[60:63], a[172:175], a[140:143], a[60:63]\n" \
"s_mulk_i32 s17, 0x480\n" \
"s_addk_i32 s17, 0x120\n" \
"v_mfma_f32_16x16x32_bf16 a[60:63], a[236:239], a[204:207], a[60:63]\n" \
"s_add_u32 s88, s41, 0x3000\n" \
"buffer_store_dwordx4 v[188:191], v20, s[80:83], s88 offen offset:2048 sc1\n" \
"v_mfma_f32_16x16x32_bf16 a[52:55], a[164:167], a[140:143], a[52:55]\n" \
"v_writelane_b32 v11, s17, 17\n" \
"s_lshl_b32 s17, s3, 5\n" \
"v_mfma_f32_16x16x32_bf16 a[52:55], a[228:231], a[204:207], a[52:55]\n" \
"s_add_i32 s11, s11, s17\n" \
"s_mul_i32 s17, s11, 0x480\n" \
"v_mfma_f32_16x16x32_bf16 a[48:51], a[160:163], a[140:143], a[48:51]\n" \
"s_mul_i32 s11, s13, 0x24000\n" \
"s_add_i32 s18, s11, 0x1b000\n" \
"v_mfma_f32_16x16x32_bf16 a[48:51], a[224:227], a[204:207], a[48:51]\n" \
"s_cmpk_lt_u32 s3, 3\n" \
"s_cselect_b32 s17, s17, s18\n" \
"s_waitcnt lgkmcnt(15)\n" \
"v_mfma_f32_16x16x32_bf16 a[64:67], a[164:167], v[248:251], a[64:67]\n" \
"s_mov_b32 s18, 0x8000\n" \
"v_writelane_b32 v11, s17, 18\n" \
"v_mfma_f32_16x16x32_bf16 a[64:67], a[228:231], v[48:51], a[64:67]\n" \
"s_mul_i32 s17, s16, 0x2400\n" \
"s_mul_i32 s16, s16, 0x9000\n" \
"v_mfma_f32_16x16x32_bf16 a[80:83], a[172:175], v[248:251], a[80:83]\n" \
"s_bitset1_b32 s17, 17\n" \
"v_writelane_b32 v11, s17, 19\n" \
"v_mfma_f32_16x16x32_bf16 a[80:83], a[236:239], v[48:51], a[80:83]\n" \
"s_add_i32 s16, s11, s16\n" \
"v_writelane_b32 v11, s16, 26\n" \
"v_mfma_f32_16x16x32_bf16 a[68:71], v[248:251], a[168:171], a[68:71]\n" \
"s_mul_i32 s16, s3, 0x2400\n" \
"s_add_i32 s17, s16, 0xa400\n" \
"v_mfma_f32_16x16x32_bf16 a[68:71], v[48:51], a[232:235], a[68:71]\n" \
"s_waitcnt vmcnt(4) lgkmcnt(0)\n" \
"s_barrier\n" \
"v_mfma_f32_16x16x32_bf16 v[52:55], a[176:179], a[128:131], v[52:55]\n" \
"s_add_u32 m0, s40, 0x0\n" \
"s_add_u32 s85, s39, 0x8000\n" \
"v_mfma_f32_16x16x32_bf16 v[52:55], a[240:243], a[192:195], v[52:55]\n" \
"buffer_load_dwordx4 v20, s[80:83], s85 offen sc1 lds\n" \
"v_mfma_f32_16x16x32_bf16 v[56:59], a[180:183], a[128:131], v[56:59]\n" \
"ds_read_b64_tr_b16 a[160:161], v32 offset:32768\n" \
"v_mfma_f32_16x16x32_bf16 v[56:59], a[244:247], a[192:195], v[56:59]\n" \
"ds_read_b64_tr_b16 a[162:163], v32 offset:33280\n" \
"v_mfma_f32_16x16x32_bf16 v[60:63], a[184:187], a[128:131], v[60:63]\n" \
"s_add_u32 m0, s40, 0x1000\n" \
"s_add_u32 s85, s39, 0x9000\n" \
"v_mfma_f32_16x16x32_bf16 v[60:63], a[248:251], a[192:195], v[60:63]\n" \
"buffer_load_dwordx4 v20, s[80:83], s85 offen sc1 lds\n" \
"v_mfma_f32_16x16x32_bf16 v[64:67], a[188:191], a[128:131], v[64:67]\n" \
"ds_read_b64_tr_b16 a[224:225], v32 offset:36864\n" \
"v_mfma_f32_16x16x32_bf16 v[64:67], a[252:255], a[192:195], v[64:67]\n" \
".fill (L_pb + 24288 - .) / 4, 4, 0xBF800000\n" \
"L_pp6:\n" \
"s_cbranch_execz L_pret\n" \
"ds_read_b128 a[128:131], v28 offset:32768\n" \
"v_mfma_f32_16x16x32_bf16 v[80:83], a[188:191], a[132:135], v[80:83]\n" \
"ds_read_b128 a[192:195], v29 offset:32768\n" \
"v_mfma_f32_16x16x32_bf16 v[80:83], a[252:255], a[196:199], v[80:83]\n" \
"s_add_u32 m0, s40, 0x3000\n" \
"s_movk_i32 s84, 0x200\n" \
"v_mfma_f32_16x16x32_bf16 v[76:79], a[184:187], a[132:135], v[76:79]\n" \
"buffer_load_dwordx4 v21, s[76:79], s84 offen offset:0 lds\n" \
"v_mfma_f32_16x16x32_bf16 v[76:79], a[248:251], a[196:199], v[76:79]\n" \
"ds_read_b64_tr_b16 a[226:227], v32 offset:37376\n" \
"v_mfma_f32_16x16x32_bf16 v[72:75], a[180:183], a[132:135], v[72:75]\n" \
"s_add_u32 m0, s30, 0x0\n" \
"s_movk_i32 s84, 0x200\n" \
"v_mfma_f32_16x16x32_bf16 v[72:75], a[244:247], a[196:199], v[72:75]\n" \
"buffer_load_dwordx4 v24, s[76:79], s84 offen offset:0 lds\n" \
"v_mfma_f32_16x16x32_bf16 v[68:71], a[176:179], a[132:135], v[68:71]\n" \
"ds_read_b64_tr_b16 a[164:165], v33 offset:32768\n" \
"v_mfma_f32_16x16x32_bf16 v[68:71], a[240:243], a[196:199], v[68:71]\n" \
"ds_read_b128 a[132:135], v28 offset:34816\n" \
"v_mfma_f32_16x16x32_bf16 v[84:87], a[176:179], a[136:139], v[84:87]\n" \
"ds_read_b128 a[196:199], v29 offset:34816\n" \
"v_mfma_f32_16x16x32_bf16 v[84:87], a[240:243], a[200:203], v[84:87]\n" \
"s_movk_i32 s84, 0x2200\n" \
"v_mfma_f32_16x16x32_bf16 v[88:91], a[180:183], a[136:139], v[88:91]\n" \
"buffer_load_dwordx4 v25, s[76:79], s84 offen offset:1024 lds\n" \
"v_mfma_f32_16x16x32_bf16 v[88:91], a[244:247], a[200:203], v[88:91]\n" \
"ds_read_b64_tr_b16 a[166:167], v33 offset:33280\n" \
"v_mfma_f32_16x16x32_bf16 v[92:95], a[184:187], a[136:139], v[92:95]\n" \
"ds_read_b64_tr_b16 a[228:229], v33 offset:36864\n" \
"v_mfma_f32_16x16x32_bf16 v[92:95], a[248:251], a[200:203], v[92:95]\n" \
"s_movk_i32 s84, 0x4200\n" \
"v_mfma_f32_16x16x32_bf16 v[96:99], a[188:191], a[136:139], v[96:99]\n" \
"buffer_load_dwordx4 v24, s[76:79], s84 offen offset:2048 lds\n" \
"v_mfma_f32_16x16x32_bf16 v[96:99], a[252:255], a[200:203], v[96:99]\n" \
"ds_read_b128 a[136:139], v28 offset:36864\n" \
"v_mfma_f32_16x16x32_bf16 v[112:115], a[188:191], a[140:143], v[112:115]\n" \
"ds_read_b128 a[200:203], v29 offset:36864\n" \
"v_mfma_f32_16x16x32_bf16 v[112:115], a[252:255], a[204:207], v[112:115]\n" \
"s_movk_i32 s84, 0x6200\n" \
"s_nop 0\n" \
"v_mfma_f32_16x16x32_bf16 v[108:111], a[184:187], a[140:143], v[108:111]\n" \
"buffer_load_dwordx4 v25, s[76:79], s84 offen offset:3072 lds\n" \
"v_mfma_f32_16x16x32_bf16 v[108:111], a[248:251], a[204:207], v[108:111]\n" \
"ds_read_b64_tr_b16 a[230:231], v33 offset:37376\n" \
"v_mfma_f32_16x16x32_bf16 v[104:107], a[180:183], a[140:143], v[104:107]\n" \
"ds_read_b64_tr_b16 a[172:173], v35 offset:32768\n" \
"v_mfma_f32_16x16x32_bf16 v[104:107], a[244:247], a[204:207], v[104:107]\n" \
"ds_read_b64_tr_b16 a[174:175], v35 offset:33280\n" \
"v_mfma_f32_16x16x32_bf16 v[100:103], a[176:179], a[140:143], v[100:103]\n" \
"ds_read_b64_tr_b16 a[236:237], v35 offset:36864\n" \
"v_mfma_f32_16x16x32_bf16 v[100:103], a[240:243], a[204:207], v[100:103]\n" \
"ds_read_b128 a[140:143], v28 offset:38912\n" \
"v_mfma_f32_16x16x32_bf16 v[116:119], a[176:179], a[144:147], v[116:119]\n" \
"ds_read_b128 a[204:207], v29 offset:38912\n" \
"v_mfma_f32_16x16x32_bf16 v[116:119], a[240:243], a[208:211], v[116:119]\n" \
"s_add_u32 m0, s31, 0x0\n" \
"s_mov_b32 s84, 0x200000\n" \
"v_mfma_f32_16x16x32_bf16 v[120:123], a[180:183], a[144:147], v[120:123]\n" \
"buffer_load_dwordx4 v26, s[68:71], s84 offen offset:0 lds\n" \
"v_mfma_f32_16x16x32_bf16 v[120:123], a[244:247], a[208:211], v[120:123]\n" \
"s_addk_i32 s84, 0x7c00\n" \
"v_mfma_f32_16x16x32_bf16 v[124:127], a[184:187], a[144:147], v[124:127]\n" \
"buffer_load_dwordx4 v26, s[68:71], s84 offen offset:1024 lds\n" \
"v_mfma_f32_16x16x32_bf16 v[124:127], a[248:251], a[208:211], v[124:127]\n" \
"ds_read_b64_tr_b16 a[238:239], v35 offset:37376\n" \
"v_mfma_f32_16x16x32_bf16 v[128:131], a[188:191], a[144:147], v[128:131]\n" \
"ds_read_b64_tr_b16 a[168:169], v34 offset:32768\n" \
"v_mfma_f32_16x16x32_bf16 v[128:131], a[252:255], a[208:211], v[128:131]\n" \
"ds_read_b128 a[144:147], v28 offset:40960\n" \
"v_mfma_f32_16x16x32_bf16 v[144:147], a[188:191], a[148:151], v[144:147]\n" \
"ds_read_b128 a[208:211], v29 offset:40960\n" \
"v_mfma_f32_16x16x32_bf16 v[144:147], a[252:255], a[212:215], v[144:147]\n" \
"s_addk_i32 s84, 0x7c00\n" \
"v_mfma_f32_16x16x32_bf16 v[140:143], a[184:187], a[148:151], v[140:143]\n" \
"buffer_load_dwordx4 v27, s[68:71], s84 offen offset:2048 lds\n" \
"v_mfma_f32_16x16x32_bf16 v[140:143], a[248:251], a[212:215], v[140:143]\n" \
"s_addk_i32 s84, 0x7c00\n" \
"v_mfma_f32_16x16x32_bf16 v[136:139], a[180:183], a[148:151], v[136:139]\n" \
"buffer_load_dwordx4 v27, s[68:71], s84 offen offset:3072 lds\n" \
"v_mfma_f32_16x16x32_bf16 v[136:139], a[244:247], a[212:215], v[136:139]\n" \
"ds_read_b64_tr_b16 a[170:171], v34 offset:33280\n" \
"v_mfma_f32_16x16x32_bf16 v[132:135], a[176:179], a[148:151], v[132:135]\n" \
"ds_read_b64_tr_b16 a[232:233], v34 offset:36864\n" \
"v_mfma_f32_16x16x32_bf16 v[132:135], a[240:243], a[212:215], v[132:135]\n" \
"ds_read_b128 a[148:151], v28 offset:43008\n" \
"v_mfma_f32_16x16x32_bf16 v[148:151], a[176:179], a[152:155], v[148:151]\n" \
"ds_read_b128 a[212:215], v29 offset:43008\n" \
"v_mfma_f32_16x16x32_bf16 v[148:151], a[240:243], a[216:219], v[148:151]\n" \
"s_add_u32 m0, s30, 0x1000\n" \
"s_mov_b32 s84, 0x9200\n" \
"v_mfma_f32_16x16x32_bf16 v[152:155], a[180:183], a[152:155], v[152:155]\n" \
"buffer_load_dwordx4 v24, s[76:79], s84 offen offset:0 lds\n" \
"v_mfma_f32_16x16x32_bf16 v[152:155], a[244:247], a[216:219], v[152:155]\n" \
"s_addk_i32 s84, 0x2000\n" \
"s_nop 0\n" \
"v_mfma_f32_16x16x32_bf16 v[156:159], a[184:187], a[152:155], v[156:159]\n" \
"buffer_load_dwordx4 v25, s[76:79], s84 offen offset:1024 lds\n" \
"v_mfma_f32_16x16x32_bf16 v[156:159], a[248:251], a[216:219], v[156:159]\n" \
"ds_read_b64_tr_b16 a[234:235], v34 offset:37376\n" \
"v_mfma_f32_16x16x32_bf16 v[160:163], a[188:191], a[152:155], v[160:163]\n" \
"ds_read_b128 v[248:251], v22 offset:32768\n" \
"v_mfma_f32_16x16x32_bf16 v[160:163], a[252:255], a[216:219], v[160:163]\n" \
"ds_read_b128 a[152:155], v28 offset:45056\n" \
"v_mfma_f32_16x16x32_bf16 v[176:179], a[188:191], a[156:159], v[176:179]\n" \
"ds_read_b128 a[216:219], v29 offset:45056\n" \
"v_mfma_f32_16x16x32_bf16 v[176:179], a[252:255], a[220:223], v[176:179]\n" \
"ds_read_b128 v[48:51], v23 offset:32768\n" \
"v_mfma_f32_16x16x32_bf16 v[172:175], a[184:187], a[156:159], v[172:175]\n" \
"s_cmpk_lt_u32 s3, 3\n" \
"s_cselect_b32 s17, s17, s18\n" \
"v_mfma_f32_16x16x32_bf16 v[172:175], a[248:251], a[220:223], v[172:175]\n" \
"s_mov_b32 s18, 0x1b000\n" \
"s_xor_b32 s19, s3, 2\n" \
"v_mfma_f32_16x16x32_bf16 v[168:171], a[180:183], a[156:159], v[168:171]\n" \
"s_cmp_lg_u32 s19, 0\n" \
"s_cselect_b32 s18, s17, s18\n" \
"v_mfma_f32_16x16x32_bf16 v[168:171], a[244:247], a[220:223], v[168:171]\n" \
"v_writelane_b32 v11, s17, 20\n" \
"s_or_b32 s17, s16, 0x10000\n" \
"v_mfma_f32_16x16x32_bf16 v[164:167], a[176:179], a[156:159], v[164:167]\n" \
"v_writelane_b32 v11, s18, 21\n" \
"s_add_i32 s18, s16, 0x19000\n" \
"v_mfma_f32_16x16x32_bf16 v[164:167], a[240:243], a[220:223], v[164:167]\n" \
"ds_read_b128 a[156:159], v28 offset:47104\n" \
"ds_read_b128 a[220:223], v29 offset:47104\n" \
"s_waitcnt lgkmcnt(15)\n" \
"v_mfma_f32_16x16x32_bf16 a[0:3], a[160:163], a[128:131], a[0:3]\n" \
"s_addk_i32 s84, 0x2000\n" \
"v_mfma_f32_16x16x32_bf16 a[0:3], a[224:227], a[192:195], a[0:3]\n" \
"buffer_load_dwordx4 v24, s[76:79], s84 offen offset:2048 lds\n" \
"v_mfma_f32_16x16x32_bf16 a[4:7], a[164:167], a[128:131], a[4:7]\n" \
"ds_read_b64_tr_b16 a[176:177], v36 offset:32768\n" \
"v_writelane_b32 v11, s17, 22\n" \
"v_mfma_f32_16x16x32_bf16 a[4:7], a[228:231], a[192:195], a[4:7]\n" \
"s_addk_i32 s84, 0x2000\n" \
"v_mfma_f32_16x16x32_bf16 a[12:15], a[172:175], a[128:131], a[12:15]\n" \
"buffer_load_dwordx4 v25, s[76:79], s84 offen offset:3072 lds\n" \
"s_waitcnt lgkmcnt(15)\n" \
"v_mfma_f32_16x16x32_bf16 a[12:15], a[236:239], a[192:195], a[12:15]\n" \
"ds_read_b64_tr_b16 a[178:179], v36 offset:33792\n" \
"s_mov_b32 s17, 0\n" \
"s_waitcnt lgkmcnt(12)\n" \
"v_mfma_f32_16x16x32_bf16 a[8:11], a[128:131], a[168:171], a[8:11]\n" \
"ds_read_b64_tr_b16 a[240:241], v36 offset:40960\n" \
"s_waitcnt lgkmcnt(9)\n" \
"v_mfma_f32_16x16x32_bf16 a[8:11], a[192:195], a[232:235], a[8:11]\n" \
"ds_read_b64_tr_b16 a[242:243], v36 offset:41984\n" \
"v_mfma_f32_16x16x32_bf16 a[24:27], a[132:135], a[168:171], a[24:27]\n" \
"ds_read_b64_tr_b16 a[180:181], v37 offset:32768\n" \
"v_mfma_f32_16x16x32_bf16 a[24:27], a[196:199], a[232:235], a[24:27]\n" \
"ds_read_b64_tr_b16 a[182:183], v37 offset:33792\n" \
"v_mfma_f32_16x16x32_bf16 a[28:31], a[172:175], a[132:135], a[28:31]\n" \
"ds_read_b64_tr_b16 a[244:245], v37 offset:40960\n" \
"v_mfma_f32_16x16x32_bf16 a[28:31], a[236:239], a[196:199], a[28:31]\n" \
"s_add_u32 s89, s42, 0x0\n" \
"buffer_store_dwordx4 v[216:219], v20, s[80:83], s89 offen offset:0 sc1\n" \
"v_mfma_f32_16x16x32_bf16 a[20:23], a[164:167], a[132:135], a[20:23]\n" \
"ds_read_b64_tr_b16 a[246:247], v37 offset:41984\n" \
"v_mfma_f32_16x16x32_bf16 a[20:23], a[228:231], a[196:199], a[20:23]\n" \
"ds_read_b64_tr_b16 a[184:185], v38 offset:32768\n" \
"v_mfma_f32_16x16x32_bf16 a[16:19], a[160:163], a[132:135], a[16:19]\n" \
"ds_read_b64_tr_b16 a[186:187], v38 offset:33792\n" \
"v_mfma_f32_16x16x32_bf16 a[16:19], a[224:227], a[196:199], a[16:19]\n" \
"s_cmpk_lt_u32 s3, 3\n" \
"s_cselect_b32 s18, s18, s17\n" \
"v_mfma_f32_16x16x32_bf16 a[32:35], a[160:163], a[136:139], a[32:35]\n" \
"s_add_u32 s88, s42, 0x1000\n" \
"buffer_store_dwordx4 v[220:223], v20, s[80:83], s88 offen offset:0 sc1\n" \
"v_mfma_f32_16x16x32_bf16 a[32:35], a[224:227], a[200:203], a[32:35]\n" \
"ds_read_b64_tr_b16 a[248:249], v38 offset:40960\n" \
"s_bitset1_b32 s16, 17\n" \
"v_mfma_f32_16x16x32_bf16 a[36:39], a[164:167], a[136:139], a[36:39]\n" \
"ds_read_b64_tr_b16 a[250:251], v38 offset:41984\n" \
"v_writelane_b32 v11, s18, 23\n" \
"v_mfma_f32_16x16x32_bf16 a[36:39], a[228:231], a[200:203], a[36:39]\n" \
"ds_read_b64_tr_b16 a[188:189], v39 offset:32768\n" \
"s_movk_i32 s18, 0x2400\n" \
"v_mfma_f32_16x16x32_bf16 a[44:47], a[172:175], a[136:139], a[44:47]\n" \
"s_cmpk_lt_u32 s3, 3\n" \
"s_cselect_b32 s16, s16, s18\n" \
"v_mfma_f32_16x16x32_bf16 a[44:47], a[236:239], a[200:203], a[44:47]\n" \
"ds_read_b64_tr_b16 a[190:191], v39 offset:33792\n" \
"s_mul_i32 s19, s3, 0x9000\n" \
"v_mfma_f32_16x16x32_bf16 a[40:43], a[136:139], a[168:171], a[40:43]\n" \
"s_add_u32 s89, s42, 0x0\n" \
"buffer_store_dwordx4 v[192:195], v20, s[80:83], s89 offen offset:2048 sc1\n" \
"v_mfma_f32_16x16x32_bf16 a[40:43], a[200:203], a[232:235], a[40:43]\n" \
"ds_read_b64_tr_b16 a[252:253], v39 offset:40960\n" \
"v_writelane_b32 v11, s16, 24\n" \
"v_mfma_f32_16x16x32_bf16 a[56:59], a[140:143], a[168:171], a[56:59]\n" \
"ds_read_b64_tr_b16 a[254:255], v39 offset:41984\n" \
"s_lshl_b32 s16, s3, 10\n" \
"v_mfma_f32_16x16x32_bf16 a[56:59], a[204:207], a[232:235], a[56:59]\n" \
"s_addk_i32 s16, 0x4800\n" \
"v_writelane_b32 v11, s16, 25\n" \
"v_mfma_f32_16x16x32_bf16 a[60:63], a[172:175], a[140:143], a[60:63]\n" \
"s_add_i32 s16, s15, 1\n" \
"s_and_b32 s16, s16, 3\n" \
"v_mfma_f32_16x16x32_bf16 a[60:63], a[236:239], a[204:207], a[60:63]\n" \
"s_add_u32 s88, s42, 0x0\n" \
"buffer_store_dwordx4 v[196:199], v20, s[80:83], s88 offen offset:3072 sc1\n" \
"v_mfma_f32_16x16x32_bf16 a[52:55], a[164:167], a[140:143], a[52:55]\n" \
"s_or_b32 s16, s16, s5\n" \
"s_mul_i32 s18, s16, 0x24000\n" \
"v_mfma_f32_16x16x32_bf16 a[52:55], a[228:231], a[204:207], a[52:55]\n" \
"s_add_i32 s18, s18, s19\n" \
"s_xor_b32 s13, s13, 2\n" \
"v_mfma_f32_16x16x32_bf16 a[48:51], a[160:163], a[140:143], a[48:51]\n" \
"s_add_i32 s15, s15, -1\n" \
"v_writelane_b32 v11, s18, 27\n" \
"v_mfma_f32_16x16x32_bf16 a[48:51], a[224:227], a[204:207], a[48:51]\n" \
"s_mul_i32 s18, s13, 0x24000\n" \
"s_and_b32 s15, s15, 3\n" \
"s_waitcnt lgkmcnt(15)\n" \
"v_mfma_f32_16x16x32_bf16 a[64:67], a[164:167], v[248:251], a[64:67]\n" \
"s_add_i32 s18, s18, s19\n" \
"s_or_b32 s15, s15, s5\n" \
"v_mfma_f32_16x16x32_bf16 a[64:67], a[228:231], v[48:51], a[64:67]\n" \
"v_writelane_b32 v11, s18, 28\n" \
"s_mul_i32 s18, s15, 0x24000\n" \
"v_mfma_f32_16x16x32_bf16 a[80:83], a[172:175], v[248:251], a[80:83]\n" \
"s_add_i32 s18, s18, s19\n" \
"v_writelane_b32 v11, s18, 29\n" \
"v_mfma_f32_16x16x32_bf16 a[80:83], a[236:239], v[48:51], a[80:83]\n" \
"s_or_b32 s18, s9, s14\n" \
"s_mul_i32 s18, s18, 0x24800\n" \
"v_mfma_f32_16x16x32_bf16 a[68:71], v[248:251], a[168:171], a[68:71]\n" \
"s_lshl_b32 s16, s16, 12\n" \
"s_add_i32 s19, s18, 0x3004800\n" \
"v_mfma_f32_16x16x32_bf16 a[68:71], v[48:51], a[232:235], a[68:71]\n" \
"s_waitcnt vmcnt(4) lgkmcnt(0)\n" \
"s_barrier\n" \
"v_mov_b32_e32 v45, s62\n" \
"s_mov_b64 exec, 1\n" \
"buffer_store_dword v45, off, s[80:83], s37 offset:0 sc1\n" \
"s_mov_b64 exec, -1\n" \
"s_nop 0\n" \
"v_mfma_f32_16x16x32_bf16 v[52:55], a[176:179], a[128:131], v[52:55]\n" \
"s_add_u32 m0, s40, 0x8000\n" \
"s_add_u32 s85, s39, 0xa000\n" \
"v_mfma_f32_16x16x32_bf16 v[52:55], a[240:243], a[192:195], v[52:55]\n" \
"buffer_load_dwordx4 v20, s[80:83], s85 offen sc1 lds\n" \
"v_mfma_f32_16x16x32_bf16 v[56:59], a[180:183], a[128:131], v[56:59]\n" \
"ds_read_b64_tr_b16 a[160:161], v32 offset:0\n" \
"v_accvgpr_read_b32 v220, a120\n" \
"v_mfma_f32_16x16x32_bf16 v[56:59], a[244:247], a[192:195], v[56:59]\n" \
"ds_read_b64_tr_b16 a[162:163], v32 offset:512\n" \
"v_accvgpr_read_b32 v221, a121\n" \
"v_mfma_f32_16x16x32_bf16 v[60:63], a[184:187], a[128:131], v[60:63]\n" \
"s_add_u32 m0, s40, 0x9000\n" \
"s_add_u32 s85, s39, 0xb000\n" \
"v_mfma_f32_16x16x32_bf16 v[60:63], a[248:251], a[192:195], v[60:63]\n" \
"buffer_load_dwordx4 v20, s[80:83], s85 offen sc1 lds\n" \
"v_mfma_f32_16x16x32_bf16 v[64:67], a[188:191], a[128:131], v[64:67]\n" \
"ds_read_b64_tr_b16 a[224:225], v32 offset:4096\n" \
"v_accvgpr_read_b32 v222, a122\n" \
"v_mfma_f32_16x16x32_bf16 v[64:67], a[252:255], a[192:195], v[64:67]\n" \
"ds_read_b128 a[128:131], v28 offset:0\n" \
"v_accvgpr_read_b32 v223, a123\n" \
"v_mfma_f32_16x16x32_bf16 v[80:83], a[188:191], a[132:135], v[80:83]\n" \
"ds_read_b128 a[192:195], v29 offset:0\n" \
"v_accvgpr_read_b32 v180, a124\n" \
"v_mfma_f32_16x16x32_bf16 v[80:83], a[252:255], a[196:199], v[80:83]\n" \
"s_add_u32 m0, s40, 0xb000\n" \
"s_movk_i32 s84, 0x280\n" \
"v_mfma_f32_16x16x32_bf16 v[76:79], a[184:187], a[132:135], v[76:79]\n" \
"buffer_load_dwordx4 v21, s[76:79], s84 offen offset:0 lds\n" \
"v_mfma_f32_16x16x32_bf16 v[76:79], a[248:251], a[196:199], v[76:79]\n" \
"ds_read_b64_tr_b16 a[226:227], v32 offset:4608\n" \
"v_accvgpr_read_b32 v181, a125\n" \
"v_mfma_f32_16x16x32_bf16 v[72:75], a[180:183], a[132:135], v[72:75]\n" \
"s_add_u32 m0, s30, 0x8000\n" \
"s_movk_i32 s84, 0x280\n" \
"v_mfma_f32_16x16x32_bf16 v[72:75], a[244:247], a[196:199], v[72:75]\n" \
"buffer_load_dwordx4 v24, s[76:79], s84 offen offset:0 lds\n" \
"v_mfma_f32_16x16x32_bf16 v[68:71], a[176:179], a[132:135], v[68:71]\n" \
"ds_read_b64_tr_b16 a[164:165], v33 offset:0\n" \
"v_accvgpr_read_b32 v182, a126\n" \
"v_mfma_f32_16x16x32_bf16 v[68:71], a[240:243], a[196:199], v[68:71]\n" \
"ds_read_b128 a[132:135], v28 offset:2048\n" \
"v_accvgpr_read_b32 v183, a127\n" \
"v_mfma_f32_16x16x32_bf16 v[84:87], a[176:179], a[136:139], v[84:87]\n" \
"ds_read_b128 a[196:199], v29 offset:2048\n" \
"v_cvt_pk_bf16_f32 v220, v220, v221\n" \
"v_mfma_f32_16x16x32_bf16 v[84:87], a[240:243], a[200:203], v[84:87]\n" \
"s_movk_i32 s84, 0x2280\n" \
"v_mfma_f32_16x16x32_bf16 v[88:91], a[180:183], a[136:139], v[88:91]\n" \
"buffer_load_dwordx4 v25, s[76:79], s84 offen offset:1024 lds\n" \
"v_mfma_f32_16x16x32_bf16 v[88:91], a[244:247], a[200:203], v[88:91]\n" \
"ds_read_b64_tr_b16 a[166:167], v33 offset:512\n" \
"v_cvt_pk_bf16_f32 v221, v222, v223\n" \
"v_mfma_f32_16x16x32_bf16 v[92:95], a[184:187], a[136:139], v[92:95]\n" \
"ds_read_b64_tr_b16 a[228:229], v33 offset:4096\n" \
"v_cvt_pk_bf16_f32 v222, v180, v181\n" \
"v_mfma_f32_16x16x32_bf16 v[92:95], a[248:251], a[200:203], v[92:95]\n" \
"s_movk_i32 s84, 0x4280\n" \
"v_mfma_f32_16x16x32_bf16 v[96:99], a[188:191], a[136:139], v[96:99]\n" \
"buffer_load_dwordx4 v24, s[76:79], s84 offen offset:2048 lds\n" \
"v_mfma_f32_16x16x32_bf16 v[96:99], a[252:255], a[200:203], v[96:99]\n" \
"ds_read_b128 a[136:139], v28 offset:4096\n" \
"v_cvt_pk_bf16_f32 v223, v182, v183\n" \
"v_mfma_f32_16x16x32_bf16 v[112:115], a[188:191], a[140:143], v[112:115]\n" \
"ds_read_b128 a[200:203], v29 offset:4096\n" \
"v_accvgpr_read_b32 v216, a112\n" \
"v_mfma_f32_16x16x32_bf16 v[112:115], a[252:255], a[204:207], v[112:115]\n" \
"s_movk_i32 s84, 0x6280\n" \
"s_nop 0\n" \
"v_mfma_f32_16x16x32_bf16 v[108:111], a[184:187], a[140:143], v[108:111]\n" \
"buffer_load_dwordx4 v25, s[76:79], s84 offen offset:3072 lds\n" \
"v_mfma_f32_16x16x32_bf16 v[108:111], a[248:251], a[204:207], v[108:111]\n" \
"ds_read_b64_tr_b16 a[230:231], v33 offset:4608\n" \
"v_accvgpr_read_b32 v217, a113\n" \
"v_mfma_f32_16x16x32_bf16 v[104:107], a[180:183], a[140:143], v[104:107]\n" \
"ds_read_b64_tr_b16 a[172:173], v35 offset:0\n" \
"v_accvgpr_read_b32 v218, a114\n" \
"v_mfma_f32_16x16x32_bf16 v[104:107], a[244:247], a[204:207], v[104:107]\n" \
"ds_read_b64_tr_b16 a[174:175], v35 offset:512\n" \
"v_accvgpr_read_b32 v219, a115\n" \
"v_mfma_f32_16x16x32_bf16 v[100:103], a[176:179], a[140:143], v[100:103]\n" \
"ds_read_b64_tr_b16 a[236:237], v35 offset:4096\n" \
"v_accvgpr_read_b32 v180, a116\n" \
"v_mfma_f32_16x16x32_bf16 v[100:103], a[240:243], a[204:207], v[100:103]\n" \
"ds_read_b128 a[140:143], v28 offset:6144\n" \
"v_accvgpr_read_b32 v181, a117\n" \
"v_mfma_f32_16x16x32_bf16 v[116:119], a[176:179], a[144:147], v[116:119]\n" \
"ds_read_b128 a[204:207], v29 offset:6144\n" \
"v_accvgpr_read_b32 v182, a118\n" \
"v_mfma_f32_16x16x32_bf16 v[116:119], a[240:243], a[208:211], v[116:119]\n" \
"s_add_u32 m0, s31, 0x8000\n" \
"s_mov_b32 s84, 0x280000\n" \
"v_mfma_f32_16x16x32_bf16 v[120:123], a[180:183], a[144:147], v[120:123]\n" \
"buffer_load_dwordx4 v26, s[68:71], s84 offen offset:0 lds\n" \
"v_mfma_f32_16x16x32_bf16 v[120:123], a[244:247], a[208:211], v[120:123]\n" \
"s_addk_i32 s84, 0x7c00\n" \
"s_nop 0\n" \
"v_mfma_f32_16x16x32_bf16 v[124:127], a[184:187], a[144:147], v[124:127]\n" \
"buffer_load_dwordx4 v26, s[68:71], s84 offen offset:1024 lds\n" \
"v_mfma_f32_16x16x32_bf16 v[124:127], a[248:251], a[208:211], v[124:127]\n" \
"ds_read_b64_tr_b16 a[238:239], v35 offset:4608\n" \
"v_accvgpr_read_b32 v183, a119\n" \
"v_mfma_f32_16x16x32_bf16 v[128:131], a[188:191], a[144:147], v[128:131]\n" \
"ds_read_b64_tr_b16 a[168:169], v34 offset:0\n" \
"v_cvt_pk_bf16_f32 v216, v216, v217\n" \
"v_mfma_f32_16x16x32_bf16 v[128:131], a[252:255], a[208:211], v[128:131]\n" \
"ds_read_b128 a[144:147], v28 offset:8192\n" \
"v_cvt_pk_bf16_f32 v217, v218, v219\n" \
"v_mfma_f32_16x16x32_bf16 v[144:147], a[188:191], a[148:151], v[144:147]\n" \
"ds_read_b128 a[208:211], v29 offset:8192\n" \
"v_cvt_pk_bf16_f32 v218, v180, v181\n" \
"v_mfma_f32_16x16x32_bf16 v[144:147], a[252:255], a[212:215], v[144:147]\n" \
"s_addk_i32 s84, 0x7c00\n" \
"v_mfma_f32_16x16x32_bf16 v[140:143], a[184:187], a[148:151], v[140:143]\n" \
"buffer_load_dwordx4 v27, s[68:71], s84 offen offset:2048 lds\n" \
"v_mfma_f32_16x16x32_bf16 v[140:143], a[248:251], a[212:215], v[140:143]\n" \
"s_addk_i32 s84, 0x7c00\n" \
"v_mfma_f32_16x16x32_bf16 v[136:139], a[180:183], a[148:151], v[136:139]\n" \
"buffer_load_dwordx4 v27, s[68:71], s84 offen offset:3072 lds\n" \
"v_mfma_f32_16x16x32_bf16 v[136:139], a[244:247], a[212:215], v[136:139]\n" \
"ds_read_b64_tr_b16 a[170:171], v34 offset:512\n" \
"v_cvt_pk_bf16_f32 v219, v182, v183\n" \
"v_mfma_f32_16x16x32_bf16 v[132:135], a[176:179], a[148:151], v[132:135]\n" \
"ds_read_b64_tr_b16 a[232:233], v34 offset:4096\n" \
"v_accvgpr_read_b32 v196, a104\n" \
"v_mfma_f32_16x16x32_bf16 v[132:135], a[240:243], a[212:215], v[132:135]\n" \
"ds_read_b128 a[148:151], v28 offset:10240\n" \
"v_accvgpr_read_b32 v197, a105\n" \
"v_mfma_f32_16x16x32_bf16 v[148:151], a[176:179], a[152:155], v[148:151]\n" \
"ds_read_b128 a[212:215], v29 offset:10240\n" \
"v_accvgpr_read_b32 v198, a106\n" \
"v_mfma_f32_16x16x32_bf16 v[148:151], a[240:243], a[216:219], v[148:151]\n" \
"s_add_u32 m0, s30, 0x9000\n" \
"s_mov_b32 s84, 0x9280\n" \
"v_mfma_f32_16x16x32_bf16 v[152:155], a[180:183], a[152:155], v[152:155]\n" \
"buffer_load_dwordx4 v24, s[76:79], s84 offen offset:0 lds\n" \
"v_mfma_f32_16x16x32_bf16 v[152:155], a[244:247], a[216:219], v[152:155]\n" \
"s_addk_i32 s84, 0x2000\n" \
"s_nop 0\n" \
"v_mfma_f32_16x16x32_bf16 v[156:159], a[184:187], a[152:155], v[156:159]\n" \
"buffer_load_dwordx4 v25, s[76:79], s84 offen offset:1024 lds\n" \
"v_mfma_f32_16x16x32_bf16 v[156:159], a[248:251], a[216:219], v[156:159]\n" \
"ds_read_b64_tr_b16 a[234:235], v34 offset:4608\n" \
"v_accvgpr_read_b32 v199, a107\n" \
"v_mfma_f32_16x16x32_bf16 v[160:163], a[188:191], a[152:155], v[160:163]\n" \
"ds_read_b128 v[248:251], v22 offset:0\n" \
"v_accvgpr_read_b32 v180, a108\n" \
"v_mfma_f32_16x16x32_bf16 v[160:163], a[252:255], a[216:219], v[160:163]\n" \
"ds_read_b128 a[152:155], v28 offset:12288\n" \
"v_accvgpr_read_b32 v181, a109\n" \
"v_mfma_f32_16x16x32_bf16 v[176:179], a[188:191], a[156:159], v[176:179]\n" \
"ds_read_b128 a[216:219], v29 offset:12288\n" \
"v_accvgpr_read_b32 v182, a110\n" \
"v_mfma_f32_16x16x32_bf16 v[176:179], a[252:255], a[220:223], v[176:179]\n" \
"ds_read_b128 v[48:51], v23 offset:0\n" \
"v_accvgpr_read_b32 v183, a111\n" \
"v_mfma_f32_16x16x32_bf16 v[172:175], a[184:187], a[156:159], v[172:175]\n" \
"v_cvt_pk_bf16_f32 v196, v196, v197\n" \
"v_cvt_pk_bf16_f32 v197, v198, v199\n" \
"v_mfma_f32_16x16x32_bf16 v[172:175], a[248:251], a[220:223], v[172:175]\n" \
"v_cvt_pk_bf16_f32 v198, v180, v181\n" \
"v_cvt_pk_bf16_f32 v199, v182, v183\n" \
"v_mfma_f32_16x16x32_bf16 v[168:171], a[180:183], a[156:159], v[168:171]\n" \
"v_accvgpr_read_b32 v192, a96\n" \
"v_accvgpr_read_b32 v193, a97\n" \
"v_mfma_f32_16x16x32_bf16 v[168:171], a[244:247], a[220:223], v[168:171]\n" \
"v_accvgpr_read_b32 v194, a98\n" \
"v_accvgpr_read_b32 v195, a99\n" \
"v_mfma_f32_16x16x32_bf16 v[164:167], a[176:179], a[156:159], v[164:167]\n" \
"v_accvgpr_read_b32 v180, a100\n" \
"v_accvgpr_read_b32 v181, a101\n" \
"v_mfma_f32_16x16x32_bf16 v[164:167], a[240:243], a[220:223], v[164:167]\n" \
"ds_read_b128 a[156:159], v28 offset:14336\n" \
"ds_read_b128 a[220:223], v29 offset:14336\n" \
"s_waitcnt lgkmcnt(15)\n" \
"v_mfma_f32_16x16x32_bf16 a[0:3], a[160:163], a[128:131], a[0:3]\n" \
"s_addk_i32 s84, 0x2000\n" \
"v_mfma_f32_16x16x32_bf16 a[0:3], a[224:227], a[192:195], a[0:3]\n" \
"buffer_load_dwordx4 v24, s[76:79], s84 offen offset:2048 lds\n" \
"v_mfma_f32_16x16x32_bf16 a[4:7], a[164:167], a[128:131], a[4:7]\n" \
"ds_read_b64_tr_b16 a[176:177], v36 offset:0\n" \
"v_accvgpr_read_b32 v182, a102\n" \
"v_mfma_f32_16x16x32_bf16 a[4:7], a[228:231], a[192:195], a[4:7]\n" \
"s_addk_i32 s84, 0x2000\n" \
"v_mfma_f32_16x16x32_bf16 a[12:15], a[172:175], a[128:131], a[12:15]\n" \
"buffer_load_dwordx4 v25, s[76:79], s84 offen offset:3072 lds\n" \
"s_waitcnt lgkmcnt(15)\n" \
"v_mfma_f32_16x16x32_bf16 a[12:15], a[236:239], a[192:195], a[12:15]\n" \
"ds_read_b64_tr_b16 a[178:179], v36 offset:1024\n" \
"v_accvgpr_read_b32 v183, a103\n" \
"s_waitcnt lgkmcnt(12)\n" \
"v_mfma_f32_16x16x32_bf16 a[8:11], a[128:131], a[168:171], a[8:11]\n" \
"ds_read_b64_tr_b16 a[240:241], v36 offset:8192\n" \
"v_cvt_pk_bf16_f32 v192, v192, v193\n" \
"s_waitcnt lgkmcnt(9)\n" \
"v_mfma_f32_16x16x32_bf16 a[8:11], a[192:195], a[232:235], a[8:11]\n" \
"ds_read_b64_tr_b16 a[242:243], v36 offset:9216\n" \
"v_cvt_pk_bf16_f32 v193, v194, v195\n" \
"v_mfma_f32_16x16x32_bf16 a[24:27], a[132:135], a[168:171], a[24:27]\n" \
"ds_read_b64_tr_b16 a[180:181], v37 offset:0\n" \
"v_cvt_pk_bf16_f32 v194, v180, v181\n" \
"v_mfma_f32_16x16x32_bf16 a[24:27], a[196:199], a[232:235], a[24:27]\n" \
"ds_read_b64_tr_b16 a[182:183], v37 offset:1024\n" \
"v_cvt_pk_bf16_f32 v195, v182, v183\n" \
"v_mfma_f32_16x16x32_bf16 a[28:31], a[172:175], a[132:135], a[28:31]\n" \
"ds_read_b64_tr_b16 a[244:245], v37 offset:8192\n" \
"v_accvgpr_read_b32 v188, a88\n" \
"v_mfma_f32_16x16x32_bf16 a[28:31], a[236:239], a[196:199], a[28:31]\n" \
"ds_read_b64_tr_b16 a[246:247], v37 offset:9216\n" \
"v_accvgpr_read_b32 v189, a89\n" \
"v_mfma_f32_16x16x32_bf16 a[20:23], a[164:167], a[132:135], a[20:23]\n" \
"v_accvgpr_read_b32 v190, a90\n" \
"v_accvgpr_read_b32 v191, a91\n" \
"v_mfma_f32_16x16x32_bf16 a[20:23], a[228:231], a[196:199], a[20:23]\n" \
"s_add_u32 s89, s42, 0x1000\n" \
"buffer_store_dwordx4 a[84:87], v20, s[80:83], s89 offen offset:3072 sc1\n" \
"v_mfma_f32_16x16x32_bf16 a[16:19], a[160:163], a[132:135], a[16:19]\n" \
"ds_read_b64_tr_b16 a[184:185], v38 offset:0\n" \
"v_accvgpr_read_b32 v180, a92\n" \
"v_mfma_f32_16x16x32_bf16 a[16:19], a[224:227], a[196:199], a[16:19]\n" \
"ds_read_b64_tr_b16 a[186:187], v38 offset:1024\n" \
"v_accvgpr_read_b32 v181, a93\n" \
"v_mfma_f32_16x16x32_bf16 a[32:35], a[160:163], a[136:139], a[32:35]\n" \
"ds_read_b64_tr_b16 a[248:249], v38 offset:8192\n" \
"v_accvgpr_read_b32 v182, a94\n" \
"v_mfma_f32_16x16x32_bf16 a[32:35], a[224:227], a[200:203], a[32:35]\n" \
"ds_read_b64_tr_b16 a[250:251], v38 offset:9216\n" \
"v_accvgpr_read_b32 v183, a95\n" \
"v_mfma_f32_16x16x32_bf16 a[36:39], a[164:167], a[136:139], a[36:39]\n" \
"v_cvt_pk_bf16_f32 v188, v188, v189\n" \
"v_cvt_pk_bf16_f32 v189, v190, v191\n" \
"v_mfma_f32_16x16x32_bf16 a[36:39], a[228:231], a[200:203], a[36:39]\n" \
"s_add_u32 s88, s42, 0x1000\n" \
"buffer_store_dwordx4 v[252:255], v20, s[80:83], s88 offen offset:2048 sc1\n" \
"v_mfma_f32_16x16x32_bf16 a[44:47], a[172:175], a[136:139], a[44:47]\n" \
"ds_read_b64_tr_b16 a[188:189], v39 offset:0\n" \
"v_cvt_pk_bf16_f32 v190, v180, v181\n" \
"v_mfma_f32_16x16x32_bf16 a[44:47], a[236:239], a[200:203], a[44:47]\n" \
"ds_read_b64_tr_b16 a[190:191], v39 offset:1024\n" \
"v_cvt_pk_bf16_f32 v191, v182, v183\n" \
"v_mfma_f32_16x16x32_bf16 a[40:43], a[136:139], a[168:171], a[40:43]\n" \
"ds_read_b64_tr_b16 a[252:253], v39 offset:8192\n" \
"v_accvgpr_read_b32 v184, a72\n" \
"v_mfma_f32_16x16x32_bf16 a[40:43], a[200:203], a[232:235], a[40:43]\n" \
"v_accvgpr_read_b32 v185, a73\n" \
"v_accvgpr_read_b32 v186, a74\n" \
"v_mfma_f32_16x16x32_bf16 a[56:59], a[140:143], a[168:171], a[56:59]\n" \
"ds_read_b64_tr_b16 a[254:255], v39 offset:9216\n" \
"v_accvgpr_read_b32 v187, a75\n" \
"v_mfma_f32_16x16x32_bf16 a[56:59], a[204:207], a[232:235], a[56:59]\n" \
"v_accvgpr_read_b32 v180, a76\n" \
"v_accvgpr_read_b32 v181, a77\n" \
"v_mfma_f32_16x16x32_bf16 a[60:63], a[172:175], a[140:143], a[60:63]\n" \
"s_add_u32 s89, s42, 0x2000\n" \
"buffer_store_dwordx4 v[200:203], v20, s[80:83], s89 offen offset:2048 sc1\n" \
"v_mfma_f32_16x16x32_bf16 a[60:63], a[236:239], a[204:207], a[60:63]\n" \
"v_accvgpr_read_b32 v182, a78\n" \
"v_accvgpr_read_b32 v183, a79\n" \
"v_mfma_f32_16x16x32_bf16 a[52:55], a[164:167], a[140:143], a[52:55]\n" \
"v_cvt_pk_bf16_f32 v184, v184, v185\n" \
"v_cvt_pk_bf16_f32 v185, v186, v187\n" \
"v_mfma_f32_16x16x32_bf16 a[52:55], a[228:231], a[204:207], a[52:55]\n" \
"v_cvt_pk_bf16_f32 v186, v180, v181\n" \
"v_cvt_pk_bf16_f32 v187, v182, v183\n" \
"v_mfma_f32_16x16x32_bf16 a[48:51], a[160:163], a[140:143], a[48:51]\n" \
"s_add_i32 s20, s19, s16\n" \
"v_writelane_b32 v11, s20, 30\n" \
"v_mfma_f32_16x16x32_bf16 a[48:51], a[224:227], a[204:207], a[48:51]\n" \
"s_lshl_b32 s13, s13, 12\n" \
"s_lshl_b32 s15, s15, 12\n" \
"s_waitcnt lgkmcnt(15)\n" \
"v_mfma_f32_16x16x32_bf16 a[64:67], a[164:167], v[248:251], a[64:67]\n" \
"s_sub_i32 s14, s9, s14\n" \
"s_add_i32 s20, s19, s13\n" \
"v_mfma_f32_16x16x32_bf16 a[64:67], a[228:231], v[48:51], a[64:67]\n" \
"v_writelane_b32 v11, s20, 31\n" \
"s_add_i32 s19, s19, s15\n" \
"v_mfma_f32_16x16x32_bf16 a[80:83], a[172:175], v[248:251], a[80:83]\n" \
"s_mul_i32 s14, s14, 0x24800\n" \
"v_writelane_b32 v11, s19, 32\n" \
"v_mfma_f32_16x16x32_bf16 a[80:83], a[236:239], v[48:51], a[80:83]\n" \
"s_add_i32 s19, s14, 0x4120800\n" \
"s_add_i32 s16, s19, s16\n" \
"v_mfma_f32_16x16x32_bf16 a[68:71], v[248:251], a[168:171], a[68:71]\n" \
"v_writelane_b32 v11, s16, 33\n" \
"s_add_i32 s13, s19, s13\n" \
"v_mfma_f32_16x16x32_bf16 a[68:71], v[48:51], a[232:235], a[68:71]\n" \
"s_waitcnt vmcnt(3) lgkmcnt(0)\n" \
"s_barrier\n" \
"v_mfma_f32_16x16x32_bf16 v[52:55], a[176:179], a[128:131], v[52:55]\n" \
"s_add_u32 m0, s40, 0x0\n" \
"s_add_u32 s85, s39, 0xc000\n" \
"v_mfma_f32_16x16x32_bf16 v[52:55], a[240:243], a[192:195], v[52:55]\n" \
"buffer_load_dwordx4 v20, s[80:83], s85 offen sc1 lds\n" \
"v_mfma_f32_16x16x32_bf16 v[56:59], a[180:183], a[128:131], v[56:59]\n" \
"ds_read_b64_tr_b16 a[160:161], v32 offset:32768\n" \
"v_writelane_b32 v11, s13, 34\n" \
"v_mfma_f32_16x16x32_bf16 v[56:59], a[244:247], a[192:195], v[56:59]\n" \
"ds_read_b64_tr_b16 a[162:163], v32 offset:33280\n" \
"s_add_i32 s19, s19, s15\n" \
"v_mfma_f32_16x16x32_bf16 v[60:63], a[184:187], a[128:131], v[60:63]\n" \
"s_add_u32 m0, s40, 0x1000\n" \
"s_add_u32 s85, s39, 0xd000\n" \
"v_mfma_f32_16x16x32_bf16 v[60:63], a[248:251], a[192:195], v[60:63]\n" \
"buffer_load_dwordx4 v20, s[80:83], s85 offen sc1 lds\n" \
"v_mfma_f32_16x16x32_bf16 v[64:67], a[188:191], a[128:131], v[64:67]\n" \
"ds_read_b64_tr_b16 a[224:225], v32 offset:36864\n" \
"s_or_b32 s13, s18, 0x3000000\n" \
"v_mfma_f32_16x16x32_bf16 v[64:67], a[252:255], a[192:195], v[64:67]\n" \
"ds_read_b128 a[128:131], v28 offset:32768\n" \
"s_lshl_b32 s15, s6, 11\n" \
"v_mfma_f32_16x16x32_bf16 v[80:83], a[188:191], a[132:135], v[80:83]\n" \
"ds_read_b128 a[192:195], v29 offset:32768\n" \
"v_writelane_b32 v11, s19, 35\n" \
"v_mfma_f32_16x16x32_bf16 v[80:83], a[252:255], a[196:199], v[80:83]\n" \
"s_add_u32 m0, s40, 0x3000\n" \
"s_movk_i32 s84, 0x300\n" \
".fill (L_pb + 28384 - .) / 4, 4, 0xBF800000\n" \
"L_pp7:\n" \
"s_cbranch_execz L_pret\n" \
"v_mfma_f32_16x16x32_bf16 v[76:79], a[184:187], a[132:135], v[76:79]\n" \
"buffer_load_dwordx4 v21, s[76:79], s84 offen offset:0 lds\n" \
"v_mfma_f32_16x16x32_bf16 v[76:79], a[248:251], a[196:199], v[76:79]\n" \
"ds_read_b64_tr_b16 a[226:227], v32 offset:37376\n" \
"s_add_i32 s16, s13, s15\n" \
"v_mfma_f32_16x16x32_bf16 v[72:75], a[180:183], a[132:135], v[72:75]\n" \
"s_add_u32 m0, s30, 0x0\n" \
"s_movk_i32 s84, 0x300\n" \
"v_mfma_f32_16x16x32_bf16 v[72:75], a[244:247], a[196:199], v[72:75]\n" \
"buffer_load_dwordx4 v24, s[76:79], s84 offen offset:0 lds\n" \
"v_mfma_f32_16x16x32_bf16 v[68:71], a[176:179], a[132:135], v[68:71]\n" \
"ds_read_b64_tr_b16 a[164:165], v33 offset:32768\n" \
"v_writelane_b32 v11, s16, 36\n" \
"v_mfma_f32_16x16x32_bf16 v[68:71], a[240:243], a[196:199], v[68:71]\n" \
"ds_read_b128 a[132:135], v28 offset:34816\n" \
"s_add_i32 s16, s14, 0x411c000\n" \
"v_mfma_f32_16x16x32_bf16 v[84:87], a[176:179], a[136:139], v[84:87]\n" \
"ds_read_b128 a[196:199], v29 offset:34816\n" \
"s_add_i32 s19, s16, s15\n" \
"v_mfma_f32_16x16x32_bf16 v[84:87], a[240:243], a[200:203], v[84:87]\n" \
"s_movk_i32 s84, 0x2300\n" \
"v_mfma_f32_16x16x32_bf16 v[88:91], a[180:183], a[136:139], v[88:91]\n" \
"buffer_load_dwordx4 v25, s[76:79], s84 offen offset:1024 lds\n" \
"v_mfma_f32_16x16x32_bf16 v[88:91], a[244:247], a[200:203], v[88:91]\n" \
"ds_read_b64_tr_b16 a[166:167], v33 offset:33280\n" \
"v_writelane_b32 v11, s19, 37\n" \
"v_mfma_f32_16x16x32_bf16 v[92:95], a[184:187], a[136:139], v[92:95]\n" \
"ds_read_b64_tr_b16 a[228:229], v33 offset:36864\n" \
"s_add_i32 s18, s18, 0x3004000\n" \
"v_mfma_f32_16x16x32_bf16 v[92:95], a[248:251], a[200:203], v[92:95]\n" \
"s_movk_i32 s84, 0x4300\n" \
"v_mfma_f32_16x16x32_bf16 v[96:99], a[188:191], a[136:139], v[96:99]\n" \
"buffer_load_dwordx4 v24, s[76:79], s84 offen offset:2048 lds\n" \
"v_mfma_f32_16x16x32_bf16 v[96:99], a[252:255], a[200:203], v[96:99]\n" \
"ds_read_b128 a[136:139], v28 offset:36864\n" \
"v_writelane_b32 v11, s18, 38\n" \
"v_mfma_f32_16x16x32_bf16 v[112:115], a[188:191], a[140:143], v[112:115]\n" \
"ds_read_b128 a[200:203], v29 offset:36864\n" \
"s_add_i32 s14, s14, 0x4120000\n" \
"v_mfma_f32_16x16x32_bf16 v[112:115], a[252:255], a[204:207], v[112:115]\n" \
"s_movk_i32 s84, 0x6300\n" \
"v_mfma_f32_16x16x32_bf16 v[108:111], a[184:187], a[140:143], v[108:111]\n" \
"buffer_load_dwordx4 v25, s[76:79], s84 offen offset:3072 lds\n" \
"v_mfma_f32_16x16x32_bf16 v[108:111], a[248:251], a[204:207], v[108:111]\n" \
"ds_read_b64_tr_b16 a[230:231], v33 offset:37376\n" \
"v_writelane_b32 v11, s14, 39\n" \
"v_mfma_f32_16x16x32_bf16 v[104:107], a[180:183], a[140:143], v[104:107]\n" \
"ds_read_b64_tr_b16 a[172:173], v35 offset:32768\n" \
"s_lshl_b32 s14, s4, 2\n" \
"v_mfma_f32_16x16x32_bf16 v[104:107], a[244:247], a[204:207], v[104:107]\n" \
"ds_read_b64_tr_b16 a[174:175], v35 offset:33280\n" \
"s_or_b32 s8, s14, s8\n" \
"v_mfma_f32_16x16x32_bf16 v[100:103], a[176:179], a[140:143], v[100:103]\n" \
"ds_read_b64_tr_b16 a[236:237], v35 offset:36864\n" \
"s_mul_i32 s8, s8, 0x90000\n" \
"v_mfma_f32_16x16x32_bf16 v[100:103], a[240:243], a[204:207], v[100:103]\n" \
"ds_read_b128 a[140:143], v28 offset:38912\n" \
"s_lshl_b32 s14, s3, 14\n" \
"v_mfma_f32_16x16x32_bf16 v[116:119], a[176:179], a[144:147], v[116:119]\n" \
"ds_read_b128 a[204:207], v29 offset:38912\n" \
"s_add_i32 s8, s8, s14\n" \
"v_mfma_f32_16x16x32_bf16 v[116:119], a[240:243], a[208:211], v[116:119]\n" \
"s_add_u32 m0, s31, 0x0\n" \
"s_mov_b32 s84, 0x300000\n" \
"v_mfma_f32_16x16x32_bf16 v[120:123], a[180:183], a[144:147], v[120:123]\n" \
"buffer_load_dwordx4 v26, s[68:71], s84 offen offset:0 lds\n" \
"v_mfma_f32_16x16x32_bf16 v[120:123], a[244:247], a[208:211], v[120:123]\n" \
"s_addk_i32 s84, 0x7c00\n" \
"v_mfma_f32_16x16x32_bf16 v[124:127], a[184:187], a[144:147], v[124:127]\n" \
"buffer_load_dwordx4 v26, s[68:71], s84 offen offset:1024 lds\n" \
"v_mfma_f32_16x16x32_bf16 v[124:127], a[248:251], a[208:211], v[124:127]\n" \
"ds_read_b64_tr_b16 a[238:239], v35 offset:37376\n" \
"s_lshl_b32 s9, s9, 4\n" \
"v_mfma_f32_16x16x32_bf16 v[128:131], a[188:191], a[144:147], v[128:131]\n" \
"ds_read_b64_tr_b16 a[168:169], v34 offset:32768\n" \
"s_lshl_b32 s14, s4, 7\n" \
"v_mfma_f32_16x16x32_bf16 v[128:131], a[252:255], a[208:211], v[128:131]\n" \
"ds_read_b128 a[144:147], v28 offset:40960\n" \
"s_or_b32 s18, s9, s14\n" \
"v_mfma_f32_16x16x32_bf16 v[144:147], a[188:191], a[148:151], v[144:147]\n" \
"ds_read_b128 a[208:211], v29 offset:40960\n" \
"s_sub_i32 s9, s9, s14\n" \
"v_mfma_f32_16x16x32_bf16 v[144:147], a[252:255], a[212:215], v[144:147]\n" \
"s_addk_i32 s84, 0x7c00\n" \
"v_mfma_f32_16x16x32_bf16 v[140:143], a[184:187], a[148:151], v[140:143]\n" \
"buffer_load_dwordx4 v27, s[68:71], s84 offen offset:2048 lds\n" \
"v_mfma_f32_16x16x32_bf16 v[140:143], a[248:251], a[212:215], v[140:143]\n" \
"s_addk_i32 s84, 0x7c00\n" \
"v_mfma_f32_16x16x32_bf16 v[136:139], a[180:183], a[148:151], v[136:139]\n" \
"buffer_load_dwordx4 v27, s[68:71], s84 offen offset:3072 lds\n" \
"v_mfma_f32_16x16x32_bf16 v[136:139], a[244:247], a[212:215], v[136:139]\n" \
"ds_read_b64_tr_b16 a[170:171], v34 offset:33280\n" \
"s_mulk_i32 s18, 0x480\n" \
"v_mfma_f32_16x16x32_bf16 v[132:135], a[176:179], a[148:151], v[132:135]\n" \
"ds_read_b64_tr_b16 a[232:233], v34 offset:36864\n" \
"s_mulk_i32 s9, 0x480\n" \
"v_mfma_f32_16x16x32_bf16 v[132:135], a[240:243], a[212:215], v[132:135]\n" \
"ds_read_b128 a[148:151], v28 offset:43008\n" \
"s_or_b32 s14, s18, s12\n" \
"v_mfma_f32_16x16x32_bf16 v[148:151], a[176:179], a[152:155], v[148:151]\n" \
"ds_read_b128 a[212:215], v29 offset:43008\n" \
"s_or_b32 s9, s9, s12\n" \
"v_mfma_f32_16x16x32_bf16 v[148:151], a[240:243], a[216:219], v[148:151]\n" \
"s_add_u32 m0, s30, 0x1000\n" \
"s_mov_b32 s84, 0x9300\n" \
"v_mfma_f32_16x16x32_bf16 v[152:155], a[180:183], a[152:155], v[152:155]\n" \
"buffer_load_dwordx4 v24, s[76:79], s84 offen offset:0 lds\n" \
"v_mfma_f32_16x16x32_bf16 v[152:155], a[244:247], a[216:219], v[152:155]\n" \
"s_addk_i32 s84, 0x2000\n" \
"v_mfma_f32_16x16x32_bf16 v[156:159], a[184:187], a[152:155], v[156:159]\n" \
"buffer_load_dwordx4 v25, s[76:79], s84 offen offset:1024 lds\n" \
"v_mfma_f32_16x16x32_bf16 v[156:159], a[248:251], a[216:219], v[156:159]\n" \
"ds_read_b64_tr_b16 a[234:235], v34 offset:37376\n" \
"s_and_b32 s12, s2, 1\n" \
"v_mfma_f32_16x16x32_bf16 v[160:163], a[188:191], a[152:155], v[160:163]\n" \
"ds_read_b128 v[248:251], v22 offset:32768\n" \
"v_mfma_f32_16x16x32_bf16 v[160:163], a[252:255], a[216:219], v[160:163]\n" \
"ds_read_b128 a[152:155], v28 offset:45056\n" \
"v_mfma_f32_16x16x32_bf16 v[176:179], a[188:191], a[156:159], v[176:179]\n" \
"ds_read_b128 a[216:219], v29 offset:45056\n" \
"v_mfma_f32_16x16x32_bf16 v[176:179], a[252:255], a[220:223], v[176:179]\n" \
"ds_read_b128 v[48:51], v23 offset:32768\n" \
"v_mfma_f32_16x16x32_bf16 v[172:175], a[184:187], a[156:159], v[172:175]\n" \
"s_cmp_lg_u32 s12, 0\n" \
"s_cselect_b32 s13, s16, s13\n" \
"v_mfma_f32_16x16x32_bf16 v[172:175], a[248:251], a[220:223], v[172:175]\n" \
"s_bfe_u32 s16, s2, 0x20001\n" \
"s_addk_i32 s13, 0x4000\n" \
"v_mfma_f32_16x16x32_bf16 v[168:171], a[180:183], a[156:159], v[168:171]\n" \
"s_xor_b32 s16, s16, s3\n" \
"v_mfma_f32_16x16x32_bf16 v[168:171], a[244:247], a[220:223], v[168:171]\n" \
"s_cmp_lg_u32 s16, 0\n" \
"s_cselect_b32 s13, s7, s13\n" \
"v_mfma_f32_16x16x32_bf16 v[164:167], a[176:179], a[156:159], v[164:167]\n" \
"s_lshl_b32 s6, s6, 16\n" \
"v_writelane_b32 v11, s13, 40\n" \
"v_mfma_f32_16x16x32_bf16 v[164:167], a[240:243], a[220:223], v[164:167]\n" \
"ds_read_b128 a[156:159], v28 offset:47104\n" \
"ds_read_b128 a[220:223], v29 offset:47104\n" \
"s_waitcnt lgkmcnt(15)\n" \
"v_mfma_f32_16x16x32_bf16 a[0:3], a[160:163], a[128:131], a[0:3]\n" \
"s_addk_i32 s84, 0x2000\n" \
"v_mfma_f32_16x16x32_bf16 a[0:3], a[224:227], a[192:195], a[0:3]\n" \
"buffer_load_dwordx4 v24, s[76:79], s84 offen offset:2048 lds\n" \
"v_mfma_f32_16x16x32_bf16 a[4:7], a[164:167], a[128:131], a[4:7]\n" \
"ds_read_b64_tr_b16 a[176:177], v36 offset:32768\n" \
"s_add_i32 s13, s8, 0x4240000\n" \
"v_mfma_f32_16x16x32_bf16 a[4:7], a[228:231], a[192:195], a[4:7]\n" \
"s_addk_i32 s84, 0x2000\n" \
"v_mfma_f32_16x16x32_bf16 a[12:15], a[172:175], a[128:131], a[12:15]\n" \
"buffer_load_dwordx4 v25, s[76:79], s84 offen offset:3072 lds\n" \
"s_waitcnt lgkmcnt(15)\n" \
"v_mfma_f32_16x16x32_bf16 a[12:15], a[236:239], a[192:195], a[12:15]\n" \
"ds_read_b64_tr_b16 a[178:179], v36 offset:33792\n" \
"s_add_i32 s6, s13, s6\n" \
"s_waitcnt lgkmcnt(12)\n" \
"v_mfma_f32_16x16x32_bf16 a[8:11], a[128:131], a[168:171], a[8:11]\n" \
"ds_read_b64_tr_b16 a[240:241], v36 offset:40960\n" \
"v_writelane_b32 v11, s6, 41\n" \
"s_waitcnt lgkmcnt(9)\n" \
"v_mfma_f32_16x16x32_bf16 a[8:11], a[192:195], a[232:235], a[8:11]\n" \
"ds_read_b64_tr_b16 a[242:243], v36 offset:41984\n" \
"s_add_i32 s8, s8, 0x42c0000\n" \
"v_mfma_f32_16x16x32_bf16 a[24:27], a[132:135], a[168:171], a[24:27]\n" \
"ds_read_b64_tr_b16 a[180:181], v37 offset:32768\n" \
"s_lshl_b32 s6, s12, 10\n" \
"v_mfma_f32_16x16x32_bf16 a[24:27], a[196:199], a[232:235], a[24:27]\n" \
"ds_read_b64_tr_b16 a[182:183], v37 offset:33792\n" \
"s_or_b32 s6, s8, s6\n" \
"v_mfma_f32_16x16x32_bf16 a[28:31], a[172:175], a[132:135], a[28:31]\n" \
"ds_read_b64_tr_b16 a[244:245], v37 offset:40960\n" \
"v_mfma_f32_16x16x32_bf16 a[28:31], a[236:239], a[196:199], a[28:31]\n" \
"ds_read_b64_tr_b16 a[246:247], v37 offset:41984\n" \
"v_mfma_f32_16x16x32_bf16 a[20:23], a[164:167], a[132:135], a[20:23]\n" \
"s_cmp_lg_u32 s16, 0\n" \
"s_cselect_b32 s6, s7, s6\n" \
"v_mfma_f32_16x16x32_bf16 a[20:23], a[228:231], a[196:199], a[20:23]\n" \
"s_add_u32 s88, s42, 0x2000\n" \
"buffer_store_dwordx4 v[204:207], v20, s[80:83], s88 offen offset:3072 sc1\n" \
"v_mfma_f32_16x16x32_bf16 a[16:19], a[160:163], a[132:135], a[16:19]\n" \
"ds_read_b64_tr_b16 a[184:185], v38 offset:32768\n" \
"s_mulk_i32 s4, 0x4800\n" \
"v_mfma_f32_16x16x32_bf16 a[16:19], a[224:227], a[196:199], a[16:19]\n" \
"ds_read_b64_tr_b16 a[186:187], v38 offset:33792\n" \
"v_writelane_b32 v11, s6, 42\n" \
"v_mfma_f32_16x16x32_bf16 a[32:35], a[160:163], a[136:139], a[32:35]\n" \
"ds_read_b64_tr_b16 a[248:249], v38 offset:40960\n" \
"s_add_i32 s6, s14, s10\n" \
"v_mfma_f32_16x16x32_bf16 a[32:35], a[224:227], a[200:203], a[32:35]\n" \
"ds_read_b64_tr_b16 a[250:251], v38 offset:41984\n" \
"v_writelane_b32 v11, s6, 43\n" \
"v_mfma_f32_16x16x32_bf16 a[36:39], a[164:167], a[136:139], a[36:39]\n" \
"s_add_i32 s6, s9, 0x21c000\n" \
"s_add_i32 s9, s6, s10\n" \
"v_mfma_f32_16x16x32_bf16 a[36:39], a[228:231], a[200:203], a[36:39]\n" \
"s_add_u32 s89, s42, 0x3000\n" \
"buffer_store_dwordx4 v[208:211], v20, s[80:83], s89 offen offset:3072 sc1\n" \
"v_mfma_f32_16x16x32_bf16 a[44:47], a[172:175], a[136:139], a[44:47]\n" \
"ds_read_b64_tr_b16 a[188:189], v39 offset:32768\n" \
"v_mfma_f32_16x16x32_bf16 a[44:47], a[236:239], a[200:203], a[44:47]\n" \
"ds_read_b64_tr_b16 a[190:191], v39 offset:33792\n" \
"v_mfma_f32_16x16x32_bf16 a[40:43], a[136:139], a[168:171], a[40:43]\n" \
"ds_read_b64_tr_b16 a[252:253], v39 offset:40960\n" \
"v_mfma_f32_16x16x32_bf16 a[40:43], a[200:203], a[232:235], a[40:43]\n" \
"s_cmp_lg_u32 s12, 0\n" \
"s_cselect_b32 s6, s6, s14\n" \
"v_mfma_f32_16x16x32_bf16 a[56:59], a[140:143], a[168:171], a[56:59]\n" \
"ds_read_b64_tr_b16 a[254:255], v39 offset:41984\n" \
"s_addk_i32 s6, 0x100\n" \
"v_mfma_f32_16x16x32_bf16 a[56:59], a[204:207], a[232:235], a[56:59]\n" \
"v_writelane_b32 v11, s9, 44\n" \
"v_mfma_f32_16x16x32_bf16 a[60:63], a[172:175], a[140:143], a[60:63]\n" \
"s_add_u32 s88, s42, 0x3000\n" \
"buffer_store_dwordx4 v[212:215], v20, s[80:83], s88 offen offset:2048 sc1\n" \
"v_mfma_f32_16x16x32_bf16 a[60:63], a[236:239], a[204:207], a[60:63]\n" \
"s_cmp_lg_u32 s16, 0\n" \
"s_cselect_b32 s6, s7, s6\n" \
"v_mfma_f32_16x16x32_bf16 a[52:55], a[164:167], a[140:143], a[52:55]\n" \
"v_writelane_b32 v11, s6, 45\n" \
"s_or_b32 s6, s8, s15\n" \
"v_mfma_f32_16x16x32_bf16 a[52:55], a[228:231], a[204:207], a[52:55]\n" \
"v_writelane_b32 v11, s6, 53\n" \
"s_lshl_b32 s6, s3, 17\n" \
"v_mfma_f32_16x16x32_bf16 a[48:51], a[160:163], a[140:143], a[48:51]\n" \
"s_add_i32 s6, s13, s6\n" \
"s_or_b32 s5, s6, s5\n" \
"v_mfma_f32_16x16x32_bf16 a[48:51], a[224:227], a[204:207], a[48:51]\n" \
"s_or_b32 s5, s5, 0x3840\n" \
"v_writelane_b32 v11, s5, 6\n" \
"s_waitcnt lgkmcnt(15)\n" \
"v_mfma_f32_16x16x32_bf16 a[64:67], a[164:167], v[248:251], a[64:67]\n" \
"s_mov_b32 s5, 0xff0000\n" \
"v_mfma_f32_16x16x32_bf16 a[64:67], a[228:231], v[48:51], a[64:67]\n" \
"s_cmp_lg_u32 s16, 0\n" \
"s_cselect_b32 s5, s17, s5\n" \
"v_mfma_f32_16x16x32_bf16 a[80:83], a[172:175], v[248:251], a[80:83]\n" \
"v_writelane_b32 v11, s5, 7\n" \
"s_mul_i32 s5, s3, 0x1200\n" \
"v_mfma_f32_16x16x32_bf16 a[80:83], a[236:239], v[48:51], a[80:83]\n" \
"s_add_i32 s4, s5, s4\n" \
"s_add_i32 s4, s4, s11\n" \
"v_mfma_f32_16x16x32_bf16 a[68:71], v[248:251], a[168:171], a[68:71]\n" \
"v_writelane_b32 v11, s4, 8\n" \
"s_lshl_b32 s4, s2, 16\n" \
"v_mfma_f32_16x16x32_bf16 a[68:71], v[48:51], a[232:235], a[68:71]\n" \
"s_waitcnt vmcnt(3) lgkmcnt(0)\n" \
"s_barrier\n" \
"v_mfma_f32_16x16x32_bf16 v[52:55], a[176:179], a[128:131], v[52:55]\n" \
"s_add_u32 m0, s40, 0x8000\n" \
"s_add_u32 s85, s39, 0xe000\n" \
"v_mfma_f32_16x16x32_bf16 v[52:55], a[240:243], a[192:195], v[52:55]\n" \
"buffer_load_dwordx4 v20, s[80:83], s85 offen sc1 lds\n" \
"v_mfma_f32_16x16x32_bf16 v[56:59], a[180:183], a[128:131], v[56:59]\n" \
"ds_read_b64_tr_b16 a[160:161], v32 offset:0\n" \
"s_add_i32 s5, s4, 0x10000\n" \
"v_mfma_f32_16x16x32_bf16 v[56:59], a[244:247], a[192:195], v[56:59]\n" \
"ds_read_b64_tr_b16 a[162:163], v32 offset:512\n" \
"s_and_b32 s5, s5, 0x70000\n" \
"v_mfma_f32_16x16x32_bf16 v[60:63], a[184:187], a[128:131], v[60:63]\n" \
"s_add_u32 m0, s40, 0x9000\n" \
"s_add_u32 s85, s39, 0xf000\n" \
"v_mfma_f32_16x16x32_bf16 v[60:63], a[248:251], a[192:195], v[60:63]\n" \
"buffer_load_dwordx4 v20, s[80:83], s85 offen sc1 lds\n" \
"v_mfma_f32_16x16x32_bf16 v[64:67], a[188:191], a[128:131], v[64:67]\n" \
"ds_read_b64_tr_b16 a[224:225], v32 offset:4096\n" \
"s_add_i32 s5, s13, s5\n" \
"v_mfma_f32_16x16x32_bf16 v[64:67], a[252:255], a[192:195], v[64:67]\n" \
"ds_read_b128 a[128:131], v28 offset:0\n" \
"v_writelane_b32 v11, s5, 46\n" \
"v_mfma_f32_16x16x32_bf16 v[80:83], a[188:191], a[132:135], v[80:83]\n" \
"ds_read_b128 a[192:195], v29 offset:0\n" \
"s_add_i32 s5, s4, 0x20000\n" \
"v_mfma_f32_16x16x32_bf16 v[80:83], a[252:255], a[196:199], v[80:83]\n" \
"s_add_u32 m0, s40, 0xb000\n" \
"s_movk_i32 s84, 0x380\n" \
"v_mfma_f32_16x16x32_bf16 v[76:79], a[184:187], a[132:135], v[76:79]\n" \
"buffer_load_dwordx4 v21, s[76:79], s84 offen offset:0 lds\n" \
"v_mfma_f32_16x16x32_bf16 v[76:79], a[248:251], a[196:199], v[76:79]\n" \
"ds_read_b64_tr_b16 a[226:227], v32 offset:4608\n" \
"s_and_b32 s5, s5, 0x70000\n" \
"v_mfma_f32_16x16x32_bf16 v[72:75], a[180:183], a[132:135], v[72:75]\n" \
"s_add_u32 m0, s30, 0x8000\n" \
"s_movk_i32 s84, 0x380\n" \
"v_mfma_f32_16x16x32_bf16 v[72:75], a[244:247], a[196:199], v[72:75]\n" \
"buffer_load_dwordx4 v24, s[76:79], s84 offen offset:0 lds\n" \
"v_mfma_f32_16x16x32_bf16 v[68:71], a[176:179], a[132:135], v[68:71]\n" \
"ds_read_b64_tr_b16 a[164:165], v33 offset:0\n" \
"s_add_i32 s5, s13, s5\n" \
"v_mfma_f32_16x16x32_bf16 v[68:71], a[240:243], a[196:199], v[68:71]\n" \
"ds_read_b128 a[132:135], v28 offset:2048\n" \
"s_bitset1_b32 s5, 11\n" \
"v_mfma_f32_16x16x32_bf16 v[84:87], a[176:179], a[136:139], v[84:87]\n" \
"ds_read_b128 a[196:199], v29 offset:2048\n" \
"v_writelane_b32 v11, s5, 47\n" \
"v_mfma_f32_16x16x32_bf16 v[84:87], a[240:243], a[200:203], v[84:87]\n" \
"s_movk_i32 s84, 0x2380\n" \
"v_mfma_f32_16x16x32_bf16 v[88:91], a[180:183], a[136:139], v[88:91]\n" \
"buffer_load_dwordx4 v25, s[76:79], s84 offen offset:1024 lds\n" \
"v_mfma_f32_16x16x32_bf16 v[88:91], a[244:247], a[200:203], v[88:91]\n" \
"ds_read_b64_tr_b16 a[166:167], v33 offset:512\n" \
"s_add_i32 s5, s4, 0x30000\n" \
"v_mfma_f32_16x16x32_bf16 v[92:95], a[184:187], a[136:139], v[92:95]\n" \
"ds_read_b64_tr_b16 a[228:229], v33 offset:4096\n" \
"s_and_b32 s5, s5, 0x70000\n" \
"v_mfma_f32_16x16x32_bf16 v[92:95], a[248:251], a[200:203], v[92:95]\n" \
"s_movk_i32 s84, 0x4380\n" \
"v_mfma_f32_16x16x32_bf16 v[96:99], a[188:191], a[136:139], v[96:99]\n" \
"buffer_load_dwordx4 v24, s[76:79], s84 offen offset:2048 lds\n" \
"v_mfma_f32_16x16x32_bf16 v[96:99], a[252:255], a[200:203], v[96:99]\n" \
"ds_read_b128 a[136:139], v28 offset:4096\n" \
"s_add_i32 s5, s13, s5\n" \
"v_mfma_f32_16x16x32_bf16 v[112:115], a[188:191], a[140:143], v[112:115]\n" \
"ds_read_b128 a[200:203], v29 offset:4096\n" \
"s_bitset1_b32 s5, 12\n" \
"v_mfma_f32_16x16x32_bf16 v[112:115], a[252:255], a[204:207], v[112:115]\n" \
"s_movk_i32 s84, 0x6380\n" \
"v_mfma_f32_16x16x32_bf16 v[108:111], a[184:187], a[140:143], v[108:111]\n" \
"buffer_load_dwordx4 v25, s[76:79], s84 offen offset:3072 lds\n" \
"v_mfma_f32_16x16x32_bf16 v[108:111], a[248:251], a[204:207], v[108:111]\n" \
"ds_read_b64_tr_b16 a[230:231], v33 offset:4608\n" \
"v_writelane_b32 v11, s5, 48\n" \
"v_mfma_f32_16x16x32_bf16 v[104:107], a[180:183], a[140:143], v[104:107]\n" \
"ds_read_b64_tr_b16 a[172:173], v35 offset:0\n" \
"s_and_b32 s5, s4, 0x70000\n" \
"v_mfma_f32_16x16x32_bf16 v[104:107], a[244:247], a[204:207], v[104:107]\n" \
"ds_read_b64_tr_b16 a[174:175], v35 offset:512\n" \
"s_xor_b32 s5, s5, 0x40000\n" \
"v_mfma_f32_16x16x32_bf16 v[100:103], a[176:179], a[140:143], v[100:103]\n" \
"ds_read_b64_tr_b16 a[236:237], v35 offset:4096\n" \
"s_add_i32 s5, s13, s5\n" \
"s_nop 0\n" \
"v_mfma_f32_16x16x32_bf16 v[100:103], a[240:243], a[204:207], v[100:103]\n" \
"ds_read_b128 a[140:143], v28 offset:6144\n" \
"s_or_b32 s5, s5, 0x1800\n" \
"v_mfma_f32_16x16x32_bf16 v[116:119], a[176:179], a[144:147], v[116:119]\n" \
"ds_read_b128 a[204:207], v29 offset:6144\n" \
"v_writelane_b32 v11, s5, 49\n" \
"v_mfma_f32_16x16x32_bf16 v[116:119], a[240:243], a[208:211], v[116:119]\n" \
"s_add_u32 m0, s31, 0x8000\n" \
"s_mov_b32 s84, 0x380000\n" \
"v_mfma_f32_16x16x32_bf16 v[120:123], a[180:183], a[144:147], v[120:123]\n" \
"buffer_load_dwordx4 v26, s[68:71], s84 offen offset:0 lds\n" \
"v_mfma_f32_16x16x32_bf16 v[120:123], a[244:247], a[208:211], v[120:123]\n" \
"s_addk_i32 s84, 0x7c00\n" \
"v_mfma_f32_16x16x32_bf16 v[124:127], a[184:187], a[144:147], v[124:127]\n" \
"buffer_load_dwordx4 v26, s[68:71], s84 offen offset:1024 lds\n" \
"v_mfma_f32_16x16x32_bf16 v[124:127], a[248:251], a[208:211], v[124:127]\n" \
"ds_read_b64_tr_b16 a[238:239], v35 offset:4608\n" \
"s_add_i32 s5, s4, 0x50000\n" \
"v_mfma_f32_16x16x32_bf16 v[128:131], a[188:191], a[144:147], v[128:131]\n" \
"ds_read_b64_tr_b16 a[168:169], v34 offset:0\n" \
"s_and_b32 s5, s5, 0x70000\n" \
"v_mfma_f32_16x16x32_bf16 v[128:131], a[252:255], a[208:211], v[128:131]\n" \
"ds_read_b128 a[144:147], v28 offset:8192\n" \
"s_add_i32 s5, s13, s5\n" \
"v_mfma_f32_16x16x32_bf16 v[144:147], a[188:191], a[148:151], v[144:147]\n" \
"ds_read_b128 a[208:211], v29 offset:8192\n" \
"s_bitset1_b32 s5, 13\n" \
"v_mfma_f32_16x16x32_bf16 v[144:147], a[252:255], a[212:215], v[144:147]\n" \
"s_addk_i32 s84, 0x7c00\n" \
"v_mfma_f32_16x16x32_bf16 v[140:143], a[184:187], a[148:151], v[140:143]\n" \
"buffer_load_dwordx4 v27, s[68:71], s84 offen offset:2048 lds\n" \
"v_mfma_f32_16x16x32_bf16 v[140:143], a[248:251], a[212:215], v[140:143]\n" \
"s_addk_i32 s84, 0x7c00\n" \
"s_nop 0\n" \
"v_mfma_f32_16x16x32_bf16 v[136:139], a[180:183], a[148:151], v[136:139]\n" \
"buffer_load_dwordx4 v27, s[68:71], s84 offen offset:3072 lds\n" \
"v_mfma_f32_16x16x32_bf16 v[136:139], a[244:247], a[212:215], v[136:139]\n" \
"ds_read_b64_tr_b16 a[170:171], v34 offset:512\n" \
"v_writelane_b32 v11, s5, 50\n" \
"v_mfma_f32_16x16x32_bf16 v[132:135], a[176:179], a[148:151], v[132:135]\n" \
"ds_read_b64_tr_b16 a[232:233], v34 offset:4096\n" \
"s_add_i32 s5, s4, 0x60000\n" \
"v_mfma_f32_16x16x32_bf16 v[132:135], a[240:243], a[212:215], v[132:135]\n" \
"ds_read_b128 a[148:151], v28 offset:10240\n" \
"s_and_b32 s5, s5, 0x70000\n" \
"v_mfma_f32_16x16x32_bf16 v[148:151], a[176:179], a[152:155], v[148:151]\n" \
"ds_read_b128 a[212:215], v29 offset:10240\n" \
"s_add_i32 s4, s4, 0x70000\n" \
"v_mfma_f32_16x16x32_bf16 v[148:151], a[240:243], a[216:219], v[148:151]\n" \
"s_add_u32 m0, s30, 0x9000\n" \
"s_mov_b32 s84, 0x9380\n" \
"v_mfma_f32_16x16x32_bf16 v[152:155], a[180:183], a[152:155], v[152:155]\n" \
"buffer_load_dwordx4 v24, s[76:79], s84 offen offset:0 lds\n" \
"v_mfma_f32_16x16x32_bf16 v[152:155], a[244:247], a[216:219], v[152:155]\n" \
"s_addk_i32 s84, 0x2000\n" \
"v_mfma_f32_16x16x32_bf16 v[156:159], a[184:187], a[152:155], v[156:159]\n" \
"buffer_load_dwordx4 v25, s[76:79], s84 offen offset:1024 lds\n" \
"v_mfma_f32_16x16x32_bf16 v[156:159], a[248:251], a[216:219], v[156:159]\n" \
"ds_read_b64_tr_b16 a[234:235], v34 offset:4608\n" \
"s_add_i32 s5, s13, s5\n" \
"v_mfma_f32_16x16x32_bf16 v[160:163], a[188:191], a[152:155], v[160:163]\n" \
"ds_read_b128 v[248:251], v22 offset:0\n" \
"s_and_b32 s4, s4, 0x70000\n" \
"v_mfma_f32_16x16x32_bf16 v[160:163], a[252:255], a[216:219], v[160:163]\n" \
"ds_read_b128 a[152:155], v28 offset:12288\n" \
"s_or_b32 s5, s5, 0x2800\n" \
"v_mfma_f32_16x16x32_bf16 v[176:179], a[188:191], a[156:159], v[176:179]\n" \
"ds_read_b128 a[216:219], v29 offset:12288\n" \
"v_writelane_b32 v11, s5, 51\n" \
"v_mfma_f32_16x16x32_bf16 v[176:179], a[252:255], a[220:223], v[176:179]\n" \
"ds_read_b128 v[48:51], v23 offset:0\n" \
"s_add_i32 s13, s13, s4\n" \
"v_mfma_f32_16x16x32_bf16 v[172:175], a[184:187], a[156:159], v[172:175]\n" \
"s_or_b32 s4, s13, 0x3000\n" \
"v_writelane_b32 v11, s4, 52\n" \
"v_mfma_f32_16x16x32_bf16 v[172:175], a[248:251], a[220:223], v[172:175]\n" \
"s_movk_i32 s11, 0x4000\n" \
"s_lshl_b32 s4, s2, 2\n" \
"v_mfma_f32_16x16x32_bf16 v[168:171], a[180:183], a[156:159], v[168:171]\n" \
"s_and_b32 s4, s4, 28\n" \
"s_bfe_u32 s5, s2, 0x20003\n" \
"v_mfma_f32_16x16x32_bf16 v[168:171], a[244:247], a[220:223], v[168:171]\n" \
"s_or_b32 s4, s4, s5\n" \
"s_lshr_b32 s5, s2, 5\n" \
"v_mfma_f32_16x16x32_bf16 v[164:167], a[176:179], a[156:159], v[164:167]\n" \
"s_lshr_b32 s19, s3, 1\n" \
"s_lshl_b32 s18, s5, 1\n" \
"v_mfma_f32_16x16x32_bf16 v[164:167], a[240:243], a[220:223], v[164:167]\n" \
"ds_read_b128 a[156:159], v28 offset:14336\n" \
"ds_read_b128 a[220:223], v29 offset:14336\n" \
"s_waitcnt lgkmcnt(15)\n" \
"v_mfma_f32_16x16x32_bf16 a[0:3], a[160:163], a[128:131], a[0:3]\n" \
"s_addk_i32 s84, 0x2000\n" \
"v_mfma_f32_16x16x32_bf16 a[0:3], a[224:227], a[192:195], a[0:3]\n" \
"buffer_load_dwordx4 v24, s[76:79], s84 offen offset:2048 lds\n" \
"v_mfma_f32_16x16x32_bf16 a[4:7], a[164:167], a[128:131], a[4:7]\n" \
"ds_read_b64_tr_b16 a[176:177], v36 offset:0\n" \
"s_lshl_b32 s6, s4, 20\n" \
"v_mfma_f32_16x16x32_bf16 a[4:7], a[228:231], a[192:195], a[4:7]\n" \
"s_addk_i32 s84, 0x2000\n" \
"v_mfma_f32_16x16x32_bf16 a[12:15], a[172:175], a[128:131], a[12:15]\n" \
"buffer_load_dwordx4 v25, s[76:79], s84 offen offset:3072 lds\n" \
"s_waitcnt lgkmcnt(15)\n" \
"v_mfma_f32_16x16x32_bf16 a[12:15], a[236:239], a[192:195], a[12:15]\n" \
"ds_read_b64_tr_b16 a[178:179], v36 offset:1024\n" \
"s_lshl_b32 s7, s4, 8\n" \
"s_waitcnt lgkmcnt(12)\n" \
"v_mfma_f32_16x16x32_bf16 a[8:11], a[128:131], a[168:171], a[8:11]\n" \
"ds_read_b64_tr_b16 a[240:241], v36 offset:8192\n" \
"s_and_b32 s13, s3, 1\n" \
"s_waitcnt lgkmcnt(9)\n" \
"v_mfma_f32_16x16x32_bf16 a[8:11], a[192:195], a[232:235], a[8:11]\n" \
"ds_read_b64_tr_b16 a[242:243], v36 offset:9216\n" \
"s_add_i32 s14, s18, s19\n" \
"v_mfma_f32_16x16x32_bf16 a[24:27], a[132:135], a[168:171], a[24:27]\n" \
"ds_read_b64_tr_b16 a[180:181], v37 offset:0\n" \
"v_mfma_f32_16x16x32_bf16 a[24:27], a[196:199], a[232:235], a[24:27]\n" \
"ds_read_b64_tr_b16 a[182:183], v37 offset:1024\n" \
"v_mfma_f32_16x16x32_bf16 a[28:31], a[172:175], a[132:135], a[28:31]\n" \
"ds_read_b64_tr_b16 a[244:245], v37 offset:8192\n" \
"v_mfma_f32_16x16x32_bf16 a[28:31], a[236:239], a[196:199], a[28:31]\n" \
"ds_read_b64_tr_b16 a[246:247], v37 offset:9216\n" \
"v_mfma_f32_16x16x32_bf16 a[20:23], a[164:167], a[132:135], a[20:23]\n" \
"s_add_u32 s6, s26, s6\n" \
"s_addc_u32 s9, s27, 0\n" \
"v_mfma_f32_16x16x32_bf16 a[20:23], a[228:231], a[196:199], a[20:23]\n" \
"s_add_u32 s89, s42, 0x0\n" \
"s_nop 0\n" \
"buffer_store_dwordx4 v[184:187], v20, s[80:83], s89 offen offset:1024 sc1\n" \
"v_mfma_f32_16x16x32_bf16 a[16:19], a[160:163], a[132:135], a[16:19]\n" \
"ds_read_b64_tr_b16 a[184:185], v38 offset:0\n" \
"v_mfma_f32_16x16x32_bf16 a[16:19], a[224:227], a[196:199], a[16:19]\n" \
"ds_read_b64_tr_b16 a[186:187], v38 offset:1024\n" \
"v_mfma_f32_16x16x32_bf16 a[32:35], a[160:163], a[136:139], a[32:35]\n" \
"ds_read_b64_tr_b16 a[248:249], v38 offset:8192\n" \
"v_mfma_f32_16x16x32_bf16 a[32:35], a[224:227], a[200:203], a[32:35]\n" \
"ds_read_b64_tr_b16 a[250:251], v38 offset:9216\n" \
"v_mfma_f32_16x16x32_bf16 a[36:39], a[164:167], a[136:139], a[36:39]\n" \
"s_add_u32 s8, s6, 0x1000000\n" \
"s_addc_u32 s6, s9, 0\n" \
"v_mfma_f32_16x16x32_bf16 a[36:39], a[228:231], a[200:203], a[36:39]\n" \
"s_add_u32 s88, s42, 0x1000\n" \
"buffer_store_dwordx4 v[188:191], v20, s[80:83], s88 offen offset:1024 sc1\n" \
"v_mfma_f32_16x16x32_bf16 a[44:47], a[172:175], a[136:139], a[44:47]\n" \
"ds_read_b64_tr_b16 a[188:189], v39 offset:0\n" \
"s_and_b32 s9, s6, 0xffff\n" \
"v_mfma_f32_16x16x32_bf16 a[44:47], a[236:239], a[200:203], a[44:47]\n" \
"ds_read_b64_tr_b16 a[190:191], v39 offset:1024\n" \
"s_lshl_b32 s6, s5, 16\n" \
"v_mfma_f32_16x16x32_bf16 a[40:43], a[136:139], a[168:171], a[40:43]\n" \
"ds_read_b64_tr_b16 a[252:253], v39 offset:8192\n" \
"s_sub_i32 s10, 0x100000, s6\n" \
"v_mfma_f32_16x16x32_bf16 a[40:43], a[200:203], a[232:235], a[40:43]\n" \
"s_mov_b32 s6, 0x8000\n" \
"v_mfma_f32_16x16x32_bf16 a[56:59], a[140:143], a[168:171], a[56:59]\n" \
"ds_read_b64_tr_b16 a[254:255], v39 offset:9216\n" \
"v_mfma_f32_16x16x32_bf16 a[56:59], a[204:207], a[232:235], a[56:59]\n" \
"s_cmp_lg_u32 s5, 0\n" \
"s_cselect_b32 s17, s11, s6\n" \
"v_mfma_f32_16x16x32_bf16 a[60:63], a[172:175], a[140:143], a[60:63]\n" \
"s_add_u32 s89, s42, 0x2000\n" \
"buffer_store_dwordx4 v[192:195], v20, s[80:83], s89 offen offset:0 sc1\n" \
"v_mfma_f32_16x16x32_bf16 a[60:63], a[236:239], a[204:207], a[60:63]\n" \
"s_lshl_b32 s6, s3, 1\n" \
"s_lshl_b32 s5, s5, 3\n" \
"v_mfma_f32_16x16x32_bf16 a[52:55], a[164:167], a[140:143], a[52:55]\n" \
"s_and_b32 s6, s6, 4\n" \
"s_or_b32 s12, s5, s6\n" \
"v_mfma_f32_16x16x32_bf16 a[52:55], a[228:231], a[204:207], a[52:55]\n" \
"s_sub_i32 s5, s13, s5\n" \
"s_or_b32 s5, s5, s6\n" \
"v_mfma_f32_16x16x32_bf16 a[48:51], a[160:163], a[140:143], a[48:51]\n" \
"s_or_b32 s6, s12, s13\n" \
"s_lshl_b32 s4, s4, 2\n" \
"v_mfma_f32_16x16x32_bf16 a[48:51], a[224:227], a[204:207], a[48:51]\n" \
"s_mulk_i32 s6, 0x92\n" \
"s_add_i32 s4, s4, 18\n" \
"s_waitcnt lgkmcnt(15)\n" \
"v_mfma_f32_16x16x32_bf16 a[64:67], a[164:167], v[248:251], a[64:67]\n" \
"s_mulk_i32 s5, 0x92\n" \
"s_add_i32 s6, s4, s6\n" \
"v_mfma_f32_16x16x32_bf16 a[64:67], a[228:231], v[48:51], a[64:67]\n" \
"s_add_i32 s5, s5, s4\n" \
"s_lshl_b32 s4, s3, 11\n" \
"v_mfma_f32_16x16x32_bf16 a[80:83], a[172:175], v[248:251], a[80:83]\n" \
"s_lshl_b32 s15, s13, 12\n" \
"s_lshl_b32 s16, s19, 14\n" \
"v_mfma_f32_16x16x32_bf16 a[80:83], a[236:239], v[48:51], a[80:83]\n" \
"s_mul_i32 s19, s19, 0xe000\n" \
"s_mov_b32 s11, 0x20000\n" \
"v_mfma_f32_16x16x32_bf16 a[68:71], v[248:251], a[168:171], a[68:71]\n" \
"s_or_b32 s12, s4, 0xc000\n" \
"s_lshl_b32 s4, s3, 8\n" \
"v_mfma_f32_16x16x32_bf16 a[68:71], v[48:51], a[232:235], a[68:71]\n" \
"s_waitcnt vmcnt(3) lgkmcnt(0)\n" \
"s_barrier\n" \
"v_mfma_f32_16x16x32_bf16 v[52:55], a[176:179], a[128:131], v[52:55]\n" \
"s_add_u32 m0, s40, 0x0\n" \
"s_add_u32 s85, s39, 0x10000\n" \
"v_mfma_f32_16x16x32_bf16 v[52:55], a[240:243], a[192:195], v[52:55]\n" \
"buffer_load_dwordx4 v20, s[80:83], s85 offen sc1 lds\n" \
"v_mfma_f32_16x16x32_bf16 v[56:59], a[180:183], a[128:131], v[56:59]\n" \
"ds_read_b64_tr_b16 a[160:161], v32 offset:32768\n" \
"s_or_b32 s22, s15, s16\n" \
"v_mfma_f32_16x16x32_bf16 v[56:59], a[244:247], a[192:195], v[56:59]\n" \
"ds_read_b64_tr_b16 a[162:163], v32 offset:33280\n" \
"s_or_b32 s23, s19, s15\n" \
"v_mfma_f32_16x16x32_bf16 v[60:63], a[184:187], a[128:131], v[60:63]\n" \
"s_add_u32 m0, s40, 0x1000\n" \
"s_add_u32 s85, s39, 0x11000\n" \
"v_mfma_f32_16x16x32_bf16 v[60:63], a[248:251], a[192:195], v[60:63]\n" \
"buffer_load_dwordx4 v20, s[80:83], s85 offen sc1 lds\n" \
"v_mfma_f32_16x16x32_bf16 v[64:67], a[188:191], a[128:131], v[64:67]\n" \
"ds_read_b64_tr_b16 a[224:225], v32 offset:36864\n" \
"s_lshl_b32 s60, s6, 10\n" \
"v_mfma_f32_16x16x32_bf16 v[64:67], a[252:255], a[192:195], v[64:67]\n" \
"ds_read_b128 a[128:131], v28 offset:32768\n" \
"s_lshl_b32 s61, s5, 10\n" \
"v_mfma_f32_16x16x32_bf16 v[80:83], a[188:191], a[132:135], v[80:83]\n" \
"ds_read_b128 a[192:195], v29 offset:32768\n" \
"s_lshl_b32 s14, s14, 15\n" \
"v_mfma_f32_16x16x32_bf16 v[80:83], a[252:255], a[196:199], v[80:83]\n" \
"s_add_u32 m0, s40, 0x3000\n" \
"s_movk_i32 s84, 0x400\n" \
"v_mfma_f32_16x16x32_bf16 v[76:79], a[184:187], a[132:135], v[76:79]\n" \
"buffer_load_dwordx4 v21, s[76:79], s84 offen offset:0 lds\n" \
"v_mfma_f32_16x16x32_bf16 v[76:79], a[248:251], a[196:199], v[76:79]\n" \
"ds_read_b64_tr_b16 a[226:227], v32 offset:37376\n" \
"s_or_b32 s20, s4, 0x26c00\n" \
"v_mfma_f32_16x16x32_bf16 v[72:75], a[180:183], a[132:135], v[72:75]\n" \
"s_add_u32 m0, s30, 0x0\n" \
"s_movk_i32 s84, 0x400\n" \
"v_mfma_f32_16x16x32_bf16 v[72:75], a[244:247], a[196:199], v[72:75]\n" \
"buffer_load_dwordx4 v24, s[76:79], s84 offen offset:0 lds\n" \
"v_mfma_f32_16x16x32_bf16 v[68:71], a[176:179], a[132:135], v[68:71]\n" \
"ds_read_b64_tr_b16 a[164:165], v33 offset:32768\n" \
"s_or_b32 s13, s22, 0x8000\n" \
"v_mfma_f32_16x16x32_bf16 v[68:71], a[240:243], a[196:199], v[68:71]\n" \
"ds_read_b128 a[132:135], v28 offset:34816\n" \
"s_add_i32 s22, s22, 0x14000\n" \
"v_mfma_f32_16x16x32_bf16 v[84:87], a[176:179], a[136:139], v[84:87]\n" \
"ds_read_b128 a[196:199], v29 offset:34816\n" \
"s_bitset1_b32 s23, 16\n" \
"v_mfma_f32_16x16x32_bf16 v[84:87], a[240:243], a[200:203], v[84:87]\n" \
"s_movk_i32 s84, 0x2400\n" \
"v_mfma_f32_16x16x32_bf16 v[88:91], a[180:183], a[136:139], v[88:91]\n" \
"buffer_load_dwordx4 v25, s[76:79], s84 offen offset:1024 lds\n" \
"v_mfma_f32_16x16x32_bf16 v[88:91], a[244:247], a[200:203], v[88:91]\n" \
"ds_read_b64_tr_b16 a[166:167], v33 offset:33280\n" \
"s_or_b32 s24, s7, 0x5be8020\n" \
".fill (L_pb + 32480 - .) / 4, 4, 0xBF800000\n" \
"L_pp8:\n" \
"s_cbranch_execz L_pret\n" \
"v_mfma_f32_16x16x32_bf16 v[92:95], a[184:187], a[136:139], v[92:95]\n" \
"ds_read_b64_tr_b16 a[228:229], v33 offset:36864\n" \
"s_and_b32 s57, s27, 0xffff\n" \
"v_mfma_f32_16x16x32_bf16 v[92:95], a[248:251], a[200:203], v[92:95]\n" \
"s_movk_i32 s84, 0x4400\n" \
"v_mfma_f32_16x16x32_bf16 v[96:99], a[188:191], a[136:139], v[96:99]\n" \
"buffer_load_dwordx4 v24, s[76:79], s84 offen offset:2048 lds\n" \
"v_mfma_f32_16x16x32_bf16 v[96:99], a[252:255], a[200:203], v[96:99]\n" \
"ds_read_b128 a[136:139], v28 offset:36864\n" \
"s_mov_b32 s58, 0x6000000\n" \
"v_mfma_f32_16x16x32_bf16 v[112:115], a[188:191], a[140:143], v[112:115]\n" \
"ds_read_b128 a[200:203], v29 offset:36864\n" \
"s_mov_b32 s56, s26\n" \
"v_mfma_f32_16x16x32_bf16 v[112:115], a[252:255], a[204:207], v[112:115]\n" \
"s_movk_i32 s84, 0x6400\n" \
"v_mfma_f32_16x16x32_bf16 v[108:111], a[184:187], a[140:143], v[108:111]\n" \
"buffer_load_dwordx4 v25, s[76:79], s84 offen offset:3072 lds\n" \
"v_mfma_f32_16x16x32_bf16 v[108:111], a[248:251], a[204:207], v[108:111]\n" \
"ds_read_b64_tr_b16 a[230:231], v33 offset:37376\n" \
"s_mov_b32 s59, s11\n" \
"v_mfma_f32_16x16x32_bf16 v[104:107], a[180:183], a[140:143], v[104:107]\n" \
"ds_read_b64_tr_b16 a[172:173], v35 offset:32768\n" \
"s_add_i32 s60, s60, 0x3000000\n" \
"v_mfma_f32_16x16x32_bf16 v[104:107], a[244:247], a[204:207], v[104:107]\n" \
"ds_read_b64_tr_b16 a[174:175], v35 offset:33280\n" \
"s_add_i32 s61, s61, 0x411c000\n" \
"v_mfma_f32_16x16x32_bf16 v[100:103], a[176:179], a[140:143], v[100:103]\n" \
"ds_read_b64_tr_b16 a[236:237], v35 offset:36864\n" \
"s_sub_i32 s19, 14, s18\n" \
"v_mfma_f32_16x16x32_bf16 v[100:103], a[240:243], a[204:207], v[100:103]\n" \
"ds_read_b128 a[140:143], v28 offset:38912\n" \
"s_mov_b32 s21, 0x71800000\n" \
"v_mfma_f32_16x16x32_bf16 v[116:119], a[176:179], a[144:147], v[116:119]\n" \
"ds_read_b128 a[204:207], v29 offset:38912\n" \
"s_mov_b32 s4, L_gsw-L_gtr\n" \
"v_mfma_f32_16x16x32_bf16 v[116:119], a[240:243], a[208:211], v[116:119]\n" \
"s_add_u32 m0, s31, 0x0\n" \
"s_mov_b32 s84, 0x400000\n" \
"v_mfma_f32_16x16x32_bf16 v[120:123], a[180:183], a[144:147], v[120:123]\n" \
"buffer_load_dwordx4 v26, s[68:71], s84 offen offset:0 lds\n" \
"v_mfma_f32_16x16x32_bf16 v[120:123], a[244:247], a[208:211], v[120:123]\n" \
"s_addk_i32 s84, 0x7c00\n" \
"v_mfma_f32_16x16x32_bf16 v[124:127], a[184:187], a[144:147], v[124:127]\n" \
"buffer_load_dwordx4 v26, s[68:71], s84 offen offset:1024 lds\n" \
"v_mfma_f32_16x16x32_bf16 v[124:127], a[248:251], a[208:211], v[124:127]\n" \
"ds_read_b64_tr_b16 a[238:239], v35 offset:37376\n" \
"v_mfma_f32_16x16x32_bf16 v[128:131], a[188:191], a[144:147], v[128:131]\n" \
"ds_read_b64_tr_b16 a[168:169], v34 offset:32768\n" \
"v_mfma_f32_16x16x32_bf16 v[128:131], a[252:255], a[208:211], v[128:131]\n" \
"ds_read_b128 a[144:147], v28 offset:40960\n" \
"v_mfma_f32_16x16x32_bf16 v[144:147], a[188:191], a[148:151], v[144:147]\n" \
"ds_read_b128 a[208:211], v29 offset:40960\n" \
"v_mfma_f32_16x16x32_bf16 v[144:147], a[252:255], a[212:215], v[144:147]\n" \
"s_addk_i32 s84, 0x7c00\n" \
"v_mfma_f32_16x16x32_bf16 v[140:143], a[184:187], a[148:151], v[140:143]\n" \
"buffer_load_dwordx4 v27, s[68:71], s84 offen offset:2048 lds\n" \
"v_mfma_f32_16x16x32_bf16 v[140:143], a[248:251], a[212:215], v[140:143]\n" \
"s_addk_i32 s84, 0x7c00\n" \
"v_mfma_f32_16x16x32_bf16 v[136:139], a[180:183], a[148:151], v[136:139]\n" \
"buffer_load_dwordx4 v27, s[68:71], s84 offen offset:3072 lds\n" \
"v_mfma_f32_16x16x32_bf16 v[136:139], a[244:247], a[212:215], v[136:139]\n" \
"ds_read_b64_tr_b16 a[170:171], v34 offset:33280\n" \
"v_mfma_f32_16x16x32_bf16 v[132:135], a[176:179], a[148:151], v[132:135]\n" \
"ds_read_b64_tr_b16 a[232:233], v34 offset:36864\n" \
"v_mfma_f32_16x16x32_bf16 v[132:135], a[240:243], a[212:215], v[132:135]\n" \
"ds_read_b128 a[148:151], v28 offset:43008\n" \
"v_mfma_f32_16x16x32_bf16 v[148:151], a[176:179], a[152:155], v[148:151]\n" \
"ds_read_b128 a[212:215], v29 offset:43008\n" \
"v_mfma_f32_16x16x32_bf16 v[148:151], a[240:243], a[216:219], v[148:151]\n" \
"s_add_u32 m0, s30, 0x1000\n" \
"s_mov_b32 s84, 0x9400\n" \
"v_mfma_f32_16x16x32_bf16 v[152:155], a[180:183], a[152:155], v[152:155]\n" \
"buffer_load_dwordx4 v24, s[76:79], s84 offen offset:0 lds\n" \
"v_mfma_f32_16x16x32_bf16 v[152:155], a[244:247], a[216:219], v[152:155]\n" \
"s_addk_i32 s84, 0x2000\n" \
"s_nop 0\n" \
"v_mfma_f32_16x16x32_bf16 v[156:159], a[184:187], a[152:155], v[156:159]\n" \
"buffer_load_dwordx4 v25, s[76:79], s84 offen offset:1024 lds\n" \
"v_mfma_f32_16x16x32_bf16 v[156:159], a[248:251], a[216:219], v[156:159]\n" \
"ds_read_b64_tr_b16 a[234:235], v34 offset:37376\n" \
"v_mfma_f32_16x16x32_bf16 v[160:163], a[188:191], a[152:155], v[160:163]\n" \
"ds_read_b128 v[248:251], v22 offset:32768\n" \
"v_mfma_f32_16x16x32_bf16 v[160:163], a[252:255], a[216:219], v[160:163]\n" \
"ds_read_b128 a[152:155], v28 offset:45056\n" \
"v_mfma_f32_16x16x32_bf16 v[176:179], a[188:191], a[156:159], v[176:179]\n" \
"ds_read_b128 a[216:219], v29 offset:45056\n" \
"v_mfma_f32_16x16x32_bf16 v[176:179], a[252:255], a[220:223], v[176:179]\n" \
"ds_read_b128 v[48:51], v23 offset:32768\n" \
"v_mfma_f32_16x16x32_bf16 v[172:175], a[184:187], a[156:159], v[172:175]\n" \
"s_cmp_eq_u32 s19, 0\n" \
"s_cselect_b32 s4, L_gs7-L_gtr, s4\n" \
"v_mfma_f32_16x16x32_bf16 v[172:175], a[248:251], a[220:223], v[172:175]\n" \
"s_cmp_eq_u32 s16, 0\n" \
"s_cselect_b32 s4, 0, s4\n" \
"v_mfma_f32_16x16x32_bf16 v[168:171], a[180:183], a[156:159], v[168:171]\n" \
"s_getpc_b64 s[28:29]\n" \
"L_gp0_%=:\n" \
"s_add_u32 s28, s28, L_gtr-L_gp0_%=\n" \
"s_addc_u32 s29, s29, 0\n" \
"v_mfma_f32_16x16x32_bf16 v[168:171], a[244:247], a[220:223], v[168:171]\n" \
"s_add_u32 s28, s28, s4\n" \
"s_addc_u32 s29, s29, 0\n" \
"v_mfma_f32_16x16x32_bf16 v[164:167], a[176:179], a[156:159], v[164:167]\n" \
"v_mfma_f32_16x16x32_bf16 v[164:167], a[240:243], a[220:223], v[164:167]\n" \
"ds_read_b128 a[156:159], v28 offset:47104\n" \
"ds_read_b128 a[220:223], v29 offset:47104\n" \
"s_waitcnt lgkmcnt(15)\n" \
"v_mfma_f32_16x16x32_bf16 a[0:3], a[160:163], a[128:131], a[0:3]\n" \
"s_add_u32 m0, s30, 0x1000\n" \
"s_mov_b32 s84, 0xd400\n" \
"v_mfma_f32_16x16x32_bf16 a[0:3], a[224:227], a[192:195], a[0:3]\n" \
"buffer_load_dwordx4 v24, s[76:79], s84 offen offset:2048 lds\n" \
"v_mfma_f32_16x16x32_bf16 a[4:7], a[164:167], a[128:131], a[4:7]\n" \
"ds_read_b64_tr_b16 a[176:177], v36 offset:32768\n" \
"v_mfma_f32_16x16x32_bf16 a[4:7], a[228:231], a[192:195], a[4:7]\n" \
"s_addk_i32 s84, 0x2000\n" \
"v_mfma_f32_16x16x32_bf16 a[12:15], a[172:175], a[128:131], a[12:15]\n" \
"buffer_load_dwordx4 v25, s[76:79], s84 offen offset:3072 lds\n" \
"s_waitcnt lgkmcnt(15)\n" \
"v_mfma_f32_16x16x32_bf16 a[12:15], a[236:239], a[192:195], a[12:15]\n" \
"ds_read_b64_tr_b16 a[178:179], v36 offset:33792\n" \
"s_waitcnt lgkmcnt(12)\n" \
"v_mfma_f32_16x16x32_bf16 a[8:11], a[128:131], a[168:171], a[8:11]\n" \
"ds_read_b64_tr_b16 a[240:241], v36 offset:40960\n" \
"s_waitcnt lgkmcnt(9)\n" \
"v_mfma_f32_16x16x32_bf16 a[8:11], a[192:195], a[232:235], a[8:11]\n" \
"ds_read_b64_tr_b16 a[242:243], v36 offset:41984\n" \
"v_mfma_f32_16x16x32_bf16 a[24:27], a[132:135], a[168:171], a[24:27]\n" \
"ds_read_b64_tr_b16 a[180:181], v37 offset:32768\n" \
"v_mfma_f32_16x16x32_bf16 a[24:27], a[196:199], a[232:235], a[24:27]\n" \
"ds_read_b64_tr_b16 a[182:183], v37 offset:33792\n" \
"v_mfma_f32_16x16x32_bf16 a[28:31], a[172:175], a[132:135], a[28:31]\n" \
"ds_read_b64_tr_b16 a[244:245], v37 offset:40960\n" \
"v_mfma_f32_16x16x32_bf16 a[28:31], a[236:239], a[196:199], a[28:31]\n" \
"ds_read_b64_tr_b16 a[246:247], v37 offset:41984\n" \
"v_mfma_f32_16x16x32_bf16 a[20:23], a[164:167], a[132:135], a[20:23]\n" \
"v_mfma_f32_16x16x32_bf16 a[20:23], a[228:231], a[196:199], a[20:23]\n" \
"s_add_u32 s88, s42, 0x2000\n" \
"buffer_store_dwordx4 v[196:199], v20, s[80:83], s88 offen offset:1024 sc1\n" \
"v_mfma_f32_16x16x32_bf16 a[16:19], a[160:163], a[132:135], a[16:19]\n" \
"ds_read_b64_tr_b16 a[184:185], v38 offset:32768\n" \
"v_mfma_f32_16x16x32_bf16 a[16:19], a[224:227], a[196:199], a[16:19]\n" \
"ds_read_b64_tr_b16 a[186:187], v38 offset:33792\n" \
"v_mfma_f32_16x16x32_bf16 a[32:35], a[160:163], a[136:139], a[32:35]\n" \
"ds_read_b64_tr_b16 a[248:249], v38 offset:40960\n" \
"v_mfma_f32_16x16x32_bf16 a[32:35], a[224:227], a[200:203], a[32:35]\n" \
"ds_read_b64_tr_b16 a[250:251], v38 offset:41984\n" \
"v_mfma_f32_16x16x32_bf16 a[36:39], a[164:167], a[136:139], a[36:39]\n" \
"v_lshlrev_b32_e32 v45, 3, v20\n" \
"v_add_u32_e32 v45, s30, v45\n" \
"v_mfma_f32_16x16x32_bf16 a[36:39], a[228:231], a[200:203], a[36:39]\n" \
"s_add_u32 s89, s42, 0x3000\n" \
"buffer_store_dwordx4 v[216:219], v20, s[80:83], s89 offen offset:0 sc1\n" \
"s_getpc_b64 s[86:87]\n" \
"L_ct0_%=:\n" \
"s_add_u32 s86, s86, L_ap0-L_ct0_%=\n" \
"s_addc_u32 s87, s87, 0\n" \
"s_add_u32 m0, s40, 0x2000\n" \
"v_mfma_f32_16x16x32_bf16 a[44:47], a[172:175], a[136:139], a[44:47]\n" \
"global_load_lds_dword v45, s[86:87]\n" \
"v_mfma_f32_16x16x32_bf16 a[44:47], a[236:239], a[200:203], a[44:47]\n" \
"ds_read_b64_tr_b16 a[188:189], v39 offset:32768\n" \
"v_mfma_f32_16x16x32_bf16 a[40:43], a[136:139], a[168:171], a[40:43]\n" \
"ds_read_b64_tr_b16 a[190:191], v39 offset:33792\n" \
"v_mfma_f32_16x16x32_bf16 a[40:43], a[200:203], a[232:235], a[40:43]\n" \
"ds_read_b64_tr_b16 a[252:253], v39 offset:40960\n" \
"v_mfma_f32_16x16x32_bf16 a[56:59], a[140:143], a[168:171], a[56:59]\n" \
"ds_read_b64_tr_b16 a[254:255], v39 offset:41984\n" \
"v_mfma_f32_16x16x32_bf16 a[56:59], a[204:207], a[232:235], a[56:59]\n" \
"v_mfma_f32_16x16x32_bf16 a[60:63], a[172:175], a[140:143], a[60:63]\n" \
"s_add_u32 s88, s42, 0x3000\n" \
"buffer_store_dwordx4 v[220:223], v20, s[80:83], s88 offen offset:1024 sc1\n" \
"v_mfma_f32_16x16x32_bf16 a[60:63], a[236:239], a[204:207], a[60:63]\n" \
"v_mfma_f32_16x16x32_bf16 a[52:55], a[164:167], a[140:143], a[52:55]\n" \
"v_mfma_f32_16x16x32_bf16 a[52:55], a[228:231], a[204:207], a[52:55]\n" \
"v_mfma_f32_16x16x32_bf16 a[48:51], a[160:163], a[140:143], a[48:51]\n" \
"v_mfma_f32_16x16x32_bf16 a[48:51], a[224:227], a[204:207], a[48:51]\n" \
"s_waitcnt lgkmcnt(15)\n" \
"v_mfma_f32_16x16x32_bf16 a[64:67], a[164:167], v[248:251], a[64:67]\n" \
"v_mfma_f32_16x16x32_bf16 a[64:67], a[228:231], v[48:51], a[64:67]\n" \
"v_mfma_f32_16x16x32_bf16 a[80:83], a[172:175], v[248:251], a[80:83]\n" \
"v_mfma_f32_16x16x32_bf16 a[80:83], a[236:239], v[48:51], a[80:83]\n" \
"v_mfma_f32_16x16x32_bf16 a[68:71], v[248:251], a[168:171], a[68:71]\n" \
"v_mfma_f32_16x16x32_bf16 a[68:71], v[48:51], a[232:235], a[68:71]\n" \
"s_waitcnt vmcnt(4) lgkmcnt(0)\n" \
"s_barrier\n" \
"s_nop 0\n" \
"v_mfma_f32_16x16x32_bf16 v[52:55], a[176:179], a[128:131], v[52:55]\n" \
"ds_read_b64_tr_b16 a[160:161], v32 offset:0\n" \
"v_accvgpr_read_b32 v184, a0\n" \
"v_mfma_f32_16x16x32_bf16 v[52:55], a[240:243], a[192:195], v[52:55]\n" \
"v_accvgpr_read_b32 v185, a1\n" \
"v_accvgpr_read_b32 v186, a2\n" \
"v_mfma_f32_16x16x32_bf16 v[56:59], a[180:183], a[128:131], v[56:59]\n" \
"ds_read_b64_tr_b16 a[162:163], v32 offset:512\n" \
"v_accvgpr_read_b32 v187, a3\n" \
"v_mfma_f32_16x16x32_bf16 v[56:59], a[244:247], a[192:195], v[56:59]\n" \
"v_accvgpr_read_b32 v188, a4\n" \
"v_accvgpr_read_b32 v189, a5\n" \
"v_mfma_f32_16x16x32_bf16 v[60:63], a[184:187], a[128:131], v[60:63]\n" \
"v_accvgpr_read_b32 v190, a6\n" \
"v_accvgpr_read_b32 v191, a7\n" \
"v_mfma_f32_16x16x32_bf16 v[60:63], a[248:251], a[192:195], v[60:63]\n" \
"ds_read_b64_tr_b16 a[224:225], v32 offset:4096\n" \
"v_accvgpr_read_b32 v196, a12\n" \
"v_mfma_f32_16x16x32_bf16 v[64:67], a[188:191], a[128:131], v[64:67]\n" \
"v_accvgpr_read_b32 v197, a13\n" \
"v_accvgpr_read_b32 v198, a14\n" \
"v_mfma_f32_16x16x32_bf16 v[64:67], a[252:255], a[192:195], v[64:67]\n" \
"ds_read_b128 a[128:131], v28 offset:0\n" \
"v_accvgpr_read_b32 v199, a15\n" \
"v_mfma_f32_16x16x32_bf16 v[80:83], a[188:191], a[132:135], v[80:83]\n" \
"ds_read_b128 a[192:195], v29 offset:0\n" \
"v_accvgpr_read_b32 v192, a8\n" \
"v_mfma_f32_16x16x32_bf16 v[80:83], a[252:255], a[196:199], v[80:83]\n" \
"ds_read_b64_tr_b16 a[226:227], v32 offset:4608\n" \
"v_accvgpr_read_b32 v193, a9\n" \
"v_mfma_f32_16x16x32_bf16 v[76:79], a[184:187], a[132:135], v[76:79]\n" \
"v_accvgpr_read_b32 v194, a10\n" \
"v_accvgpr_read_b32 v195, a11\n" \
"v_mfma_f32_16x16x32_bf16 v[76:79], a[248:251], a[196:199], v[76:79]\n" \
"ds_read_b64_tr_b16 a[164:165], v33 offset:0\n" \
"v_accvgpr_read_b32 v208, a24\n" \
"v_mfma_f32_16x16x32_bf16 v[72:75], a[180:183], a[132:135], v[72:75]\n" \
"v_accvgpr_read_b32 v209, a25\n" \
"v_accvgpr_read_b32 v210, a26\n" \
"v_mfma_f32_16x16x32_bf16 v[72:75], a[244:247], a[196:199], v[72:75]\n" \
"v_accvgpr_read_b32 v211, a27\n" \
"v_accvgpr_read_b32 v212, a28\n" \
"v_mfma_f32_16x16x32_bf16 v[68:71], a[176:179], a[132:135], v[68:71]\n" \
"ds_read_b64_tr_b16 a[166:167], v33 offset:512\n" \
"v_accvgpr_read_b32 v213, a29\n" \
"v_mfma_f32_16x16x32_bf16 v[68:71], a[240:243], a[196:199], v[68:71]\n" \
"ds_read_b128 a[132:135], v28 offset:2048\n" \
"v_accvgpr_read_b32 v214, a30\n" \
"v_mfma_f32_16x16x32_bf16 v[84:87], a[176:179], a[136:139], v[84:87]\n" \
"ds_read_b128 a[196:199], v29 offset:2048\n" \
"v_accvgpr_read_b32 v215, a31\n" \
"v_mfma_f32_16x16x32_bf16 v[84:87], a[240:243], a[200:203], v[84:87]\n" \
"ds_read_b64_tr_b16 a[228:229], v33 offset:4096\n" \
"v_accvgpr_read_b32 v204, a20\n" \
"v_mfma_f32_16x16x32_bf16 v[88:91], a[180:183], a[136:139], v[88:91]\n" \
"v_accvgpr_read_b32 v205, a21\n" \
"v_accvgpr_read_b32 v206, a22\n" \
"v_mfma_f32_16x16x32_bf16 v[88:91], a[244:247], a[200:203], v[88:91]\n" \
"v_accvgpr_read_b32 v207, a23\n" \
"v_accvgpr_read_b32 v200, a16\n" \
"v_mfma_f32_16x16x32_bf16 v[92:95], a[184:187], a[136:139], v[92:95]\n" \
"ds_read_b64_tr_b16 a[230:231], v33 offset:4608\n" \
"v_accvgpr_read_b32 v201, a17\n" \
"v_mfma_f32_16x16x32_bf16 v[92:95], a[248:251], a[200:203], v[92:95]\n" \
"v_accvgpr_read_b32 v202, a18\n" \
"v_accvgpr_read_b32 v203, a19\n" \
"v_mfma_f32_16x16x32_bf16 v[96:99], a[188:191], a[136:139], v[96:99]\n" \
"v_accvgpr_read_b32 v216, a32\n" \
"v_accvgpr_read_b32 v217, a33\n" \
"v_mfma_f32_16x16x32_bf16 v[96:99], a[252:255], a[200:203], v[96:99]\n" \
"ds_read_b128 a[136:139], v28 offset:4096\n" \
"v_accvgpr_read_b32 v218, a34\n" \
"v_mfma_f32_16x16x32_bf16 v[112:115], a[188:191], a[140:143], v[112:115]\n" \
"ds_read_b128 a[200:203], v29 offset:4096\n" \
"v_accvgpr_read_b32 v219, a35\n" \
"v_mfma_f32_16x16x32_bf16 v[112:115], a[252:255], a[204:207], v[112:115]\n" \
"ds_read_b64_tr_b16 a[172:173], v35 offset:0\n" \
"v_accvgpr_read_b32 v220, a36\n" \
"v_mfma_f32_16x16x32_bf16 v[108:111], a[184:187], a[140:143], v[108:111]\n" \
"ds_read_b64_tr_b16 a[174:175], v35 offset:512\n" \
"v_accvgpr_read_b32 v221, a37\n" \
"v_mfma_f32_16x16x32_bf16 v[108:111], a[248:251], a[204:207], v[108:111]\n" \
"v_accvgpr_read_b32 v222, a38\n" \
"v_accvgpr_read_b32 v223, a39\n" \
"v_mfma_f32_16x16x32_bf16 v[104:107], a[180:183], a[140:143], v[104:107]\n" \
"ds_read_b64_tr_b16 a[236:237], v35 offset:4096\n" \
"v_accvgpr_read_b32 v228, a44\n" \
"v_mfma_f32_16x16x32_bf16 v[104:107], a[244:247], a[204:207], v[104:107]\n" \
"v_accvgpr_read_b32 v229, a45\n" \
"v_accvgpr_read_b32 v230, a46\n" \
"v_mfma_f32_16x16x32_bf16 v[100:103], a[176:179], a[140:143], v[100:103]\n" \
"v_accvgpr_read_b32 v231, a47\n" \
"v_accvgpr_read_b32 v224, a40\n" \
"v_mfma_f32_16x16x32_bf16 v[100:103], a[240:243], a[204:207], v[100:103]\n" \
"ds_read_b128 a[140:143], v28 offset:6144\n" \
"v_accvgpr_read_b32 v225, a41\n" \
"v_mfma_f32_16x16x32_bf16 v[116:119], a[176:179], a[144:147], v[116:119]\n" \
"ds_read_b128 a[204:207], v29 offset:6144\n" \
"v_accvgpr_read_b32 v226, a42\n" \
"v_mfma_f32_16x16x32_bf16 v[116:119], a[240:243], a[208:211], v[116:119]\n" \
"ds_read_b64_tr_b16 a[238:239], v35 offset:4608\n" \
"v_accvgpr_read_b32 v227, a43\n" \
"v_mfma_f32_16x16x32_bf16 v[120:123], a[180:183], a[144:147], v[120:123]\n" \
"ds_read_b64_tr_b16 a[168:169], v34 offset:0\n" \
"v_accvgpr_read_b32 v240, a56\n" \
"v_mfma_f32_16x16x32_bf16 v[120:123], a[244:247], a[208:211], v[120:123]\n" \
"v_accvgpr_read_b32 v241, a57\n" \
"v_accvgpr_read_b32 v242, a58\n" \
"v_mfma_f32_16x16x32_bf16 v[124:127], a[184:187], a[144:147], v[124:127]\n" \
"v_accvgpr_read_b32 v243, a59\n" \
"v_accvgpr_read_b32 v244, a60\n" \
"v_mfma_f32_16x16x32_bf16 v[124:127], a[248:251], a[208:211], v[124:127]\n" \
"ds_read_b64_tr_b16 a[170:171], v34 offset:512\n" \
"v_accvgpr_read_b32 v245, a61\n" \
"v_mfma_f32_16x16x32_bf16 v[128:131], a[188:191], a[144:147], v[128:131]\n" \
"v_accvgpr_read_b32 v246, a62\n" \
"v_accvgpr_read_b32 v247, a63\n" \
"v_mfma_f32_16x16x32_bf16 v[128:131], a[252:255], a[208:211], v[128:131]\n" \
"ds_read_b128 a[144:147], v28 offset:8192\n" \
"v_accvgpr_read_b32 v236, a52\n" \
"v_mfma_f32_16x16x32_bf16 v[144:147], a[188:191], a[148:151], v[144:147]\n" \
"ds_read_b128 a[208:211], v29 offset:8192\n" \
"v_accvgpr_read_b32 v237, a53\n" \
"v_mfma_f32_16x16x32_bf16 v[144:147], a[252:255], a[212:215], v[144:147]\n" \
"ds_read_b64_tr_b16 a[232:233], v34 offset:4096\n" \
"v_accvgpr_read_b32 v238, a54\n" \
"v_mfma_f32_16x16x32_bf16 v[140:143], a[184:187], a[148:151], v[140:143]\n" \
"v_accvgpr_read_b32 v239, a55\n" \
"v_accvgpr_read_b32 v232, a48\n" \
"v_mfma_f32_16x16x32_bf16 v[140:143], a[248:251], a[212:215], v[140:143]\n" \
"ds_read_b64_tr_b16 a[234:235], v34 offset:4608\n" \
"v_accvgpr_read_b32 v233, a49\n" \
"v_mfma_f32_16x16x32_bf16 v[136:139], a[180:183], a[148:151], v[136:139]\n" \
"v_accvgpr_read_b32 v234, a50\n" \
"v_accvgpr_read_b32 v235, a51\n" \
"v_mfma_f32_16x16x32_bf16 v[136:139], a[244:247], a[212:215], v[136:139]\n" \
"v_accvgpr_read_b32 v180, a64\n" \
"v_accvgpr_read_b32 v181, a65\n" \
"v_mfma_f32_16x16x32_bf16 v[132:135], a[176:179], a[148:151], v[132:135]\n" \
"ds_read_b128 v[248:251], v22 offset:0\n" \
"v_accvgpr_read_b32 v182, a66\n" \
"v_mfma_f32_16x16x32_bf16 v[132:135], a[240:243], a[212:215], v[132:135]\n" \
"ds_read_b128 a[148:151], v28 offset:10240\n" \
"v_accvgpr_read_b32 v183, a67\n" \
"v_mfma_f32_16x16x32_bf16 v[148:151], a[176:179], a[152:155], v[148:151]\n" \
"ds_read_b128 a[212:215], v29 offset:10240\n" \
"v_accvgpr_read_b32 v252, a68\n" \
"v_mfma_f32_16x16x32_bf16 v[148:151], a[240:243], a[216:219], v[148:151]\n" \
"ds_read_b128 v[48:51], v23 offset:0\n" \
"v_accvgpr_read_b32 v253, a69\n" \
"v_mfma_f32_16x16x32_bf16 v[152:155], a[180:183], a[152:155], v[152:155]\n" \
"v_accvgpr_read_b32 v254, a70\n" \
"v_accvgpr_read_b32 v255, a71\n" \
"v_mfma_f32_16x16x32_bf16 v[152:155], a[244:247], a[216:219], v[152:155]\n" \
"v_lshrrev_b32_e64 v24, 9, v20\n" \
"v_and_b32_e32 v26, 0x1ff, v20\n" \
"v_mfma_f32_16x16x32_bf16 v[156:159], a[184:187], a[152:155], v[156:159]\n" \
"v_lshl_add_u32 v24, v24, 3, v26\n" \
"v_lshrrev_b32_e64 v25, 8, v20\n" \
"v_mfma_f32_16x16x32_bf16 v[156:159], a[248:251], a[216:219], v[156:159]\n" \
"v_mul_u32_u24_e32 v25, 0x24700, v25\n" \
"v_add_u32_e64 v25, v25, v20\n" \
"v_mfma_f32_16x16x32_bf16 v[160:163], a[188:191], a[152:155], v[160:163]\n" \
"v_lshrrev_b32_e32 v26, 2, v20\n" \
"v_add_u32_e32 v26, s45, v26\n" \
"v_mfma_f32_16x16x32_bf16 v[160:163], a[252:255], a[216:219], v[160:163]\n" \
"ds_read_b128 a[152:155], v28 offset:12288\n" \
"v_add_u32_e64 v27, s46, v20\n" \
"v_mfma_f32_16x16x32_bf16 v[176:179], a[188:191], a[156:159], v[176:179]\n" \
"ds_read_b128 a[216:219], v29 offset:12288\n" \
"v_add_u32_e64 v21, s43, v27\n" \
"v_mfma_f32_16x16x32_bf16 v[176:179], a[252:255], a[220:223], v[176:179]\n" \
"v_mfma_f32_16x16x32_bf16 v[172:175], a[184:187], a[156:159], v[172:175]\n" \
"v_mfma_f32_16x16x32_bf16 v[172:175], a[248:251], a[220:223], v[172:175]\n" \
"v_mfma_f32_16x16x32_bf16 v[168:171], a[180:183], a[156:159], v[168:171]\n" \
"v_mfma_f32_16x16x32_bf16 v[168:171], a[244:247], a[220:223], v[168:171]\n" \
"v_mfma_f32_16x16x32_bf16 v[164:167], a[176:179], a[156:159], v[164:167]\n" \
"v_mfma_f32_16x16x32_bf16 v[164:167], a[240:243], a[220:223], v[164:167]\n" \
"ds_read_b128 a[156:159], v28 offset:14336\n" \
"ds_read_b128 a[220:223], v29 offset:14336\n" \
"s_waitcnt lgkmcnt(15)\n" \
"s_nop 0\n" \
"v_mfma_f32_16x16x32_bf16 v[184:187], a[160:163], a[128:131], v[184:187]\n" \
"v_bfe_u32 v44, v20, 2, 5\n" \
"ds_read_b64_tr_b16 a[176:177], v36 offset:0\n" \
"v_mfma_f32_16x16x32_bf16 v[184:187], a[224:227], a[192:195], v[184:187]\n" \
"buffer_load_dword v44, v44, s[80:83], s38 offen offset:0 sc1\n" \
"v_mfma_f32_16x16x32_bf16 v[188:191], a[164:167], a[128:131], v[188:191]\n" \
"ds_read_b64_tr_b16 a[178:179], v36 offset:1024\n" \
"v_mfma_f32_16x16x32_bf16 v[188:191], a[228:231], a[192:195], v[188:191]\n" \
"ds_read_b64_tr_b16 a[240:241], v36 offset:8192\n" \
"v_mfma_f32_16x16x32_bf16 v[196:199], a[172:175], a[128:131], v[196:199]\n" \
"ds_read_b64_tr_b16 a[242:243], v36 offset:9216\n" \
"s_waitcnt lgkmcnt(15)\n" \
"v_mfma_f32_16x16x32_bf16 v[196:199], a[236:239], a[192:195], v[196:199]\n" \
"v_mfma_f32_16x16x32_bf16 v[192:195], a[128:131], a[168:171], v[192:195]\n" \
"ds_read_b64_tr_b16 a[180:181], v37 offset:0\n" \
"s_waitcnt lgkmcnt(13)\n" \
"v_mfma_f32_16x16x32_bf16 v[192:195], a[192:195], a[232:235], v[192:195]\n" \
"v_mfma_f32_16x16x32_bf16 v[208:211], a[132:135], a[168:171], v[208:211]\n" \
"ds_read_b64_tr_b16 a[182:183], v37 offset:1024\n" \
"v_mfma_f32_16x16x32_bf16 v[208:211], a[196:199], a[232:235], v[208:211]\n" \
"ds_read_b64_tr_b16 a[244:245], v37 offset:8192\n" \
"v_mfma_f32_16x16x32_bf16 v[212:215], a[172:175], a[132:135], v[212:215]\n" \
"v_mfma_f32_16x16x32_bf16 v[212:215], a[236:239], a[196:199], v[212:215]\n" \
"ds_read_b64_tr_b16 a[246:247], v37 offset:9216\n" \
"v_cvt_pk_bf16_f32 v188, v188, v189\n" \
"v_mfma_f32_16x16x32_bf16 v[204:207], a[164:167], a[132:135], v[204:207]\n" \
"v_cvt_pk_bf16_f32 v189, v190, v191\n" \
"v_cvt_pk_bf16_f32 v190, v198, v199\n" \
"v_mfma_f32_16x16x32_bf16 v[204:207], a[228:231], a[196:199], v[204:207]\n" \
"ds_read_b64_tr_b16 a[184:185], v38 offset:0\n" \
"v_mov_b32_e32 v191, 0x3f80\n" \
"v_mfma_f32_16x16x32_bf16 v[200:203], a[160:163], a[132:135], v[200:203]\n" \
"ds_read_b64_tr_b16 a[186:187], v38 offset:1024\n" \
"v_cvt_pk_bf16_f32 v184, v184, v185\n" \
"v_mfma_f32_16x16x32_bf16 v[200:203], a[224:227], a[196:199], v[200:203]\n" \
"v_cvt_pk_bf16_f32 v185, v186, v187\n" \
"v_cvt_pk_bf16_f32 v186, v196, v197\n" \
"v_mfma_f32_16x16x32_bf16 v[216:219], a[160:163], a[136:139], v[216:219]\n" \
"ds_read_b64_tr_b16 a[248:249], v38 offset:8192\n" \
"v_mov_b32_e64 v187, 0\n" \
"v_mfma_f32_16x16x32_bf16 v[216:219], a[224:227], a[200:203], v[216:219]\n" \
"ds_read_b64_tr_b16 a[250:251], v38 offset:9216\n" \
"v_mfma_f32_16x16x32_bf16 v[220:223], a[164:167], a[136:139], v[220:223]\n" \
"v_mfma_f32_16x16x32_bf16 v[220:223], a[228:231], a[200:203], v[220:223]\n" \
"ds_read_b64_tr_b16 a[188:189], v39 offset:0\n" \
"v_cvt_pk_bf16_f32 v204, v204, v205\n" \
"v_mfma_f32_16x16x32_bf16 v[228:231], a[172:175], a[136:139], v[228:231]\n" \
"v_cvt_pk_bf16_f32 v205, v206, v207\n" \
"v_cvt_pk_bf16_f32 v206, v214, v215\n" \
"v_mfma_f32_16x16x32_bf16 v[228:231], a[236:239], a[200:203], v[228:231]\n" \
"ds_read_b64_tr_b16 a[190:191], v39 offset:1024\n" \
"v_mov_b32_e32 v207, 0x3f80\n" \
"v_mfma_f32_16x16x32_bf16 v[224:227], a[136:139], a[168:171], v[224:227]\n" \
"ds_read_b64_tr_b16 a[252:253], v39 offset:8192\n" \
"v_cvt_pk_bf16_f32 v200, v200, v201\n" \
"v_mfma_f32_16x16x32_bf16 v[224:227], a[200:203], a[232:235], v[224:227]\n" \
"v_cvt_pk_bf16_f32 v201, v202, v203\n" \
"v_cvt_pk_bf16_f32 v202, v212, v213\n" \
"v_mfma_f32_16x16x32_bf16 v[240:243], a[140:143], a[168:171], v[240:243]\n" \
"ds_read_b64_tr_b16 a[254:255], v39 offset:9216\n" \
"v_mov_b32_e64 v203, 0\n" \
"v_mfma_f32_16x16x32_bf16 v[240:243], a[204:207], a[232:235], v[240:243]\n" \
"v_mfma_f32_16x16x32_bf16 v[244:247], a[172:175], a[140:143], v[244:247]\n" \
"v_mfma_f32_16x16x32_bf16 v[244:247], a[236:239], a[204:207], v[244:247]\n" \
"v_cvt_pk_bf16_f32 v220, v220, v221\n" \
"v_cvt_pk_bf16_f32 v221, v222, v223\n" \
"v_mfma_f32_16x16x32_bf16 v[236:239], a[164:167], a[140:143], v[236:239]\n" \
"v_cvt_pk_bf16_f32 v222, v230, v231\n" \
"v_mov_b32_e32 v223, 0x3f80\n" \
"v_mfma_f32_16x16x32_bf16 v[236:239], a[228:231], a[204:207], v[236:239]\n" \
"v_cvt_pk_bf16_f32 v216, v216, v217\n" \
"v_cvt_pk_bf16_f32 v217, v218, v219\n" \
"v_mfma_f32_16x16x32_bf16 v[232:235], a[160:163], a[140:143], v[232:235]\n" \
"v_cvt_pk_bf16_f32 v218, v228, v229\n" \
"v_mov_b32_e64 v219, 0\n" \
"v_mfma_f32_16x16x32_bf16 v[232:235], a[224:227], a[204:207], v[232:235]\n" \
"s_waitcnt lgkmcnt(15)\n" \
"s_nop 0\n" \
"v_mfma_f32_16x16x32_bf16 v[180:183], a[164:167], v[248:251], v[180:183]\n" \
"v_mfma_f32_16x16x32_bf16 v[180:183], a[228:231], v[48:51], v[180:183]\n" \
"v_mfma_f32_16x16x32_bf16 a[80:83], a[172:175], v[248:251], a[80:83]\n" \
"v_mfma_f32_16x16x32_bf16 a[80:83], a[236:239], v[48:51], a[80:83]\n" \
"v_cvt_pk_bf16_f32 v236, v236, v237\n" \
"v_cvt_pk_bf16_f32 v237, v238, v239\n" \
"v_mfma_f32_16x16x32_bf16 v[252:255], v[248:251], a[168:171], v[252:255]\n" \
"v_cvt_pk_bf16_f32 v238, v246, v247\n" \
"v_mov_b32_e32 v239, 0x3f80\n" \
"v_mfma_f32_16x16x32_bf16 v[252:255], v[48:51], a[232:235], v[252:255]\n" \
"v_cvt_pk_bf16_f32 v232, v232, v233\n" \
"v_cvt_pk_bf16_f32 v233, v234, v235\n" \
"s_waitcnt vmcnt(0) lgkmcnt(0)\n" \
"s_barrier\n" \
"v_mov_b32_e32 v45, s62\n" \
"s_mov_b64 exec, 1\n" \
"buffer_store_dword v45, off, s[80:83], s37 offset:32 sc1\n" \
"s_mov_b64 exec, -1\n" \
"v_cmp_ne_u32_e32 vcc, s62, v44\n" \
"s_nop 4\n" \
"s_cbranch_vccnz L_poll160_%=\n" \
"L_pollr160_%=:\n" \
"v_mfma_f32_16x16x32_bf16 v[52:55], a[176:179], a[128:131], v[52:55]\n" \
"s_mov_b32 s73, s50\n" \
"s_sub_u32 m0, s31, 0x10000\n" \
"v_mfma_f32_16x16x32_bf16 v[52:55], a[240:243], a[192:195], v[52:55]\n" \
"buffer_load_dwordx4 v20, s[80:83], s73 offen offset:0 sc1 lds\n" \
"v_mfma_f32_16x16x32_bf16 v[56:59], a[180:183], a[128:131], v[56:59]\n" \
"s_add_u32 s74, s50, 0x4000\n" \
"s_add_u32 s75, s50, 0x8000\n" \
"v_mfma_f32_16x16x32_bf16 v[56:59], a[244:247], a[192:195], v[56:59]\n" \
"s_mov_b32 s70, 0x3e0293ee\n" \
"s_mov_b32 s76, s52\n" \
"v_mfma_f32_16x16x32_bf16 v[60:63], a[184:187], a[128:131], v[60:63]\n" \
"v_mul_f32_e32 v52, s70, v52\n" \
"v_mul_f32_e32 v53, s70, v53\n" \
"v_mfma_f32_16x16x32_bf16 v[60:63], a[248:251], a[192:195], v[60:63]\n" \
"buffer_load_dwordx4 v20, s[80:83], s73 offen offset:1024 sc1 lds\n" \
"v_mfma_f32_16x16x32_bf16 v[64:67], a[188:191], a[128:131], v[64:67]\n" \
"v_mul_f32_e32 v54, s70, v54\n" \
"v_mul_f32_e32 v55, s70, v55\n" \
"v_mfma_f32_16x16x32_bf16 v[64:67], a[252:255], a[192:195], v[64:67]\n" \
"v_cvt_pk_bf16_f32 v52, v52, v53\n" \
"v_mul_f32_e64 v56, s70, v56\n" \
"v_mfma_f32_16x16x32_bf16 v[80:83], a[188:191], a[132:135], v[80:83]\n" \
"v_mul_f32_e32 v60, s70, v60\n" \
"v_mul_f32_e32 v61, s70, v61\n" \
"v_mfma_f32_16x16x32_bf16 v[80:83], a[252:255], a[196:199], v[80:83]\n" \
"buffer_load_dwordx4 v20, s[80:83], s73 offen offset:2048 sc1 lds\n" \
"v_mfma_f32_16x16x32_bf16 v[76:79], a[184:187], a[132:135], v[76:79]\n" \
"v_mul_f32_e32 v62, s70, v62\n" \
"v_mul_f32_e32 v63, s70, v63\n" \
"v_mfma_f32_16x16x32_bf16 v[76:79], a[248:251], a[196:199], v[76:79]\n" \
"v_cvt_pk_bf16_f32 v60, v60, v61\n" \
"v_mul_f32_e64 v57, s70, v57\n" \
"v_mfma_f32_16x16x32_bf16 v[72:75], a[180:183], a[132:135], v[72:75]\n" \
"v_mul_f32_e32 v58, s70, v58\n" \
"v_mul_f32_e32 v59, s70, v59\n" \
"v_mfma_f32_16x16x32_bf16 v[72:75], a[244:247], a[196:199], v[72:75]\n" \
"buffer_load_dwordx4 v20, s[80:83], s73 offen offset:3072 sc1 lds\n" \
"v_mfma_f32_16x16x32_bf16 v[68:71], a[176:179], a[132:135], v[68:71]\n" \
"s_sub_u32 m0, s31, s51\n" \
"v_cvt_pk_bf16_f32 v53, v54, v55\n" \
"v_mfma_f32_16x16x32_bf16 v[68:71], a[240:243], a[196:199], v[68:71]\n" \
"v_mul_f32_e32 v64, s70, v64\n" \
"v_mul_f32_e64 v65, s70, v65\n" \
"v_mfma_f32_16x16x32_bf16 v[84:87], a[176:179], a[136:139], v[84:87]\n" \
"v_mul_f32_e32 v66, s70, v66\n" \
"v_mul_f32_e32 v67, s70, v67\n" \
"v_mfma_f32_16x16x32_bf16 v[84:87], a[240:243], a[200:203], v[84:87]\n" \
"buffer_load_dwordx4 v20, s[80:83], s74 offen offset:0 sc1 lds\n" \
"v_mfma_f32_16x16x32_bf16 v[88:91], a[180:183], a[136:139], v[88:91]\n" \
"v_cvt_pk_bf16_f32 v61, v62, v63\n" \
"v_cvt_pk_bf16_f32 v54, v56, v57\n" \
"v_mfma_f32_16x16x32_bf16 v[88:91], a[244:247], a[200:203], v[88:91]\n" \
"v_cvt_pk_bf16_f32 v55, v58, v59\n" \
"v_cvt_pk_bf16_f32 v62, v64, v65\n" \
"v_mfma_f32_16x16x32_bf16 v[92:95], a[184:187], a[136:139], v[92:95]\n" \
"v_cvt_pk_bf16_f32 v63, v66, v67\n" \
"v_mul_f32_e64 v68, s70, v68\n" \
"v_mfma_f32_16x16x32_bf16 v[92:95], a[248:251], a[200:203], v[92:95]\n" \
"buffer_load_dwordx4 v20, s[80:83], s74 offen offset:1024 sc1 lds\n" \
"v_mfma_f32_16x16x32_bf16 v[96:99], a[188:191], a[136:139], v[96:99]\n" \
"ds_write_b128 v27, v[52:55] offset:0\n" \
"v_mfma_f32_16x16x32_bf16 v[96:99], a[252:255], a[200:203], v[96:99]\n" \
"v_mul_f32_e32 v69, s70, v69\n" \
"v_mul_f32_e32 v76, s70, v76\n" \
"v_mfma_f32_16x16x32_bf16 v[112:115], a[188:191], a[140:143], v[112:115]\n" \
"v_mul_f32_e32 v77, s70, v77\n" \
"v_mul_f32_e32 v70, s70, v70\n" \
"v_mfma_f32_16x16x32_bf16 v[112:115], a[252:255], a[204:207], v[112:115]\n" \
"buffer_load_dwordx4 v20, s[80:83], s74 offen offset:2048 sc1 lds\n" \
"v_mfma_f32_16x16x32_bf16 v[108:111], a[184:187], a[140:143], v[108:111]\n" \
"ds_write_b128 v27, v[60:63] offset:1024\n" \
"v_mfma_f32_16x16x32_bf16 v[108:111], a[248:251], a[204:207], v[108:111]\n" \
"v_mul_f32_e64 v71, s70, v71\n" \
"v_cvt_pk_bf16_f32 v68, v68, v69\n" \
"v_mfma_f32_16x16x32_bf16 v[104:107], a[180:183], a[140:143], v[104:107]\n" \
"v_mul_f32_e32 v78, s70, v78\n" \
"v_mul_f32_e32 v79, s70, v79\n" \
"v_mfma_f32_16x16x32_bf16 v[104:107], a[244:247], a[204:207], v[104:107]\n" \
"buffer_load_dwordx4 v20, s[80:83], s74 offen offset:3072 sc1 lds\n" \
".fill (L_pb + 36576 - .) / 4, 4, 0xBF800000\n" \
"L_pp9:\n" \
"s_cbranch_execz L_pret\n" \
"v_mfma_f32_16x16x32_bf16 v[100:103], a[176:179], a[140:143], v[100:103]\n" \
"s_sub_u32 m0, s31, 0x4000\n" \
"v_cvt_pk_bf16_f32 v76, v76, v77\n" \
"v_mfma_f32_16x16x32_bf16 v[100:103], a[240:243], a[204:207], v[100:103]\n" \
"v_mul_f32_e32 v72, s70, v72\n" \
"v_mul_f32_e64 v73, s70, v73\n" \
"v_mfma_f32_16x16x32_bf16 v[116:119], a[176:179], a[144:147], v[116:119]\n" \
"v_mul_f32_e32 v74, s70, v74\n" \
"v_mul_f32_e32 v75, s70, v75\n" \
"v_mfma_f32_16x16x32_bf16 v[116:119], a[240:243], a[208:211], v[116:119]\n" \
"buffer_load_dwordx4 v20, s[80:83], s75 offen offset:0 sc1 lds\n" \
"v_mfma_f32_16x16x32_bf16 v[120:123], a[180:183], a[144:147], v[120:123]\n" \
"v_cvt_pk_bf16_f32 v69, v70, v71\n" \
"v_mul_f32_e64 v80, s70, v80\n" \
"v_mfma_f32_16x16x32_bf16 v[120:123], a[244:247], a[208:211], v[120:123]\n" \
"v_mul_f32_e32 v81, s70, v81\n" \
"v_mul_f32_e32 v82, s70, v82\n" \
"v_mfma_f32_16x16x32_bf16 v[124:127], a[184:187], a[144:147], v[124:127]\n" \
"v_mul_f32_e64 v83, s70, v83\n" \
"v_cvt_pk_bf16_f32 v77, v78, v79\n" \
"v_mfma_f32_16x16x32_bf16 v[124:127], a[248:251], a[208:211], v[124:127]\n" \
"buffer_load_dwordx4 v20, s[80:83], s75 offen offset:1024 sc1 lds\n" \
"v_mfma_f32_16x16x32_bf16 v[128:131], a[188:191], a[144:147], v[128:131]\n" \
"v_cvt_pk_bf16_f32 v70, v72, v73\n" \
"v_cvt_pk_bf16_f32 v71, v74, v75\n" \
"v_mfma_f32_16x16x32_bf16 v[128:131], a[252:255], a[208:211], v[128:131]\n" \
"v_cvt_pk_bf16_f32 v78, v80, v81\n" \
"v_cvt_pk_bf16_f32 v79, v82, v83\n" \
"v_mfma_f32_16x16x32_bf16 v[144:147], a[188:191], a[148:151], v[144:147]\n" \
"ds_write_b128 v27, v[68:71] offset:4096\n" \
"v_mfma_f32_16x16x32_bf16 v[144:147], a[252:255], a[212:215], v[144:147]\n" \
"buffer_load_dwordx4 v20, s[80:83], s75 offen offset:2048 sc1 lds\n" \
"v_mfma_f32_16x16x32_bf16 v[140:143], a[184:187], a[148:151], v[140:143]\n" \
"v_cvt_pk_bf16_f32 v180, v180, v181\n" \
"v_accvgpr_read_b32 v50, a82\n" \
"v_mfma_f32_16x16x32_bf16 v[140:143], a[248:251], a[212:215], v[140:143]\n" \
"ds_write_b128 v27, v[76:79] offset:5120\n" \
"v_mfma_f32_16x16x32_bf16 v[136:139], a[180:183], a[148:151], v[136:139]\n" \
"v_accvgpr_read_b32 v51, a83\n" \
"v_cvt_pk_bf16_f32 v181, v182, v183\n" \
"v_mfma_f32_16x16x32_bf16 v[136:139], a[244:247], a[212:215], v[136:139]\n" \
"buffer_load_dwordx4 v20, s[80:83], s75 offen offset:3072 sc1 lds\n" \
"v_mfma_f32_16x16x32_bf16 v[132:135], a[176:179], a[148:151], v[132:135]\n" \
"s_mov_b32 m0, s53\n" \
"v_cvt_pk_bf16_f32 v182, v50, v51\n" \
"v_mfma_f32_16x16x32_bf16 v[132:135], a[240:243], a[212:215], v[132:135]\n" \
"v_mov_b32_e32 v183, 0x3f80\n" \
"v_cvt_pk_bf16_f32 v234, v244, v245\n" \
"v_mfma_f32_16x16x32_bf16 v[148:151], a[176:179], a[152:155], v[148:151]\n" \
"v_mov_b32_e32 v235, 0\n" \
"v_cvt_pk_bf16_f32 v54, v192, v193\n" \
"v_mfma_f32_16x16x32_bf16 v[148:151], a[240:243], a[216:219], v[148:151]\n" \
"buffer_load_dwordx4 v20, s[80:83], s76 offen offset:0 sc1 lds\n" \
"v_mfma_f32_16x16x32_bf16 v[152:155], a[180:183], a[152:155], v[152:155]\n" \
"v_cvt_pk_bf16_f32 v55, v194, v195\n" \
"v_cvt_pk_bf16_f32 v56, v208, v209\n" \
"v_mfma_f32_16x16x32_bf16 v[152:155], a[244:247], a[216:219], v[152:155]\n" \
"v_cvt_pk_bf16_f32 v57, v210, v211\n" \
"v_cvt_pk_bf16_f32 v58, v224, v225\n" \
"v_cvt_pk_bf16_f32 v59, v226, v227\n" \
"v_cvt_pk_bf16_f32 v60, v240, v241\n" \
"v_cvt_pk_bf16_f32 v61, v242, v243\n" \
"v_mfma_f32_16x16x32_bf16 v[32:35], v[180:183], v[184:187], 0\n" \
"buffer_load_dwordx4 v20, s[80:83], s76 offen offset:1024 sc1 lds\n" \
"v_mfma_f32_16x16x32_bf16 v[156:159], a[184:187], a[152:155], v[156:159]\n" \
"v_mfma_f32_16x16x32_bf16 v[192:195], v[188:191], v[184:187], 0\n" \
"v_mfma_f32_16x16x32_bf16 v[208:211], v[204:207], v[200:203], 0\n" \
"v_mfma_f32_16x16x32_bf16 v[36:39], v[188:191], v[200:203], 0\n" \
"v_mfma_f32_16x16x32_bf16 v[224:227], v[220:223], v[216:219], 0\n" \
"v_mfma_f32_16x16x32_bf16 v[248:251], v[204:207], v[216:219], 0\n" \
"v_mfma_f32_16x16x32_bf16 v[240:243], v[236:239], v[232:235], 0\n" \
"v_mfma_f32_16x16x32_bf16 v[64:67], v[220:223], v[232:235], 0\n" \
"s_mov_b32 s64, 0xaaaaaaaa\n" \
"s_mov_b32 s65, 0xaaaaaaaa\n" \
"v_mfma_f32_16x16x32_bf16 v[156:159], a[248:251], a[216:219], v[156:159]\n" \
"v_cndmask_b32_e64 v32, v32, v33, s[64:65]\n" \
"v_cndmask_b32_e64 v34, v34, v35, s[64:65]\n" \
"v_mfma_f32_16x16x32_bf16 v[160:163], a[188:191], a[152:155], v[160:163]\n" \
"s_mov_b32 s66, 0xfff0ffff\n" \
"s_mov_b32 s67, 0xf000ff00\n" \
"s_mov_b32 s68, 0xffc0fffc\n" \
"s_mov_b32 s69, 0xc000fc00\n" \
"v_cndmask_b32_e64 v192, v192, v193, s[64:65]\n" \
"v_add_f32_e64 v32, s47, v32\n" \
"v_cndmask_b32_e64 v194, v194, v195, s[64:65]\n" \
"v_add_f32_e64 v34, s47, v34\n" \
"v_cndmask_b32_e64 v208, v208, v209, s[64:65]\n" \
"v_cndmask_b32_e64 v36, v36, v37, s[64:65]\n" \
"v_cndmask_b32_e64 v210, v210, v211, s[64:65]\n" \
"v_cndmask_b32_e64 v38, v38, v39, s[64:65]\n" \
"v_cndmask_b32_e64 v224, v224, v225, s[64:65]\n" \
"v_cndmask_b32_e64 v248, v248, v249, s[64:65]\n" \
"v_cndmask_b32_e64 v226, v226, v227, s[64:65]\n" \
"v_cndmask_b32_e64 v250, v250, v251, s[64:65]\n" \
"v_cndmask_b32_e64 v240, v240, v241, s[64:65]\n" \
"v_cndmask_b32_e64 v64, v64, v65, s[64:65]\n" \
"v_cndmask_b32_e64 v242, v242, v243, s[64:65]\n" \
"v_cndmask_b32_e64 v66, v66, v67, s[64:65]\n" \
"v_cndmask_b32_e64 v192, v32, v192, s[66:67]\n" \
"v_cndmask_b32_e64 v194, v34, v194, s[68:69]\n" \
"v_cndmask_b32_e64 v208, v36, v208, s[66:67]\n" \
"v_cndmask_b32_e64 v210, v38, v210, s[68:69]\n" \
"v_cndmask_b32_e64 v224, v248, v224, s[66:67]\n" \
"v_cndmask_b32_e64 v226, v250, v226, s[68:69]\n" \
"v_cndmask_b32_e64 v240, v64, v240, s[66:67]\n" \
"v_cndmask_b32_e64 v242, v66, v242, s[68:69]\n" \
"v_mfma_f32_16x16x32_bf16 v[160:163], a[252:255], a[216:219], v[160:163]\n" \
"v_max_f32_e32 v193, v192, v194\n" \
"v_max_f32_e32 v209, v208, v210\n" \
"v_max_f32_e32 v225, v224, v226\n" \
"v_max_f32_e32 v241, v240, v242\n" \
"v_mul_f32_e32 v84, s70, v84\n" \
"v_permlane32_swap_b32_e32 v193, v225\n" \
"v_permlane32_swap_b32_e32 v209, v241\n" \
"v_mul_f32_e32 v85, s70, v85\n" \
"v_mul_f32_e32 v92, s70, v92\n" \
"v_mul_f32_e32 v93, s70, v93\n" \
"v_permlane32_swap_b32_e32 v196, v228\n" \
"v_permlane32_swap_b32_e32 v212, v244\n" \
"v_max_f32_e32 v193, v193, v225\n" \
"v_max_f32_e32 v209, v209, v241\n" \
"v_accvgpr_read_b32 v48, a80\n" \
"v_accvgpr_read_b32 v49, a81\n" \
"v_mul_f32_e32 v86, s70, v86\n" \
"v_mul_f32_e32 v87, s70, v87\n" \
"v_cvt_pk_bf16_f32 v84, v84, v85\n" \
"v_mul_f32_e32 v94, s70, v94\n" \
"v_mul_f32_e32 v95, s70, v95\n" \
"v_cvt_pk_bf16_f32 v92, v92, v93\n" \
"v_permlane16_swap_b32_e32 v228, v244\n" \
"v_permlane16_swap_b32_e32 v193, v209\n" \
"v_mfma_f32_16x16x32_bf16 v[176:179], a[188:191], a[156:159], v[176:179]\n" \
"s_mov_b32 s72, 0x3eae1a93\n" \
"v_mul_f32_e32 v88, s70, v88\n" \
"v_mul_f32_e32 v89, s70, v89\n" \
"v_mul_f32_e32 v90, s70, v90\n" \
"v_mul_f32_e32 v91, s70, v91\n" \
"v_cvt_pk_bf16_f32 v85, v86, v87\n" \
"v_mul_f32_e32 v96, s70, v96\n" \
"v_mul_f32_e32 v97, s70, v97\n" \
"v_mul_f32_e32 v98, s70, v98\n" \
"v_mul_f32_e32 v99, s70, v99\n" \
"v_cvt_pk_bf16_f32 v93, v94, v95\n" \
"v_mul_f32_e32 v45, 0xbfb8aa3b, v228\n" \
"v_mfma_f32_16x16x16_bf16 v[48:51], v[182:183], v[46:47], 0\n" \
"v_mfma_f32_16x16x16_bf16 v[196:199], v[190:191], v[46:47], 0\n" \
"v_mfma_f32_16x16x16_bf16 v[212:215], v[206:207], v[46:47], 0\n" \
"v_max_f32_e64 v193, v193, v209\n" \
"v_cvt_pk_bf16_f32 v86, v88, v89\n" \
"v_cvt_pk_bf16_f32 v87, v90, v91\n" \
"v_cvt_pk_bf16_f32 v94, v96, v97\n" \
"v_cvt_pk_bf16_f32 v95, v98, v99\n" \
"v_mfma_f32_16x16x16_bf16 v[228:231], v[222:223], v[46:47], 0\n" \
"v_mul_f32_e32 v193, s72, v193\n" \
"v_mov_b32_e32 v209, v193\n" \
"ds_write_b128 v27, v[84:87] offset:8192\n" \
"v_mfma_f32_16x16x16_bf16 v[244:247], v[238:239], v[46:47], 0\n" \
"v_permlane16_swap_b32_e32 v193, v209\n" \
"s_mov_b32 s71, 0x3e0293ee\n" \
"v_mfma_f32_16x16x32_bf16 v[176:179], a[252:255], a[220:223], v[176:179]\n" \
"v_mov_b32_e32 v225, v193\n" \
"v_mov_b32_e32 v241, v209\n" \
"v_mul_f32_e32 v100, s70, v100\n" \
"v_mul_f32_e64 v101, s70, v101\n" \
"ds_write_b128 v27, v[92:95] offset:9216\n" \
"v_mul_f32_e32 v108, s70, v108\n" \
"v_mul_f32_e32 v109, s70, v109\n" \
"v_pk_mul_f32 v[116:117], v[116:117], s[70:71]\n" \
"v_pk_mul_f32 v[124:125], v[124:125], s[70:71]\n" \
"v_pk_mul_f32 v[132:133], v[132:133], s[70:71]\n" \
"v_pk_mul_f32 v[140:141], v[140:141], s[70:71]\n" \
"v_pk_mul_f32 v[148:149], v[148:149], s[70:71]\n" \
"v_pk_mul_f32 v[156:157], v[156:157], s[70:71]\n" \
"v_permlane32_swap_b32_e32 v193, v225\n" \
"v_permlane32_swap_b32_e32 v209, v241\n" \
"v_mul_f32_e32 v102, s70, v102\n" \
"v_mul_f32_e32 v103, s70, v103\n" \
"v_cvt_pk_bf16_f32 v100, v100, v101\n" \
"v_mul_f32_e32 v110, s70, v110\n" \
"v_mul_f32_e32 v111, s70, v111\n" \
"v_cvt_pk_bf16_f32 v108, v108, v109\n" \
"v_pk_mul_f32 v[118:119], v[118:119], s[70:71]\n" \
"v_cvt_pk_bf16_f32 v116, v116, v117\n" \
"v_pk_mul_f32 v[126:127], v[126:127], s[70:71]\n" \
"v_cvt_pk_bf16_f32 v124, v124, v125\n" \
"v_pk_mul_f32 v[134:135], v[134:135], s[70:71]\n" \
"v_cvt_pk_bf16_f32 v132, v132, v133\n" \
"v_pk_mul_f32 v[142:143], v[142:143], s[70:71]\n" \
"v_cvt_pk_bf16_f32 v140, v140, v141\n" \
"v_pk_mul_f32 v[150:151], v[150:151], s[70:71]\n" \
"v_cvt_pk_bf16_f32 v148, v148, v149\n" \
"v_pk_mul_f32 v[158:159], v[158:159], s[70:71]\n" \
"v_cvt_pk_bf16_f32 v156, v156, v157\n" \
"v_fma_f32 v192, v192, s72, -v193\n" \
"v_fma_f32 v194, v194, s72, -v193\n" \
"v_fma_f32 v208, v208, s72, -v209\n" \
"v_fma_f32 v210, v210, s72, -v209\n" \
"v_mul_f32_e32 v104, s70, v104\n" \
"v_mul_f32_e32 v105, s70, v105\n" \
"v_mul_f32_e32 v106, s70, v106\n" \
"v_mul_f32_e32 v107, s70, v107\n" \
"v_cvt_pk_bf16_f32 v101, v102, v103\n" \
"v_mul_f32_e32 v112, s70, v112\n" \
"v_mul_f32_e32 v113, s70, v113\n" \
"v_mul_f32_e32 v114, s70, v114\n" \
"v_mul_f32_e32 v115, s70, v115\n" \
"v_cvt_pk_bf16_f32 v109, v110, v111\n" \
"v_pk_mul_f32 v[120:121], v[120:121], s[70:71]\n" \
"v_pk_mul_f32 v[122:123], v[122:123], s[70:71]\n" \
"v_cvt_pk_bf16_f32 v117, v118, v119\n" \
"v_pk_mul_f32 v[128:129], v[128:129], s[70:71]\n" \
"v_pk_mul_f32 v[130:131], v[130:131], s[70:71]\n" \
"v_cvt_pk_bf16_f32 v125, v126, v127\n" \
"v_pk_mul_f32 v[136:137], v[136:137], s[70:71]\n" \
"v_pk_mul_f32 v[138:139], v[138:139], s[70:71]\n" \
"v_cvt_pk_bf16_f32 v133, v134, v135\n" \
"v_pk_mul_f32 v[144:145], v[144:145], s[70:71]\n" \
"v_pk_mul_f32 v[146:147], v[146:147], s[70:71]\n" \
"v_cvt_pk_bf16_f32 v141, v142, v143\n" \
"v_pk_mul_f32 v[152:153], v[152:153], s[70:71]\n" \
"v_pk_mul_f32 v[154:155], v[154:155], s[70:71]\n" \
"v_cvt_pk_bf16_f32 v149, v150, v151\n" \
"v_pk_mul_f32 v[160:161], v[160:161], s[70:71]\n" \
"v_pk_mul_f32 v[162:163], v[162:163], s[70:71]\n" \
"v_cvt_pk_bf16_f32 v157, v158, v159\n" \
"v_pk_mul_f32 v[176:177], v[176:177], s[70:71]\n" \
"v_pk_mul_f32 v[178:179], v[178:179], s[70:71]\n" \
"v_exp_f32_e32 v192, v192\n" \
"v_exp_f32_e32 v194, v194\n" \
"v_exp_f32_e32 v208, v208\n" \
"v_exp_f32_e32 v210, v210\n" \
"v_fma_f32 v224, v224, s72, -v225\n" \
"v_fma_f32 v226, v226, s72, -v225\n" \
"v_mfma_f32_16x16x32_bf16 v[172:175], a[184:187], a[156:159], v[172:175]\n" \
"v_cvt_pk_bf16_f32 v102, v104, v105\n" \
"v_cvt_pk_bf16_f32 v103, v106, v107\n" \
"v_cvt_pk_bf16_f32 v110, v112, v113\n" \
"v_cvt_pk_bf16_f32 v111, v114, v115\n" \
"v_cvt_pk_bf16_f32 v118, v120, v121\n" \
"v_cvt_pk_bf16_f32 v119, v122, v123\n" \
"v_cvt_pk_bf16_f32 v126, v128, v129\n" \
"v_cvt_pk_bf16_f32 v127, v130, v131\n" \
"v_cvt_pk_bf16_f32 v134, v136, v137\n" \
"v_cvt_pk_bf16_f32 v135, v138, v139\n" \
"v_cvt_pk_bf16_f32 v142, v144, v145\n" \
"v_cvt_pk_bf16_f32 v143, v146, v147\n" \
"v_cvt_pk_bf16_f32 v150, v152, v153\n" \
"v_cvt_pk_bf16_f32 v151, v154, v155\n" \
"v_cvt_pk_bf16_f32 v158, v160, v161\n" \
"v_cvt_pk_bf16_f32 v159, v162, v163\n" \
"v_exp_f32_e32 v224, v224\n" \
"v_exp_f32_e32 v226, v226\n" \
"v_fma_f32 v240, v240, s72, -v241\n" \
"v_fma_f32 v242, v242, s72, -v241\n" \
"ds_write_b128 v27, v[100:103] offset:12288\n" \
"v_cvt_pk_bf16_f32 v192, v192, v194\n" \
"v_cvt_pk_bf16_f32 v208, v208, v210\n" \
"v_exp_f32_e32 v240, v240\n" \
"v_exp_f32_e32 v242, v242\n" \
"v_cvt_pk_bf16_f32 v68, v48, v49\n" \
"v_cvt_pk_bf16_f32 v69, v50, v51\n" \
"v_cvt_pk_bf16_f32 v70, v196, v197\n" \
"v_cvt_pk_bf16_f32 v71, v198, v199\n" \
"ds_write_b128 v27, v[108:111] offset:13312\n" \
"v_cvt_pk_bf16_f32 v72, v212, v213\n" \
"v_cvt_pk_bf16_f32 v73, v214, v215\n" \
"v_perm_b32 v184, v192, v192, v40\n" \
"v_perm_b32 v185, v192, v192, v41\n" \
"v_perm_b32 v186, v192, v192, v42\n" \
"v_perm_b32 v187, v192, v192, v43\n" \
"v_perm_b32 v200, v208, v208, v40\n" \
"v_perm_b32 v201, v208, v208, v41\n" \
"v_perm_b32 v202, v208, v208, v42\n" \
"v_perm_b32 v203, v208, v208, v43\n" \
"ds_write_b128 v21, v[116:119] offset:16384\n" \
"v_cvt_pk_bf16_f32 v224, v224, v226\n" \
"v_cvt_pk_bf16_f32 v74, v228, v229\n" \
"v_cvt_pk_bf16_f32 v75, v230, v231\n" \
"v_mfma_f32_16x16x32_bf16 v[32:35], v[68:71], v[184:187], 0\n" \
"v_mfma_f32_16x16x32_bf16 v[36:39], v[70:73], v[200:203], 0\n" \
"v_perm_b32 v216, v224, v224, v40\n" \
"v_perm_b32 v217, v224, v224, v41\n" \
"ds_write_b128 v21, v[124:127] offset:17408\n" \
"v_perm_b32 v218, v224, v224, v42\n" \
"v_perm_b32 v219, v224, v224, v43\n" \
"v_cvt_pk_bf16_f32 v240, v240, v242\n" \
"v_mfma_f32_16x16x32_bf16 v[172:175], a[248:251], a[220:223], v[172:175]\n" \
"v_cvt_pk_bf16_f32 v76, v244, v245\n" \
"v_cvt_pk_bf16_f32 v77, v246, v247\n" \
"v_mfma_f32_16x16x32_bf16 v[248:251], v[72:75], v[216:219], 0\n" \
"v_perm_b32 v232, v240, v240, v40\n" \
"v_perm_b32 v233, v240, v240, v41\n" \
"ds_write_b128 v21, v[132:135] offset:20480\n" \
"v_perm_b32 v234, v240, v240, v42\n" \
"v_perm_b32 v235, v240, v240, v43\n" \
"v_pk_mul_f32 v[172:173], v[172:173], s[70:71]\n" \
"v_pk_mul_f32 v[174:175], v[174:175], s[70:71]\n" \
"v_cvt_pk_bf16_f32 v172, v172, v173\n" \
"v_cvt_pk_bf16_f32 v173, v174, v175\n" \
"v_cvt_pk_bf16_f32 v174, v176, v177\n" \
"v_cvt_pk_bf16_f32 v175, v178, v179\n" \
"v_mfma_f32_16x16x32_bf16 v[64:67], v[74:77], v[232:235], 0\n" \
"ds_write_b128 v21, v[140:143] offset:21504\n" \
"v_mfma_f32_16x16x32_bf16 v[168:171], a[180:183], a[156:159], v[168:171]\n" \
"v_cvt_pk_bf16_f32 v52, v252, v253\n" \
"v_cvt_pk_bf16_f32 v53, v254, v255\n" \
"v_mfma_f32_16x16x32_bf16 v[168:171], a[244:247], a[220:223], v[168:171]\n" \
"v_exp_f32_e64 v45, v45\n" \
"v_mfma_f32_16x16x32_bf16 v[192:195], v[52:55], v[184:187], 0\n" \
"ds_write_b128 v21, v[148:151] offset:24576\n" \
"v_mfma_f32_16x16x32_bf16 v[208:211], v[54:57], v[200:203], 0\n" \
"v_mfma_f32_16x16x32_bf16 v[224:227], v[56:59], v[216:219], 0\n" \
"v_mfma_f32_16x16x32_bf16 v[240:243], v[58:61], v[232:235], 0\n" \
"ds_write_b128 v21, v[156:159] offset:25600\n" \
"v_mov_b32_dpp v34, v38 quad_perm:[0,1,2,3] row_mask:0x2 bank_mask:0xf\n" \
"v_pk_mul_f32 v[168:169], v[168:169], s[70:71]\n" \
"v_pk_mul_f32 v[170:171], v[170:171], s[70:71]\n" \
"v_mov_b32_dpp v34, v250 quad_perm:[0,1,2,3] row_mask:0x4 bank_mask:0xf\n" \
"v_add_f32_e64 v45, 1.0, v45\n" \
"v_mfma_f32_16x16x32_bf16 v[164:167], a[176:179], a[156:159], v[164:167]\n" \
"v_mov_b32_dpp v34, v66 quad_perm:[0,1,2,3] row_mask:0x8 bank_mask:0xf\n" \
"v_mov_b32_dpp v32, v36 quad_perm:[0,1,2,3] row_mask:0x2 bank_mask:0xf\n" \
"v_rcp_f32_e64 v45, v45\n" \
"ds_write_b128 v21, v[172:175] offset:29696\n" \
"v_mov_b32_dpp v33, v37 quad_perm:[0,1,2,3] row_mask:0x2 bank_mask:0xf\n" \
"v_rcp_f32_e64 v34, v34\n" \
"v_mov_b32_dpp v32, v248 quad_perm:[0,1,2,3] row_mask:0x4 bank_mask:0xf\n" \
"v_mov_b32_dpp v33, v249 quad_perm:[0,1,2,3] row_mask:0x4 bank_mask:0xf\n" \
"v_mul_f32_e64 v34, v45, v34\n" \
"v_mov_b32_dpp v32, v64 quad_perm:[0,1,2,3] row_mask:0x8 bank_mask:0xf\n" \
"v_mov_b32_dpp v33, v65 quad_perm:[0,1,2,3] row_mask:0x8 bank_mask:0xf\n" \
"v_mul_f32_e32 v32, v34, v32\n" \
"v_mul_f32_e32 v33, v34, v33\n" \
"v_mov_b32_e64 v38, v34\n" \
"v_mfma_f32_16x16x32_bf16 v[164:167], a[240:243], a[220:223], v[164:167]\n" \
"v_sub_f32_e32 v44, 1.0, v45\n" \
"v_permlane16_swap_b32_e32 v34, v38\n" \
"v_mov_b32_e32 v250, v34\n" \
"v_mov_b32_e32 v66, v38\n" \
"v_cvt_pk_bf16_f32 v32, v32, v33\n" \
"v_permlane32_swap_b32_e32 v34, v250\n" \
"v_permlane32_swap_b32_e32 v38, v66\n" \
"v_mul_f32_e64 v192, v34, v192\n" \
"v_pk_mul_f32 v[164:165], v[164:165], s[70:71]\n" \
"v_pk_mul_f32 v[166:167], v[166:167], s[70:71]\n" \
"v_cvt_pk_bf16_f32 v164, v164, v165\n" \
"v_cvt_pk_bf16_f32 v165, v166, v167\n" \
"v_cvt_pk_bf16_f32 v166, v168, v169\n" \
"v_cvt_pk_bf16_f32 v167, v170, v171\n" \
"ds_write_b128 v21, v[164:167] offset:28672\n" \
"v_mul_f32_e32 v193, v34, v193\n" \
"v_mul_f32_e32 v208, v38, v208\n" \
"v_mul_f32_e32 v209, v38, v209\n" \
"v_mul_f32_e32 v224, v250, v224\n" \
"v_mul_f32_e32 v225, v250, v225\n" \
"v_mul_f32_e32 v240, v66, v240\n" \
"v_mul_f32_e32 v241, v66, v241\n" \
"v_mul_f32_e32 v194, v34, v194\n" \
"v_mul_f32_e64 v195, v34, v195\n" \
"v_cvt_pk_bf16_f32 v192, v192, v193\n" \
"v_mul_f32_e32 v210, v38, v210\n" \
"v_mul_f32_e32 v211, v38, v211\n" \
"v_cvt_pk_bf16_f32 v208, v208, v209\n" \
"v_mul_f32_e32 v226, v250, v226\n" \
"v_mul_f32_e32 v227, v250, v227\n" \
"v_cvt_pk_bf16_f32 v224, v224, v225\n" \
"v_mul_f32_e32 v242, v66, v242\n" \
"v_mul_f32_e32 v243, v66, v243\n" \
"v_cvt_pk_bf16_f32 v240, v240, v241\n" \
"v_cvt_pk_bf16_f32 v193, v194, v195\n" \
"s_add_u32 s84, s48, 0x0\n" \
"s_nop 0\n" \
"v_cvt_pk_bf16_f32 v209, v210, v211\n" \
"s_add_u32 s85, s48, 0x24800\n" \
"v_cvt_pk_bf16_f32 v225, v226, v227\n" \
"s_add_u32 s86, s48, 0x49000\n" \
"v_cvt_pk_bf16_f32 v241, v242, v243\n" \
"s_add_u32 s87, s48, 0x6d800\n" \
"ds_write_b32 v26, v44\n" \
"buffer_store_dword v32, v25, s[80:83], s49 offen sc1\n" \
"buffer_store_dwordx2 v[192:193], v24, s[80:83], s84 offen sc1\n" \
"buffer_store_dwordx2 v[208:209], v24, s[80:83], s85 offen sc1\n" \
"buffer_store_dwordx2 v[224:225], v24, s[80:83], s86 offen sc1\n" \
"buffer_store_dwordx2 v[240:241], v24, s[80:83], s87 offen sc1\n" \
"s_branch L_end_%=\n" \
"L_poll60_%=:\n" \
"s_mov_b32 s84, 0\n" \
"L_pl60_%=:\n" \
"s_sleep 1\n" \
"v_bfe_u32 v44, v20, 2, 5\n" \
"buffer_load_dword v44, v44, s[80:83], s38 offen offset:128 sc1\n" \
"s_waitcnt vmcnt(0)\n" \
"v_cmp_ne_u32_e32 vcc, s62, v44\n" \
"s_add_u32 s84, s84, 1\n" \
"s_nop 3\n" \
"s_cbranch_vccz L_pollr60_%=\n" \
"s_cmp_gt_u32 s84, 0x400000\n" \
"s_cbranch_scc0 L_pl60_%=\n" \
"s_branch L_pollr60_%=\n" \
"L_poll160_%=:\n" \
"s_mov_b32 s84, 0\n" \
"L_pl160_%=:\n" \
"s_sleep 1\n" \
"v_bfe_u32 v44, v20, 2, 5\n" \
"buffer_load_dword v44, v44, s[80:83], s38 offen offset:0 sc1\n" \
"s_waitcnt vmcnt(0)\n" \
"v_cmp_ne_u32_e32 vcc, s62, v44\n" \
"s_add_u32 s84, s84, 1\n" \
"s_nop 3\n" \
"s_cbranch_vccz L_pollr160_%=\n" \
"s_cmp_gt_u32 s84, 0x400000\n" \
"s_cbranch_scc0 L_pl160_%=\n" \
"s_branch L_pollr160_%=\n" \
".fill (L_pb + 42440 - .) / 4, 4, 0xBF800000\n" \
"L_w1_%=:\n" \
"s_waitcnt vmcnt(18)\n" \
"s_barrier\n" \
"ds_read_b64_tr_b16 a[160:161], v32 offset:0\n" \
"ds_read_b64_tr_b16 a[162:163], v32 offset:1024\n" \
"ds_read_b128 a[128:131], v28 offset:16384\n" \
"ds_read_b64_tr_b16 a[224:225], v32 offset:8192\n" \
"ds_read_b64_tr_b16 a[226:227], v32 offset:9216\n" \
"ds_read_b128 a[192:195], v29 offset:16384\n" \
"ds_read_b128 a[132:135], v28 offset:18432\n" \
"ds_read_b128 a[196:199], v29 offset:18432\n" \
"ds_read_b64_tr_b16 a[164:165], v33 offset:0\n" \
"ds_read_b64_tr_b16 a[166:167], v33 offset:1024\n" \
"ds_read_b64_tr_b16 a[228:229], v33 offset:8192\n" \
"ds_read_b64_tr_b16 a[230:231], v33 offset:9216\n" \
"ds_read_b128 a[136:139], v28 offset:20480\n" \
"ds_read_b128 a[200:203], v29 offset:20480\n" \
"s_waitcnt lgkmcnt(11)\n" \
"v_mfma_f32_16x16x32_bf16 a[0:3], a[128:131], a[160:163], 0\n" \
"ds_read_b64_tr_b16 a[168:169], v34 offset:0\n" \
"v_mad_i32_i24 v217, v216, -6, v215\n" \
"s_waitcnt lgkmcnt(9)\n" \
"v_mfma_f32_16x16x32_bf16 a[0:3], a[192:195], a[224:227], a[0:3]\n" \
"ds_read_b64_tr_b16 a[170:171], v34 offset:1024\n" \
"v_add_u32_e32 v218, 0xfffffe50, v215\n" \
"s_waitcnt lgkmcnt(9)\n" \
"v_mfma_f32_16x16x32_bf16 a[16:19], a[132:135], a[160:163], 0\n" \
"ds_read_b64_tr_b16 a[232:233], v34 offset:8192\n" \
"s_mul_i32 s35, s6, 0x48\n" \
"s_waitcnt lgkmcnt(9)\n" \
"v_mfma_f32_16x16x32_bf16 a[16:19], a[196:199], a[224:227], a[16:19]\n" \
"ds_read_b64_tr_b16 a[234:235], v34 offset:9216\n" \
"v_lshrrev_b32_e32 v219, 1, v217\n" \
"s_waitcnt lgkmcnt(8)\n" \
"v_mfma_f32_16x16x32_bf16 a[20:23], a[132:135], a[164:167], 0\n" \
"ds_read_b128 a[140:143], v28 offset:22528\n" \
"v_add_u32_e32 v218, s35, v216\n" \
"s_waitcnt lgkmcnt(7)\n" \
"v_mfma_f32_16x16x32_bf16 a[20:23], a[196:199], a[228:231], a[20:23]\n" \
"ds_read_b128 a[204:207], v29 offset:22528\n" \
"v_mul_u32_u24_e32 v219, 0x480, v219\n" \
"v_mfma_f32_16x16x32_bf16 a[4:7], a[128:131], a[164:167], 0\n" \
"ds_read_b64_tr_b16 a[172:173], v35 offset:0\n" \
"v_and_b32_e64 v214, 16, v20\n" \
"v_mfma_f32_16x16x32_bf16 a[4:7], a[192:195], a[228:231], a[4:7]\n" \
"ds_read_b64_tr_b16 a[174:175], v35 offset:1024\n" \
"v_mad_u32_u24 v219, v218, s5, v219\n" \
"s_waitcnt lgkmcnt(9)\n" \
"v_mfma_f32_16x16x32_bf16 a[32:35], a[136:139], a[160:163], 0\n" \
"ds_read_b64_tr_b16 a[236:237], v35 offset:8192\n" \
"v_or_b32_e32 v192, v219, v214\n" \
"s_waitcnt lgkmcnt(9)\n" \
"v_mfma_f32_16x16x32_bf16 a[32:35], a[200:203], a[224:227], a[32:35]\n" \
"ds_read_b64_tr_b16 a[238:239], v35 offset:9216\n" \
"v_mov_b32_e32 v220, 2.0\n" \
"v_mfma_f32_16x16x32_bf16 a[36:39], a[136:139], a[164:167], 0\n" \
"ds_read_b128 a[144:147], v28 offset:0\n" \
"v_mov_b32_e64 v222, 7\n" \
"v_mfma_f32_16x16x32_bf16 a[36:39], a[200:203], a[228:231], a[36:39]\n" \
"ds_read_b128 a[208:211], v29 offset:0\n" \
"v_lshlrev_b32_e32 v220, 7, v218\n" \
"s_waitcnt lgkmcnt(10)\n" \
"v_mfma_f32_16x16x32_bf16 a[40:43], a[136:139], a[168:171], 0\n" \
"ds_read_b128 a[148:151], v28 offset:2048\n" \
"v_lshrrev_b32_e32 v218, 1, v218\n" \
"s_waitcnt lgkmcnt(9)\n" \
"v_mfma_f32_16x16x32_bf16 a[40:43], a[200:203], a[232:235], a[40:43]\n" \
"ds_read_b128 a[212:215], v29 offset:2048\n" \
"v_and_b32_e64 v218, 4, v218\n" \
"v_mfma_f32_16x16x32_bf16 a[24:27], a[132:135], a[168:171], 0\n" \
"ds_read_b128 a[152:155], v28 offset:4096\n" \
"v_and_b32_e64 v216, 2, v216\n" \
"v_mfma_f32_16x16x32_bf16 a[24:27], a[196:199], a[232:235], a[24:27]\n" \
"ds_read_b128 a[216:219], v29 offset:4096\n" \
"s_mul_i32 s7, s6, 0x2400\n" \
"v_mfma_f32_16x16x32_bf16 a[8:11], a[128:131], a[168:171], 0\n" \
"ds_read_b128 a[156:159], v28 offset:6144\n" \
"s_sub_i32 s34, 0x22000, s7\n" \
"v_mfma_f32_16x16x32_bf16 a[8:11], a[192:195], a[232:235], a[8:11]\n" \
"ds_read_b128 a[220:223], v29 offset:6144\n" \
"s_mov_b32 m0, s32\n" \
"s_waitcnt lgkmcnt(13)\n" \
"v_mfma_f32_16x16x32_bf16 a[48:51], a[140:143], a[160:163], 0\n" \
"s_mov_b32 exec_lo, 0xff000000\n" \
"s_mov_b32 exec_hi, 0x0\n" \
"global_load_lds_dword v[184:185], off\n" \
"s_mov_b64 exec, -1\n" \
"s_waitcnt lgkmcnt(12)\n" \
"v_mfma_f32_16x16x32_bf16 a[48:51], a[204:207], a[224:227], a[48:51]\n" \
"ds_read_b64_tr_b16 a[176:177], v36 offset:0\n" \
"v_bitop3_b32 v216, v217, v218, v216 bitop3:0x1e\n" \
"v_mfma_f32_16x16x32_bf16 a[52:55], a[140:143], a[164:167], 0\n" \
"ds_read_b64_tr_b16 a[178:179], v36 offset:1024\n" \
"v_or_b32_e32 v213, 0x24400, v20\n" \
"v_mfma_f32_16x16x32_bf16 a[52:55], a[204:207], a[228:231], a[52:55]\n" \
"s_mov_b32 m0, s32\n" \
"ds_read_b64_tr_b16 a[240:241], v36 offset:8192\n" \
"v_mfma_f32_16x16x32_bf16 a[56:59], a[140:143], a[168:171], 0\n" \
"s_mov_b32 exec_lo, 0xffff\n" \
"s_mov_b32 exec_hi, 0x0\n" \
"global_load_lds_dword v[186:187], off\n" \
"s_mov_b64 exec, -1\n" \
"v_mfma_f32_16x16x32_bf16 a[56:59], a[204:207], a[232:235], a[56:59]\n" \
"ds_read_b64_tr_b16 a[242:243], v36 offset:9216\n" \
"v_lshlrev_b32_e32 v216, 4, v216\n" \
"s_waitcnt lgkmcnt(14)\n" \
"v_mfma_f32_16x16x32_bf16 a[60:63], a[140:143], a[172:175], 0\n" \
"ds_read_b64_tr_b16 a[180:181], v37 offset:0\n" \
"v_add3_u32 v196, v220, s34, v216\n" \
"s_waitcnt lgkmcnt(13)\n" \
"v_mfma_f32_16x16x32_bf16 a[60:63], a[204:207], a[236:239], a[60:63]\n" \
"ds_read_b64_tr_b16 a[182:183], v37 offset:1024\n" \
"v_mov_b32_e32 v216, v213\n" \
"v_mfma_f32_16x16x32_bf16 a[44:47], a[136:139], a[172:175], 0\n" \
"ds_read_b64_tr_b16 a[244:245], v37 offset:8192\n" \
"s_movk_i32 s36, 0x1b0\n" \
"v_mfma_f32_16x16x32_bf16 a[44:47], a[200:203], a[236:239], a[44:47]\n" \
"ds_read_b64_tr_b16 a[246:247], v37 offset:9216\n" \
"v_or_b32_e32 v216, 0x100, v215\n" \
"v_mfma_f32_16x16x32_bf16 a[28:31], a[132:135], a[172:175], 0\n" \
"ds_read_b64_tr_b16 a[184:185], v38 offset:0\n" \
"v_mul_u32_u24_e32 v217, 0x2aab, v216\n" \
"v_mfma_f32_16x16x32_bf16 a[28:31], a[196:199], a[236:239], a[28:31]\n" \
"ds_read_b64_tr_b16 a[186:187], v38 offset:1024\n" \
"v_add_u32_e32 v215, 0xffffff50, v215\n" \
"v_mfma_f32_16x16x32_bf16 a[12:15], a[128:131], a[172:175], 0\n" \
"v_lshrrev_b32_e32 v217, 16, v217\n" \
"v_mov_b32_e64 v220, v215\n" \
"v_mfma_f32_16x16x32_bf16 a[12:15], a[192:195], a[236:239], a[12:15]\n" \
"v_mad_i32_i24 v218, v217, -6, v216\n" \
"v_cmp_gt_u32_e32 vcc, s36, v216\n" \
"s_waitcnt lgkmcnt(15)\n" \
"v_mfma_f32_16x16x32_bf16 a[76:79], a[144:147], a[172:175], 0\n" \
"ds_read_b64_tr_b16 a[248:249], v38 offset:8192\n" \
"v_mov_b32_e64 v221, 7\n" \
"v_mfma_f32_16x16x32_bf16 a[76:79], a[208:211], a[236:239], a[76:79]\n" \
"v_lshrrev_b32_e32 v222, 1, v218\n" \
"v_cndmask_b32_e32 v217, v220, v217, vcc\n" \
"v_mfma_f32_16x16x32_bf16 a[72:75], a[144:147], a[168:171], 0\n" \
"s_movk_i32 s4, 0x48\n" \
"v_add_u32_e32 v220, s35, v217\n" \
"v_mfma_f32_16x16x32_bf16 a[72:75], a[208:211], a[232:235], a[72:75]\n" \
"ds_read_b64_tr_b16 a[250:251], v38 offset:9216\n" \
"v_mul_u32_u24_e32 v222, 0x480, v222\n" \
"v_mfma_f32_16x16x32_bf16 a[68:71], a[144:147], a[164:167], 0\n" \
"v_mad_u32_u24 v222, v220, s5, v222\n" \
"v_cmp_gt_u32_e64 s[4:5], s4, v215\n" \
"v_mfma_f32_16x16x32_bf16 a[68:71], a[208:211], a[228:231], a[68:71]\n" \
"v_cndmask_b32_e32 v215, v221, v218, vcc\n" \
"v_lshrrev_b32_e32 v218, 1, v220\n" \
"v_mfma_f32_16x16x32_bf16 a[64:67], a[144:147], a[160:163], 0\n" \
"v_or_b32_e32 v214, v222, v214\n" \
"v_mov_b32_e32 v222, 2.0\n" \
"v_mfma_f32_16x16x32_bf16 a[64:67], a[208:211], a[224:227], a[64:67]\n" \
"ds_read_b64_tr_b16 a[188:189], v39 offset:0\n" \
"v_and_b32_e64 v218, 4, v218\n" \
"v_mfma_f32_16x16x32_bf16 a[80:83], a[148:151], a[160:163], 0\n" \
"v_and_b32_e64 v217, 2, v217\n" \
"v_bitop3_b32 v215, v215, v218, v217 bitop3:0x1e\n" \
"s_waitcnt lgkmcnt(15)\n" \
"v_mfma_f32_16x16x32_bf16 a[80:83], a[212:215], a[224:227], a[80:83]\n" \
"v_lshlrev_b32_e32 v223, 1, v220\n" \
"v_mov_b32_e64 v224, 2.0\n" \
"v_mfma_f32_16x16x32_bf16 a[84:87], a[148:151], a[164:167], 0\n" \
"ds_read_b64_tr_b16 a[190:191], v39 offset:1024\n" \
"v_cndmask_b32_e64 v193, v222, v214, vcc\n" \
"v_mfma_f32_16x16x32_bf16 a[84:87], a[212:215], a[228:231], a[84:87]\n" \
"v_lshlrev_b32_e32 v214, 7, v220\n" \
"v_lshlrev_b32_e32 v215, 4, v215\n" \
"v_mfma_f32_16x16x32_bf16 a[88:91], a[148:151], a[168:171], 0\n" \
"s_mul_i32 s39, s3, 18\n" \
"v_add3_u32 v214, v214, s34, v215\n" \
"v_mfma_f32_16x16x32_bf16 a[88:91], a[212:215], a[232:235], a[88:91]\n" \
"ds_read_b64_tr_b16 a[252:253], v39 offset:8192\n" \
"v_cndmask_b32_e64 v195, v224, v223, s[4:5]\n" \
"v_mfma_f32_16x16x32_bf16 a[92:95], a[148:151], a[172:175], 0\n" \
"s_movk_i32 s4, 0x1f8\n" \
"v_mov_b32_e64 v215, v213\n" \
"v_mfma_f32_16x16x32_bf16 a[92:95], a[212:215], a[236:239], a[92:95]\n" \
"s_add_i32 s35, s35, s39\n" \
"v_cmp_gt_u32_e32 vcc, s4, v216\n" \
"v_mfma_f32_16x16x32_bf16 a[108:111], a[152:155], a[172:175], 0\n" \
"ds_read_b64_tr_b16 a[254:255], v39 offset:9216\n" \
"v_add_u32_e64 v212, s35, v212\n" \
"v_mfma_f32_16x16x32_bf16 a[108:111], a[216:219], a[236:239], a[108:111]\n" \
"v_mov_b32_e32 v219, 2.0\n" \
"v_cndmask_b32_e32 v197, v215, v214, vcc\n" \
"v_mfma_f32_16x16x32_bf16 a[104:107], a[152:155], a[168:171], 0\n" \
"s_movk_i32 s35, 0x120\n" \
"v_mul_u32_u24_e32 v214, 0xd80, v212\n" \
"v_mfma_f32_16x16x32_bf16 a[104:107], a[216:219], a[232:235], a[104:107]\n" \
"v_cmp_gt_u32_e32 vcc, s35, v20\n" \
"v_lshrrev_b32_e64 v215, 1, v212\n" \
"v_mfma_f32_16x16x32_bf16 a[100:103], a[152:155], a[164:167], 0\n" \
"s_and_b32 s4, s2, 7\n" \
"v_cndmask_b32_e32 v194, v219, v214, vcc\n" \
"v_mfma_f32_16x16x32_bf16 a[100:103], a[216:219], a[228:231], a[100:103]\n" \
"v_lshlrev_b32_e32 v214, 7, v212\n" \
"v_lshrrev_b32_e32 v212, 2, v212\n" \
"v_mfma_f32_16x16x32_bf16 a[96:99], a[152:155], a[160:163], 0\n" \
"v_and_b32_e64 v212, 2, v212\n" \
"v_and_or_b32 v212, v215, 1, v212\n" \
"v_mfma_f32_16x16x32_bf16 a[96:99], a[216:219], a[224:227], a[96:99]\n" \
"s_lshl_b32 s5, s4, 2\n" \
"s_bfe_u32 s38, s2, 0x20003\n" \
"s_waitcnt lgkmcnt(15)\n" \
"v_mfma_f32_16x16x32_bf16 a[112:115], a[156:159], a[160:163], 0\n" \
"v_lshlrev_b32_e32 v212, 5, v212\n" \
"s_movk_i32 s35, 0x60\n" \
"v_mfma_f32_16x16x32_bf16 a[112:115], a[220:223], a[224:227], a[112:115]\n" \
"s_or_b32 s5, s5, s38\n" \
"v_bitop3_b32 v212, v212, s35, v214 bitop3:0x36\n" \
"v_mfma_f32_16x16x32_bf16 a[116:119], a[156:159], a[164:167], 0\n" \
"s_lshr_b32 s36, s2, 3\n" \
"v_add_u32_e64 v212, s34, v212\n" \
"v_mfma_f32_16x16x32_bf16 a[116:119], a[220:223], a[228:231], a[116:119]\n" \
"s_mul_i32 s34, s5, 0x12000\n" \
"s_or_b32 s40, s34, 0x6000000\n" \
"v_mfma_f32_16x16x32_bf16 a[120:123], a[156:159], a[168:171], 0\n" \
"s_mul_i32 s34, s36, 0x90000\n" \
"s_lshl_b32 s36, s3, 14\n" \
"v_mfma_f32_16x16x32_bf16 a[120:123], a[220:223], a[232:235], a[120:123]\n" \
"s_add_i32 s34, s34, s36\n" \
"s_lshl_b32 s38, s5, 8\n" \
"v_mfma_f32_16x16x32_bf16 a[124:127], a[156:159], a[172:175], 0\n" \
"s_add_i32 s41, s34, 0x4240000\n" \
"s_mul_i32 s34, s5, 36\n" \
"v_mfma_f32_16x16x32_bf16 a[124:127], a[220:223], a[236:239], a[124:127]\n" \
"s_waitcnt vmcnt(4) lgkmcnt(0)\n" \
"s_barrier\n" \
"v_mfma_f32_16x16x32_bf16 v[52:55], a[128:131], a[176:179], 0\n" \
"s_add_u32 m0, s30, 0x0\n" \
"s_movk_i32 s84, 0x100\n" \
"v_mfma_f32_16x16x32_bf16 v[52:55], a[192:195], a[240:243], v[52:55]\n" \
"buffer_load_dwordx4 v22, s[64:67], s84 offen offset:0 lds\n" \
"v_mfma_f32_16x16x32_bf16 v[56:59], a[128:131], a[180:183], 0\n" \
"ds_read_b64_tr_b16 a[160:161], v32 offset:32768\n" \
"s_add_i32 s5, s7, s40\n" \
"v_mfma_f32_16x16x32_bf16 v[56:59], a[192:195], a[244:247], v[56:59]\n" \
"s_movk_i32 s84, 0x1d00\n" \
"v_mfma_f32_16x16x32_bf16 v[60:63], a[128:131], a[184:187], 0\n" \
"buffer_load_dwordx4 v23, s[64:67], s84 offen offset:1024 lds\n" \
"v_mfma_f32_16x16x32_bf16 v[60:63], a[192:195], a[248:251], v[60:63]\n" \
"ds_read_b64_tr_b16 a[162:163], v32 offset:33792\n" \
"s_addk_i32 s5, 0x2000\n" \
"v_mfma_f32_16x16x32_bf16 v[64:67], a[128:131], a[188:191], 0\n" \
"ds_read_b64_tr_b16 a[224:225], v32 offset:40960\n" \
"s_mov_b32 s37, 2.0\n" \
"v_mfma_f32_16x16x32_bf16 v[64:67], a[192:195], a[252:255], v[64:67]\n" \
"ds_read_b128 a[128:131], v28 offset:49152\n" \
"s_or_b32 s38, s38, 0x5be8000\n" \
"v_mfma_f32_16x16x32_bf16 v[80:83], a[132:135], a[188:191], 0\n" \
"ds_read_b128 a[192:195], v29 offset:49152\n" \
"v_mfma_f32_16x16x32_bf16 v[80:83], a[196:199], a[252:255], v[80:83]\n" \
"s_movk_i32 s84, 0x3900\n" \
"v_mfma_f32_16x16x32_bf16 v[76:79], a[132:135], a[184:187], 0\n" \
"buffer_load_dwordx4 v22, s[64:67], s84 offen offset:2048 lds\n" \
"v_mfma_f32_16x16x32_bf16 v[76:79], a[196:199], a[248:251], v[76:79]\n" \
"s_movk_i32 s84, 0x5500\n" \
"v_mfma_f32_16x16x32_bf16 v[72:75], a[132:135], a[180:183], 0\n" \
"buffer_load_dwordx4 v23, s[64:67], s84 offen offset:3072 lds\n" \
"v_mfma_f32_16x16x32_bf16 v[72:75], a[196:199], a[244:247], v[72:75]\n" \
"s_add_u32 m0, s31, 0x0\n" \
"s_mov_b32 s84, 0x100000\n" \
"v_mfma_f32_16x16x32_bf16 v[68:71], a[132:135], a[176:179], 0\n" \
"buffer_load_dwordx4 v26, s[68:71], s84 offen offset:0 lds\n" \
"v_mfma_f32_16x16x32_bf16 v[68:71], a[196:199], a[240:243], v[68:71]\n" \
"ds_read_b128 a[132:135], v28 offset:51200\n" \
"v_mfma_f32_16x16x32_bf16 v[84:87], a[136:139], a[176:179], 0\n" \
"ds_read_b128 a[196:199], v29 offset:51200\n" \
"v_mfma_f32_16x16x32_bf16 v[84:87], a[200:203], a[240:243], v[84:87]\n" \
"s_addk_i32 s84, 0x7c00\n" \
"v_mfma_f32_16x16x32_bf16 v[88:91], a[136:139], a[180:183], 0\n" \
"buffer_load_dwordx4 v26, s[68:71], s84 offen offset:1024 lds\n" \
"v_mfma_f32_16x16x32_bf16 v[88:91], a[200:203], a[244:247], v[88:91]\n" \
"ds_read_b64_tr_b16 a[226:227], v32 offset:41984\n" \
"v_mfma_f32_16x16x32_bf16 v[92:95], a[136:139], a[184:187], 0\n" \
"s_addk_i32 s84, 0x7c00\n" \
"v_mfma_f32_16x16x32_bf16 v[92:95], a[200:203], a[248:251], v[92:95]\n" \
"buffer_load_dwordx4 v27, s[68:71], s84 offen offset:2048 lds\n" \
"v_mfma_f32_16x16x32_bf16 v[96:99], a[136:139], a[188:191], 0\n" \
"ds_read_b64_tr_b16 a[164:165], v33 offset:32768\n" \
"v_mfma_f32_16x16x32_bf16 v[96:99], a[200:203], a[252:255], v[96:99]\n" \
"ds_read_b128 a[136:139], v28 offset:53248\n" \
"v_mfma_f32_16x16x32_bf16 v[112:115], a[140:143], a[188:191], 0\n" \
"ds_read_b128 a[200:203], v29 offset:53248\n" \
"v_mfma_f32_16x16x32_bf16 v[112:115], a[204:207], a[252:255], v[112:115]\n" \
"s_addk_i32 s84, 0x7c00\n" \
"v_mfma_f32_16x16x32_bf16 v[108:111], a[140:143], a[184:187], 0\n" \
"buffer_load_dwordx4 v27, s[68:71], s84 offen offset:3072 lds\n" \
"v_mfma_f32_16x16x32_bf16 v[108:111], a[204:207], a[248:251], v[108:111]\n" \
"s_add_u32 m0, s30, 0x1000\n" \
"s_mov_b32 s84, 0x8100\n" \
"v_mfma_f32_16x16x32_bf16 v[104:107], a[140:143], a[180:183], 0\n" \
"buffer_load_dwordx4 v22, s[64:67], s84 offen offset:0 lds\n" \
"v_mfma_f32_16x16x32_bf16 v[104:107], a[204:207], a[244:247], v[104:107]\n" \
"ds_read_b64_tr_b16 a[166:167], v33 offset:33792\n" \
"v_mfma_f32_16x16x32_bf16 v[100:103], a[140:143], a[176:179], 0\n" \
"ds_read_b64_tr_b16 a[228:229], v33 offset:40960\n" \
"v_mfma_f32_16x16x32_bf16 v[100:103], a[204:207], a[240:243], v[100:103]\n" \
"ds_read_b128 a[140:143], v28 offset:55296\n" \
"v_mfma_f32_16x16x32_bf16 v[116:119], a[144:147], a[176:179], 0\n" \
"ds_read_b128 a[204:207], v29 offset:55296\n" \
"v_mfma_f32_16x16x32_bf16 v[116:119], a[208:211], a[240:243], v[116:119]\n" \
"s_addk_i32 s84, 0x1c00\n" \
"v_mfma_f32_16x16x32_bf16 v[120:123], a[144:147], a[180:183], 0\n" \
"buffer_load_dwordx4 v23, s[64:67], s84 offen offset:1024 lds\n" \
"v_mfma_f32_16x16x32_bf16 v[120:123], a[208:211], a[244:247], v[120:123]\n" \
"s_addk_i32 s84, 0x1c00\n" \
"v_mfma_f32_16x16x32_bf16 v[124:127], a[144:147], a[184:187], 0\n" \
"buffer_load_dwordx4 v22, s[64:67], s84 offen offset:2048 lds\n" \
"v_mfma_f32_16x16x32_bf16 v[124:127], a[208:211], a[248:251], v[124:127]\n" \
"s_addk_i32 s84, 0x1c00\n" \
"s_nop 0\n" \
"v_mfma_f32_16x16x32_bf16 v[128:131], a[144:147], a[188:191], 0\n" \
"buffer_load_dwordx4 v23, s[64:67], s84 offen offset:3072 lds\n" \
"v_mfma_f32_16x16x32_bf16 v[128:131], a[208:211], a[252:255], v[128:131]\n" \
"ds_read_b128 a[144:147], v28 offset:32768\n" \
"v_mfma_f32_16x16x32_bf16 v[144:147], a[148:151], a[188:191], 0\n" \
"ds_read_b128 a[208:211], v29 offset:32768\n" \
"v_mfma_f32_16x16x32_bf16 v[144:147], a[212:215], a[252:255], v[144:147]\n" \
"s_add_u32 m0, s31, 0x4000\n" \
"s_mov_b32 s84, 0x100000\n" \
"v_mfma_f32_16x16x32_bf16 v[140:143], a[148:151], a[184:187], 0\n" \
"buffer_load_dwordx4 v26, s[72:75], s84 offen offset:0 lds\n" \
"v_mfma_f32_16x16x32_bf16 v[140:143], a[212:215], a[248:251], v[140:143]\n" \
"ds_read_b64_tr_b16 a[230:231], v33 offset:41984\n" \
"v_mfma_f32_16x16x32_bf16 v[136:139], a[148:151], a[180:183], 0\n" \
"s_addk_i32 s84, 0x7c00\n" \
"s_nop 0\n" \
"v_mfma_f32_16x16x32_bf16 v[136:139], a[212:215], a[244:247], v[136:139]\n" \
"buffer_load_dwordx4 v26, s[72:75], s84 offen offset:1024 lds\n" \
"v_mfma_f32_16x16x32_bf16 v[132:135], a[148:151], a[176:179], 0\n" \
"ds_read_b64_tr_b16 a[168:169], v34 offset:32768\n" \
"v_mfma_f32_16x16x32_bf16 v[132:135], a[212:215], a[240:243], v[132:135]\n" \
"ds_read_b128 a[148:151], v28 offset:34816\n" \
".fill (L_pb + 44768 - .) / 4, 4, 0xBF800000\n" \
"L_pp11:\n" \
"s_cbranch_execz L_pret\n" \
"s_nop 0\n" \
"v_mfma_f32_16x16x32_bf16 v[148:151], a[152:155], a[176:179], 0\n" \
"ds_read_b128 a[212:215], v29 offset:34816\n" \
"v_mfma_f32_16x16x32_bf16 v[148:151], a[216:219], a[240:243], v[148:151]\n" \
"ds_read_b64_tr_b16 a[170:171], v34 offset:33792\n" \
"v_mfma_f32_16x16x32_bf16 v[152:155], a[152:155], a[180:183], 0\n" \
"ds_read_b64_tr_b16 a[232:233], v34 offset:40960\n" \
"v_mfma_f32_16x16x32_bf16 v[152:155], a[216:219], a[244:247], v[152:155]\n" \
"ds_read_b64_tr_b16 a[234:235], v34 offset:41984\n" \
"v_mfma_f32_16x16x32_bf16 v[156:159], a[152:155], a[184:187], 0\n" \
"ds_read_b64_tr_b16 a[172:173], v35 offset:32768\n" \
"v_mfma_f32_16x16x32_bf16 v[156:159], a[216:219], a[248:251], v[156:159]\n" \
"ds_read_b64_tr_b16 a[174:175], v35 offset:33792\n" \
"v_mfma_f32_16x16x32_bf16 v[160:163], a[152:155], a[188:191], 0\n" \
"ds_read_b64_tr_b16 a[236:237], v35 offset:40960\n" \
"v_mfma_f32_16x16x32_bf16 v[160:163], a[216:219], a[252:255], v[160:163]\n" \
"ds_read_b128 a[152:155], v28 offset:36864\n" \
"v_mfma_f32_16x16x32_bf16 v[176:179], a[156:159], a[188:191], 0\n" \
"ds_read_b128 a[216:219], v29 offset:36864\n" \
"v_mfma_f32_16x16x32_bf16 v[176:179], a[220:223], a[252:255], v[176:179]\n" \
"ds_read_b64_tr_b16 a[238:239], v35 offset:41984\n" \
"v_mfma_f32_16x16x32_bf16 v[172:175], a[156:159], a[184:187], 0\n" \
"s_cmp_lg_u32 s3, 0\n" \
"s_cselect_b32 s36, s37, s5\n" \
"v_mfma_f32_16x16x32_bf16 v[172:175], a[220:223], a[248:251], v[172:175]\n" \
"s_lshl_b32 s5, s6, 2\n" \
"s_or_b32 s5, s38, s5\n" \
"v_mfma_f32_16x16x32_bf16 v[168:171], a[156:159], a[180:183], 0\n" \
"s_lshl_b32 s35, s3, 10\n" \
"v_mfma_f32_16x16x32_bf16 v[168:171], a[220:223], a[244:247], v[168:171]\n" \
"s_cmpk_lt_u32 s3, 3\n" \
"s_cselect_b32 s37, s37, s5\n" \
"v_mfma_f32_16x16x32_bf16 v[164:167], a[156:159], a[176:179], 0\n" \
"s_lshl_b32 s5, s4, 16\n" \
"s_lshl_b32 s4, s4, 11\n" \
"v_mfma_f32_16x16x32_bf16 v[164:167], a[220:223], a[240:243], v[164:167]\n" \
"ds_read_b128 a[156:159], v28 offset:38912\n" \
"ds_read_b128 a[220:223], v29 offset:38912\n" \
"s_waitcnt lgkmcnt(15)\n" \
"v_mfma_f32_16x16x32_bf16 a[0:3], a[128:131], a[160:163], a[0:3]\n" \
"s_addk_i32 s84, 0x7c00\n" \
"v_mfma_f32_16x16x32_bf16 a[0:3], a[192:195], a[224:227], a[0:3]\n" \
"buffer_load_dwordx4 v27, s[72:75], s84 offen offset:2048 lds\n" \
"v_mfma_f32_16x16x32_bf16 a[4:7], a[128:131], a[164:167], a[4:7]\n" \
"ds_read_b64_tr_b16 a[176:177], v36 offset:32768\n" \
"v_cndmask_b32_e32 v198, v213, v212, vcc\n" \
"s_waitcnt lgkmcnt(15)\n" \
"v_mfma_f32_16x16x32_bf16 a[4:7], a[192:195], a[228:231], a[4:7]\n" \
"s_addk_i32 s84, 0x7c00\n" \
"s_waitcnt lgkmcnt(11)\n" \
"v_mfma_f32_16x16x32_bf16 a[8:11], a[128:131], a[168:171], a[8:11]\n" \
"buffer_load_dwordx4 v27, s[72:75], s84 offen offset:3072 lds\n" \
"s_waitcnt lgkmcnt(9)\n" \
"v_mfma_f32_16x16x32_bf16 a[8:11], a[192:195], a[232:235], a[8:11]\n" \
"ds_read_b64_tr_b16 a[178:179], v36 offset:33792\n" \
"v_or_b32_e32 v212, s35, v20\n" \
"s_waitcnt lgkmcnt(8)\n" \
"v_mfma_f32_16x16x32_bf16 a[12:15], a[128:131], a[172:175], a[12:15]\n" \
"ds_read_b64_tr_b16 a[240:241], v36 offset:40960\n" \
"s_or_b32 s39, s40, s35\n" \
"s_waitcnt lgkmcnt(5)\n" \
"v_mfma_f32_16x16x32_bf16 a[12:15], a[192:195], a[236:239], a[12:15]\n" \
"s_or_b32 s55, s41, s4\n" \
"v_or_b32_e32 v199, 0x22000, v212\n" \
"v_mfma_f32_16x16x32_bf16 a[28:31], a[132:135], a[172:175], a[28:31]\n" \
"s_add_i32 s35, s39, s7\n" \
"s_add_i32 s54, s41, s5\n" \
"v_mfma_f32_16x16x32_bf16 a[28:31], a[196:199], a[236:239], a[28:31]\n" \
"ds_read_b64_tr_b16 a[242:243], v36 offset:41984\n" \
"s_add_i32 s55, s55, 0x80000\n" \
"v_mfma_f32_16x16x32_bf16 a[24:27], a[132:135], a[168:171], a[24:27]\n" \
"v_mov_b32_e32 v215, 0xffff\n" \
"s_and_b32 s4, s3, 1\n" \
"v_mfma_f32_16x16x32_bf16 a[24:27], a[196:199], a[232:235], a[24:27]\n" \
"s_lshl_b32 s5, s4, 4\n" \
"v_lshrrev_b32_e32 v212, 4, v20\n" \
"v_mfma_f32_16x16x32_bf16 a[20:23], a[132:135], a[164:167], a[20:23]\n" \
"v_lshrrev_b32_e32 v218, 6, v20\n" \
"v_and_b32_e32 v219, 12, v218\n" \
"v_mfma_f32_16x16x32_bf16 a[20:23], a[196:199], a[228:231], a[20:23]\n" \
"ds_read_b64_tr_b16 a[180:181], v37 offset:32768\n" \
"v_and_or_b32 v214, v212, 15, s5\n" \
"v_mfma_f32_16x16x32_bf16 a[16:19], a[132:135], a[160:163], a[16:19]\n" \
"v_cmp_gt_u32_e64 vcc, v219, v214\n" \
"v_mov_b32_e32 v220, 0xffff0000\n" \
"v_mfma_f32_16x16x32_bf16 a[16:19], a[196:199], a[224:227], a[16:19]\n" \
"s_lshl_b32 s5, s2, 2\n" \
"v_cndmask_b32_e64 v217, v215, 0, vcc\n" \
"v_mfma_f32_16x16x32_bf16 a[32:35], a[136:139], a[160:163], a[32:35]\n" \
"s_mov_b32 m0, s32\n" \
"ds_read_b64_tr_b16 a[182:183], v37 offset:33792\n" \
"v_mfma_f32_16x16x32_bf16 a[32:35], a[200:203], a[224:227], a[32:35]\n" \
"s_mov_b32 exec_lo, 0x0\n" \
"s_mov_b32 exec_hi, 0xff\n" \
"global_load_lds_dword v[184:185], off\n" \
"s_mov_b64 exec, -1\n" \
"v_mfma_f32_16x16x32_bf16 a[36:39], a[136:139], a[164:167], a[36:39]\n" \
"v_cmp_lt_u32_e32 vcc, v219, v214\n" \
"v_mov_b32_e32 v213, s23\n" \
"v_mfma_f32_16x16x32_bf16 a[36:39], a[200:203], a[228:231], a[36:39]\n" \
"ds_read_b64_tr_b16 a[244:245], v37 offset:40960\n" \
"s_or_b32 s5, s5, s3\n" \
"v_mfma_f32_16x16x32_bf16 a[40:43], a[136:139], a[168:171], a[40:43]\n" \
"v_cndmask_b32_e32 v221, 0, v220, vcc\n" \
"v_or_b32_e64 v12, v217, v221\n" \
"v_mfma_f32_16x16x32_bf16 a[40:43], a[200:203], a[232:235], a[40:43]\n" \
"s_mov_b32 m0, s32\n" \
"v_or_b32_e32 v217, 2, v219\n" \
"v_mfma_f32_16x16x32_bf16 a[44:47], a[136:139], a[172:175], a[44:47]\n" \
"s_mov_b32 exec_lo, 0xffff0000\n" \
"s_mov_b32 exec_hi, 0x0\n" \
"global_load_lds_dword v[186:187], off\n" \
"s_mov_b64 exec, -1\n" \
"v_mfma_f32_16x16x32_bf16 a[44:47], a[200:203], a[236:239], a[44:47]\n" \
"ds_read_b64_tr_b16 a[246:247], v37 offset:41984\n" \
"v_cmp_gt_u32_e64 vcc, v217, v214\n" \
"v_mfma_f32_16x16x32_bf16 a[60:63], a[140:143], a[172:175], a[60:63]\n" \
"v_mov_b32_e32 v216, s25\n" \
"s_movk_i32 s40, 0xf400\n" \
"v_mfma_f32_16x16x32_bf16 a[60:63], a[204:207], a[236:239], a[60:63]\n" \
"v_cndmask_b32_e64 v221, v215, 0, vcc\n" \
"v_cmp_lt_u32_e64 vcc, v217, v214\n" \
"v_mfma_f32_16x16x32_bf16 a[56:59], a[140:143], a[168:171], a[56:59]\n" \
"ds_read_b64_tr_b16 a[184:185], v38 offset:32768\n" \
"s_lshl_b32 s4, s4, 6\n" \
"v_mfma_f32_16x16x32_bf16 a[56:59], a[204:207], a[232:235], a[56:59]\n" \
"v_cndmask_b32_e32 v217, 0, v220, vcc\n" \
"v_or_b32_e64 v13, v221, v217\n" \
"v_mfma_f32_16x16x32_bf16 a[52:55], a[140:143], a[164:167], a[52:55]\n" \
"v_or_b32_e32 v217, 16, v219\n" \
"v_cmp_gt_u32_e32 vcc, v217, v214\n" \
"v_mfma_f32_16x16x32_bf16 a[52:55], a[204:207], a[228:231], a[52:55]\n" \
"ds_read_b64_tr_b16 a[186:187], v38 offset:33792\n" \
"v_cndmask_b32_e64 v221, v215, 0, vcc\n" \
"v_mfma_f32_16x16x32_bf16 a[48:51], a[140:143], a[160:163], a[48:51]\n" \
"v_cmp_lt_u32_e32 vcc, v217, v214\n" \
"s_nop 1\n" \
"v_cndmask_b32_e64 v217, 0, v220, vcc\n" \
"v_mfma_f32_16x16x32_bf16 a[48:51], a[204:207], a[224:227], a[48:51]\n" \
"v_or_b32_e32 v14, v221, v217\n" \
"v_or_b32_e32 v217, 18, v219\n" \
"v_mfma_f32_16x16x32_bf16 a[64:67], a[144:147], a[160:163], a[64:67]\n" \
"ds_read_b64_tr_b16 a[248:249], v38 offset:40960\n" \
"v_cmp_gt_u32_e64 vcc, v217, v214\n" \
"v_mfma_f32_16x16x32_bf16 a[64:67], a[208:211], a[224:227], a[64:67]\n" \
"s_nop 0\n" \
"v_cndmask_b32_e64 v215, v215, 0, vcc\n" \
"v_cmp_lt_u32_e32 vcc, v217, v214\n" \
"v_mfma_f32_16x16x32_bf16 a[68:71], a[144:147], a[164:167], a[68:71]\n" \
"s_nop 0\n" \
"v_cndmask_b32_e32 v214, 0, v220, vcc\n" \
"v_or_b32_e64 v15, v215, v214\n" \
"v_mfma_f32_16x16x32_bf16 a[68:71], a[208:211], a[228:231], a[68:71]\n" \
"ds_read_b64_tr_b16 a[250:251], v38 offset:41984\n" \
"v_mad_u64_u32 v[214:215], s[6:7], s5, 42, v[212:213]\n" \
"v_mfma_f32_16x16x32_bf16 a[72:75], a[144:147], a[168:171], a[72:75]\n" \
"s_movk_i32 s5, 0x1440\n" \
"v_cmp_gt_u32_e32 vcc, s5, v214\n" \
"v_mfma_f32_16x16x32_bf16 a[72:75], a[208:211], a[232:235], a[72:75]\n" \
"v_min_u32_e32 v217, 0xa43f, v214\n" \
"v_mov_b32_e64 v214, s24\n" \
"v_mfma_f32_16x16x32_bf16 a[76:79], a[144:147], a[172:175], a[76:79]\n" \
"v_cndmask_b32_e32 v215, v216, v213, vcc\n" \
"v_mov_b32_e32 v213, s22\n" \
"v_mfma_f32_16x16x32_bf16 a[76:79], a[208:211], a[236:239], a[76:79]\n" \
"ds_read_b64_tr_b16 a[188:189], v39 offset:32768\n" \
"s_movk_i32 s6, 0xffee\n" \
"v_mfma_f32_16x16x32_bf16 a[92:95], a[148:151], a[172:175], a[92:95]\n" \
"v_cndmask_b32_e32 v214, v214, v213, vcc\n" \
"v_add_u32_e32 v213, 0xffffebc0, v217\n" \
"v_mfma_f32_16x16x32_bf16 a[92:95], a[212:215], a[236:239], a[92:95]\n" \
"s_lshl_b32 s5, s2, 5\n" \
"v_cndmask_b32_e32 v213, v213, v217, vcc\n" \
"v_mfma_f32_16x16x32_bf16 a[88:91], a[148:151], a[168:171], a[88:91]\n" \
"ds_read_b64_tr_b16 a[190:191], v39 offset:33792\n" \
"v_lshlrev_b32_e64 v216, 7, v213\n" \
"v_mfma_f32_16x16x32_bf16 a[88:91], a[212:215], a[232:235], a[88:91]\n" \
"v_mul_u32_u24_e32 v213, 0xe39, v212\n" \
"v_mov_b32_e64 v217, 0\n" \
"v_mfma_f32_16x16x32_bf16 a[84:87], a[148:151], a[164:167], a[84:87]\n" \
"v_lshrrev_b32_e64 v213, 16, v213\n" \
"v_lshl_add_u64 v[16:17], v[214:215], 0, v[216:217]\n" \
"v_mfma_f32_16x16x32_bf16 a[84:87], a[212:215], a[228:231], a[84:87]\n" \
"ds_read_b64_tr_b16 a[252:253], v39 offset:40960\n" \
"v_mad_i32_i24 v214, v213, s6, v212\n" \
"v_mfma_f32_16x16x32_bf16 a[80:83], a[148:151], a[160:163], a[80:83]\n" \
"s_add_i32 s5, s5, 32\n" \
"v_lshlrev_b32_e32 v215, 4, v214\n" \
"v_mfma_f32_16x16x32_bf16 a[80:83], a[212:215], a[224:227], a[80:83]\n" \
"v_add_u32_e64 v216, s5, v215\n" \
"v_and_b32_e32 v216, 0xf0, v216\n" \
"v_mfma_f32_16x16x32_bf16 a[96:99], a[152:155], a[160:163], a[96:99]\n" \
"ds_read_b64_tr_b16 a[254:255], v39 offset:41984\n" \
"v_cmp_gt_u32_e64 vcc, 16, v214\n" \
"v_mfma_f32_16x16x32_bf16 a[96:99], a[216:219], a[224:227], a[96:99]\n" \
"s_movk_i32 s7, 0xf800\n" \
"v_cndmask_b32_e32 v214, v215, v216, vcc\n" \
"v_mfma_f32_16x16x32_bf16 a[100:103], a[152:155], a[164:167], a[100:103]\n" \
"v_or_b32_e64 v215, 64, v212\n" \
"v_mul_u32_u24_e32 v216, 0xe39, v215\n" \
"v_mfma_f32_16x16x32_bf16 a[100:103], a[216:219], a[228:231], a[100:103]\n" \
"v_lshrrev_b32_e64 v216, 16, v216\n" \
"v_mad_i32_i24 v215, v216, s6, v215\n" \
"v_mfma_f32_16x16x32_bf16 a[104:107], a[152:155], a[168:171], a[104:107]\n" \
"v_lshlrev_b32_e32 v217, 4, v215\n" \
"v_add_u32_e32 v220, s5, v217\n" \
"v_mfma_f32_16x16x32_bf16 a[104:107], a[216:219], a[232:235], a[104:107]\n" \
"v_and_b32_e32 v220, 0xf0, v220\n" \
"v_cmp_gt_u32_e64 vcc, 16, v215\n" \
"v_mfma_f32_16x16x32_bf16 a[108:111], a[152:155], a[172:175], a[108:111]\n" \
"s_nop 0\n" \
"v_cndmask_b32_e32 v215, v217, v220, vcc\n" \
"v_or_b32_e32 v217, 0x80, v212\n" \
"v_mfma_f32_16x16x32_bf16 a[108:111], a[216:219], a[236:239], a[108:111]\n" \
"v_mul_u32_u24_e32 v220, 0xe39, v217\n" \
"v_lshrrev_b32_e32 v220, 16, v220\n" \
"s_waitcnt lgkmcnt(15)\n" \
"v_mfma_f32_16x16x32_bf16 a[124:127], a[156:159], a[172:175], a[124:127]\n" \
"v_mad_i32_i24 v217, v220, s6, v217\n" \
"v_lshlrev_b32_e64 v221, 4, v217\n" \
"v_mfma_f32_16x16x32_bf16 a[124:127], a[220:223], a[236:239], a[124:127]\n" \
"v_add_u32_e64 v222, s5, v221\n" \
"v_and_b32_e32 v222, 0xf0, v222\n" \
"v_mfma_f32_16x16x32_bf16 a[120:123], a[156:159], a[168:171], a[120:123]\n" \
"v_cmp_gt_u32_e32 vcc, 16, v217\n" \
"s_nop 1\n" \
"v_cndmask_b32_e64 v217, v221, v222, vcc\n" \
"v_mfma_f32_16x16x32_bf16 a[120:123], a[220:223], a[232:235], a[120:123]\n" \
"v_or_b32_e32 v221, 0xc0, v212\n" \
"v_mul_u32_u24_e32 v222, 0xe39, v221\n" \
"v_mfma_f32_16x16x32_bf16 a[116:119], a[156:159], a[164:167], a[116:119]\n" \
"v_lshrrev_b32_e64 v222, 16, v222\n" \
"v_mad_i32_i24 v221, v222, s6, v221\n" \
"v_mfma_f32_16x16x32_bf16 a[116:119], a[220:223], a[228:231], a[116:119]\n" \
"v_lshlrev_b32_e32 v223, 4, v221\n" \
"v_add_u32_e32 v224, s5, v223\n" \
"v_mfma_f32_16x16x32_bf16 a[112:115], a[156:159], a[160:163], a[112:115]\n" \
"v_and_b32_e32 v224, 0xf0, v224\n" \
"v_cmp_gt_u32_e64 vcc, 16, v221\n" \
"v_mfma_f32_16x16x32_bf16 a[112:115], a[220:223], a[224:227], a[112:115]\n" \
"s_waitcnt vmcnt(20)\n" \
"v_readfirstlane_b32 s62, v180\n" \
"s_nop 3\n" \
"s_add_u32 s62, s62, 0x9E3779B1\n" \
"s_waitcnt vmcnt(2) lgkmcnt(0)\n" \
"s_barrier\n" \
"v_mfma_f32_16x16x32_bf16 v[52:55], a[128:131], a[176:179], v[52:55]\n" \
"s_add_u32 m0, s30, 0x8000\n" \
"s_movk_i32 s84, 0x180\n" \
"v_mfma_f32_16x16x32_bf16 v[52:55], a[192:195], a[240:243], v[52:55]\n" \
"buffer_load_dwordx4 v22, s[64:67], s84 offen offset:0 lds\n" \
"v_mfma_f32_16x16x32_bf16 v[56:59], a[128:131], a[180:183], v[56:59]\n" \
"ds_read_b64_tr_b16 a[160:161], v32 offset:0\n" \
"v_cndmask_b32_e64 v221, v223, v224, vcc\n" \
"v_mfma_f32_16x16x32_bf16 v[56:59], a[192:195], a[244:247], v[56:59]\n" \
"s_movk_i32 s84, 0x1d80\n" \
"v_mfma_f32_16x16x32_bf16 v[60:63], a[128:131], a[184:187], v[60:63]\n" \
"buffer_load_dwordx4 v23, s[64:67], s84 offen offset:1024 lds\n" \
"v_mfma_f32_16x16x32_bf16 v[60:63], a[192:195], a[248:251], v[60:63]\n" \
"ds_read_b64_tr_b16 a[162:163], v32 offset:1024\n" \
"v_or_b32_e32 v223, 0x100, v212\n" \
"v_mfma_f32_16x16x32_bf16 v[64:67], a[128:131], a[188:191], v[64:67]\n" \
"ds_read_b64_tr_b16 a[224:225], v32 offset:8192\n" \
"v_mul_u32_u24_e32 v224, 0xe39, v223\n" \
"v_mfma_f32_16x16x32_bf16 v[64:67], a[192:195], a[252:255], v[64:67]\n" \
"ds_read_b128 a[128:131], v28 offset:16384\n" \
"v_lshrrev_b32_e32 v224, 16, v224\n" \
"v_mfma_f32_16x16x32_bf16 v[80:83], a[132:135], a[188:191], v[80:83]\n" \
"ds_read_b128 a[192:195], v29 offset:16384\n" \
"v_mad_i32_i24 v223, v224, s6, v223\n" \
"v_mfma_f32_16x16x32_bf16 v[80:83], a[196:199], a[252:255], v[80:83]\n" \
"s_movk_i32 s84, 0x3980\n" \
"v_mfma_f32_16x16x32_bf16 v[76:79], a[132:135], a[184:187], v[76:79]\n" \
"buffer_load_dwordx4 v22, s[64:67], s84 offen offset:2048 lds\n" \
"v_mfma_f32_16x16x32_bf16 v[76:79], a[196:199], a[248:251], v[76:79]\n" \
"s_movk_i32 s84, 0x5580\n" \
"v_mfma_f32_16x16x32_bf16 v[72:75], a[132:135], a[180:183], v[72:75]\n" \
"buffer_load_dwordx4 v23, s[64:67], s84 offen offset:3072 lds\n" \
"v_mfma_f32_16x16x32_bf16 v[72:75], a[196:199], a[244:247], v[72:75]\n" \
"s_add_u32 m0, s31, 0x8000\n" \
"s_mov_b32 s84, 0x180000\n" \
"v_mfma_f32_16x16x32_bf16 v[68:71], a[132:135], a[176:179], v[68:71]\n" \
"buffer_load_dwordx4 v26, s[68:71], s84 offen offset:0 lds\n" \
"v_mfma_f32_16x16x32_bf16 v[68:71], a[196:199], a[240:243], v[68:71]\n" \
"ds_read_b128 a[132:135], v28 offset:18432\n" \
"v_lshlrev_b32_e64 v225, 4, v223\n" \
"v_mfma_f32_16x16x32_bf16 v[84:87], a[136:139], a[176:179], v[84:87]\n" \
"ds_read_b128 a[196:199], v29 offset:18432\n" \
"v_add_u32_e64 v226, s5, v225\n" \
"v_mfma_f32_16x16x32_bf16 v[84:87], a[200:203], a[240:243], v[84:87]\n" \
"s_addk_i32 s84, 0x7c00\n" \
"v_mfma_f32_16x16x32_bf16 v[88:91], a[136:139], a[180:183], v[88:91]\n" \
"buffer_load_dwordx4 v26, s[68:71], s84 offen offset:1024 lds\n" \
"v_mfma_f32_16x16x32_bf16 v[88:91], a[200:203], a[244:247], v[88:91]\n" \
"ds_read_b64_tr_b16 a[226:227], v32 offset:9216\n" \
"v_and_b32_e32 v226, 0xf0, v226\n" \
"v_mfma_f32_16x16x32_bf16 v[92:95], a[136:139], a[184:187], v[92:95]\n" \
"s_addk_i32 s84, 0x7c00\n" \
"v_mfma_f32_16x16x32_bf16 v[92:95], a[200:203], a[248:251], v[92:95]\n" \
"buffer_load_dwordx4 v27, s[68:71], s84 offen offset:2048 lds\n" \
"v_mfma_f32_16x16x32_bf16 v[96:99], a[136:139], a[188:191], v[96:99]\n" \
"ds_read_b64_tr_b16 a[164:165], v33 offset:0\n" \
"v_cmp_gt_u32_e64 vcc, 16, v223\n" \
"v_mfma_f32_16x16x32_bf16 v[96:99], a[200:203], a[252:255], v[96:99]\n" \
"ds_read_b128 a[136:139], v28 offset:20480\n" \
"v_cndmask_b32_e64 v223, v225, v226, vcc\n" \
"v_mfma_f32_16x16x32_bf16 v[112:115], a[140:143], a[188:191], v[112:115]\n" \
"ds_read_b128 a[200:203], v29 offset:20480\n" \
"v_or_b32_e32 v225, 0x140, v212\n" \
"v_mfma_f32_16x16x32_bf16 v[112:115], a[204:207], a[252:255], v[112:115]\n" \
"s_addk_i32 s84, 0x7c00\n" \
"s_nop 0\n" \
"v_mfma_f32_16x16x32_bf16 v[108:111], a[140:143], a[184:187], v[108:111]\n" \
"buffer_load_dwordx4 v27, s[68:71], s84 offen offset:3072 lds\n" \
"v_mfma_f32_16x16x32_bf16 v[108:111], a[204:207], a[248:251], v[108:111]\n" \
"s_add_u32 m0, s30, 0x9000\n" \
"s_mov_b32 s84, 0x8180\n" \
"v_mfma_f32_16x16x32_bf16 v[104:107], a[140:143], a[180:183], v[104:107]\n" \
"buffer_load_dwordx4 v22, s[64:67], s84 offen offset:0 lds\n" \
"v_mfma_f32_16x16x32_bf16 v[104:107], a[204:207], a[244:247], v[104:107]\n" \
"ds_read_b64_tr_b16 a[166:167], v33 offset:1024\n" \
"v_mul_u32_u24_e32 v226, 0xe39, v225\n" \
"v_mfma_f32_16x16x32_bf16 v[100:103], a[140:143], a[176:179], v[100:103]\n" \
"ds_read_b64_tr_b16 a[228:229], v33 offset:8192\n" \
"v_lshrrev_b32_e64 v226, 16, v226\n" \
"v_mfma_f32_16x16x32_bf16 v[100:103], a[204:207], a[240:243], v[100:103]\n" \
"ds_read_b128 a[140:143], v28 offset:22528\n" \
"v_mad_i32_i24 v225, v226, s6, v225\n" \
"v_mfma_f32_16x16x32_bf16 v[116:119], a[144:147], a[176:179], v[116:119]\n" \
"ds_read_b128 a[204:207], v29 offset:22528\n" \
"v_lshlrev_b32_e64 v227, 4, v225\n" \
"v_mfma_f32_16x16x32_bf16 v[116:119], a[208:211], a[240:243], v[116:119]\n" \
"s_addk_i32 s84, 0x1c00\n" \
"v_mfma_f32_16x16x32_bf16 v[120:123], a[144:147], a[180:183], v[120:123]\n" \
"buffer_load_dwordx4 v23, s[64:67], s84 offen offset:1024 lds\n" \
"v_mfma_f32_16x16x32_bf16 v[120:123], a[208:211], a[244:247], v[120:123]\n" \
"s_addk_i32 s84, 0x1c00\n" \
"v_mfma_f32_16x16x32_bf16 v[124:127], a[144:147], a[184:187], v[124:127]\n" \
"buffer_load_dwordx4 v22, s[64:67], s84 offen offset:2048 lds\n" \
"v_mfma_f32_16x16x32_bf16 v[124:127], a[208:211], a[248:251], v[124:127]\n" \
"s_addk_i32 s84, 0x1c00\n" \
"v_mfma_f32_16x16x32_bf16 v[128:131], a[144:147], a[188:191], v[128:131]\n" \
"buffer_load_dwordx4 v23, s[64:67], s84 offen offset:3072 lds\n" \
"v_mfma_f32_16x16x32_bf16 v[128:131], a[208:211], a[252:255], v[128:131]\n" \
"ds_read_b128 a[144:147], v28 offset:0\n" \
"v_add_u32_e32 v228, s5, v227\n" \
"v_mfma_f32_16x16x32_bf16 v[144:147], a[148:151], a[188:191], v[144:147]\n" \
"ds_read_b128 a[208:211], v29 offset:0\n" \
"v_and_b32_e32 v228, 0xf0, v228\n" \
"v_mfma_f32_16x16x32_bf16 v[144:147], a[212:215], a[252:255], v[144:147]\n" \
"s_add_u32 m0, s31, 0xc000\n" \
"s_mov_b32 s84, 0x180000\n" \
"v_mfma_f32_16x16x32_bf16 v[140:143], a[148:151], a[184:187], v[140:143]\n" \
"buffer_load_dwordx4 v26, s[72:75], s84 offen offset:0 lds\n" \
"v_mfma_f32_16x16x32_bf16 v[140:143], a[212:215], a[248:251], v[140:143]\n" \
"ds_read_b64_tr_b16 a[230:231], v33 offset:9216\n" \
"v_cmp_gt_u32_e64 vcc, 16, v225\n" \
"v_mfma_f32_16x16x32_bf16 v[136:139], a[148:151], a[180:183], v[136:139]\n" \
"s_addk_i32 s84, 0x7c00\n" \
"v_mfma_f32_16x16x32_bf16 v[136:139], a[212:215], a[244:247], v[136:139]\n" \
"buffer_load_dwordx4 v26, s[72:75], s84 offen offset:1024 lds\n" \
"v_mfma_f32_16x16x32_bf16 v[132:135], a[148:151], a[176:179], v[132:135]\n" \
"ds_read_b64_tr_b16 a[168:169], v34 offset:0\n" \
"v_cndmask_b32_e32 v225, v227, v228, vcc\n" \
"v_mfma_f32_16x16x32_bf16 v[132:135], a[212:215], a[240:243], v[132:135]\n" \
"ds_read_b128 a[148:151], v28 offset:2048\n" \
"v_or_b32_e32 v227, 0x180, v212\n" \
"v_mfma_f32_16x16x32_bf16 v[148:151], a[152:155], a[176:179], v[148:151]\n" \
"ds_read_b128 a[212:215], v29 offset:2048\n" \
"v_mul_u32_u24_e32 v228, 0xe39, v227\n" \
"v_mfma_f32_16x16x32_bf16 v[148:151], a[216:219], a[240:243], v[148:151]\n" \
"ds_read_b64_tr_b16 a[170:171], v34 offset:1024\n" \
"v_lshrrev_b32_e64 v228, 16, v228\n" \
"v_mfma_f32_16x16x32_bf16 v[152:155], a[152:155], a[180:183], v[152:155]\n" \
"ds_read_b64_tr_b16 a[232:233], v34 offset:8192\n" \
"v_mad_i32_i24 v227, v228, s6, v227\n" \
"v_mfma_f32_16x16x32_bf16 v[152:155], a[216:219], a[244:247], v[152:155]\n" \
"ds_read_b64_tr_b16 a[234:235], v34 offset:9216\n" \
"v_lshlrev_b32_e64 v229, 4, v227\n" \
"v_mfma_f32_16x16x32_bf16 v[156:159], a[152:155], a[184:187], v[156:159]\n" \
"ds_read_b64_tr_b16 a[172:173], v35 offset:0\n" \
"v_add_u32_e64 v230, s5, v229\n" \
"v_mfma_f32_16x16x32_bf16 v[156:159], a[216:219], a[248:251], v[156:159]\n" \
"ds_read_b64_tr_b16 a[174:175], v35 offset:1024\n" \
"v_and_b32_e32 v230, 0xf0, v230\n" \
"v_mfma_f32_16x16x32_bf16 v[160:163], a[152:155], a[188:191], v[160:163]\n" \
"ds_read_b64_tr_b16 a[236:237], v35 offset:8192\n" \
"v_cmp_gt_u32_e64 vcc, 16, v227\n" \
"v_mfma_f32_16x16x32_bf16 v[160:163], a[216:219], a[252:255], v[160:163]\n" \
"ds_read_b128 a[152:155], v28 offset:4096\n" \
"v_cndmask_b32_e64 v227, v229, v230, vcc\n" \
"v_mfma_f32_16x16x32_bf16 v[176:179], a[156:159], a[188:191], v[176:179]\n" \
"ds_read_b128 a[216:219], v29 offset:4096\n" \
"v_or_b32_e32 v229, 0x1c0, v212\n" \
"v_mfma_f32_16x16x32_bf16 v[176:179], a[220:223], a[252:255], v[176:179]\n" \
"ds_read_b64_tr_b16 a[238:239], v35 offset:9216\n" \
"v_mul_u32_u24_e32 v230, 0xe39, v229\n" \
"v_mfma_f32_16x16x32_bf16 v[172:175], a[156:159], a[184:187], v[172:175]\n" \
"v_lshrrev_b32_e64 v230, 16, v230\n" \
"v_mad_i32_i24 v229, v230, s6, v229\n" \
"v_mfma_f32_16x16x32_bf16 v[172:175], a[220:223], a[248:251], v[172:175]\n" \
"v_lshlrev_b32_e32 v231, 4, v229\n" \
"v_add_u32_e32 v232, s5, v231\n" \
"v_mfma_f32_16x16x32_bf16 v[168:171], a[156:159], a[180:183], v[168:171]\n" \
"v_and_b32_e32 v232, 0xf0, v232\n" \
"v_cmp_gt_u32_e64 vcc, 16, v229\n" \
"v_mfma_f32_16x16x32_bf16 v[168:171], a[220:223], a[244:247], v[168:171]\n" \
"s_nop 0\n" \
"v_cndmask_b32_e32 v229, v231, v232, vcc\n" \
"v_or_b32_e32 v231, 0x200, v212\n" \
"v_mfma_f32_16x16x32_bf16 v[164:167], a[156:159], a[176:179], v[164:167]\n" \
"v_mul_u32_u24_e32 v232, 0xe39, v231\n" \
"v_lshrrev_b32_e64 v232, 16, v232\n" \
"v_mfma_f32_16x16x32_bf16 v[164:167], a[220:223], a[240:243], v[164:167]\n" \
"ds_read_b128 a[156:159], v28 offset:6144\n" \
"ds_read_b128 a[220:223], v29 offset:6144\n" \
"s_waitcnt lgkmcnt(15)\n" \
"v_mfma_f32_16x16x32_bf16 a[0:3], a[128:131], a[160:163], a[0:3]\n" \
"s_addk_i32 s84, 0x7c00\n" \
"v_mfma_f32_16x16x32_bf16 a[0:3], a[192:195], a[224:227], a[0:3]\n" \
"buffer_load_dwordx4 v27, s[72:75], s84 offen offset:2048 lds\n" \
"v_mfma_f32_16x16x32_bf16 a[4:7], a[128:131], a[164:167], a[4:7]\n" \
"ds_read_b64_tr_b16 a[176:177], v36 offset:0\n" \
"v_mad_i32_i24 v231, v232, s6, v231\n" \
"s_waitcnt lgkmcnt(15)\n" \
"v_mfma_f32_16x16x32_bf16 a[4:7], a[192:195], a[228:231], a[4:7]\n" \
"s_addk_i32 s84, 0x7c00\n" \
"s_waitcnt lgkmcnt(11)\n" \
"v_mfma_f32_16x16x32_bf16 a[8:11], a[128:131], a[168:171], a[8:11]\n" \
"buffer_load_dwordx4 v27, s[72:75], s84 offen offset:3072 lds\n" \
"s_waitcnt lgkmcnt(9)\n" \
"v_mfma_f32_16x16x32_bf16 a[8:11], a[192:195], a[232:235], a[8:11]\n" \
"ds_read_b64_tr_b16 a[178:179], v36 offset:1024\n" \
"v_lshlrev_b32_e32 v233, 4, v231\n" \
"s_waitcnt lgkmcnt(8)\n" \
"v_mfma_f32_16x16x32_bf16 a[12:15], a[128:131], a[172:175], a[12:15]\n" \
"ds_read_b64_tr_b16 a[240:241], v36 offset:8192\n" \
"v_add_u32_e32 v234, s5, v233\n" \
"s_waitcnt lgkmcnt(5)\n" \
"v_mfma_f32_16x16x32_bf16 a[12:15], a[192:195], a[236:239], a[12:15]\n" \
"s_movk_i32 s5, 0x480\n" \
"v_mad_u32_u24 v0, v213, s5, v214\n" \
"v_mfma_f32_16x16x32_bf16 a[28:31], a[132:135], a[172:175], a[28:31]\n" \
"v_mul_u32_u24_e32 v213, 0x480, v216\n" \
"s_movk_i32 s6, 0xfc00\n" \
"v_mfma_f32_16x16x32_bf16 a[28:31], a[196:199], a[236:239], a[28:31]\n" \
"ds_read_b64_tr_b16 a[242:243], v36 offset:9216\n" \
"v_add3_u32 v1, v213, v215, s6\n" \
"v_mfma_f32_16x16x32_bf16 a[24:27], a[132:135], a[168:171], a[24:27]\n" \
"buffer_load_dwordx4 v[200:203], v192, s[76:79], s34 offen offset:0\n" \
"v_mfma_f32_16x16x32_bf16 a[24:27], a[196:199], a[232:235], a[24:27]\n" \
"v_mul_u32_u24_e32 v213, 0x480, v220\n" \
"v_add3_u32 v2, v213, v217, s7\n" \
"v_mfma_f32_16x16x32_bf16 a[20:23], a[132:135], a[164:167], a[20:23]\n" \
"v_mul_u32_u24_e32 v213, 0x480, v222\n" \
"v_add3_u32 v3, v213, v221, s40\n" \
"v_mfma_f32_16x16x32_bf16 a[20:23], a[196:199], a[228:231], a[20:23]\n" \
"ds_read_b64_tr_b16 a[180:181], v37 offset:0\n" \
"v_mul_u32_u24_e32 v213, 0x480, v226\n" \
"v_mfma_f32_16x16x32_bf16 a[16:19], a[132:135], a[160:163], a[16:19]\n" \
"v_add3_u32 v5, v213, v225, s6\n" \
"v_mul_u32_u24_e32 v213, 0x480, v228\n" \
"v_mfma_f32_16x16x32_bf16 a[16:19], a[196:199], a[224:227], a[16:19]\n" \
"v_add3_u32 v6, v213, v227, s7\n" \
"v_mul_u32_u24_e32 v213, 0x480, v230\n" \
"v_mfma_f32_16x16x32_bf16 a[32:35], a[136:139], a[160:163], a[32:35]\n" \
"buffer_load_dwordx4 v[204:207], v193, s[76:79], s34 offen offset:0\n" \
"v_mfma_f32_16x16x32_bf16 a[32:35], a[200:203], a[224:227], a[32:35]\n" \
"s_mov_b32 m0, s32\n" \
"ds_read_b64_tr_b16 a[182:183], v37 offset:1024\n" \
"v_mfma_f32_16x16x32_bf16 a[36:39], a[136:139], a[164:167], a[36:39]\n" \
"s_mov_b32 exec_lo, 0x0\n" \
"s_mov_b32 exec_hi, 0xff00\n" \
"global_load_lds_dword v[184:185], off\n" \
"s_mov_b64 exec, -1\n" \
"v_mfma_f32_16x16x32_bf16 a[36:39], a[200:203], a[228:231], a[36:39]\n" \
"ds_read_b64_tr_b16 a[244:245], v37 offset:8192\n" \
"v_add3_u32 v7, v213, v229, s40\n" \
"v_mfma_f32_16x16x32_bf16 a[40:43], a[136:139], a[168:171], a[40:43]\n" \
"s_lshl_b32 s7, s3, 3\n" \
"s_lshl_b32 s40, s3, 1\n" \
"v_mfma_f32_16x16x32_bf16 a[40:43], a[200:203], a[232:235], a[40:43]\n" \
"buffer_load_dword v208, v194, s[76:79], s34 offen offset:32\n" \
"v_mfma_f32_16x16x32_bf16 a[44:47], a[136:139], a[172:175], a[44:47]\n" \
"ds_read_b64_tr_b16 a[246:247], v37 offset:9216\n" \
"s_and_b32 s7, s7, 16\n" \
"v_mfma_f32_16x16x32_bf16 a[44:47], a[200:203], a[236:239], a[44:47]\n" \
"s_and_b32 s40, s40, 2\n" \
"s_and_b32 s6, s2, 7\n" \
"v_mfma_f32_16x16x32_bf16 a[60:63], a[140:143], a[172:175], a[60:63]\n" \
"v_bfe_u32 v213, v20, 7, 1\n" \
"s_or_b32 s7, s40, s7\n" \
"v_mfma_f32_16x16x32_bf16 a[60:63], a[204:207], a[236:239], a[60:63]\n" \
"v_and_b32_e32 v234, 0xf0, v234\n" \
"v_cmp_gt_u32_e32 vcc, 16, v231\n" \
"v_mfma_f32_16x16x32_bf16 a[56:59], a[140:143], a[168:171], a[56:59]\n" \
"buffer_load_dword v209, v194, s[76:79], s34 offen offset:1184\n" \
"v_mfma_f32_16x16x32_bf16 a[56:59], a[204:207], a[232:235], a[56:59]\n" \
"ds_read_b64_tr_b16 a[184:185], v38 offset:0\n" \
"v_or3_b32 v213, s7, v213, v219\n" \
"v_mfma_f32_16x16x32_bf16 a[52:55], a[140:143], a[164:167], a[52:55]\n" \
"v_and_b32_e32 v214, 32, v20\n" \
"s_lshl_b32 s6, s6, 5\n" \
"v_mfma_f32_16x16x32_bf16 a[52:55], a[204:207], a[228:231], a[52:55]\n" \
"ds_read_b64_tr_b16 a[186:187], v38 offset:1024\n" \
"v_cndmask_b32_e64 v231, v233, v234, vcc\n" \
"v_mfma_f32_16x16x32_bf16 a[48:51], a[140:143], a[160:163], a[48:51]\n" \
"v_mul_u32_u24_e64 v213, 18, v213\n" \
"v_mov_b32_e32 v215, 0x100\n" \
"v_mfma_f32_16x16x32_bf16 a[48:51], a[204:207], a[224:227], a[48:51]\n" \
"v_mov_b32_e32 v216, s6\n" \
"v_cmp_eq_u32_e32 vcc, 0, v214\n" \
"v_mfma_f32_16x16x32_bf16 a[64:67], a[144:147], a[160:163], a[64:67]\n" \
"buffer_load_dword v211, v194, s[76:79], s34 offen offset:2336\n" \
"v_mfma_f32_16x16x32_bf16 a[64:67], a[208:211], a[224:227], a[64:67]\n" \
"ds_read_b64_tr_b16 a[248:249], v38 offset:8192\n" \
"v_and_or_b32 v213, v218, 1, v213\n" \
"v_mfma_f32_16x16x32_bf16 a[68:71], a[144:147], a[164:167], a[68:71]\n" \
"v_lshlrev_b32_e32 v212, 3, v212\n" \
"v_cndmask_b32_e32 v214, v215, v216, vcc\n" \
"v_mfma_f32_16x16x32_bf16 a[68:71], a[208:211], a[228:231], a[68:71]\n" \
"ds_read_b64_tr_b16 a[250:251], v38 offset:9216\n" \
"v_mad_u32_u24 v213, v213, s5, v214\n" \
"v_mfma_f32_16x16x32_bf16 a[72:75], a[144:147], a[168:171], a[72:75]\n" \
"v_and_or_b32 v213, v20, 16, v213\n" \
"v_add_u32_e32 v9, 0x4800, v213\n" \
"v_mfma_f32_16x16x32_bf16 a[72:75], a[208:211], a[232:235], a[72:75]\n" \
"s_mov_b32 s76, s20\n" \
"s_and_b32 s77, s21, 0xffff\n" \
"v_mfma_f32_16x16x32_bf16 a[76:79], a[144:147], a[172:175], a[76:79]\n" \
"s_mov_b32 s78, 0x480\n" \
"v_mul_u32_u24_e32 v213, 0x120, v218\n" \
"v_mfma_f32_16x16x32_bf16 a[76:79], a[208:211], a[236:239], a[76:79]\n" \
"buffer_load_ushort v210, v195, s[76:79], 0 offen\n" \
"v_mfma_f32_16x16x32_bf16 a[92:95], a[148:151], a[172:175], a[92:95]\n" \
"ds_read_b64_tr_b16 a[188:189], v39 offset:0\n" \
"v_and_or_b32 v10, v212, 24, v213\n" \
"v_mfma_f32_16x16x32_bf16 a[92:95], a[212:215], a[236:239], a[92:95]\n" \
"v_lshrrev_b32_e32 v213, 2, v20\n" \
"v_and_or_b32 v213, v213, 60, s4\n" \
"v_mfma_f32_16x16x32_bf16 a[88:91], a[148:151], a[168:171], a[88:91]\n" \
"s_mov_b32 s76, s8\n" \
"s_nop 0\n" \
"s_and_b32 s77, s9, 0xffff\n" \
"v_mfma_f32_16x16x32_bf16 a[88:91], a[212:215], a[232:235], a[88:91]\n" \
"s_mov_b32 s78, 0x240000\n" \
"ds_read_b64_tr_b16 a[190:191], v39 offset:1024\n" \
"v_mfma_f32_16x16x32_bf16 a[84:87], a[148:151], a[164:167], a[84:87]\n" \
"v_lshl_or_b32 v212, s3, 9, v212\n" \
"v_mad_u32_u24 v4, v224, s5, v223\n" \
"v_mfma_f32_16x16x32_bf16 a[84:87], a[212:215], a[228:231], a[84:87]\n" \
"ds_read_b64_tr_b16 a[252:253], v39 offset:8192\n" \
"v_mad_u32_u24 v8, v232, s5, v231\n" \
"v_mfma_f32_16x16x32_bf16 a[80:83], a[148:151], a[160:163], a[80:83]\n" \
"v_or_b32_e32 v18, 0x27000, v213\n" \
"v_or_b32_e32 v19, 0x27800, v212\n" \
"v_mfma_f32_16x16x32_bf16 a[80:83], a[212:215], a[224:227], a[80:83]\n" \
"v_mov_b32_e32 v219, 0x1000c0c\n" \
"v_mov_b32_e32 v220, 0xc0c0100\n" \
"v_mfma_f32_16x16x32_bf16 a[96:99], a[152:155], a[160:163], a[96:99]\n" \
"ds_read_b64_tr_b16 a[254:255], v39 offset:9216\n" \
"v_bfe_u32 v218, v20, 4, 1\n" \
"v_mfma_f32_16x16x32_bf16 a[96:99], a[216:219], a[224:227], a[96:99]\n" \
"v_lshrrev_b32_e32 v213, 8, v20\n" \
"v_cmp_eq_u32_e32 vcc, 0, v218\n" \
"v_mfma_f32_16x16x32_bf16 a[100:103], a[152:155], a[164:167], a[100:103]\n" \
"s_lshr_b32 s4, s2, 5\n" \
"s_and_b32 s5, s3, 1\n" \
"v_mfma_f32_16x16x32_bf16 a[100:103], a[216:219], a[228:231], a[100:103]\n" \
"v_bfe_u32 v217, v20, 4, 4\n" \
"v_cndmask_b32_e64 v219, v219, v220, vcc\n" \
"v_mfma_f32_16x16x32_bf16 a[104:107], a[152:155], a[168:171], a[104:107]\n" \
"v_mov_b32_e32 v220, 0x3020c0c\n" \
"v_mov_b32_e32 v221, 0xc0c0302\n" \
"v_mfma_f32_16x16x32_bf16 a[104:107], a[216:219], a[232:235], a[104:107]\n" \
"v_lshl_or_b32 v218, v213, 2, v218\n" \
"s_xor_b32 s6, s4, 15\n" \
"v_mfma_f32_16x16x32_bf16 a[108:111], a[152:155], a[172:175], a[108:111]\n" \
"s_cmpk_lt_u32 s3, 2\n" \
"s_cselect_b32 s7, s6, s4\n" \
"v_mfma_f32_16x16x32_bf16 a[108:111], a[216:219], a[236:239], a[108:111]\n" \
"s_lshl_b32 s40, s7, 7\n" \
".fill (L_pb + 48864 - .) / 4, 4, 0xBF800000\n" \
"L_pp12:\n" \
"s_cbranch_execz L_pret\n" \
"s_lshl_b32 s41, s5, 6\n" \
"s_waitcnt lgkmcnt(15)\n" \
"v_mfma_f32_16x16x32_bf16 a[124:127], a[156:159], a[172:175], a[124:127]\n" \
"v_cndmask_b32_e32 v220, v220, v221, vcc\n" \
"v_mov_b32_e32 v221, 0xc0c0c0c\n" \
"v_mfma_f32_16x16x32_bf16 a[124:127], a[220:223], a[236:239], a[124:127]\n" \
"v_cmp_gt_u32_e32 vcc, v218, v217\n" \
"v_or_b32_e32 v218, 2, v218\n" \
"v_mfma_f32_16x16x32_bf16 a[120:123], a[156:159], a[168:171], a[120:123]\n" \
"v_lshrrev_b32_e32 v212, 7, v20\n" \
"s_or_b32 s41, s41, s40\n" \
"v_mfma_f32_16x16x32_bf16 a[120:123], a[220:223], a[232:235], a[120:123]\n" \
"v_cndmask_b32_e32 v40, v221, v219, vcc\n" \
"v_cndmask_b32_e32 v42, v219, v221, vcc\n" \
"v_mfma_f32_16x16x32_bf16 a[116:119], a[156:159], a[164:167], a[116:119]\n" \
"v_cmp_gt_u32_e32 vcc, v218, v217\n" \
"v_and_b32_e32 v218, 16, v20\n" \
"v_mfma_f32_16x16x32_bf16 a[116:119], a[220:223], a[228:231], a[116:119]\n" \
"s_movk_i32 s42, 0x3f80\n" \
"v_or_b32_e32 v214, s41, v212\n" \
"v_mfma_f32_16x16x32_bf16 a[112:115], a[156:159], a[160:163], a[112:115]\n" \
"v_and_b32_e32 v217, 0x320, v20\n" \
"s_movk_i32 s41, 0x100\n" \
"v_mfma_f32_16x16x32_bf16 a[112:115], a[220:223], a[224:227], a[112:115]\n" \
"s_waitcnt vmcnt(7) lgkmcnt(0)\n" \
"s_barrier\n" \
"v_mfma_f32_16x16x32_bf16 v[52:55], a[128:131], a[176:179], v[52:55]\n" \
"s_add_u32 m0, s30, 0x0\n" \
"s_movk_i32 s84, 0x200\n" \
"v_mfma_f32_16x16x32_bf16 v[52:55], a[192:195], a[240:243], v[52:55]\n" \
"buffer_load_dwordx4 v22, s[64:67], s84 offen offset:0 lds\n" \
"v_mfma_f32_16x16x32_bf16 v[56:59], a[128:131], a[180:183], v[56:59]\n" \
"ds_read_b64_tr_b16 a[160:161], v32 offset:32768\n" \
"v_lshlrev_b32_e64 v218, v218, s42\n" \
"v_mfma_f32_16x16x32_bf16 v[56:59], a[192:195], a[244:247], v[56:59]\n" \
"s_movk_i32 s84, 0x1e00\n" \
"v_mfma_f32_16x16x32_bf16 v[60:63], a[128:131], a[184:187], v[60:63]\n" \
"buffer_load_dwordx4 v23, s[64:67], s84 offen offset:1024 lds\n" \
"v_mfma_f32_16x16x32_bf16 v[60:63], a[192:195], a[248:251], v[60:63]\n" \
"ds_read_b64_tr_b16 a[162:163], v32 offset:33792\n" \
"v_mov_b32_e64 v219, 0\n" \
"v_mfma_f32_16x16x32_bf16 v[64:67], a[128:131], a[188:191], v[64:67]\n" \
"ds_read_b64_tr_b16 a[224:225], v32 offset:40960\n" \
"v_lshrrev_b32_e64 v215, 6, v20\n" \
"v_mfma_f32_16x16x32_bf16 v[64:67], a[192:195], a[252:255], v[64:67]\n" \
"ds_read_b128 a[128:131], v28 offset:49152\n" \
"v_cndmask_b32_e64 v41, v221, v220, vcc\n" \
"v_mfma_f32_16x16x32_bf16 v[80:83], a[132:135], a[188:191], v[80:83]\n" \
"ds_read_b128 a[192:195], v29 offset:49152\n" \
"v_cndmask_b32_e64 v43, v220, v221, vcc\n" \
"v_mfma_f32_16x16x32_bf16 v[80:83], a[196:199], a[252:255], v[80:83]\n" \
"s_movk_i32 s84, 0x3a00\n" \
"v_mfma_f32_16x16x32_bf16 v[76:79], a[132:135], a[184:187], v[76:79]\n" \
"buffer_load_dwordx4 v22, s[64:67], s84 offen offset:2048 lds\n" \
"v_mfma_f32_16x16x32_bf16 v[76:79], a[196:199], a[248:251], v[76:79]\n" \
"s_movk_i32 s84, 0x5600\n" \
"v_mfma_f32_16x16x32_bf16 v[72:75], a[132:135], a[180:183], v[72:75]\n" \
"buffer_load_dwordx4 v23, s[64:67], s84 offen offset:3072 lds\n" \
"v_mfma_f32_16x16x32_bf16 v[72:75], a[196:199], a[244:247], v[72:75]\n" \
"s_add_u32 m0, s31, 0x0\n" \
"s_mov_b32 s84, 0x200000\n" \
"v_mfma_f32_16x16x32_bf16 v[68:71], a[132:135], a[176:179], v[68:71]\n" \
"buffer_load_dwordx4 v26, s[68:71], s84 offen offset:0 lds\n" \
"v_mfma_f32_16x16x32_bf16 v[68:71], a[196:199], a[240:243], v[68:71]\n" \
"ds_read_b128 a[132:135], v28 offset:51200\n" \
"v_cmp_eq_u32_e32 vcc, s41, v217\n" \
"v_mfma_f32_16x16x32_bf16 v[84:87], a[136:139], a[176:179], v[84:87]\n" \
"ds_read_b128 a[196:199], v29 offset:51200\n" \
"v_and_b32_e32 v217, 0x330, v20\n" \
"v_mfma_f32_16x16x32_bf16 v[84:87], a[200:203], a[240:243], v[84:87]\n" \
"s_addk_i32 s84, 0x7c00\n" \
"v_mfma_f32_16x16x32_bf16 v[88:91], a[136:139], a[180:183], v[88:91]\n" \
"buffer_load_dwordx4 v26, s[68:71], s84 offen offset:1024 lds\n" \
"v_mfma_f32_16x16x32_bf16 v[88:91], a[200:203], a[244:247], v[88:91]\n" \
"ds_read_b64_tr_b16 a[226:227], v32 offset:41984\n" \
"v_bfe_u32 v216, v20, 6, 2\n" \
"v_mfma_f32_16x16x32_bf16 v[92:95], a[136:139], a[184:187], v[92:95]\n" \
"s_addk_i32 s84, 0x7c00\n" \
"v_mfma_f32_16x16x32_bf16 v[92:95], a[200:203], a[248:251], v[92:95]\n" \
"buffer_load_dwordx4 v27, s[68:71], s84 offen offset:2048 lds\n" \
"v_mfma_f32_16x16x32_bf16 v[96:99], a[136:139], a[188:191], v[96:99]\n" \
"ds_read_b64_tr_b16 a[164:165], v33 offset:32768\n" \
"v_cndmask_b32_e64 v46, v219, v218, vcc\n" \
"v_mfma_f32_16x16x32_bf16 v[96:99], a[200:203], a[252:255], v[96:99]\n" \
"ds_read_b128 a[136:139], v28 offset:53248\n" \
"v_mov_b32_e32 v218, 0x3f80\n" \
"v_mfma_f32_16x16x32_bf16 v[112:115], a[140:143], a[188:191], v[112:115]\n" \
"ds_read_b128 a[200:203], v29 offset:53248\n" \
"v_cmp_eq_u32_e64 vcc, 32, v217\n" \
"v_mfma_f32_16x16x32_bf16 v[112:115], a[204:207], a[252:255], v[112:115]\n" \
"s_addk_i32 s84, 0x7c00\n" \
"v_mfma_f32_16x16x32_bf16 v[108:111], a[140:143], a[184:187], v[108:111]\n" \
"buffer_load_dwordx4 v27, s[68:71], s84 offen offset:3072 lds\n" \
"v_mfma_f32_16x16x32_bf16 v[108:111], a[204:207], a[248:251], v[108:111]\n" \
"s_add_u32 m0, s30, 0x1000\n" \
"s_mov_b32 s84, 0x8200\n" \
"v_mfma_f32_16x16x32_bf16 v[104:107], a[140:143], a[180:183], v[104:107]\n" \
"buffer_load_dwordx4 v22, s[64:67], s84 offen offset:0 lds\n" \
"v_mfma_f32_16x16x32_bf16 v[104:107], a[204:207], a[244:247], v[104:107]\n" \
"ds_read_b64_tr_b16 a[166:167], v33 offset:33792\n" \
"s_add_i32 s41, s40, -16\n" \
"v_mfma_f32_16x16x32_bf16 v[100:103], a[140:143], a[176:179], v[100:103]\n" \
"ds_read_b64_tr_b16 a[228:229], v33 offset:40960\n" \
"v_lshrrev_b32_e64 v219, 1, v20\n" \
"v_mfma_f32_16x16x32_bf16 v[100:103], a[204:207], a[240:243], v[100:103]\n" \
"ds_read_b128 a[140:143], v28 offset:55296\n" \
"v_bfe_u32 v215, v215, 1, 1\n" \
"v_mfma_f32_16x16x32_bf16 v[116:119], a[144:147], a[176:179], v[116:119]\n" \
"ds_read_b128 a[204:207], v29 offset:55296\n" \
"v_cndmask_b32_e64 v47, 0, v218, vcc\n" \
"v_mfma_f32_16x16x32_bf16 v[116:119], a[208:211], a[240:243], v[116:119]\n" \
"s_addk_i32 s84, 0x1c00\n" \
"v_mfma_f32_16x16x32_bf16 v[120:123], a[144:147], a[180:183], v[120:123]\n" \
"buffer_load_dwordx4 v23, s[64:67], s84 offen offset:1024 lds\n" \
"v_mfma_f32_16x16x32_bf16 v[120:123], a[208:211], a[244:247], v[120:123]\n" \
"s_addk_i32 s84, 0x1c00\n" \
"v_mfma_f32_16x16x32_bf16 v[124:127], a[144:147], a[184:187], v[124:127]\n" \
"buffer_load_dwordx4 v22, s[64:67], s84 offen offset:2048 lds\n" \
"v_mfma_f32_16x16x32_bf16 v[124:127], a[208:211], a[248:251], v[124:127]\n" \
"s_addk_i32 s84, 0x1c00\n" \
"v_mfma_f32_16x16x32_bf16 v[128:131], a[144:147], a[188:191], v[128:131]\n" \
"buffer_load_dwordx4 v23, s[64:67], s84 offen offset:3072 lds\n" \
"v_mfma_f32_16x16x32_bf16 v[128:131], a[208:211], a[252:255], v[128:131]\n" \
"ds_read_b128 a[144:147], v28 offset:32768\n" \
"s_mov_b32 s42, 0\n" \
"v_mfma_f32_16x16x32_bf16 v[144:147], a[148:151], a[188:191], v[144:147]\n" \
"ds_read_b128 a[208:211], v29 offset:32768\n" \
"v_mfma_f32_16x16x32_bf16 v[144:147], a[212:215], a[252:255], v[144:147]\n" \
"s_add_u32 m0, s31, 0x4000\n" \
"s_mov_b32 s84, 0x200000\n" \
"v_mfma_f32_16x16x32_bf16 v[140:143], a[148:151], a[184:187], v[140:143]\n" \
"buffer_load_dwordx4 v26, s[72:75], s84 offen offset:0 lds\n" \
"v_mfma_f32_16x16x32_bf16 v[140:143], a[212:215], a[248:251], v[140:143]\n" \
"ds_read_b64_tr_b16 a[230:231], v33 offset:41984\n" \
"v_mfma_f32_16x16x32_bf16 v[136:139], a[148:151], a[180:183], v[136:139]\n" \
"s_addk_i32 s84, 0x7c00\n" \
"s_nop 0\n" \
"v_mfma_f32_16x16x32_bf16 v[136:139], a[212:215], a[244:247], v[136:139]\n" \
"buffer_load_dwordx4 v26, s[72:75], s84 offen offset:1024 lds\n" \
"v_mfma_f32_16x16x32_bf16 v[132:135], a[148:151], a[176:179], v[132:135]\n" \
"ds_read_b64_tr_b16 a[168:169], v34 offset:32768\n" \
"v_mfma_f32_16x16x32_bf16 v[132:135], a[212:215], a[240:243], v[132:135]\n" \
"ds_read_b128 a[148:151], v28 offset:34816\n" \
"v_mfma_f32_16x16x32_bf16 v[148:151], a[152:155], a[176:179], v[148:151]\n" \
"ds_read_b128 a[212:215], v29 offset:34816\n" \
"v_mfma_f32_16x16x32_bf16 v[148:151], a[216:219], a[240:243], v[148:151]\n" \
"ds_read_b64_tr_b16 a[170:171], v34 offset:33792\n" \
"v_mfma_f32_16x16x32_bf16 v[152:155], a[152:155], a[180:183], v[152:155]\n" \
"ds_read_b64_tr_b16 a[232:233], v34 offset:40960\n" \
"v_mfma_f32_16x16x32_bf16 v[152:155], a[216:219], a[244:247], v[152:155]\n" \
"ds_read_b64_tr_b16 a[234:235], v34 offset:41984\n" \
"v_mfma_f32_16x16x32_bf16 v[156:159], a[152:155], a[184:187], v[156:159]\n" \
"ds_read_b64_tr_b16 a[172:173], v35 offset:32768\n" \
"v_mfma_f32_16x16x32_bf16 v[156:159], a[216:219], a[248:251], v[156:159]\n" \
"ds_read_b64_tr_b16 a[174:175], v35 offset:33792\n" \
"v_mfma_f32_16x16x32_bf16 v[160:163], a[152:155], a[188:191], v[160:163]\n" \
"ds_read_b64_tr_b16 a[236:237], v35 offset:40960\n" \
"v_mfma_f32_16x16x32_bf16 v[160:163], a[216:219], a[252:255], v[160:163]\n" \
"ds_read_b128 a[152:155], v28 offset:36864\n" \
"v_mfma_f32_16x16x32_bf16 v[176:179], a[156:159], a[188:191], v[176:179]\n" \
"ds_read_b128 a[216:219], v29 offset:36864\n" \
"v_mfma_f32_16x16x32_bf16 v[176:179], a[220:223], a[252:255], v[176:179]\n" \
"ds_read_b64_tr_b16 a[238:239], v35 offset:41984\n" \
"v_mfma_f32_16x16x32_bf16 v[172:175], a[156:159], a[184:187], v[172:175]\n" \
"s_cmp_lg_u32 s40, 0\n" \
"s_cselect_b32 s41, s41, s42\n" \
"v_mfma_f32_16x16x32_bf16 v[172:175], a[220:223], a[248:251], v[172:175]\n" \
"v_lshl_or_b32 v217, s5, 3, v212\n" \
"v_lshlrev_b32_e64 v218, 10, v213\n" \
"v_mfma_f32_16x16x32_bf16 v[168:171], a[156:159], a[180:183], v[168:171]\n" \
"v_lshlrev_b32_e32 v216, 7, v216\n" \
"v_and_b32_e32 v219, 24, v219\n" \
"v_mfma_f32_16x16x32_bf16 v[168:171], a[220:223], a[244:247], v[168:171]\n" \
"v_and_or_b32 v212, v212, 2, v215\n" \
"v_add_u32_e64 v217, s41, v217\n" \
"v_mfma_f32_16x16x32_bf16 v[164:167], a[156:159], a[176:179], v[164:167]\n" \
"v_or3_b32 v216, v216, v218, v219\n" \
"v_lshlrev_b32_e64 v212, 5, v212\n" \
"v_mfma_f32_16x16x32_bf16 v[164:167], a[220:223], a[240:243], v[164:167]\n" \
"ds_read_b128 a[156:159], v28 offset:38912\n" \
"ds_read_b128 a[220:223], v29 offset:38912\n" \
"s_waitcnt lgkmcnt(15)\n" \
"v_mfma_f32_16x16x32_bf16 a[0:3], a[128:131], a[160:163], a[0:3]\n" \
"s_addk_i32 s84, 0x7c00\n" \
"v_mfma_f32_16x16x32_bf16 a[0:3], a[192:195], a[224:227], a[0:3]\n" \
"buffer_load_dwordx4 v27, s[72:75], s84 offen offset:2048 lds\n" \
"v_mfma_f32_16x16x32_bf16 a[4:7], a[128:131], a[164:167], a[4:7]\n" \
"ds_read_b64_tr_b16 a[176:177], v36 offset:32768\n" \
"s_mov_b32 s41, 0x14000\n" \
"s_waitcnt lgkmcnt(15)\n" \
"v_mfma_f32_16x16x32_bf16 a[4:7], a[192:195], a[228:231], a[4:7]\n" \
"s_addk_i32 s84, 0x7c00\n" \
"s_waitcnt lgkmcnt(11)\n" \
"v_mfma_f32_16x16x32_bf16 a[8:11], a[128:131], a[168:171], a[8:11]\n" \
"buffer_load_dwordx4 v27, s[72:75], s84 offen offset:3072 lds\n" \
"s_waitcnt lgkmcnt(9)\n" \
"v_mfma_f32_16x16x32_bf16 a[8:11], a[192:195], a[232:235], a[8:11]\n" \
"ds_read_b64_tr_b16 a[178:179], v36 offset:33792\n" \
"v_or3_b32 v48, v216, v212, s41\n" \
"s_waitcnt lgkmcnt(8)\n" \
"v_mfma_f32_16x16x32_bf16 a[12:15], a[128:131], a[172:175], a[12:15]\n" \
"ds_read_b64_tr_b16 a[240:241], v36 offset:40960\n" \
"s_lshl_b32 s41, s5, 2\n" \
"s_waitcnt lgkmcnt(5)\n" \
"v_mfma_f32_16x16x32_bf16 a[12:15], a[192:195], a[236:239], a[12:15]\n" \
"s_lshl_b32 s7, s7, 3\n" \
"v_or_b32_e64 v212, s41, v213\n" \
"v_mfma_f32_16x16x32_bf16 a[28:31], a[132:135], a[172:175], a[28:31]\n" \
"s_or_b32 s7, s7, s41\n" \
"s_mov_b32 s41, 0xff800000\n" \
"v_mfma_f32_16x16x32_bf16 a[28:31], a[196:199], a[236:239], a[28:31]\n" \
"ds_read_b64_tr_b16 a[242:243], v36 offset:41984\n" \
"v_mfma_f32_16x16x32_bf16 a[24:27], a[132:135], a[168:171], a[24:27]\n" \
"s_cmp_lg_u32 s40, 0\n" \
"s_cselect_b32 s47, s42, s41\n" \
"v_mfma_f32_16x16x32_bf16 a[24:27], a[196:199], a[232:235], a[24:27]\n" \
"s_mov_b32 s40, 0xc000\n" \
"s_mov_b32 s41, 0x8000\n" \
"v_mfma_f32_16x16x32_bf16 a[20:23], a[132:135], a[164:167], a[20:23]\n" \
"s_cmp_lg_u32 s4, 0\n" \
"s_cselect_b32 s51, s40, s41\n" \
"v_mfma_f32_16x16x32_bf16 a[20:23], a[196:199], a[228:231], a[20:23]\n" \
"ds_read_b64_tr_b16 a[180:181], v37 offset:32768\n" \
"s_movk_i32 s40, 0x4000\n" \
"v_mfma_f32_16x16x32_bf16 a[16:19], a[132:135], a[160:163], a[16:19]\n" \
"s_cmp_lg_u32 s4, 0\n" \
"s_cselect_b32 s53, s41, s40\n" \
"v_mfma_f32_16x16x32_bf16 a[16:19], a[196:199], a[224:227], a[16:19]\n" \
"s_lshl_b32 s40, s2, 2\n" \
"s_and_b32 s40, s40, 28\n" \
"v_mfma_f32_16x16x32_bf16 a[32:35], a[136:139], a[160:163], a[32:35]\n" \
"s_waitcnt vmcnt(16)\n" \
"v_or_b32_e32 v204, v210, v204\n" \
"v_or_b32_e64 v206, v210, v206\n" \
"v_mfma_f32_16x16x32_bf16 a[32:35], a[200:203], a[224:227], a[32:35]\n" \
"v_mov_b32_e64 v210, 0\n" \
"s_add_u32 s88, s35, 0x1000\n" \
"v_mfma_f32_16x16x32_bf16 a[36:39], a[136:139], a[164:167], a[36:39]\n" \
"ds_read_b64_tr_b16 a[182:183], v37 offset:33792\n" \
"s_bfe_u32 s45, s2, 0x20003\n" \
"v_mfma_f32_16x16x32_bf16 a[36:39], a[200:203], a[228:231], a[36:39]\n" \
"ds_read_b64_tr_b16 a[244:245], v37 offset:40960\n" \
"s_lshr_b32 s44, s3, 1\n" \
"v_mfma_f32_16x16x32_bf16 a[40:43], a[136:139], a[168:171], a[40:43]\n" \
"s_mov_b32 m0, s32\n" \
"s_or_b32 s46, s40, s45\n" \
"v_mfma_f32_16x16x32_bf16 a[40:43], a[200:203], a[232:235], a[40:43]\n" \
"s_mov_b32 exec_lo, 0xffffffff\n" \
"s_mov_b32 exec_hi, 0x0\n" \
"global_load_lds_dword v[188:189], off\n" \
"s_mov_b64 exec, -1\n" \
"v_mfma_f32_16x16x32_bf16 a[44:47], a[136:139], a[172:175], a[44:47]\n" \
"ds_read_b64_tr_b16 a[246:247], v37 offset:41984\n" \
"s_lshl_b32 s43, s44, 15\n" \
"v_mfma_f32_16x16x32_bf16 a[44:47], a[200:203], a[236:239], a[44:47]\n" \
"s_lshl_b32 s41, s5, 14\n" \
"s_lshl_b32 s48, s46, 20\n" \
"v_mfma_f32_16x16x32_bf16 a[60:63], a[140:143], a[172:175], a[60:63]\n" \
"ds_write_b128 v196, v[200:203]\n" \
"v_mfma_f32_16x16x32_bf16 a[60:63], a[204:207], a[236:239], a[60:63]\n" \
"s_or_b32 s41, s43, s41\n" \
"s_add_i32 s49, s48, 0x1000000\n" \
"v_mfma_f32_16x16x32_bf16 a[56:59], a[140:143], a[168:171], a[56:59]\n" \
"ds_read_b64_tr_b16 a[184:185], v38 offset:32768\n" \
"s_or_b32 s42, s49, s41\n" \
"v_mfma_f32_16x16x32_bf16 a[56:59], a[204:207], a[232:235], a[56:59]\n" \
"s_lshl_b32 s4, s4, 16\n" \
"s_or_b32 s41, s42, s4\n" \
"v_mfma_f32_16x16x32_bf16 a[52:55], a[140:143], a[164:167], a[52:55]\n" \
"s_lshl_b32 s4, s6, 16\n" \
"s_or_b32 s42, s42, s4\n" \
"v_mfma_f32_16x16x32_bf16 a[52:55], a[204:207], a[228:231], a[52:55]\n" \
"ds_read_b64_tr_b16 a[186:187], v38 offset:33792\n" \
"s_lshl_b32 s4, s5, 11\n" \
"v_mfma_f32_16x16x32_bf16 a[48:51], a[140:143], a[160:163], a[48:51]\n" \
"s_mul_i32 s5, s7, 0x24800\n" \
"s_lshl_b32 s6, s46, 9\n" \
"v_mfma_f32_16x16x32_bf16 a[48:51], a[204:207], a[224:227], a[48:51]\n" \
"s_lshl_b32 s7, s40, 6\n" \
"s_add_i32 s6, s5, s6\n" \
"v_mfma_f32_16x16x32_bf16 a[64:67], a[144:147], a[160:163], a[64:67]\n" \
"ds_write_b128 v197, v[204:207]\n" \
"v_mfma_f32_16x16x32_bf16 a[64:67], a[208:211], a[224:227], a[64:67]\n" \
"ds_read_b64_tr_b16 a[248:249], v38 offset:40960\n" \
"s_or_b32 s5, s5, s7\n" \
"v_mfma_f32_16x16x32_bf16 a[68:71], a[144:147], a[164:167], a[68:71]\n" \
"s_lshl_b32 s7, s45, 2\n" \
"s_or_b32 s5, s5, s7\n" \
"v_mfma_f32_16x16x32_bf16 a[68:71], a[208:211], a[228:231], a[68:71]\n" \
"ds_read_b64_tr_b16 a[250:251], v38 offset:41984\n" \
"s_lshl_b32 s7, s3, 12\n" \
"v_mfma_f32_16x16x32_bf16 a[72:75], a[144:147], a[168:171], a[72:75]\n" \
"s_or_b32 s50, s49, s7\n" \
"s_lshl_b32 s7, s3, 11\n" \
"v_mfma_f32_16x16x32_bf16 a[72:75], a[208:211], a[232:235], a[72:75]\n" \
"v_lshrrev_b32_e32 v215, 4, v20\n" \
"v_bfe_u32 v216, v20, 4, 3\n" \
"v_mfma_f32_16x16x32_bf16 a[76:79], a[144:147], a[172:175], a[76:79]\n" \
"s_add_i32 s52, s48, s7\n" \
"s_add_i32 s53, s53, s7\n" \
"v_mfma_f32_16x16x32_bf16 a[76:79], a[208:211], a[236:239], a[76:79]\n" \
"ds_read_b64_tr_b16 a[188:189], v39 offset:32768\n" \
"v_mul_u32_u24_e32 v214, 0x480, v214\n" \
"v_mfma_f32_16x16x32_bf16 a[92:95], a[148:151], a[172:175], a[92:95]\n" \
"ds_write_b128 v198, v[208:211]\n" \
"v_mfma_f32_16x16x32_bf16 a[92:95], a[212:215], a[236:239], a[92:95]\n" \
"v_bitop3_b32 v218, v213, v215, 7 bitop3:0x78\n" \
"v_bitop3_b32 v213, v213, v216, 4 bitop3:0x36\n" \
"v_mfma_f32_16x16x32_bf16 a[88:91], a[148:151], a[168:171], a[88:91]\n" \
"ds_read_b64_tr_b16 a[190:191], v39 offset:33792\n" \
"s_lshl_b32 s7, s3, 10\n" \
"v_mfma_f32_16x16x32_bf16 a[88:91], a[212:215], a[232:235], a[88:91]\n" \
"s_or_b32 s4, s43, s4\n" \
"v_lshl_or_b32 v25, v213, 4, v214\n" \
"v_mfma_f32_16x16x32_bf16 a[84:87], a[148:151], a[164:167], a[84:87]\n" \
"v_mul_u32_u24_e32 v213, 0x480, v217\n" \
"v_bitop3_b32 v212, v215, v212, 7 bitop3:0x6c\n" \
"v_mfma_f32_16x16x32_bf16 a[84:87], a[212:215], a[228:231], a[84:87]\n" \
"ds_read_b64_tr_b16 a[252:253], v39 offset:40960\n" \
"s_or_b32 s40, s7, 0x14000\n" \
"v_mfma_f32_16x16x32_bf16 a[80:83], a[148:151], a[160:163], a[80:83]\n" \
"s_lshl_b32 s43, s44, 13\n" \
"s_mulk_i32 s44, 0xe800\n" \
"v_mfma_f32_16x16x32_bf16 a[80:83], a[212:215], a[224:227], a[80:83]\n" \
"s_lshl_b32 s7, s3, 8\n" \
"s_nop 0\n" \
"v_lshl_or_b32 v24, v218, 4, v214\n" \
"v_mfma_f32_16x16x32_bf16 a[96:99], a[152:155], a[160:163], a[96:99]\n" \
"ds_read_b64_tr_b16 a[254:255], v39 offset:41984\n" \
"v_lshl_or_b32 v21, v212, 4, v213\n" \
"v_mfma_f32_16x16x32_bf16 a[96:99], a[216:219], a[224:227], a[96:99]\n" \
"s_add_i32 s44, s44, 0x17000\n" \
"s_or_b32 s45, s7, 0x27000\n" \
"v_mfma_f32_16x16x32_bf16 a[100:103], a[152:155], a[164:167], a[100:103]\n" \
"s_or_b32 s46, s4, 0x10000\n" \
"s_add_i32 s48, s6, 0x3000000\n" \
"v_mfma_f32_16x16x32_bf16 a[100:103], a[216:219], a[228:231], a[100:103]\n" \
"s_add_i32 s49, s5, 0x3004000\n" \
"s_add_i32 s52, s52, 0x1010000\n" \
"v_mfma_f32_16x16x32_bf16 a[104:107], a[152:155], a[168:171], a[104:107]\n" \
"v_mfma_f32_16x16x32_bf16 a[104:107], a[216:219], a[232:235], a[104:107]\n" \
"v_mfma_f32_16x16x32_bf16 a[108:111], a[152:155], a[172:175], a[108:111]\n" \
"v_mfma_f32_16x16x32_bf16 a[108:111], a[216:219], a[236:239], a[108:111]\n" \
"s_waitcnt lgkmcnt(15)\n" \
"v_mfma_f32_16x16x32_bf16 a[124:127], a[156:159], a[172:175], a[124:127]\n" \
"v_mfma_f32_16x16x32_bf16 a[124:127], a[220:223], a[236:239], a[124:127]\n" \
"v_mfma_f32_16x16x32_bf16 a[120:123], a[156:159], a[168:171], a[120:123]\n" \
"v_mfma_f32_16x16x32_bf16 a[120:123], a[220:223], a[232:235], a[120:123]\n" \
"v_mfma_f32_16x16x32_bf16 a[116:119], a[156:159], a[164:167], a[116:119]\n" \
"v_mfma_f32_16x16x32_bf16 a[116:119], a[220:223], a[228:231], a[116:119]\n" \
"v_mfma_f32_16x16x32_bf16 a[112:115], a[156:159], a[160:163], a[112:115]\n" \
"v_mfma_f32_16x16x32_bf16 a[112:115], a[220:223], a[224:227], a[112:115]\n" \
"s_waitcnt vmcnt(1) lgkmcnt(0)\n" \
"s_barrier\n" \
"v_mfma_f32_16x16x32_bf16 v[52:55], a[128:131], a[176:179], v[52:55]\n" \
"s_add_u32 m0, s30, 0x8000\n" \
"s_movk_i32 s84, 0x280\n" \
"v_mfma_f32_16x16x32_bf16 v[52:55], a[192:195], a[240:243], v[52:55]\n" \
"buffer_load_dwordx4 v22, s[64:67], s84 offen offset:0 lds\n" \
"v_mfma_f32_16x16x32_bf16 v[56:59], a[128:131], a[180:183], v[56:59]\n" \
"s_movk_i32 s84, 0x1e80\n" \
"v_mfma_f32_16x16x32_bf16 v[56:59], a[192:195], a[244:247], v[56:59]\n" \
"buffer_load_dwordx4 v23, s[64:67], s84 offen offset:1024 lds\n" \
"v_mfma_f32_16x16x32_bf16 v[60:63], a[128:131], a[184:187], v[60:63]\n" \
"ds_read_b64_tr_b16 a[160:161], v32 offset:0\n" \
"v_mfma_f32_16x16x32_bf16 v[60:63], a[192:195], a[248:251], v[60:63]\n" \
"s_movk_i32 s84, 0x3a80\n" \
"v_mfma_f32_16x16x32_bf16 v[64:67], a[128:131], a[188:191], v[64:67]\n" \
"buffer_load_dwordx4 v22, s[64:67], s84 offen offset:2048 lds\n" \
"v_mfma_f32_16x16x32_bf16 v[64:67], a[192:195], a[252:255], v[64:67]\n" \
"ds_read_b128 a[128:131], v28 offset:16384\n" \
"v_mfma_f32_16x16x32_bf16 v[80:83], a[132:135], a[188:191], v[80:83]\n" \
"ds_read_b128 a[192:195], v29 offset:16384\n" \
"v_mfma_f32_16x16x32_bf16 v[80:83], a[196:199], a[252:255], v[80:83]\n" \
"s_movk_i32 s84, 0x5680\n" \
"s_nop 0\n" \
"v_mfma_f32_16x16x32_bf16 v[76:79], a[132:135], a[184:187], v[76:79]\n" \
"buffer_load_dwordx4 v23, s[64:67], s84 offen offset:3072 lds\n" \
"v_mfma_f32_16x16x32_bf16 v[76:79], a[196:199], a[248:251], v[76:79]\n" \
"s_add_u32 m0, s31, 0x8000\n" \
"s_mov_b32 s84, 0x280000\n" \
"v_mfma_f32_16x16x32_bf16 v[72:75], a[132:135], a[180:183], v[72:75]\n" \
"buffer_load_dwordx4 v26, s[68:71], s84 offen offset:0 lds\n" \
"v_mfma_f32_16x16x32_bf16 v[72:75], a[196:199], a[244:247], v[72:75]\n" \
"ds_read_b64_tr_b16 a[162:163], v32 offset:1024\n" \
"v_mfma_f32_16x16x32_bf16 v[68:71], a[132:135], a[176:179], v[68:71]\n" \
"ds_read_b64_tr_b16 a[224:225], v32 offset:8192\n" \
"v_mfma_f32_16x16x32_bf16 v[68:71], a[196:199], a[240:243], v[68:71]\n" \
"ds_read_b128 a[132:135], v28 offset:18432\n" \
"v_mfma_f32_16x16x32_bf16 v[84:87], a[136:139], a[176:179], v[84:87]\n" \
"ds_read_b128 a[196:199], v29 offset:18432\n" \
"v_mfma_f32_16x16x32_bf16 v[84:87], a[200:203], a[240:243], v[84:87]\n" \
"s_addk_i32 s84, 0x7c00\n" \
"v_mfma_f32_16x16x32_bf16 v[88:91], a[136:139], a[180:183], v[88:91]\n" \
"buffer_load_dwordx4 v26, s[68:71], s84 offen offset:1024 lds\n" \
"v_mfma_f32_16x16x32_bf16 v[88:91], a[200:203], a[244:247], v[88:91]\n" \
"s_addk_i32 s84, 0x7c00\n" \
"v_mfma_f32_16x16x32_bf16 v[92:95], a[136:139], a[184:187], v[92:95]\n" \
"buffer_load_dwordx4 v27, s[68:71], s84 offen offset:2048 lds\n" \
"v_mfma_f32_16x16x32_bf16 v[92:95], a[200:203], a[248:251], v[92:95]\n" \
"s_addk_i32 s84, 0x7c00\n" \
"s_nop 0\n" \
"v_mfma_f32_16x16x32_bf16 v[96:99], a[136:139], a[188:191], v[96:99]\n" \
"buffer_load_dwordx4 v27, s[68:71], s84 offen offset:3072 lds\n" \
"v_mfma_f32_16x16x32_bf16 v[96:99], a[200:203], a[252:255], v[96:99]\n" \
"ds_read_b128 a[136:139], v28 offset:20480\n" \
"v_mfma_f32_16x16x32_bf16 v[112:115], a[140:143], a[188:191], v[112:115]\n" \
"ds_read_b128 a[200:203], v29 offset:20480\n" \
"v_mfma_f32_16x16x32_bf16 v[112:115], a[204:207], a[252:255], v[112:115]\n" \
"s_add_u32 m0, s30, 0x9000\n" \
"s_mov_b32 s84, 0x8280\n" \
"v_mfma_f32_16x16x32_bf16 v[108:111], a[140:143], a[184:187], v[108:111]\n" \
"buffer_load_dwordx4 v22, s[64:67], s84 offen offset:0 lds\n" \
"v_mfma_f32_16x16x32_bf16 v[108:111], a[204:207], a[248:251], v[108:111]\n" \
"s_addk_i32 s84, 0x1c00\n" \
"v_mfma_f32_16x16x32_bf16 v[104:107], a[140:143], a[180:183], v[104:107]\n" \
"buffer_load_dwordx4 v23, s[64:67], s84 offen offset:1024 lds\n" \
"v_mfma_f32_16x16x32_bf16 v[104:107], a[204:207], a[244:247], v[104:107]\n" \
"s_addk_i32 s84, 0x1c00\n" \
"v_mfma_f32_16x16x32_bf16 v[100:103], a[140:143], a[176:179], v[100:103]\n" \
"buffer_load_dwordx4 v22, s[64:67], s84 offen offset:2048 lds\n" \
"v_mfma_f32_16x16x32_bf16 v[100:103], a[204:207], a[240:243], v[100:103]\n" \
"ds_read_b128 a[140:143], v28 offset:22528\n" \
"v_mfma_f32_16x16x32_bf16 v[116:119], a[144:147], a[176:179], v[116:119]\n" \
"ds_read_b128 a[204:207], v29 offset:22528\n" \
"v_mfma_f32_16x16x32_bf16 v[116:119], a[208:211], a[240:243], v[116:119]\n" \
"s_addk_i32 s84, 0x1c00\n" \
"v_mfma_f32_16x16x32_bf16 v[120:123], a[144:147], a[180:183], v[120:123]\n" \
"buffer_load_dwordx4 v23, s[64:67], s84 offen offset:3072 lds\n" \
"v_mfma_f32_16x16x32_bf16 v[120:123], a[208:211], a[244:247], v[120:123]\n" \
"s_add_u32 m0, s31, 0xc000\n" \
"s_mov_b32 s84, 0x280000\n" \
"v_mfma_f32_16x16x32_bf16 v[124:127], a[144:147], a[184:187], v[124:127]\n" \
"buffer_load_dwordx4 v26, s[72:75], s84 offen offset:0 lds\n" \
"v_mfma_f32_16x16x32_bf16 v[124:127], a[208:211], a[248:251], v[124:127]\n" \
"s_addk_i32 s84, 0x7c00\n" \
"v_mfma_f32_16x16x32_bf16 v[128:131], a[144:147], a[188:191], v[128:131]\n" \
"buffer_load_dwordx4 v26, s[72:75], s84 offen offset:1024 lds\n" \
"v_mfma_f32_16x16x32_bf16 v[128:131], a[208:211], a[252:255], v[128:131]\n" \
"ds_read_b128 a[144:147], v28 offset:0\n" \
"v_mfma_f32_16x16x32_bf16 v[144:147], a[148:151], a[188:191], v[144:147]\n" \
"ds_read_b128 a[208:211], v29 offset:0\n" \
"v_mfma_f32_16x16x32_bf16 v[144:147], a[212:215], a[252:255], v[144:147]\n" \
"ds_read_b128 v[200:203], v199 offset:0\n" \
"v_mfma_f32_16x16x32_bf16 v[140:143], a[148:151], a[184:187], v[140:143]\n" \
"ds_read_b128 v[204:207], v199 offset:4096\n" \
"v_mfma_f32_16x16x32_bf16 v[140:143], a[212:215], a[248:251], v[140:143]\n" \
"ds_read_b64_tr_b16 a[226:227], v32 offset:9216\n" \
"v_mfma_f32_16x16x32_bf16 v[136:139], a[148:151], a[180:183], v[136:139]\n" \
"ds_read_b64_tr_b16 a[164:165], v33 offset:0\n" \
"v_mfma_f32_16x16x32_bf16 v[136:139], a[212:215], a[244:247], v[136:139]\n" \
"ds_read_b128 v[208:211], v199 offset:8192\n" \
"v_mfma_f32_16x16x32_bf16 v[132:135], a[148:151], a[176:179], v[132:135]\n" \
"ds_read_b64_tr_b16 a[166:167], v33 offset:1024\n" \
"v_mfma_f32_16x16x32_bf16 v[132:135], a[212:215], a[240:243], v[132:135]\n" \
"ds_read_b128 a[148:151], v28 offset:2048\n" \
"v_mfma_f32_16x16x32_bf16 v[148:151], a[152:155], a[176:179], v[148:151]\n" \
"ds_read_b128 a[212:215], v29 offset:2048\n" \
"v_mfma_f32_16x16x32_bf16 v[148:151], a[216:219], a[240:243], v[148:151]\n" \
"ds_read_b64_tr_b16 a[228:229], v33 offset:8192\n" \
"v_mfma_f32_16x16x32_bf16 v[152:155], a[152:155], a[180:183], v[152:155]\n" \
"ds_read_b64_tr_b16 a[230:231], v33 offset:9216\n" \
"v_mfma_f32_16x16x32_bf16 v[152:155], a[216:219], a[244:247], v[152:155]\n" \
"ds_read_b64_tr_b16 a[168:169], v34 offset:0\n" \
"v_mfma_f32_16x16x32_bf16 v[156:159], a[152:155], a[184:187], v[156:159]\n" \
"ds_read_b64_tr_b16 a[170:171], v34 offset:1024\n" \
"v_mfma_f32_16x16x32_bf16 v[156:159], a[216:219], a[248:251], v[156:159]\n" \
"ds_read_b64_tr_b16 a[232:233], v34 offset:8192\n" \
"v_mfma_f32_16x16x32_bf16 v[160:163], a[152:155], a[188:191], v[160:163]\n" \
"ds_read_b64_tr_b16 a[234:235], v34 offset:9216\n" \
"v_mfma_f32_16x16x32_bf16 v[160:163], a[216:219], a[252:255], v[160:163]\n" \
"ds_read_b128 a[152:155], v28 offset:4096\n" \
"v_mfma_f32_16x16x32_bf16 v[176:179], a[156:159], a[188:191], v[176:179]\n" \
"ds_read_b128 a[216:219], v29 offset:4096\n" \
"v_mfma_f32_16x16x32_bf16 v[176:179], a[220:223], a[252:255], v[176:179]\n" \
"ds_read_b64_tr_b16 a[172:173], v35 offset:0\n" \
"v_mfma_f32_16x16x32_bf16 v[172:175], a[156:159], a[184:187], v[172:175]\n" \
"ds_read_b64_tr_b16 a[174:175], v35 offset:1024\n" \
"v_mfma_f32_16x16x32_bf16 v[172:175], a[220:223], a[248:251], v[172:175]\n" \
"ds_read_b64_tr_b16 a[236:237], v35 offset:8192\n" \
"v_mfma_f32_16x16x32_bf16 v[168:171], a[156:159], a[180:183], v[168:171]\n" \
"ds_read_b64_tr_b16 a[238:239], v35 offset:9216\n" \
"v_mfma_f32_16x16x32_bf16 v[168:171], a[220:223], a[244:247], v[168:171]\n" \
"v_mfma_f32_16x16x32_bf16 v[164:167], a[156:159], a[176:179], v[164:167]\n" \
"v_mfma_f32_16x16x32_bf16 v[164:167], a[220:223], a[240:243], v[164:167]\n" \
"ds_read_b128 a[156:159], v28 offset:6144\n" \
"ds_read_b128 a[220:223], v29 offset:6144\n" \
"s_waitcnt lgkmcnt(15)\n" \
"v_mfma_f32_16x16x32_bf16 a[0:3], a[128:131], a[160:163], a[0:3]\n" \
"s_addk_i32 s84, 0x7c00\n" \
"v_mfma_f32_16x16x32_bf16 a[0:3], a[192:195], a[224:227], a[0:3]\n" \
"buffer_load_dwordx4 v27, s[72:75], s84 offen offset:2048 lds\n" \
"v_mfma_f32_16x16x32_bf16 a[4:7], a[128:131], a[164:167], a[4:7]\n" \
"ds_read_b64_tr_b16 a[176:177], v36 offset:0\n" \
"s_waitcnt lgkmcnt(13)\n" \
"v_mfma_f32_16x16x32_bf16 a[4:7], a[192:195], a[228:231], a[4:7]\n" \
"s_addk_i32 s84, 0x7c00\n" \
"s_waitcnt lgkmcnt(11)\n" \
"v_mfma_f32_16x16x32_bf16 a[8:11], a[128:131], a[168:171], a[8:11]\n" \
"buffer_load_dwordx4 v27, s[72:75], s84 offen offset:3072 lds\n" \
"s_waitcnt lgkmcnt(9)\n" \
"v_mfma_f32_16x16x32_bf16 a[8:11], a[192:195], a[232:235], a[8:11]\n" \
"ds_read_b64_tr_b16 a[178:179], v36 offset:1024\n" \
"s_waitcnt lgkmcnt(6)\n" \
"v_mfma_f32_16x16x32_bf16 a[12:15], a[128:131], a[172:175], a[12:15]\n" \
"ds_read_b64_tr_b16 a[240:241], v36 offset:8192\n" \
"s_waitcnt lgkmcnt(5)\n" \
"v_mfma_f32_16x16x32_bf16 a[12:15], a[192:195], a[236:239], a[12:15]\n" \
"v_mfma_f32_16x16x32_bf16 a[28:31], a[132:135], a[172:175], a[28:31]\n" \
"v_mfma_f32_16x16x32_bf16 a[28:31], a[196:199], a[236:239], a[28:31]\n" \
"ds_read_b64_tr_b16 a[242:243], v36 offset:9216\n" \
"v_mfma_f32_16x16x32_bf16 a[24:27], a[132:135], a[168:171], a[24:27]\n" \
"v_mfma_f32_16x16x32_bf16 a[24:27], a[196:199], a[232:235], a[24:27]\n" \
"v_mfma_f32_16x16x32_bf16 a[20:23], a[132:135], a[164:167], a[20:23]\n" \
"v_mfma_f32_16x16x32_bf16 a[20:23], a[196:199], a[228:231], a[20:23]\n" \
"ds_read_b64_tr_b16 a[180:181], v37 offset:0\n" \
"v_mfma_f32_16x16x32_bf16 a[16:19], a[132:135], a[160:163], a[16:19]\n" \
"v_mfma_f32_16x16x32_bf16 a[16:19], a[196:199], a[224:227], a[16:19]\n" \
"v_mfma_f32_16x16x32_bf16 a[32:35], a[136:139], a[160:163], a[32:35]\n" \
"ds_read_b64_tr_b16 a[182:183], v37 offset:1024\n" \
"v_mfma_f32_16x16x32_bf16 a[32:35], a[200:203], a[224:227], a[32:35]\n" \
"v_mfma_f32_16x16x32_bf16 a[36:39], a[136:139], a[164:167], a[36:39]\n" \
"v_mfma_f32_16x16x32_bf16 a[36:39], a[200:203], a[228:231], a[36:39]\n" \
"buffer_store_dwordx4 v[200:203], v20, s[80:83], s35 offen sc1\n" \
"v_mfma_f32_16x16x32_bf16 a[40:43], a[136:139], a[168:171], a[40:43]\n" \
"s_mov_b32 m0, s32\n" \
"ds_read_b64_tr_b16 a[244:245], v37 offset:8192\n" \
"v_mfma_f32_16x16x32_bf16 a[40:43], a[200:203], a[232:235], a[40:43]\n" \
"s_mov_b32 exec_lo, 0x0\n" \
"s_mov_b32 exec_hi, 0xffffffff\n" \
"global_load_lds_dword v[188:189], off\n" \
"s_mov_b64 exec, -1\n" \
"v_mfma_f32_16x16x32_bf16 a[44:47], a[136:139], a[172:175], a[44:47]\n" \
"ds_read_b64_tr_b16 a[246:247], v37 offset:9216\n" \
"v_mfma_f32_16x16x32_bf16 a[44:47], a[200:203], a[236:239], a[44:47]\n" \
"v_mfma_f32_16x16x32_bf16 a[60:63], a[140:143], a[172:175], a[60:63]\n" \
"v_mfma_f32_16x16x32_bf16 a[60:63], a[204:207], a[236:239], a[60:63]\n" \
"v_mfma_f32_16x16x32_bf16 a[56:59], a[140:143], a[168:171], a[56:59]\n" \
"ds_read_b64_tr_b16 a[184:185], v38 offset:0\n" \
"v_mfma_f32_16x16x32_bf16 a[56:59], a[204:207], a[232:235], a[56:59]\n" \
"v_mfma_f32_16x16x32_bf16 a[52:55], a[140:143], a[164:167], a[52:55]\n" \
"buffer_store_dwordx4 v[204:207], v20, s[80:83], s88 offen sc1\n" \
"v_mfma_f32_16x16x32_bf16 a[52:55], a[204:207], a[228:231], a[52:55]\n" \
"ds_read_b64_tr_b16 a[186:187], v38 offset:1024\n" \
"v_mfma_f32_16x16x32_bf16 a[48:51], a[140:143], a[160:163], a[48:51]\n" \
"v_mfma_f32_16x16x32_bf16 a[48:51], a[204:207], a[224:227], a[48:51]\n" \
"v_mfma_f32_16x16x32_bf16 a[64:67], a[144:147], a[160:163], a[64:67]\n" \
"ds_read_b64_tr_b16 a[248:249], v38 offset:8192\n" \
"v_mfma_f32_16x16x32_bf16 a[64:67], a[208:211], a[224:227], a[64:67]\n" \
"v_mfma_f32_16x16x32_bf16 a[68:71], a[144:147], a[164:167], a[68:71]\n" \
"v_mfma_f32_16x16x32_bf16 a[68:71], a[208:211], a[228:231], a[68:71]\n" \
"ds_read_b64_tr_b16 a[250:251], v38 offset:9216\n" \
"v_mfma_f32_16x16x32_bf16 a[72:75], a[144:147], a[168:171], a[72:75]\n" \
"v_mfma_f32_16x16x32_bf16 a[72:75], a[208:211], a[232:235], a[72:75]\n" \
"v_mfma_f32_16x16x32_bf16 a[76:79], a[144:147], a[172:175], a[76:79]\n" \
"buffer_store_dwordx4 v[208:211], v20, s[80:83], s36 offen sc1\n" \
"v_mfma_f32_16x16x32_bf16 a[76:79], a[208:211], a[236:239], a[76:79]\n" \
"ds_read_b64_tr_b16 a[188:189], v39 offset:0\n" \
"v_mfma_f32_16x16x32_bf16 a[92:95], a[148:151], a[172:175], a[92:95]\n" \
"v_mfma_f32_16x16x32_bf16 a[92:95], a[212:215], a[236:239], a[92:95]\n" \
"v_mfma_f32_16x16x32_bf16 a[88:91], a[148:151], a[168:171], a[88:91]\n" \
"ds_read_b64_tr_b16 a[190:191], v39 offset:1024\n" \
"v_mfma_f32_16x16x32_bf16 a[88:91], a[212:215], a[232:235], a[88:91]\n" \
"v_mfma_f32_16x16x32_bf16 a[84:87], a[148:151], a[164:167], a[84:87]\n" \
"v_mfma_f32_16x16x32_bf16 a[84:87], a[212:215], a[228:231], a[84:87]\n" \
"ds_read_b64_tr_b16 a[252:253], v39 offset:8192\n" \
"v_mfma_f32_16x16x32_bf16 a[80:83], a[148:151], a[160:163], a[80:83]\n" \
"v_mfma_f32_16x16x32_bf16 a[80:83], a[212:215], a[224:227], a[80:83]\n" \
"v_mfma_f32_16x16x32_bf16 a[96:99], a[152:155], a[160:163], a[96:99]\n" \
"ds_read_b64_tr_b16 a[254:255], v39 offset:9216\n" \
"v_mfma_f32_16x16x32_bf16 a[96:99], a[216:219], a[224:227], a[96:99]\n" \
"v_mfma_f32_16x16x32_bf16 a[100:103], a[152:155], a[164:167], a[100:103]\n" \
".fill (L_pb + 52960 - .) / 4, 4, 0xBF800000\n" \
"L_pp13:\n" \
"s_cbranch_execz L_pret\n" \
"v_mfma_f32_16x16x32_bf16 a[100:103], a[216:219], a[228:231], a[100:103]\n" \
"v_mfma_f32_16x16x32_bf16 a[104:107], a[152:155], a[168:171], a[104:107]\n" \
"v_mfma_f32_16x16x32_bf16 a[104:107], a[216:219], a[232:235], a[104:107]\n" \
"v_mfma_f32_16x16x32_bf16 a[108:111], a[152:155], a[172:175], a[108:111]\n" \
"v_mfma_f32_16x16x32_bf16 a[108:111], a[216:219], a[236:239], a[108:111]\n" \
"s_waitcnt lgkmcnt(15)\n" \
"v_mfma_f32_16x16x32_bf16 a[124:127], a[156:159], a[172:175], a[124:127]\n" \
"v_mfma_f32_16x16x32_bf16 a[124:127], a[220:223], a[236:239], a[124:127]\n" \
"v_mfma_f32_16x16x32_bf16 a[120:123], a[156:159], a[168:171], a[120:123]\n" \
"v_mfma_f32_16x16x32_bf16 a[120:123], a[220:223], a[232:235], a[120:123]\n" \
"v_mfma_f32_16x16x32_bf16 a[116:119], a[156:159], a[164:167], a[116:119]\n" \
"v_mfma_f32_16x16x32_bf16 a[116:119], a[220:223], a[228:231], a[116:119]\n" \
"v_mfma_f32_16x16x32_bf16 a[112:115], a[156:159], a[160:163], a[112:115]\n" \
"v_mfma_f32_16x16x32_bf16 a[112:115], a[220:223], a[224:227], a[112:115]\n" \
"s_waitcnt vmcnt(4) lgkmcnt(0)\n" \
"s_barrier\n" \
"v_mfma_f32_16x16x32_bf16 v[52:55], a[128:131], a[176:179], v[52:55]\n" \
"s_add_u32 m0, s30, 0x0\n" \
"s_movk_i32 s84, 0x300\n" \
"v_mfma_f32_16x16x32_bf16 v[52:55], a[192:195], a[240:243], v[52:55]\n" \
"buffer_load_dwordx4 v22, s[64:67], s84 offen offset:0 lds\n" \
"v_mfma_f32_16x16x32_bf16 v[56:59], a[128:131], a[180:183], v[56:59]\n" \
"ds_read_b64_tr_b16 a[160:161], v32 offset:32768\n" \
"v_mfma_f32_16x16x32_bf16 v[56:59], a[192:195], a[244:247], v[56:59]\n" \
"s_movk_i32 s84, 0x1f00\n" \
"s_nop 0\n" \
"v_mfma_f32_16x16x32_bf16 v[60:63], a[128:131], a[184:187], v[60:63]\n" \
"buffer_load_dwordx4 v23, s[64:67], s84 offen offset:1024 lds\n" \
"v_mfma_f32_16x16x32_bf16 v[60:63], a[192:195], a[248:251], v[60:63]\n" \
"ds_read_b64_tr_b16 a[162:163], v32 offset:33792\n" \
"v_mfma_f32_16x16x32_bf16 v[64:67], a[128:131], a[188:191], v[64:67]\n" \
"ds_read_b64_tr_b16 a[224:225], v32 offset:40960\n" \
"v_mfma_f32_16x16x32_bf16 v[64:67], a[192:195], a[252:255], v[64:67]\n" \
"ds_read_b128 a[128:131], v28 offset:49152\n" \
"v_mfma_f32_16x16x32_bf16 v[80:83], a[132:135], a[188:191], v[80:83]\n" \
"ds_read_b128 a[192:195], v29 offset:49152\n" \
"v_mfma_f32_16x16x32_bf16 v[80:83], a[196:199], a[252:255], v[80:83]\n" \
"s_movk_i32 s84, 0x3b00\n" \
"v_mfma_f32_16x16x32_bf16 v[76:79], a[132:135], a[184:187], v[76:79]\n" \
"buffer_load_dwordx4 v22, s[64:67], s84 offen offset:2048 lds\n" \
"v_mfma_f32_16x16x32_bf16 v[76:79], a[196:199], a[248:251], v[76:79]\n" \
"s_movk_i32 s84, 0x5700\n" \
"v_mfma_f32_16x16x32_bf16 v[72:75], a[132:135], a[180:183], v[72:75]\n" \
"buffer_load_dwordx4 v23, s[64:67], s84 offen offset:3072 lds\n" \
"v_mfma_f32_16x16x32_bf16 v[72:75], a[196:199], a[244:247], v[72:75]\n" \
"s_add_u32 m0, s31, 0x0\n" \
"s_mov_b32 s84, 0x300000\n" \
"v_mfma_f32_16x16x32_bf16 v[68:71], a[132:135], a[176:179], v[68:71]\n" \
"buffer_load_dwordx4 v26, s[68:71], s84 offen offset:0 lds\n" \
"v_mfma_f32_16x16x32_bf16 v[68:71], a[196:199], a[240:243], v[68:71]\n" \
"ds_read_b128 a[132:135], v28 offset:51200\n" \
"v_mfma_f32_16x16x32_bf16 v[84:87], a[136:139], a[176:179], v[84:87]\n" \
"ds_read_b128 a[196:199], v29 offset:51200\n" \
"v_mfma_f32_16x16x32_bf16 v[84:87], a[200:203], a[240:243], v[84:87]\n" \
"s_addk_i32 s84, 0x7c00\n" \
"v_mfma_f32_16x16x32_bf16 v[88:91], a[136:139], a[180:183], v[88:91]\n" \
"buffer_load_dwordx4 v26, s[68:71], s84 offen offset:1024 lds\n" \
"v_mfma_f32_16x16x32_bf16 v[88:91], a[200:203], a[244:247], v[88:91]\n" \
"ds_read_b64_tr_b16 a[226:227], v32 offset:41984\n" \
"v_mfma_f32_16x16x32_bf16 v[92:95], a[136:139], a[184:187], v[92:95]\n" \
"s_addk_i32 s84, 0x7c00\n" \
"v_mfma_f32_16x16x32_bf16 v[92:95], a[200:203], a[248:251], v[92:95]\n" \
"buffer_load_dwordx4 v27, s[68:71], s84 offen offset:2048 lds\n" \
"v_mfma_f32_16x16x32_bf16 v[96:99], a[136:139], a[188:191], v[96:99]\n" \
"ds_read_b64_tr_b16 a[164:165], v33 offset:32768\n" \
"v_mfma_f32_16x16x32_bf16 v[96:99], a[200:203], a[252:255], v[96:99]\n" \
"ds_read_b128 a[136:139], v28 offset:53248\n" \
"v_mfma_f32_16x16x32_bf16 v[112:115], a[140:143], a[188:191], v[112:115]\n" \
"ds_read_b128 a[200:203], v29 offset:53248\n" \
"v_mfma_f32_16x16x32_bf16 v[112:115], a[204:207], a[252:255], v[112:115]\n" \
"s_addk_i32 s84, 0x7c00\n" \
"v_mfma_f32_16x16x32_bf16 v[108:111], a[140:143], a[184:187], v[108:111]\n" \
"buffer_load_dwordx4 v27, s[68:71], s84 offen offset:3072 lds\n" \
"v_mfma_f32_16x16x32_bf16 v[108:111], a[204:207], a[248:251], v[108:111]\n" \
"s_add_u32 m0, s30, 0x1000\n" \
"s_mov_b32 s84, 0x8300\n" \
"v_mfma_f32_16x16x32_bf16 v[104:107], a[140:143], a[180:183], v[104:107]\n" \
"buffer_load_dwordx4 v22, s[64:67], s84 offen offset:0 lds\n" \
"v_mfma_f32_16x16x32_bf16 v[104:107], a[204:207], a[244:247], v[104:107]\n" \
"ds_read_b64_tr_b16 a[166:167], v33 offset:33792\n" \
"v_mfma_f32_16x16x32_bf16 v[100:103], a[140:143], a[176:179], v[100:103]\n" \
"ds_read_b64_tr_b16 a[228:229], v33 offset:40960\n" \
"v_mfma_f32_16x16x32_bf16 v[100:103], a[204:207], a[240:243], v[100:103]\n" \
"ds_read_b128 a[140:143], v28 offset:55296\n" \
"v_mfma_f32_16x16x32_bf16 v[116:119], a[144:147], a[176:179], v[116:119]\n" \
"ds_read_b128 a[204:207], v29 offset:55296\n" \
"v_mfma_f32_16x16x32_bf16 v[116:119], a[208:211], a[240:243], v[116:119]\n" \
"s_addk_i32 s84, 0x1c00\n" \
"v_mfma_f32_16x16x32_bf16 v[120:123], a[144:147], a[180:183], v[120:123]\n" \
"buffer_load_dwordx4 v23, s[64:67], s84 offen offset:1024 lds\n" \
"v_mfma_f32_16x16x32_bf16 v[120:123], a[208:211], a[244:247], v[120:123]\n" \
"s_addk_i32 s84, 0x1c00\n" \
"v_mfma_f32_16x16x32_bf16 v[124:127], a[144:147], a[184:187], v[124:127]\n" \
"buffer_load_dwordx4 v22, s[64:67], s84 offen offset:2048 lds\n" \
"v_mfma_f32_16x16x32_bf16 v[124:127], a[208:211], a[248:251], v[124:127]\n" \
"s_addk_i32 s84, 0x1c00\n" \
"s_nop 0\n" \
"v_mfma_f32_16x16x32_bf16 v[128:131], a[144:147], a[188:191], v[128:131]\n" \
"buffer_load_dwordx4 v23, s[64:67], s84 offen offset:3072 lds\n" \
"v_mfma_f32_16x16x32_bf16 v[128:131], a[208:211], a[252:255], v[128:131]\n" \
"ds_read_b128 a[144:147], v28 offset:32768\n" \
"v_mfma_f32_16x16x32_bf16 v[144:147], a[148:151], a[188:191], v[144:147]\n" \
"ds_read_b128 a[208:211], v29 offset:32768\n" \
"v_mfma_f32_16x16x32_bf16 v[144:147], a[212:215], a[252:255], v[144:147]\n" \
"s_add_u32 m0, s31, 0x4000\n" \
"s_mov_b32 s84, 0x300000\n" \
"v_mfma_f32_16x16x32_bf16 v[140:143], a[148:151], a[184:187], v[140:143]\n" \
"buffer_load_dwordx4 v26, s[72:75], s84 offen offset:0 lds\n" \
"v_mfma_f32_16x16x32_bf16 v[140:143], a[212:215], a[248:251], v[140:143]\n" \
"ds_read_b64_tr_b16 a[230:231], v33 offset:41984\n" \
"v_mfma_f32_16x16x32_bf16 v[136:139], a[148:151], a[180:183], v[136:139]\n" \
"s_addk_i32 s84, 0x7c00\n" \
"s_nop 0\n" \
"v_mfma_f32_16x16x32_bf16 v[136:139], a[212:215], a[244:247], v[136:139]\n" \
"buffer_load_dwordx4 v26, s[72:75], s84 offen offset:1024 lds\n" \
"v_mfma_f32_16x16x32_bf16 v[132:135], a[148:151], a[176:179], v[132:135]\n" \
"ds_read_b64_tr_b16 a[168:169], v34 offset:32768\n" \
"v_mfma_f32_16x16x32_bf16 v[132:135], a[212:215], a[240:243], v[132:135]\n" \
"ds_read_b128 a[148:151], v28 offset:34816\n" \
"v_mfma_f32_16x16x32_bf16 v[148:151], a[152:155], a[176:179], v[148:151]\n" \
"ds_read_b128 a[212:215], v29 offset:34816\n" \
"v_mfma_f32_16x16x32_bf16 v[148:151], a[216:219], a[240:243], v[148:151]\n" \
"ds_read_b64_tr_b16 a[170:171], v34 offset:33792\n" \
"v_mfma_f32_16x16x32_bf16 v[152:155], a[152:155], a[180:183], v[152:155]\n" \
"ds_read_b64_tr_b16 a[232:233], v34 offset:40960\n" \
"v_mfma_f32_16x16x32_bf16 v[152:155], a[216:219], a[244:247], v[152:155]\n" \
"ds_read_b64_tr_b16 a[234:235], v34 offset:41984\n" \
"v_mfma_f32_16x16x32_bf16 v[156:159], a[152:155], a[184:187], v[156:159]\n" \
"ds_read_b64_tr_b16 a[172:173], v35 offset:32768\n" \
"v_mfma_f32_16x16x32_bf16 v[156:159], a[216:219], a[248:251], v[156:159]\n" \
"ds_read_b64_tr_b16 a[174:175], v35 offset:33792\n" \
"v_mfma_f32_16x16x32_bf16 v[160:163], a[152:155], a[188:191], v[160:163]\n" \
"ds_read_b64_tr_b16 a[236:237], v35 offset:40960\n" \
"v_mfma_f32_16x16x32_bf16 v[160:163], a[216:219], a[252:255], v[160:163]\n" \
"ds_read_b128 a[152:155], v28 offset:36864\n" \
"v_mfma_f32_16x16x32_bf16 v[176:179], a[156:159], a[188:191], v[176:179]\n" \
"ds_read_b128 a[216:219], v29 offset:36864\n" \
"v_mfma_f32_16x16x32_bf16 v[176:179], a[220:223], a[252:255], v[176:179]\n" \
"ds_read_b64_tr_b16 a[238:239], v35 offset:41984\n" \
"v_mfma_f32_16x16x32_bf16 v[172:175], a[156:159], a[184:187], v[172:175]\n" \
"v_mfma_f32_16x16x32_bf16 v[172:175], a[220:223], a[248:251], v[172:175]\n" \
"v_mfma_f32_16x16x32_bf16 v[168:171], a[156:159], a[180:183], v[168:171]\n" \
"v_mfma_f32_16x16x32_bf16 v[168:171], a[220:223], a[244:247], v[168:171]\n" \
"v_mfma_f32_16x16x32_bf16 v[164:167], a[156:159], a[176:179], v[164:167]\n" \
"v_mfma_f32_16x16x32_bf16 v[164:167], a[220:223], a[240:243], v[164:167]\n" \
"ds_read_b128 a[156:159], v28 offset:38912\n" \
"ds_read_b128 a[220:223], v29 offset:38912\n" \
"s_waitcnt lgkmcnt(15)\n" \
"v_mfma_f32_16x16x32_bf16 a[0:3], a[128:131], a[160:163], a[0:3]\n" \
"s_addk_i32 s84, 0x7c00\n" \
"v_mfma_f32_16x16x32_bf16 a[0:3], a[192:195], a[224:227], a[0:3]\n" \
"buffer_load_dwordx4 v27, s[72:75], s84 offen offset:2048 lds\n" \
"v_mfma_f32_16x16x32_bf16 a[4:7], a[128:131], a[164:167], a[4:7]\n" \
"ds_read_b64_tr_b16 a[176:177], v36 offset:32768\n" \
"s_waitcnt lgkmcnt(15)\n" \
"v_mfma_f32_16x16x32_bf16 a[4:7], a[192:195], a[228:231], a[4:7]\n" \
"s_addk_i32 s84, 0x7c00\n" \
"s_waitcnt lgkmcnt(11)\n" \
"v_mfma_f32_16x16x32_bf16 a[8:11], a[128:131], a[168:171], a[8:11]\n" \
"buffer_load_dwordx4 v27, s[72:75], s84 offen offset:3072 lds\n" \
"s_waitcnt lgkmcnt(9)\n" \
"v_mfma_f32_16x16x32_bf16 a[8:11], a[192:195], a[232:235], a[8:11]\n" \
"ds_read_b64_tr_b16 a[178:179], v36 offset:33792\n" \
"s_waitcnt lgkmcnt(8)\n" \
"v_mfma_f32_16x16x32_bf16 a[12:15], a[128:131], a[172:175], a[12:15]\n" \
"ds_read_b64_tr_b16 a[240:241], v36 offset:40960\n" \
"s_waitcnt lgkmcnt(5)\n" \
"v_mfma_f32_16x16x32_bf16 a[12:15], a[192:195], a[236:239], a[12:15]\n" \
"v_mfma_f32_16x16x32_bf16 a[28:31], a[132:135], a[172:175], a[28:31]\n" \
"v_mfma_f32_16x16x32_bf16 a[28:31], a[196:199], a[236:239], a[28:31]\n" \
"ds_read_b64_tr_b16 a[242:243], v36 offset:41984\n" \
"v_mfma_f32_16x16x32_bf16 a[24:27], a[132:135], a[168:171], a[24:27]\n" \
"v_mfma_f32_16x16x32_bf16 a[24:27], a[196:199], a[232:235], a[24:27]\n" \
"v_mfma_f32_16x16x32_bf16 a[20:23], a[132:135], a[164:167], a[20:23]\n" \
"v_mfma_f32_16x16x32_bf16 a[20:23], a[196:199], a[228:231], a[20:23]\n" \
"ds_read_b64_tr_b16 a[180:181], v37 offset:32768\n" \
"v_mfma_f32_16x16x32_bf16 a[16:19], a[132:135], a[160:163], a[16:19]\n" \
"v_mfma_f32_16x16x32_bf16 a[16:19], a[196:199], a[224:227], a[16:19]\n" \
"v_mfma_f32_16x16x32_bf16 a[32:35], a[136:139], a[160:163], a[32:35]\n" \
"ds_read_b64_tr_b16 a[182:183], v37 offset:33792\n" \
"v_mfma_f32_16x16x32_bf16 a[32:35], a[200:203], a[224:227], a[32:35]\n" \
"v_mfma_f32_16x16x32_bf16 a[36:39], a[136:139], a[164:167], a[36:39]\n" \
"v_lshlrev_b32_e32 v45, 3, v20\n" \
"v_add_u32_e32 v45, s31, v45\n" \
"v_mfma_f32_16x16x32_bf16 a[36:39], a[200:203], a[228:231], a[36:39]\n" \
"s_getpc_b64 s[86:87]\n" \
"s_sub_u32 s86, s86, 0x10800\n" \
"s_subb_u32 s87, s87, 0\n" \
"s_mov_b32 m0, s32\n" \
"s_nop 0\n" \
"v_mfma_f32_16x16x32_bf16 a[40:43], a[136:139], a[168:171], a[40:43]\n" \
"global_load_lds_dword v45, s[86:87]\n" \
"v_mfma_f32_16x16x32_bf16 a[40:43], a[200:203], a[232:235], a[40:43]\n" \
"ds_read_b64_tr_b16 a[244:245], v37 offset:40960\n" \
"v_mfma_f32_16x16x32_bf16 a[44:47], a[136:139], a[172:175], a[44:47]\n" \
"ds_read_b64_tr_b16 a[246:247], v37 offset:41984\n" \
"v_mfma_f32_16x16x32_bf16 a[44:47], a[200:203], a[236:239], a[44:47]\n" \
"v_mfma_f32_16x16x32_bf16 a[60:63], a[140:143], a[172:175], a[60:63]\n" \
"v_mfma_f32_16x16x32_bf16 a[60:63], a[204:207], a[236:239], a[60:63]\n" \
"v_mfma_f32_16x16x32_bf16 a[56:59], a[140:143], a[168:171], a[56:59]\n" \
"ds_read_b64_tr_b16 a[184:185], v38 offset:32768\n" \
"v_mfma_f32_16x16x32_bf16 a[56:59], a[204:207], a[232:235], a[56:59]\n" \
"v_mfma_f32_16x16x32_bf16 a[52:55], a[140:143], a[164:167], a[52:55]\n" \
"v_mfma_f32_16x16x32_bf16 a[52:55], a[204:207], a[228:231], a[52:55]\n" \
"ds_read_b64_tr_b16 a[186:187], v38 offset:33792\n" \
"v_mfma_f32_16x16x32_bf16 a[48:51], a[140:143], a[160:163], a[48:51]\n" \
"v_mfma_f32_16x16x32_bf16 a[48:51], a[204:207], a[224:227], a[48:51]\n" \
"v_mfma_f32_16x16x32_bf16 a[64:67], a[144:147], a[160:163], a[64:67]\n" \
"ds_read_b64_tr_b16 a[248:249], v38 offset:40960\n" \
"v_mfma_f32_16x16x32_bf16 a[64:67], a[208:211], a[224:227], a[64:67]\n" \
"v_mfma_f32_16x16x32_bf16 a[68:71], a[144:147], a[164:167], a[68:71]\n" \
"v_mfma_f32_16x16x32_bf16 a[68:71], a[208:211], a[228:231], a[68:71]\n" \
"ds_read_b64_tr_b16 a[250:251], v38 offset:41984\n" \
"v_mfma_f32_16x16x32_bf16 a[72:75], a[144:147], a[168:171], a[72:75]\n" \
"v_mfma_f32_16x16x32_bf16 a[72:75], a[208:211], a[232:235], a[72:75]\n" \
"v_mfma_f32_16x16x32_bf16 a[76:79], a[144:147], a[172:175], a[76:79]\n" \
"v_mfma_f32_16x16x32_bf16 a[76:79], a[208:211], a[236:239], a[76:79]\n" \
"ds_read_b64_tr_b16 a[188:189], v39 offset:32768\n" \
"v_mfma_f32_16x16x32_bf16 a[92:95], a[148:151], a[172:175], a[92:95]\n" \
"v_mfma_f32_16x16x32_bf16 a[92:95], a[212:215], a[236:239], a[92:95]\n" \
"v_mfma_f32_16x16x32_bf16 a[88:91], a[148:151], a[168:171], a[88:91]\n" \
"ds_read_b64_tr_b16 a[190:191], v39 offset:33792\n" \
"v_mfma_f32_16x16x32_bf16 a[88:91], a[212:215], a[232:235], a[88:91]\n" \
"v_mfma_f32_16x16x32_bf16 a[84:87], a[148:151], a[164:167], a[84:87]\n" \
"v_mfma_f32_16x16x32_bf16 a[84:87], a[212:215], a[228:231], a[84:87]\n" \
"ds_read_b64_tr_b16 a[252:253], v39 offset:40960\n" \
"v_mfma_f32_16x16x32_bf16 a[80:83], a[148:151], a[160:163], a[80:83]\n" \
"v_mfma_f32_16x16x32_bf16 a[80:83], a[212:215], a[224:227], a[80:83]\n" \
"v_mfma_f32_16x16x32_bf16 a[96:99], a[152:155], a[160:163], a[96:99]\n" \
"ds_read_b64_tr_b16 a[254:255], v39 offset:41984\n" \
"v_mfma_f32_16x16x32_bf16 a[96:99], a[216:219], a[224:227], a[96:99]\n" \
"v_mfma_f32_16x16x32_bf16 a[100:103], a[152:155], a[164:167], a[100:103]\n" \
"v_mfma_f32_16x16x32_bf16 a[100:103], a[216:219], a[228:231], a[100:103]\n" \
"v_mfma_f32_16x16x32_bf16 a[104:107], a[152:155], a[168:171], a[104:107]\n" \
"v_mfma_f32_16x16x32_bf16 a[104:107], a[216:219], a[232:235], a[104:107]\n" \
"v_mfma_f32_16x16x32_bf16 a[108:111], a[152:155], a[172:175], a[108:111]\n" \
"v_mfma_f32_16x16x32_bf16 a[108:111], a[216:219], a[236:239], a[108:111]\n" \
"s_waitcnt lgkmcnt(15)\n" \
"v_mfma_f32_16x16x32_bf16 a[124:127], a[156:159], a[172:175], a[124:127]\n" \
"v_mfma_f32_16x16x32_bf16 a[124:127], a[220:223], a[236:239], a[124:127]\n" \
"v_mfma_f32_16x16x32_bf16 a[120:123], a[156:159], a[168:171], a[120:123]\n" \
"v_mfma_f32_16x16x32_bf16 a[120:123], a[220:223], a[232:235], a[120:123]\n" \
"v_mfma_f32_16x16x32_bf16 a[116:119], a[156:159], a[164:167], a[116:119]\n" \
"v_mfma_f32_16x16x32_bf16 a[116:119], a[220:223], a[228:231], a[116:119]\n" \
"v_mfma_f32_16x16x32_bf16 a[112:115], a[156:159], a[160:163], a[112:115]\n" \
"v_mfma_f32_16x16x32_bf16 a[112:115], a[220:223], a[224:227], a[112:115]\n" \
"s_waitcnt vmcnt(1) lgkmcnt(0)\n" \
"s_barrier\n" \
"v_mov_b32_e32 v45, s62\n" \
"s_mov_b64 exec, 1\n" \
"buffer_store_dword v45, off, s[80:83], s37 offset:128 sc1\n" \
"s_mov_b64 exec, -1\n" \
"v_mfma_f32_16x16x32_bf16 v[52:55], a[128:131], a[176:179], v[52:55]\n" \
"s_add_u32 m0, s30, 0x8000\n" \
"s_movk_i32 s84, 0x380\n" \
"v_mfma_f32_16x16x32_bf16 v[52:55], a[192:195], a[240:243], v[52:55]\n" \
"buffer_load_dwordx4 v22, s[64:67], s84 offen offset:0 lds\n" \
"v_mfma_f32_16x16x32_bf16 v[56:59], a[128:131], a[180:183], v[56:59]\n" \
"ds_read_b64_tr_b16 a[160:161], v32 offset:0\n" \
"v_mfma_f32_16x16x32_bf16 v[56:59], a[192:195], a[244:247], v[56:59]\n" \
"s_movk_i32 s84, 0x1f80\n" \
"v_mfma_f32_16x16x32_bf16 v[60:63], a[128:131], a[184:187], v[60:63]\n" \
"buffer_load_dwordx4 v23, s[64:67], s84 offen offset:1024 lds\n" \
"v_mfma_f32_16x16x32_bf16 v[60:63], a[192:195], a[248:251], v[60:63]\n" \
"ds_read_b64_tr_b16 a[162:163], v32 offset:1024\n" \
"v_mfma_f32_16x16x32_bf16 v[64:67], a[128:131], a[188:191], v[64:67]\n" \
"ds_read_b64_tr_b16 a[224:225], v32 offset:8192\n" \
"v_mfma_f32_16x16x32_bf16 v[64:67], a[192:195], a[252:255], v[64:67]\n" \
"ds_read_b128 a[128:131], v28 offset:16384\n" \
"v_mfma_f32_16x16x32_bf16 v[80:83], a[132:135], a[188:191], v[80:83]\n" \
"ds_read_b128 a[192:195], v29 offset:16384\n" \
"v_mfma_f32_16x16x32_bf16 v[80:83], a[196:199], a[252:255], v[80:83]\n" \
"s_movk_i32 s84, 0x3b80\n" \
"v_mfma_f32_16x16x32_bf16 v[76:79], a[132:135], a[184:187], v[76:79]\n" \
"buffer_load_dwordx4 v22, s[64:67], s84 offen offset:2048 lds\n" \
"v_mfma_f32_16x16x32_bf16 v[76:79], a[196:199], a[248:251], v[76:79]\n" \
"s_movk_i32 s84, 0x5780\n" \
"v_mfma_f32_16x16x32_bf16 v[72:75], a[132:135], a[180:183], v[72:75]\n" \
"buffer_load_dwordx4 v23, s[64:67], s84 offen offset:3072 lds\n" \
"v_mfma_f32_16x16x32_bf16 v[72:75], a[196:199], a[244:247], v[72:75]\n" \
"s_add_u32 m0, s31, 0x8000\n" \
"s_mov_b32 s84, 0x380000\n" \
"v_mfma_f32_16x16x32_bf16 v[68:71], a[132:135], a[176:179], v[68:71]\n" \
"buffer_load_dwordx4 v26, s[68:71], s84 offen offset:0 lds\n" \
"v_mfma_f32_16x16x32_bf16 v[68:71], a[196:199], a[240:243], v[68:71]\n" \
"ds_read_b128 a[132:135], v28 offset:18432\n" \
"v_mfma_f32_16x16x32_bf16 v[84:87], a[136:139], a[176:179], v[84:87]\n" \
"ds_read_b128 a[196:199], v29 offset:18432\n" \
"v_mfma_f32_16x16x32_bf16 v[84:87], a[200:203], a[240:243], v[84:87]\n" \
"s_addk_i32 s84, 0x7c00\n" \
"v_mfma_f32_16x16x32_bf16 v[88:91], a[136:139], a[180:183], v[88:91]\n" \
"buffer_load_dwordx4 v26, s[68:71], s84 offen offset:1024 lds\n" \
"v_mfma_f32_16x16x32_bf16 v[88:91], a[200:203], a[244:247], v[88:91]\n" \
"ds_read_b64_tr_b16 a[226:227], v32 offset:9216\n" \
"v_mfma_f32_16x16x32_bf16 v[92:95], a[136:139], a[184:187], v[92:95]\n" \
"s_addk_i32 s84, 0x7c00\n" \
"v_mfma_f32_16x16x32_bf16 v[92:95], a[200:203], a[248:251], v[92:95]\n" \
"buffer_load_dwordx4 v27, s[68:71], s84 offen offset:2048 lds\n" \
"v_mfma_f32_16x16x32_bf16 v[96:99], a[136:139], a[188:191], v[96:99]\n" \
"ds_read_b64_tr_b16 a[164:165], v33 offset:0\n" \
"v_mfma_f32_16x16x32_bf16 v[96:99], a[200:203], a[252:255], v[96:99]\n" \
"ds_read_b128 a[136:139], v28 offset:20480\n" \
"v_mfma_f32_16x16x32_bf16 v[112:115], a[140:143], a[188:191], v[112:115]\n" \
"ds_read_b128 a[200:203], v29 offset:20480\n" \
"v_mfma_f32_16x16x32_bf16 v[112:115], a[204:207], a[252:255], v[112:115]\n" \
"s_addk_i32 s84, 0x7c00\n" \
"s_nop 0\n" \
"v_mfma_f32_16x16x32_bf16 v[108:111], a[140:143], a[184:187], v[108:111]\n" \
"buffer_load_dwordx4 v27, s[68:71], s84 offen offset:3072 lds\n" \
"v_mfma_f32_16x16x32_bf16 v[108:111], a[204:207], a[248:251], v[108:111]\n" \
"s_add_u32 m0, s30, 0x9000\n" \
"s_mov_b32 s84, 0x8380\n" \
"v_mfma_f32_16x16x32_bf16 v[104:107], a[140:143], a[180:183], v[104:107]\n" \
"buffer_load_dwordx4 v22, s[64:67], s84 offen offset:0 lds\n" \
"v_mfma_f32_16x16x32_bf16 v[104:107], a[204:207], a[244:247], v[104:107]\n" \
"ds_read_b64_tr_b16 a[166:167], v33 offset:1024\n" \
"v_mfma_f32_16x16x32_bf16 v[100:103], a[140:143], a[176:179], v[100:103]\n" \
"ds_read_b64_tr_b16 a[228:229], v33 offset:8192\n" \
"v_mfma_f32_16x16x32_bf16 v[100:103], a[204:207], a[240:243], v[100:103]\n" \
"ds_read_b128 a[140:143], v28 offset:22528\n" \
"v_mfma_f32_16x16x32_bf16 v[116:119], a[144:147], a[176:179], v[116:119]\n" \
"ds_read_b128 a[204:207], v29 offset:22528\n" \
"v_mfma_f32_16x16x32_bf16 v[116:119], a[208:211], a[240:243], v[116:119]\n" \
"s_addk_i32 s84, 0x1c00\n" \
"v_mfma_f32_16x16x32_bf16 v[120:123], a[144:147], a[180:183], v[120:123]\n" \
"buffer_load_dwordx4 v23, s[64:67], s84 offen offset:1024 lds\n" \
"v_mfma_f32_16x16x32_bf16 v[120:123], a[208:211], a[244:247], v[120:123]\n" \
"s_addk_i32 s84, 0x1c00\n" \
"v_mfma_f32_16x16x32_bf16 v[124:127], a[144:147], a[184:187], v[124:127]\n" \
"buffer_load_dwordx4 v22, s[64:67], s84 offen offset:2048 lds\n" \
"v_mfma_f32_16x16x32_bf16 v[124:127], a[208:211], a[248:251], v[124:127]\n" \
"s_addk_i32 s84, 0x1c00\n" \
"s_nop 0\n" \
"v_mfma_f32_16x16x32_bf16 v[128:131], a[144:147], a[188:191], v[128:131]\n" \
"buffer_load_dwordx4 v23, s[64:67], s84 offen offset:3072 lds\n" \
"v_mfma_f32_16x16x32_bf16 v[128:131], a[208:211], a[252:255], v[128:131]\n" \
"ds_read_b128 a[144:147], v28 offset:0\n" \
"v_mfma_f32_16x16x32_bf16 v[144:147], a[148:151], a[188:191], v[144:147]\n" \
"ds_read_b128 a[208:211], v29 offset:0\n" \
"v_mfma_f32_16x16x32_bf16 v[144:147], a[212:215], a[252:255], v[144:147]\n" \
"s_add_u32 m0, s31, 0xc000\n" \
"s_mov_b32 s84, 0x380000\n" \
"v_mfma_f32_16x16x32_bf16 v[140:143], a[148:151], a[184:187], v[140:143]\n" \
"buffer_load_dwordx4 v26, s[72:75], s84 offen offset:0 lds\n" \
"v_mfma_f32_16x16x32_bf16 v[140:143], a[212:215], a[248:251], v[140:143]\n" \
"ds_read_b64_tr_b16 a[230:231], v33 offset:9216\n" \
"v_mfma_f32_16x16x32_bf16 v[136:139], a[148:151], a[180:183], v[136:139]\n" \
"s_addk_i32 s84, 0x7c00\n" \
"s_nop 0\n" \
"v_mfma_f32_16x16x32_bf16 v[136:139], a[212:215], a[244:247], v[136:139]\n" \
"buffer_load_dwordx4 v26, s[72:75], s84 offen offset:1024 lds\n" \
"v_mfma_f32_16x16x32_bf16 v[132:135], a[148:151], a[176:179], v[132:135]\n" \
"ds_read_b64_tr_b16 a[168:169], v34 offset:0\n" \
"v_mfma_f32_16x16x32_bf16 v[132:135], a[212:215], a[240:243], v[132:135]\n" \
"ds_read_b128 a[148:151], v28 offset:2048\n" \
"v_mfma_f32_16x16x32_bf16 v[148:151], a[152:155], a[176:179], v[148:151]\n" \
"ds_read_b128 a[212:215], v29 offset:2048\n" \
"v_mfma_f32_16x16x32_bf16 v[148:151], a[216:219], a[240:243], v[148:151]\n" \
"ds_read_b64_tr_b16 a[170:171], v34 offset:1024\n" \
"v_mfma_f32_16x16x32_bf16 v[152:155], a[152:155], a[180:183], v[152:155]\n" \
"ds_read_b64_tr_b16 a[232:233], v34 offset:8192\n" \
"v_mfma_f32_16x16x32_bf16 v[152:155], a[216:219], a[244:247], v[152:155]\n" \
"ds_read_b64_tr_b16 a[234:235], v34 offset:9216\n" \
"v_mfma_f32_16x16x32_bf16 v[156:159], a[152:155], a[184:187], v[156:159]\n" \
"ds_read_b64_tr_b16 a[172:173], v35 offset:0\n" \
"v_mfma_f32_16x16x32_bf16 v[156:159], a[216:219], a[248:251], v[156:159]\n" \
"ds_read_b64_tr_b16 a[174:175], v35 offset:1024\n" \
"v_mfma_f32_16x16x32_bf16 v[160:163], a[152:155], a[188:191], v[160:163]\n" \
"ds_read_b64_tr_b16 a[236:237], v35 offset:8192\n" \
"v_mfma_f32_16x16x32_bf16 v[160:163], a[216:219], a[252:255], v[160:163]\n" \
"ds_read_b128 a[152:155], v28 offset:4096\n" \
"v_mfma_f32_16x16x32_bf16 v[176:179], a[156:159], a[188:191], v[176:179]\n" \
"ds_read_b128 a[216:219], v29 offset:4096\n" \
"v_mfma_f32_16x16x32_bf16 v[176:179], a[220:223], a[252:255], v[176:179]\n" \
"ds_read_b64_tr_b16 a[238:239], v35 offset:9216\n" \
"v_mfma_f32_16x16x32_bf16 v[172:175], a[156:159], a[184:187], v[172:175]\n" \
"v_mfma_f32_16x16x32_bf16 v[172:175], a[220:223], a[248:251], v[172:175]\n" \
"v_mfma_f32_16x16x32_bf16 v[168:171], a[156:159], a[180:183], v[168:171]\n" \
"v_mfma_f32_16x16x32_bf16 v[168:171], a[220:223], a[244:247], v[168:171]\n" \
"v_mfma_f32_16x16x32_bf16 v[164:167], a[156:159], a[176:179], v[164:167]\n" \
"v_mfma_f32_16x16x32_bf16 v[164:167], a[220:223], a[240:243], v[164:167]\n" \
"ds_read_b128 a[156:159], v28 offset:6144\n" \
"ds_read_b128 a[220:223], v29 offset:6144\n" \
"s_waitcnt lgkmcnt(15)\n" \
"v_mfma_f32_16x16x32_bf16 a[0:3], a[128:131], a[160:163], a[0:3]\n" \
"s_addk_i32 s84, 0x7c00\n" \
"v_mfma_f32_16x16x32_bf16 a[0:3], a[192:195], a[224:227], a[0:3]\n" \
"buffer_load_dwordx4 v27, s[72:75], s84 offen offset:2048 lds\n" \
"v_mfma_f32_16x16x32_bf16 a[4:7], a[128:131], a[164:167], a[4:7]\n" \
"ds_read_b64_tr_b16 a[176:177], v36 offset:0\n" \
"s_waitcnt lgkmcnt(15)\n" \
"v_mfma_f32_16x16x32_bf16 a[4:7], a[192:195], a[228:231], a[4:7]\n" \
"s_addk_i32 s84, 0x7c00\n" \
"s_waitcnt lgkmcnt(11)\n" \
"v_mfma_f32_16x16x32_bf16 a[8:11], a[128:131], a[168:171], a[8:11]\n" \
"buffer_load_dwordx4 v27, s[72:75], s84 offen offset:3072 lds\n" \
"s_waitcnt lgkmcnt(9)\n" \
"v_mfma_f32_16x16x32_bf16 a[8:11], a[192:195], a[232:235], a[8:11]\n" \
"ds_read_b64_tr_b16 a[178:179], v36 offset:1024\n" \
"s_waitcnt lgkmcnt(8)\n" \
"v_mfma_f32_16x16x32_bf16 a[12:15], a[128:131], a[172:175], a[12:15]\n" \
"ds_read_b64_tr_b16 a[240:241], v36 offset:8192\n" \
"s_waitcnt lgkmcnt(5)\n" \
"v_mfma_f32_16x16x32_bf16 a[12:15], a[192:195], a[236:239], a[12:15]\n" \
"v_mfma_f32_16x16x32_bf16 a[28:31], a[132:135], a[172:175], a[28:31]\n" \
"v_mfma_f32_16x16x32_bf16 a[28:31], a[196:199], a[236:239], a[28:31]\n" \
"ds_read_b64_tr_b16 a[242:243], v36 offset:9216\n" \
"v_mfma_f32_16x16x32_bf16 a[24:27], a[132:135], a[168:171], a[24:27]\n" \
"v_mfma_f32_16x16x32_bf16 a[24:27], a[196:199], a[232:235], a[24:27]\n" \
"v_mfma_f32_16x16x32_bf16 a[20:23], a[132:135], a[164:167], a[20:23]\n" \
"v_mfma_f32_16x16x32_bf16 a[20:23], a[196:199], a[228:231], a[20:23]\n" \
"ds_read_b64_tr_b16 a[180:181], v37 offset:0\n" \
"v_mfma_f32_16x16x32_bf16 a[16:19], a[132:135], a[160:163], a[16:19]\n" \
"v_mfma_f32_16x16x32_bf16 a[16:19], a[196:199], a[224:227], a[16:19]\n" \
"v_mfma_f32_16x16x32_bf16 a[32:35], a[136:139], a[160:163], a[32:35]\n" \
"ds_read_b64_tr_b16 a[182:183], v37 offset:1024\n" \
"v_mfma_f32_16x16x32_bf16 a[32:35], a[200:203], a[224:227], a[32:35]\n" \
"v_mfma_f32_16x16x32_bf16 a[36:39], a[136:139], a[164:167], a[36:39]\n" \
"v_mfma_f32_16x16x32_bf16 a[36:39], a[200:203], a[228:231], a[36:39]\n" \
"ds_read_b64_tr_b16 a[244:245], v37 offset:8192\n" \
"v_mfma_f32_16x16x32_bf16 a[40:43], a[136:139], a[168:171], a[40:43]\n" \
"v_mfma_f32_16x16x32_bf16 a[40:43], a[200:203], a[232:235], a[40:43]\n" \
"v_mfma_f32_16x16x32_bf16 a[44:47], a[136:139], a[172:175], a[44:47]\n" \
"ds_read_b64_tr_b16 a[246:247], v37 offset:9216\n" \
"v_mfma_f32_16x16x32_bf16 a[44:47], a[200:203], a[236:239], a[44:47]\n" \
"v_mfma_f32_16x16x32_bf16 a[60:63], a[140:143], a[172:175], a[60:63]\n" \
"v_mfma_f32_16x16x32_bf16 a[60:63], a[204:207], a[236:239], a[60:63]\n" \
"v_mfma_f32_16x16x32_bf16 a[56:59], a[140:143], a[168:171], a[56:59]\n" \
"ds_read_b64_tr_b16 a[184:185], v38 offset:0\n" \
"v_mfma_f32_16x16x32_bf16 a[56:59], a[204:207], a[232:235], a[56:59]\n" \
"v_mfma_f32_16x16x32_bf16 a[52:55], a[140:143], a[164:167], a[52:55]\n" \
"v_mfma_f32_16x16x32_bf16 a[52:55], a[204:207], a[228:231], a[52:55]\n" \
"ds_read_b64_tr_b16 a[186:187], v38 offset:1024\n" \
"v_mfma_f32_16x16x32_bf16 a[48:51], a[140:143], a[160:163], a[48:51]\n" \
"v_mfma_f32_16x16x32_bf16 a[48:51], a[204:207], a[224:227], a[48:51]\n" \
"v_mfma_f32_16x16x32_bf16 a[64:67], a[144:147], a[160:163], a[64:67]\n" \
"v_bfe_u32 v44, v20, 2, 5\n" \
"ds_read_b64_tr_b16 a[248:249], v38 offset:8192\n" \
"v_mfma_f32_16x16x32_bf16 a[64:67], a[208:211], a[224:227], a[64:67]\n" \
"buffer_load_dword v44, v44, s[80:83], s38 offen offset:128 sc1\n" \
"v_mfma_f32_16x16x32_bf16 a[68:71], a[144:147], a[164:167], a[68:71]\n" \
"v_mfma_f32_16x16x32_bf16 a[68:71], a[208:211], a[228:231], a[68:71]\n" \
"ds_read_b64_tr_b16 a[250:251], v38 offset:9216\n" \
"v_mfma_f32_16x16x32_bf16 a[72:75], a[144:147], a[168:171], a[72:75]\n" \
"v_mfma_f32_16x16x32_bf16 a[72:75], a[208:211], a[232:235], a[72:75]\n" \
"v_mfma_f32_16x16x32_bf16 a[76:79], a[144:147], a[172:175], a[76:79]\n" \
"v_mfma_f32_16x16x32_bf16 a[76:79], a[208:211], a[236:239], a[76:79]\n" \
"ds_read_b64_tr_b16 a[188:189], v39 offset:0\n" \
"v_mfma_f32_16x16x32_bf16 a[92:95], a[148:151], a[172:175], a[92:95]\n" \
"v_mfma_f32_16x16x32_bf16 a[92:95], a[212:215], a[236:239], a[92:95]\n" \
"v_mfma_f32_16x16x32_bf16 a[88:91], a[148:151], a[168:171], a[88:91]\n" \
"ds_read_b64_tr_b16 a[190:191], v39 offset:1024\n" \
"v_mfma_f32_16x16x32_bf16 a[88:91], a[212:215], a[232:235], a[88:91]\n" \
"v_mfma_f32_16x16x32_bf16 a[84:87], a[148:151], a[164:167], a[84:87]\n" \
"v_mfma_f32_16x16x32_bf16 a[84:87], a[212:215], a[228:231], a[84:87]\n" \
"ds_read_b64_tr_b16 a[252:253], v39 offset:8192\n" \
"v_mfma_f32_16x16x32_bf16 a[80:83], a[148:151], a[160:163], a[80:83]\n" \
"v_mfma_f32_16x16x32_bf16 a[80:83], a[212:215], a[224:227], a[80:83]\n" \
"v_mfma_f32_16x16x32_bf16 a[96:99], a[152:155], a[160:163], a[96:99]\n" \
"ds_read_b64_tr_b16 a[254:255], v39 offset:9216\n" \
"v_mfma_f32_16x16x32_bf16 a[96:99], a[216:219], a[224:227], a[96:99]\n" \
"v_mfma_f32_16x16x32_bf16 a[100:103], a[152:155], a[164:167], a[100:103]\n" \
"v_mfma_f32_16x16x32_bf16 a[100:103], a[216:219], a[228:231], a[100:103]\n" \
"v_mfma_f32_16x16x32_bf16 a[104:107], a[152:155], a[168:171], a[104:107]\n" \
"v_mfma_f32_16x16x32_bf16 a[104:107], a[216:219], a[232:235], a[104:107]\n" \
"v_mfma_f32_16x16x32_bf16 a[108:111], a[152:155], a[172:175], a[108:111]\n" \
"v_mfma_f32_16x16x32_bf16 a[108:111], a[216:219], a[236:239], a[108:111]\n" \
"s_waitcnt lgkmcnt(15)\n" \
"v_mfma_f32_16x16x32_bf16 a[124:127], a[156:159], a[172:175], a[124:127]\n" \
"v_mfma_f32_16x16x32_bf16 a[124:127], a[220:223], a[236:239], a[124:127]\n" \
"v_mfma_f32_16x16x32_bf16 a[120:123], a[156:159], a[168:171], a[120:123]\n" \
"v_mfma_f32_16x16x32_bf16 a[120:123], a[220:223], a[232:235], a[120:123]\n" \
"v_mfma_f32_16x16x32_bf16 a[116:119], a[156:159], a[164:167], a[116:119]\n" \
"v_mfma_f32_16x16x32_bf16 a[116:119], a[220:223], a[228:231], a[116:119]\n" \
"v_mfma_f32_16x16x32_bf16 a[112:115], a[156:159], a[160:163], a[112:115]\n" \
"v_mfma_f32_16x16x32_bf16 a[112:115], a[220:223], a[224:227], a[112:115]\n" \
"s_waitcnt vmcnt(0) lgkmcnt(0)\n" \
"s_barrier\n" \
"v_cmp_ne_u32_e32 vcc, s62, v44\n" \
"s_nop 4\n" \
"s_cbranch_vccnz L_poll61_%=\n" \
"L_pollr61_%=:\n" \
"s_mov_b32 s68, s12\n" \
"s_and_b32 s69, s13, 0xffff\n" \
"s_mov_b32 s70, 0x480000\n" \
"s_mov_b32 s71, 0x20000\n" \
"v_mfma_f32_16x16x32_bf16 v[52:55], a[128:131], a[176:179], v[52:55]\n" \
"s_add_u32 m0, s40, 0x0\n" \
"s_add_u32 s85, s39, 0x0\n" \
"s_nop 0\n" \
"v_mfma_f32_16x16x32_bf16 v[52:55], a[192:195], a[240:243], v[52:55]\n" \
"buffer_load_dwordx4 v20, s[80:83], s85 offen sc1 lds\n" \
"v_mfma_f32_16x16x32_bf16 v[56:59], a[128:131], a[180:183], v[56:59]\n" \
"ds_read_b64_tr_b16 a[160:161], v32 offset:32768\n" \
"v_mfma_f32_16x16x32_bf16 v[56:59], a[192:195], a[244:247], v[56:59]\n" \
"s_add_u32 m0, s40, 0x1000\n" \
"s_add_u32 s85, s39, 0x1000\n" \
"v_mfma_f32_16x16x32_bf16 v[60:63], a[128:131], a[184:187], v[60:63]\n" \
"buffer_load_dwordx4 v20, s[80:83], s85 offen sc1 lds\n" \
"v_mfma_f32_16x16x32_bf16 v[60:63], a[192:195], a[248:251], v[60:63]\n" \
"ds_read_b64_tr_b16 a[162:163], v32 offset:33792\n" \
"v_mfma_f32_16x16x32_bf16 v[64:67], a[128:131], a[188:191], v[64:67]\n" \
"ds_read_b64_tr_b16 a[224:225], v32 offset:40960\n" \
"v_mfma_f32_16x16x32_bf16 v[64:67], a[192:195], a[252:255], v[64:67]\n" \
"ds_read_b128 a[128:131], v28 offset:49152\n" \
"v_mfma_f32_16x16x32_bf16 v[80:83], a[132:135], a[188:191], v[80:83]\n" \
"ds_read_b128 a[192:195], v29 offset:49152\n" \
"v_mfma_f32_16x16x32_bf16 v[80:83], a[196:199], a[252:255], v[80:83]\n" \
"s_add_u32 m0, s40, 0x3000\n" \
"s_movk_i32 s84, 0x0\n" \
"v_mfma_f32_16x16x32_bf16 v[76:79], a[132:135], a[184:187], v[76:79]\n" \
"buffer_load_dwordx4 v21, s[76:79], s84 offen offset:0 lds\n" \
"v_mfma_f32_16x16x32_bf16 v[76:79], a[196:199], a[248:251], v[76:79]\n" \
"s_add_u32 m0, s30, 0x0\n" \
"s_movk_i32 s84, 0x0\n" \
"s_nop 0\n" \
"v_mfma_f32_16x16x32_bf16 v[72:75], a[132:135], a[180:183], v[72:75]\n" \
"buffer_load_dwordx4 v24, s[76:79], s84 offen offset:0 lds\n" \
"v_mfma_f32_16x16x32_bf16 v[72:75], a[196:199], a[244:247], v[72:75]\n" \
"ds_read_b64_tr_b16 a[226:227], v32 offset:41984\n" \
"v_mfma_f32_16x16x32_bf16 v[68:71], a[132:135], a[176:179], v[68:71]\n" \
"ds_read_b64_tr_b16 a[164:165], v33 offset:32768\n" \
"v_mfma_f32_16x16x32_bf16 v[68:71], a[196:199], a[240:243], v[68:71]\n" \
"ds_read_b128 a[132:135], v28 offset:51200\n" \
"v_mfma_f32_16x16x32_bf16 v[84:87], a[136:139], a[176:179], v[84:87]\n" \
"ds_read_b128 a[196:199], v29 offset:51200\n" \
"v_mfma_f32_16x16x32_bf16 v[84:87], a[200:203], a[240:243], v[84:87]\n" \
"s_movk_i32 s84, 0x2000\n" \
"v_mfma_f32_16x16x32_bf16 v[88:91], a[136:139], a[180:183], v[88:91]\n" \
"buffer_load_dwordx4 v25, s[76:79], s84 offen offset:1024 lds\n" \
"v_mfma_f32_16x16x32_bf16 v[88:91], a[200:203], a[244:247], v[88:91]\n" \
"s_movk_i32 s84, 0x4000\n" \
"v_mfma_f32_16x16x32_bf16 v[92:95], a[136:139], a[184:187], v[92:95]\n" \
"buffer_load_dwordx4 v24, s[76:79], s84 offen offset:2048 lds\n" \
"v_mfma_f32_16x16x32_bf16 v[92:95], a[200:203], a[248:251], v[92:95]\n" \
"ds_read_b64_tr_b16 a[166:167], v33 offset:33792\n" \
"v_mfma_f32_16x16x32_bf16 v[96:99], a[136:139], a[188:191], v[96:99]\n" \
"ds_read_b64_tr_b16 a[228:229], v33 offset:40960\n" \
"v_mfma_f32_16x16x32_bf16 v[96:99], a[200:203], a[252:255], v[96:99]\n" \
"ds_read_b128 a[136:139], v28 offset:53248\n" \
"v_mfma_f32_16x16x32_bf16 v[112:115], a[140:143], a[188:191], v[112:115]\n" \
"ds_read_b128 a[200:203], v29 offset:53248\n" \
"v_mfma_f32_16x16x32_bf16 v[112:115], a[204:207], a[252:255], v[112:115]\n" \
"s_movk_i32 s84, 0x6000\n" \
"v_mfma_f32_16x16x32_bf16 v[108:111], a[140:143], a[184:187], v[108:111]\n" \
"buffer_load_dwordx4 v25, s[76:79], s84 offen offset:3072 lds\n" \
"v_mfma_f32_16x16x32_bf16 v[108:111], a[204:207], a[248:251], v[108:111]\n" \
"s_add_u32 m0, s31, 0x0\n" \
"s_movk_i32 s84, 0x0\n" \
"v_mfma_f32_16x16x32_bf16 v[104:107], a[140:143], a[180:183], v[104:107]\n" \
"buffer_load_dwordx4 v26, s[68:71], s84 offen offset:0 lds\n" \
"v_mfma_f32_16x16x32_bf16 v[104:107], a[204:207], a[244:247], v[104:107]\n" \
"s_movk_i32 s84, 0x7c00\n" \
".fill (L_pb + 57056 - .) / 4, 4, 0xBF800000\n" \
"L_pp14:\n" \
"s_cbranch_execz L_pret\n" \
"v_mfma_f32_16x16x32_bf16 v[100:103], a[140:143], a[176:179], v[100:103]\n" \
"buffer_load_dwordx4 v26, s[68:71], s84 offen offset:1024 lds\n" \
"v_mfma_f32_16x16x32_bf16 v[100:103], a[204:207], a[240:243], v[100:103]\n" \
"ds_read_b128 a[140:143], v28 offset:55296\n" \
"v_mfma_f32_16x16x32_bf16 v[116:119], a[144:147], a[176:179], v[116:119]\n" \
"ds_read_b128 a[204:207], v29 offset:55296\n" \
"v_mfma_f32_16x16x32_bf16 v[116:119], a[208:211], a[240:243], v[116:119]\n" \
"s_addk_i32 s84, 0x7c00\n" \
"v_mfma_f32_16x16x32_bf16 v[120:123], a[144:147], a[180:183], v[120:123]\n" \
"buffer_load_dwordx4 v27, s[68:71], s84 offen offset:2048 lds\n" \
"v_mfma_f32_16x16x32_bf16 v[120:123], a[208:211], a[244:247], v[120:123]\n" \
"ds_read_b64_tr_b16 a[230:231], v33 offset:41984\n" \
"v_mfma_f32_16x16x32_bf16 v[124:127], a[144:147], a[184:187], v[124:127]\n" \
"s_addk_i32 s84, 0x7c00\n" \
"s_nop 0\n" \
"v_mfma_f32_16x16x32_bf16 v[124:127], a[208:211], a[248:251], v[124:127]\n" \
"buffer_load_dwordx4 v27, s[68:71], s84 offen offset:3072 lds\n" \
"v_mfma_f32_16x16x32_bf16 v[128:131], a[144:147], a[188:191], v[128:131]\n" \
"ds_read_b64_tr_b16 a[168:169], v34 offset:32768\n" \
"v_mfma_f32_16x16x32_bf16 v[128:131], a[208:211], a[252:255], v[128:131]\n" \
"ds_read_b128 a[144:147], v28 offset:32768\n" \
"v_mfma_f32_16x16x32_bf16 v[144:147], a[148:151], a[188:191], v[144:147]\n" \
"ds_read_b128 a[208:211], v29 offset:32768\n" \
"v_mfma_f32_16x16x32_bf16 v[144:147], a[212:215], a[252:255], v[144:147]\n" \
"s_add_u32 m0, s30, 0x1000\n" \
"s_mov_b32 s84, 0x9000\n" \
"v_mfma_f32_16x16x32_bf16 v[140:143], a[148:151], a[184:187], v[140:143]\n" \
"buffer_load_dwordx4 v24, s[76:79], s84 offen offset:0 lds\n" \
"v_mfma_f32_16x16x32_bf16 v[140:143], a[212:215], a[248:251], v[140:143]\n" \
"ds_read_b64_tr_b16 a[170:171], v34 offset:33792\n" \
"v_mfma_f32_16x16x32_bf16 v[136:139], a[148:151], a[180:183], v[136:139]\n" \
"s_addk_i32 s84, 0x2000\n" \
"s_nop 0\n" \
"v_mfma_f32_16x16x32_bf16 v[136:139], a[212:215], a[244:247], v[136:139]\n" \
"buffer_load_dwordx4 v25, s[76:79], s84 offen offset:1024 lds\n" \
"v_mfma_f32_16x16x32_bf16 v[132:135], a[148:151], a[176:179], v[132:135]\n" \
"ds_read_b64_tr_b16 a[232:233], v34 offset:40960\n" \
"v_mfma_f32_16x16x32_bf16 v[132:135], a[212:215], a[240:243], v[132:135]\n" \
"ds_read_b128 a[148:151], v28 offset:34816\n" \
"v_mfma_f32_16x16x32_bf16 v[148:151], a[152:155], a[176:179], v[148:151]\n" \
"ds_read_b128 a[212:215], v29 offset:34816\n" \
"v_mfma_f32_16x16x32_bf16 v[148:151], a[216:219], a[240:243], v[148:151]\n" \
"ds_read_b64_tr_b16 a[234:235], v34 offset:41984\n" \
"v_mfma_f32_16x16x32_bf16 v[152:155], a[152:155], a[180:183], v[152:155]\n" \
"ds_read_b64_tr_b16 a[172:173], v35 offset:32768\n" \
"v_mfma_f32_16x16x32_bf16 v[152:155], a[216:219], a[244:247], v[152:155]\n" \
"ds_read_b64_tr_b16 a[174:175], v35 offset:33792\n" \
"v_mfma_f32_16x16x32_bf16 v[156:159], a[152:155], a[184:187], v[156:159]\n" \
"ds_read_b64_tr_b16 a[236:237], v35 offset:40960\n" \
"v_mfma_f32_16x16x32_bf16 v[156:159], a[216:219], a[248:251], v[156:159]\n" \
"ds_read_b64_tr_b16 a[238:239], v35 offset:41984\n" \
"v_mfma_f32_16x16x32_bf16 v[160:163], a[152:155], a[188:191], v[160:163]\n" \
"v_mfma_f32_16x16x32_bf16 v[160:163], a[216:219], a[252:255], v[160:163]\n" \
"ds_read_b128 a[152:155], v28 offset:36864\n" \
"v_mfma_f32_16x16x32_bf16 v[176:179], a[156:159], a[188:191], v[176:179]\n" \
"ds_read_b128 a[216:219], v29 offset:36864\n" \
"v_mfma_f32_16x16x32_bf16 v[176:179], a[220:223], a[252:255], v[176:179]\n" \
"v_mfma_f32_16x16x32_bf16 v[172:175], a[156:159], a[184:187], v[172:175]\n" \
"v_mfma_f32_16x16x32_bf16 v[172:175], a[220:223], a[248:251], v[172:175]\n" \
"v_mfma_f32_16x16x32_bf16 v[168:171], a[156:159], a[180:183], v[168:171]\n" \
"v_mfma_f32_16x16x32_bf16 v[168:171], a[220:223], a[244:247], v[168:171]\n" \
"v_mfma_f32_16x16x32_bf16 v[164:167], a[156:159], a[176:179], v[164:167]\n" \
"v_mfma_f32_16x16x32_bf16 v[164:167], a[220:223], a[240:243], v[164:167]\n" \
"ds_read_b128 a[156:159], v28 offset:38912\n" \
"ds_read_b128 a[220:223], v29 offset:38912\n" \
"s_waitcnt lgkmcnt(15)\n" \
"v_mfma_f32_16x16x32_bf16 a[0:3], a[128:131], a[160:163], a[0:3]\n" \
"s_addk_i32 s84, 0x2000\n" \
"v_mfma_f32_16x16x32_bf16 a[0:3], a[192:195], a[224:227], a[0:3]\n" \
"buffer_load_dwordx4 v24, s[76:79], s84 offen offset:2048 lds\n" \
"v_mfma_f32_16x16x32_bf16 a[4:7], a[128:131], a[164:167], a[4:7]\n" \
"ds_read_b64_tr_b16 a[176:177], v36 offset:32768\n" \
"v_mfma_f32_16x16x32_bf16 a[4:7], a[192:195], a[228:231], a[4:7]\n" \
"s_addk_i32 s84, 0x2000\n" \
"s_waitcnt lgkmcnt(13)\n" \
"v_mfma_f32_16x16x32_bf16 a[8:11], a[128:131], a[168:171], a[8:11]\n" \
"buffer_load_dwordx4 v25, s[76:79], s84 offen offset:3072 lds\n" \
"s_waitcnt lgkmcnt(9)\n" \
"v_mfma_f32_16x16x32_bf16 a[8:11], a[192:195], a[232:235], a[8:11]\n" \
"ds_read_b64_tr_b16 a[178:179], v36 offset:33792\n" \
"s_waitcnt lgkmcnt(8)\n" \
"v_mfma_f32_16x16x32_bf16 a[12:15], a[128:131], a[172:175], a[12:15]\n" \
"ds_read_b64_tr_b16 a[240:241], v36 offset:40960\n" \
"s_waitcnt lgkmcnt(7)\n" \
"v_mfma_f32_16x16x32_bf16 a[12:15], a[192:195], a[236:239], a[12:15]\n" \
"v_mfma_f32_16x16x32_bf16 a[28:31], a[132:135], a[172:175], a[28:31]\n" \
"v_mfma_f32_16x16x32_bf16 a[28:31], a[196:199], a[236:239], a[28:31]\n" \
"ds_read_b64_tr_b16 a[242:243], v36 offset:41984\n" \
"v_mfma_f32_16x16x32_bf16 a[24:27], a[132:135], a[168:171], a[24:27]\n" \
"v_mfma_f32_16x16x32_bf16 a[24:27], a[196:199], a[232:235], a[24:27]\n" \
"v_mfma_f32_16x16x32_bf16 a[20:23], a[132:135], a[164:167], a[20:23]\n" \
"s_nop 1\n" \
"v_accvgpr_read_b32 v184, a12\n" \
"v_accvgpr_read_b32 v185, a13\n" \
"v_mfma_f32_16x16x32_bf16 a[20:23], a[196:199], a[228:231], a[20:23]\n" \
"ds_read_b64_tr_b16 a[180:181], v37 offset:32768\n" \
"v_accvgpr_read_b32 v186, a14\n" \
"v_mfma_f32_16x16x32_bf16 a[16:19], a[132:135], a[160:163], a[16:19]\n" \
"v_accvgpr_read_b32 v187, a15\n" \
"v_accvgpr_read_b32 v180, a28\n" \
"v_mfma_f32_16x16x32_bf16 a[16:19], a[196:199], a[224:227], a[16:19]\n" \
"v_accvgpr_read_b32 v181, a29\n" \
"v_accvgpr_read_b32 v182, a30\n" \
"v_mfma_f32_16x16x32_bf16 a[32:35], a[136:139], a[160:163], a[32:35]\n" \
"ds_read_b64_tr_b16 a[182:183], v37 offset:33792\n" \
"v_accvgpr_read_b32 v183, a31\n" \
"v_mfma_f32_16x16x32_bf16 a[32:35], a[200:203], a[224:227], a[32:35]\n" \
"v_cvt_pk_bf16_f32 v184, v184, v185\n" \
"v_cvt_pk_bf16_f32 v185, v186, v187\n" \
"v_mfma_f32_16x16x32_bf16 a[36:39], a[136:139], a[164:167], a[36:39]\n" \
"v_lshlrev_b32_e32 v45, 3, v20\n" \
"v_add_u32_e32 v45, s31, v45\n" \
"v_mfma_f32_16x16x32_bf16 a[36:39], a[200:203], a[228:231], a[36:39]\n" \
"s_getpc_b64 s[86:87]\n" \
"s_sub_u32 s86, s86, 0x10800\n" \
"s_subb_u32 s87, s87, 0\n" \
"s_mov_b32 m0, s32\n" \
"s_nop 0\n" \
"v_mfma_f32_16x16x32_bf16 a[40:43], a[136:139], a[168:171], a[40:43]\n" \
"global_load_lds_dword v45, s[86:87]\n" \
"v_mfma_f32_16x16x32_bf16 a[40:43], a[200:203], a[232:235], a[40:43]\n" \
"ds_read_b64_tr_b16 a[244:245], v37 offset:40960\n" \
"v_cvt_pk_bf16_f32 v186, v180, v181\n" \
"v_mfma_f32_16x16x32_bf16 a[44:47], a[136:139], a[172:175], a[44:47]\n" \
"ds_read_b64_tr_b16 a[246:247], v37 offset:41984\n" \
"v_cvt_pk_bf16_f32 v187, v182, v183\n" \
"v_mfma_f32_16x16x32_bf16 a[44:47], a[200:203], a[236:239], a[44:47]\n" \
"v_accvgpr_read_b32 v188, a8\n" \
"v_accvgpr_read_b32 v189, a9\n" \
"v_mfma_f32_16x16x32_bf16 a[60:63], a[140:143], a[172:175], a[60:63]\n" \
"v_accvgpr_read_b32 v190, a10\n" \
"v_accvgpr_read_b32 v191, a11\n" \
"v_mfma_f32_16x16x32_bf16 a[60:63], a[204:207], a[236:239], a[60:63]\n" \
"v_accvgpr_read_b32 v180, a24\n" \
"v_accvgpr_read_b32 v181, a25\n" \
"v_mfma_f32_16x16x32_bf16 a[56:59], a[140:143], a[168:171], a[56:59]\n" \
"ds_read_b64_tr_b16 a[184:185], v38 offset:32768\n" \
"v_accvgpr_read_b32 v182, a26\n" \
"v_mfma_f32_16x16x32_bf16 a[56:59], a[204:207], a[232:235], a[56:59]\n" \
"v_accvgpr_read_b32 v183, a27\n" \
"v_cvt_pk_bf16_f32 v188, v188, v189\n" \
"v_mfma_f32_16x16x32_bf16 a[52:55], a[140:143], a[164:167], a[52:55]\n" \
"v_cvt_pk_bf16_f32 v189, v190, v191\n" \
"v_cvt_pk_bf16_f32 v190, v180, v181\n" \
"v_mfma_f32_16x16x32_bf16 a[52:55], a[204:207], a[228:231], a[52:55]\n" \
"ds_read_b64_tr_b16 a[186:187], v38 offset:33792\n" \
"v_cvt_pk_bf16_f32 v191, v182, v183\n" \
"v_mfma_f32_16x16x32_bf16 a[48:51], a[140:143], a[160:163], a[48:51]\n" \
"v_accvgpr_read_b32 v192, a4\n" \
"v_accvgpr_read_b32 v193, a5\n" \
"v_mfma_f32_16x16x32_bf16 a[48:51], a[204:207], a[224:227], a[48:51]\n" \
"v_accvgpr_read_b32 v194, a6\n" \
"v_accvgpr_read_b32 v195, a7\n" \
"v_mfma_f32_16x16x32_bf16 a[64:67], a[144:147], a[160:163], a[64:67]\n" \
"ds_read_b64_tr_b16 a[248:249], v38 offset:40960\n" \
"v_accvgpr_read_b32 v180, a20\n" \
"v_mfma_f32_16x16x32_bf16 a[64:67], a[208:211], a[224:227], a[64:67]\n" \
"v_accvgpr_read_b32 v181, a21\n" \
"v_accvgpr_read_b32 v182, a22\n" \
"v_mfma_f32_16x16x32_bf16 a[68:71], a[144:147], a[164:167], a[68:71]\n" \
"v_accvgpr_read_b32 v183, a23\n" \
"v_cvt_pk_bf16_f32 v192, v192, v193\n" \
"v_mfma_f32_16x16x32_bf16 a[68:71], a[208:211], a[228:231], a[68:71]\n" \
"ds_read_b64_tr_b16 a[250:251], v38 offset:41984\n" \
"v_cvt_pk_bf16_f32 v193, v194, v195\n" \
"v_mfma_f32_16x16x32_bf16 a[72:75], a[144:147], a[168:171], a[72:75]\n" \
"v_cvt_pk_bf16_f32 v194, v180, v181\n" \
"v_cvt_pk_bf16_f32 v195, v182, v183\n" \
"v_mfma_f32_16x16x32_bf16 a[72:75], a[208:211], a[232:235], a[72:75]\n" \
"v_accvgpr_read_b32 v196, a0\n" \
"v_accvgpr_read_b32 v197, a1\n" \
"v_mfma_f32_16x16x32_bf16 a[76:79], a[144:147], a[172:175], a[76:79]\n" \
"v_accvgpr_read_b32 v198, a2\n" \
"v_accvgpr_read_b32 v199, a3\n" \
"v_mfma_f32_16x16x32_bf16 a[76:79], a[208:211], a[236:239], a[76:79]\n" \
"ds_read_b64_tr_b16 a[188:189], v39 offset:32768\n" \
"v_accvgpr_read_b32 v180, a16\n" \
"v_mfma_f32_16x16x32_bf16 a[92:95], a[148:151], a[172:175], a[92:95]\n" \
"s_add_u32 s89, s41, 0x0\n" \
"s_nop 0\n" \
"buffer_store_dwordx4 v[184:187], v20, s[80:83], s89 offen offset:3072 sc1\n" \
"v_mfma_f32_16x16x32_bf16 a[92:95], a[212:215], a[236:239], a[92:95]\n" \
"v_accvgpr_read_b32 v181, a17\n" \
"v_accvgpr_read_b32 v182, a18\n" \
"v_mfma_f32_16x16x32_bf16 a[88:91], a[148:151], a[168:171], a[88:91]\n" \
"ds_read_b64_tr_b16 a[190:191], v39 offset:33792\n" \
"v_accvgpr_read_b32 v183, a19\n" \
"v_mfma_f32_16x16x32_bf16 a[88:91], a[212:215], a[232:235], a[88:91]\n" \
"v_cvt_pk_bf16_f32 v196, v196, v197\n" \
"v_cvt_pk_bf16_f32 v197, v198, v199\n" \
"v_mfma_f32_16x16x32_bf16 a[84:87], a[148:151], a[164:167], a[84:87]\n" \
"v_cvt_pk_bf16_f32 v198, v180, v181\n" \
"v_cvt_pk_bf16_f32 v199, v182, v183\n" \
"v_mfma_f32_16x16x32_bf16 a[84:87], a[212:215], a[228:231], a[84:87]\n" \
"ds_read_b64_tr_b16 a[252:253], v39 offset:40960\n" \
"v_accvgpr_read_b32 v200, a44\n" \
"v_mfma_f32_16x16x32_bf16 a[80:83], a[148:151], a[160:163], a[80:83]\n" \
"s_add_u32 s88, s41, 0x0\n" \
"buffer_store_dwordx4 v[188:191], v20, s[80:83], s88 offen offset:2048 sc1\n" \
"v_mfma_f32_16x16x32_bf16 a[80:83], a[212:215], a[224:227], a[80:83]\n" \
"v_accvgpr_read_b32 v201, a45\n" \
"v_accvgpr_read_b32 v202, a46\n" \
"s_waitcnt lgkmcnt(15)\n" \
"v_mfma_f32_16x16x32_bf16 a[96:99], a[152:155], a[160:163], a[96:99]\n" \
"ds_read_b64_tr_b16 a[254:255], v39 offset:41984\n" \
"v_accvgpr_read_b32 v203, a47\n" \
"v_mfma_f32_16x16x32_bf16 a[96:99], a[216:219], a[224:227], a[96:99]\n" \
"v_accvgpr_read_b32 v180, a60\n" \
"v_accvgpr_read_b32 v181, a61\n" \
"v_mfma_f32_16x16x32_bf16 a[100:103], a[152:155], a[164:167], a[100:103]\n" \
"v_accvgpr_read_b32 v182, a62\n" \
"v_accvgpr_read_b32 v183, a63\n" \
"v_mfma_f32_16x16x32_bf16 a[100:103], a[216:219], a[228:231], a[100:103]\n" \
"v_cvt_pk_bf16_f32 v200, v200, v201\n" \
"v_cvt_pk_bf16_f32 v201, v202, v203\n" \
"v_mfma_f32_16x16x32_bf16 a[104:107], a[152:155], a[168:171], a[104:107]\n" \
"s_add_u32 s89, s41, 0x0\n" \
"s_nop 0\n" \
"buffer_store_dwordx4 v[192:195], v20, s[80:83], s89 offen offset:1024 sc1\n" \
"v_mfma_f32_16x16x32_bf16 a[104:107], a[216:219], a[232:235], a[104:107]\n" \
"v_cvt_pk_bf16_f32 v202, v180, v181\n" \
"v_cvt_pk_bf16_f32 v203, v182, v183\n" \
"v_mfma_f32_16x16x32_bf16 a[108:111], a[152:155], a[172:175], a[108:111]\n" \
"v_accvgpr_read_b32 v204, a40\n" \
"v_accvgpr_read_b32 v205, a41\n" \
"v_mfma_f32_16x16x32_bf16 a[108:111], a[216:219], a[236:239], a[108:111]\n" \
"v_accvgpr_read_b32 v206, a42\n" \
"v_accvgpr_read_b32 v207, a43\n" \
"v_mfma_f32_16x16x32_bf16 a[124:127], a[156:159], a[172:175], a[124:127]\n" \
"v_accvgpr_read_b32 v180, a56\n" \
"v_accvgpr_read_b32 v181, a57\n" \
"v_mfma_f32_16x16x32_bf16 a[124:127], a[220:223], a[236:239], a[124:127]\n" \
"v_accvgpr_read_b32 v182, a58\n" \
"v_accvgpr_read_b32 v183, a59\n" \
"v_mfma_f32_16x16x32_bf16 a[120:123], a[156:159], a[168:171], a[120:123]\n" \
"s_add_u32 s88, s41, 0x0\n" \
"s_nop 0\n" \
"buffer_store_dwordx4 v[196:199], v20, s[80:83], s88 offen offset:0 sc1\n" \
"v_mfma_f32_16x16x32_bf16 a[120:123], a[220:223], a[232:235], a[120:123]\n" \
"v_cvt_pk_bf16_f32 v204, v204, v205\n" \
"v_cvt_pk_bf16_f32 v205, v206, v207\n" \
"v_mfma_f32_16x16x32_bf16 a[116:119], a[156:159], a[164:167], a[116:119]\n" \
"v_cvt_pk_bf16_f32 v206, v180, v181\n" \
"v_cvt_pk_bf16_f32 v207, v182, v183\n" \
"v_mfma_f32_16x16x32_bf16 a[116:119], a[220:223], a[228:231], a[116:119]\n" \
"v_accvgpr_read_b32 v208, a36\n" \
"v_accvgpr_read_b32 v209, a37\n" \
"v_mfma_f32_16x16x32_bf16 a[112:115], a[156:159], a[160:163], a[112:115]\n" \
"v_accvgpr_read_b32 v210, a38\n" \
"v_accvgpr_read_b32 v211, a39\n" \
"v_mfma_f32_16x16x32_bf16 a[112:115], a[220:223], a[224:227], a[112:115]\n" \
"v_accvgpr_read_b32 v180, a52\n" \
"v_accvgpr_read_b32 v181, a53\n" \
"s_waitcnt vmcnt(5) lgkmcnt(0)\n" \
"s_barrier\n" \
"v_xor_b32_e32 v36, 0x4000, v36\n" \
"v_xor_b32_e64 v32, 0x0, v48\n" \
"v_xor_b32_e32 v37, 0x4000, v37\n" \
"v_xor_b32_e64 v33, 0x20, v48\n" \
"v_xor_b32_e32 v38, 0x4000, v38\n" \
"v_xor_b32_e64 v34, 0x40, v48\n" \
"v_xor_b32_e32 v39, 0x4000, v39\n" \
"v_xor_b32_e32 v35, 0x60, v48\n" \
"v_add_u32_e32 v28, s43, v28\n" \
"v_add_u32_e32 v29, s43, v29\n" \
"v_mfma_f32_16x16x32_bf16 v[116:119], a[144:147], a[176:179], v[116:119]\n" \
"s_add_u32 m0, s40, 0x8000\n" \
"s_add_u32 s85, s39, 0x2000\n" \
"v_mfma_f32_16x16x32_bf16 v[116:119], a[208:211], a[240:243], v[116:119]\n" \
"buffer_load_dwordx4 v20, s[80:83], s85 offen sc1 lds\n" \
"v_mfma_f32_16x16x32_bf16 v[120:123], a[144:147], a[180:183], v[120:123]\n" \
"ds_read_b64_tr_b16 a[160:161], v32 offset:0\n" \
"v_accvgpr_read_b32 v182, a54\n" \
"v_mfma_f32_16x16x32_bf16 v[120:123], a[208:211], a[244:247], v[120:123]\n" \
"s_add_u32 m0, s40, 0x9000\n" \
"s_add_u32 s85, s39, 0x3000\n" \
"v_mfma_f32_16x16x32_bf16 v[124:127], a[144:147], a[184:187], v[124:127]\n" \
"buffer_load_dwordx4 v20, s[80:83], s85 offen sc1 lds\n" \
"v_mfma_f32_16x16x32_bf16 v[124:127], a[208:211], a[248:251], v[124:127]\n" \
"ds_read_b64_tr_b16 a[162:163], v32 offset:512\n" \
"v_accvgpr_read_b32 v183, a55\n" \
"v_mfma_f32_16x16x32_bf16 v[128:131], a[144:147], a[188:191], v[128:131]\n" \
"ds_read_b64_tr_b16 a[224:225], v32 offset:4096\n" \
"v_cvt_pk_bf16_f32 v208, v208, v209\n" \
"v_mfma_f32_16x16x32_bf16 v[128:131], a[208:211], a[252:255], v[128:131]\n" \
"ds_read_b128 a[144:147], v28 offset:8192\n" \
"v_cvt_pk_bf16_f32 v209, v210, v211\n" \
"v_mfma_f32_16x16x32_bf16 v[144:147], a[148:151], a[188:191], v[144:147]\n" \
"ds_read_b128 a[208:211], v29 offset:8192\n" \
"v_cvt_pk_bf16_f32 v210, v180, v181\n" \
"v_mfma_f32_16x16x32_bf16 v[144:147], a[212:215], a[252:255], v[144:147]\n" \
"s_add_u32 m0, s40, 0xb000\n" \
"s_movk_i32 s84, 0x80\n" \
"v_mfma_f32_16x16x32_bf16 v[140:143], a[148:151], a[184:187], v[140:143]\n" \
"buffer_load_dwordx4 v21, s[76:79], s84 offen offset:0 lds\n" \
"v_mfma_f32_16x16x32_bf16 v[140:143], a[212:215], a[248:251], v[140:143]\n" \
"s_add_u32 m0, s30, 0x8000\n" \
"s_movk_i32 s84, 0x80\n" \
"v_mfma_f32_16x16x32_bf16 v[136:139], a[148:151], a[180:183], v[136:139]\n" \
"buffer_load_dwordx4 v24, s[76:79], s84 offen offset:0 lds\n" \
"v_mfma_f32_16x16x32_bf16 v[136:139], a[212:215], a[244:247], v[136:139]\n" \
"ds_read_b64_tr_b16 a[226:227], v32 offset:4608\n" \
"v_cvt_pk_bf16_f32 v211, v182, v183\n" \
"v_mfma_f32_16x16x32_bf16 v[132:135], a[148:151], a[176:179], v[132:135]\n" \
"ds_read_b64_tr_b16 a[164:165], v33 offset:0\n" \
"v_accvgpr_read_b32 v212, a32\n" \
"v_mfma_f32_16x16x32_bf16 v[132:135], a[212:215], a[240:243], v[132:135]\n" \
"ds_read_b128 a[148:151], v28 offset:10240\n" \
"v_accvgpr_read_b32 v213, a33\n" \
"v_mfma_f32_16x16x32_bf16 v[148:151], a[152:155], a[176:179], v[148:151]\n" \
"ds_read_b128 a[212:215], v29 offset:10240\n" \
"v_accvgpr_read_b32 v214, a34\n" \
"v_mfma_f32_16x16x32_bf16 v[148:151], a[216:219], a[240:243], v[148:151]\n" \
"s_movk_i32 s84, 0x2080\n" \
"v_mfma_f32_16x16x32_bf16 v[152:155], a[152:155], a[180:183], v[152:155]\n" \
"buffer_load_dwordx4 v25, s[76:79], s84 offen offset:1024 lds\n" \
"v_mfma_f32_16x16x32_bf16 v[152:155], a[216:219], a[244:247], v[152:155]\n" \
"s_movk_i32 s84, 0x4080\n" \
"v_mfma_f32_16x16x32_bf16 v[156:159], a[152:155], a[184:187], v[156:159]\n" \
"buffer_load_dwordx4 v24, s[76:79], s84 offen offset:2048 lds\n" \
"v_mfma_f32_16x16x32_bf16 v[156:159], a[216:219], a[248:251], v[156:159]\n" \
"ds_read_b64_tr_b16 a[166:167], v33 offset:512\n" \
"v_accvgpr_read_b32 v215, a35\n" \
"v_mfma_f32_16x16x32_bf16 v[160:163], a[152:155], a[188:191], v[160:163]\n" \
"ds_read_b64_tr_b16 a[228:229], v33 offset:4096\n" \
"v_accvgpr_read_b32 v180, a48\n" \
"v_mfma_f32_16x16x32_bf16 v[160:163], a[216:219], a[252:255], v[160:163]\n" \
"ds_read_b128 a[152:155], v28 offset:12288\n" \
"v_accvgpr_read_b32 v181, a49\n" \
"v_mfma_f32_16x16x32_bf16 v[176:179], a[156:159], a[188:191], v[176:179]\n" \
"ds_read_b128 a[216:219], v29 offset:12288\n" \
"v_accvgpr_read_b32 v182, a50\n" \
"v_mfma_f32_16x16x32_bf16 v[176:179], a[220:223], a[252:255], v[176:179]\n" \
"s_movk_i32 s84, 0x6080\n" \
"v_mfma_f32_16x16x32_bf16 v[172:175], a[156:159], a[184:187], v[172:175]\n" \
"buffer_load_dwordx4 v25, s[76:79], s84 offen offset:3072 lds\n" \
"v_mfma_f32_16x16x32_bf16 v[172:175], a[220:223], a[248:251], v[172:175]\n" \
"s_add_u32 m0, s31, 0x8000\n" \
"s_mov_b32 s84, 0x80000\n" \
"v_mfma_f32_16x16x32_bf16 v[168:171], a[156:159], a[180:183], v[168:171]\n" \
"buffer_load_dwordx4 v26, s[68:71], s84 offen offset:0 lds\n" \
"v_mfma_f32_16x16x32_bf16 v[168:171], a[220:223], a[244:247], v[168:171]\n" \
"s_addk_i32 s84, 0x7c00\n" \
"v_mfma_f32_16x16x32_bf16 v[164:167], a[156:159], a[176:179], v[164:167]\n" \
"buffer_load_dwordx4 v26, s[68:71], s84 offen offset:1024 lds\n" \
"v_mfma_f32_16x16x32_bf16 v[164:167], a[220:223], a[240:243], v[164:167]\n" \
"ds_read_b128 a[156:159], v28 offset:14336\n" \
"v_accvgpr_read_b32 v183, a51\n" \
"v_mfma_f32_16x16x32_bf16 v[84:87], a[136:139], a[176:179], v[84:87]\n" \
"ds_read_b128 a[220:223], v29 offset:14336\n" \
"v_cvt_pk_bf16_f32 v212, v212, v213\n" \
"v_mfma_f32_16x16x32_bf16 v[84:87], a[200:203], a[240:243], v[84:87]\n" \
"s_addk_i32 s84, 0x7c00\n" \
"v_mfma_f32_16x16x32_bf16 v[88:91], a[136:139], a[180:183], v[88:91]\n" \
"buffer_load_dwordx4 v27, s[68:71], s84 offen offset:2048 lds\n" \
"v_mfma_f32_16x16x32_bf16 v[88:91], a[200:203], a[244:247], v[88:91]\n" \
"ds_read_b64_tr_b16 a[230:231], v33 offset:4608\n" \
"v_cvt_pk_bf16_f32 v213, v214, v215\n" \
"v_mfma_f32_16x16x32_bf16 v[92:95], a[136:139], a[184:187], v[92:95]\n" \
"s_addk_i32 s84, 0x7c00\n" \
"v_mfma_f32_16x16x32_bf16 v[92:95], a[200:203], a[248:251], v[92:95]\n" \
"buffer_load_dwordx4 v27, s[68:71], s84 offen offset:3072 lds\n" \
"v_mfma_f32_16x16x32_bf16 v[96:99], a[136:139], a[188:191], v[96:99]\n" \
"ds_read_b64_tr_b16 a[172:173], v35 offset:0\n" \
"v_cvt_pk_bf16_f32 v214, v180, v181\n" \
"v_mfma_f32_16x16x32_bf16 v[96:99], a[200:203], a[252:255], v[96:99]\n" \
"ds_read_b128 a[136:139], v28 offset:4096\n" \
"v_cvt_pk_bf16_f32 v215, v182, v183\n" \
"v_mfma_f32_16x16x32_bf16 v[112:115], a[140:143], a[188:191], v[112:115]\n" \
"ds_read_b128 a[200:203], v29 offset:4096\n" \
"v_accvgpr_read_b32 v216, a68\n" \
"v_mfma_f32_16x16x32_bf16 v[112:115], a[204:207], a[252:255], v[112:115]\n" \
"s_add_u32 m0, s30, 0x9000\n" \
"s_mov_b32 s84, 0x9080\n" \
"v_mfma_f32_16x16x32_bf16 v[108:111], a[140:143], a[184:187], v[108:111]\n" \
"buffer_load_dwordx4 v24, s[76:79], s84 offen offset:0 lds\n" \
"v_mfma_f32_16x16x32_bf16 v[108:111], a[204:207], a[248:251], v[108:111]\n" \
"ds_read_b64_tr_b16 a[174:175], v35 offset:512\n" \
"v_accvgpr_read_b32 v217, a69\n" \
"v_mfma_f32_16x16x32_bf16 v[104:107], a[140:143], a[180:183], v[104:107]\n" \
"s_addk_i32 s84, 0x2000\n" \
"s_nop 0\n" \
"v_mfma_f32_16x16x32_bf16 v[104:107], a[204:207], a[244:247], v[104:107]\n" \
"buffer_load_dwordx4 v25, s[76:79], s84 offen offset:1024 lds\n" \
"v_mfma_f32_16x16x32_bf16 v[100:103], a[140:143], a[176:179], v[100:103]\n" \
"ds_read_b64_tr_b16 a[236:237], v35 offset:4096\n" \
"v_accvgpr_read_b32 v218, a70\n" \
"v_mfma_f32_16x16x32_bf16 v[100:103], a[204:207], a[240:243], v[100:103]\n" \
"ds_read_b128 a[140:143], v28 offset:6144\n" \
"v_accvgpr_read_b32 v219, a71\n" \
"v_mfma_f32_16x16x32_bf16 v[52:55], a[128:131], a[176:179], v[52:55]\n" \
"ds_read_b128 a[204:207], v29 offset:6144\n" \
"v_accvgpr_read_b32 v180, a84\n" \
"v_mfma_f32_16x16x32_bf16 v[52:55], a[192:195], a[240:243], v[52:55]\n" \
"ds_read_b64_tr_b16 a[238:239], v35 offset:4608\n" \
"v_accvgpr_read_b32 v181, a85\n" \
"v_mfma_f32_16x16x32_bf16 v[56:59], a[128:131], a[180:183], v[56:59]\n" \
"ds_read_b64_tr_b16 a[168:169], v34 offset:0\n" \
"v_accvgpr_read_b32 v182, a86\n" \
"v_mfma_f32_16x16x32_bf16 v[56:59], a[192:195], a[244:247], v[56:59]\n" \
"ds_read_b64_tr_b16 a[170:171], v34 offset:512\n" \
"v_accvgpr_read_b32 v183, a87\n" \
"v_mfma_f32_16x16x32_bf16 v[60:63], a[128:131], a[184:187], v[60:63]\n" \
"ds_read_b64_tr_b16 a[232:233], v34 offset:4096\n" \
"v_cvt_pk_bf16_f32 v216, v216, v217\n" \
"v_mfma_f32_16x16x32_bf16 v[60:63], a[192:195], a[248:251], v[60:63]\n" \
"ds_read_b64_tr_b16 a[234:235], v34 offset:4608\n" \
"v_cvt_pk_bf16_f32 v217, v218, v219\n" \
"v_mfma_f32_16x16x32_bf16 v[64:67], a[128:131], a[188:191], v[64:67]\n" \
"v_cvt_pk_bf16_f32 v218, v180, v181\n" \
"v_cvt_pk_bf16_f32 v219, v182, v183\n" \
"v_mfma_f32_16x16x32_bf16 v[64:67], a[192:195], a[252:255], v[64:67]\n" \
"ds_read_b128 a[128:131], v28 offset:0\n" \
"v_accvgpr_read_b32 v220, a64\n" \
"v_mfma_f32_16x16x32_bf16 v[80:83], a[132:135], a[188:191], v[80:83]\n" \
"ds_read_b128 a[192:195], v29 offset:0\n" \
"v_accvgpr_read_b32 v221, a65\n" \
"v_mfma_f32_16x16x32_bf16 v[80:83], a[196:199], a[252:255], v[80:83]\n" \
"v_accvgpr_read_b32 v222, a66\n" \
"v_accvgpr_read_b32 v223, a67\n" \
"v_mfma_f32_16x16x32_bf16 v[76:79], a[132:135], a[184:187], v[76:79]\n" \
"v_accvgpr_read_b32 v180, a80\n" \
"v_accvgpr_read_b32 v181, a81\n" \
"v_mfma_f32_16x16x32_bf16 v[76:79], a[196:199], a[248:251], v[76:79]\n" \
"v_accvgpr_read_b32 v182, a82\n" \
"v_accvgpr_read_b32 v183, a83\n" \
"v_mfma_f32_16x16x32_bf16 v[72:75], a[132:135], a[180:183], v[72:75]\n" \
"v_cvt_pk_bf16_f32 v220, v220, v221\n" \
"v_cvt_pk_bf16_f32 v221, v222, v223\n" \
"v_mfma_f32_16x16x32_bf16 v[72:75], a[196:199], a[244:247], v[72:75]\n" \
"v_cvt_pk_bf16_f32 v222, v180, v181\n" \
"v_cvt_pk_bf16_f32 v223, v182, v183\n" \
"v_mfma_f32_16x16x32_bf16 v[68:71], a[132:135], a[176:179], v[68:71]\n" \
"v_cvt_pk_bf16_f32 v224, v128, v129\n" \
"v_cvt_pk_bf16_f32 v225, v130, v131\n" \
"v_mfma_f32_16x16x32_bf16 v[68:71], a[196:199], a[240:243], v[68:71]\n" \
"ds_read_b128 a[132:135], v28 offset:2048\n" \
"ds_read_b128 a[196:199], v29 offset:2048\n" \
"s_waitcnt lgkmcnt(15)\n" \
"v_mfma_f32_16x16x32_bf16 a[0:3], a[160:163], a[144:147], 0\n" \
"s_addk_i32 s84, 0x2000\n" \
"v_cvt_pk_bf16_f32 v226, v144, v145\n" \
"v_mfma_f32_16x16x32_bf16 a[0:3], a[224:227], a[208:211], a[0:3]\n" \
"buffer_load_dwordx4 v24, s[76:79], s84 offen offset:2048 lds\n" \
"v_cvt_pk_bf16_f32 v227, v146, v147\n" \
"v_mfma_f32_16x16x32_bf16 a[4:7], a[164:167], a[144:147], 0\n" \
"ds_read_b64_tr_b16 a[176:177], v36 offset:0\n" \
"v_cvt_pk_bf16_f32 v228, v124, v125\n" \
"v_mfma_f32_16x16x32_bf16 a[4:7], a[228:231], a[208:211], a[4:7]\n" \
"s_addk_i32 s84, 0x2000\n" \
"v_cvt_pk_bf16_f32 v229, v126, v127\n" \
"s_waitcnt lgkmcnt(13)\n" \
"v_mfma_f32_16x16x32_bf16 a[12:15], a[172:175], a[144:147], 0\n" \
"buffer_load_dwordx4 v25, s[76:79], s84 offen offset:3072 lds\n" \
"v_cvt_pk_bf16_f32 v230, v140, v141\n" \
"s_waitcnt lgkmcnt(9)\n" \
"v_mfma_f32_16x16x32_bf16 a[12:15], a[236:239], a[208:211], a[12:15]\n" \
"ds_read_b64_tr_b16 a[178:179], v36 offset:1024\n" \
"v_cvt_pk_bf16_f32 v231, v142, v143\n" \
"s_waitcnt lgkmcnt(8)\n" \
"v_mfma_f32_16x16x32_bf16 a[8:11], a[144:147], a[168:171], 0\n" \
"ds_read_b64_tr_b16 a[240:241], v36 offset:8192\n" \
"v_cvt_pk_bf16_f32 v232, v120, v121\n" \
"s_waitcnt lgkmcnt(7)\n" \
"s_nop 0\n" \
"v_mfma_f32_16x16x32_bf16 a[8:11], a[208:211], a[232:235], a[8:11]\n" \
"ds_read_b64_tr_b16 a[242:243], v36 offset:9216\n" \
"v_cvt_pk_bf16_f32 v233, v122, v123\n" \
"v_mfma_f32_16x16x32_bf16 a[24:27], a[148:151], a[168:171], 0\n" \
"ds_read_b64_tr_b16 a[180:181], v37 offset:0\n" \
"v_cvt_pk_bf16_f32 v234, v136, v137\n" \
"v_mfma_f32_16x16x32_bf16 a[24:27], a[212:215], a[232:235], a[24:27]\n" \
"ds_read_b64_tr_b16 a[182:183], v37 offset:1024\n" \
"v_cvt_pk_bf16_f32 v235, v138, v139\n" \
"v_mfma_f32_16x16x32_bf16 a[28:31], a[172:175], a[148:151], 0\n" \
"ds_read_b64_tr_b16 a[244:245], v37 offset:8192\n" \
"v_cvt_pk_bf16_f32 v236, v116, v117\n" \
"v_mfma_f32_16x16x32_bf16 a[28:31], a[236:239], a[212:215], a[28:31]\n" \
"s_add_u32 s89, s41, 0x2000\n" \
"buffer_store_dwordx4 v[200:203], v20, s[80:83], s89 offen offset:3072 sc1\n" \
"v_mfma_f32_16x16x32_bf16 a[20:23], a[164:167], a[148:151], 0\n" \
"ds_read_b64_tr_b16 a[246:247], v37 offset:9216\n" \
"v_cvt_pk_bf16_f32 v237, v118, v119\n" \
"v_mfma_f32_16x16x32_bf16 a[20:23], a[228:231], a[212:215], a[20:23]\n" \
"ds_read_b64_tr_b16 a[184:185], v38 offset:0\n" \
"v_cvt_pk_bf16_f32 v238, v132, v133\n" \
"v_mfma_f32_16x16x32_bf16 a[16:19], a[160:163], a[148:151], 0\n" \
"ds_read_b64_tr_b16 a[186:187], v38 offset:1024\n" \
"v_cvt_pk_bf16_f32 v239, v134, v135\n" \
"v_mfma_f32_16x16x32_bf16 a[16:19], a[224:227], a[212:215], a[16:19]\n" \
"v_cvt_pk_bf16_f32 v240, v160, v161\n" \
"v_cvt_pk_bf16_f32 v241, v162, v163\n" \
"v_cvt_pk_bf16_f32 v242, v176, v177\n" \
"v_mfma_f32_16x16x32_bf16 a[32:35], a[160:163], a[152:155], 0\n" \
"s_add_u32 s88, s41, 0x2000\n" \
"buffer_store_dwordx4 v[204:207], v20, s[80:83], s88 offen offset:2048 sc1\n" \
"v_mfma_f32_16x16x32_bf16 a[32:35], a[224:227], a[216:219], a[32:35]\n" \
"ds_read_b64_tr_b16 a[248:249], v38 offset:8192\n" \
"v_cvt_pk_bf16_f32 v243, v178, v179\n" \
"v_mfma_f32_16x16x32_bf16 a[36:39], a[164:167], a[152:155], 0\n" \
"ds_read_b64_tr_b16 a[250:251], v38 offset:9216\n" \
"v_cvt_pk_bf16_f32 v244, v156, v157\n" \
"v_mfma_f32_16x16x32_bf16 a[36:39], a[228:231], a[216:219], a[36:39]\n" \
"ds_read_b64_tr_b16 a[188:189], v39 offset:0\n" \
"v_cvt_pk_bf16_f32 v245, v158, v159\n" \
"v_mfma_f32_16x16x32_bf16 a[44:47], a[172:175], a[152:155], 0\n" \
"v_cvt_pk_bf16_f32 v246, v172, v173\n" \
"v_cvt_pk_bf16_f32 v247, v174, v175\n" \
"v_cvt_pk_bf16_f32 v184, v152, v153\n" \
"v_mfma_f32_16x16x32_bf16 a[44:47], a[236:239], a[216:219], a[44:47]\n" \
"ds_read_b64_tr_b16 a[190:191], v39 offset:1024\n" \
"v_cvt_pk_bf16_f32 v185, v154, v155\n" \
"v_mfma_f32_16x16x32_bf16 a[40:43], a[152:155], a[168:171], 0\n" \
"s_add_u32 s89, s41, 0x2000\n" \
"buffer_store_dwordx4 v[208:211], v20, s[80:83], s89 offen offset:1024 sc1\n" \
"v_mfma_f32_16x16x32_bf16 a[40:43], a[216:219], a[232:235], a[40:43]\n" \
"ds_read_b64_tr_b16 a[252:253], v39 offset:8192\n" \
"v_cvt_pk_bf16_f32 v186, v168, v169\n" \
"v_mfma_f32_16x16x32_bf16 a[56:59], a[156:159], a[168:171], 0\n" \
"ds_read_b64_tr_b16 a[254:255], v39 offset:9216\n" \
"v_cvt_pk_bf16_f32 v187, v170, v171\n" \
"v_mfma_f32_16x16x32_bf16 a[56:59], a[220:223], a[232:235], a[56:59]\n" \
"v_cvt_pk_bf16_f32 v188, v148, v149\n" \
"v_cvt_pk_bf16_f32 v189, v150, v151\n" \
"v_cvt_pk_bf16_f32 v190, v164, v165\n" \
"v_mfma_f32_16x16x32_bf16 a[60:63], a[172:175], a[156:159], 0\n" \
"v_cvt_pk_bf16_f32 v191, v166, v167\n" \
"v_cvt_pk_bf16_f32 v192, v96, v97\n" \
"v_cvt_pk_bf16_f32 v193, v98, v99\n" \
"v_mfma_f32_16x16x32_bf16 a[60:63], a[236:239], a[220:223], a[60:63]\n" \
"s_add_u32 s88, s41, 0x2000\n" \
"buffer_store_dwordx4 v[212:215], v20, s[80:83], s88 offen offset:0 sc1\n" \
"v_mfma_f32_16x16x32_bf16 a[52:55], a[164:167], a[156:159], 0\n" \
"v_cvt_pk_bf16_f32 v194, v112, v113\n" \
"v_cvt_pk_bf16_f32 v195, v114, v115\n" \
"v_cvt_pk_bf16_f32 v196, v92, v93\n" \
".fill (L_pb + 61152 - .) / 4, 4, 0xBF800000\n" \
"L_pp15:\n" \
"s_cbranch_execz L_pret\n" \
"s_nop 0\n" \
"v_mfma_f32_16x16x32_bf16 a[52:55], a[228:231], a[220:223], a[52:55]\n" \
"v_cvt_pk_bf16_f32 v197, v94, v95\n" \
"v_cvt_pk_bf16_f32 v198, v108, v109\n" \
"v_cvt_pk_bf16_f32 v199, v110, v111\n" \
"v_mfma_f32_16x16x32_bf16 a[48:51], a[160:163], a[156:159], 0\n" \
"v_cvt_pk_bf16_f32 v252, v88, v89\n" \
"v_cvt_pk_bf16_f32 v253, v90, v91\n" \
"v_cvt_pk_bf16_f32 v254, v104, v105\n" \
"v_mfma_f32_16x16x32_bf16 a[48:51], a[224:227], a[220:223], a[48:51]\n" \
"v_cvt_pk_bf16_f32 v255, v106, v107\n" \
"v_accvgpr_write_b32 a84, v252\n" \
"v_accvgpr_write_b32 a85, v253\n" \
"v_mfma_f32_16x16x32_bf16 a[64:67], a[164:167], a[140:143], 0\n" \
"v_accvgpr_write_b32 a86, v254\n" \
"v_accvgpr_write_b32 a87, v255\n" \
"v_cvt_pk_bf16_f32 v252, v84, v85\n" \
"v_mfma_f32_16x16x32_bf16 a[64:67], a[228:231], a[204:207], a[64:67]\n" \
"v_cvt_pk_bf16_f32 v253, v86, v87\n" \
"v_cvt_pk_bf16_f32 v254, v100, v101\n" \
"v_cvt_pk_bf16_f32 v255, v102, v103\n" \
"v_mfma_f32_16x16x32_bf16 a[80:83], a[172:175], a[140:143], 0\n" \
"v_mfma_f32_16x16x32_bf16 a[80:83], a[236:239], a[204:207], a[80:83]\n" \
"v_mfma_f32_16x16x32_bf16 a[68:71], a[140:143], a[168:171], 0\n" \
"v_mfma_f32_16x16x32_bf16 a[68:71], a[204:207], a[232:235], a[68:71]\n" \
"s_waitcnt vmcnt(4) lgkmcnt(0)\n" \
"s_barrier\n" \
"v_mfma_f32_16x16x32_bf16 v[116:119], a[176:179], a[144:147], 0\n" \
"s_add_u32 m0, s40, 0x0\n" \
"s_nop 0\n" \
"s_add_u32 s85, s39, 0x4000\n" \
"v_cvt_pk_bf16_f32 v200, v64, v65\n" \
"v_mfma_f32_16x16x32_bf16 v[116:119], a[240:243], a[208:211], v[116:119]\n" \
"buffer_load_dwordx4 v20, s[80:83], s85 offen sc1 lds\n" \
"v_cvt_pk_bf16_f32 v201, v66, v67\n" \
"v_mfma_f32_16x16x32_bf16 v[120:123], a[180:183], a[144:147], 0\n" \
"ds_read_b64_tr_b16 a[160:161], v32 offset:32768\n" \
"v_cvt_pk_bf16_f32 v202, v80, v81\n" \
"v_mfma_f32_16x16x32_bf16 v[120:123], a[244:247], a[208:211], v[120:123]\n" \
"ds_read_b64_tr_b16 a[162:163], v32 offset:33280\n" \
"v_cvt_pk_bf16_f32 v203, v82, v83\n" \
"v_mfma_f32_16x16x32_bf16 v[124:127], a[184:187], a[144:147], 0\n" \
"s_add_u32 m0, s40, 0x1000\n" \
"s_add_u32 s85, s39, 0x5000\n" \
"v_cvt_pk_bf16_f32 v204, v60, v61\n" \
"v_mfma_f32_16x16x32_bf16 v[124:127], a[248:251], a[208:211], v[124:127]\n" \
"buffer_load_dwordx4 v20, s[80:83], s85 offen sc1 lds\n" \
"v_cvt_pk_bf16_f32 v205, v62, v63\n" \
"v_mfma_f32_16x16x32_bf16 v[128:131], a[188:191], a[144:147], 0\n" \
"ds_read_b64_tr_b16 a[224:225], v32 offset:36864\n" \
"v_cvt_pk_bf16_f32 v206, v76, v77\n" \
"v_mfma_f32_16x16x32_bf16 v[128:131], a[252:255], a[208:211], v[128:131]\n" \
"ds_read_b128 a[144:147], v28 offset:40960\n" \
"v_cvt_pk_bf16_f32 v207, v78, v79\n" \
"v_mfma_f32_16x16x32_bf16 v[144:147], a[188:191], a[148:151], 0\n" \
"ds_read_b128 a[208:211], v29 offset:40960\n" \
"v_cvt_pk_bf16_f32 v208, v56, v57\n" \
"v_mfma_f32_16x16x32_bf16 v[144:147], a[252:255], a[212:215], v[144:147]\n" \
"s_add_u32 m0, s40, 0x3000\n" \
"s_movk_i32 s84, 0x100\n" \
"v_cvt_pk_bf16_f32 v209, v58, v59\n" \
"v_mfma_f32_16x16x32_bf16 v[140:143], a[184:187], a[148:151], 0\n" \
"buffer_load_dwordx4 v21, s[76:79], s84 offen offset:0 lds\n" \
"v_cvt_pk_bf16_f32 v210, v72, v73\n" \
"v_mfma_f32_16x16x32_bf16 v[140:143], a[248:251], a[212:215], v[140:143]\n" \
"ds_read_b64_tr_b16 a[226:227], v32 offset:37376\n" \
"v_cvt_pk_bf16_f32 v211, v74, v75\n" \
"v_mfma_f32_16x16x32_bf16 v[136:139], a[180:183], a[148:151], 0\n" \
"s_add_u32 m0, s30, 0x0\n" \
"s_movk_i32 s84, 0x100\n" \
"s_nop 0\n" \
"v_cvt_pk_bf16_f32 v212, v52, v53\n" \
"v_mfma_f32_16x16x32_bf16 v[136:139], a[244:247], a[212:215], v[136:139]\n" \
"buffer_load_dwordx4 v24, s[76:79], s84 offen offset:0 lds\n" \
"v_cvt_pk_bf16_f32 v213, v54, v55\n" \
"v_mfma_f32_16x16x32_bf16 v[132:135], a[176:179], a[148:151], 0\n" \
"ds_read_b64_tr_b16 a[164:165], v33 offset:32768\n" \
"v_cvt_pk_bf16_f32 v214, v68, v69\n" \
"v_mfma_f32_16x16x32_bf16 v[132:135], a[240:243], a[212:215], v[132:135]\n" \
"ds_read_b128 a[148:151], v28 offset:43008\n" \
"v_cvt_pk_bf16_f32 v215, v70, v71\n" \
"v_mfma_f32_16x16x32_bf16 v[148:151], a[176:179], a[152:155], 0\n" \
"ds_read_b128 a[212:215], v29 offset:43008\n" \
"v_mfma_f32_16x16x32_bf16 v[148:151], a[240:243], a[216:219], v[148:151]\n" \
"s_movk_i32 s84, 0x2100\n" \
"v_mfma_f32_16x16x32_bf16 v[152:155], a[180:183], a[152:155], 0\n" \
"buffer_load_dwordx4 v25, s[76:79], s84 offen offset:1024 lds\n" \
"v_mfma_f32_16x16x32_bf16 v[152:155], a[244:247], a[216:219], v[152:155]\n" \
"ds_read_b64_tr_b16 a[166:167], v33 offset:33280\n" \
"v_mfma_f32_16x16x32_bf16 v[156:159], a[184:187], a[152:155], 0\n" \
"ds_read_b64_tr_b16 a[228:229], v33 offset:36864\n" \
"v_mfma_f32_16x16x32_bf16 v[156:159], a[248:251], a[216:219], v[156:159]\n" \
"s_movk_i32 s84, 0x4100\n" \
"v_mfma_f32_16x16x32_bf16 v[160:163], a[188:191], a[152:155], 0\n" \
"buffer_load_dwordx4 v24, s[76:79], s84 offen offset:2048 lds\n" \
"v_mfma_f32_16x16x32_bf16 v[160:163], a[252:255], a[216:219], v[160:163]\n" \
"ds_read_b128 a[152:155], v28 offset:45056\n" \
"v_mfma_f32_16x16x32_bf16 v[176:179], a[188:191], a[156:159], 0\n" \
"ds_read_b128 a[216:219], v29 offset:45056\n" \
"v_mfma_f32_16x16x32_bf16 v[176:179], a[252:255], a[220:223], v[176:179]\n" \
"s_movk_i32 s84, 0x6100\n" \
"s_nop 0\n" \
"v_mfma_f32_16x16x32_bf16 v[172:175], a[184:187], a[156:159], 0\n" \
"buffer_load_dwordx4 v25, s[76:79], s84 offen offset:3072 lds\n" \
"v_mfma_f32_16x16x32_bf16 v[172:175], a[248:251], a[220:223], v[172:175]\n" \
"ds_read_b64_tr_b16 a[230:231], v33 offset:37376\n" \
"v_mfma_f32_16x16x32_bf16 v[168:171], a[180:183], a[156:159], 0\n" \
"ds_read_b64_tr_b16 a[172:173], v35 offset:32768\n" \
"v_mfma_f32_16x16x32_bf16 v[168:171], a[244:247], a[220:223], v[168:171]\n" \
"ds_read_b64_tr_b16 a[174:175], v35 offset:33280\n" \
"v_mfma_f32_16x16x32_bf16 v[164:167], a[176:179], a[156:159], 0\n" \
"v_mfma_f32_16x16x32_bf16 v[164:167], a[240:243], a[220:223], v[164:167]\n" \
"ds_read_b128 a[156:159], v28 offset:47104\n" \
"v_mfma_f32_16x16x32_bf16 v[84:87], a[176:179], a[136:139], 0\n" \
"ds_read_b128 a[220:223], v29 offset:47104\n" \
"v_mfma_f32_16x16x32_bf16 v[84:87], a[240:243], a[200:203], v[84:87]\n" \
"s_add_u32 m0, s31, 0x0\n" \
"s_mov_b32 s84, 0x100000\n" \
"v_mfma_f32_16x16x32_bf16 v[88:91], a[180:183], a[136:139], 0\n" \
"buffer_load_dwordx4 v26, s[68:71], s84 offen offset:0 lds\n" \
"v_mfma_f32_16x16x32_bf16 v[88:91], a[244:247], a[200:203], v[88:91]\n" \
"s_addk_i32 s84, 0x7c00\n" \
"v_mfma_f32_16x16x32_bf16 v[92:95], a[184:187], a[136:139], 0\n" \
"buffer_load_dwordx4 v26, s[68:71], s84 offen offset:1024 lds\n" \
"v_mfma_f32_16x16x32_bf16 v[92:95], a[248:251], a[200:203], v[92:95]\n" \
"ds_read_b64_tr_b16 a[236:237], v35 offset:36864\n" \
"v_mfma_f32_16x16x32_bf16 v[96:99], a[188:191], a[136:139], 0\n" \
"ds_read_b64_tr_b16 a[238:239], v35 offset:37376\n" \
"v_mfma_f32_16x16x32_bf16 v[96:99], a[252:255], a[200:203], v[96:99]\n" \
"ds_read_b128 a[136:139], v28 offset:36864\n" \
"v_mfma_f32_16x16x32_bf16 v[112:115], a[188:191], a[140:143], 0\n" \
"ds_read_b128 a[200:203], v29 offset:36864\n" \
"v_mfma_f32_16x16x32_bf16 v[112:115], a[252:255], a[204:207], v[112:115]\n" \
"s_addk_i32 s84, 0x7c00\n" \
"v_mfma_f32_16x16x32_bf16 v[108:111], a[184:187], a[140:143], 0\n" \
"buffer_load_dwordx4 v27, s[68:71], s84 offen offset:2048 lds\n" \
"v_mfma_f32_16x16x32_bf16 v[108:111], a[248:251], a[204:207], v[108:111]\n" \
"s_addk_i32 s84, 0x7c00\n" \
"v_mfma_f32_16x16x32_bf16 v[104:107], a[180:183], a[140:143], 0\n" \
"buffer_load_dwordx4 v27, s[68:71], s84 offen offset:3072 lds\n" \
"v_mfma_f32_16x16x32_bf16 v[104:107], a[244:247], a[204:207], v[104:107]\n" \
"ds_read_b64_tr_b16 a[168:169], v34 offset:32768\n" \
"v_mfma_f32_16x16x32_bf16 v[100:103], a[176:179], a[140:143], 0\n" \
"ds_read_b64_tr_b16 a[170:171], v34 offset:33280\n" \
"v_mfma_f32_16x16x32_bf16 v[100:103], a[240:243], a[204:207], v[100:103]\n" \
"ds_read_b128 a[140:143], v28 offset:38912\n" \
"v_mfma_f32_16x16x32_bf16 v[52:55], a[176:179], a[128:131], 0\n" \
"ds_read_b128 a[204:207], v29 offset:38912\n" \
"v_mfma_f32_16x16x32_bf16 v[52:55], a[240:243], a[192:195], v[52:55]\n" \
"s_add_u32 m0, s30, 0x1000\n" \
"s_mov_b32 s84, 0x9100\n" \
"v_mfma_f32_16x16x32_bf16 v[56:59], a[180:183], a[128:131], 0\n" \
"buffer_load_dwordx4 v24, s[76:79], s84 offen offset:0 lds\n" \
"v_mfma_f32_16x16x32_bf16 v[56:59], a[244:247], a[192:195], v[56:59]\n" \
"s_addk_i32 s84, 0x2000\n" \
"s_nop 0\n" \
"v_mfma_f32_16x16x32_bf16 v[60:63], a[184:187], a[128:131], 0\n" \
"buffer_load_dwordx4 v25, s[76:79], s84 offen offset:1024 lds\n" \
"v_mfma_f32_16x16x32_bf16 v[60:63], a[248:251], a[192:195], v[60:63]\n" \
"ds_read_b64_tr_b16 a[232:233], v34 offset:36864\n" \
"v_mfma_f32_16x16x32_bf16 v[64:67], a[188:191], a[128:131], 0\n" \
"ds_read_b64_tr_b16 a[234:235], v34 offset:37376\n" \
"v_mfma_f32_16x16x32_bf16 v[64:67], a[252:255], a[192:195], v[64:67]\n" \
"ds_read_b128 a[128:131], v28 offset:32768\n" \
"v_mfma_f32_16x16x32_bf16 v[80:83], a[188:191], a[132:135], 0\n" \
"ds_read_b128 a[192:195], v29 offset:32768\n" \
"v_mfma_f32_16x16x32_bf16 v[80:83], a[252:255], a[196:199], v[80:83]\n" \
"v_mfma_f32_16x16x32_bf16 v[76:79], a[184:187], a[132:135], 0\n" \
"v_mfma_f32_16x16x32_bf16 v[76:79], a[248:251], a[196:199], v[76:79]\n" \
"v_mfma_f32_16x16x32_bf16 v[72:75], a[180:183], a[132:135], 0\n" \
"v_mfma_f32_16x16x32_bf16 v[72:75], a[244:247], a[196:199], v[72:75]\n" \
"v_mfma_f32_16x16x32_bf16 v[68:71], a[176:179], a[132:135], 0\n" \
"v_mfma_f32_16x16x32_bf16 v[68:71], a[240:243], a[196:199], v[68:71]\n" \
"ds_read_b128 a[132:135], v28 offset:34816\n" \
"ds_read_b128 a[196:199], v29 offset:34816\n" \
"s_waitcnt lgkmcnt(15)\n" \
"v_mfma_f32_16x16x32_bf16 a[0:3], a[160:163], a[144:147], a[0:3]\n" \
"s_addk_i32 s84, 0x2000\n" \
"v_mfma_f32_16x16x32_bf16 a[0:3], a[224:227], a[208:211], a[0:3]\n" \
"buffer_load_dwordx4 v24, s[76:79], s84 offen offset:2048 lds\n" \
"v_mfma_f32_16x16x32_bf16 a[4:7], a[164:167], a[144:147], a[4:7]\n" \
"ds_read_b64_tr_b16 a[176:177], v36 offset:32768\n" \
"v_mov_b32_e64 v11, 0\n" \
"v_mfma_f32_16x16x32_bf16 a[4:7], a[228:231], a[208:211], a[4:7]\n" \
"s_addk_i32 s84, 0x2000\n" \
"v_mfma_f32_16x16x32_bf16 a[12:15], a[172:175], a[144:147], a[12:15]\n" \
"buffer_load_dwordx4 v25, s[76:79], s84 offen offset:3072 lds\n" \
"s_waitcnt lgkmcnt(13)\n" \
"v_mfma_f32_16x16x32_bf16 a[12:15], a[236:239], a[208:211], a[12:15]\n" \
"ds_read_b64_tr_b16 a[178:179], v36 offset:33792\n" \
"s_and_b32 s6, s2, 7\n" \
"s_waitcnt lgkmcnt(10)\n" \
"v_mfma_f32_16x16x32_bf16 a[8:11], a[144:147], a[168:171], a[8:11]\n" \
"ds_read_b64_tr_b16 a[240:241], v36 offset:40960\n" \
"s_lshl_b32 s7, s3, 1\n" \
"s_waitcnt lgkmcnt(7)\n" \
"v_mfma_f32_16x16x32_bf16 a[8:11], a[208:211], a[232:235], a[8:11]\n" \
"ds_read_b64_tr_b16 a[242:243], v36 offset:41984\n" \
"s_lshr_b32 s4, s2, 5\n" \
"v_mfma_f32_16x16x32_bf16 a[24:27], a[148:151], a[168:171], a[24:27]\n" \
"ds_read_b64_tr_b16 a[180:181], v37 offset:32768\n" \
"s_lshl_b32 s5, s6, 2\n" \
"v_mfma_f32_16x16x32_bf16 a[24:27], a[212:215], a[232:235], a[24:27]\n" \
"ds_read_b64_tr_b16 a[182:183], v37 offset:33792\n" \
"s_bfe_u32 s8, s2, 0x20003\n" \
"v_mfma_f32_16x16x32_bf16 a[28:31], a[172:175], a[148:151], a[28:31]\n" \
"ds_read_b64_tr_b16 a[244:245], v37 offset:40960\n" \
"s_and_b32 s7, s7, 4\n" \
"v_mfma_f32_16x16x32_bf16 a[28:31], a[236:239], a[212:215], a[28:31]\n" \
"s_add_u32 s89, s41, 0x3000\n" \
"buffer_store_dwordx4 v[192:195], v20, s[80:83], s89 offen offset:3072 sc1\n" \
"v_mfma_f32_16x16x32_bf16 a[20:23], a[164:167], a[148:151], a[20:23]\n" \
"ds_read_b64_tr_b16 a[246:247], v37 offset:41984\n" \
"s_and_b32 s9, s3, 1\n" \
"v_mfma_f32_16x16x32_bf16 a[20:23], a[228:231], a[212:215], a[20:23]\n" \
"ds_read_b64_tr_b16 a[184:185], v38 offset:32768\n" \
"s_or_b32 s13, s5, s8\n" \
"v_mfma_f32_16x16x32_bf16 a[16:19], a[160:163], a[148:151], a[16:19]\n" \
"ds_read_b64_tr_b16 a[186:187], v38 offset:33792\n" \
"s_or_b32 s9, s7, s9\n" \
"v_mfma_f32_16x16x32_bf16 a[16:19], a[224:227], a[212:215], a[16:19]\n" \
"s_mul_i32 s7, s4, 0xa00\n" \
"s_add_i32 s7, s7, 0x5bc1000\n" \
"v_mfma_f32_16x16x32_bf16 a[32:35], a[160:163], a[152:155], a[32:35]\n" \
"s_add_u32 s88, s41, 0x3000\n" \
"buffer_store_dwordx4 v[196:199], v20, s[80:83], s88 offen offset:2048 sc1\n" \
"v_mfma_f32_16x16x32_bf16 a[32:35], a[224:227], a[216:219], a[32:35]\n" \
"ds_read_b64_tr_b16 a[248:249], v38 offset:40960\n" \
"s_lshl_b32 s10, s13, 2\n" \
"v_mfma_f32_16x16x32_bf16 a[36:39], a[164:167], a[152:155], a[36:39]\n" \
"ds_read_b64_tr_b16 a[250:251], v38 offset:41984\n" \
"s_or_b32 s10, s10, s7\n" \
"v_mfma_f32_16x16x32_bf16 a[36:39], a[228:231], a[216:219], a[36:39]\n" \
"ds_read_b64_tr_b16 a[188:189], v39 offset:32768\n" \
"v_writelane_b32 v11, s10, 0\n" \
"v_mfma_f32_16x16x32_bf16 a[44:47], a[172:175], a[152:155], a[44:47]\n" \
"s_lshl_b32 s14, s4, 3\n" \
"v_writelane_b32 v11, s7, 1\n" \
"v_mfma_f32_16x16x32_bf16 a[44:47], a[236:239], a[216:219], a[44:47]\n" \
"ds_read_b64_tr_b16 a[190:191], v39 offset:33792\n" \
"s_or_b32 s7, s14, s6\n" \
"v_mfma_f32_16x16x32_bf16 a[40:43], a[152:155], a[168:171], a[40:43]\n" \
"s_add_u32 s89, s41, 0x3000\n" \
"buffer_store_dwordx4 a[84:87], v20, s[80:83], s89 offen offset:1024 sc1\n" \
"v_mfma_f32_16x16x32_bf16 a[40:43], a[216:219], a[232:235], a[40:43]\n" \
"ds_read_b64_tr_b16 a[252:253], v39 offset:40960\n" \
"s_lshl_b32 s7, s7, 10\n" \
"v_mfma_f32_16x16x32_bf16 a[56:59], a[156:159], a[168:171], a[56:59]\n" \
"ds_read_b64_tr_b16 a[254:255], v39 offset:41984\n" \
"s_lshl_b32 s10, s3, 8\n" \
"v_mfma_f32_16x16x32_bf16 a[56:59], a[220:223], a[232:235], a[56:59]\n" \
"s_or_b32 s7, s7, s10\n" \
"s_or_b32 s10, s7, 0x5c00200\n" \
"v_mfma_f32_16x16x32_bf16 a[60:63], a[172:175], a[156:159], a[60:63]\n" \
"v_writelane_b32 v11, s10, 2\n" \
"s_lshl_b32 s10, s8, 2\n" \
"v_mfma_f32_16x16x32_bf16 a[60:63], a[236:239], a[220:223], a[60:63]\n" \
"s_add_u32 s88, s41, 0x3000\n" \
"buffer_store_dwordx4 v[252:255], v20, s[80:83], s88 offen offset:0 sc1\n" \
"v_mfma_f32_16x16x32_bf16 a[52:55], a[164:167], a[156:159], a[52:55]\n" \
"s_or_b32 s11, s10, s7\n" \
"s_or_b32 s11, s11, 0x5c00000\n" \
"v_mfma_f32_16x16x32_bf16 a[52:55], a[228:231], a[220:223], a[52:55]\n" \
"v_writelane_b32 v11, s11, 3\n" \
"s_or_b32 s7, s7, 0x5c00080\n" \
"v_mfma_f32_16x16x32_bf16 a[48:51], a[160:163], a[156:159], a[48:51]\n" \
"v_writelane_b32 v11, s7, 4\n" \
"s_or_b32 s7, s7, s10\n" \
"v_mfma_f32_16x16x32_bf16 a[48:51], a[224:227], a[220:223], a[48:51]\n" \
"s_add_i32 s10, s3, s2\n" \
"v_writelane_b32 v11, s7, 5\n" \
"v_mfma_f32_16x16x32_bf16 a[64:67], a[164:167], a[140:143], a[64:67]\n" \
"s_mov_b32 s7, 2.0\n" \
"s_mov_b32 s11, 0x5bc0000\n" \
"v_mfma_f32_16x16x32_bf16 a[64:67], a[228:231], a[204:207], a[64:67]\n" \
"s_cmp_lg_u32 s10, 0\n" \
"s_cselect_b32 s10, s7, s11\n" \
"v_mfma_f32_16x16x32_bf16 a[80:83], a[172:175], a[140:143], a[80:83]\n" \
"s_lshr_b32 s15, s2, 3\n" \
"v_writelane_b32 v11, s10, 54\n" \
"v_mfma_f32_16x16x32_bf16 a[80:83], a[236:239], a[204:207], a[80:83]\n" \
"s_lshl_b32 s10, s6, 5\n" \
"s_min_u32 s16, s3, 2\n" \
"v_mfma_f32_16x16x32_bf16 a[68:71], a[140:143], a[168:171], a[68:71]\n" \
"s_mul_i32 s12, s8, 0x120\n" \
"v_mfma_f32_16x16x32_bf16 a[68:71], a[204:207], a[232:235], a[68:71]\n" \
"s_waitcnt vmcnt(4) lgkmcnt(0)\n" \
"s_barrier\n" \
"s_nop 0\n" \
"v_mfma_f32_16x16x32_bf16 v[116:119], a[176:179], a[144:147], v[116:119]\n" \
"s_add_u32 m0, s40, 0x8000\n" \
"s_add_u32 s85, s39, 0x6000\n" \
"v_mfma_f32_16x16x32_bf16 v[116:119], a[240:243], a[208:211], v[116:119]\n" \
"buffer_load_dwordx4 v20, s[80:83], s85 offen sc1 lds\n" \
"v_mfma_f32_16x16x32_bf16 v[120:123], a[180:183], a[144:147], v[120:123]\n" \
"ds_read_b64_tr_b16 a[160:161], v32 offset:0\n" \
"v_mfma_f32_16x16x32_bf16 v[120:123], a[244:247], a[208:211], v[120:123]\n" \
"ds_read_b64_tr_b16 a[162:163], v32 offset:512\n" \
"v_mfma_f32_16x16x32_bf16 v[124:127], a[184:187], a[144:147], v[124:127]\n" \
"s_add_u32 m0, s40, 0x9000\n" \
"s_add_u32 s85, s39, 0x7000\n" \
"v_mfma_f32_16x16x32_bf16 v[124:127], a[248:251], a[208:211], v[124:127]\n" \
"buffer_load_dwordx4 v20, s[80:83], s85 offen sc1 lds\n" \
"v_mfma_f32_16x16x32_bf16 v[128:131], a[188:191], a[144:147], v[128:131]\n" \
"ds_read_b64_tr_b16 a[224:225], v32 offset:4096\n" \
"v_mfma_f32_16x16x32_bf16 v[128:131], a[252:255], a[208:211], v[128:131]\n" \
"ds_read_b128 a[144:147], v28 offset:8192\n" \
"v_mfma_f32_16x16x32_bf16 v[144:147], a[188:191], a[148:151], v[144:147]\n" \
"ds_read_b128 a[208:211], v29 offset:8192\n" \
"v_mfma_f32_16x16x32_bf16 v[144:147], a[252:255], a[212:215], v[144:147]\n" \
"s_add_u32 m0, s40, 0xb000\n" \
"s_movk_i32 s84, 0x180\n" \
"v_mfma_f32_16x16x32_bf16 v[140:143], a[184:187], a[148:151], v[140:143]\n" \
"buffer_load_dwordx4 v21, s[76:79], s84 offen offset:0 lds\n" \
"v_mfma_f32_16x16x32_bf16 v[140:143], a[248:251], a[212:215], v[140:143]\n" \
"ds_read_b64_tr_b16 a[226:227], v32 offset:4608\n" \
"v_mfma_f32_16x16x32_bf16 v[136:139], a[180:183], a[148:151], v[136:139]\n" \
"s_add_u32 m0, s30, 0x8000\n" \
"s_movk_i32 s84, 0x180\n" \
"v_mfma_f32_16x16x32_bf16 v[136:139], a[244:247], a[212:215], v[136:139]\n" \
"buffer_load_dwordx4 v24, s[76:79], s84 offen offset:0 lds\n" \
"v_mfma_f32_16x16x32_bf16 v[132:135], a[176:179], a[148:151], v[132:135]\n" \
"ds_read_b64_tr_b16 a[164:165], v33 offset:0\n" \
"v_mfma_f32_16x16x32_bf16 v[132:135], a[240:243], a[212:215], v[132:135]\n" \
"ds_read_b128 a[148:151], v28 offset:10240\n" \
"v_mfma_f32_16x16x32_bf16 v[148:151], a[176:179], a[152:155], v[148:151]\n" \
"ds_read_b128 a[212:215], v29 offset:10240\n" \
"v_mfma_f32_16x16x32_bf16 v[148:151], a[240:243], a[216:219], v[148:151]\n" \
"s_movk_i32 s84, 0x2180\n" \
"v_mfma_f32_16x16x32_bf16 v[152:155], a[180:183], a[152:155], v[152:155]\n" \
"buffer_load_dwordx4 v25, s[76:79], s84 offen offset:1024 lds\n" \
"v_mfma_f32_16x16x32_bf16 v[152:155], a[244:247], a[216:219], v[152:155]\n" \
"ds_read_b64_tr_b16 a[166:167], v33 offset:512\n" \
"v_mfma_f32_16x16x32_bf16 v[156:159], a[184:187], a[152:155], v[156:159]\n" \
"ds_read_b64_tr_b16 a[228:229], v33 offset:4096\n" \
"v_mfma_f32_16x16x32_bf16 v[156:159], a[248:251], a[216:219], v[156:159]\n" \
"s_movk_i32 s84, 0x4180\n" \
"v_mfma_f32_16x16x32_bf16 v[160:163], a[188:191], a[152:155], v[160:163]\n" \
"buffer_load_dwordx4 v24, s[76:79], s84 offen offset:2048 lds\n" \
"v_mfma_f32_16x16x32_bf16 v[160:163], a[252:255], a[216:219], v[160:163]\n" \
"ds_read_b128 a[152:155], v28 offset:12288\n" \
"v_mfma_f32_16x16x32_bf16 v[176:179], a[188:191], a[156:159], v[176:179]\n" \
"ds_read_b128 a[216:219], v29 offset:12288\n" \
"v_mfma_f32_16x16x32_bf16 v[176:179], a[252:255], a[220:223], v[176:179]\n" \
"s_movk_i32 s84, 0x6180\n" \
"v_mfma_f32_16x16x32_bf16 v[172:175], a[184:187], a[156:159], v[172:175]\n" \
"buffer_load_dwordx4 v25, s[76:79], s84 offen offset:3072 lds\n" \
"v_mfma_f32_16x16x32_bf16 v[172:175], a[248:251], a[220:223], v[172:175]\n" \
"ds_read_b64_tr_b16 a[230:231], v33 offset:4608\n" \
"v_mfma_f32_16x16x32_bf16 v[168:171], a[180:183], a[156:159], v[168:171]\n" \
"ds_read_b64_tr_b16 a[172:173], v35 offset:0\n" \
"v_mfma_f32_16x16x32_bf16 v[168:171], a[244:247], a[220:223], v[168:171]\n" \
"ds_read_b64_tr_b16 a[174:175], v35 offset:512\n" \
"v_mfma_f32_16x16x32_bf16 v[164:167], a[176:179], a[156:159], v[164:167]\n" \
"s_add_u32 s11, s22, s12\n" \
"s_addc_u32 s17, s23, 0\n" \
"v_mfma_f32_16x16x32_bf16 v[164:167], a[240:243], a[220:223], v[164:167]\n" \
"ds_read_b128 a[156:159], v28 offset:14336\n" \
"v_mfma_f32_16x16x32_bf16 v[84:87], a[176:179], a[136:139], v[84:87]\n" \
"ds_read_b128 a[220:223], v29 offset:14336\n" \
"v_mfma_f32_16x16x32_bf16 v[84:87], a[240:243], a[200:203], v[84:87]\n" \
"s_add_u32 m0, s31, 0x8000\n" \
"s_mov_b32 s84, 0x180000\n" \
"v_mfma_f32_16x16x32_bf16 v[88:91], a[180:183], a[136:139], v[88:91]\n" \
"buffer_load_dwordx4 v26, s[68:71], s84 offen offset:0 lds\n" \
"v_mfma_f32_16x16x32_bf16 v[88:91], a[244:247], a[200:203], v[88:91]\n" \
"s_addk_i32 s84, 0x7c00\n" \
"v_mfma_f32_16x16x32_bf16 v[92:95], a[184:187], a[136:139], v[92:95]\n" \
"buffer_load_dwordx4 v26, s[68:71], s84 offen offset:1024 lds\n" \
"v_mfma_f32_16x16x32_bf16 v[92:95], a[248:251], a[200:203], v[92:95]\n" \
"ds_read_b64_tr_b16 a[236:237], v35 offset:4096\n" \
"v_mfma_f32_16x16x32_bf16 v[96:99], a[188:191], a[136:139], v[96:99]\n" \
"ds_read_b64_tr_b16 a[238:239], v35 offset:4608\n" \
"v_mfma_f32_16x16x32_bf16 v[96:99], a[252:255], a[200:203], v[96:99]\n" \
"ds_read_b128 a[136:139], v28 offset:4096\n" \
"v_mfma_f32_16x16x32_bf16 v[112:115], a[188:191], a[140:143], v[112:115]\n" \
"ds_read_b128 a[200:203], v29 offset:4096\n" \
"v_mfma_f32_16x16x32_bf16 v[112:115], a[252:255], a[204:207], v[112:115]\n" \
"s_addk_i32 s84, 0x7c00\n" \
"v_mfma_f32_16x16x32_bf16 v[108:111], a[184:187], a[140:143], v[108:111]\n" \
"buffer_load_dwordx4 v27, s[68:71], s84 offen offset:2048 lds\n" \
"v_mfma_f32_16x16x32_bf16 v[108:111], a[248:251], a[204:207], v[108:111]\n" \
"s_addk_i32 s84, 0x7c00\n" \
"v_mfma_f32_16x16x32_bf16 v[104:107], a[180:183], a[140:143], v[104:107]\n" \
"buffer_load_dwordx4 v27, s[68:71], s84 offen offset:3072 lds\n" \
"v_mfma_f32_16x16x32_bf16 v[104:107], a[244:247], a[204:207], v[104:107]\n" \
"ds_read_b64_tr_b16 a[168:169], v34 offset:0\n" \
"v_mfma_f32_16x16x32_bf16 v[100:103], a[176:179], a[140:143], v[100:103]\n" \
"ds_read_b64_tr_b16 a[170:171], v34 offset:512\n" \
"v_mfma_f32_16x16x32_bf16 v[100:103], a[240:243], a[204:207], v[100:103]\n" \
"ds_read_b128 a[140:143], v28 offset:6144\n" \
"v_mfma_f32_16x16x32_bf16 v[52:55], a[176:179], a[128:131], v[52:55]\n" \
"ds_read_b128 a[204:207], v29 offset:6144\n" \
"v_mfma_f32_16x16x32_bf16 v[52:55], a[240:243], a[192:195], v[52:55]\n" \
"s_add_u32 m0, s30, 0x9000\n" \
"s_mov_b32 s84, 0x9180\n" \
"v_mfma_f32_16x16x32_bf16 v[56:59], a[180:183], a[128:131], v[56:59]\n" \
"buffer_load_dwordx4 v24, s[76:79], s84 offen offset:0 lds\n" \
"v_mfma_f32_16x16x32_bf16 v[56:59], a[244:247], a[192:195], v[56:59]\n" \
"s_addk_i32 s84, 0x2000\n" \
"s_nop 0\n" \
"v_mfma_f32_16x16x32_bf16 v[60:63], a[184:187], a[128:131], v[60:63]\n" \
"buffer_load_dwordx4 v25, s[76:79], s84 offen offset:1024 lds\n" \
"v_mfma_f32_16x16x32_bf16 v[60:63], a[248:251], a[192:195], v[60:63]\n" \
"ds_read_b64_tr_b16 a[232:233], v34 offset:4096\n" \
"v_mfma_f32_16x16x32_bf16 v[64:67], a[188:191], a[128:131], v[64:67]\n" \
"ds_read_b64_tr_b16 a[234:235], v34 offset:4608\n" \
"v_mfma_f32_16x16x32_bf16 v[64:67], a[252:255], a[192:195], v[64:67]\n" \
"ds_read_b128 a[128:131], v28 offset:0\n" \
"v_mfma_f32_16x16x32_bf16 v[80:83], a[188:191], a[132:135], v[80:83]\n" \
"ds_read_b128 a[192:195], v29 offset:0\n" \
"v_mfma_f32_16x16x32_bf16 v[80:83], a[252:255], a[196:199], v[80:83]\n" \
"s_add_u32 s18, s24, s12\n" \
"v_writelane_b32 v11, s11, 9\n" \
"s_addc_u32 s19, s25, 0\n" \
"v_mfma_f32_16x16x32_bf16 v[76:79], a[184:187], a[132:135], v[76:79]\n" \
"s_and_b32 s20, s17, 0xffff\n" \
"v_writelane_b32 v11, s20, 10\n" \
"v_mfma_f32_16x16x32_bf16 v[76:79], a[248:251], a[196:199], v[76:79]\n" \
"s_and_b32 s20, s19, 0xffff\n" \
"v_writelane_b32 v11, s18, 11\n" \
"v_mfma_f32_16x16x32_bf16 v[72:75], a[180:183], a[132:135], v[72:75]\n" \
"s_cmpk_lt_u32 s3, 3\n" \
"s_cselect_b32 s11, s11, s18\n" \
"v_mfma_f32_16x16x32_bf16 v[72:75], a[244:247], a[196:199], v[72:75]\n" \
"s_movk_i32 s18, 0xfff\n" \
"v_writelane_b32 v11, s20, 12\n" \
"v_mfma_f32_16x16x32_bf16 v[68:71], a[176:179], a[132:135], v[68:71]\n" \
"s_and_b32 s20, s29, 0xffff\n" \
"v_writelane_b32 v11, s28, 13\n" \
"v_mfma_f32_16x16x32_bf16 v[68:71], a[240:243], a[196:199], v[68:71]\n" \
"ds_read_b128 a[132:135], v28 offset:2048\n" \
"ds_read_b128 a[196:199], v29 offset:2048\n" \
"s_waitcnt lgkmcnt(15)\n" \
"v_mfma_f32_16x16x32_bf16 a[0:3], a[160:163], a[144:147], a[0:3]\n" \
"s_addk_i32 s84, 0x2000\n" \
"v_mfma_f32_16x16x32_bf16 a[0:3], a[224:227], a[208:211], a[0:3]\n" \
"buffer_load_dwordx4 v24, s[76:79], s84 offen offset:2048 lds\n" \
"v_mfma_f32_16x16x32_bf16 a[4:7], a[164:167], a[144:147], a[4:7]\n" \
"ds_read_b64_tr_b16 a[176:177], v36 offset:0\n" \
"v_writelane_b32 v11, s20, 14\n" \
"v_mfma_f32_16x16x32_bf16 a[4:7], a[228:231], a[208:211], a[4:7]\n" \
"s_addk_i32 s84, 0x2000\n" \
"v_mfma_f32_16x16x32_bf16 a[12:15], a[172:175], a[144:147], a[12:15]\n" \
"buffer_load_dwordx4 v25, s[76:79], s84 offen offset:3072 lds\n" \
"s_waitcnt lgkmcnt(13)\n" \
"v_mfma_f32_16x16x32_bf16 a[12:15], a[236:239], a[208:211], a[12:15]\n" \
"ds_read_b64_tr_b16 a[178:179], v36 offset:1024\n" \
"v_writelane_b32 v11, s11, 15\n" \
"s_waitcnt lgkmcnt(10)\n" \
"v_mfma_f32_16x16x32_bf16 a[8:11], a[144:147], a[168:171], a[8:11]\n" \
"ds_read_b64_tr_b16 a[240:241], v36 offset:8192\n" \
"s_waitcnt lgkmcnt(7)\n" \
"s_nop 0\n" \
"v_mfma_f32_16x16x32_bf16 a[8:11], a[208:211], a[232:235], a[8:11]\n" \
"ds_read_b64_tr_b16 a[242:243], v36 offset:9216\n" \
"v_mfma_f32_16x16x32_bf16 a[24:27], a[148:151], a[168:171], a[24:27]\n" \
"ds_read_b64_tr_b16 a[180:181], v37 offset:0\n" \
"v_mfma_f32_16x16x32_bf16 a[24:27], a[212:215], a[232:235], a[24:27]\n" \
"ds_read_b64_tr_b16 a[182:183], v37 offset:1024\n" \
"v_mfma_f32_16x16x32_bf16 a[28:31], a[172:175], a[148:151], a[28:31]\n" \
"ds_read_b64_tr_b16 a[244:245], v37 offset:8192\n" \
"v_mfma_f32_16x16x32_bf16 a[28:31], a[236:239], a[212:215], a[28:31]\n" \
"s_add_u32 s89, s41, 0x1000\n" \
"buffer_store_dwordx4 v[200:203], v20, s[80:83], s89 offen offset:3072 sc1\n" \
"v_mfma_f32_16x16x32_bf16 a[20:23], a[164:167], a[148:151], a[20:23]\n" \
"ds_read_b64_tr_b16 a[246:247], v37 offset:9216\n" \
"v_mfma_f32_16x16x32_bf16 a[20:23], a[228:231], a[212:215], a[20:23]\n" \
"ds_read_b64_tr_b16 a[184:185], v38 offset:0\n" \
"v_mfma_f32_16x16x32_bf16 a[16:19], a[160:163], a[148:151], a[16:19]\n" \
"ds_read_b64_tr_b16 a[186:187], v38 offset:1024\n" \
"v_mfma_f32_16x16x32_bf16 a[16:19], a[224:227], a[212:215], a[16:19]\n" \
"s_cmpk_lt_u32 s3, 3\n" \
"s_cselect_b32 s11, s17, s19\n" \
"v_mfma_f32_16x16x32_bf16 a[32:35], a[160:163], a[152:155], a[32:35]\n" \
"s_add_u32 s88, s41, 0x1000\n" \
"buffer_store_dwordx4 v[204:207], v20, s[80:83], s88 offen offset:2048 sc1\n" \
"v_mfma_f32_16x16x32_bf16 a[32:35], a[224:227], a[216:219], a[32:35]\n" \
"ds_read_b64_tr_b16 a[248:249], v38 offset:8192\n" \
"s_and_b32 s11, s11, 0xffff\n" \
"v_mfma_f32_16x16x32_bf16 a[36:39], a[164:167], a[152:155], a[36:39]\n" \
"ds_read_b64_tr_b16 a[250:251], v38 offset:9216\n" \
"v_writelane_b32 v11, s11, 16\n" \
"v_mfma_f32_16x16x32_bf16 a[36:39], a[228:231], a[216:219], a[36:39]\n" \
"ds_read_b64_tr_b16 a[188:189], v39 offset:0\n" \
"s_mul_i32 s11, s6, 0x48\n" \
"v_mfma_f32_16x16x32_bf16 a[44:47], a[172:175], a[152:155], a[44:47]\n" \
"s_add_i32 s17, s11, 0x47\n" \
"v_mfma_f32_16x16x32_bf16 a[44:47], a[236:239], a[216:219], a[44:47]\n" \
"ds_read_b64_tr_b16 a[190:191], v39 offset:1024\n" \
"v_mfma_f32_16x16x32_bf16 a[40:43], a[152:155], a[168:171], a[40:43]\n" \
"s_add_u32 s89, s41, 0x1000\n" \
"buffer_store_dwordx4 v[208:211], v20, s[80:83], s89 offen offset:1024 sc1\n" \
"v_mfma_f32_16x16x32_bf16 a[40:43], a[216:219], a[232:235], a[40:43]\n" \
"ds_read_b64_tr_b16 a[252:253], v39 offset:8192\n" \
"v_mfma_f32_16x16x32_bf16 a[56:59], a[156:159], a[168:171], a[56:59]\n" \
"ds_read_b64_tr_b16 a[254:255], v39 offset:9216\n" \
"v_mfma_f32_16x16x32_bf16 a[56:59], a[220:223], a[232:235], a[56:59]\n" \
"s_cmpk_lt_u32 s3, 3\n" \
"s_cselect_b32 s17, s17, s18\n" \
"v_mfma_f32_16x16x32_bf16 a[60:63], a[172:175], a[156:159], a[60:63]\n" \
"s_mulk_i32 s17, 0x480\n" \
"s_addk_i32 s17, 0x120\n" \
"v_mfma_f32_16x16x32_bf16 a[60:63], a[236:239], a[220:223], a[60:63]\n" \
"s_add_u32 s88, s41, 0x1000\n" \
"buffer_store_dwordx4 v[212:215], v20, s[80:83], s88 offen offset:0 sc1\n" \
"v_mfma_f32_16x16x32_bf16 a[52:55], a[164:167], a[156:159], a[52:55]\n" \
"v_writelane_b32 v11, s17, 17\n" \
"s_lshl_b32 s17, s3, 5\n" \
"v_mfma_f32_16x16x32_bf16 a[52:55], a[228:231], a[220:223], a[52:55]\n" \
"s_add_i32 s11, s11, s17\n" \
"s_mul_i32 s17, s11, 0x480\n" \
"v_mfma_f32_16x16x32_bf16 a[48:51], a[160:163], a[156:159], a[48:51]\n" \
"s_mul_i32 s11, s13, 0x24000\n" \
"s_add_i32 s18, s11, 0x1b000\n" \
"v_mfma_f32_16x16x32_bf16 a[48:51], a[224:227], a[220:223], a[48:51]\n" \
"s_cmpk_lt_u32 s3, 3\n" \
"s_cselect_b32 s17, s17, s18\n" \
"v_mfma_f32_16x16x32_bf16 a[64:67], a[164:167], a[140:143], a[64:67]\n" \
"s_mov_b32 s18, 0x8000\n" \
"v_writelane_b32 v11, s17, 18\n" \
"v_mfma_f32_16x16x32_bf16 a[64:67], a[228:231], a[204:207], a[64:67]\n" \
"s_mul_i32 s17, s16, 0x2400\n" \
"s_mul_i32 s16, s16, 0x9000\n" \
"v_mfma_f32_16x16x32_bf16 a[80:83], a[172:175], a[140:143], a[80:83]\n" \
"s_bitset1_b32 s17, 17\n" \
"v_writelane_b32 v11, s17, 19\n" \
"v_mfma_f32_16x16x32_bf16 a[80:83], a[236:239], a[204:207], a[80:83]\n" \
"s_add_i32 s16, s11, s16\n" \
"v_writelane_b32 v11, s16, 26\n" \
"v_mfma_f32_16x16x32_bf16 a[68:71], a[140:143], a[168:171], a[68:71]\n" \
"s_mul_i32 s16, s3, 0x2400\n" \
"s_add_i32 s17, s16, 0xa400\n" \
"v_mfma_f32_16x16x32_bf16 a[68:71], a[204:207], a[232:235], a[68:71]\n" \
"s_waitcnt vmcnt(4) lgkmcnt(0)\n" \
"s_barrier\n" \
"v_mfma_f32_16x16x32_bf16 v[116:119], a[176:179], a[144:147], v[116:119]\n" \
"s_add_u32 m0, s40, 0x0\n" \
"s_nop 0\n" \
"s_add_u32 s85, s39, 0x8000\n" \
"v_mfma_f32_16x16x32_bf16 v[116:119], a[240:243], a[208:211], v[116:119]\n" \
"buffer_load_dwordx4 v20, s[80:83], s85 offen sc1 lds\n" \
"v_mfma_f32_16x16x32_bf16 v[120:123], a[180:183], a[144:147], v[120:123]\n" \
"ds_read_b64_tr_b16 a[160:161], v32 offset:32768\n" \
"v_mfma_f32_16x16x32_bf16 v[120:123], a[244:247], a[208:211], v[120:123]\n" \
"ds_read_b64_tr_b16 a[162:163], v32 offset:33280\n" \
"v_mfma_f32_16x16x32_bf16 v[124:127], a[184:187], a[144:147], v[124:127]\n" \
"s_add_u32 m0, s40, 0x1000\n" \
"s_add_u32 s85, s39, 0x9000\n" \
"v_mfma_f32_16x16x32_bf16 v[124:127], a[248:251], a[208:211], v[124:127]\n" \
"buffer_load_dwordx4 v20, s[80:83], s85 offen sc1 lds\n" \
"v_mfma_f32_16x16x32_bf16 v[128:131], a[188:191], a[144:147], v[128:131]\n" \
"ds_read_b64_tr_b16 a[224:225], v32 offset:36864\n" \
"v_mfma_f32_16x16x32_bf16 v[128:131], a[252:255], a[208:211], v[128:131]\n" \
"ds_read_b128 a[144:147], v28 offset:40960\n" \
"v_mfma_f32_16x16x32_bf16 v[144:147], a[188:191], a[148:151], v[144:147]\n" \
"ds_read_b128 a[208:211], v29 offset:40960\n" \
"v_mfma_f32_16x16x32_bf16 v[144:147], a[252:255], a[212:215], v[144:147]\n" \
".fill (L_pb + 65248 - .) / 4, 4, 0xBF800000\n" \
"L_pp16:\n" \
"s_cbranch_execz L_pret\n" \
"s_add_u32 m0, s40, 0x3000\n" \
"s_movk_i32 s84, 0x200\n" \
"v_mfma_f32_16x16x32_bf16 v[140:143], a[184:187], a[148:151], v[140:143]\n" \
"buffer_load_dwordx4 v21, s[76:79], s84 offen offset:0 lds\n" \
"v_mfma_f32_16x16x32_bf16 v[140:143], a[248:251], a[212:215], v[140:143]\n" \
"ds_read_b64_tr_b16 a[226:227], v32 offset:37376\n" \
"v_mfma_f32_16x16x32_bf16 v[136:139], a[180:183], a[148:151], v[136:139]\n" \
"s_add_u32 m0, s30, 0x0\n" \
"s_movk_i32 s84, 0x200\n" \
"v_mfma_f32_16x16x32_bf16 v[136:139], a[244:247], a[212:215], v[136:139]\n" \
"buffer_load_dwordx4 v24, s[76:79], s84 offen offset:0 lds\n" \
"v_mfma_f32_16x16x32_bf16 v[132:135], a[176:179], a[148:151], v[132:135]\n" \
"ds_read_b64_tr_b16 a[164:165], v33 offset:32768\n" \
"v_mfma_f32_16x16x32_bf16 v[132:135], a[240:243], a[212:215], v[132:135]\n" \
"ds_read_b128 a[148:151], v28 offset:43008\n" \
"v_mfma_f32_16x16x32_bf16 v[148:151], a[176:179], a[152:155], v[148:151]\n" \
"ds_read_b128 a[212:215], v29 offset:43008\n" \
"v_mfma_f32_16x16x32_bf16 v[148:151], a[240:243], a[216:219], v[148:151]\n" \
"s_movk_i32 s84, 0x2200\n" \
"v_mfma_f32_16x16x32_bf16 v[152:155], a[180:183], a[152:155], v[152:155]\n" \
"buffer_load_dwordx4 v25, s[76:79], s84 offen offset:1024 lds\n" \
"v_mfma_f32_16x16x32_bf16 v[152:155], a[244:247], a[216:219], v[152:155]\n" \
"ds_read_b64_tr_b16 a[166:167], v33 offset:33280\n" \
"v_mfma_f32_16x16x32_bf16 v[156:159], a[184:187], a[152:155], v[156:159]\n" \
"ds_read_b64_tr_b16 a[228:229], v33 offset:36864\n" \
"v_mfma_f32_16x16x32_bf16 v[156:159], a[248:251], a[216:219], v[156:159]\n" \
"s_movk_i32 s84, 0x4200\n" \
"v_mfma_f32_16x16x32_bf16 v[160:163], a[188:191], a[152:155], v[160:163]\n" \
"buffer_load_dwordx4 v24, s[76:79], s84 offen offset:2048 lds\n" \
"v_mfma_f32_16x16x32_bf16 v[160:163], a[252:255], a[216:219], v[160:163]\n" \
"ds_read_b128 a[152:155], v28 offset:45056\n" \
"v_mfma_f32_16x16x32_bf16 v[176:179], a[188:191], a[156:159], v[176:179]\n" \
"ds_read_b128 a[216:219], v29 offset:45056\n" \
"v_mfma_f32_16x16x32_bf16 v[176:179], a[252:255], a[220:223], v[176:179]\n" \
"s_movk_i32 s84, 0x6200\n" \
"v_mfma_f32_16x16x32_bf16 v[172:175], a[184:187], a[156:159], v[172:175]\n" \
"buffer_load_dwordx4 v25, s[76:79], s84 offen offset:3072 lds\n" \
"v_mfma_f32_16x16x32_bf16 v[172:175], a[248:251], a[220:223], v[172:175]\n" \
"ds_read_b64_tr_b16 a[230:231], v33 offset:37376\n" \
"v_mfma_f32_16x16x32_bf16 v[168:171], a[180:183], a[156:159], v[168:171]\n" \
"ds_read_b64_tr_b16 a[172:173], v35 offset:32768\n" \
"v_mfma_f32_16x16x32_bf16 v[168:171], a[244:247], a[220:223], v[168:171]\n" \
"ds_read_b64_tr_b16 a[174:175], v35 offset:33280\n" \
"v_mfma_f32_16x16x32_bf16 v[164:167], a[176:179], a[156:159], v[164:167]\n" \
"s_cmpk_lt_u32 s3, 3\n" \
"s_cselect_b32 s17, s17, s18\n" \
"v_mfma_f32_16x16x32_bf16 v[164:167], a[240:243], a[220:223], v[164:167]\n" \
"ds_read_b128 a[156:159], v28 offset:47104\n" \
"s_mov_b32 s18, 0x1b000\n" \
"v_mfma_f32_16x16x32_bf16 v[84:87], a[176:179], a[136:139], v[84:87]\n" \
"ds_read_b128 a[220:223], v29 offset:47104\n" \
"s_xor_b32 s19, s3, 2\n" \
"v_mfma_f32_16x16x32_bf16 v[84:87], a[240:243], a[200:203], v[84:87]\n" \
"s_add_u32 m0, s31, 0x0\n" \
"s_mov_b32 s84, 0x200000\n" \
"v_mfma_f32_16x16x32_bf16 v[88:91], a[180:183], a[136:139], v[88:91]\n" \
"buffer_load_dwordx4 v26, s[68:71], s84 offen offset:0 lds\n" \
"v_mfma_f32_16x16x32_bf16 v[88:91], a[244:247], a[200:203], v[88:91]\n" \
"s_addk_i32 s84, 0x7c00\n" \
"v_mfma_f32_16x16x32_bf16 v[92:95], a[184:187], a[136:139], v[92:95]\n" \
"buffer_load_dwordx4 v26, s[68:71], s84 offen offset:1024 lds\n" \
"v_mfma_f32_16x16x32_bf16 v[92:95], a[248:251], a[200:203], v[92:95]\n" \
"ds_read_b64_tr_b16 a[236:237], v35 offset:36864\n" \
"v_mfma_f32_16x16x32_bf16 v[96:99], a[188:191], a[136:139], v[96:99]\n" \
"ds_read_b64_tr_b16 a[238:239], v35 offset:37376\n" \
"v_mfma_f32_16x16x32_bf16 v[96:99], a[252:255], a[200:203], v[96:99]\n" \
"ds_read_b128 a[136:139], v28 offset:36864\n" \
"v_mfma_f32_16x16x32_bf16 v[112:115], a[188:191], a[140:143], v[112:115]\n" \
"ds_read_b128 a[200:203], v29 offset:36864\n" \
"v_mfma_f32_16x16x32_bf16 v[112:115], a[252:255], a[204:207], v[112:115]\n" \
"s_addk_i32 s84, 0x7c00\n" \
"v_mfma_f32_16x16x32_bf16 v[108:111], a[184:187], a[140:143], v[108:111]\n" \
"buffer_load_dwordx4 v27, s[68:71], s84 offen offset:2048 lds\n" \
"v_mfma_f32_16x16x32_bf16 v[108:111], a[248:251], a[204:207], v[108:111]\n" \
"s_addk_i32 s84, 0x7c00\n" \
"v_mfma_f32_16x16x32_bf16 v[104:107], a[180:183], a[140:143], v[104:107]\n" \
"buffer_load_dwordx4 v27, s[68:71], s84 offen offset:3072 lds\n" \
"v_mfma_f32_16x16x32_bf16 v[104:107], a[244:247], a[204:207], v[104:107]\n" \
"ds_read_b64_tr_b16 a[168:169], v34 offset:32768\n" \
"v_mfma_f32_16x16x32_bf16 v[100:103], a[176:179], a[140:143], v[100:103]\n" \
"ds_read_b64_tr_b16 a[170:171], v34 offset:33280\n" \
"v_mfma_f32_16x16x32_bf16 v[100:103], a[240:243], a[204:207], v[100:103]\n" \
"ds_read_b128 a[140:143], v28 offset:38912\n" \
"v_mfma_f32_16x16x32_bf16 v[52:55], a[176:179], a[128:131], v[52:55]\n" \
"ds_read_b128 a[204:207], v29 offset:38912\n" \
"v_mfma_f32_16x16x32_bf16 v[52:55], a[240:243], a[192:195], v[52:55]\n" \
"s_add_u32 m0, s30, 0x1000\n" \
"s_mov_b32 s84, 0x9200\n" \
"v_mfma_f32_16x16x32_bf16 v[56:59], a[180:183], a[128:131], v[56:59]\n" \
"buffer_load_dwordx4 v24, s[76:79], s84 offen offset:0 lds\n" \
"v_mfma_f32_16x16x32_bf16 v[56:59], a[244:247], a[192:195], v[56:59]\n" \
"s_addk_i32 s84, 0x2000\n" \
"s_nop 0\n" \
"v_mfma_f32_16x16x32_bf16 v[60:63], a[184:187], a[128:131], v[60:63]\n" \
"buffer_load_dwordx4 v25, s[76:79], s84 offen offset:1024 lds\n" \
"v_mfma_f32_16x16x32_bf16 v[60:63], a[248:251], a[192:195], v[60:63]\n" \
"ds_read_b64_tr_b16 a[232:233], v34 offset:36864\n" \
"v_mfma_f32_16x16x32_bf16 v[64:67], a[188:191], a[128:131], v[64:67]\n" \
"ds_read_b64_tr_b16 a[234:235], v34 offset:37376\n" \
"v_mfma_f32_16x16x32_bf16 v[64:67], a[252:255], a[192:195], v[64:67]\n" \
"ds_read_b128 a[128:131], v28 offset:32768\n" \
"v_mfma_f32_16x16x32_bf16 v[80:83], a[188:191], a[132:135], v[80:83]\n" \
"ds_read_b128 a[192:195], v29 offset:32768\n" \
"v_mfma_f32_16x16x32_bf16 v[80:83], a[252:255], a[196:199], v[80:83]\n" \
"s_cmp_lg_u32 s19, 0\n" \
"s_cselect_b32 s18, s17, s18\n" \
"v_mfma_f32_16x16x32_bf16 v[76:79], a[184:187], a[132:135], v[76:79]\n" \
"v_writelane_b32 v11, s17, 20\n" \
"s_or_b32 s17, s16, 0x10000\n" \
"v_mfma_f32_16x16x32_bf16 v[76:79], a[248:251], a[196:199], v[76:79]\n" \
"v_writelane_b32 v11, s18, 21\n" \
"s_add_i32 s18, s16, 0x19000\n" \
"v_mfma_f32_16x16x32_bf16 v[72:75], a[180:183], a[132:135], v[72:75]\n" \
"v_writelane_b32 v11, s17, 22\n" \
"s_mov_b32 s17, 0\n" \
"v_mfma_f32_16x16x32_bf16 v[72:75], a[244:247], a[196:199], v[72:75]\n" \
"s_cmpk_lt_u32 s3, 3\n" \
"s_cselect_b32 s18, s18, s17\n" \
"v_mfma_f32_16x16x32_bf16 v[68:71], a[176:179], a[132:135], v[68:71]\n" \
"s_bitset1_b32 s16, 17\n" \
"v_writelane_b32 v11, s18, 23\n" \
"v_mfma_f32_16x16x32_bf16 v[68:71], a[240:243], a[196:199], v[68:71]\n" \
"ds_read_b128 a[132:135], v28 offset:34816\n" \
"ds_read_b128 a[196:199], v29 offset:34816\n" \
"s_waitcnt lgkmcnt(15)\n" \
"v_mfma_f32_16x16x32_bf16 a[0:3], a[160:163], a[144:147], a[0:3]\n" \
"s_addk_i32 s84, 0x2000\n" \
"v_mfma_f32_16x16x32_bf16 a[0:3], a[224:227], a[208:211], a[0:3]\n" \
"buffer_load_dwordx4 v24, s[76:79], s84 offen offset:2048 lds\n" \
"v_mfma_f32_16x16x32_bf16 a[4:7], a[164:167], a[144:147], a[4:7]\n" \
"ds_read_b64_tr_b16 a[176:177], v36 offset:32768\n" \
"s_movk_i32 s18, 0x2400\n" \
"v_mfma_f32_16x16x32_bf16 a[4:7], a[228:231], a[208:211], a[4:7]\n" \
"s_addk_i32 s84, 0x2000\n" \
"v_mfma_f32_16x16x32_bf16 a[12:15], a[172:175], a[144:147], a[12:15]\n" \
"buffer_load_dwordx4 v25, s[76:79], s84 offen offset:3072 lds\n" \
"s_waitcnt lgkmcnt(13)\n" \
"v_mfma_f32_16x16x32_bf16 a[12:15], a[236:239], a[208:211], a[12:15]\n" \
"ds_read_b64_tr_b16 a[178:179], v36 offset:33792\n" \
"s_waitcnt lgkmcnt(10)\n" \
"v_mfma_f32_16x16x32_bf16 a[8:11], a[144:147], a[168:171], a[8:11]\n" \
"ds_read_b64_tr_b16 a[240:241], v36 offset:40960\n" \
"s_waitcnt lgkmcnt(7)\n" \
"v_mfma_f32_16x16x32_bf16 a[8:11], a[208:211], a[232:235], a[8:11]\n" \
"ds_read_b64_tr_b16 a[242:243], v36 offset:41984\n" \
"v_mfma_f32_16x16x32_bf16 a[24:27], a[148:151], a[168:171], a[24:27]\n" \
"ds_read_b64_tr_b16 a[180:181], v37 offset:32768\n" \
"v_mfma_f32_16x16x32_bf16 a[24:27], a[212:215], a[232:235], a[24:27]\n" \
"ds_read_b64_tr_b16 a[182:183], v37 offset:33792\n" \
"v_mfma_f32_16x16x32_bf16 a[28:31], a[172:175], a[148:151], a[28:31]\n" \
"ds_read_b64_tr_b16 a[244:245], v37 offset:40960\n" \
"v_mfma_f32_16x16x32_bf16 a[28:31], a[236:239], a[212:215], a[28:31]\n" \
"s_add_u32 s89, s42, 0x0\n" \
"buffer_store_dwordx4 v[216:219], v20, s[80:83], s89 offen offset:1024 sc1\n" \
"v_mfma_f32_16x16x32_bf16 a[20:23], a[164:167], a[148:151], a[20:23]\n" \
"ds_read_b64_tr_b16 a[246:247], v37 offset:41984\n" \
"v_mfma_f32_16x16x32_bf16 a[20:23], a[228:231], a[212:215], a[20:23]\n" \
"ds_read_b64_tr_b16 a[184:185], v38 offset:32768\n" \
"v_mfma_f32_16x16x32_bf16 a[16:19], a[160:163], a[148:151], a[16:19]\n" \
"ds_read_b64_tr_b16 a[186:187], v38 offset:33792\n" \
"v_mfma_f32_16x16x32_bf16 a[16:19], a[224:227], a[212:215], a[16:19]\n" \
"s_cmpk_lt_u32 s3, 3\n" \
"s_cselect_b32 s16, s16, s18\n" \
"v_mfma_f32_16x16x32_bf16 a[32:35], a[160:163], a[152:155], a[32:35]\n" \
"s_add_u32 s88, s42, 0x0\n" \
"buffer_store_dwordx4 v[220:223], v20, s[80:83], s88 offen offset:0 sc1\n" \
"v_mfma_f32_16x16x32_bf16 a[32:35], a[224:227], a[216:219], a[32:35]\n" \
"ds_read_b64_tr_b16 a[248:249], v38 offset:40960\n" \
"s_mul_i32 s19, s3, 0x9000\n" \
"v_mfma_f32_16x16x32_bf16 a[36:39], a[164:167], a[152:155], a[36:39]\n" \
"ds_read_b64_tr_b16 a[250:251], v38 offset:41984\n" \
"v_writelane_b32 v11, s16, 24\n" \
"v_mfma_f32_16x16x32_bf16 a[36:39], a[228:231], a[216:219], a[36:39]\n" \
"ds_read_b64_tr_b16 a[188:189], v39 offset:32768\n" \
"s_lshl_b32 s16, s3, 10\n" \
"v_mfma_f32_16x16x32_bf16 a[44:47], a[172:175], a[152:155], a[44:47]\n" \
"s_addk_i32 s16, 0x4800\n" \
"v_writelane_b32 v11, s16, 25\n" \
"v_mfma_f32_16x16x32_bf16 a[44:47], a[236:239], a[216:219], a[44:47]\n" \
"ds_read_b64_tr_b16 a[190:191], v39 offset:33792\n" \
"s_add_i32 s16, s15, 1\n" \
"v_mfma_f32_16x16x32_bf16 a[40:43], a[152:155], a[168:171], a[40:43]\n" \
"s_add_u32 s89, s42, 0x1000\n" \
"buffer_store_dwordx4 v[224:227], v20, s[80:83], s89 offen offset:3072 sc1\n" \
"v_mfma_f32_16x16x32_bf16 a[40:43], a[216:219], a[232:235], a[40:43]\n" \
"ds_read_b64_tr_b16 a[252:253], v39 offset:40960\n" \
"s_and_b32 s16, s16, 3\n" \
"v_mfma_f32_16x16x32_bf16 a[56:59], a[156:159], a[168:171], a[56:59]\n" \
"ds_read_b64_tr_b16 a[254:255], v39 offset:41984\n" \
"s_or_b32 s16, s16, s5\n" \
"v_mfma_f32_16x16x32_bf16 a[56:59], a[220:223], a[232:235], a[56:59]\n" \
"s_mul_i32 s18, s16, 0x24000\n" \
"s_add_i32 s18, s18, s19\n" \
"v_mfma_f32_16x16x32_bf16 a[60:63], a[172:175], a[156:159], a[60:63]\n" \
"s_xor_b32 s13, s13, 2\n" \
"s_add_i32 s15, s15, -1\n" \
"v_mfma_f32_16x16x32_bf16 a[60:63], a[236:239], a[220:223], a[60:63]\n" \
"s_add_u32 s88, s42, 0x1000\n" \
"buffer_store_dwordx4 v[228:231], v20, s[80:83], s88 offen offset:2048 sc1\n" \
"v_mfma_f32_16x16x32_bf16 a[52:55], a[164:167], a[156:159], a[52:55]\n" \
"v_writelane_b32 v11, s18, 27\n" \
"s_mul_i32 s18, s13, 0x24000\n" \
"v_mfma_f32_16x16x32_bf16 a[52:55], a[228:231], a[220:223], a[52:55]\n" \
"s_and_b32 s15, s15, 3\n" \
"s_add_i32 s18, s18, s19\n" \
"v_mfma_f32_16x16x32_bf16 a[48:51], a[160:163], a[156:159], a[48:51]\n" \
"s_or_b32 s15, s15, s5\n" \
"v_writelane_b32 v11, s18, 28\n" \
"v_mfma_f32_16x16x32_bf16 a[48:51], a[224:227], a[220:223], a[48:51]\n" \
"s_mul_i32 s18, s15, 0x24000\n" \
"s_add_i32 s18, s18, s19\n" \
"v_mfma_f32_16x16x32_bf16 a[64:67], a[164:167], a[140:143], a[64:67]\n" \
"v_writelane_b32 v11, s18, 29\n" \
"s_or_b32 s18, s9, s14\n" \
"v_mfma_f32_16x16x32_bf16 a[64:67], a[228:231], a[204:207], a[64:67]\n" \
"s_mul_i32 s18, s18, 0x24800\n" \
"s_lshl_b32 s16, s16, 12\n" \
"v_mfma_f32_16x16x32_bf16 a[80:83], a[172:175], a[140:143], a[80:83]\n" \
"s_add_i32 s19, s18, 0x3004800\n" \
"s_add_i32 s20, s19, s16\n" \
"v_mfma_f32_16x16x32_bf16 a[80:83], a[236:239], a[204:207], a[80:83]\n" \
"v_writelane_b32 v11, s20, 30\n" \
"s_lshl_b32 s13, s13, 12\n" \
"v_mfma_f32_16x16x32_bf16 a[68:71], a[140:143], a[168:171], a[68:71]\n" \
"s_lshl_b32 s15, s15, 12\n" \
"s_sub_i32 s14, s9, s14\n" \
"v_mfma_f32_16x16x32_bf16 a[68:71], a[204:207], a[232:235], a[68:71]\n" \
"s_waitcnt vmcnt(4) lgkmcnt(0)\n" \
"s_barrier\n" \
"v_mov_b32_e32 v45, s62\n" \
"s_mov_b64 exec, 1\n" \
"buffer_store_dword v45, off, s[80:83], s37 offset:0 sc1\n" \
"s_mov_b64 exec, -1\n" \
"v_mfma_f32_16x16x32_bf16 v[116:119], a[176:179], a[144:147], v[116:119]\n" \
"s_add_u32 m0, s40, 0x8000\n" \
"s_add_u32 s85, s39, 0xa000\n" \
"v_mfma_f32_16x16x32_bf16 v[116:119], a[240:243], a[208:211], v[116:119]\n" \
"buffer_load_dwordx4 v20, s[80:83], s85 offen sc1 lds\n" \
"v_mfma_f32_16x16x32_bf16 v[120:123], a[180:183], a[144:147], v[120:123]\n" \
"ds_read_b64_tr_b16 a[160:161], v32 offset:0\n" \
"v_accvgpr_read_b32 v212, a108\n" \
"v_mfma_f32_16x16x32_bf16 v[120:123], a[244:247], a[208:211], v[120:123]\n" \
"ds_read_b64_tr_b16 a[162:163], v32 offset:512\n" \
"v_accvgpr_read_b32 v213, a109\n" \
"v_mfma_f32_16x16x32_bf16 v[124:127], a[184:187], a[144:147], v[124:127]\n" \
"s_add_u32 m0, s40, 0x9000\n" \
"s_add_u32 s85, s39, 0xb000\n" \
"v_mfma_f32_16x16x32_bf16 v[124:127], a[248:251], a[208:211], v[124:127]\n" \
"buffer_load_dwordx4 v20, s[80:83], s85 offen sc1 lds\n" \
"v_mfma_f32_16x16x32_bf16 v[128:131], a[188:191], a[144:147], v[128:131]\n" \
"ds_read_b64_tr_b16 a[224:225], v32 offset:4096\n" \
"v_accvgpr_read_b32 v214, a110\n" \
"v_mfma_f32_16x16x32_bf16 v[128:131], a[252:255], a[208:211], v[128:131]\n" \
"ds_read_b128 a[144:147], v28 offset:8192\n" \
"v_accvgpr_read_b32 v215, a111\n" \
"v_mfma_f32_16x16x32_bf16 v[144:147], a[188:191], a[148:151], v[144:147]\n" \
"ds_read_b128 a[208:211], v29 offset:8192\n" \
"v_accvgpr_read_b32 v180, a124\n" \
"v_mfma_f32_16x16x32_bf16 v[144:147], a[252:255], a[212:215], v[144:147]\n" \
"s_add_u32 m0, s40, 0xb000\n" \
"s_movk_i32 s84, 0x280\n" \
"v_mfma_f32_16x16x32_bf16 v[140:143], a[184:187], a[148:151], v[140:143]\n" \
"buffer_load_dwordx4 v21, s[76:79], s84 offen offset:0 lds\n" \
"v_mfma_f32_16x16x32_bf16 v[140:143], a[248:251], a[212:215], v[140:143]\n" \
"ds_read_b64_tr_b16 a[226:227], v32 offset:4608\n" \
"v_accvgpr_read_b32 v181, a125\n" \
"v_mfma_f32_16x16x32_bf16 v[136:139], a[180:183], a[148:151], v[136:139]\n" \
"s_add_u32 m0, s30, 0x8000\n" \
"s_movk_i32 s84, 0x280\n" \
"v_mfma_f32_16x16x32_bf16 v[136:139], a[244:247], a[212:215], v[136:139]\n" \
"buffer_load_dwordx4 v24, s[76:79], s84 offen offset:0 lds\n" \
"v_mfma_f32_16x16x32_bf16 v[132:135], a[176:179], a[148:151], v[132:135]\n" \
"ds_read_b64_tr_b16 a[164:165], v33 offset:0\n" \
"v_accvgpr_read_b32 v182, a126\n" \
"v_mfma_f32_16x16x32_bf16 v[132:135], a[240:243], a[212:215], v[132:135]\n" \
"ds_read_b128 a[148:151], v28 offset:10240\n" \
"v_accvgpr_read_b32 v183, a127\n" \
"v_mfma_f32_16x16x32_bf16 v[148:151], a[176:179], a[152:155], v[148:151]\n" \
"ds_read_b128 a[212:215], v29 offset:10240\n" \
"v_cvt_pk_bf16_f32 v212, v212, v213\n" \
"v_mfma_f32_16x16x32_bf16 v[148:151], a[240:243], a[216:219], v[148:151]\n" \
"s_movk_i32 s84, 0x2280\n" \
"v_mfma_f32_16x16x32_bf16 v[152:155], a[180:183], a[152:155], v[152:155]\n" \
"buffer_load_dwordx4 v25, s[76:79], s84 offen offset:1024 lds\n" \
"v_mfma_f32_16x16x32_bf16 v[152:155], a[244:247], a[216:219], v[152:155]\n" \
"ds_read_b64_tr_b16 a[166:167], v33 offset:512\n" \
"v_cvt_pk_bf16_f32 v213, v214, v215\n" \
"v_mfma_f32_16x16x32_bf16 v[156:159], a[184:187], a[152:155], v[156:159]\n" \
"ds_read_b64_tr_b16 a[228:229], v33 offset:4096\n" \
"v_cvt_pk_bf16_f32 v214, v180, v181\n" \
"v_mfma_f32_16x16x32_bf16 v[156:159], a[248:251], a[216:219], v[156:159]\n" \
"s_movk_i32 s84, 0x4280\n" \
"v_mfma_f32_16x16x32_bf16 v[160:163], a[188:191], a[152:155], v[160:163]\n" \
"buffer_load_dwordx4 v24, s[76:79], s84 offen offset:2048 lds\n" \
"v_mfma_f32_16x16x32_bf16 v[160:163], a[252:255], a[216:219], v[160:163]\n" \
"ds_read_b128 a[152:155], v28 offset:12288\n" \
"v_cvt_pk_bf16_f32 v215, v182, v183\n" \
"v_mfma_f32_16x16x32_bf16 v[176:179], a[188:191], a[156:159], v[176:179]\n" \
"ds_read_b128 a[216:219], v29 offset:12288\n" \
"v_accvgpr_read_b32 v208, a104\n" \
"v_mfma_f32_16x16x32_bf16 v[176:179], a[252:255], a[220:223], v[176:179]\n" \
"s_movk_i32 s84, 0x6280\n" \
"s_nop 0\n" \
"v_mfma_f32_16x16x32_bf16 v[172:175], a[184:187], a[156:159], v[172:175]\n" \
"buffer_load_dwordx4 v25, s[76:79], s84 offen offset:3072 lds\n" \
"v_mfma_f32_16x16x32_bf16 v[172:175], a[248:251], a[220:223], v[172:175]\n" \
"ds_read_b64_tr_b16 a[230:231], v33 offset:4608\n" \
"v_accvgpr_read_b32 v209, a105\n" \
"v_mfma_f32_16x16x32_bf16 v[168:171], a[180:183], a[156:159], v[168:171]\n" \
"ds_read_b64_tr_b16 a[172:173], v35 offset:0\n" \
"v_accvgpr_read_b32 v210, a106\n" \
"v_mfma_f32_16x16x32_bf16 v[168:171], a[244:247], a[220:223], v[168:171]\n" \
"ds_read_b64_tr_b16 a[174:175], v35 offset:512\n" \
"v_accvgpr_read_b32 v211, a107\n" \
"v_mfma_f32_16x16x32_bf16 v[164:167], a[176:179], a[156:159], v[164:167]\n" \
"v_accvgpr_read_b32 v180, a120\n" \
"v_accvgpr_read_b32 v181, a121\n" \
"v_mfma_f32_16x16x32_bf16 v[164:167], a[240:243], a[220:223], v[164:167]\n" \
"ds_read_b128 a[156:159], v28 offset:14336\n" \
"v_accvgpr_read_b32 v182, a122\n" \
"v_mfma_f32_16x16x32_bf16 v[84:87], a[176:179], a[136:139], v[84:87]\n" \
"ds_read_b128 a[220:223], v29 offset:14336\n" \
"v_accvgpr_read_b32 v183, a123\n" \
"v_mfma_f32_16x16x32_bf16 v[84:87], a[240:243], a[200:203], v[84:87]\n" \
"s_add_u32 m0, s31, 0x8000\n" \
"s_mov_b32 s84, 0x280000\n" \
"v_mfma_f32_16x16x32_bf16 v[88:91], a[180:183], a[136:139], v[88:91]\n" \
"buffer_load_dwordx4 v26, s[68:71], s84 offen offset:0 lds\n" \
"v_mfma_f32_16x16x32_bf16 v[88:91], a[244:247], a[200:203], v[88:91]\n" \
"s_addk_i32 s84, 0x7c00\n" \
"s_nop 0\n" \
"v_mfma_f32_16x16x32_bf16 v[92:95], a[184:187], a[136:139], v[92:95]\n" \
"buffer_load_dwordx4 v26, s[68:71], s84 offen offset:1024 lds\n" \
"v_mfma_f32_16x16x32_bf16 v[92:95], a[248:251], a[200:203], v[92:95]\n" \
"ds_read_b64_tr_b16 a[236:237], v35 offset:4096\n" \
"v_cvt_pk_bf16_f32 v208, v208, v209\n" \
"v_mfma_f32_16x16x32_bf16 v[96:99], a[188:191], a[136:139], v[96:99]\n" \
"ds_read_b64_tr_b16 a[238:239], v35 offset:4608\n" \
"v_cvt_pk_bf16_f32 v209, v210, v211\n" \
"v_mfma_f32_16x16x32_bf16 v[96:99], a[252:255], a[200:203], v[96:99]\n" \
"ds_read_b128 a[136:139], v28 offset:4096\n" \
"v_cvt_pk_bf16_f32 v210, v180, v181\n" \
"v_mfma_f32_16x16x32_bf16 v[112:115], a[188:191], a[140:143], v[112:115]\n" \
"ds_read_b128 a[200:203], v29 offset:4096\n" \
"v_cvt_pk_bf16_f32 v211, v182, v183\n" \
"v_mfma_f32_16x16x32_bf16 v[112:115], a[252:255], a[204:207], v[112:115]\n" \
"s_addk_i32 s84, 0x7c00\n" \
"v_mfma_f32_16x16x32_bf16 v[108:111], a[184:187], a[140:143], v[108:111]\n" \
"buffer_load_dwordx4 v27, s[68:71], s84 offen offset:2048 lds\n" \
"v_mfma_f32_16x16x32_bf16 v[108:111], a[248:251], a[204:207], v[108:111]\n" \
"s_addk_i32 s84, 0x7c00\n" \
"v_mfma_f32_16x16x32_bf16 v[104:107], a[180:183], a[140:143], v[104:107]\n" \
"buffer_load_dwordx4 v27, s[68:71], s84 offen offset:3072 lds\n" \
"v_mfma_f32_16x16x32_bf16 v[104:107], a[244:247], a[204:207], v[104:107]\n" \
"ds_read_b64_tr_b16 a[168:169], v34 offset:0\n" \
"v_accvgpr_read_b32 v204, a100\n" \
"v_mfma_f32_16x16x32_bf16 v[100:103], a[176:179], a[140:143], v[100:103]\n" \
"ds_read_b64_tr_b16 a[170:171], v34 offset:512\n" \
"v_accvgpr_read_b32 v205, a101\n" \
"v_mfma_f32_16x16x32_bf16 v[100:103], a[240:243], a[204:207], v[100:103]\n" \
"ds_read_b128 a[140:143], v28 offset:6144\n" \
"v_accvgpr_read_b32 v206, a102\n" \
"v_mfma_f32_16x16x32_bf16 v[52:55], a[176:179], a[128:131], v[52:55]\n" \
"ds_read_b128 a[204:207], v29 offset:6144\n" \
"v_accvgpr_read_b32 v207, a103\n" \
"v_mfma_f32_16x16x32_bf16 v[52:55], a[240:243], a[192:195], v[52:55]\n" \
"s_add_u32 m0, s30, 0x9000\n" \
"s_mov_b32 s84, 0x9280\n" \
"v_mfma_f32_16x16x32_bf16 v[56:59], a[180:183], a[128:131], v[56:59]\n" \
"buffer_load_dwordx4 v24, s[76:79], s84 offen offset:0 lds\n" \
"v_mfma_f32_16x16x32_bf16 v[56:59], a[244:247], a[192:195], v[56:59]\n" \
"s_addk_i32 s84, 0x2000\n" \
"s_nop 0\n" \
"v_mfma_f32_16x16x32_bf16 v[60:63], a[184:187], a[128:131], v[60:63]\n" \
"buffer_load_dwordx4 v25, s[76:79], s84 offen offset:1024 lds\n" \
"v_mfma_f32_16x16x32_bf16 v[60:63], a[248:251], a[192:195], v[60:63]\n" \
"ds_read_b64_tr_b16 a[232:233], v34 offset:4096\n" \
"v_accvgpr_read_b32 v180, a116\n" \
"v_mfma_f32_16x16x32_bf16 v[64:67], a[188:191], a[128:131], v[64:67]\n" \
"ds_read_b64_tr_b16 a[234:235], v34 offset:4608\n" \
"v_accvgpr_read_b32 v181, a117\n" \
"v_mfma_f32_16x16x32_bf16 v[64:67], a[252:255], a[192:195], v[64:67]\n" \
"ds_read_b128 a[128:131], v28 offset:0\n" \
"v_accvgpr_read_b32 v182, a118\n" \
"v_mfma_f32_16x16x32_bf16 v[80:83], a[188:191], a[132:135], v[80:83]\n" \
"ds_read_b128 a[192:195], v29 offset:0\n" \
"v_accvgpr_read_b32 v183, a119\n" \
"v_mfma_f32_16x16x32_bf16 v[80:83], a[252:255], a[196:199], v[80:83]\n" \
"v_cvt_pk_bf16_f32 v204, v204, v205\n" \
"v_cvt_pk_bf16_f32 v205, v206, v207\n" \
"v_mfma_f32_16x16x32_bf16 v[76:79], a[184:187], a[132:135], v[76:79]\n" \
"v_cvt_pk_bf16_f32 v206, v180, v181\n" \
"v_cvt_pk_bf16_f32 v207, v182, v183\n" \
"v_mfma_f32_16x16x32_bf16 v[76:79], a[248:251], a[196:199], v[76:79]\n" \
"v_accvgpr_read_b32 v200, a96\n" \
"v_accvgpr_read_b32 v201, a97\n" \
"v_mfma_f32_16x16x32_bf16 v[72:75], a[180:183], a[132:135], v[72:75]\n" \
"v_accvgpr_read_b32 v202, a98\n" \
"v_accvgpr_read_b32 v203, a99\n" \
"v_mfma_f32_16x16x32_bf16 v[72:75], a[244:247], a[196:199], v[72:75]\n" \
"v_accvgpr_read_b32 v180, a112\n" \
"v_accvgpr_read_b32 v181, a113\n" \
"v_mfma_f32_16x16x32_bf16 v[68:71], a[176:179], a[132:135], v[68:71]\n" \
"v_accvgpr_read_b32 v182, a114\n" \
"v_accvgpr_read_b32 v183, a115\n" \
"v_mfma_f32_16x16x32_bf16 v[68:71], a[240:243], a[196:199], v[68:71]\n" \
"ds_read_b128 a[132:135], v28 offset:2048\n" \
"ds_read_b128 a[196:199], v29 offset:2048\n" \
"s_waitcnt lgkmcnt(15)\n" \
"v_mfma_f32_16x16x32_bf16 a[0:3], a[160:163], a[144:147], a[0:3]\n" \
"s_addk_i32 s84, 0x2000\n" \
"v_mfma_f32_16x16x32_bf16 a[0:3], a[224:227], a[208:211], a[0:3]\n" \
"buffer_load_dwordx4 v24, s[76:79], s84 offen offset:2048 lds\n" \
"v_mfma_f32_16x16x32_bf16 a[4:7], a[164:167], a[144:147], a[4:7]\n" \
"ds_read_b64_tr_b16 a[176:177], v36 offset:0\n" \
"v_cvt_pk_bf16_f32 v200, v200, v201\n" \
"v_mfma_f32_16x16x32_bf16 a[4:7], a[228:231], a[208:211], a[4:7]\n" \
"s_addk_i32 s84, 0x2000\n" \
"v_mfma_f32_16x16x32_bf16 a[12:15], a[172:175], a[144:147], a[12:15]\n" \
"buffer_load_dwordx4 v25, s[76:79], s84 offen offset:3072 lds\n" \
"s_waitcnt lgkmcnt(13)\n" \
"v_mfma_f32_16x16x32_bf16 a[12:15], a[236:239], a[208:211], a[12:15]\n" \
"ds_read_b64_tr_b16 a[178:179], v36 offset:1024\n" \
"v_cvt_pk_bf16_f32 v201, v202, v203\n" \
"s_waitcnt lgkmcnt(10)\n" \
"v_mfma_f32_16x16x32_bf16 a[8:11], a[144:147], a[168:171], a[8:11]\n" \
"ds_read_b64_tr_b16 a[240:241], v36 offset:8192\n" \
"v_cvt_pk_bf16_f32 v202, v180, v181\n" \
"s_waitcnt lgkmcnt(7)\n" \
"v_mfma_f32_16x16x32_bf16 a[8:11], a[208:211], a[232:235], a[8:11]\n" \
"ds_read_b64_tr_b16 a[242:243], v36 offset:9216\n" \
"v_cvt_pk_bf16_f32 v203, v182, v183\n" \
"v_mfma_f32_16x16x32_bf16 a[24:27], a[148:151], a[168:171], a[24:27]\n" \
"ds_read_b64_tr_b16 a[180:181], v37 offset:0\n" \
"v_accvgpr_read_b32 v196, a76\n" \
"v_mfma_f32_16x16x32_bf16 a[24:27], a[212:215], a[232:235], a[24:27]\n" \
"ds_read_b64_tr_b16 a[182:183], v37 offset:1024\n" \
"v_accvgpr_read_b32 v197, a77\n" \
"v_mfma_f32_16x16x32_bf16 a[28:31], a[172:175], a[148:151], a[28:31]\n" \
"ds_read_b64_tr_b16 a[244:245], v37 offset:8192\n" \
"v_accvgpr_read_b32 v198, a78\n" \
"v_mfma_f32_16x16x32_bf16 a[28:31], a[236:239], a[212:215], a[28:31]\n" \
"ds_read_b64_tr_b16 a[246:247], v37 offset:9216\n" \
"v_accvgpr_read_b32 v199, a79\n" \
"v_mfma_f32_16x16x32_bf16 a[20:23], a[164:167], a[148:151], a[20:23]\n" \
"v_accvgpr_read_b32 v180, a92\n" \
"v_accvgpr_read_b32 v181, a93\n" \
"v_mfma_f32_16x16x32_bf16 a[20:23], a[228:231], a[212:215], a[20:23]\n" \
"s_add_u32 s89, s42, 0x1000\n" \
"buffer_store_dwordx4 v[232:235], v20, s[80:83], s89 offen offset:1024 sc1\n" \
"v_mfma_f32_16x16x32_bf16 a[16:19], a[160:163], a[148:151], a[16:19]\n" \
"ds_read_b64_tr_b16 a[184:185], v38 offset:0\n" \
"v_accvgpr_read_b32 v182, a94\n" \
"v_mfma_f32_16x16x32_bf16 a[16:19], a[224:227], a[212:215], a[16:19]\n" \
"ds_read_b64_tr_b16 a[186:187], v38 offset:1024\n" \
"v_accvgpr_read_b32 v183, a95\n" \
"v_mfma_f32_16x16x32_bf16 a[32:35], a[160:163], a[152:155], a[32:35]\n" \
"ds_read_b64_tr_b16 a[248:249], v38 offset:8192\n" \
"v_cvt_pk_bf16_f32 v196, v196, v197\n" \
"v_mfma_f32_16x16x32_bf16 a[32:35], a[224:227], a[216:219], a[32:35]\n" \
"ds_read_b64_tr_b16 a[250:251], v38 offset:9216\n" \
"v_cvt_pk_bf16_f32 v197, v198, v199\n" \
"v_mfma_f32_16x16x32_bf16 a[36:39], a[164:167], a[152:155], a[36:39]\n" \
"v_cvt_pk_bf16_f32 v198, v180, v181\n" \
"v_cvt_pk_bf16_f32 v199, v182, v183\n" \
"v_mfma_f32_16x16x32_bf16 a[36:39], a[228:231], a[216:219], a[36:39]\n" \
"s_add_u32 s88, s42, 0x1000\n" \
"buffer_store_dwordx4 v[236:239], v20, s[80:83], s88 offen offset:0 sc1\n" \
"v_mfma_f32_16x16x32_bf16 a[44:47], a[172:175], a[152:155], a[44:47]\n" \
"ds_read_b64_tr_b16 a[188:189], v39 offset:0\n" \
"v_accvgpr_read_b32 v192, a72\n" \
"v_mfma_f32_16x16x32_bf16 a[44:47], a[236:239], a[216:219], a[44:47]\n" \
"ds_read_b64_tr_b16 a[190:191], v39 offset:1024\n" \
"v_accvgpr_read_b32 v193, a73\n" \
"v_mfma_f32_16x16x32_bf16 a[40:43], a[152:155], a[168:171], a[40:43]\n" \
"ds_read_b64_tr_b16 a[252:253], v39 offset:8192\n" \
"v_accvgpr_read_b32 v194, a74\n" \
"v_mfma_f32_16x16x32_bf16 a[40:43], a[216:219], a[232:235], a[40:43]\n" \
"v_accvgpr_read_b32 v195, a75\n" \
"v_accvgpr_read_b32 v180, a88\n" \
"v_mfma_f32_16x16x32_bf16 a[56:59], a[156:159], a[168:171], a[56:59]\n" \
"ds_read_b64_tr_b16 a[254:255], v39 offset:9216\n" \
"v_accvgpr_read_b32 v181, a89\n" \
"v_mfma_f32_16x16x32_bf16 a[56:59], a[220:223], a[232:235], a[56:59]\n" \
"v_accvgpr_read_b32 v182, a90\n" \
"v_accvgpr_read_b32 v183, a91\n" \
"v_mfma_f32_16x16x32_bf16 a[60:63], a[172:175], a[156:159], a[60:63]\n" \
"s_add_u32 s89, s42, 0x3000\n" \
"buffer_store_dwordx4 v[240:243], v20, s[80:83], s89 offen offset:3072 sc1\n" \
"v_mfma_f32_16x16x32_bf16 a[60:63], a[236:239], a[220:223], a[60:63]\n" \
"v_cvt_pk_bf16_f32 v192, v192, v193\n" \
"v_cvt_pk_bf16_f32 v193, v194, v195\n" \
"v_mfma_f32_16x16x32_bf16 a[52:55], a[164:167], a[156:159], a[52:55]\n" \
"v_cvt_pk_bf16_f32 v194, v180, v181\n" \
"v_cvt_pk_bf16_f32 v195, v182, v183\n" \
"v_mfma_f32_16x16x32_bf16 a[52:55], a[228:231], a[220:223], a[52:55]\n" \
"s_add_i32 s20, s19, s13\n" \
"v_writelane_b32 v11, s20, 31\n" \
"v_mfma_f32_16x16x32_bf16 a[48:51], a[160:163], a[156:159], a[48:51]\n" \
"s_add_i32 s19, s19, s15\n" \
"s_mul_i32 s14, s14, 0x24800\n" \
"v_mfma_f32_16x16x32_bf16 a[48:51], a[224:227], a[220:223], a[48:51]\n" \
"v_writelane_b32 v11, s19, 32\n" \
"s_add_i32 s19, s14, 0x4120800\n" \
"v_mfma_f32_16x16x32_bf16 a[64:67], a[164:167], a[140:143], a[64:67]\n" \
"s_add_i32 s16, s19, s16\n" \
"v_writelane_b32 v11, s16, 33\n" \
"v_mfma_f32_16x16x32_bf16 a[64:67], a[228:231], a[204:207], a[64:67]\n" \
"s_add_i32 s13, s19, s13\n" \
"v_writelane_b32 v11, s13, 34\n" \
"v_mfma_f32_16x16x32_bf16 a[80:83], a[172:175], a[140:143], a[80:83]\n" \
"s_add_i32 s19, s19, s15\n" \
"s_or_b32 s13, s18, 0x3000000\n" \
"v_mfma_f32_16x16x32_bf16 a[80:83], a[236:239], a[204:207], a[80:83]\n" \
"s_lshl_b32 s15, s6, 11\n" \
"v_writelane_b32 v11, s19, 35\n" \
"v_mfma_f32_16x16x32_bf16 a[68:71], a[140:143], a[168:171], a[68:71]\n" \
"s_add_i32 s16, s13, s15\n" \
"v_writelane_b32 v11, s16, 36\n" \
"v_mfma_f32_16x16x32_bf16 a[68:71], a[204:207], a[232:235], a[68:71]\n" \
"s_waitcnt vmcnt(3) lgkmcnt(0)\n" \
"s_barrier\n" \
"v_mfma_f32_16x16x32_bf16 v[116:119], a[176:179], a[144:147], v[116:119]\n" \
"s_add_u32 m0, s40, 0x0\n" \
"s_add_u32 s85, s39, 0xc000\n" \
"v_mfma_f32_16x16x32_bf16 v[116:119], a[240:243], a[208:211], v[116:119]\n" \
"buffer_load_dwordx4 v20, s[80:83], s85 offen sc1 lds\n" \
"v_mfma_f32_16x16x32_bf16 v[120:123], a[180:183], a[144:147], v[120:123]\n" \
"ds_read_b64_tr_b16 a[160:161], v32 offset:32768\n" \
"s_add_i32 s16, s14, 0x411c000\n" \
"v_mfma_f32_16x16x32_bf16 v[120:123], a[244:247], a[208:211], v[120:123]\n" \
"ds_read_b64_tr_b16 a[162:163], v32 offset:33280\n" \
"s_add_i32 s19, s16, s15\n" \
"s_nop 0\n" \
"v_mfma_f32_16x16x32_bf16 v[124:127], a[184:187], a[144:147], v[124:127]\n" \
"s_add_u32 m0, s40, 0x1000\n" \
"s_add_u32 s85, s39, 0xd000\n" \
"v_mfma_f32_16x16x32_bf16 v[124:127], a[248:251], a[208:211], v[124:127]\n" \
"buffer_load_dwordx4 v20, s[80:83], s85 offen sc1 lds\n" \
"v_mfma_f32_16x16x32_bf16 v[128:131], a[188:191], a[144:147], v[128:131]\n" \
"ds_read_b64_tr_b16 a[224:225], v32 offset:36864\n" \
"v_writelane_b32 v11, s19, 37\n" \
"v_mfma_f32_16x16x32_bf16 v[128:131], a[252:255], a[208:211], v[128:131]\n" \
"ds_read_b128 a[144:147], v28 offset:40960\n" \
"s_add_i32 s18, s18, 0x3004000\n" \
"v_mfma_f32_16x16x32_bf16 v[144:147], a[188:191], a[148:151], v[144:147]\n" \
"ds_read_b128 a[208:211], v29 offset:40960\n" \
"v_writelane_b32 v11, s18, 38\n" \
"v_mfma_f32_16x16x32_bf16 v[144:147], a[252:255], a[212:215], v[144:147]\n" \
"s_add_u32 m0, s40, 0x3000\n" \
"s_movk_i32 s84, 0x300\n" \
"v_mfma_f32_16x16x32_bf16 v[140:143], a[184:187], a[148:151], v[140:143]\n" \
"buffer_load_dwordx4 v21, s[76:79], s84 offen offset:0 lds\n" \
"v_mfma_f32_16x16x32_bf16 v[140:143], a[248:251], a[212:215], v[140:143]\n" \
".fill (L_pb + 69344 - .) / 4, 4, 0xBF800000\n" \
"L_pp17:\n" \
"s_cbranch_execz L_pret\n" \
"ds_read_b64_tr_b16 a[226:227], v32 offset:37376\n" \
"s_add_i32 s14, s14, 0x4120000\n" \
"v_mfma_f32_16x16x32_bf16 v[136:139], a[180:183], a[148:151], v[136:139]\n" \
"s_add_u32 m0, s30, 0x0\n" \
"s_movk_i32 s84, 0x300\n" \
"v_mfma_f32_16x16x32_bf16 v[136:139], a[244:247], a[212:215], v[136:139]\n" \
"buffer_load_dwordx4 v24, s[76:79], s84 offen offset:0 lds\n" \
"v_mfma_f32_16x16x32_bf16 v[132:135], a[176:179], a[148:151], v[132:135]\n" \
"ds_read_b64_tr_b16 a[164:165], v33 offset:32768\n" \
"v_writelane_b32 v11, s14, 39\n" \
"v_mfma_f32_16x16x32_bf16 v[132:135], a[240:243], a[212:215], v[132:135]\n" \
"ds_read_b128 a[148:151], v28 offset:43008\n" \
"s_lshl_b32 s14, s4, 2\n" \
"v_mfma_f32_16x16x32_bf16 v[148:151], a[176:179], a[152:155], v[148:151]\n" \
"ds_read_b128 a[212:215], v29 offset:43008\n" \
"s_or_b32 s8, s14, s8\n" \
"v_mfma_f32_16x16x32_bf16 v[148:151], a[240:243], a[216:219], v[148:151]\n" \
"s_movk_i32 s84, 0x2300\n" \
"v_mfma_f32_16x16x32_bf16 v[152:155], a[180:183], a[152:155], v[152:155]\n" \
"buffer_load_dwordx4 v25, s[76:79], s84 offen offset:1024 lds\n" \
"v_mfma_f32_16x16x32_bf16 v[152:155], a[244:247], a[216:219], v[152:155]\n" \
"ds_read_b64_tr_b16 a[166:167], v33 offset:33280\n" \
"s_mul_i32 s8, s8, 0x90000\n" \
"v_mfma_f32_16x16x32_bf16 v[156:159], a[184:187], a[152:155], v[156:159]\n" \
"ds_read_b64_tr_b16 a[228:229], v33 offset:36864\n" \
"s_lshl_b32 s14, s3, 14\n" \
"v_mfma_f32_16x16x32_bf16 v[156:159], a[248:251], a[216:219], v[156:159]\n" \
"s_movk_i32 s84, 0x4300\n" \
"v_mfma_f32_16x16x32_bf16 v[160:163], a[188:191], a[152:155], v[160:163]\n" \
"buffer_load_dwordx4 v24, s[76:79], s84 offen offset:2048 lds\n" \
"v_mfma_f32_16x16x32_bf16 v[160:163], a[252:255], a[216:219], v[160:163]\n" \
"ds_read_b128 a[152:155], v28 offset:45056\n" \
"s_add_i32 s8, s8, s14\n" \
"v_mfma_f32_16x16x32_bf16 v[176:179], a[188:191], a[156:159], v[176:179]\n" \
"ds_read_b128 a[216:219], v29 offset:45056\n" \
"s_lshl_b32 s9, s9, 4\n" \
"v_mfma_f32_16x16x32_bf16 v[176:179], a[252:255], a[220:223], v[176:179]\n" \
"s_movk_i32 s84, 0x6300\n" \
"v_mfma_f32_16x16x32_bf16 v[172:175], a[184:187], a[156:159], v[172:175]\n" \
"buffer_load_dwordx4 v25, s[76:79], s84 offen offset:3072 lds\n" \
"v_mfma_f32_16x16x32_bf16 v[172:175], a[248:251], a[220:223], v[172:175]\n" \
"ds_read_b64_tr_b16 a[230:231], v33 offset:37376\n" \
"s_lshl_b32 s14, s4, 7\n" \
"v_mfma_f32_16x16x32_bf16 v[168:171], a[180:183], a[156:159], v[168:171]\n" \
"ds_read_b64_tr_b16 a[172:173], v35 offset:32768\n" \
"s_or_b32 s18, s9, s14\n" \
"v_mfma_f32_16x16x32_bf16 v[168:171], a[244:247], a[220:223], v[168:171]\n" \
"ds_read_b64_tr_b16 a[174:175], v35 offset:33280\n" \
"s_sub_i32 s9, s9, s14\n" \
"v_mfma_f32_16x16x32_bf16 v[164:167], a[176:179], a[156:159], v[164:167]\n" \
"s_mulk_i32 s18, 0x480\n" \
"s_mulk_i32 s9, 0x480\n" \
"v_mfma_f32_16x16x32_bf16 v[164:167], a[240:243], a[220:223], v[164:167]\n" \
"ds_read_b128 a[156:159], v28 offset:47104\n" \
"s_or_b32 s14, s18, s12\n" \
"v_mfma_f32_16x16x32_bf16 v[84:87], a[176:179], a[136:139], v[84:87]\n" \
"ds_read_b128 a[220:223], v29 offset:47104\n" \
"s_or_b32 s9, s9, s12\n" \
"v_mfma_f32_16x16x32_bf16 v[84:87], a[240:243], a[200:203], v[84:87]\n" \
"s_add_u32 m0, s31, 0x0\n" \
"s_mov_b32 s84, 0x300000\n" \
"v_mfma_f32_16x16x32_bf16 v[88:91], a[180:183], a[136:139], v[88:91]\n" \
"buffer_load_dwordx4 v26, s[68:71], s84 offen offset:0 lds\n" \
"v_mfma_f32_16x16x32_bf16 v[88:91], a[244:247], a[200:203], v[88:91]\n" \
"s_addk_i32 s84, 0x7c00\n" \
"v_mfma_f32_16x16x32_bf16 v[92:95], a[184:187], a[136:139], v[92:95]\n" \
"buffer_load_dwordx4 v26, s[68:71], s84 offen offset:1024 lds\n" \
"v_mfma_f32_16x16x32_bf16 v[92:95], a[248:251], a[200:203], v[92:95]\n" \
"ds_read_b64_tr_b16 a[236:237], v35 offset:36864\n" \
"s_and_b32 s12, s2, 1\n" \
"v_mfma_f32_16x16x32_bf16 v[96:99], a[188:191], a[136:139], v[96:99]\n" \
"ds_read_b64_tr_b16 a[238:239], v35 offset:37376\n" \
"v_mfma_f32_16x16x32_bf16 v[96:99], a[252:255], a[200:203], v[96:99]\n" \
"ds_read_b128 a[136:139], v28 offset:36864\n" \
"v_mfma_f32_16x16x32_bf16 v[112:115], a[188:191], a[140:143], v[112:115]\n" \
"ds_read_b128 a[200:203], v29 offset:36864\n" \
"v_mfma_f32_16x16x32_bf16 v[112:115], a[252:255], a[204:207], v[112:115]\n" \
"s_addk_i32 s84, 0x7c00\n" \
"v_mfma_f32_16x16x32_bf16 v[108:111], a[184:187], a[140:143], v[108:111]\n" \
"buffer_load_dwordx4 v27, s[68:71], s84 offen offset:2048 lds\n" \
"v_mfma_f32_16x16x32_bf16 v[108:111], a[248:251], a[204:207], v[108:111]\n" \
"s_addk_i32 s84, 0x7c00\n" \
"s_nop 0\n" \
"v_mfma_f32_16x16x32_bf16 v[104:107], a[180:183], a[140:143], v[104:107]\n" \
"buffer_load_dwordx4 v27, s[68:71], s84 offen offset:3072 lds\n" \
"v_mfma_f32_16x16x32_bf16 v[104:107], a[244:247], a[204:207], v[104:107]\n" \
"ds_read_b64_tr_b16 a[168:169], v34 offset:32768\n" \
"v_mfma_f32_16x16x32_bf16 v[100:103], a[176:179], a[140:143], v[100:103]\n" \
"ds_read_b64_tr_b16 a[170:171], v34 offset:33280\n" \
"v_mfma_f32_16x16x32_bf16 v[100:103], a[240:243], a[204:207], v[100:103]\n" \
"ds_read_b128 a[140:143], v28 offset:38912\n" \
"v_mfma_f32_16x16x32_bf16 v[52:55], a[176:179], a[128:131], v[52:55]\n" \
"ds_read_b128 a[204:207], v29 offset:38912\n" \
"v_mfma_f32_16x16x32_bf16 v[52:55], a[240:243], a[192:195], v[52:55]\n" \
"s_add_u32 m0, s30, 0x1000\n" \
"s_mov_b32 s84, 0x9300\n" \
"v_mfma_f32_16x16x32_bf16 v[56:59], a[180:183], a[128:131], v[56:59]\n" \
"buffer_load_dwordx4 v24, s[76:79], s84 offen offset:0 lds\n" \
"v_mfma_f32_16x16x32_bf16 v[56:59], a[244:247], a[192:195], v[56:59]\n" \
"s_addk_i32 s84, 0x2000\n" \
"s_nop 0\n" \
"v_mfma_f32_16x16x32_bf16 v[60:63], a[184:187], a[128:131], v[60:63]\n" \
"buffer_load_dwordx4 v25, s[76:79], s84 offen offset:1024 lds\n" \
"v_mfma_f32_16x16x32_bf16 v[60:63], a[248:251], a[192:195], v[60:63]\n" \
"ds_read_b64_tr_b16 a[232:233], v34 offset:36864\n" \
"v_mfma_f32_16x16x32_bf16 v[64:67], a[188:191], a[128:131], v[64:67]\n" \
"ds_read_b64_tr_b16 a[234:235], v34 offset:37376\n" \
"v_mfma_f32_16x16x32_bf16 v[64:67], a[252:255], a[192:195], v[64:67]\n" \
"ds_read_b128 a[128:131], v28 offset:32768\n" \
"v_mfma_f32_16x16x32_bf16 v[80:83], a[188:191], a[132:135], v[80:83]\n" \
"ds_read_b128 a[192:195], v29 offset:32768\n" \
"v_mfma_f32_16x16x32_bf16 v[80:83], a[252:255], a[196:199], v[80:83]\n" \
"s_cmp_lg_u32 s12, 0\n" \
"s_cselect_b32 s13, s16, s13\n" \
"v_mfma_f32_16x16x32_bf16 v[76:79], a[184:187], a[132:135], v[76:79]\n" \
"s_bfe_u32 s16, s2, 0x20001\n" \
"s_addk_i32 s13, 0x4000\n" \
"v_mfma_f32_16x16x32_bf16 v[76:79], a[248:251], a[196:199], v[76:79]\n" \
"s_xor_b32 s16, s16, s3\n" \
"v_mfma_f32_16x16x32_bf16 v[72:75], a[180:183], a[132:135], v[72:75]\n" \
"s_cmp_lg_u32 s16, 0\n" \
"s_cselect_b32 s13, s7, s13\n" \
"v_mfma_f32_16x16x32_bf16 v[72:75], a[244:247], a[196:199], v[72:75]\n" \
"s_lshl_b32 s6, s6, 16\n" \
"v_writelane_b32 v11, s13, 40\n" \
"v_mfma_f32_16x16x32_bf16 v[68:71], a[176:179], a[132:135], v[68:71]\n" \
"s_add_i32 s13, s8, 0x4240000\n" \
"s_add_i32 s6, s13, s6\n" \
"v_mfma_f32_16x16x32_bf16 v[68:71], a[240:243], a[196:199], v[68:71]\n" \
"ds_read_b128 a[132:135], v28 offset:34816\n" \
"ds_read_b128 a[196:199], v29 offset:34816\n" \
"s_waitcnt lgkmcnt(15)\n" \
"v_mfma_f32_16x16x32_bf16 a[0:3], a[160:163], a[144:147], a[0:3]\n" \
"s_addk_i32 s84, 0x2000\n" \
"v_mfma_f32_16x16x32_bf16 a[0:3], a[224:227], a[208:211], a[0:3]\n" \
"buffer_load_dwordx4 v24, s[76:79], s84 offen offset:2048 lds\n" \
"v_mfma_f32_16x16x32_bf16 a[4:7], a[164:167], a[144:147], a[4:7]\n" \
"ds_read_b64_tr_b16 a[176:177], v36 offset:32768\n" \
"v_writelane_b32 v11, s6, 41\n" \
"v_mfma_f32_16x16x32_bf16 a[4:7], a[228:231], a[208:211], a[4:7]\n" \
"s_addk_i32 s84, 0x2000\n" \
"v_mfma_f32_16x16x32_bf16 a[12:15], a[172:175], a[144:147], a[12:15]\n" \
"buffer_load_dwordx4 v25, s[76:79], s84 offen offset:3072 lds\n" \
"s_waitcnt lgkmcnt(13)\n" \
"v_mfma_f32_16x16x32_bf16 a[12:15], a[236:239], a[208:211], a[12:15]\n" \
"ds_read_b64_tr_b16 a[178:179], v36 offset:33792\n" \
"s_add_i32 s8, s8, 0x42c0000\n" \
"s_waitcnt lgkmcnt(10)\n" \
"v_mfma_f32_16x16x32_bf16 a[8:11], a[144:147], a[168:171], a[8:11]\n" \
"ds_read_b64_tr_b16 a[240:241], v36 offset:40960\n" \
"s_lshl_b32 s6, s12, 10\n" \
"s_waitcnt lgkmcnt(7)\n" \
"v_mfma_f32_16x16x32_bf16 a[8:11], a[208:211], a[232:235], a[8:11]\n" \
"ds_read_b64_tr_b16 a[242:243], v36 offset:41984\n" \
"s_or_b32 s6, s8, s6\n" \
"v_mfma_f32_16x16x32_bf16 a[24:27], a[148:151], a[168:171], a[24:27]\n" \
"ds_read_b64_tr_b16 a[180:181], v37 offset:32768\n" \
"v_mfma_f32_16x16x32_bf16 a[24:27], a[212:215], a[232:235], a[24:27]\n" \
"ds_read_b64_tr_b16 a[182:183], v37 offset:33792\n" \
"v_mfma_f32_16x16x32_bf16 a[28:31], a[172:175], a[148:151], a[28:31]\n" \
"ds_read_b64_tr_b16 a[244:245], v37 offset:40960\n" \
"v_mfma_f32_16x16x32_bf16 a[28:31], a[236:239], a[212:215], a[28:31]\n" \
"ds_read_b64_tr_b16 a[246:247], v37 offset:41984\n" \
"v_mfma_f32_16x16x32_bf16 a[20:23], a[164:167], a[148:151], a[20:23]\n" \
"s_cmp_lg_u32 s16, 0\n" \
"s_cselect_b32 s6, s7, s6\n" \
"v_mfma_f32_16x16x32_bf16 a[20:23], a[228:231], a[212:215], a[20:23]\n" \
"s_add_u32 s88, s42, 0x3000\n" \
"buffer_store_dwordx4 v[244:247], v20, s[80:83], s88 offen offset:2048 sc1\n" \
"v_mfma_f32_16x16x32_bf16 a[16:19], a[160:163], a[148:151], a[16:19]\n" \
"ds_read_b64_tr_b16 a[184:185], v38 offset:32768\n" \
"s_mulk_i32 s4, 0x4800\n" \
"v_mfma_f32_16x16x32_bf16 a[16:19], a[224:227], a[212:215], a[16:19]\n" \
"ds_read_b64_tr_b16 a[186:187], v38 offset:33792\n" \
"v_writelane_b32 v11, s6, 42\n" \
"v_mfma_f32_16x16x32_bf16 a[32:35], a[160:163], a[152:155], a[32:35]\n" \
"ds_read_b64_tr_b16 a[248:249], v38 offset:40960\n" \
"s_add_i32 s6, s14, s10\n" \
"v_mfma_f32_16x16x32_bf16 a[32:35], a[224:227], a[216:219], a[32:35]\n" \
"ds_read_b64_tr_b16 a[250:251], v38 offset:41984\n" \
"v_writelane_b32 v11, s6, 43\n" \
"v_mfma_f32_16x16x32_bf16 a[36:39], a[164:167], a[152:155], a[36:39]\n" \
"s_add_i32 s6, s9, 0x21c000\n" \
"s_add_i32 s9, s6, s10\n" \
"v_mfma_f32_16x16x32_bf16 a[36:39], a[228:231], a[216:219], a[36:39]\n" \
"s_add_u32 s89, s42, 0x3000\n" \
"buffer_store_dwordx4 v[184:187], v20, s[80:83], s89 offen offset:1024 sc1\n" \
"v_mfma_f32_16x16x32_bf16 a[44:47], a[172:175], a[152:155], a[44:47]\n" \
"ds_read_b64_tr_b16 a[188:189], v39 offset:32768\n" \
"v_mfma_f32_16x16x32_bf16 a[44:47], a[236:239], a[216:219], a[44:47]\n" \
"ds_read_b64_tr_b16 a[190:191], v39 offset:33792\n" \
"v_mfma_f32_16x16x32_bf16 a[40:43], a[152:155], a[168:171], a[40:43]\n" \
"ds_read_b64_tr_b16 a[252:253], v39 offset:40960\n" \
"v_mfma_f32_16x16x32_bf16 a[40:43], a[216:219], a[232:235], a[40:43]\n" \
"s_cmp_lg_u32 s12, 0\n" \
"s_cselect_b32 s6, s6, s14\n" \
"v_mfma_f32_16x16x32_bf16 a[56:59], a[156:159], a[168:171], a[56:59]\n" \
"ds_read_b64_tr_b16 a[254:255], v39 offset:41984\n" \
"s_addk_i32 s6, 0x100\n" \
"v_mfma_f32_16x16x32_bf16 a[56:59], a[220:223], a[232:235], a[56:59]\n" \
"v_writelane_b32 v11, s9, 44\n" \
"v_mfma_f32_16x16x32_bf16 a[60:63], a[172:175], a[156:159], a[60:63]\n" \
"s_add_u32 s88, s42, 0x3000\n" \
"buffer_store_dwordx4 v[188:191], v20, s[80:83], s88 offen offset:0 sc1\n" \
"v_mfma_f32_16x16x32_bf16 a[60:63], a[236:239], a[220:223], a[60:63]\n" \
"s_cmp_lg_u32 s16, 0\n" \
"s_cselect_b32 s6, s7, s6\n" \
"v_mfma_f32_16x16x32_bf16 a[52:55], a[164:167], a[156:159], a[52:55]\n" \
"v_writelane_b32 v11, s6, 45\n" \
"s_or_b32 s6, s8, s15\n" \
"v_mfma_f32_16x16x32_bf16 a[52:55], a[228:231], a[220:223], a[52:55]\n" \
"v_writelane_b32 v11, s6, 53\n" \
"s_lshl_b32 s6, s3, 17\n" \
"v_mfma_f32_16x16x32_bf16 a[48:51], a[160:163], a[156:159], a[48:51]\n" \
"s_add_i32 s6, s13, s6\n" \
"s_or_b32 s5, s6, s5\n" \
"v_mfma_f32_16x16x32_bf16 a[48:51], a[224:227], a[220:223], a[48:51]\n" \
"s_or_b32 s5, s5, 0x3840\n" \
"v_writelane_b32 v11, s5, 6\n" \
"v_mfma_f32_16x16x32_bf16 a[64:67], a[164:167], a[140:143], a[64:67]\n" \
"s_mov_b32 s5, 0xff0000\n" \
"v_mfma_f32_16x16x32_bf16 a[64:67], a[228:231], a[204:207], a[64:67]\n" \
"s_cmp_lg_u32 s16, 0\n" \
"s_cselect_b32 s5, s17, s5\n" \
"v_mfma_f32_16x16x32_bf16 a[80:83], a[172:175], a[140:143], a[80:83]\n" \
"v_writelane_b32 v11, s5, 7\n" \
"s_mul_i32 s5, s3, 0x1200\n" \
"v_mfma_f32_16x16x32_bf16 a[80:83], a[236:239], a[204:207], a[80:83]\n" \
"s_add_i32 s4, s5, s4\n" \
"s_add_i32 s4, s4, s11\n" \
"v_mfma_f32_16x16x32_bf16 a[68:71], a[140:143], a[168:171], a[68:71]\n" \
"v_writelane_b32 v11, s4, 8\n" \
"s_lshl_b32 s4, s2, 16\n" \
"v_mfma_f32_16x16x32_bf16 a[68:71], a[204:207], a[232:235], a[68:71]\n" \
"s_waitcnt vmcnt(3) lgkmcnt(0)\n" \
"s_barrier\n" \
"s_nop 0\n" \
"v_mfma_f32_16x16x32_bf16 v[116:119], a[176:179], a[144:147], v[116:119]\n" \
"s_add_u32 m0, s40, 0x8000\n" \
"s_add_u32 s85, s39, 0xe000\n" \
"v_mfma_f32_16x16x32_bf16 v[116:119], a[240:243], a[208:211], v[116:119]\n" \
"buffer_load_dwordx4 v20, s[80:83], s85 offen sc1 lds\n" \
"v_mfma_f32_16x16x32_bf16 v[120:123], a[180:183], a[144:147], v[120:123]\n" \
"ds_read_b64_tr_b16 a[160:161], v32 offset:0\n" \
"s_add_i32 s5, s4, 0x10000\n" \
"v_mfma_f32_16x16x32_bf16 v[120:123], a[244:247], a[208:211], v[120:123]\n" \
"ds_read_b64_tr_b16 a[162:163], v32 offset:512\n" \
"s_and_b32 s5, s5, 0x70000\n" \
"v_mfma_f32_16x16x32_bf16 v[124:127], a[184:187], a[144:147], v[124:127]\n" \
"s_add_u32 m0, s40, 0x9000\n" \
"s_add_u32 s85, s39, 0xf000\n" \
"v_mfma_f32_16x16x32_bf16 v[124:127], a[248:251], a[208:211], v[124:127]\n" \
"buffer_load_dwordx4 v20, s[80:83], s85 offen sc1 lds\n" \
"v_mfma_f32_16x16x32_bf16 v[128:131], a[188:191], a[144:147], v[128:131]\n" \
"ds_read_b64_tr_b16 a[224:225], v32 offset:4096\n" \
"s_add_i32 s5, s13, s5\n" \
"v_mfma_f32_16x16x32_bf16 v[128:131], a[252:255], a[208:211], v[128:131]\n" \
"ds_read_b128 a[144:147], v28 offset:8192\n" \
"v_writelane_b32 v11, s5, 46\n" \
"v_mfma_f32_16x16x32_bf16 v[144:147], a[188:191], a[148:151], v[144:147]\n" \
"ds_read_b128 a[208:211], v29 offset:8192\n" \
"s_add_i32 s5, s4, 0x20000\n" \
"v_mfma_f32_16x16x32_bf16 v[144:147], a[252:255], a[212:215], v[144:147]\n" \
"s_add_u32 m0, s40, 0xb000\n" \
"s_movk_i32 s84, 0x380\n" \
"v_mfma_f32_16x16x32_bf16 v[140:143], a[184:187], a[148:151], v[140:143]\n" \
"buffer_load_dwordx4 v21, s[76:79], s84 offen offset:0 lds\n" \
"v_mfma_f32_16x16x32_bf16 v[140:143], a[248:251], a[212:215], v[140:143]\n" \
"ds_read_b64_tr_b16 a[226:227], v32 offset:4608\n" \
"s_and_b32 s5, s5, 0x70000\n" \
"v_mfma_f32_16x16x32_bf16 v[136:139], a[180:183], a[148:151], v[136:139]\n" \
"s_add_u32 m0, s30, 0x8000\n" \
"s_movk_i32 s84, 0x380\n" \
"v_mfma_f32_16x16x32_bf16 v[136:139], a[244:247], a[212:215], v[136:139]\n" \
"buffer_load_dwordx4 v24, s[76:79], s84 offen offset:0 lds\n" \
"v_mfma_f32_16x16x32_bf16 v[132:135], a[176:179], a[148:151], v[132:135]\n" \
"ds_read_b64_tr_b16 a[164:165], v33 offset:0\n" \
"s_add_i32 s5, s13, s5\n" \
"v_mfma_f32_16x16x32_bf16 v[132:135], a[240:243], a[212:215], v[132:135]\n" \
"ds_read_b128 a[148:151], v28 offset:10240\n" \
"s_bitset1_b32 s5, 11\n" \
"v_mfma_f32_16x16x32_bf16 v[148:151], a[176:179], a[152:155], v[148:151]\n" \
"ds_read_b128 a[212:215], v29 offset:10240\n" \
"v_writelane_b32 v11, s5, 47\n" \
"v_mfma_f32_16x16x32_bf16 v[148:151], a[240:243], a[216:219], v[148:151]\n" \
"s_movk_i32 s84, 0x2380\n" \
"v_mfma_f32_16x16x32_bf16 v[152:155], a[180:183], a[152:155], v[152:155]\n" \
"buffer_load_dwordx4 v25, s[76:79], s84 offen offset:1024 lds\n" \
"v_mfma_f32_16x16x32_bf16 v[152:155], a[244:247], a[216:219], v[152:155]\n" \
"ds_read_b64_tr_b16 a[166:167], v33 offset:512\n" \
"s_add_i32 s5, s4, 0x30000\n" \
"v_mfma_f32_16x16x32_bf16 v[156:159], a[184:187], a[152:155], v[156:159]\n" \
"ds_read_b64_tr_b16 a[228:229], v33 offset:4096\n" \
"s_and_b32 s5, s5, 0x70000\n" \
"v_mfma_f32_16x16x32_bf16 v[156:159], a[248:251], a[216:219], v[156:159]\n" \
"s_movk_i32 s84, 0x4380\n" \
"v_mfma_f32_16x16x32_bf16 v[160:163], a[188:191], a[152:155], v[160:163]\n" \
"buffer_load_dwordx4 v24, s[76:79], s84 offen offset:2048 lds\n" \
"v_mfma_f32_16x16x32_bf16 v[160:163], a[252:255], a[216:219], v[160:163]\n" \
"ds_read_b128 a[152:155], v28 offset:12288\n" \
"s_add_i32 s5, s13, s5\n" \
"v_mfma_f32_16x16x32_bf16 v[176:179], a[188:191], a[156:159], v[176:179]\n" \
"ds_read_b128 a[216:219], v29 offset:12288\n" \
"s_bitset1_b32 s5, 12\n" \
"v_mfma_f32_16x16x32_bf16 v[176:179], a[252:255], a[220:223], v[176:179]\n" \
"s_movk_i32 s84, 0x6380\n" \
"v_mfma_f32_16x16x32_bf16 v[172:175], a[184:187], a[156:159], v[172:175]\n" \
"buffer_load_dwordx4 v25, s[76:79], s84 offen offset:3072 lds\n" \
"v_mfma_f32_16x16x32_bf16 v[172:175], a[248:251], a[220:223], v[172:175]\n" \
"ds_read_b64_tr_b16 a[230:231], v33 offset:4608\n" \
"v_writelane_b32 v11, s5, 48\n" \
"v_mfma_f32_16x16x32_bf16 v[168:171], a[180:183], a[156:159], v[168:171]\n" \
"ds_read_b64_tr_b16 a[172:173], v35 offset:0\n" \
"s_and_b32 s5, s4, 0x70000\n" \
"v_mfma_f32_16x16x32_bf16 v[168:171], a[244:247], a[220:223], v[168:171]\n" \
"ds_read_b64_tr_b16 a[174:175], v35 offset:512\n" \
"s_xor_b32 s5, s5, 0x40000\n" \
"v_mfma_f32_16x16x32_bf16 v[164:167], a[176:179], a[156:159], v[164:167]\n" \
"s_add_i32 s5, s13, s5\n" \
"s_nop 0\n" \
"s_or_b32 s5, s5, 0x1800\n" \
"v_mfma_f32_16x16x32_bf16 v[164:167], a[240:243], a[220:223], v[164:167]\n" \
"ds_read_b128 a[156:159], v28 offset:14336\n" \
"v_writelane_b32 v11, s5, 49\n" \
"v_mfma_f32_16x16x32_bf16 v[84:87], a[176:179], a[136:139], v[84:87]\n" \
"ds_read_b128 a[220:223], v29 offset:14336\n" \
"s_add_i32 s5, s4, 0x50000\n" \
"v_mfma_f32_16x16x32_bf16 v[84:87], a[240:243], a[200:203], v[84:87]\n" \
"s_add_u32 m0, s31, 0x8000\n" \
"s_mov_b32 s84, 0x380000\n" \
"v_mfma_f32_16x16x32_bf16 v[88:91], a[180:183], a[136:139], v[88:91]\n" \
"buffer_load_dwordx4 v26, s[68:71], s84 offen offset:0 lds\n" \
"v_mfma_f32_16x16x32_bf16 v[88:91], a[244:247], a[200:203], v[88:91]\n" \
"s_addk_i32 s84, 0x7c00\n" \
"v_mfma_f32_16x16x32_bf16 v[92:95], a[184:187], a[136:139], v[92:95]\n" \
"buffer_load_dwordx4 v26, s[68:71], s84 offen offset:1024 lds\n" \
"v_mfma_f32_16x16x32_bf16 v[92:95], a[248:251], a[200:203], v[92:95]\n" \
"ds_read_b64_tr_b16 a[236:237], v35 offset:4096\n" \
"s_and_b32 s5, s5, 0x70000\n" \
"v_mfma_f32_16x16x32_bf16 v[96:99], a[188:191], a[136:139], v[96:99]\n" \
"ds_read_b64_tr_b16 a[238:239], v35 offset:4608\n" \
"s_add_i32 s5, s13, s5\n" \
"v_mfma_f32_16x16x32_bf16 v[96:99], a[252:255], a[200:203], v[96:99]\n" \
"ds_read_b128 a[136:139], v28 offset:4096\n" \
"s_bitset1_b32 s5, 13\n" \
"v_mfma_f32_16x16x32_bf16 v[112:115], a[188:191], a[140:143], v[112:115]\n" \
"ds_read_b128 a[200:203], v29 offset:4096\n" \
"v_writelane_b32 v11, s5, 50\n" \
"v_mfma_f32_16x16x32_bf16 v[112:115], a[252:255], a[204:207], v[112:115]\n" \
"s_addk_i32 s84, 0x7c00\n" \
"v_mfma_f32_16x16x32_bf16 v[108:111], a[184:187], a[140:143], v[108:111]\n" \
"buffer_load_dwordx4 v27, s[68:71], s84 offen offset:2048 lds\n" \
"v_mfma_f32_16x16x32_bf16 v[108:111], a[248:251], a[204:207], v[108:111]\n" \
"s_addk_i32 s84, 0x7c00\n" \
"s_nop 0\n" \
"v_mfma_f32_16x16x32_bf16 v[104:107], a[180:183], a[140:143], v[104:107]\n" \
"buffer_load_dwordx4 v27, s[68:71], s84 offen offset:3072 lds\n" \
"v_mfma_f32_16x16x32_bf16 v[104:107], a[244:247], a[204:207], v[104:107]\n" \
"ds_read_b64_tr_b16 a[168:169], v34 offset:0\n" \
"s_add_i32 s5, s4, 0x60000\n" \
"v_mfma_f32_16x16x32_bf16 v[100:103], a[176:179], a[140:143], v[100:103]\n" \
"ds_read_b64_tr_b16 a[170:171], v34 offset:512\n" \
"s_and_b32 s5, s5, 0x70000\n" \
"v_mfma_f32_16x16x32_bf16 v[100:103], a[240:243], a[204:207], v[100:103]\n" \
"ds_read_b128 a[140:143], v28 offset:6144\n" \
"s_add_i32 s4, s4, 0x70000\n" \
"v_mfma_f32_16x16x32_bf16 v[52:55], a[176:179], a[128:131], v[52:55]\n" \
"ds_read_b128 a[204:207], v29 offset:6144\n" \
"s_add_i32 s5, s13, s5\n" \
"v_mfma_f32_16x16x32_bf16 v[52:55], a[240:243], a[192:195], v[52:55]\n" \
"s_add_u32 m0, s30, 0x9000\n" \
"s_mov_b32 s84, 0x9380\n" \
"v_mfma_f32_16x16x32_bf16 v[56:59], a[180:183], a[128:131], v[56:59]\n" \
"buffer_load_dwordx4 v24, s[76:79], s84 offen offset:0 lds\n" \
"v_mfma_f32_16x16x32_bf16 v[56:59], a[244:247], a[192:195], v[56:59]\n" \
"s_addk_i32 s84, 0x2000\n" \
"v_mfma_f32_16x16x32_bf16 v[60:63], a[184:187], a[128:131], v[60:63]\n" \
"buffer_load_dwordx4 v25, s[76:79], s84 offen offset:1024 lds\n" \
"v_mfma_f32_16x16x32_bf16 v[60:63], a[248:251], a[192:195], v[60:63]\n" \
"ds_read_b64_tr_b16 a[232:233], v34 offset:4096\n" \
"s_and_b32 s4, s4, 0x70000\n" \
"v_mfma_f32_16x16x32_bf16 v[64:67], a[188:191], a[128:131], v[64:67]\n" \
"ds_read_b64_tr_b16 a[234:235], v34 offset:4608\n" \
"s_or_b32 s5, s5, 0x2800\n" \
"v_mfma_f32_16x16x32_bf16 v[64:67], a[252:255], a[192:195], v[64:67]\n" \
"ds_read_b128 a[128:131], v28 offset:0\n" \
"v_writelane_b32 v11, s5, 51\n" \
"v_mfma_f32_16x16x32_bf16 v[80:83], a[188:191], a[132:135], v[80:83]\n" \
"ds_read_b128 a[192:195], v29 offset:0\n" \
"s_add_i32 s13, s13, s4\n" \
"v_mfma_f32_16x16x32_bf16 v[80:83], a[252:255], a[196:199], v[80:83]\n" \
"s_or_b32 s4, s13, 0x3000\n" \
"v_writelane_b32 v11, s4, 52\n" \
"v_mfma_f32_16x16x32_bf16 v[76:79], a[184:187], a[132:135], v[76:79]\n" \
"s_movk_i32 s11, 0x4000\n" \
"s_lshl_b32 s4, s2, 2\n" \
"v_mfma_f32_16x16x32_bf16 v[76:79], a[248:251], a[196:199], v[76:79]\n" \
"s_and_b32 s4, s4, 28\n" \
"s_bfe_u32 s5, s2, 0x20003\n" \
"v_mfma_f32_16x16x32_bf16 v[72:75], a[180:183], a[132:135], v[72:75]\n" \
"s_or_b32 s4, s4, s5\n" \
"s_lshr_b32 s5, s2, 5\n" \
"v_mfma_f32_16x16x32_bf16 v[72:75], a[244:247], a[196:199], v[72:75]\n" \
"s_lshr_b32 s19, s3, 1\n" \
"s_lshl_b32 s18, s5, 1\n" \
"v_mfma_f32_16x16x32_bf16 v[68:71], a[176:179], a[132:135], v[68:71]\n" \
"s_lshl_b32 s6, s4, 20\n" \
"s_lshl_b32 s7, s4, 8\n" \
"v_mfma_f32_16x16x32_bf16 v[68:71], a[240:243], a[196:199], v[68:71]\n" \
"ds_read_b128 a[132:135], v28 offset:2048\n" \
"ds_read_b128 a[196:199], v29 offset:2048\n" \
"s_waitcnt lgkmcnt(15)\n" \
"v_mfma_f32_16x16x32_bf16 a[0:3], a[160:163], a[144:147], a[0:3]\n" \
"s_addk_i32 s84, 0x2000\n" \
"v_mfma_f32_16x16x32_bf16 a[0:3], a[224:227], a[208:211], a[0:3]\n" \
"buffer_load_dwordx4 v24, s[76:79], s84 offen offset:2048 lds\n" \
"v_mfma_f32_16x16x32_bf16 a[4:7], a[164:167], a[144:147], a[4:7]\n" \
"ds_read_b64_tr_b16 a[176:177], v36 offset:0\n" \
"s_and_b32 s13, s3, 1\n" \
"v_mfma_f32_16x16x32_bf16 a[4:7], a[228:231], a[208:211], a[4:7]\n" \
"s_addk_i32 s84, 0x2000\n" \
"v_mfma_f32_16x16x32_bf16 a[12:15], a[172:175], a[144:147], a[12:15]\n" \
"buffer_load_dwordx4 v25, s[76:79], s84 offen offset:3072 lds\n" \
"s_waitcnt lgkmcnt(13)\n" \
"v_mfma_f32_16x16x32_bf16 a[12:15], a[236:239], a[208:211], a[12:15]\n" \
"ds_read_b64_tr_b16 a[178:179], v36 offset:1024\n" \
"s_add_i32 s14, s18, s19\n" \
"s_waitcnt lgkmcnt(10)\n" \
"v_mfma_f32_16x16x32_bf16 a[8:11], a[144:147], a[168:171], a[8:11]\n" \
"ds_read_b64_tr_b16 a[240:241], v36 offset:8192\n" \
"s_waitcnt lgkmcnt(7)\n" \
"v_mfma_f32_16x16x32_bf16 a[8:11], a[208:211], a[232:235], a[8:11]\n" \
"ds_read_b64_tr_b16 a[242:243], v36 offset:9216\n" \
"v_mfma_f32_16x16x32_bf16 a[24:27], a[148:151], a[168:171], a[24:27]\n" \
"ds_read_b64_tr_b16 a[180:181], v37 offset:0\n" \
"v_mfma_f32_16x16x32_bf16 a[24:27], a[212:215], a[232:235], a[24:27]\n" \
"ds_read_b64_tr_b16 a[182:183], v37 offset:1024\n" \
"v_mfma_f32_16x16x32_bf16 a[28:31], a[172:175], a[148:151], a[28:31]\n" \
"ds_read_b64_tr_b16 a[244:245], v37 offset:8192\n" \
"v_mfma_f32_16x16x32_bf16 a[28:31], a[236:239], a[212:215], a[28:31]\n" \
"ds_read_b64_tr_b16 a[246:247], v37 offset:9216\n" \
"v_mfma_f32_16x16x32_bf16 a[20:23], a[164:167], a[148:151], a[20:23]\n" \
"s_add_u32 s6, s26, s6\n" \
"s_addc_u32 s9, s27, 0\n" \
"v_mfma_f32_16x16x32_bf16 a[20:23], a[228:231], a[212:215], a[20:23]\n" \
"s_add_u32 s89, s42, 0x0\n" \
"s_nop 0\n" \
"buffer_store_dwordx4 v[192:195], v20, s[80:83], s89 offen offset:2048 sc1\n" \
"v_mfma_f32_16x16x32_bf16 a[16:19], a[160:163], a[148:151], a[16:19]\n" \
"ds_read_b64_tr_b16 a[184:185], v38 offset:0\n" \
"v_mfma_f32_16x16x32_bf16 a[16:19], a[224:227], a[212:215], a[16:19]\n" \
"ds_read_b64_tr_b16 a[186:187], v38 offset:1024\n" \
"v_mfma_f32_16x16x32_bf16 a[32:35], a[160:163], a[152:155], a[32:35]\n" \
"ds_read_b64_tr_b16 a[248:249], v38 offset:8192\n" \
"v_mfma_f32_16x16x32_bf16 a[32:35], a[224:227], a[216:219], a[32:35]\n" \
"ds_read_b64_tr_b16 a[250:251], v38 offset:9216\n" \
"v_mfma_f32_16x16x32_bf16 a[36:39], a[164:167], a[152:155], a[36:39]\n" \
"s_add_u32 s8, s6, 0x1000000\n" \
"s_addc_u32 s6, s9, 0\n" \
"v_mfma_f32_16x16x32_bf16 a[36:39], a[228:231], a[216:219], a[36:39]\n" \
"s_add_u32 s88, s42, 0x0\n" \
"buffer_store_dwordx4 v[196:199], v20, s[80:83], s88 offen offset:3072 sc1\n" \
"v_mfma_f32_16x16x32_bf16 a[44:47], a[172:175], a[152:155], a[44:47]\n" \
"ds_read_b64_tr_b16 a[188:189], v39 offset:0\n" \
"s_and_b32 s9, s6, 0xffff\n" \
"v_mfma_f32_16x16x32_bf16 a[44:47], a[236:239], a[216:219], a[44:47]\n" \
"ds_read_b64_tr_b16 a[190:191], v39 offset:1024\n" \
"s_lshl_b32 s6, s5, 16\n" \
"v_mfma_f32_16x16x32_bf16 a[40:43], a[152:155], a[168:171], a[40:43]\n" \
"ds_read_b64_tr_b16 a[252:253], v39 offset:8192\n" \
"s_sub_i32 s10, 0x100000, s6\n" \
"v_mfma_f32_16x16x32_bf16 a[40:43], a[216:219], a[232:235], a[40:43]\n" \
"s_mov_b32 s6, 0x8000\n" \
"v_mfma_f32_16x16x32_bf16 a[56:59], a[156:159], a[168:171], a[56:59]\n" \
"ds_read_b64_tr_b16 a[254:255], v39 offset:9216\n" \
"v_mfma_f32_16x16x32_bf16 a[56:59], a[220:223], a[232:235], a[56:59]\n" \
"s_cmp_lg_u32 s5, 0\n" \
"s_cselect_b32 s17, s11, s6\n" \
"v_mfma_f32_16x16x32_bf16 a[60:63], a[172:175], a[156:159], a[60:63]\n" \
"s_add_u32 s89, s42, 0x2000\n" \
"buffer_store_dwordx4 v[200:203], v20, s[80:83], s89 offen offset:0 sc1\n" \
"v_mfma_f32_16x16x32_bf16 a[60:63], a[236:239], a[220:223], a[60:63]\n" \
"s_lshl_b32 s6, s3, 1\n" \
"s_lshl_b32 s5, s5, 3\n" \
"v_mfma_f32_16x16x32_bf16 a[52:55], a[164:167], a[156:159], a[52:55]\n" \
"s_and_b32 s6, s6, 4\n" \
"s_or_b32 s12, s5, s6\n" \
"v_mfma_f32_16x16x32_bf16 a[52:55], a[228:231], a[220:223], a[52:55]\n" \
"s_sub_i32 s5, s13, s5\n" \
"s_or_b32 s5, s5, s6\n" \
"v_mfma_f32_16x16x32_bf16 a[48:51], a[160:163], a[156:159], a[48:51]\n" \
"s_or_b32 s6, s12, s13\n" \
"s_lshl_b32 s4, s4, 2\n" \
"v_mfma_f32_16x16x32_bf16 a[48:51], a[224:227], a[220:223], a[48:51]\n" \
"s_mulk_i32 s6, 0x92\n" \
"s_add_i32 s4, s4, 18\n" \
"v_mfma_f32_16x16x32_bf16 a[64:67], a[164:167], a[140:143], a[64:67]\n" \
"s_mulk_i32 s5, 0x92\n" \
"s_add_i32 s6, s4, s6\n" \
"v_mfma_f32_16x16x32_bf16 a[64:67], a[228:231], a[204:207], a[64:67]\n" \
"s_add_i32 s5, s5, s4\n" \
"s_lshl_b32 s4, s3, 11\n" \
"v_mfma_f32_16x16x32_bf16 a[80:83], a[172:175], a[140:143], a[80:83]\n" \
"s_lshl_b32 s15, s13, 12\n" \
"s_lshl_b32 s16, s19, 14\n" \
"v_mfma_f32_16x16x32_bf16 a[80:83], a[236:239], a[204:207], a[80:83]\n" \
"s_mul_i32 s19, s19, 0xe000\n" \
"s_mov_b32 s11, 0x20000\n" \
"v_mfma_f32_16x16x32_bf16 a[68:71], a[140:143], a[168:171], a[68:71]\n" \
"s_or_b32 s12, s4, 0xc000\n" \
"s_lshl_b32 s4, s3, 8\n" \
"v_mfma_f32_16x16x32_bf16 a[68:71], a[204:207], a[232:235], a[68:71]\n" \
"s_waitcnt vmcnt(3) lgkmcnt(0)\n" \
"s_barrier\n" \
"v_mfma_f32_16x16x32_bf16 v[116:119], a[176:179], a[144:147], v[116:119]\n" \
"s_add_u32 m0, s40, 0x0\n" \
"s_add_u32 s85, s39, 0x10000\n" \
"v_mfma_f32_16x16x32_bf16 v[116:119], a[240:243], a[208:211], v[116:119]\n" \
"buffer_load_dwordx4 v20, s[80:83], s85 offen sc1 lds\n" \
"v_mfma_f32_16x16x32_bf16 v[120:123], a[180:183], a[144:147], v[120:123]\n" \
"ds_read_b64_tr_b16 a[160:161], v32 offset:32768\n" \
"s_or_b32 s22, s15, s16\n" \
"v_mfma_f32_16x16x32_bf16 v[120:123], a[244:247], a[208:211], v[120:123]\n" \
"ds_read_b64_tr_b16 a[162:163], v32 offset:33280\n" \
"s_or_b32 s23, s19, s15\n" \
"v_mfma_f32_16x16x32_bf16 v[124:127], a[184:187], a[144:147], v[124:127]\n" \
"s_add_u32 m0, s40, 0x1000\n" \
"s_add_u32 s85, s39, 0x11000\n" \
"v_mfma_f32_16x16x32_bf16 v[124:127], a[248:251], a[208:211], v[124:127]\n" \
"buffer_load_dwordx4 v20, s[80:83], s85 offen sc1 lds\n" \
"v_mfma_f32_16x16x32_bf16 v[128:131], a[188:191], a[144:147], v[128:131]\n" \
"ds_read_b64_tr_b16 a[224:225], v32 offset:36864\n" \
"s_lshl_b32 s60, s6, 10\n" \
"v_mfma_f32_16x16x32_bf16 v[128:131], a[252:255], a[208:211], v[128:131]\n" \
"ds_read_b128 a[144:147], v28 offset:40960\n" \
"s_lshl_b32 s61, s5, 10\n" \
"v_mfma_f32_16x16x32_bf16 v[144:147], a[188:191], a[148:151], v[144:147]\n" \
"ds_read_b128 a[208:211], v29 offset:40960\n" \
"s_lshl_b32 s14, s14, 15\n" \
"v_mfma_f32_16x16x32_bf16 v[144:147], a[252:255], a[212:215], v[144:147]\n" \
"s_add_u32 m0, s40, 0x3000\n" \
"s_movk_i32 s84, 0x400\n" \
"v_mfma_f32_16x16x32_bf16 v[140:143], a[184:187], a[148:151], v[140:143]\n" \
"buffer_load_dwordx4 v21, s[76:79], s84 offen offset:0 lds\n" \
"v_mfma_f32_16x16x32_bf16 v[140:143], a[248:251], a[212:215], v[140:143]\n" \
"ds_read_b64_tr_b16 a[226:227], v32 offset:37376\n" \
"s_or_b32 s20, s4, 0x26c00\n" \
"v_mfma_f32_16x16x32_bf16 v[136:139], a[180:183], a[148:151], v[136:139]\n" \
"s_add_u32 m0, s30, 0x0\n" \
"s_movk_i32 s84, 0x400\n" \
"v_mfma_f32_16x16x32_bf16 v[136:139], a[244:247], a[212:215], v[136:139]\n" \
"buffer_load_dwordx4 v24, s[76:79], s84 offen offset:0 lds\n" \
"v_mfma_f32_16x16x32_bf16 v[132:135], a[176:179], a[148:151], v[132:135]\n" \
"ds_read_b64_tr_b16 a[164:165], v33 offset:32768\n" \
"s_or_b32 s13, s22, 0x8000\n" \
"v_mfma_f32_16x16x32_bf16 v[132:135], a[240:243], a[212:215], v[132:135]\n" \
"ds_read_b128 a[148:151], v28 offset:43008\n" \
"s_add_i32 s22, s22, 0x14000\n" \
"v_mfma_f32_16x16x32_bf16 v[148:151], a[176:179], a[152:155], v[148:151]\n" \
"ds_read_b128 a[212:215], v29 offset:43008\n" \
"s_bitset1_b32 s23, 16\n" \
"v_mfma_f32_16x16x32_bf16 v[148:151], a[240:243], a[216:219], v[148:151]\n" \
"s_movk_i32 s84, 0x2400\n" \
"v_mfma_f32_16x16x32_bf16 v[152:155], a[180:183], a[152:155], v[152:155]\n" \
"buffer_load_dwordx4 v25, s[76:79], s84 offen offset:1024 lds\n" \
"v_mfma_f32_16x16x32_bf16 v[152:155], a[244:247], a[216:219], v[152:155]\n" \
"ds_read_b64_tr_b16 a[166:167], v33 offset:33280\n" \
"s_or_b32 s24, s7, 0x5be8020\n" \
"v_mfma_f32_16x16x32_bf16 v[156:159], a[184:187], a[152:155], v[156:159]\n" \
"ds_read_b64_tr_b16 a[228:229], v33 offset:36864\n" \
"s_and_b32 s57, s27, 0xffff\n" \
"v_mfma_f32_16x16x32_bf16 v[156:159], a[248:251], a[216:219], v[156:159]\n" \
"s_movk_i32 s84, 0x4400\n" \
"v_mfma_f32_16x16x32_bf16 v[160:163], a[188:191], a[152:155], v[160:163]\n" \
"buffer_load_dwordx4 v24, s[76:79], s84 offen offset:2048 lds\n" \
"v_mfma_f32_16x16x32_bf16 v[160:163], a[252:255], a[216:219], v[160:163]\n" \
"ds_read_b128 a[152:155], v28 offset:45056\n" \
"s_mov_b32 s58, 0x6000000\n" \
"v_mfma_f32_16x16x32_bf16 v[176:179], a[188:191], a[156:159], v[176:179]\n" \
"ds_read_b128 a[216:219], v29 offset:45056\n" \
"s_mov_b32 s56, s26\n" \
"v_mfma_f32_16x16x32_bf16 v[176:179], a[252:255], a[220:223], v[176:179]\n" \
"s_movk_i32 s84, 0x6400\n" \
".fill (L_pb + 73440 - .) / 4, 4, 0xBF800000\n" \
"L_pp18:\n" \
"s_cbranch_execz L_pret\n" \
"v_mfma_f32_16x16x32_bf16 v[172:175], a[184:187], a[156:159], v[172:175]\n" \
"buffer_load_dwordx4 v25, s[76:79], s84 offen offset:3072 lds\n" \
"v_mfma_f32_16x16x32_bf16 v[172:175], a[248:251], a[220:223], v[172:175]\n" \
"ds_read_b64_tr_b16 a[230:231], v33 offset:37376\n" \
"s_mov_b32 s59, s11\n" \
"v_mfma_f32_16x16x32_bf16 v[168:171], a[180:183], a[156:159], v[168:171]\n" \
"ds_read_b64_tr_b16 a[172:173], v35 offset:32768\n" \
"s_add_i32 s60, s60, 0x3000000\n" \
"v_mfma_f32_16x16x32_bf16 v[168:171], a[244:247], a[220:223], v[168:171]\n" \
"ds_read_b64_tr_b16 a[174:175], v35 offset:33280\n" \
"s_add_i32 s61, s61, 0x411c000\n" \
"v_mfma_f32_16x16x32_bf16 v[164:167], a[176:179], a[156:159], v[164:167]\n" \
"s_sub_i32 s19, 14, s18\n" \
"s_mov_b32 s21, 0x71800000\n" \
"v_mfma_f32_16x16x32_bf16 v[164:167], a[240:243], a[220:223], v[164:167]\n" \
"ds_read_b128 a[156:159], v28 offset:47104\n" \
"s_mov_b32 s4, L_gsw-L_gtr\n" \
"v_mfma_f32_16x16x32_bf16 v[84:87], a[176:179], a[136:139], v[84:87]\n" \
"ds_read_b128 a[220:223], v29 offset:47104\n" \
"v_mfma_f32_16x16x32_bf16 v[84:87], a[240:243], a[200:203], v[84:87]\n" \
"s_add_u32 m0, s31, 0x0\n" \
"s_mov_b32 s84, 0x400000\n" \
"v_mfma_f32_16x16x32_bf16 v[88:91], a[180:183], a[136:139], v[88:91]\n" \
"buffer_load_dwordx4 v26, s[68:71], s84 offen offset:0 lds\n" \
"v_mfma_f32_16x16x32_bf16 v[88:91], a[244:247], a[200:203], v[88:91]\n" \
"s_addk_i32 s84, 0x7c00\n" \
"s_nop 0\n" \
"v_mfma_f32_16x16x32_bf16 v[92:95], a[184:187], a[136:139], v[92:95]\n" \
"buffer_load_dwordx4 v26, s[68:71], s84 offen offset:1024 lds\n" \
"v_mfma_f32_16x16x32_bf16 v[92:95], a[248:251], a[200:203], v[92:95]\n" \
"ds_read_b64_tr_b16 a[236:237], v35 offset:36864\n" \
"v_mfma_f32_16x16x32_bf16 v[96:99], a[188:191], a[136:139], v[96:99]\n" \
"ds_read_b64_tr_b16 a[238:239], v35 offset:37376\n" \
"v_mfma_f32_16x16x32_bf16 v[96:99], a[252:255], a[200:203], v[96:99]\n" \
"ds_read_b128 a[136:139], v28 offset:36864\n" \
"v_mfma_f32_16x16x32_bf16 v[112:115], a[188:191], a[140:143], v[112:115]\n" \
"ds_read_b128 a[200:203], v29 offset:36864\n" \
"v_mfma_f32_16x16x32_bf16 v[112:115], a[252:255], a[204:207], v[112:115]\n" \
"s_addk_i32 s84, 0x7c00\n" \
"v_mfma_f32_16x16x32_bf16 v[108:111], a[184:187], a[140:143], v[108:111]\n" \
"buffer_load_dwordx4 v27, s[68:71], s84 offen offset:2048 lds\n" \
"v_mfma_f32_16x16x32_bf16 v[108:111], a[248:251], a[204:207], v[108:111]\n" \
"s_addk_i32 s84, 0x7c00\n" \
"v_mfma_f32_16x16x32_bf16 v[104:107], a[180:183], a[140:143], v[104:107]\n" \
"buffer_load_dwordx4 v27, s[68:71], s84 offen offset:3072 lds\n" \
"v_mfma_f32_16x16x32_bf16 v[104:107], a[244:247], a[204:207], v[104:107]\n" \
"ds_read_b64_tr_b16 a[168:169], v34 offset:32768\n" \
"v_mfma_f32_16x16x32_bf16 v[100:103], a[176:179], a[140:143], v[100:103]\n" \
"ds_read_b64_tr_b16 a[170:171], v34 offset:33280\n" \
"v_mfma_f32_16x16x32_bf16 v[100:103], a[240:243], a[204:207], v[100:103]\n" \
"ds_read_b128 a[140:143], v28 offset:38912\n" \
"v_mfma_f32_16x16x32_bf16 v[52:55], a[176:179], a[128:131], v[52:55]\n" \
"ds_read_b128 a[204:207], v29 offset:38912\n" \
"v_mfma_f32_16x16x32_bf16 v[52:55], a[240:243], a[192:195], v[52:55]\n" \
"s_add_u32 m0, s30, 0x1000\n" \
"s_mov_b32 s84, 0x9400\n" \
"v_mfma_f32_16x16x32_bf16 v[56:59], a[180:183], a[128:131], v[56:59]\n" \
"buffer_load_dwordx4 v24, s[76:79], s84 offen offset:0 lds\n" \
"v_mfma_f32_16x16x32_bf16 v[56:59], a[244:247], a[192:195], v[56:59]\n" \
"s_addk_i32 s84, 0x2000\n" \
"s_nop 0\n" \
"v_mfma_f32_16x16x32_bf16 v[60:63], a[184:187], a[128:131], v[60:63]\n" \
"buffer_load_dwordx4 v25, s[76:79], s84 offen offset:1024 lds\n" \
"v_mfma_f32_16x16x32_bf16 v[60:63], a[248:251], a[192:195], v[60:63]\n" \
"ds_read_b64_tr_b16 a[232:233], v34 offset:36864\n" \
"v_mfma_f32_16x16x32_bf16 v[64:67], a[188:191], a[128:131], v[64:67]\n" \
"ds_read_b64_tr_b16 a[234:235], v34 offset:37376\n" \
"v_mfma_f32_16x16x32_bf16 v[64:67], a[252:255], a[192:195], v[64:67]\n" \
"ds_read_b128 a[128:131], v28 offset:32768\n" \
"v_mfma_f32_16x16x32_bf16 v[80:83], a[188:191], a[132:135], v[80:83]\n" \
"ds_read_b128 a[192:195], v29 offset:32768\n" \
"v_mfma_f32_16x16x32_bf16 v[80:83], a[252:255], a[196:199], v[80:83]\n" \
"s_cmp_eq_u32 s19, 0\n" \
"s_cselect_b32 s4, L_gs7-L_gtr, s4\n" \
"v_mfma_f32_16x16x32_bf16 v[76:79], a[184:187], a[132:135], v[76:79]\n" \
"s_cmp_eq_u32 s16, 0\n" \
"s_cselect_b32 s4, 0, s4\n" \
"v_mfma_f32_16x16x32_bf16 v[76:79], a[248:251], a[196:199], v[76:79]\n" \
"s_getpc_b64 s[28:29]\n" \
"L_gp1_%=:\n" \
"s_add_u32 s28, s28, L_gtr-L_gp1_%=\n" \
"s_addc_u32 s29, s29, 0\n" \
"v_mfma_f32_16x16x32_bf16 v[72:75], a[180:183], a[132:135], v[72:75]\n" \
"s_add_u32 s28, s28, s4\n" \
"s_addc_u32 s29, s29, 0\n" \
"v_mfma_f32_16x16x32_bf16 v[72:75], a[244:247], a[196:199], v[72:75]\n" \
"v_mfma_f32_16x16x32_bf16 v[68:71], a[176:179], a[132:135], v[68:71]\n" \
"v_mfma_f32_16x16x32_bf16 v[68:71], a[240:243], a[196:199], v[68:71]\n" \
"ds_read_b128 a[132:135], v28 offset:34816\n" \
"ds_read_b128 a[196:199], v29 offset:34816\n" \
"s_waitcnt lgkmcnt(15)\n" \
"v_mfma_f32_16x16x32_bf16 a[0:3], a[160:163], a[144:147], a[0:3]\n" \
"s_add_u32 m0, s30, 0x1000\n" \
"s_mov_b32 s84, 0xd400\n" \
"v_mfma_f32_16x16x32_bf16 a[0:3], a[224:227], a[208:211], a[0:3]\n" \
"buffer_load_dwordx4 v24, s[76:79], s84 offen offset:2048 lds\n" \
"v_mfma_f32_16x16x32_bf16 a[4:7], a[164:167], a[144:147], a[4:7]\n" \
"ds_read_b64_tr_b16 a[176:177], v36 offset:32768\n" \
"v_mfma_f32_16x16x32_bf16 a[4:7], a[228:231], a[208:211], a[4:7]\n" \
"s_addk_i32 s84, 0x2000\n" \
"v_mfma_f32_16x16x32_bf16 a[12:15], a[172:175], a[144:147], a[12:15]\n" \
"buffer_load_dwordx4 v25, s[76:79], s84 offen offset:3072 lds\n" \
"s_waitcnt lgkmcnt(13)\n" \
"v_mfma_f32_16x16x32_bf16 a[12:15], a[236:239], a[208:211], a[12:15]\n" \
"ds_read_b64_tr_b16 a[178:179], v36 offset:33792\n" \
"s_waitcnt lgkmcnt(10)\n" \
"v_mfma_f32_16x16x32_bf16 a[8:11], a[144:147], a[168:171], a[8:11]\n" \
"ds_read_b64_tr_b16 a[240:241], v36 offset:40960\n" \
"s_waitcnt lgkmcnt(7)\n" \
"v_mfma_f32_16x16x32_bf16 a[8:11], a[208:211], a[232:235], a[8:11]\n" \
"ds_read_b64_tr_b16 a[242:243], v36 offset:41984\n" \
"v_mfma_f32_16x16x32_bf16 a[24:27], a[148:151], a[168:171], a[24:27]\n" \
"ds_read_b64_tr_b16 a[180:181], v37 offset:32768\n" \
"v_mfma_f32_16x16x32_bf16 a[24:27], a[212:215], a[232:235], a[24:27]\n" \
"ds_read_b64_tr_b16 a[182:183], v37 offset:33792\n" \
"v_mfma_f32_16x16x32_bf16 a[28:31], a[172:175], a[148:151], a[28:31]\n" \
"ds_read_b64_tr_b16 a[244:245], v37 offset:40960\n" \
"v_mfma_f32_16x16x32_bf16 a[28:31], a[236:239], a[212:215], a[28:31]\n" \
"ds_read_b64_tr_b16 a[246:247], v37 offset:41984\n" \
"v_mfma_f32_16x16x32_bf16 a[20:23], a[164:167], a[148:151], a[20:23]\n" \
"v_mfma_f32_16x16x32_bf16 a[20:23], a[228:231], a[212:215], a[20:23]\n" \
"s_add_u32 s88, s42, 0x2000\n" \
"buffer_store_dwordx4 v[204:207], v20, s[80:83], s88 offen offset:1024 sc1\n" \
"v_mfma_f32_16x16x32_bf16 a[16:19], a[160:163], a[148:151], a[16:19]\n" \
"ds_read_b64_tr_b16 a[184:185], v38 offset:32768\n" \
"v_mfma_f32_16x16x32_bf16 a[16:19], a[224:227], a[212:215], a[16:19]\n" \
"ds_read_b64_tr_b16 a[186:187], v38 offset:33792\n" \
"v_mfma_f32_16x16x32_bf16 a[32:35], a[160:163], a[152:155], a[32:35]\n" \
"ds_read_b64_tr_b16 a[248:249], v38 offset:40960\n" \
"v_mfma_f32_16x16x32_bf16 a[32:35], a[224:227], a[216:219], a[32:35]\n" \
"ds_read_b64_tr_b16 a[250:251], v38 offset:41984\n" \
"v_mfma_f32_16x16x32_bf16 a[36:39], a[164:167], a[152:155], a[36:39]\n" \
"v_lshlrev_b32_e32 v45, 3, v20\n" \
"v_add_u32_e32 v45, s30, v45\n" \
"v_mfma_f32_16x16x32_bf16 a[36:39], a[228:231], a[216:219], a[36:39]\n" \
"s_add_u32 s89, s42, 0x2000\n" \
"buffer_store_dwordx4 v[208:211], v20, s[80:83], s89 offen offset:2048 sc1\n" \
"s_getpc_b64 s[86:87]\n" \
"L_ct1_%=:\n" \
"s_add_u32 s86, s86, L_ap0-L_ct1_%=\n" \
"s_addc_u32 s87, s87, 0\n" \
"s_add_u32 m0, s40, 0x2000\n" \
"v_mfma_f32_16x16x32_bf16 a[44:47], a[172:175], a[152:155], a[44:47]\n" \
"global_load_lds_dword v45, s[86:87]\n" \
"v_mfma_f32_16x16x32_bf16 a[44:47], a[236:239], a[216:219], a[44:47]\n" \
"ds_read_b64_tr_b16 a[188:189], v39 offset:32768\n" \
"v_mfma_f32_16x16x32_bf16 a[40:43], a[152:155], a[168:171], a[40:43]\n" \
"ds_read_b64_tr_b16 a[190:191], v39 offset:33792\n" \
"v_mfma_f32_16x16x32_bf16 a[40:43], a[216:219], a[232:235], a[40:43]\n" \
"ds_read_b64_tr_b16 a[252:253], v39 offset:40960\n" \
"v_mfma_f32_16x16x32_bf16 a[56:59], a[156:159], a[168:171], a[56:59]\n" \
"ds_read_b64_tr_b16 a[254:255], v39 offset:41984\n" \
"v_mfma_f32_16x16x32_bf16 a[56:59], a[220:223], a[232:235], a[56:59]\n" \
"v_mfma_f32_16x16x32_bf16 a[60:63], a[172:175], a[156:159], a[60:63]\n" \
"s_add_u32 s88, s42, 0x2000\n" \
"buffer_store_dwordx4 v[212:215], v20, s[80:83], s88 offen offset:3072 sc1\n" \
"v_mfma_f32_16x16x32_bf16 a[60:63], a[236:239], a[220:223], a[60:63]\n" \
"v_mfma_f32_16x16x32_bf16 a[52:55], a[164:167], a[156:159], a[52:55]\n" \
"v_mfma_f32_16x16x32_bf16 a[52:55], a[228:231], a[220:223], a[52:55]\n" \
"v_mfma_f32_16x16x32_bf16 a[48:51], a[160:163], a[156:159], a[48:51]\n" \
"v_mfma_f32_16x16x32_bf16 a[48:51], a[224:227], a[220:223], a[48:51]\n" \
"v_mfma_f32_16x16x32_bf16 a[64:67], a[164:167], a[140:143], a[64:67]\n" \
"v_mfma_f32_16x16x32_bf16 a[64:67], a[228:231], a[204:207], a[64:67]\n" \
"v_mfma_f32_16x16x32_bf16 a[80:83], a[172:175], a[140:143], a[80:83]\n" \
"v_mfma_f32_16x16x32_bf16 a[80:83], a[236:239], a[204:207], a[80:83]\n" \
"v_mfma_f32_16x16x32_bf16 a[68:71], a[140:143], a[168:171], a[68:71]\n" \
"v_mfma_f32_16x16x32_bf16 a[68:71], a[204:207], a[232:235], a[68:71]\n" \
"s_waitcnt vmcnt(4) lgkmcnt(0)\n" \
"s_barrier\n" \
"v_mfma_f32_16x16x32_bf16 v[116:119], a[176:179], a[144:147], v[116:119]\n" \
"ds_read_b64_tr_b16 a[160:161], v32 offset:0\n" \
"v_accvgpr_read_b32 v184, a0\n" \
"v_mfma_f32_16x16x32_bf16 v[116:119], a[240:243], a[208:211], v[116:119]\n" \
"v_accvgpr_read_b32 v185, a1\n" \
"v_accvgpr_read_b32 v186, a2\n" \
"v_mfma_f32_16x16x32_bf16 v[120:123], a[180:183], a[144:147], v[120:123]\n" \
"v_accvgpr_read_b32 v187, a3\n" \
"v_accvgpr_read_b32 v188, a4\n" \
"v_mfma_f32_16x16x32_bf16 v[120:123], a[244:247], a[208:211], v[120:123]\n" \
"ds_read_b64_tr_b16 a[162:163], v32 offset:512\n" \
"v_accvgpr_read_b32 v189, a5\n" \
"v_mfma_f32_16x16x32_bf16 v[124:127], a[184:187], a[144:147], v[124:127]\n" \
"v_accvgpr_read_b32 v190, a6\n" \
"v_accvgpr_read_b32 v191, a7\n" \
"v_mfma_f32_16x16x32_bf16 v[124:127], a[248:251], a[208:211], v[124:127]\n" \
"v_accvgpr_read_b32 v196, a12\n" \
"v_accvgpr_read_b32 v197, a13\n" \
"v_mfma_f32_16x16x32_bf16 v[128:131], a[188:191], a[144:147], v[128:131]\n" \
"ds_read_b64_tr_b16 a[224:225], v32 offset:4096\n" \
"v_accvgpr_read_b32 v198, a14\n" \
"v_mfma_f32_16x16x32_bf16 v[128:131], a[252:255], a[208:211], v[128:131]\n" \
"ds_read_b128 a[144:147], v28 offset:8192\n" \
"v_accvgpr_read_b32 v199, a15\n" \
"v_mfma_f32_16x16x32_bf16 v[144:147], a[188:191], a[148:151], v[144:147]\n" \
"ds_read_b128 a[208:211], v29 offset:8192\n" \
"v_accvgpr_read_b32 v192, a8\n" \
"v_mfma_f32_16x16x32_bf16 v[144:147], a[252:255], a[212:215], v[144:147]\n" \
"ds_read_b64_tr_b16 a[226:227], v32 offset:4608\n" \
"v_accvgpr_read_b32 v193, a9\n" \
"v_mfma_f32_16x16x32_bf16 v[140:143], a[184:187], a[148:151], v[140:143]\n" \
"v_accvgpr_read_b32 v194, a10\n" \
"v_accvgpr_read_b32 v195, a11\n" \
"v_mfma_f32_16x16x32_bf16 v[140:143], a[248:251], a[212:215], v[140:143]\n" \
"v_accvgpr_read_b32 v208, a24\n" \
"v_accvgpr_read_b32 v209, a25\n" \
"v_mfma_f32_16x16x32_bf16 v[136:139], a[180:183], a[148:151], v[136:139]\n" \
"v_accvgpr_read_b32 v210, a26\n" \
"v_accvgpr_read_b32 v211, a27\n" \
"v_mfma_f32_16x16x32_bf16 v[136:139], a[244:247], a[212:215], v[136:139]\n" \
"ds_read_b64_tr_b16 a[164:165], v33 offset:0\n" \
"v_accvgpr_read_b32 v212, a28\n" \
"v_mfma_f32_16x16x32_bf16 v[132:135], a[176:179], a[148:151], v[132:135]\n" \
"v_accvgpr_read_b32 v213, a29\n" \
"v_accvgpr_read_b32 v214, a30\n" \
"v_mfma_f32_16x16x32_bf16 v[132:135], a[240:243], a[212:215], v[132:135]\n" \
"ds_read_b128 a[148:151], v28 offset:10240\n" \
"v_accvgpr_read_b32 v215, a31\n" \
"v_mfma_f32_16x16x32_bf16 v[148:151], a[176:179], a[152:155], v[148:151]\n" \
"ds_read_b128 a[212:215], v29 offset:10240\n" \
"v_accvgpr_read_b32 v204, a20\n" \
"v_mfma_f32_16x16x32_bf16 v[148:151], a[240:243], a[216:219], v[148:151]\n" \
"ds_read_b64_tr_b16 a[166:167], v33 offset:512\n" \
"v_accvgpr_read_b32 v205, a21\n" \
"v_mfma_f32_16x16x32_bf16 v[152:155], a[180:183], a[152:155], v[152:155]\n" \
"v_accvgpr_read_b32 v206, a22\n" \
"v_accvgpr_read_b32 v207, a23\n" \
"v_mfma_f32_16x16x32_bf16 v[152:155], a[244:247], a[216:219], v[152:155]\n" \
"ds_read_b64_tr_b16 a[228:229], v33 offset:4096\n" \
"v_accvgpr_read_b32 v200, a16\n" \
"v_mfma_f32_16x16x32_bf16 v[156:159], a[184:187], a[152:155], v[156:159]\n" \
"v_accvgpr_read_b32 v201, a17\n" \
"v_accvgpr_read_b32 v202, a18\n" \
"v_mfma_f32_16x16x32_bf16 v[156:159], a[248:251], a[216:219], v[156:159]\n" \
"v_accvgpr_read_b32 v203, a19\n" \
"v_accvgpr_read_b32 v216, a32\n" \
"v_mfma_f32_16x16x32_bf16 v[160:163], a[188:191], a[152:155], v[160:163]\n" \
"ds_read_b64_tr_b16 a[230:231], v33 offset:4608\n" \
"v_accvgpr_read_b32 v217, a33\n" \
"v_mfma_f32_16x16x32_bf16 v[160:163], a[252:255], a[216:219], v[160:163]\n" \
"ds_read_b128 a[152:155], v28 offset:12288\n" \
"v_accvgpr_read_b32 v218, a34\n" \
"v_mfma_f32_16x16x32_bf16 v[176:179], a[188:191], a[156:159], v[176:179]\n" \
"ds_read_b128 a[216:219], v29 offset:12288\n" \
"v_accvgpr_read_b32 v219, a35\n" \
"v_mfma_f32_16x16x32_bf16 v[176:179], a[252:255], a[220:223], v[176:179]\n" \
"v_accvgpr_read_b32 v220, a36\n" \
"v_accvgpr_read_b32 v221, a37\n" \
"v_mfma_f32_16x16x32_bf16 v[172:175], a[184:187], a[156:159], v[172:175]\n" \
"ds_read_b64_tr_b16 a[172:173], v35 offset:0\n" \
"v_accvgpr_read_b32 v222, a38\n" \
"v_mfma_f32_16x16x32_bf16 v[172:175], a[248:251], a[220:223], v[172:175]\n" \
"v_accvgpr_read_b32 v223, a39\n" \
"v_accvgpr_read_b32 v228, a44\n" \
"v_mfma_f32_16x16x32_bf16 v[168:171], a[180:183], a[156:159], v[168:171]\n" \
"v_accvgpr_read_b32 v229, a45\n" \
"v_accvgpr_read_b32 v230, a46\n" \
"v_mfma_f32_16x16x32_bf16 v[168:171], a[244:247], a[220:223], v[168:171]\n" \
"ds_read_b64_tr_b16 a[174:175], v35 offset:512\n" \
"v_accvgpr_read_b32 v231, a47\n" \
"v_mfma_f32_16x16x32_bf16 v[164:167], a[176:179], a[156:159], v[164:167]\n" \
"v_accvgpr_read_b32 v224, a40\n" \
"v_accvgpr_read_b32 v225, a41\n" \
"v_mfma_f32_16x16x32_bf16 v[164:167], a[240:243], a[220:223], v[164:167]\n" \
"ds_read_b128 a[156:159], v28 offset:14336\n" \
"v_accvgpr_read_b32 v226, a42\n" \
"v_mfma_f32_16x16x32_bf16 v[84:87], a[176:179], a[136:139], v[84:87]\n" \
"ds_read_b128 a[220:223], v29 offset:14336\n" \
"v_accvgpr_read_b32 v227, a43\n" \
"v_mfma_f32_16x16x32_bf16 v[84:87], a[240:243], a[200:203], v[84:87]\n" \
"ds_read_b64_tr_b16 a[236:237], v35 offset:4096\n" \
"v_accvgpr_read_b32 v240, a56\n" \
"v_mfma_f32_16x16x32_bf16 v[88:91], a[180:183], a[136:139], v[88:91]\n" \
"v_accvgpr_read_b32 v241, a57\n" \
"v_accvgpr_read_b32 v242, a58\n" \
"v_mfma_f32_16x16x32_bf16 v[88:91], a[244:247], a[200:203], v[88:91]\n" \
"ds_read_b64_tr_b16 a[238:239], v35 offset:4608\n" \
"v_accvgpr_read_b32 v243, a59\n" \
"v_mfma_f32_16x16x32_bf16 v[92:95], a[184:187], a[136:139], v[92:95]\n" \
"v_accvgpr_read_b32 v244, a60\n" \
"v_accvgpr_read_b32 v245, a61\n" \
"v_mfma_f32_16x16x32_bf16 v[92:95], a[248:251], a[200:203], v[92:95]\n" \
"v_accvgpr_read_b32 v246, a62\n" \
"v_accvgpr_read_b32 v247, a63\n" \
"v_mfma_f32_16x16x32_bf16 v[96:99], a[188:191], a[136:139], v[96:99]\n" \
"v_accvgpr_read_b32 v236, a52\n" \
"v_accvgpr_read_b32 v237, a53\n" \
"v_mfma_f32_16x16x32_bf16 v[96:99], a[252:255], a[200:203], v[96:99]\n" \
"ds_read_b128 a[136:139], v28 offset:4096\n" \
"v_accvgpr_read_b32 v238, a54\n" \
"v_mfma_f32_16x16x32_bf16 v[112:115], a[188:191], a[140:143], v[112:115]\n" \
"ds_read_b128 a[200:203], v29 offset:4096\n" \
"v_accvgpr_read_b32 v239, a55\n" \
"v_mfma_f32_16x16x32_bf16 v[112:115], a[252:255], a[204:207], v[112:115]\n" \
"ds_read_b64_tr_b16 a[168:169], v34 offset:0\n" \
"v_accvgpr_read_b32 v232, a48\n" \
"v_mfma_f32_16x16x32_bf16 v[108:111], a[184:187], a[140:143], v[108:111]\n" \
"ds_read_b64_tr_b16 a[170:171], v34 offset:512\n" \
"v_accvgpr_read_b32 v233, a49\n" \
"v_mfma_f32_16x16x32_bf16 v[108:111], a[248:251], a[204:207], v[108:111]\n" \
"v_accvgpr_read_b32 v234, a50\n" \
"v_accvgpr_read_b32 v235, a51\n" \
"v_mfma_f32_16x16x32_bf16 v[104:107], a[180:183], a[140:143], v[104:107]\n" \
"v_accvgpr_read_b32 v180, a64\n" \
"v_accvgpr_read_b32 v181, a65\n" \
"v_mfma_f32_16x16x32_bf16 v[104:107], a[244:247], a[204:207], v[104:107]\n" \
"ds_read_b64_tr_b16 a[232:233], v34 offset:4096\n" \
"v_accvgpr_read_b32 v182, a66\n" \
"v_mfma_f32_16x16x32_bf16 v[100:103], a[176:179], a[140:143], v[100:103]\n" \
"v_accvgpr_read_b32 v183, a67\n" \
"v_accvgpr_read_b32 v48, a80\n" \
"v_mfma_f32_16x16x32_bf16 v[100:103], a[240:243], a[204:207], v[100:103]\n" \
"ds_read_b128 a[140:143], v28 offset:6144\n" \
"v_accvgpr_read_b32 v49, a81\n" \
"v_mfma_f32_16x16x32_bf16 v[52:55], a[176:179], a[128:131], v[52:55]\n" \
"ds_read_b128 a[204:207], v29 offset:6144\n" \
"v_accvgpr_read_b32 v50, a82\n" \
"v_mfma_f32_16x16x32_bf16 v[52:55], a[240:243], a[192:195], v[52:55]\n" \
"ds_read_b64_tr_b16 a[234:235], v34 offset:4608\n" \
"v_accvgpr_read_b32 v51, a83\n" \
"v_mfma_f32_16x16x32_bf16 v[56:59], a[180:183], a[128:131], v[56:59]\n" \
"v_accvgpr_read_b32 v252, a68\n" \
"v_accvgpr_read_b32 v253, a69\n" \
"v_mfma_f32_16x16x32_bf16 v[56:59], a[244:247], a[192:195], v[56:59]\n" \
"v_accvgpr_read_b32 v254, a70\n" \
"v_accvgpr_read_b32 v255, a71\n" \
"v_mfma_f32_16x16x32_bf16 v[60:63], a[184:187], a[128:131], v[60:63]\n" \
"v_lshrrev_b32_e64 v24, 9, v20\n" \
"v_and_b32_e32 v26, 0x1ff, v20\n" \
"v_mfma_f32_16x16x32_bf16 v[60:63], a[248:251], a[192:195], v[60:63]\n" \
"v_lshl_add_u32 v24, v24, 3, v26\n" \
"v_lshrrev_b32_e64 v25, 8, v20\n" \
"v_mfma_f32_16x16x32_bf16 v[64:67], a[188:191], a[128:131], v[64:67]\n" \
"v_mul_u32_u24_e32 v25, 0x24700, v25\n" \
"v_add_u32_e64 v25, v25, v20\n" \
"v_mfma_f32_16x16x32_bf16 v[64:67], a[252:255], a[192:195], v[64:67]\n" \
"ds_read_b128 a[128:131], v28 offset:0\n" \
"v_lshrrev_b32_e64 v26, 2, v20\n" \
"v_mfma_f32_16x16x32_bf16 v[80:83], a[188:191], a[132:135], v[80:83]\n" \
"ds_read_b128 a[192:195], v29 offset:0\n" \
"v_add_u32_e64 v26, s45, v26\n" \
"v_mfma_f32_16x16x32_bf16 v[80:83], a[252:255], a[196:199], v[80:83]\n" \
"v_add_u32_e32 v27, s46, v20\n" \
"v_add_u32_e32 v21, s43, v27\n" \
"v_mfma_f32_16x16x32_bf16 v[76:79], a[184:187], a[132:135], v[76:79]\n" \
"v_mfma_f32_16x16x32_bf16 v[76:79], a[248:251], a[196:199], v[76:79]\n" \
"v_mfma_f32_16x16x32_bf16 v[72:75], a[180:183], a[132:135], v[72:75]\n" \
"v_mfma_f32_16x16x32_bf16 v[72:75], a[244:247], a[196:199], v[72:75]\n" \
"v_mfma_f32_16x16x32_bf16 v[68:71], a[176:179], a[132:135], v[68:71]\n" \
"v_mfma_f32_16x16x32_bf16 v[68:71], a[240:243], a[196:199], v[68:71]\n" \
"ds_read_b128 a[132:135], v28 offset:2048\n" \
"ds_read_b128 a[196:199], v29 offset:2048\n" \
"s_waitcnt lgkmcnt(15)\n" \
"s_nop 0\n" \
"v_mfma_f32_16x16x32_bf16 v[184:187], a[160:163], a[144:147], v[184:187]\n" \
"v_bfe_u32 v44, v20, 2, 5\n" \
"ds_read_b64_tr_b16 a[176:177], v36 offset:0\n" \
"v_mfma_f32_16x16x32_bf16 v[184:187], a[224:227], a[208:211], v[184:187]\n" \
"buffer_load_dword v44, v44, s[80:83], s38 offen offset:0 sc1\n" \
"v_mfma_f32_16x16x32_bf16 v[188:191], a[164:167], a[144:147], v[188:191]\n" \
"ds_read_b64_tr_b16 a[178:179], v36 offset:1024\n" \
"v_mfma_f32_16x16x32_bf16 v[188:191], a[228:231], a[208:211], v[188:191]\n" \
"ds_read_b64_tr_b16 a[240:241], v36 offset:8192\n" \
"v_mfma_f32_16x16x32_bf16 v[196:199], a[172:175], a[144:147], v[196:199]\n" \
"ds_read_b64_tr_b16 a[242:243], v36 offset:9216\n" \
"s_waitcnt lgkmcnt(15)\n" \
"v_mfma_f32_16x16x32_bf16 v[196:199], a[236:239], a[208:211], v[196:199]\n" \
"s_waitcnt lgkmcnt(12)\n" \
"v_mfma_f32_16x16x32_bf16 v[192:195], a[144:147], a[168:171], v[192:195]\n" \
"ds_read_b64_tr_b16 a[180:181], v37 offset:0\n" \
"s_waitcnt lgkmcnt(9)\n" \
"s_nop 0\n" \
"v_mfma_f32_16x16x32_bf16 v[192:195], a[208:211], a[232:235], v[192:195]\n" \
"v_mfma_f32_16x16x32_bf16 v[208:211], a[148:151], a[168:171], v[208:211]\n" \
"ds_read_b64_tr_b16 a[182:183], v37 offset:1024\n" \
"v_mfma_f32_16x16x32_bf16 v[208:211], a[212:215], a[232:235], v[208:211]\n" \
"ds_read_b64_tr_b16 a[244:245], v37 offset:8192\n" \
"v_mfma_f32_16x16x32_bf16 v[212:215], a[172:175], a[148:151], v[212:215]\n" \
"v_mfma_f32_16x16x32_bf16 v[212:215], a[236:239], a[212:215], v[212:215]\n" \
"ds_read_b64_tr_b16 a[246:247], v37 offset:9216\n" \
"v_cvt_pk_bf16_f32 v188, v188, v189\n" \
"v_mfma_f32_16x16x32_bf16 v[204:207], a[164:167], a[148:151], v[204:207]\n" \
"v_cvt_pk_bf16_f32 v189, v190, v191\n" \
"v_cvt_pk_bf16_f32 v190, v198, v199\n" \
"v_mfma_f32_16x16x32_bf16 v[204:207], a[228:231], a[212:215], v[204:207]\n" \
"ds_read_b64_tr_b16 a[184:185], v38 offset:0\n" \
"v_mov_b32_e32 v191, 0x3f80\n" \
"v_mfma_f32_16x16x32_bf16 v[200:203], a[160:163], a[148:151], v[200:203]\n" \
"ds_read_b64_tr_b16 a[186:187], v38 offset:1024\n" \
"v_cvt_pk_bf16_f32 v184, v184, v185\n" \
"v_mfma_f32_16x16x32_bf16 v[200:203], a[224:227], a[212:215], v[200:203]\n" \
"v_cvt_pk_bf16_f32 v185, v186, v187\n" \
"v_cvt_pk_bf16_f32 v186, v196, v197\n" \
"v_mfma_f32_16x16x32_bf16 v[216:219], a[160:163], a[152:155], v[216:219]\n" \
"ds_read_b64_tr_b16 a[248:249], v38 offset:8192\n" \
"v_mov_b32_e64 v187, 0\n" \
"v_mfma_f32_16x16x32_bf16 v[216:219], a[224:227], a[216:219], v[216:219]\n" \
"ds_read_b64_tr_b16 a[250:251], v38 offset:9216\n" \
"v_mfma_f32_16x16x32_bf16 v[220:223], a[164:167], a[152:155], v[220:223]\n" \
"v_mfma_f32_16x16x32_bf16 v[220:223], a[228:231], a[216:219], v[220:223]\n" \
"ds_read_b64_tr_b16 a[188:189], v39 offset:0\n" \
"v_cvt_pk_bf16_f32 v204, v204, v205\n" \
"v_mfma_f32_16x16x32_bf16 v[228:231], a[172:175], a[152:155], v[228:231]\n" \
"v_cvt_pk_bf16_f32 v205, v206, v207\n" \
"v_cvt_pk_bf16_f32 v206, v214, v215\n" \
"v_mfma_f32_16x16x32_bf16 v[228:231], a[236:239], a[216:219], v[228:231]\n" \
"ds_read_b64_tr_b16 a[190:191], v39 offset:1024\n" \
"v_mov_b32_e32 v207, 0x3f80\n" \
"v_mfma_f32_16x16x32_bf16 v[224:227], a[152:155], a[168:171], v[224:227]\n" \
"ds_read_b64_tr_b16 a[252:253], v39 offset:8192\n" \
"v_cvt_pk_bf16_f32 v200, v200, v201\n" \
"v_mfma_f32_16x16x32_bf16 v[224:227], a[216:219], a[232:235], v[224:227]\n" \
"v_cvt_pk_bf16_f32 v201, v202, v203\n" \
"v_cvt_pk_bf16_f32 v202, v212, v213\n" \
"v_mfma_f32_16x16x32_bf16 v[240:243], a[156:159], a[168:171], v[240:243]\n" \
"ds_read_b64_tr_b16 a[254:255], v39 offset:9216\n" \
"v_mov_b32_e64 v203, 0\n" \
"v_mfma_f32_16x16x32_bf16 v[240:243], a[220:223], a[232:235], v[240:243]\n" \
"v_mfma_f32_16x16x32_bf16 v[244:247], a[172:175], a[156:159], v[244:247]\n" \
"v_mfma_f32_16x16x32_bf16 v[244:247], a[236:239], a[220:223], v[244:247]\n" \
"v_cvt_pk_bf16_f32 v220, v220, v221\n" \
"v_cvt_pk_bf16_f32 v221, v222, v223\n" \
"v_mfma_f32_16x16x32_bf16 v[236:239], a[164:167], a[156:159], v[236:239]\n" \
"v_cvt_pk_bf16_f32 v222, v230, v231\n" \
"v_mov_b32_e32 v223, 0x3f80\n" \
"v_mfma_f32_16x16x32_bf16 v[236:239], a[228:231], a[220:223], v[236:239]\n" \
"v_cvt_pk_bf16_f32 v216, v216, v217\n" \
"v_cvt_pk_bf16_f32 v217, v218, v219\n" \
"v_mfma_f32_16x16x32_bf16 v[232:235], a[160:163], a[156:159], v[232:235]\n" \
"v_cvt_pk_bf16_f32 v218, v228, v229\n" \
"v_mov_b32_e64 v219, 0\n" \
"v_mfma_f32_16x16x32_bf16 v[232:235], a[224:227], a[220:223], v[232:235]\n" \
"v_mfma_f32_16x16x32_bf16 v[180:183], a[164:167], a[140:143], v[180:183]\n" \
"v_mfma_f32_16x16x32_bf16 v[180:183], a[228:231], a[204:207], v[180:183]\n" \
"v_mfma_f32_16x16x32_bf16 v[48:51], a[172:175], a[140:143], v[48:51]\n" \
"v_mfma_f32_16x16x32_bf16 v[48:51], a[236:239], a[204:207], v[48:51]\n" \
"v_cvt_pk_bf16_f32 v236, v236, v237\n" \
"v_cvt_pk_bf16_f32 v237, v238, v239\n" \
"v_mfma_f32_16x16x32_bf16 v[252:255], a[140:143], a[168:171], v[252:255]\n" \
"v_cvt_pk_bf16_f32 v238, v246, v247\n" \
"v_mov_b32_e32 v239, 0x3f80\n" \
"v_mfma_f32_16x16x32_bf16 v[252:255], a[204:207], a[232:235], v[252:255]\n" \
"v_cvt_pk_bf16_f32 v232, v232, v233\n" \
"v_cvt_pk_bf16_f32 v233, v234, v235\n" \
"s_waitcnt vmcnt(0) lgkmcnt(0)\n" \
"s_barrier\n" \
"v_mov_b32_e32 v45, s62\n" \
"s_mov_b64 exec, 1\n" \
"buffer_store_dword v45, off, s[80:83], s37 offset:32 sc1\n" \
"s_mov_b64 exec, -1\n" \
"v_cmp_ne_u32_e32 vcc, s62, v44\n" \
"s_nop 4\n" \
"s_cbranch_vccnz L_poll161_%=\n" \
"L_pollr161_%=:\n" \
"v_mfma_f32_16x16x32_bf16 v[116:119], a[176:179], a[144:147], v[116:119]\n" \
"s_mov_b32 s73, s50\n" \
"s_sub_u32 m0, s31, 0x10000\n" \
"v_mfma_f32_16x16x32_bf16 v[116:119], a[240:243], a[208:211], v[116:119]\n" \
"buffer_load_dwordx4 v20, s[80:83], s73 offen offset:0 sc1 lds\n" \
"v_mfma_f32_16x16x32_bf16 v[120:123], a[180:183], a[144:147], v[120:123]\n" \
"s_add_u32 s74, s50, 0x4000\n" \
"s_add_u32 s75, s50, 0x8000\n" \
"v_mfma_f32_16x16x32_bf16 v[120:123], a[244:247], a[208:211], v[120:123]\n" \
"s_mov_b32 s70, 0x3e0293ee\n" \
"s_mov_b32 s76, s52\n" \
"v_mfma_f32_16x16x32_bf16 v[124:127], a[184:187], a[144:147], v[124:127]\n" \
"v_mul_f32_e32 v116, s70, v116\n" \
"v_mul_f32_e32 v117, s70, v117\n" \
"v_mfma_f32_16x16x32_bf16 v[124:127], a[248:251], a[208:211], v[124:127]\n" \
"buffer_load_dwordx4 v20, s[80:83], s73 offen offset:1024 sc1 lds\n" \
"v_mfma_f32_16x16x32_bf16 v[128:131], a[188:191], a[144:147], v[128:131]\n" \
"v_mul_f32_e32 v118, s70, v118\n" \
"v_mul_f32_e32 v119, s70, v119\n" \
"v_mfma_f32_16x16x32_bf16 v[128:131], a[252:255], a[208:211], v[128:131]\n" \
"v_cvt_pk_bf16_f32 v116, v116, v117\n" \
"v_mul_f32_e64 v120, s70, v120\n" \
"v_mfma_f32_16x16x32_bf16 v[144:147], a[188:191], a[148:151], v[144:147]\n" \
"v_mul_f32_e32 v124, s70, v124\n" \
"v_mul_f32_e32 v125, s70, v125\n" \
"v_mfma_f32_16x16x32_bf16 v[144:147], a[252:255], a[212:215], v[144:147]\n" \
"buffer_load_dwordx4 v20, s[80:83], s73 offen offset:2048 sc1 lds\n" \
"v_mfma_f32_16x16x32_bf16 v[140:143], a[184:187], a[148:151], v[140:143]\n" \
"v_mul_f32_e32 v126, s70, v126\n" \
"v_mul_f32_e32 v127, s70, v127\n" \
"v_mfma_f32_16x16x32_bf16 v[140:143], a[248:251], a[212:215], v[140:143]\n" \
"v_cvt_pk_bf16_f32 v124, v124, v125\n" \
"v_mul_f32_e64 v121, s70, v121\n" \
"v_mfma_f32_16x16x32_bf16 v[136:139], a[180:183], a[148:151], v[136:139]\n" \
"v_mul_f32_e32 v122, s70, v122\n" \
"v_mul_f32_e32 v123, s70, v123\n" \
"v_mfma_f32_16x16x32_bf16 v[136:139], a[244:247], a[212:215], v[136:139]\n" \
"buffer_load_dwordx4 v20, s[80:83], s73 offen offset:3072 sc1 lds\n" \
"v_mfma_f32_16x16x32_bf16 v[132:135], a[176:179], a[148:151], v[132:135]\n" \
"s_sub_u32 m0, s31, s51\n" \
"v_cvt_pk_bf16_f32 v117, v118, v119\n" \
"v_mfma_f32_16x16x32_bf16 v[132:135], a[240:243], a[212:215], v[132:135]\n" \
"v_mul_f32_e32 v128, s70, v128\n" \
"v_mul_f32_e64 v129, s70, v129\n" \
"v_mfma_f32_16x16x32_bf16 v[148:151], a[176:179], a[152:155], v[148:151]\n" \
"v_mul_f32_e32 v130, s70, v130\n" \
"v_mul_f32_e32 v131, s70, v131\n" \
"v_mfma_f32_16x16x32_bf16 v[148:151], a[240:243], a[216:219], v[148:151]\n" \
"buffer_load_dwordx4 v20, s[80:83], s74 offen offset:0 sc1 lds\n" \
"v_mfma_f32_16x16x32_bf16 v[152:155], a[180:183], a[152:155], v[152:155]\n" \
"v_cvt_pk_bf16_f32 v125, v126, v127\n" \
"v_cvt_pk_bf16_f32 v118, v120, v121\n" \
"v_mfma_f32_16x16x32_bf16 v[152:155], a[244:247], a[216:219], v[152:155]\n" \
"v_cvt_pk_bf16_f32 v119, v122, v123\n" \
"v_cvt_pk_bf16_f32 v126, v128, v129\n" \
"v_mfma_f32_16x16x32_bf16 v[156:159], a[184:187], a[152:155], v[156:159]\n" \
"v_cvt_pk_bf16_f32 v127, v130, v131\n" \
"v_mul_f32_e64 v132, s70, v132\n" \
"v_mfma_f32_16x16x32_bf16 v[156:159], a[248:251], a[216:219], v[156:159]\n" \
"buffer_load_dwordx4 v20, s[80:83], s74 offen offset:1024 sc1 lds\n" \
"v_mfma_f32_16x16x32_bf16 v[160:163], a[188:191], a[152:155], v[160:163]\n" \
"ds_write_b128 v21, v[116:119] offset:16384\n" \
"v_mfma_f32_16x16x32_bf16 v[160:163], a[252:255], a[216:219], v[160:163]\n" \
"v_mul_f32_e32 v133, s70, v133\n" \
"v_mul_f32_e32 v140, s70, v140\n" \
"v_mfma_f32_16x16x32_bf16 v[176:179], a[188:191], a[156:159], v[176:179]\n" \
"v_mul_f32_e32 v141, s70, v141\n" \
"v_mul_f32_e32 v134, s70, v134\n" \
"v_mfma_f32_16x16x32_bf16 v[176:179], a[252:255], a[220:223], v[176:179]\n" \
"buffer_load_dwordx4 v20, s[80:83], s74 offen offset:2048 sc1 lds\n" \
"v_mfma_f32_16x16x32_bf16 v[172:175], a[184:187], a[156:159], v[172:175]\n" \
"ds_write_b128 v21, v[124:127] offset:17408\n" \
"v_mfma_f32_16x16x32_bf16 v[172:175], a[248:251], a[220:223], v[172:175]\n" \
"v_mul_f32_e64 v135, s70, v135\n" \
"v_cvt_pk_bf16_f32 v132, v132, v133\n" \
"v_mfma_f32_16x16x32_bf16 v[168:171], a[180:183], a[156:159], v[168:171]\n" \
"v_mul_f32_e32 v142, s70, v142\n" \
"v_mul_f32_e32 v143, s70, v143\n" \
"v_mfma_f32_16x16x32_bf16 v[168:171], a[244:247], a[220:223], v[168:171]\n" \
"buffer_load_dwordx4 v20, s[80:83], s74 offen offset:3072 sc1 lds\n" \
"v_mfma_f32_16x16x32_bf16 v[164:167], a[176:179], a[156:159], v[164:167]\n" \
"s_sub_u32 m0, s31, 0x4000\n" \
"v_cvt_pk_bf16_f32 v140, v140, v141\n" \
"v_mfma_f32_16x16x32_bf16 v[164:167], a[240:243], a[220:223], v[164:167]\n" \
"v_mul_f32_e32 v136, s70, v136\n" \
"v_mul_f32_e32 v137, s70, v137\n" \
"v_mfma_f32_16x16x32_bf16 v[84:87], a[176:179], a[136:139], v[84:87]\n" \
"v_mul_f32_e32 v138, s70, v138\n" \
"v_mul_f32_e32 v139, s70, v139\n" \
"v_mfma_f32_16x16x32_bf16 v[84:87], a[240:243], a[200:203], v[84:87]\n" \
"buffer_load_dwordx4 v20, s[80:83], s75 offen offset:0 sc1 lds\n" \
"v_mfma_f32_16x16x32_bf16 v[88:91], a[180:183], a[136:139], v[88:91]\n" \
"v_cvt_pk_bf16_f32 v133, v134, v135\n" \
"v_mul_f32_e64 v144, s70, v144\n" \
"v_mfma_f32_16x16x32_bf16 v[88:91], a[244:247], a[200:203], v[88:91]\n" \
"v_mul_f32_e32 v145, s70, v145\n" \
"v_mul_f32_e32 v146, s70, v146\n" \
".fill (L_pb + 77536 - .) / 4, 4, 0xBF800000\n" \
"L_pp19:\n" \
"s_cbranch_execz L_pret\n" \
"v_mfma_f32_16x16x32_bf16 v[92:95], a[184:187], a[136:139], v[92:95]\n" \
"v_mul_f32_e32 v147, s70, v147\n" \
"v_cvt_pk_bf16_f32 v141, v142, v143\n" \
"v_mfma_f32_16x16x32_bf16 v[92:95], a[248:251], a[200:203], v[92:95]\n" \
"buffer_load_dwordx4 v20, s[80:83], s75 offen offset:1024 sc1 lds\n" \
"v_mfma_f32_16x16x32_bf16 v[96:99], a[188:191], a[136:139], v[96:99]\n" \
"v_cvt_pk_bf16_f32 v134, v136, v137\n" \
"v_cvt_pk_bf16_f32 v135, v138, v139\n" \
"v_mfma_f32_16x16x32_bf16 v[96:99], a[252:255], a[200:203], v[96:99]\n" \
"v_cvt_pk_bf16_f32 v142, v144, v145\n" \
"v_cvt_pk_bf16_f32 v143, v146, v147\n" \
"v_mfma_f32_16x16x32_bf16 v[112:115], a[188:191], a[140:143], v[112:115]\n" \
"ds_write_b128 v21, v[132:135] offset:20480\n" \
"v_mfma_f32_16x16x32_bf16 v[112:115], a[252:255], a[204:207], v[112:115]\n" \
"buffer_load_dwordx4 v20, s[80:83], s75 offen offset:2048 sc1 lds\n" \
"v_mfma_f32_16x16x32_bf16 v[108:111], a[184:187], a[140:143], v[108:111]\n" \
"v_cvt_pk_bf16_f32 v180, v180, v181\n" \
"v_cvt_pk_bf16_f32 v181, v182, v183\n" \
"v_mfma_f32_16x16x32_bf16 v[108:111], a[248:251], a[204:207], v[108:111]\n" \
"ds_write_b128 v21, v[140:143] offset:21504\n" \
"v_mfma_f32_16x16x32_bf16 v[104:107], a[180:183], a[140:143], v[104:107]\n" \
"v_cvt_pk_bf16_f32 v234, v244, v245\n" \
"v_mov_b32_e64 v235, 0\n" \
"v_mfma_f32_16x16x32_bf16 v[104:107], a[244:247], a[204:207], v[104:107]\n" \
"buffer_load_dwordx4 v20, s[80:83], s75 offen offset:3072 sc1 lds\n" \
"v_mfma_f32_16x16x32_bf16 v[100:103], a[176:179], a[140:143], v[100:103]\n" \
"s_mov_b32 m0, s53\n" \
"s_nop 0\n" \
"v_cvt_pk_bf16_f32 v182, v50, v51\n" \
"v_mfma_f32_16x16x32_bf16 v[100:103], a[240:243], a[204:207], v[100:103]\n" \
"v_mov_b32_e32 v183, 0x3f80\n" \
"v_cvt_pk_bf16_f32 v118, v192, v193\n" \
"v_mfma_f32_16x16x32_bf16 v[52:55], a[176:179], a[128:131], v[52:55]\n" \
"v_cvt_pk_bf16_f32 v119, v194, v195\n" \
"v_cvt_pk_bf16_f32 v120, v208, v209\n" \
"v_mfma_f32_16x16x32_bf16 v[52:55], a[240:243], a[192:195], v[52:55]\n" \
"buffer_load_dwordx4 v20, s[80:83], s76 offen offset:0 sc1 lds\n" \
"v_mfma_f32_16x16x32_bf16 v[56:59], a[180:183], a[128:131], v[56:59]\n" \
"v_cvt_pk_bf16_f32 v121, v210, v211\n" \
"v_cvt_pk_bf16_f32 v122, v224, v225\n" \
"v_mfma_f32_16x16x32_bf16 v[56:59], a[244:247], a[192:195], v[56:59]\n" \
"v_cvt_pk_bf16_f32 v123, v226, v227\n" \
"v_cvt_pk_bf16_f32 v124, v240, v241\n" \
"v_cvt_pk_bf16_f32 v125, v242, v243\n" \
"v_mfma_f32_16x16x32_bf16 v[60:63], a[184:187], a[128:131], v[60:63]\n" \
"v_mfma_f32_16x16x32_bf16 v[192:195], v[188:191], v[184:187], 0\n" \
"buffer_load_dwordx4 v20, s[80:83], s76 offen offset:1024 sc1 lds\n" \
"v_mfma_f32_16x16x32_bf16 v[32:35], v[180:183], v[184:187], 0\n" \
"v_mfma_f32_16x16x32_bf16 v[208:211], v[204:207], v[200:203], 0\n" \
"v_mfma_f32_16x16x32_bf16 v[36:39], v[188:191], v[200:203], 0\n" \
"v_mfma_f32_16x16x32_bf16 v[224:227], v[220:223], v[216:219], 0\n" \
"v_mfma_f32_16x16x32_bf16 v[248:251], v[204:207], v[216:219], 0\n" \
"v_mfma_f32_16x16x32_bf16 v[240:243], v[236:239], v[232:235], 0\n" \
"v_mfma_f32_16x16x32_bf16 v[128:131], v[220:223], v[232:235], 0\n" \
"s_mov_b32 s64, 0xaaaaaaaa\n" \
"s_mov_b32 s65, 0xaaaaaaaa\n" \
"v_mfma_f32_16x16x32_bf16 v[60:63], a[248:251], a[192:195], v[60:63]\n" \
"s_mov_b32 s66, 0xfff0ffff\n" \
"s_mov_b32 s67, 0xf000ff00\n" \
"v_mfma_f32_16x16x32_bf16 v[64:67], a[188:191], a[128:131], v[64:67]\n" \
"s_mov_b32 s68, 0xffc0fffc\n" \
"s_mov_b32 s69, 0xc000fc00\n" \
"v_cndmask_b32_e64 v192, v192, v193, s[64:65]\n" \
"v_cndmask_b32_e64 v32, v32, v33, s[64:65]\n" \
"v_cndmask_b32_e64 v194, v194, v195, s[64:65]\n" \
"v_cndmask_b32_e64 v34, v34, v35, s[64:65]\n" \
"v_cndmask_b32_e64 v208, v208, v209, s[64:65]\n" \
"v_cndmask_b32_e64 v36, v36, v37, s[64:65]\n" \
"v_cndmask_b32_e64 v210, v210, v211, s[64:65]\n" \
"v_cndmask_b32_e64 v38, v38, v39, s[64:65]\n" \
"v_cndmask_b32_e64 v224, v224, v225, s[64:65]\n" \
"v_cndmask_b32_e64 v248, v248, v249, s[64:65]\n" \
"v_cndmask_b32_e64 v226, v226, v227, s[64:65]\n" \
"v_cndmask_b32_e64 v250, v250, v251, s[64:65]\n" \
"v_cndmask_b32_e64 v240, v240, v241, s[64:65]\n" \
"v_cndmask_b32_e64 v128, v128, v129, s[64:65]\n" \
"v_cndmask_b32_e64 v242, v242, v243, s[64:65]\n" \
"v_cndmask_b32_e64 v130, v130, v131, s[64:65]\n" \
"v_cndmask_b32_e64 v192, v32, v192, s[66:67]\n" \
"v_cndmask_b32_e64 v194, v34, v194, s[68:69]\n" \
"v_cndmask_b32_e64 v208, v36, v208, s[66:67]\n" \
"v_cndmask_b32_e64 v210, v38, v210, s[68:69]\n" \
"v_cndmask_b32_e64 v224, v248, v224, s[66:67]\n" \
"v_cndmask_b32_e64 v226, v250, v226, s[68:69]\n" \
"v_cndmask_b32_e64 v240, v128, v240, s[66:67]\n" \
"v_cndmask_b32_e64 v242, v130, v242, s[68:69]\n" \
"v_mfma_f32_16x16x32_bf16 v[64:67], a[252:255], a[192:195], v[64:67]\n" \
"v_max_f32_e32 v193, v192, v194\n" \
"v_max_f32_e32 v209, v208, v210\n" \
"v_max_f32_e32 v225, v224, v226\n" \
"v_max_f32_e32 v241, v240, v242\n" \
"v_mul_f32_e32 v148, s70, v148\n" \
"v_permlane32_swap_b32_e32 v193, v225\n" \
"v_permlane32_swap_b32_e32 v209, v241\n" \
"v_mul_f32_e32 v149, s70, v149\n" \
"v_mul_f32_e32 v156, s70, v156\n" \
"v_mul_f32_e32 v157, s70, v157\n" \
"v_permlane32_swap_b32_e32 v196, v228\n" \
"v_permlane32_swap_b32_e32 v212, v244\n" \
"v_max_f32_e32 v193, v193, v225\n" \
"v_max_f32_e32 v209, v209, v241\n" \
"v_mul_f32_e32 v150, s70, v150\n" \
"v_mul_f32_e32 v151, s70, v151\n" \
"v_cvt_pk_bf16_f32 v148, v148, v149\n" \
"v_mul_f32_e32 v158, s70, v158\n" \
"v_mul_f32_e32 v159, s70, v159\n" \
"v_cvt_pk_bf16_f32 v156, v156, v157\n" \
"v_permlane16_swap_b32_e32 v228, v244\n" \
"v_permlane16_swap_b32_e32 v193, v209\n" \
"v_mfma_f32_16x16x32_bf16 v[80:83], a[188:191], a[132:135], v[80:83]\n" \
"s_mov_b32 s72, 0x3eae1a93\n" \
"v_mul_f32_e32 v152, s70, v152\n" \
"v_mul_f32_e32 v153, s70, v153\n" \
"v_mul_f32_e32 v154, s70, v154\n" \
"v_mul_f32_e32 v155, s70, v155\n" \
"v_cvt_pk_bf16_f32 v149, v150, v151\n" \
"v_mul_f32_e32 v160, s70, v160\n" \
"v_mul_f32_e32 v161, s70, v161\n" \
"v_mul_f32_e32 v162, s70, v162\n" \
"v_mul_f32_e32 v163, s70, v163\n" \
"v_cvt_pk_bf16_f32 v157, v158, v159\n" \
"v_mul_f32_e32 v45, 0xbfb8aa3b, v228\n" \
"v_mfma_f32_16x16x16_bf16 v[48:51], v[182:183], v[46:47], 0\n" \
"v_mfma_f32_16x16x16_bf16 v[196:199], v[190:191], v[46:47], 0\n" \
"v_mfma_f32_16x16x16_bf16 v[212:215], v[206:207], v[46:47], 0\n" \
"v_max_f32_e64 v193, v193, v209\n" \
"v_cvt_pk_bf16_f32 v150, v152, v153\n" \
"v_cvt_pk_bf16_f32 v151, v154, v155\n" \
"v_cvt_pk_bf16_f32 v158, v160, v161\n" \
"v_cvt_pk_bf16_f32 v159, v162, v163\n" \
"v_mfma_f32_16x16x16_bf16 v[228:231], v[222:223], v[46:47], 0\n" \
"v_mul_f32_e32 v193, s72, v193\n" \
"v_mov_b32_e32 v209, v193\n" \
"ds_write_b128 v21, v[148:151] offset:24576\n" \
"v_mfma_f32_16x16x16_bf16 v[244:247], v[238:239], v[46:47], 0\n" \
"v_permlane16_swap_b32_e32 v193, v209\n" \
"s_mov_b32 s71, 0x3e0293ee\n" \
"v_mfma_f32_16x16x32_bf16 v[80:83], a[252:255], a[196:199], v[80:83]\n" \
"v_mov_b32_e32 v225, v193\n" \
"v_mov_b32_e32 v241, v209\n" \
"v_mul_f32_e32 v164, s70, v164\n" \
"v_mul_f32_e64 v165, s70, v165\n" \
"ds_write_b128 v21, v[156:159] offset:25600\n" \
"v_mul_f32_e32 v172, s70, v172\n" \
"v_mul_f32_e32 v173, s70, v173\n" \
"v_pk_mul_f32 v[84:85], v[84:85], s[70:71]\n" \
"v_pk_mul_f32 v[92:93], v[92:93], s[70:71]\n" \
"v_pk_mul_f32 v[100:101], v[100:101], s[70:71]\n" \
"v_pk_mul_f32 v[108:109], v[108:109], s[70:71]\n" \
"v_pk_mul_f32 v[52:53], v[52:53], s[70:71]\n" \
"v_pk_mul_f32 v[60:61], v[60:61], s[70:71]\n" \
"v_permlane32_swap_b32_e32 v193, v225\n" \
"v_permlane32_swap_b32_e32 v209, v241\n" \
"v_mul_f32_e32 v166, s70, v166\n" \
"v_mul_f32_e32 v167, s70, v167\n" \
"v_cvt_pk_bf16_f32 v164, v164, v165\n" \
"v_mul_f32_e32 v174, s70, v174\n" \
"v_mul_f32_e32 v175, s70, v175\n" \
"v_cvt_pk_bf16_f32 v172, v172, v173\n" \
"v_pk_mul_f32 v[86:87], v[86:87], s[70:71]\n" \
"v_cvt_pk_bf16_f32 v84, v84, v85\n" \
"v_pk_mul_f32 v[94:95], v[94:95], s[70:71]\n" \
"v_cvt_pk_bf16_f32 v92, v92, v93\n" \
"v_pk_mul_f32 v[102:103], v[102:103], s[70:71]\n" \
"v_cvt_pk_bf16_f32 v100, v100, v101\n" \
"v_pk_mul_f32 v[110:111], v[110:111], s[70:71]\n" \
"v_cvt_pk_bf16_f32 v108, v108, v109\n" \
"v_pk_mul_f32 v[54:55], v[54:55], s[70:71]\n" \
"v_cvt_pk_bf16_f32 v52, v52, v53\n" \
"v_pk_mul_f32 v[62:63], v[62:63], s[70:71]\n" \
"v_cvt_pk_bf16_f32 v60, v60, v61\n" \
"v_fma_f32 v192, v192, s72, -v193\n" \
"v_fma_f32 v194, v194, s72, -v193\n" \
"v_fma_f32 v208, v208, s72, -v209\n" \
"v_fma_f32 v210, v210, s72, -v209\n" \
"v_mul_f32_e32 v168, s70, v168\n" \
"v_mul_f32_e32 v169, s70, v169\n" \
"v_mul_f32_e32 v170, s70, v170\n" \
"v_mul_f32_e32 v171, s70, v171\n" \
"v_cvt_pk_bf16_f32 v165, v166, v167\n" \
"v_mul_f32_e32 v176, s70, v176\n" \
"v_mul_f32_e32 v177, s70, v177\n" \
"v_mul_f32_e32 v178, s70, v178\n" \
"v_mul_f32_e32 v179, s70, v179\n" \
"v_cvt_pk_bf16_f32 v173, v174, v175\n" \
"v_pk_mul_f32 v[88:89], v[88:89], s[70:71]\n" \
"v_pk_mul_f32 v[90:91], v[90:91], s[70:71]\n" \
"v_cvt_pk_bf16_f32 v85, v86, v87\n" \
"v_pk_mul_f32 v[96:97], v[96:97], s[70:71]\n" \
"v_pk_mul_f32 v[98:99], v[98:99], s[70:71]\n" \
"v_cvt_pk_bf16_f32 v93, v94, v95\n" \
"v_pk_mul_f32 v[104:105], v[104:105], s[70:71]\n" \
"v_pk_mul_f32 v[106:107], v[106:107], s[70:71]\n" \
"v_cvt_pk_bf16_f32 v101, v102, v103\n" \
"v_pk_mul_f32 v[112:113], v[112:113], s[70:71]\n" \
"v_pk_mul_f32 v[114:115], v[114:115], s[70:71]\n" \
"v_cvt_pk_bf16_f32 v109, v110, v111\n" \
"v_pk_mul_f32 v[56:57], v[56:57], s[70:71]\n" \
"v_pk_mul_f32 v[58:59], v[58:59], s[70:71]\n" \
"v_cvt_pk_bf16_f32 v53, v54, v55\n" \
"v_pk_mul_f32 v[64:65], v[64:65], s[70:71]\n" \
"v_pk_mul_f32 v[66:67], v[66:67], s[70:71]\n" \
"v_cvt_pk_bf16_f32 v61, v62, v63\n" \
"v_pk_mul_f32 v[80:81], v[80:81], s[70:71]\n" \
"v_pk_mul_f32 v[82:83], v[82:83], s[70:71]\n" \
"v_exp_f32_e32 v192, v192\n" \
"v_exp_f32_e32 v194, v194\n" \
"v_exp_f32_e32 v208, v208\n" \
"v_exp_f32_e32 v210, v210\n" \
"v_fma_f32 v224, v224, s72, -v225\n" \
"v_fma_f32 v226, v226, s72, -v225\n" \
"v_mfma_f32_16x16x32_bf16 v[76:79], a[184:187], a[132:135], v[76:79]\n" \
"v_cvt_pk_bf16_f32 v166, v168, v169\n" \
"v_cvt_pk_bf16_f32 v167, v170, v171\n" \
"v_cvt_pk_bf16_f32 v174, v176, v177\n" \
"v_cvt_pk_bf16_f32 v175, v178, v179\n" \
"v_cvt_pk_bf16_f32 v86, v88, v89\n" \
"v_cvt_pk_bf16_f32 v87, v90, v91\n" \
"v_cvt_pk_bf16_f32 v94, v96, v97\n" \
"v_cvt_pk_bf16_f32 v95, v98, v99\n" \
"v_cvt_pk_bf16_f32 v102, v104, v105\n" \
"v_cvt_pk_bf16_f32 v103, v106, v107\n" \
"v_cvt_pk_bf16_f32 v110, v112, v113\n" \
"v_cvt_pk_bf16_f32 v111, v114, v115\n" \
"v_cvt_pk_bf16_f32 v54, v56, v57\n" \
"v_cvt_pk_bf16_f32 v55, v58, v59\n" \
"v_cvt_pk_bf16_f32 v62, v64, v65\n" \
"v_cvt_pk_bf16_f32 v63, v66, v67\n" \
"v_exp_f32_e32 v224, v224\n" \
"v_exp_f32_e32 v226, v226\n" \
"v_fma_f32 v240, v240, s72, -v241\n" \
"v_fma_f32 v242, v242, s72, -v241\n" \
"ds_write_b128 v21, v[164:167] offset:28672\n" \
"v_cvt_pk_bf16_f32 v192, v192, v194\n" \
"v_cvt_pk_bf16_f32 v208, v208, v210\n" \
"v_exp_f32_e32 v240, v240\n" \
"v_exp_f32_e32 v242, v242\n" \
"v_cvt_pk_bf16_f32 v132, v48, v49\n" \
"v_cvt_pk_bf16_f32 v133, v50, v51\n" \
"v_cvt_pk_bf16_f32 v134, v196, v197\n" \
"v_cvt_pk_bf16_f32 v135, v198, v199\n" \
"ds_write_b128 v21, v[172:175] offset:29696\n" \
"v_cvt_pk_bf16_f32 v136, v212, v213\n" \
"v_cvt_pk_bf16_f32 v137, v214, v215\n" \
"v_perm_b32 v184, v192, v192, v40\n" \
"v_perm_b32 v185, v192, v192, v41\n" \
"v_perm_b32 v186, v192, v192, v42\n" \
"v_perm_b32 v187, v192, v192, v43\n" \
"v_perm_b32 v200, v208, v208, v40\n" \
"v_perm_b32 v201, v208, v208, v41\n" \
"v_perm_b32 v202, v208, v208, v42\n" \
"v_perm_b32 v203, v208, v208, v43\n" \
"ds_write_b128 v27, v[84:87] offset:8192\n" \
"v_cvt_pk_bf16_f32 v224, v224, v226\n" \
"v_cvt_pk_bf16_f32 v138, v228, v229\n" \
"v_cvt_pk_bf16_f32 v139, v230, v231\n" \
"v_mfma_f32_16x16x32_bf16 v[32:35], v[132:135], v[184:187], 0\n" \
"v_mfma_f32_16x16x32_bf16 v[36:39], v[134:137], v[200:203], 0\n" \
"v_perm_b32 v216, v224, v224, v40\n" \
"v_perm_b32 v217, v224, v224, v41\n" \
"ds_write_b128 v27, v[92:95] offset:9216\n" \
"v_perm_b32 v218, v224, v224, v42\n" \
"v_perm_b32 v219, v224, v224, v43\n" \
"v_cvt_pk_bf16_f32 v240, v240, v242\n" \
"v_mfma_f32_16x16x32_bf16 v[76:79], a[248:251], a[196:199], v[76:79]\n" \
"v_cvt_pk_bf16_f32 v140, v244, v245\n" \
"v_cvt_pk_bf16_f32 v141, v246, v247\n" \
"v_mfma_f32_16x16x32_bf16 v[248:251], v[136:139], v[216:219], 0\n" \
"v_perm_b32 v232, v240, v240, v40\n" \
"v_perm_b32 v233, v240, v240, v41\n" \
"ds_write_b128 v27, v[100:103] offset:12288\n" \
"v_perm_b32 v234, v240, v240, v42\n" \
"v_perm_b32 v235, v240, v240, v43\n" \
"v_pk_mul_f32 v[76:77], v[76:77], s[70:71]\n" \
"v_pk_mul_f32 v[78:79], v[78:79], s[70:71]\n" \
"v_cvt_pk_bf16_f32 v76, v76, v77\n" \
"v_cvt_pk_bf16_f32 v77, v78, v79\n" \
"v_cvt_pk_bf16_f32 v78, v80, v81\n" \
"v_cvt_pk_bf16_f32 v79, v82, v83\n" \
"v_mfma_f32_16x16x32_bf16 v[128:131], v[138:141], v[232:235], 0\n" \
"ds_write_b128 v27, v[108:111] offset:13312\n" \
"v_mfma_f32_16x16x32_bf16 v[72:75], a[180:183], a[132:135], v[72:75]\n" \
"v_cvt_pk_bf16_f32 v116, v252, v253\n" \
"v_cvt_pk_bf16_f32 v117, v254, v255\n" \
"v_mfma_f32_16x16x32_bf16 v[72:75], a[244:247], a[196:199], v[72:75]\n" \
"v_exp_f32_e64 v45, v45\n" \
"v_mfma_f32_16x16x32_bf16 v[192:195], v[116:119], v[184:187], 0\n" \
"ds_write_b128 v27, v[52:55] offset:0\n" \
"v_mfma_f32_16x16x32_bf16 v[208:211], v[118:121], v[200:203], 0\n" \
"v_mfma_f32_16x16x32_bf16 v[224:227], v[120:123], v[216:219], 0\n" \
"v_mfma_f32_16x16x32_bf16 v[240:243], v[122:125], v[232:235], 0\n" \
"ds_write_b128 v27, v[60:63] offset:1024\n" \
"v_mov_b32_dpp v34, v38 quad_perm:[0,1,2,3] row_mask:0x2 bank_mask:0xf\n" \
"v_pk_mul_f32 v[72:73], v[72:73], s[70:71]\n" \
"v_pk_mul_f32 v[74:75], v[74:75], s[70:71]\n" \
"v_mov_b32_dpp v34, v250 quad_perm:[0,1,2,3] row_mask:0x4 bank_mask:0xf\n" \
"v_add_f32_e64 v45, 1.0, v45\n" \
"v_mfma_f32_16x16x32_bf16 v[68:71], a[176:179], a[132:135], v[68:71]\n" \
"v_mov_b32_dpp v34, v130 quad_perm:[0,1,2,3] row_mask:0x8 bank_mask:0xf\n" \
"v_mov_b32_dpp v32, v36 quad_perm:[0,1,2,3] row_mask:0x2 bank_mask:0xf\n" \
"v_rcp_f32_e64 v45, v45\n" \
"ds_write_b128 v27, v[76:79] offset:5120\n" \
"v_mov_b32_dpp v33, v37 quad_perm:[0,1,2,3] row_mask:0x2 bank_mask:0xf\n" \
"v_rcp_f32_e64 v34, v34\n" \
"v_mov_b32_dpp v32, v248 quad_perm:[0,1,2,3] row_mask:0x4 bank_mask:0xf\n" \
"v_mov_b32_dpp v33, v249 quad_perm:[0,1,2,3] row_mask:0x4 bank_mask:0xf\n" \
"v_mul_f32_e64 v34, v45, v34\n" \
"v_mov_b32_dpp v32, v128 quad_perm:[0,1,2,3] row_mask:0x8 bank_mask:0xf\n" \
"v_mov_b32_dpp v33, v129 quad_perm:[0,1,2,3] row_mask:0x8 bank_mask:0xf\n" \
"v_mul_f32_e32 v32, v34, v32\n" \
"v_mul_f32_e32 v33, v34, v33\n" \
"v_mov_b32_e64 v38, v34\n" \
"v_mfma_f32_16x16x32_bf16 v[68:71], a[240:243], a[196:199], v[68:71]\n" \
"v_sub_f32_e32 v44, 1.0, v45\n" \
"v_permlane16_swap_b32_e32 v34, v38\n" \
"v_mov_b32_e32 v250, v34\n" \
"v_mov_b32_e32 v130, v38\n" \
"v_cvt_pk_bf16_f32 v32, v32, v33\n" \
"v_permlane32_swap_b32_e32 v34, v250\n" \
"v_permlane32_swap_b32_e32 v38, v130\n" \
"v_mul_f32_e64 v192, v34, v192\n" \
"v_pk_mul_f32 v[68:69], v[68:69], s[70:71]\n" \
"v_pk_mul_f32 v[70:71], v[70:71], s[70:71]\n" \
"v_cvt_pk_bf16_f32 v68, v68, v69\n" \
"v_cvt_pk_bf16_f32 v69, v70, v71\n" \
"v_cvt_pk_bf16_f32 v70, v72, v73\n" \
"v_cvt_pk_bf16_f32 v71, v74, v75\n" \
"ds_write_b128 v27, v[68:71] offset:4096\n" \
"v_mul_f32_e32 v193, v34, v193\n" \
"v_mul_f32_e32 v208, v38, v208\n" \
"v_mul_f32_e32 v209, v38, v209\n" \
"v_mul_f32_e32 v224, v250, v224\n" \
"v_mul_f32_e32 v225, v250, v225\n" \
"v_mul_f32_e32 v240, v130, v240\n" \
"v_mul_f32_e32 v241, v130, v241\n" \
"v_mul_f32_e32 v194, v34, v194\n" \
"v_mul_f32_e64 v195, v34, v195\n" \
"v_cvt_pk_bf16_f32 v192, v192, v193\n" \
"v_mul_f32_e32 v210, v38, v210\n" \
"v_mul_f32_e32 v211, v38, v211\n" \
"v_cvt_pk_bf16_f32 v208, v208, v209\n" \
"v_mul_f32_e32 v226, v250, v226\n" \
"v_mul_f32_e32 v227, v250, v227\n" \
"v_cvt_pk_bf16_f32 v224, v224, v225\n" \
"v_mul_f32_e32 v242, v130, v242\n" \
"v_mul_f32_e32 v243, v130, v243\n" \
"v_cvt_pk_bf16_f32 v240, v240, v241\n" \
"v_cvt_pk_bf16_f32 v193, v194, v195\n" \
"s_add_u32 s84, s48, 0x0\n" \
"s_nop 0\n" \
"v_cvt_pk_bf16_f32 v209, v210, v211\n" \
"s_add_u32 s85, s48, 0x24800\n" \
"v_cvt_pk_bf16_f32 v225, v226, v227\n" \
"s_add_u32 s86, s48, 0x49000\n" \
"v_cvt_pk_bf16_f32 v241, v242, v243\n" \
"s_add_u32 s87, s48, 0x6d800\n" \
"ds_write_b32 v26, v44\n" \
"buffer_store_dword v32, v25, s[80:83], s49 offen sc1\n" \
"buffer_store_dwordx2 v[192:193], v24, s[80:83], s84 offen sc1\n" \
"buffer_store_dwordx2 v[208:209], v24, s[80:83], s85 offen sc1\n" \
"buffer_store_dwordx2 v[224:225], v24, s[80:83], s86 offen sc1\n" \
"buffer_store_dwordx2 v[240:241], v24, s[80:83], s87 offen sc1\n" \
"s_branch L_end_%=\n" \
"L_poll61_%=:\n" \
"s_mov_b32 s84, 0\n" \
"L_pl61_%=:\n" \
"s_sleep 1\n" \
"v_bfe_u32 v44, v20, 2, 5\n" \
"buffer_load_dword v44, v44, s[80:83], s38 offen offset:128 sc1\n" \
"s_waitcnt vmcnt(0)\n" \
"v_cmp_ne_u32_e32 vcc, s62, v44\n" \
"s_add_u32 s84, s84, 1\n" \
"s_nop 3\n" \
"s_cbranch_vccz L_pollr61_%=\n" \
"s_cmp_gt_u32 s84, 0x400000\n" \
"s_cbranch_scc0 L_pl61_%=\n" \
"s_branch L_pollr61_%=\n" \
"L_poll161_%=:\n" \
"s_mov_b32 s84, 0\n" \
"L_pl161_%=:\n" \
"s_sleep 1\n" \
"v_bfe_u32 v44, v20, 2, 5\n" \
"buffer_load_dword v44, v44, s[80:83], s38 offen offset:0 sc1\n" \
"s_waitcnt vmcnt(0)\n" \
"v_cmp_ne_u32_e32 vcc, s62, v44\n" \
"s_add_u32 s84, s84, 1\n" \
"s_nop 3\n" \
"s_cbranch_vccz L_pollr161_%=\n" \
"s_cmp_gt_u32 s84, 0x400000\n" \
"s_cbranch_scc0 L_pl161_%=\n" \
"s_branch L_pollr161_%=\n" \
"L_end_%=:\n"
#define PROJ_VCLOB "v21","v24","v25","v28","v29","v32","v33","v34","v35","v36","v37","v38","v39","v40","v41","v42","v43","v44","v45","v46","v47","v48","v49","v50","v51","v52","v53","v54","v55","v56","v57","v58","v59","v60","v61","v62","v63","v64","v65","v66","v67","v68","v69","v70","v71","v72","v73","v74","v75","v76","v77","v78","v79","v80","v81","v82","v83","v84","v85","v86","v87","v88","v89","v90","v91","v92","v93","v94","v95","v96","v97","v98","v99","v100","v101","v102","v103","v104","v105","v106","v107","v108","v109","v110","v111","v112","v113","v114","v115","v116","v117","v118","v119","v120","v121","v122","v123","v124","v125","v126","v127","v128","v129","v130","v131","v132","v133","v134","v135","v136","v137","v138","v139","v140","v141","v142","v143","v144","v145","v146","v147","v148","v149","v150","v151","v152","v153","v154","v155","v156","v157","v158","v159","v160","v161","v162","v163","v164","v165","v166","v167","v168","v169","v170","v171","v172","v173","v174","v175","v176","v177","v178","v179","v180","v181","v182","v183","v184","v185","v186","v187","v188","v189","v190","v192","v193","v194","v195","v196","v197","v198","v199","v200","v201","v202","v203","v204","v205","v206","v207","v208","v209","v210","v211","v212","v213","v214","v215","v216","v217","v218","v219","v220","v221","v222","v223","v224","v225","v226","v227","v228","v229","v230","v231","v232","v233","v234","v235","v236","v237","v238","v239","v240","v241","v242","v243","v244","v245","v246","v247","v248","v249","v250","v251","v252","v253","v254","v255"
#define PROJ_SCLOB "s4","s5","s6","s7","s32","s33","s34","s35","s36","s37","s38","s39","s40","s41","s42","s43","s44","s45","s46","s47","s48","s49","s50","s51","s52","s53","s54","s55","s63","s64","s65","s66","s67","s68","s69","s70","s71","s72","s73","s74","s75","s76","s77","s78","s79","s80","s81","s82","s83","s84","s85","s86","s87","s88","s89","s90","s91","s92","s93","s94","s95","s96","s97","s98","s99","s100","s101"

typedef const u16* const __attribute__((address_space(4)))* kaptr;
__device__ __forceinline__ void proj_phase(kaptr& ka, char*& ws, const unsigned wid, u32x4 (&vp)[5], unsigned& vl, u32x4 (&sa)[5], u32x2 s24, u32x2& s28, unsigned tdum, u32x4& srdA, u32x2& sd, unsigned& tag
    ) {
    const unsigned b = blockIdx.x, h = (b & 7) * 4 + ((b >> 3) & 3), mt = b >> 5;
    const unsigned rb = (wid < 2u ? 15u - mt : mt) * 128u;
    const unsigned lane = threadIdx.x & 63u, wc = wid & 1u;
    u32x2 vAc, vB;
#pragma unroll
    for (int par = 0; par < 2; ++par) {
        const unsigned lr = lane >> 3, cc = (lane & 7u) ^ (4u * par + (lr >> 1)), r0 = rb + wc * 64u + lr;
        vAc[par] = r0 * (DC * 2) + cc * 16u;
        const unsigned br = lane >> 4, p16 = lane & 15u, sc = (((p16 >> 1) ^ (br | (par << 2))) << 1) | (p16 & 1u);
        vB[par] = (16u * wid + br) * (NBIG * 2) + h * (HD * 2) + sc * 16u;
    }
    vl = lane * 16u;
    u32x2 s30 = {wid * 8192u, 65536u + wid * 4096u};
    asm volatile(PROJ_ASM
                 : "={s62}"(tag), "={s[56:59]}"(srdA), "={s[60:61]}"(sd), "+{s[8:11]}"(sa[0]), "+{s[12:15]}"(sa[1]), "+{s[16:19]}"(sa[2]), "+{s[20:23]}"(sa[3]), "+{s[24:25]}"(s24), "+{s[28:29]}"(s28), "+{s[30:31]}"(s30),
                   "={v[0:3]}"(vp[0]), "={v[4:7]}"(vp[1]), "={v[8:11]}"(vp[2]), "={v[12:15]}"(vp[3]), "={v[16:19]}"(vp[4]), "+{v[22:23]}"(vAc), "+{v[26:27]}"(vB), "+{v191}"(tdum), "+{s[26:27]}"(ws), "+{s[0:1]}"(ka)
                 : "{s2}"(b), "{s3}"(wid), "{v20}"(vl)
                 : "memory", "m0", "vcc", "scc", PROJ_VCLOB, PROJ_SCLOB, ACLOB_LO, ACLOB_HI);
    sa[4] = (u32x4){s24[0], 0u, 0u, 0u};
}

namespace att {
#define DI __device__ __forceinline__
typedef __bf16 bfx8 __attribute__((ext_vector_type(8)));
DI unsigned rfl(unsigned x) { return __builtin_amdgcn_readfirstlane(x); }
DI f32x4 mfma(u32x4 a, u32x4 b, f32x4 c) { return __builtin_amdgcn_mfma_f32_16x16x32_bf16(__builtin_bit_cast(bfx8, a), __builtin_bit_cast(bfx8, b), c, 0, 0, 0); }
typedef __bf16 bfx2 __attribute__((ext_vector_type(2)));
typedef float f32x2 __attribute__((ext_vector_type(2)));
DI unsigned cvtpk2(float lo, float hi) { f32x2 v = {lo, hi}; bfx2 b = __builtin_convertvector(v, bfx2); return __builtin_bit_cast(unsigned, b); }
DI void att_exact(const u16* __restrict__ x, const u16* __restrict__ wq, char* __restrict__ ws, unsigned h, unsigned r0, float g1) {
    const unsigned lane = lane_id(), g = lane >> 4, c = lane & 15u;
    f32x4 o[8];
#pragma unroll
    for (int n = 0; n < 8; ++n) o[n] = (f32x4){0.f, 0.f, 0.f, 0.f};
#pragma unroll 1
    for (unsigned kk = 0; kk < (unsigned)DM; kk += 32u) {
        const unsigned k = kk + 8u * g;
        const u32x4 xf = *reinterpret_cast<const u32x4*>(x + (r0 + c) * DM + k);
#pragma unroll
        for (int n = 0; n < 8; ++n) {
            const u16* w = wq + k * NBIG + h * HD + n * 16 + c; u32x4 wf;
#pragma unroll
            for (int e = 0; e < 4; ++e) wf[e] = (unsigned)w[2 * e * NBIG] | ((unsigned)w[(2 * e + 1) * NBIG] << 16);
            o[n] = mfma(wf, xf, o[n]);
        }
    }
    u32x4 q[4];
#pragma unroll
    for (int p = 0; p < 4; ++p) {
        const f32x4 a = o[2 * p] * 0.12751743082f, b = o[2 * p + 1] * 0.12751743082f;
        q[p] = (u32x4){cvtpk2(a[0], a[1]), cvtpk2(a[2], a[3]), cvtpk2(b[0], b[1]), cvtpk2(b[2], b[3])};
    }
#pragma unroll
    for (int n = 0; n < 8; ++n) o[n] = (f32x4){0.f, 0.f, 0.f, 0.f};
    const __amdgpu_buffer_rsrc_t srd = __builtin_amdgcn_make_buffer_rsrc((void*)(ws + WS_K + (size_t)h * 1048576), (short)0, 1048576, 0x00020000);
    float m = -3.0e38f, l = 0.f;
#pragma unroll 1
    for (unsigned k0 = 0; k0 <= r0 + 15u; k0 += 32u) {
        const unsigned tb = (k0 >> 6) * 32768u + (k0 & 32u) * 256u;
        f32x4 s[2]; float mx = m;
#pragma unroll
        for (int kt = 0; kt < 2; ++kt) {
            s[kt] = (f32x4){0.f, 0.f, 0.f, 0.f};
#pragma unroll
            for (int p = 0; p < 4; ++p) s[kt] = mfma(__builtin_amdgcn_raw_buffer_load_b128(srd, lane * 16u + (kt * 4 + p) * 1024u, tb, 16), q[p], s[kt]);
#pragma unroll
            for (int e = 0; e < 4; ++e) { if (k0 + 16u * kt + 4u * g + e > r0 + c) s[kt][e] = -3.0e38f; mx = fmaxf(mx, s[kt][e]); }
        }
        mx = fmaxf(mx, __shfl_xor(mx, 16)); mx = fmaxf(mx, __shfl_xor(mx, 32));
        const float al = __builtin_amdgcn_exp2f(m - mx); m = mx;
        u32x4 pf;
#pragma unroll
        for (int e = 0; e < 4; ++e) pf[e] = cvtpk2(__builtin_amdgcn_exp2f(s[e >> 1][2 * (e & 1)] - m), __builtin_amdgcn_exp2f(s[e >> 1][2 * (e & 1) + 1] - m));
        l *= al;
#pragma unroll
        for (int e = 0; e < 4; ++e) l += __uint_as_float(pf[e] << 16) + __uint_as_float(pf[e] & 0xffff0000u);
#pragma unroll
        for (int n = 0; n < 8; ++n) o[n] = mfma(__builtin_amdgcn_raw_buffer_load_b128(srd, lane * 16u + 16384u + n * 1024u, tb, 16), pf, o[n] * al);
    }
    l += __shfl_xor(l, 16); l += __shfl_xor(l, 32);
    const float f = g1 / l;
    const __amdgpu_buffer_rsrc_t srdA = __builtin_amdgcn_make_buffer_rsrc((void*)(ws + WS_AF), (short)0, 128 * KS_TOT * 1024, 0x00020000);
#pragma unroll
    for (int p = 0; p < 4; ++p) {
        const f32x4 a = o[2 * p] * f, b = o[2 * p + 1] * f;
        __builtin_amdgcn_raw_buffer_store_b128((u32x4){cvtpk2(a[0], a[1]), cvtpk2(a[2], a[3]), cvtpk2(b[0], b[1]), cvtpk2(b[2], b[3])}, srdA, lane * 16u + p * 1024u, ((r0 >> 4) * KS_TOT + 18u + 4u * h) * 1024u, 16);
    }
}
}

#define ATT_ASM \
"s_branch L_ago_%=\n" \
".p2align 12\n" \
"L_ab:\n" \
".fill (L_ab + 0 - .) / 4, 4, 0xBF800000\n" \
"L_ap0:\n" \
"s_setpc_b64 s[88:89]\n" \
"L_ago_%=:\n" \
"s_mov_b32 s101, 0\n" \
"s_waitcnt vmcnt(15) lgkmcnt(0)\n" \
"s_barrier\n" \
"v_accvgpr_write_b32 a216, 0\n" \
"v_accvgpr_write_b32 a217, 0\n" \
"v_accvgpr_write_b32 a218, 0\n" \
"v_accvgpr_write_b32 a219, 0\n" \
"v_bfe_u32 v61, v20, 2, 5\n" \
"s_mov_b32 s96, s24\n" \
"v_mfma_f32_32x32x16_bf16 v[128:143], a[216:219], a[216:219], 0\n" \
"buffer_load_dword v57, v61, s[56:59], s96 offen sc1\n" \
"v_and_b32_e32 v63, 0x300, v20\n" \
"ds_read_b128 v[96:99], v63 offset:0\n" \
"v_add_u32_e32 v58, s22, v20\n" \
"ds_read_b128 a[0:3], v58 offset:0\n" \
"v_mfma_f32_32x32x16_bf16 v[144:159], a[216:219], a[216:219], 0\n" \
"ds_read_b128 a[16:19], v58 offset:8192\n" \
"v_add_u32_e32 v59, s23, v20\n" \
"ds_read_b128 a[32:35], v59 offset:0\n" \
"ds_read_b128 a[48:51], v59 offset:8192\n" \
"ds_read_b128 v[100:103], v63 offset:1024\n" \
"ds_read_b128 a[4:7], v58 offset:1024\n" \
"v_mfma_f32_32x32x16_bf16 v[160:175], a[216:219], a[216:219], 0\n" \
"ds_read_b128 a[20:23], v58 offset:9216\n" \
"ds_read_b128 a[36:39], v59 offset:1024\n" \
"ds_read_b128 a[52:55], v59 offset:9216\n" \
"s_sub_u32 s67, s12, 0xc000\n" \
"ds_read_b128 v[104:107], v63 offset:2048\n" \
"s_mov_b32 s65, s12\n" \
"v_mfma_f32_32x32x16_bf16 v[176:191], a[216:219], a[216:219], 0\n" \
"v_add_u32_e32 v55, s67, v20\n" \
"v_add_u32_e32 v52, 0xc000, v55\n" \
"s_xor_b32 m0, s65, 0x10000\n" \
"ds_read_b128 a[8:11], v58 offset:2048\n" \
"buffer_load_dwordx4 v52, s[8:11], 0 offen offset:0 sc1 lds\n" \
"v_mfma_f32_32x32x16_bf16 v[192:207], a[216:219], a[216:219], 0\n" \
"ds_read_b128 a[24:27], v58 offset:10240\n" \
"ds_read_b128 a[40:43], v59 offset:2048\n" \
"ds_read_b128 a[56:59], v59 offset:10240\n" \
"ds_read_b128 v[108:111], v63 offset:3072\n" \
"buffer_load_dwordx4 v52, s[8:11], 0 offen offset:1024 sc1 lds\n" \
"v_mfma_f32_32x32x16_bf16 v[208:223], a[216:219], a[216:219], 0\n" \
"ds_read_b128 a[12:15], v58 offset:3072\n" \
"ds_read_b128 a[28:31], v58 offset:11264\n" \
"ds_read_b128 a[44:47], v59 offset:3072\n" \
"ds_read_b128 a[60:63], v59 offset:11264\n" \
"s_waitcnt lgkmcnt(15)\n" \
"v_xor_b32_e32 v96, 0x80008000, v96\n" \
"v_mfma_f32_32x32x16_bf16 v[224:239], a[216:219], a[216:219], 0\n" \
"v_xor_b32_e32 v97, 0x80008000, v97\n" \
"v_xor_b32_e32 v98, 0x80008000, v98\n" \
"v_xor_b32_e32 v99, 0x80008000, v99\n" \
"ds_read_b128 a[160:163], v20 offset:0\n" \
"ds_read_b128 a[164:167], v20 offset:1024\n" \
"s_waitcnt lgkmcnt(15)\n" \
"v_xor_b32_e32 v100, 0x80008000, v100\n" \
"v_mfma_f32_32x32x16_bf16 v[240:255], a[216:219], a[216:219], 0\n" \
"v_xor_b32_e32 v101, 0x80008000, v101\n" \
"v_xor_b32_e32 v102, 0x80008000, v102\n" \
"v_xor_b32_e32 v103, 0x80008000, v103\n" \
"ds_read_b128 a[168:171], v20 offset:2048\n" \
"ds_read_b128 a[172:175], v20 offset:3072\n" \
"ds_read_b128 a[188:191], v20 offset:7168\n" \
"v_mfma_f32_16x16x32_bf16 a[192:195], a[216:219], a[216:219], 0\n" \
"ds_read_b128 a[184:187], v20 offset:6144\n" \
"s_waitcnt lgkmcnt(15)\n" \
"v_xor_b32_e32 v104, 0x80008000, v104\n" \
"v_mfma_f32_16x16x32_bf16 a[196:199], a[216:219], a[216:219], 0\n" \
"v_xor_b32_e32 v105, 0x80008000, v105\n" \
"v_xor_b32_e32 v106, 0x80008000, v106\n" \
"v_mfma_f32_16x16x32_bf16 v[32:35], v[96:99], a[0:3], 0\n" \
"v_xor_b32_e32 v107, 0x80008000, v107\n" \
"ds_read_b128 a[180:183], v20 offset:5120\n" \
"v_mfma_f32_16x16x32_bf16 v[36:39], v[96:99], a[16:19], 0\n" \
"ds_read_b128 a[176:179], v20 offset:4096\n" \
"s_waitcnt lgkmcnt(12)\n" \
"v_xor_b32_e32 v108, 0x80008000, v108\n" \
"v_mfma_f32_16x16x32_bf16 v[40:43], v[96:99], a[32:35], 0\n" \
"v_xor_b32_e32 v109, 0x80008000, v109\n" \
"v_xor_b32_e32 v110, 0x80008000, v110\n" \
"v_mfma_f32_16x16x32_bf16 v[44:47], v[96:99], a[48:51], 0\n" \
"v_xor_b32_e32 v111, 0x80008000, v111\n" \
"v_add_u32_e32 v60, s15, v20\n" \
"v_mfma_f32_16x16x32_bf16 v[32:35], v[100:103], a[4:7], v[32:35]\n" \
"v_add_u32_e32 v60, 0x10000, v60\n" \
"ds_read_b128 a[128:131], v20 offset:8192\n" \
"v_mfma_f32_16x16x32_bf16 v[36:39], v[100:103], a[20:23], v[36:39]\n" \
"ds_read_b128 a[144:147], v20 offset:12288\n" \
"ds_read_b128 a[132:135], v20 offset:9216\n" \
"v_mfma_f32_16x16x32_bf16 v[40:43], v[100:103], a[36:39], v[40:43]\n" \
"ds_read_b128 a[148:151], v20 offset:13312\n" \
"ds_read_b128 a[136:139], v20 offset:10240\n" \
"v_mfma_f32_16x16x32_bf16 v[44:47], v[100:103], a[52:55], v[44:47]\n" \
"ds_read_b128 a[152:155], v20 offset:14336\n" \
"ds_read_b128 a[140:143], v20 offset:11264\n" \
"v_mfma_f32_16x16x32_bf16 v[32:35], v[104:107], a[8:11], v[32:35]\n" \
"ds_read_b128 a[156:159], v20 offset:15360\n" \
"ds_read_b128 a[64:67], v60 offset:16384\n" \
"v_mfma_f32_16x16x32_bf16 v[36:39], v[104:107], a[24:27], v[36:39]\n" \
"ds_read_b128 a[68:71], v60 offset:17408\n" \
"ds_read_b128 a[72:75], v60 offset:18432\n" \
"v_mfma_f32_16x16x32_bf16 v[40:43], v[104:107], a[40:43], v[40:43]\n" \
"ds_read_b128 a[76:79], v60 offset:19456\n" \
"ds_read_b128 a[80:83], v60 offset:24576\n" \
"v_mfma_f32_16x16x32_bf16 v[44:47], v[104:107], a[56:59], v[44:47]\n" \
"ds_read_b128 a[84:87], v60 offset:25600\n" \
"s_waitcnt lgkmcnt(15)\n" \
"v_mfma_f32_16x16x32_bf16 v[32:35], v[108:111], a[12:15], v[32:35]\n" \
"ds_read_b128 a[88:91], v60 offset:26624\n" \
"ds_read_b128 a[92:95], v60 offset:27648\n" \
"v_mfma_f32_16x16x32_bf16 v[36:39], v[108:111], a[28:31], v[36:39]\n" \
"ds_read_b128 a[96:99], v60 offset:0\n" \
"ds_read_b128 a[100:103], v60 offset:1024\n" \
"v_mfma_f32_16x16x32_bf16 v[40:43], v[108:111], a[44:47], v[40:43]\n" \
"ds_read_b128 a[104:107], v60 offset:2048\n" \
"ds_read_b128 a[108:111], v60 offset:3072\n" \
"v_mfma_f32_16x16x32_bf16 v[44:47], v[108:111], a[60:63], v[44:47]\n" \
"ds_read_b128 a[112:115], v60 offset:8192\n" \
"ds_read_b128 a[116:119], v60 offset:9216\n" \
"v_mfma_f32_16x16x32_bf16 v[64:67], a[160:163], a[0:3], v[32:35]\n" \
"ds_read_b128 a[120:123], v60 offset:10240\n" \
"ds_read_b128 a[124:127], v60 offset:11264\n" \
"v_mfma_f32_16x16x32_bf16 v[64:67], a[164:167], a[4:7], v[64:67]\n" \
"v_bfe_u32 v26, v20, 8, 2\n" \
"s_mov_b32 m0, s20\n" \
"v_mfma_f32_16x16x32_bf16 v[64:67], a[168:171], a[8:11], v[64:67]\n" \
"v_bfe_u32 v25, v20, 5, 3\n" \
"v_lshlrev_b32_e32 v26, 1, v26\n" \
"v_mfma_f32_16x16x32_bf16 v[64:67], a[172:175], a[12:15], v[64:67]\n" \
"global_load_lds_dword v[16:17], off\n" \
"v_mfma_f32_16x16x32_bf16 v[68:71], a[188:191], a[12:15], v[32:35]\n" \
"v_sub_u32_e32 v25, v25, v26\n" \
"v_and_b32_e32 v26, 16, v20\n" \
"v_mfma_f32_16x16x32_bf16 v[68:71], a[184:187], a[8:11], v[68:71]\n" \
"v_mov_b32_e32 v27, 0x3f80\n" \
"v_lshlrev_b32_e32 v26, v26, v27\n" \
"v_mfma_f32_16x16x32_bf16 v[68:71], a[180:183], a[4:7], v[68:71]\n" \
"v_mov_b32_e32 v54, 0x3f803f80\n" \
"s_waitcnt lgkmcnt(15)\n" \
"v_mfma_f32_16x16x32_bf16 v[68:71], a[176:179], a[0:3], v[68:71]\n" \
"s_mov_b32 s84, 0x33333333\n" \
"s_mov_b32 s85, 0xcccccccc\n" \
"v_mfma_f32_16x16x32_bf16 v[76:79], a[176:179], a[16:19], v[36:39]\n" \
"s_mov_b32 s86, 0x11111111\n" \
"s_mov_b32 s87, 0x11111111\n" \
"v_mfma_f32_16x16x32_bf16 v[76:79], a[180:183], a[20:23], v[76:79]\n" \
"s_mov_b32 s88, 0x44444444\n" \
"s_mov_b32 s89, 0x44444444\n" \
"v_mfma_f32_16x16x32_bf16 v[76:79], a[184:187], a[24:27], v[76:79]\n" \
"v_exp_f32_e32 v70, v70\n" \
"v_mfma_f32_16x16x32_bf16 v[76:79], a[188:191], a[28:31], v[76:79]\n" \
"v_exp_f32_e32 v71, v71\n" \
"v_mfma_f32_16x16x32_bf16 v[72:75], a[172:175], a[28:31], v[36:39]\n" \
"v_exp_f32_e32 v68, v68\n" \
"v_mfma_f32_16x16x32_bf16 v[72:75], a[168:171], a[24:27], v[72:75]\n" \
"v_exp_f32_e32 v69, v69\n" \
"v_mfma_f32_16x16x32_bf16 v[72:75], a[164:167], a[20:23], v[72:75]\n" \
"v_bfe_i32 v21, v20, 4, 1\n" \
"v_cvt_pk_bf16_f32 v71, v70, v71\n" \
"v_mfma_f32_16x16x32_bf16 v[72:75], a[160:163], a[16:19], v[72:75]\n" \
"v_exp_f32_e32 v66, v66\n" \
"v_mfma_f32_16x16x32_bf16 v[80:83], a[160:163], a[32:35], v[40:43]\n" \
"v_exp_f32_e32 v67, v67\n" \
"v_mfma_f32_16x16x32_bf16 v[80:83], a[164:167], a[36:39], v[80:83]\n" \
"v_exp_f32_e32 v64, v64\n" \
"v_mfma_f32_16x16x32_bf16 v[80:83], a[168:171], a[40:43], v[80:83]\n" \
"v_exp_f32_e32 v65, v65\n" \
"v_mfma_f32_16x16x32_bf16 v[80:83], a[172:175], a[44:47], v[80:83]\n" \
"v_exp_f32_e32 v72, v72\n" \
"v_mfma_f32_16x16x32_bf16 v[84:87], a[188:191], a[44:47], v[40:43]\n" \
"s_waitcnt vmcnt(3)\n" \
"v_cmp_ne_u32_e32 vcc, s62, v57\n" \
"v_mfma_f32_16x16x32_bf16 v[84:87], a[184:187], a[40:43], v[84:87]\n" \
"v_exp_f32_e32 v73, v73\n" \
"v_mfma_f32_16x16x32_bf16 v[84:87], a[180:183], a[36:39], v[84:87]\n" \
"v_exp_f32_e32 v74, v74\n" \
"v_mfma_f32_16x16x32_bf16 v[84:87], a[176:179], a[32:35], v[84:87]\n" \
"s_cbranch_vccnz L_skb_%=\n" \
"L_rkb_%=:\n" \
"v_cmp_eq_u32_e32 vcc, 0, v25\n" \
"v_mfma_f32_16x16x32_bf16 v[92:95], a[176:179], a[48:51], v[44:47]\n" \
"v_exp_f32_e32 v75, v75\n" \
"v_mfma_f32_16x16x32_bf16 v[92:95], a[180:183], a[52:55], v[92:95]\n" \
"v_cndmask_b32_e32 v28, 0, v26, vcc\n" \
"v_cvt_pk_bf16_f32 v72, v72, v73\n" \
"v_mfma_f32_16x16x32_bf16 v[92:95], a[184:187], a[56:59], v[92:95]\n" \
"v_exp_f32_e32 v76, v76\n" \
"v_mfma_f32_16x16x32_bf16 v[92:95], a[188:191], a[60:63], v[92:95]\n" \
"v_exp_f32_e32 v77, v77\n" \
"v_mfma_f32_16x16x32_bf16 v[88:91], a[172:175], a[60:63], v[44:47]\n" \
"v_exp_f32_e32 v78, v78\n" \
"v_mfma_f32_16x16x32_bf16 v[88:91], a[168:171], a[56:59], v[88:91]\n" \
"v_exp_f32_e32 v79, v79\n" \
"v_mfma_f32_16x16x32_bf16 v[88:91], a[164:167], a[52:55], v[88:91]\n" \
"v_cndmask_b32_e64 v22, 0, v54, s[84:85]\n" \
"v_cndmask_b32_e64 v23, 0, v54, s[86:87]\n" \
"v_mfma_f32_16x16x32_bf16 v[88:91], a[160:163], a[48:51], v[88:91]\n" \
"v_cndmask_b32_e64 v24, 0, v54, s[88:89]\n" \
"v_and_b32_e32 v21, 0xaaaaaaaa, v21\n" \
"s_waitcnt vmcnt(4) lgkmcnt(0)\n" \
"s_barrier\n" \
"v_cmp_eq_u32_e32 vcc, 1, v25\n" \
"v_cvt_pk_bf16_f32 v70, v68, v69\n" \
"v_cvt_pk_bf16_f32 v73, v74, v75\n" \
"v_mov_b32_e32 v62, 0\n" \
"v_accvgpr_write_b32 a208, 0\n" \
"v_accvgpr_write_b32 a209, 0\n" \
"v_accvgpr_write_b32 a210, 0\n" \
"v_accvgpr_write_b32 a211, 0\n" \
"v_accvgpr_write_b32 a200, v22\n" \
"v_accvgpr_write_b32 a201, v22\n" \
"v_accvgpr_write_b32 a202, v22\n" \
"v_accvgpr_write_b32 a203, v22\n" \
"v_accvgpr_write_b32 a204, v23\n" \
"v_accvgpr_write_b32 a205, v23\n" \
"v_accvgpr_write_b32 a206, v23\n" \
"v_accvgpr_write_b32 a207, v23\n" \
"v_accvgpr_write_b32 a212, v24\n" \
"v_accvgpr_write_b32 a213, v24\n" \
"v_accvgpr_write_b32 a214, v24\n" \
"v_accvgpr_write_b32 a215, v24\n" \
"v_or_b32_e32 v21, 0x44444444, v21\n" \
"s_mov_b32 s64, 0x8000\n" \
"v_add_u32_e32 v50, 0x6000, v55\n" \
"v_add_u32_e32 v51, 0xa000, v55\n" \
"v_add_u32_e32 v53, 0x10000, v55\n" \
"v_add_u32_e32 v48, 0xc000, v20\n" \
"v_add_u32_e32 v49, s17, v20\n" \
"v_cndmask_b32_e32 v29, 0, v26, vcc\n" \
"v_accvgpr_write_b32 a220, v96\n" \
"v_accvgpr_write_b32 a221, v97\n" \
"v_accvgpr_write_b32 a222, v98\n" \
"v_accvgpr_write_b32 a223, v99\n" \
"v_accvgpr_write_b32 a224, v100\n" \
"v_accvgpr_write_b32 a225, v101\n" \
"v_accvgpr_write_b32 a226, v102\n" \
"v_accvgpr_write_b32 a227, v103\n" \
"v_accvgpr_write_b32 a228, v104\n" \
"v_accvgpr_write_b32 a229, v105\n" \
"v_accvgpr_write_b32 a230, v106\n" \
"v_accvgpr_write_b32 a231, v107\n" \
"v_accvgpr_write_b32 a232, v108\n" \
"v_accvgpr_write_b32 a233, v109\n" \
"v_accvgpr_write_b32 a234, v110\n" \
"v_accvgpr_write_b32 a235, v111\n" \
"v_cvt_pk_bf16_f32 v69, v66, v67\n" \
"v_cvt_pk_bf16_f32 v68, v64, v65\n" \
"v_cvt_pk_bf16_f32 v74, v76, v77\n" \
"v_cvt_pk_bf16_f32 v75, v78, v79\n" \
"s_sub_u32 s66, s18, 1\n" \
"s_cbranch_scc1 L_tr_%=\n" \
".fill (L_ab + 1856 - .) / 4, 4, 0xBF800000\n" \
"L_1_%=:\n" \
"v_mfma_f32_16x16x32_bf16 v[96:99], a[128:131], a[0:3], v[32:35]\n" \
"s_xor_b32 m0, s65, 0x12000\n" \
"ds_read_b128 a[160:163], v49 offset:0\n" \
"v_mfma_f32_16x16x32_bf16 v[96:99], a[132:135], a[4:7], v[96:99]\n" \
"buffer_load_dwordx4 v50, s[8:11], s64 offen offset:0 sc1 lds\n" \
"v_mfma_f32_16x16x32_bf16 v[96:99], a[136:139], a[8:11], v[96:99]\n" \
"ds_read_b128 a[164:167], v49 offset:1024\n" \
"ds_read_b128 a[168:171], v49 offset:2048\n" \
"v_mfma_f32_16x16x32_bf16 v[96:99], a[140:143], a[12:15], v[96:99]\n" \
"ds_read_b128 a[172:175], v49 offset:3072\n" \
"ds_read_b128 a[176:179], v49 offset:4096\n" \
"v_mfma_f32_16x16x32_bf16 v[100:103], a[156:159], a[12:15], v[32:35]\n" \
"ds_read_b128 a[180:183], v49 offset:5120\n" \
"ds_read_b128 a[184:187], v49 offset:6144\n" \
"v_mfma_f32_16x16x32_bf16 v[100:103], a[152:155], a[8:11], v[100:103]\n" \
"v_exp_f32_e64 v86, v86\n" \
"v_mfma_f32_16x16x32_bf16 v[100:103], a[148:151], a[4:7], v[100:103]\n" \
"v_exp_f32_e64 v87, v87\n" \
"v_mfma_f32_16x16x32_bf16 v[100:103], a[144:147], a[0:3], v[100:103]\n" \
"ds_read_b128 a[188:191], v49 offset:7168\n" \
"v_cvt_pk_bf16_f32 v87, v86, v87\n" \
"v_mfma_f32_16x16x32_bf16 v[108:111], a[144:147], a[16:19], v[36:39]\n" \
"v_exp_f32_e64 v84, v84\n" \
"v_mfma_f32_16x16x32_bf16 v[108:111], a[148:151], a[20:23], v[108:111]\n" \
"v_exp_f32_e64 v85, v85\n" \
"v_mfma_f32_16x16x32_bf16 v[108:111], a[152:155], a[24:27], v[108:111]\n" \
"v_exp_f32_e64 v82, v82\n" \
"v_mfma_f32_16x16x32_bf16 v[108:111], a[156:159], a[28:31], v[108:111]\n" \
"v_exp_f32_e64 v83, v83\n" \
"v_mfma_f32_16x16x32_bf16 v[104:107], a[140:143], a[28:31], v[36:39]\n" \
"v_exp_f32_e64 v80, v80\n" \
"v_mfma_f32_16x16x32_bf16 v[104:107], a[136:139], a[24:27], v[104:107]\n" \
"v_exp_f32_e64 v81, v81\n" \
"v_mfma_f32_16x16x32_bf16 v[104:107], a[132:135], a[20:23], v[104:107]\n" \
"v_exp_f32_e64 v88, v88\n" \
"v_mfma_f32_16x16x32_bf16 v[104:107], a[128:131], a[16:19], v[104:107]\n" \
"buffer_load_dwordx4 v50, s[8:11], s64 offen offset:1024 sc1 lds\n" \
"v_mfma_f32_16x16x32_bf16 v[112:115], a[128:131], a[32:35], v[40:43]\n" \
"s_xor_b32 m0, s65, 0x6000\n" \
"v_cvt_pk_bf16_f32 v86, v84, v85\n" \
"v_mfma_f32_16x16x32_bf16 v[112:115], a[132:135], a[36:39], v[112:115]\n" \
"v_cvt_pk_bf16_f32 v85, v82, v83\n" \
"v_cvt_pk_bf16_f32 v84, v80, v81\n" \
"v_mfma_f32_16x16x32_bf16 v[112:115], a[136:139], a[40:43], v[112:115]\n" \
"v_exp_f32_e64 v89, v89\n" \
"v_mfma_f32_16x16x32_bf16 v[112:115], a[140:143], a[44:47], v[112:115]\n" \
"v_exp_f32_e64 v90, v90\n" \
"v_mfma_f32_16x16x32_bf16 v[116:119], a[156:159], a[44:47], v[40:43]\n" \
"v_exp_f32_e64 v91, v91\n" \
"v_mfma_f32_16x16x32_bf16 v[116:119], a[152:155], a[40:43], v[116:119]\n" \
"v_exp_f32_e64 v92, v92\n" \
"v_mfma_f32_16x16x32_bf16 v[116:119], a[148:151], a[36:39], v[116:119]\n" \
"v_exp_f32_e64 v93, v93\n" \
"v_mfma_f32_16x16x32_bf16 v[116:119], a[144:147], a[32:35], v[116:119]\n" \
"v_exp_f32_e64 v94, v94\n" \
"v_mfma_f32_16x16x32_bf16 v[124:127], a[144:147], a[48:51], v[44:47]\n" \
"v_cvt_pk_bf16_f32 v88, v88, v89\n" \
"v_cvt_pk_bf16_f32 v89, v90, v91\n" \
"v_mfma_f32_16x16x32_bf16 v[124:127], a[148:151], a[52:55], v[124:127]\n" \
"v_exp_f32_e64 v95, v95\n" \
"v_mfma_f32_16x16x32_bf16 v[124:127], a[152:155], a[56:59], v[124:127]\n" \
"v_cvt_pk_bf16_f32 v90, v92, v93\n" \
"v_cvt_pk_bf16_f32 v91, v94, v95\n" \
"v_mfma_f32_16x16x32_bf16 v[124:127], a[156:159], a[60:63], v[124:127]\n" \
"ds_read_b128 a[144:147], v48 offset:4096\n" \
"ds_read_b128 a[148:151], v48 offset:5120\n" \
"v_mfma_f32_16x16x32_bf16 v[120:123], a[140:143], a[60:63], v[44:47]\n" \
"ds_read_b128 a[152:155], v48 offset:6144\n" \
"ds_read_b128 a[140:143], v48 offset:3072\n" \
"v_mfma_f32_16x16x32_bf16 v[120:123], a[136:139], a[56:59], v[120:123]\n" \
"ds_read_b128 a[136:139], v48 offset:2048\n" \
"ds_read_b128 a[156:159], v48 offset:7168\n" \
"v_mfma_f32_16x16x32_bf16 v[120:123], a[132:135], a[52:55], v[120:123]\n" \
"buffer_load_dwordx4 v51, s[8:11], s64 offen offset:0 sc1 lds\n" \
"v_mfma_f32_16x16x32_bf16 v[120:123], a[128:131], a[48:51], v[120:123]\n" \
"ds_read_b128 a[128:131], v48 offset:0\n" \
"s_waitcnt lgkmcnt(7)\n" \
"v_mfma_f32_16x16x32_bf16 v[128:131], a[160:163], v[68:71], v[128:131]\n" \
"v_exp_f32_e32 v102, v102\n" \
"v_mfma_f32_16x16x32_bf16 v[160:163], a[160:163], v[72:75], v[160:163]\n" \
"v_exp_f32_e64 v103, v103\n" \
"v_mfma_f32_16x16x32_bf16 v[132:135], a[164:167], v[68:71], v[132:135]\n" \
"ds_read_b128 a[132:135], v48 offset:1024\n" \
"v_cvt_pk_bf16_f32 v103, v102, v103\n" \
"v_mfma_f32_16x16x32_bf16 v[164:167], a[164:167], v[72:75], v[164:167]\n" \
"v_exp_f32_e64 v100, v100\n" \
"v_mfma_f32_16x16x32_bf16 v[136:139], a[168:171], v[68:71], v[136:139]\n" \
"v_exp_f32_e64 v101, v101\n" \
"v_mfma_f32_16x16x32_bf16 v[168:171], a[168:171], v[72:75], v[168:171]\n" \
"v_exp_f32_e64 v98, v98\n" \
"v_mfma_f32_16x16x32_bf16 v[140:143], a[172:175], v[68:71], v[140:143]\n" \
"v_exp_f32_e64 v99, v99\n" \
"v_mfma_f32_16x16x32_bf16 v[172:175], a[172:175], v[72:75], v[172:175]\n" \
"v_exp_f32_e64 v96, v96\n" \
"v_mfma_f32_16x16x32_bf16 v[144:147], a[176:179], v[68:71], v[144:147]\n" \
"v_exp_f32_e64 v97, v97\n" \
"v_mfma_f32_16x16x32_bf16 v[176:179], a[176:179], v[72:75], v[176:179]\n" \
"v_exp_f32_e64 v104, v104\n" \
"v_mfma_f32_16x16x32_bf16 v[148:151], a[180:183], v[68:71], v[148:151]\n" \
"v_exp_f32_e64 v105, v105\n" \
"v_mfma_f32_16x16x32_bf16 v[180:183], a[180:183], v[72:75], v[180:183]\n" \
"v_cvt_pk_bf16_f32 v102, v100, v101\n" \
"v_cvt_pk_bf16_f32 v101, v98, v99\n" \
"v_mfma_f32_16x16x32_bf16 v[152:155], a[184:187], v[68:71], v[152:155]\n" \
"v_cvt_pk_bf16_f32 v100, v96, v97\n" \
"v_cvt_pk_bf16_f32 v104, v104, v105\n" \
"v_mfma_f32_16x16x32_bf16 v[184:187], a[184:187], v[72:75], v[184:187]\n" \
"buffer_load_dwordx4 v51, s[8:11], s64 offen offset:1024 sc1 lds\n" \
"v_mfma_f32_16x16x32_bf16 v[156:159], a[188:191], v[68:71], v[156:159]\n" \
"v_exp_f32_e64 v106, v106\n" \
"v_mfma_f32_16x16x32_bf16 v[188:191], a[188:191], v[72:75], v[188:191]\n" \
"v_exp_f32_e64 v107, v107\n" \
"v_smfmac_f32_16x16x64_bf16 a[192:195], a[200:203], v[68:75], v21\n" \
"v_exp_f32_e64 v108, v108\n" \
"v_mfma_f32_16x16x32_bf16 v[220:223], a[188:191], v[84:87], v[220:223]\n" \
"v_exp_f32_e64 v109, v109\n" \
"v_mfma_f32_16x16x32_bf16 v[252:255], a[188:191], v[88:91], v[252:255]\n" \
"v_exp_f32_e64 v110, v110\n" \
"v_mfma_f32_16x16x32_bf16 v[216:219], a[184:187], v[84:87], v[216:219]\n" \
"v_exp_f32_e64 v111, v111\n" \
"v_mfma_f32_16x16x32_bf16 v[248:251], a[184:187], v[88:91], v[248:251]\n" \
"v_exp_f32_e64 v118, v118\n" \
"v_mfma_f32_16x16x32_bf16 v[212:215], a[180:183], v[84:87], v[212:215]\n" \
"v_exp_f32_e64 v119, v119\n" \
"v_mfma_f32_16x16x32_bf16 v[244:247], a[180:183], v[88:91], v[244:247]\n" \
"v_cvt_pk_bf16_f32 v105, v106, v107\n" \
"v_cvt_pk_bf16_f32 v106, v108, v109\n" \
"v_mfma_f32_16x16x32_bf16 v[208:211], a[176:179], v[84:87], v[208:211]\n" \
"v_cvt_pk_bf16_f32 v107, v110, v111\n" \
"v_cvt_pk_bf16_f32 v119, v118, v119\n" \
"v_mfma_f32_16x16x32_bf16 v[240:243], a[176:179], v[88:91], v[240:243]\n" \
"v_exp_f32_e64 v116, v116\n" \
"v_mfma_f32_16x16x32_bf16 v[204:207], a[172:175], v[84:87], v[204:207]\n" \
"v_exp_f32_e64 v117, v117\n" \
"v_mfma_f32_16x16x32_bf16 v[236:239], a[172:175], v[88:91], v[236:239]\n" \
"v_exp_f32_e64 v114, v114\n" \
"v_mfma_f32_16x16x32_bf16 v[200:203], a[168:171], v[84:87], v[200:203]\n" \
"v_exp_f32_e64 v115, v115\n" \
"v_mfma_f32_16x16x32_bf16 v[232:235], a[168:171], v[88:91], v[232:235]\n" \
"v_exp_f32_e64 v112, v112\n" \
"v_mfma_f32_16x16x32_bf16 v[196:199], a[164:167], v[84:87], v[196:199]\n" \
"v_exp_f32_e64 v113, v113\n" \
"v_mfma_f32_16x16x32_bf16 v[228:231], a[164:167], v[88:91], v[228:231]\n" \
"v_exp_f32_e64 v120, v120\n" \
"v_mfma_f32_16x16x32_bf16 v[192:195], a[160:163], v[84:87], v[192:195]\n" \
"v_exp_f32_e64 v121, v121\n" \
"v_mfma_f32_16x16x32_bf16 v[224:227], a[160:163], v[88:91], v[224:227]\n" \
"v_cvt_pk_bf16_f32 v118, v116, v117\n" \
"v_cvt_pk_bf16_f32 v117, v114, v115\n" \
"v_smfmac_f32_16x16x64_bf16 a[196:199], a[200:203], v[84:91], v21\n" \
"v_cvt_pk_bf16_f32 v116, v112, v113\n" \
"v_cvt_pk_bf16_f32 v120, v120, v121\n" \
"s_waitcnt vmcnt(4) lgkmcnt(0)\n" \
"s_barrier\n" \
"v_mfma_f32_16x16x32_bf16 v[64:67], a[128:131], a[0:3], v[32:35]\n" \
"s_xor_b32 m0, s65, 0x14000\n" \
"ds_read_b128 a[160:163], v49 offset:8192\n" \
"v_mfma_f32_16x16x32_bf16 v[64:67], a[132:135], a[4:7], v[64:67]\n" \
"buffer_load_dwordx4 v52, s[8:11], s64 offen offset:0 sc1 lds\n" \
"v_mfma_f32_16x16x32_bf16 v[64:67], a[136:139], a[8:11], v[64:67]\n" \
"ds_read_b128 a[164:167], v49 offset:9216\n" \
"ds_read_b128 a[168:171], v49 offset:10240\n" \
"v_mfma_f32_16x16x32_bf16 v[64:67], a[140:143], a[12:15], v[64:67]\n" \
"ds_read_b128 a[172:175], v49 offset:11264\n" \
"ds_read_b128 a[176:179], v49 offset:12288\n" \
"v_mfma_f32_16x16x32_bf16 v[68:71], a[156:159], a[12:15], v[32:35]\n" \
"ds_read_b128 a[180:183], v49 offset:13312\n" \
"ds_read_b128 a[184:187], v49 offset:14336\n" \
"v_mfma_f32_16x16x32_bf16 v[68:71], a[152:155], a[8:11], v[68:71]\n" \
"v_exp_f32_e64 v122, v122\n" \
"v_mfma_f32_16x16x32_bf16 v[68:71], a[148:151], a[4:7], v[68:71]\n" \
"v_exp_f32_e64 v123, v123\n" \
"v_mfma_f32_16x16x32_bf16 v[68:71], a[144:147], a[0:3], v[68:71]\n" \
"ds_read_b128 a[188:191], v49 offset:15360\n" \
"v_cvt_pk_bf16_f32 v121, v122, v123\n" \
"v_mfma_f32_16x16x32_bf16 v[76:79], a[144:147], a[16:19], v[36:39]\n" \
"v_exp_f32_e64 v124, v124\n" \
"v_mfma_f32_16x16x32_bf16 v[76:79], a[148:151], a[20:23], v[76:79]\n" \
"v_exp_f32_e64 v125, v125\n" \
"v_mfma_f32_16x16x32_bf16 v[76:79], a[152:155], a[24:27], v[76:79]\n" \
"v_exp_f32_e64 v126, v126\n" \
"v_mfma_f32_16x16x32_bf16 v[76:79], a[156:159], a[28:31], v[76:79]\n" \
"v_exp_f32_e64 v127, v127\n" \
"v_mfma_f32_16x16x32_bf16 v[72:75], a[140:143], a[28:31], v[36:39]\n" \
"v_cvt_pk_bf16_f32 v122, v124, v125\n" \
"v_cvt_pk_bf16_f32 v123, v126, v127\n" \
"v_mfma_f32_16x16x32_bf16 v[72:75], a[136:139], a[24:27], v[72:75]\n" \
"v_exp_f32_e64 v70, v70\n" \
"v_mfma_f32_16x16x32_bf16 v[72:75], a[132:135], a[20:23], v[72:75]\n" \
"v_exp_f32_e64 v71, v71\n" \
"v_mfma_f32_16x16x32_bf16 v[72:75], a[128:131], a[16:19], v[72:75]\n" \
"buffer_load_dwordx4 v52, s[8:11], s64 offen offset:1024 sc1 lds\n" \
"v_mfma_f32_16x16x32_bf16 v[80:83], a[128:131], a[32:35], v[40:43]\n" \
"s_mov_b32 m0, s65\n" \
"v_cvt_pk_bf16_f32 v71, v70, v71\n" \
"v_mfma_f32_16x16x32_bf16 v[80:83], a[132:135], a[36:39], v[80:83]\n" \
"v_exp_f32_e32 v68, v68\n" \
"v_mfma_f32_16x16x32_bf16 v[80:83], a[136:139], a[40:43], v[80:83]\n" \
"v_exp_f32_e64 v69, v69\n" \
"v_mfma_f32_16x16x32_bf16 v[80:83], a[140:143], a[44:47], v[80:83]\n" \
"v_exp_f32_e64 v72, v72\n" \
"v_mfma_f32_16x16x32_bf16 v[84:87], a[156:159], a[44:47], v[40:43]\n" \
"v_exp_f32_e64 v73, v73\n" \
"v_mfma_f32_16x16x32_bf16 v[84:87], a[152:155], a[40:43], v[84:87]\n" \
"v_exp_f32_e64 v74, v74\n" \
"v_mfma_f32_16x16x32_bf16 v[84:87], a[148:151], a[36:39], v[84:87]\n" \
"v_exp_f32_e64 v75, v75\n" \
"v_mfma_f32_16x16x32_bf16 v[84:87], a[144:147], a[32:35], v[84:87]\n" \
"v_exp_f32_e64 v66, v66\n" \
"v_mfma_f32_16x16x32_bf16 v[92:95], a[144:147], a[48:51], v[44:47]\n" \
"ds_read_b128 a[144:147], v48 offset:12288\n" \
"v_cvt_pk_bf16_f32 v72, v72, v73\n" \
"v_mfma_f32_16x16x32_bf16 v[92:95], a[148:151], a[52:55], v[92:95]\n" \
"ds_read_b128 a[148:151], v48 offset:13312\n" \
"v_cvt_pk_bf16_f32 v70, v68, v69\n" \
"v_mfma_f32_16x16x32_bf16 v[92:95], a[152:155], a[56:59], v[92:95]\n" \
"ds_read_b128 a[152:155], v48 offset:14336\n" \
"v_cvt_pk_bf16_f32 v73, v74, v75\n" \
"v_mfma_f32_16x16x32_bf16 v[92:95], a[156:159], a[60:63], v[92:95]\n" \
"ds_read_b128 a[156:159], v48 offset:15360\n" \
"v_add_u32_e32 v49, 0x10000, v48\n" \
"v_mfma_f32_16x16x32_bf16 v[88:91], a[140:143], a[60:63], v[44:47]\n" \
"ds_read_b128 a[140:143], v48 offset:11264\n" \
"s_xor_b32 s65, s65, 0x4000\n" \
"v_mfma_f32_16x16x32_bf16 v[88:91], a[136:139], a[56:59], v[88:91]\n" \
"v_exp_f32_e64 v67, v67\n" \
"v_mfma_f32_16x16x32_bf16 v[88:91], a[132:135], a[52:55], v[88:91]\n" \
"buffer_load_dwordx4 v53, s[8:11], s64 offen offset:0 sc1 lds\n" \
"v_mfma_f32_16x16x32_bf16 v[88:91], a[128:131], a[48:51], v[88:91]\n" \
"ds_read_b128 a[128:131], v48 offset:8192\n" \
"s_waitcnt lgkmcnt(6)\n" \
"v_mfma_f32_16x16x32_bf16 v[128:131], a[160:163], v[100:103], v[128:131]\n" \
"ds_read_b128 a[132:135], v48 offset:9216\n" \
"ds_read_b128 a[136:139], v48 offset:10240\n" \
"v_mfma_f32_16x16x32_bf16 v[160:163], a[160:163], v[104:107], v[160:163]\n" \
"v_exp_f32_e32 v64, v64\n" \
"v_mfma_f32_16x16x32_bf16 v[132:135], a[164:167], v[100:103], v[132:135]\n" \
"v_exp_f32_e64 v65, v65\n" \
"v_mfma_f32_16x16x32_bf16 v[164:167], a[164:167], v[104:107], v[164:167]\n" \
"v_exp_f32_e64 v76, v76\n" \
"v_mfma_f32_16x16x32_bf16 v[136:139], a[168:171], v[100:103], v[136:139]\n" \
"v_exp_f32_e64 v77, v77\n" \
"v_mfma_f32_16x16x32_bf16 v[168:171], a[168:171], v[104:107], v[168:171]\n" \
"v_exp_f32_e64 v78, v78\n" \
"v_mfma_f32_16x16x32_bf16 v[140:143], a[172:175], v[100:103], v[140:143]\n" \
"v_exp_f32_e64 v79, v79\n" \
"v_mfma_f32_16x16x32_bf16 v[172:175], a[172:175], v[104:107], v[172:175]\n" \
"v_cvt_pk_bf16_f32 v69, v66, v67\n" \
"v_cvt_pk_bf16_f32 v68, v64, v65\n" \
"v_mfma_f32_16x16x32_bf16 v[144:147], a[176:179], v[100:103], v[144:147]\n" \
"v_cvt_pk_bf16_f32 v74, v76, v77\n" \
"v_cvt_pk_bf16_f32 v75, v78, v79\n" \
"v_mfma_f32_16x16x32_bf16 v[176:179], a[176:179], v[104:107], v[176:179]\n" \
"v_xor_b32_e32 v48, 0x4000, v48\n" \
"v_mfma_f32_16x16x32_bf16 v[148:151], a[180:183], v[100:103], v[148:151]\n" \
"v_mfma_f32_16x16x32_bf16 v[180:183], a[180:183], v[104:107], v[180:183]\n" \
"v_mfma_f32_16x16x32_bf16 v[152:155], a[184:187], v[100:103], v[152:155]\n" \
"v_mfma_f32_16x16x32_bf16 v[184:187], a[184:187], v[104:107], v[184:187]\n" \
"buffer_load_dwordx4 v53, s[8:11], s64 offen offset:1024 sc1 lds\n" \
"v_mfma_f32_16x16x32_bf16 v[156:159], a[188:191], v[100:103], v[156:159]\n" \
"s_add_u32 s64, s64, 0x8000\n" \
"s_sub_u32 s66, s66, 1\n" \
"s_nop 0\n" \
"v_mfma_f32_16x16x32_bf16 v[188:191], a[188:191], v[104:107], v[188:191]\n" \
"v_smfmac_f32_16x16x64_bf16 a[192:195], a[200:203], v[100:107], v21\n" \
"v_mfma_f32_16x16x32_bf16 v[220:223], a[188:191], v[116:119], v[220:223]\n" \
"v_mfma_f32_16x16x32_bf16 v[252:255], a[188:191], v[120:123], v[252:255]\n" \
"v_mfma_f32_16x16x32_bf16 v[216:219], a[184:187], v[116:119], v[216:219]\n" \
"v_mfma_f32_16x16x32_bf16 v[248:251], a[184:187], v[120:123], v[248:251]\n" \
"v_mfma_f32_16x16x32_bf16 v[212:215], a[180:183], v[116:119], v[212:215]\n" \
"v_mfma_f32_16x16x32_bf16 v[244:247], a[180:183], v[120:123], v[244:247]\n" \
"v_mfma_f32_16x16x32_bf16 v[208:211], a[176:179], v[116:119], v[208:211]\n" \
"v_mfma_f32_16x16x32_bf16 v[240:243], a[176:179], v[120:123], v[240:243]\n" \
"v_mfma_f32_16x16x32_bf16 v[204:207], a[172:175], v[116:119], v[204:207]\n" \
"v_mfma_f32_16x16x32_bf16 v[236:239], a[172:175], v[120:123], v[236:239]\n" \
"v_mfma_f32_16x16x32_bf16 v[200:203], a[168:171], v[116:119], v[200:203]\n" \
"v_mfma_f32_16x16x32_bf16 v[232:235], a[168:171], v[120:123], v[232:235]\n" \
"v_mfma_f32_16x16x32_bf16 v[196:199], a[164:167], v[116:119], v[196:199]\n" \
"v_mfma_f32_16x16x32_bf16 v[228:231], a[164:167], v[120:123], v[228:231]\n" \
"v_mfma_f32_16x16x32_bf16 v[192:195], a[160:163], v[116:119], v[192:195]\n" \
"v_mfma_f32_16x16x32_bf16 v[224:227], a[160:163], v[120:123], v[224:227]\n" \
"v_smfmac_f32_16x16x64_bf16 a[196:199], a[200:203], v[116:123], v21\n" \
"s_waitcnt vmcnt(4) lgkmcnt(0)\n" \
"s_barrier\n" \
"s_cbranch_scc0 L_1_%=\n" \
"L_tr_%=:\n" \
"s_setpc_b64 s[28:29]\n" \
".fill (L_ab + 4096 - .) / 4, 4, 0xBF800000\n" \
".fill (L_ab + 4096 - .) / 4, 4, 0xBF800000\n" \
"L_ap1:\n" \
"s_setpc_b64 s[88:89]\n" \
"L_gtr:\n" \
"v_mfma_f32_16x16x32_bf16 v[96:99], a[128:131], a[0:3], v[32:35]\n" \
"v_mov_b32_e32 v56, s62\n" \
"v_readlane_b32 s68, v11, 0\n" \
"v_mfma_f32_16x16x32_bf16 v[96:99], a[132:135], a[4:7], v[96:99]\n" \
"s_xor_b32 m0, s65, 0x12000\n" \
"s_mov_b32 s65, s13\n" \
"v_mfma_f32_16x16x32_bf16 v[96:99], a[136:139], a[8:11], v[96:99]\n" \
"buffer_load_dwordx4 v50, s[8:11], s64 offen offset:0 sc1 lds\n" \
"v_mfma_f32_16x16x32_bf16 v[96:99], a[140:143], a[12:15], v[96:99]\n" \
"v_add_u32_e32 v63, s15, v20\n" \
"s_add_u32 s67, s14, 40960\n" \
"v_mfma_f32_16x16x32_bf16 v[100:103], a[156:159], a[12:15], v[32:35]\n" \
"buffer_load_dwordx4 v50, s[8:11], s64 offen offset:1024 sc1 lds\n" \
"s_mov_b64 exec, 1\n" \
"buffer_store_dword v56, off, s[56:59], s68 offset:0 sc1\n" \
"s_mov_b64 exec, -1\n" \
"v_mfma_f32_16x16x32_bf16 v[100:103], a[152:155], a[8:11], v[100:103]\n" \
"v_add_u32_e32 v50, 0x6000, v63\n" \
"s_xor_b32 m0, s65, 0x8000\n" \
"v_mfma_f32_16x16x32_bf16 v[100:103], a[148:151], a[4:7], v[100:103]\n" \
"ds_read_b128 a[160:163], v49 offset:0\n" \
"ds_read_b128 a[164:167], v49 offset:1024\n" \
"v_mfma_f32_16x16x32_bf16 v[100:103], a[144:147], a[0:3], v[100:103]\n" \
"ds_read_b128 a[168:171], v49 offset:2048\n" \
"ds_read_b128 a[172:175], v49 offset:3072\n" \
"v_mfma_f32_16x16x32_bf16 v[108:111], a[144:147], a[16:19], v[36:39]\n" \
"buffer_load_dwordx4 v50, s[8:11], s67 offen offset:0 sc1 lds\n" \
"v_mfma_f32_16x16x32_bf16 v[108:111], a[148:151], a[20:23], v[108:111]\n" \
"ds_read_b128 a[176:179], v49 offset:4096\n" \
"ds_read_b128 a[180:183], v49 offset:5120\n" \
"v_mfma_f32_16x16x32_bf16 v[108:111], a[152:155], a[24:27], v[108:111]\n" \
"ds_read_b128 a[184:187], v49 offset:6144\n" \
"ds_read_b128 a[188:191], v49 offset:7168\n" \
"v_mfma_f32_16x16x32_bf16 v[108:111], a[156:159], a[28:31], v[108:111]\n" \
"s_mov_b32 s64, s14\n" \
"v_add_u32_e32 v51, 0x12000, v63\n" \
"v_mfma_f32_16x16x32_bf16 v[104:107], a[140:143], a[28:31], v[36:39]\n" \
"buffer_load_dwordx4 v50, s[8:11], s67 offen offset:1024 sc1 lds\n" \
"v_mfma_f32_16x16x32_bf16 v[104:107], a[136:139], a[24:27], v[104:107]\n" \
"v_exp_f32_e32 v86, v86\n" \
"v_mfma_f32_16x16x32_bf16 v[104:107], a[132:135], a[20:23], v[104:107]\n" \
"v_exp_f32_e32 v87, v87\n" \
"v_mfma_f32_16x16x32_bf16 v[104:107], a[128:131], a[16:19], v[104:107]\n" \
"v_exp_f32_e32 v84, v84\n" \
"v_mfma_f32_16x16x32_bf16 v[112:115], a[128:131], a[32:35], v[40:43]\n" \
"v_exp_f32_e32 v85, v85\n" \
"v_mfma_f32_16x16x32_bf16 v[112:115], a[132:135], a[36:39], v[112:115]\n" \
"buffer_load_dwordx4 v50, s[8:11], s67 offen offset:2048 sc1 lds\n" \
"v_mfma_f32_16x16x32_bf16 v[112:115], a[136:139], a[40:43], v[112:115]\n" \
"v_cvt_pk_bf16_f32 v87, v86, v87\n" \
"v_cvt_pk_bf16_f32 v86, v84, v85\n" \
"v_mfma_f32_16x16x32_bf16 v[112:115], a[140:143], a[44:47], v[112:115]\n" \
"v_exp_f32_e32 v82, v82\n" \
"v_mfma_f32_16x16x32_bf16 v[116:119], a[156:159], a[44:47], v[40:43]\n" \
"v_exp_f32_e32 v83, v83\n" \
"v_mfma_f32_16x16x32_bf16 v[116:119], a[152:155], a[40:43], v[116:119]\n" \
"buffer_load_dwordx4 v50, s[8:11], s67 offen offset:3072 sc1 lds\n" \
"v_mfma_f32_16x16x32_bf16 v[116:119], a[148:151], a[36:39], v[116:119]\n" \
"s_xor_b32 m0, s65, 0xa000\n" \
"v_cvt_pk_bf16_f32 v85, v82, v83\n" \
"v_mfma_f32_16x16x32_bf16 v[116:119], a[144:147], a[32:35], v[116:119]\n" \
"v_exp_f32_e32 v80, v80\n" \
"v_mfma_f32_16x16x32_bf16 v[124:127], a[144:147], a[48:51], v[44:47]\n" \
"v_exp_f32_e32 v81, v81\n" \
"v_mfma_f32_16x16x32_bf16 v[124:127], a[148:151], a[52:55], v[124:127]\n" \
"buffer_load_dwordx4 v51, s[8:11], s64 offen offset:0 sc1 lds\n" \
"v_mfma_f32_16x16x32_bf16 v[124:127], a[152:155], a[56:59], v[124:127]\n" \
"v_cvt_pk_bf16_f32 v84, v80, v81\n" \
"v_readlane_b32 s93, v11, 19\n" \
"v_mfma_f32_16x16x32_bf16 v[124:127], a[156:159], a[60:63], v[124:127]\n" \
"v_exp_f32_e32 v88, v88\n" \
"v_mfma_f32_16x16x32_bf16 v[120:123], a[140:143], a[60:63], v[44:47]\n" \
"v_exp_f32_e32 v89, v89\n" \
"v_mfma_f32_16x16x32_bf16 v[120:123], a[136:139], a[56:59], v[120:123]\n" \
"v_exp_f32_e32 v90, v90\n" \
"v_mfma_f32_16x16x32_bf16 v[120:123], a[132:135], a[52:55], v[120:123]\n" \
"buffer_load_dwordx4 v51, s[8:11], s64 offen offset:1024 sc1 lds\n" \
"v_mfma_f32_16x16x32_bf16 v[120:123], a[128:131], a[48:51], v[120:123]\n" \
"v_cvt_pk_bf16_f32 v88, v88, v89\n" \
"s_waitcnt lgkmcnt(0)\n" \
"v_mfma_f32_16x16x32_bf16 v[128:131], a[160:163], v[68:71], v[128:131]\n" \
"v_exp_f32_e32 v91, v91\n" \
"v_mfma_f32_16x16x32_bf16 v[160:163], a[160:163], v[72:75], v[160:163]\n" \
"v_exp_f32_e32 v92, v92\n" \
"v_mfma_f32_16x16x32_bf16 v[132:135], a[164:167], v[68:71], v[132:135]\n" \
"buffer_load_dwordx4 v51, s[8:11], s64 offen offset:2048 sc1 lds\n" \
"v_mfma_f32_16x16x32_bf16 v[164:167], a[164:167], v[72:75], v[164:167]\n" \
"v_exp_f32_e32 v93, v93\n" \
"v_mfma_f32_16x16x32_bf16 v[136:139], a[168:171], v[68:71], v[136:139]\n" \
"v_exp_f32_e32 v94, v94\n" \
"v_mfma_f32_16x16x32_bf16 v[168:171], a[168:171], v[72:75], v[168:171]\n" \
"v_exp_f32_e32 v95, v95\n" \
"v_mfma_f32_16x16x32_bf16 v[140:143], a[172:175], v[68:71], v[140:143]\n" \
"buffer_load_dwordx4 v51, s[8:11], s64 offen offset:3072 sc1 lds\n" \
"v_mfma_f32_16x16x32_bf16 v[172:175], a[172:175], v[72:75], v[172:175]\n" \
"v_cvt_pk_bf16_f32 v89, v90, v91\n" \
"v_cvt_pk_bf16_f32 v90, v92, v93\n" \
"v_mfma_f32_16x16x32_bf16 v[144:147], a[176:179], v[68:71], v[144:147]\n" \
"v_cvt_pk_bf16_f32 v91, v94, v95\n" \
"v_readlane_b32 s80, v11, 11\n" \
"v_mfma_f32_16x16x32_bf16 v[176:179], a[176:179], v[72:75], v[176:179]\n" \
"v_readlane_b32 s81, v11, 12\n" \
"s_mov_b32 s82, 0x47fca0\n" \
"v_mfma_f32_16x16x32_bf16 v[148:151], a[180:183], v[68:71], v[148:151]\n" \
"s_mov_b32 s83, 0x20000\n" \
"v_readlane_b32 s92, v11, 26\n" \
"v_mfma_f32_16x16x32_bf16 v[180:183], a[180:183], v[72:75], v[180:183]\n" \
"s_add_u32 m0, s93, 0x0\n" \
"ds_read_b128 a[128:131], v48 offset:0\n" \
"v_mfma_f32_16x16x32_bf16 v[152:155], a[184:187], v[68:71], v[152:155]\n" \
"ds_read_b128 a[144:147], v48 offset:4096\n" \
"ds_read_b128 a[132:135], v48 offset:1024\n" \
"v_mfma_f32_16x16x32_bf16 v[184:187], a[184:187], v[72:75], v[184:187]\n" \
"buffer_load_dwordx4 v0, s[80:83], s92 offen offset:0 lds\n" \
"v_mfma_f32_16x16x32_bf16 v[156:159], a[188:191], v[68:71], v[156:159]\n" \
"ds_read_b128 a[148:151], v48 offset:5120\n" \
"ds_read_b128 a[136:139], v48 offset:2048\n" \
"v_mfma_f32_16x16x32_bf16 v[188:191], a[188:191], v[72:75], v[188:191]\n" \
"ds_read_b128 a[152:155], v48 offset:6144\n" \
"ds_read_b128 a[140:143], v48 offset:3072\n" \
"v_smfmac_f32_16x16x64_bf16 a[192:195], a[200:203], v[68:75], v21\n" \
"ds_read_b128 a[156:159], v48 offset:7168\n" \
"v_add_u32_e32 v52, 0x14000, v63\n" \
"v_mfma_f32_16x16x32_bf16 v[220:223], a[188:191], v[84:87], v[220:223]\n" \
"buffer_load_dwordx4 v1, s[80:83], s92 offen offset:1024 lds\n" \
"v_mfma_f32_16x16x32_bf16 v[252:255], a[188:191], v[88:91], v[252:255]\n" \
"v_exp_f32_e32 v102, v102\n" \
"v_mfma_f32_16x16x32_bf16 v[216:219], a[184:187], v[84:87], v[216:219]\n" \
"buffer_load_dwordx4 v2, s[80:83], s92 offen offset:2048 lds\n" \
"v_mfma_f32_16x16x32_bf16 v[248:251], a[184:187], v[88:91], v[248:251]\n" \
"v_exp_f32_e32 v103, v103\n" \
"v_mfma_f32_16x16x32_bf16 v[212:215], a[180:183], v[84:87], v[212:215]\n" \
"v_add_u32_e32 v53, 0x20000, v63\n" \
"v_cvt_pk_bf16_f32 v103, v102, v103\n" \
"v_mfma_f32_16x16x32_bf16 v[244:247], a[180:183], v[88:91], v[244:247]\n" \
"v_exp_f32_e32 v100, v100\n" \
"v_mfma_f32_16x16x32_bf16 v[208:211], a[176:179], v[84:87], v[208:211]\n" \
"buffer_load_dwordx4 v3, s[80:83], s92 offen offset:3072 lds\n" \
"v_mfma_f32_16x16x32_bf16 v[240:243], a[176:179], v[88:91], v[240:243]\n" \
"v_exp_f32_e32 v101, v101\n" \
"v_mfma_f32_16x16x32_bf16 v[204:207], a[172:175], v[84:87], v[204:207]\n" \
"v_exp_f32_e32 v98, v98\n" \
"v_mfma_f32_16x16x32_bf16 v[236:239], a[172:175], v[88:91], v[236:239]\n" \
"v_exp_f32_e32 v99, v99\n" \
"v_mfma_f32_16x16x32_bf16 v[200:203], a[168:171], v[84:87], v[200:203]\n" \
"v_exp_f32_e32 v96, v96\n" \
"v_mfma_f32_16x16x32_bf16 v[232:235], a[168:171], v[88:91], v[232:235]\n" \
"v_exp_f32_e32 v97, v97\n" \
"v_mfma_f32_16x16x32_bf16 v[196:199], a[164:167], v[84:87], v[196:199]\n" \
"v_exp_f32_e32 v104, v104\n" \
"v_mfma_f32_16x16x32_bf16 v[228:231], a[164:167], v[88:91], v[228:231]\n" \
"v_exp_f32_e32 v105, v105\n" \
"v_mfma_f32_16x16x32_bf16 v[192:195], a[160:163], v[84:87], v[192:195]\n" \
"v_cvt_pk_bf16_f32 v102, v100, v101\n" \
"v_cvt_pk_bf16_f32 v101, v98, v99\n" \
"v_mfma_f32_16x16x32_bf16 v[224:227], a[160:163], v[88:91], v[224:227]\n" \
"v_cvt_pk_bf16_f32 v100, v96, v97\n" \
"v_cvt_pk_bf16_f32 v104, v104, v105\n" \
"v_smfmac_f32_16x16x64_bf16 a[196:199], a[200:203], v[84:91], v21\n" \
"v_exp_f32_e32 v106, v106\n" \
"s_waitcnt vmcnt(15) lgkmcnt(0)\n" \
"s_barrier\n" \
"v_mfma_f32_16x16x32_bf16 v[64:67], a[128:131], a[0:3], v[32:35]\n" \
"s_xor_b32 m0, s65, 0x18000\n" \
"ds_read_b128 a[160:163], v49 offset:8192\n" \
"v_mfma_f32_16x16x32_bf16 v[64:67], a[132:135], a[4:7], v[64:67]\n" \
"buffer_load_dwordx4 v52, s[8:11], s64 offen offset:0 sc1 lds\n" \
"v_mfma_f32_16x16x32_bf16 v[64:67], a[136:139], a[8:11], v[64:67]\n" \
"ds_read_b128 a[164:167], v49 offset:9216\n" \
"ds_read_b128 a[168:171], v49 offset:10240\n" \
"v_mfma_f32_16x16x32_bf16 v[64:67], a[140:143], a[12:15], v[64:67]\n" \
"ds_read_b128 a[172:175], v49 offset:11264\n" \
"ds_read_b128 a[176:179], v49 offset:12288\n" \
"v_mfma_f32_16x16x32_bf16 v[68:71], a[156:159], a[12:15], v[32:35]\n" \
"buffer_load_dwordx4 v52, s[8:11], s64 offen offset:1024 sc1 lds\n" \
"v_mfma_f32_16x16x32_bf16 v[68:71], a[152:155], a[8:11], v[68:71]\n" \
"ds_read_b128 a[180:183], v49 offset:13312\n" \
"ds_read_b128 a[184:187], v49 offset:14336\n" \
"v_mfma_f32_16x16x32_bf16 v[68:71], a[148:151], a[4:7], v[68:71]\n" \
"v_exp_f32_e32 v107, v107\n" \
"v_mfma_f32_16x16x32_bf16 v[68:71], a[144:147], a[0:3], v[68:71]\n" \
"v_exp_f32_e32 v108, v108\n" \
"v_mfma_f32_16x16x32_bf16 v[76:79], a[144:147], a[16:19], v[36:39]\n" \
"v_exp_f32_e32 v109, v109\n" \
"v_mfma_f32_16x16x32_bf16 v[76:79], a[148:151], a[20:23], v[76:79]\n" \
"buffer_load_dwordx4 v52, s[8:11], s64 offen offset:2048 sc1 lds\n" \
"v_mfma_f32_16x16x32_bf16 v[76:79], a[152:155], a[24:27], v[76:79]\n" \
"v_exp_f32_e32 v110, v110\n" \
"v_mfma_f32_16x16x32_bf16 v[76:79], a[156:159], a[28:31], v[76:79]\n" \
"v_exp_f32_e32 v111, v111\n" \
"v_mfma_f32_16x16x32_bf16 v[72:75], a[140:143], a[28:31], v[36:39]\n" \
"v_cvt_pk_bf16_f32 v105, v106, v107\n" \
"v_cvt_pk_bf16_f32 v106, v108, v109\n" \
"v_mfma_f32_16x16x32_bf16 v[72:75], a[136:139], a[24:27], v[72:75]\n" \
"buffer_load_dwordx4 v52, s[8:11], s64 offen offset:3072 sc1 lds\n" \
"v_mfma_f32_16x16x32_bf16 v[72:75], a[132:135], a[20:23], v[72:75]\n" \
"s_mov_b32 m0, s65\n" \
"v_cvt_pk_bf16_f32 v107, v110, v111\n" \
"v_mfma_f32_16x16x32_bf16 v[72:75], a[128:131], a[16:19], v[72:75]\n" \
"ds_read_b128 a[188:191], v49 offset:15360\n" \
"v_add_u32_e32 v49, 0x10000, v48\n" \
"v_mfma_f32_16x16x32_bf16 v[80:83], a[128:131], a[32:35], v[40:43]\n" \
"v_exp_f32_e32 v118, v118\n" \
"v_mfma_f32_16x16x32_bf16 v[80:83], a[132:135], a[36:39], v[80:83]\n" \
"v_exp_f32_e32 v119, v119\n" \
"v_mfma_f32_16x16x32_bf16 v[80:83], a[136:139], a[40:43], v[80:83]\n" \
"buffer_load_dwordx4 v53, s[8:11], s64 offen offset:0 sc1 lds\n" \
"v_mfma_f32_16x16x32_bf16 v[80:83], a[140:143], a[44:47], v[80:83]\n" \
"v_exp_f32_e32 v116, v116\n" \
"v_mfma_f32_16x16x32_bf16 v[84:87], a[156:159], a[44:47], v[40:43]\n" \
"v_exp_f32_e32 v117, v117\n" \
"v_mfma_f32_16x16x32_bf16 v[84:87], a[152:155], a[40:43], v[84:87]\n" \
"v_cvt_pk_bf16_f32 v119, v118, v119\n" \
"v_cvt_pk_bf16_f32 v118, v116, v117\n" \
"v_mfma_f32_16x16x32_bf16 v[84:87], a[148:151], a[36:39], v[84:87]\n" \
"v_exp_f32_e32 v114, v114\n" \
"v_mfma_f32_16x16x32_bf16 v[84:87], a[144:147], a[32:35], v[84:87]\n" \
"buffer_load_dwordx4 v53, s[8:11], s64 offen offset:1024 sc1 lds\n" \
"v_mfma_f32_16x16x32_bf16 v[92:95], a[144:147], a[48:51], v[44:47]\n" \
"v_exp_f32_e32 v115, v115\n" \
"v_mfma_f32_16x16x32_bf16 v[92:95], a[148:151], a[52:55], v[92:95]\n" \
"v_exp_f32_e32 v112, v112\n" \
"v_mfma_f32_16x16x32_bf16 v[92:95], a[152:155], a[56:59], v[92:95]\n" \
"v_exp_f32_e32 v113, v113\n" \
"v_mfma_f32_16x16x32_bf16 v[92:95], a[156:159], a[60:63], v[92:95]\n" \
"buffer_load_dwordx4 v53, s[8:11], s64 offen offset:2048 sc1 lds\n" \
"v_mfma_f32_16x16x32_bf16 v[88:91], a[140:143], a[60:63], v[44:47]\n" \
"v_cvt_pk_bf16_f32 v117, v114, v115\n" \
"v_cvt_pk_bf16_f32 v116, v112, v113\n" \
"v_mfma_f32_16x16x32_bf16 v[88:91], a[136:139], a[56:59], v[88:91]\n" \
"v_exp_f32_e32 v120, v120\n" \
"v_mfma_f32_16x16x32_bf16 v[88:91], a[132:135], a[52:55], v[88:91]\n" \
"v_exp_f32_e32 v121, v121\n" \
"v_mfma_f32_16x16x32_bf16 v[88:91], a[128:131], a[48:51], v[88:91]\n" \
"v_cvt_pk_bf16_f32 v120, v120, v121\n" \
"s_waitcnt lgkmcnt(0)\n" \
"v_mfma_f32_16x16x32_bf16 v[128:131], a[160:163], v[100:103], v[128:131]\n" \
"buffer_load_dwordx4 v53, s[8:11], s64 offen offset:3072 sc1 lds\n" \
"v_mfma_f32_16x16x32_bf16 v[160:163], a[160:163], v[104:107], v[160:163]\n" \
"s_add_u32 m0, s93, 0x1000\n" \
"ds_read_b128 a[128:131], v48 offset:8192\n" \
"v_mfma_f32_16x16x32_bf16 v[132:135], a[164:167], v[100:103], v[132:135]\n" \
"v_exp_f32_e32 v122, v122\n" \
"v_mfma_f32_16x16x32_bf16 v[164:167], a[164:167], v[104:107], v[164:167]\n" \
"v_exp_f32_e32 v123, v123\n" \
"v_mfma_f32_16x16x32_bf16 v[136:139], a[168:171], v[100:103], v[136:139]\n" \
"buffer_load_dwordx4 v4, s[80:83], s92 offen offset:0 lds\n" \
"v_mfma_f32_16x16x32_bf16 v[168:171], a[168:171], v[104:107], v[168:171]\n" \
"v_exp_f32_e32 v124, v124\n" \
"v_mfma_f32_16x16x32_bf16 v[140:143], a[172:175], v[100:103], v[140:143]\n" \
"v_exp_f32_e32 v125, v125\n" \
"v_mfma_f32_16x16x32_bf16 v[172:175], a[172:175], v[104:107], v[172:175]\n" \
"v_exp_f32_e32 v126, v126\n" \
"v_mfma_f32_16x16x32_bf16 v[144:147], a[176:179], v[100:103], v[144:147]\n" \
"v_exp_f32_e32 v127, v127\n" \
"v_mfma_f32_16x16x32_bf16 v[176:179], a[176:179], v[104:107], v[176:179]\n" \
"v_cvt_pk_bf16_f32 v121, v122, v123\n" \
"v_cvt_pk_bf16_f32 v122, v124, v125\n" \
"v_mfma_f32_16x16x32_bf16 v[148:151], a[180:183], v[100:103], v[148:151]\n" \
"buffer_load_dwordx4 v5, s[80:83], s92 offen offset:1024 lds\n" \
"v_mfma_f32_16x16x32_bf16 v[180:183], a[180:183], v[104:107], v[180:183]\n" \
"v_cvt_pk_bf16_f32 v123, v126, v127\n" \
"ds_read_b128 a[144:147], v48 offset:12288\n" \
"v_mfma_f32_16x16x32_bf16 v[152:155], a[184:187], v[100:103], v[152:155]\n" \
"ds_read_b128 a[132:135], v48 offset:9216\n" \
"ds_read_b128 a[148:151], v48 offset:13312\n" \
"v_mfma_f32_16x16x32_bf16 v[184:187], a[184:187], v[104:107], v[184:187]\n" \
"ds_read_b128 a[136:139], v48 offset:10240\n" \
"ds_read_b128 a[152:155], v48 offset:14336\n" \
"v_mfma_f32_16x16x32_bf16 v[156:159], a[188:191], v[100:103], v[156:159]\n" \
"ds_read_b128 a[140:143], v48 offset:11264\n" \
"ds_read_b128 a[156:159], v48 offset:15360\n" \
"v_mfma_f32_16x16x32_bf16 v[188:191], a[188:191], v[104:107], v[188:191]\n" \
"buffer_load_dwordx4 v6, s[80:83], s92 offen offset:2048 lds\n" \
"v_smfmac_f32_16x16x64_bf16 a[192:195], a[200:203], v[100:107], v21\n" \
"v_exp_f32_e32 v70, v70\n" \
"v_mfma_f32_16x16x32_bf16 v[220:223], a[188:191], v[116:119], v[220:223]\n" \
"v_exp_f32_e32 v71, v71\n" \
"v_mfma_f32_16x16x32_bf16 v[252:255], a[188:191], v[120:123], v[252:255]\n" \
"buffer_load_dwordx4 v7, s[80:83], s92 offen offset:3072 lds\n" \
"v_mfma_f32_16x16x32_bf16 v[216:219], a[184:187], v[116:119], v[216:219]\n" \
"s_add_u32 m0, s93, 0x2000\n" \
"v_cvt_pk_bf16_f32 v71, v70, v71\n" \
"v_mfma_f32_16x16x32_bf16 v[248:251], a[184:187], v[120:123], v[248:251]\n" \
"v_exp_f32_e32 v72, v72\n" \
"v_mfma_f32_16x16x32_bf16 v[212:215], a[180:183], v[116:119], v[212:215]\n" \
"v_exp_f32_e32 v73, v73\n" \
"v_mfma_f32_16x16x32_bf16 v[244:247], a[180:183], v[120:123], v[244:247]\n" \
"v_exp_f32_e32 v68, v68\n" \
"v_mfma_f32_16x16x32_bf16 v[208:211], a[176:179], v[116:119], v[208:211]\n" \
"buffer_load_dwordx4 v8, s[80:83], s92 offen offset:0 lds\n" \
"v_mfma_f32_16x16x32_bf16 v[240:243], a[176:179], v[120:123], v[240:243]\n" \
"v_exp_f32_e32 v69, v69\n" \
"v_mfma_f32_16x16x32_bf16 v[204:207], a[172:175], v[116:119], v[204:207]\n" \
"v_exp_f32_e32 v74, v74\n" \
"v_mfma_f32_16x16x32_bf16 v[236:239], a[172:175], v[120:123], v[236:239]\n" \
"v_exp_f32_e32 v75, v75\n" \
"v_mfma_f32_16x16x32_bf16 v[200:203], a[168:171], v[116:119], v[200:203]\n" \
"v_exp_f32_e32 v66, v66\n" \
"v_mfma_f32_16x16x32_bf16 v[232:235], a[168:171], v[120:123], v[232:235]\n" \
"v_exp_f32_e32 v67, v67\n" \
"v_mfma_f32_16x16x32_bf16 v[196:199], a[164:167], v[116:119], v[196:199]\n" \
"v_exp_f32_e32 v64, v64\n" \
"v_mfma_f32_16x16x32_bf16 v[228:231], a[164:167], v[120:123], v[228:231]\n" \
"v_exp_f32_e32 v65, v65\n" \
"v_mfma_f32_16x16x32_bf16 v[192:195], a[160:163], v[116:119], v[192:195]\n" \
"v_cvt_pk_bf16_f32 v72, v72, v73\n" \
"v_cvt_pk_bf16_f32 v70, v68, v69\n" \
"v_mfma_f32_16x16x32_bf16 v[224:227], a[160:163], v[120:123], v[224:227]\n" \
"v_exp_f32_e32 v76, v76\n" \
"v_smfmac_f32_16x16x64_bf16 a[196:199], a[200:203], v[116:123], v21\n" \
"v_exp_f32_e32 v77, v77\n" \
"s_waitcnt vmcnt(13) lgkmcnt(0)\n" \
"s_barrier\n" \
"v_exp_f32_e32 v78, v78\n" \
"v_exp_f32_e32 v79, v79\n" \
"v_cvt_pk_bf16_f32 v73, v74, v75\n" \
"v_cvt_pk_bf16_f32 v69, v66, v67\n" \
"v_cvt_pk_bf16_f32 v68, v64, v65\n" \
"v_cvt_pk_bf16_f32 v74, v76, v77\n" \
"v_cvt_pk_bf16_f32 v75, v78, v79\n" \
"v_add_u32_e32 v48, s16, v20\n" \
"s_xor_b32 s65, s65, 0x8000\n" \
"s_add_u32 s64, s64, 0x10000\n" \
"s_getpc_b64 s[86:87]\n" \
"L_hpt_%=:\n" \
"s_add_u32 s86, s86, L_ap8-L_hpt_%=\n" \
"s_addc_u32 s87, s87, 0\n" \
"s_add_u32 s86, s86, s15\n" \
"s_addc_u32 s87, s87, 0\n" \
"s_mov_b64 exec, 0\n" \
"s_swappc_b64 s[88:89], s[86:87]\n" \
"s_mov_b64 exec, -1\n" \
"s_sub_u32 s66, s19, 1\n" \
"s_cbranch_scc1 L_la_%=\n" \
".fill (L_ab + 6464 - .) / 4, 4, 0xBF800000\n" \
"L_2_%=:\n" \
"v_mfma_f32_16x16x32_bf16 v[96:99], a[128:131], a[64:67], v[32:35]\n" \
"s_xor_b32 m0, s65, 0x12000\n" \
"ds_read_b128 a[160:163], v49 offset:0\n" \
"v_mfma_f32_16x16x32_bf16 v[96:99], a[132:135], a[68:71], v[96:99]\n" \
"buffer_load_dwordx4 v50, s[8:11], s64 offen offset:0 sc1 lds\n" \
"v_mfma_f32_16x16x32_bf16 v[96:99], a[136:139], a[72:75], v[96:99]\n" \
"ds_read_b128 a[164:167], v49 offset:1024\n" \
"ds_read_b128 a[168:171], v49 offset:2048\n" \
"v_mfma_f32_16x16x32_bf16 v[96:99], a[140:143], a[76:79], v[96:99]\n" \
"ds_read_b128 a[172:175], v49 offset:3072\n" \
"ds_read_b128 a[176:179], v49 offset:4096\n" \
"v_mfma_f32_16x16x32_bf16 v[100:103], a[156:159], a[76:79], v[32:35]\n" \
"ds_read_b128 a[180:183], v49 offset:5120\n" \
"ds_read_b128 a[184:187], v49 offset:6144\n" \
"v_mfma_f32_16x16x32_bf16 v[100:103], a[152:155], a[72:75], v[100:103]\n" \
"v_exp_f32_e64 v86, v86\n" \
"v_mfma_f32_16x16x32_bf16 v[100:103], a[148:151], a[68:71], v[100:103]\n" \
"v_exp_f32_e64 v87, v87\n" \
"v_mfma_f32_16x16x32_bf16 v[100:103], a[144:147], a[64:67], v[100:103]\n" \
"buffer_load_dwordx4 v50, s[8:11], s64 offen offset:1024 sc1 lds\n" \
"v_mfma_f32_16x16x32_bf16 v[108:111], a[144:147], a[80:83], v[36:39]\n" \
"ds_read_b128 a[188:191], v49 offset:7168\n" \
"v_cvt_pk_bf16_f32 v87, v86, v87\n" \
"v_mfma_f32_16x16x32_bf16 v[108:111], a[148:151], a[84:87], v[108:111]\n" \
"v_exp_f32_e64 v84, v84\n" \
"v_mfma_f32_16x16x32_bf16 v[108:111], a[152:155], a[88:91], v[108:111]\n" \
"v_exp_f32_e64 v85, v85\n" \
"v_mfma_f32_16x16x32_bf16 v[108:111], a[156:159], a[92:95], v[108:111]\n" \
"v_exp_f32_e64 v82, v82\n" \
"v_mfma_f32_16x16x32_bf16 v[104:107], a[140:143], a[92:95], v[36:39]\n" \
"v_exp_f32_e64 v83, v83\n" \
"v_mfma_f32_16x16x32_bf16 v[104:107], a[136:139], a[88:91], v[104:107]\n" \
"v_exp_f32_e64 v80, v80\n" \
"v_mfma_f32_16x16x32_bf16 v[104:107], a[132:135], a[84:87], v[104:107]\n" \
"v_exp_f32_e64 v81, v81\n" \
"v_mfma_f32_16x16x32_bf16 v[104:107], a[128:131], a[80:83], v[104:107]\n" \
"buffer_load_dwordx4 v50, s[8:11], s64 offen offset:2048 sc1 lds\n" \
"v_mfma_f32_16x16x32_bf16 v[112:115], a[128:131], a[96:99], v[40:43]\n" \
"v_exp_f32_e64 v88, v88\n" \
"v_mfma_f32_16x16x32_bf16 v[112:115], a[132:135], a[100:103], v[112:115]\n" \
"v_exp_f32_e64 v89, v89\n" \
"v_mfma_f32_16x16x32_bf16 v[112:115], a[136:139], a[104:107], v[112:115]\n" \
"v_cvt_pk_bf16_f32 v86, v84, v85\n" \
"v_cvt_pk_bf16_f32 v85, v82, v83\n" \
"v_mfma_f32_16x16x32_bf16 v[112:115], a[140:143], a[108:111], v[112:115]\n" \
"v_cvt_pk_bf16_f32 v84, v80, v81\n" \
"v_cvt_pk_bf16_f32 v88, v88, v89\n" \
"v_mfma_f32_16x16x32_bf16 v[116:119], a[156:159], a[108:111], v[40:43]\n" \
"v_exp_f32_e64 v90, v90\n" \
"v_mfma_f32_16x16x32_bf16 v[116:119], a[152:155], a[104:107], v[116:119]\n" \
"v_exp_f32_e64 v91, v91\n" \
"v_mfma_f32_16x16x32_bf16 v[116:119], a[148:151], a[100:103], v[116:119]\n" \
"buffer_load_dwordx4 v50, s[8:11], s64 offen offset:3072 sc1 lds\n" \
"v_mfma_f32_16x16x32_bf16 v[116:119], a[144:147], a[96:99], v[116:119]\n" \
"s_xor_b32 m0, s65, 0xa000\n" \
"v_cvt_pk_bf16_f32 v89, v90, v91\n" \
"v_mfma_f32_16x16x32_bf16 v[124:127], a[144:147], a[112:115], v[44:47]\n" \
"v_exp_f32_e64 v92, v92\n" \
"v_mfma_f32_16x16x32_bf16 v[124:127], a[148:151], a[116:119], v[124:127]\n" \
"v_exp_f32_e64 v93, v93\n" \
"v_mfma_f32_16x16x32_bf16 v[124:127], a[152:155], a[120:123], v[124:127]\n" \
"v_exp_f32_e64 v94, v94\n" \
"v_mfma_f32_16x16x32_bf16 v[124:127], a[156:159], a[124:127], v[124:127]\n" \
"v_exp_f32_e64 v95, v95\n" \
"v_mfma_f32_16x16x32_bf16 v[120:123], a[140:143], a[124:127], v[44:47]\n" \
"v_cvt_pk_bf16_f32 v90, v92, v93\n" \
"v_cvt_pk_bf16_f32 v91, v94, v95\n" \
"v_mfma_f32_16x16x32_bf16 v[120:123], a[136:139], a[120:123], v[120:123]\n" \
"ds_read_b128 a[144:147], v48 offset:4096\n" \
"ds_read_b128 a[148:151], v48 offset:5120\n" \
"v_mfma_f32_16x16x32_bf16 v[120:123], a[132:135], a[116:119], v[120:123]\n" \
"buffer_load_dwordx4 v51, s[8:11], s64 offen offset:0 sc1 lds\n" \
"v_mfma_f32_16x16x32_bf16 v[120:123], a[128:131], a[112:115], v[120:123]\n" \
"ds_read_b128 a[128:131], v48 offset:0\n" \
"s_waitcnt lgkmcnt(3)\n" \
"v_mfma_f32_16x16x32_bf16 v[128:131], a[160:163], v[68:71], v[128:131]\n" \
"ds_read_b128 a[132:135], v48 offset:1024\n" \
"ds_read_b128 a[136:139], v48 offset:2048\n" \
"v_mfma_f32_16x16x32_bf16 v[160:163], a[160:163], v[72:75], v[160:163]\n" \
"ds_read_b128 a[152:155], v48 offset:6144\n" \
"ds_read_b128 a[140:143], v48 offset:3072\n" \
"v_mfma_f32_16x16x32_bf16 v[132:135], a[164:167], v[68:71], v[132:135]\n" \
"v_exp_f32_e32 v102, v102\n" \
"v_mfma_f32_16x16x32_bf16 v[164:167], a[164:167], v[72:75], v[164:167]\n" \
"v_exp_f32_e64 v103, v103\n" \
"v_mfma_f32_16x16x32_bf16 v[136:139], a[168:171], v[68:71], v[136:139]\n" \
"ds_read_b128 a[156:159], v48 offset:7168\n" \
"v_cvt_pk_bf16_f32 v103, v102, v103\n" \
"v_mfma_f32_16x16x32_bf16 v[168:171], a[168:171], v[72:75], v[168:171]\n" \
"buffer_load_dwordx4 v51, s[8:11], s64 offen offset:1024 sc1 lds\n" \
"v_mfma_f32_16x16x32_bf16 v[140:143], a[172:175], v[68:71], v[140:143]\n" \
"v_exp_f32_e64 v100, v100\n" \
"v_mfma_f32_16x16x32_bf16 v[172:175], a[172:175], v[72:75], v[172:175]\n" \
"v_exp_f32_e64 v101, v101\n" \
"v_mfma_f32_16x16x32_bf16 v[144:147], a[176:179], v[68:71], v[144:147]\n" \
"v_exp_f32_e64 v98, v98\n" \
"v_mfma_f32_16x16x32_bf16 v[176:179], a[176:179], v[72:75], v[176:179]\n" \
"v_exp_f32_e64 v99, v99\n" \
"v_mfma_f32_16x16x32_bf16 v[148:151], a[180:183], v[68:71], v[148:151]\n" \
"v_exp_f32_e64 v96, v96\n" \
"v_mfma_f32_16x16x32_bf16 v[180:183], a[180:183], v[72:75], v[180:183]\n" \
"v_exp_f32_e64 v97, v97\n" \
"v_mfma_f32_16x16x32_bf16 v[152:155], a[184:187], v[68:71], v[152:155]\n" \
"v_exp_f32_e64 v104, v104\n" \
"v_mfma_f32_16x16x32_bf16 v[184:187], a[184:187], v[72:75], v[184:187]\n" \
"buffer_load_dwordx4 v51, s[8:11], s64 offen offset:2048 sc1 lds\n" \
"v_mfma_f32_16x16x32_bf16 v[156:159], a[188:191], v[68:71], v[156:159]\n" \
"v_exp_f32_e64 v105, v105\n" \
"v_mfma_f32_16x16x32_bf16 v[188:191], a[188:191], v[72:75], v[188:191]\n" \
"v_cvt_pk_bf16_f32 v102, v100, v101\n" \
"v_cvt_pk_bf16_f32 v101, v98, v99\n" \
"v_smfmac_f32_16x16x64_bf16 a[192:195], a[200:203], v[68:75], v21\n" \
"v_cvt_pk_bf16_f32 v100, v96, v97\n" \
"v_cvt_pk_bf16_f32 v104, v104, v105\n" \
"v_mfma_f32_16x16x32_bf16 v[220:223], a[188:191], v[84:87], v[220:223]\n" \
"v_exp_f32_e64 v106, v106\n" \
"v_mfma_f32_16x16x32_bf16 v[252:255], a[188:191], v[88:91], v[252:255]\n" \
"v_exp_f32_e64 v107, v107\n" \
"v_mfma_f32_16x16x32_bf16 v[216:219], a[184:187], v[84:87], v[216:219]\n" \
"v_exp_f32_e64 v108, v108\n" \
"v_mfma_f32_16x16x32_bf16 v[248:251], a[184:187], v[88:91], v[248:251]\n" \
"buffer_load_dwordx4 v51, s[8:11], s64 offen offset:3072 sc1 lds\n" \
"v_mfma_f32_16x16x32_bf16 v[212:215], a[180:183], v[84:87], v[212:215]\n" \
"v_exp_f32_e64 v109, v109\n" \
"v_mfma_f32_16x16x32_bf16 v[244:247], a[180:183], v[88:91], v[244:247]\n" \
"v_exp_f32_e64 v110, v110\n" \
"v_mfma_f32_16x16x32_bf16 v[208:211], a[176:179], v[84:87], v[208:211]\n" \
"v_exp_f32_e64 v111, v111\n" \
"v_mfma_f32_16x16x32_bf16 v[240:243], a[176:179], v[88:91], v[240:243]\n" \
"v_exp_f32_e64 v118, v118\n" \
"v_mfma_f32_16x16x32_bf16 v[204:207], a[172:175], v[84:87], v[204:207]\n" \
"v_exp_f32_e64 v119, v119\n" \
"v_mfma_f32_16x16x32_bf16 v[236:239], a[172:175], v[88:91], v[236:239]\n" \
"v_cvt_pk_bf16_f32 v105, v106, v107\n" \
"v_cvt_pk_bf16_f32 v106, v108, v109\n" \
"v_mfma_f32_16x16x32_bf16 v[200:203], a[168:171], v[84:87], v[200:203]\n" \
"v_cvt_pk_bf16_f32 v107, v110, v111\n" \
"v_cvt_pk_bf16_f32 v119, v118, v119\n" \
"v_mfma_f32_16x16x32_bf16 v[232:235], a[168:171], v[88:91], v[232:235]\n" \
"v_exp_f32_e64 v116, v116\n" \
"v_mfma_f32_16x16x32_bf16 v[196:199], a[164:167], v[84:87], v[196:199]\n" \
"v_exp_f32_e64 v117, v117\n" \
"v_mfma_f32_16x16x32_bf16 v[228:231], a[164:167], v[88:91], v[228:231]\n" \
"v_exp_f32_e64 v114, v114\n" \
"v_mfma_f32_16x16x32_bf16 v[192:195], a[160:163], v[84:87], v[192:195]\n" \
"v_exp_f32_e64 v115, v115\n" \
"v_mfma_f32_16x16x32_bf16 v[224:227], a[160:163], v[88:91], v[224:227]\n" \
"v_exp_f32_e64 v112, v112\n" \
"v_smfmac_f32_16x16x64_bf16 a[196:199], a[200:203], v[84:91], v21\n" \
"v_exp_f32_e32 v113, v113\n" \
"s_waitcnt vmcnt(8) lgkmcnt(0)\n" \
"s_barrier\n" \
"s_nop 0\n" \
"v_mfma_f32_16x16x32_bf16 v[64:67], a[128:131], a[64:67], v[32:35]\n" \
"s_xor_b32 m0, s65, 0x18000\n" \
"ds_read_b128 a[160:163], v49 offset:8192\n" \
"v_mfma_f32_16x16x32_bf16 v[64:67], a[132:135], a[68:71], v[64:67]\n" \
"buffer_load_dwordx4 v52, s[8:11], s64 offen offset:0 sc1 lds\n" \
"v_mfma_f32_16x16x32_bf16 v[64:67], a[136:139], a[72:75], v[64:67]\n" \
"ds_read_b128 a[164:167], v49 offset:9216\n" \
"ds_read_b128 a[168:171], v49 offset:10240\n" \
"v_mfma_f32_16x16x32_bf16 v[64:67], a[140:143], a[76:79], v[64:67]\n" \
"ds_read_b128 a[172:175], v49 offset:11264\n" \
"ds_read_b128 a[176:179], v49 offset:12288\n" \
"v_mfma_f32_16x16x32_bf16 v[68:71], a[156:159], a[76:79], v[32:35]\n" \
"ds_read_b128 a[180:183], v49 offset:13312\n" \
"ds_read_b128 a[184:187], v49 offset:14336\n" \
"v_mfma_f32_16x16x32_bf16 v[68:71], a[152:155], a[72:75], v[68:71]\n" \
"ds_read_b128 a[188:191], v49 offset:15360\n" \
"v_cvt_pk_bf16_f32 v118, v116, v117\n" \
"v_mfma_f32_16x16x32_bf16 v[68:71], a[148:151], a[68:71], v[68:71]\n" \
"v_cvt_pk_bf16_f32 v117, v114, v115\n" \
"v_cvt_pk_bf16_f32 v116, v112, v113\n" \
"v_mfma_f32_16x16x32_bf16 v[68:71], a[144:147], a[64:67], v[68:71]\n" \
"buffer_load_dwordx4 v52, s[8:11], s64 offen offset:1024 sc1 lds\n" \
"v_mfma_f32_16x16x32_bf16 v[76:79], a[144:147], a[80:83], v[36:39]\n" \
"v_exp_f32_e64 v120, v120\n" \
"v_mfma_f32_16x16x32_bf16 v[76:79], a[148:151], a[84:87], v[76:79]\n" \
"v_exp_f32_e64 v121, v121\n" \
"v_mfma_f32_16x16x32_bf16 v[76:79], a[152:155], a[88:91], v[76:79]\n" \
"v_exp_f32_e64 v122, v122\n" \
"v_mfma_f32_16x16x32_bf16 v[76:79], a[156:159], a[92:95], v[76:79]\n" \
"v_exp_f32_e64 v123, v123\n" \
"v_mfma_f32_16x16x32_bf16 v[72:75], a[140:143], a[92:95], v[36:39]\n" \
"v_cvt_pk_bf16_f32 v120, v120, v121\n" \
"v_cvt_pk_bf16_f32 v121, v122, v123\n" \
"v_mfma_f32_16x16x32_bf16 v[72:75], a[136:139], a[88:91], v[72:75]\n" \
"v_exp_f32_e64 v124, v124\n" \
"v_mfma_f32_16x16x32_bf16 v[72:75], a[132:135], a[84:87], v[72:75]\n" \
"v_exp_f32_e64 v125, v125\n" \
"v_mfma_f32_16x16x32_bf16 v[72:75], a[128:131], a[80:83], v[72:75]\n" \
"buffer_load_dwordx4 v52, s[8:11], s64 offen offset:2048 sc1 lds\n" \
"v_mfma_f32_16x16x32_bf16 v[80:83], a[128:131], a[96:99], v[40:43]\n" \
"v_exp_f32_e64 v126, v126\n" \
"v_mfma_f32_16x16x32_bf16 v[80:83], a[132:135], a[100:103], v[80:83]\n" \
"v_exp_f32_e64 v127, v127\n" \
"v_mfma_f32_16x16x32_bf16 v[80:83], a[136:139], a[104:107], v[80:83]\n" \
"v_cvt_pk_bf16_f32 v122, v124, v125\n" \
"v_cvt_pk_bf16_f32 v123, v126, v127\n" \
"v_mfma_f32_16x16x32_bf16 v[80:83], a[140:143], a[108:111], v[80:83]\n" \
"v_exp_f32_e64 v70, v70\n" \
"v_mfma_f32_16x16x32_bf16 v[84:87], a[156:159], a[108:111], v[40:43]\n" \
"v_exp_f32_e64 v71, v71\n" \
"v_mfma_f32_16x16x32_bf16 v[84:87], a[152:155], a[104:107], v[84:87]\n" \
"v_exp_f32_e64 v72, v72\n" \
"v_mfma_f32_16x16x32_bf16 v[84:87], a[148:151], a[100:103], v[84:87]\n" \
"buffer_load_dwordx4 v52, s[8:11], s64 offen offset:3072 sc1 lds\n" \
"v_mfma_f32_16x16x32_bf16 v[84:87], a[144:147], a[96:99], v[84:87]\n" \
"s_mov_b32 m0, s65\n" \
"v_cvt_pk_bf16_f32 v71, v70, v71\n" \
"v_mfma_f32_16x16x32_bf16 v[92:95], a[144:147], a[112:115], v[44:47]\n" \
"ds_read_b128 a[144:147], v48 offset:12288\n" \
"v_add_u32_e32 v49, 0x10000, v48\n" \
"v_mfma_f32_16x16x32_bf16 v[92:95], a[148:151], a[116:119], v[92:95]\n" \
"ds_read_b128 a[148:151], v48 offset:13312\n" \
"s_xor_b32 s65, s65, 0x8000\n" \
"v_mfma_f32_16x16x32_bf16 v[92:95], a[152:155], a[120:123], v[92:95]\n" \
"v_exp_f32_e32 v73, v73\n" \
"v_mfma_f32_16x16x32_bf16 v[92:95], a[156:159], a[124:127], v[92:95]\n" \
"ds_read_b128 a[152:155], v48 offset:14336\n" \
".fill (L_ab + 8192 - .) / 4, 4, 0xBF800000\n" \
"L_ap2:\n" \
"s_cbranch_execz L_ap0\n" \
"ds_read_b128 a[156:159], v48 offset:15360\n" \
"v_mfma_f32_16x16x32_bf16 v[88:91], a[140:143], a[124:127], v[44:47]\n" \
"ds_read_b128 a[140:143], v48 offset:11264\n" \
"v_cvt_pk_bf16_f32 v72, v72, v73\n" \
"v_mfma_f32_16x16x32_bf16 v[88:91], a[136:139], a[120:123], v[88:91]\n" \
"v_exp_f32_e32 v68, v68\n" \
"v_mfma_f32_16x16x32_bf16 v[88:91], a[132:135], a[116:119], v[88:91]\n" \
"buffer_load_dwordx4 v53, s[8:11], s64 offen offset:0 sc1 lds\n" \
"v_mfma_f32_16x16x32_bf16 v[88:91], a[128:131], a[112:115], v[88:91]\n" \
"ds_read_b128 a[128:131], v48 offset:8192\n" \
"s_waitcnt lgkmcnt(6)\n" \
"v_mfma_f32_16x16x32_bf16 v[128:131], a[160:163], v[100:103], v[128:131]\n" \
"ds_read_b128 a[132:135], v48 offset:9216\n" \
"ds_read_b128 a[136:139], v48 offset:10240\n" \
"v_mfma_f32_16x16x32_bf16 v[160:163], a[160:163], v[104:107], v[160:163]\n" \
"v_exp_f32_e32 v69, v69\n" \
"v_mfma_f32_16x16x32_bf16 v[132:135], a[164:167], v[100:103], v[132:135]\n" \
"v_exp_f32_e64 v74, v74\n" \
"v_mfma_f32_16x16x32_bf16 v[164:167], a[164:167], v[104:107], v[164:167]\n" \
"v_exp_f32_e64 v75, v75\n" \
"v_mfma_f32_16x16x32_bf16 v[136:139], a[168:171], v[100:103], v[136:139]\n" \
"v_exp_f32_e64 v66, v66\n" \
"v_mfma_f32_16x16x32_bf16 v[168:171], a[168:171], v[104:107], v[168:171]\n" \
"buffer_load_dwordx4 v53, s[8:11], s64 offen offset:1024 sc1 lds\n" \
"v_mfma_f32_16x16x32_bf16 v[140:143], a[172:175], v[100:103], v[140:143]\n" \
"v_exp_f32_e64 v67, v67\n" \
"v_mfma_f32_16x16x32_bf16 v[172:175], a[172:175], v[104:107], v[172:175]\n" \
"v_exp_f32_e64 v64, v64\n" \
"v_mfma_f32_16x16x32_bf16 v[144:147], a[176:179], v[100:103], v[144:147]\n" \
"v_exp_f32_e64 v65, v65\n" \
"v_mfma_f32_16x16x32_bf16 v[176:179], a[176:179], v[104:107], v[176:179]\n" \
"v_exp_f32_e64 v76, v76\n" \
"v_mfma_f32_16x16x32_bf16 v[148:151], a[180:183], v[100:103], v[148:151]\n" \
"v_exp_f32_e64 v77, v77\n" \
"v_mfma_f32_16x16x32_bf16 v[180:183], a[180:183], v[104:107], v[180:183]\n" \
"v_exp_f32_e64 v78, v78\n" \
"v_mfma_f32_16x16x32_bf16 v[152:155], a[184:187], v[100:103], v[152:155]\n" \
"v_exp_f32_e64 v79, v79\n" \
"v_mfma_f32_16x16x32_bf16 v[184:187], a[184:187], v[104:107], v[184:187]\n" \
"buffer_load_dwordx4 v53, s[8:11], s64 offen offset:2048 sc1 lds\n" \
"v_mfma_f32_16x16x32_bf16 v[156:159], a[188:191], v[100:103], v[156:159]\n" \
"v_cvt_pk_bf16_f32 v70, v68, v69\n" \
"v_cvt_pk_bf16_f32 v73, v74, v75\n" \
"v_mfma_f32_16x16x32_bf16 v[188:191], a[188:191], v[104:107], v[188:191]\n" \
"v_cvt_pk_bf16_f32 v69, v66, v67\n" \
"v_cvt_pk_bf16_f32 v68, v64, v65\n" \
"v_smfmac_f32_16x16x64_bf16 a[192:195], a[200:203], v[100:107], v21\n" \
"v_cvt_pk_bf16_f32 v74, v76, v77\n" \
"v_cvt_pk_bf16_f32 v75, v78, v79\n" \
"v_mfma_f32_16x16x32_bf16 v[220:223], a[188:191], v[116:119], v[220:223]\n" \
"v_xor_b32_e32 v48, 0x8000, v48\n" \
"v_mfma_f32_16x16x32_bf16 v[252:255], a[188:191], v[120:123], v[252:255]\n" \
"v_mfma_f32_16x16x32_bf16 v[216:219], a[184:187], v[116:119], v[216:219]\n" \
"v_mfma_f32_16x16x32_bf16 v[248:251], a[184:187], v[120:123], v[248:251]\n" \
"buffer_load_dwordx4 v53, s[8:11], s64 offen offset:3072 sc1 lds\n" \
"v_mfma_f32_16x16x32_bf16 v[212:215], a[180:183], v[116:119], v[212:215]\n" \
"s_add_u32 s64, s64, 0x10000\n" \
"s_sub_u32 s66, s66, 1\n" \
"s_nop 0\n" \
"v_mfma_f32_16x16x32_bf16 v[244:247], a[180:183], v[120:123], v[244:247]\n" \
"v_mfma_f32_16x16x32_bf16 v[208:211], a[176:179], v[116:119], v[208:211]\n" \
"v_mfma_f32_16x16x32_bf16 v[240:243], a[176:179], v[120:123], v[240:243]\n" \
"v_mfma_f32_16x16x32_bf16 v[204:207], a[172:175], v[116:119], v[204:207]\n" \
"v_mfma_f32_16x16x32_bf16 v[236:239], a[172:175], v[120:123], v[236:239]\n" \
"v_mfma_f32_16x16x32_bf16 v[200:203], a[168:171], v[116:119], v[200:203]\n" \
"v_mfma_f32_16x16x32_bf16 v[232:235], a[168:171], v[120:123], v[232:235]\n" \
"v_mfma_f32_16x16x32_bf16 v[196:199], a[164:167], v[116:119], v[196:199]\n" \
"v_mfma_f32_16x16x32_bf16 v[228:231], a[164:167], v[120:123], v[228:231]\n" \
"v_mfma_f32_16x16x32_bf16 v[192:195], a[160:163], v[116:119], v[192:195]\n" \
"v_mfma_f32_16x16x32_bf16 v[224:227], a[160:163], v[120:123], v[224:227]\n" \
"v_smfmac_f32_16x16x64_bf16 a[196:199], a[200:203], v[116:123], v21\n" \
"s_waitcnt vmcnt(8) lgkmcnt(0)\n" \
"s_barrier\n" \
"s_cbranch_scc0 L_2_%=\n" \
"s_nop 0\n" \
"L_la_%=:\n" \
"v_mfma_f32_16x16x32_bf16 v[96:99], a[128:131], a[64:67], v[32:35]\n" \
"v_readlane_b32 s96, v11, 1\n" \
"v_bfe_u32 v27, v20, 2, 7\n" \
"v_mfma_f32_16x16x32_bf16 v[96:99], a[132:135], a[68:71], v[96:99]\n" \
"s_xor_b32 m0, s65, 0x12000\n" \
"ds_read_b128 a[160:163], v49 offset:0\n" \
"v_mfma_f32_16x16x32_bf16 v[96:99], a[136:139], a[72:75], v[96:99]\n" \
"buffer_load_dword v26, v27, s[56:59], s96 offen sc1\n" \
"v_mfma_f32_16x16x32_bf16 v[96:99], a[140:143], a[76:79], v[96:99]\n" \
"buffer_load_dwordx4 v50, s[8:11], s64 offen offset:0 sc1 lds\n" \
"v_mfma_f32_16x16x32_bf16 v[100:103], a[156:159], a[76:79], v[32:35]\n" \
"ds_read_b128 a[164:167], v49 offset:1024\n" \
"ds_read_b128 a[168:171], v49 offset:2048\n" \
"v_mfma_f32_16x16x32_bf16 v[100:103], a[152:155], a[72:75], v[100:103]\n" \
"ds_read_b128 a[172:175], v49 offset:3072\n" \
"ds_read_b128 a[176:179], v49 offset:4096\n" \
"v_mfma_f32_16x16x32_bf16 v[100:103], a[148:151], a[68:71], v[100:103]\n" \
"ds_read_b128 a[180:183], v49 offset:5120\n" \
"ds_read_b128 a[184:187], v49 offset:6144\n" \
"v_mfma_f32_16x16x32_bf16 v[100:103], a[144:147], a[64:67], v[100:103]\n" \
"ds_read_b128 a[188:191], v49 offset:7168\n" \
"v_readlane_b32 s97, v11, 36\n" \
"v_mfma_f32_16x16x32_bf16 v[108:111], a[144:147], a[80:83], v[36:39]\n" \
"v_exp_f32_e64 v86, v86\n" \
"v_mfma_f32_16x16x32_bf16 v[108:111], a[148:151], a[84:87], v[108:111]\n" \
"v_exp_f32_e64 v87, v87\n" \
"v_mfma_f32_16x16x32_bf16 v[108:111], a[152:155], a[88:91], v[108:111]\n" \
"buffer_load_dwordx4 v50, s[8:11], s64 offen offset:1024 sc1 lds\n" \
"v_mfma_f32_16x16x32_bf16 v[108:111], a[156:159], a[92:95], v[108:111]\n" \
"v_exp_f32_e64 v84, v84\n" \
"v_mfma_f32_16x16x32_bf16 v[104:107], a[140:143], a[92:95], v[36:39]\n" \
"v_exp_f32_e64 v85, v85\n" \
"v_mfma_f32_16x16x32_bf16 v[104:107], a[136:139], a[88:91], v[104:107]\n" \
"v_cvt_pk_bf16_f32 v87, v86, v87\n" \
"v_cvt_pk_bf16_f32 v86, v84, v85\n" \
"v_mfma_f32_16x16x32_bf16 v[104:107], a[132:135], a[84:87], v[104:107]\n" \
"v_exp_f32_e64 v82, v82\n" \
"v_mfma_f32_16x16x32_bf16 v[104:107], a[128:131], a[80:83], v[104:107]\n" \
"v_exp_f32_e64 v83, v83\n" \
"v_mfma_f32_16x16x32_bf16 v[112:115], a[128:131], a[96:99], v[40:43]\n" \
"v_exp_f32_e64 v80, v80\n" \
"v_mfma_f32_16x16x32_bf16 v[112:115], a[132:135], a[100:103], v[112:115]\n" \
"v_exp_f32_e64 v81, v81\n" \
"v_mfma_f32_16x16x32_bf16 v[112:115], a[136:139], a[104:107], v[112:115]\n" \
"buffer_load_dwordx4 v50, s[8:11], s64 offen offset:2048 sc1 lds\n" \
"v_mfma_f32_16x16x32_bf16 v[112:115], a[140:143], a[108:111], v[112:115]\n" \
"v_cvt_pk_bf16_f32 v85, v82, v83\n" \
"v_cvt_pk_bf16_f32 v84, v80, v81\n" \
"v_mfma_f32_16x16x32_bf16 v[116:119], a[156:159], a[108:111], v[40:43]\n" \
"v_exp_f32_e64 v88, v88\n" \
"v_mfma_f32_16x16x32_bf16 v[116:119], a[152:155], a[104:107], v[116:119]\n" \
"v_exp_f32_e64 v89, v89\n" \
"v_mfma_f32_16x16x32_bf16 v[116:119], a[148:151], a[100:103], v[116:119]\n" \
"v_exp_f32_e64 v90, v90\n" \
"v_mfma_f32_16x16x32_bf16 v[116:119], a[144:147], a[96:99], v[116:119]\n" \
"v_exp_f32_e64 v91, v91\n" \
"v_mfma_f32_16x16x32_bf16 v[124:127], a[144:147], a[112:115], v[44:47]\n" \
"v_cvt_pk_bf16_f32 v88, v88, v89\n" \
"v_cvt_pk_bf16_f32 v89, v90, v91\n" \
"v_mfma_f32_16x16x32_bf16 v[124:127], a[148:151], a[116:119], v[124:127]\n" \
"buffer_load_dwordx4 v50, s[8:11], s64 offen offset:3072 sc1 lds\n" \
"v_mfma_f32_16x16x32_bf16 v[124:127], a[152:155], a[120:123], v[124:127]\n" \
"v_exp_f32_e64 v92, v92\n" \
"v_mfma_f32_16x16x32_bf16 v[124:127], a[156:159], a[124:127], v[124:127]\n" \
"v_exp_f32_e64 v93, v93\n" \
"v_mfma_f32_16x16x32_bf16 v[120:123], a[140:143], a[124:127], v[44:47]\n" \
"v_exp_f32_e64 v94, v94\n" \
"v_mfma_f32_16x16x32_bf16 v[120:123], a[136:139], a[120:123], v[120:123]\n" \
"v_exp_f32_e64 v95, v95\n" \
"v_mfma_f32_16x16x32_bf16 v[120:123], a[132:135], a[116:119], v[120:123]\n" \
"v_cvt_pk_bf16_f32 v90, v92, v93\n" \
"v_cvt_pk_bf16_f32 v91, v94, v95\n" \
"v_mfma_f32_16x16x32_bf16 v[120:123], a[128:131], a[112:115], v[120:123]\n" \
"ds_read_b128 a[128:131], v48 offset:0\n" \
"s_waitcnt lgkmcnt(1)\n" \
"s_nop 0\n" \
"v_mfma_f32_16x16x32_bf16 v[128:131], a[160:163], v[68:71], v[128:131]\n" \
"ds_read_b128 a[144:147], v48 offset:4096\n" \
"ds_read_b128 a[132:135], v48 offset:1024\n" \
"v_mfma_f32_16x16x32_bf16 v[160:163], a[160:163], v[72:75], v[160:163]\n" \
"ds_read_b128 a[148:151], v48 offset:5120\n" \
"ds_read_b128 a[136:139], v48 offset:2048\n" \
"v_mfma_f32_16x16x32_bf16 v[132:135], a[164:167], v[68:71], v[132:135]\n" \
"ds_read_b128 a[152:155], v48 offset:6144\n" \
"ds_read_b128 a[140:143], v48 offset:3072\n" \
"v_mfma_f32_16x16x32_bf16 v[164:167], a[164:167], v[72:75], v[164:167]\n" \
"ds_read_b128 a[156:159], v48 offset:7168\n" \
"v_add_u32_e32 v17, 0x49000, v20\n" \
"v_mfma_f32_16x16x32_bf16 v[136:139], a[168:171], v[68:71], v[136:139]\n" \
"v_readlane_b32 s98, v11, 37\n" \
"v_readlane_b32 s93, v11, 20\n" \
"v_mfma_f32_16x16x32_bf16 v[168:171], a[168:171], v[72:75], v[168:171]\n" \
"v_readlane_b32 s84, v11, 15\n" \
"v_readlane_b32 s85, v11, 16\n" \
"v_mfma_f32_16x16x32_bf16 v[140:143], a[172:175], v[68:71], v[140:143]\n" \
"v_readlane_b32 s86, v11, 17\n" \
"s_mov_b32 s87, 0x20000\n" \
"v_mfma_f32_16x16x32_bf16 v[172:175], a[172:175], v[72:75], v[172:175]\n" \
"v_readlane_b32 s92, v11, 18\n" \
"v_readlane_b32 s91, v11, 21\n" \
"v_mfma_f32_16x16x32_bf16 v[144:147], a[176:179], v[68:71], v[144:147]\n" \
"v_exp_f32_e64 v102, v102\n" \
"v_mfma_f32_16x16x32_bf16 v[176:179], a[176:179], v[72:75], v[176:179]\n" \
"v_exp_f32_e64 v103, v103\n" \
"v_mfma_f32_16x16x32_bf16 v[148:151], a[180:183], v[68:71], v[148:151]\n" \
"v_exp_f32_e64 v100, v100\n" \
"v_mfma_f32_16x16x32_bf16 v[180:183], a[180:183], v[72:75], v[180:183]\n" \
"v_exp_f32_e64 v101, v101\n" \
"v_mfma_f32_16x16x32_bf16 v[152:155], a[184:187], v[68:71], v[152:155]\n" \
"v_cvt_pk_bf16_f32 v103, v102, v103\n" \
"v_cvt_pk_bf16_f32 v102, v100, v101\n" \
"v_mfma_f32_16x16x32_bf16 v[184:187], a[184:187], v[72:75], v[184:187]\n" \
"v_exp_f32_e64 v98, v98\n" \
"v_mfma_f32_16x16x32_bf16 v[156:159], a[188:191], v[68:71], v[156:159]\n" \
"v_exp_f32_e64 v99, v99\n" \
"v_mfma_f32_16x16x32_bf16 v[188:191], a[188:191], v[72:75], v[188:191]\n" \
"s_waitcnt vmcnt(4)\n" \
"v_cmp_ne_u32_e32 vcc, s62, v26\n" \
"v_smfmac_f32_16x16x64_bf16 a[192:195], a[200:203], v[68:75], v21\n" \
"v_exp_f32_e64 v96, v96\n" \
"v_mfma_f32_16x16x32_bf16 v[220:223], a[188:191], v[84:87], v[220:223]\n" \
"v_exp_f32_e64 v97, v97\n" \
"v_mfma_f32_16x16x32_bf16 v[252:255], a[188:191], v[88:91], v[252:255]\n" \
"s_cbranch_vccnz L_sol_%=\n" \
"L_rol_%=:\n" \
"v_cvt_pk_bf16_f32 v101, v98, v99\n" \
"buffer_load_dwordx4 a[220:223], v20, s[56:59], s97 offen offset:0 sc1\n" \
"v_mfma_f32_16x16x32_bf16 v[216:219], a[184:187], v[84:87], v[216:219]\n" \
"v_cvt_pk_bf16_f32 v100, v96, v97\n" \
"s_xor_b32 s65, s65, 0x8000\n" \
"v_mfma_f32_16x16x32_bf16 v[248:251], a[184:187], v[88:91], v[248:251]\n" \
"v_exp_f32_e32 v104, v104\n" \
"v_mfma_f32_16x16x32_bf16 v[212:215], a[180:183], v[84:87], v[212:215]\n" \
"v_exp_f32_e64 v105, v105\n" \
"v_mfma_f32_16x16x32_bf16 v[244:247], a[180:183], v[88:91], v[244:247]\n" \
"v_exp_f32_e64 v106, v106\n" \
"v_mfma_f32_16x16x32_bf16 v[208:211], a[176:179], v[84:87], v[208:211]\n" \
"v_exp_f32_e64 v107, v107\n" \
"v_mfma_f32_16x16x32_bf16 v[240:243], a[176:179], v[88:91], v[240:243]\n" \
"buffer_load_dwordx4 a[228:231], v17, s[56:59], s97 offen offset:0 sc1\n" \
"v_mfma_f32_16x16x32_bf16 v[204:207], a[172:175], v[84:87], v[204:207]\n" \
"v_cvt_pk_bf16_f32 v104, v104, v105\n" \
"v_cvt_pk_bf16_f32 v105, v106, v107\n" \
"v_mfma_f32_16x16x32_bf16 v[236:239], a[172:175], v[88:91], v[236:239]\n" \
"v_exp_f32_e64 v108, v108\n" \
"v_mfma_f32_16x16x32_bf16 v[200:203], a[168:171], v[84:87], v[200:203]\n" \
"v_exp_f32_e64 v109, v109\n" \
"v_mfma_f32_16x16x32_bf16 v[232:235], a[168:171], v[88:91], v[232:235]\n" \
"v_exp_f32_e64 v110, v110\n" \
"v_mfma_f32_16x16x32_bf16 v[196:199], a[164:167], v[84:87], v[196:199]\n" \
"v_exp_f32_e64 v111, v111\n" \
"v_mfma_f32_16x16x32_bf16 v[228:231], a[164:167], v[88:91], v[228:231]\n" \
"v_cvt_pk_bf16_f32 v106, v108, v109\n" \
"v_cvt_pk_bf16_f32 v107, v110, v111\n" \
"v_mfma_f32_16x16x32_bf16 v[192:195], a[160:163], v[84:87], v[192:195]\n" \
"v_exp_f32_e64 v118, v118\n" \
"v_mfma_f32_16x16x32_bf16 v[224:227], a[160:163], v[88:91], v[224:227]\n" \
"v_exp_f32_e64 v119, v119\n" \
"v_smfmac_f32_16x16x64_bf16 a[196:199], a[200:203], v[84:91], v21\n" \
"buffer_load_dwordx4 a[236:239], v20, s[56:59], s98 offen offset:0 sc1\n" \
"s_waitcnt vmcnt(8) lgkmcnt(0)\n" \
"s_barrier\n" \
"v_mfma_f32_16x16x32_bf16 v[64:67], a[128:131], a[64:67], v[32:35]\n" \
"s_add_u32 m0, s93, 0x0\n" \
"s_nop 0\n" \
"ds_read_b128 a[160:163], v49 offset:8192\n" \
"v_mfma_f32_16x16x32_bf16 v[64:67], a[132:135], a[68:71], v[64:67]\n" \
"buffer_load_dwordx4 v0, s[84:87], s92 offen offset:0 lds\n" \
"v_mfma_f32_16x16x32_bf16 v[64:67], a[136:139], a[72:75], v[64:67]\n" \
"ds_read_b128 a[164:167], v49 offset:9216\n" \
"ds_read_b128 a[168:171], v49 offset:10240\n" \
"v_mfma_f32_16x16x32_bf16 v[64:67], a[140:143], a[76:79], v[64:67]\n" \
"ds_read_b128 a[172:175], v49 offset:11264\n" \
"ds_read_b128 a[176:179], v49 offset:12288\n" \
"v_mfma_f32_16x16x32_bf16 v[68:71], a[156:159], a[76:79], v[32:35]\n" \
"ds_read_b128 a[180:183], v49 offset:13312\n" \
"ds_read_b128 a[184:187], v49 offset:14336\n" \
"v_mfma_f32_16x16x32_bf16 v[68:71], a[152:155], a[72:75], v[68:71]\n" \
"ds_read_b128 a[188:191], v49 offset:15360\n" \
"v_cvt_pk_bf16_f32 v119, v118, v119\n" \
"v_mfma_f32_16x16x32_bf16 v[68:71], a[148:151], a[68:71], v[68:71]\n" \
"buffer_load_dwordx4 v1, s[84:87], s92 offen offset:1024 lds\n" \
"v_mfma_f32_16x16x32_bf16 v[68:71], a[144:147], a[64:67], v[68:71]\n" \
"buffer_load_dwordx4 a[244:247], v17, s[56:59], s98 offen offset:0 sc1\n" \
"v_mfma_f32_16x16x32_bf16 v[76:79], a[144:147], a[80:83], v[36:39]\n" \
"v_exp_f32_e64 v116, v116\n" \
"v_mfma_f32_16x16x32_bf16 v[76:79], a[148:151], a[84:87], v[76:79]\n" \
"v_exp_f32_e64 v117, v117\n" \
"v_mfma_f32_16x16x32_bf16 v[76:79], a[152:155], a[88:91], v[76:79]\n" \
"v_exp_f32_e64 v114, v114\n" \
"v_mfma_f32_16x16x32_bf16 v[76:79], a[156:159], a[92:95], v[76:79]\n" \
"v_exp_f32_e64 v115, v115\n" \
"v_mfma_f32_16x16x32_bf16 v[72:75], a[140:143], a[92:95], v[36:39]\n" \
"v_exp_f32_e64 v112, v112\n" \
"v_mfma_f32_16x16x32_bf16 v[72:75], a[136:139], a[88:91], v[72:75]\n" \
"buffer_load_dwordx4 v2, s[84:87], s92 offen offset:2048 lds\n" \
"v_mfma_f32_16x16x32_bf16 v[72:75], a[132:135], a[84:87], v[72:75]\n" \
"v_exp_f32_e64 v113, v113\n" \
"v_mfma_f32_16x16x32_bf16 v[72:75], a[128:131], a[80:83], v[72:75]\n" \
"v_cvt_pk_bf16_f32 v118, v116, v117\n" \
"v_cvt_pk_bf16_f32 v117, v114, v115\n" \
"v_mfma_f32_16x16x32_bf16 v[80:83], a[128:131], a[96:99], v[40:43]\n" \
"buffer_load_dwordx4 a[224:227], v20, s[56:59], s97 offen offset:1024 sc1\n" \
"v_mfma_f32_16x16x32_bf16 v[80:83], a[132:135], a[100:103], v[80:83]\n" \
"v_cvt_pk_bf16_f32 v116, v112, v113\n" \
"v_add_u32_e32 v49, 0x10000, v48\n" \
"v_mfma_f32_16x16x32_bf16 v[80:83], a[136:139], a[104:107], v[80:83]\n" \
"v_exp_f32_e64 v120, v120\n" \
"v_mfma_f32_16x16x32_bf16 v[80:83], a[140:143], a[108:111], v[80:83]\n" \
"v_exp_f32_e64 v121, v121\n" \
"v_mfma_f32_16x16x32_bf16 v[84:87], a[156:159], a[108:111], v[40:43]\n" \
"buffer_load_dwordx4 v3, s[84:87], s92 offen offset:3072 lds\n" \
"v_mfma_f32_16x16x32_bf16 v[84:87], a[152:155], a[104:107], v[84:87]\n" \
"s_add_u32 m0, s91, 0x1000\n" \
"v_cvt_pk_bf16_f32 v120, v120, v121\n" \
"v_mfma_f32_16x16x32_bf16 v[84:87], a[148:151], a[100:103], v[84:87]\n" \
"v_exp_f32_e64 v122, v122\n" \
"v_mfma_f32_16x16x32_bf16 v[84:87], a[144:147], a[96:99], v[84:87]\n" \
"v_exp_f32_e64 v123, v123\n" \
"v_mfma_f32_16x16x32_bf16 v[92:95], a[144:147], a[112:115], v[44:47]\n" \
"v_exp_f32_e64 v124, v124\n" \
"v_mfma_f32_16x16x32_bf16 v[92:95], a[148:151], a[116:119], v[92:95]\n" \
"buffer_load_dwordx4 a[232:235], v17, s[56:59], s97 offen offset:1024 sc1\n" \
"v_mfma_f32_16x16x32_bf16 v[92:95], a[152:155], a[120:123], v[92:95]\n" \
"buffer_load_dwordx4 v4, s[84:87], s92 offen offset:0 lds\n" \
"v_mfma_f32_16x16x32_bf16 v[92:95], a[156:159], a[124:127], v[92:95]\n" \
"v_exp_f32_e64 v125, v125\n" \
"v_mfma_f32_16x16x32_bf16 v[88:91], a[140:143], a[124:127], v[44:47]\n" \
"v_exp_f32_e64 v126, v126\n" \
"v_mfma_f32_16x16x32_bf16 v[88:91], a[136:139], a[120:123], v[88:91]\n" \
"v_exp_f32_e64 v127, v127\n" \
"v_mfma_f32_16x16x32_bf16 v[88:91], a[132:135], a[116:119], v[88:91]\n" \
"v_cvt_pk_bf16_f32 v121, v122, v123\n" \
"v_cvt_pk_bf16_f32 v122, v124, v125\n" \
"v_mfma_f32_16x16x32_bf16 v[88:91], a[128:131], a[112:115], v[88:91]\n" \
"v_cvt_pk_bf16_f32 v123, v126, v127\n" \
"s_waitcnt lgkmcnt(0)\n" \
"s_nop 0\n" \
"v_mfma_f32_16x16x32_bf16 v[128:131], a[160:163], v[100:103], v[128:131]\n" \
"ds_read_b128 a[128:131], v48 offset:8192\n" \
"ds_read_b128 a[144:147], v48 offset:12288\n" \
"v_mfma_f32_16x16x32_bf16 v[160:163], a[160:163], v[104:107], v[160:163]\n" \
"buffer_load_dwordx4 v5, s[84:87], s92 offen offset:1024 lds\n" \
"v_mfma_f32_16x16x32_bf16 v[132:135], a[164:167], v[100:103], v[132:135]\n" \
"buffer_load_dwordx4 a[240:243], v20, s[56:59], s98 offen offset:1024 sc1\n" \
"v_mfma_f32_16x16x32_bf16 v[164:167], a[164:167], v[104:107], v[164:167]\n" \
"ds_read_b128 a[132:135], v48 offset:9216\n" \
"ds_read_b128 a[148:151], v48 offset:13312\n" \
"v_mfma_f32_16x16x32_bf16 v[136:139], a[168:171], v[100:103], v[136:139]\n" \
"ds_read_b128 a[136:139], v48 offset:10240\n" \
"ds_read_b128 a[152:155], v48 offset:14336\n" \
"v_mfma_f32_16x16x32_bf16 v[168:171], a[168:171], v[104:107], v[168:171]\n" \
"ds_read_b128 a[140:143], v48 offset:11264\n" \
"ds_read_b128 a[156:159], v48 offset:15360\n" \
"v_mfma_f32_16x16x32_bf16 v[140:143], a[172:175], v[100:103], v[140:143]\n" \
"v_exp_f32_e64 v70, v70\n" \
"v_mfma_f32_16x16x32_bf16 v[172:175], a[172:175], v[104:107], v[172:175]\n" \
"v_exp_f32_e64 v71, v71\n" \
"v_mfma_f32_16x16x32_bf16 v[144:147], a[176:179], v[100:103], v[144:147]\n" \
"buffer_load_dwordx4 v6, s[84:87], s92 offen offset:2048 lds\n" \
"v_mfma_f32_16x16x32_bf16 v[176:179], a[176:179], v[104:107], v[176:179]\n" \
"v_exp_f32_e64 v72, v72\n" \
"v_mfma_f32_16x16x32_bf16 v[148:151], a[180:183], v[100:103], v[148:151]\n" \
"v_exp_f32_e64 v73, v73\n" \
"v_mfma_f32_16x16x32_bf16 v[180:183], a[180:183], v[104:107], v[180:183]\n" \
"buffer_load_dwordx4 a[248:251], v17, s[56:59], s98 offen offset:1024 sc1\n" \
"v_mfma_f32_16x16x32_bf16 v[152:155], a[184:187], v[100:103], v[152:155]\n" \
"v_exp_f32_e64 v68, v68\n" \
"v_mfma_f32_16x16x32_bf16 v[184:187], a[184:187], v[104:107], v[184:187]\n" \
"v_exp_f32_e64 v69, v69\n" \
"v_mfma_f32_16x16x32_bf16 v[156:159], a[188:191], v[100:103], v[156:159]\n" \
"buffer_load_dwordx4 v7, s[84:87], s92 offen offset:3072 lds\n" \
"v_mfma_f32_16x16x32_bf16 v[188:191], a[188:191], v[104:107], v[188:191]\n" \
"s_add_u32 m0, s91, 0x2000\n" \
"v_cvt_pk_bf16_f32 v71, v70, v71\n" \
"v_smfmac_f32_16x16x64_bf16 a[192:195], a[200:203], v[100:107], v21\n" \
"v_exp_f32_e64 v74, v74\n" \
"v_mfma_f32_16x16x32_bf16 v[220:223], a[188:191], v[116:119], v[220:223]\n" \
"v_exp_f32_e64 v75, v75\n" \
"v_mfma_f32_16x16x32_bf16 v[252:255], a[188:191], v[120:123], v[252:255]\n" \
"v_exp_f32_e64 v66, v66\n" \
"v_mfma_f32_16x16x32_bf16 v[216:219], a[184:187], v[116:119], v[216:219]\n" \
"v_exp_f32_e64 v67, v67\n" \
"v_mfma_f32_16x16x32_bf16 v[248:251], a[184:187], v[120:123], v[248:251]\n" \
"v_exp_f32_e64 v64, v64\n" \
"v_mfma_f32_16x16x32_bf16 v[212:215], a[180:183], v[116:119], v[212:215]\n" \
"buffer_load_dwordx4 v8, s[84:87], s92 offen offset:0 lds\n" \
"v_mfma_f32_16x16x32_bf16 v[244:247], a[180:183], v[120:123], v[244:247]\n" \
"v_exp_f32_e64 v65, v65\n" \
"v_mfma_f32_16x16x32_bf16 v[208:211], a[176:179], v[116:119], v[208:211]\n" \
"v_cvt_pk_bf16_f32 v72, v72, v73\n" \
"v_cvt_pk_bf16_f32 v70, v68, v69\n" \
"v_mfma_f32_16x16x32_bf16 v[240:243], a[176:179], v[120:123], v[240:243]\n" \
"v_exp_f32_e64 v76, v76\n" \
"v_mfma_f32_16x16x32_bf16 v[204:207], a[172:175], v[116:119], v[204:207]\n" \
"v_exp_f32_e64 v77, v77\n" \
"v_mfma_f32_16x16x32_bf16 v[236:239], a[172:175], v[120:123], v[236:239]\n" \
"v_exp_f32_e64 v78, v78\n" \
"v_mfma_f32_16x16x32_bf16 v[200:203], a[168:171], v[116:119], v[200:203]\n" \
"v_exp_f32_e64 v79, v79\n" \
"v_mfma_f32_16x16x32_bf16 v[232:235], a[168:171], v[120:123], v[232:235]\n" \
"v_cvt_pk_bf16_f32 v73, v74, v75\n" \
"v_cvt_pk_bf16_f32 v69, v66, v67\n" \
"v_mfma_f32_16x16x32_bf16 v[196:199], a[164:167], v[116:119], v[196:199]\n" \
"v_cvt_pk_bf16_f32 v68, v64, v65\n" \
"v_cvt_pk_bf16_f32 v74, v76, v77\n" \
"v_mfma_f32_16x16x32_bf16 v[228:231], a[164:167], v[120:123], v[228:231]\n" \
"v_cvt_pk_bf16_f32 v75, v78, v79\n" \
"v_xor_b32_e32 v48, 0x8000, v48\n" \
"v_mfma_f32_16x16x32_bf16 v[192:195], a[160:163], v[116:119], v[192:195]\n" \
"s_add_u32 s64, s64, 0x10000\n" \
"v_mfma_f32_16x16x32_bf16 v[224:227], a[160:163], v[120:123], v[224:227]\n" \
"v_smfmac_f32_16x16x64_bf16 a[196:199], a[200:203], v[116:123], v21\n" \
"s_waitcnt vmcnt(14) lgkmcnt(0)\n" \
"s_barrier\n" \
"s_cmp_lg_u32 s16, 0\n" \
"s_cbranch_scc1 L_f1_%=\n" \
"ds_read_b128 a[160:163], v49 offset:0\n" \
"ds_read_b128 a[164:167], v49 offset:1024\n" \
"ds_read_b128 a[168:171], v49 offset:2048\n" \
"ds_read_b128 a[172:175], v49 offset:3072\n" \
"ds_read_b128 a[176:179], v49 offset:4096\n" \
"ds_read_b128 a[180:183], v49 offset:5120\n" \
"ds_read_b128 a[184:187], v49 offset:6144\n" \
"ds_read_b128 a[188:191], v49 offset:7168\n" \
"v_add_u32_e32 v54, 0x49000, v20\n" \
"buffer_load_dwordx4 a[96:99], v20, s[56:59], s60 offen offset:0 sc1\n" \
"v_add_u32_e32 v50, s15, v20\n" \
"v_add_u32_e32 v51, 0x10000, v50\n" \
"ds_read_b32 v60, v18 offset:0\n" \
"ds_read_b32 v61, v18 offset:128\n" \
"ds_read_b128 a[0:3], v49 offset:8192\n" \
"ds_read_b128 a[4:7], v49 offset:9216\n" \
"ds_read_b128 a[8:11], v49 offset:10240\n" \
"ds_read_b128 a[12:15], v49 offset:11264\n" \
"ds_read_b128 a[16:19], v49 offset:12288\n" \
"ds_read_b128 a[20:23], v49 offset:13312\n" \
"ds_read_b128 a[24:27], v49 offset:14336\n" \
"ds_read_b128 a[28:31], v49 offset:15360\n" \
"v_exp_f32_e32 v86, v86\n" \
"v_exp_f32_e32 v87, v87\n" \
"v_exp_f32_e32 v84, v84\n" \
"v_exp_f32_e32 v85, v85\n" \
"v_cvt_pk_bf16_f32 v87, v86, v87\n" \
"buffer_load_dwordx4 a[100:103], v20, s[56:59], s60 offen offset:1024 sc1\n" \
"v_exp_f32_e32 v82, v82\n" \
"v_exp_f32_e32 v83, v83\n" \
"v_exp_f32_e32 v80, v80\n" \
"v_exp_f32_e32 v81, v81\n" \
"s_waitcnt lgkmcnt(15)\n" \
"v_mfma_f32_16x16x32_bf16 v[128:131], a[160:163], v[68:71], v[128:131]\n" \
"v_cvt_pk_bf16_f32 v86, v84, v85\n" \
"v_cvt_pk_bf16_f32 v85, v82, v83\n" \
"v_mfma_f32_16x16x32_bf16 v[160:163], a[160:163], v[72:75], v[160:163]\n" \
"v_cvt_pk_bf16_f32 v84, v80, v81\n" \
"v_and_b32_e32 v84, v12, v84\n" \
"v_mfma_f32_16x16x32_bf16 v[132:135], a[164:167], v[68:71], v[132:135]\n" \
"v_and_b32_e32 v85, v13, v85\n" \
"v_and_b32_e32 v86, v14, v86\n" \
"v_mfma_f32_16x16x32_bf16 v[164:167], a[164:167], v[72:75], v[164:167]\n" \
"buffer_load_dwordx4 a[104:107], v20, s[56:59], s60 offen offset:2048 sc1\n" \
"v_mfma_f32_16x16x32_bf16 v[136:139], a[168:171], v[68:71], v[136:139]\n" \
"v_cvt_pk_bf16_f32 v128, v128, v129\n" \
"v_cvt_pk_bf16_f32 v160, v160, v161\n" \
"v_mfma_f32_16x16x32_bf16 v[168:171], a[168:171], v[72:75], v[168:171]\n" \
"v_cvt_pk_bf16_f32 v129, v130, v131\n" \
"s_waitcnt lgkmcnt(10)\n" \
"v_mfma_f32_16x16x32_bf16 v[140:143], a[172:175], v[68:71], v[140:143]\n" \
"v_cvt_pk_bf16_f32 v161, v162, v163\n" \
"v_cvt_pk_bf16_f32 v130, v132, v133\n" \
"v_mfma_f32_16x16x32_bf16 v[172:175], a[172:175], v[72:75], v[172:175]\n" \
"v_cvt_pk_bf16_f32 v131, v134, v135\n" \
"v_cvt_pk_bf16_f32 v162, v164, v165\n" \
"v_mfma_f32_16x16x32_bf16 v[144:147], a[176:179], v[68:71], v[144:147]\n" \
"v_cvt_pk_bf16_f32 v163, v166, v167\n" \
"v_cvt_pk_bf16_f32 v132, v136, v137\n" \
"v_mfma_f32_16x16x32_bf16 v[176:179], a[176:179], v[72:75], v[176:179]\n" \
"buffer_load_dwordx4 a[108:111], v20, s[56:59], s60 offen offset:3072 sc1\n" \
"v_mfma_f32_16x16x32_bf16 v[148:151], a[180:183], v[68:71], v[148:151]\n" \
"v_cvt_pk_bf16_f32 v133, v138, v139\n" \
"v_cvt_pk_bf16_f32 v134, v140, v141\n" \
"v_mfma_f32_16x16x32_bf16 v[180:183], a[180:183], v[72:75], v[180:183]\n" \
"v_cvt_pk_bf16_f32 v135, v142, v143\n" \
"v_cvt_pk_bf16_f32 v164, v168, v169\n" \
"v_mfma_f32_16x16x32_bf16 v[152:155], a[184:187], v[68:71], v[152:155]\n" \
"v_cvt_pk_bf16_f32 v165, v170, v171\n" \
"v_cvt_pk_bf16_f32 v166, v172, v173\n" \
"v_mfma_f32_16x16x32_bf16 v[184:187], a[184:187], v[72:75], v[184:187]\n" \
"v_cvt_pk_bf16_f32 v167, v174, v175\n" \
"v_cvt_pk_bf16_f32 v136, v144, v145\n" \
"v_mfma_f32_16x16x32_bf16 v[156:159], a[188:191], v[68:71], v[156:159]\n" \
"v_cvt_pk_bf16_f32 v137, v146, v147\n" \
"v_cvt_pk_bf16_f32 v138, v148, v149\n" \
"v_mfma_f32_16x16x32_bf16 v[188:191], a[188:191], v[72:75], v[188:191]\n" \
"v_cvt_pk_bf16_f32 v139, v150, v151\n" \
"v_cvt_pk_bf16_f32 v140, v152, v153\n" \
"v_smfmac_f32_16x16x64_bf16 a[192:195], a[200:203], v[68:75], v21\n" \
"v_cvt_pk_bf16_f32 v141, v154, v155\n" \
"v_cvt_pk_bf16_f32 v168, v176, v177\n" \
"v_mfma_f32_16x16x32_bf16 v[120:123], a[128:131], a[112:115], v[44:47]\n" \
"v_cvt_pk_bf16_f32 v142, v156, v157\n" \
"v_cvt_pk_bf16_f32 v143, v158, v159\n" \
"v_mfma_f32_16x16x32_bf16 v[120:123], a[132:135], a[116:119], v[120:123]\n" \
"v_cvt_pk_bf16_f32 v169, v178, v179\n" \
"v_cvt_pk_bf16_f32 v170, v180, v181\n" \
"v_cvt_pk_bf16_f32 v171, v182, v183\n" \
"v_cvt_pk_bf16_f32 v172, v184, v185\n" \
"v_cvt_pk_bf16_f32 v173, v186, v187\n" \
"v_cvt_pk_bf16_f32 v174, v188, v189\n" \
"v_cvt_pk_bf16_f32 v175, v190, v191\n" \
"ds_write_b128 v50, v[128:131] offset:0\n" \
"v_mfma_f32_16x16x32_bf16 v[120:123], a[136:139], a[120:123], v[120:123]\n" \
"v_and_b32_e32 v87, v15, v87\n" \
"v_accvgpr_read_b32 v40, a192\n" \
"v_mfma_f32_16x16x32_bf16 v[120:123], a[140:143], a[124:127], v[120:123]\n" \
"ds_write_b128 v50, v[132:135] offset:1024\n" \
"v_mfma_f32_16x16x32_bf16 v[124:127], a[156:159], a[124:127], v[44:47]\n" \
"buffer_load_dwordx4 a[124:127], v54, s[56:59], s60 offen offset:3072 sc1\n" \
"v_mfma_f32_16x16x32_bf16 v[124:127], a[152:155], a[120:123], v[124:127]\n" \
"ds_write_b128 v50, v[136:139] offset:2048\n" \
"v_mfma_f32_16x16x32_bf16 v[124:127], a[148:151], a[116:119], v[124:127]\n" \
"v_accvgpr_read_b32 v22, a193\n" \
"v_accvgpr_read_b32 v41, a194\n" \
"v_mfma_f32_16x16x32_bf16 v[124:127], a[144:147], a[112:115], v[124:127]\n" \
"ds_write_b128 v50, v[140:143] offset:3072\n" \
"v_mfma_f32_16x16x32_bf16 a[196:199], a[204:207], v[84:87], a[196:199]\n" \
"v_accvgpr_read_b32 v23, a195\n" \
"v_add_f32_e32 v40, v40, v22\n" \
"v_mfma_f32_16x16x32_bf16 v[192:195], a[160:163], v[84:87], v[192:195]\n" \
"ds_write_b128 v51, v[160:163] offset:32768\n" \
"v_mfma_f32_16x16x32_bf16 v[196:199], a[164:167], v[84:87], v[196:199]\n" \
"buffer_load_dwordx4 a[112:115], v54, s[56:59], s60 offen offset:0 sc1\n" \
"v_mfma_f32_16x16x32_bf16 v[200:203], a[168:171], v[84:87], v[200:203]\n" \
"ds_write_b128 v51, v[164:167] offset:33792\n" \
"v_add_f32_e32 v41, v41, v23\n" \
"ds_write_b64 v19, v[40:41]\n" \
"v_mfma_f32_16x16x32_bf16 v[204:207], a[172:175], v[84:87], v[204:207]\n" \
"v_lshl_add_u32 v52, s15, 2, v20\n" \
"v_add_u32_e32 v52, 0x1a000, v52\n" \
"ds_write_b128 v51, v[168:171] offset:34816\n" \
"v_mfma_f32_16x16x32_bf16 v[208:211], a[176:179], v[84:87], v[208:211]\n" \
"v_exp_f32_e32 v88, v88\n" \
"v_mfma_f32_16x16x32_bf16 v[212:215], a[180:183], v[84:87], v[212:215]\n" \
"ds_write_b128 v51, v[172:175] offset:35840\n" \
"v_mfma_f32_16x16x32_bf16 v[216:219], a[184:187], v[84:87], v[216:219]\n" \
"buffer_load_dwordx4 a[116:119], v54, s[56:59], s60 offen offset:1024 sc1\n" \
"v_mfma_f32_16x16x32_bf16 v[220:223], a[188:191], v[84:87], v[220:223]\n" \
"v_exp_f32_e32 v89, v89\n" \
"v_exp_f32_e32 v90, v90\n" \
"v_exp_f32_e32 v91, v91\n" \
"v_cvt_pk_bf16_f32 v88, v88, v89\n" \
"v_exp_f32_e32 v92, v92\n" \
"v_exp_f32_e32 v93, v93\n" \
"v_exp_f32_e32 v94, v94\n" \
"v_exp_f32_e32 v95, v95\n" \
"v_cvt_pk_bf16_f32 v89, v90, v91\n" \
"v_cvt_pk_bf16_f32 v90, v92, v93\n" \
"v_cvt_pk_bf16_f32 v91, v94, v95\n" \
"v_xor_b32_e32 v63, 0x400, v19\n" \
"buffer_load_dwordx4 a[120:123], v54, s[56:59], s60 offen offset:2048 sc1\n" \
"v_exp_f32_e32 v120, v120\n" \
"v_exp_f32_e32 v121, v121\n" \
"v_exp_f32_e32 v122, v122\n" \
"v_exp_f32_e32 v123, v123\n" \
".fill (L_ab + 12288 - .) / 4, 4, 0xBF800000\n" \
"L_ap3:\n" \
"s_cbranch_execz L_ap0\n" \
"v_cvt_pk_bf16_f32 v120, v120, v121\n" \
"v_exp_f32_e32 v124, v124\n" \
"v_exp_f32_e32 v125, v125\n" \
"s_waitcnt lgkmcnt(0)\n" \
"s_barrier\n" \
"ds_read_b128 v[144:147], v52 offset:0\n" \
"ds_read_b128 v[148:151], v52 offset:1024\n" \
"ds_read_b128 v[152:155], v52 offset:2048\n" \
"ds_read_b128 v[156:159], v52 offset:3072\n" \
"ds_read_b128 v[176:179], v52 offset:4096\n" \
"ds_read_b128 v[180:183], v52 offset:5120\n" \
"ds_read_b128 v[184:187], v52 offset:6144\n" \
"ds_read_b128 v[188:191], v52 offset:7168\n" \
"v_mfma_f32_16x16x32_bf16 a[196:199], a[212:215], v[88:91], a[196:199]\n" \
"ds_read_b64 v[42:43], v63\n" \
"v_cvt_pk_bf16_f32 v121, v122, v123\n" \
"v_mfma_f32_16x16x32_bf16 v[224:227], a[160:163], v[88:91], v[224:227]\n" \
"v_exp_f32_e32 v126, v126\n" \
"v_mfma_f32_16x16x32_bf16 v[228:231], a[164:167], v[88:91], v[228:231]\n" \
"v_exp_f32_e32 v127, v127\n" \
"v_mfma_f32_16x16x32_bf16 v[232:235], a[168:171], v[88:91], v[232:235]\n" \
"v_cvt_pk_bf16_f32 v122, v124, v125\n" \
"v_cvt_pk_bf16_f32 v123, v126, v127\n" \
"v_mfma_f32_16x16x32_bf16 v[236:239], a[172:175], v[88:91], v[236:239]\n" \
"v_and_b32_e32 v120, v12, v120\n" \
"v_and_b32_e32 v121, v13, v121\n" \
"v_mfma_f32_16x16x32_bf16 v[240:243], a[176:179], v[88:91], v[240:243]\n" \
"v_and_b32_e32 v122, v14, v122\n" \
"v_and_b32_e32 v123, v15, v123\n" \
"v_mfma_f32_16x16x32_bf16 v[244:247], a[180:183], v[88:91], v[244:247]\n" \
"v_readlane_b32 s93, v11, 22\n" \
"v_readlane_b32 s92, v11, 27\n" \
"v_mfma_f32_16x16x32_bf16 v[248:251], a[184:187], v[88:91], v[248:251]\n" \
"s_add_u32 m0, s93, 0x0\n" \
"v_mov_b32_e32 v130, v28\n" \
"v_mfma_f32_16x16x32_bf16 v[252:255], a[188:191], v[88:91], v[252:255]\n" \
"v_mov_b32_e32 v131, v29\n" \
"v_mov_b32_e32 v132, 0\n" \
"v_mov_b32_e32 v133, 0\n" \
"v_mov_b32_e32 v128, 0\n" \
"v_mov_b32_e32 v129, 0\n" \
"s_waitcnt vmcnt(8) lgkmcnt(1)\n" \
"s_barrier\n" \
"v_mfma_f32_16x16x32_bf16 a[196:199], a[212:215], v[120:123], a[196:199]\n" \
"buffer_load_dwordx4 v0, s[80:83], s92 offen offset:0 lds\n" \
"v_mfma_f32_16x16x32_bf16 v[224:227], a[0:3], v[120:123], v[224:227]\n" \
"v_mfma_f32_16x16x32_bf16 v[228:231], a[4:7], v[120:123], v[228:231]\n" \
"v_mfma_f32_16x16x32_bf16 v[232:235], a[8:11], v[120:123], v[232:235]\n" \
"v_mfma_f32_16x16x32_bf16 v[236:239], a[12:15], v[120:123], v[236:239]\n" \
"v_mfma_f32_16x16x32_bf16 v[240:243], a[16:19], v[120:123], v[240:243]\n" \
"v_mfma_f32_16x16x32_bf16 v[244:247], a[20:23], v[120:123], v[244:247]\n" \
"buffer_load_dwordx4 v1, s[80:83], s92 offen offset:1024 lds\n" \
"v_mfma_f32_16x16x32_bf16 v[248:251], a[24:27], v[120:123], v[248:251]\n" \
"v_accvgpr_read_b32 v56, a196\n" \
"v_accvgpr_read_b32 v24, a197\n" \
"v_mfma_f32_16x16x32_bf16 v[252:255], a[28:31], v[120:123], v[252:255]\n" \
"v_add_f32_e32 v56, v56, v24\n" \
"s_waitcnt lgkmcnt(0)\n" \
"v_add_f32_e32 v56, v56, v42\n" \
"v_mfma_f32_16x16x32_bf16 v[192:195], v[130:133], v[144:147], v[192:195]\n" \
"v_max_f32_e32 v62, v62, v56\n" \
"v_accvgpr_read_b32 v57, a198\n" \
"v_mfma_f32_16x16x32_bf16 v[196:199], v[128:131], v[144:147], v[196:199]\n" \
"v_rcp_f32_e32 v56, v56\n" \
"v_mfma_f32_16x16x32_bf16 v[200:203], v[130:133], v[148:151], v[200:203]\n" \
"v_mul_f32_e32 v56, v56, v60\n" \
"v_accvgpr_read_b32 v25, a199\n" \
"buffer_load_dwordx4 v2, s[80:83], s92 offen offset:2048 lds\n" \
"v_mfma_f32_16x16x32_bf16 v[204:207], v[128:131], v[148:151], v[204:207]\n" \
"v_mul_f32_e32 v192, v56, v192\n" \
"v_mul_f32_e32 v193, v56, v193\n" \
"v_mfma_f32_16x16x32_bf16 v[208:211], v[130:133], v[152:155], v[208:211]\n" \
"v_mul_f32_e32 v194, v56, v194\n" \
"v_mul_f32_e32 v195, v56, v195\n" \
"v_mfma_f32_16x16x32_bf16 v[212:215], v[128:131], v[152:155], v[212:215]\n" \
"v_mul_f32_e32 v196, v56, v196\n" \
"v_mul_f32_e32 v197, v56, v197\n" \
"v_mfma_f32_16x16x32_bf16 v[216:219], v[130:133], v[156:159], v[216:219]\n" \
"v_mul_f32_e32 v198, v56, v198\n" \
"v_mul_f32_e32 v199, v56, v199\n" \
"v_mfma_f32_16x16x32_bf16 v[220:223], v[128:131], v[156:159], v[220:223]\n" \
"v_cvt_pk_bf16_f32 v64, v192, v193\n" \
"v_cvt_pk_bf16_f32 v65, v194, v195\n" \
"v_mfma_f32_16x16x32_bf16 v[224:227], v[130:133], v[176:179], v[224:227]\n" \
"buffer_load_dwordx4 v3, s[80:83], s92 offen offset:3072 lds\n" \
"s_add_u32 m0, s93, 0x1000\n" \
"v_mfma_f32_16x16x32_bf16 v[228:231], v[128:131], v[176:179], v[228:231]\n" \
"v_cvt_pk_bf16_f32 v66, v196, v197\n" \
"v_cvt_pk_bf16_f32 v67, v198, v199\n" \
"v_mfma_f32_16x16x32_bf16 v[232:235], v[130:133], v[180:183], v[232:235]\n" \
"v_mul_f32_e32 v200, v56, v200\n" \
"v_mul_f32_e32 v201, v56, v201\n" \
"v_mfma_f32_16x16x32_bf16 v[236:239], v[128:131], v[180:183], v[236:239]\n" \
"v_mfma_f32_16x16x32_bf16 a[208:211], a[216:219], v[64:67], a[208:211]\n" \
"v_mul_f32_e32 v202, v56, v202\n" \
"v_mul_f32_e32 v203, v56, v203\n" \
"v_mfma_f32_16x16x32_bf16 v[240:243], v[130:133], v[184:187], v[240:243]\n" \
"v_mul_f32_e32 v204, v56, v204\n" \
"v_mul_f32_e32 v205, v56, v205\n" \
"buffer_load_dwordx4 v4, s[80:83], s92 offen offset:0 lds\n" \
"v_mul_f32_e32 v206, v56, v206\n" \
"v_mul_f32_e32 v207, v56, v207\n" \
"v_add_f32_e32 v57, v57, v25\n" \
"v_cvt_pk_bf16_f32 v68, v200, v201\n" \
"v_cvt_pk_bf16_f32 v69, v202, v203\n" \
"v_cvt_pk_bf16_f32 v70, v204, v205\n" \
"v_cvt_pk_bf16_f32 v71, v206, v207\n" \
"v_add_f32_e32 v57, v57, v43\n" \
"v_mul_f32_e32 v208, v56, v208\n" \
"v_mfma_f32_16x16x32_bf16 a[208:211], a[216:219], v[68:71], a[208:211]\n" \
"v_mul_f32_e32 v209, v56, v209\n" \
"v_mul_f32_e32 v210, v56, v210\n" \
"v_mul_f32_e32 v211, v56, v211\n" \
"v_mul_f32_e32 v212, v56, v212\n" \
"v_mul_f32_e32 v213, v56, v213\n" \
"v_mul_f32_e32 v214, v56, v214\n" \
"v_mul_f32_e32 v215, v56, v215\n" \
"v_max_f32_e32 v62, v62, v57\n" \
"v_cvt_pk_bf16_f32 v72, v208, v209\n" \
"v_cvt_pk_bf16_f32 v73, v210, v211\n" \
"buffer_load_dwordx4 v5, s[80:83], s92 offen offset:1024 lds\n" \
"v_cvt_pk_bf16_f32 v74, v212, v213\n" \
"v_cvt_pk_bf16_f32 v75, v214, v215\n" \
"v_rcp_f32_e32 v57, v57\n" \
"v_mfma_f32_16x16x32_bf16 v[244:247], v[128:131], v[184:187], v[244:247]\n" \
"v_mfma_f32_16x16x32_bf16 a[208:211], a[216:219], v[72:75], a[208:211]\n" \
"v_mul_f32_e32 v216, v56, v216\n" \
"v_mul_f32_e32 v217, v56, v217\n" \
"v_mul_f32_e32 v218, v56, v218\n" \
"v_mul_f32_e32 v219, v56, v219\n" \
"v_mul_f32_e32 v220, v56, v220\n" \
"v_mul_f32_e32 v221, v56, v221\n" \
"v_mul_f32_e32 v222, v56, v222\n" \
"v_mul_f32_e32 v223, v56, v223\n" \
"v_cvt_pk_bf16_f32 v76, v216, v217\n" \
"v_cvt_pk_bf16_f32 v77, v218, v219\n" \
"v_cvt_pk_bf16_f32 v78, v220, v221\n" \
"buffer_load_dwordx4 v6, s[80:83], s92 offen offset:2048 lds\n" \
"v_cvt_pk_bf16_f32 v79, v222, v223\n" \
"v_mul_f32_e32 v57, v57, v61\n" \
"v_mul_f32_e32 v224, v57, v224\n" \
"v_mfma_f32_16x16x32_bf16 a[208:211], a[216:219], v[76:79], a[208:211]\n" \
"v_mul_f32_e32 v225, v57, v225\n" \
"v_mul_f32_e32 v226, v57, v226\n" \
"v_mul_f32_e32 v227, v57, v227\n" \
"v_mul_f32_e32 v228, v57, v228\n" \
"v_mul_f32_e32 v229, v57, v229\n" \
"v_mul_f32_e32 v230, v57, v230\n" \
"v_mul_f32_e32 v231, v57, v231\n" \
"v_cvt_pk_bf16_f32 v80, v224, v225\n" \
"v_cvt_pk_bf16_f32 v81, v226, v227\n" \
"v_cvt_pk_bf16_f32 v82, v228, v229\n" \
"v_cvt_pk_bf16_f32 v83, v230, v231\n" \
"v_mfma_f32_16x16x32_bf16 v[248:251], v[130:133], v[188:191], v[248:251]\n" \
"v_mul_f32_e32 v232, v57, v232\n" \
"v_mul_f32_e32 v233, v57, v233\n" \
"v_mfma_f32_16x16x32_bf16 a[208:211], a[216:219], v[80:83], a[208:211]\n" \
"buffer_load_dwordx4 v7, s[80:83], s92 offen offset:3072 lds\n" \
"s_add_u32 m0, s93, 0x2000\n" \
"v_mul_f32_e32 v234, v57, v234\n" \
"v_mul_f32_e32 v235, v57, v235\n" \
"v_mul_f32_e32 v236, v57, v236\n" \
"v_mul_f32_e32 v237, v57, v237\n" \
"v_mul_f32_e32 v238, v57, v238\n" \
"v_mul_f32_e32 v239, v57, v239\n" \
"v_cvt_pk_bf16_f32 v84, v232, v233\n" \
"v_cvt_pk_bf16_f32 v85, v234, v235\n" \
"v_cvt_pk_bf16_f32 v86, v236, v237\n" \
"v_cvt_pk_bf16_f32 v87, v238, v239\n" \
"v_mul_f32_e32 v240, v57, v240\n" \
"v_mul_f32_e32 v241, v57, v241\n" \
"v_mfma_f32_16x16x32_bf16 a[208:211], a[216:219], v[84:87], a[208:211]\n" \
"v_mul_f32_e32 v242, v57, v242\n" \
"v_mul_f32_e32 v243, v57, v243\n" \
"v_mul_f32_e32 v244, v57, v244\n" \
"v_mul_f32_e32 v245, v57, v245\n" \
"v_mul_f32_e32 v246, v57, v246\n" \
"v_mul_f32_e32 v247, v57, v247\n" \
"buffer_load_dwordx4 v8, s[80:83], s92 offen offset:0 lds\n" \
"v_cvt_pk_bf16_f32 v96, v240, v241\n" \
"v_cvt_pk_bf16_f32 v97, v242, v243\n" \
"v_cvt_pk_bf16_f32 v98, v244, v245\n" \
"v_cvt_pk_bf16_f32 v99, v246, v247\n" \
"v_mfma_f32_16x16x32_bf16 v[252:255], v[128:131], v[188:191], v[252:255]\n" \
"v_mul_f32_e32 v248, v57, v248\n" \
"v_mfma_f32_16x16x32_bf16 a[208:211], a[216:219], v[96:99], a[208:211]\n" \
"v_mul_f32_e32 v249, v57, v249\n" \
"v_mul_f32_e32 v250, v57, v250\n" \
"v_mul_f32_e32 v251, v57, v251\n" \
"v_cvt_pk_bf16_f32 v100, v248, v249\n" \
"v_cvt_pk_bf16_f32 v101, v250, v251\n" \
"v_cmp_lt_f32_e64 s[72:73], s21, v62\n" \
"v_mul_f32_e32 v252, v57, v252\n" \
"v_mul_f32_e32 v253, v57, v253\n" \
"v_mul_f32_e32 v254, v57, v254\n" \
"v_mul_f32_e32 v255, v57, v255\n" \
"v_cvt_pk_bf16_f32 v102, v252, v253\n" \
"v_cvt_pk_bf16_f32 v103, v254, v255\n" \
"s_nop 1\n" \
"v_mfma_f32_16x16x32_bf16 a[208:211], a[216:219], v[100:103], a[208:211]\n" \
"s_nop 7\n" \
"v_accvgpr_read_b32 v63, a208\n" \
"v_cmp_u_f32_e32 vcc, v63, v63\n" \
"s_nop 0\n" \
"s_or_b64 vcc, s[72:73], vcc\n" \
"s_cbranch_scc0 L_out_%=\n" \
"s_branch L_end_%=\n" \
"L_skb_%=:\n" \
"s_sleep 1\n" \
"buffer_load_dword v57, v61, s[56:59], s96 offen sc1\n" \
"s_add_u32 s101, s101, 1\n" \
"s_waitcnt vmcnt(0)\n" \
"v_cmp_ne_u32_e32 vcc, s62, v57\n" \
"s_cmp_gt_u32 s101, 0x100000\n" \
"s_cbranch_scc1 L_rkb_%=\n" \
"s_nop 2\n" \
"s_cbranch_vccnz L_skb_%=\n" \
"s_branch L_rkb_%=\n" \
"L_sol_%=:\n" \
"s_sleep 1\n" \
"buffer_load_dword v26, v27, s[56:59], s96 offen sc1\n" \
"s_add_u32 s101, s101, 1\n" \
"s_waitcnt vmcnt(0)\n" \
"v_cmp_ne_u32_e32 vcc, s62, v26\n" \
"s_cmp_gt_u32 s101, 0x100000\n" \
"s_cbranch_scc1 L_rol_%=\n" \
"s_nop 2\n" \
"s_cbranch_vccnz L_sol_%=\n" \
"s_branch L_rol_%=\n" \
"L_sos_%=:\n" \
"s_sleep 1\n" \
"buffer_load_dword v26, v27, s[56:59], s96 offen sc1\n" \
"s_add_u32 s101, s101, 1\n" \
"s_waitcnt vmcnt(0)\n" \
"v_cmp_ne_u32_e32 vcc, s62, v26\n" \
"s_cmp_gt_u32 s101, 0x100000\n" \
"s_cbranch_scc1 L_ros_%=\n" \
"s_nop 2\n" \
"s_cbranch_vccnz L_sos_%=\n" \
"s_branch L_ros_%=\n" \
"L_sa_%=:\n" \
"s_sleep 1\n" \
"buffer_load_dword v26, v38, s[56:59], s64 offen sc1\n" \
"s_add_u32 s101, s101, 1\n" \
"s_waitcnt vmcnt(0)\n" \
"v_cmp_ne_u32_e32 vcc, s62, v26\n" \
"s_cmp_gt_u32 s101, 0x100000\n" \
"s_cbranch_scc1 L_ra_%=\n" \
"s_nop 2\n" \
"s_cbranch_vccnz L_sa_%=\n" \
"s_branch L_ra_%=\n" \
"L_sb_%=:\n" \
"s_sleep 1\n" \
"buffer_load_dword v47, v38, s[56:59], s71 offen sc1\n" \
"s_add_u32 s101, s101, 1\n" \
"s_waitcnt vmcnt(0)\n" \
"v_cmp_ne_u32_e32 vcc, s62, v47\n" \
"s_cmp_gt_u32 s101, 0x100000\n" \
"s_cbranch_scc1 L_rb_%=\n" \
"s_nop 2\n" \
"s_cbranch_vccnz L_sb_%=\n" \
"s_branch L_rb_%=\n" \
"L_sx0_%=:\n" \
"s_sleep 1\n" \
"buffer_load_dword v37, v40, s[56:59], s74 offen sc1\n" \
"s_add_u32 s101, s101, 1\n" \
"s_waitcnt vmcnt(0)\n" \
"v_cmp_eq_u32_e64 s[98:99], s62, v37\n" \
"s_cmp_gt_u32 s101, 0x100000\n" \
"s_cbranch_scc1 L_rx0_%=\n" \
"s_or_b32 s75, 0x7f, s100\n" \
"s_andn2_b32 s75, s75, s98\n" \
"s_cbranch_scc1 L_sx0_%=\n" \
"s_branch L_rx0_%=\n" \
"L_sx1_%=:\n" \
"s_sleep 1\n" \
"buffer_load_dword v37, v40, s[56:59], s74 offen sc1\n" \
"s_add_u32 s101, s101, 1\n" \
"s_waitcnt vmcnt(0)\n" \
"v_cmp_eq_u32_e64 s[98:99], s62, v37\n" \
"s_cmp_gt_u32 s101, 0x100000\n" \
"s_cbranch_scc1 L_rx1_%=\n" \
"s_andn2_b32 s75, 0x100, s98\n" \
"s_cbranch_scc1 L_sx1_%=\n" \
"s_branch L_rx1_%=\n" \
"L_sx2_%=:\n" \
"s_sleep 1\n" \
"buffer_load_dword v37, v40, s[56:59], s74 offen sc1\n" \
"s_add_u32 s101, s101, 1\n" \
"s_waitcnt vmcnt(0)\n" \
"v_cmp_eq_u32_e64 s[98:99], s62, v37\n" \
"s_cmp_gt_u32 s101, 0x100000\n" \
"s_cbranch_scc1 L_rx2_%=\n" \
"s_andn2_b32 s75, 0x200, s98\n" \
"s_cbranch_scc1 L_sx2_%=\n" \
"s_branch L_rx2_%=\n" \
"L_sx3_%=:\n" \
"s_sleep 1\n" \
"buffer_load_dword v37, v40, s[56:59], s74 offen sc1\n" \
"s_add_u32 s101, s101, 1\n" \
"s_waitcnt vmcnt(0)\n" \
"v_cmp_eq_u32_e64 s[98:99], s62, v37\n" \
"s_cmp_gt_u32 s101, 0x100000\n" \
"s_cbranch_scc1 L_rx3_%=\n" \
"s_andn2_b32 s75, 0x400, s98\n" \
"s_cbranch_scc1 L_sx3_%=\n" \
"s_branch L_rx3_%=\n" \
"L_sx4_%=:\n" \
"s_sleep 1\n" \
"buffer_load_dword v37, v40, s[56:59], s74 offen sc1\n" \
"s_add_u32 s101, s101, 1\n" \
"s_waitcnt vmcnt(0)\n" \
"v_cmp_eq_u32_e64 s[98:99], s62, v37\n" \
"s_cmp_gt_u32 s101, 0x100000\n" \
"s_cbranch_scc1 L_rx4_%=\n" \
"s_andn2_b32 s75, 0x800, s98\n" \
"s_cbranch_scc1 L_sx4_%=\n" \
"s_branch L_rx4_%=\n" \
"L_sx5_%=:\n" \
"s_sleep 1\n" \
"buffer_load_dword v37, v40, s[56:59], s74 offen sc1\n" \
"s_add_u32 s101, s101, 1\n" \
"s_waitcnt vmcnt(0)\n" \
"v_cmp_eq_u32_e64 s[98:99], s62, v37\n" \
"s_cmp_gt_u32 s101, 0x100000\n" \
"s_cbranch_scc1 L_rx5_%=\n" \
"s_andn2_b32 s75, 0x1000, s98\n" \
"s_cbranch_scc1 L_sx5_%=\n" \
"s_branch L_rx5_%=\n" \
"L_sx6_%=:\n" \
"s_sleep 1\n" \
"buffer_load_dword v37, v40, s[56:59], s74 offen sc1\n" \
"s_add_u32 s101, s101, 1\n" \
"s_waitcnt vmcnt(0)\n" \
"v_cmp_eq_u32_e64 s[98:99], s62, v37\n" \
"s_cmp_gt_u32 s101, 0x100000\n" \
"s_cbranch_scc1 L_rx6_%=\n" \
"s_andn2_b32 s75, 0x2000, s98\n" \
"s_cbranch_scc1 L_sx6_%=\n" \
"s_branch L_rx6_%=\n" \
"L_sx7_%=:\n" \
"s_sleep 1\n" \
"buffer_load_dword v37, v40, s[56:59], s74 offen sc1\n" \
"s_add_u32 s101, s101, 1\n" \
"s_waitcnt vmcnt(0)\n" \
"v_cmp_eq_u32_e64 s[98:99], s62, v37\n" \
"s_cmp_gt_u32 s101, 0x100000\n" \
"s_cbranch_scc1 L_rx7_%=\n" \
"s_andn2_b32 s75, 0x4000, s98\n" \
"s_cbranch_scc1 L_sx7_%=\n" \
"s_branch L_rx7_%=\n" \
"L_att_stub:\n" \
"s_mov_b32 s101, 0\n" \
"v_add_u32_e32 v32, 0x49000, v20\n" \
"v_readlane_b32 s96, v11, 36\n" \
"v_readlane_b32 s97, v11, 37\n" \
"buffer_load_dwordx4 a[96:99], v20, s[56:59], s60 offen offset:0 sc1\n" \
"buffer_load_dwordx4 a[112:115], v32, s[56:59], s60 offen offset:0 sc1\n" \
"buffer_load_dwordx4 a[100:103], v20, s[56:59], s60 offen offset:1024 sc1\n" \
"buffer_load_dwordx4 a[116:119], v32, s[56:59], s60 offen offset:1024 sc1\n" \
"buffer_load_dwordx4 a[104:107], v20, s[56:59], s60 offen offset:2048 sc1\n" \
"buffer_load_dwordx4 a[120:123], v32, s[56:59], s60 offen offset:2048 sc1\n" \
"buffer_load_dwordx4 a[108:111], v20, s[56:59], s60 offen offset:3072 sc1\n" \
"buffer_load_dwordx4 a[124:127], v32, s[56:59], s60 offen offset:3072 sc1\n" \
"buffer_load_dwordx4 a[220:223], v20, s[56:59], s96 offen offset:0 sc1\n" \
"buffer_load_dwordx4 a[228:231], v32, s[56:59], s96 offen offset:0 sc1\n" \
"buffer_load_dwordx4 a[236:239], v20, s[56:59], s97 offen offset:0 sc1\n" \
"buffer_load_dwordx4 a[244:247], v32, s[56:59], s97 offen offset:0 sc1\n" \
"buffer_load_dwordx4 a[224:227], v20, s[56:59], s96 offen offset:1024 sc1\n" \
"buffer_load_dwordx4 a[232:235], v32, s[56:59], s96 offen offset:1024 sc1\n" \
"buffer_load_dwordx4 a[240:243], v20, s[56:59], s97 offen offset:1024 sc1\n" \
"buffer_load_dwordx4 a[248:251], v32, s[56:59], s97 offen offset:1024 sc1\n" \
"buffer_load_dwordx4 v[64:67], v20, s[56:59], s61 offen offset:0 sc1\n" \
"buffer_load_dwordx4 v[68:71], v20, s[56:59], s61 offen offset:1024 sc1\n" \
"buffer_load_dwordx4 v[72:75], v20, s[56:59], s61 offen offset:2048 sc1\n" \
"buffer_load_dwordx4 v[76:79], v20, s[56:59], s61 offen offset:3072 sc1\n" \
"buffer_load_dwordx4 v[80:83], v32, s[56:59], s61 offen offset:0 sc1\n" \
"buffer_load_dwordx4 v[84:87], v32, s[56:59], s61 offen offset:1024 sc1\n" \
"buffer_load_dwordx4 v[96:99], v32, s[56:59], s61 offen offset:2048 sc1\n" \
"buffer_load_dwordx4 v[100:103], v32, s[56:59], s61 offen offset:3072 sc1\n" \
"v_not_b32_e32 v26, s62\n" \
"s_waitcnt vmcnt(0)\n" \
"s_branch L_out_%=\n" \
".fill (L_ab + 16384 - .) / 4, 4, 0xBF800000\n" \
".fill (L_ab + 16384 - .) / 4, 4, 0xBF800000\n" \
"L_ap4:\n" \
"s_setpc_b64 s[88:89]\n" \
"L_gsw:\n" \
"v_mfma_f32_16x16x32_bf16 v[104:107], a[128:131], a[16:19], v[36:39]\n" \
"s_add_u32 s68, s60, 0xfff6e000\n" \
"s_add_u32 s69, s60, 0xfffb7000\n" \
"s_add_u32 s70, s60, 0x0\n" \
"s_add_u32 s71, s60, 0x49000\n" \
"v_mfma_f32_16x16x32_bf16 v[104:107], a[132:135], a[20:23], v[104:107]\n" \
"s_xor_b32 m0, s65, 0x12000\n" \
"ds_read_b128 a[160:163], v49 offset:0\n" \
"v_mfma_f32_16x16x32_bf16 v[104:107], a[136:139], a[24:27], v[104:107]\n" \
"buffer_load_dwordx4 v50, s[8:11], s64 offen offset:0 sc1 lds\n" \
"v_mfma_f32_16x16x32_bf16 v[104:107], a[140:143], a[28:31], v[104:107]\n" \
"ds_read_b128 a[164:167], v49 offset:1024\n" \
"ds_read_b128 a[168:171], v49 offset:2048\n" \
"v_mfma_f32_16x16x32_bf16 v[108:111], a[156:159], a[28:31], v[36:39]\n" \
"ds_read_b128 a[172:175], v49 offset:3072\n" \
"ds_read_b128 a[176:179], v49 offset:4096\n" \
"v_mfma_f32_16x16x32_bf16 v[108:111], a[152:155], a[24:27], v[108:111]\n" \
"buffer_load_dwordx4 v50, s[8:11], s64 offen offset:1024 sc1 lds\n" \
"v_mfma_f32_16x16x32_bf16 v[108:111], a[148:151], a[20:23], v[108:111]\n" \
"s_mov_b32 s65, s13\n" \
"v_add_u32_e32 v63, s15, v20\n" \
"v_mfma_f32_16x16x32_bf16 v[108:111], a[144:147], a[16:19], v[108:111]\n" \
"s_add_u32 s67, s14, 40960\n" \
"v_add_u32_e32 v50, 0x6000, v63\n" \
"v_mfma_f32_16x16x32_bf16 v[116:119], a[144:147], a[32:35], v[40:43]\n" \
"s_xor_b32 m0, s65, 0x8000\n" \
"ds_read_b128 a[180:183], v49 offset:5120\n" \
"v_mfma_f32_16x16x32_bf16 v[116:119], a[148:151], a[36:39], v[116:119]\n" \
"ds_read_b128 a[184:187], v49 offset:6144\n" \
"v_and_b32_e32 v68, v12, v68\n" \
"v_mfma_f32_16x16x32_bf16 v[116:119], a[152:155], a[40:43], v[116:119]\n" \
"v_and_b32_e32 v69, v13, v69\n" \
"v_and_b32_e32 v70, v14, v70\n" \
"v_mfma_f32_16x16x32_bf16 v[116:119], a[156:159], a[44:47], v[116:119]\n" \
"buffer_load_dwordx4 v50, s[8:11], s67 offen offset:0 sc1 lds\n" \
"v_mfma_f32_16x16x32_bf16 v[112:115], a[140:143], a[44:47], v[40:43]\n" \
"v_and_b32_e32 v71, v15, v71\n" \
"ds_read_b128 a[188:191], v49 offset:7168\n" \
"v_mfma_f32_16x16x32_bf16 v[112:115], a[136:139], a[40:43], v[112:115]\n" \
"v_exp_f32_e32 v86, v86\n" \
"v_mfma_f32_16x16x32_bf16 v[112:115], a[132:135], a[36:39], v[112:115]\n" \
"v_exp_f32_e32 v87, v87\n" \
"v_mfma_f32_16x16x32_bf16 v[112:115], a[128:131], a[32:35], v[112:115]\n" \
"v_exp_f32_e32 v84, v84\n" \
"v_mfma_f32_16x16x32_bf16 v[120:123], a[128:131], a[48:51], v[44:47]\n" \
"buffer_load_dwordx4 v50, s[8:11], s67 offen offset:1024 sc1 lds\n" \
"v_mfma_f32_16x16x32_bf16 v[120:123], a[132:135], a[52:55], v[120:123]\n" \
"v_exp_f32_e32 v85, v85\n" \
"v_mfma_f32_16x16x32_bf16 v[120:123], a[136:139], a[56:59], v[120:123]\n" \
"v_cvt_pk_bf16_f32 v87, v86, v87\n" \
"v_cvt_pk_bf16_f32 v86, v84, v85\n" \
"v_mfma_f32_16x16x32_bf16 v[120:123], a[140:143], a[60:63], v[120:123]\n" \
"v_exp_f32_e32 v82, v82\n" \
"v_mfma_f32_16x16x32_bf16 v[124:127], a[156:159], a[60:63], v[44:47]\n" \
"v_exp_f32_e32 v83, v83\n" \
"v_mfma_f32_16x16x32_bf16 v[124:127], a[152:155], a[56:59], v[124:127]\n" \
"v_exp_f32_e32 v80, v80\n" \
"v_mfma_f32_16x16x32_bf16 v[124:127], a[148:151], a[52:55], v[124:127]\n" \
"buffer_load_dwordx4 v50, s[8:11], s67 offen offset:2048 sc1 lds\n" \
"v_mfma_f32_16x16x32_bf16 v[124:127], a[144:147], a[48:51], v[124:127]\n" \
"v_cvt_pk_bf16_f32 v85, v82, v83\n" \
"s_waitcnt lgkmcnt(1)\n" \
"v_mfma_f32_16x16x32_bf16 v[128:131], a[160:163], v[68:71], v[128:131]\n" \
"v_exp_f32_e32 v81, v81\n" \
"v_mfma_f32_16x16x32_bf16 v[160:163], a[160:163], v[72:75], v[160:163]\n" \
"v_cvt_pk_bf16_f32 v84, v80, v81\n" \
"s_mov_b32 s64, s14\n" \
"v_mfma_f32_16x16x32_bf16 v[132:135], a[164:167], v[68:71], v[132:135]\n" \
"v_exp_f32_e32 v88, v88\n" \
"v_mfma_f32_16x16x32_bf16 v[164:167], a[164:167], v[72:75], v[164:167]\n" \
"v_exp_f32_e32 v89, v89\n" \
"v_mfma_f32_16x16x32_bf16 v[136:139], a[168:171], v[68:71], v[136:139]\n" \
"buffer_load_dwordx4 v50, s[8:11], s67 offen offset:3072 sc1 lds\n" \
"v_mfma_f32_16x16x32_bf16 v[168:171], a[168:171], v[72:75], v[168:171]\n" \
"v_exp_f32_e32 v90, v90\n" \
"v_mfma_f32_16x16x32_bf16 v[140:143], a[172:175], v[68:71], v[140:143]\n" \
"v_exp_f32_e32 v91, v91\n" \
"v_mfma_f32_16x16x32_bf16 v[172:175], a[172:175], v[72:75], v[172:175]\n" \
"v_cvt_pk_bf16_f32 v88, v88, v89\n" \
"v_cvt_pk_bf16_f32 v89, v90, v91\n" \
"v_mfma_f32_16x16x32_bf16 v[144:147], a[176:179], v[68:71], v[144:147]\n" \
"v_exp_f32_e32 v92, v92\n" \
"v_mfma_f32_16x16x32_bf16 v[176:179], a[176:179], v[72:75], v[176:179]\n" \
"v_exp_f32_e32 v93, v93\n" \
"v_mfma_f32_16x16x32_bf16 v[148:151], a[180:183], v[68:71], v[148:151]\n" \
"v_exp_f32_e32 v94, v94\n" \
"v_mfma_f32_16x16x32_bf16 v[180:183], a[180:183], v[72:75], v[180:183]\n" \
"v_exp_f32_e32 v95, v95\n" \
"v_mfma_f32_16x16x32_bf16 v[152:155], a[184:187], v[68:71], v[152:155]\n" \
"v_cvt_pk_bf16_f32 v90, v92, v93\n" \
"v_cvt_pk_bf16_f32 v91, v94, v95\n" \
"v_mfma_f32_16x16x32_bf16 v[184:187], a[184:187], v[72:75], v[184:187]\n" \
"v_add_u32_e32 v51, 0x12000, v63\n" \
"s_waitcnt lgkmcnt(0)\n" \
"v_mfma_f32_16x16x32_bf16 v[156:159], a[188:191], v[68:71], v[156:159]\n" \
"s_xor_b32 m0, s65, 0xa000\n" \
"ds_read_b32 v58, v18 offset:512\n" \
"v_mfma_f32_16x16x32_bf16 v[188:191], a[188:191], v[72:75], v[188:191]\n" \
"buffer_load_dwordx4 v51, s[8:11], s64 offen offset:0 sc1 lds\n" \
"v_smfmac_f32_16x16x64_bf16 a[192:195], a[200:203], v[68:75], v21\n" \
"buffer_load_dwordx4 v51, s[8:11], s64 offen offset:1024 sc1 lds\n" \
"v_mfma_f32_16x16x32_bf16 v[220:223], a[188:191], v[84:87], v[220:223]\n" \
"ds_read_b32 v59, v18 offset:640\n" \
"ds_read_b32 v60, v18 offset:768\n" \
"v_mfma_f32_16x16x32_bf16 v[252:255], a[188:191], v[88:91], v[252:255]\n" \
"ds_read_b32 v61, v18 offset:896\n" \
"ds_read_b128 a[128:131], v48 offset:0\n" \
"v_mfma_f32_16x16x32_bf16 v[216:219], a[184:187], v[84:87], v[216:219]\n" \
"ds_read_b128 a[144:147], v48 offset:4096\n" \
"ds_read_b128 a[132:135], v48 offset:1024\n" \
"v_mfma_f32_16x16x32_bf16 v[248:251], a[184:187], v[88:91], v[248:251]\n" \
"ds_read_b128 a[148:151], v48 offset:5120\n" \
"ds_read_b128 a[136:139], v48 offset:2048\n" \
"v_mfma_f32_16x16x32_bf16 v[212:215], a[180:183], v[84:87], v[212:215]\n" \
"ds_read_b128 a[152:155], v48 offset:6144\n" \
"ds_read_b128 a[140:143], v48 offset:3072\n" \
"v_mfma_f32_16x16x32_bf16 v[244:247], a[180:183], v[88:91], v[244:247]\n" \
"ds_read_b128 a[156:159], v48 offset:7168\n" \
"v_add_u32_e32 v52, 0x14000, v63\n" \
"v_mfma_f32_16x16x32_bf16 v[208:211], a[176:179], v[84:87], v[208:211]\n" \
"buffer_load_dwordx4 v51, s[8:11], s64 offen offset:2048 sc1 lds\n" \
"v_mfma_f32_16x16x32_bf16 v[240:243], a[176:179], v[88:91], v[240:243]\n" \
"v_exp_f32_e32 v104, v104\n" \
"v_mfma_f32_16x16x32_bf16 v[204:207], a[172:175], v[84:87], v[204:207]\n" \
"v_exp_f32_e32 v105, v105\n" \
"v_mfma_f32_16x16x32_bf16 v[236:239], a[172:175], v[88:91], v[236:239]\n" \
"buffer_load_dwordx4 v51, s[8:11], s64 offen offset:3072 sc1 lds\n" \
"v_mfma_f32_16x16x32_bf16 v[200:203], a[168:171], v[84:87], v[200:203]\n" \
"v_exp_f32_e32 v106, v106\n" \
"v_mfma_f32_16x16x32_bf16 v[232:235], a[168:171], v[88:91], v[232:235]\n" \
"v_exp_f32_e32 v107, v107\n" \
"v_mfma_f32_16x16x32_bf16 v[196:199], a[164:167], v[84:87], v[196:199]\n" \
"v_cvt_pk_bf16_f32 v104, v104, v105\n" \
"v_cvt_pk_bf16_f32 v105, v106, v107\n" \
"v_mfma_f32_16x16x32_bf16 v[228:231], a[164:167], v[88:91], v[228:231]\n" \
"v_exp_f32_e32 v108, v108\n" \
"v_mfma_f32_16x16x32_bf16 v[192:195], a[160:163], v[84:87], v[192:195]\n" \
"v_exp_f32_e32 v109, v109\n" \
"v_mfma_f32_16x16x32_bf16 v[224:227], a[160:163], v[88:91], v[224:227]\n" \
"v_exp_f32_e32 v110, v110\n" \
"v_smfmac_f32_16x16x64_bf16 a[196:199], a[200:203], v[84:91], v21\n" \
"v_exp_f32_e32 v111, v111\n" \
"v_cvt_pk_bf16_f32 v106, v108, v109\n" \
"v_cvt_pk_bf16_f32 v107, v110, v111\n" \
"v_and_b32_e32 v104, v12, v104\n" \
"v_and_b32_e32 v105, v13, v105\n" \
"v_and_b32_e32 v106, v14, v106\n" \
"v_and_b32_e32 v107, v15, v107\n" \
"v_accvgpr_read_b32 v54, a192\n" \
"v_accvgpr_read_b32 v22, a193\n" \
"v_add_u32_e32 v53, 0x20000, v63\n" \
"v_exp_f32_e32 v118, v118\n" \
"v_exp_f32_e32 v119, v119\n" \
"v_exp_f32_e32 v116, v116\n" \
"v_exp_f32_e32 v117, v117\n" \
"v_cvt_pk_bf16_f32 v119, v118, v119\n" \
"v_exp_f32_e32 v114, v114\n" \
"v_exp_f32_e32 v115, v115\n" \
"v_exp_f32_e32 v112, v112\n" \
"v_exp_f32_e32 v113, v113\n" \
"v_cvt_pk_bf16_f32 v118, v116, v117\n" \
"v_cvt_pk_bf16_f32 v117, v114, v115\n" \
"v_cvt_pk_bf16_f32 v116, v112, v113\n" \
"v_readlane_b32 s93, v11, 19\n" \
"s_waitcnt vmcnt(10) lgkmcnt(0)\n" \
"s_barrier\n" \
"v_mfma_f32_16x16x32_bf16 v[80:83], a[128:131], a[32:35], v[40:43]\n" \
"ds_read_b128 a[160:163], v49 offset:8192\n" \
"ds_read_b128 a[164:167], v49 offset:9216\n" \
"v_mfma_f32_16x16x32_bf16 v[80:83], a[132:135], a[36:39], v[80:83]\n" \
"ds_read_b128 a[168:171], v49 offset:10240\n" \
"s_xor_b32 m0, s65, 0x18000\n" \
"v_mfma_f32_16x16x32_bf16 v[80:83], a[136:139], a[40:43], v[80:83]\n" \
"buffer_load_dwordx4 v52, s[8:11], s64 offen offset:0 sc1 lds\n" \
"v_mfma_f32_16x16x32_bf16 v[80:83], a[140:143], a[44:47], v[80:83]\n" \
"ds_read_b128 a[172:175], v49 offset:11264\n" \
"ds_read_b128 a[176:179], v49 offset:12288\n" \
"v_mfma_f32_16x16x32_bf16 v[84:87], a[156:159], a[44:47], v[40:43]\n" \
"ds_read_b128 a[180:183], v49 offset:13312\n" \
"ds_read_b128 a[184:187], v49 offset:14336\n" \
"v_mfma_f32_16x16x32_bf16 v[84:87], a[152:155], a[40:43], v[84:87]\n" \
"buffer_load_dwordx4 v52, s[8:11], s64 offen offset:1024 sc1 lds\n" \
"v_mfma_f32_16x16x32_bf16 v[84:87], a[148:151], a[36:39], v[84:87]\n" \
"ds_read_b128 a[188:191], v49 offset:15360\n" \
"v_readlane_b32 s80, v11, 11\n" \
"v_mfma_f32_16x16x32_bf16 v[84:87], a[144:147], a[32:35], v[84:87]\n" \
"buffer_load_dwordx4 v52, s[8:11], s64 offen offset:2048 sc1 lds\n" \
"v_mfma_f32_16x16x32_bf16 v[92:95], a[144:147], a[48:51], v[44:47]\n" \
"v_exp_f32_e32 v120, v120\n" \
"v_mfma_f32_16x16x32_bf16 v[92:95], a[148:151], a[52:55], v[92:95]\n" \
"v_exp_f32_e32 v121, v121\n" \
"v_mfma_f32_16x16x32_bf16 v[92:95], a[152:155], a[56:59], v[92:95]\n" \
"buffer_load_dwordx4 v52, s[8:11], s64 offen offset:3072 sc1 lds\n" \
"v_mfma_f32_16x16x32_bf16 v[92:95], a[156:159], a[60:63], v[92:95]\n" \
"s_mov_b32 m0, s65\n" \
"v_cvt_pk_bf16_f32 v120, v120, v121\n" \
"v_mfma_f32_16x16x32_bf16 v[88:91], a[140:143], a[60:63], v[44:47]\n" \
"v_exp_f32_e32 v122, v122\n" \
"v_mfma_f32_16x16x32_bf16 v[88:91], a[136:139], a[56:59], v[88:91]\n" \
"buffer_load_dwordx4 v53, s[8:11], s64 offen offset:0 sc1 lds\n" \
"v_mfma_f32_16x16x32_bf16 v[88:91], a[132:135], a[52:55], v[88:91]\n" \
"v_exp_f32_e32 v123, v123\n" \
"v_mfma_f32_16x16x32_bf16 v[88:91], a[128:131], a[48:51], v[88:91]\n" \
"v_exp_f32_e32 v124, v124\n" \
"v_mfma_f32_16x16x32_bf16 a[192:195], a[212:215], v[104:107], a[192:195]\n" \
"v_cvt_pk_bf16_f32 v121, v122, v123\n" \
"s_waitcnt lgkmcnt(5)\n" \
"v_mfma_f32_16x16x32_bf16 v[160:163], a[160:163], v[104:107], v[160:163]\n" \
"buffer_load_dwordx4 v53, s[8:11], s64 offen offset:1024 sc1 lds\n" \
"v_mfma_f32_16x16x32_bf16 v[164:167], a[164:167], v[104:107], v[164:167]\n" \
"v_exp_f32_e32 v125, v125\n" \
"v_mfma_f32_16x16x32_bf16 v[168:171], a[168:171], v[104:107], v[168:171]\n" \
"v_cvt_pk_bf16_f32 v122, v124, v125\n" \
"s_waitcnt lgkmcnt(1)\n" \
"v_mfma_f32_16x16x32_bf16 v[172:175], a[172:175], v[104:107], v[172:175]\n" \
"v_exp_f32_e32 v126, v126\n" \
"v_mfma_f32_16x16x32_bf16 v[176:179], a[176:179], v[104:107], v[176:179]\n" \
"v_exp_f32_e32 v127, v127\n" \
"v_mfma_f32_16x16x32_bf16 v[180:183], a[180:183], v[104:107], v[180:183]\n" \
"v_cvt_pk_bf16_f32 v123, v126, v127\n" \
"v_readlane_b32 s81, v11, 12\n" \
"v_mfma_f32_16x16x32_bf16 v[184:187], a[184:187], v[104:107], v[184:187]\n" \
"s_mov_b32 s82, 0x47fca0\n" \
"s_waitcnt lgkmcnt(0)\n" \
"v_mfma_f32_16x16x32_bf16 v[188:191], a[188:191], v[104:107], v[188:191]\n" \
"buffer_load_dwordx4 v53, s[8:11], s64 offen offset:2048 sc1 lds\n" \
"v_mfma_f32_16x16x32_bf16 v[220:223], a[188:191], v[116:119], v[220:223]\n" \
"buffer_load_dwordx4 v53, s[8:11], s64 offen offset:3072 sc1 lds\n" \
"v_mfma_f32_16x16x32_bf16 v[252:255], a[188:191], v[120:123], v[252:255]\n" \
"s_mov_b32 s83, 0x20000\n" \
"v_readlane_b32 s92, v11, 26\n" \
"v_mfma_f32_16x16x32_bf16 v[216:219], a[184:187], v[116:119], v[216:219]\n" \
"s_add_u32 m0, s93, 0x0\n" \
"ds_read_b128 a[128:131], v48 offset:8192\n" \
"v_mfma_f32_16x16x32_bf16 v[248:251], a[184:187], v[120:123], v[248:251]\n" \
"ds_read_b128 a[144:147], v48 offset:12288\n" \
"ds_read_b128 a[132:135], v48 offset:9216\n" \
"v_mfma_f32_16x16x32_bf16 v[212:215], a[180:183], v[116:119], v[212:215]\n" \
"buffer_load_dwordx4 v0, s[80:83], s92 offen offset:0 lds\n" \
"v_mfma_f32_16x16x32_bf16 v[244:247], a[180:183], v[120:123], v[244:247]\n" \
"buffer_load_dwordx4 v1, s[80:83], s92 offen offset:1024 lds\n" \
"v_mfma_f32_16x16x32_bf16 v[208:211], a[176:179], v[116:119], v[208:211]\n" \
"ds_read_b128 a[148:151], v48 offset:13312\n" \
"ds_read_b128 a[136:139], v48 offset:10240\n" \
"v_mfma_f32_16x16x32_bf16 v[240:243], a[176:179], v[120:123], v[240:243]\n" \
"ds_read_b128 a[152:155], v48 offset:14336\n" \
"ds_read_b128 a[140:143], v48 offset:11264\n" \
"v_mfma_f32_16x16x32_bf16 v[204:207], a[172:175], v[116:119], v[204:207]\n" \
"buffer_load_dwordx4 v2, s[80:83], s92 offen offset:2048 lds\n" \
"v_mfma_f32_16x16x32_bf16 v[236:239], a[172:175], v[120:123], v[236:239]\n" \
"ds_read_b128 a[156:159], v48 offset:15360\n" \
"s_xor_b32 s65, s65, 0x8000\n" \
"v_mfma_f32_16x16x32_bf16 v[200:203], a[168:171], v[116:119], v[200:203]\n" \
"s_add_u32 s64, s64, 0x10000\n" \
"v_add_u32_e32 v49, 0x10000, v48\n" \
"v_mfma_f32_16x16x32_bf16 v[232:235], a[168:171], v[120:123], v[232:235]\n" \
"buffer_load_dwordx4 v3, s[80:83], s92 offen offset:3072 lds\n" \
"v_mfma_f32_16x16x32_bf16 v[196:199], a[164:167], v[116:119], v[196:199]\n" \
"v_exp_f32_e32 v86, v86\n" \
"v_mfma_f32_16x16x32_bf16 v[228:231], a[164:167], v[120:123], v[228:231]\n" \
"v_exp_f32_e32 v87, v87\n" \
"v_mfma_f32_16x16x32_bf16 v[192:195], a[160:163], v[116:119], v[192:195]\n" \
"v_exp_f32_e32 v84, v84\n" \
"v_mfma_f32_16x16x32_bf16 v[224:227], a[160:163], v[120:123], v[224:227]\n" \
"v_exp_f32_e32 v85, v85\n" \
"v_smfmac_f32_16x16x64_bf16 a[196:199], a[200:203], v[116:123], v21\n" \
"v_exp_f32_e32 v88, v88\n" \
"v_exp_f32_e32 v89, v89\n" \
"v_cvt_pk_bf16_f32 v87, v86, v87\n" \
"v_exp_f32_e32 v82, v82\n" \
"v_exp_f32_e32 v83, v83\n" \
"v_exp_f32_e32 v80, v80\n" \
"v_exp_f32_e32 v81, v81\n" \
"v_exp_f32_e32 v90, v90\n" \
"v_exp_f32_e32 v91, v91\n" \
"v_cvt_pk_bf16_f32 v86, v84, v85\n" \
"v_cvt_pk_bf16_f32 v88, v88, v89\n" \
"v_exp_f32_e32 v92, v92\n" \
"v_exp_f32_e32 v93, v93\n" \
"v_exp_f32_e32 v94, v94\n" \
"v_exp_f32_e32 v95, v95\n" \
"v_cvt_pk_bf16_f32 v85, v82, v83\n" \
"v_cvt_pk_bf16_f32 v84, v80, v81\n" \
"v_cvt_pk_bf16_f32 v89, v90, v91\n" \
"v_and_b32_e32 v84, v12, v84\n" \
"v_and_b32_e32 v85, v13, v85\n" \
"v_and_b32_e32 v86, v14, v86\n" \
"v_and_b32_e32 v87, v15, v87\n" \
"v_cvt_pk_bf16_f32 v90, v92, v93\n" \
"v_cvt_pk_bf16_f32 v91, v94, v95\n" \
"v_add_u32_e32 v48, s16, v20\n" \
"v_add_f32_e32 v54, v54, v22\n" \
"v_accvgpr_read_b32 v55, a194\n" \
"v_accvgpr_read_b32 v23, a195\n" \
"v_mfma_f32_16x16x32_bf16 a[192:195], a[216:219], a[216:219], 0\n" \
"v_max_f32_e32 v62, v62, v54\n" \
"v_add_f32_e32 v55, v55, v23\n" \
"v_max_f32_e32 v62, v62, v55\n" \
"v_rcp_f32_e32 v54, v54\n" \
"v_rcp_f32_e32 v55, v55\n" \
"v_mul_f32_e32 v54, v54, v58\n" \
"v_mul_f32_e32 v55, v55, v59\n" \
"v_mul_f32_e32 v128, v54, v128\n" \
"v_mul_f32_e32 v129, v54, v129\n" \
"v_mul_f32_e32 v136, v54, v136\n" \
"v_mul_f32_e32 v137, v54, v137\n" \
"v_mul_f32_e32 v144, v54, v144\n" \
"v_mul_f32_e32 v145, v54, v145\n" \
"v_mul_f32_e32 v152, v54, v152\n" \
"v_mul_f32_e32 v153, v54, v153\n" \
"v_mul_f32_e32 v160, v55, v160\n" \
"v_mul_f32_e32 v161, v55, v161\n" \
"v_mul_f32_e32 v168, v55, v168\n" \
"v_mul_f32_e32 v169, v55, v169\n" \
"v_mul_f32_e32 v176, v55, v176\n" \
"v_mul_f32_e32 v177, v55, v177\n" \
"v_mul_f32_e32 v184, v55, v184\n" \
"v_mul_f32_e32 v185, v55, v185\n" \
"v_cvt_pk_bf16_f32 v128, v128, v129\n" \
"v_mul_f32_e32 v130, v54, v130\n" \
"v_mul_f32_e32 v131, v54, v131\n" \
"v_cvt_pk_bf16_f32 v136, v136, v137\n" \
"v_mul_f32_e32 v138, v54, v138\n" \
"v_mul_f32_e32 v139, v54, v139\n" \
"v_cvt_pk_bf16_f32 v144, v144, v145\n" \
"v_mul_f32_e32 v146, v54, v146\n" \
"v_mul_f32_e32 v147, v54, v147\n" \
"v_cvt_pk_bf16_f32 v152, v152, v153\n" \
"v_mul_f32_e32 v154, v54, v154\n" \
"v_mul_f32_e32 v155, v54, v155\n" \
"v_cvt_pk_bf16_f32 v160, v160, v161\n" \
"v_mul_f32_e32 v162, v55, v162\n" \
"v_mul_f32_e32 v163, v55, v163\n" \
"v_cvt_pk_bf16_f32 v168, v168, v169\n" \
"v_mul_f32_e32 v170, v55, v170\n" \
"v_mul_f32_e32 v171, v55, v171\n" \
"v_cvt_pk_bf16_f32 v176, v176, v177\n" \
"v_mul_f32_e32 v178, v55, v178\n" \
"v_mul_f32_e32 v179, v55, v179\n" \
"v_cvt_pk_bf16_f32 v184, v184, v185\n" \
"v_mul_f32_e32 v186, v55, v186\n" \
"v_mul_f32_e32 v187, v55, v187\n" \
"v_cvt_pk_bf16_f32 v129, v130, v131\n" \
"v_mul_f32_e32 v132, v54, v132\n" \
"v_mul_f32_e32 v133, v54, v133\n" \
"v_mul_f32_e32 v134, v54, v134\n" \
"v_mul_f32_e32 v135, v54, v135\n" \
"v_cvt_pk_bf16_f32 v137, v138, v139\n" \
"v_mul_f32_e32 v140, v54, v140\n" \
"s_waitcnt vmcnt(12) lgkmcnt(0)\n" \
"s_barrier\n" \
"v_mfma_f32_16x16x32_bf16 v[120:123], a[128:131], a[48:51], v[44:47]\n" \
"s_xor_b32 m0, s65, 0x12000\n" \
"ds_read_b128 a[188:191], v49 offset:7168\n" \
"v_mfma_f32_16x16x32_bf16 v[120:123], a[132:135], a[52:55], v[120:123]\n" \
"buffer_load_dwordx4 v50, s[8:11], s64 offen offset:0 sc1 lds\n" \
"v_mfma_f32_16x16x32_bf16 v[120:123], a[136:139], a[56:59], v[120:123]\n" \
"ds_read_b128 a[184:187], v49 offset:6144\n" \
"ds_read_b128 a[180:183], v49 offset:5120\n" \
"v_mfma_f32_16x16x32_bf16 v[120:123], a[140:143], a[60:63], v[120:123]\n" \
"buffer_load_dwordx4 v50, s[8:11], s64 offen offset:1024 sc1 lds\n" \
"v_mfma_f32_16x16x32_bf16 v[124:127], a[156:159], a[60:63], v[44:47]\n" \
"ds_read_b128 a[176:179], v49 offset:4096\n" \
"ds_read_b128 a[172:175], v49 offset:3072\n" \
"v_mfma_f32_16x16x32_bf16 v[124:127], a[152:155], a[56:59], v[124:127]\n" \
"ds_read_b128 a[168:171], v49 offset:2048\n" \
"ds_read_b128 a[164:167], v49 offset:1024\n" \
"v_mfma_f32_16x16x32_bf16 v[124:127], a[148:151], a[52:55], v[124:127]\n" \
"ds_read_b128 a[160:163], v49 offset:0\n" \
"ds_read_b128 a[0:3], v49 offset:8192\n" \
"v_mfma_f32_16x16x32_bf16 v[124:127], a[144:147], a[48:51], v[124:127]\n" \
"buffer_load_dwordx4 v50, s[8:11], s64 offen offset:2048 sc1 lds\n" \
"v_mfma_f32_16x16x32_bf16 v[32:35], a[220:223], a[64:67], 0\n" \
"ds_read_b128 a[4:7], v49 offset:9216\n" \
"ds_read_b128 a[8:11], v49 offset:10240\n" \
"v_mfma_f32_16x16x32_bf16 v[36:39], a[220:223], a[80:83], 0\n" \
"ds_read_b128 a[12:15], v49 offset:11264\n" \
"ds_read_b128 a[16:19], v49 offset:12288\n" \
"v_mfma_f32_16x16x32_bf16 v[40:43], a[220:223], a[96:99], 0\n" \
"ds_read_b128 a[20:23], v49 offset:13312\n" \
"ds_read_b128 a[24:27], v49 offset:14336\n" \
"v_mfma_f32_16x16x32_bf16 v[44:47], a[220:223], a[112:115], 0\n" \
"buffer_load_dwordx4 v50, s[8:11], s64 offen offset:3072 sc1 lds\n" \
"v_mfma_f32_16x16x32_bf16 v[32:35], a[224:227], a[68:71], v[32:35]\n" \
"s_xor_b32 m0, s65, 0xa000\n" \
"ds_read_b128 a[28:31], v49 offset:15360\n" \
"v_mfma_f32_16x16x32_bf16 v[36:39], a[224:227], a[84:87], v[36:39]\n" \
"v_exp_f32_e32 v120, v120\n" \
"v_mfma_f32_16x16x32_bf16 v[40:43], a[224:227], a[100:103], v[40:43]\n" \
"buffer_load_dwordx4 v51, s[8:11], s64 offen offset:0 sc1 lds\n" \
"v_mfma_f32_16x16x32_bf16 v[44:47], a[224:227], a[116:119], v[44:47]\n" \
"v_exp_f32_e32 v121, v121\n" \
"v_mfma_f32_16x16x32_bf16 v[32:35], a[228:231], a[72:75], v[32:35]\n" \
"v_exp_f32_e32 v122, v122\n" \
"v_mfma_f32_16x16x32_bf16 v[36:39], a[228:231], a[88:91], v[36:39]\n" \
"v_exp_f32_e32 v123, v123\n" \
"v_mfma_f32_16x16x32_bf16 v[40:43], a[228:231], a[104:107], v[40:43]\n" \
"buffer_load_dwordx4 v51, s[8:11], s64 offen offset:1024 sc1 lds\n" \
"v_mfma_f32_16x16x32_bf16 v[44:47], a[228:231], a[120:123], v[44:47]\n" \
"v_cvt_pk_bf16_f32 v120, v120, v121\n" \
"v_cvt_pk_bf16_f32 v121, v122, v123\n" \
"v_mfma_f32_16x16x32_bf16 v[32:35], a[232:235], a[76:79], v[32:35]\n" \
"v_exp_f32_e32 v124, v124\n" \
"v_mfma_f32_16x16x32_bf16 v[36:39], a[232:235], a[92:95], v[36:39]\n" \
"v_exp_f32_e32 v125, v125\n" \
"v_mfma_f32_16x16x32_bf16 v[40:43], a[232:235], a[108:111], v[40:43]\n" \
"buffer_load_dwordx4 v51, s[8:11], s64 offen offset:2048 sc1 lds\n" \
"v_mfma_f32_16x16x32_bf16 v[44:47], a[232:235], a[124:127], v[44:47]\n" \
"v_exp_f32_e32 v126, v126\n" \
"v_smfmac_f32_16x16x64_bf16 a[196:199], a[200:203], v[84:91], v21\n" \
"v_cvt_pk_bf16_f32 v122, v124, v125\n" \
"s_waitcnt lgkmcnt(1)\n" \
"v_mfma_f32_16x16x32_bf16 v[220:223], a[188:191], v[84:87], v[220:223]\n" \
"buffer_load_dwordx4 v51, s[8:11], s64 offen offset:3072 sc1 lds\n" \
"v_mfma_f32_16x16x32_bf16 v[252:255], a[188:191], v[88:91], v[252:255]\n" \
"s_add_u32 m0, s93, 0x1000\n" \
"v_and_b32_e32 v120, v12, v120\n" \
"v_mfma_f32_16x16x32_bf16 v[216:219], a[184:187], v[84:87], v[216:219]\n" \
"v_exp_f32_e32 v127, v127\n" \
"v_mfma_f32_16x16x32_bf16 v[248:251], a[184:187], v[88:91], v[248:251]\n" \
"v_cvt_pk_bf16_f32 v123, v126, v127\n" \
"v_and_b32_e32 v121, v13, v121\n" \
"v_mfma_f32_16x16x32_bf16 v[212:215], a[180:183], v[84:87], v[212:215]\n" \
"buffer_load_dwordx4 v4, s[80:83], s92 offen offset:0 lds\n" \
"v_mfma_f32_16x16x32_bf16 v[244:247], a[180:183], v[88:91], v[244:247]\n" \
"v_and_b32_e32 v122, v14, v122\n" \
"v_and_b32_e32 v123, v15, v123\n" \
"v_mfma_f32_16x16x32_bf16 v[208:211], a[176:179], v[84:87], v[208:211]\n" \
"v_accvgpr_read_b32 v56, a196\n" \
"v_accvgpr_read_b32 v24, a197\n" \
"v_mfma_f32_16x16x32_bf16 v[240:243], a[176:179], v[88:91], v[240:243]\n" \
"ds_read_b128 a[128:131], v48 offset:0\n" \
"ds_read_b128 a[144:147], v48 offset:4096\n" \
"v_mfma_f32_16x16x32_bf16 v[204:207], a[172:175], v[84:87], v[204:207]\n" \
"buffer_load_dwordx4 v5, s[80:83], s92 offen offset:1024 lds\n" \
"v_mfma_f32_16x16x32_bf16 v[236:239], a[172:175], v[88:91], v[236:239]\n" \
"ds_read_b128 a[132:135], v48 offset:1024\n" \
"ds_read_b128 a[148:151], v48 offset:5120\n" \
"v_mfma_f32_16x16x32_bf16 v[200:203], a[168:171], v[84:87], v[200:203]\n" \
"ds_read_b128 a[136:139], v48 offset:2048\n" \
"ds_read_b128 a[152:155], v48 offset:6144\n" \
"v_mfma_f32_16x16x32_bf16 v[232:235], a[168:171], v[88:91], v[232:235]\n" \
"buffer_load_dwordx4 v6, s[80:83], s92 offen offset:2048 lds\n" \
"v_mfma_f32_16x16x32_bf16 v[196:199], a[164:167], v[84:87], v[196:199]\n" \
"ds_read_b128 a[140:143], v48 offset:3072\n" \
"ds_read_b128 a[156:159], v48 offset:7168\n" \
"v_mfma_f32_16x16x32_bf16 v[228:231], a[164:167], v[88:91], v[228:231]\n" \
"v_mul_f32_e32 v141, v54, v141\n" \
"v_mul_f32_e32 v142, v54, v142\n" \
"v_mfma_f32_16x16x32_bf16 v[192:195], a[160:163], v[84:87], v[192:195]\n" \
"v_mul_f32_e32 v143, v54, v143\n" \
"v_cvt_pk_bf16_f32 v145, v146, v147\n" \
"v_mfma_f32_16x16x32_bf16 v[224:227], a[160:163], v[88:91], v[224:227]\n" \
"buffer_load_dwordx4 v7, s[80:83], s92 offen offset:3072 lds\n" \
"v_mfma_f32_16x16x32_bf16 a[196:199], a[212:215], v[120:123], a[196:199]\n" \
"v_mul_f32_e32 v148, v54, v148\n" \
"v_mul_f32_e32 v149, v54, v149\n" \
"v_mfma_f32_16x16x32_bf16 v[224:227], a[0:3], v[120:123], v[224:227]\n" \
"v_mul_f32_e32 v150, v54, v150\n" \
"v_mul_f32_e32 v151, v54, v151\n" \
"v_mfma_f32_16x16x32_bf16 v[228:231], a[4:7], v[120:123], v[228:231]\n" \
"v_cvt_pk_bf16_f32 v153, v154, v155\n" \
"v_mul_f32_e32 v156, v54, v156\n" \
"v_mfma_f32_16x16x32_bf16 v[232:235], a[8:11], v[120:123], v[232:235]\n" \
"v_mul_f32_e32 v157, v54, v157\n" \
"v_mul_f32_e32 v158, v54, v158\n" \
"v_mfma_f32_16x16x32_bf16 v[236:239], a[12:15], v[120:123], v[236:239]\n" \
"v_mul_f32_e32 v159, v54, v159\n" \
"v_cvt_pk_bf16_f32 v161, v162, v163\n" \
"v_mfma_f32_16x16x32_bf16 v[240:243], a[16:19], v[120:123], v[240:243]\n" \
"v_mul_f32_e32 v164, v55, v164\n" \
"v_mul_f32_e32 v165, v55, v165\n" \
"v_mfma_f32_16x16x32_bf16 v[244:247], a[20:23], v[120:123], v[244:247]\n" \
"v_mul_f32_e32 v166, v55, v166\n" \
"v_mul_f32_e32 v167, v55, v167\n" \
"v_mfma_f32_16x16x32_bf16 v[248:251], a[24:27], v[120:123], v[248:251]\n" \
"v_cvt_pk_bf16_f32 v169, v170, v171\n" \
"s_waitcnt lgkmcnt(4)\n" \
"v_mfma_f32_16x16x32_bf16 v[252:255], a[28:31], v[120:123], v[252:255]\n" \
"v_mul_f32_e32 v172, v55, v172\n" \
"v_mul_f32_e32 v173, v55, v173\n" \
"v_mul_f32_e32 v174, v55, v174\n" \
"v_mul_f32_e32 v175, v55, v175\n" \
"v_cvt_pk_bf16_f32 v177, v178, v179\n" \
"v_mul_f32_e32 v180, v55, v180\n" \
"v_mul_f32_e32 v181, v55, v181\n" \
"v_mul_f32_e32 v182, v55, v182\n" \
"v_mul_f32_e32 v183, v55, v183\n" \
"v_cvt_pk_bf16_f32 v185, v186, v187\n" \
"v_mul_f32_e32 v188, v55, v188\n" \
"v_mul_f32_e32 v189, v55, v189\n" \
"v_mul_f32_e32 v190, v55, v190\n" \
"v_mul_f32_e32 v191, v55, v191\n" \
"v_cvt_pk_bf16_f32 v130, v132, v133\n" \
"v_cvt_pk_bf16_f32 v131, v134, v135\n" \
"buffer_store_dwordx4 v[128:131], v20, s[56:59], s68 offen offset:0 sc1\n" \
"v_cvt_pk_bf16_f32 v138, v140, v141\n" \
"v_cvt_pk_bf16_f32 v139, v142, v143\n" \
"buffer_store_dwordx4 v[136:139], v20, s[56:59], s68 offen offset:1024 sc1\n" \
"v_cvt_pk_bf16_f32 v146, v148, v149\n" \
"v_cvt_pk_bf16_f32 v147, v150, v151\n" \
"buffer_store_dwordx4 v[144:147], v20, s[56:59], s68 offen offset:2048 sc1\n" \
"v_cvt_pk_bf16_f32 v154, v156, v157\n" \
"v_cvt_pk_bf16_f32 v155, v158, v159\n" \
"buffer_store_dwordx4 v[152:155], v20, s[56:59], s68 offen offset:3072 sc1\n" \
"v_cvt_pk_bf16_f32 v162, v164, v165\n" \
"v_cvt_pk_bf16_f32 v163, v166, v167\n" \
"buffer_store_dwordx4 v[160:163], v20, s[56:59], s69 offen offset:0 sc1\n" \
"v_cvt_pk_bf16_f32 v170, v172, v173\n" \
"v_cvt_pk_bf16_f32 v171, v174, v175\n" \
"buffer_store_dwordx4 v[168:171], v20, s[56:59], s69 offen offset:1024 sc1\n" \
"v_cvt_pk_bf16_f32 v178, v180, v181\n" \
"v_cvt_pk_bf16_f32 v179, v182, v183\n" \
"buffer_store_dwordx4 v[176:179], v20, s[56:59], s69 offen offset:2048 sc1\n" \
"v_cvt_pk_bf16_f32 v186, v188, v189\n" \
"v_cvt_pk_bf16_f32 v187, v190, v191\n" \
"buffer_store_dwordx4 v[184:187], v20, s[56:59], s69 offen offset:3072 sc1\n" \
"s_add_u32 m0, s93, 0x2000\n" \
"v_mfma_f32_16x16x32_bf16 a[208:211], a[216:219], v[128:131], a[208:211]\n" \
"buffer_load_dwordx4 v8, s[80:83], s92 offen offset:0 lds\n" \
"v_mfma_f32_16x16x32_bf16 a[208:211], a[216:219], v[136:139], a[208:211]\n" \
"v_mfma_f32_32x32x16_bf16 v[128:143], a[216:219], a[216:219], 0\n" \
"v_mfma_f32_16x16x32_bf16 a[208:211], a[216:219], v[144:147], a[208:211]\n" \
"v_mfma_f32_16x16x32_bf16 a[208:211], a[216:219], v[152:155], a[208:211]\n" \
"v_mfma_f32_32x32x16_bf16 v[144:159], a[216:219], a[216:219], 0\n" \
"v_add_f32_e32 v56, v56, v24\n" \
"v_mfma_f32_16x16x32_bf16 a[208:211], a[216:219], v[160:163], a[208:211]\n" \
"v_max_f32_e32 v62, v62, v56\n" \
"v_accvgpr_read_b32 v57, a198\n" \
"s_waitcnt vmcnt(21) lgkmcnt(0)\n" \
"s_barrier\n" \
"v_mfma_f32_16x16x32_bf16 v[64:67], a[128:131], a[64:67], v[32:35]\n" \
"s_xor_b32 m0, s65, 0x18000\n" \
"v_accvgpr_read_b32 v25, a199\n" \
"v_mfma_f32_16x16x32_bf16 a[196:199], a[216:219], a[216:219], 0\n" \
"buffer_load_dwordx4 v52, s[8:11], s64 offen offset:0 sc1 lds\n" \
"v_mfma_f32_16x16x32_bf16 v[64:67], a[132:135], a[68:71], v[64:67]\n" \
"v_rcp_f32_e32 v56, v56\n" \
"v_mfma_f32_16x16x32_bf16 v[64:67], a[136:139], a[72:75], v[64:67]\n" \
"v_mfma_f32_16x16x32_bf16 a[208:211], a[216:219], v[168:171], a[208:211]\n" \
"v_mfma_f32_32x32x16_bf16 v[160:175], a[216:219], a[216:219], 0\n" \
"v_mul_f32_e32 v56, v56, v60\n" \
"v_mfma_f32_16x16x32_bf16 a[208:211], a[216:219], v[176:179], a[208:211]\n" \
"v_mul_f32_e32 v192, v56, v192\n" \
"v_mul_f32_e32 v193, v56, v193\n" \
"v_mfma_f32_16x16x32_bf16 v[64:67], a[140:143], a[76:79], v[64:67]\n" \
"buffer_load_dwordx4 v52, s[8:11], s64 offen offset:1024 sc1 lds\n" \
"v_mfma_f32_16x16x32_bf16 v[68:71], a[156:159], a[76:79], v[32:35]\n" \
"v_cvt_pk_bf16_f32 v192, v192, v193\n" \
"v_mul_f32_e32 v194, v56, v194\n" \
"v_mfma_f32_16x16x32_bf16 v[68:71], a[152:155], a[72:75], v[68:71]\n" \
"v_mul_f32_e32 v195, v56, v195\n" \
"v_cvt_pk_bf16_f32 v193, v194, v195\n" \
"v_mfma_f32_16x16x32_bf16 v[68:71], a[148:151], a[68:71], v[68:71]\n" \
"v_mfma_f32_16x16x32_bf16 a[208:211], a[216:219], v[184:187], a[208:211]\n" \
"v_mfma_f32_32x32x16_bf16 v[176:191], a[216:219], a[216:219], 0\n" \
"v_mul_f32_e32 v196, v56, v196\n" \
"v_mul_f32_e32 v197, v56, v197\n" \
"v_mul_f32_e32 v198, v56, v198\n" \
"v_mul_f32_e32 v199, v56, v199\n" \
"v_mul_f32_e32 v200, v56, v200\n" \
"v_mul_f32_e32 v201, v56, v201\n" \
"v_mfma_f32_16x16x32_bf16 v[68:71], a[144:147], a[64:67], v[68:71]\n" \
"buffer_load_dwordx4 v52, s[8:11], s64 offen offset:2048 sc1 lds\n" \
"v_mfma_f32_16x16x32_bf16 v[76:79], a[144:147], a[80:83], v[36:39]\n" \
"v_cvt_pk_bf16_f32 v194, v196, v197\n" \
"v_cvt_pk_bf16_f32 v195, v198, v199\n" \
"buffer_store_dwordx4 v[192:195], v20, s[56:59], s70 offen offset:0 sc1\n" \
"v_mfma_f32_16x16x32_bf16 v[76:79], a[148:151], a[84:87], v[76:79]\n" \
"v_cvt_pk_bf16_f32 v200, v200, v201\n" \
"v_mul_f32_e32 v202, v56, v202\n" \
"v_mfma_f32_16x16x32_bf16 v[76:79], a[152:155], a[88:91], v[76:79]\n" \
"v_mul_f32_e32 v203, v56, v203\n" \
"v_add_f32_e32 v57, v57, v25\n" \
"v_mfma_f32_16x16x32_bf16 v[76:79], a[156:159], a[92:95], v[76:79]\n" \
"buffer_load_dwordx4 v52, s[8:11], s64 offen offset:3072 sc1 lds\n" \
"v_mfma_f32_16x16x32_bf16 v[72:75], a[140:143], a[92:95], v[36:39]\n" \
"s_mov_b32 m0, s65\n" \
"v_cvt_pk_bf16_f32 v201, v202, v203\n" \
"v_mfma_f32_16x16x32_bf16 v[72:75], a[136:139], a[88:91], v[72:75]\n" \
"v_mfma_f32_16x16x32_bf16 a[208:211], a[216:219], v[192:195], a[208:211]\n" \
"v_mul_f32_e32 v204, v56, v204\n" \
"v_mul_f32_e32 v205, v56, v205\n" \
"v_mfma_f32_16x16x32_bf16 v[72:75], a[132:135], a[84:87], v[72:75]\n" \
"v_mul_f32_e32 v206, v56, v206\n" \
"v_mul_f32_e32 v207, v56, v207\n" \
"v_mfma_f32_16x16x32_bf16 v[72:75], a[128:131], a[80:83], v[72:75]\n" \
"buffer_load_dwordx4 v53, s[8:11], s64 offen offset:0 sc1 lds\n" \
"v_mfma_f32_16x16x32_bf16 v[80:83], a[128:131], a[96:99], v[40:43]\n" \
"v_mul_f32_e32 v208, v56, v208\n" \
"v_mul_f32_e32 v209, v56, v209\n" \
"v_mfma_f32_16x16x32_bf16 v[80:83], a[132:135], a[100:103], v[80:83]\n" \
"v_max_f32_e32 v62, v62, v57\n" \
"v_cvt_pk_bf16_f32 v202, v204, v205\n" \
"v_mfma_f32_16x16x32_bf16 v[80:83], a[136:139], a[104:107], v[80:83]\n" \
"buffer_load_dwordx4 v53, s[8:11], s64 offen offset:1024 sc1 lds\n" \
"v_mfma_f32_16x16x32_bf16 v[80:83], a[140:143], a[108:111], v[80:83]\n" \
"v_cvt_pk_bf16_f32 v203, v206, v207\n" \
"v_cvt_pk_bf16_f32 v208, v208, v209\n" \
"buffer_store_dwordx4 v[200:203], v20, s[56:59], s70 offen offset:1024 sc1\n" \
"v_mfma_f32_16x16x32_bf16 v[84:87], a[156:159], a[108:111], v[40:43]\n" \
"v_mul_f32_e32 v210, v56, v210\n" \
"v_mul_f32_e32 v211, v56, v211\n" \
"v_mfma_f32_16x16x32_bf16 v[84:87], a[152:155], a[104:107], v[84:87]\n" \
"v_rcp_f32_e32 v57, v57\n" \
"v_mfma_f32_16x16x32_bf16 v[84:87], a[148:151], a[100:103], v[84:87]\n" \
"buffer_load_dwordx4 v53, s[8:11], s64 offen offset:2048 sc1 lds\n" \
"v_mfma_f32_16x16x32_bf16 v[84:87], a[144:147], a[96:99], v[84:87]\n" \
"v_mfma_f32_16x16x32_bf16 a[208:211], a[216:219], v[200:203], a[208:211]\n" \
"v_mfma_f32_32x32x16_bf16 v[192:207], a[216:219], a[216:219], 0\n" \
"v_cvt_pk_bf16_f32 v209, v210, v211\n" \
"v_mul_f32_e32 v212, v56, v212\n" \
".fill (L_ab + 20480 - .) / 4, 4, 0xBF800000\n" \
"L_ap5:\n" \
"s_cbranch_execz L_ap0\n" \
"v_mul_f32_e32 v213, v56, v213\n" \
"v_mul_f32_e32 v214, v56, v214\n" \
"v_mul_f32_e32 v215, v56, v215\n" \
"v_mul_f32_e32 v216, v56, v216\n" \
"v_mfma_f32_16x16x32_bf16 v[92:95], a[144:147], a[112:115], v[44:47]\n" \
"ds_read_b128 a[144:147], v48 offset:12288\n" \
"v_mul_f32_e32 v217, v56, v217\n" \
"v_mfma_f32_16x16x32_bf16 v[92:95], a[148:151], a[116:119], v[92:95]\n" \
"ds_read_b128 a[148:151], v48 offset:13312\n" \
"v_cvt_pk_bf16_f32 v210, v212, v213\n" \
"v_mfma_f32_16x16x32_bf16 v[92:95], a[152:155], a[120:123], v[92:95]\n" \
"ds_read_b128 a[152:155], v48 offset:14336\n" \
"v_cvt_pk_bf16_f32 v211, v214, v215\n" \
"buffer_store_dwordx4 v[208:211], v20, s[56:59], s70 offen offset:2048 sc1\n" \
"v_mfma_f32_16x16x32_bf16 v[92:95], a[156:159], a[124:127], v[92:95]\n" \
"ds_read_b128 a[156:159], v48 offset:15360\n" \
"v_cvt_pk_bf16_f32 v216, v216, v217\n" \
"v_mfma_f32_16x16x32_bf16 v[88:91], a[140:143], a[124:127], v[44:47]\n" \
"ds_read_b128 a[140:143], v48 offset:11264\n" \
"v_mul_f32_e32 v218, v56, v218\n" \
"v_mfma_f32_16x16x32_bf16 v[88:91], a[136:139], a[120:123], v[88:91]\n" \
"ds_read_b128 a[136:139], v48 offset:10240\n" \
"v_mul_f32_e32 v219, v56, v219\n" \
"v_mfma_f32_16x16x32_bf16 v[88:91], a[132:135], a[116:119], v[88:91]\n" \
"ds_read_b128 a[132:135], v48 offset:9216\n" \
"v_mul_f32_e32 v57, v57, v61\n" \
"v_mfma_f32_16x16x32_bf16 v[88:91], a[128:131], a[112:115], v[88:91]\n" \
"ds_read_b128 a[128:131], v48 offset:8192\n" \
"v_mfma_f32_16x16x32_bf16 a[208:211], a[216:219], v[208:211], a[208:211]\n" \
"v_cvt_pk_bf16_f32 v217, v218, v219\n" \
"v_mul_f32_e32 v220, v56, v220\n" \
"v_mul_f32_e32 v221, v56, v221\n" \
"v_mul_f32_e32 v222, v56, v222\n" \
"v_mul_f32_e32 v223, v56, v223\n" \
"v_mul_f32_e32 v224, v57, v224\n" \
"v_mul_f32_e32 v225, v57, v225\n" \
"v_cvt_pk_bf16_f32 v218, v220, v221\n" \
"v_cvt_pk_bf16_f32 v219, v222, v223\n" \
"buffer_store_dwordx4 v[216:219], v20, s[56:59], s70 offen offset:3072 sc1\n" \
"v_cvt_pk_bf16_f32 v224, v224, v225\n" \
"v_mul_f32_e32 v226, v57, v226\n" \
"v_mul_f32_e32 v227, v57, v227\n" \
"v_mfma_f32_16x16x32_bf16 a[208:211], a[216:219], v[216:219], a[208:211]\n" \
"v_mfma_f32_32x32x16_bf16 v[208:223], a[216:219], a[216:219], 0\n" \
"v_cvt_pk_bf16_f32 v225, v226, v227\n" \
"v_mul_f32_e32 v228, v57, v228\n" \
"v_mul_f32_e32 v229, v57, v229\n" \
"v_mul_f32_e32 v230, v57, v230\n" \
"v_mul_f32_e32 v231, v57, v231\n" \
"v_mul_f32_e32 v232, v57, v232\n" \
"v_mul_f32_e32 v233, v57, v233\n" \
"v_cvt_pk_bf16_f32 v226, v228, v229\n" \
"v_cvt_pk_bf16_f32 v227, v230, v231\n" \
"buffer_store_dwordx4 v[224:227], v20, s[56:59], s71 offen offset:0 sc1\n" \
"v_cvt_pk_bf16_f32 v232, v232, v233\n" \
"v_mul_f32_e32 v234, v57, v234\n" \
"v_mul_f32_e32 v235, v57, v235\n" \
"v_mfma_f32_16x16x32_bf16 a[208:211], a[216:219], v[224:227], a[208:211]\n" \
"v_cvt_pk_bf16_f32 v233, v234, v235\n" \
"v_mul_f32_e32 v236, v57, v236\n" \
"v_mul_f32_e32 v237, v57, v237\n" \
"v_mul_f32_e32 v238, v57, v238\n" \
"v_mul_f32_e32 v239, v57, v239\n" \
"v_mul_f32_e32 v240, v57, v240\n" \
"v_mul_f32_e32 v241, v57, v241\n" \
"v_cvt_pk_bf16_f32 v234, v236, v237\n" \
"v_cvt_pk_bf16_f32 v235, v238, v239\n" \
"buffer_store_dwordx4 v[232:235], v20, s[56:59], s71 offen offset:1024 sc1\n" \
"v_cvt_pk_bf16_f32 v240, v240, v241\n" \
"v_mul_f32_e32 v242, v57, v242\n" \
"v_mul_f32_e32 v243, v57, v243\n" \
"v_mfma_f32_16x16x32_bf16 a[208:211], a[216:219], v[232:235], a[208:211]\n" \
"v_mfma_f32_32x32x16_bf16 v[224:239], a[216:219], a[216:219], 0\n" \
"v_cvt_pk_bf16_f32 v241, v242, v243\n" \
"v_mul_f32_e32 v244, v57, v244\n" \
"v_mul_f32_e32 v245, v57, v245\n" \
"v_mul_f32_e32 v246, v57, v246\n" \
"v_mul_f32_e32 v247, v57, v247\n" \
"v_mul_f32_e32 v248, v57, v248\n" \
"v_mul_f32_e32 v249, v57, v249\n" \
"v_cvt_pk_bf16_f32 v242, v244, v245\n" \
"v_cvt_pk_bf16_f32 v243, v246, v247\n" \
"buffer_store_dwordx4 v[240:243], v20, s[56:59], s71 offen offset:2048 sc1\n" \
"v_cvt_pk_bf16_f32 v248, v248, v249\n" \
"v_mul_f32_e32 v250, v57, v250\n" \
"v_mul_f32_e32 v251, v57, v251\n" \
"v_mfma_f32_16x16x32_bf16 a[208:211], a[216:219], v[240:243], a[208:211]\n" \
"v_cvt_pk_bf16_f32 v249, v250, v251\n" \
"v_mul_f32_e32 v252, v57, v252\n" \
"v_mul_f32_e32 v253, v57, v253\n" \
"v_mul_f32_e32 v254, v57, v254\n" \
"v_mul_f32_e32 v255, v57, v255\n" \
"v_cvt_pk_bf16_f32 v250, v252, v253\n" \
"v_cvt_pk_bf16_f32 v251, v254, v255\n" \
"buffer_store_dwordx4 v[248:251], v20, s[56:59], s71 offen offset:3072 sc1\n" \
"buffer_load_dwordx4 v53, s[8:11], s64 offen offset:3072 sc1 lds\n" \
"s_waitcnt vmcnt(16) lgkmcnt(0)\n" \
"s_barrier\n" \
"v_mfma_f32_16x16x32_bf16 a[208:211], a[216:219], v[248:251], a[208:211]\n" \
"v_mfma_f32_32x32x16_bf16 v[240:255], a[216:219], a[216:219], 0\n" \
"v_exp_f32_e32 v70, v70\n" \
"v_exp_f32_e32 v71, v71\n" \
"v_exp_f32_e32 v72, v72\n" \
"v_exp_f32_e32 v73, v73\n" \
"s_add_u32 s64, s64, 0x10000\n" \
"v_cmp_lt_f32_e64 s[72:73], s21, v62\n" \
"v_readlane_b32 s95, v11, 3\n" \
"v_accvgpr_read_b32 v63, a208\n" \
"v_cmp_u_f32_e32 vcc, v63, v63\n" \
"v_exp_f32_e32 v68, v68\n" \
"v_exp_f32_e32 v69, v69\n" \
"v_exp_f32_e32 v74, v74\n" \
"v_exp_f32_e32 v75, v75\n" \
"s_or_b64 vcc, s[72:73], vcc\n" \
"s_cselect_b32 s95, 0x40000000, s95\n" \
"s_cselect_b32 s94, 0x7f800000, 0\n" \
"v_cvt_pk_bf16_f32 v71, v70, v71\n" \
"v_exp_f32_e32 v66, v66\n" \
"v_exp_f32_e32 v67, v67\n" \
"v_exp_f32_e32 v64, v64\n" \
"v_exp_f32_e32 v65, v65\n" \
"v_cvt_pk_bf16_f32 v72, v72, v73\n" \
"v_exp_f32_e32 v76, v76\n" \
"v_exp_f32_e32 v77, v77\n" \
"v_exp_f32_e32 v78, v78\n" \
"v_exp_f32_e32 v79, v79\n" \
"v_cvt_pk_bf16_f32 v70, v68, v69\n" \
"v_cvt_pk_bf16_f32 v73, v74, v75\n" \
"v_add_u32_e32 v49, 0x10000, v48\n" \
"s_xor_b32 s65, s65, 0x8000\n" \
"v_cvt_pk_bf16_f32 v69, v66, v67\n" \
"v_cvt_pk_bf16_f32 v68, v64, v65\n" \
"v_cvt_pk_bf16_f32 v74, v76, v77\n" \
"v_cvt_pk_bf16_f32 v75, v78, v79\n" \
"v_xor_b32_e32 v48, 0x8000, v48\n" \
"s_sub_u32 s66, s19, 2\n" \
"s_branch L_2_%=\n" \
"L_f1_%=:\n" \
"ds_read_b128 a[160:163], v49 offset:0\n" \
"ds_read_b128 a[164:167], v49 offset:1024\n" \
"ds_read_b128 a[168:171], v49 offset:2048\n" \
"v_mfma_f32_16x16x32_bf16 v[104:107], a[128:131], a[80:83], v[36:39]\n" \
"v_cvt_pk_bf16_f32 v192, v192, v193\n" \
"v_cvt_pk_bf16_f32 v224, v224, v225\n" \
"v_cvt_pk_bf16_f32 v193, v194, v195\n" \
"v_cvt_pk_bf16_f32 v225, v226, v227\n" \
"ds_read_b128 a[172:175], v49 offset:3072\n" \
"v_cvt_pk_bf16_f32 v194, v196, v197\n" \
"v_cvt_pk_bf16_f32 v195, v198, v199\n" \
"v_cvt_pk_bf16_f32 v226, v228, v229\n" \
"v_cvt_pk_bf16_f32 v227, v230, v231\n" \
"v_mfma_f32_16x16x32_bf16 v[104:107], a[132:135], a[84:87], v[104:107]\n" \
"v_lshl_add_u32 v50, s15, 2, v20\n" \
"v_cvt_pk_bf16_f32 v196, v200, v201\n" \
"v_cvt_pk_bf16_f32 v197, v202, v203\n" \
"v_cvt_pk_bf16_f32 v198, v204, v205\n" \
"v_cvt_pk_bf16_f32 v199, v206, v207\n" \
"v_cvt_pk_bf16_f32 v228, v232, v233\n" \
"v_cvt_pk_bf16_f32 v229, v234, v235\n" \
"v_cvt_pk_bf16_f32 v230, v236, v237\n" \
"v_cvt_pk_bf16_f32 v231, v238, v239\n" \
"v_add_u32_e32 v50, 0x1a000, v50\n" \
"v_mov_b32_e32 v61, s62\n" \
"v_cvt_pk_bf16_f32 v200, v208, v209\n" \
"v_cvt_pk_bf16_f32 v201, v210, v211\n" \
"v_cvt_pk_bf16_f32 v202, v212, v213\n" \
"v_cvt_pk_bf16_f32 v203, v214, v215\n" \
"v_cvt_pk_bf16_f32 v204, v216, v217\n" \
"v_cvt_pk_bf16_f32 v205, v218, v219\n" \
"v_cvt_pk_bf16_f32 v206, v220, v221\n" \
"v_cvt_pk_bf16_f32 v207, v222, v223\n" \
"v_cvt_pk_bf16_f32 v232, v240, v241\n" \
"v_cvt_pk_bf16_f32 v233, v242, v243\n" \
"v_cvt_pk_bf16_f32 v234, v244, v245\n" \
"v_cvt_pk_bf16_f32 v235, v246, v247\n" \
"v_cvt_pk_bf16_f32 v236, v248, v249\n" \
"v_cvt_pk_bf16_f32 v237, v250, v251\n" \
"v_cvt_pk_bf16_f32 v238, v252, v253\n" \
"v_cvt_pk_bf16_f32 v239, v254, v255\n" \
"ds_write_b128 v50, v[192:195] offset:0\n" \
"v_mfma_f32_16x16x32_bf16 v[104:107], a[136:139], a[88:91], v[104:107]\n" \
"ds_read_b128 a[176:179], v49 offset:4096\n" \
"v_add_u32_e32 v56, 0x49000, v20\n" \
"v_mfma_f32_16x16x32_bf16 v[104:107], a[140:143], a[92:95], v[104:107]\n" \
"ds_write_b128 v50, v[196:199] offset:1024\n" \
"v_mfma_f32_16x16x32_bf16 v[108:111], a[156:159], a[92:95], v[36:39]\n" \
"ds_read_b128 a[180:183], v49 offset:5120\n" \
"ds_read_b128 a[184:187], v49 offset:6144\n" \
"v_mfma_f32_16x16x32_bf16 v[108:111], a[152:155], a[88:91], v[108:111]\n" \
"ds_write_b128 v50, v[200:203] offset:2048\n" \
"v_mfma_f32_16x16x32_bf16 v[108:111], a[148:151], a[84:87], v[108:111]\n" \
"v_and_b32_e32 v68, v12, v68\n" \
"v_and_b32_e32 v69, v13, v69\n" \
"v_mfma_f32_16x16x32_bf16 v[108:111], a[144:147], a[80:83], v[108:111]\n" \
"ds_write_b128 v50, v[204:207] offset:3072\n" \
"v_and_b32_e32 v70, v14, v70\n" \
"v_and_b32_e32 v71, v15, v71\n" \
"ds_read_b128 a[188:191], v49 offset:7168\n" \
"ds_read_b128 a[0:3], v20 offset:8192\n" \
"s_waitcnt lgkmcnt(9)\n" \
"v_mfma_f32_16x16x32_bf16 v[128:131], a[160:163], v[68:71], v[128:131]\n" \
"ds_write_b128 v50, v[224:227] offset:4096\n" \
"v_mfma_f32_16x16x32_bf16 v[160:163], a[160:163], v[72:75], v[160:163]\n" \
"ds_read_b128 a[4:7], v20 offset:9216\n" \
"ds_read_b128 a[8:11], v20 offset:10240\n" \
"v_mfma_f32_16x16x32_bf16 v[132:135], a[164:167], v[68:71], v[132:135]\n" \
"ds_write_b128 v50, v[228:231] offset:5120\n" \
"v_mfma_f32_16x16x32_bf16 v[164:167], a[164:167], v[72:75], v[164:167]\n" \
"ds_read_b128 a[12:15], v20 offset:11264\n" \
"ds_read_b128 a[28:31], v20 offset:15360\n" \
"v_mfma_f32_16x16x32_bf16 v[136:139], a[168:171], v[68:71], v[136:139]\n" \
"ds_write_b128 v50, v[232:235] offset:6144\n" \
"v_mfma_f32_16x16x32_bf16 v[168:171], a[168:171], v[72:75], v[168:171]\n" \
"ds_read_b128 a[24:27], v20 offset:14336\n" \
"ds_read_b128 a[20:23], v20 offset:13312\n" \
"v_mfma_f32_16x16x32_bf16 v[140:143], a[172:175], v[68:71], v[140:143]\n" \
"ds_write_b128 v50, v[236:239] offset:7168\n" \
"v_mfma_f32_16x16x32_bf16 v[172:175], a[172:175], v[72:75], v[172:175]\n" \
"ds_read_b128 a[16:19], v20 offset:12288\n" \
"s_waitcnt lgkmcnt(15)\n" \
"v_mfma_f32_16x16x32_bf16 v[144:147], a[176:179], v[68:71], v[144:147]\n" \
"v_xor_b32_e32 v48, 0x4000, v49\n" \
"ds_read_b32 v58, v18 offset:256\n" \
"v_mfma_f32_16x16x32_bf16 v[176:179], a[176:179], v[72:75], v[176:179]\n" \
"ds_read_b32 v59, v18 offset:384\n" \
"ds_read_b128 a[32:35], v48 offset:8192\n" \
"v_mfma_f32_16x16x32_bf16 v[148:151], a[180:183], v[68:71], v[148:151]\n" \
"ds_read_b128 a[36:39], v48 offset:9216\n" \
"ds_read_b128 a[40:43], v48 offset:10240\n" \
"v_mfma_f32_16x16x32_bf16 v[180:183], a[180:183], v[72:75], v[180:183]\n" \
"ds_read_b128 a[44:47], v48 offset:11264\n" \
"ds_read_b128 a[48:51], v48 offset:12288\n" \
"v_mfma_f32_16x16x32_bf16 v[152:155], a[184:187], v[68:71], v[152:155]\n" \
"ds_read_b128 a[52:55], v48 offset:13312\n" \
"ds_read_b128 a[56:59], v48 offset:14336\n" \
"v_mfma_f32_16x16x32_bf16 v[184:187], a[184:187], v[72:75], v[184:187]\n" \
"ds_read_b128 a[60:63], v48 offset:15360\n" \
"s_waitcnt lgkmcnt(15)\n" \
"v_mfma_f32_16x16x32_bf16 v[156:159], a[188:191], v[68:71], v[156:159]\n" \
"ds_read_b128 a[128:131], v49 offset:8192\n" \
"ds_read_b128 a[132:135], v49 offset:9216\n" \
"v_mfma_f32_16x16x32_bf16 v[188:191], a[188:191], v[72:75], v[188:191]\n" \
"ds_read_b128 a[136:139], v49 offset:10240\n" \
"ds_read_b128 a[140:143], v49 offset:11264\n" \
"v_smfmac_f32_16x16x64_bf16 a[192:195], a[200:203], v[68:75], v21\n" \
"ds_read_b128 a[144:147], v49 offset:12288\n" \
"ds_read_b128 a[148:151], v49 offset:13312\n" \
"v_mfma_f32_16x16x32_bf16 v[112:115], a[0:3], a[64:67], v[32:35]\n" \
"ds_read_b128 a[152:155], v49 offset:14336\n" \
"ds_read_b128 a[156:159], v49 offset:15360\n" \
"v_mfma_f32_16x16x32_bf16 v[112:115], a[4:7], a[68:71], v[112:115]\n" \
"v_accvgpr_read_b32 v40, a196\n" \
"v_accvgpr_read_b32 v24, a197\n" \
"v_mfma_f32_16x16x32_bf16 v[112:115], a[8:11], a[72:75], v[112:115]\n" \
"v_accvgpr_read_b32 v41, a198\n" \
"v_accvgpr_read_b32 v25, a199\n" \
"v_mfma_f32_16x16x32_bf16 v[112:115], a[12:15], a[76:79], v[112:115]\n" \
"v_add_f32_e32 v40, v40, v24\n" \
"v_add_f32_e32 v41, v41, v25\n" \
"v_mfma_f32_16x16x32_bf16 v[116:119], a[28:31], a[76:79], v[32:35]\n" \
"v_add_f32_e32 v40, s94, v40\n" \
"s_waitcnt lgkmcnt(15)\n" \
"v_mfma_f32_16x16x32_bf16 v[116:119], a[24:27], a[72:75], v[116:119]\n" \
"v_add_f32_e32 v41, s94, v41\n" \
"ds_write_b64 v19, v[40:41]\n" \
"v_mfma_f32_16x16x32_bf16 v[116:119], a[20:23], a[68:71], v[116:119]\n" \
"v_add_u32_e32 v52, s15, v20\n" \
"v_add_u32_e32 v51, 0x10000, v52\n" \
"v_mfma_f32_16x16x32_bf16 v[116:119], a[16:19], a[64:67], v[116:119]\n" \
"v_exp_f32_e32 v104, v104\n" \
"v_mfma_f32_16x16x32_bf16 v[124:127], a[16:19], a[80:83], v[36:39]\n" \
"v_exp_f32_e32 v105, v105\n" \
"v_mfma_f32_16x16x32_bf16 v[124:127], a[20:23], a[84:87], v[124:127]\n" \
"v_exp_f32_e32 v106, v106\n" \
"v_mfma_f32_16x16x32_bf16 v[124:127], a[24:27], a[88:91], v[124:127]\n" \
"v_exp_f32_e32 v107, v107\n" \
"v_mfma_f32_16x16x32_bf16 v[124:127], a[28:31], a[92:95], v[124:127]\n" \
"v_cvt_pk_bf16_f32 v104, v104, v105\n" \
"v_cvt_pk_bf16_f32 v105, v106, v107\n" \
"v_mfma_f32_16x16x32_bf16 v[120:123], a[12:15], a[92:95], v[36:39]\n" \
"v_exp_f32_e32 v108, v108\n" \
"v_mfma_f32_16x16x32_bf16 v[120:123], a[8:11], a[88:91], v[120:123]\n" \
"v_exp_f32_e32 v109, v109\n" \
"v_mfma_f32_16x16x32_bf16 v[120:123], a[4:7], a[84:87], v[120:123]\n" \
"v_exp_f32_e32 v110, v110\n" \
"v_mfma_f32_16x16x32_bf16 v[120:123], a[0:3], a[80:83], v[120:123]\n" \
"v_exp_f32_e32 v111, v111\n" \
"s_waitcnt lgkmcnt(0)\n" \
"s_barrier\n" \
"ds_read_b128 v[208:211], v52 offset:0\n" \
"ds_read_b128 v[212:215], v52 offset:1024\n" \
"ds_read_b128 v[216:219], v52 offset:2048\n" \
"ds_read_b128 v[220:223], v52 offset:3072\n" \
"ds_read_b128 v[240:243], v51 offset:32768\n" \
"ds_read_b128 v[244:247], v51 offset:33792\n" \
"ds_read_b128 v[248:251], v51 offset:34816\n" \
"ds_read_b128 v[252:255], v51 offset:35840\n" \
"v_cvt_pk_bf16_f32 v106, v108, v109\n" \
"v_cvt_pk_bf16_f32 v107, v110, v111\n" \
"v_and_b32_e32 v104, v12, v104\n" \
"v_and_b32_e32 v105, v13, v105\n" \
"v_and_b32_e32 v106, v14, v106\n" \
"s_waitcnt vmcnt(0)\n" \
"s_mov_b64 exec, 1\n" \
"buffer_store_dword v61, off, s[56:59], s95 offset:0 sc1\n" \
"s_mov_b64 exec, -1\n" \
"v_and_b32_e32 v107, v15, v107\n" \
"v_xor_b32_e32 v63, 0x400, v19\n" \
"ds_read_b64 v[42:43], v63\n" \
"v_mfma_f32_16x16x32_bf16 a[192:195], a[212:215], v[104:107], a[192:195]\n" \
"v_mov_b32_e32 v194, v28\n" \
"v_mov_b32_e32 v195, v29\n" \
"v_mfma_f32_16x16x32_bf16 v[160:163], a[128:131], v[104:107], v[160:163]\n" \
"v_mov_b32_e32 v196, 0\n" \
"v_mov_b32_e32 v197, 0\n" \
"v_mfma_f32_16x16x32_bf16 v[164:167], a[132:135], v[104:107], v[164:167]\n" \
"v_mov_b32_e32 v192, 0\n" \
"v_mov_b32_e32 v193, 0\n" \
"v_mfma_f32_16x16x32_bf16 v[168:171], a[136:139], v[104:107], v[168:171]\n" \
"v_readlane_b32 s93, v11, 22\n" \
"v_readlane_b32 s92, v11, 27\n" \
"v_mfma_f32_16x16x32_bf16 v[172:175], a[140:143], v[104:107], v[172:175]\n" \
"buffer_load_dwordx4 a[96:99], v20, s[56:59], s60 offen offset:0 sc1\n" \
"v_mfma_f32_16x16x32_bf16 v[176:179], a[144:147], v[104:107], v[176:179]\n" \
"v_exp_f32_e32 v118, v118\n" \
"v_mfma_f32_16x16x32_bf16 v[180:183], a[148:151], v[104:107], v[180:183]\n" \
"v_exp_f32_e32 v119, v119\n" \
"v_mfma_f32_16x16x32_bf16 v[184:187], a[152:155], v[104:107], v[184:187]\n" \
"s_add_u32 m0, s93, 0x0\n" \
"v_cvt_pk_bf16_f32 v119, v118, v119\n" \
"v_mfma_f32_16x16x32_bf16 v[188:191], a[156:159], v[104:107], v[188:191]\n" \
"v_exp_f32_e32 v120, v120\n" \
"s_waitcnt lgkmcnt(1)\n" \
"s_barrier\n" \
"buffer_load_dwordx4 a[112:115], v56, s[56:59], s60 offen offset:0 sc1\n" \
"v_mfma_f32_16x16x32_bf16 v[128:131], v[194:197], v[208:211], v[128:131]\n" \
"v_exp_f32_e32 v121, v121\n" \
"v_mfma_f32_16x16x32_bf16 v[132:135], v[192:195], v[208:211], v[132:135]\n" \
"v_exp_f32_e32 v116, v116\n" \
"v_mfma_f32_16x16x32_bf16 v[136:139], v[194:197], v[212:215], v[136:139]\n" \
"v_exp_f32_e32 v117, v117\n" \
"v_mfma_f32_16x16x32_bf16 v[140:143], v[192:195], v[212:215], v[140:143]\n" \
"v_exp_f32_e32 v122, v122\n" \
"v_mfma_f32_16x16x32_bf16 v[144:147], v[194:197], v[216:219], v[144:147]\n" \
"v_exp_f32_e32 v123, v123\n" \
"v_mfma_f32_16x16x32_bf16 v[148:151], v[192:195], v[216:219], v[148:151]\n" \
"buffer_load_dwordx4 a[100:103], v20, s[56:59], s60 offen offset:1024 sc1\n" \
"v_mfma_f32_16x16x32_bf16 v[152:155], v[194:197], v[220:223], v[152:155]\n" \
"v_exp_f32_e32 v114, v114\n" \
"v_mfma_f32_16x16x32_bf16 v[156:159], v[192:195], v[220:223], v[156:159]\n" \
"v_exp_f32_e32 v115, v115\n" \
"v_mfma_f32_16x16x32_bf16 v[160:163], v[194:197], v[240:243], v[160:163]\n" \
"v_exp_f32_e32 v112, v112\n" \
"v_mfma_f32_16x16x32_bf16 v[164:167], v[192:195], v[240:243], v[164:167]\n" \
"v_exp_f32_e32 v113, v113\n" \
"v_mfma_f32_16x16x32_bf16 v[168:171], v[194:197], v[244:247], v[168:171]\n" \
"v_cvt_pk_bf16_f32 v120, v120, v121\n" \
"v_cvt_pk_bf16_f32 v118, v116, v117\n" \
"v_mfma_f32_16x16x32_bf16 v[172:175], v[192:195], v[244:247], v[172:175]\n" \
"buffer_load_dwordx4 a[116:119], v56, s[56:59], s60 offen offset:1024 sc1\n" \
"v_mfma_f32_16x16x32_bf16 v[176:179], v[194:197], v[248:251], v[176:179]\n" \
"v_exp_f32_e32 v124, v124\n" \
"v_mfma_f32_16x16x32_bf16 v[180:183], v[192:195], v[248:251], v[180:183]\n" \
"v_exp_f32_e32 v125, v125\n" \
"v_mfma_f32_16x16x32_bf16 v[184:187], v[194:197], v[252:255], v[184:187]\n" \
"v_exp_f32_e32 v126, v126\n" \
"v_mfma_f32_16x16x32_bf16 v[188:191], v[192:195], v[252:255], v[188:191]\n" \
"v_exp_f32_e32 v127, v127\n" \
"v_cvt_pk_bf16_f32 v121, v122, v123\n" \
"v_cvt_pk_bf16_f32 v117, v114, v115\n" \
"v_cvt_pk_bf16_f32 v116, v112, v113\n" \
"v_cvt_pk_bf16_f32 v122, v124, v125\n" \
"v_cvt_pk_bf16_f32 v123, v126, v127\n" \
"buffer_load_dwordx4 a[104:107], v20, s[56:59], s60 offen offset:2048 sc1\n" \
"s_nop 0\n" \
"v_smfmac_f32_16x16x64_bf16 a[192:195], a[200:203], v[116:123], v21\n" \
"v_mfma_f32_16x16x32_bf16 v[128:131], a[32:35], v[116:119], v[128:131]\n" \
"v_mfma_f32_16x16x32_bf16 v[160:163], a[32:35], v[120:123], v[160:163]\n" \
"v_mfma_f32_16x16x32_bf16 v[132:135], a[36:39], v[116:119], v[132:135]\n" \
"v_mfma_f32_16x16x32_bf16 v[164:167], a[36:39], v[120:123], v[164:167]\n" \
"v_mfma_f32_16x16x32_bf16 v[136:139], a[40:43], v[116:119], v[136:139]\n" \
"buffer_load_dwordx4 a[120:123], v56, s[56:59], s60 offen offset:2048 sc1\n" \
"v_mfma_f32_16x16x32_bf16 v[168:171], a[40:43], v[120:123], v[168:171]\n" \
"v_mfma_f32_16x16x32_bf16 v[140:143], a[44:47], v[116:119], v[140:143]\n" \
"v_accvgpr_read_b32 v54, a192\n" \
"v_accvgpr_read_b32 v22, a193\n" \
"v_mfma_f32_16x16x32_bf16 v[172:175], a[44:47], v[120:123], v[172:175]\n" \
"v_add_f32_e32 v54, v54, v22\n" \
"s_waitcnt lgkmcnt(0)\n" \
"v_add_f32_e32 v54, v54, v42\n" \
"v_mfma_f32_16x16x32_bf16 v[144:147], a[48:51], v[116:119], v[144:147]\n" \
"v_max_f32_e32 v62, v62, v54\n" \
"v_accvgpr_read_b32 v55, a194\n" \
"v_mfma_f32_16x16x32_bf16 v[176:179], a[48:51], v[120:123], v[176:179]\n" \
"v_rcp_f32_e32 v54, v54\n" \
"buffer_load_dwordx4 a[108:111], v20, s[56:59], s60 offen offset:3072 sc1\n" \
"v_mfma_f32_16x16x32_bf16 v[148:151], a[52:55], v[116:119], v[148:151]\n" \
"v_mul_f32_e32 v54, v54, v58\n" \
"v_mul_f32_e32 v128, v54, v128\n" \
"v_mfma_f32_16x16x32_bf16 v[180:183], a[52:55], v[120:123], v[180:183]\n" \
"v_mul_f32_e32 v129, v54, v129\n" \
"v_mul_f32_e32 v130, v54, v130\n" \
"v_mul_f32_e32 v131, v54, v131\n" \
"v_mul_f32_e32 v132, v54, v132\n" \
"v_mul_f32_e32 v133, v54, v133\n" \
"v_mul_f32_e32 v134, v54, v134\n" \
"v_mul_f32_e32 v135, v54, v135\n" \
"v_cvt_pk_bf16_f32 v64, v128, v129\n" \
"v_cvt_pk_bf16_f32 v65, v130, v131\n" \
"v_cvt_pk_bf16_f32 v66, v132, v133\n" \
"v_cvt_pk_bf16_f32 v67, v134, v135\n" \
"v_accvgpr_read_b32 v23, a195\n" \
"v_mfma_f32_16x16x32_bf16 v[152:155], a[56:59], v[116:119], v[152:155]\n" \
"v_mfma_f32_16x16x32_bf16 a[208:211], a[216:219], v[64:67], a[208:211]\n" \
"buffer_load_dwordx4 a[124:127], v56, s[56:59], s60 offen offset:3072 sc1\n" \
"v_mul_f32_e32 v136, v54, v136\n" \
"v_mul_f32_e32 v137, v54, v137\n" \
"v_mul_f32_e32 v138, v54, v138\n" \
"v_mul_f32_e32 v139, v54, v139\n" \
"v_mul_f32_e32 v140, v54, v140\n" \
"v_mul_f32_e32 v141, v54, v141\n" \
"v_mul_f32_e32 v142, v54, v142\n" \
"v_mul_f32_e32 v143, v54, v143\n" \
"v_add_f32_e32 v55, v55, v23\n" \
"v_cvt_pk_bf16_f32 v68, v136, v137\n" \
"v_cvt_pk_bf16_f32 v69, v138, v139\n" \
"v_cvt_pk_bf16_f32 v70, v140, v141\n" \
"v_cvt_pk_bf16_f32 v71, v142, v143\n" \
"v_add_f32_e32 v55, v55, v43\n" \
"v_mul_f32_e32 v144, v54, v144\n" \
"v_mfma_f32_16x16x32_bf16 a[208:211], a[216:219], v[68:71], a[208:211]\n" \
"v_mul_f32_e32 v145, v54, v145\n" \
"v_mul_f32_e32 v146, v54, v146\n" \
"v_mul_f32_e32 v147, v54, v147\n" \
"v_mul_f32_e32 v148, v54, v148\n" \
"buffer_load_dwordx4 v0, s[80:83], s92 offen offset:0 lds\n" \
"v_mul_f32_e32 v149, v54, v149\n" \
"v_mul_f32_e32 v150, v54, v150\n" \
"v_mul_f32_e32 v151, v54, v151\n" \
"v_max_f32_e32 v62, v62, v55\n" \
"v_cvt_pk_bf16_f32 v72, v144, v145\n" \
"v_cvt_pk_bf16_f32 v73, v146, v147\n" \
"v_cvt_pk_bf16_f32 v74, v148, v149\n" \
"v_cvt_pk_bf16_f32 v75, v150, v151\n" \
"v_rcp_f32_e32 v55, v55\n" \
"v_mfma_f32_16x16x32_bf16 v[184:187], a[56:59], v[120:123], v[184:187]\n" \
"v_mfma_f32_16x16x32_bf16 a[208:211], a[216:219], v[72:75], a[208:211]\n" \
"v_mul_f32_e32 v152, v54, v152\n" \
"v_mul_f32_e32 v153, v54, v153\n" \
"v_mul_f32_e32 v154, v54, v154\n" \
"v_mul_f32_e32 v155, v54, v155\n" \
"v_cvt_pk_bf16_f32 v76, v152, v153\n" \
"buffer_load_dwordx4 v1, s[80:83], s92 offen offset:1024 lds\n" \
"v_cvt_pk_bf16_f32 v77, v154, v155\n" \
"v_mul_f32_e32 v55, v55, v59\n" \
"v_mul_f32_e32 v160, v55, v160\n" \
"v_mul_f32_e32 v161, v55, v161\n" \
"v_mul_f32_e32 v162, v55, v162\n" \
"v_mul_f32_e32 v163, v55, v163\n" \
"v_mul_f32_e32 v164, v55, v164\n" \
"v_mul_f32_e32 v165, v55, v165\n" \
"v_mul_f32_e32 v166, v55, v166\n" \
"v_mul_f32_e32 v167, v55, v167\n" \
"v_cvt_pk_bf16_f32 v80, v160, v161\n" \
"v_cvt_pk_bf16_f32 v81, v162, v163\n" \
"v_cvt_pk_bf16_f32 v82, v164, v165\n" \
"v_cvt_pk_bf16_f32 v83, v166, v167\n" \
"v_mfma_f32_16x16x32_bf16 v[156:159], a[60:63], v[116:119], v[156:159]\n" \
"v_mul_f32_e32 v168, v55, v168\n" \
"v_mul_f32_e32 v169, v55, v169\n" \
"v_mul_f32_e32 v170, v55, v170\n" \
"v_mul_f32_e32 v171, v55, v171\n" \
"v_mul_f32_e32 v172, v55, v172\n" \
"buffer_load_dwordx4 v2, s[80:83], s92 offen offset:2048 lds\n" \
"v_mul_f32_e32 v173, v55, v173\n" \
"v_mul_f32_e32 v174, v55, v174\n" \
"v_mul_f32_e32 v156, v54, v156\n" \
"v_mul_f32_e32 v157, v54, v157\n" \
"v_mul_f32_e32 v158, v54, v158\n" \
"v_mul_f32_e32 v159, v54, v159\n" \
"v_cvt_pk_bf16_f32 v78, v156, v157\n" \
"v_cvt_pk_bf16_f32 v79, v158, v159\n" \
"v_mul_f32_e32 v175, v55, v175\n" \
"v_cvt_pk_bf16_f32 v84, v168, v169\n" \
"v_mfma_f32_16x16x32_bf16 a[208:211], a[216:219], v[76:79], a[208:211]\n" \
"v_mfma_f32_16x16x32_bf16 a[208:211], a[216:219], v[80:83], a[208:211]\n" \
"v_cvt_pk_bf16_f32 v85, v170, v171\n" \
"v_cvt_pk_bf16_f32 v86, v172, v173\n" \
"v_cvt_pk_bf16_f32 v87, v174, v175\n" \
"v_mul_f32_e32 v176, v55, v176\n" \
"v_mul_f32_e32 v177, v55, v177\n" \
"buffer_load_dwordx4 v3, s[80:83], s92 offen offset:3072 lds\n" \
"s_add_u32 m0, s93, 0x1000\n" \
"v_mfma_f32_16x16x32_bf16 a[208:211], a[216:219], v[84:87], a[208:211]\n" \
"v_mul_f32_e32 v178, v55, v178\n" \
"v_mul_f32_e32 v179, v55, v179\n" \
"v_mul_f32_e32 v180, v55, v180\n" \
"v_mul_f32_e32 v181, v55, v181\n" \
"v_mul_f32_e32 v182, v55, v182\n" \
"v_mul_f32_e32 v183, v55, v183\n" \
"v_cvt_pk_bf16_f32 v96, v176, v177\n" \
"v_cvt_pk_bf16_f32 v97, v178, v179\n" \
"v_cvt_pk_bf16_f32 v98, v180, v181\n" \
"v_cvt_pk_bf16_f32 v99, v182, v183\n" \
"v_mfma_f32_16x16x32_bf16 v[188:191], a[60:63], v[120:123], v[188:191]\n" \
"v_mul_f32_e32 v184, v55, v184\n" \
"v_mfma_f32_16x16x32_bf16 a[208:211], a[216:219], v[96:99], a[208:211]\n" \
"v_mul_f32_e32 v185, v55, v185\n" \
"v_mul_f32_e32 v186, v55, v186\n" \
"buffer_load_dwordx4 v4, s[80:83], s92 offen offset:0 lds\n" \
"v_mul_f32_e32 v187, v55, v187\n" \
"v_cvt_pk_bf16_f32 v100, v184, v185\n" \
"v_cvt_pk_bf16_f32 v101, v186, v187\n" \
"v_mul_f32_e32 v188, v55, v188\n" \
"v_mul_f32_e32 v189, v55, v189\n" \
"v_mul_f32_e32 v190, v55, v190\n" \
"v_mul_f32_e32 v191, v55, v191\n" \
"v_cvt_pk_bf16_f32 v102, v188, v189\n" \
"v_cvt_pk_bf16_f32 v103, v190, v191\n" \
"v_cmp_lt_f32_e64 s[72:73], s21, v62\n" \
"buffer_load_dwordx4 v5, s[80:83], s92 offen offset:1024 lds\n" \
"v_mfma_f32_16x16x32_bf16 a[208:211], a[216:219], v[100:103], a[208:211]\n" \
"buffer_load_dwordx4 v6, s[80:83], s92 offen offset:2048 lds\n" \
"buffer_load_dwordx4 v7, s[80:83], s92 offen offset:3072 lds\n" \
"s_add_u32 m0, s93, 0x2000\n" \
"s_nop 0\n" \
"buffer_load_dwordx4 v8, s[80:83], s92 offen offset:0 lds\n" \
"s_nop 2\n" \
"v_accvgpr_read_b32 v63, a208\n" \
"v_cmp_u_f32_e32 vcc, v63, v63\n" \
"s_nop 0\n" \
"s_or_b64 vcc, s[72:73], vcc\n" \
"s_cbranch_scc0 L_out_%=\n" \
"s_branch L_end_%=\n" \
".fill (L_ab + 24576 - .) / 4, 4, 0xBF800000\n" \
".fill (L_ab + 24576 - .) / 4, 4, 0xBF800000\n" \
"L_ap6:\n" \
"s_setpc_b64 s[88:89]\n" \
"L_gs7:\n" \
"v_mfma_f32_16x16x32_bf16 v[104:107], a[128:131], a[16:19], v[36:39]\n" \
"s_add_u32 s68, s60, 0xfff6e000\n" \
"s_add_u32 s69, s60, 0xfffb7000\n" \
"s_add_u32 s70, s60, 0x0\n" \
"s_add_u32 s71, s60, 0x49000\n" \
"v_mfma_f32_16x16x32_bf16 v[104:107], a[132:135], a[20:23], v[104:107]\n" \
"s_xor_b32 m0, s65, 0x12000\n" \
"ds_read_b128 a[160:163], v49 offset:0\n" \
"v_mfma_f32_16x16x32_bf16 v[104:107], a[136:139], a[24:27], v[104:107]\n" \
"buffer_load_dwordx4 v50, s[8:11], s64 offen offset:0 sc1 lds\n" \
"v_mfma_f32_16x16x32_bf16 v[104:107], a[140:143], a[28:31], v[104:107]\n" \
"ds_read_b128 a[164:167], v49 offset:1024\n" \
"ds_read_b128 a[168:171], v49 offset:2048\n" \
"v_mfma_f32_16x16x32_bf16 v[108:111], a[156:159], a[28:31], v[36:39]\n" \
"ds_read_b128 a[172:175], v49 offset:3072\n" \
"ds_read_b128 a[176:179], v49 offset:4096\n" \
"v_mfma_f32_16x16x32_bf16 v[108:111], a[152:155], a[24:27], v[108:111]\n" \
"s_mov_b32 s65, s13\n" \
"v_add_u32_e32 v63, s15, v20\n" \
"v_mfma_f32_16x16x32_bf16 v[108:111], a[148:151], a[20:23], v[108:111]\n" \
"buffer_load_dwordx4 v50, s[8:11], s64 offen offset:1024 sc1 lds\n" \
"v_mfma_f32_16x16x32_bf16 v[108:111], a[144:147], a[16:19], v[108:111]\n" \
"s_add_u32 s67, s14, 40960\n" \
"v_add_u32_e32 v50, 0x6000, v63\n" \
"v_mfma_f32_16x16x32_bf16 v[116:119], a[144:147], a[32:35], v[40:43]\n" \
"s_xor_b32 m0, s65, 0x8000\n" \
"ds_read_b128 a[180:183], v49 offset:5120\n" \
"v_mfma_f32_16x16x32_bf16 v[116:119], a[148:151], a[36:39], v[116:119]\n" \
"ds_read_b128 a[184:187], v49 offset:6144\n" \
"v_and_b32_e32 v68, v12, v68\n" \
"v_mfma_f32_16x16x32_bf16 v[116:119], a[152:155], a[40:43], v[116:119]\n" \
"v_and_b32_e32 v69, v13, v69\n" \
"v_and_b32_e32 v70, v14, v70\n" \
"v_mfma_f32_16x16x32_bf16 v[116:119], a[156:159], a[44:47], v[116:119]\n" \
"buffer_load_dwordx4 v50, s[8:11], s67 offen offset:0 sc1 lds\n" \
"v_mfma_f32_16x16x32_bf16 v[112:115], a[140:143], a[44:47], v[40:43]\n" \
"v_and_b32_e32 v71, v15, v71\n" \
"ds_read_b128 a[188:191], v49 offset:7168\n" \
"v_mfma_f32_16x16x32_bf16 v[112:115], a[136:139], a[40:43], v[112:115]\n" \
"v_exp_f32_e32 v86, v86\n" \
"v_mfma_f32_16x16x32_bf16 v[112:115], a[132:135], a[36:39], v[112:115]\n" \
"v_exp_f32_e32 v87, v87\n" \
"v_mfma_f32_16x16x32_bf16 v[112:115], a[128:131], a[32:35], v[112:115]\n" \
"v_exp_f32_e32 v84, v84\n" \
"v_mfma_f32_16x16x32_bf16 v[120:123], a[128:131], a[48:51], v[44:47]\n" \
"buffer_load_dwordx4 v50, s[8:11], s67 offen offset:1024 sc1 lds\n" \
"v_mfma_f32_16x16x32_bf16 v[120:123], a[132:135], a[52:55], v[120:123]\n" \
"v_exp_f32_e32 v85, v85\n" \
"v_mfma_f32_16x16x32_bf16 v[120:123], a[136:139], a[56:59], v[120:123]\n" \
"v_cvt_pk_bf16_f32 v87, v86, v87\n" \
"v_cvt_pk_bf16_f32 v86, v84, v85\n" \
"v_mfma_f32_16x16x32_bf16 v[120:123], a[140:143], a[60:63], v[120:123]\n" \
"v_exp_f32_e32 v82, v82\n" \
"v_mfma_f32_16x16x32_bf16 v[124:127], a[156:159], a[60:63], v[44:47]\n" \
"v_exp_f32_e32 v83, v83\n" \
"v_mfma_f32_16x16x32_bf16 v[124:127], a[152:155], a[56:59], v[124:127]\n" \
"v_exp_f32_e32 v80, v80\n" \
"v_mfma_f32_16x16x32_bf16 v[124:127], a[148:151], a[52:55], v[124:127]\n" \
"buffer_load_dwordx4 v50, s[8:11], s67 offen offset:2048 sc1 lds\n" \
"v_mfma_f32_16x16x32_bf16 v[124:127], a[144:147], a[48:51], v[124:127]\n" \
"v_cvt_pk_bf16_f32 v85, v82, v83\n" \
"s_waitcnt lgkmcnt(1)\n" \
"v_mfma_f32_16x16x32_bf16 v[128:131], a[160:163], v[68:71], v[128:131]\n" \
"v_exp_f32_e32 v81, v81\n" \
"v_mfma_f32_16x16x32_bf16 v[160:163], a[160:163], v[72:75], v[160:163]\n" \
"v_cvt_pk_bf16_f32 v84, v80, v81\n" \
"s_mov_b32 s64, s14\n" \
"v_mfma_f32_16x16x32_bf16 v[132:135], a[164:167], v[68:71], v[132:135]\n" \
"v_exp_f32_e32 v88, v88\n" \
"v_mfma_f32_16x16x32_bf16 v[164:167], a[164:167], v[72:75], v[164:167]\n" \
"v_exp_f32_e32 v89, v89\n" \
"v_mfma_f32_16x16x32_bf16 v[136:139], a[168:171], v[68:71], v[136:139]\n" \
"buffer_load_dwordx4 v50, s[8:11], s67 offen offset:3072 sc1 lds\n" \
"v_mfma_f32_16x16x32_bf16 v[168:171], a[168:171], v[72:75], v[168:171]\n" \
"v_exp_f32_e32 v90, v90\n" \
"v_mfma_f32_16x16x32_bf16 v[140:143], a[172:175], v[68:71], v[140:143]\n" \
"v_exp_f32_e32 v91, v91\n" \
"v_mfma_f32_16x16x32_bf16 v[172:175], a[172:175], v[72:75], v[172:175]\n" \
"v_cvt_pk_bf16_f32 v88, v88, v89\n" \
"v_cvt_pk_bf16_f32 v89, v90, v91\n" \
"v_mfma_f32_16x16x32_bf16 v[144:147], a[176:179], v[68:71], v[144:147]\n" \
"v_exp_f32_e32 v92, v92\n" \
"v_mfma_f32_16x16x32_bf16 v[176:179], a[176:179], v[72:75], v[176:179]\n" \
"v_exp_f32_e32 v93, v93\n" \
"v_mfma_f32_16x16x32_bf16 v[148:151], a[180:183], v[68:71], v[148:151]\n" \
"v_exp_f32_e32 v94, v94\n" \
"v_mfma_f32_16x16x32_bf16 v[180:183], a[180:183], v[72:75], v[180:183]\n" \
"v_exp_f32_e32 v95, v95\n" \
"v_mfma_f32_16x16x32_bf16 v[152:155], a[184:187], v[68:71], v[152:155]\n" \
"v_cvt_pk_bf16_f32 v90, v92, v93\n" \
"v_cvt_pk_bf16_f32 v91, v94, v95\n" \
"v_mfma_f32_16x16x32_bf16 v[184:187], a[184:187], v[72:75], v[184:187]\n" \
"v_add_u32_e32 v51, 0x12000, v63\n" \
"s_waitcnt lgkmcnt(0)\n" \
"v_mfma_f32_16x16x32_bf16 v[156:159], a[188:191], v[68:71], v[156:159]\n" \
"s_xor_b32 m0, s65, 0xa000\n" \
"ds_read_b32 v58, v18 offset:512\n" \
"v_mfma_f32_16x16x32_bf16 v[188:191], a[188:191], v[72:75], v[188:191]\n" \
"buffer_load_dwordx4 v51, s[8:11], s64 offen offset:0 sc1 lds\n" \
"v_smfmac_f32_16x16x64_bf16 a[192:195], a[200:203], v[68:75], v21\n" \
"ds_read_b32 v59, v18 offset:640\n" \
"ds_read_b32 v60, v18 offset:768\n" \
"v_mfma_f32_16x16x32_bf16 v[220:223], a[188:191], v[84:87], v[220:223]\n" \
"ds_read_b32 v61, v18 offset:896\n" \
"ds_read_b128 a[128:131], v48 offset:0\n" \
"v_mfma_f32_16x16x32_bf16 v[252:255], a[188:191], v[88:91], v[252:255]\n" \
"ds_read_b128 a[144:147], v48 offset:4096\n" \
"ds_read_b128 a[132:135], v48 offset:1024\n" \
"v_mfma_f32_16x16x32_bf16 v[216:219], a[184:187], v[84:87], v[216:219]\n" \
"buffer_load_dwordx4 v51, s[8:11], s64 offen offset:1024 sc1 lds\n" \
"v_mfma_f32_16x16x32_bf16 v[248:251], a[184:187], v[88:91], v[248:251]\n" \
"ds_read_b128 a[148:151], v48 offset:5120\n" \
"ds_read_b128 a[136:139], v48 offset:2048\n" \
"v_mfma_f32_16x16x32_bf16 v[212:215], a[180:183], v[84:87], v[212:215]\n" \
"ds_read_b128 a[152:155], v48 offset:6144\n" \
"ds_read_b128 a[140:143], v48 offset:3072\n" \
"v_mfma_f32_16x16x32_bf16 v[244:247], a[180:183], v[88:91], v[244:247]\n" \
"ds_read_b128 a[156:159], v48 offset:7168\n" \
"v_add_u32_e32 v52, 0x14000, v63\n" \
"v_mfma_f32_16x16x32_bf16 v[208:211], a[176:179], v[84:87], v[208:211]\n" \
"buffer_load_dwordx4 v51, s[8:11], s64 offen offset:2048 sc1 lds\n" \
"v_mfma_f32_16x16x32_bf16 v[240:243], a[176:179], v[88:91], v[240:243]\n" \
"v_exp_f32_e32 v104, v104\n" \
"v_mfma_f32_16x16x32_bf16 v[204:207], a[172:175], v[84:87], v[204:207]\n" \
"v_exp_f32_e32 v105, v105\n" \
"v_mfma_f32_16x16x32_bf16 v[236:239], a[172:175], v[88:91], v[236:239]\n" \
"v_exp_f32_e32 v106, v106\n" \
"v_mfma_f32_16x16x32_bf16 v[200:203], a[168:171], v[84:87], v[200:203]\n" \
"buffer_load_dwordx4 v51, s[8:11], s64 offen offset:3072 sc1 lds\n" \
"v_mfma_f32_16x16x32_bf16 v[232:235], a[168:171], v[88:91], v[232:235]\n" \
"v_exp_f32_e32 v107, v107\n" \
"v_mfma_f32_16x16x32_bf16 v[196:199], a[164:167], v[84:87], v[196:199]\n" \
"v_cvt_pk_bf16_f32 v104, v104, v105\n" \
"v_cvt_pk_bf16_f32 v105, v106, v107\n" \
"v_mfma_f32_16x16x32_bf16 v[228:231], a[164:167], v[88:91], v[228:231]\n" \
"v_exp_f32_e32 v108, v108\n" \
"v_mfma_f32_16x16x32_bf16 v[192:195], a[160:163], v[84:87], v[192:195]\n" \
"v_exp_f32_e32 v109, v109\n" \
"v_mfma_f32_16x16x32_bf16 v[224:227], a[160:163], v[88:91], v[224:227]\n" \
"v_exp_f32_e32 v110, v110\n" \
"v_smfmac_f32_16x16x64_bf16 a[196:199], a[200:203], v[84:91], v21\n" \
"v_exp_f32_e32 v111, v111\n" \
"v_cvt_pk_bf16_f32 v106, v108, v109\n" \
"v_cvt_pk_bf16_f32 v107, v110, v111\n" \
"v_and_b32_e32 v104, v12, v104\n" \
"v_and_b32_e32 v105, v13, v105\n" \
"v_and_b32_e32 v106, v14, v106\n" \
"v_and_b32_e32 v107, v15, v107\n" \
"v_accvgpr_read_b32 v54, a192\n" \
"v_accvgpr_read_b32 v22, a193\n" \
"v_readlane_b32 s93, v11, 19\n" \
"v_readlane_b32 s80, v11, 11\n" \
"v_readlane_b32 s81, v11, 12\n" \
"s_mov_b32 s82, 0x47fca0\n" \
"s_mov_b32 s83, 0x20000\n" \
"v_readlane_b32 s92, v11, 26\n" \
"v_exp_f32_e32 v118, v118\n" \
"v_exp_f32_e32 v119, v119\n" \
"v_exp_f32_e32 v116, v116\n" \
"v_exp_f32_e32 v117, v117\n" \
"v_cvt_pk_bf16_f32 v119, v118, v119\n" \
"v_exp_f32_e32 v114, v114\n" \
"v_exp_f32_e32 v115, v115\n" \
"v_exp_f32_e32 v112, v112\n" \
"v_cvt_pk_bf16_f32 v118, v116, v117\n" \
"s_waitcnt vmcnt(10) lgkmcnt(0)\n" \
"s_barrier\n" \
"v_mfma_f32_16x16x32_bf16 v[80:83], a[128:131], a[32:35], v[40:43]\n" \
"ds_read_b128 a[160:163], v49 offset:8192\n" \
"ds_read_b128 a[164:167], v49 offset:9216\n" \
"v_mfma_f32_16x16x32_bf16 v[80:83], a[132:135], a[36:39], v[80:83]\n" \
"ds_read_b128 a[168:171], v49 offset:10240\n" \
"s_xor_b32 m0, s65, 0x18000\n" \
"v_mfma_f32_16x16x32_bf16 v[80:83], a[136:139], a[40:43], v[80:83]\n" \
"buffer_load_dwordx4 v52, s[8:11], s64 offen offset:0 sc1 lds\n" \
"v_mfma_f32_16x16x32_bf16 v[80:83], a[140:143], a[44:47], v[80:83]\n" \
"ds_read_b128 a[172:175], v49 offset:11264\n" \
"ds_read_b128 a[176:179], v49 offset:12288\n" \
"v_mfma_f32_16x16x32_bf16 v[84:87], a[156:159], a[44:47], v[40:43]\n" \
"ds_read_b128 a[180:183], v49 offset:13312\n" \
"ds_read_b128 a[184:187], v49 offset:14336\n" \
"v_mfma_f32_16x16x32_bf16 v[84:87], a[152:155], a[40:43], v[84:87]\n" \
"ds_read_b128 a[188:191], v49 offset:15360\n" \
"v_cvt_pk_bf16_f32 v117, v114, v115\n" \
"v_mfma_f32_16x16x32_bf16 v[84:87], a[148:151], a[36:39], v[84:87]\n" \
"buffer_load_dwordx4 v52, s[8:11], s64 offen offset:1024 sc1 lds\n" \
"v_mfma_f32_16x16x32_bf16 v[84:87], a[144:147], a[32:35], v[84:87]\n" \
"v_exp_f32_e32 v113, v113\n" \
"v_mfma_f32_16x16x32_bf16 v[92:95], a[144:147], a[48:51], v[44:47]\n" \
"v_cvt_pk_bf16_f32 v116, v112, v113\n" \
"ds_read_b128 a[144:147], v48 offset:12288\n" \
"v_mfma_f32_16x16x32_bf16 v[92:95], a[148:151], a[52:55], v[92:95]\n" \
"v_exp_f32_e32 v120, v120\n" \
"v_mfma_f32_16x16x32_bf16 v[92:95], a[152:155], a[56:59], v[92:95]\n" \
"buffer_load_dwordx4 v52, s[8:11], s64 offen offset:2048 sc1 lds\n" \
"v_mfma_f32_16x16x32_bf16 v[92:95], a[156:159], a[60:63], v[92:95]\n" \
"v_exp_f32_e32 v121, v121\n" \
"v_mfma_f32_16x16x32_bf16 v[88:91], a[140:143], a[60:63], v[44:47]\n" \
"v_exp_f32_e32 v122, v122\n" \
"v_mfma_f32_16x16x32_bf16 v[88:91], a[136:139], a[56:59], v[88:91]\n" \
"v_exp_f32_e32 v123, v123\n" \
"v_mfma_f32_16x16x32_bf16 v[88:91], a[132:135], a[52:55], v[88:91]\n" \
"buffer_load_dwordx4 v52, s[8:11], s64 offen offset:3072 sc1 lds\n" \
"v_mfma_f32_16x16x32_bf16 v[88:91], a[128:131], a[48:51], v[88:91]\n" \
"s_add_u32 m0, s93, 0x0\n" \
"v_cvt_pk_bf16_f32 v120, v120, v121\n" \
"v_mfma_f32_16x16x32_bf16 a[192:195], a[212:215], v[104:107], a[192:195]\n" \
"v_cvt_pk_bf16_f32 v121, v122, v123\n" \
"s_waitcnt lgkmcnt(6)\n" \
"v_mfma_f32_16x16x32_bf16 v[160:163], a[160:163], v[104:107], v[160:163]\n" \
"v_exp_f32_e32 v124, v124\n" \
"v_mfma_f32_16x16x32_bf16 v[164:167], a[164:167], v[104:107], v[164:167]\n" \
"buffer_load_dwordx4 v0, s[80:83], s92 offen offset:0 lds\n" \
"v_mfma_f32_16x16x32_bf16 v[168:171], a[168:171], v[104:107], v[168:171]\n" \
"ds_read_b128 a[128:131], v48 offset:8192\n" \
"s_waitcnt lgkmcnt(2)\n" \
"v_mfma_f32_16x16x32_bf16 v[172:175], a[172:175], v[104:107], v[172:175]\n" \
"v_exp_f32_e32 v125, v125\n" \
"v_mfma_f32_16x16x32_bf16 v[176:179], a[176:179], v[104:107], v[176:179]\n" \
"v_exp_f32_e32 v126, v126\n" \
"buffer_load_dwordx4 v1, s[80:83], s92 offen offset:1024 lds\n" \
"v_mfma_f32_16x16x32_bf16 v[180:183], a[180:183], v[104:107], v[180:183]\n" \
"v_exp_f32_e32 v127, v127\n" \
"v_mfma_f32_16x16x32_bf16 v[184:187], a[184:187], v[104:107], v[184:187]\n" \
"v_cvt_pk_bf16_f32 v122, v124, v125\n" \
"v_cvt_pk_bf16_f32 v123, v126, v127\n" \
"v_mfma_f32_16x16x32_bf16 v[188:191], a[188:191], v[104:107], v[188:191]\n" \
"ds_read_b128 a[132:135], v48 offset:9216\n" \
"ds_read_b128 a[148:151], v48 offset:13312\n" \
"buffer_load_dwordx4 v2, s[80:83], s92 offen offset:2048 lds\n" \
"v_mfma_f32_16x16x32_bf16 v[220:223], a[188:191], v[116:119], v[220:223]\n" \
"ds_read_b128 a[136:139], v48 offset:10240\n" \
"ds_read_b128 a[152:155], v48 offset:14336\n" \
"v_mfma_f32_16x16x32_bf16 v[252:255], a[188:191], v[120:123], v[252:255]\n" \
"ds_read_b128 a[140:143], v48 offset:11264\n" \
"ds_read_b128 a[156:159], v48 offset:15360\n" \
"v_mfma_f32_16x16x32_bf16 v[216:219], a[184:187], v[116:119], v[216:219]\n" \
"v_readlane_b32 s96, v11, 1\n" \
"v_bfe_u32 v27, v20, 2, 7\n" \
"buffer_load_dwordx4 v3, s[80:83], s92 offen offset:3072 lds\n" \
"v_mfma_f32_16x16x32_bf16 v[248:251], a[184:187], v[120:123], v[248:251]\n" \
"s_add_u32 m0, s93, 0x1000\n" \
"v_add_f32_e32 v54, v54, v22\n" \
"v_mfma_f32_16x16x32_bf16 v[212:215], a[180:183], v[116:119], v[212:215]\n" \
"v_max_f32_e32 v62, v62, v54\n" \
"v_add_u32_e32 v49, 0x10000, v48\n" \
"v_mfma_f32_16x16x32_bf16 v[244:247], a[180:183], v[120:123], v[244:247]\n" \
"v_rcp_f32_e32 v54, v54\n" \
"buffer_load_dwordx4 v4, s[80:83], s92 offen offset:0 lds\n" \
"v_mfma_f32_16x16x32_bf16 v[208:211], a[176:179], v[116:119], v[208:211]\n" \
"v_mul_f32_e32 v54, v54, v58\n" \
"v_mul_f32_e32 v128, v54, v128\n" \
"v_mfma_f32_16x16x32_bf16 v[240:243], a[176:179], v[120:123], v[240:243]\n" \
"v_mul_f32_e32 v129, v54, v129\n" \
"v_mul_f32_e32 v136, v54, v136\n" \
"v_mfma_f32_16x16x32_bf16 v[204:207], a[172:175], v[116:119], v[204:207]\n" \
"v_mul_f32_e32 v137, v54, v137\n" \
"v_mul_f32_e32 v144, v54, v144\n" \
"buffer_load_dwordx4 v5, s[80:83], s92 offen offset:1024 lds\n" \
"v_mfma_f32_16x16x32_bf16 v[236:239], a[172:175], v[120:123], v[236:239]\n" \
"v_mul_f32_e32 v145, v54, v145\n" \
"v_mul_f32_e32 v152, v54, v152\n" \
"v_mfma_f32_16x16x32_bf16 v[200:203], a[168:171], v[116:119], v[200:203]\n" \
"v_mul_f32_e32 v153, v54, v153\n" \
"v_cvt_pk_bf16_f32 v128, v128, v129\n" \
"v_mfma_f32_16x16x32_bf16 v[232:235], a[168:171], v[120:123], v[232:235]\n" \
"v_mul_f32_e32 v130, v54, v130\n" \
"v_mul_f32_e32 v131, v54, v131\n" \
"buffer_load_dwordx4 v6, s[80:83], s92 offen offset:2048 lds\n" \
"v_mfma_f32_16x16x32_bf16 v[196:199], a[164:167], v[116:119], v[196:199]\n" \
"v_cvt_pk_bf16_f32 v136, v136, v137\n" \
"v_mul_f32_e32 v138, v54, v138\n" \
"v_mfma_f32_16x16x32_bf16 v[228:231], a[164:167], v[120:123], v[228:231]\n" \
"v_mul_f32_e32 v139, v54, v139\n" \
"v_cvt_pk_bf16_f32 v144, v144, v145\n" \
"v_mfma_f32_16x16x32_bf16 v[192:195], a[160:163], v[116:119], v[192:195]\n" \
"v_mul_f32_e32 v146, v54, v146\n" \
"v_mul_f32_e32 v147, v54, v147\n" \
"buffer_load_dwordx4 v7, s[80:83], s92 offen offset:3072 lds\n" \
"v_mfma_f32_16x16x32_bf16 v[224:227], a[160:163], v[120:123], v[224:227]\n" \
"v_cvt_pk_bf16_f32 v152, v152, v153\n" \
"v_mul_f32_e32 v154, v54, v154\n" \
"v_smfmac_f32_16x16x64_bf16 a[196:199], a[200:203], v[116:123], v21\n" \
"v_mul_f32_e32 v155, v54, v155\n" \
"v_cvt_pk_bf16_f32 v129, v130, v131\n" \
"v_mul_f32_e32 v132, v54, v132\n" \
"v_mul_f32_e32 v133, v54, v133\n" \
"v_mul_f32_e32 v134, v54, v134\n" \
"v_mul_f32_e32 v135, v54, v135\n" \
"v_cvt_pk_bf16_f32 v137, v138, v139\n" \
"v_mul_f32_e32 v140, v54, v140\n" \
"v_mul_f32_e32 v141, v54, v141\n" \
"v_mul_f32_e32 v142, v54, v142\n" \
"v_mul_f32_e32 v143, v54, v143\n" \
"v_cvt_pk_bf16_f32 v145, v146, v147\n" \
"v_mul_f32_e32 v148, v54, v148\n" \
"v_mul_f32_e32 v149, v54, v149\n" \
"v_mul_f32_e32 v150, v54, v150\n" \
"v_mul_f32_e32 v151, v54, v151\n" \
"v_cvt_pk_bf16_f32 v153, v154, v155\n" \
"v_mul_f32_e32 v156, v54, v156\n" \
"v_mul_f32_e32 v157, v54, v157\n" \
"v_mul_f32_e32 v158, v54, v158\n" \
"v_mul_f32_e32 v159, v54, v159\n" \
"v_cvt_pk_bf16_f32 v130, v132, v133\n" \
"v_cvt_pk_bf16_f32 v131, v134, v135\n" \
"buffer_store_dwordx4 v[128:131], v20, s[56:59], s68 offen offset:0 sc1\n" \
"v_cvt_pk_bf16_f32 v138, v140, v141\n" \
"v_cvt_pk_bf16_f32 v139, v142, v143\n" \
"v_cvt_pk_bf16_f32 v146, v148, v149\n" \
"v_cvt_pk_bf16_f32 v147, v150, v151\n" \
"v_cvt_pk_bf16_f32 v154, v156, v157\n" \
"v_cvt_pk_bf16_f32 v155, v158, v159\n" \
"s_add_u32 m0, s93, 0x2000\n" \
"s_xor_b32 s65, s65, 0x8000\n" \
"s_add_u32 s64, s64, 0x10000\n" \
"v_exp_f32_e32 v86, v86\n" \
"buffer_store_dwordx4 v[136:139], v20, s[56:59], s68 offen offset:1024 sc1\n" \
"v_exp_f32_e32 v87, v87\n" \
"v_exp_f32_e32 v84, v84\n" \
"v_exp_f32_e32 v85, v85\n" \
"v_exp_f32_e32 v88, v88\n" \
"v_exp_f32_e32 v89, v89\n" \
"v_cvt_pk_bf16_f32 v87, v86, v87\n" \
"buffer_store_dwordx4 v[144:147], v20, s[56:59], s68 offen offset:2048 sc1\n" \
"v_exp_f32_e32 v82, v82\n" \
"v_exp_f32_e32 v83, v83\n" \
"v_exp_f32_e32 v80, v80\n" \
"v_exp_f32_e32 v81, v81\n" \
"v_exp_f32_e32 v90, v90\n" \
"v_exp_f32_e32 v91, v91\n" \
"buffer_store_dwordx4 v[152:155], v20, s[56:59], s68 offen offset:3072 sc1\n" \
"v_cvt_pk_bf16_f32 v86, v84, v85\n" \
"v_cvt_pk_bf16_f32 v88, v88, v89\n" \
"v_exp_f32_e32 v92, v92\n" \
"v_exp_f32_e32 v93, v93\n" \
"v_exp_f32_e32 v94, v94\n" \
"v_exp_f32_e32 v95, v95\n" \
"v_cvt_pk_bf16_f32 v85, v82, v83\n" \
"buffer_load_dwordx4 v8, s[80:83], s92 offen offset:0 lds\n" \
"v_cvt_pk_bf16_f32 v84, v80, v81\n" \
"v_cvt_pk_bf16_f32 v89, v90, v91\n" \
"v_and_b32_e32 v84, v12, v84\n" \
"v_and_b32_e32 v85, v13, v85\n" \
"v_and_b32_e32 v86, v14, v86\n" \
"v_and_b32_e32 v87, v15, v87\n" \
"s_waitcnt vmcnt(17) lgkmcnt(0)\n" \
"s_barrier\n" \
"v_mfma_f32_16x16x32_bf16 v[120:123], a[128:131], a[48:51], v[44:47]\n" \
"buffer_load_dword v26, v27, s[56:59], s96 offen sc1\n" \
"v_mfma_f32_16x16x32_bf16 v[120:123], a[132:135], a[52:55], v[120:123]\n" \
"ds_read_b128 a[188:191], v49 offset:7168\n" \
"ds_read_b128 a[184:187], v49 offset:6144\n" \
"v_mfma_f32_16x16x32_bf16 v[120:123], a[136:139], a[56:59], v[120:123]\n" \
"s_xor_b32 m0, s65, 0x12000\n" \
"ds_read_b128 a[180:183], v49 offset:5120\n" \
"v_mfma_f32_16x16x32_bf16 v[120:123], a[140:143], a[60:63], v[120:123]\n" \
"ds_read_b128 a[176:179], v49 offset:4096\n" \
"ds_read_b128 a[172:175], v49 offset:3072\n" \
"v_mfma_f32_16x16x32_bf16 v[124:127], a[156:159], a[60:63], v[44:47]\n" \
"buffer_load_dwordx4 v50, s[8:11], s64 offen offset:0 sc1 lds\n" \
"v_mfma_f32_16x16x32_bf16 v[124:127], a[152:155], a[56:59], v[124:127]\n" \
"ds_read_b128 a[168:171], v49 offset:2048\n" \
"v_cvt_pk_bf16_f32 v90, v92, v93\n" \
"v_mfma_f32_16x16x32_bf16 v[124:127], a[148:151], a[52:55], v[124:127]\n" \
"v_cvt_pk_bf16_f32 v91, v94, v95\n" \
"ds_read_b128 a[164:167], v49 offset:1024\n" \
"v_mfma_f32_16x16x32_bf16 v[124:127], a[144:147], a[48:51], v[124:127]\n" \
"ds_read_b128 a[160:163], v49 offset:0\n" \
"ds_read_b128 a[0:3], v49 offset:8192\n" \
"v_mfma_f32_16x16x32_bf16 v[32:35], a[220:223], a[64:67], 0\n" \
"buffer_load_dwordx4 v50, s[8:11], s64 offen offset:1024 sc1 lds\n" \
"v_mfma_f32_16x16x32_bf16 v[36:39], a[220:223], a[80:83], 0\n" \
"ds_read_b128 a[4:7], v49 offset:9216\n" \
"ds_read_b128 a[8:11], v49 offset:10240\n" \
"v_mfma_f32_16x16x32_bf16 v[32:35], a[224:227], a[68:71], v[32:35]\n" \
"ds_read_b128 a[12:15], v49 offset:11264\n" \
"ds_read_b128 a[16:19], v49 offset:12288\n" \
"v_mfma_f32_16x16x32_bf16 v[36:39], a[224:227], a[84:87], v[36:39]\n" \
"ds_read_b128 a[20:23], v49 offset:13312\n" \
"ds_read_b128 a[24:27], v49 offset:14336\n" \
"v_mfma_f32_16x16x32_bf16 v[32:35], a[228:231], a[72:75], v[32:35]\n" \
"buffer_load_dwordx4 v50, s[8:11], s64 offen offset:2048 sc1 lds\n" \
"v_mfma_f32_16x16x32_bf16 v[36:39], a[228:231], a[88:91], v[36:39]\n" \
"ds_read_b128 a[28:31], v49 offset:15360\n" \
"v_readlane_b32 s97, v11, 36\n" \
"v_mfma_f32_16x16x32_bf16 v[32:35], a[232:235], a[76:79], v[32:35]\n" \
"v_exp_f32_e32 v120, v120\n" \
"v_mfma_f32_16x16x32_bf16 v[36:39], a[232:235], a[92:95], v[36:39]\n" \
"v_exp_f32_e32 v121, v121\n" \
"v_smfmac_f32_16x16x64_bf16 a[196:199], a[200:203], v[84:91], v21\n" \
"v_cvt_pk_bf16_f32 v120, v120, v121\n" \
"s_waitcnt lgkmcnt(13)\n" \
"v_mfma_f32_16x16x32_bf16 v[220:223], a[188:191], v[84:87], v[220:223]\n" \
"buffer_load_dwordx4 v50, s[8:11], s64 offen offset:3072 sc1 lds\n" \
"v_mfma_f32_16x16x32_bf16 v[252:255], a[188:191], v[88:91], v[252:255]\n" \
"v_exp_f32_e32 v122, v122\n" \
"v_mfma_f32_16x16x32_bf16 v[216:219], a[184:187], v[84:87], v[216:219]\n" \
"v_exp_f32_e32 v123, v123\n" \
"v_mfma_f32_16x16x32_bf16 v[248:251], a[184:187], v[88:91], v[248:251]\n" \
"v_exp_f32_e32 v124, v124\n" \
"v_mfma_f32_16x16x32_bf16 v[212:215], a[180:183], v[84:87], v[212:215]\n" \
"v_exp_f32_e32 v125, v125\n" \
"v_mfma_f32_16x16x32_bf16 v[244:247], a[180:183], v[88:91], v[244:247]\n" \
"v_cvt_pk_bf16_f32 v121, v122, v123\n" \
"s_waitcnt lgkmcnt(7)\n" \
"v_mfma_f32_16x16x32_bf16 v[208:211], a[176:179], v[84:87], v[208:211]\n" \
"v_exp_f32_e32 v126, v126\n" \
"v_mfma_f32_16x16x32_bf16 v[240:243], a[176:179], v[88:91], v[240:243]\n" \
"v_exp_f32_e32 v127, v127\n" \
"v_mfma_f32_16x16x32_bf16 v[204:207], a[172:175], v[84:87], v[204:207]\n" \
"v_cvt_pk_bf16_f32 v122, v124, v125\n" \
"v_cvt_pk_bf16_f32 v123, v126, v127\n" \
"v_mfma_f32_16x16x32_bf16 v[236:239], a[172:175], v[88:91], v[236:239]\n" \
"v_and_b32_e32 v120, v12, v120\n" \
"v_and_b32_e32 v121, v13, v121\n" \
"v_mfma_f32_16x16x32_bf16 v[200:203], a[168:171], v[84:87], v[200:203]\n" \
"v_and_b32_e32 v122, v14, v122\n" \
"v_and_b32_e32 v123, v15, v123\n" \
"v_mfma_f32_16x16x32_bf16 v[232:235], a[168:171], v[88:91], v[232:235]\n" \
"v_accvgpr_read_b32 v56, a196\n" \
"v_accvgpr_read_b32 v24, a197\n" \
"v_mfma_f32_16x16x32_bf16 v[196:199], a[164:167], v[84:87], v[196:199]\n" \
"v_add_u32_e32 v17, 0x49000, v20\n" \
"v_readlane_b32 s98, v11, 37\n" \
"v_mfma_f32_16x16x32_bf16 v[228:231], a[164:167], v[88:91], v[228:231]\n" \
"v_add_u32_e32 v48, s16, v20\n" \
"ds_read_b128 a[128:131], v48 offset:0\n" \
"v_mfma_f32_16x16x32_bf16 v[192:195], a[160:163], v[84:87], v[192:195]\n" \
"ds_read_b128 a[144:147], v48 offset:4096\n" \
"ds_read_b128 a[132:135], v48 offset:1024\n" \
"v_mfma_f32_16x16x32_bf16 v[224:227], a[160:163], v[88:91], v[224:227]\n" \
"ds_read_b128 a[148:151], v48 offset:5120\n" \
"ds_read_b128 a[136:139], v48 offset:2048\n" \
"v_mfma_f32_16x16x32_bf16 a[196:199], a[212:215], v[120:123], a[196:199]\n" \
"ds_read_b128 a[152:155], v48 offset:6144\n" \
"ds_read_b128 a[140:143], v48 offset:3072\n" \
"v_mfma_f32_16x16x32_bf16 v[224:227], a[0:3], v[120:123], v[224:227]\n" \
"ds_read_b128 a[156:159], v48 offset:7168\n" \
"s_waitcnt lgkmcnt(8)\n" \
"v_mfma_f32_16x16x32_bf16 v[228:231], a[4:7], v[120:123], v[228:231]\n" \
"v_accvgpr_read_b32 v55, a194\n" \
"v_accvgpr_read_b32 v23, a195\n" \
"v_mfma_f32_16x16x32_bf16 a[192:195], a[216:219], a[216:219], 0\n" \
"v_add_f32_e32 v55, v55, v23\n" \
"v_max_f32_e32 v62, v62, v55\n" \
"v_mfma_f32_16x16x32_bf16 v[232:235], a[8:11], v[120:123], v[232:235]\n" \
"v_rcp_f32_e32 v55, v55\n" \
"v_mfma_f32_16x16x32_bf16 v[236:239], a[12:15], v[120:123], v[236:239]\n" \
"v_mul_f32_e32 v55, v55, v59\n" \
"v_mul_f32_e32 v160, v55, v160\n" \
"v_mfma_f32_16x16x32_bf16 v[240:243], a[16:19], v[120:123], v[240:243]\n" \
"v_mul_f32_e32 v161, v55, v161\n" \
"v_mul_f32_e32 v168, v55, v168\n" \
"v_mfma_f32_16x16x32_bf16 v[244:247], a[20:23], v[120:123], v[244:247]\n" \
"v_mul_f32_e32 v169, v55, v169\n" \
"v_mul_f32_e32 v176, v55, v176\n" \
"v_mfma_f32_16x16x32_bf16 v[248:251], a[24:27], v[120:123], v[248:251]\n" \
"v_mul_f32_e32 v177, v55, v177\n" \
"v_mul_f32_e32 v184, v55, v184\n" \
"v_mfma_f32_16x16x32_bf16 v[252:255], a[28:31], v[120:123], v[252:255]\n" \
"v_mul_f32_e32 v185, v55, v185\n" \
"v_cvt_pk_bf16_f32 v160, v160, v161\n" \
"v_mul_f32_e32 v162, v55, v162\n" \
"v_mul_f32_e32 v163, v55, v163\n" \
"v_cvt_pk_bf16_f32 v168, v168, v169\n" \
"v_mul_f32_e32 v170, v55, v170\n" \
"v_mul_f32_e32 v171, v55, v171\n" \
"v_cvt_pk_bf16_f32 v176, v176, v177\n" \
"v_mul_f32_e32 v178, v55, v178\n" \
"s_waitcnt vmcnt(4)\n" \
"v_cmp_ne_u32_e32 vcc, s62, v26\n" \
"v_mul_f32_e32 v179, v55, v179\n" \
"v_cvt_pk_bf16_f32 v184, v184, v185\n" \
"v_mul_f32_e32 v186, v55, v186\n" \
"v_mul_f32_e32 v187, v55, v187\n" \
"v_cvt_pk_bf16_f32 v161, v162, v163\n" \
"s_cbranch_vccnz L_sos_%=\n" \
"L_ros_%=:\n" \
"buffer_load_dwordx4 a[220:223], v20, s[56:59], s97 offen offset:0 sc1\n" \
"v_mul_f32_e32 v164, v55, v164\n" \
"v_mul_f32_e32 v165, v55, v165\n" \
"v_mul_f32_e32 v166, v55, v166\n" \
"v_mul_f32_e32 v167, v55, v167\n" \
"v_cvt_pk_bf16_f32 v169, v170, v171\n" \
"v_mul_f32_e32 v172, v55, v172\n" \
"v_mul_f32_e32 v173, v55, v173\n" \
"v_mul_f32_e32 v174, v55, v174\n" \
"v_mul_f32_e32 v175, v55, v175\n" \
"v_cvt_pk_bf16_f32 v177, v178, v179\n" \
"v_mul_f32_e32 v180, v55, v180\n" \
"buffer_load_dwordx4 a[228:231], v17, s[56:59], s97 offen offset:0 sc1\n" \
"v_mul_f32_e32 v181, v55, v181\n" \
"v_mul_f32_e32 v182, v55, v182\n" \
"v_mul_f32_e32 v183, v55, v183\n" \
"v_cvt_pk_bf16_f32 v185, v186, v187\n" \
"v_mul_f32_e32 v188, v55, v188\n" \
"v_mul_f32_e32 v189, v55, v189\n" \
"v_mul_f32_e32 v190, v55, v190\n" \
"v_mul_f32_e32 v191, v55, v191\n" \
"v_cvt_pk_bf16_f32 v162, v164, v165\n" \
"v_cvt_pk_bf16_f32 v163, v166, v167\n" \
"v_cvt_pk_bf16_f32 v170, v172, v173\n" \
"buffer_store_dwordx4 v[160:163], v20, s[56:59], s69 offen offset:0 sc1\n" \
"v_cvt_pk_bf16_f32 v171, v174, v175\n" \
"v_cvt_pk_bf16_f32 v178, v180, v181\n" \
"v_cvt_pk_bf16_f32 v179, v182, v183\n" \
"v_cvt_pk_bf16_f32 v186, v188, v189\n" \
"v_cvt_pk_bf16_f32 v187, v190, v191\n" \
"v_readlane_b32 s93, v11, 20\n" \
"v_readlane_b32 s84, v11, 15\n" \
"v_readlane_b32 s85, v11, 16\n" \
"v_readlane_b32 s86, v11, 17\n" \
"s_mov_b32 s87, 0x20000\n" \
"v_readlane_b32 s92, v11, 18\n" \
"buffer_store_dwordx4 v[168:171], v20, s[56:59], s69 offen offset:1024 sc1\n" \
"v_readlane_b32 s91, v11, 21\n" \
"v_mfma_f32_16x16x32_bf16 a[208:211], a[216:219], v[128:131], a[208:211]\n" \
"v_mfma_f32_16x16x32_bf16 a[208:211], a[216:219], v[136:139], a[208:211]\n" \
"v_mfma_f32_32x32x16_bf16 v[128:143], a[216:219], a[216:219], 0\n" \
"buffer_store_dwordx4 v[176:179], v20, s[56:59], s69 offen offset:2048 sc1\n" \
"v_mfma_f32_16x16x32_bf16 a[208:211], a[216:219], v[144:147], a[208:211]\n" \
"v_mfma_f32_16x16x32_bf16 a[208:211], a[216:219], v[152:155], a[208:211]\n" \
"v_mfma_f32_32x32x16_bf16 v[144:159], a[216:219], a[216:219], 0\n" \
"buffer_store_dwordx4 v[184:187], v20, s[56:59], s69 offen offset:3072 sc1\n" \
"v_add_f32_e32 v56, v56, v24\n" \
"v_mfma_f32_16x16x32_bf16 a[208:211], a[216:219], v[160:163], a[208:211]\n" \
"v_max_f32_e32 v62, v62, v56\n" \
"v_rcp_f32_e32 v56, v56\n" \
"v_mfma_f32_16x16x32_bf16 a[208:211], a[216:219], v[168:171], a[208:211]\n" \
"v_mfma_f32_32x32x16_bf16 v[160:175], a[216:219], a[216:219], 0\n" \
"buffer_load_dwordx4 a[236:239], v20, s[56:59], s98 offen offset:0 sc1\n" \
"v_mul_f32_e32 v56, v56, v60\n" \
"v_mfma_f32_16x16x32_bf16 a[208:211], a[216:219], v[176:179], a[208:211]\n" \
"v_mul_f32_e32 v192, v56, v192\n" \
"v_mul_f32_e32 v193, v56, v193\n" \
"v_cvt_pk_bf16_f32 v192, v192, v193\n" \
"v_mul_f32_e32 v194, v56, v194\n" \
"v_mul_f32_e32 v195, v56, v195\n" \
"buffer_load_dwordx4 a[244:247], v17, s[56:59], s98 offen offset:0 sc1\n" \
"v_mfma_f32_16x16x32_bf16 a[208:211], a[216:219], v[184:187], a[208:211]\n" \
"v_mfma_f32_32x32x16_bf16 v[176:191], a[216:219], a[216:219], 0\n" \
"v_cvt_pk_bf16_f32 v193, v194, v195\n" \
"v_mul_f32_e32 v196, v56, v196\n" \
"v_mul_f32_e32 v197, v56, v197\n" \
"v_mul_f32_e32 v198, v56, v198\n" \
"v_mul_f32_e32 v199, v56, v199\n" \
"buffer_load_dwordx4 a[224:227], v20, s[56:59], s97 offen offset:1024 sc1\n" \
"v_mul_f32_e32 v200, v56, v200\n" \
"v_mul_f32_e32 v201, v56, v201\n" \
"v_accvgpr_read_b32 v57, a198\n" \
"v_accvgpr_read_b32 v25, a199\n" \
"v_mfma_f32_16x16x32_bf16 a[196:199], a[216:219], a[216:219], 0\n" \
"v_cvt_pk_bf16_f32 v194, v196, v197\n" \
"v_cvt_pk_bf16_f32 v195, v198, v199\n" \
"v_cvt_pk_bf16_f32 v200, v200, v201\n" \
"v_mul_f32_e32 v202, v56, v202\n" \
"v_mul_f32_e32 v203, v56, v203\n" \
"buffer_store_dwordx4 v[192:195], v20, s[56:59], s70 offen offset:0 sc1\n" \
"v_add_f32_e32 v57, v57, v25\n" \
"v_mfma_f32_16x16x32_bf16 a[208:211], a[216:219], v[192:195], a[208:211]\n" \
"v_cvt_pk_bf16_f32 v201, v202, v203\n" \
"v_mul_f32_e32 v204, v56, v204\n" \
"v_mul_f32_e32 v205, v56, v205\n" \
"v_mul_f32_e32 v206, v56, v206\n" \
"v_mul_f32_e32 v207, v56, v207\n" \
"v_mul_f32_e32 v208, v56, v208\n" \
"v_mul_f32_e32 v209, v56, v209\n" \
"v_max_f32_e32 v62, v62, v57\n" \
"buffer_load_dwordx4 a[232:235], v17, s[56:59], s97 offen offset:1024 sc1\n" \
"s_waitcnt vmcnt(16) lgkmcnt(0)\n" \
"s_barrier\n" \
"v_mfma_f32_16x16x32_bf16 v[64:67], a[128:131], a[64:67], v[32:35]\n" \
"s_add_u32 m0, s93, 0x0\n" \
"v_cvt_pk_bf16_f32 v202, v204, v205\n" \
"v_mfma_f32_16x16x32_bf16 v[64:67], a[132:135], a[68:71], v[64:67]\n" \
"buffer_load_dwordx4 a[240:243], v20, s[56:59], s98 offen offset:1024 sc1\n" \
"v_mfma_f32_16x16x32_bf16 v[64:67], a[136:139], a[72:75], v[64:67]\n" \
"v_cvt_pk_bf16_f32 v203, v206, v207\n" \
"v_cvt_pk_bf16_f32 v208, v208, v209\n" \
"v_mfma_f32_16x16x32_bf16 v[64:67], a[140:143], a[76:79], v[64:67]\n" \
"v_mul_f32_e32 v210, v56, v210\n" \
"v_mul_f32_e32 v211, v56, v211\n" \
"v_mfma_f32_16x16x32_bf16 v[68:71], a[156:159], a[76:79], v[32:35]\n" \
"v_rcp_f32_e32 v57, v57\n" \
"buffer_store_dwordx4 v[200:203], v20, s[56:59], s70 offen offset:1024 sc1\n" \
"v_mfma_f32_16x16x32_bf16 v[68:71], a[152:155], a[72:75], v[68:71]\n" \
"v_mfma_f32_16x16x32_bf16 a[208:211], a[216:219], v[200:203], a[208:211]\n" \
"v_mfma_f32_32x32x16_bf16 v[192:207], a[216:219], a[216:219], 0\n" \
"v_cvt_pk_bf16_f32 v209, v210, v211\n" \
"buffer_load_dwordx4 a[248:251], v17, s[56:59], s98 offen offset:1024 sc1\n" \
"v_mul_f32_e32 v212, v56, v212\n" \
"v_mul_f32_e32 v213, v56, v213\n" \
"v_mul_f32_e32 v214, v56, v214\n" \
"v_mfma_f32_16x16x32_bf16 v[68:71], a[148:151], a[68:71], v[68:71]\n" \
"v_mul_f32_e32 v215, v56, v215\n" \
"v_mul_f32_e32 v216, v56, v216\n" \
"v_mfma_f32_16x16x32_bf16 v[68:71], a[144:147], a[64:67], v[68:71]\n" \
"v_mul_f32_e32 v217, v56, v217\n" \
"v_cvt_pk_bf16_f32 v210, v212, v213\n" \
"buffer_load_dwordx4 v0, s[84:87], s92 offen offset:0 lds\n" \
"v_mfma_f32_16x16x32_bf16 v[76:79], a[144:147], a[80:83], v[36:39]\n" \
"ds_read_b128 a[144:147], v48 offset:12288\n" \
"v_cvt_pk_bf16_f32 v211, v214, v215\n" \
"v_mfma_f32_16x16x32_bf16 v[76:79], a[148:151], a[84:87], v[76:79]\n" \
"ds_read_b128 a[148:151], v48 offset:13312\n" \
"v_cvt_pk_bf16_f32 v216, v216, v217\n" \
"v_mfma_f32_16x16x32_bf16 v[76:79], a[152:155], a[88:91], v[76:79]\n" \
"ds_read_b128 a[152:155], v48 offset:14336\n" \
"v_mul_f32_e32 v218, v56, v218\n" \
"buffer_store_dwordx4 v[208:211], v20, s[56:59], s70 offen offset:2048 sc1\n" \
"v_mfma_f32_16x16x32_bf16 v[76:79], a[156:159], a[92:95], v[76:79]\n" \
"ds_read_b128 a[156:159], v48 offset:15360\n" \
"v_mul_f32_e32 v219, v56, v219\n" \
"v_mfma_f32_16x16x32_bf16 v[72:75], a[140:143], a[92:95], v[36:39]\n" \
"ds_read_b128 a[140:143], v48 offset:11264\n" \
"v_mul_f32_e32 v57, v57, v61\n" \
"v_mfma_f32_16x16x32_bf16 v[72:75], a[136:139], a[88:91], v[72:75]\n" \
".fill (L_ab + 28672 - .) / 4, 4, 0xBF800000\n" \
"L_ap7:\n" \
"s_cbranch_execz L_ap0\n" \
"ds_read_b128 a[136:139], v48 offset:10240\n" \
"v_cvt_pk_bf16_f32 v217, v218, v219\n" \
"buffer_load_dwordx4 v1, s[84:87], s92 offen offset:1024 lds\n" \
"v_mfma_f32_16x16x32_bf16 v[72:75], a[132:135], a[84:87], v[72:75]\n" \
"ds_read_b128 a[132:135], v48 offset:9216\n" \
"v_mul_f32_e32 v220, v56, v220\n" \
"v_mfma_f32_16x16x32_bf16 v[72:75], a[128:131], a[80:83], v[72:75]\n" \
"ds_read_b128 a[128:131], v48 offset:8192\n" \
"v_mfma_f32_16x16x32_bf16 a[208:211], a[216:219], v[208:211], a[208:211]\n" \
"v_mul_f32_e32 v221, v56, v221\n" \
"buffer_load_dwordx4 v2, s[84:87], s92 offen offset:2048 lds\n" \
"v_mul_f32_e32 v222, v56, v222\n" \
"v_mul_f32_e32 v223, v56, v223\n" \
"v_mul_f32_e32 v224, v57, v224\n" \
"v_mul_f32_e32 v225, v57, v225\n" \
"v_cvt_pk_bf16_f32 v218, v220, v221\n" \
"v_cvt_pk_bf16_f32 v219, v222, v223\n" \
"v_cvt_pk_bf16_f32 v224, v224, v225\n" \
"v_mul_f32_e32 v226, v57, v226\n" \
"v_mul_f32_e32 v227, v57, v227\n" \
"v_mfma_f32_16x16x32_bf16 a[208:211], a[216:219], v[216:219], a[208:211]\n" \
"buffer_store_dwordx4 v[216:219], v20, s[56:59], s70 offen offset:3072 sc1\n" \
"v_cvt_pk_bf16_f32 v225, v226, v227\n" \
"v_mul_f32_e32 v228, v57, v228\n" \
"v_mfma_f32_32x32x16_bf16 v[208:223], a[216:219], a[216:219], 0\n" \
"v_mul_f32_e32 v229, v57, v229\n" \
"v_mul_f32_e32 v230, v57, v230\n" \
"v_mul_f32_e32 v231, v57, v231\n" \
"v_mul_f32_e32 v232, v57, v232\n" \
"v_mul_f32_e32 v233, v57, v233\n" \
"v_cvt_pk_bf16_f32 v226, v228, v229\n" \
"v_cvt_pk_bf16_f32 v227, v230, v231\n" \
"buffer_store_dwordx4 v[224:227], v20, s[56:59], s71 offen offset:0 sc1\n" \
"v_cvt_pk_bf16_f32 v232, v232, v233\n" \
"v_mul_f32_e32 v234, v57, v234\n" \
"v_mul_f32_e32 v235, v57, v235\n" \
"v_mfma_f32_16x16x32_bf16 a[208:211], a[216:219], v[224:227], a[208:211]\n" \
"v_cvt_pk_bf16_f32 v233, v234, v235\n" \
"v_mul_f32_e32 v236, v57, v236\n" \
"v_mul_f32_e32 v237, v57, v237\n" \
"v_mul_f32_e32 v238, v57, v238\n" \
"v_mul_f32_e32 v239, v57, v239\n" \
"v_mul_f32_e32 v240, v57, v240\n" \
"buffer_load_dwordx4 v3, s[84:87], s92 offen offset:3072 lds\n" \
"s_add_u32 m0, s91, 0x1000\n" \
"v_mul_f32_e32 v241, v57, v241\n" \
"v_cvt_pk_bf16_f32 v234, v236, v237\n" \
"v_cvt_pk_bf16_f32 v235, v238, v239\n" \
"v_cvt_pk_bf16_f32 v240, v240, v241\n" \
"v_mul_f32_e32 v242, v57, v242\n" \
"v_mul_f32_e32 v243, v57, v243\n" \
"v_mfma_f32_16x16x32_bf16 a[208:211], a[216:219], v[232:235], a[208:211]\n" \
"v_cvt_pk_bf16_f32 v241, v242, v243\n" \
"v_mul_f32_e32 v244, v57, v244\n" \
"buffer_store_dwordx4 v[232:235], v20, s[56:59], s71 offen offset:1024 sc1\n" \
"v_mul_f32_e32 v245, v57, v245\n" \
"v_mul_f32_e32 v246, v57, v246\n" \
"v_mfma_f32_32x32x16_bf16 v[224:239], a[216:219], a[216:219], 0\n" \
"v_mul_f32_e32 v247, v57, v247\n" \
"v_mul_f32_e32 v248, v57, v248\n" \
"v_mul_f32_e32 v249, v57, v249\n" \
"v_cvt_pk_bf16_f32 v242, v244, v245\n" \
"v_cvt_pk_bf16_f32 v243, v246, v247\n" \
"v_cvt_pk_bf16_f32 v248, v248, v249\n" \
"v_mul_f32_e32 v250, v57, v250\n" \
"buffer_store_dwordx4 v[240:243], v20, s[56:59], s71 offen offset:2048 sc1\n" \
"v_mul_f32_e32 v251, v57, v251\n" \
"v_mfma_f32_16x16x32_bf16 a[208:211], a[216:219], v[240:243], a[208:211]\n" \
"v_cvt_pk_bf16_f32 v249, v250, v251\n" \
"v_mul_f32_e32 v252, v57, v252\n" \
"v_mul_f32_e32 v253, v57, v253\n" \
"v_mul_f32_e32 v254, v57, v254\n" \
"v_mul_f32_e32 v255, v57, v255\n" \
"v_cvt_pk_bf16_f32 v250, v252, v253\n" \
"v_cvt_pk_bf16_f32 v251, v254, v255\n" \
"v_add_u32_e32 v53, 0x20000, v63\n" \
"buffer_store_dwordx4 v[248:251], v20, s[56:59], s71 offen offset:3072 sc1\n" \
"v_mfma_f32_16x16x32_bf16 a[208:211], a[216:219], v[248:251], a[208:211]\n" \
"v_exp_f32_e32 v70, v70\n" \
"v_mfma_f32_32x32x16_bf16 v[240:255], a[216:219], a[216:219], 0\n" \
"v_exp_f32_e32 v71, v71\n" \
"v_exp_f32_e32 v72, v72\n" \
"v_exp_f32_e32 v73, v73\n" \
"buffer_load_dwordx4 v4, s[84:87], s92 offen offset:0 lds\n" \
"s_add_u32 s64, s64, 0x10000\n" \
"v_cmp_lt_f32_e64 s[72:73], s21, v62\n" \
"v_accvgpr_read_b32 v63, a208\n" \
"v_cmp_u_f32_e32 vcc, v63, v63\n" \
"v_readlane_b32 s95, v11, 3\n" \
"v_exp_f32_e32 v68, v68\n" \
"v_exp_f32_e32 v69, v69\n" \
"v_exp_f32_e32 v74, v74\n" \
"buffer_load_dwordx4 v5, s[84:87], s92 offen offset:1024 lds\n" \
"v_exp_f32_e32 v75, v75\n" \
"s_or_b64 vcc, s[72:73], vcc\n" \
"s_cselect_b32 s95, 0x40000000, s95\n" \
"s_cselect_b32 s94, 0x7f800000, 0\n" \
"v_cvt_pk_bf16_f32 v71, v70, v71\n" \
"v_exp_f32_e32 v66, v66\n" \
"v_exp_f32_e32 v67, v67\n" \
"v_exp_f32_e32 v64, v64\n" \
"buffer_load_dwordx4 v6, s[84:87], s92 offen offset:2048 lds\n" \
"v_exp_f32_e32 v65, v65\n" \
"v_cvt_pk_bf16_f32 v72, v72, v73\n" \
"v_exp_f32_e32 v76, v76\n" \
"v_exp_f32_e32 v77, v77\n" \
"v_exp_f32_e32 v78, v78\n" \
"v_exp_f32_e32 v79, v79\n" \
"buffer_load_dwordx4 v7, s[84:87], s92 offen offset:3072 lds\n" \
"s_add_u32 m0, s91, 0x2000\n" \
"v_cvt_pk_bf16_f32 v70, v68, v69\n" \
"v_cvt_pk_bf16_f32 v73, v74, v75\n" \
"v_add_u32_e32 v49, 0x10000, v48\n" \
"s_xor_b32 s65, s65, 0x8000\n" \
"v_cvt_pk_bf16_f32 v69, v66, v67\n" \
"v_cvt_pk_bf16_f32 v68, v64, v65\n" \
"v_cvt_pk_bf16_f32 v74, v76, v77\n" \
"v_cvt_pk_bf16_f32 v75, v78, v79\n" \
"v_xor_b32_e32 v48, 0x8000, v48\n" \
"s_sub_u32 s66, s19, 2\n" \
"buffer_load_dwordx4 v8, s[84:87], s92 offen offset:0 lds\n" \
"s_waitcnt vmcnt(18) lgkmcnt(0)\n" \
"s_barrier\n" \
"s_branch L_f1_%=\n" \
".fill (L_ab + 32768 - .) / 4, 4, 0xBF800000\n" \
".fill (L_ab + 32768 - .) / 4, 4, 0xBF800000\n" \
"L_ap8:\n" \
"s_setpc_b64 s[88:89]\n" \
"L_out_%=:\n" \
"v_bfe_u32 v38, v20, 2, 4\n" \
"v_readlane_b32 s64, v11, 2\n" \
"v_add_u32_e32 v32, 0x49000, v20\n" \
"v_add_u32_e32 v34, 0x20000, v10\n" \
"ds_read_b64_tr_b16 a[0:1], v34 offset:0\n" \
"ds_read_b64_tr_b16 a[2:3], v34 offset:4608\n" \
"ds_read_b64_tr_b16 a[4:5], v34 offset:32\n" \
"buffer_load_dword v26, v38, s[56:59], s64 offen sc1\n" \
"ds_read_b64_tr_b16 a[6:7], v34 offset:4640\n" \
"ds_read_b64_tr_b16 a[8:9], v34 offset:64\n" \
"ds_read_b64_tr_b16 a[10:11], v34 offset:4672\n" \
"ds_read_b64_tr_b16 a[12:13], v34 offset:96\n" \
"buffer_store_dwordx4 v[64:67], v20, s[56:59], s61 offen offset:0 sc1\n" \
"ds_read_b64_tr_b16 a[14:15], v34 offset:4704\n" \
"v_mov_b32_e32 v39, s62\n" \
"v_readlane_b32 s70, v11, 5\n" \
"v_readlane_b32 s71, v11, 4\n" \
"buffer_store_dwordx4 v[68:71], v20, s[56:59], s61 offen offset:1024 sc1\n" \
"ds_read_b64_tr_b16 a[16:17], v34 offset:128\n" \
"ds_read_b64_tr_b16 a[18:19], v34 offset:4736\n" \
"ds_read_b64_tr_b16 a[20:21], v34 offset:160\n" \
"ds_read_b64_tr_b16 a[22:23], v34 offset:4768\n" \
"buffer_store_dwordx4 v[72:75], v20, s[56:59], s61 offen offset:2048 sc1\n" \
"ds_read_b64_tr_b16 a[24:25], v34 offset:192\n" \
"ds_read_b64_tr_b16 a[26:27], v34 offset:4800\n" \
"ds_read_b64_tr_b16 a[28:29], v34 offset:224\n" \
"ds_read_b64_tr_b16 a[30:31], v34 offset:4832\n" \
"buffer_store_dwordx4 v[76:79], v20, s[56:59], s61 offen offset:3072 sc1\n" \
"ds_read_b64_tr_b16 a[192:193], v34 offset:256\n" \
"ds_read_b64_tr_b16 a[194:195], v34 offset:4864\n" \
"ds_read_b64_tr_b16 a[196:197], v34 offset:9216\n" \
"ds_read_b64_tr_b16 a[198:199], v34 offset:13824\n" \
"buffer_store_dwordx4 v[80:83], v32, s[56:59], s61 offen offset:0 sc1\n" \
"ds_read_b64_tr_b16 a[200:201], v34 offset:9248\n" \
"ds_read_b64_tr_b16 a[202:203], v34 offset:13856\n" \
"ds_read_b64_tr_b16 a[204:205], v34 offset:9280\n" \
"ds_read_b64_tr_b16 a[206:207], v34 offset:13888\n" \
"buffer_store_dwordx4 v[84:87], v32, s[56:59], s61 offen offset:1024 sc1\n" \
"ds_read_b64_tr_b16 a[208:209], v34 offset:9312\n" \
"s_waitcnt vmcnt(16) lgkmcnt(15)\n" \
"v_mfma_f32_16x16x32_bf16 v[112:115], a[0:3], a[96:99], 0\n" \
"buffer_store_dwordx4 v[96:99], v32, s[56:59], s61 offen offset:2048 sc1\n" \
"v_mfma_f32_16x16x32_bf16 v[116:119], a[0:3], a[112:115], 0\n" \
"ds_read_b64_tr_b16 a[210:211], v34 offset:13920\n" \
"ds_read_b64_tr_b16 a[212:213], v34 offset:9344\n" \
"buffer_store_dwordx4 v[100:103], v32, s[56:59], s61 offen offset:3072 sc1\n" \
"v_mfma_f32_16x16x32_bf16 v[184:187], a[0:3], v[64:67], 0\n" \
"ds_read_b64_tr_b16 a[214:215], v34 offset:13952\n" \
"ds_read_b64_tr_b16 a[216:217], v34 offset:9376\n" \
"v_mfma_f32_16x16x32_bf16 v[188:191], a[0:3], v[80:83], 0\n" \
"ds_read_b64_tr_b16 a[218:219], v34 offset:13984\n" \
"ds_read_b64_tr_b16 a[0:1], v34 offset:9408\n" \
"v_mfma_f32_16x16x32_bf16 v[120:123], a[4:7], a[96:99], 0\n" \
"ds_read_b64_tr_b16 a[2:3], v34 offset:14016\n" \
"v_readlane_b32 s65, v11, 30\n" \
"v_mfma_f32_16x16x32_bf16 v[124:127], a[4:7], a[112:115], 0\n" \
"v_lshrrev_b32_e32 v44, 9, v20\n" \
"v_mul_u32_u24_e32 v44, 0xb40, v44\n" \
"v_mfma_f32_16x16x32_bf16 v[192:195], a[4:7], v[64:67], 0\n" \
"v_add_u32_e32 v35, v44, v10\n" \
"v_readlane_b32 s67, v11, 23\n" \
"v_mfma_f32_16x16x32_bf16 v[196:199], a[4:7], v[80:83], 0\n" \
"ds_read_b64_tr_b16 a[4:5], v34 offset:9440\n" \
"ds_read_b64_tr_b16 a[6:7], v34 offset:14048\n" \
"v_mfma_f32_16x16x32_bf16 v[128:131], a[8:11], a[96:99], 0\n" \
"v_readlane_b32 s80, v11, 11\n" \
"v_readlane_b32 s81, v11, 12\n" \
"v_mfma_f32_16x16x32_bf16 v[132:135], a[8:11], a[112:115], 0\n" \
"s_mov_b32 s82, 0x47fca0\n" \
"s_mov_b32 s83, 0x20000\n" \
"v_mfma_f32_16x16x32_bf16 v[200:203], a[8:11], v[64:67], 0\n" \
"v_readlane_b32 s66, v11, 28\n" \
"s_add_u32 m0, s67, 0x0\n" \
"v_mfma_f32_16x16x32_bf16 v[204:207], a[8:11], v[80:83], 0\n" \
"ds_read_b64_tr_b16 a[8:9], v34 offset:9472\n" \
"ds_read_b64_tr_b16 a[10:11], v34 offset:14080\n" \
"v_mfma_f32_16x16x32_bf16 v[136:139], a[12:15], a[96:99], 0\n" \
"buffer_load_dwordx4 v0, s[80:83], s66 offen offset:0 lds\n" \
"v_mfma_f32_16x16x32_bf16 v[140:143], a[12:15], a[112:115], 0\n" \
"v_readlane_b32 s92, v11, 24\n" \
"v_readlane_b32 s73, v11, 29\n" \
"v_mfma_f32_16x16x32_bf16 v[208:211], a[12:15], v[64:67], 0\n" \
"v_readlane_b32 s94, v11, 25\n" \
"v_readlane_b32 s84, v11, 9\n" \
"v_mfma_f32_16x16x32_bf16 v[212:215], a[12:15], v[80:83], 0\n" \
"ds_read_b64_tr_b16 a[12:13], v34 offset:18432\n" \
"ds_read_b64_tr_b16 a[14:15], v34 offset:23040\n" \
"v_mfma_f32_16x16x32_bf16 v[144:147], a[16:19], a[96:99], 0\n" \
"v_readlane_b32 s85, v11, 10\n" \
"s_mov_b32 s86, 0xa1ca0\n" \
"v_mfma_f32_16x16x32_bf16 v[148:151], a[16:19], a[112:115], 0\n" \
"buffer_load_dwordx4 v1, s[80:83], s66 offen offset:1024 lds\n" \
"v_mfma_f32_16x16x32_bf16 v[216:219], a[16:19], v[64:67], 0\n" \
"s_mov_b32 s87, 0x20000\n" \
"v_readlane_b32 s68, v11, 31\n" \
"v_mfma_f32_16x16x32_bf16 v[220:223], a[16:19], v[80:83], 0\n" \
"ds_read_b64_tr_b16 a[16:17], v34 offset:18464\n" \
"s_waitcnt lgkmcnt(7)\n" \
"v_mfma_f32_16x16x32_bf16 v[152:155], a[20:23], a[96:99], 0\n" \
"ds_read_b64_tr_b16 a[18:19], v34 offset:23072\n" \
"v_add_u32_e32 v33, 0x10000, v10\n" \
"v_mfma_f32_16x16x32_bf16 v[156:159], a[20:23], a[112:115], 0\n" \
"v_readlane_b32 s69, v11, 32\n" \
"v_readlane_b32 s72, v11, 34\n" \
"v_mfma_f32_16x16x32_bf16 v[224:227], a[20:23], v[64:67], 0\n" \
"buffer_load_dwordx4 v2, s[80:83], s66 offen offset:2048 lds\n" \
"v_mfma_f32_16x16x32_bf16 v[228:231], a[20:23], v[80:83], 0\n" \
"ds_read_b64_tr_b16 a[20:21], v34 offset:18496\n" \
"ds_read_b64_tr_b16 a[22:23], v34 offset:23104\n" \
"v_mfma_f32_16x16x32_bf16 v[160:163], a[24:27], a[96:99], 0\n" \
"v_readlane_b32 s93, v11, 35\n" \
"v_lshlrev_b32_e32 v41, 12, v20\n" \
"v_mfma_f32_16x16x32_bf16 v[164:167], a[24:27], a[112:115], 0\n" \
"v_readlane_b32 s95, v11, 38\n" \
"v_readlane_b32 s96, v11, 39\n" \
"v_mfma_f32_16x16x32_bf16 v[232:235], a[24:27], v[64:67], 0\n" \
"v_readlane_b32 s63, v11, 41\n" \
"v_bfe_u32 v40, v20, 2, 7\n" \
"v_mfma_f32_16x16x32_bf16 v[236:239], a[24:27], v[80:83], 0\n" \
"ds_read_b64_tr_b16 a[24:25], v34 offset:18528\n" \
"ds_read_b64_tr_b16 a[26:27], v34 offset:23136\n" \
"v_mfma_f32_16x16x32_bf16 v[168:171], a[28:31], a[96:99], 0\n" \
"buffer_load_dwordx4 v3, s[80:83], s66 offen offset:3072 lds\n" \
"v_mfma_f32_16x16x32_bf16 v[172:175], a[28:31], a[112:115], 0\n" \
"s_add_u32 m0, s67, 0x1000\n" \
"s_add_u32 s74, s63, 0x3800\n" \
"v_mfma_f32_16x16x32_bf16 v[240:243], a[28:31], v[64:67], 0\n" \
"v_readlane_b32 s100, v11, 7\n" \
"v_readlane_b32 s97, v11, 40\n" \
"v_mfma_f32_16x16x32_bf16 v[244:247], a[28:31], v[80:83], 0\n" \
"ds_read_b64_tr_b16 a[28:29], v34 offset:18560\n" \
"ds_read_b64_tr_b16 a[30:31], v34 offset:23168\n" \
"v_mfma_f32_16x16x32_bf16 v[176:179], a[192:195], a[96:99], 0\n" \
"v_bfe_u32 v44, v20, 4, 4\n" \
"v_bfe_u32 v45, v20, 8, 2\n" \
"v_mfma_f32_16x16x32_bf16 v[180:183], a[192:195], a[112:115], 0\n" \
"buffer_load_dwordx4 v4, s[80:83], s66 offen offset:0 lds\n" \
"v_mfma_f32_16x16x32_bf16 v[248:251], a[192:195], v[64:67], 0\n" \
"v_mul_u32_u24_e32 v42, 0x480, v44\n" \
"v_lshl_add_u32 v42, v45, 3, v42\n" \
"v_mfma_f32_16x16x32_bf16 v[252:255], a[192:195], v[80:83], 0\n" \
"ds_read_b64_tr_b16 a[192:193], v34 offset:18592\n" \
"ds_read_b64_tr_b16 a[194:195], v34 offset:23200\n" \
"v_mfma_f32_16x16x32_bf16 v[112:115], a[196:199], a[100:103], v[112:115]\n" \
"v_lshrrev_b32_e32 v44, 1, v44\n" \
"v_lshlrev_b32_e32 v45, 1, v45\n" \
"v_mfma_f32_16x16x32_bf16 v[116:119], a[196:199], a[116:119], v[116:119]\n" \
"v_sub_u32_e32 v44, v44, v45\n" \
"v_and_b32_e32 v45, 16, v20\n" \
"v_mfma_f32_16x16x32_bf16 v[184:187], a[196:199], v[68:71], v[184:187]\n" \
"buffer_load_dwordx4 v5, s[80:83], s66 offen offset:1024 lds\n" \
"v_mfma_f32_16x16x32_bf16 v[188:191], a[196:199], v[84:87], v[188:191]\n" \
"ds_read_b64_tr_b16 a[196:197], v34 offset:18624\n" \
"ds_read_b64_tr_b16 a[198:199], v34 offset:23232\n" \
"v_mfma_f32_16x16x32_bf16 v[120:123], a[200:203], a[100:103], v[120:123]\n" \
"v_mov_b32_e32 v46, 0x3f80\n" \
"v_lshlrev_b32_e32 v45, v45, v46\n" \
"v_mfma_f32_16x16x32_bf16 v[124:127], a[200:203], a[116:119], v[124:127]\n" \
"v_mov_b32_e32 v52, 0\n" \
"v_mov_b32_e32 v53, 0\n" \
"v_mfma_f32_16x16x32_bf16 v[192:195], a[200:203], v[68:71], v[192:195]\n" \
"s_waitcnt vmcnt(14)\n" \
"v_cmp_ne_u32_e32 vcc, s62, v26\n" \
"v_mfma_f32_16x16x32_bf16 v[196:199], a[200:203], v[84:87], v[196:199]\n" \
"ds_read_b64_tr_b16 a[200:201], v34 offset:18656\n" \
"ds_read_b64_tr_b16 a[202:203], v34 offset:23264\n" \
"v_mfma_f32_16x16x32_bf16 v[128:131], a[204:207], a[100:103], v[128:131]\n" \
"buffer_load_dwordx4 v6, s[80:83], s66 offen offset:2048 lds\n" \
"v_mfma_f32_16x16x32_bf16 v[132:135], a[204:207], a[116:119], v[132:135]\n" \
"s_cbranch_vccnz L_sa_%=\n" \
"L_ra_%=:\n" \
"v_readlane_b32 s64, v11, 33\n" \
"v_mfma_f32_16x16x32_bf16 v[200:203], a[204:207], v[68:71], v[200:203]\n" \
"v_mov_b32_e32 v48, 0\n" \
"v_mov_b32_e32 v49, 0\n" \
"v_mfma_f32_16x16x32_bf16 v[204:207], a[204:207], v[84:87], v[204:207]\n" \
"ds_read_b64_tr_b16 a[204:205], v34 offset:18688\n" \
"ds_read_b64_tr_b16 a[206:207], v34 offset:23296\n" \
"v_mfma_f32_16x16x32_bf16 v[136:139], a[208:211], a[100:103], v[136:139]\n" \
"v_add_u32_e32 v43, 0x9000, v42\n" \
"v_readlane_b32 s88, v11, 13\n" \
"v_mfma_f32_16x16x32_bf16 v[140:143], a[208:211], a[116:119], v[140:143]\n" \
"buffer_load_dwordx4 a[64:67], v20, s[56:59], s65 offen offset:0 sc1\n" \
"v_mfma_f32_16x16x32_bf16 v[208:211], a[208:211], v[68:71], v[208:211]\n" \
"v_readlane_b32 s89, v11, 14\n" \
"s_mov_b32 s90, 0x240000\n" \
"v_mfma_f32_16x16x32_bf16 v[212:215], a[208:211], v[84:87], v[212:215]\n" \
"s_mov_b32 s91, 0x20000\n" \
"v_mov_b32_e32 v46, s62\n" \
"v_mfma_f32_16x16x32_bf16 v[144:147], a[212:215], a[100:103], v[144:147]\n" \
"v_mfma_f32_16x16x32_bf16 v[148:151], a[212:215], a[116:119], v[148:151]\n" \
"v_mfma_f32_16x16x32_bf16 v[216:219], a[212:215], v[68:71], v[216:219]\n" \
"buffer_load_dwordx4 a[80:83], v32, s[56:59], s65 offen offset:0 sc1\n" \
"v_mfma_f32_16x16x32_bf16 v[220:223], a[212:215], v[84:87], v[220:223]\n" \
"v_mfma_f32_16x16x32_bf16 v[152:155], a[216:219], a[100:103], v[152:155]\n" \
"v_mfma_f32_16x16x32_bf16 v[156:159], a[216:219], a[116:119], v[156:159]\n" \
"v_mfma_f32_16x16x32_bf16 v[224:227], a[216:219], v[68:71], v[224:227]\n" \
"v_mfma_f32_16x16x32_bf16 v[228:231], a[216:219], v[84:87], v[228:231]\n" \
"buffer_load_dwordx4 a[68:71], v20, s[56:59], s65 offen offset:1024 sc1\n" \
"v_mfma_f32_16x16x32_bf16 v[160:163], a[0:3], a[100:103], v[160:163]\n" \
"v_mfma_f32_16x16x32_bf16 v[164:167], a[0:3], a[116:119], v[164:167]\n" \
"v_mfma_f32_16x16x32_bf16 v[232:235], a[0:3], v[68:71], v[232:235]\n" \
"v_mfma_f32_16x16x32_bf16 v[236:239], a[0:3], v[84:87], v[236:239]\n" \
"s_waitcnt lgkmcnt(0)\n" \
"v_mfma_f32_16x16x32_bf16 v[168:171], a[4:7], a[100:103], v[168:171]\n" \
"buffer_load_dwordx4 a[84:87], v32, s[56:59], s65 offen offset:1024 sc1\n" \
"v_mfma_f32_16x16x32_bf16 v[172:175], a[4:7], a[116:119], v[172:175]\n" \
"v_mfma_f32_16x16x32_bf16 v[240:243], a[4:7], v[68:71], v[240:243]\n" \
"v_mfma_f32_16x16x32_bf16 v[244:247], a[4:7], v[84:87], v[244:247]\n" \
"v_mfma_f32_16x16x32_bf16 v[176:179], a[8:11], a[100:103], v[176:179]\n" \
"v_mfma_f32_16x16x32_bf16 v[180:183], a[8:11], a[116:119], v[180:183]\n" \
"buffer_load_dwordx4 a[72:75], v20, s[56:59], s65 offen offset:2048 sc1\n" \
"v_mfma_f32_16x16x32_bf16 v[248:251], a[8:11], v[68:71], v[248:251]\n" \
"v_mfma_f32_16x16x32_bf16 v[252:255], a[8:11], v[84:87], v[252:255]\n" \
"v_mfma_f32_16x16x32_bf16 v[112:115], a[12:15], a[104:107], v[112:115]\n" \
"v_mfma_f32_16x16x32_bf16 v[116:119], a[12:15], a[120:123], v[116:119]\n" \
"v_mfma_f32_16x16x32_bf16 v[184:187], a[12:15], v[72:75], v[184:187]\n" \
"buffer_load_dwordx4 a[88:91], v32, s[56:59], s65 offen offset:2048 sc1\n" \
"v_mfma_f32_16x16x32_bf16 v[188:191], a[12:15], v[96:99], v[188:191]\n" \
"v_mfma_f32_16x16x32_bf16 v[120:123], a[16:19], a[104:107], v[120:123]\n" \
"v_mfma_f32_16x16x32_bf16 v[124:127], a[16:19], a[120:123], v[124:127]\n" \
"v_mfma_f32_16x16x32_bf16 v[192:195], a[16:19], v[72:75], v[192:195]\n" \
"v_mfma_f32_16x16x32_bf16 v[196:199], a[16:19], v[96:99], v[196:199]\n" \
"buffer_load_dwordx4 v7, s[80:83], s66 offen offset:3072 lds\n" \
"v_mfma_f32_16x16x32_bf16 v[128:131], a[20:23], a[104:107], v[128:131]\n" \
"s_add_u32 m0, s67, 0x2000\n" \
"v_readlane_b32 s67, v11, 48\n" \
"v_mfma_f32_16x16x32_bf16 v[132:135], a[20:23], a[120:123], v[132:135]\n" \
"v_mfma_f32_16x16x32_bf16 v[200:203], a[20:23], v[72:75], v[200:203]\n" \
"v_mfma_f32_16x16x32_bf16 v[204:207], a[20:23], v[96:99], v[204:207]\n" \
"s_barrier\n" \
"v_mfma_f32_16x16x32_bf16 v[136:139], a[24:27], a[104:107], v[136:139]\n" \
"ds_read_b64_tr_b16 a[208:209], v10 offset:32768\n" \
"ds_read_b64_tr_b16 a[210:211], v10 offset:37376\n" \
"v_mfma_f32_16x16x32_bf16 v[140:143], a[24:27], a[120:123], v[140:143]\n" \
"ds_read_b64_tr_b16 a[212:213], v10 offset:32800\n" \
"ds_read_b64_tr_b16 a[214:215], v10 offset:37408\n" \
"v_mfma_f32_16x16x32_bf16 v[208:211], a[24:27], v[72:75], v[208:211]\n" \
"ds_read_b64_tr_b16 a[216:217], v10 offset:32832\n" \
"ds_read_b64_tr_b16 a[218:219], v10 offset:37440\n" \
"v_mfma_f32_16x16x32_bf16 v[212:215], a[24:27], v[96:99], v[212:215]\n" \
"ds_read_b64_tr_b16 a[0:1], v10 offset:32864\n" \
"ds_read_b64_tr_b16 a[2:3], v10 offset:37472\n" \
"v_mfma_f32_16x16x32_bf16 v[144:147], a[28:31], a[104:107], v[144:147]\n" \
"ds_read_b64_tr_b16 a[4:5], v10 offset:32896\n" \
"ds_read_b64_tr_b16 a[6:7], v10 offset:37504\n" \
"v_mfma_f32_16x16x32_bf16 v[148:151], a[28:31], a[120:123], v[148:151]\n" \
"ds_read_b64_tr_b16 a[8:9], v10 offset:32928\n" \
"ds_read_b64_tr_b16 a[10:11], v10 offset:37536\n" \
"v_mfma_f32_16x16x32_bf16 v[216:219], a[28:31], v[72:75], v[216:219]\n" \
"ds_read_b64_tr_b16 a[12:13], v10 offset:32960\n" \
"ds_read_b64_tr_b16 a[14:15], v10 offset:37568\n" \
"v_mfma_f32_16x16x32_bf16 v[220:223], a[28:31], v[96:99], v[220:223]\n" \
"ds_read_b64_tr_b16 a[16:17], v10 offset:32992\n" \
"ds_read_b64_tr_b16 a[18:19], v10 offset:37600\n" \
"v_mfma_f32_16x16x32_bf16 v[152:155], a[192:195], a[104:107], v[152:155]\n" \
"ds_read_b64_tr_b16 a[20:21], v10 offset:33024\n" \
"ds_read_b64_tr_b16 a[22:23], v10 offset:37632\n" \
"v_mfma_f32_16x16x32_bf16 v[156:159], a[192:195], a[120:123], v[156:159]\n" \
"ds_read_b64_tr_b16 a[24:25], v35 offset:41984\n" \
"ds_read_b64_tr_b16 a[26:27], v35 offset:44288\n" \
"v_mfma_f32_16x16x32_bf16 v[224:227], a[192:195], v[72:75], v[224:227]\n" \
"ds_read_b64_tr_b16 a[28:29], v35 offset:42016\n" \
"ds_read_b64_tr_b16 a[30:31], v35 offset:44320\n" \
"v_mfma_f32_16x16x32_bf16 v[228:231], a[192:195], v[96:99], v[228:231]\n" \
"ds_read_b64_tr_b16 a[192:193], v35 offset:42048\n" \
"ds_read_b64_tr_b16 a[194:195], v35 offset:44352\n" \
"v_mfma_f32_16x16x32_bf16 v[160:163], a[196:199], a[104:107], v[160:163]\n" \
"buffer_load_dwordx4 v8, s[80:83], s66 offen offset:0 lds\n" \
"v_mfma_f32_16x16x32_bf16 v[164:167], a[196:199], a[120:123], v[164:167]\n" \
"s_add_u32 m0, s92, 0x0\n" \
"v_readlane_b32 s66, v11, 47\n" \
"v_mfma_f32_16x16x32_bf16 v[232:235], a[196:199], v[72:75], v[232:235]\n" \
"v_mfma_f32_16x16x32_bf16 v[236:239], a[196:199], v[96:99], v[236:239]\n" \
"ds_read_b64_tr_b16 a[196:197], v35 offset:42080\n" \
"ds_read_b64_tr_b16 a[198:199], v35 offset:44384\n" \
"v_mfma_f32_16x16x32_bf16 v[168:171], a[200:203], a[104:107], v[168:171]\n" \
"v_mfma_f32_16x16x32_bf16 v[172:175], a[200:203], a[120:123], v[172:175]\n" \
"buffer_load_dwordx4 v0, s[80:83], s73 offen offset:0 lds\n" \
"v_mfma_f32_16x16x32_bf16 v[240:243], a[200:203], v[72:75], v[240:243]\n" \
"v_mfma_f32_16x16x32_bf16 v[244:247], a[200:203], v[96:99], v[244:247]\n" \
"ds_read_b64_tr_b16 a[200:201], v35 offset:42112\n" \
"ds_read_b64_tr_b16 a[202:203], v35 offset:44416\n" \
"v_mfma_f32_16x16x32_bf16 v[176:179], a[204:207], a[104:107], v[176:179]\n" \
"v_mfma_f32_16x16x32_bf16 v[180:183], a[204:207], a[120:123], v[180:183]\n" \
"v_mfma_f32_16x16x32_bf16 v[248:251], a[204:207], v[72:75], v[248:251]\n" \
"buffer_load_dwordx4 v1, s[80:83], s73 offen offset:1024 lds\n" \
"v_mfma_f32_16x16x32_bf16 v[252:255], a[204:207], v[96:99], v[252:255]\n" \
"ds_read_b64_tr_b16 a[204:205], v35 offset:42144\n" \
"s_waitcnt lgkmcnt(9)\n" \
"v_mfma_f32_16x16x32_bf16 v[112:115], a[208:211], a[108:111], v[112:115]\n" \
"ds_read_b64_tr_b16 a[206:207], v35 offset:44448\n" \
"v_mfma_f32_16x16x32_bf16 v[116:119], a[208:211], a[124:127], v[116:119]\n" \
"v_mfma_f32_16x16x32_bf16 v[184:187], a[208:211], v[76:79], v[184:187]\n" \
"v_mfma_f32_16x16x32_bf16 v[188:191], a[208:211], v[100:103], v[188:191]\n" \
"ds_read_b64_tr_b16 a[208:209], v35 offset:42176\n" \
"ds_read_b64_tr_b16 a[210:211], v35 offset:44480\n" \
"v_mfma_f32_16x16x32_bf16 v[120:123], a[212:215], a[108:111], v[120:123]\n" \
"buffer_load_dwordx4 v2, s[80:83], s73 offen offset:2048 lds\n" \
"v_mfma_f32_16x16x32_bf16 v[124:127], a[212:215], a[124:127], v[124:127]\n" \
"v_mfma_f32_16x16x32_bf16 v[192:195], a[212:215], v[76:79], v[192:195]\n" \
"s_waitcnt vmcnt(18)\n" \
"s_mov_b64 exec, 1\n" \
"buffer_store_dword v39, off, s[56:59], s70 offset:0 sc1\n" \
"s_mov_b64 exec, -1\n" \
"v_mfma_f32_16x16x32_bf16 v[196:199], a[212:215], v[100:103], v[196:199]\n" \
"ds_read_b64_tr_b16 a[212:213], v35 offset:42208\n" \
"ds_read_b64_tr_b16 a[214:215], v35 offset:44512\n" \
"v_mfma_f32_16x16x32_bf16 v[128:131], a[216:219], a[108:111], v[128:131]\n" \
"v_readlane_b32 s70, v11, 51\n" \
"v_mfma_f32_16x16x32_bf16 v[132:135], a[216:219], a[124:127], v[132:135]\n" \
"v_mfma_f32_16x16x32_bf16 v[200:203], a[216:219], v[76:79], v[200:203]\n" \
"buffer_load_dwordx4 v3, s[80:83], s73 offen offset:3072 lds\n" \
"v_mfma_f32_16x16x32_bf16 v[204:207], a[216:219], v[100:103], v[204:207]\n" \
"ds_read_b64_tr_b16 a[216:217], v35 offset:42240\n" \
"ds_read_b64_tr_b16 a[218:219], v35 offset:44544\n" \
"v_mfma_f32_16x16x32_bf16 v[136:139], a[0:3], a[108:111], v[136:139]\n" \
"s_add_u32 m0, s92, 0x1000\n" \
"v_mfma_f32_16x16x32_bf16 v[140:143], a[0:3], a[124:127], v[140:143]\n" \
"v_mfma_f32_16x16x32_bf16 v[208:211], a[0:3], v[76:79], v[208:211]\n" \
"v_mfma_f32_16x16x32_bf16 v[212:215], a[0:3], v[100:103], v[212:215]\n" \
"ds_read_b64_tr_b16 a[0:1], v35 offset:52352\n" \
"ds_read_b64_tr_b16 a[2:3], v35 offset:54656\n" \
"v_mfma_f32_16x16x32_bf16 v[144:147], a[4:7], a[108:111], v[144:147]\n" \
"buffer_load_dwordx4 v4, s[80:83], s73 offen offset:0 lds\n" \
"v_mfma_f32_16x16x32_bf16 v[148:151], a[4:7], a[124:127], v[148:151]\n" \
"v_mfma_f32_16x16x32_bf16 v[216:219], a[4:7], v[76:79], v[216:219]\n" \
"v_mfma_f32_16x16x32_bf16 v[220:223], a[4:7], v[100:103], v[220:223]\n" \
"ds_read_b64_tr_b16 a[4:5], v35 offset:52384\n" \
"ds_read_b64_tr_b16 a[6:7], v35 offset:54688\n" \
"v_mfma_f32_16x16x32_bf16 v[152:155], a[8:11], a[108:111], v[152:155]\n" \
"v_mfma_f32_16x16x32_bf16 v[156:159], a[8:11], a[124:127], v[156:159]\n" \
"buffer_load_dwordx4 v5, s[80:83], s73 offen offset:1024 lds\n" \
"v_mfma_f32_16x16x32_bf16 v[224:227], a[8:11], v[76:79], v[224:227]\n" \
"v_mfma_f32_16x16x32_bf16 v[228:231], a[8:11], v[100:103], v[228:231]\n" \
"ds_read_b64_tr_b16 a[8:9], v35 offset:52416\n" \
"ds_read_b64_tr_b16 a[10:11], v35 offset:54720\n" \
"v_mfma_f32_16x16x32_bf16 v[160:163], a[12:15], a[108:111], v[160:163]\n" \
"v_mfma_f32_16x16x32_bf16 v[164:167], a[12:15], a[124:127], v[164:167]\n" \
"v_mfma_f32_16x16x32_bf16 v[232:235], a[12:15], v[76:79], v[232:235]\n" \
"buffer_load_dwordx4 v6, s[80:83], s73 offen offset:2048 lds\n" \
"v_mfma_f32_16x16x32_bf16 v[236:239], a[12:15], v[100:103], v[236:239]\n" \
"ds_read_b64_tr_b16 a[12:13], v35 offset:52448\n" \
"ds_read_b64_tr_b16 a[14:15], v35 offset:54752\n" \
"v_mfma_f32_16x16x32_bf16 v[168:171], a[16:19], a[108:111], v[168:171]\n" \
"v_mfma_f32_16x16x32_bf16 v[172:175], a[16:19], a[124:127], v[172:175]\n" \
"v_mfma_f32_16x16x32_bf16 v[240:243], a[16:19], v[76:79], v[240:243]\n" \
"v_mfma_f32_16x16x32_bf16 v[244:247], a[16:19], v[100:103], v[244:247]\n" \
"buffer_load_dwordx4 v7, s[80:83], s73 offen offset:3072 lds\n" \
"v_mfma_f32_16x16x32_bf16 v[176:179], a[20:23], a[108:111], v[176:179]\n" \
"s_add_u32 m0, s92, 0x2000\n" \
"ds_read_b64_tr_b16 a[16:17], v35 offset:52480\n" \
"v_mfma_f32_16x16x32_bf16 v[180:183], a[20:23], a[124:127], v[180:183]\n" \
"ds_read_b64_tr_b16 a[18:19], v35 offset:54784\n" \
"v_readlane_b32 s92, v11, 46\n" \
"v_mfma_f32_16x16x32_bf16 v[248:251], a[20:23], v[76:79], v[248:251]\n" \
"v_mfma_f32_16x16x32_bf16 v[252:255], a[20:23], v[100:103], v[252:255]\n" \
"ds_read_b64_tr_b16 a[20:21], v35 offset:52512\n" \
"ds_read_b64_tr_b16 a[22:23], v35 offset:54816\n" \
"v_mfma_f32_16x16x32_bf16 v[112:115], a[24:27], a[220:223], v[112:115]\n" \
"buffer_load_dwordx4 v8, s[80:83], s73 offen offset:0 lds\n" \
"v_mfma_f32_16x16x32_bf16 v[116:119], a[24:27], a[228:231], v[116:119]\n" \
"s_mov_b32 m0, s94\n" \
"v_readlane_b32 s94, v11, 48\n" \
"v_mfma_f32_16x16x32_bf16 v[184:187], a[24:27], a[236:239], v[184:187]\n" \
"v_readlane_b32 s73, v11, 6\n" \
"v_mfma_f32_16x16x32_bf16 v[188:191], a[24:27], a[244:247], v[188:191]\n" \
"ds_read_b64_tr_b16 a[24:25], v35 offset:52544\n" \
"s_waitcnt lgkmcnt(7)\n" \
"v_mfma_f32_16x16x32_bf16 v[120:123], a[28:31], a[220:223], v[120:123]\n" \
"ds_read_b64_tr_b16 a[26:27], v35 offset:54848\n" \
"buffer_load_dword v47, v38, s[56:59], s71 offen sc1\n" \
"v_mfma_f32_16x16x32_bf16 v[124:127], a[28:31], a[228:231], v[124:127]\n" \
"v_mfma_f32_16x16x32_bf16 v[192:195], a[28:31], a[236:239], v[192:195]\n" \
"v_mfma_f32_16x16x32_bf16 v[196:199], a[28:31], a[244:247], v[196:199]\n" \
"ds_read_b64_tr_b16 a[28:29], v35 offset:52576\n" \
"ds_read_b64_tr_b16 a[30:31], v35 offset:54880\n" \
"v_mfma_f32_16x16x32_bf16 v[128:131], a[192:195], a[220:223], v[128:131]\n" \
"v_mfma_f32_16x16x32_bf16 v[132:135], a[192:195], a[228:231], v[132:135]\n" \
"buffer_load_dwordx4 v9, s[84:87], 0 offen lds\n" \
"v_mfma_f32_16x16x32_bf16 v[200:203], a[192:195], a[236:239], v[200:203]\n" \
"v_mfma_f32_16x16x32_bf16 v[204:207], a[192:195], a[244:247], v[204:207]\n" \
"ds_read_b64_tr_b16 a[192:193], v35 offset:52608\n" \
"ds_read_b64_tr_b16 a[194:195], v35 offset:54912\n" \
"v_mfma_f32_16x16x32_bf16 v[136:139], a[196:199], a[220:223], v[136:139]\n" \
"v_mfma_f32_16x16x32_bf16 v[140:143], a[196:199], a[228:231], v[140:143]\n" \
"v_mfma_f32_16x16x32_bf16 v[208:211], a[196:199], a[236:239], v[208:211]\n" \
"buffer_load_dwordx4 a[76:79], v20, s[56:59], s65 offen offset:3072 sc1\n" \
"v_mfma_f32_16x16x32_bf16 v[212:215], a[196:199], a[244:247], v[212:215]\n" \
"v_mfma_f32_16x16x32_bf16 v[144:147], a[200:203], a[220:223], v[144:147]\n" \
"v_mfma_f32_16x16x32_bf16 v[148:151], a[200:203], a[228:231], v[148:151]\n" \
"v_mfma_f32_16x16x32_bf16 v[216:219], a[200:203], a[236:239], v[216:219]\n" \
"v_mfma_f32_16x16x32_bf16 v[220:223], a[200:203], a[244:247], v[220:223]\n" \
"buffer_load_dwordx4 a[92:95], v32, s[56:59], s65 offen offset:3072 sc1\n" \
"v_mfma_f32_16x16x32_bf16 v[152:155], a[204:207], a[220:223], v[152:155]\n" \
"v_readlane_b32 s65, v11, 46\n" \
"v_mfma_f32_16x16x32_bf16 v[156:159], a[204:207], a[228:231], v[156:159]\n" \
"v_mfma_f32_16x16x32_bf16 v[224:227], a[204:207], a[236:239], v[224:227]\n" \
"v_mfma_f32_16x16x32_bf16 v[228:231], a[204:207], a[244:247], v[228:231]\n" \
"v_mfma_f32_16x16x32_bf16 v[160:163], a[208:211], a[220:223], v[160:163]\n" \
"buffer_load_dwordx4 a[128:131], v20, s[56:59], s68 offen offset:0 sc1\n" \
"v_mfma_f32_16x16x32_bf16 v[164:167], a[208:211], a[228:231], v[164:167]\n" \
"v_mfma_f32_16x16x32_bf16 v[232:235], a[208:211], a[236:239], v[232:235]\n" \
"v_mfma_f32_16x16x32_bf16 v[236:239], a[208:211], a[244:247], v[236:239]\n" \
"v_mfma_f32_16x16x32_bf16 v[168:171], a[212:215], a[220:223], v[168:171]\n" \
"v_mfma_f32_16x16x32_bf16 v[172:175], a[212:215], a[228:231], v[172:175]\n" \
"buffer_load_dwordx4 a[144:147], v32, s[56:59], s68 offen offset:0 sc1\n" \
"v_mfma_f32_16x16x32_bf16 v[240:243], a[212:215], a[236:239], v[240:243]\n" \
"v_mfma_f32_16x16x32_bf16 v[244:247], a[212:215], a[244:247], v[244:247]\n" \
"v_mfma_f32_16x16x32_bf16 v[176:179], a[216:219], a[220:223], v[176:179]\n" \
"v_mfma_f32_16x16x32_bf16 v[180:183], a[216:219], a[228:231], v[180:183]\n" \
"v_mfma_f32_16x16x32_bf16 v[248:251], a[216:219], a[236:239], v[248:251]\n" \
"buffer_load_dwordx4 a[132:135], v20, s[56:59], s68 offen offset:1024 sc1\n" \
"v_mfma_f32_16x16x32_bf16 v[252:255], a[216:219], a[244:247], v[252:255]\n" \
"v_mfma_f32_16x16x32_bf16 v[112:115], a[0:3], a[224:227], v[112:115]\n" \
"v_mfma_f32_16x16x32_bf16 v[116:119], a[0:3], a[232:235], v[116:119]\n" \
"v_mfma_f32_16x16x32_bf16 v[184:187], a[0:3], a[240:243], v[184:187]\n" \
"v_mfma_f32_16x16x32_bf16 v[188:191], a[0:3], a[248:251], v[188:191]\n" \
"buffer_load_dwordx4 a[148:151], v32, s[56:59], s68 offen offset:1024 sc1\n" \
"v_mfma_f32_16x16x32_bf16 v[120:123], a[4:7], a[224:227], v[120:123]\n" \
"v_mfma_f32_16x16x32_bf16 v[124:127], a[4:7], a[232:235], v[124:127]\n" \
"v_mfma_f32_16x16x32_bf16 v[192:195], a[4:7], a[240:243], v[192:195]\n" \
"v_mfma_f32_16x16x32_bf16 v[196:199], a[4:7], a[248:251], v[196:199]\n" \
"v_mfma_f32_16x16x32_bf16 v[128:131], a[8:11], a[224:227], v[128:131]\n" \
"buffer_load_dwordx4 a[136:139], v20, s[56:59], s68 offen offset:2048 sc1\n" \
"v_mfma_f32_16x16x32_bf16 v[132:135], a[8:11], a[232:235], v[132:135]\n" \
"v_mfma_f32_16x16x32_bf16 v[200:203], a[8:11], a[240:243], v[200:203]\n" \
"v_mfma_f32_16x16x32_bf16 v[204:207], a[8:11], a[248:251], v[204:207]\n" \
"s_waitcnt lgkmcnt(0)\n" \
"v_mfma_f32_16x16x32_bf16 v[136:139], a[12:15], a[224:227], v[136:139]\n" \
"v_mfma_f32_16x16x32_bf16 v[140:143], a[12:15], a[232:235], v[140:143]\n" \
"buffer_load_dwordx4 a[152:155], v32, s[56:59], s68 offen offset:2048 sc1\n" \
"v_mfma_f32_16x16x32_bf16 v[208:211], a[12:15], a[240:243], v[208:211]\n" \
"v_mfma_f32_16x16x32_bf16 v[212:215], a[12:15], a[248:251], v[212:215]\n" \
"s_barrier\n" \
"v_mfma_f32_16x16x32_bf16 v[144:147], a[16:19], a[224:227], v[144:147]\n" \
"ds_read_b64_tr_b16 a[196:197], v33 offset:0\n" \
"ds_read_b64_tr_b16 a[198:199], v33 offset:4608\n" \
"v_mfma_f32_16x16x32_bf16 v[148:151], a[16:19], a[232:235], v[148:151]\n" \
"buffer_load_dwordx4 a[140:143], v20, s[56:59], s68 offen offset:3072 sc1\n" \
"v_mfma_f32_16x16x32_bf16 v[216:219], a[16:19], a[240:243], v[216:219]\n" \
"ds_read_b64_tr_b16 a[200:201], v33 offset:32\n" \
"ds_read_b64_tr_b16 a[202:203], v33 offset:4640\n" \
"v_mfma_f32_16x16x32_bf16 v[220:223], a[16:19], a[248:251], v[220:223]\n" \
"ds_read_b64_tr_b16 a[204:205], v33 offset:64\n" \
"ds_read_b64_tr_b16 a[206:207], v33 offset:4672\n" \
"v_mfma_f32_16x16x32_bf16 v[152:155], a[20:23], a[224:227], v[152:155]\n" \
"ds_read_b64_tr_b16 a[208:209], v33 offset:96\n" \
"ds_read_b64_tr_b16 a[210:211], v33 offset:4704\n" \
"v_mfma_f32_16x16x32_bf16 v[156:159], a[20:23], a[232:235], v[156:159]\n" \
"ds_read_b64_tr_b16 a[212:213], v33 offset:128\n" \
"ds_read_b64_tr_b16 a[214:215], v33 offset:4736\n" \
"v_mfma_f32_16x16x32_bf16 v[224:227], a[20:23], a[240:243], v[224:227]\n" \
"buffer_load_dwordx4 a[156:159], v32, s[56:59], s68 offen offset:3072 sc1\n" \
"v_mfma_f32_16x16x32_bf16 v[228:231], a[20:23], a[248:251], v[228:231]\n" \
"ds_read_b64_tr_b16 a[216:217], v33 offset:160\n" \
"ds_read_b64_tr_b16 a[218:219], v33 offset:4768\n" \
"v_mfma_f32_16x16x32_bf16 v[160:163], a[24:27], a[224:227], v[160:163]\n" \
"ds_read_b64_tr_b16 a[0:1], v33 offset:192\n" \
"ds_read_b64_tr_b16 a[2:3], v33 offset:4800\n" \
"v_mfma_f32_16x16x32_bf16 v[164:167], a[24:27], a[232:235], v[164:167]\n" \
"ds_read_b64_tr_b16 a[4:5], v33 offset:224\n" \
"ds_read_b64_tr_b16 a[6:7], v33 offset:4832\n" \
"v_mfma_f32_16x16x32_bf16 v[232:235], a[24:27], a[240:243], v[232:235]\n" \
"ds_read_b64_tr_b16 a[8:9], v33 offset:256\n" \
"ds_read_b64_tr_b16 a[10:11], v33 offset:4864\n" \
"buffer_load_dwordx4 a[32:35], v20, s[56:59], s69 offen offset:0 sc1\n" \
"v_mfma_f32_16x16x32_bf16 v[236:239], a[24:27], a[248:251], v[236:239]\n" \
"ds_read_b64_tr_b16 a[12:13], v33 offset:9216\n" \
"ds_read_b64_tr_b16 a[14:15], v33 offset:13824\n" \
"v_mfma_f32_16x16x32_bf16 v[168:171], a[28:31], a[224:227], v[168:171]\n" \
"ds_read_b64_tr_b16 a[16:17], v33 offset:9248\n" \
"ds_read_b64_tr_b16 a[18:19], v33 offset:13856\n" \
"v_mfma_f32_16x16x32_bf16 v[172:175], a[28:31], a[232:235], v[172:175]\n" \
"ds_read_b64_tr_b16 a[20:21], v33 offset:9280\n" \
"ds_read_b64_tr_b16 a[22:23], v33 offset:13888\n" \
"v_mfma_f32_16x16x32_bf16 v[240:243], a[28:31], a[240:243], v[240:243]\n" \
"ds_read_b64_tr_b16 a[24:25], v33 offset:9312\n" \
"ds_read_b64_tr_b16 a[26:27], v33 offset:13920\n" \
"buffer_load_dwordx4 a[48:51], v32, s[56:59], s69 offen offset:0 sc1\n" \
"v_mfma_f32_16x16x32_bf16 v[244:247], a[28:31], a[248:251], v[244:247]\n" \
"ds_read_b64_tr_b16 a[28:29], v33 offset:9344\n" \
"ds_read_b64_tr_b16 a[30:31], v33 offset:13952\n" \
"v_mfma_f32_16x16x32_bf16 v[176:179], a[192:195], a[224:227], v[176:179]\n" \
"v_readlane_b32 s68, v11, 49\n" \
"s_waitcnt vmcnt(13)\n" \
"v_cmp_ne_u32_e32 vcc, s62, v47\n" \
"v_mfma_f32_16x16x32_bf16 v[180:183], a[192:195], a[232:235], v[180:183]\n" \
"v_mfma_f32_16x16x32_bf16 v[248:251], a[192:195], a[240:243], v[248:251]\n" \
"buffer_load_dwordx4 a[36:39], v20, s[56:59], s69 offen offset:1024 sc1\n" \
"v_mfma_f32_16x16x32_bf16 v[252:255], a[192:195], a[248:251], v[252:255]\n" \
"ds_read_b64_tr_b16 a[192:193], v33 offset:9376\n" \
"s_waitcnt lgkmcnt(15)\n" \
"v_mfma_f32_16x16x32_bf16 v[112:115], a[196:199], a[64:67], v[112:115]\n" \
"ds_read_b64_tr_b16 a[194:195], v33 offset:13984\n" \
"s_cbranch_vccnz L_sb_%=\n" \
"L_rb_%=:\n" \
"v_mfma_f32_16x16x32_bf16 v[116:119], a[196:199], a[80:83], v[116:119]\n" \
"ds_read_b64_tr_b16 a[196:197], v33 offset:9408\n" \
"ds_read_b64_tr_b16 a[198:199], v33 offset:14016\n" \
"v_mfma_f32_16x16x32_bf16 v[120:123], a[200:203], a[64:67], v[120:123]\n" \
"v_readlane_b32 s71, v11, 52\n" \
"v_cmp_eq_u32_e32 vcc, 0, v44\n" \
"buffer_load_dwordx4 a[52:55], v32, s[56:59], s69 offen offset:1024 sc1\n" \
"v_mfma_f32_16x16x32_bf16 v[124:127], a[200:203], a[80:83], v[124:127]\n" \
"ds_read_b64_tr_b16 a[200:201], v33 offset:9440\n" \
"ds_read_b64_tr_b16 a[202:203], v33 offset:14048\n" \
"v_mfma_f32_16x16x32_bf16 v[128:131], a[204:207], a[64:67], v[128:131]\n" \
"v_cndmask_b32_e32 v50, 0, v45, vcc\n" \
"v_cmp_eq_u32_e32 vcc, 1, v44\n" \
"v_mfma_f32_16x16x32_bf16 v[132:135], a[204:207], a[80:83], v[132:135]\n" \
"ds_read_b64_tr_b16 a[204:205], v33 offset:9472\n" \
"ds_read_b64_tr_b16 a[206:207], v33 offset:14080\n" \
"v_mfma_f32_16x16x32_bf16 v[136:139], a[208:211], a[64:67], v[136:139]\n" \
"v_cndmask_b32_e32 v51, 0, v45, vcc\n" \
"v_bfe_u32 v45, v20, 1, 5\n" \
"buffer_load_dwordx4 a[40:43], v20, s[56:59], s69 offen offset:2048 sc1\n" \
"v_mfma_f32_16x16x32_bf16 v[140:143], a[208:211], a[80:83], v[140:143]\n" \
"ds_read_b64_tr_b16 a[208:209], v33 offset:18432\n" \
"ds_read_b64_tr_b16 a[210:211], v33 offset:23040\n" \
"v_mfma_f32_16x16x32_bf16 v[144:147], a[212:215], a[64:67], v[144:147]\n" \
"v_and_b32_e32 v45, 24, v45\n" \
"v_and_b32_e32 v44, 0x3c0, v20\n" \
"v_mfma_f32_16x16x32_bf16 v[148:151], a[212:215], a[80:83], v[148:151]\n" \
"ds_read_b64_tr_b16 a[212:213], v33 offset:18464\n" \
"ds_read_b64_tr_b16 a[214:215], v33 offset:23072\n" \
"v_mfma_f32_16x16x32_bf16 v[152:155], a[216:219], a[64:67], v[152:155]\n" \
"v_add_u32_e32 v45, 0x4800, v45\n" \
"v_add_u32_e32 v36, v44, v45\n" \
"buffer_load_dwordx4 a[56:59], v32, s[56:59], s69 offen offset:2048 sc1\n" \
"v_mfma_f32_16x16x32_bf16 v[156:159], a[216:219], a[80:83], v[156:159]\n" \
"ds_read_b64_tr_b16 a[216:217], v33 offset:18496\n" \
"ds_read_b64_tr_b16 a[218:219], v33 offset:23104\n" \
"v_mfma_f32_16x16x32_bf16 v[160:163], a[0:3], a[64:67], v[160:163]\n" \
".fill (L_ab + 36864 - .) / 4, 4, 0xBF800000\n" \
"L_ap9:\n" \
"s_cbranch_execz L_ap0\n" \
"v_mfma_f32_16x16x32_bf16 v[164:167], a[0:3], a[80:83], v[164:167]\n" \
"ds_read_b64_tr_b16 a[0:1], v33 offset:18528\n" \
"s_waitcnt lgkmcnt(13)\n" \
"v_mfma_f32_16x16x32_bf16 v[168:171], a[4:7], a[64:67], v[168:171]\n" \
"ds_read_b64_tr_b16 a[2:3], v33 offset:23136\n" \
"buffer_load_dwordx4 a[44:47], v20, s[56:59], s69 offen offset:3072 sc1\n" \
"v_mfma_f32_16x16x32_bf16 v[172:175], a[4:7], a[80:83], v[172:175]\n" \
"ds_read_b64_tr_b16 a[4:5], v33 offset:18560\n" \
"ds_read_b64_tr_b16 a[6:7], v33 offset:23168\n" \
"v_mfma_f32_16x16x32_bf16 v[176:179], a[8:11], a[64:67], v[176:179]\n" \
"v_mfma_f32_16x16x32_bf16 v[180:183], a[8:11], a[80:83], v[180:183]\n" \
"ds_read_b64_tr_b16 a[8:9], v33 offset:18592\n" \
"ds_read_b64_tr_b16 a[10:11], v33 offset:23200\n" \
"v_mfma_f32_16x16x32_bf16 v[112:115], a[12:15], a[68:71], v[112:115]\n" \
"v_mfma_f32_16x16x32_bf16 v[116:119], a[12:15], a[84:87], v[116:119]\n" \
"buffer_load_dwordx4 a[60:63], v32, s[56:59], s69 offen offset:3072 sc1\n" \
"v_mfma_f32_16x16x32_bf16 v[120:123], a[16:19], a[68:71], v[120:123]\n" \
"ds_read_b64_tr_b16 a[12:13], v33 offset:18624\n" \
"ds_read_b64_tr_b16 a[14:15], v33 offset:23232\n" \
"v_mfma_f32_16x16x32_bf16 v[124:127], a[16:19], a[84:87], v[124:127]\n" \
"ds_read_b64_tr_b16 a[16:17], v33 offset:18656\n" \
"ds_read_b64_tr_b16 a[18:19], v33 offset:23264\n" \
"v_mfma_f32_16x16x32_bf16 v[128:131], a[20:23], a[68:71], v[128:131]\n" \
"v_readlane_b32 s69, v11, 50\n" \
"v_mfma_f32_16x16x32_bf16 v[132:135], a[20:23], a[84:87], v[132:135]\n" \
"ds_read_b64_tr_b16 a[20:21], v33 offset:18688\n" \
"ds_read_b64_tr_b16 a[22:23], v33 offset:23296\n" \
"v_mfma_f32_16x16x32_bf16 v[136:139], a[24:27], a[68:71], v[136:139]\n" \
"buffer_load_dwordx4 a[220:223], v20, s[56:59], s64 offen offset:0 sc1\n" \
"v_mfma_f32_16x16x32_bf16 v[140:143], a[24:27], a[84:87], v[140:143]\n" \
"ds_read_b64_tr_b16 a[24:25], v33 offset:27648\n" \
"ds_read_b64_tr_b16 a[26:27], v33 offset:32256\n" \
"v_mfma_f32_16x16x32_bf16 v[144:147], a[28:31], a[68:71], v[144:147]\n" \
"v_mfma_f32_16x16x32_bf16 v[148:151], a[28:31], a[84:87], v[148:151]\n" \
"ds_read_b64_tr_b16 a[28:29], v33 offset:27680\n" \
"ds_read_b64_tr_b16 a[30:31], v33 offset:32288\n" \
"v_mfma_f32_16x16x32_bf16 v[152:155], a[192:195], a[68:71], v[152:155]\n" \
"v_mfma_f32_16x16x32_bf16 v[156:159], a[192:195], a[84:87], v[156:159]\n" \
"ds_read_b64_tr_b16 a[192:193], v33 offset:27712\n" \
"s_waitcnt lgkmcnt(13)\n" \
"v_mfma_f32_16x16x32_bf16 v[160:163], a[196:199], a[68:71], v[160:163]\n" \
"buffer_load_dwordx4 a[236:239], v32, s[56:59], s64 offen offset:0 sc1\n" \
"v_mfma_f32_16x16x32_bf16 v[164:167], a[196:199], a[84:87], v[164:167]\n" \
"ds_read_b64_tr_b16 a[194:195], v33 offset:32320\n" \
"ds_read_b64_tr_b16 a[196:197], v33 offset:27744\n" \
"v_mfma_f32_16x16x32_bf16 v[168:171], a[200:203], a[68:71], v[168:171]\n" \
"ds_read_b64_tr_b16 a[198:199], v33 offset:32352\n" \
"v_mfma_f32_16x16x32_bf16 v[172:175], a[200:203], a[84:87], v[172:175]\n" \
"ds_read_b64_tr_b16 a[200:201], v33 offset:27776\n" \
"ds_read_b64_tr_b16 a[202:203], v33 offset:32384\n" \
"v_mfma_f32_16x16x32_bf16 v[176:179], a[204:207], a[68:71], v[176:179]\n" \
"v_mfma_f32_16x16x32_bf16 v[180:183], a[204:207], a[84:87], v[180:183]\n" \
"ds_read_b64_tr_b16 a[204:205], v33 offset:27808\n" \
"ds_read_b64_tr_b16 a[206:207], v33 offset:32416\n" \
"v_mfma_f32_16x16x32_bf16 v[112:115], a[208:211], a[72:75], v[112:115]\n" \
"buffer_load_dwordx4 a[224:227], v20, s[56:59], s64 offen offset:1024 sc1\n" \
"v_mfma_f32_16x16x32_bf16 v[116:119], a[208:211], a[88:91], v[116:119]\n" \
"ds_read_b64_tr_b16 a[208:209], v33 offset:27840\n" \
"ds_read_b64_tr_b16 a[210:211], v33 offset:32448\n" \
"v_mfma_f32_16x16x32_bf16 v[120:123], a[212:215], a[72:75], v[120:123]\n" \
"v_mfma_f32_16x16x32_bf16 v[124:127], a[212:215], a[88:91], v[124:127]\n" \
"ds_read_b64_tr_b16 a[212:213], v33 offset:27872\n" \
"ds_read_b64_tr_b16 a[214:215], v33 offset:32480\n" \
"v_mfma_f32_16x16x32_bf16 v[128:131], a[216:219], a[72:75], v[128:131]\n" \
"v_mfma_f32_16x16x32_bf16 v[132:135], a[216:219], a[88:91], v[132:135]\n" \
"ds_read_b64_tr_b16 a[216:217], v33 offset:27904\n" \
"ds_read_b64_tr_b16 a[218:219], v33 offset:32512\n" \
"v_mfma_f32_16x16x32_bf16 v[136:139], a[0:3], a[72:75], v[136:139]\n" \
"buffer_load_dwordx4 a[240:243], v32, s[56:59], s64 offen offset:1024 sc1\n" \
"v_mfma_f32_16x16x32_bf16 v[140:143], a[0:3], a[88:91], v[140:143]\n" \
"v_mfma_f32_16x16x32_bf16 v[144:147], a[4:7], a[72:75], v[144:147]\n" \
"v_mfma_f32_16x16x32_bf16 v[148:151], a[4:7], a[88:91], v[148:151]\n" \
"s_waitcnt lgkmcnt(12)\n" \
"v_mfma_f32_16x16x32_bf16 v[152:155], a[8:11], a[72:75], v[152:155]\n" \
"v_mfma_f32_16x16x32_bf16 v[156:159], a[8:11], a[88:91], v[156:159]\n" \
"buffer_load_dwordx4 a[228:231], v20, s[56:59], s64 offen offset:2048 sc1\n" \
"v_mfma_f32_16x16x32_bf16 v[160:163], a[12:15], a[72:75], v[160:163]\n" \
"v_mfma_f32_16x16x32_bf16 v[164:167], a[12:15], a[88:91], v[164:167]\n" \
"v_mfma_f32_16x16x32_bf16 v[168:171], a[16:19], a[72:75], v[168:171]\n" \
"v_mfma_f32_16x16x32_bf16 v[172:175], a[16:19], a[88:91], v[172:175]\n" \
"v_mfma_f32_16x16x32_bf16 v[176:179], a[20:23], a[72:75], v[176:179]\n" \
"buffer_load_dwordx4 a[244:247], v32, s[56:59], s64 offen offset:2048 sc1\n" \
"v_mfma_f32_16x16x32_bf16 v[180:183], a[20:23], a[88:91], v[180:183]\n" \
"s_waitcnt vmcnt(24)\n" \
"s_barrier\n" \
"s_waitcnt vmcnt(23)\n" \
"v_mfma_f32_16x16x32_bf16 v[112:115], a[24:27], a[76:79], v[112:115]\n" \
"ds_read_b64_tr_b16 a[0:1], v33 offset:36864\n" \
"s_waitcnt vmcnt(22)\n" \
"v_mfma_f32_16x16x32_bf16 v[116:119], a[24:27], a[92:95], v[116:119]\n" \
"ds_read_b64_tr_b16 a[2:3], v33 offset:41472\n" \
"ds_read_b64_tr_b16 a[4:5], v33 offset:46080\n" \
"v_mfma_f32_16x16x32_bf16 v[120:123], a[28:31], a[76:79], v[120:123]\n" \
"ds_read_b64_tr_b16 a[6:7], v33 offset:50688\n" \
"ds_read_b64_tr_b16 a[8:9], v33 offset:55296\n" \
"v_mfma_f32_16x16x32_bf16 v[124:127], a[28:31], a[92:95], v[124:127]\n" \
"ds_read_b64_tr_b16 a[10:11], v33 offset:59904\n" \
"ds_read_b64_tr_b16 a[12:13], v10 offset:0\n" \
"v_mfma_f32_16x16x32_bf16 v[128:131], a[192:195], a[76:79], v[128:131]\n" \
"ds_read_b64_tr_b16 a[14:15], v10 offset:4608\n" \
"ds_read_b64_tr_b16 a[16:17], v34 offset:0\n" \
"v_mfma_f32_16x16x32_bf16 v[132:135], a[192:195], a[92:95], v[132:135]\n" \
"ds_read_b64_tr_b16 a[18:19], v34 offset:4608\n" \
"s_waitcnt lgkmcnt(10)\n" \
"v_mfma_f32_16x16x32_bf16 v[136:139], a[196:199], a[76:79], v[136:139]\n" \
"ds_read_b64_tr_b16 a[20:21], v34 offset:9216\n" \
"ds_read_b64_tr_b16 a[22:23], v34 offset:13824\n" \
"v_mfma_f32_16x16x32_bf16 v[140:143], a[196:199], a[92:95], v[140:143]\n" \
"ds_read_b64_tr_b16 a[24:25], v34 offset:18432\n" \
"ds_read_b64_tr_b16 a[26:27], v34 offset:23040\n" \
"v_mfma_f32_16x16x32_bf16 v[144:147], a[200:203], a[76:79], v[144:147]\n" \
"ds_read_b64_tr_b16 a[28:29], v10 offset:9216\n" \
"ds_read_b64_tr_b16 a[30:31], v10 offset:13824\n" \
"v_mfma_f32_16x16x32_bf16 v[148:151], a[200:203], a[92:95], v[148:151]\n" \
"ds_read_b64_tr_b16 a[192:193], v33 offset:36896\n" \
"ds_read_b64_tr_b16 a[194:195], v33 offset:41504\n" \
"v_mfma_f32_16x16x32_bf16 v[152:155], a[204:207], a[76:79], v[152:155]\n" \
"ds_read_b64_tr_b16 a[196:197], v33 offset:46112\n" \
"ds_read_b64_tr_b16 a[198:199], v33 offset:50720\n" \
"v_mfma_f32_16x16x32_bf16 v[156:159], a[204:207], a[92:95], v[156:159]\n" \
"ds_read_b64_tr_b16 a[200:201], v33 offset:55328\n" \
"ds_read_b64_tr_b16 a[202:203], v33 offset:59936\n" \
"v_mfma_f32_16x16x32_bf16 v[160:163], a[208:211], a[76:79], v[160:163]\n" \
"ds_read_b64_tr_b16 a[204:205], v10 offset:32\n" \
"ds_read_b64_tr_b16 a[206:207], v10 offset:4640\n" \
"v_mfma_f32_16x16x32_bf16 v[164:167], a[208:211], a[92:95], v[164:167]\n" \
"ds_read_b64_tr_b16 a[208:209], v34 offset:32\n" \
"ds_read_b64_tr_b16 a[210:211], v34 offset:4640\n" \
"v_mfma_f32_16x16x32_bf16 v[168:171], a[212:215], a[76:79], v[168:171]\n" \
"buffer_load_dwordx4 a[232:235], v20, s[56:59], s64 offen offset:3072 sc1\n" \
"v_mfma_f32_16x16x32_bf16 v[172:175], a[212:215], a[92:95], v[172:175]\n" \
"ds_read_b64_tr_b16 a[212:213], v34 offset:9248\n" \
"ds_read_b64_tr_b16 a[214:215], v34 offset:13856\n" \
"v_mfma_f32_16x16x32_bf16 v[176:179], a[216:219], a[76:79], v[176:179]\n" \
"v_mfma_f32_16x16x32_bf16 v[180:183], a[216:219], a[92:95], v[180:183]\n" \
"ds_read_b64_tr_b16 a[216:217], v34 offset:18464\n" \
"s_waitcnt vmcnt(22) lgkmcnt(15)\n" \
"v_mfma_f32_16x16x32_bf16 v[112:115], a[0:3], a[128:131], v[112:115]\n" \
"ds_read_b64_tr_b16 a[218:219], v34 offset:23072\n" \
"s_waitcnt vmcnt(21)\n" \
"v_mfma_f32_16x16x32_bf16 v[116:119], a[0:3], a[144:147], v[116:119]\n" \
"ds_read_b64_tr_b16 a[0:1], v10 offset:9248\n" \
"s_waitcnt vmcnt(20)\n" \
"v_mfma_f32_16x16x32_bf16 v[112:115], a[4:7], a[132:135], v[112:115]\n" \
"ds_read_b64_tr_b16 a[2:3], v10 offset:13856\n" \
"s_waitcnt vmcnt(19)\n" \
"v_mfma_f32_16x16x32_bf16 v[116:119], a[4:7], a[148:151], v[116:119]\n" \
"ds_read_b64_tr_b16 a[4:5], v33 offset:36928\n" \
"s_waitcnt vmcnt(18)\n" \
"v_mfma_f32_16x16x32_bf16 v[112:115], a[8:11], a[136:139], v[112:115]\n" \
"ds_read_b64_tr_b16 a[6:7], v33 offset:41536\n" \
"s_waitcnt vmcnt(17)\n" \
"v_mfma_f32_16x16x32_bf16 v[116:119], a[8:11], a[152:155], v[116:119]\n" \
"ds_read_b64_tr_b16 a[8:9], v33 offset:46144\n" \
"s_waitcnt vmcnt(16)\n" \
"v_mfma_f32_16x16x32_bf16 v[112:115], a[12:15], a[140:143], v[112:115]\n" \
"ds_read_b64_tr_b16 a[10:11], v33 offset:50752\n" \
"s_waitcnt vmcnt(15)\n" \
"v_mfma_f32_16x16x32_bf16 v[116:119], a[12:15], a[156:159], v[116:119]\n" \
"ds_read_b64_tr_b16 a[12:13], v33 offset:55360\n" \
"ds_read_b64_tr_b16 a[14:15], v33 offset:59968\n" \
"buffer_load_dwordx4 a[248:251], v32, s[56:59], s64 offen offset:3072 sc1\n" \
"s_waitcnt vmcnt(15)\n" \
"v_mfma_f32_16x16x32_bf16 v[112:115], a[16:19], a[32:35], v[112:115]\n" \
"buffer_load_dwordx4 a[160:163], v20, s[56:59], s72 offen offset:0 sc1\n" \
"s_waitcnt vmcnt(15)\n" \
"v_mfma_f32_16x16x32_bf16 v[116:119], a[16:19], a[48:51], v[116:119]\n" \
"ds_read_b64_tr_b16 a[16:17], v10 offset:64\n" \
"ds_read_b64_tr_b16 a[18:19], v10 offset:4672\n" \
"buffer_load_dwordx4 a[176:179], v32, s[56:59], s72 offen offset:0 sc1\n" \
"s_waitcnt vmcnt(15)\n" \
"v_mfma_f32_16x16x32_bf16 v[112:115], a[20:23], a[36:39], v[112:115]\n" \
"buffer_load_dwordx4 a[164:167], v20, s[56:59], s72 offen offset:1024 sc1\n" \
"s_waitcnt vmcnt(15)\n" \
"v_mfma_f32_16x16x32_bf16 v[116:119], a[20:23], a[52:55], v[116:119]\n" \
"ds_read_b64_tr_b16 a[20:21], v34 offset:64\n" \
"ds_read_b64_tr_b16 a[22:23], v34 offset:4672\n" \
"buffer_load_dwordx4 a[180:183], v32, s[56:59], s72 offen offset:1024 sc1\n" \
"s_waitcnt vmcnt(15)\n" \
"v_mfma_f32_16x16x32_bf16 v[112:115], a[24:27], a[40:43], v[112:115]\n" \
"buffer_load_dwordx4 a[168:171], v20, s[56:59], s72 offen offset:2048 sc1\n" \
"s_waitcnt vmcnt(15)\n" \
"v_mfma_f32_16x16x32_bf16 v[116:119], a[24:27], a[56:59], v[116:119]\n" \
"ds_read_b64_tr_b16 a[24:25], v34 offset:9280\n" \
"ds_read_b64_tr_b16 a[26:27], v34 offset:13888\n" \
"buffer_load_dwordx4 a[184:187], v32, s[56:59], s72 offen offset:2048 sc1\n" \
"s_waitcnt vmcnt(15) lgkmcnt(4)\n" \
"v_mfma_f32_16x16x32_bf16 v[112:115], a[28:31], a[44:47], v[112:115]\n" \
"buffer_load_dwordx4 a[172:175], v20, s[56:59], s72 offen offset:3072 sc1\n" \
"s_waitcnt vmcnt(15)\n" \
"v_mfma_f32_16x16x32_bf16 v[116:119], a[28:31], a[60:63], v[116:119]\n" \
"ds_read_b64_tr_b16 a[28:29], v34 offset:18496\n" \
"ds_read_b64_tr_b16 a[30:31], v34 offset:23104\n" \
"v_mfma_f32_16x16x32_bf16 v[120:123], a[192:195], a[128:131], v[120:123]\n" \
"buffer_load_dwordx4 a[188:191], v32, s[56:59], s72 offen offset:3072 sc1\n" \
"v_mfma_f32_16x16x32_bf16 v[124:127], a[192:195], a[144:147], v[124:127]\n" \
"v_cvt_pk_bf16_f32 v112, v112, v113\n" \
"v_cvt_pk_bf16_f32 v113, v114, v115\n" \
"v_mfma_f32_16x16x32_bf16 v[120:123], a[196:199], a[132:135], v[120:123]\n" \
"v_cvt_pk_bf16_f32 v114, v116, v117\n" \
"v_cvt_pk_bf16_f32 v115, v118, v119\n" \
"buffer_store_dwordx4 v[112:115], v20, s[56:59], s65 offen offset:0 sc1\n" \
"s_add_u32 s65, s65, 0x3800\n" \
"v_mfma_f32_16x16x32_bf16 v[124:127], a[196:199], a[148:151], v[124:127]\n" \
"ds_read_b64_tr_b16 a[192:193], v10 offset:9280\n" \
"ds_read_b64_tr_b16 a[194:195], v10 offset:13888\n" \
"v_mfma_f32_16x16x32_bf16 v[120:123], a[200:203], a[136:139], v[120:123]\n" \
"ds_read_b64_tr_b16 a[196:197], v33 offset:36960\n" \
"ds_read_b64_tr_b16 a[198:199], v33 offset:41568\n" \
"v_mfma_f32_16x16x32_bf16 v[124:127], a[200:203], a[152:155], v[124:127]\n" \
"ds_read_b64_tr_b16 a[200:201], v33 offset:46176\n" \
"ds_read_b64_tr_b16 a[202:203], v33 offset:50784\n" \
"v_mfma_f32_16x16x32_bf16 v[120:123], a[204:207], a[140:143], v[120:123]\n" \
"buffer_load_dwordx4 a[96:99], v20, s[56:59], s93 offen offset:0 sc1\n" \
"v_mfma_f32_16x16x32_bf16 v[124:127], a[204:207], a[156:159], v[124:127]\n" \
"ds_read_b64_tr_b16 a[204:205], v33 offset:55392\n" \
"ds_read_b64_tr_b16 a[206:207], v33 offset:60000\n" \
"v_mfma_f32_16x16x32_bf16 v[120:123], a[208:211], a[32:35], v[120:123]\n" \
"v_readlane_b32 s72, v11, 53\n" \
"v_mfma_f32_16x16x32_bf16 v[124:127], a[208:211], a[48:51], v[124:127]\n" \
"ds_read_b64_tr_b16 a[208:209], v10 offset:96\n" \
"ds_read_b64_tr_b16 a[210:211], v10 offset:4704\n" \
"v_mfma_f32_16x16x32_bf16 v[120:123], a[212:215], a[36:39], v[120:123]\n" \
"v_mfma_f32_16x16x32_bf16 v[124:127], a[212:215], a[52:55], v[124:127]\n" \
"ds_read_b64_tr_b16 a[212:213], v34 offset:96\n" \
"ds_read_b64_tr_b16 a[214:215], v34 offset:4704\n" \
"v_mfma_f32_16x16x32_bf16 v[120:123], a[216:219], a[40:43], v[120:123]\n" \
"buffer_load_dwordx4 a[112:115], v32, s[56:59], s93 offen offset:0 sc1\n" \
"v_mfma_f32_16x16x32_bf16 v[124:127], a[216:219], a[56:59], v[124:127]\n" \
"ds_read_b64_tr_b16 a[216:217], v34 offset:9312\n" \
"ds_read_b64_tr_b16 a[218:219], v34 offset:13920\n" \
"v_mfma_f32_16x16x32_bf16 v[120:123], a[0:3], a[44:47], v[120:123]\n" \
"v_mfma_f32_16x16x32_bf16 v[124:127], a[0:3], a[60:63], v[124:127]\n" \
"ds_read_b64_tr_b16 a[0:1], v34 offset:18528\n" \
"ds_read_b64_tr_b16 a[2:3], v34 offset:23136\n" \
"v_mfma_f32_16x16x32_bf16 v[128:131], a[4:7], a[128:131], v[128:131]\n" \
"v_mfma_f32_16x16x32_bf16 v[132:135], a[4:7], a[144:147], v[132:135]\n" \
"buffer_load_dwordx4 a[100:103], v20, s[56:59], s93 offen offset:1024 sc1\n" \
"v_mfma_f32_16x16x32_bf16 v[128:131], a[8:11], a[132:135], v[128:131]\n" \
"ds_read_b64_tr_b16 a[4:5], v10 offset:9312\n" \
"v_cvt_pk_bf16_f32 v120, v120, v121\n" \
"v_cvt_pk_bf16_f32 v121, v122, v123\n" \
"v_cvt_pk_bf16_f32 v122, v124, v125\n" \
"v_cvt_pk_bf16_f32 v123, v126, v127\n" \
"v_mfma_f32_16x16x32_bf16 v[132:135], a[8:11], a[148:151], v[132:135]\n" \
"buffer_store_dwordx4 v[120:123], v20, s[56:59], s66 offen offset:0 sc1\n" \
"s_add_u32 s66, s66, 0x3000\n" \
"v_mfma_f32_16x16x32_bf16 v[128:131], a[12:15], a[136:139], v[128:131]\n" \
"ds_read_b64_tr_b16 a[6:7], v10 offset:13920\n" \
"ds_read_b64_tr_b16 a[8:9], v33 offset:36992\n" \
"v_mfma_f32_16x16x32_bf16 v[132:135], a[12:15], a[152:155], v[132:135]\n" \
"ds_read_b64_tr_b16 a[10:11], v33 offset:41600\n" \
"ds_read_b64_tr_b16 a[12:13], v33 offset:46208\n" \
"v_mfma_f32_16x16x32_bf16 v[128:131], a[16:19], a[140:143], v[128:131]\n" \
"ds_read_b64_tr_b16 a[14:15], v33 offset:50816\n" \
"v_mfma_f32_16x16x32_bf16 v[132:135], a[16:19], a[156:159], v[132:135]\n" \
"ds_read_b64_tr_b16 a[16:17], v33 offset:55424\n" \
"s_waitcnt lgkmcnt(13)\n" \
"v_mfma_f32_16x16x32_bf16 v[128:131], a[20:23], a[32:35], v[128:131]\n" \
"buffer_load_dwordx4 a[116:119], v32, s[56:59], s93 offen offset:1024 sc1\n" \
"v_mfma_f32_16x16x32_bf16 v[132:135], a[20:23], a[48:51], v[132:135]\n" \
"ds_read_b64_tr_b16 a[18:19], v33 offset:60032\n" \
"ds_read_b64_tr_b16 a[20:21], v10 offset:128\n" \
"v_mfma_f32_16x16x32_bf16 v[128:131], a[24:27], a[36:39], v[128:131]\n" \
"ds_read_b64_tr_b16 a[22:23], v10 offset:4736\n" \
"v_mfma_f32_16x16x32_bf16 v[132:135], a[24:27], a[52:55], v[132:135]\n" \
"ds_read_b64_tr_b16 a[24:25], v34 offset:128\n" \
"ds_read_b64_tr_b16 a[26:27], v34 offset:4736\n" \
"v_mfma_f32_16x16x32_bf16 v[128:131], a[28:31], a[40:43], v[128:131]\n" \
"v_mfma_f32_16x16x32_bf16 v[132:135], a[28:31], a[56:59], v[132:135]\n" \
"buffer_load_dwordx4 a[104:107], v20, s[56:59], s93 offen offset:2048 sc1\n" \
"v_mfma_f32_16x16x32_bf16 v[128:131], a[192:195], a[44:47], v[128:131]\n" \
"ds_read_b64_tr_b16 a[28:29], v34 offset:9344\n" \
"ds_read_b64_tr_b16 a[30:31], v34 offset:13952\n" \
"v_mfma_f32_16x16x32_bf16 v[132:135], a[192:195], a[60:63], v[132:135]\n" \
"ds_read_b64_tr_b16 a[192:193], v34 offset:18560\n" \
"ds_read_b64_tr_b16 a[194:195], v34 offset:23168\n" \
"v_mfma_f32_16x16x32_bf16 v[136:139], a[196:199], a[128:131], v[136:139]\n" \
"v_mfma_f32_16x16x32_bf16 v[140:143], a[196:199], a[144:147], v[140:143]\n" \
"ds_read_b64_tr_b16 a[196:197], v10 offset:9344\n" \
"v_cvt_pk_bf16_f32 v128, v128, v129\n" \
"v_cvt_pk_bf16_f32 v129, v130, v131\n" \
"v_mfma_f32_16x16x32_bf16 v[136:139], a[200:203], a[132:135], v[136:139]\n" \
"v_cvt_pk_bf16_f32 v130, v132, v133\n" \
"v_cvt_pk_bf16_f32 v131, v134, v135\n" \
"buffer_store_dwordx4 v[128:131], v20, s[56:59], s67 offen offset:0 sc1\n" \
"s_add_u32 s67, s67, 0x2800\n" \
"v_mfma_f32_16x16x32_bf16 v[140:143], a[200:203], a[148:151], v[140:143]\n" \
"ds_read_b64_tr_b16 a[198:199], v10 offset:13952\n" \
"ds_read_b64_tr_b16 a[200:201], v33 offset:37024\n" \
"v_mfma_f32_16x16x32_bf16 v[136:139], a[204:207], a[136:139], v[136:139]\n" \
"ds_read_b64_tr_b16 a[202:203], v33 offset:41632\n" \
"v_mfma_f32_16x16x32_bf16 v[140:143], a[204:207], a[152:155], v[140:143]\n" \
"ds_read_b64_tr_b16 a[204:205], v33 offset:46240\n" \
"ds_read_b64_tr_b16 a[206:207], v33 offset:50848\n" \
"v_mfma_f32_16x16x32_bf16 v[136:139], a[208:211], a[140:143], v[136:139]\n" \
"buffer_load_dwordx4 a[120:123], v32, s[56:59], s93 offen offset:2048 sc1\n" \
"v_mfma_f32_16x16x32_bf16 v[140:143], a[208:211], a[156:159], v[140:143]\n" \
"ds_read_b64_tr_b16 a[208:209], v33 offset:55456\n" \
"s_waitcnt lgkmcnt(13)\n" \
"v_mfma_f32_16x16x32_bf16 v[136:139], a[212:215], a[32:35], v[136:139]\n" \
"ds_read_b64_tr_b16 a[210:211], v33 offset:60064\n" \
"v_mfma_f32_16x16x32_bf16 v[140:143], a[212:215], a[48:51], v[140:143]\n" \
"ds_read_b64_tr_b16 a[212:213], v10 offset:160\n" \
"ds_read_b64_tr_b16 a[214:215], v10 offset:4768\n" \
"v_mfma_f32_16x16x32_bf16 v[136:139], a[216:219], a[36:39], v[136:139]\n" \
"v_mfma_f32_16x16x32_bf16 v[140:143], a[216:219], a[52:55], v[140:143]\n" \
"buffer_load_dwordx4 a[108:111], v20, s[56:59], s93 offen offset:3072 sc1\n" \
"v_mfma_f32_16x16x32_bf16 v[136:139], a[0:3], a[40:43], v[136:139]\n" \
"ds_read_b64_tr_b16 a[216:217], v34 offset:160\n" \
"ds_read_b64_tr_b16 a[218:219], v34 offset:4768\n" \
"v_mfma_f32_16x16x32_bf16 v[140:143], a[0:3], a[56:59], v[140:143]\n" \
"ds_read_b64_tr_b16 a[0:1], v34 offset:9376\n" \
"ds_read_b64_tr_b16 a[2:3], v34 offset:13984\n" \
"v_mfma_f32_16x16x32_bf16 v[136:139], a[4:7], a[44:47], v[136:139]\n" \
"v_mfma_f32_16x16x32_bf16 v[140:143], a[4:7], a[60:63], v[140:143]\n" \
"ds_read_b64_tr_b16 a[4:5], v34 offset:18592\n" \
"ds_read_b64_tr_b16 a[6:7], v34 offset:23200\n" \
"buffer_load_dwordx4 a[124:127], v32, s[56:59], s93 offen offset:3072 sc1\n" \
"v_mfma_f32_16x16x32_bf16 v[144:147], a[8:11], a[128:131], v[144:147]\n" \
"v_readlane_b32 s93, v11, 47\n" \
"v_mfma_f32_16x16x32_bf16 v[148:151], a[8:11], a[144:147], v[148:151]\n" \
"ds_read_b64_tr_b16 a[8:9], v10 offset:9376\n" \
"v_cvt_pk_bf16_f32 v136, v136, v137\n" \
"v_cvt_pk_bf16_f32 v137, v138, v139\n" \
"v_cvt_pk_bf16_f32 v138, v140, v141\n" \
"v_cvt_pk_bf16_f32 v139, v142, v143\n" \
"buffer_store_dwordx4 v[136:139], v20, s[56:59], s68 offen offset:0 sc1\n" \
"s_add_u32 s68, s68, 0x2000\n" \
"v_mfma_f32_16x16x32_bf16 v[144:147], a[12:15], a[132:135], v[144:147]\n" \
"ds_read_b64_tr_b16 a[10:11], v10 offset:13984\n" \
"v_mfma_f32_16x16x32_bf16 v[148:151], a[12:15], a[148:151], v[148:151]\n" \
"ds_read_b64_tr_b16 a[12:13], v33 offset:37056\n" \
"ds_read_b64_tr_b16 a[14:15], v33 offset:41664\n" \
"v_mfma_f32_16x16x32_bf16 v[144:147], a[16:19], a[136:139], v[144:147]\n" \
"v_mfma_f32_16x16x32_bf16 v[148:151], a[16:19], a[152:155], v[148:151]\n" \
"ds_read_b64_tr_b16 a[16:17], v33 offset:46272\n" \
"ds_read_b64_tr_b16 a[18:19], v33 offset:50880\n" \
"v_mfma_f32_16x16x32_bf16 v[144:147], a[20:23], a[140:143], v[144:147]\n" \
"buffer_load_dwordx4 v[64:67], v20, s[56:59], s95 offen offset:0 sc1\n" \
"v_mfma_f32_16x16x32_bf16 v[148:151], a[20:23], a[156:159], v[148:151]\n" \
"ds_read_b64_tr_b16 a[20:21], v33 offset:55488\n" \
"s_waitcnt lgkmcnt(13)\n" \
"v_mfma_f32_16x16x32_bf16 v[144:147], a[24:27], a[32:35], v[144:147]\n" \
"ds_read_b64_tr_b16 a[22:23], v33 offset:60096\n" \
"v_mfma_f32_16x16x32_bf16 v[148:151], a[24:27], a[48:51], v[148:151]\n" \
"ds_read_b64_tr_b16 a[24:25], v10 offset:192\n" \
"ds_read_b64_tr_b16 a[26:27], v10 offset:4800\n" \
"v_mfma_f32_16x16x32_bf16 v[144:147], a[28:31], a[36:39], v[144:147]\n" \
"v_mfma_f32_16x16x32_bf16 v[148:151], a[28:31], a[52:55], v[148:151]\n" \
"ds_read_b64_tr_b16 a[28:29], v34 offset:192\n" \
"ds_read_b64_tr_b16 a[30:31], v34 offset:4800\n" \
"v_mfma_f32_16x16x32_bf16 v[144:147], a[192:195], a[40:43], v[144:147]\n" \
"buffer_load_dwordx4 v[68:71], v20, s[56:59], s95 offen offset:1024 sc1\n" \
"v_mfma_f32_16x16x32_bf16 v[148:151], a[192:195], a[56:59], v[148:151]\n" \
"ds_read_b64_tr_b16 a[192:193], v34 offset:9408\n" \
"ds_read_b64_tr_b16 a[194:195], v34 offset:14016\n" \
"v_mfma_f32_16x16x32_bf16 v[144:147], a[196:199], a[44:47], v[144:147]\n" \
"v_mfma_f32_16x16x32_bf16 v[148:151], a[196:199], a[60:63], v[148:151]\n" \
"ds_read_b64_tr_b16 a[196:197], v34 offset:18624\n" \
"ds_read_b64_tr_b16 a[198:199], v34 offset:23232\n" \
"v_mfma_f32_16x16x32_bf16 v[152:155], a[200:203], a[128:131], v[152:155]\n" \
"v_mfma_f32_16x16x32_bf16 v[156:159], a[200:203], a[144:147], v[156:159]\n" \
"ds_read_b64_tr_b16 a[200:201], v10 offset:9408\n" \
"ds_read_b64_tr_b16 a[202:203], v10 offset:14016\n" \
"v_mfma_f32_16x16x32_bf16 v[152:155], a[204:207], a[132:135], v[152:155]\n" \
"v_cvt_pk_bf16_f32 v144, v144, v145\n" \
"v_cvt_pk_bf16_f32 v145, v146, v147\n" \
"v_cvt_pk_bf16_f32 v146, v148, v149\n" \
"v_cvt_pk_bf16_f32 v147, v150, v151\n" \
"buffer_store_dwordx4 v[144:147], v20, s[56:59], s69 offen offset:0 sc1\n" \
"s_add_u32 s69, s69, 0x1800\n" \
"v_mfma_f32_16x16x32_bf16 v[156:159], a[204:207], a[148:151], v[156:159]\n" \
"ds_read_b64_tr_b16 a[204:205], v33 offset:256\n" \
"ds_read_b64_tr_b16 a[206:207], v33 offset:4864\n" \
"v_mfma_f32_16x16x32_bf16 v[152:155], a[208:211], a[136:139], v[152:155]\n" \
"v_mfma_f32_16x16x32_bf16 v[156:159], a[208:211], a[152:155], v[156:159]\n" \
"ds_read_b64_tr_b16 a[208:209], v33 offset:9472\n" \
"ds_read_b64_tr_b16 a[210:211], v33 offset:14080\n" \
"v_mfma_f32_16x16x32_bf16 v[152:155], a[212:215], a[140:143], v[152:155]\n" \
"buffer_load_dwordx4 v[72:75], v32, s[56:59], s95 offen offset:0 sc1\n" \
"v_mfma_f32_16x16x32_bf16 v[156:159], a[212:215], a[156:159], v[156:159]\n" \
"ds_read_b64_tr_b16 a[212:213], v33 offset:18688\n" \
"s_waitcnt lgkmcnt(13)\n" \
"v_mfma_f32_16x16x32_bf16 v[152:155], a[216:219], a[32:35], v[152:155]\n" \
"ds_read_b64_tr_b16 a[214:215], v33 offset:23296\n" \
"v_mfma_f32_16x16x32_bf16 v[156:159], a[216:219], a[48:51], v[156:159]\n" \
"ds_read_b64_tr_b16 a[216:217], v33 offset:27904\n" \
"ds_read_b64_tr_b16 a[218:219], v33 offset:32512\n" \
"v_mfma_f32_16x16x32_bf16 v[152:155], a[0:3], a[36:39], v[152:155]\n" \
"v_mfma_f32_16x16x32_bf16 v[156:159], a[0:3], a[52:55], v[156:159]\n" \
"ds_read_b64_tr_b16 a[0:1], v33 offset:37120\n" \
"ds_read_b64_tr_b16 a[2:3], v33 offset:41728\n" \
"v_mfma_f32_16x16x32_bf16 v[152:155], a[4:7], a[40:43], v[152:155]\n" \
"buffer_load_dwordx4 v[76:79], v32, s[56:59], s95 offen offset:1024 sc1\n" \
"v_mfma_f32_16x16x32_bf16 v[156:159], a[4:7], a[56:59], v[156:159]\n" \
"ds_read_b64_tr_b16 a[4:5], v33 offset:46336\n" \
"ds_read_b64_tr_b16 a[6:7], v33 offset:50944\n" \
"v_mfma_f32_16x16x32_bf16 v[152:155], a[8:11], a[44:47], v[152:155]\n" \
"v_readlane_b32 s95, v11, 49\n" \
"v_mfma_f32_16x16x32_bf16 v[156:159], a[8:11], a[60:63], v[156:159]\n" \
"ds_read_b64_tr_b16 a[8:9], v33 offset:55552\n" \
"ds_read_b64_tr_b16 a[10:11], v33 offset:60160\n" \
"v_mfma_f32_16x16x32_bf16 v[160:163], a[12:15], a[128:131], v[160:163]\n" \
"v_mfma_f32_16x16x32_bf16 v[164:167], a[12:15], a[144:147], v[164:167]\n" \
"ds_read_b64_tr_b16 a[12:13], v10 offset:256\n" \
"ds_read_b64_tr_b16 a[14:15], v10 offset:4864\n" \
"v_cvt_pk_bf16_f32 v152, v152, v153\n" \
"v_cvt_pk_bf16_f32 v153, v154, v155\n" \
"v_cvt_pk_bf16_f32 v154, v156, v157\n" \
"v_cvt_pk_bf16_f32 v155, v158, v159\n" \
"buffer_store_dwordx4 v[152:155], v20, s[56:59], s70 offen offset:0 sc1\n" \
"s_add_u32 s70, s70, 0x1000\n" \
"v_mfma_f32_16x16x32_bf16 v[160:163], a[16:19], a[132:135], v[160:163]\n" \
"v_mfma_f32_16x16x32_bf16 v[164:167], a[16:19], a[148:151], v[164:167]\n" \
"ds_read_b64_tr_b16 a[16:17], v34 offset:256\n" \
"ds_read_b64_tr_b16 a[18:19], v34 offset:4864\n" \
"v_mfma_f32_16x16x32_bf16 v[160:163], a[20:23], a[136:139], v[160:163]\n" \
"v_mfma_f32_16x16x32_bf16 v[164:167], a[20:23], a[152:155], v[164:167]\n" \
"ds_read_b64_tr_b16 a[20:21], v34 offset:9472\n" \
"ds_read_b64_tr_b16 a[22:23], v34 offset:14080\n" \
"v_mfma_f32_16x16x32_bf16 v[160:163], a[24:27], a[140:143], v[160:163]\n" \
"buffer_load_dwordx4 v[80:83], v20, s[56:59], s96 offen offset:0 sc1\n" \
"v_mfma_f32_16x16x32_bf16 v[164:167], a[24:27], a[156:159], v[164:167]\n" \
"ds_read_b64_tr_b16 a[24:25], v34 offset:18688\n" \
"s_waitcnt lgkmcnt(12)\n" \
"v_mfma_f32_16x16x32_bf16 v[160:163], a[28:31], a[32:35], v[160:163]\n" \
"ds_read_b64_tr_b16 a[26:27], v34 offset:23296\n" \
"v_mfma_f32_16x16x32_bf16 v[164:167], a[28:31], a[48:51], v[164:167]\n" \
"ds_read_b64_tr_b16 a[28:29], v10 offset:9472\n" \
"ds_read_b64_tr_b16 a[30:31], v10 offset:14080\n" \
"s_waitcnt vmcnt(18)\n" \
"s_mov_b64 exec, 1\n" \
"buffer_store_dword v39, off, s[56:59], s65 offset:0 sc1\n" \
"s_mov_b64 exec, -1\n" \
"v_mfma_f32_16x16x32_bf16 v[160:163], a[192:195], a[36:39], v[160:163]\n" \
"v_mfma_f32_16x16x32_bf16 v[164:167], a[192:195], a[52:55], v[164:167]\n" \
"ds_read_b64_tr_b16 a[192:193], v33 offset:0\n" \
"ds_read_b64_tr_b16 a[194:195], v33 offset:4608\n" \
"v_mfma_f32_16x16x32_bf16 v[160:163], a[196:199], a[40:43], v[160:163]\n" \
"v_mfma_f32_16x16x32_bf16 v[164:167], a[196:199], a[56:59], v[164:167]\n" \
"ds_read_b64_tr_b16 a[196:197], v33 offset:9216\n" \
"ds_read_b64_tr_b16 a[198:199], v33 offset:13824\n" \
"v_mfma_f32_16x16x32_bf16 v[160:163], a[200:203], a[44:47], v[160:163]\n" \
"buffer_load_dwordx4 v[84:87], v20, s[56:59], s96 offen offset:1024 sc1\n" \
"v_mfma_f32_16x16x32_bf16 v[164:167], a[200:203], a[60:63], v[164:167]\n" \
"ds_read_b64_tr_b16 a[200:201], v33 offset:18432\n" \
"ds_read_b64_tr_b16 a[202:203], v33 offset:23040\n" \
"v_mfma_f32_16x16x32_bf16 v[248:251], a[204:207], a[220:223], v[248:251]\n" \
"v_mfma_f32_16x16x32_bf16 v[252:255], a[204:207], a[236:239], v[252:255]\n" \
"ds_read_b64_tr_b16 a[204:205], v33 offset:27648\n" \
"ds_read_b64_tr_b16 a[206:207], v33 offset:32256\n" \
"v_cvt_pk_bf16_f32 v160, v160, v161\n" \
"v_cvt_pk_bf16_f32 v161, v162, v163\n" \
"v_cvt_pk_bf16_f32 v162, v164, v165\n" \
"v_cvt_pk_bf16_f32 v163, v166, v167\n" \
"buffer_store_dwordx4 v[160:163], v20, s[56:59], s71 offen offset:0 sc1\n" \
"s_add_u32 s71, s71, 0x800\n" \
"v_mfma_f32_16x16x32_bf16 v[248:251], a[208:211], a[224:227], v[248:251]\n" \
"v_mfma_f32_16x16x32_bf16 v[252:255], a[208:211], a[240:243], v[252:255]\n" \
"ds_read_b64_tr_b16 a[208:209], v33 offset:36864\n" \
"ds_read_b64_tr_b16 a[210:211], v33 offset:41472\n" \
"v_mfma_f32_16x16x32_bf16 v[248:251], a[212:215], a[228:231], v[248:251]\n" \
"v_mfma_f32_16x16x32_bf16 v[252:255], a[212:215], a[244:247], v[252:255]\n" \
"ds_read_b64_tr_b16 a[212:213], v33 offset:46080\n" \
"ds_read_b64_tr_b16 a[214:215], v33 offset:50688\n" \
"v_mfma_f32_16x16x32_bf16 v[248:251], a[216:219], a[232:235], v[248:251]\n" \
"buffer_load_dwordx4 v[88:91], v32, s[56:59], s96 offen offset:0 sc1\n" \
"v_mfma_f32_16x16x32_bf16 v[252:255], a[216:219], a[248:251], v[252:255]\n" \
"ds_read_b64_tr_b16 a[216:217], v33 offset:55296\n" \
"s_waitcnt lgkmcnt(12)\n" \
"v_mfma_f32_16x16x32_bf16 v[176:179], a[0:3], a[128:131], v[176:179]\n" \
"ds_read_b64_tr_b16 a[218:219], v33 offset:59904\n" \
"s_waitcnt vmcnt(18)\n" \
"s_mov_b64 exec, 1\n" \
"buffer_store_dword v39, off, s[56:59], s66 offset:4 sc1\n" \
"s_mov_b64 exec, -1\n" \
"v_mfma_f32_16x16x32_bf16 v[180:183], a[0:3], a[144:147], v[180:183]\n" \
"v_mfma_f32_16x16x32_bf16 v[248:251], a[0:3], a[160:163], v[248:251]\n" \
"v_mfma_f32_16x16x32_bf16 v[252:255], a[0:3], a[176:179], v[252:255]\n" \
"ds_read_b64_tr_b16 a[0:1], v10 offset:0\n" \
"ds_read_b64_tr_b16 a[2:3], v10 offset:4608\n" \
"v_mfma_f32_16x16x32_bf16 v[176:179], a[4:7], a[132:135], v[176:179]\n" \
"buffer_load_dwordx4 v[92:95], v32, s[56:59], s96 offen offset:1024 sc1\n" \
"v_mfma_f32_16x16x32_bf16 v[180:183], a[4:7], a[148:151], v[180:183]\n" \
"v_readlane_b32 s96, v11, 50\n" \
"v_mfma_f32_16x16x32_bf16 v[248:251], a[4:7], a[164:167], v[248:251]\n" \
"v_mfma_f32_16x16x32_bf16 v[252:255], a[4:7], a[180:183], v[252:255]\n" \
"ds_read_b64_tr_b16 a[4:5], v34 offset:0\n" \
"ds_read_b64_tr_b16 a[6:7], v34 offset:4608\n" \
"v_mfma_f32_16x16x32_bf16 v[176:179], a[8:11], a[136:139], v[176:179]\n" \
"v_mfma_f32_16x16x32_bf16 v[180:183], a[8:11], a[152:155], v[180:183]\n" \
"buffer_load_dwordx4 v[96:99], v20, s[56:59], s97 offen offset:0 sc1\n" \
"v_mfma_f32_16x16x32_bf16 v[248:251], a[8:11], a[168:171], v[248:251]\n" \
"v_mfma_f32_16x16x32_bf16 v[252:255], a[8:11], a[184:187], v[252:255]\n" \
"ds_read_b64_tr_b16 a[8:9], v34 offset:9216\n" \
"ds_read_b64_tr_b16 a[10:11], v34 offset:13824\n" \
"v_mfma_f32_16x16x32_bf16 v[176:179], a[12:15], a[140:143], v[176:179]\n" \
"v_mfma_f32_16x16x32_bf16 v[180:183], a[12:15], a[156:159], v[180:183]\n" \
"v_mfma_f32_16x16x32_bf16 v[248:251], a[12:15], a[172:175], v[248:251]\n" \
"buffer_load_dwordx4 v[104:107], v32, s[56:59], s97 offen offset:0 sc1\n" \
"v_mfma_f32_16x16x32_bf16 v[252:255], a[12:15], a[188:191], v[252:255]\n" \
"ds_read_b64_tr_b16 a[12:13], v34 offset:18432\n" \
"ds_read_b64_tr_b16 a[14:15], v34 offset:23040\n" \
"s_waitcnt vmcnt(19)\n" \
"s_mov_b64 exec, 1\n" \
"buffer_store_dword v39, off, s[56:59], s67 offset:8 sc1\n" \
"s_mov_b64 exec, -1\n" \
"v_mfma_f32_16x16x32_bf16 v[176:179], a[16:19], a[32:35], v[176:179]\n" \
"v_mfma_f32_16x16x32_bf16 v[180:183], a[16:19], a[48:51], v[180:183]\n" \
"v_mfma_f32_16x16x32_bf16 v[248:251], a[16:19], a[96:99], v[248:251]\n" \
"v_mfma_f32_16x16x32_bf16 v[252:255], a[16:19], a[112:115], v[252:255]\n" \
"ds_read_b64_tr_b16 a[16:17], v10 offset:9216\n" \
"ds_read_b64_tr_b16 a[18:19], v10 offset:13824\n" \
"v_mfma_f32_16x16x32_bf16 v[176:179], a[20:23], a[36:39], v[176:179]\n" \
"buffer_load_dwordx4 v[100:103], v20, s[56:59], s97 offen offset:1024 sc1\n" \
"v_mfma_f32_16x16x32_bf16 v[180:183], a[20:23], a[52:55], v[180:183]\n" \
"v_mfma_f32_16x16x32_bf16 v[248:251], a[20:23], a[100:103], v[248:251]\n" \
"v_mfma_f32_16x16x32_bf16 v[252:255], a[20:23], a[116:119], v[252:255]\n" \
"ds_read_b64_tr_b16 a[20:21], v33 offset:32\n" \
"ds_read_b64_tr_b16 a[22:23], v33 offset:4640\n" \
"v_mfma_f32_16x16x32_bf16 v[176:179], a[24:27], a[40:43], v[176:179]\n" \
"v_mfma_f32_16x16x32_bf16 v[180:183], a[24:27], a[56:59], v[180:183]\n" \
"buffer_load_dwordx4 v[108:111], v32, s[56:59], s97 offen offset:1024 sc1\n" \
"v_mfma_f32_16x16x32_bf16 v[248:251], a[24:27], a[104:107], v[248:251]\n" \
"s_waitcnt vmcnt(21)\n" \
"v_mfma_f32_16x16x32_bf16 v[252:255], a[24:27], a[120:123], v[252:255]\n" \
"ds_read_b64_tr_b16 a[24:25], v33 offset:9248\n" \
"ds_read_b64_tr_b16 a[26:27], v33 offset:13856\n" \
"v_mfma_f32_16x16x32_bf16 v[176:179], a[28:31], a[44:47], v[176:179]\n" \
"v_mfma_f32_16x16x32_bf16 v[180:183], a[28:31], a[60:63], v[180:183]\n" \
"s_waitcnt vmcnt(20)\n" \
".fill (L_ab + 40960 - .) / 4, 4, 0xBF800000\n" \
"L_ap10:\n" \
"s_cbranch_execz L_ap0\n" \
"v_mfma_f32_16x16x32_bf16 v[248:251], a[28:31], a[108:111], v[248:251]\n" \
"s_waitcnt vmcnt(19)\n" \
"v_mfma_f32_16x16x32_bf16 v[252:255], a[28:31], a[124:127], v[252:255]\n" \
"ds_read_b64_tr_b16 a[28:29], v33 offset:18464\n" \
"s_waitcnt vmcnt(18)\n" \
"s_mov_b64 exec, 1\n" \
"buffer_store_dword v39, off, s[56:59], s68 offset:12 sc1\n" \
"s_mov_b64 exec, -1\n" \
"v_cvt_pk_bf16_f32 v176, v176, v177\n" \
"v_cvt_pk_bf16_f32 v248, v248, v249\n" \
"v_cvt_pk_bf16_f32 v177, v178, v179\n" \
"v_cvt_pk_bf16_f32 v249, v250, v251\n" \
"v_cvt_pk_bf16_f32 v178, v180, v181\n" \
"v_cvt_pk_bf16_f32 v179, v182, v183\n" \
"v_cvt_pk_bf16_f32 v250, v252, v253\n" \
"v_cvt_pk_bf16_f32 v251, v254, v255\n" \
"buffer_store_dwordx4 v[176:179], v20, s[56:59], s72 offen offset:0 sc1\n" \
"s_waitcnt lgkmcnt(5)\n" \
"v_mfma_f32_16x16x32_bf16 v[184:187], a[192:195], a[220:223], v[184:187]\n" \
"ds_read_b64_tr_b16 a[30:31], v33 offset:23072\n" \
"v_mfma_f32_16x16x32_bf16 v[188:191], a[192:195], a[236:239], v[188:191]\n" \
"ds_read_b64_tr_b16 a[192:193], v33 offset:27680\n" \
"ds_read_b64_tr_b16 a[194:195], v33 offset:32288\n" \
"buffer_store_dwordx4 v[248:251], v20, s[56:59], s72 offen offset:1024 sc1\n" \
"v_mfma_f32_16x16x32_bf16 v[184:187], a[196:199], a[224:227], v[184:187]\n" \
"v_readlane_b32 s72, v11, 52\n" \
"v_mfma_f32_16x16x32_bf16 v[188:191], a[196:199], a[240:243], v[188:191]\n" \
"ds_read_b64_tr_b16 a[196:197], v33 offset:36896\n" \
"ds_read_b64_tr_b16 a[198:199], v33 offset:41504\n" \
"v_mfma_f32_16x16x32_bf16 v[184:187], a[200:203], a[228:231], v[184:187]\n" \
"v_mfma_f32_16x16x32_bf16 v[188:191], a[200:203], a[244:247], v[188:191]\n" \
"ds_read_b64_tr_b16 a[200:201], v33 offset:46112\n" \
"ds_read_b64_tr_b16 a[202:203], v33 offset:50720\n" \
"v_mfma_f32_16x16x32_bf16 v[184:187], a[204:207], a[232:235], v[184:187]\n" \
"v_mfma_f32_16x16x32_bf16 v[188:191], a[204:207], a[248:251], v[188:191]\n" \
"ds_read_b64_tr_b16 a[204:205], v33 offset:55328\n" \
"ds_read_b64_tr_b16 a[206:207], v33 offset:59936\n" \
"v_mfma_f32_16x16x32_bf16 v[184:187], a[208:211], a[160:163], v[184:187]\n" \
"v_mfma_f32_16x16x32_bf16 v[188:191], a[208:211], a[176:179], v[188:191]\n" \
"ds_read_b64_tr_b16 a[208:209], v10 offset:32\n" \
"ds_read_b64_tr_b16 a[210:211], v10 offset:4640\n" \
"v_mfma_f32_16x16x32_bf16 v[184:187], a[212:215], a[164:167], v[184:187]\n" \
"v_mfma_f32_16x16x32_bf16 v[188:191], a[212:215], a[180:183], v[188:191]\n" \
"ds_read_b64_tr_b16 a[212:213], v34 offset:32\n" \
"ds_read_b64_tr_b16 a[214:215], v34 offset:4640\n" \
"v_mfma_f32_16x16x32_bf16 v[184:187], a[216:219], a[168:171], v[184:187]\n" \
"v_mfma_f32_16x16x32_bf16 v[188:191], a[216:219], a[184:187], v[188:191]\n" \
"ds_read_b64_tr_b16 a[216:217], v34 offset:9248\n" \
"ds_read_b64_tr_b16 a[218:219], v34 offset:13856\n" \
"s_waitcnt vmcnt(18)\n" \
"s_mov_b64 exec, 1\n" \
"buffer_store_dword v39, off, s[56:59], s69 offset:16 sc1\n" \
"s_mov_b64 exec, -1\n" \
"v_mfma_f32_16x16x32_bf16 v[184:187], a[0:3], a[172:175], v[184:187]\n" \
"v_mfma_f32_16x16x32_bf16 v[188:191], a[0:3], a[188:191], v[188:191]\n" \
"ds_read_b64_tr_b16 a[0:1], v34 offset:18464\n" \
"ds_read_b64_tr_b16 a[2:3], v34 offset:23072\n" \
"v_mfma_f32_16x16x32_bf16 v[184:187], a[4:7], a[96:99], v[184:187]\n" \
"v_mfma_f32_16x16x32_bf16 v[188:191], a[4:7], a[112:115], v[188:191]\n" \
"ds_read_b64_tr_b16 a[4:5], v10 offset:9248\n" \
"ds_read_b64_tr_b16 a[6:7], v10 offset:13856\n" \
"v_mfma_f32_16x16x32_bf16 v[184:187], a[8:11], a[100:103], v[184:187]\n" \
"v_mfma_f32_16x16x32_bf16 v[188:191], a[8:11], a[116:119], v[188:191]\n" \
"ds_read_b64_tr_b16 a[8:9], v33 offset:64\n" \
"ds_read_b64_tr_b16 a[10:11], v33 offset:4672\n" \
"v_mfma_f32_16x16x32_bf16 v[184:187], a[12:15], a[104:107], v[184:187]\n" \
"v_mfma_f32_16x16x32_bf16 v[188:191], a[12:15], a[120:123], v[188:191]\n" \
"ds_read_b64_tr_b16 a[12:13], v33 offset:9280\n" \
"ds_read_b64_tr_b16 a[14:15], v33 offset:13888\n" \
"v_mfma_f32_16x16x32_bf16 v[184:187], a[16:19], a[108:111], v[184:187]\n" \
"v_mfma_f32_16x16x32_bf16 v[188:191], a[16:19], a[124:127], v[188:191]\n" \
"ds_read_b64_tr_b16 a[16:17], v33 offset:18496\n" \
"s_waitcnt lgkmcnt(13)\n" \
"v_mfma_f32_16x16x32_bf16 v[192:195], a[20:23], a[220:223], v[192:195]\n" \
"ds_read_b64_tr_b16 a[18:19], v33 offset:23104\n" \
"v_mfma_f32_16x16x32_bf16 v[196:199], a[20:23], a[236:239], v[196:199]\n" \
"ds_read_b64_tr_b16 a[20:21], v33 offset:27712\n" \
"ds_read_b64_tr_b16 a[22:23], v33 offset:32320\n" \
"v_cvt_pk_bf16_f32 v184, v184, v185\n" \
"v_cvt_pk_bf16_f32 v185, v186, v187\n" \
"v_cvt_pk_bf16_f32 v186, v188, v189\n" \
"v_cvt_pk_bf16_f32 v187, v190, v191\n" \
"buffer_store_dwordx4 v[184:187], v20, s[56:59], s92 offen offset:1024 sc1\n" \
"s_add_u32 s92, s92, 0x3800\n" \
"v_mfma_f32_16x16x32_bf16 v[192:195], a[24:27], a[224:227], v[192:195]\n" \
"v_mfma_f32_16x16x32_bf16 v[196:199], a[24:27], a[240:243], v[196:199]\n" \
"ds_read_b64_tr_b16 a[24:25], v33 offset:36928\n" \
"ds_read_b64_tr_b16 a[26:27], v33 offset:41536\n" \
"s_waitcnt vmcnt(17)\n" \
"s_mov_b64 exec, 1\n" \
"buffer_store_dword v39, off, s[56:59], s70 offset:20 sc1\n" \
"s_mov_b64 exec, -1\n" \
"v_mfma_f32_16x16x32_bf16 v[192:195], a[28:31], a[228:231], v[192:195]\n" \
"v_readlane_b32 s70, v11, 42\n" \
"v_mfma_f32_16x16x32_bf16 v[196:199], a[28:31], a[244:247], v[196:199]\n" \
"ds_read_b64_tr_b16 a[28:29], v33 offset:46144\n" \
"ds_read_b64_tr_b16 a[30:31], v33 offset:50752\n" \
"v_mfma_f32_16x16x32_bf16 v[192:195], a[192:195], a[232:235], v[192:195]\n" \
"v_mfma_f32_16x16x32_bf16 v[196:199], a[192:195], a[248:251], v[196:199]\n" \
"ds_read_b64_tr_b16 a[192:193], v33 offset:55360\n" \
"ds_read_b64_tr_b16 a[194:195], v33 offset:59968\n" \
"v_mfma_f32_16x16x32_bf16 v[192:195], a[196:199], a[160:163], v[192:195]\n" \
"v_mfma_f32_16x16x32_bf16 v[196:199], a[196:199], a[176:179], v[196:199]\n" \
"ds_read_b64_tr_b16 a[196:197], v10 offset:64\n" \
"ds_read_b64_tr_b16 a[198:199], v10 offset:4672\n" \
"v_mfma_f32_16x16x32_bf16 v[192:195], a[200:203], a[164:167], v[192:195]\n" \
"v_mfma_f32_16x16x32_bf16 v[196:199], a[200:203], a[180:183], v[196:199]\n" \
"ds_read_b64_tr_b16 a[200:201], v34 offset:64\n" \
"ds_read_b64_tr_b16 a[202:203], v34 offset:4672\n" \
"v_mfma_f32_16x16x32_bf16 v[192:195], a[204:207], a[168:171], v[192:195]\n" \
"v_mfma_f32_16x16x32_bf16 v[196:199], a[204:207], a[184:187], v[196:199]\n" \
"ds_read_b64_tr_b16 a[204:205], v34 offset:9280\n" \
"ds_read_b64_tr_b16 a[206:207], v34 offset:13888\n" \
"v_mfma_f32_16x16x32_bf16 v[192:195], a[208:211], a[172:175], v[192:195]\n" \
"v_mfma_f32_16x16x32_bf16 v[196:199], a[208:211], a[188:191], v[196:199]\n" \
"ds_read_b64_tr_b16 a[208:209], v34 offset:18496\n" \
"s_waitcnt lgkmcnt(13)\n" \
"v_mfma_f32_16x16x32_bf16 v[192:195], a[212:215], a[96:99], v[192:195]\n" \
"ds_read_b64_tr_b16 a[210:211], v34 offset:23104\n" \
"v_mfma_f32_16x16x32_bf16 v[196:199], a[212:215], a[112:115], v[196:199]\n" \
"ds_read_b64_tr_b16 a[212:213], v10 offset:9280\n" \
"ds_read_b64_tr_b16 a[214:215], v10 offset:13888\n" \
"v_mfma_f32_16x16x32_bf16 v[192:195], a[216:219], a[100:103], v[192:195]\n" \
"v_mfma_f32_16x16x32_bf16 v[196:199], a[216:219], a[116:119], v[196:199]\n" \
"ds_read_b64_tr_b16 a[216:217], v33 offset:96\n" \
"ds_read_b64_tr_b16 a[218:219], v33 offset:4704\n" \
"v_mfma_f32_16x16x32_bf16 v[192:195], a[0:3], a[104:107], v[192:195]\n" \
"v_mfma_f32_16x16x32_bf16 v[196:199], a[0:3], a[120:123], v[196:199]\n" \
"ds_read_b64_tr_b16 a[0:1], v33 offset:9312\n" \
"ds_read_b64_tr_b16 a[2:3], v33 offset:13920\n" \
"s_waitcnt vmcnt(14)\n" \
"s_mov_b64 exec, 1\n" \
"buffer_store_dword v39, off, s[56:59], s71 offset:24 sc1\n" \
"s_mov_b64 exec, -1\n" \
"v_mfma_f32_16x16x32_bf16 v[192:195], a[4:7], a[108:111], v[192:195]\n" \
"v_readlane_b32 s71, v11, 51\n" \
"v_mfma_f32_16x16x32_bf16 v[196:199], a[4:7], a[124:127], v[196:199]\n" \
"ds_read_b64_tr_b16 a[4:5], v33 offset:18528\n" \
"ds_read_b64_tr_b16 a[6:7], v33 offset:23136\n" \
"v_mfma_f32_16x16x32_bf16 v[200:203], a[8:11], a[220:223], v[200:203]\n" \
"v_mfma_f32_16x16x32_bf16 v[204:207], a[8:11], a[236:239], v[204:207]\n" \
"ds_read_b64_tr_b16 a[8:9], v33 offset:27744\n" \
"ds_read_b64_tr_b16 a[10:11], v33 offset:32352\n" \
"v_cvt_pk_bf16_f32 v192, v192, v193\n" \
"v_cvt_pk_bf16_f32 v193, v194, v195\n" \
"v_cvt_pk_bf16_f32 v194, v196, v197\n" \
"v_cvt_pk_bf16_f32 v195, v198, v199\n" \
"buffer_store_dwordx4 v[192:195], v20, s[56:59], s93 offen offset:1024 sc1\n" \
"s_add_u32 s93, s93, 0x3000\n" \
"v_mfma_f32_16x16x32_bf16 v[200:203], a[12:15], a[224:227], v[200:203]\n" \
"v_mfma_f32_16x16x32_bf16 v[204:207], a[12:15], a[240:243], v[204:207]\n" \
"ds_read_b64_tr_b16 a[12:13], v33 offset:36960\n" \
"ds_read_b64_tr_b16 a[14:15], v33 offset:41568\n" \
"v_mfma_f32_16x16x32_bf16 v[200:203], a[16:19], a[228:231], v[200:203]\n" \
"v_mfma_f32_16x16x32_bf16 v[204:207], a[16:19], a[244:247], v[204:207]\n" \
"ds_read_b64_tr_b16 a[16:17], v33 offset:46176\n" \
"ds_read_b64_tr_b16 a[18:19], v33 offset:50784\n" \
"v_mfma_f32_16x16x32_bf16 v[200:203], a[20:23], a[232:235], v[200:203]\n" \
"v_mfma_f32_16x16x32_bf16 v[204:207], a[20:23], a[248:251], v[204:207]\n" \
"ds_read_b64_tr_b16 a[20:21], v33 offset:55392\n" \
"s_waitcnt lgkmcnt(11)\n" \
"v_mfma_f32_16x16x32_bf16 v[200:203], a[24:27], a[160:163], v[200:203]\n" \
"ds_read_b64_tr_b16 a[22:23], v33 offset:60000\n" \
"v_mfma_f32_16x16x32_bf16 v[204:207], a[24:27], a[176:179], v[204:207]\n" \
"ds_read_b64_tr_b16 a[24:25], v10 offset:96\n" \
"ds_read_b64_tr_b16 a[26:27], v10 offset:4704\n" \
"v_mfma_f32_16x16x32_bf16 v[200:203], a[28:31], a[164:167], v[200:203]\n" \
"v_mfma_f32_16x16x32_bf16 v[204:207], a[28:31], a[180:183], v[204:207]\n" \
"ds_read_b64_tr_b16 a[28:29], v34 offset:96\n" \
"ds_read_b64_tr_b16 a[30:31], v34 offset:4704\n" \
"v_mfma_f32_16x16x32_bf16 v[200:203], a[192:195], a[168:171], v[200:203]\n" \
"v_mfma_f32_16x16x32_bf16 v[204:207], a[192:195], a[184:187], v[204:207]\n" \
"ds_read_b64_tr_b16 a[192:193], v34 offset:9312\n" \
"ds_read_b64_tr_b16 a[194:195], v34 offset:13920\n" \
"v_mfma_f32_16x16x32_bf16 v[200:203], a[196:199], a[172:175], v[200:203]\n" \
"v_mfma_f32_16x16x32_bf16 v[204:207], a[196:199], a[188:191], v[204:207]\n" \
"ds_read_b64_tr_b16 a[196:197], v34 offset:18528\n" \
"ds_read_b64_tr_b16 a[198:199], v34 offset:23136\n" \
"v_mfma_f32_16x16x32_bf16 v[200:203], a[200:203], a[96:99], v[200:203]\n" \
"v_mfma_f32_16x16x32_bf16 v[204:207], a[200:203], a[112:115], v[204:207]\n" \
"ds_read_b64_tr_b16 a[200:201], v10 offset:9312\n" \
"ds_read_b64_tr_b16 a[202:203], v10 offset:13920\n" \
"v_mfma_f32_16x16x32_bf16 v[200:203], a[204:207], a[100:103], v[200:203]\n" \
"v_mfma_f32_16x16x32_bf16 v[204:207], a[204:207], a[116:119], v[204:207]\n" \
"ds_read_b64_tr_b16 a[204:205], v33 offset:128\n" \
"ds_read_b64_tr_b16 a[206:207], v33 offset:4736\n" \
"v_mfma_f32_16x16x32_bf16 v[200:203], a[208:211], a[104:107], v[200:203]\n" \
"v_mfma_f32_16x16x32_bf16 v[204:207], a[208:211], a[120:123], v[204:207]\n" \
"ds_read_b64_tr_b16 a[208:209], v33 offset:9344\n" \
"ds_read_b64_tr_b16 a[210:211], v33 offset:13952\n" \
"v_mfma_f32_16x16x32_bf16 v[200:203], a[212:215], a[108:111], v[200:203]\n" \
"v_mfma_f32_16x16x32_bf16 v[204:207], a[212:215], a[124:127], v[204:207]\n" \
"ds_read_b64_tr_b16 a[212:213], v33 offset:18560\n" \
"ds_read_b64_tr_b16 a[214:215], v33 offset:23168\n" \
"v_mfma_f32_16x16x32_bf16 v[208:211], a[216:219], a[220:223], v[208:211]\n" \
"v_mfma_f32_16x16x32_bf16 v[212:215], a[216:219], a[236:239], v[212:215]\n" \
"ds_read_b64_tr_b16 a[216:217], v33 offset:27776\n" \
"s_waitcnt lgkmcnt(13)\n" \
"v_mfma_f32_16x16x32_bf16 v[208:211], a[0:3], a[224:227], v[208:211]\n" \
"v_cvt_pk_bf16_f32 v200, v200, v201\n" \
"v_cvt_pk_bf16_f32 v201, v202, v203\n" \
"v_cvt_pk_bf16_f32 v202, v204, v205\n" \
"v_cvt_pk_bf16_f32 v203, v206, v207\n" \
"buffer_store_dwordx4 v[200:203], v20, s[56:59], s94 offen offset:1024 sc1\n" \
"s_add_u32 s94, s94, 0x2800\n" \
"v_mfma_f32_16x16x32_bf16 v[212:215], a[0:3], a[240:243], v[212:215]\n" \
"ds_read_b64_tr_b16 a[218:219], v33 offset:32384\n" \
"ds_read_b64_tr_b16 a[0:1], v33 offset:36992\n" \
"v_mfma_f32_16x16x32_bf16 v[208:211], a[4:7], a[228:231], v[208:211]\n" \
"ds_read_b64_tr_b16 a[2:3], v33 offset:41600\n" \
"v_mfma_f32_16x16x32_bf16 v[212:215], a[4:7], a[244:247], v[212:215]\n" \
"ds_read_b64_tr_b16 a[4:5], v33 offset:46208\n" \
"ds_read_b64_tr_b16 a[6:7], v33 offset:50816\n" \
"v_mfma_f32_16x16x32_bf16 v[208:211], a[8:11], a[232:235], v[208:211]\n" \
"v_mfma_f32_16x16x32_bf16 v[212:215], a[8:11], a[248:251], v[212:215]\n" \
"ds_read_b64_tr_b16 a[8:9], v33 offset:55424\n" \
"ds_read_b64_tr_b16 a[10:11], v33 offset:60032\n" \
"v_mfma_f32_16x16x32_bf16 v[208:211], a[12:15], a[160:163], v[208:211]\n" \
"v_mfma_f32_16x16x32_bf16 v[212:215], a[12:15], a[176:179], v[212:215]\n" \
"ds_read_b64_tr_b16 a[12:13], v10 offset:128\n" \
"ds_read_b64_tr_b16 a[14:15], v10 offset:4736\n" \
"v_mfma_f32_16x16x32_bf16 v[208:211], a[16:19], a[164:167], v[208:211]\n" \
"v_mfma_f32_16x16x32_bf16 v[212:215], a[16:19], a[180:183], v[212:215]\n" \
"ds_read_b64_tr_b16 a[16:17], v34 offset:128\n" \
"ds_read_b64_tr_b16 a[18:19], v34 offset:4736\n" \
"v_mfma_f32_16x16x32_bf16 v[208:211], a[20:23], a[168:171], v[208:211]\n" \
"v_mfma_f32_16x16x32_bf16 v[212:215], a[20:23], a[184:187], v[212:215]\n" \
"ds_read_b64_tr_b16 a[20:21], v34 offset:9344\n" \
"ds_read_b64_tr_b16 a[22:23], v34 offset:13952\n" \
"v_mfma_f32_16x16x32_bf16 v[208:211], a[24:27], a[172:175], v[208:211]\n" \
"v_mfma_f32_16x16x32_bf16 v[212:215], a[24:27], a[188:191], v[212:215]\n" \
"ds_read_b64_tr_b16 a[24:25], v34 offset:18560\n" \
"ds_read_b64_tr_b16 a[26:27], v34 offset:23168\n" \
"s_waitcnt vmcnt(6)\n" \
"s_mov_b64 exec, 3\n" \
"buffer_store_dword v39, v41, s[56:59], s73 offen sc1\n" \
"s_mov_b64 exec, -1\n" \
"v_mfma_f32_16x16x32_bf16 v[208:211], a[28:31], a[96:99], v[208:211]\n" \
"v_mfma_f32_16x16x32_bf16 v[212:215], a[28:31], a[112:115], v[212:215]\n" \
"ds_read_b64_tr_b16 a[28:29], v10 offset:9344\n" \
"s_waitcnt lgkmcnt(13)\n" \
"v_mfma_f32_16x16x32_bf16 v[208:211], a[192:195], a[100:103], v[208:211]\n" \
"ds_read_b64_tr_b16 a[30:31], v10 offset:13952\n" \
"v_mfma_f32_16x16x32_bf16 v[212:215], a[192:195], a[116:119], v[212:215]\n" \
"ds_read_b64_tr_b16 a[192:193], v33 offset:160\n" \
"ds_read_b64_tr_b16 a[194:195], v33 offset:4768\n" \
"v_mfma_f32_16x16x32_bf16 v[208:211], a[196:199], a[104:107], v[208:211]\n" \
"v_mfma_f32_16x16x32_bf16 v[212:215], a[196:199], a[120:123], v[212:215]\n" \
"ds_read_b64_tr_b16 a[196:197], v33 offset:9376\n" \
"ds_read_b64_tr_b16 a[198:199], v33 offset:13984\n" \
"v_mfma_f32_16x16x32_bf16 v[208:211], a[200:203], a[108:111], v[208:211]\n" \
"v_mfma_f32_16x16x32_bf16 v[212:215], a[200:203], a[124:127], v[212:215]\n" \
"ds_read_b64_tr_b16 a[200:201], v33 offset:18592\n" \
"ds_read_b64_tr_b16 a[202:203], v33 offset:23200\n" \
"v_mfma_f32_16x16x32_bf16 v[216:219], a[204:207], a[220:223], v[216:219]\n" \
"v_mfma_f32_16x16x32_bf16 v[220:223], a[204:207], a[236:239], v[220:223]\n" \
"ds_read_b64_tr_b16 a[204:205], v33 offset:27808\n" \
"ds_read_b64_tr_b16 a[206:207], v33 offset:32416\n" \
"v_mfma_f32_16x16x32_bf16 v[216:219], a[208:211], a[224:227], v[216:219]\n" \
"v_cvt_pk_bf16_f32 v208, v208, v209\n" \
"v_cvt_pk_bf16_f32 v209, v210, v211\n" \
"v_cvt_pk_bf16_f32 v210, v212, v213\n" \
"v_cvt_pk_bf16_f32 v211, v214, v215\n" \
"buffer_store_dwordx4 v[208:211], v20, s[56:59], s95 offen offset:1024 sc1\n" \
"s_add_u32 s95, s95, 0x2000\n" \
"v_mfma_f32_16x16x32_bf16 v[220:223], a[208:211], a[240:243], v[220:223]\n" \
"ds_read_b64_tr_b16 a[208:209], v33 offset:37024\n" \
"ds_read_b64_tr_b16 a[210:211], v33 offset:41632\n" \
"v_mfma_f32_16x16x32_bf16 v[216:219], a[212:215], a[228:231], v[216:219]\n" \
"v_mfma_f32_16x16x32_bf16 v[220:223], a[212:215], a[244:247], v[220:223]\n" \
"ds_read_b64_tr_b16 a[212:213], v33 offset:46240\n" \
"ds_read_b64_tr_b16 a[214:215], v33 offset:50848\n" \
"v_mfma_f32_16x16x32_bf16 v[216:219], a[216:219], a[232:235], v[216:219]\n" \
"v_mfma_f32_16x16x32_bf16 v[220:223], a[216:219], a[248:251], v[220:223]\n" \
"ds_read_b64_tr_b16 a[216:217], v33 offset:55456\n" \
"ds_read_b64_tr_b16 a[218:219], v33 offset:60064\n" \
"v_mfma_f32_16x16x32_bf16 v[216:219], a[0:3], a[160:163], v[216:219]\n" \
"v_mfma_f32_16x16x32_bf16 v[220:223], a[0:3], a[176:179], v[220:223]\n" \
"ds_read_b64_tr_b16 a[0:1], v10 offset:160\n" \
"s_waitcnt lgkmcnt(13)\n" \
"v_mfma_f32_16x16x32_bf16 v[216:219], a[4:7], a[164:167], v[216:219]\n" \
"ds_read_b64_tr_b16 a[2:3], v10 offset:4768\n" \
"v_mfma_f32_16x16x32_bf16 v[220:223], a[4:7], a[180:183], v[220:223]\n" \
"ds_read_b64_tr_b16 a[4:5], v34 offset:160\n" \
"ds_read_b64_tr_b16 a[6:7], v34 offset:4768\n" \
"v_mfma_f32_16x16x32_bf16 v[216:219], a[8:11], a[168:171], v[216:219]\n" \
"v_mfma_f32_16x16x32_bf16 v[220:223], a[8:11], a[184:187], v[220:223]\n" \
"ds_read_b64_tr_b16 a[8:9], v34 offset:9376\n" \
"ds_read_b64_tr_b16 a[10:11], v34 offset:13984\n" \
"v_mfma_f32_16x16x32_bf16 v[216:219], a[12:15], a[172:175], v[216:219]\n" \
"v_mfma_f32_16x16x32_bf16 v[220:223], a[12:15], a[188:191], v[220:223]\n" \
"ds_read_b64_tr_b16 a[12:13], v34 offset:18592\n" \
"ds_read_b64_tr_b16 a[14:15], v34 offset:23200\n" \
"s_waitcnt vmcnt(6)\n" \
"s_mov_b64 exec, 1\n" \
"buffer_store_dword v39, off, s[56:59], s92 offset:32 sc1\n" \
"s_mov_b64 exec, -1\n" \
"v_mfma_f32_16x16x32_bf16 v[216:219], a[16:19], a[96:99], v[216:219]\n" \
"v_mfma_f32_16x16x32_bf16 v[220:223], a[16:19], a[112:115], v[220:223]\n" \
"ds_read_b64_tr_b16 a[16:17], v10 offset:9376\n" \
"ds_read_b64_tr_b16 a[18:19], v10 offset:13984\n" \
"v_mfma_f32_16x16x32_bf16 v[216:219], a[20:23], a[100:103], v[216:219]\n" \
"v_mfma_f32_16x16x32_bf16 v[220:223], a[20:23], a[116:119], v[220:223]\n" \
"ds_read_b64_tr_b16 a[20:21], v33 offset:192\n" \
"ds_read_b64_tr_b16 a[22:23], v33 offset:4800\n" \
"v_mfma_f32_16x16x32_bf16 v[216:219], a[24:27], a[104:107], v[216:219]\n" \
"v_mfma_f32_16x16x32_bf16 v[220:223], a[24:27], a[120:123], v[220:223]\n" \
"ds_read_b64_tr_b16 a[24:25], v33 offset:9408\n" \
"ds_read_b64_tr_b16 a[26:27], v33 offset:14016\n" \
"v_mfma_f32_16x16x32_bf16 v[216:219], a[28:31], a[108:111], v[216:219]\n" \
"v_mfma_f32_16x16x32_bf16 v[220:223], a[28:31], a[124:127], v[220:223]\n" \
"buffer_load_dword v37, v40, s[56:59], s74 offen sc1\n" \
"v_mfma_f32_16x16x32_bf16 v[224:227], a[192:195], a[220:223], v[224:227]\n" \
"ds_read_b64_tr_b16 a[28:29], v33 offset:18624\n" \
"ds_read_b64_tr_b16 a[30:31], v33 offset:23232\n" \
"v_mfma_f32_16x16x32_bf16 v[228:231], a[192:195], a[236:239], v[228:231]\n" \
"ds_read_b64_tr_b16 a[192:193], v33 offset:27840\n" \
"s_waitcnt lgkmcnt(13)\n" \
"v_mfma_f32_16x16x32_bf16 v[224:227], a[196:199], a[224:227], v[224:227]\n" \
"v_cvt_pk_bf16_f32 v216, v216, v217\n" \
"v_cvt_pk_bf16_f32 v217, v218, v219\n" \
"v_cvt_pk_bf16_f32 v218, v220, v221\n" \
"v_cvt_pk_bf16_f32 v219, v222, v223\n" \
"buffer_store_dwordx4 v[216:219], v20, s[56:59], s96 offen offset:1024 sc1\n" \
"s_add_u32 s96, s96, 0x1800\n" \
"v_mfma_f32_16x16x32_bf16 v[228:231], a[196:199], a[240:243], v[228:231]\n" \
"ds_read_b64_tr_b16 a[194:195], v33 offset:32448\n" \
"ds_read_b64_tr_b16 a[196:197], v33 offset:37056\n" \
"v_mfma_f32_16x16x32_bf16 v[224:227], a[200:203], a[228:231], v[224:227]\n" \
"ds_read_b64_tr_b16 a[198:199], v33 offset:41664\n" \
"v_mfma_f32_16x16x32_bf16 v[228:231], a[200:203], a[244:247], v[228:231]\n" \
"ds_read_b64_tr_b16 a[200:201], v33 offset:46272\n" \
"ds_read_b64_tr_b16 a[202:203], v33 offset:50880\n" \
"v_mfma_f32_16x16x32_bf16 v[224:227], a[204:207], a[232:235], v[224:227]\n" \
"v_mfma_f32_16x16x32_bf16 v[228:231], a[204:207], a[248:251], v[228:231]\n" \
"ds_read_b64_tr_b16 a[204:205], v33 offset:55488\n" \
"ds_read_b64_tr_b16 a[206:207], v33 offset:60096\n" \
"v_mfma_f32_16x16x32_bf16 v[224:227], a[208:211], a[160:163], v[224:227]\n" \
"v_mfma_f32_16x16x32_bf16 v[228:231], a[208:211], a[176:179], v[228:231]\n" \
"ds_read_b64_tr_b16 a[208:209], v10 offset:192\n" \
"ds_read_b64_tr_b16 a[210:211], v10 offset:4800\n" \
"v_mfma_f32_16x16x32_bf16 v[224:227], a[212:215], a[164:167], v[224:227]\n" \
"v_mfma_f32_16x16x32_bf16 v[228:231], a[212:215], a[180:183], v[228:231]\n" \
"ds_read_b64_tr_b16 a[212:213], v34 offset:192\n" \
"ds_read_b64_tr_b16 a[214:215], v34 offset:4800\n" \
"v_mfma_f32_16x16x32_bf16 v[224:227], a[216:219], a[168:171], v[224:227]\n" \
"v_mfma_f32_16x16x32_bf16 v[228:231], a[216:219], a[184:187], v[228:231]\n" \
"ds_read_b64_tr_b16 a[216:217], v34 offset:9408\n" \
"ds_read_b64_tr_b16 a[218:219], v34 offset:14016\n" \
"v_mfma_f32_16x16x32_bf16 v[224:227], a[0:3], a[172:175], v[224:227]\n" \
"v_mfma_f32_16x16x32_bf16 v[228:231], a[0:3], a[188:191], v[228:231]\n" \
"ds_read_b64_tr_b16 a[0:1], v34 offset:18624\n" \
"ds_read_b64_tr_b16 a[2:3], v34 offset:23232\n" \
"v_mfma_f32_16x16x32_bf16 v[224:227], a[4:7], a[96:99], v[224:227]\n" \
"v_mfma_f32_16x16x32_bf16 v[228:231], a[4:7], a[112:115], v[228:231]\n" \
"ds_read_b64_tr_b16 a[4:5], v10 offset:9408\n" \
"s_waitcnt vmcnt(6)\n" \
"s_mov_b64 exec, 1\n" \
"buffer_store_dword v39, off, s[56:59], s93 offset:36 sc1\n" \
"s_mov_b64 exec, -1\n" \
"s_waitcnt lgkmcnt(13)\n" \
"v_mfma_f32_16x16x32_bf16 v[224:227], a[8:11], a[100:103], v[224:227]\n" \
"ds_read_b64_tr_b16 a[6:7], v10 offset:14016\n" \
"v_mfma_f32_16x16x32_bf16 v[228:231], a[8:11], a[116:119], v[228:231]\n" \
"ds_read_b64_tr_b16 a[8:9], v33 offset:224\n" \
"ds_read_b64_tr_b16 a[10:11], v33 offset:4832\n" \
"v_mfma_f32_16x16x32_bf16 v[224:227], a[12:15], a[104:107], v[224:227]\n" \
"v_mfma_f32_16x16x32_bf16 v[228:231], a[12:15], a[120:123], v[228:231]\n" \
"ds_read_b64_tr_b16 a[12:13], v33 offset:9440\n" \
"ds_read_b64_tr_b16 a[14:15], v33 offset:14048\n" \
"v_mfma_f32_16x16x32_bf16 v[224:227], a[16:19], a[108:111], v[224:227]\n" \
"v_mfma_f32_16x16x32_bf16 v[228:231], a[16:19], a[124:127], v[228:231]\n" \
"ds_read_b64_tr_b16 a[16:17], v33 offset:18656\n" \
"ds_read_b64_tr_b16 a[18:19], v33 offset:23264\n" \
"v_mfma_f32_16x16x32_bf16 v[232:235], a[20:23], a[220:223], v[232:235]\n" \
"v_mfma_f32_16x16x32_bf16 v[236:239], a[20:23], a[236:239], v[236:239]\n" \
"ds_read_b64_tr_b16 a[20:21], v33 offset:27872\n" \
"ds_read_b64_tr_b16 a[22:23], v33 offset:32480\n" \
"v_mfma_f32_16x16x32_bf16 v[232:235], a[24:27], a[224:227], v[232:235]\n" \
"v_cvt_pk_bf16_f32 v224, v224, v225\n" \
"v_cvt_pk_bf16_f32 v225, v226, v227\n" \
"v_cvt_pk_bf16_f32 v226, v228, v229\n" \
"v_cvt_pk_bf16_f32 v227, v230, v231\n" \
"buffer_store_dwordx4 v[224:227], v20, s[56:59], s71 offen offset:1024 sc1\n" \
"v_mfma_f32_16x16x32_bf16 v[236:239], a[24:27], a[240:243], v[236:239]\n" \
"ds_read_b64_tr_b16 a[24:25], v33 offset:37088\n" \
"ds_read_b64_tr_b16 a[26:27], v33 offset:41696\n" \
"v_mfma_f32_16x16x32_bf16 v[232:235], a[28:31], a[228:231], v[232:235]\n" \
"v_mfma_f32_16x16x32_bf16 v[236:239], a[28:31], a[244:247], v[236:239]\n" \
"ds_read_b64_tr_b16 a[28:29], v33 offset:46304\n" \
"ds_read_b64_tr_b16 a[30:31], v33 offset:50912\n" \
"v_mfma_f32_16x16x32_bf16 v[232:235], a[192:195], a[232:235], v[232:235]\n" \
"v_mfma_f32_16x16x32_bf16 v[236:239], a[192:195], a[248:251], v[236:239]\n" \
"ds_read_b64_tr_b16 a[192:193], v33 offset:55520\n" \
"ds_read_b64_tr_b16 a[194:195], v33 offset:60128\n" \
"v_mfma_f32_16x16x32_bf16 v[232:235], a[196:199], a[160:163], v[232:235]\n" \
"v_mfma_f32_16x16x32_bf16 v[236:239], a[196:199], a[176:179], v[236:239]\n" \
"ds_read_b64_tr_b16 a[196:197], v10 offset:224\n" \
"s_waitcnt lgkmcnt(13)\n" \
"v_mfma_f32_16x16x32_bf16 v[232:235], a[200:203], a[164:167], v[232:235]\n" \
"ds_read_b64_tr_b16 a[198:199], v10 offset:4832\n" \
"v_mfma_f32_16x16x32_bf16 v[236:239], a[200:203], a[180:183], v[236:239]\n" \
"ds_read_b64_tr_b16 a[200:201], v34 offset:224\n" \
"ds_read_b64_tr_b16 a[202:203], v34 offset:4832\n" \
"v_mfma_f32_16x16x32_bf16 v[232:235], a[204:207], a[168:171], v[232:235]\n" \
"v_mfma_f32_16x16x32_bf16 v[236:239], a[204:207], a[184:187], v[236:239]\n" \
"ds_read_b64_tr_b16 a[204:205], v34 offset:9440\n" \
"ds_read_b64_tr_b16 a[206:207], v34 offset:14048\n" \
"v_mfma_f32_16x16x32_bf16 v[232:235], a[208:211], a[172:175], v[232:235]\n" \
"v_mfma_f32_16x16x32_bf16 v[236:239], a[208:211], a[188:191], v[236:239]\n" \
"ds_read_b64_tr_b16 a[208:209], v34 offset:18656\n" \
"ds_read_b64_tr_b16 a[210:211], v34 offset:23264\n" \
"v_mfma_f32_16x16x32_bf16 v[232:235], a[212:215], a[96:99], v[232:235]\n" \
"v_mfma_f32_16x16x32_bf16 v[236:239], a[212:215], a[112:115], v[236:239]\n" \
"ds_read_b64_tr_b16 a[212:213], v10 offset:9440\n" \
"ds_read_b64_tr_b16 a[214:215], v10 offset:14048\n" \
"s_waitcnt vmcnt(7)\n" \
"s_mov_b64 exec, 1\n" \
"buffer_store_dword v39, off, s[56:59], s94 offset:40 sc1\n" \
"s_mov_b64 exec, -1\n" \
"v_mfma_f32_16x16x32_bf16 v[232:235], a[216:219], a[100:103], v[232:235]\n" \
"v_mfma_f32_16x16x32_bf16 v[236:239], a[216:219], a[116:119], v[236:239]\n" \
"ds_read_b64_tr_b16 a[216:217], v36 offset:0\n" \
"ds_read_b64_tr_b16 a[218:219], v36 offset:1024\n" \
"v_mfma_f32_16x16x32_bf16 v[232:235], a[0:3], a[104:107], v[232:235]\n" \
"v_mfma_f32_16x16x32_bf16 v[236:239], a[0:3], a[120:123], v[236:239]\n" \
"ds_read_b64_tr_b16 a[0:1], v36 offset:2048\n" \
"ds_read_b64_tr_b16 a[2:3], v36 offset:3072\n" \
"v_mfma_f32_16x16x32_bf16 v[232:235], a[4:7], a[108:111], v[232:235]\n" \
"v_mfma_f32_16x16x32_bf16 v[236:239], a[4:7], a[124:127], v[236:239]\n" \
"ds_read_b64_tr_b16 a[4:5], v36 offset:32\n" \
"ds_read_b64_tr_b16 a[6:7], v36 offset:1056\n" \
"v_mfma_f32_16x16x32_bf16 v[240:243], a[8:11], a[220:223], v[240:243]\n" \
"v_mfma_f32_16x16x32_bf16 v[244:247], a[8:11], a[236:239], v[244:247]\n" \
"ds_read_b64_tr_b16 a[8:9], v36 offset:2080\n" \
"s_waitcnt lgkmcnt(13)\n" \
"v_mfma_f32_16x16x32_bf16 v[240:243], a[12:15], a[224:227], v[240:243]\n" \
"v_cvt_pk_bf16_f32 v232, v232, v233\n" \
"v_cvt_pk_bf16_f32 v233, v234, v235\n" \
"v_cvt_pk_bf16_f32 v234, v236, v237\n" \
"v_cvt_pk_bf16_f32 v235, v238, v239\n" \
"buffer_store_dwordx4 v[232:235], v20, s[56:59], s72 offen offset:1024 sc1\n" \
"s_waitcnt vmcnt(5)\n" \
"v_cmp_eq_u32_e64 s[98:99], s62, v37\n" \
"s_mov_b64 exec, 1\n" \
"buffer_store_dword v39, off, s[56:59], s95 offset:44 sc1\n" \
"s_mov_b64 exec, -1\n" \
"v_mfma_f32_16x16x32_bf16 v[244:247], a[12:15], a[240:243], v[244:247]\n" \
"ds_read_b64_tr_b16 a[10:11], v36 offset:3104\n" \
"s_or_b32 s75, 0x7f, s100\n" \
"s_andn2_b32 s75, s75, s98\n" \
"s_cbranch_scc1 L_sx0_%=\n" \
"L_rx0_%=:\n" \
"buffer_load_dword v37, v40, s[56:59], s74 offen sc1\n" \
"s_add_u32 s97, s63, 0x0\n" \
"s_add_u32 s64, s63, 0x800\n" \
"s_add_u32 s65, s63, 0x1000\n" \
"s_add_u32 s66, s63, 0x1800\n" \
"s_add_u32 s67, s63, 0x2000\n" \
"s_add_u32 s68, s63, 0x2800\n" \
"s_add_u32 s69, s63, 0x3000\n" \
"s_add_u32 s71, s71, 0x1000\n" \
"s_add_u32 s72, s72, 0x800\n" \
"v_mfma_f32_16x16x32_bf16 v[240:243], a[16:19], a[228:231], v[240:243]\n" \
"v_mfma_f32_16x16x32_bf16 v[244:247], a[16:19], a[244:247], v[244:247]\n" \
"buffer_load_dwordx4 v[116:119], v20, s[56:59], s97 offen offset:0 sc1\n" \
"v_mfma_f32_16x16x32_bf16 v[240:243], a[20:23], a[232:235], v[240:243]\n" \
"v_mfma_f32_16x16x32_bf16 v[244:247], a[20:23], a[248:251], v[244:247]\n" \
"v_mfma_f32_16x16x32_bf16 v[168:171], a[24:27], a[128:131], v[168:171]\n" \
"v_mfma_f32_16x16x32_bf16 v[172:175], a[24:27], a[144:147], v[172:175]\n" \
"buffer_load_dwordx4 v[124:127], v20, s[56:59], s64 offen offset:0 sc1\n" \
"v_mfma_f32_16x16x32_bf16 v[240:243], a[24:27], a[160:163], v[240:243]\n" \
"v_readlane_b32 s64, v11, 54\n" \
"v_mfma_f32_16x16x32_bf16 v[244:247], a[24:27], a[176:179], v[244:247]\n" \
"v_mfma_f32_16x16x32_bf16 v[168:171], a[28:31], a[132:135], v[168:171]\n" \
"v_mfma_f32_16x16x32_bf16 v[172:175], a[28:31], a[148:151], v[172:175]\n" \
"buffer_load_dwordx4 v[132:135], v20, s[56:59], s65 offen offset:0 sc1\n" \
"v_mfma_f32_16x16x32_bf16 v[240:243], a[28:31], a[164:167], v[240:243]\n" \
"v_readlane_b32 s65, v11, 45\n" \
"v_mfma_f32_16x16x32_bf16 v[244:247], a[28:31], a[180:183], v[244:247]\n" \
"v_mfma_f32_16x16x32_bf16 v[168:171], a[192:195], a[136:139], v[168:171]\n" \
"v_mfma_f32_16x16x32_bf16 v[172:175], a[192:195], a[152:155], v[172:175]\n" \
"buffer_load_dwordx4 v[140:143], v20, s[56:59], s66 offen offset:0 sc1\n" \
"v_mfma_f32_16x16x32_bf16 v[240:243], a[192:195], a[168:171], v[240:243]\n" \
"v_readlane_b32 s66, v11, 43\n" \
"v_mfma_f32_16x16x32_bf16 v[244:247], a[192:195], a[184:187], v[244:247]\n" \
"v_mfma_f32_16x16x32_bf16 v[168:171], a[196:199], a[140:143], v[168:171]\n" \
"v_mfma_f32_16x16x32_bf16 v[172:175], a[196:199], a[156:159], v[172:175]\n" \
"buffer_load_dwordx4 v[148:151], v20, s[56:59], s67 offen offset:0 sc1\n" \
"v_mfma_f32_16x16x32_bf16 v[240:243], a[196:199], a[172:175], v[240:243]\n" \
"v_readlane_b32 s67, v11, 44\n" \
"v_mfma_f32_16x16x32_bf16 v[244:247], a[196:199], a[188:191], v[244:247]\n" \
"v_mfma_f32_16x16x32_bf16 v[168:171], a[200:203], a[32:35], v[168:171]\n" \
"v_mfma_f32_16x16x32_bf16 v[172:175], a[200:203], a[48:51], v[172:175]\n" \
"buffer_load_dwordx4 v[156:159], v20, s[56:59], s68 offen offset:0 sc1\n" \
"v_mfma_f32_16x16x32_bf16 v[240:243], a[200:203], a[96:99], v[240:243]\n" \
"v_mfma_f32_16x16x32_bf16 v[244:247], a[200:203], a[112:115], v[244:247]\n" \
"s_waitcnt lgkmcnt(0)\n" \
"v_mfma_f32_16x16x32_bf16 v[168:171], a[204:207], a[36:39], v[168:171]\n" \
"v_mfma_f32_16x16x32_bf16 v[172:175], a[204:207], a[52:55], v[172:175]\n" \
"buffer_load_dwordx4 v[164:167], v20, s[56:59], s69 offen offset:0 sc1\n" \
"v_mfma_f32_16x16x32_bf16 v[240:243], a[204:207], a[100:103], v[240:243]\n" \
"v_mfma_f32_16x16x32_bf16 v[244:247], a[204:207], a[116:119], v[244:247]\n" \
"v_mfma_f32_16x16x32_bf16 v[168:171], a[208:211], a[40:43], v[168:171]\n" \
"v_mfma_f32_16x16x32_bf16 v[172:175], a[208:211], a[56:59], v[172:175]\n" \
"buffer_load_dwordx4 v[112:115], v20, s[56:59], s70 offen offset:0 sc1\n" \
"v_mfma_f32_16x16x32_bf16 v[240:243], a[208:211], a[104:107], v[240:243]\n" \
"v_mfma_f32_16x16x32_bf16 v[244:247], a[208:211], a[120:123], v[244:247]\n" \
"v_mfma_f32_16x16x32_bf16 v[168:171], a[212:215], a[44:47], v[168:171]\n" \
"v_mfma_f32_16x16x32_bf16 v[172:175], a[212:215], a[60:63], v[172:175]\n" \
"buffer_load_dwordx4 v[184:187], v20, s[56:59], s70 offen offset:2048 sc1\n" \
"s_add_u32 s70, s70, 0x1000\n" \
"v_mfma_f32_16x16x32_bf16 v[240:243], a[212:215], a[108:111], v[240:243]\n" \
"v_mfma_f32_16x16x32_bf16 v[244:247], a[212:215], a[124:127], v[244:247]\n" \
"s_waitcnt vmcnt(15)\n" \
"s_mov_b64 exec, 1\n" \
"buffer_store_dword v39, off, s[56:59], s96 offset:48 sc1\n" \
"s_mov_b64 exec, -1\n" \
"v_mfma_f32_16x16x32_bf16 v[168:171], a[216:219], v[64:67], v[168:171]\n" \
"v_mfma_f32_16x16x32_bf16 v[172:175], a[216:219], v[72:75], v[172:175]\n" \
"v_mfma_f32_16x16x32_bf16 v[240:243], a[216:219], v[80:83], v[240:243]\n" \
"v_mfma_f32_16x16x32_bf16 v[244:247], a[216:219], v[88:91], v[244:247]\n" \
"buffer_load_dwordx4 v[120:123], v20, s[56:59], s70 offen offset:0 sc1\n" \
"v_mfma_f32_16x16x32_bf16 v[168:171], a[0:3], v[68:71], v[168:171]\n" \
"v_mfma_f32_16x16x32_bf16 v[172:175], a[0:3], v[76:79], v[172:175]\n" \
"v_mfma_f32_16x16x32_bf16 v[240:243], a[0:3], v[84:87], v[240:243]\n" \
"v_mfma_f32_16x16x32_bf16 v[244:247], a[0:3], v[92:95], v[244:247]\n" \
"buffer_load_dwordx4 v[192:195], v20, s[56:59], s70 offen offset:2048 sc1\n" \
"s_add_u32 s70, s70, 0x1000\n" \
"v_mfma_f32_16x16x32_bf16 v[56:59], a[4:7], v[96:99], 0\n" \
"v_mfma_f32_16x16x32_bf16 v[60:63], a[4:7], v[104:107], 0\n" \
"v_mfma_f32_16x16x32_bf16 v[56:59], a[8:11], v[100:103], v[56:59]\n" \
"v_mfma_f32_16x16x32_bf16 v[60:63], a[8:11], v[108:111], v[60:63]\n" \
"buffer_load_dwordx4 v[128:131], v20, s[56:59], s70 offen offset:0 sc1\n" \
"buffer_load_dwordx4 v[200:203], v20, s[56:59], s70 offen offset:2048 sc1\n" \
".fill (L_ab + 45056 - .) / 4, 4, 0xBF800000\n" \
"L_ap11:\n" \
"s_cbranch_execz L_ap0\n" \
"s_add_u32 s70, s70, 0x1000\n" \
"buffer_load_dwordx4 v[176:179], v20, s[56:59], s70 offen offset:0 sc1\n" \
"buffer_load_dwordx4 v[248:251], v20, s[56:59], s70 offen offset:2048 sc1\n" \
"s_waitcnt vmcnt(15)\n" \
"v_mfma_f32_16x16x32_bf16 v[168:171], v[50:53], v[116:119], v[168:171]\n" \
"s_mov_b64 exec, 1\n" \
"buffer_store_dword v39, off, s[56:59], s71 offset:52 sc1\n" \
"s_mov_b64 exec, -1\n" \
"s_mov_b64 exec, 1\n" \
"buffer_store_dword v39, off, s[56:59], s72 offset:56 sc1\n" \
"s_mov_b64 exec, -1\n" \
"v_cmp_eq_u32_e64 s[98:99], s62, v37\n" \
"v_mfma_f32_16x16x32_bf16 v[172:175], v[48:51], v[116:119], v[172:175]\n" \
"s_andn2_b32 s75, 0x100, s98\n" \
"s_cbranch_scc1 L_sx1_%=\n" \
"L_rx1_%=:\n" \
"s_add_u32 s73, s63, 0x0\n" \
"s_waitcnt vmcnt(16)\n" \
"v_mfma_f32_16x16x32_bf16 v[168:171], v[50:53], v[124:127], v[168:171]\n" \
"v_mfma_f32_16x16x32_bf16 v[172:175], v[48:51], v[124:127], v[172:175]\n" \
"buffer_load_dwordx4 v[188:191], v20, s[56:59], s73 offen offset:1024 sc1\n" \
"s_andn2_b32 s75, 0x200, s98\n" \
"s_cbranch_scc1 L_sx2_%=\n" \
"L_rx2_%=:\n" \
"s_add_u32 s92, s63, 0x800\n" \
"s_waitcnt vmcnt(16)\n" \
"v_mfma_f32_16x16x32_bf16 v[168:171], v[50:53], v[132:135], v[168:171]\n" \
"v_mfma_f32_16x16x32_bf16 v[172:175], v[48:51], v[132:135], v[172:175]\n" \
"buffer_load_dwordx4 v[196:199], v20, s[56:59], s92 offen offset:1024 sc1\n" \
"s_andn2_b32 s75, 0x400, s98\n" \
"s_cbranch_scc1 L_sx3_%=\n" \
"L_rx3_%=:\n" \
"s_add_u32 s93, s63, 0x1000\n" \
"s_waitcnt vmcnt(16)\n" \
"v_mfma_f32_16x16x32_bf16 v[168:171], v[50:53], v[140:143], v[168:171]\n" \
"v_mfma_f32_16x16x32_bf16 v[172:175], v[48:51], v[140:143], v[172:175]\n" \
"buffer_load_dwordx4 v[204:207], v20, s[56:59], s93 offen offset:1024 sc1\n" \
"s_andn2_b32 s75, 0x800, s98\n" \
"s_cbranch_scc1 L_sx4_%=\n" \
"L_rx4_%=:\n" \
"s_add_u32 s94, s63, 0x1800\n" \
"s_waitcnt vmcnt(16)\n" \
"v_mfma_f32_16x16x32_bf16 v[168:171], v[50:53], v[148:151], v[168:171]\n" \
"v_mfma_f32_16x16x32_bf16 v[172:175], v[48:51], v[148:151], v[172:175]\n" \
"buffer_load_dwordx4 v[212:215], v20, s[56:59], s94 offen offset:1024 sc1\n" \
"s_andn2_b32 s75, 0x1000, s98\n" \
"s_cbranch_scc1 L_sx5_%=\n" \
"L_rx5_%=:\n" \
"s_add_u32 s95, s63, 0x2000\n" \
"s_waitcnt vmcnt(16)\n" \
"v_mfma_f32_16x16x32_bf16 v[168:171], v[50:53], v[156:159], v[168:171]\n" \
"v_mfma_f32_16x16x32_bf16 v[172:175], v[48:51], v[156:159], v[172:175]\n" \
"buffer_load_dwordx4 v[220:223], v20, s[56:59], s95 offen offset:1024 sc1\n" \
"s_andn2_b32 s75, 0x2000, s98\n" \
"s_cbranch_scc1 L_sx6_%=\n" \
"L_rx6_%=:\n" \
"s_add_u32 s96, s63, 0x2800\n" \
"s_waitcnt vmcnt(16)\n" \
"v_mfma_f32_16x16x32_bf16 v[168:171], v[50:53], v[164:167], v[168:171]\n" \
"v_mfma_f32_16x16x32_bf16 v[172:175], v[48:51], v[164:167], v[172:175]\n" \
"buffer_load_dwordx4 v[228:231], v20, s[56:59], s96 offen offset:1024 sc1\n" \
"s_andn2_b32 s75, 0x4000, s98\n" \
"s_cbranch_scc1 L_sx7_%=\n" \
"L_rx7_%=:\n" \
"s_add_u32 s97, s63, 0x3000\n" \
"s_waitcnt vmcnt(16)\n" \
"v_mfma_f32_16x16x32_bf16 v[56:59], v[50:53], v[112:115], v[56:59]\n" \
"v_mfma_f32_16x16x32_bf16 v[60:63], v[48:51], v[112:115], v[60:63]\n" \
"buffer_load_dwordx4 v[236:239], v20, s[56:59], s97 offen offset:1024 sc1\n" \
"v_cvt_pk_bf16_f32 v168, v168, v169\n" \
"v_cvt_pk_bf16_f32 v172, v172, v173\n" \
"v_cvt_pk_bf16_f32 v169, v170, v171\n" \
"v_cvt_pk_bf16_f32 v173, v174, v175\n" \
"buffer_store_dwordx2 v[168:169], v42, s[88:91], s66 offen sc1\n" \
"buffer_store_dwordx2 v[172:173], v43, s[88:91], s66 offen sc1\n" \
"s_waitcnt vmcnt(18)\n" \
"v_mfma_f32_16x16x32_bf16 v[56:59], v[50:53], v[184:187], v[56:59]\n" \
"v_mfma_f32_16x16x32_bf16 v[60:63], v[48:51], v[184:187], v[60:63]\n" \
"s_waitcnt vmcnt(16)\n" \
"v_mfma_f32_16x16x32_bf16 v[56:59], v[50:53], v[120:123], v[56:59]\n" \
"v_mfma_f32_16x16x32_bf16 v[60:63], v[48:51], v[120:123], v[60:63]\n" \
"s_waitcnt vmcnt(15)\n" \
"v_mfma_f32_16x16x32_bf16 v[56:59], v[50:53], v[192:195], v[56:59]\n" \
"v_mfma_f32_16x16x32_bf16 v[60:63], v[48:51], v[192:195], v[60:63]\n" \
"s_waitcnt vmcnt(14)\n" \
"v_mfma_f32_16x16x32_bf16 v[56:59], v[50:53], v[128:131], v[56:59]\n" \
"v_mfma_f32_16x16x32_bf16 v[60:63], v[48:51], v[128:131], v[60:63]\n" \
"s_waitcnt vmcnt(13)\n" \
"v_mfma_f32_16x16x32_bf16 v[56:59], v[50:53], v[200:203], v[56:59]\n" \
"v_mfma_f32_16x16x32_bf16 v[60:63], v[48:51], v[200:203], v[60:63]\n" \
"s_waitcnt vmcnt(12)\n" \
"v_mfma_f32_16x16x32_bf16 v[56:59], v[50:53], v[176:179], v[56:59]\n" \
"v_mfma_f32_16x16x32_bf16 v[60:63], v[48:51], v[176:179], v[60:63]\n" \
"s_waitcnt vmcnt(11)\n" \
"v_mfma_f32_16x16x32_bf16 v[56:59], v[50:53], v[248:251], v[56:59]\n" \
"v_mfma_f32_16x16x32_bf16 v[60:63], v[48:51], v[248:251], v[60:63]\n" \
"s_mov_b64 exec, 1\n" \
"buffer_store_dword v46, off, s[56:59], s64 offset:0 sc1\n" \
"s_mov_b64 exec, -1\n" \
"s_waitcnt vmcnt(9)\n" \
"v_mfma_f32_16x16x32_bf16 v[240:243], v[50:53], v[188:191], v[240:243]\n" \
"v_mfma_f32_16x16x32_bf16 v[244:247], v[48:51], v[188:191], v[244:247]\n" \
"s_waitcnt vmcnt(8)\n" \
"v_mfma_f32_16x16x32_bf16 v[240:243], v[50:53], v[196:199], v[240:243]\n" \
"v_cvt_pk_bf16_f32 v56, v56, v57\n" \
"v_cvt_pk_bf16_f32 v60, v60, v61\n" \
"v_mfma_f32_16x16x32_bf16 v[244:247], v[48:51], v[196:199], v[244:247]\n" \
"v_cvt_pk_bf16_f32 v57, v58, v59\n" \
"v_cvt_pk_bf16_f32 v61, v62, v63\n" \
"buffer_store_dwordx2 v[56:57], v42, s[88:91], s65 offen sc1\n" \
"buffer_store_dwordx2 v[60:61], v43, s[88:91], s65 offen sc1\n" \
"s_waitcnt vmcnt(9)\n" \
"v_mfma_f32_16x16x32_bf16 v[240:243], v[50:53], v[204:207], v[240:243]\n" \
"v_mfma_f32_16x16x32_bf16 v[244:247], v[48:51], v[204:207], v[244:247]\n" \
"s_waitcnt vmcnt(8)\n" \
"v_mfma_f32_16x16x32_bf16 v[240:243], v[50:53], v[212:215], v[240:243]\n" \
"v_mfma_f32_16x16x32_bf16 v[244:247], v[48:51], v[212:215], v[244:247]\n" \
"s_waitcnt vmcnt(7)\n" \
"v_mfma_f32_16x16x32_bf16 v[240:243], v[50:53], v[220:223], v[240:243]\n" \
"v_mfma_f32_16x16x32_bf16 v[244:247], v[48:51], v[220:223], v[244:247]\n" \
"s_waitcnt vmcnt(6)\n" \
"v_mfma_f32_16x16x32_bf16 v[240:243], v[50:53], v[228:231], v[240:243]\n" \
"v_mfma_f32_16x16x32_bf16 v[244:247], v[48:51], v[228:231], v[244:247]\n" \
"s_waitcnt vmcnt(5)\n" \
"v_mfma_f32_16x16x32_bf16 v[240:243], v[50:53], v[236:239], v[240:243]\n" \
"v_mfma_f32_16x16x32_bf16 v[244:247], v[48:51], v[236:239], v[244:247]\n" \
"s_nop 6\n" \
"v_cvt_pk_bf16_f32 v240, v240, v241\n" \
"v_cvt_pk_bf16_f32 v244, v244, v245\n" \
"v_cvt_pk_bf16_f32 v241, v242, v243\n" \
"v_cvt_pk_bf16_f32 v245, v246, v247\n" \
"buffer_store_dwordx2 v[240:241], v42, s[88:91], s67 offen sc1\n" \
"buffer_store_dwordx2 v[244:245], v43, s[88:91], s67 offen sc1\n"
#define ATT_VCLOB "v21","v22","v23","v24","v25","v26","v27","v28","v29","v32","v33","v34","v35","v36","v37","v38","v39","v40","v41","v42","v43","v44","v45","v46","v47","v48","v49","v50","v51","v52","v53","v54","v55","v56","v57","v58","v59","v60","v61","v62","v63","v64","v65","v66","v67","v68","v69","v70","v71","v72","v73","v74","v75","v76","v77","v78","v79","v80","v81","v82","v83","v84","v85","v86","v87","v88","v89","v90","v91","v92","v93","v94","v95","v96","v97","v98","v99","v100","v101","v102","v103","v104","v105","v106","v107","v108","v109","v110","v111","v112","v113","v114","v115","v116","v117","v118","v119","v120","v121","v122","v123","v124","v125","v126","v127","v128","v129","v130","v131","v132","v133","v134","v135","v136","v137","v138","v139","v140","v141","v142","v143","v144","v145","v146","v147","v148","v149","v150","v151","v152","v153","v154","v155","v156","v157","v158","v159","v160","v161","v162","v163","v164","v165","v166","v167","v168","v169","v170","v171","v172","v173","v174","v175","v176","v177","v178","v179","v180","v181","v182","v183","v184","v185","v186","v187","v188","v189","v190","v191","v192","v193","v194","v195","v196","v197","v198","v199","v200","v201","v202","v203","v204","v205","v206","v207","v208","v209","v210","v211","v212","v213","v214","v215","v216","v217","v218","v219","v220","v221","v222","v223","v224","v225","v226","v227","v228","v229","v230","v231","v232","v233","v234","v235","v236","v237","v238","v239","v240","v241","v242","v243","v244","v245","v246","v247","v248","v249","v250","v251","v252","v253","v254","v255"
#define ATT_SCLOB "s63","s64","s65","s66","s67","s68","s69","s70","s71","s72","s73","s74","s75","s76","s77","s78","s79","s80","s81","s82","s83","s84","s85","s86","s87","s88","s89","s90","s91","s92","s93","s95","s96","s97","s98","s99","s100","s101"
#define ATT_WTCH 0
#define ATT_PEEL 1
#define ATT_PEEL_FOW 0
enum { T_fo, T_fj, T_fa, T_fas, T_fb, T_fbs, T_f8, T_mk8, T_wt, T_wol, T_woh, T_wbl, T_wbh, T_outl, T_outh, T_r1l, T_r1h, T_r1n, T_r1s, T_m0, T_m1, T_m1b, T_mw, T_m3, T_m4, T_ml, T_w0, T_w1, T_w2, T_w3, T_A1, T_A2, T_A3, T_B1, T_B2, T_B3, T_Ao, T_Bo, T_Al, T_Bl, T_l8, T_xr, T_x8r, T_oA, T_oB, T_o8, T_x0, T_x1, T_x2, T_x3, T_x4, T_x5, T_x6, T_x8, T_ep };

__device__ __forceinline__ void out_consts(u32x4 (&vp)[3], const u16* wo, const u16* wob, const u16* out, const unsigned b, const unsigned w, const unsigned lane) {
    const unsigned sp = b & 7u, nt = (b >> 3) & 3u, j = b >> 5, head = 4u * sp + nt, OOR = 0x40000000u;
    unsigned vw[9];
#pragma unroll
    for (unsigned i = 0; i < 9u; ++i) {
        const unsigned q = 64u * i + lane, row = __umul24(q, 3641u) >> 16, c = q - __umul24(row, 18u);
        vw[i] = __umul24(row, DM * 2) + (c < 16u ? (c * 16u + sp * 32u + 32u) & 255u : c * 16u) - (i & 3u) * 1024u;
    }
    const unsigned row = 16u * w + (lane >> 2), r9 = __umul24(16u * (row >> 5) + (row & 12u) + 2u * ((row >> 4) & 1u) + ((row >> 1) & 1u), 18u) + 16u + (row & 1u);
    unsigned vwl = __umul24(r9, DM * 2) + ((lane & 2u) ? 256u : sp * 32u) + (lane & 1u) * 16u, vrd = __umul24(lane >> 2, 288u) + (lane & 3u) * 8u;
    unsigned tab = 0;
#define TB(n, v) asm("v_writelane_b32 %0, %1, %2" : "+v"(tab) : "s"((unsigned)(v)), "n"((int)T_##n))
    const unsigned FJ = (unsigned)WS_F + (F_J + 640u * j) * 4u, mt = 2u * (w & 2u) + (w & 1u), w2 = w < 2u ? w : 2u, hw = head * 147456u;
    const unsigned FR = (unsigned)WS_F + F_R * 4u + (8u * j + sp) * 1024u;
    TB(fo, FJ + (ATT_PEEL_FOW ? 16u * head + 4u * w : 4u * head)); TB(fj, FJ); TB(fa, FR + (w | 2u) * 256u); TB(fas, FR + w * 256u + nt * 4u);
    TB(fb, FR + 128u + w * 256u); TB(fbs, FR + 128u + w * 256u + nt * 4u);
    TB(ep, ssel(b + w, OOR, (unsigned)WS_F + F_EPOCH * 4u));
    const u16* pwo = wo + nt * 144u; const u16* pwb = wob + nt * 144u;
    TB(wol, PLO(pwo)); TB(woh, PHI(pwo) & 0xffffu); TB(wbl, PLO(pwb)); TB(wbh, PHI(pwb) & 0xffffu); TB(outl, PLO(out)); TB(outh, PHI(out) & 0xffffu); TB(r1l, sslt<3>(w, PLO(pwo), PLO(pwb))); TB(r1h, sslt<3>(w, PHI(pwo), PHI(pwb)) & 0xffffu);
    TB(r1n, sslt<3>(w, 72u * sp + 71u, 4095u) * 1152u + 288u); TB(r1s, sslt<3>(w, (72u * sp + 32u * w) * 1152u, hw + 110592u));
    TB(m0, 131072u + 9216u * w2); TB(w0, hw + 36864u * w2); TB(m1, ATT_PEEL ? sslt<3>(w, 41984u + 9216u * w, 32768u) : 24576u + 9216u * w); TB(m1b, ssel(w ^ 2u, sslt<3>(w, 41984u + 9216u * w, 32768u), 114688u - 4096u)); TB(mw, 65536u + 9216u * w); TB(m3, sslt<3>(w, 102400u + 9216u * w, 0u)); TB(m4, sslt<3>(w, 131072u + 9216u * w, 9216u)); TB(ml, (ATT_PEEL ? 18432u : 61440u) + 1024u * w);
    const unsigned tA = (unsigned)WS_AF + (8u * j + mt) * (KS_TOT * 1024u), tB = (unsigned)WS_AF + (120u - 8u * j + mt) * (KS_TOT * 1024u);
#define HK(k) (4u * sp + ((nt + k) & 3u))
    TB(w1, HK(1u) * 147456u + w * 36864u); TB(w2, HK(2u) * 147456u + w * 36864u); TB(w3, HK(3u) * 147456u + w * 36864u);
    TB(A1, tA + 18432u + HK(1u) * 4096u); TB(A2, tA + 18432u + HK(2u) * 4096u); TB(A3, tA + 18432u + HK(3u) * 4096u);
    TB(B1, tB + 18432u + HK(1u) * 4096u); TB(B2, tB + 18432u + HK(2u) * 4096u); TB(B3, tB + 18432u + HK(3u) * 4096u);
    TB(Ao, tA + sp * 2048u); TB(Bo, tB + sp * 2048u); TB(Al, tA + 16384u); TB(Bl, tB + 16384u);
    const unsigned nred = w ^ (sp >> 1), blk = sp & 1u;
    const unsigned xg = (unsigned)WS_X + (4u * j + nt) * 589824u + w * 16384u, oA = (128u * j + 16u * mt) * 1152u + nt * 288u, oB = (1920u - 128u * j + 16u * mt) * 1152u + nt * 288u;
    TB(l8, ssel(nred, OOR, ssel(blk, tB, tA) + 16384u)); TB(xr, xg + sp * 65536u); TB(x8r, ssel(nred, OOR, xg + 524288u + (sp & 1u) * 1024u));
    TB(oA, oA + sp * 32u); TB(oB, oB + sp * 32u); TB(o8, ssel(nred, OOR, ssel(blk, oB, oA) + 256u)); TB(x8, xg + 524288u + sp * 2048u);
    TB(f8, xg + w * 131072u + 14400u + sp * 4u); TB(mk8, ssel(nred, 0u, 0xff0000u)); TB(wt, hw + j * 18432u + w * 4608u);
#define XI(i) TB(x##i, xg + ((sp + 1u + i) & 7u) * 65536u + i * 2048u)
    XI(0); XI(1); XI(2); XI(3); XI(4); XI(5); XI(6);
#undef XI
#undef HK
#undef TB
    vp[0] = (u32x4){vw[0], vw[1], vw[2], vw[3]}; vp[1] = (u32x4){vw[4], vw[5], vw[6], vw[7]}; vp[2] = (u32x4){vw[8], vwl, vrd, tab};
}
__global__ __launch_bounds__(256, 1) __attribute__((aligned(4096))) void fused_kernel(const u16* __restrict__ x, const u16* __restrict__ ckv, const u16* __restrict__ wq, const u16* __restrict__ wuk,
                                                       const u16* __restrict__ wuv, const u16* __restrict__ wqkv, const u16* __restrict__ gw,
                                                       const u16* __restrict__ wo, const u16* __restrict__ wob, char* __restrict__ ws_, u16* __restrict__ out) {
    using namespace att;
    extern __shared__ __attribute__((aligned(16))) char lds[];
    u32x4 sa[5]; u32x2 s24, s28; char* ws; unsigned tdum; kaptr ka = (kaptr)__builtin_amdgcn_kernarg_segment_ptr();
    asm volatile("s_load_dwordx8 s[8:15], %[ka], 0x0\n\ts_load_dwordx8 s[16:23], %[ka], 0x20\n\ts_getpc_b64 s[4:5]\n\tv_lshlrev_b32_e32 v191, 7, %[t]\n\ts_bfe_u32 s6, %[b], 0x20003\n\t"
                 "v_lshl_add_u32 v191, s6, 15, v191\n\tv_min_u32_e32 v191, %[e], v191\n\tglobal_load_dword v191, v191, s[4:5]\n\ts_load_dwordx4 s[24:27], %[ka], 0x40\n\ts_load_dwordx2 s[28:29], %[ka], 0x50"
                 : "={s[8:11]}"(sa[0]), "={s[12:15]}"(sa[1]), "={s[16:19]}"(sa[2]), "={s[20:23]}"(sa[3]), "={s[24:25]}"(s24), "={s[26:27]}"(ws), "={s[28:29]}"(s28), "={v191}"(tdum), [ka] "+{s[0:1]}"(ka)
                 : [t] "v"(threadIdx.x), [b] "s"(blockIdx.x), [e] "i"(CT_END) : "s4", "s5", "s6", "scc");
    SBAR();
    const unsigned wid = rfl(threadIdx.x >> 6);
    unsigned tag, vl;
    const int b = blockIdx.x;
    u32x4 vp[5], srdA; u32x2 sd;
    proj_phase(ka, ws, wid, vp, vl, sa, s24, s28, tdum, srdA, sd, tag
    );
    unsigned* const flags = (unsigned*)(ws + WS_F);
    RTS(2);
#define AST(L) do {} while (0)
#define ATT_TAIL "s_endpgm\n"
    unsigned abad;
    asm volatile(ATT_ASM ATT_TAIL "L_end_%=:\n s_nop 1"
                 : "={s94}"(abad)
                 : "{v[0:3]}"(vp[0]), "{v[4:7]}"(vp[1]), "{v[8:11]}"(vp[2]), "{v[12:15]}"(vp[3]), "{v[16:19]}"(vp[4]), "{v20}"(vl), "{s[56:59]}"(srdA), "{s[60:61]}"(sd), "{s62}"(tag),
                   "{s[8:11]}"(sa[0]), "{s[12:15]}"(sa[1]), "{s[16:19]}"(sa[2]), "{s[20:23]}"(sa[3]), "{s24}"(sa[4][0]), "{s3}"(wid), "{s[28:29]}"(s28)
                 : "memory", "m0", "vcc", "scc", ATT_VCLOB, ATT_SCLOB, ACLOB_LO, ACLOB_HI);
    {
        unsigned b2 = b, w2 = wid;
        asm volatile("s_waitcnt vmcnt(0)" : "+s"(ka), "+s"(b2), "+s"(w2), "+s"(ws) :: "memory");
        const unsigned j2 = b2 >> 5, p2 = w2 & 1u, h2 = (b2 & 7u) * 4u + ((b2 >> 3) & 3u);
#pragma unroll 1
        for (unsigned i = w2 & 2u; i < ((w2 & 2u) ? (abad ? 8u : 4u) : 2u); ++i)
            att_exact(ka[0], ka[2], ws, h2, (i < 4u ? 1920u - 128u * j2 : 128u * j2 - 128u) + 32u * i + 16u * p2, *reinterpret_cast<const float*>(lds + LDS_G + (32u * i + 16u * p2 + (lane_id() & 15u)) * 4u));
        asm volatile("s_waitcnt vmcnt(0)" ::: "memory");
        unsigned* fa = (unsigned*)(ws + WS_F) + F_R + (8u * j2 + (b2 & 7u)) * 256u + (2u + p2) * 64u + ((b2 >> 3) & 3u);
        if (w2 & 2u) st_flag(fa, tag);
        wait_flags(fa, ld_flag(fa), tag);
        const unsigned l2 = lane_id(); u32x4 c[3]; out_consts(c, ka[7], ka[8], ka[10], b2, w2, l2);
        asm volatile("s_waitcnt vmcnt(0) lgkmcnt(0)\n\ts_nop 7\n\ts_branch L_att_stub" :: "{v[0:3]}"(c[0]), "{v[4:7]}"(c[1]), "{v[8:11]}"(c[2]), "{v20}"(l2 * 16u), "{s[56:59]}"(SRDA), "{s60}"(SD(8u * j2, h2, w2)),
                     "{s61}"(SD(120u - 8u * j2, h2, w2)), "{s62}"(tag) : "memory");
    }
    RTS(5);
}

extern "C" void kernel_launch(void* const* d_in, const int* in_sizes, int n_in, void* d_out, int out_size, void* d_ws, size_t ws_size,
                              hipStream_t stream) {
    (void)hipFuncSetAttribute((const void*)fused_kernel, hipFuncAttributeMaxDynamicSharedMemorySize, att::LDS_BYTES);
    fused_kernel<<<dim3(256), 256, att::LDS_BYTES, stream>>>((const u16*)d_in[0], (const u16*)d_in[4], (const u16*)d_in[5], (const u16*)d_in[6], (const u16*)d_in[7], (const u16*)d_in[1],
                                                             (const u16*)d_in[9], (const u16*)d_in[2], (const u16*)d_in[8], (char*)d_ws, (u16*)d_out);
}
```

```cpp
#include <hip/hip_runtime.h>
#include <hip/hip_bf16.h>
#include <stdint.h>
#include <utility>

typedef short bf16x8 __attribute__((ext_vector_type(8)));
typedef short s16x4 __attribute__((ext_vector_type(4)));
typedef float f32x16 __attribute__((ext_vector_type(16)));
typedef float f32x4 __attribute__((ext_vector_type(4)));
typedef unsigned u32x4 __attribute__((ext_vector_type(4)));
typedef unsigned u32x2 __attribute__((ext_vector_type(2)));
typedef unsigned short u16;

constexpr int S = 2048, DM = 576, NH = 32, HD = 128, DC = 512, NQKV = 1728, DHA = 18;
constexpr int NBIG = NH * HD;
constexpr int KS_TOT = 146;

#define SBAR() __builtin_amdgcn_sched_barrier(0)
constexpr size_t WS_Q = 0, WS_K = WS_Q + (size_t)S * NBIG * 2, WS_V = WS_K + (size_t)S * NBIG * 2, WS_AF = WS_V + (size_t)S * NBIG * 2, WS_X = WS_AF + (size_t)128 * KS_TOT * 1024,
                 WS_P = WS_X + (size_t)64 * 16 * 9216, WS_F = WS_P + (size_t)128 * (4 * 32 * 256 + 1024) * 4;
constexpr size_t WS_L = (size_t)96 << 20;
static_assert(WS_F == 96206848, "workspace map is baked into the generated asm");
constexpr int F_EPOCH = 0, F_J = 1024, F_ST = 8192, F_KV = 40960, F_R = 65536;
constexpr unsigned TAG_STEP = 0x9E3779B1u;
__device__ __forceinline__ unsigned ld_flag(const unsigned* p) { return __hip_atomic_load(p, __ATOMIC_RELAXED, __HIP_MEMORY_SCOPE_AGENT); }
__device__ __forceinline__ void st_flag(unsigned* p, unsigned v) { __hip_atomic_store(p, v, __ATOMIC_RELAXED, __HIP_MEMORY_SCOPE_AGENT); }
__device__ __forceinline__ unsigned ld_flag_async(const unsigned* p) { unsigned v; asm volatile("global_load_dword %0, %1, off sc1" : "=v"(v) : "v"(p) : "memory"); return v; }
#define WAITV(v, N) asm volatile("s_waitcnt vmcnt(" #N ")" : "+v"(v) :: "memory")
__device__ __forceinline__ void wait_flags(const unsigned* p, unsigned v, unsigned tag) {
    if (__builtin_expect(__builtin_amdgcn_ballot_w64(v != tag) != 0ull, 0)) {
        unsigned spins = 0;
        do { __builtin_amdgcn_s_sleep(1); } while (__builtin_amdgcn_ballot_w64(ld_flag(p) != tag) != 0ull && ++spins < (1u << 22));
    }
    asm volatile("" ::: "memory");
}

__device__ __forceinline__ unsigned cvtpk(float lo, float hi) {
    unsigned r; asm volatile("v_cvt_pk_bf16_f32 %0, %1, %2" : "=v"(r) : "v"(lo), "v"(hi)); return r;
}
__device__ __forceinline__ float bf2f(u16 v) { return __uint_as_float(((unsigned)v) << 16); }
__device__ __forceinline__ int crow(int r, int hi) { return (r & 3) + 8 * (r >> 2) + 4 * hi; }

typedef __attribute__((address_space(3))) s16x4* lds_s16x4_p;
__device__ __forceinline__ s16x4 trrd(unsigned addr) { return __builtin_amdgcn_ds_read_tr16_b64_v4i16((lds_s16x4_p)addr); }

__device__ __forceinline__ unsigned lane_id() { unsigned l; asm volatile("v_mbcnt_lo_u32_b32 %0, -1, 0\n\tv_mbcnt_hi_u32_b32 %0, -1, %0" : "=v"(l)); return l; }
#define RTS(slot) do {} while (0)
#define CT_END 0x1f300
#define ACLOB_LO "a0","a1","a2","a3","a4","a5","a6","a7","a8","a9","a10","a11","a12","a13","a14","a15","a16","a17","a18","a19","a20","a21","a22","a23","a24","a25","a26","a27","a28","a29","a30","a31","a32","a33","a34","a35","a36","a37","a38","a39","a40","a41","a42","a43","a44","a45","a46","a47","a48","a49","a50","a51","a52","a53","a54","a55","a56","a57","a58","a59","a60","a61","a62","a63","a64","a65","a66","a67","a68","a69","a70","a71","a72","a73","a74","a75","a76","a77","a78","a79","a80","a81","a82","a83","a84","a85","a86","a87","a88","a89","a90","a91","a92","a93","a94","a95","a96","a97","a98","a99","a100","a101","a102","a103","a104","a105","a106","a107","a108","a109","a110","a111","a112","a113","a114","a115","a116","a117","a118","a119","a120","a121","a122","a123","a124","a125","a126","a127"
#define ACLOB_HI "a128","a129","a130","a131","a132","a133","a134","a135","a136","a137","a138","a139","a140","a141","a142","a143","a144","a145","a146","a147","a148","a149","a150","a151","a152","a153","a154","a155","a156","a157","a158","a159","a160","a161","a162","a163","a164","a165","a166","a167","a168","a169","a170","a171","a172","a173","a174","a175","a176","a177","a178","a179","a180","a181","a182","a183","a184","a185","a186","a187","a188","a189","a190","a191","a192","a193","a194","a195","a196","a197","a198","a199","a200","a201","a202","a203","a204","a205","a206","a207","a208","a209","a210","a211","a212","a213","a214","a215","a216","a217","a218","a219","a220","a221","a222","a223","a224","a225","a226","a227","a228","a229","a230","a231","a232","a233","a234","a235","a236","a237","a238","a239","a240","a241","a242","a243","a244","a245","a246","a247","a248","a249","a250","a251","a252","a253","a254","a255"
#define PLO(p) ((unsigned)(size_t)(p))
#define PHI(p) ((unsigned)((size_t)(p) >> 32))
__device__ __forceinline__ unsigned vsel(bool c, unsigned a, unsigned b) { asm("" : "+v"(a), "+v"(b)); return c ? a : b; }
__device__ __forceinline__ unsigned ssel(unsigned c, unsigned a, unsigned b) { unsigned r; asm("s_cmp_lg_u32 %1, 0\n\ts_cselect_b32 %0, %2, %3" : "=s"(r) : "s"(c), "s"(a), "s"(b) : "scc"); return r; }
template <int N> __device__ __forceinline__ unsigned sslt(unsigned c, unsigned a, unsigned b) { unsigned r; asm("s_cmpk_lt_u32 %1, %4\n\ts_cselect_b32 %0, %2, %3" : "=s"(r) : "s"(c), "s"(a), "s"(b), "n"(N) : "scc"); return r; }
namespace att {
constexpr int LDS_BYTES = 163840;
constexpr unsigned LDS_G = 159744u, LDS_L = 161792u;
}
#define SD(J8, H, W) ((unsigned)WS_AF + (((J8) + 2u * ((W) & 2u) + ((W) & 1u)) * KS_TOT + 18u + 4u * (H)) * 1024u)
#define SRDA (u32x4){PLO(ws), PHI(ws) & 0xffffu, (unsigned)WS_L, 0x00020000u}
#define PROJ_ASM \
".p2align 3\n" \
"L_pb:\n" \
"s_waitcnt lgkmcnt(0)\n" \
"s_mov_b32 s64, s10\n" \
"s_and_b32 s65, s11, 0xffff\n" \
"s_mov_b32 s66, 0x200000\n" \
"s_mov_b32 s67, 0x20000\n" \
"s_mov_b32 s68, s14\n" \
"s_and_b32 s69, s15, 0xffff\n" \
"s_mov_b32 s70, 0x400000\n" \
"s_mov_b32 s71, 0x20000\n" \
"s_mov_b32 s72, s16\n" \
"s_and_b32 s73, s17, 0xffff\n" \
"s_mov_b32 s74, 0x400000\n" \
"s_mov_b32 s75, 0x20000\n" \
"s_add_u32 m0, s30, 0x0\n" \
"s_movk_i32 s84, 0x0\n" \
"buffer_load_dwordx4 v22, s[64:67], s84 offen offset:0 lds\n" \
"s_movk_i32 s84, 0x1c00\n" \
"buffer_load_dwordx4 v23, s[64:67], s84 offen offset:1024 lds\n" \
"s_movk_i32 s84, 0x3800\n" \
"buffer_load_dwordx4 v22, s[64:67], s84 offen offset:2048 lds\n" \
"s_movk_i32 s84, 0x5400\n" \
"buffer_load_dwordx4 v23, s[64:67], s84 offen offset:3072 lds\n" \
"s_add_u32 m0, s31, 0x0\n" \
"s_movk_i32 s84, 0x0\n" \
"buffer_load_dwordx4 v26, s[68:71], s84 offen offset:0 lds\n" \
"s_movk_i32 s84, 0x7c00\n" \
"buffer_load_dwordx4 v26, s[68:71], s84 offen offset:1024 lds\n" \
"s_addk_i32 s84, 0x7c00\n" \
"buffer_load_dwordx4 v27, s[68:71], s84 offen offset:2048 lds\n" \
"s_addk_i32 s84, 0x7c00\n" \
"buffer_load_dwordx4 v27, s[68:71], s84 offen offset:3072 lds\n" \
"s_movk_i32 s7, 0x300\n" \
"s_lshr_b32 s4, s2, 1\n" \
"s_and_b32 s4, s4, 60\n" \
"s_or_b32 s4, s4, s3\n" \
"v_lshrrev_b32_e32 v212, 4, v20\n" \
"v_min_u32_e32 v212, 59, v212\n" \
"s_cmpk_lt_u32 s2, 0x80\n" \
"s_cselect_b32 s6, s14, s16\n" \
"v_subrev_u32_e32 v214, 48, v212\n" \
"v_mov_b32_e32 v212, s10\n" \
"v_mov_b32_e32 v215, s6\n" \
"s_cmpk_lt_u32 s2, 0x80\n" \
"s_cselect_b32 s5, s15, s17\n" \
"v_cmp_gt_u32_e32 vcc, s7, v20\n" \
"v_mov_b32_e64 v213, s11\n" \
"v_bfe_u32 v216, v20, 4, 3\n" \
"v_cndmask_b32_e32 v212, v212, v215, vcc\n" \
"v_mov_b32_e32 v215, s5\n" \
"s_lshl_b32 s5, s3, 4\n" \
"v_cndmask_b32_e32 v213, v213, v215, vcc\n" \
"v_lshlrev_b32_e64 v215, 5, v20\n" \
"v_and_b32_e32 v215, 0x7000, v215\n" \
"v_lshl_add_u32 v215, s4, 6, v215\n" \
"s_lshl_b32 s4, s2, 3\n" \
"s_and_b32 s4, s4, 56\n" \
"v_or3_b32 v215, v215, s4, v216\n" \
"s_lshl_b32 s4, s2, 6\n" \
"s_or_b32 s6, s5, s4\n" \
"v_cmp_gt_u32_e64 s[4:5], 6, v214\n" \
"v_add_u32_e32 v215, 0x2000, v215\n" \
"s_nop 0\n" \
"v_cndmask_b32_e64 v216, 4, 2, s[4:5]\n" \
"v_add3_u32 v214, s6, v214, v216\n" \
"s_lshl_b32 s4, s3, 9\n" \
"v_cndmask_b32_e32 v214, v214, v215, vcc\n" \
"v_lshlrev_b32_e32 v214, 7, v214\n" \
"v_mov_b32_e64 v215, 0\n" \
"v_lshl_add_u64 v[184:185], v[212:213], 0, v[214:215]\n" \
"s_or_b32 s32, s4, 0x25000\n" \
"s_add_u32 m0, s30, 0x1000\n" \
"s_mov_b32 s84, 0x8000\n" \
"buffer_load_dwordx4 v22, s[64:67], s84 offen offset:0 lds\n" \
"s_and_b32 s33, s3, 1\n" \
"v_lshrrev_b32_e32 v212, 4, v20\n" \
"v_lshrrev_b32_e64 v214, 8, v20\n" \
"v_bfe_u32 v212, v212, 1, 3\n" \
"v_lshlrev_b32_e32 v215, 2, v20\n" \
"s_addk_i32 s84, 0x1c00\n" \
"buffer_load_dwordx4 v23, s[64:67], s84 offen offset:1024 lds\n" \
"v_lshrrev_b32_e64 v216, 1, v20\n" \
"v_bfe_u32 v213, v20, 4, 4\n" \
"s_lshl_b32 s4, s3, 5\n" \
"v_xor_b32_e32 v212, v212, v214\n" \
"v_lshlrev_b32_e32 v214, 11, v214\n" \
"s_addk_i32 s84, 0x1c00\n" \
"buffer_load_dwordx4 v22, s[64:67], s84 offen offset:2048 lds\n" \
"v_and_b32_e32 v215, 0x300, v215\n" \
"v_and_b32_e64 v217, 24, v216\n" \
"v_and_or_b32 v213, s4, 64, v213\n" \
"v_or3_b32 v214, v215, v214, v217\n" \
"s_lshl_b32 s4, s33, 14\n" \
"s_addk_i32 s84, 0x1c00\n" \
"buffer_load_dwordx4 v23, s[64:67], s84 offen offset:3072 lds\n" \
"v_and_b32_e32 v215, 0xe0, v216\n" \
"v_lshlrev_b32_e32 v213, 7, v213\n" \
"v_lshlrev_b32_e32 v212, 4, v212\n" \
"v_or3_b32 v214, v214, s4, v215\n" \
"v_or_b32_e64 v28, v212, v213\n" \
"s_add_u32 m0, s31, 0x4000\n" \
"s_movk_i32 s84, 0x0\n" \
"buffer_load_dwordx4 v26, s[72:75], s84 offen offset:0 lds\n" \
"v_add_u32_e32 v32, 0x10000, v214\n" \
"v_bitop3_b32 v29, v212, 64, v213 bitop3:0x36\n" \
"v_xor_b32_e32 v33, 0x20, v32\n" \
"v_xor_b32_e64 v34, 0x40, v32\n" \
"v_xor_b32_e32 v35, 0x60, v32\n" \
"s_movk_i32 s84, 0x7c00\n" \
"buffer_load_dwordx4 v26, s[72:75], s84 offen offset:1024 lds\n" \
"v_xor_b32_e32 v36, 0x80, v32\n" \
"v_xor_b32_e32 v37, 0xa0, v32\n" \
"v_xor_b32_e32 v38, 0xc0, v32\n" \
"v_xor_b32_e32 v39, 0xe0, v32\n" \
"s_mov_b32 s80, s26\n" \
"s_addk_i32 s84, 0x7c00\n" \
"buffer_load_dwordx4 v27, s[72:75], s84 offen offset:2048 lds\n" \
"s_and_b32 s81, s27, 0xffff\n" \
"s_mov_b32 s82, 0x10000000\n" \
"s_mov_b32 s83, 0x20000\n" \
"s_mov_b32 s76, s18\n" \
"s_and_b32 s77, s19, 0xffff\n" \
"s_addk_i32 s84, 0x7c00\n" \
"buffer_load_dwordx4 v27, s[72:75], s84 offen offset:3072 lds\n" \
"s_mov_b32 s78, 0x1e6000\n" \
"s_mov_b32 s79, 0x20000\n" \
"s_branch L_pgo_%=\n" \
"L_pret:\n" \
"s_setpc_b64 s[88:89]\n" \
"L_pgo_%=:\n" \
"s_mul_i32 s88, s33, 0xa000\n" \
"s_lshl_b32 s89, s3, 11\n" \
"s_and_b32 s89, s89, 0x1000\n" \
"s_add_u32 s88, s88, s89\n" \
"s_getpc_b64 s[86:87]\n" \
"L_hpa_%=:\n" \
"s_add_u32 s86, s86, L_pp1-L_hpa_%=\n" \
"s_addc_u32 s87, s87, 0\n" \
"s_add_u32 s86, s86, s88\n" \
"s_addc_u32 s87, s87, 0\n" \
"s_add_u32 m0, s30, 0x8000\n" \
"s_movk_i32 s84, 0x80\n" \
"buffer_load_dwordx4 v22, s[64:67], s84 offen offset:0 lds\n" \
"s_lshr_b32 s4, s2, 1\n" \
"s_and_b32 s4, s4, 0x7c\n" \
"v_bfe_u32 v212, v20, 4, 5\n" \
"s_movk_i32 s84, 0x1c80\n" \
"buffer_load_dwordx4 v23, s[64:67], s84 offen offset:1024 lds\n" \
"s_or_b32 s4, s4, s3\n" \
"v_min_u32_e32 v212, 26, v212\n" \
"v_mad_u64_u32 v[212:213], s[4:5], s4, 27, v[212:213]\n" \
"s_movk_i32 s84, 0x3880\n" \
"buffer_load_dwordx4 v22, s[64:67], s84 offen offset:2048 lds\n" \
"v_mul_u32_u24_e32 v213, 0xaaab, v212\n" \
"v_lshrrev_b32_e32 v213, 18, v213\n" \
"s_mov_b32 s4, 0x1fffffa\n" \
"s_movk_i32 s84, 0x5480\n" \
"buffer_load_dwordx4 v23, s[64:67], s84 offen offset:3072 lds\n" \
"v_mad_u64_u32 v[214:215], s[4:5], v213, s4, v[212:213]\n" \
"s_and_b32 s6, s2, 7\n" \
"v_mul_u32_u24_e32 v213, 27, v213\n" \
"s_mov_b64 exec, 0\n" \
"s_swappc_b64 s[88:89], s[86:87]\n" \
"s_mov_b64 exec, -1\n" \
"s_add_u32 s86, s86, 0x2000\n" \
"s_addc_u32 s87, s87, 0\n" \
"s_add_u32 m0, s31, 0x8000\n" \
"s_mov_b32 s84, 0x80000\n" \
"buffer_load_dwordx4 v26, s[68:71], s84 offen offset:0 lds\n" \
"v_lshrrev_b32_e32 v214, 1, v214\n" \
"v_and_b32_e32 v212, 1, v212\n" \
"v_mul_u32_u24_e32 v214, 9, v214\n" \
"s_addk_i32 s84, 0x7c00\n" \
"buffer_load_dwordx4 v26, s[68:71], s84 offen offset:1024 lds\n" \
"v_add3_u32 v212, v212, s6, v213\n" \
"v_add_lshl_u32 v212, v212, v214, 7\n" \
"v_mov_b32_e32 v213, 0\n" \
"s_mov_b64 exec, 0\n" \
"s_swappc_b64 s[88:89], s[86:87]\n" \
"s_mov_b64 exec, -1\n" \
"s_add_u32 s86, s86, 0x2000\n" \
"s_addc_u32 s87, s87, 0\n" \
"s_addk_i32 s84, 0x7c00\n" \
"buffer_load_dwordx4 v27, s[68:71], s84 offen offset:2048 lds\n" \
"v_lshl_add_u64 v[186:187], s[18:19], 0, v[212:213]\n" \
"s_movk_i32 s5, 0xd80\n" \
"s_lshr_b32 s6, s2, 5\n" \
"s_addk_i32 s84, 0x7c00\n" \
"buffer_load_dwordx4 v27, s[68:71], s84 offen offset:3072 lds\n" \
"v_lshrrev_b32_e64 v212, 4, v20\n" \
"v_lshl_or_b32 v215, s3, 6, v212\n" \
"v_mul_u32_u24_e32 v216, 0x2aab, v215\n" \
"s_mov_b64 exec, 0\n" \
"s_swappc_b64 s[88:89], s[86:87]\n" \
"s_mov_b64 exec, -1\n" \
"s_add_u32 s86, s86, 0x2000\n" \
"s_addc_u32 s87, s87, 0\n" \
"s_add_u32 m0, s30, 0x9000\n" \
"s_mov_b32 s84, 0x8080\n" \
"buffer_load_dwordx4 v22, s[64:67], s84 offen offset:0 lds\n" \
"v_lshrrev_b32_e64 v216, 16, v216\n" \
"v_mad_i32_i24 v217, v216, -6, v215\n" \
"v_add_u32_e32 v218, 0xfffffe50, v215\n" \
"s_addk_i32 s84, 0x1c00\n" \
"buffer_load_dwordx4 v23, s[64:67], s84 offen offset:1024 lds\n" \
"s_mul_i32 s35, s6, 0x48\n" \
"v_lshrrev_b32_e32 v219, 1, v217\n" \
"v_add_u32_e32 v218, s35, v216\n" \
"s_addk_i32 s84, 0x1c00\n" \
"buffer_load_dwordx4 v22, s[64:67], s84 offen offset:2048 lds\n" \
"v_mul_u32_u24_e32 v219, 0x480, v219\n" \
"v_and_b32_e64 v214, 16, v20\n" \
"v_mad_u32_u24 v219, v218, s5, v219\n" \
"s_mov_b64 exec, 0\n" \
"s_swappc_b64 s[88:89], s[86:87]\n" \
"s_mov_b64 exec, -1\n" \
"s_andn2_b32 s85, 2, s3\n" \
"s_lshl_b32 s85, s85, 12\n" \
"s_add_u32 s86, s86, s85\n" \
"s_addc_u32 s87, s87, 0\n" \
"s_addk_i32 s84, 0x1c00\n" \
"buffer_load_dwordx4 v23, s[64:67], s84 offen offset:3072 lds\n" \
"v_or_b32_e32 v192, v219, v214\n" \
"v_mov_b32_e32 v220, 2.0\n" \
"v_mov_b32_e64 v222, 7\n" \
"s_add_u32 m0, s31, 0xc000\n" \
"s_mov_b32 s84, 0x80000\n" \
"buffer_load_dwordx4 v26, s[72:75], s84 offen offset:0 lds\n" \
"v_lshlrev_b32_e32 v220, 7, v218\n" \
"v_lshrrev_b32_e32 v218, 1, v218\n" \
"v_and_b32_e32 v218, 4, v218\n" \
"s_mov_b64 exec, 0\n" \
"s_swappc_b64 s[88:89], s[86:87]\n" \
"s_mov_b64 exec, -1\n" \
"s_addk_i32 s84, 0x7c00\n" \
"buffer_load_dwordx4 v26, s[72:75], s84 offen offset:1024 lds\n" \
"v_and_b32_e32 v216, 2, v216\n" \
"s_mul_i32 s7, s6, 0x2400\n" \
"s_sub_i32 s34, 0x22000, s7\n" \
"s_addk_i32 s84, 0x7c00\n" \
"buffer_load_dwordx4 v27, s[72:75], s84 offen offset:2048 lds\n" \
"v_bitop3_b32 v216, v217, v218, v216 bitop3:0x1e\n" \
"v_or_b32_e32 v213, 0x24400, v20\n" \
"v_lshlrev_b32_e32 v216, 4, v216\n" \
"s_addk_i32 s84, 0x7c00\n" \
"buffer_load_dwordx4 v27, s[72:75], s84 offen offset:3072 lds\n" \
"v_add3_u32 v196, v220, s34, v216\n" \
"v_mov_b32_e32 v216, v213\n" \
"s_movk_i32 s36, 0x1b0\n" \
"s_mov_b32 m0, s32\n" \
"s_mov_b32 exec_lo, 0xffffff\n" \
"s_mov_b32 exec_hi, 0xfff0000\n" \
"global_load_lds_dword v[184:185], off\n" \
"s_mov_b64 exec, -1\n" \
"s_mov_b32 s84, 0x5bc0000\n" \
"buffer_load_dword v180, off, s[80:83], s84 sc1\n" \
"s_cmp_lg_u32 s33, 0\n" \
"s_cbranch_scc1 L_w1_%=\n" \
"L_w0_%=:\n" \
"s_waitcnt vmcnt(18)\n" \
"s_barrier\n" \
"s_nop 0\n" \
"ds_read_b64_tr_b16 a[160:161], v32 offset:0\n" \
"ds_read_b64_tr_b16 a[162:163], v32 offset:1024\n" \
"ds_read_b128 a[128:131], v28 offset:16384\n" \
"ds_read_b64_tr_b16 a[224:225], v32 offset:8192\n" \
"ds_read_b64_tr_b16 a[226:227], v32 offset:9216\n" \
"ds_read_b128 a[192:195], v29 offset:16384\n" \
"ds_read_b128 a[132:135], v28 offset:18432\n" \
"ds_read_b128 a[196:199], v29 offset:18432\n" \
"ds_read_b64_tr_b16 a[164:165], v33 offset:0\n" \
"ds_read_b64_tr_b16 a[166:167], v33 offset:1024\n" \
"ds_read_b64_tr_b16 a[228:229], v33 offset:8192\n" \
"ds_read_b64_tr_b16 a[230:231], v33 offset:9216\n" \
"ds_read_b128 a[136:139], v28 offset:20480\n" \
"ds_read_b128 a[200:203], v29 offset:20480\n" \
"s_waitcnt lgkmcnt(11)\n" \
"v_mfma_f32_16x16x32_bf16 a[0:3], a[160:163], a[128:131], 0\n" \
"ds_read_b64_tr_b16 a[168:169], v34 offset:0\n" \
"v_or_b32_e32 v216, 0x100, v215\n" \
"s_waitcnt lgkmcnt(9)\n" \
"v_mfma_f32_16x16x32_bf16 a[0:3], a[224:227], a[192:195], a[0:3]\n" \
"ds_read_b64_tr_b16 a[170:171], v34 offset:1024\n" \
"v_mul_u32_u24_e32 v217, 0x2aab, v216\n" \
"s_waitcnt lgkmcnt(9)\n" \
"v_mfma_f32_16x16x32_bf16 a[16:19], a[160:163], a[132:135], 0\n" \
"ds_read_b64_tr_b16 a[232:233], v34 offset:8192\n" \
"v_add_u32_e32 v215, 0xffffff50, v215\n" \
"s_waitcnt lgkmcnt(9)\n" \
"v_mfma_f32_16x16x32_bf16 a[16:19], a[224:227], a[196:199], a[16:19]\n" \
"ds_read_b64_tr_b16 a[234:235], v34 offset:9216\n" \
"v_lshrrev_b32_e32 v217, 16, v217\n" \
"s_waitcnt lgkmcnt(8)\n" \
"v_mfma_f32_16x16x32_bf16 a[20:23], a[164:167], a[132:135], 0\n" \
"ds_read_b128 a[140:143], v28 offset:22528\n" \
"v_mov_b32_e32 v220, v215\n" \
"s_waitcnt lgkmcnt(7)\n" \
"v_mfma_f32_16x16x32_bf16 a[20:23], a[228:231], a[196:199], a[20:23]\n" \
"ds_read_b128 a[204:207], v29 offset:22528\n" \
"v_mad_i32_i24 v218, v217, -6, v216\n" \
"v_mfma_f32_16x16x32_bf16 a[4:7], a[164:167], a[128:131], 0\n" \
"ds_read_b64_tr_b16 a[172:173], v35 offset:0\n" \
"v_cmp_gt_u32_e64 vcc, s36, v216\n" \
"v_mfma_f32_16x16x32_bf16 a[4:7], a[228:231], a[192:195], a[4:7]\n" \
"ds_read_b64_tr_b16 a[174:175], v35 offset:1024\n" \
"v_mov_b32_e32 v221, 7\n" \
"s_waitcnt lgkmcnt(9)\n" \
"v_mfma_f32_16x16x32_bf16 a[32:35], a[160:163], a[136:139], 0\n" \
"ds_read_b64_tr_b16 a[236:237], v35 offset:8192\n" \
"v_lshrrev_b32_e32 v222, 1, v218\n" \
"s_waitcnt lgkmcnt(9)\n" \
"v_mfma_f32_16x16x32_bf16 a[32:35], a[224:227], a[200:203], a[32:35]\n" \
"ds_read_b64_tr_b16 a[238:239], v35 offset:9216\n" \
"v_cndmask_b32_e64 v217, v220, v217, vcc\n" \
"v_mfma_f32_16x16x32_bf16 a[36:39], a[164:167], a[136:139], 0\n" \
"ds_read_b128 a[144:147], v28 offset:0\n" \
"s_movk_i32 s4, 0x48\n" \
"v_mfma_f32_16x16x32_bf16 a[36:39], a[228:231], a[200:203], a[36:39]\n" \
"ds_read_b128 a[208:211], v29 offset:0\n" \
"v_add_u32_e32 v220, s35, v217\n" \
"s_waitcnt lgkmcnt(10)\n" \
"v_mfma_f32_16x16x32_bf16 a[40:43], a[168:171], a[136:139], 0\n" \
"ds_read_b128 a[148:151], v28 offset:2048\n" \
"v_mul_u32_u24_e32 v222, 0x480, v222\n" \
"s_waitcnt lgkmcnt(9)\n" \
"v_mfma_f32_16x16x32_bf16 a[40:43], a[232:235], a[200:203], a[40:43]\n" \
"ds_read_b128 a[212:215], v29 offset:2048\n" \
"v_mad_u32_u24 v222, v220, s5, v222\n" \
"v_mfma_f32_16x16x32_bf16 a[24:27], a[168:171], a[132:135], 0\n" \
"ds_read_b128 a[152:155], v28 offset:4096\n" \
"v_cmp_gt_u32_e64 s[4:5], s4, v215\n" \
"v_mfma_f32_16x16x32_bf16 a[24:27], a[232:235], a[196:199], a[24:27]\n" \
"ds_read_b128 a[216:219], v29 offset:4096\n" \
"v_cndmask_b32_e64 v215, v221, v218, vcc\n" \
"v_mfma_f32_16x16x32_bf16 a[8:11], a[168:171], a[128:131], 0\n" \
"ds_read_b128 a[156:159], v28 offset:6144\n" \
"v_lshrrev_b32_e64 v218, 1, v220\n" \
"v_mfma_f32_16x16x32_bf16 a[8:11], a[232:235], a[192:195], a[8:11]\n" \
"ds_read_b128 a[220:223], v29 offset:6144\n" \
"s_mov_b32 m0, s32\n" \
"s_waitcnt lgkmcnt(13)\n" \
"v_mfma_f32_16x16x32_bf16 a[48:51], a[160:163], a[140:143], 0\n" \
"s_mov_b32 exec_lo, 0xff000000\n" \
"s_mov_b32 exec_hi, 0x0\n" \
"global_load_lds_dword v[184:185], off\n" \
"s_mov_b64 exec, -1\n" \
"s_waitcnt lgkmcnt(12)\n" \
"v_mfma_f32_16x16x32_bf16 a[48:51], a[224:227], a[204:207], a[48:51]\n" \
"ds_read_b64_tr_b16 a[176:177], v36 offset:0\n" \
"v_or_b32_e32 v214, v222, v214\n" \
"v_mfma_f32_16x16x32_bf16 a[52:55], a[164:167], a[140:143], 0\n" \
"ds_read_b64_tr_b16 a[178:179], v36 offset:1024\n" \
"v_mov_b32_e64 v222, 2.0\n" \
"v_mfma_f32_16x16x32_bf16 a[52:55], a[228:231], a[204:207], a[52:55]\n" \
"s_mov_b32 m0, s32\n" \
"ds_read_b64_tr_b16 a[240:241], v36 offset:8192\n" \
"v_mfma_f32_16x16x32_bf16 a[56:59], a[168:171], a[140:143], 0\n" \
"s_mov_b32 exec_lo, 0xffff\n" \
"s_mov_b32 exec_hi, 0x0\n" \
"global_load_lds_dword v[186:187], off\n" \
"s_mov_b64 exec, -1\n" \
"v_mfma_f32_16x16x32_bf16 a[56:59], a[232:235], a[204:207], a[56:59]\n" \
"ds_read_b64_tr_b16 a[242:243], v36 offset:9216\n" \
"v_and_b32_e32 v218, 4, v218\n" \
"s_waitcnt lgkmcnt(14)\n" \
"v_mfma_f32_16x16x32_bf16 a[60:63], a[172:175], a[140:143], 0\n" \
"ds_read_b64_tr_b16 a[180:181], v37 offset:0\n" \
"v_and_b32_e32 v217, 2, v217\n" \
"s_waitcnt lgkmcnt(13)\n" \
"v_mfma_f32_16x16x32_bf16 a[60:63], a[236:239], a[204:207], a[60:63]\n" \
"ds_read_b64_tr_b16 a[182:183], v37 offset:1024\n" \
"v_bitop3_b32 v215, v215, v218, v217 bitop3:0x1e\n" \
"v_mfma_f32_16x16x32_bf16 a[44:47], a[172:175], a[136:139], 0\n" \
"ds_read_b64_tr_b16 a[244:245], v37 offset:8192\n" \
"v_lshlrev_b32_e32 v223, 1, v220\n" \
"v_mfma_f32_16x16x32_bf16 a[44:47], a[236:239], a[200:203], a[44:47]\n" \
"ds_read_b64_tr_b16 a[246:247], v37 offset:9216\n" \
"v_mov_b32_e64 v224, 2.0\n" \
"v_mfma_f32_16x16x32_bf16 a[28:31], a[172:175], a[132:135], 0\n" \
"ds_read_b64_tr_b16 a[184:185], v38 offset:0\n" \
"v_cndmask_b32_e64 v193, v222, v214, vcc\n" \
"v_mfma_f32_16x16x32_bf16 a[28:31], a[236:239], a[196:199], a[28:31]\n" \
"ds_read_b64_tr_b16 a[186:187], v38 offset:1024\n" \
"v_lshlrev_b32_e64 v214, 7, v220\n" \
"v_mfma_f32_16x16x32_bf16 a[12:15], a[172:175], a[128:131], 0\n" \
"v_lshlrev_b32_e32 v215, 4, v215\n" \
"s_mul_i32 s39, s3, 18\n" \
"v_mfma_f32_16x16x32_bf16 a[12:15], a[236:239], a[192:195], a[12:15]\n" \
"v_add3_u32 v214, v214, s34, v215\n" \
"v_cndmask_b32_e64 v195, v224, v223, s[4:5]\n" \
"s_waitcnt lgkmcnt(15)\n" \
"v_mfma_f32_16x16x32_bf16 a[76:79], a[172:175], a[144:147], 0\n" \
"ds_read_b64_tr_b16 a[248:249], v38 offset:8192\n" \
"s_movk_i32 s4, 0x1f8\n" \
"v_mfma_f32_16x16x32_bf16 a[76:79], a[236:239], a[208:211], a[76:79]\n" \
"v_mov_b32_e32 v215, v213\n" \
"s_add_i32 s35, s35, s39\n" \
"v_mfma_f32_16x16x32_bf16 a[72:75], a[168:171], a[144:147], 0\n" \
"v_cmp_gt_u32_e32 vcc, s4, v216\n" \
"v_add_u32_e32 v212, s35, v212\n" \
"v_mfma_f32_16x16x32_bf16 a[72:75], a[232:235], a[208:211], a[72:75]\n" \
"ds_read_b64_tr_b16 a[250:251], v38 offset:9216\n" \
"v_mov_b32_e64 v219, 2.0\n" \
"v_mfma_f32_16x16x32_bf16 a[68:71], a[164:167], a[144:147], 0\n" \
"v_cndmask_b32_e32 v197, v215, v214, vcc\n" \
"s_movk_i32 s35, 0x120\n" \
"v_mfma_f32_16x16x32_bf16 a[68:71], a[228:231], a[208:211], a[68:71]\n" \
"v_mul_u32_u24_e32 v214, 0xd80, v212\n" \
"v_cmp_gt_u32_e64 vcc, s35, v20\n" \
"v_mfma_f32_16x16x32_bf16 a[64:67], a[160:163], a[144:147], 0\n" \
"v_lshrrev_b32_e32 v215, 1, v212\n" \
"s_and_b32 s4, s2, 7\n" \
"v_mfma_f32_16x16x32_bf16 a[64:67], a[224:227], a[208:211], a[64:67]\n" \
"ds_read_b64_tr_b16 a[188:189], v39 offset:0\n" \
"v_cndmask_b32_e64 v194, v219, v214, vcc\n" \
"v_mfma_f32_16x16x32_bf16 a[80:83], a[160:163], a[148:151], 0\n" \
"v_lshlrev_b32_e32 v214, 7, v212\n" \
"v_lshrrev_b32_e32 v212, 2, v212\n" \
"s_waitcnt lgkmcnt(15)\n" \
"v_mfma_f32_16x16x32_bf16 a[80:83], a[224:227], a[212:215], a[80:83]\n" \
"v_and_b32_e32 v212, 2, v212\n" \
"v_and_or_b32 v212, v215, 1, v212\n" \
"v_mfma_f32_16x16x32_bf16 a[84:87], a[164:167], a[148:151], 0\n" \
"ds_read_b64_tr_b16 a[190:191], v39 offset:1024\n" \
"s_lshl_b32 s5, s4, 2\n" \
"v_mfma_f32_16x16x32_bf16 a[84:87], a[228:231], a[212:215], a[84:87]\n" \
"s_bfe_u32 s38, s2, 0x20003\n" \
"v_lshlrev_b32_e32 v212, 5, v212\n" \
"v_mfma_f32_16x16x32_bf16 a[88:91], a[168:171], a[148:151], 0\n" \
"s_movk_i32 s35, 0x60\n" \
"s_or_b32 s5, s5, s38\n" \
"v_mfma_f32_16x16x32_bf16 a[88:91], a[232:235], a[212:215], a[88:91]\n" \
"ds_read_b64_tr_b16 a[252:253], v39 offset:8192\n" \
"v_bitop3_b32 v212, v212, s35, v214 bitop3:0x36\n" \
"v_mfma_f32_16x16x32_bf16 a[92:95], a[172:175], a[148:151], 0\n" \
"s_lshr_b32 s36, s2, 3\n" \
"v_add_u32_e32 v212, s34, v212\n" \
"v_mfma_f32_16x16x32_bf16 a[92:95], a[236:239], a[212:215], a[92:95]\n" \
"s_mul_i32 s34, s5, 0x12000\n" \
"s_or_b32 s40, s34, 0x6000000\n" \
"v_mfma_f32_16x16x32_bf16 a[108:111], a[172:175], a[152:155], 0\n" \
"ds_read_b64_tr_b16 a[254:255], v39 offset:9216\n" \
"s_mul_i32 s34, s36, 0x90000\n" \
"v_mfma_f32_16x16x32_bf16 a[108:111], a[236:239], a[216:219], a[108:111]\n" \
"s_lshl_b32 s36, s3, 14\n" \
"s_add_i32 s34, s34, s36\n" \
"v_mfma_f32_16x16x32_bf16 a[104:107], a[168:171], a[152:155], 0\n" \
"s_lshl_b32 s38, s5, 8\n" \
"s_add_i32 s41, s34, 0x4240000\n" \
"v_mfma_f32_16x16x32_bf16 a[104:107], a[232:235], a[216:219], a[104:107]\n" \
"s_mul_i32 s34, s5, 36\n" \
"s_add_i32 s5, s7, s40\n" \
"v_mfma_f32_16x16x32_bf16 a[100:103], a[164:167], a[152:155], 0\n" \
"s_addk_i32 s5, 0x2000\n" \
"s_mov_b32 s37, 2.0\n" \
"v_mfma_f32_16x16x32_bf16 a[100:103], a[228:231], a[216:219], a[100:103]\n" \
"s_or_b32 s38, s38, 0x5be8000\n" \
"v_mfma_f32_16x16x32_bf16 a[96:99], a[160:163], a[152:155], 0\n" \
"s_cmp_lg_u32 s3, 0\n" \
"s_cselect_b32 s36, s37, s5\n" \
"v_mfma_f32_16x16x32_bf16 a[96:99], a[224:227], a[216:219], a[96:99]\n" \
"s_lshl_b32 s5, s6, 2\n" \
"s_or_b32 s5, s38, s5\n" \
"s_waitcnt lgkmcnt(15)\n" \
"v_mfma_f32_16x16x32_bf16 a[112:115], a[160:163], a[156:159], 0\n" \
"s_lshl_b32 s35, s3, 10\n" \
"v_mfma_f32_16x16x32_bf16 a[112:115], a[224:227], a[220:223], a[112:115]\n" \
"s_cmpk_lt_u32 s3, 3\n" \
"s_cselect_b32 s37, s37, s5\n" \
"v_mfma_f32_16x16x32_bf16 a[116:119], a[164:167], a[156:159], 0\n" \
"s_lshl_b32 s5, s4, 16\n" \
"s_lshl_b32 s4, s4, 11\n" \
"v_mfma_f32_16x16x32_bf16 a[116:119], a[228:231], a[220:223], a[116:119]\n" \
"v_cndmask_b32_e32 v198, v213, v212, vcc\n" \
"v_or_b32_e64 v212, s35, v20\n" \
"v_mfma_f32_16x16x32_bf16 a[120:123], a[168:171], a[156:159], 0\n" \
"s_or_b32 s39, s40, s35\n" \
"s_or_b32 s55, s41, s4\n" \
"v_mfma_f32_16x16x32_bf16 a[120:123], a[232:235], a[220:223], a[120:123]\n" \
"v_or_b32_e32 v199, 0x22000, v212\n" \
"s_add_i32 s35, s39, s7\n" \
"v_mfma_f32_16x16x32_bf16 a[124:127], a[172:175], a[156:159], 0\n" \
"s_add_i32 s54, s41, s5\n" \
"s_add_i32 s55, s55, 0x80000\n" \
"v_mfma_f32_16x16x32_bf16 a[124:127], a[236:239], a[220:223], a[124:127]\n" \
"s_waitcnt vmcnt(4) lgkmcnt(0)\n" \
"s_barrier\n" \
"v_mfma_f32_16x16x32_bf16 v[52:55], a[176:179], a[128:131], 0\n" \
"s_add_u32 m0, s30, 0x0\n" \
"s_movk_i32 s84, 0x100\n" \
"v_mfma_f32_16x16x32_bf16 v[52:55], a[240:243], a[192:195], v[52:55]\n" \
"buffer_load_dwordx4 v22, s[64:67], s84 offen offset:0 lds\n" \
"v_mfma_f32_16x16x32_bf16 v[56:59], a[180:183], a[128:131], 0\n" \
"ds_read_b64_tr_b16 a[160:161], v32 offset:32768\n" \
"s_lshr_b32 s7, s2, 3\n" \
"v_mfma_f32_16x16x32_bf16 v[56:59], a[244:247], a[192:195], v[56:59]\n" \
"s_movk_i32 s84, 0x1d00\n" \
"v_mfma_f32_16x16x32_bf16 v[60:63], a[184:187], a[128:131], 0\n" \
"buffer_load_dwordx4 v23, s[64:67], s84 offen offset:1024 lds\n" \
"v_mfma_f32_16x16x32_bf16 v[60:63], a[248:251], a[192:195], v[60:63]\n" \
"ds_read_b64_tr_b16 a[162:163], v32 offset:33792\n" \
"s_and_b32 s6, s2, 7\n" \
"v_mfma_f32_16x16x32_bf16 v[64:67], a[188:191], a[128:131], 0\n" \
"ds_read_b64_tr_b16 a[224:225], v32 offset:40960\n" \
"s_lshl_b32 s4, s7, 2\n" \
"v_mfma_f32_16x16x32_bf16 v[64:67], a[252:255], a[192:195], v[64:67]\n" \
"ds_read_b128 a[128:131], v28 offset:49152\n" \
"s_lshr_b32 s5, s2, 2\n" \
"v_mfma_f32_16x16x32_bf16 v[80:83], a[188:191], a[132:135], 0\n" \
"ds_read_b128 a[192:195], v29 offset:49152\n" \
"s_or_b32 s4, s4, s3\n" \
"v_mfma_f32_16x16x32_bf16 v[80:83], a[252:255], a[196:199], v[80:83]\n" \
"s_movk_i32 s84, 0x3900\n" \
"v_mfma_f32_16x16x32_bf16 v[76:79], a[184:187], a[132:135], 0\n" \
"buffer_load_dwordx4 v22, s[64:67], s84 offen offset:2048 lds\n" \
"v_mfma_f32_16x16x32_bf16 v[76:79], a[248:251], a[196:199], v[76:79]\n" \
"s_movk_i32 s84, 0x5500\n" \
"v_mfma_f32_16x16x32_bf16 v[72:75], a[180:183], a[132:135], 0\n" \
"buffer_load_dwordx4 v23, s[64:67], s84 offen offset:3072 lds\n" \
"v_mfma_f32_16x16x32_bf16 v[72:75], a[244:247], a[196:199], v[72:75]\n" \
"s_add_u32 m0, s31, 0x0\n" \
"s_mov_b32 s84, 0x100000\n" \
"v_mfma_f32_16x16x32_bf16 v[68:71], a[176:179], a[132:135], 0\n" \
"buffer_load_dwordx4 v26, s[68:71], s84 offen offset:0 lds\n" \
"v_mfma_f32_16x16x32_bf16 v[68:71], a[240:243], a[196:199], v[68:71]\n" \
"ds_read_b128 a[132:135], v28 offset:51200\n" \
"v_lshrrev_b32_e32 v212, 4, v20\n" \
"v_mfma_f32_16x16x32_bf16 v[84:87], a[176:179], a[136:139], 0\n" \
"ds_read_b128 a[196:199], v29 offset:51200\n" \
"s_lshl_b32 s41, s6, 3\n" \
"v_mfma_f32_16x16x32_bf16 v[84:87], a[240:243], a[200:203], v[84:87]\n" \
"s_addk_i32 s84, 0x7c00\n" \
"v_mfma_f32_16x16x32_bf16 v[88:91], a[180:183], a[136:139], 0\n" \
"buffer_load_dwordx4 v26, s[68:71], s84 offen offset:1024 lds\n" \
"v_mfma_f32_16x16x32_bf16 v[88:91], a[244:247], a[200:203], v[88:91]\n" \
"ds_read_b64_tr_b16 a[226:227], v32 offset:41984\n" \
"s_and_b32 s5, s5, 6\n" \
"v_mfma_f32_16x16x32_bf16 v[92:95], a[184:187], a[136:139], 0\n" \
"s_addk_i32 s84, 0x7c00\n" \
"v_mfma_f32_16x16x32_bf16 v[92:95], a[248:251], a[200:203], v[92:95]\n" \
"buffer_load_dwordx4 v27, s[68:71], s84 offen offset:2048 lds\n" \
"v_mfma_f32_16x16x32_bf16 v[96:99], a[188:191], a[136:139], 0\n" \
"ds_read_b64_tr_b16 a[164:165], v33 offset:32768\n" \
"s_lshr_b32 s40, s2, 5\n" \
"v_mfma_f32_16x16x32_bf16 v[96:99], a[252:255], a[200:203], v[96:99]\n" \
"ds_read_b128 a[136:139], v28 offset:53248\n" \
"s_or_b32 s42, s41, s5\n" \
"v_mfma_f32_16x16x32_bf16 v[112:115], a[188:191], a[140:143], 0\n" \
"ds_read_b128 a[200:203], v29 offset:53248\n" \
"v_mad_u64_u32 v[214:215], s[4:5], s4, 36, v[212:213]\n" \
"v_mfma_f32_16x16x32_bf16 v[112:115], a[252:255], a[204:207], v[112:115]\n" \
"s_addk_i32 s84, 0x7c00\n" \
"v_mfma_f32_16x16x32_bf16 v[108:111], a[184:187], a[140:143], 0\n" \
"buffer_load_dwordx4 v27, s[68:71], s84 offen offset:3072 lds\n" \
"v_mfma_f32_16x16x32_bf16 v[108:111], a[248:251], a[204:207], v[108:111]\n" \
"s_add_u32 m0, s30, 0x1000\n" \
"s_mov_b32 s84, 0x8100\n" \
"v_mfma_f32_16x16x32_bf16 v[104:107], a[180:183], a[140:143], 0\n" \
"buffer_load_dwordx4 v22, s[64:67], s84 offen offset:0 lds\n" \
"v_mfma_f32_16x16x32_bf16 v[104:107], a[244:247], a[204:207], v[104:107]\n" \
"ds_read_b64_tr_b16 a[166:167], v33 offset:33792\n" \
"s_lshl_b32 s4, s40, 6\n" \
"v_mfma_f32_16x16x32_bf16 v[100:103], a[176:179], a[140:143], 0\n" \
"ds_read_b64_tr_b16 a[228:229], v33 offset:40960\n" \
"s_lshl_b32 s5, s3, 1\n" \
"v_mfma_f32_16x16x32_bf16 v[100:103], a[240:243], a[204:207], v[100:103]\n" \
"ds_read_b128 a[140:143], v28 offset:55296\n" \
"s_or_b32 s4, s4, s5\n" \
"v_mfma_f32_16x16x32_bf16 v[116:119], a[176:179], a[144:147], 0\n" \
"ds_read_b128 a[204:207], v29 offset:55296\n" \
"s_lshl_b32 s5, s2, 2\n" \
"v_mfma_f32_16x16x32_bf16 v[116:119], a[240:243], a[208:211], v[116:119]\n" \
"s_addk_i32 s84, 0x1c00\n" \
"v_mfma_f32_16x16x32_bf16 v[120:123], a[180:183], a[144:147], 0\n" \
"buffer_load_dwordx4 v23, s[64:67], s84 offen offset:1024 lds\n" \
"v_mfma_f32_16x16x32_bf16 v[120:123], a[244:247], a[208:211], v[120:123]\n" \
"s_addk_i32 s84, 0x1c00\n" \
"v_mfma_f32_16x16x32_bf16 v[124:127], a[184:187], a[144:147], 0\n" \
"buffer_load_dwordx4 v22, s[64:67], s84 offen offset:2048 lds\n" \
"v_mfma_f32_16x16x32_bf16 v[124:127], a[248:251], a[208:211], v[124:127]\n" \
"s_addk_i32 s84, 0x1c00\n" \
"v_mfma_f32_16x16x32_bf16 v[128:131], a[188:191], a[144:147], 0\n" \
"buffer_load_dwordx4 v23, s[64:67], s84 offen offset:3072 lds\n" \
"v_mfma_f32_16x16x32_bf16 v[128:131], a[252:255], a[208:211], v[128:131]\n" \
"ds_read_b128 a[144:147], v28 offset:32768\n" \
"s_or_b32 s43, s5, s3\n" \
"v_mfma_f32_16x16x32_bf16 v[144:147], a[188:191], a[148:151], 0\n" \
"ds_read_b128 a[208:211], v29 offset:32768\n" \
"s_or_b32 s4, s4, s41\n" \
"v_mfma_f32_16x16x32_bf16 v[144:147], a[252:255], a[212:215], v[144:147]\n" \
"s_add_u32 m0, s31, 0x4000\n" \
"s_mov_b32 s84, 0x100000\n" \
"v_mfma_f32_16x16x32_bf16 v[140:143], a[184:187], a[148:151], 0\n" \
"buffer_load_dwordx4 v26, s[72:75], s84 offen offset:0 lds\n" \
"v_mfma_f32_16x16x32_bf16 v[140:143], a[248:251], a[212:215], v[140:143]\n" \
"ds_read_b64_tr_b16 a[230:231], v33 offset:41984\n" \
"s_lshr_b32 s5, s2, 1\n" \
"v_mfma_f32_16x16x32_bf16 v[136:139], a[180:183], a[148:151], 0\n" \
"s_addk_i32 s84, 0x7c00\n" \
"v_mfma_f32_16x16x32_bf16 v[136:139], a[244:247], a[212:215], v[136:139]\n" \
".fill (L_pb + 3808 - .) / 4, 4, 0xBF800000\n" \
"L_pp1:\n" \
"s_cbranch_execz L_pret\n" \
"buffer_load_dwordx4 v26, s[72:75], s84 offen offset:1024 lds\n" \
"v_mfma_f32_16x16x32_bf16 v[132:135], a[176:179], a[148:151], 0\n" \
"ds_read_b64_tr_b16 a[168:169], v34 offset:32768\n" \
"s_lshl_b32 s41, s43, 1\n" \
"v_mfma_f32_16x16x32_bf16 v[132:135], a[240:243], a[212:215], v[132:135]\n" \
"ds_read_b128 a[148:151], v28 offset:34816\n" \
"s_mulk_i32 s5, 0x490\n" \
"v_mfma_f32_16x16x32_bf16 v[148:151], a[176:179], a[152:155], 0\n" \
"ds_read_b128 a[212:215], v29 offset:34816\n" \
"s_and_b32 s41, s41, 14\n" \
"v_mfma_f32_16x16x32_bf16 v[148:151], a[240:243], a[216:219], v[148:151]\n" \
"ds_read_b64_tr_b16 a[170:171], v34 offset:33792\n" \
"s_or_b32 s5, s5, s41\n" \
"v_mfma_f32_16x16x32_bf16 v[152:155], a[180:183], a[152:155], 0\n" \
"ds_read_b64_tr_b16 a[232:233], v34 offset:40960\n" \
"s_or_b32 s41, s42, 0xb7d00\n" \
"v_mfma_f32_16x16x32_bf16 v[152:155], a[244:247], a[216:219], v[152:155]\n" \
"ds_read_b64_tr_b16 a[234:235], v34 offset:41984\n" \
"v_mov_b32_e32 v220, s41\n" \
"v_mfma_f32_16x16x32_bf16 v[156:159], a[184:187], a[152:155], 0\n" \
"ds_read_b64_tr_b16 a[172:173], v35 offset:32768\n" \
"s_mul_i32 s7, s7, 0x90000\n" \
"v_mfma_f32_16x16x32_bf16 v[156:159], a[248:251], a[216:219], v[156:159]\n" \
"ds_read_b64_tr_b16 a[174:175], v35 offset:33792\n" \
"s_lshl_b32 s41, s3, 14\n" \
"v_mfma_f32_16x16x32_bf16 v[160:163], a[188:191], a[152:155], 0\n" \
"ds_read_b64_tr_b16 a[236:237], v35 offset:40960\n" \
"s_mulk_i32 s40, 0xa00\n" \
"v_mfma_f32_16x16x32_bf16 v[160:163], a[252:255], a[216:219], v[160:163]\n" \
"ds_read_b128 a[152:155], v28 offset:36864\n" \
"s_add_i32 s7, s7, s41\n" \
"v_mfma_f32_16x16x32_bf16 v[176:179], a[188:191], a[156:159], 0\n" \
"ds_read_b128 a[216:219], v29 offset:36864\n" \
"s_lshl_b32 s41, s6, 16\n" \
"v_mfma_f32_16x16x32_bf16 v[176:179], a[252:255], a[220:223], v[176:179]\n" \
"ds_read_b64_tr_b16 a[238:239], v35 offset:41984\n" \
"s_add_i32 s40, s40, 0x5bc1000\n" \
"v_mfma_f32_16x16x32_bf16 v[172:175], a[184:187], a[156:159], 0\n" \
"s_add_i32 s7, s7, s41\n" \
"s_lshr_b32 s40, s40, 7\n" \
"v_mfma_f32_16x16x32_bf16 v[172:175], a[248:251], a[220:223], v[172:175]\n" \
"s_add_i32 s7, s7, 0x4240000\n" \
"v_mov_b32_e64 v219, s40\n" \
"v_mfma_f32_16x16x32_bf16 v[168:171], a[180:183], a[156:159], 0\n" \
"s_movk_i32 s40, 0x3c0\n" \
"s_lshr_b32 s7, s7, 7\n" \
"v_mfma_f32_16x16x32_bf16 v[168:171], a[244:247], a[220:223], v[168:171]\n" \
"v_cmp_gt_u32_e64 vcc, s40, v20\n" \
"s_or_b32 s7, s7, 0x70\n" \
"v_mfma_f32_16x16x32_bf16 v[164:167], a[176:179], a[156:159], 0\n" \
"v_bfe_u32 v216, v20, 4, 1\n" \
"s_add_i32 s5, s5, 0x60080\n" \
"v_mfma_f32_16x16x32_bf16 v[164:167], a[240:243], a[220:223], v[164:167]\n" \
"ds_read_b128 a[156:159], v28 offset:38912\n" \
"ds_read_b128 a[220:223], v29 offset:38912\n" \
"s_waitcnt lgkmcnt(15)\n" \
"v_mfma_f32_16x16x32_bf16 a[0:3], a[160:163], a[128:131], a[0:3]\n" \
"s_addk_i32 s84, 0x7c00\n" \
"v_mfma_f32_16x16x32_bf16 a[0:3], a[224:227], a[192:195], a[0:3]\n" \
"buffer_load_dwordx4 v27, s[72:75], s84 offen offset:2048 lds\n" \
"v_mfma_f32_16x16x32_bf16 a[4:7], a[164:167], a[128:131], a[4:7]\n" \
"ds_read_b64_tr_b16 a[176:177], v36 offset:32768\n" \
"v_cndmask_b32_e32 v219, v220, v219, vcc\n" \
"s_waitcnt lgkmcnt(15)\n" \
"v_mfma_f32_16x16x32_bf16 a[4:7], a[228:231], a[192:195], a[4:7]\n" \
"s_addk_i32 s84, 0x7c00\n" \
"s_waitcnt lgkmcnt(11)\n" \
"v_mfma_f32_16x16x32_bf16 a[8:11], a[168:171], a[128:131], a[8:11]\n" \
"buffer_load_dwordx4 v27, s[72:75], s84 offen offset:3072 lds\n" \
"s_waitcnt lgkmcnt(9)\n" \
"v_mfma_f32_16x16x32_bf16 a[8:11], a[232:235], a[192:195], a[8:11]\n" \
"ds_read_b64_tr_b16 a[178:179], v36 offset:33792\n" \
"s_movk_i32 s40, 0x3b0\n" \
"s_waitcnt lgkmcnt(8)\n" \
"v_mfma_f32_16x16x32_bf16 a[12:15], a[172:175], a[128:131], a[12:15]\n" \
"ds_read_b64_tr_b16 a[240:241], v36 offset:40960\n" \
"v_mov_b32_e32 v220, s7\n" \
"s_waitcnt lgkmcnt(5)\n" \
"v_mfma_f32_16x16x32_bf16 a[12:15], a[236:239], a[192:195], a[12:15]\n" \
"v_or_b32_e32 v218, s5, v216\n" \
"v_or_b32_e64 v216, s4, v216\n" \
"v_mfma_f32_16x16x32_bf16 a[28:31], a[172:175], a[132:135], a[28:31]\n" \
"v_cmp_gt_u32_e32 vcc, s40, v20\n" \
"s_movk_i32 s4, 0x3a0\n" \
"v_mfma_f32_16x16x32_bf16 a[28:31], a[236:239], a[196:199], a[28:31]\n" \
"ds_read_b64_tr_b16 a[242:243], v36 offset:41984\n" \
"v_or_b32_e32 v216, 0xb8000, v216\n" \
"v_mfma_f32_16x16x32_bf16 a[24:27], a[168:171], a[132:135], a[24:27]\n" \
"v_cndmask_b32_e32 v219, v219, v220, vcc\n" \
"v_cmp_gt_u32_e32 vcc, s4, v20\n" \
"v_mfma_f32_16x16x32_bf16 a[24:27], a[232:235], a[196:199], a[24:27]\n" \
"s_movk_i32 s5, 0x380\n" \
"v_mov_b32_e32 v215, s9\n" \
"v_mfma_f32_16x16x32_bf16 a[20:23], a[164:167], a[132:135], a[20:23]\n" \
"v_cndmask_b32_e32 v216, v219, v216, vcc\n" \
"v_mov_b32_e32 v217, s27\n" \
"v_mfma_f32_16x16x32_bf16 a[20:23], a[228:231], a[196:199], a[20:23]\n" \
"ds_read_b64_tr_b16 a[180:181], v37 offset:32768\n" \
"v_cmp_gt_u32_e64 vcc, s5, v20\n" \
"v_mfma_f32_16x16x32_bf16 a[16:19], a[160:163], a[132:135], a[16:19]\n" \
"s_movk_i32 s5, 0x360\n" \
"v_mov_b32_e32 v213, s13\n" \
"v_mfma_f32_16x16x32_bf16 a[16:19], a[224:227], a[196:199], a[16:19]\n" \
"v_cndmask_b32_e32 v218, v216, v218, vcc\n" \
"v_cmp_gt_u32_e32 vcc, s5, v20\n" \
"v_mfma_f32_16x16x32_bf16 a[32:35], a[160:163], a[136:139], a[32:35]\n" \
"s_mov_b32 m0, s32\n" \
"ds_read_b64_tr_b16 a[182:183], v37 offset:33792\n" \
"v_mfma_f32_16x16x32_bf16 a[32:35], a[224:227], a[200:203], a[32:35]\n" \
"s_mov_b32 exec_lo, 0x0\n" \
"s_mov_b32 exec_hi, 0xff\n" \
"global_load_lds_dword v[184:185], off\n" \
"s_mov_b64 exec, -1\n" \
"v_mfma_f32_16x16x32_bf16 a[36:39], a[164:167], a[136:139], a[36:39]\n" \
"s_movk_i32 s4, 0x240\n" \
"v_cmp_gt_u32_e64 s[4:5], s4, v20\n" \
"v_mfma_f32_16x16x32_bf16 a[36:39], a[228:231], a[200:203], a[36:39]\n" \
"ds_read_b64_tr_b16 a[244:245], v37 offset:40960\n" \
"v_cndmask_b32_e64 v215, v217, v215, vcc\n" \
"v_mfma_f32_16x16x32_bf16 a[40:43], a[168:171], a[136:139], a[40:43]\n" \
"v_mov_b32_e64 v219, s8\n" \
"v_cndmask_b32_e64 v217, v215, v213, s[4:5]\n" \
"v_mfma_f32_16x16x32_bf16 a[40:43], a[232:235], a[200:203], a[40:43]\n" \
"s_mov_b32 m0, s32\n" \
"v_and_b32_e32 v213, 0xffffff8, v214\n" \
"v_mfma_f32_16x16x32_bf16 a[44:47], a[172:175], a[136:139], a[44:47]\n" \
"s_mov_b32 exec_lo, 0xffff0000\n" \
"s_mov_b32 exec_hi, 0x0\n" \
"global_load_lds_dword v[186:187], off\n" \
"s_mov_b64 exec, -1\n" \
"v_mfma_f32_16x16x32_bf16 a[44:47], a[236:239], a[200:203], a[44:47]\n" \
"ds_read_b64_tr_b16 a[246:247], v37 offset:41984\n" \
"v_or_b32_e32 v213, s6, v213\n" \
"v_mfma_f32_16x16x32_bf16 a[60:63], a[172:175], a[140:143], a[60:63]\n" \
"v_and_b32_e64 v214, 7, v214\n" \
"v_lshl_or_b32 v214, v213, 3, v214\n" \
"v_mfma_f32_16x16x32_bf16 a[60:63], a[236:239], a[204:207], a[60:63]\n" \
"v_mad_u64_u32 v[212:213], s[6:7], s43, 18, v[212:213]\n" \
"v_subrev_u32_e64 v212, 36, v212\n" \
"v_mfma_f32_16x16x32_bf16 a[56:59], a[168:171], a[140:143], a[56:59]\n" \
"ds_read_b64_tr_b16 a[184:185], v38 offset:32768\n" \
"v_mov_b32_e64 v220, s26\n" \
"v_mfma_f32_16x16x32_bf16 a[56:59], a[232:235], a[204:207], a[56:59]\n" \
"v_mov_b32_e32 v216, s12\n" \
"v_cndmask_b32_e32 v212, v218, v212, vcc\n" \
"v_mfma_f32_16x16x32_bf16 a[52:55], a[164:167], a[140:143], a[52:55]\n" \
"v_cndmask_b32_e32 v219, v220, v219, vcc\n" \
"v_mov_b32_e32 v213, 0\n" \
"v_mfma_f32_16x16x32_bf16 a[52:55], a[228:231], a[204:207], a[52:55]\n" \
"ds_read_b64_tr_b16 a[186:187], v38 offset:33792\n" \
"v_cndmask_b32_e64 v212, v212, v214, s[4:5]\n" \
"v_mfma_f32_16x16x32_bf16 a[48:51], a[160:163], a[140:143], a[48:51]\n" \
"v_cndmask_b32_e64 v216, v219, v216, s[4:5]\n" \
"v_lshlrev_b32_e64 v212, 7, v212\n" \
"v_mfma_f32_16x16x32_bf16 a[48:51], a[224:227], a[204:207], a[48:51]\n" \
"v_lshl_add_u64 v[188:189], v[216:217], 0, v[212:213]\n" \
"v_mov_b32_e32 v215, 0xffff\n" \
"v_mfma_f32_16x16x32_bf16 a[64:67], a[160:163], a[144:147], a[64:67]\n" \
"ds_read_b64_tr_b16 a[248:249], v38 offset:40960\n" \
"s_and_b32 s4, s3, 1\n" \
"v_mfma_f32_16x16x32_bf16 a[64:67], a[224:227], a[208:211], a[64:67]\n" \
"s_lshl_b32 s5, s4, 4\n" \
"v_lshrrev_b32_e64 v212, 4, v20\n" \
"v_mfma_f32_16x16x32_bf16 a[68:71], a[164:167], a[144:147], a[68:71]\n" \
"v_lshrrev_b32_e32 v218, 6, v20\n" \
"v_and_b32_e32 v219, 12, v218\n" \
"v_mfma_f32_16x16x32_bf16 a[68:71], a[228:231], a[208:211], a[68:71]\n" \
"ds_read_b64_tr_b16 a[250:251], v38 offset:41984\n" \
"v_and_or_b32 v214, v212, 15, s5\n" \
"v_mfma_f32_16x16x32_bf16 a[72:75], a[168:171], a[144:147], a[72:75]\n" \
"v_cmp_gt_u32_e64 vcc, v219, v214\n" \
"v_mov_b32_e32 v220, 0xffff0000\n" \
"v_mfma_f32_16x16x32_bf16 a[72:75], a[232:235], a[208:211], a[72:75]\n" \
"s_lshl_b32 s5, s2, 2\n" \
"v_cndmask_b32_e64 v217, v215, 0, vcc\n" \
"v_mfma_f32_16x16x32_bf16 a[76:79], a[172:175], a[144:147], a[76:79]\n" \
"v_cmp_lt_u32_e32 vcc, v219, v214\n" \
"v_mov_b32_e64 v213, s23\n" \
"v_mfma_f32_16x16x32_bf16 a[76:79], a[236:239], a[208:211], a[76:79]\n" \
"ds_read_b64_tr_b16 a[188:189], v39 offset:32768\n" \
"s_or_b32 s5, s5, s3\n" \
"v_mfma_f32_16x16x32_bf16 a[92:95], a[172:175], a[148:151], a[92:95]\n" \
"v_cndmask_b32_e32 v221, 0, v220, vcc\n" \
"v_or_b32_e64 v12, v217, v221\n" \
"v_mfma_f32_16x16x32_bf16 a[92:95], a[236:239], a[212:215], a[92:95]\n" \
"v_or_b32_e32 v217, 2, v219\n" \
"v_cmp_gt_u32_e32 vcc, v217, v214\n" \
"v_mfma_f32_16x16x32_bf16 a[88:91], a[168:171], a[148:151], a[88:91]\n" \
"ds_read_b64_tr_b16 a[190:191], v39 offset:33792\n" \
"v_mov_b32_e64 v216, s25\n" \
"v_mfma_f32_16x16x32_bf16 a[88:91], a[232:235], a[212:215], a[88:91]\n" \
"s_movk_i32 s40, 0xf400\n" \
"v_cndmask_b32_e64 v221, v215, 0, vcc\n" \
"v_mfma_f32_16x16x32_bf16 a[84:87], a[164:167], a[148:151], a[84:87]\n" \
"v_cmp_lt_u32_e32 vcc, v217, v214\n" \
"s_lshl_b32 s4, s4, 6\n" \
"v_mfma_f32_16x16x32_bf16 a[84:87], a[228:231], a[212:215], a[84:87]\n" \
"ds_read_b64_tr_b16 a[252:253], v39 offset:40960\n" \
"v_cndmask_b32_e32 v217, 0, v220, vcc\n" \
"v_mfma_f32_16x16x32_bf16 a[80:83], a[160:163], a[148:151], a[80:83]\n" \
"v_or_b32_e32 v13, v221, v217\n" \
"v_or_b32_e32 v217, 16, v219\n" \
"v_mfma_f32_16x16x32_bf16 a[80:83], a[224:227], a[212:215], a[80:83]\n" \
"v_cmp_gt_u32_e32 vcc, v217, v214\n" \
"s_nop 1\n" \
"v_cndmask_b32_e64 v221, v215, 0, vcc\n" \
"v_mfma_f32_16x16x32_bf16 a[96:99], a[160:163], a[152:155], a[96:99]\n" \
"ds_read_b64_tr_b16 a[254:255], v39 offset:41984\n" \
"v_cmp_lt_u32_e64 vcc, v217, v214\n" \
"v_mfma_f32_16x16x32_bf16 a[96:99], a[224:227], a[216:219], a[96:99]\n" \
"s_nop 0\n" \
"v_cndmask_b32_e32 v217, 0, v220, vcc\n" \
"v_or_b32_e64 v14, v221, v217\n" \
"v_mfma_f32_16x16x32_bf16 a[100:103], a[164:167], a[152:155], a[100:103]\n" \
"v_or_b32_e32 v217, 18, v219\n" \
"v_cmp_gt_u32_e32 vcc, v217, v214\n" \
"v_mfma_f32_16x16x32_bf16 a[100:103], a[228:231], a[216:219], a[100:103]\n" \
"s_nop 0\n" \
"v_cndmask_b32_e64 v215, v215, 0, vcc\n" \
"v_cmp_lt_u32_e32 vcc, v217, v214\n" \
"v_mfma_f32_16x16x32_bf16 a[104:107], a[168:171], a[152:155], a[104:107]\n" \
"s_nop 0\n" \
"v_cndmask_b32_e32 v214, 0, v220, vcc\n" \
"v_or_b32_e64 v15, v215, v214\n" \
"v_mfma_f32_16x16x32_bf16 a[104:107], a[232:235], a[216:219], a[104:107]\n" \
"v_mad_u64_u32 v[214:215], s[6:7], s5, 42, v[212:213]\n" \
"s_movk_i32 s5, 0x1440\n" \
"v_mfma_f32_16x16x32_bf16 a[108:111], a[172:175], a[152:155], a[108:111]\n" \
"v_cmp_gt_u32_e32 vcc, s5, v214\n" \
"v_min_u32_e32 v217, 0xa43f, v214\n" \
"v_mfma_f32_16x16x32_bf16 a[108:111], a[236:239], a[216:219], a[108:111]\n" \
"v_mov_b32_e32 v214, s24\n" \
"v_cndmask_b32_e32 v215, v216, v213, vcc\n" \
"s_waitcnt lgkmcnt(15)\n" \
"v_mfma_f32_16x16x32_bf16 a[124:127], a[172:175], a[156:159], a[124:127]\n" \
"v_mov_b32_e32 v213, s22\n" \
"s_movk_i32 s6, 0xffee\n" \
"v_mfma_f32_16x16x32_bf16 a[124:127], a[236:239], a[220:223], a[124:127]\n" \
"v_cndmask_b32_e32 v214, v214, v213, vcc\n" \
"v_add_u32_e32 v213, 0xffffebc0, v217\n" \
"v_mfma_f32_16x16x32_bf16 a[120:123], a[168:171], a[156:159], a[120:123]\n" \
"s_lshl_b32 s5, s2, 5\n" \
"v_cndmask_b32_e32 v213, v213, v217, vcc\n" \
"v_mfma_f32_16x16x32_bf16 a[120:123], a[232:235], a[220:223], a[120:123]\n" \
"v_lshlrev_b32_e64 v216, 7, v213\n" \
"v_mul_u32_u24_e32 v213, 0xe39, v212\n" \
"v_mfma_f32_16x16x32_bf16 a[116:119], a[164:167], a[156:159], a[116:119]\n" \
"v_mov_b32_e32 v217, 0\n" \
"v_lshrrev_b32_e32 v213, 16, v213\n" \
"v_mfma_f32_16x16x32_bf16 a[116:119], a[228:231], a[220:223], a[116:119]\n" \
"v_lshl_add_u64 v[16:17], v[214:215], 0, v[216:217]\n" \
"v_mad_i32_i24 v214, v213, s6, v212\n" \
"v_mfma_f32_16x16x32_bf16 a[112:115], a[160:163], a[156:159], a[112:115]\n" \
"s_add_i32 s5, s5, 32\n" \
"v_lshlrev_b32_e32 v215, 4, v214\n" \
"v_mfma_f32_16x16x32_bf16 a[112:115], a[224:227], a[220:223], a[112:115]\n" \
"s_waitcnt vmcnt(20)\n" \
"v_readfirstlane_b32 s62, v180\n" \
"s_nop 3\n" \
"s_add_u32 s62, s62, 0x9E3779B1\n" \
"s_waitcnt vmcnt(2) lgkmcnt(0)\n" \
"s_barrier\n" \
"v_mfma_f32_16x16x32_bf16 v[52:55], a[176:179], a[128:131], v[52:55]\n" \
"s_add_u32 m0, s30, 0x8000\n" \
"s_movk_i32 s84, 0x180\n" \
"v_mfma_f32_16x16x32_bf16 v[52:55], a[240:243], a[192:195], v[52:55]\n" \
"buffer_load_dwordx4 v22, s[64:67], s84 offen offset:0 lds\n" \
"v_mfma_f32_16x16x32_bf16 v[56:59], a[180:183], a[128:131], v[56:59]\n" \
"ds_read_b64_tr_b16 a[160:161], v32 offset:0\n" \
"v_add_u32_e64 v216, s5, v215\n" \
"v_mfma_f32_16x16x32_bf16 v[56:59], a[244:247], a[192:195], v[56:59]\n" \
"s_movk_i32 s84, 0x1d80\n" \
"v_mfma_f32_16x16x32_bf16 v[60:63], a[184:187], a[128:131], v[60:63]\n" \
"buffer_load_dwordx4 v23, s[64:67], s84 offen offset:1024 lds\n" \
"v_mfma_f32_16x16x32_bf16 v[60:63], a[248:251], a[192:195], v[60:63]\n" \
"ds_read_b64_tr_b16 a[162:163], v32 offset:1024\n" \
"v_and_b32_e32 v216, 0xf0, v216\n" \
"v_mfma_f32_16x16x32_bf16 v[64:67], a[188:191], a[128:131], v[64:67]\n" \
"ds_read_b64_tr_b16 a[224:225], v32 offset:8192\n" \
"v_cmp_gt_u32_e32 vcc, 16, v214\n" \
"v_mfma_f32_16x16x32_bf16 v[64:67], a[252:255], a[192:195], v[64:67]\n" \
"ds_read_b128 a[128:131], v28 offset:16384\n" \
"s_movk_i32 s7, 0xf800\n" \
"v_mfma_f32_16x16x32_bf16 v[80:83], a[188:191], a[132:135], v[80:83]\n" \
"ds_read_b128 a[192:195], v29 offset:16384\n" \
"v_cndmask_b32_e32 v214, v215, v216, vcc\n" \
"v_mfma_f32_16x16x32_bf16 v[80:83], a[252:255], a[196:199], v[80:83]\n" \
"s_movk_i32 s84, 0x3980\n" \
"v_mfma_f32_16x16x32_bf16 v[76:79], a[184:187], a[132:135], v[76:79]\n" \
"buffer_load_dwordx4 v22, s[64:67], s84 offen offset:2048 lds\n" \
"v_mfma_f32_16x16x32_bf16 v[76:79], a[248:251], a[196:199], v[76:79]\n" \
"s_movk_i32 s84, 0x5580\n" \
"v_mfma_f32_16x16x32_bf16 v[72:75], a[180:183], a[132:135], v[72:75]\n" \
"buffer_load_dwordx4 v23, s[64:67], s84 offen offset:3072 lds\n" \
"v_mfma_f32_16x16x32_bf16 v[72:75], a[244:247], a[196:199], v[72:75]\n" \
"s_add_u32 m0, s31, 0x8000\n" \
"s_mov_b32 s84, 0x180000\n" \
"v_mfma_f32_16x16x32_bf16 v[68:71], a[176:179], a[132:135], v[68:71]\n" \
"buffer_load_dwordx4 v26, s[68:71], s84 offen offset:0 lds\n" \
"v_mfma_f32_16x16x32_bf16 v[68:71], a[240:243], a[196:199], v[68:71]\n" \
"ds_read_b128 a[132:135], v28 offset:18432\n" \
"v_or_b32_e64 v215, 64, v212\n" \
"v_mfma_f32_16x16x32_bf16 v[84:87], a[176:179], a[136:139], v[84:87]\n" \
"ds_read_b128 a[196:199], v29 offset:18432\n" \
"v_mul_u32_u24_e32 v216, 0xe39, v215\n" \
"v_mfma_f32_16x16x32_bf16 v[84:87], a[240:243], a[200:203], v[84:87]\n" \
"s_addk_i32 s84, 0x7c00\n" \
"v_mfma_f32_16x16x32_bf16 v[88:91], a[180:183], a[136:139], v[88:91]\n" \
"buffer_load_dwordx4 v26, s[68:71], s84 offen offset:1024 lds\n" \
"v_mfma_f32_16x16x32_bf16 v[88:91], a[244:247], a[200:203], v[88:91]\n" \
"ds_read_b64_tr_b16 a[226:227], v32 offset:9216\n" \
"v_lshrrev_b32_e32 v216, 16, v216\n" \
"v_mfma_f32_16x16x32_bf16 v[92:95], a[184:187], a[136:139], v[92:95]\n" \
"s_addk_i32 s84, 0x7c00\n" \
"v_mfma_f32_16x16x32_bf16 v[92:95], a[248:251], a[200:203], v[92:95]\n" \
"buffer_load_dwordx4 v27, s[68:71], s84 offen offset:2048 lds\n" \
"v_mfma_f32_16x16x32_bf16 v[96:99], a[188:191], a[136:139], v[96:99]\n" \
"ds_read_b64_tr_b16 a[164:165], v33 offset:0\n" \
"v_mad_i32_i24 v215, v216, s6, v215\n" \
"v_mfma_f32_16x16x32_bf16 v[96:99], a[252:255], a[200:203], v[96:99]\n" \
"ds_read_b128 a[136:139], v28 offset:20480\n" \
"v_lshlrev_b32_e32 v217, 4, v215\n" \
"v_mfma_f32_16x16x32_bf16 v[112:115], a[188:191], a[140:143], v[112:115]\n" \
"ds_read_b128 a[200:203], v29 offset:20480\n" \
"v_add_u32_e64 v220, s5, v217\n" \
"v_mfma_f32_16x16x32_bf16 v[112:115], a[252:255], a[204:207], v[112:115]\n" \
"s_addk_i32 s84, 0x7c00\n" \
"s_nop 0\n" \
"v_mfma_f32_16x16x32_bf16 v[108:111], a[184:187], a[140:143], v[108:111]\n" \
"buffer_load_dwordx4 v27, s[68:71], s84 offen offset:3072 lds\n" \
"v_mfma_f32_16x16x32_bf16 v[108:111], a[248:251], a[204:207], v[108:111]\n" \
"s_add_u32 m0, s30, 0x9000\n" \
"s_mov_b32 s84, 0x8180\n" \
"v_mfma_f32_16x16x32_bf16 v[104:107], a[180:183], a[140:143], v[104:107]\n" \
"buffer_load_dwordx4 v22, s[64:67], s84 offen offset:0 lds\n" \
"v_mfma_f32_16x16x32_bf16 v[104:107], a[244:247], a[204:207], v[104:107]\n" \
"ds_read_b64_tr_b16 a[166:167], v33 offset:1024\n" \
"v_and_b32_e32 v220, 0xf0, v220\n" \
"v_mfma_f32_16x16x32_bf16 v[100:103], a[176:179], a[140:143], v[100:103]\n" \
"ds_read_b64_tr_b16 a[228:229], v33 offset:8192\n" \
"v_cmp_gt_u32_e64 vcc, 16, v215\n" \
"v_mfma_f32_16x16x32_bf16 v[100:103], a[240:243], a[204:207], v[100:103]\n" \
"ds_read_b128 a[140:143], v28 offset:22528\n" \
"v_cndmask_b32_e64 v215, v217, v220, vcc\n" \
"v_mfma_f32_16x16x32_bf16 v[116:119], a[176:179], a[144:147], v[116:119]\n" \
"ds_read_b128 a[204:207], v29 offset:22528\n" \
"v_or_b32_e32 v217, 0x80, v212\n" \
"v_mfma_f32_16x16x32_bf16 v[116:119], a[240:243], a[208:211], v[116:119]\n" \
"s_addk_i32 s84, 0x1c00\n" \
"v_mfma_f32_16x16x32_bf16 v[120:123], a[180:183], a[144:147], v[120:123]\n" \
"buffer_load_dwordx4 v23, s[64:67], s84 offen offset:1024 lds\n" \
"v_mfma_f32_16x16x32_bf16 v[120:123], a[244:247], a[208:211], v[120:123]\n" \
"s_addk_i32 s84, 0x1c00\n" \
"v_mfma_f32_16x16x32_bf16 v[124:127], a[184:187], a[144:147], v[124:127]\n" \
"buffer_load_dwordx4 v22, s[64:67], s84 offen offset:2048 lds\n" \
"v_mfma_f32_16x16x32_bf16 v[124:127], a[248:251], a[208:211], v[124:127]\n" \
"s_addk_i32 s84, 0x1c00\n" \
"v_mfma_f32_16x16x32_bf16 v[128:131], a[188:191], a[144:147], v[128:131]\n" \
"buffer_load_dwordx4 v23, s[64:67], s84 offen offset:3072 lds\n" \
"v_mfma_f32_16x16x32_bf16 v[128:131], a[252:255], a[208:211], v[128:131]\n" \
"ds_read_b128 a[144:147], v28 offset:0\n" \
"v_mul_u32_u24_e32 v220, 0xe39, v217\n" \
"v_mfma_f32_16x16x32_bf16 v[144:147], a[188:191], a[148:151], v[144:147]\n" \
"ds_read_b128 a[208:211], v29 offset:0\n" \
"v_lshrrev_b32_e32 v220, 16, v220\n" \
"v_mfma_f32_16x16x32_bf16 v[144:147], a[252:255], a[212:215], v[144:147]\n" \
"s_add_u32 m0, s31, 0xc000\n" \
"s_mov_b32 s84, 0x180000\n" \
"v_mfma_f32_16x16x32_bf16 v[140:143], a[184:187], a[148:151], v[140:143]\n" \
"buffer_load_dwordx4 v26, s[72:75], s84 offen offset:0 lds\n" \
"v_mfma_f32_16x16x32_bf16 v[140:143], a[248:251], a[212:215], v[140:143]\n" \
"ds_read_b64_tr_b16 a[230:231], v33 offset:9216\n" \
"v_mad_i32_i24 v217, v220, s6, v217\n" \
"v_mfma_f32_16x16x32_bf16 v[136:139], a[180:183], a[148:151], v[136:139]\n" \
"s_addk_i32 s84, 0x7c00\n" \
"v_mfma_f32_16x16x32_bf16 v[136:139], a[244:247], a[212:215], v[136:139]\n" \
"buffer_load_dwordx4 v26, s[72:75], s84 offen offset:1024 lds\n" \
"v_mfma_f32_16x16x32_bf16 v[132:135], a[176:179], a[148:151], v[132:135]\n" \
"ds_read_b64_tr_b16 a[168:169], v34 offset:0\n" \
"v_lshlrev_b32_e32 v221, 4, v217\n" \
"v_mfma_f32_16x16x32_bf16 v[132:135], a[240:243], a[212:215], v[132:135]\n" \
"ds_read_b128 a[148:151], v28 offset:2048\n" \
"v_add_u32_e64 v222, s5, v221\n" \
"v_mfma_f32_16x16x32_bf16 v[148:151], a[176:179], a[152:155], v[148:151]\n" \
"ds_read_b128 a[212:215], v29 offset:2048\n" \
"v_and_b32_e32 v222, 0xf0, v222\n" \
"v_mfma_f32_16x16x32_bf16 v[148:151], a[240:243], a[216:219], v[148:151]\n" \
"ds_read_b64_tr_b16 a[170:171], v34 offset:1024\n" \
"v_cmp_gt_u32_e64 vcc, 16, v217\n" \
"v_mfma_f32_16x16x32_bf16 v[152:155], a[180:183], a[152:155], v[152:155]\n" \
"ds_read_b64_tr_b16 a[232:233], v34 offset:8192\n" \
"v_cndmask_b32_e64 v217, v221, v222, vcc\n" \
"v_mfma_f32_16x16x32_bf16 v[152:155], a[244:247], a[216:219], v[152:155]\n" \
"ds_read_b64_tr_b16 a[234:235], v34 offset:9216\n" \
"v_or_b32_e32 v221, 0xc0, v212\n" \
"v_mfma_f32_16x16x32_bf16 v[156:159], a[184:187], a[152:155], v[156:159]\n" \
"ds_read_b64_tr_b16 a[172:173], v35 offset:0\n" \
"v_mul_u32_u24_e32 v222, 0xe39, v221\n" \
"v_mfma_f32_16x16x32_bf16 v[156:159], a[248:251], a[216:219], v[156:159]\n" \
"ds_read_b64_tr_b16 a[174:175], v35 offset:1024\n" \
"v_lshrrev_b32_e64 v222, 16, v222\n" \
"v_mfma_f32_16x16x32_bf16 v[160:163], a[188:191], a[152:155], v[160:163]\n" \
"ds_read_b64_tr_b16 a[236:237], v35 offset:8192\n" \
"v_mad_i32_i24 v221, v222, s6, v221\n" \
"v_mfma_f32_16x16x32_bf16 v[160:163], a[252:255], a[216:219], v[160:163]\n" \
"ds_read_b128 a[152:155], v28 offset:4096\n" \
"v_lshlrev_b32_e64 v223, 4, v221\n" \
"v_mfma_f32_16x16x32_bf16 v[176:179], a[188:191], a[156:159], v[176:179]\n" \
"ds_read_b128 a[216:219], v29 offset:4096\n" \
"v_add_u32_e64 v224, s5, v223\n" \
"v_mfma_f32_16x16x32_bf16 v[176:179], a[252:255], a[220:223], v[176:179]\n" \
"ds_read_b64_tr_b16 a[238:239], v35 offset:9216\n" \
"v_and_b32_e32 v224, 0xf0, v224\n" \
"v_mfma_f32_16x16x32_bf16 v[172:175], a[184:187], a[156:159], v[172:175]\n" \
"v_cmp_gt_u32_e32 vcc, 16, v221\n" \
"s_nop 1\n" \
"v_cndmask_b32_e64 v221, v223, v224, vcc\n" \
"v_mfma_f32_16x16x32_bf16 v[172:175], a[248:251], a[220:223], v[172:175]\n" \
"v_or_b32_e32 v223, 0x100, v212\n" \
"v_mul_u32_u24_e32 v224, 0xe39, v223\n" \
"v_mfma_f32_16x16x32_bf16 v[168:171], a[180:183], a[156:159], v[168:171]\n" \
"v_lshrrev_b32_e64 v224, 16, v224\n" \
"v_mad_i32_i24 v223, v224, s6, v223\n" \
"v_mfma_f32_16x16x32_bf16 v[168:171], a[244:247], a[220:223], v[168:171]\n" \
"v_lshlrev_b32_e32 v225, 4, v223\n" \
"v_add_u32_e32 v226, s5, v225\n" \
"v_mfma_f32_16x16x32_bf16 v[164:167], a[176:179], a[156:159], v[164:167]\n" \
"v_and_b32_e32 v226, 0xf0, v226\n" \
"v_cmp_gt_u32_e64 vcc, 16, v223\n" \
"v_mfma_f32_16x16x32_bf16 v[164:167], a[240:243], a[220:223], v[164:167]\n" \
"ds_read_b128 a[156:159], v28 offset:6144\n" \
"ds_read_b128 a[220:223], v29 offset:6144\n" \
"s_waitcnt lgkmcnt(15)\n" \
"v_mfma_f32_16x16x32_bf16 a[0:3], a[160:163], a[128:131], a[0:3]\n" \
"s_addk_i32 s84, 0x7c00\n" \
"v_mfma_f32_16x16x32_bf16 a[0:3], a[224:227], a[192:195], a[0:3]\n" \
"buffer_load_dwordx4 v27, s[72:75], s84 offen offset:2048 lds\n" \
"v_mfma_f32_16x16x32_bf16 a[4:7], a[164:167], a[128:131], a[4:7]\n" \
"ds_read_b64_tr_b16 a[176:177], v36 offset:0\n" \
"v_cndmask_b32_e32 v223, v225, v226, vcc\n" \
"s_waitcnt lgkmcnt(15)\n" \
"v_mfma_f32_16x16x32_bf16 a[4:7], a[228:231], a[192:195], a[4:7]\n" \
"s_addk_i32 s84, 0x7c00\n" \
"s_waitcnt lgkmcnt(11)\n" \
"v_mfma_f32_16x16x32_bf16 a[8:11], a[168:171], a[128:131], a[8:11]\n" \
"buffer_load_dwordx4 v27, s[72:75], s84 offen offset:3072 lds\n" \
"s_waitcnt lgkmcnt(9)\n" \
"v_mfma_f32_16x16x32_bf16 a[8:11], a[232:235], a[192:195], a[8:11]\n" \
"ds_read_b64_tr_b16 a[178:179], v36 offset:1024\n" \
"v_or_b32_e32 v225, 0x140, v212\n" \
"s_waitcnt lgkmcnt(8)\n" \
"v_mfma_f32_16x16x32_bf16 a[12:15], a[172:175], a[128:131], a[12:15]\n" \
"ds_read_b64_tr_b16 a[240:241], v36 offset:8192\n" \
"v_mul_u32_u24_e32 v226, 0xe39, v225\n" \
"s_waitcnt lgkmcnt(5)\n" \
"v_mfma_f32_16x16x32_bf16 a[12:15], a[236:239], a[192:195], a[12:15]\n" \
"v_lshrrev_b32_e32 v226, 16, v226\n" \
"v_mad_i32_i24 v225, v226, s6, v225\n" \
"v_mfma_f32_16x16x32_bf16 a[28:31], a[172:175], a[132:135], a[28:31]\n" \
"v_lshlrev_b32_e32 v227, 4, v225\n" \
"v_add_u32_e32 v228, s5, v227\n" \
"v_mfma_f32_16x16x32_bf16 a[28:31], a[236:239], a[196:199], a[28:31]\n" \
"ds_read_b64_tr_b16 a[242:243], v36 offset:9216\n" \
"v_and_b32_e32 v228, 0xf0, v228\n" \
"v_mfma_f32_16x16x32_bf16 a[24:27], a[168:171], a[132:135], a[24:27]\n" \
"buffer_load_dwordx4 v[200:203], v192, s[76:79], s34 offen offset:0\n" \
"v_mfma_f32_16x16x32_bf16 a[24:27], a[232:235], a[196:199], a[24:27]\n" \
"v_cmp_gt_u32_e32 vcc, 16, v225\n" \
"s_nop 1\n" \
"v_cndmask_b32_e64 v225, v227, v228, vcc\n" \
"v_mfma_f32_16x16x32_bf16 a[20:23], a[164:167], a[132:135], a[20:23]\n" \
"v_or_b32_e32 v227, 0x180, v212\n" \
"v_mul_u32_u24_e32 v228, 0xe39, v227\n" \
"v_mfma_f32_16x16x32_bf16 a[20:23], a[228:231], a[196:199], a[20:23]\n" \
"ds_read_b64_tr_b16 a[180:181], v37 offset:0\n" \
"v_lshrrev_b32_e64 v228, 16, v228\n" \
"v_mfma_f32_16x16x32_bf16 a[16:19], a[160:163], a[132:135], a[16:19]\n" \
"v_mad_i32_i24 v227, v228, s6, v227\n" \
"v_lshlrev_b32_e64 v229, 4, v227\n" \
"v_mfma_f32_16x16x32_bf16 a[16:19], a[224:227], a[196:199], a[16:19]\n" \
"v_add_u32_e64 v230, s5, v229\n" \
"v_and_b32_e32 v230, 0xf0, v230\n" \
"v_mfma_f32_16x16x32_bf16 a[32:35], a[160:163], a[136:139], a[32:35]\n" \
"buffer_load_dwordx4 v[204:207], v193, s[76:79], s34 offen offset:0\n" \
"v_mfma_f32_16x16x32_bf16 a[32:35], a[224:227], a[200:203], a[32:35]\n" \
"s_mov_b32 m0, s32\n" \
"ds_read_b64_tr_b16 a[182:183], v37 offset:1024\n" \
"v_mfma_f32_16x16x32_bf16 a[36:39], a[164:167], a[136:139], a[36:39]\n" \
"s_mov_b32 exec_lo, 0x0\n" \
"s_mov_b32 exec_hi, 0xff00\n" \
"global_load_lds_dword v[184:185], off\n" \
"s_mov_b64 exec, -1\n" \
"v_mfma_f32_16x16x32_bf16 a[36:39], a[228:231], a[200:203], a[36:39]\n" \
"ds_read_b64_tr_b16 a[244:245], v37 offset:8192\n" \
"v_cmp_gt_u32_e32 vcc, 16, v227\n" \
"v_mfma_f32_16x16x32_bf16 a[40:43], a[168:171], a[136:139], a[40:43]\n" \
"s_nop 0\n" \
"v_cndmask_b32_e32 v227, v229, v230, vcc\n" \
"v_or_b32_e32 v229, 0x1c0, v212\n" \
"v_mfma_f32_16x16x32_bf16 a[40:43], a[232:235], a[200:203], a[40:43]\n" \
"buffer_load_dword v208, v194, s[76:79], s34 offen offset:32\n" \
"v_mfma_f32_16x16x32_bf16 a[44:47], a[172:175], a[136:139], a[44:47]\n" \
"ds_read_b64_tr_b16 a[246:247], v37 offset:9216\n" \
"v_mul_u32_u24_e32 v230, 0xe39, v229\n" \
"v_mfma_f32_16x16x32_bf16 a[44:47], a[236:239], a[200:203], a[44:47]\n" \
"v_lshrrev_b32_e64 v230, 16, v230\n" \
"v_mad_i32_i24 v229, v230, s6, v229\n" \
"v_mfma_f32_16x16x32_bf16 a[60:63], a[172:175], a[140:143], a[60:63]\n" \
"v_lshlrev_b32_e32 v231, 4, v229\n" \
"v_add_u32_e32 v232, s5, v231\n" \
"v_mfma_f32_16x16x32_bf16 a[60:63], a[236:239], a[204:207], a[60:63]\n" \
"v_and_b32_e32 v232, 0xf0, v232\n" \
"v_cmp_gt_u32_e64 vcc, 16, v229\n" \
"v_mfma_f32_16x16x32_bf16 a[56:59], a[168:171], a[140:143], a[56:59]\n" \
"buffer_load_dword v209, v194, s[76:79], s34 offen offset:1184\n" \
"v_mfma_f32_16x16x32_bf16 a[56:59], a[232:235], a[204:207], a[56:59]\n" \
"ds_read_b64_tr_b16 a[184:185], v38 offset:0\n" \
"v_cndmask_b32_e64 v229, v231, v232, vcc\n" \
"v_mfma_f32_16x16x32_bf16 a[52:55], a[164:167], a[140:143], a[52:55]\n" \
"v_or_b32_e32 v231, 0x200, v212\n" \
"v_mul_u32_u24_e32 v232, 0xe39, v231\n" \
"v_mfma_f32_16x16x32_bf16 a[52:55], a[228:231], a[204:207], a[52:55]\n" \
"ds_read_b64_tr_b16 a[186:187], v38 offset:1024\n" \
"v_lshrrev_b32_e64 v232, 16, v232\n" \
"v_mfma_f32_16x16x32_bf16 a[48:51], a[160:163], a[140:143], a[48:51]\n" \
"v_mad_i32_i24 v231, v232, s6, v231\n" \
"v_lshlrev_b32_e64 v233, 4, v231\n" \
"v_mfma_f32_16x16x32_bf16 a[48:51], a[224:227], a[204:207], a[48:51]\n" \
"v_add_u32_e32 v234, s5, v233\n" \
"s_movk_i32 s5, 0x480\n" \
"v_mfma_f32_16x16x32_bf16 a[64:67], a[160:163], a[144:147], a[64:67]\n" \
"buffer_load_dword v211, v194, s[76:79], s34 offen offset:2336\n" \
"v_mfma_f32_16x16x32_bf16 a[64:67], a[224:227], a[208:211], a[64:67]\n" \
"ds_read_b64_tr_b16 a[248:249], v38 offset:8192\n" \
"v_mad_u32_u24 v0, v213, s5, v214\n" \
"v_mfma_f32_16x16x32_bf16 a[68:71], a[164:167], a[144:147], a[68:71]\n" \
"v_mul_u32_u24_e32 v213, 0x480, v216\n" \
"s_movk_i32 s6, 0xfc00\n" \
"v_mfma_f32_16x16x32_bf16 a[68:71], a[228:231], a[208:211], a[68:71]\n" \
"ds_read_b64_tr_b16 a[250:251], v38 offset:9216\n" \
"v_add3_u32 v1, v213, v215, s6\n" \
"v_mfma_f32_16x16x32_bf16 a[72:75], a[168:171], a[144:147], a[72:75]\n" \
"v_mul_u32_u24_e32 v213, 0x480, v220\n" \
"v_add3_u32 v2, v213, v217, s7\n" \
"v_mfma_f32_16x16x32_bf16 a[72:75], a[232:235], a[208:211], a[72:75]\n" \
"s_mov_b32 s76, s20\n" \
"s_and_b32 s77, s21, 0xffff\n" \
"v_mfma_f32_16x16x32_bf16 a[76:79], a[172:175], a[144:147], a[76:79]\n" \
"s_mov_b32 s78, 0x480\n" \
"v_mul_u32_u24_e32 v213, 0x480, v222\n" \
"v_mfma_f32_16x16x32_bf16 a[76:79], a[236:239], a[208:211], a[76:79]\n" \
"buffer_load_ushort v210, v195, s[76:79], 0 offen\n" \
"v_mfma_f32_16x16x32_bf16 a[92:95], a[172:175], a[148:151], a[92:95]\n" \
"ds_read_b64_tr_b16 a[188:189], v39 offset:0\n" \
"v_add3_u32 v3, v213, v221, s40\n" \
"v_mfma_f32_16x16x32_bf16 a[92:95], a[236:239], a[212:215], a[92:95]\n" \
"v_mul_u32_u24_e32 v213, 0x480, v226\n" \
"v_add3_u32 v5, v213, v225, s6\n" \
"v_mfma_f32_16x16x32_bf16 a[88:91], a[168:171], a[148:151], a[88:91]\n" \
"s_mov_b32 s76, s8\n" \
"s_nop 0\n" \
"s_and_b32 s77, s9, 0xffff\n" \
"v_mfma_f32_16x16x32_bf16 a[88:91], a[232:235], a[212:215], a[88:91]\n" \
"s_mov_b32 s78, 0x240000\n" \
"ds_read_b64_tr_b16 a[190:191], v39 offset:1024\n" \
"v_mfma_f32_16x16x32_bf16 a[84:87], a[164:167], a[148:151], a[84:87]\n" \
"v_mul_u32_u24_e32 v213, 0x480, v228\n" \
"v_add3_u32 v6, v213, v227, s7\n" \
"v_mfma_f32_16x16x32_bf16 a[84:87], a[228:231], a[212:215], a[84:87]\n" \
"ds_read_b64_tr_b16 a[252:253], v39 offset:8192\n" \
"v_mul_u32_u24_e32 v213, 0x480, v230\n" \
"v_mfma_f32_16x16x32_bf16 a[80:83], a[160:163], a[148:151], a[80:83]\n" \
"v_add3_u32 v7, v213, v229, s40\n" \
"s_lshl_b32 s7, s3, 3\n" \
"v_mfma_f32_16x16x32_bf16 a[80:83], a[224:227], a[212:215], a[80:83]\n" \
"s_lshl_b32 s40, s3, 1\n" \
"s_and_b32 s7, s7, 16\n" \
"v_mfma_f32_16x16x32_bf16 a[96:99], a[160:163], a[152:155], a[96:99]\n" \
"ds_read_b64_tr_b16 a[254:255], v39 offset:9216\n" \
"s_and_b32 s40, s40, 2\n" \
"v_mfma_f32_16x16x32_bf16 a[96:99], a[224:227], a[216:219], a[96:99]\n" \
"s_and_b32 s6, s2, 7\n" \
"v_bfe_u32 v213, v20, 7, 1\n" \
"v_mfma_f32_16x16x32_bf16 a[100:103], a[164:167], a[152:155], a[100:103]\n" \
"s_or_b32 s7, s40, s7\n" \
"v_and_b32_e32 v234, 0xf0, v234\n" \
"v_mfma_f32_16x16x32_bf16 a[100:103], a[228:231], a[216:219], a[100:103]\n" \
".fill (L_pb + 7904 - .) / 4, 4, 0xBF800000\n" \
"L_pp2:\n" \
"s_cbranch_execz L_pret\n" \
"v_cmp_gt_u32_e32 vcc, 16, v231\n" \
"v_or3_b32 v213, s7, v213, v219\n" \
"v_mfma_f32_16x16x32_bf16 a[104:107], a[168:171], a[152:155], a[104:107]\n" \
"v_and_b32_e32 v214, 32, v20\n" \
"s_lshl_b32 s6, s6, 5\n" \
"v_mfma_f32_16x16x32_bf16 a[104:107], a[232:235], a[216:219], a[104:107]\n" \
"v_cndmask_b32_e32 v231, v233, v234, vcc\n" \
"v_mul_u32_u24_e32 v213, 18, v213\n" \
"v_mfma_f32_16x16x32_bf16 a[108:111], a[172:175], a[152:155], a[108:111]\n" \
"v_mov_b32_e32 v215, 0x100\n" \
"v_mov_b32_e64 v216, s6\n" \
"v_mfma_f32_16x16x32_bf16 a[108:111], a[236:239], a[216:219], a[108:111]\n" \
"v_cmp_eq_u32_e64 vcc, 0, v214\n" \
"v_and_or_b32 v213, v218, 1, v213\n" \
"s_waitcnt lgkmcnt(15)\n" \
"v_mfma_f32_16x16x32_bf16 a[124:127], a[172:175], a[156:159], a[124:127]\n" \
"v_lshlrev_b32_e32 v212, 3, v212\n" \
"v_cndmask_b32_e64 v214, v215, v216, vcc\n" \
"v_mfma_f32_16x16x32_bf16 a[124:127], a[236:239], a[220:223], a[124:127]\n" \
"v_mad_u32_u24 v213, v213, s5, v214\n" \
"v_and_or_b32 v213, v20, 16, v213\n" \
"v_mfma_f32_16x16x32_bf16 a[120:123], a[168:171], a[156:159], a[120:123]\n" \
"v_add_u32_e32 v9, 0x4800, v213\n" \
"v_mul_u32_u24_e32 v213, 0x120, v218\n" \
"v_mfma_f32_16x16x32_bf16 a[120:123], a[232:235], a[220:223], a[120:123]\n" \
"v_and_or_b32 v10, v212, 24, v213\n" \
"v_lshrrev_b32_e64 v213, 2, v20\n" \
"v_mfma_f32_16x16x32_bf16 a[116:119], a[164:167], a[156:159], a[116:119]\n" \
"v_and_or_b32 v213, v213, 60, s4\n" \
"v_lshl_or_b32 v212, s3, 9, v212\n" \
"v_mfma_f32_16x16x32_bf16 a[116:119], a[228:231], a[220:223], a[116:119]\n" \
"v_mad_u32_u24 v4, v224, s5, v223\n" \
"v_mad_u32_u24 v8, v232, s5, v231\n" \
"v_mfma_f32_16x16x32_bf16 a[112:115], a[160:163], a[156:159], a[112:115]\n" \
"v_or_b32_e32 v18, 0x27000, v213\n" \
"v_or_b32_e32 v19, 0x27800, v212\n" \
"v_mfma_f32_16x16x32_bf16 a[112:115], a[224:227], a[220:223], a[112:115]\n" \
"s_waitcnt vmcnt(7) lgkmcnt(0)\n" \
"s_barrier\n" \
"v_mfma_f32_16x16x32_bf16 v[52:55], a[176:179], a[128:131], v[52:55]\n" \
"s_add_u32 m0, s30, 0x0\n" \
"s_movk_i32 s84, 0x200\n" \
"v_mfma_f32_16x16x32_bf16 v[52:55], a[240:243], a[192:195], v[52:55]\n" \
"buffer_load_dwordx4 v22, s[64:67], s84 offen offset:0 lds\n" \
"v_mfma_f32_16x16x32_bf16 v[56:59], a[180:183], a[128:131], v[56:59]\n" \
"ds_read_b64_tr_b16 a[160:161], v32 offset:32768\n" \
"v_mov_b32_e32 v215, 0x1000c0c\n" \
"v_mfma_f32_16x16x32_bf16 v[56:59], a[244:247], a[192:195], v[56:59]\n" \
"s_movk_i32 s84, 0x1e00\n" \
"v_mfma_f32_16x16x32_bf16 v[60:63], a[184:187], a[128:131], v[60:63]\n" \
"buffer_load_dwordx4 v23, s[64:67], s84 offen offset:1024 lds\n" \
"v_mfma_f32_16x16x32_bf16 v[60:63], a[248:251], a[192:195], v[60:63]\n" \
"ds_read_b64_tr_b16 a[162:163], v32 offset:33792\n" \
"v_mov_b32_e32 v216, 0xc0c0100\n" \
"v_mfma_f32_16x16x32_bf16 v[64:67], a[188:191], a[128:131], v[64:67]\n" \
"ds_read_b64_tr_b16 a[224:225], v32 offset:40960\n" \
"v_bfe_u32 v214, v20, 4, 1\n" \
"v_mfma_f32_16x16x32_bf16 v[64:67], a[252:255], a[192:195], v[64:67]\n" \
"ds_read_b128 a[128:131], v28 offset:49152\n" \
"v_lshrrev_b32_e32 v212, 8, v20\n" \
"v_mfma_f32_16x16x32_bf16 v[80:83], a[188:191], a[132:135], v[80:83]\n" \
"ds_read_b128 a[192:195], v29 offset:49152\n" \
"v_cmp_eq_u32_e64 vcc, 0, v214\n" \
"v_mfma_f32_16x16x32_bf16 v[80:83], a[252:255], a[196:199], v[80:83]\n" \
"s_movk_i32 s84, 0x3a00\n" \
"v_mfma_f32_16x16x32_bf16 v[76:79], a[184:187], a[132:135], v[76:79]\n" \
"buffer_load_dwordx4 v22, s[64:67], s84 offen offset:2048 lds\n" \
"v_mfma_f32_16x16x32_bf16 v[76:79], a[248:251], a[196:199], v[76:79]\n" \
"s_movk_i32 s84, 0x5600\n" \
"v_mfma_f32_16x16x32_bf16 v[72:75], a[180:183], a[132:135], v[72:75]\n" \
"buffer_load_dwordx4 v23, s[64:67], s84 offen offset:3072 lds\n" \
"v_mfma_f32_16x16x32_bf16 v[72:75], a[244:247], a[196:199], v[72:75]\n" \
"s_add_u32 m0, s31, 0x0\n" \
"s_mov_b32 s84, 0x200000\n" \
"v_mfma_f32_16x16x32_bf16 v[68:71], a[176:179], a[132:135], v[68:71]\n" \
"buffer_load_dwordx4 v26, s[68:71], s84 offen offset:0 lds\n" \
"v_mfma_f32_16x16x32_bf16 v[68:71], a[240:243], a[196:199], v[68:71]\n" \
"ds_read_b128 a[132:135], v28 offset:51200\n" \
"v_bfe_u32 v213, v20, 4, 4\n" \
"v_mfma_f32_16x16x32_bf16 v[84:87], a[176:179], a[136:139], v[84:87]\n" \
"ds_read_b128 a[196:199], v29 offset:51200\n" \
"v_mov_b32_e32 v217, 0xc0c0302\n" \
"v_mfma_f32_16x16x32_bf16 v[84:87], a[240:243], a[200:203], v[84:87]\n" \
"s_addk_i32 s84, 0x7c00\n" \
"v_mfma_f32_16x16x32_bf16 v[88:91], a[180:183], a[136:139], v[88:91]\n" \
"buffer_load_dwordx4 v26, s[68:71], s84 offen offset:1024 lds\n" \
"v_mfma_f32_16x16x32_bf16 v[88:91], a[244:247], a[200:203], v[88:91]\n" \
"ds_read_b64_tr_b16 a[226:227], v32 offset:41984\n" \
"v_cndmask_b32_e64 v215, v215, v216, vcc\n" \
"v_mfma_f32_16x16x32_bf16 v[92:95], a[184:187], a[136:139], v[92:95]\n" \
"s_addk_i32 s84, 0x7c00\n" \
"v_mfma_f32_16x16x32_bf16 v[92:95], a[248:251], a[200:203], v[92:95]\n" \
"buffer_load_dwordx4 v27, s[68:71], s84 offen offset:2048 lds\n" \
"v_mfma_f32_16x16x32_bf16 v[96:99], a[188:191], a[136:139], v[96:99]\n" \
"ds_read_b64_tr_b16 a[164:165], v33 offset:32768\n" \
"v_mov_b32_e32 v216, 0x3020c0c\n" \
"v_mfma_f32_16x16x32_bf16 v[96:99], a[252:255], a[200:203], v[96:99]\n" \
"ds_read_b128 a[136:139], v28 offset:53248\n" \
"v_lshl_or_b32 v214, v212, 2, v214\n" \
"v_mfma_f32_16x16x32_bf16 v[112:115], a[188:191], a[140:143], v[112:115]\n" \
"ds_read_b128 a[200:203], v29 offset:53248\n" \
"v_cndmask_b32_e32 v216, v216, v217, vcc\n" \
"v_mfma_f32_16x16x32_bf16 v[112:115], a[252:255], a[204:207], v[112:115]\n" \
"s_addk_i32 s84, 0x7c00\n" \
"s_nop 0\n" \
"v_mfma_f32_16x16x32_bf16 v[108:111], a[184:187], a[140:143], v[108:111]\n" \
"buffer_load_dwordx4 v27, s[68:71], s84 offen offset:3072 lds\n" \
"v_mfma_f32_16x16x32_bf16 v[108:111], a[248:251], a[204:207], v[108:111]\n" \
"s_add_u32 m0, s30, 0x1000\n" \
"s_mov_b32 s84, 0x8200\n" \
"v_mfma_f32_16x16x32_bf16 v[104:107], a[180:183], a[140:143], v[104:107]\n" \
"buffer_load_dwordx4 v22, s[64:67], s84 offen offset:0 lds\n" \
"v_mfma_f32_16x16x32_bf16 v[104:107], a[244:247], a[204:207], v[104:107]\n" \
"ds_read_b64_tr_b16 a[166:167], v33 offset:33792\n" \
"v_mov_b32_e32 v217, 0xc0c0c0c\n" \
"v_mfma_f32_16x16x32_bf16 v[100:103], a[176:179], a[140:143], v[100:103]\n" \
"ds_read_b64_tr_b16 a[228:229], v33 offset:40960\n" \
"v_cmp_gt_u32_e64 vcc, v214, v213\n" \
"v_mfma_f32_16x16x32_bf16 v[100:103], a[240:243], a[204:207], v[100:103]\n" \
"ds_read_b128 a[140:143], v28 offset:55296\n" \
"v_or_b32_e64 v214, 2, v214\n" \
"v_mfma_f32_16x16x32_bf16 v[116:119], a[176:179], a[144:147], v[116:119]\n" \
"ds_read_b128 a[204:207], v29 offset:55296\n" \
"s_lshr_b32 s4, s2, 5\n" \
"v_mfma_f32_16x16x32_bf16 v[116:119], a[240:243], a[208:211], v[116:119]\n" \
"s_addk_i32 s84, 0x1c00\n" \
"v_mfma_f32_16x16x32_bf16 v[120:123], a[180:183], a[144:147], v[120:123]\n" \
"buffer_load_dwordx4 v23, s[64:67], s84 offen offset:1024 lds\n" \
"v_mfma_f32_16x16x32_bf16 v[120:123], a[244:247], a[208:211], v[120:123]\n" \
"s_addk_i32 s84, 0x1c00\n" \
"v_mfma_f32_16x16x32_bf16 v[124:127], a[184:187], a[144:147], v[124:127]\n" \
"buffer_load_dwordx4 v22, s[64:67], s84 offen offset:2048 lds\n" \
"v_mfma_f32_16x16x32_bf16 v[124:127], a[248:251], a[208:211], v[124:127]\n" \
"s_addk_i32 s84, 0x1c00\n" \
"v_mfma_f32_16x16x32_bf16 v[128:131], a[188:191], a[144:147], v[128:131]\n" \
"buffer_load_dwordx4 v23, s[64:67], s84 offen offset:3072 lds\n" \
"v_mfma_f32_16x16x32_bf16 v[128:131], a[252:255], a[208:211], v[128:131]\n" \
"ds_read_b128 a[144:147], v28 offset:32768\n" \
"v_cndmask_b32_e64 v40, v217, v215, vcc\n" \
"v_mfma_f32_16x16x32_bf16 v[144:147], a[188:191], a[148:151], v[144:147]\n" \
"ds_read_b128 a[208:211], v29 offset:32768\n" \
"v_cndmask_b32_e64 v42, v215, v217, vcc\n" \
"v_mfma_f32_16x16x32_bf16 v[144:147], a[252:255], a[212:215], v[144:147]\n" \
"s_add_u32 m0, s31, 0x4000\n" \
"s_mov_b32 s84, 0x200000\n" \
"v_mfma_f32_16x16x32_bf16 v[140:143], a[184:187], a[148:151], v[140:143]\n" \
"buffer_load_dwordx4 v26, s[72:75], s84 offen offset:0 lds\n" \
"v_mfma_f32_16x16x32_bf16 v[140:143], a[248:251], a[212:215], v[140:143]\n" \
"ds_read_b64_tr_b16 a[230:231], v33 offset:41984\n" \
"v_cmp_gt_u32_e64 vcc, v214, v213\n" \
"v_mfma_f32_16x16x32_bf16 v[136:139], a[180:183], a[148:151], v[136:139]\n" \
"s_addk_i32 s84, 0x7c00\n" \
"v_mfma_f32_16x16x32_bf16 v[136:139], a[244:247], a[212:215], v[136:139]\n" \
"buffer_load_dwordx4 v26, s[72:75], s84 offen offset:1024 lds\n" \
"v_mfma_f32_16x16x32_bf16 v[132:135], a[176:179], a[148:151], v[132:135]\n" \
"ds_read_b64_tr_b16 a[168:169], v34 offset:32768\n" \
"v_and_b32_e32 v214, 16, v20\n" \
"v_mfma_f32_16x16x32_bf16 v[132:135], a[240:243], a[212:215], v[132:135]\n" \
"ds_read_b128 a[148:151], v28 offset:34816\n" \
"s_movk_i32 s42, 0x3f80\n" \
"v_mfma_f32_16x16x32_bf16 v[148:151], a[176:179], a[152:155], v[148:151]\n" \
"ds_read_b128 a[212:215], v29 offset:34816\n" \
"s_xor_b32 s5, s4, 15\n" \
"v_mfma_f32_16x16x32_bf16 v[148:151], a[240:243], a[216:219], v[148:151]\n" \
"ds_read_b64_tr_b16 a[170:171], v34 offset:33792\n" \
"v_mfma_f32_16x16x32_bf16 v[152:155], a[180:183], a[152:155], v[152:155]\n" \
"ds_read_b64_tr_b16 a[232:233], v34 offset:40960\n" \
"v_mfma_f32_16x16x32_bf16 v[152:155], a[244:247], a[216:219], v[152:155]\n" \
"ds_read_b64_tr_b16 a[234:235], v34 offset:41984\n" \
"v_mfma_f32_16x16x32_bf16 v[156:159], a[184:187], a[152:155], v[156:159]\n" \
"ds_read_b64_tr_b16 a[172:173], v35 offset:32768\n" \
"v_mfma_f32_16x16x32_bf16 v[156:159], a[248:251], a[216:219], v[156:159]\n" \
"ds_read_b64_tr_b16 a[174:175], v35 offset:33792\n" \
"v_mfma_f32_16x16x32_bf16 v[160:163], a[188:191], a[152:155], v[160:163]\n" \
"ds_read_b64_tr_b16 a[236:237], v35 offset:40960\n" \
"v_mfma_f32_16x16x32_bf16 v[160:163], a[252:255], a[216:219], v[160:163]\n" \
"ds_read_b128 a[152:155], v28 offset:36864\n" \
"v_mfma_f32_16x16x32_bf16 v[176:179], a[188:191], a[156:159], v[176:179]\n" \
"ds_read_b128 a[216:219], v29 offset:36864\n" \
"v_mfma_f32_16x16x32_bf16 v[176:179], a[252:255], a[220:223], v[176:179]\n" \
"ds_read_b64_tr_b16 a[238:239], v35 offset:41984\n" \
"v_mfma_f32_16x16x32_bf16 v[172:175], a[184:187], a[156:159], v[172:175]\n" \
"s_cmpk_lt_u32 s3, 2\n" \
"s_cselect_b32 s6, s5, s4\n" \
"v_mfma_f32_16x16x32_bf16 v[172:175], a[248:251], a[220:223], v[172:175]\n" \
"s_lshl_b32 s7, s6, 7\n" \
"v_and_b32_e32 v213, 0x320, v20\n" \
"v_mfma_f32_16x16x32_bf16 v[168:171], a[180:183], a[156:159], v[168:171]\n" \
"s_movk_i32 s41, 0x100\n" \
"v_lshlrev_b32_e64 v214, v214, s42\n" \
"v_mfma_f32_16x16x32_bf16 v[168:171], a[244:247], a[220:223], v[168:171]\n" \
"v_mov_b32_e32 v215, 0\n" \
"s_add_i32 s40, s7, -16\n" \
"v_mfma_f32_16x16x32_bf16 v[164:167], a[176:179], a[156:159], v[164:167]\n" \
"v_cndmask_b32_e32 v41, v217, v216, vcc\n" \
"v_cndmask_b32_e32 v43, v216, v217, vcc\n" \
"v_mfma_f32_16x16x32_bf16 v[164:167], a[240:243], a[220:223], v[164:167]\n" \
"ds_read_b128 a[156:159], v28 offset:38912\n" \
"ds_read_b128 a[220:223], v29 offset:38912\n" \
"s_waitcnt lgkmcnt(15)\n" \
"v_mfma_f32_16x16x32_bf16 a[0:3], a[160:163], a[128:131], a[0:3]\n" \
"s_addk_i32 s84, 0x7c00\n" \
"v_mfma_f32_16x16x32_bf16 a[0:3], a[224:227], a[192:195], a[0:3]\n" \
"buffer_load_dwordx4 v27, s[72:75], s84 offen offset:2048 lds\n" \
"v_mfma_f32_16x16x32_bf16 a[4:7], a[164:167], a[128:131], a[4:7]\n" \
"ds_read_b64_tr_b16 a[176:177], v36 offset:32768\n" \
"v_cmp_eq_u32_e32 vcc, s41, v213\n" \
"s_waitcnt lgkmcnt(15)\n" \
"v_mfma_f32_16x16x32_bf16 a[4:7], a[228:231], a[192:195], a[4:7]\n" \
"s_addk_i32 s84, 0x7c00\n" \
"s_waitcnt lgkmcnt(11)\n" \
"v_mfma_f32_16x16x32_bf16 a[8:11], a[168:171], a[128:131], a[8:11]\n" \
"buffer_load_dwordx4 v27, s[72:75], s84 offen offset:3072 lds\n" \
"s_waitcnt lgkmcnt(9)\n" \
"v_mfma_f32_16x16x32_bf16 a[8:11], a[232:235], a[192:195], a[8:11]\n" \
"ds_read_b64_tr_b16 a[178:179], v36 offset:33792\n" \
"s_mov_b32 s41, 0\n" \
"s_waitcnt lgkmcnt(8)\n" \
"v_mfma_f32_16x16x32_bf16 a[12:15], a[172:175], a[128:131], a[12:15]\n" \
"ds_read_b64_tr_b16 a[240:241], v36 offset:40960\n" \
"s_and_b32 s43, s3, 1\n" \
"s_waitcnt lgkmcnt(5)\n" \
"v_mfma_f32_16x16x32_bf16 a[12:15], a[236:239], a[192:195], a[12:15]\n" \
"v_cndmask_b32_e32 v46, v215, v214, vcc\n" \
"v_and_b32_e32 v213, 0x330, v20\n" \
"v_mfma_f32_16x16x32_bf16 a[28:31], a[172:175], a[132:135], a[28:31]\n" \
"s_cmp_lg_u32 s7, 0\n" \
"s_cselect_b32 s40, s40, s41\n" \
"v_mfma_f32_16x16x32_bf16 a[28:31], a[236:239], a[196:199], a[28:31]\n" \
"ds_read_b64_tr_b16 a[242:243], v36 offset:41984\n" \
"s_mov_b32 s42, 0xff800000\n" \
"v_mfma_f32_16x16x32_bf16 a[24:27], a[168:171], a[132:135], a[24:27]\n" \
"s_cmp_lg_u32 s7, 0\n" \
"s_cselect_b32 s47, s41, s42\n" \
"v_mfma_f32_16x16x32_bf16 a[24:27], a[232:235], a[196:199], a[24:27]\n" \
"s_lshl_b32 s41, s43, 6\n" \
"v_lshrrev_b32_e32 v215, 6, v20\n" \
"v_mfma_f32_16x16x32_bf16 a[20:23], a[164:167], a[132:135], a[20:23]\n" \
"v_mov_b32_e32 v214, 0x3f80\n" \
"v_cmp_eq_u32_e64 vcc, 32, v213\n" \
"v_mfma_f32_16x16x32_bf16 a[20:23], a[228:231], a[196:199], a[20:23]\n" \
"ds_read_b64_tr_b16 a[180:181], v37 offset:32768\n" \
"s_or_b32 s7, s41, s7\n" \
"v_mfma_f32_16x16x32_bf16 a[16:19], a[160:163], a[132:135], a[16:19]\n" \
"v_lshrrev_b32_e32 v213, 7, v20\n" \
"v_bfe_u32 v216, v20, 6, 2\n" \
"v_mfma_f32_16x16x32_bf16 a[16:19], a[224:227], a[196:199], a[16:19]\n" \
"v_lshrrev_b32_e64 v219, 1, v20\n" \
"v_bfe_u32 v215, v215, 1, 1\n" \
"v_mfma_f32_16x16x32_bf16 a[32:35], a[160:163], a[136:139], a[32:35]\n" \
"s_waitcnt vmcnt(16)\n" \
"v_or_b32_e32 v204, v210, v204\n" \
"v_or_b32_e64 v206, v210, v206\n" \
"v_mfma_f32_16x16x32_bf16 a[32:35], a[224:227], a[200:203], a[32:35]\n" \
"v_mov_b32_e64 v210, 0\n" \
"s_add_u32 s88, s35, 0x1000\n" \
"v_mfma_f32_16x16x32_bf16 a[36:39], a[164:167], a[136:139], a[36:39]\n" \
"ds_read_b64_tr_b16 a[182:183], v37 offset:33792\n" \
"v_cndmask_b32_e64 v47, 0, v214, vcc\n" \
"v_mfma_f32_16x16x32_bf16 a[36:39], a[228:231], a[200:203], a[36:39]\n" \
"ds_read_b64_tr_b16 a[244:245], v37 offset:40960\n" \
"v_or_b32_e64 v214, s7, v213\n" \
"v_mfma_f32_16x16x32_bf16 a[40:43], a[168:171], a[136:139], a[40:43]\n" \
"s_mov_b32 m0, s32\n" \
"v_lshl_or_b32 v217, s43, 3, v213\n" \
"v_mfma_f32_16x16x32_bf16 a[40:43], a[232:235], a[200:203], a[40:43]\n" \
"s_mov_b32 exec_lo, 0xffffffff\n" \
"s_mov_b32 exec_hi, 0x0\n" \
"global_load_lds_dword v[188:189], off\n" \
"s_mov_b64 exec, -1\n" \
"v_mfma_f32_16x16x32_bf16 a[44:47], a[172:175], a[136:139], a[44:47]\n" \
"ds_read_b64_tr_b16 a[246:247], v37 offset:41984\n" \
"v_lshlrev_b32_e64 v218, 10, v212\n" \
"v_mfma_f32_16x16x32_bf16 a[44:47], a[236:239], a[200:203], a[44:47]\n" \
"v_lshlrev_b32_e32 v216, 7, v216\n" \
"v_and_b32_e32 v219, 24, v219\n" \
"v_mfma_f32_16x16x32_bf16 a[60:63], a[172:175], a[140:143], a[60:63]\n" \
"ds_write_b128 v196, v[200:203]\n" \
"v_mfma_f32_16x16x32_bf16 a[60:63], a[236:239], a[204:207], a[60:63]\n" \
"v_and_or_b32 v213, v213, 2, v215\n" \
"v_add_u32_e64 v217, s40, v217\n" \
"v_mfma_f32_16x16x32_bf16 a[56:59], a[168:171], a[140:143], a[56:59]\n" \
"ds_read_b64_tr_b16 a[184:185], v38 offset:32768\n" \
"v_or3_b32 v216, v216, v218, v219\n" \
"v_mfma_f32_16x16x32_bf16 a[56:59], a[232:235], a[204:207], a[56:59]\n" \
"v_lshlrev_b32_e64 v213, 5, v213\n" \
"s_mov_b32 s40, 0x14000\n" \
"v_mfma_f32_16x16x32_bf16 a[52:55], a[164:167], a[140:143], a[52:55]\n" \
"s_lshl_b32 s7, s2, 2\n" \
"v_or3_b32 v48, v216, v213, s40\n" \
"v_mfma_f32_16x16x32_bf16 a[52:55], a[228:231], a[204:207], a[52:55]\n" \
"ds_read_b64_tr_b16 a[186:187], v38 offset:33792\n" \
"s_lshl_b32 s40, s43, 2\n" \
"v_mfma_f32_16x16x32_bf16 a[48:51], a[160:163], a[140:143], a[48:51]\n" \
"s_lshl_b32 s6, s6, 3\n" \
"v_or_b32_e32 v213, s40, v212\n" \
"v_mfma_f32_16x16x32_bf16 a[48:51], a[224:227], a[204:207], a[48:51]\n" \
"s_or_b32 s6, s6, s40\n" \
"s_and_b32 s40, s7, 0x7c\n" \
"v_mfma_f32_16x16x32_bf16 a[64:67], a[160:163], a[144:147], a[64:67]\n" \
"ds_write_b128 v197, v[204:207]\n" \
"v_mfma_f32_16x16x32_bf16 a[64:67], a[224:227], a[208:211], a[64:67]\n" \
"ds_read_b64_tr_b16 a[248:249], v38 offset:40960\n" \
"s_or_b32 s40, s40, s3\n" \
"v_mfma_f32_16x16x32_bf16 a[68:71], a[164:167], a[144:147], a[68:71]\n" \
"s_cmpk_lt_u32 s40, 64\n" \
"s_cselect_b32 s41, s4, s5\n" \
"v_mfma_f32_16x16x32_bf16 a[68:71], a[228:231], a[208:211], a[68:71]\n" \
"ds_read_b64_tr_b16 a[250:251], v38 offset:41984\n" \
"s_lshl_b32 s40, s40, 1\n" \
"v_mfma_f32_16x16x32_bf16 a[72:75], a[168:171], a[144:147], a[72:75]\n" \
"s_lshl_b32 s41, s41, 7\n" \
"s_and_b32 s40, s40, 0x7e\n" \
"v_mfma_f32_16x16x32_bf16 a[72:75], a[232:235], a[208:211], a[72:75]\n" \
"s_or_b32 s40, s41, s40\n" \
"s_mulk_i32 s40, 0x480\n" \
"v_mfma_f32_16x16x32_bf16 a[76:79], a[172:175], a[144:147], a[76:79]\n" \
"v_add_u32_e64 v49, s40, v20\n" \
"s_mov_b32 s40, 0xc000\n" \
"v_mfma_f32_16x16x32_bf16 a[76:79], a[236:239], a[208:211], a[76:79]\n" \
"ds_read_b64_tr_b16 a[188:189], v39 offset:32768\n" \
"s_and_b32 s7, s7, 28\n" \
"v_mfma_f32_16x16x32_bf16 a[92:95], a[172:175], a[148:151], a[92:95]\n" \
"ds_write_b128 v198, v[208:211]\n" \
"v_mfma_f32_16x16x32_bf16 a[92:95], a[236:239], a[212:215], a[92:95]\n" \
"s_bfe_u32 s45, s2, 0x20003\n" \
"s_mov_b32 s41, 0x8000\n" \
"v_mfma_f32_16x16x32_bf16 a[88:91], a[168:171], a[148:151], a[88:91]\n" \
"ds_read_b64_tr_b16 a[190:191], v39 offset:33792\n" \
"v_mfma_f32_16x16x32_bf16 a[88:91], a[232:235], a[212:215], a[88:91]\n" \
"s_cmp_lg_u32 s4, 0\n" \
"s_cselect_b32 s51, s40, s41\n" \
"v_mfma_f32_16x16x32_bf16 a[84:87], a[164:167], a[148:151], a[84:87]\n" \
"s_movk_i32 s40, 0x4000\n" \
"s_lshr_b32 s44, s3, 1\n" \
"v_mfma_f32_16x16x32_bf16 a[84:87], a[228:231], a[212:215], a[84:87]\n" \
"ds_read_b64_tr_b16 a[252:253], v39 offset:40960\n" \
"s_or_b32 s46, s7, s45\n" \
"v_mfma_f32_16x16x32_bf16 a[80:83], a[160:163], a[148:151], a[80:83]\n" \
"s_cmp_lg_u32 s4, 0\n" \
"s_cselect_b32 s53, s41, s40\n" \
"v_mfma_f32_16x16x32_bf16 a[80:83], a[224:227], a[212:215], a[80:83]\n" \
"s_lshl_b32 s40, s44, 15\n" \
"s_lshl_b32 s41, s43, 14\n" \
"v_mfma_f32_16x16x32_bf16 a[96:99], a[160:163], a[152:155], a[96:99]\n" \
"ds_read_b64_tr_b16 a[254:255], v39 offset:41984\n" \
"s_lshl_b32 s48, s46, 20\n" \
"v_mfma_f32_16x16x32_bf16 a[96:99], a[224:227], a[216:219], a[96:99]\n" \
"s_or_b32 s41, s40, s41\n" \
"s_add_i32 s49, s48, 0x1000000\n" \
"v_mfma_f32_16x16x32_bf16 a[100:103], a[164:167], a[152:155], a[100:103]\n" \
"s_or_b32 s42, s49, s41\n" \
"s_lshl_b32 s4, s4, 16\n" \
"v_mfma_f32_16x16x32_bf16 a[100:103], a[228:231], a[216:219], a[100:103]\n" \
"s_or_b32 s41, s42, s4\n" \
"s_lshl_b32 s4, s5, 16\n" \
"v_mfma_f32_16x16x32_bf16 a[104:107], a[168:171], a[152:155], a[104:107]\n" \
"s_mul_i32 s5, s6, 0x24800\n" \
"s_lshl_b32 s6, s46, 9\n" \
"v_mfma_f32_16x16x32_bf16 a[104:107], a[232:235], a[216:219], a[104:107]\n" \
"s_lshl_b32 s7, s7, 6\n" \
"s_add_i32 s6, s5, s6\n" \
"v_mfma_f32_16x16x32_bf16 a[108:111], a[172:175], a[152:155], a[108:111]\n" \
"s_or_b32 s5, s5, s7\n" \
"s_lshl_b32 s7, s45, 2\n" \
"v_mfma_f32_16x16x32_bf16 a[108:111], a[236:239], a[216:219], a[108:111]\n" \
"s_or_b32 s5, s5, s7\n" \
"s_lshl_b32 s7, s3, 12\n" \
"s_waitcnt lgkmcnt(15)\n" \
"v_mfma_f32_16x16x32_bf16 a[124:127], a[172:175], a[156:159], a[124:127]\n" \
"s_or_b32 s50, s49, s7\n" \
"s_lshl_b32 s7, s3, 11\n" \
"v_mfma_f32_16x16x32_bf16 a[124:127], a[236:239], a[220:223], a[124:127]\n" \
"v_lshrrev_b32_e64 v215, 4, v20\n" \
"v_bfe_u32 v216, v20, 4, 3\n" \
"v_mfma_f32_16x16x32_bf16 a[120:123], a[168:171], a[156:159], a[120:123]\n" \
"s_or_b32 s42, s42, s4\n" \
"s_lshl_b32 s4, s43, 11\n" \
"v_mfma_f32_16x16x32_bf16 a[120:123], a[232:235], a[220:223], a[120:123]\n" \
"s_add_i32 s52, s48, s7\n" \
"s_add_i32 s53, s53, s7\n" \
"v_mfma_f32_16x16x32_bf16 a[116:119], a[164:167], a[156:159], a[116:119]\n" \
"v_mul_u32_u24_e32 v214, 0x480, v214\n" \
"v_bitop3_b32 v218, v212, v215, 7 bitop3:0x78\n" \
"v_mfma_f32_16x16x32_bf16 a[116:119], a[228:231], a[220:223], a[116:119]\n" \
"v_bitop3_b32 v212, v212, v216, 4 bitop3:0x36\n" \
"s_lshl_b32 s7, s3, 10\n" \
"v_mfma_f32_16x16x32_bf16 a[112:115], a[160:163], a[156:159], a[112:115]\n" \
"s_or_b32 s4, s40, s4\n" \
"v_lshl_or_b32 v25, v212, 4, v214\n" \
"v_mfma_f32_16x16x32_bf16 a[112:115], a[224:227], a[220:223], a[112:115]\n" \
"s_waitcnt vmcnt(1) lgkmcnt(0)\n" \
"s_barrier\n" \
"v_mfma_f32_16x16x32_bf16 v[52:55], a[176:179], a[128:131], v[52:55]\n" \
"s_add_u32 m0, s30, 0x8000\n" \
"s_movk_i32 s84, 0x280\n" \
"v_mfma_f32_16x16x32_bf16 v[52:55], a[240:243], a[192:195], v[52:55]\n" \
"buffer_load_dwordx4 v22, s[64:67], s84 offen offset:0 lds\n" \
"v_mfma_f32_16x16x32_bf16 v[56:59], a[180:183], a[128:131], v[56:59]\n" \
"s_movk_i32 s84, 0x1e80\n" \
"v_mfma_f32_16x16x32_bf16 v[56:59], a[244:247], a[192:195], v[56:59]\n" \
"buffer_load_dwordx4 v23, s[64:67], s84 offen offset:1024 lds\n" \
"v_mfma_f32_16x16x32_bf16 v[60:63], a[184:187], a[128:131], v[60:63]\n" \
"ds_read_b64_tr_b16 a[160:161], v32 offset:0\n" \
"v_mul_u32_u24_e32 v212, 0x480, v217\n" \
"v_mfma_f32_16x16x32_bf16 v[60:63], a[248:251], a[192:195], v[60:63]\n" \
"s_movk_i32 s84, 0x3a80\n" \
"v_mfma_f32_16x16x32_bf16 v[64:67], a[188:191], a[128:131], v[64:67]\n" \
"buffer_load_dwordx4 v22, s[64:67], s84 offen offset:2048 lds\n" \
"v_mfma_f32_16x16x32_bf16 v[64:67], a[252:255], a[192:195], v[64:67]\n" \
"ds_read_b128 a[128:131], v28 offset:16384\n" \
"v_bitop3_b32 v213, v215, v213, 7 bitop3:0x6c\n" \
"v_mfma_f32_16x16x32_bf16 v[80:83], a[188:191], a[132:135], v[80:83]\n" \
"ds_read_b128 a[192:195], v29 offset:16384\n" \
"s_or_b32 s40, s7, 0x14000\n" \
"v_mfma_f32_16x16x32_bf16 v[80:83], a[252:255], a[196:199], v[80:83]\n" \
"s_movk_i32 s84, 0x5680\n" \
"v_mfma_f32_16x16x32_bf16 v[76:79], a[184:187], a[132:135], v[76:79]\n" \
"buffer_load_dwordx4 v23, s[64:67], s84 offen offset:3072 lds\n" \
"v_mfma_f32_16x16x32_bf16 v[76:79], a[248:251], a[196:199], v[76:79]\n" \
"s_add_u32 m0, s31, 0x8000\n" \
"s_mov_b32 s84, 0x280000\n" \
"v_mfma_f32_16x16x32_bf16 v[72:75], a[180:183], a[132:135], v[72:75]\n" \
"buffer_load_dwordx4 v26, s[68:71], s84 offen offset:0 lds\n" \
"v_mfma_f32_16x16x32_bf16 v[72:75], a[244:247], a[196:199], v[72:75]\n" \
"ds_read_b64_tr_b16 a[162:163], v32 offset:1024\n" \
"s_lshl_b32 s43, s44, 13\n" \
"v_mfma_f32_16x16x32_bf16 v[68:71], a[176:179], a[132:135], v[68:71]\n" \
"ds_read_b64_tr_b16 a[224:225], v32 offset:8192\n" \
"s_mulk_i32 s44, 0xe800\n" \
"v_mfma_f32_16x16x32_bf16 v[68:71], a[240:243], a[196:199], v[68:71]\n" \
"ds_read_b128 a[132:135], v28 offset:18432\n" \
"s_lshl_b32 s7, s3, 8\n" \
"v_mfma_f32_16x16x32_bf16 v[84:87], a[176:179], a[136:139], v[84:87]\n" \
"ds_read_b128 a[196:199], v29 offset:18432\n" \
"v_lshl_or_b32 v24, v218, 4, v214\n" \
"v_mfma_f32_16x16x32_bf16 v[84:87], a[240:243], a[200:203], v[84:87]\n" \
"s_addk_i32 s84, 0x7c00\n" \
"v_mfma_f32_16x16x32_bf16 v[88:91], a[180:183], a[136:139], v[88:91]\n" \
"buffer_load_dwordx4 v26, s[68:71], s84 offen offset:1024 lds\n" \
"v_mfma_f32_16x16x32_bf16 v[88:91], a[244:247], a[200:203], v[88:91]\n" \
"s_addk_i32 s84, 0x7c00\n" \
"v_mfma_f32_16x16x32_bf16 v[92:95], a[184:187], a[136:139], v[92:95]\n" \
"buffer_load_dwordx4 v27, s[68:71], s84 offen offset:2048 lds\n" \
"v_mfma_f32_16x16x32_bf16 v[92:95], a[248:251], a[200:203], v[92:95]\n" \
"s_addk_i32 s84, 0x7c00\n" \
"v_mfma_f32_16x16x32_bf16 v[96:99], a[188:191], a[136:139], v[96:99]\n" \
"buffer_load_dwordx4 v27, s[68:71], s84 offen offset:3072 lds\n" \
"v_mfma_f32_16x16x32_bf16 v[96:99], a[252:255], a[200:203], v[96:99]\n" \
"ds_read_b128 a[136:139], v28 offset:20480\n" \
"v_lshl_or_b32 v21, v213, 4, v212\n" \
"v_mfma_f32_16x16x32_bf16 v[112:115], a[188:191], a[140:143], v[112:115]\n" \
"ds_read_b128 a[200:203], v29 offset:20480\n" \
"s_add_i32 s44, s44, 0x17000\n" \
"v_mfma_f32_16x16x32_bf16 v[112:115], a[252:255], a[204:207], v[112:115]\n" \
"s_add_u32 m0, s30, 0x9000\n" \
"s_mov_b32 s84, 0x8280\n" \
"v_mfma_f32_16x16x32_bf16 v[108:111], a[184:187], a[140:143], v[108:111]\n" \
"buffer_load_dwordx4 v22, s[64:67], s84 offen offset:0 lds\n" \
"v_mfma_f32_16x16x32_bf16 v[108:111], a[248:251], a[204:207], v[108:111]\n" \
"s_addk_i32 s84, 0x1c00\n" \
"v_mfma_f32_16x16x32_bf16 v[104:107], a[180:183], a[140:143], v[104:107]\n" \
"buffer_load_dwordx4 v23, s[64:67], s84 offen offset:1024 lds\n" \
"v_mfma_f32_16x16x32_bf16 v[104:107], a[244:247], a[204:207], v[104:107]\n" \
"s_addk_i32 s84, 0x1c00\n" \
"v_mfma_f32_16x16x32_bf16 v[100:103], a[176:179], a[140:143], v[100:103]\n" \
"buffer_load_dwordx4 v22, s[64:67], s84 offen offset:2048 lds\n" \
"v_mfma_f32_16x16x32_bf16 v[100:103], a[240:243], a[204:207], v[100:103]\n" \
"ds_read_b128 a[140:143], v28 offset:22528\n" \
"s_or_b32 s45, s7, 0x27000\n" \
"v_mfma_f32_16x16x32_bf16 v[116:119], a[176:179], a[144:147], v[116:119]\n" \
"ds_read_b128 a[204:207], v29 offset:22528\n" \
"s_or_b32 s46, s4, 0x10000\n" \
"v_mfma_f32_16x16x32_bf16 v[116:119], a[240:243], a[208:211], v[116:119]\n" \
"s_addk_i32 s84, 0x1c00\n" \
"v_mfma_f32_16x16x32_bf16 v[120:123], a[180:183], a[144:147], v[120:123]\n" \
"buffer_load_dwordx4 v23, s[64:67], s84 offen offset:3072 lds\n" \
"v_mfma_f32_16x16x32_bf16 v[120:123], a[244:247], a[208:211], v[120:123]\n" \
"s_add_u32 m0, s31, 0xc000\n" \
"s_mov_b32 s84, 0x280000\n" \
"v_mfma_f32_16x16x32_bf16 v[124:127], a[184:187], a[144:147], v[124:127]\n" \
"buffer_load_dwordx4 v26, s[72:75], s84 offen offset:0 lds\n" \
"v_mfma_f32_16x16x32_bf16 v[124:127], a[248:251], a[208:211], v[124:127]\n" \
"s_addk_i32 s84, 0x7c00\n" \
"v_mfma_f32_16x16x32_bf16 v[128:131], a[188:191], a[144:147], v[128:131]\n" \
"buffer_load_dwordx4 v26, s[72:75], s84 offen offset:1024 lds\n" \
"v_mfma_f32_16x16x32_bf16 v[128:131], a[252:255], a[208:211], v[128:131]\n" \
"ds_read_b128 a[144:147], v28 offset:0\n" \
"s_add_i32 s48, s6, 0x3000000\n" \
"v_mfma_f32_16x16x32_bf16 v[144:147], a[188:191], a[148:151], v[144:147]\n" \
"ds_read_b128 a[208:211], v29 offset:0\n" \
"s_add_i32 s49, s5, 0x3004000\n" \
"v_mfma_f32_16x16x32_bf16 v[144:147], a[252:255], a[212:215], v[144:147]\n" \
"ds_read_b128 v[200:203], v199 offset:0\n" \
"s_add_i32 s52, s52, 0x1010000\n" \
"v_mfma_f32_16x16x32_bf16 v[140:143], a[184:187], a[148:151], v[140:143]\n" \
"ds_read_b128 v[204:207], v199 offset:4096\n" \
"v_mfma_f32_16x16x32_bf16 v[140:143], a[248:251], a[212:215], v[140:143]\n" \
"ds_read_b64_tr_b16 a[226:227], v32 offset:9216\n" \
"v_mfma_f32_16x16x32_bf16 v[136:139], a[180:183], a[148:151], v[136:139]\n" \
"ds_read_b64_tr_b16 a[164:165], v33 offset:0\n" \
"v_mfma_f32_16x16x32_bf16 v[136:139], a[244:247], a[212:215], v[136:139]\n" \
"ds_read_b128 v[208:211], v199 offset:8192\n" \
"v_mfma_f32_16x16x32_bf16 v[132:135], a[176:179], a[148:151], v[132:135]\n" \
"ds_read_b64_tr_b16 a[166:167], v33 offset:1024\n" \
"v_mfma_f32_16x16x32_bf16 v[132:135], a[240:243], a[212:215], v[132:135]\n" \
"ds_read_b128 a[148:151], v28 offset:2048\n" \
"v_mfma_f32_16x16x32_bf16 v[148:151], a[176:179], a[152:155], v[148:151]\n" \
"ds_read_b128 a[212:215], v29 offset:2048\n" \
"v_mfma_f32_16x16x32_bf16 v[148:151], a[240:243], a[216:219], v[148:151]\n" \
"ds_read_b64_tr_b16 a[228:229], v33 offset:8192\n" \
"v_mfma_f32_16x16x32_bf16 v[152:155], a[180:183], a[152:155], v[152:155]\n" \
"ds_read_b64_tr_b16 a[230:231], v33 offset:9216\n" \
"v_mfma_f32_16x16x32_bf16 v[152:155], a[244:247], a[216:219], v[152:155]\n" \
"ds_read_b64_tr_b16 a[168:169], v34 offset:0\n" \
"v_mfma_f32_16x16x32_bf16 v[156:159], a[184:187], a[152:155], v[156:159]\n" \
"ds_read_b64_tr_b16 a[170:171], v34 offset:1024\n" \
"v_mfma_f32_16x16x32_bf16 v[156:159], a[248:251], a[216:219], v[156:159]\n" \
"ds_read_b64_tr_b16 a[232:233], v34 offset:8192\n" \
"v_mfma_f32_16x16x32_bf16 v[160:163], a[188:191], a[152:155], v[160:163]\n" \
"ds_read_b64_tr_b16 a[234:235], v34 offset:9216\n" \
"v_mfma_f32_16x16x32_bf16 v[160:163], a[252:255], a[216:219], v[160:163]\n" \
"ds_read_b128 a[152:155], v28 offset:4096\n" \
"v_mfma_f32_16x16x32_bf16 v[176:179], a[188:191], a[156:159], v[176:179]\n" \
"ds_read_b128 a[216:219], v29 offset:4096\n" \
"v_mfma_f32_16x16x32_bf16 v[176:179], a[252:255], a[220:223], v[176:179]\n" \
"ds_read_b64_tr_b16 a[172:173], v35 offset:0\n" \
"v_mfma_f32_16x16x32_bf16 v[172:175], a[184:187], a[156:159], v[172:175]\n" \
"ds_read_b64_tr_b16 a[174:175], v35 offset:1024\n" \
"v_mfma_f32_16x16x32_bf16 v[172:175], a[248:251], a[220:223], v[172:175]\n" \
"ds_read_b64_tr_b16 a[236:237], v35 offset:8192\n" \
"v_mfma_f32_16x16x32_bf16 v[168:171], a[180:183], a[156:159], v[168:171]\n" \
"ds_read_b64_tr_b16 a[238:239], v35 offset:9216\n" \
"v_mfma_f32_16x16x32_bf16 v[168:171], a[244:247], a[220:223], v[168:171]\n" \
"v_mfma_f32_16x16x32_bf16 v[164:167], a[176:179], a[156:159], v[164:167]\n" \
"v_mfma_f32_16x16x32_bf16 v[164:167], a[240:243], a[220:223], v[164:167]\n" \
"ds_read_b128 a[156:159], v28 offset:6144\n" \
"ds_read_b128 a[220:223], v29 offset:6144\n" \
"s_waitcnt lgkmcnt(15)\n" \
"v_mfma_f32_16x16x32_bf16 a[0:3], a[160:163], a[128:131], a[0:3]\n" \
"s_addk_i32 s84, 0x7c00\n" \
"v_mfma_f32_16x16x32_bf16 a[0:3], a[224:227], a[192:195], a[0:3]\n" \
"buffer_load_dwordx4 v27, s[72:75], s84 offen offset:2048 lds\n" \
"v_mfma_f32_16x16x32_bf16 a[4:7], a[164:167], a[128:131], a[4:7]\n" \
"ds_read_b64_tr_b16 a[176:177], v36 offset:0\n" \
"s_waitcnt lgkmcnt(13)\n" \
"v_mfma_f32_16x16x32_bf16 a[4:7], a[228:231], a[192:195], a[4:7]\n" \
"s_addk_i32 s84, 0x7c00\n" \
"s_waitcnt lgkmcnt(11)\n" \
"v_mfma_f32_16x16x32_bf16 a[8:11], a[168:171], a[128:131], a[8:11]\n" \
"buffer_load_dwordx4 v27, s[72:75], s84 offen offset:3072 lds\n" \
"s_waitcnt lgkmcnt(9)\n" \
"v_mfma_f32_16x16x32_bf16 a[8:11], a[232:235], a[192:195], a[8:11]\n" \
"ds_read_b64_tr_b16 a[178:179], v36 offset:1024\n" \
"s_waitcnt lgkmcnt(6)\n" \
"v_mfma_f32_16x16x32_bf16 a[12:15], a[172:175], a[128:131], a[12:15]\n" \
"ds_read_b64_tr_b16 a[240:241], v36 offset:8192\n" \
"s_waitcnt lgkmcnt(5)\n" \
"v_mfma_f32_16x16x32_bf16 a[12:15], a[236:239], a[192:195], a[12:15]\n" \
"v_mfma_f32_16x16x32_bf16 a[28:31], a[172:175], a[132:135], a[28:31]\n" \
"v_mfma_f32_16x16x32_bf16 a[28:31], a[236:239], a[196:199], a[28:31]\n" \
"ds_read_b64_tr_b16 a[242:243], v36 offset:9216\n" \
"v_mfma_f32_16x16x32_bf16 a[24:27], a[168:171], a[132:135], a[24:27]\n" \
"v_mfma_f32_16x16x32_bf16 a[24:27], a[232:235], a[196:199], a[24:27]\n" \
"v_mfma_f32_16x16x32_bf16 a[20:23], a[164:167], a[132:135], a[20:23]\n" \
"v_mfma_f32_16x16x32_bf16 a[20:23], a[228:231], a[196:199], a[20:23]\n" \
"ds_read_b64_tr_b16 a[180:181], v37 offset:0\n" \
"v_mfma_f32_16x16x32_bf16 a[16:19], a[160:163], a[132:135], a[16:19]\n" \
"v_mfma_f32_16x16x32_bf16 a[16:19], a[224:227], a[196:199], a[16:19]\n" \
"v_mfma_f32_16x16x32_bf16 a[32:35], a[160:163], a[136:139], a[32:35]\n" \
"ds_read_b64_tr_b16 a[182:183], v37 offset:1024\n" \
"v_mfma_f32_16x16x32_bf16 a[32:35], a[224:227], a[200:203], a[32:35]\n" \
"v_mfma_f32_16x16x32_bf16 a[36:39], a[164:167], a[136:139], a[36:39]\n" \
"v_mfma_f32_16x16x32_bf16 a[36:39], a[228:231], a[200:203], a[36:39]\n" \
"buffer_store_dwordx4 v[200:203], v20, s[80:83], s35 offen sc1\n" \
"v_mfma_f32_16x16x32_bf16 a[40:43], a[168:171], a[136:139], a[40:43]\n" \
"s_mov_b32 m0, s32\n" \
"ds_read_b64_tr_b16 a[244:245], v37 offset:8192\n" \
".fill (L_pb + 12000 - .) / 4, 4, 0xBF800000\n" \
"L_pp3:\n" \
"s_cbranch_execz L_pret\n" \
"v_mfma_f32_16x16x32_bf16 a[40:43], a[232:235], a[200:203], a[40:43]\n" \
"s_mov_b32 exec_lo, 0x0\n" \
"s_mov_b32 exec_hi, 0xffffffff\n" \
"global_load_lds_dword v[188:189], off\n" \
"s_mov_b64 exec, -1\n" \
"v_mfma_f32_16x16x32_bf16 a[44:47], a[172:175], a[136:139], a[44:47]\n" \
"ds_read_b64_tr_b16 a[246:247], v37 offset:9216\n" \
"v_mfma_f32_16x16x32_bf16 a[44:47], a[236:239], a[200:203], a[44:47]\n" \
"v_mfma_f32_16x16x32_bf16 a[60:63], a[172:175], a[140:143], a[60:63]\n" \
"v_mfma_f32_16x16x32_bf16 a[60:63], a[236:239], a[204:207], a[60:63]\n" \
"v_mfma_f32_16x16x32_bf16 a[56:59], a[168:171], a[140:143], a[56:59]\n" \
"ds_read_b64_tr_b16 a[184:185], v38 offset:0\n" \
"v_mfma_f32_16x16x32_bf16 a[56:59], a[232:235], a[204:207], a[56:59]\n" \
"v_mfma_f32_16x16x32_bf16 a[52:55], a[164:167], a[140:143], a[52:55]\n" \
"buffer_store_dwordx4 v[204:207], v20, s[80:83], s88 offen sc1\n" \
"v_mfma_f32_16x16x32_bf16 a[52:55], a[228:231], a[204:207], a[52:55]\n" \
"ds_read_b64_tr_b16 a[186:187], v38 offset:1024\n" \
"v_mfma_f32_16x16x32_bf16 a[48:51], a[160:163], a[140:143], a[48:51]\n" \
"v_mfma_f32_16x16x32_bf16 a[48:51], a[224:227], a[204:207], a[48:51]\n" \
"v_mfma_f32_16x16x32_bf16 a[64:67], a[160:163], a[144:147], a[64:67]\n" \
"ds_read_b64_tr_b16 a[248:249], v38 offset:8192\n" \
"v_mfma_f32_16x16x32_bf16 a[64:67], a[224:227], a[208:211], a[64:67]\n" \
"v_mfma_f32_16x16x32_bf16 a[68:71], a[164:167], a[144:147], a[68:71]\n" \
"v_mfma_f32_16x16x32_bf16 a[68:71], a[228:231], a[208:211], a[68:71]\n" \
"ds_read_b64_tr_b16 a[250:251], v38 offset:9216\n" \
"v_mfma_f32_16x16x32_bf16 a[72:75], a[168:171], a[144:147], a[72:75]\n" \
"v_mfma_f32_16x16x32_bf16 a[72:75], a[232:235], a[208:211], a[72:75]\n" \
"v_mfma_f32_16x16x32_bf16 a[76:79], a[172:175], a[144:147], a[76:79]\n" \
"buffer_store_dwordx4 v[208:211], v20, s[80:83], s36 offen sc1\n" \
"v_mfma_f32_16x16x32_bf16 a[76:79], a[236:239], a[208:211], a[76:79]\n" \
"ds_read_b64_tr_b16 a[188:189], v39 offset:0\n" \
"v_mfma_f32_16x16x32_bf16 a[92:95], a[172:175], a[148:151], a[92:95]\n" \
"v_mfma_f32_16x16x32_bf16 a[92:95], a[236:239], a[212:215], a[92:95]\n" \
"v_mfma_f32_16x16x32_bf16 a[88:91], a[168:171], a[148:151], a[88:91]\n" \
"ds_read_b64_tr_b16 a[190:191], v39 offset:1024\n" \
"v_mfma_f32_16x16x32_bf16 a[88:91], a[232:235], a[212:215], a[88:91]\n" \
"v_mfma_f32_16x16x32_bf16 a[84:87], a[164:167], a[148:151], a[84:87]\n" \
"v_mfma_f32_16x16x32_bf16 a[84:87], a[228:231], a[212:215], a[84:87]\n" \
"ds_read_b64_tr_b16 a[252:253], v39 offset:8192\n" \
"v_mfma_f32_16x16x32_bf16 a[80:83], a[160:163], a[148:151], a[80:83]\n" \
"v_mfma_f32_16x16x32_bf16 a[80:83], a[224:227], a[212:215], a[80:83]\n" \
"v_mfma_f32_16x16x32_bf16 a[96:99], a[160:163], a[152:155], a[96:99]\n" \
"ds_read_b64_tr_b16 a[254:255], v39 offset:9216\n" \
"v_mfma_f32_16x16x32_bf16 a[96:99], a[224:227], a[216:219], a[96:99]\n" \
"v_mfma_f32_16x16x32_bf16 a[100:103], a[164:167], a[152:155], a[100:103]\n" \
"v_mfma_f32_16x16x32_bf16 a[100:103], a[228:231], a[216:219], a[100:103]\n" \
"v_mfma_f32_16x16x32_bf16 a[104:107], a[168:171], a[152:155], a[104:107]\n" \
"v_mfma_f32_16x16x32_bf16 a[104:107], a[232:235], a[216:219], a[104:107]\n" \
"v_mfma_f32_16x16x32_bf16 a[108:111], a[172:175], a[152:155], a[108:111]\n" \
"v_mfma_f32_16x16x32_bf16 a[108:111], a[236:239], a[216:219], a[108:111]\n" \
"s_waitcnt lgkmcnt(15)\n" \
"v_mfma_f32_16x16x32_bf16 a[124:127], a[172:175], a[156:159], a[124:127]\n" \
"v_mfma_f32_16x16x32_bf16 a[124:127], a[236:239], a[220:223], a[124:127]\n" \
"v_mfma_f32_16x16x32_bf16 a[120:123], a[168:171], a[156:159], a[120:123]\n" \
"v_mfma_f32_16x16x32_bf16 a[120:123], a[232:235], a[220:223], a[120:123]\n" \
"v_mfma_f32_16x16x32_bf16 a[116:119], a[164:167], a[156:159], a[116:119]\n" \
"v_mfma_f32_16x16x32_bf16 a[116:119], a[228:231], a[220:223], a[116:119]\n" \
"v_mfma_f32_16x16x32_bf16 a[112:115], a[160:163], a[156:159], a[112:115]\n" \
"v_mfma_f32_16x16x32_bf16 a[112:115], a[224:227], a[220:223], a[112:115]\n" \
"s_waitcnt vmcnt(4) lgkmcnt(0)\n" \
"s_barrier\n" \
"v_mfma_f32_16x16x32_bf16 v[52:55], a[176:179], a[128:131], v[52:55]\n" \
"s_add_u32 m0, s30, 0x0\n" \
"s_movk_i32 s84, 0x300\n" \
"v_mfma_f32_16x16x32_bf16 v[52:55], a[240:243], a[192:195], v[52:55]\n" \
"buffer_load_dwordx4 v22, s[64:67], s84 offen offset:0 lds\n" \
"v_mfma_f32_16x16x32_bf16 v[56:59], a[180:183], a[128:131], v[56:59]\n" \
"ds_read_b64_tr_b16 a[160:161], v32 offset:32768\n" \
"v_mfma_f32_16x16x32_bf16 v[56:59], a[244:247], a[192:195], v[56:59]\n" \
"s_movk_i32 s84, 0x1f00\n" \
"v_mfma_f32_16x16x32_bf16 v[60:63], a[184:187], a[128:131], v[60:63]\n" \
"buffer_load_dwordx4 v23, s[64:67], s84 offen offset:1024 lds\n" \
"v_mfma_f32_16x16x32_bf16 v[60:63], a[248:251], a[192:195], v[60:63]\n" \
"ds_read_b64_tr_b16 a[162:163], v32 offset:33792\n" \
"v_mfma_f32_16x16x32_bf16 v[64:67], a[188:191], a[128:131], v[64:67]\n" \
"ds_read_b64_tr_b16 a[224:225], v32 offset:40960\n" \
"v_mfma_f32_16x16x32_bf16 v[64:67], a[252:255], a[192:195], v[64:67]\n" \
"ds_read_b128 a[128:131], v28 offset:49152\n" \
"v_mfma_f32_16x16x32_bf16 v[80:83], a[188:191], a[132:135], v[80:83]\n" \
"ds_read_b128 a[192:195], v29 offset:49152\n" \
"v_mfma_f32_16x16x32_bf16 v[80:83], a[252:255], a[196:199], v[80:83]\n" \
"s_movk_i32 s84, 0x3b00\n" \
"v_mfma_f32_16x16x32_bf16 v[76:79], a[184:187], a[132:135], v[76:79]\n" \
"buffer_load_dwordx4 v22, s[64:67], s84 offen offset:2048 lds\n" \
"v_mfma_f32_16x16x32_bf16 v[76:79], a[248:251], a[196:199], v[76:79]\n" \
"s_movk_i32 s84, 0x5700\n" \
"v_mfma_f32_16x16x32_bf16 v[72:75], a[180:183], a[132:135], v[72:75]\n" \
"buffer_load_dwordx4 v23, s[64:67], s84 offen offset:3072 lds\n" \
"v_mfma_f32_16x16x32_bf16 v[72:75], a[244:247], a[196:199], v[72:75]\n" \
"s_add_u32 m0, s31, 0x0\n" \
"s_mov_b32 s84, 0x300000\n" \
"v_mfma_f32_16x16x32_bf16 v[68:71], a[176:179], a[132:135], v[68:71]\n" \
"buffer_load_dwordx4 v26, s[68:71], s84 offen offset:0 lds\n" \
"v_mfma_f32_16x16x32_bf16 v[68:71], a[240:243], a[196:199], v[68:71]\n" \
"ds_read_b128 a[132:135], v28 offset:51200\n" \
"v_mfma_f32_16x16x32_bf16 v[84:87], a[176:179], a[136:139], v[84:87]\n" \
"ds_read_b128 a[196:199], v29 offset:51200\n" \
"v_mfma_f32_16x16x32_bf16 v[84:87], a[240:243], a[200:203], v[84:87]\n" \
"s_addk_i32 s84, 0x7c00\n" \
"v_mfma_f32_16x16x32_bf16 v[88:91], a[180:183], a[136:139], v[88:91]\n" \
"buffer_load_dwordx4 v26, s[68:71], s84 offen offset:1024 lds\n" \
"v_mfma_f32_16x16x32_bf16 v[88:91], a[244:247], a[200:203], v[88:91]\n" \
"ds_read_b64_tr_b16 a[226:227], v32 offset:41984\n" \
"v_mfma_f32_16x16x32_bf16 v[92:95], a[184:187], a[136:139], v[92:95]\n" \
"s_addk_i32 s84, 0x7c00\n" \
"v_mfma_f32_16x16x32_bf16 v[92:95], a[248:251], a[200:203], v[92:95]\n" \
"buffer_load_dwordx4 v27, s[68:71], s84 offen offset:2048 lds\n" \
"v_mfma_f32_16x16x32_bf16 v[96:99], a[188:191], a[136:139], v[96:99]\n" \
"ds_read_b64_tr_b16 a[164:165], v33 offset:32768\n" \
"v_mfma_f32_16x16x32_bf16 v[96:99], a[252:255], a[200:203], v[96:99]\n" \
"ds_read_b128 a[136:139], v28 offset:53248\n" \
"v_mfma_f32_16x16x32_bf16 v[112:115], a[188:191], a[140:143], v[112:115]\n" \
"ds_read_b128 a[200:203], v29 offset:53248\n" \
"v_mfma_f32_16x16x32_bf16 v[112:115], a[252:255], a[204:207], v[112:115]\n" \
"s_addk_i32 s84, 0x7c00\n" \
"v_mfma_f32_16x16x32_bf16 v[108:111], a[184:187], a[140:143], v[108:111]\n" \
"buffer_load_dwordx4 v27, s[68:71], s84 offen offset:3072 lds\n" \
"v_mfma_f32_16x16x32_bf16 v[108:111], a[248:251], a[204:207], v[108:111]\n" \
"s_add_u32 m0, s30, 0x1000\n" \
"s_mov_b32 s84, 0x8300\n" \
"v_mfma_f32_16x16x32_bf16 v[104:107], a[180:183], a[140:143], v[104:107]\n" \
"buffer_load_dwordx4 v22, s[64:67], s84 offen offset:0 lds\n" \
"v_mfma_f32_16x16x32_bf16 v[104:107], a[244:247], a[204:207], v[104:107]\n" \
"ds_read_b64_tr_b16 a[166:167], v33 offset:33792\n" \
"v_mfma_f32_16x16x32_bf16 v[100:103], a[176:179], a[140:143], v[100:103]\n" \
"ds_read_b64_tr_b16 a[228:229], v33 offset:40960\n" \
"v_mfma_f32_16x16x32_bf16 v[100:103], a[240:243], a[204:207], v[100:103]\n" \
"ds_read_b128 a[140:143], v28 offset:55296\n" \
"v_mfma_f32_16x16x32_bf16 v[116:119], a[176:179], a[144:147], v[116:119]\n" \
"ds_read_b128 a[204:207], v29 offset:55296\n" \
"v_mfma_f32_16x16x32_bf16 v[116:119], a[240:243], a[208:211], v[116:119]\n" \
"s_addk_i32 s84, 0x1c00\n" \
"v_mfma_f32_16x16x32_bf16 v[120:123], a[180:183], a[144:147], v[120:123]\n" \
"buffer_load_dwordx4 v23, s[64:67], s84 offen offset:1024 lds\n" \
"v_mfma_f32_16x16x32_bf16 v[120:123], a[244:247], a[208:211], v[120:123]\n" \
"s_addk_i32 s84, 0x1c00\n" \
"v_mfma_f32_16x16x32_bf16 v[124:127], a[184:187], a[144:147], v[124:127]\n" \
"buffer_load_dwordx4 v22, s[64:67], s84 offen offset:2048 lds\n" \
"v_mfma_f32_16x16x32_bf16 v[124:127], a[248:251], a[208:211], v[124:127]\n" \
"s_addk_i32 s84, 0x1c00\n" \
"s_nop 0\n" \
"v_mfma_f32_16x16x32_bf16 v[128:131], a[188:191], a[144:147], v[128:131]\n" \
"buffer_load_dwordx4 v23, s[64:67], s84 offen offset:3072 lds\n" \
"v_mfma_f32_16x16x32_bf16 v[128:131], a[252:255], a[208:211], v[128:131]\n" \
"ds_read_b128 a[144:147], v28 offset:32768\n" \
"v_mfma_f32_16x16x32_bf16 v[144:147], a[188:191], a[148:151], v[144:147]\n" \
"ds_read_b128 a[208:211], v29 offset:32768\n" \
"v_mfma_f32_16x16x32_bf16 v[144:147], a[252:255], a[212:215], v[144:147]\n" \
"s_add_u32 m0, s31, 0x4000\n" \
"s_mov_b32 s84, 0x300000\n" \
"v_mfma_f32_16x16x32_bf16 v[140:143], a[184:187], a[148:151], v[140:143]\n" \
"buffer_load_dwordx4 v26, s[72:75], s84 offen offset:0 lds\n" \
"v_mfma_f32_16x16x32_bf16 v[140:143], a[248:251], a[212:215], v[140:143]\n" \
"ds_read_b64_tr_b16 a[230:231], v33 offset:41984\n" \
"v_mfma_f32_16x16x32_bf16 v[136:139], a[180:183], a[148:151], v[136:139]\n" \
"s_addk_i32 s84, 0x7c00\n" \
"s_nop 0\n" \
"v_mfma_f32_16x16x32_bf16 v[136:139], a[244:247], a[212:215], v[136:139]\n" \
"buffer_load_dwordx4 v26, s[72:75], s84 offen offset:1024 lds\n" \
"v_mfma_f32_16x16x32_bf16 v[132:135], a[176:179], a[148:151], v[132:135]\n" \
"ds_read_b64_tr_b16 a[168:169], v34 offset:32768\n" \
"v_mfma_f32_16x16x32_bf16 v[132:135], a[240:243], a[212:215], v[132:135]\n" \
"ds_read_b128 a[148:151], v28 offset:34816\n" \
"v_mfma_f32_16x16x32_bf16 v[148:151], a[176:179], a[152:155], v[148:151]\n" \
"ds_read_b128 a[212:215], v29 offset:34816\n" \
"v_mfma_f32_16x16x32_bf16 v[148:151], a[240:243], a[216:219], v[148:151]\n" \
"ds_read_b64_tr_b16 a[170:171], v34 offset:33792\n" \
"v_mfma_f32_16x16x32_bf16 v[152:155], a[180:183], a[152:155], v[152:155]\n" \
"ds_read_b64_tr_b16 a[232:233], v34 offset:40960\n" \
"v_mfma_f32_16x16x32_bf16 v[152:155], a[244:247], a[216:219], v[152:155]\n" \
"ds_read_b64_tr_b16 a[234:235], v34 offset:41984\n" \
"v_mfma_f32_16x16x32_bf16 v[156:159], a[184:187], a[152:155], v[156:159]\n" \
"ds_read_b64_tr_b16 a[172:173], v35 offset:32768\n" \
"v_mfma_f32_16x16x32_bf16 v[156:159], a[248:251], a[216:219], v[156:159]\n" \
"ds_read_b64_tr_b16 a[174:175], v35 offset:33792\n" \
"v_mfma_f32_16x16x32_bf16 v[160:163], a[188:191], a[152:155], v[160:163]\n" \
"ds_read_b64_tr_b16 a[236:237], v35 offset:40960\n" \
"v_mfma_f32_16x16x32_bf16 v[160:163], a[252:255], a[216:219], v[160:163]\n" \
"ds_read_b128 a[152:155], v28 offset:36864\n" \
"v_mfma_f32_16x16x32_bf16 v[176:179], a[188:191], a[156:159], v[176:179]\n" \
"ds_read_b128 a[216:219], v29 offset:36864\n" \
"v_mfma_f32_16x16x32_bf16 v[176:179], a[252:255], a[220:223], v[176:179]\n" \
"ds_read_b64_tr_b16 a[238:239], v35 offset:41984\n" \
"v_mfma_f32_16x16x32_bf16 v[172:175], a[184:187], a[156:159], v[172:175]\n" \
"v_mfma_f32_16x16x32_bf16 v[172:175], a[248:251], a[220:223], v[172:175]\n" \
"v_mfma_f32_16x16x32_bf16 v[168:171], a[180:183], a[156:159], v[168:171]\n" \
"v_mfma_f32_16x16x32_bf16 v[168:171], a[244:247], a[220:223], v[168:171]\n" \
"v_mfma_f32_16x16x32_bf16 v[164:167], a[176:179], a[156:159], v[164:167]\n" \
"v_mfma_f32_16x16x32_bf16 v[164:167], a[240:243], a[220:223], v[164:167]\n" \
"ds_read_b128 a[156:159], v28 offset:38912\n" \
"ds_read_b128 a[220:223], v29 offset:38912\n" \
"s_waitcnt lgkmcnt(15)\n" \
"v_mfma_f32_16x16x32_bf16 a[0:3], a[160:163], a[128:131], a[0:3]\n" \
"s_addk_i32 s84, 0x7c00\n" \
"v_mfma_f32_16x16x32_bf16 a[0:3], a[224:227], a[192:195], a[0:3]\n" \
"buffer_load_dwordx4 v27, s[72:75], s84 offen offset:2048 lds\n" \
"v_mfma_f32_16x16x32_bf16 a[4:7], a[164:167], a[128:131], a[4:7]\n" \
"ds_read_b64_tr_b16 a[176:177], v36 offset:32768\n" \
"s_waitcnt lgkmcnt(15)\n" \
"v_mfma_f32_16x16x32_bf16 a[4:7], a[228:231], a[192:195], a[4:7]\n" \
"s_addk_i32 s84, 0x7c00\n" \
"s_waitcnt lgkmcnt(11)\n" \
"v_mfma_f32_16x16x32_bf16 a[8:11], a[168:171], a[128:131], a[8:11]\n" \
"buffer_load_dwordx4 v27, s[72:75], s84 offen offset:3072 lds\n" \
"s_waitcnt lgkmcnt(9)\n" \
"v_mfma_f32_16x16x32_bf16 a[8:11], a[232:235], a[192:195], a[8:11]\n" \
"ds_read_b64_tr_b16 a[178:179], v36 offset:33792\n" \
"s_waitcnt lgkmcnt(8)\n" \
"v_mfma_f32_16x16x32_bf16 a[12:15], a[172:175], a[128:131], a[12:15]\n" \
"ds_read_b64_tr_b16 a[240:241], v36 offset:40960\n" \
"s_waitcnt lgkmcnt(5)\n" \
"v_mfma_f32_16x16x32_bf16 a[12:15], a[236:239], a[192:195], a[12:15]\n" \
"v_mfma_f32_16x16x32_bf16 a[28:31], a[172:175], a[132:135], a[28:31]\n" \
"v_mfma_f32_16x16x32_bf16 a[28:31], a[236:239], a[196:199], a[28:31]\n" \
"ds_read_b64_tr_b16 a[242:243], v36 offset:41984\n" \
"v_mfma_f32_16x16x32_bf16 a[24:27], a[168:171], a[132:135], a[24:27]\n" \
"v_mfma_f32_16x16x32_bf16 a[24:27], a[232:235], a[196:199], a[24:27]\n" \
"v_mfma_f32_16x16x32_bf16 a[20:23], a[164:167], a[132:135], a[20:23]\n" \
"v_mfma_f32_16x16x32_bf16 a[20:23], a[228:231], a[196:199], a[20:23]\n" \
"ds_read_b64_tr_b16 a[180:181], v37 offset:32768\n" \
"v_mfma_f32_16x16x32_bf16 a[16:19], a[160:163], a[132:135], a[16:19]\n" \
"v_mfma_f32_16x16x32_bf16 a[16:19], a[224:227], a[196:199], a[16:19]\n" \
"v_mfma_f32_16x16x32_bf16 a[32:35], a[160:163], a[136:139], a[32:35]\n" \
"ds_read_b64_tr_b16 a[182:183], v37 offset:33792\n" \
"v_mfma_f32_16x16x32_bf16 a[32:35], a[224:227], a[200:203], a[32:35]\n" \
"v_mfma_f32_16x16x32_bf16 a[36:39], a[164:167], a[136:139], a[36:39]\n" \
"v_lshlrev_b32_e32 v45, 3, v20\n" \
"v_add_u32_e32 v45, s31, v45\n" \
"v_mfma_f32_16x16x32_bf16 a[36:39], a[228:231], a[200:203], a[36:39]\n" \
"s_getpc_b64 s[86:87]\n" \
"s_sub_u32 s86, s86, 0xf800\n" \
"s_subb_u32 s87, s87, 0\n" \
"s_mov_b32 m0, s32\n" \
"s_nop 0\n" \
"v_mfma_f32_16x16x32_bf16 a[40:43], a[168:171], a[136:139], a[40:43]\n" \
"global_load_lds_dword v45, s[86:87]\n" \
"v_mfma_f32_16x16x32_bf16 a[40:43], a[232:235], a[200:203], a[40:43]\n" \
"ds_read_b64_tr_b16 a[244:245], v37 offset:40960\n" \
"v_mfma_f32_16x16x32_bf16 a[44:47], a[172:175], a[136:139], a[44:47]\n" \
"ds_read_b64_tr_b16 a[246:247], v37 offset:41984\n" \
"v_mfma_f32_16x16x32_bf16 a[44:47], a[236:239], a[200:203], a[44:47]\n" \
"v_mfma_f32_16x16x32_bf16 a[60:63], a[172:175], a[140:143], a[60:63]\n" \
"v_mfma_f32_16x16x32_bf16 a[60:63], a[236:239], a[204:207], a[60:63]\n" \
"v_mfma_f32_16x16x32_bf16 a[56:59], a[168:171], a[140:143], a[56:59]\n" \
"ds_read_b64_tr_b16 a[184:185], v38 offset:32768\n" \
"v_mfma_f32_16x16x32_bf16 a[56:59], a[232:235], a[204:207], a[56:59]\n" \
"v_mfma_f32_16x16x32_bf16 a[52:55], a[164:167], a[140:143], a[52:55]\n" \
"v_mfma_f32_16x16x32_bf16 a[52:55], a[228:231], a[204:207], a[52:55]\n" \
"ds_read_b64_tr_b16 a[186:187], v38 offset:33792\n" \
"v_mfma_f32_16x16x32_bf16 a[48:51], a[160:163], a[140:143], a[48:51]\n" \
"v_mfma_f32_16x16x32_bf16 a[48:51], a[224:227], a[204:207], a[48:51]\n" \
"v_mfma_f32_16x16x32_bf16 a[64:67], a[160:163], a[144:147], a[64:67]\n" \
"ds_read_b64_tr_b16 a[248:249], v38 offset:40960\n" \
"v_mfma_f32_16x16x32_bf16 a[64:67], a[224:227], a[208:211], a[64:67]\n" \
"v_mfma_f32_16x16x32_bf16 a[68:71], a[164:167], a[144:147], a[68:71]\n" \
"v_mfma_f32_16x16x32_bf16 a[68:71], a[228:231], a[208:211], a[68:71]\n" \
"ds_read_b64_tr_b16 a[250:251], v38 offset:41984\n" \
"v_mfma_f32_16x16x32_bf16 a[72:75], a[168:171], a[144:147], a[72:75]\n" \
"v_mfma_f32_16x16x32_bf16 a[72:75], a[232:235], a[208:211], a[72:75]\n" \
"v_mfma_f32_16x16x32_bf16 a[76:79], a[172:175], a[144:147], a[76:79]\n" \
"v_mfma_f32_16x16x32_bf16 a[76:79], a[236:239], a[208:211], a[76:79]\n" \
"ds_read_b64_tr_b16 a[188:189], v39 offset:32768\n" \
"v_mfma_f32_16x16x32_bf16 a[92:95], a[172:175], a[148:151], a[92:95]\n" \
"v_mfma_f32_16x16x32_bf16 a[92:95], a[236:239], a[212:215], a[92:95]\n" \
"v_mfma_f32_16x16x32_bf16 a[88:91], a[168:171], a[148:151], a[88:91]\n" \
"ds_read_b64_tr_b16 a[190:191], v39 offset:33792\n" \
"v_mfma_f32_16x16x32_bf16 a[88:91], a[232:235], a[212:215], a[88:91]\n" \
"v_mfma_f32_16x16x32_bf16 a[84:87], a[164:167], a[148:151], a[84:87]\n" \
"v_mfma_f32_16x16x32_bf16 a[84:87], a[228:231], a[212:215], a[84:87]\n" \
"ds_read_b64_tr_b16 a[252:253], v39 offset:40960\n" \
"v_mfma_f32_16x16x32_bf16 a[80:83], a[160:163], a[148:151], a[80:83]\n" \
"v_mfma_f32_16x16x32_bf16 a[80:83], a[224:227], a[212:215], a[80:83]\n" \
"v_mfma_f32_16x16x32_bf16 a[96:99], a[160:163], a[152:155], a[96:99]\n" \
"ds_read_b64_tr_b16 a[254:255], v39 offset:41984\n" \
"v_mfma_f32_16x16x32_bf16 a[96:99], a[224:227], a[216:219], a[96:99]\n" \
"v_mfma_f32_16x16x32_bf16 a[100:103], a[164:167], a[152:155], a[100:103]\n" \
"v_mfma_f32_16x16x32_bf16 a[100:103], a[228:231], a[216:219], a[100:103]\n" \
"v_mfma_f32_16x16x32_bf16 a[104:107], a[168:171], a[152:155], a[104:107]\n" \
"v_mfma_f32_16x16x32_bf16 a[104:107], a[232:235], a[216:219], a[104:107]\n" \
"v_mfma_f32_16x16x32_bf16 a[108:111], a[172:175], a[152:155], a[108:111]\n" \
"v_mfma_f32_16x16x32_bf16 a[108:111], a[236:239], a[216:219], a[108:111]\n" \
"s_waitcnt lgkmcnt(15)\n" \
"v_mfma_f32_16x16x32_bf16 a[124:127], a[172:175], a[156:159], a[124:127]\n" \
"v_mfma_f32_16x16x32_bf16 a[124:127], a[236:239], a[220:223], a[124:127]\n" \
"v_mfma_f32_16x16x32_bf16 a[120:123], a[168:171], a[156:159], a[120:123]\n" \
"v_mfma_f32_16x16x32_bf16 a[120:123], a[232:235], a[220:223], a[120:123]\n" \
"v_mfma_f32_16x16x32_bf16 a[116:119], a[164:167], a[156:159], a[116:119]\n" \
"v_mfma_f32_16x16x32_bf16 a[116:119], a[228:231], a[220:223], a[116:119]\n" \
"v_mfma_f32_16x16x32_bf16 a[112:115], a[160:163], a[156:159], a[112:115]\n" \
"v_mfma_f32_16x16x32_bf16 a[112:115], a[224:227], a[220:223], a[112:115]\n" \
"s_waitcnt vmcnt(1) lgkmcnt(0)\n" \
"s_barrier\n" \
"v_mov_b32_e32 v45, s62\n" \
"s_mov_b64 exec, 1\n" \
"buffer_store_dword v45, off, s[80:83], s37 offset:128 sc1\n" \
"s_mov_b64 exec, -1\n" \
"v_mfma_f32_16x16x32_bf16 v[52:55], a[176:179], a[128:131], v[52:55]\n" \
"s_add_u32 m0, s30, 0x8000\n" \
"s_movk_i32 s84, 0x380\n" \
"v_mfma_f32_16x16x32_bf16 v[52:55], a[240:243], a[192:195], v[52:55]\n" \
"buffer_load_dwordx4 v22, s[64:67], s84 offen offset:0 lds\n" \
"v_mfma_f32_16x16x32_bf16 v[56:59], a[180:183], a[128:131], v[56:59]\n" \
"ds_read_b64_tr_b16 a[160:161], v32 offset:0\n" \
"v_mfma_f32_16x16x32_bf16 v[56:59], a[244:247], a[192:195], v[56:59]\n" \
"s_movk_i32 s84, 0x1f80\n" \
"v_mfma_f32_16x16x32_bf16 v[60:63], a[184:187], a[128:131], v[60:63]\n" \
"buffer_load_dwordx4 v23, s[64:67], s84 offen offset:1024 lds\n" \
"v_mfma_f32_16x16x32_bf16 v[60:63], a[248:251], a[192:195], v[60:63]\n" \
"ds_read_b64_tr_b16 a[162:163], v32 offset:1024\n" \
"v_mfma_f32_16x16x32_bf16 v[64:67], a[188:191], a[128:131], v[64:67]\n" \
"ds_read_b64_tr_b16 a[224:225], v32 offset:8192\n" \
"v_mfma_f32_16x16x32_bf16 v[64:67], a[252:255], a[192:195], v[64:67]\n" \
"ds_read_b128 a[128:131], v28 offset:16384\n" \
"v_mfma_f32_16x16x32_bf16 v[80:83], a[188:191], a[132:135], v[80:83]\n" \
"ds_read_b128 a[192:195], v29 offset:16384\n" \
"v_mfma_f32_16x16x32_bf16 v[80:83], a[252:255], a[196:199], v[80:83]\n" \
"s_movk_i32 s84, 0x3b80\n" \
"v_mfma_f32_16x16x32_bf16 v[76:79], a[184:187], a[132:135], v[76:79]\n" \
"buffer_load_dwordx4 v22, s[64:67], s84 offen offset:2048 lds\n" \
"v_mfma_f32_16x16x32_bf16 v[76:79], a[248:251], a[196:199], v[76:79]\n" \
"s_movk_i32 s84, 0x5780\n" \
"v_mfma_f32_16x16x32_bf16 v[72:75], a[180:183], a[132:135], v[72:75]\n" \
"buffer_load_dwordx4 v23, s[64:67], s84 offen offset:3072 lds\n" \
"v_mfma_f32_16x16x32_bf16 v[72:75], a[244:247], a[196:199], v[72:75]\n" \
"s_add_u32 m0, s31, 0x8000\n" \
"s_mov_b32 s84, 0x380000\n" \
"v_mfma_f32_16x16x32_bf16 v[68:71], a[176:179], a[132:135], v[68:71]\n" \
"buffer_load_dwordx4 v26, s[68:71], s84 offen offset:0 lds\n" \
"v_mfma_f32_16x16x32_bf16 v[68:71], a[240:243], a[196:199], v[68:71]\n" \
"ds_read_b128 a[132:135], v28 offset:18432\n" \
"v_mfma_f32_16x16x32_bf16 v[84:87], a[176:179], a[136:139], v[84:87]\n" \
"ds_read_b128 a[196:199], v29 offset:18432\n" \
"v_mfma_f32_16x16x32_bf16 v[84:87], a[240:243], a[200:203], v[84:87]\n" \
"s_addk_i32 s84, 0x7c00\n" \
"v_mfma_f32_16x16x32_bf16 v[88:91], a[180:183], a[136:139], v[88:91]\n" \
"buffer_load_dwordx4 v26, s[68:71], s84 offen offset:1024 lds\n" \
"v_mfma_f32_16x16x32_bf16 v[88:91], a[244:247], a[200:203], v[88:91]\n" \
"ds_read_b64_tr_b16 a[226:227], v32 offset:9216\n" \
"v_mfma_f32_16x16x32_bf16 v[92:95], a[184:187], a[136:139], v[92:95]\n" \
"s_addk_i32 s84, 0x7c00\n" \
"v_mfma_f32_16x16x32_bf16 v[92:95], a[248:251], a[200:203], v[92:95]\n" \
"buffer_load_dwordx4 v27, s[68:71], s84 offen offset:2048 lds\n" \
"v_mfma_f32_16x16x32_bf16 v[96:99], a[188:191], a[136:139], v[96:99]\n" \
"ds_read_b64_tr_b16 a[164:165], v33 offset:0\n" \
"v_mfma_f32_16x16x32_bf16 v[96:99], a[252:255], a[200:203], v[96:99]\n" \
"ds_read_b128 a[136:139], v28 offset:20480\n" \
"v_mfma_f32_16x16x32_bf16 v[112:115], a[188:191], a[140:143], v[112:115]\n" \
"ds_read_b128 a[200:203], v29 offset:20480\n" \
"v_mfma_f32_16x16x32_bf16 v[112:115], a[252:255], a[204:207], v[112:115]\n" \
"s_addk_i32 s84, 0x7c00\n" \
"s_nop 0\n" \
"v_mfma_f32_16x16x32_bf16 v[108:111], a[184:187], a[140:143], v[108:111]\n" \
"buffer_load_dwordx4 v27, s[68:71], s84 offen offset:3072 lds\n" \
"v_mfma_f32_16x16x32_bf16 v[108:111], a[248:251], a[204:207], v[108:111]\n" \
"s_add_u32 m0, s30, 0x9000\n" \
"s_mov_b32 s84, 0x8380\n" \
"v_mfma_f32_16x16x32_bf16 v[104:107], a[180:183], a[140:143], v[104:107]\n" \
"buffer_load_dwordx4 v22, s[64:67], s84 offen offset:0 lds\n" \
"v_mfma_f32_16x16x32_bf16 v[104:107], a[244:247], a[204:207], v[104:107]\n" \
"ds_read_b64_tr_b16 a[166:167], v33 offset:1024\n" \
"v_mfma_f32_16x16x32_bf16 v[100:103], a[176:179], a[140:143], v[100:103]\n" \
"ds_read_b64_tr_b16 a[228:229], v33 offset:8192\n" \
"v_mfma_f32_16x16x32_bf16 v[100:103], a[240:243], a[204:207], v[100:103]\n" \
"ds_read_b128 a[140:143], v28 offset:22528\n" \
"v_mfma_f32_16x16x32_bf16 v[116:119], a[176:179], a[144:147], v[116:119]\n" \
"ds_read_b128 a[204:207], v29 offset:22528\n" \
"v_mfma_f32_16x16x32_bf16 v[116:119], a[240:243], a[208:211], v[116:119]\n" \
"s_addk_i32 s84, 0x1c00\n" \
"v_mfma_f32_16x16x32_bf16 v[120:123], a[180:183], a[144:147], v[120:123]\n" \
"buffer_load_dwordx4 v23, s[64:67], s84 offen offset:1024 lds\n" \
"v_mfma_f32_16x16x32_bf16 v[120:123], a[244:247], a[208:211], v[120:123]\n" \
"s_addk_i32 s84, 0x1c00\n" \
"v_mfma_f32_16x16x32_bf16 v[124:127], a[184:187], a[144:147], v[124:127]\n" \
"buffer_load_dwordx4 v22, s[64:67], s84 offen offset:2048 lds\n" \
"v_mfma_f32_16x16x32_bf16 v[124:127], a[248:251], a[208:211], v[124:127]\n" \
"s_addk_i32 s84, 0x1c00\n" \
"s_nop 0\n" \
"v_mfma_f32_16x16x32_bf16 v[128:131], a[188:191], a[144:147], v[128:131]\n" \
"buffer_load_dwordx4 v23, s[64:67], s84 offen offset:3072 lds\n" \
"v_mfma_f32_16x16x32_bf16 v[128:131], a[252:255], a[208:211], v[128:131]\n" \
"ds_read_b128 a[144:147], v28 offset:0\n" \
"v_mfma_f32_16x16x32_bf16 v[144:147], a[188:191], a[148:151], v[144:147]\n" \
"ds_read_b128 a[208:211], v29 offset:0\n" \
"v_mfma_f32_16x16x32_bf16 v[144:147], a[252:255], a[212:215], v[144:147]\n" \
"s_add_u32 m0, s31, 0xc000\n" \
"s_mov_b32 s84, 0x380000\n" \
"v_mfma_f32_16x16x32_bf16 v[140:143], a[184:187], a[148:151], v[140:143]\n" \
"buffer_load_dwordx4 v26, s[72:75], s84 offen offset:0 lds\n" \
"v_mfma_f32_16x16x32_bf16 v[140:143], a[248:251], a[212:215], v[140:143]\n" \
"ds_read_b64_tr_b16 a[230:231], v33 offset:9216\n" \
"v_mfma_f32_16x16x32_bf16 v[136:139], a[180:183], a[148:151], v[136:139]\n" \
"s_addk_i32 s84, 0x7c00\n" \
"s_nop 0\n" \
"v_mfma_f32_16x16x32_bf16 v[136:139], a[244:247], a[212:215], v[136:139]\n" \
"buffer_load_dwordx4 v26, s[72:75], s84 offen offset:1024 lds\n" \
"v_mfma_f32_16x16x32_bf16 v[132:135], a[176:179], a[148:151], v[132:135]\n" \
"ds_read_b64_tr_b16 a[168:169], v34 offset:0\n" \
"v_mfma_f32_16x16x32_bf16 v[132:135], a[240:243], a[212:215], v[132:135]\n" \
"ds_read_b128 a[148:151], v28 offset:2048\n" \
"v_mfma_f32_16x16x32_bf16 v[148:151], a[176:179], a[152:155], v[148:151]\n" \
"ds_read_b128 a[212:215], v29 offset:2048\n" \
"v_mfma_f32_16x16x32_bf16 v[148:151], a[240:243], a[216:219], v[148:151]\n" \
"ds_read_b64_tr_b16 a[170:171], v34 offset:1024\n" \
"v_mfma_f32_16x16x32_bf16 v[152:155], a[180:183], a[152:155], v[152:155]\n" \
"ds_read_b64_tr_b16 a[232:233], v34 offset:8192\n" \
"v_mfma_f32_16x16x32_bf16 v[152:155], a[244:247], a[216:219], v[152:155]\n" \
"ds_read_b64_tr_b16 a[234:235], v34 offset:9216\n" \
"v_mfma_f32_16x16x32_bf16 v[156:159], a[184:187], a[152:155], v[156:159]\n" \
"ds_read_b64_tr_b16 a[172:173], v35 offset:0\n" \
"v_mfma_f32_16x16x32_bf16 v[156:159], a[248:251], a[216:219], v[156:159]\n" \
"ds_read_b64_tr_b16 a[174:175], v35 offset:1024\n" \
"v_mfma_f32_16x16x32_bf16 v[160:163], a[188:191], a[152:155], v[160:163]\n" \
"ds_read_b64_tr_b16 a[236:237], v35 offset:8192\n" \
"v_mfma_f32_16x16x32_bf16 v[160:163], a[252:255], a[216:219], v[160:163]\n" \
"ds_read_b128 a[152:155], v28 offset:4096\n" \
"v_mfma_f32_16x16x32_bf16 v[176:179], a[188:191], a[156:159], v[176:179]\n" \
"ds_read_b128 a[216:219], v29 offset:4096\n" \
"v_mfma_f32_16x16x32_bf16 v[176:179], a[252:255], a[220:223], v[176:179]\n" \
"ds_read_b64_tr_b16 a[238:239], v35 offset:9216\n" \
"v_mfma_f32_16x16x32_bf16 v[172:175], a[184:187], a[156:159], v[172:175]\n" \
"v_mfma_f32_16x16x32_bf16 v[172:175], a[248:251], a[220:223], v[172:175]\n" \
"v_mfma_f32_16x16x32_bf16 v[168:171], a[180:183], a[156:159], v[168:171]\n" \
"v_mfma_f32_16x16x32_bf16 v[168:171], a[244:247], a[220:223], v[168:171]\n" \
"v_mfma_f32_16x16x32_bf16 v[164:167], a[176:179], a[156:159], v[164:167]\n" \
"v_mfma_f32_16x16x32_bf16 v[164:167], a[240:243], a[220:223], v[164:167]\n" \
"ds_read_b128 a[156:159], v28 offset:6144\n" \
"ds_read_b128 a[220:223], v29 offset:6144\n" \
"s_waitcnt lgkmcnt(15)\n" \
"v_mfma_f32_16x16x32_bf16 a[0:3], a[160:163], a[128:131], a[0:3]\n" \
"s_addk_i32 s84, 0x7c00\n" \
"v_mfma_f32_16x16x32_bf16 a[0:3], a[224:227], a[192:195], a[0:3]\n" \
"buffer_load_dwordx4 v27, s[72:75], s84 offen offset:2048 lds\n" \
"v_mfma_f32_16x16x32_bf16 a[4:7], a[164:167], a[128:131], a[4:7]\n" \
"ds_read_b64_tr_b16 a[176:177], v36 offset:0\n" \
"s_waitcnt lgkmcnt(15)\n" \
"v_mfma_f32_16x16x32_bf16 a[4:7], a[228:231], a[192:195], a[4:7]\n" \
"s_addk_i32 s84, 0x7c00\n" \
"s_waitcnt lgkmcnt(11)\n" \
"v_mfma_f32_16x16x32_bf16 a[8:11], a[168:171], a[128:131], a[8:11]\n" \
"buffer_load_dwordx4 v27, s[72:75], s84 offen offset:3072 lds\n" \
"s_waitcnt lgkmcnt(9)\n" \
"v_mfma_f32_16x16x32_bf16 a[8:11], a[232:235], a[192:195], a[8:11]\n" \
"ds_read_b64_tr_b16 a[178:179], v36 offset:1024\n" \
"s_waitcnt lgkmcnt(8)\n" \
"v_mfma_f32_16x16x32_bf16 a[12:15], a[172:175], a[128:131], a[12:15]\n" \
"ds_read_b64_tr_b16 a[240:241], v36 offset:8192\n" \
"s_waitcnt lgkmcnt(5)\n" \
"v_mfma_f32_16x16x32_bf16 a[12:15], a[236:239], a[192:195], a[12:15]\n" \
"v_mfma_f32_16x16x32_bf16 a[28:31], a[172:175], a[132:135], a[28:31]\n" \
"v_mfma_f32_16x16x32_bf16 a[28:31], a[236:239], a[196:199], a[28:31]\n" \
"ds_read_b64_tr_b16 a[242:243], v36 offset:9216\n" \
"v_mfma_f32_16x16x32_bf16 a[24:27], a[168:171], a[132:135], a[24:27]\n" \
"v_mfma_f32_16x16x32_bf16 a[24:27], a[232:235], a[196:199], a[24:27]\n" \
"v_mfma_f32_16x16x32_bf16 a[20:23], a[164:167], a[132:135], a[20:23]\n" \
"v_mfma_f32_16x16x32_bf16 a[20:23], a[228:231], a[196:199], a[20:23]\n" \
"ds_read_b64_tr_b16 a[180:181], v37 offset:0\n" \
"v_mfma_f32_16x16x32_bf16 a[16:19], a[160:163], a[132:135], a[16:19]\n" \
"v_mfma_f32_16x16x32_bf16 a[16:19], a[224:227], a[196:199], a[16:19]\n" \
"v_mfma_f32_16x16x32_bf16 a[32:35], a[160:163], a[136:139], a[32:35]\n" \
"v_bfe_u32 v44, v20, 2, 5\n" \
"ds_read_b64_tr_b16 a[182:183], v37 offset:1024\n" \
"v_mfma_f32_16x16x32_bf16 a[32:35], a[224:227], a[200:203], a[32:35]\n" \
"buffer_load_dword v44, v44, s[80:83], s38 offen offset:128 sc1\n" \
"v_mfma_f32_16x16x32_bf16 a[36:39], a[164:167], a[136:139], a[36:39]\n" \
"v_mfma_f32_16x16x32_bf16 a[36:39], a[228:231], a[200:203], a[36:39]\n" \
"ds_read_b64_tr_b16 a[244:245], v37 offset:8192\n" \
"v_mfma_f32_16x16x32_bf16 a[40:43], a[168:171], a[136:139], a[40:43]\n" \
"v_mfma_f32_16x16x32_bf16 a[40:43], a[232:235], a[200:203], a[40:43]\n" \
"v_mfma_f32_16x16x32_bf16 a[44:47], a[172:175], a[136:139], a[44:47]\n" \
"ds_read_b64_tr_b16 a[246:247], v37 offset:9216\n" \
"v_mfma_f32_16x16x32_bf16 a[44:47], a[236:239], a[200:203], a[44:47]\n" \
"v_mfma_f32_16x16x32_bf16 a[60:63], a[172:175], a[140:143], a[60:63]\n" \
"global_store_dwordx4 v49, v[0:3], s[28:29] offset:0 sc1\n" \
"v_mfma_f32_16x16x32_bf16 a[60:63], a[236:239], a[204:207], a[60:63]\n" \
"v_mfma_f32_16x16x32_bf16 a[56:59], a[168:171], a[140:143], a[56:59]\n" \
"ds_read_b64_tr_b16 a[184:185], v38 offset:0\n" \
"v_mfma_f32_16x16x32_bf16 a[56:59], a[232:235], a[204:207], a[56:59]\n" \
"v_mfma_f32_16x16x32_bf16 a[52:55], a[164:167], a[140:143], a[52:55]\n" \
"v_mfma_f32_16x16x32_bf16 a[52:55], a[228:231], a[204:207], a[52:55]\n" \
"ds_read_b64_tr_b16 a[186:187], v38 offset:1024\n" \
"v_mfma_f32_16x16x32_bf16 a[48:51], a[160:163], a[140:143], a[48:51]\n" \
"v_mfma_f32_16x16x32_bf16 a[48:51], a[224:227], a[204:207], a[48:51]\n" \
"v_mfma_f32_16x16x32_bf16 a[64:67], a[160:163], a[144:147], a[64:67]\n" \
"global_store_dwordx4 v49, v[0:3], s[28:29] offset:1024 sc1\n" \
"v_mfma_f32_16x16x32_bf16 a[64:67], a[224:227], a[208:211], a[64:67]\n" \
"ds_read_b64_tr_b16 a[248:249], v38 offset:8192\n" \
"v_mfma_f32_16x16x32_bf16 a[68:71], a[164:167], a[144:147], a[68:71]\n" \
"v_mfma_f32_16x16x32_bf16 a[68:71], a[228:231], a[208:211], a[68:71]\n" \
"ds_read_b64_tr_b16 a[250:251], v38 offset:9216\n" \
"v_mfma_f32_16x16x32_bf16 a[72:75], a[168:171], a[144:147], a[72:75]\n" \
"v_mfma_f32_16x16x32_bf16 a[72:75], a[232:235], a[208:211], a[72:75]\n" \
"v_mfma_f32_16x16x32_bf16 a[76:79], a[172:175], a[144:147], a[76:79]\n" \
"v_mfma_f32_16x16x32_bf16 a[76:79], a[236:239], a[208:211], a[76:79]\n" \
"ds_read_b64_tr_b16 a[188:189], v39 offset:0\n" \
"v_mfma_f32_16x16x32_bf16 a[92:95], a[172:175], a[148:151], a[92:95]\n" \
"s_mov_b64 exec, 0xffff\n" \
"global_store_dwordx4 v49, v[0:3], s[28:29] offset:2048 sc1\n" \
"s_mov_b64 exec, -1\n" \
"s_nop 0\n" \
"v_mfma_f32_16x16x32_bf16 a[92:95], a[236:239], a[212:215], a[92:95]\n" \
"v_mfma_f32_16x16x32_bf16 a[88:91], a[168:171], a[148:151], a[88:91]\n" \
"ds_read_b64_tr_b16 a[190:191], v39 offset:1024\n" \
"v_mfma_f32_16x16x32_bf16 a[88:91], a[232:235], a[212:215], a[88:91]\n" \
"v_mfma_f32_16x16x32_bf16 a[84:87], a[164:167], a[148:151], a[84:87]\n" \
"v_mfma_f32_16x16x32_bf16 a[84:87], a[228:231], a[212:215], a[84:87]\n" \
"ds_read_b64_tr_b16 a[252:253], v39 offset:8192\n" \
"v_mfma_f32_16x16x32_bf16 a[80:83], a[160:163], a[148:151], a[80:83]\n" \
"v_mfma_f32_16x16x32_bf16 a[80:83], a[224:227], a[212:215], a[80:83]\n" \
"v_mfma_f32_16x16x32_bf16 a[96:99], a[160:163], a[152:155], a[96:99]\n" \
"ds_read_b64_tr_b16 a[254:255], v39 offset:9216\n" \
"v_mfma_f32_16x16x32_bf16 a[96:99], a[224:227], a[216:219], a[96:99]\n" \
"v_mfma_f32_16x16x32_bf16 a[100:103], a[164:167], a[152:155], a[100:103]\n" \
"v_mfma_f32_16x16x32_bf16 a[100:103], a[228:231], a[216:219], a[100:103]\n" \
"v_mfma_f32_16x16x32_bf16 a[104:107], a[168:171], a[152:155], a[104:107]\n" \
"v_mfma_f32_16x16x32_bf16 a[104:107], a[232:235], a[216:219], a[104:107]\n" \
"v_mfma_f32_16x16x32_bf16 a[108:111], a[172:175], a[152:155], a[108:111]\n" \
"v_mfma_f32_16x16x32_bf16 a[108:111], a[236:239], a[216:219], a[108:111]\n" \
"s_waitcnt lgkmcnt(15)\n" \
"v_mfma_f32_16x16x32_bf16 a[124:127], a[172:175], a[156:159], a[124:127]\n" \
"v_mfma_f32_16x16x32_bf16 a[124:127], a[236:239], a[220:223], a[124:127]\n" \
"v_mfma_f32_16x16x32_bf16 a[120:123], a[168:171], a[156:159], a[120:123]\n" \
"v_mfma_f32_16x16x32_bf16 a[120:123], a[232:235], a[220:223], a[120:123]\n" \
"v_mfma_f32_16x16x32_bf16 a[116:119], a[164:167], a[156:159], a[116:119]\n" \
"v_mfma_f32_16x16x32_bf16 a[116:119], a[228:231], a[220:223], a[116:119]\n" \
"v_mfma_f32_16x16x32_bf16 a[112:115], a[160:163], a[156:159], a[112:115]\n" \
"v_mfma_f32_16x16x32_bf16 a[112:115], a[224:227], a[220:223], a[112:115]\n" \
"s_waitcnt vmcnt(3) lgkmcnt(0)\n" \
"s_barrier\n" \
"v_cmp_ne_u32_e32 vcc, s62, v44\n" \
"s_nop 4\n" \
"s_cbranch_vccnz L_poll60_%=\n" \
"L_pollr60_%=:\n" \
"s_mov_b32 s68, s12\n" \
"s_and_b32 s69, s13, 0xffff\n" \
"s_mov_b32 s70, 0x480000\n" \
"s_mov_b32 s71, 0x20000\n" \
"v_mfma_f32_16x16x32_bf16 v[52:55], a[176:179], a[128:131], v[52:55]\n" \
"s_add_u32 m0, s40, 0x0\n" \
"s_add_u32 s85, s39, 0x0\n" \
"s_nop 0\n" \
"v_mfma_f32_16x16x32_bf16 v[52:55], a[240:243], a[192:195], v[52:55]\n" \
"buffer_load_dwordx4 v20, s[80:83], s85 offen sc1 lds\n" \
"v_mfma_f32_16x16x32_bf16 v[56:59], a[180:183], a[128:131], v[56:59]\n" \
"ds_read_b64_tr_b16 a[160:161], v32 offset:32768\n" \
"v_mfma_f32_16x16x32_bf16 v[56:59], a[244:247], a[192:195], v[56:59]\n" \
"s_add_u32 m0, s40, 0x1000\n" \
"s_add_u32 s85, s39, 0x1000\n" \
"v_mfma_f32_16x16x32_bf16 v[60:63], a[184:187], a[128:131], v[60:63]\n" \
"buffer_load_dwordx4 v20, s[80:83], s85 offen sc1 lds\n" \
"v_mfma_f32_16x16x32_bf16 v[60:63], a[248:251], a[192:195], v[60:63]\n" \
".fill (L_pb + 16096 - .) / 4, 4, 0xBF800000\n" \
"L_pp4:\n" \
"s_cbranch_execz L_pret\n" \
"ds_read_b64_tr_b16 a[162:163], v32 offset:33792\n" \
"v_mfma_f32_16x16x32_bf16 v[64:67], a[188:191], a[128:131], v[64:67]\n" \
"ds_read_b64_tr_b16 a[224:225], v32 offset:40960\n" \
"v_mfma_f32_16x16x32_bf16 v[64:67], a[252:255], a[192:195], v[64:67]\n" \
"ds_read_b128 a[128:131], v28 offset:49152\n" \
"v_mfma_f32_16x16x32_bf16 v[80:83], a[188:191], a[132:135], v[80:83]\n" \
"ds_read_b128 a[192:195], v29 offset:49152\n" \
"v_mfma_f32_16x16x32_bf16 v[80:83], a[252:255], a[196:199], v[80:83]\n" \
"s_add_u32 m0, s40, 0x3000\n" \
"s_movk_i32 s84, 0x0\n" \
"v_mfma_f32_16x16x32_bf16 v[76:79], a[184:187], a[132:135], v[76:79]\n" \
"buffer_load_dwordx4 v21, s[76:79], s84 offen offset:0 lds\n" \
"v_mfma_f32_16x16x32_bf16 v[76:79], a[248:251], a[196:199], v[76:79]\n" \
"s_add_u32 m0, s30, 0x0\n" \
"s_movk_i32 s84, 0x0\n" \
"v_mfma_f32_16x16x32_bf16 v[72:75], a[180:183], a[132:135], v[72:75]\n" \
"buffer_load_dwordx4 v24, s[76:79], s84 offen offset:0 lds\n" \
"v_mfma_f32_16x16x32_bf16 v[72:75], a[244:247], a[196:199], v[72:75]\n" \
"ds_read_b64_tr_b16 a[226:227], v32 offset:41984\n" \
"v_mfma_f32_16x16x32_bf16 v[68:71], a[176:179], a[132:135], v[68:71]\n" \
"ds_read_b64_tr_b16 a[164:165], v33 offset:32768\n" \
"v_mfma_f32_16x16x32_bf16 v[68:71], a[240:243], a[196:199], v[68:71]\n" \
"ds_read_b128 a[132:135], v28 offset:51200\n" \
"v_mfma_f32_16x16x32_bf16 v[84:87], a[176:179], a[136:139], v[84:87]\n" \
"ds_read_b128 a[196:199], v29 offset:51200\n" \
"v_mfma_f32_16x16x32_bf16 v[84:87], a[240:243], a[200:203], v[84:87]\n" \
"s_movk_i32 s84, 0x2000\n" \
"v_mfma_f32_16x16x32_bf16 v[88:91], a[180:183], a[136:139], v[88:91]\n" \
"buffer_load_dwordx4 v25, s[76:79], s84 offen offset:1024 lds\n" \
"v_mfma_f32_16x16x32_bf16 v[88:91], a[244:247], a[200:203], v[88:91]\n" \
"s_movk_i32 s84, 0x4000\n" \
"v_mfma_f32_16x16x32_bf16 v[92:95], a[184:187], a[136:139], v[92:95]\n" \
"buffer_load_dwordx4 v24, s[76:79], s84 offen offset:2048 lds\n" \
"v_mfma_f32_16x16x32_bf16 v[92:95], a[248:251], a[200:203], v[92:95]\n" \
"ds_read_b64_tr_b16 a[166:167], v33 offset:33792\n" \
"v_mfma_f32_16x16x32_bf16 v[96:99], a[188:191], a[136:139], v[96:99]\n" \
"ds_read_b64_tr_b16 a[228:229], v33 offset:40960\n" \
"v_mfma_f32_16x16x32_bf16 v[96:99], a[252:255], a[200:203], v[96:99]\n" \
"ds_read_b128 a[136:139], v28 offset:53248\n" \
"v_mfma_f32_16x16x32_bf16 v[112:115], a[188:191], a[140:143], v[112:115]\n" \
"ds_read_b128 a[200:203], v29 offset:53248\n" \
"v_mfma_f32_16x16x32_bf16 v[112:115], a[252:255], a[204:207], v[112:115]\n" \
"s_movk_i32 s84, 0x6000\n" \
"v_mfma_f32_16x16x32_bf16 v[108:111], a[184:187], a[140:143], v[108:111]\n" \
"buffer_load_dwordx4 v25, s[76:79], s84 offen offset:3072 lds\n" \
"v_mfma_f32_16x16x32_bf16 v[108:111], a[248:251], a[204:207], v[108:111]\n" \
"s_add_u32 m0, s31, 0x0\n" \
"s_movk_i32 s84, 0x0\n" \
"v_mfma_f32_16x16x32_bf16 v[104:107], a[180:183], a[140:143], v[104:107]\n" \
"buffer_load_dwordx4 v26, s[68:71], s84 offen offset:0 lds\n" \
"v_mfma_f32_16x16x32_bf16 v[104:107], a[244:247], a[204:207], v[104:107]\n" \
"s_movk_i32 s84, 0x7c00\n" \
"v_mfma_f32_16x16x32_bf16 v[100:103], a[176:179], a[140:143], v[100:103]\n" \
"buffer_load_dwordx4 v26, s[68:71], s84 offen offset:1024 lds\n" \
"v_mfma_f32_16x16x32_bf16 v[100:103], a[240:243], a[204:207], v[100:103]\n" \
"ds_read_b128 a[140:143], v28 offset:55296\n" \
"v_mfma_f32_16x16x32_bf16 v[116:119], a[176:179], a[144:147], v[116:119]\n" \
"ds_read_b128 a[204:207], v29 offset:55296\n" \
"v_mfma_f32_16x16x32_bf16 v[116:119], a[240:243], a[208:211], v[116:119]\n" \
"s_addk_i32 s84, 0x7c00\n" \
"v_mfma_f32_16x16x32_bf16 v[120:123], a[180:183], a[144:147], v[120:123]\n" \
"buffer_load_dwordx4 v27, s[68:71], s84 offen offset:2048 lds\n" \
"v_mfma_f32_16x16x32_bf16 v[120:123], a[244:247], a[208:211], v[120:123]\n" \
"ds_read_b64_tr_b16 a[230:231], v33 offset:41984\n" \
"v_mfma_f32_16x16x32_bf16 v[124:127], a[184:187], a[144:147], v[124:127]\n" \
"s_addk_i32 s84, 0x7c00\n" \
"v_mfma_f32_16x16x32_bf16 v[124:127], a[248:251], a[208:211], v[124:127]\n" \
"buffer_load_dwordx4 v27, s[68:71], s84 offen offset:3072 lds\n" \
"v_mfma_f32_16x16x32_bf16 v[128:131], a[188:191], a[144:147], v[128:131]\n" \
"ds_read_b64_tr_b16 a[168:169], v34 offset:32768\n" \
"v_mfma_f32_16x16x32_bf16 v[128:131], a[252:255], a[208:211], v[128:131]\n" \
"ds_read_b128 a[144:147], v28 offset:32768\n" \
"v_mfma_f32_16x16x32_bf16 v[144:147], a[188:191], a[148:151], v[144:147]\n" \
"ds_read_b128 a[208:211], v29 offset:32768\n" \
"v_mfma_f32_16x16x32_bf16 v[144:147], a[252:255], a[212:215], v[144:147]\n" \
"s_add_u32 m0, s30, 0x1000\n" \
"s_mov_b32 s84, 0x9000\n" \
"v_mfma_f32_16x16x32_bf16 v[140:143], a[184:187], a[148:151], v[140:143]\n" \
"buffer_load_dwordx4 v24, s[76:79], s84 offen offset:0 lds\n" \
"v_mfma_f32_16x16x32_bf16 v[140:143], a[248:251], a[212:215], v[140:143]\n" \
"ds_read_b64_tr_b16 a[170:171], v34 offset:33792\n" \
"v_mfma_f32_16x16x32_bf16 v[136:139], a[180:183], a[148:151], v[136:139]\n" \
"s_addk_i32 s84, 0x2000\n" \
"s_nop 0\n" \
"v_mfma_f32_16x16x32_bf16 v[136:139], a[244:247], a[212:215], v[136:139]\n" \
"buffer_load_dwordx4 v25, s[76:79], s84 offen offset:1024 lds\n" \
"v_mfma_f32_16x16x32_bf16 v[132:135], a[176:179], a[148:151], v[132:135]\n" \
"ds_read_b64_tr_b16 a[232:233], v34 offset:40960\n" \
"v_mfma_f32_16x16x32_bf16 v[132:135], a[240:243], a[212:215], v[132:135]\n" \
"ds_read_b128 a[148:151], v28 offset:34816\n" \
"v_mfma_f32_16x16x32_bf16 v[148:151], a[176:179], a[152:155], v[148:151]\n" \
"ds_read_b128 a[212:215], v29 offset:34816\n" \
"v_mfma_f32_16x16x32_bf16 v[148:151], a[240:243], a[216:219], v[148:151]\n" \
"ds_read_b64_tr_b16 a[234:235], v34 offset:41984\n" \
"v_mfma_f32_16x16x32_bf16 v[152:155], a[180:183], a[152:155], v[152:155]\n" \
"ds_read_b64_tr_b16 a[172:173], v35 offset:32768\n" \
"v_mfma_f32_16x16x32_bf16 v[152:155], a[244:247], a[216:219], v[152:155]\n" \
"ds_read_b64_tr_b16 a[174:175], v35 offset:33792\n" \
"v_mfma_f32_16x16x32_bf16 v[156:159], a[184:187], a[152:155], v[156:159]\n" \
"ds_read_b64_tr_b16 a[236:237], v35 offset:40960\n" \
"v_mfma_f32_16x16x32_bf16 v[156:159], a[248:251], a[216:219], v[156:159]\n" \
"ds_read_b64_tr_b16 a[238:239], v35 offset:41984\n" \
"v_mfma_f32_16x16x32_bf16 v[160:163], a[188:191], a[152:155], v[160:163]\n" \
"v_mfma_f32_16x16x32_bf16 v[160:163], a[252:255], a[216:219], v[160:163]\n" \
"ds_read_b128 a[152:155], v28 offset:36864\n" \
"v_mfma_f32_16x16x32_bf16 v[176:179], a[188:191], a[156:159], v[176:179]\n" \
"ds_read_b128 a[216:219], v29 offset:36864\n" \
"v_mfma_f32_16x16x32_bf16 v[176:179], a[252:255], a[220:223], v[176:179]\n" \
"v_mfma_f32_16x16x32_bf16 v[172:175], a[184:187], a[156:159], v[172:175]\n" \
"v_mfma_f32_16x16x32_bf16 v[172:175], a[248:251], a[220:223], v[172:175]\n" \
"v_mfma_f32_16x16x32_bf16 v[168:171], a[180:183], a[156:159], v[168:171]\n" \
"v_mfma_f32_16x16x32_bf16 v[168:171], a[244:247], a[220:223], v[168:171]\n" \
"v_mfma_f32_16x16x32_bf16 v[164:167], a[176:179], a[156:159], v[164:167]\n" \
"v_mfma_f32_16x16x32_bf16 v[164:167], a[240:243], a[220:223], v[164:167]\n" \
"ds_read_b128 a[156:159], v28 offset:38912\n" \
"ds_read_b128 a[220:223], v29 offset:38912\n" \
"s_waitcnt lgkmcnt(15)\n" \
"v_mfma_f32_16x16x32_bf16 a[0:3], a[160:163], a[128:131], a[0:3]\n" \
"s_addk_i32 s84, 0x2000\n" \
"v_mfma_f32_16x16x32_bf16 a[0:3], a[224:227], a[192:195], a[0:3]\n" \
"buffer_load_dwordx4 v24, s[76:79], s84 offen offset:2048 lds\n" \
"v_mfma_f32_16x16x32_bf16 a[4:7], a[164:167], a[128:131], a[4:7]\n" \
"ds_read_b64_tr_b16 a[176:177], v36 offset:32768\n" \
"v_mfma_f32_16x16x32_bf16 a[4:7], a[228:231], a[192:195], a[4:7]\n" \
"s_addk_i32 s84, 0x2000\n" \
"s_waitcnt lgkmcnt(13)\n" \
"v_mfma_f32_16x16x32_bf16 a[8:11], a[168:171], a[128:131], a[8:11]\n" \
"buffer_load_dwordx4 v25, s[76:79], s84 offen offset:3072 lds\n" \
"s_waitcnt lgkmcnt(9)\n" \
"v_mfma_f32_16x16x32_bf16 a[8:11], a[232:235], a[192:195], a[8:11]\n" \
"ds_read_b64_tr_b16 a[178:179], v36 offset:33792\n" \
"s_waitcnt lgkmcnt(8)\n" \
"v_mfma_f32_16x16x32_bf16 a[12:15], a[172:175], a[128:131], a[12:15]\n" \
"ds_read_b64_tr_b16 a[240:241], v36 offset:40960\n" \
"v_accvgpr_read_b32 v184, a0\n" \
"s_waitcnt lgkmcnt(7)\n" \
"s_nop 0\n" \
"v_mfma_f32_16x16x32_bf16 a[12:15], a[236:239], a[192:195], a[12:15]\n" \
"v_accvgpr_read_b32 v185, a1\n" \
"v_accvgpr_read_b32 v186, a2\n" \
"v_mfma_f32_16x16x32_bf16 a[28:31], a[172:175], a[132:135], a[28:31]\n" \
"v_accvgpr_read_b32 v187, a3\n" \
"v_accvgpr_read_b32 v180, a4\n" \
"v_mfma_f32_16x16x32_bf16 a[28:31], a[236:239], a[196:199], a[28:31]\n" \
"ds_read_b64_tr_b16 a[242:243], v36 offset:41984\n" \
"v_accvgpr_read_b32 v181, a5\n" \
"v_mfma_f32_16x16x32_bf16 a[24:27], a[168:171], a[132:135], a[24:27]\n" \
"v_accvgpr_read_b32 v182, a6\n" \
"v_accvgpr_read_b32 v183, a7\n" \
"v_mfma_f32_16x16x32_bf16 a[24:27], a[232:235], a[196:199], a[24:27]\n" \
"v_cvt_pk_bf16_f32 v184, v184, v185\n" \
"v_cvt_pk_bf16_f32 v185, v186, v187\n" \
"v_mfma_f32_16x16x32_bf16 a[20:23], a[164:167], a[132:135], a[20:23]\n" \
"v_cvt_pk_bf16_f32 v186, v180, v181\n" \
"v_cvt_pk_bf16_f32 v187, v182, v183\n" \
"v_mfma_f32_16x16x32_bf16 a[20:23], a[228:231], a[196:199], a[20:23]\n" \
"ds_read_b64_tr_b16 a[180:181], v37 offset:32768\n" \
"v_accvgpr_read_b32 v188, a8\n" \
"v_mfma_f32_16x16x32_bf16 a[16:19], a[160:163], a[132:135], a[16:19]\n" \
"v_accvgpr_read_b32 v189, a9\n" \
"v_accvgpr_read_b32 v190, a10\n" \
"v_mfma_f32_16x16x32_bf16 a[16:19], a[224:227], a[196:199], a[16:19]\n" \
"v_accvgpr_read_b32 v191, a11\n" \
"v_accvgpr_read_b32 v180, a12\n" \
"v_mfma_f32_16x16x32_bf16 a[32:35], a[160:163], a[136:139], a[32:35]\n" \
"ds_read_b64_tr_b16 a[182:183], v37 offset:33792\n" \
"v_accvgpr_read_b32 v181, a13\n" \
"v_mfma_f32_16x16x32_bf16 a[32:35], a[224:227], a[200:203], a[32:35]\n" \
"v_accvgpr_read_b32 v182, a14\n" \
"v_accvgpr_read_b32 v183, a15\n" \
"v_mfma_f32_16x16x32_bf16 a[36:39], a[164:167], a[136:139], a[36:39]\n" \
"v_lshlrev_b32_e32 v45, 3, v20\n" \
"v_add_u32_e32 v45, s31, v45\n" \
"v_mfma_f32_16x16x32_bf16 a[36:39], a[228:231], a[200:203], a[36:39]\n" \
"s_getpc_b64 s[86:87]\n" \
"s_sub_u32 s86, s86, 0xf800\n" \
"s_subb_u32 s87, s87, 0\n" \
"s_mov_b32 m0, s32\n" \
"s_nop 0\n" \
"v_mfma_f32_16x16x32_bf16 a[40:43], a[168:171], a[136:139], a[40:43]\n" \
"global_load_lds_dword v45, s[86:87]\n" \
"v_mfma_f32_16x16x32_bf16 a[40:43], a[232:235], a[200:203], a[40:43]\n" \
"ds_read_b64_tr_b16 a[244:245], v37 offset:40960\n" \
"v_cvt_pk_bf16_f32 v188, v188, v189\n" \
"v_mfma_f32_16x16x32_bf16 a[44:47], a[172:175], a[136:139], a[44:47]\n" \
"ds_read_b64_tr_b16 a[246:247], v37 offset:41984\n" \
"v_cvt_pk_bf16_f32 v189, v190, v191\n" \
"v_mfma_f32_16x16x32_bf16 a[44:47], a[236:239], a[200:203], a[44:47]\n" \
"v_cvt_pk_bf16_f32 v190, v180, v181\n" \
"v_cvt_pk_bf16_f32 v191, v182, v183\n" \
"v_mfma_f32_16x16x32_bf16 a[60:63], a[172:175], a[140:143], a[60:63]\n" \
"v_accvgpr_read_b32 v192, a24\n" \
"v_accvgpr_read_b32 v193, a25\n" \
"v_mfma_f32_16x16x32_bf16 a[60:63], a[236:239], a[204:207], a[60:63]\n" \
"v_accvgpr_read_b32 v194, a26\n" \
"v_accvgpr_read_b32 v195, a27\n" \
"v_mfma_f32_16x16x32_bf16 a[56:59], a[168:171], a[140:143], a[56:59]\n" \
"ds_read_b64_tr_b16 a[184:185], v38 offset:32768\n" \
"v_accvgpr_read_b32 v180, a28\n" \
"v_mfma_f32_16x16x32_bf16 a[56:59], a[232:235], a[204:207], a[56:59]\n" \
"v_accvgpr_read_b32 v181, a29\n" \
"v_accvgpr_read_b32 v182, a30\n" \
"v_mfma_f32_16x16x32_bf16 a[52:55], a[164:167], a[140:143], a[52:55]\n" \
"v_accvgpr_read_b32 v183, a31\n" \
"v_cvt_pk_bf16_f32 v192, v192, v193\n" \
"v_mfma_f32_16x16x32_bf16 a[52:55], a[228:231], a[204:207], a[52:55]\n" \
"ds_read_b64_tr_b16 a[186:187], v38 offset:33792\n" \
"v_cvt_pk_bf16_f32 v193, v194, v195\n" \
"v_mfma_f32_16x16x32_bf16 a[48:51], a[160:163], a[140:143], a[48:51]\n" \
"v_cvt_pk_bf16_f32 v194, v180, v181\n" \
"v_cvt_pk_bf16_f32 v195, v182, v183\n" \
"v_mfma_f32_16x16x32_bf16 a[48:51], a[224:227], a[204:207], a[48:51]\n" \
"v_accvgpr_read_b32 v196, a16\n" \
"v_accvgpr_read_b32 v197, a17\n" \
"v_mfma_f32_16x16x32_bf16 a[64:67], a[160:163], a[144:147], a[64:67]\n" \
"ds_read_b64_tr_b16 a[248:249], v38 offset:40960\n" \
"v_accvgpr_read_b32 v198, a18\n" \
"v_mfma_f32_16x16x32_bf16 a[64:67], a[224:227], a[208:211], a[64:67]\n" \
"v_accvgpr_read_b32 v199, a19\n" \
"v_accvgpr_read_b32 v180, a20\n" \
"v_mfma_f32_16x16x32_bf16 a[68:71], a[164:167], a[144:147], a[68:71]\n" \
"v_accvgpr_read_b32 v181, a21\n" \
"v_accvgpr_read_b32 v182, a22\n" \
"v_mfma_f32_16x16x32_bf16 a[68:71], a[228:231], a[208:211], a[68:71]\n" \
"ds_read_b64_tr_b16 a[250:251], v38 offset:41984\n" \
"v_accvgpr_read_b32 v183, a23\n" \
"v_mfma_f32_16x16x32_bf16 a[72:75], a[168:171], a[144:147], a[72:75]\n" \
"v_cvt_pk_bf16_f32 v196, v196, v197\n" \
"v_cvt_pk_bf16_f32 v197, v198, v199\n" \
"v_mfma_f32_16x16x32_bf16 a[72:75], a[232:235], a[208:211], a[72:75]\n" \
"v_cvt_pk_bf16_f32 v198, v180, v181\n" \
"v_cvt_pk_bf16_f32 v199, v182, v183\n" \
"v_mfma_f32_16x16x32_bf16 a[76:79], a[172:175], a[144:147], a[76:79]\n" \
"v_accvgpr_read_b32 v200, a32\n" \
"v_accvgpr_read_b32 v201, a33\n" \
"v_mfma_f32_16x16x32_bf16 a[76:79], a[236:239], a[208:211], a[76:79]\n" \
"ds_read_b64_tr_b16 a[188:189], v39 offset:32768\n" \
"v_accvgpr_read_b32 v202, a34\n" \
"v_mfma_f32_16x16x32_bf16 a[92:95], a[172:175], a[148:151], a[92:95]\n" \
"s_add_u32 s89, s41, 0x0\n" \
"s_nop 0\n" \
"buffer_store_dwordx4 v[184:187], v20, s[80:83], s89 offen offset:0 sc1\n" \
"v_mfma_f32_16x16x32_bf16 a[92:95], a[236:239], a[212:215], a[92:95]\n" \
"v_accvgpr_read_b32 v203, a35\n" \
"v_accvgpr_read_b32 v180, a36\n" \
"v_mfma_f32_16x16x32_bf16 a[88:91], a[168:171], a[148:151], a[88:91]\n" \
"ds_read_b64_tr_b16 a[190:191], v39 offset:33792\n" \
"v_accvgpr_read_b32 v181, a37\n" \
"v_mfma_f32_16x16x32_bf16 a[88:91], a[232:235], a[212:215], a[88:91]\n" \
"v_accvgpr_read_b32 v182, a38\n" \
"v_accvgpr_read_b32 v183, a39\n" \
"v_mfma_f32_16x16x32_bf16 a[84:87], a[164:167], a[148:151], a[84:87]\n" \
"v_cvt_pk_bf16_f32 v200, v200, v201\n" \
"v_cvt_pk_bf16_f32 v201, v202, v203\n" \
"v_mfma_f32_16x16x32_bf16 a[84:87], a[228:231], a[212:215], a[84:87]\n" \
"ds_read_b64_tr_b16 a[252:253], v39 offset:40960\n" \
"v_cvt_pk_bf16_f32 v202, v180, v181\n" \
"v_mfma_f32_16x16x32_bf16 a[80:83], a[160:163], a[148:151], a[80:83]\n" \
"s_add_u32 s88, s41, 0x0\n" \
"buffer_store_dwordx4 v[188:191], v20, s[80:83], s88 offen offset:1024 sc1\n" \
"v_mfma_f32_16x16x32_bf16 a[80:83], a[224:227], a[212:215], a[80:83]\n" \
"v_cvt_pk_bf16_f32 v203, v182, v183\n" \
"v_accvgpr_read_b32 v204, a40\n" \
"s_waitcnt lgkmcnt(15)\n" \
"v_mfma_f32_16x16x32_bf16 a[96:99], a[160:163], a[152:155], a[96:99]\n" \
"ds_read_b64_tr_b16 a[254:255], v39 offset:41984\n" \
"v_accvgpr_read_b32 v205, a41\n" \
"v_mfma_f32_16x16x32_bf16 a[96:99], a[224:227], a[216:219], a[96:99]\n" \
"v_accvgpr_read_b32 v206, a42\n" \
"v_accvgpr_read_b32 v207, a43\n" \
"v_mfma_f32_16x16x32_bf16 a[100:103], a[164:167], a[152:155], a[100:103]\n" \
"v_accvgpr_read_b32 v180, a44\n" \
"v_accvgpr_read_b32 v181, a45\n" \
"v_mfma_f32_16x16x32_bf16 a[100:103], a[228:231], a[216:219], a[100:103]\n" \
"v_accvgpr_read_b32 v182, a46\n" \
"v_accvgpr_read_b32 v183, a47\n" \
"v_mfma_f32_16x16x32_bf16 a[104:107], a[168:171], a[152:155], a[104:107]\n" \
"s_add_u32 s89, s41, 0x1000\n" \
"buffer_store_dwordx4 v[192:195], v20, s[80:83], s89 offen offset:1024 sc1\n" \
"v_mfma_f32_16x16x32_bf16 a[104:107], a[232:235], a[216:219], a[104:107]\n" \
"v_cvt_pk_bf16_f32 v204, v204, v205\n" \
"v_cvt_pk_bf16_f32 v205, v206, v207\n" \
"v_mfma_f32_16x16x32_bf16 a[108:111], a[172:175], a[152:155], a[108:111]\n" \
"v_cvt_pk_bf16_f32 v206, v180, v181\n" \
"v_cvt_pk_bf16_f32 v207, v182, v183\n" \
"v_mfma_f32_16x16x32_bf16 a[108:111], a[236:239], a[216:219], a[108:111]\n" \
"v_accvgpr_read_b32 v208, a56\n" \
"v_accvgpr_read_b32 v209, a57\n" \
"v_mfma_f32_16x16x32_bf16 a[124:127], a[172:175], a[156:159], a[124:127]\n" \
"v_accvgpr_read_b32 v210, a58\n" \
"v_accvgpr_read_b32 v211, a59\n" \
"v_mfma_f32_16x16x32_bf16 a[124:127], a[236:239], a[220:223], a[124:127]\n" \
"v_accvgpr_read_b32 v180, a60\n" \
"v_accvgpr_read_b32 v181, a61\n" \
"v_mfma_f32_16x16x32_bf16 a[120:123], a[168:171], a[156:159], a[120:123]\n" \
"s_add_u32 s88, s41, 0x1000\n" \
"buffer_store_dwordx4 v[196:199], v20, s[80:83], s88 offen offset:0 sc1\n" \
"v_mfma_f32_16x16x32_bf16 a[120:123], a[232:235], a[220:223], a[120:123]\n" \
"v_accvgpr_read_b32 v182, a62\n" \
"v_accvgpr_read_b32 v183, a63\n" \
"v_mfma_f32_16x16x32_bf16 a[116:119], a[164:167], a[156:159], a[116:119]\n" \
"v_cvt_pk_bf16_f32 v208, v208, v209\n" \
"v_cvt_pk_bf16_f32 v209, v210, v211\n" \
"v_mfma_f32_16x16x32_bf16 a[116:119], a[228:231], a[220:223], a[116:119]\n" \
"v_cvt_pk_bf16_f32 v210, v180, v181\n" \
"v_cvt_pk_bf16_f32 v211, v182, v183\n" \
"v_mfma_f32_16x16x32_bf16 a[112:115], a[160:163], a[156:159], a[112:115]\n" \
"v_accvgpr_read_b32 v212, a48\n" \
"v_accvgpr_read_b32 v213, a49\n" \
"v_mfma_f32_16x16x32_bf16 a[112:115], a[224:227], a[220:223], a[112:115]\n" \
"v_accvgpr_read_b32 v214, a50\n" \
"v_accvgpr_read_b32 v215, a51\n" \
"s_waitcnt vmcnt(5) lgkmcnt(0)\n" \
"s_barrier\n" \
"v_mov_b32_e32 v36, v32\n" \
"v_xor_b32_e32 v32, 0x0, v48\n" \
"v_mov_b32_e32 v37, v33\n" \
"v_xor_b32_e32 v33, 0x20, v48\n" \
"v_mov_b32_e32 v38, v34\n" \
"v_xor_b32_e32 v34, 0x40, v48\n" \
"v_mov_b32_e64 v39, v35\n" \
"v_xor_b32_e32 v35, 0x60, v48\n" \
"v_add_u32_e32 v22, s44, v28\n" \
"v_add_u32_e32 v28, s43, v28\n" \
"v_add_u32_e32 v23, s44, v29\n" \
"v_add_u32_e32 v29, s43, v29\n" \
"v_mfma_f32_16x16x32_bf16 v[52:55], a[176:179], a[128:131], v[52:55]\n" \
"s_add_u32 m0, s40, 0x8000\n" \
"s_add_u32 s85, s39, 0x2000\n" \
"v_mfma_f32_16x16x32_bf16 v[52:55], a[240:243], a[192:195], v[52:55]\n" \
"buffer_load_dwordx4 v20, s[80:83], s85 offen sc1 lds\n" \
"v_mfma_f32_16x16x32_bf16 v[56:59], a[180:183], a[128:131], v[56:59]\n" \
"ds_read_b64_tr_b16 a[160:161], v32 offset:0\n" \
"v_accvgpr_read_b32 v180, a52\n" \
"v_mfma_f32_16x16x32_bf16 v[56:59], a[244:247], a[192:195], v[56:59]\n" \
"s_add_u32 m0, s40, 0x9000\n" \
"s_add_u32 s85, s39, 0x3000\n" \
"v_mfma_f32_16x16x32_bf16 v[60:63], a[184:187], a[128:131], v[60:63]\n" \
"buffer_load_dwordx4 v20, s[80:83], s85 offen sc1 lds\n" \
"v_mfma_f32_16x16x32_bf16 v[60:63], a[248:251], a[192:195], v[60:63]\n" \
"ds_read_b64_tr_b16 a[162:163], v32 offset:512\n" \
"v_accvgpr_read_b32 v181, a53\n" \
"v_mfma_f32_16x16x32_bf16 v[64:67], a[188:191], a[128:131], v[64:67]\n" \
"ds_read_b64_tr_b16 a[224:225], v32 offset:4096\n" \
"v_accvgpr_read_b32 v182, a54\n" \
"v_mfma_f32_16x16x32_bf16 v[64:67], a[252:255], a[192:195], v[64:67]\n" \
"ds_read_b128 a[128:131], v28 offset:0\n" \
"v_accvgpr_read_b32 v183, a55\n" \
"v_mfma_f32_16x16x32_bf16 v[80:83], a[188:191], a[132:135], v[80:83]\n" \
"ds_read_b128 a[192:195], v29 offset:0\n" \
"v_cvt_pk_bf16_f32 v212, v212, v213\n" \
"v_mfma_f32_16x16x32_bf16 v[80:83], a[252:255], a[196:199], v[80:83]\n" \
"s_add_u32 m0, s40, 0xb000\n" \
"s_movk_i32 s84, 0x80\n" \
"v_mfma_f32_16x16x32_bf16 v[76:79], a[184:187], a[132:135], v[76:79]\n" \
"buffer_load_dwordx4 v21, s[76:79], s84 offen offset:0 lds\n" \
"v_mfma_f32_16x16x32_bf16 v[76:79], a[248:251], a[196:199], v[76:79]\n" \
"s_add_u32 m0, s30, 0x8000\n" \
"s_movk_i32 s84, 0x80\n" \
"v_mfma_f32_16x16x32_bf16 v[72:75], a[180:183], a[132:135], v[72:75]\n" \
"buffer_load_dwordx4 v24, s[76:79], s84 offen offset:0 lds\n" \
"v_mfma_f32_16x16x32_bf16 v[72:75], a[244:247], a[196:199], v[72:75]\n" \
"ds_read_b64_tr_b16 a[226:227], v32 offset:4608\n" \
"v_cvt_pk_bf16_f32 v213, v214, v215\n" \
"v_mfma_f32_16x16x32_bf16 v[68:71], a[176:179], a[132:135], v[68:71]\n" \
"ds_read_b64_tr_b16 a[164:165], v33 offset:0\n" \
"v_cvt_pk_bf16_f32 v214, v180, v181\n" \
"v_mfma_f32_16x16x32_bf16 v[68:71], a[240:243], a[196:199], v[68:71]\n" \
"ds_read_b128 a[132:135], v28 offset:2048\n" \
"v_cvt_pk_bf16_f32 v215, v182, v183\n" \
"v_mfma_f32_16x16x32_bf16 v[84:87], a[176:179], a[136:139], v[84:87]\n" \
"ds_read_b128 a[196:199], v29 offset:2048\n" \
"v_accvgpr_read_b32 v216, a64\n" \
"v_mfma_f32_16x16x32_bf16 v[84:87], a[240:243], a[200:203], v[84:87]\n" \
"s_movk_i32 s84, 0x2080\n" \
"v_mfma_f32_16x16x32_bf16 v[88:91], a[180:183], a[136:139], v[88:91]\n" \
"buffer_load_dwordx4 v25, s[76:79], s84 offen offset:1024 lds\n" \
"v_mfma_f32_16x16x32_bf16 v[88:91], a[244:247], a[200:203], v[88:91]\n" \
"s_movk_i32 s84, 0x4080\n" \
"v_mfma_f32_16x16x32_bf16 v[92:95], a[184:187], a[136:139], v[92:95]\n" \
"buffer_load_dwordx4 v24, s[76:79], s84 offen offset:2048 lds\n" \
"v_mfma_f32_16x16x32_bf16 v[92:95], a[248:251], a[200:203], v[92:95]\n" \
"ds_read_b64_tr_b16 a[166:167], v33 offset:512\n" \
"v_accvgpr_read_b32 v217, a65\n" \
"v_mfma_f32_16x16x32_bf16 v[96:99], a[188:191], a[136:139], v[96:99]\n" \
"ds_read_b64_tr_b16 a[228:229], v33 offset:4096\n" \
"v_accvgpr_read_b32 v218, a66\n" \
"v_mfma_f32_16x16x32_bf16 v[96:99], a[252:255], a[200:203], v[96:99]\n" \
"ds_read_b128 a[136:139], v28 offset:4096\n" \
"v_accvgpr_read_b32 v219, a67\n" \
"v_mfma_f32_16x16x32_bf16 v[112:115], a[188:191], a[140:143], v[112:115]\n" \
"ds_read_b128 a[200:203], v29 offset:4096\n" \
"v_accvgpr_read_b32 v180, a68\n" \
"v_mfma_f32_16x16x32_bf16 v[112:115], a[252:255], a[204:207], v[112:115]\n" \
"s_movk_i32 s84, 0x6080\n" \
"v_mfma_f32_16x16x32_bf16 v[108:111], a[184:187], a[140:143], v[108:111]\n" \
"buffer_load_dwordx4 v25, s[76:79], s84 offen offset:3072 lds\n" \
"v_mfma_f32_16x16x32_bf16 v[108:111], a[248:251], a[204:207], v[108:111]\n" \
"s_add_u32 m0, s31, 0x8000\n" \
"s_mov_b32 s84, 0x80000\n" \
"v_mfma_f32_16x16x32_bf16 v[104:107], a[180:183], a[140:143], v[104:107]\n" \
"buffer_load_dwordx4 v26, s[68:71], s84 offen offset:0 lds\n" \
"v_mfma_f32_16x16x32_bf16 v[104:107], a[244:247], a[204:207], v[104:107]\n" \
"s_addk_i32 s84, 0x7c00\n" \
"v_mfma_f32_16x16x32_bf16 v[100:103], a[176:179], a[140:143], v[100:103]\n" \
"buffer_load_dwordx4 v26, s[68:71], s84 offen offset:1024 lds\n" \
"v_mfma_f32_16x16x32_bf16 v[100:103], a[240:243], a[204:207], v[100:103]\n" \
"ds_read_b128 a[140:143], v28 offset:6144\n" \
"v_accvgpr_read_b32 v181, a69\n" \
"v_mfma_f32_16x16x32_bf16 v[116:119], a[176:179], a[144:147], v[116:119]\n" \
"ds_read_b128 a[204:207], v29 offset:6144\n" \
"v_accvgpr_read_b32 v182, a70\n" \
"v_mfma_f32_16x16x32_bf16 v[116:119], a[240:243], a[208:211], v[116:119]\n" \
"s_addk_i32 s84, 0x7c00\n" \
"v_mfma_f32_16x16x32_bf16 v[120:123], a[180:183], a[144:147], v[120:123]\n" \
"buffer_load_dwordx4 v27, s[68:71], s84 offen offset:2048 lds\n" \
"v_mfma_f32_16x16x32_bf16 v[120:123], a[244:247], a[208:211], v[120:123]\n" \
"ds_read_b64_tr_b16 a[230:231], v33 offset:4608\n" \
"v_accvgpr_read_b32 v183, a71\n" \
"v_mfma_f32_16x16x32_bf16 v[124:127], a[184:187], a[144:147], v[124:127]\n" \
"s_addk_i32 s84, 0x7c00\n" \
"v_mfma_f32_16x16x32_bf16 v[124:127], a[248:251], a[208:211], v[124:127]\n" \
"buffer_load_dwordx4 v27, s[68:71], s84 offen offset:3072 lds\n" \
"v_mfma_f32_16x16x32_bf16 v[128:131], a[188:191], a[144:147], v[128:131]\n" \
"ds_read_b64_tr_b16 a[172:173], v35 offset:0\n" \
"v_cvt_pk_bf16_f32 v216, v216, v217\n" \
"v_mfma_f32_16x16x32_bf16 v[128:131], a[252:255], a[208:211], v[128:131]\n" \
"ds_read_b128 a[144:147], v28 offset:8192\n" \
"v_cvt_pk_bf16_f32 v217, v218, v219\n" \
"v_mfma_f32_16x16x32_bf16 v[144:147], a[188:191], a[148:151], v[144:147]\n" \
"ds_read_b128 a[208:211], v29 offset:8192\n" \
"v_cvt_pk_bf16_f32 v218, v180, v181\n" \
"v_mfma_f32_16x16x32_bf16 v[144:147], a[252:255], a[212:215], v[144:147]\n" \
"s_add_u32 m0, s30, 0x9000\n" \
"s_mov_b32 s84, 0x9080\n" \
"v_mfma_f32_16x16x32_bf16 v[140:143], a[184:187], a[148:151], v[140:143]\n" \
"buffer_load_dwordx4 v24, s[76:79], s84 offen offset:0 lds\n" \
"v_mfma_f32_16x16x32_bf16 v[140:143], a[248:251], a[212:215], v[140:143]\n" \
"ds_read_b64_tr_b16 a[174:175], v35 offset:512\n" \
"v_cvt_pk_bf16_f32 v219, v182, v183\n" \
"v_mfma_f32_16x16x32_bf16 v[136:139], a[180:183], a[148:151], v[136:139]\n" \
"s_addk_i32 s84, 0x2000\n" \
"s_nop 0\n" \
"v_mfma_f32_16x16x32_bf16 v[136:139], a[244:247], a[212:215], v[136:139]\n" \
"buffer_load_dwordx4 v25, s[76:79], s84 offen offset:1024 lds\n" \
"v_mfma_f32_16x16x32_bf16 v[132:135], a[176:179], a[148:151], v[132:135]\n" \
"ds_read_b64_tr_b16 a[236:237], v35 offset:4096\n" \
"v_accvgpr_read_b32 v220, a80\n" \
"v_mfma_f32_16x16x32_bf16 v[132:135], a[240:243], a[212:215], v[132:135]\n" \
"ds_read_b128 a[148:151], v28 offset:10240\n" \
"v_accvgpr_read_b32 v221, a81\n" \
"v_mfma_f32_16x16x32_bf16 v[148:151], a[176:179], a[152:155], v[148:151]\n" \
"ds_read_b128 a[212:215], v29 offset:10240\n" \
"v_accvgpr_read_b32 v222, a82\n" \
"v_mfma_f32_16x16x32_bf16 v[148:151], a[240:243], a[216:219], v[148:151]\n" \
"ds_read_b64_tr_b16 a[238:239], v35 offset:4608\n" \
"v_accvgpr_read_b32 v223, a83\n" \
"v_mfma_f32_16x16x32_bf16 v[152:155], a[180:183], a[152:155], v[152:155]\n" \
"ds_read_b64_tr_b16 a[168:169], v34 offset:0\n" \
"v_accvgpr_read_b32 v180, a84\n" \
"v_mfma_f32_16x16x32_bf16 v[152:155], a[244:247], a[216:219], v[152:155]\n" \
"ds_read_b64_tr_b16 a[170:171], v34 offset:512\n" \
"v_accvgpr_read_b32 v181, a85\n" \
"v_mfma_f32_16x16x32_bf16 v[156:159], a[184:187], a[152:155], v[156:159]\n" \
"ds_read_b64_tr_b16 a[232:233], v34 offset:4096\n" \
"v_accvgpr_read_b32 v182, a86\n" \
"v_mfma_f32_16x16x32_bf16 v[156:159], a[248:251], a[216:219], v[156:159]\n" \
"ds_read_b64_tr_b16 a[234:235], v34 offset:4608\n" \
"v_accvgpr_read_b32 v183, a87\n" \
"v_mfma_f32_16x16x32_bf16 v[160:163], a[188:191], a[152:155], v[160:163]\n" \
"ds_read_b128 v[248:251], v22 offset:0\n" \
"v_cvt_pk_bf16_f32 v220, v220, v221\n" \
"v_mfma_f32_16x16x32_bf16 v[160:163], a[252:255], a[216:219], v[160:163]\n" \
"ds_read_b128 a[152:155], v28 offset:12288\n" \
"v_cvt_pk_bf16_f32 v221, v222, v223\n" \
"v_mfma_f32_16x16x32_bf16 v[176:179], a[188:191], a[156:159], v[176:179]\n" \
"ds_read_b128 a[216:219], v29 offset:12288\n" \
"v_cvt_pk_bf16_f32 v222, v180, v181\n" \
"v_mfma_f32_16x16x32_bf16 v[176:179], a[252:255], a[220:223], v[176:179]\n" \
"ds_read_b128 v[48:51], v23 offset:0\n" \
"v_cvt_pk_bf16_f32 v223, v182, v183\n" \
"v_mfma_f32_16x16x32_bf16 v[172:175], a[184:187], a[156:159], v[172:175]\n" \
"v_cvt_pk_bf16_f32 v224, v52, v53\n" \
"v_cvt_pk_bf16_f32 v225, v54, v55\n" \
"v_mfma_f32_16x16x32_bf16 v[172:175], a[248:251], a[220:223], v[172:175]\n" \
"v_cvt_pk_bf16_f32 v226, v56, v57\n" \
"v_cvt_pk_bf16_f32 v227, v58, v59\n" \
"v_mfma_f32_16x16x32_bf16 v[168:171], a[180:183], a[156:159], v[168:171]\n" \
"v_cvt_pk_bf16_f32 v228, v60, v61\n" \
"v_cvt_pk_bf16_f32 v229, v62, v63\n" \
"v_mfma_f32_16x16x32_bf16 v[168:171], a[244:247], a[220:223], v[168:171]\n" \
"v_cvt_pk_bf16_f32 v230, v64, v65\n" \
"v_cvt_pk_bf16_f32 v231, v66, v67\n" \
"v_mfma_f32_16x16x32_bf16 v[164:167], a[176:179], a[156:159], v[164:167]\n" \
"v_cvt_pk_bf16_f32 v232, v76, v77\n" \
"v_cvt_pk_bf16_f32 v233, v78, v79\n" \
"v_mfma_f32_16x16x32_bf16 v[164:167], a[240:243], a[220:223], v[164:167]\n" \
"ds_read_b128 a[156:159], v28 offset:14336\n" \
"ds_read_b128 a[220:223], v29 offset:14336\n" \
"s_waitcnt lgkmcnt(15)\n" \
"v_mfma_f32_16x16x32_bf16 a[0:3], a[160:163], a[128:131], 0\n" \
"s_addk_i32 s84, 0x2000\n" \
"v_cvt_pk_bf16_f32 v234, v80, v81\n" \
"v_mfma_f32_16x16x32_bf16 a[0:3], a[224:227], a[192:195], a[0:3]\n" \
"buffer_load_dwordx4 v24, s[76:79], s84 offen offset:2048 lds\n" \
"v_cvt_pk_bf16_f32 v235, v82, v83\n" \
"v_mfma_f32_16x16x32_bf16 a[4:7], a[164:167], a[128:131], 0\n" \
"ds_read_b64_tr_b16 a[176:177], v36 offset:0\n" \
"v_cvt_pk_bf16_f32 v236, v68, v69\n" \
"v_mfma_f32_16x16x32_bf16 a[4:7], a[228:231], a[192:195], a[4:7]\n" \
"s_addk_i32 s84, 0x2000\n" \
"v_cvt_pk_bf16_f32 v237, v70, v71\n" \
"s_waitcnt lgkmcnt(15)\n" \
"v_mfma_f32_16x16x32_bf16 a[12:15], a[172:175], a[128:131], 0\n" \
"buffer_load_dwordx4 v25, s[76:79], s84 offen offset:3072 lds\n" \
"v_cvt_pk_bf16_f32 v238, v72, v73\n" \
"s_waitcnt lgkmcnt(11)\n" \
"v_mfma_f32_16x16x32_bf16 a[12:15], a[236:239], a[192:195], a[12:15]\n" \
"ds_read_b64_tr_b16 a[178:179], v36 offset:1024\n" \
"v_cvt_pk_bf16_f32 v239, v74, v75\n" \
"s_waitcnt lgkmcnt(10)\n" \
"v_mfma_f32_16x16x32_bf16 a[8:11], a[128:131], a[168:171], 0\n" \
"ds_read_b64_tr_b16 a[240:241], v36 offset:8192\n" \
"v_cvt_pk_bf16_f32 v240, v84, v85\n" \
"s_waitcnt lgkmcnt(9)\n" \
"s_nop 0\n" \
"v_mfma_f32_16x16x32_bf16 a[8:11], a[192:195], a[232:235], a[8:11]\n" \
"ds_read_b64_tr_b16 a[242:243], v36 offset:9216\n" \
"v_cvt_pk_bf16_f32 v241, v86, v87\n" \
"v_mfma_f32_16x16x32_bf16 a[24:27], a[132:135], a[168:171], 0\n" \
"ds_read_b64_tr_b16 a[180:181], v37 offset:0\n" \
"v_cvt_pk_bf16_f32 v242, v88, v89\n" \
"v_mfma_f32_16x16x32_bf16 a[24:27], a[196:199], a[232:235], a[24:27]\n" \
"ds_read_b64_tr_b16 a[182:183], v37 offset:1024\n" \
"v_cvt_pk_bf16_f32 v243, v90, v91\n" \
"v_mfma_f32_16x16x32_bf16 a[28:31], a[172:175], a[132:135], 0\n" \
"ds_read_b64_tr_b16 a[244:245], v37 offset:8192\n" \
"v_cvt_pk_bf16_f32 v244, v92, v93\n" \
"v_mfma_f32_16x16x32_bf16 a[28:31], a[236:239], a[196:199], a[28:31]\n" \
"s_add_u32 s89, s41, 0x2000\n" \
"buffer_store_dwordx4 v[200:203], v20, s[80:83], s89 offen offset:0 sc1\n" \
"v_mfma_f32_16x16x32_bf16 a[20:23], a[164:167], a[132:135], 0\n" \
".fill (L_pb + 20192 - .) / 4, 4, 0xBF800000\n" \
"L_pp5:\n" \
"s_cbranch_execz L_pret\n" \
"s_nop 0\n" \
"ds_read_b64_tr_b16 a[246:247], v37 offset:9216\n" \
"v_cvt_pk_bf16_f32 v245, v94, v95\n" \
"v_mfma_f32_16x16x32_bf16 a[20:23], a[228:231], a[196:199], a[20:23]\n" \
"ds_read_b64_tr_b16 a[184:185], v38 offset:0\n" \
"v_cvt_pk_bf16_f32 v246, v96, v97\n" \
"v_mfma_f32_16x16x32_bf16 a[16:19], a[160:163], a[132:135], 0\n" \
"ds_read_b64_tr_b16 a[186:187], v38 offset:1024\n" \
"v_cvt_pk_bf16_f32 v247, v98, v99\n" \
"v_mfma_f32_16x16x32_bf16 a[16:19], a[224:227], a[196:199], a[16:19]\n" \
"v_cvt_pk_bf16_f32 v184, v108, v109\n" \
"v_cvt_pk_bf16_f32 v185, v110, v111\n" \
"v_cvt_pk_bf16_f32 v186, v112, v113\n" \
"v_mfma_f32_16x16x32_bf16 a[32:35], a[160:163], a[136:139], 0\n" \
"s_add_u32 s88, s41, 0x2000\n" \
"buffer_store_dwordx4 v[204:207], v20, s[80:83], s88 offen offset:1024 sc1\n" \
"v_mfma_f32_16x16x32_bf16 a[32:35], a[224:227], a[200:203], a[32:35]\n" \
"ds_read_b64_tr_b16 a[248:249], v38 offset:8192\n" \
"v_cvt_pk_bf16_f32 v187, v114, v115\n" \
"v_mfma_f32_16x16x32_bf16 a[36:39], a[164:167], a[136:139], 0\n" \
"ds_read_b64_tr_b16 a[250:251], v38 offset:9216\n" \
"v_cvt_pk_bf16_f32 v188, v100, v101\n" \
"v_mfma_f32_16x16x32_bf16 a[36:39], a[228:231], a[200:203], a[36:39]\n" \
"ds_read_b64_tr_b16 a[188:189], v39 offset:0\n" \
"v_cvt_pk_bf16_f32 v189, v102, v103\n" \
"v_mfma_f32_16x16x32_bf16 a[44:47], a[172:175], a[136:139], 0\n" \
"v_cvt_pk_bf16_f32 v190, v104, v105\n" \
"v_cvt_pk_bf16_f32 v191, v106, v107\n" \
"v_cvt_pk_bf16_f32 v192, v116, v117\n" \
"v_mfma_f32_16x16x32_bf16 a[44:47], a[236:239], a[200:203], a[44:47]\n" \
"ds_read_b64_tr_b16 a[190:191], v39 offset:1024\n" \
"v_cvt_pk_bf16_f32 v193, v118, v119\n" \
"v_mfma_f32_16x16x32_bf16 a[40:43], a[136:139], a[168:171], 0\n" \
"s_add_u32 s89, s41, 0x3000\n" \
"buffer_store_dwordx4 v[208:211], v20, s[80:83], s89 offen offset:1024 sc1\n" \
"v_mfma_f32_16x16x32_bf16 a[40:43], a[200:203], a[232:235], a[40:43]\n" \
"ds_read_b64_tr_b16 a[252:253], v39 offset:8192\n" \
"v_cvt_pk_bf16_f32 v194, v120, v121\n" \
"v_mfma_f32_16x16x32_bf16 a[56:59], a[140:143], a[168:171], 0\n" \
"ds_read_b64_tr_b16 a[254:255], v39 offset:9216\n" \
"v_cvt_pk_bf16_f32 v195, v122, v123\n" \
"v_mfma_f32_16x16x32_bf16 a[56:59], a[204:207], a[232:235], a[56:59]\n" \
"v_cvt_pk_bf16_f32 v196, v124, v125\n" \
"v_cvt_pk_bf16_f32 v197, v126, v127\n" \
"v_cvt_pk_bf16_f32 v198, v128, v129\n" \
"v_mfma_f32_16x16x32_bf16 a[60:63], a[172:175], a[140:143], 0\n" \
"v_cvt_pk_bf16_f32 v199, v130, v131\n" \
"v_cvt_pk_bf16_f32 v252, v140, v141\n" \
"v_cvt_pk_bf16_f32 v253, v142, v143\n" \
"v_mfma_f32_16x16x32_bf16 a[60:63], a[236:239], a[204:207], a[60:63]\n" \
"s_add_u32 s88, s41, 0x3000\n" \
"buffer_store_dwordx4 v[212:215], v20, s[80:83], s88 offen offset:0 sc1\n" \
"v_mfma_f32_16x16x32_bf16 a[52:55], a[164:167], a[140:143], 0\n" \
"v_cvt_pk_bf16_f32 v254, v144, v145\n" \
"v_cvt_pk_bf16_f32 v255, v146, v147\n" \
"v_accvgpr_write_b32 a84, v252\n" \
"v_mfma_f32_16x16x32_bf16 a[52:55], a[228:231], a[204:207], a[52:55]\n" \
"v_accvgpr_write_b32 a85, v253\n" \
"v_accvgpr_write_b32 a86, v254\n" \
"v_accvgpr_write_b32 a87, v255\n" \
"v_mfma_f32_16x16x32_bf16 a[48:51], a[160:163], a[140:143], 0\n" \
"v_cvt_pk_bf16_f32 v252, v132, v133\n" \
"v_cvt_pk_bf16_f32 v253, v134, v135\n" \
"v_cvt_pk_bf16_f32 v254, v136, v137\n" \
"v_mfma_f32_16x16x32_bf16 a[48:51], a[224:227], a[204:207], a[48:51]\n" \
"v_cvt_pk_bf16_f32 v255, v138, v139\n" \
"s_waitcnt lgkmcnt(15)\n" \
"v_mfma_f32_16x16x32_bf16 a[64:67], a[164:167], v[248:251], 0\n" \
"v_mfma_f32_16x16x32_bf16 a[64:67], a[228:231], v[48:51], a[64:67]\n" \
"v_mfma_f32_16x16x32_bf16 a[80:83], a[172:175], v[248:251], 0\n" \
"v_mfma_f32_16x16x32_bf16 a[80:83], a[236:239], v[48:51], a[80:83]\n" \
"v_mfma_f32_16x16x32_bf16 a[68:71], v[248:251], a[168:171], 0\n" \
"v_mfma_f32_16x16x32_bf16 a[68:71], v[48:51], a[232:235], a[68:71]\n" \
"s_waitcnt vmcnt(4) lgkmcnt(0)\n" \
"s_barrier\n" \
"v_mfma_f32_16x16x32_bf16 v[52:55], a[176:179], a[128:131], 0\n" \
"s_add_u32 m0, s40, 0x0\n" \
"s_add_u32 s85, s39, 0x4000\n" \
"v_cvt_pk_bf16_f32 v200, v148, v149\n" \
"v_mfma_f32_16x16x32_bf16 v[52:55], a[240:243], a[192:195], v[52:55]\n" \
"buffer_load_dwordx4 v20, s[80:83], s85 offen sc1 lds\n" \
"v_cvt_pk_bf16_f32 v201, v150, v151\n" \
"v_mfma_f32_16x16x32_bf16 v[56:59], a[180:183], a[128:131], 0\n" \
"ds_read_b64_tr_b16 a[160:161], v32 offset:32768\n" \
"v_cvt_pk_bf16_f32 v202, v152, v153\n" \
"v_mfma_f32_16x16x32_bf16 v[56:59], a[244:247], a[192:195], v[56:59]\n" \
"ds_read_b64_tr_b16 a[162:163], v32 offset:33280\n" \
"v_cvt_pk_bf16_f32 v203, v154, v155\n" \
"v_mfma_f32_16x16x32_bf16 v[60:63], a[184:187], a[128:131], 0\n" \
"s_add_u32 m0, s40, 0x1000\n" \
"s_add_u32 s85, s39, 0x5000\n" \
"v_cvt_pk_bf16_f32 v204, v156, v157\n" \
"v_mfma_f32_16x16x32_bf16 v[60:63], a[248:251], a[192:195], v[60:63]\n" \
"buffer_load_dwordx4 v20, s[80:83], s85 offen sc1 lds\n" \
"v_cvt_pk_bf16_f32 v205, v158, v159\n" \
"v_mfma_f32_16x16x32_bf16 v[64:67], a[188:191], a[128:131], 0\n" \
"ds_read_b64_tr_b16 a[224:225], v32 offset:36864\n" \
"v_cvt_pk_bf16_f32 v206, v160, v161\n" \
"v_mfma_f32_16x16x32_bf16 v[64:67], a[252:255], a[192:195], v[64:67]\n" \
"ds_read_b128 a[128:131], v28 offset:32768\n" \
"v_cvt_pk_bf16_f32 v207, v162, v163\n" \
"v_mfma_f32_16x16x32_bf16 v[80:83], a[188:191], a[132:135], 0\n" \
"ds_read_b128 a[192:195], v29 offset:32768\n" \
"v_cvt_pk_bf16_f32 v208, v172, v173\n" \
"v_mfma_f32_16x16x32_bf16 v[80:83], a[252:255], a[196:199], v[80:83]\n" \
"s_add_u32 m0, s40, 0x3000\n" \
"s_movk_i32 s84, 0x100\n" \
"v_cvt_pk_bf16_f32 v209, v174, v175\n" \
"v_mfma_f32_16x16x32_bf16 v[76:79], a[184:187], a[132:135], 0\n" \
"buffer_load_dwordx4 v21, s[76:79], s84 offen offset:0 lds\n" \
"v_cvt_pk_bf16_f32 v210, v176, v177\n" \
"v_mfma_f32_16x16x32_bf16 v[76:79], a[248:251], a[196:199], v[76:79]\n" \
"ds_read_b64_tr_b16 a[226:227], v32 offset:37376\n" \
"v_cvt_pk_bf16_f32 v211, v178, v179\n" \
"v_mfma_f32_16x16x32_bf16 v[72:75], a[180:183], a[132:135], 0\n" \
"s_add_u32 m0, s30, 0x0\n" \
"s_movk_i32 s84, 0x100\n" \
"s_nop 0\n" \
"v_cvt_pk_bf16_f32 v212, v164, v165\n" \
"v_mfma_f32_16x16x32_bf16 v[72:75], a[244:247], a[196:199], v[72:75]\n" \
"buffer_load_dwordx4 v24, s[76:79], s84 offen offset:0 lds\n" \
"v_cvt_pk_bf16_f32 v213, v166, v167\n" \
"v_mfma_f32_16x16x32_bf16 v[68:71], a[176:179], a[132:135], 0\n" \
"ds_read_b64_tr_b16 a[164:165], v33 offset:32768\n" \
"v_cvt_pk_bf16_f32 v214, v168, v169\n" \
"v_mfma_f32_16x16x32_bf16 v[68:71], a[240:243], a[196:199], v[68:71]\n" \
"ds_read_b128 a[132:135], v28 offset:34816\n" \
"v_cvt_pk_bf16_f32 v215, v170, v171\n" \
"v_mfma_f32_16x16x32_bf16 v[84:87], a[176:179], a[136:139], 0\n" \
"ds_read_b128 a[196:199], v29 offset:34816\n" \
"v_mfma_f32_16x16x32_bf16 v[84:87], a[240:243], a[200:203], v[84:87]\n" \
"s_movk_i32 s84, 0x2100\n" \
"v_mfma_f32_16x16x32_bf16 v[88:91], a[180:183], a[136:139], 0\n" \
"buffer_load_dwordx4 v25, s[76:79], s84 offen offset:1024 lds\n" \
"v_mfma_f32_16x16x32_bf16 v[88:91], a[244:247], a[200:203], v[88:91]\n" \
"ds_read_b64_tr_b16 a[166:167], v33 offset:33280\n" \
"v_mfma_f32_16x16x32_bf16 v[92:95], a[184:187], a[136:139], 0\n" \
"ds_read_b64_tr_b16 a[228:229], v33 offset:36864\n" \
"v_mfma_f32_16x16x32_bf16 v[92:95], a[248:251], a[200:203], v[92:95]\n" \
"s_movk_i32 s84, 0x4100\n" \
"v_mfma_f32_16x16x32_bf16 v[96:99], a[188:191], a[136:139], 0\n" \
"buffer_load_dwordx4 v24, s[76:79], s84 offen offset:2048 lds\n" \
"v_mfma_f32_16x16x32_bf16 v[96:99], a[252:255], a[200:203], v[96:99]\n" \
"ds_read_b128 a[136:139], v28 offset:36864\n" \
"v_mfma_f32_16x16x32_bf16 v[112:115], a[188:191], a[140:143], 0\n" \
"ds_read_b128 a[200:203], v29 offset:36864\n" \
"v_mfma_f32_16x16x32_bf16 v[112:115], a[252:255], a[204:207], v[112:115]\n" \
"s_movk_i32 s84, 0x6100\n" \
"s_nop 0\n" \
"v_mfma_f32_16x16x32_bf16 v[108:111], a[184:187], a[140:143], 0\n" \
"buffer_load_dwordx4 v25, s[76:79], s84 offen offset:3072 lds\n" \
"v_mfma_f32_16x16x32_bf16 v[108:111], a[248:251], a[204:207], v[108:111]\n" \
"ds_read_b64_tr_b16 a[230:231], v33 offset:37376\n" \
"v_mfma_f32_16x16x32_bf16 v[104:107], a[180:183], a[140:143], 0\n" \
"ds_read_b64_tr_b16 a[172:173], v35 offset:32768\n" \
"v_mfma_f32_16x16x32_bf16 v[104:107], a[244:247], a[204:207], v[104:107]\n" \
"ds_read_b64_tr_b16 a[174:175], v35 offset:33280\n" \
"v_mfma_f32_16x16x32_bf16 v[100:103], a[176:179], a[140:143], 0\n" \
"ds_read_b64_tr_b16 a[236:237], v35 offset:36864\n" \
"v_mfma_f32_16x16x32_bf16 v[100:103], a[240:243], a[204:207], v[100:103]\n" \
"ds_read_b128 a[140:143], v28 offset:38912\n" \
"v_mfma_f32_16x16x32_bf16 v[116:119], a[176:179], a[144:147], 0\n" \
"ds_read_b128 a[204:207], v29 offset:38912\n" \
"v_mfma_f32_16x16x32_bf16 v[116:119], a[240:243], a[208:211], v[116:119]\n" \
"s_add_u32 m0, s31, 0x0\n" \
"s_mov_b32 s84, 0x100000\n" \
"v_mfma_f32_16x16x32_bf16 v[120:123], a[180:183], a[144:147], 0\n" \
"buffer_load_dwordx4 v26, s[68:71], s84 offen offset:0 lds\n" \
"v_mfma_f32_16x16x32_bf16 v[120:123], a[244:247], a[208:211], v[120:123]\n" \
"s_addk_i32 s84, 0x7c00\n" \
"v_mfma_f32_16x16x32_bf16 v[124:127], a[184:187], a[144:147], 0\n" \
"buffer_load_dwordx4 v26, s[68:71], s84 offen offset:1024 lds\n" \
"v_mfma_f32_16x16x32_bf16 v[124:127], a[248:251], a[208:211], v[124:127]\n" \
"ds_read_b64_tr_b16 a[238:239], v35 offset:37376\n" \
"v_mfma_f32_16x16x32_bf16 v[128:131], a[188:191], a[144:147], 0\n" \
"ds_read_b64_tr_b16 a[168:169], v34 offset:32768\n" \
"v_mfma_f32_16x16x32_bf16 v[128:131], a[252:255], a[208:211], v[128:131]\n" \
"ds_read_b128 a[144:147], v28 offset:40960\n" \
"v_mfma_f32_16x16x32_bf16 v[144:147], a[188:191], a[148:151], 0\n" \
"ds_read_b128 a[208:211], v29 offset:40960\n" \
"v_mfma_f32_16x16x32_bf16 v[144:147], a[252:255], a[212:215], v[144:147]\n" \
"s_addk_i32 s84, 0x7c00\n" \
"v_mfma_f32_16x16x32_bf16 v[140:143], a[184:187], a[148:151], 0\n" \
"buffer_load_dwordx4 v27, s[68:71], s84 offen offset:2048 lds\n" \
"v_mfma_f32_16x16x32_bf16 v[140:143], a[248:251], a[212:215], v[140:143]\n" \
"s_addk_i32 s84, 0x7c00\n" \
"v_mfma_f32_16x16x32_bf16 v[136:139], a[180:183], a[148:151], 0\n" \
"buffer_load_dwordx4 v27, s[68:71], s84 offen offset:3072 lds\n" \
"v_mfma_f32_16x16x32_bf16 v[136:139], a[244:247], a[212:215], v[136:139]\n" \
"ds_read_b64_tr_b16 a[170:171], v34 offset:33280\n" \
"v_mfma_f32_16x16x32_bf16 v[132:135], a[176:179], a[148:151], 0\n" \
"ds_read_b64_tr_b16 a[232:233], v34 offset:36864\n" \
"v_mfma_f32_16x16x32_bf16 v[132:135], a[240:243], a[212:215], v[132:135]\n" \
"ds_read_b128 a[148:151], v28 offset:43008\n" \
"v_mfma_f32_16x16x32_bf16 v[148:151], a[176:179], a[152:155], 0\n" \
"ds_read_b128 a[212:215], v29 offset:43008\n" \
"v_mfma_f32_16x16x32_bf16 v[148:151], a[240:243], a[216:219], v[148:151]\n" \
"s_add_u32 m0, s30, 0x1000\n" \
"s_mov_b32 s84, 0x9100\n" \
"v_mfma_f32_16x16x32_bf16 v[152:155], a[180:183], a[152:155], 0\n" \
"buffer_load_dwordx4 v24, s[76:79], s84 offen offset:0 lds\n" \
"v_mfma_f32_16x16x32_bf16 v[152:155], a[244:247], a[216:219], v[152:155]\n" \
"s_addk_i32 s84, 0x2000\n" \
"s_nop 0\n" \
"v_mfma_f32_16x16x32_bf16 v[156:159], a[184:187], a[152:155], 0\n" \
"buffer_load_dwordx4 v25, s[76:79], s84 offen offset:1024 lds\n" \
"v_mfma_f32_16x16x32_bf16 v[156:159], a[248:251], a[216:219], v[156:159]\n" \
"ds_read_b64_tr_b16 a[234:235], v34 offset:37376\n" \
"v_mfma_f32_16x16x32_bf16 v[160:163], a[188:191], a[152:155], 0\n" \
"ds_read_b128 v[248:251], v22 offset:32768\n" \
"v_mfma_f32_16x16x32_bf16 v[160:163], a[252:255], a[216:219], v[160:163]\n" \
"ds_read_b128 a[152:155], v28 offset:45056\n" \
"v_mfma_f32_16x16x32_bf16 v[176:179], a[188:191], a[156:159], 0\n" \
"ds_read_b128 a[216:219], v29 offset:45056\n" \
"v_mfma_f32_16x16x32_bf16 v[176:179], a[252:255], a[220:223], v[176:179]\n" \
"ds_read_b128 v[48:51], v23 offset:32768\n" \
"v_mfma_f32_16x16x32_bf16 v[172:175], a[184:187], a[156:159], 0\n" \
"v_mfma_f32_16x16x32_bf16 v[172:175], a[248:251], a[220:223], v[172:175]\n" \
"v_mfma_f32_16x16x32_bf16 v[168:171], a[180:183], a[156:159], 0\n" \
"v_mfma_f32_16x16x32_bf16 v[168:171], a[244:247], a[220:223], v[168:171]\n" \
"v_mfma_f32_16x16x32_bf16 v[164:167], a[176:179], a[156:159], 0\n" \
"v_mfma_f32_16x16x32_bf16 v[164:167], a[240:243], a[220:223], v[164:167]\n" \
"ds_read_b128 a[156:159], v28 offset:47104\n" \
"ds_read_b128 a[220:223], v29 offset:47104\n" \
"s_waitcnt lgkmcnt(15)\n" \
"v_mfma_f32_16x16x32_bf16 a[0:3], a[160:163], a[128:131], a[0:3]\n" \
"s_addk_i32 s84, 0x2000\n" \
"v_mfma_f32_16x16x32_bf16 a[0:3], a[224:227], a[192:195], a[0:3]\n" \
"buffer_load_dwordx4 v24, s[76:79], s84 offen offset:2048 lds\n" \
"v_mfma_f32_16x16x32_bf16 a[4:7], a[164:167], a[128:131], a[4:7]\n" \
"ds_read_b64_tr_b16 a[176:177], v36 offset:32768\n" \
"v_mov_b32_e64 v11, 0\n" \
"v_mfma_f32_16x16x32_bf16 a[4:7], a[228:231], a[192:195], a[4:7]\n" \
"s_addk_i32 s84, 0x2000\n" \
"v_mfma_f32_16x16x32_bf16 a[12:15], a[172:175], a[128:131], a[12:15]\n" \
"buffer_load_dwordx4 v25, s[76:79], s84 offen offset:3072 lds\n" \
"s_waitcnt lgkmcnt(15)\n" \
"v_mfma_f32_16x16x32_bf16 a[12:15], a[236:239], a[192:195], a[12:15]\n" \
"ds_read_b64_tr_b16 a[178:179], v36 offset:33792\n" \
"s_and_b32 s6, s2, 7\n" \
"s_waitcnt lgkmcnt(12)\n" \
"v_mfma_f32_16x16x32_bf16 a[8:11], a[128:131], a[168:171], a[8:11]\n" \
"ds_read_b64_tr_b16 a[240:241], v36 offset:40960\n" \
"s_lshl_b32 s7, s3, 1\n" \
"s_waitcnt lgkmcnt(9)\n" \
"v_mfma_f32_16x16x32_bf16 a[8:11], a[192:195], a[232:235], a[8:11]\n" \
"ds_read_b64_tr_b16 a[242:243], v36 offset:41984\n" \
"s_lshr_b32 s4, s2, 5\n" \
"v_mfma_f32_16x16x32_bf16 a[24:27], a[132:135], a[168:171], a[24:27]\n" \
"ds_read_b64_tr_b16 a[180:181], v37 offset:32768\n" \
"s_lshl_b32 s5, s6, 2\n" \
"v_mfma_f32_16x16x32_bf16 a[24:27], a[196:199], a[232:235], a[24:27]\n" \
"ds_read_b64_tr_b16 a[182:183], v37 offset:33792\n" \
"s_bfe_u32 s8, s2, 0x20003\n" \
"v_mfma_f32_16x16x32_bf16 a[28:31], a[172:175], a[132:135], a[28:31]\n" \
"ds_read_b64_tr_b16 a[244:245], v37 offset:40960\n" \
"s_and_b32 s7, s7, 4\n" \
"v_mfma_f32_16x16x32_bf16 a[28:31], a[236:239], a[196:199], a[28:31]\n" \
"s_add_u32 s89, s41, 0x0\n" \
"buffer_store_dwordx4 v[224:227], v20, s[80:83], s89 offen offset:2048 sc1\n" \
"v_mfma_f32_16x16x32_bf16 a[20:23], a[164:167], a[132:135], a[20:23]\n" \
"ds_read_b64_tr_b16 a[246:247], v37 offset:41984\n" \
"s_and_b32 s9, s3, 1\n" \
"v_mfma_f32_16x16x32_bf16 a[20:23], a[228:231], a[196:199], a[20:23]\n" \
"ds_read_b64_tr_b16 a[184:185], v38 offset:32768\n" \
"s_or_b32 s13, s5, s8\n" \
"v_mfma_f32_16x16x32_bf16 a[16:19], a[160:163], a[132:135], a[16:19]\n" \
"ds_read_b64_tr_b16 a[186:187], v38 offset:33792\n" \
"s_or_b32 s9, s7, s9\n" \
"v_mfma_f32_16x16x32_bf16 a[16:19], a[224:227], a[196:199], a[16:19]\n" \
"s_mul_i32 s7, s4, 0xa00\n" \
"s_add_i32 s7, s7, 0x5bc1000\n" \
"v_mfma_f32_16x16x32_bf16 a[32:35], a[160:163], a[136:139], a[32:35]\n" \
"s_add_u32 s88, s41, 0x0\n" \
"buffer_store_dwordx4 v[228:231], v20, s[80:83], s88 offen offset:3072 sc1\n" \
"v_mfma_f32_16x16x32_bf16 a[32:35], a[224:227], a[200:203], a[32:35]\n" \
"ds_read_b64_tr_b16 a[248:249], v38 offset:40960\n" \
"s_lshl_b32 s10, s13, 2\n" \
"v_mfma_f32_16x16x32_bf16 a[36:39], a[164:167], a[136:139], a[36:39]\n" \
"ds_read_b64_tr_b16 a[250:251], v38 offset:41984\n" \
"s_or_b32 s10, s10, s7\n" \
"v_mfma_f32_16x16x32_bf16 a[36:39], a[228:231], a[200:203], a[36:39]\n" \
"ds_read_b64_tr_b16 a[188:189], v39 offset:32768\n" \
"v_writelane_b32 v11, s10, 0\n" \
"v_mfma_f32_16x16x32_bf16 a[44:47], a[172:175], a[136:139], a[44:47]\n" \
"s_lshl_b32 s14, s4, 3\n" \
"v_writelane_b32 v11, s7, 1\n" \
"v_mfma_f32_16x16x32_bf16 a[44:47], a[236:239], a[200:203], a[44:47]\n" \
"ds_read_b64_tr_b16 a[190:191], v39 offset:33792\n" \
"s_or_b32 s7, s14, s6\n" \
"v_mfma_f32_16x16x32_bf16 a[40:43], a[136:139], a[168:171], a[40:43]\n" \
"s_add_u32 s89, s41, 0x1000\n" \
"buffer_store_dwordx4 v[232:235], v20, s[80:83], s89 offen offset:3072 sc1\n" \
"v_mfma_f32_16x16x32_bf16 a[40:43], a[200:203], a[232:235], a[40:43]\n" \
"ds_read_b64_tr_b16 a[252:253], v39 offset:40960\n" \
"s_lshl_b32 s7, s7, 10\n" \
"v_mfma_f32_16x16x32_bf16 a[56:59], a[140:143], a[168:171], a[56:59]\n" \
"ds_read_b64_tr_b16 a[254:255], v39 offset:41984\n" \
"s_lshl_b32 s10, s3, 8\n" \
"v_mfma_f32_16x16x32_bf16 a[56:59], a[204:207], a[232:235], a[56:59]\n" \
"s_or_b32 s7, s7, s10\n" \
"s_or_b32 s10, s7, 0x5c00200\n" \
"v_mfma_f32_16x16x32_bf16 a[60:63], a[172:175], a[140:143], a[60:63]\n" \
"v_writelane_b32 v11, s10, 2\n" \
"s_lshl_b32 s10, s8, 2\n" \
"v_mfma_f32_16x16x32_bf16 a[60:63], a[236:239], a[204:207], a[60:63]\n" \
"s_add_u32 s88, s41, 0x1000\n" \
"buffer_store_dwordx4 v[236:239], v20, s[80:83], s88 offen offset:2048 sc1\n" \
"v_mfma_f32_16x16x32_bf16 a[52:55], a[164:167], a[140:143], a[52:55]\n" \
"s_or_b32 s11, s10, s7\n" \
"s_or_b32 s11, s11, 0x5c00000\n" \
"v_mfma_f32_16x16x32_bf16 a[52:55], a[228:231], a[204:207], a[52:55]\n" \
"v_writelane_b32 v11, s11, 3\n" \
"s_or_b32 s7, s7, 0x5c00080\n" \
"v_mfma_f32_16x16x32_bf16 a[48:51], a[160:163], a[140:143], a[48:51]\n" \
"v_writelane_b32 v11, s7, 4\n" \
"s_or_b32 s7, s7, s10\n" \
"v_mfma_f32_16x16x32_bf16 a[48:51], a[224:227], a[204:207], a[48:51]\n" \
"s_add_i32 s10, s3, s2\n" \
"v_writelane_b32 v11, s7, 5\n" \
"s_waitcnt lgkmcnt(15)\n" \
"v_mfma_f32_16x16x32_bf16 a[64:67], a[164:167], v[248:251], a[64:67]\n" \
"s_mov_b32 s7, 2.0\n" \
"s_mov_b32 s11, 0x5bc0000\n" \
"v_mfma_f32_16x16x32_bf16 a[64:67], a[228:231], v[48:51], a[64:67]\n" \
"s_cmp_lg_u32 s10, 0\n" \
"s_cselect_b32 s10, s7, s11\n" \
"v_mfma_f32_16x16x32_bf16 a[80:83], a[172:175], v[248:251], a[80:83]\n" \
"s_lshr_b32 s15, s2, 3\n" \
"v_writelane_b32 v11, s10, 54\n" \
"v_mfma_f32_16x16x32_bf16 a[80:83], a[236:239], v[48:51], a[80:83]\n" \
"s_lshl_b32 s10, s6, 5\n" \
"s_min_u32 s16, s3, 2\n" \
"v_mfma_f32_16x16x32_bf16 a[68:71], v[248:251], a[168:171], a[68:71]\n" \
"s_mul_i32 s12, s8, 0x120\n" \
"v_mfma_f32_16x16x32_bf16 a[68:71], v[48:51], a[232:235], a[68:71]\n" \
"s_waitcnt vmcnt(4) lgkmcnt(0)\n" \
"s_barrier\n" \
"v_mfma_f32_16x16x32_bf16 v[52:55], a[176:179], a[128:131], v[52:55]\n" \
"s_add_u32 m0, s40, 0x8000\n" \
"s_add_u32 s85, s39, 0x6000\n" \
"v_mfma_f32_16x16x32_bf16 v[52:55], a[240:243], a[192:195], v[52:55]\n" \
"buffer_load_dwordx4 v20, s[80:83], s85 offen sc1 lds\n" \
"v_mfma_f32_16x16x32_bf16 v[56:59], a[180:183], a[128:131], v[56:59]\n" \
"ds_read_b64_tr_b16 a[160:161], v32 offset:0\n" \
"v_mfma_f32_16x16x32_bf16 v[56:59], a[244:247], a[192:195], v[56:59]\n" \
"ds_read_b64_tr_b16 a[162:163], v32 offset:512\n" \
"v_mfma_f32_16x16x32_bf16 v[60:63], a[184:187], a[128:131], v[60:63]\n" \
"s_add_u32 m0, s40, 0x9000\n" \
"s_add_u32 s85, s39, 0x7000\n" \
"v_mfma_f32_16x16x32_bf16 v[60:63], a[248:251], a[192:195], v[60:63]\n" \
"buffer_load_dwordx4 v20, s[80:83], s85 offen sc1 lds\n" \
"v_mfma_f32_16x16x32_bf16 v[64:67], a[188:191], a[128:131], v[64:67]\n" \
"ds_read_b64_tr_b16 a[224:225], v32 offset:4096\n" \
"v_mfma_f32_16x16x32_bf16 v[64:67], a[252:255], a[192:195], v[64:67]\n" \
"ds_read_b128 a[128:131], v28 offset:0\n" \
"v_mfma_f32_16x16x32_bf16 v[80:83], a[188:191], a[132:135], v[80:83]\n" \
"ds_read_b128 a[192:195], v29 offset:0\n" \
"v_mfma_f32_16x16x32_bf16 v[80:83], a[252:255], a[196:199], v[80:83]\n" \
"s_add_u32 m0, s40, 0xb000\n" \
"s_movk_i32 s84, 0x180\n" \
"v_mfma_f32_16x16x32_bf16 v[76:79], a[184:187], a[132:135], v[76:79]\n" \
"buffer_load_dwordx4 v21, s[76:79], s84 offen offset:0 lds\n" \
"v_mfma_f32_16x16x32_bf16 v[76:79], a[248:251], a[196:199], v[76:79]\n" \
"ds_read_b64_tr_b16 a[226:227], v32 offset:4608\n" \
"v_mfma_f32_16x16x32_bf16 v[72:75], a[180:183], a[132:135], v[72:75]\n" \
"s_add_u32 m0, s30, 0x8000\n" \
"s_movk_i32 s84, 0x180\n" \
"v_mfma_f32_16x16x32_bf16 v[72:75], a[244:247], a[196:199], v[72:75]\n" \
"buffer_load_dwordx4 v24, s[76:79], s84 offen offset:0 lds\n" \
"v_mfma_f32_16x16x32_bf16 v[68:71], a[176:179], a[132:135], v[68:71]\n" \
"ds_read_b64_tr_b16 a[164:165], v33 offset:0\n" \
"v_mfma_f32_16x16x32_bf16 v[68:71], a[240:243], a[196:199], v[68:71]\n" \
"ds_read_b128 a[132:135], v28 offset:2048\n" \
"v_mfma_f32_16x16x32_bf16 v[84:87], a[176:179], a[136:139], v[84:87]\n" \
"ds_read_b128 a[196:199], v29 offset:2048\n" \
"v_mfma_f32_16x16x32_bf16 v[84:87], a[240:243], a[200:203], v[84:87]\n" \
"s_movk_i32 s84, 0x2180\n" \
"v_mfma_f32_16x16x32_bf16 v[88:91], a[180:183], a[136:139], v[88:91]\n" \
"buffer_load_dwordx4 v25, s[76:79], s84 offen offset:1024 lds\n" \
"v_mfma_f32_16x16x32_bf16 v[88:91], a[244:247], a[200:203], v[88:91]\n" \
"ds_read_b64_tr_b16 a[166:167], v33 offset:512\n" \
"v_mfma_f32_16x16x32_bf16 v[92:95], a[184:187], a[136:139], v[92:95]\n" \
"ds_read_b64_tr_b16 a[228:229], v33 offset:4096\n" \
"v_mfma_f32_16x16x32_bf16 v[92:95], a[248:251], a[200:203], v[92:95]\n" \
"s_movk_i32 s84, 0x4180\n" \
"v_mfma_f32_16x16x32_bf16 v[96:99], a[188:191], a[136:139], v[96:99]\n" \
"buffer_load_dwordx4 v24, s[76:79], s84 offen offset:2048 lds\n" \
"v_mfma_f32_16x16x32_bf16 v[96:99], a[252:255], a[200:203], v[96:99]\n" \
"ds_read_b128 a[136:139], v28 offset:4096\n" \
"v_mfma_f32_16x16x32_bf16 v[112:115], a[188:191], a[140:143], v[112:115]\n" \
"ds_read_b128 a[200:203], v29 offset:4096\n" \
"v_mfma_f32_16x16x32_bf16 v[112:115], a[252:255], a[204:207], v[112:115]\n" \
"s_movk_i32 s84, 0x6180\n" \
"s_nop 0\n" \
"v_mfma_f32_16x16x32_bf16 v[108:111], a[184:187], a[140:143], v[108:111]\n" \
"buffer_load_dwordx4 v25, s[76:79], s84 offen offset:3072 lds\n" \
"v_mfma_f32_16x16x32_bf16 v[108:111], a[248:251], a[204:207], v[108:111]\n" \
"ds_read_b64_tr_b16 a[230:231], v33 offset:4608\n" \
"v_mfma_f32_16x16x32_bf16 v[104:107], a[180:183], a[140:143], v[104:107]\n" \
"ds_read_b64_tr_b16 a[172:173], v35 offset:0\n" \
"v_mfma_f32_16x16x32_bf16 v[104:107], a[244:247], a[204:207], v[104:107]\n" \
"ds_read_b64_tr_b16 a[174:175], v35 offset:512\n" \
"v_mfma_f32_16x16x32_bf16 v[100:103], a[176:179], a[140:143], v[100:103]\n" \
"ds_read_b64_tr_b16 a[236:237], v35 offset:4096\n" \
"v_mfma_f32_16x16x32_bf16 v[100:103], a[240:243], a[204:207], v[100:103]\n" \
"ds_read_b128 a[140:143], v28 offset:6144\n" \
"v_mfma_f32_16x16x32_bf16 v[116:119], a[176:179], a[144:147], v[116:119]\n" \
"ds_read_b128 a[204:207], v29 offset:6144\n" \
"v_mfma_f32_16x16x32_bf16 v[116:119], a[240:243], a[208:211], v[116:119]\n" \
"s_add_u32 m0, s31, 0x8000\n" \
"s_mov_b32 s84, 0x180000\n" \
"v_mfma_f32_16x16x32_bf16 v[120:123], a[180:183], a[144:147], v[120:123]\n" \
"buffer_load_dwordx4 v26, s[68:71], s84 offen offset:0 lds\n" \
"v_mfma_f32_16x16x32_bf16 v[120:123], a[244:247], a[208:211], v[120:123]\n" \
"s_addk_i32 s84, 0x7c00\n" \
"s_nop 0\n" \
"v_mfma_f32_16x16x32_bf16 v[124:127], a[184:187], a[144:147], v[124:127]\n" \
"buffer_load_dwordx4 v26, s[68:71], s84 offen offset:1024 lds\n" \
"v_mfma_f32_16x16x32_bf16 v[124:127], a[248:251], a[208:211], v[124:127]\n" \
"ds_read_b64_tr_b16 a[238:239], v35 offset:4608\n" \
"v_mfma_f32_16x16x32_bf16 v[128:131], a[188:191], a[144:147], v[128:131]\n" \
"ds_read_b64_tr_b16 a[168:169], v34 offset:0\n" \
"v_mfma_f32_16x16x32_bf16 v[128:131], a[252:255], a[208:211], v[128:131]\n" \
"ds_read_b128 a[144:147], v28 offset:8192\n" \
"v_mfma_f32_16x16x32_bf16 v[144:147], a[188:191], a[148:151], v[144:147]\n" \
"ds_read_b128 a[208:211], v29 offset:8192\n" \
"v_mfma_f32_16x16x32_bf16 v[144:147], a[252:255], a[212:215], v[144:147]\n" \
"s_addk_i32 s84, 0x7c00\n" \
"v_mfma_f32_16x16x32_bf16 v[140:143], a[184:187], a[148:151], v[140:143]\n" \
"buffer_load_dwordx4 v27, s[68:71], s84 offen offset:2048 lds\n" \
"v_mfma_f32_16x16x32_bf16 v[140:143], a[248:251], a[212:215], v[140:143]\n" \
"s_addk_i32 s84, 0x7c00\n" \
"v_mfma_f32_16x16x32_bf16 v[136:139], a[180:183], a[148:151], v[136:139]\n" \
"buffer_load_dwordx4 v27, s[68:71], s84 offen offset:3072 lds\n" \
"v_mfma_f32_16x16x32_bf16 v[136:139], a[244:247], a[212:215], v[136:139]\n" \
"ds_read_b64_tr_b16 a[170:171], v34 offset:512\n" \
"v_mfma_f32_16x16x32_bf16 v[132:135], a[176:179], a[148:151], v[132:135]\n" \
"ds_read_b64_tr_b16 a[232:233], v34 offset:4096\n" \
"v_mfma_f32_16x16x32_bf16 v[132:135], a[240:243], a[212:215], v[132:135]\n" \
"ds_read_b128 a[148:151], v28 offset:10240\n" \
"v_mfma_f32_16x16x32_bf16 v[148:151], a[176:179], a[152:155], v[148:151]\n" \
"ds_read_b128 a[212:215], v29 offset:10240\n" \
"v_mfma_f32_16x16x32_bf16 v[148:151], a[240:243], a[216:219], v[148:151]\n" \
"s_add_u32 m0, s30, 0x9000\n" \
"s_mov_b32 s84, 0x9180\n" \
"v_mfma_f32_16x16x32_bf16 v[152:155], a[180:183], a[152:155], v[152:155]\n" \
"buffer_load_dwordx4 v24, s[76:79], s84 offen offset:0 lds\n" \
"v_mfma_f32_16x16x32_bf16 v[152:155], a[244:247], a[216:219], v[152:155]\n" \
"s_addk_i32 s84, 0x2000\n" \
"s_nop 0\n" \
"v_mfma_f32_16x16x32_bf16 v[156:159], a[184:187], a[152:155], v[156:159]\n" \
"buffer_load_dwordx4 v25, s[76:79], s84 offen offset:1024 lds\n" \
"v_mfma_f32_16x16x32_bf16 v[156:159], a[248:251], a[216:219], v[156:159]\n" \
"ds_read_b64_tr_b16 a[234:235], v34 offset:4608\n" \
"v_mfma_f32_16x16x32_bf16 v[160:163], a[188:191], a[152:155], v[160:163]\n" \
"ds_read_b128 v[248:251], v22 offset:0\n" \
"v_mfma_f32_16x16x32_bf16 v[160:163], a[252:255], a[216:219], v[160:163]\n" \
"ds_read_b128 a[152:155], v28 offset:12288\n" \
"v_mfma_f32_16x16x32_bf16 v[176:179], a[188:191], a[156:159], v[176:179]\n" \
"ds_read_b128 a[216:219], v29 offset:12288\n" \
"v_mfma_f32_16x16x32_bf16 v[176:179], a[252:255], a[220:223], v[176:179]\n" \
"ds_read_b128 v[48:51], v23 offset:0\n" \
"v_mfma_f32_16x16x32_bf16 v[172:175], a[184:187], a[156:159], v[172:175]\n" \
"s_add_u32 s11, s22, s12\n" \
"s_addc_u32 s17, s23, 0\n" \
"v_mfma_f32_16x16x32_bf16 v[172:175], a[248:251], a[220:223], v[172:175]\n" \
"s_add_u32 s18, s24, s12\n" \
"v_writelane_b32 v11, s11, 9\n" \
"s_addc_u32 s19, s25, 0\n" \
"v_mfma_f32_16x16x32_bf16 v[168:171], a[180:183], a[156:159], v[168:171]\n" \
"s_and_b32 s20, s17, 0xffff\n" \
"v_writelane_b32 v11, s20, 10\n" \
"v_mfma_f32_16x16x32_bf16 v[168:171], a[244:247], a[220:223], v[168:171]\n" \
"s_and_b32 s20, s19, 0xffff\n" \
"v_writelane_b32 v11, s18, 11\n" \
"v_mfma_f32_16x16x32_bf16 v[164:167], a[176:179], a[156:159], v[164:167]\n" \
"s_cmpk_lt_u32 s3, 3\n" \
"s_cselect_b32 s11, s11, s18\n" \
"v_mfma_f32_16x16x32_bf16 v[164:167], a[240:243], a[220:223], v[164:167]\n" \
"ds_read_b128 a[156:159], v28 offset:14336\n" \
"ds_read_b128 a[220:223], v29 offset:14336\n" \
"s_waitcnt lgkmcnt(15)\n" \
"v_mfma_f32_16x16x32_bf16 a[0:3], a[160:163], a[128:131], a[0:3]\n" \
"s_addk_i32 s84, 0x2000\n" \
"v_mfma_f32_16x16x32_bf16 a[0:3], a[224:227], a[192:195], a[0:3]\n" \
"buffer_load_dwordx4 v24, s[76:79], s84 offen offset:2048 lds\n" \
"v_mfma_f32_16x16x32_bf16 a[4:7], a[164:167], a[128:131], a[4:7]\n" \
"ds_read_b64_tr_b16 a[176:177], v36 offset:0\n" \
"s_movk_i32 s18, 0xfff\n" \
"v_mfma_f32_16x16x32_bf16 a[4:7], a[228:231], a[192:195], a[4:7]\n" \
"s_addk_i32 s84, 0x2000\n" \
"v_mfma_f32_16x16x32_bf16 a[12:15], a[172:175], a[128:131], a[12:15]\n" \
"buffer_load_dwordx4 v25, s[76:79], s84 offen offset:3072 lds\n" \
"s_waitcnt lgkmcnt(15)\n" \
"v_mfma_f32_16x16x32_bf16 a[12:15], a[236:239], a[192:195], a[12:15]\n" \
"ds_read_b64_tr_b16 a[178:179], v36 offset:1024\n" \
"v_writelane_b32 v11, s20, 12\n" \
"s_waitcnt lgkmcnt(12)\n" \
"v_mfma_f32_16x16x32_bf16 a[8:11], a[128:131], a[168:171], a[8:11]\n" \
"ds_read_b64_tr_b16 a[240:241], v36 offset:8192\n" \
"s_and_b32 s20, s29, 0xffff\n" \
"s_waitcnt lgkmcnt(9)\n" \
"s_nop 0\n" \
"v_mfma_f32_16x16x32_bf16 a[8:11], a[192:195], a[232:235], a[8:11]\n" \
"ds_read_b64_tr_b16 a[242:243], v36 offset:9216\n" \
"v_writelane_b32 v11, s28, 13\n" \
"v_mfma_f32_16x16x32_bf16 a[24:27], a[132:135], a[168:171], a[24:27]\n" \
"ds_read_b64_tr_b16 a[180:181], v37 offset:0\n" \
"v_writelane_b32 v11, s20, 14\n" \
"v_mfma_f32_16x16x32_bf16 a[24:27], a[196:199], a[232:235], a[24:27]\n" \
"ds_read_b64_tr_b16 a[182:183], v37 offset:1024\n" \
"v_writelane_b32 v11, s11, 15\n" \
"v_mfma_f32_16x16x32_bf16 a[28:31], a[172:175], a[132:135], a[28:31]\n" \
"ds_read_b64_tr_b16 a[244:245], v37 offset:8192\n" \
"v_mfma_f32_16x16x32_bf16 a[28:31], a[236:239], a[196:199], a[28:31]\n" \
"s_add_u32 s89, s41, 0x2000\n" \
"buffer_store_dwordx4 v[240:243], v20, s[80:83], s89 offen offset:2048 sc1\n" \
"v_mfma_f32_16x16x32_bf16 a[20:23], a[164:167], a[132:135], a[20:23]\n" \
"ds_read_b64_tr_b16 a[246:247], v37 offset:9216\n" \
"v_mfma_f32_16x16x32_bf16 a[20:23], a[228:231], a[196:199], a[20:23]\n" \
"ds_read_b64_tr_b16 a[184:185], v38 offset:0\n" \
"v_mfma_f32_16x16x32_bf16 a[16:19], a[160:163], a[132:135], a[16:19]\n" \
"ds_read_b64_tr_b16 a[186:187], v38 offset:1024\n" \
"v_mfma_f32_16x16x32_bf16 a[16:19], a[224:227], a[196:199], a[16:19]\n" \
"s_cmpk_lt_u32 s3, 3\n" \
"s_cselect_b32 s11, s17, s19\n" \
"v_mfma_f32_16x16x32_bf16 a[32:35], a[160:163], a[136:139], a[32:35]\n" \
"s_add_u32 s88, s41, 0x2000\n" \
"buffer_store_dwordx4 v[244:247], v20, s[80:83], s88 offen offset:3072 sc1\n" \
"v_mfma_f32_16x16x32_bf16 a[32:35], a[224:227], a[200:203], a[32:35]\n" \
"ds_read_b64_tr_b16 a[248:249], v38 offset:8192\n" \
"s_and_b32 s11, s11, 0xffff\n" \
"v_mfma_f32_16x16x32_bf16 a[36:39], a[164:167], a[136:139], a[36:39]\n" \
"ds_read_b64_tr_b16 a[250:251], v38 offset:9216\n" \
"v_writelane_b32 v11, s11, 16\n" \
"v_mfma_f32_16x16x32_bf16 a[36:39], a[228:231], a[200:203], a[36:39]\n" \
"ds_read_b64_tr_b16 a[188:189], v39 offset:0\n" \
"s_mul_i32 s11, s6, 0x48\n" \
"v_mfma_f32_16x16x32_bf16 a[44:47], a[172:175], a[136:139], a[44:47]\n" \
"s_add_i32 s17, s11, 0x47\n" \
"v_mfma_f32_16x16x32_bf16 a[44:47], a[236:239], a[200:203], a[44:47]\n" \
"ds_read_b64_tr_b16 a[190:191], v39 offset:1024\n" \
"v_mfma_f32_16x16x32_bf16 a[40:43], a[136:139], a[168:171], a[40:43]\n" \
"s_add_u32 s89, s41, 0x3000\n" \
"buffer_store_dwordx4 v[184:187], v20, s[80:83], s89 offen offset:3072 sc1\n" \
"v_mfma_f32_16x16x32_bf16 a[40:43], a[200:203], a[232:235], a[40:43]\n" \
"ds_read_b64_tr_b16 a[252:253], v39 offset:8192\n" \
"v_mfma_f32_16x16x32_bf16 a[56:59], a[140:143], a[168:171], a[56:59]\n" \
"ds_read_b64_tr_b16 a[254:255], v39 offset:9216\n" \
"v_mfma_f32_16x16x32_bf16 a[56:59], a[204:207], a[232:235], a[56:59]\n" \
"s_cmpk_lt_u32 s3, 3\n" \
"s_cselect_b32 s17, s17, s18\n" \
"v_mfma_f32_16x16x32_bf16 a[60:63], a[172:175], a[140:143], a[60:63]\n" \
".fill (L_pb + 24288 - .) / 4, 4, 0xBF800000\n" \
"L_pp6:\n" \
"s_cbranch_execz L_pret\n" \
"s_mulk_i32 s17, 0x480\n" \
"s_addk_i32 s17, 0x120\n" \
"v_mfma_f32_16x16x32_bf16 a[60:63], a[236:239], a[204:207], a[60:63]\n" \
"s_add_u32 s88, s41, 0x3000\n" \
"buffer_store_dwordx4 v[188:191], v20, s[80:83], s88 offen offset:2048 sc1\n" \
"v_mfma_f32_16x16x32_bf16 a[52:55], a[164:167], a[140:143], a[52:55]\n" \
"v_writelane_b32 v11, s17, 17\n" \
"s_lshl_b32 s17, s3, 5\n" \
"v_mfma_f32_16x16x32_bf16 a[52:55], a[228:231], a[204:207], a[52:55]\n" \
"s_add_i32 s11, s11, s17\n" \
"s_mul_i32 s17, s11, 0x480\n" \
"v_mfma_f32_16x16x32_bf16 a[48:51], a[160:163], a[140:143], a[48:51]\n" \
"s_mul_i32 s11, s13, 0x24000\n" \
"s_add_i32 s18, s11, 0x1b000\n" \
"v_mfma_f32_16x16x32_bf16 a[48:51], a[224:227], a[204:207], a[48:51]\n" \
"s_cmpk_lt_u32 s3, 3\n" \
"s_cselect_b32 s17, s17, s18\n" \
"s_waitcnt lgkmcnt(15)\n" \
"v_mfma_f32_16x16x32_bf16 a[64:67], a[164:167], v[248:251], a[64:67]\n" \
"s_mov_b32 s18, 0x8000\n" \
"v_writelane_b32 v11, s17, 18\n" \
"v_mfma_f32_16x16x32_bf16 a[64:67], a[228:231], v[48:51], a[64:67]\n" \
"s_mul_i32 s17, s16, 0x2400\n" \
"s_mul_i32 s16, s16, 0x9000\n" \
"v_mfma_f32_16x16x32_bf16 a[80:83], a[172:175], v[248:251], a[80:83]\n" \
"s_bitset1_b32 s17, 17\n" \
"v_writelane_b32 v11, s17, 19\n" \
"v_mfma_f32_16x16x32_bf16 a[80:83], a[236:239], v[48:51], a[80:83]\n" \
"s_add_i32 s16, s11, s16\n" \
"v_writelane_b32 v11, s16, 26\n" \
"v_mfma_f32_16x16x32_bf16 a[68:71], v[248:251], a[168:171], a[68:71]\n" \
"s_mul_i32 s16, s3, 0x2400\n" \
"s_add_i32 s17, s16, 0xa400\n" \
"v_mfma_f32_16x16x32_bf16 a[68:71], v[48:51], a[232:235], a[68:71]\n" \
"s_waitcnt vmcnt(4) lgkmcnt(0)\n" \
"s_barrier\n" \
"v_mfma_f32_16x16x32_bf16 v[52:55], a[176:179], a[128:131], v[52:55]\n" \
"s_add_u32 m0, s40, 0x0\n" \
"s_nop 0\n" \
"s_add_u32 s85, s39, 0x8000\n" \
"v_mfma_f32_16x16x32_bf16 v[52:55], a[240:243], a[192:195], v[52:55]\n" \
"buffer_load_dwordx4 v20, s[80:83], s85 offen sc1 lds\n" \
"v_mfma_f32_16x16x32_bf16 v[56:59], a[180:183], a[128:131], v[56:59]\n" \
"ds_read_b64_tr_b16 a[160:161], v32 offset:32768\n" \
"v_mfma_f32_16x16x32_bf16 v[56:59], a[244:247], a[192:195], v[56:59]\n" \
"ds_read_b64_tr_b16 a[162:163], v32 offset:33280\n" \
"v_mfma_f32_16x16x32_bf16 v[60:63], a[184:187], a[128:131], v[60:63]\n" \
"s_add_u32 m0, s40, 0x1000\n" \
"s_add_u32 s85, s39, 0x9000\n" \
"v_mfma_f32_16x16x32_bf16 v[60:63], a[248:251], a[192:195], v[60:63]\n" \
"buffer_load_dwordx4 v20, s[80:83], s85 offen sc1 lds\n" \
"v_mfma_f32_16x16x32_bf16 v[64:67], a[188:191], a[128:131], v[64:67]\n" \
"ds_read_b64_tr_b16 a[224:225], v32 offset:36864\n" \
"v_mfma_f32_16x16x32_bf16 v[64:67], a[252:255], a[192:195], v[64:67]\n" \
"ds_read_b128 a[128:131], v28 offset:32768\n" \
"v_mfma_f32_16x16x32_bf16 v[80:83], a[188:191], a[132:135], v[80:83]\n" \
"ds_read_b128 a[192:195], v29 offset:32768\n" \
"v_mfma_f32_16x16x32_bf16 v[80:83], a[252:255], a[196:199], v[80:83]\n" \
"s_add_u32 m0, s40, 0x3000\n" \
"s_movk_i32 s84, 0x200\n" \
"v_mfma_f32_16x16x32_bf16 v[76:79], a[184:187], a[132:135], v[76:79]\n" \
"buffer_load_dwordx4 v21, s[76:79], s84 offen offset:0 lds\n" \
"v_mfma_f32_16x16x32_bf16 v[76:79], a[248:251], a[196:199], v[76:79]\n" \
"ds_read_b64_tr_b16 a[226:227], v32 offset:37376\n" \
"v_mfma_f32_16x16x32_bf16 v[72:75], a[180:183], a[132:135], v[72:75]\n" \
"s_add_u32 m0, s30, 0x0\n" \
"s_movk_i32 s84, 0x200\n" \
"v_mfma_f32_16x16x32_bf16 v[72:75], a[244:247], a[196:199], v[72:75]\n" \
"buffer_load_dwordx4 v24, s[76:79], s84 offen offset:0 lds\n" \
"v_mfma_f32_16x16x32_bf16 v[68:71], a[176:179], a[132:135], v[68:71]\n" \
"ds_read_b64_tr_b16 a[164:165], v33 offset:32768\n" \
"v_mfma_f32_16x16x32_bf16 v[68:71], a[240:243], a[196:199], v[68:71]\n" \
"ds_read_b128 a[132:135], v28 offset:34816\n" \
"v_mfma_f32_16x16x32_bf16 v[84:87], a[176:179], a[136:139], v[84:87]\n" \
"ds_read_b128 a[196:199], v29 offset:34816\n" \
"v_mfma_f32_16x16x32_bf16 v[84:87], a[240:243], a[200:203], v[84:87]\n" \
"s_movk_i32 s84, 0x2200\n" \
"v_mfma_f32_16x16x32_bf16 v[88:91], a[180:183], a[136:139], v[88:91]\n" \
"buffer_load_dwordx4 v25, s[76:79], s84 offen offset:1024 lds\n" \
"v_mfma_f32_16x16x32_bf16 v[88:91], a[244:247], a[200:203], v[88:91]\n" \
"ds_read_b64_tr_b16 a[166:167], v33 offset:33280\n" \
"v_mfma_f32_16x16x32_bf16 v[92:95], a[184:187], a[136:139], v[92:95]\n" \
"ds_read_b64_tr_b16 a[228:229], v33 offset:36864\n" \
"v_mfma_f32_16x16x32_bf16 v[92:95], a[248:251], a[200:203], v[92:95]\n" \
"s_movk_i32 s84, 0x4200\n" \
"v_mfma_f32_16x16x32_bf16 v[96:99], a[188:191], a[136:139], v[96:99]\n" \
"buffer_load_dwordx4 v24, s[76:79], s84 offen offset:2048 lds\n" \
"v_mfma_f32_16x16x32_bf16 v[96:99], a[252:255], a[200:203], v[96:99]\n" \
"ds_read_b128 a[136:139], v28 offset:36864\n" \
"v_mfma_f32_16x16x32_bf16 v[112:115], a[188:191], a[140:143], v[112:115]\n" \
"ds_read_b128 a[200:203], v29 offset:36864\n" \
"v_mfma_f32_16x16x32_bf16 v[112:115], a[252:255], a[204:207], v[112:115]\n" \
"s_movk_i32 s84, 0x6200\n" \
"v_mfma_f32_16x16x32_bf16 v[108:111], a[184:187], a[140:143], v[108:111]\n" \
"buffer_load_dwordx4 v25, s[76:79], s84 offen offset:3072 lds\n" \
"v_mfma_f32_16x16x32_bf16 v[108:111], a[248:251], a[204:207], v[108:111]\n" \
"ds_read_b64_tr_b16 a[230:231], v33 offset:37376\n" \
"v_mfma_f32_16x16x32_bf16 v[104:107], a[180:183], a[140:143], v[104:107]\n" \
"ds_read_b64_tr_b16 a[172:173], v35 offset:32768\n" \
"v_mfma_f32_16x16x32_bf16 v[104:107], a[244:247], a[204:207], v[104:107]\n" \
"ds_read_b64_tr_b16 a[174:175], v35 offset:33280\n" \
"v_mfma_f32_16x16x32_bf16 v[100:103], a[176:179], a[140:143], v[100:103]\n" \
"ds_read_b64_tr_b16 a[236:237], v35 offset:36864\n" \
"v_mfma_f32_16x16x32_bf16 v[100:103], a[240:243], a[204:207], v[100:103]\n" \
"ds_read_b128 a[140:143], v28 offset:38912\n" \
"v_mfma_f32_16x16x32_bf16 v[116:119], a[176:179], a[144:147], v[116:119]\n" \
"ds_read_b128 a[204:207], v29 offset:38912\n" \
"v_mfma_f32_16x16x32_bf16 v[116:119], a[240:243], a[208:211], v[116:119]\n" \
"s_add_u32 m0, s31, 0x0\n" \
"s_mov_b32 s84, 0x200000\n" \
"v_mfma_f32_16x16x32_bf16 v[120:123], a[180:183], a[144:147], v[120:123]\n" \
"buffer_load_dwordx4 v26, s[68:71], s84 offen offset:0 lds\n" \
"v_mfma_f32_16x16x32_bf16 v[120:123], a[244:247], a[208:211], v[120:123]\n" \
"s_addk_i32 s84, 0x7c00\n" \
"v_mfma_f32_16x16x32_bf16 v[124:127], a[184:187], a[144:147], v[124:127]\n" \
"buffer_load_dwordx4 v26, s[68:71], s84 offen offset:1024 lds\n" \
"v_mfma_f32_16x16x32_bf16 v[124:127], a[248:251], a[208:211], v[124:127]\n" \
"ds_read_b64_tr_b16 a[238:239], v35 offset:37376\n" \
"v_mfma_f32_16x16x32_bf16 v[128:131], a[188:191], a[144:147], v[128:131]\n" \
"ds_read_b64_tr_b16 a[168:169], v34 offset:32768\n" \
"v_mfma_f32_16x16x32_bf16 v[128:131], a[252:255], a[208:211], v[128:131]\n" \
"ds_read_b128 a[144:147], v28 offset:40960\n" \
"v_mfma_f32_16x16x32_bf16 v[144:147], a[188:191], a[148:151], v[144:147]\n" \
"ds_read_b128 a[208:211], v29 offset:40960\n" \
"v_mfma_f32_16x16x32_bf16 v[144:147], a[252:255], a[212:215], v[144:147]\n" \
"s_addk_i32 s84, 0x7c00\n" \
"v_mfma_f32_16x16x32_bf16 v[140:143], a[184:187], a[148:151], v[140:143]\n" \
"buffer_load_dwordx4 v27, s[68:71], s84 offen offset:2048 lds\n" \
"v_mfma_f32_16x16x32_bf16 v[140:143], a[248:251], a[212:215], v[140:143]\n" \
"s_addk_i32 s84, 0x7c00\n" \
"v_mfma_f32_16x16x32_bf16 v[136:139], a[180:183], a[148:151], v[136:139]\n" \
"buffer_load_dwordx4 v27, s[68:71], s84 offen offset:3072 lds\n" \
"v_mfma_f32_16x16x32_bf16 v[136:139], a[244:247], a[212:215], v[136:139]\n" \
"ds_read_b64_tr_b16 a[170:171], v34 offset:33280\n" \
"v_mfma_f32_16x16x32_bf16 v[132:135], a[176:179], a[148:151], v[132:135]\n" \
"ds_read_b64_tr_b16 a[232:233], v34 offset:36864\n" \
"v_mfma_f32_16x16x32_bf16 v[132:135], a[240:243], a[212:215], v[132:135]\n" \
"ds_read_b128 a[148:151], v28 offset:43008\n" \
"v_mfma_f32_16x16x32_bf16 v[148:151], a[176:179], a[152:155], v[148:151]\n" \
"ds_read_b128 a[212:215], v29 offset:43008\n" \
"v_mfma_f32_16x16x32_bf16 v[148:151], a[240:243], a[216:219], v[148:151]\n" \
"s_add_u32 m0, s30, 0x1000\n" \
"s_mov_b32 s84, 0x9200\n" \
"v_mfma_f32_16x16x32_bf16 v[152:155], a[180:183], a[152:155], v[152:155]\n" \
"buffer_load_dwordx4 v24, s[76:79], s84 offen offset:0 lds\n" \
"v_mfma_f32_16x16x32_bf16 v[152:155], a[244:247], a[216:219], v[152:155]\n" \
"s_addk_i32 s84, 0x2000\n" \
"s_nop 0\n" \
"v_mfma_f32_16x16x32_bf16 v[156:159], a[184:187], a[152:155], v[156:159]\n" \
"buffer_load_dwordx4 v25, s[76:79], s84 offen offset:1024 lds\n" \
"v_mfma_f32_16x16x32_bf16 v[156:159], a[248:251], a[216:219], v[156:159]\n" \
"ds_read_b64_tr_b16 a[234:235], v34 offset:37376\n" \
"v_mfma_f32_16x16x32_bf16 v[160:163], a[188:191], a[152:155], v[160:163]\n" \
"ds_read_b128 v[248:251], v22 offset:32768\n" \
"v_mfma_f32_16x16x32_bf16 v[160:163], a[252:255], a[216:219], v[160:163]\n" \
"ds_read_b128 a[152:155], v28 offset:45056\n" \
"v_mfma_f32_16x16x32_bf16 v[176:179], a[188:191], a[156:159], v[176:179]\n" \
"ds_read_b128 a[216:219], v29 offset:45056\n" \
"v_mfma_f32_16x16x32_bf16 v[176:179], a[252:255], a[220:223], v[176:179]\n" \
"ds_read_b128 v[48:51], v23 offset:32768\n" \
"v_mfma_f32_16x16x32_bf16 v[172:175], a[184:187], a[156:159], v[172:175]\n" \
"s_cmpk_lt_u32 s3, 3\n" \
"s_cselect_b32 s17, s17, s18\n" \
"v_mfma_f32_16x16x32_bf16 v[172:175], a[248:251], a[220:223], v[172:175]\n" \
"s_mov_b32 s18, 0x1b000\n" \
"s_xor_b32 s19, s3, 2\n" \
"v_mfma_f32_16x16x32_bf16 v[168:171], a[180:183], a[156:159], v[168:171]\n" \
"s_cmp_lg_u32 s19, 0\n" \
"s_cselect_b32 s18, s17, s18\n" \
"v_mfma_f32_16x16x32_bf16 v[168:171], a[244:247], a[220:223], v[168:171]\n" \
"v_writelane_b32 v11, s17, 20\n" \
"s_or_b32 s17, s16, 0x10000\n" \
"v_mfma_f32_16x16x32_bf16 v[164:167], a[176:179], a[156:159], v[164:167]\n" \
"v_writelane_b32 v11, s18, 21\n" \
"s_add_i32 s18, s16, 0x19000\n" \
"v_mfma_f32_16x16x32_bf16 v[164:167], a[240:243], a[220:223], v[164:167]\n" \
"ds_read_b128 a[156:159], v28 offset:47104\n" \
"ds_read_b128 a[220:223], v29 offset:47104\n" \
"s_waitcnt lgkmcnt(15)\n" \
"v_mfma_f32_16x16x32_bf16 a[0:3], a[160:163], a[128:131], a[0:3]\n" \
"s_addk_i32 s84, 0x2000\n" \
"v_mfma_f32_16x16x32_bf16 a[0:3], a[224:227], a[192:195], a[0:3]\n" \
"buffer_load_dwordx4 v24, s[76:79], s84 offen offset:2048 lds\n" \
"v_mfma_f32_16x16x32_bf16 a[4:7], a[164:167], a[128:131], a[4:7]\n" \
"ds_read_b64_tr_b16 a[176:177], v36 offset:32768\n" \
"v_writelane_b32 v11, s17, 22\n" \
"v_mfma_f32_16x16x32_bf16 a[4:7], a[228:231], a[192:195], a[4:7]\n" \
"s_addk_i32 s84, 0x2000\n" \
"v_mfma_f32_16x16x32_bf16 a[12:15], a[172:175], a[128:131], a[12:15]\n" \
"buffer_load_dwordx4 v25, s[76:79], s84 offen offset:3072 lds\n" \
"s_waitcnt lgkmcnt(15)\n" \
"v_mfma_f32_16x16x32_bf16 a[12:15], a[236:239], a[192:195], a[12:15]\n" \
"ds_read_b64_tr_b16 a[178:179], v36 offset:33792\n" \
"s_mov_b32 s17, 0\n" \
"s_waitcnt lgkmcnt(12)\n" \
"v_mfma_f32_16x16x32_bf16 a[8:11], a[128:131], a[168:171], a[8:11]\n" \
"ds_read_b64_tr_b16 a[240:241], v36 offset:40960\n" \
"s_waitcnt lgkmcnt(9)\n" \
"v_mfma_f32_16x16x32_bf16 a[8:11], a[192:195], a[232:235], a[8:11]\n" \
"ds_read_b64_tr_b16 a[242:243], v36 offset:41984\n" \
"v_mfma_f32_16x16x32_bf16 a[24:27], a[132:135], a[168:171], a[24:27]\n" \
"ds_read_b64_tr_b16 a[180:181], v37 offset:32768\n" \
"v_mfma_f32_16x16x32_bf16 a[24:27], a[196:199], a[232:235], a[24:27]\n" \
"ds_read_b64_tr_b16 a[182:183], v37 offset:33792\n" \
"v_mfma_f32_16x16x32_bf16 a[28:31], a[172:175], a[132:135], a[28:31]\n" \
"ds_read_b64_tr_b16 a[244:245], v37 offset:40960\n" \
"v_mfma_f32_16x16x32_bf16 a[28:31], a[236:239], a[196:199], a[28:31]\n" \
"s_add_u32 s89, s42, 0x0\n" \
"buffer_store_dwordx4 v[216:219], v20, s[80:83], s89 offen offset:0 sc1\n" \
"v_mfma_f32_16x16x32_bf16 a[20:23], a[164:167], a[132:135], a[20:23]\n" \
"ds_read_b64_tr_b16 a[246:247], v37 offset:41984\n" \
"v_mfma_f32_16x16x32_bf16 a[20:23], a[228:231], a[196:199], a[20:23]\n" \
"ds_read_b64_tr_b16 a[184:185], v38 offset:32768\n" \
"v_mfma_f32_16x16x32_bf16 a[16:19], a[160:163], a[132:135], a[16:19]\n" \
"ds_read_b64_tr_b16 a[186:187], v38 offset:33792\n" \
"v_mfma_f32_16x16x32_bf16 a[16:19], a[224:227], a[196:199], a[16:19]\n" \
"s_cmpk_lt_u32 s3, 3\n" \
"s_cselect_b32 s18, s18, s17\n" \
"v_mfma_f32_16x16x32_bf16 a[32:35], a[160:163], a[136:139], a[32:35]\n" \
"s_add_u32 s88, s42, 0x1000\n" \
"buffer_store_dwordx4 v[220:223], v20, s[80:83], s88 offen offset:0 sc1\n" \
"v_mfma_f32_16x16x32_bf16 a[32:35], a[224:227], a[200:203], a[32:35]\n" \
"ds_read_b64_tr_b16 a[248:249], v38 offset:40960\n" \
"s_bitset1_b32 s16, 17\n" \
"v_mfma_f32_16x16x32_bf16 a[36:39], a[164:167], a[136:139], a[36:39]\n" \
"ds_read_b64_tr_b16 a[250:251], v38 offset:41984\n" \
"v_writelane_b32 v11, s18, 23\n" \
"v_mfma_f32_16x16x32_bf16 a[36:39], a[228:231], a[200:203], a[36:39]\n" \
"ds_read_b64_tr_b16 a[188:189], v39 offset:32768\n" \
"s_movk_i32 s18, 0x2400\n" \
"v_mfma_f32_16x16x32_bf16 a[44:47], a[172:175], a[136:139], a[44:47]\n" \
"s_cmpk_lt_u32 s3, 3\n" \
"s_cselect_b32 s16, s16, s18\n" \
"v_mfma_f32_16x16x32_bf16 a[44:47], a[236:239], a[200:203], a[44:47]\n" \
"ds_read_b64_tr_b16 a[190:191], v39 offset:33792\n" \
"s_mul_i32 s19, s3, 0x9000\n" \
"v_mfma_f32_16x16x32_bf16 a[40:43], a[136:139], a[168:171], a[40:43]\n" \
"s_add_u32 s89, s42, 0x0\n" \
"buffer_store_dwordx4 v[192:195], v20, s[80:83], s89 offen offset:2048 sc1\n" \
"v_mfma_f32_16x16x32_bf16 a[40:43], a[200:203], a[232:235], a[40:43]\n" \
"ds_read_b64_tr_b16 a[252:253], v39 offset:40960\n" \
"v_writelane_b32 v11, s16, 24\n" \
"v_mfma_f32_16x16x32_bf16 a[56:59], a[140:143], a[168:171], a[56:59]\n" \
"ds_read_b64_tr_b16 a[254:255], v39 offset:41984\n" \
"s_lshl_b32 s16, s3, 10\n" \
"v_mfma_f32_16x16x32_bf16 a[56:59], a[204:207], a[232:235], a[56:59]\n" \
"s_addk_i32 s16, 0x4800\n" \
"v_writelane_b32 v11, s16, 25\n" \
"v_mfma_f32_16x16x32_bf16 a[60:63], a[172:175], a[140:143], a[60:63]\n" \
"s_add_i32 s16, s15, 1\n" \
"s_and_b32 s16, s16, 3\n" \
"v_mfma_f32_16x16x32_bf16 a[60:63], a[236:239], a[204:207], a[60:63]\n" \
"s_add_u32 s88, s42, 0x0\n" \
"buffer_store_dwordx4 v[196:199], v20, s[80:83], s88 offen offset:3072 sc1\n" \
"v_mfma_f32_16x16x32_bf16 a[52:55], a[164:167], a[140:143], a[52:55]\n" \
"s_or_b32 s16, s16, s5\n" \
"s_mul_i32 s18, s16, 0x24000\n" \
"v_mfma_f32_16x16x32_bf16 a[52:55], a[228:231], a[204:207], a[52:55]\n" \
"s_add_i32 s18, s18, s19\n" \
"s_xor_b32 s13, s13, 2\n" \
"v_mfma_f32_16x16x32_bf16 a[48:51], a[160:163], a[140:143], a[48:51]\n" \
"s_add_i32 s15, s15, -1\n" \
"v_writelane_b32 v11, s18, 27\n" \
"v_mfma_f32_16x16x32_bf16 a[48:51], a[224:227], a[204:207], a[48:51]\n" \
"s_mul_i32 s18, s13, 0x24000\n" \
"s_and_b32 s15, s15, 3\n" \
"s_waitcnt lgkmcnt(15)\n" \
"v_mfma_f32_16x16x32_bf16 a[64:67], a[164:167], v[248:251], a[64:67]\n" \
"s_add_i32 s18, s18, s19\n" \
"s_or_b32 s15, s15, s5\n" \
"v_mfma_f32_16x16x32_bf16 a[64:67], a[228:231], v[48:51], a[64:67]\n" \
"v_writelane_b32 v11, s18, 28\n" \
"s_mul_i32 s18, s15, 0x24000\n" \
"v_mfma_f32_16x16x32_bf16 a[80:83], a[172:175], v[248:251], a[80:83]\n" \
"s_add_i32 s18, s18, s19\n" \
"v_writelane_b32 v11, s18, 29\n" \
"v_mfma_f32_16x16x32_bf16 a[80:83], a[236:239], v[48:51], a[80:83]\n" \
"s_or_b32 s18, s9, s14\n" \
"s_mul_i32 s18, s18, 0x24800\n" \
"v_mfma_f32_16x16x32_bf16 a[68:71], v[248:251], a[168:171], a[68:71]\n" \
"s_lshl_b32 s16, s16, 12\n" \
"s_add_i32 s19, s18, 0x3004800\n" \
"v_mfma_f32_16x16x32_bf16 a[68:71], v[48:51], a[232:235], a[68:71]\n" \
"s_waitcnt vmcnt(4) lgkmcnt(0)\n" \
"s_barrier\n" \
"v_mov_b32_e32 v45, s62\n" \
"s_mov_b64 exec, 1\n" \
"buffer_store_dword v45, off, s[80:83], s37 offset:0 sc1\n" \
"s_mov_b64 exec, -1\n" \
"s_nop 0\n" \
"v_mfma_f32_16x16x32_bf16 v[52:55], a[176:179], a[128:131], v[52:55]\n" \
"s_add_u32 m0, s40, 0x8000\n" \
"s_add_u32 s85, s39, 0xa000\n" \
"v_mfma_f32_16x16x32_bf16 v[52:55], a[240:243], a[192:195], v[52:55]\n" \
"buffer_load_dwordx4 v20, s[80:83], s85 offen sc1 lds\n" \
"v_mfma_f32_16x16x32_bf16 v[56:59], a[180:183], a[128:131], v[56:59]\n" \
"ds_read_b64_tr_b16 a[160:161], v32 offset:0\n" \
"v_accvgpr_read_b32 v220, a120\n" \
"v_mfma_f32_16x16x32_bf16 v[56:59], a[244:247], a[192:195], v[56:59]\n" \
"ds_read_b64_tr_b16 a[162:163], v32 offset:512\n" \
"v_accvgpr_read_b32 v221, a121\n" \
"v_mfma_f32_16x16x32_bf16 v[60:63], a[184:187], a[128:131], v[60:63]\n" \
"s_add_u32 m0, s40, 0x9000\n" \
"s_add_u32 s85, s39, 0xb000\n" \
"v_mfma_f32_16x16x32_bf16 v[60:63], a[248:251], a[192:195], v[60:63]\n" \
"buffer_load_dwordx4 v20, s[80:83], s85 offen sc1 lds\n" \
"v_mfma_f32_16x16x32_bf16 v[64:67], a[188:191], a[128:131], v[64:67]\n" \
"ds_read_b64_tr_b16 a[224:225], v32 offset:4096\n" \
"v_accvgpr_read_b32 v222, a122\n" \
"v_mfma_f32_16x16x32_bf16 v[64:67], a[252:255], a[192:195], v[64:67]\n" \
"ds_read_b128 a[128:131], v28 offset:0\n" \
"v_accvgpr_read_b32 v223, a123\n" \
"v_mfma_f32_16x16x32_bf16 v[80:83], a[188:191], a[132:135], v[80:83]\n" \
"ds_read_b128 a[192:195], v29 offset:0\n" \
"v_accvgpr_read_b32 v180, a124\n" \
"v_mfma_f32_16x16x32_bf16 v[80:83], a[252:255], a[196:199], v[80:83]\n" \
"s_add_u32 m0, s40, 0xb000\n" \
"s_movk_i32 s84, 0x280\n" \
"v_mfma_f32_16x16x32_bf16 v[76:79], a[184:187], a[132:135], v[76:79]\n" \
"buffer_load_dwordx4 v21, s[76:79], s84 offen offset:0 lds\n" \
"v_mfma_f32_16x16x32_bf16 v[76:79], a[248:251], a[196:199], v[76:79]\n" \
"ds_read_b64_tr_b16 a[226:227], v32 offset:4608\n" \
"v_accvgpr_read_b32 v181, a125\n" \
"v_mfma_f32_16x16x32_bf16 v[72:75], a[180:183], a[132:135], v[72:75]\n" \
"s_add_u32 m0, s30, 0x8000\n" \
"s_movk_i32 s84, 0x280\n" \
"v_mfma_f32_16x16x32_bf16 v[72:75], a[244:247], a[196:199], v[72:75]\n" \
"buffer_load_dwordx4 v24, s[76:79], s84 offen offset:0 lds\n" \
"v_mfma_f32_16x16x32_bf16 v[68:71], a[176:179], a[132:135], v[68:71]\n" \
"ds_read_b64_tr_b16 a[164:165], v33 offset:0\n" \
"v_accvgpr_read_b32 v182, a126\n" \
"v_mfma_f32_16x16x32_bf16 v[68:71], a[240:243], a[196:199], v[68:71]\n" \
"ds_read_b128 a[132:135], v28 offset:2048\n" \
"v_accvgpr_read_b32 v183, a127\n" \
"v_mfma_f32_16x16x32_bf16 v[84:87], a[176:179], a[136:139], v[84:87]\n" \
"ds_read_b128 a[196:199], v29 offset:2048\n" \
"v_cvt_pk_bf16_f32 v220, v220, v221\n" \
"v_mfma_f32_16x16x32_bf16 v[84:87], a[240:243], a[200:203], v[84:87]\n" \
"s_movk_i32 s84, 0x2280\n" \
"v_mfma_f32_16x16x32_bf16 v[88:91], a[180:183], a[136:139], v[88:91]\n" \
"buffer_load_dwordx4 v25, s[76:79], s84 offen offset:1024 lds\n" \
"v_mfma_f32_16x16x32_bf16 v[88:91], a[244:247], a[200:203], v[88:91]\n" \
"ds_read_b64_tr_b16 a[166:167], v33 offset:512\n" \
"v_cvt_pk_bf16_f32 v221, v222, v223\n" \
"v_mfma_f32_16x16x32_bf16 v[92:95], a[184:187], a[136:139], v[92:95]\n" \
"ds_read_b64_tr_b16 a[228:229], v33 offset:4096\n" \
"v_cvt_pk_bf16_f32 v222, v180, v181\n" \
"v_mfma_f32_16x16x32_bf16 v[92:95], a[248:251], a[200:203], v[92:95]\n" \
"s_movk_i32 s84, 0x4280\n" \
"v_mfma_f32_16x16x32_bf16 v[96:99], a[188:191], a[136:139], v[96:99]\n" \
"buffer_load_dwordx4 v24, s[76:79], s84 offen offset:2048 lds\n" \
"v_mfma_f32_16x16x32_bf16 v[96:99], a[252:255], a[200:203], v[96:99]\n" \
"ds_read_b128 a[136:139], v28 offset:4096\n" \
"v_cvt_pk_bf16_f32 v223, v182, v183\n" \
"v_mfma_f32_16x16x32_bf16 v[112:115], a[188:191], a[140:143], v[112:115]\n" \
"ds_read_b128 a[200:203], v29 offset:4096\n" \
"v_accvgpr_read_b32 v216, a112\n" \
"v_mfma_f32_16x16x32_bf16 v[112:115], a[252:255], a[204:207], v[112:115]\n" \
"s_movk_i32 s84, 0x6280\n" \
"s_nop 0\n" \
"v_mfma_f32_16x16x32_bf16 v[108:111], a[184:187], a[140:143], v[108:111]\n" \
"buffer_load_dwordx4 v25, s[76:79], s84 offen offset:3072 lds\n" \
"v_mfma_f32_16x16x32_bf16 v[108:111], a[248:251], a[204:207], v[108:111]\n" \
"ds_read_b64_tr_b16 a[230:231], v33 offset:4608\n" \
"v_accvgpr_read_b32 v217, a113\n" \
"v_mfma_f32_16x16x32_bf16 v[104:107], a[180:183], a[140:143], v[104:107]\n" \
"ds_read_b64_tr_b16 a[172:173], v35 offset:0\n" \
"v_accvgpr_read_b32 v218, a114\n" \
"v_mfma_f32_16x16x32_bf16 v[104:107], a[244:247], a[204:207], v[104:107]\n" \
"ds_read_b64_tr_b16 a[174:175], v35 offset:512\n" \
"v_accvgpr_read_b32 v219, a115\n" \
"v_mfma_f32_16x16x32_bf16 v[100:103], a[176:179], a[140:143], v[100:103]\n" \
"ds_read_b64_tr_b16 a[236:237], v35 offset:4096\n" \
"v_accvgpr_read_b32 v180, a116\n" \
"v_mfma_f32_16x16x32_bf16 v[100:103], a[240:243], a[204:207], v[100:103]\n" \
"ds_read_b128 a[140:143], v28 offset:6144\n" \
"v_accvgpr_read_b32 v181, a117\n" \
"v_mfma_f32_16x16x32_bf16 v[116:119], a[176:179], a[144:147], v[116:119]\n" \
"ds_read_b128 a[204:207], v29 offset:6144\n" \
"v_accvgpr_read_b32 v182, a118\n" \
"v_mfma_f32_16x16x32_bf16 v[116:119], a[240:243], a[208:211], v[116:119]\n" \
"s_add_u32 m0, s31, 0x8000\n" \
"s_mov_b32 s84, 0x280000\n" \
"v_mfma_f32_16x16x32_bf16 v[120:123], a[180:183], a[144:147], v[120:123]\n" \
"buffer_load_dwordx4 v26, s[68:71], s84 offen offset:0 lds\n" \
"v_mfma_f32_16x16x32_bf16 v[120:123], a[244:247], a[208:211], v[120:123]\n" \
"s_addk_i32 s84, 0x7c00\n" \
"s_nop 0\n" \
"v_mfma_f32_16x16x32_bf16 v[124:127], a[184:187], a[144:147], v[124:127]\n" \
"buffer_load_dwordx4 v26, s[68:71], s84 offen offset:1024 lds\n" \
"v_mfma_f32_16x16x32_bf16 v[124:127], a[248:251], a[208:211], v[124:127]\n" \
"ds_read_b64_tr_b16 a[238:239], v35 offset:4608\n" \
"v_accvgpr_read_b32 v183, a119\n" \
"v_mfma_f32_16x16x32_bf16 v[128:131], a[188:191], a[144:147], v[128:131]\n" \
"ds_read_b64_tr_b16 a[168:169], v34 offset:0\n" \
"v_cvt_pk_bf16_f32 v216, v216, v217\n" \
"v_mfma_f32_16x16x32_bf16 v[128:131], a[252:255], a[208:211], v[128:131]\n" \
"ds_read_b128 a[144:147], v28 offset:8192\n" \
"v_cvt_pk_bf16_f32 v217, v218, v219\n" \
"v_mfma_f32_16x16x32_bf16 v[144:147], a[188:191], a[148:151], v[144:147]\n" \
"ds_read_b128 a[208:211], v29 offset:8192\n" \
"v_cvt_pk_bf16_f32 v218, v180, v181\n" \
"v_mfma_f32_16x16x32_bf16 v[144:147], a[252:255], a[212:215], v[144:147]\n" \
"s_addk_i32 s84, 0x7c00\n" \
"v_mfma_f32_16x16x32_bf16 v[140:143], a[184:187], a[148:151], v[140:143]\n" \
"buffer_load_dwordx4 v27, s[68:71], s84 offen offset:2048 lds\n" \
"v_mfma_f32_16x16x32_bf16 v[140:143], a[248:251], a[212:215], v[140:143]\n" \
"s_addk_i32 s84, 0x7c00\n" \
"v_mfma_f32_16x16x32_bf16 v[136:139], a[180:183], a[148:151], v[136:139]\n" \
"buffer_load_dwordx4 v27, s[68:71], s84 offen offset:3072 lds\n" \
"v_mfma_f32_16x16x32_bf16 v[136:139], a[244:247], a[212:215], v[136:139]\n" \
"ds_read_b64_tr_b16 a[170:171], v34 offset:512\n" \
"v_cvt_pk_bf16_f32 v219, v182, v183\n" \
"v_mfma_f32_16x16x32_bf16 v[132:135], a[176:179], a[148:151], v[132:135]\n" \
"ds_read_b64_tr_b16 a[232:233], v34 offset:4096\n" \
"v_accvgpr_read_b32 v196, a104\n" \
"v_mfma_f32_16x16x32_bf16 v[132:135], a[240:243], a[212:215], v[132:135]\n" \
"ds_read_b128 a[148:151], v28 offset:10240\n" \
"v_accvgpr_read_b32 v197, a105\n" \
"v_mfma_f32_16x16x32_bf16 v[148:151], a[176:179], a[152:155], v[148:151]\n" \
"ds_read_b128 a[212:215], v29 offset:10240\n" \
"v_accvgpr_read_b32 v198, a106\n" \
"v_mfma_f32_16x16x32_bf16 v[148:151], a[240:243], a[216:219], v[148:151]\n" \
"s_add_u32 m0, s30, 0x9000\n" \
"s_mov_b32 s84, 0x9280\n" \
"v_mfma_f32_16x16x32_bf16 v[152:155], a[180:183], a[152:155], v[152:155]\n" \
"buffer_load_dwordx4 v24, s[76:79], s84 offen offset:0 lds\n" \
"v_mfma_f32_16x16x32_bf16 v[152:155], a[244:247], a[216:219], v[152:155]\n" \
"s_addk_i32 s84, 0x2000\n" \
"s_nop 0\n" \
"v_mfma_f32_16x16x32_bf16 v[156:159], a[184:187], a[152:155], v[156:159]\n" \
"buffer_load_dwordx4 v25, s[76:79], s84 offen offset:1024 lds\n" \
"v_mfma_f32_16x16x32_bf16 v[156:159], a[248:251], a[216:219], v[156:159]\n" \
"ds_read_b64_tr_b16 a[234:235], v34 offset:4608\n" \
"v_accvgpr_read_b32 v199, a107\n" \
"v_mfma_f32_16x16x32_bf16 v[160:163], a[188:191], a[152:155], v[160:163]\n" \
"ds_read_b128 v[248:251], v22 offset:0\n" \
"v_accvgpr_read_b32 v180, a108\n" \
"v_mfma_f32_16x16x32_bf16 v[160:163], a[252:255], a[216:219], v[160:163]\n" \
"ds_read_b128 a[152:155], v28 offset:12288\n" \
"v_accvgpr_read_b32 v181, a109\n" \
"v_mfma_f32_16x16x32_bf16 v[176:179], a[188:191], a[156:159], v[176:179]\n" \
"ds_read_b128 a[216:219], v29 offset:12288\n" \
"v_accvgpr_read_b32 v182, a110\n" \
"v_mfma_f32_16x16x32_bf16 v[176:179], a[252:255], a[220:223], v[176:179]\n" \
"ds_read_b128 v[48:51], v23 offset:0\n" \
"v_accvgpr_read_b32 v183, a111\n" \
"v_mfma_f32_16x16x32_bf16 v[172:175], a[184:187], a[156:159], v[172:175]\n" \
"v_cvt_pk_bf16_f32 v196, v196, v197\n" \
"v_cvt_pk_bf16_f32 v197, v198, v199\n" \
"v_mfma_f32_16x16x32_bf16 v[172:175], a[248:251], a[220:223], v[172:175]\n" \
"v_cvt_pk_bf16_f32 v198, v180, v181\n" \
"v_cvt_pk_bf16_f32 v199, v182, v183\n" \
"v_mfma_f32_16x16x32_bf16 v[168:171], a[180:183], a[156:159], v[168:171]\n" \
"v_accvgpr_read_b32 v192, a96\n" \
"v_accvgpr_read_b32 v193, a97\n" \
"v_mfma_f32_16x16x32_bf16 v[168:171], a[244:247], a[220:223], v[168:171]\n" \
"v_accvgpr_read_b32 v194, a98\n" \
"v_accvgpr_read_b32 v195, a99\n" \
"v_mfma_f32_16x16x32_bf16 v[164:167], a[176:179], a[156:159], v[164:167]\n" \
"v_accvgpr_read_b32 v180, a100\n" \
"v_accvgpr_read_b32 v181, a101\n" \
"v_mfma_f32_16x16x32_bf16 v[164:167], a[240:243], a[220:223], v[164:167]\n" \
"ds_read_b128 a[156:159], v28 offset:14336\n" \
"ds_read_b128 a[220:223], v29 offset:14336\n" \
"s_waitcnt lgkmcnt(15)\n" \
"v_mfma_f32_16x16x32_bf16 a[0:3], a[160:163], a[128:131], a[0:3]\n" \
"s_addk_i32 s84, 0x2000\n" \
"v_mfma_f32_16x16x32_bf16 a[0:3], a[224:227], a[192:195], a[0:3]\n" \
"buffer_load_dwordx4 v24, s[76:79], s84 offen offset:2048 lds\n" \
"v_mfma_f32_16x16x32_bf16 a[4:7], a[164:167], a[128:131], a[4:7]\n" \
"ds_read_b64_tr_b16 a[176:177], v36 offset:0\n" \
"v_accvgpr_read_b32 v182, a102\n" \
"v_mfma_f32_16x16x32_bf16 a[4:7], a[228:231], a[192:195], a[4:7]\n" \
"s_addk_i32 s84, 0x2000\n" \
"v_mfma_f32_16x16x32_bf16 a[12:15], a[172:175], a[128:131], a[12:15]\n" \
"buffer_load_dwordx4 v25, s[76:79], s84 offen offset:3072 lds\n" \
"s_waitcnt lgkmcnt(15)\n" \
"v_mfma_f32_16x16x32_bf16 a[12:15], a[236:239], a[192:195], a[12:15]\n" \
"ds_read_b64_tr_b16 a[178:179], v36 offset:1024\n" \
"v_accvgpr_read_b32 v183, a103\n" \
"s_waitcnt lgkmcnt(12)\n" \
"v_mfma_f32_16x16x32_bf16 a[8:11], a[128:131], a[168:171], a[8:11]\n" \
"ds_read_b64_tr_b16 a[240:241], v36 offset:8192\n" \
"v_cvt_pk_bf16_f32 v192, v192, v193\n" \
"s_waitcnt lgkmcnt(9)\n" \
"v_mfma_f32_16x16x32_bf16 a[8:11], a[192:195], a[232:235], a[8:11]\n" \
"ds_read_b64_tr_b16 a[242:243], v36 offset:9216\n" \
"v_cvt_pk_bf16_f32 v193, v194, v195\n" \
"v_mfma_f32_16x16x32_bf16 a[24:27], a[132:135], a[168:171], a[24:27]\n" \
"ds_read_b64_tr_b16 a[180:181], v37 offset:0\n" \
"v_cvt_pk_bf16_f32 v194, v180, v181\n" \
"v_mfma_f32_16x16x32_bf16 a[24:27], a[196:199], a[232:235], a[24:27]\n" \
"ds_read_b64_tr_b16 a[182:183], v37 offset:1024\n" \
"v_cvt_pk_bf16_f32 v195, v182, v183\n" \
"v_mfma_f32_16x16x32_bf16 a[28:31], a[172:175], a[132:135], a[28:31]\n" \
"ds_read_b64_tr_b16 a[244:245], v37 offset:8192\n" \
"v_accvgpr_read_b32 v188, a88\n" \
"v_mfma_f32_16x16x32_bf16 a[28:31], a[236:239], a[196:199], a[28:31]\n" \
"ds_read_b64_tr_b16 a[246:247], v37 offset:9216\n" \
"v_accvgpr_read_b32 v189, a89\n" \
"v_mfma_f32_16x16x32_bf16 a[20:23], a[164:167], a[132:135], a[20:23]\n" \
"v_accvgpr_read_b32 v190, a90\n" \
"v_accvgpr_read_b32 v191, a91\n" \
"v_mfma_f32_16x16x32_bf16 a[20:23], a[228:231], a[196:199], a[20:23]\n" \
"s_add_u32 s89, s42, 0x1000\n" \
"buffer_store_dwordx4 a[84:87], v20, s[80:83], s89 offen offset:3072 sc1\n" \
"v_mfma_f32_16x16x32_bf16 a[16:19], a[160:163], a[132:135], a[16:19]\n" \
"ds_read_b64_tr_b16 a[184:185], v38 offset:0\n" \
"v_accvgpr_read_b32 v180, a92\n" \
"v_mfma_f32_16x16x32_bf16 a[16:19], a[224:227], a[196:199], a[16:19]\n" \
"ds_read_b64_tr_b16 a[186:187], v38 offset:1024\n" \
"v_accvgpr_read_b32 v181, a93\n" \
"v_mfma_f32_16x16x32_bf16 a[32:35], a[160:163], a[136:139], a[32:35]\n" \
"ds_read_b64_tr_b16 a[248:249], v38 offset:8192\n" \
"v_accvgpr_read_b32 v182, a94\n" \
"v_mfma_f32_16x16x32_bf16 a[32:35], a[224:227], a[200:203], a[32:35]\n" \
"ds_read_b64_tr_b16 a[250:251], v38 offset:9216\n" \
"v_accvgpr_read_b32 v183, a95\n" \
"v_mfma_f32_16x16x32_bf16 a[36:39], a[164:167], a[136:139], a[36:39]\n" \
"v_cvt_pk_bf16_f32 v188, v188, v189\n" \
"v_cvt_pk_bf16_f32 v189, v190, v191\n" \
"v_mfma_f32_16x16x32_bf16 a[36:39], a[228:231], a[200:203], a[36:39]\n" \
"s_add_u32 s88, s42, 0x1000\n" \
"buffer_store_dwordx4 v[252:255], v20, s[80:83], s88 offen offset:2048 sc1\n" \
"v_mfma_f32_16x16x32_bf16 a[44:47], a[172:175], a[136:139], a[44:47]\n" \
"ds_read_b64_tr_b16 a[188:189], v39 offset:0\n" \
"v_cvt_pk_bf16_f32 v190, v180, v181\n" \
"v_mfma_f32_16x16x32_bf16 a[44:47], a[236:239], a[200:203], a[44:47]\n" \
"ds_read_b64_tr_b16 a[190:191], v39 offset:1024\n" \
"v_cvt_pk_bf16_f32 v191, v182, v183\n" \
"v_mfma_f32_16x16x32_bf16 a[40:43], a[136:139], a[168:171], a[40:43]\n" \
"ds_read_b64_tr_b16 a[252:253], v39 offset:8192\n" \
"v_accvgpr_read_b32 v184, a72\n" \
"v_mfma_f32_16x16x32_bf16 a[40:43], a[200:203], a[232:235], a[40:43]\n" \
"v_accvgpr_read_b32 v185, a73\n" \
"v_accvgpr_read_b32 v186, a74\n" \
"v_mfma_f32_16x16x32_bf16 a[56:59], a[140:143], a[168:171], a[56:59]\n" \
"ds_read_b64_tr_b16 a[254:255], v39 offset:9216\n" \
"v_accvgpr_read_b32 v187, a75\n" \
"v_mfma_f32_16x16x32_bf16 a[56:59], a[204:207], a[232:235], a[56:59]\n" \
"v_accvgpr_read_b32 v180, a76\n" \
"v_accvgpr_read_b32 v181, a77\n" \
"v_mfma_f32_16x16x32_bf16 a[60:63], a[172:175], a[140:143], a[60:63]\n" \
"s_add_u32 s89, s42, 0x2000\n" \
"buffer_store_dwordx4 v[200:203], v20, s[80:83], s89 offen offset:2048 sc1\n" \
"v_mfma_f32_16x16x32_bf16 a[60:63], a[236:239], a[204:207], a[60:63]\n" \
"v_accvgpr_read_b32 v182, a78\n" \
"v_accvgpr_read_b32 v183, a79\n" \
"v_mfma_f32_16x16x32_bf16 a[52:55], a[164:167], a[140:143], a[52:55]\n" \
"v_cvt_pk_bf16_f32 v184, v184, v185\n" \
"v_cvt_pk_bf16_f32 v185, v186, v187\n" \
"v_mfma_f32_16x16x32_bf16 a[52:55], a[228:231], a[204:207], a[52:55]\n" \
"v_cvt_pk_bf16_f32 v186, v180, v181\n" \
".fill (L_pb + 28384 - .) / 4, 4, 0xBF800000\n" \
"L_pp7:\n" \
"s_cbranch_execz L_pret\n" \
"v_cvt_pk_bf16_f32 v187, v182, v183\n" \
"v_mfma_f32_16x16x32_bf16 a[48:51], a[160:163], a[140:143], a[48:51]\n" \
"s_add_i32 s20, s19, s16\n" \
"v_writelane_b32 v11, s20, 30\n" \
"v_mfma_f32_16x16x32_bf16 a[48:51], a[224:227], a[204:207], a[48:51]\n" \
"s_lshl_b32 s13, s13, 12\n" \
"s_lshl_b32 s15, s15, 12\n" \
"s_waitcnt lgkmcnt(15)\n" \
"v_mfma_f32_16x16x32_bf16 a[64:67], a[164:167], v[248:251], a[64:67]\n" \
"s_sub_i32 s14, s9, s14\n" \
"s_add_i32 s20, s19, s13\n" \
"v_mfma_f32_16x16x32_bf16 a[64:67], a[228:231], v[48:51], a[64:67]\n" \
"v_writelane_b32 v11, s20, 31\n" \
"s_add_i32 s19, s19, s15\n" \
"v_mfma_f32_16x16x32_bf16 a[80:83], a[172:175], v[248:251], a[80:83]\n" \
"s_mul_i32 s14, s14, 0x24800\n" \
"v_writelane_b32 v11, s19, 32\n" \
"v_mfma_f32_16x16x32_bf16 a[80:83], a[236:239], v[48:51], a[80:83]\n" \
"s_add_i32 s19, s14, 0x4120800\n" \
"s_add_i32 s16, s19, s16\n" \
"v_mfma_f32_16x16x32_bf16 a[68:71], v[248:251], a[168:171], a[68:71]\n" \
"v_writelane_b32 v11, s16, 33\n" \
"s_add_i32 s13, s19, s13\n" \
"v_mfma_f32_16x16x32_bf16 a[68:71], v[48:51], a[232:235], a[68:71]\n" \
"s_waitcnt vmcnt(3) lgkmcnt(0)\n" \
"s_barrier\n" \
"v_mfma_f32_16x16x32_bf16 v[52:55], a[176:179], a[128:131], v[52:55]\n" \
"s_add_u32 m0, s40, 0x0\n" \
"s_add_u32 s85, s39, 0xc000\n" \
"v_mfma_f32_16x16x32_bf16 v[52:55], a[240:243], a[192:195], v[52:55]\n" \
"buffer_load_dwordx4 v20, s[80:83], s85 offen sc1 lds\n" \
"v_mfma_f32_16x16x32_bf16 v[56:59], a[180:183], a[128:131], v[56:59]\n" \
"ds_read_b64_tr_b16 a[160:161], v32 offset:32768\n" \
"v_writelane_b32 v11, s13, 34\n" \
"v_mfma_f32_16x16x32_bf16 v[56:59], a[244:247], a[192:195], v[56:59]\n" \
"ds_read_b64_tr_b16 a[162:163], v32 offset:33280\n" \
"s_add_i32 s19, s19, s15\n" \
"v_mfma_f32_16x16x32_bf16 v[60:63], a[184:187], a[128:131], v[60:63]\n" \
"s_add_u32 m0, s40, 0x1000\n" \
"s_add_u32 s85, s39, 0xd000\n" \
"v_mfma_f32_16x16x32_bf16 v[60:63], a[248:251], a[192:195], v[60:63]\n" \
"buffer_load_dwordx4 v20, s[80:83], s85 offen sc1 lds\n" \
"v_mfma_f32_16x16x32_bf16 v[64:67], a[188:191], a[128:131], v[64:67]\n" \
"ds_read_b64_tr_b16 a[224:225], v32 offset:36864\n" \
"s_or_b32 s13, s18, 0x3000000\n" \
"v_mfma_f32_16x16x32_bf16 v[64:67], a[252:255], a[192:195], v[64:67]\n" \
"ds_read_b128 a[128:131], v28 offset:32768\n" \
"s_lshl_b32 s15, s6, 11\n" \
"v_mfma_f32_16x16x32_bf16 v[80:83], a[188:191], a[132:135], v[80:83]\n" \
"ds_read_b128 a[192:195], v29 offset:32768\n" \
"v_writelane_b32 v11, s19, 35\n" \
"v_mfma_f32_16x16x32_bf16 v[80:83], a[252:255], a[196:199], v[80:83]\n" \
"s_add_u32 m0, s40, 0x3000\n" \
"s_movk_i32 s84, 0x300\n" \
"v_mfma_f32_16x16x32_bf16 v[76:79], a[184:187], a[132:135], v[76:79]\n" \
"buffer_load_dwordx4 v21, s[76:79], s84 offen offset:0 lds\n" \
"v_mfma_f32_16x16x32_bf16 v[76:79], a[248:251], a[196:199], v[76:79]\n" \
"ds_read_b64_tr_b16 a[226:227], v32 offset:37376\n" \
"s_add_i32 s16, s13, s15\n" \
"v_mfma_f32_16x16x32_bf16 v[72:75], a[180:183], a[132:135], v[72:75]\n" \
"s_add_u32 m0, s30, 0x0\n" \
"s_movk_i32 s84, 0x300\n" \
"v_mfma_f32_16x16x32_bf16 v[72:75], a[244:247], a[196:199], v[72:75]\n" \
"buffer_load_dwordx4 v24, s[76:79], s84 offen offset:0 lds\n" \
"v_mfma_f32_16x16x32_bf16 v[68:71], a[176:179], a[132:135], v[68:71]\n" \
"ds_read_b64_tr_b16 a[164:165], v33 offset:32768\n" \
"v_writelane_b32 v11, s16, 36\n" \
"v_mfma_f32_16x16x32_bf16 v[68:71], a[240:243], a[196:199], v[68:71]\n" \
"ds_read_b128 a[132:135], v28 offset:34816\n" \
"s_add_i32 s16, s14, 0x411c000\n" \
"v_mfma_f32_16x16x32_bf16 v[84:87], a[176:179], a[136:139], v[84:87]\n" \
"ds_read_b128 a[196:199], v29 offset:34816\n" \
"s_add_i32 s19, s16, s15\n" \
"v_mfma_f32_16x16x32_bf16 v[84:87], a[240:243], a[200:203], v[84:87]\n" \
"s_movk_i32 s84, 0x2300\n" \
"v_mfma_f32_16x16x32_bf16 v[88:91], a[180:183], a[136:139], v[88:91]\n" \
"buffer_load_dwordx4 v25, s[76:79], s84 offen offset:1024 lds\n" \
"v_mfma_f32_16x16x32_bf16 v[88:91], a[244:247], a[200:203], v[88:91]\n" \
"ds_read_b64_tr_b16 a[166:167], v33 offset:33280\n" \
"v_writelane_b32 v11, s19, 37\n" \
"v_mfma_f32_16x16x32_bf16 v[92:95], a[184:187], a[136:139], v[92:95]\n" \
"ds_read_b64_tr_b16 a[228:229], v33 offset:36864\n" \
"s_add_i32 s18, s18, 0x3004000\n" \
"v_mfma_f32_16x16x32_bf16 v[92:95], a[248:251], a[200:203], v[92:95]\n" \
"s_movk_i32 s84, 0x4300\n" \
"v_mfma_f32_16x16x32_bf16 v[96:99], a[188:191], a[136:139], v[96:99]\n" \
"buffer_load_dwordx4 v24, s[76:79], s84 offen offset:2048 lds\n" \
"v_mfma_f32_16x16x32_bf16 v[96:99], a[252:255], a[200:203], v[96:99]\n" \
"ds_read_b128 a[136:139], v28 offset:36864\n" \
"v_writelane_b32 v11, s18, 38\n" \
"v_mfma_f32_16x16x32_bf16 v[112:115], a[188:191], a[140:143], v[112:115]\n" \
"ds_read_b128 a[200:203], v29 offset:36864\n" \
"s_add_i32 s14, s14, 0x4120000\n" \
"v_mfma_f32_16x16x32_bf16 v[112:115], a[252:255], a[204:207], v[112:115]\n" \
"s_movk_i32 s84, 0x6300\n" \
"v_mfma_f32_16x16x32_bf16 v[108:111], a[184:187], a[140:143], v[108:111]\n" \
"buffer_load_dwordx4 v25, s[76:79], s84 offen offset:3072 lds\n" \
"v_mfma_f32_16x16x32_bf16 v[108:111], a[248:251], a[204:207], v[108:111]\n" \
"ds_read_b64_tr_b16 a[230:231], v33 offset:37376\n" \
"v_writelane_b32 v11, s14, 39\n" \
"v_mfma_f32_16x16x32_bf16 v[104:107], a[180:183], a[140:143], v[104:107]\n" \
"ds_read_b64_tr_b16 a[172:173], v35 offset:32768\n" \
"s_lshl_b32 s14, s4, 2\n" \
"v_mfma_f32_16x16x32_bf16 v[104:107], a[244:247], a[204:207], v[104:107]\n" \
"ds_read_b64_tr_b16 a[174:175], v35 offset:33280\n" \
"s_or_b32 s8, s14, s8\n" \
"v_mfma_f32_16x16x32_bf16 v[100:103], a[176:179], a[140:143], v[100:103]\n" \
"ds_read_b64_tr_b16 a[236:237], v35 offset:36864\n" \
"s_mul_i32 s8, s8, 0x90000\n" \
"v_mfma_f32_16x16x32_bf16 v[100:103], a[240:243], a[204:207], v[100:103]\n" \
"ds_read_b128 a[140:143], v28 offset:38912\n" \
"s_lshl_b32 s14, s3, 14\n" \
"v_mfma_f32_16x16x32_bf16 v[116:119], a[176:179], a[144:147], v[116:119]\n" \
"ds_read_b128 a[204:207], v29 offset:38912\n" \
"s_add_i32 s8, s8, s14\n" \
"v_mfma_f32_16x16x32_bf16 v[116:119], a[240:243], a[208:211], v[116:119]\n" \
"s_add_u32 m0, s31, 0x0\n" \
"s_mov_b32 s84, 0x300000\n" \
"v_mfma_f32_16x16x32_bf16 v[120:123], a[180:183], a[144:147], v[120:123]\n" \
"buffer_load_dwordx4 v26, s[68:71], s84 offen offset:0 lds\n" \
"v_mfma_f32_16x16x32_bf16 v[120:123], a[244:247], a[208:211], v[120:123]\n" \
"s_addk_i32 s84, 0x7c00\n" \
"v_mfma_f32_16x16x32_bf16 v[124:127], a[184:187], a[144:147], v[124:127]\n" \
"buffer_load_dwordx4 v26, s[68:71], s84 offen offset:1024 lds\n" \
"v_mfma_f32_16x16x32_bf16 v[124:127], a[248:251], a[208:211], v[124:127]\n" \
"ds_read_b64_tr_b16 a[238:239], v35 offset:37376\n" \
"s_lshl_b32 s9, s9, 4\n" \
"v_mfma_f32_16x16x32_bf16 v[128:131], a[188:191], a[144:147], v[128:131]\n" \
"ds_read_b64_tr_b16 a[168:169], v34 offset:32768\n" \
"s_lshl_b32 s14, s4, 7\n" \
"v_mfma_f32_16x16x32_bf16 v[128:131], a[252:255], a[208:211], v[128:131]\n" \
"ds_read_b128 a[144:147], v28 offset:40960\n" \
"s_or_b32 s18, s9, s14\n" \
"v_mfma_f32_16x16x32_bf16 v[144:147], a[188:191], a[148:151], v[144:147]\n" \
"ds_read_b128 a[208:211], v29 offset:40960\n" \
"s_sub_i32 s9, s9, s14\n" \
"v_mfma_f32_16x16x32_bf16 v[144:147], a[252:255], a[212:215], v[144:147]\n" \
"s_addk_i32 s84, 0x7c00\n" \
"v_mfma_f32_16x16x32_bf16 v[140:143], a[184:187], a[148:151], v[140:143]\n" \
"buffer_load_dwordx4 v27, s[68:71], s84 offen offset:2048 lds\n" \
"v_mfma_f32_16x16x32_bf16 v[140:143], a[248:251], a[212:215], v[140:143]\n" \
"s_addk_i32 s84, 0x7c00\n" \
"v_mfma_f32_16x16x32_bf16 v[136:139], a[180:183], a[148:151], v[136:139]\n" \
"buffer_load_dwordx4 v27, s[68:71], s84 offen offset:3072 lds\n" \
"v_mfma_f32_16x16x32_bf16 v[136:139], a[244:247], a[212:215], v[136:139]\n" \
"ds_read_b64_tr_b16 a[170:171], v34 offset:33280\n" \
"s_mulk_i32 s18, 0x480\n" \
"v_mfma_f32_16x16x32_bf16 v[132:135], a[176:179], a[148:151], v[132:135]\n" \
"ds_read_b64_tr_b16 a[232:233], v34 offset:36864\n" \
"s_mulk_i32 s9, 0x480\n" \
"v_mfma_f32_16x16x32_bf16 v[132:135], a[240:243], a[212:215], v[132:135]\n" \
"ds_read_b128 a[148:151], v28 offset:43008\n" \
"s_or_b32 s14, s18, s12\n" \
"v_mfma_f32_16x16x32_bf16 v[148:151], a[176:179], a[152:155], v[148:151]\n" \
"ds_read_b128 a[212:215], v29 offset:43008\n" \
"s_or_b32 s9, s9, s12\n" \
"v_mfma_f32_16x16x32_bf16 v[148:151], a[240:243], a[216:219], v[148:151]\n" \
"s_add_u32 m0, s30, 0x1000\n" \
"s_mov_b32 s84, 0x9300\n" \
"v_mfma_f32_16x16x32_bf16 v[152:155], a[180:183], a[152:155], v[152:155]\n" \
"buffer_load_dwordx4 v24, s[76:79], s84 offen offset:0 lds\n" \
"v_mfma_f32_16x16x32_bf16 v[152:155], a[244:247], a[216:219], v[152:155]\n" \
"s_addk_i32 s84, 0x2000\n" \
"v_mfma_f32_16x16x32_bf16 v[156:159], a[184:187], a[152:155], v[156:159]\n" \
"buffer_load_dwordx4 v25, s[76:79], s84 offen offset:1024 lds\n" \
"v_mfma_f32_16x16x32_bf16 v[156:159], a[248:251], a[216:219], v[156:159]\n" \
"ds_read_b64_tr_b16 a[234:235], v34 offset:37376\n" \
"s_and_b32 s12, s2, 1\n" \
"v_mfma_f32_16x16x32_bf16 v[160:163], a[188:191], a[152:155], v[160:163]\n" \
"ds_read_b128 v[248:251], v22 offset:32768\n" \
"v_mfma_f32_16x16x32_bf16 v[160:163], a[252:255], a[216:219], v[160:163]\n" \
"ds_read_b128 a[152:155], v28 offset:45056\n" \
"v_mfma_f32_16x16x32_bf16 v[176:179], a[188:191], a[156:159], v[176:179]\n" \
"ds_read_b128 a[216:219], v29 offset:45056\n" \
"v_mfma_f32_16x16x32_bf16 v[176:179], a[252:255], a[220:223], v[176:179]\n" \
"ds_read_b128 v[48:51], v23 offset:32768\n" \
"v_mfma_f32_16x16x32_bf16 v[172:175], a[184:187], a[156:159], v[172:175]\n" \
"s_cmp_lg_u32 s12, 0\n" \
"s_cselect_b32 s13, s16, s13\n" \
"v_mfma_f32_16x16x32_bf16 v[172:175], a[248:251], a[220:223], v[172:175]\n" \
"s_bfe_u32 s16, s2, 0x20001\n" \
"s_addk_i32 s13, 0x4000\n" \
"v_mfma_f32_16x16x32_bf16 v[168:171], a[180:183], a[156:159], v[168:171]\n" \
"s_xor_b32 s16, s16, s3\n" \
"v_mfma_f32_16x16x32_bf16 v[168:171], a[244:247], a[220:223], v[168:171]\n" \
"s_cmp_lg_u32 s16, 0\n" \
"s_cselect_b32 s13, s7, s13\n" \
"v_mfma_f32_16x16x32_bf16 v[164:167], a[176:179], a[156:159], v[164:167]\n" \
"s_lshl_b32 s6, s6, 16\n" \
"v_writelane_b32 v11, s13, 40\n" \
"v_mfma_f32_16x16x32_bf16 v[164:167], a[240:243], a[220:223], v[164:167]\n" \
"ds_read_b128 a[156:159], v28 offset:47104\n" \
"ds_read_b128 a[220:223], v29 offset:47104\n" \
"s_waitcnt lgkmcnt(15)\n" \
"v_mfma_f32_16x16x32_bf16 a[0:3], a[160:163], a[128:131], a[0:3]\n" \
"s_addk_i32 s84, 0x2000\n" \
"v_mfma_f32_16x16x32_bf16 a[0:3], a[224:227], a[192:195], a[0:3]\n" \
"buffer_load_dwordx4 v24, s[76:79], s84 offen offset:2048 lds\n" \
"v_mfma_f32_16x16x32_bf16 a[4:7], a[164:167], a[128:131], a[4:7]\n" \
"ds_read_b64_tr_b16 a[176:177], v36 offset:32768\n" \
"s_add_i32 s13, s8, 0x4240000\n" \
"v_mfma_f32_16x16x32_bf16 a[4:7], a[228:231], a[192:195], a[4:7]\n" \
"s_addk_i32 s84, 0x2000\n" \
"v_mfma_f32_16x16x32_bf16 a[12:15], a[172:175], a[128:131], a[12:15]\n" \
"buffer_load_dwordx4 v25, s[76:79], s84 offen offset:3072 lds\n" \
"s_waitcnt lgkmcnt(15)\n" \
"v_mfma_f32_16x16x32_bf16 a[12:15], a[236:239], a[192:195], a[12:15]\n" \
"ds_read_b64_tr_b16 a[178:179], v36 offset:33792\n" \
"s_add_i32 s6, s13, s6\n" \
"s_waitcnt lgkmcnt(12)\n" \
"v_mfma_f32_16x16x32_bf16 a[8:11], a[128:131], a[168:171], a[8:11]\n" \
"ds_read_b64_tr_b16 a[240:241], v36 offset:40960\n" \
"v_writelane_b32 v11, s6, 41\n" \
"s_waitcnt lgkmcnt(9)\n" \
"v_mfma_f32_16x16x32_bf16 a[8:11], a[192:195], a[232:235], a[8:11]\n" \
"ds_read_b64_tr_b16 a[242:243], v36 offset:41984\n" \
"s_add_i32 s8, s8, 0x42c0000\n" \
"v_mfma_f32_16x16x32_bf16 a[24:27], a[132:135], a[168:171], a[24:27]\n" \
"ds_read_b64_tr_b16 a[180:181], v37 offset:32768\n" \
"s_lshl_b32 s6, s12, 10\n" \
"v_mfma_f32_16x16x32_bf16 a[24:27], a[196:199], a[232:235], a[24:27]\n" \
"ds_read_b64_tr_b16 a[182:183], v37 offset:33792\n" \
"s_or_b32 s6, s8, s6\n" \
"v_mfma_f32_16x16x32_bf16 a[28:31], a[172:175], a[132:135], a[28:31]\n" \
"ds_read_b64_tr_b16 a[244:245], v37 offset:40960\n" \
"v_mfma_f32_16x16x32_bf16 a[28:31], a[236:239], a[196:199], a[28:31]\n" \
"ds_read_b64_tr_b16 a[246:247], v37 offset:41984\n" \
"v_mfma_f32_16x16x32_bf16 a[20:23], a[164:167], a[132:135], a[20:23]\n" \
"s_cmp_lg_u32 s16, 0\n" \
"s_cselect_b32 s6, s7, s6\n" \
"v_mfma_f32_16x16x32_bf16 a[20:23], a[228:231], a[196:199], a[20:23]\n" \
"s_add_u32 s88, s42, 0x2000\n" \
"buffer_store_dwordx4 v[204:207], v20, s[80:83], s88 offen offset:3072 sc1\n" \
"v_mfma_f32_16x16x32_bf16 a[16:19], a[160:163], a[132:135], a[16:19]\n" \
"ds_read_b64_tr_b16 a[184:185], v38 offset:32768\n" \
"s_mulk_i32 s4, 0x4800\n" \
"v_mfma_f32_16x16x32_bf16 a[16:19], a[224:227], a[196:199], a[16:19]\n" \
"ds_read_b64_tr_b16 a[186:187], v38 offset:33792\n" \
"v_writelane_b32 v11, s6, 42\n" \
"v_mfma_f32_16x16x32_bf16 a[32:35], a[160:163], a[136:139], a[32:35]\n" \
"ds_read_b64_tr_b16 a[248:249], v38 offset:40960\n" \
"s_add_i32 s6, s14, s10\n" \
"v_mfma_f32_16x16x32_bf16 a[32:35], a[224:227], a[200:203], a[32:35]\n" \
"ds_read_b64_tr_b16 a[250:251], v38 offset:41984\n" \
"v_writelane_b32 v11, s6, 43\n" \
"v_mfma_f32_16x16x32_bf16 a[36:39], a[164:167], a[136:139], a[36:39]\n" \
"s_add_i32 s6, s9, 0x21c000\n" \
"s_add_i32 s9, s6, s10\n" \
"v_mfma_f32_16x16x32_bf16 a[36:39], a[228:231], a[200:203], a[36:39]\n" \
"s_add_u32 s89, s42, 0x3000\n" \
"buffer_store_dwordx4 v[208:211], v20, s[80:83], s89 offen offset:3072 sc1\n" \
"v_mfma_f32_16x16x32_bf16 a[44:47], a[172:175], a[136:139], a[44:47]\n" \
"ds_read_b64_tr_b16 a[188:189], v39 offset:32768\n" \
"v_mfma_f32_16x16x32_bf16 a[44:47], a[236:239], a[200:203], a[44:47]\n" \
"ds_read_b64_tr_b16 a[190:191], v39 offset:33792\n" \
"v_mfma_f32_16x16x32_bf16 a[40:43], a[136:139], a[168:171], a[40:43]\n" \
"ds_read_b64_tr_b16 a[252:253], v39 offset:40960\n" \
"v_mfma_f32_16x16x32_bf16 a[40:43], a[200:203], a[232:235], a[40:43]\n" \
"s_cmp_lg_u32 s12, 0\n" \
"s_cselect_b32 s6, s6, s14\n" \
"v_mfma_f32_16x16x32_bf16 a[56:59], a[140:143], a[168:171], a[56:59]\n" \
"ds_read_b64_tr_b16 a[254:255], v39 offset:41984\n" \
"s_addk_i32 s6, 0x100\n" \
"v_mfma_f32_16x16x32_bf16 a[56:59], a[204:207], a[232:235], a[56:59]\n" \
"v_writelane_b32 v11, s9, 44\n" \
"v_mfma_f32_16x16x32_bf16 a[60:63], a[172:175], a[140:143], a[60:63]\n" \
"s_add_u32 s88, s42, 0x3000\n" \
"buffer_store_dwordx4 v[212:215], v20, s[80:83], s88 offen offset:2048 sc1\n" \
"v_mfma_f32_16x16x32_bf16 a[60:63], a[236:239], a[204:207], a[60:63]\n" \
"s_cmp_lg_u32 s16, 0\n" \
"s_cselect_b32 s6, s7, s6\n" \
"v_mfma_f32_16x16x32_bf16 a[52:55], a[164:167], a[140:143], a[52:55]\n" \
"v_writelane_b32 v11, s6, 45\n" \
"s_or_b32 s6, s8, s15\n" \
"v_mfma_f32_16x16x32_bf16 a[52:55], a[228:231], a[204:207], a[52:55]\n" \
"v_writelane_b32 v11, s6, 53\n" \
"s_lshl_b32 s6, s3, 17\n" \
"v_mfma_f32_16x16x32_bf16 a[48:51], a[160:163], a[140:143], a[48:51]\n" \
"s_add_i32 s6, s13, s6\n" \
"s_or_b32 s5, s6, s5\n" \
"v_mfma_f32_16x16x32_bf16 a[48:51], a[224:227], a[204:207], a[48:51]\n" \
"s_or_b32 s5, s5, 0x3840\n" \
"v_writelane_b32 v11, s5, 6\n" \
"s_waitcnt lgkmcnt(15)\n" \
"v_mfma_f32_16x16x32_bf16 a[64:67], a[164:167], v[248:251], a[64:67]\n" \
"s_mov_b32 s5, 0xff0000\n" \
"v_mfma_f32_16x16x32_bf16 a[64:67], a[228:231], v[48:51], a[64:67]\n" \
"s_cmp_lg_u32 s16, 0\n" \
"s_cselect_b32 s5, s17, s5\n" \
"v_mfma_f32_16x16x32_bf16 a[80:83], a[172:175], v[248:251], a[80:83]\n" \
"v_writelane_b32 v11, s5, 7\n" \
"s_mul_i32 s5, s3, 0x1200\n" \
"v_mfma_f32_16x16x32_bf16 a[80:83], a[236:239], v[48:51], a[80:83]\n" \
"s_add_i32 s4, s5, s4\n" \
"s_add_i32 s4, s4, s11\n" \
"v_mfma_f32_16x16x32_bf16 a[68:71], v[248:251], a[168:171], a[68:71]\n" \
"v_writelane_b32 v11, s4, 8\n" \
"s_lshl_b32 s4, s2, 16\n" \
"v_mfma_f32_16x16x32_bf16 a[68:71], v[48:51], a[232:235], a[68:71]\n" \
"s_waitcnt vmcnt(3) lgkmcnt(0)\n" \
"s_barrier\n" \
"s_nop 0\n" \
"v_mfma_f32_16x16x32_bf16 v[52:55], a[176:179], a[128:131], v[52:55]\n" \
"s_add_u32 m0, s40, 0x8000\n" \
"s_add_u32 s85, s39, 0xe000\n" \
"v_mfma_f32_16x16x32_bf16 v[52:55], a[240:243], a[192:195], v[52:55]\n" \
"buffer_load_dwordx4 v20, s[80:83], s85 offen sc1 lds\n" \
"v_mfma_f32_16x16x32_bf16 v[56:59], a[180:183], a[128:131], v[56:59]\n" \
"ds_read_b64_tr_b16 a[160:161], v32 offset:0\n" \
"s_add_i32 s5, s4, 0x10000\n" \
"v_mfma_f32_16x16x32_bf16 v[56:59], a[244:247], a[192:195], v[56:59]\n" \
"ds_read_b64_tr_b16 a[162:163], v32 offset:512\n" \
"s_and_b32 s5, s5, 0x70000\n" \
"v_mfma_f32_16x16x32_bf16 v[60:63], a[184:187], a[128:131], v[60:63]\n" \
"s_add_u32 m0, s40, 0x9000\n" \
"s_add_u32 s85, s39, 0xf000\n" \
"v_mfma_f32_16x16x32_bf16 v[60:63], a[248:251], a[192:195], v[60:63]\n" \
"buffer_load_dwordx4 v20, s[80:83], s85 offen sc1 lds\n" \
"v_mfma_f32_16x16x32_bf16 v[64:67], a[188:191], a[128:131], v[64:67]\n" \
"ds_read_b64_tr_b16 a[224:225], v32 offset:4096\n" \
"s_add_i32 s5, s13, s5\n" \
"v_mfma_f32_16x16x32_bf16 v[64:67], a[252:255], a[192:195], v[64:67]\n" \
"ds_read_b128 a[128:131], v28 offset:0\n" \
"v_writelane_b32 v11, s5, 46\n" \
"v_mfma_f32_16x16x32_bf16 v[80:83], a[188:191], a[132:135], v[80:83]\n" \
"ds_read_b128 a[192:195], v29 offset:0\n" \
"s_add_i32 s5, s4, 0x20000\n" \
"v_mfma_f32_16x16x32_bf16 v[80:83], a[252:255], a[196:199], v[80:83]\n" \
"s_add_u32 m0, s40, 0xb000\n" \
"s_movk_i32 s84, 0x380\n" \
"v_mfma_f32_16x16x32_bf16 v[76:79], a[184:187], a[132:135], v[76:79]\n" \
"buffer_load_dwordx4 v21, s[76:79], s84 offen offset:0 lds\n" \
"v_mfma_f32_16x16x32_bf16 v[76:79], a[248:251], a[196:199], v[76:79]\n" \
"ds_read_b64_tr_b16 a[226:227], v32 offset:4608\n" \
"s_and_b32 s5, s5, 0x70000\n" \
"v_mfma_f32_16x16x32_bf16 v[72:75], a[180:183], a[132:135], v[72:75]\n" \
"s_add_u32 m0, s30, 0x8000\n" \
"s_movk_i32 s84, 0x380\n" \
"v_mfma_f32_16x16x32_bf16 v[72:75], a[244:247], a[196:199], v[72:75]\n" \
"buffer_load_dwordx4 v24, s[76:79], s84 offen offset:0 lds\n" \
"v_mfma_f32_16x16x32_bf16 v[68:71], a[176:179], a[132:135], v[68:71]\n" \
"ds_read_b64_tr_b16 a[164:165], v33 offset:0\n" \
"s_add_i32 s5, s13, s5\n" \
"v_mfma_f32_16x16x32_bf16 v[68:71], a[240:243], a[196:199], v[68:71]\n" \
"ds_read_b128 a[132:135], v28 offset:2048\n" \
"s_bitset1_b32 s5, 11\n" \
"v_mfma_f32_16x16x32_bf16 v[84:87], a[176:179], a[136:139], v[84:87]\n" \
"ds_read_b128 a[196:199], v29 offset:2048\n" \
"v_writelane_b32 v11, s5, 47\n" \
"v_mfma_f32_16x16x32_bf16 v[84:87], a[240:243], a[200:203], v[84:87]\n" \
"s_movk_i32 s84, 0x2380\n" \
"v_mfma_f32_16x16x32_bf16 v[88:91], a[180:183], a[136:139], v[88:91]\n" \
"buffer_load_dwordx4 v25, s[76:79], s84 offen offset:1024 lds\n" \
"v_mfma_f32_16x16x32_bf16 v[88:91], a[244:247], a[200:203], v[88:91]\n" \
"ds_read_b64_tr_b16 a[166:167], v33 offset:512\n" \
"s_add_i32 s5, s4, 0x30000\n" \
"v_mfma_f32_16x16x32_bf16 v[92:95], a[184:187], a[136:139], v[92:95]\n" \
"ds_read_b64_tr_b16 a[228:229], v33 offset:4096\n" \
"s_and_b32 s5, s5, 0x70000\n" \
"v_mfma_f32_16x16x32_bf16 v[92:95], a[248:251], a[200:203], v[92:95]\n" \
"s_movk_i32 s84, 0x4380\n" \
"v_mfma_f32_16x16x32_bf16 v[96:99], a[188:191], a[136:139], v[96:99]\n" \
"buffer_load_dwordx4 v24, s[76:79], s84 offen offset:2048 lds\n" \
"v_mfma_f32_16x16x32_bf16 v[96:99], a[252:255], a[200:203], v[96:99]\n" \
"ds_read_b128 a[136:139], v28 offset:4096\n" \
"s_add_i32 s5, s13, s5\n" \
"v_mfma_f32_16x16x32_bf16 v[112:115], a[188:191], a[140:143], v[112:115]\n" \
"ds_read_b128 a[200:203], v29 offset:4096\n" \
"s_bitset1_b32 s5, 12\n" \
"v_mfma_f32_16x16x32_bf16 v[112:115], a[252:255], a[204:207], v[112:115]\n" \
"s_movk_i32 s84, 0x6380\n" \
"v_mfma_f32_16x16x32_bf16 v[108:111], a[184:187], a[140:143], v[108:111]\n" \
"buffer_load_dwordx4 v25, s[76:79], s84 offen offset:3072 lds\n" \
"v_mfma_f32_16x16x32_bf16 v[108:111], a[248:251], a[204:207], v[108:111]\n" \
"ds_read_b64_tr_b16 a[230:231], v33 offset:4608\n" \
"v_writelane_b32 v11, s5, 48\n" \
"v_mfma_f32_16x16x32_bf16 v[104:107], a[180:183], a[140:143], v[104:107]\n" \
"ds_read_b64_tr_b16 a[172:173], v35 offset:0\n" \
"s_and_b32 s5, s4, 0x70000\n" \
"v_mfma_f32_16x16x32_bf16 v[104:107], a[244:247], a[204:207], v[104:107]\n" \
"ds_read_b64_tr_b16 a[174:175], v35 offset:512\n" \
"s_xor_b32 s5, s5, 0x40000\n" \
"v_mfma_f32_16x16x32_bf16 v[100:103], a[176:179], a[140:143], v[100:103]\n" \
"ds_read_b64_tr_b16 a[236:237], v35 offset:4096\n" \
"s_add_i32 s5, s13, s5\n" \
"s_nop 0\n" \
"v_mfma_f32_16x16x32_bf16 v[100:103], a[240:243], a[204:207], v[100:103]\n" \
"ds_read_b128 a[140:143], v28 offset:6144\n" \
"s_or_b32 s5, s5, 0x1800\n" \
"v_mfma_f32_16x16x32_bf16 v[116:119], a[176:179], a[144:147], v[116:119]\n" \
"ds_read_b128 a[204:207], v29 offset:6144\n" \
"v_writelane_b32 v11, s5, 49\n" \
"v_mfma_f32_16x16x32_bf16 v[116:119], a[240:243], a[208:211], v[116:119]\n" \
"s_add_u32 m0, s31, 0x8000\n" \
"s_mov_b32 s84, 0x380000\n" \
"v_mfma_f32_16x16x32_bf16 v[120:123], a[180:183], a[144:147], v[120:123]\n" \
"buffer_load_dwordx4 v26, s[68:71], s84 offen offset:0 lds\n" \
"v_mfma_f32_16x16x32_bf16 v[120:123], a[244:247], a[208:211], v[120:123]\n" \
"s_addk_i32 s84, 0x7c00\n" \
"v_mfma_f32_16x16x32_bf16 v[124:127], a[184:187], a[144:147], v[124:127]\n" \
"buffer_load_dwordx4 v26, s[68:71], s84 offen offset:1024 lds\n" \
"v_mfma_f32_16x16x32_bf16 v[124:127], a[248:251], a[208:211], v[124:127]\n" \
"ds_read_b64_tr_b16 a[238:239], v35 offset:4608\n" \
"s_add_i32 s5, s4, 0x50000\n" \
"v_mfma_f32_16x16x32_bf16 v[128:131], a[188:191], a[144:147], v[128:131]\n" \
"ds_read_b64_tr_b16 a[168:169], v34 offset:0\n" \
"s_and_b32 s5, s5, 0x70000\n" \
"v_mfma_f32_16x16x32_bf16 v[128:131], a[252:255], a[208:211], v[128:131]\n" \
"ds_read_b128 a[144:147], v28 offset:8192\n" \
"s_add_i32 s5, s13, s5\n" \
"v_mfma_f32_16x16x32_bf16 v[144:147], a[188:191], a[148:151], v[144:147]\n" \
"ds_read_b128 a[208:211], v29 offset:8192\n" \
"s_bitset1_b32 s5, 13\n" \
"v_mfma_f32_16x16x32_bf16 v[144:147], a[252:255], a[212:215], v[144:147]\n" \
"s_addk_i32 s84, 0x7c00\n" \
"v_mfma_f32_16x16x32_bf16 v[140:143], a[184:187], a[148:151], v[140:143]\n" \
"buffer_load_dwordx4 v27, s[68:71], s84 offen offset:2048 lds\n" \
"v_mfma_f32_16x16x32_bf16 v[140:143], a[248:251], a[212:215], v[140:143]\n" \
"s_addk_i32 s84, 0x7c00\n" \
"s_nop 0\n" \
"v_mfma_f32_16x16x32_bf16 v[136:139], a[180:183], a[148:151], v[136:139]\n" \
"buffer_load_dwordx4 v27, s[68:71], s84 offen offset:3072 lds\n" \
"v_mfma_f32_16x16x32_bf16 v[136:139], a[244:247], a[212:215], v[136:139]\n" \
"ds_read_b64_tr_b16 a[170:171], v34 offset:512\n" \
"v_writelane_b32 v11, s5, 50\n" \
"v_mfma_f32_16x16x32_bf16 v[132:135], a[176:179], a[148:151], v[132:135]\n" \
"ds_read_b64_tr_b16 a[232:233], v34 offset:4096\n" \
"s_add_i32 s5, s4, 0x60000\n" \
"v_mfma_f32_16x16x32_bf16 v[132:135], a[240:243], a[212:215], v[132:135]\n" \
"ds_read_b128 a[148:151], v28 offset:10240\n" \
"s_and_b32 s5, s5, 0x70000\n" \
"v_mfma_f32_16x16x32_bf16 v[148:151], a[176:179], a[152:155], v[148:151]\n" \
"ds_read_b128 a[212:215], v29 offset:10240\n" \
"s_add_i32 s4, s4, 0x70000\n" \
"v_mfma_f32_16x16x32_bf16 v[148:151], a[240:243], a[216:219], v[148:151]\n" \
"s_add_u32 m0, s30, 0x9000\n" \
"s_mov_b32 s84, 0x9380\n" \
"v_mfma_f32_16x16x32_bf16 v[152:155], a[180:183], a[152:155], v[152:155]\n" \
"buffer_load_dwordx4 v24, s[76:79], s84 offen offset:0 lds\n" \
"v_mfma_f32_16x16x32_bf16 v[152:155], a[244:247], a[216:219], v[152:155]\n" \
"s_addk_i32 s84, 0x2000\n" \
"v_mfma_f32_16x16x32_bf16 v[156:159], a[184:187], a[152:155], v[156:159]\n" \
"buffer_load_dwordx4 v25, s[76:79], s84 offen offset:1024 lds\n" \
"v_mfma_f32_16x16x32_bf16 v[156:159], a[248:251], a[216:219], v[156:159]\n" \
"ds_read_b64_tr_b16 a[234:235], v34 offset:4608\n" \
"s_add_i32 s5, s13, s5\n" \
"v_mfma_f32_16x16x32_bf16 v[160:163], a[188:191], a[152:155], v[160:163]\n" \
"ds_read_b128 v[248:251], v22 offset:0\n" \
"s_and_b32 s4, s4, 0x70000\n" \
"v_mfma_f32_16x16x32_bf16 v[160:163], a[252:255], a[216:219], v[160:163]\n" \
"ds_read_b128 a[152:155], v28 offset:12288\n" \
"s_or_b32 s5, s5, 0x2800\n" \
"v_mfma_f32_16x16x32_bf16 v[176:179], a[188:191], a[156:159], v[176:179]\n" \
"ds_read_b128 a[216:219], v29 offset:12288\n" \
"v_writelane_b32 v11, s5, 51\n" \
"v_mfma_f32_16x16x32_bf16 v[176:179], a[252:255], a[220:223], v[176:179]\n" \
"ds_read_b128 v[48:51], v23 offset:0\n" \
"s_add_i32 s13, s13, s4\n" \
"v_mfma_f32_16x16x32_bf16 v[172:175], a[184:187], a[156:159], v[172:175]\n" \
"s_or_b32 s4, s13, 0x3000\n" \
"v_writelane_b32 v11, s4, 52\n" \
"v_mfma_f32_16x16x32_bf16 v[172:175], a[248:251], a[220:223], v[172:175]\n" \
"s_movk_i32 s11, 0x4000\n" \
"s_lshl_b32 s4, s2, 2\n" \
"v_mfma_f32_16x16x32_bf16 v[168:171], a[180:183], a[156:159], v[168:171]\n" \
"s_and_b32 s4, s4, 28\n" \
"s_bfe_u32 s5, s2, 0x20003\n" \
"v_mfma_f32_16x16x32_bf16 v[168:171], a[244:247], a[220:223], v[168:171]\n" \
"s_or_b32 s4, s4, s5\n" \
"s_lshr_b32 s5, s2, 5\n" \
"v_mfma_f32_16x16x32_bf16 v[164:167], a[176:179], a[156:159], v[164:167]\n" \
"s_lshr_b32 s19, s3, 1\n" \
"s_lshl_b32 s18, s5, 1\n" \
"v_mfma_f32_16x16x32_bf16 v[164:167], a[240:243], a[220:223], v[164:167]\n" \
"ds_read_b128 a[156:159], v28 offset:14336\n" \
"ds_read_b128 a[220:223], v29 offset:14336\n" \
"s_waitcnt lgkmcnt(15)\n" \
"v_mfma_f32_16x16x32_bf16 a[0:3], a[160:163], a[128:131], a[0:3]\n" \
"s_addk_i32 s84, 0x2000\n" \
"v_mfma_f32_16x16x32_bf16 a[0:3], a[224:227], a[192:195], a[0:3]\n" \
"buffer_load_dwordx4 v24, s[76:79], s84 offen offset:2048 lds\n" \
"v_mfma_f32_16x16x32_bf16 a[4:7], a[164:167], a[128:131], a[4:7]\n" \
"ds_read_b64_tr_b16 a[176:177], v36 offset:0\n" \
"s_lshl_b32 s6, s4, 20\n" \
"v_mfma_f32_16x16x32_bf16 a[4:7], a[228:231], a[192:195], a[4:7]\n" \
"s_addk_i32 s84, 0x2000\n" \
"v_mfma_f32_16x16x32_bf16 a[12:15], a[172:175], a[128:131], a[12:15]\n" \
"buffer_load_dwordx4 v25, s[76:79], s84 offen offset:3072 lds\n" \
"s_waitcnt lgkmcnt(15)\n" \
"v_mfma_f32_16x16x32_bf16 a[12:15], a[236:239], a[192:195], a[12:15]\n" \
"ds_read_b64_tr_b16 a[178:179], v36 offset:1024\n" \
"s_lshl_b32 s7, s4, 8\n" \
"s_waitcnt lgkmcnt(12)\n" \
"v_mfma_f32_16x16x32_bf16 a[8:11], a[128:131], a[168:171], a[8:11]\n" \
"ds_read_b64_tr_b16 a[240:241], v36 offset:8192\n" \
"s_and_b32 s13, s3, 1\n" \
"s_waitcnt lgkmcnt(9)\n" \
"v_mfma_f32_16x16x32_bf16 a[8:11], a[192:195], a[232:235], a[8:11]\n" \
"ds_read_b64_tr_b16 a[242:243], v36 offset:9216\n" \
"s_add_i32 s14, s18, s19\n" \
"v_mfma_f32_16x16x32_bf16 a[24:27], a[132:135], a[168:171], a[24:27]\n" \
"ds_read_b64_tr_b16 a[180:181], v37 offset:0\n" \
"v_mfma_f32_16x16x32_bf16 a[24:27], a[196:199], a[232:235], a[24:27]\n" \
"ds_read_b64_tr_b16 a[182:183], v37 offset:1024\n" \
"v_mfma_f32_16x16x32_bf16 a[28:31], a[172:175], a[132:135], a[28:31]\n" \
"ds_read_b64_tr_b16 a[244:245], v37 offset:8192\n" \
"v_mfma_f32_16x16x32_bf16 a[28:31], a[236:239], a[196:199], a[28:31]\n" \
"ds_read_b64_tr_b16 a[246:247], v37 offset:9216\n" \
"v_mfma_f32_16x16x32_bf16 a[20:23], a[164:167], a[132:135], a[20:23]\n" \
"s_add_u32 s6, s26, s6\n" \
"s_addc_u32 s9, s27, 0\n" \
"v_mfma_f32_16x16x32_bf16 a[20:23], a[228:231], a[196:199], a[20:23]\n" \
"s_add_u32 s89, s42, 0x0\n" \
"s_nop 0\n" \
"buffer_store_dwordx4 v[184:187], v20, s[80:83], s89 offen offset:1024 sc1\n" \
"v_mfma_f32_16x16x32_bf16 a[16:19], a[160:163], a[132:135], a[16:19]\n" \
"ds_read_b64_tr_b16 a[184:185], v38 offset:0\n" \
"v_mfma_f32_16x16x32_bf16 a[16:19], a[224:227], a[196:199], a[16:19]\n" \
"ds_read_b64_tr_b16 a[186:187], v38 offset:1024\n" \
"v_mfma_f32_16x16x32_bf16 a[32:35], a[160:163], a[136:139], a[32:35]\n" \
"ds_read_b64_tr_b16 a[248:249], v38 offset:8192\n" \
"v_mfma_f32_16x16x32_bf16 a[32:35], a[224:227], a[200:203], a[32:35]\n" \
"ds_read_b64_tr_b16 a[250:251], v38 offset:9216\n" \
"v_mfma_f32_16x16x32_bf16 a[36:39], a[164:167], a[136:139], a[36:39]\n" \
"s_add_u32 s8, s6, 0x1000000\n" \
"s_addc_u32 s6, s9, 0\n" \
"v_mfma_f32_16x16x32_bf16 a[36:39], a[228:231], a[200:203], a[36:39]\n" \
"s_add_u32 s88, s42, 0x1000\n" \
"buffer_store_dwordx4 v[188:191], v20, s[80:83], s88 offen offset:1024 sc1\n" \
"v_mfma_f32_16x16x32_bf16 a[44:47], a[172:175], a[136:139], a[44:47]\n" \
"ds_read_b64_tr_b16 a[188:189], v39 offset:0\n" \
"s_and_b32 s9, s6, 0xffff\n" \
"v_mfma_f32_16x16x32_bf16 a[44:47], a[236:239], a[200:203], a[44:47]\n" \
"ds_read_b64_tr_b16 a[190:191], v39 offset:1024\n" \
"s_lshl_b32 s6, s5, 16\n" \
"v_mfma_f32_16x16x32_bf16 a[40:43], a[136:139], a[168:171], a[40:43]\n" \
"ds_read_b64_tr_b16 a[252:253], v39 offset:8192\n" \
"s_sub_i32 s10, 0x100000, s6\n" \
"v_mfma_f32_16x16x32_bf16 a[40:43], a[200:203], a[232:235], a[40:43]\n" \
"s_mov_b32 s6, 0x8000\n" \
"v_mfma_f32_16x16x32_bf16 a[56:59], a[140:143], a[168:171], a[56:59]\n" \
"ds_read_b64_tr_b16 a[254:255], v39 offset:9216\n" \
"v_mfma_f32_16x16x32_bf16 a[56:59], a[204:207], a[232:235], a[56:59]\n" \
"s_cmp_lg_u32 s5, 0\n" \
"s_cselect_b32 s17, s11, s6\n" \
"v_mfma_f32_16x16x32_bf16 a[60:63], a[172:175], a[140:143], a[60:63]\n" \
"s_add_u32 s89, s42, 0x2000\n" \
"buffer_store_dwordx4 v[192:195], v20, s[80:83], s89 offen offset:0 sc1\n" \
"v_mfma_f32_16x16x32_bf16 a[60:63], a[236:239], a[204:207], a[60:63]\n" \
"s_lshl_b32 s6, s3, 1\n" \
"s_lshl_b32 s5, s5, 3\n" \
"v_mfma_f32_16x16x32_bf16 a[52:55], a[164:167], a[140:143], a[52:55]\n" \
"s_and_b32 s6, s6, 4\n" \
"s_or_b32 s12, s5, s6\n" \
"v_mfma_f32_16x16x32_bf16 a[52:55], a[228:231], a[204:207], a[52:55]\n" \
"s_sub_i32 s5, s13, s5\n" \
"s_or_b32 s5, s5, s6\n" \
"v_mfma_f32_16x16x32_bf16 a[48:51], a[160:163], a[140:143], a[48:51]\n" \
"s_or_b32 s6, s12, s13\n" \
"s_lshl_b32 s4, s4, 2\n" \
"v_mfma_f32_16x16x32_bf16 a[48:51], a[224:227], a[204:207], a[48:51]\n" \
"s_mulk_i32 s6, 0x92\n" \
"s_add_i32 s4, s4, 18\n" \
"s_waitcnt lgkmcnt(15)\n" \
"v_mfma_f32_16x16x32_bf16 a[64:67], a[164:167], v[248:251], a[64:67]\n" \
"s_mulk_i32 s5, 0x92\n" \
"s_add_i32 s6, s4, s6\n" \
"v_mfma_f32_16x16x32_bf16 a[64:67], a[228:231], v[48:51], a[64:67]\n" \
"s_add_i32 s5, s5, s4\n" \
"s_lshl_b32 s4, s3, 11\n" \
"v_mfma_f32_16x16x32_bf16 a[80:83], a[172:175], v[248:251], a[80:83]\n" \
"s_lshl_b32 s15, s13, 12\n" \
"s_lshl_b32 s16, s19, 14\n" \
"v_mfma_f32_16x16x32_bf16 a[80:83], a[236:239], v[48:51], a[80:83]\n" \
"s_mul_i32 s19, s19, 0xe000\n" \
"s_mov_b32 s11, 0x20000\n" \
"v_mfma_f32_16x16x32_bf16 a[68:71], v[248:251], a[168:171], a[68:71]\n" \
"s_or_b32 s12, s4, 0xc000\n" \
"s_lshl_b32 s4, s3, 8\n" \
"v_mfma_f32_16x16x32_bf16 a[68:71], v[48:51], a[232:235], a[68:71]\n" \
"s_waitcnt vmcnt(3) lgkmcnt(0)\n" \
"s_barrier\n" \
"v_mfma_f32_16x16x32_bf16 v[52:55], a[176:179], a[128:131], v[52:55]\n" \
"s_add_u32 m0, s40, 0x0\n" \
".fill (L_pb + 32480 - .) / 4, 4, 0xBF800000\n" \
"L_pp8:\n" \
"s_cbranch_execz L_pret\n" \
"s_add_u32 s85, s39, 0x10000\n" \
"v_mfma_f32_16x16x32_bf16 v[52:55], a[240:243], a[192:195], v[52:55]\n" \
"buffer_load_dwordx4 v20, s[80:83], s85 offen sc1 lds\n" \
"v_mfma_f32_16x16x32_bf16 v[56:59], a[180:183], a[128:131], v[56:59]\n" \
"ds_read_b64_tr_b16 a[160:161], v32 offset:32768\n" \
"s_or_b32 s22, s15, s16\n" \
"v_mfma_f32_16x16x32_bf16 v[56:59], a[244:247], a[192:195], v[56:59]\n" \
"ds_read_b64_tr_b16 a[162:163], v32 offset:33280\n" \
"s_or_b32 s23, s19, s15\n" \
"v_mfma_f32_16x16x32_bf16 v[60:63], a[184:187], a[128:131], v[60:63]\n" \
"s_add_u32 m0, s40, 0x1000\n" \
"s_add_u32 s85, s39, 0x11000\n" \
"v_mfma_f32_16x16x32_bf16 v[60:63], a[248:251], a[192:195], v[60:63]\n" \
"buffer_load_dwordx4 v20, s[80:83], s85 offen sc1 lds\n" \
"v_mfma_f32_16x16x32_bf16 v[64:67], a[188:191], a[128:131], v[64:67]\n" \
"ds_read_b64_tr_b16 a[224:225], v32 offset:36864\n" \
"s_lshl_b32 s60, s6, 10\n" \
"v_mfma_f32_16x16x32_bf16 v[64:67], a[252:255], a[192:195], v[64:67]\n" \
"ds_read_b128 a[128:131], v28 offset:32768\n" \
"s_lshl_b32 s61, s5, 10\n" \
"v_mfma_f32_16x16x32_bf16 v[80:83], a[188:191], a[132:135], v[80:83]\n" \
"ds_read_b128 a[192:195], v29 offset:32768\n" \
"s_lshl_b32 s14, s14, 15\n" \
"v_mfma_f32_16x16x32_bf16 v[80:83], a[252:255], a[196:199], v[80:83]\n" \
"s_add_u32 m0, s40, 0x3000\n" \
"s_movk_i32 s84, 0x400\n" \
"v_mfma_f32_16x16x32_bf16 v[76:79], a[184:187], a[132:135], v[76:79]\n" \
"buffer_load_dwordx4 v21, s[76:79], s84 offen offset:0 lds\n" \
"v_mfma_f32_16x16x32_bf16 v[76:79], a[248:251], a[196:199], v[76:79]\n" \
"ds_read_b64_tr_b16 a[226:227], v32 offset:37376\n" \
"s_or_b32 s20, s4, 0x26c00\n" \
"v_mfma_f32_16x16x32_bf16 v[72:75], a[180:183], a[132:135], v[72:75]\n" \
"s_add_u32 m0, s30, 0x0\n" \
"s_movk_i32 s84, 0x400\n" \
"v_mfma_f32_16x16x32_bf16 v[72:75], a[244:247], a[196:199], v[72:75]\n" \
"buffer_load_dwordx4 v24, s[76:79], s84 offen offset:0 lds\n" \
"v_mfma_f32_16x16x32_bf16 v[68:71], a[176:179], a[132:135], v[68:71]\n" \
"ds_read_b64_tr_b16 a[164:165], v33 offset:32768\n" \
"s_or_b32 s13, s22, 0x8000\n" \
"v_mfma_f32_16x16x32_bf16 v[68:71], a[240:243], a[196:199], v[68:71]\n" \
"ds_read_b128 a[132:135], v28 offset:34816\n" \
"s_add_i32 s22, s22, 0x14000\n" \
"v_mfma_f32_16x16x32_bf16 v[84:87], a[176:179], a[136:139], v[84:87]\n" \
"ds_read_b128 a[196:199], v29 offset:34816\n" \
"s_bitset1_b32 s23, 16\n" \
"v_mfma_f32_16x16x32_bf16 v[84:87], a[240:243], a[200:203], v[84:87]\n" \
"s_movk_i32 s84, 0x2400\n" \
"v_mfma_f32_16x16x32_bf16 v[88:91], a[180:183], a[136:139], v[88:91]\n" \
"buffer_load_dwordx4 v25, s[76:79], s84 offen offset:1024 lds\n" \
"v_mfma_f32_16x16x32_bf16 v[88:91], a[244:247], a[200:203], v[88:91]\n" \
"ds_read_b64_tr_b16 a[166:167], v33 offset:33280\n" \
"s_or_b32 s24, s7, 0x5be8020\n" \
"v_mfma_f32_16x16x32_bf16 v[92:95], a[184:187], a[136:139], v[92:95]\n" \
"ds_read_b64_tr_b16 a[228:229], v33 offset:36864\n" \
"s_and_b32 s57, s27, 0xffff\n" \
"v_mfma_f32_16x16x32_bf16 v[92:95], a[248:251], a[200:203], v[92:95]\n" \
"s_movk_i32 s84, 0x4400\n" \
"v_mfma_f32_16x16x32_bf16 v[96:99], a[188:191], a[136:139], v[96:99]\n" \
"buffer_load_dwordx4 v24, s[76:79], s84 offen offset:2048 lds\n" \
"v_mfma_f32_16x16x32_bf16 v[96:99], a[252:255], a[200:203], v[96:99]\n" \
"ds_read_b128 a[136:139], v28 offset:36864\n" \
"s_mov_b32 s58, 0x6000000\n" \
"v_mfma_f32_16x16x32_bf16 v[112:115], a[188:191], a[140:143], v[112:115]\n" \
"ds_read_b128 a[200:203], v29 offset:36864\n" \
"s_mov_b32 s56, s26\n" \
"v_mfma_f32_16x16x32_bf16 v[112:115], a[252:255], a[204:207], v[112:115]\n" \
"s_movk_i32 s84, 0x6400\n" \
"v_mfma_f32_16x16x32_bf16 v[108:111], a[184:187], a[140:143], v[108:111]\n" \
"buffer_load_dwordx4 v25, s[76:79], s84 offen offset:3072 lds\n" \
"v_mfma_f32_16x16x32_bf16 v[108:111], a[248:251], a[204:207], v[108:111]\n" \
"ds_read_b64_tr_b16 a[230:231], v33 offset:37376\n" \
"s_mov_b32 s59, s11\n" \
"v_mfma_f32_16x16x32_bf16 v[104:107], a[180:183], a[140:143], v[104:107]\n" \
"ds_read_b64_tr_b16 a[172:173], v35 offset:32768\n" \
"s_add_i32 s60, s60, 0x3000000\n" \
"v_mfma_f32_16x16x32_bf16 v[104:107], a[244:247], a[204:207], v[104:107]\n" \
"ds_read_b64_tr_b16 a[174:175], v35 offset:33280\n" \
"s_add_i32 s61, s61, 0x411c000\n" \
"v_mfma_f32_16x16x32_bf16 v[100:103], a[176:179], a[140:143], v[100:103]\n" \
"ds_read_b64_tr_b16 a[236:237], v35 offset:36864\n" \
"s_sub_i32 s19, 14, s18\n" \
"v_mfma_f32_16x16x32_bf16 v[100:103], a[240:243], a[204:207], v[100:103]\n" \
"ds_read_b128 a[140:143], v28 offset:38912\n" \
"s_mov_b32 s21, 0x71800000\n" \
"v_mfma_f32_16x16x32_bf16 v[116:119], a[176:179], a[144:147], v[116:119]\n" \
"ds_read_b128 a[204:207], v29 offset:38912\n" \
"s_mov_b32 s4, L_gsw-L_gtr\n" \
"v_mfma_f32_16x16x32_bf16 v[116:119], a[240:243], a[208:211], v[116:119]\n" \
"s_add_u32 m0, s31, 0x0\n" \
"s_mov_b32 s84, 0x400000\n" \
"v_mfma_f32_16x16x32_bf16 v[120:123], a[180:183], a[144:147], v[120:123]\n" \
"buffer_load_dwordx4 v26, s[68:71], s84 offen offset:0 lds\n" \
"v_mfma_f32_16x16x32_bf16 v[120:123], a[244:247], a[208:211], v[120:123]\n" \
"s_addk_i32 s84, 0x7c00\n" \
"v_mfma_f32_16x16x32_bf16 v[124:127], a[184:187], a[144:147], v[124:127]\n" \
"buffer_load_dwordx4 v26, s[68:71], s84 offen offset:1024 lds\n" \
"v_mfma_f32_16x16x32_bf16 v[124:127], a[248:251], a[208:211], v[124:127]\n" \
"ds_read_b64_tr_b16 a[238:239], v35 offset:37376\n" \
"v_mfma_f32_16x16x32_bf16 v[128:131], a[188:191], a[144:147], v[128:131]\n" \
"ds_read_b64_tr_b16 a[168:169], v34 offset:32768\n" \
"v_mfma_f32_16x16x32_bf16 v[128:131], a[252:255], a[208:211], v[128:131]\n" \
"ds_read_b128 a[144:147], v28 offset:40960\n" \
"v_mfma_f32_16x16x32_bf16 v[144:147], a[188:191], a[148:151], v[144:147]\n" \
"ds_read_b128 a[208:211], v29 offset:40960\n" \
"v_mfma_f32_16x16x32_bf16 v[144:147], a[252:255], a[212:215], v[144:147]\n" \
"s_addk_i32 s84, 0x7c00\n" \
"v_mfma_f32_16x16x32_bf16 v[140:143], a[184:187], a[148:151], v[140:143]\n" \
"buffer_load_dwordx4 v27, s[68:71], s84 offen offset:2048 lds\n" \
"v_mfma_f32_16x16x32_bf16 v[140:143], a[248:251], a[212:215], v[140:143]\n" \
"s_addk_i32 s84, 0x7c00\n" \
"v_mfma_f32_16x16x32_bf16 v[136:139], a[180:183], a[148:151], v[136:139]\n" \
"buffer_load_dwordx4 v27, s[68:71], s84 offen offset:3072 lds\n" \
"v_mfma_f32_16x16x32_bf16 v[136:139], a[244:247], a[212:215], v[136:139]\n" \
"ds_read_b64_tr_b16 a[170:171], v34 offset:33280\n" \
"v_mfma_f32_16x16x32_bf16 v[132:135], a[176:179], a[148:151], v[132:135]\n" \
"ds_read_b64_tr_b16 a[232:233], v34 offset:36864\n" \
"v_mfma_f32_16x16x32_bf16 v[132:135], a[240:243], a[212:215], v[132:135]\n" \
"ds_read_b128 a[148:151], v28 offset:43008\n" \
"v_mfma_f32_16x16x32_bf16 v[148:151], a[176:179], a[152:155], v[148:151]\n" \
"ds_read_b128 a[212:215], v29 offset:43008\n" \
"v_mfma_f32_16x16x32_bf16 v[148:151], a[240:243], a[216:219], v[148:151]\n" \
"s_add_u32 m0, s30, 0x1000\n" \
"s_mov_b32 s84, 0x9400\n" \
"v_mfma_f32_16x16x32_bf16 v[152:155], a[180:183], a[152:155], v[152:155]\n" \
"buffer_load_dwordx4 v24, s[76:79], s84 offen offset:0 lds\n" \
"v_mfma_f32_16x16x32_bf16 v[152:155], a[244:247], a[216:219], v[152:155]\n" \
"s_addk_i32 s84, 0x2000\n" \
"s_nop 0\n" \
"v_mfma_f32_16x16x32_bf16 v[156:159], a[184:187], a[152:155], v[156:159]\n" \
"buffer_load_dwordx4 v25, s[76:79], s84 offen offset:1024 lds\n" \
"v_mfma_f32_16x16x32_bf16 v[156:159], a[248:251], a[216:219], v[156:159]\n" \
"ds_read_b64_tr_b16 a[234:235], v34 offset:37376\n" \
"v_mfma_f32_16x16x32_bf16 v[160:163], a[188:191], a[152:155], v[160:163]\n" \
"ds_read_b128 v[248:251], v22 offset:32768\n" \
"v_mfma_f32_16x16x32_bf16 v[160:163], a[252:255], a[216:219], v[160:163]\n" \
"ds_read_b128 a[152:155], v28 offset:45056\n" \
"v_mfma_f32_16x16x32_bf16 v[176:179], a[188:191], a[156:159], v[176:179]\n" \
"ds_read_b128 a[216:219], v29 offset:45056\n" \
"v_mfma_f32_16x16x32_bf16 v[176:179], a[252:255], a[220:223], v[176:179]\n" \
"ds_read_b128 v[48:51], v23 offset:32768\n" \
"v_mfma_f32_16x16x32_bf16 v[172:175], a[184:187], a[156:159], v[172:175]\n" \
"s_cmp_eq_u32 s19, 0\n" \
"s_cselect_b32 s4, L_gs7-L_gtr, s4\n" \
"v_mfma_f32_16x16x32_bf16 v[172:175], a[248:251], a[220:223], v[172:175]\n" \
"s_cmp_eq_u32 s16, 0\n" \
"s_cselect_b32 s4, 0, s4\n" \
"v_mfma_f32_16x16x32_bf16 v[168:171], a[180:183], a[156:159], v[168:171]\n" \
"s_getpc_b64 s[28:29]\n" \
"L_gp0_%=:\n" \
"s_add_u32 s28, s28, L_gtr-L_gp0_%=\n" \
"s_addc_u32 s29, s29, 0\n" \
"v_mfma_f32_16x16x32_bf16 v[168:171], a[244:247], a[220:223], v[168:171]\n" \
"s_add_u32 s28, s28, s4\n" \
"s_addc_u32 s29, s29, 0\n" \
"v_mfma_f32_16x16x32_bf16 v[164:167], a[176:179], a[156:159], v[164:167]\n" \
"v_mfma_f32_16x16x32_bf16 v[164:167], a[240:243], a[220:223], v[164:167]\n" \
"ds_read_b128 a[156:159], v28 offset:47104\n" \
"ds_read_b128 a[220:223], v29 offset:47104\n" \
"s_waitcnt lgkmcnt(15)\n" \
"v_mfma_f32_16x16x32_bf16 a[0:3], a[160:163], a[128:131], a[0:3]\n" \
"s_add_u32 m0, s30, 0x1000\n" \
"s_mov_b32 s84, 0xd400\n" \
"v_mfma_f32_16x16x32_bf16 a[0:3], a[224:227], a[192:195], a[0:3]\n" \
"buffer_load_dwordx4 v24, s[76:79], s84 offen offset:2048 lds\n" \
"v_mfma_f32_16x16x32_bf16 a[4:7], a[164:167], a[128:131], a[4:7]\n" \
"ds_read_b64_tr_b16 a[176:177], v36 offset:32768\n" \
"v_mfma_f32_16x16x32_bf16 a[4:7], a[228:231], a[192:195], a[4:7]\n" \
"s_addk_i32 s84, 0x2000\n" \
"v_mfma_f32_16x16x32_bf16 a[12:15], a[172:175], a[128:131], a[12:15]\n" \
"buffer_load_dwordx4 v25, s[76:79], s84 offen offset:3072 lds\n" \
"s_waitcnt lgkmcnt(15)\n" \
"v_mfma_f32_16x16x32_bf16 a[12:15], a[236:239], a[192:195], a[12:15]\n" \
"ds_read_b64_tr_b16 a[178:179], v36 offset:33792\n" \
"s_waitcnt lgkmcnt(12)\n" \
"v_mfma_f32_16x16x32_bf16 a[8:11], a[128:131], a[168:171], a[8:11]\n" \
"ds_read_b64_tr_b16 a[240:241], v36 offset:40960\n" \
"s_waitcnt lgkmcnt(9)\n" \
"v_mfma_f32_16x16x32_bf16 a[8:11], a[192:195], a[232:235], a[8:11]\n" \
"ds_read_b64_tr_b16 a[242:243], v36 offset:41984\n" \
"v_mfma_f32_16x16x32_bf16 a[24:27], a[132:135], a[168:171], a[24:27]\n" \
"ds_read_b64_tr_b16 a[180:181], v37 offset:32768\n" \
"v_mfma_f32_16x16x32_bf16 a[24:27], a[196:199], a[232:235], a[24:27]\n" \
"ds_read_b64_tr_b16 a[182:183], v37 offset:33792\n" \
"v_mfma_f32_16x16x32_bf16 a[28:31], a[172:175], a[132:135], a[28:31]\n" \
"ds_read_b64_tr_b16 a[244:245], v37 offset:40960\n" \
"v_mfma_f32_16x16x32_bf16 a[28:31], a[236:239], a[196:199], a[28:31]\n" \
"ds_read_b64_tr_b16 a[246:247], v37 offset:41984\n" \
"v_mfma_f32_16x16x32_bf16 a[20:23], a[164:167], a[132:135], a[20:23]\n" \
"v_mfma_f32_16x16x32_bf16 a[20:23], a[228:231], a[196:199], a[20:23]\n" \
"s_add_u32 s88, s42, 0x2000\n" \
"buffer_store_dwordx4 v[196:199], v20, s[80:83], s88 offen offset:1024 sc1\n" \
"v_mfma_f32_16x16x32_bf16 a[16:19], a[160:163], a[132:135], a[16:19]\n" \
"ds_read_b64_tr_b16 a[184:185], v38 offset:32768\n" \
"v_mfma_f32_16x16x32_bf16 a[16:19], a[224:227], a[196:199], a[16:19]\n" \
"ds_read_b64_tr_b16 a[186:187], v38 offset:33792\n" \
"v_mfma_f32_16x16x32_bf16 a[32:35], a[160:163], a[136:139], a[32:35]\n" \
"ds_read_b64_tr_b16 a[248:249], v38 offset:40960\n" \
"v_mfma_f32_16x16x32_bf16 a[32:35], a[224:227], a[200:203], a[32:35]\n" \
"ds_read_b64_tr_b16 a[250:251], v38 offset:41984\n" \
"v_mfma_f32_16x16x32_bf16 a[36:39], a[164:167], a[136:139], a[36:39]\n" \
"v_lshlrev_b32_e32 v45, 3, v20\n" \
"v_add_u32_e32 v45, s30, v45\n" \
"v_mfma_f32_16x16x32_bf16 a[36:39], a[228:231], a[200:203], a[36:39]\n" \
"s_add_u32 s89, s42, 0x3000\n" \
"buffer_store_dwordx4 v[216:219], v20, s[80:83], s89 offen offset:0 sc1\n" \
"s_getpc_b64 s[86:87]\n" \
"L_ct0_%=:\n" \
"s_add_u32 s86, s86, L_ap0-L_ct0_%=\n" \
"s_addc_u32 s87, s87, 0\n" \
"s_add_u32 m0, s40, 0x2000\n" \
"v_mfma_f32_16x16x32_bf16 a[44:47], a[172:175], a[136:139], a[44:47]\n" \
"global_load_lds_dword v45, s[86:87]\n" \
"v_mfma_f32_16x16x32_bf16 a[44:47], a[236:239], a[200:203], a[44:47]\n" \
"ds_read_b64_tr_b16 a[188:189], v39 offset:32768\n" \
"v_mfma_f32_16x16x32_bf16 a[40:43], a[136:139], a[168:171], a[40:43]\n" \
"ds_read_b64_tr_b16 a[190:191], v39 offset:33792\n" \
"v_mfma_f32_16x16x32_bf16 a[40:43], a[200:203], a[232:235], a[40:43]\n" \
"ds_read_b64_tr_b16 a[252:253], v39 offset:40960\n" \
"v_mfma_f32_16x16x32_bf16 a[56:59], a[140:143], a[168:171], a[56:59]\n" \
"ds_read_b64_tr_b16 a[254:255], v39 offset:41984\n" \
"v_mfma_f32_16x16x32_bf16 a[56:59], a[204:207], a[232:235], a[56:59]\n" \
"v_mfma_f32_16x16x32_bf16 a[60:63], a[172:175], a[140:143], a[60:63]\n" \
"s_add_u32 s88, s42, 0x3000\n" \
"buffer_store_dwordx4 v[220:223], v20, s[80:83], s88 offen offset:1024 sc1\n" \
"v_mfma_f32_16x16x32_bf16 a[60:63], a[236:239], a[204:207], a[60:63]\n" \
"v_mfma_f32_16x16x32_bf16 a[52:55], a[164:167], a[140:143], a[52:55]\n" \
"v_mfma_f32_16x16x32_bf16 a[52:55], a[228:231], a[204:207], a[52:55]\n" \
"v_mfma_f32_16x16x32_bf16 a[48:51], a[160:163], a[140:143], a[48:51]\n" \
"v_mfma_f32_16x16x32_bf16 a[48:51], a[224:227], a[204:207], a[48:51]\n" \
"s_waitcnt lgkmcnt(15)\n" \
"v_mfma_f32_16x16x32_bf16 a[64:67], a[164:167], v[248:251], a[64:67]\n" \
"v_mfma_f32_16x16x32_bf16 a[64:67], a[228:231], v[48:51], a[64:67]\n" \
"v_mfma_f32_16x16x32_bf16 a[80:83], a[172:175], v[248:251], a[80:83]\n" \
"v_mfma_f32_16x16x32_bf16 a[80:83], a[236:239], v[48:51], a[80:83]\n" \
"v_mfma_f32_16x16x32_bf16 a[68:71], v[248:251], a[168:171], a[68:71]\n" \
"v_mfma_f32_16x16x32_bf16 a[68:71], v[48:51], a[232:235], a[68:71]\n" \
"s_waitcnt vmcnt(4) lgkmcnt(0)\n" \
"s_barrier\n" \
"s_nop 0\n" \
"v_mfma_f32_16x16x32_bf16 v[52:55], a[176:179], a[128:131], v[52:55]\n" \
"ds_read_b64_tr_b16 a[160:161], v32 offset:0\n" \
"v_accvgpr_read_b32 v184, a0\n" \
"v_mfma_f32_16x16x32_bf16 v[52:55], a[240:243], a[192:195], v[52:55]\n" \
"v_accvgpr_read_b32 v185, a1\n" \
"v_accvgpr_read_b32 v186, a2\n" \
"v_mfma_f32_16x16x32_bf16 v[56:59], a[180:183], a[128:131], v[56:59]\n" \
"ds_read_b64_tr_b16 a[162:163], v32 offset:512\n" \
"v_accvgpr_read_b32 v187, a3\n" \
"v_mfma_f32_16x16x32_bf16 v[56:59], a[244:247], a[192:195], v[56:59]\n" \
"v_accvgpr_read_b32 v188, a4\n" \
"v_accvgpr_read_b32 v189, a5\n" \
"v_mfma_f32_16x16x32_bf16 v[60:63], a[184:187], a[128:131], v[60:63]\n" \
"v_accvgpr_read_b32 v190, a6\n" \
"v_accvgpr_read_b32 v191, a7\n" \
"v_mfma_f32_16x16x32_bf16 v[60:63], a[248:251], a[192:195], v[60:63]\n" \
"ds_read_b64_tr_b16 a[224:225], v32 offset:4096\n" \
"v_accvgpr_read_b32 v196, a12\n" \
"v_mfma_f32_16x16x32_bf16 v[64:67], a[188:191], a[128:131], v[64:67]\n" \
"v_accvgpr_read_b32 v197, a13\n" \
"v_accvgpr_read_b32 v198, a14\n" \
"v_mfma_f32_16x16x32_bf16 v[64:67], a[252:255], a[192:195], v[64:67]\n" \
"ds_read_b128 a[128:131], v28 offset:0\n" \
"v_accvgpr_read_b32 v199, a15\n" \
"v_mfma_f32_16x16x32_bf16 v[80:83], a[188:191], a[132:135], v[80:83]\n" \
"ds_read_b128 a[192:195], v29 offset:0\n" \
"v_accvgpr_read_b32 v192, a8\n" \
"v_mfma_f32_16x16x32_bf16 v[80:83], a[252:255], a[196:199], v[80:83]\n" \
"ds_read_b64_tr_b16 a[226:227], v32 offset:4608\n" \
"v_accvgpr_read_b32 v193, a9\n" \
"v_mfma_f32_16x16x32_bf16 v[76:79], a[184:187], a[132:135], v[76:79]\n" \
"v_accvgpr_read_b32 v194, a10\n" \
"v_accvgpr_read_b32 v195, a11\n" \
"v_mfma_f32_16x16x32_bf16 v[76:79], a[248:251], a[196:199], v[76:79]\n" \
"ds_read_b64_tr_b16 a[164:165], v33 offset:0\n" \
"v_accvgpr_read_b32 v208, a24\n" \
"v_mfma_f32_16x16x32_bf16 v[72:75], a[180:183], a[132:135], v[72:75]\n" \
"v_accvgpr_read_b32 v209, a25\n" \
"v_accvgpr_read_b32 v210, a26\n" \
"v_mfma_f32_16x16x32_bf16 v[72:75], a[244:247], a[196:199], v[72:75]\n" \
"v_accvgpr_read_b32 v211, a27\n" \
"v_accvgpr_read_b32 v212, a28\n" \
"v_mfma_f32_16x16x32_bf16 v[68:71], a[176:179], a[132:135], v[68:71]\n" \
"ds_read_b64_tr_b16 a[166:167], v33 offset:512\n" \
"v_accvgpr_read_b32 v213, a29\n" \
"v_mfma_f32_16x16x32_bf16 v[68:71], a[240:243], a[196:199], v[68:71]\n" \
"ds_read_b128 a[132:135], v28 offset:2048\n" \
"v_accvgpr_read_b32 v214, a30\n" \
"v_mfma_f32_16x16x32_bf16 v[84:87], a[176:179], a[136:139], v[84:87]\n" \
"ds_read_b128 a[196:199], v29 offset:2048\n" \
"v_accvgpr_read_b32 v215, a31\n" \
"v_mfma_f32_16x16x32_bf16 v[84:87], a[240:243], a[200:203], v[84:87]\n" \
"ds_read_b64_tr_b16 a[228:229], v33 offset:4096\n" \
"v_accvgpr_read_b32 v204, a20\n" \
"v_mfma_f32_16x16x32_bf16 v[88:91], a[180:183], a[136:139], v[88:91]\n" \
"v_accvgpr_read_b32 v205, a21\n" \
"v_accvgpr_read_b32 v206, a22\n" \
"v_mfma_f32_16x16x32_bf16 v[88:91], a[244:247], a[200:203], v[88:91]\n" \
"v_accvgpr_read_b32 v207, a23\n" \
"v_accvgpr_read_b32 v200, a16\n" \
"v_mfma_f32_16x16x32_bf16 v[92:95], a[184:187], a[136:139], v[92:95]\n" \
"ds_read_b64_tr_b16 a[230:231], v33 offset:4608\n" \
"v_accvgpr_read_b32 v201, a17\n" \
"v_mfma_f32_16x16x32_bf16 v[92:95], a[248:251], a[200:203], v[92:95]\n" \
"v_accvgpr_read_b32 v202, a18\n" \
"v_accvgpr_read_b32 v203, a19\n" \
"v_mfma_f32_16x16x32_bf16 v[96:99], a[188:191], a[136:139], v[96:99]\n" \
"v_accvgpr_read_b32 v216, a32\n" \
"v_accvgpr_read_b32 v217, a33\n" \
"v_mfma_f32_16x16x32_bf16 v[96:99], a[252:255], a[200:203], v[96:99]\n" \
"ds_read_b128 a[136:139], v28 offset:4096\n" \
"v_accvgpr_read_b32 v218, a34\n" \
"v_mfma_f32_16x16x32_bf16 v[112:115], a[188:191], a[140:143], v[112:115]\n" \
"ds_read_b128 a[200:203], v29 offset:4096\n" \
"v_accvgpr_read_b32 v219, a35\n" \
"v_mfma_f32_16x16x32_bf16 v[112:115], a[252:255], a[204:207], v[112:115]\n" \
"ds_read_b64_tr_b16 a[172:173], v35 offset:0\n" \
"v_accvgpr_read_b32 v220, a36\n" \
"v_mfma_f32_16x16x32_bf16 v[108:111], a[184:187], a[140:143], v[108:111]\n" \
"ds_read_b64_tr_b16 a[174:175], v35 offset:512\n" \
"v_accvgpr_read_b32 v221, a37\n" \
"v_mfma_f32_16x16x32_bf16 v[108:111], a[248:251], a[204:207], v[108:111]\n" \
"v_accvgpr_read_b32 v222, a38\n" \
"v_accvgpr_read_b32 v223, a39\n" \
"v_mfma_f32_16x16x32_bf16 v[104:107], a[180:183], a[140:143], v[104:107]\n" \
"ds_read_b64_tr_b16 a[236:237], v35 offset:4096\n" \
"v_accvgpr_read_b32 v228, a44\n" \
"v_mfma_f32_16x16x32_bf16 v[104:107], a[244:247], a[204:207], v[104:107]\n" \
"v_accvgpr_read_b32 v229, a45\n" \
"v_accvgpr_read_b32 v230, a46\n" \
"v_mfma_f32_16x16x32_bf16 v[100:103], a[176:179], a[140:143], v[100:103]\n" \
"v_accvgpr_read_b32 v231, a47\n" \
"v_accvgpr_read_b32 v224, a40\n" \
"v_mfma_f32_16x16x32_bf16 v[100:103], a[240:243], a[204:207], v[100:103]\n" \
"ds_read_b128 a[140:143], v28 offset:6144\n" \
"v_accvgpr_read_b32 v225, a41\n" \
"v_mfma_f32_16x16x32_bf16 v[116:119], a[176:179], a[144:147], v[116:119]\n" \
"ds_read_b128 a[204:207], v29 offset:6144\n" \
"v_accvgpr_read_b32 v226, a42\n" \
"v_mfma_f32_16x16x32_bf16 v[116:119], a[240:243], a[208:211], v[116:119]\n" \
"ds_read_b64_tr_b16 a[238:239], v35 offset:4608\n" \
"v_accvgpr_read_b32 v227, a43\n" \
"v_mfma_f32_16x16x32_bf16 v[120:123], a[180:183], a[144:147], v[120:123]\n" \
"ds_read_b64_tr_b16 a[168:169], v34 offset:0\n" \
"v_accvgpr_read_b32 v240, a56\n" \
"v_mfma_f32_16x16x32_bf16 v[120:123], a[244:247], a[208:211], v[120:123]\n" \
"v_accvgpr_read_b32 v241, a57\n" \
"v_accvgpr_read_b32 v242, a58\n" \
"v_mfma_f32_16x16x32_bf16 v[124:127], a[184:187], a[144:147], v[124:127]\n" \
"v_accvgpr_read_b32 v243, a59\n" \
"v_accvgpr_read_b32 v244, a60\n" \
"v_mfma_f32_16x16x32_bf16 v[124:127], a[248:251], a[208:211], v[124:127]\n" \
"ds_read_b64_tr_b16 a[170:171], v34 offset:512\n" \
"v_accvgpr_read_b32 v245, a61\n" \
"v_mfma_f32_16x16x32_bf16 v[128:131], a[188:191], a[144:147], v[128:131]\n" \
"v_accvgpr_read_b32 v246, a62\n" \
"v_accvgpr_read_b32 v247, a63\n" \
"v_mfma_f32_16x16x32_bf16 v[128:131], a[252:255], a[208:211], v[128:131]\n" \
"ds_read_b128 a[144:147], v28 offset:8192\n" \
"v_accvgpr_read_b32 v236, a52\n" \
"v_mfma_f32_16x16x32_bf16 v[144:147], a[188:191], a[148:151], v[144:147]\n" \
"ds_read_b128 a[208:211], v29 offset:8192\n" \
"v_accvgpr_read_b32 v237, a53\n" \
"v_mfma_f32_16x16x32_bf16 v[144:147], a[252:255], a[212:215], v[144:147]\n" \
"ds_read_b64_tr_b16 a[232:233], v34 offset:4096\n" \
"v_accvgpr_read_b32 v238, a54\n" \
"v_mfma_f32_16x16x32_bf16 v[140:143], a[184:187], a[148:151], v[140:143]\n" \
"v_accvgpr_read_b32 v239, a55\n" \
"v_accvgpr_read_b32 v232, a48\n" \
"v_mfma_f32_16x16x32_bf16 v[140:143], a[248:251], a[212:215], v[140:143]\n" \
"ds_read_b64_tr_b16 a[234:235], v34 offset:4608\n" \
"v_accvgpr_read_b32 v233, a49\n" \
"v_mfma_f32_16x16x32_bf16 v[136:139], a[180:183], a[148:151], v[136:139]\n" \
"v_accvgpr_read_b32 v234, a50\n" \
"v_accvgpr_read_b32 v235, a51\n" \
"v_mfma_f32_16x16x32_bf16 v[136:139], a[244:247], a[212:215], v[136:139]\n" \
"v_accvgpr_read_b32 v180, a64\n" \
"v_accvgpr_read_b32 v181, a65\n" \
"v_mfma_f32_16x16x32_bf16 v[132:135], a[176:179], a[148:151], v[132:135]\n" \
"ds_read_b128 v[248:251], v22 offset:0\n" \
"v_accvgpr_read_b32 v182, a66\n" \
"v_mfma_f32_16x16x32_bf16 v[132:135], a[240:243], a[212:215], v[132:135]\n" \
"ds_read_b128 a[148:151], v28 offset:10240\n" \
"v_accvgpr_read_b32 v183, a67\n" \
"v_mfma_f32_16x16x32_bf16 v[148:151], a[176:179], a[152:155], v[148:151]\n" \
"ds_read_b128 a[212:215], v29 offset:10240\n" \
"v_accvgpr_read_b32 v252, a68\n" \
"v_mfma_f32_16x16x32_bf16 v[148:151], a[240:243], a[216:219], v[148:151]\n" \
"ds_read_b128 v[48:51], v23 offset:0\n" \
"v_accvgpr_read_b32 v253, a69\n" \
"v_mfma_f32_16x16x32_bf16 v[152:155], a[180:183], a[152:155], v[152:155]\n" \
"v_accvgpr_read_b32 v254, a70\n" \
"v_accvgpr_read_b32 v255, a71\n" \
"v_mfma_f32_16x16x32_bf16 v[152:155], a[244:247], a[216:219], v[152:155]\n" \
"v_lshrrev_b32_e64 v24, 9, v20\n" \
"v_and_b32_e32 v26, 0x1ff, v20\n" \
"v_mfma_f32_16x16x32_bf16 v[156:159], a[184:187], a[152:155], v[156:159]\n" \
"v_lshl_add_u32 v24, v24, 3, v26\n" \
"v_lshrrev_b32_e64 v25, 8, v20\n" \
"v_mfma_f32_16x16x32_bf16 v[156:159], a[248:251], a[216:219], v[156:159]\n" \
"v_mul_u32_u24_e32 v25, 0x24700, v25\n" \
"v_add_u32_e64 v25, v25, v20\n" \
"v_mfma_f32_16x16x32_bf16 v[160:163], a[188:191], a[152:155], v[160:163]\n" \
"v_lshrrev_b32_e32 v26, 2, v20\n" \
"v_add_u32_e32 v26, s45, v26\n" \
"v_mfma_f32_16x16x32_bf16 v[160:163], a[252:255], a[216:219], v[160:163]\n" \
"ds_read_b128 a[152:155], v28 offset:12288\n" \
"v_add_u32_e64 v27, s46, v20\n" \
"v_mfma_f32_16x16x32_bf16 v[176:179], a[188:191], a[156:159], v[176:179]\n" \
"ds_read_b128 a[216:219], v29 offset:12288\n" \
"v_add_u32_e64 v21, s43, v27\n" \
"v_mfma_f32_16x16x32_bf16 v[176:179], a[252:255], a[220:223], v[176:179]\n" \
"v_mfma_f32_16x16x32_bf16 v[172:175], a[184:187], a[156:159], v[172:175]\n" \
"v_mfma_f32_16x16x32_bf16 v[172:175], a[248:251], a[220:223], v[172:175]\n" \
"v_mfma_f32_16x16x32_bf16 v[168:171], a[180:183], a[156:159], v[168:171]\n" \
"v_mfma_f32_16x16x32_bf16 v[168:171], a[244:247], a[220:223], v[168:171]\n" \
"v_mfma_f32_16x16x32_bf16 v[164:167], a[176:179], a[156:159], v[164:167]\n" \
"v_mfma_f32_16x16x32_bf16 v[164:167], a[240:243], a[220:223], v[164:167]\n" \
"ds_read_b128 a[156:159], v28 offset:14336\n" \
"ds_read_b128 a[220:223], v29 offset:14336\n" \
"s_waitcnt lgkmcnt(15)\n" \
"s_nop 0\n" \
"v_mfma_f32_16x16x32_bf16 v[184:187], a[160:163], a[128:131], v[184:187]\n" \
"v_bfe_u32 v44, v20, 2, 5\n" \
"ds_read_b64_tr_b16 a[176:177], v36 offset:0\n" \
"v_mfma_f32_16x16x32_bf16 v[184:187], a[224:227], a[192:195], v[184:187]\n" \
"buffer_load_dword v44, v44, s[80:83], s38 offen offset:0 sc1\n" \
"v_mfma_f32_16x16x32_bf16 v[188:191], a[164:167], a[128:131], v[188:191]\n" \
"ds_read_b64_tr_b16 a[178:179], v36 offset:1024\n" \
"v_mfma_f32_16x16x32_bf16 v[188:191], a[228:231], a[192:195], v[188:191]\n" \
"ds_read_b64_tr_b16 a[240:241], v36 offset:8192\n" \
"v_mfma_f32_16x16x32_bf16 v[196:199], a[172:175], a[128:131], v[196:199]\n" \
"ds_read_b64_tr_b16 a[242:243], v36 offset:9216\n" \
"s_waitcnt lgkmcnt(15)\n" \
"v_mfma_f32_16x16x32_bf16 v[196:199], a[236:239], a[192:195], v[196:199]\n" \
"v_mfma_f32_16x16x32_bf16 v[192:195], a[128:131], a[168:171], v[192:195]\n" \
"ds_read_b64_tr_b16 a[180:181], v37 offset:0\n" \
"s_waitcnt lgkmcnt(13)\n" \
"v_mfma_f32_16x16x32_bf16 v[192:195], a[192:195], a[232:235], v[192:195]\n" \
"v_mfma_f32_16x16x32_bf16 v[208:211], a[132:135], a[168:171], v[208:211]\n" \
"ds_read_b64_tr_b16 a[182:183], v37 offset:1024\n" \
"v_mfma_f32_16x16x32_bf16 v[208:211], a[196:199], a[232:235], v[208:211]\n" \
"ds_read_b64_tr_b16 a[244:245], v37 offset:8192\n" \
"v_mfma_f32_16x16x32_bf16 v[212:215], a[172:175], a[132:135], v[212:215]\n" \
"v_mfma_f32_16x16x32_bf16 v[212:215], a[236:239], a[196:199], v[212:215]\n" \
"ds_read_b64_tr_b16 a[246:247], v37 offset:9216\n" \
"v_cvt_pk_bf16_f32 v188, v188, v189\n" \
"v_mfma_f32_16x16x32_bf16 v[204:207], a[164:167], a[132:135], v[204:207]\n" \
"v_cvt_pk_bf16_f32 v189, v190, v191\n" \
"v_cvt_pk_bf16_f32 v190, v198, v199\n" \
"v_mfma_f32_16x16x32_bf16 v[204:207], a[228:231], a[196:199], v[204:207]\n" \
"ds_read_b64_tr_b16 a[184:185], v38 offset:0\n" \
"v_mov_b32_e32 v191, 0x3f80\n" \
"v_mfma_f32_16x16x32_bf16 v[200:203], a[160:163], a[132:135], v[200:203]\n" \
"ds_read_b64_tr_b16 a[186:187], v38 offset:1024\n" \
"v_cvt_pk_bf16_f32 v184, v184, v185\n" \
"v_mfma_f32_16x16x32_bf16 v[200:203], a[224:227], a[196:199], v[200:203]\n" \
"v_cvt_pk_bf16_f32 v185, v186, v187\n" \
"v_cvt_pk_bf16_f32 v186, v196, v197\n" \
"v_mfma_f32_16x16x32_bf16 v[216:219], a[160:163], a[136:139], v[216:219]\n" \
"ds_read_b64_tr_b16 a[248:249], v38 offset:8192\n" \
"v_mov_b32_e64 v187, 0\n" \
"v_mfma_f32_16x16x32_bf16 v[216:219], a[224:227], a[200:203], v[216:219]\n" \
"ds_read_b64_tr_b16 a[250:251], v38 offset:9216\n" \
"v_mfma_f32_16x16x32_bf16 v[220:223], a[164:167], a[136:139], v[220:223]\n" \
"v_mfma_f32_16x16x32_bf16 v[220:223], a[228:231], a[200:203], v[220:223]\n" \
"ds_read_b64_tr_b16 a[188:189], v39 offset:0\n" \
"v_cvt_pk_bf16_f32 v204, v204, v205\n" \
"v_mfma_f32_16x16x32_bf16 v[228:231], a[172:175], a[136:139], v[228:231]\n" \
"v_cvt_pk_bf16_f32 v205, v206, v207\n" \
"v_cvt_pk_bf16_f32 v206, v214, v215\n" \
"v_mfma_f32_16x16x32_bf16 v[228:231], a[236:239], a[200:203], v[228:231]\n" \
"ds_read_b64_tr_b16 a[190:191], v39 offset:1024\n" \
"v_mov_b32_e32 v207, 0x3f80\n" \
"v_mfma_f32_16x16x32_bf16 v[224:227], a[136:139], a[168:171], v[224:227]\n" \
"ds_read_b64_tr_b16 a[252:253], v39 offset:8192\n" \
"v_cvt_pk_bf16_f32 v200, v200, v201\n" \
"v_mfma_f32_16x16x32_bf16 v[224:227], a[200:203], a[232:235], v[224:227]\n" \
"v_cvt_pk_bf16_f32 v201, v202, v203\n" \
"v_cvt_pk_bf16_f32 v202, v212, v213\n" \
"v_mfma_f32_16x16x32_bf16 v[240:243], a[140:143], a[168:171], v[240:243]\n" \
"ds_read_b64_tr_b16 a[254:255], v39 offset:9216\n" \
"v_mov_b32_e64 v203, 0\n" \
"v_mfma_f32_16x16x32_bf16 v[240:243], a[204:207], a[232:235], v[240:243]\n" \
"v_mfma_f32_16x16x32_bf16 v[244:247], a[172:175], a[140:143], v[244:247]\n" \
"v_mfma_f32_16x16x32_bf16 v[244:247], a[236:239], a[204:207], v[244:247]\n" \
"v_cvt_pk_bf16_f32 v220, v220, v221\n" \
"v_cvt_pk_bf16_f32 v221, v222, v223\n" \
"v_mfma_f32_16x16x32_bf16 v[236:239], a[164:167], a[140:143], v[236:239]\n" \
"v_cvt_pk_bf16_f32 v222, v230, v231\n" \
"v_mov_b32_e32 v223, 0x3f80\n" \
"v_mfma_f32_16x16x32_bf16 v[236:239], a[228:231], a[204:207], v[236:239]\n" \
"v_cvt_pk_bf16_f32 v216, v216, v217\n" \
"v_cvt_pk_bf16_f32 v217, v218, v219\n" \
"v_mfma_f32_16x16x32_bf16 v[232:235], a[160:163], a[140:143], v[232:235]\n" \
"v_cvt_pk_bf16_f32 v218, v228, v229\n" \
"v_mov_b32_e64 v219, 0\n" \
"v_mfma_f32_16x16x32_bf16 v[232:235], a[224:227], a[204:207], v[232:235]\n" \
"s_waitcnt lgkmcnt(15)\n" \
"s_nop 0\n" \
"v_mfma_f32_16x16x32_bf16 v[180:183], a[164:167], v[248:251], v[180:183]\n" \
"v_mfma_f32_16x16x32_bf16 v[180:183], a[228:231], v[48:51], v[180:183]\n" \
"v_mfma_f32_16x16x32_bf16 a[80:83], a[172:175], v[248:251], a[80:83]\n" \
"v_mfma_f32_16x16x32_bf16 a[80:83], a[236:239], v[48:51], a[80:83]\n" \
"v_cvt_pk_bf16_f32 v236, v236, v237\n" \
"v_cvt_pk_bf16_f32 v237, v238, v239\n" \
"v_mfma_f32_16x16x32_bf16 v[252:255], v[248:251], a[168:171], v[252:255]\n" \
"v_cvt_pk_bf16_f32 v238, v246, v247\n" \
"v_mov_b32_e32 v239, 0x3f80\n" \
"v_mfma_f32_16x16x32_bf16 v[252:255], v[48:51], a[232:235], v[252:255]\n" \
"v_cvt_pk_bf16_f32 v232, v232, v233\n" \
"v_cvt_pk_bf16_f32 v233, v234, v235\n" \
"s_waitcnt vmcnt(0) lgkmcnt(0)\n" \
"s_barrier\n" \
"v_mov_b32_e32 v45, s62\n" \
"s_mov_b64 exec, 1\n" \
"buffer_store_dword v45, off, s[80:83], s37 offset:32 sc1\n" \
"s_mov_b64 exec, -1\n" \
"v_cmp_ne_u32_e32 vcc, s62, v44\n" \
"s_nop 4\n" \
"s_cbranch_vccnz L_poll160_%=\n" \
"L_pollr160_%=:\n" \
"v_mfma_f32_16x16x32_bf16 v[52:55], a[176:179], a[128:131], v[52:55]\n" \
"s_mov_b32 s73, s50\n" \
"s_sub_u32 m0, s31, 0x10000\n" \
"v_mfma_f32_16x16x32_bf16 v[52:55], a[240:243], a[192:195], v[52:55]\n" \
"buffer_load_dwordx4 v20, s[80:83], s73 offen offset:0 sc1 lds\n" \
"v_mfma_f32_16x16x32_bf16 v[56:59], a[180:183], a[128:131], v[56:59]\n" \
"s_add_u32 s74, s50, 0x4000\n" \
"s_add_u32 s75, s50, 0x8000\n" \
"v_mfma_f32_16x16x32_bf16 v[56:59], a[244:247], a[192:195], v[56:59]\n" \
"s_mov_b32 s70, 0x3e0293ee\n" \
"s_mov_b32 s76, s52\n" \
"v_mfma_f32_16x16x32_bf16 v[60:63], a[184:187], a[128:131], v[60:63]\n" \
"v_mul_f32_e32 v52, s70, v52\n" \
"v_mul_f32_e32 v53, s70, v53\n" \
"v_mfma_f32_16x16x32_bf16 v[60:63], a[248:251], a[192:195], v[60:63]\n" \
"buffer_load_dwordx4 v20, s[80:83], s73 offen offset:1024 sc1 lds\n" \
"v_mfma_f32_16x16x32_bf16 v[64:67], a[188:191], a[128:131], v[64:67]\n" \
"v_mul_f32_e32 v54, s70, v54\n" \
"v_mul_f32_e32 v55, s70, v55\n" \
"v_mfma_f32_16x16x32_bf16 v[64:67], a[252:255], a[192:195], v[64:67]\n" \
"v_cvt_pk_bf16_f32 v52, v52, v53\n" \
"v_mul_f32_e64 v56, s70, v56\n" \
"v_mfma_f32_16x16x32_bf16 v[80:83], a[188:191], a[132:135], v[80:83]\n" \
"v_mul_f32_e32 v60, s70, v60\n" \
"v_mul_f32_e32 v61, s70, v61\n" \
"v_mfma_f32_16x16x32_bf16 v[80:83], a[252:255], a[196:199], v[80:83]\n" \
".fill (L_pb + 36576 - .) / 4, 4, 0xBF800000\n" \
"L_pp9:\n" \
"s_cbranch_execz L_pret\n" \
"buffer_load_dwordx4 v20, s[80:83], s73 offen offset:2048 sc1 lds\n" \
"v_mfma_f32_16x16x32_bf16 v[76:79], a[184:187], a[132:135], v[76:79]\n" \
"v_mul_f32_e32 v62, s70, v62\n" \
"v_mul_f32_e64 v63, s70, v63\n" \
"v_mfma_f32_16x16x32_bf16 v[76:79], a[248:251], a[196:199], v[76:79]\n" \
"v_cvt_pk_bf16_f32 v60, v60, v61\n" \
"v_mul_f32_e64 v57, s70, v57\n" \
"v_mfma_f32_16x16x32_bf16 v[72:75], a[180:183], a[132:135], v[72:75]\n" \
"v_mul_f32_e32 v58, s70, v58\n" \
"v_mul_f32_e32 v59, s70, v59\n" \
"v_mfma_f32_16x16x32_bf16 v[72:75], a[244:247], a[196:199], v[72:75]\n" \
"buffer_load_dwordx4 v20, s[80:83], s73 offen offset:3072 sc1 lds\n" \
"v_mfma_f32_16x16x32_bf16 v[68:71], a[176:179], a[132:135], v[68:71]\n" \
"s_sub_u32 m0, s31, s51\n" \
"v_cvt_pk_bf16_f32 v53, v54, v55\n" \
"v_mfma_f32_16x16x32_bf16 v[68:71], a[240:243], a[196:199], v[68:71]\n" \
"v_mul_f32_e32 v64, s70, v64\n" \
"v_mul_f32_e64 v65, s70, v65\n" \
"v_mfma_f32_16x16x32_bf16 v[84:87], a[176:179], a[136:139], v[84:87]\n" \
"v_mul_f32_e32 v66, s70, v66\n" \
"v_mul_f32_e32 v67, s70, v67\n" \
"v_mfma_f32_16x16x32_bf16 v[84:87], a[240:243], a[200:203], v[84:87]\n" \
"buffer_load_dwordx4 v20, s[80:83], s74 offen offset:0 sc1 lds\n" \
"v_mfma_f32_16x16x32_bf16 v[88:91], a[180:183], a[136:139], v[88:91]\n" \
"v_cvt_pk_bf16_f32 v61, v62, v63\n" \
"v_cvt_pk_bf16_f32 v54, v56, v57\n" \
"v_mfma_f32_16x16x32_bf16 v[88:91], a[244:247], a[200:203], v[88:91]\n" \
"v_cvt_pk_bf16_f32 v55, v58, v59\n" \
"v_cvt_pk_bf16_f32 v62, v64, v65\n" \
"v_mfma_f32_16x16x32_bf16 v[92:95], a[184:187], a[136:139], v[92:95]\n" \
"v_cvt_pk_bf16_f32 v63, v66, v67\n" \
"v_mul_f32_e64 v68, s70, v68\n" \
"v_mfma_f32_16x16x32_bf16 v[92:95], a[248:251], a[200:203], v[92:95]\n" \
"buffer_load_dwordx4 v20, s[80:83], s74 offen offset:1024 sc1 lds\n" \
"v_mfma_f32_16x16x32_bf16 v[96:99], a[188:191], a[136:139], v[96:99]\n" \
"ds_write_b128 v27, v[52:55] offset:0\n" \
"v_mfma_f32_16x16x32_bf16 v[96:99], a[252:255], a[200:203], v[96:99]\n" \
"v_mul_f32_e32 v69, s70, v69\n" \
"v_mul_f32_e32 v76, s70, v76\n" \
"v_mfma_f32_16x16x32_bf16 v[112:115], a[188:191], a[140:143], v[112:115]\n" \
"v_mul_f32_e32 v77, s70, v77\n" \
"v_mul_f32_e32 v70, s70, v70\n" \
"v_mfma_f32_16x16x32_bf16 v[112:115], a[252:255], a[204:207], v[112:115]\n" \
"buffer_load_dwordx4 v20, s[80:83], s74 offen offset:2048 sc1 lds\n" \
"v_mfma_f32_16x16x32_bf16 v[108:111], a[184:187], a[140:143], v[108:111]\n" \
"ds_write_b128 v27, v[60:63] offset:1024\n" \
"v_mfma_f32_16x16x32_bf16 v[108:111], a[248:251], a[204:207], v[108:111]\n" \
"v_mul_f32_e64 v71, s70, v71\n" \
"v_cvt_pk_bf16_f32 v68, v68, v69\n" \
"v_mfma_f32_16x16x32_bf16 v[104:107], a[180:183], a[140:143], v[104:107]\n" \
"v_mul_f32_e32 v78, s70, v78\n" \
"v_mul_f32_e32 v79, s70, v79\n" \
"v_mfma_f32_16x16x32_bf16 v[104:107], a[244:247], a[204:207], v[104:107]\n" \
"buffer_load_dwordx4 v20, s[80:83], s74 offen offset:3072 sc1 lds\n" \
"v_mfma_f32_16x16x32_bf16 v[100:103], a[176:179], a[140:143], v[100:103]\n" \
"s_sub_u32 m0, s31, 0x4000\n" \
"v_cvt_pk_bf16_f32 v76, v76, v77\n" \
"v_mfma_f32_16x16x32_bf16 v[100:103], a[240:243], a[204:207], v[100:103]\n" \
"v_mul_f32_e32 v72, s70, v72\n" \
"v_mul_f32_e32 v73, s70, v73\n" \
"v_mfma_f32_16x16x32_bf16 v[116:119], a[176:179], a[144:147], v[116:119]\n" \
"v_mul_f32_e32 v74, s70, v74\n" \
"v_mul_f32_e32 v75, s70, v75\n" \
"v_mfma_f32_16x16x32_bf16 v[116:119], a[240:243], a[208:211], v[116:119]\n" \
"buffer_load_dwordx4 v20, s[80:83], s75 offen offset:0 sc1 lds\n" \
"v_mfma_f32_16x16x32_bf16 v[120:123], a[180:183], a[144:147], v[120:123]\n" \
"v_cvt_pk_bf16_f32 v69, v70, v71\n" \
"v_mul_f32_e64 v80, s70, v80\n" \
"v_mfma_f32_16x16x32_bf16 v[120:123], a[244:247], a[208:211], v[120:123]\n" \
"v_mul_f32_e32 v81, s70, v81\n" \
"v_mul_f32_e32 v82, s70, v82\n" \
"v_mfma_f32_16x16x32_bf16 v[124:127], a[184:187], a[144:147], v[124:127]\n" \
"v_mul_f32_e64 v83, s70, v83\n" \
"v_cvt_pk_bf16_f32 v77, v78, v79\n" \
"v_mfma_f32_16x16x32_bf16 v[124:127], a[248:251], a[208:211], v[124:127]\n" \
"buffer_load_dwordx4 v20, s[80:83], s75 offen offset:1024 sc1 lds\n" \
"v_mfma_f32_16x16x32_bf16 v[128:131], a[188:191], a[144:147], v[128:131]\n" \
"v_cvt_pk_bf16_f32 v70, v72, v73\n" \
"v_cvt_pk_bf16_f32 v71, v74, v75\n" \
"v_mfma_f32_16x16x32_bf16 v[128:131], a[252:255], a[208:211], v[128:131]\n" \
"v_cvt_pk_bf16_f32 v78, v80, v81\n" \
"v_cvt_pk_bf16_f32 v79, v82, v83\n" \
"v_mfma_f32_16x16x32_bf16 v[144:147], a[188:191], a[148:151], v[144:147]\n" \
"ds_write_b128 v27, v[68:71] offset:4096\n" \
"v_mfma_f32_16x16x32_bf16 v[144:147], a[252:255], a[212:215], v[144:147]\n" \
"buffer_load_dwordx4 v20, s[80:83], s75 offen offset:2048 sc1 lds\n" \
"v_mfma_f32_16x16x32_bf16 v[140:143], a[184:187], a[148:151], v[140:143]\n" \
"v_cvt_pk_bf16_f32 v180, v180, v181\n" \
"v_accvgpr_read_b32 v50, a82\n" \
"v_mfma_f32_16x16x32_bf16 v[140:143], a[248:251], a[212:215], v[140:143]\n" \
"ds_write_b128 v27, v[76:79] offset:5120\n" \
"v_mfma_f32_16x16x32_bf16 v[136:139], a[180:183], a[148:151], v[136:139]\n" \
"v_accvgpr_read_b32 v51, a83\n" \
"v_cvt_pk_bf16_f32 v181, v182, v183\n" \
"v_mfma_f32_16x16x32_bf16 v[136:139], a[244:247], a[212:215], v[136:139]\n" \
"buffer_load_dwordx4 v20, s[80:83], s75 offen offset:3072 sc1 lds\n" \
"v_mfma_f32_16x16x32_bf16 v[132:135], a[176:179], a[148:151], v[132:135]\n" \
"s_mov_b32 m0, s53\n" \
"v_cvt_pk_bf16_f32 v182, v50, v51\n" \
"v_mfma_f32_16x16x32_bf16 v[132:135], a[240:243], a[212:215], v[132:135]\n" \
"v_mov_b32_e32 v183, 0x3f80\n" \
"v_cvt_pk_bf16_f32 v234, v244, v245\n" \
"v_mfma_f32_16x16x32_bf16 v[148:151], a[176:179], a[152:155], v[148:151]\n" \
"v_mov_b32_e32 v235, 0\n" \
"v_cvt_pk_bf16_f32 v54, v192, v193\n" \
"v_mfma_f32_16x16x32_bf16 v[148:151], a[240:243], a[216:219], v[148:151]\n" \
"buffer_load_dwordx4 v20, s[80:83], s76 offen offset:0 sc1 lds\n" \
"v_mfma_f32_16x16x32_bf16 v[152:155], a[180:183], a[152:155], v[152:155]\n" \
"v_cvt_pk_bf16_f32 v55, v194, v195\n" \
"v_cvt_pk_bf16_f32 v56, v208, v209\n" \
"v_mfma_f32_16x16x32_bf16 v[152:155], a[244:247], a[216:219], v[152:155]\n" \
"v_cvt_pk_bf16_f32 v57, v210, v211\n" \
"v_cvt_pk_bf16_f32 v58, v224, v225\n" \
"v_cvt_pk_bf16_f32 v59, v226, v227\n" \
"v_cvt_pk_bf16_f32 v60, v240, v241\n" \
"v_cvt_pk_bf16_f32 v61, v242, v243\n" \
"v_mfma_f32_16x16x32_bf16 v[32:35], v[180:183], v[184:187], 0\n" \
"buffer_load_dwordx4 v20, s[80:83], s76 offen offset:1024 sc1 lds\n" \
"v_mfma_f32_16x16x32_bf16 v[156:159], a[184:187], a[152:155], v[156:159]\n" \
"v_mfma_f32_16x16x32_bf16 v[192:195], v[188:191], v[184:187], 0\n" \
"v_mfma_f32_16x16x32_bf16 v[208:211], v[204:207], v[200:203], 0\n" \
"v_mfma_f32_16x16x32_bf16 v[36:39], v[188:191], v[200:203], 0\n" \
"v_mfma_f32_16x16x32_bf16 v[224:227], v[220:223], v[216:219], 0\n" \
"v_mfma_f32_16x16x32_bf16 v[248:251], v[204:207], v[216:219], 0\n" \
"v_mfma_f32_16x16x32_bf16 v[240:243], v[236:239], v[232:235], 0\n" \
"v_mfma_f32_16x16x32_bf16 v[64:67], v[220:223], v[232:235], 0\n" \
"s_mov_b32 s64, 0xaaaaaaaa\n" \
"s_mov_b32 s65, 0xaaaaaaaa\n" \
"v_mfma_f32_16x16x32_bf16 v[156:159], a[248:251], a[216:219], v[156:159]\n" \
"v_cndmask_b32_e64 v32, v32, v33, s[64:65]\n" \
"v_cndmask_b32_e64 v34, v34, v35, s[64:65]\n" \
"v_mfma_f32_16x16x32_bf16 v[160:163], a[188:191], a[152:155], v[160:163]\n" \
"s_mov_b32 s66, 0xfff0ffff\n" \
"s_mov_b32 s67, 0xf000ff00\n" \
"s_mov_b32 s68, 0xffc0fffc\n" \
"s_mov_b32 s69, 0xc000fc00\n" \
"v_cndmask_b32_e64 v192, v192, v193, s[64:65]\n" \
"v_add_f32_e64 v32, s47, v32\n" \
"v_cndmask_b32_e64 v194, v194, v195, s[64:65]\n" \
"v_add_f32_e64 v34, s47, v34\n" \
"v_cndmask_b32_e64 v208, v208, v209, s[64:65]\n" \
"v_cndmask_b32_e64 v36, v36, v37, s[64:65]\n" \
"v_cndmask_b32_e64 v210, v210, v211, s[64:65]\n" \
"v_cndmask_b32_e64 v38, v38, v39, s[64:65]\n" \
"v_cndmask_b32_e64 v224, v224, v225, s[64:65]\n" \
"v_cndmask_b32_e64 v248, v248, v249, s[64:65]\n" \
"v_cndmask_b32_e64 v226, v226, v227, s[64:65]\n" \
"v_cndmask_b32_e64 v250, v250, v251, s[64:65]\n" \
"v_cndmask_b32_e64 v240, v240, v241, s[64:65]\n" \
"v_cndmask_b32_e64 v64, v64, v65, s[64:65]\n" \
"v_cndmask_b32_e64 v242, v242, v243, s[64:65]\n" \
"v_cndmask_b32_e64 v66, v66, v67, s[64:65]\n" \
"v_cndmask_b32_e64 v192, v32, v192, s[66:67]\n" \
"v_cndmask_b32_e64 v194, v34, v194, s[68:69]\n" \
"v_cndmask_b32_e64 v208, v36, v208, s[66:67]\n" \
"v_cndmask_b32_e64 v210, v38, v210, s[68:69]\n" \
"v_cndmask_b32_e64 v224, v248, v224, s[66:67]\n" \
"v_cndmask_b32_e64 v226, v250, v226, s[68:69]\n" \
"v_cndmask_b32_e64 v240, v64, v240, s[66:67]\n" \
"v_cndmask_b32_e64 v242, v66, v242, s[68:69]\n" \
"v_mfma_f32_16x16x32_bf16 v[160:163], a[252:255], a[216:219], v[160:163]\n" \
"v_max_f32_e32 v193, v192, v194\n" \
"v_max_f32_e32 v209, v208, v210\n" \
"v_max_f32_e32 v225, v224, v226\n" \
"v_max_f32_e32 v241, v240, v242\n" \
"v_mul_f32_e32 v84, s70, v84\n" \
"v_permlane32_swap_b32_e32 v193, v225\n" \
"v_permlane32_swap_b32_e32 v209, v241\n" \
"v_mul_f32_e32 v85, s70, v85\n" \
"v_mul_f32_e32 v92, s70, v92\n" \
"v_mul_f32_e32 v93, s70, v93\n" \
"v_permlane32_swap_b32_e32 v196, v228\n" \
"v_permlane32_swap_b32_e32 v212, v244\n" \
"v_max_f32_e32 v193, v193, v225\n" \
"v_max_f32_e32 v209, v209, v241\n" \
"v_accvgpr_read_b32 v48, a80\n" \
"v_accvgpr_read_b32 v49, a81\n" \
"v_mul_f32_e32 v86, s70, v86\n" \
"v_mul_f32_e32 v87, s70, v87\n" \
"v_cvt_pk_bf16_f32 v84, v84, v85\n" \
"v_mul_f32_e32 v94, s70, v94\n" \
"v_mul_f32_e32 v95, s70, v95\n" \
"v_cvt_pk_bf16_f32 v92, v92, v93\n" \
"v_permlane16_swap_b32_e32 v228, v244\n" \
"v_permlane16_swap_b32_e32 v193, v209\n" \
"v_mfma_f32_16x16x32_bf16 v[176:179], a[188:191], a[156:159], v[176:179]\n" \
"s_mov_b32 s72, 0x3eae1a93\n" \
"v_mul_f32_e32 v88, s70, v88\n" \
"v_mul_f32_e32 v89, s70, v89\n" \
"v_mul_f32_e32 v90, s70, v90\n" \
"v_mul_f32_e32 v91, s70, v91\n" \
"v_cvt_pk_bf16_f32 v85, v86, v87\n" \
"v_mul_f32_e32 v96, s70, v96\n" \
"v_mul_f32_e32 v97, s70, v97\n" \
"v_mul_f32_e32 v98, s70, v98\n" \
"v_mul_f32_e32 v99, s70, v99\n" \
"v_cvt_pk_bf16_f32 v93, v94, v95\n" \
"v_mul_f32_e32 v45, 0xbfb8aa3b, v228\n" \
"v_mfma_f32_16x16x16_bf16 v[48:51], v[182:183], v[46:47], 0\n" \
"v_mfma_f32_16x16x16_bf16 v[196:199], v[190:191], v[46:47], 0\n" \
"v_mfma_f32_16x16x16_bf16 v[212:215], v[206:207], v[46:47], 0\n" \
"v_max_f32_e64 v193, v193, v209\n" \
"v_cvt_pk_bf16_f32 v86, v88, v89\n" \
"v_cvt_pk_bf16_f32 v87, v90, v91\n" \
"v_cvt_pk_bf16_f32 v94, v96, v97\n" \
"v_cvt_pk_bf16_f32 v95, v98, v99\n" \
"v_mfma_f32_16x16x16_bf16 v[228:231], v[222:223], v[46:47], 0\n" \
"v_mul_f32_e32 v193, s72, v193\n" \
"v_mov_b32_e32 v209, v193\n" \
"ds_write_b128 v27, v[84:87] offset:8192\n" \
"v_mfma_f32_16x16x16_bf16 v[244:247], v[238:239], v[46:47], 0\n" \
"v_permlane16_swap_b32_e32 v193, v209\n" \
"s_mov_b32 s71, 0x3e0293ee\n" \
"v_mfma_f32_16x16x32_bf16 v[176:179], a[252:255], a[220:223], v[176:179]\n" \
"v_mov_b32_e32 v225, v193\n" \
"v_mov_b32_e32 v241, v209\n" \
"v_mul_f32_e32 v100, s70, v100\n" \
"v_mul_f32_e64 v101, s70, v101\n" \
"ds_write_b128 v27, v[92:95] offset:9216\n" \
"v_mul_f32_e32 v108, s70, v108\n" \
"v_mul_f32_e32 v109, s70, v109\n" \
"v_pk_mul_f32 v[116:117], v[116:117], s[70:71]\n" \
"v_pk_mul_f32 v[124:125], v[124:125], s[70:71]\n" \
"v_pk_mul_f32 v[132:133], v[132:133], s[70:71]\n" \
"v_pk_mul_f32 v[140:141], v[140:141], s[70:71]\n" \
"v_pk_mul_f32 v[148:149], v[148:149], s[70:71]\n" \
"v_pk_mul_f32 v[156:157], v[156:157], s[70:71]\n" \
"v_permlane32_swap_b32_e32 v193, v225\n" \
"v_permlane32_swap_b32_e32 v209, v241\n" \
"v_mul_f32_e32 v102, s70, v102\n" \
"v_mul_f32_e32 v103, s70, v103\n" \
"v_cvt_pk_bf16_f32 v100, v100, v101\n" \
"v_mul_f32_e32 v110, s70, v110\n" \
"v_mul_f32_e32 v111, s70, v111\n" \
"v_cvt_pk_bf16_f32 v108, v108, v109\n" \
"v_pk_mul_f32 v[118:119], v[118:119], s[70:71]\n" \
"v_cvt_pk_bf16_f32 v116, v116, v117\n" \
"v_pk_mul_f32 v[126:127], v[126:127], s[70:71]\n" \
"v_cvt_pk_bf16_f32 v124, v124, v125\n" \
"v_pk_mul_f32 v[134:135], v[134:135], s[70:71]\n" \
"v_cvt_pk_bf16_f32 v132, v132, v133\n" \
"v_pk_mul_f32 v[142:143], v[142:143], s[70:71]\n" \
"v_cvt_pk_bf16_f32 v140, v140, v141\n" \
"v_pk_mul_f32 v[150:151], v[150:151], s[70:71]\n" \
"v_cvt_pk_bf16_f32 v148, v148, v149\n" \
"v_pk_mul_f32 v[158:159], v[158:159], s[70:71]\n" \
"v_cvt_pk_bf16_f32 v156, v156, v157\n" \
"v_fma_f32 v192, v192, s72, -v193\n" \
"v_fma_f32 v194, v194, s72, -v193\n" \
"v_fma_f32 v208, v208, s72, -v209\n" \
"v_fma_f32 v210, v210, s72, -v209\n" \
"v_mul_f32_e32 v104, s70, v104\n" \
"v_mul_f32_e32 v105, s70, v105\n" \
"v_mul_f32_e32 v106, s70, v106\n" \
"v_mul_f32_e32 v107, s70, v107\n" \
"v_cvt_pk_bf16_f32 v101, v102, v103\n" \
"v_mul_f32_e32 v112, s70, v112\n" \
"v_mul_f32_e32 v113, s70, v113\n" \
"v_mul_f32_e32 v114, s70, v114\n" \
"v_mul_f32_e32 v115, s70, v115\n" \
"v_cvt_pk_bf16_f32 v109, v110, v111\n" \
"v_pk_mul_f32 v[120:121], v[120:121], s[70:71]\n" \
"v_pk_mul_f32 v[122:123], v[122:123], s[70:71]\n" \
"v_cvt_pk_bf16_f32 v117, v118, v119\n" \
"v_pk_mul_f32 v[128:129], v[128:129], s[70:71]\n" \
"v_pk_mul_f32 v[130:131], v[130:131], s[70:71]\n" \
"v_cvt_pk_bf16_f32 v125, v126, v127\n" \
"v_pk_mul_f32 v[136:137], v[136:137], s[70:71]\n" \
"v_pk_mul_f32 v[138:139], v[138:139], s[70:71]\n" \
"v_cvt_pk_bf16_f32 v133, v134, v135\n" \
"v_pk_mul_f32 v[144:145], v[144:145], s[70:71]\n" \
"v_pk_mul_f32 v[146:147], v[146:147], s[70:71]\n" \
"v_cvt_pk_bf16_f32 v141, v142, v143\n" \
"v_pk_mul_f32 v[152:153], v[152:153], s[70:71]\n" \
"v_pk_mul_f32 v[154:155], v[154:155], s[70:71]\n" \
"v_cvt_pk_bf16_f32 v149, v150, v151\n" \
"v_pk_mul_f32 v[160:161], v[160:161], s[70:71]\n" \
"v_pk_mul_f32 v[162:163], v[162:163], s[70:71]\n" \
"v_cvt_pk_bf16_f32 v157, v158, v159\n" \
"v_pk_mul_f32 v[176:177], v[176:177], s[70:71]\n" \
"v_pk_mul_f32 v[178:179], v[178:179], s[70:71]\n" \
"v_exp_f32_e32 v192, v192\n" \
"v_exp_f32_e32 v194, v194\n" \
"v_exp_f32_e32 v208, v208\n" \
"v_exp_f32_e32 v210, v210\n" \
"v_fma_f32 v224, v224, s72, -v225\n" \
"v_fma_f32 v226, v226, s72, -v225\n" \
"v_mfma_f32_16x16x32_bf16 v[172:175], a[184:187], a[156:159], v[172:175]\n" \
"v_cvt_pk_bf16_f32 v102, v104, v105\n" \
"v_cvt_pk_bf16_f32 v103, v106, v107\n" \
"v_cvt_pk_bf16_f32 v110, v112, v113\n" \
"v_cvt_pk_bf16_f32 v111, v114, v115\n" \
"v_cvt_pk_bf16_f32 v118, v120, v121\n" \
"v_cvt_pk_bf16_f32 v119, v122, v123\n" \
"v_cvt_pk_bf16_f32 v126, v128, v129\n" \
"v_cvt_pk_bf16_f32 v127, v130, v131\n" \
"v_cvt_pk_bf16_f32 v134, v136, v137\n" \
"v_cvt_pk_bf16_f32 v135, v138, v139\n" \
"v_cvt_pk_bf16_f32 v142, v144, v145\n" \
"v_cvt_pk_bf16_f32 v143, v146, v147\n" \
"v_cvt_pk_bf16_f32 v150, v152, v153\n" \
"v_cvt_pk_bf16_f32 v151, v154, v155\n" \
"v_cvt_pk_bf16_f32 v158, v160, v161\n" \
"v_cvt_pk_bf16_f32 v159, v162, v163\n" \
"v_exp_f32_e32 v224, v224\n" \
"v_exp_f32_e32 v226, v226\n" \
"v_fma_f32 v240, v240, s72, -v241\n" \
"v_fma_f32 v242, v242, s72, -v241\n" \
"ds_write_b128 v27, v[100:103] offset:12288\n" \
"v_cvt_pk_bf16_f32 v192, v192, v194\n" \
"v_cvt_pk_bf16_f32 v208, v208, v210\n" \
"v_exp_f32_e32 v240, v240\n" \
"v_exp_f32_e32 v242, v242\n" \
"v_cvt_pk_bf16_f32 v68, v48, v49\n" \
"v_cvt_pk_bf16_f32 v69, v50, v51\n" \
"v_cvt_pk_bf16_f32 v70, v196, v197\n" \
"v_cvt_pk_bf16_f32 v71, v198, v199\n" \
"ds_write_b128 v27, v[108:111] offset:13312\n" \
"v_cvt_pk_bf16_f32 v72, v212, v213\n" \
"v_cvt_pk_bf16_f32 v73, v214, v215\n" \
"v_perm_b32 v184, v192, v192, v40\n" \
"v_perm_b32 v185, v192, v192, v41\n" \
"v_perm_b32 v186, v192, v192, v42\n" \
"v_perm_b32 v187, v192, v192, v43\n" \
"v_perm_b32 v200, v208, v208, v40\n" \
"v_perm_b32 v201, v208, v208, v41\n" \
"v_perm_b32 v202, v208, v208, v42\n" \
"v_perm_b32 v203, v208, v208, v43\n" \
"ds_write_b128 v21, v[116:119] offset:16384\n" \
"v_cvt_pk_bf16_f32 v224, v224, v226\n" \
"v_cvt_pk_bf16_f32 v74, v228, v229\n" \
"v_cvt_pk_bf16_f32 v75, v230, v231\n" \
"v_mfma_f32_16x16x32_bf16 v[32:35], v[68:71], v[184:187], 0\n" \
"v_mfma_f32_16x16x32_bf16 v[36:39], v[70:73], v[200:203], 0\n" \
"v_perm_b32 v216, v224, v224, v40\n" \
"v_perm_b32 v217, v224, v224, v41\n" \
"ds_write_b128 v21, v[124:127] offset:17408\n" \
"v_perm_b32 v218, v224, v224, v42\n" \
"v_perm_b32 v219, v224, v224, v43\n" \
"v_cvt_pk_bf16_f32 v240, v240, v242\n" \
"v_mfma_f32_16x16x32_bf16 v[172:175], a[248:251], a[220:223], v[172:175]\n" \
"v_cvt_pk_bf16_f32 v76, v244, v245\n" \
"v_cvt_pk_bf16_f32 v77, v246, v247\n" \
"v_mfma_f32_16x16x32_bf16 v[248:251], v[72:75], v[216:219], 0\n" \
"v_perm_b32 v232, v240, v240, v40\n" \
"v_perm_b32 v233, v240, v240, v41\n" \
"ds_write_b128 v21, v[132:135] offset:20480\n" \
"v_perm_b32 v234, v240, v240, v42\n" \
"v_perm_b32 v235, v240, v240, v43\n" \
"v_pk_mul_f32 v[172:173], v[172:173], s[70:71]\n" \
"v_pk_mul_f32 v[174:175], v[174:175], s[70:71]\n" \
"v_cvt_pk_bf16_f32 v172, v172, v173\n" \
"v_cvt_pk_bf16_f32 v173, v174, v175\n" \
"v_cvt_pk_bf16_f32 v174, v176, v177\n" \
"v_cvt_pk_bf16_f32 v175, v178, v179\n" \
"v_mfma_f32_16x16x32_bf16 v[64:67], v[74:77], v[232:235], 0\n" \
"ds_write_b128 v21, v[140:143] offset:21504\n" \
"v_mfma_f32_16x16x32_bf16 v[168:171], a[180:183], a[156:159], v[168:171]\n" \
"v_cvt_pk_bf16_f32 v52, v252, v253\n" \
"v_cvt_pk_bf16_f32 v53, v254, v255\n" \
"v_mfma_f32_16x16x32_bf16 v[168:171], a[244:247], a[220:223], v[168:171]\n" \
"v_exp_f32_e64 v45, v45\n" \
"v_mfma_f32_16x16x32_bf16 v[192:195], v[52:55], v[184:187], 0\n" \
"ds_write_b128 v21, v[148:151] offset:24576\n" \
"v_mfma_f32_16x16x32_bf16 v[208:211], v[54:57], v[200:203], 0\n" \
"v_mfma_f32_16x16x32_bf16 v[224:227], v[56:59], v[216:219], 0\n" \
"v_mfma_f32_16x16x32_bf16 v[240:243], v[58:61], v[232:235], 0\n" \
"ds_write_b128 v21, v[156:159] offset:25600\n" \
"v_mov_b32_dpp v34, v38 quad_perm:[0,1,2,3] row_mask:0x2 bank_mask:0xf\n" \
"v_pk_mul_f32 v[168:169], v[168:169], s[70:71]\n" \
"v_pk_mul_f32 v[170:171], v[170:171], s[70:71]\n" \
"v_mov_b32_dpp v34, v250 quad_perm:[0,1,2,3] row_mask:0x4 bank_mask:0xf\n" \
"v_add_f32_e64 v45, 1.0, v45\n" \
"v_mfma_f32_16x16x32_bf16 v[164:167], a[176:179], a[156:159], v[164:167]\n" \
"v_mov_b32_dpp v34, v66 quad_perm:[0,1,2,3] row_mask:0x8 bank_mask:0xf\n" \
"v_mov_b32_dpp v32, v36 quad_perm:[0,1,2,3] row_mask:0x2 bank_mask:0xf\n" \
"v_rcp_f32_e64 v45, v45\n" \
"ds_write_b128 v21, v[172:175] offset:29696\n" \
"v_mov_b32_dpp v33, v37 quad_perm:[0,1,2,3] row_mask:0x2 bank_mask:0xf\n" \
"v_rcp_f32_e64 v34, v34\n" \
"v_mov_b32_dpp v32, v248 quad_perm:[0,1,2,3] row_mask:0x4 bank_mask:0xf\n" \
"v_mov_b32_dpp v33, v249 quad_perm:[0,1,2,3] row_mask:0x4 bank_mask:0xf\n" \
"v_mul_f32_e64 v34, v45, v34\n" \
"v_mov_b32_dpp v32, v64 quad_perm:[0,1,2,3] row_mask:0x8 bank_mask:0xf\n" \
"v_mov_b32_dpp v33, v65 quad_perm:[0,1,2,3] row_mask:0x8 bank_mask:0xf\n" \
"v_mul_f32_e32 v32, v34, v32\n" \
"v_mul_f32_e32 v33, v34, v33\n" \
"v_mov_b32_e64 v38, v34\n" \
"v_mfma_f32_16x16x32_bf16 v[164:167], a[240:243], a[220:223], v[164:167]\n" \
"v_sub_f32_e32 v44, 1.0, v45\n" \
"v_permlane16_swap_b32_e32 v34, v38\n" \
"v_mov_b32_e32 v250, v34\n" \
"v_mov_b32_e32 v66, v38\n" \
"v_cvt_pk_bf16_f32 v32, v32, v33\n" \
"v_permlane32_swap_b32_e32 v34, v250\n" \
"v_permlane32_swap_b32_e32 v38, v66\n" \
"v_mul_f32_e64 v192, v34, v192\n" \
"v_pk_mul_f32 v[164:165], v[164:165], s[70:71]\n" \
"v_pk_mul_f32 v[166:167], v[166:167], s[70:71]\n" \
"v_cvt_pk_bf16_f32 v164, v164, v165\n" \
"v_cvt_pk_bf16_f32 v165, v166, v167\n" \
"v_cvt_pk_bf16_f32 v166, v168, v169\n" \
"v_cvt_pk_bf16_f32 v167, v170, v171\n" \
"ds_write_b128 v21, v[164:167] offset:28672\n" \
"v_mul_f32_e32 v193, v34, v193\n" \
"v_mul_f32_e32 v208, v38, v208\n" \
"v_mul_f32_e32 v209, v38, v209\n" \
"v_mul_f32_e32 v224, v250, v224\n" \
"v_mul_f32_e32 v225, v250, v225\n" \
"v_mul_f32_e32 v240, v66, v240\n" \
"v_mul_f32_e32 v241, v66, v241\n" \
"v_mul_f32_e32 v194, v34, v194\n" \
"v_mul_f32_e64 v195, v34, v195\n" \
"v_cvt_pk_bf16_f32 v192, v192, v193\n" \
"v_mul_f32_e32 v210, v38, v210\n" \
"v_mul_f32_e32 v211, v38, v211\n" \
"v_cvt_pk_bf16_f32 v208, v208, v209\n" \
"v_mul_f32_e32 v226, v250, v226\n" \
"v_mul_f32_e32 v227, v250, v227\n" \
"v_cvt_pk_bf16_f32 v224, v224, v225\n" \
"v_mul_f32_e32 v242, v66, v242\n" \
"v_mul_f32_e32 v243, v66, v243\n" \
"v_cvt_pk_bf16_f32 v240, v240, v241\n" \
"v_cvt_pk_bf16_f32 v193, v194, v195\n" \
"s_add_u32 s84, s48, 0x0\n" \
"s_nop 0\n" \
"v_cvt_pk_bf16_f32 v209, v210, v211\n" \
"s_add_u32 s85, s48, 0x24800\n" \
"v_cvt_pk_bf16_f32 v225, v226, v227\n" \
"s_add_u32 s86, s48, 0x49000\n" \
"v_cvt_pk_bf16_f32 v241, v242, v243\n" \
"s_add_u32 s87, s48, 0x6d800\n" \
"ds_write_b32 v26, v44\n" \
"buffer_store_dword v32, v25, s[80:83], s49 offen sc1\n" \
"buffer_store_dwordx2 v[192:193], v24, s[80:83], s84 offen sc1\n" \
"buffer_store_dwordx2 v[208:209], v24, s[80:83], s85 offen sc1\n" \
"buffer_store_dwordx2 v[224:225], v24, s[80:83], s86 offen sc1\n" \
"buffer_store_dwordx2 v[240:241], v24, s[80:83], s87 offen sc1\n" \
"s_branch L_end_%=\n" \
"L_poll60_%=:\n" \
"s_mov_b32 s84, 0\n" \
"L_pl60_%=:\n" \
"s_sleep 1\n" \
"v_bfe_u32 v44, v20, 2, 5\n" \
"buffer_load_dword v44, v44, s[80:83], s38 offen offset:128 sc1\n" \
"s_waitcnt vmcnt(0)\n" \
"v_cmp_ne_u32_e32 vcc, s62, v44\n" \
"s_add_u32 s84, s84, 1\n" \
"s_nop 3\n" \
"s_cbranch_vccz L_pollr60_%=\n" \
"s_cmp_gt_u32 s84, 0x400000\n" \
"s_cbranch_scc0 L_pl60_%=\n" \
"s_branch L_pollr60_%=\n" \
"L_poll160_%=:\n" \
"s_mov_b32 s84, 0\n" \
"L_pl160_%=:\n" \
"s_sleep 1\n" \
"v_bfe_u32 v44, v20, 2, 5\n" \
"buffer_load_dword v44, v44, s[80:83], s38 offen offset:0 sc1\n" \
"s_waitcnt vmcnt(0)\n" \
"v_cmp_ne_u32_e32 vcc, s62, v44\n" \
"s_add_u32 s84, s84, 1\n" \
"s_nop 3\n" \
"s_cbranch_vccz L_pollr160_%=\n" \
"s_cmp_gt_u32 s84, 0x400000\n" \
"s_cbranch_scc0 L_pl160_%=\n" \
"s_branch L_pollr160_%=\n" \
".fill (L_pb + 42476 - .) / 4, 4, 0xBF800000\n" \
"L_w1_%=:\n" \
"s_waitcnt vmcnt(18)\n" \
"s_barrier\n" \
"s_nop 0\n" \
"ds_read_b64_tr_b16 a[160:161], v32 offset:0\n" \
"ds_read_b64_tr_b16 a[162:163], v32 offset:1024\n" \
"ds_read_b128 a[128:131], v28 offset:16384\n" \
"ds_read_b64_tr_b16 a[224:225], v32 offset:8192\n" \
"ds_read_b64_tr_b16 a[226:227], v32 offset:9216\n" \
"ds_read_b128 a[192:195], v29 offset:16384\n" \
"ds_read_b128 a[132:135], v28 offset:18432\n" \
"ds_read_b128 a[196:199], v29 offset:18432\n" \
"ds_read_b64_tr_b16 a[164:165], v33 offset:0\n" \
"ds_read_b64_tr_b16 a[166:167], v33 offset:1024\n" \
"ds_read_b64_tr_b16 a[228:229], v33 offset:8192\n" \
"ds_read_b64_tr_b16 a[230:231], v33 offset:9216\n" \
"ds_read_b128 a[136:139], v28 offset:20480\n" \
"ds_read_b128 a[200:203], v29 offset:20480\n" \
"s_waitcnt lgkmcnt(11)\n" \
"v_mfma_f32_16x16x32_bf16 a[0:3], a[128:131], a[160:163], 0\n" \
"ds_read_b64_tr_b16 a[168:169], v34 offset:0\n" \
"v_or_b32_e32 v216, 0x100, v215\n" \
"s_waitcnt lgkmcnt(9)\n" \
"v_mfma_f32_16x16x32_bf16 a[0:3], a[192:195], a[224:227], a[0:3]\n" \
"ds_read_b64_tr_b16 a[170:171], v34 offset:1024\n" \
"v_mul_u32_u24_e32 v217, 0x2aab, v216\n" \
"s_waitcnt lgkmcnt(9)\n" \
"v_mfma_f32_16x16x32_bf16 a[16:19], a[132:135], a[160:163], 0\n" \
"ds_read_b64_tr_b16 a[232:233], v34 offset:8192\n" \
"v_add_u32_e32 v215, 0xffffff50, v215\n" \
"s_waitcnt lgkmcnt(9)\n" \
"v_mfma_f32_16x16x32_bf16 a[16:19], a[196:199], a[224:227], a[16:19]\n" \
"ds_read_b64_tr_b16 a[234:235], v34 offset:9216\n" \
"v_lshrrev_b32_e32 v217, 16, v217\n" \
"s_waitcnt lgkmcnt(8)\n" \
"v_mfma_f32_16x16x32_bf16 a[20:23], a[132:135], a[164:167], 0\n" \
"ds_read_b128 a[140:143], v28 offset:22528\n" \
"v_mov_b32_e32 v220, v215\n" \
"s_waitcnt lgkmcnt(7)\n" \
"v_mfma_f32_16x16x32_bf16 a[20:23], a[196:199], a[228:231], a[20:23]\n" \
"ds_read_b128 a[204:207], v29 offset:22528\n" \
"v_mad_i32_i24 v218, v217, -6, v216\n" \
"v_mfma_f32_16x16x32_bf16 a[4:7], a[128:131], a[164:167], 0\n" \
"ds_read_b64_tr_b16 a[172:173], v35 offset:0\n" \
"v_cmp_gt_u32_e64 vcc, s36, v216\n" \
"v_mfma_f32_16x16x32_bf16 a[4:7], a[192:195], a[228:231], a[4:7]\n" \
"ds_read_b64_tr_b16 a[174:175], v35 offset:1024\n" \
"v_mov_b32_e32 v221, 7\n" \
"s_waitcnt lgkmcnt(9)\n" \
"v_mfma_f32_16x16x32_bf16 a[32:35], a[136:139], a[160:163], 0\n" \
"ds_read_b64_tr_b16 a[236:237], v35 offset:8192\n" \
"v_lshrrev_b32_e32 v222, 1, v218\n" \
"s_waitcnt lgkmcnt(9)\n" \
"v_mfma_f32_16x16x32_bf16 a[32:35], a[200:203], a[224:227], a[32:35]\n" \
"ds_read_b64_tr_b16 a[238:239], v35 offset:9216\n" \
"v_cndmask_b32_e64 v217, v220, v217, vcc\n" \
"v_mfma_f32_16x16x32_bf16 a[36:39], a[136:139], a[164:167], 0\n" \
"ds_read_b128 a[144:147], v28 offset:0\n" \
"s_movk_i32 s4, 0x48\n" \
"v_mfma_f32_16x16x32_bf16 a[36:39], a[200:203], a[228:231], a[36:39]\n" \
"ds_read_b128 a[208:211], v29 offset:0\n" \
"v_add_u32_e32 v220, s35, v217\n" \
"s_waitcnt lgkmcnt(10)\n" \
"v_mfma_f32_16x16x32_bf16 a[40:43], a[136:139], a[168:171], 0\n" \
"ds_read_b128 a[148:151], v28 offset:2048\n" \
"v_mul_u32_u24_e32 v222, 0x480, v222\n" \
"s_waitcnt lgkmcnt(9)\n" \
"v_mfma_f32_16x16x32_bf16 a[40:43], a[200:203], a[232:235], a[40:43]\n" \
"ds_read_b128 a[212:215], v29 offset:2048\n" \
"v_mad_u32_u24 v222, v220, s5, v222\n" \
"v_mfma_f32_16x16x32_bf16 a[24:27], a[132:135], a[168:171], 0\n" \
"ds_read_b128 a[152:155], v28 offset:4096\n" \
"v_cmp_gt_u32_e64 s[4:5], s4, v215\n" \
"v_mfma_f32_16x16x32_bf16 a[24:27], a[196:199], a[232:235], a[24:27]\n" \
"ds_read_b128 a[216:219], v29 offset:4096\n" \
"v_cndmask_b32_e64 v215, v221, v218, vcc\n" \
"v_mfma_f32_16x16x32_bf16 a[8:11], a[128:131], a[168:171], 0\n" \
"ds_read_b128 a[156:159], v28 offset:6144\n" \
"v_lshrrev_b32_e64 v218, 1, v220\n" \
"v_mfma_f32_16x16x32_bf16 a[8:11], a[192:195], a[232:235], a[8:11]\n" \
"ds_read_b128 a[220:223], v29 offset:6144\n" \
"s_mov_b32 m0, s32\n" \
"s_waitcnt lgkmcnt(13)\n" \
"v_mfma_f32_16x16x32_bf16 a[48:51], a[140:143], a[160:163], 0\n" \
"s_mov_b32 exec_lo, 0xff000000\n" \
"s_mov_b32 exec_hi, 0x0\n" \
"global_load_lds_dword v[184:185], off\n" \
"s_mov_b64 exec, -1\n" \
"s_waitcnt lgkmcnt(12)\n" \
"v_mfma_f32_16x16x32_bf16 a[48:51], a[204:207], a[224:227], a[48:51]\n" \
"ds_read_b64_tr_b16 a[176:177], v36 offset:0\n" \
"v_or_b32_e32 v214, v222, v214\n" \
"v_mfma_f32_16x16x32_bf16 a[52:55], a[140:143], a[164:167], 0\n" \
"ds_read_b64_tr_b16 a[178:179], v36 offset:1024\n" \
"v_mov_b32_e64 v222, 2.0\n" \
"v_mfma_f32_16x16x32_bf16 a[52:55], a[204:207], a[228:231], a[52:55]\n" \
"s_mov_b32 m0, s32\n" \
"ds_read_b64_tr_b16 a[240:241], v36 offset:8192\n" \
"v_mfma_f32_16x16x32_bf16 a[56:59], a[140:143], a[168:171], 0\n" \
"s_mov_b32 exec_lo, 0xffff\n" \
"s_mov_b32 exec_hi, 0x0\n" \
"global_load_lds_dword v[186:187], off\n" \
"s_mov_b64 exec, -1\n" \
"v_mfma_f32_16x16x32_bf16 a[56:59], a[204:207], a[232:235], a[56:59]\n" \
"ds_read_b64_tr_b16 a[242:243], v36 offset:9216\n" \
"v_and_b32_e32 v218, 4, v218\n" \
"s_waitcnt lgkmcnt(14)\n" \
"v_mfma_f32_16x16x32_bf16 a[60:63], a[140:143], a[172:175], 0\n" \
"ds_read_b64_tr_b16 a[180:181], v37 offset:0\n" \
"v_and_b32_e32 v217, 2, v217\n" \
"s_waitcnt lgkmcnt(13)\n" \
"v_mfma_f32_16x16x32_bf16 a[60:63], a[204:207], a[236:239], a[60:63]\n" \
"ds_read_b64_tr_b16 a[182:183], v37 offset:1024\n" \
"v_bitop3_b32 v215, v215, v218, v217 bitop3:0x1e\n" \
"v_mfma_f32_16x16x32_bf16 a[44:47], a[136:139], a[172:175], 0\n" \
"ds_read_b64_tr_b16 a[244:245], v37 offset:8192\n" \
"v_lshlrev_b32_e32 v223, 1, v220\n" \
"v_mfma_f32_16x16x32_bf16 a[44:47], a[200:203], a[236:239], a[44:47]\n" \
"ds_read_b64_tr_b16 a[246:247], v37 offset:9216\n" \
"v_mov_b32_e64 v224, 2.0\n" \
"v_mfma_f32_16x16x32_bf16 a[28:31], a[132:135], a[172:175], 0\n" \
"ds_read_b64_tr_b16 a[184:185], v38 offset:0\n" \
"v_cndmask_b32_e64 v193, v222, v214, vcc\n" \
"v_mfma_f32_16x16x32_bf16 a[28:31], a[196:199], a[236:239], a[28:31]\n" \
"ds_read_b64_tr_b16 a[186:187], v38 offset:1024\n" \
"v_lshlrev_b32_e64 v214, 7, v220\n" \
"v_mfma_f32_16x16x32_bf16 a[12:15], a[128:131], a[172:175], 0\n" \
"v_lshlrev_b32_e32 v215, 4, v215\n" \
"s_mul_i32 s39, s3, 18\n" \
"v_mfma_f32_16x16x32_bf16 a[12:15], a[192:195], a[236:239], a[12:15]\n" \
"v_add3_u32 v214, v214, s34, v215\n" \
"v_cndmask_b32_e64 v195, v224, v223, s[4:5]\n" \
"s_waitcnt lgkmcnt(15)\n" \
"v_mfma_f32_16x16x32_bf16 a[76:79], a[144:147], a[172:175], 0\n" \
"ds_read_b64_tr_b16 a[248:249], v38 offset:8192\n" \
"s_movk_i32 s4, 0x1f8\n" \
"v_mfma_f32_16x16x32_bf16 a[76:79], a[208:211], a[236:239], a[76:79]\n" \
"v_mov_b32_e32 v215, v213\n" \
"s_add_i32 s35, s35, s39\n" \
"v_mfma_f32_16x16x32_bf16 a[72:75], a[144:147], a[168:171], 0\n" \
"v_cmp_gt_u32_e32 vcc, s4, v216\n" \
"v_add_u32_e32 v212, s35, v212\n" \
"v_mfma_f32_16x16x32_bf16 a[72:75], a[208:211], a[232:235], a[72:75]\n" \
"ds_read_b64_tr_b16 a[250:251], v38 offset:9216\n" \
"v_mov_b32_e64 v219, 2.0\n" \
"v_mfma_f32_16x16x32_bf16 a[68:71], a[144:147], a[164:167], 0\n" \
"v_cndmask_b32_e32 v197, v215, v214, vcc\n" \
"s_movk_i32 s35, 0x120\n" \
"v_mfma_f32_16x16x32_bf16 a[68:71], a[208:211], a[228:231], a[68:71]\n" \
"v_mul_u32_u24_e32 v214, 0xd80, v212\n" \
"v_cmp_gt_u32_e64 vcc, s35, v20\n" \
"v_mfma_f32_16x16x32_bf16 a[64:67], a[144:147], a[160:163], 0\n" \
"v_lshrrev_b32_e32 v215, 1, v212\n" \
"s_and_b32 s4, s2, 7\n" \
"v_mfma_f32_16x16x32_bf16 a[64:67], a[208:211], a[224:227], a[64:67]\n" \
"ds_read_b64_tr_b16 a[188:189], v39 offset:0\n" \
"v_cndmask_b32_e64 v194, v219, v214, vcc\n" \
"v_mfma_f32_16x16x32_bf16 a[80:83], a[148:151], a[160:163], 0\n" \
"v_lshlrev_b32_e32 v214, 7, v212\n" \
"v_lshrrev_b32_e32 v212, 2, v212\n" \
"s_waitcnt lgkmcnt(15)\n" \
"v_mfma_f32_16x16x32_bf16 a[80:83], a[212:215], a[224:227], a[80:83]\n" \
"v_and_b32_e32 v212, 2, v212\n" \
"v_and_or_b32 v212, v215, 1, v212\n" \
"v_mfma_f32_16x16x32_bf16 a[84:87], a[148:151], a[164:167], 0\n" \
"ds_read_b64_tr_b16 a[190:191], v39 offset:1024\n" \
"s_lshl_b32 s5, s4, 2\n" \
"v_mfma_f32_16x16x32_bf16 a[84:87], a[212:215], a[228:231], a[84:87]\n" \
"s_bfe_u32 s38, s2, 0x20003\n" \
"v_lshlrev_b32_e32 v212, 5, v212\n" \
"v_mfma_f32_16x16x32_bf16 a[88:91], a[148:151], a[168:171], 0\n" \
"s_movk_i32 s35, 0x60\n" \
"s_or_b32 s5, s5, s38\n" \
"v_mfma_f32_16x16x32_bf16 a[88:91], a[212:215], a[232:235], a[88:91]\n" \
"ds_read_b64_tr_b16 a[252:253], v39 offset:8192\n" \
"v_bitop3_b32 v212, v212, s35, v214 bitop3:0x36\n" \
"v_mfma_f32_16x16x32_bf16 a[92:95], a[148:151], a[172:175], 0\n" \
"s_lshr_b32 s36, s2, 3\n" \
"v_add_u32_e32 v212, s34, v212\n" \
"v_mfma_f32_16x16x32_bf16 a[92:95], a[212:215], a[236:239], a[92:95]\n" \
"s_mul_i32 s34, s5, 0x12000\n" \
"s_or_b32 s40, s34, 0x6000000\n" \
"v_mfma_f32_16x16x32_bf16 a[108:111], a[152:155], a[172:175], 0\n" \
"ds_read_b64_tr_b16 a[254:255], v39 offset:9216\n" \
"s_mul_i32 s34, s36, 0x90000\n" \
"v_mfma_f32_16x16x32_bf16 a[108:111], a[216:219], a[236:239], a[108:111]\n" \
"s_lshl_b32 s36, s3, 14\n" \
"s_add_i32 s34, s34, s36\n" \
"v_mfma_f32_16x16x32_bf16 a[104:107], a[152:155], a[168:171], 0\n" \
"s_lshl_b32 s38, s5, 8\n" \
"s_add_i32 s41, s34, 0x4240000\n" \
"v_mfma_f32_16x16x32_bf16 a[104:107], a[216:219], a[232:235], a[104:107]\n" \
"s_mul_i32 s34, s5, 36\n" \
"s_add_i32 s5, s7, s40\n" \
"v_mfma_f32_16x16x32_bf16 a[100:103], a[152:155], a[164:167], 0\n" \
"s_addk_i32 s5, 0x2000\n" \
"s_mov_b32 s37, 2.0\n" \
"v_mfma_f32_16x16x32_bf16 a[100:103], a[216:219], a[228:231], a[100:103]\n" \
"s_or_b32 s38, s38, 0x5be8000\n" \
"v_mfma_f32_16x16x32_bf16 a[96:99], a[152:155], a[160:163], 0\n" \
"s_cmp_lg_u32 s3, 0\n" \
"s_cselect_b32 s36, s37, s5\n" \
"v_mfma_f32_16x16x32_bf16 a[96:99], a[216:219], a[224:227], a[96:99]\n" \
"s_lshl_b32 s5, s6, 2\n" \
"s_or_b32 s5, s38, s5\n" \
"s_waitcnt lgkmcnt(15)\n" \
"v_mfma_f32_16x16x32_bf16 a[112:115], a[156:159], a[160:163], 0\n" \
"s_lshl_b32 s35, s3, 10\n" \
"v_mfma_f32_16x16x32_bf16 a[112:115], a[220:223], a[224:227], a[112:115]\n" \
"s_cmpk_lt_u32 s3, 3\n" \
"s_cselect_b32 s37, s37, s5\n" \
"v_mfma_f32_16x16x32_bf16 a[116:119], a[156:159], a[164:167], 0\n" \
"s_lshl_b32 s5, s4, 16\n" \
"s_lshl_b32 s4, s4, 11\n" \
"v_mfma_f32_16x16x32_bf16 a[116:119], a[220:223], a[228:231], a[116:119]\n" \
"v_cndmask_b32_e32 v198, v213, v212, vcc\n" \
"v_or_b32_e64 v212, s35, v20\n" \
"v_mfma_f32_16x16x32_bf16 a[120:123], a[156:159], a[168:171], 0\n" \
"s_or_b32 s39, s40, s35\n" \
"s_or_b32 s55, s41, s4\n" \
"v_mfma_f32_16x16x32_bf16 a[120:123], a[220:223], a[232:235], a[120:123]\n" \
"v_or_b32_e32 v199, 0x22000, v212\n" \
"s_add_i32 s35, s39, s7\n" \
"v_mfma_f32_16x16x32_bf16 a[124:127], a[156:159], a[172:175], 0\n" \
"s_add_i32 s54, s41, s5\n" \
"s_add_i32 s55, s55, 0x80000\n" \
"v_mfma_f32_16x16x32_bf16 a[124:127], a[220:223], a[236:239], a[124:127]\n" \
"s_waitcnt vmcnt(4) lgkmcnt(0)\n" \
"s_barrier\n" \
"v_mfma_f32_16x16x32_bf16 v[52:55], a[128:131], a[176:179], 0\n" \
"s_add_u32 m0, s30, 0x0\n" \
"s_movk_i32 s84, 0x100\n" \
"v_mfma_f32_16x16x32_bf16 v[52:55], a[192:195], a[240:243], v[52:55]\n" \
"buffer_load_dwordx4 v22, s[64:67], s84 offen offset:0 lds\n" \
"v_mfma_f32_16x16x32_bf16 v[56:59], a[128:131], a[180:183], 0\n" \
"ds_read_b64_tr_b16 a[160:161], v32 offset:32768\n" \
"s_lshr_b32 s7, s2, 3\n" \
"v_mfma_f32_16x16x32_bf16 v[56:59], a[192:195], a[244:247], v[56:59]\n" \
"s_movk_i32 s84, 0x1d00\n" \
"v_mfma_f32_16x16x32_bf16 v[60:63], a[128:131], a[184:187], 0\n" \
"buffer_load_dwordx4 v23, s[64:67], s84 offen offset:1024 lds\n" \
"v_mfma_f32_16x16x32_bf16 v[60:63], a[192:195], a[248:251], v[60:63]\n" \
"ds_read_b64_tr_b16 a[162:163], v32 offset:33792\n" \
"s_and_b32 s6, s2, 7\n" \
"v_mfma_f32_16x16x32_bf16 v[64:67], a[128:131], a[188:191], 0\n" \
"ds_read_b64_tr_b16 a[224:225], v32 offset:40960\n" \
"s_lshl_b32 s4, s7, 2\n" \
"v_mfma_f32_16x16x32_bf16 v[64:67], a[192:195], a[252:255], v[64:67]\n" \
"ds_read_b128 a[128:131], v28 offset:49152\n" \
"s_lshr_b32 s5, s2, 2\n" \
"v_mfma_f32_16x16x32_bf16 v[80:83], a[132:135], a[188:191], 0\n" \
"ds_read_b128 a[192:195], v29 offset:49152\n" \
"s_or_b32 s4, s4, s3\n" \
"v_mfma_f32_16x16x32_bf16 v[80:83], a[196:199], a[252:255], v[80:83]\n" \
"s_movk_i32 s84, 0x3900\n" \
"v_mfma_f32_16x16x32_bf16 v[76:79], a[132:135], a[184:187], 0\n" \
"buffer_load_dwordx4 v22, s[64:67], s84 offen offset:2048 lds\n" \
"v_mfma_f32_16x16x32_bf16 v[76:79], a[196:199], a[248:251], v[76:79]\n" \
"s_movk_i32 s84, 0x5500\n" \
"v_mfma_f32_16x16x32_bf16 v[72:75], a[132:135], a[180:183], 0\n" \
"buffer_load_dwordx4 v23, s[64:67], s84 offen offset:3072 lds\n" \
"v_mfma_f32_16x16x32_bf16 v[72:75], a[196:199], a[244:247], v[72:75]\n" \
"s_add_u32 m0, s31, 0x0\n" \
"s_mov_b32 s84, 0x100000\n" \
"v_mfma_f32_16x16x32_bf16 v[68:71], a[132:135], a[176:179], 0\n" \
"buffer_load_dwordx4 v26, s[68:71], s84 offen offset:0 lds\n" \
"v_mfma_f32_16x16x32_bf16 v[68:71], a[196:199], a[240:243], v[68:71]\n" \
"ds_read_b128 a[132:135], v28 offset:51200\n" \
"v_lshrrev_b32_e32 v212, 4, v20\n" \
"v_mfma_f32_16x16x32_bf16 v[84:87], a[136:139], a[176:179], 0\n" \
"ds_read_b128 a[196:199], v29 offset:51200\n" \
"s_lshl_b32 s41, s6, 3\n" \
"v_mfma_f32_16x16x32_bf16 v[84:87], a[200:203], a[240:243], v[84:87]\n" \
"s_addk_i32 s84, 0x7c00\n" \
"v_mfma_f32_16x16x32_bf16 v[88:91], a[136:139], a[180:183], 0\n" \
"buffer_load_dwordx4 v26, s[68:71], s84 offen offset:1024 lds\n" \
"v_mfma_f32_16x16x32_bf16 v[88:91], a[200:203], a[244:247], v[88:91]\n" \
"ds_read_b64_tr_b16 a[226:227], v32 offset:41984\n" \
"s_and_b32 s5, s5, 6\n" \
"v_mfma_f32_16x16x32_bf16 v[92:95], a[136:139], a[184:187], 0\n" \
"s_addk_i32 s84, 0x7c00\n" \
"v_mfma_f32_16x16x32_bf16 v[92:95], a[200:203], a[248:251], v[92:95]\n" \
"buffer_load_dwordx4 v27, s[68:71], s84 offen offset:2048 lds\n" \
"v_mfma_f32_16x16x32_bf16 v[96:99], a[136:139], a[188:191], 0\n" \
"ds_read_b64_tr_b16 a[164:165], v33 offset:32768\n" \
"s_lshr_b32 s40, s2, 5\n" \
"v_mfma_f32_16x16x32_bf16 v[96:99], a[200:203], a[252:255], v[96:99]\n" \
"ds_read_b128 a[136:139], v28 offset:53248\n" \
"s_or_b32 s42, s41, s5\n" \
"v_mfma_f32_16x16x32_bf16 v[112:115], a[140:143], a[188:191], 0\n" \
"ds_read_b128 a[200:203], v29 offset:53248\n" \
"v_mad_u64_u32 v[214:215], s[4:5], s4, 36, v[212:213]\n" \
"v_mfma_f32_16x16x32_bf16 v[112:115], a[204:207], a[252:255], v[112:115]\n" \
"s_addk_i32 s84, 0x7c00\n" \
"v_mfma_f32_16x16x32_bf16 v[108:111], a[140:143], a[184:187], 0\n" \
"buffer_load_dwordx4 v27, s[68:71], s84 offen offset:3072 lds\n" \
"v_mfma_f32_16x16x32_bf16 v[108:111], a[204:207], a[248:251], v[108:111]\n" \
"s_add_u32 m0, s30, 0x1000\n" \
"s_mov_b32 s84, 0x8100\n" \
"v_mfma_f32_16x16x32_bf16 v[104:107], a[140:143], a[180:183], 0\n" \
"buffer_load_dwordx4 v22, s[64:67], s84 offen offset:0 lds\n" \
"v_mfma_f32_16x16x32_bf16 v[104:107], a[204:207], a[244:247], v[104:107]\n" \
"ds_read_b64_tr_b16 a[166:167], v33 offset:33792\n" \
"s_lshl_b32 s4, s40, 6\n" \
"v_mfma_f32_16x16x32_bf16 v[100:103], a[140:143], a[176:179], 0\n" \
"ds_read_b64_tr_b16 a[228:229], v33 offset:40960\n" \
"s_lshl_b32 s5, s3, 1\n" \
"v_mfma_f32_16x16x32_bf16 v[100:103], a[204:207], a[240:243], v[100:103]\n" \
"ds_read_b128 a[140:143], v28 offset:55296\n" \
"s_or_b32 s4, s4, s5\n" \
"v_mfma_f32_16x16x32_bf16 v[116:119], a[144:147], a[176:179], 0\n" \
"ds_read_b128 a[204:207], v29 offset:55296\n" \
"s_lshl_b32 s5, s2, 2\n" \
"v_mfma_f32_16x16x32_bf16 v[116:119], a[208:211], a[240:243], v[116:119]\n" \
"s_addk_i32 s84, 0x1c00\n" \
"v_mfma_f32_16x16x32_bf16 v[120:123], a[144:147], a[180:183], 0\n" \
"buffer_load_dwordx4 v23, s[64:67], s84 offen offset:1024 lds\n" \
"v_mfma_f32_16x16x32_bf16 v[120:123], a[208:211], a[244:247], v[120:123]\n" \
"s_addk_i32 s84, 0x1c00\n" \
"v_mfma_f32_16x16x32_bf16 v[124:127], a[144:147], a[184:187], 0\n" \
"buffer_load_dwordx4 v22, s[64:67], s84 offen offset:2048 lds\n" \
"v_mfma_f32_16x16x32_bf16 v[124:127], a[208:211], a[248:251], v[124:127]\n" \
"s_addk_i32 s84, 0x1c00\n" \
"v_mfma_f32_16x16x32_bf16 v[128:131], a[144:147], a[188:191], 0\n" \
"buffer_load_dwordx4 v23, s[64:67], s84 offen offset:3072 lds\n" \
"v_mfma_f32_16x16x32_bf16 v[128:131], a[208:211], a[252:255], v[128:131]\n" \
"ds_read_b128 a[144:147], v28 offset:32768\n" \
"s_or_b32 s43, s5, s3\n" \
"v_mfma_f32_16x16x32_bf16 v[144:147], a[148:151], a[188:191], 0\n" \
"ds_read_b128 a[208:211], v29 offset:32768\n" \
"s_or_b32 s4, s4, s41\n" \
"v_mfma_f32_16x16x32_bf16 v[144:147], a[212:215], a[252:255], v[144:147]\n" \
"s_add_u32 m0, s31, 0x4000\n" \
"s_mov_b32 s84, 0x100000\n" \
"v_mfma_f32_16x16x32_bf16 v[140:143], a[148:151], a[184:187], 0\n" \
"buffer_load_dwordx4 v26, s[72:75], s84 offen offset:0 lds\n" \
"v_mfma_f32_16x16x32_bf16 v[140:143], a[212:215], a[248:251], v[140:143]\n" \
"ds_read_b64_tr_b16 a[230:231], v33 offset:41984\n" \
"s_lshr_b32 s5, s2, 1\n" \
"v_mfma_f32_16x16x32_bf16 v[136:139], a[148:151], a[180:183], 0\n" \
"s_addk_i32 s84, 0x7c00\n" \
"v_mfma_f32_16x16x32_bf16 v[136:139], a[212:215], a[244:247], v[136:139]\n" \
".fill (L_pb + 44768 - .) / 4, 4, 0xBF800000\n" \
"L_pp11:\n" \
"s_cbranch_execz L_pret\n" \
"buffer_load_dwordx4 v26, s[72:75], s84 offen offset:1024 lds\n" \
"v_mfma_f32_16x16x32_bf16 v[132:135], a[148:151], a[176:179], 0\n" \
"ds_read_b64_tr_b16 a[168:169], v34 offset:32768\n" \
"s_lshl_b32 s41, s43, 1\n" \
"v_mfma_f32_16x16x32_bf16 v[132:135], a[212:215], a[240:243], v[132:135]\n" \
"ds_read_b128 a[148:151], v28 offset:34816\n" \
"s_mulk_i32 s5, 0x490\n" \
"v_mfma_f32_16x16x32_bf16 v[148:151], a[152:155], a[176:179], 0\n" \
"ds_read_b128 a[212:215], v29 offset:34816\n" \
"s_and_b32 s41, s41, 14\n" \
"v_mfma_f32_16x16x32_bf16 v[148:151], a[216:219], a[240:243], v[148:151]\n" \
"ds_read_b64_tr_b16 a[170:171], v34 offset:33792\n" \
"s_or_b32 s5, s5, s41\n" \
"v_mfma_f32_16x16x32_bf16 v[152:155], a[152:155], a[180:183], 0\n" \
"ds_read_b64_tr_b16 a[232:233], v34 offset:40960\n" \
"s_or_b32 s41, s42, 0xb7d00\n" \
"v_mfma_f32_16x16x32_bf16 v[152:155], a[216:219], a[244:247], v[152:155]\n" \
"ds_read_b64_tr_b16 a[234:235], v34 offset:41984\n" \
"v_mov_b32_e32 v220, s41\n" \
"v_mfma_f32_16x16x32_bf16 v[156:159], a[152:155], a[184:187], 0\n" \
"ds_read_b64_tr_b16 a[172:173], v35 offset:32768\n" \
"s_mul_i32 s7, s7, 0x90000\n" \
"v_mfma_f32_16x16x32_bf16 v[156:159], a[216:219], a[248:251], v[156:159]\n" \
"ds_read_b64_tr_b16 a[174:175], v35 offset:33792\n" \
"s_lshl_b32 s41, s3, 14\n" \
"v_mfma_f32_16x16x32_bf16 v[160:163], a[152:155], a[188:191], 0\n" \
"ds_read_b64_tr_b16 a[236:237], v35 offset:40960\n" \
"s_mulk_i32 s40, 0xa00\n" \
"v_mfma_f32_16x16x32_bf16 v[160:163], a[216:219], a[252:255], v[160:163]\n" \
"ds_read_b128 a[152:155], v28 offset:36864\n" \
"s_add_i32 s7, s7, s41\n" \
"v_mfma_f32_16x16x32_bf16 v[176:179], a[156:159], a[188:191], 0\n" \
"ds_read_b128 a[216:219], v29 offset:36864\n" \
"s_lshl_b32 s41, s6, 16\n" \
"v_mfma_f32_16x16x32_bf16 v[176:179], a[220:223], a[252:255], v[176:179]\n" \
"ds_read_b64_tr_b16 a[238:239], v35 offset:41984\n" \
"s_add_i32 s40, s40, 0x5bc1000\n" \
"v_mfma_f32_16x16x32_bf16 v[172:175], a[156:159], a[184:187], 0\n" \
"s_add_i32 s7, s7, s41\n" \
"s_lshr_b32 s40, s40, 7\n" \
"v_mfma_f32_16x16x32_bf16 v[172:175], a[220:223], a[248:251], v[172:175]\n" \
"s_add_i32 s7, s7, 0x4240000\n" \
"v_mov_b32_e64 v219, s40\n" \
"v_mfma_f32_16x16x32_bf16 v[168:171], a[156:159], a[180:183], 0\n" \
"s_movk_i32 s40, 0x3c0\n" \
"s_lshr_b32 s7, s7, 7\n" \
"v_mfma_f32_16x16x32_bf16 v[168:171], a[220:223], a[244:247], v[168:171]\n" \
"v_cmp_gt_u32_e64 vcc, s40, v20\n" \
"s_or_b32 s7, s7, 0x70\n" \
"v_mfma_f32_16x16x32_bf16 v[164:167], a[156:159], a[176:179], 0\n" \
"v_bfe_u32 v216, v20, 4, 1\n" \
"s_add_i32 s5, s5, 0x60080\n" \
"v_mfma_f32_16x16x32_bf16 v[164:167], a[220:223], a[240:243], v[164:167]\n" \
"ds_read_b128 a[156:159], v28 offset:38912\n" \
"ds_read_b128 a[220:223], v29 offset:38912\n" \
"s_waitcnt lgkmcnt(15)\n" \
"v_mfma_f32_16x16x32_bf16 a[0:3], a[128:131], a[160:163], a[0:3]\n" \
"s_addk_i32 s84, 0x7c00\n" \
"v_mfma_f32_16x16x32_bf16 a[0:3], a[192:195], a[224:227], a[0:3]\n" \
"buffer_load_dwordx4 v27, s[72:75], s84 offen offset:2048 lds\n" \
"v_mfma_f32_16x16x32_bf16 a[4:7], a[128:131], a[164:167], a[4:7]\n" \
"ds_read_b64_tr_b16 a[176:177], v36 offset:32768\n" \
"v_cndmask_b32_e32 v219, v220, v219, vcc\n" \
"s_waitcnt lgkmcnt(15)\n" \
"v_mfma_f32_16x16x32_bf16 a[4:7], a[192:195], a[228:231], a[4:7]\n" \
"s_addk_i32 s84, 0x7c00\n" \
"s_waitcnt lgkmcnt(11)\n" \
"v_mfma_f32_16x16x32_bf16 a[8:11], a[128:131], a[168:171], a[8:11]\n" \
"buffer_load_dwordx4 v27, s[72:75], s84 offen offset:3072 lds\n" \
"s_waitcnt lgkmcnt(9)\n" \
"v_mfma_f32_16x16x32_bf16 a[8:11], a[192:195], a[232:235], a[8:11]\n" \
"ds_read_b64_tr_b16 a[178:179], v36 offset:33792\n" \
"s_movk_i32 s40, 0x3b0\n" \
"s_waitcnt lgkmcnt(8)\n" \
"v_mfma_f32_16x16x32_bf16 a[12:15], a[128:131], a[172:175], a[12:15]\n" \
"ds_read_b64_tr_b16 a[240:241], v36 offset:40960\n" \
"v_mov_b32_e32 v220, s7\n" \
"s_waitcnt lgkmcnt(5)\n" \
"v_mfma_f32_16x16x32_bf16 a[12:15], a[192:195], a[236:239], a[12:15]\n" \
"v_or_b32_e32 v218, s5, v216\n" \
"v_or_b32_e64 v216, s4, v216\n" \
"v_mfma_f32_16x16x32_bf16 a[28:31], a[132:135], a[172:175], a[28:31]\n" \
"v_cmp_gt_u32_e32 vcc, s40, v20\n" \
"s_movk_i32 s4, 0x3a0\n" \
"v_mfma_f32_16x16x32_bf16 a[28:31], a[196:199], a[236:239], a[28:31]\n" \
"ds_read_b64_tr_b16 a[242:243], v36 offset:41984\n" \
"v_or_b32_e32 v216, 0xb8000, v216\n" \
"v_mfma_f32_16x16x32_bf16 a[24:27], a[132:135], a[168:171], a[24:27]\n" \
"v_cndmask_b32_e32 v219, v219, v220, vcc\n" \
"v_cmp_gt_u32_e32 vcc, s4, v20\n" \
"v_mfma_f32_16x16x32_bf16 a[24:27], a[196:199], a[232:235], a[24:27]\n" \
"s_movk_i32 s5, 0x380\n" \
"v_mov_b32_e32 v215, s9\n" \
"v_mfma_f32_16x16x32_bf16 a[20:23], a[132:135], a[164:167], a[20:23]\n" \
"v_cndmask_b32_e32 v216, v219, v216, vcc\n" \
"v_mov_b32_e32 v217, s27\n" \
"v_mfma_f32_16x16x32_bf16 a[20:23], a[196:199], a[228:231], a[20:23]\n" \
"ds_read_b64_tr_b16 a[180:181], v37 offset:32768\n" \
"v_cmp_gt_u32_e64 vcc, s5, v20\n" \
"v_mfma_f32_16x16x32_bf16 a[16:19], a[132:135], a[160:163], a[16:19]\n" \
"s_movk_i32 s5, 0x360\n" \
"v_mov_b32_e32 v213, s13\n" \
"v_mfma_f32_16x16x32_bf16 a[16:19], a[196:199], a[224:227], a[16:19]\n" \
"v_cndmask_b32_e32 v218, v216, v218, vcc\n" \
"v_cmp_gt_u32_e32 vcc, s5, v20\n" \
"v_mfma_f32_16x16x32_bf16 a[32:35], a[136:139], a[160:163], a[32:35]\n" \
"s_mov_b32 m0, s32\n" \
"ds_read_b64_tr_b16 a[182:183], v37 offset:33792\n" \
"v_mfma_f32_16x16x32_bf16 a[32:35], a[200:203], a[224:227], a[32:35]\n" \
"s_mov_b32 exec_lo, 0x0\n" \
"s_mov_b32 exec_hi, 0xff\n" \
"global_load_lds_dword v[184:185], off\n" \
"s_mov_b64 exec, -1\n" \
"v_mfma_f32_16x16x32_bf16 a[36:39], a[136:139], a[164:167], a[36:39]\n" \
"s_movk_i32 s4, 0x240\n" \
"v_cmp_gt_u32_e64 s[4:5], s4, v20\n" \
"v_mfma_f32_16x16x32_bf16 a[36:39], a[200:203], a[228:231], a[36:39]\n" \
"ds_read_b64_tr_b16 a[244:245], v37 offset:40960\n" \
"v_cndmask_b32_e64 v215, v217, v215, vcc\n" \
"v_mfma_f32_16x16x32_bf16 a[40:43], a[136:139], a[168:171], a[40:43]\n" \
"v_mov_b32_e64 v219, s8\n" \
"v_cndmask_b32_e64 v217, v215, v213, s[4:5]\n" \
"v_mfma_f32_16x16x32_bf16 a[40:43], a[200:203], a[232:235], a[40:43]\n" \
"s_mov_b32 m0, s32\n" \
"v_and_b32_e32 v213, 0xffffff8, v214\n" \
"v_mfma_f32_16x16x32_bf16 a[44:47], a[136:139], a[172:175], a[44:47]\n" \
"s_mov_b32 exec_lo, 0xffff0000\n" \
"s_mov_b32 exec_hi, 0x0\n" \
"global_load_lds_dword v[186:187], off\n" \
"s_mov_b64 exec, -1\n" \
"v_mfma_f32_16x16x32_bf16 a[44:47], a[200:203], a[236:239], a[44:47]\n" \
"ds_read_b64_tr_b16 a[246:247], v37 offset:41984\n" \
"v_or_b32_e32 v213, s6, v213\n" \
"v_mfma_f32_16x16x32_bf16 a[60:63], a[140:143], a[172:175], a[60:63]\n" \
"v_and_b32_e64 v214, 7, v214\n" \
"v_lshl_or_b32 v214, v213, 3, v214\n" \
"v_mfma_f32_16x16x32_bf16 a[60:63], a[204:207], a[236:239], a[60:63]\n" \
"v_mad_u64_u32 v[212:213], s[6:7], s43, 18, v[212:213]\n" \
"v_subrev_u32_e64 v212, 36, v212\n" \
"v_mfma_f32_16x16x32_bf16 a[56:59], a[140:143], a[168:171], a[56:59]\n" \
"ds_read_b64_tr_b16 a[184:185], v38 offset:32768\n" \
"v_mov_b32_e64 v220, s26\n" \
"v_mfma_f32_16x16x32_bf16 a[56:59], a[204:207], a[232:235], a[56:59]\n" \
"v_mov_b32_e32 v216, s12\n" \
"v_cndmask_b32_e32 v212, v218, v212, vcc\n" \
"v_mfma_f32_16x16x32_bf16 a[52:55], a[140:143], a[164:167], a[52:55]\n" \
"v_cndmask_b32_e32 v219, v220, v219, vcc\n" \
"v_mov_b32_e32 v213, 0\n" \
"v_mfma_f32_16x16x32_bf16 a[52:55], a[204:207], a[228:231], a[52:55]\n" \
"ds_read_b64_tr_b16 a[186:187], v38 offset:33792\n" \
"v_cndmask_b32_e64 v212, v212, v214, s[4:5]\n" \
"v_mfma_f32_16x16x32_bf16 a[48:51], a[140:143], a[160:163], a[48:51]\n" \
"v_cndmask_b32_e64 v216, v219, v216, s[4:5]\n" \
"v_lshlrev_b32_e64 v212, 7, v212\n" \
"v_mfma_f32_16x16x32_bf16 a[48:51], a[204:207], a[224:227], a[48:51]\n" \
"v_lshl_add_u64 v[188:189], v[216:217], 0, v[212:213]\n" \
"v_mov_b32_e32 v215, 0xffff\n" \
"v_mfma_f32_16x16x32_bf16 a[64:67], a[144:147], a[160:163], a[64:67]\n" \
"ds_read_b64_tr_b16 a[248:249], v38 offset:40960\n" \
"s_and_b32 s4, s3, 1\n" \
"v_mfma_f32_16x16x32_bf16 a[64:67], a[208:211], a[224:227], a[64:67]\n" \
"s_lshl_b32 s5, s4, 4\n" \
"v_lshrrev_b32_e64 v212, 4, v20\n" \
"v_mfma_f32_16x16x32_bf16 a[68:71], a[144:147], a[164:167], a[68:71]\n" \
"v_lshrrev_b32_e32 v218, 6, v20\n" \
"v_and_b32_e32 v219, 12, v218\n" \
"v_mfma_f32_16x16x32_bf16 a[68:71], a[208:211], a[228:231], a[68:71]\n" \
"ds_read_b64_tr_b16 a[250:251], v38 offset:41984\n" \
"v_and_or_b32 v214, v212, 15, s5\n" \
"v_mfma_f32_16x16x32_bf16 a[72:75], a[144:147], a[168:171], a[72:75]\n" \
"v_cmp_gt_u32_e64 vcc, v219, v214\n" \
"v_mov_b32_e32 v220, 0xffff0000\n" \
"v_mfma_f32_16x16x32_bf16 a[72:75], a[208:211], a[232:235], a[72:75]\n" \
"s_lshl_b32 s5, s2, 2\n" \
"v_cndmask_b32_e64 v217, v215, 0, vcc\n" \
"v_mfma_f32_16x16x32_bf16 a[76:79], a[144:147], a[172:175], a[76:79]\n" \
"v_cmp_lt_u32_e32 vcc, v219, v214\n" \
"v_mov_b32_e64 v213, s23\n" \
"v_mfma_f32_16x16x32_bf16 a[76:79], a[208:211], a[236:239], a[76:79]\n" \
"ds_read_b64_tr_b16 a[188:189], v39 offset:32768\n" \
"s_or_b32 s5, s5, s3\n" \
"v_mfma_f32_16x16x32_bf16 a[92:95], a[148:151], a[172:175], a[92:95]\n" \
"v_cndmask_b32_e32 v221, 0, v220, vcc\n" \
"v_or_b32_e64 v12, v217, v221\n" \
"v_mfma_f32_16x16x32_bf16 a[92:95], a[212:215], a[236:239], a[92:95]\n" \
"v_or_b32_e32 v217, 2, v219\n" \
"v_cmp_gt_u32_e32 vcc, v217, v214\n" \
"v_mfma_f32_16x16x32_bf16 a[88:91], a[148:151], a[168:171], a[88:91]\n" \
"ds_read_b64_tr_b16 a[190:191], v39 offset:33792\n" \
"v_mov_b32_e64 v216, s25\n" \
"v_mfma_f32_16x16x32_bf16 a[88:91], a[212:215], a[232:235], a[88:91]\n" \
"s_movk_i32 s40, 0xf400\n" \
"v_cndmask_b32_e64 v221, v215, 0, vcc\n" \
"v_mfma_f32_16x16x32_bf16 a[84:87], a[148:151], a[164:167], a[84:87]\n" \
"v_cmp_lt_u32_e32 vcc, v217, v214\n" \
"s_lshl_b32 s4, s4, 6\n" \
"v_mfma_f32_16x16x32_bf16 a[84:87], a[212:215], a[228:231], a[84:87]\n" \
"ds_read_b64_tr_b16 a[252:253], v39 offset:40960\n" \
"v_cndmask_b32_e32 v217, 0, v220, vcc\n" \
"v_mfma_f32_16x16x32_bf16 a[80:83], a[148:151], a[160:163], a[80:83]\n" \
"v_or_b32_e32 v13, v221, v217\n" \
"v_or_b32_e32 v217, 16, v219\n" \
"v_mfma_f32_16x16x32_bf16 a[80:83], a[212:215], a[224:227], a[80:83]\n" \
"v_cmp_gt_u32_e32 vcc, v217, v214\n" \
"s_nop 1\n" \
"v_cndmask_b32_e64 v221, v215, 0, vcc\n" \
"v_mfma_f32_16x16x32_bf16 a[96:99], a[152:155], a[160:163], a[96:99]\n" \
"ds_read_b64_tr_b16 a[254:255], v39 offset:41984\n" \
"v_cmp_lt_u32_e64 vcc, v217, v214\n" \
"v_mfma_f32_16x16x32_bf16 a[96:99], a[216:219], a[224:227], a[96:99]\n" \
"s_nop 0\n" \
"v_cndmask_b32_e32 v217, 0, v220, vcc\n" \
"v_or_b32_e64 v14, v221, v217\n" \
"v_mfma_f32_16x16x32_bf16 a[100:103], a[152:155], a[164:167], a[100:103]\n" \
"v_or_b32_e32 v217, 18, v219\n" \
"v_cmp_gt_u32_e32 vcc, v217, v214\n" \
"v_mfma_f32_16x16x32_bf16 a[100:103], a[216:219], a[228:231], a[100:103]\n" \
"s_nop 0\n" \
"v_cndmask_b32_e64 v215, v215, 0, vcc\n" \
"v_cmp_lt_u32_e32 vcc, v217, v214\n" \
"v_mfma_f32_16x16x32_bf16 a[104:107], a[152:155], a[168:171], a[104:107]\n" \
"s_nop 0\n" \
"v_cndmask_b32_e32 v214, 0, v220, vcc\n" \
"v_or_b32_e64 v15, v215, v214\n" \
"v_mfma_f32_16x16x32_bf16 a[104:107], a[216:219], a[232:235], a[104:107]\n" \
"v_mad_u64_u32 v[214:215], s[6:7], s5, 42, v[212:213]\n" \
"s_movk_i32 s5, 0x1440\n" \
"v_mfma_f32_16x16x32_bf16 a[108:111], a[152:155], a[172:175], a[108:111]\n" \
"v_cmp_gt_u32_e32 vcc, s5, v214\n" \
"v_min_u32_e32 v217, 0xa43f, v214\n" \
"v_mfma_f32_16x16x32_bf16 a[108:111], a[216:219], a[236:239], a[108:111]\n" \
"v_mov_b32_e32 v214, s24\n" \
"v_cndmask_b32_e32 v215, v216, v213, vcc\n" \
"s_waitcnt lgkmcnt(15)\n" \
"v_mfma_f32_16x16x32_bf16 a[124:127], a[156:159], a[172:175], a[124:127]\n" \
"v_mov_b32_e32 v213, s22\n" \
"s_movk_i32 s6, 0xffee\n" \
"v_mfma_f32_16x16x32_bf16 a[124:127], a[220:223], a[236:239], a[124:127]\n" \
"v_cndmask_b32_e32 v214, v214, v213, vcc\n" \
"v_add_u32_e32 v213, 0xffffebc0, v217\n" \
"v_mfma_f32_16x16x32_bf16 a[120:123], a[156:159], a[168:171], a[120:123]\n" \
"s_lshl_b32 s5, s2, 5\n" \
"v_cndmask_b32_e32 v213, v213, v217, vcc\n" \
"v_mfma_f32_16x16x32_bf16 a[120:123], a[220:223], a[232:235], a[120:123]\n" \
"v_lshlrev_b32_e64 v216, 7, v213\n" \
"v_mul_u32_u24_e32 v213, 0xe39, v212\n" \
"v_mfma_f32_16x16x32_bf16 a[116:119], a[156:159], a[164:167], a[116:119]\n" \
"v_mov_b32_e32 v217, 0\n" \
"v_lshrrev_b32_e32 v213, 16, v213\n" \
"v_mfma_f32_16x16x32_bf16 a[116:119], a[220:223], a[228:231], a[116:119]\n" \
"v_lshl_add_u64 v[16:17], v[214:215], 0, v[216:217]\n" \
"v_mad_i32_i24 v214, v213, s6, v212\n" \
"v_mfma_f32_16x16x32_bf16 a[112:115], a[156:159], a[160:163], a[112:115]\n" \
"s_add_i32 s5, s5, 32\n" \
"v_lshlrev_b32_e32 v215, 4, v214\n" \
"v_mfma_f32_16x16x32_bf16 a[112:115], a[220:223], a[224:227], a[112:115]\n" \
"s_waitcnt vmcnt(20)\n" \
"v_readfirstlane_b32 s62, v180\n" \
"s_nop 3\n" \
"s_add_u32 s62, s62, 0x9E3779B1\n" \
"s_waitcnt vmcnt(2) lgkmcnt(0)\n" \
"s_barrier\n" \
"v_mfma_f32_16x16x32_bf16 v[52:55], a[128:131], a[176:179], v[52:55]\n" \
"s_add_u32 m0, s30, 0x8000\n" \
"s_movk_i32 s84, 0x180\n" \
"v_mfma_f32_16x16x32_bf16 v[52:55], a[192:195], a[240:243], v[52:55]\n" \
"buffer_load_dwordx4 v22, s[64:67], s84 offen offset:0 lds\n" \
"v_mfma_f32_16x16x32_bf16 v[56:59], a[128:131], a[180:183], v[56:59]\n" \
"ds_read_b64_tr_b16 a[160:161], v32 offset:0\n" \
"v_add_u32_e64 v216, s5, v215\n" \
"v_mfma_f32_16x16x32_bf16 v[56:59], a[192:195], a[244:247], v[56:59]\n" \
"s_movk_i32 s84, 0x1d80\n" \
"v_mfma_f32_16x16x32_bf16 v[60:63], a[128:131], a[184:187], v[60:63]\n" \
"buffer_load_dwordx4 v23, s[64:67], s84 offen offset:1024 lds\n" \
"v_mfma_f32_16x16x32_bf16 v[60:63], a[192:195], a[248:251], v[60:63]\n" \
"ds_read_b64_tr_b16 a[162:163], v32 offset:1024\n" \
"v_and_b32_e32 v216, 0xf0, v216\n" \
"v_mfma_f32_16x16x32_bf16 v[64:67], a[128:131], a[188:191], v[64:67]\n" \
"ds_read_b64_tr_b16 a[224:225], v32 offset:8192\n" \
"v_cmp_gt_u32_e32 vcc, 16, v214\n" \
"v_mfma_f32_16x16x32_bf16 v[64:67], a[192:195], a[252:255], v[64:67]\n" \
"ds_read_b128 a[128:131], v28 offset:16384\n" \
"s_movk_i32 s7, 0xf800\n" \
"v_mfma_f32_16x16x32_bf16 v[80:83], a[132:135], a[188:191], v[80:83]\n" \
"ds_read_b128 a[192:195], v29 offset:16384\n" \
"v_cndmask_b32_e32 v214, v215, v216, vcc\n" \
"v_mfma_f32_16x16x32_bf16 v[80:83], a[196:199], a[252:255], v[80:83]\n" \
"s_movk_i32 s84, 0x3980\n" \
"v_mfma_f32_16x16x32_bf16 v[76:79], a[132:135], a[184:187], v[76:79]\n" \
"buffer_load_dwordx4 v22, s[64:67], s84 offen offset:2048 lds\n" \
"v_mfma_f32_16x16x32_bf16 v[76:79], a[196:199], a[248:251], v[76:79]\n" \
"s_movk_i32 s84, 0x5580\n" \
"v_mfma_f32_16x16x32_bf16 v[72:75], a[132:135], a[180:183], v[72:75]\n" \
"buffer_load_dwordx4 v23, s[64:67], s84 offen offset:3072 lds\n" \
"v_mfma_f32_16x16x32_bf16 v[72:75], a[196:199], a[244:247], v[72:75]\n" \
"s_add_u32 m0, s31, 0x8000\n" \
"s_mov_b32 s84, 0x180000\n" \
"v_mfma_f32_16x16x32_bf16 v[68:71], a[132:135], a[176:179], v[68:71]\n" \
"buffer_load_dwordx4 v26, s[68:71], s84 offen offset:0 lds\n" \
"v_mfma_f32_16x16x32_bf16 v[68:71], a[196:199], a[240:243], v[68:71]\n" \
"ds_read_b128 a[132:135], v28 offset:18432\n" \
"v_or_b32_e64 v215, 64, v212\n" \
"v_mfma_f32_16x16x32_bf16 v[84:87], a[136:139], a[176:179], v[84:87]\n" \
"ds_read_b128 a[196:199], v29 offset:18432\n" \
"v_mul_u32_u24_e32 v216, 0xe39, v215\n" \
"v_mfma_f32_16x16x32_bf16 v[84:87], a[200:203], a[240:243], v[84:87]\n" \
"s_addk_i32 s84, 0x7c00\n" \
"v_mfma_f32_16x16x32_bf16 v[88:91], a[136:139], a[180:183], v[88:91]\n" \
"buffer_load_dwordx4 v26, s[68:71], s84 offen offset:1024 lds\n" \
"v_mfma_f32_16x16x32_bf16 v[88:91], a[200:203], a[244:247], v[88:91]\n" \
"ds_read_b64_tr_b16 a[226:227], v32 offset:9216\n" \
"v_lshrrev_b32_e32 v216, 16, v216\n" \
"v_mfma_f32_16x16x32_bf16 v[92:95], a[136:139], a[184:187], v[92:95]\n" \
"s_addk_i32 s84, 0x7c00\n" \
"v_mfma_f32_16x16x32_bf16 v[92:95], a[200:203], a[248:251], v[92:95]\n" \
"buffer_load_dwordx4 v27, s[68:71], s84 offen offset:2048 lds\n" \
"v_mfma_f32_16x16x32_bf16 v[96:99], a[136:139], a[188:191], v[96:99]\n" \
"ds_read_b64_tr_b16 a[164:165], v33 offset:0\n" \
"v_mad_i32_i24 v215, v216, s6, v215\n" \
"v_mfma_f32_16x16x32_bf16 v[96:99], a[200:203], a[252:255], v[96:99]\n" \
"ds_read_b128 a[136:139], v28 offset:20480\n" \
"v_lshlrev_b32_e32 v217, 4, v215\n" \
"v_mfma_f32_16x16x32_bf16 v[112:115], a[140:143], a[188:191], v[112:115]\n" \
"ds_read_b128 a[200:203], v29 offset:20480\n" \
"v_add_u32_e64 v220, s5, v217\n" \
"v_mfma_f32_16x16x32_bf16 v[112:115], a[204:207], a[252:255], v[112:115]\n" \
"s_addk_i32 s84, 0x7c00\n" \
"s_nop 0\n" \
"v_mfma_f32_16x16x32_bf16 v[108:111], a[140:143], a[184:187], v[108:111]\n" \
"buffer_load_dwordx4 v27, s[68:71], s84 offen offset:3072 lds\n" \
"v_mfma_f32_16x16x32_bf16 v[108:111], a[204:207], a[248:251], v[108:111]\n" \
"s_add_u32 m0, s30, 0x9000\n" \
"s_mov_b32 s84, 0x8180\n" \
"v_mfma_f32_16x16x32_bf16 v[104:107], a[140:143], a[180:183], v[104:107]\n" \
"buffer_load_dwordx4 v22, s[64:67], s84 offen offset:0 lds\n" \
"v_mfma_f32_16x16x32_bf16 v[104:107], a[204:207], a[244:247], v[104:107]\n" \
"ds_read_b64_tr_b16 a[166:167], v33 offset:1024\n" \
"v_and_b32_e32 v220, 0xf0, v220\n" \
"v_mfma_f32_16x16x32_bf16 v[100:103], a[140:143], a[176:179], v[100:103]\n" \
"ds_read_b64_tr_b16 a[228:229], v33 offset:8192\n" \
"v_cmp_gt_u32_e64 vcc, 16, v215\n" \
"v_mfma_f32_16x16x32_bf16 v[100:103], a[204:207], a[240:243], v[100:103]\n" \
"ds_read_b128 a[140:143], v28 offset:22528\n" \
"v_cndmask_b32_e64 v215, v217, v220, vcc\n" \
"v_mfma_f32_16x16x32_bf16 v[116:119], a[144:147], a[176:179], v[116:119]\n" \
"ds_read_b128 a[204:207], v29 offset:22528\n" \
"v_or_b32_e32 v217, 0x80, v212\n" \
"v_mfma_f32_16x16x32_bf16 v[116:119], a[208:211], a[240:243], v[116:119]\n" \
"s_addk_i32 s84, 0x1c00\n" \
"v_mfma_f32_16x16x32_bf16 v[120:123], a[144:147], a[180:183], v[120:123]\n" \
"buffer_load_dwordx4 v23, s[64:67], s84 offen offset:1024 lds\n" \
"v_mfma_f32_16x16x32_bf16 v[120:123], a[208:211], a[244:247], v[120:123]\n" \
"s_addk_i32 s84, 0x1c00\n" \
"v_mfma_f32_16x16x32_bf16 v[124:127], a[144:147], a[184:187], v[124:127]\n" \
"buffer_load_dwordx4 v22, s[64:67], s84 offen offset:2048 lds\n" \
"v_mfma_f32_16x16x32_bf16 v[124:127], a[208:211], a[248:251], v[124:127]\n" \
"s_addk_i32 s84, 0x1c00\n" \
"v_mfma_f32_16x16x32_bf16 v[128:131], a[144:147], a[188:191], v[128:131]\n" \
"buffer_load_dwordx4 v23, s[64:67], s84 offen offset:3072 lds\n" \
"v_mfma_f32_16x16x32_bf16 v[128:131], a[208:211], a[252:255], v[128:131]\n" \
"ds_read_b128 a[144:147], v28 offset:0\n" \
"v_mul_u32_u24_e32 v220, 0xe39, v217\n" \
"v_mfma_f32_16x16x32_bf16 v[144:147], a[148:151], a[188:191], v[144:147]\n" \
"ds_read_b128 a[208:211], v29 offset:0\n" \
"v_lshrrev_b32_e32 v220, 16, v220\n" \
"v_mfma_f32_16x16x32_bf16 v[144:147], a[212:215], a[252:255], v[144:147]\n" \
"s_add_u32 m0, s31, 0xc000\n" \
"s_mov_b32 s84, 0x180000\n" \
"v_mfma_f32_16x16x32_bf16 v[140:143], a[148:151], a[184:187], v[140:143]\n" \
"buffer_load_dwordx4 v26, s[72:75], s84 offen offset:0 lds\n" \
"v_mfma_f32_16x16x32_bf16 v[140:143], a[212:215], a[248:251], v[140:143]\n" \
"ds_read_b64_tr_b16 a[230:231], v33 offset:9216\n" \
"v_mad_i32_i24 v217, v220, s6, v217\n" \
"v_mfma_f32_16x16x32_bf16 v[136:139], a[148:151], a[180:183], v[136:139]\n" \
"s_addk_i32 s84, 0x7c00\n" \
"v_mfma_f32_16x16x32_bf16 v[136:139], a[212:215], a[244:247], v[136:139]\n" \
"buffer_load_dwordx4 v26, s[72:75], s84 offen offset:1024 lds\n" \
"v_mfma_f32_16x16x32_bf16 v[132:135], a[148:151], a[176:179], v[132:135]\n" \
"ds_read_b64_tr_b16 a[168:169], v34 offset:0\n" \
"v_lshlrev_b32_e32 v221, 4, v217\n" \
"v_mfma_f32_16x16x32_bf16 v[132:135], a[212:215], a[240:243], v[132:135]\n" \
"ds_read_b128 a[148:151], v28 offset:2048\n" \
"v_add_u32_e64 v222, s5, v221\n" \
"v_mfma_f32_16x16x32_bf16 v[148:151], a[152:155], a[176:179], v[148:151]\n" \
"ds_read_b128 a[212:215], v29 offset:2048\n" \
"v_and_b32_e32 v222, 0xf0, v222\n" \
"v_mfma_f32_16x16x32_bf16 v[148:151], a[216:219], a[240:243], v[148:151]\n" \
"ds_read_b64_tr_b16 a[170:171], v34 offset:1024\n" \
"v_cmp_gt_u32_e64 vcc, 16, v217\n" \
"v_mfma_f32_16x16x32_bf16 v[152:155], a[152:155], a[180:183], v[152:155]\n" \
"ds_read_b64_tr_b16 a[232:233], v34 offset:8192\n" \
"v_cndmask_b32_e64 v217, v221, v222, vcc\n" \
"v_mfma_f32_16x16x32_bf16 v[152:155], a[216:219], a[244:247], v[152:155]\n" \
"ds_read_b64_tr_b16 a[234:235], v34 offset:9216\n" \
"v_or_b32_e32 v221, 0xc0, v212\n" \
"v_mfma_f32_16x16x32_bf16 v[156:159], a[152:155], a[184:187], v[156:159]\n" \
"ds_read_b64_tr_b16 a[172:173], v35 offset:0\n" \
"v_mul_u32_u24_e32 v222, 0xe39, v221\n" \
"v_mfma_f32_16x16x32_bf16 v[156:159], a[216:219], a[248:251], v[156:159]\n" \
"ds_read_b64_tr_b16 a[174:175], v35 offset:1024\n" \
"v_lshrrev_b32_e64 v222, 16, v222\n" \
"v_mfma_f32_16x16x32_bf16 v[160:163], a[152:155], a[188:191], v[160:163]\n" \
"ds_read_b64_tr_b16 a[236:237], v35 offset:8192\n" \
"v_mad_i32_i24 v221, v222, s6, v221\n" \
"v_mfma_f32_16x16x32_bf16 v[160:163], a[216:219], a[252:255], v[160:163]\n" \
"ds_read_b128 a[152:155], v28 offset:4096\n" \
"v_lshlrev_b32_e64 v223, 4, v221\n" \
"v_mfma_f32_16x16x32_bf16 v[176:179], a[156:159], a[188:191], v[176:179]\n" \
"ds_read_b128 a[216:219], v29 offset:4096\n" \
"v_add_u32_e64 v224, s5, v223\n" \
"v_mfma_f32_16x16x32_bf16 v[176:179], a[220:223], a[252:255], v[176:179]\n" \
"ds_read_b64_tr_b16 a[238:239], v35 offset:9216\n" \
"v_and_b32_e32 v224, 0xf0, v224\n" \
"v_mfma_f32_16x16x32_bf16 v[172:175], a[156:159], a[184:187], v[172:175]\n" \
"v_cmp_gt_u32_e32 vcc, 16, v221\n" \
"s_nop 1\n" \
"v_cndmask_b32_e64 v221, v223, v224, vcc\n" \
"v_mfma_f32_16x16x32_bf16 v[172:175], a[220:223], a[248:251], v[172:175]\n" \
"v_or_b32_e32 v223, 0x100, v212\n" \
"v_mul_u32_u24_e32 v224, 0xe39, v223\n" \
"v_mfma_f32_16x16x32_bf16 v[168:171], a[156:159], a[180:183], v[168:171]\n" \
"v_lshrrev_b32_e64 v224, 16, v224\n" \
"v_mad_i32_i24 v223, v224, s6, v223\n" \
"v_mfma_f32_16x16x32_bf16 v[168:171], a[220:223], a[244:247], v[168:171]\n" \
"v_lshlrev_b32_e32 v225, 4, v223\n" \
"v_add_u32_e32 v226, s5, v225\n" \
"v_mfma_f32_16x16x32_bf16 v[164:167], a[156:159], a[176:179], v[164:167]\n" \
"v_and_b32_e32 v226, 0xf0, v226\n" \
"v_cmp_gt_u32_e64 vcc, 16, v223\n" \
"v_mfma_f32_16x16x32_bf16 v[164:167], a[220:223], a[240:243], v[164:167]\n" \
"ds_read_b128 a[156:159], v28 offset:6144\n" \
"ds_read_b128 a[220:223], v29 offset:6144\n" \
"s_waitcnt lgkmcnt(15)\n" \
"v_mfma_f32_16x16x32_bf16 a[0:3], a[128:131], a[160:163], a[0:3]\n" \
"s_addk_i32 s84, 0x7c00\n" \
"v_mfma_f32_16x16x32_bf16 a[0:3], a[192:195], a[224:227], a[0:3]\n" \
"buffer_load_dwordx4 v27, s[72:75], s84 offen offset:2048 lds\n" \
"v_mfma_f32_16x16x32_bf16 a[4:7], a[128:131], a[164:167], a[4:7]\n" \
"ds_read_b64_tr_b16 a[176:177], v36 offset:0\n" \
"v_cndmask_b32_e32 v223, v225, v226, vcc\n" \
"s_waitcnt lgkmcnt(15)\n" \
"v_mfma_f32_16x16x32_bf16 a[4:7], a[192:195], a[228:231], a[4:7]\n" \
"s_addk_i32 s84, 0x7c00\n" \
"s_waitcnt lgkmcnt(11)\n" \
"v_mfma_f32_16x16x32_bf16 a[8:11], a[128:131], a[168:171], a[8:11]\n" \
"buffer_load_dwordx4 v27, s[72:75], s84 offen offset:3072 lds\n" \
"s_waitcnt lgkmcnt(9)\n" \
"v_mfma_f32_16x16x32_bf16 a[8:11], a[192:195], a[232:235], a[8:11]\n" \
"ds_read_b64_tr_b16 a[178:179], v36 offset:1024\n" \
"v_or_b32_e32 v225, 0x140, v212\n" \
"s_waitcnt lgkmcnt(8)\n" \
"v_mfma_f32_16x16x32_bf16 a[12:15], a[128:131], a[172:175], a[12:15]\n" \
"ds_read_b64_tr_b16 a[240:241], v36 offset:8192\n" \
"v_mul_u32_u24_e32 v226, 0xe39, v225\n" \
"s_waitcnt lgkmcnt(5)\n" \
"v_mfma_f32_16x16x32_bf16 a[12:15], a[192:195], a[236:239], a[12:15]\n" \
"v_lshrrev_b32_e32 v226, 16, v226\n" \
"v_mad_i32_i24 v225, v226, s6, v225\n" \
"v_mfma_f32_16x16x32_bf16 a[28:31], a[132:135], a[172:175], a[28:31]\n" \
"v_lshlrev_b32_e32 v227, 4, v225\n" \
"v_add_u32_e32 v228, s5, v227\n" \
"v_mfma_f32_16x16x32_bf16 a[28:31], a[196:199], a[236:239], a[28:31]\n" \
"ds_read_b64_tr_b16 a[242:243], v36 offset:9216\n" \
"v_and_b32_e32 v228, 0xf0, v228\n" \
"v_mfma_f32_16x16x32_bf16 a[24:27], a[132:135], a[168:171], a[24:27]\n" \
"buffer_load_dwordx4 v[200:203], v192, s[76:79], s34 offen offset:0\n" \
"v_mfma_f32_16x16x32_bf16 a[24:27], a[196:199], a[232:235], a[24:27]\n" \
"v_cmp_gt_u32_e32 vcc, 16, v225\n" \
"s_nop 1\n" \
"v_cndmask_b32_e64 v225, v227, v228, vcc\n" \
"v_mfma_f32_16x16x32_bf16 a[20:23], a[132:135], a[164:167], a[20:23]\n" \
"v_or_b32_e32 v227, 0x180, v212\n" \
"v_mul_u32_u24_e32 v228, 0xe39, v227\n" \
"v_mfma_f32_16x16x32_bf16 a[20:23], a[196:199], a[228:231], a[20:23]\n" \
"ds_read_b64_tr_b16 a[180:181], v37 offset:0\n" \
"v_lshrrev_b32_e64 v228, 16, v228\n" \
"v_mfma_f32_16x16x32_bf16 a[16:19], a[132:135], a[160:163], a[16:19]\n" \
"v_mad_i32_i24 v227, v228, s6, v227\n" \
"v_lshlrev_b32_e64 v229, 4, v227\n" \
"v_mfma_f32_16x16x32_bf16 a[16:19], a[196:199], a[224:227], a[16:19]\n" \
"v_add_u32_e64 v230, s5, v229\n" \
"v_and_b32_e32 v230, 0xf0, v230\n" \
"v_mfma_f32_16x16x32_bf16 a[32:35], a[136:139], a[160:163], a[32:35]\n" \
"buffer_load_dwordx4 v[204:207], v193, s[76:79], s34 offen offset:0\n" \
"v_mfma_f32_16x16x32_bf16 a[32:35], a[200:203], a[224:227], a[32:35]\n" \
"s_mov_b32 m0, s32\n" \
"ds_read_b64_tr_b16 a[182:183], v37 offset:1024\n" \
"v_mfma_f32_16x16x32_bf16 a[36:39], a[136:139], a[164:167], a[36:39]\n" \
"s_mov_b32 exec_lo, 0x0\n" \
"s_mov_b32 exec_hi, 0xff00\n" \
"global_load_lds_dword v[184:185], off\n" \
"s_mov_b64 exec, -1\n" \
"v_mfma_f32_16x16x32_bf16 a[36:39], a[200:203], a[228:231], a[36:39]\n" \
"ds_read_b64_tr_b16 a[244:245], v37 offset:8192\n" \
"v_cmp_gt_u32_e32 vcc, 16, v227\n" \
"v_mfma_f32_16x16x32_bf16 a[40:43], a[136:139], a[168:171], a[40:43]\n" \
"s_nop 0\n" \
"v_cndmask_b32_e32 v227, v229, v230, vcc\n" \
"v_or_b32_e32 v229, 0x1c0, v212\n" \
"v_mfma_f32_16x16x32_bf16 a[40:43], a[200:203], a[232:235], a[40:43]\n" \
"buffer_load_dword v208, v194, s[76:79], s34 offen offset:32\n" \
"v_mfma_f32_16x16x32_bf16 a[44:47], a[136:139], a[172:175], a[44:47]\n" \
"ds_read_b64_tr_b16 a[246:247], v37 offset:9216\n" \
"v_mul_u32_u24_e32 v230, 0xe39, v229\n" \
"v_mfma_f32_16x16x32_bf16 a[44:47], a[200:203], a[236:239], a[44:47]\n" \
"v_lshrrev_b32_e64 v230, 16, v230\n" \
"v_mad_i32_i24 v229, v230, s6, v229\n" \
"v_mfma_f32_16x16x32_bf16 a[60:63], a[140:143], a[172:175], a[60:63]\n" \
"v_lshlrev_b32_e32 v231, 4, v229\n" \
"v_add_u32_e32 v232, s5, v231\n" \
"v_mfma_f32_16x16x32_bf16 a[60:63], a[204:207], a[236:239], a[60:63]\n" \
"v_and_b32_e32 v232, 0xf0, v232\n" \
"v_cmp_gt_u32_e64 vcc, 16, v229\n" \
"v_mfma_f32_16x16x32_bf16 a[56:59], a[140:143], a[168:171], a[56:59]\n" \
"buffer_load_dword v209, v194, s[76:79], s34 offen offset:1184\n" \
"v_mfma_f32_16x16x32_bf16 a[56:59], a[204:207], a[232:235], a[56:59]\n" \
"ds_read_b64_tr_b16 a[184:185], v38 offset:0\n" \
"v_cndmask_b32_e64 v229, v231, v232, vcc\n" \
"v_mfma_f32_16x16x32_bf16 a[52:55], a[140:143], a[164:167], a[52:55]\n" \
"v_or_b32_e32 v231, 0x200, v212\n" \
"v_mul_u32_u24_e32 v232, 0xe39, v231\n" \
"v_mfma_f32_16x16x32_bf16 a[52:55], a[204:207], a[228:231], a[52:55]\n" \
"ds_read_b64_tr_b16 a[186:187], v38 offset:1024\n" \
"v_lshrrev_b32_e64 v232, 16, v232\n" \
"v_mfma_f32_16x16x32_bf16 a[48:51], a[140:143], a[160:163], a[48:51]\n" \
"v_mad_i32_i24 v231, v232, s6, v231\n" \
"v_lshlrev_b32_e64 v233, 4, v231\n" \
"v_mfma_f32_16x16x32_bf16 a[48:51], a[204:207], a[224:227], a[48:51]\n" \
"v_add_u32_e32 v234, s5, v233\n" \
"s_movk_i32 s5, 0x480\n" \
"v_mfma_f32_16x16x32_bf16 a[64:67], a[144:147], a[160:163], a[64:67]\n" \
"buffer_load_dword v211, v194, s[76:79], s34 offen offset:2336\n" \
"v_mfma_f32_16x16x32_bf16 a[64:67], a[208:211], a[224:227], a[64:67]\n" \
"ds_read_b64_tr_b16 a[248:249], v38 offset:8192\n" \
"v_mad_u32_u24 v0, v213, s5, v214\n" \
"v_mfma_f32_16x16x32_bf16 a[68:71], a[144:147], a[164:167], a[68:71]\n" \
"v_mul_u32_u24_e32 v213, 0x480, v216\n" \
"s_movk_i32 s6, 0xfc00\n" \
"v_mfma_f32_16x16x32_bf16 a[68:71], a[208:211], a[228:231], a[68:71]\n" \
"ds_read_b64_tr_b16 a[250:251], v38 offset:9216\n" \
"v_add3_u32 v1, v213, v215, s6\n" \
"v_mfma_f32_16x16x32_bf16 a[72:75], a[144:147], a[168:171], a[72:75]\n" \
"v_mul_u32_u24_e32 v213, 0x480, v220\n" \
"v_add3_u32 v2, v213, v217, s7\n" \
"v_mfma_f32_16x16x32_bf16 a[72:75], a[208:211], a[232:235], a[72:75]\n" \
"s_mov_b32 s76, s20\n" \
"s_and_b32 s77, s21, 0xffff\n" \
"v_mfma_f32_16x16x32_bf16 a[76:79], a[144:147], a[172:175], a[76:79]\n" \
"s_mov_b32 s78, 0x480\n" \
"v_mul_u32_u24_e32 v213, 0x480, v222\n" \
"v_mfma_f32_16x16x32_bf16 a[76:79], a[208:211], a[236:239], a[76:79]\n" \
"buffer_load_ushort v210, v195, s[76:79], 0 offen\n" \
"v_mfma_f32_16x16x32_bf16 a[92:95], a[148:151], a[172:175], a[92:95]\n" \
"ds_read_b64_tr_b16 a[188:189], v39 offset:0\n" \
"v_add3_u32 v3, v213, v221, s40\n" \
"v_mfma_f32_16x16x32_bf16 a[92:95], a[212:215], a[236:239], a[92:95]\n" \
"v_mul_u32_u24_e32 v213, 0x480, v226\n" \
"v_add3_u32 v5, v213, v225, s6\n" \
"v_mfma_f32_16x16x32_bf16 a[88:91], a[148:151], a[168:171], a[88:91]\n" \
"s_mov_b32 s76, s8\n" \
"s_nop 0\n" \
"s_and_b32 s77, s9, 0xffff\n" \
"v_mfma_f32_16x16x32_bf16 a[88:91], a[212:215], a[232:235], a[88:91]\n" \
"s_mov_b32 s78, 0x240000\n" \
"ds_read_b64_tr_b16 a[190:191], v39 offset:1024\n" \
"v_mfma_f32_16x16x32_bf16 a[84:87], a[148:151], a[164:167], a[84:87]\n" \
"v_mul_u32_u24_e32 v213, 0x480, v228\n" \
"v_add3_u32 v6, v213, v227, s7\n" \
"v_mfma_f32_16x16x32_bf16 a[84:87], a[212:215], a[228:231], a[84:87]\n" \
"ds_read_b64_tr_b16 a[252:253], v39 offset:8192\n" \
"v_mul_u32_u24_e32 v213, 0x480, v230\n" \
"v_mfma_f32_16x16x32_bf16 a[80:83], a[148:151], a[160:163], a[80:83]\n" \
"v_add3_u32 v7, v213, v229, s40\n" \
"s_lshl_b32 s7, s3, 3\n" \
"v_mfma_f32_16x16x32_bf16 a[80:83], a[212:215], a[224:227], a[80:83]\n" \
"s_lshl_b32 s40, s3, 1\n" \
"s_and_b32 s7, s7, 16\n" \
"v_mfma_f32_16x16x32_bf16 a[96:99], a[152:155], a[160:163], a[96:99]\n" \
"ds_read_b64_tr_b16 a[254:255], v39 offset:9216\n" \
"s_and_b32 s40, s40, 2\n" \
"v_mfma_f32_16x16x32_bf16 a[96:99], a[216:219], a[224:227], a[96:99]\n" \
"s_and_b32 s6, s2, 7\n" \
"v_bfe_u32 v213, v20, 7, 1\n" \
"v_mfma_f32_16x16x32_bf16 a[100:103], a[152:155], a[164:167], a[100:103]\n" \
"s_or_b32 s7, s40, s7\n" \
"v_and_b32_e32 v234, 0xf0, v234\n" \
"v_mfma_f32_16x16x32_bf16 a[100:103], a[216:219], a[228:231], a[100:103]\n" \
".fill (L_pb + 48864 - .) / 4, 4, 0xBF800000\n" \
"L_pp12:\n" \
"s_cbranch_execz L_pret\n" \
"v_cmp_gt_u32_e32 vcc, 16, v231\n" \
"v_or3_b32 v213, s7, v213, v219\n" \
"v_mfma_f32_16x16x32_bf16 a[104:107], a[152:155], a[168:171], a[104:107]\n" \
"v_and_b32_e32 v214, 32, v20\n" \
"s_lshl_b32 s6, s6, 5\n" \
"v_mfma_f32_16x16x32_bf16 a[104:107], a[216:219], a[232:235], a[104:107]\n" \
"v_cndmask_b32_e32 v231, v233, v234, vcc\n" \
"v_mul_u32_u24_e32 v213, 18, v213\n" \
"v_mfma_f32_16x16x32_bf16 a[108:111], a[152:155], a[172:175], a[108:111]\n" \
"v_mov_b32_e32 v215, 0x100\n" \
"v_mov_b32_e64 v216, s6\n" \
"v_mfma_f32_16x16x32_bf16 a[108:111], a[216:219], a[236:239], a[108:111]\n" \
"v_cmp_eq_u32_e64 vcc, 0, v214\n" \
"v_and_or_b32 v213, v218, 1, v213\n" \
"s_waitcnt lgkmcnt(15)\n" \
"v_mfma_f32_16x16x32_bf16 a[124:127], a[156:159], a[172:175], a[124:127]\n" \
"v_lshlrev_b32_e32 v212, 3, v212\n" \
"v_cndmask_b32_e64 v214, v215, v216, vcc\n" \
"v_mfma_f32_16x16x32_bf16 a[124:127], a[220:223], a[236:239], a[124:127]\n" \
"v_mad_u32_u24 v213, v213, s5, v214\n" \
"v_and_or_b32 v213, v20, 16, v213\n" \
"v_mfma_f32_16x16x32_bf16 a[120:123], a[156:159], a[168:171], a[120:123]\n" \
"v_add_u32_e32 v9, 0x4800, v213\n" \
"v_mul_u32_u24_e32 v213, 0x120, v218\n" \
"v_mfma_f32_16x16x32_bf16 a[120:123], a[220:223], a[232:235], a[120:123]\n" \
"v_and_or_b32 v10, v212, 24, v213\n" \
"v_lshrrev_b32_e64 v213, 2, v20\n" \
"v_mfma_f32_16x16x32_bf16 a[116:119], a[156:159], a[164:167], a[116:119]\n" \
"v_and_or_b32 v213, v213, 60, s4\n" \
"v_lshl_or_b32 v212, s3, 9, v212\n" \
"v_mfma_f32_16x16x32_bf16 a[116:119], a[220:223], a[228:231], a[116:119]\n" \
"v_mad_u32_u24 v4, v224, s5, v223\n" \
"v_mad_u32_u24 v8, v232, s5, v231\n" \
"v_mfma_f32_16x16x32_bf16 a[112:115], a[156:159], a[160:163], a[112:115]\n" \
"v_or_b32_e32 v18, 0x27000, v213\n" \
"v_or_b32_e32 v19, 0x27800, v212\n" \
"v_mfma_f32_16x16x32_bf16 a[112:115], a[220:223], a[224:227], a[112:115]\n" \
"s_waitcnt vmcnt(7) lgkmcnt(0)\n" \
"s_barrier\n" \
"v_mfma_f32_16x16x32_bf16 v[52:55], a[128:131], a[176:179], v[52:55]\n" \
"s_add_u32 m0, s30, 0x0\n" \
"s_movk_i32 s84, 0x200\n" \
"v_mfma_f32_16x16x32_bf16 v[52:55], a[192:195], a[240:243], v[52:55]\n" \
"buffer_load_dwordx4 v22, s[64:67], s84 offen offset:0 lds\n" \
"v_mfma_f32_16x16x32_bf16 v[56:59], a[128:131], a[180:183], v[56:59]\n" \
"ds_read_b64_tr_b16 a[160:161], v32 offset:32768\n" \
"v_mov_b32_e32 v215, 0x1000c0c\n" \
"v_mfma_f32_16x16x32_bf16 v[56:59], a[192:195], a[244:247], v[56:59]\n" \
"s_movk_i32 s84, 0x1e00\n" \
"v_mfma_f32_16x16x32_bf16 v[60:63], a[128:131], a[184:187], v[60:63]\n" \
"buffer_load_dwordx4 v23, s[64:67], s84 offen offset:1024 lds\n" \
"v_mfma_f32_16x16x32_bf16 v[60:63], a[192:195], a[248:251], v[60:63]\n" \
"ds_read_b64_tr_b16 a[162:163], v32 offset:33792\n" \
"v_mov_b32_e32 v216, 0xc0c0100\n" \
"v_mfma_f32_16x16x32_bf16 v[64:67], a[128:131], a[188:191], v[64:67]\n" \
"ds_read_b64_tr_b16 a[224:225], v32 offset:40960\n" \
"v_bfe_u32 v214, v20, 4, 1\n" \
"v_mfma_f32_16x16x32_bf16 v[64:67], a[192:195], a[252:255], v[64:67]\n" \
"ds_read_b128 a[128:131], v28 offset:49152\n" \
"v_lshrrev_b32_e32 v212, 8, v20\n" \
"v_mfma_f32_16x16x32_bf16 v[80:83], a[132:135], a[188:191], v[80:83]\n" \
"ds_read_b128 a[192:195], v29 offset:49152\n" \
"v_cmp_eq_u32_e64 vcc, 0, v214\n" \
"v_mfma_f32_16x16x32_bf16 v[80:83], a[196:199], a[252:255], v[80:83]\n" \
"s_movk_i32 s84, 0x3a00\n" \
"v_mfma_f32_16x16x32_bf16 v[76:79], a[132:135], a[184:187], v[76:79]\n" \
"buffer_load_dwordx4 v22, s[64:67], s84 offen offset:2048 lds\n" \
"v_mfma_f32_16x16x32_bf16 v[76:79], a[196:199], a[248:251], v[76:79]\n" \
"s_movk_i32 s84, 0x5600\n" \
"v_mfma_f32_16x16x32_bf16 v[72:75], a[132:135], a[180:183], v[72:75]\n" \
"buffer_load_dwordx4 v23, s[64:67], s84 offen offset:3072 lds\n" \
"v_mfma_f32_16x16x32_bf16 v[72:75], a[196:199], a[244:247], v[72:75]\n" \
"s_add_u32 m0, s31, 0x0\n" \
"s_mov_b32 s84, 0x200000\n" \
"v_mfma_f32_16x16x32_bf16 v[68:71], a[132:135], a[176:179], v[68:71]\n" \
"buffer_load_dwordx4 v26, s[68:71], s84 offen offset:0 lds\n" \
"v_mfma_f32_16x16x32_bf16 v[68:71], a[196:199], a[240:243], v[68:71]\n" \
"ds_read_b128 a[132:135], v28 offset:51200\n" \
"v_bfe_u32 v213, v20, 4, 4\n" \
"v_mfma_f32_16x16x32_bf16 v[84:87], a[136:139], a[176:179], v[84:87]\n" \
"ds_read_b128 a[196:199], v29 offset:51200\n" \
"v_mov_b32_e32 v217, 0xc0c0302\n" \
"v_mfma_f32_16x16x32_bf16 v[84:87], a[200:203], a[240:243], v[84:87]\n" \
"s_addk_i32 s84, 0x7c00\n" \
"v_mfma_f32_16x16x32_bf16 v[88:91], a[136:139], a[180:183], v[88:91]\n" \
"buffer_load_dwordx4 v26, s[68:71], s84 offen offset:1024 lds\n" \
"v_mfma_f32_16x16x32_bf16 v[88:91], a[200:203], a[244:247], v[88:91]\n" \
"ds_read_b64_tr_b16 a[226:227], v32 offset:41984\n" \
"v_cndmask_b32_e64 v215, v215, v216, vcc\n" \
"v_mfma_f32_16x16x32_bf16 v[92:95], a[136:139], a[184:187], v[92:95]\n" \
"s_addk_i32 s84, 0x7c00\n" \
"v_mfma_f32_16x16x32_bf16 v[92:95], a[200:203], a[248:251], v[92:95]\n" \
"buffer_load_dwordx4 v27, s[68:71], s84 offen offset:2048 lds\n" \
"v_mfma_f32_16x16x32_bf16 v[96:99], a[136:139], a[188:191], v[96:99]\n" \
"ds_read_b64_tr_b16 a[164:165], v33 offset:32768\n" \
"v_mov_b32_e32 v216, 0x3020c0c\n" \
"v_mfma_f32_16x16x32_bf16 v[96:99], a[200:203], a[252:255], v[96:99]\n" \
"ds_read_b128 a[136:139], v28 offset:53248\n" \
"v_lshl_or_b32 v214, v212, 2, v214\n" \
"v_mfma_f32_16x16x32_bf16 v[112:115], a[140:143], a[188:191], v[112:115]\n" \
"ds_read_b128 a[200:203], v29 offset:53248\n" \
"v_cndmask_b32_e32 v216, v216, v217, vcc\n" \
"v_mfma_f32_16x16x32_bf16 v[112:115], a[204:207], a[252:255], v[112:115]\n" \
"s_addk_i32 s84, 0x7c00\n" \
"s_nop 0\n" \
"v_mfma_f32_16x16x32_bf16 v[108:111], a[140:143], a[184:187], v[108:111]\n" \
"buffer_load_dwordx4 v27, s[68:71], s84 offen offset:3072 lds\n" \
"v_mfma_f32_16x16x32_bf16 v[108:111], a[204:207], a[248:251], v[108:111]\n" \
"s_add_u32 m0, s30, 0x1000\n" \
"s_mov_b32 s84, 0x8200\n" \
"v_mfma_f32_16x16x32_bf16 v[104:107], a[140:143], a[180:183], v[104:107]\n" \
"buffer_load_dwordx4 v22, s[64:67], s84 offen offset:0 lds\n" \
"v_mfma_f32_16x16x32_bf16 v[104:107], a[204:207], a[244:247], v[104:107]\n" \
"ds_read_b64_tr_b16 a[166:167], v33 offset:33792\n" \
"v_mov_b32_e32 v217, 0xc0c0c0c\n" \
"v_mfma_f32_16x16x32_bf16 v[100:103], a[140:143], a[176:179], v[100:103]\n" \
"ds_read_b64_tr_b16 a[228:229], v33 offset:40960\n" \
"v_cmp_gt_u32_e64 vcc, v214, v213\n" \
"v_mfma_f32_16x16x32_bf16 v[100:103], a[204:207], a[240:243], v[100:103]\n" \
"ds_read_b128 a[140:143], v28 offset:55296\n" \
"v_or_b32_e64 v214, 2, v214\n" \
"v_mfma_f32_16x16x32_bf16 v[116:119], a[144:147], a[176:179], v[116:119]\n" \
"ds_read_b128 a[204:207], v29 offset:55296\n" \
"s_lshr_b32 s4, s2, 5\n" \
"v_mfma_f32_16x16x32_bf16 v[116:119], a[208:211], a[240:243], v[116:119]\n" \
"s_addk_i32 s84, 0x1c00\n" \
"v_mfma_f32_16x16x32_bf16 v[120:123], a[144:147], a[180:183], v[120:123]\n" \
"buffer_load_dwordx4 v23, s[64:67], s84 offen offset:1024 lds\n" \
"v_mfma_f32_16x16x32_bf16 v[120:123], a[208:211], a[244:247], v[120:123]\n" \
"s_addk_i32 s84, 0x1c00\n" \
"v_mfma_f32_16x16x32_bf16 v[124:127], a[144:147], a[184:187], v[124:127]\n" \
"buffer_load_dwordx4 v22, s[64:67], s84 offen offset:2048 lds\n" \
"v_mfma_f32_16x16x32_bf16 v[124:127], a[208:211], a[248:251], v[124:127]\n" \
"s_addk_i32 s84, 0x1c00\n" \
"v_mfma_f32_16x16x32_bf16 v[128:131], a[144:147], a[188:191], v[128:131]\n" \
"buffer_load_dwordx4 v23, s[64:67], s84 offen offset:3072 lds\n" \
"v_mfma_f32_16x16x32_bf16 v[128:131], a[208:211], a[252:255], v[128:131]\n" \
"ds_read_b128 a[144:147], v28 offset:32768\n" \
"v_cndmask_b32_e64 v40, v217, v215, vcc\n" \
"v_mfma_f32_16x16x32_bf16 v[144:147], a[148:151], a[188:191], v[144:147]\n" \
"ds_read_b128 a[208:211], v29 offset:32768\n" \
"v_cndmask_b32_e64 v42, v215, v217, vcc\n" \
"v_mfma_f32_16x16x32_bf16 v[144:147], a[212:215], a[252:255], v[144:147]\n" \
"s_add_u32 m0, s31, 0x4000\n" \
"s_mov_b32 s84, 0x200000\n" \
"v_mfma_f32_16x16x32_bf16 v[140:143], a[148:151], a[184:187], v[140:143]\n" \
"buffer_load_dwordx4 v26, s[72:75], s84 offen offset:0 lds\n" \
"v_mfma_f32_16x16x32_bf16 v[140:143], a[212:215], a[248:251], v[140:143]\n" \
"ds_read_b64_tr_b16 a[230:231], v33 offset:41984\n" \
"v_cmp_gt_u32_e64 vcc, v214, v213\n" \
"v_mfma_f32_16x16x32_bf16 v[136:139], a[148:151], a[180:183], v[136:139]\n" \
"s_addk_i32 s84, 0x7c00\n" \
"v_mfma_f32_16x16x32_bf16 v[136:139], a[212:215], a[244:247], v[136:139]\n" \
"buffer_load_dwordx4 v26, s[72:75], s84 offen offset:1024 lds\n" \
"v_mfma_f32_16x16x32_bf16 v[132:135], a[148:151], a[176:179], v[132:135]\n" \
"ds_read_b64_tr_b16 a[168:169], v34 offset:32768\n" \
"v_and_b32_e32 v214, 16, v20\n" \
"v_mfma_f32_16x16x32_bf16 v[132:135], a[212:215], a[240:243], v[132:135]\n" \
"ds_read_b128 a[148:151], v28 offset:34816\n" \
"s_movk_i32 s42, 0x3f80\n" \
"v_mfma_f32_16x16x32_bf16 v[148:151], a[152:155], a[176:179], v[148:151]\n" \
"ds_read_b128 a[212:215], v29 offset:34816\n" \
"s_xor_b32 s5, s4, 15\n" \
"v_mfma_f32_16x16x32_bf16 v[148:151], a[216:219], a[240:243], v[148:151]\n" \
"ds_read_b64_tr_b16 a[170:171], v34 offset:33792\n" \
"v_mfma_f32_16x16x32_bf16 v[152:155], a[152:155], a[180:183], v[152:155]\n" \
"ds_read_b64_tr_b16 a[232:233], v34 offset:40960\n" \
"v_mfma_f32_16x16x32_bf16 v[152:155], a[216:219], a[244:247], v[152:155]\n" \
"ds_read_b64_tr_b16 a[234:235], v34 offset:41984\n" \
"v_mfma_f32_16x16x32_bf16 v[156:159], a[152:155], a[184:187], v[156:159]\n" \
"ds_read_b64_tr_b16 a[172:173], v35 offset:32768\n" \
"v_mfma_f32_16x16x32_bf16 v[156:159], a[216:219], a[248:251], v[156:159]\n" \
"ds_read_b64_tr_b16 a[174:175], v35 offset:33792\n" \
"v_mfma_f32_16x16x32_bf16 v[160:163], a[152:155], a[188:191], v[160:163]\n" \
"ds_read_b64_tr_b16 a[236:237], v35 offset:40960\n" \
"v_mfma_f32_16x16x32_bf16 v[160:163], a[216:219], a[252:255], v[160:163]\n" \
"ds_read_b128 a[152:155], v28 offset:36864\n" \
"v_mfma_f32_16x16x32_bf16 v[176:179], a[156:159], a[188:191], v[176:179]\n" \
"ds_read_b128 a[216:219], v29 offset:36864\n" \
"v_mfma_f32_16x16x32_bf16 v[176:179], a[220:223], a[252:255], v[176:179]\n" \
"ds_read_b64_tr_b16 a[238:239], v35 offset:41984\n" \
"v_mfma_f32_16x16x32_bf16 v[172:175], a[156:159], a[184:187], v[172:175]\n" \
"s_cmpk_lt_u32 s3, 2\n" \
"s_cselect_b32 s6, s5, s4\n" \
"v_mfma_f32_16x16x32_bf16 v[172:175], a[220:223], a[248:251], v[172:175]\n" \
"s_lshl_b32 s7, s6, 7\n" \
"v_and_b32_e32 v213, 0x320, v20\n" \
"v_mfma_f32_16x16x32_bf16 v[168:171], a[156:159], a[180:183], v[168:171]\n" \
"s_movk_i32 s41, 0x100\n" \
"v_lshlrev_b32_e64 v214, v214, s42\n" \
"v_mfma_f32_16x16x32_bf16 v[168:171], a[220:223], a[244:247], v[168:171]\n" \
"v_mov_b32_e32 v215, 0\n" \
"s_add_i32 s40, s7, -16\n" \
"v_mfma_f32_16x16x32_bf16 v[164:167], a[156:159], a[176:179], v[164:167]\n" \
"v_cndmask_b32_e32 v41, v217, v216, vcc\n" \
"v_cndmask_b32_e32 v43, v216, v217, vcc\n" \
"v_mfma_f32_16x16x32_bf16 v[164:167], a[220:223], a[240:243], v[164:167]\n" \
"ds_read_b128 a[156:159], v28 offset:38912\n" \
"ds_read_b128 a[220:223], v29 offset:38912\n" \
"s_waitcnt lgkmcnt(15)\n" \
"v_mfma_f32_16x16x32_bf16 a[0:3], a[128:131], a[160:163], a[0:3]\n" \
"s_addk_i32 s84, 0x7c00\n" \
"v_mfma_f32_16x16x32_bf16 a[0:3], a[192:195], a[224:227], a[0:3]\n" \
"buffer_load_dwordx4 v27, s[72:75], s84 offen offset:2048 lds\n" \
"v_mfma_f32_16x16x32_bf16 a[4:7], a[128:131], a[164:167], a[4:7]\n" \
"ds_read_b64_tr_b16 a[176:177], v36 offset:32768\n" \
"v_cmp_eq_u32_e32 vcc, s41, v213\n" \
"s_waitcnt lgkmcnt(15)\n" \
"v_mfma_f32_16x16x32_bf16 a[4:7], a[192:195], a[228:231], a[4:7]\n" \
"s_addk_i32 s84, 0x7c00\n" \
"s_waitcnt lgkmcnt(11)\n" \
"v_mfma_f32_16x16x32_bf16 a[8:11], a[128:131], a[168:171], a[8:11]\n" \
"buffer_load_dwordx4 v27, s[72:75], s84 offen offset:3072 lds\n" \
"s_waitcnt lgkmcnt(9)\n" \
"v_mfma_f32_16x16x32_bf16 a[8:11], a[192:195], a[232:235], a[8:11]\n" \
"ds_read_b64_tr_b16 a[178:179], v36 offset:33792\n" \
"s_mov_b32 s41, 0\n" \
"s_waitcnt lgkmcnt(8)\n" \
"v_mfma_f32_16x16x32_bf16 a[12:15], a[128:131], a[172:175], a[12:15]\n" \
"ds_read_b64_tr_b16 a[240:241], v36 offset:40960\n" \
"s_and_b32 s43, s3, 1\n" \
"s_waitcnt lgkmcnt(5)\n" \
"v_mfma_f32_16x16x32_bf16 a[12:15], a[192:195], a[236:239], a[12:15]\n" \
"v_cndmask_b32_e32 v46, v215, v214, vcc\n" \
"v_and_b32_e32 v213, 0x330, v20\n" \
"v_mfma_f32_16x16x32_bf16 a[28:31], a[132:135], a[172:175], a[28:31]\n" \
"s_cmp_lg_u32 s7, 0\n" \
"s_cselect_b32 s40, s40, s41\n" \
"v_mfma_f32_16x16x32_bf16 a[28:31], a[196:199], a[236:239], a[28:31]\n" \
"ds_read_b64_tr_b16 a[242:243], v36 offset:41984\n" \
"s_mov_b32 s42, 0xff800000\n" \
"v_mfma_f32_16x16x32_bf16 a[24:27], a[132:135], a[168:171], a[24:27]\n" \
"s_cmp_lg_u32 s7, 0\n" \
"s_cselect_b32 s47, s41, s42\n" \
"v_mfma_f32_16x16x32_bf16 a[24:27], a[196:199], a[232:235], a[24:27]\n" \
"s_lshl_b32 s41, s43, 6\n" \
"v_lshrrev_b32_e32 v215, 6, v20\n" \
"v_mfma_f32_16x16x32_bf16 a[20:23], a[132:135], a[164:167], a[20:23]\n" \
"v_mov_b32_e32 v214, 0x3f80\n" \
"v_cmp_eq_u32_e64 vcc, 32, v213\n" \
"v_mfma_f32_16x16x32_bf16 a[20:23], a[196:199], a[228:231], a[20:23]\n" \
"ds_read_b64_tr_b16 a[180:181], v37 offset:32768\n" \
"s_or_b32 s7, s41, s7\n" \
"v_mfma_f32_16x16x32_bf16 a[16:19], a[132:135], a[160:163], a[16:19]\n" \
"v_lshrrev_b32_e32 v213, 7, v20\n" \
"v_bfe_u32 v216, v20, 6, 2\n" \
"v_mfma_f32_16x16x32_bf16 a[16:19], a[196:199], a[224:227], a[16:19]\n" \
"v_lshrrev_b32_e64 v219, 1, v20\n" \
"v_bfe_u32 v215, v215, 1, 1\n" \
"v_mfma_f32_16x16x32_bf16 a[32:35], a[136:139], a[160:163], a[32:35]\n" \
"s_waitcnt vmcnt(16)\n" \
"v_or_b32_e32 v204, v210, v204\n" \
"v_or_b32_e64 v206, v210, v206\n" \
"v_mfma_f32_16x16x32_bf16 a[32:35], a[200:203], a[224:227], a[32:35]\n" \
"v_mov_b32_e64 v210, 0\n" \
"s_add_u32 s88, s35, 0x1000\n" \
"v_mfma_f32_16x16x32_bf16 a[36:39], a[136:139], a[164:167], a[36:39]\n" \
"ds_read_b64_tr_b16 a[182:183], v37 offset:33792\n" \
"v_cndmask_b32_e64 v47, 0, v214, vcc\n" \
"v_mfma_f32_16x16x32_bf16 a[36:39], a[200:203], a[228:231], a[36:39]\n" \
"ds_read_b64_tr_b16 a[244:245], v37 offset:40960\n" \
"v_or_b32_e64 v214, s7, v213\n" \
"v_mfma_f32_16x16x32_bf16 a[40:43], a[136:139], a[168:171], a[40:43]\n" \
"s_mov_b32 m0, s32\n" \
"v_lshl_or_b32 v217, s43, 3, v213\n" \
"v_mfma_f32_16x16x32_bf16 a[40:43], a[200:203], a[232:235], a[40:43]\n" \
"s_mov_b32 exec_lo, 0xffffffff\n" \
"s_mov_b32 exec_hi, 0x0\n" \
"global_load_lds_dword v[188:189], off\n" \
"s_mov_b64 exec, -1\n" \
"v_mfma_f32_16x16x32_bf16 a[44:47], a[136:139], a[172:175], a[44:47]\n" \
"ds_read_b64_tr_b16 a[246:247], v37 offset:41984\n" \
"v_lshlrev_b32_e64 v218, 10, v212\n" \
"v_mfma_f32_16x16x32_bf16 a[44:47], a[200:203], a[236:239], a[44:47]\n" \
"v_lshlrev_b32_e32 v216, 7, v216\n" \
"v_and_b32_e32 v219, 24, v219\n" \
"v_mfma_f32_16x16x32_bf16 a[60:63], a[140:143], a[172:175], a[60:63]\n" \
"ds_write_b128 v196, v[200:203]\n" \
"v_mfma_f32_16x16x32_bf16 a[60:63], a[204:207], a[236:239], a[60:63]\n" \
"v_and_or_b32 v213, v213, 2, v215\n" \
"v_add_u32_e64 v217, s40, v217\n" \
"v_mfma_f32_16x16x32_bf16 a[56:59], a[140:143], a[168:171], a[56:59]\n" \
"ds_read_b64_tr_b16 a[184:185], v38 offset:32768\n" \
"v_or3_b32 v216, v216, v218, v219\n" \
"v_mfma_f32_16x16x32_bf16 a[56:59], a[204:207], a[232:235], a[56:59]\n" \
"v_lshlrev_b32_e64 v213, 5, v213\n" \
"s_mov_b32 s40, 0x14000\n" \
"v_mfma_f32_16x16x32_bf16 a[52:55], a[140:143], a[164:167], a[52:55]\n" \
"s_lshl_b32 s7, s2, 2\n" \
"v_or3_b32 v48, v216, v213, s40\n" \
"v_mfma_f32_16x16x32_bf16 a[52:55], a[204:207], a[228:231], a[52:55]\n" \
"ds_read_b64_tr_b16 a[186:187], v38 offset:33792\n" \
"s_lshl_b32 s40, s43, 2\n" \
"v_mfma_f32_16x16x32_bf16 a[48:51], a[140:143], a[160:163], a[48:51]\n" \
"s_lshl_b32 s6, s6, 3\n" \
"v_or_b32_e32 v213, s40, v212\n" \
"v_mfma_f32_16x16x32_bf16 a[48:51], a[204:207], a[224:227], a[48:51]\n" \
"s_or_b32 s6, s6, s40\n" \
"s_and_b32 s40, s7, 0x7c\n" \
"v_mfma_f32_16x16x32_bf16 a[64:67], a[144:147], a[160:163], a[64:67]\n" \
"ds_write_b128 v197, v[204:207]\n" \
"v_mfma_f32_16x16x32_bf16 a[64:67], a[208:211], a[224:227], a[64:67]\n" \
"ds_read_b64_tr_b16 a[248:249], v38 offset:40960\n" \
"s_or_b32 s40, s40, s3\n" \
"v_mfma_f32_16x16x32_bf16 a[68:71], a[144:147], a[164:167], a[68:71]\n" \
"s_cmpk_lt_u32 s40, 64\n" \
"s_cselect_b32 s41, s4, s5\n" \
"v_mfma_f32_16x16x32_bf16 a[68:71], a[208:211], a[228:231], a[68:71]\n" \
"ds_read_b64_tr_b16 a[250:251], v38 offset:41984\n" \
"s_lshl_b32 s40, s40, 1\n" \
"v_mfma_f32_16x16x32_bf16 a[72:75], a[144:147], a[168:171], a[72:75]\n" \
"s_lshl_b32 s41, s41, 7\n" \
"s_and_b32 s40, s40, 0x7e\n" \
"v_mfma_f32_16x16x32_bf16 a[72:75], a[208:211], a[232:235], a[72:75]\n" \
"s_or_b32 s40, s41, s40\n" \
"s_mulk_i32 s40, 0x480\n" \
"v_mfma_f32_16x16x32_bf16 a[76:79], a[144:147], a[172:175], a[76:79]\n" \
"v_add_u32_e64 v49, s40, v20\n" \
"s_mov_b32 s40, 0xc000\n" \
"v_mfma_f32_16x16x32_bf16 a[76:79], a[208:211], a[236:239], a[76:79]\n" \
"ds_read_b64_tr_b16 a[188:189], v39 offset:32768\n" \
"s_and_b32 s7, s7, 28\n" \
"v_mfma_f32_16x16x32_bf16 a[92:95], a[148:151], a[172:175], a[92:95]\n" \
"ds_write_b128 v198, v[208:211]\n" \
"v_mfma_f32_16x16x32_bf16 a[92:95], a[212:215], a[236:239], a[92:95]\n" \
"s_bfe_u32 s45, s2, 0x20003\n" \
"s_mov_b32 s41, 0x8000\n" \
"v_mfma_f32_16x16x32_bf16 a[88:91], a[148:151], a[168:171], a[88:91]\n" \
"ds_read_b64_tr_b16 a[190:191], v39 offset:33792\n" \
"v_mfma_f32_16x16x32_bf16 a[88:91], a[212:215], a[232:235], a[88:91]\n" \
"s_cmp_lg_u32 s4, 0\n" \
"s_cselect_b32 s51, s40, s41\n" \
"v_mfma_f32_16x16x32_bf16 a[84:87], a[148:151], a[164:167], a[84:87]\n" \
"s_movk_i32 s40, 0x4000\n" \
"s_lshr_b32 s44, s3, 1\n" \
"v_mfma_f32_16x16x32_bf16 a[84:87], a[212:215], a[228:231], a[84:87]\n" \
"ds_read_b64_tr_b16 a[252:253], v39 offset:40960\n" \
"s_or_b32 s46, s7, s45\n" \
"v_mfma_f32_16x16x32_bf16 a[80:83], a[148:151], a[160:163], a[80:83]\n" \
"s_cmp_lg_u32 s4, 0\n" \
"s_cselect_b32 s53, s41, s40\n" \
"v_mfma_f32_16x16x32_bf16 a[80:83], a[212:215], a[224:227], a[80:83]\n" \
"s_lshl_b32 s40, s44, 15\n" \
"s_lshl_b32 s41, s43, 14\n" \
"v_mfma_f32_16x16x32_bf16 a[96:99], a[152:155], a[160:163], a[96:99]\n" \
"ds_read_b64_tr_b16 a[254:255], v39 offset:41984\n" \
"s_lshl_b32 s48, s46, 20\n" \
"v_mfma_f32_16x16x32_bf16 a[96:99], a[216:219], a[224:227], a[96:99]\n" \
"s_or_b32 s41, s40, s41\n" \
"s_add_i32 s49, s48, 0x1000000\n" \
"v_mfma_f32_16x16x32_bf16 a[100:103], a[152:155], a[164:167], a[100:103]\n" \
"s_or_b32 s42, s49, s41\n" \
"s_lshl_b32 s4, s4, 16\n" \
"v_mfma_f32_16x16x32_bf16 a[100:103], a[216:219], a[228:231], a[100:103]\n" \
"s_or_b32 s41, s42, s4\n" \
"s_lshl_b32 s4, s5, 16\n" \
"v_mfma_f32_16x16x32_bf16 a[104:107], a[152:155], a[168:171], a[104:107]\n" \
"s_mul_i32 s5, s6, 0x24800\n" \
"s_lshl_b32 s6, s46, 9\n" \
"v_mfma_f32_16x16x32_bf16 a[104:107], a[216:219], a[232:235], a[104:107]\n" \
"s_lshl_b32 s7, s7, 6\n" \
"s_add_i32 s6, s5, s6\n" \
"v_mfma_f32_16x16x32_bf16 a[108:111], a[152:155], a[172:175], a[108:111]\n" \
"s_or_b32 s5, s5, s7\n" \
"s_lshl_b32 s7, s45, 2\n" \
"v_mfma_f32_16x16x32_bf16 a[108:111], a[216:219], a[236:239], a[108:111]\n" \
"s_or_b32 s5, s5, s7\n" \
"s_lshl_b32 s7, s3, 12\n" \
"s_waitcnt lgkmcnt(15)\n" \
"v_mfma_f32_16x16x32_bf16 a[124:127], a[156:159], a[172:175], a[124:127]\n" \
"s_or_b32 s50, s49, s7\n" \
"s_lshl_b32 s7, s3, 11\n" \
"v_mfma_f32_16x16x32_bf16 a[124:127], a[220:223], a[236:239], a[124:127]\n" \
"v_lshrrev_b32_e64 v215, 4, v20\n" \
"v_bfe_u32 v216, v20, 4, 3\n" \
"v_mfma_f32_16x16x32_bf16 a[120:123], a[156:159], a[168:171], a[120:123]\n" \
"s_or_b32 s42, s42, s4\n" \
"s_lshl_b32 s4, s43, 11\n" \
"v_mfma_f32_16x16x32_bf16 a[120:123], a[220:223], a[232:235], a[120:123]\n" \
"s_add_i32 s52, s48, s7\n" \
"s_add_i32 s53, s53, s7\n" \
"v_mfma_f32_16x16x32_bf16 a[116:119], a[156:159], a[164:167], a[116:119]\n" \
"v_mul_u32_u24_e32 v214, 0x480, v214\n" \
"v_bitop3_b32 v218, v212, v215, 7 bitop3:0x78\n" \
"v_mfma_f32_16x16x32_bf16 a[116:119], a[220:223], a[228:231], a[116:119]\n" \
"v_bitop3_b32 v212, v212, v216, 4 bitop3:0x36\n" \
"s_lshl_b32 s7, s3, 10\n" \
"v_mfma_f32_16x16x32_bf16 a[112:115], a[156:159], a[160:163], a[112:115]\n" \
"s_or_b32 s4, s40, s4\n" \
"v_lshl_or_b32 v25, v212, 4, v214\n" \
"v_mfma_f32_16x16x32_bf16 a[112:115], a[220:223], a[224:227], a[112:115]\n" \
"s_waitcnt vmcnt(1) lgkmcnt(0)\n" \
"s_barrier\n" \
"v_mfma_f32_16x16x32_bf16 v[52:55], a[128:131], a[176:179], v[52:55]\n" \
"s_add_u32 m0, s30, 0x8000\n" \
"s_movk_i32 s84, 0x280\n" \
"v_mfma_f32_16x16x32_bf16 v[52:55], a[192:195], a[240:243], v[52:55]\n" \
"buffer_load_dwordx4 v22, s[64:67], s84 offen offset:0 lds\n" \
"v_mfma_f32_16x16x32_bf16 v[56:59], a[128:131], a[180:183], v[56:59]\n" \
"s_movk_i32 s84, 0x1e80\n" \
"v_mfma_f32_16x16x32_bf16 v[56:59], a[192:195], a[244:247], v[56:59]\n" \
"buffer_load_dwordx4 v23, s[64:67], s84 offen offset:1024 lds\n" \
"v_mfma_f32_16x16x32_bf16 v[60:63], a[128:131], a[184:187], v[60:63]\n" \
"ds_read_b64_tr_b16 a[160:161], v32 offset:0\n" \
"v_mul_u32_u24_e32 v212, 0x480, v217\n" \
"v_mfma_f32_16x16x32_bf16 v[60:63], a[192:195], a[248:251], v[60:63]\n" \
"s_movk_i32 s84, 0x3a80\n" \
"v_mfma_f32_16x16x32_bf16 v[64:67], a[128:131], a[188:191], v[64:67]\n" \
"buffer_load_dwordx4 v22, s[64:67], s84 offen offset:2048 lds\n" \
"v_mfma_f32_16x16x32_bf16 v[64:67], a[192:195], a[252:255], v[64:67]\n" \
"ds_read_b128 a[128:131], v28 offset:16384\n" \
"v_bitop3_b32 v213, v215, v213, 7 bitop3:0x6c\n" \
"v_mfma_f32_16x16x32_bf16 v[80:83], a[132:135], a[188:191], v[80:83]\n" \
"ds_read_b128 a[192:195], v29 offset:16384\n" \
"s_or_b32 s40, s7, 0x14000\n" \
"v_mfma_f32_16x16x32_bf16 v[80:83], a[196:199], a[252:255], v[80:83]\n" \
"s_movk_i32 s84, 0x5680\n" \
"v_mfma_f32_16x16x32_bf16 v[76:79], a[132:135], a[184:187], v[76:79]\n" \
"buffer_load_dwordx4 v23, s[64:67], s84 offen offset:3072 lds\n" \
"v_mfma_f32_16x16x32_bf16 v[76:79], a[196:199], a[248:251], v[76:79]\n" \
"s_add_u32 m0, s31, 0x8000\n" \
"s_mov_b32 s84, 0x280000\n" \
"v_mfma_f32_16x16x32_bf16 v[72:75], a[132:135], a[180:183], v[72:75]\n" \
"buffer_load_dwordx4 v26, s[68:71], s84 offen offset:0 lds\n" \
"v_mfma_f32_16x16x32_bf16 v[72:75], a[196:199], a[244:247], v[72:75]\n" \
"ds_read_b64_tr_b16 a[162:163], v32 offset:1024\n" \
"s_lshl_b32 s43, s44, 13\n" \
"v_mfma_f32_16x16x32_bf16 v[68:71], a[132:135], a[176:179], v[68:71]\n" \
"ds_read_b64_tr_b16 a[224:225], v32 offset:8192\n" \
"s_mulk_i32 s44, 0xe800\n" \
"v_mfma_f32_16x16x32_bf16 v[68:71], a[196:199], a[240:243], v[68:71]\n" \
"ds_read_b128 a[132:135], v28 offset:18432\n" \
"s_lshl_b32 s7, s3, 8\n" \
"v_mfma_f32_16x16x32_bf16 v[84:87], a[136:139], a[176:179], v[84:87]\n" \
"ds_read_b128 a[196:199], v29 offset:18432\n" \
"v_lshl_or_b32 v24, v218, 4, v214\n" \
"v_mfma_f32_16x16x32_bf16 v[84:87], a[200:203], a[240:243], v[84:87]\n" \
"s_addk_i32 s84, 0x7c00\n" \
"v_mfma_f32_16x16x32_bf16 v[88:91], a[136:139], a[180:183], v[88:91]\n" \
"buffer_load_dwordx4 v26, s[68:71], s84 offen offset:1024 lds\n" \
"v_mfma_f32_16x16x32_bf16 v[88:91], a[200:203], a[244:247], v[88:91]\n" \
"s_addk_i32 s84, 0x7c00\n" \
"v_mfma_f32_16x16x32_bf16 v[92:95], a[136:139], a[184:187], v[92:95]\n" \
"buffer_load_dwordx4 v27, s[68:71], s84 offen offset:2048 lds\n" \
"v_mfma_f32_16x16x32_bf16 v[92:95], a[200:203], a[248:251], v[92:95]\n" \
"s_addk_i32 s84, 0x7c00\n" \
"v_mfma_f32_16x16x32_bf16 v[96:99], a[136:139], a[188:191], v[96:99]\n" \
"buffer_load_dwordx4 v27, s[68:71], s84 offen offset:3072 lds\n" \
"v_mfma_f32_16x16x32_bf16 v[96:99], a[200:203], a[252:255], v[96:99]\n" \
"ds_read_b128 a[136:139], v28 offset:20480\n" \
"v_lshl_or_b32 v21, v213, 4, v212\n" \
"v_mfma_f32_16x16x32_bf16 v[112:115], a[140:143], a[188:191], v[112:115]\n" \
"ds_read_b128 a[200:203], v29 offset:20480\n" \
"s_add_i32 s44, s44, 0x17000\n" \
"v_mfma_f32_16x16x32_bf16 v[112:115], a[204:207], a[252:255], v[112:115]\n" \
"s_add_u32 m0, s30, 0x9000\n" \
"s_mov_b32 s84, 0x8280\n" \
"v_mfma_f32_16x16x32_bf16 v[108:111], a[140:143], a[184:187], v[108:111]\n" \
"buffer_load_dwordx4 v22, s[64:67], s84 offen offset:0 lds\n" \
"v_mfma_f32_16x16x32_bf16 v[108:111], a[204:207], a[248:251], v[108:111]\n" \
"s_addk_i32 s84, 0x1c00\n" \
"v_mfma_f32_16x16x32_bf16 v[104:107], a[140:143], a[180:183], v[104:107]\n" \
"buffer_load_dwordx4 v23, s[64:67], s84 offen offset:1024 lds\n" \
"v_mfma_f32_16x16x32_bf16 v[104:107], a[204:207], a[244:247], v[104:107]\n" \
"s_addk_i32 s84, 0x1c00\n" \
"v_mfma_f32_16x16x32_bf16 v[100:103], a[140:143], a[176:179], v[100:103]\n" \
"buffer_load_dwordx4 v22, s[64:67], s84 offen offset:2048 lds\n" \
"v_mfma_f32_16x16x32_bf16 v[100:103], a[204:207], a[240:243], v[100:103]\n" \
"ds_read_b128 a[140:143], v28 offset:22528\n" \
"s_or_b32 s45, s7, 0x27000\n" \
"v_mfma_f32_16x16x32_bf16 v[116:119], a[144:147], a[176:179], v[116:119]\n" \
"ds_read_b128 a[204:207], v29 offset:22528\n" \
"s_or_b32 s46, s4, 0x10000\n" \
"v_mfma_f32_16x16x32_bf16 v[116:119], a[208:211], a[240:243], v[116:119]\n" \
"s_addk_i32 s84, 0x1c00\n" \
"v_mfma_f32_16x16x32_bf16 v[120:123], a[144:147], a[180:183], v[120:123]\n" \
"buffer_load_dwordx4 v23, s[64:67], s84 offen offset:3072 lds\n" \
"v_mfma_f32_16x16x32_bf16 v[120:123], a[208:211], a[244:247], v[120:123]\n" \
"s_add_u32 m0, s31, 0xc000\n" \
"s_mov_b32 s84, 0x280000\n" \
"v_mfma_f32_16x16x32_bf16 v[124:127], a[144:147], a[184:187], v[124:127]\n" \
"buffer_load_dwordx4 v26, s[72:75], s84 offen offset:0 lds\n" \
"v_mfma_f32_16x16x32_bf16 v[124:127], a[208:211], a[248:251], v[124:127]\n" \
"s_addk_i32 s84, 0x7c00\n" \
"v_mfma_f32_16x16x32_bf16 v[128:131], a[144:147], a[188:191], v[128:131]\n" \
"buffer_load_dwordx4 v26, s[72:75], s84 offen offset:1024 lds\n" \
"v_mfma_f32_16x16x32_bf16 v[128:131], a[208:211], a[252:255], v[128:131]\n" \
"ds_read_b128 a[144:147], v28 offset:0\n" \
"s_add_i32 s48, s6, 0x3000000\n" \
"v_mfma_f32_16x16x32_bf16 v[144:147], a[148:151], a[188:191], v[144:147]\n" \
"ds_read_b128 a[208:211], v29 offset:0\n" \
"s_add_i32 s49, s5, 0x3004000\n" \
"v_mfma_f32_16x16x32_bf16 v[144:147], a[212:215], a[252:255], v[144:147]\n" \
"ds_read_b128 v[200:203], v199 offset:0\n" \
"s_add_i32 s52, s52, 0x1010000\n" \
"v_mfma_f32_16x16x32_bf16 v[140:143], a[148:151], a[184:187], v[140:143]\n" \
"ds_read_b128 v[204:207], v199 offset:4096\n" \
"v_mfma_f32_16x16x32_bf16 v[140:143], a[212:215], a[248:251], v[140:143]\n" \
"ds_read_b64_tr_b16 a[226:227], v32 offset:9216\n" \
"v_mfma_f32_16x16x32_bf16 v[136:139], a[148:151], a[180:183], v[136:139]\n" \
"ds_read_b64_tr_b16 a[164:165], v33 offset:0\n" \
"v_mfma_f32_16x16x32_bf16 v[136:139], a[212:215], a[244:247], v[136:139]\n" \
"ds_read_b128 v[208:211], v199 offset:8192\n" \
"v_mfma_f32_16x16x32_bf16 v[132:135], a[148:151], a[176:179], v[132:135]\n" \
"ds_read_b64_tr_b16 a[166:167], v33 offset:1024\n" \
"v_mfma_f32_16x16x32_bf16 v[132:135], a[212:215], a[240:243], v[132:135]\n" \
"ds_read_b128 a[148:151], v28 offset:2048\n" \
"v_mfma_f32_16x16x32_bf16 v[148:151], a[152:155], a[176:179], v[148:151]\n" \
"ds_read_b128 a[212:215], v29 offset:2048\n" \
"v_mfma_f32_16x16x32_bf16 v[148:151], a[216:219], a[240:243], v[148:151]\n" \
"ds_read_b64_tr_b16 a[228:229], v33 offset:8192\n" \
"v_mfma_f32_16x16x32_bf16 v[152:155], a[152:155], a[180:183], v[152:155]\n" \
"ds_read_b64_tr_b16 a[230:231], v33 offset:9216\n" \
"v_mfma_f32_16x16x32_bf16 v[152:155], a[216:219], a[244:247], v[152:155]\n" \
"ds_read_b64_tr_b16 a[168:169], v34 offset:0\n" \
"v_mfma_f32_16x16x32_bf16 v[156:159], a[152:155], a[184:187], v[156:159]\n" \
"ds_read_b64_tr_b16 a[170:171], v34 offset:1024\n" \
"v_mfma_f32_16x16x32_bf16 v[156:159], a[216:219], a[248:251], v[156:159]\n" \
"ds_read_b64_tr_b16 a[232:233], v34 offset:8192\n" \
"v_mfma_f32_16x16x32_bf16 v[160:163], a[152:155], a[188:191], v[160:163]\n" \
"ds_read_b64_tr_b16 a[234:235], v34 offset:9216\n" \
"v_mfma_f32_16x16x32_bf16 v[160:163], a[216:219], a[252:255], v[160:163]\n" \
"ds_read_b128 a[152:155], v28 offset:4096\n" \
"v_mfma_f32_16x16x32_bf16 v[176:179], a[156:159], a[188:191], v[176:179]\n" \
"ds_read_b128 a[216:219], v29 offset:4096\n" \
"v_mfma_f32_16x16x32_bf16 v[176:179], a[220:223], a[252:255], v[176:179]\n" \
"ds_read_b64_tr_b16 a[172:173], v35 offset:0\n" \
"v_mfma_f32_16x16x32_bf16 v[172:175], a[156:159], a[184:187], v[172:175]\n" \
"ds_read_b64_tr_b16 a[174:175], v35 offset:1024\n" \
"v_mfma_f32_16x16x32_bf16 v[172:175], a[220:223], a[248:251], v[172:175]\n" \
"ds_read_b64_tr_b16 a[236:237], v35 offset:8192\n" \
"v_mfma_f32_16x16x32_bf16 v[168:171], a[156:159], a[180:183], v[168:171]\n" \
"ds_read_b64_tr_b16 a[238:239], v35 offset:9216\n" \
"v_mfma_f32_16x16x32_bf16 v[168:171], a[220:223], a[244:247], v[168:171]\n" \
"v_mfma_f32_16x16x32_bf16 v[164:167], a[156:159], a[176:179], v[164:167]\n" \
"v_mfma_f32_16x16x32_bf16 v[164:167], a[220:223], a[240:243], v[164:167]\n" \
"ds_read_b128 a[156:159], v28 offset:6144\n" \
"ds_read_b128 a[220:223], v29 offset:6144\n" \
"s_waitcnt lgkmcnt(15)\n" \
"v_mfma_f32_16x16x32_bf16 a[0:3], a[128:131], a[160:163], a[0:3]\n" \
"s_addk_i32 s84, 0x7c00\n" \
"v_mfma_f32_16x16x32_bf16 a[0:3], a[192:195], a[224:227], a[0:3]\n" \
"buffer_load_dwordx4 v27, s[72:75], s84 offen offset:2048 lds\n" \
"v_mfma_f32_16x16x32_bf16 a[4:7], a[128:131], a[164:167], a[4:7]\n" \
"ds_read_b64_tr_b16 a[176:177], v36 offset:0\n" \
"s_waitcnt lgkmcnt(13)\n" \
"v_mfma_f32_16x16x32_bf16 a[4:7], a[192:195], a[228:231], a[4:7]\n" \
"s_addk_i32 s84, 0x7c00\n" \
"s_waitcnt lgkmcnt(11)\n" \
"v_mfma_f32_16x16x32_bf16 a[8:11], a[128:131], a[168:171], a[8:11]\n" \
"buffer_load_dwordx4 v27, s[72:75], s84 offen offset:3072 lds\n" \
"s_waitcnt lgkmcnt(9)\n" \
"v_mfma_f32_16x16x32_bf16 a[8:11], a[192:195], a[232:235], a[8:11]\n" \
"ds_read_b64_tr_b16 a[178:179], v36 offset:1024\n" \
"s_waitcnt lgkmcnt(6)\n" \
"v_mfma_f32_16x16x32_bf16 a[12:15], a[128:131], a[172:175], a[12:15]\n" \
"ds_read_b64_tr_b16 a[240:241], v36 offset:8192\n" \
"s_waitcnt lgkmcnt(5)\n" \
"v_mfma_f32_16x16x32_bf16 a[12:15], a[192:195], a[236:239], a[12:15]\n" \
"v_mfma_f32_16x16x32_bf16 a[28:31], a[132:135], a[172:175], a[28:31]\n" \
"v_mfma_f32_16x16x32_bf16 a[28:31], a[196:199], a[236:239], a[28:31]\n" \
"ds_read_b64_tr_b16 a[242:243], v36 offset:9216\n" \
"v_mfma_f32_16x16x32_bf16 a[24:27], a[132:135], a[168:171], a[24:27]\n" \
"v_mfma_f32_16x16x32_bf16 a[24:27], a[196:199], a[232:235], a[24:27]\n" \
"v_mfma_f32_16x16x32_bf16 a[20:23], a[132:135], a[164:167], a[20:23]\n" \
"v_mfma_f32_16x16x32_bf16 a[20:23], a[196:199], a[228:231], a[20:23]\n" \
"ds_read_b64_tr_b16 a[180:181], v37 offset:0\n" \
"v_mfma_f32_16x16x32_bf16 a[16:19], a[132:135], a[160:163], a[16:19]\n" \
"v_mfma_f32_16x16x32_bf16 a[16:19], a[196:199], a[224:227], a[16:19]\n" \
"v_mfma_f32_16x16x32_bf16 a[32:35], a[136:139], a[160:163], a[32:35]\n" \
"ds_read_b64_tr_b16 a[182:183], v37 offset:1024\n" \
"v_mfma_f32_16x16x32_bf16 a[32:35], a[200:203], a[224:227], a[32:35]\n" \
"v_mfma_f32_16x16x32_bf16 a[36:39], a[136:139], a[164:167], a[36:39]\n" \
"v_mfma_f32_16x16x32_bf16 a[36:39], a[200:203], a[228:231], a[36:39]\n" \
"buffer_store_dwordx4 v[200:203], v20, s[80:83], s35 offen sc1\n" \
"v_mfma_f32_16x16x32_bf16 a[40:43], a[136:139], a[168:171], a[40:43]\n" \
"s_mov_b32 m0, s32\n" \
"ds_read_b64_tr_b16 a[244:245], v37 offset:8192\n" \
".fill (L_pb + 52960 - .) / 4, 4, 0xBF800000\n" \
"L_pp13:\n" \
"s_cbranch_execz L_pret\n" \
"v_mfma_f32_16x16x32_bf16 a[40:43], a[200:203], a[232:235], a[40:43]\n" \
"s_mov_b32 exec_lo, 0x0\n" \
"s_mov_b32 exec_hi, 0xffffffff\n" \
"global_load_lds_dword v[188:189], off\n" \
"s_mov_b64 exec, -1\n" \
"v_mfma_f32_16x16x32_bf16 a[44:47], a[136:139], a[172:175], a[44:47]\n" \
"ds_read_b64_tr_b16 a[246:247], v37 offset:9216\n" \
"v_mfma_f32_16x16x32_bf16 a[44:47], a[200:203], a[236:239], a[44:47]\n" \
"v_mfma_f32_16x16x32_bf16 a[60:63], a[140:143], a[172:175], a[60:63]\n" \
"v_mfma_f32_16x16x32_bf16 a[60:63], a[204:207], a[236:239], a[60:63]\n" \
"v_mfma_f32_16x16x32_bf16 a[56:59], a[140:143], a[168:171], a[56:59]\n" \
"ds_read_b64_tr_b16 a[184:185], v38 offset:0\n" \
"v_mfma_f32_16x16x32_bf16 a[56:59], a[204:207], a[232:235], a[56:59]\n" \
"v_mfma_f32_16x16x32_bf16 a[52:55], a[140:143], a[164:167], a[52:55]\n" \
"buffer_store_dwordx4 v[204:207], v20, s[80:83], s88 offen sc1\n" \
"v_mfma_f32_16x16x32_bf16 a[52:55], a[204:207], a[228:231], a[52:55]\n" \
"ds_read_b64_tr_b16 a[186:187], v38 offset:1024\n" \
"v_mfma_f32_16x16x32_bf16 a[48:51], a[140:143], a[160:163], a[48:51]\n" \
"v_mfma_f32_16x16x32_bf16 a[48:51], a[204:207], a[224:227], a[48:51]\n" \
"v_mfma_f32_16x16x32_bf16 a[64:67], a[144:147], a[160:163], a[64:67]\n" \
"ds_read_b64_tr_b16 a[248:249], v38 offset:8192\n" \
"v_mfma_f32_16x16x32_bf16 a[64:67], a[208:211], a[224:227], a[64:67]\n" \
"v_mfma_f32_16x16x32_bf16 a[68:71], a[144:147], a[164:167], a[68:71]\n" \
"v_mfma_f32_16x16x32_bf16 a[68:71], a[208:211], a[228:231], a[68:71]\n" \
"ds_read_b64_tr_b16 a[250:251], v38 offset:9216\n" \
"v_mfma_f32_16x16x32_bf16 a[72:75], a[144:147], a[168:171], a[72:75]\n" \
"v_mfma_f32_16x16x32_bf16 a[72:75], a[208:211], a[232:235], a[72:75]\n" \
"v_mfma_f32_16x16x32_bf16 a[76:79], a[144:147], a[172:175], a[76:79]\n" \
"buffer_store_dwordx4 v[208:211], v20, s[80:83], s36 offen sc1\n" \
"v_mfma_f32_16x16x32_bf16 a[76:79], a[208:211], a[236:239], a[76:79]\n" \
"ds_read_b64_tr_b16 a[188:189], v39 offset:0\n" \
"v_mfma_f32_16x16x32_bf16 a[92:95], a[148:151], a[172:175], a[92:95]\n" \
"v_mfma_f32_16x16x32_bf16 a[92:95], a[212:215], a[236:239], a[92:95]\n" \
"v_mfma_f32_16x16x32_bf16 a[88:91], a[148:151], a[168:171], a[88:91]\n" \
"ds_read_b64_tr_b16 a[190:191], v39 offset:1024\n" \
"v_mfma_f32_16x16x32_bf16 a[88:91], a[212:215], a[232:235], a[88:91]\n" \
"v_mfma_f32_16x16x32_bf16 a[84:87], a[148:151], a[164:167], a[84:87]\n" \
"v_mfma_f32_16x16x32_bf16 a[84:87], a[212:215], a[228:231], a[84:87]\n" \
"ds_read_b64_tr_b16 a[252:253], v39 offset:8192\n" \
"v_mfma_f32_16x16x32_bf16 a[80:83], a[148:151], a[160:163], a[80:83]\n" \
"v_mfma_f32_16x16x32_bf16 a[80:83], a[212:215], a[224:227], a[80:83]\n" \
"v_mfma_f32_16x16x32_bf16 a[96:99], a[152:155], a[160:163], a[96:99]\n" \
"ds_read_b64_tr_b16 a[254:255], v39 offset:9216\n" \
"v_mfma_f32_16x16x32_bf16 a[96:99], a[216:219], a[224:227], a[96:99]\n" \
"v_mfma_f32_16x16x32_bf16 a[100:103], a[152:155], a[164:167], a[100:103]\n" \
"v_mfma_f32_16x16x32_bf16 a[100:103], a[216:219], a[228:231], a[100:103]\n" \
"v_mfma_f32_16x16x32_bf16 a[104:107], a[152:155], a[168:171], a[104:107]\n" \
"v_mfma_f32_16x16x32_bf16 a[104:107], a[216:219], a[232:235], a[104:107]\n" \
"v_mfma_f32_16x16x32_bf16 a[108:111], a[152:155], a[172:175], a[108:111]\n" \
"v_mfma_f32_16x16x32_bf16 a[108:111], a[216:219], a[236:239], a[108:111]\n" \
"s_waitcnt lgkmcnt(15)\n" \
"v_mfma_f32_16x16x32_bf16 a[124:127], a[156:159], a[172:175], a[124:127]\n" \
"v_mfma_f32_16x16x32_bf16 a[124:127], a[220:223], a[236:239], a[124:127]\n" \
"v_mfma_f32_16x16x32_bf16 a[120:123], a[156:159], a[168:171], a[120:123]\n" \
"v_mfma_f32_16x16x32_bf16 a[120:123], a[220:223], a[232:235], a[120:123]\n" \
"v_mfma_f32_16x16x32_bf16 a[116:119], a[156:159], a[164:167], a[116:119]\n" \
"v_mfma_f32_16x16x32_bf16 a[116:119], a[220:223], a[228:231], a[116:119]\n" \
"v_mfma_f32_16x16x32_bf16 a[112:115], a[156:159], a[160:163], a[112:115]\n" \
"v_mfma_f32_16x16x32_bf16 a[112:115], a[220:223], a[224:227], a[112:115]\n" \
"s_waitcnt vmcnt(4) lgkmcnt(0)\n" \
"s_barrier\n" \
"v_mfma_f32_16x16x32_bf16 v[52:55], a[128:131], a[176:179], v[52:55]\n" \
"s_add_u32 m0, s30, 0x0\n" \
"s_movk_i32 s84, 0x300\n" \
"v_mfma_f32_16x16x32_bf16 v[52:55], a[192:195], a[240:243], v[52:55]\n" \
"buffer_load_dwordx4 v22, s[64:67], s84 offen offset:0 lds\n" \
"v_mfma_f32_16x16x32_bf16 v[56:59], a[128:131], a[180:183], v[56:59]\n" \
"ds_read_b64_tr_b16 a[160:161], v32 offset:32768\n" \
"v_mfma_f32_16x16x32_bf16 v[56:59], a[192:195], a[244:247], v[56:59]\n" \
"s_movk_i32 s84, 0x1f00\n" \
"v_mfma_f32_16x16x32_bf16 v[60:63], a[128:131], a[184:187], v[60:63]\n" \
"buffer_load_dwordx4 v23, s[64:67], s84 offen offset:1024 lds\n" \
"v_mfma_f32_16x16x32_bf16 v[60:63], a[192:195], a[248:251], v[60:63]\n" \
"ds_read_b64_tr_b16 a[162:163], v32 offset:33792\n" \
"v_mfma_f32_16x16x32_bf16 v[64:67], a[128:131], a[188:191], v[64:67]\n" \
"ds_read_b64_tr_b16 a[224:225], v32 offset:40960\n" \
"v_mfma_f32_16x16x32_bf16 v[64:67], a[192:195], a[252:255], v[64:67]\n" \
"ds_read_b128 a[128:131], v28 offset:49152\n" \
"v_mfma_f32_16x16x32_bf16 v[80:83], a[132:135], a[188:191], v[80:83]\n" \
"ds_read_b128 a[192:195], v29 offset:49152\n" \
"v_mfma_f32_16x16x32_bf16 v[80:83], a[196:199], a[252:255], v[80:83]\n" \
"s_movk_i32 s84, 0x3b00\n" \
"v_mfma_f32_16x16x32_bf16 v[76:79], a[132:135], a[184:187], v[76:79]\n" \
"buffer_load_dwordx4 v22, s[64:67], s84 offen offset:2048 lds\n" \
"v_mfma_f32_16x16x32_bf16 v[76:79], a[196:199], a[248:251], v[76:79]\n" \
"s_movk_i32 s84, 0x5700\n" \
"v_mfma_f32_16x16x32_bf16 v[72:75], a[132:135], a[180:183], v[72:75]\n" \
"buffer_load_dwordx4 v23, s[64:67], s84 offen offset:3072 lds\n" \
"v_mfma_f32_16x16x32_bf16 v[72:75], a[196:199], a[244:247], v[72:75]\n" \
"s_add_u32 m0, s31, 0x0\n" \
"s_mov_b32 s84, 0x300000\n" \
"v_mfma_f32_16x16x32_bf16 v[68:71], a[132:135], a[176:179], v[68:71]\n" \
"buffer_load_dwordx4 v26, s[68:71], s84 offen offset:0 lds\n" \
"v_mfma_f32_16x16x32_bf16 v[68:71], a[196:199], a[240:243], v[68:71]\n" \
"ds_read_b128 a[132:135], v28 offset:51200\n" \
"v_mfma_f32_16x16x32_bf16 v[84:87], a[136:139], a[176:179], v[84:87]\n" \
"ds_read_b128 a[196:199], v29 offset:51200\n" \
"v_mfma_f32_16x16x32_bf16 v[84:87], a[200:203], a[240:243], v[84:87]\n" \
"s_addk_i32 s84, 0x7c00\n" \
"v_mfma_f32_16x16x32_bf16 v[88:91], a[136:139], a[180:183], v[88:91]\n" \
"buffer_load_dwordx4 v26, s[68:71], s84 offen offset:1024 lds\n" \
"v_mfma_f32_16x16x32_bf16 v[88:91], a[200:203], a[244:247], v[88:91]\n" \
"ds_read_b64_tr_b16 a[226:227], v32 offset:41984\n" \
"v_mfma_f32_16x16x32_bf16 v[92:95], a[136:139], a[184:187], v[92:95]\n" \
"s_addk_i32 s84, 0x7c00\n" \
"v_mfma_f32_16x16x32_bf16 v[92:95], a[200:203], a[248:251], v[92:95]\n" \
"buffer_load_dwordx4 v27, s[68:71], s84 offen offset:2048 lds\n" \
"v_mfma_f32_16x16x32_bf16 v[96:99], a[136:139], a[188:191], v[96:99]\n" \
"ds_read_b64_tr_b16 a[164:165], v33 offset:32768\n" \
"v_mfma_f32_16x16x32_bf16 v[96:99], a[200:203], a[252:255], v[96:99]\n" \
"ds_read_b128 a[136:139], v28 offset:53248\n" \
"v_mfma_f32_16x16x32_bf16 v[112:115], a[140:143], a[188:191], v[112:115]\n" \
"ds_read_b128 a[200:203], v29 offset:53248\n" \
"v_mfma_f32_16x16x32_bf16 v[112:115], a[204:207], a[252:255], v[112:115]\n" \
"s_addk_i32 s84, 0x7c00\n" \
"v_mfma_f32_16x16x32_bf16 v[108:111], a[140:143], a[184:187], v[108:111]\n" \
"buffer_load_dwordx4 v27, s[68:71], s84 offen offset:3072 lds\n" \
"v_mfma_f32_16x16x32_bf16 v[108:111], a[204:207], a[248:251], v[108:111]\n" \
"s_add_u32 m0, s30, 0x1000\n" \
"s_mov_b32 s84, 0x8300\n" \
"v_mfma_f32_16x16x32_bf16 v[104:107], a[140:143], a[180:183], v[104:107]\n" \
"buffer_load_dwordx4 v22, s[64:67], s84 offen offset:0 lds\n" \
"v_mfma_f32_16x16x32_bf16 v[104:107], a[204:207], a[244:247], v[104:107]\n" \
"ds_read_b64_tr_b16 a[166:167], v33 offset:33792\n" \
"v_mfma_f32_16x16x32_bf16 v[100:103], a[140:143], a[176:179], v[100:103]\n" \
"ds_read_b64_tr_b16 a[228:229], v33 offset:40960\n" \
"v_mfma_f32_16x16x32_bf16 v[100:103], a[204:207], a[240:243], v[100:103]\n" \
"ds_read_b128 a[140:143], v28 offset:55296\n" \
"v_mfma_f32_16x16x32_bf16 v[116:119], a[144:147], a[176:179], v[116:119]\n" \
"ds_read_b128 a[204:207], v29 offset:55296\n" \
"v_mfma_f32_16x16x32_bf16 v[116:119], a[208:211], a[240:243], v[116:119]\n" \
"s_addk_i32 s84, 0x1c00\n" \
"v_mfma_f32_16x16x32_bf16 v[120:123], a[144:147], a[180:183], v[120:123]\n" \
"buffer_load_dwordx4 v23, s[64:67], s84 offen offset:1024 lds\n" \
"v_mfma_f32_16x16x32_bf16 v[120:123], a[208:211], a[244:247], v[120:123]\n" \
"s_addk_i32 s84, 0x1c00\n" \
"v_mfma_f32_16x16x32_bf16 v[124:127], a[144:147], a[184:187], v[124:127]\n" \
"buffer_load_dwordx4 v22, s[64:67], s84 offen offset:2048 lds\n" \
"v_mfma_f32_16x16x32_bf16 v[124:127], a[208:211], a[248:251], v[124:127]\n" \
"s_addk_i32 s84, 0x1c00\n" \
"s_nop 0\n" \
"v_mfma_f32_16x16x32_bf16 v[128:131], a[144:147], a[188:191], v[128:131]\n" \
"buffer_load_dwordx4 v23, s[64:67], s84 offen offset:3072 lds\n" \
"v_mfma_f32_16x16x32_bf16 v[128:131], a[208:211], a[252:255], v[128:131]\n" \
"ds_read_b128 a[144:147], v28 offset:32768\n" \
"v_mfma_f32_16x16x32_bf16 v[144:147], a[148:151], a[188:191], v[144:147]\n" \
"ds_read_b128 a[208:211], v29 offset:32768\n" \
"v_mfma_f32_16x16x32_bf16 v[144:147], a[212:215], a[252:255], v[144:147]\n" \
"s_add_u32 m0, s31, 0x4000\n" \
"s_mov_b32 s84, 0x300000\n" \
"v_mfma_f32_16x16x32_bf16 v[140:143], a[148:151], a[184:187], v[140:143]\n" \
"buffer_load_dwordx4 v26, s[72:75], s84 offen offset:0 lds\n" \
"v_mfma_f32_16x16x32_bf16 v[140:143], a[212:215], a[248:251], v[140:143]\n" \
"ds_read_b64_tr_b16 a[230:231], v33 offset:41984\n" \
"v_mfma_f32_16x16x32_bf16 v[136:139], a[148:151], a[180:183], v[136:139]\n" \
"s_addk_i32 s84, 0x7c00\n" \
"s_nop 0\n" \
"v_mfma_f32_16x16x32_bf16 v[136:139], a[212:215], a[244:247], v[136:139]\n" \
"buffer_load_dwordx4 v26, s[72:75], s84 offen offset:1024 lds\n" \
"v_mfma_f32_16x16x32_bf16 v[132:135], a[148:151], a[176:179], v[132:135]\n" \
"ds_read_b64_tr_b16 a[168:169], v34 offset:32768\n" \
"v_mfma_f32_16x16x32_bf16 v[132:135], a[212:215], a[240:243], v[132:135]\n" \
"ds_read_b128 a[148:151], v28 offset:34816\n" \
"v_mfma_f32_16x16x32_bf16 v[148:151], a[152:155], a[176:179], v[148:151]\n" \
"ds_read_b128 a[212:215], v29 offset:34816\n" \
"v_mfma_f32_16x16x32_bf16 v[148:151], a[216:219], a[240:243], v[148:151]\n" \
"ds_read_b64_tr_b16 a[170:171], v34 offset:33792\n" \
"v_mfma_f32_16x16x32_bf16 v[152:155], a[152:155], a[180:183], v[152:155]\n" \
"ds_read_b64_tr_b16 a[232:233], v34 offset:40960\n" \
"v_mfma_f32_16x16x32_bf16 v[152:155], a[216:219], a[244:247], v[152:155]\n" \
"ds_read_b64_tr_b16 a[234:235], v34 offset:41984\n" \
"v_mfma_f32_16x16x32_bf16 v[156:159], a[152:155], a[184:187], v[156:159]\n" \
"ds_read_b64_tr_b16 a[172:173], v35 offset:32768\n" \
"v_mfma_f32_16x16x32_bf16 v[156:159], a[216:219], a[248:251], v[156:159]\n" \
"ds_read_b64_tr_b16 a[174:175], v35 offset:33792\n" \
"v_mfma_f32_16x16x32_bf16 v[160:163], a[152:155], a[188:191], v[160:163]\n" \
"ds_read_b64_tr_b16 a[236:237], v35 offset:40960\n" \
"v_mfma_f32_16x16x32_bf16 v[160:163], a[216:219], a[252:255], v[160:163]\n" \
"ds_read_b128 a[152:155], v28 offset:36864\n" \
"v_mfma_f32_16x16x32_bf16 v[176:179], a[156:159], a[188:191], v[176:179]\n" \
"ds_read_b128 a[216:219], v29 offset:36864\n" \
"v_mfma_f32_16x16x32_bf16 v[176:179], a[220:223], a[252:255], v[176:179]\n" \
"ds_read_b64_tr_b16 a[238:239], v35 offset:41984\n" \
"v_mfma_f32_16x16x32_bf16 v[172:175], a[156:159], a[184:187], v[172:175]\n" \
"v_mfma_f32_16x16x32_bf16 v[172:175], a[220:223], a[248:251], v[172:175]\n" \
"v_mfma_f32_16x16x32_bf16 v[168:171], a[156:159], a[180:183], v[168:171]\n" \
"v_mfma_f32_16x16x32_bf16 v[168:171], a[220:223], a[244:247], v[168:171]\n" \
"v_mfma_f32_16x16x32_bf16 v[164:167], a[156:159], a[176:179], v[164:167]\n" \
"v_mfma_f32_16x16x32_bf16 v[164:167], a[220:223], a[240:243], v[164:167]\n" \
"ds_read_b128 a[156:159], v28 offset:38912\n" \
"ds_read_b128 a[220:223], v29 offset:38912\n" \
"s_waitcnt lgkmcnt(15)\n" \
"v_mfma_f32_16x16x32_bf16 a[0:3], a[128:131], a[160:163], a[0:3]\n" \
"s_addk_i32 s84, 0x7c00\n" \
"v_mfma_f32_16x16x32_bf16 a[0:3], a[192:195], a[224:227], a[0:3]\n" \
"buffer_load_dwordx4 v27, s[72:75], s84 offen offset:2048 lds\n" \
"v_mfma_f32_16x16x32_bf16 a[4:7], a[128:131], a[164:167], a[4:7]\n" \
"ds_read_b64_tr_b16 a[176:177], v36 offset:32768\n" \
"s_waitcnt lgkmcnt(15)\n" \
"v_mfma_f32_16x16x32_bf16 a[4:7], a[192:195], a[228:231], a[4:7]\n" \
"s_addk_i32 s84, 0x7c00\n" \
"s_waitcnt lgkmcnt(11)\n" \
"v_mfma_f32_16x16x32_bf16 a[8:11], a[128:131], a[168:171], a[8:11]\n" \
"buffer_load_dwordx4 v27, s[72:75], s84 offen offset:3072 lds\n" \
"s_waitcnt lgkmcnt(9)\n" \
"v_mfma_f32_16x16x32_bf16 a[8:11], a[192:195], a[232:235], a[8:11]\n" \
"ds_read_b64_tr_b16 a[178:179], v36 offset:33792\n" \
"s_waitcnt lgkmcnt(8)\n" \
"v_mfma_f32_16x16x32_bf16 a[12:15], a[128:131], a[172:175], a[12:15]\n" \
"ds_read_b64_tr_b16 a[240:241], v36 offset:40960\n" \
"s_waitcnt lgkmcnt(5)\n" \
"v_mfma_f32_16x16x32_bf16 a[12:15], a[192:195], a[236:239], a[12:15]\n" \
"v_mfma_f32_16x16x32_bf16 a[28:31], a[132:135], a[172:175], a[28:31]\n" \
"v_mfma_f32_16x16x32_bf16 a[28:31], a[196:199], a[236:239], a[28:31]\n" \
"ds_read_b64_tr_b16 a[242:243], v36 offset:41984\n" \
"v_mfma_f32_16x16x32_bf16 a[24:27], a[132:135], a[168:171], a[24:27]\n" \
"v_mfma_f32_16x16x32_bf16 a[24:27], a[196:199], a[232:235], a[24:27]\n" \
"v_mfma_f32_16x16x32_bf16 a[20:23], a[132:135], a[164:167], a[20:23]\n" \
"v_mfma_f32_16x16x32_bf16 a[20:23], a[196:199], a[228:231], a[20:23]\n" \
"ds_read_b64_tr_b16 a[180:181], v37 offset:32768\n" \
"v_mfma_f32_16x16x32_bf16 a[16:19], a[132:135], a[160:163], a[16:19]\n" \
"v_mfma_f32_16x16x32_bf16 a[16:19], a[196:199], a[224:227], a[16:19]\n" \
"v_mfma_f32_16x16x32_bf16 a[32:35], a[136:139], a[160:163], a[32:35]\n" \
"ds_read_b64_tr_b16 a[182:183], v37 offset:33792\n" \
"v_mfma_f32_16x16x32_bf16 a[32:35], a[200:203], a[224:227], a[32:35]\n" \
"v_mfma_f32_16x16x32_bf16 a[36:39], a[136:139], a[164:167], a[36:39]\n" \
"v_lshlrev_b32_e32 v45, 3, v20\n" \
"v_add_u32_e32 v45, s31, v45\n" \
"v_mfma_f32_16x16x32_bf16 a[36:39], a[200:203], a[228:231], a[36:39]\n" \
"s_getpc_b64 s[86:87]\n" \
"s_sub_u32 s86, s86, 0x10800\n" \
"s_subb_u32 s87, s87, 0\n" \
"s_mov_b32 m0, s32\n" \
"s_nop 0\n" \
"v_mfma_f32_16x16x32_bf16 a[40:43], a[136:139], a[168:171], a[40:43]\n" \
"global_load_lds_dword v45, s[86:87]\n" \
"v_mfma_f32_16x16x32_bf16 a[40:43], a[200:203], a[232:235], a[40:43]\n" \
"ds_read_b64_tr_b16 a[244:245], v37 offset:40960\n" \
"v_mfma_f32_16x16x32_bf16 a[44:47], a[136:139], a[172:175], a[44:47]\n" \
"ds_read_b64_tr_b16 a[246:247], v37 offset:41984\n" \
"v_mfma_f32_16x16x32_bf16 a[44:47], a[200:203], a[236:239], a[44:47]\n" \
"v_mfma_f32_16x16x32_bf16 a[60:63], a[140:143], a[172:175], a[60:63]\n" \
"v_mfma_f32_16x16x32_bf16 a[60:63], a[204:207], a[236:239], a[60:63]\n" \
"v_mfma_f32_16x16x32_bf16 a[56:59], a[140:143], a[168:171], a[56:59]\n" \
"ds_read_b64_tr_b16 a[184:185], v38 offset:32768\n" \
"v_mfma_f32_16x16x32_bf16 a[56:59], a[204:207], a[232:235], a[56:59]\n" \
"v_mfma_f32_16x16x32_bf16 a[52:55], a[140:143], a[164:167], a[52:55]\n" \
"v_mfma_f32_16x16x32_bf16 a[52:55], a[204:207], a[228:231], a[52:55]\n" \
"ds_read_b64_tr_b16 a[186:187], v38 offset:33792\n" \
"v_mfma_f32_16x16x32_bf16 a[48:51], a[140:143], a[160:163], a[48:51]\n" \
"v_mfma_f32_16x16x32_bf16 a[48:51], a[204:207], a[224:227], a[48:51]\n" \
"v_mfma_f32_16x16x32_bf16 a[64:67], a[144:147], a[160:163], a[64:67]\n" \
"ds_read_b64_tr_b16 a[248:249], v38 offset:40960\n" \
"v_mfma_f32_16x16x32_bf16 a[64:67], a[208:211], a[224:227], a[64:67]\n" \
"v_mfma_f32_16x16x32_bf16 a[68:71], a[144:147], a[164:167], a[68:71]\n" \
"v_mfma_f32_16x16x32_bf16 a[68:71], a[208:211], a[228:231], a[68:71]\n" \
"ds_read_b64_tr_b16 a[250:251], v38 offset:41984\n" \
"v_mfma_f32_16x16x32_bf16 a[72:75], a[144:147], a[168:171], a[72:75]\n" \
"v_mfma_f32_16x16x32_bf16 a[72:75], a[208:211], a[232:235], a[72:75]\n" \
"v_mfma_f32_16x16x32_bf16 a[76:79], a[144:147], a[172:175], a[76:79]\n" \
"v_mfma_f32_16x16x32_bf16 a[76:79], a[208:211], a[236:239], a[76:79]\n" \
"ds_read_b64_tr_b16 a[188:189], v39 offset:32768\n" \
"v_mfma_f32_16x16x32_bf16 a[92:95], a[148:151], a[172:175], a[92:95]\n" \
"v_mfma_f32_16x16x32_bf16 a[92:95], a[212:215], a[236:239], a[92:95]\n" \
"v_mfma_f32_16x16x32_bf16 a[88:91], a[148:151], a[168:171], a[88:91]\n" \
"ds_read_b64_tr_b16 a[190:191], v39 offset:33792\n" \
"v_mfma_f32_16x16x32_bf16 a[88:91], a[212:215], a[232:235], a[88:91]\n" \
"v_mfma_f32_16x16x32_bf16 a[84:87], a[148:151], a[164:167], a[84:87]\n" \
"v_mfma_f32_16x16x32_bf16 a[84:87], a[212:215], a[228:231], a[84:87]\n" \
"ds_read_b64_tr_b16 a[252:253], v39 offset:40960\n" \
"v_mfma_f32_16x16x32_bf16 a[80:83], a[148:151], a[160:163], a[80:83]\n" \
"v_mfma_f32_16x16x32_bf16 a[80:83], a[212:215], a[224:227], a[80:83]\n" \
"v_mfma_f32_16x16x32_bf16 a[96:99], a[152:155], a[160:163], a[96:99]\n" \
"ds_read_b64_tr_b16 a[254:255], v39 offset:41984\n" \
"v_mfma_f32_16x16x32_bf16 a[96:99], a[216:219], a[224:227], a[96:99]\n" \
"v_mfma_f32_16x16x32_bf16 a[100:103], a[152:155], a[164:167], a[100:103]\n" \
"v_mfma_f32_16x16x32_bf16 a[100:103], a[216:219], a[228:231], a[100:103]\n" \
"v_mfma_f32_16x16x32_bf16 a[104:107], a[152:155], a[168:171], a[104:107]\n" \
"v_mfma_f32_16x16x32_bf16 a[104:107], a[216:219], a[232:235], a[104:107]\n" \
"v_mfma_f32_16x16x32_bf16 a[108:111], a[152:155], a[172:175], a[108:111]\n" \
"v_mfma_f32_16x16x32_bf16 a[108:111], a[216:219], a[236:239], a[108:111]\n" \
"s_waitcnt lgkmcnt(15)\n" \
"v_mfma_f32_16x16x32_bf16 a[124:127], a[156:159], a[172:175], a[124:127]\n" \
"v_mfma_f32_16x16x32_bf16 a[124:127], a[220:223], a[236:239], a[124:127]\n" \
"v_mfma_f32_16x16x32_bf16 a[120:123], a[156:159], a[168:171], a[120:123]\n" \
"v_mfma_f32_16x16x32_bf16 a[120:123], a[220:223], a[232:235], a[120:123]\n" \
"v_mfma_f32_16x16x32_bf16 a[116:119], a[156:159], a[164:167], a[116:119]\n" \
"v_mfma_f32_16x16x32_bf16 a[116:119], a[220:223], a[228:231], a[116:119]\n" \
"v_mfma_f32_16x16x32_bf16 a[112:115], a[156:159], a[160:163], a[112:115]\n" \
"v_mfma_f32_16x16x32_bf16 a[112:115], a[220:223], a[224:227], a[112:115]\n" \
"s_waitcnt vmcnt(1) lgkmcnt(0)\n" \
"s_barrier\n" \
"v_mov_b32_e32 v45, s62\n" \
"s_mov_b64 exec, 1\n" \
"buffer_store_dword v45, off, s[80:83], s37 offset:128 sc1\n" \
"s_mov_b64 exec, -1\n" \
"v_mfma_f32_16x16x32_bf16 v[52:55], a[128:131], a[176:179], v[52:55]\n" \
"s_add_u32 m0, s30, 0x8000\n" \
"s_movk_i32 s84, 0x380\n" \
"v_mfma_f32_16x16x32_bf16 v[52:55], a[192:195], a[240:243], v[52:55]\n" \
"buffer_load_dwordx4 v22, s[64:67], s84 offen offset:0 lds\n" \
"v_mfma_f32_16x16x32_bf16 v[56:59], a[128:131], a[180:183], v[56:59]\n" \
"ds_read_b64_tr_b16 a[160:161], v32 offset:0\n" \
"v_mfma_f32_16x16x32_bf16 v[56:59], a[192:195], a[244:247], v[56:59]\n" \
"s_movk_i32 s84, 0x1f80\n" \
"v_mfma_f32_16x16x32_bf16 v[60:63], a[128:131], a[184:187], v[60:63]\n" \
"buffer_load_dwordx4 v23, s[64:67], s84 offen offset:1024 lds\n" \
"v_mfma_f32_16x16x32_bf16 v[60:63], a[192:195], a[248:251], v[60:63]\n" \
"ds_read_b64_tr_b16 a[162:163], v32 offset:1024\n" \
"v_mfma_f32_16x16x32_bf16 v[64:67], a[128:131], a[188:191], v[64:67]\n" \
"ds_read_b64_tr_b16 a[224:225], v32 offset:8192\n" \
"v_mfma_f32_16x16x32_bf16 v[64:67], a[192:195], a[252:255], v[64:67]\n" \
"ds_read_b128 a[128:131], v28 offset:16384\n" \
"v_mfma_f32_16x16x32_bf16 v[80:83], a[132:135], a[188:191], v[80:83]\n" \
"ds_read_b128 a[192:195], v29 offset:16384\n" \
"v_mfma_f32_16x16x32_bf16 v[80:83], a[196:199], a[252:255], v[80:83]\n" \
"s_movk_i32 s84, 0x3b80\n" \
"v_mfma_f32_16x16x32_bf16 v[76:79], a[132:135], a[184:187], v[76:79]\n" \
"buffer_load_dwordx4 v22, s[64:67], s84 offen offset:2048 lds\n" \
"v_mfma_f32_16x16x32_bf16 v[76:79], a[196:199], a[248:251], v[76:79]\n" \
"s_movk_i32 s84, 0x5780\n" \
"v_mfma_f32_16x16x32_bf16 v[72:75], a[132:135], a[180:183], v[72:75]\n" \
"buffer_load_dwordx4 v23, s[64:67], s84 offen offset:3072 lds\n" \
"v_mfma_f32_16x16x32_bf16 v[72:75], a[196:199], a[244:247], v[72:75]\n" \
"s_add_u32 m0, s31, 0x8000\n" \
"s_mov_b32 s84, 0x380000\n" \
"v_mfma_f32_16x16x32_bf16 v[68:71], a[132:135], a[176:179], v[68:71]\n" \
"buffer_load_dwordx4 v26, s[68:71], s84 offen offset:0 lds\n" \
"v_mfma_f32_16x16x32_bf16 v[68:71], a[196:199], a[240:243], v[68:71]\n" \
"ds_read_b128 a[132:135], v28 offset:18432\n" \
"v_mfma_f32_16x16x32_bf16 v[84:87], a[136:139], a[176:179], v[84:87]\n" \
"ds_read_b128 a[196:199], v29 offset:18432\n" \
"v_mfma_f32_16x16x32_bf16 v[84:87], a[200:203], a[240:243], v[84:87]\n" \
"s_addk_i32 s84, 0x7c00\n" \
"v_mfma_f32_16x16x32_bf16 v[88:91], a[136:139], a[180:183], v[88:91]\n" \
"buffer_load_dwordx4 v26, s[68:71], s84 offen offset:1024 lds\n" \
"v_mfma_f32_16x16x32_bf16 v[88:91], a[200:203], a[244:247], v[88:91]\n" \
"ds_read_b64_tr_b16 a[226:227], v32 offset:9216\n" \
"v_mfma_f32_16x16x32_bf16 v[92:95], a[136:139], a[184:187], v[92:95]\n" \
"s_addk_i32 s84, 0x7c00\n" \
"v_mfma_f32_16x16x32_bf16 v[92:95], a[200:203], a[248:251], v[92:95]\n" \
"buffer_load_dwordx4 v27, s[68:71], s84 offen offset:2048 lds\n" \
"v_mfma_f32_16x16x32_bf16 v[96:99], a[136:139], a[188:191], v[96:99]\n" \
"ds_read_b64_tr_b16 a[164:165], v33 offset:0\n" \
"v_mfma_f32_16x16x32_bf16 v[96:99], a[200:203], a[252:255], v[96:99]\n" \
"ds_read_b128 a[136:139], v28 offset:20480\n" \
"v_mfma_f32_16x16x32_bf16 v[112:115], a[140:143], a[188:191], v[112:115]\n" \
"ds_read_b128 a[200:203], v29 offset:20480\n" \
"v_mfma_f32_16x16x32_bf16 v[112:115], a[204:207], a[252:255], v[112:115]\n" \
"s_addk_i32 s84, 0x7c00\n" \
"s_nop 0\n" \
"v_mfma_f32_16x16x32_bf16 v[108:111], a[140:143], a[184:187], v[108:111]\n" \
"buffer_load_dwordx4 v27, s[68:71], s84 offen offset:3072 lds\n" \
"v_mfma_f32_16x16x32_bf16 v[108:111], a[204:207], a[248:251], v[108:111]\n" \
"s_add_u32 m0, s30, 0x9000\n" \
"s_mov_b32 s84, 0x8380\n" \
"v_mfma_f32_16x16x32_bf16 v[104:107], a[140:143], a[180:183], v[104:107]\n" \
"buffer_load_dwordx4 v22, s[64:67], s84 offen offset:0 lds\n" \
"v_mfma_f32_16x16x32_bf16 v[104:107], a[204:207], a[244:247], v[104:107]\n" \
"ds_read_b64_tr_b16 a[166:167], v33 offset:1024\n" \
"v_mfma_f32_16x16x32_bf16 v[100:103], a[140:143], a[176:179], v[100:103]\n" \
"ds_read_b64_tr_b16 a[228:229], v33 offset:8192\n" \
"v_mfma_f32_16x16x32_bf16 v[100:103], a[204:207], a[240:243], v[100:103]\n" \
"ds_read_b128 a[140:143], v28 offset:22528\n" \
"v_mfma_f32_16x16x32_bf16 v[116:119], a[144:147], a[176:179], v[116:119]\n" \
"ds_read_b128 a[204:207], v29 offset:22528\n" \
"v_mfma_f32_16x16x32_bf16 v[116:119], a[208:211], a[240:243], v[116:119]\n" \
"s_addk_i32 s84, 0x1c00\n" \
"v_mfma_f32_16x16x32_bf16 v[120:123], a[144:147], a[180:183], v[120:123]\n" \
"buffer_load_dwordx4 v23, s[64:67], s84 offen offset:1024 lds\n" \
"v_mfma_f32_16x16x32_bf16 v[120:123], a[208:211], a[244:247], v[120:123]\n" \
"s_addk_i32 s84, 0x1c00\n" \
"v_mfma_f32_16x16x32_bf16 v[124:127], a[144:147], a[184:187], v[124:127]\n" \
"buffer_load_dwordx4 v22, s[64:67], s84 offen offset:2048 lds\n" \
"v_mfma_f32_16x16x32_bf16 v[124:127], a[208:211], a[248:251], v[124:127]\n" \
"s_addk_i32 s84, 0x1c00\n" \
"s_nop 0\n" \
"v_mfma_f32_16x16x32_bf16 v[128:131], a[144:147], a[188:191], v[128:131]\n" \
"buffer_load_dwordx4 v23, s[64:67], s84 offen offset:3072 lds\n" \
"v_mfma_f32_16x16x32_bf16 v[128:131], a[208:211], a[252:255], v[128:131]\n" \
"ds_read_b128 a[144:147], v28 offset:0\n" \
"v_mfma_f32_16x16x32_bf16 v[144:147], a[148:151], a[188:191], v[144:147]\n" \
"ds_read_b128 a[208:211], v29 offset:0\n" \
"v_mfma_f32_16x16x32_bf16 v[144:147], a[212:215], a[252:255], v[144:147]\n" \
"s_add_u32 m0, s31, 0xc000\n" \
"s_mov_b32 s84, 0x380000\n" \
"v_mfma_f32_16x16x32_bf16 v[140:143], a[148:151], a[184:187], v[140:143]\n" \
"buffer_load_dwordx4 v26, s[72:75], s84 offen offset:0 lds\n" \
"v_mfma_f32_16x16x32_bf16 v[140:143], a[212:215], a[248:251], v[140:143]\n" \
"ds_read_b64_tr_b16 a[230:231], v33 offset:9216\n" \
"v_mfma_f32_16x16x32_bf16 v[136:139], a[148:151], a[180:183], v[136:139]\n" \
"s_addk_i32 s84, 0x7c00\n" \
"s_nop 0\n" \
"v_mfma_f32_16x16x32_bf16 v[136:139], a[212:215], a[244:247], v[136:139]\n" \
"buffer_load_dwordx4 v26, s[72:75], s84 offen offset:1024 lds\n" \
"v_mfma_f32_16x16x32_bf16 v[132:135], a[148:151], a[176:179], v[132:135]\n" \
"ds_read_b64_tr_b16 a[168:169], v34 offset:0\n" \
"v_mfma_f32_16x16x32_bf16 v[132:135], a[212:215], a[240:243], v[132:135]\n" \
"ds_read_b128 a[148:151], v28 offset:2048\n" \
"v_mfma_f32_16x16x32_bf16 v[148:151], a[152:155], a[176:179], v[148:151]\n" \
"ds_read_b128 a[212:215], v29 offset:2048\n" \
"v_mfma_f32_16x16x32_bf16 v[148:151], a[216:219], a[240:243], v[148:151]\n" \
"ds_read_b64_tr_b16 a[170:171], v34 offset:1024\n" \
"v_mfma_f32_16x16x32_bf16 v[152:155], a[152:155], a[180:183], v[152:155]\n" \
"ds_read_b64_tr_b16 a[232:233], v34 offset:8192\n" \
"v_mfma_f32_16x16x32_bf16 v[152:155], a[216:219], a[244:247], v[152:155]\n" \
"ds_read_b64_tr_b16 a[234:235], v34 offset:9216\n" \
"v_mfma_f32_16x16x32_bf16 v[156:159], a[152:155], a[184:187], v[156:159]\n" \
"ds_read_b64_tr_b16 a[172:173], v35 offset:0\n" \
"v_mfma_f32_16x16x32_bf16 v[156:159], a[216:219], a[248:251], v[156:159]\n" \
"ds_read_b64_tr_b16 a[174:175], v35 offset:1024\n" \
"v_mfma_f32_16x16x32_bf16 v[160:163], a[152:155], a[188:191], v[160:163]\n" \
"ds_read_b64_tr_b16 a[236:237], v35 offset:8192\n" \
"v_mfma_f32_16x16x32_bf16 v[160:163], a[216:219], a[252:255], v[160:163]\n" \
"ds_read_b128 a[152:155], v28 offset:4096\n" \
"v_mfma_f32_16x16x32_bf16 v[176:179], a[156:159], a[188:191], v[176:179]\n" \
"ds_read_b128 a[216:219], v29 offset:4096\n" \
"v_mfma_f32_16x16x32_bf16 v[176:179], a[220:223], a[252:255], v[176:179]\n" \
"ds_read_b64_tr_b16 a[238:239], v35 offset:9216\n" \
"v_mfma_f32_16x16x32_bf16 v[172:175], a[156:159], a[184:187], v[172:175]\n" \
"v_mfma_f32_16x16x32_bf16 v[172:175], a[220:223], a[248:251], v[172:175]\n" \
"v_mfma_f32_16x16x32_bf16 v[168:171], a[156:159], a[180:183], v[168:171]\n" \
"v_mfma_f32_16x16x32_bf16 v[168:171], a[220:223], a[244:247], v[168:171]\n" \
"v_mfma_f32_16x16x32_bf16 v[164:167], a[156:159], a[176:179], v[164:167]\n" \
"v_mfma_f32_16x16x32_bf16 v[164:167], a[220:223], a[240:243], v[164:167]\n" \
"ds_read_b128 a[156:159], v28 offset:6144\n" \
"ds_read_b128 a[220:223], v29 offset:6144\n" \
"s_waitcnt lgkmcnt(15)\n" \
"v_mfma_f32_16x16x32_bf16 a[0:3], a[128:131], a[160:163], a[0:3]\n" \
"s_addk_i32 s84, 0x7c00\n" \
"v_mfma_f32_16x16x32_bf16 a[0:3], a[192:195], a[224:227], a[0:3]\n" \
"buffer_load_dwordx4 v27, s[72:75], s84 offen offset:2048 lds\n" \
"v_mfma_f32_16x16x32_bf16 a[4:7], a[128:131], a[164:167], a[4:7]\n" \
"ds_read_b64_tr_b16 a[176:177], v36 offset:0\n" \
"s_waitcnt lgkmcnt(15)\n" \
"v_mfma_f32_16x16x32_bf16 a[4:7], a[192:195], a[228:231], a[4:7]\n" \
"s_addk_i32 s84, 0x7c00\n" \
"s_waitcnt lgkmcnt(11)\n" \
"v_mfma_f32_16x16x32_bf16 a[8:11], a[128:131], a[168:171], a[8:11]\n" \
"buffer_load_dwordx4 v27, s[72:75], s84 offen offset:3072 lds\n" \
"s_waitcnt lgkmcnt(9)\n" \
"v_mfma_f32_16x16x32_bf16 a[8:11], a[192:195], a[232:235], a[8:11]\n" \
"ds_read_b64_tr_b16 a[178:179], v36 offset:1024\n" \
"s_waitcnt lgkmcnt(8)\n" \
"v_mfma_f32_16x16x32_bf16 a[12:15], a[128:131], a[172:175], a[12:15]\n" \
"ds_read_b64_tr_b16 a[240:241], v36 offset:8192\n" \
"s_waitcnt lgkmcnt(5)\n" \
"v_mfma_f32_16x16x32_bf16 a[12:15], a[192:195], a[236:239], a[12:15]\n" \
"v_mfma_f32_16x16x32_bf16 a[28:31], a[132:135], a[172:175], a[28:31]\n" \
"v_mfma_f32_16x16x32_bf16 a[28:31], a[196:199], a[236:239], a[28:31]\n" \
"ds_read_b64_tr_b16 a[242:243], v36 offset:9216\n" \
"v_mfma_f32_16x16x32_bf16 a[24:27], a[132:135], a[168:171], a[24:27]\n" \
"v_mfma_f32_16x16x32_bf16 a[24:27], a[196:199], a[232:235], a[24:27]\n" \
"v_mfma_f32_16x16x32_bf16 a[20:23], a[132:135], a[164:167], a[20:23]\n" \
"v_mfma_f32_16x16x32_bf16 a[20:23], a[196:199], a[228:231], a[20:23]\n" \
"ds_read_b64_tr_b16 a[180:181], v37 offset:0\n" \
"v_mfma_f32_16x16x32_bf16 a[16:19], a[132:135], a[160:163], a[16:19]\n" \
"v_mfma_f32_16x16x32_bf16 a[16:19], a[196:199], a[224:227], a[16:19]\n" \
"v_mfma_f32_16x16x32_bf16 a[32:35], a[136:139], a[160:163], a[32:35]\n" \
"v_bfe_u32 v44, v20, 2, 5\n" \
"ds_read_b64_tr_b16 a[182:183], v37 offset:1024\n" \
"v_mfma_f32_16x16x32_bf16 a[32:35], a[200:203], a[224:227], a[32:35]\n" \
"buffer_load_dword v44, v44, s[80:83], s38 offen offset:128 sc1\n" \
"v_mfma_f32_16x16x32_bf16 a[36:39], a[136:139], a[164:167], a[36:39]\n" \
"v_mfma_f32_16x16x32_bf16 a[36:39], a[200:203], a[228:231], a[36:39]\n" \
"ds_read_b64_tr_b16 a[244:245], v37 offset:8192\n" \
"v_mfma_f32_16x16x32_bf16 a[40:43], a[136:139], a[168:171], a[40:43]\n" \
"v_mfma_f32_16x16x32_bf16 a[40:43], a[200:203], a[232:235], a[40:43]\n" \
"v_mfma_f32_16x16x32_bf16 a[44:47], a[136:139], a[172:175], a[44:47]\n" \
"ds_read_b64_tr_b16 a[246:247], v37 offset:9216\n" \
"v_mfma_f32_16x16x32_bf16 a[44:47], a[200:203], a[236:239], a[44:47]\n" \
"v_mfma_f32_16x16x32_bf16 a[60:63], a[140:143], a[172:175], a[60:63]\n" \
"global_store_dwordx4 v49, v[0:3], s[28:29] offset:0 sc1\n" \
"v_mfma_f32_16x16x32_bf16 a[60:63], a[204:207], a[236:239], a[60:63]\n" \
"v_mfma_f32_16x16x32_bf16 a[56:59], a[140:143], a[168:171], a[56:59]\n" \
"ds_read_b64_tr_b16 a[184:185], v38 offset:0\n" \
"v_mfma_f32_16x16x32_bf16 a[56:59], a[204:207], a[232:235], a[56:59]\n" \
"v_mfma_f32_16x16x32_bf16 a[52:55], a[140:143], a[164:167], a[52:55]\n" \
"v_mfma_f32_16x16x32_bf16 a[52:55], a[204:207], a[228:231], a[52:55]\n" \
"ds_read_b64_tr_b16 a[186:187], v38 offset:1024\n" \
"v_mfma_f32_16x16x32_bf16 a[48:51], a[140:143], a[160:163], a[48:51]\n" \
"v_mfma_f32_16x16x32_bf16 a[48:51], a[204:207], a[224:227], a[48:51]\n" \
"v_mfma_f32_16x16x32_bf16 a[64:67], a[144:147], a[160:163], a[64:67]\n" \
"global_store_dwordx4 v49, v[0:3], s[28:29] offset:1024 sc1\n" \
"v_mfma_f32_16x16x32_bf16 a[64:67], a[208:211], a[224:227], a[64:67]\n" \
"ds_read_b64_tr_b16 a[248:249], v38 offset:8192\n" \
"v_mfma_f32_16x16x32_bf16 a[68:71], a[144:147], a[164:167], a[68:71]\n" \
"v_mfma_f32_16x16x32_bf16 a[68:71], a[208:211], a[228:231], a[68:71]\n" \
"ds_read_b64_tr_b16 a[250:251], v38 offset:9216\n" \
"v_mfma_f32_16x16x32_bf16 a[72:75], a[144:147], a[168:171], a[72:75]\n" \
"v_mfma_f32_16x16x32_bf16 a[72:75], a[208:211], a[232:235], a[72:75]\n" \
"v_mfma_f32_16x16x32_bf16 a[76:79], a[144:147], a[172:175], a[76:79]\n" \
"v_mfma_f32_16x16x32_bf16 a[76:79], a[208:211], a[236:239], a[76:79]\n" \
"ds_read_b64_tr_b16 a[188:189], v39 offset:0\n" \
"v_mfma_f32_16x16x32_bf16 a[92:95], a[148:151], a[172:175], a[92:95]\n" \
"s_mov_b64 exec, 0xffff\n" \
"global_store_dwordx4 v49, v[0:3], s[28:29] offset:2048 sc1\n" \
"s_mov_b64 exec, -1\n" \
"s_nop 0\n" \
"v_mfma_f32_16x16x32_bf16 a[92:95], a[212:215], a[236:239], a[92:95]\n" \
"v_mfma_f32_16x16x32_bf16 a[88:91], a[148:151], a[168:171], a[88:91]\n" \
"ds_read_b64_tr_b16 a[190:191], v39 offset:1024\n" \
"v_mfma_f32_16x16x32_bf16 a[88:91], a[212:215], a[232:235], a[88:91]\n" \
"v_mfma_f32_16x16x32_bf16 a[84:87], a[148:151], a[164:167], a[84:87]\n" \
"v_mfma_f32_16x16x32_bf16 a[84:87], a[212:215], a[228:231], a[84:87]\n" \
"ds_read_b64_tr_b16 a[252:253], v39 offset:8192\n" \
"v_mfma_f32_16x16x32_bf16 a[80:83], a[148:151], a[160:163], a[80:83]\n" \
"v_mfma_f32_16x16x32_bf16 a[80:83], a[212:215], a[224:227], a[80:83]\n" \
"v_mfma_f32_16x16x32_bf16 a[96:99], a[152:155], a[160:163], a[96:99]\n" \
"ds_read_b64_tr_b16 a[254:255], v39 offset:9216\n" \
"v_mfma_f32_16x16x32_bf16 a[96:99], a[216:219], a[224:227], a[96:99]\n" \
"v_mfma_f32_16x16x32_bf16 a[100:103], a[152:155], a[164:167], a[100:103]\n" \
"v_mfma_f32_16x16x32_bf16 a[100:103], a[216:219], a[228:231], a[100:103]\n" \
"v_mfma_f32_16x16x32_bf16 a[104:107], a[152:155], a[168:171], a[104:107]\n" \
"v_mfma_f32_16x16x32_bf16 a[104:107], a[216:219], a[232:235], a[104:107]\n" \
"v_mfma_f32_16x16x32_bf16 a[108:111], a[152:155], a[172:175], a[108:111]\n" \
"v_mfma_f32_16x16x32_bf16 a[108:111], a[216:219], a[236:239], a[108:111]\n" \
"s_waitcnt lgkmcnt(15)\n" \
"v_mfma_f32_16x16x32_bf16 a[124:127], a[156:159], a[172:175], a[124:127]\n" \
"v_mfma_f32_16x16x32_bf16 a[124:127], a[220:223], a[236:239], a[124:127]\n" \
"v_mfma_f32_16x16x32_bf16 a[120:123], a[156:159], a[168:171], a[120:123]\n" \
"v_mfma_f32_16x16x32_bf16 a[120:123], a[220:223], a[232:235], a[120:123]\n" \
"v_mfma_f32_16x16x32_bf16 a[116:119], a[156:159], a[164:167], a[116:119]\n" \
"v_mfma_f32_16x16x32_bf16 a[116:119], a[220:223], a[228:231], a[116:119]\n" \
"v_mfma_f32_16x16x32_bf16 a[112:115], a[156:159], a[160:163], a[112:115]\n" \
"v_mfma_f32_16x16x32_bf16 a[112:115], a[220:223], a[224:227], a[112:115]\n" \
"s_waitcnt vmcnt(3) lgkmcnt(0)\n" \
"s_barrier\n" \
"v_cmp_ne_u32_e32 vcc, s62, v44\n" \
"s_nop 4\n" \
"s_cbranch_vccnz L_poll61_%=\n" \
"L_pollr61_%=:\n" \
"s_mov_b32 s68, s12\n" \
"s_and_b32 s69, s13, 0xffff\n" \
"s_mov_b32 s70, 0x480000\n" \
"s_mov_b32 s71, 0x20000\n" \
"v_mfma_f32_16x16x32_bf16 v[52:55], a[128:131], a[176:179], v[52:55]\n" \
"s_add_u32 m0, s40, 0x0\n" \
"s_add_u32 s85, s39, 0x0\n" \
"s_nop 0\n" \
"v_mfma_f32_16x16x32_bf16 v[52:55], a[192:195], a[240:243], v[52:55]\n" \
"buffer_load_dwordx4 v20, s[80:83], s85 offen sc1 lds\n" \
"v_mfma_f32_16x16x32_bf16 v[56:59], a[128:131], a[180:183], v[56:59]\n" \
"ds_read_b64_tr_b16 a[160:161], v32 offset:32768\n" \
"v_mfma_f32_16x16x32_bf16 v[56:59], a[192:195], a[244:247], v[56:59]\n" \
"s_add_u32 m0, s40, 0x1000\n" \
"s_add_u32 s85, s39, 0x1000\n" \
"v_mfma_f32_16x16x32_bf16 v[60:63], a[128:131], a[184:187], v[60:63]\n" \
"buffer_load_dwordx4 v20, s[80:83], s85 offen sc1 lds\n" \
"v_mfma_f32_16x16x32_bf16 v[60:63], a[192:195], a[248:251], v[60:63]\n" \
".fill (L_pb + 57056 - .) / 4, 4, 0xBF800000\n" \
"L_pp14:\n" \
"s_cbranch_execz L_pret\n" \
"ds_read_b64_tr_b16 a[162:163], v32 offset:33792\n" \
"v_mfma_f32_16x16x32_bf16 v[64:67], a[128:131], a[188:191], v[64:67]\n" \
"ds_read_b64_tr_b16 a[224:225], v32 offset:40960\n" \
"v_mfma_f32_16x16x32_bf16 v[64:67], a[192:195], a[252:255], v[64:67]\n" \
"ds_read_b128 a[128:131], v28 offset:49152\n" \
"v_mfma_f32_16x16x32_bf16 v[80:83], a[132:135], a[188:191], v[80:83]\n" \
"ds_read_b128 a[192:195], v29 offset:49152\n" \
"v_mfma_f32_16x16x32_bf16 v[80:83], a[196:199], a[252:255], v[80:83]\n" \
"s_add_u32 m0, s40, 0x3000\n" \
"s_movk_i32 s84, 0x0\n" \
"v_mfma_f32_16x16x32_bf16 v[76:79], a[132:135], a[184:187], v[76:79]\n" \
"buffer_load_dwordx4 v21, s[76:79], s84 offen offset:0 lds\n" \
"v_mfma_f32_16x16x32_bf16 v[76:79], a[196:199], a[248:251], v[76:79]\n" \
"s_add_u32 m0, s30, 0x0\n" \
"s_movk_i32 s84, 0x0\n" \
"v_mfma_f32_16x16x32_bf16 v[72:75], a[132:135], a[180:183], v[72:75]\n" \
"buffer_load_dwordx4 v24, s[76:79], s84 offen offset:0 lds\n" \
"v_mfma_f32_16x16x32_bf16 v[72:75], a[196:199], a[244:247], v[72:75]\n" \
"ds_read_b64_tr_b16 a[226:227], v32 offset:41984\n" \
"v_mfma_f32_16x16x32_bf16 v[68:71], a[132:135], a[176:179], v[68:71]\n" \
"ds_read_b64_tr_b16 a[164:165], v33 offset:32768\n" \
"v_mfma_f32_16x16x32_bf16 v[68:71], a[196:199], a[240:243], v[68:71]\n" \
"ds_read_b128 a[132:135], v28 offset:51200\n" \
"v_mfma_f32_16x16x32_bf16 v[84:87], a[136:139], a[176:179], v[84:87]\n" \
"ds_read_b128 a[196:199], v29 offset:51200\n" \
"v_mfma_f32_16x16x32_bf16 v[84:87], a[200:203], a[240:243], v[84:87]\n" \
"s_movk_i32 s84, 0x2000\n" \
"v_mfma_f32_16x16x32_bf16 v[88:91], a[136:139], a[180:183], v[88:91]\n" \
"buffer_load_dwordx4 v25, s[76:79], s84 offen offset:1024 lds\n" \
"v_mfma_f32_16x16x32_bf16 v[88:91], a[200:203], a[244:247], v[88:91]\n" \
"s_movk_i32 s84, 0x4000\n" \
"v_mfma_f32_16x16x32_bf16 v[92:95], a[136:139], a[184:187], v[92:95]\n" \
"buffer_load_dwordx4 v24, s[76:79], s84 offen offset:2048 lds\n" \
"v_mfma_f32_16x16x32_bf16 v[92:95], a[200:203], a[248:251], v[92:95]\n" \
"ds_read_b64_tr_b16 a[166:167], v33 offset:33792\n" \
"v_mfma_f32_16x16x32_bf16 v[96:99], a[136:139], a[188:191], v[96:99]\n" \
"ds_read_b64_tr_b16 a[228:229], v33 offset:40960\n" \
"v_mfma_f32_16x16x32_bf16 v[96:99], a[200:203], a[252:255], v[96:99]\n" \
"ds_read_b128 a[136:139], v28 offset:53248\n" \
"v_mfma_f32_16x16x32_bf16 v[112:115], a[140:143], a[188:191], v[112:115]\n" \
"ds_read_b128 a[200:203], v29 offset:53248\n" \
"v_mfma_f32_16x16x32_bf16 v[112:115], a[204:207], a[252:255], v[112:115]\n" \
"s_movk_i32 s84, 0x6000\n" \
"v_mfma_f32_16x16x32_bf16 v[108:111], a[140:143], a[184:187], v[108:111]\n" \
"buffer_load_dwordx4 v25, s[76:79], s84 offen offset:3072 lds\n" \
"v_mfma_f32_16x16x32_bf16 v[108:111], a[204:207], a[248:251], v[108:111]\n" \
"s_add_u32 m0, s31, 0x0\n" \
"s_movk_i32 s84, 0x0\n" \
"v_mfma_f32_16x16x32_bf16 v[104:107], a[140:143], a[180:183], v[104:107]\n" \
"buffer_load_dwordx4 v26, s[68:71], s84 offen offset:0 lds\n" \
"v_mfma_f32_16x16x32_bf16 v[104:107], a[204:207], a[244:247], v[104:107]\n" \
"s_movk_i32 s84, 0x7c00\n" \
"v_mfma_f32_16x16x32_bf16 v[100:103], a[140:143], a[176:179], v[100:103]\n" \
"buffer_load_dwordx4 v26, s[68:71], s84 offen offset:1024 lds\n" \
"v_mfma_f32_16x16x32_bf16 v[100:103], a[204:207], a[240:243], v[100:103]\n" \
"ds_read_b128 a[140:143], v28 offset:55296\n" \
"v_mfma_f32_16x16x32_bf16 v[116:119], a[144:147], a[176:179], v[116:119]\n" \
"ds_read_b128 a[204:207], v29 offset:55296\n" \
"v_mfma_f32_16x16x32_bf16 v[116:119], a[208:211], a[240:243], v[116:119]\n" \
"s_addk_i32 s84, 0x7c00\n" \
"v_mfma_f32_16x16x32_bf16 v[120:123], a[144:147], a[180:183], v[120:123]\n" \
"buffer_load_dwordx4 v27, s[68:71], s84 offen offset:2048 lds\n" \
"v_mfma_f32_16x16x32_bf16 v[120:123], a[208:211], a[244:247], v[120:123]\n" \
"ds_read_b64_tr_b16 a[230:231], v33 offset:41984\n" \
"v_mfma_f32_16x16x32_bf16 v[124:127], a[144:147], a[184:187], v[124:127]\n" \
"s_addk_i32 s84, 0x7c00\n" \
"v_mfma_f32_16x16x32_bf16 v[124:127], a[208:211], a[248:251], v[124:127]\n" \
"buffer_load_dwordx4 v27, s[68:71], s84 offen offset:3072 lds\n" \
"v_mfma_f32_16x16x32_bf16 v[128:131], a[144:147], a[188:191], v[128:131]\n" \
"ds_read_b64_tr_b16 a[168:169], v34 offset:32768\n" \
"v_mfma_f32_16x16x32_bf16 v[128:131], a[208:211], a[252:255], v[128:131]\n" \
"ds_read_b128 a[144:147], v28 offset:32768\n" \
"v_mfma_f32_16x16x32_bf16 v[144:147], a[148:151], a[188:191], v[144:147]\n" \
"ds_read_b128 a[208:211], v29 offset:32768\n" \
"v_mfma_f32_16x16x32_bf16 v[144:147], a[212:215], a[252:255], v[144:147]\n" \
"s_add_u32 m0, s30, 0x1000\n" \
"s_mov_b32 s84, 0x9000\n" \
"v_mfma_f32_16x16x32_bf16 v[140:143], a[148:151], a[184:187], v[140:143]\n" \
"buffer_load_dwordx4 v24, s[76:79], s84 offen offset:0 lds\n" \
"v_mfma_f32_16x16x32_bf16 v[140:143], a[212:215], a[248:251], v[140:143]\n" \
"ds_read_b64_tr_b16 a[170:171], v34 offset:33792\n" \
"v_mfma_f32_16x16x32_bf16 v[136:139], a[148:151], a[180:183], v[136:139]\n" \
"s_addk_i32 s84, 0x2000\n" \
"s_nop 0\n" \
"v_mfma_f32_16x16x32_bf16 v[136:139], a[212:215], a[244:247], v[136:139]\n" \
"buffer_load_dwordx4 v25, s[76:79], s84 offen offset:1024 lds\n" \
"v_mfma_f32_16x16x32_bf16 v[132:135], a[148:151], a[176:179], v[132:135]\n" \
"ds_read_b64_tr_b16 a[232:233], v34 offset:40960\n" \
"v_mfma_f32_16x16x32_bf16 v[132:135], a[212:215], a[240:243], v[132:135]\n" \
"ds_read_b128 a[148:151], v28 offset:34816\n" \
"v_mfma_f32_16x16x32_bf16 v[148:151], a[152:155], a[176:179], v[148:151]\n" \
"ds_read_b128 a[212:215], v29 offset:34816\n" \
"v_mfma_f32_16x16x32_bf16 v[148:151], a[216:219], a[240:243], v[148:151]\n" \
"ds_read_b64_tr_b16 a[234:235], v34 offset:41984\n" \
"v_mfma_f32_16x16x32_bf16 v[152:155], a[152:155], a[180:183], v[152:155]\n" \
"ds_read_b64_tr_b16 a[172:173], v35 offset:32768\n" \
"v_mfma_f32_16x16x32_bf16 v[152:155], a[216:219], a[244:247], v[152:155]\n" \
"ds_read_b64_tr_b16 a[174:175], v35 offset:33792\n" \
"v_mfma_f32_16x16x32_bf16 v[156:159], a[152:155], a[184:187], v[156:159]\n" \
"ds_read_b64_tr_b16 a[236:237], v35 offset:40960\n" \
"v_mfma_f32_16x16x32_bf16 v[156:159], a[216:219], a[248:251], v[156:159]\n" \
"ds_read_b64_tr_b16 a[238:239], v35 offset:41984\n" \
"v_mfma_f32_16x16x32_bf16 v[160:163], a[152:155], a[188:191], v[160:163]\n" \
"v_mfma_f32_16x16x32_bf16 v[160:163], a[216:219], a[252:255], v[160:163]\n" \
"ds_read_b128 a[152:155], v28 offset:36864\n" \
"v_mfma_f32_16x16x32_bf16 v[176:179], a[156:159], a[188:191], v[176:179]\n" \
"ds_read_b128 a[216:219], v29 offset:36864\n" \
"v_mfma_f32_16x16x32_bf16 v[176:179], a[220:223], a[252:255], v[176:179]\n" \
"v_mfma_f32_16x16x32_bf16 v[172:175], a[156:159], a[184:187], v[172:175]\n" \
"v_mfma_f32_16x16x32_bf16 v[172:175], a[220:223], a[248:251], v[172:175]\n" \
"v_mfma_f32_16x16x32_bf16 v[168:171], a[156:159], a[180:183], v[168:171]\n" \
"v_mfma_f32_16x16x32_bf16 v[168:171], a[220:223], a[244:247], v[168:171]\n" \
"v_mfma_f32_16x16x32_bf16 v[164:167], a[156:159], a[176:179], v[164:167]\n" \
"v_mfma_f32_16x16x32_bf16 v[164:167], a[220:223], a[240:243], v[164:167]\n" \
"ds_read_b128 a[156:159], v28 offset:38912\n" \
"ds_read_b128 a[220:223], v29 offset:38912\n" \
"s_waitcnt lgkmcnt(15)\n" \
"v_mfma_f32_16x16x32_bf16 a[0:3], a[128:131], a[160:163], a[0:3]\n" \
"s_addk_i32 s84, 0x2000\n" \
"v_mfma_f32_16x16x32_bf16 a[0:3], a[192:195], a[224:227], a[0:3]\n" \
"buffer_load_dwordx4 v24, s[76:79], s84 offen offset:2048 lds\n" \
"v_mfma_f32_16x16x32_bf16 a[4:7], a[128:131], a[164:167], a[4:7]\n" \
"ds_read_b64_tr_b16 a[176:177], v36 offset:32768\n" \
"v_mfma_f32_16x16x32_bf16 a[4:7], a[192:195], a[228:231], a[4:7]\n" \
"s_addk_i32 s84, 0x2000\n" \
"s_waitcnt lgkmcnt(13)\n" \
"v_mfma_f32_16x16x32_bf16 a[8:11], a[128:131], a[168:171], a[8:11]\n" \
"buffer_load_dwordx4 v25, s[76:79], s84 offen offset:3072 lds\n" \
"s_waitcnt lgkmcnt(9)\n" \
"v_mfma_f32_16x16x32_bf16 a[8:11], a[192:195], a[232:235], a[8:11]\n" \
"ds_read_b64_tr_b16 a[178:179], v36 offset:33792\n" \
"s_waitcnt lgkmcnt(8)\n" \
"v_mfma_f32_16x16x32_bf16 a[12:15], a[128:131], a[172:175], a[12:15]\n" \
"ds_read_b64_tr_b16 a[240:241], v36 offset:40960\n" \
"s_waitcnt lgkmcnt(7)\n" \
"v_mfma_f32_16x16x32_bf16 a[12:15], a[192:195], a[236:239], a[12:15]\n" \
"v_mfma_f32_16x16x32_bf16 a[28:31], a[132:135], a[172:175], a[28:31]\n" \
"v_mfma_f32_16x16x32_bf16 a[28:31], a[196:199], a[236:239], a[28:31]\n" \
"ds_read_b64_tr_b16 a[242:243], v36 offset:41984\n" \
"v_mfma_f32_16x16x32_bf16 a[24:27], a[132:135], a[168:171], a[24:27]\n" \
"v_mfma_f32_16x16x32_bf16 a[24:27], a[196:199], a[232:235], a[24:27]\n" \
"v_mfma_f32_16x16x32_bf16 a[20:23], a[132:135], a[164:167], a[20:23]\n" \
"s_nop 1\n" \
"v_accvgpr_read_b32 v184, a12\n" \
"v_accvgpr_read_b32 v185, a13\n" \
"v_mfma_f32_16x16x32_bf16 a[20:23], a[196:199], a[228:231], a[20:23]\n" \
"ds_read_b64_tr_b16 a[180:181], v37 offset:32768\n" \
"v_accvgpr_read_b32 v186, a14\n" \
"v_mfma_f32_16x16x32_bf16 a[16:19], a[132:135], a[160:163], a[16:19]\n" \
"v_accvgpr_read_b32 v187, a15\n" \
"v_accvgpr_read_b32 v180, a28\n" \
"v_mfma_f32_16x16x32_bf16 a[16:19], a[196:199], a[224:227], a[16:19]\n" \
"v_accvgpr_read_b32 v181, a29\n" \
"v_accvgpr_read_b32 v182, a30\n" \
"v_mfma_f32_16x16x32_bf16 a[32:35], a[136:139], a[160:163], a[32:35]\n" \
"ds_read_b64_tr_b16 a[182:183], v37 offset:33792\n" \
"v_accvgpr_read_b32 v183, a31\n" \
"v_mfma_f32_16x16x32_bf16 a[32:35], a[200:203], a[224:227], a[32:35]\n" \
"v_cvt_pk_bf16_f32 v184, v184, v185\n" \
"v_cvt_pk_bf16_f32 v185, v186, v187\n" \
"v_mfma_f32_16x16x32_bf16 a[36:39], a[136:139], a[164:167], a[36:39]\n" \
"v_lshlrev_b32_e32 v45, 3, v20\n" \
"v_add_u32_e32 v45, s31, v45\n" \
"v_mfma_f32_16x16x32_bf16 a[36:39], a[200:203], a[228:231], a[36:39]\n" \
"s_getpc_b64 s[86:87]\n" \
"s_sub_u32 s86, s86, 0x10800\n" \
"s_subb_u32 s87, s87, 0\n" \
"s_mov_b32 m0, s32\n" \
"s_nop 0\n" \
"v_mfma_f32_16x16x32_bf16 a[40:43], a[136:139], a[168:171], a[40:43]\n" \
"global_load_lds_dword v45, s[86:87]\n" \
"v_mfma_f32_16x16x32_bf16 a[40:43], a[200:203], a[232:235], a[40:43]\n" \
"ds_read_b64_tr_b16 a[244:245], v37 offset:40960\n" \
"v_cvt_pk_bf16_f32 v186, v180, v181\n" \
"v_mfma_f32_16x16x32_bf16 a[44:47], a[136:139], a[172:175], a[44:47]\n" \
"ds_read_b64_tr_b16 a[246:247], v37 offset:41984\n" \
"v_cvt_pk_bf16_f32 v187, v182, v183\n" \
"v_mfma_f32_16x16x32_bf16 a[44:47], a[200:203], a[236:239], a[44:47]\n" \
"v_accvgpr_read_b32 v188, a8\n" \
"v_accvgpr_read_b32 v189, a9\n" \
"v_mfma_f32_16x16x32_bf16 a[60:63], a[140:143], a[172:175], a[60:63]\n" \
"v_accvgpr_read_b32 v190, a10\n" \
"v_accvgpr_read_b32 v191, a11\n" \
"v_mfma_f32_16x16x32_bf16 a[60:63], a[204:207], a[236:239], a[60:63]\n" \
"v_accvgpr_read_b32 v180, a24\n" \
"v_accvgpr_read_b32 v181, a25\n" \
"v_mfma_f32_16x16x32_bf16 a[56:59], a[140:143], a[168:171], a[56:59]\n" \
"ds_read_b64_tr_b16 a[184:185], v38 offset:32768\n" \
"v_accvgpr_read_b32 v182, a26\n" \
"v_mfma_f32_16x16x32_bf16 a[56:59], a[204:207], a[232:235], a[56:59]\n" \
"v_accvgpr_read_b32 v183, a27\n" \
"v_cvt_pk_bf16_f32 v188, v188, v189\n" \
"v_mfma_f32_16x16x32_bf16 a[52:55], a[140:143], a[164:167], a[52:55]\n" \
"v_cvt_pk_bf16_f32 v189, v190, v191\n" \
"v_cvt_pk_bf16_f32 v190, v180, v181\n" \
"v_mfma_f32_16x16x32_bf16 a[52:55], a[204:207], a[228:231], a[52:55]\n" \
"ds_read_b64_tr_b16 a[186:187], v38 offset:33792\n" \
"v_cvt_pk_bf16_f32 v191, v182, v183\n" \
"v_mfma_f32_16x16x32_bf16 a[48:51], a[140:143], a[160:163], a[48:51]\n" \
"v_accvgpr_read_b32 v192, a4\n" \
"v_accvgpr_read_b32 v193, a5\n" \
"v_mfma_f32_16x16x32_bf16 a[48:51], a[204:207], a[224:227], a[48:51]\n" \
"v_accvgpr_read_b32 v194, a6\n" \
"v_accvgpr_read_b32 v195, a7\n" \
"v_mfma_f32_16x16x32_bf16 a[64:67], a[144:147], a[160:163], a[64:67]\n" \
"ds_read_b64_tr_b16 a[248:249], v38 offset:40960\n" \
"v_accvgpr_read_b32 v180, a20\n" \
"v_mfma_f32_16x16x32_bf16 a[64:67], a[208:211], a[224:227], a[64:67]\n" \
"v_accvgpr_read_b32 v181, a21\n" \
"v_accvgpr_read_b32 v182, a22\n" \
"v_mfma_f32_16x16x32_bf16 a[68:71], a[144:147], a[164:167], a[68:71]\n" \
"v_accvgpr_read_b32 v183, a23\n" \
"v_cvt_pk_bf16_f32 v192, v192, v193\n" \
"v_mfma_f32_16x16x32_bf16 a[68:71], a[208:211], a[228:231], a[68:71]\n" \
"ds_read_b64_tr_b16 a[250:251], v38 offset:41984\n" \
"v_cvt_pk_bf16_f32 v193, v194, v195\n" \
"v_mfma_f32_16x16x32_bf16 a[72:75], a[144:147], a[168:171], a[72:75]\n" \
"v_cvt_pk_bf16_f32 v194, v180, v181\n" \
"v_cvt_pk_bf16_f32 v195, v182, v183\n" \
"v_mfma_f32_16x16x32_bf16 a[72:75], a[208:211], a[232:235], a[72:75]\n" \
"v_accvgpr_read_b32 v196, a0\n" \
"v_accvgpr_read_b32 v197, a1\n" \
"v_mfma_f32_16x16x32_bf16 a[76:79], a[144:147], a[172:175], a[76:79]\n" \
"v_accvgpr_read_b32 v198, a2\n" \
"v_accvgpr_read_b32 v199, a3\n" \
"v_mfma_f32_16x16x32_bf16 a[76:79], a[208:211], a[236:239], a[76:79]\n" \
"ds_read_b64_tr_b16 a[188:189], v39 offset:32768\n" \
"v_accvgpr_read_b32 v180, a16\n" \
"v_mfma_f32_16x16x32_bf16 a[92:95], a[148:151], a[172:175], a[92:95]\n" \
"s_add_u32 s89, s41, 0x0\n" \
"s_nop 0\n" \
"buffer_store_dwordx4 v[184:187], v20, s[80:83], s89 offen offset:3072 sc1\n" \
"v_mfma_f32_16x16x32_bf16 a[92:95], a[212:215], a[236:239], a[92:95]\n" \
"v_accvgpr_read_b32 v181, a17\n" \
"v_accvgpr_read_b32 v182, a18\n" \
"v_mfma_f32_16x16x32_bf16 a[88:91], a[148:151], a[168:171], a[88:91]\n" \
"ds_read_b64_tr_b16 a[190:191], v39 offset:33792\n" \
"v_accvgpr_read_b32 v183, a19\n" \
"v_mfma_f32_16x16x32_bf16 a[88:91], a[212:215], a[232:235], a[88:91]\n" \
"v_cvt_pk_bf16_f32 v196, v196, v197\n" \
"v_cvt_pk_bf16_f32 v197, v198, v199\n" \
"v_mfma_f32_16x16x32_bf16 a[84:87], a[148:151], a[164:167], a[84:87]\n" \
"v_cvt_pk_bf16_f32 v198, v180, v181\n" \
"v_cvt_pk_bf16_f32 v199, v182, v183\n" \
"v_mfma_f32_16x16x32_bf16 a[84:87], a[212:215], a[228:231], a[84:87]\n" \
"ds_read_b64_tr_b16 a[252:253], v39 offset:40960\n" \
"v_accvgpr_read_b32 v200, a44\n" \
"v_mfma_f32_16x16x32_bf16 a[80:83], a[148:151], a[160:163], a[80:83]\n" \
"s_add_u32 s88, s41, 0x0\n" \
"buffer_store_dwordx4 v[188:191], v20, s[80:83], s88 offen offset:2048 sc1\n" \
"v_mfma_f32_16x16x32_bf16 a[80:83], a[212:215], a[224:227], a[80:83]\n" \
"v_accvgpr_read_b32 v201, a45\n" \
"v_accvgpr_read_b32 v202, a46\n" \
"s_waitcnt lgkmcnt(15)\n" \
"v_mfma_f32_16x16x32_bf16 a[96:99], a[152:155], a[160:163], a[96:99]\n" \
"ds_read_b64_tr_b16 a[254:255], v39 offset:41984\n" \
"v_accvgpr_read_b32 v203, a47\n" \
"v_mfma_f32_16x16x32_bf16 a[96:99], a[216:219], a[224:227], a[96:99]\n" \
"v_accvgpr_read_b32 v180, a60\n" \
"v_accvgpr_read_b32 v181, a61\n" \
"v_mfma_f32_16x16x32_bf16 a[100:103], a[152:155], a[164:167], a[100:103]\n" \
"v_accvgpr_read_b32 v182, a62\n" \
"v_accvgpr_read_b32 v183, a63\n" \
"v_mfma_f32_16x16x32_bf16 a[100:103], a[216:219], a[228:231], a[100:103]\n" \
"v_cvt_pk_bf16_f32 v200, v200, v201\n" \
"v_cvt_pk_bf16_f32 v201, v202, v203\n" \
"v_mfma_f32_16x16x32_bf16 a[104:107], a[152:155], a[168:171], a[104:107]\n" \
"s_add_u32 s89, s41, 0x0\n" \
"s_nop 0\n" \
"buffer_store_dwordx4 v[192:195], v20, s[80:83], s89 offen offset:1024 sc1\n" \
"v_mfma_f32_16x16x32_bf16 a[104:107], a[216:219], a[232:235], a[104:107]\n" \
"v_cvt_pk_bf16_f32 v202, v180, v181\n" \
"v_cvt_pk_bf16_f32 v203, v182, v183\n" \
"v_mfma_f32_16x16x32_bf16 a[108:111], a[152:155], a[172:175], a[108:111]\n" \
"v_accvgpr_read_b32 v204, a40\n" \
"v_accvgpr_read_b32 v205, a41\n" \
"v_mfma_f32_16x16x32_bf16 a[108:111], a[216:219], a[236:239], a[108:111]\n" \
"v_accvgpr_read_b32 v206, a42\n" \
"v_accvgpr_read_b32 v207, a43\n" \
"v_mfma_f32_16x16x32_bf16 a[124:127], a[156:159], a[172:175], a[124:127]\n" \
"v_accvgpr_read_b32 v180, a56\n" \
"v_accvgpr_read_b32 v181, a57\n" \
"v_mfma_f32_16x16x32_bf16 a[124:127], a[220:223], a[236:239], a[124:127]\n" \
"v_accvgpr_read_b32 v182, a58\n" \
"v_accvgpr_read_b32 v183, a59\n" \
"v_mfma_f32_16x16x32_bf16 a[120:123], a[156:159], a[168:171], a[120:123]\n" \
"s_add_u32 s88, s41, 0x0\n" \
"s_nop 0\n" \
"buffer_store_dwordx4 v[196:199], v20, s[80:83], s88 offen offset:0 sc1\n" \
"v_mfma_f32_16x16x32_bf16 a[120:123], a[220:223], a[232:235], a[120:123]\n" \
"v_cvt_pk_bf16_f32 v204, v204, v205\n" \
"v_cvt_pk_bf16_f32 v205, v206, v207\n" \
"v_mfma_f32_16x16x32_bf16 a[116:119], a[156:159], a[164:167], a[116:119]\n" \
"v_cvt_pk_bf16_f32 v206, v180, v181\n" \
"v_cvt_pk_bf16_f32 v207, v182, v183\n" \
"v_mfma_f32_16x16x32_bf16 a[116:119], a[220:223], a[228:231], a[116:119]\n" \
"v_accvgpr_read_b32 v208, a36\n" \
"v_accvgpr_read_b32 v209, a37\n" \
"v_mfma_f32_16x16x32_bf16 a[112:115], a[156:159], a[160:163], a[112:115]\n" \
"v_accvgpr_read_b32 v210, a38\n" \
"v_accvgpr_read_b32 v211, a39\n" \
"v_mfma_f32_16x16x32_bf16 a[112:115], a[220:223], a[224:227], a[112:115]\n" \
"v_accvgpr_read_b32 v180, a52\n" \
"v_accvgpr_read_b32 v181, a53\n" \
"s_waitcnt vmcnt(5) lgkmcnt(0)\n" \
"s_barrier\n" \
"v_xor_b32_e32 v36, 0x4000, v36\n" \
"v_xor_b32_e64 v32, 0x0, v48\n" \
"v_xor_b32_e32 v37, 0x4000, v37\n" \
"v_xor_b32_e64 v33, 0x20, v48\n" \
"v_xor_b32_e32 v38, 0x4000, v38\n" \
"v_xor_b32_e64 v34, 0x40, v48\n" \
"v_xor_b32_e32 v39, 0x4000, v39\n" \
"v_xor_b32_e32 v35, 0x60, v48\n" \
"v_add_u32_e32 v28, s43, v28\n" \
"v_add_u32_e32 v29, s43, v29\n" \
"v_mfma_f32_16x16x32_bf16 v[116:119], a[144:147], a[176:179], v[116:119]\n" \
"s_add_u32 m0, s40, 0x8000\n" \
"s_add_u32 s85, s39, 0x2000\n" \
"v_mfma_f32_16x16x32_bf16 v[116:119], a[208:211], a[240:243], v[116:119]\n" \
"buffer_load_dwordx4 v20, s[80:83], s85 offen sc1 lds\n" \
"v_mfma_f32_16x16x32_bf16 v[120:123], a[144:147], a[180:183], v[120:123]\n" \
"ds_read_b64_tr_b16 a[160:161], v32 offset:0\n" \
"v_accvgpr_read_b32 v182, a54\n" \
"v_mfma_f32_16x16x32_bf16 v[120:123], a[208:211], a[244:247], v[120:123]\n" \
"s_add_u32 m0, s40, 0x9000\n" \
"s_add_u32 s85, s39, 0x3000\n" \
"v_mfma_f32_16x16x32_bf16 v[124:127], a[144:147], a[184:187], v[124:127]\n" \
"buffer_load_dwordx4 v20, s[80:83], s85 offen sc1 lds\n" \
"v_mfma_f32_16x16x32_bf16 v[124:127], a[208:211], a[248:251], v[124:127]\n" \
"ds_read_b64_tr_b16 a[162:163], v32 offset:512\n" \
"v_accvgpr_read_b32 v183, a55\n" \
"v_mfma_f32_16x16x32_bf16 v[128:131], a[144:147], a[188:191], v[128:131]\n" \
"ds_read_b64_tr_b16 a[224:225], v32 offset:4096\n" \
"v_cvt_pk_bf16_f32 v208, v208, v209\n" \
"v_mfma_f32_16x16x32_bf16 v[128:131], a[208:211], a[252:255], v[128:131]\n" \
"ds_read_b128 a[144:147], v28 offset:8192\n" \
"v_cvt_pk_bf16_f32 v209, v210, v211\n" \
"v_mfma_f32_16x16x32_bf16 v[144:147], a[148:151], a[188:191], v[144:147]\n" \
"ds_read_b128 a[208:211], v29 offset:8192\n" \
"v_cvt_pk_bf16_f32 v210, v180, v181\n" \
"v_mfma_f32_16x16x32_bf16 v[144:147], a[212:215], a[252:255], v[144:147]\n" \
"s_add_u32 m0, s40, 0xb000\n" \
"s_movk_i32 s84, 0x80\n" \
"v_mfma_f32_16x16x32_bf16 v[140:143], a[148:151], a[184:187], v[140:143]\n" \
"buffer_load_dwordx4 v21, s[76:79], s84 offen offset:0 lds\n" \
"v_mfma_f32_16x16x32_bf16 v[140:143], a[212:215], a[248:251], v[140:143]\n" \
"s_add_u32 m0, s30, 0x8000\n" \
"s_movk_i32 s84, 0x80\n" \
"v_mfma_f32_16x16x32_bf16 v[136:139], a[148:151], a[180:183], v[136:139]\n" \
"buffer_load_dwordx4 v24, s[76:79], s84 offen offset:0 lds\n" \
"v_mfma_f32_16x16x32_bf16 v[136:139], a[212:215], a[244:247], v[136:139]\n" \
"ds_read_b64_tr_b16 a[226:227], v32 offset:4608\n" \
"v_cvt_pk_bf16_f32 v211, v182, v183\n" \
"v_mfma_f32_16x16x32_bf16 v[132:135], a[148:151], a[176:179], v[132:135]\n" \
"ds_read_b64_tr_b16 a[164:165], v33 offset:0\n" \
"v_accvgpr_read_b32 v212, a32\n" \
"v_mfma_f32_16x16x32_bf16 v[132:135], a[212:215], a[240:243], v[132:135]\n" \
"ds_read_b128 a[148:151], v28 offset:10240\n" \
"v_accvgpr_read_b32 v213, a33\n" \
"v_mfma_f32_16x16x32_bf16 v[148:151], a[152:155], a[176:179], v[148:151]\n" \
"ds_read_b128 a[212:215], v29 offset:10240\n" \
"v_accvgpr_read_b32 v214, a34\n" \
"v_mfma_f32_16x16x32_bf16 v[148:151], a[216:219], a[240:243], v[148:151]\n" \
"s_movk_i32 s84, 0x2080\n" \
"v_mfma_f32_16x16x32_bf16 v[152:155], a[152:155], a[180:183], v[152:155]\n" \
"buffer_load_dwordx4 v25, s[76:79], s84 offen offset:1024 lds\n" \
"v_mfma_f32_16x16x32_bf16 v[152:155], a[216:219], a[244:247], v[152:155]\n" \
"s_movk_i32 s84, 0x4080\n" \
"v_mfma_f32_16x16x32_bf16 v[156:159], a[152:155], a[184:187], v[156:159]\n" \
"buffer_load_dwordx4 v24, s[76:79], s84 offen offset:2048 lds\n" \
"v_mfma_f32_16x16x32_bf16 v[156:159], a[216:219], a[248:251], v[156:159]\n" \
"ds_read_b64_tr_b16 a[166:167], v33 offset:512\n" \
"v_accvgpr_read_b32 v215, a35\n" \
"v_mfma_f32_16x16x32_bf16 v[160:163], a[152:155], a[188:191], v[160:163]\n" \
"ds_read_b64_tr_b16 a[228:229], v33 offset:4096\n" \
"v_accvgpr_read_b32 v180, a48\n" \
"v_mfma_f32_16x16x32_bf16 v[160:163], a[216:219], a[252:255], v[160:163]\n" \
"ds_read_b128 a[152:155], v28 offset:12288\n" \
"v_accvgpr_read_b32 v181, a49\n" \
"v_mfma_f32_16x16x32_bf16 v[176:179], a[156:159], a[188:191], v[176:179]\n" \
"ds_read_b128 a[216:219], v29 offset:12288\n" \
"v_accvgpr_read_b32 v182, a50\n" \
"v_mfma_f32_16x16x32_bf16 v[176:179], a[220:223], a[252:255], v[176:179]\n" \
"s_movk_i32 s84, 0x6080\n" \
"v_mfma_f32_16x16x32_bf16 v[172:175], a[156:159], a[184:187], v[172:175]\n" \
"buffer_load_dwordx4 v25, s[76:79], s84 offen offset:3072 lds\n" \
"v_mfma_f32_16x16x32_bf16 v[172:175], a[220:223], a[248:251], v[172:175]\n" \
"s_add_u32 m0, s31, 0x8000\n" \
"s_mov_b32 s84, 0x80000\n" \
"v_mfma_f32_16x16x32_bf16 v[168:171], a[156:159], a[180:183], v[168:171]\n" \
"buffer_load_dwordx4 v26, s[68:71], s84 offen offset:0 lds\n" \
"v_mfma_f32_16x16x32_bf16 v[168:171], a[220:223], a[244:247], v[168:171]\n" \
"s_addk_i32 s84, 0x7c00\n" \
"v_mfma_f32_16x16x32_bf16 v[164:167], a[156:159], a[176:179], v[164:167]\n" \
"buffer_load_dwordx4 v26, s[68:71], s84 offen offset:1024 lds\n" \
"v_mfma_f32_16x16x32_bf16 v[164:167], a[220:223], a[240:243], v[164:167]\n" \
"ds_read_b128 a[156:159], v28 offset:14336\n" \
"v_accvgpr_read_b32 v183, a51\n" \
"v_mfma_f32_16x16x32_bf16 v[84:87], a[136:139], a[176:179], v[84:87]\n" \
"ds_read_b128 a[220:223], v29 offset:14336\n" \
"v_cvt_pk_bf16_f32 v212, v212, v213\n" \
"v_mfma_f32_16x16x32_bf16 v[84:87], a[200:203], a[240:243], v[84:87]\n" \
"s_addk_i32 s84, 0x7c00\n" \
"v_mfma_f32_16x16x32_bf16 v[88:91], a[136:139], a[180:183], v[88:91]\n" \
"buffer_load_dwordx4 v27, s[68:71], s84 offen offset:2048 lds\n" \
"v_mfma_f32_16x16x32_bf16 v[88:91], a[200:203], a[244:247], v[88:91]\n" \
"ds_read_b64_tr_b16 a[230:231], v33 offset:4608\n" \
"v_cvt_pk_bf16_f32 v213, v214, v215\n" \
"v_mfma_f32_16x16x32_bf16 v[92:95], a[136:139], a[184:187], v[92:95]\n" \
"s_addk_i32 s84, 0x7c00\n" \
"v_mfma_f32_16x16x32_bf16 v[92:95], a[200:203], a[248:251], v[92:95]\n" \
"buffer_load_dwordx4 v27, s[68:71], s84 offen offset:3072 lds\n" \
"v_mfma_f32_16x16x32_bf16 v[96:99], a[136:139], a[188:191], v[96:99]\n" \
"ds_read_b64_tr_b16 a[172:173], v35 offset:0\n" \
"v_cvt_pk_bf16_f32 v214, v180, v181\n" \
"v_mfma_f32_16x16x32_bf16 v[96:99], a[200:203], a[252:255], v[96:99]\n" \
"ds_read_b128 a[136:139], v28 offset:4096\n" \
"v_cvt_pk_bf16_f32 v215, v182, v183\n" \
"v_mfma_f32_16x16x32_bf16 v[112:115], a[140:143], a[188:191], v[112:115]\n" \
"ds_read_b128 a[200:203], v29 offset:4096\n" \
"v_accvgpr_read_b32 v216, a68\n" \
"v_mfma_f32_16x16x32_bf16 v[112:115], a[204:207], a[252:255], v[112:115]\n" \
"s_add_u32 m0, s30, 0x9000\n" \
"s_mov_b32 s84, 0x9080\n" \
"v_mfma_f32_16x16x32_bf16 v[108:111], a[140:143], a[184:187], v[108:111]\n" \
"buffer_load_dwordx4 v24, s[76:79], s84 offen offset:0 lds\n" \
"v_mfma_f32_16x16x32_bf16 v[108:111], a[204:207], a[248:251], v[108:111]\n" \
"ds_read_b64_tr_b16 a[174:175], v35 offset:512\n" \
"v_accvgpr_read_b32 v217, a69\n" \
"v_mfma_f32_16x16x32_bf16 v[104:107], a[140:143], a[180:183], v[104:107]\n" \
"s_addk_i32 s84, 0x2000\n" \
"s_nop 0\n" \
"v_mfma_f32_16x16x32_bf16 v[104:107], a[204:207], a[244:247], v[104:107]\n" \
"buffer_load_dwordx4 v25, s[76:79], s84 offen offset:1024 lds\n" \
"v_mfma_f32_16x16x32_bf16 v[100:103], a[140:143], a[176:179], v[100:103]\n" \
"ds_read_b64_tr_b16 a[236:237], v35 offset:4096\n" \
"v_accvgpr_read_b32 v218, a70\n" \
"v_mfma_f32_16x16x32_bf16 v[100:103], a[204:207], a[240:243], v[100:103]\n" \
"ds_read_b128 a[140:143], v28 offset:6144\n" \
"v_accvgpr_read_b32 v219, a71\n" \
"v_mfma_f32_16x16x32_bf16 v[52:55], a[128:131], a[176:179], v[52:55]\n" \
"ds_read_b128 a[204:207], v29 offset:6144\n" \
"v_accvgpr_read_b32 v180, a84\n" \
"v_mfma_f32_16x16x32_bf16 v[52:55], a[192:195], a[240:243], v[52:55]\n" \
"ds_read_b64_tr_b16 a[238:239], v35 offset:4608\n" \
"v_accvgpr_read_b32 v181, a85\n" \
"v_mfma_f32_16x16x32_bf16 v[56:59], a[128:131], a[180:183], v[56:59]\n" \
"ds_read_b64_tr_b16 a[168:169], v34 offset:0\n" \
"v_accvgpr_read_b32 v182, a86\n" \
"v_mfma_f32_16x16x32_bf16 v[56:59], a[192:195], a[244:247], v[56:59]\n" \
"ds_read_b64_tr_b16 a[170:171], v34 offset:512\n" \
"v_accvgpr_read_b32 v183, a87\n" \
"v_mfma_f32_16x16x32_bf16 v[60:63], a[128:131], a[184:187], v[60:63]\n" \
"ds_read_b64_tr_b16 a[232:233], v34 offset:4096\n" \
"v_cvt_pk_bf16_f32 v216, v216, v217\n" \
"v_mfma_f32_16x16x32_bf16 v[60:63], a[192:195], a[248:251], v[60:63]\n" \
"ds_read_b64_tr_b16 a[234:235], v34 offset:4608\n" \
"v_cvt_pk_bf16_f32 v217, v218, v219\n" \
"v_mfma_f32_16x16x32_bf16 v[64:67], a[128:131], a[188:191], v[64:67]\n" \
"v_cvt_pk_bf16_f32 v218, v180, v181\n" \
"v_cvt_pk_bf16_f32 v219, v182, v183\n" \
"v_mfma_f32_16x16x32_bf16 v[64:67], a[192:195], a[252:255], v[64:67]\n" \
"ds_read_b128 a[128:131], v28 offset:0\n" \
"v_accvgpr_read_b32 v220, a64\n" \
"v_mfma_f32_16x16x32_bf16 v[80:83], a[132:135], a[188:191], v[80:83]\n" \
"ds_read_b128 a[192:195], v29 offset:0\n" \
"v_accvgpr_read_b32 v221, a65\n" \
"v_mfma_f32_16x16x32_bf16 v[80:83], a[196:199], a[252:255], v[80:83]\n" \
"v_accvgpr_read_b32 v222, a66\n" \
"v_accvgpr_read_b32 v223, a67\n" \
"v_mfma_f32_16x16x32_bf16 v[76:79], a[132:135], a[184:187], v[76:79]\n" \
"v_accvgpr_read_b32 v180, a80\n" \
"v_accvgpr_read_b32 v181, a81\n" \
"v_mfma_f32_16x16x32_bf16 v[76:79], a[196:199], a[248:251], v[76:79]\n" \
"v_accvgpr_read_b32 v182, a82\n" \
"v_accvgpr_read_b32 v183, a83\n" \
"v_mfma_f32_16x16x32_bf16 v[72:75], a[132:135], a[180:183], v[72:75]\n" \
"v_cvt_pk_bf16_f32 v220, v220, v221\n" \
"v_cvt_pk_bf16_f32 v221, v222, v223\n" \
"v_mfma_f32_16x16x32_bf16 v[72:75], a[196:199], a[244:247], v[72:75]\n" \
"v_cvt_pk_bf16_f32 v222, v180, v181\n" \
"v_cvt_pk_bf16_f32 v223, v182, v183\n" \
"v_mfma_f32_16x16x32_bf16 v[68:71], a[132:135], a[176:179], v[68:71]\n" \
"v_cvt_pk_bf16_f32 v224, v128, v129\n" \
"v_cvt_pk_bf16_f32 v225, v130, v131\n" \
"v_mfma_f32_16x16x32_bf16 v[68:71], a[196:199], a[240:243], v[68:71]\n" \
"ds_read_b128 a[132:135], v28 offset:2048\n" \
"ds_read_b128 a[196:199], v29 offset:2048\n" \
"s_waitcnt lgkmcnt(15)\n" \
"v_mfma_f32_16x16x32_bf16 a[0:3], a[160:163], a[144:147], 0\n" \
"s_addk_i32 s84, 0x2000\n" \
"v_cvt_pk_bf16_f32 v226, v144, v145\n" \
"v_mfma_f32_16x16x32_bf16 a[0:3], a[224:227], a[208:211], a[0:3]\n" \
"buffer_load_dwordx4 v24, s[76:79], s84 offen offset:2048 lds\n" \
"v_cvt_pk_bf16_f32 v227, v146, v147\n" \
"v_mfma_f32_16x16x32_bf16 a[4:7], a[164:167], a[144:147], 0\n" \
"ds_read_b64_tr_b16 a[176:177], v36 offset:0\n" \
"v_cvt_pk_bf16_f32 v228, v124, v125\n" \
"v_mfma_f32_16x16x32_bf16 a[4:7], a[228:231], a[208:211], a[4:7]\n" \
"s_addk_i32 s84, 0x2000\n" \
"v_cvt_pk_bf16_f32 v229, v126, v127\n" \
"s_waitcnt lgkmcnt(13)\n" \
"v_mfma_f32_16x16x32_bf16 a[12:15], a[172:175], a[144:147], 0\n" \
"buffer_load_dwordx4 v25, s[76:79], s84 offen offset:3072 lds\n" \
"v_cvt_pk_bf16_f32 v230, v140, v141\n" \
"s_waitcnt lgkmcnt(9)\n" \
"v_mfma_f32_16x16x32_bf16 a[12:15], a[236:239], a[208:211], a[12:15]\n" \
"ds_read_b64_tr_b16 a[178:179], v36 offset:1024\n" \
"v_cvt_pk_bf16_f32 v231, v142, v143\n" \
"s_waitcnt lgkmcnt(8)\n" \
"v_mfma_f32_16x16x32_bf16 a[8:11], a[144:147], a[168:171], 0\n" \
"ds_read_b64_tr_b16 a[240:241], v36 offset:8192\n" \
"v_cvt_pk_bf16_f32 v232, v120, v121\n" \
"s_waitcnt lgkmcnt(7)\n" \
"s_nop 0\n" \
"v_mfma_f32_16x16x32_bf16 a[8:11], a[208:211], a[232:235], a[8:11]\n" \
"ds_read_b64_tr_b16 a[242:243], v36 offset:9216\n" \
"v_cvt_pk_bf16_f32 v233, v122, v123\n" \
"v_mfma_f32_16x16x32_bf16 a[24:27], a[148:151], a[168:171], 0\n" \
"ds_read_b64_tr_b16 a[180:181], v37 offset:0\n" \
"v_cvt_pk_bf16_f32 v234, v136, v137\n" \
"v_mfma_f32_16x16x32_bf16 a[24:27], a[212:215], a[232:235], a[24:27]\n" \
"ds_read_b64_tr_b16 a[182:183], v37 offset:1024\n" \
"v_cvt_pk_bf16_f32 v235, v138, v139\n" \
"v_mfma_f32_16x16x32_bf16 a[28:31], a[172:175], a[148:151], 0\n" \
"ds_read_b64_tr_b16 a[244:245], v37 offset:8192\n" \
"v_cvt_pk_bf16_f32 v236, v116, v117\n" \
"v_mfma_f32_16x16x32_bf16 a[28:31], a[236:239], a[212:215], a[28:31]\n" \
"s_add_u32 s89, s41, 0x2000\n" \
"buffer_store_dwordx4 v[200:203], v20, s[80:83], s89 offen offset:3072 sc1\n" \
"v_mfma_f32_16x16x32_bf16 a[20:23], a[164:167], a[148:151], 0\n" \
"ds_read_b64_tr_b16 a[246:247], v37 offset:9216\n" \
"v_cvt_pk_bf16_f32 v237, v118, v119\n" \
"v_mfma_f32_16x16x32_bf16 a[20:23], a[228:231], a[212:215], a[20:23]\n" \
"ds_read_b64_tr_b16 a[184:185], v38 offset:0\n" \
"v_cvt_pk_bf16_f32 v238, v132, v133\n" \
"v_mfma_f32_16x16x32_bf16 a[16:19], a[160:163], a[148:151], 0\n" \
"ds_read_b64_tr_b16 a[186:187], v38 offset:1024\n" \
"v_cvt_pk_bf16_f32 v239, v134, v135\n" \
".fill (L_pb + 61152 - .) / 4, 4, 0xBF800000\n" \
"L_pp15:\n" \
"s_cbranch_execz L_pret\n" \
"s_nop 0\n" \
"v_mfma_f32_16x16x32_bf16 a[16:19], a[224:227], a[212:215], a[16:19]\n" \
"v_cvt_pk_bf16_f32 v240, v160, v161\n" \
"v_cvt_pk_bf16_f32 v241, v162, v163\n" \
"v_cvt_pk_bf16_f32 v242, v176, v177\n" \
"v_mfma_f32_16x16x32_bf16 a[32:35], a[160:163], a[152:155], 0\n" \
"s_add_u32 s88, s41, 0x2000\n" \
"buffer_store_dwordx4 v[204:207], v20, s[80:83], s88 offen offset:2048 sc1\n" \
"v_mfma_f32_16x16x32_bf16 a[32:35], a[224:227], a[216:219], a[32:35]\n" \
"ds_read_b64_tr_b16 a[248:249], v38 offset:8192\n" \
"v_cvt_pk_bf16_f32 v243, v178, v179\n" \
"v_mfma_f32_16x16x32_bf16 a[36:39], a[164:167], a[152:155], 0\n" \
"ds_read_b64_tr_b16 a[250:251], v38 offset:9216\n" \
"v_cvt_pk_bf16_f32 v244, v156, v157\n" \
"v_mfma_f32_16x16x32_bf16 a[36:39], a[228:231], a[216:219], a[36:39]\n" \
"ds_read_b64_tr_b16 a[188:189], v39 offset:0\n" \
"v_cvt_pk_bf16_f32 v245, v158, v159\n" \
"v_mfma_f32_16x16x32_bf16 a[44:47], a[172:175], a[152:155], 0\n" \
"v_cvt_pk_bf16_f32 v246, v172, v173\n" \
"v_cvt_pk_bf16_f32 v247, v174, v175\n" \
"v_cvt_pk_bf16_f32 v184, v152, v153\n" \
"v_mfma_f32_16x16x32_bf16 a[44:47], a[236:239], a[216:219], a[44:47]\n" \
"ds_read_b64_tr_b16 a[190:191], v39 offset:1024\n" \
"v_cvt_pk_bf16_f32 v185, v154, v155\n" \
"v_mfma_f32_16x16x32_bf16 a[40:43], a[152:155], a[168:171], 0\n" \
"s_add_u32 s89, s41, 0x2000\n" \
"buffer_store_dwordx4 v[208:211], v20, s[80:83], s89 offen offset:1024 sc1\n" \
"v_mfma_f32_16x16x32_bf16 a[40:43], a[216:219], a[232:235], a[40:43]\n" \
"ds_read_b64_tr_b16 a[252:253], v39 offset:8192\n" \
"v_cvt_pk_bf16_f32 v186, v168, v169\n" \
"v_mfma_f32_16x16x32_bf16 a[56:59], a[156:159], a[168:171], 0\n" \
"ds_read_b64_tr_b16 a[254:255], v39 offset:9216\n" \
"v_cvt_pk_bf16_f32 v187, v170, v171\n" \
"v_mfma_f32_16x16x32_bf16 a[56:59], a[220:223], a[232:235], a[56:59]\n" \
"v_cvt_pk_bf16_f32 v188, v148, v149\n" \
"v_cvt_pk_bf16_f32 v189, v150, v151\n" \
"v_cvt_pk_bf16_f32 v190, v164, v165\n" \
"v_mfma_f32_16x16x32_bf16 a[60:63], a[172:175], a[156:159], 0\n" \
"v_cvt_pk_bf16_f32 v191, v166, v167\n" \
"v_cvt_pk_bf16_f32 v192, v96, v97\n" \
"v_cvt_pk_bf16_f32 v193, v98, v99\n" \
"v_mfma_f32_16x16x32_bf16 a[60:63], a[236:239], a[220:223], a[60:63]\n" \
"s_add_u32 s88, s41, 0x2000\n" \
"buffer_store_dwordx4 v[212:215], v20, s[80:83], s88 offen offset:0 sc1\n" \
"v_mfma_f32_16x16x32_bf16 a[52:55], a[164:167], a[156:159], 0\n" \
"v_cvt_pk_bf16_f32 v194, v112, v113\n" \
"v_cvt_pk_bf16_f32 v195, v114, v115\n" \
"v_cvt_pk_bf16_f32 v196, v92, v93\n" \
"v_mfma_f32_16x16x32_bf16 a[52:55], a[228:231], a[220:223], a[52:55]\n" \
"v_cvt_pk_bf16_f32 v197, v94, v95\n" \
"v_cvt_pk_bf16_f32 v198, v108, v109\n" \
"v_cvt_pk_bf16_f32 v199, v110, v111\n" \
"v_mfma_f32_16x16x32_bf16 a[48:51], a[160:163], a[156:159], 0\n" \
"v_cvt_pk_bf16_f32 v252, v88, v89\n" \
"v_cvt_pk_bf16_f32 v253, v90, v91\n" \
"v_cvt_pk_bf16_f32 v254, v104, v105\n" \
"v_mfma_f32_16x16x32_bf16 a[48:51], a[224:227], a[220:223], a[48:51]\n" \
"v_cvt_pk_bf16_f32 v255, v106, v107\n" \
"v_accvgpr_write_b32 a84, v252\n" \
"v_accvgpr_write_b32 a85, v253\n" \
"v_mfma_f32_16x16x32_bf16 a[64:67], a[164:167], a[140:143], 0\n" \
"v_accvgpr_write_b32 a86, v254\n" \
"v_accvgpr_write_b32 a87, v255\n" \
"v_cvt_pk_bf16_f32 v252, v84, v85\n" \
"v_mfma_f32_16x16x32_bf16 a[64:67], a[228:231], a[204:207], a[64:67]\n" \
"v_cvt_pk_bf16_f32 v253, v86, v87\n" \
"v_cvt_pk_bf16_f32 v254, v100, v101\n" \
"v_cvt_pk_bf16_f32 v255, v102, v103\n" \
"v_mfma_f32_16x16x32_bf16 a[80:83], a[172:175], a[140:143], 0\n" \
"v_mfma_f32_16x16x32_bf16 a[80:83], a[236:239], a[204:207], a[80:83]\n" \
"v_mfma_f32_16x16x32_bf16 a[68:71], a[140:143], a[168:171], 0\n" \
"v_mfma_f32_16x16x32_bf16 a[68:71], a[204:207], a[232:235], a[68:71]\n" \
"s_waitcnt vmcnt(4) lgkmcnt(0)\n" \
"s_barrier\n" \
"v_mfma_f32_16x16x32_bf16 v[116:119], a[176:179], a[144:147], 0\n" \
"s_add_u32 m0, s40, 0x0\n" \
"s_nop 0\n" \
"s_add_u32 s85, s39, 0x4000\n" \
"v_cvt_pk_bf16_f32 v200, v64, v65\n" \
"v_mfma_f32_16x16x32_bf16 v[116:119], a[240:243], a[208:211], v[116:119]\n" \
"buffer_load_dwordx4 v20, s[80:83], s85 offen sc1 lds\n" \
"v_cvt_pk_bf16_f32 v201, v66, v67\n" \
"v_mfma_f32_16x16x32_bf16 v[120:123], a[180:183], a[144:147], 0\n" \
"ds_read_b64_tr_b16 a[160:161], v32 offset:32768\n" \
"v_cvt_pk_bf16_f32 v202, v80, v81\n" \
"v_mfma_f32_16x16x32_bf16 v[120:123], a[244:247], a[208:211], v[120:123]\n" \
"ds_read_b64_tr_b16 a[162:163], v32 offset:33280\n" \
"v_cvt_pk_bf16_f32 v203, v82, v83\n" \
"v_mfma_f32_16x16x32_bf16 v[124:127], a[184:187], a[144:147], 0\n" \
"s_add_u32 m0, s40, 0x1000\n" \
"s_add_u32 s85, s39, 0x5000\n" \
"v_cvt_pk_bf16_f32 v204, v60, v61\n" \
"v_mfma_f32_16x16x32_bf16 v[124:127], a[248:251], a[208:211], v[124:127]\n" \
"buffer_load_dwordx4 v20, s[80:83], s85 offen sc1 lds\n" \
"v_cvt_pk_bf16_f32 v205, v62, v63\n" \
"v_mfma_f32_16x16x32_bf16 v[128:131], a[188:191], a[144:147], 0\n" \
"ds_read_b64_tr_b16 a[224:225], v32 offset:36864\n" \
"v_cvt_pk_bf16_f32 v206, v76, v77\n" \
"v_mfma_f32_16x16x32_bf16 v[128:131], a[252:255], a[208:211], v[128:131]\n" \
"ds_read_b128 a[144:147], v28 offset:40960\n" \
"v_cvt_pk_bf16_f32 v207, v78, v79\n" \
"v_mfma_f32_16x16x32_bf16 v[144:147], a[188:191], a[148:151], 0\n" \
"ds_read_b128 a[208:211], v29 offset:40960\n" \
"v_cvt_pk_bf16_f32 v208, v56, v57\n" \
"v_mfma_f32_16x16x32_bf16 v[144:147], a[252:255], a[212:215], v[144:147]\n" \
"s_add_u32 m0, s40, 0x3000\n" \
"s_movk_i32 s84, 0x100\n" \
"v_cvt_pk_bf16_f32 v209, v58, v59\n" \
"v_mfma_f32_16x16x32_bf16 v[140:143], a[184:187], a[148:151], 0\n" \
"buffer_load_dwordx4 v21, s[76:79], s84 offen offset:0 lds\n" \
"v_cvt_pk_bf16_f32 v210, v72, v73\n" \
"v_mfma_f32_16x16x32_bf16 v[140:143], a[248:251], a[212:215], v[140:143]\n" \
"ds_read_b64_tr_b16 a[226:227], v32 offset:37376\n" \
"v_cvt_pk_bf16_f32 v211, v74, v75\n" \
"v_mfma_f32_16x16x32_bf16 v[136:139], a[180:183], a[148:151], 0\n" \
"s_add_u32 m0, s30, 0x0\n" \
"s_movk_i32 s84, 0x100\n" \
"s_nop 0\n" \
"v_cvt_pk_bf16_f32 v212, v52, v53\n" \
"v_mfma_f32_16x16x32_bf16 v[136:139], a[244:247], a[212:215], v[136:139]\n" \
"buffer_load_dwordx4 v24, s[76:79], s84 offen offset:0 lds\n" \
"v_cvt_pk_bf16_f32 v213, v54, v55\n" \
"v_mfma_f32_16x16x32_bf16 v[132:135], a[176:179], a[148:151], 0\n" \
"ds_read_b64_tr_b16 a[164:165], v33 offset:32768\n" \
"v_cvt_pk_bf16_f32 v214, v68, v69\n" \
"v_mfma_f32_16x16x32_bf16 v[132:135], a[240:243], a[212:215], v[132:135]\n" \
"ds_read_b128 a[148:151], v28 offset:43008\n" \
"v_cvt_pk_bf16_f32 v215, v70, v71\n" \
"v_mfma_f32_16x16x32_bf16 v[148:151], a[176:179], a[152:155], 0\n" \
"ds_read_b128 a[212:215], v29 offset:43008\n" \
"v_mfma_f32_16x16x32_bf16 v[148:151], a[240:243], a[216:219], v[148:151]\n" \
"s_movk_i32 s84, 0x2100\n" \
"v_mfma_f32_16x16x32_bf16 v[152:155], a[180:183], a[152:155], 0\n" \
"buffer_load_dwordx4 v25, s[76:79], s84 offen offset:1024 lds\n" \
"v_mfma_f32_16x16x32_bf16 v[152:155], a[244:247], a[216:219], v[152:155]\n" \
"ds_read_b64_tr_b16 a[166:167], v33 offset:33280\n" \
"v_mfma_f32_16x16x32_bf16 v[156:159], a[184:187], a[152:155], 0\n" \
"ds_read_b64_tr_b16 a[228:229], v33 offset:36864\n" \
"v_mfma_f32_16x16x32_bf16 v[156:159], a[248:251], a[216:219], v[156:159]\n" \
"s_movk_i32 s84, 0x4100\n" \
"v_mfma_f32_16x16x32_bf16 v[160:163], a[188:191], a[152:155], 0\n" \
"buffer_load_dwordx4 v24, s[76:79], s84 offen offset:2048 lds\n" \
"v_mfma_f32_16x16x32_bf16 v[160:163], a[252:255], a[216:219], v[160:163]\n" \
"ds_read_b128 a[152:155], v28 offset:45056\n" \
"v_mfma_f32_16x16x32_bf16 v[176:179], a[188:191], a[156:159], 0\n" \
"ds_read_b128 a[216:219], v29 offset:45056\n" \
"v_mfma_f32_16x16x32_bf16 v[176:179], a[252:255], a[220:223], v[176:179]\n" \
"s_movk_i32 s84, 0x6100\n" \
"s_nop 0\n" \
"v_mfma_f32_16x16x32_bf16 v[172:175], a[184:187], a[156:159], 0\n" \
"buffer_load_dwordx4 v25, s[76:79], s84 offen offset:3072 lds\n" \
"v_mfma_f32_16x16x32_bf16 v[172:175], a[248:251], a[220:223], v[172:175]\n" \
"ds_read_b64_tr_b16 a[230:231], v33 offset:37376\n" \
"v_mfma_f32_16x16x32_bf16 v[168:171], a[180:183], a[156:159], 0\n" \
"ds_read_b64_tr_b16 a[172:173], v35 offset:32768\n" \
"v_mfma_f32_16x16x32_bf16 v[168:171], a[244:247], a[220:223], v[168:171]\n" \
"ds_read_b64_tr_b16 a[174:175], v35 offset:33280\n" \
"v_mfma_f32_16x16x32_bf16 v[164:167], a[176:179], a[156:159], 0\n" \
"v_mfma_f32_16x16x32_bf16 v[164:167], a[240:243], a[220:223], v[164:167]\n" \
"ds_read_b128 a[156:159], v28 offset:47104\n" \
"v_mfma_f32_16x16x32_bf16 v[84:87], a[176:179], a[136:139], 0\n" \
"ds_read_b128 a[220:223], v29 offset:47104\n" \
"v_mfma_f32_16x16x32_bf16 v[84:87], a[240:243], a[200:203], v[84:87]\n" \
"s_add_u32 m0, s31, 0x0\n" \
"s_mov_b32 s84, 0x100000\n" \
"v_mfma_f32_16x16x32_bf16 v[88:91], a[180:183], a[136:139], 0\n" \
"buffer_load_dwordx4 v26, s[68:71], s84 offen offset:0 lds\n" \
"v_mfma_f32_16x16x32_bf16 v[88:91], a[244:247], a[200:203], v[88:91]\n" \
"s_addk_i32 s84, 0x7c00\n" \
"v_mfma_f32_16x16x32_bf16 v[92:95], a[184:187], a[136:139], 0\n" \
"buffer_load_dwordx4 v26, s[68:71], s84 offen offset:1024 lds\n" \
"v_mfma_f32_16x16x32_bf16 v[92:95], a[248:251], a[200:203], v[92:95]\n" \
"ds_read_b64_tr_b16 a[236:237], v35 offset:36864\n" \
"v_mfma_f32_16x16x32_bf16 v[96:99], a[188:191], a[136:139], 0\n" \
"ds_read_b64_tr_b16 a[238:239], v35 offset:37376\n" \
"v_mfma_f32_16x16x32_bf16 v[96:99], a[252:255], a[200:203], v[96:99]\n" \
"ds_read_b128 a[136:139], v28 offset:36864\n" \
"v_mfma_f32_16x16x32_bf16 v[112:115], a[188:191], a[140:143], 0\n" \
"ds_read_b128 a[200:203], v29 offset:36864\n" \
"v_mfma_f32_16x16x32_bf16 v[112:115], a[252:255], a[204:207], v[112:115]\n" \
"s_addk_i32 s84, 0x7c00\n" \
"v_mfma_f32_16x16x32_bf16 v[108:111], a[184:187], a[140:143], 0\n" \
"buffer_load_dwordx4 v27, s[68:71], s84 offen offset:2048 lds\n" \
"v_mfma_f32_16x16x32_bf16 v[108:111], a[248:251], a[204:207], v[108:111]\n" \
"s_addk_i32 s84, 0x7c00\n" \
"v_mfma_f32_16x16x32_bf16 v[104:107], a[180:183], a[140:143], 0\n" \
"buffer_load_dwordx4 v27, s[68:71], s84 offen offset:3072 lds\n" \
"v_mfma_f32_16x16x32_bf16 v[104:107], a[244:247], a[204:207], v[104:107]\n" \
"ds_read_b64_tr_b16 a[168:169], v34 offset:32768\n" \
"v_mfma_f32_16x16x32_bf16 v[100:103], a[176:179], a[140:143], 0\n" \
"ds_read_b64_tr_b16 a[170:171], v34 offset:33280\n" \
"v_mfma_f32_16x16x32_bf16 v[100:103], a[240:243], a[204:207], v[100:103]\n" \
"ds_read_b128 a[140:143], v28 offset:38912\n" \
"v_mfma_f32_16x16x32_bf16 v[52:55], a[176:179], a[128:131], 0\n" \
"ds_read_b128 a[204:207], v29 offset:38912\n" \
"v_mfma_f32_16x16x32_bf16 v[52:55], a[240:243], a[192:195], v[52:55]\n" \
"s_add_u32 m0, s30, 0x1000\n" \
"s_mov_b32 s84, 0x9100\n" \
"v_mfma_f32_16x16x32_bf16 v[56:59], a[180:183], a[128:131], 0\n" \
"buffer_load_dwordx4 v24, s[76:79], s84 offen offset:0 lds\n" \
"v_mfma_f32_16x16x32_bf16 v[56:59], a[244:247], a[192:195], v[56:59]\n" \
"s_addk_i32 s84, 0x2000\n" \
"s_nop 0\n" \
"v_mfma_f32_16x16x32_bf16 v[60:63], a[184:187], a[128:131], 0\n" \
"buffer_load_dwordx4 v25, s[76:79], s84 offen offset:1024 lds\n" \
"v_mfma_f32_16x16x32_bf16 v[60:63], a[248:251], a[192:195], v[60:63]\n" \
"ds_read_b64_tr_b16 a[232:233], v34 offset:36864\n" \
"v_mfma_f32_16x16x32_bf16 v[64:67], a[188:191], a[128:131], 0\n" \
"ds_read_b64_tr_b16 a[234:235], v34 offset:37376\n" \
"v_mfma_f32_16x16x32_bf16 v[64:67], a[252:255], a[192:195], v[64:67]\n" \
"ds_read_b128 a[128:131], v28 offset:32768\n" \
"v_mfma_f32_16x16x32_bf16 v[80:83], a[188:191], a[132:135], 0\n" \
"ds_read_b128 a[192:195], v29 offset:32768\n" \
"v_mfma_f32_16x16x32_bf16 v[80:83], a[252:255], a[196:199], v[80:83]\n" \
"v_mfma_f32_16x16x32_bf16 v[76:79], a[184:187], a[132:135], 0\n" \
"v_mfma_f32_16x16x32_bf16 v[76:79], a[248:251], a[196:199], v[76:79]\n" \
"v_mfma_f32_16x16x32_bf16 v[72:75], a[180:183], a[132:135], 0\n" \
"v_mfma_f32_16x16x32_bf16 v[72:75], a[244:247], a[196:199], v[72:75]\n" \
"v_mfma_f32_16x16x32_bf16 v[68:71], a[176:179], a[132:135], 0\n" \
"v_mfma_f32_16x16x32_bf16 v[68:71], a[240:243], a[196:199], v[68:71]\n" \
"ds_read_b128 a[132:135], v28 offset:34816\n" \
"ds_read_b128 a[196:199], v29 offset:34816\n" \
"s_waitcnt lgkmcnt(15)\n" \
"v_mfma_f32_16x16x32_bf16 a[0:3], a[160:163], a[144:147], a[0:3]\n" \
"s_addk_i32 s84, 0x2000\n" \
"v_mfma_f32_16x16x32_bf16 a[0:3], a[224:227], a[208:211], a[0:3]\n" \
"buffer_load_dwordx4 v24, s[76:79], s84 offen offset:2048 lds\n" \
"v_mfma_f32_16x16x32_bf16 a[4:7], a[164:167], a[144:147], a[4:7]\n" \
"ds_read_b64_tr_b16 a[176:177], v36 offset:32768\n" \
"v_mov_b32_e64 v11, 0\n" \
"v_mfma_f32_16x16x32_bf16 a[4:7], a[228:231], a[208:211], a[4:7]\n" \
"s_addk_i32 s84, 0x2000\n" \
"v_mfma_f32_16x16x32_bf16 a[12:15], a[172:175], a[144:147], a[12:15]\n" \
"buffer_load_dwordx4 v25, s[76:79], s84 offen offset:3072 lds\n" \
"s_waitcnt lgkmcnt(13)\n" \
"v_mfma_f32_16x16x32_bf16 a[12:15], a[236:239], a[208:211], a[12:15]\n" \
"ds_read_b64_tr_b16 a[178:179], v36 offset:33792\n" \
"s_and_b32 s6, s2, 7\n" \
"s_waitcnt lgkmcnt(10)\n" \
"v_mfma_f32_16x16x32_bf16 a[8:11], a[144:147], a[168:171], a[8:11]\n" \
"ds_read_b64_tr_b16 a[240:241], v36 offset:40960\n" \
"s_lshl_b32 s7, s3, 1\n" \
"s_waitcnt lgkmcnt(7)\n" \
"v_mfma_f32_16x16x32_bf16 a[8:11], a[208:211], a[232:235], a[8:11]\n" \
"ds_read_b64_tr_b16 a[242:243], v36 offset:41984\n" \
"s_lshr_b32 s4, s2, 5\n" \
"v_mfma_f32_16x16x32_bf16 a[24:27], a[148:151], a[168:171], a[24:27]\n" \
"ds_read_b64_tr_b16 a[180:181], v37 offset:32768\n" \
"s_lshl_b32 s5, s6, 2\n" \
"v_mfma_f32_16x16x32_bf16 a[24:27], a[212:215], a[232:235], a[24:27]\n" \
"ds_read_b64_tr_b16 a[182:183], v37 offset:33792\n" \
"s_bfe_u32 s8, s2, 0x20003\n" \
"v_mfma_f32_16x16x32_bf16 a[28:31], a[172:175], a[148:151], a[28:31]\n" \
"ds_read_b64_tr_b16 a[244:245], v37 offset:40960\n" \
"s_and_b32 s7, s7, 4\n" \
"v_mfma_f32_16x16x32_bf16 a[28:31], a[236:239], a[212:215], a[28:31]\n" \
"s_add_u32 s89, s41, 0x3000\n" \
"buffer_store_dwordx4 v[192:195], v20, s[80:83], s89 offen offset:3072 sc1\n" \
"v_mfma_f32_16x16x32_bf16 a[20:23], a[164:167], a[148:151], a[20:23]\n" \
"ds_read_b64_tr_b16 a[246:247], v37 offset:41984\n" \
"s_and_b32 s9, s3, 1\n" \
"v_mfma_f32_16x16x32_bf16 a[20:23], a[228:231], a[212:215], a[20:23]\n" \
"ds_read_b64_tr_b16 a[184:185], v38 offset:32768\n" \
"s_or_b32 s13, s5, s8\n" \
"v_mfma_f32_16x16x32_bf16 a[16:19], a[160:163], a[148:151], a[16:19]\n" \
"ds_read_b64_tr_b16 a[186:187], v38 offset:33792\n" \
"s_or_b32 s9, s7, s9\n" \
"v_mfma_f32_16x16x32_bf16 a[16:19], a[224:227], a[212:215], a[16:19]\n" \
"s_mul_i32 s7, s4, 0xa00\n" \
"s_add_i32 s7, s7, 0x5bc1000\n" \
"v_mfma_f32_16x16x32_bf16 a[32:35], a[160:163], a[152:155], a[32:35]\n" \
"s_add_u32 s88, s41, 0x3000\n" \
"buffer_store_dwordx4 v[196:199], v20, s[80:83], s88 offen offset:2048 sc1\n" \
"v_mfma_f32_16x16x32_bf16 a[32:35], a[224:227], a[216:219], a[32:35]\n" \
"ds_read_b64_tr_b16 a[248:249], v38 offset:40960\n" \
"s_lshl_b32 s10, s13, 2\n" \
"v_mfma_f32_16x16x32_bf16 a[36:39], a[164:167], a[152:155], a[36:39]\n" \
"ds_read_b64_tr_b16 a[250:251], v38 offset:41984\n" \
"s_or_b32 s10, s10, s7\n" \
"v_mfma_f32_16x16x32_bf16 a[36:39], a[228:231], a[216:219], a[36:39]\n" \
"ds_read_b64_tr_b16 a[188:189], v39 offset:32768\n" \
"v_writelane_b32 v11, s10, 0\n" \
"v_mfma_f32_16x16x32_bf16 a[44:47], a[172:175], a[152:155], a[44:47]\n" \
"s_lshl_b32 s14, s4, 3\n" \
"v_writelane_b32 v11, s7, 1\n" \
"v_mfma_f32_16x16x32_bf16 a[44:47], a[236:239], a[216:219], a[44:47]\n" \
"ds_read_b64_tr_b16 a[190:191], v39 offset:33792\n" \
"s_or_b32 s7, s14, s6\n" \
"v_mfma_f32_16x16x32_bf16 a[40:43], a[152:155], a[168:171], a[40:43]\n" \
"s_add_u32 s89, s41, 0x3000\n" \
"buffer_store_dwordx4 a[84:87], v20, s[80:83], s89 offen offset:1024 sc1\n" \
"v_mfma_f32_16x16x32_bf16 a[40:43], a[216:219], a[232:235], a[40:43]\n" \
"ds_read_b64_tr_b16 a[252:253], v39 offset:40960\n" \
"s_lshl_b32 s7, s7, 10\n" \
"v_mfma_f32_16x16x32_bf16 a[56:59], a[156:159], a[168:171], a[56:59]\n" \
"ds_read_b64_tr_b16 a[254:255], v39 offset:41984\n" \
"s_lshl_b32 s10, s3, 8\n" \
"v_mfma_f32_16x16x32_bf16 a[56:59], a[220:223], a[232:235], a[56:59]\n" \
"s_or_b32 s7, s7, s10\n" \
"s_or_b32 s10, s7, 0x5c00200\n" \
"v_mfma_f32_16x16x32_bf16 a[60:63], a[172:175], a[156:159], a[60:63]\n" \
"v_writelane_b32 v11, s10, 2\n" \
"s_lshl_b32 s10, s8, 2\n" \
"v_mfma_f32_16x16x32_bf16 a[60:63], a[236:239], a[220:223], a[60:63]\n" \
"s_add_u32 s88, s41, 0x3000\n" \
"buffer_store_dwordx4 v[252:255], v20, s[80:83], s88 offen offset:0 sc1\n" \
"v_mfma_f32_16x16x32_bf16 a[52:55], a[164:167], a[156:159], a[52:55]\n" \
"s_or_b32 s11, s10, s7\n" \
"s_or_b32 s11, s11, 0x5c00000\n" \
"v_mfma_f32_16x16x32_bf16 a[52:55], a[228:231], a[220:223], a[52:55]\n" \
"v_writelane_b32 v11, s11, 3\n" \
"s_or_b32 s7, s7, 0x5c00080\n" \
"v_mfma_f32_16x16x32_bf16 a[48:51], a[160:163], a[156:159], a[48:51]\n" \
"v_writelane_b32 v11, s7, 4\n" \
"s_or_b32 s7, s7, s10\n" \
"v_mfma_f32_16x16x32_bf16 a[48:51], a[224:227], a[220:223], a[48:51]\n" \
"s_add_i32 s10, s3, s2\n" \
"v_writelane_b32 v11, s7, 5\n" \
"v_mfma_f32_16x16x32_bf16 a[64:67], a[164:167], a[140:143], a[64:67]\n" \
"s_mov_b32 s7, 2.0\n" \
"s_mov_b32 s11, 0x5bc0000\n" \
"v_mfma_f32_16x16x32_bf16 a[64:67], a[228:231], a[204:207], a[64:67]\n" \
"s_cmp_lg_u32 s10, 0\n" \
"s_cselect_b32 s10, s7, s11\n" \
"v_mfma_f32_16x16x32_bf16 a[80:83], a[172:175], a[140:143], a[80:83]\n" \
"s_lshr_b32 s15, s2, 3\n" \
"v_writelane_b32 v11, s10, 54\n" \
"v_mfma_f32_16x16x32_bf16 a[80:83], a[236:239], a[204:207], a[80:83]\n" \
"s_lshl_b32 s10, s6, 5\n" \
"s_min_u32 s16, s3, 2\n" \
"v_mfma_f32_16x16x32_bf16 a[68:71], a[140:143], a[168:171], a[68:71]\n" \
"s_mul_i32 s12, s8, 0x120\n" \
"v_mfma_f32_16x16x32_bf16 a[68:71], a[204:207], a[232:235], a[68:71]\n" \
"s_waitcnt vmcnt(4) lgkmcnt(0)\n" \
"s_barrier\n" \
"s_nop 0\n" \
"v_mfma_f32_16x16x32_bf16 v[116:119], a[176:179], a[144:147], v[116:119]\n" \
"s_add_u32 m0, s40, 0x8000\n" \
"s_add_u32 s85, s39, 0x6000\n" \
"v_mfma_f32_16x16x32_bf16 v[116:119], a[240:243], a[208:211], v[116:119]\n" \
"buffer_load_dwordx4 v20, s[80:83], s85 offen sc1 lds\n" \
"v_mfma_f32_16x16x32_bf16 v[120:123], a[180:183], a[144:147], v[120:123]\n" \
"ds_read_b64_tr_b16 a[160:161], v32 offset:0\n" \
"v_mfma_f32_16x16x32_bf16 v[120:123], a[244:247], a[208:211], v[120:123]\n" \
"ds_read_b64_tr_b16 a[162:163], v32 offset:512\n" \
"v_mfma_f32_16x16x32_bf16 v[124:127], a[184:187], a[144:147], v[124:127]\n" \
"s_add_u32 m0, s40, 0x9000\n" \
"s_add_u32 s85, s39, 0x7000\n" \
"v_mfma_f32_16x16x32_bf16 v[124:127], a[248:251], a[208:211], v[124:127]\n" \
"buffer_load_dwordx4 v20, s[80:83], s85 offen sc1 lds\n" \
"v_mfma_f32_16x16x32_bf16 v[128:131], a[188:191], a[144:147], v[128:131]\n" \
"ds_read_b64_tr_b16 a[224:225], v32 offset:4096\n" \
"v_mfma_f32_16x16x32_bf16 v[128:131], a[252:255], a[208:211], v[128:131]\n" \
"ds_read_b128 a[144:147], v28 offset:8192\n" \
"v_mfma_f32_16x16x32_bf16 v[144:147], a[188:191], a[148:151], v[144:147]\n" \
"ds_read_b128 a[208:211], v29 offset:8192\n" \
"v_mfma_f32_16x16x32_bf16 v[144:147], a[252:255], a[212:215], v[144:147]\n" \
"s_add_u32 m0, s40, 0xb000\n" \
"s_movk_i32 s84, 0x180\n" \
"v_mfma_f32_16x16x32_bf16 v[140:143], a[184:187], a[148:151], v[140:143]\n" \
"buffer_load_dwordx4 v21, s[76:79], s84 offen offset:0 lds\n" \
"v_mfma_f32_16x16x32_bf16 v[140:143], a[248:251], a[212:215], v[140:143]\n" \
"ds_read_b64_tr_b16 a[226:227], v32 offset:4608\n" \
"v_mfma_f32_16x16x32_bf16 v[136:139], a[180:183], a[148:151], v[136:139]\n" \
"s_add_u32 m0, s30, 0x8000\n" \
"s_movk_i32 s84, 0x180\n" \
"v_mfma_f32_16x16x32_bf16 v[136:139], a[244:247], a[212:215], v[136:139]\n" \
"buffer_load_dwordx4 v24, s[76:79], s84 offen offset:0 lds\n" \
"v_mfma_f32_16x16x32_bf16 v[132:135], a[176:179], a[148:151], v[132:135]\n" \
"ds_read_b64_tr_b16 a[164:165], v33 offset:0\n" \
"v_mfma_f32_16x16x32_bf16 v[132:135], a[240:243], a[212:215], v[132:135]\n" \
"ds_read_b128 a[148:151], v28 offset:10240\n" \
"v_mfma_f32_16x16x32_bf16 v[148:151], a[176:179], a[152:155], v[148:151]\n" \
"ds_read_b128 a[212:215], v29 offset:10240\n" \
"v_mfma_f32_16x16x32_bf16 v[148:151], a[240:243], a[216:219], v[148:151]\n" \
"s_movk_i32 s84, 0x2180\n" \
"v_mfma_f32_16x16x32_bf16 v[152:155], a[180:183], a[152:155], v[152:155]\n" \
"buffer_load_dwordx4 v25, s[76:79], s84 offen offset:1024 lds\n" \
"v_mfma_f32_16x16x32_bf16 v[152:155], a[244:247], a[216:219], v[152:155]\n" \
"ds_read_b64_tr_b16 a[166:167], v33 offset:512\n" \
"v_mfma_f32_16x16x32_bf16 v[156:159], a[184:187], a[152:155], v[156:159]\n" \
"ds_read_b64_tr_b16 a[228:229], v33 offset:4096\n" \
"v_mfma_f32_16x16x32_bf16 v[156:159], a[248:251], a[216:219], v[156:159]\n" \
"s_movk_i32 s84, 0x4180\n" \
"v_mfma_f32_16x16x32_bf16 v[160:163], a[188:191], a[152:155], v[160:163]\n" \
"buffer_load_dwordx4 v24, s[76:79], s84 offen offset:2048 lds\n" \
"v_mfma_f32_16x16x32_bf16 v[160:163], a[252:255], a[216:219], v[160:163]\n" \
"ds_read_b128 a[152:155], v28 offset:12288\n" \
"v_mfma_f32_16x16x32_bf16 v[176:179], a[188:191], a[156:159], v[176:179]\n" \
"ds_read_b128 a[216:219], v29 offset:12288\n" \
"v_mfma_f32_16x16x32_bf16 v[176:179], a[252:255], a[220:223], v[176:179]\n" \
"s_movk_i32 s84, 0x6180\n" \
"v_mfma_f32_16x16x32_bf16 v[172:175], a[184:187], a[156:159], v[172:175]\n" \
"buffer_load_dwordx4 v25, s[76:79], s84 offen offset:3072 lds\n" \
"v_mfma_f32_16x16x32_bf16 v[172:175], a[248:251], a[220:223], v[172:175]\n" \
"ds_read_b64_tr_b16 a[230:231], v33 offset:4608\n" \
"v_mfma_f32_16x16x32_bf16 v[168:171], a[180:183], a[156:159], v[168:171]\n" \
"ds_read_b64_tr_b16 a[172:173], v35 offset:0\n" \
"v_mfma_f32_16x16x32_bf16 v[168:171], a[244:247], a[220:223], v[168:171]\n" \
"ds_read_b64_tr_b16 a[174:175], v35 offset:512\n" \
"v_mfma_f32_16x16x32_bf16 v[164:167], a[176:179], a[156:159], v[164:167]\n" \
"s_add_u32 s11, s22, s12\n" \
"s_addc_u32 s17, s23, 0\n" \
"v_mfma_f32_16x16x32_bf16 v[164:167], a[240:243], a[220:223], v[164:167]\n" \
"ds_read_b128 a[156:159], v28 offset:14336\n" \
"v_mfma_f32_16x16x32_bf16 v[84:87], a[176:179], a[136:139], v[84:87]\n" \
"ds_read_b128 a[220:223], v29 offset:14336\n" \
"v_mfma_f32_16x16x32_bf16 v[84:87], a[240:243], a[200:203], v[84:87]\n" \
"s_add_u32 m0, s31, 0x8000\n" \
"s_mov_b32 s84, 0x180000\n" \
"v_mfma_f32_16x16x32_bf16 v[88:91], a[180:183], a[136:139], v[88:91]\n" \
"buffer_load_dwordx4 v26, s[68:71], s84 offen offset:0 lds\n" \
"v_mfma_f32_16x16x32_bf16 v[88:91], a[244:247], a[200:203], v[88:91]\n" \
"s_addk_i32 s84, 0x7c00\n" \
"v_mfma_f32_16x16x32_bf16 v[92:95], a[184:187], a[136:139], v[92:95]\n" \
"buffer_load_dwordx4 v26, s[68:71], s84 offen offset:1024 lds\n" \
"v_mfma_f32_16x16x32_bf16 v[92:95], a[248:251], a[200:203], v[92:95]\n" \
"ds_read_b64_tr_b16 a[236:237], v35 offset:4096\n" \
"v_mfma_f32_16x16x32_bf16 v[96:99], a[188:191], a[136:139], v[96:99]\n" \
"ds_read_b64_tr_b16 a[238:239], v35 offset:4608\n" \
"v_mfma_f32_16x16x32_bf16 v[96:99], a[252:255], a[200:203], v[96:99]\n" \
"ds_read_b128 a[136:139], v28 offset:4096\n" \
"v_mfma_f32_16x16x32_bf16 v[112:115], a[188:191], a[140:143], v[112:115]\n" \
"ds_read_b128 a[200:203], v29 offset:4096\n" \
"v_mfma_f32_16x16x32_bf16 v[112:115], a[252:255], a[204:207], v[112:115]\n" \
"s_addk_i32 s84, 0x7c00\n" \
"v_mfma_f32_16x16x32_bf16 v[108:111], a[184:187], a[140:143], v[108:111]\n" \
"buffer_load_dwordx4 v27, s[68:71], s84 offen offset:2048 lds\n" \
"v_mfma_f32_16x16x32_bf16 v[108:111], a[248:251], a[204:207], v[108:111]\n" \
"s_addk_i32 s84, 0x7c00\n" \
"v_mfma_f32_16x16x32_bf16 v[104:107], a[180:183], a[140:143], v[104:107]\n" \
"buffer_load_dwordx4 v27, s[68:71], s84 offen offset:3072 lds\n" \
"v_mfma_f32_16x16x32_bf16 v[104:107], a[244:247], a[204:207], v[104:107]\n" \
"ds_read_b64_tr_b16 a[168:169], v34 offset:0\n" \
"v_mfma_f32_16x16x32_bf16 v[100:103], a[176:179], a[140:143], v[100:103]\n" \
"ds_read_b64_tr_b16 a[170:171], v34 offset:512\n" \
"v_mfma_f32_16x16x32_bf16 v[100:103], a[240:243], a[204:207], v[100:103]\n" \
"ds_read_b128 a[140:143], v28 offset:6144\n" \
"v_mfma_f32_16x16x32_bf16 v[52:55], a[176:179], a[128:131], v[52:55]\n" \
"ds_read_b128 a[204:207], v29 offset:6144\n" \
"v_mfma_f32_16x16x32_bf16 v[52:55], a[240:243], a[192:195], v[52:55]\n" \
"s_add_u32 m0, s30, 0x9000\n" \
"s_mov_b32 s84, 0x9180\n" \
"v_mfma_f32_16x16x32_bf16 v[56:59], a[180:183], a[128:131], v[56:59]\n" \
"buffer_load_dwordx4 v24, s[76:79], s84 offen offset:0 lds\n" \
"v_mfma_f32_16x16x32_bf16 v[56:59], a[244:247], a[192:195], v[56:59]\n" \
"s_addk_i32 s84, 0x2000\n" \
"s_nop 0\n" \
"v_mfma_f32_16x16x32_bf16 v[60:63], a[184:187], a[128:131], v[60:63]\n" \
"buffer_load_dwordx4 v25, s[76:79], s84 offen offset:1024 lds\n" \
"v_mfma_f32_16x16x32_bf16 v[60:63], a[248:251], a[192:195], v[60:63]\n" \
"ds_read_b64_tr_b16 a[232:233], v34 offset:4096\n" \
"v_mfma_f32_16x16x32_bf16 v[64:67], a[188:191], a[128:131], v[64:67]\n" \
"ds_read_b64_tr_b16 a[234:235], v34 offset:4608\n" \
"v_mfma_f32_16x16x32_bf16 v[64:67], a[252:255], a[192:195], v[64:67]\n" \
"ds_read_b128 a[128:131], v28 offset:0\n" \
"v_mfma_f32_16x16x32_bf16 v[80:83], a[188:191], a[132:135], v[80:83]\n" \
"ds_read_b128 a[192:195], v29 offset:0\n" \
"v_mfma_f32_16x16x32_bf16 v[80:83], a[252:255], a[196:199], v[80:83]\n" \
"s_add_u32 s18, s24, s12\n" \
"v_writelane_b32 v11, s11, 9\n" \
"s_addc_u32 s19, s25, 0\n" \
"v_mfma_f32_16x16x32_bf16 v[76:79], a[184:187], a[132:135], v[76:79]\n" \
"s_and_b32 s20, s17, 0xffff\n" \
"v_writelane_b32 v11, s20, 10\n" \
"v_mfma_f32_16x16x32_bf16 v[76:79], a[248:251], a[196:199], v[76:79]\n" \
"s_and_b32 s20, s19, 0xffff\n" \
"v_writelane_b32 v11, s18, 11\n" \
"v_mfma_f32_16x16x32_bf16 v[72:75], a[180:183], a[132:135], v[72:75]\n" \
"s_cmpk_lt_u32 s3, 3\n" \
"s_cselect_b32 s11, s11, s18\n" \
"v_mfma_f32_16x16x32_bf16 v[72:75], a[244:247], a[196:199], v[72:75]\n" \
"s_movk_i32 s18, 0xfff\n" \
"v_writelane_b32 v11, s20, 12\n" \
"v_mfma_f32_16x16x32_bf16 v[68:71], a[176:179], a[132:135], v[68:71]\n" \
"s_and_b32 s20, s29, 0xffff\n" \
"v_writelane_b32 v11, s28, 13\n" \
"v_mfma_f32_16x16x32_bf16 v[68:71], a[240:243], a[196:199], v[68:71]\n" \
"ds_read_b128 a[132:135], v28 offset:2048\n" \
"ds_read_b128 a[196:199], v29 offset:2048\n" \
"s_waitcnt lgkmcnt(15)\n" \
"v_mfma_f32_16x16x32_bf16 a[0:3], a[160:163], a[144:147], a[0:3]\n" \
"s_addk_i32 s84, 0x2000\n" \
"v_mfma_f32_16x16x32_bf16 a[0:3], a[224:227], a[208:211], a[0:3]\n" \
"buffer_load_dwordx4 v24, s[76:79], s84 offen offset:2048 lds\n" \
"v_mfma_f32_16x16x32_bf16 a[4:7], a[164:167], a[144:147], a[4:7]\n" \
"ds_read_b64_tr_b16 a[176:177], v36 offset:0\n" \
"v_writelane_b32 v11, s20, 14\n" \
"v_mfma_f32_16x16x32_bf16 a[4:7], a[228:231], a[208:211], a[4:7]\n" \
"s_addk_i32 s84, 0x2000\n" \
"v_mfma_f32_16x16x32_bf16 a[12:15], a[172:175], a[144:147], a[12:15]\n" \
"buffer_load_dwordx4 v25, s[76:79], s84 offen offset:3072 lds\n" \
"s_waitcnt lgkmcnt(13)\n" \
"v_mfma_f32_16x16x32_bf16 a[12:15], a[236:239], a[208:211], a[12:15]\n" \
"ds_read_b64_tr_b16 a[178:179], v36 offset:1024\n" \
"v_writelane_b32 v11, s11, 15\n" \
"s_waitcnt lgkmcnt(10)\n" \
"v_mfma_f32_16x16x32_bf16 a[8:11], a[144:147], a[168:171], a[8:11]\n" \
"ds_read_b64_tr_b16 a[240:241], v36 offset:8192\n" \
"s_waitcnt lgkmcnt(7)\n" \
"s_nop 0\n" \
"v_mfma_f32_16x16x32_bf16 a[8:11], a[208:211], a[232:235], a[8:11]\n" \
"ds_read_b64_tr_b16 a[242:243], v36 offset:9216\n" \
"v_mfma_f32_16x16x32_bf16 a[24:27], a[148:151], a[168:171], a[24:27]\n" \
"ds_read_b64_tr_b16 a[180:181], v37 offset:0\n" \
"v_mfma_f32_16x16x32_bf16 a[24:27], a[212:215], a[232:235], a[24:27]\n" \
"ds_read_b64_tr_b16 a[182:183], v37 offset:1024\n" \
"v_mfma_f32_16x16x32_bf16 a[28:31], a[172:175], a[148:151], a[28:31]\n" \
"ds_read_b64_tr_b16 a[244:245], v37 offset:8192\n" \
"v_mfma_f32_16x16x32_bf16 a[28:31], a[236:239], a[212:215], a[28:31]\n" \
"s_add_u32 s89, s41, 0x1000\n" \
"buffer_store_dwordx4 v[200:203], v20, s[80:83], s89 offen offset:3072 sc1\n" \
"v_mfma_f32_16x16x32_bf16 a[20:23], a[164:167], a[148:151], a[20:23]\n" \
"ds_read_b64_tr_b16 a[246:247], v37 offset:9216\n" \
"v_mfma_f32_16x16x32_bf16 a[20:23], a[228:231], a[212:215], a[20:23]\n" \
"ds_read_b64_tr_b16 a[184:185], v38 offset:0\n" \
"v_mfma_f32_16x16x32_bf16 a[16:19], a[160:163], a[148:151], a[16:19]\n" \
"ds_read_b64_tr_b16 a[186:187], v38 offset:1024\n" \
"v_mfma_f32_16x16x32_bf16 a[16:19], a[224:227], a[212:215], a[16:19]\n" \
"s_cmpk_lt_u32 s3, 3\n" \
"s_cselect_b32 s11, s17, s19\n" \
"v_mfma_f32_16x16x32_bf16 a[32:35], a[160:163], a[152:155], a[32:35]\n" \
"s_add_u32 s88, s41, 0x1000\n" \
"buffer_store_dwordx4 v[204:207], v20, s[80:83], s88 offen offset:2048 sc1\n" \
"v_mfma_f32_16x16x32_bf16 a[32:35], a[224:227], a[216:219], a[32:35]\n" \
"ds_read_b64_tr_b16 a[248:249], v38 offset:8192\n" \
"s_and_b32 s11, s11, 0xffff\n" \
"v_mfma_f32_16x16x32_bf16 a[36:39], a[164:167], a[152:155], a[36:39]\n" \
"ds_read_b64_tr_b16 a[250:251], v38 offset:9216\n" \
"v_writelane_b32 v11, s11, 16\n" \
"v_mfma_f32_16x16x32_bf16 a[36:39], a[228:231], a[216:219], a[36:39]\n" \
"ds_read_b64_tr_b16 a[188:189], v39 offset:0\n" \
"s_mul_i32 s11, s6, 0x48\n" \
"v_mfma_f32_16x16x32_bf16 a[44:47], a[172:175], a[152:155], a[44:47]\n" \
"s_add_i32 s17, s11, 0x47\n" \
"v_mfma_f32_16x16x32_bf16 a[44:47], a[236:239], a[216:219], a[44:47]\n" \
"ds_read_b64_tr_b16 a[190:191], v39 offset:1024\n" \
"v_mfma_f32_16x16x32_bf16 a[40:43], a[152:155], a[168:171], a[40:43]\n" \
"s_add_u32 s89, s41, 0x1000\n" \
"buffer_store_dwordx4 v[208:211], v20, s[80:83], s89 offen offset:1024 sc1\n" \
"v_mfma_f32_16x16x32_bf16 a[40:43], a[216:219], a[232:235], a[40:43]\n" \
"ds_read_b64_tr_b16 a[252:253], v39 offset:8192\n" \
"v_mfma_f32_16x16x32_bf16 a[56:59], a[156:159], a[168:171], a[56:59]\n" \
"ds_read_b64_tr_b16 a[254:255], v39 offset:9216\n" \
"v_mfma_f32_16x16x32_bf16 a[56:59], a[220:223], a[232:235], a[56:59]\n" \
"s_cmpk_lt_u32 s3, 3\n" \
"s_cselect_b32 s17, s17, s18\n" \
"v_mfma_f32_16x16x32_bf16 a[60:63], a[172:175], a[156:159], a[60:63]\n" \
"s_mulk_i32 s17, 0x480\n" \
"s_addk_i32 s17, 0x120\n" \
"v_mfma_f32_16x16x32_bf16 a[60:63], a[236:239], a[220:223], a[60:63]\n" \
"s_add_u32 s88, s41, 0x1000\n" \
"buffer_store_dwordx4 v[212:215], v20, s[80:83], s88 offen offset:0 sc1\n" \
".fill (L_pb + 65248 - .) / 4, 4, 0xBF800000\n" \
"L_pp16:\n" \
"s_cbranch_execz L_pret\n" \
"v_mfma_f32_16x16x32_bf16 a[52:55], a[164:167], a[156:159], a[52:55]\n" \
"v_writelane_b32 v11, s17, 17\n" \
"s_lshl_b32 s17, s3, 5\n" \
"v_mfma_f32_16x16x32_bf16 a[52:55], a[228:231], a[220:223], a[52:55]\n" \
"s_add_i32 s11, s11, s17\n" \
"s_mul_i32 s17, s11, 0x480\n" \
"v_mfma_f32_16x16x32_bf16 a[48:51], a[160:163], a[156:159], a[48:51]\n" \
"s_mul_i32 s11, s13, 0x24000\n" \
"s_add_i32 s18, s11, 0x1b000\n" \
"v_mfma_f32_16x16x32_bf16 a[48:51], a[224:227], a[220:223], a[48:51]\n" \
"s_cmpk_lt_u32 s3, 3\n" \
"s_cselect_b32 s17, s17, s18\n" \
"v_mfma_f32_16x16x32_bf16 a[64:67], a[164:167], a[140:143], a[64:67]\n" \
"s_mov_b32 s18, 0x8000\n" \
"v_writelane_b32 v11, s17, 18\n" \
"v_mfma_f32_16x16x32_bf16 a[64:67], a[228:231], a[204:207], a[64:67]\n" \
"s_mul_i32 s17, s16, 0x2400\n" \
"s_mul_i32 s16, s16, 0x9000\n" \
"v_mfma_f32_16x16x32_bf16 a[80:83], a[172:175], a[140:143], a[80:83]\n" \
"s_bitset1_b32 s17, 17\n" \
"v_writelane_b32 v11, s17, 19\n" \
"v_mfma_f32_16x16x32_bf16 a[80:83], a[236:239], a[204:207], a[80:83]\n" \
"s_add_i32 s16, s11, s16\n" \
"v_writelane_b32 v11, s16, 26\n" \
"v_mfma_f32_16x16x32_bf16 a[68:71], a[140:143], a[168:171], a[68:71]\n" \
"s_mul_i32 s16, s3, 0x2400\n" \
"s_add_i32 s17, s16, 0xa400\n" \
"v_mfma_f32_16x16x32_bf16 a[68:71], a[204:207], a[232:235], a[68:71]\n" \
"s_waitcnt vmcnt(4) lgkmcnt(0)\n" \
"s_barrier\n" \
"v_mfma_f32_16x16x32_bf16 v[116:119], a[176:179], a[144:147], v[116:119]\n" \
"s_add_u32 m0, s40, 0x0\n" \
"s_add_u32 s85, s39, 0x8000\n" \
"v_mfma_f32_16x16x32_bf16 v[116:119], a[240:243], a[208:211], v[116:119]\n" \
"buffer_load_dwordx4 v20, s[80:83], s85 offen sc1 lds\n" \
"v_mfma_f32_16x16x32_bf16 v[120:123], a[180:183], a[144:147], v[120:123]\n" \
"ds_read_b64_tr_b16 a[160:161], v32 offset:32768\n" \
"v_mfma_f32_16x16x32_bf16 v[120:123], a[244:247], a[208:211], v[120:123]\n" \
"ds_read_b64_tr_b16 a[162:163], v32 offset:33280\n" \
"v_mfma_f32_16x16x32_bf16 v[124:127], a[184:187], a[144:147], v[124:127]\n" \
"s_add_u32 m0, s40, 0x1000\n" \
"s_add_u32 s85, s39, 0x9000\n" \
"v_mfma_f32_16x16x32_bf16 v[124:127], a[248:251], a[208:211], v[124:127]\n" \
"buffer_load_dwordx4 v20, s[80:83], s85 offen sc1 lds\n" \
"v_mfma_f32_16x16x32_bf16 v[128:131], a[188:191], a[144:147], v[128:131]\n" \
"ds_read_b64_tr_b16 a[224:225], v32 offset:36864\n" \
"v_mfma_f32_16x16x32_bf16 v[128:131], a[252:255], a[208:211], v[128:131]\n" \
"ds_read_b128 a[144:147], v28 offset:40960\n" \
"v_mfma_f32_16x16x32_bf16 v[144:147], a[188:191], a[148:151], v[144:147]\n" \
"ds_read_b128 a[208:211], v29 offset:40960\n" \
"v_mfma_f32_16x16x32_bf16 v[144:147], a[252:255], a[212:215], v[144:147]\n" \
"s_add_u32 m0, s40, 0x3000\n" \
"s_movk_i32 s84, 0x200\n" \
"v_mfma_f32_16x16x32_bf16 v[140:143], a[184:187], a[148:151], v[140:143]\n" \
"buffer_load_dwordx4 v21, s[76:79], s84 offen offset:0 lds\n" \
"v_mfma_f32_16x16x32_bf16 v[140:143], a[248:251], a[212:215], v[140:143]\n" \
"ds_read_b64_tr_b16 a[226:227], v32 offset:37376\n" \
"v_mfma_f32_16x16x32_bf16 v[136:139], a[180:183], a[148:151], v[136:139]\n" \
"s_add_u32 m0, s30, 0x0\n" \
"s_movk_i32 s84, 0x200\n" \
"v_mfma_f32_16x16x32_bf16 v[136:139], a[244:247], a[212:215], v[136:139]\n" \
"buffer_load_dwordx4 v24, s[76:79], s84 offen offset:0 lds\n" \
"v_mfma_f32_16x16x32_bf16 v[132:135], a[176:179], a[148:151], v[132:135]\n" \
"ds_read_b64_tr_b16 a[164:165], v33 offset:32768\n" \
"v_mfma_f32_16x16x32_bf16 v[132:135], a[240:243], a[212:215], v[132:135]\n" \
"ds_read_b128 a[148:151], v28 offset:43008\n" \
"v_mfma_f32_16x16x32_bf16 v[148:151], a[176:179], a[152:155], v[148:151]\n" \
"ds_read_b128 a[212:215], v29 offset:43008\n" \
"v_mfma_f32_16x16x32_bf16 v[148:151], a[240:243], a[216:219], v[148:151]\n" \
"s_movk_i32 s84, 0x2200\n" \
"v_mfma_f32_16x16x32_bf16 v[152:155], a[180:183], a[152:155], v[152:155]\n" \
"buffer_load_dwordx4 v25, s[76:79], s84 offen offset:1024 lds\n" \
"v_mfma_f32_16x16x32_bf16 v[152:155], a[244:247], a[216:219], v[152:155]\n" \
"ds_read_b64_tr_b16 a[166:167], v33 offset:33280\n" \
"v_mfma_f32_16x16x32_bf16 v[156:159], a[184:187], a[152:155], v[156:159]\n" \
"ds_read_b64_tr_b16 a[228:229], v33 offset:36864\n" \
"v_mfma_f32_16x16x32_bf16 v[156:159], a[248:251], a[216:219], v[156:159]\n" \
"s_movk_i32 s84, 0x4200\n" \
"v_mfma_f32_16x16x32_bf16 v[160:163], a[188:191], a[152:155], v[160:163]\n" \
"buffer_load_dwordx4 v24, s[76:79], s84 offen offset:2048 lds\n" \
"v_mfma_f32_16x16x32_bf16 v[160:163], a[252:255], a[216:219], v[160:163]\n" \
"ds_read_b128 a[152:155], v28 offset:45056\n" \
"v_mfma_f32_16x16x32_bf16 v[176:179], a[188:191], a[156:159], v[176:179]\n" \
"ds_read_b128 a[216:219], v29 offset:45056\n" \
"v_mfma_f32_16x16x32_bf16 v[176:179], a[252:255], a[220:223], v[176:179]\n" \
"s_movk_i32 s84, 0x6200\n" \
"v_mfma_f32_16x16x32_bf16 v[172:175], a[184:187], a[156:159], v[172:175]\n" \
"buffer_load_dwordx4 v25, s[76:79], s84 offen offset:3072 lds\n" \
"v_mfma_f32_16x16x32_bf16 v[172:175], a[248:251], a[220:223], v[172:175]\n" \
"ds_read_b64_tr_b16 a[230:231], v33 offset:37376\n" \
"v_mfma_f32_16x16x32_bf16 v[168:171], a[180:183], a[156:159], v[168:171]\n" \
"ds_read_b64_tr_b16 a[172:173], v35 offset:32768\n" \
"v_mfma_f32_16x16x32_bf16 v[168:171], a[244:247], a[220:223], v[168:171]\n" \
"ds_read_b64_tr_b16 a[174:175], v35 offset:33280\n" \
"v_mfma_f32_16x16x32_bf16 v[164:167], a[176:179], a[156:159], v[164:167]\n" \
"s_cmpk_lt_u32 s3, 3\n" \
"s_cselect_b32 s17, s17, s18\n" \
"v_mfma_f32_16x16x32_bf16 v[164:167], a[240:243], a[220:223], v[164:167]\n" \
"ds_read_b128 a[156:159], v28 offset:47104\n" \
"s_mov_b32 s18, 0x1b000\n" \
"v_mfma_f32_16x16x32_bf16 v[84:87], a[176:179], a[136:139], v[84:87]\n" \
"ds_read_b128 a[220:223], v29 offset:47104\n" \
"s_xor_b32 s19, s3, 2\n" \
"v_mfma_f32_16x16x32_bf16 v[84:87], a[240:243], a[200:203], v[84:87]\n" \
"s_add_u32 m0, s31, 0x0\n" \
"s_mov_b32 s84, 0x200000\n" \
"v_mfma_f32_16x16x32_bf16 v[88:91], a[180:183], a[136:139], v[88:91]\n" \
"buffer_load_dwordx4 v26, s[68:71], s84 offen offset:0 lds\n" \
"v_mfma_f32_16x16x32_bf16 v[88:91], a[244:247], a[200:203], v[88:91]\n" \
"s_addk_i32 s84, 0x7c00\n" \
"s_nop 0\n" \
"v_mfma_f32_16x16x32_bf16 v[92:95], a[184:187], a[136:139], v[92:95]\n" \
"buffer_load_dwordx4 v26, s[68:71], s84 offen offset:1024 lds\n" \
"v_mfma_f32_16x16x32_bf16 v[92:95], a[248:251], a[200:203], v[92:95]\n" \
"ds_read_b64_tr_b16 a[236:237], v35 offset:36864\n" \
"v_mfma_f32_16x16x32_bf16 v[96:99], a[188:191], a[136:139], v[96:99]\n" \
"ds_read_b64_tr_b16 a[238:239], v35 offset:37376\n" \
"v_mfma_f32_16x16x32_bf16 v[96:99], a[252:255], a[200:203], v[96:99]\n" \
"ds_read_b128 a[136:139], v28 offset:36864\n" \
"v_mfma_f32_16x16x32_bf16 v[112:115], a[188:191], a[140:143], v[112:115]\n" \
"ds_read_b128 a[200:203], v29 offset:36864\n" \
"v_mfma_f32_16x16x32_bf16 v[112:115], a[252:255], a[204:207], v[112:115]\n" \
"s_addk_i32 s84, 0x7c00\n" \
"v_mfma_f32_16x16x32_bf16 v[108:111], a[184:187], a[140:143], v[108:111]\n" \
"buffer_load_dwordx4 v27, s[68:71], s84 offen offset:2048 lds\n" \
"v_mfma_f32_16x16x32_bf16 v[108:111], a[248:251], a[204:207], v[108:111]\n" \
"s_addk_i32 s84, 0x7c00\n" \
"v_mfma_f32_16x16x32_bf16 v[104:107], a[180:183], a[140:143], v[104:107]\n" \
"buffer_load_dwordx4 v27, s[68:71], s84 offen offset:3072 lds\n" \
"v_mfma_f32_16x16x32_bf16 v[104:107], a[244:247], a[204:207], v[104:107]\n" \
"ds_read_b64_tr_b16 a[168:169], v34 offset:32768\n" \
"v_mfma_f32_16x16x32_bf16 v[100:103], a[176:179], a[140:143], v[100:103]\n" \
"ds_read_b64_tr_b16 a[170:171], v34 offset:33280\n" \
"v_mfma_f32_16x16x32_bf16 v[100:103], a[240:243], a[204:207], v[100:103]\n" \
"ds_read_b128 a[140:143], v28 offset:38912\n" \
"v_mfma_f32_16x16x32_bf16 v[52:55], a[176:179], a[128:131], v[52:55]\n" \
"ds_read_b128 a[204:207], v29 offset:38912\n" \
"v_mfma_f32_16x16x32_bf16 v[52:55], a[240:243], a[192:195], v[52:55]\n" \
"s_add_u32 m0, s30, 0x1000\n" \
"s_mov_b32 s84, 0x9200\n" \
"v_mfma_f32_16x16x32_bf16 v[56:59], a[180:183], a[128:131], v[56:59]\n" \
"buffer_load_dwordx4 v24, s[76:79], s84 offen offset:0 lds\n" \
"v_mfma_f32_16x16x32_bf16 v[56:59], a[244:247], a[192:195], v[56:59]\n" \
"s_addk_i32 s84, 0x2000\n" \
"s_nop 0\n" \
"v_mfma_f32_16x16x32_bf16 v[60:63], a[184:187], a[128:131], v[60:63]\n" \
"buffer_load_dwordx4 v25, s[76:79], s84 offen offset:1024 lds\n" \
"v_mfma_f32_16x16x32_bf16 v[60:63], a[248:251], a[192:195], v[60:63]\n" \
"ds_read_b64_tr_b16 a[232:233], v34 offset:36864\n" \
"v_mfma_f32_16x16x32_bf16 v[64:67], a[188:191], a[128:131], v[64:67]\n" \
"ds_read_b64_tr_b16 a[234:235], v34 offset:37376\n" \
"v_mfma_f32_16x16x32_bf16 v[64:67], a[252:255], a[192:195], v[64:67]\n" \
"ds_read_b128 a[128:131], v28 offset:32768\n" \
"v_mfma_f32_16x16x32_bf16 v[80:83], a[188:191], a[132:135], v[80:83]\n" \
"ds_read_b128 a[192:195], v29 offset:32768\n" \
"v_mfma_f32_16x16x32_bf16 v[80:83], a[252:255], a[196:199], v[80:83]\n" \
"s_cmp_lg_u32 s19, 0\n" \
"s_cselect_b32 s18, s17, s18\n" \
"v_mfma_f32_16x16x32_bf16 v[76:79], a[184:187], a[132:135], v[76:79]\n" \
"v_writelane_b32 v11, s17, 20\n" \
"s_or_b32 s17, s16, 0x10000\n" \
"v_mfma_f32_16x16x32_bf16 v[76:79], a[248:251], a[196:199], v[76:79]\n" \
"v_writelane_b32 v11, s18, 21\n" \
"s_add_i32 s18, s16, 0x19000\n" \
"v_mfma_f32_16x16x32_bf16 v[72:75], a[180:183], a[132:135], v[72:75]\n" \
"v_writelane_b32 v11, s17, 22\n" \
"s_mov_b32 s17, 0\n" \
"v_mfma_f32_16x16x32_bf16 v[72:75], a[244:247], a[196:199], v[72:75]\n" \
"s_cmpk_lt_u32 s3, 3\n" \
"s_cselect_b32 s18, s18, s17\n" \
"v_mfma_f32_16x16x32_bf16 v[68:71], a[176:179], a[132:135], v[68:71]\n" \
"s_bitset1_b32 s16, 17\n" \
"v_writelane_b32 v11, s18, 23\n" \
"v_mfma_f32_16x16x32_bf16 v[68:71], a[240:243], a[196:199], v[68:71]\n" \
"ds_read_b128 a[132:135], v28 offset:34816\n" \
"ds_read_b128 a[196:199], v29 offset:34816\n" \
"s_waitcnt lgkmcnt(15)\n" \
"v_mfma_f32_16x16x32_bf16 a[0:3], a[160:163], a[144:147], a[0:3]\n" \
"s_addk_i32 s84, 0x2000\n" \
"v_mfma_f32_16x16x32_bf16 a[0:3], a[224:227], a[208:211], a[0:3]\n" \
"buffer_load_dwordx4 v24, s[76:79], s84 offen offset:2048 lds\n" \
"v_mfma_f32_16x16x32_bf16 a[4:7], a[164:167], a[144:147], a[4:7]\n" \
"ds_read_b64_tr_b16 a[176:177], v36 offset:32768\n" \
"s_movk_i32 s18, 0x2400\n" \
"v_mfma_f32_16x16x32_bf16 a[4:7], a[228:231], a[208:211], a[4:7]\n" \
"s_addk_i32 s84, 0x2000\n" \
"v_mfma_f32_16x16x32_bf16 a[12:15], a[172:175], a[144:147], a[12:15]\n" \
"buffer_load_dwordx4 v25, s[76:79], s84 offen offset:3072 lds\n" \
"s_waitcnt lgkmcnt(13)\n" \
"v_mfma_f32_16x16x32_bf16 a[12:15], a[236:239], a[208:211], a[12:15]\n" \
"ds_read_b64_tr_b16 a[178:179], v36 offset:33792\n" \
"s_waitcnt lgkmcnt(10)\n" \
"v_mfma_f32_16x16x32_bf16 a[8:11], a[144:147], a[168:171], a[8:11]\n" \
"ds_read_b64_tr_b16 a[240:241], v36 offset:40960\n" \
"s_waitcnt lgkmcnt(7)\n" \
"v_mfma_f32_16x16x32_bf16 a[8:11], a[208:211], a[232:235], a[8:11]\n" \
"ds_read_b64_tr_b16 a[242:243], v36 offset:41984\n" \
"v_mfma_f32_16x16x32_bf16 a[24:27], a[148:151], a[168:171], a[24:27]\n" \
"ds_read_b64_tr_b16 a[180:181], v37 offset:32768\n" \
"v_mfma_f32_16x16x32_bf16 a[24:27], a[212:215], a[232:235], a[24:27]\n" \
"ds_read_b64_tr_b16 a[182:183], v37 offset:33792\n" \
"v_mfma_f32_16x16x32_bf16 a[28:31], a[172:175], a[148:151], a[28:31]\n" \
"ds_read_b64_tr_b16 a[244:245], v37 offset:40960\n" \
"v_mfma_f32_16x16x32_bf16 a[28:31], a[236:239], a[212:215], a[28:31]\n" \
"s_add_u32 s89, s42, 0x0\n" \
"buffer_store_dwordx4 v[216:219], v20, s[80:83], s89 offen offset:1024 sc1\n" \
"v_mfma_f32_16x16x32_bf16 a[20:23], a[164:167], a[148:151], a[20:23]\n" \
"ds_read_b64_tr_b16 a[246:247], v37 offset:41984\n" \
"v_mfma_f32_16x16x32_bf16 a[20:23], a[228:231], a[212:215], a[20:23]\n" \
"ds_read_b64_tr_b16 a[184:185], v38 offset:32768\n" \
"v_mfma_f32_16x16x32_bf16 a[16:19], a[160:163], a[148:151], a[16:19]\n" \
"ds_read_b64_tr_b16 a[186:187], v38 offset:33792\n" \
"v_mfma_f32_16x16x32_bf16 a[16:19], a[224:227], a[212:215], a[16:19]\n" \
"s_cmpk_lt_u32 s3, 3\n" \
"s_cselect_b32 s16, s16, s18\n" \
"v_mfma_f32_16x16x32_bf16 a[32:35], a[160:163], a[152:155], a[32:35]\n" \
"s_add_u32 s88, s42, 0x0\n" \
"buffer_store_dwordx4 v[220:223], v20, s[80:83], s88 offen offset:0 sc1\n" \
"v_mfma_f32_16x16x32_bf16 a[32:35], a[224:227], a[216:219], a[32:35]\n" \
"ds_read_b64_tr_b16 a[248:249], v38 offset:40960\n" \
"s_mul_i32 s19, s3, 0x9000\n" \
"v_mfma_f32_16x16x32_bf16 a[36:39], a[164:167], a[152:155], a[36:39]\n" \
"ds_read_b64_tr_b16 a[250:251], v38 offset:41984\n" \
"v_writelane_b32 v11, s16, 24\n" \
"v_mfma_f32_16x16x32_bf16 a[36:39], a[228:231], a[216:219], a[36:39]\n" \
"ds_read_b64_tr_b16 a[188:189], v39 offset:32768\n" \
"s_lshl_b32 s16, s3, 10\n" \
"v_mfma_f32_16x16x32_bf16 a[44:47], a[172:175], a[152:155], a[44:47]\n" \
"s_addk_i32 s16, 0x4800\n" \
"v_writelane_b32 v11, s16, 25\n" \
"v_mfma_f32_16x16x32_bf16 a[44:47], a[236:239], a[216:219], a[44:47]\n" \
"ds_read_b64_tr_b16 a[190:191], v39 offset:33792\n" \
"s_add_i32 s16, s15, 1\n" \
"v_mfma_f32_16x16x32_bf16 a[40:43], a[152:155], a[168:171], a[40:43]\n" \
"s_add_u32 s89, s42, 0x1000\n" \
"buffer_store_dwordx4 v[224:227], v20, s[80:83], s89 offen offset:3072 sc1\n" \
"v_mfma_f32_16x16x32_bf16 a[40:43], a[216:219], a[232:235], a[40:43]\n" \
"ds_read_b64_tr_b16 a[252:253], v39 offset:40960\n" \
"s_and_b32 s16, s16, 3\n" \
"v_mfma_f32_16x16x32_bf16 a[56:59], a[156:159], a[168:171], a[56:59]\n" \
"ds_read_b64_tr_b16 a[254:255], v39 offset:41984\n" \
"s_or_b32 s16, s16, s5\n" \
"v_mfma_f32_16x16x32_bf16 a[56:59], a[220:223], a[232:235], a[56:59]\n" \
"s_mul_i32 s18, s16, 0x24000\n" \
"s_add_i32 s18, s18, s19\n" \
"v_mfma_f32_16x16x32_bf16 a[60:63], a[172:175], a[156:159], a[60:63]\n" \
"s_xor_b32 s13, s13, 2\n" \
"s_add_i32 s15, s15, -1\n" \
"v_mfma_f32_16x16x32_bf16 a[60:63], a[236:239], a[220:223], a[60:63]\n" \
"s_add_u32 s88, s42, 0x1000\n" \
"buffer_store_dwordx4 v[228:231], v20, s[80:83], s88 offen offset:2048 sc1\n" \
"v_mfma_f32_16x16x32_bf16 a[52:55], a[164:167], a[156:159], a[52:55]\n" \
"v_writelane_b32 v11, s18, 27\n" \
"s_mul_i32 s18, s13, 0x24000\n" \
"v_mfma_f32_16x16x32_bf16 a[52:55], a[228:231], a[220:223], a[52:55]\n" \
"s_and_b32 s15, s15, 3\n" \
"s_add_i32 s18, s18, s19\n" \
"v_mfma_f32_16x16x32_bf16 a[48:51], a[160:163], a[156:159], a[48:51]\n" \
"s_or_b32 s15, s15, s5\n" \
"v_writelane_b32 v11, s18, 28\n" \
"v_mfma_f32_16x16x32_bf16 a[48:51], a[224:227], a[220:223], a[48:51]\n" \
"s_mul_i32 s18, s15, 0x24000\n" \
"s_add_i32 s18, s18, s19\n" \
"v_mfma_f32_16x16x32_bf16 a[64:67], a[164:167], a[140:143], a[64:67]\n" \
"v_writelane_b32 v11, s18, 29\n" \
"s_or_b32 s18, s9, s14\n" \
"v_mfma_f32_16x16x32_bf16 a[64:67], a[228:231], a[204:207], a[64:67]\n" \
"s_mul_i32 s18, s18, 0x24800\n" \
"s_lshl_b32 s16, s16, 12\n" \
"v_mfma_f32_16x16x32_bf16 a[80:83], a[172:175], a[140:143], a[80:83]\n" \
"s_add_i32 s19, s18, 0x3004800\n" \
"s_add_i32 s20, s19, s16\n" \
"v_mfma_f32_16x16x32_bf16 a[80:83], a[236:239], a[204:207], a[80:83]\n" \
"v_writelane_b32 v11, s20, 30\n" \
"s_lshl_b32 s13, s13, 12\n" \
"v_mfma_f32_16x16x32_bf16 a[68:71], a[140:143], a[168:171], a[68:71]\n" \
"s_lshl_b32 s15, s15, 12\n" \
"s_sub_i32 s14, s9, s14\n" \
"v_mfma_f32_16x16x32_bf16 a[68:71], a[204:207], a[232:235], a[68:71]\n" \
"s_waitcnt vmcnt(4) lgkmcnt(0)\n" \
"s_barrier\n" \
"v_mov_b32_e32 v45, s62\n" \
"s_mov_b64 exec, 1\n" \
"buffer_store_dword v45, off, s[80:83], s37 offset:0 sc1\n" \
"s_mov_b64 exec, -1\n" \
"v_mfma_f32_16x16x32_bf16 v[116:119], a[176:179], a[144:147], v[116:119]\n" \
"s_add_u32 m0, s40, 0x8000\n" \
"s_add_u32 s85, s39, 0xa000\n" \
"v_mfma_f32_16x16x32_bf16 v[116:119], a[240:243], a[208:211], v[116:119]\n" \
"buffer_load_dwordx4 v20, s[80:83], s85 offen sc1 lds\n" \
"v_mfma_f32_16x16x32_bf16 v[120:123], a[180:183], a[144:147], v[120:123]\n" \
"ds_read_b64_tr_b16 a[160:161], v32 offset:0\n" \
"v_accvgpr_read_b32 v212, a108\n" \
"v_mfma_f32_16x16x32_bf16 v[120:123], a[244:247], a[208:211], v[120:123]\n" \
"ds_read_b64_tr_b16 a[162:163], v32 offset:512\n" \
"v_accvgpr_read_b32 v213, a109\n" \
"v_mfma_f32_16x16x32_bf16 v[124:127], a[184:187], a[144:147], v[124:127]\n" \
"s_add_u32 m0, s40, 0x9000\n" \
"s_add_u32 s85, s39, 0xb000\n" \
"v_mfma_f32_16x16x32_bf16 v[124:127], a[248:251], a[208:211], v[124:127]\n" \
"buffer_load_dwordx4 v20, s[80:83], s85 offen sc1 lds\n" \
"v_mfma_f32_16x16x32_bf16 v[128:131], a[188:191], a[144:147], v[128:131]\n" \
"ds_read_b64_tr_b16 a[224:225], v32 offset:4096\n" \
"v_accvgpr_read_b32 v214, a110\n" \
"v_mfma_f32_16x16x32_bf16 v[128:131], a[252:255], a[208:211], v[128:131]\n" \
"ds_read_b128 a[144:147], v28 offset:8192\n" \
"v_accvgpr_read_b32 v215, a111\n" \
"v_mfma_f32_16x16x32_bf16 v[144:147], a[188:191], a[148:151], v[144:147]\n" \
"ds_read_b128 a[208:211], v29 offset:8192\n" \
"v_accvgpr_read_b32 v180, a124\n" \
"v_mfma_f32_16x16x32_bf16 v[144:147], a[252:255], a[212:215], v[144:147]\n" \
"s_add_u32 m0, s40, 0xb000\n" \
"s_movk_i32 s84, 0x280\n" \
"v_mfma_f32_16x16x32_bf16 v[140:143], a[184:187], a[148:151], v[140:143]\n" \
"buffer_load_dwordx4 v21, s[76:79], s84 offen offset:0 lds\n" \
"v_mfma_f32_16x16x32_bf16 v[140:143], a[248:251], a[212:215], v[140:143]\n" \
"ds_read_b64_tr_b16 a[226:227], v32 offset:4608\n" \
"v_accvgpr_read_b32 v181, a125\n" \
"v_mfma_f32_16x16x32_bf16 v[136:139], a[180:183], a[148:151], v[136:139]\n" \
"s_add_u32 m0, s30, 0x8000\n" \
"s_movk_i32 s84, 0x280\n" \
"v_mfma_f32_16x16x32_bf16 v[136:139], a[244:247], a[212:215], v[136:139]\n" \
"buffer_load_dwordx4 v24, s[76:79], s84 offen offset:0 lds\n" \
"v_mfma_f32_16x16x32_bf16 v[132:135], a[176:179], a[148:151], v[132:135]\n" \
"ds_read_b64_tr_b16 a[164:165], v33 offset:0\n" \
"v_accvgpr_read_b32 v182, a126\n" \
"v_mfma_f32_16x16x32_bf16 v[132:135], a[240:243], a[212:215], v[132:135]\n" \
"ds_read_b128 a[148:151], v28 offset:10240\n" \
"v_accvgpr_read_b32 v183, a127\n" \
"v_mfma_f32_16x16x32_bf16 v[148:151], a[176:179], a[152:155], v[148:151]\n" \
"ds_read_b128 a[212:215], v29 offset:10240\n" \
"v_cvt_pk_bf16_f32 v212, v212, v213\n" \
"v_mfma_f32_16x16x32_bf16 v[148:151], a[240:243], a[216:219], v[148:151]\n" \
"s_movk_i32 s84, 0x2280\n" \
"v_mfma_f32_16x16x32_bf16 v[152:155], a[180:183], a[152:155], v[152:155]\n" \
"buffer_load_dwordx4 v25, s[76:79], s84 offen offset:1024 lds\n" \
"v_mfma_f32_16x16x32_bf16 v[152:155], a[244:247], a[216:219], v[152:155]\n" \
"ds_read_b64_tr_b16 a[166:167], v33 offset:512\n" \
"v_cvt_pk_bf16_f32 v213, v214, v215\n" \
"v_mfma_f32_16x16x32_bf16 v[156:159], a[184:187], a[152:155], v[156:159]\n" \
"ds_read_b64_tr_b16 a[228:229], v33 offset:4096\n" \
"v_cvt_pk_bf16_f32 v214, v180, v181\n" \
"v_mfma_f32_16x16x32_bf16 v[156:159], a[248:251], a[216:219], v[156:159]\n" \
"s_movk_i32 s84, 0x4280\n" \
"v_mfma_f32_16x16x32_bf16 v[160:163], a[188:191], a[152:155], v[160:163]\n" \
"buffer_load_dwordx4 v24, s[76:79], s84 offen offset:2048 lds\n" \
"v_mfma_f32_16x16x32_bf16 v[160:163], a[252:255], a[216:219], v[160:163]\n" \
"ds_read_b128 a[152:155], v28 offset:12288\n" \
"v_cvt_pk_bf16_f32 v215, v182, v183\n" \
"v_mfma_f32_16x16x32_bf16 v[176:179], a[188:191], a[156:159], v[176:179]\n" \
"ds_read_b128 a[216:219], v29 offset:12288\n" \
"v_accvgpr_read_b32 v208, a104\n" \
"v_mfma_f32_16x16x32_bf16 v[176:179], a[252:255], a[220:223], v[176:179]\n" \
"s_movk_i32 s84, 0x6280\n" \
"s_nop 0\n" \
"v_mfma_f32_16x16x32_bf16 v[172:175], a[184:187], a[156:159], v[172:175]\n" \
"buffer_load_dwordx4 v25, s[76:79], s84 offen offset:3072 lds\n" \
"v_mfma_f32_16x16x32_bf16 v[172:175], a[248:251], a[220:223], v[172:175]\n" \
"ds_read_b64_tr_b16 a[230:231], v33 offset:4608\n" \
"v_accvgpr_read_b32 v209, a105\n" \
"v_mfma_f32_16x16x32_bf16 v[168:171], a[180:183], a[156:159], v[168:171]\n" \
"ds_read_b64_tr_b16 a[172:173], v35 offset:0\n" \
"v_accvgpr_read_b32 v210, a106\n" \
"v_mfma_f32_16x16x32_bf16 v[168:171], a[244:247], a[220:223], v[168:171]\n" \
"ds_read_b64_tr_b16 a[174:175], v35 offset:512\n" \
"v_accvgpr_read_b32 v211, a107\n" \
"v_mfma_f32_16x16x32_bf16 v[164:167], a[176:179], a[156:159], v[164:167]\n" \
"v_accvgpr_read_b32 v180, a120\n" \
"v_accvgpr_read_b32 v181, a121\n" \
"v_mfma_f32_16x16x32_bf16 v[164:167], a[240:243], a[220:223], v[164:167]\n" \
"ds_read_b128 a[156:159], v28 offset:14336\n" \
"v_accvgpr_read_b32 v182, a122\n" \
"v_mfma_f32_16x16x32_bf16 v[84:87], a[176:179], a[136:139], v[84:87]\n" \
"ds_read_b128 a[220:223], v29 offset:14336\n" \
"v_accvgpr_read_b32 v183, a123\n" \
"v_mfma_f32_16x16x32_bf16 v[84:87], a[240:243], a[200:203], v[84:87]\n" \
"s_add_u32 m0, s31, 0x8000\n" \
"s_mov_b32 s84, 0x280000\n" \
"v_mfma_f32_16x16x32_bf16 v[88:91], a[180:183], a[136:139], v[88:91]\n" \
"buffer_load_dwordx4 v26, s[68:71], s84 offen offset:0 lds\n" \
"v_mfma_f32_16x16x32_bf16 v[88:91], a[244:247], a[200:203], v[88:91]\n" \
"s_addk_i32 s84, 0x7c00\n" \
"s_nop 0\n" \
"v_mfma_f32_16x16x32_bf16 v[92:95], a[184:187], a[136:139], v[92:95]\n" \
"buffer_load_dwordx4 v26, s[68:71], s84 offen offset:1024 lds\n" \
"v_mfma_f32_16x16x32_bf16 v[92:95], a[248:251], a[200:203], v[92:95]\n" \
"ds_read_b64_tr_b16 a[236:237], v35 offset:4096\n" \
"v_cvt_pk_bf16_f32 v208, v208, v209\n" \
"v_mfma_f32_16x16x32_bf16 v[96:99], a[188:191], a[136:139], v[96:99]\n" \
"ds_read_b64_tr_b16 a[238:239], v35 offset:4608\n" \
"v_cvt_pk_bf16_f32 v209, v210, v211\n" \
"v_mfma_f32_16x16x32_bf16 v[96:99], a[252:255], a[200:203], v[96:99]\n" \
"ds_read_b128 a[136:139], v28 offset:4096\n" \
"v_cvt_pk_bf16_f32 v210, v180, v181\n" \
"v_mfma_f32_16x16x32_bf16 v[112:115], a[188:191], a[140:143], v[112:115]\n" \
"ds_read_b128 a[200:203], v29 offset:4096\n" \
"v_cvt_pk_bf16_f32 v211, v182, v183\n" \
"v_mfma_f32_16x16x32_bf16 v[112:115], a[252:255], a[204:207], v[112:115]\n" \
"s_addk_i32 s84, 0x7c00\n" \
"v_mfma_f32_16x16x32_bf16 v[108:111], a[184:187], a[140:143], v[108:111]\n" \
"buffer_load_dwordx4 v27, s[68:71], s84 offen offset:2048 lds\n" \
"v_mfma_f32_16x16x32_bf16 v[108:111], a[248:251], a[204:207], v[108:111]\n" \
"s_addk_i32 s84, 0x7c00\n" \
"v_mfma_f32_16x16x32_bf16 v[104:107], a[180:183], a[140:143], v[104:107]\n" \
"buffer_load_dwordx4 v27, s[68:71], s84 offen offset:3072 lds\n" \
"v_mfma_f32_16x16x32_bf16 v[104:107], a[244:247], a[204:207], v[104:107]\n" \
"ds_read_b64_tr_b16 a[168:169], v34 offset:0\n" \
"v_accvgpr_read_b32 v204, a100\n" \
"v_mfma_f32_16x16x32_bf16 v[100:103], a[176:179], a[140:143], v[100:103]\n" \
"ds_read_b64_tr_b16 a[170:171], v34 offset:512\n" \
"v_accvgpr_read_b32 v205, a101\n" \
"v_mfma_f32_16x16x32_bf16 v[100:103], a[240:243], a[204:207], v[100:103]\n" \
"ds_read_b128 a[140:143], v28 offset:6144\n" \
"v_accvgpr_read_b32 v206, a102\n" \
"v_mfma_f32_16x16x32_bf16 v[52:55], a[176:179], a[128:131], v[52:55]\n" \
"ds_read_b128 a[204:207], v29 offset:6144\n" \
"v_accvgpr_read_b32 v207, a103\n" \
"v_mfma_f32_16x16x32_bf16 v[52:55], a[240:243], a[192:195], v[52:55]\n" \
"s_add_u32 m0, s30, 0x9000\n" \
"s_mov_b32 s84, 0x9280\n" \
"v_mfma_f32_16x16x32_bf16 v[56:59], a[180:183], a[128:131], v[56:59]\n" \
"buffer_load_dwordx4 v24, s[76:79], s84 offen offset:0 lds\n" \
"v_mfma_f32_16x16x32_bf16 v[56:59], a[244:247], a[192:195], v[56:59]\n" \
"s_addk_i32 s84, 0x2000\n" \
"s_nop 0\n" \
"v_mfma_f32_16x16x32_bf16 v[60:63], a[184:187], a[128:131], v[60:63]\n" \
"buffer_load_dwordx4 v25, s[76:79], s84 offen offset:1024 lds\n" \
"v_mfma_f32_16x16x32_bf16 v[60:63], a[248:251], a[192:195], v[60:63]\n" \
"ds_read_b64_tr_b16 a[232:233], v34 offset:4096\n" \
"v_accvgpr_read_b32 v180, a116\n" \
"v_mfma_f32_16x16x32_bf16 v[64:67], a[188:191], a[128:131], v[64:67]\n" \
"ds_read_b64_tr_b16 a[234:235], v34 offset:4608\n" \
"v_accvgpr_read_b32 v181, a117\n" \
"v_mfma_f32_16x16x32_bf16 v[64:67], a[252:255], a[192:195], v[64:67]\n" \
"ds_read_b128 a[128:131], v28 offset:0\n" \
"v_accvgpr_read_b32 v182, a118\n" \
"v_mfma_f32_16x16x32_bf16 v[80:83], a[188:191], a[132:135], v[80:83]\n" \
"ds_read_b128 a[192:195], v29 offset:0\n" \
"v_accvgpr_read_b32 v183, a119\n" \
"v_mfma_f32_16x16x32_bf16 v[80:83], a[252:255], a[196:199], v[80:83]\n" \
"v_cvt_pk_bf16_f32 v204, v204, v205\n" \
"v_cvt_pk_bf16_f32 v205, v206, v207\n" \
"v_mfma_f32_16x16x32_bf16 v[76:79], a[184:187], a[132:135], v[76:79]\n" \
"v_cvt_pk_bf16_f32 v206, v180, v181\n" \
"v_cvt_pk_bf16_f32 v207, v182, v183\n" \
"v_mfma_f32_16x16x32_bf16 v[76:79], a[248:251], a[196:199], v[76:79]\n" \
"v_accvgpr_read_b32 v200, a96\n" \
"v_accvgpr_read_b32 v201, a97\n" \
"v_mfma_f32_16x16x32_bf16 v[72:75], a[180:183], a[132:135], v[72:75]\n" \
"v_accvgpr_read_b32 v202, a98\n" \
"v_accvgpr_read_b32 v203, a99\n" \
"v_mfma_f32_16x16x32_bf16 v[72:75], a[244:247], a[196:199], v[72:75]\n" \
"v_accvgpr_read_b32 v180, a112\n" \
"v_accvgpr_read_b32 v181, a113\n" \
"v_mfma_f32_16x16x32_bf16 v[68:71], a[176:179], a[132:135], v[68:71]\n" \
"v_accvgpr_read_b32 v182, a114\n" \
"v_accvgpr_read_b32 v183, a115\n" \
"v_mfma_f32_16x16x32_bf16 v[68:71], a[240:243], a[196:199], v[68:71]\n" \
"ds_read_b128 a[132:135], v28 offset:2048\n" \
"ds_read_b128 a[196:199], v29 offset:2048\n" \
"s_waitcnt lgkmcnt(15)\n" \
"v_mfma_f32_16x16x32_bf16 a[0:3], a[160:163], a[144:147], a[0:3]\n" \
"s_addk_i32 s84, 0x2000\n" \
"v_mfma_f32_16x16x32_bf16 a[0:3], a[224:227], a[208:211], a[0:3]\n" \
"buffer_load_dwordx4 v24, s[76:79], s84 offen offset:2048 lds\n" \
"v_mfma_f32_16x16x32_bf16 a[4:7], a[164:167], a[144:147], a[4:7]\n" \
"ds_read_b64_tr_b16 a[176:177], v36 offset:0\n" \
"v_cvt_pk_bf16_f32 v200, v200, v201\n" \
"v_mfma_f32_16x16x32_bf16 a[4:7], a[228:231], a[208:211], a[4:7]\n" \
"s_addk_i32 s84, 0x2000\n" \
"v_mfma_f32_16x16x32_bf16 a[12:15], a[172:175], a[144:147], a[12:15]\n" \
"buffer_load_dwordx4 v25, s[76:79], s84 offen offset:3072 lds\n" \
"s_waitcnt lgkmcnt(13)\n" \
"v_mfma_f32_16x16x32_bf16 a[12:15], a[236:239], a[208:211], a[12:15]\n" \
"ds_read_b64_tr_b16 a[178:179], v36 offset:1024\n" \
"v_cvt_pk_bf16_f32 v201, v202, v203\n" \
"s_waitcnt lgkmcnt(10)\n" \
"v_mfma_f32_16x16x32_bf16 a[8:11], a[144:147], a[168:171], a[8:11]\n" \
"ds_read_b64_tr_b16 a[240:241], v36 offset:8192\n" \
"v_cvt_pk_bf16_f32 v202, v180, v181\n" \
"s_waitcnt lgkmcnt(7)\n" \
"v_mfma_f32_16x16x32_bf16 a[8:11], a[208:211], a[232:235], a[8:11]\n" \
"ds_read_b64_tr_b16 a[242:243], v36 offset:9216\n" \
"v_cvt_pk_bf16_f32 v203, v182, v183\n" \
"v_mfma_f32_16x16x32_bf16 a[24:27], a[148:151], a[168:171], a[24:27]\n" \
"ds_read_b64_tr_b16 a[180:181], v37 offset:0\n" \
"v_accvgpr_read_b32 v196, a76\n" \
"v_mfma_f32_16x16x32_bf16 a[24:27], a[212:215], a[232:235], a[24:27]\n" \
"ds_read_b64_tr_b16 a[182:183], v37 offset:1024\n" \
"v_accvgpr_read_b32 v197, a77\n" \
"v_mfma_f32_16x16x32_bf16 a[28:31], a[172:175], a[148:151], a[28:31]\n" \
"ds_read_b64_tr_b16 a[244:245], v37 offset:8192\n" \
"v_accvgpr_read_b32 v198, a78\n" \
"v_mfma_f32_16x16x32_bf16 a[28:31], a[236:239], a[212:215], a[28:31]\n" \
"ds_read_b64_tr_b16 a[246:247], v37 offset:9216\n" \
"v_accvgpr_read_b32 v199, a79\n" \
"v_mfma_f32_16x16x32_bf16 a[20:23], a[164:167], a[148:151], a[20:23]\n" \
"v_accvgpr_read_b32 v180, a92\n" \
"v_accvgpr_read_b32 v181, a93\n" \
"v_mfma_f32_16x16x32_bf16 a[20:23], a[228:231], a[212:215], a[20:23]\n" \
"s_add_u32 s89, s42, 0x1000\n" \
"buffer_store_dwordx4 v[232:235], v20, s[80:83], s89 offen offset:1024 sc1\n" \
"v_mfma_f32_16x16x32_bf16 a[16:19], a[160:163], a[148:151], a[16:19]\n" \
"ds_read_b64_tr_b16 a[184:185], v38 offset:0\n" \
"v_accvgpr_read_b32 v182, a94\n" \
"v_mfma_f32_16x16x32_bf16 a[16:19], a[224:227], a[212:215], a[16:19]\n" \
"ds_read_b64_tr_b16 a[186:187], v38 offset:1024\n" \
"v_accvgpr_read_b32 v183, a95\n" \
"v_mfma_f32_16x16x32_bf16 a[32:35], a[160:163], a[152:155], a[32:35]\n" \
"ds_read_b64_tr_b16 a[248:249], v38 offset:8192\n" \
"v_cvt_pk_bf16_f32 v196, v196, v197\n" \
"v_mfma_f32_16x16x32_bf16 a[32:35], a[224:227], a[216:219], a[32:35]\n" \
"ds_read_b64_tr_b16 a[250:251], v38 offset:9216\n" \
"v_cvt_pk_bf16_f32 v197, v198, v199\n" \
"v_mfma_f32_16x16x32_bf16 a[36:39], a[164:167], a[152:155], a[36:39]\n" \
"v_cvt_pk_bf16_f32 v198, v180, v181\n" \
"v_cvt_pk_bf16_f32 v199, v182, v183\n" \
"v_mfma_f32_16x16x32_bf16 a[36:39], a[228:231], a[216:219], a[36:39]\n" \
"s_add_u32 s88, s42, 0x1000\n" \
"buffer_store_dwordx4 v[236:239], v20, s[80:83], s88 offen offset:0 sc1\n" \
"v_mfma_f32_16x16x32_bf16 a[44:47], a[172:175], a[152:155], a[44:47]\n" \
"ds_read_b64_tr_b16 a[188:189], v39 offset:0\n" \
"v_accvgpr_read_b32 v192, a72\n" \
"v_mfma_f32_16x16x32_bf16 a[44:47], a[236:239], a[216:219], a[44:47]\n" \
"ds_read_b64_tr_b16 a[190:191], v39 offset:1024\n" \
"v_accvgpr_read_b32 v193, a73\n" \
"v_mfma_f32_16x16x32_bf16 a[40:43], a[152:155], a[168:171], a[40:43]\n" \
"ds_read_b64_tr_b16 a[252:253], v39 offset:8192\n" \
"v_accvgpr_read_b32 v194, a74\n" \
"v_mfma_f32_16x16x32_bf16 a[40:43], a[216:219], a[232:235], a[40:43]\n" \
"v_accvgpr_read_b32 v195, a75\n" \
"v_accvgpr_read_b32 v180, a88\n" \
"v_mfma_f32_16x16x32_bf16 a[56:59], a[156:159], a[168:171], a[56:59]\n" \
"ds_read_b64_tr_b16 a[254:255], v39 offset:9216\n" \
"v_accvgpr_read_b32 v181, a89\n" \
"v_mfma_f32_16x16x32_bf16 a[56:59], a[220:223], a[232:235], a[56:59]\n" \
"v_accvgpr_read_b32 v182, a90\n" \
"v_accvgpr_read_b32 v183, a91\n" \
"v_mfma_f32_16x16x32_bf16 a[60:63], a[172:175], a[156:159], a[60:63]\n" \
"s_add_u32 s89, s42, 0x3000\n" \
"buffer_store_dwordx4 v[240:243], v20, s[80:83], s89 offen offset:3072 sc1\n" \
"v_mfma_f32_16x16x32_bf16 a[60:63], a[236:239], a[220:223], a[60:63]\n" \
"v_cvt_pk_bf16_f32 v192, v192, v193\n" \
"v_cvt_pk_bf16_f32 v193, v194, v195\n" \
"v_mfma_f32_16x16x32_bf16 a[52:55], a[164:167], a[156:159], a[52:55]\n" \
"v_cvt_pk_bf16_f32 v194, v180, v181\n" \
"v_cvt_pk_bf16_f32 v195, v182, v183\n" \
"v_mfma_f32_16x16x32_bf16 a[52:55], a[228:231], a[220:223], a[52:55]\n" \
"s_add_i32 s20, s19, s13\n" \
"v_writelane_b32 v11, s20, 31\n" \
"v_mfma_f32_16x16x32_bf16 a[48:51], a[160:163], a[156:159], a[48:51]\n" \
"s_add_i32 s19, s19, s15\n" \
"s_mul_i32 s14, s14, 0x24800\n" \
"v_mfma_f32_16x16x32_bf16 a[48:51], a[224:227], a[220:223], a[48:51]\n" \
".fill (L_pb + 69344 - .) / 4, 4, 0xBF800000\n" \
"L_pp17:\n" \
"s_cbranch_execz L_pret\n" \
"v_writelane_b32 v11, s19, 32\n" \
"s_add_i32 s19, s14, 0x4120800\n" \
"v_mfma_f32_16x16x32_bf16 a[64:67], a[164:167], a[140:143], a[64:67]\n" \
"s_add_i32 s16, s19, s16\n" \
"v_writelane_b32 v11, s16, 33\n" \
"v_mfma_f32_16x16x32_bf16 a[64:67], a[228:231], a[204:207], a[64:67]\n" \
"s_add_i32 s13, s19, s13\n" \
"v_writelane_b32 v11, s13, 34\n" \
"v_mfma_f32_16x16x32_bf16 a[80:83], a[172:175], a[140:143], a[80:83]\n" \
"s_add_i32 s19, s19, s15\n" \
"s_or_b32 s13, s18, 0x3000000\n" \
"v_mfma_f32_16x16x32_bf16 a[80:83], a[236:239], a[204:207], a[80:83]\n" \
"s_lshl_b32 s15, s6, 11\n" \
"v_writelane_b32 v11, s19, 35\n" \
"v_mfma_f32_16x16x32_bf16 a[68:71], a[140:143], a[168:171], a[68:71]\n" \
"s_add_i32 s16, s13, s15\n" \
"v_writelane_b32 v11, s16, 36\n" \
"v_mfma_f32_16x16x32_bf16 a[68:71], a[204:207], a[232:235], a[68:71]\n" \
"s_waitcnt vmcnt(3) lgkmcnt(0)\n" \
"s_barrier\n" \
"v_mfma_f32_16x16x32_bf16 v[116:119], a[176:179], a[144:147], v[116:119]\n" \
"s_add_u32 m0, s40, 0x0\n" \
"s_add_u32 s85, s39, 0xc000\n" \
"v_mfma_f32_16x16x32_bf16 v[116:119], a[240:243], a[208:211], v[116:119]\n" \
"buffer_load_dwordx4 v20, s[80:83], s85 offen sc1 lds\n" \
"v_mfma_f32_16x16x32_bf16 v[120:123], a[180:183], a[144:147], v[120:123]\n" \
"ds_read_b64_tr_b16 a[160:161], v32 offset:32768\n" \
"s_add_i32 s16, s14, 0x411c000\n" \
"v_mfma_f32_16x16x32_bf16 v[120:123], a[244:247], a[208:211], v[120:123]\n" \
"ds_read_b64_tr_b16 a[162:163], v32 offset:33280\n" \
"s_add_i32 s19, s16, s15\n" \
"v_mfma_f32_16x16x32_bf16 v[124:127], a[184:187], a[144:147], v[124:127]\n" \
"s_add_u32 m0, s40, 0x1000\n" \
"s_add_u32 s85, s39, 0xd000\n" \
"v_mfma_f32_16x16x32_bf16 v[124:127], a[248:251], a[208:211], v[124:127]\n" \
"buffer_load_dwordx4 v20, s[80:83], s85 offen sc1 lds\n" \
"v_mfma_f32_16x16x32_bf16 v[128:131], a[188:191], a[144:147], v[128:131]\n" \
"ds_read_b64_tr_b16 a[224:225], v32 offset:36864\n" \
"v_writelane_b32 v11, s19, 37\n" \
"v_mfma_f32_16x16x32_bf16 v[128:131], a[252:255], a[208:211], v[128:131]\n" \
"ds_read_b128 a[144:147], v28 offset:40960\n" \
"s_add_i32 s18, s18, 0x3004000\n" \
"v_mfma_f32_16x16x32_bf16 v[144:147], a[188:191], a[148:151], v[144:147]\n" \
"ds_read_b128 a[208:211], v29 offset:40960\n" \
"v_writelane_b32 v11, s18, 38\n" \
"v_mfma_f32_16x16x32_bf16 v[144:147], a[252:255], a[212:215], v[144:147]\n" \
"s_add_u32 m0, s40, 0x3000\n" \
"s_movk_i32 s84, 0x300\n" \
"v_mfma_f32_16x16x32_bf16 v[140:143], a[184:187], a[148:151], v[140:143]\n" \
"buffer_load_dwordx4 v21, s[76:79], s84 offen offset:0 lds\n" \
"v_mfma_f32_16x16x32_bf16 v[140:143], a[248:251], a[212:215], v[140:143]\n" \
"ds_read_b64_tr_b16 a[226:227], v32 offset:37376\n" \
"s_add_i32 s14, s14, 0x4120000\n" \
"v_mfma_f32_16x16x32_bf16 v[136:139], a[180:183], a[148:151], v[136:139]\n" \
"s_add_u32 m0, s30, 0x0\n" \
"s_movk_i32 s84, 0x300\n" \
"v_mfma_f32_16x16x32_bf16 v[136:139], a[244:247], a[212:215], v[136:139]\n" \
"buffer_load_dwordx4 v24, s[76:79], s84 offen offset:0 lds\n" \
"v_mfma_f32_16x16x32_bf16 v[132:135], a[176:179], a[148:151], v[132:135]\n" \
"ds_read_b64_tr_b16 a[164:165], v33 offset:32768\n" \
"v_writelane_b32 v11, s14, 39\n" \
"v_mfma_f32_16x16x32_bf16 v[132:135], a[240:243], a[212:215], v[132:135]\n" \
"ds_read_b128 a[148:151], v28 offset:43008\n" \
"s_lshl_b32 s14, s4, 2\n" \
"v_mfma_f32_16x16x32_bf16 v[148:151], a[176:179], a[152:155], v[148:151]\n" \
"ds_read_b128 a[212:215], v29 offset:43008\n" \
"s_or_b32 s8, s14, s8\n" \
"v_mfma_f32_16x16x32_bf16 v[148:151], a[240:243], a[216:219], v[148:151]\n" \
"s_movk_i32 s84, 0x2300\n" \
"v_mfma_f32_16x16x32_bf16 v[152:155], a[180:183], a[152:155], v[152:155]\n" \
"buffer_load_dwordx4 v25, s[76:79], s84 offen offset:1024 lds\n" \
"v_mfma_f32_16x16x32_bf16 v[152:155], a[244:247], a[216:219], v[152:155]\n" \
"ds_read_b64_tr_b16 a[166:167], v33 offset:33280\n" \
"s_mul_i32 s8, s8, 0x90000\n" \
"v_mfma_f32_16x16x32_bf16 v[156:159], a[184:187], a[152:155], v[156:159]\n" \
"ds_read_b64_tr_b16 a[228:229], v33 offset:36864\n" \
"s_lshl_b32 s14, s3, 14\n" \
"v_mfma_f32_16x16x32_bf16 v[156:159], a[248:251], a[216:219], v[156:159]\n" \
"s_movk_i32 s84, 0x4300\n" \
"v_mfma_f32_16x16x32_bf16 v[160:163], a[188:191], a[152:155], v[160:163]\n" \
"buffer_load_dwordx4 v24, s[76:79], s84 offen offset:2048 lds\n" \
"v_mfma_f32_16x16x32_bf16 v[160:163], a[252:255], a[216:219], v[160:163]\n" \
"ds_read_b128 a[152:155], v28 offset:45056\n" \
"s_add_i32 s8, s8, s14\n" \
"v_mfma_f32_16x16x32_bf16 v[176:179], a[188:191], a[156:159], v[176:179]\n" \
"ds_read_b128 a[216:219], v29 offset:45056\n" \
"s_lshl_b32 s9, s9, 4\n" \
"v_mfma_f32_16x16x32_bf16 v[176:179], a[252:255], a[220:223], v[176:179]\n" \
"s_movk_i32 s84, 0x6300\n" \
"v_mfma_f32_16x16x32_bf16 v[172:175], a[184:187], a[156:159], v[172:175]\n" \
"buffer_load_dwordx4 v25, s[76:79], s84 offen offset:3072 lds\n" \
"v_mfma_f32_16x16x32_bf16 v[172:175], a[248:251], a[220:223], v[172:175]\n" \
"ds_read_b64_tr_b16 a[230:231], v33 offset:37376\n" \
"s_lshl_b32 s14, s4, 7\n" \
"v_mfma_f32_16x16x32_bf16 v[168:171], a[180:183], a[156:159], v[168:171]\n" \
"ds_read_b64_tr_b16 a[172:173], v35 offset:32768\n" \
"s_or_b32 s18, s9, s14\n" \
"v_mfma_f32_16x16x32_bf16 v[168:171], a[244:247], a[220:223], v[168:171]\n" \
"ds_read_b64_tr_b16 a[174:175], v35 offset:33280\n" \
"s_sub_i32 s9, s9, s14\n" \
"v_mfma_f32_16x16x32_bf16 v[164:167], a[176:179], a[156:159], v[164:167]\n" \
"s_mulk_i32 s18, 0x480\n" \
"s_mulk_i32 s9, 0x480\n" \
"v_mfma_f32_16x16x32_bf16 v[164:167], a[240:243], a[220:223], v[164:167]\n" \
"ds_read_b128 a[156:159], v28 offset:47104\n" \
"s_or_b32 s14, s18, s12\n" \
"v_mfma_f32_16x16x32_bf16 v[84:87], a[176:179], a[136:139], v[84:87]\n" \
"ds_read_b128 a[220:223], v29 offset:47104\n" \
"s_or_b32 s9, s9, s12\n" \
"v_mfma_f32_16x16x32_bf16 v[84:87], a[240:243], a[200:203], v[84:87]\n" \
"s_add_u32 m0, s31, 0x0\n" \
"s_mov_b32 s84, 0x300000\n" \
"v_mfma_f32_16x16x32_bf16 v[88:91], a[180:183], a[136:139], v[88:91]\n" \
"buffer_load_dwordx4 v26, s[68:71], s84 offen offset:0 lds\n" \
"v_mfma_f32_16x16x32_bf16 v[88:91], a[244:247], a[200:203], v[88:91]\n" \
"s_addk_i32 s84, 0x7c00\n" \
"v_mfma_f32_16x16x32_bf16 v[92:95], a[184:187], a[136:139], v[92:95]\n" \
"buffer_load_dwordx4 v26, s[68:71], s84 offen offset:1024 lds\n" \
"v_mfma_f32_16x16x32_bf16 v[92:95], a[248:251], a[200:203], v[92:95]\n" \
"ds_read_b64_tr_b16 a[236:237], v35 offset:36864\n" \
"s_and_b32 s12, s2, 1\n" \
"v_mfma_f32_16x16x32_bf16 v[96:99], a[188:191], a[136:139], v[96:99]\n" \
"ds_read_b64_tr_b16 a[238:239], v35 offset:37376\n" \
"v_mfma_f32_16x16x32_bf16 v[96:99], a[252:255], a[200:203], v[96:99]\n" \
"ds_read_b128 a[136:139], v28 offset:36864\n" \
"v_mfma_f32_16x16x32_bf16 v[112:115], a[188:191], a[140:143], v[112:115]\n" \
"ds_read_b128 a[200:203], v29 offset:36864\n" \
"v_mfma_f32_16x16x32_bf16 v[112:115], a[252:255], a[204:207], v[112:115]\n" \
"s_addk_i32 s84, 0x7c00\n" \
"v_mfma_f32_16x16x32_bf16 v[108:111], a[184:187], a[140:143], v[108:111]\n" \
"buffer_load_dwordx4 v27, s[68:71], s84 offen offset:2048 lds\n" \
"v_mfma_f32_16x16x32_bf16 v[108:111], a[248:251], a[204:207], v[108:111]\n" \
"s_addk_i32 s84, 0x7c00\n" \
"s_nop 0\n" \
"v_mfma_f32_16x16x32_bf16 v[104:107], a[180:183], a[140:143], v[104:107]\n" \
"buffer_load_dwordx4 v27, s[68:71], s84 offen offset:3072 lds\n" \
"v_mfma_f32_16x16x32_bf16 v[104:107], a[244:247], a[204:207], v[104:107]\n" \
"ds_read_b64_tr_b16 a[168:169], v34 offset:32768\n" \
"v_mfma_f32_16x16x32_bf16 v[100:103], a[176:179], a[140:143], v[100:103]\n" \
"ds_read_b64_tr_b16 a[170:171], v34 offset:33280\n" \
"v_mfma_f32_16x16x32_bf16 v[100:103], a[240:243], a[204:207], v[100:103]\n" \
"ds_read_b128 a[140:143], v28 offset:38912\n" \
"v_mfma_f32_16x16x32_bf16 v[52:55], a[176:179], a[128:131], v[52:55]\n" \
"ds_read_b128 a[204:207], v29 offset:38912\n" \
"v_mfma_f32_16x16x32_bf16 v[52:55], a[240:243], a[192:195], v[52:55]\n" \
"s_add_u32 m0, s30, 0x1000\n" \
"s_mov_b32 s84, 0x9300\n" \
"v_mfma_f32_16x16x32_bf16 v[56:59], a[180:183], a[128:131], v[56:59]\n" \
"buffer_load_dwordx4 v24, s[76:79], s84 offen offset:0 lds\n" \
"v_mfma_f32_16x16x32_bf16 v[56:59], a[244:247], a[192:195], v[56:59]\n" \
"s_addk_i32 s84, 0x2000\n" \
"s_nop 0\n" \
"v_mfma_f32_16x16x32_bf16 v[60:63], a[184:187], a[128:131], v[60:63]\n" \
"buffer_load_dwordx4 v25, s[76:79], s84 offen offset:1024 lds\n" \
"v_mfma_f32_16x16x32_bf16 v[60:63], a[248:251], a[192:195], v[60:63]\n" \
"ds_read_b64_tr_b16 a[232:233], v34 offset:36864\n" \
"v_mfma_f32_16x16x32_bf16 v[64:67], a[188:191], a[128:131], v[64:67]\n" \
"ds_read_b64_tr_b16 a[234:235], v34 offset:37376\n" \
"v_mfma_f32_16x16x32_bf16 v[64:67], a[252:255], a[192:195], v[64:67]\n" \
"ds_read_b128 a[128:131], v28 offset:32768\n" \
"v_mfma_f32_16x16x32_bf16 v[80:83], a[188:191], a[132:135], v[80:83]\n" \
"ds_read_b128 a[192:195], v29 offset:32768\n" \
"v_mfma_f32_16x16x32_bf16 v[80:83], a[252:255], a[196:199], v[80:83]\n" \
"s_cmp_lg_u32 s12, 0\n" \
"s_cselect_b32 s13, s16, s13\n" \
"v_mfma_f32_16x16x32_bf16 v[76:79], a[184:187], a[132:135], v[76:79]\n" \
"s_bfe_u32 s16, s2, 0x20001\n" \
"s_addk_i32 s13, 0x4000\n" \
"v_mfma_f32_16x16x32_bf16 v[76:79], a[248:251], a[196:199], v[76:79]\n" \
"s_xor_b32 s16, s16, s3\n" \
"v_mfma_f32_16x16x32_bf16 v[72:75], a[180:183], a[132:135], v[72:75]\n" \
"s_cmp_lg_u32 s16, 0\n" \
"s_cselect_b32 s13, s7, s13\n" \
"v_mfma_f32_16x16x32_bf16 v[72:75], a[244:247], a[196:199], v[72:75]\n" \
"s_lshl_b32 s6, s6, 16\n" \
"v_writelane_b32 v11, s13, 40\n" \
"v_mfma_f32_16x16x32_bf16 v[68:71], a[176:179], a[132:135], v[68:71]\n" \
"s_add_i32 s13, s8, 0x4240000\n" \
"s_add_i32 s6, s13, s6\n" \
"v_mfma_f32_16x16x32_bf16 v[68:71], a[240:243], a[196:199], v[68:71]\n" \
"ds_read_b128 a[132:135], v28 offset:34816\n" \
"ds_read_b128 a[196:199], v29 offset:34816\n" \
"s_waitcnt lgkmcnt(15)\n" \
"v_mfma_f32_16x16x32_bf16 a[0:3], a[160:163], a[144:147], a[0:3]\n" \
"s_addk_i32 s84, 0x2000\n" \
"v_mfma_f32_16x16x32_bf16 a[0:3], a[224:227], a[208:211], a[0:3]\n" \
"buffer_load_dwordx4 v24, s[76:79], s84 offen offset:2048 lds\n" \
"v_mfma_f32_16x16x32_bf16 a[4:7], a[164:167], a[144:147], a[4:7]\n" \
"ds_read_b64_tr_b16 a[176:177], v36 offset:32768\n" \
"v_writelane_b32 v11, s6, 41\n" \
"v_mfma_f32_16x16x32_bf16 a[4:7], a[228:231], a[208:211], a[4:7]\n" \
"s_addk_i32 s84, 0x2000\n" \
"v_mfma_f32_16x16x32_bf16 a[12:15], a[172:175], a[144:147], a[12:15]\n" \
"buffer_load_dwordx4 v25, s[76:79], s84 offen offset:3072 lds\n" \
"s_waitcnt lgkmcnt(13)\n" \
"v_mfma_f32_16x16x32_bf16 a[12:15], a[236:239], a[208:211], a[12:15]\n" \
"ds_read_b64_tr_b16 a[178:179], v36 offset:33792\n" \
"s_add_i32 s8, s8, 0x42c0000\n" \
"s_waitcnt lgkmcnt(10)\n" \
"v_mfma_f32_16x16x32_bf16 a[8:11], a[144:147], a[168:171], a[8:11]\n" \
"ds_read_b64_tr_b16 a[240:241], v36 offset:40960\n" \
"s_lshl_b32 s6, s12, 10\n" \
"s_waitcnt lgkmcnt(7)\n" \
"v_mfma_f32_16x16x32_bf16 a[8:11], a[208:211], a[232:235], a[8:11]\n" \
"ds_read_b64_tr_b16 a[242:243], v36 offset:41984\n" \
"s_or_b32 s6, s8, s6\n" \
"v_mfma_f32_16x16x32_bf16 a[24:27], a[148:151], a[168:171], a[24:27]\n" \
"ds_read_b64_tr_b16 a[180:181], v37 offset:32768\n" \
"v_mfma_f32_16x16x32_bf16 a[24:27], a[212:215], a[232:235], a[24:27]\n" \
"ds_read_b64_tr_b16 a[182:183], v37 offset:33792\n" \
"v_mfma_f32_16x16x32_bf16 a[28:31], a[172:175], a[148:151], a[28:31]\n" \
"ds_read_b64_tr_b16 a[244:245], v37 offset:40960\n" \
"v_mfma_f32_16x16x32_bf16 a[28:31], a[236:239], a[212:215], a[28:31]\n" \
"ds_read_b64_tr_b16 a[246:247], v37 offset:41984\n" \
"v_mfma_f32_16x16x32_bf16 a[20:23], a[164:167], a[148:151], a[20:23]\n" \
"s_cmp_lg_u32 s16, 0\n" \
"s_cselect_b32 s6, s7, s6\n" \
"v_mfma_f32_16x16x32_bf16 a[20:23], a[228:231], a[212:215], a[20:23]\n" \
"s_add_u32 s88, s42, 0x3000\n" \
"buffer_store_dwordx4 v[244:247], v20, s[80:83], s88 offen offset:2048 sc1\n" \
"v_mfma_f32_16x16x32_bf16 a[16:19], a[160:163], a[148:151], a[16:19]\n" \
"ds_read_b64_tr_b16 a[184:185], v38 offset:32768\n" \
"s_mulk_i32 s4, 0x4800\n" \
"v_mfma_f32_16x16x32_bf16 a[16:19], a[224:227], a[212:215], a[16:19]\n" \
"ds_read_b64_tr_b16 a[186:187], v38 offset:33792\n" \
"v_writelane_b32 v11, s6, 42\n" \
"v_mfma_f32_16x16x32_bf16 a[32:35], a[160:163], a[152:155], a[32:35]\n" \
"ds_read_b64_tr_b16 a[248:249], v38 offset:40960\n" \
"s_add_i32 s6, s14, s10\n" \
"v_mfma_f32_16x16x32_bf16 a[32:35], a[224:227], a[216:219], a[32:35]\n" \
"ds_read_b64_tr_b16 a[250:251], v38 offset:41984\n" \
"v_writelane_b32 v11, s6, 43\n" \
"v_mfma_f32_16x16x32_bf16 a[36:39], a[164:167], a[152:155], a[36:39]\n" \
"s_add_i32 s6, s9, 0x21c000\n" \
"s_add_i32 s9, s6, s10\n" \
"v_mfma_f32_16x16x32_bf16 a[36:39], a[228:231], a[216:219], a[36:39]\n" \
"s_add_u32 s89, s42, 0x3000\n" \
"buffer_store_dwordx4 v[184:187], v20, s[80:83], s89 offen offset:1024 sc1\n" \
"v_mfma_f32_16x16x32_bf16 a[44:47], a[172:175], a[152:155], a[44:47]\n" \
"ds_read_b64_tr_b16 a[188:189], v39 offset:32768\n" \
"v_mfma_f32_16x16x32_bf16 a[44:47], a[236:239], a[216:219], a[44:47]\n" \
"ds_read_b64_tr_b16 a[190:191], v39 offset:33792\n" \
"v_mfma_f32_16x16x32_bf16 a[40:43], a[152:155], a[168:171], a[40:43]\n" \
"ds_read_b64_tr_b16 a[252:253], v39 offset:40960\n" \
"v_mfma_f32_16x16x32_bf16 a[40:43], a[216:219], a[232:235], a[40:43]\n" \
"s_cmp_lg_u32 s12, 0\n" \
"s_cselect_b32 s6, s6, s14\n" \
"v_mfma_f32_16x16x32_bf16 a[56:59], a[156:159], a[168:171], a[56:59]\n" \
"ds_read_b64_tr_b16 a[254:255], v39 offset:41984\n" \
"s_addk_i32 s6, 0x100\n" \
"v_mfma_f32_16x16x32_bf16 a[56:59], a[220:223], a[232:235], a[56:59]\n" \
"v_writelane_b32 v11, s9, 44\n" \
"v_mfma_f32_16x16x32_bf16 a[60:63], a[172:175], a[156:159], a[60:63]\n" \
"s_add_u32 s88, s42, 0x3000\n" \
"buffer_store_dwordx4 v[188:191], v20, s[80:83], s88 offen offset:0 sc1\n" \
"v_mfma_f32_16x16x32_bf16 a[60:63], a[236:239], a[220:223], a[60:63]\n" \
"s_cmp_lg_u32 s16, 0\n" \
"s_cselect_b32 s6, s7, s6\n" \
"v_mfma_f32_16x16x32_bf16 a[52:55], a[164:167], a[156:159], a[52:55]\n" \
"v_writelane_b32 v11, s6, 45\n" \
"s_or_b32 s6, s8, s15\n" \
"v_mfma_f32_16x16x32_bf16 a[52:55], a[228:231], a[220:223], a[52:55]\n" \
"v_writelane_b32 v11, s6, 53\n" \
"s_lshl_b32 s6, s3, 17\n" \
"v_mfma_f32_16x16x32_bf16 a[48:51], a[160:163], a[156:159], a[48:51]\n" \
"s_add_i32 s6, s13, s6\n" \
"s_or_b32 s5, s6, s5\n" \
"v_mfma_f32_16x16x32_bf16 a[48:51], a[224:227], a[220:223], a[48:51]\n" \
"s_or_b32 s5, s5, 0x3840\n" \
"v_writelane_b32 v11, s5, 6\n" \
"v_mfma_f32_16x16x32_bf16 a[64:67], a[164:167], a[140:143], a[64:67]\n" \
"s_mov_b32 s5, 0xff0000\n" \
"v_mfma_f32_16x16x32_bf16 a[64:67], a[228:231], a[204:207], a[64:67]\n" \
"s_cmp_lg_u32 s16, 0\n" \
"s_cselect_b32 s5, s17, s5\n" \
"v_mfma_f32_16x16x32_bf16 a[80:83], a[172:175], a[140:143], a[80:83]\n" \
"v_writelane_b32 v11, s5, 7\n" \
"s_mul_i32 s5, s3, 0x1200\n" \
"v_mfma_f32_16x16x32_bf16 a[80:83], a[236:239], a[204:207], a[80:83]\n" \
"s_add_i32 s4, s5, s4\n" \
"s_add_i32 s4, s4, s11\n" \
"v_mfma_f32_16x16x32_bf16 a[68:71], a[140:143], a[168:171], a[68:71]\n" \
"v_writelane_b32 v11, s4, 8\n" \
"s_lshl_b32 s4, s2, 16\n" \
"v_mfma_f32_16x16x32_bf16 a[68:71], a[204:207], a[232:235], a[68:71]\n" \
"s_waitcnt vmcnt(3) lgkmcnt(0)\n" \
"s_barrier\n" \
"s_nop 0\n" \
"v_mfma_f32_16x16x32_bf16 v[116:119], a[176:179], a[144:147], v[116:119]\n" \
"s_add_u32 m0, s40, 0x8000\n" \
"s_add_u32 s85, s39, 0xe000\n" \
"v_mfma_f32_16x16x32_bf16 v[116:119], a[240:243], a[208:211], v[116:119]\n" \
"buffer_load_dwordx4 v20, s[80:83], s85 offen sc1 lds\n" \
"v_mfma_f32_16x16x32_bf16 v[120:123], a[180:183], a[144:147], v[120:123]\n" \
"ds_read_b64_tr_b16 a[160:161], v32 offset:0\n" \
"s_add_i32 s5, s4, 0x10000\n" \
"v_mfma_f32_16x16x32_bf16 v[120:123], a[244:247], a[208:211], v[120:123]\n" \
"ds_read_b64_tr_b16 a[162:163], v32 offset:512\n" \
"s_and_b32 s5, s5, 0x70000\n" \
"v_mfma_f32_16x16x32_bf16 v[124:127], a[184:187], a[144:147], v[124:127]\n" \
"s_add_u32 m0, s40, 0x9000\n" \
"s_add_u32 s85, s39, 0xf000\n" \
"v_mfma_f32_16x16x32_bf16 v[124:127], a[248:251], a[208:211], v[124:127]\n" \
"buffer_load_dwordx4 v20, s[80:83], s85 offen sc1 lds\n" \
"v_mfma_f32_16x16x32_bf16 v[128:131], a[188:191], a[144:147], v[128:131]\n" \
"ds_read_b64_tr_b16 a[224:225], v32 offset:4096\n" \
"s_add_i32 s5, s13, s5\n" \
"v_mfma_f32_16x16x32_bf16 v[128:131], a[252:255], a[208:211], v[128:131]\n" \
"ds_read_b128 a[144:147], v28 offset:8192\n" \
"v_writelane_b32 v11, s5, 46\n" \
"v_mfma_f32_16x16x32_bf16 v[144:147], a[188:191], a[148:151], v[144:147]\n" \
"ds_read_b128 a[208:211], v29 offset:8192\n" \
"s_add_i32 s5, s4, 0x20000\n" \
"v_mfma_f32_16x16x32_bf16 v[144:147], a[252:255], a[212:215], v[144:147]\n" \
"s_add_u32 m0, s40, 0xb000\n" \
"s_movk_i32 s84, 0x380\n" \
"v_mfma_f32_16x16x32_bf16 v[140:143], a[184:187], a[148:151], v[140:143]\n" \
"buffer_load_dwordx4 v21, s[76:79], s84 offen offset:0 lds\n" \
"v_mfma_f32_16x16x32_bf16 v[140:143], a[248:251], a[212:215], v[140:143]\n" \
"ds_read_b64_tr_b16 a[226:227], v32 offset:4608\n" \
"s_and_b32 s5, s5, 0x70000\n" \
"v_mfma_f32_16x16x32_bf16 v[136:139], a[180:183], a[148:151], v[136:139]\n" \
"s_add_u32 m0, s30, 0x8000\n" \
"s_movk_i32 s84, 0x380\n" \
"v_mfma_f32_16x16x32_bf16 v[136:139], a[244:247], a[212:215], v[136:139]\n" \
"buffer_load_dwordx4 v24, s[76:79], s84 offen offset:0 lds\n" \
"v_mfma_f32_16x16x32_bf16 v[132:135], a[176:179], a[148:151], v[132:135]\n" \
"ds_read_b64_tr_b16 a[164:165], v33 offset:0\n" \
"s_add_i32 s5, s13, s5\n" \
"v_mfma_f32_16x16x32_bf16 v[132:135], a[240:243], a[212:215], v[132:135]\n" \
"ds_read_b128 a[148:151], v28 offset:10240\n" \
"s_bitset1_b32 s5, 11\n" \
"v_mfma_f32_16x16x32_bf16 v[148:151], a[176:179], a[152:155], v[148:151]\n" \
"ds_read_b128 a[212:215], v29 offset:10240\n" \
"v_writelane_b32 v11, s5, 47\n" \
"v_mfma_f32_16x16x32_bf16 v[148:151], a[240:243], a[216:219], v[148:151]\n" \
"s_movk_i32 s84, 0x2380\n" \
"v_mfma_f32_16x16x32_bf16 v[152:155], a[180:183], a[152:155], v[152:155]\n" \
"buffer_load_dwordx4 v25, s[76:79], s84 offen offset:1024 lds\n" \
"v_mfma_f32_16x16x32_bf16 v[152:155], a[244:247], a[216:219], v[152:155]\n" \
"ds_read_b64_tr_b16 a[166:167], v33 offset:512\n" \
"s_add_i32 s5, s4, 0x30000\n" \
"v_mfma_f32_16x16x32_bf16 v[156:159], a[184:187], a[152:155], v[156:159]\n" \
"ds_read_b64_tr_b16 a[228:229], v33 offset:4096\n" \
"s_and_b32 s5, s5, 0x70000\n" \
"v_mfma_f32_16x16x32_bf16 v[156:159], a[248:251], a[216:219], v[156:159]\n" \
"s_movk_i32 s84, 0x4380\n" \
"v_mfma_f32_16x16x32_bf16 v[160:163], a[188:191], a[152:155], v[160:163]\n" \
"buffer_load_dwordx4 v24, s[76:79], s84 offen offset:2048 lds\n" \
"v_mfma_f32_16x16x32_bf16 v[160:163], a[252:255], a[216:219], v[160:163]\n" \
"ds_read_b128 a[152:155], v28 offset:12288\n" \
"s_add_i32 s5, s13, s5\n" \
"v_mfma_f32_16x16x32_bf16 v[176:179], a[188:191], a[156:159], v[176:179]\n" \
"ds_read_b128 a[216:219], v29 offset:12288\n" \
"s_bitset1_b32 s5, 12\n" \
"v_mfma_f32_16x16x32_bf16 v[176:179], a[252:255], a[220:223], v[176:179]\n" \
"s_movk_i32 s84, 0x6380\n" \
"v_mfma_f32_16x16x32_bf16 v[172:175], a[184:187], a[156:159], v[172:175]\n" \
"buffer_load_dwordx4 v25, s[76:79], s84 offen offset:3072 lds\n" \
"v_mfma_f32_16x16x32_bf16 v[172:175], a[248:251], a[220:223], v[172:175]\n" \
"ds_read_b64_tr_b16 a[230:231], v33 offset:4608\n" \
"v_writelane_b32 v11, s5, 48\n" \
"v_mfma_f32_16x16x32_bf16 v[168:171], a[180:183], a[156:159], v[168:171]\n" \
"ds_read_b64_tr_b16 a[172:173], v35 offset:0\n" \
"s_and_b32 s5, s4, 0x70000\n" \
"v_mfma_f32_16x16x32_bf16 v[168:171], a[244:247], a[220:223], v[168:171]\n" \
"ds_read_b64_tr_b16 a[174:175], v35 offset:512\n" \
"s_xor_b32 s5, s5, 0x40000\n" \
"v_mfma_f32_16x16x32_bf16 v[164:167], a[176:179], a[156:159], v[164:167]\n" \
"s_add_i32 s5, s13, s5\n" \
"s_nop 0\n" \
"s_or_b32 s5, s5, 0x1800\n" \
"v_mfma_f32_16x16x32_bf16 v[164:167], a[240:243], a[220:223], v[164:167]\n" \
"ds_read_b128 a[156:159], v28 offset:14336\n" \
"v_writelane_b32 v11, s5, 49\n" \
"v_mfma_f32_16x16x32_bf16 v[84:87], a[176:179], a[136:139], v[84:87]\n" \
"ds_read_b128 a[220:223], v29 offset:14336\n" \
"s_add_i32 s5, s4, 0x50000\n" \
"v_mfma_f32_16x16x32_bf16 v[84:87], a[240:243], a[200:203], v[84:87]\n" \
"s_add_u32 m0, s31, 0x8000\n" \
"s_mov_b32 s84, 0x380000\n" \
"v_mfma_f32_16x16x32_bf16 v[88:91], a[180:183], a[136:139], v[88:91]\n" \
"buffer_load_dwordx4 v26, s[68:71], s84 offen offset:0 lds\n" \
"v_mfma_f32_16x16x32_bf16 v[88:91], a[244:247], a[200:203], v[88:91]\n" \
"s_addk_i32 s84, 0x7c00\n" \
"v_mfma_f32_16x16x32_bf16 v[92:95], a[184:187], a[136:139], v[92:95]\n" \
"buffer_load_dwordx4 v26, s[68:71], s84 offen offset:1024 lds\n" \
"v_mfma_f32_16x16x32_bf16 v[92:95], a[248:251], a[200:203], v[92:95]\n" \
"ds_read_b64_tr_b16 a[236:237], v35 offset:4096\n" \
"s_and_b32 s5, s5, 0x70000\n" \
"v_mfma_f32_16x16x32_bf16 v[96:99], a[188:191], a[136:139], v[96:99]\n" \
"ds_read_b64_tr_b16 a[238:239], v35 offset:4608\n" \
"s_add_i32 s5, s13, s5\n" \
"v_mfma_f32_16x16x32_bf16 v[96:99], a[252:255], a[200:203], v[96:99]\n" \
"ds_read_b128 a[136:139], v28 offset:4096\n" \
"s_bitset1_b32 s5, 13\n" \
"v_mfma_f32_16x16x32_bf16 v[112:115], a[188:191], a[140:143], v[112:115]\n" \
"ds_read_b128 a[200:203], v29 offset:4096\n" \
"v_writelane_b32 v11, s5, 50\n" \
"v_mfma_f32_16x16x32_bf16 v[112:115], a[252:255], a[204:207], v[112:115]\n" \
"s_addk_i32 s84, 0x7c00\n" \
"v_mfma_f32_16x16x32_bf16 v[108:111], a[184:187], a[140:143], v[108:111]\n" \
"buffer_load_dwordx4 v27, s[68:71], s84 offen offset:2048 lds\n" \
"v_mfma_f32_16x16x32_bf16 v[108:111], a[248:251], a[204:207], v[108:111]\n" \
"s_addk_i32 s84, 0x7c00\n" \
"s_nop 0\n" \
"v_mfma_f32_16x16x32_bf16 v[104:107], a[180:183], a[140:143], v[104:107]\n" \
"buffer_load_dwordx4 v27, s[68:71], s84 offen offset:3072 lds\n" \
"v_mfma_f32_16x16x32_bf16 v[104:107], a[244:247], a[204:207], v[104:107]\n" \
"ds_read_b64_tr_b16 a[168:169], v34 offset:0\n" \
"s_add_i32 s5, s4, 0x60000\n" \
"v_mfma_f32_16x16x32_bf16 v[100:103], a[176:179], a[140:143], v[100:103]\n" \
"ds_read_b64_tr_b16 a[170:171], v34 offset:512\n" \
"s_and_b32 s5, s5, 0x70000\n" \
"v_mfma_f32_16x16x32_bf16 v[100:103], a[240:243], a[204:207], v[100:103]\n" \
"ds_read_b128 a[140:143], v28 offset:6144\n" \
"s_add_i32 s4, s4, 0x70000\n" \
"v_mfma_f32_16x16x32_bf16 v[52:55], a[176:179], a[128:131], v[52:55]\n" \
"ds_read_b128 a[204:207], v29 offset:6144\n" \
"s_add_i32 s5, s13, s5\n" \
"v_mfma_f32_16x16x32_bf16 v[52:55], a[240:243], a[192:195], v[52:55]\n" \
"s_add_u32 m0, s30, 0x9000\n" \
"s_mov_b32 s84, 0x9380\n" \
"v_mfma_f32_16x16x32_bf16 v[56:59], a[180:183], a[128:131], v[56:59]\n" \
"buffer_load_dwordx4 v24, s[76:79], s84 offen offset:0 lds\n" \
"v_mfma_f32_16x16x32_bf16 v[56:59], a[244:247], a[192:195], v[56:59]\n" \
"s_addk_i32 s84, 0x2000\n" \
"v_mfma_f32_16x16x32_bf16 v[60:63], a[184:187], a[128:131], v[60:63]\n" \
"buffer_load_dwordx4 v25, s[76:79], s84 offen offset:1024 lds\n" \
"v_mfma_f32_16x16x32_bf16 v[60:63], a[248:251], a[192:195], v[60:63]\n" \
"ds_read_b64_tr_b16 a[232:233], v34 offset:4096\n" \
"s_and_b32 s4, s4, 0x70000\n" \
"v_mfma_f32_16x16x32_bf16 v[64:67], a[188:191], a[128:131], v[64:67]\n" \
"ds_read_b64_tr_b16 a[234:235], v34 offset:4608\n" \
"s_or_b32 s5, s5, 0x2800\n" \
"v_mfma_f32_16x16x32_bf16 v[64:67], a[252:255], a[192:195], v[64:67]\n" \
"ds_read_b128 a[128:131], v28 offset:0\n" \
"v_writelane_b32 v11, s5, 51\n" \
"v_mfma_f32_16x16x32_bf16 v[80:83], a[188:191], a[132:135], v[80:83]\n" \
"ds_read_b128 a[192:195], v29 offset:0\n" \
"s_add_i32 s13, s13, s4\n" \
"v_mfma_f32_16x16x32_bf16 v[80:83], a[252:255], a[196:199], v[80:83]\n" \
"s_or_b32 s4, s13, 0x3000\n" \
"v_writelane_b32 v11, s4, 52\n" \
"v_mfma_f32_16x16x32_bf16 v[76:79], a[184:187], a[132:135], v[76:79]\n" \
"s_movk_i32 s11, 0x4000\n" \
"s_lshl_b32 s4, s2, 2\n" \
"v_mfma_f32_16x16x32_bf16 v[76:79], a[248:251], a[196:199], v[76:79]\n" \
"s_and_b32 s4, s4, 28\n" \
"s_bfe_u32 s5, s2, 0x20003\n" \
"v_mfma_f32_16x16x32_bf16 v[72:75], a[180:183], a[132:135], v[72:75]\n" \
"s_or_b32 s4, s4, s5\n" \
"s_lshr_b32 s5, s2, 5\n" \
"v_mfma_f32_16x16x32_bf16 v[72:75], a[244:247], a[196:199], v[72:75]\n" \
"s_lshr_b32 s19, s3, 1\n" \
"s_lshl_b32 s18, s5, 1\n" \
"v_mfma_f32_16x16x32_bf16 v[68:71], a[176:179], a[132:135], v[68:71]\n" \
"s_lshl_b32 s6, s4, 20\n" \
"s_lshl_b32 s7, s4, 8\n" \
"v_mfma_f32_16x16x32_bf16 v[68:71], a[240:243], a[196:199], v[68:71]\n" \
"ds_read_b128 a[132:135], v28 offset:2048\n" \
"ds_read_b128 a[196:199], v29 offset:2048\n" \
"s_waitcnt lgkmcnt(15)\n" \
"v_mfma_f32_16x16x32_bf16 a[0:3], a[160:163], a[144:147], a[0:3]\n" \
"s_addk_i32 s84, 0x2000\n" \
"v_mfma_f32_16x16x32_bf16 a[0:3], a[224:227], a[208:211], a[0:3]\n" \
"buffer_load_dwordx4 v24, s[76:79], s84 offen offset:2048 lds\n" \
"v_mfma_f32_16x16x32_bf16 a[4:7], a[164:167], a[144:147], a[4:7]\n" \
"ds_read_b64_tr_b16 a[176:177], v36 offset:0\n" \
"s_and_b32 s13, s3, 1\n" \
"v_mfma_f32_16x16x32_bf16 a[4:7], a[228:231], a[208:211], a[4:7]\n" \
"s_addk_i32 s84, 0x2000\n" \
"v_mfma_f32_16x16x32_bf16 a[12:15], a[172:175], a[144:147], a[12:15]\n" \
"buffer_load_dwordx4 v25, s[76:79], s84 offen offset:3072 lds\n" \
"s_waitcnt lgkmcnt(13)\n" \
"v_mfma_f32_16x16x32_bf16 a[12:15], a[236:239], a[208:211], a[12:15]\n" \
"ds_read_b64_tr_b16 a[178:179], v36 offset:1024\n" \
"s_add_i32 s14, s18, s19\n" \
"s_waitcnt lgkmcnt(10)\n" \
"v_mfma_f32_16x16x32_bf16 a[8:11], a[144:147], a[168:171], a[8:11]\n" \
"ds_read_b64_tr_b16 a[240:241], v36 offset:8192\n" \
"s_waitcnt lgkmcnt(7)\n" \
"v_mfma_f32_16x16x32_bf16 a[8:11], a[208:211], a[232:235], a[8:11]\n" \
"ds_read_b64_tr_b16 a[242:243], v36 offset:9216\n" \
"v_mfma_f32_16x16x32_bf16 a[24:27], a[148:151], a[168:171], a[24:27]\n" \
"ds_read_b64_tr_b16 a[180:181], v37 offset:0\n" \
"v_mfma_f32_16x16x32_bf16 a[24:27], a[212:215], a[232:235], a[24:27]\n" \
"ds_read_b64_tr_b16 a[182:183], v37 offset:1024\n" \
"v_mfma_f32_16x16x32_bf16 a[28:31], a[172:175], a[148:151], a[28:31]\n" \
"ds_read_b64_tr_b16 a[244:245], v37 offset:8192\n" \
"v_mfma_f32_16x16x32_bf16 a[28:31], a[236:239], a[212:215], a[28:31]\n" \
"ds_read_b64_tr_b16 a[246:247], v37 offset:9216\n" \
"v_mfma_f32_16x16x32_bf16 a[20:23], a[164:167], a[148:151], a[20:23]\n" \
"s_add_u32 s6, s26, s6\n" \
"s_addc_u32 s9, s27, 0\n" \
"v_mfma_f32_16x16x32_bf16 a[20:23], a[228:231], a[212:215], a[20:23]\n" \
"s_add_u32 s89, s42, 0x0\n" \
"s_nop 0\n" \
"buffer_store_dwordx4 v[192:195], v20, s[80:83], s89 offen offset:2048 sc1\n" \
"v_mfma_f32_16x16x32_bf16 a[16:19], a[160:163], a[148:151], a[16:19]\n" \
"ds_read_b64_tr_b16 a[184:185], v38 offset:0\n" \
"v_mfma_f32_16x16x32_bf16 a[16:19], a[224:227], a[212:215], a[16:19]\n" \
"ds_read_b64_tr_b16 a[186:187], v38 offset:1024\n" \
"v_mfma_f32_16x16x32_bf16 a[32:35], a[160:163], a[152:155], a[32:35]\n" \
"ds_read_b64_tr_b16 a[248:249], v38 offset:8192\n" \
"v_mfma_f32_16x16x32_bf16 a[32:35], a[224:227], a[216:219], a[32:35]\n" \
"ds_read_b64_tr_b16 a[250:251], v38 offset:9216\n" \
"v_mfma_f32_16x16x32_bf16 a[36:39], a[164:167], a[152:155], a[36:39]\n" \
"s_add_u32 s8, s6, 0x1000000\n" \
"s_addc_u32 s6, s9, 0\n" \
"v_mfma_f32_16x16x32_bf16 a[36:39], a[228:231], a[216:219], a[36:39]\n" \
"s_add_u32 s88, s42, 0x0\n" \
"buffer_store_dwordx4 v[196:199], v20, s[80:83], s88 offen offset:3072 sc1\n" \
"v_mfma_f32_16x16x32_bf16 a[44:47], a[172:175], a[152:155], a[44:47]\n" \
"ds_read_b64_tr_b16 a[188:189], v39 offset:0\n" \
"s_and_b32 s9, s6, 0xffff\n" \
"v_mfma_f32_16x16x32_bf16 a[44:47], a[236:239], a[216:219], a[44:47]\n" \
"ds_read_b64_tr_b16 a[190:191], v39 offset:1024\n" \
"s_lshl_b32 s6, s5, 16\n" \
"v_mfma_f32_16x16x32_bf16 a[40:43], a[152:155], a[168:171], a[40:43]\n" \
"ds_read_b64_tr_b16 a[252:253], v39 offset:8192\n" \
"s_sub_i32 s10, 0x100000, s6\n" \
"v_mfma_f32_16x16x32_bf16 a[40:43], a[216:219], a[232:235], a[40:43]\n" \
"s_mov_b32 s6, 0x8000\n" \
"v_mfma_f32_16x16x32_bf16 a[56:59], a[156:159], a[168:171], a[56:59]\n" \
"ds_read_b64_tr_b16 a[254:255], v39 offset:9216\n" \
"v_mfma_f32_16x16x32_bf16 a[56:59], a[220:223], a[232:235], a[56:59]\n" \
"s_cmp_lg_u32 s5, 0\n" \
"s_cselect_b32 s17, s11, s6\n" \
"v_mfma_f32_16x16x32_bf16 a[60:63], a[172:175], a[156:159], a[60:63]\n" \
"s_add_u32 s89, s42, 0x2000\n" \
"buffer_store_dwordx4 v[200:203], v20, s[80:83], s89 offen offset:0 sc1\n" \
"v_mfma_f32_16x16x32_bf16 a[60:63], a[236:239], a[220:223], a[60:63]\n" \
"s_lshl_b32 s6, s3, 1\n" \
"s_lshl_b32 s5, s5, 3\n" \
"v_mfma_f32_16x16x32_bf16 a[52:55], a[164:167], a[156:159], a[52:55]\n" \
"s_and_b32 s6, s6, 4\n" \
"s_or_b32 s12, s5, s6\n" \
"v_mfma_f32_16x16x32_bf16 a[52:55], a[228:231], a[220:223], a[52:55]\n" \
"s_sub_i32 s5, s13, s5\n" \
"s_or_b32 s5, s5, s6\n" \
"v_mfma_f32_16x16x32_bf16 a[48:51], a[160:163], a[156:159], a[48:51]\n" \
"s_or_b32 s6, s12, s13\n" \
"s_lshl_b32 s4, s4, 2\n" \
"v_mfma_f32_16x16x32_bf16 a[48:51], a[224:227], a[220:223], a[48:51]\n" \
"s_mulk_i32 s6, 0x92\n" \
"s_add_i32 s4, s4, 18\n" \
"v_mfma_f32_16x16x32_bf16 a[64:67], a[164:167], a[140:143], a[64:67]\n" \
"s_mulk_i32 s5, 0x92\n" \
"s_add_i32 s6, s4, s6\n" \
"v_mfma_f32_16x16x32_bf16 a[64:67], a[228:231], a[204:207], a[64:67]\n" \
"s_add_i32 s5, s5, s4\n" \
"s_lshl_b32 s4, s3, 11\n" \
"v_mfma_f32_16x16x32_bf16 a[80:83], a[172:175], a[140:143], a[80:83]\n" \
"s_lshl_b32 s15, s13, 12\n" \
"s_lshl_b32 s16, s19, 14\n" \
"v_mfma_f32_16x16x32_bf16 a[80:83], a[236:239], a[204:207], a[80:83]\n" \
"s_mul_i32 s19, s19, 0xe000\n" \
"s_mov_b32 s11, 0x20000\n" \
"v_mfma_f32_16x16x32_bf16 a[68:71], a[140:143], a[168:171], a[68:71]\n" \
"s_or_b32 s12, s4, 0xc000\n" \
"s_lshl_b32 s4, s3, 8\n" \
"v_mfma_f32_16x16x32_bf16 a[68:71], a[204:207], a[232:235], a[68:71]\n" \
"s_waitcnt vmcnt(3) lgkmcnt(0)\n" \
"s_barrier\n" \
"v_mfma_f32_16x16x32_bf16 v[116:119], a[176:179], a[144:147], v[116:119]\n" \
"s_add_u32 m0, s40, 0x0\n" \
"s_add_u32 s85, s39, 0x10000\n" \
"v_mfma_f32_16x16x32_bf16 v[116:119], a[240:243], a[208:211], v[116:119]\n" \
"buffer_load_dwordx4 v20, s[80:83], s85 offen sc1 lds\n" \
"v_mfma_f32_16x16x32_bf16 v[120:123], a[180:183], a[144:147], v[120:123]\n" \
"ds_read_b64_tr_b16 a[160:161], v32 offset:32768\n" \
"s_or_b32 s22, s15, s16\n" \
"v_mfma_f32_16x16x32_bf16 v[120:123], a[244:247], a[208:211], v[120:123]\n" \
"ds_read_b64_tr_b16 a[162:163], v32 offset:33280\n" \
"s_or_b32 s23, s19, s15\n" \
"v_mfma_f32_16x16x32_bf16 v[124:127], a[184:187], a[144:147], v[124:127]\n" \
"s_add_u32 m0, s40, 0x1000\n" \
"s_add_u32 s85, s39, 0x11000\n" \
"v_mfma_f32_16x16x32_bf16 v[124:127], a[248:251], a[208:211], v[124:127]\n" \
"buffer_load_dwordx4 v20, s[80:83], s85 offen sc1 lds\n" \
"v_mfma_f32_16x16x32_bf16 v[128:131], a[188:191], a[144:147], v[128:131]\n" \
".fill (L_pb + 73440 - .) / 4, 4, 0xBF800000\n" \
"L_pp18:\n" \
"s_cbranch_execz L_pret\n" \
"ds_read_b64_tr_b16 a[224:225], v32 offset:36864\n" \
"s_lshl_b32 s60, s6, 10\n" \
"v_mfma_f32_16x16x32_bf16 v[128:131], a[252:255], a[208:211], v[128:131]\n" \
"ds_read_b128 a[144:147], v28 offset:40960\n" \
"s_lshl_b32 s61, s5, 10\n" \
"v_mfma_f32_16x16x32_bf16 v[144:147], a[188:191], a[148:151], v[144:147]\n" \
"ds_read_b128 a[208:211], v29 offset:40960\n" \
"s_lshl_b32 s14, s14, 15\n" \
"v_mfma_f32_16x16x32_bf16 v[144:147], a[252:255], a[212:215], v[144:147]\n" \
"s_add_u32 m0, s40, 0x3000\n" \
"s_movk_i32 s84, 0x400\n" \
"v_mfma_f32_16x16x32_bf16 v[140:143], a[184:187], a[148:151], v[140:143]\n" \
"buffer_load_dwordx4 v21, s[76:79], s84 offen offset:0 lds\n" \
"v_mfma_f32_16x16x32_bf16 v[140:143], a[248:251], a[212:215], v[140:143]\n" \
"ds_read_b64_tr_b16 a[226:227], v32 offset:37376\n" \
"s_or_b32 s20, s4, 0x26c00\n" \
"v_mfma_f32_16x16x32_bf16 v[136:139], a[180:183], a[148:151], v[136:139]\n" \
"s_add_u32 m0, s30, 0x0\n" \
"s_movk_i32 s84, 0x400\n" \
"v_mfma_f32_16x16x32_bf16 v[136:139], a[244:247], a[212:215], v[136:139]\n" \
"buffer_load_dwordx4 v24, s[76:79], s84 offen offset:0 lds\n" \
"v_mfma_f32_16x16x32_bf16 v[132:135], a[176:179], a[148:151], v[132:135]\n" \
"ds_read_b64_tr_b16 a[164:165], v33 offset:32768\n" \
"s_or_b32 s13, s22, 0x8000\n" \
"v_mfma_f32_16x16x32_bf16 v[132:135], a[240:243], a[212:215], v[132:135]\n" \
"ds_read_b128 a[148:151], v28 offset:43008\n" \
"s_add_i32 s22, s22, 0x14000\n" \
"v_mfma_f32_16x16x32_bf16 v[148:151], a[176:179], a[152:155], v[148:151]\n" \
"ds_read_b128 a[212:215], v29 offset:43008\n" \
"s_bitset1_b32 s23, 16\n" \
"v_mfma_f32_16x16x32_bf16 v[148:151], a[240:243], a[216:219], v[148:151]\n" \
"s_movk_i32 s84, 0x2400\n" \
"v_mfma_f32_16x16x32_bf16 v[152:155], a[180:183], a[152:155], v[152:155]\n" \
"buffer_load_dwordx4 v25, s[76:79], s84 offen offset:1024 lds\n" \
"v_mfma_f32_16x16x32_bf16 v[152:155], a[244:247], a[216:219], v[152:155]\n" \
"ds_read_b64_tr_b16 a[166:167], v33 offset:33280\n" \
"s_or_b32 s24, s7, 0x5be8020\n" \
"v_mfma_f32_16x16x32_bf16 v[156:159], a[184:187], a[152:155], v[156:159]\n" \
"ds_read_b64_tr_b16 a[228:229], v33 offset:36864\n" \
"s_and_b32 s57, s27, 0xffff\n" \
"v_mfma_f32_16x16x32_bf16 v[156:159], a[248:251], a[216:219], v[156:159]\n" \
"s_movk_i32 s84, 0x4400\n" \
"v_mfma_f32_16x16x32_bf16 v[160:163], a[188:191], a[152:155], v[160:163]\n" \
"buffer_load_dwordx4 v24, s[76:79], s84 offen offset:2048 lds\n" \
"v_mfma_f32_16x16x32_bf16 v[160:163], a[252:255], a[216:219], v[160:163]\n" \
"ds_read_b128 a[152:155], v28 offset:45056\n" \
"s_mov_b32 s58, 0x6000000\n" \
"v_mfma_f32_16x16x32_bf16 v[176:179], a[188:191], a[156:159], v[176:179]\n" \
"ds_read_b128 a[216:219], v29 offset:45056\n" \
"s_mov_b32 s56, s26\n" \
"v_mfma_f32_16x16x32_bf16 v[176:179], a[252:255], a[220:223], v[176:179]\n" \
"s_movk_i32 s84, 0x6400\n" \
"v_mfma_f32_16x16x32_bf16 v[172:175], a[184:187], a[156:159], v[172:175]\n" \
"buffer_load_dwordx4 v25, s[76:79], s84 offen offset:3072 lds\n" \
"v_mfma_f32_16x16x32_bf16 v[172:175], a[248:251], a[220:223], v[172:175]\n" \
"ds_read_b64_tr_b16 a[230:231], v33 offset:37376\n" \
"s_mov_b32 s59, s11\n" \
"v_mfma_f32_16x16x32_bf16 v[168:171], a[180:183], a[156:159], v[168:171]\n" \
"ds_read_b64_tr_b16 a[172:173], v35 offset:32768\n" \
"s_add_i32 s60, s60, 0x3000000\n" \
"v_mfma_f32_16x16x32_bf16 v[168:171], a[244:247], a[220:223], v[168:171]\n" \
"ds_read_b64_tr_b16 a[174:175], v35 offset:33280\n" \
"s_add_i32 s61, s61, 0x411c000\n" \
"v_mfma_f32_16x16x32_bf16 v[164:167], a[176:179], a[156:159], v[164:167]\n" \
"s_sub_i32 s19, 14, s18\n" \
"s_mov_b32 s21, 0x71800000\n" \
"v_mfma_f32_16x16x32_bf16 v[164:167], a[240:243], a[220:223], v[164:167]\n" \
"ds_read_b128 a[156:159], v28 offset:47104\n" \
"s_mov_b32 s4, L_gsw-L_gtr\n" \
"v_mfma_f32_16x16x32_bf16 v[84:87], a[176:179], a[136:139], v[84:87]\n" \
"ds_read_b128 a[220:223], v29 offset:47104\n" \
"v_mfma_f32_16x16x32_bf16 v[84:87], a[240:243], a[200:203], v[84:87]\n" \
"s_add_u32 m0, s31, 0x0\n" \
"s_mov_b32 s84, 0x400000\n" \
"v_mfma_f32_16x16x32_bf16 v[88:91], a[180:183], a[136:139], v[88:91]\n" \
"buffer_load_dwordx4 v26, s[68:71], s84 offen offset:0 lds\n" \
"v_mfma_f32_16x16x32_bf16 v[88:91], a[244:247], a[200:203], v[88:91]\n" \
"s_addk_i32 s84, 0x7c00\n" \
"v_mfma_f32_16x16x32_bf16 v[92:95], a[184:187], a[136:139], v[92:95]\n" \
"buffer_load_dwordx4 v26, s[68:71], s84 offen offset:1024 lds\n" \
"v_mfma_f32_16x16x32_bf16 v[92:95], a[248:251], a[200:203], v[92:95]\n" \
"ds_read_b64_tr_b16 a[236:237], v35 offset:36864\n" \
"v_mfma_f32_16x16x32_bf16 v[96:99], a[188:191], a[136:139], v[96:99]\n" \
"ds_read_b64_tr_b16 a[238:239], v35 offset:37376\n" \
"v_mfma_f32_16x16x32_bf16 v[96:99], a[252:255], a[200:203], v[96:99]\n" \
"ds_read_b128 a[136:139], v28 offset:36864\n" \
"v_mfma_f32_16x16x32_bf16 v[112:115], a[188:191], a[140:143], v[112:115]\n" \
"ds_read_b128 a[200:203], v29 offset:36864\n" \
"v_mfma_f32_16x16x32_bf16 v[112:115], a[252:255], a[204:207], v[112:115]\n" \
"s_addk_i32 s84, 0x7c00\n" \
"v_mfma_f32_16x16x32_bf16 v[108:111], a[184:187], a[140:143], v[108:111]\n" \
"buffer_load_dwordx4 v27, s[68:71], s84 offen offset:2048 lds\n" \
"v_mfma_f32_16x16x32_bf16 v[108:111], a[248:251], a[204:207], v[108:111]\n" \
"s_addk_i32 s84, 0x7c00\n" \
"v_mfma_f32_16x16x32_bf16 v[104:107], a[180:183], a[140:143], v[104:107]\n" \
"buffer_load_dwordx4 v27, s[68:71], s84 offen offset:3072 lds\n" \
"v_mfma_f32_16x16x32_bf16 v[104:107], a[244:247], a[204:207], v[104:107]\n" \
"ds_read_b64_tr_b16 a[168:169], v34 offset:32768\n" \
"v_mfma_f32_16x16x32_bf16 v[100:103], a[176:179], a[140:143], v[100:103]\n" \
"ds_read_b64_tr_b16 a[170:171], v34 offset:33280\n" \
"v_mfma_f32_16x16x32_bf16 v[100:103], a[240:243], a[204:207], v[100:103]\n" \
"ds_read_b128 a[140:143], v28 offset:38912\n" \
"v_mfma_f32_16x16x32_bf16 v[52:55], a[176:179], a[128:131], v[52:55]\n" \
"ds_read_b128 a[204:207], v29 offset:38912\n" \
"v_mfma_f32_16x16x32_bf16 v[52:55], a[240:243], a[192:195], v[52:55]\n" \
"s_add_u32 m0, s30, 0x1000\n" \
"s_mov_b32 s84, 0x9400\n" \
"v_mfma_f32_16x16x32_bf16 v[56:59], a[180:183], a[128:131], v[56:59]\n" \
"buffer_load_dwordx4 v24, s[76:79], s84 offen offset:0 lds\n" \
"v_mfma_f32_16x16x32_bf16 v[56:59], a[244:247], a[192:195], v[56:59]\n" \
"s_addk_i32 s84, 0x2000\n" \
"s_nop 0\n" \
"v_mfma_f32_16x16x32_bf16 v[60:63], a[184:187], a[128:131], v[60:63]\n" \
"buffer_load_dwordx4 v25, s[76:79], s84 offen offset:1024 lds\n" \
"v_mfma_f32_16x16x32_bf16 v[60:63], a[248:251], a[192:195], v[60:63]\n" \
"ds_read_b64_tr_b16 a[232:233], v34 offset:36864\n" \
"v_mfma_f32_16x16x32_bf16 v[64:67], a[188:191], a[128:131], v[64:67]\n" \
"ds_read_b64_tr_b16 a[234:235], v34 offset:37376\n" \
"v_mfma_f32_16x16x32_bf16 v[64:67], a[252:255], a[192:195], v[64:67]\n" \
"ds_read_b128 a[128:131], v28 offset:32768\n" \
"v_mfma_f32_16x16x32_bf16 v[80:83], a[188:191], a[132:135], v[80:83]\n" \
"ds_read_b128 a[192:195], v29 offset:32768\n" \
"v_mfma_f32_16x16x32_bf16 v[80:83], a[252:255], a[196:199], v[80:83]\n" \
"s_cmp_eq_u32 s19, 0\n" \
"s_cselect_b32 s4, L_gs7-L_gtr, s4\n" \
"v_mfma_f32_16x16x32_bf16 v[76:79], a[184:187], a[132:135], v[76:79]\n" \
"s_cmp_eq_u32 s16, 0\n" \
"s_cselect_b32 s4, 0, s4\n" \
"v_mfma_f32_16x16x32_bf16 v[76:79], a[248:251], a[196:199], v[76:79]\n" \
"s_getpc_b64 s[28:29]\n" \
"L_gp1_%=:\n" \
"s_add_u32 s28, s28, L_gtr-L_gp1_%=\n" \
"s_addc_u32 s29, s29, 0\n" \
"v_mfma_f32_16x16x32_bf16 v[72:75], a[180:183], a[132:135], v[72:75]\n" \
"s_add_u32 s28, s28, s4\n" \
"s_addc_u32 s29, s29, 0\n" \
"v_mfma_f32_16x16x32_bf16 v[72:75], a[244:247], a[196:199], v[72:75]\n" \
"v_mfma_f32_16x16x32_bf16 v[68:71], a[176:179], a[132:135], v[68:71]\n" \
"v_mfma_f32_16x16x32_bf16 v[68:71], a[240:243], a[196:199], v[68:71]\n" \
"ds_read_b128 a[132:135], v28 offset:34816\n" \
"ds_read_b128 a[196:199], v29 offset:34816\n" \
"s_waitcnt lgkmcnt(15)\n" \
"v_mfma_f32_16x16x32_bf16 a[0:3], a[160:163], a[144:147], a[0:3]\n" \
"s_add_u32 m0, s30, 0x1000\n" \
"s_mov_b32 s84, 0xd400\n" \
"v_mfma_f32_16x16x32_bf16 a[0:3], a[224:227], a[208:211], a[0:3]\n" \
"buffer_load_dwordx4 v24, s[76:79], s84 offen offset:2048 lds\n" \
"v_mfma_f32_16x16x32_bf16 a[4:7], a[164:167], a[144:147], a[4:7]\n" \
"ds_read_b64_tr_b16 a[176:177], v36 offset:32768\n" \
"v_mfma_f32_16x16x32_bf16 a[4:7], a[228:231], a[208:211], a[4:7]\n" \
"s_addk_i32 s84, 0x2000\n" \
"v_mfma_f32_16x16x32_bf16 a[12:15], a[172:175], a[144:147], a[12:15]\n" \
"buffer_load_dwordx4 v25, s[76:79], s84 offen offset:3072 lds\n" \
"s_waitcnt lgkmcnt(13)\n" \
"v_mfma_f32_16x16x32_bf16 a[12:15], a[236:239], a[208:211], a[12:15]\n" \
"ds_read_b64_tr_b16 a[178:179], v36 offset:33792\n" \
"s_waitcnt lgkmcnt(10)\n" \
"v_mfma_f32_16x16x32_bf16 a[8:11], a[144:147], a[168:171], a[8:11]\n" \
"ds_read_b64_tr_b16 a[240:241], v36 offset:40960\n" \
"s_waitcnt lgkmcnt(7)\n" \
"v_mfma_f32_16x16x32_bf16 a[8:11], a[208:211], a[232:235], a[8:11]\n" \
"ds_read_b64_tr_b16 a[242:243], v36 offset:41984\n" \
"v_mfma_f32_16x16x32_bf16 a[24:27], a[148:151], a[168:171], a[24:27]\n" \
"ds_read_b64_tr_b16 a[180:181], v37 offset:32768\n" \
"v_mfma_f32_16x16x32_bf16 a[24:27], a[212:215], a[232:235], a[24:27]\n" \
"ds_read_b64_tr_b16 a[182:183], v37 offset:33792\n" \
"v_mfma_f32_16x16x32_bf16 a[28:31], a[172:175], a[148:151], a[28:31]\n" \
"ds_read_b64_tr_b16 a[244:245], v37 offset:40960\n" \
"v_mfma_f32_16x16x32_bf16 a[28:31], a[236:239], a[212:215], a[28:31]\n" \
"ds_read_b64_tr_b16 a[246:247], v37 offset:41984\n" \
"v_mfma_f32_16x16x32_bf16 a[20:23], a[164:167], a[148:151], a[20:23]\n" \
"v_mfma_f32_16x16x32_bf16 a[20:23], a[228:231], a[212:215], a[20:23]\n" \
"s_add_u32 s88, s42, 0x2000\n" \
"buffer_store_dwordx4 v[204:207], v20, s[80:83], s88 offen offset:1024 sc1\n" \
"v_mfma_f32_16x16x32_bf16 a[16:19], a[160:163], a[148:151], a[16:19]\n" \
"ds_read_b64_tr_b16 a[184:185], v38 offset:32768\n" \
"v_mfma_f32_16x16x32_bf16 a[16:19], a[224:227], a[212:215], a[16:19]\n" \
"ds_read_b64_tr_b16 a[186:187], v38 offset:33792\n" \
"v_mfma_f32_16x16x32_bf16 a[32:35], a[160:163], a[152:155], a[32:35]\n" \
"ds_read_b64_tr_b16 a[248:249], v38 offset:40960\n" \
"v_mfma_f32_16x16x32_bf16 a[32:35], a[224:227], a[216:219], a[32:35]\n" \
"ds_read_b64_tr_b16 a[250:251], v38 offset:41984\n" \
"v_mfma_f32_16x16x32_bf16 a[36:39], a[164:167], a[152:155], a[36:39]\n" \
"v_lshlrev_b32_e32 v45, 3, v20\n" \
"v_add_u32_e32 v45, s30, v45\n" \
"v_mfma_f32_16x16x32_bf16 a[36:39], a[228:231], a[216:219], a[36:39]\n" \
"s_add_u32 s89, s42, 0x2000\n" \
"buffer_store_dwordx4 v[208:211], v20, s[80:83], s89 offen offset:2048 sc1\n" \
"s_getpc_b64 s[86:87]\n" \
"L_ct1_%=:\n" \
"s_add_u32 s86, s86, L_ap0-L_ct1_%=\n" \
"s_addc_u32 s87, s87, 0\n" \
"s_add_u32 m0, s40, 0x2000\n" \
"v_mfma_f32_16x16x32_bf16 a[44:47], a[172:175], a[152:155], a[44:47]\n" \
"global_load_lds_dword v45, s[86:87]\n" \
"v_mfma_f32_16x16x32_bf16 a[44:47], a[236:239], a[216:219], a[44:47]\n" \
"ds_read_b64_tr_b16 a[188:189], v39 offset:32768\n" \
"v_mfma_f32_16x16x32_bf16 a[40:43], a[152:155], a[168:171], a[40:43]\n" \
"ds_read_b64_tr_b16 a[190:191], v39 offset:33792\n" \
"v_mfma_f32_16x16x32_bf16 a[40:43], a[216:219], a[232:235], a[40:43]\n" \
"ds_read_b64_tr_b16 a[252:253], v39 offset:40960\n" \
"v_mfma_f32_16x16x32_bf16 a[56:59], a[156:159], a[168:171], a[56:59]\n" \
"ds_read_b64_tr_b16 a[254:255], v39 offset:41984\n" \
"v_mfma_f32_16x16x32_bf16 a[56:59], a[220:223], a[232:235], a[56:59]\n" \
"v_mfma_f32_16x16x32_bf16 a[60:63], a[172:175], a[156:159], a[60:63]\n" \
"s_add_u32 s88, s42, 0x2000\n" \
"buffer_store_dwordx4 v[212:215], v20, s[80:83], s88 offen offset:3072 sc1\n" \
"v_mfma_f32_16x16x32_bf16 a[60:63], a[236:239], a[220:223], a[60:63]\n" \
"v_mfma_f32_16x16x32_bf16 a[52:55], a[164:167], a[156:159], a[52:55]\n" \
"v_mfma_f32_16x16x32_bf16 a[52:55], a[228:231], a[220:223], a[52:55]\n" \
"v_mfma_f32_16x16x32_bf16 a[48:51], a[160:163], a[156:159], a[48:51]\n" \
"v_mfma_f32_16x16x32_bf16 a[48:51], a[224:227], a[220:223], a[48:51]\n" \
"v_mfma_f32_16x16x32_bf16 a[64:67], a[164:167], a[140:143], a[64:67]\n" \
"v_mfma_f32_16x16x32_bf16 a[64:67], a[228:231], a[204:207], a[64:67]\n" \
"v_mfma_f32_16x16x32_bf16 a[80:83], a[172:175], a[140:143], a[80:83]\n" \
"v_mfma_f32_16x16x32_bf16 a[80:83], a[236:239], a[204:207], a[80:83]\n" \
"v_mfma_f32_16x16x32_bf16 a[68:71], a[140:143], a[168:171], a[68:71]\n" \
"v_mfma_f32_16x16x32_bf16 a[68:71], a[204:207], a[232:235], a[68:71]\n" \
"s_waitcnt vmcnt(4) lgkmcnt(0)\n" \
"s_barrier\n" \
"v_mfma_f32_16x16x32_bf16 v[116:119], a[176:179], a[144:147], v[116:119]\n" \
"ds_read_b64_tr_b16 a[160:161], v32 offset:0\n" \
"v_accvgpr_read_b32 v184, a0\n" \
"v_mfma_f32_16x16x32_bf16 v[116:119], a[240:243], a[208:211], v[116:119]\n" \
"v_accvgpr_read_b32 v185, a1\n" \
"v_accvgpr_read_b32 v186, a2\n" \
"v_mfma_f32_16x16x32_bf16 v[120:123], a[180:183], a[144:147], v[120:123]\n" \
"v_accvgpr_read_b32 v187, a3\n" \
"v_accvgpr_read_b32 v188, a4\n" \
"v_mfma_f32_16x16x32_bf16 v[120:123], a[244:247], a[208:211], v[120:123]\n" \
"ds_read_b64_tr_b16 a[162:163], v32 offset:512\n" \
"v_accvgpr_read_b32 v189, a5\n" \
"v_mfma_f32_16x16x32_bf16 v[124:127], a[184:187], a[144:147], v[124:127]\n" \
"v_accvgpr_read_b32 v190, a6\n" \
"v_accvgpr_read_b32 v191, a7\n" \
"v_mfma_f32_16x16x32_bf16 v[124:127], a[248:251], a[208:211], v[124:127]\n" \
"v_accvgpr_read_b32 v196, a12\n" \
"v_accvgpr_read_b32 v197, a13\n" \
"v_mfma_f32_16x16x32_bf16 v[128:131], a[188:191], a[144:147], v[128:131]\n" \
"ds_read_b64_tr_b16 a[224:225], v32 offset:4096\n" \
"v_accvgpr_read_b32 v198, a14\n" \
"v_mfma_f32_16x16x32_bf16 v[128:131], a[252:255], a[208:211], v[128:131]\n" \
"ds_read_b128 a[144:147], v28 offset:8192\n" \
"v_accvgpr_read_b32 v199, a15\n" \
"v_mfma_f32_16x16x32_bf16 v[144:147], a[188:191], a[148:151], v[144:147]\n" \
"ds_read_b128 a[208:211], v29 offset:8192\n" \
"v_accvgpr_read_b32 v192, a8\n" \
"v_mfma_f32_16x16x32_bf16 v[144:147], a[252:255], a[212:215], v[144:147]\n" \
"ds_read_b64_tr_b16 a[226:227], v32 offset:4608\n" \
"v_accvgpr_read_b32 v193, a9\n" \
"v_mfma_f32_16x16x32_bf16 v[140:143], a[184:187], a[148:151], v[140:143]\n" \
"v_accvgpr_read_b32 v194, a10\n" \
"v_accvgpr_read_b32 v195, a11\n" \
"v_mfma_f32_16x16x32_bf16 v[140:143], a[248:251], a[212:215], v[140:143]\n" \
"v_accvgpr_read_b32 v208, a24\n" \
"v_accvgpr_read_b32 v209, a25\n" \
"v_mfma_f32_16x16x32_bf16 v[136:139], a[180:183], a[148:151], v[136:139]\n" \
"v_accvgpr_read_b32 v210, a26\n" \
"v_accvgpr_read_b32 v211, a27\n" \
"v_mfma_f32_16x16x32_bf16 v[136:139], a[244:247], a[212:215], v[136:139]\n" \
"ds_read_b64_tr_b16 a[164:165], v33 offset:0\n" \
"v_accvgpr_read_b32 v212, a28\n" \
"v_mfma_f32_16x16x32_bf16 v[132:135], a[176:179], a[148:151], v[132:135]\n" \
"v_accvgpr_read_b32 v213, a29\n" \
"v_accvgpr_read_b32 v214, a30\n" \
"v_mfma_f32_16x16x32_bf16 v[132:135], a[240:243], a[212:215], v[132:135]\n" \
"ds_read_b128 a[148:151], v28 offset:10240\n" \
"v_accvgpr_read_b32 v215, a31\n" \
"v_mfma_f32_16x16x32_bf16 v[148:151], a[176:179], a[152:155], v[148:151]\n" \
"ds_read_b128 a[212:215], v29 offset:10240\n" \
"v_accvgpr_read_b32 v204, a20\n" \
"v_mfma_f32_16x16x32_bf16 v[148:151], a[240:243], a[216:219], v[148:151]\n" \
"ds_read_b64_tr_b16 a[166:167], v33 offset:512\n" \
"v_accvgpr_read_b32 v205, a21\n" \
"v_mfma_f32_16x16x32_bf16 v[152:155], a[180:183], a[152:155], v[152:155]\n" \
"v_accvgpr_read_b32 v206, a22\n" \
"v_accvgpr_read_b32 v207, a23\n" \
"v_mfma_f32_16x16x32_bf16 v[152:155], a[244:247], a[216:219], v[152:155]\n" \
"ds_read_b64_tr_b16 a[228:229], v33 offset:4096\n" \
"v_accvgpr_read_b32 v200, a16\n" \
"v_mfma_f32_16x16x32_bf16 v[156:159], a[184:187], a[152:155], v[156:159]\n" \
"v_accvgpr_read_b32 v201, a17\n" \
"v_accvgpr_read_b32 v202, a18\n" \
"v_mfma_f32_16x16x32_bf16 v[156:159], a[248:251], a[216:219], v[156:159]\n" \
"v_accvgpr_read_b32 v203, a19\n" \
"v_accvgpr_read_b32 v216, a32\n" \
"v_mfma_f32_16x16x32_bf16 v[160:163], a[188:191], a[152:155], v[160:163]\n" \
"ds_read_b64_tr_b16 a[230:231], v33 offset:4608\n" \
"v_accvgpr_read_b32 v217, a33\n" \
"v_mfma_f32_16x16x32_bf16 v[160:163], a[252:255], a[216:219], v[160:163]\n" \
"ds_read_b128 a[152:155], v28 offset:12288\n" \
"v_accvgpr_read_b32 v218, a34\n" \
"v_mfma_f32_16x16x32_bf16 v[176:179], a[188:191], a[156:159], v[176:179]\n" \
"ds_read_b128 a[216:219], v29 offset:12288\n" \
"v_accvgpr_read_b32 v219, a35\n" \
"v_mfma_f32_16x16x32_bf16 v[176:179], a[252:255], a[220:223], v[176:179]\n" \
"v_accvgpr_read_b32 v220, a36\n" \
"v_accvgpr_read_b32 v221, a37\n" \
"v_mfma_f32_16x16x32_bf16 v[172:175], a[184:187], a[156:159], v[172:175]\n" \
"ds_read_b64_tr_b16 a[172:173], v35 offset:0\n" \
"v_accvgpr_read_b32 v222, a38\n" \
"v_mfma_f32_16x16x32_bf16 v[172:175], a[248:251], a[220:223], v[172:175]\n" \
"v_accvgpr_read_b32 v223, a39\n" \
"v_accvgpr_read_b32 v228, a44\n" \
"v_mfma_f32_16x16x32_bf16 v[168:171], a[180:183], a[156:159], v[168:171]\n" \
"v_accvgpr_read_b32 v229, a45\n" \
"v_accvgpr_read_b32 v230, a46\n" \
"v_mfma_f32_16x16x32_bf16 v[168:171], a[244:247], a[220:223], v[168:171]\n" \
"ds_read_b64_tr_b16 a[174:175], v35 offset:512\n" \
"v_accvgpr_read_b32 v231, a47\n" \
"v_mfma_f32_16x16x32_bf16 v[164:167], a[176:179], a[156:159], v[164:167]\n" \
"v_accvgpr_read_b32 v224, a40\n" \
"v_accvgpr_read_b32 v225, a41\n" \
"v_mfma_f32_16x16x32_bf16 v[164:167], a[240:243], a[220:223], v[164:167]\n" \
"ds_read_b128 a[156:159], v28 offset:14336\n" \
"v_accvgpr_read_b32 v226, a42\n" \
"v_mfma_f32_16x16x32_bf16 v[84:87], a[176:179], a[136:139], v[84:87]\n" \
"ds_read_b128 a[220:223], v29 offset:14336\n" \
"v_accvgpr_read_b32 v227, a43\n" \
"v_mfma_f32_16x16x32_bf16 v[84:87], a[240:243], a[200:203], v[84:87]\n" \
"ds_read_b64_tr_b16 a[236:237], v35 offset:4096\n" \
"v_accvgpr_read_b32 v240, a56\n" \
"v_mfma_f32_16x16x32_bf16 v[88:91], a[180:183], a[136:139], v[88:91]\n" \
"v_accvgpr_read_b32 v241, a57\n" \
"v_accvgpr_read_b32 v242, a58\n" \
"v_mfma_f32_16x16x32_bf16 v[88:91], a[244:247], a[200:203], v[88:91]\n" \
"ds_read_b64_tr_b16 a[238:239], v35 offset:4608\n" \
"v_accvgpr_read_b32 v243, a59\n" \
"v_mfma_f32_16x16x32_bf16 v[92:95], a[184:187], a[136:139], v[92:95]\n" \
"v_accvgpr_read_b32 v244, a60\n" \
"v_accvgpr_read_b32 v245, a61\n" \
"v_mfma_f32_16x16x32_bf16 v[92:95], a[248:251], a[200:203], v[92:95]\n" \
"v_accvgpr_read_b32 v246, a62\n" \
"v_accvgpr_read_b32 v247, a63\n" \
"v_mfma_f32_16x16x32_bf16 v[96:99], a[188:191], a[136:139], v[96:99]\n" \
"v_accvgpr_read_b32 v236, a52\n" \
"v_accvgpr_read_b32 v237, a53\n" \
"v_mfma_f32_16x16x32_bf16 v[96:99], a[252:255], a[200:203], v[96:99]\n" \
"ds_read_b128 a[136:139], v28 offset:4096\n" \
"v_accvgpr_read_b32 v238, a54\n" \
"v_mfma_f32_16x16x32_bf16 v[112:115], a[188:191], a[140:143], v[112:115]\n" \
"ds_read_b128 a[200:203], v29 offset:4096\n" \
"v_accvgpr_read_b32 v239, a55\n" \
"v_mfma_f32_16x16x32_bf16 v[112:115], a[252:255], a[204:207], v[112:115]\n" \
"ds_read_b64_tr_b16 a[168:169], v34 offset:0\n" \
"v_accvgpr_read_b32 v232, a48\n" \
"v_mfma_f32_16x16x32_bf16 v[108:111], a[184:187], a[140:143], v[108:111]\n" \
"ds_read_b64_tr_b16 a[170:171], v34 offset:512\n" \
"v_accvgpr_read_b32 v233, a49\n" \
"v_mfma_f32_16x16x32_bf16 v[108:111], a[248:251], a[204:207], v[108:111]\n" \
"v_accvgpr_read_b32 v234, a50\n" \
"v_accvgpr_read_b32 v235, a51\n" \
"v_mfma_f32_16x16x32_bf16 v[104:107], a[180:183], a[140:143], v[104:107]\n" \
"v_accvgpr_read_b32 v180, a64\n" \
"v_accvgpr_read_b32 v181, a65\n" \
"v_mfma_f32_16x16x32_bf16 v[104:107], a[244:247], a[204:207], v[104:107]\n" \
"ds_read_b64_tr_b16 a[232:233], v34 offset:4096\n" \
"v_accvgpr_read_b32 v182, a66\n" \
"v_mfma_f32_16x16x32_bf16 v[100:103], a[176:179], a[140:143], v[100:103]\n" \
"v_accvgpr_read_b32 v183, a67\n" \
"v_accvgpr_read_b32 v48, a80\n" \
"v_mfma_f32_16x16x32_bf16 v[100:103], a[240:243], a[204:207], v[100:103]\n" \
"ds_read_b128 a[140:143], v28 offset:6144\n" \
"v_accvgpr_read_b32 v49, a81\n" \
"v_mfma_f32_16x16x32_bf16 v[52:55], a[176:179], a[128:131], v[52:55]\n" \
"ds_read_b128 a[204:207], v29 offset:6144\n" \
"v_accvgpr_read_b32 v50, a82\n" \
"v_mfma_f32_16x16x32_bf16 v[52:55], a[240:243], a[192:195], v[52:55]\n" \
"ds_read_b64_tr_b16 a[234:235], v34 offset:4608\n" \
"v_accvgpr_read_b32 v51, a83\n" \
"v_mfma_f32_16x16x32_bf16 v[56:59], a[180:183], a[128:131], v[56:59]\n" \
"v_accvgpr_read_b32 v252, a68\n" \
"v_accvgpr_read_b32 v253, a69\n" \
"v_mfma_f32_16x16x32_bf16 v[56:59], a[244:247], a[192:195], v[56:59]\n" \
"v_accvgpr_read_b32 v254, a70\n" \
"v_accvgpr_read_b32 v255, a71\n" \
"v_mfma_f32_16x16x32_bf16 v[60:63], a[184:187], a[128:131], v[60:63]\n" \
"v_lshrrev_b32_e64 v24, 9, v20\n" \
"v_and_b32_e32 v26, 0x1ff, v20\n" \
"v_mfma_f32_16x16x32_bf16 v[60:63], a[248:251], a[192:195], v[60:63]\n" \
"v_lshl_add_u32 v24, v24, 3, v26\n" \
"v_lshrrev_b32_e64 v25, 8, v20\n" \
"v_mfma_f32_16x16x32_bf16 v[64:67], a[188:191], a[128:131], v[64:67]\n" \
"v_mul_u32_u24_e32 v25, 0x24700, v25\n" \
"v_add_u32_e64 v25, v25, v20\n" \
"v_mfma_f32_16x16x32_bf16 v[64:67], a[252:255], a[192:195], v[64:67]\n" \
"ds_read_b128 a[128:131], v28 offset:0\n" \
"v_lshrrev_b32_e64 v26, 2, v20\n" \
"v_mfma_f32_16x16x32_bf16 v[80:83], a[188:191], a[132:135], v[80:83]\n" \
"ds_read_b128 a[192:195], v29 offset:0\n" \
"v_add_u32_e64 v26, s45, v26\n" \
"v_mfma_f32_16x16x32_bf16 v[80:83], a[252:255], a[196:199], v[80:83]\n" \
"v_add_u32_e32 v27, s46, v20\n" \
"v_add_u32_e32 v21, s43, v27\n" \
"v_mfma_f32_16x16x32_bf16 v[76:79], a[184:187], a[132:135], v[76:79]\n" \
"v_mfma_f32_16x16x32_bf16 v[76:79], a[248:251], a[196:199], v[76:79]\n" \
"v_mfma_f32_16x16x32_bf16 v[72:75], a[180:183], a[132:135], v[72:75]\n" \
"v_mfma_f32_16x16x32_bf16 v[72:75], a[244:247], a[196:199], v[72:75]\n" \
"v_mfma_f32_16x16x32_bf16 v[68:71], a[176:179], a[132:135], v[68:71]\n" \
"v_mfma_f32_16x16x32_bf16 v[68:71], a[240:243], a[196:199], v[68:71]\n" \
"ds_read_b128 a[132:135], v28 offset:2048\n" \
"ds_read_b128 a[196:199], v29 offset:2048\n" \
"s_waitcnt lgkmcnt(15)\n" \
"s_nop 0\n" \
"v_mfma_f32_16x16x32_bf16 v[184:187], a[160:163], a[144:147], v[184:187]\n" \
"v_bfe_u32 v44, v20, 2, 5\n" \
"ds_read_b64_tr_b16 a[176:177], v36 offset:0\n" \
"v_mfma_f32_16x16x32_bf16 v[184:187], a[224:227], a[208:211], v[184:187]\n" \
"buffer_load_dword v44, v44, s[80:83], s38 offen offset:0 sc1\n" \
"v_mfma_f32_16x16x32_bf16 v[188:191], a[164:167], a[144:147], v[188:191]\n" \
"ds_read_b64_tr_b16 a[178:179], v36 offset:1024\n" \
"v_mfma_f32_16x16x32_bf16 v[188:191], a[228:231], a[208:211], v[188:191]\n" \
"ds_read_b64_tr_b16 a[240:241], v36 offset:8192\n" \
"v_mfma_f32_16x16x32_bf16 v[196:199], a[172:175], a[144:147], v[196:199]\n" \
"ds_read_b64_tr_b16 a[242:243], v36 offset:9216\n" \
"s_waitcnt lgkmcnt(15)\n" \
"v_mfma_f32_16x16x32_bf16 v[196:199], a[236:239], a[208:211], v[196:199]\n" \
"s_waitcnt lgkmcnt(12)\n" \
"v_mfma_f32_16x16x32_bf16 v[192:195], a[144:147], a[168:171], v[192:195]\n" \
"ds_read_b64_tr_b16 a[180:181], v37 offset:0\n" \
"s_waitcnt lgkmcnt(9)\n" \
"s_nop 0\n" \
"v_mfma_f32_16x16x32_bf16 v[192:195], a[208:211], a[232:235], v[192:195]\n" \
"v_mfma_f32_16x16x32_bf16 v[208:211], a[148:151], a[168:171], v[208:211]\n" \
"ds_read_b64_tr_b16 a[182:183], v37 offset:1024\n" \
"v_mfma_f32_16x16x32_bf16 v[208:211], a[212:215], a[232:235], v[208:211]\n" \
"ds_read_b64_tr_b16 a[244:245], v37 offset:8192\n" \
"v_mfma_f32_16x16x32_bf16 v[212:215], a[172:175], a[148:151], v[212:215]\n" \
"v_mfma_f32_16x16x32_bf16 v[212:215], a[236:239], a[212:215], v[212:215]\n" \
"ds_read_b64_tr_b16 a[246:247], v37 offset:9216\n" \
"v_cvt_pk_bf16_f32 v188, v188, v189\n" \
"v_mfma_f32_16x16x32_bf16 v[204:207], a[164:167], a[148:151], v[204:207]\n" \
"v_cvt_pk_bf16_f32 v189, v190, v191\n" \
"v_cvt_pk_bf16_f32 v190, v198, v199\n" \
"v_mfma_f32_16x16x32_bf16 v[204:207], a[228:231], a[212:215], v[204:207]\n" \
"ds_read_b64_tr_b16 a[184:185], v38 offset:0\n" \
"v_mov_b32_e32 v191, 0x3f80\n" \
"v_mfma_f32_16x16x32_bf16 v[200:203], a[160:163], a[148:151], v[200:203]\n" \
"ds_read_b64_tr_b16 a[186:187], v38 offset:1024\n" \
"v_cvt_pk_bf16_f32 v184, v184, v185\n" \
"v_mfma_f32_16x16x32_bf16 v[200:203], a[224:227], a[212:215], v[200:203]\n" \
"v_cvt_pk_bf16_f32 v185, v186, v187\n" \
"v_cvt_pk_bf16_f32 v186, v196, v197\n" \
"v_mfma_f32_16x16x32_bf16 v[216:219], a[160:163], a[152:155], v[216:219]\n" \
"ds_read_b64_tr_b16 a[248:249], v38 offset:8192\n" \
"v_mov_b32_e64 v187, 0\n" \
"v_mfma_f32_16x16x32_bf16 v[216:219], a[224:227], a[216:219], v[216:219]\n" \
"ds_read_b64_tr_b16 a[250:251], v38 offset:9216\n" \
"v_mfma_f32_16x16x32_bf16 v[220:223], a[164:167], a[152:155], v[220:223]\n" \
"v_mfma_f32_16x16x32_bf16 v[220:223], a[228:231], a[216:219], v[220:223]\n" \
"ds_read_b64_tr_b16 a[188:189], v39 offset:0\n" \
"v_cvt_pk_bf16_f32 v204, v204, v205\n" \
"v_mfma_f32_16x16x32_bf16 v[228:231], a[172:175], a[152:155], v[228:231]\n" \
"v_cvt_pk_bf16_f32 v205, v206, v207\n" \
"v_cvt_pk_bf16_f32 v206, v214, v215\n" \
"v_mfma_f32_16x16x32_bf16 v[228:231], a[236:239], a[216:219], v[228:231]\n" \
"ds_read_b64_tr_b16 a[190:191], v39 offset:1024\n" \
"v_mov_b32_e32 v207, 0x3f80\n" \
"v_mfma_f32_16x16x32_bf16 v[224:227], a[152:155], a[168:171], v[224:227]\n" \
"ds_read_b64_tr_b16 a[252:253], v39 offset:8192\n" \
"v_cvt_pk_bf16_f32 v200, v200, v201\n" \
"v_mfma_f32_16x16x32_bf16 v[224:227], a[216:219], a[232:235], v[224:227]\n" \
"v_cvt_pk_bf16_f32 v201, v202, v203\n" \
"v_cvt_pk_bf16_f32 v202, v212, v213\n" \
"v_mfma_f32_16x16x32_bf16 v[240:243], a[156:159], a[168:171], v[240:243]\n" \
"ds_read_b64_tr_b16 a[254:255], v39 offset:9216\n" \
"v_mov_b32_e64 v203, 0\n" \
"v_mfma_f32_16x16x32_bf16 v[240:243], a[220:223], a[232:235], v[240:243]\n" \
"v_mfma_f32_16x16x32_bf16 v[244:247], a[172:175], a[156:159], v[244:247]\n" \
"v_mfma_f32_16x16x32_bf16 v[244:247], a[236:239], a[220:223], v[244:247]\n" \
"v_cvt_pk_bf16_f32 v220, v220, v221\n" \
"v_cvt_pk_bf16_f32 v221, v222, v223\n" \
"v_mfma_f32_16x16x32_bf16 v[236:239], a[164:167], a[156:159], v[236:239]\n" \
"v_cvt_pk_bf16_f32 v222, v230, v231\n" \
"v_mov_b32_e32 v223, 0x3f80\n" \
"v_mfma_f32_16x16x32_bf16 v[236:239], a[228:231], a[220:223], v[236:239]\n" \
"v_cvt_pk_bf16_f32 v216, v216, v217\n" \
"v_cvt_pk_bf16_f32 v217, v218, v219\n" \
"v_mfma_f32_16x16x32_bf16 v[232:235], a[160:163], a[156:159], v[232:235]\n" \
"v_cvt_pk_bf16_f32 v218, v228, v229\n" \
"v_mov_b32_e64 v219, 0\n" \
"v_mfma_f32_16x16x32_bf16 v[232:235], a[224:227], a[220:223], v[232:235]\n" \
"v_mfma_f32_16x16x32_bf16 v[180:183], a[164:167], a[140:143], v[180:183]\n" \
"v_mfma_f32_16x16x32_bf16 v[180:183], a[228:231], a[204:207], v[180:183]\n" \
"v_mfma_f32_16x16x32_bf16 v[48:51], a[172:175], a[140:143], v[48:51]\n" \
"v_mfma_f32_16x16x32_bf16 v[48:51], a[236:239], a[204:207], v[48:51]\n" \
"v_cvt_pk_bf16_f32 v236, v236, v237\n" \
"v_cvt_pk_bf16_f32 v237, v238, v239\n" \
"v_mfma_f32_16x16x32_bf16 v[252:255], a[140:143], a[168:171], v[252:255]\n" \
"v_cvt_pk_bf16_f32 v238, v246, v247\n" \
"v_mov_b32_e32 v239, 0x3f80\n" \
"v_mfma_f32_16x16x32_bf16 v[252:255], a[204:207], a[232:235], v[252:255]\n" \
"v_cvt_pk_bf16_f32 v232, v232, v233\n" \
"v_cvt_pk_bf16_f32 v233, v234, v235\n" \
"s_waitcnt vmcnt(0) lgkmcnt(0)\n" \
"s_barrier\n" \
"v_mov_b32_e32 v45, s62\n" \
"s_mov_b64 exec, 1\n" \
"buffer_store_dword v45, off, s[80:83], s37 offset:32 sc1\n" \
"s_mov_b64 exec, -1\n" \
"v_cmp_ne_u32_e32 vcc, s62, v44\n" \
"s_nop 4\n" \
"s_cbranch_vccnz L_poll161_%=\n" \
"L_pollr161_%=:\n" \
"v_mfma_f32_16x16x32_bf16 v[116:119], a[176:179], a[144:147], v[116:119]\n" \
"s_mov_b32 s73, s50\n" \
"s_sub_u32 m0, s31, 0x10000\n" \
"v_mfma_f32_16x16x32_bf16 v[116:119], a[240:243], a[208:211], v[116:119]\n" \
"buffer_load_dwordx4 v20, s[80:83], s73 offen offset:0 sc1 lds\n" \
"v_mfma_f32_16x16x32_bf16 v[120:123], a[180:183], a[144:147], v[120:123]\n" \
"s_add_u32 s74, s50, 0x4000\n" \
"s_add_u32 s75, s50, 0x8000\n" \
"v_mfma_f32_16x16x32_bf16 v[120:123], a[244:247], a[208:211], v[120:123]\n" \
"s_mov_b32 s70, 0x3e0293ee\n" \
"s_mov_b32 s76, s52\n" \
"v_mfma_f32_16x16x32_bf16 v[124:127], a[184:187], a[144:147], v[124:127]\n" \
"v_mul_f32_e32 v116, s70, v116\n" \
"v_mul_f32_e32 v117, s70, v117\n" \
"v_mfma_f32_16x16x32_bf16 v[124:127], a[248:251], a[208:211], v[124:127]\n" \
"buffer_load_dwordx4 v20, s[80:83], s73 offen offset:1024 sc1 lds\n" \
"v_mfma_f32_16x16x32_bf16 v[128:131], a[188:191], a[144:147], v[128:131]\n" \
"v_mul_f32_e32 v118, s70, v118\n" \
"v_mul_f32_e32 v119, s70, v119\n" \
"v_mfma_f32_16x16x32_bf16 v[128:131], a[252:255], a[208:211], v[128:131]\n" \
"v_cvt_pk_bf16_f32 v116, v116, v117\n" \
"v_mul_f32_e64 v120, s70, v120\n" \
"v_mfma_f32_16x16x32_bf16 v[144:147], a[188:191], a[148:151], v[144:147]\n" \
"v_mul_f32_e32 v124, s70, v124\n" \
"v_mul_f32_e32 v125, s70, v125\n" \
"v_mfma_f32_16x16x32_bf16 v[144:147], a[252:255], a[212:215], v[144:147]\n" \
"buffer_load_dwordx4 v20, s[80:83], s73 offen offset:2048 sc1 lds\n" \
"v_mfma_f32_16x16x32_bf16 v[140:143], a[184:187], a[148:151], v[140:143]\n" \
"v_mul_f32_e32 v126, s70, v126\n" \
"v_mul_f32_e32 v127, s70, v127\n" \
"v_mfma_f32_16x16x32_bf16 v[140:143], a[248:251], a[212:215], v[140:143]\n" \
"v_cvt_pk_bf16_f32 v124, v124, v125\n" \
"v_mul_f32_e64 v121, s70, v121\n" \
"v_mfma_f32_16x16x32_bf16 v[136:139], a[180:183], a[148:151], v[136:139]\n" \
"v_mul_f32_e32 v122, s70, v122\n" \
"v_mul_f32_e32 v123, s70, v123\n" \
"v_mfma_f32_16x16x32_bf16 v[136:139], a[244:247], a[212:215], v[136:139]\n" \
"buffer_load_dwordx4 v20, s[80:83], s73 offen offset:3072 sc1 lds\n" \
"v_mfma_f32_16x16x32_bf16 v[132:135], a[176:179], a[148:151], v[132:135]\n" \
"s_sub_u32 m0, s31, s51\n" \
"v_cvt_pk_bf16_f32 v117, v118, v119\n" \
"v_mfma_f32_16x16x32_bf16 v[132:135], a[240:243], a[212:215], v[132:135]\n" \
"v_mul_f32_e32 v128, s70, v128\n" \
"v_mul_f32_e32 v129, s70, v129\n" \
".fill (L_pb + 77536 - .) / 4, 4, 0xBF800000\n" \
"L_pp19:\n" \
"s_cbranch_execz L_pret\n" \
"v_mfma_f32_16x16x32_bf16 v[148:151], a[176:179], a[152:155], v[148:151]\n" \
"v_mul_f32_e32 v130, s70, v130\n" \
"v_mul_f32_e64 v131, s70, v131\n" \
"v_mfma_f32_16x16x32_bf16 v[148:151], a[240:243], a[216:219], v[148:151]\n" \
"buffer_load_dwordx4 v20, s[80:83], s74 offen offset:0 sc1 lds\n" \
"v_mfma_f32_16x16x32_bf16 v[152:155], a[180:183], a[152:155], v[152:155]\n" \
"v_cvt_pk_bf16_f32 v125, v126, v127\n" \
"v_cvt_pk_bf16_f32 v118, v120, v121\n" \
"v_mfma_f32_16x16x32_bf16 v[152:155], a[244:247], a[216:219], v[152:155]\n" \
"v_cvt_pk_bf16_f32 v119, v122, v123\n" \
"v_cvt_pk_bf16_f32 v126, v128, v129\n" \
"v_mfma_f32_16x16x32_bf16 v[156:159], a[184:187], a[152:155], v[156:159]\n" \
"v_cvt_pk_bf16_f32 v127, v130, v131\n" \
"v_mul_f32_e64 v132, s70, v132\n" \
"v_mfma_f32_16x16x32_bf16 v[156:159], a[248:251], a[216:219], v[156:159]\n" \
"buffer_load_dwordx4 v20, s[80:83], s74 offen offset:1024 sc1 lds\n" \
"v_mfma_f32_16x16x32_bf16 v[160:163], a[188:191], a[152:155], v[160:163]\n" \
"ds_write_b128 v21, v[116:119] offset:16384\n" \
"v_mfma_f32_16x16x32_bf16 v[160:163], a[252:255], a[216:219], v[160:163]\n" \
"v_mul_f32_e32 v133, s70, v133\n" \
"v_mul_f32_e32 v140, s70, v140\n" \
"v_mfma_f32_16x16x32_bf16 v[176:179], a[188:191], a[156:159], v[176:179]\n" \
"v_mul_f32_e32 v141, s70, v141\n" \
"v_mul_f32_e32 v134, s70, v134\n" \
"v_mfma_f32_16x16x32_bf16 v[176:179], a[252:255], a[220:223], v[176:179]\n" \
"buffer_load_dwordx4 v20, s[80:83], s74 offen offset:2048 sc1 lds\n" \
"v_mfma_f32_16x16x32_bf16 v[172:175], a[184:187], a[156:159], v[172:175]\n" \
"ds_write_b128 v21, v[124:127] offset:17408\n" \
"v_mfma_f32_16x16x32_bf16 v[172:175], a[248:251], a[220:223], v[172:175]\n" \
"v_mul_f32_e64 v135, s70, v135\n" \
"v_cvt_pk_bf16_f32 v132, v132, v133\n" \
"v_mfma_f32_16x16x32_bf16 v[168:171], a[180:183], a[156:159], v[168:171]\n" \
"v_mul_f32_e32 v142, s70, v142\n" \
"v_mul_f32_e32 v143, s70, v143\n" \
"v_mfma_f32_16x16x32_bf16 v[168:171], a[244:247], a[220:223], v[168:171]\n" \
"buffer_load_dwordx4 v20, s[80:83], s74 offen offset:3072 sc1 lds\n" \
"v_mfma_f32_16x16x32_bf16 v[164:167], a[176:179], a[156:159], v[164:167]\n" \
"s_sub_u32 m0, s31, 0x4000\n" \
"v_cvt_pk_bf16_f32 v140, v140, v141\n" \
"v_mfma_f32_16x16x32_bf16 v[164:167], a[240:243], a[220:223], v[164:167]\n" \
"v_mul_f32_e32 v136, s70, v136\n" \
"v_mul_f32_e32 v137, s70, v137\n" \
"v_mfma_f32_16x16x32_bf16 v[84:87], a[176:179], a[136:139], v[84:87]\n" \
"v_mul_f32_e32 v138, s70, v138\n" \
"v_mul_f32_e32 v139, s70, v139\n" \
"v_mfma_f32_16x16x32_bf16 v[84:87], a[240:243], a[200:203], v[84:87]\n" \
"buffer_load_dwordx4 v20, s[80:83], s75 offen offset:0 sc1 lds\n" \
"v_mfma_f32_16x16x32_bf16 v[88:91], a[180:183], a[136:139], v[88:91]\n" \
"v_cvt_pk_bf16_f32 v133, v134, v135\n" \
"v_mul_f32_e64 v144, s70, v144\n" \
"v_mfma_f32_16x16x32_bf16 v[88:91], a[244:247], a[200:203], v[88:91]\n" \
"v_mul_f32_e32 v145, s70, v145\n" \
"v_mul_f32_e32 v146, s70, v146\n" \
"v_mfma_f32_16x16x32_bf16 v[92:95], a[184:187], a[136:139], v[92:95]\n" \
"v_mul_f32_e64 v147, s70, v147\n" \
"v_cvt_pk_bf16_f32 v141, v142, v143\n" \
"v_mfma_f32_16x16x32_bf16 v[92:95], a[248:251], a[200:203], v[92:95]\n" \
"buffer_load_dwordx4 v20, s[80:83], s75 offen offset:1024 sc1 lds\n" \
"v_mfma_f32_16x16x32_bf16 v[96:99], a[188:191], a[136:139], v[96:99]\n" \
"v_cvt_pk_bf16_f32 v134, v136, v137\n" \
"v_cvt_pk_bf16_f32 v135, v138, v139\n" \
"v_mfma_f32_16x16x32_bf16 v[96:99], a[252:255], a[200:203], v[96:99]\n" \
"v_cvt_pk_bf16_f32 v142, v144, v145\n" \
"v_cvt_pk_bf16_f32 v143, v146, v147\n" \
"v_mfma_f32_16x16x32_bf16 v[112:115], a[188:191], a[140:143], v[112:115]\n" \
"ds_write_b128 v21, v[132:135] offset:20480\n" \
"v_mfma_f32_16x16x32_bf16 v[112:115], a[252:255], a[204:207], v[112:115]\n" \
"buffer_load_dwordx4 v20, s[80:83], s75 offen offset:2048 sc1 lds\n" \
"v_mfma_f32_16x16x32_bf16 v[108:111], a[184:187], a[140:143], v[108:111]\n" \
"v_cvt_pk_bf16_f32 v180, v180, v181\n" \
"v_cvt_pk_bf16_f32 v181, v182, v183\n" \
"v_mfma_f32_16x16x32_bf16 v[108:111], a[248:251], a[204:207], v[108:111]\n" \
"ds_write_b128 v21, v[140:143] offset:21504\n" \
"v_mfma_f32_16x16x32_bf16 v[104:107], a[180:183], a[140:143], v[104:107]\n" \
"v_cvt_pk_bf16_f32 v234, v244, v245\n" \
"v_mov_b32_e64 v235, 0\n" \
"v_mfma_f32_16x16x32_bf16 v[104:107], a[244:247], a[204:207], v[104:107]\n" \
"buffer_load_dwordx4 v20, s[80:83], s75 offen offset:3072 sc1 lds\n" \
"v_mfma_f32_16x16x32_bf16 v[100:103], a[176:179], a[140:143], v[100:103]\n" \
"s_mov_b32 m0, s53\n" \
"s_nop 0\n" \
"v_cvt_pk_bf16_f32 v182, v50, v51\n" \
"v_mfma_f32_16x16x32_bf16 v[100:103], a[240:243], a[204:207], v[100:103]\n" \
"v_mov_b32_e32 v183, 0x3f80\n" \
"v_cvt_pk_bf16_f32 v118, v192, v193\n" \
"v_mfma_f32_16x16x32_bf16 v[52:55], a[176:179], a[128:131], v[52:55]\n" \
"v_cvt_pk_bf16_f32 v119, v194, v195\n" \
"v_cvt_pk_bf16_f32 v120, v208, v209\n" \
"v_mfma_f32_16x16x32_bf16 v[52:55], a[240:243], a[192:195], v[52:55]\n" \
"buffer_load_dwordx4 v20, s[80:83], s76 offen offset:0 sc1 lds\n" \
"v_mfma_f32_16x16x32_bf16 v[56:59], a[180:183], a[128:131], v[56:59]\n" \
"v_cvt_pk_bf16_f32 v121, v210, v211\n" \
"v_cvt_pk_bf16_f32 v122, v224, v225\n" \
"v_mfma_f32_16x16x32_bf16 v[56:59], a[244:247], a[192:195], v[56:59]\n" \
"v_cvt_pk_bf16_f32 v123, v226, v227\n" \
"v_cvt_pk_bf16_f32 v124, v240, v241\n" \
"v_cvt_pk_bf16_f32 v125, v242, v243\n" \
"v_mfma_f32_16x16x32_bf16 v[60:63], a[184:187], a[128:131], v[60:63]\n" \
"v_mfma_f32_16x16x32_bf16 v[192:195], v[188:191], v[184:187], 0\n" \
"buffer_load_dwordx4 v20, s[80:83], s76 offen offset:1024 sc1 lds\n" \
"v_mfma_f32_16x16x32_bf16 v[32:35], v[180:183], v[184:187], 0\n" \
"v_mfma_f32_16x16x32_bf16 v[208:211], v[204:207], v[200:203], 0\n" \
"v_mfma_f32_16x16x32_bf16 v[36:39], v[188:191], v[200:203], 0\n" \
"v_mfma_f32_16x16x32_bf16 v[224:227], v[220:223], v[216:219], 0\n" \
"v_mfma_f32_16x16x32_bf16 v[248:251], v[204:207], v[216:219], 0\n" \
"v_mfma_f32_16x16x32_bf16 v[240:243], v[236:239], v[232:235], 0\n" \
"v_mfma_f32_16x16x32_bf16 v[128:131], v[220:223], v[232:235], 0\n" \
"s_mov_b32 s64, 0xaaaaaaaa\n" \
"s_mov_b32 s65, 0xaaaaaaaa\n" \
"v_mfma_f32_16x16x32_bf16 v[60:63], a[248:251], a[192:195], v[60:63]\n" \
"s_mov_b32 s66, 0xfff0ffff\n" \
"s_mov_b32 s67, 0xf000ff00\n" \
"v_mfma_f32_16x16x32_bf16 v[64:67], a[188:191], a[128:131], v[64:67]\n" \
"s_mov_b32 s68, 0xffc0fffc\n" \
"s_mov_b32 s69, 0xc000fc00\n" \
"v_cndmask_b32_e64 v192, v192, v193, s[64:65]\n" \
"v_cndmask_b32_e64 v32, v32, v33, s[64:65]\n" \
"v_cndmask_b32_e64 v194, v194, v195, s[64:65]\n" \
"v_cndmask_b32_e64 v34, v34, v35, s[64:65]\n" \
"v_cndmask_b32_e64 v208, v208, v209, s[64:65]\n" \
"v_cndmask_b32_e64 v36, v36, v37, s[64:65]\n" \
"v_cndmask_b32_e64 v210, v210, v211, s[64:65]\n" \
"v_cndmask_b32_e64 v38, v38, v39, s[64:65]\n" \
"v_cndmask_b32_e64 v224, v224, v225, s[64:65]\n" \
"v_cndmask_b32_e64 v248, v248, v249, s[64:65]\n" \
"v_cndmask_b32_e64 v226, v226, v227, s[64:65]\n" \
"v_cndmask_b32_e64 v250, v250, v251, s[64:65]\n" \
"v_cndmask_b32_e64 v240, v240, v241, s[64:65]\n" \
"v_cndmask_b32_e64 v128, v128, v129, s[64:65]\n" \
"v_cndmask_b32_e64 v242, v242, v243, s[64:65]\n" \
"v_cndmask_b32_e64 v130, v130, v131, s[64:65]\n" \
"v_cndmask_b32_e64 v192, v32, v192, s[66:67]\n" \
"v_cndmask_b32_e64 v194, v34, v194, s[68:69]\n" \
"v_cndmask_b32_e64 v208, v36, v208, s[66:67]\n" \
"v_cndmask_b32_e64 v210, v38, v210, s[68:69]\n" \
"v_cndmask_b32_e64 v224, v248, v224, s[66:67]\n" \
"v_cndmask_b32_e64 v226, v250, v226, s[68:69]\n" \
"v_cndmask_b32_e64 v240, v128, v240, s[66:67]\n" \
"v_cndmask_b32_e64 v242, v130, v242, s[68:69]\n" \
"v_mfma_f32_16x16x32_bf16 v[64:67], a[252:255], a[192:195], v[64:67]\n" \
"v_max_f32_e32 v193, v192, v194\n" \
"v_max_f32_e32 v209, v208, v210\n" \
"v_max_f32_e32 v225, v224, v226\n" \
"v_max_f32_e32 v241, v240, v242\n" \
"v_mul_f32_e32 v148, s70, v148\n" \
"v_permlane32_swap_b32_e32 v193, v225\n" \
"v_permlane32_swap_b32_e32 v209, v241\n" \
"v_mul_f32_e32 v149, s70, v149\n" \
"v_mul_f32_e32 v156, s70, v156\n" \
"v_mul_f32_e32 v157, s70, v157\n" \
"v_permlane32_swap_b32_e32 v196, v228\n" \
"v_permlane32_swap_b32_e32 v212, v244\n" \
"v_max_f32_e32 v193, v193, v225\n" \
"v_max_f32_e32 v209, v209, v241\n" \
"v_mul_f32_e32 v150, s70, v150\n" \
"v_mul_f32_e32 v151, s70, v151\n" \
"v_cvt_pk_bf16_f32 v148, v148, v149\n" \
"v_mul_f32_e32 v158, s70, v158\n" \
"v_mul_f32_e32 v159, s70, v159\n" \
"v_cvt_pk_bf16_f32 v156, v156, v157\n" \
"v_permlane16_swap_b32_e32 v228, v244\n" \
"v_permlane16_swap_b32_e32 v193, v209\n" \
"v_mfma_f32_16x16x32_bf16 v[80:83], a[188:191], a[132:135], v[80:83]\n" \
"s_mov_b32 s72, 0x3eae1a93\n" \
"v_mul_f32_e32 v152, s70, v152\n" \
"v_mul_f32_e32 v153, s70, v153\n" \
"v_mul_f32_e32 v154, s70, v154\n" \
"v_mul_f32_e32 v155, s70, v155\n" \
"v_cvt_pk_bf16_f32 v149, v150, v151\n" \
"v_mul_f32_e32 v160, s70, v160\n" \
"v_mul_f32_e32 v161, s70, v161\n" \
"v_mul_f32_e32 v162, s70, v162\n" \
"v_mul_f32_e32 v163, s70, v163\n" \
"v_cvt_pk_bf16_f32 v157, v158, v159\n" \
"v_mul_f32_e32 v45, 0xbfb8aa3b, v228\n" \
"v_mfma_f32_16x16x16_bf16 v[48:51], v[182:183], v[46:47], 0\n" \
"v_mfma_f32_16x16x16_bf16 v[196:199], v[190:191], v[46:47], 0\n" \
"v_mfma_f32_16x16x16_bf16 v[212:215], v[206:207], v[46:47], 0\n" \
"v_max_f32_e64 v193, v193, v209\n" \
"v_cvt_pk_bf16_f32 v150, v152, v153\n" \
"v_cvt_pk_bf16_f32 v151, v154, v155\n" \
"v_cvt_pk_bf16_f32 v158, v160, v161\n" \
"v_cvt_pk_bf16_f32 v159, v162, v163\n" \
"v_mfma_f32_16x16x16_bf16 v[228:231], v[222:223], v[46:47], 0\n" \
"v_mul_f32_e32 v193, s72, v193\n" \
"v_mov_b32_e32 v209, v193\n" \
"ds_write_b128 v21, v[148:151] offset:24576\n" \
"v_mfma_f32_16x16x16_bf16 v[244:247], v[238:239], v[46:47], 0\n" \
"v_permlane16_swap_b32_e32 v193, v209\n" \
"s_mov_b32 s71, 0x3e0293ee\n" \
"v_mfma_f32_16x16x32_bf16 v[80:83], a[252:255], a[196:199], v[80:83]\n" \
"v_mov_b32_e32 v225, v193\n" \
"v_mov_b32_e32 v241, v209\n" \
"v_mul_f32_e32 v164, s70, v164\n" \
"v_mul_f32_e64 v165, s70, v165\n" \
"ds_write_b128 v21, v[156:159] offset:25600\n" \
"v_mul_f32_e32 v172, s70, v172\n" \
"v_mul_f32_e32 v173, s70, v173\n" \
"v_pk_mul_f32 v[84:85], v[84:85], s[70:71]\n" \
"v_pk_mul_f32 v[92:93], v[92:93], s[70:71]\n" \
"v_pk_mul_f32 v[100:101], v[100:101], s[70:71]\n" \
"v_pk_mul_f32 v[108:109], v[108:109], s[70:71]\n" \
"v_pk_mul_f32 v[52:53], v[52:53], s[70:71]\n" \
"v_pk_mul_f32 v[60:61], v[60:61], s[70:71]\n" \
"v_permlane32_swap_b32_e32 v193, v225\n" \
"v_permlane32_swap_b32_e32 v209, v241\n" \
"v_mul_f32_e32 v166, s70, v166\n" \
"v_mul_f32_e32 v167, s70, v167\n" \
"v_cvt_pk_bf16_f32 v164, v164, v165\n" \
"v_mul_f32_e32 v174, s70, v174\n" \
"v_mul_f32_e32 v175, s70, v175\n" \
"v_cvt_pk_bf16_f32 v172, v172, v173\n" \
"v_pk_mul_f32 v[86:87], v[86:87], s[70:71]\n" \
"v_cvt_pk_bf16_f32 v84, v84, v85\n" \
"v_pk_mul_f32 v[94:95], v[94:95], s[70:71]\n" \
"v_cvt_pk_bf16_f32 v92, v92, v93\n" \
"v_pk_mul_f32 v[102:103], v[102:103], s[70:71]\n" \
"v_cvt_pk_bf16_f32 v100, v100, v101\n" \
"v_pk_mul_f32 v[110:111], v[110:111], s[70:71]\n" \
"v_cvt_pk_bf16_f32 v108, v108, v109\n" \
"v_pk_mul_f32 v[54:55], v[54:55], s[70:71]\n" \
"v_cvt_pk_bf16_f32 v52, v52, v53\n" \
"v_pk_mul_f32 v[62:63], v[62:63], s[70:71]\n" \
"v_cvt_pk_bf16_f32 v60, v60, v61\n" \
"v_fma_f32 v192, v192, s72, -v193\n" \
"v_fma_f32 v194, v194, s72, -v193\n" \
"v_fma_f32 v208, v208, s72, -v209\n" \
"v_fma_f32 v210, v210, s72, -v209\n" \
"v_mul_f32_e32 v168, s70, v168\n" \
"v_mul_f32_e32 v169, s70, v169\n" \
"v_mul_f32_e32 v170, s70, v170\n" \
"v_mul_f32_e32 v171, s70, v171\n" \
"v_cvt_pk_bf16_f32 v165, v166, v167\n" \
"v_mul_f32_e32 v176, s70, v176\n" \
"v_mul_f32_e32 v177, s70, v177\n" \
"v_mul_f32_e32 v178, s70, v178\n" \
"v_mul_f32_e32 v179, s70, v179\n" \
"v_cvt_pk_bf16_f32 v173, v174, v175\n" \
"v_pk_mul_f32 v[88:89], v[88:89], s[70:71]\n" \
"v_pk_mul_f32 v[90:91], v[90:91], s[70:71]\n" \
"v_cvt_pk_bf16_f32 v85, v86, v87\n" \
"v_pk_mul_f32 v[96:97], v[96:97], s[70:71]\n" \
"v_pk_mul_f32 v[98:99], v[98:99], s[70:71]\n" \
"v_cvt_pk_bf16_f32 v93, v94, v95\n" \
"v_pk_mul_f32 v[104:105], v[104:105], s[70:71]\n" \
"v_pk_mul_f32 v[106:107], v[106:107], s[70:71]\n" \
"v_cvt_pk_bf16_f32 v101, v102, v103\n" \
"v_pk_mul_f32 v[112:113], v[112:113], s[70:71]\n" \
"v_pk_mul_f32 v[114:115], v[114:115], s[70:71]\n" \
"v_cvt_pk_bf16_f32 v109, v110, v111\n" \
"v_pk_mul_f32 v[56:57], v[56:57], s[70:71]\n" \
"v_pk_mul_f32 v[58:59], v[58:59], s[70:71]\n" \
"v_cvt_pk_bf16_f32 v53, v54, v55\n" \
"v_pk_mul_f32 v[64:65], v[64:65], s[70:71]\n" \
"v_pk_mul_f32 v[66:67], v[66:67], s[70:71]\n" \
"v_cvt_pk_bf16_f32 v61, v62, v63\n" \
"v_pk_mul_f32 v[80:81], v[80:81], s[70:71]\n" \
"v_pk_mul_f32 v[82:83], v[82:83], s[70:71]\n" \
"v_exp_f32_e32 v192, v192\n" \
"v_exp_f32_e32 v194, v194\n" \
"v_exp_f32_e32 v208, v208\n" \
"v_exp_f32_e32 v210, v210\n" \
"v_fma_f32 v224, v224, s72, -v225\n" \
"v_fma_f32 v226, v226, s72, -v225\n" \
"v_mfma_f32_16x16x32_bf16 v[76:79], a[184:187], a[132:135], v[76:79]\n" \
"v_cvt_pk_bf16_f32 v166, v168, v169\n" \
"v_cvt_pk_bf16_f32 v167, v170, v171\n" \
"v_cvt_pk_bf16_f32 v174, v176, v177\n" \
"v_cvt_pk_bf16_f32 v175, v178, v179\n" \
"v_cvt_pk_bf16_f32 v86, v88, v89\n" \
"v_cvt_pk_bf16_f32 v87, v90, v91\n" \
"v_cvt_pk_bf16_f32 v94, v96, v97\n" \
"v_cvt_pk_bf16_f32 v95, v98, v99\n" \
"v_cvt_pk_bf16_f32 v102, v104, v105\n" \
"v_cvt_pk_bf16_f32 v103, v106, v107\n" \
"v_cvt_pk_bf16_f32 v110, v112, v113\n" \
"v_cvt_pk_bf16_f32 v111, v114, v115\n" \
"v_cvt_pk_bf16_f32 v54, v56, v57\n" \
"v_cvt_pk_bf16_f32 v55, v58, v59\n" \
"v_cvt_pk_bf16_f32 v62, v64, v65\n" \
"v_cvt_pk_bf16_f32 v63, v66, v67\n" \
"v_exp_f32_e32 v224, v224\n" \
"v_exp_f32_e32 v226, v226\n" \
"v_fma_f32 v240, v240, s72, -v241\n" \
"v_fma_f32 v242, v242, s72, -v241\n" \
"ds_write_b128 v21, v[164:167] offset:28672\n" \
"v_cvt_pk_bf16_f32 v192, v192, v194\n" \
"v_cvt_pk_bf16_f32 v208, v208, v210\n" \
"v_exp_f32_e32 v240, v240\n" \
"v_exp_f32_e32 v242, v242\n" \
"v_cvt_pk_bf16_f32 v132, v48, v49\n" \
"v_cvt_pk_bf16_f32 v133, v50, v51\n" \
"v_cvt_pk_bf16_f32 v134, v196, v197\n" \
"v_cvt_pk_bf16_f32 v135, v198, v199\n" \
"ds_write_b128 v21, v[172:175] offset:29696\n" \
"v_cvt_pk_bf16_f32 v136, v212, v213\n" \
"v_cvt_pk_bf16_f32 v137, v214, v215\n" \
"v_perm_b32 v184, v192, v192, v40\n" \
"v_perm_b32 v185, v192, v192, v41\n" \
"v_perm_b32 v186, v192, v192, v42\n" \
"v_perm_b32 v187, v192, v192, v43\n" \
"v_perm_b32 v200, v208, v208, v40\n" \
"v_perm_b32 v201, v208, v208, v41\n" \
"v_perm_b32 v202, v208, v208, v42\n" \
"v_perm_b32 v203, v208, v208, v43\n" \
"ds_write_b128 v27, v[84:87] offset:8192\n" \
"v_cvt_pk_bf16_f32 v224, v224, v226\n" \
"v_cvt_pk_bf16_f32 v138, v228, v229\n" \
"v_cvt_pk_bf16_f32 v139, v230, v231\n" \
"v_mfma_f32_16x16x32_bf16 v[32:35], v[132:135], v[184:187], 0\n" \
"v_mfma_f32_16x16x32_bf16 v[36:39], v[134:137], v[200:203], 0\n" \
"v_perm_b32 v216, v224, v224, v40\n" \
"v_perm_b32 v217, v224, v224, v41\n" \
"ds_write_b128 v27, v[92:95] offset:9216\n" \
"v_perm_b32 v218, v224, v224, v42\n" \
"v_perm_b32 v219, v224, v224, v43\n" \
"v_cvt_pk_bf16_f32 v240, v240, v242\n" \
"v_mfma_f32_16x16x32_bf16 v[76:79], a[248:251], a[196:199], v[76:79]\n" \
"v_cvt_pk_bf16_f32 v140, v244, v245\n" \
"v_cvt_pk_bf16_f32 v141, v246, v247\n" \
"v_mfma_f32_16x16x32_bf16 v[248:251], v[136:139], v[216:219], 0\n" \
"v_perm_b32 v232, v240, v240, v40\n" \
"v_perm_b32 v233, v240, v240, v41\n" \
"ds_write_b128 v27, v[100:103] offset:12288\n" \
"v_perm_b32 v234, v240, v240, v42\n" \
"v_perm_b32 v235, v240, v240, v43\n" \
"v_pk_mul_f32 v[76:77], v[76:77], s[70:71]\n" \
"v_pk_mul_f32 v[78:79], v[78:79], s[70:71]\n" \
"v_cvt_pk_bf16_f32 v76, v76, v77\n" \
"v_cvt_pk_bf16_f32 v77, v78, v79\n" \
"v_cvt_pk_bf16_f32 v78, v80, v81\n" \
"v_cvt_pk_bf16_f32 v79, v82, v83\n" \
"v_mfma_f32_16x16x32_bf16 v[128:131], v[138:141], v[232:235], 0\n" \
"ds_write_b128 v27, v[108:111] offset:13312\n" \
"v_mfma_f32_16x16x32_bf16 v[72:75], a[180:183], a[132:135], v[72:75]\n" \
"v_cvt_pk_bf16_f32 v116, v252, v253\n" \
"v_cvt_pk_bf16_f32 v117, v254, v255\n" \
"v_mfma_f32_16x16x32_bf16 v[72:75], a[244:247], a[196:199], v[72:75]\n" \
"v_exp_f32_e64 v45, v45\n" \
"v_mfma_f32_16x16x32_bf16 v[192:195], v[116:119], v[184:187], 0\n" \
"ds_write_b128 v27, v[52:55] offset:0\n" \
"v_mfma_f32_16x16x32_bf16 v[208:211], v[118:121], v[200:203], 0\n" \
"v_mfma_f32_16x16x32_bf16 v[224:227], v[120:123], v[216:219], 0\n" \
"v_mfma_f32_16x16x32_bf16 v[240:243], v[122:125], v[232:235], 0\n" \
"ds_write_b128 v27, v[60:63] offset:1024\n" \
"v_mov_b32_dpp v34, v38 quad_perm:[0,1,2,3] row_mask:0x2 bank_mask:0xf\n" \
"v_pk_mul_f32 v[72:73], v[72:73], s[70:71]\n" \
"v_pk_mul_f32 v[74:75], v[74:75], s[70:71]\n" \
"v_mov_b32_dpp v34, v250 quad_perm:[0,1,2,3] row_mask:0x4 bank_mask:0xf\n" \
"v_add_f32_e64 v45, 1.0, v45\n" \
"v_mfma_f32_16x16x32_bf16 v[68:71], a[176:179], a[132:135], v[68:71]\n" \
"v_mov_b32_dpp v34, v130 quad_perm:[0,1,2,3] row_mask:0x8 bank_mask:0xf\n" \
"v_mov_b32_dpp v32, v36 quad_perm:[0,1,2,3] row_mask:0x2 bank_mask:0xf\n" \
"v_rcp_f32_e64 v45, v45\n" \
"ds_write_b128 v27, v[76:79] offset:5120\n" \
"v_mov_b32_dpp v33, v37 quad_perm:[0,1,2,3] row_mask:0x2 bank_mask:0xf\n" \
"v_rcp_f32_e64 v34, v34\n" \
"v_mov_b32_dpp v32, v248 quad_perm:[0,1,2,3] row_mask:0x4 bank_mask:0xf\n" \
"v_mov_b32_dpp v33, v249 quad_perm:[0,1,2,3] row_mask:0x4 bank_mask:0xf\n" \
"v_mul_f32_e64 v34, v45, v34\n" \
"v_mov_b32_dpp v32, v128 quad_perm:[0,1,2,3] row_mask:0x8 bank_mask:0xf\n" \
"v_mov_b32_dpp v33, v129 quad_perm:[0,1,2,3] row_mask:0x8 bank_mask:0xf\n" \
"v_mul_f32_e32 v32, v34, v32\n" \
"v_mul_f32_e32 v33, v34, v33\n" \
"v_mov_b32_e64 v38, v34\n" \
"v_mfma_f32_16x16x32_bf16 v[68:71], a[240:243], a[196:199], v[68:71]\n" \
"v_sub_f32_e32 v44, 1.0, v45\n" \
"v_permlane16_swap_b32_e32 v34, v38\n" \
"v_mov_b32_e32 v250, v34\n" \
"v_mov_b32_e32 v130, v38\n" \
"v_cvt_pk_bf16_f32 v32, v32, v33\n" \
"v_permlane32_swap_b32_e32 v34, v250\n" \
"v_permlane32_swap_b32_e32 v38, v130\n" \
"v_mul_f32_e64 v192, v34, v192\n" \
"v_pk_mul_f32 v[68:69], v[68:69], s[70:71]\n" \
"v_pk_mul_f32 v[70:71], v[70:71], s[70:71]\n" \
"v_cvt_pk_bf16_f32 v68, v68, v69\n" \
"v_cvt_pk_bf16_f32 v69, v70, v71\n" \
"v_cvt_pk_bf16_f32 v70, v72, v73\n" \
"v_cvt_pk_bf16_f32 v71, v74, v75\n" \
"ds_write_b128 v27, v[68:71] offset:4096\n" \
"v_mul_f32_e32 v193, v34, v193\n" \
"v_mul_f32_e32 v208, v38, v208\n" \
"v_mul_f32_e32 v209, v38, v209\n" \
"v_mul_f32_e32 v224, v250, v224\n" \
"v_mul_f32_e32 v225, v250, v225\n" \
"v_mul_f32_e32 v240, v130, v240\n" \
"v_mul_f32_e32 v241, v130, v241\n" \
"v_mul_f32_e32 v194, v34, v194\n" \
"v_mul_f32_e64 v195, v34, v195\n" \
"v_cvt_pk_bf16_f32 v192, v192, v193\n" \
"v_mul_f32_e32 v210, v38, v210\n" \
"v_mul_f32_e32 v211, v38, v211\n" \
"v_cvt_pk_bf16_f32 v208, v208, v209\n" \
"v_mul_f32_e32 v226, v250, v226\n" \
"v_mul_f32_e32 v227, v250, v227\n" \
"v_cvt_pk_bf16_f32 v224, v224, v225\n" \
"v_mul_f32_e32 v242, v130, v242\n" \
"v_mul_f32_e32 v243, v130, v243\n" \
"v_cvt_pk_bf16_f32 v240, v240, v241\n" \
"v_cvt_pk_bf16_f32 v193, v194, v195\n" \
"s_add_u32 s84, s48, 0x0\n" \
"s_nop 0\n" \
"v_cvt_pk_bf16_f32 v209, v210, v211\n" \
"s_add_u32 s85, s48, 0x24800\n" \
"v_cvt_pk_bf16_f32 v225, v226, v227\n" \
"s_add_u32 s86, s48, 0x49000\n" \
"v_cvt_pk_bf16_f32 v241, v242, v243\n" \
"s_add_u32 s87, s48, 0x6d800\n" \
"ds_write_b32 v26, v44\n" \
"buffer_store_dword v32, v25, s[80:83], s49 offen sc1\n" \
"buffer_store_dwordx2 v[192:193], v24, s[80:83], s84 offen sc1\n" \
"buffer_store_dwordx2 v[208:209], v24, s[80:83], s85 offen sc1\n" \
"buffer_store_dwordx2 v[224:225], v24, s[80:83], s86 offen sc1\n" \
"buffer_store_dwordx2 v[240:241], v24, s[80:83], s87 offen sc1\n" \
"s_branch L_end_%=\n" \
"L_poll61_%=:\n" \
"s_mov_b32 s84, 0\n" \
"L_pl61_%=:\n" \
"s_sleep 1\n" \
"v_bfe_u32 v44, v20, 2, 5\n" \
"buffer_load_dword v44, v44, s[80:83], s38 offen offset:128 sc1\n" \
"s_waitcnt vmcnt(0)\n" \
"v_cmp_ne_u32_e32 vcc, s62, v44\n" \
"s_add_u32 s84, s84, 1\n" \
"s_nop 3\n" \
"s_cbranch_vccz L_pollr61_%=\n" \
"s_cmp_gt_u32 s84, 0x400000\n" \
"s_cbranch_scc0 L_pl61_%=\n" \
"s_branch L_pollr61_%=\n" \
"L_poll161_%=:\n" \
"s_mov_b32 s84, 0\n" \
"L_pl161_%=:\n" \
"s_sleep 1\n" \
"v_bfe_u32 v44, v20, 2, 5\n" \
"buffer_load_dword v44, v44, s[80:83], s38 offen offset:0 sc1\n" \
"s_waitcnt vmcnt(0)\n" \
"v_cmp_ne_u32_e32 vcc, s62, v44\n" \
"s_add_u32 s84, s84, 1\n" \
"s_nop 3\n" \
"s_cbranch_vccz L_pollr161_%=\n" \
"s_cmp_gt_u32 s84, 0x400000\n" \
"s_cbranch_scc0 L_pl161_%=\n" \
"s_branch L_pollr161_%=\n" \
"L_end_%=:\n"
#define PROJ_VCLOB "v21","v24","v25","v28","v29","v32","v33","v34","v35","v36","v37","v38","v39","v40","v41","v42","v43","v44","v45","v46","v47","v48","v49","v50","v51","v52","v53","v54","v55","v56","v57","v58","v59","v60","v61","v62","v63","v64","v65","v66","v67","v68","v69","v70","v71","v72","v73","v74","v75","v76","v77","v78","v79","v80","v81","v82","v83","v84","v85","v86","v87","v88","v89","v90","v91","v92","v93","v94","v95","v96","v97","v98","v99","v100","v101","v102","v103","v104","v105","v106","v107","v108","v109","v110","v111","v112","v113","v114","v115","v116","v117","v118","v119","v120","v121","v122","v123","v124","v125","v126","v127","v128","v129","v130","v131","v132","v133","v134","v135","v136","v137","v138","v139","v140","v141","v142","v143","v144","v145","v146","v147","v148","v149","v150","v151","v152","v153","v154","v155","v156","v157","v158","v159","v160","v161","v162","v163","v164","v165","v166","v167","v168","v169","v170","v171","v172","v173","v174","v175","v176","v177","v178","v179","v180","v181","v182","v183","v184","v185","v186","v187","v188","v189","v190","v192","v193","v194","v195","v196","v197","v198","v199","v200","v201","v202","v203","v204","v205","v206","v207","v208","v209","v210","v211","v212","v213","v214","v215","v216","v217","v218","v219","v220","v221","v222","v223","v224","v225","v226","v227","v228","v229","v230","v231","v232","v233","v234","v235","v236","v237","v238","v239","v240","v241","v242","v243","v244","v245","v246","v247","v248","v249","v250","v251","v252","v253","v254","v255"
#define PROJ_SCLOB "s4","s5","s6","s7","s32","s33","s34","s35","s36","s37","s38","s39","s40","s41","s42","s43","s44","s45","s46","s47","s48","s49","s50","s51","s52","s53","s54","s55","s63","s64","s65","s66","s67","s68","s69","s70","s71","s72","s73","s74","s75","s76","s77","s78","s79","s80","s81","s82","s83","s84","s85","s86","s87","s88","s89","s90","s91","s92","s93","s94","s95","s96","s97","s98","s99","s100","s101"

typedef const u16* const __attribute__((address_space(4)))* kaptr;
__device__ __forceinline__ void proj_phase(kaptr& ka, char*& ws, const unsigned wid, u32x4 (&vp)[5], unsigned& vl, u32x4 (&sa)[5], u32x2 s24, u32x2& s28, unsigned tdum, u32x4& srdA, u32x2& sd, unsigned& tag
    ) {
    const unsigned b = blockIdx.x, h = (b & 7) * 4 + ((b >> 3) & 3), mt = b >> 5;
    const unsigned rb = (wid < 2u ? 15u - mt : mt) * 128u;
    const unsigned lane = threadIdx.x & 63u, wc = wid & 1u;
    u32x2 vAc, vB;
#pragma unroll
    for (int par = 0; par < 2; ++par) {
        const unsigned lr = lane >> 3, cc = (lane & 7u) ^ (4u * par + (lr >> 1)), r0 = rb + wc * 64u + lr;
        vAc[par] = r0 * (DC * 2) + cc * 16u;
        const unsigned br = lane >> 4, p16 = lane & 15u, sc = (((p16 >> 1) ^ (br | (par << 2))) << 1) | (p16 & 1u);
        vB[par] = (16u * wid + br) * (NBIG * 2) + h * (HD * 2) + sc * 16u;
    }
    vl = lane * 16u;
    u32x2 s30 = {wid * 8192u, 65536u + wid * 4096u};
    asm volatile(PROJ_ASM
                 : "={s62}"(tag), "={s[56:59]}"(srdA), "={s[60:61]}"(sd), "+{s[8:11]}"(sa[0]), "+{s[12:15]}"(sa[1]), "+{s[16:19]}"(sa[2]), "+{s[20:23]}"(sa[3]), "+{s[24:25]}"(s24), "+{s[28:29]}"(s28), "+{s[30:31]}"(s30),
                   "={v[0:3]}"(vp[0]), "={v[4:7]}"(vp[1]), "={v[8:11]}"(vp[2]), "={v[12:15]}"(vp[3]), "={v[16:19]}"(vp[4]), "+{v[22:23]}"(vAc), "+{v[26:27]}"(vB), "+{v191}"(tdum), "+{s[26:27]}"(ws), "+{s[0:1]}"(ka)
                 : "{s2}"(b), "{s3}"(wid), "{v20}"(vl)
                 : "memory", "m0", "vcc", "scc", PROJ_VCLOB, PROJ_SCLOB, ACLOB_LO, ACLOB_HI);
    sa[4] = (u32x4){s24[0], 0u, 0u, 0u};
}

namespace att {
#define DI __device__ __forceinline__
typedef __bf16 bfx8 __attribute__((ext_vector_type(8)));
DI unsigned rfl(unsigned x) { return __builtin_amdgcn_readfirstlane(x); }
DI f32x4 mfma(u32x4 a, u32x4 b, f32x4 c) { return __builtin_amdgcn_mfma_f32_16x16x32_bf16(__builtin_bit_cast(bfx8, a), __builtin_bit_cast(bfx8, b), c, 0, 0, 0); }
typedef __bf16 bfx2 __attribute__((ext_vector_type(2)));
typedef float f32x2 __attribute__((ext_vector_type(2)));
DI unsigned cvtpk2(float lo, float hi) { f32x2 v = {lo, hi}; bfx2 b = __builtin_convertvector(v, bfx2); return __builtin_bit_cast(unsigned, b); }
DI void att_exact(const u16* __restrict__ x, const u16* __restrict__ wq, char* __restrict__ ws, unsigned h, unsigned r0, float g1) {
    const unsigned lane = lane_id(), g = lane >> 4, c = lane & 15u;
    f32x4 o[8];
#pragma unroll
    for (int n = 0; n < 8; ++n) o[n] = (f32x4){0.f, 0.f, 0.f, 0.f};
#pragma unroll 1
    for (unsigned kk = 0; kk < (unsigned)DM; kk += 32u) {
        const unsigned k = kk + 8u * g;
        const u32x4 xf = *reinterpret_cast<const u32x4*>(x + (r0 + c) * DM + k);
#pragma unroll
        for (int n = 0; n < 8; ++n) {
            const u16* w = wq + k * NBIG + h * HD + n * 16 + c; u32x4 wf;
#pragma unroll
            for (int e = 0; e < 4; ++e) wf[e] = (unsigned)w[2 * e * NBIG] | ((unsigned)w[(2 * e + 1) * NBIG] << 16);
            o[n] = mfma(wf, xf, o[n]);
        }
    }
    u32x4 q[4];
#pragma unroll
    for (int p = 0; p < 4; ++p) {
        const f32x4 a = o[2 * p] * 0.12751743082f, b = o[2 * p + 1] * 0.12751743082f;
        q[p] = (u32x4){cvtpk2(a[0], a[1]), cvtpk2(a[2], a[3]), cvtpk2(b[0], b[1]), cvtpk2(b[2], b[3])};
    }
#pragma unroll
    for (int n = 0; n < 8; ++n) o[n] = (f32x4){0.f, 0.f, 0.f, 0.f};
    const __amdgpu_buffer_rsrc_t srd = __builtin_amdgcn_make_buffer_rsrc((void*)(ws + WS_K + (size_t)h * 1048576), (short)0, 1048576, 0x00020000);
    float m = -3.0e38f, l = 0.f;
#pragma unroll 1
    for (unsigned k0 = 0; k0 <= r0 + 15u; k0 += 32u) {
        const unsigned tb = (k0 >> 6) * 32768u + (k0 & 32u) * 256u;
        f32x4 s[2]; float mx = m;
#pragma unroll
        for (int kt = 0; kt < 2; ++kt) {
            s[kt] = (f32x4){0.f, 0.f, 0.f, 0.f};
#pragma unroll
            for (int p = 0; p < 4; ++p) s[kt] = mfma(__builtin_amdgcn_raw_buffer_load_b128(srd, lane * 16u + (kt * 4 + p) * 1024u, tb, 16), q[p], s[kt]);
#pragma unroll
            for (int e = 0; e < 4; ++e) { if (k0 + 16u * kt + 4u * g + e > r0 + c) s[kt][e] = -3.0e38f; mx = fmaxf(mx, s[kt][e]); }
        }
        mx = fmaxf(mx, __shfl_xor(mx, 16)); mx = fmaxf(mx, __shfl_xor(mx, 32));
        const float al = __builtin_amdgcn_exp2f(m - mx); m = mx;
        u32x4 pf;
#pragma unroll
        for (int e = 0; e < 4; ++e) pf[e] = cvtpk2(__builtin_amdgcn_exp2f(s[e >> 1][2 * (e & 1)] - m), __builtin_amdgcn_exp2f(s[e >> 1][2 * (e & 1) + 1] - m));
        l *= al;
#pragma unroll
        for (int e = 0; e < 4; ++e) l += __uint_as_float(pf[e] << 16) + __uint_as_float(pf[e] & 0xffff0000u);
#pragma unroll
        for (int n = 0; n < 8; ++n) o[n] = mfma(__builtin_amdgcn_raw_buffer_load_b128(srd, lane * 16u + 16384u + n * 1024u, tb, 16), pf, o[n] * al);
    }
    l += __shfl_xor(l, 16); l += __shfl_xor(l, 32);
    const float f = g1 / l;
    const __amdgpu_buffer_rsrc_t srdA = __builtin_amdgcn_make_buffer_rsrc((void*)(ws + WS_AF), (short)0, 128 * KS_TOT * 1024, 0x00020000);
#pragma unroll
    for (int p = 0; p < 4; ++p) {
        const f32x4 a = o[2 * p] * f, b = o[2 * p + 1] * f;
        __builtin_amdgcn_raw_buffer_store_b128((u32x4){cvtpk2(a[0], a[1]), cvtpk2(a[2], a[3]), cvtpk2(b[0], b[1]), cvtpk2(b[2], b[3])}, srdA, lane * 16u + p * 1024u, ((r0 >> 4) * KS_TOT + 18u + 4u * h) * 1024u, 16);
    }
}
}

#define ATT_ASM \
"s_branch L_ago_%=\n" \
".p2align 12\n" \
"L_ab:\n" \
".fill (L_ab + 0 - .) / 4, 4, 0xBF800000\n" \
"L_ap0:\n" \
"s_setpc_b64 s[88:89]\n" \
"L_ago_%=:\n" \
"s_mov_b32 s101, 0\n" \
"s_waitcnt vmcnt(15) lgkmcnt(0)\n" \
"s_barrier\n" \
"v_accvgpr_write_b32 a216, 0\n" \
"v_accvgpr_write_b32 a217, 0\n" \
"v_accvgpr_write_b32 a218, 0\n" \
"v_accvgpr_write_b32 a219, 0\n" \
"v_bfe_u32 v61, v20, 2, 5\n" \
"s_mov_b32 s96, s24\n" \
"v_mfma_f32_32x32x16_bf16 v[128:143], a[216:219], a[216:219], 0\n" \
"buffer_load_dword v57, v61, s[56:59], s96 offen sc1\n" \
"v_and_b32_e32 v63, 0x300, v20\n" \
"ds_read_b128 v[96:99], v63 offset:0\n" \
"v_add_u32_e32 v58, s22, v20\n" \
"ds_read_b128 a[0:3], v58 offset:0\n" \
"v_mfma_f32_32x32x16_bf16 v[144:159], a[216:219], a[216:219], 0\n" \
"ds_read_b128 a[16:19], v58 offset:8192\n" \
"v_add_u32_e32 v59, s23, v20\n" \
"ds_read_b128 a[32:35], v59 offset:0\n" \
"ds_read_b128 a[48:51], v59 offset:8192\n" \
"ds_read_b128 v[100:103], v63 offset:1024\n" \
"ds_read_b128 a[4:7], v58 offset:1024\n" \
"v_mfma_f32_32x32x16_bf16 v[160:175], a[216:219], a[216:219], 0\n" \
"ds_read_b128 a[20:23], v58 offset:9216\n" \
"ds_read_b128 a[36:39], v59 offset:1024\n" \
"ds_read_b128 a[52:55], v59 offset:9216\n" \
"s_sub_u32 s67, s12, 0xc000\n" \
"ds_read_b128 v[104:107], v63 offset:2048\n" \
"s_mov_b32 s65, s12\n" \
"v_mfma_f32_32x32x16_bf16 v[176:191], a[216:219], a[216:219], 0\n" \
"v_add_u32_e32 v55, s67, v20\n" \
"v_add_u32_e32 v52, 0xc000, v55\n" \
"s_xor_b32 m0, s65, 0x10000\n" \
"ds_read_b128 a[8:11], v58 offset:2048\n" \
"buffer_load_dwordx4 v52, s[8:11], 0 offen offset:0 sc1 lds\n" \
"v_mfma_f32_32x32x16_bf16 v[192:207], a[216:219], a[216:219], 0\n" \
"ds_read_b128 a[24:27], v58 offset:10240\n" \
"ds_read_b128 a[40:43], v59 offset:2048\n" \
"ds_read_b128 a[56:59], v59 offset:10240\n" \
"ds_read_b128 v[108:111], v63 offset:3072\n" \
"buffer_load_dwordx4 v52, s[8:11], 0 offen offset:1024 sc1 lds\n" \
"v_mfma_f32_32x32x16_bf16 v[208:223], a[216:219], a[216:219], 0\n" \
"ds_read_b128 a[12:15], v58 offset:3072\n" \
"ds_read_b128 a[28:31], v58 offset:11264\n" \
"ds_read_b128 a[44:47], v59 offset:3072\n" \
"ds_read_b128 a[60:63], v59 offset:11264\n" \
"s_waitcnt lgkmcnt(15)\n" \
"v_xor_b32_e32 v96, 0x80008000, v96\n" \
"v_mfma_f32_32x32x16_bf16 v[224:239], a[216:219], a[216:219], 0\n" \
"v_xor_b32_e32 v97, 0x80008000, v97\n" \
"v_xor_b32_e32 v98, 0x80008000, v98\n" \
"v_xor_b32_e32 v99, 0x80008000, v99\n" \
"ds_read_b128 a[160:163], v20 offset:0\n" \
"ds_read_b128 a[164:167], v20 offset:1024\n" \
"s_waitcnt lgkmcnt(15)\n" \
"v_xor_b32_e32 v100, 0x80008000, v100\n" \
"v_mfma_f32_32x32x16_bf16 v[240:255], a[216:219], a[216:219], 0\n" \
"v_xor_b32_e32 v101, 0x80008000, v101\n" \
"v_xor_b32_e32 v102, 0x80008000, v102\n" \
"v_xor_b32_e32 v103, 0x80008000, v103\n" \
"ds_read_b128 a[168:171], v20 offset:2048\n" \
"ds_read_b128 a[172:175], v20 offset:3072\n" \
"ds_read_b128 a[188:191], v20 offset:7168\n" \
"v_mfma_f32_16x16x32_bf16 a[192:195], a[216:219], a[216:219], 0\n" \
"ds_read_b128 a[184:187], v20 offset:6144\n" \
"s_waitcnt lgkmcnt(15)\n" \
"v_xor_b32_e32 v104, 0x80008000, v104\n" \
"v_mfma_f32_16x16x32_bf16 a[196:199], a[216:219], a[216:219], 0\n" \
"v_xor_b32_e32 v105, 0x80008000, v105\n" \
"v_xor_b32_e32 v106, 0x80008000, v106\n" \
"v_mfma_f32_16x16x32_bf16 v[32:35], v[96:99], a[0:3], 0\n" \
"v_xor_b32_e32 v107, 0x80008000, v107\n" \
"ds_read_b128 a[180:183], v20 offset:5120\n" \
"v_mfma_f32_16x16x32_bf16 v[36:39], v[96:99], a[16:19], 0\n" \
"ds_read_b128 a[176:179], v20 offset:4096\n" \
"s_waitcnt lgkmcnt(12)\n" \
"v_xor_b32_e32 v108, 0x80008000, v108\n" \
"v_mfma_f32_16x16x32_bf16 v[40:43], v[96:99], a[32:35], 0\n" \
"v_xor_b32_e32 v109, 0x80008000, v109\n" \
"v_xor_b32_e32 v110, 0x80008000, v110\n" \
"v_mfma_f32_16x16x32_bf16 v[44:47], v[96:99], a[48:51], 0\n" \
"v_xor_b32_e32 v111, 0x80008000, v111\n" \
"v_add_u32_e32 v60, s15, v20\n" \
"v_mfma_f32_16x16x32_bf16 v[32:35], v[100:103], a[4:7], v[32:35]\n" \
"v_add_u32_e32 v60, 0x10000, v60\n" \
"ds_read_b128 a[128:131], v20 offset:8192\n" \
"v_mfma_f32_16x16x32_bf16 v[36:39], v[100:103], a[20:23], v[36:39]\n" \
"ds_read_b128 a[144:147], v20 offset:12288\n" \
"ds_read_b128 a[132:135], v20 offset:9216\n" \
"v_mfma_f32_16x16x32_bf16 v[40:43], v[100:103], a[36:39], v[40:43]\n" \
"ds_read_b128 a[148:151], v20 offset:13312\n" \
"ds_read_b128 a[136:139], v20 offset:10240\n" \
"v_mfma_f32_16x16x32_bf16 v[44:47], v[100:103], a[52:55], v[44:47]\n" \
"ds_read_b128 a[152:155], v20 offset:14336\n" \
"ds_read_b128 a[140:143], v20 offset:11264\n" \
"v_mfma_f32_16x16x32_bf16 v[32:35], v[104:107], a[8:11], v[32:35]\n" \
"ds_read_b128 a[156:159], v20 offset:15360\n" \
"ds_read_b128 a[64:67], v60 offset:16384\n" \
"v_mfma_f32_16x16x32_bf16 v[36:39], v[104:107], a[24:27], v[36:39]\n" \
"ds_read_b128 a[68:71], v60 offset:17408\n" \
"ds_read_b128 a[72:75], v60 offset:18432\n" \
"v_mfma_f32_16x16x32_bf16 v[40:43], v[104:107], a[40:43], v[40:43]\n" \
"ds_read_b128 a[76:79], v60 offset:19456\n" \
"ds_read_b128 a[80:83], v60 offset:24576\n" \
"v_mfma_f32_16x16x32_bf16 v[44:47], v[104:107], a[56:59], v[44:47]\n" \
"ds_read_b128 a[84:87], v60 offset:25600\n" \
"s_waitcnt lgkmcnt(15)\n" \
"v_mfma_f32_16x16x32_bf16 v[32:35], v[108:111], a[12:15], v[32:35]\n" \
"ds_read_b128 a[88:91], v60 offset:26624\n" \
"ds_read_b128 a[92:95], v60 offset:27648\n" \
"v_mfma_f32_16x16x32_bf16 v[36:39], v[108:111], a[28:31], v[36:39]\n" \
"ds_read_b128 a[96:99], v60 offset:0\n" \
"ds_read_b128 a[100:103], v60 offset:1024\n" \
"v_mfma_f32_16x16x32_bf16 v[40:43], v[108:111], a[44:47], v[40:43]\n" \
"ds_read_b128 a[104:107], v60 offset:2048\n" \
"ds_read_b128 a[108:111], v60 offset:3072\n" \
"v_mfma_f32_16x16x32_bf16 v[44:47], v[108:111], a[60:63], v[44:47]\n" \
"ds_read_b128 a[112:115], v60 offset:8192\n" \
"ds_read_b128 a[116:119], v60 offset:9216\n" \
"v_mfma_f32_16x16x32_bf16 v[64:67], a[160:163], a[0:3], v[32:35]\n" \
"ds_read_b128 a[120:123], v60 offset:10240\n" \
"ds_read_b128 a[124:127], v60 offset:11264\n" \
"v_mfma_f32_16x16x32_bf16 v[64:67], a[164:167], a[4:7], v[64:67]\n" \
"v_bfe_u32 v26, v20, 8, 2\n" \
"s_mov_b32 m0, s20\n" \
"v_mfma_f32_16x16x32_bf16 v[64:67], a[168:171], a[8:11], v[64:67]\n" \
"v_bfe_u32 v25, v20, 5, 3\n" \
"v_lshlrev_b32_e32 v26, 1, v26\n" \
"v_mfma_f32_16x16x32_bf16 v[64:67], a[172:175], a[12:15], v[64:67]\n" \
"global_load_lds_dword v[16:17], off\n" \
"v_mfma_f32_16x16x32_bf16 v[68:71], a[188:191], a[12:15], v[32:35]\n" \
"v_sub_u32_e32 v25, v25, v26\n" \
"v_and_b32_e32 v26, 16, v20\n" \
"v_mfma_f32_16x16x32_bf16 v[68:71], a[184:187], a[8:11], v[68:71]\n" \
"v_mov_b32_e32 v27, 0x3f80\n" \
"v_lshlrev_b32_e32 v26, v26, v27\n" \
"v_mfma_f32_16x16x32_bf16 v[68:71], a[180:183], a[4:7], v[68:71]\n" \
"v_mov_b32_e32 v54, 0x3f803f80\n" \
"s_waitcnt lgkmcnt(15)\n" \
"v_mfma_f32_16x16x32_bf16 v[68:71], a[176:179], a[0:3], v[68:71]\n" \
"s_mov_b32 s84, 0x33333333\n" \
"s_mov_b32 s85, 0xcccccccc\n" \
"v_mfma_f32_16x16x32_bf16 v[76:79], a[176:179], a[16:19], v[36:39]\n" \
"s_mov_b32 s86, 0x11111111\n" \
"s_mov_b32 s87, 0x11111111\n" \
"v_mfma_f32_16x16x32_bf16 v[76:79], a[180:183], a[20:23], v[76:79]\n" \
"s_mov_b32 s88, 0x44444444\n" \
"s_mov_b32 s89, 0x44444444\n" \
"v_mfma_f32_16x16x32_bf16 v[76:79], a[184:187], a[24:27], v[76:79]\n" \
"v_exp_f32_e32 v70, v70\n" \
"v_mfma_f32_16x16x32_bf16 v[76:79], a[188:191], a[28:31], v[76:79]\n" \
"v_exp_f32_e32 v71, v71\n" \
"v_mfma_f32_16x16x32_bf16 v[72:75], a[172:175], a[28:31], v[36:39]\n" \
"v_exp_f32_e32 v68, v68\n" \
"v_mfma_f32_16x16x32_bf16 v[72:75], a[168:171], a[24:27], v[72:75]\n" \
"v_exp_f32_e32 v69, v69\n" \
"v_mfma_f32_16x16x32_bf16 v[72:75], a[164:167], a[20:23], v[72:75]\n" \
"v_bfe_i32 v21, v20, 4, 1\n" \
"v_cvt_pk_bf16_f32 v71, v70, v71\n" \
"v_mfma_f32_16x16x32_bf16 v[72:75], a[160:163], a[16:19], v[72:75]\n" \
"v_exp_f32_e32 v66, v66\n" \
"v_mfma_f32_16x16x32_bf16 v[80:83], a[160:163], a[32:35], v[40:43]\n" \
"v_exp_f32_e32 v67, v67\n" \
"v_mfma_f32_16x16x32_bf16 v[80:83], a[164:167], a[36:39], v[80:83]\n" \
"v_exp_f32_e32 v64, v64\n" \
"v_mfma_f32_16x16x32_bf16 v[80:83], a[168:171], a[40:43], v[80:83]\n" \
"v_exp_f32_e32 v65, v65\n" \
"v_mfma_f32_16x16x32_bf16 v[80:83], a[172:175], a[44:47], v[80:83]\n" \
"v_exp_f32_e32 v72, v72\n" \
"v_mfma_f32_16x16x32_bf16 v[84:87], a[188:191], a[44:47], v[40:43]\n" \
"s_waitcnt vmcnt(3)\n" \
"v_cmp_ne_u32_e32 vcc, s62, v57\n" \
"v_mfma_f32_16x16x32_bf16 v[84:87], a[184:187], a[40:43], v[84:87]\n" \
"v_exp_f32_e32 v73, v73\n" \
"v_mfma_f32_16x16x32_bf16 v[84:87], a[180:183], a[36:39], v[84:87]\n" \
"v_exp_f32_e32 v74, v74\n" \
"v_mfma_f32_16x16x32_bf16 v[84:87], a[176:179], a[32:35], v[84:87]\n" \
"s_cbranch_vccnz L_skb_%=\n" \
"L_rkb_%=:\n" \
"v_cmp_eq_u32_e32 vcc, 0, v25\n" \
"v_mfma_f32_16x16x32_bf16 v[92:95], a[176:179], a[48:51], v[44:47]\n" \
"v_exp_f32_e32 v75, v75\n" \
"v_mfma_f32_16x16x32_bf16 v[92:95], a[180:183], a[52:55], v[92:95]\n" \
"v_cndmask_b32_e32 v28, 0, v26, vcc\n" \
"v_cvt_pk_bf16_f32 v72, v72, v73\n" \
"v_mfma_f32_16x16x32_bf16 v[92:95], a[184:187], a[56:59], v[92:95]\n" \
"v_exp_f32_e32 v76, v76\n" \
"v_mfma_f32_16x16x32_bf16 v[92:95], a[188:191], a[60:63], v[92:95]\n" \
"v_exp_f32_e32 v77, v77\n" \
"v_mfma_f32_16x16x32_bf16 v[88:91], a[172:175], a[60:63], v[44:47]\n" \
"v_exp_f32_e32 v78, v78\n" \
"v_mfma_f32_16x16x32_bf16 v[88:91], a[168:171], a[56:59], v[88:91]\n" \
"v_exp_f32_e32 v79, v79\n" \
"v_mfma_f32_16x16x32_bf16 v[88:91], a[164:167], a[52:55], v[88:91]\n" \
"v_cndmask_b32_e64 v22, 0, v54, s[84:85]\n" \
"v_cndmask_b32_e64 v23, 0, v54, s[86:87]\n" \
"v_mfma_f32_16x16x32_bf16 v[88:91], a[160:163], a[48:51], v[88:91]\n" \
"v_cndmask_b32_e64 v24, 0, v54, s[88:89]\n" \
"v_and_b32_e32 v21, 0xaaaaaaaa, v21\n" \
"s_waitcnt vmcnt(4) lgkmcnt(0)\n" \
"s_barrier\n" \
"v_cmp_eq_u32_e32 vcc, 1, v25\n" \
"v_cvt_pk_bf16_f32 v70, v68, v69\n" \
"v_cvt_pk_bf16_f32 v73, v74, v75\n" \
"v_mov_b32_e32 v62, 0\n" \
"v_accvgpr_write_b32 a208, 0\n" \
"v_accvgpr_write_b32 a209, 0\n" \
"v_accvgpr_write_b32 a210, 0\n" \
"v_accvgpr_write_b32 a211, 0\n" \
"v_accvgpr_write_b32 a200, v22\n" \
"v_accvgpr_write_b32 a201, v22\n" \
"v_accvgpr_write_b32 a202, v22\n" \
"v_accvgpr_write_b32 a203, v22\n" \
"v_accvgpr_write_b32 a204, v23\n" \
"v_accvgpr_write_b32 a205, v23\n" \
"v_accvgpr_write_b32 a206, v23\n" \
"v_accvgpr_write_b32 a207, v23\n" \
"v_accvgpr_write_b32 a212, v24\n" \
"v_accvgpr_write_b32 a213, v24\n" \
"v_accvgpr_write_b32 a214, v24\n" \
"v_accvgpr_write_b32 a215, v24\n" \
"v_or_b32_e32 v21, 0x44444444, v21\n" \
"s_mov_b32 s64, 0x8000\n" \
"v_add_u32_e32 v50, 0x6000, v55\n" \
"v_add_u32_e32 v51, 0xa000, v55\n" \
"v_add_u32_e32 v53, 0x10000, v55\n" \
"v_add_u32_e32 v48, 0xc000, v20\n" \
"v_add_u32_e32 v49, s17, v20\n" \
"v_cndmask_b32_e32 v29, 0, v26, vcc\n" \
"v_accvgpr_write_b32 a220, v96\n" \
"v_accvgpr_write_b32 a221, v97\n" \
"v_accvgpr_write_b32 a222, v98\n" \
"v_accvgpr_write_b32 a223, v99\n" \
"v_accvgpr_write_b32 a224, v100\n" \
"v_accvgpr_write_b32 a225, v101\n" \
"v_accvgpr_write_b32 a226, v102\n" \
"v_accvgpr_write_b32 a227, v103\n" \
"v_accvgpr_write_b32 a228, v104\n" \
"v_accvgpr_write_b32 a229, v105\n" \
"v_accvgpr_write_b32 a230, v106\n" \
"v_accvgpr_write_b32 a231, v107\n" \
"v_accvgpr_write_b32 a232, v108\n" \
"v_accvgpr_write_b32 a233, v109\n" \
"v_accvgpr_write_b32 a234, v110\n" \
"v_accvgpr_write_b32 a235, v111\n" \
"v_cvt_pk_bf16_f32 v69, v66, v67\n" \
"v_cvt_pk_bf16_f32 v68, v64, v65\n" \
"v_cvt_pk_bf16_f32 v74, v76, v77\n" \
"v_cvt_pk_bf16_f32 v75, v78, v79\n" \
"s_sub_u32 s66, s18, 1\n" \
"s_cbranch_scc1 L_tr_%=\n" \
".fill (L_ab + 1856 - .) / 4, 4, 0xBF800000\n" \
"L_1_%=:\n" \
"v_mfma_f32_16x16x32_bf16 v[96:99], a[128:131], a[0:3], v[32:35]\n" \
"s_xor_b32 m0, s65, 0x12000\n" \
"ds_read_b128 a[160:163], v49 offset:0\n" \
"v_mfma_f32_16x16x32_bf16 v[96:99], a[132:135], a[4:7], v[96:99]\n" \
"buffer_load_dwordx4 v50, s[8:11], s64 offen offset:0 sc1 lds\n" \
"v_mfma_f32_16x16x32_bf16 v[96:99], a[136:139], a[8:11], v[96:99]\n" \
"ds_read_b128 a[164:167], v49 offset:1024\n" \
"ds_read_b128 a[168:171], v49 offset:2048\n" \
"v_mfma_f32_16x16x32_bf16 v[96:99], a[140:143], a[12:15], v[96:99]\n" \
"ds_read_b128 a[172:175], v49 offset:3072\n" \
"ds_read_b128 a[176:179], v49 offset:4096\n" \
"v_mfma_f32_16x16x32_bf16 v[100:103], a[156:159], a[12:15], v[32:35]\n" \
"ds_read_b128 a[180:183], v49 offset:5120\n" \
"ds_read_b128 a[184:187], v49 offset:6144\n" \
"v_mfma_f32_16x16x32_bf16 v[100:103], a[152:155], a[8:11], v[100:103]\n" \
"v_exp_f32_e64 v86, v86\n" \
"v_mfma_f32_16x16x32_bf16 v[100:103], a[148:151], a[4:7], v[100:103]\n" \
"v_exp_f32_e64 v87, v87\n" \
"v_mfma_f32_16x16x32_bf16 v[100:103], a[144:147], a[0:3], v[100:103]\n" \
"ds_read_b128 a[188:191], v49 offset:7168\n" \
"v_cvt_pk_bf16_f32 v87, v86, v87\n" \
"v_mfma_f32_16x16x32_bf16 v[108:111], a[144:147], a[16:19], v[36:39]\n" \
"v_exp_f32_e64 v84, v84\n" \
"v_mfma_f32_16x16x32_bf16 v[108:111], a[148:151], a[20:23], v[108:111]\n" \
"v_exp_f32_e64 v85, v85\n" \
"v_mfma_f32_16x16x32_bf16 v[108:111], a[152:155], a[24:27], v[108:111]\n" \
"v_exp_f32_e64 v82, v82\n" \
"v_mfma_f32_16x16x32_bf16 v[108:111], a[156:159], a[28:31], v[108:111]\n" \
"v_exp_f32_e64 v83, v83\n" \
"v_mfma_f32_16x16x32_bf16 v[104:107], a[140:143], a[28:31], v[36:39]\n" \
"v_exp_f32_e64 v80, v80\n" \
"v_mfma_f32_16x16x32_bf16 v[104:107], a[136:139], a[24:27], v[104:107]\n" \
"v_exp_f32_e64 v81, v81\n" \
"v_mfma_f32_16x16x32_bf16 v[104:107], a[132:135], a[20:23], v[104:107]\n" \
"v_exp_f32_e64 v88, v88\n" \
"v_mfma_f32_16x16x32_bf16 v[104:107], a[128:131], a[16:19], v[104:107]\n" \
"buffer_load_dwordx4 v50, s[8:11], s64 offen offset:1024 sc1 lds\n" \
"v_mfma_f32_16x16x32_bf16 v[112:115], a[128:131], a[32:35], v[40:43]\n" \
"s_xor_b32 m0, s65, 0x6000\n" \
"v_cvt_pk_bf16_f32 v86, v84, v85\n" \
"v_mfma_f32_16x16x32_bf16 v[112:115], a[132:135], a[36:39], v[112:115]\n" \
"v_cvt_pk_bf16_f32 v85, v82, v83\n" \
"v_cvt_pk_bf16_f32 v84, v80, v81\n" \
"v_mfma_f32_16x16x32_bf16 v[112:115], a[136:139], a[40:43], v[112:115]\n" \
"v_exp_f32_e64 v89, v89\n" \
"v_mfma_f32_16x16x32_bf16 v[112:115], a[140:143], a[44:47], v[112:115]\n" \
"v_exp_f32_e64 v90, v90\n" \
"v_mfma_f32_16x16x32_bf16 v[116:119], a[156:159], a[44:47], v[40:43]\n" \
"v_exp_f32_e64 v91, v91\n" \
"v_mfma_f32_16x16x32_bf16 v[116:119], a[152:155], a[40:43], v[116:119]\n" \
"v_exp_f32_e64 v92, v92\n" \
"v_mfma_f32_16x16x32_bf16 v[116:119], a[148:151], a[36:39], v[116:119]\n" \
"v_exp_f32_e64 v93, v93\n" \
"v_mfma_f32_16x16x32_bf16 v[116:119], a[144:147], a[32:35], v[116:119]\n" \
"v_exp_f32_e64 v94, v94\n" \
"v_mfma_f32_16x16x32_bf16 v[124:127], a[144:147], a[48:51], v[44:47]\n" \
"v_cvt_pk_bf16_f32 v88, v88, v89\n" \
"v_cvt_pk_bf16_f32 v89, v90, v91\n" \
"v_mfma_f32_16x16x32_bf16 v[124:127], a[148:151], a[52:55], v[124:127]\n" \
"v_exp_f32_e64 v95, v95\n" \
"v_mfma_f32_16x16x32_bf16 v[124:127], a[152:155], a[56:59], v[124:127]\n" \
"v_cvt_pk_bf16_f32 v90, v92, v93\n" \
"v_cvt_pk_bf16_f32 v91, v94, v95\n" \
"v_mfma_f32_16x16x32_bf16 v[124:127], a[156:159], a[60:63], v[124:127]\n" \
"ds_read_b128 a[144:147], v48 offset:4096\n" \
"ds_read_b128 a[148:151], v48 offset:5120\n" \
"v_mfma_f32_16x16x32_bf16 v[120:123], a[140:143], a[60:63], v[44:47]\n" \
"ds_read_b128 a[152:155], v48 offset:6144\n" \
"ds_read_b128 a[140:143], v48 offset:3072\n" \
"v_mfma_f32_16x16x32_bf16 v[120:123], a[136:139], a[56:59], v[120:123]\n" \
"ds_read_b128 a[136:139], v48 offset:2048\n" \
"ds_read_b128 a[156:159], v48 offset:7168\n" \
"v_mfma_f32_16x16x32_bf16 v[120:123], a[132:135], a[52:55], v[120:123]\n" \
"buffer_load_dwordx4 v51, s[8:11], s64 offen offset:0 sc1 lds\n" \
"v_mfma_f32_16x16x32_bf16 v[120:123], a[128:131], a[48:51], v[120:123]\n" \
"ds_read_b128 a[128:131], v48 offset:0\n" \
"s_waitcnt lgkmcnt(7)\n" \
"v_mfma_f32_16x16x32_bf16 v[128:131], a[160:163], v[68:71], v[128:131]\n" \
"v_exp_f32_e32 v102, v102\n" \
"v_mfma_f32_16x16x32_bf16 v[160:163], a[160:163], v[72:75], v[160:163]\n" \
"v_exp_f32_e64 v103, v103\n" \
"v_mfma_f32_16x16x32_bf16 v[132:135], a[164:167], v[68:71], v[132:135]\n" \
"ds_read_b128 a[132:135], v48 offset:1024\n" \
"v_cvt_pk_bf16_f32 v103, v102, v103\n" \
"v_mfma_f32_16x16x32_bf16 v[164:167], a[164:167], v[72:75], v[164:167]\n" \
"v_exp_f32_e64 v100, v100\n" \
"v_mfma_f32_16x16x32_bf16 v[136:139], a[168:171], v[68:71], v[136:139]\n" \
"v_exp_f32_e64 v101, v101\n" \
"v_mfma_f32_16x16x32_bf16 v[168:171], a[168:171], v[72:75], v[168:171]\n" \
"v_exp_f32_e64 v98, v98\n" \
"v_mfma_f32_16x16x32_bf16 v[140:143], a[172:175], v[68:71], v[140:143]\n" \
"v_exp_f32_e64 v99, v99\n" \
"v_mfma_f32_16x16x32_bf16 v[172:175], a[172:175], v[72:75], v[172:175]\n" \
"v_exp_f32_e64 v96, v96\n" \
"v_mfma_f32_16x16x32_bf16 v[144:147], a[176:179], v[68:71], v[144:147]\n" \
"v_exp_f32_e64 v97, v97\n" \
"v_mfma_f32_16x16x32_bf16 v[176:179], a[176:179], v[72:75], v[176:179]\n" \
"v_exp_f32_e64 v104, v104\n" \
"v_mfma_f32_16x16x32_bf16 v[148:151], a[180:183], v[68:71], v[148:151]\n" \
"v_exp_f32_e64 v105, v105\n" \
"v_mfma_f32_16x16x32_bf16 v[180:183], a[180:183], v[72:75], v[180:183]\n" \
"v_cvt_pk_bf16_f32 v102, v100, v101\n" \
"v_cvt_pk_bf16_f32 v101, v98, v99\n" \
"v_mfma_f32_16x16x32_bf16 v[152:155], a[184:187], v[68:71], v[152:155]\n" \
"v_cvt_pk_bf16_f32 v100, v96, v97\n" \
"v_cvt_pk_bf16_f32 v104, v104, v105\n" \
"v_mfma_f32_16x16x32_bf16 v[184:187], a[184:187], v[72:75], v[184:187]\n" \
"buffer_load_dwordx4 v51, s[8:11], s64 offen offset:1024 sc1 lds\n" \
"v_mfma_f32_16x16x32_bf16 v[156:159], a[188:191], v[68:71], v[156:159]\n" \
"v_exp_f32_e64 v106, v106\n" \
"v_mfma_f32_16x16x32_bf16 v[188:191], a[188:191], v[72:75], v[188:191]\n" \
"v_exp_f32_e64 v107, v107\n" \
"v_smfmac_f32_16x16x64_bf16 a[192:195], a[200:203], v[68:75], v21\n" \
"v_exp_f32_e64 v108, v108\n" \
"v_mfma_f32_16x16x32_bf16 v[220:223], a[188:191], v[84:87], v[220:223]\n" \
"v_exp_f32_e64 v109, v109\n" \
"v_mfma_f32_16x16x32_bf16 v[252:255], a[188:191], v[88:91], v[252:255]\n" \
"v_exp_f32_e64 v110, v110\n" \
"v_mfma_f32_16x16x32_bf16 v[216:219], a[184:187], v[84:87], v[216:219]\n" \
"v_exp_f32_e64 v111, v111\n" \
"v_mfma_f32_16x16x32_bf16 v[248:251], a[184:187], v[88:91], v[248:251]\n" \
"v_exp_f32_e64 v118, v118\n" \
"v_mfma_f32_16x16x32_bf16 v[212:215], a[180:183], v[84:87], v[212:215]\n" \
"v_exp_f32_e64 v119, v119\n" \
"v_mfma_f32_16x16x32_bf16 v[244:247], a[180:183], v[88:91], v[244:247]\n" \
"v_cvt_pk_bf16_f32 v105, v106, v107\n" \
"v_cvt_pk_bf16_f32 v106, v108, v109\n" \
"v_mfma_f32_16x16x32_bf16 v[208:211], a[176:179], v[84:87], v[208:211]\n" \
"v_cvt_pk_bf16_f32 v107, v110, v111\n" \
"v_cvt_pk_bf16_f32 v119, v118, v119\n" \
"v_mfma_f32_16x16x32_bf16 v[240:243], a[176:179], v[88:91], v[240:243]\n" \
"v_exp_f32_e64 v116, v116\n" \
"v_mfma_f32_16x16x32_bf16 v[204:207], a[172:175], v[84:87], v[204:207]\n" \
"v_exp_f32_e64 v117, v117\n" \
"v_mfma_f32_16x16x32_bf16 v[236:239], a[172:175], v[88:91], v[236:239]\n" \
"v_exp_f32_e64 v114, v114\n" \
"v_mfma_f32_16x16x32_bf16 v[200:203], a[168:171], v[84:87], v[200:203]\n" \
"v_exp_f32_e64 v115, v115\n" \
"v_mfma_f32_16x16x32_bf16 v[232:235], a[168:171], v[88:91], v[232:235]\n" \
"v_exp_f32_e64 v112, v112\n" \
"v_mfma_f32_16x16x32_bf16 v[196:199], a[164:167], v[84:87], v[196:199]\n" \
"v_exp_f32_e64 v113, v113\n" \
"v_mfma_f32_16x16x32_bf16 v[228:231], a[164:167], v[88:91], v[228:231]\n" \
"v_exp_f32_e64 v120, v120\n" \
"v_mfma_f32_16x16x32_bf16 v[192:195], a[160:163], v[84:87], v[192:195]\n" \
"v_exp_f32_e64 v121, v121\n" \
"v_mfma_f32_16x16x32_bf16 v[224:227], a[160:163], v[88:91], v[224:227]\n" \
"v_cvt_pk_bf16_f32 v118, v116, v117\n" \
"v_cvt_pk_bf16_f32 v117, v114, v115\n" \
"v_smfmac_f32_16x16x64_bf16 a[196:199], a[200:203], v[84:91], v21\n" \
"v_cvt_pk_bf16_f32 v116, v112, v113\n" \
"v_cvt_pk_bf16_f32 v120, v120, v121\n" \
"s_waitcnt vmcnt(4) lgkmcnt(0)\n" \
"s_barrier\n" \
"v_mfma_f32_16x16x32_bf16 v[64:67], a[128:131], a[0:3], v[32:35]\n" \
"s_xor_b32 m0, s65, 0x14000\n" \
"ds_read_b128 a[160:163], v49 offset:8192\n" \
"v_mfma_f32_16x16x32_bf16 v[64:67], a[132:135], a[4:7], v[64:67]\n" \
"buffer_load_dwordx4 v52, s[8:11], s64 offen offset:0 sc1 lds\n" \
"v_mfma_f32_16x16x32_bf16 v[64:67], a[136:139], a[8:11], v[64:67]\n" \
"ds_read_b128 a[164:167], v49 offset:9216\n" \
"ds_read_b128 a[168:171], v49 offset:10240\n" \
"v_mfma_f32_16x16x32_bf16 v[64:67], a[140:143], a[12:15], v[64:67]\n" \
"ds_read_b128 a[172:175], v49 offset:11264\n" \
"ds_read_b128 a[176:179], v49 offset:12288\n" \
"v_mfma_f32_16x16x32_bf16 v[68:71], a[156:159], a[12:15], v[32:35]\n" \
"ds_read_b128 a[180:183], v49 offset:13312\n" \
"ds_read_b128 a[184:187], v49 offset:14336\n" \
"v_mfma_f32_16x16x32_bf16 v[68:71], a[152:155], a[8:11], v[68:71]\n" \
"v_exp_f32_e64 v122, v122\n" \
"v_mfma_f32_16x16x32_bf16 v[68:71], a[148:151], a[4:7], v[68:71]\n" \
"v_exp_f32_e64 v123, v123\n" \
"v_mfma_f32_16x16x32_bf16 v[68:71], a[144:147], a[0:3], v[68:71]\n" \
"ds_read_b128 a[188:191], v49 offset:15360\n" \
"v_cvt_pk_bf16_f32 v121, v122, v123\n" \
"v_mfma_f32_16x16x32_bf16 v[76:79], a[144:147], a[16:19], v[36:39]\n" \
"v_exp_f32_e64 v124, v124\n" \
"v_mfma_f32_16x16x32_bf16 v[76:79], a[148:151], a[20:23], v[76:79]\n" \
"v_exp_f32_e64 v125, v125\n" \
"v_mfma_f32_16x16x32_bf16 v[76:79], a[152:155], a[24:27], v[76:79]\n" \
"v_exp_f32_e64 v126, v126\n" \
"v_mfma_f32_16x16x32_bf16 v[76:79], a[156:159], a[28:31], v[76:79]\n" \
"v_exp_f32_e64 v127, v127\n" \
"v_mfma_f32_16x16x32_bf16 v[72:75], a[140:143], a[28:31], v[36:39]\n" \
"v_cvt_pk_bf16_f32 v122, v124, v125\n" \
"v_cvt_pk_bf16_f32 v123, v126, v127\n" \
"v_mfma_f32_16x16x32_bf16 v[72:75], a[136:139], a[24:27], v[72:75]\n" \
"v_exp_f32_e64 v70, v70\n" \
"v_mfma_f32_16x16x32_bf16 v[72:75], a[132:135], a[20:23], v[72:75]\n" \
"v_exp_f32_e64 v71, v71\n" \
"v_mfma_f32_16x16x32_bf16 v[72:75], a[128:131], a[16:19], v[72:75]\n" \
"buffer_load_dwordx4 v52, s[8:11], s64 offen offset:1024 sc1 lds\n" \
"v_mfma_f32_16x16x32_bf16 v[80:83], a[128:131], a[32:35], v[40:43]\n" \
"s_mov_b32 m0, s65\n" \
"v_cvt_pk_bf16_f32 v71, v70, v71\n" \
"v_mfma_f32_16x16x32_bf16 v[80:83], a[132:135], a[36:39], v[80:83]\n" \
"v_exp_f32_e32 v68, v68\n" \
"v_mfma_f32_16x16x32_bf16 v[80:83], a[136:139], a[40:43], v[80:83]\n" \
"v_exp_f32_e64 v69, v69\n" \
"v_mfma_f32_16x16x32_bf16 v[80:83], a[140:143], a[44:47], v[80:83]\n" \
"v_exp_f32_e64 v72, v72\n" \
"v_mfma_f32_16x16x32_bf16 v[84:87], a[156:159], a[44:47], v[40:43]\n" \
"v_exp_f32_e64 v73, v73\n" \
"v_mfma_f32_16x16x32_bf16 v[84:87], a[152:155], a[40:43], v[84:87]\n" \
"v_exp_f32_e64 v74, v74\n" \
"v_mfma_f32_16x16x32_bf16 v[84:87], a[148:151], a[36:39], v[84:87]\n" \
"v_exp_f32_e64 v75, v75\n" \
"v_mfma_f32_16x16x32_bf16 v[84:87], a[144:147], a[32:35], v[84:87]\n" \
"v_exp_f32_e64 v66, v66\n" \
"v_mfma_f32_16x16x32_bf16 v[92:95], a[144:147], a[48:51], v[44:47]\n" \
"ds_read_b128 a[144:147], v48 offset:12288\n" \
"v_cvt_pk_bf16_f32 v72, v72, v73\n" \
"v_mfma_f32_16x16x32_bf16 v[92:95], a[148:151], a[52:55], v[92:95]\n" \
"ds_read_b128 a[148:151], v48 offset:13312\n" \
"v_cvt_pk_bf16_f32 v70, v68, v69\n" \
"v_mfma_f32_16x16x32_bf16 v[92:95], a[152:155], a[56:59], v[92:95]\n" \
"ds_read_b128 a[152:155], v48 offset:14336\n" \
"v_cvt_pk_bf16_f32 v73, v74, v75\n" \
"v_mfma_f32_16x16x32_bf16 v[92:95], a[156:159], a[60:63], v[92:95]\n" \
"ds_read_b128 a[156:159], v48 offset:15360\n" \
"v_add_u32_e32 v49, 0x10000, v48\n" \
"v_mfma_f32_16x16x32_bf16 v[88:91], a[140:143], a[60:63], v[44:47]\n" \
"ds_read_b128 a[140:143], v48 offset:11264\n" \
"s_xor_b32 s65, s65, 0x4000\n" \
"v_mfma_f32_16x16x32_bf16 v[88:91], a[136:139], a[56:59], v[88:91]\n" \
"v_exp_f32_e64 v67, v67\n" \
"v_mfma_f32_16x16x32_bf16 v[88:91], a[132:135], a[52:55], v[88:91]\n" \
"buffer_load_dwordx4 v53, s[8:11], s64 offen offset:0 sc1 lds\n" \
"v_mfma_f32_16x16x32_bf16 v[88:91], a[128:131], a[48:51], v[88:91]\n" \
"ds_read_b128 a[128:131], v48 offset:8192\n" \
"s_waitcnt lgkmcnt(6)\n" \
"v_mfma_f32_16x16x32_bf16 v[128:131], a[160:163], v[100:103], v[128:131]\n" \
"ds_read_b128 a[132:135], v48 offset:9216\n" \
"ds_read_b128 a[136:139], v48 offset:10240\n" \
"v_mfma_f32_16x16x32_bf16 v[160:163], a[160:163], v[104:107], v[160:163]\n" \
"v_exp_f32_e32 v64, v64\n" \
"v_mfma_f32_16x16x32_bf16 v[132:135], a[164:167], v[100:103], v[132:135]\n" \
"v_exp_f32_e64 v65, v65\n" \
"v_mfma_f32_16x16x32_bf16 v[164:167], a[164:167], v[104:107], v[164:167]\n" \
"v_exp_f32_e64 v76, v76\n" \
"v_mfma_f32_16x16x32_bf16 v[136:139], a[168:171], v[100:103], v[136:139]\n" \
"v_exp_f32_e64 v77, v77\n" \
"v_mfma_f32_16x16x32_bf16 v[168:171], a[168:171], v[104:107], v[168:171]\n" \
"v_exp_f32_e64 v78, v78\n" \
"v_mfma_f32_16x16x32_bf16 v[140:143], a[172:175], v[100:103], v[140:143]\n" \
"v_exp_f32_e64 v79, v79\n" \
"v_mfma_f32_16x16x32_bf16 v[172:175], a[172:175], v[104:107], v[172:175]\n" \
"v_cvt_pk_bf16_f32 v69, v66, v67\n" \
"v_cvt_pk_bf16_f32 v68, v64, v65\n" \
"v_mfma_f32_16x16x32_bf16 v[144:147], a[176:179], v[100:103], v[144:147]\n" \
"v_cvt_pk_bf16_f32 v74, v76, v77\n" \
"v_cvt_pk_bf16_f32 v75, v78, v79\n" \
"v_mfma_f32_16x16x32_bf16 v[176:179], a[176:179], v[104:107], v[176:179]\n" \
"v_xor_b32_e32 v48, 0x4000, v48\n" \
"v_mfma_f32_16x16x32_bf16 v[148:151], a[180:183], v[100:103], v[148:151]\n" \
"v_mfma_f32_16x16x32_bf16 v[180:183], a[180:183], v[104:107], v[180:183]\n" \
"v_mfma_f32_16x16x32_bf16 v[152:155], a[184:187], v[100:103], v[152:155]\n" \
"v_mfma_f32_16x16x32_bf16 v[184:187], a[184:187], v[104:107], v[184:187]\n" \
"buffer_load_dwordx4 v53, s[8:11], s64 offen offset:1024 sc1 lds\n" \
"v_mfma_f32_16x16x32_bf16 v[156:159], a[188:191], v[100:103], v[156:159]\n" \
"s_add_u32 s64, s64, 0x8000\n" \
"s_sub_u32 s66, s66, 1\n" \
"s_nop 0\n" \
"v_mfma_f32_16x16x32_bf16 v[188:191], a[188:191], v[104:107], v[188:191]\n" \
"v_smfmac_f32_16x16x64_bf16 a[192:195], a[200:203], v[100:107], v21\n" \
"v_mfma_f32_16x16x32_bf16 v[220:223], a[188:191], v[116:119], v[220:223]\n" \
"v_mfma_f32_16x16x32_bf16 v[252:255], a[188:191], v[120:123], v[252:255]\n" \
"v_mfma_f32_16x16x32_bf16 v[216:219], a[184:187], v[116:119], v[216:219]\n" \
"v_mfma_f32_16x16x32_bf16 v[248:251], a[184:187], v[120:123], v[248:251]\n" \
"v_mfma_f32_16x16x32_bf16 v[212:215], a[180:183], v[116:119], v[212:215]\n" \
"v_mfma_f32_16x16x32_bf16 v[244:247], a[180:183], v[120:123], v[244:247]\n" \
"v_mfma_f32_16x16x32_bf16 v[208:211], a[176:179], v[116:119], v[208:211]\n" \
"v_mfma_f32_16x16x32_bf16 v[240:243], a[176:179], v[120:123], v[240:243]\n" \
"v_mfma_f32_16x16x32_bf16 v[204:207], a[172:175], v[116:119], v[204:207]\n" \
"v_mfma_f32_16x16x32_bf16 v[236:239], a[172:175], v[120:123], v[236:239]\n" \
"v_mfma_f32_16x16x32_bf16 v[200:203], a[168:171], v[116:119], v[200:203]\n" \
"v_mfma_f32_16x16x32_bf16 v[232:235], a[168:171], v[120:123], v[232:235]\n" \
"v_mfma_f32_16x16x32_bf16 v[196:199], a[164:167], v[116:119], v[196:199]\n" \
"v_mfma_f32_16x16x32_bf16 v[228:231], a[164:167], v[120:123], v[228:231]\n" \
"v_mfma_f32_16x16x32_bf16 v[192:195], a[160:163], v[116:119], v[192:195]\n" \
"v_mfma_f32_16x16x32_bf16 v[224:227], a[160:163], v[120:123], v[224:227]\n" \
"v_smfmac_f32_16x16x64_bf16 a[196:199], a[200:203], v[116:123], v21\n" \
"s_waitcnt vmcnt(4) lgkmcnt(0)\n" \
"s_barrier\n" \
"s_cbranch_scc0 L_1_%=\n" \
"L_tr_%=:\n" \
"s_setpc_b64 s[28:29]\n" \
".fill (L_ab + 4096 - .) / 4, 4, 0xBF800000\n" \
".fill (L_ab + 4096 - .) / 4, 4, 0xBF800000\n" \
"L_ap1:\n" \
"s_setpc_b64 s[88:89]\n" \
"L_gtr:\n" \
"v_mfma_f32_16x16x32_bf16 v[96:99], a[128:131], a[0:3], v[32:35]\n" \
"v_mov_b32_e32 v56, s62\n" \
"v_readlane_b32 s68, v11, 0\n" \
"v_mfma_f32_16x16x32_bf16 v[96:99], a[132:135], a[4:7], v[96:99]\n" \
"s_xor_b32 m0, s65, 0x12000\n" \
"s_mov_b32 s65, s13\n" \
"v_mfma_f32_16x16x32_bf16 v[96:99], a[136:139], a[8:11], v[96:99]\n" \
"buffer_load_dwordx4 v50, s[8:11], s64 offen offset:0 sc1 lds\n" \
"v_mfma_f32_16x16x32_bf16 v[96:99], a[140:143], a[12:15], v[96:99]\n" \
"v_add_u32_e32 v63, s15, v20\n" \
"s_add_u32 s67, s14, 40960\n" \
"v_mfma_f32_16x16x32_bf16 v[100:103], a[156:159], a[12:15], v[32:35]\n" \
"buffer_load_dwordx4 v50, s[8:11], s64 offen offset:1024 sc1 lds\n" \
"s_mov_b64 exec, 1\n" \
"buffer_store_dword v56, off, s[56:59], s68 offset:0 sc1\n" \
"s_mov_b64 exec, -1\n" \
"v_mfma_f32_16x16x32_bf16 v[100:103], a[152:155], a[8:11], v[100:103]\n" \
"v_add_u32_e32 v50, 0x6000, v63\n" \
"s_xor_b32 m0, s65, 0x8000\n" \
"v_mfma_f32_16x16x32_bf16 v[100:103], a[148:151], a[4:7], v[100:103]\n" \
"ds_read_b128 a[160:163], v49 offset:0\n" \
"ds_read_b128 a[164:167], v49 offset:1024\n" \
"v_mfma_f32_16x16x32_bf16 v[100:103], a[144:147], a[0:3], v[100:103]\n" \
"ds_read_b128 a[168:171], v49 offset:2048\n" \
"ds_read_b128 a[172:175], v49 offset:3072\n" \
"v_mfma_f32_16x16x32_bf16 v[108:111], a[144:147], a[16:19], v[36:39]\n" \
"buffer_load_dwordx4 v50, s[8:11], s67 offen offset:0 sc1 lds\n" \
"v_mfma_f32_16x16x32_bf16 v[108:111], a[148:151], a[20:23], v[108:111]\n" \
"ds_read_b128 a[176:179], v49 offset:4096\n" \
"ds_read_b128 a[180:183], v49 offset:5120\n" \
"v_mfma_f32_16x16x32_bf16 v[108:111], a[152:155], a[24:27], v[108:111]\n" \
"ds_read_b128 a[184:187], v49 offset:6144\n" \
"ds_read_b128 a[188:191], v49 offset:7168\n" \
"v_mfma_f32_16x16x32_bf16 v[108:111], a[156:159], a[28:31], v[108:111]\n" \
"s_mov_b32 s64, s14\n" \
"v_add_u32_e32 v51, 0x12000, v63\n" \
"v_mfma_f32_16x16x32_bf16 v[104:107], a[140:143], a[28:31], v[36:39]\n" \
"buffer_load_dwordx4 v50, s[8:11], s67 offen offset:1024 sc1 lds\n" \
"v_mfma_f32_16x16x32_bf16 v[104:107], a[136:139], a[24:27], v[104:107]\n" \
"v_exp_f32_e32 v86, v86\n" \
"v_mfma_f32_16x16x32_bf16 v[104:107], a[132:135], a[20:23], v[104:107]\n" \
"v_exp_f32_e32 v87, v87\n" \
"v_mfma_f32_16x16x32_bf16 v[104:107], a[128:131], a[16:19], v[104:107]\n" \
"v_exp_f32_e32 v84, v84\n" \
"v_mfma_f32_16x16x32_bf16 v[112:115], a[128:131], a[32:35], v[40:43]\n" \
"v_exp_f32_e32 v85, v85\n" \
"v_mfma_f32_16x16x32_bf16 v[112:115], a[132:135], a[36:39], v[112:115]\n" \
"buffer_load_dwordx4 v50, s[8:11], s67 offen offset:2048 sc1 lds\n" \
"v_mfma_f32_16x16x32_bf16 v[112:115], a[136:139], a[40:43], v[112:115]\n" \
"v_cvt_pk_bf16_f32 v87, v86, v87\n" \
"v_cvt_pk_bf16_f32 v86, v84, v85\n" \
"v_mfma_f32_16x16x32_bf16 v[112:115], a[140:143], a[44:47], v[112:115]\n" \
"v_exp_f32_e32 v82, v82\n" \
"v_mfma_f32_16x16x32_bf16 v[116:119], a[156:159], a[44:47], v[40:43]\n" \
"v_exp_f32_e32 v83, v83\n" \
"v_mfma_f32_16x16x32_bf16 v[116:119], a[152:155], a[40:43], v[116:119]\n" \
"buffer_load_dwordx4 v50, s[8:11], s67 offen offset:3072 sc1 lds\n" \
"v_mfma_f32_16x16x32_bf16 v[116:119], a[148:151], a[36:39], v[116:119]\n" \
"s_xor_b32 m0, s65, 0xa000\n" \
"v_cvt_pk_bf16_f32 v85, v82, v83\n" \
"v_mfma_f32_16x16x32_bf16 v[116:119], a[144:147], a[32:35], v[116:119]\n" \
"v_exp_f32_e32 v80, v80\n" \
"v_mfma_f32_16x16x32_bf16 v[124:127], a[144:147], a[48:51], v[44:47]\n" \
"v_exp_f32_e32 v81, v81\n" \
"v_mfma_f32_16x16x32_bf16 v[124:127], a[148:151], a[52:55], v[124:127]\n" \
"buffer_load_dwordx4 v51, s[8:11], s64 offen offset:0 sc1 lds\n" \
"v_mfma_f32_16x16x32_bf16 v[124:127], a[152:155], a[56:59], v[124:127]\n" \
"v_cvt_pk_bf16_f32 v84, v80, v81\n" \
"v_readlane_b32 s93, v11, 19\n" \
"v_mfma_f32_16x16x32_bf16 v[124:127], a[156:159], a[60:63], v[124:127]\n" \
"v_exp_f32_e32 v88, v88\n" \
"v_mfma_f32_16x16x32_bf16 v[120:123], a[140:143], a[60:63], v[44:47]\n" \
"v_exp_f32_e32 v89, v89\n" \
"v_mfma_f32_16x16x32_bf16 v[120:123], a[136:139], a[56:59], v[120:123]\n" \
"v_exp_f32_e32 v90, v90\n" \
"v_mfma_f32_16x16x32_bf16 v[120:123], a[132:135], a[52:55], v[120:123]\n" \
"buffer_load_dwordx4 v51, s[8:11], s64 offen offset:1024 sc1 lds\n" \
"v_mfma_f32_16x16x32_bf16 v[120:123], a[128:131], a[48:51], v[120:123]\n" \
"v_cvt_pk_bf16_f32 v88, v88, v89\n" \
"s_waitcnt lgkmcnt(0)\n" \
"v_mfma_f32_16x16x32_bf16 v[128:131], a[160:163], v[68:71], v[128:131]\n" \
"v_exp_f32_e32 v91, v91\n" \
"v_mfma_f32_16x16x32_bf16 v[160:163], a[160:163], v[72:75], v[160:163]\n" \
"v_exp_f32_e32 v92, v92\n" \
"v_mfma_f32_16x16x32_bf16 v[132:135], a[164:167], v[68:71], v[132:135]\n" \
"buffer_load_dwordx4 v51, s[8:11], s64 offen offset:2048 sc1 lds\n" \
"v_mfma_f32_16x16x32_bf16 v[164:167], a[164:167], v[72:75], v[164:167]\n" \
"v_exp_f32_e32 v93, v93\n" \
"v_mfma_f32_16x16x32_bf16 v[136:139], a[168:171], v[68:71], v[136:139]\n" \
"v_exp_f32_e32 v94, v94\n" \
"v_mfma_f32_16x16x32_bf16 v[168:171], a[168:171], v[72:75], v[168:171]\n" \
"v_exp_f32_e32 v95, v95\n" \
"v_mfma_f32_16x16x32_bf16 v[140:143], a[172:175], v[68:71], v[140:143]\n" \
"buffer_load_dwordx4 v51, s[8:11], s64 offen offset:3072 sc1 lds\n" \
"v_mfma_f32_16x16x32_bf16 v[172:175], a[172:175], v[72:75], v[172:175]\n" \
"v_cvt_pk_bf16_f32 v89, v90, v91\n" \
"v_cvt_pk_bf16_f32 v90, v92, v93\n" \
"v_mfma_f32_16x16x32_bf16 v[144:147], a[176:179], v[68:71], v[144:147]\n" \
"v_cvt_pk_bf16_f32 v91, v94, v95\n" \
"v_readlane_b32 s80, v11, 11\n" \
"v_mfma_f32_16x16x32_bf16 v[176:179], a[176:179], v[72:75], v[176:179]\n" \
"v_readlane_b32 s81, v11, 12\n" \
"s_mov_b32 s82, 0x47fca0\n" \
"v_mfma_f32_16x16x32_bf16 v[148:151], a[180:183], v[68:71], v[148:151]\n" \
"s_mov_b32 s83, 0x20000\n" \
"v_readlane_b32 s92, v11, 26\n" \
"v_mfma_f32_16x16x32_bf16 v[180:183], a[180:183], v[72:75], v[180:183]\n" \
"s_add_u32 m0, s93, 0x0\n" \
"ds_read_b128 a[128:131], v48 offset:0\n" \
"v_mfma_f32_16x16x32_bf16 v[152:155], a[184:187], v[68:71], v[152:155]\n" \
"ds_read_b128 a[144:147], v48 offset:4096\n" \
"ds_read_b128 a[132:135], v48 offset:1024\n" \
"v_mfma_f32_16x16x32_bf16 v[184:187], a[184:187], v[72:75], v[184:187]\n" \
"buffer_load_dwordx4 v0, s[80:83], s92 offen offset:0 lds\n" \
"v_mfma_f32_16x16x32_bf16 v[156:159], a[188:191], v[68:71], v[156:159]\n" \
"ds_read_b128 a[148:151], v48 offset:5120\n" \
"ds_read_b128 a[136:139], v48 offset:2048\n" \
"v_mfma_f32_16x16x32_bf16 v[188:191], a[188:191], v[72:75], v[188:191]\n" \
"ds_read_b128 a[152:155], v48 offset:6144\n" \
"ds_read_b128 a[140:143], v48 offset:3072\n" \
"v_smfmac_f32_16x16x64_bf16 a[192:195], a[200:203], v[68:75], v21\n" \
"ds_read_b128 a[156:159], v48 offset:7168\n" \
"v_add_u32_e32 v52, 0x14000, v63\n" \
"v_mfma_f32_16x16x32_bf16 v[220:223], a[188:191], v[84:87], v[220:223]\n" \
"buffer_load_dwordx4 v1, s[80:83], s92 offen offset:1024 lds\n" \
"v_mfma_f32_16x16x32_bf16 v[252:255], a[188:191], v[88:91], v[252:255]\n" \
"v_exp_f32_e32 v102, v102\n" \
"v_mfma_f32_16x16x32_bf16 v[216:219], a[184:187], v[84:87], v[216:219]\n" \
"buffer_load_dwordx4 v2, s[80:83], s92 offen offset:2048 lds\n" \
"v_mfma_f32_16x16x32_bf16 v[248:251], a[184:187], v[88:91], v[248:251]\n" \
"v_exp_f32_e32 v103, v103\n" \
"v_mfma_f32_16x16x32_bf16 v[212:215], a[180:183], v[84:87], v[212:215]\n" \
"v_add_u32_e32 v53, 0x20000, v63\n" \
"v_cvt_pk_bf16_f32 v103, v102, v103\n" \
"v_mfma_f32_16x16x32_bf16 v[244:247], a[180:183], v[88:91], v[244:247]\n" \
"v_exp_f32_e32 v100, v100\n" \
"v_mfma_f32_16x16x32_bf16 v[208:211], a[176:179], v[84:87], v[208:211]\n" \
"buffer_load_dwordx4 v3, s[80:83], s92 offen offset:3072 lds\n" \
"v_mfma_f32_16x16x32_bf16 v[240:243], a[176:179], v[88:91], v[240:243]\n" \
"v_exp_f32_e32 v101, v101\n" \
"v_mfma_f32_16x16x32_bf16 v[204:207], a[172:175], v[84:87], v[204:207]\n" \
"v_exp_f32_e32 v98, v98\n" \
"v_mfma_f32_16x16x32_bf16 v[236:239], a[172:175], v[88:91], v[236:239]\n" \
"v_exp_f32_e32 v99, v99\n" \
"v_mfma_f32_16x16x32_bf16 v[200:203], a[168:171], v[84:87], v[200:203]\n" \
"v_exp_f32_e32 v96, v96\n" \
"v_mfma_f32_16x16x32_bf16 v[232:235], a[168:171], v[88:91], v[232:235]\n" \
"v_exp_f32_e32 v97, v97\n" \
"v_mfma_f32_16x16x32_bf16 v[196:199], a[164:167], v[84:87], v[196:199]\n" \
"v_exp_f32_e32 v104, v104\n" \
"v_mfma_f32_16x16x32_bf16 v[228:231], a[164:167], v[88:91], v[228:231]\n" \
"v_exp_f32_e32 v105, v105\n" \
"v_mfma_f32_16x16x32_bf16 v[192:195], a[160:163], v[84:87], v[192:195]\n" \
"v_cvt_pk_bf16_f32 v102, v100, v101\n" \
"v_cvt_pk_bf16_f32 v101, v98, v99\n" \
"v_mfma_f32_16x16x32_bf16 v[224:227], a[160:163], v[88:91], v[224:227]\n" \
"v_cvt_pk_bf16_f32 v100, v96, v97\n" \
"v_cvt_pk_bf16_f32 v104, v104, v105\n" \
"v_smfmac_f32_16x16x64_bf16 a[196:199], a[200:203], v[84:91], v21\n" \
"v_exp_f32_e32 v106, v106\n" \
"s_waitcnt vmcnt(15) lgkmcnt(0)\n" \
"s_barrier\n" \
"v_mfma_f32_16x16x32_bf16 v[64:67], a[128:131], a[0:3], v[32:35]\n" \
"s_xor_b32 m0, s65, 0x18000\n" \
"ds_read_b128 a[160:163], v49 offset:8192\n" \
"v_mfma_f32_16x16x32_bf16 v[64:67], a[132:135], a[4:7], v[64:67]\n" \
"buffer_load_dwordx4 v52, s[8:11], s64 offen offset:0 sc1 lds\n" \
"v_mfma_f32_16x16x32_bf16 v[64:67], a[136:139], a[8:11], v[64:67]\n" \
"ds_read_b128 a[164:167], v49 offset:9216\n" \
"ds_read_b128 a[168:171], v49 offset:10240\n" \
"v_mfma_f32_16x16x32_bf16 v[64:67], a[140:143], a[12:15], v[64:67]\n" \
"ds_read_b128 a[172:175], v49 offset:11264\n" \
"ds_read_b128 a[176:179], v49 offset:12288\n" \
"v_mfma_f32_16x16x32_bf16 v[68:71], a[156:159], a[12:15], v[32:35]\n" \
"buffer_load_dwordx4 v52, s[8:11], s64 offen offset:1024 sc1 lds\n" \
"v_mfma_f32_16x16x32_bf16 v[68:71], a[152:155], a[8:11], v[68:71]\n" \
"ds_read_b128 a[180:183], v49 offset:13312\n" \
"ds_read_b128 a[184:187], v49 offset:14336\n" \
"v_mfma_f32_16x16x32_bf16 v[68:71], a[148:151], a[4:7], v[68:71]\n" \
"v_exp_f32_e32 v107, v107\n" \
"v_mfma_f32_16x16x32_bf16 v[68:71], a[144:147], a[0:3], v[68:71]\n" \
"v_exp_f32_e32 v108, v108\n" \
"v_mfma_f32_16x16x32_bf16 v[76:79], a[144:147], a[16:19], v[36:39]\n" \
"v_exp_f32_e32 v109, v109\n" \
"v_mfma_f32_16x16x32_bf16 v[76:79], a[148:151], a[20:23], v[76:79]\n" \
"buffer_load_dwordx4 v52, s[8:11], s64 offen offset:2048 sc1 lds\n" \
"v_mfma_f32_16x16x32_bf16 v[76:79], a[152:155], a[24:27], v[76:79]\n" \
"v_exp_f32_e32 v110, v110\n" \
"v_mfma_f32_16x16x32_bf16 v[76:79], a[156:159], a[28:31], v[76:79]\n" \
"v_exp_f32_e32 v111, v111\n" \
"v_mfma_f32_16x16x32_bf16 v[72:75], a[140:143], a[28:31], v[36:39]\n" \
"v_cvt_pk_bf16_f32 v105, v106, v107\n" \
"v_cvt_pk_bf16_f32 v106, v108, v109\n" \
"v_mfma_f32_16x16x32_bf16 v[72:75], a[136:139], a[24:27], v[72:75]\n" \
"buffer_load_dwordx4 v52, s[8:11], s64 offen offset:3072 sc1 lds\n" \
"v_mfma_f32_16x16x32_bf16 v[72:75], a[132:135], a[20:23], v[72:75]\n" \
"s_mov_b32 m0, s65\n" \
"v_cvt_pk_bf16_f32 v107, v110, v111\n" \
"v_mfma_f32_16x16x32_bf16 v[72:75], a[128:131], a[16:19], v[72:75]\n" \
"ds_read_b128 a[188:191], v49 offset:15360\n" \
"v_add_u32_e32 v49, 0x10000, v48\n" \
"v_mfma_f32_16x16x32_bf16 v[80:83], a[128:131], a[32:35], v[40:43]\n" \
"v_exp_f32_e32 v118, v118\n" \
"v_mfma_f32_16x16x32_bf16 v[80:83], a[132:135], a[36:39], v[80:83]\n" \
"v_exp_f32_e32 v119, v119\n" \
"v_mfma_f32_16x16x32_bf16 v[80:83], a[136:139], a[40:43], v[80:83]\n" \
"buffer_load_dwordx4 v53, s[8:11], s64 offen offset:0 sc1 lds\n" \
"v_mfma_f32_16x16x32_bf16 v[80:83], a[140:143], a[44:47], v[80:83]\n" \
"v_exp_f32_e32 v116, v116\n" \
"v_mfma_f32_16x16x32_bf16 v[84:87], a[156:159], a[44:47], v[40:43]\n" \
"v_exp_f32_e32 v117, v117\n" \
"v_mfma_f32_16x16x32_bf16 v[84:87], a[152:155], a[40:43], v[84:87]\n" \
"v_cvt_pk_bf16_f32 v119, v118, v119\n" \
"v_cvt_pk_bf16_f32 v118, v116, v117\n" \
"v_mfma_f32_16x16x32_bf16 v[84:87], a[148:151], a[36:39], v[84:87]\n" \
"v_exp_f32_e32 v114, v114\n" \
"v_mfma_f32_16x16x32_bf16 v[84:87], a[144:147], a[32:35], v[84:87]\n" \
"buffer_load_dwordx4 v53, s[8:11], s64 offen offset:1024 sc1 lds\n" \
"v_mfma_f32_16x16x32_bf16 v[92:95], a[144:147], a[48:51], v[44:47]\n" \
"v_exp_f32_e32 v115, v115\n" \
"v_mfma_f32_16x16x32_bf16 v[92:95], a[148:151], a[52:55], v[92:95]\n" \
"v_exp_f32_e32 v112, v112\n" \
"v_mfma_f32_16x16x32_bf16 v[92:95], a[152:155], a[56:59], v[92:95]\n" \
"v_exp_f32_e32 v113, v113\n" \
"v_mfma_f32_16x16x32_bf16 v[92:95], a[156:159], a[60:63], v[92:95]\n" \
"buffer_load_dwordx4 v53, s[8:11], s64 offen offset:2048 sc1 lds\n" \
"v_mfma_f32_16x16x32_bf16 v[88:91], a[140:143], a[60:63], v[44:47]\n" \
"v_cvt_pk_bf16_f32 v117, v114, v115\n" \
"v_cvt_pk_bf16_f32 v116, v112, v113\n" \
"v_mfma_f32_16x16x32_bf16 v[88:91], a[136:139], a[56:59], v[88:91]\n" \
"v_exp_f32_e32 v120, v120\n" \
"v_mfma_f32_16x16x32_bf16 v[88:91], a[132:135], a[52:55], v[88:91]\n" \
"v_exp_f32_e32 v121, v121\n" \
"v_mfma_f32_16x16x32_bf16 v[88:91], a[128:131], a[48:51], v[88:91]\n" \
"v_cvt_pk_bf16_f32 v120, v120, v121\n" \
"s_waitcnt lgkmcnt(0)\n" \
"v_mfma_f32_16x16x32_bf16 v[128:131], a[160:163], v[100:103], v[128:131]\n" \
"buffer_load_dwordx4 v53, s[8:11], s64 offen offset:3072 sc1 lds\n" \
"v_mfma_f32_16x16x32_bf16 v[160:163], a[160:163], v[104:107], v[160:163]\n" \
"s_add_u32 m0, s93, 0x1000\n" \
"ds_read_b128 a[128:131], v48 offset:8192\n" \
"v_mfma_f32_16x16x32_bf16 v[132:135], a[164:167], v[100:103], v[132:135]\n" \
"v_exp_f32_e32 v122, v122\n" \
"v_mfma_f32_16x16x32_bf16 v[164:167], a[164:167], v[104:107], v[164:167]\n" \
"v_exp_f32_e32 v123, v123\n" \
"v_mfma_f32_16x16x32_bf16 v[136:139], a[168:171], v[100:103], v[136:139]\n" \
"buffer_load_dwordx4 v4, s[80:83], s92 offen offset:0 lds\n" \
"v_mfma_f32_16x16x32_bf16 v[168:171], a[168:171], v[104:107], v[168:171]\n" \
"v_exp_f32_e32 v124, v124\n" \
"v_mfma_f32_16x16x32_bf16 v[140:143], a[172:175], v[100:103], v[140:143]\n" \
"v_exp_f32_e32 v125, v125\n" \
"v_mfma_f32_16x16x32_bf16 v[172:175], a[172:175], v[104:107], v[172:175]\n" \
"v_exp_f32_e32 v126, v126\n" \
"v_mfma_f32_16x16x32_bf16 v[144:147], a[176:179], v[100:103], v[144:147]\n" \
"v_exp_f32_e32 v127, v127\n" \
"v_mfma_f32_16x16x32_bf16 v[176:179], a[176:179], v[104:107], v[176:179]\n" \
"v_cvt_pk_bf16_f32 v121, v122, v123\n" \
"v_cvt_pk_bf16_f32 v122, v124, v125\n" \
"v_mfma_f32_16x16x32_bf16 v[148:151], a[180:183], v[100:103], v[148:151]\n" \
"buffer_load_dwordx4 v5, s[80:83], s92 offen offset:1024 lds\n" \
"v_mfma_f32_16x16x32_bf16 v[180:183], a[180:183], v[104:107], v[180:183]\n" \
"v_cvt_pk_bf16_f32 v123, v126, v127\n" \
"ds_read_b128 a[144:147], v48 offset:12288\n" \
"v_mfma_f32_16x16x32_bf16 v[152:155], a[184:187], v[100:103], v[152:155]\n" \
"ds_read_b128 a[132:135], v48 offset:9216\n" \
"ds_read_b128 a[148:151], v48 offset:13312\n" \
"v_mfma_f32_16x16x32_bf16 v[184:187], a[184:187], v[104:107], v[184:187]\n" \
"ds_read_b128 a[136:139], v48 offset:10240\n" \
"ds_read_b128 a[152:155], v48 offset:14336\n" \
"v_mfma_f32_16x16x32_bf16 v[156:159], a[188:191], v[100:103], v[156:159]\n" \
"ds_read_b128 a[140:143], v48 offset:11264\n" \
"ds_read_b128 a[156:159], v48 offset:15360\n" \
"v_mfma_f32_16x16x32_bf16 v[188:191], a[188:191], v[104:107], v[188:191]\n" \
"buffer_load_dwordx4 v6, s[80:83], s92 offen offset:2048 lds\n" \
"v_smfmac_f32_16x16x64_bf16 a[192:195], a[200:203], v[100:107], v21\n" \
"v_exp_f32_e32 v70, v70\n" \
"v_mfma_f32_16x16x32_bf16 v[220:223], a[188:191], v[116:119], v[220:223]\n" \
"v_exp_f32_e32 v71, v71\n" \
"v_mfma_f32_16x16x32_bf16 v[252:255], a[188:191], v[120:123], v[252:255]\n" \
"buffer_load_dwordx4 v7, s[80:83], s92 offen offset:3072 lds\n" \
"v_mfma_f32_16x16x32_bf16 v[216:219], a[184:187], v[116:119], v[216:219]\n" \
"s_add_u32 m0, s93, 0x2000\n" \
"v_cvt_pk_bf16_f32 v71, v70, v71\n" \
"v_mfma_f32_16x16x32_bf16 v[248:251], a[184:187], v[120:123], v[248:251]\n" \
"v_exp_f32_e32 v72, v72\n" \
"v_mfma_f32_16x16x32_bf16 v[212:215], a[180:183], v[116:119], v[212:215]\n" \
"v_exp_f32_e32 v73, v73\n" \
"v_mfma_f32_16x16x32_bf16 v[244:247], a[180:183], v[120:123], v[244:247]\n" \
"v_exp_f32_e32 v68, v68\n" \
"v_mfma_f32_16x16x32_bf16 v[208:211], a[176:179], v[116:119], v[208:211]\n" \
"buffer_load_dwordx4 v8, s[80:83], s92 offen offset:0 lds\n" \
"v_mfma_f32_16x16x32_bf16 v[240:243], a[176:179], v[120:123], v[240:243]\n" \
"v_exp_f32_e32 v69, v69\n" \
"v_mfma_f32_16x16x32_bf16 v[204:207], a[172:175], v[116:119], v[204:207]\n" \
"v_exp_f32_e32 v74, v74\n" \
"v_mfma_f32_16x16x32_bf16 v[236:239], a[172:175], v[120:123], v[236:239]\n" \
"v_exp_f32_e32 v75, v75\n" \
"v_mfma_f32_16x16x32_bf16 v[200:203], a[168:171], v[116:119], v[200:203]\n" \
"v_exp_f32_e32 v66, v66\n" \
"v_mfma_f32_16x16x32_bf16 v[232:235], a[168:171], v[120:123], v[232:235]\n" \
"v_exp_f32_e32 v67, v67\n" \
"v_mfma_f32_16x16x32_bf16 v[196:199], a[164:167], v[116:119], v[196:199]\n" \
"v_exp_f32_e32 v64, v64\n" \
"v_mfma_f32_16x16x32_bf16 v[228:231], a[164:167], v[120:123], v[228:231]\n" \
"v_exp_f32_e32 v65, v65\n" \
"v_mfma_f32_16x16x32_bf16 v[192:195], a[160:163], v[116:119], v[192:195]\n" \
"v_cvt_pk_bf16_f32 v72, v72, v73\n" \
"v_cvt_pk_bf16_f32 v70, v68, v69\n" \
"v_mfma_f32_16x16x32_bf16 v[224:227], a[160:163], v[120:123], v[224:227]\n" \
"v_exp_f32_e32 v76, v76\n" \
"v_smfmac_f32_16x16x64_bf16 a[196:199], a[200:203], v[116:123], v21\n" \
"v_exp_f32_e32 v77, v77\n" \
"s_waitcnt vmcnt(13) lgkmcnt(0)\n" \
"s_barrier\n" \
"v_exp_f32_e32 v78, v78\n" \
"v_exp_f32_e32 v79, v79\n" \
"v_cvt_pk_bf16_f32 v73, v74, v75\n" \
"v_cvt_pk_bf16_f32 v69, v66, v67\n" \
"v_cvt_pk_bf16_f32 v68, v64, v65\n" \
"v_cvt_pk_bf16_f32 v74, v76, v77\n" \
"v_cvt_pk_bf16_f32 v75, v78, v79\n" \
"v_add_u32_e32 v48, s16, v20\n" \
"s_xor_b32 s65, s65, 0x8000\n" \
"s_add_u32 s64, s64, 0x10000\n" \
"s_getpc_b64 s[86:87]\n" \
"L_hpt_%=:\n" \
"s_add_u32 s86, s86, L_ap8-L_hpt_%=\n" \
"s_addc_u32 s87, s87, 0\n" \
"s_add_u32 s86, s86, s15\n" \
"s_addc_u32 s87, s87, 0\n" \
"s_mov_b64 exec, 0\n" \
"s_swappc_b64 s[88:89], s[86:87]\n" \
"s_mov_b64 exec, -1\n" \
"s_sub_u32 s66, s19, 1\n" \
"s_cbranch_scc1 L_la_%=\n" \
".fill (L_ab + 6464 - .) / 4, 4, 0xBF800000\n" \
"L_2_%=:\n" \
"v_mfma_f32_16x16x32_bf16 v[96:99], a[128:131], a[64:67], v[32:35]\n" \
"s_xor_b32 m0, s65, 0x12000\n" \
"ds_read_b128 a[160:163], v49 offset:0\n" \
"v_mfma_f32_16x16x32_bf16 v[96:99], a[132:135], a[68:71], v[96:99]\n" \
"buffer_load_dwordx4 v50, s[8:11], s64 offen offset:0 sc1 lds\n" \
"v_mfma_f32_16x16x32_bf16 v[96:99], a[136:139], a[72:75], v[96:99]\n" \
"ds_read_b128 a[164:167], v49 offset:1024\n" \
"ds_read_b128 a[168:171], v49 offset:2048\n" \
"v_mfma_f32_16x16x32_bf16 v[96:99], a[140:143], a[76:79], v[96:99]\n" \
"ds_read_b128 a[172:175], v49 offset:3072\n" \
"ds_read_b128 a[176:179], v49 offset:4096\n" \
"v_mfma_f32_16x16x32_bf16 v[100:103], a[156:159], a[76:79], v[32:35]\n" \
"ds_read_b128 a[180:183], v49 offset:5120\n" \
"ds_read_b128 a[184:187], v49 offset:6144\n" \
"v_mfma_f32_16x16x32_bf16 v[100:103], a[152:155], a[72:75], v[100:103]\n" \
"v_exp_f32_e64 v86, v86\n" \
"v_mfma_f32_16x16x32_bf16 v[100:103], a[148:151], a[68:71], v[100:103]\n" \
"v_exp_f32_e64 v87, v87\n" \
"v_mfma_f32_16x16x32_bf16 v[100:103], a[144:147], a[64:67], v[100:103]\n" \
"buffer_load_dwordx4 v50, s[8:11], s64 offen offset:1024 sc1 lds\n" \
"v_mfma_f32_16x16x32_bf16 v[108:111], a[144:147], a[80:83], v[36:39]\n" \
"ds_read_b128 a[188:191], v49 offset:7168\n" \
"v_cvt_pk_bf16_f32 v87, v86, v87\n" \
"v_mfma_f32_16x16x32_bf16 v[108:111], a[148:151], a[84:87], v[108:111]\n" \
"v_exp_f32_e64 v84, v84\n" \
"v_mfma_f32_16x16x32_bf16 v[108:111], a[152:155], a[88:91], v[108:111]\n" \
"v_exp_f32_e64 v85, v85\n" \
"v_mfma_f32_16x16x32_bf16 v[108:111], a[156:159], a[92:95], v[108:111]\n" \
"v_exp_f32_e64 v82, v82\n" \
"v_mfma_f32_16x16x32_bf16 v[104:107], a[140:143], a[92:95], v[36:39]\n" \
"v_exp_f32_e64 v83, v83\n" \
"v_mfma_f32_16x16x32_bf16 v[104:107], a[136:139], a[88:91], v[104:107]\n" \
"v_exp_f32_e64 v80, v80\n" \
"v_mfma_f32_16x16x32_bf16 v[104:107], a[132:135], a[84:87], v[104:107]\n" \
"v_exp_f32_e64 v81, v81\n" \
"v_mfma_f32_16x16x32_bf16 v[104:107], a[128:131], a[80:83], v[104:107]\n" \
"buffer_load_dwordx4 v50, s[8:11], s64 offen offset:2048 sc1 lds\n" \
"v_mfma_f32_16x16x32_bf16 v[112:115], a[128:131], a[96:99], v[40:43]\n" \
"v_exp_f32_e64 v88, v88\n" \
"v_mfma_f32_16x16x32_bf16 v[112:115], a[132:135], a[100:103], v[112:115]\n" \
"v_exp_f32_e64 v89, v89\n" \
"v_mfma_f32_16x16x32_bf16 v[112:115], a[136:139], a[104:107], v[112:115]\n" \
"v_cvt_pk_bf16_f32 v86, v84, v85\n" \
"v_cvt_pk_bf16_f32 v85, v82, v83\n" \
"v_mfma_f32_16x16x32_bf16 v[112:115], a[140:143], a[108:111], v[112:115]\n" \
"v_cvt_pk_bf16_f32 v84, v80, v81\n" \
"v_cvt_pk_bf16_f32 v88, v88, v89\n" \
"v_mfma_f32_16x16x32_bf16 v[116:119], a[156:159], a[108:111], v[40:43]\n" \
"v_exp_f32_e64 v90, v90\n" \
"v_mfma_f32_16x16x32_bf16 v[116:119], a[152:155], a[104:107], v[116:119]\n" \
"v_exp_f32_e64 v91, v91\n" \
"v_mfma_f32_16x16x32_bf16 v[116:119], a[148:151], a[100:103], v[116:119]\n" \
"buffer_load_dwordx4 v50, s[8:11], s64 offen offset:3072 sc1 lds\n" \
"v_mfma_f32_16x16x32_bf16 v[116:119], a[144:147], a[96:99], v[116:119]\n" \
"s_xor_b32 m0, s65, 0xa000\n" \
"v_cvt_pk_bf16_f32 v89, v90, v91\n" \
"v_mfma_f32_16x16x32_bf16 v[124:127], a[144:147], a[112:115], v[44:47]\n" \
"v_exp_f32_e64 v92, v92\n" \
"v_mfma_f32_16x16x32_bf16 v[124:127], a[148:151], a[116:119], v[124:127]\n" \
"v_exp_f32_e64 v93, v93\n" \
"v_mfma_f32_16x16x32_bf16 v[124:127], a[152:155], a[120:123], v[124:127]\n" \
"v_exp_f32_e64 v94, v94\n" \
"v_mfma_f32_16x16x32_bf16 v[124:127], a[156:159], a[124:127], v[124:127]\n" \
"v_exp_f32_e64 v95, v95\n" \
"v_mfma_f32_16x16x32_bf16 v[120:123], a[140:143], a[124:127], v[44:47]\n" \
"v_cvt_pk_bf16_f32 v90, v92, v93\n" \
"v_cvt_pk_bf16_f32 v91, v94, v95\n" \
"v_mfma_f32_16x16x32_bf16 v[120:123], a[136:139], a[120:123], v[120:123]\n" \
"ds_read_b128 a[144:147], v48 offset:4096\n" \
"ds_read_b128 a[148:151], v48 offset:5120\n" \
"v_mfma_f32_16x16x32_bf16 v[120:123], a[132:135], a[116:119], v[120:123]\n" \
"buffer_load_dwordx4 v51, s[8:11], s64 offen offset:0 sc1 lds\n" \
"v_mfma_f32_16x16x32_bf16 v[120:123], a[128:131], a[112:115], v[120:123]\n" \
"ds_read_b128 a[128:131], v48 offset:0\n" \
"s_waitcnt lgkmcnt(3)\n" \
"v_mfma_f32_16x16x32_bf16 v[128:131], a[160:163], v[68:71], v[128:131]\n" \
"ds_read_b128 a[132:135], v48 offset:1024\n" \
"ds_read_b128 a[136:139], v48 offset:2048\n" \
"v_mfma_f32_16x16x32_bf16 v[160:163], a[160:163], v[72:75], v[160:163]\n" \
"ds_read_b128 a[152:155], v48 offset:6144\n" \
"ds_read_b128 a[140:143], v48 offset:3072\n" \
"v_mfma_f32_16x16x32_bf16 v[132:135], a[164:167], v[68:71], v[132:135]\n" \
"v_exp_f32_e32 v102, v102\n" \
"v_mfma_f32_16x16x32_bf16 v[164:167], a[164:167], v[72:75], v[164:167]\n" \
"v_exp_f32_e64 v103, v103\n" \
"v_mfma_f32_16x16x32_bf16 v[136:139], a[168:171], v[68:71], v[136:139]\n" \
"ds_read_b128 a[156:159], v48 offset:7168\n" \
"v_cvt_pk_bf16_f32 v103, v102, v103\n" \
"v_mfma_f32_16x16x32_bf16 v[168:171], a[168:171], v[72:75], v[168:171]\n" \
"buffer_load_dwordx4 v51, s[8:11], s64 offen offset:1024 sc1 lds\n" \
"v_mfma_f32_16x16x32_bf16 v[140:143], a[172:175], v[68:71], v[140:143]\n" \
"v_exp_f32_e64 v100, v100\n" \
"v_mfma_f32_16x16x32_bf16 v[172:175], a[172:175], v[72:75], v[172:175]\n" \
"v_exp_f32_e64 v101, v101\n" \
"v_mfma_f32_16x16x32_bf16 v[144:147], a[176:179], v[68:71], v[144:147]\n" \
"v_exp_f32_e64 v98, v98\n" \
"v_mfma_f32_16x16x32_bf16 v[176:179], a[176:179], v[72:75], v[176:179]\n" \
"v_exp_f32_e64 v99, v99\n" \
"v_mfma_f32_16x16x32_bf16 v[148:151], a[180:183], v[68:71], v[148:151]\n" \
"v_exp_f32_e64 v96, v96\n" \
"v_mfma_f32_16x16x32_bf16 v[180:183], a[180:183], v[72:75], v[180:183]\n" \
"v_exp_f32_e64 v97, v97\n" \
"v_mfma_f32_16x16x32_bf16 v[152:155], a[184:187], v[68:71], v[152:155]\n" \
"v_exp_f32_e64 v104, v104\n" \
"v_mfma_f32_16x16x32_bf16 v[184:187], a[184:187], v[72:75], v[184:187]\n" \
"buffer_load_dwordx4 v51, s[8:11], s64 offen offset:2048 sc1 lds\n" \
"v_mfma_f32_16x16x32_bf16 v[156:159], a[188:191], v[68:71], v[156:159]\n" \
"v_exp_f32_e64 v105, v105\n" \
"v_mfma_f32_16x16x32_bf16 v[188:191], a[188:191], v[72:75], v[188:191]\n" \
"v_cvt_pk_bf16_f32 v102, v100, v101\n" \
"v_cvt_pk_bf16_f32 v101, v98, v99\n" \
"v_smfmac_f32_16x16x64_bf16 a[192:195], a[200:203], v[68:75], v21\n" \
"v_cvt_pk_bf16_f32 v100, v96, v97\n" \
"v_cvt_pk_bf16_f32 v104, v104, v105\n" \
"v_mfma_f32_16x16x32_bf16 v[220:223], a[188:191], v[84:87], v[220:223]\n" \
"v_exp_f32_e64 v106, v106\n" \
"v_mfma_f32_16x16x32_bf16 v[252:255], a[188:191], v[88:91], v[252:255]\n" \
"v_exp_f32_e64 v107, v107\n" \
"v_mfma_f32_16x16x32_bf16 v[216:219], a[184:187], v[84:87], v[216:219]\n" \
"v_exp_f32_e64 v108, v108\n" \
"v_mfma_f32_16x16x32_bf16 v[248:251], a[184:187], v[88:91], v[248:251]\n" \
"buffer_load_dwordx4 v51, s[8:11], s64 offen offset:3072 sc1 lds\n" \
"v_mfma_f32_16x16x32_bf16 v[212:215], a[180:183], v[84:87], v[212:215]\n" \
"v_exp_f32_e64 v109, v109\n" \
"v_mfma_f32_16x16x32_bf16 v[244:247], a[180:183], v[88:91], v[244:247]\n" \
"v_exp_f32_e64 v110, v110\n" \
"v_mfma_f32_16x16x32_bf16 v[208:211], a[176:179], v[84:87], v[208:211]\n" \
"v_exp_f32_e64 v111, v111\n" \
"v_mfma_f32_16x16x32_bf16 v[240:243], a[176:179], v[88:91], v[240:243]\n" \
"v_exp_f32_e64 v118, v118\n" \
"v_mfma_f32_16x16x32_bf16 v[204:207], a[172:175], v[84:87], v[204:207]\n" \
"v_exp_f32_e64 v119, v119\n" \
"v_mfma_f32_16x16x32_bf16 v[236:239], a[172:175], v[88:91], v[236:239]\n" \
"v_cvt_pk_bf16_f32 v105, v106, v107\n" \
"v_cvt_pk_bf16_f32 v106, v108, v109\n" \
"v_mfma_f32_16x16x32_bf16 v[200:203], a[168:171], v[84:87], v[200:203]\n" \
"v_cvt_pk_bf16_f32 v107, v110, v111\n" \
"v_cvt_pk_bf16_f32 v119, v118, v119\n" \
"v_mfma_f32_16x16x32_bf16 v[232:235], a[168:171], v[88:91], v[232:235]\n" \
"v_exp_f32_e64 v116, v116\n" \
"v_mfma_f32_16x16x32_bf16 v[196:199], a[164:167], v[84:87], v[196:199]\n" \
"v_exp_f32_e64 v117, v117\n" \
"v_mfma_f32_16x16x32_bf16 v[228:231], a[164:167], v[88:91], v[228:231]\n" \
"v_exp_f32_e64 v114, v114\n" \
"v_mfma_f32_16x16x32_bf16 v[192:195], a[160:163], v[84:87], v[192:195]\n" \
"v_exp_f32_e64 v115, v115\n" \
"v_mfma_f32_16x16x32_bf16 v[224:227], a[160:163], v[88:91], v[224:227]\n" \
"v_exp_f32_e64 v112, v112\n" \
"v_smfmac_f32_16x16x64_bf16 a[196:199], a[200:203], v[84:91], v21\n" \
"v_exp_f32_e32 v113, v113\n" \
"s_waitcnt vmcnt(8) lgkmcnt(0)\n" \
"s_barrier\n" \
"s_nop 0\n" \
"v_mfma_f32_16x16x32_bf16 v[64:67], a[128:131], a[64:67], v[32:35]\n" \
"s_xor_b32 m0, s65, 0x18000\n" \
"ds_read_b128 a[160:163], v49 offset:8192\n" \
"v_mfma_f32_16x16x32_bf16 v[64:67], a[132:135], a[68:71], v[64:67]\n" \
"buffer_load_dwordx4 v52, s[8:11], s64 offen offset:0 sc1 lds\n" \
"v_mfma_f32_16x16x32_bf16 v[64:67], a[136:139], a[72:75], v[64:67]\n" \
"ds_read_b128 a[164:167], v49 offset:9216\n" \
"ds_read_b128 a[168:171], v49 offset:10240\n" \
"v_mfma_f32_16x16x32_bf16 v[64:67], a[140:143], a[76:79], v[64:67]\n" \
"ds_read_b128 a[172:175], v49 offset:11264\n" \
"ds_read_b128 a[176:179], v49 offset:12288\n" \
"v_mfma_f32_16x16x32_bf16 v[68:71], a[156:159], a[76:79], v[32:35]\n" \
"ds_read_b128 a[180:183], v49 offset:13312\n" \
"ds_read_b128 a[184:187], v49 offset:14336\n" \
"v_mfma_f32_16x16x32_bf16 v[68:71], a[152:155], a[72:75], v[68:71]\n" \
"ds_read_b128 a[188:191], v49 offset:15360\n" \
"v_cvt_pk_bf16_f32 v118, v116, v117\n" \
"v_mfma_f32_16x16x32_bf16 v[68:71], a[148:151], a[68:71], v[68:71]\n" \
"v_cvt_pk_bf16_f32 v117, v114, v115\n" \
"v_cvt_pk_bf16_f32 v116, v112, v113\n" \
"v_mfma_f32_16x16x32_bf16 v[68:71], a[144:147], a[64:67], v[68:71]\n" \
"buffer_load_dwordx4 v52, s[8:11], s64 offen offset:1024 sc1 lds\n" \
"v_mfma_f32_16x16x32_bf16 v[76:79], a[144:147], a[80:83], v[36:39]\n" \
"v_exp_f32_e64 v120, v120\n" \
"v_mfma_f32_16x16x32_bf16 v[76:79], a[148:151], a[84:87], v[76:79]\n" \
"v_exp_f32_e64 v121, v121\n" \
"v_mfma_f32_16x16x32_bf16 v[76:79], a[152:155], a[88:91], v[76:79]\n" \
"v_exp_f32_e64 v122, v122\n" \
"v_mfma_f32_16x16x32_bf16 v[76:79], a[156:159], a[92:95], v[76:79]\n" \
"v_exp_f32_e64 v123, v123\n" \
"v_mfma_f32_16x16x32_bf16 v[72:75], a[140:143], a[92:95], v[36:39]\n" \
"v_cvt_pk_bf16_f32 v120, v120, v121\n" \
"v_cvt_pk_bf16_f32 v121, v122, v123\n" \
"v_mfma_f32_16x16x32_bf16 v[72:75], a[136:139], a[88:91], v[72:75]\n" \
"v_exp_f32_e64 v124, v124\n" \
"v_mfma_f32_16x16x32_bf16 v[72:75], a[132:135], a[84:87], v[72:75]\n" \
"v_exp_f32_e64 v125, v125\n" \
"v_mfma_f32_16x16x32_bf16 v[72:75], a[128:131], a[80:83], v[72:75]\n" \
"buffer_load_dwordx4 v52, s[8:11], s64 offen offset:2048 sc1 lds\n" \
"v_mfma_f32_16x16x32_bf16 v[80:83], a[128:131], a[96:99], v[40:43]\n" \
"v_exp_f32_e64 v126, v126\n" \
"v_mfma_f32_16x16x32_bf16 v[80:83], a[132:135], a[100:103], v[80:83]\n" \
"v_exp_f32_e64 v127, v127\n" \
"v_mfma_f32_16x16x32_bf16 v[80:83], a[136:139], a[104:107], v[80:83]\n" \
"v_cvt_pk_bf16_f32 v122, v124, v125\n" \
"v_cvt_pk_bf16_f32 v123, v126, v127\n" \
"v_mfma_f32_16x16x32_bf16 v[80:83], a[140:143], a[108:111], v[80:83]\n" \
"v_exp_f32_e64 v70, v70\n" \
"v_mfma_f32_16x16x32_bf16 v[84:87], a[156:159], a[108:111], v[40:43]\n" \
"v_exp_f32_e64 v71, v71\n" \
"v_mfma_f32_16x16x32_bf16 v[84:87], a[152:155], a[104:107], v[84:87]\n" \
"v_exp_f32_e64 v72, v72\n" \
"v_mfma_f32_16x16x32_bf16 v[84:87], a[148:151], a[100:103], v[84:87]\n" \
"buffer_load_dwordx4 v52, s[8:11], s64 offen offset:3072 sc1 lds\n" \
"v_mfma_f32_16x16x32_bf16 v[84:87], a[144:147], a[96:99], v[84:87]\n" \
"s_mov_b32 m0, s65\n" \
"v_cvt_pk_bf16_f32 v71, v70, v71\n" \
"v_mfma_f32_16x16x32_bf16 v[92:95], a[144:147], a[112:115], v[44:47]\n" \
"ds_read_b128 a[144:147], v48 offset:12288\n" \
"v_add_u32_e32 v49, 0x10000, v48\n" \
"v_mfma_f32_16x16x32_bf16 v[92:95], a[148:151], a[116:119], v[92:95]\n" \
"ds_read_b128 a[148:151], v48 offset:13312\n" \
"s_xor_b32 s65, s65, 0x8000\n" \
"v_mfma_f32_16x16x32_bf16 v[92:95], a[152:155], a[120:123], v[92:95]\n" \
"v_exp_f32_e32 v73, v73\n" \
"v_mfma_f32_16x16x32_bf16 v[92:95], a[156:159], a[124:127], v[92:95]\n" \
"ds_read_b128 a[152:155], v48 offset:14336\n" \
".fill (L_ab + 8192 - .) / 4, 4, 0xBF800000\n" \
"L_ap2:\n" \
"s_cbranch_execz L_ap0\n" \
"ds_read_b128 a[156:159], v48 offset:15360\n" \
"v_mfma_f32_16x16x32_bf16 v[88:91], a[140:143], a[124:127], v[44:47]\n" \
"ds_read_b128 a[140:143], v48 offset:11264\n" \
"v_cvt_pk_bf16_f32 v72, v72, v73\n" \
"v_mfma_f32_16x16x32_bf16 v[88:91], a[136:139], a[120:123], v[88:91]\n" \
"v_exp_f32_e32 v68, v68\n" \
"v_mfma_f32_16x16x32_bf16 v[88:91], a[132:135], a[116:119], v[88:91]\n" \
"buffer_load_dwordx4 v53, s[8:11], s64 offen offset:0 sc1 lds\n" \
"v_mfma_f32_16x16x32_bf16 v[88:91], a[128:131], a[112:115], v[88:91]\n" \
"ds_read_b128 a[128:131], v48 offset:8192\n" \
"s_waitcnt lgkmcnt(6)\n" \
"v_mfma_f32_16x16x32_bf16 v[128:131], a[160:163], v[100:103], v[128:131]\n" \
"ds_read_b128 a[132:135], v48 offset:9216\n" \
"ds_read_b128 a[136:139], v48 offset:10240\n" \
"v_mfma_f32_16x16x32_bf16 v[160:163], a[160:163], v[104:107], v[160:163]\n" \
"v_exp_f32_e32 v69, v69\n" \
"v_mfma_f32_16x16x32_bf16 v[132:135], a[164:167], v[100:103], v[132:135]\n" \
"v_exp_f32_e64 v74, v74\n" \
"v_mfma_f32_16x16x32_bf16 v[164:167], a[164:167], v[104:107], v[164:167]\n" \
"v_exp_f32_e64 v75, v75\n" \
"v_mfma_f32_16x16x32_bf16 v[136:139], a[168:171], v[100:103], v[136:139]\n" \
"v_exp_f32_e64 v66, v66\n" \
"v_mfma_f32_16x16x32_bf16 v[168:171], a[168:171], v[104:107], v[168:171]\n" \
"buffer_load_dwordx4 v53, s[8:11], s64 offen offset:1024 sc1 lds\n" \
"v_mfma_f32_16x16x32_bf16 v[140:143], a[172:175], v[100:103], v[140:143]\n" \
"v_exp_f32_e64 v67, v67\n" \
"v_mfma_f32_16x16x32_bf16 v[172:175], a[172:175], v[104:107], v[172:175]\n" \
"v_exp_f32_e64 v64, v64\n" \
"v_mfma_f32_16x16x32_bf16 v[144:147], a[176:179], v[100:103], v[144:147]\n" \
"v_exp_f32_e64 v65, v65\n" \
"v_mfma_f32_16x16x32_bf16 v[176:179], a[176:179], v[104:107], v[176:179]\n" \
"v_exp_f32_e64 v76, v76\n" \
"v_mfma_f32_16x16x32_bf16 v[148:151], a[180:183], v[100:103], v[148:151]\n" \
"v_exp_f32_e64 v77, v77\n" \
"v_mfma_f32_16x16x32_bf16 v[180:183], a[180:183], v[104:107], v[180:183]\n" \
"v_exp_f32_e64 v78, v78\n" \
"v_mfma_f32_16x16x32_bf16 v[152:155], a[184:187], v[100:103], v[152:155]\n" \
"v_exp_f32_e64 v79, v79\n" \
"v_mfma_f32_16x16x32_bf16 v[184:187], a[184:187], v[104:107], v[184:187]\n" \
"buffer_load_dwordx4 v53, s[8:11], s64 offen offset:2048 sc1 lds\n" \
"v_mfma_f32_16x16x32_bf16 v[156:159], a[188:191], v[100:103], v[156:159]\n" \
"v_cvt_pk_bf16_f32 v70, v68, v69\n" \
"v_cvt_pk_bf16_f32 v73, v74, v75\n" \
"v_mfma_f32_16x16x32_bf16 v[188:191], a[188:191], v[104:107], v[188:191]\n" \
"v_cvt_pk_bf16_f32 v69, v66, v67\n" \
"v_cvt_pk_bf16_f32 v68, v64, v65\n" \
"v_smfmac_f32_16x16x64_bf16 a[192:195], a[200:203], v[100:107], v21\n" \
"v_cvt_pk_bf16_f32 v74, v76, v77\n" \
"v_cvt_pk_bf16_f32 v75, v78, v79\n" \
"v_mfma_f32_16x16x32_bf16 v[220:223], a[188:191], v[116:119], v[220:223]\n" \
"v_xor_b32_e32 v48, 0x8000, v48\n" \
"v_mfma_f32_16x16x32_bf16 v[252:255], a[188:191], v[120:123], v[252:255]\n" \
"v_mfma_f32_16x16x32_bf16 v[216:219], a[184:187], v[116:119], v[216:219]\n" \
"v_mfma_f32_16x16x32_bf16 v[248:251], a[184:187], v[120:123], v[248:251]\n" \
"buffer_load_dwordx4 v53, s[8:11], s64 offen offset:3072 sc1 lds\n" \
"v_mfma_f32_16x16x32_bf16 v[212:215], a[180:183], v[116:119], v[212:215]\n" \
"s_add_u32 s64, s64, 0x10000\n" \
"s_sub_u32 s66, s66, 1\n" \
"s_nop 0\n" \
"v_mfma_f32_16x16x32_bf16 v[244:247], a[180:183], v[120:123], v[244:247]\n" \
"v_mfma_f32_16x16x32_bf16 v[208:211], a[176:179], v[116:119], v[208:211]\n" \
"v_mfma_f32_16x16x32_bf16 v[240:243], a[176:179], v[120:123], v[240:243]\n" \
"v_mfma_f32_16x16x32_bf16 v[204:207], a[172:175], v[116:119], v[204:207]\n" \
"v_mfma_f32_16x16x32_bf16 v[236:239], a[172:175], v[120:123], v[236:239]\n" \
"v_mfma_f32_16x16x32_bf16 v[200:203], a[168:171], v[116:119], v[200:203]\n" \
"v_mfma_f32_16x16x32_bf16 v[232:235], a[168:171], v[120:123], v[232:235]\n" \
"v_mfma_f32_16x16x32_bf16 v[196:199], a[164:167], v[116:119], v[196:199]\n" \
"v_mfma_f32_16x16x32_bf16 v[228:231], a[164:167], v[120:123], v[228:231]\n" \
"v_mfma_f32_16x16x32_bf16 v[192:195], a[160:163], v[116:119], v[192:195]\n" \
"v_mfma_f32_16x16x32_bf16 v[224:227], a[160:163], v[120:123], v[224:227]\n" \
"v_smfmac_f32_16x16x64_bf16 a[196:199], a[200:203], v[116:123], v21\n" \
"s_waitcnt vmcnt(8) lgkmcnt(0)\n" \
"s_barrier\n" \
"s_cbranch_scc0 L_2_%=\n" \
"s_nop 0\n" \
"L_la_%=:\n" \
"v_mfma_f32_16x16x32_bf16 v[96:99], a[128:131], a[64:67], v[32:35]\n" \
"v_readlane_b32 s96, v11, 1\n" \
"v_bfe_u32 v27, v20, 2, 7\n" \
"v_mfma_f32_16x16x32_bf16 v[96:99], a[132:135], a[68:71], v[96:99]\n" \
"s_xor_b32 m0, s65, 0x12000\n" \
"ds_read_b128 a[160:163], v49 offset:0\n" \
"v_mfma_f32_16x16x32_bf16 v[96:99], a[136:139], a[72:75], v[96:99]\n" \
"buffer_load_dword v26, v27, s[56:59], s96 offen sc1\n" \
"v_mfma_f32_16x16x32_bf16 v[96:99], a[140:143], a[76:79], v[96:99]\n" \
"buffer_load_dwordx4 v50, s[8:11], s64 offen offset:0 sc1 lds\n" \
"v_mfma_f32_16x16x32_bf16 v[100:103], a[156:159], a[76:79], v[32:35]\n" \
"ds_read_b128 a[164:167], v49 offset:1024\n" \
"ds_read_b128 a[168:171], v49 offset:2048\n" \
"v_mfma_f32_16x16x32_bf16 v[100:103], a[152:155], a[72:75], v[100:103]\n" \
"ds_read_b128 a[172:175], v49 offset:3072\n" \
"ds_read_b128 a[176:179], v49 offset:4096\n" \
"v_mfma_f32_16x16x32_bf16 v[100:103], a[148:151], a[68:71], v[100:103]\n" \
"ds_read_b128 a[180:183], v49 offset:5120\n" \
"ds_read_b128 a[184:187], v49 offset:6144\n" \
"v_mfma_f32_16x16x32_bf16 v[100:103], a[144:147], a[64:67], v[100:103]\n" \
"ds_read_b128 a[188:191], v49 offset:7168\n" \
"v_readlane_b32 s97, v11, 36\n" \
"v_mfma_f32_16x16x32_bf16 v[108:111], a[144:147], a[80:83], v[36:39]\n" \
"v_exp_f32_e64 v86, v86\n" \
"v_mfma_f32_16x16x32_bf16 v[108:111], a[148:151], a[84:87], v[108:111]\n" \
"v_exp_f32_e64 v87, v87\n" \
"v_mfma_f32_16x16x32_bf16 v[108:111], a[152:155], a[88:91], v[108:111]\n" \
"buffer_load_dwordx4 v50, s[8:11], s64 offen offset:1024 sc1 lds\n" \
"v_mfma_f32_16x16x32_bf16 v[108:111], a[156:159], a[92:95], v[108:111]\n" \
"v_exp_f32_e64 v84, v84\n" \
"v_mfma_f32_16x16x32_bf16 v[104:107], a[140:143], a[92:95], v[36:39]\n" \
"v_exp_f32_e64 v85, v85\n" \
"v_mfma_f32_16x16x32_bf16 v[104:107], a[136:139], a[88:91], v[104:107]\n" \
"v_cvt_pk_bf16_f32 v87, v86, v87\n" \
"v_cvt_pk_bf16_f32 v86, v84, v85\n" \
"v_mfma_f32_16x16x32_bf16 v[104:107], a[132:135], a[84:87], v[104:107]\n" \
"v_exp_f32_e64 v82, v82\n" \
"v_mfma_f32_16x16x32_bf16 v[104:107], a[128:131], a[80:83], v[104:107]\n" \
"v_exp_f32_e64 v83, v83\n" \
"v_mfma_f32_16x16x32_bf16 v[112:115], a[128:131], a[96:99], v[40:43]\n" \
"v_exp_f32_e64 v80, v80\n" \
"v_mfma_f32_16x16x32_bf16 v[112:115], a[132:135], a[100:103], v[112:115]\n" \
"v_exp_f32_e64 v81, v81\n" \
"v_mfma_f32_16x16x32_bf16 v[112:115], a[136:139], a[104:107], v[112:115]\n" \
"buffer_load_dwordx4 v50, s[8:11], s64 offen offset:2048 sc1 lds\n" \
"v_mfma_f32_16x16x32_bf16 v[112:115], a[140:143], a[108:111], v[112:115]\n" \
"v_cvt_pk_bf16_f32 v85, v82, v83\n" \
"v_cvt_pk_bf16_f32 v84, v80, v81\n" \
"v_mfma_f32_16x16x32_bf16 v[116:119], a[156:159], a[108:111], v[40:43]\n" \
"v_exp_f32_e64 v88, v88\n" \
"v_mfma_f32_16x16x32_bf16 v[116:119], a[152:155], a[104:107], v[116:119]\n" \
"v_exp_f32_e64 v89, v89\n" \
"v_mfma_f32_16x16x32_bf16 v[116:119], a[148:151], a[100:103], v[116:119]\n" \
"v_exp_f32_e64 v90, v90\n" \
"v_mfma_f32_16x16x32_bf16 v[116:119], a[144:147], a[96:99], v[116:119]\n" \
"v_exp_f32_e64 v91, v91\n" \
"v_mfma_f32_16x16x32_bf16 v[124:127], a[144:147], a[112:115], v[44:47]\n" \
"v_cvt_pk_bf16_f32 v88, v88, v89\n" \
"v_cvt_pk_bf16_f32 v89, v90, v91\n" \
"v_mfma_f32_16x16x32_bf16 v[124:127], a[148:151], a[116:119], v[124:127]\n" \
"buffer_load_dwordx4 v50, s[8:11], s64 offen offset:3072 sc1 lds\n" \
"v_mfma_f32_16x16x32_bf16 v[124:127], a[152:155], a[120:123], v[124:127]\n" \
"v_exp_f32_e64 v92, v92\n" \
"v_mfma_f32_16x16x32_bf16 v[124:127], a[156:159], a[124:127], v[124:127]\n" \
"v_exp_f32_e64 v93, v93\n" \
"v_mfma_f32_16x16x32_bf16 v[120:123], a[140:143], a[124:127], v[44:47]\n" \
"v_exp_f32_e64 v94, v94\n" \
"v_mfma_f32_16x16x32_bf16 v[120:123], a[136:139], a[120:123], v[120:123]\n" \
"v_exp_f32_e64 v95, v95\n" \
"v_mfma_f32_16x16x32_bf16 v[120:123], a[132:135], a[116:119], v[120:123]\n" \
"v_cvt_pk_bf16_f32 v90, v92, v93\n" \
"v_cvt_pk_bf16_f32 v91, v94, v95\n" \
"v_mfma_f32_16x16x32_bf16 v[120:123], a[128:131], a[112:115], v[120:123]\n" \
"ds_read_b128 a[128:131], v48 offset:0\n" \
"s_waitcnt lgkmcnt(1)\n" \
"s_nop 0\n" \
"v_mfma_f32_16x16x32_bf16 v[128:131], a[160:163], v[68:71], v[128:131]\n" \
"ds_read_b128 a[144:147], v48 offset:4096\n" \
"ds_read_b128 a[132:135], v48 offset:1024\n" \
"v_mfma_f32_16x16x32_bf16 v[160:163], a[160:163], v[72:75], v[160:163]\n" \
"ds_read_b128 a[148:151], v48 offset:5120\n" \
"ds_read_b128 a[136:139], v48 offset:2048\n" \
"v_mfma_f32_16x16x32_bf16 v[132:135], a[164:167], v[68:71], v[132:135]\n" \
"ds_read_b128 a[152:155], v48 offset:6144\n" \
"ds_read_b128 a[140:143], v48 offset:3072\n" \
"v_mfma_f32_16x16x32_bf16 v[164:167], a[164:167], v[72:75], v[164:167]\n" \
"ds_read_b128 a[156:159], v48 offset:7168\n" \
"v_add_u32_e32 v17, 0x49000, v20\n" \
"v_mfma_f32_16x16x32_bf16 v[136:139], a[168:171], v[68:71], v[136:139]\n" \
"v_readlane_b32 s98, v11, 37\n" \
"v_readlane_b32 s93, v11, 20\n" \
"v_mfma_f32_16x16x32_bf16 v[168:171], a[168:171], v[72:75], v[168:171]\n" \
"v_readlane_b32 s84, v11, 15\n" \
"v_readlane_b32 s85, v11, 16\n" \
"v_mfma_f32_16x16x32_bf16 v[140:143], a[172:175], v[68:71], v[140:143]\n" \
"v_readlane_b32 s86, v11, 17\n" \
"s_mov_b32 s87, 0x20000\n" \
"v_mfma_f32_16x16x32_bf16 v[172:175], a[172:175], v[72:75], v[172:175]\n" \
"v_readlane_b32 s92, v11, 18\n" \
"v_readlane_b32 s91, v11, 21\n" \
"v_mfma_f32_16x16x32_bf16 v[144:147], a[176:179], v[68:71], v[144:147]\n" \
"v_exp_f32_e64 v102, v102\n" \
"v_mfma_f32_16x16x32_bf16 v[176:179], a[176:179], v[72:75], v[176:179]\n" \
"v_exp_f32_e64 v103, v103\n" \
"v_mfma_f32_16x16x32_bf16 v[148:151], a[180:183], v[68:71], v[148:151]\n" \
"v_exp_f32_e64 v100, v100\n" \
"v_mfma_f32_16x16x32_bf16 v[180:183], a[180:183], v[72:75], v[180:183]\n" \
"v_exp_f32_e64 v101, v101\n" \
"v_mfma_f32_16x16x32_bf16 v[152:155], a[184:187], v[68:71], v[152:155]\n" \
"v_cvt_pk_bf16_f32 v103, v102, v103\n" \
"v_cvt_pk_bf16_f32 v102, v100, v101\n" \
"v_mfma_f32_16x16x32_bf16 v[184:187], a[184:187], v[72:75], v[184:187]\n" \
"v_exp_f32_e64 v98, v98\n" \
"v_mfma_f32_16x16x32_bf16 v[156:159], a[188:191], v[68:71], v[156:159]\n" \
"v_exp_f32_e64 v99, v99\n" \
"v_mfma_f32_16x16x32_bf16 v[188:191], a[188:191], v[72:75], v[188:191]\n" \
"s_waitcnt vmcnt(4)\n" \
"v_cmp_ne_u32_e32 vcc, s62, v26\n" \
"v_smfmac_f32_16x16x64_bf16 a[192:195], a[200:203], v[68:75], v21\n" \
"v_exp_f32_e64 v96, v96\n" \
"v_mfma_f32_16x16x32_bf16 v[220:223], a[188:191], v[84:87], v[220:223]\n" \
"v_exp_f32_e64 v97, v97\n" \
"v_mfma_f32_16x16x32_bf16 v[252:255], a[188:191], v[88:91], v[252:255]\n" \
"s_cbranch_vccnz L_sol_%=\n" \
"L_rol_%=:\n" \
"v_cvt_pk_bf16_f32 v101, v98, v99\n" \
"buffer_load_dwordx4 a[220:223], v20, s[56:59], s97 offen offset:0 sc1\n" \
"v_mfma_f32_16x16x32_bf16 v[216:219], a[184:187], v[84:87], v[216:219]\n" \
"v_cvt_pk_bf16_f32 v100, v96, v97\n" \
"s_xor_b32 s65, s65, 0x8000\n" \
"v_mfma_f32_16x16x32_bf16 v[248:251], a[184:187], v[88:91], v[248:251]\n" \
"v_exp_f32_e32 v104, v104\n" \
"v_mfma_f32_16x16x32_bf16 v[212:215], a[180:183], v[84:87], v[212:215]\n" \
"v_exp_f32_e64 v105, v105\n" \
"v_mfma_f32_16x16x32_bf16 v[244:247], a[180:183], v[88:91], v[244:247]\n" \
"v_exp_f32_e64 v106, v106\n" \
"v_mfma_f32_16x16x32_bf16 v[208:211], a[176:179], v[84:87], v[208:211]\n" \
"v_exp_f32_e64 v107, v107\n" \
"v_mfma_f32_16x16x32_bf16 v[240:243], a[176:179], v[88:91], v[240:243]\n" \
"buffer_load_dwordx4 a[228:231], v17, s[56:59], s97 offen offset:0 sc1\n" \
"v_mfma_f32_16x16x32_bf16 v[204:207], a[172:175], v[84:87], v[204:207]\n" \
"v_cvt_pk_bf16_f32 v104, v104, v105\n" \
"v_cvt_pk_bf16_f32 v105, v106, v107\n" \
"v_mfma_f32_16x16x32_bf16 v[236:239], a[172:175], v[88:91], v[236:239]\n" \
"v_exp_f32_e64 v108, v108\n" \
"v_mfma_f32_16x16x32_bf16 v[200:203], a[168:171], v[84:87], v[200:203]\n" \
"v_exp_f32_e64 v109, v109\n" \
"v_mfma_f32_16x16x32_bf16 v[232:235], a[168:171], v[88:91], v[232:235]\n" \
"v_exp_f32_e64 v110, v110\n" \
"v_mfma_f32_16x16x32_bf16 v[196:199], a[164:167], v[84:87], v[196:199]\n" \
"v_exp_f32_e64 v111, v111\n" \
"v_mfma_f32_16x16x32_bf16 v[228:231], a[164:167], v[88:91], v[228:231]\n" \
"v_cvt_pk_bf16_f32 v106, v108, v109\n" \
"v_cvt_pk_bf16_f32 v107, v110, v111\n" \
"v_mfma_f32_16x16x32_bf16 v[192:195], a[160:163], v[84:87], v[192:195]\n" \
"v_exp_f32_e64 v118, v118\n" \
"v_mfma_f32_16x16x32_bf16 v[224:227], a[160:163], v[88:91], v[224:227]\n" \
"v_exp_f32_e64 v119, v119\n" \
"v_smfmac_f32_16x16x64_bf16 a[196:199], a[200:203], v[84:91], v21\n" \
"buffer_load_dwordx4 a[236:239], v20, s[56:59], s98 offen offset:0 sc1\n" \
"s_waitcnt vmcnt(8) lgkmcnt(0)\n" \
"s_barrier\n" \
"v_mfma_f32_16x16x32_bf16 v[64:67], a[128:131], a[64:67], v[32:35]\n" \
"s_add_u32 m0, s93, 0x0\n" \
"s_nop 0\n" \
"ds_read_b128 a[160:163], v49 offset:8192\n" \
"v_mfma_f32_16x16x32_bf16 v[64:67], a[132:135], a[68:71], v[64:67]\n" \
"buffer_load_dwordx4 v0, s[84:87], s92 offen offset:0 lds\n" \
"v_mfma_f32_16x16x32_bf16 v[64:67], a[136:139], a[72:75], v[64:67]\n" \
"ds_read_b128 a[164:167], v49 offset:9216\n" \
"ds_read_b128 a[168:171], v49 offset:10240\n" \
"v_mfma_f32_16x16x32_bf16 v[64:67], a[140:143], a[76:79], v[64:67]\n" \
"ds_read_b128 a[172:175], v49 offset:11264\n" \
"ds_read_b128 a[176:179], v49 offset:12288\n" \
"v_mfma_f32_16x16x32_bf16 v[68:71], a[156:159], a[76:79], v[32:35]\n" \
"ds_read_b128 a[180:183], v49 offset:13312\n" \
"ds_read_b128 a[184:187], v49 offset:14336\n" \
"v_mfma_f32_16x16x32_bf16 v[68:71], a[152:155], a[72:75], v[68:71]\n" \
"ds_read_b128 a[188:191], v49 offset:15360\n" \
"v_cvt_pk_bf16_f32 v119, v118, v119\n" \
"v_mfma_f32_16x16x32_bf16 v[68:71], a[148:151], a[68:71], v[68:71]\n" \
"buffer_load_dwordx4 v1, s[84:87], s92 offen offset:1024 lds\n" \
"v_mfma_f32_16x16x32_bf16 v[68:71], a[144:147], a[64:67], v[68:71]\n" \
"buffer_load_dwordx4 a[244:247], v17, s[56:59], s98 offen offset:0 sc1\n" \
"v_mfma_f32_16x16x32_bf16 v[76:79], a[144:147], a[80:83], v[36:39]\n" \
"v_exp_f32_e64 v116, v116\n" \
"v_mfma_f32_16x16x32_bf16 v[76:79], a[148:151], a[84:87], v[76:79]\n" \
"v_exp_f32_e64 v117, v117\n" \
"v_mfma_f32_16x16x32_bf16 v[76:79], a[152:155], a[88:91], v[76:79]\n" \
"v_exp_f32_e64 v114, v114\n" \
"v_mfma_f32_16x16x32_bf16 v[76:79], a[156:159], a[92:95], v[76:79]\n" \
"v_exp_f32_e64 v115, v115\n" \
"v_mfma_f32_16x16x32_bf16 v[72:75], a[140:143], a[92:95], v[36:39]\n" \
"v_exp_f32_e64 v112, v112\n" \
"v_mfma_f32_16x16x32_bf16 v[72:75], a[136:139], a[88:91], v[72:75]\n" \
"buffer_load_dwordx4 v2, s[84:87], s92 offen offset:2048 lds\n" \
"v_mfma_f32_16x16x32_bf16 v[72:75], a[132:135], a[84:87], v[72:75]\n" \
"v_exp_f32_e64 v113, v113\n" \
"v_mfma_f32_16x16x32_bf16 v[72:75], a[128:131], a[80:83], v[72:75]\n" \
"v_cvt_pk_bf16_f32 v118, v116, v117\n" \
"v_cvt_pk_bf16_f32 v117, v114, v115\n" \
"v_mfma_f32_16x16x32_bf16 v[80:83], a[128:131], a[96:99], v[40:43]\n" \
"buffer_load_dwordx4 a[224:227], v20, s[56:59], s97 offen offset:1024 sc1\n" \
"v_mfma_f32_16x16x32_bf16 v[80:83], a[132:135], a[100:103], v[80:83]\n" \
"v_cvt_pk_bf16_f32 v116, v112, v113\n" \
"v_add_u32_e32 v49, 0x10000, v48\n" \
"v_mfma_f32_16x16x32_bf16 v[80:83], a[136:139], a[104:107], v[80:83]\n" \
"v_exp_f32_e64 v120, v120\n" \
"v_mfma_f32_16x16x32_bf16 v[80:83], a[140:143], a[108:111], v[80:83]\n" \
"v_exp_f32_e64 v121, v121\n" \
"v_mfma_f32_16x16x32_bf16 v[84:87], a[156:159], a[108:111], v[40:43]\n" \
"buffer_load_dwordx4 v3, s[84:87], s92 offen offset:3072 lds\n" \
"v_mfma_f32_16x16x32_bf16 v[84:87], a[152:155], a[104:107], v[84:87]\n" \
"s_add_u32 m0, s91, 0x1000\n" \
"v_cvt_pk_bf16_f32 v120, v120, v121\n" \
"v_mfma_f32_16x16x32_bf16 v[84:87], a[148:151], a[100:103], v[84:87]\n" \
"v_exp_f32_e64 v122, v122\n" \
"v_mfma_f32_16x16x32_bf16 v[84:87], a[144:147], a[96:99], v[84:87]\n" \
"v_exp_f32_e64 v123, v123\n" \
"v_mfma_f32_16x16x32_bf16 v[92:95], a[144:147], a[112:115], v[44:47]\n" \
"v_exp_f32_e64 v124, v124\n" \
"v_mfma_f32_16x16x32_bf16 v[92:95], a[148:151], a[116:119], v[92:95]\n" \
"buffer_load_dwordx4 a[232:235], v17, s[56:59], s97 offen offset:1024 sc1\n" \
"v_mfma_f32_16x16x32_bf16 v[92:95], a[152:155], a[120:123], v[92:95]\n" \
"buffer_load_dwordx4 v4, s[84:87], s92 offen offset:0 lds\n" \
"v_mfma_f32_16x16x32_bf16 v[92:95], a[156:159], a[124:127], v[92:95]\n" \
"v_exp_f32_e64 v125, v125\n" \
"v_mfma_f32_16x16x32_bf16 v[88:91], a[140:143], a[124:127], v[44:47]\n" \
"v_exp_f32_e64 v126, v126\n" \
"v_mfma_f32_16x16x32_bf16 v[88:91], a[136:139], a[120:123], v[88:91]\n" \
"v_exp_f32_e64 v127, v127\n" \
"v_mfma_f32_16x16x32_bf16 v[88:91], a[132:135], a[116:119], v[88:91]\n" \
"v_cvt_pk_bf16_f32 v121, v122, v123\n" \
"v_cvt_pk_bf16_f32 v122, v124, v125\n" \
"v_mfma_f32_16x16x32_bf16 v[88:91], a[128:131], a[112:115], v[88:91]\n" \
"v_cvt_pk_bf16_f32 v123, v126, v127\n" \
"s_waitcnt lgkmcnt(0)\n" \
"s_nop 0\n" \
"v_mfma_f32_16x16x32_bf16 v[128:131], a[160:163], v[100:103], v[128:131]\n" \
"ds_read_b128 a[128:131], v48 offset:8192\n" \
"ds_read_b128 a[144:147], v48 offset:12288\n" \
"v_mfma_f32_16x16x32_bf16 v[160:163], a[160:163], v[104:107], v[160:163]\n" \
"buffer_load_dwordx4 v5, s[84:87], s92 offen offset:1024 lds\n" \
"v_mfma_f32_16x16x32_bf16 v[132:135], a[164:167], v[100:103], v[132:135]\n" \
"buffer_load_dwordx4 a[240:243], v20, s[56:59], s98 offen offset:1024 sc1\n" \
"v_mfma_f32_16x16x32_bf16 v[164:167], a[164:167], v[104:107], v[164:167]\n" \
"ds_read_b128 a[132:135], v48 offset:9216\n" \
"ds_read_b128 a[148:151], v48 offset:13312\n" \
"v_mfma_f32_16x16x32_bf16 v[136:139], a[168:171], v[100:103], v[136:139]\n" \
"ds_read_b128 a[136:139], v48 offset:10240\n" \
"ds_read_b128 a[152:155], v48 offset:14336\n" \
"v_mfma_f32_16x16x32_bf16 v[168:171], a[168:171], v[104:107], v[168:171]\n" \
"ds_read_b128 a[140:143], v48 offset:11264\n" \
"ds_read_b128 a[156:159], v48 offset:15360\n" \
"v_mfma_f32_16x16x32_bf16 v[140:143], a[172:175], v[100:103], v[140:143]\n" \
"v_exp_f32_e64 v70, v70\n" \
"v_mfma_f32_16x16x32_bf16 v[172:175], a[172:175], v[104:107], v[172:175]\n" \
"v_exp_f32_e64 v71, v71\n" \
"v_mfma_f32_16x16x32_bf16 v[144:147], a[176:179], v[100:103], v[144:147]\n" \
"buffer_load_dwordx4 v6, s[84:87], s92 offen offset:2048 lds\n" \
"v_mfma_f32_16x16x32_bf16 v[176:179], a[176:179], v[104:107], v[176:179]\n" \
"v_exp_f32_e64 v72, v72\n" \
"v_mfma_f32_16x16x32_bf16 v[148:151], a[180:183], v[100:103], v[148:151]\n" \
"v_exp_f32_e64 v73, v73\n" \
"v_mfma_f32_16x16x32_bf16 v[180:183], a[180:183], v[104:107], v[180:183]\n" \
"buffer_load_dwordx4 a[248:251], v17, s[56:59], s98 offen offset:1024 sc1\n" \
"v_mfma_f32_16x16x32_bf16 v[152:155], a[184:187], v[100:103], v[152:155]\n" \
"v_exp_f32_e64 v68, v68\n" \
"v_mfma_f32_16x16x32_bf16 v[184:187], a[184:187], v[104:107], v[184:187]\n" \
"v_exp_f32_e64 v69, v69\n" \
"v_mfma_f32_16x16x32_bf16 v[156:159], a[188:191], v[100:103], v[156:159]\n" \
"buffer_load_dwordx4 v7, s[84:87], s92 offen offset:3072 lds\n" \
"v_mfma_f32_16x16x32_bf16 v[188:191], a[188:191], v[104:107], v[188:191]\n" \
"s_add_u32 m0, s91, 0x2000\n" \
"v_cvt_pk_bf16_f32 v71, v70, v71\n" \
"v_smfmac_f32_16x16x64_bf16 a[192:195], a[200:203], v[100:107], v21\n" \
"v_exp_f32_e64 v74, v74\n" \
"v_mfma_f32_16x16x32_bf16 v[220:223], a[188:191], v[116:119], v[220:223]\n" \
"v_exp_f32_e64 v75, v75\n" \
"v_mfma_f32_16x16x32_bf16 v[252:255], a[188:191], v[120:123], v[252:255]\n" \
"v_exp_f32_e64 v66, v66\n" \
"v_mfma_f32_16x16x32_bf16 v[216:219], a[184:187], v[116:119], v[216:219]\n" \
"v_exp_f32_e64 v67, v67\n" \
"v_mfma_f32_16x16x32_bf16 v[248:251], a[184:187], v[120:123], v[248:251]\n" \
"v_exp_f32_e64 v64, v64\n" \
"v_mfma_f32_16x16x32_bf16 v[212:215], a[180:183], v[116:119], v[212:215]\n" \
"buffer_load_dwordx4 v8, s[84:87], s92 offen offset:0 lds\n" \
"v_mfma_f32_16x16x32_bf16 v[244:247], a[180:183], v[120:123], v[244:247]\n" \
"v_exp_f32_e64 v65, v65\n" \
"v_mfma_f32_16x16x32_bf16 v[208:211], a[176:179], v[116:119], v[208:211]\n" \
"v_cvt_pk_bf16_f32 v72, v72, v73\n" \
"v_cvt_pk_bf16_f32 v70, v68, v69\n" \
"v_mfma_f32_16x16x32_bf16 v[240:243], a[176:179], v[120:123], v[240:243]\n" \
"v_exp_f32_e64 v76, v76\n" \
"v_mfma_f32_16x16x32_bf16 v[204:207], a[172:175], v[116:119], v[204:207]\n" \
"v_exp_f32_e64 v77, v77\n" \
"v_mfma_f32_16x16x32_bf16 v[236:239], a[172:175], v[120:123], v[236:239]\n" \
"v_exp_f32_e64 v78, v78\n" \
"v_mfma_f32_16x16x32_bf16 v[200:203], a[168:171], v[116:119], v[200:203]\n" \
"v_exp_f32_e64 v79, v79\n" \
"v_mfma_f32_16x16x32_bf16 v[232:235], a[168:171], v[120:123], v[232:235]\n" \
"v_cvt_pk_bf16_f32 v73, v74, v75\n" \
"v_cvt_pk_bf16_f32 v69, v66, v67\n" \
"v_mfma_f32_16x16x32_bf16 v[196:199], a[164:167], v[116:119], v[196:199]\n" \
"v_cvt_pk_bf16_f32 v68, v64, v65\n" \
"v_cvt_pk_bf16_f32 v74, v76, v77\n" \
"v_mfma_f32_16x16x32_bf16 v[228:231], a[164:167], v[120:123], v[228:231]\n" \
"v_cvt_pk_bf16_f32 v75, v78, v79\n" \
"v_xor_b32_e32 v48, 0x8000, v48\n" \
"v_mfma_f32_16x16x32_bf16 v[192:195], a[160:163], v[116:119], v[192:195]\n" \
"s_add_u32 s64, s64, 0x10000\n" \
"v_mfma_f32_16x16x32_bf16 v[224:227], a[160:163], v[120:123], v[224:227]\n" \
"v_smfmac_f32_16x16x64_bf16 a[196:199], a[200:203], v[116:123], v21\n" \
"s_waitcnt vmcnt(14) lgkmcnt(0)\n" \
"s_barrier\n" \
"s_cmp_lg_u32 s16, 0\n" \
"s_cbranch_scc1 L_f1_%=\n" \
"ds_read_b128 a[160:163], v49 offset:0\n" \
"ds_read_b128 a[164:167], v49 offset:1024\n" \
"ds_read_b128 a[168:171], v49 offset:2048\n" \
"ds_read_b128 a[172:175], v49 offset:3072\n" \
"ds_read_b128 a[176:179], v49 offset:4096\n" \
"ds_read_b128 a[180:183], v49 offset:5120\n" \
"ds_read_b128 a[184:187], v49 offset:6144\n" \
"ds_read_b128 a[188:191], v49 offset:7168\n" \
"v_add_u32_e32 v54, 0x49000, v20\n" \
"buffer_load_dwordx4 a[96:99], v20, s[56:59], s60 offen offset:0 sc1\n" \
"v_add_u32_e32 v50, s15, v20\n" \
"v_add_u32_e32 v51, 0x10000, v50\n" \
"ds_read_b32 v60, v18 offset:0\n" \
"ds_read_b32 v61, v18 offset:128\n" \
"ds_read_b128 a[0:3], v49 offset:8192\n" \
"ds_read_b128 a[4:7], v49 offset:9216\n" \
"ds_read_b128 a[8:11], v49 offset:10240\n" \
"ds_read_b128 a[12:15], v49 offset:11264\n" \
"ds_read_b128 a[16:19], v49 offset:12288\n" \
"ds_read_b128 a[20:23], v49 offset:13312\n" \
"ds_read_b128 a[24:27], v49 offset:14336\n" \
"ds_read_b128 a[28:31], v49 offset:15360\n" \
"v_exp_f32_e32 v86, v86\n" \
"v_exp_f32_e32 v87, v87\n" \
"v_exp_f32_e32 v84, v84\n" \
"v_exp_f32_e32 v85, v85\n" \
"v_cvt_pk_bf16_f32 v87, v86, v87\n" \
"buffer_load_dwordx4 a[100:103], v20, s[56:59], s60 offen offset:1024 sc1\n" \
"v_exp_f32_e32 v82, v82\n" \
"v_exp_f32_e32 v83, v83\n" \
"v_exp_f32_e32 v80, v80\n" \
"v_exp_f32_e32 v81, v81\n" \
"s_waitcnt lgkmcnt(15)\n" \
"v_mfma_f32_16x16x32_bf16 v[128:131], a[160:163], v[68:71], v[128:131]\n" \
"v_cvt_pk_bf16_f32 v86, v84, v85\n" \
"v_cvt_pk_bf16_f32 v85, v82, v83\n" \
"v_mfma_f32_16x16x32_bf16 v[160:163], a[160:163], v[72:75], v[160:163]\n" \
"v_cvt_pk_bf16_f32 v84, v80, v81\n" \
"v_and_b32_e32 v84, v12, v84\n" \
"v_mfma_f32_16x16x32_bf16 v[132:135], a[164:167], v[68:71], v[132:135]\n" \
"v_and_b32_e32 v85, v13, v85\n" \
"v_and_b32_e32 v86, v14, v86\n" \
"v_mfma_f32_16x16x32_bf16 v[164:167], a[164:167], v[72:75], v[164:167]\n" \
"buffer_load_dwordx4 a[104:107], v20, s[56:59], s60 offen offset:2048 sc1\n" \
"v_mfma_f32_16x16x32_bf16 v[136:139], a[168:171], v[68:71], v[136:139]\n" \
"v_cvt_pk_bf16_f32 v128, v128, v129\n" \
"v_cvt_pk_bf16_f32 v160, v160, v161\n" \
"v_mfma_f32_16x16x32_bf16 v[168:171], a[168:171], v[72:75], v[168:171]\n" \
"v_cvt_pk_bf16_f32 v129, v130, v131\n" \
"s_waitcnt lgkmcnt(10)\n" \
"v_mfma_f32_16x16x32_bf16 v[140:143], a[172:175], v[68:71], v[140:143]\n" \
"v_cvt_pk_bf16_f32 v161, v162, v163\n" \
"v_cvt_pk_bf16_f32 v130, v132, v133\n" \
"v_mfma_f32_16x16x32_bf16 v[172:175], a[172:175], v[72:75], v[172:175]\n" \
"v_cvt_pk_bf16_f32 v131, v134, v135\n" \
"v_cvt_pk_bf16_f32 v162, v164, v165\n" \
"v_mfma_f32_16x16x32_bf16 v[144:147], a[176:179], v[68:71], v[144:147]\n" \
"v_cvt_pk_bf16_f32 v163, v166, v167\n" \
"v_cvt_pk_bf16_f32 v132, v136, v137\n" \
"v_mfma_f32_16x16x32_bf16 v[176:179], a[176:179], v[72:75], v[176:179]\n" \
"buffer_load_dwordx4 a[108:111], v20, s[56:59], s60 offen offset:3072 sc1\n" \
"v_mfma_f32_16x16x32_bf16 v[148:151], a[180:183], v[68:71], v[148:151]\n" \
"v_cvt_pk_bf16_f32 v133, v138, v139\n" \
"v_cvt_pk_bf16_f32 v134, v140, v141\n" \
"v_mfma_f32_16x16x32_bf16 v[180:183], a[180:183], v[72:75], v[180:183]\n" \
"v_cvt_pk_bf16_f32 v135, v142, v143\n" \
"v_cvt_pk_bf16_f32 v164, v168, v169\n" \
"v_mfma_f32_16x16x32_bf16 v[152:155], a[184:187], v[68:71], v[152:155]\n" \
"v_cvt_pk_bf16_f32 v165, v170, v171\n" \
"v_cvt_pk_bf16_f32 v166, v172, v173\n" \
"v_mfma_f32_16x16x32_bf16 v[184:187], a[184:187], v[72:75], v[184:187]\n" \
"v_cvt_pk_bf16_f32 v167, v174, v175\n" \
"v_cvt_pk_bf16_f32 v136, v144, v145\n" \
"v_mfma_f32_16x16x32_bf16 v[156:159], a[188:191], v[68:71], v[156:159]\n" \
"v_cvt_pk_bf16_f32 v137, v146, v147\n" \
"v_cvt_pk_bf16_f32 v138, v148, v149\n" \
"v_mfma_f32_16x16x32_bf16 v[188:191], a[188:191], v[72:75], v[188:191]\n" \
"v_cvt_pk_bf16_f32 v139, v150, v151\n" \
"v_cvt_pk_bf16_f32 v140, v152, v153\n" \
"v_smfmac_f32_16x16x64_bf16 a[192:195], a[200:203], v[68:75], v21\n" \
"v_cvt_pk_bf16_f32 v141, v154, v155\n" \
"v_cvt_pk_bf16_f32 v168, v176, v177\n" \
"v_mfma_f32_16x16x32_bf16 v[120:123], a[128:131], a[112:115], v[44:47]\n" \
"v_cvt_pk_bf16_f32 v142, v156, v157\n" \
"v_cvt_pk_bf16_f32 v143, v158, v159\n" \
"v_mfma_f32_16x16x32_bf16 v[120:123], a[132:135], a[116:119], v[120:123]\n" \
"v_cvt_pk_bf16_f32 v169, v178, v179\n" \
"v_cvt_pk_bf16_f32 v170, v180, v181\n" \
"v_cvt_pk_bf16_f32 v171, v182, v183\n" \
"v_cvt_pk_bf16_f32 v172, v184, v185\n" \
"v_cvt_pk_bf16_f32 v173, v186, v187\n" \
"v_cvt_pk_bf16_f32 v174, v188, v189\n" \
"v_cvt_pk_bf16_f32 v175, v190, v191\n" \
"ds_write_b128 v50, v[128:131] offset:0\n" \
"v_mfma_f32_16x16x32_bf16 v[120:123], a[136:139], a[120:123], v[120:123]\n" \
"v_and_b32_e32 v87, v15, v87\n" \
"v_accvgpr_read_b32 v40, a192\n" \
"v_mfma_f32_16x16x32_bf16 v[120:123], a[140:143], a[124:127], v[120:123]\n" \
"ds_write_b128 v50, v[132:135] offset:1024\n" \
"v_mfma_f32_16x16x32_bf16 v[124:127], a[156:159], a[124:127], v[44:47]\n" \
"buffer_load_dwordx4 a[124:127], v54, s[56:59], s60 offen offset:3072 sc1\n" \
"v_mfma_f32_16x16x32_bf16 v[124:127], a[152:155], a[120:123], v[124:127]\n" \
"ds_write_b128 v50, v[136:139] offset:2048\n" \
"v_mfma_f32_16x16x32_bf16 v[124:127], a[148:151], a[116:119], v[124:127]\n" \
"v_accvgpr_read_b32 v22, a193\n" \
"v_accvgpr_read_b32 v41, a194\n" \
"v_mfma_f32_16x16x32_bf16 v[124:127], a[144:147], a[112:115], v[124:127]\n" \
"ds_write_b128 v50, v[140:143] offset:3072\n" \
"v_mfma_f32_16x16x32_bf16 a[196:199], a[204:207], v[84:87], a[196:199]\n" \
"v_accvgpr_read_b32 v23, a195\n" \
"v_add_f32_e32 v40, v40, v22\n" \
"v_mfma_f32_16x16x32_bf16 v[192:195], a[160:163], v[84:87], v[192:195]\n" \
"ds_write_b128 v51, v[160:163] offset:32768\n" \
"v_mfma_f32_16x16x32_bf16 v[196:199], a[164:167], v[84:87], v[196:199]\n" \
"buffer_load_dwordx4 a[112:115], v54, s[56:59], s60 offen offset:0 sc1\n" \
"v_mfma_f32_16x16x32_bf16 v[200:203], a[168:171], v[84:87], v[200:203]\n" \
"ds_write_b128 v51, v[164:167] offset:33792\n" \
"v_add_f32_e32 v41, v41, v23\n" \
"ds_write_b64 v19, v[40:41]\n" \
"v_mfma_f32_16x16x32_bf16 v[204:207], a[172:175], v[84:87], v[204:207]\n" \
"v_lshl_add_u32 v52, s15, 2, v20\n" \
"v_add_u32_e32 v52, 0x1a000, v52\n" \
"ds_write_b128 v51, v[168:171] offset:34816\n" \
"v_mfma_f32_16x16x32_bf16 v[208:211], a[176:179], v[84:87], v[208:211]\n" \
"v_exp_f32_e32 v88, v88\n" \
"v_mfma_f32_16x16x32_bf16 v[212:215], a[180:183], v[84:87], v[212:215]\n" \
"ds_write_b128 v51, v[172:175] offset:35840\n" \
"v_mfma_f32_16x16x32_bf16 v[216:219], a[184:187], v[84:87], v[216:219]\n" \
"buffer_load_dwordx4 a[116:119], v54, s[56:59], s60 offen offset:1024 sc1\n" \
"v_mfma_f32_16x16x32_bf16 v[220:223], a[188:191], v[84:87], v[220:223]\n" \
"v_exp_f32_e32 v89, v89\n" \
"v_exp_f32_e32 v90, v90\n" \
"v_exp_f32_e32 v91, v91\n" \
"v_cvt_pk_bf16_f32 v88, v88, v89\n" \
"v_exp_f32_e32 v92, v92\n" \
"v_exp_f32_e32 v93, v93\n" \
"v_exp_f32_e32 v94, v94\n" \
"v_exp_f32_e32 v95, v95\n" \
"v_cvt_pk_bf16_f32 v89, v90, v91\n" \
"v_cvt_pk_bf16_f32 v90, v92, v93\n" \
"v_cvt_pk_bf16_f32 v91, v94, v95\n" \
"v_xor_b32_e32 v63, 0x400, v19\n" \
"buffer_load_dwordx4 a[120:123], v54, s[56:59], s60 offen offset:2048 sc1\n" \
"v_exp_f32_e32 v120, v120\n" \
"v_exp_f32_e32 v121, v121\n" \
"v_exp_f32_e32 v122, v122\n" \
"v_exp_f32_e32 v123, v123\n" \
".fill (L_ab + 12288 - .) / 4, 4, 0xBF800000\n" \
"L_ap3:\n" \
"s_cbranch_execz L_ap0\n" \
"v_cvt_pk_bf16_f32 v120, v120, v121\n" \
"v_exp_f32_e32 v124, v124\n" \
"v_exp_f32_e32 v125, v125\n" \
"s_waitcnt lgkmcnt(0)\n" \
"s_barrier\n" \
"ds_read_b128 v[144:147], v52 offset:0\n" \
"ds_read_b128 v[148:151], v52 offset:1024\n" \
"ds_read_b128 v[152:155], v52 offset:2048\n" \
"ds_read_b128 v[156:159], v52 offset:3072\n" \
"ds_read_b128 v[176:179], v52 offset:4096\n" \
"ds_read_b128 v[180:183], v52 offset:5120\n" \
"ds_read_b128 v[184:187], v52 offset:6144\n" \
"ds_read_b128 v[188:191], v52 offset:7168\n" \
"v_mfma_f32_16x16x32_bf16 a[196:199], a[212:215], v[88:91], a[196:199]\n" \
"ds_read_b64 v[42:43], v63\n" \
"v_cvt_pk_bf16_f32 v121, v122, v123\n" \
"v_mfma_f32_16x16x32_bf16 v[224:227], a[160:163], v[88:91], v[224:227]\n" \
"v_exp_f32_e32 v126, v126\n" \
"v_mfma_f32_16x16x32_bf16 v[228:231], a[164:167], v[88:91], v[228:231]\n" \
"v_exp_f32_e32 v127, v127\n" \
"v_mfma_f32_16x16x32_bf16 v[232:235], a[168:171], v[88:91], v[232:235]\n" \
"v_cvt_pk_bf16_f32 v122, v124, v125\n" \
"v_cvt_pk_bf16_f32 v123, v126, v127\n" \
"v_mfma_f32_16x16x32_bf16 v[236:239], a[172:175], v[88:91], v[236:239]\n" \
"v_and_b32_e32 v120, v12, v120\n" \
"v_and_b32_e32 v121, v13, v121\n" \
"v_mfma_f32_16x16x32_bf16 v[240:243], a[176:179], v[88:91], v[240:243]\n" \
"v_and_b32_e32 v122, v14, v122\n" \
"v_and_b32_e32 v123, v15, v123\n" \
"v_mfma_f32_16x16x32_bf16 v[244:247], a[180:183], v[88:91], v[244:247]\n" \
"v_readlane_b32 s93, v11, 22\n" \
"v_readlane_b32 s92, v11, 27\n" \
"v_mfma_f32_16x16x32_bf16 v[248:251], a[184:187], v[88:91], v[248:251]\n" \
"s_add_u32 m0, s93, 0x0\n" \
"v_mov_b32_e32 v130, v28\n" \
"v_mfma_f32_16x16x32_bf16 v[252:255], a[188:191], v[88:91], v[252:255]\n" \
"v_mov_b32_e32 v131, v29\n" \
"v_mov_b32_e32 v132, 0\n" \
"v_mov_b32_e32 v133, 0\n" \
"v_mov_b32_e32 v128, 0\n" \
"v_mov_b32_e32 v129, 0\n" \
"s_waitcnt vmcnt(8) lgkmcnt(1)\n" \
"s_barrier\n" \
"v_mfma_f32_16x16x32_bf16 a[196:199], a[212:215], v[120:123], a[196:199]\n" \
"buffer_load_dwordx4 v0, s[80:83], s92 offen offset:0 lds\n" \
"v_mfma_f32_16x16x32_bf16 v[224:227], a[0:3], v[120:123], v[224:227]\n" \
"v_mfma_f32_16x16x32_bf16 v[228:231], a[4:7], v[120:123], v[228:231]\n" \
"v_mfma_f32_16x16x32_bf16 v[232:235], a[8:11], v[120:123], v[232:235]\n" \
"v_mfma_f32_16x16x32_bf16 v[236:239], a[12:15], v[120:123], v[236:239]\n" \
"v_mfma_f32_16x16x32_bf16 v[240:243], a[16:19], v[120:123], v[240:243]\n" \
"v_mfma_f32_16x16x32_bf16 v[244:247], a[20:23], v[120:123], v[244:247]\n" \
"buffer_load_dwordx4 v1, s[80:83], s92 offen offset:1024 lds\n" \
"v_mfma_f32_16x16x32_bf16 v[248:251], a[24:27], v[120:123], v[248:251]\n" \
"v_accvgpr_read_b32 v56, a196\n" \
"v_accvgpr_read_b32 v24, a197\n" \
"v_mfma_f32_16x16x32_bf16 v[252:255], a[28:31], v[120:123], v[252:255]\n" \
"v_add_f32_e32 v56, v56, v24\n" \
"s_waitcnt lgkmcnt(0)\n" \
"v_add_f32_e32 v56, v56, v42\n" \
"v_mfma_f32_16x16x32_bf16 v[192:195], v[130:133], v[144:147], v[192:195]\n" \
"v_max_f32_e32 v62, v62, v56\n" \
"v_accvgpr_read_b32 v57, a198\n" \
"v_mfma_f32_16x16x32_bf16 v[196:199], v[128:131], v[144:147], v[196:199]\n" \
"v_rcp_f32_e32 v56, v56\n" \
"v_mfma_f32_16x16x32_bf16 v[200:203], v[130:133], v[148:151], v[200:203]\n" \
"v_mul_f32_e32 v56, v56, v60\n" \
"v_accvgpr_read_b32 v25, a199\n" \
"buffer_load_dwordx4 v2, s[80:83], s92 offen offset:2048 lds\n" \
"v_mfma_f32_16x16x32_bf16 v[204:207], v[128:131], v[148:151], v[204:207]\n" \
"v_mul_f32_e32 v192, v56, v192\n" \
"v_mul_f32_e32 v193, v56, v193\n" \
"v_mfma_f32_16x16x32_bf16 v[208:211], v[130:133], v[152:155], v[208:211]\n" \
"v_mul_f32_e32 v194, v56, v194\n" \
"v_mul_f32_e32 v195, v56, v195\n" \
"v_mfma_f32_16x16x32_bf16 v[212:215], v[128:131], v[152:155], v[212:215]\n" \
"v_mul_f32_e32 v196, v56, v196\n" \
"v_mul_f32_e32 v197, v56, v197\n" \
"v_mfma_f32_16x16x32_bf16 v[216:219], v[130:133], v[156:159], v[216:219]\n" \
"v_mul_f32_e32 v198, v56, v198\n" \
"v_mul_f32_e32 v199, v56, v199\n" \
"v_mfma_f32_16x16x32_bf16 v[220:223], v[128:131], v[156:159], v[220:223]\n" \
"v_cvt_pk_bf16_f32 v64, v192, v193\n" \
"v_cvt_pk_bf16_f32 v65, v194, v195\n" \
"v_mfma_f32_16x16x32_bf16 v[224:227], v[130:133], v[176:179], v[224:227]\n" \
"buffer_load_dwordx4 v3, s[80:83], s92 offen offset:3072 lds\n" \
"s_add_u32 m0, s93, 0x1000\n" \
"v_mfma_f32_16x16x32_bf16 v[228:231], v[128:131], v[176:179], v[228:231]\n" \
"v_cvt_pk_bf16_f32 v66, v196, v197\n" \
"v_cvt_pk_bf16_f32 v67, v198, v199\n" \
"v_mfma_f32_16x16x32_bf16 v[232:235], v[130:133], v[180:183], v[232:235]\n" \
"v_mul_f32_e32 v200, v56, v200\n" \
"v_mul_f32_e32 v201, v56, v201\n" \
"v_mfma_f32_16x16x32_bf16 v[236:239], v[128:131], v[180:183], v[236:239]\n" \
"v_mfma_f32_16x16x32_bf16 a[208:211], a[216:219], v[64:67], a[208:211]\n" \
"v_mul_f32_e32 v202, v56, v202\n" \
"v_mul_f32_e32 v203, v56, v203\n" \
"v_mfma_f32_16x16x32_bf16 v[240:243], v[130:133], v[184:187], v[240:243]\n" \
"v_mul_f32_e32 v204, v56, v204\n" \
"v_mul_f32_e32 v205, v56, v205\n" \
"buffer_load_dwordx4 v4, s[80:83], s92 offen offset:0 lds\n" \
"v_mul_f32_e32 v206, v56, v206\n" \
"v_mul_f32_e32 v207, v56, v207\n" \
"v_add_f32_e32 v57, v57, v25\n" \
"v_cvt_pk_bf16_f32 v68, v200, v201\n" \
"v_cvt_pk_bf16_f32 v69, v202, v203\n" \
"v_cvt_pk_bf16_f32 v70, v204, v205\n" \
"v_cvt_pk_bf16_f32 v71, v206, v207\n" \
"v_add_f32_e32 v57, v57, v43\n" \
"v_mul_f32_e32 v208, v56, v208\n" \
"v_mfma_f32_16x16x32_bf16 a[208:211], a[216:219], v[68:71], a[208:211]\n" \
"v_mul_f32_e32 v209, v56, v209\n" \
"v_mul_f32_e32 v210, v56, v210\n" \
"v_mul_f32_e32 v211, v56, v211\n" \
"v_mul_f32_e32 v212, v56, v212\n" \
"v_mul_f32_e32 v213, v56, v213\n" \
"v_mul_f32_e32 v214, v56, v214\n" \
"v_mul_f32_e32 v215, v56, v215\n" \
"v_max_f32_e32 v62, v62, v57\n" \
"v_cvt_pk_bf16_f32 v72, v208, v209\n" \
"v_cvt_pk_bf16_f32 v73, v210, v211\n" \
"buffer_load_dwordx4 v5, s[80:83], s92 offen offset:1024 lds\n" \
"v_cvt_pk_bf16_f32 v74, v212, v213\n" \
"v_cvt_pk_bf16_f32 v75, v214, v215\n" \
"v_rcp_f32_e32 v57, v57\n" \
"v_mfma_f32_16x16x32_bf16 v[244:247], v[128:131], v[184:187], v[244:247]\n" \
"v_mfma_f32_16x16x32_bf16 a[208:211], a[216:219], v[72:75], a[208:211]\n" \
"v_mul_f32_e32 v216, v56, v216\n" \
"v_mul_f32_e32 v217, v56, v217\n" \
"v_mul_f32_e32 v218, v56, v218\n" \
"v_mul_f32_e32 v219, v56, v219\n" \
"v_mul_f32_e32 v220, v56, v220\n" \
"v_mul_f32_e32 v221, v56, v221\n" \
"v_mul_f32_e32 v222, v56, v222\n" \
"v_mul_f32_e32 v223, v56, v223\n" \
"v_cvt_pk_bf16_f32 v76, v216, v217\n" \
"v_cvt_pk_bf16_f32 v77, v218, v219\n" \
"v_cvt_pk_bf16_f32 v78, v220, v221\n" \
"buffer_load_dwordx4 v6, s[80:83], s92 offen offset:2048 lds\n" \
"v_cvt_pk_bf16_f32 v79, v222, v223\n" \
"v_mul_f32_e32 v57, v57, v61\n" \
"v_mul_f32_e32 v224, v57, v224\n" \
"v_mfma_f32_16x16x32_bf16 a[208:211], a[216:219], v[76:79], a[208:211]\n" \
"v_mul_f32_e32 v225, v57, v225\n" \
"v_mul_f32_e32 v226, v57, v226\n" \
"v_mul_f32_e32 v227, v57, v227\n" \
"v_mul_f32_e32 v228, v57, v228\n" \
"v_mul_f32_e32 v229, v57, v229\n" \
"v_mul_f32_e32 v230, v57, v230\n" \
"v_mul_f32_e32 v231, v57, v231\n" \
"v_cvt_pk_bf16_f32 v80, v224, v225\n" \
"v_cvt_pk_bf16_f32 v81, v226, v227\n" \
"v_cvt_pk_bf16_f32 v82, v228, v229\n" \
"v_cvt_pk_bf16_f32 v83, v230, v231\n" \
"v_mfma_f32_16x16x32_bf16 v[248:251], v[130:133], v[188:191], v[248:251]\n" \
"v_mul_f32_e32 v232, v57, v232\n" \
"v_mul_f32_e32 v233, v57, v233\n" \
"v_mfma_f32_16x16x32_bf16 a[208:211], a[216:219], v[80:83], a[208:211]\n" \
"buffer_load_dwordx4 v7, s[80:83], s92 offen offset:3072 lds\n" \
"s_add_u32 m0, s93, 0x2000\n" \
"v_mul_f32_e32 v234, v57, v234\n" \
"v_mul_f32_e32 v235, v57, v235\n" \
"v_mul_f32_e32 v236, v57, v236\n" \
"v_mul_f32_e32 v237, v57, v237\n" \
"v_mul_f32_e32 v238, v57, v238\n" \
"v_mul_f32_e32 v239, v57, v239\n" \
"v_cvt_pk_bf16_f32 v84, v232, v233\n" \
"v_cvt_pk_bf16_f32 v85, v234, v235\n" \
"v_cvt_pk_bf16_f32 v86, v236, v237\n" \
"v_cvt_pk_bf16_f32 v87, v238, v239\n" \
"v_mul_f32_e32 v240, v57, v240\n" \
"v_mul_f32_e32 v241, v57, v241\n" \
"v_mfma_f32_16x16x32_bf16 a[208:211], a[216:219], v[84:87], a[208:211]\n" \
"v_mul_f32_e32 v242, v57, v242\n" \
"v_mul_f32_e32 v243, v57, v243\n" \
"v_mul_f32_e32 v244, v57, v244\n" \
"v_mul_f32_e32 v245, v57, v245\n" \
"v_mul_f32_e32 v246, v57, v246\n" \
"v_mul_f32_e32 v247, v57, v247\n" \
"buffer_load_dwordx4 v8, s[80:83], s92 offen offset:0 lds\n" \
"v_cvt_pk_bf16_f32 v96, v240, v241\n" \
"v_cvt_pk_bf16_f32 v97, v242, v243\n" \
"v_cvt_pk_bf16_f32 v98, v244, v245\n" \
"v_cvt_pk_bf16_f32 v99, v246, v247\n" \
"v_mfma_f32_16x16x32_bf16 v[252:255], v[128:131], v[188:191], v[252:255]\n" \
"v_mul_f32_e32 v248, v57, v248\n" \
"v_mfma_f32_16x16x32_bf16 a[208:211], a[216:219], v[96:99], a[208:211]\n" \
"v_mul_f32_e32 v249, v57, v249\n" \
"v_mul_f32_e32 v250, v57, v250\n" \
"v_mul_f32_e32 v251, v57, v251\n" \
"v_cvt_pk_bf16_f32 v100, v248, v249\n" \
"v_cvt_pk_bf16_f32 v101, v250, v251\n" \
"v_cmp_lt_f32_e64 s[72:73], s21, v62\n" \
"v_mul_f32_e32 v252, v57, v252\n" \
"v_mul_f32_e32 v253, v57, v253\n" \
"v_mul_f32_e32 v254, v57, v254\n" \
"v_mul_f32_e32 v255, v57, v255\n" \
"v_cvt_pk_bf16_f32 v102, v252, v253\n" \
"v_cvt_pk_bf16_f32 v103, v254, v255\n" \
"s_nop 1\n" \
"v_mfma_f32_16x16x32_bf16 a[208:211], a[216:219], v[100:103], a[208:211]\n" \
"s_nop 7\n" \
"v_accvgpr_read_b32 v63, a208\n" \
"v_cmp_u_f32_e32 vcc, v63, v63\n" \
"s_nop 0\n" \
"s_or_b64 vcc, s[72:73], vcc\n" \
"s_cbranch_scc0 L_out_%=\n" \
"s_branch L_end_%=\n" \
"L_skb_%=:\n" \
"s_sleep 1\n" \
"buffer_load_dword v57, v61, s[56:59], s96 offen sc1\n" \
"s_add_u32 s101, s101, 1\n" \
"s_waitcnt vmcnt(0)\n" \
"v_cmp_ne_u32_e32 vcc, s62, v57\n" \
"s_cmp_gt_u32 s101, 0x100000\n" \
"s_cbranch_scc1 L_rkb_%=\n" \
"s_nop 2\n" \
"s_cbranch_vccnz L_skb_%=\n" \
"s_branch L_rkb_%=\n" \
"L_sol_%=:\n" \
"s_sleep 1\n" \
"buffer_load_dword v26, v27, s[56:59], s96 offen sc1\n" \
"s_add_u32 s101, s101, 1\n" \
"s_waitcnt vmcnt(0)\n" \
"v_cmp_ne_u32_e32 vcc, s62, v26\n" \
"s_cmp_gt_u32 s101, 0x100000\n" \
"s_cbranch_scc1 L_rol_%=\n" \
"s_nop 2\n" \
"s_cbranch_vccnz L_sol_%=\n" \
"s_branch L_rol_%=\n" \
"L_sos_%=:\n" \
"s_sleep 1\n" \
"buffer_load_dword v26, v27, s[56:59], s96 offen sc1\n" \
"s_add_u32 s101, s101, 1\n" \
"s_waitcnt vmcnt(0)\n" \
"v_cmp_ne_u32_e32 vcc, s62, v26\n" \
"s_cmp_gt_u32 s101, 0x100000\n" \
"s_cbranch_scc1 L_ros_%=\n" \
"s_nop 2\n" \
"s_cbranch_vccnz L_sos_%=\n" \
"s_branch L_ros_%=\n" \
"L_sa_%=:\n" \
"s_sleep 1\n" \
"buffer_load_dword v26, v38, s[56:59], s64 offen sc1\n" \
"s_add_u32 s101, s101, 1\n" \
"s_waitcnt vmcnt(0)\n" \
"v_cmp_ne_u32_e32 vcc, s62, v26\n" \
"s_cmp_gt_u32 s101, 0x100000\n" \
"s_cbranch_scc1 L_ra_%=\n" \
"s_nop 2\n" \
"s_cbranch_vccnz L_sa_%=\n" \
"s_branch L_ra_%=\n" \
"L_sb_%=:\n" \
"s_sleep 1\n" \
"buffer_load_dword v47, v38, s[56:59], s71 offen sc1\n" \
"s_add_u32 s101, s101, 1\n" \
"s_waitcnt vmcnt(0)\n" \
"v_cmp_ne_u32_e32 vcc, s62, v47\n" \
"s_cmp_gt_u32 s101, 0x100000\n" \
"s_cbranch_scc1 L_rb_%=\n" \
"s_nop 2\n" \
"s_cbranch_vccnz L_sb_%=\n" \
"s_branch L_rb_%=\n" \
"L_sx0_%=:\n" \
"s_sleep 1\n" \
"buffer_load_dword v37, v40, s[56:59], s74 offen sc1\n" \
"s_add_u32 s101, s101, 1\n" \
"s_waitcnt vmcnt(0)\n" \
"v_cmp_eq_u32_e64 s[98:99], s62, v37\n" \
"s_cmp_gt_u32 s101, 0x100000\n" \
"s_cbranch_scc1 L_rx0_%=\n" \
"s_or_b32 s75, 0x7f, s100\n" \
"s_andn2_b32 s75, s75, s98\n" \
"s_cbranch_scc1 L_sx0_%=\n" \
"s_branch L_rx0_%=\n" \
"L_sx1_%=:\n" \
"s_sleep 1\n" \
"buffer_load_dword v37, v40, s[56:59], s74 offen sc1\n" \
"s_add_u32 s101, s101, 1\n" \
"s_waitcnt vmcnt(0)\n" \
"v_cmp_eq_u32_e64 s[98:99], s62, v37\n" \
"s_cmp_gt_u32 s101, 0x100000\n" \
"s_cbranch_scc1 L_rx1_%=\n" \
"s_andn2_b32 s75, 0x100, s98\n" \
"s_cbranch_scc1 L_sx1_%=\n" \
"s_branch L_rx1_%=\n" \
"L_sx2_%=:\n" \
"s_sleep 1\n" \
"buffer_load_dword v37, v40, s[56:59], s74 offen sc1\n" \
"s_add_u32 s101, s101, 1\n" \
"s_waitcnt vmcnt(0)\n" \
"v_cmp_eq_u32_e64 s[98:99], s62, v37\n" \
"s_cmp_gt_u32 s101, 0x100000\n" \
"s_cbranch_scc1 L_rx2_%=\n" \
"s_andn2_b32 s75, 0x200, s98\n" \
"s_cbranch_scc1 L_sx2_%=\n" \
"s_branch L_rx2_%=\n" \
"L_sx3_%=:\n" \
"s_sleep 1\n" \
"buffer_load_dword v37, v40, s[56:59], s74 offen sc1\n" \
"s_add_u32 s101, s101, 1\n" \
"s_waitcnt vmcnt(0)\n" \
"v_cmp_eq_u32_e64 s[98:99], s62, v37\n" \
"s_cmp_gt_u32 s101, 0x100000\n" \
"s_cbranch_scc1 L_rx3_%=\n" \
"s_andn2_b32 s75, 0x400, s98\n" \
"s_cbranch_scc1 L_sx3_%=\n" \
"s_branch L_rx3_%=\n" \
"L_sx4_%=:\n" \
"s_sleep 1\n" \
"buffer_load_dword v37, v40, s[56:59], s74 offen sc1\n" \
"s_add_u32 s101, s101, 1\n" \
"s_waitcnt vmcnt(0)\n" \
"v_cmp_eq_u32_e64 s[98:99], s62, v37\n" \
"s_cmp_gt_u32 s101, 0x100000\n" \
"s_cbranch_scc1 L_rx4_%=\n" \
"s_andn2_b32 s75, 0x800, s98\n" \
"s_cbranch_scc1 L_sx4_%=\n" \
"s_branch L_rx4_%=\n" \
"L_sx5_%=:\n" \
"s_sleep 1\n" \
"buffer_load_dword v37, v40, s[56:59], s74 offen sc1\n" \
"s_add_u32 s101, s101, 1\n" \
"s_waitcnt vmcnt(0)\n" \
"v_cmp_eq_u32_e64 s[98:99], s62, v37\n" \
"s_cmp_gt_u32 s101, 0x100000\n" \
"s_cbranch_scc1 L_rx5_%=\n" \
"s_andn2_b32 s75, 0x1000, s98\n" \
"s_cbranch_scc1 L_sx5_%=\n" \
"s_branch L_rx5_%=\n" \
"L_sx6_%=:\n" \
"s_sleep 1\n" \
"buffer_load_dword v37, v40, s[56:59], s74 offen sc1\n" \
"s_add_u32 s101, s101, 1\n" \
"s_waitcnt vmcnt(0)\n" \
"v_cmp_eq_u32_e64 s[98:99], s62, v37\n" \
"s_cmp_gt_u32 s101, 0x100000\n" \
"s_cbranch_scc1 L_rx6_%=\n" \
"s_andn2_b32 s75, 0x2000, s98\n" \
"s_cbranch_scc1 L_sx6_%=\n" \
"s_branch L_rx6_%=\n" \
"L_sx7_%=:\n" \
"s_sleep 1\n" \
"buffer_load_dword v37, v40, s[56:59], s74 offen sc1\n" \
"s_add_u32 s101, s101, 1\n" \
"s_waitcnt vmcnt(0)\n" \
"v_cmp_eq_u32_e64 s[98:99], s62, v37\n" \
"s_cmp_gt_u32 s101, 0x100000\n" \
"s_cbranch_scc1 L_rx7_%=\n" \
"s_andn2_b32 s75, 0x4000, s98\n" \
"s_cbranch_scc1 L_sx7_%=\n" \
"s_branch L_rx7_%=\n" \
"L_att_stub:\n" \
"s_mov_b32 s101, 0\n" \
"v_add_u32_e32 v32, 0x49000, v20\n" \
"v_readlane_b32 s96, v11, 36\n" \
"v_readlane_b32 s97, v11, 37\n" \
"buffer_load_dwordx4 a[96:99], v20, s[56:59], s60 offen offset:0 sc1\n" \
"buffer_load_dwordx4 a[112:115], v32, s[56:59], s60 offen offset:0 sc1\n" \
"buffer_load_dwordx4 a[100:103], v20, s[56:59], s60 offen offset:1024 sc1\n" \
"buffer_load_dwordx4 a[116:119], v32, s[56:59], s60 offen offset:1024 sc1\n" \
"buffer_load_dwordx4 a[104:107], v20, s[56:59], s60 offen offset:2048 sc1\n" \
"buffer_load_dwordx4 a[120:123], v32, s[56:59], s60 offen offset:2048 sc1\n" \
"buffer_load_dwordx4 a[108:111], v20, s[56:59], s60 offen offset:3072 sc1\n" \
"buffer_load_dwordx4 a[124:127], v32, s[56:59], s60 offen offset:3072 sc1\n" \
"buffer_load_dwordx4 a[220:223], v20, s[56:59], s96 offen offset:0 sc1\n" \
"buffer_load_dwordx4 a[228:231], v32, s[56:59], s96 offen offset:0 sc1\n" \
"buffer_load_dwordx4 a[236:239], v20, s[56:59], s97 offen offset:0 sc1\n" \
"buffer_load_dwordx4 a[244:247], v32, s[56:59], s97 offen offset:0 sc1\n" \
"buffer_load_dwordx4 a[224:227], v20, s[56:59], s96 offen offset:1024 sc1\n" \
"buffer_load_dwordx4 a[232:235], v32, s[56:59], s96 offen offset:1024 sc1\n" \
"buffer_load_dwordx4 a[240:243], v20, s[56:59], s97 offen offset:1024 sc1\n" \
"buffer_load_dwordx4 a[248:251], v32, s[56:59], s97 offen offset:1024 sc1\n" \
"buffer_load_dwordx4 v[64:67], v20, s[56:59], s61 offen offset:0 sc1\n" \
"buffer_load_dwordx4 v[68:71], v20, s[56:59], s61 offen offset:1024 sc1\n" \
"buffer_load_dwordx4 v[72:75], v20, s[56:59], s61 offen offset:2048 sc1\n" \
"buffer_load_dwordx4 v[76:79], v20, s[56:59], s61 offen offset:3072 sc1\n" \
"buffer_load_dwordx4 v[80:83], v32, s[56:59], s61 offen offset:0 sc1\n" \
"buffer_load_dwordx4 v[84:87], v32, s[56:59], s61 offen offset:1024 sc1\n" \
"buffer_load_dwordx4 v[96:99], v32, s[56:59], s61 offen offset:2048 sc1\n" \
"buffer_load_dwordx4 v[100:103], v32, s[56:59], s61 offen offset:3072 sc1\n" \
"v_not_b32_e32 v26, s62\n" \
"s_waitcnt vmcnt(0)\n" \
"s_branch L_out_%=\n" \
".fill (L_ab + 16384 - .) / 4, 4, 0xBF800000\n" \
".fill (L_ab + 16384 - .) / 4, 4, 0xBF800000\n" \
"L_ap4:\n" \
"s_setpc_b64 s[88:89]\n" \
"L_gsw:\n" \
"v_mfma_f32_16x16x32_bf16 v[104:107], a[128:131], a[16:19], v[36:39]\n" \
"s_add_u32 s68, s60, 0xfff6e000\n" \
"s_add_u32 s69, s60, 0xfffb7000\n" \
"s_add_u32 s70, s60, 0x0\n" \
"s_add_u32 s71, s60, 0x49000\n" \
"v_mfma_f32_16x16x32_bf16 v[104:107], a[132:135], a[20:23], v[104:107]\n" \
"s_xor_b32 m0, s65, 0x12000\n" \
"ds_read_b128 a[160:163], v49 offset:0\n" \
"v_mfma_f32_16x16x32_bf16 v[104:107], a[136:139], a[24:27], v[104:107]\n" \
"buffer_load_dwordx4 v50, s[8:11], s64 offen offset:0 sc1 lds\n" \
"v_mfma_f32_16x16x32_bf16 v[104:107], a[140:143], a[28:31], v[104:107]\n" \
"ds_read_b128 a[164:167], v49 offset:1024\n" \
"ds_read_b128 a[168:171], v49 offset:2048\n" \
"v_mfma_f32_16x16x32_bf16 v[108:111], a[156:159], a[28:31], v[36:39]\n" \
"ds_read_b128 a[172:175], v49 offset:3072\n" \
"ds_read_b128 a[176:179], v49 offset:4096\n" \
"v_mfma_f32_16x16x32_bf16 v[108:111], a[152:155], a[24:27], v[108:111]\n" \
"buffer_load_dwordx4 v50, s[8:11], s64 offen offset:1024 sc1 lds\n" \
"v_mfma_f32_16x16x32_bf16 v[108:111], a[148:151], a[20:23], v[108:111]\n" \
"s_mov_b32 s65, s13\n" \
"v_add_u32_e32 v63, s15, v20\n" \
"v_mfma_f32_16x16x32_bf16 v[108:111], a[144:147], a[16:19], v[108:111]\n" \
"s_add_u32 s67, s14, 40960\n" \
"v_add_u32_e32 v50, 0x6000, v63\n" \
"v_mfma_f32_16x16x32_bf16 v[116:119], a[144:147], a[32:35], v[40:43]\n" \
"s_xor_b32 m0, s65, 0x8000\n" \
"ds_read_b128 a[180:183], v49 offset:5120\n" \
"v_mfma_f32_16x16x32_bf16 v[116:119], a[148:151], a[36:39], v[116:119]\n" \
"ds_read_b128 a[184:187], v49 offset:6144\n" \
"v_and_b32_e32 v68, v12, v68\n" \
"v_mfma_f32_16x16x32_bf16 v[116:119], a[152:155], a[40:43], v[116:119]\n" \
"v_and_b32_e32 v69, v13, v69\n" \
"v_and_b32_e32 v70, v14, v70\n" \
"v_mfma_f32_16x16x32_bf16 v[116:119], a[156:159], a[44:47], v[116:119]\n" \
"buffer_load_dwordx4 v50, s[8:11], s67 offen offset:0 sc1 lds\n" \
"v_mfma_f32_16x16x32_bf16 v[112:115], a[140:143], a[44:47], v[40:43]\n" \
"v_and_b32_e32 v71, v15, v71\n" \
"ds_read_b128 a[188:191], v49 offset:7168\n" \
"v_mfma_f32_16x16x32_bf16 v[112:115], a[136:139], a[40:43], v[112:115]\n" \
"v_exp_f32_e32 v86, v86\n" \
"v_mfma_f32_16x16x32_bf16 v[112:115], a[132:135], a[36:39], v[112:115]\n" \
"v_exp_f32_e32 v87, v87\n" \
"v_mfma_f32_16x16x32_bf16 v[112:115], a[128:131], a[32:35], v[112:115]\n" \
"v_exp_f32_e32 v84, v84\n" \
"v_mfma_f32_16x16x32_bf16 v[120:123], a[128:131], a[48:51], v[44:47]\n" \
"buffer_load_dwordx4 v50, s[8:11], s67 offen offset:1024 sc1 lds\n" \
"v_mfma_f32_16x16x32_bf16 v[120:123], a[132:135], a[52:55], v[120:123]\n" \
"v_exp_f32_e32 v85, v85\n" \
"v_mfma_f32_16x16x32_bf16 v[120:123], a[136:139], a[56:59], v[120:123]\n" \
"v_cvt_pk_bf16_f32 v87, v86, v87\n" \
"v_cvt_pk_bf16_f32 v86, v84, v85\n" \
"v_mfma_f32_16x16x32_bf16 v[120:123], a[140:143], a[60:63], v[120:123]\n" \
"v_exp_f32_e32 v82, v82\n" \
"v_mfma_f32_16x16x32_bf16 v[124:127], a[156:159], a[60:63], v[44:47]\n" \
"v_exp_f32_e32 v83, v83\n" \
"v_mfma_f32_16x16x32_bf16 v[124:127], a[152:155], a[56:59], v[124:127]\n" \
"v_exp_f32_e32 v80, v80\n" \
"v_mfma_f32_16x16x32_bf16 v[124:127], a[148:151], a[52:55], v[124:127]\n" \
"buffer_load_dwordx4 v50, s[8:11], s67 offen offset:2048 sc1 lds\n" \
"v_mfma_f32_16x16x32_bf16 v[124:127], a[144:147], a[48:51], v[124:127]\n" \
"v_cvt_pk_bf16_f32 v85, v82, v83\n" \
"s_waitcnt lgkmcnt(1)\n" \
"v_mfma_f32_16x16x32_bf16 v[128:131], a[160:163], v[68:71], v[128:131]\n" \
"v_exp_f32_e32 v81, v81\n" \
"v_mfma_f32_16x16x32_bf16 v[160:163], a[160:163], v[72:75], v[160:163]\n" \
"v_cvt_pk_bf16_f32 v84, v80, v81\n" \
"s_mov_b32 s64, s14\n" \
"v_mfma_f32_16x16x32_bf16 v[132:135], a[164:167], v[68:71], v[132:135]\n" \
"v_exp_f32_e32 v88, v88\n" \
"v_mfma_f32_16x16x32_bf16 v[164:167], a[164:167], v[72:75], v[164:167]\n" \
"v_exp_f32_e32 v89, v89\n" \
"v_mfma_f32_16x16x32_bf16 v[136:139], a[168:171], v[68:71], v[136:139]\n" \
"buffer_load_dwordx4 v50, s[8:11], s67 offen offset:3072 sc1 lds\n" \
"v_mfma_f32_16x16x32_bf16 v[168:171], a[168:171], v[72:75], v[168:171]\n" \
"v_exp_f32_e32 v90, v90\n" \
"v_mfma_f32_16x16x32_bf16 v[140:143], a[172:175], v[68:71], v[140:143]\n" \
"v_exp_f32_e32 v91, v91\n" \
"v_mfma_f32_16x16x32_bf16 v[172:175], a[172:175], v[72:75], v[172:175]\n" \
"v_cvt_pk_bf16_f32 v88, v88, v89\n" \
"v_cvt_pk_bf16_f32 v89, v90, v91\n" \
"v_mfma_f32_16x16x32_bf16 v[144:147], a[176:179], v[68:71], v[144:147]\n" \
"v_exp_f32_e32 v92, v92\n" \
"v_mfma_f32_16x16x32_bf16 v[176:179], a[176:179], v[72:75], v[176:179]\n" \
"v_exp_f32_e32 v93, v93\n" \
"v_mfma_f32_16x16x32_bf16 v[148:151], a[180:183], v[68:71], v[148:151]\n" \
"v_exp_f32_e32 v94, v94\n" \
"v_mfma_f32_16x16x32_bf16 v[180:183], a[180:183], v[72:75], v[180:183]\n" \
"v_exp_f32_e32 v95, v95\n" \
"v_mfma_f32_16x16x32_bf16 v[152:155], a[184:187], v[68:71], v[152:155]\n" \
"v_cvt_pk_bf16_f32 v90, v92, v93\n" \
"v_cvt_pk_bf16_f32 v91, v94, v95\n" \
"v_mfma_f32_16x16x32_bf16 v[184:187], a[184:187], v[72:75], v[184:187]\n" \
"v_add_u32_e32 v51, 0x12000, v63\n" \
"s_waitcnt lgkmcnt(0)\n" \
"v_mfma_f32_16x16x32_bf16 v[156:159], a[188:191], v[68:71], v[156:159]\n" \
"s_xor_b32 m0, s65, 0xa000\n" \
"ds_read_b32 v58, v18 offset:512\n" \
"v_mfma_f32_16x16x32_bf16 v[188:191], a[188:191], v[72:75], v[188:191]\n" \
"buffer_load_dwordx4 v51, s[8:11], s64 offen offset:0 sc1 lds\n" \
"v_smfmac_f32_16x16x64_bf16 a[192:195], a[200:203], v[68:75], v21\n" \
"buffer_load_dwordx4 v51, s[8:11], s64 offen offset:1024 sc1 lds\n" \
"v_mfma_f32_16x16x32_bf16 v[220:223], a[188:191], v[84:87], v[220:223]\n" \
"ds_read_b32 v59, v18 offset:640\n" \
"ds_read_b32 v60, v18 offset:768\n" \
"v_mfma_f32_16x16x32_bf16 v[252:255], a[188:191], v[88:91], v[252:255]\n" \
"ds_read_b32 v61, v18 offset:896\n" \
"ds_read_b128 a[128:131], v48 offset:0\n" \
"v_mfma_f32_16x16x32_bf16 v[216:219], a[184:187], v[84:87], v[216:219]\n" \
"ds_read_b128 a[144:147], v48 offset:4096\n" \
"ds_read_b128 a[132:135], v48 offset:1024\n" \
"v_mfma_f32_16x16x32_bf16 v[248:251], a[184:187], v[88:91], v[248:251]\n" \
"ds_read_b128 a[148:151], v48 offset:5120\n" \
"ds_read_b128 a[136:139], v48 offset:2048\n" \
"v_mfma_f32_16x16x32_bf16 v[212:215], a[180:183], v[84:87], v[212:215]\n" \
"ds_read_b128 a[152:155], v48 offset:6144\n" \
"ds_read_b128 a[140:143], v48 offset:3072\n" \
"v_mfma_f32_16x16x32_bf16 v[244:247], a[180:183], v[88:91], v[244:247]\n" \
"ds_read_b128 a[156:159], v48 offset:7168\n" \
"v_add_u32_e32 v52, 0x14000, v63\n" \
"v_mfma_f32_16x16x32_bf16 v[208:211], a[176:179], v[84:87], v[208:211]\n" \
"buffer_load_dwordx4 v51, s[8:11], s64 offen offset:2048 sc1 lds\n" \
"v_mfma_f32_16x16x32_bf16 v[240:243], a[176:179], v[88:91], v[240:243]\n" \
"v_exp_f32_e32 v104, v104\n" \
"v_mfma_f32_16x16x32_bf16 v[204:207], a[172:175], v[84:87], v[204:207]\n" \
"v_exp_f32_e32 v105, v105\n" \
"v_mfma_f32_16x16x32_bf16 v[236:239], a[172:175], v[88:91], v[236:239]\n" \
"buffer_load_dwordx4 v51, s[8:11], s64 offen offset:3072 sc1 lds\n" \
"v_mfma_f32_16x16x32_bf16 v[200:203], a[168:171], v[84:87], v[200:203]\n" \
"v_exp_f32_e32 v106, v106\n" \
"v_mfma_f32_16x16x32_bf16 v[232:235], a[168:171], v[88:91], v[232:235]\n" \
"v_exp_f32_e32 v107, v107\n" \
"v_mfma_f32_16x16x32_bf16 v[196:199], a[164:167], v[84:87], v[196:199]\n" \
"v_cvt_pk_bf16_f32 v104, v104, v105\n" \
"v_cvt_pk_bf16_f32 v105, v106, v107\n" \
"v_mfma_f32_16x16x32_bf16 v[228:231], a[164:167], v[88:91], v[228:231]\n" \
"v_exp_f32_e32 v108, v108\n" \
"v_mfma_f32_16x16x32_bf16 v[192:195], a[160:163], v[84:87], v[192:195]\n" \
"v_exp_f32_e32 v109, v109\n" \
"v_mfma_f32_16x16x32_bf16 v[224:227], a[160:163], v[88:91], v[224:227]\n" \
"v_exp_f32_e32 v110, v110\n" \
"v_smfmac_f32_16x16x64_bf16 a[196:199], a[200:203], v[84:91], v21\n" \
"v_exp_f32_e32 v111, v111\n" \
"v_cvt_pk_bf16_f32 v106, v108, v109\n" \
"v_cvt_pk_bf16_f32 v107, v110, v111\n" \
"v_and_b32_e32 v104, v12, v104\n" \
"v_and_b32_e32 v105, v13, v105\n" \
"v_and_b32_e32 v106, v14, v106\n" \
"v_and_b32_e32 v107, v15, v107\n" \
"v_accvgpr_read_b32 v54, a192\n" \
"v_accvgpr_read_b32 v22, a193\n" \
"v_add_u32_e32 v53, 0x20000, v63\n" \
"v_exp_f32_e32 v118, v118\n" \
"v_exp_f32_e32 v119, v119\n" \
"v_exp_f32_e32 v116, v116\n" \
"v_exp_f32_e32 v117, v117\n" \
"v_cvt_pk_bf16_f32 v119, v118, v119\n" \
"v_exp_f32_e32 v114, v114\n" \
"v_exp_f32_e32 v115, v115\n" \
"v_exp_f32_e32 v112, v112\n" \
"v_exp_f32_e32 v113, v113\n" \
"v_cvt_pk_bf16_f32 v118, v116, v117\n" \
"v_cvt_pk_bf16_f32 v117, v114, v115\n" \
"v_cvt_pk_bf16_f32 v116, v112, v113\n" \
"v_readlane_b32 s93, v11, 19\n" \
"s_waitcnt vmcnt(10) lgkmcnt(0)\n" \
"s_barrier\n" \
"v_mfma_f32_16x16x32_bf16 v[80:83], a[128:131], a[32:35], v[40:43]\n" \
"ds_read_b128 a[160:163], v49 offset:8192\n" \
"ds_read_b128 a[164:167], v49 offset:9216\n" \
"v_mfma_f32_16x16x32_bf16 v[80:83], a[132:135], a[36:39], v[80:83]\n" \
"ds_read_b128 a[168:171], v49 offset:10240\n" \
"s_xor_b32 m0, s65, 0x18000\n" \
"v_mfma_f32_16x16x32_bf16 v[80:83], a[136:139], a[40:43], v[80:83]\n" \
"buffer_load_dwordx4 v52, s[8:11], s64 offen offset:0 sc1 lds\n" \
"v_mfma_f32_16x16x32_bf16 v[80:83], a[140:143], a[44:47], v[80:83]\n" \
"ds_read_b128 a[172:175], v49 offset:11264\n" \
"ds_read_b128 a[176:179], v49 offset:12288\n" \
"v_mfma_f32_16x16x32_bf16 v[84:87], a[156:159], a[44:47], v[40:43]\n" \
"ds_read_b128 a[180:183], v49 offset:13312\n" \
"ds_read_b128 a[184:187], v49 offset:14336\n" \
"v_mfma_f32_16x16x32_bf16 v[84:87], a[152:155], a[40:43], v[84:87]\n" \
"buffer_load_dwordx4 v52, s[8:11], s64 offen offset:1024 sc1 lds\n" \
"v_mfma_f32_16x16x32_bf16 v[84:87], a[148:151], a[36:39], v[84:87]\n" \
"ds_read_b128 a[188:191], v49 offset:15360\n" \
"v_readlane_b32 s80, v11, 11\n" \
"v_mfma_f32_16x16x32_bf16 v[84:87], a[144:147], a[32:35], v[84:87]\n" \
"buffer_load_dwordx4 v52, s[8:11], s64 offen offset:2048 sc1 lds\n" \
"v_mfma_f32_16x16x32_bf16 v[92:95], a[144:147], a[48:51], v[44:47]\n" \
"v_exp_f32_e32 v120, v120\n" \
"v_mfma_f32_16x16x32_bf16 v[92:95], a[148:151], a[52:55], v[92:95]\n" \
"v_exp_f32_e32 v121, v121\n" \
"v_mfma_f32_16x16x32_bf16 v[92:95], a[152:155], a[56:59], v[92:95]\n" \
"buffer_load_dwordx4 v52, s[8:11], s64 offen offset:3072 sc1 lds\n" \
"v_mfma_f32_16x16x32_bf16 v[92:95], a[156:159], a[60:63], v[92:95]\n" \
"s_mov_b32 m0, s65\n" \
"v_cvt_pk_bf16_f32 v120, v120, v121\n" \
"v_mfma_f32_16x16x32_bf16 v[88:91], a[140:143], a[60:63], v[44:47]\n" \
"v_exp_f32_e32 v122, v122\n" \
"v_mfma_f32_16x16x32_bf16 v[88:91], a[136:139], a[56:59], v[88:91]\n" \
"buffer_load_dwordx4 v53, s[8:11], s64 offen offset:0 sc1 lds\n" \
"v_mfma_f32_16x16x32_bf16 v[88:91], a[132:135], a[52:55], v[88:91]\n" \
"v_exp_f32_e32 v123, v123\n" \
"v_mfma_f32_16x16x32_bf16 v[88:91], a[128:131], a[48:51], v[88:91]\n" \
"v_exp_f32_e32 v124, v124\n" \
"v_mfma_f32_16x16x32_bf16 a[192:195], a[212:215], v[104:107], a[192:195]\n" \
"v_cvt_pk_bf16_f32 v121, v122, v123\n" \
"s_waitcnt lgkmcnt(5)\n" \
"v_mfma_f32_16x16x32_bf16 v[160:163], a[160:163], v[104:107], v[160:163]\n" \
"buffer_load_dwordx4 v53, s[8:11], s64 offen offset:1024 sc1 lds\n" \
"v_mfma_f32_16x16x32_bf16 v[164:167], a[164:167], v[104:107], v[164:167]\n" \
"v_exp_f32_e32 v125, v125\n" \
"v_mfma_f32_16x16x32_bf16 v[168:171], a[168:171], v[104:107], v[168:171]\n" \
"v_cvt_pk_bf16_f32 v122, v124, v125\n" \
"s_waitcnt lgkmcnt(1)\n" \
"v_mfma_f32_16x16x32_bf16 v[172:175], a[172:175], v[104:107], v[172:175]\n" \
"v_exp_f32_e32 v126, v126\n" \
"v_mfma_f32_16x16x32_bf16 v[176:179], a[176:179], v[104:107], v[176:179]\n" \
"v_exp_f32_e32 v127, v127\n" \
"v_mfma_f32_16x16x32_bf16 v[180:183], a[180:183], v[104:107], v[180:183]\n" \
"v_cvt_pk_bf16_f32 v123, v126, v127\n" \
"v_readlane_b32 s81, v11, 12\n" \
"v_mfma_f32_16x16x32_bf16 v[184:187], a[184:187], v[104:107], v[184:187]\n" \
"s_mov_b32 s82, 0x47fca0\n" \
"s_waitcnt lgkmcnt(0)\n" \
"v_mfma_f32_16x16x32_bf16 v[188:191], a[188:191], v[104:107], v[188:191]\n" \
"buffer_load_dwordx4 v53, s[8:11], s64 offen offset:2048 sc1 lds\n" \
"v_mfma_f32_16x16x32_bf16 v[220:223], a[188:191], v[116:119], v[220:223]\n" \
"buffer_load_dwordx4 v53, s[8:11], s64 offen offset:3072 sc1 lds\n" \
"v_mfma_f32_16x16x32_bf16 v[252:255], a[188:191], v[120:123], v[252:255]\n" \
"s_mov_b32 s83, 0x20000\n" \
"v_readlane_b32 s92, v11, 26\n" \
"v_mfma_f32_16x16x32_bf16 v[216:219], a[184:187], v[116:119], v[216:219]\n" \
"s_add_u32 m0, s93, 0x0\n" \
"ds_read_b128 a[128:131], v48 offset:8192\n" \
"v_mfma_f32_16x16x32_bf16 v[248:251], a[184:187], v[120:123], v[248:251]\n" \
"ds_read_b128 a[144:147], v48 offset:12288\n" \
"ds_read_b128 a[132:135], v48 offset:9216\n" \
"v_mfma_f32_16x16x32_bf16 v[212:215], a[180:183], v[116:119], v[212:215]\n" \
"buffer_load_dwordx4 v0, s[80:83], s92 offen offset:0 lds\n" \
"v_mfma_f32_16x16x32_bf16 v[244:247], a[180:183], v[120:123], v[244:247]\n" \
"buffer_load_dwordx4 v1, s[80:83], s92 offen offset:1024 lds\n" \
"v_mfma_f32_16x16x32_bf16 v[208:211], a[176:179], v[116:119], v[208:211]\n" \
"ds_read_b128 a[148:151], v48 offset:13312\n" \
"ds_read_b128 a[136:139], v48 offset:10240\n" \
"v_mfma_f32_16x16x32_bf16 v[240:243], a[176:179], v[120:123], v[240:243]\n" \
"ds_read_b128 a[152:155], v48 offset:14336\n" \
"ds_read_b128 a[140:143], v48 offset:11264\n" \
"v_mfma_f32_16x16x32_bf16 v[204:207], a[172:175], v[116:119], v[204:207]\n" \
"buffer_load_dwordx4 v2, s[80:83], s92 offen offset:2048 lds\n" \
"v_mfma_f32_16x16x32_bf16 v[236:239], a[172:175], v[120:123], v[236:239]\n" \
"ds_read_b128 a[156:159], v48 offset:15360\n" \
"s_xor_b32 s65, s65, 0x8000\n" \
"v_mfma_f32_16x16x32_bf16 v[200:203], a[168:171], v[116:119], v[200:203]\n" \
"s_add_u32 s64, s64, 0x10000\n" \
"v_add_u32_e32 v49, 0x10000, v48\n" \
"v_mfma_f32_16x16x32_bf16 v[232:235], a[168:171], v[120:123], v[232:235]\n" \
"buffer_load_dwordx4 v3, s[80:83], s92 offen offset:3072 lds\n" \
"v_mfma_f32_16x16x32_bf16 v[196:199], a[164:167], v[116:119], v[196:199]\n" \
"v_exp_f32_e32 v86, v86\n" \
"v_mfma_f32_16x16x32_bf16 v[228:231], a[164:167], v[120:123], v[228:231]\n" \
"v_exp_f32_e32 v87, v87\n" \
"v_mfma_f32_16x16x32_bf16 v[192:195], a[160:163], v[116:119], v[192:195]\n" \
"v_exp_f32_e32 v84, v84\n" \
"v_mfma_f32_16x16x32_bf16 v[224:227], a[160:163], v[120:123], v[224:227]\n" \
"v_exp_f32_e32 v85, v85\n" \
"v_smfmac_f32_16x16x64_bf16 a[196:199], a[200:203], v[116:123], v21\n" \
"v_exp_f32_e32 v88, v88\n" \
"v_exp_f32_e32 v89, v89\n" \
"v_cvt_pk_bf16_f32 v87, v86, v87\n" \
"v_exp_f32_e32 v82, v82\n" \
"v_exp_f32_e32 v83, v83\n" \
"v_exp_f32_e32 v80, v80\n" \
"v_exp_f32_e32 v81, v81\n" \
"v_exp_f32_e32 v90, v90\n" \
"v_exp_f32_e32 v91, v91\n" \
"v_cvt_pk_bf16_f32 v86, v84, v85\n" \
"v_cvt_pk_bf16_f32 v88, v88, v89\n" \
"v_exp_f32_e32 v92, v92\n" \
"v_exp_f32_e32 v93, v93\n" \
"v_exp_f32_e32 v94, v94\n" \
"v_exp_f32_e32 v95, v95\n" \
"v_cvt_pk_bf16_f32 v85, v82, v83\n" \
"v_cvt_pk_bf16_f32 v84, v80, v81\n" \
"v_cvt_pk_bf16_f32 v89, v90, v91\n" \
"v_and_b32_e32 v84, v12, v84\n" \
"v_and_b32_e32 v85, v13, v85\n" \
"v_and_b32_e32 v86, v14, v86\n" \
"v_and_b32_e32 v87, v15, v87\n" \
"v_cvt_pk_bf16_f32 v90, v92, v93\n" \
"v_cvt_pk_bf16_f32 v91, v94, v95\n" \
"v_add_u32_e32 v48, s16, v20\n" \
"v_add_f32_e32 v54, v54, v22\n" \
"v_accvgpr_read_b32 v55, a194\n" \
"v_accvgpr_read_b32 v23, a195\n" \
"v_mfma_f32_16x16x32_bf16 a[192:195], a[216:219], a[216:219], 0\n" \
"v_max_f32_e32 v62, v62, v54\n" \
"v_add_f32_e32 v55, v55, v23\n" \
"v_max_f32_e32 v62, v62, v55\n" \
"v_rcp_f32_e32 v54, v54\n" \
"v_rcp_f32_e32 v55, v55\n" \
"v_mul_f32_e32 v54, v54, v58\n" \
"v_mul_f32_e32 v55, v55, v59\n" \
"v_mul_f32_e32 v128, v54, v128\n" \
"v_mul_f32_e32 v129, v54, v129\n" \
"v_mul_f32_e32 v136, v54, v136\n" \
"v_mul_f32_e32 v137, v54, v137\n" \
"v_mul_f32_e32 v144, v54, v144\n" \
"v_mul_f32_e32 v145, v54, v145\n" \
"v_mul_f32_e32 v152, v54, v152\n" \
"v_mul_f32_e32 v153, v54, v153\n" \
"v_mul_f32_e32 v160, v55, v160\n" \
"v_mul_f32_e32 v161, v55, v161\n" \
"v_mul_f32_e32 v168, v55, v168\n" \
"v_mul_f32_e32 v169, v55, v169\n" \
"v_mul_f32_e32 v176, v55, v176\n" \
"v_mul_f32_e32 v177, v55, v177\n" \
"v_mul_f32_e32 v184, v55, v184\n" \
"v_mul_f32_e32 v185, v55, v185\n" \
"v_cvt_pk_bf16_f32 v128, v128, v129\n" \
"v_mul_f32_e32 v130, v54, v130\n" \
"v_mul_f32_e32 v131, v54, v131\n" \
"v_cvt_pk_bf16_f32 v136, v136, v137\n" \
"v_mul_f32_e32 v138, v54, v138\n" \
"v_mul_f32_e32 v139, v54, v139\n" \
"v_cvt_pk_bf16_f32 v144, v144, v145\n" \
"v_mul_f32_e32 v146, v54, v146\n" \
"v_mul_f32_e32 v147, v54, v147\n" \
"v_cvt_pk_bf16_f32 v152, v152, v153\n" \
"v_mul_f32_e32 v154, v54, v154\n" \
"v_mul_f32_e32 v155, v54, v155\n" \
"v_cvt_pk_bf16_f32 v160, v160, v161\n" \
"v_mul_f32_e32 v162, v55, v162\n" \
"v_mul_f32_e32 v163, v55, v163\n" \
"v_cvt_pk_bf16_f32 v168, v168, v169\n" \
"v_mul_f32_e32 v170, v55, v170\n" \
"v_mul_f32_e32 v171, v55, v171\n" \
"v_cvt_pk_bf16_f32 v176, v176, v177\n" \
"v_mul_f32_e32 v178, v55, v178\n" \
"v_mul_f32_e32 v179, v55, v179\n" \
"v_cvt_pk_bf16_f32 v184, v184, v185\n" \
"v_mul_f32_e32 v186, v55, v186\n" \
"v_mul_f32_e32 v187, v55, v187\n" \
"v_cvt_pk_bf16_f32 v129, v130, v131\n" \
"v_mul_f32_e32 v132, v54, v132\n" \
"v_mul_f32_e32 v133, v54, v133\n" \
"v_mul_f32_e32 v134, v54, v134\n" \
"v_mul_f32_e32 v135, v54, v135\n" \
"v_cvt_pk_bf16_f32 v137, v138, v139\n" \
"v_mul_f32_e32 v140, v54, v140\n" \
"s_waitcnt vmcnt(12) lgkmcnt(0)\n" \
"s_barrier\n" \
"v_mfma_f32_16x16x32_bf16 v[120:123], a[128:131], a[48:51], v[44:47]\n" \
"s_xor_b32 m0, s65, 0x12000\n" \
"ds_read_b128 a[188:191], v49 offset:7168\n" \
"v_mfma_f32_16x16x32_bf16 v[120:123], a[132:135], a[52:55], v[120:123]\n" \
"buffer_load_dwordx4 v50, s[8:11], s64 offen offset:0 sc1 lds\n" \
"v_mfma_f32_16x16x32_bf16 v[120:123], a[136:139], a[56:59], v[120:123]\n" \
"ds_read_b128 a[184:187], v49 offset:6144\n" \
"ds_read_b128 a[180:183], v49 offset:5120\n" \
"v_mfma_f32_16x16x32_bf16 v[120:123], a[140:143], a[60:63], v[120:123]\n" \
"buffer_load_dwordx4 v50, s[8:11], s64 offen offset:1024 sc1 lds\n" \
"v_mfma_f32_16x16x32_bf16 v[124:127], a[156:159], a[60:63], v[44:47]\n" \
"ds_read_b128 a[176:179], v49 offset:4096\n" \
"ds_read_b128 a[172:175], v49 offset:3072\n" \
"v_mfma_f32_16x16x32_bf16 v[124:127], a[152:155], a[56:59], v[124:127]\n" \
"ds_read_b128 a[168:171], v49 offset:2048\n" \
"ds_read_b128 a[164:167], v49 offset:1024\n" \
"v_mfma_f32_16x16x32_bf16 v[124:127], a[148:151], a[52:55], v[124:127]\n" \
"ds_read_b128 a[160:163], v49 offset:0\n" \
"ds_read_b128 a[0:3], v49 offset:8192\n" \
"v_mfma_f32_16x16x32_bf16 v[124:127], a[144:147], a[48:51], v[124:127]\n" \
"buffer_load_dwordx4 v50, s[8:11], s64 offen offset:2048 sc1 lds\n" \
"v_mfma_f32_16x16x32_bf16 v[32:35], a[220:223], a[64:67], 0\n" \
"ds_read_b128 a[4:7], v49 offset:9216\n" \
"ds_read_b128 a[8:11], v49 offset:10240\n" \
"v_mfma_f32_16x16x32_bf16 v[36:39], a[220:223], a[80:83], 0\n" \
"ds_read_b128 a[12:15], v49 offset:11264\n" \
"ds_read_b128 a[16:19], v49 offset:12288\n" \
"v_mfma_f32_16x16x32_bf16 v[40:43], a[220:223], a[96:99], 0\n" \
"ds_read_b128 a[20:23], v49 offset:13312\n" \
"ds_read_b128 a[24:27], v49 offset:14336\n" \
"v_mfma_f32_16x16x32_bf16 v[44:47], a[220:223], a[112:115], 0\n" \
"buffer_load_dwordx4 v50, s[8:11], s64 offen offset:3072 sc1 lds\n" \
"v_mfma_f32_16x16x32_bf16 v[32:35], a[224:227], a[68:71], v[32:35]\n" \
"s_xor_b32 m0, s65, 0xa000\n" \
"ds_read_b128 a[28:31], v49 offset:15360\n" \
"v_mfma_f32_16x16x32_bf16 v[36:39], a[224:227], a[84:87], v[36:39]\n" \
"v_exp_f32_e32 v120, v120\n" \
"v_mfma_f32_16x16x32_bf16 v[40:43], a[224:227], a[100:103], v[40:43]\n" \
"buffer_load_dwordx4 v51, s[8:11], s64 offen offset:0 sc1 lds\n" \
"v_mfma_f32_16x16x32_bf16 v[44:47], a[224:227], a[116:119], v[44:47]\n" \
"v_exp_f32_e32 v121, v121\n" \
"v_mfma_f32_16x16x32_bf16 v[32:35], a[228:231], a[72:75], v[32:35]\n" \
"v_exp_f32_e32 v122, v122\n" \
"v_mfma_f32_16x16x32_bf16 v[36:39], a[228:231], a[88:91], v[36:39]\n" \
"v_exp_f32_e32 v123, v123\n" \
"v_mfma_f32_16x16x32_bf16 v[40:43], a[228:231], a[104:107], v[40:43]\n" \
"buffer_load_dwordx4 v51, s[8:11], s64 offen offset:1024 sc1 lds\n" \
"v_mfma_f32_16x16x32_bf16 v[44:47], a[228:231], a[120:123], v[44:47]\n" \
"v_cvt_pk_bf16_f32 v120, v120, v121\n" \
"v_cvt_pk_bf16_f32 v121, v122, v123\n" \
"v_mfma_f32_16x16x32_bf16 v[32:35], a[232:235], a[76:79], v[32:35]\n" \
"v_exp_f32_e32 v124, v124\n" \
"v_mfma_f32_16x16x32_bf16 v[36:39], a[232:235], a[92:95], v[36:39]\n" \
"v_exp_f32_e32 v125, v125\n" \
"v_mfma_f32_16x16x32_bf16 v[40:43], a[232:235], a[108:111], v[40:43]\n" \
"buffer_load_dwordx4 v51, s[8:11], s64 offen offset:2048 sc1 lds\n" \
"v_mfma_f32_16x16x32_bf16 v[44:47], a[232:235], a[124:127], v[44:47]\n" \
"v_exp_f32_e32 v126, v126\n" \
"v_smfmac_f32_16x16x64_bf16 a[196:199], a[200:203], v[84:91], v21\n" \
"v_cvt_pk_bf16_f32 v122, v124, v125\n" \
"s_waitcnt lgkmcnt(1)\n" \
"v_mfma_f32_16x16x32_bf16 v[220:223], a[188:191], v[84:87], v[220:223]\n" \
"buffer_load_dwordx4 v51, s[8:11], s64 offen offset:3072 sc1 lds\n" \
"v_mfma_f32_16x16x32_bf16 v[252:255], a[188:191], v[88:91], v[252:255]\n" \
"s_add_u32 m0, s93, 0x1000\n" \
"v_and_b32_e32 v120, v12, v120\n" \
"v_mfma_f32_16x16x32_bf16 v[216:219], a[184:187], v[84:87], v[216:219]\n" \
"v_exp_f32_e32 v127, v127\n" \
"v_mfma_f32_16x16x32_bf16 v[248:251], a[184:187], v[88:91], v[248:251]\n" \
"v_cvt_pk_bf16_f32 v123, v126, v127\n" \
"v_and_b32_e32 v121, v13, v121\n" \
"v_mfma_f32_16x16x32_bf16 v[212:215], a[180:183], v[84:87], v[212:215]\n" \
"buffer_load_dwordx4 v4, s[80:83], s92 offen offset:0 lds\n" \
"v_mfma_f32_16x16x32_bf16 v[244:247], a[180:183], v[88:91], v[244:247]\n" \
"v_and_b32_e32 v122, v14, v122\n" \
"v_and_b32_e32 v123, v15, v123\n" \
"v_mfma_f32_16x16x32_bf16 v[208:211], a[176:179], v[84:87], v[208:211]\n" \
"v_accvgpr_read_b32 v56, a196\n" \
"v_accvgpr_read_b32 v24, a197\n" \
"v_mfma_f32_16x16x32_bf16 v[240:243], a[176:179], v[88:91], v[240:243]\n" \
"ds_read_b128 a[128:131], v48 offset:0\n" \
"ds_read_b128 a[144:147], v48 offset:4096\n" \
"v_mfma_f32_16x16x32_bf16 v[204:207], a[172:175], v[84:87], v[204:207]\n" \
"buffer_load_dwordx4 v5, s[80:83], s92 offen offset:1024 lds\n" \
"v_mfma_f32_16x16x32_bf16 v[236:239], a[172:175], v[88:91], v[236:239]\n" \
"ds_read_b128 a[132:135], v48 offset:1024\n" \
"ds_read_b128 a[148:151], v48 offset:5120\n" \
"v_mfma_f32_16x16x32_bf16 v[200:203], a[168:171], v[84:87], v[200:203]\n" \
"ds_read_b128 a[136:139], v48 offset:2048\n" \
"ds_read_b128 a[152:155], v48 offset:6144\n" \
"v_mfma_f32_16x16x32_bf16 v[232:235], a[168:171], v[88:91], v[232:235]\n" \
"buffer_load_dwordx4 v6, s[80:83], s92 offen offset:2048 lds\n" \
"v_mfma_f32_16x16x32_bf16 v[196:199], a[164:167], v[84:87], v[196:199]\n" \
"ds_read_b128 a[140:143], v48 offset:3072\n" \
"ds_read_b128 a[156:159], v48 offset:7168\n" \
"v_mfma_f32_16x16x32_bf16 v[228:231], a[164:167], v[88:91], v[228:231]\n" \
"v_mul_f32_e32 v141, v54, v141\n" \
"v_mul_f32_e32 v142, v54, v142\n" \
"v_mfma_f32_16x16x32_bf16 v[192:195], a[160:163], v[84:87], v[192:195]\n" \
"v_mul_f32_e32 v143, v54, v143\n" \
"v_cvt_pk_bf16_f32 v145, v146, v147\n" \
"v_mfma_f32_16x16x32_bf16 v[224:227], a[160:163], v[88:91], v[224:227]\n" \
"buffer_load_dwordx4 v7, s[80:83], s92 offen offset:3072 lds\n" \
"v_mfma_f32_16x16x32_bf16 a[196:199], a[212:215], v[120:123], a[196:199]\n" \
"v_mul_f32_e32 v148, v54, v148\n" \
"v_mul_f32_e32 v149, v54, v149\n" \
"v_mfma_f32_16x16x32_bf16 v[224:227], a[0:3], v[120:123], v[224:227]\n" \
"v_mul_f32_e32 v150, v54, v150\n" \
"v_mul_f32_e32 v151, v54, v151\n" \
"v_mfma_f32_16x16x32_bf16 v[228:231], a[4:7], v[120:123], v[228:231]\n" \
"v_cvt_pk_bf16_f32 v153, v154, v155\n" \
"v_mul_f32_e32 v156, v54, v156\n" \
"v_mfma_f32_16x16x32_bf16 v[232:235], a[8:11], v[120:123], v[232:235]\n" \
"v_mul_f32_e32 v157, v54, v157\n" \
"v_mul_f32_e32 v158, v54, v158\n" \
"v_mfma_f32_16x16x32_bf16 v[236:239], a[12:15], v[120:123], v[236:239]\n" \
"v_mul_f32_e32 v159, v54, v159\n" \
"v_cvt_pk_bf16_f32 v161, v162, v163\n" \
"v_mfma_f32_16x16x32_bf16 v[240:243], a[16:19], v[120:123], v[240:243]\n" \
"v_mul_f32_e32 v164, v55, v164\n" \
"v_mul_f32_e32 v165, v55, v165\n" \
"v_mfma_f32_16x16x32_bf16 v[244:247], a[20:23], v[120:123], v[244:247]\n" \
"v_mul_f32_e32 v166, v55, v166\n" \
"v_mul_f32_e32 v167, v55, v167\n" \
"v_mfma_f32_16x16x32_bf16 v[248:251], a[24:27], v[120:123], v[248:251]\n" \
"v_cvt_pk_bf16_f32 v169, v170, v171\n" \
"s_waitcnt lgkmcnt(4)\n" \
"v_mfma_f32_16x16x32_bf16 v[252:255], a[28:31], v[120:123], v[252:255]\n" \
"v_mul_f32_e32 v172, v55, v172\n" \
"v_mul_f32_e32 v173, v55, v173\n" \
"v_mul_f32_e32 v174, v55, v174\n" \
"v_mul_f32_e32 v175, v55, v175\n" \
"v_cvt_pk_bf16_f32 v177, v178, v179\n" \
"v_mul_f32_e32 v180, v55, v180\n" \
"v_mul_f32_e32 v181, v55, v181\n" \
"v_mul_f32_e32 v182, v55, v182\n" \
"v_mul_f32_e32 v183, v55, v183\n" \
"v_cvt_pk_bf16_f32 v185, v186, v187\n" \
"v_mul_f32_e32 v188, v55, v188\n" \
"v_mul_f32_e32 v189, v55, v189\n" \
"v_mul_f32_e32 v190, v55, v190\n" \
"v_mul_f32_e32 v191, v55, v191\n" \
"v_cvt_pk_bf16_f32 v130, v132, v133\n" \
"v_cvt_pk_bf16_f32 v131, v134, v135\n" \
"buffer_store_dwordx4 v[128:131], v20, s[56:59], s68 offen offset:0 sc1\n" \
"v_cvt_pk_bf16_f32 v138, v140, v141\n" \
"v_cvt_pk_bf16_f32 v139, v142, v143\n" \
"buffer_store_dwordx4 v[136:139], v20, s[56:59], s68 offen offset:1024 sc1\n" \
"v_cvt_pk_bf16_f32 v146, v148, v149\n" \
"v_cvt_pk_bf16_f32 v147, v150, v151\n" \
"buffer_store_dwordx4 v[144:147], v20, s[56:59], s68 offen offset:2048 sc1\n" \
"v_cvt_pk_bf16_f32 v154, v156, v157\n" \
"v_cvt_pk_bf16_f32 v155, v158, v159\n" \
"buffer_store_dwordx4 v[152:155], v20, s[56:59], s68 offen offset:3072 sc1\n" \
"v_cvt_pk_bf16_f32 v162, v164, v165\n" \
"v_cvt_pk_bf16_f32 v163, v166, v167\n" \
"buffer_store_dwordx4 v[160:163], v20, s[56:59], s69 offen offset:0 sc1\n" \
"v_cvt_pk_bf16_f32 v170, v172, v173\n" \
"v_cvt_pk_bf16_f32 v171, v174, v175\n" \
"buffer_store_dwordx4 v[168:171], v20, s[56:59], s69 offen offset:1024 sc1\n" \
"v_cvt_pk_bf16_f32 v178, v180, v181\n" \
"v_cvt_pk_bf16_f32 v179, v182, v183\n" \
"buffer_store_dwordx4 v[176:179], v20, s[56:59], s69 offen offset:2048 sc1\n" \
"v_cvt_pk_bf16_f32 v186, v188, v189\n" \
"v_cvt_pk_bf16_f32 v187, v190, v191\n" \
"buffer_store_dwordx4 v[184:187], v20, s[56:59], s69 offen offset:3072 sc1\n" \
"s_add_u32 m0, s93, 0x2000\n" \
"v_mfma_f32_16x16x32_bf16 a[208:211], a[216:219], v[128:131], a[208:211]\n" \
"buffer_load_dwordx4 v8, s[80:83], s92 offen offset:0 lds\n" \
"v_mfma_f32_16x16x32_bf16 a[208:211], a[216:219], v[136:139], a[208:211]\n" \
"v_mfma_f32_32x32x16_bf16 v[128:143], a[216:219], a[216:219], 0\n" \
"v_mfma_f32_16x16x32_bf16 a[208:211], a[216:219], v[144:147], a[208:211]\n" \
"v_mfma_f32_16x16x32_bf16 a[208:211], a[216:219], v[152:155], a[208:211]\n" \
"v_mfma_f32_32x32x16_bf16 v[144:159], a[216:219], a[216:219], 0\n" \
"v_add_f32_e32 v56, v56, v24\n" \
"v_mfma_f32_16x16x32_bf16 a[208:211], a[216:219], v[160:163], a[208:211]\n" \
"v_max_f32_e32 v62, v62, v56\n" \
"v_accvgpr_read_b32 v57, a198\n" \
"s_waitcnt vmcnt(21) lgkmcnt(0)\n" \
"s_barrier\n" \
"v_mfma_f32_16x16x32_bf16 v[64:67], a[128:131], a[64:67], v[32:35]\n" \
"s_xor_b32 m0, s65, 0x18000\n" \
"v_accvgpr_read_b32 v25, a199\n" \
"v_mfma_f32_16x16x32_bf16 a[196:199], a[216:219], a[216:219], 0\n" \
"buffer_load_dwordx4 v52, s[8:11], s64 offen offset:0 sc1 lds\n" \
"v_mfma_f32_16x16x32_bf16 v[64:67], a[132:135], a[68:71], v[64:67]\n" \
"v_rcp_f32_e32 v56, v56\n" \
"v_mfma_f32_16x16x32_bf16 v[64:67], a[136:139], a[72:75], v[64:67]\n" \
"v_mfma_f32_16x16x32_bf16 a[208:211], a[216:219], v[168:171], a[208:211]\n" \
"v_mfma_f32_32x32x16_bf16 v[160:175], a[216:219], a[216:219], 0\n" \
"v_mul_f32_e32 v56, v56, v60\n" \
"v_mfma_f32_16x16x32_bf16 a[208:211], a[216:219], v[176:179], a[208:211]\n" \
"v_mul_f32_e32 v192, v56, v192\n" \
"v_mul_f32_e32 v193, v56, v193\n" \
"v_mfma_f32_16x16x32_bf16 v[64:67], a[140:143], a[76:79], v[64:67]\n" \
"buffer_load_dwordx4 v52, s[8:11], s64 offen offset:1024 sc1 lds\n" \
"v_mfma_f32_16x16x32_bf16 v[68:71], a[156:159], a[76:79], v[32:35]\n" \
"v_cvt_pk_bf16_f32 v192, v192, v193\n" \
"v_mul_f32_e32 v194, v56, v194\n" \
"v_mfma_f32_16x16x32_bf16 v[68:71], a[152:155], a[72:75], v[68:71]\n" \
"v_mul_f32_e32 v195, v56, v195\n" \
"v_cvt_pk_bf16_f32 v193, v194, v195\n" \
"v_mfma_f32_16x16x32_bf16 v[68:71], a[148:151], a[68:71], v[68:71]\n" \
"v_mfma_f32_16x16x32_bf16 a[208:211], a[216:219], v[184:187], a[208:211]\n" \
"v_mfma_f32_32x32x16_bf16 v[176:191], a[216:219], a[216:219], 0\n" \
"v_mul_f32_e32 v196, v56, v196\n" \
"v_mul_f32_e32 v197, v56, v197\n" \
"v_mul_f32_e32 v198, v56, v198\n" \
"v_mul_f32_e32 v199, v56, v199\n" \
"v_mul_f32_e32 v200, v56, v200\n" \
"v_mul_f32_e32 v201, v56, v201\n" \
"v_mfma_f32_16x16x32_bf16 v[68:71], a[144:147], a[64:67], v[68:71]\n" \
"buffer_load_dwordx4 v52, s[8:11], s64 offen offset:2048 sc1 lds\n" \
"v_mfma_f32_16x16x32_bf16 v[76:79], a[144:147], a[80:83], v[36:39]\n" \
"v_cvt_pk_bf16_f32 v194, v196, v197\n" \
"v_cvt_pk_bf16_f32 v195, v198, v199\n" \
"buffer_store_dwordx4 v[192:195], v20, s[56:59], s70 offen offset:0 sc1\n" \
"v_mfma_f32_16x16x32_bf16 v[76:79], a[148:151], a[84:87], v[76:79]\n" \
"v_cvt_pk_bf16_f32 v200, v200, v201\n" \
"v_mul_f32_e32 v202, v56, v202\n" \
"v_mfma_f32_16x16x32_bf16 v[76:79], a[152:155], a[88:91], v[76:79]\n" \
"v_mul_f32_e32 v203, v56, v203\n" \
"v_add_f32_e32 v57, v57, v25\n" \
"v_mfma_f32_16x16x32_bf16 v[76:79], a[156:159], a[92:95], v[76:79]\n" \
"buffer_load_dwordx4 v52, s[8:11], s64 offen offset:3072 sc1 lds\n" \
"v_mfma_f32_16x16x32_bf16 v[72:75], a[140:143], a[92:95], v[36:39]\n" \
"s_mov_b32 m0, s65\n" \
"v_cvt_pk_bf16_f32 v201, v202, v203\n" \
"v_mfma_f32_16x16x32_bf16 v[72:75], a[136:139], a[88:91], v[72:75]\n" \
"v_mfma_f32_16x16x32_bf16 a[208:211], a[216:219], v[192:195], a[208:211]\n" \
"v_mul_f32_e32 v204, v56, v204\n" \
"v_mul_f32_e32 v205, v56, v205\n" \
"v_mfma_f32_16x16x32_bf16 v[72:75], a[132:135], a[84:87], v[72:75]\n" \
"v_mul_f32_e32 v206, v56, v206\n" \
"v_mul_f32_e32 v207, v56, v207\n" \
"v_mfma_f32_16x16x32_bf16 v[72:75], a[128:131], a[80:83], v[72:75]\n" \
"buffer_load_dwordx4 v53, s[8:11], s64 offen offset:0 sc1 lds\n" \
"v_mfma_f32_16x16x32_bf16 v[80:83], a[128:131], a[96:99], v[40:43]\n" \
"v_mul_f32_e32 v208, v56, v208\n" \
"v_mul_f32_e32 v209, v56, v209\n" \
"v_mfma_f32_16x16x32_bf16 v[80:83], a[132:135], a[100:103], v[80:83]\n" \
"v_max_f32_e32 v62, v62, v57\n" \
"v_cvt_pk_bf16_f32 v202, v204, v205\n" \
"v_mfma_f32_16x16x32_bf16 v[80:83], a[136:139], a[104:107], v[80:83]\n" \
"buffer_load_dwordx4 v53, s[8:11], s64 offen offset:1024 sc1 lds\n" \
"v_mfma_f32_16x16x32_bf16 v[80:83], a[140:143], a[108:111], v[80:83]\n" \
"v_cvt_pk_bf16_f32 v203, v206, v207\n" \
"v_cvt_pk_bf16_f32 v208, v208, v209\n" \
"buffer_store_dwordx4 v[200:203], v20, s[56:59], s70 offen offset:1024 sc1\n" \
"v_mfma_f32_16x16x32_bf16 v[84:87], a[156:159], a[108:111], v[40:43]\n" \
"v_mul_f32_e32 v210, v56, v210\n" \
"v_mul_f32_e32 v211, v56, v211\n" \
"v_mfma_f32_16x16x32_bf16 v[84:87], a[152:155], a[104:107], v[84:87]\n" \
"v_rcp_f32_e32 v57, v57\n" \
"v_mfma_f32_16x16x32_bf16 v[84:87], a[148:151], a[100:103], v[84:87]\n" \
"buffer_load_dwordx4 v53, s[8:11], s64 offen offset:2048 sc1 lds\n" \
"v_mfma_f32_16x16x32_bf16 v[84:87], a[144:147], a[96:99], v[84:87]\n" \
"v_mfma_f32_16x16x32_bf16 a[208:211], a[216:219], v[200:203], a[208:211]\n" \
"v_mfma_f32_32x32x16_bf16 v[192:207], a[216:219], a[216:219], 0\n" \
"v_cvt_pk_bf16_f32 v209, v210, v211\n" \
"v_mul_f32_e32 v212, v56, v212\n" \
".fill (L_ab + 20480 - .) / 4, 4, 0xBF800000\n" \
"L_ap5:\n" \
"s_cbranch_execz L_ap0\n" \
"v_mul_f32_e32 v213, v56, v213\n" \
"v_mul_f32_e32 v214, v56, v214\n" \
"v_mul_f32_e32 v215, v56, v215\n" \
"v_mul_f32_e32 v216, v56, v216\n" \
"v_mfma_f32_16x16x32_bf16 v[92:95], a[144:147], a[112:115], v[44:47]\n" \
"ds_read_b128 a[144:147], v48 offset:12288\n" \
"v_mul_f32_e32 v217, v56, v217\n" \
"v_mfma_f32_16x16x32_bf16 v[92:95], a[148:151], a[116:119], v[92:95]\n" \
"ds_read_b128 a[148:151], v48 offset:13312\n" \
"v_cvt_pk_bf16_f32 v210, v212, v213\n" \
"v_mfma_f32_16x16x32_bf16 v[92:95], a[152:155], a[120:123], v[92:95]\n" \
"ds_read_b128 a[152:155], v48 offset:14336\n" \
"v_cvt_pk_bf16_f32 v211, v214, v215\n" \
"buffer_store_dwordx4 v[208:211], v20, s[56:59], s70 offen offset:2048 sc1\n" \
"v_mfma_f32_16x16x32_bf16 v[92:95], a[156:159], a[124:127], v[92:95]\n" \
"ds_read_b128 a[156:159], v48 offset:15360\n" \
"v_cvt_pk_bf16_f32 v216, v216, v217\n" \
"v_mfma_f32_16x16x32_bf16 v[88:91], a[140:143], a[124:127], v[44:47]\n" \
"ds_read_b128 a[140:143], v48 offset:11264\n" \
"v_mul_f32_e32 v218, v56, v218\n" \
"v_mfma_f32_16x16x32_bf16 v[88:91], a[136:139], a[120:123], v[88:91]\n" \
"ds_read_b128 a[136:139], v48 offset:10240\n" \
"v_mul_f32_e32 v219, v56, v219\n" \
"v_mfma_f32_16x16x32_bf16 v[88:91], a[132:135], a[116:119], v[88:91]\n" \
"ds_read_b128 a[132:135], v48 offset:9216\n" \
"v_mul_f32_e32 v57, v57, v61\n" \
"v_mfma_f32_16x16x32_bf16 v[88:91], a[128:131], a[112:115], v[88:91]\n" \
"ds_read_b128 a[128:131], v48 offset:8192\n" \
"v_mfma_f32_16x16x32_bf16 a[208:211], a[216:219], v[208:211], a[208:211]\n" \
"v_cvt_pk_bf16_f32 v217, v218, v219\n" \
"v_mul_f32_e32 v220, v56, v220\n" \
"v_mul_f32_e32 v221, v56, v221\n" \
"v_mul_f32_e32 v222, v56, v222\n" \
"v_mul_f32_e32 v223, v56, v223\n" \
"v_mul_f32_e32 v224, v57, v224\n" \
"v_mul_f32_e32 v225, v57, v225\n" \
"v_cvt_pk_bf16_f32 v218, v220, v221\n" \
"v_cvt_pk_bf16_f32 v219, v222, v223\n" \
"buffer_store_dwordx4 v[216:219], v20, s[56:59], s70 offen offset:3072 sc1\n" \
"v_cvt_pk_bf16_f32 v224, v224, v225\n" \
"v_mul_f32_e32 v226, v57, v226\n" \
"v_mul_f32_e32 v227, v57, v227\n" \
"v_mfma_f32_16x16x32_bf16 a[208:211], a[216:219], v[216:219], a[208:211]\n" \
"v_mfma_f32_32x32x16_bf16 v[208:223], a[216:219], a[216:219], 0\n" \
"v_cvt_pk_bf16_f32 v225, v226, v227\n" \
"v_mul_f32_e32 v228, v57, v228\n" \
"v_mul_f32_e32 v229, v57, v229\n" \
"v_mul_f32_e32 v230, v57, v230\n" \
"v_mul_f32_e32 v231, v57, v231\n" \
"v_mul_f32_e32 v232, v57, v232\n" \
"v_mul_f32_e32 v233, v57, v233\n" \
"v_cvt_pk_bf16_f32 v226, v228, v229\n" \
"v_cvt_pk_bf16_f32 v227, v230, v231\n" \
"buffer_store_dwordx4 v[224:227], v20, s[56:59], s71 offen offset:0 sc1\n" \
"v_cvt_pk_bf16_f32 v232, v232, v233\n" \
"v_mul_f32_e32 v234, v57, v234\n" \
"v_mul_f32_e32 v235, v57, v235\n" \
"v_mfma_f32_16x16x32_bf16 a[208:211], a[216:219], v[224:227], a[208:211]\n" \
"v_cvt_pk_bf16_f32 v233, v234, v235\n" \
"v_mul_f32_e32 v236, v57, v236\n" \
"v_mul_f32_e32 v237, v57, v237\n" \
"v_mul_f32_e32 v238, v57, v238\n" \
"v_mul_f32_e32 v239, v57, v239\n" \
"v_mul_f32_e32 v240, v57, v240\n" \
"v_mul_f32_e32 v241, v57, v241\n" \
"v_cvt_pk_bf16_f32 v234, v236, v237\n" \
"v_cvt_pk_bf16_f32 v235, v238, v239\n" \
"buffer_store_dwordx4 v[232:235], v20, s[56:59], s71 offen offset:1024 sc1\n" \
"v_cvt_pk_bf16_f32 v240, v240, v241\n" \
"v_mul_f32_e32 v242, v57, v242\n" \
"v_mul_f32_e32 v243, v57, v243\n" \
"v_mfma_f32_16x16x32_bf16 a[208:211], a[216:219], v[232:235], a[208:211]\n" \
"v_mfma_f32_32x32x16_bf16 v[224:239], a[216:219], a[216:219], 0\n" \
"v_cvt_pk_bf16_f32 v241, v242, v243\n" \
"v_mul_f32_e32 v244, v57, v244\n" \
"v_mul_f32_e32 v245, v57, v245\n" \
"v_mul_f32_e32 v246, v57, v246\n" \
"v_mul_f32_e32 v247, v57, v247\n" \
"v_mul_f32_e32 v248, v57, v248\n" \
"v_mul_f32_e32 v249, v57, v249\n" \
"v_cvt_pk_bf16_f32 v242, v244, v245\n" \
"v_cvt_pk_bf16_f32 v243, v246, v247\n" \
"buffer_store_dwordx4 v[240:243], v20, s[56:59], s71 offen offset:2048 sc1\n" \
"v_cvt_pk_bf16_f32 v248, v248, v249\n" \
"v_mul_f32_e32 v250, v57, v250\n" \
"v_mul_f32_e32 v251, v57, v251\n" \
"v_mfma_f32_16x16x32_bf16 a[208:211], a[216:219], v[240:243], a[208:211]\n" \
"v_cvt_pk_bf16_f32 v249, v250, v251\n" \
"v_mul_f32_e32 v252, v57, v252\n" \
"v_mul_f32_e32 v253, v57, v253\n" \
"v_mul_f32_e32 v254, v57, v254\n" \
"v_mul_f32_e32 v255, v57, v255\n" \
"v_cvt_pk_bf16_f32 v250, v252, v253\n" \
"v_cvt_pk_bf16_f32 v251, v254, v255\n" \
"buffer_store_dwordx4 v[248:251], v20, s[56:59], s71 offen offset:3072 sc1\n" \
"buffer_load_dwordx4 v53, s[8:11], s64 offen offset:3072 sc1 lds\n" \
"s_waitcnt vmcnt(16) lgkmcnt(0)\n" \
"s_barrier\n" \
"v_mfma_f32_16x16x32_bf16 a[208:211], a[216:219], v[248:251], a[208:211]\n" \
"v_mfma_f32_32x32x16_bf16 v[240:255], a[216:219], a[216:219], 0\n" \
"v_exp_f32_e32 v70, v70\n" \
"v_exp_f32_e32 v71, v71\n" \
"v_exp_f32_e32 v72, v72\n" \
"v_exp_f32_e32 v73, v73\n" \
"s_add_u32 s64, s64, 0x10000\n" \
"v_cmp_lt_f32_e64 s[72:73], s21, v62\n" \
"v_readlane_b32 s95, v11, 3\n" \
"v_accvgpr_read_b32 v63, a208\n" \
"v_cmp_u_f32_e32 vcc, v63, v63\n" \
"v_exp_f32_e32 v68, v68\n" \
"v_exp_f32_e32 v69, v69\n" \
"v_exp_f32_e32 v74, v74\n" \
"v_exp_f32_e32 v75, v75\n" \
"s_or_b64 vcc, s[72:73], vcc\n" \
"s_cselect_b32 s95, 0x40000000, s95\n" \
"s_cselect_b32 s94, 0x7f800000, 0\n" \
"v_cvt_pk_bf16_f32 v71, v70, v71\n" \
"v_exp_f32_e32 v66, v66\n" \
"v_exp_f32_e32 v67, v67\n" \
"v_exp_f32_e32 v64, v64\n" \
"v_exp_f32_e32 v65, v65\n" \
"v_cvt_pk_bf16_f32 v72, v72, v73\n" \
"v_exp_f32_e32 v76, v76\n" \
"v_exp_f32_e32 v77, v77\n" \
"v_exp_f32_e32 v78, v78\n" \
"v_exp_f32_e32 v79, v79\n" \
"v_cvt_pk_bf16_f32 v70, v68, v69\n" \
"v_cvt_pk_bf16_f32 v73, v74, v75\n" \
"v_add_u32_e32 v49, 0x10000, v48\n" \
"s_xor_b32 s65, s65, 0x8000\n" \
"v_cvt_pk_bf16_f32 v69, v66, v67\n" \
"v_cvt_pk_bf16_f32 v68, v64, v65\n" \
"v_cvt_pk_bf16_f32 v74, v76, v77\n" \
"v_cvt_pk_bf16_f32 v75, v78, v79\n" \
"v_xor_b32_e32 v48, 0x8000, v48\n" \
"s_sub_u32 s66, s19, 2\n" \
"s_branch L_2_%=\n" \
"L_f1_%=:\n" \
"ds_read_b128 a[160:163], v49 offset:0\n" \
"ds_read_b128 a[164:167], v49 offset:1024\n" \
"ds_read_b128 a[168:171], v49 offset:2048\n" \
"v_mfma_f32_16x16x32_bf16 v[104:107], a[128:131], a[80:83], v[36:39]\n" \
"v_cvt_pk_bf16_f32 v192, v192, v193\n" \
"v_cvt_pk_bf16_f32 v224, v224, v225\n" \
"v_cvt_pk_bf16_f32 v193, v194, v195\n" \
"v_cvt_pk_bf16_f32 v225, v226, v227\n" \
"ds_read_b128 a[172:175], v49 offset:3072\n" \
"v_cvt_pk_bf16_f32 v194, v196, v197\n" \
"v_cvt_pk_bf16_f32 v195, v198, v199\n" \
"v_cvt_pk_bf16_f32 v226, v228, v229\n" \
"v_cvt_pk_bf16_f32 v227, v230, v231\n" \
"v_mfma_f32_16x16x32_bf16 v[104:107], a[132:135], a[84:87], v[104:107]\n" \
"v_lshl_add_u32 v50, s15, 2, v20\n" \
"v_cvt_pk_bf16_f32 v196, v200, v201\n" \
"v_cvt_pk_bf16_f32 v197, v202, v203\n" \
"v_cvt_pk_bf16_f32 v198, v204, v205\n" \
"v_cvt_pk_bf16_f32 v199, v206, v207\n" \
"v_cvt_pk_bf16_f32 v228, v232, v233\n" \
"v_cvt_pk_bf16_f32 v229, v234, v235\n" \
"v_cvt_pk_bf16_f32 v230, v236, v237\n" \
"v_cvt_pk_bf16_f32 v231, v238, v239\n" \
"v_add_u32_e32 v50, 0x1a000, v50\n" \
"v_mov_b32_e32 v61, s62\n" \
"v_cvt_pk_bf16_f32 v200, v208, v209\n" \
"v_cvt_pk_bf16_f32 v201, v210, v211\n" \
"v_cvt_pk_bf16_f32 v202, v212, v213\n" \
"v_cvt_pk_bf16_f32 v203, v214, v215\n" \
"v_cvt_pk_bf16_f32 v204, v216, v217\n" \
"v_cvt_pk_bf16_f32 v205, v218, v219\n" \
"v_cvt_pk_bf16_f32 v206, v220, v221\n" \
"v_cvt_pk_bf16_f32 v207, v222, v223\n" \
"v_cvt_pk_bf16_f32 v232, v240, v241\n" \
"v_cvt_pk_bf16_f32 v233, v242, v243\n" \
"v_cvt_pk_bf16_f32 v234, v244, v245\n" \
"v_cvt_pk_bf16_f32 v235, v246, v247\n" \
"v_cvt_pk_bf16_f32 v236, v248, v249\n" \
"v_cvt_pk_bf16_f32 v237, v250, v251\n" \
"v_cvt_pk_bf16_f32 v238, v252, v253\n" \
"v_cvt_pk_bf16_f32 v239, v254, v255\n" \
"ds_write_b128 v50, v[192:195] offset:0\n" \
"v_mfma_f32_16x16x32_bf16 v[104:107], a[136:139], a[88:91], v[104:107]\n" \
"ds_read_b128 a[176:179], v49 offset:4096\n" \
"v_add_u32_e32 v56, 0x49000, v20\n" \
"v_mfma_f32_16x16x32_bf16 v[104:107], a[140:143], a[92:95], v[104:107]\n" \
"ds_write_b128 v50, v[196:199] offset:1024\n" \
"v_mfma_f32_16x16x32_bf16 v[108:111], a[156:159], a[92:95], v[36:39]\n" \
"ds_read_b128 a[180:183], v49 offset:5120\n" \
"ds_read_b128 a[184:187], v49 offset:6144\n" \
"v_mfma_f32_16x16x32_bf16 v[108:111], a[152:155], a[88:91], v[108:111]\n" \
"ds_write_b128 v50, v[200:203] offset:2048\n" \
"v_mfma_f32_16x16x32_bf16 v[108:111], a[148:151], a[84:87], v[108:111]\n" \
"v_and_b32_e32 v68, v12, v68\n" \
"v_and_b32_e32 v69, v13, v69\n" \
"v_mfma_f32_16x16x32_bf16 v[108:111], a[144:147], a[80:83], v[108:111]\n" \
"ds_write_b128 v50, v[204:207] offset:3072\n" \
"v_and_b32_e32 v70, v14, v70\n" \
"v_and_b32_e32 v71, v15, v71\n" \
"ds_read_b128 a[188:191], v49 offset:7168\n" \
"ds_read_b128 a[0:3], v20 offset:8192\n" \
"s_waitcnt lgkmcnt(9)\n" \
"v_mfma_f32_16x16x32_bf16 v[128:131], a[160:163], v[68:71], v[128:131]\n" \
"ds_write_b128 v50, v[224:227] offset:4096\n" \
"v_mfma_f32_16x16x32_bf16 v[160:163], a[160:163], v[72:75], v[160:163]\n" \
"ds_read_b128 a[4:7], v20 offset:9216\n" \
"ds_read_b128 a[8:11], v20 offset:10240\n" \
"v_mfma_f32_16x16x32_bf16 v[132:135], a[164:167], v[68:71], v[132:135]\n" \
"ds_write_b128 v50, v[228:231] offset:5120\n" \
"v_mfma_f32_16x16x32_bf16 v[164:167], a[164:167], v[72:75], v[164:167]\n" \
"ds_read_b128 a[12:15], v20 offset:11264\n" \
"ds_read_b128 a[28:31], v20 offset:15360\n" \
"v_mfma_f32_16x16x32_bf16 v[136:139], a[168:171], v[68:71], v[136:139]\n" \
"ds_write_b128 v50, v[232:235] offset:6144\n" \
"v_mfma_f32_16x16x32_bf16 v[168:171], a[168:171], v[72:75], v[168:171]\n" \
"ds_read_b128 a[24:27], v20 offset:14336\n" \
"ds_read_b128 a[20:23], v20 offset:13312\n" \
"v_mfma_f32_16x16x32_bf16 v[140:143], a[172:175], v[68:71], v[140:143]\n" \
"ds_write_b128 v50, v[236:239] offset:7168\n" \
"v_mfma_f32_16x16x32_bf16 v[172:175], a[172:175], v[72:75], v[172:175]\n" \
"ds_read_b128 a[16:19], v20 offset:12288\n" \
"s_waitcnt lgkmcnt(15)\n" \
"v_mfma_f32_16x16x32_bf16 v[144:147], a[176:179], v[68:71], v[144:147]\n" \
"v_xor_b32_e32 v48, 0x4000, v49\n" \
"ds_read_b32 v58, v18 offset:256\n" \
"v_mfma_f32_16x16x32_bf16 v[176:179], a[176:179], v[72:75], v[176:179]\n" \
"ds_read_b32 v59, v18 offset:384\n" \
"ds_read_b128 a[32:35], v48 offset:8192\n" \
"v_mfma_f32_16x16x32_bf16 v[148:151], a[180:183], v[68:71], v[148:151]\n" \
"ds_read_b128 a[36:39], v48 offset:9216\n" \
"ds_read_b128 a[40:43], v48 offset:10240\n" \
"v_mfma_f32_16x16x32_bf16 v[180:183], a[180:183], v[72:75], v[180:183]\n" \
"ds_read_b128 a[44:47], v48 offset:11264\n" \
"ds_read_b128 a[48:51], v48 offset:12288\n" \
"v_mfma_f32_16x16x32_bf16 v[152:155], a[184:187], v[68:71], v[152:155]\n" \
"ds_read_b128 a[52:55], v48 offset:13312\n" \
"ds_read_b128 a[56:59], v48 offset:14336\n" \
"v_mfma_f32_16x16x32_bf16 v[184:187], a[184:187], v[72:75], v[184:187]\n" \
"ds_read_b128 a[60:63], v48 offset:15360\n" \
"s_waitcnt lgkmcnt(15)\n" \
"v_mfma_f32_16x16x32_bf16 v[156:159], a[188:191], v[68:71], v[156:159]\n" \
"ds_read_b128 a[128:131], v49 offset:8192\n" \
"ds_read_b128 a[132:135], v49 offset:9216\n" \
"v_mfma_f32_16x16x32_bf16 v[188:191], a[188:191], v[72:75], v[188:191]\n" \
"ds_read_b128 a[136:139], v49 offset:10240\n" \
"ds_read_b128 a[140:143], v49 offset:11264\n" \
"v_smfmac_f32_16x16x64_bf16 a[192:195], a[200:203], v[68:75], v21\n" \
"ds_read_b128 a[144:147], v49 offset:12288\n" \
"ds_read_b128 a[148:151], v49 offset:13312\n" \
"v_mfma_f32_16x16x32_bf16 v[112:115], a[0:3], a[64:67], v[32:35]\n" \
"ds_read_b128 a[152:155], v49 offset:14336\n" \
"ds_read_b128 a[156:159], v49 offset:15360\n" \
"v_mfma_f32_16x16x32_bf16 v[112:115], a[4:7], a[68:71], v[112:115]\n" \
"v_accvgpr_read_b32 v40, a196\n" \
"v_accvgpr_read_b32 v24, a197\n" \
"v_mfma_f32_16x16x32_bf16 v[112:115], a[8:11], a[72:75], v[112:115]\n" \
"v_accvgpr_read_b32 v41, a198\n" \
"v_accvgpr_read_b32 v25, a199\n" \
"v_mfma_f32_16x16x32_bf16 v[112:115], a[12:15], a[76:79], v[112:115]\n" \
"v_add_f32_e32 v40, v40, v24\n" \
"v_add_f32_e32 v41, v41, v25\n" \
"v_mfma_f32_16x16x32_bf16 v[116:119], a[28:31], a[76:79], v[32:35]\n" \
"v_add_f32_e32 v40, s94, v40\n" \
"s_waitcnt lgkmcnt(15)\n" \
"v_mfma_f32_16x16x32_bf16 v[116:119], a[24:27], a[72:75], v[116:119]\n" \
"v_add_f32_e32 v41, s94, v41\n" \
"ds_write_b64 v19, v[40:41]\n" \
"v_mfma_f32_16x16x32_bf16 v[116:119], a[20:23], a[68:71], v[116:119]\n" \
"v_add_u32_e32 v52, s15, v20\n" \
"v_add_u32_e32 v51, 0x10000, v52\n" \
"v_mfma_f32_16x16x32_bf16 v[116:119], a[16:19], a[64:67], v[116:119]\n" \
"v_exp_f32_e32 v104, v104\n" \
"v_mfma_f32_16x16x32_bf16 v[124:127], a[16:19], a[80:83], v[36:39]\n" \
"v_exp_f32_e32 v105, v105\n" \
"v_mfma_f32_16x16x32_bf16 v[124:127], a[20:23], a[84:87], v[124:127]\n" \
"v_exp_f32_e32 v106, v106\n" \
"v_mfma_f32_16x16x32_bf16 v[124:127], a[24:27], a[88:91], v[124:127]\n" \
"v_exp_f32_e32 v107, v107\n" \
"v_mfma_f32_16x16x32_bf16 v[124:127], a[28:31], a[92:95], v[124:127]\n" \
"v_cvt_pk_bf16_f32 v104, v104, v105\n" \
"v_cvt_pk_bf16_f32 v105, v106, v107\n" \
"v_mfma_f32_16x16x32_bf16 v[120:123], a[12:15], a[92:95], v[36:39]\n" \
"v_exp_f32_e32 v108, v108\n" \
"v_mfma_f32_16x16x32_bf16 v[120:123], a[8:11], a[88:91], v[120:123]\n" \
"v_exp_f32_e32 v109, v109\n" \
"v_mfma_f32_16x16x32_bf16 v[120:123], a[4:7], a[84:87], v[120:123]\n" \
"v_exp_f32_e32 v110, v110\n" \
"v_mfma_f32_16x16x32_bf16 v[120:123], a[0:3], a[80:83], v[120:123]\n" \
"v_exp_f32_e32 v111, v111\n" \
"s_waitcnt lgkmcnt(0)\n" \
"s_barrier\n" \
"ds_read_b128 v[208:211], v52 offset:0\n" \
"ds_read_b128 v[212:215], v52 offset:1024\n" \
"ds_read_b128 v[216:219], v52 offset:2048\n" \
"ds_read_b128 v[220:223], v52 offset:3072\n" \
"ds_read_b128 v[240:243], v51 offset:32768\n" \
"ds_read_b128 v[244:247], v51 offset:33792\n" \
"ds_read_b128 v[248:251], v51 offset:34816\n" \
"ds_read_b128 v[252:255], v51 offset:35840\n" \
"v_cvt_pk_bf16_f32 v106, v108, v109\n" \
"v_cvt_pk_bf16_f32 v107, v110, v111\n" \
"v_and_b32_e32 v104, v12, v104\n" \
"v_and_b32_e32 v105, v13, v105\n" \
"v_and_b32_e32 v106, v14, v106\n" \
"s_waitcnt vmcnt(0)\n" \
"s_mov_b64 exec, 1\n" \
"buffer_store_dword v61, off, s[56:59], s95 offset:0 sc1\n" \
"s_mov_b64 exec, -1\n" \
"v_and_b32_e32 v107, v15, v107\n" \
"v_xor_b32_e32 v63, 0x400, v19\n" \
"ds_read_b64 v[42:43], v63\n" \
"v_mfma_f32_16x16x32_bf16 a[192:195], a[212:215], v[104:107], a[192:195]\n" \
"v_mov_b32_e32 v194, v28\n" \
"v_mov_b32_e32 v195, v29\n" \
"v_mfma_f32_16x16x32_bf16 v[160:163], a[128:131], v[104:107], v[160:163]\n" \
"v_mov_b32_e32 v196, 0\n" \
"v_mov_b32_e32 v197, 0\n" \
"v_mfma_f32_16x16x32_bf16 v[164:167], a[132:135], v[104:107], v[164:167]\n" \
"v_mov_b32_e32 v192, 0\n" \
"v_mov_b32_e32 v193, 0\n" \
"v_mfma_f32_16x16x32_bf16 v[168:171], a[136:139], v[104:107], v[168:171]\n" \
"v_readlane_b32 s93, v11, 22\n" \
"v_readlane_b32 s92, v11, 27\n" \
"v_mfma_f32_16x16x32_bf16 v[172:175], a[140:143], v[104:107], v[172:175]\n" \
"buffer_load_dwordx4 a[96:99], v20, s[56:59], s60 offen offset:0 sc1\n" \
"v_mfma_f32_16x16x32_bf16 v[176:179], a[144:147], v[104:107], v[176:179]\n" \
"v_exp_f32_e32 v118, v118\n" \
"v_mfma_f32_16x16x32_bf16 v[180:183], a[148:151], v[104:107], v[180:183]\n" \
"v_exp_f32_e32 v119, v119\n" \
"v_mfma_f32_16x16x32_bf16 v[184:187], a[152:155], v[104:107], v[184:187]\n" \
"s_add_u32 m0, s93, 0x0\n" \
"v_cvt_pk_bf16_f32 v119, v118, v119\n" \
"v_mfma_f32_16x16x32_bf16 v[188:191], a[156:159], v[104:107], v[188:191]\n" \
"v_exp_f32_e32 v120, v120\n" \
"s_waitcnt lgkmcnt(1)\n" \
"s_barrier\n" \
"buffer_load_dwordx4 a[112:115], v56, s[56:59], s60 offen offset:0 sc1\n" \
"v_mfma_f32_16x16x32_bf16 v[128:131], v[194:197], v[208:211], v[128:131]\n" \
"v_exp_f32_e32 v121, v121\n" \
"v_mfma_f32_16x16x32_bf16 v[132:135], v[192:195], v[208:211], v[132:135]\n" \
"v_exp_f32_e32 v116, v116\n" \
"v_mfma_f32_16x16x32_bf16 v[136:139], v[194:197], v[212:215], v[136:139]\n" \
"v_exp_f32_e32 v117, v117\n" \
"v_mfma_f32_16x16x32_bf16 v[140:143], v[192:195], v[212:215], v[140:143]\n" \
"v_exp_f32_e32 v122, v122\n" \
"v_mfma_f32_16x16x32_bf16 v[144:147], v[194:197], v[216:219], v[144:147]\n" \
"v_exp_f32_e32 v123, v123\n" \
"v_mfma_f32_16x16x32_bf16 v[148:151], v[192:195], v[216:219], v[148:151]\n" \
"buffer_load_dwordx4 a[100:103], v20, s[56:59], s60 offen offset:1024 sc1\n" \
"v_mfma_f32_16x16x32_bf16 v[152:155], v[194:197], v[220:223], v[152:155]\n" \
"v_exp_f32_e32 v114, v114\n" \
"v_mfma_f32_16x16x32_bf16 v[156:159], v[192:195], v[220:223], v[156:159]\n" \
"v_exp_f32_e32 v115, v115\n" \
"v_mfma_f32_16x16x32_bf16 v[160:163], v[194:197], v[240:243], v[160:163]\n" \
"v_exp_f32_e32 v112, v112\n" \
"v_mfma_f32_16x16x32_bf16 v[164:167], v[192:195], v[240:243], v[164:167]\n" \
"v_exp_f32_e32 v113, v113\n" \
"v_mfma_f32_16x16x32_bf16 v[168:171], v[194:197], v[244:247], v[168:171]\n" \
"v_cvt_pk_bf16_f32 v120, v120, v121\n" \
"v_cvt_pk_bf16_f32 v118, v116, v117\n" \
"v_mfma_f32_16x16x32_bf16 v[172:175], v[192:195], v[244:247], v[172:175]\n" \
"buffer_load_dwordx4 a[116:119], v56, s[56:59], s60 offen offset:1024 sc1\n" \
"v_mfma_f32_16x16x32_bf16 v[176:179], v[194:197], v[248:251], v[176:179]\n" \
"v_exp_f32_e32 v124, v124\n" \
"v_mfma_f32_16x16x32_bf16 v[180:183], v[192:195], v[248:251], v[180:183]\n" \
"v_exp_f32_e32 v125, v125\n" \
"v_mfma_f32_16x16x32_bf16 v[184:187], v[194:197], v[252:255], v[184:187]\n" \
"v_exp_f32_e32 v126, v126\n" \
"v_mfma_f32_16x16x32_bf16 v[188:191], v[192:195], v[252:255], v[188:191]\n" \
"v_exp_f32_e32 v127, v127\n" \
"v_cvt_pk_bf16_f32 v121, v122, v123\n" \
"v_cvt_pk_bf16_f32 v117, v114, v115\n" \
"v_cvt_pk_bf16_f32 v116, v112, v113\n" \
"v_cvt_pk_bf16_f32 v122, v124, v125\n" \
"v_cvt_pk_bf16_f32 v123, v126, v127\n" \
"buffer_load_dwordx4 a[104:107], v20, s[56:59], s60 offen offset:2048 sc1\n" \
"s_nop 0\n" \
"v_smfmac_f32_16x16x64_bf16 a[192:195], a[200:203], v[116:123], v21\n" \
"v_mfma_f32_16x16x32_bf16 v[128:131], a[32:35], v[116:119], v[128:131]\n" \
"v_mfma_f32_16x16x32_bf16 v[160:163], a[32:35], v[120:123], v[160:163]\n" \
"v_mfma_f32_16x16x32_bf16 v[132:135], a[36:39], v[116:119], v[132:135]\n" \
"v_mfma_f32_16x16x32_bf16 v[164:167], a[36:39], v[120:123], v[164:167]\n" \
"v_mfma_f32_16x16x32_bf16 v[136:139], a[40:43], v[116:119], v[136:139]\n" \
"buffer_load_dwordx4 a[120:123], v56, s[56:59], s60 offen offset:2048 sc1\n" \
"v_mfma_f32_16x16x32_bf16 v[168:171], a[40:43], v[120:123], v[168:171]\n" \
"v_mfma_f32_16x16x32_bf16 v[140:143], a[44:47], v[116:119], v[140:143]\n" \
"v_accvgpr_read_b32 v54, a192\n" \
"v_accvgpr_read_b32 v22, a193\n" \
"v_mfma_f32_16x16x32_bf16 v[172:175], a[44:47], v[120:123], v[172:175]\n" \
"v_add_f32_e32 v54, v54, v22\n" \
"s_waitcnt lgkmcnt(0)\n" \
"v_add_f32_e32 v54, v54, v42\n" \
"v_mfma_f32_16x16x32_bf16 v[144:147], a[48:51], v[116:119], v[144:147]\n" \
"v_max_f32_e32 v62, v62, v54\n" \
"v_accvgpr_read_b32 v55, a194\n" \
"v_mfma_f32_16x16x32_bf16 v[176:179], a[48:51], v[120:123], v[176:179]\n" \
"v_rcp_f32_e32 v54, v54\n" \
"buffer_load_dwordx4 a[108:111], v20, s[56:59], s60 offen offset:3072 sc1\n" \
"v_mfma_f32_16x16x32_bf16 v[148:151], a[52:55], v[116:119], v[148:151]\n" \
"v_mul_f32_e32 v54, v54, v58\n" \
"v_mul_f32_e32 v128, v54, v128\n" \
"v_mfma_f32_16x16x32_bf16 v[180:183], a[52:55], v[120:123], v[180:183]\n" \
"v_mul_f32_e32 v129, v54, v129\n" \
"v_mul_f32_e32 v130, v54, v130\n" \
"v_mul_f32_e32 v131, v54, v131\n" \
"v_mul_f32_e32 v132, v54, v132\n" \
"v_mul_f32_e32 v133, v54, v133\n" \
"v_mul_f32_e32 v134, v54, v134\n" \
"v_mul_f32_e32 v135, v54, v135\n" \
"v_cvt_pk_bf16_f32 v64, v128, v129\n" \
"v_cvt_pk_bf16_f32 v65, v130, v131\n" \
"v_cvt_pk_bf16_f32 v66, v132, v133\n" \
"v_cvt_pk_bf16_f32 v67, v134, v135\n" \
"v_accvgpr_read_b32 v23, a195\n" \
"v_mfma_f32_16x16x32_bf16 v[152:155], a[56:59], v[116:119], v[152:155]\n" \
"v_mfma_f32_16x16x32_bf16 a[208:211], a[216:219], v[64:67], a[208:211]\n" \
"buffer_load_dwordx4 a[124:127], v56, s[56:59], s60 offen offset:3072 sc1\n" \
"v_mul_f32_e32 v136, v54, v136\n" \
"v_mul_f32_e32 v137, v54, v137\n" \
"v_mul_f32_e32 v138, v54, v138\n" \
"v_mul_f32_e32 v139, v54, v139\n" \
"v_mul_f32_e32 v140, v54, v140\n" \
"v_mul_f32_e32 v141, v54, v141\n" \
"v_mul_f32_e32 v142, v54, v142\n" \
"v_mul_f32_e32 v143, v54, v143\n" \
"v_add_f32_e32 v55, v55, v23\n" \
"v_cvt_pk_bf16_f32 v68, v136, v137\n" \
"v_cvt_pk_bf16_f32 v69, v138, v139\n" \
"v_cvt_pk_bf16_f32 v70, v140, v141\n" \
"v_cvt_pk_bf16_f32 v71, v142, v143\n" \
"v_add_f32_e32 v55, v55, v43\n" \
"v_mul_f32_e32 v144, v54, v144\n" \
"v_mfma_f32_16x16x32_bf16 a[208:211], a[216:219], v[68:71], a[208:211]\n" \
"v_mul_f32_e32 v145, v54, v145\n" \
"v_mul_f32_e32 v146, v54, v146\n" \
"v_mul_f32_e32 v147, v54, v147\n" \
"v_mul_f32_e32 v148, v54, v148\n" \
"buffer_load_dwordx4 v0, s[80:83], s92 offen offset:0 lds\n" \
"v_mul_f32_e32 v149, v54, v149\n" \
"v_mul_f32_e32 v150, v54, v150\n" \
"v_mul_f32_e32 v151, v54, v151\n" \
"v_max_f32_e32 v62, v62, v55\n" \
"v_cvt_pk_bf16_f32 v72, v144, v145\n" \
"v_cvt_pk_bf16_f32 v73, v146, v147\n" \
"v_cvt_pk_bf16_f32 v74, v148, v149\n" \
"v_cvt_pk_bf16_f32 v75, v150, v151\n" \
"v_rcp_f32_e32 v55, v55\n" \
"v_mfma_f32_16x16x32_bf16 v[184:187], a[56:59], v[120:123], v[184:187]\n" \
"v_mfma_f32_16x16x32_bf16 a[208:211], a[216:219], v[72:75], a[208:211]\n" \
"v_mul_f32_e32 v152, v54, v152\n" \
"v_mul_f32_e32 v153, v54, v153\n" \
"v_mul_f32_e32 v154, v54, v154\n" \
"v_mul_f32_e32 v155, v54, v155\n" \
"v_cvt_pk_bf16_f32 v76, v152, v153\n" \
"buffer_load_dwordx4 v1, s[80:83], s92 offen offset:1024 lds\n" \
"v_cvt_pk_bf16_f32 v77, v154, v155\n" \
"v_mul_f32_e32 v55, v55, v59\n" \
"v_mul_f32_e32 v160, v55, v160\n" \
"v_mul_f32_e32 v161, v55, v161\n" \
"v_mul_f32_e32 v162, v55, v162\n" \
"v_mul_f32_e32 v163, v55, v163\n" \
"v_mul_f32_e32 v164, v55, v164\n" \
"v_mul_f32_e32 v165, v55, v165\n" \
"v_mul_f32_e32 v166, v55, v166\n" \
"v_mul_f32_e32 v167, v55, v167\n" \
"v_cvt_pk_bf16_f32 v80, v160, v161\n" \
"v_cvt_pk_bf16_f32 v81, v162, v163\n" \
"v_cvt_pk_bf16_f32 v82, v164, v165\n" \
"v_cvt_pk_bf16_f32 v83, v166, v167\n" \
"v_mfma_f32_16x16x32_bf16 v[156:159], a[60:63], v[116:119], v[156:159]\n" \
"v_mul_f32_e32 v168, v55, v168\n" \
"v_mul_f32_e32 v169, v55, v169\n" \
"v_mul_f32_e32 v170, v55, v170\n" \
"v_mul_f32_e32 v171, v55, v171\n" \
"v_mul_f32_e32 v172, v55, v172\n" \
"buffer_load_dwordx4 v2, s[80:83], s92 offen offset:2048 lds\n" \
"v_mul_f32_e32 v173, v55, v173\n" \
"v_mul_f32_e32 v174, v55, v174\n" \
"v_mul_f32_e32 v156, v54, v156\n" \
"v_mul_f32_e32 v157, v54, v157\n" \
"v_mul_f32_e32 v158, v54, v158\n" \
"v_mul_f32_e32 v159, v54, v159\n" \
"v_cvt_pk_bf16_f32 v78, v156, v157\n" \
"v_cvt_pk_bf16_f32 v79, v158, v159\n" \
"v_mul_f32_e32 v175, v55, v175\n" \
"v_cvt_pk_bf16_f32 v84, v168, v169\n" \
"v_mfma_f32_16x16x32_bf16 a[208:211], a[216:219], v[76:79], a[208:211]\n" \
"v_mfma_f32_16x16x32_bf16 a[208:211], a[216:219], v[80:83], a[208:211]\n" \
"v_cvt_pk_bf16_f32 v85, v170, v171\n" \
"v_cvt_pk_bf16_f32 v86, v172, v173\n" \
"v_cvt_pk_bf16_f32 v87, v174, v175\n" \
"v_mul_f32_e32 v176, v55, v176\n" \
"v_mul_f32_e32 v177, v55, v177\n" \
"buffer_load_dwordx4 v3, s[80:83], s92 offen offset:3072 lds\n" \
"s_add_u32 m0, s93, 0x1000\n" \
"v_mfma_f32_16x16x32_bf16 a[208:211], a[216:219], v[84:87], a[208:211]\n" \
"v_mul_f32_e32 v178, v55, v178\n" \
"v_mul_f32_e32 v179, v55, v179\n" \
"v_mul_f32_e32 v180, v55, v180\n" \
"v_mul_f32_e32 v181, v55, v181\n" \
"v_mul_f32_e32 v182, v55, v182\n" \
"v_mul_f32_e32 v183, v55, v183\n" \
"v_cvt_pk_bf16_f32 v96, v176, v177\n" \
"v_cvt_pk_bf16_f32 v97, v178, v179\n" \
"v_cvt_pk_bf16_f32 v98, v180, v181\n" \
"v_cvt_pk_bf16_f32 v99, v182, v183\n" \
"v_mfma_f32_16x16x32_bf16 v[188:191], a[60:63], v[120:123], v[188:191]\n" \
"v_mul_f32_e32 v184, v55, v184\n" \
"v_mfma_f32_16x16x32_bf16 a[208:211], a[216:219], v[96:99], a[208:211]\n" \
"v_mul_f32_e32 v185, v55, v185\n" \
"v_mul_f32_e32 v186, v55, v186\n" \
"buffer_load_dwordx4 v4, s[80:83], s92 offen offset:0 lds\n" \
"v_mul_f32_e32 v187, v55, v187\n" \
"v_cvt_pk_bf16_f32 v100, v184, v185\n" \
"v_cvt_pk_bf16_f32 v101, v186, v187\n" \
"v_mul_f32_e32 v188, v55, v188\n" \
"v_mul_f32_e32 v189, v55, v189\n" \
"v_mul_f32_e32 v190, v55, v190\n" \
"v_mul_f32_e32 v191, v55, v191\n" \
"v_cvt_pk_bf16_f32 v102, v188, v189\n" \
"v_cvt_pk_bf16_f32 v103, v190, v191\n" \
"v_cmp_lt_f32_e64 s[72:73], s21, v62\n" \
"buffer_load_dwordx4 v5, s[80:83], s92 offen offset:1024 lds\n" \
"v_mfma_f32_16x16x32_bf16 a[208:211], a[216:219], v[100:103], a[208:211]\n" \
"buffer_load_dwordx4 v6, s[80:83], s92 offen offset:2048 lds\n" \
"buffer_load_dwordx4 v7, s[80:83], s92 offen offset:3072 lds\n" \
"s_add_u32 m0, s93, 0x2000\n" \
"s_nop 0\n" \
"buffer_load_dwordx4 v8, s[80:83], s92 offen offset:0 lds\n" \
"s_nop 2\n" \
"v_accvgpr_read_b32 v63, a208\n" \
"v_cmp_u_f32_e32 vcc, v63, v63\n" \
"s_nop 0\n" \
"s_or_b64 vcc, s[72:73], vcc\n" \
"s_cbranch_scc0 L_out_%=\n" \
"s_branch L_end_%=\n" \
".fill (L_ab + 24576 - .) / 4, 4, 0xBF800000\n" \
".fill (L_ab + 24576 - .) / 4, 4, 0xBF800000\n" \
"L_ap6:\n" \
"s_setpc_b64 s[88:89]\n" \
"L_gs7:\n" \
"v_mfma_f32_16x16x32_bf16 v[104:107], a[128:131], a[16:19], v[36:39]\n" \
"s_add_u32 s68, s60, 0xfff6e000\n" \
"s_add_u32 s69, s60, 0xfffb7000\n" \
"s_add_u32 s70, s60, 0x0\n" \
"s_add_u32 s71, s60, 0x49000\n" \
"v_mfma_f32_16x16x32_bf16 v[104:107], a[132:135], a[20:23], v[104:107]\n" \
"s_xor_b32 m0, s65, 0x12000\n" \
"ds_read_b128 a[160:163], v49 offset:0\n" \
"v_mfma_f32_16x16x32_bf16 v[104:107], a[136:139], a[24:27], v[104:107]\n" \
"buffer_load_dwordx4 v50, s[8:11], s64 offen offset:0 sc1 lds\n" \
"v_mfma_f32_16x16x32_bf16 v[104:107], a[140:143], a[28:31], v[104:107]\n" \
"ds_read_b128 a[164:167], v49 offset:1024\n" \
"ds_read_b128 a[168:171], v49 offset:2048\n" \
"v_mfma_f32_16x16x32_bf16 v[108:111], a[156:159], a[28:31], v[36:39]\n" \
"ds_read_b128 a[172:175], v49 offset:3072\n" \
"ds_read_b128 a[176:179], v49 offset:4096\n" \
"v_mfma_f32_16x16x32_bf16 v[108:111], a[152:155], a[24:27], v[108:111]\n" \
"s_mov_b32 s65, s13\n" \
"v_add_u32_e32 v63, s15, v20\n" \
"v_mfma_f32_16x16x32_bf16 v[108:111], a[148:151], a[20:23], v[108:111]\n" \
"buffer_load_dwordx4 v50, s[8:11], s64 offen offset:1024 sc1 lds\n" \
"v_mfma_f32_16x16x32_bf16 v[108:111], a[144:147], a[16:19], v[108:111]\n" \
"s_add_u32 s67, s14, 40960\n" \
"v_add_u32_e32 v50, 0x6000, v63\n" \
"v_mfma_f32_16x16x32_bf16 v[116:119], a[144:147], a[32:35], v[40:43]\n" \
"s_xor_b32 m0, s65, 0x8000\n" \
"ds_read_b128 a[180:183], v49 offset:5120\n" \
"v_mfma_f32_16x16x32_bf16 v[116:119], a[148:151], a[36:39], v[116:119]\n" \
"ds_read_b128 a[184:187], v49 offset:6144\n" \
"v_and_b32_e32 v68, v12, v68\n" \
"v_mfma_f32_16x16x32_bf16 v[116:119], a[152:155], a[40:43], v[116:119]\n" \
"v_and_b32_e32 v69, v13, v69\n" \
"v_and_b32_e32 v70, v14, v70\n" \
"v_mfma_f32_16x16x32_bf16 v[116:119], a[156:159], a[44:47], v[116:119]\n" \
"buffer_load_dwordx4 v50, s[8:11], s67 offen offset:0 sc1 lds\n" \
"v_mfma_f32_16x16x32_bf16 v[112:115], a[140:143], a[44:47], v[40:43]\n" \
"v_and_b32_e32 v71, v15, v71\n" \
"ds_read_b128 a[188:191], v49 offset:7168\n" \
"v_mfma_f32_16x16x32_bf16 v[112:115], a[136:139], a[40:43], v[112:115]\n" \
"v_exp_f32_e32 v86, v86\n" \
"v_mfma_f32_16x16x32_bf16 v[112:115], a[132:135], a[36:39], v[112:115]\n" \
"v_exp_f32_e32 v87, v87\n" \
"v_mfma_f32_16x16x32_bf16 v[112:115], a[128:131], a[32:35], v[112:115]\n" \
"v_exp_f32_e32 v84, v84\n" \
"v_mfma_f32_16x16x32_bf16 v[120:123], a[128:131], a[48:51], v[44:47]\n" \
"buffer_load_dwordx4 v50, s[8:11], s67 offen offset:1024 sc1 lds\n" \
"v_mfma_f32_16x16x32_bf16 v[120:123], a[132:135], a[52:55], v[120:123]\n" \
"v_exp_f32_e32 v85, v85\n" \
"v_mfma_f32_16x16x32_bf16 v[120:123], a[136:139], a[56:59], v[120:123]\n" \
"v_cvt_pk_bf16_f32 v87, v86, v87\n" \
"v_cvt_pk_bf16_f32 v86, v84, v85\n" \
"v_mfma_f32_16x16x32_bf16 v[120:123], a[140:143], a[60:63], v[120:123]\n" \
"v_exp_f32_e32 v82, v82\n" \
"v_mfma_f32_16x16x32_bf16 v[124:127], a[156:159], a[60:63], v[44:47]\n" \
"v_exp_f32_e32 v83, v83\n" \
"v_mfma_f32_16x16x32_bf16 v[124:127], a[152:155], a[56:59], v[124:127]\n" \
"v_exp_f32_e32 v80, v80\n" \
"v_mfma_f32_16x16x32_bf16 v[124:127], a[148:151], a[52:55], v[124:127]\n" \
"buffer_load_dwordx4 v50, s[8:11], s67 offen offset:2048 sc1 lds\n" \
"v_mfma_f32_16x16x32_bf16 v[124:127], a[144:147], a[48:51], v[124:127]\n" \
"v_cvt_pk_bf16_f32 v85, v82, v83\n" \
"s_waitcnt lgkmcnt(1)\n" \
"v_mfma_f32_16x16x32_bf16 v[128:131], a[160:163], v[68:71], v[128:131]\n" \
"v_exp_f32_e32 v81, v81\n" \
"v_mfma_f32_16x16x32_bf16 v[160:163], a[160:163], v[72:75], v[160:163]\n" \
"v_cvt_pk_bf16_f32 v84, v80, v81\n" \
"s_mov_b32 s64, s14\n" \
"v_mfma_f32_16x16x32_bf16 v[132:135], a[164:167], v[68:71], v[132:135]\n" \
"v_exp_f32_e32 v88, v88\n" \
"v_mfma_f32_16x16x32_bf16 v[164:167], a[164:167], v[72:75], v[164:167]\n" \
"v_exp_f32_e32 v89, v89\n" \
"v_mfma_f32_16x16x32_bf16 v[136:139], a[168:171], v[68:71], v[136:139]\n" \
"buffer_load_dwordx4 v50, s[8:11], s67 offen offset:3072 sc1 lds\n" \
"v_mfma_f32_16x16x32_bf16 v[168:171], a[168:171], v[72:75], v[168:171]\n" \
"v_exp_f32_e32 v90, v90\n" \
"v_mfma_f32_16x16x32_bf16 v[140:143], a[172:175], v[68:71], v[140:143]\n" \
"v_exp_f32_e32 v91, v91\n" \
"v_mfma_f32_16x16x32_bf16 v[172:175], a[172:175], v[72:75], v[172:175]\n" \
"v_cvt_pk_bf16_f32 v88, v88, v89\n" \
"v_cvt_pk_bf16_f32 v89, v90, v91\n" \
"v_mfma_f32_16x16x32_bf16 v[144:147], a[176:179], v[68:71], v[144:147]\n" \
"v_exp_f32_e32 v92, v92\n" \
"v_mfma_f32_16x16x32_bf16 v[176:179], a[176:179], v[72:75], v[176:179]\n" \
"v_exp_f32_e32 v93, v93\n" \
"v_mfma_f32_16x16x32_bf16 v[148:151], a[180:183], v[68:71], v[148:151]\n" \
"v_exp_f32_e32 v94, v94\n" \
"v_mfma_f32_16x16x32_bf16 v[180:183], a[180:183], v[72:75], v[180:183]\n" \
"v_exp_f32_e32 v95, v95\n" \
"v_mfma_f32_16x16x32_bf16 v[152:155], a[184:187], v[68:71], v[152:155]\n" \
"v_cvt_pk_bf16_f32 v90, v92, v93\n" \
"v_cvt_pk_bf16_f32 v91, v94, v95\n" \
"v_mfma_f32_16x16x32_bf16 v[184:187], a[184:187], v[72:75], v[184:187]\n" \
"v_add_u32_e32 v51, 0x12000, v63\n" \
"s_waitcnt lgkmcnt(0)\n" \
"v_mfma_f32_16x16x32_bf16 v[156:159], a[188:191], v[68:71], v[156:159]\n" \
"s_xor_b32 m0, s65, 0xa000\n" \
"ds_read_b32 v58, v18 offset:512\n" \
"v_mfma_f32_16x16x32_bf16 v[188:191], a[188:191], v[72:75], v[188:191]\n" \
"buffer_load_dwordx4 v51, s[8:11], s64 offen offset:0 sc1 lds\n" \
"v_smfmac_f32_16x16x64_bf16 a[192:195], a[200:203], v[68:75], v21\n" \
"ds_read_b32 v59, v18 offset:640\n" \
"ds_read_b32 v60, v18 offset:768\n" \
"v_mfma_f32_16x16x32_bf16 v[220:223], a[188:191], v[84:87], v[220:223]\n" \
"ds_read_b32 v61, v18 offset:896\n" \
"ds_read_b128 a[128:131], v48 offset:0\n" \
"v_mfma_f32_16x16x32_bf16 v[252:255], a[188:191], v[88:91], v[252:255]\n" \
"ds_read_b128 a[144:147], v48 offset:4096\n" \
"ds_read_b128 a[132:135], v48 offset:1024\n" \
"v_mfma_f32_16x16x32_bf16 v[216:219], a[184:187], v[84:87], v[216:219]\n" \
"buffer_load_dwordx4 v51, s[8:11], s64 offen offset:1024 sc1 lds\n" \
"v_mfma_f32_16x16x32_bf16 v[248:251], a[184:187], v[88:91], v[248:251]\n" \
"ds_read_b128 a[148:151], v48 offset:5120\n" \
"ds_read_b128 a[136:139], v48 offset:2048\n" \
"v_mfma_f32_16x16x32_bf16 v[212:215], a[180:183], v[84:87], v[212:215]\n" \
"ds_read_b128 a[152:155], v48 offset:6144\n" \
"ds_read_b128 a[140:143], v48 offset:3072\n" \
"v_mfma_f32_16x16x32_bf16 v[244:247], a[180:183], v[88:91], v[244:247]\n" \
"ds_read_b128 a[156:159], v48 offset:7168\n" \
"v_add_u32_e32 v52, 0x14000, v63\n" \
"v_mfma_f32_16x16x32_bf16 v[208:211], a[176:179], v[84:87], v[208:211]\n" \
"buffer_load_dwordx4 v51, s[8:11], s64 offen offset:2048 sc1 lds\n" \
"v_mfma_f32_16x16x32_bf16 v[240:243], a[176:179], v[88:91], v[240:243]\n" \
"v_exp_f32_e32 v104, v104\n" \
"v_mfma_f32_16x16x32_bf16 v[204:207], a[172:175], v[84:87], v[204:207]\n" \
"v_exp_f32_e32 v105, v105\n" \
"v_mfma_f32_16x16x32_bf16 v[236:239], a[172:175], v[88:91], v[236:239]\n" \
"v_exp_f32_e32 v106, v106\n" \
"v_mfma_f32_16x16x32_bf16 v[200:203], a[168:171], v[84:87], v[200:203]\n" \
"buffer_load_dwordx4 v51, s[8:11], s64 offen offset:3072 sc1 lds\n" \
"v_mfma_f32_16x16x32_bf16 v[232:235], a[168:171], v[88:91], v[232:235]\n" \
"v_exp_f32_e32 v107, v107\n" \
"v_mfma_f32_16x16x32_bf16 v[196:199], a[164:167], v[84:87], v[196:199]\n" \
"v_cvt_pk_bf16_f32 v104, v104, v105\n" \
"v_cvt_pk_bf16_f32 v105, v106, v107\n" \
"v_mfma_f32_16x16x32_bf16 v[228:231], a[164:167], v[88:91], v[228:231]\n" \
"v_exp_f32_e32 v108, v108\n" \
"v_mfma_f32_16x16x32_bf16 v[192:195], a[160:163], v[84:87], v[192:195]\n" \
"v_exp_f32_e32 v109, v109\n" \
"v_mfma_f32_16x16x32_bf16 v[224:227], a[160:163], v[88:91], v[224:227]\n" \
"v_exp_f32_e32 v110, v110\n" \
"v_smfmac_f32_16x16x64_bf16 a[196:199], a[200:203], v[84:91], v21\n" \
"v_exp_f32_e32 v111, v111\n" \
"v_cvt_pk_bf16_f32 v106, v108, v109\n" \
"v_cvt_pk_bf16_f32 v107, v110, v111\n" \
"v_and_b32_e32 v104, v12, v104\n" \
"v_and_b32_e32 v105, v13, v105\n" \
"v_and_b32_e32 v106, v14, v106\n" \
"v_and_b32_e32 v107, v15, v107\n" \
"v_accvgpr_read_b32 v54, a192\n" \
"v_accvgpr_read_b32 v22, a193\n" \
"v_readlane_b32 s93, v11, 19\n" \
"v_readlane_b32 s80, v11, 11\n" \
"v_readlane_b32 s81, v11, 12\n" \
"s_mov_b32 s82, 0x47fca0\n" \
"s_mov_b32 s83, 0x20000\n" \
"v_readlane_b32 s92, v11, 26\n" \
"v_exp_f32_e32 v118, v118\n" \
"v_exp_f32_e32 v119, v119\n" \
"v_exp_f32_e32 v116, v116\n" \
"v_exp_f32_e32 v117, v117\n" \
"v_cvt_pk_bf16_f32 v119, v118, v119\n" \
"v_exp_f32_e32 v114, v114\n" \
"v_exp_f32_e32 v115, v115\n" \
"v_exp_f32_e32 v112, v112\n" \
"v_cvt_pk_bf16_f32 v118, v116, v117\n" \
"s_waitcnt vmcnt(10) lgkmcnt(0)\n" \
"s_barrier\n" \
"v_mfma_f32_16x16x32_bf16 v[80:83], a[128:131], a[32:35], v[40:43]\n" \
"ds_read_b128 a[160:163], v49 offset:8192\n" \
"ds_read_b128 a[164:167], v49 offset:9216\n" \
"v_mfma_f32_16x16x32_bf16 v[80:83], a[132:135], a[36:39], v[80:83]\n" \
"ds_read_b128 a[168:171], v49 offset:10240\n" \
"s_xor_b32 m0, s65, 0x18000\n" \
"v_mfma_f32_16x16x32_bf16 v[80:83], a[136:139], a[40:43], v[80:83]\n" \
"buffer_load_dwordx4 v52, s[8:11], s64 offen offset:0 sc1 lds\n" \
"v_mfma_f32_16x16x32_bf16 v[80:83], a[140:143], a[44:47], v[80:83]\n" \
"ds_read_b128 a[172:175], v49 offset:11264\n" \
"ds_read_b128 a[176:179], v49 offset:12288\n" \
"v_mfma_f32_16x16x32_bf16 v[84:87], a[156:159], a[44:47], v[40:43]\n" \
"ds_read_b128 a[180:183], v49 offset:13312\n" \
"ds_read_b128 a[184:187], v49 offset:14336\n" \
"v_mfma_f32_16x16x32_bf16 v[84:87], a[152:155], a[40:43], v[84:87]\n" \
"ds_read_b128 a[188:191], v49 offset:15360\n" \
"v_cvt_pk_bf16_f32 v117, v114, v115\n" \
"v_mfma_f32_16x16x32_bf16 v[84:87], a[148:151], a[36:39], v[84:87]\n" \
"buffer_load_dwordx4 v52, s[8:11], s64 offen offset:1024 sc1 lds\n" \
"v_mfma_f32_16x16x32_bf16 v[84:87], a[144:147], a[32:35], v[84:87]\n" \
"v_exp_f32_e32 v113, v113\n" \
"v_mfma_f32_16x16x32_bf16 v[92:95], a[144:147], a[48:51], v[44:47]\n" \
"v_cvt_pk_bf16_f32 v116, v112, v113\n" \
"ds_read_b128 a[144:147], v48 offset:12288\n" \
"v_mfma_f32_16x16x32_bf16 v[92:95], a[148:151], a[52:55], v[92:95]\n" \
"v_exp_f32_e32 v120, v120\n" \
"v_mfma_f32_16x16x32_bf16 v[92:95], a[152:155], a[56:59], v[92:95]\n" \
"buffer_load_dwordx4 v52, s[8:11], s64 offen offset:2048 sc1 lds\n" \
"v_mfma_f32_16x16x32_bf16 v[92:95], a[156:159], a[60:63], v[92:95]\n" \
"v_exp_f32_e32 v121, v121\n" \
"v_mfma_f32_16x16x32_bf16 v[88:91], a[140:143], a[60:63], v[44:47]\n" \
"v_exp_f32_e32 v122, v122\n" \
"v_mfma_f32_16x16x32_bf16 v[88:91], a[136:139], a[56:59], v[88:91]\n" \
"v_exp_f32_e32 v123, v123\n" \
"v_mfma_f32_16x16x32_bf16 v[88:91], a[132:135], a[52:55], v[88:91]\n" \
"buffer_load_dwordx4 v52, s[8:11], s64 offen offset:3072 sc1 lds\n" \
"v_mfma_f32_16x16x32_bf16 v[88:91], a[128:131], a[48:51], v[88:91]\n" \
"s_add_u32 m0, s93, 0x0\n" \
"v_cvt_pk_bf16_f32 v120, v120, v121\n" \
"v_mfma_f32_16x16x32_bf16 a[192:195], a[212:215], v[104:107], a[192:195]\n" \
"v_cvt_pk_bf16_f32 v121, v122, v123\n" \
"s_waitcnt lgkmcnt(6)\n" \
"v_mfma_f32_16x16x32_bf16 v[160:163], a[160:163], v[104:107], v[160:163]\n" \
"v_exp_f32_e32 v124, v124\n" \
"v_mfma_f32_16x16x32_bf16 v[164:167], a[164:167], v[104:107], v[164:167]\n" \
"buffer_load_dwordx4 v0, s[80:83], s92 offen offset:0 lds\n" \
"v_mfma_f32_16x16x32_bf16 v[168:171], a[168:171], v[104:107], v[168:171]\n" \
"ds_read_b128 a[128:131], v48 offset:8192\n" \
"s_waitcnt lgkmcnt(2)\n" \
"v_mfma_f32_16x16x32_bf16 v[172:175], a[172:175], v[104:107], v[172:175]\n" \
"v_exp_f32_e32 v125, v125\n" \
"v_mfma_f32_16x16x32_bf16 v[176:179], a[176:179], v[104:107], v[176:179]\n" \
"v_exp_f32_e32 v126, v126\n" \
"buffer_load_dwordx4 v1, s[80:83], s92 offen offset:1024 lds\n" \
"v_mfma_f32_16x16x32_bf16 v[180:183], a[180:183], v[104:107], v[180:183]\n" \
"v_exp_f32_e32 v127, v127\n" \
"v_mfma_f32_16x16x32_bf16 v[184:187], a[184:187], v[104:107], v[184:187]\n" \
"v_cvt_pk_bf16_f32 v122, v124, v125\n" \
"v_cvt_pk_bf16_f32 v123, v126, v127\n" \
"v_mfma_f32_16x16x32_bf16 v[188:191], a[188:191], v[104:107], v[188:191]\n" \
"ds_read_b128 a[132:135], v48 offset:9216\n" \
"ds_read_b128 a[148:151], v48 offset:13312\n" \
"buffer_load_dwordx4 v2, s[80:83], s92 offen offset:2048 lds\n" \
"v_mfma_f32_16x16x32_bf16 v[220:223], a[188:191], v[116:119], v[220:223]\n" \
"ds_read_b128 a[136:139], v48 offset:10240\n" \
"ds_read_b128 a[152:155], v48 offset:14336\n" \
"v_mfma_f32_16x16x32_bf16 v[252:255], a[188:191], v[120:123], v[252:255]\n" \
"ds_read_b128 a[140:143], v48 offset:11264\n" \
"ds_read_b128 a[156:159], v48 offset:15360\n" \
"v_mfma_f32_16x16x32_bf16 v[216:219], a[184:187], v[116:119], v[216:219]\n" \
"v_readlane_b32 s96, v11, 1\n" \
"v_bfe_u32 v27, v20, 2, 7\n" \
"buffer_load_dwordx4 v3, s[80:83], s92 offen offset:3072 lds\n" \
"v_mfma_f32_16x16x32_bf16 v[248:251], a[184:187], v[120:123], v[248:251]\n" \
"s_add_u32 m0, s93, 0x1000\n" \
"v_add_f32_e32 v54, v54, v22\n" \
"v_mfma_f32_16x16x32_bf16 v[212:215], a[180:183], v[116:119], v[212:215]\n" \
"v_max_f32_e32 v62, v62, v54\n" \
"v_add_u32_e32 v49, 0x10000, v48\n" \
"v_mfma_f32_16x16x32_bf16 v[244:247], a[180:183], v[120:123], v[244:247]\n" \
"v_rcp_f32_e32 v54, v54\n" \
"buffer_load_dwordx4 v4, s[80:83], s92 offen offset:0 lds\n" \
"v_mfma_f32_16x16x32_bf16 v[208:211], a[176:179], v[116:119], v[208:211]\n" \
"v_mul_f32_e32 v54, v54, v58\n" \
"v_mul_f32_e32 v128, v54, v128\n" \
"v_mfma_f32_16x16x32_bf16 v[240:243], a[176:179], v[120:123], v[240:243]\n" \
"v_mul_f32_e32 v129, v54, v129\n" \
"v_mul_f32_e32 v136, v54, v136\n" \
"v_mfma_f32_16x16x32_bf16 v[204:207], a[172:175], v[116:119], v[204:207]\n" \
"v_mul_f32_e32 v137, v54, v137\n" \
"v_mul_f32_e32 v144, v54, v144\n" \
"buffer_load_dwordx4 v5, s[80:83], s92 offen offset:1024 lds\n" \
"v_mfma_f32_16x16x32_bf16 v[236:239], a[172:175], v[120:123], v[236:239]\n" \
"v_mul_f32_e32 v145, v54, v145\n" \
"v_mul_f32_e32 v152, v54, v152\n" \
"v_mfma_f32_16x16x32_bf16 v[200:203], a[168:171], v[116:119], v[200:203]\n" \
"v_mul_f32_e32 v153, v54, v153\n" \
"v_cvt_pk_bf16_f32 v128, v128, v129\n" \
"v_mfma_f32_16x16x32_bf16 v[232:235], a[168:171], v[120:123], v[232:235]\n" \
"v_mul_f32_e32 v130, v54, v130\n" \
"v_mul_f32_e32 v131, v54, v131\n" \
"buffer_load_dwordx4 v6, s[80:83], s92 offen offset:2048 lds\n" \
"v_mfma_f32_16x16x32_bf16 v[196:199], a[164:167], v[116:119], v[196:199]\n" \
"v_cvt_pk_bf16_f32 v136, v136, v137\n" \
"v_mul_f32_e32 v138, v54, v138\n" \
"v_mfma_f32_16x16x32_bf16 v[228:231], a[164:167], v[120:123], v[228:231]\n" \
"v_mul_f32_e32 v139, v54, v139\n" \
"v_cvt_pk_bf16_f32 v144, v144, v145\n" \
"v_mfma_f32_16x16x32_bf16 v[192:195], a[160:163], v[116:119], v[192:195]\n" \
"v_mul_f32_e32 v146, v54, v146\n" \
"v_mul_f32_e32 v147, v54, v147\n" \
"buffer_load_dwordx4 v7, s[80:83], s92 offen offset:3072 lds\n" \
"v_mfma_f32_16x16x32_bf16 v[224:227], a[160:163], v[120:123], v[224:227]\n" \
"v_cvt_pk_bf16_f32 v152, v152, v153\n" \
"v_mul_f32_e32 v154, v54, v154\n" \
"v_smfmac_f32_16x16x64_bf16 a[196:199], a[200:203], v[116:123], v21\n" \
"v_mul_f32_e32 v155, v54, v155\n" \
"v_cvt_pk_bf16_f32 v129, v130, v131\n" \
"v_mul_f32_e32 v132, v54, v132\n" \
"v_mul_f32_e32 v133, v54, v133\n" \
"v_mul_f32_e32 v134, v54, v134\n" \
"v_mul_f32_e32 v135, v54, v135\n" \
"v_cvt_pk_bf16_f32 v137, v138, v139\n" \
"v_mul_f32_e32 v140, v54, v140\n" \
"v_mul_f32_e32 v141, v54, v141\n" \
"v_mul_f32_e32 v142, v54, v142\n" \
"v_mul_f32_e32 v143, v54, v143\n" \
"v_cvt_pk_bf16_f32 v145, v146, v147\n" \
"v_mul_f32_e32 v148, v54, v148\n" \
"v_mul_f32_e32 v149, v54, v149\n" \
"v_mul_f32_e32 v150, v54, v150\n" \
"v_mul_f32_e32 v151, v54, v151\n" \
"v_cvt_pk_bf16_f32 v153, v154, v155\n" \
"v_mul_f32_e32 v156, v54, v156\n" \
"v_mul_f32_e32 v157, v54, v157\n" \
"v_mul_f32_e32 v158, v54, v158\n" \
"v_mul_f32_e32 v159, v54, v159\n" \
"v_cvt_pk_bf16_f32 v130, v132, v133\n" \
"v_cvt_pk_bf16_f32 v131, v134, v135\n" \
"buffer_store_dwordx4 v[128:131], v20, s[56:59], s68 offen offset:0 sc1\n" \
"v_cvt_pk_bf16_f32 v138, v140, v141\n" \
"v_cvt_pk_bf16_f32 v139, v142, v143\n" \
"v_cvt_pk_bf16_f32 v146, v148, v149\n" \
"v_cvt_pk_bf16_f32 v147, v150, v151\n" \
"v_cvt_pk_bf16_f32 v154, v156, v157\n" \
"v_cvt_pk_bf16_f32 v155, v158, v159\n" \
"s_add_u32 m0, s93, 0x2000\n" \
"s_xor_b32 s65, s65, 0x8000\n" \
"s_add_u32 s64, s64, 0x10000\n" \
"v_exp_f32_e32 v86, v86\n" \
"buffer_store_dwordx4 v[136:139], v20, s[56:59], s68 offen offset:1024 sc1\n" \
"v_exp_f32_e32 v87, v87\n" \
"v_exp_f32_e32 v84, v84\n" \
"v_exp_f32_e32 v85, v85\n" \
"v_exp_f32_e32 v88, v88\n" \
"v_exp_f32_e32 v89, v89\n" \
"v_cvt_pk_bf16_f32 v87, v86, v87\n" \
"buffer_store_dwordx4 v[144:147], v20, s[56:59], s68 offen offset:2048 sc1\n" \
"v_exp_f32_e32 v82, v82\n" \
"v_exp_f32_e32 v83, v83\n" \
"v_exp_f32_e32 v80, v80\n" \
"v_exp_f32_e32 v81, v81\n" \
"v_exp_f32_e32 v90, v90\n" \
"v_exp_f32_e32 v91, v91\n" \
"buffer_store_dwordx4 v[152:155], v20, s[56:59], s68 offen offset:3072 sc1\n" \
"v_cvt_pk_bf16_f32 v86, v84, v85\n" \
"v_cvt_pk_bf16_f32 v88, v88, v89\n" \
"v_exp_f32_e32 v92, v92\n" \
"v_exp_f32_e32 v93, v93\n" \
"v_exp_f32_e32 v94, v94\n" \
"v_exp_f32_e32 v95, v95\n" \
"v_cvt_pk_bf16_f32 v85, v82, v83\n" \
"buffer_load_dwordx4 v8, s[80:83], s92 offen offset:0 lds\n" \
"v_cvt_pk_bf16_f32 v84, v80, v81\n" \
"v_cvt_pk_bf16_f32 v89, v90, v91\n" \
"v_and_b32_e32 v84, v12, v84\n" \
"v_and_b32_e32 v85, v13, v85\n" \
"v_and_b32_e32 v86, v14, v86\n" \
"v_and_b32_e32 v87, v15, v87\n" \
"s_waitcnt vmcnt(17) lgkmcnt(0)\n" \
"s_barrier\n" \
"v_mfma_f32_16x16x32_bf16 v[120:123], a[128:131], a[48:51], v[44:47]\n" \
"buffer_load_dword v26, v27, s[56:59], s96 offen sc1\n" \
"v_mfma_f32_16x16x32_bf16 v[120:123], a[132:135], a[52:55], v[120:123]\n" \
"ds_read_b128 a[188:191], v49 offset:7168\n" \
"ds_read_b128 a[184:187], v49 offset:6144\n" \
"v_mfma_f32_16x16x32_bf16 v[120:123], a[136:139], a[56:59], v[120:123]\n" \
"s_xor_b32 m0, s65, 0x12000\n" \
"ds_read_b128 a[180:183], v49 offset:5120\n" \
"v_mfma_f32_16x16x32_bf16 v[120:123], a[140:143], a[60:63], v[120:123]\n" \
"ds_read_b128 a[176:179], v49 offset:4096\n" \
"ds_read_b128 a[172:175], v49 offset:3072\n" \
"v_mfma_f32_16x16x32_bf16 v[124:127], a[156:159], a[60:63], v[44:47]\n" \
"buffer_load_dwordx4 v50, s[8:11], s64 offen offset:0 sc1 lds\n" \
"v_mfma_f32_16x16x32_bf16 v[124:127], a[152:155], a[56:59], v[124:127]\n" \
"ds_read_b128 a[168:171], v49 offset:2048\n" \
"v_cvt_pk_bf16_f32 v90, v92, v93\n" \
"v_mfma_f32_16x16x32_bf16 v[124:127], a[148:151], a[52:55], v[124:127]\n" \
"v_cvt_pk_bf16_f32 v91, v94, v95\n" \
"ds_read_b128 a[164:167], v49 offset:1024\n" \
"v_mfma_f32_16x16x32_bf16 v[124:127], a[144:147], a[48:51], v[124:127]\n" \
"ds_read_b128 a[160:163], v49 offset:0\n" \
"ds_read_b128 a[0:3], v49 offset:8192\n" \
"v_mfma_f32_16x16x32_bf16 v[32:35], a[220:223], a[64:67], 0\n" \
"buffer_load_dwordx4 v50, s[8:11], s64 offen offset:1024 sc1 lds\n" \
"v_mfma_f32_16x16x32_bf16 v[36:39], a[220:223], a[80:83], 0\n" \
"ds_read_b128 a[4:7], v49 offset:9216\n" \
"ds_read_b128 a[8:11], v49 offset:10240\n" \
"v_mfma_f32_16x16x32_bf16 v[32:35], a[224:227], a[68:71], v[32:35]\n" \
"ds_read_b128 a[12:15], v49 offset:11264\n" \
"ds_read_b128 a[16:19], v49 offset:12288\n" \
"v_mfma_f32_16x16x32_bf16 v[36:39], a[224:227], a[84:87], v[36:39]\n" \
"ds_read_b128 a[20:23], v49 offset:13312\n" \
"ds_read_b128 a[24:27], v49 offset:14336\n" \
"v_mfma_f32_16x16x32_bf16 v[32:35], a[228:231], a[72:75], v[32:35]\n" \
"buffer_load_dwordx4 v50, s[8:11], s64 offen offset:2048 sc1 lds\n" \
"v_mfma_f32_16x16x32_bf16 v[36:39], a[228:231], a[88:91], v[36:39]\n" \
"ds_read_b128 a[28:31], v49 offset:15360\n" \
"v_readlane_b32 s97, v11, 36\n" \
"v_mfma_f32_16x16x32_bf16 v[32:35], a[232:235], a[76:79], v[32:35]\n" \
"v_exp_f32_e32 v120, v120\n" \
"v_mfma_f32_16x16x32_bf16 v[36:39], a[232:235], a[92:95], v[36:39]\n" \
"v_exp_f32_e32 v121, v121\n" \
"v_smfmac_f32_16x16x64_bf16 a[196:199], a[200:203], v[84:91], v21\n" \
"v_cvt_pk_bf16_f32 v120, v120, v121\n" \
"s_waitcnt lgkmcnt(13)\n" \
"v_mfma_f32_16x16x32_bf16 v[220:223], a[188:191], v[84:87], v[220:223]\n" \
"buffer_load_dwordx4 v50, s[8:11], s64 offen offset:3072 sc1 lds\n" \
"v_mfma_f32_16x16x32_bf16 v[252:255], a[188:191], v[88:91], v[252:255]\n" \
"v_exp_f32_e32 v122, v122\n" \
"v_mfma_f32_16x16x32_bf16 v[216:219], a[184:187], v[84:87], v[216:219]\n" \
"v_exp_f32_e32 v123, v123\n" \
"v_mfma_f32_16x16x32_bf16 v[248:251], a[184:187], v[88:91], v[248:251]\n" \
"v_exp_f32_e32 v124, v124\n" \
"v_mfma_f32_16x16x32_bf16 v[212:215], a[180:183], v[84:87], v[212:215]\n" \
"v_exp_f32_e32 v125, v125\n" \
"v_mfma_f32_16x16x32_bf16 v[244:247], a[180:183], v[88:91], v[244:247]\n" \
"v_cvt_pk_bf16_f32 v121, v122, v123\n" \
"s_waitcnt lgkmcnt(7)\n" \
"v_mfma_f32_16x16x32_bf16 v[208:211], a[176:179], v[84:87], v[208:211]\n" \
"v_exp_f32_e32 v126, v126\n" \
"v_mfma_f32_16x16x32_bf16 v[240:243], a[176:179], v[88:91], v[240:243]\n" \
"v_exp_f32_e32 v127, v127\n" \
"v_mfma_f32_16x16x32_bf16 v[204:207], a[172:175], v[84:87], v[204:207]\n" \
"v_cvt_pk_bf16_f32 v122, v124, v125\n" \
"v_cvt_pk_bf16_f32 v123, v126, v127\n" \
"v_mfma_f32_16x16x32_bf16 v[236:239], a[172:175], v[88:91], v[236:239]\n" \
"v_and_b32_e32 v120, v12, v120\n" \
"v_and_b32_e32 v121, v13, v121\n" \
"v_mfma_f32_16x16x32_bf16 v[200:203], a[168:171], v[84:87], v[200:203]\n" \
"v_and_b32_e32 v122, v14, v122\n" \
"v_and_b32_e32 v123, v15, v123\n" \
"v_mfma_f32_16x16x32_bf16 v[232:235], a[168:171], v[88:91], v[232:235]\n" \
"v_accvgpr_read_b32 v56, a196\n" \
"v_accvgpr_read_b32 v24, a197\n" \
"v_mfma_f32_16x16x32_bf16 v[196:199], a[164:167], v[84:87], v[196:199]\n" \
"v_add_u32_e32 v17, 0x49000, v20\n" \
"v_readlane_b32 s98, v11, 37\n" \
"v_mfma_f32_16x16x32_bf16 v[228:231], a[164:167], v[88:91], v[228:231]\n" \
"v_add_u32_e32 v48, s16, v20\n" \
"ds_read_b128 a[128:131], v48 offset:0\n" \
"v_mfma_f32_16x16x32_bf16 v[192:195], a[160:163], v[84:87], v[192:195]\n" \
"ds_read_b128 a[144:147], v48 offset:4096\n" \
"ds_read_b128 a[132:135], v48 offset:1024\n" \
"v_mfma_f32_16x16x32_bf16 v[224:227], a[160:163], v[88:91], v[224:227]\n" \
"ds_read_b128 a[148:151], v48 offset:5120\n" \
"ds_read_b128 a[136:139], v48 offset:2048\n" \
"v_mfma_f32_16x16x32_bf16 a[196:199], a[212:215], v[120:123], a[196:199]\n" \
"ds_read_b128 a[152:155], v48 offset:6144\n" \
"ds_read_b128 a[140:143], v48 offset:3072\n" \
"v_mfma_f32_16x16x32_bf16 v[224:227], a[0:3], v[120:123], v[224:227]\n" \
"ds_read_b128 a[156:159], v48 offset:7168\n" \
"s_waitcnt lgkmcnt(8)\n" \
"v_mfma_f32_16x16x32_bf16 v[228:231], a[4:7], v[120:123], v[228:231]\n" \
"v_accvgpr_read_b32 v55, a194\n" \
"v_accvgpr_read_b32 v23, a195\n" \
"v_mfma_f32_16x16x32_bf16 a[192:195], a[216:219], a[216:219], 0\n" \
"v_add_f32_e32 v55, v55, v23\n" \
"v_max_f32_e32 v62, v62, v55\n" \
"v_mfma_f32_16x16x32_bf16 v[232:235], a[8:11], v[120:123], v[232:235]\n" \
"v_rcp_f32_e32 v55, v55\n" \
"v_mfma_f32_16x16x32_bf16 v[236:239], a[12:15], v[120:123], v[236:239]\n" \
"v_mul_f32_e32 v55, v55, v59\n" \
"v_mul_f32_e32 v160, v55, v160\n" \
"v_mfma_f32_16x16x32_bf16 v[240:243], a[16:19], v[120:123], v[240:243]\n" \
"v_mul_f32_e32 v161, v55, v161\n" \
"v_mul_f32_e32 v168, v55, v168\n" \
"v_mfma_f32_16x16x32_bf16 v[244:247], a[20:23], v[120:123], v[244:247]\n" \
"v_mul_f32_e32 v169, v55, v169\n" \
"v_mul_f32_e32 v176, v55, v176\n" \
"v_mfma_f32_16x16x32_bf16 v[248:251], a[24:27], v[120:123], v[248:251]\n" \
"v_mul_f32_e32 v177, v55, v177\n" \
"v_mul_f32_e32 v184, v55, v184\n" \
"v_mfma_f32_16x16x32_bf16 v[252:255], a[28:31], v[120:123], v[252:255]\n" \
"v_mul_f32_e32 v185, v55, v185\n" \
"v_cvt_pk_bf16_f32 v160, v160, v161\n" \
"v_mul_f32_e32 v162, v55, v162\n" \
"v_mul_f32_e32 v163, v55, v163\n" \
"v_cvt_pk_bf16_f32 v168, v168, v169\n" \
"v_mul_f32_e32 v170, v55, v170\n" \
"v_mul_f32_e32 v171, v55, v171\n" \
"v_cvt_pk_bf16_f32 v176, v176, v177\n" \
"v_mul_f32_e32 v178, v55, v178\n" \
"s_waitcnt vmcnt(4)\n" \
"v_cmp_ne_u32_e32 vcc, s62, v26\n" \
"v_mul_f32_e32 v179, v55, v179\n" \
"v_cvt_pk_bf16_f32 v184, v184, v185\n" \
"v_mul_f32_e32 v186, v55, v186\n" \
"v_mul_f32_e32 v187, v55, v187\n" \
"v_cvt_pk_bf16_f32 v161, v162, v163\n" \
"s_cbranch_vccnz L_sos_%=\n" \
"L_ros_%=:\n" \
"buffer_load_dwordx4 a[220:223], v20, s[56:59], s97 offen offset:0 sc1\n" \
"v_mul_f32_e32 v164, v55, v164\n" \
"v_mul_f32_e32 v165, v55, v165\n" \
"v_mul_f32_e32 v166, v55, v166\n" \
"v_mul_f32_e32 v167, v55, v167\n" \
"v_cvt_pk_bf16_f32 v169, v170, v171\n" \
"v_mul_f32_e32 v172, v55, v172\n" \
"v_mul_f32_e32 v173, v55, v173\n" \
"v_mul_f32_e32 v174, v55, v174\n" \
"v_mul_f32_e32 v175, v55, v175\n" \
"v_cvt_pk_bf16_f32 v177, v178, v179\n" \
"v_mul_f32_e32 v180, v55, v180\n" \
"buffer_load_dwordx4 a[228:231], v17, s[56:59], s97 offen offset:0 sc1\n" \
"v_mul_f32_e32 v181, v55, v181\n" \
"v_mul_f32_e32 v182, v55, v182\n" \
"v_mul_f32_e32 v183, v55, v183\n" \
"v_cvt_pk_bf16_f32 v185, v186, v187\n" \
"v_mul_f32_e32 v188, v55, v188\n" \
"v_mul_f32_e32 v189, v55, v189\n" \
"v_mul_f32_e32 v190, v55, v190\n" \
"v_mul_f32_e32 v191, v55, v191\n" \
"v_cvt_pk_bf16_f32 v162, v164, v165\n" \
"v_cvt_pk_bf16_f32 v163, v166, v167\n" \
"v_cvt_pk_bf16_f32 v170, v172, v173\n" \
"buffer_store_dwordx4 v[160:163], v20, s[56:59], s69 offen offset:0 sc1\n" \
"v_cvt_pk_bf16_f32 v171, v174, v175\n" \
"v_cvt_pk_bf16_f32 v178, v180, v181\n" \
"v_cvt_pk_bf16_f32 v179, v182, v183\n" \
"v_cvt_pk_bf16_f32 v186, v188, v189\n" \
"v_cvt_pk_bf16_f32 v187, v190, v191\n" \
"v_readlane_b32 s93, v11, 20\n" \
"v_readlane_b32 s84, v11, 15\n" \
"v_readlane_b32 s85, v11, 16\n" \
"v_readlane_b32 s86, v11, 17\n" \
"s_mov_b32 s87, 0x20000\n" \
"v_readlane_b32 s92, v11, 18\n" \
"buffer_store_dwordx4 v[168:171], v20, s[56:59], s69 offen offset:1024 sc1\n" \
"v_readlane_b32 s91, v11, 21\n" \
"v_mfma_f32_16x16x32_bf16 a[208:211], a[216:219], v[128:131], a[208:211]\n" \
"v_mfma_f32_16x16x32_bf16 a[208:211], a[216:219], v[136:139], a[208:211]\n" \
"v_mfma_f32_32x32x16_bf16 v[128:143], a[216:219], a[216:219], 0\n" \
"buffer_store_dwordx4 v[176:179], v20, s[56:59], s69 offen offset:2048 sc1\n" \
"v_mfma_f32_16x16x32_bf16 a[208:211], a[216:219], v[144:147], a[208:211]\n" \
"v_mfma_f32_16x16x32_bf16 a[208:211], a[216:219], v[152:155], a[208:211]\n" \
"v_mfma_f32_32x32x16_bf16 v[144:159], a[216:219], a[216:219], 0\n" \
"buffer_store_dwordx4 v[184:187], v20, s[56:59], s69 offen offset:3072 sc1\n" \
"v_add_f32_e32 v56, v56, v24\n" \
"v_mfma_f32_16x16x32_bf16 a[208:211], a[216:219], v[160:163], a[208:211]\n" \
"v_max_f32_e32 v62, v62, v56\n" \
"v_rcp_f32_e32 v56, v56\n" \
"v_mfma_f32_16x16x32_bf16 a[208:211], a[216:219], v[168:171], a[208:211]\n" \
"v_mfma_f32_32x32x16_bf16 v[160:175], a[216:219], a[216:219], 0\n" \
"buffer_load_dwordx4 a[236:239], v20, s[56:59], s98 offen offset:0 sc1\n" \
"v_mul_f32_e32 v56, v56, v60\n" \
"v_mfma_f32_16x16x32_bf16 a[208:211], a[216:219], v[176:179], a[208:211]\n" \
"v_mul_f32_e32 v192, v56, v192\n" \
"v_mul_f32_e32 v193, v56, v193\n" \
"v_cvt_pk_bf16_f32 v192, v192, v193\n" \
"v_mul_f32_e32 v194, v56, v194\n" \
"v_mul_f32_e32 v195, v56, v195\n" \
"buffer_load_dwordx4 a[244:247], v17, s[56:59], s98 offen offset:0 sc1\n" \
"v_mfma_f32_16x16x32_bf16 a[208:211], a[216:219], v[184:187], a[208:211]\n" \
"v_mfma_f32_32x32x16_bf16 v[176:191], a[216:219], a[216:219], 0\n" \
"v_cvt_pk_bf16_f32 v193, v194, v195\n" \
"v_mul_f32_e32 v196, v56, v196\n" \
"v_mul_f32_e32 v197, v56, v197\n" \
"v_mul_f32_e32 v198, v56, v198\n" \
"v_mul_f32_e32 v199, v56, v199\n" \
"buffer_load_dwordx4 a[224:227], v20, s[56:59], s97 offen offset:1024 sc1\n" \
"v_mul_f32_e32 v200, v56, v200\n" \
"v_mul_f32_e32 v201, v56, v201\n" \
"v_accvgpr_read_b32 v57, a198\n" \
"v_accvgpr_read_b32 v25, a199\n" \
"v_mfma_f32_16x16x32_bf16 a[196:199], a[216:219], a[216:219], 0\n" \
"v_cvt_pk_bf16_f32 v194, v196, v197\n" \
"v_cvt_pk_bf16_f32 v195, v198, v199\n" \
"v_cvt_pk_bf16_f32 v200, v200, v201\n" \
"v_mul_f32_e32 v202, v56, v202\n" \
"v_mul_f32_e32 v203, v56, v203\n" \
"buffer_store_dwordx4 v[192:195], v20, s[56:59], s70 offen offset:0 sc1\n" \
"v_add_f32_e32 v57, v57, v25\n" \
"v_mfma_f32_16x16x32_bf16 a[208:211], a[216:219], v[192:195], a[208:211]\n" \
"v_cvt_pk_bf16_f32 v201, v202, v203\n" \
"v_mul_f32_e32 v204, v56, v204\n" \
"v_mul_f32_e32 v205, v56, v205\n" \
"v_mul_f32_e32 v206, v56, v206\n" \
"v_mul_f32_e32 v207, v56, v207\n" \
"v_mul_f32_e32 v208, v56, v208\n" \
"v_mul_f32_e32 v209, v56, v209\n" \
"v_max_f32_e32 v62, v62, v57\n" \
"buffer_load_dwordx4 a[232:235], v17, s[56:59], s97 offen offset:1024 sc1\n" \
"s_waitcnt vmcnt(16) lgkmcnt(0)\n" \
"s_barrier\n" \
"v_mfma_f32_16x16x32_bf16 v[64:67], a[128:131], a[64:67], v[32:35]\n" \
"s_add_u32 m0, s93, 0x0\n" \
"v_cvt_pk_bf16_f32 v202, v204, v205\n" \
"v_mfma_f32_16x16x32_bf16 v[64:67], a[132:135], a[68:71], v[64:67]\n" \
"buffer_load_dwordx4 a[240:243], v20, s[56:59], s98 offen offset:1024 sc1\n" \
"v_mfma_f32_16x16x32_bf16 v[64:67], a[136:139], a[72:75], v[64:67]\n" \
"v_cvt_pk_bf16_f32 v203, v206, v207\n" \
"v_cvt_pk_bf16_f32 v208, v208, v209\n" \
"v_mfma_f32_16x16x32_bf16 v[64:67], a[140:143], a[76:79], v[64:67]\n" \
"v_mul_f32_e32 v210, v56, v210\n" \
"v_mul_f32_e32 v211, v56, v211\n" \
"v_mfma_f32_16x16x32_bf16 v[68:71], a[156:159], a[76:79], v[32:35]\n" \
"v_rcp_f32_e32 v57, v57\n" \
"buffer_store_dwordx4 v[200:203], v20, s[56:59], s70 offen offset:1024 sc1\n" \
"v_mfma_f32_16x16x32_bf16 v[68:71], a[152:155], a[72:75], v[68:71]\n" \
"v_mfma_f32_16x16x32_bf16 a[208:211], a[216:219], v[200:203], a[208:211]\n" \
"v_mfma_f32_32x32x16_bf16 v[192:207], a[216:219], a[216:219], 0\n" \
"v_cvt_pk_bf16_f32 v209, v210, v211\n" \
"buffer_load_dwordx4 a[248:251], v17, s[56:59], s98 offen offset:1024 sc1\n" \
"v_mul_f32_e32 v212, v56, v212\n" \
"v_mul_f32_e32 v213, v56, v213\n" \
"v_mul_f32_e32 v214, v56, v214\n" \
"v_mfma_f32_16x16x32_bf16 v[68:71], a[148:151], a[68:71], v[68:71]\n" \
"v_mul_f32_e32 v215, v56, v215\n" \
"v_mul_f32_e32 v216, v56, v216\n" \
"v_mfma_f32_16x16x32_bf16 v[68:71], a[144:147], a[64:67], v[68:71]\n" \
"v_mul_f32_e32 v217, v56, v217\n" \
"v_cvt_pk_bf16_f32 v210, v212, v213\n" \
"buffer_load_dwordx4 v0, s[84:87], s92 offen offset:0 lds\n" \
"v_mfma_f32_16x16x32_bf16 v[76:79], a[144:147], a[80:83], v[36:39]\n" \
"ds_read_b128 a[144:147], v48 offset:12288\n" \
"v_cvt_pk_bf16_f32 v211, v214, v215\n" \
"v_mfma_f32_16x16x32_bf16 v[76:79], a[148:151], a[84:87], v[76:79]\n" \
"ds_read_b128 a[148:151], v48 offset:13312\n" \
"v_cvt_pk_bf16_f32 v216, v216, v217\n" \
"v_mfma_f32_16x16x32_bf16 v[76:79], a[152:155], a[88:91], v[76:79]\n" \
"ds_read_b128 a[152:155], v48 offset:14336\n" \
"v_mul_f32_e32 v218, v56, v218\n" \
"buffer_store_dwordx4 v[208:211], v20, s[56:59], s70 offen offset:2048 sc1\n" \
"v_mfma_f32_16x16x32_bf16 v[76:79], a[156:159], a[92:95], v[76:79]\n" \
"ds_read_b128 a[156:159], v48 offset:15360\n" \
"v_mul_f32_e32 v219, v56, v219\n" \
"v_mfma_f32_16x16x32_bf16 v[72:75], a[140:143], a[92:95], v[36:39]\n" \
"ds_read_b128 a[140:143], v48 offset:11264\n" \
"v_mul_f32_e32 v57, v57, v61\n" \
"v_mfma_f32_16x16x32_bf16 v[72:75], a[136:139], a[88:91], v[72:75]\n" \
".fill (L_ab + 28672 - .) / 4, 4, 0xBF800000\n" \
"L_ap7:\n" \
"s_cbranch_execz L_ap0\n" \
"ds_read_b128 a[136:139], v48 offset:10240\n" \
"v_cvt_pk_bf16_f32 v217, v218, v219\n" \
"buffer_load_dwordx4 v1, s[84:87], s92 offen offset:1024 lds\n" \
"v_mfma_f32_16x16x32_bf16 v[72:75], a[132:135], a[84:87], v[72:75]\n" \
"ds_read_b128 a[132:135], v48 offset:9216\n" \
"v_mul_f32_e32 v220, v56, v220\n" \
"v_mfma_f32_16x16x32_bf16 v[72:75], a[128:131], a[80:83], v[72:75]\n" \
"ds_read_b128 a[128:131], v48 offset:8192\n" \
"v_mfma_f32_16x16x32_bf16 a[208:211], a[216:219], v[208:211], a[208:211]\n" \
"v_mul_f32_e32 v221, v56, v221\n" \
"buffer_load_dwordx4 v2, s[84:87], s92 offen offset:2048 lds\n" \
"v_mul_f32_e32 v222, v56, v222\n" \
"v_mul_f32_e32 v223, v56, v223\n" \
"v_mul_f32_e32 v224, v57, v224\n" \
"v_mul_f32_e32 v225, v57, v225\n" \
"v_cvt_pk_bf16_f32 v218, v220, v221\n" \
"v_cvt_pk_bf16_f32 v219, v222, v223\n" \
"v_cvt_pk_bf16_f32 v224, v224, v225\n" \
"v_mul_f32_e32 v226, v57, v226\n" \
"v_mul_f32_e32 v227, v57, v227\n" \
"v_mfma_f32_16x16x32_bf16 a[208:211], a[216:219], v[216:219], a[208:211]\n" \
"buffer_store_dwordx4 v[216:219], v20, s[56:59], s70 offen offset:3072 sc1\n" \
"v_cvt_pk_bf16_f32 v225, v226, v227\n" \
"v_mul_f32_e32 v228, v57, v228\n" \
"v_mfma_f32_32x32x16_bf16 v[208:223], a[216:219], a[216:219], 0\n" \
"v_mul_f32_e32 v229, v57, v229\n" \
"v_mul_f32_e32 v230, v57, v230\n" \
"v_mul_f32_e32 v231, v57, v231\n" \
"v_mul_f32_e32 v232, v57, v232\n" \
"v_mul_f32_e32 v233, v57, v233\n" \
"v_cvt_pk_bf16_f32 v226, v228, v229\n" \
"v_cvt_pk_bf16_f32 v227, v230, v231\n" \
"buffer_store_dwordx4 v[224:227], v20, s[56:59], s71 offen offset:0 sc1\n" \
"v_cvt_pk_bf16_f32 v232, v232, v233\n" \
"v_mul_f32_e32 v234, v57, v234\n" \
"v_mul_f32_e32 v235, v57, v235\n" \
"v_mfma_f32_16x16x32_bf16 a[208:211], a[216:219], v[224:227], a[208:211]\n" \
"v_cvt_pk_bf16_f32 v233, v234, v235\n" \
"v_mul_f32_e32 v236, v57, v236\n" \
"v_mul_f32_e32 v237, v57, v237\n" \
"v_mul_f32_e32 v238, v57, v238\n" \
"v_mul_f32_e32 v239, v57, v239\n" \
"v_mul_f32_e32 v240, v57, v240\n" \
"buffer_load_dwordx4 v3, s[84:87], s92 offen offset:3072 lds\n" \
"s_add_u32 m0, s91, 0x1000\n" \
"v_mul_f32_e32 v241, v57, v241\n" \
"v_cvt_pk_bf16_f32 v234, v236, v237\n" \
"v_cvt_pk_bf16_f32 v235, v238, v239\n" \
"v_cvt_pk_bf16_f32 v240, v240, v241\n" \
"v_mul_f32_e32 v242, v57, v242\n" \
"v_mul_f32_e32 v243, v57, v243\n" \
"v_mfma_f32_16x16x32_bf16 a[208:211], a[216:219], v[232:235], a[208:211]\n" \
"v_cvt_pk_bf16_f32 v241, v242, v243\n" \
"v_mul_f32_e32 v244, v57, v244\n" \
"buffer_store_dwordx4 v[232:235], v20, s[56:59], s71 offen offset:1024 sc1\n" \
"v_mul_f32_e32 v245, v57, v245\n" \
"v_mul_f32_e32 v246, v57, v246\n" \
"v_mfma_f32_32x32x16_bf16 v[224:239], a[216:219], a[216:219], 0\n" \
"v_mul_f32_e32 v247, v57, v247\n" \
"v_mul_f32_e32 v248, v57, v248\n" \
"v_mul_f32_e32 v249, v57, v249\n" \
"v_cvt_pk_bf16_f32 v242, v244, v245\n" \
"v_cvt_pk_bf16_f32 v243, v246, v247\n" \
"v_cvt_pk_bf16_f32 v248, v248, v249\n" \
"v_mul_f32_e32 v250, v57, v250\n" \
"buffer_store_dwordx4 v[240:243], v20, s[56:59], s71 offen offset:2048 sc1\n" \
"v_mul_f32_e32 v251, v57, v251\n" \
"v_mfma_f32_16x16x32_bf16 a[208:211], a[216:219], v[240:243], a[208:211]\n" \
"v_cvt_pk_bf16_f32 v249, v250, v251\n" \
"v_mul_f32_e32 v252, v57, v252\n" \
"v_mul_f32_e32 v253, v57, v253\n" \
"v_mul_f32_e32 v254, v57, v254\n" \
"v_mul_f32_e32 v255, v57, v255\n" \
"v_cvt_pk_bf16_f32 v250, v252, v253\n" \
"v_cvt_pk_bf16_f32 v251, v254, v255\n" \
"v_add_u32_e32 v53, 0x20000, v63\n" \
"buffer_store_dwordx4 v[248:251], v20, s[56:59], s71 offen offset:3072 sc1\n" \
"v_mfma_f32_16x16x32_bf16 a[208:211], a[216:219], v[248:251], a[208:211]\n" \
"v_exp_f32_e32 v70, v70\n" \
"v_mfma_f32_32x32x16_bf16 v[240:255], a[216:219], a[216:219], 0\n" \
"v_exp_f32_e32 v71, v71\n" \
"v_exp_f32_e32 v72, v72\n" \
"v_exp_f32_e32 v73, v73\n" \
"buffer_load_dwordx4 v4, s[84:87], s92 offen offset:0 lds\n" \
"s_add_u32 s64, s64, 0x10000\n" \
"v_cmp_lt_f32_e64 s[72:73], s21, v62\n" \
"v_accvgpr_read_b32 v63, a208\n" \
"v_cmp_u_f32_e32 vcc, v63, v63\n" \
"v_readlane_b32 s95, v11, 3\n" \
"v_exp_f32_e32 v68, v68\n" \
"v_exp_f32_e32 v69, v69\n" \
"v_exp_f32_e32 v74, v74\n" \
"buffer_load_dwordx4 v5, s[84:87], s92 offen offset:1024 lds\n" \
"v_exp_f32_e32 v75, v75\n" \
"s_or_b64 vcc, s[72:73], vcc\n" \
"s_cselect_b32 s95, 0x40000000, s95\n" \
"s_cselect_b32 s94, 0x7f800000, 0\n" \
"v_cvt_pk_bf16_f32 v71, v70, v71\n" \
"v_exp_f32_e32 v66, v66\n" \
"v_exp_f32_e32 v67, v67\n" \
"v_exp_f32_e32 v64, v64\n" \
"buffer_load_dwordx4 v6, s[84:87], s92 offen offset:2048 lds\n" \
"v_exp_f32_e32 v65, v65\n" \
"v_cvt_pk_bf16_f32 v72, v72, v73\n" \
"v_exp_f32_e32 v76, v76\n" \
"v_exp_f32_e32 v77, v77\n" \
"v_exp_f32_e32 v78, v78\n" \
"v_exp_f32_e32 v79, v79\n" \
"buffer_load_dwordx4 v7, s[84:87], s92 offen offset:3072 lds\n" \
"s_add_u32 m0, s91, 0x2000\n" \
"v_cvt_pk_bf16_f32 v70, v68, v69\n" \
"v_cvt_pk_bf16_f32 v73, v74, v75\n" \
"v_add_u32_e32 v49, 0x10000, v48\n" \
"s_xor_b32 s65, s65, 0x8000\n" \
"v_cvt_pk_bf16_f32 v69, v66, v67\n" \
"v_cvt_pk_bf16_f32 v68, v64, v65\n" \
"v_cvt_pk_bf16_f32 v74, v76, v77\n" \
"v_cvt_pk_bf16_f32 v75, v78, v79\n" \
"v_xor_b32_e32 v48, 0x8000, v48\n" \
"s_sub_u32 s66, s19, 2\n" \
"buffer_load_dwordx4 v8, s[84:87], s92 offen offset:0 lds\n" \
"s_waitcnt vmcnt(18) lgkmcnt(0)\n" \
"s_barrier\n" \
"s_branch L_f1_%=\n" \
".fill (L_ab + 32768 - .) / 4, 4, 0xBF800000\n" \
".fill (L_ab + 32768 - .) / 4, 4, 0xBF800000\n" \
"L_ap8:\n" \
"s_setpc_b64 s[88:89]\n" \
"L_out_%=:\n" \
"v_bfe_u32 v38, v20, 2, 4\n" \
"v_readlane_b32 s64, v11, 2\n" \
"v_add_u32_e32 v32, 0x49000, v20\n" \
"v_add_u32_e32 v34, 0x20000, v10\n" \
"ds_read_b64_tr_b16 a[0:1], v34 offset:0\n" \
"ds_read_b64_tr_b16 a[2:3], v34 offset:4608\n" \
"ds_read_b64_tr_b16 a[4:5], v34 offset:32\n" \
"buffer_load_dword v26, v38, s[56:59], s64 offen sc1\n" \
"ds_read_b64_tr_b16 a[6:7], v34 offset:4640\n" \
"ds_read_b64_tr_b16 a[8:9], v34 offset:64\n" \
"ds_read_b64_tr_b16 a[10:11], v34 offset:4672\n" \
"ds_read_b64_tr_b16 a[12:13], v34 offset:96\n" \
"buffer_store_dwordx4 v[64:67], v20, s[56:59], s61 offen offset:0 sc1\n" \
"ds_read_b64_tr_b16 a[14:15], v34 offset:4704\n" \
"v_mov_b32_e32 v39, s62\n" \
"v_readlane_b32 s70, v11, 5\n" \
"v_readlane_b32 s71, v11, 4\n" \
"buffer_store_dwordx4 v[68:71], v20, s[56:59], s61 offen offset:1024 sc1\n" \
"ds_read_b64_tr_b16 a[16:17], v34 offset:128\n" \
"ds_read_b64_tr_b16 a[18:19], v34 offset:4736\n" \
"ds_read_b64_tr_b16 a[20:21], v34 offset:160\n" \
"ds_read_b64_tr_b16 a[22:23], v34 offset:4768\n" \
"buffer_store_dwordx4 v[72:75], v20, s[56:59], s61 offen offset:2048 sc1\n" \
"ds_read_b64_tr_b16 a[24:25], v34 offset:192\n" \
"ds_read_b64_tr_b16 a[26:27], v34 offset:4800\n" \
"ds_read_b64_tr_b16 a[28:29], v34 offset:224\n" \
"ds_read_b64_tr_b16 a[30:31], v34 offset:4832\n" \
"buffer_store_dwordx4 v[76:79], v20, s[56:59], s61 offen offset:3072 sc1\n" \
"ds_read_b64_tr_b16 a[192:193], v34 offset:256\n" \
"ds_read_b64_tr_b16 a[194:195], v34 offset:4864\n" \
"ds_read_b64_tr_b16 a[196:197], v34 offset:9216\n" \
"ds_read_b64_tr_b16 a[198:199], v34 offset:13824\n" \
"buffer_store_dwordx4 v[80:83], v32, s[56:59], s61 offen offset:0 sc1\n" \
"ds_read_b64_tr_b16 a[200:201], v34 offset:9248\n" \
"ds_read_b64_tr_b16 a[202:203], v34 offset:13856\n" \
"ds_read_b64_tr_b16 a[204:205], v34 offset:9280\n" \
"ds_read_b64_tr_b16 a[206:207], v34 offset:13888\n" \
"buffer_store_dwordx4 v[84:87], v32, s[56:59], s61 offen offset:1024 sc1\n" \
"ds_read_b64_tr_b16 a[208:209], v34 offset:9312\n" \
"s_waitcnt vmcnt(16) lgkmcnt(15)\n" \
"v_mfma_f32_16x16x32_bf16 v[112:115], a[0:3], a[96:99], 0\n" \
"buffer_store_dwordx4 v[96:99], v32, s[56:59], s61 offen offset:2048 sc1\n" \
"v_mfma_f32_16x16x32_bf16 v[116:119], a[0:3], a[112:115], 0\n" \
"ds_read_b64_tr_b16 a[210:211], v34 offset:13920\n" \
"ds_read_b64_tr_b16 a[212:213], v34 offset:9344\n" \
"buffer_store_dwordx4 v[100:103], v32, s[56:59], s61 offen offset:3072 sc1\n" \
"v_mfma_f32_16x16x32_bf16 v[184:187], a[0:3], v[64:67], 0\n" \
"ds_read_b64_tr_b16 a[214:215], v34 offset:13952\n" \
"ds_read_b64_tr_b16 a[216:217], v34 offset:9376\n" \
"v_mfma_f32_16x16x32_bf16 v[188:191], a[0:3], v[80:83], 0\n" \
"ds_read_b64_tr_b16 a[218:219], v34 offset:13984\n" \
"ds_read_b64_tr_b16 a[0:1], v34 offset:9408\n" \
"v_mfma_f32_16x16x32_bf16 v[120:123], a[4:7], a[96:99], 0\n" \
"ds_read_b64_tr_b16 a[2:3], v34 offset:14016\n" \
"v_readlane_b32 s65, v11, 30\n" \
"v_mfma_f32_16x16x32_bf16 v[124:127], a[4:7], a[112:115], 0\n" \
"v_lshrrev_b32_e32 v44, 9, v20\n" \
"v_mul_u32_u24_e32 v44, 0xb40, v44\n" \
"v_mfma_f32_16x16x32_bf16 v[192:195], a[4:7], v[64:67], 0\n" \
"v_add_u32_e32 v35, v44, v10\n" \
"v_readlane_b32 s67, v11, 23\n" \
"v_mfma_f32_16x16x32_bf16 v[196:199], a[4:7], v[80:83], 0\n" \
"ds_read_b64_tr_b16 a[4:5], v34 offset:9440\n" \
"ds_read_b64_tr_b16 a[6:7], v34 offset:14048\n" \
"v_mfma_f32_16x16x32_bf16 v[128:131], a[8:11], a[96:99], 0\n" \
"v_readlane_b32 s80, v11, 11\n" \
"v_readlane_b32 s81, v11, 12\n" \
"v_mfma_f32_16x16x32_bf16 v[132:135], a[8:11], a[112:115], 0\n" \
"s_mov_b32 s82, 0x47fca0\n" \
"s_mov_b32 s83, 0x20000\n" \
"v_mfma_f32_16x16x32_bf16 v[200:203], a[8:11], v[64:67], 0\n" \
"v_readlane_b32 s66, v11, 28\n" \
"s_add_u32 m0, s67, 0x0\n" \
"v_mfma_f32_16x16x32_bf16 v[204:207], a[8:11], v[80:83], 0\n" \
"ds_read_b64_tr_b16 a[8:9], v34 offset:9472\n" \
"ds_read_b64_tr_b16 a[10:11], v34 offset:14080\n" \
"v_mfma_f32_16x16x32_bf16 v[136:139], a[12:15], a[96:99], 0\n" \
"buffer_load_dwordx4 v0, s[80:83], s66 offen offset:0 lds\n" \
"v_mfma_f32_16x16x32_bf16 v[140:143], a[12:15], a[112:115], 0\n" \
"v_readlane_b32 s92, v11, 24\n" \
"v_readlane_b32 s73, v11, 29\n" \
"v_mfma_f32_16x16x32_bf16 v[208:211], a[12:15], v[64:67], 0\n" \
"v_readlane_b32 s94, v11, 25\n" \
"v_readlane_b32 s84, v11, 9\n" \
"v_mfma_f32_16x16x32_bf16 v[212:215], a[12:15], v[80:83], 0\n" \
"ds_read_b64_tr_b16 a[12:13], v34 offset:18432\n" \
"ds_read_b64_tr_b16 a[14:15], v34 offset:23040\n" \
"v_mfma_f32_16x16x32_bf16 v[144:147], a[16:19], a[96:99], 0\n" \
"v_readlane_b32 s85, v11, 10\n" \
"s_mov_b32 s86, 0xa1ca0\n" \
"v_mfma_f32_16x16x32_bf16 v[148:151], a[16:19], a[112:115], 0\n" \
"buffer_load_dwordx4 v1, s[80:83], s66 offen offset:1024 lds\n" \
"v_mfma_f32_16x16x32_bf16 v[216:219], a[16:19], v[64:67], 0\n" \
"s_mov_b32 s87, 0x20000\n" \
"v_readlane_b32 s68, v11, 31\n" \
"v_mfma_f32_16x16x32_bf16 v[220:223], a[16:19], v[80:83], 0\n" \
"ds_read_b64_tr_b16 a[16:17], v34 offset:18464\n" \
"s_waitcnt lgkmcnt(7)\n" \
"v_mfma_f32_16x16x32_bf16 v[152:155], a[20:23], a[96:99], 0\n" \
"ds_read_b64_tr_b16 a[18:19], v34 offset:23072\n" \
"v_add_u32_e32 v33, 0x10000, v10\n" \
"v_mfma_f32_16x16x32_bf16 v[156:159], a[20:23], a[112:115], 0\n" \
"v_readlane_b32 s69, v11, 32\n" \
"v_readlane_b32 s72, v11, 34\n" \
"v_mfma_f32_16x16x32_bf16 v[224:227], a[20:23], v[64:67], 0\n" \
"buffer_load_dwordx4 v2, s[80:83], s66 offen offset:2048 lds\n" \
"v_mfma_f32_16x16x32_bf16 v[228:231], a[20:23], v[80:83], 0\n" \
"ds_read_b64_tr_b16 a[20:21], v34 offset:18496\n" \
"ds_read_b64_tr_b16 a[22:23], v34 offset:23104\n" \
"v_mfma_f32_16x16x32_bf16 v[160:163], a[24:27], a[96:99], 0\n" \
"v_readlane_b32 s93, v11, 35\n" \
"v_lshlrev_b32_e32 v41, 12, v20\n" \
"v_mfma_f32_16x16x32_bf16 v[164:167], a[24:27], a[112:115], 0\n" \
"v_readlane_b32 s95, v11, 38\n" \
"v_readlane_b32 s96, v11, 39\n" \
"v_mfma_f32_16x16x32_bf16 v[232:235], a[24:27], v[64:67], 0\n" \
"v_readlane_b32 s63, v11, 41\n" \
"v_bfe_u32 v40, v20, 2, 7\n" \
"v_mfma_f32_16x16x32_bf16 v[236:239], a[24:27], v[80:83], 0\n" \
"ds_read_b64_tr_b16 a[24:25], v34 offset:18528\n" \
"ds_read_b64_tr_b16 a[26:27], v34 offset:23136\n" \
"v_mfma_f32_16x16x32_bf16 v[168:171], a[28:31], a[96:99], 0\n" \
"buffer_load_dwordx4 v3, s[80:83], s66 offen offset:3072 lds\n" \
"v_mfma_f32_16x16x32_bf16 v[172:175], a[28:31], a[112:115], 0\n" \
"s_add_u32 m0, s67, 0x1000\n" \
"s_add_u32 s74, s63, 0x3800\n" \
"v_mfma_f32_16x16x32_bf16 v[240:243], a[28:31], v[64:67], 0\n" \
"v_readlane_b32 s100, v11, 7\n" \
"v_readlane_b32 s97, v11, 40\n" \
"v_mfma_f32_16x16x32_bf16 v[244:247], a[28:31], v[80:83], 0\n" \
"ds_read_b64_tr_b16 a[28:29], v34 offset:18560\n" \
"ds_read_b64_tr_b16 a[30:31], v34 offset:23168\n" \
"v_mfma_f32_16x16x32_bf16 v[176:179], a[192:195], a[96:99], 0\n" \
"v_bfe_u32 v44, v20, 4, 4\n" \
"v_bfe_u32 v45, v20, 8, 2\n" \
"v_mfma_f32_16x16x32_bf16 v[180:183], a[192:195], a[112:115], 0\n" \
"buffer_load_dwordx4 v4, s[80:83], s66 offen offset:0 lds\n" \
"v_mfma_f32_16x16x32_bf16 v[248:251], a[192:195], v[64:67], 0\n" \
"v_mul_u32_u24_e32 v42, 0x480, v44\n" \
"v_lshl_add_u32 v42, v45, 3, v42\n" \
"v_mfma_f32_16x16x32_bf16 v[252:255], a[192:195], v[80:83], 0\n" \
"ds_read_b64_tr_b16 a[192:193], v34 offset:18592\n" \
"ds_read_b64_tr_b16 a[194:195], v34 offset:23200\n" \
"v_mfma_f32_16x16x32_bf16 v[112:115], a[196:199], a[100:103], v[112:115]\n" \
"v_lshrrev_b32_e32 v44, 1, v44\n" \
"v_lshlrev_b32_e32 v45, 1, v45\n" \
"v_mfma_f32_16x16x32_bf16 v[116:119], a[196:199], a[116:119], v[116:119]\n" \
"v_sub_u32_e32 v44, v44, v45\n" \
"v_and_b32_e32 v45, 16, v20\n" \
"v_mfma_f32_16x16x32_bf16 v[184:187], a[196:199], v[68:71], v[184:187]\n" \
"buffer_load_dwordx4 v5, s[80:83], s66 offen offset:1024 lds\n" \
"v_mfma_f32_16x16x32_bf16 v[188:191], a[196:199], v[84:87], v[188:191]\n" \
"ds_read_b64_tr_b16 a[196:197], v34 offset:18624\n" \
"ds_read_b64_tr_b16 a[198:199], v34 offset:23232\n" \
"v_mfma_f32_16x16x32_bf16 v[120:123], a[200:203], a[100:103], v[120:123]\n" \
"v_mov_b32_e32 v46, 0x3f80\n" \
"v_lshlrev_b32_e32 v45, v45, v46\n" \
"v_mfma_f32_16x16x32_bf16 v[124:127], a[200:203], a[116:119], v[124:127]\n" \
"v_mov_b32_e32 v52, 0\n" \
"v_mov_b32_e32 v53, 0\n" \
"v_mfma_f32_16x16x32_bf16 v[192:195], a[200:203], v[68:71], v[192:195]\n" \
"s_waitcnt vmcnt(14)\n" \
"v_cmp_ne_u32_e32 vcc, s62, v26\n" \
"v_mfma_f32_16x16x32_bf16 v[196:199], a[200:203], v[84:87], v[196:199]\n" \
"ds_read_b64_tr_b16 a[200:201], v34 offset:18656\n" \
"ds_read_b64_tr_b16 a[202:203], v34 offset:23264\n" \
"v_mfma_f32_16x16x32_bf16 v[128:131], a[204:207], a[100:103], v[128:131]\n" \
"buffer_load_dwordx4 v6, s[80:83], s66 offen offset:2048 lds\n" \
"v_mfma_f32_16x16x32_bf16 v[132:135], a[204:207], a[116:119], v[132:135]\n" \
"s_cbranch_vccnz L_sa_%=\n" \
"L_ra_%=:\n" \
"v_readlane_b32 s64, v11, 33\n" \
"v_mfma_f32_16x16x32_bf16 v[200:203], a[204:207], v[68:71], v[200:203]\n" \
"v_mov_b32_e32 v48, 0\n" \
"v_mov_b32_e32 v49, 0\n" \
"v_mfma_f32_16x16x32_bf16 v[204:207], a[204:207], v[84:87], v[204:207]\n" \
"ds_read_b64_tr_b16 a[204:205], v34 offset:18688\n" \
"ds_read_b64_tr_b16 a[206:207], v34 offset:23296\n" \
"v_mfma_f32_16x16x32_bf16 v[136:139], a[208:211], a[100:103], v[136:139]\n" \
"v_add_u32_e32 v43, 0x9000, v42\n" \
"v_readlane_b32 s88, v11, 13\n" \
"v_mfma_f32_16x16x32_bf16 v[140:143], a[208:211], a[116:119], v[140:143]\n" \
"buffer_load_dwordx4 a[64:67], v20, s[56:59], s65 offen offset:0 sc1\n" \
"v_mfma_f32_16x16x32_bf16 v[208:211], a[208:211], v[68:71], v[208:211]\n" \
"v_readlane_b32 s89, v11, 14\n" \
"s_mov_b32 s90, 0x240000\n" \
"v_mfma_f32_16x16x32_bf16 v[212:215], a[208:211], v[84:87], v[212:215]\n" \
"s_mov_b32 s91, 0x20000\n" \
"v_mov_b32_e32 v46, s62\n" \
"v_mfma_f32_16x16x32_bf16 v[144:147], a[212:215], a[100:103], v[144:147]\n" \
"v_mfma_f32_16x16x32_bf16 v[148:151], a[212:215], a[116:119], v[148:151]\n" \
"v_mfma_f32_16x16x32_bf16 v[216:219], a[212:215], v[68:71], v[216:219]\n" \
"buffer_load_dwordx4 a[80:83], v32, s[56:59], s65 offen offset:0 sc1\n" \
"v_mfma_f32_16x16x32_bf16 v[220:223], a[212:215], v[84:87], v[220:223]\n" \
"v_mfma_f32_16x16x32_bf16 v[152:155], a[216:219], a[100:103], v[152:155]\n" \
"v_mfma_f32_16x16x32_bf16 v[156:159], a[216:219], a[116:119], v[156:159]\n" \
"v_mfma_f32_16x16x32_bf16 v[224:227], a[216:219], v[68:71], v[224:227]\n" \
"v_mfma_f32_16x16x32_bf16 v[228:231], a[216:219], v[84:87], v[228:231]\n" \
"buffer_load_dwordx4 a[68:71], v20, s[56:59], s65 offen offset:1024 sc1\n" \
"v_mfma_f32_16x16x32_bf16 v[160:163], a[0:3], a[100:103], v[160:163]\n" \
"v_mfma_f32_16x16x32_bf16 v[164:167], a[0:3], a[116:119], v[164:167]\n" \
"v_mfma_f32_16x16x32_bf16 v[232:235], a[0:3], v[68:71], v[232:235]\n" \
"v_mfma_f32_16x16x32_bf16 v[236:239], a[0:3], v[84:87], v[236:239]\n" \
"s_waitcnt lgkmcnt(0)\n" \
"v_mfma_f32_16x16x32_bf16 v[168:171], a[4:7], a[100:103], v[168:171]\n" \
"buffer_load_dwordx4 a[84:87], v32, s[56:59], s65 offen offset:1024 sc1\n" \
"v_mfma_f32_16x16x32_bf16 v[172:175], a[4:7], a[116:119], v[172:175]\n" \
"v_mfma_f32_16x16x32_bf16 v[240:243], a[4:7], v[68:71], v[240:243]\n" \
"v_mfma_f32_16x16x32_bf16 v[244:247], a[4:7], v[84:87], v[244:247]\n" \
"v_mfma_f32_16x16x32_bf16 v[176:179], a[8:11], a[100:103], v[176:179]\n" \
"v_mfma_f32_16x16x32_bf16 v[180:183], a[8:11], a[116:119], v[180:183]\n" \
"buffer_load_dwordx4 a[72:75], v20, s[56:59], s65 offen offset:2048 sc1\n" \
"v_mfma_f32_16x16x32_bf16 v[248:251], a[8:11], v[68:71], v[248:251]\n" \
"v_mfma_f32_16x16x32_bf16 v[252:255], a[8:11], v[84:87], v[252:255]\n" \
"v_mfma_f32_16x16x32_bf16 v[112:115], a[12:15], a[104:107], v[112:115]\n" \
"v_mfma_f32_16x16x32_bf16 v[116:119], a[12:15], a[120:123], v[116:119]\n" \
"v_mfma_f32_16x16x32_bf16 v[184:187], a[12:15], v[72:75], v[184:187]\n" \
"buffer_load_dwordx4 a[88:91], v32, s[56:59], s65 offen offset:2048 sc1\n" \
"v_mfma_f32_16x16x32_bf16 v[188:191], a[12:15], v[96:99], v[188:191]\n" \
"v_mfma_f32_16x16x32_bf16 v[120:123], a[16:19], a[104:107], v[120:123]\n" \
"v_mfma_f32_16x16x32_bf16 v[124:127], a[16:19], a[120:123], v[124:127]\n" \
"v_mfma_f32_16x16x32_bf16 v[192:195], a[16:19], v[72:75], v[192:195]\n" \
"v_mfma_f32_16x16x32_bf16 v[196:199], a[16:19], v[96:99], v[196:199]\n" \
"buffer_load_dwordx4 v7, s[80:83], s66 offen offset:3072 lds\n" \
"v_mfma_f32_16x16x32_bf16 v[128:131], a[20:23], a[104:107], v[128:131]\n" \
"s_add_u32 m0, s67, 0x2000\n" \
"v_readlane_b32 s67, v11, 48\n" \
"v_mfma_f32_16x16x32_bf16 v[132:135], a[20:23], a[120:123], v[132:135]\n" \
"v_mfma_f32_16x16x32_bf16 v[200:203], a[20:23], v[72:75], v[200:203]\n" \
"v_mfma_f32_16x16x32_bf16 v[204:207], a[20:23], v[96:99], v[204:207]\n" \
"s_barrier\n" \
"v_mfma_f32_16x16x32_bf16 v[136:139], a[24:27], a[104:107], v[136:139]\n" \
"ds_read_b64_tr_b16 a[208:209], v10 offset:32768\n" \
"ds_read_b64_tr_b16 a[210:211], v10 offset:37376\n" \
"v_mfma_f32_16x16x32_bf16 v[140:143], a[24:27], a[120:123], v[140:143]\n" \
"ds_read_b64_tr_b16 a[212:213], v10 offset:32800\n" \
"ds_read_b64_tr_b16 a[214:215], v10 offset:37408\n" \
"v_mfma_f32_16x16x32_bf16 v[208:211], a[24:27], v[72:75], v[208:211]\n" \
"ds_read_b64_tr_b16 a[216:217], v10 offset:32832\n" \
"ds_read_b64_tr_b16 a[218:219], v10 offset:37440\n" \
"v_mfma_f32_16x16x32_bf16 v[212:215], a[24:27], v[96:99], v[212:215]\n" \
"ds_read_b64_tr_b16 a[0:1], v10 offset:32864\n" \
"ds_read_b64_tr_b16 a[2:3], v10 offset:37472\n" \
"v_mfma_f32_16x16x32_bf16 v[144:147], a[28:31], a[104:107], v[144:147]\n" \
"ds_read_b64_tr_b16 a[4:5], v10 offset:32896\n" \
"ds_read_b64_tr_b16 a[6:7], v10 offset:37504\n" \
"v_mfma_f32_16x16x32_bf16 v[148:151], a[28:31], a[120:123], v[148:151]\n" \
"ds_read_b64_tr_b16 a[8:9], v10 offset:32928\n" \
"ds_read_b64_tr_b16 a[10:11], v10 offset:37536\n" \
"v_mfma_f32_16x16x32_bf16 v[216:219], a[28:31], v[72:75], v[216:219]\n" \
"ds_read_b64_tr_b16 a[12:13], v10 offset:32960\n" \
"ds_read_b64_tr_b16 a[14:15], v10 offset:37568\n" \
"v_mfma_f32_16x16x32_bf16 v[220:223], a[28:31], v[96:99], v[220:223]\n" \
"ds_read_b64_tr_b16 a[16:17], v10 offset:32992\n" \
"ds_read_b64_tr_b16 a[18:19], v10 offset:37600\n" \
"v_mfma_f32_16x16x32_bf16 v[152:155], a[192:195], a[104:107], v[152:155]\n" \
"ds_read_b64_tr_b16 a[20:21], v10 offset:33024\n" \
"ds_read_b64_tr_b16 a[22:23], v10 offset:37632\n" \
"v_mfma_f32_16x16x32_bf16 v[156:159], a[192:195], a[120:123], v[156:159]\n" \
"ds_read_b64_tr_b16 a[24:25], v35 offset:41984\n" \
"ds_read_b64_tr_b16 a[26:27], v35 offset:44288\n" \
"v_mfma_f32_16x16x32_bf16 v[224:227], a[192:195], v[72:75], v[224:227]\n" \
"ds_read_b64_tr_b16 a[28:29], v35 offset:42016\n" \
"ds_read_b64_tr_b16 a[30:31], v35 offset:44320\n" \
"v_mfma_f32_16x16x32_bf16 v[228:231], a[192:195], v[96:99], v[228:231]\n" \
"ds_read_b64_tr_b16 a[192:193], v35 offset:42048\n" \
"ds_read_b64_tr_b16 a[194:195], v35 offset:44352\n" \
"v_mfma_f32_16x16x32_bf16 v[160:163], a[196:199], a[104:107], v[160:163]\n" \
"buffer_load_dwordx4 v8, s[80:83], s66 offen offset:0 lds\n" \
"v_mfma_f32_16x16x32_bf16 v[164:167], a[196:199], a[120:123], v[164:167]\n" \
"s_add_u32 m0, s92, 0x0\n" \
"v_readlane_b32 s66, v11, 47\n" \
"v_mfma_f32_16x16x32_bf16 v[232:235], a[196:199], v[72:75], v[232:235]\n" \
"v_mfma_f32_16x16x32_bf16 v[236:239], a[196:199], v[96:99], v[236:239]\n" \
"ds_read_b64_tr_b16 a[196:197], v35 offset:42080\n" \
"ds_read_b64_tr_b16 a[198:199], v35 offset:44384\n" \
"v_mfma_f32_16x16x32_bf16 v[168:171], a[200:203], a[104:107], v[168:171]\n" \
"v_mfma_f32_16x16x32_bf16 v[172:175], a[200:203], a[120:123], v[172:175]\n" \
"buffer_load_dwordx4 v0, s[80:83], s73 offen offset:0 lds\n" \
"v_mfma_f32_16x16x32_bf16 v[240:243], a[200:203], v[72:75], v[240:243]\n" \
"v_mfma_f32_16x16x32_bf16 v[244:247], a[200:203], v[96:99], v[244:247]\n" \
"ds_read_b64_tr_b16 a[200:201], v35 offset:42112\n" \
"ds_read_b64_tr_b16 a[202:203], v35 offset:44416\n" \
"v_mfma_f32_16x16x32_bf16 v[176:179], a[204:207], a[104:107], v[176:179]\n" \
"v_mfma_f32_16x16x32_bf16 v[180:183], a[204:207], a[120:123], v[180:183]\n" \
"v_mfma_f32_16x16x32_bf16 v[248:251], a[204:207], v[72:75], v[248:251]\n" \
"buffer_load_dwordx4 v1, s[80:83], s73 offen offset:1024 lds\n" \
"v_mfma_f32_16x16x32_bf16 v[252:255], a[204:207], v[96:99], v[252:255]\n" \
"ds_read_b64_tr_b16 a[204:205], v35 offset:42144\n" \
"s_waitcnt lgkmcnt(9)\n" \
"v_mfma_f32_16x16x32_bf16 v[112:115], a[208:211], a[108:111], v[112:115]\n" \
"ds_read_b64_tr_b16 a[206:207], v35 offset:44448\n" \
"v_mfma_f32_16x16x32_bf16 v[116:119], a[208:211], a[124:127], v[116:119]\n" \
"v_mfma_f32_16x16x32_bf16 v[184:187], a[208:211], v[76:79], v[184:187]\n" \
"v_mfma_f32_16x16x32_bf16 v[188:191], a[208:211], v[100:103], v[188:191]\n" \
"ds_read_b64_tr_b16 a[208:209], v35 offset:42176\n" \
"ds_read_b64_tr_b16 a[210:211], v35 offset:44480\n" \
"v_mfma_f32_16x16x32_bf16 v[120:123], a[212:215], a[108:111], v[120:123]\n" \
"buffer_load_dwordx4 v2, s[80:83], s73 offen offset:2048 lds\n" \
"v_mfma_f32_16x16x32_bf16 v[124:127], a[212:215], a[124:127], v[124:127]\n" \
"v_mfma_f32_16x16x32_bf16 v[192:195], a[212:215], v[76:79], v[192:195]\n" \
"s_waitcnt vmcnt(18)\n" \
"s_mov_b64 exec, 1\n" \
"buffer_store_dword v39, off, s[56:59], s70 offset:0 sc1\n" \
"s_mov_b64 exec, -1\n" \
"v_mfma_f32_16x16x32_bf16 v[196:199], a[212:215], v[100:103], v[196:199]\n" \
"ds_read_b64_tr_b16 a[212:213], v35 offset:42208\n" \
"ds_read_b64_tr_b16 a[214:215], v35 offset:44512\n" \
"v_mfma_f32_16x16x32_bf16 v[128:131], a[216:219], a[108:111], v[128:131]\n" \
"v_readlane_b32 s70, v11, 51\n" \
"v_mfma_f32_16x16x32_bf16 v[132:135], a[216:219], a[124:127], v[132:135]\n" \
"v_mfma_f32_16x16x32_bf16 v[200:203], a[216:219], v[76:79], v[200:203]\n" \
"buffer_load_dwordx4 v3, s[80:83], s73 offen offset:3072 lds\n" \
"v_mfma_f32_16x16x32_bf16 v[204:207], a[216:219], v[100:103], v[204:207]\n" \
"ds_read_b64_tr_b16 a[216:217], v35 offset:42240\n" \
"ds_read_b64_tr_b16 a[218:219], v35 offset:44544\n" \
"v_mfma_f32_16x16x32_bf16 v[136:139], a[0:3], a[108:111], v[136:139]\n" \
"s_add_u32 m0, s92, 0x1000\n" \
"v_mfma_f32_16x16x32_bf16 v[140:143], a[0:3], a[124:127], v[140:143]\n" \
"v_mfma_f32_16x16x32_bf16 v[208:211], a[0:3], v[76:79], v[208:211]\n" \
"v_mfma_f32_16x16x32_bf16 v[212:215], a[0:3], v[100:103], v[212:215]\n" \
"ds_read_b64_tr_b16 a[0:1], v35 offset:52352\n" \
"ds_read_b64_tr_b16 a[2:3], v35 offset:54656\n" \
"v_mfma_f32_16x16x32_bf16 v[144:147], a[4:7], a[108:111], v[144:147]\n" \
"buffer_load_dwordx4 v4, s[80:83], s73 offen offset:0 lds\n" \
"v_mfma_f32_16x16x32_bf16 v[148:151], a[4:7], a[124:127], v[148:151]\n" \
"v_mfma_f32_16x16x32_bf16 v[216:219], a[4:7], v[76:79], v[216:219]\n" \
"v_mfma_f32_16x16x32_bf16 v[220:223], a[4:7], v[100:103], v[220:223]\n" \
"ds_read_b64_tr_b16 a[4:5], v35 offset:52384\n" \
"ds_read_b64_tr_b16 a[6:7], v35 offset:54688\n" \
"v_mfma_f32_16x16x32_bf16 v[152:155], a[8:11], a[108:111], v[152:155]\n" \
"v_mfma_f32_16x16x32_bf16 v[156:159], a[8:11], a[124:127], v[156:159]\n" \
"buffer_load_dwordx4 v5, s[80:83], s73 offen offset:1024 lds\n" \
"v_mfma_f32_16x16x32_bf16 v[224:227], a[8:11], v[76:79], v[224:227]\n" \
"v_mfma_f32_16x16x32_bf16 v[228:231], a[8:11], v[100:103], v[228:231]\n" \
"ds_read_b64_tr_b16 a[8:9], v35 offset:52416\n" \
"ds_read_b64_tr_b16 a[10:11], v35 offset:54720\n" \
"v_mfma_f32_16x16x32_bf16 v[160:163], a[12:15], a[108:111], v[160:163]\n" \
"v_mfma_f32_16x16x32_bf16 v[164:167], a[12:15], a[124:127], v[164:167]\n" \
"v_mfma_f32_16x16x32_bf16 v[232:235], a[12:15], v[76:79], v[232:235]\n" \
"buffer_load_dwordx4 v6, s[80:83], s73 offen offset:2048 lds\n" \
"v_mfma_f32_16x16x32_bf16 v[236:239], a[12:15], v[100:103], v[236:239]\n" \
"ds_read_b64_tr_b16 a[12:13], v35 offset:52448\n" \
"ds_read_b64_tr_b16 a[14:15], v35 offset:54752\n" \
"v_mfma_f32_16x16x32_bf16 v[168:171], a[16:19], a[108:111], v[168:171]\n" \
"v_mfma_f32_16x16x32_bf16 v[172:175], a[16:19], a[124:127], v[172:175]\n" \
"v_mfma_f32_16x16x32_bf16 v[240:243], a[16:19], v[76:79], v[240:243]\n" \
"v_mfma_f32_16x16x32_bf16 v[244:247], a[16:19], v[100:103], v[244:247]\n" \
"buffer_load_dwordx4 v7, s[80:83], s73 offen offset:3072 lds\n" \
"v_mfma_f32_16x16x32_bf16 v[176:179], a[20:23], a[108:111], v[176:179]\n" \
"s_add_u32 m0, s92, 0x2000\n" \
"ds_read_b64_tr_b16 a[16:17], v35 offset:52480\n" \
"v_mfma_f32_16x16x32_bf16 v[180:183], a[20:23], a[124:127], v[180:183]\n" \
"ds_read_b64_tr_b16 a[18:19], v35 offset:54784\n" \
"v_readlane_b32 s92, v11, 46\n" \
"v_mfma_f32_16x16x32_bf16 v[248:251], a[20:23], v[76:79], v[248:251]\n" \
"v_mfma_f32_16x16x32_bf16 v[252:255], a[20:23], v[100:103], v[252:255]\n" \
"ds_read_b64_tr_b16 a[20:21], v35 offset:52512\n" \
"ds_read_b64_tr_b16 a[22:23], v35 offset:54816\n" \
"v_mfma_f32_16x16x32_bf16 v[112:115], a[24:27], a[220:223], v[112:115]\n" \
"buffer_load_dwordx4 v8, s[80:83], s73 offen offset:0 lds\n" \
"v_mfma_f32_16x16x32_bf16 v[116:119], a[24:27], a[228:231], v[116:119]\n" \
"s_mov_b32 m0, s94\n" \
"v_readlane_b32 s94, v11, 48\n" \
"v_mfma_f32_16x16x32_bf16 v[184:187], a[24:27], a[236:239], v[184:187]\n" \
"v_readlane_b32 s73, v11, 6\n" \
"v_mfma_f32_16x16x32_bf16 v[188:191], a[24:27], a[244:247], v[188:191]\n" \
"ds_read_b64_tr_b16 a[24:25], v35 offset:52544\n" \
"s_waitcnt lgkmcnt(7)\n" \
"v_mfma_f32_16x16x32_bf16 v[120:123], a[28:31], a[220:223], v[120:123]\n" \
"ds_read_b64_tr_b16 a[26:27], v35 offset:54848\n" \
"buffer_load_dword v47, v38, s[56:59], s71 offen sc1\n" \
"v_mfma_f32_16x16x32_bf16 v[124:127], a[28:31], a[228:231], v[124:127]\n" \
"v_mfma_f32_16x16x32_bf16 v[192:195], a[28:31], a[236:239], v[192:195]\n" \
"v_mfma_f32_16x16x32_bf16 v[196:199], a[28:31], a[244:247], v[196:199]\n" \
"ds_read_b64_tr_b16 a[28:29], v35 offset:52576\n" \
"ds_read_b64_tr_b16 a[30:31], v35 offset:54880\n" \
"v_mfma_f32_16x16x32_bf16 v[128:131], a[192:195], a[220:223], v[128:131]\n" \
"v_mfma_f32_16x16x32_bf16 v[132:135], a[192:195], a[228:231], v[132:135]\n" \
"buffer_load_dwordx4 v9, s[84:87], 0 offen lds\n" \
"v_mfma_f32_16x16x32_bf16 v[200:203], a[192:195], a[236:239], v[200:203]\n" \
"v_mfma_f32_16x16x32_bf16 v[204:207], a[192:195], a[244:247], v[204:207]\n" \
"ds_read_b64_tr_b16 a[192:193], v35 offset:52608\n" \
"ds_read_b64_tr_b16 a[194:195], v35 offset:54912\n" \
"v_mfma_f32_16x16x32_bf16 v[136:139], a[196:199], a[220:223], v[136:139]\n" \
"v_mfma_f32_16x16x32_bf16 v[140:143], a[196:199], a[228:231], v[140:143]\n" \
"v_mfma_f32_16x16x32_bf16 v[208:211], a[196:199], a[236:239], v[208:211]\n" \
"buffer_load_dwordx4 a[76:79], v20, s[56:59], s65 offen offset:3072 sc1\n" \
"v_mfma_f32_16x16x32_bf16 v[212:215], a[196:199], a[244:247], v[212:215]\n" \
"v_mfma_f32_16x16x32_bf16 v[144:147], a[200:203], a[220:223], v[144:147]\n" \
"v_mfma_f32_16x16x32_bf16 v[148:151], a[200:203], a[228:231], v[148:151]\n" \
"v_mfma_f32_16x16x32_bf16 v[216:219], a[200:203], a[236:239], v[216:219]\n" \
"v_mfma_f32_16x16x32_bf16 v[220:223], a[200:203], a[244:247], v[220:223]\n" \
"buffer_load_dwordx4 a[92:95], v32, s[56:59], s65 offen offset:3072 sc1\n" \
"v_mfma_f32_16x16x32_bf16 v[152:155], a[204:207], a[220:223], v[152:155]\n" \
"v_readlane_b32 s65, v11, 46\n" \
"v_mfma_f32_16x16x32_bf16 v[156:159], a[204:207], a[228:231], v[156:159]\n" \
"v_mfma_f32_16x16x32_bf16 v[224:227], a[204:207], a[236:239], v[224:227]\n" \
"v_mfma_f32_16x16x32_bf16 v[228:231], a[204:207], a[244:247], v[228:231]\n" \
"v_mfma_f32_16x16x32_bf16 v[160:163], a[208:211], a[220:223], v[160:163]\n" \
"buffer_load_dwordx4 a[128:131], v20, s[56:59], s68 offen offset:0 sc1\n" \
"v_mfma_f32_16x16x32_bf16 v[164:167], a[208:211], a[228:231], v[164:167]\n" \
"v_mfma_f32_16x16x32_bf16 v[232:235], a[208:211], a[236:239], v[232:235]\n" \
"v_mfma_f32_16x16x32_bf16 v[236:239], a[208:211], a[244:247], v[236:239]\n" \
"v_mfma_f32_16x16x32_bf16 v[168:171], a[212:215], a[220:223], v[168:171]\n" \
"v_mfma_f32_16x16x32_bf16 v[172:175], a[212:215], a[228:231], v[172:175]\n" \
"buffer_load_dwordx4 a[144:147], v32, s[56:59], s68 offen offset:0 sc1\n" \
"v_mfma_f32_16x16x32_bf16 v[240:243], a[212:215], a[236:239], v[240:243]\n" \
"v_mfma_f32_16x16x32_bf16 v[244:247], a[212:215], a[244:247], v[244:247]\n" \
"v_mfma_f32_16x16x32_bf16 v[176:179], a[216:219], a[220:223], v[176:179]\n" \
"v_mfma_f32_16x16x32_bf16 v[180:183], a[216:219], a[228:231], v[180:183]\n" \
"v_mfma_f32_16x16x32_bf16 v[248:251], a[216:219], a[236:239], v[248:251]\n" \
"buffer_load_dwordx4 a[132:135], v20, s[56:59], s68 offen offset:1024 sc1\n" \
"v_mfma_f32_16x16x32_bf16 v[252:255], a[216:219], a[244:247], v[252:255]\n" \
"v_mfma_f32_16x16x32_bf16 v[112:115], a[0:3], a[224:227], v[112:115]\n" \
"v_mfma_f32_16x16x32_bf16 v[116:119], a[0:3], a[232:235], v[116:119]\n" \
"v_mfma_f32_16x16x32_bf16 v[184:187], a[0:3], a[240:243], v[184:187]\n" \
"v_mfma_f32_16x16x32_bf16 v[188:191], a[0:3], a[248:251], v[188:191]\n" \
"buffer_load_dwordx4 a[148:151], v32, s[56:59], s68 offen offset:1024 sc1\n" \
"v_mfma_f32_16x16x32_bf16 v[120:123], a[4:7], a[224:227], v[120:123]\n" \
"v_mfma_f32_16x16x32_bf16 v[124:127], a[4:7], a[232:235], v[124:127]\n" \
"v_mfma_f32_16x16x32_bf16 v[192:195], a[4:7], a[240:243], v[192:195]\n" \
"v_mfma_f32_16x16x32_bf16 v[196:199], a[4:7], a[248:251], v[196:199]\n" \
"v_mfma_f32_16x16x32_bf16 v[128:131], a[8:11], a[224:227], v[128:131]\n" \
"buffer_load_dwordx4 a[136:139], v20, s[56:59], s68 offen offset:2048 sc1\n" \
"v_mfma_f32_16x16x32_bf16 v[132:135], a[8:11], a[232:235], v[132:135]\n" \
"v_mfma_f32_16x16x32_bf16 v[200:203], a[8:11], a[240:243], v[200:203]\n" \
"v_mfma_f32_16x16x32_bf16 v[204:207], a[8:11], a[248:251], v[204:207]\n" \
"s_waitcnt lgkmcnt(0)\n" \
"v_mfma_f32_16x16x32_bf16 v[136:139], a[12:15], a[224:227], v[136:139]\n" \
"v_mfma_f32_16x16x32_bf16 v[140:143], a[12:15], a[232:235], v[140:143]\n" \
"buffer_load_dwordx4 a[152:155], v32, s[56:59], s68 offen offset:2048 sc1\n" \
"v_mfma_f32_16x16x32_bf16 v[208:211], a[12:15], a[240:243], v[208:211]\n" \
"v_mfma_f32_16x16x32_bf16 v[212:215], a[12:15], a[248:251], v[212:215]\n" \
"s_barrier\n" \
"v_mfma_f32_16x16x32_bf16 v[144:147], a[16:19], a[224:227], v[144:147]\n" \
"ds_read_b64_tr_b16 a[196:197], v33 offset:0\n" \
"ds_read_b64_tr_b16 a[198:199], v33 offset:4608\n" \
"v_mfma_f32_16x16x32_bf16 v[148:151], a[16:19], a[232:235], v[148:151]\n" \
"buffer_load_dwordx4 a[140:143], v20, s[56:59], s68 offen offset:3072 sc1\n" \
"v_mfma_f32_16x16x32_bf16 v[216:219], a[16:19], a[240:243], v[216:219]\n" \
"ds_read_b64_tr_b16 a[200:201], v33 offset:32\n" \
"ds_read_b64_tr_b16 a[202:203], v33 offset:4640\n" \
"v_mfma_f32_16x16x32_bf16 v[220:223], a[16:19], a[248:251], v[220:223]\n" \
"ds_read_b64_tr_b16 a[204:205], v33 offset:64\n" \
"ds_read_b64_tr_b16 a[206:207], v33 offset:4672\n" \
"v_mfma_f32_16x16x32_bf16 v[152:155], a[20:23], a[224:227], v[152:155]\n" \
"ds_read_b64_tr_b16 a[208:209], v33 offset:96\n" \
"ds_read_b64_tr_b16 a[210:211], v33 offset:4704\n" \
"v_mfma_f32_16x16x32_bf16 v[156:159], a[20:23], a[232:235], v[156:159]\n" \
"ds_read_b64_tr_b16 a[212:213], v33 offset:128\n" \
"ds_read_b64_tr_b16 a[214:215], v33 offset:4736\n" \
"v_mfma_f32_16x16x32_bf16 v[224:227], a[20:23], a[240:243], v[224:227]\n" \
"buffer_load_dwordx4 a[156:159], v32, s[56:59], s68 offen offset:3072 sc1\n" \
"v_mfma_f32_16x16x32_bf16 v[228:231], a[20:23], a[248:251], v[228:231]\n" \
"ds_read_b64_tr_b16 a[216:217], v33 offset:160\n" \
"ds_read_b64_tr_b16 a[218:219], v33 offset:4768\n" \
"v_mfma_f32_16x16x32_bf16 v[160:163], a[24:27], a[224:227], v[160:163]\n" \
"ds_read_b64_tr_b16 a[0:1], v33 offset:192\n" \
"ds_read_b64_tr_b16 a[2:3], v33 offset:4800\n" \
"v_mfma_f32_16x16x32_bf16 v[164:167], a[24:27], a[232:235], v[164:167]\n" \
"ds_read_b64_tr_b16 a[4:5], v33 offset:224\n" \
"ds_read_b64_tr_b16 a[6:7], v33 offset:4832\n" \
"v_mfma_f32_16x16x32_bf16 v[232:235], a[24:27], a[240:243], v[232:235]\n" \
"ds_read_b64_tr_b16 a[8:9], v33 offset:256\n" \
"ds_read_b64_tr_b16 a[10:11], v33 offset:4864\n" \
"buffer_load_dwordx4 a[32:35], v20, s[56:59], s69 offen offset:0 sc1\n" \
"v_mfma_f32_16x16x32_bf16 v[236:239], a[24:27], a[248:251], v[236:239]\n" \
"ds_read_b64_tr_b16 a[12:13], v33 offset:9216\n" \
"ds_read_b64_tr_b16 a[14:15], v33 offset:13824\n" \
"v_mfma_f32_16x16x32_bf16 v[168:171], a[28:31], a[224:227], v[168:171]\n" \
"ds_read_b64_tr_b16 a[16:17], v33 offset:9248\n" \
"ds_read_b64_tr_b16 a[18:19], v33 offset:13856\n" \
"v_mfma_f32_16x16x32_bf16 v[172:175], a[28:31], a[232:235], v[172:175]\n" \
"ds_read_b64_tr_b16 a[20:21], v33 offset:9280\n" \
"ds_read_b64_tr_b16 a[22:23], v33 offset:13888\n" \
"v_mfma_f32_16x16x32_bf16 v[240:243], a[28:31], a[240:243], v[240:243]\n" \
"ds_read_b64_tr_b16 a[24:25], v33 offset:9312\n" \
"ds_read_b64_tr_b16 a[26:27], v33 offset:13920\n" \
"buffer_load_dwordx4 a[48:51], v32, s[56:59], s69 offen offset:0 sc1\n" \
"v_mfma_f32_16x16x32_bf16 v[244:247], a[28:31], a[248:251], v[244:247]\n" \
"ds_read_b64_tr_b16 a[28:29], v33 offset:9344\n" \
"ds_read_b64_tr_b16 a[30:31], v33 offset:13952\n" \
"v_mfma_f32_16x16x32_bf16 v[176:179], a[192:195], a[224:227], v[176:179]\n" \
"v_readlane_b32 s68, v11, 49\n" \
"s_waitcnt vmcnt(13)\n" \
"v_cmp_ne_u32_e32 vcc, s62, v47\n" \
"v_mfma_f32_16x16x32_bf16 v[180:183], a[192:195], a[232:235], v[180:183]\n" \
"v_mfma_f32_16x16x32_bf16 v[248:251], a[192:195], a[240:243], v[248:251]\n" \
"buffer_load_dwordx4 a[36:39], v20, s[56:59], s69 offen offset:1024 sc1\n" \
"v_mfma_f32_16x16x32_bf16 v[252:255], a[192:195], a[248:251], v[252:255]\n" \
"ds_read_b64_tr_b16 a[192:193], v33 offset:9376\n" \
"s_waitcnt lgkmcnt(15)\n" \
"v_mfma_f32_16x16x32_bf16 v[112:115], a[196:199], a[64:67], v[112:115]\n" \
"ds_read_b64_tr_b16 a[194:195], v33 offset:13984\n" \
"s_cbranch_vccnz L_sb_%=\n" \
"L_rb_%=:\n" \
"v_mfma_f32_16x16x32_bf16 v[116:119], a[196:199], a[80:83], v[116:119]\n" \
"ds_read_b64_tr_b16 a[196:197], v33 offset:9408\n" \
"ds_read_b64_tr_b16 a[198:199], v33 offset:14016\n" \
"v_mfma_f32_16x16x32_bf16 v[120:123], a[200:203], a[64:67], v[120:123]\n" \
"v_readlane_b32 s71, v11, 52\n" \
"v_cmp_eq_u32_e32 vcc, 0, v44\n" \
"buffer_load_dwordx4 a[52:55], v32, s[56:59], s69 offen offset:1024 sc1\n" \
"v_mfma_f32_16x16x32_bf16 v[124:127], a[200:203], a[80:83], v[124:127]\n" \
"ds_read_b64_tr_b16 a[200:201], v33 offset:9440\n" \
"ds_read_b64_tr_b16 a[202:203], v33 offset:14048\n" \
"v_mfma_f32_16x16x32_bf16 v[128:131], a[204:207], a[64:67], v[128:131]\n" \
"v_cndmask_b32_e32 v50, 0, v45, vcc\n" \
"v_cmp_eq_u32_e32 vcc, 1, v44\n" \
"v_mfma_f32_16x16x32_bf16 v[132:135], a[204:207], a[80:83], v[132:135]\n" \
"ds_read_b64_tr_b16 a[204:205], v33 offset:9472\n" \
"ds_read_b64_tr_b16 a[206:207], v33 offset:14080\n" \
"v_mfma_f32_16x16x32_bf16 v[136:139], a[208:211], a[64:67], v[136:139]\n" \
"v_cndmask_b32_e32 v51, 0, v45, vcc\n" \
"v_bfe_u32 v45, v20, 1, 5\n" \
"buffer_load_dwordx4 a[40:43], v20, s[56:59], s69 offen offset:2048 sc1\n" \
"v_mfma_f32_16x16x32_bf16 v[140:143], a[208:211], a[80:83], v[140:143]\n" \
"ds_read_b64_tr_b16 a[208:209], v33 offset:18432\n" \
"ds_read_b64_tr_b16 a[210:211], v33 offset:23040\n" \
"v_mfma_f32_16x16x32_bf16 v[144:147], a[212:215], a[64:67], v[144:147]\n" \
"v_and_b32_e32 v45, 24, v45\n" \
"v_and_b32_e32 v44, 0x3c0, v20\n" \
"v_mfma_f32_16x16x32_bf16 v[148:151], a[212:215], a[80:83], v[148:151]\n" \
"ds_read_b64_tr_b16 a[212:213], v33 offset:18464\n" \
"ds_read_b64_tr_b16 a[214:215], v33 offset:23072\n" \
"v_mfma_f32_16x16x32_bf16 v[152:155], a[216:219], a[64:67], v[152:155]\n" \
"v_add_u32_e32 v45, 0x4800, v45\n" \
"v_add_u32_e32 v36, v44, v45\n" \
"buffer_load_dwordx4 a[56:59], v32, s[56:59], s69 offen offset:2048 sc1\n" \
"v_mfma_f32_16x16x32_bf16 v[156:159], a[216:219], a[80:83], v[156:159]\n" \
"ds_read_b64_tr_b16 a[216:217], v33 offset:18496\n" \
"ds_read_b64_tr_b16 a[218:219], v33 offset:23104\n" \
"v_mfma_f32_16x16x32_bf16 v[160:163], a[0:3], a[64:67], v[160:163]\n" \
".fill (L_ab + 36864 - .) / 4, 4, 0xBF800000\n" \
"L_ap9:\n" \
"s_cbranch_execz L_ap0\n" \
"v_mfma_f32_16x16x32_bf16 v[164:167], a[0:3], a[80:83], v[164:167]\n" \
"ds_read_b64_tr_b16 a[0:1], v33 offset:18528\n" \
"s_waitcnt lgkmcnt(13)\n" \
"v_mfma_f32_16x16x32_bf16 v[168:171], a[4:7], a[64:67], v[168:171]\n" \
"ds_read_b64_tr_b16 a[2:3], v33 offset:23136\n" \
"buffer_load_dwordx4 a[44:47], v20, s[56:59], s69 offen offset:3072 sc1\n" \
"v_mfma_f32_16x16x32_bf16 v[172:175], a[4:7], a[80:83], v[172:175]\n" \
"ds_read_b64_tr_b16 a[4:5], v33 offset:18560\n" \
"ds_read_b64_tr_b16 a[6:7], v33 offset:23168\n" \
"v_mfma_f32_16x16x32_bf16 v[176:179], a[8:11], a[64:67], v[176:179]\n" \
"v_mfma_f32_16x16x32_bf16 v[180:183], a[8:11], a[80:83], v[180:183]\n" \
"ds_read_b64_tr_b16 a[8:9], v33 offset:18592\n" \
"ds_read_b64_tr_b16 a[10:11], v33 offset:23200\n" \
"v_mfma_f32_16x16x32_bf16 v[112:115], a[12:15], a[68:71], v[112:115]\n" \
"v_mfma_f32_16x16x32_bf16 v[116:119], a[12:15], a[84:87], v[116:119]\n" \
"buffer_load_dwordx4 a[60:63], v32, s[56:59], s69 offen offset:3072 sc1\n" \
"v_mfma_f32_16x16x32_bf16 v[120:123], a[16:19], a[68:71], v[120:123]\n" \
"ds_read_b64_tr_b16 a[12:13], v33 offset:18624\n" \
"ds_read_b64_tr_b16 a[14:15], v33 offset:23232\n" \
"v_mfma_f32_16x16x32_bf16 v[124:127], a[16:19], a[84:87], v[124:127]\n" \
"ds_read_b64_tr_b16 a[16:17], v33 offset:18656\n" \
"ds_read_b64_tr_b16 a[18:19], v33 offset:23264\n" \
"v_mfma_f32_16x16x32_bf16 v[128:131], a[20:23], a[68:71], v[128:131]\n" \
"v_readlane_b32 s69, v11, 50\n" \
"v_mfma_f32_16x16x32_bf16 v[132:135], a[20:23], a[84:87], v[132:135]\n" \
"ds_read_b64_tr_b16 a[20:21], v33 offset:18688\n" \
"ds_read_b64_tr_b16 a[22:23], v33 offset:23296\n" \
"v_mfma_f32_16x16x32_bf16 v[136:139], a[24:27], a[68:71], v[136:139]\n" \
"buffer_load_dwordx4 a[220:223], v20, s[56:59], s64 offen offset:0 sc1\n" \
"v_mfma_f32_16x16x32_bf16 v[140:143], a[24:27], a[84:87], v[140:143]\n" \
"ds_read_b64_tr_b16 a[24:25], v33 offset:27648\n" \
"ds_read_b64_tr_b16 a[26:27], v33 offset:32256\n" \
"v_mfma_f32_16x16x32_bf16 v[144:147], a[28:31], a[68:71], v[144:147]\n" \
"v_mfma_f32_16x16x32_bf16 v[148:151], a[28:31], a[84:87], v[148:151]\n" \
"ds_read_b64_tr_b16 a[28:29], v33 offset:27680\n" \
"ds_read_b64_tr_b16 a[30:31], v33 offset:32288\n" \
"v_mfma_f32_16x16x32_bf16 v[152:155], a[192:195], a[68:71], v[152:155]\n" \
"v_mfma_f32_16x16x32_bf16 v[156:159], a[192:195], a[84:87], v[156:159]\n" \
"ds_read_b64_tr_b16 a[192:193], v33 offset:27712\n" \
"s_waitcnt lgkmcnt(13)\n" \
"v_mfma_f32_16x16x32_bf16 v[160:163], a[196:199], a[68:71], v[160:163]\n" \
"buffer_load_dwordx4 a[236:239], v32, s[56:59], s64 offen offset:0 sc1\n" \
"v_mfma_f32_16x16x32_bf16 v[164:167], a[196:199], a[84:87], v[164:167]\n" \
"ds_read_b64_tr_b16 a[194:195], v33 offset:32320\n" \
"ds_read_b64_tr_b16 a[196:197], v33 offset:27744\n" \
"v_mfma_f32_16x16x32_bf16 v[168:171], a[200:203], a[68:71], v[168:171]\n" \
"ds_read_b64_tr_b16 a[198:199], v33 offset:32352\n" \
"v_mfma_f32_16x16x32_bf16 v[172:175], a[200:203], a[84:87], v[172:175]\n" \
"ds_read_b64_tr_b16 a[200:201], v33 offset:27776\n" \
"ds_read_b64_tr_b16 a[202:203], v33 offset:32384\n" \
"v_mfma_f32_16x16x32_bf16 v[176:179], a[204:207], a[68:71], v[176:179]\n" \
"v_mfma_f32_16x16x32_bf16 v[180:183], a[204:207], a[84:87], v[180:183]\n" \
"ds_read_b64_tr_b16 a[204:205], v33 offset:27808\n" \
"ds_read_b64_tr_b16 a[206:207], v33 offset:32416\n" \
"v_mfma_f32_16x16x32_bf16 v[112:115], a[208:211], a[72:75], v[112:115]\n" \
"buffer_load_dwordx4 a[224:227], v20, s[56:59], s64 offen offset:1024 sc1\n" \
"v_mfma_f32_16x16x32_bf16 v[116:119], a[208:211], a[88:91], v[116:119]\n" \
"ds_read_b64_tr_b16 a[208:209], v33 offset:27840\n" \
"ds_read_b64_tr_b16 a[210:211], v33 offset:32448\n" \
"v_mfma_f32_16x16x32_bf16 v[120:123], a[212:215], a[72:75], v[120:123]\n" \
"v_mfma_f32_16x16x32_bf16 v[124:127], a[212:215], a[88:91], v[124:127]\n" \
"ds_read_b64_tr_b16 a[212:213], v33 offset:27872\n" \
"ds_read_b64_tr_b16 a[214:215], v33 offset:32480\n" \
"v_mfma_f32_16x16x32_bf16 v[128:131], a[216:219], a[72:75], v[128:131]\n" \
"v_mfma_f32_16x16x32_bf16 v[132:135], a[216:219], a[88:91], v[132:135]\n" \
"ds_read_b64_tr_b16 a[216:217], v33 offset:27904\n" \
"ds_read_b64_tr_b16 a[218:219], v33 offset:32512\n" \
"v_mfma_f32_16x16x32_bf16 v[136:139], a[0:3], a[72:75], v[136:139]\n" \
"buffer_load_dwordx4 a[240:243], v32, s[56:59], s64 offen offset:1024 sc1\n" \
"v_mfma_f32_16x16x32_bf16 v[140:143], a[0:3], a[88:91], v[140:143]\n" \
"v_mfma_f32_16x16x32_bf16 v[144:147], a[4:7], a[72:75], v[144:147]\n" \
"v_mfma_f32_16x16x32_bf16 v[148:151], a[4:7], a[88:91], v[148:151]\n" \
"s_waitcnt lgkmcnt(12)\n" \
"v_mfma_f32_16x16x32_bf16 v[152:155], a[8:11], a[72:75], v[152:155]\n" \
"v_mfma_f32_16x16x32_bf16 v[156:159], a[8:11], a[88:91], v[156:159]\n" \
"buffer_load_dwordx4 a[228:231], v20, s[56:59], s64 offen offset:2048 sc1\n" \
"v_mfma_f32_16x16x32_bf16 v[160:163], a[12:15], a[72:75], v[160:163]\n" \
"v_mfma_f32_16x16x32_bf16 v[164:167], a[12:15], a[88:91], v[164:167]\n" \
"v_mfma_f32_16x16x32_bf16 v[168:171], a[16:19], a[72:75], v[168:171]\n" \
"v_mfma_f32_16x16x32_bf16 v[172:175], a[16:19], a[88:91], v[172:175]\n" \
"v_mfma_f32_16x16x32_bf16 v[176:179], a[20:23], a[72:75], v[176:179]\n" \
"buffer_load_dwordx4 a[244:247], v32, s[56:59], s64 offen offset:2048 sc1\n" \
"v_mfma_f32_16x16x32_bf16 v[180:183], a[20:23], a[88:91], v[180:183]\n" \
"s_waitcnt vmcnt(24)\n" \
"s_barrier\n" \
"s_waitcnt vmcnt(23)\n" \
"v_mfma_f32_16x16x32_bf16 v[112:115], a[24:27], a[76:79], v[112:115]\n" \
"ds_read_b64_tr_b16 a[0:1], v33 offset:36864\n" \
"s_waitcnt vmcnt(22)\n" \
"v_mfma_f32_16x16x32_bf16 v[116:119], a[24:27], a[92:95], v[116:119]\n" \
"ds_read_b64_tr_b16 a[2:3], v33 offset:41472\n" \
"ds_read_b64_tr_b16 a[4:5], v33 offset:46080\n" \
"v_mfma_f32_16x16x32_bf16 v[120:123], a[28:31], a[76:79], v[120:123]\n" \
"ds_read_b64_tr_b16 a[6:7], v33 offset:50688\n" \
"ds_read_b64_tr_b16 a[8:9], v33 offset:55296\n" \
"v_mfma_f32_16x16x32_bf16 v[124:127], a[28:31], a[92:95], v[124:127]\n" \
"ds_read_b64_tr_b16 a[10:11], v33 offset:59904\n" \
"ds_read_b64_tr_b16 a[12:13], v10 offset:0\n" \
"v_mfma_f32_16x16x32_bf16 v[128:131], a[192:195], a[76:79], v[128:131]\n" \
"ds_read_b64_tr_b16 a[14:15], v10 offset:4608\n" \
"ds_read_b64_tr_b16 a[16:17], v34 offset:0\n" \
"v_mfma_f32_16x16x32_bf16 v[132:135], a[192:195], a[92:95], v[132:135]\n" \
"ds_read_b64_tr_b16 a[18:19], v34 offset:4608\n" \
"s_waitcnt lgkmcnt(10)\n" \
"v_mfma_f32_16x16x32_bf16 v[136:139], a[196:199], a[76:79], v[136:139]\n" \
"ds_read_b64_tr_b16 a[20:21], v34 offset:9216\n" \
"ds_read_b64_tr_b16 a[22:23], v34 offset:13824\n" \
"v_mfma_f32_16x16x32_bf16 v[140:143], a[196:199], a[92:95], v[140:143]\n" \
"ds_read_b64_tr_b16 a[24:25], v34 offset:18432\n" \
"ds_read_b64_tr_b16 a[26:27], v34 offset:23040\n" \
"v_mfma_f32_16x16x32_bf16 v[144:147], a[200:203], a[76:79], v[144:147]\n" \
"ds_read_b64_tr_b16 a[28:29], v10 offset:9216\n" \
"ds_read_b64_tr_b16 a[30:31], v10 offset:13824\n" \
"v_mfma_f32_16x16x32_bf16 v[148:151], a[200:203], a[92:95], v[148:151]\n" \
"ds_read_b64_tr_b16 a[192:193], v33 offset:36896\n" \
"ds_read_b64_tr_b16 a[194:195], v33 offset:41504\n" \
"v_mfma_f32_16x16x32_bf16 v[152:155], a[204:207], a[76:79], v[152:155]\n" \
"ds_read_b64_tr_b16 a[196:197], v33 offset:46112\n" \
"ds_read_b64_tr_b16 a[198:199], v33 offset:50720\n" \
"v_mfma_f32_16x16x32_bf16 v[156:159], a[204:207], a[92:95], v[156:159]\n" \
"ds_read_b64_tr_b16 a[200:201], v33 offset:55328\n" \
"ds_read_b64_tr_b16 a[202:203], v33 offset:59936\n" \
"v_mfma_f32_16x16x32_bf16 v[160:163], a[208:211], a[76:79], v[160:163]\n" \
"ds_read_b64_tr_b16 a[204:205], v10 offset:32\n" \
"ds_read_b64_tr_b16 a[206:207], v10 offset:4640\n" \
"v_mfma_f32_16x16x32_bf16 v[164:167], a[208:211], a[92:95], v[164:167]\n" \
"ds_read_b64_tr_b16 a[208:209], v34 offset:32\n" \
"ds_read_b64_tr_b16 a[210:211], v34 offset:4640\n" \
"v_mfma_f32_16x16x32_bf16 v[168:171], a[212:215], a[76:79], v[168:171]\n" \
"buffer_load_dwordx4 a[232:235], v20, s[56:59], s64 offen offset:3072 sc1\n" \
"v_mfma_f32_16x16x32_bf16 v[172:175], a[212:215], a[92:95], v[172:175]\n" \
"ds_read_b64_tr_b16 a[212:213], v34 offset:9248\n" \
"ds_read_b64_tr_b16 a[214:215], v34 offset:13856\n" \
"v_mfma_f32_16x16x32_bf16 v[176:179], a[216:219], a[76:79], v[176:179]\n" \
"v_mfma_f32_16x16x32_bf16 v[180:183], a[216:219], a[92:95], v[180:183]\n" \
"ds_read_b64_tr_b16 a[216:217], v34 offset:18464\n" \
"s_waitcnt vmcnt(22) lgkmcnt(15)\n" \
"v_mfma_f32_16x16x32_bf16 v[112:115], a[0:3], a[128:131], v[112:115]\n" \
"ds_read_b64_tr_b16 a[218:219], v34 offset:23072\n" \
"s_waitcnt vmcnt(21)\n" \
"v_mfma_f32_16x16x32_bf16 v[116:119], a[0:3], a[144:147], v[116:119]\n" \
"ds_read_b64_tr_b16 a[0:1], v10 offset:9248\n" \
"s_waitcnt vmcnt(20)\n" \
"v_mfma_f32_16x16x32_bf16 v[112:115], a[4:7], a[132:135], v[112:115]\n" \
"ds_read_b64_tr_b16 a[2:3], v10 offset:13856\n" \
"s_waitcnt vmcnt(19)\n" \
"v_mfma_f32_16x16x32_bf16 v[116:119], a[4:7], a[148:151], v[116:119]\n" \
"ds_read_b64_tr_b16 a[4:5], v33 offset:36928\n" \
"s_waitcnt vmcnt(18)\n" \
"v_mfma_f32_16x16x32_bf16 v[112:115], a[8:11], a[136:139], v[112:115]\n" \
"ds_read_b64_tr_b16 a[6:7], v33 offset:41536\n" \
"s_waitcnt vmcnt(17)\n" \
"v_mfma_f32_16x16x32_bf16 v[116:119], a[8:11], a[152:155], v[116:119]\n" \
"ds_read_b64_tr_b16 a[8:9], v33 offset:46144\n" \
"s_waitcnt vmcnt(16)\n" \
"v_mfma_f32_16x16x32_bf16 v[112:115], a[12:15], a[140:143], v[112:115]\n" \
"ds_read_b64_tr_b16 a[10:11], v33 offset:50752\n" \
"s_waitcnt vmcnt(15)\n" \
"v_mfma_f32_16x16x32_bf16 v[116:119], a[12:15], a[156:159], v[116:119]\n" \
"ds_read_b64_tr_b16 a[12:13], v33 offset:55360\n" \
"ds_read_b64_tr_b16 a[14:15], v33 offset:59968\n" \
"buffer_load_dwordx4 a[248:251], v32, s[56:59], s64 offen offset:3072 sc1\n" \
"s_waitcnt vmcnt(15)\n" \
"v_mfma_f32_16x16x32_bf16 v[112:115], a[16:19], a[32:35], v[112:115]\n" \
"buffer_load_dwordx4 a[160:163], v20, s[56:59], s72 offen offset:0 sc1\n" \
"s_waitcnt vmcnt(15)\n" \
"v_mfma_f32_16x16x32_bf16 v[116:119], a[16:19], a[48:51], v[116:119]\n" \
"ds_read_b64_tr_b16 a[16:17], v10 offset:64\n" \
"ds_read_b64_tr_b16 a[18:19], v10 offset:4672\n" \
"buffer_load_dwordx4 a[176:179], v32, s[56:59], s72 offen offset:0 sc1\n" \
"s_waitcnt vmcnt(15)\n" \
"v_mfma_f32_16x16x32_bf16 v[112:115], a[20:23], a[36:39], v[112:115]\n" \
"buffer_load_dwordx4 a[164:167], v20, s[56:59], s72 offen offset:1024 sc1\n" \
"s_waitcnt vmcnt(15)\n" \
"v_mfma_f32_16x16x32_bf16 v[116:119], a[20:23], a[52:55], v[116:119]\n" \
"ds_read_b64_tr_b16 a[20:21], v34 offset:64\n" \
"ds_read_b64_tr_b16 a[22:23], v34 offset:4672\n" \
"buffer_load_dwordx4 a[180:183], v32, s[56:59], s72 offen offset:1024 sc1\n" \
"s_waitcnt vmcnt(15)\n" \
"v_mfma_f32_16x16x32_bf16 v[112:115], a[24:27], a[40:43], v[112:115]\n" \
"buffer_load_dwordx4 a[168:171], v20, s[56:59], s72 offen offset:2048 sc1\n" \
"s_waitcnt vmcnt(15)\n" \
"v_mfma_f32_16x16x32_bf16 v[116:119], a[24:27], a[56:59], v[116:119]\n" \
"ds_read_b64_tr_b16 a[24:25], v34 offset:9280\n" \
"ds_read_b64_tr_b16 a[26:27], v34 offset:13888\n" \
"buffer_load_dwordx4 a[184:187], v32, s[56:59], s72 offen offset:2048 sc1\n" \
"s_waitcnt vmcnt(15) lgkmcnt(4)\n" \
"v_mfma_f32_16x16x32_bf16 v[112:115], a[28:31], a[44:47], v[112:115]\n" \
"buffer_load_dwordx4 a[172:175], v20, s[56:59], s72 offen offset:3072 sc1\n" \
"s_waitcnt vmcnt(15)\n" \
"v_mfma_f32_16x16x32_bf16 v[116:119], a[28:31], a[60:63], v[116:119]\n" \
"ds_read_b64_tr_b16 a[28:29], v34 offset:18496\n" \
"ds_read_b64_tr_b16 a[30:31], v34 offset:23104\n" \
"v_mfma_f32_16x16x32_bf16 v[120:123], a[192:195], a[128:131], v[120:123]\n" \
"buffer_load_dwordx4 a[188:191], v32, s[56:59], s72 offen offset:3072 sc1\n" \
"v_mfma_f32_16x16x32_bf16 v[124:127], a[192:195], a[144:147], v[124:127]\n" \
"v_cvt_pk_bf16_f32 v112, v112, v113\n" \
"v_cvt_pk_bf16_f32 v113, v114, v115\n" \
"v_mfma_f32_16x16x32_bf16 v[120:123], a[196:199], a[132:135], v[120:123]\n" \
"v_cvt_pk_bf16_f32 v114, v116, v117\n" \
"v_cvt_pk_bf16_f32 v115, v118, v119\n" \
"buffer_store_dwordx4 v[112:115], v20, s[56:59], s65 offen offset:0 sc1\n" \
"s_add_u32 s65, s65, 0x3800\n" \
"v_mfma_f32_16x16x32_bf16 v[124:127], a[196:199], a[148:151], v[124:127]\n" \
"ds_read_b64_tr_b16 a[192:193], v10 offset:9280\n" \
"ds_read_b64_tr_b16 a[194:195], v10 offset:13888\n" \
"v_mfma_f32_16x16x32_bf16 v[120:123], a[200:203], a[136:139], v[120:123]\n" \
"ds_read_b64_tr_b16 a[196:197], v33 offset:36960\n" \
"ds_read_b64_tr_b16 a[198:199], v33 offset:41568\n" \
"v_mfma_f32_16x16x32_bf16 v[124:127], a[200:203], a[152:155], v[124:127]\n" \
"ds_read_b64_tr_b16 a[200:201], v33 offset:46176\n" \
"ds_read_b64_tr_b16 a[202:203], v33 offset:50784\n" \
"v_mfma_f32_16x16x32_bf16 v[120:123], a[204:207], a[140:143], v[120:123]\n" \
"buffer_load_dwordx4 a[96:99], v20, s[56:59], s93 offen offset:0 sc1\n" \
"v_mfma_f32_16x16x32_bf16 v[124:127], a[204:207], a[156:159], v[124:127]\n" \
"ds_read_b64_tr_b16 a[204:205], v33 offset:55392\n" \
"ds_read_b64_tr_b16 a[206:207], v33 offset:60000\n" \
"v_mfma_f32_16x16x32_bf16 v[120:123], a[208:211], a[32:35], v[120:123]\n" \
"v_readlane_b32 s72, v11, 53\n" \
"v_mfma_f32_16x16x32_bf16 v[124:127], a[208:211], a[48:51], v[124:127]\n" \
"ds_read_b64_tr_b16 a[208:209], v10 offset:96\n" \
"ds_read_b64_tr_b16 a[210:211], v10 offset:4704\n" \
"v_mfma_f32_16x16x32_bf16 v[120:123], a[212:215], a[36:39], v[120:123]\n" \
"v_mfma_f32_16x16x32_bf16 v[124:127], a[212:215], a[52:55], v[124:127]\n" \
"ds_read_b64_tr_b16 a[212:213], v34 offset:96\n" \
"ds_read_b64_tr_b16 a[214:215], v34 offset:4704\n" \
"v_mfma_f32_16x16x32_bf16 v[120:123], a[216:219], a[40:43], v[120:123]\n" \
"buffer_load_dwordx4 a[112:115], v32, s[56:59], s93 offen offset:0 sc1\n" \
"v_mfma_f32_16x16x32_bf16 v[124:127], a[216:219], a[56:59], v[124:127]\n" \
"ds_read_b64_tr_b16 a[216:217], v34 offset:9312\n" \
"ds_read_b64_tr_b16 a[218:219], v34 offset:13920\n" \
"v_mfma_f32_16x16x32_bf16 v[120:123], a[0:3], a[44:47], v[120:123]\n" \
"v_mfma_f32_16x16x32_bf16 v[124:127], a[0:3], a[60:63], v[124:127]\n" \
"ds_read_b64_tr_b16 a[0:1], v34 offset:18528\n" \
"ds_read_b64_tr_b16 a[2:3], v34 offset:23136\n" \
"v_mfma_f32_16x16x32_bf16 v[128:131], a[4:7], a[128:131], v[128:131]\n" \
"v_mfma_f32_16x16x32_bf16 v[132:135], a[4:7], a[144:147], v[132:135]\n" \
"buffer_load_dwordx4 a[100:103], v20, s[56:59], s93 offen offset:1024 sc1\n" \
"v_mfma_f32_16x16x32_bf16 v[128:131], a[8:11], a[132:135], v[128:131]\n" \
"ds_read_b64_tr_b16 a[4:5], v10 offset:9312\n" \
"v_cvt_pk_bf16_f32 v120, v120, v121\n" \
"v_cvt_pk_bf16_f32 v121, v122, v123\n" \
"v_cvt_pk_bf16_f32 v122, v124, v125\n" \
"v_cvt_pk_bf16_f32 v123, v126, v127\n" \
"v_mfma_f32_16x16x32_bf16 v[132:135], a[8:11], a[148:151], v[132:135]\n" \
"buffer_store_dwordx4 v[120:123], v20, s[56:59], s66 offen offset:0 sc1\n" \
"s_add_u32 s66, s66, 0x3000\n" \
"v_mfma_f32_16x16x32_bf16 v[128:131], a[12:15], a[136:139], v[128:131]\n" \
"ds_read_b64_tr_b16 a[6:7], v10 offset:13920\n" \
"ds_read_b64_tr_b16 a[8:9], v33 offset:36992\n" \
"v_mfma_f32_16x16x32_bf16 v[132:135], a[12:15], a[152:155], v[132:135]\n" \
"ds_read_b64_tr_b16 a[10:11], v33 offset:41600\n" \
"ds_read_b64_tr_b16 a[12:13], v33 offset:46208\n" \
"v_mfma_f32_16x16x32_bf16 v[128:131], a[16:19], a[140:143], v[128:131]\n" \
"ds_read_b64_tr_b16 a[14:15], v33 offset:50816\n" \
"v_mfma_f32_16x16x32_bf16 v[132:135], a[16:19], a[156:159], v[132:135]\n" \
"ds_read_b64_tr_b16 a[16:17], v33 offset:55424\n" \
"s_waitcnt lgkmcnt(13)\n" \
"v_mfma_f32_16x16x32_bf16 v[128:131], a[20:23], a[32:35], v[128:131]\n" \
"buffer_load_dwordx4 a[116:119], v32, s[56:59], s93 offen offset:1024 sc1\n" \
"v_mfma_f32_16x16x32_bf16 v[132:135], a[20:23], a[48:51], v[132:135]\n" \
"ds_read_b64_tr_b16 a[18:19], v33 offset:60032\n" \
"ds_read_b64_tr_b16 a[20:21], v10 offset:128\n" \
"v_mfma_f32_16x16x32_bf16 v[128:131], a[24:27], a[36:39], v[128:131]\n" \
"ds_read_b64_tr_b16 a[22:23], v10 offset:4736\n" \
"v_mfma_f32_16x16x32_bf16 v[132:135], a[24:27], a[52:55], v[132:135]\n" \
"ds_read_b64_tr_b16 a[24:25], v34 offset:128\n" \
"ds_read_b64_tr_b16 a[26:27], v34 offset:4736\n" \
"v_mfma_f32_16x16x32_bf16 v[128:131], a[28:31], a[40:43], v[128:131]\n" \
"v_mfma_f32_16x16x32_bf16 v[132:135], a[28:31], a[56:59], v[132:135]\n" \
"buffer_load_dwordx4 a[104:107], v20, s[56:59], s93 offen offset:2048 sc1\n" \
"v_mfma_f32_16x16x32_bf16 v[128:131], a[192:195], a[44:47], v[128:131]\n" \
"ds_read_b64_tr_b16 a[28:29], v34 offset:9344\n" \
"ds_read_b64_tr_b16 a[30:31], v34 offset:13952\n" \
"v_mfma_f32_16x16x32_bf16 v[132:135], a[192:195], a[60:63], v[132:135]\n" \
"ds_read_b64_tr_b16 a[192:193], v34 offset:18560\n" \
"ds_read_b64_tr_b16 a[194:195], v34 offset:23168\n" \
"v_mfma_f32_16x16x32_bf16 v[136:139], a[196:199], a[128:131], v[136:139]\n" \
"v_mfma_f32_16x16x32_bf16 v[140:143], a[196:199], a[144:147], v[140:143]\n" \
"ds_read_b64_tr_b16 a[196:197], v10 offset:9344\n" \
"v_cvt_pk_bf16_f32 v128, v128, v129\n" \
"v_cvt_pk_bf16_f32 v129, v130, v131\n" \
"v_mfma_f32_16x16x32_bf16 v[136:139], a[200:203], a[132:135], v[136:139]\n" \
"v_cvt_pk_bf16_f32 v130, v132, v133\n" \
"v_cvt_pk_bf16_f32 v131, v134, v135\n" \
"buffer_store_dwordx4 v[128:131], v20, s[56:59], s67 offen offset:0 sc1\n" \
"s_add_u32 s67, s67, 0x2800\n" \
"v_mfma_f32_16x16x32_bf16 v[140:143], a[200:203], a[148:151], v[140:143]\n" \
"ds_read_b64_tr_b16 a[198:199], v10 offset:13952\n" \
"ds_read_b64_tr_b16 a[200:201], v33 offset:37024\n" \
"v_mfma_f32_16x16x32_bf16 v[136:139], a[204:207], a[136:139], v[136:139]\n" \
"ds_read_b64_tr_b16 a[202:203], v33 offset:41632\n" \
"v_mfma_f32_16x16x32_bf16 v[140:143], a[204:207], a[152:155], v[140:143]\n" \
"ds_read_b64_tr_b16 a[204:205], v33 offset:46240\n" \
"ds_read_b64_tr_b16 a[206:207], v33 offset:50848\n" \
"v_mfma_f32_16x16x32_bf16 v[136:139], a[208:211], a[140:143], v[136:139]\n" \
"buffer_load_dwordx4 a[120:123], v32, s[56:59], s93 offen offset:2048 sc1\n" \
"v_mfma_f32_16x16x32_bf16 v[140:143], a[208:211], a[156:159], v[140:143]\n" \
"ds_read_b64_tr_b16 a[208:209], v33 offset:55456\n" \
"s_waitcnt lgkmcnt(13)\n" \
"v_mfma_f32_16x16x32_bf16 v[136:139], a[212:215], a[32:35], v[136:139]\n" \
"ds_read_b64_tr_b16 a[210:211], v33 offset:60064\n" \
"v_mfma_f32_16x16x32_bf16 v[140:143], a[212:215], a[48:51], v[140:143]\n" \
"ds_read_b64_tr_b16 a[212:213], v10 offset:160\n" \
"ds_read_b64_tr_b16 a[214:215], v10 offset:4768\n" \
"v_mfma_f32_16x16x32_bf16 v[136:139], a[216:219], a[36:39], v[136:139]\n" \
"v_mfma_f32_16x16x32_bf16 v[140:143], a[216:219], a[52:55], v[140:143]\n" \
"buffer_load_dwordx4 a[108:111], v20, s[56:59], s93 offen offset:3072 sc1\n" \
"v_mfma_f32_16x16x32_bf16 v[136:139], a[0:3], a[40:43], v[136:139]\n" \
"ds_read_b64_tr_b16 a[216:217], v34 offset:160\n" \
"ds_read_b64_tr_b16 a[218:219], v34 offset:4768\n" \
"v_mfma_f32_16x16x32_bf16 v[140:143], a[0:3], a[56:59], v[140:143]\n" \
"ds_read_b64_tr_b16 a[0:1], v34 offset:9376\n" \
"ds_read_b64_tr_b16 a[2:3], v34 offset:13984\n" \
"v_mfma_f32_16x16x32_bf16 v[136:139], a[4:7], a[44:47], v[136:139]\n" \
"v_mfma_f32_16x16x32_bf16 v[140:143], a[4:7], a[60:63], v[140:143]\n" \
"ds_read_b64_tr_b16 a[4:5], v34 offset:18592\n" \
"ds_read_b64_tr_b16 a[6:7], v34 offset:23200\n" \
"buffer_load_dwordx4 a[124:127], v32, s[56:59], s93 offen offset:3072 sc1\n" \
"v_mfma_f32_16x16x32_bf16 v[144:147], a[8:11], a[128:131], v[144:147]\n" \
"v_readlane_b32 s93, v11, 47\n" \
"v_mfma_f32_16x16x32_bf16 v[148:151], a[8:11], a[144:147], v[148:151]\n" \
"ds_read_b64_tr_b16 a[8:9], v10 offset:9376\n" \
"v_cvt_pk_bf16_f32 v136, v136, v137\n" \
"v_cvt_pk_bf16_f32 v137, v138, v139\n" \
"v_cvt_pk_bf16_f32 v138, v140, v141\n" \
"v_cvt_pk_bf16_f32 v139, v142, v143\n" \
"buffer_store_dwordx4 v[136:139], v20, s[56:59], s68 offen offset:0 sc1\n" \
"s_add_u32 s68, s68, 0x2000\n" \
"v_mfma_f32_16x16x32_bf16 v[144:147], a[12:15], a[132:135], v[144:147]\n" \
"ds_read_b64_tr_b16 a[10:11], v10 offset:13984\n" \
"v_mfma_f32_16x16x32_bf16 v[148:151], a[12:15], a[148:151], v[148:151]\n" \
"ds_read_b64_tr_b16 a[12:13], v33 offset:37056\n" \
"ds_read_b64_tr_b16 a[14:15], v33 offset:41664\n" \
"v_mfma_f32_16x16x32_bf16 v[144:147], a[16:19], a[136:139], v[144:147]\n" \
"v_mfma_f32_16x16x32_bf16 v[148:151], a[16:19], a[152:155], v[148:151]\n" \
"ds_read_b64_tr_b16 a[16:17], v33 offset:46272\n" \
"ds_read_b64_tr_b16 a[18:19], v33 offset:50880\n" \
"v_mfma_f32_16x16x32_bf16 v[144:147], a[20:23], a[140:143], v[144:147]\n" \
"buffer_load_dwordx4 v[64:67], v20, s[56:59], s95 offen offset:0 sc1\n" \
"v_mfma_f32_16x16x32_bf16 v[148:151], a[20:23], a[156:159], v[148:151]\n" \
"ds_read_b64_tr_b16 a[20:21], v33 offset:55488\n" \
"s_waitcnt lgkmcnt(13)\n" \
"v_mfma_f32_16x16x32_bf16 v[144:147], a[24:27], a[32:35], v[144:147]\n" \
"ds_read_b64_tr_b16 a[22:23], v33 offset:60096\n" \
"v_mfma_f32_16x16x32_bf16 v[148:151], a[24:27], a[48:51], v[148:151]\n" \
"ds_read_b64_tr_b16 a[24:25], v10 offset:192\n" \
"ds_read_b64_tr_b16 a[26:27], v10 offset:4800\n" \
"v_mfma_f32_16x16x32_bf16 v[144:147], a[28:31], a[36:39], v[144:147]\n" \
"v_mfma_f32_16x16x32_bf16 v[148:151], a[28:31], a[52:55], v[148:151]\n" \
"ds_read_b64_tr_b16 a[28:29], v34 offset:192\n" \
"ds_read_b64_tr_b16 a[30:31], v34 offset:4800\n" \
"v_mfma_f32_16x16x32_bf16 v[144:147], a[192:195], a[40:43], v[144:147]\n" \
"buffer_load_dwordx4 v[68:71], v20, s[56:59], s95 offen offset:1024 sc1\n" \
"v_mfma_f32_16x16x32_bf16 v[148:151], a[192:195], a[56:59], v[148:151]\n" \
"ds_read_b64_tr_b16 a[192:193], v34 offset:9408\n" \
"ds_read_b64_tr_b16 a[194:195], v34 offset:14016\n" \
"v_mfma_f32_16x16x32_bf16 v[144:147], a[196:199], a[44:47], v[144:147]\n" \
"v_mfma_f32_16x16x32_bf16 v[148:151], a[196:199], a[60:63], v[148:151]\n" \
"ds_read_b64_tr_b16 a[196:197], v34 offset:18624\n" \
"ds_read_b64_tr_b16 a[198:199], v34 offset:23232\n" \
"v_mfma_f32_16x16x32_bf16 v[152:155], a[200:203], a[128:131], v[152:155]\n" \
"v_mfma_f32_16x16x32_bf16 v[156:159], a[200:203], a[144:147], v[156:159]\n" \
"ds_read_b64_tr_b16 a[200:201], v10 offset:9408\n" \
"ds_read_b64_tr_b16 a[202:203], v10 offset:14016\n" \
"v_mfma_f32_16x16x32_bf16 v[152:155], a[204:207], a[132:135], v[152:155]\n" \
"v_cvt_pk_bf16_f32 v144, v144, v145\n" \
"v_cvt_pk_bf16_f32 v145, v146, v147\n" \
"v_cvt_pk_bf16_f32 v146, v148, v149\n" \
"v_cvt_pk_bf16_f32 v147, v150, v151\n" \
"buffer_store_dwordx4 v[144:147], v20, s[56:59], s69 offen offset:0 sc1\n" \
"s_add_u32 s69, s69, 0x1800\n" \
"v_mfma_f32_16x16x32_bf16 v[156:159], a[204:207], a[148:151], v[156:159]\n" \
"ds_read_b64_tr_b16 a[204:205], v33 offset:256\n" \
"ds_read_b64_tr_b16 a[206:207], v33 offset:4864\n" \
"v_mfma_f32_16x16x32_bf16 v[152:155], a[208:211], a[136:139], v[152:155]\n" \
"v_mfma_f32_16x16x32_bf16 v[156:159], a[208:211], a[152:155], v[156:159]\n" \
"ds_read_b64_tr_b16 a[208:209], v33 offset:9472\n" \
"ds_read_b64_tr_b16 a[210:211], v33 offset:14080\n" \
"v_mfma_f32_16x16x32_bf16 v[152:155], a[212:215], a[140:143], v[152:155]\n" \
"buffer_load_dwordx4 v[72:75], v32, s[56:59], s95 offen offset:0 sc1\n" \
"v_mfma_f32_16x16x32_bf16 v[156:159], a[212:215], a[156:159], v[156:159]\n" \
"ds_read_b64_tr_b16 a[212:213], v33 offset:18688\n" \
"s_waitcnt lgkmcnt(13)\n" \
"v_mfma_f32_16x16x32_bf16 v[152:155], a[216:219], a[32:35], v[152:155]\n" \
"ds_read_b64_tr_b16 a[214:215], v33 offset:23296\n" \
"v_mfma_f32_16x16x32_bf16 v[156:159], a[216:219], a[48:51], v[156:159]\n" \
"ds_read_b64_tr_b16 a[216:217], v33 offset:27904\n" \
"ds_read_b64_tr_b16 a[218:219], v33 offset:32512\n" \
"v_mfma_f32_16x16x32_bf16 v[152:155], a[0:3], a[36:39], v[152:155]\n" \
"v_mfma_f32_16x16x32_bf16 v[156:159], a[0:3], a[52:55], v[156:159]\n" \
"ds_read_b64_tr_b16 a[0:1], v33 offset:37120\n" \
"ds_read_b64_tr_b16 a[2:3], v33 offset:41728\n" \
"v_mfma_f32_16x16x32_bf16 v[152:155], a[4:7], a[40:43], v[152:155]\n" \
"buffer_load_dwordx4 v[76:79], v32, s[56:59], s95 offen offset:1024 sc1\n" \
"v_mfma_f32_16x16x32_bf16 v[156:159], a[4:7], a[56:59], v[156:159]\n" \
"ds_read_b64_tr_b16 a[4:5], v33 offset:46336\n" \
"ds_read_b64_tr_b16 a[6:7], v33 offset:50944\n" \
"v_mfma_f32_16x16x32_bf16 v[152:155], a[8:11], a[44:47], v[152:155]\n" \
"v_readlane_b32 s95, v11, 49\n" \
"v_mfma_f32_16x16x32_bf16 v[156:159], a[8:11], a[60:63], v[156:159]\n" \
"ds_read_b64_tr_b16 a[8:9], v33 offset:55552\n" \
"ds_read_b64_tr_b16 a[10:11], v33 offset:60160\n" \
"v_mfma_f32_16x16x32_bf16 v[160:163], a[12:15], a[128:131], v[160:163]\n" \
"v_mfma_f32_16x16x32_bf16 v[164:167], a[12:15], a[144:147], v[164:167]\n" \
"ds_read_b64_tr_b16 a[12:13], v10 offset:256\n" \
"ds_read_b64_tr_b16 a[14:15], v10 offset:4864\n" \
"v_cvt_pk_bf16_f32 v152, v152, v153\n" \
"v_cvt_pk_bf16_f32 v153, v154, v155\n" \
"v_cvt_pk_bf16_f32 v154, v156, v157\n" \
"v_cvt_pk_bf16_f32 v155, v158, v159\n" \
"buffer_store_dwordx4 v[152:155], v20, s[56:59], s70 offen offset:0 sc1\n" \
"s_add_u32 s70, s70, 0x1000\n" \
"v_mfma_f32_16x16x32_bf16 v[160:163], a[16:19], a[132:135], v[160:163]\n" \
"v_mfma_f32_16x16x32_bf16 v[164:167], a[16:19], a[148:151], v[164:167]\n" \
"ds_read_b64_tr_b16 a[16:17], v34 offset:256\n" \
"ds_read_b64_tr_b16 a[18:19], v34 offset:4864\n" \
"v_mfma_f32_16x16x32_bf16 v[160:163], a[20:23], a[136:139], v[160:163]\n" \
"v_mfma_f32_16x16x32_bf16 v[164:167], a[20:23], a[152:155], v[164:167]\n" \
"ds_read_b64_tr_b16 a[20:21], v34 offset:9472\n" \
"ds_read_b64_tr_b16 a[22:23], v34 offset:14080\n" \
"v_mfma_f32_16x16x32_bf16 v[160:163], a[24:27], a[140:143], v[160:163]\n" \
"buffer_load_dwordx4 v[80:83], v20, s[56:59], s96 offen offset:0 sc1\n" \
"v_mfma_f32_16x16x32_bf16 v[164:167], a[24:27], a[156:159], v[164:167]\n" \
"ds_read_b64_tr_b16 a[24:25], v34 offset:18688\n" \
"s_waitcnt lgkmcnt(12)\n" \
"v_mfma_f32_16x16x32_bf16 v[160:163], a[28:31], a[32:35], v[160:163]\n" \
"ds_read_b64_tr_b16 a[26:27], v34 offset:23296\n" \
"v_mfma_f32_16x16x32_bf16 v[164:167], a[28:31], a[48:51], v[164:167]\n" \
"ds_read_b64_tr_b16 a[28:29], v10 offset:9472\n" \
"ds_read_b64_tr_b16 a[30:31], v10 offset:14080\n" \
"s_waitcnt vmcnt(18)\n" \
"s_mov_b64 exec, 1\n" \
"buffer_store_dword v39, off, s[56:59], s65 offset:0 sc1\n" \
"s_mov_b64 exec, -1\n" \
"v_mfma_f32_16x16x32_bf16 v[160:163], a[192:195], a[36:39], v[160:163]\n" \
"v_mfma_f32_16x16x32_bf16 v[164:167], a[192:195], a[52:55], v[164:167]\n" \
"ds_read_b64_tr_b16 a[192:193], v33 offset:0\n" \
"ds_read_b64_tr_b16 a[194:195], v33 offset:4608\n" \
"v_mfma_f32_16x16x32_bf16 v[160:163], a[196:199], a[40:43], v[160:163]\n" \
"v_mfma_f32_16x16x32_bf16 v[164:167], a[196:199], a[56:59], v[164:167]\n" \
"ds_read_b64_tr_b16 a[196:197], v33 offset:9216\n" \
"ds_read_b64_tr_b16 a[198:199], v33 offset:13824\n" \
"v_mfma_f32_16x16x32_bf16 v[160:163], a[200:203], a[44:47], v[160:163]\n" \
"buffer_load_dwordx4 v[84:87], v20, s[56:59], s96 offen offset:1024 sc1\n" \
"v_mfma_f32_16x16x32_bf16 v[164:167], a[200:203], a[60:63], v[164:167]\n" \
"ds_read_b64_tr_b16 a[200:201], v33 offset:18432\n" \
"ds_read_b64_tr_b16 a[202:203], v33 offset:23040\n" \
"v_mfma_f32_16x16x32_bf16 v[248:251], a[204:207], a[220:223], v[248:251]\n" \
"v_mfma_f32_16x16x32_bf16 v[252:255], a[204:207], a[236:239], v[252:255]\n" \
"ds_read_b64_tr_b16 a[204:205], v33 offset:27648\n" \
"ds_read_b64_tr_b16 a[206:207], v33 offset:32256\n" \
"v_cvt_pk_bf16_f32 v160, v160, v161\n" \
"v_cvt_pk_bf16_f32 v161, v162, v163\n" \
"v_cvt_pk_bf16_f32 v162, v164, v165\n" \
"v_cvt_pk_bf16_f32 v163, v166, v167\n" \
"buffer_store_dwordx4 v[160:163], v20, s[56:59], s71 offen offset:0 sc1\n" \
"s_add_u32 s71, s71, 0x800\n" \
"v_mfma_f32_16x16x32_bf16 v[248:251], a[208:211], a[224:227], v[248:251]\n" \
"v_mfma_f32_16x16x32_bf16 v[252:255], a[208:211], a[240:243], v[252:255]\n" \
"ds_read_b64_tr_b16 a[208:209], v33 offset:36864\n" \
"ds_read_b64_tr_b16 a[210:211], v33 offset:41472\n" \
"v_mfma_f32_16x16x32_bf16 v[248:251], a[212:215], a[228:231], v[248:251]\n" \
"v_mfma_f32_16x16x32_bf16 v[252:255], a[212:215], a[244:247], v[252:255]\n" \
"ds_read_b64_tr_b16 a[212:213], v33 offset:46080\n" \
"ds_read_b64_tr_b16 a[214:215], v33 offset:50688\n" \
"v_mfma_f32_16x16x32_bf16 v[248:251], a[216:219], a[232:235], v[248:251]\n" \
"buffer_load_dwordx4 v[88:91], v32, s[56:59], s96 offen offset:0 sc1\n" \
"v_mfma_f32_16x16x32_bf16 v[252:255], a[216:219], a[248:251], v[252:255]\n" \
"ds_read_b64_tr_b16 a[216:217], v33 offset:55296\n" \
"s_waitcnt lgkmcnt(12)\n" \
"v_mfma_f32_16x16x32_bf16 v[176:179], a[0:3], a[128:131], v[176:179]\n" \
"ds_read_b64_tr_b16 a[218:219], v33 offset:59904\n" \
"s_waitcnt vmcnt(18)\n" \
"s_mov_b64 exec, 1\n" \
"buffer_store_dword v39, off, s[56:59], s66 offset:4 sc1\n" \
"s_mov_b64 exec, -1\n" \
"v_mfma_f32_16x16x32_bf16 v[180:183], a[0:3], a[144:147], v[180:183]\n" \
"v_mfma_f32_16x16x32_bf16 v[248:251], a[0:3], a[160:163], v[248:251]\n" \
"v_mfma_f32_16x16x32_bf16 v[252:255], a[0:3], a[176:179], v[252:255]\n" \
"ds_read_b64_tr_b16 a[0:1], v10 offset:0\n" \
"ds_read_b64_tr_b16 a[2:3], v10 offset:4608\n" \
"v_mfma_f32_16x16x32_bf16 v[176:179], a[4:7], a[132:135], v[176:179]\n" \
"buffer_load_dwordx4 v[92:95], v32, s[56:59], s96 offen offset:1024 sc1\n" \
"v_mfma_f32_16x16x32_bf16 v[180:183], a[4:7], a[148:151], v[180:183]\n" \
"v_readlane_b32 s96, v11, 50\n" \
"v_mfma_f32_16x16x32_bf16 v[248:251], a[4:7], a[164:167], v[248:251]\n" \
"v_mfma_f32_16x16x32_bf16 v[252:255], a[4:7], a[180:183], v[252:255]\n" \
"ds_read_b64_tr_b16 a[4:5], v34 offset:0\n" \
"ds_read_b64_tr_b16 a[6:7], v34 offset:4608\n" \
"v_mfma_f32_16x16x32_bf16 v[176:179], a[8:11], a[136:139], v[176:179]\n" \
"v_mfma_f32_16x16x32_bf16 v[180:183], a[8:11], a[152:155], v[180:183]\n" \
"buffer_load_dwordx4 v[96:99], v20, s[56:59], s97 offen offset:0 sc1\n" \
"v_mfma_f32_16x16x32_bf16 v[248:251], a[8:11], a[168:171], v[248:251]\n" \
"v_mfma_f32_16x16x32_bf16 v[252:255], a[8:11], a[184:187], v[252:255]\n" \
"ds_read_b64_tr_b16 a[8:9], v34 offset:9216\n" \
"ds_read_b64_tr_b16 a[10:11], v34 offset:13824\n" \
"v_mfma_f32_16x16x32_bf16 v[176:179], a[12:15], a[140:143], v[176:179]\n" \
"v_mfma_f32_16x16x32_bf16 v[180:183], a[12:15], a[156:159], v[180:183]\n" \
"v_mfma_f32_16x16x32_bf16 v[248:251], a[12:15], a[172:175], v[248:251]\n" \
"buffer_load_dwordx4 v[104:107], v32, s[56:59], s97 offen offset:0 sc1\n" \
"v_mfma_f32_16x16x32_bf16 v[252:255], a[12:15], a[188:191], v[252:255]\n" \
"ds_read_b64_tr_b16 a[12:13], v34 offset:18432\n" \
"ds_read_b64_tr_b16 a[14:15], v34 offset:23040\n" \
"s_waitcnt vmcnt(19)\n" \
"s_mov_b64 exec, 1\n" \
"buffer_store_dword v39, off, s[56:59], s67 offset:8 sc1\n" \
"s_mov_b64 exec, -1\n" \
"v_mfma_f32_16x16x32_bf16 v[176:179], a[16:19], a[32:35], v[176:179]\n" \
"v_mfma_f32_16x16x32_bf16 v[180:183], a[16:19], a[48:51], v[180:183]\n" \
"v_mfma_f32_16x16x32_bf16 v[248:251], a[16:19], a[96:99], v[248:251]\n" \
"v_mfma_f32_16x16x32_bf16 v[252:255], a[16:19], a[112:115], v[252:255]\n" \
"ds_read_b64_tr_b16 a[16:17], v10 offset:9216\n" \
"ds_read_b64_tr_b16 a[18:19], v10 offset:13824\n" \
"v_mfma_f32_16x16x32_bf16 v[176:179], a[20:23], a[36:39], v[176:179]\n" \
"buffer_load_dwordx4 v[100:103], v20, s[56:59], s97 offen offset:1024 sc1\n" \
"v_mfma_f32_16x16x32_bf16 v[180:183], a[20:23], a[52:55], v[180:183]\n" \
"v_mfma_f32_16x16x32_bf16 v[248:251], a[20:23], a[100:103], v[248:251]\n" \
"v_mfma_f32_16x16x32_bf16 v[252:255], a[20:23], a[116:119], v[252:255]\n" \
"ds_read_b64_tr_b16 a[20:21], v33 offset:32\n" \
"ds_read_b64_tr_b16 a[22:23], v33 offset:4640\n" \
"v_mfma_f32_16x16x32_bf16 v[176:179], a[24:27], a[40:43], v[176:179]\n" \
"v_mfma_f32_16x16x32_bf16 v[180:183], a[24:27], a[56:59], v[180:183]\n" \
"buffer_load_dwordx4 v[108:111], v32, s[56:59], s97 offen offset:1024 sc1\n" \
"v_mfma_f32_16x16x32_bf16 v[248:251], a[24:27], a[104:107], v[248:251]\n" \
"s_waitcnt vmcnt(21)\n" \
"v_mfma_f32_16x16x32_bf16 v[252:255], a[24:27], a[120:123], v[252:255]\n" \
"ds_read_b64_tr_b16 a[24:25], v33 offset:9248\n" \
"ds_read_b64_tr_b16 a[26:27], v33 offset:13856\n" \
"v_mfma_f32_16x16x32_bf16 v[176:179], a[28:31], a[44:47], v[176:179]\n" \
"v_mfma_f32_16x16x32_bf16 v[180:183], a[28:31], a[60:63], v[180:183]\n" \
"s_waitcnt vmcnt(20)\n" \
".fill (L_ab + 40960 - .) / 4, 4, 0xBF800000\n" \
"L_ap10:\n" \
"s_cbranch_execz L_ap0\n" \
"v_mfma_f32_16x16x32_bf16 v[248:251], a[28:31], a[108:111], v[248:251]\n" \
"s_waitcnt vmcnt(19)\n" \
"v_mfma_f32_16x16x32_bf16 v[252:255], a[28:31], a[124:127], v[252:255]\n" \
"ds_read_b64_tr_b16 a[28:29], v33 offset:18464\n" \
"s_waitcnt vmcnt(18)\n" \
"s_mov_b64 exec, 1\n" \
"buffer_store_dword v39, off, s[56:59], s68 offset:12 sc1\n" \
"s_mov_b64 exec, -1\n" \
"v_cvt_pk_bf16_f32 v176, v176, v177\n" \
"v_cvt_pk_bf16_f32 v248, v248, v249\n" \
"v_cvt_pk_bf16_f32 v177, v178, v179\n" \
"v_cvt_pk_bf16_f32 v249, v250, v251\n" \
"v_cvt_pk_bf16_f32 v178, v180, v181\n" \
"v_cvt_pk_bf16_f32 v179, v182, v183\n" \
"v_cvt_pk_bf16_f32 v250, v252, v253\n" \
"v_cvt_pk_bf16_f32 v251, v254, v255\n" \
"buffer_store_dwordx4 v[176:179], v20, s[56:59], s72 offen offset:0 sc1\n" \
"s_waitcnt lgkmcnt(5)\n" \
"v_mfma_f32_16x16x32_bf16 v[184:187], a[192:195], a[220:223], v[184:187]\n" \
"ds_read_b64_tr_b16 a[30:31], v33 offset:23072\n" \
"v_mfma_f32_16x16x32_bf16 v[188:191], a[192:195], a[236:239], v[188:191]\n" \
"ds_read_b64_tr_b16 a[192:193], v33 offset:27680\n" \
"ds_read_b64_tr_b16 a[194:195], v33 offset:32288\n" \
"buffer_store_dwordx4 v[248:251], v20, s[56:59], s72 offen offset:1024 sc1\n" \
"v_mfma_f32_16x16x32_bf16 v[184:187], a[196:199], a[224:227], v[184:187]\n" \
"v_readlane_b32 s72, v11, 52\n" \
"v_mfma_f32_16x16x32_bf16 v[188:191], a[196:199], a[240:243], v[188:191]\n" \
"ds_read_b64_tr_b16 a[196:197], v33 offset:36896\n" \
"ds_read_b64_tr_b16 a[198:199], v33 offset:41504\n" \
"v_mfma_f32_16x16x32_bf16 v[184:187], a[200:203], a[228:231], v[184:187]\n" \
"v_mfma_f32_16x16x32_bf16 v[188:191], a[200:203], a[244:247], v[188:191]\n" \
"ds_read_b64_tr_b16 a[200:201], v33 offset:46112\n" \
"ds_read_b64_tr_b16 a[202:203], v33 offset:50720\n" \
"v_mfma_f32_16x16x32_bf16 v[184:187], a[204:207], a[232:235], v[184:187]\n" \
"v_mfma_f32_16x16x32_bf16 v[188:191], a[204:207], a[248:251], v[188:191]\n" \
"ds_read_b64_tr_b16 a[204:205], v33 offset:55328\n" \
"ds_read_b64_tr_b16 a[206:207], v33 offset:59936\n" \
"v_mfma_f32_16x16x32_bf16 v[184:187], a[208:211], a[160:163], v[184:187]\n" \
"v_mfma_f32_16x16x32_bf16 v[188:191], a[208:211], a[176:179], v[188:191]\n" \
"ds_read_b64_tr_b16 a[208:209], v10 offset:32\n" \
"ds_read_b64_tr_b16 a[210:211], v10 offset:4640\n" \
"v_mfma_f32_16x16x32_bf16 v[184:187], a[212:215], a[164:167], v[184:187]\n" \
"v_mfma_f32_16x16x32_bf16 v[188:191], a[212:215], a[180:183], v[188:191]\n" \
"ds_read_b64_tr_b16 a[212:213], v34 offset:32\n" \
"ds_read_b64_tr_b16 a[214:215], v34 offset:4640\n" \
"v_mfma_f32_16x16x32_bf16 v[184:187], a[216:219], a[168:171], v[184:187]\n" \
"v_mfma_f32_16x16x32_bf16 v[188:191], a[216:219], a[184:187], v[188:191]\n" \
"ds_read_b64_tr_b16 a[216:217], v34 offset:9248\n" \
"ds_read_b64_tr_b16 a[218:219], v34 offset:13856\n" \
"s_waitcnt vmcnt(18)\n" \
"s_mov_b64 exec, 1\n" \
"buffer_store_dword v39, off, s[56:59], s69 offset:16 sc1\n" \
"s_mov_b64 exec, -1\n" \
"v_mfma_f32_16x16x32_bf16 v[184:187], a[0:3], a[172:175], v[184:187]\n" \
"v_mfma_f32_16x16x32_bf16 v[188:191], a[0:3], a[188:191], v[188:191]\n" \
"ds_read_b64_tr_b16 a[0:1], v34 offset:18464\n" \
"ds_read_b64_tr_b16 a[2:3], v34 offset:23072\n" \
"v_mfma_f32_16x16x32_bf16 v[184:187], a[4:7], a[96:99], v[184:187]\n" \
"v_mfma_f32_16x16x32_bf16 v[188:191], a[4:7], a[112:115], v[188:191]\n" \
"ds_read_b64_tr_b16 a[4:5], v10 offset:9248\n" \
"ds_read_b64_tr_b16 a[6:7], v10 offset:13856\n" \
"v_mfma_f32_16x16x32_bf16 v[184:187], a[8:11], a[100:103], v[184:187]\n" \
"v_mfma_f32_16x16x32_bf16 v[188:191], a[8:11], a[116:119], v[188:191]\n" \
"ds_read_b64_tr_b16 a[8:9], v33 offset:64\n" \
"ds_read_b64_tr_b16 a[10:11], v33 offset:4672\n" \
"v_mfma_f32_16x16x32_bf16 v[184:187], a[12:15], a[104:107], v[184:187]\n" \
"v_mfma_f32_16x16x32_bf16 v[188:191], a[12:15], a[120:123], v[188:191]\n" \
"ds_read_b64_tr_b16 a[12:13], v33 offset:9280\n" \
"ds_read_b64_tr_b16 a[14:15], v33 offset:13888\n" \
"v_mfma_f32_16x16x32_bf16 v[184:187], a[16:19], a[108:111], v[184:187]\n" \
"v_mfma_f32_16x16x32_bf16 v[188:191], a[16:19], a[124:127], v[188:191]\n" \
"ds_read_b64_tr_b16 a[16:17], v33 offset:18496\n" \
"s_waitcnt lgkmcnt(13)\n" \
"v_mfma_f32_16x16x32_bf16 v[192:195], a[20:23], a[220:223], v[192:195]\n" \
"ds_read_b64_tr_b16 a[18:19], v33 offset:23104\n" \
"v_mfma_f32_16x16x32_bf16 v[196:199], a[20:23], a[236:239], v[196:199]\n" \
"ds_read_b64_tr_b16 a[20:21], v33 offset:27712\n" \
"ds_read_b64_tr_b16 a[22:23], v33 offset:32320\n" \
"v_cvt_pk_bf16_f32 v184, v184, v185\n" \
"v_cvt_pk_bf16_f32 v185, v186, v187\n" \
"v_cvt_pk_bf16_f32 v186, v188, v189\n" \
"v_cvt_pk_bf16_f32 v187, v190, v191\n" \
"buffer_store_dwordx4 v[184:187], v20, s[56:59], s92 offen offset:1024 sc1\n" \
"s_add_u32 s92, s92, 0x3800\n" \
"v_mfma_f32_16x16x32_bf16 v[192:195], a[24:27], a[224:227], v[192:195]\n" \
"v_mfma_f32_16x16x32_bf16 v[196:199], a[24:27], a[240:243], v[196:199]\n" \
"ds_read_b64_tr_b16 a[24:25], v33 offset:36928\n" \
"ds_read_b64_tr_b16 a[26:27], v33 offset:41536\n" \
"s_waitcnt vmcnt(17)\n" \
"s_mov_b64 exec, 1\n" \
"buffer_store_dword v39, off, s[56:59], s70 offset:20 sc1\n" \
"s_mov_b64 exec, -1\n" \
"v_mfma_f32_16x16x32_bf16 v[192:195], a[28:31], a[228:231], v[192:195]\n" \
"v_readlane_b32 s70, v11, 42\n" \
"v_mfma_f32_16x16x32_bf16 v[196:199], a[28:31], a[244:247], v[196:199]\n" \
"ds_read_b64_tr_b16 a[28:29], v33 offset:46144\n" \
"ds_read_b64_tr_b16 a[30:31], v33 offset:50752\n" \
"v_mfma_f32_16x16x32_bf16 v[192:195], a[192:195], a[232:235], v[192:195]\n" \
"v_mfma_f32_16x16x32_bf16 v[196:199], a[192:195], a[248:251], v[196:199]\n" \
"ds_read_b64_tr_b16 a[192:193], v33 offset:55360\n" \
"ds_read_b64_tr_b16 a[194:195], v33 offset:59968\n" \
"v_mfma_f32_16x16x32_bf16 v[192:195], a[196:199], a[160:163], v[192:195]\n" \
"v_mfma_f32_16x16x32_bf16 v[196:199], a[196:199], a[176:179], v[196:199]\n" \
"ds_read_b64_tr_b16 a[196:197], v10 offset:64\n" \
"ds_read_b64_tr_b16 a[198:199], v10 offset:4672\n" \
"v_mfma_f32_16x16x32_bf16 v[192:195], a[200:203], a[164:167], v[192:195]\n" \
"v_mfma_f32_16x16x32_bf16 v[196:199], a[200:203], a[180:183], v[196:199]\n" \
"ds_read_b64_tr_b16 a[200:201], v34 offset:64\n" \
"ds_read_b64_tr_b16 a[202:203], v34 offset:4672\n" \
"v_mfma_f32_16x16x32_bf16 v[192:195], a[204:207], a[168:171], v[192:195]\n" \
"v_mfma_f32_16x16x32_bf16 v[196:199], a[204:207], a[184:187], v[196:199]\n" \
"ds_read_b64_tr_b16 a[204:205], v34 offset:9280\n" \
"ds_read_b64_tr_b16 a[206:207], v34 offset:13888\n" \
"v_mfma_f32_16x16x32_bf16 v[192:195], a[208:211], a[172:175], v[192:195]\n" \
"v_mfma_f32_16x16x32_bf16 v[196:199], a[208:211], a[188:191], v[196:199]\n" \
"ds_read_b64_tr_b16 a[208:209], v34 offset:18496\n" \
"s_waitcnt lgkmcnt(13)\n" \
"v_mfma_f32_16x16x32_bf16 v[192:195], a[212:215], a[96:99], v[192:195]\n" \
"ds_read_b64_tr_b16 a[210:211], v34 offset:23104\n" \
"v_mfma_f32_16x16x32_bf16 v[196:199], a[212:215], a[112:115], v[196:199]\n" \
"ds_read_b64_tr_b16 a[212:213], v10 offset:9280\n" \
"ds_read_b64_tr_b16 a[214:215], v10 offset:13888\n" \
"v_mfma_f32_16x16x32_bf16 v[192:195], a[216:219], a[100:103], v[192:195]\n" \
"v_mfma_f32_16x16x32_bf16 v[196:199], a[216:219], a[116:119], v[196:199]\n" \
"ds_read_b64_tr_b16 a[216:217], v33 offset:96\n" \
"ds_read_b64_tr_b16 a[218:219], v33 offset:4704\n" \
"v_mfma_f32_16x16x32_bf16 v[192:195], a[0:3], a[104:107], v[192:195]\n" \
"v_mfma_f32_16x16x32_bf16 v[196:199], a[0:3], a[120:123], v[196:199]\n" \
"ds_read_b64_tr_b16 a[0:1], v33 offset:9312\n" \
"ds_read_b64_tr_b16 a[2:3], v33 offset:13920\n" \
"s_waitcnt vmcnt(14)\n" \
"s_mov_b64 exec, 1\n" \
"buffer_store_dword v39, off, s[56:59], s71 offset:24 sc1\n" \
"s_mov_b64 exec, -1\n" \
"v_mfma_f32_16x16x32_bf16 v[192:195], a[4:7], a[108:111], v[192:195]\n" \
"v_readlane_b32 s71, v11, 51\n" \
"v_mfma_f32_16x16x32_bf16 v[196:199], a[4:7], a[124:127], v[196:199]\n" \
"ds_read_b64_tr_b16 a[4:5], v33 offset:18528\n" \
"ds_read_b64_tr_b16 a[6:7], v33 offset:23136\n" \
"v_mfma_f32_16x16x32_bf16 v[200:203], a[8:11], a[220:223], v[200:203]\n" \
"v_mfma_f32_16x16x32_bf16 v[204:207], a[8:11], a[236:239], v[204:207]\n" \
"ds_read_b64_tr_b16 a[8:9], v33 offset:27744\n" \
"ds_read_b64_tr_b16 a[10:11], v33 offset:32352\n" \
"v_cvt_pk_bf16_f32 v192, v192, v193\n" \
"v_cvt_pk_bf16_f32 v193, v194, v195\n" \
"v_cvt_pk_bf16_f32 v194, v196, v197\n" \
"v_cvt_pk_bf16_f32 v195, v198, v199\n" \
"buffer_store_dwordx4 v[192:195], v20, s[56:59], s93 offen offset:1024 sc1\n" \
"s_add_u32 s93, s93, 0x3000\n" \
"v_mfma_f32_16x16x32_bf16 v[200:203], a[12:15], a[224:227], v[200:203]\n" \
"v_mfma_f32_16x16x32_bf16 v[204:207], a[12:15], a[240:243], v[204:207]\n" \
"ds_read_b64_tr_b16 a[12:13], v33 offset:36960\n" \
"ds_read_b64_tr_b16 a[14:15], v33 offset:41568\n" \
"v_mfma_f32_16x16x32_bf16 v[200:203], a[16:19], a[228:231], v[200:203]\n" \
"v_mfma_f32_16x16x32_bf16 v[204:207], a[16:19], a[244:247], v[204:207]\n" \
"ds_read_b64_tr_b16 a[16:17], v33 offset:46176\n" \
"ds_read_b64_tr_b16 a[18:19], v33 offset:50784\n" \
"v_mfma_f32_16x16x32_bf16 v[200:203], a[20:23], a[232:235], v[200:203]\n" \
"v_mfma_f32_16x16x32_bf16 v[204:207], a[20:23], a[248:251], v[204:207]\n" \
"ds_read_b64_tr_b16 a[20:21], v33 offset:55392\n" \
"s_waitcnt lgkmcnt(11)\n" \
"v_mfma_f32_16x16x32_bf16 v[200:203], a[24:27], a[160:163], v[200:203]\n" \
"ds_read_b64_tr_b16 a[22:23], v33 offset:60000\n" \
"v_mfma_f32_16x16x32_bf16 v[204:207], a[24:27], a[176:179], v[204:207]\n" \
"ds_read_b64_tr_b16 a[24:25], v10 offset:96\n" \
"ds_read_b64_tr_b16 a[26:27], v10 offset:4704\n" \
"v_mfma_f32_16x16x32_bf16 v[200:203], a[28:31], a[164:167], v[200:203]\n" \
"v_mfma_f32_16x16x32_bf16 v[204:207], a[28:31], a[180:183], v[204:207]\n" \
"ds_read_b64_tr_b16 a[28:29], v34 offset:96\n" \
"ds_read_b64_tr_b16 a[30:31], v34 offset:4704\n" \
"v_mfma_f32_16x16x32_bf16 v[200:203], a[192:195], a[168:171], v[200:203]\n" \
"v_mfma_f32_16x16x32_bf16 v[204:207], a[192:195], a[184:187], v[204:207]\n" \
"ds_read_b64_tr_b16 a[192:193], v34 offset:9312\n" \
"ds_read_b64_tr_b16 a[194:195], v34 offset:13920\n" \
"v_mfma_f32_16x16x32_bf16 v[200:203], a[196:199], a[172:175], v[200:203]\n" \
"v_mfma_f32_16x16x32_bf16 v[204:207], a[196:199], a[188:191], v[204:207]\n" \
"ds_read_b64_tr_b16 a[196:197], v34 offset:18528\n" \
"ds_read_b64_tr_b16 a[198:199], v34 offset:23136\n" \
"v_mfma_f32_16x16x32_bf16 v[200:203], a[200:203], a[96:99], v[200:203]\n" \
"v_mfma_f32_16x16x32_bf16 v[204:207], a[200:203], a[112:115], v[204:207]\n" \
"ds_read_b64_tr_b16 a[200:201], v10 offset:9312\n" \
"ds_read_b64_tr_b16 a[202:203], v10 offset:13920\n" \
"v_mfma_f32_16x16x32_bf16 v[200:203], a[204:207], a[100:103], v[200:203]\n" \
"v_mfma_f32_16x16x32_bf16 v[204:207], a[204:207], a[116:119], v[204:207]\n" \
"ds_read_b64_tr_b16 a[204:205], v33 offset:128\n" \
"ds_read_b64_tr_b16 a[206:207], v33 offset:4736\n" \
"v_mfma_f32_16x16x32_bf16 v[200:203], a[208:211], a[104:107], v[200:203]\n" \
"v_mfma_f32_16x16x32_bf16 v[204:207], a[208:211], a[120:123], v[204:207]\n" \
"ds_read_b64_tr_b16 a[208:209], v33 offset:9344\n" \
"ds_read_b64_tr_b16 a[210:211], v33 offset:13952\n" \
"v_mfma_f32_16x16x32_bf16 v[200:203], a[212:215], a[108:111], v[200:203]\n" \
"v_mfma_f32_16x16x32_bf16 v[204:207], a[212:215], a[124:127], v[204:207]\n" \
"ds_read_b64_tr_b16 a[212:213], v33 offset:18560\n" \
"ds_read_b64_tr_b16 a[214:215], v33 offset:23168\n" \
"v_mfma_f32_16x16x32_bf16 v[208:211], a[216:219], a[220:223], v[208:211]\n" \
"v_mfma_f32_16x16x32_bf16 v[212:215], a[216:219], a[236:239], v[212:215]\n" \
"ds_read_b64_tr_b16 a[216:217], v33 offset:27776\n" \
"s_waitcnt lgkmcnt(13)\n" \
"v_mfma_f32_16x16x32_bf16 v[208:211], a[0:3], a[224:227], v[208:211]\n" \
"v_cvt_pk_bf16_f32 v200, v200, v201\n" \
"v_cvt_pk_bf16_f32 v201, v202, v203\n" \
"v_cvt_pk_bf16_f32 v202, v204, v205\n" \
"v_cvt_pk_bf16_f32 v203, v206, v207\n" \
"buffer_store_dwordx4 v[200:203], v20, s[56:59], s94 offen offset:1024 sc1\n" \
"s_add_u32 s94, s94, 0x2800\n" \
"v_mfma_f32_16x16x32_bf16 v[212:215], a[0:3], a[240:243], v[212:215]\n" \
"ds_read_b64_tr_b16 a[218:219], v33 offset:32384\n" \
"ds_read_b64_tr_b16 a[0:1], v33 offset:36992\n" \
"v_mfma_f32_16x16x32_bf16 v[208:211], a[4:7], a[228:231], v[208:211]\n" \
"ds_read_b64_tr_b16 a[2:3], v33 offset:41600\n" \
"v_mfma_f32_16x16x32_bf16 v[212:215], a[4:7], a[244:247], v[212:215]\n" \
"ds_read_b64_tr_b16 a[4:5], v33 offset:46208\n" \
"ds_read_b64_tr_b16 a[6:7], v33 offset:50816\n" \
"v_mfma_f32_16x16x32_bf16 v[208:211], a[8:11], a[232:235], v[208:211]\n" \
"v_mfma_f32_16x16x32_bf16 v[212:215], a[8:11], a[248:251], v[212:215]\n" \
"ds_read_b64_tr_b16 a[8:9], v33 offset:55424\n" \
"ds_read_b64_tr_b16 a[10:11], v33 offset:60032\n" \
"v_mfma_f32_16x16x32_bf16 v[208:211], a[12:15], a[160:163], v[208:211]\n" \
"v_mfma_f32_16x16x32_bf16 v[212:215], a[12:15], a[176:179], v[212:215]\n" \
"ds_read_b64_tr_b16 a[12:13], v10 offset:128\n" \
"ds_read_b64_tr_b16 a[14:15], v10 offset:4736\n" \
"v_mfma_f32_16x16x32_bf16 v[208:211], a[16:19], a[164:167], v[208:211]\n" \
"v_mfma_f32_16x16x32_bf16 v[212:215], a[16:19], a[180:183], v[212:215]\n" \
"ds_read_b64_tr_b16 a[16:17], v34 offset:128\n" \
"ds_read_b64_tr_b16 a[18:19], v34 offset:4736\n" \
"v_mfma_f32_16x16x32_bf16 v[208:211], a[20:23], a[168:171], v[208:211]\n" \
"v_mfma_f32_16x16x32_bf16 v[212:215], a[20:23], a[184:187], v[212:215]\n" \
"ds_read_b64_tr_b16 a[20:21], v34 offset:9344\n" \
"ds_read_b64_tr_b16 a[22:23], v34 offset:13952\n" \
"v_mfma_f32_16x16x32_bf16 v[208:211], a[24:27], a[172:175], v[208:211]\n" \
"v_mfma_f32_16x16x32_bf16 v[212:215], a[24:27], a[188:191], v[212:215]\n" \
"ds_read_b64_tr_b16 a[24:25], v34 offset:18560\n" \
"ds_read_b64_tr_b16 a[26:27], v34 offset:23168\n" \
"s_waitcnt vmcnt(6)\n" \
"s_mov_b64 exec, 3\n" \
"buffer_store_dword v39, v41, s[56:59], s73 offen sc1\n" \
"s_mov_b64 exec, -1\n" \
"v_mfma_f32_16x16x32_bf16 v[208:211], a[28:31], a[96:99], v[208:211]\n" \
"v_mfma_f32_16x16x32_bf16 v[212:215], a[28:31], a[112:115], v[212:215]\n" \
"ds_read_b64_tr_b16 a[28:29], v10 offset:9344\n" \
"s_waitcnt lgkmcnt(13)\n" \
"v_mfma_f32_16x16x32_bf16 v[208:211], a[192:195], a[100:103], v[208:211]\n" \
"ds_read_b64_tr_b16 a[30:31], v10 offset:13952\n" \
"v_mfma_f32_16x16x32_bf16 v[212:215], a[192:195], a[116:119], v[212:215]\n" \
"ds_read_b64_tr_b16 a[192:193], v33 offset:160\n" \
"ds_read_b64_tr_b16 a[194:195], v33 offset:4768\n" \
"v_mfma_f32_16x16x32_bf16 v[208:211], a[196:199], a[104:107], v[208:211]\n" \
"v_mfma_f32_16x16x32_bf16 v[212:215], a[196:199], a[120:123], v[212:215]\n" \
"ds_read_b64_tr_b16 a[196:197], v33 offset:9376\n" \
"ds_read_b64_tr_b16 a[198:199], v33 offset:13984\n" \
"v_mfma_f32_16x16x32_bf16 v[208:211], a[200:203], a[108:111], v[208:211]\n" \
"v_mfma_f32_16x16x32_bf16 v[212:215], a[200:203], a[124:127], v[212:215]\n" \
"ds_read_b64_tr_b16 a[200:201], v33 offset:18592\n" \
"ds_read_b64_tr_b16 a[202:203], v33 offset:23200\n" \
"v_mfma_f32_16x16x32_bf16 v[216:219], a[204:207], a[220:223], v[216:219]\n" \
"v_mfma_f32_16x16x32_bf16 v[220:223], a[204:207], a[236:239], v[220:223]\n" \
"ds_read_b64_tr_b16 a[204:205], v33 offset:27808\n" \
"ds_read_b64_tr_b16 a[206:207], v33 offset:32416\n" \
"v_mfma_f32_16x16x32_bf16 v[216:219], a[208:211], a[224:227], v[216:219]\n" \
"v_cvt_pk_bf16_f32 v208, v208, v209\n" \
"v_cvt_pk_bf16_f32 v209, v210, v211\n" \
"v_cvt_pk_bf16_f32 v210, v212, v213\n" \
"v_cvt_pk_bf16_f32 v211, v214, v215\n" \
"buffer_store_dwordx4 v[208:211], v20, s[56:59], s95 offen offset:1024 sc1\n" \
"s_add_u32 s95, s95, 0x2000\n" \
"v_mfma_f32_16x16x32_bf16 v[220:223], a[208:211], a[240:243], v[220:223]\n" \
"ds_read_b64_tr_b16 a[208:209], v33 offset:37024\n" \
"ds_read_b64_tr_b16 a[210:211], v33 offset:41632\n" \
"v_mfma_f32_16x16x32_bf16 v[216:219], a[212:215], a[228:231], v[216:219]\n" \
"v_mfma_f32_16x16x32_bf16 v[220:223], a[212:215], a[244:247], v[220:223]\n" \
"ds_read_b64_tr_b16 a[212:213], v33 offset:46240\n" \
"ds_read_b64_tr_b16 a[214:215], v33 offset:50848\n" \
"v_mfma_f32_16x16x32_bf16 v[216:219], a[216:219], a[232:235], v[216:219]\n" \
"v_mfma_f32_16x16x32_bf16 v[220:223], a[216:219], a[248:251], v[220:223]\n" \
"ds_read_b64_tr_b16 a[216:217], v33 offset:55456\n" \
"ds_read_b64_tr_b16 a[218:219], v33 offset:60064\n" \
"v_mfma_f32_16x16x32_bf16 v[216:219], a[0:3], a[160:163], v[216:219]\n" \
"v_mfma_f32_16x16x32_bf16 v[220:223], a[0:3], a[176:179], v[220:223]\n" \
"ds_read_b64_tr_b16 a[0:1], v10 offset:160\n" \
"s_waitcnt lgkmcnt(13)\n" \
"v_mfma_f32_16x16x32_bf16 v[216:219], a[4:7], a[164:167], v[216:219]\n" \
"ds_read_b64_tr_b16 a[2:3], v10 offset:4768\n" \
"v_mfma_f32_16x16x32_bf16 v[220:223], a[4:7], a[180:183], v[220:223]\n" \
"ds_read_b64_tr_b16 a[4:5], v34 offset:160\n" \
"ds_read_b64_tr_b16 a[6:7], v34 offset:4768\n" \
"v_mfma_f32_16x16x32_bf16 v[216:219], a[8:11], a[168:171], v[216:219]\n" \
"v_mfma_f32_16x16x32_bf16 v[220:223], a[8:11], a[184:187], v[220:223]\n" \
"ds_read_b64_tr_b16 a[8:9], v34 offset:9376\n" \
"ds_read_b64_tr_b16 a[10:11], v34 offset:13984\n" \
"v_mfma_f32_16x16x32_bf16 v[216:219], a[12:15], a[172:175], v[216:219]\n" \
"v_mfma_f32_16x16x32_bf16 v[220:223], a[12:15], a[188:191], v[220:223]\n" \
"ds_read_b64_tr_b16 a[12:13], v34 offset:18592\n" \
"ds_read_b64_tr_b16 a[14:15], v34 offset:23200\n" \
"s_waitcnt vmcnt(6)\n" \
"s_mov_b64 exec, 1\n" \
"buffer_store_dword v39, off, s[56:59], s92 offset:32 sc1\n" \
"s_mov_b64 exec, -1\n" \
"v_mfma_f32_16x16x32_bf16 v[216:219], a[16:19], a[96:99], v[216:219]\n" \
"v_mfma_f32_16x16x32_bf16 v[220:223], a[16:19], a[112:115], v[220:223]\n" \
"ds_read_b64_tr_b16 a[16:17], v10 offset:9376\n" \
"ds_read_b64_tr_b16 a[18:19], v10 offset:13984\n" \
"v_mfma_f32_16x16x32_bf16 v[216:219], a[20:23], a[100:103], v[216:219]\n" \
"v_mfma_f32_16x16x32_bf16 v[220:223], a[20:23], a[116:119], v[220:223]\n" \
"ds_read_b64_tr_b16 a[20:21], v33 offset:192\n" \
"ds_read_b64_tr_b16 a[22:23], v33 offset:4800\n" \
"v_mfma_f32_16x16x32_bf16 v[216:219], a[24:27], a[104:107], v[216:219]\n" \
"v_mfma_f32_16x16x32_bf16 v[220:223], a[24:27], a[120:123], v[220:223]\n" \
"ds_read_b64_tr_b16 a[24:25], v33 offset:9408\n" \
"ds_read_b64_tr_b16 a[26:27], v33 offset:14016\n" \
"v_mfma_f32_16x16x32_bf16 v[216:219], a[28:31], a[108:111], v[216:219]\n" \
"v_mfma_f32_16x16x32_bf16 v[220:223], a[28:31], a[124:127], v[220:223]\n" \
"buffer_load_dword v37, v40, s[56:59], s74 offen sc1\n" \
"v_mfma_f32_16x16x32_bf16 v[224:227], a[192:195], a[220:223], v[224:227]\n" \
"ds_read_b64_tr_b16 a[28:29], v33 offset:18624\n" \
"ds_read_b64_tr_b16 a[30:31], v33 offset:23232\n" \
"v_mfma_f32_16x16x32_bf16 v[228:231], a[192:195], a[236:239], v[228:231]\n" \
"ds_read_b64_tr_b16 a[192:193], v33 offset:27840\n" \
"s_waitcnt lgkmcnt(13)\n" \
"v_mfma_f32_16x16x32_bf16 v[224:227], a[196:199], a[224:227], v[224:227]\n" \
"v_cvt_pk_bf16_f32 v216, v216, v217\n" \
"v_cvt_pk_bf16_f32 v217, v218, v219\n" \
"v_cvt_pk_bf16_f32 v218, v220, v221\n" \
"v_cvt_pk_bf16_f32 v219, v222, v223\n" \
"buffer_store_dwordx4 v[216:219], v20, s[56:59], s96 offen offset:1024 sc1\n" \
"s_add_u32 s96, s96, 0x1800\n" \
"v_mfma_f32_16x16x32_bf16 v[228:231], a[196:199], a[240:243], v[228:231]\n" \
"ds_read_b64_tr_b16 a[194:195], v33 offset:32448\n" \
"ds_read_b64_tr_b16 a[196:197], v33 offset:37056\n" \
"v_mfma_f32_16x16x32_bf16 v[224:227], a[200:203], a[228:231], v[224:227]\n" \
"ds_read_b64_tr_b16 a[198:199], v33 offset:41664\n" \
"v_mfma_f32_16x16x32_bf16 v[228:231], a[200:203], a[244:247], v[228:231]\n" \
"ds_read_b64_tr_b16 a[200:201], v33 offset:46272\n" \
"ds_read_b64_tr_b16 a[202:203], v33 offset:50880\n" \
"v_mfma_f32_16x16x32_bf16 v[224:227], a[204:207], a[232:235], v[224:227]\n" \
"v_mfma_f32_16x16x32_bf16 v[228:231], a[204:207], a[248:251], v[228:231]\n" \
"ds_read_b64_tr_b16 a[204:205], v33 offset:55488\n" \
"ds_read_b64_tr_b16 a[206:207], v33 offset:60096\n" \
"v_mfma_f32_16x16x32_bf16 v[224:227], a[208:211], a[160:163], v[224:227]\n" \
"v_mfma_f32_16x16x32_bf16 v[228:231], a[208:211], a[176:179], v[228:231]\n" \
"ds_read_b64_tr_b16 a[208:209], v10 offset:192\n" \
"ds_read_b64_tr_b16 a[210:211], v10 offset:4800\n" \
"v_mfma_f32_16x16x32_bf16 v[224:227], a[212:215], a[164:167], v[224:227]\n" \
"v_mfma_f32_16x16x32_bf16 v[228:231], a[212:215], a[180:183], v[228:231]\n" \
"ds_read_b64_tr_b16 a[212:213], v34 offset:192\n" \
"ds_read_b64_tr_b16 a[214:215], v34 offset:4800\n" \
"v_mfma_f32_16x16x32_bf16 v[224:227], a[216:219], a[168:171], v[224:227]\n" \
"v_mfma_f32_16x16x32_bf16 v[228:231], a[216:219], a[184:187], v[228:231]\n" \
"ds_read_b64_tr_b16 a[216:217], v34 offset:9408\n" \
"ds_read_b64_tr_b16 a[218:219], v34 offset:14016\n" \
"v_mfma_f32_16x16x32_bf16 v[224:227], a[0:3], a[172:175], v[224:227]\n" \
"v_mfma_f32_16x16x32_bf16 v[228:231], a[0:3], a[188:191], v[228:231]\n" \
"ds_read_b64_tr_b16 a[0:1], v34 offset:18624\n" \
"ds_read_b64_tr_b16 a[2:3], v34 offset:23232\n" \
"v_mfma_f32_16x16x32_bf16 v[224:227], a[4:7], a[96:99], v[224:227]\n" \
"v_mfma_f32_16x16x32_bf16 v[228:231], a[4:7], a[112:115], v[228:231]\n" \
"ds_read_b64_tr_b16 a[4:5], v10 offset:9408\n" \
"s_waitcnt vmcnt(6)\n" \
"s_mov_b64 exec, 1\n" \
"buffer_store_dword v39, off, s[56:59], s93 offset:36 sc1\n" \
"s_mov_b64 exec, -1\n" \
"s_waitcnt lgkmcnt(13)\n" \
"v_mfma_f32_16x16x32_bf16 v[224:227], a[8:11], a[100:103], v[224:227]\n" \
"ds_read_b64_tr_b16 a[6:7], v10 offset:14016\n" \
"v_mfma_f32_16x16x32_bf16 v[228:231], a[8:11], a[116:119], v[228:231]\n" \
"ds_read_b64_tr_b16 a[8:9], v33 offset:224\n" \
"ds_read_b64_tr_b16 a[10:11], v33 offset:4832\n" \
"v_mfma_f32_16x16x32_bf16 v[224:227], a[12:15], a[104:107], v[224:227]\n" \
"v_mfma_f32_16x16x32_bf16 v[228:231], a[12:15], a[120:123], v[228:231]\n" \
"ds_read_b64_tr_b16 a[12:13], v33 offset:9440\n" \
"ds_read_b64_tr_b16 a[14:15], v33 offset:14048\n" \
"v_mfma_f32_16x16x32_bf16 v[224:227], a[16:19], a[108:111], v[224:227]\n" \
"v_mfma_f32_16x16x32_bf16 v[228:231], a[16:19], a[124:127], v[228:231]\n" \
"ds_read_b64_tr_b16 a[16:17], v33 offset:18656\n" \
"ds_read_b64_tr_b16 a[18:19], v33 offset:23264\n" \
"v_mfma_f32_16x16x32_bf16 v[232:235], a[20:23], a[220:223], v[232:235]\n" \
"v_mfma_f32_16x16x32_bf16 v[236:239], a[20:23], a[236:239], v[236:239]\n" \
"ds_read_b64_tr_b16 a[20:21], v33 offset:27872\n" \
"ds_read_b64_tr_b16 a[22:23], v33 offset:32480\n" \
"v_mfma_f32_16x16x32_bf16 v[232:235], a[24:27], a[224:227], v[232:235]\n" \
"v_cvt_pk_bf16_f32 v224, v224, v225\n" \
"v_cvt_pk_bf16_f32 v225, v226, v227\n" \
"v_cvt_pk_bf16_f32 v226, v228, v229\n" \
"v_cvt_pk_bf16_f32 v227, v230, v231\n" \
"buffer_store_dwordx4 v[224:227], v20, s[56:59], s71 offen offset:1024 sc1\n" \
"v_mfma_f32_16x16x32_bf16 v[236:239], a[24:27], a[240:243], v[236:239]\n" \
"ds_read_b64_tr_b16 a[24:25], v33 offset:37088\n" \
"ds_read_b64_tr_b16 a[26:27], v33 offset:41696\n" \
"v_mfma_f32_16x16x32_bf16 v[232:235], a[28:31], a[228:231], v[232:235]\n" \
"v_mfma_f32_16x16x32_bf16 v[236:239], a[28:31], a[244:247], v[236:239]\n" \
"ds_read_b64_tr_b16 a[28:29], v33 offset:46304\n" \
"ds_read_b64_tr_b16 a[30:31], v33 offset:50912\n" \
"v_mfma_f32_16x16x32_bf16 v[232:235], a[192:195], a[232:235], v[232:235]\n" \
"v_mfma_f32_16x16x32_bf16 v[236:239], a[192:195], a[248:251], v[236:239]\n" \
"ds_read_b64_tr_b16 a[192:193], v33 offset:55520\n" \
"ds_read_b64_tr_b16 a[194:195], v33 offset:60128\n" \
"v_mfma_f32_16x16x32_bf16 v[232:235], a[196:199], a[160:163], v[232:235]\n" \
"v_mfma_f32_16x16x32_bf16 v[236:239], a[196:199], a[176:179], v[236:239]\n" \
"ds_read_b64_tr_b16 a[196:197], v10 offset:224\n" \
"s_waitcnt lgkmcnt(13)\n" \
"v_mfma_f32_16x16x32_bf16 v[232:235], a[200:203], a[164:167], v[232:235]\n" \
"ds_read_b64_tr_b16 a[198:199], v10 offset:4832\n" \
"v_mfma_f32_16x16x32_bf16 v[236:239], a[200:203], a[180:183], v[236:239]\n" \
"ds_read_b64_tr_b16 a[200:201], v34 offset:224\n" \
"ds_read_b64_tr_b16 a[202:203], v34 offset:4832\n" \
"v_mfma_f32_16x16x32_bf16 v[232:235], a[204:207], a[168:171], v[232:235]\n" \
"v_mfma_f32_16x16x32_bf16 v[236:239], a[204:207], a[184:187], v[236:239]\n" \
"ds_read_b64_tr_b16 a[204:205], v34 offset:9440\n" \
"ds_read_b64_tr_b16 a[206:207], v34 offset:14048\n" \
"v_mfma_f32_16x16x32_bf16 v[232:235], a[208:211], a[172:175], v[232:235]\n" \
"v_mfma_f32_16x16x32_bf16 v[236:239], a[208:211], a[188:191], v[236:239]\n" \
"ds_read_b64_tr_b16 a[208:209], v34 offset:18656\n" \
"ds_read_b64_tr_b16 a[210:211], v34 offset:23264\n" \
"v_mfma_f32_16x16x32_bf16 v[232:235], a[212:215], a[96:99], v[232:235]\n" \
"v_mfma_f32_16x16x32_bf16 v[236:239], a[212:215], a[112:115], v[236:239]\n" \
"ds_read_b64_tr_b16 a[212:213], v10 offset:9440\n" \
"ds_read_b64_tr_b16 a[214:215], v10 offset:14048\n" \
"s_waitcnt vmcnt(7)\n" \
"s_mov_b64 exec, 1\n" \
"buffer_store_dword v39, off, s[56:59], s94 offset:40 sc1\n" \
"s_mov_b64 exec, -1\n" \
"v_mfma_f32_16x16x32_bf16 v[232:235], a[216:219], a[100:103], v[232:235]\n" \
"v_mfma_f32_16x16x32_bf16 v[236:239], a[216:219], a[116:119], v[236:239]\n" \
"ds_read_b64_tr_b16 a[216:217], v36 offset:0\n" \
"ds_read_b64_tr_b16 a[218:219], v36 offset:1024\n" \
"v_mfma_f32_16x16x32_bf16 v[232:235], a[0:3], a[104:107], v[232:235]\n" \
"v_mfma_f32_16x16x32_bf16 v[236:239], a[0:3], a[120:123], v[236:239]\n" \
"ds_read_b64_tr_b16 a[0:1], v36 offset:2048\n" \
"ds_read_b64_tr_b16 a[2:3], v36 offset:3072\n" \
"v_mfma_f32_16x16x32_bf16 v[232:235], a[4:7], a[108:111], v[232:235]\n" \
"v_mfma_f32_16x16x32_bf16 v[236:239], a[4:7], a[124:127], v[236:239]\n" \
"ds_read_b64_tr_b16 a[4:5], v36 offset:32\n" \
"ds_read_b64_tr_b16 a[6:7], v36 offset:1056\n" \
"v_mfma_f32_16x16x32_bf16 v[240:243], a[8:11], a[220:223], v[240:243]\n" \
"v_mfma_f32_16x16x32_bf16 v[244:247], a[8:11], a[236:239], v[244:247]\n" \
"ds_read_b64_tr_b16 a[8:9], v36 offset:2080\n" \
"s_waitcnt lgkmcnt(13)\n" \
"v_mfma_f32_16x16x32_bf16 v[240:243], a[12:15], a[224:227], v[240:243]\n" \
"v_cvt_pk_bf16_f32 v232, v232, v233\n" \
"v_cvt_pk_bf16_f32 v233, v234, v235\n" \
"v_cvt_pk_bf16_f32 v234, v236, v237\n" \
"v_cvt_pk_bf16_f32 v235, v238, v239\n" \
"buffer_store_dwordx4 v[232:235], v20, s[56:59], s72 offen offset:1024 sc1\n" \
"s_waitcnt vmcnt(5)\n" \
"v_cmp_eq_u32_e64 s[98:99], s62, v37\n" \
"s_mov_b64 exec, 1\n" \
"buffer_store_dword v39, off, s[56:59], s95 offset:44 sc1\n" \
"s_mov_b64 exec, -1\n" \
"v_mfma_f32_16x16x32_bf16 v[244:247], a[12:15], a[240:243], v[244:247]\n" \
"ds_read_b64_tr_b16 a[10:11], v36 offset:3104\n" \
"s_or_b32 s75, 0x7f, s100\n" \
"s_andn2_b32 s75, s75, s98\n" \
"s_cbranch_scc1 L_sx0_%=\n" \
"L_rx0_%=:\n" \
"buffer_load_dword v37, v40, s[56:59], s74 offen sc1\n" \
"s_add_u32 s97, s63, 0x0\n" \
"s_add_u32 s64, s63, 0x800\n" \
"s_add_u32 s65, s63, 0x1000\n" \
"s_add_u32 s66, s63, 0x1800\n" \
"s_add_u32 s67, s63, 0x2000\n" \
"s_add_u32 s68, s63, 0x2800\n" \
"s_add_u32 s69, s63, 0x3000\n" \
"s_add_u32 s71, s71, 0x1000\n" \
"s_add_u32 s72, s72, 0x800\n" \
"v_mfma_f32_16x16x32_bf16 v[240:243], a[16:19], a[228:231], v[240:243]\n" \
"v_mfma_f32_16x16x32_bf16 v[244:247], a[16:19], a[244:247], v[244:247]\n" \
"buffer_load_dwordx4 v[116:119], v20, s[56:59], s97 offen offset:0 sc1\n" \
"v_mfma_f32_16x16x32_bf16 v[240:243], a[20:23], a[232:235], v[240:243]\n" \
"v_mfma_f32_16x16x32_bf16 v[244:247], a[20:23], a[248:251], v[244:247]\n" \
"v_mfma_f32_16x16x32_bf16 v[168:171], a[24:27], a[128:131], v[168:171]\n" \
"v_mfma_f32_16x16x32_bf16 v[172:175], a[24:27], a[144:147], v[172:175]\n" \
"buffer_load_dwordx4 v[124:127], v20, s[56:59], s64 offen offset:0 sc1\n" \
"v_mfma_f32_16x16x32_bf16 v[240:243], a[24:27], a[160:163], v[240:243]\n" \
"v_readlane_b32 s64, v11, 54\n" \
"v_mfma_f32_16x16x32_bf16 v[244:247], a[24:27], a[176:179], v[244:247]\n" \
"v_mfma_f32_16x16x32_bf16 v[168:171], a[28:31], a[132:135], v[168:171]\n" \
"v_mfma_f32_16x16x32_bf16 v[172:175], a[28:31], a[148:151], v[172:175]\n" \
"buffer_load_dwordx4 v[132:135], v20, s[56:59], s65 offen offset:0 sc1\n" \
"v_mfma_f32_16x16x32_bf16 v[240:243], a[28:31], a[164:167], v[240:243]\n" \
"v_readlane_b32 s65, v11, 45\n" \
"v_mfma_f32_16x16x32_bf16 v[244:247], a[28:31], a[180:183], v[244:247]\n" \
"v_mfma_f32_16x16x32_bf16 v[168:171], a[192:195], a[136:139], v[168:171]\n" \
"v_mfma_f32_16x16x32_bf16 v[172:175], a[192:195], a[152:155], v[172:175]\n" \
"buffer_load_dwordx4 v[140:143], v20, s[56:59], s66 offen offset:0 sc1\n" \
"v_mfma_f32_16x16x32_bf16 v[240:243], a[192:195], a[168:171], v[240:243]\n" \
"v_readlane_b32 s66, v11, 43\n" \
"v_mfma_f32_16x16x32_bf16 v[244:247], a[192:195], a[184:187], v[244:247]\n" \
"v_mfma_f32_16x16x32_bf16 v[168:171], a[196:199], a[140:143], v[168:171]\n" \
"v_mfma_f32_16x16x32_bf16 v[172:175], a[196:199], a[156:159], v[172:175]\n" \
"buffer_load_dwordx4 v[148:151], v20, s[56:59], s67 offen offset:0 sc1\n" \
"v_mfma_f32_16x16x32_bf16 v[240:243], a[196:199], a[172:175], v[240:243]\n" \
"v_readlane_b32 s67, v11, 44\n" \
"v_mfma_f32_16x16x32_bf16 v[244:247], a[196:199], a[188:191], v[244:247]\n" \
"v_mfma_f32_16x16x32_bf16 v[168:171], a[200:203], a[32:35], v[168:171]\n" \
"v_mfma_f32_16x16x32_bf16 v[172:175], a[200:203], a[48:51], v[172:175]\n" \
"buffer_load_dwordx4 v[156:159], v20, s[56:59], s68 offen offset:0 sc1\n" \
"v_mfma_f32_16x16x32_bf16 v[240:243], a[200:203], a[96:99], v[240:243]\n" \
"v_mfma_f32_16x16x32_bf16 v[244:247], a[200:203], a[112:115], v[244:247]\n" \
"s_waitcnt lgkmcnt(0)\n" \
"v_mfma_f32_16x16x32_bf16 v[168:171], a[204:207], a[36:39], v[168:171]\n" \
"v_mfma_f32_16x16x32_bf16 v[172:175], a[204:207], a[52:55], v[172:175]\n" \
"buffer_load_dwordx4 v[164:167], v20, s[56:59], s69 offen offset:0 sc1\n" \
"v_mfma_f32_16x16x32_bf16 v[240:243], a[204:207], a[100:103], v[240:243]\n" \
"v_mfma_f32_16x16x32_bf16 v[244:247], a[204:207], a[116:119], v[244:247]\n" \
"v_mfma_f32_16x16x32_bf16 v[168:171], a[208:211], a[40:43], v[168:171]\n" \
"v_mfma_f32_16x16x32_bf16 v[172:175], a[208:211], a[56:59], v[172:175]\n" \
"buffer_load_dwordx4 v[112:115], v20, s[56:59], s70 offen offset:0 sc1\n" \
"v_mfma_f32_16x16x32_bf16 v[240:243], a[208:211], a[104:107], v[240:243]\n" \
"v_mfma_f32_16x16x32_bf16 v[244:247], a[208:211], a[120:123], v[244:247]\n" \
"v_mfma_f32_16x16x32_bf16 v[168:171], a[212:215], a[44:47], v[168:171]\n" \
"v_mfma_f32_16x16x32_bf16 v[172:175], a[212:215], a[60:63], v[172:175]\n" \
"buffer_load_dwordx4 v[184:187], v20, s[56:59], s70 offen offset:2048 sc1\n" \
"s_add_u32 s70, s70, 0x1000\n" \
"v_mfma_f32_16x16x32_bf16 v[240:243], a[212:215], a[108:111], v[240:243]\n" \
"v_mfma_f32_16x16x32_bf16 v[244:247], a[212:215], a[124:127], v[244:247]\n" \
"s_waitcnt vmcnt(15)\n" \
"s_mov_b64 exec, 1\n" \
"buffer_store_dword v39, off, s[56:59], s96 offset:48 sc1\n" \
"s_mov_b64 exec, -1\n" \
"v_mfma_f32_16x16x32_bf16 v[168:171], a[216:219], v[64:67], v[168:171]\n" \
"v_mfma_f32_16x16x32_bf16 v[172:175], a[216:219], v[72:75], v[172:175]\n" \
"v_mfma_f32_16x16x32_bf16 v[240:243], a[216:219], v[80:83], v[240:243]\n" \
"v_mfma_f32_16x16x32_bf16 v[244:247], a[216:219], v[88:91], v[244:247]\n" \
"buffer_load_dwordx4 v[120:123], v20, s[56:59], s70 offen offset:0 sc1\n" \
"v_mfma_f32_16x16x32_bf16 v[168:171], a[0:3], v[68:71], v[168:171]\n" \
"v_mfma_f32_16x16x32_bf16 v[172:175], a[0:3], v[76:79], v[172:175]\n" \
"v_mfma_f32_16x16x32_bf16 v[240:243], a[0:3], v[84:87], v[240:243]\n" \
"v_mfma_f32_16x16x32_bf16 v[244:247], a[0:3], v[92:95], v[244:247]\n" \
"buffer_load_dwordx4 v[192:195], v20, s[56:59], s70 offen offset:2048 sc1\n" \
"s_add_u32 s70, s70, 0x1000\n" \
"v_mfma_f32_16x16x32_bf16 v[56:59], a[4:7], v[96:99], 0\n" \
"v_mfma_f32_16x16x32_bf16 v[60:63], a[4:7], v[104:107], 0\n" \
"v_mfma_f32_16x16x32_bf16 v[56:59], a[8:11], v[100:103], v[56:59]\n" \
"v_mfma_f32_16x16x32_bf16 v[60:63], a[8:11], v[108:111], v[60:63]\n" \
"buffer_load_dwordx4 v[128:131], v20, s[56:59], s70 offen offset:0 sc1\n" \
"buffer_load_dwordx4 v[200:203], v20, s[56:59], s70 offen offset:2048 sc1\n" \
".fill (L_ab + 45056 - .) / 4, 4, 0xBF800000\n" \
"L_ap11:\n" \
"s_cbranch_execz L_ap0\n" \
"s_add_u32 s70, s70, 0x1000\n" \
"buffer_load_dwordx4 v[176:179], v20, s[56:59], s70 offen offset:0 sc1\n" \
"buffer_load_dwordx4 v[248:251], v20, s[56:59], s70 offen offset:2048 sc1\n" \
"s_waitcnt vmcnt(15)\n" \
"v_mfma_f32_16x16x32_bf16 v[168:171], v[50:53], v[116:119], v[168:171]\n" \
"s_mov_b64 exec, 1\n" \
"buffer_store_dword v39, off, s[56:59], s71 offset:52 sc1\n" \
"s_mov_b64 exec, -1\n" \
"s_mov_b64 exec, 1\n" \
"buffer_store_dword v39, off, s[56:59], s72 offset:56 sc1\n" \
"s_mov_b64 exec, -1\n" \
"v_cmp_eq_u32_e64 s[98:99], s62, v37\n" \
"v_mfma_f32_16x16x32_bf16 v[172:175], v[48:51], v[116:119], v[172:175]\n" \
"s_andn2_b32 s75, 0x100, s98\n" \
"s_cbranch_scc1 L_sx1_%=\n" \
"L_rx1_%=:\n" \
"s_add_u32 s73, s63, 0x0\n" \
"s_waitcnt vmcnt(16)\n" \
"v_mfma_f32_16x16x32_bf16 v[168:171], v[50:53], v[124:127], v[168:171]\n" \
"v_mfma_f32_16x16x32_bf16 v[172:175], v[48:51], v[124:127], v[172:175]\n" \
"buffer_load_dwordx4 v[188:191], v20, s[56:59], s73 offen offset:1024 sc1\n" \
"s_andn2_b32 s75, 0x200, s98\n" \
"s_cbranch_scc1 L_sx2_%=\n" \
"L_rx2_%=:\n" \
"s_add_u32 s92, s63, 0x800\n" \
"s_waitcnt vmcnt(16)\n" \
"v_mfma_f32_16x16x32_bf16 v[168:171], v[50:53], v[132:135], v[168:171]\n" \
"v_mfma_f32_16x16x32_bf16 v[172:175], v[48:51], v[132:135], v[172:175]\n" \
"buffer_load_dwordx4 v[196:199], v20, s[56:59], s92 offen offset:1024 sc1\n" \
"s_andn2_b32 s75, 0x400, s98\n" \
"s_cbranch_scc1 L_sx3_%=\n" \
"L_rx3_%=:\n" \
"s_add_u32 s93, s63, 0x1000\n" \
"s_waitcnt vmcnt(16)\n" \
"v_mfma_f32_16x16x32_bf16 v[168:171], v[50:53], v[140:143], v[168:171]\n" \
"v_mfma_f32_16x16x32_bf16 v[172:175], v[48:51], v[140:143], v[172:175]\n" \
"buffer_load_dwordx4 v[204:207], v20, s[56:59], s93 offen offset:1024 sc1\n" \
"s_andn2_b32 s75, 0x800, s98\n" \
"s_cbranch_scc1 L_sx4_%=\n" \
"L_rx4_%=:\n" \
"s_add_u32 s94, s63, 0x1800\n" \
"s_waitcnt vmcnt(16)\n" \
"v_mfma_f32_16x16x32_bf16 v[168:171], v[50:53], v[148:151], v[168:171]\n" \
"v_mfma_f32_16x16x32_bf16 v[172:175], v[48:51], v[148:151], v[172:175]\n" \
"buffer_load_dwordx4 v[212:215], v20, s[56:59], s94 offen offset:1024 sc1\n" \
"s_andn2_b32 s75, 0x1000, s98\n" \
"s_cbranch_scc1 L_sx5_%=\n" \
"L_rx5_%=:\n" \
"s_add_u32 s95, s63, 0x2000\n" \
"s_waitcnt vmcnt(16)\n" \
"v_mfma_f32_16x16x32_bf16 v[168:171], v[50:53], v[156:159], v[168:171]\n" \
"v_mfma_f32_16x16x32_bf16 v[172:175], v[48:51], v[156:159], v[172:175]\n" \
"buffer_load_dwordx4 v[220:223], v20, s[56:59], s95 offen offset:1024 sc1\n" \
"s_andn2_b32 s75, 0x2000, s98\n" \
"s_cbranch_scc1 L_sx6_%=\n" \
"L_rx6_%=:\n" \
"s_add_u32 s96, s63, 0x2800\n" \
"s_waitcnt vmcnt(16)\n" \
"v_mfma_f32_16x16x32_bf16 v[168:171], v[50:53], v[164:167], v[168:171]\n" \
"v_mfma_f32_16x16x32_bf16 v[172:175], v[48:51], v[164:167], v[172:175]\n" \
"buffer_load_dwordx4 v[228:231], v20, s[56:59], s96 offen offset:1024 sc1\n" \
"s_andn2_b32 s75, 0x4000, s98\n" \
"s_cbranch_scc1 L_sx7_%=\n" \
"L_rx7_%=:\n" \
"s_add_u32 s97, s63, 0x3000\n" \
"s_waitcnt vmcnt(16)\n" \
"v_mfma_f32_16x16x32_bf16 v[56:59], v[50:53], v[112:115], v[56:59]\n" \
"v_mfma_f32_16x16x32_bf16 v[60:63], v[48:51], v[112:115], v[60:63]\n" \
"buffer_load_dwordx4 v[236:239], v20, s[56:59], s97 offen offset:1024 sc1\n" \
"v_cvt_pk_bf16_f32 v168, v168, v169\n" \
"v_cvt_pk_bf16_f32 v172, v172, v173\n" \
"v_cvt_pk_bf16_f32 v169, v170, v171\n" \
"v_cvt_pk_bf16_f32 v173, v174, v175\n" \
"buffer_store_dwordx2 v[168:169], v42, s[88:91], s66 offen sc1\n" \
"buffer_store_dwordx2 v[172:173], v43, s[88:91], s66 offen sc1\n" \
"s_waitcnt vmcnt(18)\n" \
"v_mfma_f32_16x16x32_bf16 v[56:59], v[50:53], v[184:187], v[56:59]\n" \
"v_mfma_f32_16x16x32_bf16 v[60:63], v[48:51], v[184:187], v[60:63]\n" \
"s_waitcnt vmcnt(16)\n" \
"v_mfma_f32_16x16x32_bf16 v[56:59], v[50:53], v[120:123], v[56:59]\n" \
"v_mfma_f32_16x16x32_bf16 v[60:63], v[48:51], v[120:123], v[60:63]\n" \
"s_waitcnt vmcnt(15)\n" \
"v_mfma_f32_16x16x32_bf16 v[56:59], v[50:53], v[192:195], v[56:59]\n" \
"v_mfma_f32_16x16x32_bf16 v[60:63], v[48:51], v[192:195], v[60:63]\n" \
"s_waitcnt vmcnt(14)\n" \
"v_mfma_f32_16x16x32_bf16 v[56:59], v[50:53], v[128:131], v[56:59]\n" \
"v_mfma_f32_16x16x32_bf16 v[60:63], v[48:51], v[128:131], v[60:63]\n" \
"s_waitcnt vmcnt(13)\n" \
"v_mfma_f32_16x16x32_bf16 v[56:59], v[50:53], v[200:203], v[56:59]\n" \
"v_mfma_f32_16x16x32_bf16 v[60:63], v[48:51], v[200:203], v[60:63]\n" \
"s_waitcnt vmcnt(12)\n" \
"v_mfma_f32_16x16x32_bf16 v[56:59], v[50:53], v[176:179], v[56:59]\n" \
"v_mfma_f32_16x16x32_bf16 v[60:63], v[48:51], v[176:179], v[60:63]\n" \
"s_waitcnt vmcnt(11)\n" \
"v_mfma_f32_16x16x32_bf16 v[56:59], v[50:53], v[248:251], v[56:59]\n" \
"v_mfma_f32_16x16x32_bf16 v[60:63], v[48:51], v[248:251], v[60:63]\n" \
"s_mov_b64 exec, 1\n" \
"buffer_store_dword v46, off, s[56:59], s64 offset:0 sc1\n" \
"s_mov_b64 exec, -1\n" \
"s_waitcnt vmcnt(9)\n" \
"v_mfma_f32_16x16x32_bf16 v[240:243], v[50:53], v[188:191], v[240:243]\n" \
"v_mfma_f32_16x16x32_bf16 v[244:247], v[48:51], v[188:191], v[244:247]\n" \
"s_waitcnt vmcnt(8)\n" \
"v_mfma_f32_16x16x32_bf16 v[240:243], v[50:53], v[196:199], v[240:243]\n" \
"v_cvt_pk_bf16_f32 v56, v56, v57\n" \
"v_cvt_pk_bf16_f32 v60, v60, v61\n" \
"v_mfma_f32_16x16x32_bf16 v[244:247], v[48:51], v[196:199], v[244:247]\n" \
"v_cvt_pk_bf16_f32 v57, v58, v59\n" \
"v_cvt_pk_bf16_f32 v61, v62, v63\n" \
"buffer_store_dwordx2 v[56:57], v42, s[88:91], s65 offen sc1\n" \
"buffer_store_dwordx2 v[60:61], v43, s[88:91], s65 offen sc1\n" \
"s_waitcnt vmcnt(9)\n" \
"v_mfma_f32_16x16x32_bf16 v[240:243], v[50:53], v[204:207], v[240:243]\n" \
"v_mfma_f32_16x16x32_bf16 v[244:247], v[48:51], v[204:207], v[244:247]\n" \
"s_waitcnt vmcnt(8)\n" \
"v_mfma_f32_16x16x32_bf16 v[240:243], v[50:53], v[212:215], v[240:243]\n" \
"v_mfma_f32_16x16x32_bf16 v[244:247], v[48:51], v[212:215], v[244:247]\n" \
"s_waitcnt vmcnt(7)\n" \
"v_mfma_f32_16x16x32_bf16 v[240:243], v[50:53], v[220:223], v[240:243]\n" \
"v_mfma_f32_16x16x32_bf16 v[244:247], v[48:51], v[220:223], v[244:247]\n" \
"s_waitcnt vmcnt(6)\n" \
"v_mfma_f32_16x16x32_bf16 v[240:243], v[50:53], v[228:231], v[240:243]\n" \
"v_mfma_f32_16x16x32_bf16 v[244:247], v[48:51], v[228:231], v[244:247]\n" \
"s_waitcnt vmcnt(5)\n" \
"v_mfma_f32_16x16x32_bf16 v[240:243], v[50:53], v[236:239], v[240:243]\n" \
"v_mfma_f32_16x16x32_bf16 v[244:247], v[48:51], v[236:239], v[244:247]\n" \
"s_nop 6\n" \
"v_cvt_pk_bf16_f32 v240, v240, v241\n" \
"v_cvt_pk_bf16_f32 v244, v244, v245\n" \
"v_cvt_pk_bf16_f32 v241, v242, v243\n" \
"v_cvt_pk_bf16_f32 v245, v246, v247\n" \
"buffer_store_dwordx2 v[240:241], v42, s[88:91], s67 offen sc1\n" \
"buffer_store_dwordx2 v[244:245], v43, s[88:91], s67 offen sc1\n"
#define ATT_VCLOB "v21","v22","v23","v24","v25","v26","v27","v28","v29","v32","v33","v34","v35","v36","v37","v38","v39","v40","v41","v42","v43","v44","v45","v46","v47","v48","v49","v50","v51","v52","v53","v54","v55","v56","v57","v58","v59","v60","v61","v62","v63","v64","v65","v66","v67","v68","v69","v70","v71","v72","v73","v74","v75","v76","v77","v78","v79","v80","v81","v82","v83","v84","v85","v86","v87","v88","v89","v90","v91","v92","v93","v94","v95","v96","v97","v98","v99","v100","v101","v102","v103","v104","v105","v106","v107","v108","v109","v110","v111","v112","v113","v114","v115","v116","v117","v118","v119","v120","v121","v122","v123","v124","v125","v126","v127","v128","v129","v130","v131","v132","v133","v134","v135","v136","v137","v138","v139","v140","v141","v142","v143","v144","v145","v146","v147","v148","v149","v150","v151","v152","v153","v154","v155","v156","v157","v158","v159","v160","v161","v162","v163","v164","v165","v166","v167","v168","v169","v170","v171","v172","v173","v174","v175","v176","v177","v178","v179","v180","v181","v182","v183","v184","v185","v186","v187","v188","v189","v190","v191","v192","v193","v194","v195","v196","v197","v198","v199","v200","v201","v202","v203","v204","v205","v206","v207","v208","v209","v210","v211","v212","v213","v214","v215","v216","v217","v218","v219","v220","v221","v222","v223","v224","v225","v226","v227","v228","v229","v230","v231","v232","v233","v234","v235","v236","v237","v238","v239","v240","v241","v242","v243","v244","v245","v246","v247","v248","v249","v250","v251","v252","v253","v254","v255"
#define ATT_SCLOB "s63","s64","s65","s66","s67","s68","s69","s70","s71","s72","s73","s74","s75","s76","s77","s78","s79","s80","s81","s82","s83","s84","s85","s86","s87","s88","s89","s90","s91","s92","s93","s95","s96","s97","s98","s99","s100","s101"
#define ATT_WTCH 0
#define ATT_PEEL 1
#define ATT_PEEL_FOW 0
enum { T_fo, T_fj, T_fa, T_fas, T_fb, T_fbs, T_f8, T_mk8, T_wt, T_wol, T_woh, T_wbl, T_wbh, T_outl, T_outh, T_r1l, T_r1h, T_r1n, T_r1s, T_m0, T_m1, T_m1b, T_mw, T_m3, T_m4, T_ml, T_w0, T_w1, T_w2, T_w3, T_A1, T_A2, T_A3, T_B1, T_B2, T_B3, T_Ao, T_Bo, T_Al, T_Bl, T_l8, T_xr, T_x8r, T_oA, T_oB, T_o8, T_x0, T_x1, T_x2, T_x3, T_x4, T_x5, T_x6, T_x8, T_ep };

__device__ __forceinline__ void out_consts(u32x4 (&vp)[3], const u16* wo, const u16* wob, const u16* out, const unsigned b, const unsigned w, const unsigned lane) {
    const unsigned sp = b & 7u, nt = (b >> 3) & 3u, j = b >> 5, head = 4u * sp + nt, OOR = 0x40000000u;
    unsigned vw[9];
#pragma unroll
    for (unsigned i = 0; i < 9u; ++i) {
        const unsigned q = 64u * i + lane, row = __umul24(q, 3641u) >> 16, c = q - __umul24(row, 18u);
        vw[i] = __umul24(row, DM * 2) + (c < 16u ? (c * 16u + sp * 32u + 32u) & 255u : c * 16u) - (i & 3u) * 1024u;
    }
    const unsigned row = 16u * w + (lane >> 2), r9 = __umul24(16u * (row >> 5) + (row & 12u) + 2u * ((row >> 4) & 1u) + ((row >> 1) & 1u), 18u) + 16u + (row & 1u);
    unsigned vwl = __umul24(r9, DM * 2) + ((lane & 2u) ? 256u : sp * 32u) + (lane & 1u) * 16u, vrd = __umul24(lane >> 2, 288u) + (lane & 3u) * 8u;
    unsigned tab = 0;
#define TB(n, v) asm("v_writelane_b32 %0, %1, %2" : "+v"(tab) : "s"((unsigned)(v)), "n"((int)T_##n))
    const unsigned FJ = (unsigned)WS_F + (F_J + 640u * j) * 4u, mt = 2u * (w & 2u) + (w & 1u), w2 = w < 2u ? w : 2u, hw = head * 147456u;
    const unsigned FR = (unsigned)WS_F + F_R * 4u + (8u * j + sp) * 1024u;
    TB(fo, FJ + (ATT_PEEL_FOW ? 16u * head + 4u * w : 4u * head)); TB(fj, FJ); TB(fa, FR + (w | 2u) * 256u); TB(fas, FR + w * 256u + nt * 4u);
    TB(fb, FR + 128u + w * 256u); TB(fbs, FR + 128u + w * 256u + nt * 4u);
    TB(ep, ssel(b + w, OOR, (unsigned)WS_F + F_EPOCH * 4u));
    const u16* pwo = wo + nt * 144u; const u16* pwb = wob + nt * 144u;
    TB(wol, PLO(pwo)); TB(woh, PHI(pwo) & 0xffffu); TB(wbl, PLO(pwb)); TB(wbh, PHI(pwb) & 0xffffu); TB(outl, PLO(out)); TB(outh, PHI(out) & 0xffffu); TB(r1l, sslt<3>(w, PLO(pwo), PLO(pwb))); TB(r1h, sslt<3>(w, PHI(pwo), PHI(pwb)) & 0xffffu);
    TB(r1n, sslt<3>(w, 72u * sp + 71u, 4095u) * 1152u + 288u); TB(r1s, sslt<3>(w, (72u * sp + 32u * w) * 1152u, hw + 110592u));
    TB(m0, 131072u + 9216u * w2); TB(w0, hw + 36864u * w2); TB(m1, ATT_PEEL ? sslt<3>(w, 41984u + 9216u * w, 32768u) : 24576u + 9216u * w); TB(m1b, ssel(w ^ 2u, sslt<3>(w, 41984u + 9216u * w, 32768u), 114688u - 4096u)); TB(mw, 65536u + 9216u * w); TB(m3, sslt<3>(w, 102400u + 9216u * w, 0u)); TB(m4, sslt<3>(w, 131072u + 9216u * w, 9216u)); TB(ml, (ATT_PEEL ? 18432u : 61440u) + 1024u * w);
    const unsigned tA = (unsigned)WS_AF + (8u * j + mt) * (KS_TOT * 1024u), tB = (unsigned)WS_AF + (120u - 8u * j + mt) * (KS_TOT * 1024u);
#define HK(k) (4u * sp + ((nt + k) & 3u))
    TB(w1, HK(1u) * 147456u + w * 36864u); TB(w2, HK(2u) * 147456u + w * 36864u); TB(w3, HK(3u) * 147456u + w * 36864u);
    TB(A1, tA + 18432u + HK(1u) * 4096u); TB(A2, tA + 18432u + HK(2u) * 4096u); TB(A3, tA + 18432u + HK(3u) * 4096u);
    TB(B1, tB + 18432u + HK(1u) * 4096u); TB(B2, tB + 18432u + HK(2u) * 4096u); TB(B3, tB + 18432u + HK(3u) * 4096u);
    TB(Ao, tA + sp * 2048u); TB(Bo, tB + sp * 2048u); TB(Al, tA + 16384u); TB(Bl, tB + 16384u);
    const unsigned nred = w ^ (sp >> 1), blk = sp & 1u;
    const unsigned xg = (unsigned)WS_X + (4u * j + nt) * 589824u + w * 16384u, oA = (128u * j + 16u * mt) * 1152u + nt * 288u, oB = (1920u - 128u * j + 16u * mt) * 1152u + nt * 288u;
    TB(l8, ssel(nred, OOR, ssel(blk, tB, tA) + 16384u)); TB(xr, xg + sp * 65536u); TB(x8r, ssel(nred, OOR, xg + 524288u + (sp & 1u) * 1024u));
    TB(oA, oA + sp * 32u); TB(oB, oB + sp * 32u); TB(o8, ssel(nred, OOR, ssel(blk, oB, oA) + 256u)); TB(x8, xg + 524288u + sp * 2048u);
    TB(f8, xg + w * 131072u + 14400u + sp * 4u); TB(mk8, ssel(nred, 0u, 0xff0000u)); TB(wt, hw + j * 18432u + w * 4608u);
#define XI(i) TB(x##i, xg + ((sp + 1u + i) & 7u) * 65536u + i * 2048u)
    XI(0); XI(1); XI(2); XI(3); XI(4); XI(5); XI(6);
#undef XI
#undef HK
#undef TB
    vp[0] = (u32x4){vw[0], vw[1], vw[2], vw[3]}; vp[1] = (u32x4){vw[4], vw[5], vw[6], vw[7]}; vp[2] = (u32x4){vw[8], vwl, vrd, tab};
}
__global__ __launch_bounds__(256, 1) __attribute__((aligned(4096))) void fused_kernel(const u16* __restrict__ x, const u16* __restrict__ ckv, const u16* __restrict__ wq, const u16* __restrict__ wuk,
                                                       const u16* __restrict__ wuv, const u16* __restrict__ wqkv, const u16* __restrict__ gw,
                                                       const u16* __restrict__ wo, const u16* __restrict__ wob, char* __restrict__ ws_, u16* __restrict__ out) {
    using namespace att;
    extern __shared__ __attribute__((aligned(16))) char lds[];
    u32x4 sa[5]; u32x2 s24, s28; char* ws; unsigned tdum; kaptr ka = (kaptr)__builtin_amdgcn_kernarg_segment_ptr();
    asm volatile("s_load_dwordx8 s[8:15], %[ka], 0x0\n\ts_load_dwordx8 s[16:23], %[ka], 0x20\n\ts_getpc_b64 s[4:5]\n\tv_lshlrev_b32_e32 v191, 7, %[t]\n\ts_bfe_u32 s6, %[b], 0x20003\n\t"
                 "v_lshl_add_u32 v191, s6, 15, v191\n\tv_min_u32_e32 v191, %[e], v191\n\tglobal_load_dword v191, v191, s[4:5]\n\ts_load_dwordx4 s[24:27], %[ka], 0x40\n\ts_load_dwordx2 s[28:29], %[ka], 0x50"
                 : "={s[8:11]}"(sa[0]), "={s[12:15]}"(sa[1]), "={s[16:19]}"(sa[2]), "={s[20:23]}"(sa[3]), "={s[24:25]}"(s24), "={s[26:27]}"(ws), "={s[28:29]}"(s28), "={v191}"(tdum), [ka] "+{s[0:1]}"(ka)
                 : [t] "v"(threadIdx.x), [b] "s"(blockIdx.x), [e] "i"(CT_END) : "s4", "s5", "s6", "scc");
    SBAR();
    const unsigned wid = rfl(threadIdx.x >> 6);
    unsigned tag, vl;
    const int b = blockIdx.x;
    u32x4 vp[5], srdA; u32x2 sd;
    proj_phase(ka, ws, wid, vp, vl, sa, s24, s28, tdum, srdA, sd, tag
    );
    unsigned* const flags = (unsigned*)(ws + WS_F);
    RTS(2);
#define AST(L) do {} while (0)
#define ATT_TAIL "s_endpgm\n"
    unsigned abad;
    asm volatile(ATT_ASM ATT_TAIL "L_end_%=:\n s_nop 1"
                 : "={s94}"(abad)
                 : "{v[0:3]}"(vp[0]), "{v[4:7]}"(vp[1]), "{v[8:11]}"(vp[2]), "{v[12:15]}"(vp[3]), "{v[16:19]}"(vp[4]), "{v20}"(vl), "{s[56:59]}"(srdA), "{s[60:61]}"(sd), "{s62}"(tag),
                   "{s[8:11]}"(sa[0]), "{s[12:15]}"(sa[1]), "{s[16:19]}"(sa[2]), "{s[20:23]}"(sa[3]), "{s24}"(sa[4][0]), "{s3}"(wid), "{s[28:29]}"(s28)
                 : "memory", "m0", "vcc", "scc", ATT_VCLOB, ATT_SCLOB, ACLOB_LO, ACLOB_HI);
    {
        unsigned b2 = b, w2 = wid;
        asm volatile("s_waitcnt vmcnt(0)" : "+s"(ka), "+s"(b2), "+s"(w2), "+s"(ws) :: "memory");
        const unsigned j2 = b2 >> 5, p2 = w2 & 1u, h2 = (b2 & 7u) * 4u + ((b2 >> 3) & 3u);
#pragma unroll 1
        for (unsigned i = w2 & 2u; i < ((w2 & 2u) ? (abad ? 8u : 4u) : 2u); ++i)
            att_exact(ka[0], ka[2], ws, h2, (i < 4u ? 1920u - 128u * j2 : 128u * j2 - 128u) + 32u * i + 16u * p2, *reinterpret_cast<const float*>(lds + LDS_G + (32u * i + 16u * p2 + (lane_id() & 15u)) * 4u));
        asm volatile("s_waitcnt vmcnt(0)" ::: "memory");
        unsigned* fa = (unsigned*)(ws + WS_F) + F_R + (8u * j2 + (b2 & 7u)) * 256u + (2u + p2) * 64u + ((b2 >> 3) & 3u);
        if (w2 & 2u) st_flag(fa, tag);
        wait_flags(fa, ld_flag(fa), tag);
        const unsigned l2 = lane_id(); u32x4 c[3]; out_consts(c, ka[7], ka[8], ka[10], b2, w2, l2);
        asm volatile("s_waitcnt vmcnt(0) lgkmcnt(0)\n\ts_nop 7\n\ts_branch L_att_stub" :: "{v[0:3]}"(c[0]), "{v[4:7]}"(c[1]), "{v[8:11]}"(c[2]), "{v20}"(l2 * 16u), "{s[56:59]}"(SRDA), "{s60}"(SD(8u * j2, h2, w2)),
                     "{s61}"(SD(120u - 8u * j2, h2, w2)), "{s62}"(tag) : "memory");
    }
    RTS(5);
}

extern "C" void kernel_launch(void* const* d_in, const int* in_sizes, int n_in, void* d_out, int out_size, void* d_ws, size_t ws_size,
                              hipStream_t stream) {
    (void)hipFuncSetAttribute((const void*)fused_kernel, hipFuncAttributeMaxDynamicSharedMemorySize, att::LDS_BYTES);
    fused_kernel<<<dim3(256), 256, att::LDS_BYTES, stream>>>((const u16*)d_in[0], (const u16*)d_in[4], (const u16*)d_in[5], (const u16*)d_in[6], (const u16*)d_in[7], (const u16*)d_in[1],
                                                             (const u16*)d_in[9], (const u16*)d_in[2], (const u16*)d_in[8], (char*)d_ws, (u16*)d_out);
}
```

```cpp
#include <hip/hip_runtime.h>
#include <hip/hip_bf16.h>
#include <stdint.h>
#include <utility>

typedef short bf16x8 __attribute__((ext_vector_type(8)));
typedef short s16x4 __attribute__((ext_vector_type(4)));
typedef float f32x16 __attribute__((ext_vector_type(16)));
typedef float f32x4 __attribute__((ext_vector_type(4)));
typedef unsigned u32x4 __attribute__((ext_vector_type(4)));
typedef unsigned u32x2 __attribute__((ext_vector_type(2)));
typedef unsigned short u16;

constexpr int S = 2048, DM = 576, NH = 32, HD = 128, DC = 512, NQKV = 1728, DHA = 18;
constexpr int NBIG = NH * HD;
constexpr int KS_TOT = 146;

#define SBAR() __builtin_amdgcn_sched_barrier(0)
constexpr size_t WS_Q = 0, WS_K = WS_Q + (size_t)S * NBIG * 2, WS_V = WS_K + (size_t)S * NBIG * 2, WS_AF = WS_V + (size_t)S * NBIG * 2, WS_X = WS_AF + (size_t)128 * KS_TOT * 1024,
                 WS_P = WS_X + (size_t)64 * 16 * 9216, WS_F = WS_P + (size_t)128 * (4 * 32 * 256 + 1024) * 4;
constexpr size_t WS_L = (size_t)96 << 20;
static_assert(WS_F == 96206848, "workspace map is baked into the generated asm");
constexpr int F_EPOCH = 0, F_J = 1024, F_ST = 8192, F_KV = 40960, F_R = 65536;
constexpr unsigned TAG_STEP = 0x9E3779B1u;
__device__ __forceinline__ unsigned ld_flag(const unsigned* p) { return __hip_atomic_load(p, __ATOMIC_RELAXED, __HIP_MEMORY_SCOPE_AGENT); }
__device__ __forceinline__ void st_flag(unsigned* p, unsigned v) { __hip_atomic_store(p, v, __ATOMIC_RELAXED, __HIP_MEMORY_SCOPE_AGENT); }
__device__ __forceinline__ unsigned ld_flag_async(const unsigned* p) { unsigned v; asm volatile("global_load_dword %0, %1, off sc1" : "=v"(v) : "v"(p) : "memory"); return v; }
#define WAITV(v, N) asm volatile("s_waitcnt vmcnt(" #N ")" : "+v"(v) :: "memory")
__device__ __forceinline__ void wait_flags(const unsigned* p, unsigned v, unsigned tag) {
    if (__builtin_expect(__builtin_amdgcn_ballot_w64(v != tag) != 0ull, 0)) {
        unsigned spins = 0;
        do { __builtin_amdgcn_s_sleep(1); } while (__builtin_amdgcn_ballot_w64(ld_flag(p) != tag) != 0ull && ++spins < (1u << 22));
    }
    asm volatile("" ::: "memory");
}

__device__ __forceinline__ unsigned cvtpk(float lo, float hi) {
    unsigned r; asm volatile("v_cvt_pk_bf16_f32 %0, %1, %2" : "=v"(r) : "v"(lo), "v"(hi)); return r;
}
__device__ __forceinline__ float bf2f(u16 v) { return __uint_as_float(((unsigned)v) << 16); }
__device__ __forceinline__ int crow(int r, int hi) { return (r & 3) + 8 * (r >> 2) + 4 * hi; }

typedef __attribute__((address_space(3))) s16x4* lds_s16x4_p;
__device__ __forceinline__ s16x4 trrd(unsigned addr) { return __builtin_amdgcn_ds_read_tr16_b64_v4i16((lds_s16x4_p)addr); }

__device__ __forceinline__ unsigned lane_id() { unsigned l; asm volatile("v_mbcnt_lo_u32_b32 %0, -1, 0\n\tv_mbcnt_hi_u32_b32 %0, -1, %0" : "=v"(l)); return l; }
#define RTS(slot) do {} while (0)
#define CT_END 0x1f300
#define ACLOB_LO "a0","a1","a2","a3","a4","a5","a6","a7","a8","a9","a10","a11","a12","a13","a14","a15","a16","a17","a18","a19","a20","a21","a22","a23","a24","a25","a26","a27","a28","a29","a30","a31","a32","a33","a34","a35","a36","a37","a38","a39","a40","a41","a42","a43","a44","a45","a46","a47","a48","a49","a50","a51","a52","a53","a54","a55","a56","a57","a58","a59","a60","a61","a62","a63","a64","a65","a66","a67","a68","a69","a70","a71","a72","a73","a74","a75","a76","a77","a78","a79","a80","a81","a82","a83","a84","a85","a86","a87","a88","a89","a90","a91","a92","a93","a94","a95","a96","a97","a98","a99","a100","a101","a102","a103","a104","a105","a106","a107","a108","a109","a110","a111","a112","a113","a114","a115","a116","a117","a118","a119","a120","a121","a122","a123","a124","a125","a126","a127"
#define ACLOB_HI "a128","a129","a130","a131","a132","a133","a134","a135","a136","a137","a138","a139","a140","a141","a142","a143","a144","a145","a146","a147","a148","a149","a150","a151","a152","a153","a154","a155","a156","a157","a158","a159","a160","a161","a162","a163","a164","a165","a166","a167","a168","a169","a170","a171","a172","a173","a174","a175","a176","a177","a178","a179","a180","a181","a182","a183","a184","a185","a186","a187","a188","a189","a190","a191","a192","a193","a194","a195","a196","a197","a198","a199","a200","a201","a202","a203","a204","a205","a206","a207","a208","a209","a210","a211","a212","a213","a214","a215","a216","a217","a218","a219","a220","a221","a222","a223","a224","a225","a226","a227","a228","a229","a230","a231","a232","a233","a234","a235","a236","a237","a238","a239","a240","a241","a242","a243","a244","a245","a246","a247","a248","a249","a250","a251","a252","a253","a254","a255"
#define PLO(p) ((unsigned)(size_t)(p))
#define PHI(p) ((unsigned)((size_t)(p) >> 32))
__device__ __forceinline__ unsigned vsel(bool c, unsigned a, unsigned b) { asm("" : "+v"(a), "+v"(b)); return c ? a : b; }
__device__ __forceinline__ unsigned ssel(unsigned c, unsigned a, unsigned b) { unsigned r; asm("s_cmp_lg_u32 %1, 0\n\ts_cselect_b32 %0, %2, %3" : "=s"(r) : "s"(c), "s"(a), "s"(b) : "scc"); return r; }
template <int N> __device__ __forceinline__ unsigned sslt(unsigned c, unsigned a, unsigned b) { unsigned r; asm("s_cmpk_lt_u32 %1, %4\n\ts_cselect_b32 %0, %2, %3" : "=s"(r) : "s"(c), "s"(a), "s"(b), "n"(N) : "scc"); return r; }
namespace att {
constexpr int LDS_BYTES = 163840;
constexpr unsigned LDS_G = 159744u, LDS_L = 161792u;
}
#define SD(J8, H, W) ((unsigned)WS_AF + (((J8) + 2u * ((W) & 2u) + ((W) & 1u)) * KS_TOT + 18u + 4u * (H)) * 1024u)
#define SRDA (u32x4){PLO(ws), PHI(ws) & 0xffffu, (unsigned)WS_L, 0x00020000u}
#define PROJ_ASM \
".p2align 3\n" \
"L_pb:\n" \
"s_waitcnt lgkmcnt(0)\n" \
"s_mov_b32 s64, s10\n" \
"s_and_b32 s65, s11, 0xffff\n" \
"s_mov_b32 s66, 0x200000\n" \
"s_mov_b32 s67, 0x20000\n" \
"s_mov_b32 s68, s14\n" \
"s_and_b32 s69, s15, 0xffff\n" \
"s_mov_b32 s70, 0x400000\n" \
"s_mov_b32 s71, 0x20000\n" \
"s_mov_b32 s72, s16\n" \
"s_and_b32 s73, s17, 0xffff\n" \
"s_mov_b32 s74, 0x400000\n" \
"s_mov_b32 s75, 0x20000\n" \
"s_add_u32 m0, s30, 0x0\n" \
"s_movk_i32 s84, 0x0\n" \
"buffer_load_dwordx4 v22, s[64:67], s84 offen offset:0 lds\n" \
"s_movk_i32 s84, 0x1c00\n" \
"buffer_load_dwordx4 v23, s[64:67], s84 offen offset:1024 lds\n" \
"s_movk_i32 s84, 0x3800\n" \
"buffer_load_dwordx4 v22, s[64:67], s84 offen offset:2048 lds\n" \
"s_movk_i32 s84, 0x5400\n" \
"buffer_load_dwordx4 v23, s[64:67], s84 offen offset:3072 lds\n" \
"s_add_u32 m0, s31, 0x0\n" \
"s_movk_i32 s84, 0x0\n" \
"buffer_load_dwordx4 v26, s[68:71], s84 offen offset:0 lds\n" \
"s_movk_i32 s84, 0x7c00\n" \
"buffer_load_dwordx4 v26, s[68:71], s84 offen offset:1024 lds\n" \
"s_addk_i32 s84, 0x7c00\n" \
"buffer_load_dwordx4 v27, s[68:71], s84 offen offset:2048 lds\n" \
"s_addk_i32 s84, 0x7c00\n" \
"buffer_load_dwordx4 v27, s[68:71], s84 offen offset:3072 lds\n" \
"s_movk_i32 s7, 0x300\n" \
"s_lshr_b32 s4, s2, 1\n" \
"s_and_b32 s4, s4, 60\n" \
"s_or_b32 s4, s4, s3\n" \
"v_lshrrev_b32_e32 v212, 4, v20\n" \
"v_min_u32_e32 v212, 59, v212\n" \
"s_cmpk_lt_u32 s2, 0x80\n" \
"s_cselect_b32 s6, s14, s16\n" \
"v_subrev_u32_e32 v214, 48, v212\n" \
"v_mov_b32_e32 v212, s10\n" \
"v_mov_b32_e32 v215, s6\n" \
"s_cmpk_lt_u32 s2, 0x80\n" \
"s_cselect_b32 s5, s15, s17\n" \
"v_cmp_gt_u32_e32 vcc, s7, v20\n" \
"v_mov_b32_e64 v213, s11\n" \
"v_bfe_u32 v216, v20, 4, 3\n" \
"v_cndmask_b32_e32 v212, v212, v215, vcc\n" \
"v_mov_b32_e32 v215, s5\n" \
"s_lshl_b32 s5, s3, 4\n" \
"v_cndmask_b32_e32 v213, v213, v215, vcc\n" \
"v_lshlrev_b32_e64 v215, 5, v20\n" \
"v_and_b32_e32 v215, 0x7000, v215\n" \
"v_lshl_add_u32 v215, s4, 6, v215\n" \
"s_lshl_b32 s4, s2, 3\n" \
"s_and_b32 s4, s4, 56\n" \
"v_or3_b32 v215, v215, s4, v216\n" \
"s_lshl_b32 s4, s2, 6\n" \
"s_or_b32 s6, s5, s4\n" \
"v_cmp_gt_u32_e64 s[4:5], 6, v214\n" \
"v_add_u32_e32 v215, 0x2000, v215\n" \
"s_nop 0\n" \
"v_cndmask_b32_e64 v216, 4, 2, s[4:5]\n" \
"v_add3_u32 v214, s6, v214, v216\n" \
"s_lshl_b32 s4, s3, 9\n" \
"v_cndmask_b32_e32 v214, v214, v215, vcc\n" \
"v_lshlrev_b32_e32 v214, 7, v214\n" \
"v_mov_b32_e64 v215, 0\n" \
"v_lshl_add_u64 v[184:185], v[212:213], 0, v[214:215]\n" \
"s_or_b32 s32, s4, 0x25000\n" \
"s_add_u32 m0, s30, 0x1000\n" \
"s_mov_b32 s84, 0x8000\n" \
"buffer_load_dwordx4 v22, s[64:67], s84 offen offset:0 lds\n" \
"s_and_b32 s33, s3, 1\n" \
"v_lshrrev_b32_e32 v212, 4, v20\n" \
"v_lshrrev_b32_e64 v214, 8, v20\n" \
"v_bfe_u32 v212, v212, 1, 3\n" \
"v_lshlrev_b32_e32 v215, 2, v20\n" \
"s_addk_i32 s84, 0x1c00\n" \
"buffer_load_dwordx4 v23, s[64:67], s84 offen offset:1024 lds\n" \
"v_lshrrev_b32_e64 v216, 1, v20\n" \
"v_bfe_u32 v213, v20, 4, 4\n" \
"s_lshl_b32 s4, s3, 5\n" \
"v_xor_b32_e32 v212, v212, v214\n" \
"v_lshlrev_b32_e32 v214, 11, v214\n" \
"s_addk_i32 s84, 0x1c00\n" \
"buffer_load_dwordx4 v22, s[64:67], s84 offen offset:2048 lds\n" \
"v_and_b32_e32 v215, 0x300, v215\n" \
"v_and_b32_e64 v217, 24, v216\n" \
"v_and_or_b32 v213, s4, 64, v213\n" \
"v_or3_b32 v214, v215, v214, v217\n" \
"s_lshl_b32 s4, s33, 14\n" \
"s_addk_i32 s84, 0x1c00\n" \
"buffer_load_dwordx4 v23, s[64:67], s84 offen offset:3072 lds\n" \
"v_and_b32_e32 v215, 0xe0, v216\n" \
"v_lshlrev_b32_e32 v213, 7, v213\n" \
"v_lshlrev_b32_e32 v212, 4, v212\n" \
"v_or3_b32 v214, v214, s4, v215\n" \
"v_or_b32_e64 v28, v212, v213\n" \
"s_add_u32 m0, s31, 0x4000\n" \
"s_movk_i32 s84, 0x0\n" \
"buffer_load_dwordx4 v26, s[72:75], s84 offen offset:0 lds\n" \
"v_add_u32_e32 v32, 0x10000, v214\n" \
"v_bitop3_b32 v29, v212, 64, v213 bitop3:0x36\n" \
"v_xor_b32_e32 v33, 0x20, v32\n" \
"v_xor_b32_e64 v34, 0x40, v32\n" \
"v_xor_b32_e32 v35, 0x60, v32\n" \
"s_movk_i32 s84, 0x7c00\n" \
"buffer_load_dwordx4 v26, s[72:75], s84 offen offset:1024 lds\n" \
"v_xor_b32_e32 v36, 0x80, v32\n" \
"v_xor_b32_e32 v37, 0xa0, v32\n" \
"v_xor_b32_e32 v38, 0xc0, v32\n" \
"v_xor_b32_e32 v39, 0xe0, v32\n" \
"s_mov_b32 s80, s26\n" \
"s_addk_i32 s84, 0x7c00\n" \
"buffer_load_dwordx4 v27, s[72:75], s84 offen offset:2048 lds\n" \
"s_and_b32 s81, s27, 0xffff\n" \
"s_mov_b32 s82, 0x10000000\n" \
"s_mov_b32 s83, 0x20000\n" \
"s_mov_b32 s76, s18\n" \
"s_and_b32 s77, s19, 0xffff\n" \
"s_addk_i32 s84, 0x7c00\n" \
"buffer_load_dwordx4 v27, s[72:75], s84 offen offset:3072 lds\n" \
"s_mov_b32 s78, 0x1e6000\n" \
"s_mov_b32 s79, 0x20000\n" \
"s_branch L_pgo_%=\n" \
"L_pret:\n" \
"s_setpc_b64 s[88:89]\n" \
"L_pgo_%=:\n" \
"s_mul_i32 s88, s33, 0xa000\n" \
"s_lshl_b32 s89, s3, 11\n" \
"s_and_b32 s89, s89, 0x1000\n" \
"s_add_u32 s88, s88, s89\n" \
"s_getpc_b64 s[86:87]\n" \
"L_hpa_%=:\n" \
"s_add_u32 s86, s86, L_pp1-L_hpa_%=\n" \
"s_addc_u32 s87, s87, 0\n" \
"s_add_u32 s86, s86, s88\n" \
"s_addc_u32 s87, s87, 0\n" \
"s_add_u32 m0, s30, 0x8000\n" \
"s_movk_i32 s84, 0x80\n" \
"buffer_load_dwordx4 v22, s[64:67], s84 offen offset:0 lds\n" \
"s_lshr_b32 s4, s2, 1\n" \
"s_and_b32 s4, s4, 0x7c\n" \
"v_bfe_u32 v212, v20, 4, 5\n" \
"s_movk_i32 s84, 0x1c80\n" \
"buffer_load_dwordx4 v23, s[64:67], s84 offen offset:1024 lds\n" \
"s_or_b32 s4, s4, s3\n" \
"v_min_u32_e32 v212, 26, v212\n" \
"v_mad_u64_u32 v[212:213], s[4:5], s4, 27, v[212:213]\n" \
"s_movk_i32 s84, 0x3880\n" \
"buffer_load_dwordx4 v22, s[64:67], s84 offen offset:2048 lds\n" \
"v_mul_u32_u24_e32 v213, 0xaaab, v212\n" \
"v_lshrrev_b32_e32 v213, 18, v213\n" \
"s_mov_b32 s4, 0x1fffffa\n" \
"s_movk_i32 s84, 0x5480\n" \
"buffer_load_dwordx4 v23, s[64:67], s84 offen offset:3072 lds\n" \
"v_mad_u64_u32 v[214:215], s[4:5], v213, s4, v[212:213]\n" \
"s_and_b32 s6, s2, 7\n" \
"v_mul_u32_u24_e32 v213, 27, v213\n" \
"s_mov_b64 exec, 0\n" \
"s_swappc_b64 s[88:89], s[86:87]\n" \
"s_mov_b64 exec, -1\n" \
"s_add_u32 s86, s86, 0x2000\n" \
"s_addc_u32 s87, s87, 0\n" \
"s_add_u32 m0, s31, 0x8000\n" \
"s_mov_b32 s84, 0x80000\n" \
"buffer_load_dwordx4 v26, s[68:71], s84 offen offset:0 lds\n" \
"v_lshrrev_b32_e32 v214, 1, v214\n" \
"v_and_b32_e32 v212, 1, v212\n" \
"v_mul_u32_u24_e32 v214, 9, v214\n" \
"s_addk_i32 s84, 0x7c00\n" \
"buffer_load_dwordx4 v26, s[68:71], s84 offen offset:1024 lds\n" \
"v_add3_u32 v212, v212, s6, v213\n" \
"v_add_lshl_u32 v212, v212, v214, 7\n" \
"v_mov_b32_e32 v213, 0\n" \
"s_mov_b64 exec, 0\n" \
"s_swappc_b64 s[88:89], s[86:87]\n" \
"s_mov_b64 exec, -1\n" \
"s_add_u32 s86, s86, 0x2000\n" \
"s_addc_u32 s87, s87, 0\n" \
"s_addk_i32 s84, 0x7c00\n" \
"buffer_load_dwordx4 v27, s[68:71], s84 offen offset:2048 lds\n" \
"v_lshl_add_u64 v[186:187], s[18:19], 0, v[212:213]\n" \
"s_lshl_b32 s35, s2, 10\n" \
"s_lshr_b32 s37, s2, 1\n" \
"s_addk_i32 s84, 0x7c00\n" \
"buffer_load_dwordx4 v27, s[68:71], s84 offen offset:3072 lds\n" \
"s_mul_i32 s37, s37, 0x24800\n" \
"s_and_b32 s35, s35, 0x400\n" \
"s_lshl_b32 s36, s3, 8\n" \
"s_mov_b64 exec, 0\n" \
"s_swappc_b64 s[88:89], s[86:87]\n" \
"s_mov_b64 exec, -1\n" \
"s_add_u32 s86, s86, 0x2000\n" \
"s_addc_u32 s87, s87, 0\n" \
"s_add_u32 m0, s30, 0x9000\n" \
"s_mov_b32 s84, 0x8080\n" \
"buffer_load_dwordx4 v22, s[64:67], s84 offen offset:0 lds\n" \
"s_or_b32 s35, s37, s35\n" \
"s_and_b32 s4, s2, 7\n" \
"s_lshr_b32 s6, s2, 5\n" \
"s_addk_i32 s84, 0x1c00\n" \
"buffer_load_dwordx4 v23, s[64:67], s84 offen offset:1024 lds\n" \
"s_lshl_b32 s34, s2, 5\n" \
"s_add_i32 s35, s35, s36\n" \
"s_lshl_b32 s7, s4, 10\n" \
"s_addk_i32 s84, 0x1c00\n" \
"buffer_load_dwordx4 v22, s[64:67], s84 offen offset:2048 lds\n" \
"s_and_b32 s34, s34, 0x300\n" \
"s_add_i32 s35, s35, 0x3004000\n" \
"s_lshl_b32 s37, s6, 13\n" \
"s_mov_b64 exec, 0\n" \
"s_swappc_b64 s[88:89], s[86:87]\n" \
"s_mov_b64 exec, -1\n" \
"s_andn2_b32 s85, 2, s3\n" \
"s_lshl_b32 s85, s85, 12\n" \
"s_add_u32 s86, s86, s85\n" \
"s_addc_u32 s87, s87, 0\n" \
"s_addk_i32 s84, 0x1c00\n" \
"buffer_load_dwordx4 v23, s[64:67], s84 offen offset:3072 lds\n" \
"s_or_b32 s34, s7, s34\n" \
"s_or_b32 s7, s37, s7\n" \
"v_mov_b32_e64 v213, s35\n" \
"s_add_u32 m0, s31, 0xc000\n" \
"s_mov_b32 s84, 0x80000\n" \
"buffer_load_dwordx4 v26, s[72:75], s84 offen offset:0 lds\n" \
"s_or_b32 s7, s7, s36\n" \
"s_lshr_b32 s5, s2, 3\n" \
"s_or_b32 s7, s7, 0x5c00000\n" \
"s_mov_b64 exec, 0\n" \
"s_swappc_b64 s[88:89], s[86:87]\n" \
"s_mov_b64 exec, -1\n" \
"s_addk_i32 s84, 0x7c00\n" \
"buffer_load_dwordx4 v26, s[72:75], s84 offen offset:1024 lds\n" \
"v_writelane_b32 v213, s7, 2\n" \
"s_mul_i32 s5, s5, 0x90000\n" \
"v_writelane_b32 v213, s7, 3\n" \
"s_addk_i32 s84, 0x7c00\n" \
"buffer_load_dwordx4 v27, s[72:75], s84 offen offset:2048 lds\n" \
"s_lshl_b32 s7, s3, 14\n" \
"s_lshl_b32 s4, s4, 16\n" \
"s_add_i32 s5, s5, s7\n" \
"s_addk_i32 s84, 0x7c00\n" \
"buffer_load_dwordx4 v27, s[72:75], s84 offen offset:3072 lds\n" \
"s_or_b32 s34, s34, 0x5be8000\n" \
"v_writelane_b32 v213, s34, 4\n" \
"s_add_i32 s4, s5, s4\n" \
"v_writelane_b32 v213, s34, 5\n" \
"s_add_i32 s4, s4, 0x4243800\n" \
"v_lshlrev_b32_e64 v212, 3, v20\n" \
"v_and_b32_e32 v212, 0x80, v212\n" \
"v_writelane_b32 v213, s4, 6\n" \
"s_mul_i32 s4, s6, 0xa00\n" \
"s_add_i32 s4, s4, 0x5bc0f80\n" \
"v_writelane_b32 v213, s4, 7\n" \
"v_add_u32_e32 v49, v213, v212\n" \
"s_mov_b32 m0, s32\n" \
"s_mov_b32 exec_lo, 0xffffff\n" \
"s_mov_b32 exec_hi, 0xfff0000\n" \
"global_load_lds_dword v[184:185], off\n" \
"s_mov_b64 exec, -1\n" \
"s_mov_b32 m0, s32\n" \
"s_mov_b64 exec, 0xff\n" \
"buffer_load_dword v49, s[80:83], 0 offen lds\n" \
"s_mov_b64 exec, -1\n" \
"s_mov_b32 s84, 0x5bc0000\n" \
"buffer_load_dword v180, off, s[80:83], s84 sc1\n" \
"s_cmp_lg_u32 s33, 0\n" \
"s_cbranch_scc1 L_w1_%=\n" \
"L_w0_%=:\n" \
"s_waitcnt vmcnt(19)\n" \
"s_barrier\n" \
"s_nop 0\n" \
"ds_read_b64_tr_b16 a[160:161], v32 offset:0\n" \
"ds_read_b64_tr_b16 a[162:163], v32 offset:1024\n" \
"ds_read_b128 a[128:131], v28 offset:16384\n" \
"ds_read_b64_tr_b16 a[224:225], v32 offset:8192\n" \
"ds_read_b64_tr_b16 a[226:227], v32 offset:9216\n" \
"ds_read_b128 a[192:195], v29 offset:16384\n" \
"ds_read_b128 a[132:135], v28 offset:18432\n" \
"ds_read_b128 a[196:199], v29 offset:18432\n" \
"ds_read_b64_tr_b16 a[164:165], v33 offset:0\n" \
"ds_read_b64_tr_b16 a[166:167], v33 offset:1024\n" \
"ds_read_b64_tr_b16 a[228:229], v33 offset:8192\n" \
"ds_read_b64_tr_b16 a[230:231], v33 offset:9216\n" \
"ds_read_b128 a[136:139], v28 offset:20480\n" \
"ds_read_b128 a[200:203], v29 offset:20480\n" \
"s_waitcnt lgkmcnt(11)\n" \
"v_mfma_f32_16x16x32_bf16 a[0:3], a[160:163], a[128:131], 0\n" \
"ds_read_b64_tr_b16 a[168:169], v34 offset:0\n" \
"s_movk_i32 s5, 0xd80\n" \
"s_waitcnt lgkmcnt(9)\n" \
"v_mfma_f32_16x16x32_bf16 a[0:3], a[224:227], a[192:195], a[0:3]\n" \
"ds_read_b64_tr_b16 a[170:171], v34 offset:1024\n" \
"s_lshr_b32 s6, s2, 5\n" \
"s_waitcnt lgkmcnt(9)\n" \
"v_mfma_f32_16x16x32_bf16 a[16:19], a[160:163], a[132:135], 0\n" \
"ds_read_b64_tr_b16 a[232:233], v34 offset:8192\n" \
"v_lshrrev_b32_e32 v212, 4, v20\n" \
"s_waitcnt lgkmcnt(9)\n" \
"v_mfma_f32_16x16x32_bf16 a[16:19], a[224:227], a[196:199], a[16:19]\n" \
"ds_read_b64_tr_b16 a[234:235], v34 offset:9216\n" \
"v_lshl_or_b32 v215, s3, 6, v212\n" \
"s_waitcnt lgkmcnt(8)\n" \
"v_mfma_f32_16x16x32_bf16 a[20:23], a[164:167], a[132:135], 0\n" \
"ds_read_b128 a[140:143], v28 offset:22528\n" \
"v_mul_u32_u24_e32 v216, 0x2aab, v215\n" \
"s_waitcnt lgkmcnt(7)\n" \
"v_mfma_f32_16x16x32_bf16 a[20:23], a[228:231], a[196:199], a[20:23]\n" \
"ds_read_b128 a[204:207], v29 offset:22528\n" \
"v_lshrrev_b32_e32 v216, 16, v216\n" \
"v_mfma_f32_16x16x32_bf16 a[4:7], a[164:167], a[128:131], 0\n" \
"ds_read_b64_tr_b16 a[172:173], v35 offset:0\n" \
"v_mad_i32_i24 v217, v216, -6, v215\n" \
"v_mfma_f32_16x16x32_bf16 a[4:7], a[228:231], a[192:195], a[4:7]\n" \
"ds_read_b64_tr_b16 a[174:175], v35 offset:1024\n" \
"v_add_u32_e32 v218, 0xfffffe50, v215\n" \
"s_waitcnt lgkmcnt(9)\n" \
"v_mfma_f32_16x16x32_bf16 a[32:35], a[160:163], a[136:139], 0\n" \
"ds_read_b64_tr_b16 a[236:237], v35 offset:8192\n" \
"s_mul_i32 s35, s6, 0x48\n" \
"s_waitcnt lgkmcnt(9)\n" \
"v_mfma_f32_16x16x32_bf16 a[32:35], a[224:227], a[200:203], a[32:35]\n" \
"ds_read_b64_tr_b16 a[238:239], v35 offset:9216\n" \
"v_lshrrev_b32_e64 v219, 1, v217\n" \
"v_mfma_f32_16x16x32_bf16 a[36:39], a[164:167], a[136:139], 0\n" \
"ds_read_b128 a[144:147], v28 offset:0\n" \
"v_add_u32_e64 v218, s35, v216\n" \
"v_mfma_f32_16x16x32_bf16 a[36:39], a[228:231], a[200:203], a[36:39]\n" \
"ds_read_b128 a[208:211], v29 offset:0\n" \
"v_mul_u32_u24_e32 v219, 0x480, v219\n" \
"s_waitcnt lgkmcnt(10)\n" \
"v_mfma_f32_16x16x32_bf16 a[40:43], a[168:171], a[136:139], 0\n" \
"ds_read_b128 a[148:151], v28 offset:2048\n" \
"v_and_b32_e32 v214, 16, v20\n" \
"s_waitcnt lgkmcnt(9)\n" \
"v_mfma_f32_16x16x32_bf16 a[40:43], a[232:235], a[200:203], a[40:43]\n" \
"ds_read_b128 a[212:215], v29 offset:2048\n" \
"v_mad_u32_u24 v219, v218, s5, v219\n" \
"v_mfma_f32_16x16x32_bf16 a[24:27], a[168:171], a[132:135], 0\n" \
"ds_read_b128 a[152:155], v28 offset:4096\n" \
"v_or_b32_e32 v192, v219, v214\n" \
"v_mfma_f32_16x16x32_bf16 a[24:27], a[232:235], a[196:199], a[24:27]\n" \
"ds_read_b128 a[216:219], v29 offset:4096\n" \
"v_mov_b32_e64 v220, 2.0\n" \
"v_mfma_f32_16x16x32_bf16 a[8:11], a[168:171], a[128:131], 0\n" \
"ds_read_b128 a[156:159], v28 offset:6144\n" \
"v_mov_b32_e64 v222, 7\n" \
"v_mfma_f32_16x16x32_bf16 a[8:11], a[232:235], a[192:195], a[8:11]\n" \
"ds_read_b128 a[220:223], v29 offset:6144\n" \
"s_mov_b32 m0, s32\n" \
"s_waitcnt lgkmcnt(13)\n" \
"v_mfma_f32_16x16x32_bf16 a[48:51], a[160:163], a[140:143], 0\n" \
"s_mov_b32 exec_lo, 0xff000000\n" \
"s_mov_b32 exec_hi, 0x0\n" \
"global_load_lds_dword v[184:185], off\n" \
"s_mov_b64 exec, -1\n" \
"s_waitcnt lgkmcnt(12)\n" \
"v_mfma_f32_16x16x32_bf16 a[48:51], a[224:227], a[204:207], a[48:51]\n" \
"ds_read_b64_tr_b16 a[176:177], v36 offset:0\n" \
"v_lshlrev_b32_e32 v220, 7, v218\n" \
"v_mfma_f32_16x16x32_bf16 a[52:55], a[164:167], a[140:143], 0\n" \
"ds_read_b64_tr_b16 a[178:179], v36 offset:1024\n" \
"v_lshrrev_b32_e64 v218, 1, v218\n" \
"v_mfma_f32_16x16x32_bf16 a[52:55], a[228:231], a[204:207], a[52:55]\n" \
"s_mov_b32 m0, s32\n" \
"ds_read_b64_tr_b16 a[240:241], v36 offset:8192\n" \
"v_mfma_f32_16x16x32_bf16 a[56:59], a[168:171], a[140:143], 0\n" \
"s_mov_b32 exec_lo, 0xffff\n" \
"s_mov_b32 exec_hi, 0x0\n" \
"global_load_lds_dword v[186:187], off\n" \
"s_mov_b64 exec, -1\n" \
"v_mfma_f32_16x16x32_bf16 a[56:59], a[232:235], a[204:207], a[56:59]\n" \
"ds_read_b64_tr_b16 a[242:243], v36 offset:9216\n" \
"v_and_b32_e32 v218, 4, v218\n" \
"s_waitcnt lgkmcnt(14)\n" \
"v_mfma_f32_16x16x32_bf16 a[60:63], a[172:175], a[140:143], 0\n" \
"ds_read_b64_tr_b16 a[180:181], v37 offset:0\n" \
"v_and_b32_e32 v216, 2, v216\n" \
"s_waitcnt lgkmcnt(13)\n" \
"s_nop 0\n" \
"v_mfma_f32_16x16x32_bf16 a[60:63], a[236:239], a[204:207], a[60:63]\n" \
"ds_read_b64_tr_b16 a[182:183], v37 offset:1024\n" \
"s_mul_i32 s7, s6, 0x2400\n" \
"v_mfma_f32_16x16x32_bf16 a[44:47], a[172:175], a[136:139], 0\n" \
"ds_read_b64_tr_b16 a[244:245], v37 offset:8192\n" \
"s_sub_i32 s34, 0x22000, s7\n" \
"v_mfma_f32_16x16x32_bf16 a[44:47], a[236:239], a[200:203], a[44:47]\n" \
"ds_read_b64_tr_b16 a[246:247], v37 offset:9216\n" \
"v_bitop3_b32 v216, v217, v218, v216 bitop3:0x1e\n" \
"v_mfma_f32_16x16x32_bf16 a[28:31], a[172:175], a[132:135], 0\n" \
"ds_read_b64_tr_b16 a[184:185], v38 offset:0\n" \
"v_or_b32_e32 v213, 0x24400, v20\n" \
"v_mfma_f32_16x16x32_bf16 a[28:31], a[236:239], a[196:199], a[28:31]\n" \
"ds_read_b64_tr_b16 a[186:187], v38 offset:1024\n" \
"v_lshlrev_b32_e64 v216, 4, v216\n" \
"v_mfma_f32_16x16x32_bf16 a[12:15], a[172:175], a[128:131], 0\n" \
"v_add3_u32 v196, v220, s34, v216\n" \
"v_mov_b32_e64 v216, v213\n" \
"v_mfma_f32_16x16x32_bf16 a[12:15], a[236:239], a[192:195], a[12:15]\n" \
"s_movk_i32 s36, 0x1b0\n" \
"v_or_b32_e32 v216, 0x100, v215\n" \
"s_waitcnt lgkmcnt(15)\n" \
"v_mfma_f32_16x16x32_bf16 a[76:79], a[172:175], a[144:147], 0\n" \
"ds_read_b64_tr_b16 a[248:249], v38 offset:8192\n" \
"v_mul_u32_u24_e32 v217, 0x2aab, v216\n" \
"v_mfma_f32_16x16x32_bf16 a[76:79], a[236:239], a[208:211], a[76:79]\n" \
"v_add_u32_e32 v215, 0xffffff50, v215\n" \
"v_lshrrev_b32_e64 v217, 16, v217\n" \
"v_mfma_f32_16x16x32_bf16 a[72:75], a[168:171], a[144:147], 0\n" \
"v_mov_b32_e64 v220, v215\n" \
"v_mad_i32_i24 v218, v217, -6, v216\n" \
"v_mfma_f32_16x16x32_bf16 a[72:75], a[232:235], a[208:211], a[72:75]\n" \
"ds_read_b64_tr_b16 a[250:251], v38 offset:9216\n" \
"v_cmp_gt_u32_e64 vcc, s36, v216\n" \
"v_mfma_f32_16x16x32_bf16 a[68:71], a[164:167], a[144:147], 0\n" \
"v_mov_b32_e32 v221, 7\n" \
"v_lshrrev_b32_e32 v222, 1, v218\n" \
"v_mfma_f32_16x16x32_bf16 a[68:71], a[228:231], a[208:211], a[68:71]\n" \
"v_cndmask_b32_e32 v217, v220, v217, vcc\n" \
"s_movk_i32 s4, 0x48\n" \
"v_mfma_f32_16x16x32_bf16 a[64:67], a[160:163], a[144:147], 0\n" \
"v_add_u32_e64 v220, s35, v217\n" \
"v_mul_u32_u24_e32 v222, 0x480, v222\n" \
"v_mfma_f32_16x16x32_bf16 a[64:67], a[224:227], a[208:211], a[64:67]\n" \
"ds_read_b64_tr_b16 a[188:189], v39 offset:0\n" \
"v_mad_u32_u24 v222, v220, s5, v222\n" \
"v_mfma_f32_16x16x32_bf16 a[80:83], a[160:163], a[148:151], 0\n" \
"v_cmp_gt_u32_e64 s[4:5], s4, v215\n" \
"v_cndmask_b32_e32 v215, v221, v218, vcc\n" \
"s_waitcnt lgkmcnt(15)\n" \
"v_mfma_f32_16x16x32_bf16 a[80:83], a[224:227], a[212:215], a[80:83]\n" \
"v_lshrrev_b32_e32 v218, 1, v220\n" \
"v_or_b32_e32 v214, v222, v214\n" \
"v_mfma_f32_16x16x32_bf16 a[84:87], a[164:167], a[148:151], 0\n" \
"ds_read_b64_tr_b16 a[190:191], v39 offset:1024\n" \
"v_mov_b32_e64 v222, 2.0\n" \
"v_mfma_f32_16x16x32_bf16 a[84:87], a[228:231], a[212:215], a[84:87]\n" \
"v_and_b32_e32 v218, 4, v218\n" \
"v_and_b32_e32 v217, 2, v217\n" \
"v_mfma_f32_16x16x32_bf16 a[88:91], a[168:171], a[148:151], 0\n" \
"v_bitop3_b32 v215, v215, v218, v217 bitop3:0x1e\n" \
"v_lshlrev_b32_e64 v223, 1, v220\n" \
"v_mfma_f32_16x16x32_bf16 a[88:91], a[232:235], a[212:215], a[88:91]\n" \
"ds_read_b64_tr_b16 a[252:253], v39 offset:8192\n" \
"v_mov_b32_e64 v224, 2.0\n" \
"v_mfma_f32_16x16x32_bf16 a[92:95], a[172:175], a[148:151], 0\n" \
"v_cndmask_b32_e32 v193, v222, v214, vcc\n" \
"v_lshlrev_b32_e32 v214, 7, v220\n" \
"v_mfma_f32_16x16x32_bf16 a[92:95], a[236:239], a[212:215], a[92:95]\n" \
"v_lshlrev_b32_e32 v215, 4, v215\n" \
"s_mul_i32 s39, s3, 18\n" \
"v_mfma_f32_16x16x32_bf16 a[108:111], a[172:175], a[152:155], 0\n" \
"ds_read_b64_tr_b16 a[254:255], v39 offset:9216\n" \
"v_add3_u32 v214, v214, s34, v215\n" \
"v_mfma_f32_16x16x32_bf16 a[108:111], a[236:239], a[216:219], a[108:111]\n" \
"v_cndmask_b32_e64 v195, v224, v223, s[4:5]\n" \
"s_movk_i32 s4, 0x1f8\n" \
"v_mfma_f32_16x16x32_bf16 a[104:107], a[168:171], a[152:155], 0\n" \
"v_mov_b32_e32 v215, v213\n" \
"s_add_i32 s35, s35, s39\n" \
"v_mfma_f32_16x16x32_bf16 a[104:107], a[232:235], a[216:219], a[104:107]\n" \
"v_cmp_gt_u32_e32 vcc, s4, v216\n" \
"v_add_u32_e64 v212, s35, v212\n" \
"v_mfma_f32_16x16x32_bf16 a[100:103], a[164:167], a[152:155], 0\n" \
"v_mov_b32_e32 v219, 2.0\n" \
"v_cndmask_b32_e32 v197, v215, v214, vcc\n" \
"v_mfma_f32_16x16x32_bf16 a[100:103], a[228:231], a[216:219], a[100:103]\n" \
"s_movk_i32 s35, 0x120\n" \
"v_mul_u32_u24_e32 v214, 0xd80, v212\n" \
"v_mfma_f32_16x16x32_bf16 a[96:99], a[160:163], a[152:155], 0\n" \
"v_cmp_gt_u32_e32 vcc, s35, v20\n" \
"v_lshrrev_b32_e64 v215, 1, v212\n" \
"v_mfma_f32_16x16x32_bf16 a[96:99], a[224:227], a[216:219], a[96:99]\n" \
"s_and_b32 s4, s2, 7\n" \
"v_cndmask_b32_e32 v194, v219, v214, vcc\n" \
"s_waitcnt lgkmcnt(15)\n" \
"v_mfma_f32_16x16x32_bf16 a[112:115], a[160:163], a[156:159], 0\n" \
"v_lshlrev_b32_e32 v214, 7, v212\n" \
"v_lshrrev_b32_e64 v212, 2, v212\n" \
"v_mfma_f32_16x16x32_bf16 a[112:115], a[224:227], a[220:223], a[112:115]\n" \
"v_and_b32_e64 v212, 2, v212\n" \
"v_and_or_b32 v212, v215, 1, v212\n" \
"v_mfma_f32_16x16x32_bf16 a[116:119], a[164:167], a[156:159], 0\n" \
"s_lshl_b32 s5, s4, 2\n" \
"s_bfe_u32 s38, s2, 0x20003\n" \
"v_mfma_f32_16x16x32_bf16 a[116:119], a[228:231], a[220:223], a[116:119]\n" \
"v_lshlrev_b32_e32 v212, 5, v212\n" \
"s_movk_i32 s35, 0x60\n" \
"v_mfma_f32_16x16x32_bf16 a[120:123], a[168:171], a[156:159], 0\n" \
"s_or_b32 s5, s5, s38\n" \
"v_bitop3_b32 v212, v212, s35, v214 bitop3:0x36\n" \
"v_mfma_f32_16x16x32_bf16 a[120:123], a[232:235], a[220:223], a[120:123]\n" \
"s_lshr_b32 s36, s2, 3\n" \
"v_add_u32_e32 v212, s34, v212\n" \
"v_mfma_f32_16x16x32_bf16 a[124:127], a[172:175], a[156:159], 0\n" \
"s_mul_i32 s34, s5, 0x12000\n" \
"s_or_b32 s40, s34, 0x6000000\n" \
"v_mfma_f32_16x16x32_bf16 a[124:127], a[236:239], a[220:223], a[124:127]\n" \
"s_waitcnt vmcnt(5) lgkmcnt(0)\n" \
"s_barrier\n" \
"v_mfma_f32_16x16x32_bf16 v[52:55], a[176:179], a[128:131], 0\n" \
"s_add_u32 m0, s30, 0x0\n" \
"s_movk_i32 s84, 0x100\n" \
"v_mfma_f32_16x16x32_bf16 v[52:55], a[240:243], a[192:195], v[52:55]\n" \
"buffer_load_dwordx4 v22, s[64:67], s84 offen offset:0 lds\n" \
"v_mfma_f32_16x16x32_bf16 v[56:59], a[180:183], a[128:131], 0\n" \
"ds_read_b64_tr_b16 a[160:161], v32 offset:32768\n" \
"s_mul_i32 s34, s36, 0x90000\n" \
"v_mfma_f32_16x16x32_bf16 v[56:59], a[244:247], a[192:195], v[56:59]\n" \
"s_movk_i32 s84, 0x1d00\n" \
"v_mfma_f32_16x16x32_bf16 v[60:63], a[184:187], a[128:131], 0\n" \
"buffer_load_dwordx4 v23, s[64:67], s84 offen offset:1024 lds\n" \
"v_mfma_f32_16x16x32_bf16 v[60:63], a[248:251], a[192:195], v[60:63]\n" \
"ds_read_b64_tr_b16 a[162:163], v32 offset:33792\n" \
"s_lshl_b32 s36, s3, 14\n" \
"v_mfma_f32_16x16x32_bf16 v[64:67], a[188:191], a[128:131], 0\n" \
"ds_read_b64_tr_b16 a[224:225], v32 offset:40960\n" \
"s_add_i32 s34, s34, s36\n" \
"v_mfma_f32_16x16x32_bf16 v[64:67], a[252:255], a[192:195], v[64:67]\n" \
"ds_read_b128 a[128:131], v28 offset:49152\n" \
"s_lshl_b32 s38, s5, 8\n" \
"v_mfma_f32_16x16x32_bf16 v[80:83], a[188:191], a[132:135], 0\n" \
"ds_read_b128 a[192:195], v29 offset:49152\n" \
"s_add_i32 s41, s34, 0x4240000\n" \
"v_mfma_f32_16x16x32_bf16 v[80:83], a[252:255], a[196:199], v[80:83]\n" \
"s_movk_i32 s84, 0x3900\n" \
"v_mfma_f32_16x16x32_bf16 v[76:79], a[184:187], a[132:135], 0\n" \
"buffer_load_dwordx4 v22, s[64:67], s84 offen offset:2048 lds\n" \
"v_mfma_f32_16x16x32_bf16 v[76:79], a[248:251], a[196:199], v[76:79]\n" \
"s_movk_i32 s84, 0x5500\n" \
"v_mfma_f32_16x16x32_bf16 v[72:75], a[180:183], a[132:135], 0\n" \
"buffer_load_dwordx4 v23, s[64:67], s84 offen offset:3072 lds\n" \
"v_mfma_f32_16x16x32_bf16 v[72:75], a[244:247], a[196:199], v[72:75]\n" \
"s_add_u32 m0, s31, 0x0\n" \
"s_mov_b32 s84, 0x100000\n" \
"v_mfma_f32_16x16x32_bf16 v[68:71], a[176:179], a[132:135], 0\n" \
"buffer_load_dwordx4 v26, s[68:71], s84 offen offset:0 lds\n" \
"v_mfma_f32_16x16x32_bf16 v[68:71], a[240:243], a[196:199], v[68:71]\n" \
"ds_read_b128 a[132:135], v28 offset:51200\n" \
"s_mul_i32 s34, s5, 36\n" \
"v_mfma_f32_16x16x32_bf16 v[84:87], a[176:179], a[136:139], 0\n" \
"ds_read_b128 a[196:199], v29 offset:51200\n" \
"s_add_i32 s5, s7, s40\n" \
"v_mfma_f32_16x16x32_bf16 v[84:87], a[240:243], a[200:203], v[84:87]\n" \
"s_addk_i32 s84, 0x7c00\n" \
"v_mfma_f32_16x16x32_bf16 v[88:91], a[180:183], a[136:139], 0\n" \
"buffer_load_dwordx4 v26, s[68:71], s84 offen offset:1024 lds\n" \
"v_mfma_f32_16x16x32_bf16 v[88:91], a[244:247], a[200:203], v[88:91]\n" \
"ds_read_b64_tr_b16 a[226:227], v32 offset:41984\n" \
"s_addk_i32 s5, 0x2000\n" \
"v_mfma_f32_16x16x32_bf16 v[92:95], a[184:187], a[136:139], 0\n" \
"s_addk_i32 s84, 0x7c00\n" \
"v_mfma_f32_16x16x32_bf16 v[92:95], a[248:251], a[200:203], v[92:95]\n" \
"buffer_load_dwordx4 v27, s[68:71], s84 offen offset:2048 lds\n" \
"v_mfma_f32_16x16x32_bf16 v[96:99], a[188:191], a[136:139], 0\n" \
"ds_read_b64_tr_b16 a[164:165], v33 offset:32768\n" \
"s_mov_b32 s37, 2.0\n" \
"v_mfma_f32_16x16x32_bf16 v[96:99], a[252:255], a[200:203], v[96:99]\n" \
"ds_read_b128 a[136:139], v28 offset:53248\n" \
"s_or_b32 s38, s38, 0x5be8000\n" \
"v_mfma_f32_16x16x32_bf16 v[112:115], a[188:191], a[140:143], 0\n" \
"ds_read_b128 a[200:203], v29 offset:53248\n" \
"v_mfma_f32_16x16x32_bf16 v[112:115], a[252:255], a[204:207], v[112:115]\n" \
"s_addk_i32 s84, 0x7c00\n" \
"v_mfma_f32_16x16x32_bf16 v[108:111], a[184:187], a[140:143], 0\n" \
"buffer_load_dwordx4 v27, s[68:71], s84 offen offset:3072 lds\n" \
"v_mfma_f32_16x16x32_bf16 v[108:111], a[248:251], a[204:207], v[108:111]\n" \
"s_add_u32 m0, s30, 0x1000\n" \
"s_mov_b32 s84, 0x8100\n" \
"v_mfma_f32_16x16x32_bf16 v[104:107], a[180:183], a[140:143], 0\n" \
"buffer_load_dwordx4 v22, s[64:67], s84 offen offset:0 lds\n" \
"v_mfma_f32_16x16x32_bf16 v[104:107], a[244:247], a[204:207], v[104:107]\n" \
"ds_read_b64_tr_b16 a[166:167], v33 offset:33792\n" \
"v_mfma_f32_16x16x32_bf16 v[100:103], a[176:179], a[140:143], 0\n" \
"ds_read_b64_tr_b16 a[228:229], v33 offset:40960\n" \
"v_mfma_f32_16x16x32_bf16 v[100:103], a[240:243], a[204:207], v[100:103]\n" \
"ds_read_b128 a[140:143], v28 offset:55296\n" \
"v_mfma_f32_16x16x32_bf16 v[116:119], a[176:179], a[144:147], 0\n" \
"ds_read_b128 a[204:207], v29 offset:55296\n" \
"v_mfma_f32_16x16x32_bf16 v[116:119], a[240:243], a[208:211], v[116:119]\n" \
"s_addk_i32 s84, 0x1c00\n" \
"v_mfma_f32_16x16x32_bf16 v[120:123], a[180:183], a[144:147], 0\n" \
"buffer_load_dwordx4 v23, s[64:67], s84 offen offset:1024 lds\n" \
"v_mfma_f32_16x16x32_bf16 v[120:123], a[244:247], a[208:211], v[120:123]\n" \
"s_addk_i32 s84, 0x1c00\n" \
"v_mfma_f32_16x16x32_bf16 v[124:127], a[184:187], a[144:147], 0\n" \
"buffer_load_dwordx4 v22, s[64:67], s84 offen offset:2048 lds\n" \
"v_mfma_f32_16x16x32_bf16 v[124:127], a[248:251], a[208:211], v[124:127]\n" \
"s_addk_i32 s84, 0x1c00\n" \
"s_nop 0\n" \
"v_mfma_f32_16x16x32_bf16 v[128:131], a[188:191], a[144:147], 0\n" \
".fill (L_pb + 3808 - .) / 4, 4, 0xBF800000\n" \
"L_pp1:\n" \
"s_cbranch_execz L_pret\n" \
"s_nop 0\n" \
"buffer_load_dwordx4 v23, s[64:67], s84 offen offset:3072 lds\n" \
"v_mfma_f32_16x16x32_bf16 v[128:131], a[252:255], a[208:211], v[128:131]\n" \
"ds_read_b128 a[144:147], v28 offset:32768\n" \
"v_mfma_f32_16x16x32_bf16 v[144:147], a[188:191], a[148:151], 0\n" \
"ds_read_b128 a[208:211], v29 offset:32768\n" \
"v_mfma_f32_16x16x32_bf16 v[144:147], a[252:255], a[212:215], v[144:147]\n" \
"s_add_u32 m0, s31, 0x4000\n" \
"s_mov_b32 s84, 0x100000\n" \
"v_mfma_f32_16x16x32_bf16 v[140:143], a[184:187], a[148:151], 0\n" \
"buffer_load_dwordx4 v26, s[72:75], s84 offen offset:0 lds\n" \
"v_mfma_f32_16x16x32_bf16 v[140:143], a[248:251], a[212:215], v[140:143]\n" \
"ds_read_b64_tr_b16 a[230:231], v33 offset:41984\n" \
"v_mfma_f32_16x16x32_bf16 v[136:139], a[180:183], a[148:151], 0\n" \
"s_addk_i32 s84, 0x7c00\n" \
"s_nop 0\n" \
"v_mfma_f32_16x16x32_bf16 v[136:139], a[244:247], a[212:215], v[136:139]\n" \
"buffer_load_dwordx4 v26, s[72:75], s84 offen offset:1024 lds\n" \
"v_mfma_f32_16x16x32_bf16 v[132:135], a[176:179], a[148:151], 0\n" \
"ds_read_b64_tr_b16 a[168:169], v34 offset:32768\n" \
"v_mfma_f32_16x16x32_bf16 v[132:135], a[240:243], a[212:215], v[132:135]\n" \
"ds_read_b128 a[148:151], v28 offset:34816\n" \
"v_mfma_f32_16x16x32_bf16 v[148:151], a[176:179], a[152:155], 0\n" \
"ds_read_b128 a[212:215], v29 offset:34816\n" \
"v_mfma_f32_16x16x32_bf16 v[148:151], a[240:243], a[216:219], v[148:151]\n" \
"ds_read_b64_tr_b16 a[170:171], v34 offset:33792\n" \
"v_mfma_f32_16x16x32_bf16 v[152:155], a[180:183], a[152:155], 0\n" \
"ds_read_b64_tr_b16 a[232:233], v34 offset:40960\n" \
"v_mfma_f32_16x16x32_bf16 v[152:155], a[244:247], a[216:219], v[152:155]\n" \
"ds_read_b64_tr_b16 a[234:235], v34 offset:41984\n" \
"v_mfma_f32_16x16x32_bf16 v[156:159], a[184:187], a[152:155], 0\n" \
"ds_read_b64_tr_b16 a[172:173], v35 offset:32768\n" \
"v_mfma_f32_16x16x32_bf16 v[156:159], a[248:251], a[216:219], v[156:159]\n" \
"ds_read_b64_tr_b16 a[174:175], v35 offset:33792\n" \
"v_mfma_f32_16x16x32_bf16 v[160:163], a[188:191], a[152:155], 0\n" \
"ds_read_b64_tr_b16 a[236:237], v35 offset:40960\n" \
"v_mfma_f32_16x16x32_bf16 v[160:163], a[252:255], a[216:219], v[160:163]\n" \
"ds_read_b128 a[152:155], v28 offset:36864\n" \
"v_mfma_f32_16x16x32_bf16 v[176:179], a[188:191], a[156:159], 0\n" \
"ds_read_b128 a[216:219], v29 offset:36864\n" \
"v_mfma_f32_16x16x32_bf16 v[176:179], a[252:255], a[220:223], v[176:179]\n" \
"ds_read_b64_tr_b16 a[238:239], v35 offset:41984\n" \
"v_mfma_f32_16x16x32_bf16 v[172:175], a[184:187], a[156:159], 0\n" \
"s_cmp_lg_u32 s3, 0\n" \
"s_cselect_b32 s36, s37, s5\n" \
"v_mfma_f32_16x16x32_bf16 v[172:175], a[248:251], a[220:223], v[172:175]\n" \
"s_lshl_b32 s5, s6, 2\n" \
"s_or_b32 s5, s38, s5\n" \
"v_mfma_f32_16x16x32_bf16 v[168:171], a[180:183], a[156:159], 0\n" \
"s_lshl_b32 s35, s3, 10\n" \
"v_mfma_f32_16x16x32_bf16 v[168:171], a[244:247], a[220:223], v[168:171]\n" \
"s_cmpk_lt_u32 s3, 3\n" \
"s_cselect_b32 s37, s37, s5\n" \
"v_mfma_f32_16x16x32_bf16 v[164:167], a[176:179], a[156:159], 0\n" \
"s_lshl_b32 s5, s4, 16\n" \
"s_lshl_b32 s4, s4, 11\n" \
"v_mfma_f32_16x16x32_bf16 v[164:167], a[240:243], a[220:223], v[164:167]\n" \
"ds_read_b128 a[156:159], v28 offset:38912\n" \
"ds_read_b128 a[220:223], v29 offset:38912\n" \
"s_waitcnt lgkmcnt(15)\n" \
"v_mfma_f32_16x16x32_bf16 a[0:3], a[160:163], a[128:131], a[0:3]\n" \
"s_addk_i32 s84, 0x7c00\n" \
"v_mfma_f32_16x16x32_bf16 a[0:3], a[224:227], a[192:195], a[0:3]\n" \
"buffer_load_dwordx4 v27, s[72:75], s84 offen offset:2048 lds\n" \
"v_mfma_f32_16x16x32_bf16 a[4:7], a[164:167], a[128:131], a[4:7]\n" \
"ds_read_b64_tr_b16 a[176:177], v36 offset:32768\n" \
"v_cndmask_b32_e32 v198, v213, v212, vcc\n" \
"s_waitcnt lgkmcnt(15)\n" \
"v_mfma_f32_16x16x32_bf16 a[4:7], a[228:231], a[192:195], a[4:7]\n" \
"s_addk_i32 s84, 0x7c00\n" \
"s_waitcnt lgkmcnt(11)\n" \
"v_mfma_f32_16x16x32_bf16 a[8:11], a[168:171], a[128:131], a[8:11]\n" \
"buffer_load_dwordx4 v27, s[72:75], s84 offen offset:3072 lds\n" \
"s_waitcnt lgkmcnt(9)\n" \
"v_mfma_f32_16x16x32_bf16 a[8:11], a[232:235], a[192:195], a[8:11]\n" \
"ds_read_b64_tr_b16 a[178:179], v36 offset:33792\n" \
"v_or_b32_e32 v212, s35, v20\n" \
"s_waitcnt lgkmcnt(8)\n" \
"v_mfma_f32_16x16x32_bf16 a[12:15], a[172:175], a[128:131], a[12:15]\n" \
"ds_read_b64_tr_b16 a[240:241], v36 offset:40960\n" \
"s_or_b32 s39, s40, s35\n" \
"s_waitcnt lgkmcnt(5)\n" \
"v_mfma_f32_16x16x32_bf16 a[12:15], a[236:239], a[192:195], a[12:15]\n" \
"s_or_b32 s55, s41, s4\n" \
"v_or_b32_e32 v199, 0x22000, v212\n" \
"v_mfma_f32_16x16x32_bf16 a[28:31], a[172:175], a[132:135], a[28:31]\n" \
"s_add_i32 s35, s39, s7\n" \
"s_add_i32 s54, s41, s5\n" \
"v_mfma_f32_16x16x32_bf16 a[28:31], a[236:239], a[196:199], a[28:31]\n" \
"ds_read_b64_tr_b16 a[242:243], v36 offset:41984\n" \
"s_add_i32 s55, s55, 0x80000\n" \
"v_mfma_f32_16x16x32_bf16 a[24:27], a[168:171], a[132:135], a[24:27]\n" \
"s_lshr_b32 s4, s2, 1\n" \
"s_and_b32 s4, s4, 0x7c\n" \
"v_mfma_f32_16x16x32_bf16 a[24:27], a[232:235], a[196:199], a[24:27]\n" \
"v_mov_b32_e32 v213, s13\n" \
"s_or_b32 s4, s4, s3\n" \
"v_mfma_f32_16x16x32_bf16 a[20:23], a[164:167], a[132:135], a[20:23]\n" \
"v_lshrrev_b32_e64 v212, 4, v20\n" \
"v_mad_u64_u32 v[214:215], s[4:5], s4, 36, v[212:213]\n" \
"v_mfma_f32_16x16x32_bf16 a[20:23], a[228:231], a[196:199], a[20:23]\n" \
"ds_read_b64_tr_b16 a[180:181], v37 offset:32768\n" \
"v_mov_b32_e64 v217, s9\n" \
"v_mfma_f32_16x16x32_bf16 a[16:19], a[160:163], a[132:135], a[16:19]\n" \
"s_movk_i32 s4, 0x240\n" \
"v_cmp_gt_u32_e32 vcc, s4, v20\n" \
"v_mfma_f32_16x16x32_bf16 a[16:19], a[224:227], a[196:199], a[16:19]\n" \
"s_lshl_b32 s4, s2, 3\n" \
"s_and_b32 s4, s4, 56\n" \
"v_mfma_f32_16x16x32_bf16 a[32:35], a[160:163], a[136:139], a[32:35]\n" \
"s_mov_b32 m0, s32\n" \
"ds_read_b64_tr_b16 a[182:183], v37 offset:33792\n" \
"v_mfma_f32_16x16x32_bf16 a[32:35], a[224:227], a[200:203], a[32:35]\n" \
"s_mov_b32 exec_lo, 0x0\n" \
"s_mov_b32 exec_hi, 0xff\n" \
"global_load_lds_dword v[184:185], off\n" \
"s_mov_b64 exec, -1\n" \
"v_mfma_f32_16x16x32_bf16 a[36:39], a[164:167], a[136:139], a[36:39]\n" \
"v_cndmask_b32_e32 v217, v217, v213, vcc\n" \
"v_lshlrev_b32_e64 v213, 3, v214\n" \
"v_mfma_f32_16x16x32_bf16 a[36:39], a[228:231], a[200:203], a[36:39]\n" \
"ds_read_b64_tr_b16 a[244:245], v37 offset:40960\n" \
"v_and_b32_e32 v213, 0x1ffc0, v213\n" \
"v_mfma_f32_16x16x32_bf16 a[40:43], a[168:171], a[136:139], a[40:43]\n" \
"v_and_b32_e64 v214, 7, v214\n" \
"v_or3_b32 v213, v214, s4, v213\n" \
"v_mfma_f32_16x16x32_bf16 a[40:43], a[232:235], a[200:203], a[40:43]\n" \
"s_mov_b32 m0, s32\n" \
"s_lshl_b32 s4, s2, 2\n" \
"v_mfma_f32_16x16x32_bf16 a[44:47], a[172:175], a[136:139], a[44:47]\n" \
"s_mov_b32 exec_lo, 0xffff0000\n" \
"s_mov_b32 exec_hi, 0x0\n" \
"global_load_lds_dword v[186:187], off\n" \
"s_mov_b64 exec, -1\n" \
"v_mfma_f32_16x16x32_bf16 a[44:47], a[236:239], a[200:203], a[44:47]\n" \
"ds_read_b64_tr_b16 a[246:247], v37 offset:41984\n" \
"s_or_b32 s4, s4, s3\n" \
"v_mfma_f32_16x16x32_bf16 a[60:63], a[172:175], a[140:143], a[60:63]\n" \
"s_mul_i32 s4, s4, 18\n" \
"v_min_u32_e64 v212, 53, v212\n" \
"v_mfma_f32_16x16x32_bf16 a[60:63], a[236:239], a[204:207], a[60:63]\n" \
"s_sub_i32 s4, s4, 36\n" \
"v_add_u32_e32 v212, s4, v212\n" \
"v_mfma_f32_16x16x32_bf16 a[56:59], a[168:171], a[140:143], a[56:59]\n" \
"ds_read_b64_tr_b16 a[184:185], v38 offset:32768\n" \
"v_mov_b32_e64 v215, s12\n" \
"v_mfma_f32_16x16x32_bf16 a[56:59], a[232:235], a[204:207], a[56:59]\n" \
"v_mov_b32_e32 v216, s8\n" \
"v_cndmask_b32_e32 v212, v212, v213, vcc\n" \
"v_mfma_f32_16x16x32_bf16 a[52:55], a[164:167], a[140:143], a[52:55]\n" \
"v_cndmask_b32_e32 v216, v216, v215, vcc\n" \
"v_lshlrev_b32_e32 v212, 7, v212\n" \
"v_mfma_f32_16x16x32_bf16 a[52:55], a[228:231], a[204:207], a[52:55]\n" \
"ds_read_b64_tr_b16 a[186:187], v38 offset:33792\n" \
"v_mov_b32_e64 v213, 0\n" \
"v_mfma_f32_16x16x32_bf16 a[48:51], a[160:163], a[140:143], a[48:51]\n" \
"v_lshl_add_u64 v[188:189], v[216:217], 0, v[212:213]\n" \
"v_mov_b32_e32 v215, 0xffff\n" \
"v_mfma_f32_16x16x32_bf16 a[48:51], a[224:227], a[204:207], a[48:51]\n" \
"s_and_b32 s4, s3, 1\n" \
"s_lshl_b32 s5, s4, 4\n" \
"v_mfma_f32_16x16x32_bf16 a[64:67], a[160:163], a[144:147], a[64:67]\n" \
"ds_read_b64_tr_b16 a[248:249], v38 offset:40960\n" \
"v_lshrrev_b32_e64 v212, 4, v20\n" \
"v_mfma_f32_16x16x32_bf16 a[64:67], a[224:227], a[208:211], a[64:67]\n" \
"v_lshrrev_b32_e32 v218, 6, v20\n" \
"v_and_b32_e32 v219, 12, v218\n" \
"v_mfma_f32_16x16x32_bf16 a[68:71], a[164:167], a[144:147], a[68:71]\n" \
"v_and_or_b32 v214, v212, 15, s5\n" \
"v_cmp_gt_u32_e64 vcc, v219, v214\n" \
"v_mfma_f32_16x16x32_bf16 a[68:71], a[228:231], a[208:211], a[68:71]\n" \
"ds_read_b64_tr_b16 a[250:251], v38 offset:41984\n" \
"v_mov_b32_e32 v220, 0xffff0000\n" \
"v_mfma_f32_16x16x32_bf16 a[72:75], a[168:171], a[144:147], a[72:75]\n" \
"s_lshl_b32 s5, s2, 2\n" \
"v_cndmask_b32_e64 v217, v215, 0, vcc\n" \
"v_mfma_f32_16x16x32_bf16 a[72:75], a[232:235], a[208:211], a[72:75]\n" \
"v_cmp_lt_u32_e32 vcc, v219, v214\n" \
"v_mov_b32_e64 v213, s23\n" \
"v_mfma_f32_16x16x32_bf16 a[76:79], a[172:175], a[144:147], a[76:79]\n" \
"s_or_b32 s5, s5, s3\n" \
"v_cndmask_b32_e32 v221, 0, v220, vcc\n" \
"v_mfma_f32_16x16x32_bf16 a[76:79], a[236:239], a[208:211], a[76:79]\n" \
"ds_read_b64_tr_b16 a[188:189], v39 offset:32768\n" \
"v_or_b32_e64 v12, v217, v221\n" \
"v_mfma_f32_16x16x32_bf16 a[92:95], a[172:175], a[148:151], a[92:95]\n" \
"v_or_b32_e32 v217, 2, v219\n" \
"v_cmp_gt_u32_e32 vcc, v217, v214\n" \
"v_mfma_f32_16x16x32_bf16 a[92:95], a[236:239], a[212:215], a[92:95]\n" \
"v_mov_b32_e32 v216, s25\n" \
"s_movk_i32 s40, 0xf400\n" \
"v_mfma_f32_16x16x32_bf16 a[88:91], a[168:171], a[148:151], a[88:91]\n" \
"ds_read_b64_tr_b16 a[190:191], v39 offset:33792\n" \
"v_cndmask_b32_e64 v221, v215, 0, vcc\n" \
"v_mfma_f32_16x16x32_bf16 a[88:91], a[232:235], a[212:215], a[88:91]\n" \
"v_cmp_lt_u32_e32 vcc, v217, v214\n" \
"s_lshl_b32 s4, s4, 6\n" \
"v_mfma_f32_16x16x32_bf16 a[84:87], a[164:167], a[148:151], a[84:87]\n" \
"v_cndmask_b32_e32 v217, 0, v220, vcc\n" \
"v_or_b32_e32 v13, v221, v217\n" \
"v_mfma_f32_16x16x32_bf16 a[84:87], a[228:231], a[212:215], a[84:87]\n" \
"ds_read_b64_tr_b16 a[252:253], v39 offset:40960\n" \
"v_or_b32_e64 v217, 16, v219\n" \
"v_mfma_f32_16x16x32_bf16 a[80:83], a[160:163], a[148:151], a[80:83]\n" \
"v_cmp_gt_u32_e32 vcc, v217, v214\n" \
"s_nop 1\n" \
"v_cndmask_b32_e64 v221, v215, 0, vcc\n" \
"v_mfma_f32_16x16x32_bf16 a[80:83], a[224:227], a[212:215], a[80:83]\n" \
"v_cmp_lt_u32_e32 vcc, v217, v214\n" \
"s_nop 1\n" \
"v_cndmask_b32_e64 v217, 0, v220, vcc\n" \
"v_mfma_f32_16x16x32_bf16 a[96:99], a[160:163], a[152:155], a[96:99]\n" \
"ds_read_b64_tr_b16 a[254:255], v39 offset:41984\n" \
"v_or_b32_e64 v14, v221, v217\n" \
"v_mfma_f32_16x16x32_bf16 a[96:99], a[224:227], a[216:219], a[96:99]\n" \
"v_or_b32_e32 v217, 18, v219\n" \
"v_cmp_gt_u32_e32 vcc, v217, v214\n" \
"v_mfma_f32_16x16x32_bf16 a[100:103], a[164:167], a[152:155], a[100:103]\n" \
"s_nop 0\n" \
"v_cndmask_b32_e64 v215, v215, 0, vcc\n" \
"v_cmp_lt_u32_e32 vcc, v217, v214\n" \
"v_mfma_f32_16x16x32_bf16 a[100:103], a[228:231], a[216:219], a[100:103]\n" \
"s_nop 0\n" \
"v_cndmask_b32_e32 v214, 0, v220, vcc\n" \
"v_or_b32_e64 v15, v215, v214\n" \
"v_mfma_f32_16x16x32_bf16 a[104:107], a[168:171], a[152:155], a[104:107]\n" \
"v_mad_u64_u32 v[214:215], s[6:7], s5, 42, v[212:213]\n" \
"s_movk_i32 s5, 0x1440\n" \
"v_mfma_f32_16x16x32_bf16 a[104:107], a[232:235], a[216:219], a[104:107]\n" \
"v_cmp_gt_u32_e32 vcc, s5, v214\n" \
"v_min_u32_e32 v217, 0xa43f, v214\n" \
"v_mfma_f32_16x16x32_bf16 a[108:111], a[172:175], a[152:155], a[108:111]\n" \
"v_mov_b32_e32 v214, s24\n" \
"v_cndmask_b32_e32 v215, v216, v213, vcc\n" \
"v_mfma_f32_16x16x32_bf16 a[108:111], a[236:239], a[216:219], a[108:111]\n" \
"v_mov_b32_e32 v213, s22\n" \
"s_movk_i32 s6, 0xffee\n" \
"s_waitcnt lgkmcnt(15)\n" \
"v_mfma_f32_16x16x32_bf16 a[124:127], a[172:175], a[156:159], a[124:127]\n" \
"v_cndmask_b32_e32 v214, v214, v213, vcc\n" \
"v_add_u32_e32 v213, 0xffffebc0, v217\n" \
"v_mfma_f32_16x16x32_bf16 a[124:127], a[236:239], a[220:223], a[124:127]\n" \
"s_lshl_b32 s5, s2, 5\n" \
"v_cndmask_b32_e32 v213, v213, v217, vcc\n" \
"v_mfma_f32_16x16x32_bf16 a[120:123], a[168:171], a[156:159], a[120:123]\n" \
"v_lshlrev_b32_e64 v216, 7, v213\n" \
"v_mul_u32_u24_e32 v213, 0xe39, v212\n" \
"v_mfma_f32_16x16x32_bf16 a[120:123], a[232:235], a[220:223], a[120:123]\n" \
"v_mov_b32_e32 v217, 0\n" \
"v_lshrrev_b32_e32 v213, 16, v213\n" \
"v_mfma_f32_16x16x32_bf16 a[116:119], a[164:167], a[156:159], a[116:119]\n" \
"v_lshl_add_u64 v[16:17], v[214:215], 0, v[216:217]\n" \
"v_mad_i32_i24 v214, v213, s6, v212\n" \
"v_mfma_f32_16x16x32_bf16 a[116:119], a[228:231], a[220:223], a[116:119]\n" \
"s_add_i32 s5, s5, 32\n" \
"v_lshlrev_b32_e32 v215, 4, v214\n" \
"v_mfma_f32_16x16x32_bf16 a[112:115], a[160:163], a[156:159], a[112:115]\n" \
"v_add_u32_e64 v216, s5, v215\n" \
"v_and_b32_e32 v216, 0xf0, v216\n" \
"v_mfma_f32_16x16x32_bf16 a[112:115], a[224:227], a[220:223], a[112:115]\n" \
"s_waitcnt vmcnt(20)\n" \
"v_readfirstlane_b32 s62, v180\n" \
"s_nop 3\n" \
"s_add_u32 s62, s62, 0x9E3779B1\n" \
"s_waitcnt vmcnt(2) lgkmcnt(0)\n" \
"s_barrier\n" \
"v_mfma_f32_16x16x32_bf16 v[52:55], a[176:179], a[128:131], v[52:55]\n" \
"s_add_u32 m0, s30, 0x8000\n" \
"s_movk_i32 s84, 0x180\n" \
"v_mfma_f32_16x16x32_bf16 v[52:55], a[240:243], a[192:195], v[52:55]\n" \
"buffer_load_dwordx4 v22, s[64:67], s84 offen offset:0 lds\n" \
"v_mfma_f32_16x16x32_bf16 v[56:59], a[180:183], a[128:131], v[56:59]\n" \
"ds_read_b64_tr_b16 a[160:161], v32 offset:0\n" \
"v_cmp_gt_u32_e64 vcc, 16, v214\n" \
"v_mfma_f32_16x16x32_bf16 v[56:59], a[244:247], a[192:195], v[56:59]\n" \
"s_movk_i32 s84, 0x1d80\n" \
"v_mfma_f32_16x16x32_bf16 v[60:63], a[184:187], a[128:131], v[60:63]\n" \
"buffer_load_dwordx4 v23, s[64:67], s84 offen offset:1024 lds\n" \
"v_mfma_f32_16x16x32_bf16 v[60:63], a[248:251], a[192:195], v[60:63]\n" \
"ds_read_b64_tr_b16 a[162:163], v32 offset:1024\n" \
"s_movk_i32 s7, 0xf800\n" \
"v_mfma_f32_16x16x32_bf16 v[64:67], a[188:191], a[128:131], v[64:67]\n" \
"ds_read_b64_tr_b16 a[224:225], v32 offset:8192\n" \
"v_cndmask_b32_e64 v214, v215, v216, vcc\n" \
"v_mfma_f32_16x16x32_bf16 v[64:67], a[252:255], a[192:195], v[64:67]\n" \
"ds_read_b128 a[128:131], v28 offset:16384\n" \
"v_or_b32_e64 v215, 64, v212\n" \
"v_mfma_f32_16x16x32_bf16 v[80:83], a[188:191], a[132:135], v[80:83]\n" \
"ds_read_b128 a[192:195], v29 offset:16384\n" \
"v_mul_u32_u24_e32 v216, 0xe39, v215\n" \
"v_mfma_f32_16x16x32_bf16 v[80:83], a[252:255], a[196:199], v[80:83]\n" \
"s_movk_i32 s84, 0x3980\n" \
"v_mfma_f32_16x16x32_bf16 v[76:79], a[184:187], a[132:135], v[76:79]\n" \
"buffer_load_dwordx4 v22, s[64:67], s84 offen offset:2048 lds\n" \
"v_mfma_f32_16x16x32_bf16 v[76:79], a[248:251], a[196:199], v[76:79]\n" \
"s_movk_i32 s84, 0x5580\n" \
"v_mfma_f32_16x16x32_bf16 v[72:75], a[180:183], a[132:135], v[72:75]\n" \
"buffer_load_dwordx4 v23, s[64:67], s84 offen offset:3072 lds\n" \
"v_mfma_f32_16x16x32_bf16 v[72:75], a[244:247], a[196:199], v[72:75]\n" \
"s_add_u32 m0, s31, 0x8000\n" \
"s_mov_b32 s84, 0x180000\n" \
"v_mfma_f32_16x16x32_bf16 v[68:71], a[176:179], a[132:135], v[68:71]\n" \
"buffer_load_dwordx4 v26, s[68:71], s84 offen offset:0 lds\n" \
"v_mfma_f32_16x16x32_bf16 v[68:71], a[240:243], a[196:199], v[68:71]\n" \
"ds_read_b128 a[132:135], v28 offset:18432\n" \
"v_lshrrev_b32_e64 v216, 16, v216\n" \
"v_mfma_f32_16x16x32_bf16 v[84:87], a[176:179], a[136:139], v[84:87]\n" \
"ds_read_b128 a[196:199], v29 offset:18432\n" \
"v_mad_i32_i24 v215, v216, s6, v215\n" \
"v_mfma_f32_16x16x32_bf16 v[84:87], a[240:243], a[200:203], v[84:87]\n" \
"s_addk_i32 s84, 0x7c00\n" \
"v_mfma_f32_16x16x32_bf16 v[88:91], a[180:183], a[136:139], v[88:91]\n" \
"buffer_load_dwordx4 v26, s[68:71], s84 offen offset:1024 lds\n" \
"v_mfma_f32_16x16x32_bf16 v[88:91], a[244:247], a[200:203], v[88:91]\n" \
"ds_read_b64_tr_b16 a[226:227], v32 offset:9216\n" \
"v_lshlrev_b32_e32 v217, 4, v215\n" \
"v_mfma_f32_16x16x32_bf16 v[92:95], a[184:187], a[136:139], v[92:95]\n" \
"s_addk_i32 s84, 0x7c00\n" \
"v_mfma_f32_16x16x32_bf16 v[92:95], a[248:251], a[200:203], v[92:95]\n" \
"buffer_load_dwordx4 v27, s[68:71], s84 offen offset:2048 lds\n" \
"v_mfma_f32_16x16x32_bf16 v[96:99], a[188:191], a[136:139], v[96:99]\n" \
"ds_read_b64_tr_b16 a[164:165], v33 offset:0\n" \
"v_add_u32_e32 v220, s5, v217\n" \
"v_mfma_f32_16x16x32_bf16 v[96:99], a[252:255], a[200:203], v[96:99]\n" \
"ds_read_b128 a[136:139], v28 offset:20480\n" \
"v_and_b32_e32 v220, 0xf0, v220\n" \
"v_mfma_f32_16x16x32_bf16 v[112:115], a[188:191], a[140:143], v[112:115]\n" \
"ds_read_b128 a[200:203], v29 offset:20480\n" \
"v_cmp_gt_u32_e64 vcc, 16, v215\n" \
"v_mfma_f32_16x16x32_bf16 v[112:115], a[252:255], a[204:207], v[112:115]\n" \
"s_addk_i32 s84, 0x7c00\n" \
"v_mfma_f32_16x16x32_bf16 v[108:111], a[184:187], a[140:143], v[108:111]\n" \
"buffer_load_dwordx4 v27, s[68:71], s84 offen offset:3072 lds\n" \
"v_mfma_f32_16x16x32_bf16 v[108:111], a[248:251], a[204:207], v[108:111]\n" \
"s_add_u32 m0, s30, 0x9000\n" \
"s_mov_b32 s84, 0x8180\n" \
"v_mfma_f32_16x16x32_bf16 v[104:107], a[180:183], a[140:143], v[104:107]\n" \
"buffer_load_dwordx4 v22, s[64:67], s84 offen offset:0 lds\n" \
"v_mfma_f32_16x16x32_bf16 v[104:107], a[244:247], a[204:207], v[104:107]\n" \
"ds_read_b64_tr_b16 a[166:167], v33 offset:1024\n" \
"v_cndmask_b32_e32 v215, v217, v220, vcc\n" \
"v_mfma_f32_16x16x32_bf16 v[100:103], a[176:179], a[140:143], v[100:103]\n" \
"ds_read_b64_tr_b16 a[228:229], v33 offset:8192\n" \
"v_or_b32_e32 v217, 0x80, v212\n" \
"v_mfma_f32_16x16x32_bf16 v[100:103], a[240:243], a[204:207], v[100:103]\n" \
"ds_read_b128 a[140:143], v28 offset:22528\n" \
"v_mul_u32_u24_e32 v220, 0xe39, v217\n" \
"v_mfma_f32_16x16x32_bf16 v[116:119], a[176:179], a[144:147], v[116:119]\n" \
"ds_read_b128 a[204:207], v29 offset:22528\n" \
"v_lshrrev_b32_e64 v220, 16, v220\n" \
"v_mfma_f32_16x16x32_bf16 v[116:119], a[240:243], a[208:211], v[116:119]\n" \
"s_addk_i32 s84, 0x1c00\n" \
"v_mfma_f32_16x16x32_bf16 v[120:123], a[180:183], a[144:147], v[120:123]\n" \
"buffer_load_dwordx4 v23, s[64:67], s84 offen offset:1024 lds\n" \
"v_mfma_f32_16x16x32_bf16 v[120:123], a[244:247], a[208:211], v[120:123]\n" \
"s_addk_i32 s84, 0x1c00\n" \
"v_mfma_f32_16x16x32_bf16 v[124:127], a[184:187], a[144:147], v[124:127]\n" \
"buffer_load_dwordx4 v22, s[64:67], s84 offen offset:2048 lds\n" \
"v_mfma_f32_16x16x32_bf16 v[124:127], a[248:251], a[208:211], v[124:127]\n" \
"s_addk_i32 s84, 0x1c00\n" \
"v_mfma_f32_16x16x32_bf16 v[128:131], a[188:191], a[144:147], v[128:131]\n" \
"buffer_load_dwordx4 v23, s[64:67], s84 offen offset:3072 lds\n" \
"v_mfma_f32_16x16x32_bf16 v[128:131], a[252:255], a[208:211], v[128:131]\n" \
"ds_read_b128 a[144:147], v28 offset:0\n" \
"v_mad_i32_i24 v217, v220, s6, v217\n" \
"v_mfma_f32_16x16x32_bf16 v[144:147], a[188:191], a[148:151], v[144:147]\n" \
"ds_read_b128 a[208:211], v29 offset:0\n" \
"v_lshlrev_b32_e32 v221, 4, v217\n" \
"v_mfma_f32_16x16x32_bf16 v[144:147], a[252:255], a[212:215], v[144:147]\n" \
"s_add_u32 m0, s31, 0xc000\n" \
"s_mov_b32 s84, 0x180000\n" \
"v_mfma_f32_16x16x32_bf16 v[140:143], a[184:187], a[148:151], v[140:143]\n" \
"buffer_load_dwordx4 v26, s[72:75], s84 offen offset:0 lds\n" \
"v_mfma_f32_16x16x32_bf16 v[140:143], a[248:251], a[212:215], v[140:143]\n" \
"ds_read_b64_tr_b16 a[230:231], v33 offset:9216\n" \
"v_add_u32_e64 v222, s5, v221\n" \
"v_mfma_f32_16x16x32_bf16 v[136:139], a[180:183], a[148:151], v[136:139]\n" \
"s_addk_i32 s84, 0x7c00\n" \
"v_mfma_f32_16x16x32_bf16 v[136:139], a[244:247], a[212:215], v[136:139]\n" \
"buffer_load_dwordx4 v26, s[72:75], s84 offen offset:1024 lds\n" \
"v_mfma_f32_16x16x32_bf16 v[132:135], a[176:179], a[148:151], v[132:135]\n" \
"ds_read_b64_tr_b16 a[168:169], v34 offset:0\n" \
"v_and_b32_e32 v222, 0xf0, v222\n" \
"v_mfma_f32_16x16x32_bf16 v[132:135], a[240:243], a[212:215], v[132:135]\n" \
"ds_read_b128 a[148:151], v28 offset:2048\n" \
"v_cmp_gt_u32_e32 vcc, 16, v217\n" \
"v_mfma_f32_16x16x32_bf16 v[148:151], a[176:179], a[152:155], v[148:151]\n" \
"ds_read_b128 a[212:215], v29 offset:2048\n" \
"v_cndmask_b32_e64 v217, v221, v222, vcc\n" \
"v_mfma_f32_16x16x32_bf16 v[148:151], a[240:243], a[216:219], v[148:151]\n" \
"ds_read_b64_tr_b16 a[170:171], v34 offset:1024\n" \
"v_or_b32_e32 v221, 0xc0, v212\n" \
"v_mfma_f32_16x16x32_bf16 v[152:155], a[180:183], a[152:155], v[152:155]\n" \
"ds_read_b64_tr_b16 a[232:233], v34 offset:8192\n" \
"v_mul_u32_u24_e32 v222, 0xe39, v221\n" \
"v_mfma_f32_16x16x32_bf16 v[152:155], a[244:247], a[216:219], v[152:155]\n" \
"ds_read_b64_tr_b16 a[234:235], v34 offset:9216\n" \
"v_lshrrev_b32_e64 v222, 16, v222\n" \
"v_mfma_f32_16x16x32_bf16 v[156:159], a[184:187], a[152:155], v[156:159]\n" \
"ds_read_b64_tr_b16 a[172:173], v35 offset:0\n" \
"v_mad_i32_i24 v221, v222, s6, v221\n" \
"v_mfma_f32_16x16x32_bf16 v[156:159], a[248:251], a[216:219], v[156:159]\n" \
"ds_read_b64_tr_b16 a[174:175], v35 offset:1024\n" \
"v_lshlrev_b32_e64 v223, 4, v221\n" \
"v_mfma_f32_16x16x32_bf16 v[160:163], a[188:191], a[152:155], v[160:163]\n" \
"ds_read_b64_tr_b16 a[236:237], v35 offset:8192\n" \
"v_add_u32_e64 v224, s5, v223\n" \
"v_mfma_f32_16x16x32_bf16 v[160:163], a[252:255], a[216:219], v[160:163]\n" \
"ds_read_b128 a[152:155], v28 offset:4096\n" \
"v_and_b32_e32 v224, 0xf0, v224\n" \
"v_mfma_f32_16x16x32_bf16 v[176:179], a[188:191], a[156:159], v[176:179]\n" \
"ds_read_b128 a[216:219], v29 offset:4096\n" \
"v_cmp_gt_u32_e64 vcc, 16, v221\n" \
"v_mfma_f32_16x16x32_bf16 v[176:179], a[252:255], a[220:223], v[176:179]\n" \
"ds_read_b64_tr_b16 a[238:239], v35 offset:9216\n" \
"v_cndmask_b32_e64 v221, v223, v224, vcc\n" \
"v_mfma_f32_16x16x32_bf16 v[172:175], a[184:187], a[156:159], v[172:175]\n" \
"v_or_b32_e32 v223, 0x100, v212\n" \
"v_mul_u32_u24_e32 v224, 0xe39, v223\n" \
"v_mfma_f32_16x16x32_bf16 v[172:175], a[248:251], a[220:223], v[172:175]\n" \
"v_lshrrev_b32_e64 v224, 16, v224\n" \
"v_mad_i32_i24 v223, v224, s6, v223\n" \
"v_mfma_f32_16x16x32_bf16 v[168:171], a[180:183], a[156:159], v[168:171]\n" \
"v_lshlrev_b32_e32 v225, 4, v223\n" \
"v_add_u32_e32 v226, s5, v225\n" \
"v_mfma_f32_16x16x32_bf16 v[168:171], a[244:247], a[220:223], v[168:171]\n" \
"v_and_b32_e32 v226, 0xf0, v226\n" \
"v_cmp_gt_u32_e64 vcc, 16, v223\n" \
"v_mfma_f32_16x16x32_bf16 v[164:167], a[176:179], a[156:159], v[164:167]\n" \
"s_nop 0\n" \
"v_cndmask_b32_e32 v223, v225, v226, vcc\n" \
"v_or_b32_e32 v225, 0x140, v212\n" \
"v_mfma_f32_16x16x32_bf16 v[164:167], a[240:243], a[220:223], v[164:167]\n" \
"ds_read_b128 a[156:159], v28 offset:6144\n" \
"ds_read_b128 a[220:223], v29 offset:6144\n" \
"s_waitcnt lgkmcnt(15)\n" \
"v_mfma_f32_16x16x32_bf16 a[0:3], a[160:163], a[128:131], a[0:3]\n" \
"s_addk_i32 s84, 0x7c00\n" \
"v_mfma_f32_16x16x32_bf16 a[0:3], a[224:227], a[192:195], a[0:3]\n" \
"buffer_load_dwordx4 v27, s[72:75], s84 offen offset:2048 lds\n" \
"v_mfma_f32_16x16x32_bf16 a[4:7], a[164:167], a[128:131], a[4:7]\n" \
"ds_read_b64_tr_b16 a[176:177], v36 offset:0\n" \
"v_mul_u32_u24_e32 v226, 0xe39, v225\n" \
"s_waitcnt lgkmcnt(15)\n" \
"v_mfma_f32_16x16x32_bf16 a[4:7], a[228:231], a[192:195], a[4:7]\n" \
"s_addk_i32 s84, 0x7c00\n" \
"s_waitcnt lgkmcnt(11)\n" \
"v_mfma_f32_16x16x32_bf16 a[8:11], a[168:171], a[128:131], a[8:11]\n" \
"buffer_load_dwordx4 v27, s[72:75], s84 offen offset:3072 lds\n" \
"s_waitcnt lgkmcnt(9)\n" \
"v_mfma_f32_16x16x32_bf16 a[8:11], a[232:235], a[192:195], a[8:11]\n" \
"ds_read_b64_tr_b16 a[178:179], v36 offset:1024\n" \
"v_lshrrev_b32_e32 v226, 16, v226\n" \
"s_waitcnt lgkmcnt(8)\n" \
"v_mfma_f32_16x16x32_bf16 a[12:15], a[172:175], a[128:131], a[12:15]\n" \
"ds_read_b64_tr_b16 a[240:241], v36 offset:8192\n" \
"v_mad_i32_i24 v225, v226, s6, v225\n" \
"s_waitcnt lgkmcnt(5)\n" \
"v_mfma_f32_16x16x32_bf16 a[12:15], a[236:239], a[192:195], a[12:15]\n" \
"v_lshlrev_b32_e32 v227, 4, v225\n" \
"v_add_u32_e64 v228, s5, v227\n" \
"v_mfma_f32_16x16x32_bf16 a[28:31], a[172:175], a[132:135], a[28:31]\n" \
"v_and_b32_e32 v228, 0xf0, v228\n" \
"v_cmp_gt_u32_e64 vcc, 16, v225\n" \
"v_mfma_f32_16x16x32_bf16 a[28:31], a[236:239], a[196:199], a[28:31]\n" \
"ds_read_b64_tr_b16 a[242:243], v36 offset:9216\n" \
"v_cndmask_b32_e64 v225, v227, v228, vcc\n" \
"v_mfma_f32_16x16x32_bf16 a[24:27], a[168:171], a[132:135], a[24:27]\n" \
"buffer_load_dwordx4 v[200:203], v192, s[76:79], s34 offen offset:0\n" \
"v_mfma_f32_16x16x32_bf16 a[24:27], a[232:235], a[196:199], a[24:27]\n" \
"v_or_b32_e32 v227, 0x180, v212\n" \
"v_mul_u32_u24_e32 v228, 0xe39, v227\n" \
"v_mfma_f32_16x16x32_bf16 a[20:23], a[164:167], a[132:135], a[20:23]\n" \
"v_lshrrev_b32_e64 v228, 16, v228\n" \
"v_mad_i32_i24 v227, v228, s6, v227\n" \
"v_mfma_f32_16x16x32_bf16 a[20:23], a[228:231], a[196:199], a[20:23]\n" \
"ds_read_b64_tr_b16 a[180:181], v37 offset:0\n" \
"v_lshlrev_b32_e64 v229, 4, v227\n" \
"v_mfma_f32_16x16x32_bf16 a[16:19], a[160:163], a[132:135], a[16:19]\n" \
"v_add_u32_e64 v230, s5, v229\n" \
"v_and_b32_e32 v230, 0xf0, v230\n" \
"v_mfma_f32_16x16x32_bf16 a[16:19], a[224:227], a[196:199], a[16:19]\n" \
"v_cmp_gt_u32_e32 vcc, 16, v227\n" \
"s_nop 1\n" \
"v_cndmask_b32_e64 v227, v229, v230, vcc\n" \
"v_mfma_f32_16x16x32_bf16 a[32:35], a[160:163], a[136:139], a[32:35]\n" \
"buffer_load_dwordx4 v[204:207], v193, s[76:79], s34 offen offset:0\n" \
"v_mfma_f32_16x16x32_bf16 a[32:35], a[224:227], a[200:203], a[32:35]\n" \
"s_mov_b32 m0, s32\n" \
"ds_read_b64_tr_b16 a[182:183], v37 offset:1024\n" \
"v_mfma_f32_16x16x32_bf16 a[36:39], a[164:167], a[136:139], a[36:39]\n" \
"s_mov_b32 exec_lo, 0x0\n" \
"s_mov_b32 exec_hi, 0xff00\n" \
"global_load_lds_dword v[184:185], off\n" \
"s_mov_b64 exec, -1\n" \
"v_mfma_f32_16x16x32_bf16 a[36:39], a[228:231], a[200:203], a[36:39]\n" \
"ds_read_b64_tr_b16 a[244:245], v37 offset:8192\n" \
"v_or_b32_e32 v229, 0x1c0, v212\n" \
"v_mfma_f32_16x16x32_bf16 a[40:43], a[168:171], a[136:139], a[40:43]\n" \
"v_mul_u32_u24_e32 v230, 0xe39, v229\n" \
"v_lshrrev_b32_e32 v230, 16, v230\n" \
"v_mfma_f32_16x16x32_bf16 a[40:43], a[232:235], a[200:203], a[40:43]\n" \
"buffer_load_dword v208, v194, s[76:79], s34 offen offset:32\n" \
"v_mfma_f32_16x16x32_bf16 a[44:47], a[172:175], a[136:139], a[44:47]\n" \
"ds_read_b64_tr_b16 a[246:247], v37 offset:9216\n" \
"v_mad_i32_i24 v229, v230, s6, v229\n" \
"v_mfma_f32_16x16x32_bf16 a[44:47], a[236:239], a[200:203], a[44:47]\n" \
"v_lshlrev_b32_e32 v231, 4, v229\n" \
"v_add_u32_e32 v232, s5, v231\n" \
"v_mfma_f32_16x16x32_bf16 a[60:63], a[172:175], a[140:143], a[60:63]\n" \
"v_and_b32_e32 v232, 0xf0, v232\n" \
"v_cmp_gt_u32_e64 vcc, 16, v229\n" \
"v_mfma_f32_16x16x32_bf16 a[60:63], a[236:239], a[204:207], a[60:63]\n" \
"s_nop 0\n" \
"v_cndmask_b32_e32 v229, v231, v232, vcc\n" \
"v_or_b32_e32 v231, 0x200, v212\n" \
"v_mfma_f32_16x16x32_bf16 a[56:59], a[168:171], a[140:143], a[56:59]\n" \
"buffer_load_dword v209, v194, s[76:79], s34 offen offset:1184\n" \
"v_mfma_f32_16x16x32_bf16 a[56:59], a[232:235], a[204:207], a[56:59]\n" \
"ds_read_b64_tr_b16 a[184:185], v38 offset:0\n" \
"v_mul_u32_u24_e32 v232, 0xe39, v231\n" \
"v_mfma_f32_16x16x32_bf16 a[52:55], a[164:167], a[140:143], a[52:55]\n" \
"v_lshrrev_b32_e64 v232, 16, v232\n" \
"v_mad_i32_i24 v231, v232, s6, v231\n" \
"v_mfma_f32_16x16x32_bf16 a[52:55], a[228:231], a[204:207], a[52:55]\n" \
"ds_read_b64_tr_b16 a[186:187], v38 offset:1024\n" \
"v_lshlrev_b32_e64 v233, 4, v231\n" \
"v_mfma_f32_16x16x32_bf16 a[48:51], a[160:163], a[140:143], a[48:51]\n" \
"v_add_u32_e32 v234, s5, v233\n" \
"s_movk_i32 s5, 0x480\n" \
"v_mfma_f32_16x16x32_bf16 a[48:51], a[224:227], a[204:207], a[48:51]\n" \
"v_mad_u32_u24 v0, v213, s5, v214\n" \
"v_mul_u32_u24_e32 v213, 0x480, v216\n" \
"v_mfma_f32_16x16x32_bf16 a[64:67], a[160:163], a[144:147], a[64:67]\n" \
"buffer_load_dword v211, v194, s[76:79], s34 offen offset:2336\n" \
"v_mfma_f32_16x16x32_bf16 a[64:67], a[224:227], a[208:211], a[64:67]\n" \
"ds_read_b64_tr_b16 a[248:249], v38 offset:8192\n" \
"s_movk_i32 s6, 0xfc00\n" \
"v_mfma_f32_16x16x32_bf16 a[68:71], a[164:167], a[144:147], a[68:71]\n" \
"v_add3_u32 v1, v213, v215, s6\n" \
"v_mul_u32_u24_e32 v213, 0x480, v220\n" \
"v_mfma_f32_16x16x32_bf16 a[68:71], a[228:231], a[208:211], a[68:71]\n" \
"ds_read_b64_tr_b16 a[250:251], v38 offset:9216\n" \
"v_add3_u32 v2, v213, v217, s7\n" \
"v_mfma_f32_16x16x32_bf16 a[72:75], a[168:171], a[144:147], a[72:75]\n" \
"v_mul_u32_u24_e32 v213, 0x480, v222\n" \
"v_add3_u32 v3, v213, v221, s40\n" \
"v_mfma_f32_16x16x32_bf16 a[72:75], a[232:235], a[208:211], a[72:75]\n" \
"s_mov_b32 s76, s20\n" \
"s_and_b32 s77, s21, 0xffff\n" \
"v_mfma_f32_16x16x32_bf16 a[76:79], a[172:175], a[144:147], a[76:79]\n" \
"s_mov_b32 s78, 0x480\n" \
"v_mul_u32_u24_e32 v213, 0x480, v226\n" \
"v_mfma_f32_16x16x32_bf16 a[76:79], a[236:239], a[208:211], a[76:79]\n" \
"buffer_load_ushort v210, v195, s[76:79], 0 offen\n" \
"v_mfma_f32_16x16x32_bf16 a[92:95], a[172:175], a[148:151], a[92:95]\n" \
"ds_read_b64_tr_b16 a[188:189], v39 offset:0\n" \
"v_add3_u32 v5, v213, v225, s6\n" \
"v_mfma_f32_16x16x32_bf16 a[92:95], a[236:239], a[212:215], a[92:95]\n" \
"v_mul_u32_u24_e32 v213, 0x480, v228\n" \
"v_add3_u32 v6, v213, v227, s7\n" \
"v_mfma_f32_16x16x32_bf16 a[88:91], a[168:171], a[148:151], a[88:91]\n" \
"s_mov_b32 s76, s8\n" \
"s_and_b32 s77, s9, 0xffff\n" \
"v_mfma_f32_16x16x32_bf16 a[88:91], a[232:235], a[212:215], a[88:91]\n" \
"s_mov_b32 s78, 0x240000\n" \
"ds_read_b64_tr_b16 a[190:191], v39 offset:1024\n" \
"v_mfma_f32_16x16x32_bf16 a[84:87], a[164:167], a[148:151], a[84:87]\n" \
"v_mul_u32_u24_e32 v213, 0x480, v230\n" \
"v_add3_u32 v7, v213, v229, s40\n" \
"v_mfma_f32_16x16x32_bf16 a[84:87], a[228:231], a[212:215], a[84:87]\n" \
"ds_read_b64_tr_b16 a[252:253], v39 offset:8192\n" \
"s_lshl_b32 s7, s3, 3\n" \
"v_mfma_f32_16x16x32_bf16 a[80:83], a[160:163], a[148:151], a[80:83]\n" \
"s_lshl_b32 s40, s3, 1\n" \
"s_and_b32 s7, s7, 16\n" \
"v_mfma_f32_16x16x32_bf16 a[80:83], a[224:227], a[212:215], a[80:83]\n" \
"s_and_b32 s40, s40, 2\n" \
"s_and_b32 s6, s2, 7\n" \
"v_mfma_f32_16x16x32_bf16 a[96:99], a[160:163], a[152:155], a[96:99]\n" \
"ds_read_b64_tr_b16 a[254:255], v39 offset:9216\n" \
"v_bfe_u32 v213, v20, 7, 1\n" \
"v_mfma_f32_16x16x32_bf16 a[96:99], a[224:227], a[216:219], a[96:99]\n" \
"s_or_b32 s7, s40, s7\n" \
".fill (L_pb + 7904 - .) / 4, 4, 0xBF800000\n" \
"L_pp2:\n" \
"s_cbranch_execz L_pret\n" \
"v_and_b32_e32 v234, 0xf0, v234\n" \
"v_mfma_f32_16x16x32_bf16 a[100:103], a[164:167], a[152:155], a[100:103]\n" \
"v_cmp_gt_u32_e32 vcc, 16, v231\n" \
"v_or3_b32 v213, s7, v213, v219\n" \
"v_mfma_f32_16x16x32_bf16 a[100:103], a[228:231], a[216:219], a[100:103]\n" \
"v_and_b32_e32 v214, 32, v20\n" \
"s_lshl_b32 s6, s6, 5\n" \
"v_mfma_f32_16x16x32_bf16 a[104:107], a[168:171], a[152:155], a[104:107]\n" \
"v_cndmask_b32_e32 v231, v233, v234, vcc\n" \
"v_mul_u32_u24_e32 v213, 18, v213\n" \
"v_mfma_f32_16x16x32_bf16 a[104:107], a[232:235], a[216:219], a[104:107]\n" \
"v_mov_b32_e32 v215, 0x100\n" \
"v_mov_b32_e64 v216, s6\n" \
"v_mfma_f32_16x16x32_bf16 a[108:111], a[172:175], a[152:155], a[108:111]\n" \
"v_cmp_eq_u32_e64 vcc, 0, v214\n" \
"v_and_or_b32 v213, v218, 1, v213\n" \
"v_mfma_f32_16x16x32_bf16 a[108:111], a[236:239], a[216:219], a[108:111]\n" \
"v_lshlrev_b32_e32 v212, 3, v212\n" \
"v_cndmask_b32_e32 v214, v215, v216, vcc\n" \
"s_waitcnt lgkmcnt(15)\n" \
"v_mfma_f32_16x16x32_bf16 a[124:127], a[172:175], a[156:159], a[124:127]\n" \
"v_mad_u32_u24 v213, v213, s5, v214\n" \
"v_and_or_b32 v213, v20, 16, v213\n" \
"v_mfma_f32_16x16x32_bf16 a[124:127], a[236:239], a[220:223], a[124:127]\n" \
"v_add_u32_e32 v9, 0x4800, v213\n" \
"v_mul_u32_u24_e32 v213, 0x120, v218\n" \
"v_mfma_f32_16x16x32_bf16 a[120:123], a[168:171], a[156:159], a[120:123]\n" \
"v_and_or_b32 v10, v212, 24, v213\n" \
"v_lshrrev_b32_e32 v213, 2, v20\n" \
"v_mfma_f32_16x16x32_bf16 a[120:123], a[232:235], a[220:223], a[120:123]\n" \
"v_and_or_b32 v213, v213, 60, s4\n" \
"v_lshl_or_b32 v212, s3, 9, v212\n" \
"v_mfma_f32_16x16x32_bf16 a[116:119], a[164:167], a[156:159], a[116:119]\n" \
"v_mad_u32_u24 v4, v224, s5, v223\n" \
"v_mad_u32_u24 v8, v232, s5, v231\n" \
"v_mfma_f32_16x16x32_bf16 a[116:119], a[228:231], a[220:223], a[116:119]\n" \
"v_or_b32_e32 v18, 0x27000, v213\n" \
"v_or_b32_e32 v19, 0x27800, v212\n" \
"v_mfma_f32_16x16x32_bf16 a[112:115], a[160:163], a[156:159], a[112:115]\n" \
"v_mov_b32_e32 v215, 0x1000c0c\n" \
"v_mov_b32_e32 v216, 0xc0c0100\n" \
"v_mfma_f32_16x16x32_bf16 a[112:115], a[224:227], a[220:223], a[112:115]\n" \
"s_waitcnt vmcnt(7) lgkmcnt(0)\n" \
"s_barrier\n" \
"v_mfma_f32_16x16x32_bf16 v[52:55], a[176:179], a[128:131], v[52:55]\n" \
"s_add_u32 m0, s30, 0x0\n" \
"s_movk_i32 s84, 0x200\n" \
"v_mfma_f32_16x16x32_bf16 v[52:55], a[240:243], a[192:195], v[52:55]\n" \
"buffer_load_dwordx4 v22, s[64:67], s84 offen offset:0 lds\n" \
"v_mfma_f32_16x16x32_bf16 v[56:59], a[180:183], a[128:131], v[56:59]\n" \
"ds_read_b64_tr_b16 a[160:161], v32 offset:32768\n" \
"v_bfe_u32 v214, v20, 4, 1\n" \
"v_mfma_f32_16x16x32_bf16 v[56:59], a[244:247], a[192:195], v[56:59]\n" \
"s_movk_i32 s84, 0x1e00\n" \
"v_mfma_f32_16x16x32_bf16 v[60:63], a[184:187], a[128:131], v[60:63]\n" \
"buffer_load_dwordx4 v23, s[64:67], s84 offen offset:1024 lds\n" \
"v_mfma_f32_16x16x32_bf16 v[60:63], a[248:251], a[192:195], v[60:63]\n" \
"ds_read_b64_tr_b16 a[162:163], v32 offset:33792\n" \
"v_lshrrev_b32_e32 v212, 8, v20\n" \
"v_mfma_f32_16x16x32_bf16 v[64:67], a[188:191], a[128:131], v[64:67]\n" \
"ds_read_b64_tr_b16 a[224:225], v32 offset:40960\n" \
"v_cmp_eq_u32_e64 vcc, 0, v214\n" \
"v_mfma_f32_16x16x32_bf16 v[64:67], a[252:255], a[192:195], v[64:67]\n" \
"ds_read_b128 a[128:131], v28 offset:49152\n" \
"v_bfe_u32 v213, v20, 4, 4\n" \
"v_mfma_f32_16x16x32_bf16 v[80:83], a[188:191], a[132:135], v[80:83]\n" \
"ds_read_b128 a[192:195], v29 offset:49152\n" \
"v_mov_b32_e32 v217, 0xc0c0302\n" \
"v_mfma_f32_16x16x32_bf16 v[80:83], a[252:255], a[196:199], v[80:83]\n" \
"s_movk_i32 s84, 0x3a00\n" \
"v_mfma_f32_16x16x32_bf16 v[76:79], a[184:187], a[132:135], v[76:79]\n" \
"buffer_load_dwordx4 v22, s[64:67], s84 offen offset:2048 lds\n" \
"v_mfma_f32_16x16x32_bf16 v[76:79], a[248:251], a[196:199], v[76:79]\n" \
"s_movk_i32 s84, 0x5600\n" \
"v_mfma_f32_16x16x32_bf16 v[72:75], a[180:183], a[132:135], v[72:75]\n" \
"buffer_load_dwordx4 v23, s[64:67], s84 offen offset:3072 lds\n" \
"v_mfma_f32_16x16x32_bf16 v[72:75], a[244:247], a[196:199], v[72:75]\n" \
"s_add_u32 m0, s31, 0x0\n" \
"s_mov_b32 s84, 0x200000\n" \
"v_mfma_f32_16x16x32_bf16 v[68:71], a[176:179], a[132:135], v[68:71]\n" \
"buffer_load_dwordx4 v26, s[68:71], s84 offen offset:0 lds\n" \
"v_mfma_f32_16x16x32_bf16 v[68:71], a[240:243], a[196:199], v[68:71]\n" \
"ds_read_b128 a[132:135], v28 offset:51200\n" \
"v_cndmask_b32_e32 v215, v215, v216, vcc\n" \
"v_mfma_f32_16x16x32_bf16 v[84:87], a[176:179], a[136:139], v[84:87]\n" \
"ds_read_b128 a[196:199], v29 offset:51200\n" \
"v_mov_b32_e32 v216, 0x3020c0c\n" \
"v_mfma_f32_16x16x32_bf16 v[84:87], a[240:243], a[200:203], v[84:87]\n" \
"s_addk_i32 s84, 0x7c00\n" \
"v_mfma_f32_16x16x32_bf16 v[88:91], a[180:183], a[136:139], v[88:91]\n" \
"buffer_load_dwordx4 v26, s[68:71], s84 offen offset:1024 lds\n" \
"v_mfma_f32_16x16x32_bf16 v[88:91], a[244:247], a[200:203], v[88:91]\n" \
"ds_read_b64_tr_b16 a[226:227], v32 offset:41984\n" \
"v_lshl_or_b32 v214, v212, 2, v214\n" \
"v_mfma_f32_16x16x32_bf16 v[92:95], a[184:187], a[136:139], v[92:95]\n" \
"s_addk_i32 s84, 0x7c00\n" \
"v_mfma_f32_16x16x32_bf16 v[92:95], a[248:251], a[200:203], v[92:95]\n" \
"buffer_load_dwordx4 v27, s[68:71], s84 offen offset:2048 lds\n" \
"v_mfma_f32_16x16x32_bf16 v[96:99], a[188:191], a[136:139], v[96:99]\n" \
"ds_read_b64_tr_b16 a[164:165], v33 offset:32768\n" \
"v_cndmask_b32_e64 v216, v216, v217, vcc\n" \
"v_mfma_f32_16x16x32_bf16 v[96:99], a[252:255], a[200:203], v[96:99]\n" \
"ds_read_b128 a[136:139], v28 offset:53248\n" \
"v_mov_b32_e32 v217, 0xc0c0c0c\n" \
"v_mfma_f32_16x16x32_bf16 v[112:115], a[188:191], a[140:143], v[112:115]\n" \
"ds_read_b128 a[200:203], v29 offset:53248\n" \
"v_cmp_gt_u32_e64 vcc, v214, v213\n" \
"v_mfma_f32_16x16x32_bf16 v[112:115], a[252:255], a[204:207], v[112:115]\n" \
"s_addk_i32 s84, 0x7c00\n" \
"v_mfma_f32_16x16x32_bf16 v[108:111], a[184:187], a[140:143], v[108:111]\n" \
"buffer_load_dwordx4 v27, s[68:71], s84 offen offset:3072 lds\n" \
"v_mfma_f32_16x16x32_bf16 v[108:111], a[248:251], a[204:207], v[108:111]\n" \
"s_add_u32 m0, s30, 0x1000\n" \
"s_mov_b32 s84, 0x8200\n" \
"v_mfma_f32_16x16x32_bf16 v[104:107], a[180:183], a[140:143], v[104:107]\n" \
"buffer_load_dwordx4 v22, s[64:67], s84 offen offset:0 lds\n" \
"v_mfma_f32_16x16x32_bf16 v[104:107], a[244:247], a[204:207], v[104:107]\n" \
"ds_read_b64_tr_b16 a[166:167], v33 offset:33792\n" \
"v_or_b32_e32 v214, 2, v214\n" \
"v_mfma_f32_16x16x32_bf16 v[100:103], a[176:179], a[140:143], v[100:103]\n" \
"ds_read_b64_tr_b16 a[228:229], v33 offset:40960\n" \
"s_lshr_b32 s4, s2, 5\n" \
"v_mfma_f32_16x16x32_bf16 v[100:103], a[240:243], a[204:207], v[100:103]\n" \
"ds_read_b128 a[140:143], v28 offset:55296\n" \
"v_cndmask_b32_e32 v40, v217, v215, vcc\n" \
"v_mfma_f32_16x16x32_bf16 v[116:119], a[176:179], a[144:147], v[116:119]\n" \
"ds_read_b128 a[204:207], v29 offset:55296\n" \
"v_cndmask_b32_e64 v42, v215, v217, vcc\n" \
"v_mfma_f32_16x16x32_bf16 v[116:119], a[240:243], a[208:211], v[116:119]\n" \
"s_addk_i32 s84, 0x1c00\n" \
"v_mfma_f32_16x16x32_bf16 v[120:123], a[180:183], a[144:147], v[120:123]\n" \
"buffer_load_dwordx4 v23, s[64:67], s84 offen offset:1024 lds\n" \
"v_mfma_f32_16x16x32_bf16 v[120:123], a[244:247], a[208:211], v[120:123]\n" \
"s_addk_i32 s84, 0x1c00\n" \
"v_mfma_f32_16x16x32_bf16 v[124:127], a[184:187], a[144:147], v[124:127]\n" \
"buffer_load_dwordx4 v22, s[64:67], s84 offen offset:2048 lds\n" \
"v_mfma_f32_16x16x32_bf16 v[124:127], a[248:251], a[208:211], v[124:127]\n" \
"s_addk_i32 s84, 0x1c00\n" \
"v_mfma_f32_16x16x32_bf16 v[128:131], a[188:191], a[144:147], v[128:131]\n" \
"buffer_load_dwordx4 v23, s[64:67], s84 offen offset:3072 lds\n" \
"v_mfma_f32_16x16x32_bf16 v[128:131], a[252:255], a[208:211], v[128:131]\n" \
"ds_read_b128 a[144:147], v28 offset:32768\n" \
"v_cmp_gt_u32_e32 vcc, v214, v213\n" \
"v_mfma_f32_16x16x32_bf16 v[144:147], a[188:191], a[148:151], v[144:147]\n" \
"ds_read_b128 a[208:211], v29 offset:32768\n" \
"v_and_b32_e64 v214, 16, v20\n" \
"v_mfma_f32_16x16x32_bf16 v[144:147], a[252:255], a[212:215], v[144:147]\n" \
"s_add_u32 m0, s31, 0x4000\n" \
"s_mov_b32 s84, 0x200000\n" \
"v_mfma_f32_16x16x32_bf16 v[140:143], a[184:187], a[148:151], v[140:143]\n" \
"buffer_load_dwordx4 v26, s[72:75], s84 offen offset:0 lds\n" \
"v_mfma_f32_16x16x32_bf16 v[140:143], a[248:251], a[212:215], v[140:143]\n" \
"ds_read_b64_tr_b16 a[230:231], v33 offset:41984\n" \
"s_movk_i32 s42, 0x3f80\n" \
"v_mfma_f32_16x16x32_bf16 v[136:139], a[180:183], a[148:151], v[136:139]\n" \
"s_addk_i32 s84, 0x7c00\n" \
"v_mfma_f32_16x16x32_bf16 v[136:139], a[244:247], a[212:215], v[136:139]\n" \
"buffer_load_dwordx4 v26, s[72:75], s84 offen offset:1024 lds\n" \
"v_mfma_f32_16x16x32_bf16 v[132:135], a[176:179], a[148:151], v[132:135]\n" \
"ds_read_b64_tr_b16 a[168:169], v34 offset:32768\n" \
"s_xor_b32 s5, s4, 15\n" \
"s_nop 0\n" \
"v_mfma_f32_16x16x32_bf16 v[132:135], a[240:243], a[212:215], v[132:135]\n" \
"ds_read_b128 a[148:151], v28 offset:34816\n" \
"v_mfma_f32_16x16x32_bf16 v[148:151], a[176:179], a[152:155], v[148:151]\n" \
"ds_read_b128 a[212:215], v29 offset:34816\n" \
"v_mfma_f32_16x16x32_bf16 v[148:151], a[240:243], a[216:219], v[148:151]\n" \
"ds_read_b64_tr_b16 a[170:171], v34 offset:33792\n" \
"v_mfma_f32_16x16x32_bf16 v[152:155], a[180:183], a[152:155], v[152:155]\n" \
"ds_read_b64_tr_b16 a[232:233], v34 offset:40960\n" \
"v_mfma_f32_16x16x32_bf16 v[152:155], a[244:247], a[216:219], v[152:155]\n" \
"ds_read_b64_tr_b16 a[234:235], v34 offset:41984\n" \
"v_mfma_f32_16x16x32_bf16 v[156:159], a[184:187], a[152:155], v[156:159]\n" \
"ds_read_b64_tr_b16 a[172:173], v35 offset:32768\n" \
"v_mfma_f32_16x16x32_bf16 v[156:159], a[248:251], a[216:219], v[156:159]\n" \
"ds_read_b64_tr_b16 a[174:175], v35 offset:33792\n" \
"v_mfma_f32_16x16x32_bf16 v[160:163], a[188:191], a[152:155], v[160:163]\n" \
"ds_read_b64_tr_b16 a[236:237], v35 offset:40960\n" \
"v_mfma_f32_16x16x32_bf16 v[160:163], a[252:255], a[216:219], v[160:163]\n" \
"ds_read_b128 a[152:155], v28 offset:36864\n" \
"v_mfma_f32_16x16x32_bf16 v[176:179], a[188:191], a[156:159], v[176:179]\n" \
"ds_read_b128 a[216:219], v29 offset:36864\n" \
"v_mfma_f32_16x16x32_bf16 v[176:179], a[252:255], a[220:223], v[176:179]\n" \
"ds_read_b64_tr_b16 a[238:239], v35 offset:41984\n" \
"v_mfma_f32_16x16x32_bf16 v[172:175], a[184:187], a[156:159], v[172:175]\n" \
"s_cmpk_lt_u32 s3, 2\n" \
"s_cselect_b32 s6, s5, s4\n" \
"v_mfma_f32_16x16x32_bf16 v[172:175], a[248:251], a[220:223], v[172:175]\n" \
"s_lshl_b32 s7, s6, 7\n" \
"v_and_b32_e32 v213, 0x320, v20\n" \
"v_mfma_f32_16x16x32_bf16 v[168:171], a[180:183], a[156:159], v[168:171]\n" \
"s_movk_i32 s41, 0x100\n" \
"v_lshlrev_b32_e64 v214, v214, s42\n" \
"v_mfma_f32_16x16x32_bf16 v[168:171], a[244:247], a[220:223], v[168:171]\n" \
"v_mov_b32_e32 v215, 0\n" \
"s_add_i32 s40, s7, -16\n" \
"v_mfma_f32_16x16x32_bf16 v[164:167], a[176:179], a[156:159], v[164:167]\n" \
"v_cndmask_b32_e32 v41, v217, v216, vcc\n" \
"v_cndmask_b32_e32 v43, v216, v217, vcc\n" \
"v_mfma_f32_16x16x32_bf16 v[164:167], a[240:243], a[220:223], v[164:167]\n" \
"ds_read_b128 a[156:159], v28 offset:38912\n" \
"ds_read_b128 a[220:223], v29 offset:38912\n" \
"s_waitcnt lgkmcnt(15)\n" \
"v_mfma_f32_16x16x32_bf16 a[0:3], a[160:163], a[128:131], a[0:3]\n" \
"s_addk_i32 s84, 0x7c00\n" \
"v_mfma_f32_16x16x32_bf16 a[0:3], a[224:227], a[192:195], a[0:3]\n" \
"buffer_load_dwordx4 v27, s[72:75], s84 offen offset:2048 lds\n" \
"v_mfma_f32_16x16x32_bf16 a[4:7], a[164:167], a[128:131], a[4:7]\n" \
"ds_read_b64_tr_b16 a[176:177], v36 offset:32768\n" \
"v_cmp_eq_u32_e32 vcc, s41, v213\n" \
"s_waitcnt lgkmcnt(15)\n" \
"v_mfma_f32_16x16x32_bf16 a[4:7], a[228:231], a[192:195], a[4:7]\n" \
"s_addk_i32 s84, 0x7c00\n" \
"s_waitcnt lgkmcnt(11)\n" \
"v_mfma_f32_16x16x32_bf16 a[8:11], a[168:171], a[128:131], a[8:11]\n" \
"buffer_load_dwordx4 v27, s[72:75], s84 offen offset:3072 lds\n" \
"s_waitcnt lgkmcnt(9)\n" \
"v_mfma_f32_16x16x32_bf16 a[8:11], a[232:235], a[192:195], a[8:11]\n" \
"ds_read_b64_tr_b16 a[178:179], v36 offset:33792\n" \
"s_mov_b32 s41, 0\n" \
"s_waitcnt lgkmcnt(8)\n" \
"v_mfma_f32_16x16x32_bf16 a[12:15], a[172:175], a[128:131], a[12:15]\n" \
"ds_read_b64_tr_b16 a[240:241], v36 offset:40960\n" \
"s_and_b32 s43, s3, 1\n" \
"s_waitcnt lgkmcnt(5)\n" \
"v_mfma_f32_16x16x32_bf16 a[12:15], a[236:239], a[192:195], a[12:15]\n" \
"v_cndmask_b32_e32 v46, v215, v214, vcc\n" \
"v_and_b32_e32 v213, 0x330, v20\n" \
"v_mfma_f32_16x16x32_bf16 a[28:31], a[172:175], a[132:135], a[28:31]\n" \
"s_cmp_lg_u32 s7, 0\n" \
"s_cselect_b32 s40, s40, s41\n" \
"v_mfma_f32_16x16x32_bf16 a[28:31], a[236:239], a[196:199], a[28:31]\n" \
"ds_read_b64_tr_b16 a[242:243], v36 offset:41984\n" \
"s_mov_b32 s42, 0xff800000\n" \
"v_mfma_f32_16x16x32_bf16 a[24:27], a[168:171], a[132:135], a[24:27]\n" \
"s_cmp_lg_u32 s7, 0\n" \
"s_cselect_b32 s47, s41, s42\n" \
"v_mfma_f32_16x16x32_bf16 a[24:27], a[232:235], a[196:199], a[24:27]\n" \
"s_lshl_b32 s41, s43, 6\n" \
"v_lshrrev_b32_e32 v215, 6, v20\n" \
"v_mfma_f32_16x16x32_bf16 a[20:23], a[164:167], a[132:135], a[20:23]\n" \
"v_mov_b32_e32 v214, 0x3f80\n" \
"v_cmp_eq_u32_e64 vcc, 32, v213\n" \
"v_mfma_f32_16x16x32_bf16 a[20:23], a[228:231], a[196:199], a[20:23]\n" \
"ds_read_b64_tr_b16 a[180:181], v37 offset:32768\n" \
"s_or_b32 s7, s41, s7\n" \
"v_mfma_f32_16x16x32_bf16 a[16:19], a[160:163], a[132:135], a[16:19]\n" \
"v_lshrrev_b32_e32 v213, 7, v20\n" \
"v_bfe_u32 v216, v20, 6, 2\n" \
"v_mfma_f32_16x16x32_bf16 a[16:19], a[224:227], a[196:199], a[16:19]\n" \
"v_lshrrev_b32_e64 v219, 1, v20\n" \
"v_bfe_u32 v215, v215, 1, 1\n" \
"v_mfma_f32_16x16x32_bf16 a[32:35], a[160:163], a[136:139], a[32:35]\n" \
"s_waitcnt vmcnt(16)\n" \
"v_or_b32_e32 v204, v210, v204\n" \
"v_or_b32_e64 v206, v210, v206\n" \
"v_mfma_f32_16x16x32_bf16 a[32:35], a[224:227], a[200:203], a[32:35]\n" \
"v_mov_b32_e64 v210, 0\n" \
"s_add_u32 s88, s35, 0x1000\n" \
"v_mfma_f32_16x16x32_bf16 a[36:39], a[164:167], a[136:139], a[36:39]\n" \
"ds_read_b64_tr_b16 a[182:183], v37 offset:33792\n" \
"v_cndmask_b32_e64 v47, 0, v214, vcc\n" \
"v_mfma_f32_16x16x32_bf16 a[36:39], a[228:231], a[200:203], a[36:39]\n" \
"ds_read_b64_tr_b16 a[244:245], v37 offset:40960\n" \
"v_or_b32_e64 v214, s7, v213\n" \
"v_mfma_f32_16x16x32_bf16 a[40:43], a[168:171], a[136:139], a[40:43]\n" \
"s_mov_b32 m0, s32\n" \
"v_lshl_or_b32 v217, s43, 3, v213\n" \
"v_mfma_f32_16x16x32_bf16 a[40:43], a[232:235], a[200:203], a[40:43]\n" \
"s_mov_b32 exec_lo, 0xffffffff\n" \
"s_mov_b32 exec_hi, 0x0\n" \
"global_load_lds_dword v[188:189], off\n" \
"s_mov_b64 exec, -1\n" \
"v_mfma_f32_16x16x32_bf16 a[44:47], a[172:175], a[136:139], a[44:47]\n" \
"ds_read_b64_tr_b16 a[246:247], v37 offset:41984\n" \
"v_lshlrev_b32_e64 v218, 10, v212\n" \
"v_mfma_f32_16x16x32_bf16 a[44:47], a[236:239], a[200:203], a[44:47]\n" \
"v_lshlrev_b32_e32 v216, 7, v216\n" \
"v_and_b32_e32 v219, 24, v219\n" \
"v_mfma_f32_16x16x32_bf16 a[60:63], a[172:175], a[140:143], a[60:63]\n" \
"ds_write_b128 v196, v[200:203]\n" \
"v_mfma_f32_16x16x32_bf16 a[60:63], a[236:239], a[204:207], a[60:63]\n" \
"v_and_or_b32 v213, v213, 2, v215\n" \
"v_add_u32_e64 v217, s40, v217\n" \
"v_mfma_f32_16x16x32_bf16 a[56:59], a[168:171], a[140:143], a[56:59]\n" \
"ds_read_b64_tr_b16 a[184:185], v38 offset:32768\n" \
"v_or3_b32 v216, v216, v218, v219\n" \
"v_mfma_f32_16x16x32_bf16 a[56:59], a[232:235], a[204:207], a[56:59]\n" \
"v_lshlrev_b32_e64 v213, 5, v213\n" \
"s_mov_b32 s40, 0x14000\n" \
"v_mfma_f32_16x16x32_bf16 a[52:55], a[164:167], a[140:143], a[52:55]\n" \
"s_lshl_b32 s7, s2, 2\n" \
"v_or3_b32 v48, v216, v213, s40\n" \
"v_mfma_f32_16x16x32_bf16 a[52:55], a[228:231], a[204:207], a[52:55]\n" \
"ds_read_b64_tr_b16 a[186:187], v38 offset:33792\n" \
"s_lshl_b32 s40, s43, 2\n" \
"v_mfma_f32_16x16x32_bf16 a[48:51], a[160:163], a[140:143], a[48:51]\n" \
"s_lshl_b32 s6, s6, 3\n" \
"v_or_b32_e32 v213, s40, v212\n" \
"v_mfma_f32_16x16x32_bf16 a[48:51], a[224:227], a[204:207], a[48:51]\n" \
"s_or_b32 s6, s6, s40\n" \
"s_and_b32 s40, s7, 0x7c\n" \
"v_mfma_f32_16x16x32_bf16 a[64:67], a[160:163], a[144:147], a[64:67]\n" \
"ds_write_b128 v197, v[204:207]\n" \
"v_mfma_f32_16x16x32_bf16 a[64:67], a[224:227], a[208:211], a[64:67]\n" \
"ds_read_b64_tr_b16 a[248:249], v38 offset:40960\n" \
"s_or_b32 s40, s40, s3\n" \
"v_mfma_f32_16x16x32_bf16 a[68:71], a[164:167], a[144:147], a[68:71]\n" \
"s_cmpk_lt_u32 s40, 64\n" \
"s_cselect_b32 s41, s4, s5\n" \
"v_mfma_f32_16x16x32_bf16 a[68:71], a[228:231], a[208:211], a[68:71]\n" \
"ds_read_b64_tr_b16 a[250:251], v38 offset:41984\n" \
"s_lshl_b32 s40, s40, 1\n" \
"v_mfma_f32_16x16x32_bf16 a[72:75], a[168:171], a[144:147], a[72:75]\n" \
"s_lshl_b32 s41, s41, 7\n" \
"s_and_b32 s40, s40, 0x7e\n" \
"v_mfma_f32_16x16x32_bf16 a[72:75], a[232:235], a[208:211], a[72:75]\n" \
"s_or_b32 s40, s41, s40\n" \
"s_mulk_i32 s40, 0x480\n" \
"v_mfma_f32_16x16x32_bf16 a[76:79], a[172:175], a[144:147], a[76:79]\n" \
"v_add_u32_e64 v49, s40, v20\n" \
"s_mov_b32 s40, 0xc000\n" \
"v_mfma_f32_16x16x32_bf16 a[76:79], a[236:239], a[208:211], a[76:79]\n" \
"ds_read_b64_tr_b16 a[188:189], v39 offset:32768\n" \
"s_and_b32 s7, s7, 28\n" \
"v_mfma_f32_16x16x32_bf16 a[92:95], a[172:175], a[148:151], a[92:95]\n" \
"ds_write_b128 v198, v[208:211]\n" \
"v_mfma_f32_16x16x32_bf16 a[92:95], a[236:239], a[212:215], a[92:95]\n" \
"s_bfe_u32 s45, s2, 0x20003\n" \
"s_mov_b32 s41, 0x8000\n" \
"v_mfma_f32_16x16x32_bf16 a[88:91], a[168:171], a[148:151], a[88:91]\n" \
"ds_read_b64_tr_b16 a[190:191], v39 offset:33792\n" \
"v_mfma_f32_16x16x32_bf16 a[88:91], a[232:235], a[212:215], a[88:91]\n" \
"s_cmp_lg_u32 s4, 0\n" \
"s_cselect_b32 s51, s40, s41\n" \
"v_mfma_f32_16x16x32_bf16 a[84:87], a[164:167], a[148:151], a[84:87]\n" \
"s_movk_i32 s40, 0x4000\n" \
"s_lshr_b32 s44, s3, 1\n" \
"v_mfma_f32_16x16x32_bf16 a[84:87], a[228:231], a[212:215], a[84:87]\n" \
"ds_read_b64_tr_b16 a[252:253], v39 offset:40960\n" \
"s_or_b32 s46, s7, s45\n" \
"v_mfma_f32_16x16x32_bf16 a[80:83], a[160:163], a[148:151], a[80:83]\n" \
"s_cmp_lg_u32 s4, 0\n" \
"s_cselect_b32 s53, s41, s40\n" \
"v_mfma_f32_16x16x32_bf16 a[80:83], a[224:227], a[212:215], a[80:83]\n" \
"s_lshl_b32 s40, s44, 15\n" \
"s_lshl_b32 s41, s43, 14\n" \
"v_mfma_f32_16x16x32_bf16 a[96:99], a[160:163], a[152:155], a[96:99]\n" \
"ds_read_b64_tr_b16 a[254:255], v39 offset:41984\n" \
"s_lshl_b32 s48, s46, 20\n" \
"v_mfma_f32_16x16x32_bf16 a[96:99], a[224:227], a[216:219], a[96:99]\n" \
"s_or_b32 s41, s40, s41\n" \
"s_add_i32 s49, s48, 0x1000000\n" \
"v_mfma_f32_16x16x32_bf16 a[100:103], a[164:167], a[152:155], a[100:103]\n" \
"s_or_b32 s42, s49, s41\n" \
"s_lshl_b32 s4, s4, 16\n" \
"v_mfma_f32_16x16x32_bf16 a[100:103], a[228:231], a[216:219], a[100:103]\n" \
"s_or_b32 s41, s42, s4\n" \
"s_lshl_b32 s4, s5, 16\n" \
"v_mfma_f32_16x16x32_bf16 a[104:107], a[168:171], a[152:155], a[104:107]\n" \
"s_mul_i32 s5, s6, 0x24800\n" \
"s_lshl_b32 s6, s46, 9\n" \
"v_mfma_f32_16x16x32_bf16 a[104:107], a[232:235], a[216:219], a[104:107]\n" \
"s_lshl_b32 s7, s7, 6\n" \
"s_add_i32 s6, s5, s6\n" \
"v_mfma_f32_16x16x32_bf16 a[108:111], a[172:175], a[152:155], a[108:111]\n" \
"s_or_b32 s5, s5, s7\n" \
"s_lshl_b32 s7, s45, 2\n" \
"v_mfma_f32_16x16x32_bf16 a[108:111], a[236:239], a[216:219], a[108:111]\n" \
"s_or_b32 s5, s5, s7\n" \
"s_lshl_b32 s7, s3, 12\n" \
"s_waitcnt lgkmcnt(15)\n" \
"v_mfma_f32_16x16x32_bf16 a[124:127], a[172:175], a[156:159], a[124:127]\n" \
"s_or_b32 s50, s49, s7\n" \
"s_lshl_b32 s7, s3, 11\n" \
"v_mfma_f32_16x16x32_bf16 a[124:127], a[236:239], a[220:223], a[124:127]\n" \
"v_lshrrev_b32_e64 v215, 4, v20\n" \
"v_bfe_u32 v216, v20, 4, 3\n" \
"v_mfma_f32_16x16x32_bf16 a[120:123], a[168:171], a[156:159], a[120:123]\n" \
"s_or_b32 s42, s42, s4\n" \
"s_lshl_b32 s4, s43, 11\n" \
"v_mfma_f32_16x16x32_bf16 a[120:123], a[232:235], a[220:223], a[120:123]\n" \
"s_add_i32 s52, s48, s7\n" \
"s_add_i32 s53, s53, s7\n" \
"v_mfma_f32_16x16x32_bf16 a[116:119], a[164:167], a[156:159], a[116:119]\n" \
"v_mul_u32_u24_e32 v214, 0x480, v214\n" \
"v_bitop3_b32 v218, v212, v215, 7 bitop3:0x78\n" \
"v_mfma_f32_16x16x32_bf16 a[116:119], a[228:231], a[220:223], a[116:119]\n" \
"v_bitop3_b32 v212, v212, v216, 4 bitop3:0x36\n" \
"s_lshl_b32 s7, s3, 10\n" \
"v_mfma_f32_16x16x32_bf16 a[112:115], a[160:163], a[156:159], a[112:115]\n" \
"s_or_b32 s4, s40, s4\n" \
"v_lshl_or_b32 v25, v212, 4, v214\n" \
"v_mfma_f32_16x16x32_bf16 a[112:115], a[224:227], a[220:223], a[112:115]\n" \
"s_waitcnt vmcnt(1) lgkmcnt(0)\n" \
"s_barrier\n" \
"v_mfma_f32_16x16x32_bf16 v[52:55], a[176:179], a[128:131], v[52:55]\n" \
"s_add_u32 m0, s30, 0x8000\n" \
"s_movk_i32 s84, 0x280\n" \
"v_mfma_f32_16x16x32_bf16 v[52:55], a[240:243], a[192:195], v[52:55]\n" \
"buffer_load_dwordx4 v22, s[64:67], s84 offen offset:0 lds\n" \
"v_mfma_f32_16x16x32_bf16 v[56:59], a[180:183], a[128:131], v[56:59]\n" \
"s_movk_i32 s84, 0x1e80\n" \
"v_mfma_f32_16x16x32_bf16 v[56:59], a[244:247], a[192:195], v[56:59]\n" \
"buffer_load_dwordx4 v23, s[64:67], s84 offen offset:1024 lds\n" \
"v_mfma_f32_16x16x32_bf16 v[60:63], a[184:187], a[128:131], v[60:63]\n" \
"ds_read_b64_tr_b16 a[160:161], v32 offset:0\n" \
"v_mul_u32_u24_e32 v212, 0x480, v217\n" \
"v_mfma_f32_16x16x32_bf16 v[60:63], a[248:251], a[192:195], v[60:63]\n" \
"s_movk_i32 s84, 0x3a80\n" \
"v_mfma_f32_16x16x32_bf16 v[64:67], a[188:191], a[128:131], v[64:67]\n" \
"buffer_load_dwordx4 v22, s[64:67], s84 offen offset:2048 lds\n" \
"v_mfma_f32_16x16x32_bf16 v[64:67], a[252:255], a[192:195], v[64:67]\n" \
"ds_read_b128 a[128:131], v28 offset:16384\n" \
"v_bitop3_b32 v213, v215, v213, 7 bitop3:0x6c\n" \
"v_mfma_f32_16x16x32_bf16 v[80:83], a[188:191], a[132:135], v[80:83]\n" \
"ds_read_b128 a[192:195], v29 offset:16384\n" \
"s_or_b32 s40, s7, 0x14000\n" \
"v_mfma_f32_16x16x32_bf16 v[80:83], a[252:255], a[196:199], v[80:83]\n" \
"s_movk_i32 s84, 0x5680\n" \
"v_mfma_f32_16x16x32_bf16 v[76:79], a[184:187], a[132:135], v[76:79]\n" \
"buffer_load_dwordx4 v23, s[64:67], s84 offen offset:3072 lds\n" \
"v_mfma_f32_16x16x32_bf16 v[76:79], a[248:251], a[196:199], v[76:79]\n" \
"s_add_u32 m0, s31, 0x8000\n" \
"s_mov_b32 s84, 0x280000\n" \
"v_mfma_f32_16x16x32_bf16 v[72:75], a[180:183], a[132:135], v[72:75]\n" \
"buffer_load_dwordx4 v26, s[68:71], s84 offen offset:0 lds\n" \
"v_mfma_f32_16x16x32_bf16 v[72:75], a[244:247], a[196:199], v[72:75]\n" \
"ds_read_b64_tr_b16 a[162:163], v32 offset:1024\n" \
"s_lshl_b32 s43, s44, 13\n" \
"v_mfma_f32_16x16x32_bf16 v[68:71], a[176:179], a[132:135], v[68:71]\n" \
"ds_read_b64_tr_b16 a[224:225], v32 offset:8192\n" \
"s_mulk_i32 s44, 0xe800\n" \
"v_mfma_f32_16x16x32_bf16 v[68:71], a[240:243], a[196:199], v[68:71]\n" \
"ds_read_b128 a[132:135], v28 offset:18432\n" \
"s_lshl_b32 s7, s3, 8\n" \
"v_mfma_f32_16x16x32_bf16 v[84:87], a[176:179], a[136:139], v[84:87]\n" \
"ds_read_b128 a[196:199], v29 offset:18432\n" \
"v_lshl_or_b32 v24, v218, 4, v214\n" \
"v_mfma_f32_16x16x32_bf16 v[84:87], a[240:243], a[200:203], v[84:87]\n" \
"s_addk_i32 s84, 0x7c00\n" \
"v_mfma_f32_16x16x32_bf16 v[88:91], a[180:183], a[136:139], v[88:91]\n" \
"buffer_load_dwordx4 v26, s[68:71], s84 offen offset:1024 lds\n" \
"v_mfma_f32_16x16x32_bf16 v[88:91], a[244:247], a[200:203], v[88:91]\n" \
"s_addk_i32 s84, 0x7c00\n" \
"v_mfma_f32_16x16x32_bf16 v[92:95], a[184:187], a[136:139], v[92:95]\n" \
"buffer_load_dwordx4 v27, s[68:71], s84 offen offset:2048 lds\n" \
"v_mfma_f32_16x16x32_bf16 v[92:95], a[248:251], a[200:203], v[92:95]\n" \
"s_addk_i32 s84, 0x7c00\n" \
"v_mfma_f32_16x16x32_bf16 v[96:99], a[188:191], a[136:139], v[96:99]\n" \
"buffer_load_dwordx4 v27, s[68:71], s84 offen offset:3072 lds\n" \
"v_mfma_f32_16x16x32_bf16 v[96:99], a[252:255], a[200:203], v[96:99]\n" \
"ds_read_b128 a[136:139], v28 offset:20480\n" \
"v_lshl_or_b32 v21, v213, 4, v212\n" \
"v_mfma_f32_16x16x32_bf16 v[112:115], a[188:191], a[140:143], v[112:115]\n" \
"ds_read_b128 a[200:203], v29 offset:20480\n" \
"s_add_i32 s44, s44, 0x17000\n" \
"v_mfma_f32_16x16x32_bf16 v[112:115], a[252:255], a[204:207], v[112:115]\n" \
"s_add_u32 m0, s30, 0x9000\n" \
"s_mov_b32 s84, 0x8280\n" \
"v_mfma_f32_16x16x32_bf16 v[108:111], a[184:187], a[140:143], v[108:111]\n" \
"buffer_load_dwordx4 v22, s[64:67], s84 offen offset:0 lds\n" \
"v_mfma_f32_16x16x32_bf16 v[108:111], a[248:251], a[204:207], v[108:111]\n" \
"s_addk_i32 s84, 0x1c00\n" \
"v_mfma_f32_16x16x32_bf16 v[104:107], a[180:183], a[140:143], v[104:107]\n" \
"buffer_load_dwordx4 v23, s[64:67], s84 offen offset:1024 lds\n" \
"v_mfma_f32_16x16x32_bf16 v[104:107], a[244:247], a[204:207], v[104:107]\n" \
"s_addk_i32 s84, 0x1c00\n" \
"v_mfma_f32_16x16x32_bf16 v[100:103], a[176:179], a[140:143], v[100:103]\n" \
"buffer_load_dwordx4 v22, s[64:67], s84 offen offset:2048 lds\n" \
"v_mfma_f32_16x16x32_bf16 v[100:103], a[240:243], a[204:207], v[100:103]\n" \
"ds_read_b128 a[140:143], v28 offset:22528\n" \
"s_or_b32 s45, s7, 0x27000\n" \
"v_mfma_f32_16x16x32_bf16 v[116:119], a[176:179], a[144:147], v[116:119]\n" \
"ds_read_b128 a[204:207], v29 offset:22528\n" \
"s_or_b32 s46, s4, 0x10000\n" \
"v_mfma_f32_16x16x32_bf16 v[116:119], a[240:243], a[208:211], v[116:119]\n" \
"s_addk_i32 s84, 0x1c00\n" \
"v_mfma_f32_16x16x32_bf16 v[120:123], a[180:183], a[144:147], v[120:123]\n" \
"buffer_load_dwordx4 v23, s[64:67], s84 offen offset:3072 lds\n" \
"v_mfma_f32_16x16x32_bf16 v[120:123], a[244:247], a[208:211], v[120:123]\n" \
"s_add_u32 m0, s31, 0xc000\n" \
"s_mov_b32 s84, 0x280000\n" \
"v_mfma_f32_16x16x32_bf16 v[124:127], a[184:187], a[144:147], v[124:127]\n" \
"buffer_load_dwordx4 v26, s[72:75], s84 offen offset:0 lds\n" \
"v_mfma_f32_16x16x32_bf16 v[124:127], a[248:251], a[208:211], v[124:127]\n" \
"s_addk_i32 s84, 0x7c00\n" \
"v_mfma_f32_16x16x32_bf16 v[128:131], a[188:191], a[144:147], v[128:131]\n" \
"buffer_load_dwordx4 v26, s[72:75], s84 offen offset:1024 lds\n" \
"v_mfma_f32_16x16x32_bf16 v[128:131], a[252:255], a[208:211], v[128:131]\n" \
"ds_read_b128 a[144:147], v28 offset:0\n" \
"s_add_i32 s48, s6, 0x3000000\n" \
"v_mfma_f32_16x16x32_bf16 v[144:147], a[188:191], a[148:151], v[144:147]\n" \
"ds_read_b128 a[208:211], v29 offset:0\n" \
"s_add_i32 s49, s5, 0x3004000\n" \
"v_mfma_f32_16x16x32_bf16 v[144:147], a[252:255], a[212:215], v[144:147]\n" \
"ds_read_b128 v[200:203], v199 offset:0\n" \
"s_add_i32 s52, s52, 0x1010000\n" \
"v_mfma_f32_16x16x32_bf16 v[140:143], a[184:187], a[148:151], v[140:143]\n" \
"ds_read_b128 v[204:207], v199 offset:4096\n" \
"v_mfma_f32_16x16x32_bf16 v[140:143], a[248:251], a[212:215], v[140:143]\n" \
"ds_read_b64_tr_b16 a[226:227], v32 offset:9216\n" \
"v_mfma_f32_16x16x32_bf16 v[136:139], a[180:183], a[148:151], v[136:139]\n" \
"ds_read_b64_tr_b16 a[164:165], v33 offset:0\n" \
"v_mfma_f32_16x16x32_bf16 v[136:139], a[244:247], a[212:215], v[136:139]\n" \
"ds_read_b128 v[208:211], v199 offset:8192\n" \
"v_mfma_f32_16x16x32_bf16 v[132:135], a[176:179], a[148:151], v[132:135]\n" \
"ds_read_b64_tr_b16 a[166:167], v33 offset:1024\n" \
"v_mfma_f32_16x16x32_bf16 v[132:135], a[240:243], a[212:215], v[132:135]\n" \
"ds_read_b128 a[148:151], v28 offset:2048\n" \
"v_mfma_f32_16x16x32_bf16 v[148:151], a[176:179], a[152:155], v[148:151]\n" \
"ds_read_b128 a[212:215], v29 offset:2048\n" \
"v_mfma_f32_16x16x32_bf16 v[148:151], a[240:243], a[216:219], v[148:151]\n" \
"ds_read_b64_tr_b16 a[228:229], v33 offset:8192\n" \
"v_mfma_f32_16x16x32_bf16 v[152:155], a[180:183], a[152:155], v[152:155]\n" \
"ds_read_b64_tr_b16 a[230:231], v33 offset:9216\n" \
"v_mfma_f32_16x16x32_bf16 v[152:155], a[244:247], a[216:219], v[152:155]\n" \
"ds_read_b64_tr_b16 a[168:169], v34 offset:0\n" \
"v_mfma_f32_16x16x32_bf16 v[156:159], a[184:187], a[152:155], v[156:159]\n" \
"ds_read_b64_tr_b16 a[170:171], v34 offset:1024\n" \
"v_mfma_f32_16x16x32_bf16 v[156:159], a[248:251], a[216:219], v[156:159]\n" \
"ds_read_b64_tr_b16 a[232:233], v34 offset:8192\n" \
"v_mfma_f32_16x16x32_bf16 v[160:163], a[188:191], a[152:155], v[160:163]\n" \
"ds_read_b64_tr_b16 a[234:235], v34 offset:9216\n" \
"v_mfma_f32_16x16x32_bf16 v[160:163], a[252:255], a[216:219], v[160:163]\n" \
"ds_read_b128 a[152:155], v28 offset:4096\n" \
"v_mfma_f32_16x16x32_bf16 v[176:179], a[188:191], a[156:159], v[176:179]\n" \
"ds_read_b128 a[216:219], v29 offset:4096\n" \
"v_mfma_f32_16x16x32_bf16 v[176:179], a[252:255], a[220:223], v[176:179]\n" \
"ds_read_b64_tr_b16 a[172:173], v35 offset:0\n" \
"v_mfma_f32_16x16x32_bf16 v[172:175], a[184:187], a[156:159], v[172:175]\n" \
"ds_read_b64_tr_b16 a[174:175], v35 offset:1024\n" \
"v_mfma_f32_16x16x32_bf16 v[172:175], a[248:251], a[220:223], v[172:175]\n" \
"ds_read_b64_tr_b16 a[236:237], v35 offset:8192\n" \
"v_mfma_f32_16x16x32_bf16 v[168:171], a[180:183], a[156:159], v[168:171]\n" \
"ds_read_b64_tr_b16 a[238:239], v35 offset:9216\n" \
"v_mfma_f32_16x16x32_bf16 v[168:171], a[244:247], a[220:223], v[168:171]\n" \
"v_mfma_f32_16x16x32_bf16 v[164:167], a[176:179], a[156:159], v[164:167]\n" \
"v_mfma_f32_16x16x32_bf16 v[164:167], a[240:243], a[220:223], v[164:167]\n" \
"ds_read_b128 a[156:159], v28 offset:6144\n" \
"ds_read_b128 a[220:223], v29 offset:6144\n" \
"s_waitcnt lgkmcnt(15)\n" \
"v_mfma_f32_16x16x32_bf16 a[0:3], a[160:163], a[128:131], a[0:3]\n" \
"s_addk_i32 s84, 0x7c00\n" \
"v_mfma_f32_16x16x32_bf16 a[0:3], a[224:227], a[192:195], a[0:3]\n" \
"buffer_load_dwordx4 v27, s[72:75], s84 offen offset:2048 lds\n" \
"v_mfma_f32_16x16x32_bf16 a[4:7], a[164:167], a[128:131], a[4:7]\n" \
"ds_read_b64_tr_b16 a[176:177], v36 offset:0\n" \
"s_waitcnt lgkmcnt(13)\n" \
"v_mfma_f32_16x16x32_bf16 a[4:7], a[228:231], a[192:195], a[4:7]\n" \
"s_addk_i32 s84, 0x7c00\n" \
"s_waitcnt lgkmcnt(11)\n" \
"v_mfma_f32_16x16x32_bf16 a[8:11], a[168:171], a[128:131], a[8:11]\n" \
"buffer_load_dwordx4 v27, s[72:75], s84 offen offset:3072 lds\n" \
"s_waitcnt lgkmcnt(9)\n" \
"v_mfma_f32_16x16x32_bf16 a[8:11], a[232:235], a[192:195], a[8:11]\n" \
"ds_read_b64_tr_b16 a[178:179], v36 offset:1024\n" \
"s_waitcnt lgkmcnt(6)\n" \
"v_mfma_f32_16x16x32_bf16 a[12:15], a[172:175], a[128:131], a[12:15]\n" \
"ds_read_b64_tr_b16 a[240:241], v36 offset:8192\n" \
"s_waitcnt lgkmcnt(5)\n" \
"v_mfma_f32_16x16x32_bf16 a[12:15], a[236:239], a[192:195], a[12:15]\n" \
"v_mfma_f32_16x16x32_bf16 a[28:31], a[172:175], a[132:135], a[28:31]\n" \
"v_mfma_f32_16x16x32_bf16 a[28:31], a[236:239], a[196:199], a[28:31]\n" \
"ds_read_b64_tr_b16 a[242:243], v36 offset:9216\n" \
"v_mfma_f32_16x16x32_bf16 a[24:27], a[168:171], a[132:135], a[24:27]\n" \
"v_mfma_f32_16x16x32_bf16 a[24:27], a[232:235], a[196:199], a[24:27]\n" \
"v_mfma_f32_16x16x32_bf16 a[20:23], a[164:167], a[132:135], a[20:23]\n" \
"v_mfma_f32_16x16x32_bf16 a[20:23], a[228:231], a[196:199], a[20:23]\n" \
"ds_read_b64_tr_b16 a[180:181], v37 offset:0\n" \
"v_mfma_f32_16x16x32_bf16 a[16:19], a[160:163], a[132:135], a[16:19]\n" \
"v_mfma_f32_16x16x32_bf16 a[16:19], a[224:227], a[196:199], a[16:19]\n" \
"v_mfma_f32_16x16x32_bf16 a[32:35], a[160:163], a[136:139], a[32:35]\n" \
"ds_read_b64_tr_b16 a[182:183], v37 offset:1024\n" \
"v_mfma_f32_16x16x32_bf16 a[32:35], a[224:227], a[200:203], a[32:35]\n" \
"v_mfma_f32_16x16x32_bf16 a[36:39], a[164:167], a[136:139], a[36:39]\n" \
"v_mfma_f32_16x16x32_bf16 a[36:39], a[228:231], a[200:203], a[36:39]\n" \
"buffer_store_dwordx4 v[200:203], v20, s[80:83], s35 offen sc1\n" \
"v_mfma_f32_16x16x32_bf16 a[40:43], a[168:171], a[136:139], a[40:43]\n" \
"s_mov_b32 m0, s32\n" \
".fill (L_pb + 12000 - .) / 4, 4, 0xBF800000\n" \
"L_pp3:\n" \
"s_cbranch_execz L_pret\n" \
"ds_read_b64_tr_b16 a[244:245], v37 offset:8192\n" \
"v_mfma_f32_16x16x32_bf16 a[40:43], a[232:235], a[200:203], a[40:43]\n" \
"s_mov_b32 exec_lo, 0x0\n" \
"s_mov_b32 exec_hi, 0xffffffff\n" \
"global_load_lds_dword v[188:189], off\n" \
"s_mov_b64 exec, -1\n" \
"v_mfma_f32_16x16x32_bf16 a[44:47], a[172:175], a[136:139], a[44:47]\n" \
"ds_read_b64_tr_b16 a[246:247], v37 offset:9216\n" \
"v_mfma_f32_16x16x32_bf16 a[44:47], a[236:239], a[200:203], a[44:47]\n" \
"v_mfma_f32_16x16x32_bf16 a[60:63], a[172:175], a[140:143], a[60:63]\n" \
"v_mfma_f32_16x16x32_bf16 a[60:63], a[236:239], a[204:207], a[60:63]\n" \
"v_mfma_f32_16x16x32_bf16 a[56:59], a[168:171], a[140:143], a[56:59]\n" \
"ds_read_b64_tr_b16 a[184:185], v38 offset:0\n" \
"v_mfma_f32_16x16x32_bf16 a[56:59], a[232:235], a[204:207], a[56:59]\n" \
"v_mfma_f32_16x16x32_bf16 a[52:55], a[164:167], a[140:143], a[52:55]\n" \
"buffer_store_dwordx4 v[204:207], v20, s[80:83], s88 offen sc1\n" \
"v_mfma_f32_16x16x32_bf16 a[52:55], a[228:231], a[204:207], a[52:55]\n" \
"ds_read_b64_tr_b16 a[186:187], v38 offset:1024\n" \
"v_mfma_f32_16x16x32_bf16 a[48:51], a[160:163], a[140:143], a[48:51]\n" \
"v_mfma_f32_16x16x32_bf16 a[48:51], a[224:227], a[204:207], a[48:51]\n" \
"v_mfma_f32_16x16x32_bf16 a[64:67], a[160:163], a[144:147], a[64:67]\n" \
"ds_read_b64_tr_b16 a[248:249], v38 offset:8192\n" \
"v_mfma_f32_16x16x32_bf16 a[64:67], a[224:227], a[208:211], a[64:67]\n" \
"v_mfma_f32_16x16x32_bf16 a[68:71], a[164:167], a[144:147], a[68:71]\n" \
"v_mfma_f32_16x16x32_bf16 a[68:71], a[228:231], a[208:211], a[68:71]\n" \
"ds_read_b64_tr_b16 a[250:251], v38 offset:9216\n" \
"v_mfma_f32_16x16x32_bf16 a[72:75], a[168:171], a[144:147], a[72:75]\n" \
"v_mfma_f32_16x16x32_bf16 a[72:75], a[232:235], a[208:211], a[72:75]\n" \
"v_mfma_f32_16x16x32_bf16 a[76:79], a[172:175], a[144:147], a[76:79]\n" \
"buffer_store_dwordx4 v[208:211], v20, s[80:83], s36 offen sc1\n" \
"v_mfma_f32_16x16x32_bf16 a[76:79], a[236:239], a[208:211], a[76:79]\n" \
"ds_read_b64_tr_b16 a[188:189], v39 offset:0\n" \
"v_mfma_f32_16x16x32_bf16 a[92:95], a[172:175], a[148:151], a[92:95]\n" \
"v_mfma_f32_16x16x32_bf16 a[92:95], a[236:239], a[212:215], a[92:95]\n" \
"v_mfma_f32_16x16x32_bf16 a[88:91], a[168:171], a[148:151], a[88:91]\n" \
"ds_read_b64_tr_b16 a[190:191], v39 offset:1024\n" \
"v_mfma_f32_16x16x32_bf16 a[88:91], a[232:235], a[212:215], a[88:91]\n" \
"v_mfma_f32_16x16x32_bf16 a[84:87], a[164:167], a[148:151], a[84:87]\n" \
"v_mfma_f32_16x16x32_bf16 a[84:87], a[228:231], a[212:215], a[84:87]\n" \
"ds_read_b64_tr_b16 a[252:253], v39 offset:8192\n" \
"v_mfma_f32_16x16x32_bf16 a[80:83], a[160:163], a[148:151], a[80:83]\n" \
"v_mfma_f32_16x16x32_bf16 a[80:83], a[224:227], a[212:215], a[80:83]\n" \
"v_mfma_f32_16x16x32_bf16 a[96:99], a[160:163], a[152:155], a[96:99]\n" \
"ds_read_b64_tr_b16 a[254:255], v39 offset:9216\n" \
"v_mfma_f32_16x16x32_bf16 a[96:99], a[224:227], a[216:219], a[96:99]\n" \
"v_mfma_f32_16x16x32_bf16 a[100:103], a[164:167], a[152:155], a[100:103]\n" \
"v_mfma_f32_16x16x32_bf16 a[100:103], a[228:231], a[216:219], a[100:103]\n" \
"v_mfma_f32_16x16x32_bf16 a[104:107], a[168:171], a[152:155], a[104:107]\n" \
"v_mfma_f32_16x16x32_bf16 a[104:107], a[232:235], a[216:219], a[104:107]\n" \
"v_mfma_f32_16x16x32_bf16 a[108:111], a[172:175], a[152:155], a[108:111]\n" \
"v_mfma_f32_16x16x32_bf16 a[108:111], a[236:239], a[216:219], a[108:111]\n" \
"s_waitcnt lgkmcnt(15)\n" \
"v_mfma_f32_16x16x32_bf16 a[124:127], a[172:175], a[156:159], a[124:127]\n" \
"v_mfma_f32_16x16x32_bf16 a[124:127], a[236:239], a[220:223], a[124:127]\n" \
"v_mfma_f32_16x16x32_bf16 a[120:123], a[168:171], a[156:159], a[120:123]\n" \
"v_mfma_f32_16x16x32_bf16 a[120:123], a[232:235], a[220:223], a[120:123]\n" \
"v_mfma_f32_16x16x32_bf16 a[116:119], a[164:167], a[156:159], a[116:119]\n" \
"v_mfma_f32_16x16x32_bf16 a[116:119], a[228:231], a[220:223], a[116:119]\n" \
"v_mfma_f32_16x16x32_bf16 a[112:115], a[160:163], a[156:159], a[112:115]\n" \
"v_mfma_f32_16x16x32_bf16 a[112:115], a[224:227], a[220:223], a[112:115]\n" \
"s_waitcnt vmcnt(4) lgkmcnt(0)\n" \
"s_barrier\n" \
"v_mfma_f32_16x16x32_bf16 v[52:55], a[176:179], a[128:131], v[52:55]\n" \
"s_add_u32 m0, s30, 0x0\n" \
"s_movk_i32 s84, 0x300\n" \
"v_mfma_f32_16x16x32_bf16 v[52:55], a[240:243], a[192:195], v[52:55]\n" \
"buffer_load_dwordx4 v22, s[64:67], s84 offen offset:0 lds\n" \
"v_mfma_f32_16x16x32_bf16 v[56:59], a[180:183], a[128:131], v[56:59]\n" \
"ds_read_b64_tr_b16 a[160:161], v32 offset:32768\n" \
"v_mfma_f32_16x16x32_bf16 v[56:59], a[244:247], a[192:195], v[56:59]\n" \
"s_movk_i32 s84, 0x1f00\n" \
"v_mfma_f32_16x16x32_bf16 v[60:63], a[184:187], a[128:131], v[60:63]\n" \
"buffer_load_dwordx4 v23, s[64:67], s84 offen offset:1024 lds\n" \
"v_mfma_f32_16x16x32_bf16 v[60:63], a[248:251], a[192:195], v[60:63]\n" \
"ds_read_b64_tr_b16 a[162:163], v32 offset:33792\n" \
"v_mfma_f32_16x16x32_bf16 v[64:67], a[188:191], a[128:131], v[64:67]\n" \
"ds_read_b64_tr_b16 a[224:225], v32 offset:40960\n" \
"v_mfma_f32_16x16x32_bf16 v[64:67], a[252:255], a[192:195], v[64:67]\n" \
"ds_read_b128 a[128:131], v28 offset:49152\n" \
"v_mfma_f32_16x16x32_bf16 v[80:83], a[188:191], a[132:135], v[80:83]\n" \
"ds_read_b128 a[192:195], v29 offset:49152\n" \
"v_mfma_f32_16x16x32_bf16 v[80:83], a[252:255], a[196:199], v[80:83]\n" \
"s_movk_i32 s84, 0x3b00\n" \
"v_mfma_f32_16x16x32_bf16 v[76:79], a[184:187], a[132:135], v[76:79]\n" \
"buffer_load_dwordx4 v22, s[64:67], s84 offen offset:2048 lds\n" \
"v_mfma_f32_16x16x32_bf16 v[76:79], a[248:251], a[196:199], v[76:79]\n" \
"s_movk_i32 s84, 0x5700\n" \
"v_mfma_f32_16x16x32_bf16 v[72:75], a[180:183], a[132:135], v[72:75]\n" \
"buffer_load_dwordx4 v23, s[64:67], s84 offen offset:3072 lds\n" \
"v_mfma_f32_16x16x32_bf16 v[72:75], a[244:247], a[196:199], v[72:75]\n" \
"s_add_u32 m0, s31, 0x0\n" \
"s_mov_b32 s84, 0x300000\n" \
"v_mfma_f32_16x16x32_bf16 v[68:71], a[176:179], a[132:135], v[68:71]\n" \
"buffer_load_dwordx4 v26, s[68:71], s84 offen offset:0 lds\n" \
"v_mfma_f32_16x16x32_bf16 v[68:71], a[240:243], a[196:199], v[68:71]\n" \
"ds_read_b128 a[132:135], v28 offset:51200\n" \
"v_mfma_f32_16x16x32_bf16 v[84:87], a[176:179], a[136:139], v[84:87]\n" \
"ds_read_b128 a[196:199], v29 offset:51200\n" \
"v_mfma_f32_16x16x32_bf16 v[84:87], a[240:243], a[200:203], v[84:87]\n" \
"s_addk_i32 s84, 0x7c00\n" \
"v_mfma_f32_16x16x32_bf16 v[88:91], a[180:183], a[136:139], v[88:91]\n" \
"buffer_load_dwordx4 v26, s[68:71], s84 offen offset:1024 lds\n" \
"v_mfma_f32_16x16x32_bf16 v[88:91], a[244:247], a[200:203], v[88:91]\n" \
"ds_read_b64_tr_b16 a[226:227], v32 offset:41984\n" \
"v_mfma_f32_16x16x32_bf16 v[92:95], a[184:187], a[136:139], v[92:95]\n" \
"s_addk_i32 s84, 0x7c00\n" \
"v_mfma_f32_16x16x32_bf16 v[92:95], a[248:251], a[200:203], v[92:95]\n" \
"buffer_load_dwordx4 v27, s[68:71], s84 offen offset:2048 lds\n" \
"v_mfma_f32_16x16x32_bf16 v[96:99], a[188:191], a[136:139], v[96:99]\n" \
"ds_read_b64_tr_b16 a[164:165], v33 offset:32768\n" \
"v_mfma_f32_16x16x32_bf16 v[96:99], a[252:255], a[200:203], v[96:99]\n" \
"ds_read_b128 a[136:139], v28 offset:53248\n" \
"v_mfma_f32_16x16x32_bf16 v[112:115], a[188:191], a[140:143], v[112:115]\n" \
"ds_read_b128 a[200:203], v29 offset:53248\n" \
"v_mfma_f32_16x16x32_bf16 v[112:115], a[252:255], a[204:207], v[112:115]\n" \
"s_addk_i32 s84, 0x7c00\n" \
"v_mfma_f32_16x16x32_bf16 v[108:111], a[184:187], a[140:143], v[108:111]\n" \
"buffer_load_dwordx4 v27, s[68:71], s84 offen offset:3072 lds\n" \
"v_mfma_f32_16x16x32_bf16 v[108:111], a[248:251], a[204:207], v[108:111]\n" \
"s_add_u32 m0, s30, 0x1000\n" \
"s_mov_b32 s84, 0x8300\n" \
"v_mfma_f32_16x16x32_bf16 v[104:107], a[180:183], a[140:143], v[104:107]\n" \
"buffer_load_dwordx4 v22, s[64:67], s84 offen offset:0 lds\n" \
"v_mfma_f32_16x16x32_bf16 v[104:107], a[244:247], a[204:207], v[104:107]\n" \
"ds_read_b64_tr_b16 a[166:167], v33 offset:33792\n" \
"v_mfma_f32_16x16x32_bf16 v[100:103], a[176:179], a[140:143], v[100:103]\n" \
"ds_read_b64_tr_b16 a[228:229], v33 offset:40960\n" \
"v_mfma_f32_16x16x32_bf16 v[100:103], a[240:243], a[204:207], v[100:103]\n" \
"ds_read_b128 a[140:143], v28 offset:55296\n" \
"v_mfma_f32_16x16x32_bf16 v[116:119], a[176:179], a[144:147], v[116:119]\n" \
"ds_read_b128 a[204:207], v29 offset:55296\n" \
"v_mfma_f32_16x16x32_bf16 v[116:119], a[240:243], a[208:211], v[116:119]\n" \
"s_addk_i32 s84, 0x1c00\n" \
"v_mfma_f32_16x16x32_bf16 v[120:123], a[180:183], a[144:147], v[120:123]\n" \
"buffer_load_dwordx4 v23, s[64:67], s84 offen offset:1024 lds\n" \
"v_mfma_f32_16x16x32_bf16 v[120:123], a[244:247], a[208:211], v[120:123]\n" \
"s_addk_i32 s84, 0x1c00\n" \
"v_mfma_f32_16x16x32_bf16 v[124:127], a[184:187], a[144:147], v[124:127]\n" \
"buffer_load_dwordx4 v22, s[64:67], s84 offen offset:2048 lds\n" \
"v_mfma_f32_16x16x32_bf16 v[124:127], a[248:251], a[208:211], v[124:127]\n" \
"s_addk_i32 s84, 0x1c00\n" \
"s_nop 0\n" \
"v_mfma_f32_16x16x32_bf16 v[128:131], a[188:191], a[144:147], v[128:131]\n" \
"buffer_load_dwordx4 v23, s[64:67], s84 offen offset:3072 lds\n" \
"v_mfma_f32_16x16x32_bf16 v[128:131], a[252:255], a[208:211], v[128:131]\n" \
"ds_read_b128 a[144:147], v28 offset:32768\n" \
"v_mfma_f32_16x16x32_bf16 v[144:147], a[188:191], a[148:151], v[144:147]\n" \
"ds_read_b128 a[208:211], v29 offset:32768\n" \
"v_mfma_f32_16x16x32_bf16 v[144:147], a[252:255], a[212:215], v[144:147]\n" \
"s_add_u32 m0, s31, 0x4000\n" \
"s_mov_b32 s84, 0x300000\n" \
"v_mfma_f32_16x16x32_bf16 v[140:143], a[184:187], a[148:151], v[140:143]\n" \
"buffer_load_dwordx4 v26, s[72:75], s84 offen offset:0 lds\n" \
"v_mfma_f32_16x16x32_bf16 v[140:143], a[248:251], a[212:215], v[140:143]\n" \
"ds_read_b64_tr_b16 a[230:231], v33 offset:41984\n" \
"v_mfma_f32_16x16x32_bf16 v[136:139], a[180:183], a[148:151], v[136:139]\n" \
"s_addk_i32 s84, 0x7c00\n" \
"s_nop 0\n" \
"v_mfma_f32_16x16x32_bf16 v[136:139], a[244:247], a[212:215], v[136:139]\n" \
"buffer_load_dwordx4 v26, s[72:75], s84 offen offset:1024 lds\n" \
"v_mfma_f32_16x16x32_bf16 v[132:135], a[176:179], a[148:151], v[132:135]\n" \
"ds_read_b64_tr_b16 a[168:169], v34 offset:32768\n" \
"v_mfma_f32_16x16x32_bf16 v[132:135], a[240:243], a[212:215], v[132:135]\n" \
"ds_read_b128 a[148:151], v28 offset:34816\n" \
"v_mfma_f32_16x16x32_bf16 v[148:151], a[176:179], a[152:155], v[148:151]\n" \
"ds_read_b128 a[212:215], v29 offset:34816\n" \
"v_mfma_f32_16x16x32_bf16 v[148:151], a[240:243], a[216:219], v[148:151]\n" \
"ds_read_b64_tr_b16 a[170:171], v34 offset:33792\n" \
"v_mfma_f32_16x16x32_bf16 v[152:155], a[180:183], a[152:155], v[152:155]\n" \
"ds_read_b64_tr_b16 a[232:233], v34 offset:40960\n" \
"v_mfma_f32_16x16x32_bf16 v[152:155], a[244:247], a[216:219], v[152:155]\n" \
"ds_read_b64_tr_b16 a[234:235], v34 offset:41984\n" \
"v_mfma_f32_16x16x32_bf16 v[156:159], a[184:187], a[152:155], v[156:159]\n" \
"ds_read_b64_tr_b16 a[172:173], v35 offset:32768\n" \
"v_mfma_f32_16x16x32_bf16 v[156:159], a[248:251], a[216:219], v[156:159]\n" \
"ds_read_b64_tr_b16 a[174:175], v35 offset:33792\n" \
"v_mfma_f32_16x16x32_bf16 v[160:163], a[188:191], a[152:155], v[160:163]\n" \
"ds_read_b64_tr_b16 a[236:237], v35 offset:40960\n" \
"v_mfma_f32_16x16x32_bf16 v[160:163], a[252:255], a[216:219], v[160:163]\n" \
"ds_read_b128 a[152:155], v28 offset:36864\n" \
"v_mfma_f32_16x16x32_bf16 v[176:179], a[188:191], a[156:159], v[176:179]\n" \
"ds_read_b128 a[216:219], v29 offset:36864\n" \
"v_mfma_f32_16x16x32_bf16 v[176:179], a[252:255], a[220:223], v[176:179]\n" \
"ds_read_b64_tr_b16 a[238:239], v35 offset:41984\n" \
"v_mfma_f32_16x16x32_bf16 v[172:175], a[184:187], a[156:159], v[172:175]\n" \
"v_mfma_f32_16x16x32_bf16 v[172:175], a[248:251], a[220:223], v[172:175]\n" \
"v_mfma_f32_16x16x32_bf16 v[168:171], a[180:183], a[156:159], v[168:171]\n" \
"v_mfma_f32_16x16x32_bf16 v[168:171], a[244:247], a[220:223], v[168:171]\n" \
"v_mfma_f32_16x16x32_bf16 v[164:167], a[176:179], a[156:159], v[164:167]\n" \
"v_mfma_f32_16x16x32_bf16 v[164:167], a[240:243], a[220:223], v[164:167]\n" \
"ds_read_b128 a[156:159], v28 offset:38912\n" \
"ds_read_b128 a[220:223], v29 offset:38912\n" \
"s_waitcnt lgkmcnt(15)\n" \
"v_mfma_f32_16x16x32_bf16 a[0:3], a[160:163], a[128:131], a[0:3]\n" \
"s_addk_i32 s84, 0x7c00\n" \
"v_mfma_f32_16x16x32_bf16 a[0:3], a[224:227], a[192:195], a[0:3]\n" \
"buffer_load_dwordx4 v27, s[72:75], s84 offen offset:2048 lds\n" \
"v_mfma_f32_16x16x32_bf16 a[4:7], a[164:167], a[128:131], a[4:7]\n" \
"ds_read_b64_tr_b16 a[176:177], v36 offset:32768\n" \
"s_waitcnt lgkmcnt(15)\n" \
"v_mfma_f32_16x16x32_bf16 a[4:7], a[228:231], a[192:195], a[4:7]\n" \
"s_addk_i32 s84, 0x7c00\n" \
"s_waitcnt lgkmcnt(11)\n" \
"v_mfma_f32_16x16x32_bf16 a[8:11], a[168:171], a[128:131], a[8:11]\n" \
"buffer_load_dwordx4 v27, s[72:75], s84 offen offset:3072 lds\n" \
"s_waitcnt lgkmcnt(9)\n" \
"v_mfma_f32_16x16x32_bf16 a[8:11], a[232:235], a[192:195], a[8:11]\n" \
"ds_read_b64_tr_b16 a[178:179], v36 offset:33792\n" \
"s_waitcnt lgkmcnt(8)\n" \
"v_mfma_f32_16x16x32_bf16 a[12:15], a[172:175], a[128:131], a[12:15]\n" \
"ds_read_b64_tr_b16 a[240:241], v36 offset:40960\n" \
"s_waitcnt lgkmcnt(5)\n" \
"v_mfma_f32_16x16x32_bf16 a[12:15], a[236:239], a[192:195], a[12:15]\n" \
"v_mfma_f32_16x16x32_bf16 a[28:31], a[172:175], a[132:135], a[28:31]\n" \
"v_mfma_f32_16x16x32_bf16 a[28:31], a[236:239], a[196:199], a[28:31]\n" \
"ds_read_b64_tr_b16 a[242:243], v36 offset:41984\n" \
"v_mfma_f32_16x16x32_bf16 a[24:27], a[168:171], a[132:135], a[24:27]\n" \
"v_mfma_f32_16x16x32_bf16 a[24:27], a[232:235], a[196:199], a[24:27]\n" \
"v_mfma_f32_16x16x32_bf16 a[20:23], a[164:167], a[132:135], a[20:23]\n" \
"v_mfma_f32_16x16x32_bf16 a[20:23], a[228:231], a[196:199], a[20:23]\n" \
"ds_read_b64_tr_b16 a[180:181], v37 offset:32768\n" \
"v_mfma_f32_16x16x32_bf16 a[16:19], a[160:163], a[132:135], a[16:19]\n" \
"v_mfma_f32_16x16x32_bf16 a[16:19], a[224:227], a[196:199], a[16:19]\n" \
"v_mfma_f32_16x16x32_bf16 a[32:35], a[160:163], a[136:139], a[32:35]\n" \
"ds_read_b64_tr_b16 a[182:183], v37 offset:33792\n" \
"v_mfma_f32_16x16x32_bf16 a[32:35], a[224:227], a[200:203], a[32:35]\n" \
"v_mfma_f32_16x16x32_bf16 a[36:39], a[164:167], a[136:139], a[36:39]\n" \
"v_lshlrev_b32_e32 v45, 3, v20\n" \
"v_add_u32_e32 v45, s31, v45\n" \
"v_mfma_f32_16x16x32_bf16 a[36:39], a[228:231], a[200:203], a[36:39]\n" \
"s_getpc_b64 s[86:87]\n" \
"s_sub_u32 s86, s86, 0xf800\n" \
"s_subb_u32 s87, s87, 0\n" \
"s_mov_b32 m0, s32\n" \
"s_nop 0\n" \
"v_mfma_f32_16x16x32_bf16 a[40:43], a[168:171], a[136:139], a[40:43]\n" \
"global_load_lds_dword v45, s[86:87]\n" \
"v_mfma_f32_16x16x32_bf16 a[40:43], a[232:235], a[200:203], a[40:43]\n" \
"ds_read_b64_tr_b16 a[244:245], v37 offset:40960\n" \
"v_mfma_f32_16x16x32_bf16 a[44:47], a[172:175], a[136:139], a[44:47]\n" \
"ds_read_b64_tr_b16 a[246:247], v37 offset:41984\n" \
"v_mfma_f32_16x16x32_bf16 a[44:47], a[236:239], a[200:203], a[44:47]\n" \
"v_mfma_f32_16x16x32_bf16 a[60:63], a[172:175], a[140:143], a[60:63]\n" \
"v_mfma_f32_16x16x32_bf16 a[60:63], a[236:239], a[204:207], a[60:63]\n" \
"v_mfma_f32_16x16x32_bf16 a[56:59], a[168:171], a[140:143], a[56:59]\n" \
"ds_read_b64_tr_b16 a[184:185], v38 offset:32768\n" \
"v_mfma_f32_16x16x32_bf16 a[56:59], a[232:235], a[204:207], a[56:59]\n" \
"v_mfma_f32_16x16x32_bf16 a[52:55], a[164:167], a[140:143], a[52:55]\n" \
"v_mfma_f32_16x16x32_bf16 a[52:55], a[228:231], a[204:207], a[52:55]\n" \
"ds_read_b64_tr_b16 a[186:187], v38 offset:33792\n" \
"v_mfma_f32_16x16x32_bf16 a[48:51], a[160:163], a[140:143], a[48:51]\n" \
"v_mfma_f32_16x16x32_bf16 a[48:51], a[224:227], a[204:207], a[48:51]\n" \
"v_mfma_f32_16x16x32_bf16 a[64:67], a[160:163], a[144:147], a[64:67]\n" \
"ds_read_b64_tr_b16 a[248:249], v38 offset:40960\n" \
"v_mfma_f32_16x16x32_bf16 a[64:67], a[224:227], a[208:211], a[64:67]\n" \
"v_mfma_f32_16x16x32_bf16 a[68:71], a[164:167], a[144:147], a[68:71]\n" \
"v_mfma_f32_16x16x32_bf16 a[68:71], a[228:231], a[208:211], a[68:71]\n" \
"ds_read_b64_tr_b16 a[250:251], v38 offset:41984\n" \
"v_mfma_f32_16x16x32_bf16 a[72:75], a[168:171], a[144:147], a[72:75]\n" \
"v_mfma_f32_16x16x32_bf16 a[72:75], a[232:235], a[208:211], a[72:75]\n" \
"v_mfma_f32_16x16x32_bf16 a[76:79], a[172:175], a[144:147], a[76:79]\n" \
"v_mfma_f32_16x16x32_bf16 a[76:79], a[236:239], a[208:211], a[76:79]\n" \
"ds_read_b64_tr_b16 a[188:189], v39 offset:32768\n" \
"v_mfma_f32_16x16x32_bf16 a[92:95], a[172:175], a[148:151], a[92:95]\n" \
"v_mfma_f32_16x16x32_bf16 a[92:95], a[236:239], a[212:215], a[92:95]\n" \
"v_mfma_f32_16x16x32_bf16 a[88:91], a[168:171], a[148:151], a[88:91]\n" \
"ds_read_b64_tr_b16 a[190:191], v39 offset:33792\n" \
"v_mfma_f32_16x16x32_bf16 a[88:91], a[232:235], a[212:215], a[88:91]\n" \
"v_mfma_f32_16x16x32_bf16 a[84:87], a[164:167], a[148:151], a[84:87]\n" \
"v_mfma_f32_16x16x32_bf16 a[84:87], a[228:231], a[212:215], a[84:87]\n" \
"ds_read_b64_tr_b16 a[252:253], v39 offset:40960\n" \
"v_mfma_f32_16x16x32_bf16 a[80:83], a[160:163], a[148:151], a[80:83]\n" \
"v_mfma_f32_16x16x32_bf16 a[80:83], a[224:227], a[212:215], a[80:83]\n" \
"v_mfma_f32_16x16x32_bf16 a[96:99], a[160:163], a[152:155], a[96:99]\n" \
"ds_read_b64_tr_b16 a[254:255], v39 offset:41984\n" \
"v_mfma_f32_16x16x32_bf16 a[96:99], a[224:227], a[216:219], a[96:99]\n" \
"v_mfma_f32_16x16x32_bf16 a[100:103], a[164:167], a[152:155], a[100:103]\n" \
"v_mfma_f32_16x16x32_bf16 a[100:103], a[228:231], a[216:219], a[100:103]\n" \
"v_mfma_f32_16x16x32_bf16 a[104:107], a[168:171], a[152:155], a[104:107]\n" \
"v_mfma_f32_16x16x32_bf16 a[104:107], a[232:235], a[216:219], a[104:107]\n" \
"v_mfma_f32_16x16x32_bf16 a[108:111], a[172:175], a[152:155], a[108:111]\n" \
"v_mfma_f32_16x16x32_bf16 a[108:111], a[236:239], a[216:219], a[108:111]\n" \
"s_waitcnt lgkmcnt(15)\n" \
"v_mfma_f32_16x16x32_bf16 a[124:127], a[172:175], a[156:159], a[124:127]\n" \
"v_mfma_f32_16x16x32_bf16 a[124:127], a[236:239], a[220:223], a[124:127]\n" \
"v_mfma_f32_16x16x32_bf16 a[120:123], a[168:171], a[156:159], a[120:123]\n" \
"v_mfma_f32_16x16x32_bf16 a[120:123], a[232:235], a[220:223], a[120:123]\n" \
"v_mfma_f32_16x16x32_bf16 a[116:119], a[164:167], a[156:159], a[116:119]\n" \
"v_mfma_f32_16x16x32_bf16 a[116:119], a[228:231], a[220:223], a[116:119]\n" \
"v_mfma_f32_16x16x32_bf16 a[112:115], a[160:163], a[156:159], a[112:115]\n" \
"v_mfma_f32_16x16x32_bf16 a[112:115], a[224:227], a[220:223], a[112:115]\n" \
"s_waitcnt vmcnt(1) lgkmcnt(0)\n" \
"s_barrier\n" \
"v_mov_b32_e32 v45, s62\n" \
"s_mov_b64 exec, 1\n" \
"buffer_store_dword v45, off, s[80:83], s37 offset:128 sc1\n" \
"s_mov_b64 exec, -1\n" \
"v_mfma_f32_16x16x32_bf16 v[52:55], a[176:179], a[128:131], v[52:55]\n" \
"s_add_u32 m0, s30, 0x8000\n" \
"s_movk_i32 s84, 0x380\n" \
"v_mfma_f32_16x16x32_bf16 v[52:55], a[240:243], a[192:195], v[52:55]\n" \
"buffer_load_dwordx4 v22, s[64:67], s84 offen offset:0 lds\n" \
"v_mfma_f32_16x16x32_bf16 v[56:59], a[180:183], a[128:131], v[56:59]\n" \
"ds_read_b64_tr_b16 a[160:161], v32 offset:0\n" \
"v_mfma_f32_16x16x32_bf16 v[56:59], a[244:247], a[192:195], v[56:59]\n" \
"s_movk_i32 s84, 0x1f80\n" \
"v_mfma_f32_16x16x32_bf16 v[60:63], a[184:187], a[128:131], v[60:63]\n" \
"buffer_load_dwordx4 v23, s[64:67], s84 offen offset:1024 lds\n" \
"v_mfma_f32_16x16x32_bf16 v[60:63], a[248:251], a[192:195], v[60:63]\n" \
"ds_read_b64_tr_b16 a[162:163], v32 offset:1024\n" \
"v_mfma_f32_16x16x32_bf16 v[64:67], a[188:191], a[128:131], v[64:67]\n" \
"ds_read_b64_tr_b16 a[224:225], v32 offset:8192\n" \
"v_mfma_f32_16x16x32_bf16 v[64:67], a[252:255], a[192:195], v[64:67]\n" \
"ds_read_b128 a[128:131], v28 offset:16384\n" \
"v_mfma_f32_16x16x32_bf16 v[80:83], a[188:191], a[132:135], v[80:83]\n" \
"ds_read_b128 a[192:195], v29 offset:16384\n" \
"v_mfma_f32_16x16x32_bf16 v[80:83], a[252:255], a[196:199], v[80:83]\n" \
"s_movk_i32 s84, 0x3b80\n" \
"v_mfma_f32_16x16x32_bf16 v[76:79], a[184:187], a[132:135], v[76:79]\n" \
"buffer_load_dwordx4 v22, s[64:67], s84 offen offset:2048 lds\n" \
"v_mfma_f32_16x16x32_bf16 v[76:79], a[248:251], a[196:199], v[76:79]\n" \
"s_movk_i32 s84, 0x5780\n" \
"v_mfma_f32_16x16x32_bf16 v[72:75], a[180:183], a[132:135], v[72:75]\n" \
"buffer_load_dwordx4 v23, s[64:67], s84 offen offset:3072 lds\n" \
"v_mfma_f32_16x16x32_bf16 v[72:75], a[244:247], a[196:199], v[72:75]\n" \
"s_add_u32 m0, s31, 0x8000\n" \
"s_mov_b32 s84, 0x380000\n" \
"v_mfma_f32_16x16x32_bf16 v[68:71], a[176:179], a[132:135], v[68:71]\n" \
"buffer_load_dwordx4 v26, s[68:71], s84 offen offset:0 lds\n" \
"v_mfma_f32_16x16x32_bf16 v[68:71], a[240:243], a[196:199], v[68:71]\n" \
"ds_read_b128 a[132:135], v28 offset:18432\n" \
"v_mfma_f32_16x16x32_bf16 v[84:87], a[176:179], a[136:139], v[84:87]\n" \
"ds_read_b128 a[196:199], v29 offset:18432\n" \
"v_mfma_f32_16x16x32_bf16 v[84:87], a[240:243], a[200:203], v[84:87]\n" \
"s_addk_i32 s84, 0x7c00\n" \
"v_mfma_f32_16x16x32_bf16 v[88:91], a[180:183], a[136:139], v[88:91]\n" \
"buffer_load_dwordx4 v26, s[68:71], s84 offen offset:1024 lds\n" \
"v_mfma_f32_16x16x32_bf16 v[88:91], a[244:247], a[200:203], v[88:91]\n" \
"ds_read_b64_tr_b16 a[226:227], v32 offset:9216\n" \
"v_mfma_f32_16x16x32_bf16 v[92:95], a[184:187], a[136:139], v[92:95]\n" \
"s_addk_i32 s84, 0x7c00\n" \
"v_mfma_f32_16x16x32_bf16 v[92:95], a[248:251], a[200:203], v[92:95]\n" \
"buffer_load_dwordx4 v27, s[68:71], s84 offen offset:2048 lds\n" \
"v_mfma_f32_16x16x32_bf16 v[96:99], a[188:191], a[136:139], v[96:99]\n" \
"ds_read_b64_tr_b16 a[164:165], v33 offset:0\n" \
"v_mfma_f32_16x16x32_bf16 v[96:99], a[252:255], a[200:203], v[96:99]\n" \
"ds_read_b128 a[136:139], v28 offset:20480\n" \
"v_mfma_f32_16x16x32_bf16 v[112:115], a[188:191], a[140:143], v[112:115]\n" \
"ds_read_b128 a[200:203], v29 offset:20480\n" \
"v_mfma_f32_16x16x32_bf16 v[112:115], a[252:255], a[204:207], v[112:115]\n" \
"s_addk_i32 s84, 0x7c00\n" \
"s_nop 0\n" \
"v_mfma_f32_16x16x32_bf16 v[108:111], a[184:187], a[140:143], v[108:111]\n" \
"buffer_load_dwordx4 v27, s[68:71], s84 offen offset:3072 lds\n" \
"v_mfma_f32_16x16x32_bf16 v[108:111], a[248:251], a[204:207], v[108:111]\n" \
"s_add_u32 m0, s30, 0x9000\n" \
"s_mov_b32 s84, 0x8380\n" \
"v_mfma_f32_16x16x32_bf16 v[104:107], a[180:183], a[140:143], v[104:107]\n" \
"buffer_load_dwordx4 v22, s[64:67], s84 offen offset:0 lds\n" \
"v_mfma_f32_16x16x32_bf16 v[104:107], a[244:247], a[204:207], v[104:107]\n" \
"ds_read_b64_tr_b16 a[166:167], v33 offset:1024\n" \
"v_mfma_f32_16x16x32_bf16 v[100:103], a[176:179], a[140:143], v[100:103]\n" \
"ds_read_b64_tr_b16 a[228:229], v33 offset:8192\n" \
"v_mfma_f32_16x16x32_bf16 v[100:103], a[240:243], a[204:207], v[100:103]\n" \
"ds_read_b128 a[140:143], v28 offset:22528\n" \
"v_mfma_f32_16x16x32_bf16 v[116:119], a[176:179], a[144:147], v[116:119]\n" \
"ds_read_b128 a[204:207], v29 offset:22528\n" \
"v_mfma_f32_16x16x32_bf16 v[116:119], a[240:243], a[208:211], v[116:119]\n" \
"s_addk_i32 s84, 0x1c00\n" \
"v_mfma_f32_16x16x32_bf16 v[120:123], a[180:183], a[144:147], v[120:123]\n" \
"buffer_load_dwordx4 v23, s[64:67], s84 offen offset:1024 lds\n" \
"v_mfma_f32_16x16x32_bf16 v[120:123], a[244:247], a[208:211], v[120:123]\n" \
"s_addk_i32 s84, 0x1c00\n" \
"v_mfma_f32_16x16x32_bf16 v[124:127], a[184:187], a[144:147], v[124:127]\n" \
"buffer_load_dwordx4 v22, s[64:67], s84 offen offset:2048 lds\n" \
"v_mfma_f32_16x16x32_bf16 v[124:127], a[248:251], a[208:211], v[124:127]\n" \
"s_addk_i32 s84, 0x1c00\n" \
"s_nop 0\n" \
"v_mfma_f32_16x16x32_bf16 v[128:131], a[188:191], a[144:147], v[128:131]\n" \
"buffer_load_dwordx4 v23, s[64:67], s84 offen offset:3072 lds\n" \
"v_mfma_f32_16x16x32_bf16 v[128:131], a[252:255], a[208:211], v[128:131]\n" \
"ds_read_b128 a[144:147], v28 offset:0\n" \
"v_mfma_f32_16x16x32_bf16 v[144:147], a[188:191], a[148:151], v[144:147]\n" \
"ds_read_b128 a[208:211], v29 offset:0\n" \
"v_mfma_f32_16x16x32_bf16 v[144:147], a[252:255], a[212:215], v[144:147]\n" \
"s_add_u32 m0, s31, 0xc000\n" \
"s_mov_b32 s84, 0x380000\n" \
"v_mfma_f32_16x16x32_bf16 v[140:143], a[184:187], a[148:151], v[140:143]\n" \
"buffer_load_dwordx4 v26, s[72:75], s84 offen offset:0 lds\n" \
"v_mfma_f32_16x16x32_bf16 v[140:143], a[248:251], a[212:215], v[140:143]\n" \
"ds_read_b64_tr_b16 a[230:231], v33 offset:9216\n" \
"v_mfma_f32_16x16x32_bf16 v[136:139], a[180:183], a[148:151], v[136:139]\n" \
"s_addk_i32 s84, 0x7c00\n" \
"s_nop 0\n" \
"v_mfma_f32_16x16x32_bf16 v[136:139], a[244:247], a[212:215], v[136:139]\n" \
"buffer_load_dwordx4 v26, s[72:75], s84 offen offset:1024 lds\n" \
"v_mfma_f32_16x16x32_bf16 v[132:135], a[176:179], a[148:151], v[132:135]\n" \
"ds_read_b64_tr_b16 a[168:169], v34 offset:0\n" \
"v_mfma_f32_16x16x32_bf16 v[132:135], a[240:243], a[212:215], v[132:135]\n" \
"ds_read_b128 a[148:151], v28 offset:2048\n" \
"v_mfma_f32_16x16x32_bf16 v[148:151], a[176:179], a[152:155], v[148:151]\n" \
"ds_read_b128 a[212:215], v29 offset:2048\n" \
"v_mfma_f32_16x16x32_bf16 v[148:151], a[240:243], a[216:219], v[148:151]\n" \
"ds_read_b64_tr_b16 a[170:171], v34 offset:1024\n" \
"v_mfma_f32_16x16x32_bf16 v[152:155], a[180:183], a[152:155], v[152:155]\n" \
"ds_read_b64_tr_b16 a[232:233], v34 offset:8192\n" \
"v_mfma_f32_16x16x32_bf16 v[152:155], a[244:247], a[216:219], v[152:155]\n" \
"ds_read_b64_tr_b16 a[234:235], v34 offset:9216\n" \
"v_mfma_f32_16x16x32_bf16 v[156:159], a[184:187], a[152:155], v[156:159]\n" \
"ds_read_b64_tr_b16 a[172:173], v35 offset:0\n" \
"v_mfma_f32_16x16x32_bf16 v[156:159], a[248:251], a[216:219], v[156:159]\n" \
"ds_read_b64_tr_b16 a[174:175], v35 offset:1024\n" \
"v_mfma_f32_16x16x32_bf16 v[160:163], a[188:191], a[152:155], v[160:163]\n" \
"ds_read_b64_tr_b16 a[236:237], v35 offset:8192\n" \
"v_mfma_f32_16x16x32_bf16 v[160:163], a[252:255], a[216:219], v[160:163]\n" \
"ds_read_b128 a[152:155], v28 offset:4096\n" \
"v_mfma_f32_16x16x32_bf16 v[176:179], a[188:191], a[156:159], v[176:179]\n" \
"ds_read_b128 a[216:219], v29 offset:4096\n" \
"v_mfma_f32_16x16x32_bf16 v[176:179], a[252:255], a[220:223], v[176:179]\n" \
"ds_read_b64_tr_b16 a[238:239], v35 offset:9216\n" \
"v_mfma_f32_16x16x32_bf16 v[172:175], a[184:187], a[156:159], v[172:175]\n" \
"v_mfma_f32_16x16x32_bf16 v[172:175], a[248:251], a[220:223], v[172:175]\n" \
"v_mfma_f32_16x16x32_bf16 v[168:171], a[180:183], a[156:159], v[168:171]\n" \
"v_mfma_f32_16x16x32_bf16 v[168:171], a[244:247], a[220:223], v[168:171]\n" \
"v_mfma_f32_16x16x32_bf16 v[164:167], a[176:179], a[156:159], v[164:167]\n" \
"v_mfma_f32_16x16x32_bf16 v[164:167], a[240:243], a[220:223], v[164:167]\n" \
"ds_read_b128 a[156:159], v28 offset:6144\n" \
"ds_read_b128 a[220:223], v29 offset:6144\n" \
"s_waitcnt lgkmcnt(15)\n" \
"v_mfma_f32_16x16x32_bf16 a[0:3], a[160:163], a[128:131], a[0:3]\n" \
"s_addk_i32 s84, 0x7c00\n" \
"v_mfma_f32_16x16x32_bf16 a[0:3], a[224:227], a[192:195], a[0:3]\n" \
"buffer_load_dwordx4 v27, s[72:75], s84 offen offset:2048 lds\n" \
"v_mfma_f32_16x16x32_bf16 a[4:7], a[164:167], a[128:131], a[4:7]\n" \
"ds_read_b64_tr_b16 a[176:177], v36 offset:0\n" \
"s_waitcnt lgkmcnt(15)\n" \
"v_mfma_f32_16x16x32_bf16 a[4:7], a[228:231], a[192:195], a[4:7]\n" \
"s_addk_i32 s84, 0x7c00\n" \
"s_waitcnt lgkmcnt(11)\n" \
"v_mfma_f32_16x16x32_bf16 a[8:11], a[168:171], a[128:131], a[8:11]\n" \
"buffer_load_dwordx4 v27, s[72:75], s84 offen offset:3072 lds\n" \
"s_waitcnt lgkmcnt(9)\n" \
"v_mfma_f32_16x16x32_bf16 a[8:11], a[232:235], a[192:195], a[8:11]\n" \
"ds_read_b64_tr_b16 a[178:179], v36 offset:1024\n" \
"s_waitcnt lgkmcnt(8)\n" \
"v_mfma_f32_16x16x32_bf16 a[12:15], a[172:175], a[128:131], a[12:15]\n" \
"ds_read_b64_tr_b16 a[240:241], v36 offset:8192\n" \
"s_waitcnt lgkmcnt(5)\n" \
"v_mfma_f32_16x16x32_bf16 a[12:15], a[236:239], a[192:195], a[12:15]\n" \
"v_mfma_f32_16x16x32_bf16 a[28:31], a[172:175], a[132:135], a[28:31]\n" \
"v_mfma_f32_16x16x32_bf16 a[28:31], a[236:239], a[196:199], a[28:31]\n" \
"ds_read_b64_tr_b16 a[242:243], v36 offset:9216\n" \
"v_mfma_f32_16x16x32_bf16 a[24:27], a[168:171], a[132:135], a[24:27]\n" \
"v_mfma_f32_16x16x32_bf16 a[24:27], a[232:235], a[196:199], a[24:27]\n" \
"v_mfma_f32_16x16x32_bf16 a[20:23], a[164:167], a[132:135], a[20:23]\n" \
"v_mfma_f32_16x16x32_bf16 a[20:23], a[228:231], a[196:199], a[20:23]\n" \
"ds_read_b64_tr_b16 a[180:181], v37 offset:0\n" \
"v_mfma_f32_16x16x32_bf16 a[16:19], a[160:163], a[132:135], a[16:19]\n" \
"v_mfma_f32_16x16x32_bf16 a[16:19], a[224:227], a[196:199], a[16:19]\n" \
"v_mfma_f32_16x16x32_bf16 a[32:35], a[160:163], a[136:139], a[32:35]\n" \
"v_bfe_u32 v44, v20, 2, 5\n" \
"ds_read_b64_tr_b16 a[182:183], v37 offset:1024\n" \
"v_mfma_f32_16x16x32_bf16 a[32:35], a[224:227], a[200:203], a[32:35]\n" \
"buffer_load_dword v44, v44, s[80:83], s38 offen offset:128 sc1\n" \
"v_mfma_f32_16x16x32_bf16 a[36:39], a[164:167], a[136:139], a[36:39]\n" \
"v_mfma_f32_16x16x32_bf16 a[36:39], a[228:231], a[200:203], a[36:39]\n" \
"ds_read_b64_tr_b16 a[244:245], v37 offset:8192\n" \
"v_mfma_f32_16x16x32_bf16 a[40:43], a[168:171], a[136:139], a[40:43]\n" \
"v_mfma_f32_16x16x32_bf16 a[40:43], a[232:235], a[200:203], a[40:43]\n" \
"v_mfma_f32_16x16x32_bf16 a[44:47], a[172:175], a[136:139], a[44:47]\n" \
"ds_read_b64_tr_b16 a[246:247], v37 offset:9216\n" \
"v_mfma_f32_16x16x32_bf16 a[44:47], a[236:239], a[200:203], a[44:47]\n" \
"v_mfma_f32_16x16x32_bf16 a[60:63], a[172:175], a[140:143], a[60:63]\n" \
"global_store_dwordx4 v49, v[0:3], s[28:29] offset:0 sc1\n" \
"v_mfma_f32_16x16x32_bf16 a[60:63], a[236:239], a[204:207], a[60:63]\n" \
"v_mfma_f32_16x16x32_bf16 a[56:59], a[168:171], a[140:143], a[56:59]\n" \
"ds_read_b64_tr_b16 a[184:185], v38 offset:0\n" \
"v_mfma_f32_16x16x32_bf16 a[56:59], a[232:235], a[204:207], a[56:59]\n" \
"v_mfma_f32_16x16x32_bf16 a[52:55], a[164:167], a[140:143], a[52:55]\n" \
"v_mfma_f32_16x16x32_bf16 a[52:55], a[228:231], a[204:207], a[52:55]\n" \
"ds_read_b64_tr_b16 a[186:187], v38 offset:1024\n" \
"v_mfma_f32_16x16x32_bf16 a[48:51], a[160:163], a[140:143], a[48:51]\n" \
"v_mfma_f32_16x16x32_bf16 a[48:51], a[224:227], a[204:207], a[48:51]\n" \
"v_mfma_f32_16x16x32_bf16 a[64:67], a[160:163], a[144:147], a[64:67]\n" \
"global_store_dwordx4 v49, v[0:3], s[28:29] offset:1024 sc1\n" \
"v_mfma_f32_16x16x32_bf16 a[64:67], a[224:227], a[208:211], a[64:67]\n" \
"ds_read_b64_tr_b16 a[248:249], v38 offset:8192\n" \
"v_mfma_f32_16x16x32_bf16 a[68:71], a[164:167], a[144:147], a[68:71]\n" \
"v_mfma_f32_16x16x32_bf16 a[68:71], a[228:231], a[208:211], a[68:71]\n" \
"ds_read_b64_tr_b16 a[250:251], v38 offset:9216\n" \
"v_mfma_f32_16x16x32_bf16 a[72:75], a[168:171], a[144:147], a[72:75]\n" \
"v_mfma_f32_16x16x32_bf16 a[72:75], a[232:235], a[208:211], a[72:75]\n" \
"v_mfma_f32_16x16x32_bf16 a[76:79], a[172:175], a[144:147], a[76:79]\n" \
"v_mfma_f32_16x16x32_bf16 a[76:79], a[236:239], a[208:211], a[76:79]\n" \
"ds_read_b64_tr_b16 a[188:189], v39 offset:0\n" \
"v_mfma_f32_16x16x32_bf16 a[92:95], a[172:175], a[148:151], a[92:95]\n" \
"s_mov_b64 exec, 0xffff\n" \
"global_store_dwordx4 v49, v[0:3], s[28:29] offset:2048 sc1\n" \
"s_mov_b64 exec, -1\n" \
"s_nop 0\n" \
"v_mfma_f32_16x16x32_bf16 a[92:95], a[236:239], a[212:215], a[92:95]\n" \
"v_mfma_f32_16x16x32_bf16 a[88:91], a[168:171], a[148:151], a[88:91]\n" \
"ds_read_b64_tr_b16 a[190:191], v39 offset:1024\n" \
"v_mfma_f32_16x16x32_bf16 a[88:91], a[232:235], a[212:215], a[88:91]\n" \
"v_mfma_f32_16x16x32_bf16 a[84:87], a[164:167], a[148:151], a[84:87]\n" \
"v_mfma_f32_16x16x32_bf16 a[84:87], a[228:231], a[212:215], a[84:87]\n" \
"ds_read_b64_tr_b16 a[252:253], v39 offset:8192\n" \
"v_mfma_f32_16x16x32_bf16 a[80:83], a[160:163], a[148:151], a[80:83]\n" \
"v_mfma_f32_16x16x32_bf16 a[80:83], a[224:227], a[212:215], a[80:83]\n" \
"v_mfma_f32_16x16x32_bf16 a[96:99], a[160:163], a[152:155], a[96:99]\n" \
"ds_read_b64_tr_b16 a[254:255], v39 offset:9216\n" \
"v_mfma_f32_16x16x32_bf16 a[96:99], a[224:227], a[216:219], a[96:99]\n" \
"v_mfma_f32_16x16x32_bf16 a[100:103], a[164:167], a[152:155], a[100:103]\n" \
"v_mfma_f32_16x16x32_bf16 a[100:103], a[228:231], a[216:219], a[100:103]\n" \
"v_mfma_f32_16x16x32_bf16 a[104:107], a[168:171], a[152:155], a[104:107]\n" \
"v_mfma_f32_16x16x32_bf16 a[104:107], a[232:235], a[216:219], a[104:107]\n" \
"v_mfma_f32_16x16x32_bf16 a[108:111], a[172:175], a[152:155], a[108:111]\n" \
"v_mfma_f32_16x16x32_bf16 a[108:111], a[236:239], a[216:219], a[108:111]\n" \
"s_waitcnt lgkmcnt(15)\n" \
"v_mfma_f32_16x16x32_bf16 a[124:127], a[172:175], a[156:159], a[124:127]\n" \
"v_mfma_f32_16x16x32_bf16 a[124:127], a[236:239], a[220:223], a[124:127]\n" \
"v_mfma_f32_16x16x32_bf16 a[120:123], a[168:171], a[156:159], a[120:123]\n" \
"v_mfma_f32_16x16x32_bf16 a[120:123], a[232:235], a[220:223], a[120:123]\n" \
"v_mfma_f32_16x16x32_bf16 a[116:119], a[164:167], a[156:159], a[116:119]\n" \
"v_mfma_f32_16x16x32_bf16 a[116:119], a[228:231], a[220:223], a[116:119]\n" \
"v_mfma_f32_16x16x32_bf16 a[112:115], a[160:163], a[156:159], a[112:115]\n" \
"v_mfma_f32_16x16x32_bf16 a[112:115], a[224:227], a[220:223], a[112:115]\n" \
"s_waitcnt vmcnt(3) lgkmcnt(0)\n" \
"s_barrier\n" \
"v_cmp_ne_u32_e32 vcc, s62, v44\n" \
"s_nop 4\n" \
"s_cbranch_vccnz L_poll60_%=\n" \
"L_pollr60_%=:\n" \
"s_mov_b32 s68, s12\n" \
"s_and_b32 s69, s13, 0xffff\n" \
"s_mov_b32 s70, 0x480000\n" \
"s_mov_b32 s71, 0x20000\n" \
"v_mfma_f32_16x16x32_bf16 v[52:55], a[176:179], a[128:131], v[52:55]\n" \
"s_add_u32 m0, s40, 0x0\n" \
"s_add_u32 s85, s39, 0x0\n" \
"s_nop 0\n" \
"v_mfma_f32_16x16x32_bf16 v[52:55], a[240:243], a[192:195], v[52:55]\n" \
"buffer_load_dwordx4 v20, s[80:83], s85 offen sc1 lds\n" \
"v_mfma_f32_16x16x32_bf16 v[56:59], a[180:183], a[128:131], v[56:59]\n" \
"ds_read_b64_tr_b16 a[160:161], v32 offset:32768\n" \
"v_mfma_f32_16x16x32_bf16 v[56:59], a[244:247], a[192:195], v[56:59]\n" \
"s_add_u32 m0, s40, 0x1000\n" \
"s_add_u32 s85, s39, 0x1000\n" \
"v_mfma_f32_16x16x32_bf16 v[60:63], a[184:187], a[128:131], v[60:63]\n" \
"buffer_load_dwordx4 v20, s[80:83], s85 offen sc1 lds\n" \
".fill (L_pb + 16096 - .) / 4, 4, 0xBF800000\n" \
"L_pp4:\n" \
"s_cbranch_execz L_pret\n" \
"v_mfma_f32_16x16x32_bf16 v[60:63], a[248:251], a[192:195], v[60:63]\n" \
"ds_read_b64_tr_b16 a[162:163], v32 offset:33792\n" \
"v_mfma_f32_16x16x32_bf16 v[64:67], a[188:191], a[128:131], v[64:67]\n" \
"ds_read_b64_tr_b16 a[224:225], v32 offset:40960\n" \
"v_mfma_f32_16x16x32_bf16 v[64:67], a[252:255], a[192:195], v[64:67]\n" \
"ds_read_b128 a[128:131], v28 offset:49152\n" \
"v_mfma_f32_16x16x32_bf16 v[80:83], a[188:191], a[132:135], v[80:83]\n" \
"ds_read_b128 a[192:195], v29 offset:49152\n" \
"v_mfma_f32_16x16x32_bf16 v[80:83], a[252:255], a[196:199], v[80:83]\n" \
"s_add_u32 m0, s40, 0x3000\n" \
"s_movk_i32 s84, 0x0\n" \
"v_mfma_f32_16x16x32_bf16 v[76:79], a[184:187], a[132:135], v[76:79]\n" \
"buffer_load_dwordx4 v21, s[76:79], s84 offen offset:0 lds\n" \
"v_mfma_f32_16x16x32_bf16 v[76:79], a[248:251], a[196:199], v[76:79]\n" \
"s_add_u32 m0, s30, 0x0\n" \
"s_movk_i32 s84, 0x0\n" \
"v_mfma_f32_16x16x32_bf16 v[72:75], a[180:183], a[132:135], v[72:75]\n" \
"buffer_load_dwordx4 v24, s[76:79], s84 offen offset:0 lds\n" \
"v_mfma_f32_16x16x32_bf16 v[72:75], a[244:247], a[196:199], v[72:75]\n" \
"ds_read_b64_tr_b16 a[226:227], v32 offset:41984\n" \
"v_mfma_f32_16x16x32_bf16 v[68:71], a[176:179], a[132:135], v[68:71]\n" \
"ds_read_b64_tr_b16 a[164:165], v33 offset:32768\n" \
"v_mfma_f32_16x16x32_bf16 v[68:71], a[240:243], a[196:199], v[68:71]\n" \
"ds_read_b128 a[132:135], v28 offset:51200\n" \
"v_mfma_f32_16x16x32_bf16 v[84:87], a[176:179], a[136:139], v[84:87]\n" \
"ds_read_b128 a[196:199], v29 offset:51200\n" \
"v_mfma_f32_16x16x32_bf16 v[84:87], a[240:243], a[200:203], v[84:87]\n" \
"s_movk_i32 s84, 0x2000\n" \
"v_mfma_f32_16x16x32_bf16 v[88:91], a[180:183], a[136:139], v[88:91]\n" \
"buffer_load_dwordx4 v25, s[76:79], s84 offen offset:1024 lds\n" \
"v_mfma_f32_16x16x32_bf16 v[88:91], a[244:247], a[200:203], v[88:91]\n" \
"s_movk_i32 s84, 0x4000\n" \
"v_mfma_f32_16x16x32_bf16 v[92:95], a[184:187], a[136:139], v[92:95]\n" \
"buffer_load_dwordx4 v24, s[76:79], s84 offen offset:2048 lds\n" \
"v_mfma_f32_16x16x32_bf16 v[92:95], a[248:251], a[200:203], v[92:95]\n" \
"ds_read_b64_tr_b16 a[166:167], v33 offset:33792\n" \
"v_mfma_f32_16x16x32_bf16 v[96:99], a[188:191], a[136:139], v[96:99]\n" \
"ds_read_b64_tr_b16 a[228:229], v33 offset:40960\n" \
"v_mfma_f32_16x16x32_bf16 v[96:99], a[252:255], a[200:203], v[96:99]\n" \
"ds_read_b128 a[136:139], v28 offset:53248\n" \
"v_mfma_f32_16x16x32_bf16 v[112:115], a[188:191], a[140:143], v[112:115]\n" \
"ds_read_b128 a[200:203], v29 offset:53248\n" \
"v_mfma_f32_16x16x32_bf16 v[112:115], a[252:255], a[204:207], v[112:115]\n" \
"s_movk_i32 s84, 0x6000\n" \
"v_mfma_f32_16x16x32_bf16 v[108:111], a[184:187], a[140:143], v[108:111]\n" \
"buffer_load_dwordx4 v25, s[76:79], s84 offen offset:3072 lds\n" \
"v_mfma_f32_16x16x32_bf16 v[108:111], a[248:251], a[204:207], v[108:111]\n" \
"s_add_u32 m0, s31, 0x0\n" \
"s_movk_i32 s84, 0x0\n" \
"v_mfma_f32_16x16x32_bf16 v[104:107], a[180:183], a[140:143], v[104:107]\n" \
"buffer_load_dwordx4 v26, s[68:71], s84 offen offset:0 lds\n" \
"v_mfma_f32_16x16x32_bf16 v[104:107], a[244:247], a[204:207], v[104:107]\n" \
"s_movk_i32 s84, 0x7c00\n" \
"v_mfma_f32_16x16x32_bf16 v[100:103], a[176:179], a[140:143], v[100:103]\n" \
"buffer_load_dwordx4 v26, s[68:71], s84 offen offset:1024 lds\n" \
"v_mfma_f32_16x16x32_bf16 v[100:103], a[240:243], a[204:207], v[100:103]\n" \
"ds_read_b128 a[140:143], v28 offset:55296\n" \
"v_mfma_f32_16x16x32_bf16 v[116:119], a[176:179], a[144:147], v[116:119]\n" \
"ds_read_b128 a[204:207], v29 offset:55296\n" \
"v_mfma_f32_16x16x32_bf16 v[116:119], a[240:243], a[208:211], v[116:119]\n" \
"s_addk_i32 s84, 0x7c00\n" \
"v_mfma_f32_16x16x32_bf16 v[120:123], a[180:183], a[144:147], v[120:123]\n" \
"buffer_load_dwordx4 v27, s[68:71], s84 offen offset:2048 lds\n" \
"v_mfma_f32_16x16x32_bf16 v[120:123], a[244:247], a[208:211], v[120:123]\n" \
"ds_read_b64_tr_b16 a[230:231], v33 offset:41984\n" \
"v_mfma_f32_16x16x32_bf16 v[124:127], a[184:187], a[144:147], v[124:127]\n" \
"s_addk_i32 s84, 0x7c00\n" \
"v_mfma_f32_16x16x32_bf16 v[124:127], a[248:251], a[208:211], v[124:127]\n" \
"buffer_load_dwordx4 v27, s[68:71], s84 offen offset:3072 lds\n" \
"v_mfma_f32_16x16x32_bf16 v[128:131], a[188:191], a[144:147], v[128:131]\n" \
"ds_read_b64_tr_b16 a[168:169], v34 offset:32768\n" \
"v_mfma_f32_16x16x32_bf16 v[128:131], a[252:255], a[208:211], v[128:131]\n" \
"ds_read_b128 a[144:147], v28 offset:32768\n" \
"v_mfma_f32_16x16x32_bf16 v[144:147], a[188:191], a[148:151], v[144:147]\n" \
"ds_read_b128 a[208:211], v29 offset:32768\n" \
"v_mfma_f32_16x16x32_bf16 v[144:147], a[252:255], a[212:215], v[144:147]\n" \
"s_add_u32 m0, s30, 0x1000\n" \
"s_mov_b32 s84, 0x9000\n" \
"v_mfma_f32_16x16x32_bf16 v[140:143], a[184:187], a[148:151], v[140:143]\n" \
"buffer_load_dwordx4 v24, s[76:79], s84 offen offset:0 lds\n" \
"v_mfma_f32_16x16x32_bf16 v[140:143], a[248:251], a[212:215], v[140:143]\n" \
"ds_read_b64_tr_b16 a[170:171], v34 offset:33792\n" \
"v_mfma_f32_16x16x32_bf16 v[136:139], a[180:183], a[148:151], v[136:139]\n" \
"s_addk_i32 s84, 0x2000\n" \
"s_nop 0\n" \
"v_mfma_f32_16x16x32_bf16 v[136:139], a[244:247], a[212:215], v[136:139]\n" \
"buffer_load_dwordx4 v25, s[76:79], s84 offen offset:1024 lds\n" \
"v_mfma_f32_16x16x32_bf16 v[132:135], a[176:179], a[148:151], v[132:135]\n" \
"ds_read_b64_tr_b16 a[232:233], v34 offset:40960\n" \
"v_mfma_f32_16x16x32_bf16 v[132:135], a[240:243], a[212:215], v[132:135]\n" \
"ds_read_b128 a[148:151], v28 offset:34816\n" \
"v_mfma_f32_16x16x32_bf16 v[148:151], a[176:179], a[152:155], v[148:151]\n" \
"ds_read_b128 a[212:215], v29 offset:34816\n" \
"v_mfma_f32_16x16x32_bf16 v[148:151], a[240:243], a[216:219], v[148:151]\n" \
"ds_read_b64_tr_b16 a[234:235], v34 offset:41984\n" \
"v_mfma_f32_16x16x32_bf16 v[152:155], a[180:183], a[152:155], v[152:155]\n" \
"ds_read_b64_tr_b16 a[172:173], v35 offset:32768\n" \
"v_mfma_f32_16x16x32_bf16 v[152:155], a[244:247], a[216:219], v[152:155]\n" \
"ds_read_b64_tr_b16 a[174:175], v35 offset:33792\n" \
"v_mfma_f32_16x16x32_bf16 v[156:159], a[184:187], a[152:155], v[156:159]\n" \
"ds_read_b64_tr_b16 a[236:237], v35 offset:40960\n" \
"v_mfma_f32_16x16x32_bf16 v[156:159], a[248:251], a[216:219], v[156:159]\n" \
"ds_read_b64_tr_b16 a[238:239], v35 offset:41984\n" \
"v_mfma_f32_16x16x32_bf16 v[160:163], a[188:191], a[152:155], v[160:163]\n" \
"v_mfma_f32_16x16x32_bf16 v[160:163], a[252:255], a[216:219], v[160:163]\n" \
"ds_read_b128 a[152:155], v28 offset:36864\n" \
"v_mfma_f32_16x16x32_bf16 v[176:179], a[188:191], a[156:159], v[176:179]\n" \
"ds_read_b128 a[216:219], v29 offset:36864\n" \
"v_mfma_f32_16x16x32_bf16 v[176:179], a[252:255], a[220:223], v[176:179]\n" \
"v_mfma_f32_16x16x32_bf16 v[172:175], a[184:187], a[156:159], v[172:175]\n" \
"v_mfma_f32_16x16x32_bf16 v[172:175], a[248:251], a[220:223], v[172:175]\n" \
"v_mfma_f32_16x16x32_bf16 v[168:171], a[180:183], a[156:159], v[168:171]\n" \
"v_mfma_f32_16x16x32_bf16 v[168:171], a[244:247], a[220:223], v[168:171]\n" \
"v_mfma_f32_16x16x32_bf16 v[164:167], a[176:179], a[156:159], v[164:167]\n" \
"v_mfma_f32_16x16x32_bf16 v[164:167], a[240:243], a[220:223], v[164:167]\n" \
"ds_read_b128 a[156:159], v28 offset:38912\n" \
"ds_read_b128 a[220:223], v29 offset:38912\n" \
"s_waitcnt lgkmcnt(15)\n" \
"v_mfma_f32_16x16x32_bf16 a[0:3], a[160:163], a[128:131], a[0:3]\n" \
"s_addk_i32 s84, 0x2000\n" \
"v_mfma_f32_16x16x32_bf16 a[0:3], a[224:227], a[192:195], a[0:3]\n" \
"buffer_load_dwordx4 v24, s[76:79], s84 offen offset:2048 lds\n" \
"v_mfma_f32_16x16x32_bf16 a[4:7], a[164:167], a[128:131], a[4:7]\n" \
"ds_read_b64_tr_b16 a[176:177], v36 offset:32768\n" \
"v_mfma_f32_16x16x32_bf16 a[4:7], a[228:231], a[192:195], a[4:7]\n" \
"s_addk_i32 s84, 0x2000\n" \
"s_waitcnt lgkmcnt(13)\n" \
"v_mfma_f32_16x16x32_bf16 a[8:11], a[168:171], a[128:131], a[8:11]\n" \
"buffer_load_dwordx4 v25, s[76:79], s84 offen offset:3072 lds\n" \
"s_waitcnt lgkmcnt(9)\n" \
"v_mfma_f32_16x16x32_bf16 a[8:11], a[232:235], a[192:195], a[8:11]\n" \
"ds_read_b64_tr_b16 a[178:179], v36 offset:33792\n" \
"s_waitcnt lgkmcnt(8)\n" \
"v_mfma_f32_16x16x32_bf16 a[12:15], a[172:175], a[128:131], a[12:15]\n" \
"ds_read_b64_tr_b16 a[240:241], v36 offset:40960\n" \
"v_accvgpr_read_b32 v184, a0\n" \
"s_waitcnt lgkmcnt(7)\n" \
"v_mfma_f32_16x16x32_bf16 a[12:15], a[236:239], a[192:195], a[12:15]\n" \
"v_accvgpr_read_b32 v185, a1\n" \
"v_accvgpr_read_b32 v186, a2\n" \
"v_mfma_f32_16x16x32_bf16 a[28:31], a[172:175], a[132:135], a[28:31]\n" \
"v_accvgpr_read_b32 v187, a3\n" \
"v_accvgpr_read_b32 v180, a4\n" \
"v_mfma_f32_16x16x32_bf16 a[28:31], a[236:239], a[196:199], a[28:31]\n" \
"s_add_u32 m0, s30, 0x20000\n" \
"s_movk_i32 s84, 0x100\n" \
"v_mfma_f32_16x16x32_bf16 a[24:27], a[168:171], a[132:135], a[24:27]\n" \
"buffer_load_dwordx4 v24, s[76:79], s84 offen offset:0 lds\n" \
"v_mfma_f32_16x16x32_bf16 a[24:27], a[232:235], a[196:199], a[24:27]\n" \
"ds_read_b64_tr_b16 a[242:243], v36 offset:41984\n" \
"v_accvgpr_read_b32 v181, a5\n" \
"v_mfma_f32_16x16x32_bf16 a[20:23], a[164:167], a[132:135], a[20:23]\n" \
"v_accvgpr_read_b32 v182, a6\n" \
"v_accvgpr_read_b32 v183, a7\n" \
"v_mfma_f32_16x16x32_bf16 a[20:23], a[228:231], a[196:199], a[20:23]\n" \
"ds_read_b64_tr_b16 a[180:181], v37 offset:32768\n" \
"v_cvt_pk_bf16_f32 v184, v184, v185\n" \
"v_mfma_f32_16x16x32_bf16 a[16:19], a[160:163], a[132:135], a[16:19]\n" \
"s_movk_i32 s84, 0x2100\n" \
"s_nop 0\n" \
"v_mfma_f32_16x16x32_bf16 a[16:19], a[224:227], a[196:199], a[16:19]\n" \
"buffer_load_dwordx4 v25, s[76:79], s84 offen offset:1024 lds\n" \
"v_mfma_f32_16x16x32_bf16 a[32:35], a[160:163], a[136:139], a[32:35]\n" \
"ds_read_b64_tr_b16 a[182:183], v37 offset:33792\n" \
"v_cvt_pk_bf16_f32 v185, v186, v187\n" \
"v_mfma_f32_16x16x32_bf16 a[32:35], a[224:227], a[200:203], a[32:35]\n" \
"v_cvt_pk_bf16_f32 v186, v180, v181\n" \
"v_cvt_pk_bf16_f32 v187, v182, v183\n" \
"v_mfma_f32_16x16x32_bf16 a[36:39], a[164:167], a[136:139], a[36:39]\n" \
"v_accvgpr_read_b32 v188, a8\n" \
"v_accvgpr_read_b32 v189, a9\n" \
"v_mfma_f32_16x16x32_bf16 a[36:39], a[228:231], a[200:203], a[36:39]\n" \
"s_movk_i32 s84, 0x4100\n" \
"s_nop 0\n" \
"v_mfma_f32_16x16x32_bf16 a[40:43], a[168:171], a[136:139], a[40:43]\n" \
"buffer_load_dwordx4 v24, s[76:79], s84 offen offset:2048 lds\n" \
"v_mfma_f32_16x16x32_bf16 a[40:43], a[232:235], a[200:203], a[40:43]\n" \
"ds_read_b64_tr_b16 a[244:245], v37 offset:40960\n" \
"v_accvgpr_read_b32 v190, a10\n" \
"v_mfma_f32_16x16x32_bf16 a[44:47], a[172:175], a[136:139], a[44:47]\n" \
"ds_read_b64_tr_b16 a[246:247], v37 offset:41984\n" \
"v_accvgpr_read_b32 v191, a11\n" \
"v_mfma_f32_16x16x32_bf16 a[44:47], a[236:239], a[200:203], a[44:47]\n" \
"v_accvgpr_read_b32 v180, a12\n" \
"v_accvgpr_read_b32 v181, a13\n" \
"v_mfma_f32_16x16x32_bf16 a[60:63], a[172:175], a[140:143], a[60:63]\n" \
"s_movk_i32 s84, 0x6100\n" \
"v_mfma_f32_16x16x32_bf16 a[60:63], a[236:239], a[204:207], a[60:63]\n" \
"buffer_load_dwordx4 v25, s[76:79], s84 offen offset:3072 lds\n" \
"v_mfma_f32_16x16x32_bf16 a[56:59], a[168:171], a[140:143], a[56:59]\n" \
"ds_read_b64_tr_b16 a[184:185], v38 offset:32768\n" \
"v_accvgpr_read_b32 v182, a14\n" \
"v_mfma_f32_16x16x32_bf16 a[56:59], a[232:235], a[204:207], a[56:59]\n" \
"v_accvgpr_read_b32 v183, a15\n" \
"v_cvt_pk_bf16_f32 v188, v188, v189\n" \
"v_mfma_f32_16x16x32_bf16 a[52:55], a[164:167], a[140:143], a[52:55]\n" \
"s_add_u32 m0, s30, 0x21000\n" \
"s_addk_i32 s84, 0x3000\n" \
"v_mfma_f32_16x16x32_bf16 a[52:55], a[228:231], a[204:207], a[52:55]\n" \
"buffer_load_dwordx4 v24, s[76:79], s84 offen offset:0 lds\n" \
"v_mfma_f32_16x16x32_bf16 a[48:51], a[160:163], a[140:143], a[48:51]\n" \
"ds_read_b64_tr_b16 a[186:187], v38 offset:33792\n" \
"v_cvt_pk_bf16_f32 v189, v190, v191\n" \
"v_mfma_f32_16x16x32_bf16 a[48:51], a[224:227], a[204:207], a[48:51]\n" \
"v_cvt_pk_bf16_f32 v190, v180, v181\n" \
"v_cvt_pk_bf16_f32 v191, v182, v183\n" \
"v_mfma_f32_16x16x32_bf16 a[64:67], a[160:163], a[144:147], a[64:67]\n" \
"ds_read_b64_tr_b16 a[248:249], v38 offset:40960\n" \
"v_accvgpr_read_b32 v192, a24\n" \
"v_mfma_f32_16x16x32_bf16 a[64:67], a[224:227], a[208:211], a[64:67]\n" \
"s_addk_i32 s84, 0x2000\n" \
"s_nop 0\n" \
"v_mfma_f32_16x16x32_bf16 a[68:71], a[164:167], a[144:147], a[68:71]\n" \
"buffer_load_dwordx4 v25, s[76:79], s84 offen offset:1024 lds\n" \
"v_mfma_f32_16x16x32_bf16 a[68:71], a[228:231], a[208:211], a[68:71]\n" \
"ds_read_b64_tr_b16 a[250:251], v38 offset:41984\n" \
"v_accvgpr_read_b32 v193, a25\n" \
"v_mfma_f32_16x16x32_bf16 a[72:75], a[168:171], a[144:147], a[72:75]\n" \
"v_accvgpr_read_b32 v194, a26\n" \
"v_accvgpr_read_b32 v195, a27\n" \
"v_mfma_f32_16x16x32_bf16 a[72:75], a[232:235], a[208:211], a[72:75]\n" \
"v_accvgpr_read_b32 v180, a28\n" \
"v_accvgpr_read_b32 v181, a29\n" \
"v_mfma_f32_16x16x32_bf16 a[76:79], a[172:175], a[144:147], a[76:79]\n" \
"s_addk_i32 s84, 0x2000\n" \
"v_mfma_f32_16x16x32_bf16 a[76:79], a[236:239], a[208:211], a[76:79]\n" \
"buffer_load_dwordx4 v24, s[76:79], s84 offen offset:2048 lds\n" \
"v_mfma_f32_16x16x32_bf16 a[92:95], a[172:175], a[148:151], a[92:95]\n" \
"s_add_u32 s89, s41, 0x0\n" \
"buffer_store_dwordx4 v[184:187], v20, s[80:83], s89 offen offset:0 sc1\n" \
"v_mfma_f32_16x16x32_bf16 a[92:95], a[236:239], a[212:215], a[92:95]\n" \
"ds_read_b64_tr_b16 a[188:189], v39 offset:32768\n" \
"v_accvgpr_read_b32 v182, a30\n" \
"v_mfma_f32_16x16x32_bf16 a[88:91], a[168:171], a[148:151], a[88:91]\n" \
"ds_read_b64_tr_b16 a[190:191], v39 offset:33792\n" \
"v_accvgpr_read_b32 v183, a31\n" \
"v_mfma_f32_16x16x32_bf16 a[88:91], a[232:235], a[212:215], a[88:91]\n" \
"s_addk_i32 s84, 0x2000\n" \
"v_mfma_f32_16x16x32_bf16 a[84:87], a[164:167], a[148:151], a[84:87]\n" \
"buffer_load_dwordx4 v25, s[76:79], s84 offen offset:3072 lds\n" \
"v_mfma_f32_16x16x32_bf16 a[84:87], a[228:231], a[212:215], a[84:87]\n" \
"ds_read_b64_tr_b16 a[252:253], v39 offset:40960\n" \
"v_cvt_pk_bf16_f32 v192, v192, v193\n" \
"v_mfma_f32_16x16x32_bf16 a[80:83], a[160:163], a[148:151], a[80:83]\n" \
"s_add_u32 s88, s41, 0x0\n" \
"buffer_store_dwordx4 v[188:191], v20, s[80:83], s88 offen offset:1024 sc1\n" \
"v_mfma_f32_16x16x32_bf16 a[80:83], a[224:227], a[212:215], a[80:83]\n" \
"v_cvt_pk_bf16_f32 v193, v194, v195\n" \
"v_cvt_pk_bf16_f32 v194, v180, v181\n" \
"s_waitcnt lgkmcnt(15)\n" \
"s_nop 0\n" \
"v_mfma_f32_16x16x32_bf16 a[96:99], a[160:163], a[152:155], a[96:99]\n" \
"ds_read_b64_tr_b16 a[254:255], v39 offset:41984\n" \
"v_cvt_pk_bf16_f32 v195, v182, v183\n" \
"v_mfma_f32_16x16x32_bf16 a[96:99], a[224:227], a[216:219], a[96:99]\n" \
"v_accvgpr_read_b32 v196, a16\n" \
"v_accvgpr_read_b32 v197, a17\n" \
"v_mfma_f32_16x16x32_bf16 a[100:103], a[164:167], a[152:155], a[100:103]\n" \
"v_accvgpr_read_b32 v198, a18\n" \
"v_accvgpr_read_b32 v199, a19\n" \
"v_mfma_f32_16x16x32_bf16 a[100:103], a[228:231], a[216:219], a[100:103]\n" \
"v_accvgpr_read_b32 v180, a20\n" \
"v_accvgpr_read_b32 v181, a21\n" \
"v_mfma_f32_16x16x32_bf16 a[104:107], a[168:171], a[152:155], a[104:107]\n" \
"s_add_u32 s89, s41, 0x1000\n" \
"buffer_store_dwordx4 v[192:195], v20, s[80:83], s89 offen offset:1024 sc1\n" \
"v_mfma_f32_16x16x32_bf16 a[104:107], a[232:235], a[216:219], a[104:107]\n" \
"v_accvgpr_read_b32 v182, a22\n" \
"v_accvgpr_read_b32 v183, a23\n" \
"v_mfma_f32_16x16x32_bf16 a[108:111], a[172:175], a[152:155], a[108:111]\n" \
"v_cvt_pk_bf16_f32 v196, v196, v197\n" \
"v_cvt_pk_bf16_f32 v197, v198, v199\n" \
"v_mfma_f32_16x16x32_bf16 a[108:111], a[236:239], a[216:219], a[108:111]\n" \
"v_cvt_pk_bf16_f32 v198, v180, v181\n" \
"v_cvt_pk_bf16_f32 v199, v182, v183\n" \
"v_mfma_f32_16x16x32_bf16 a[124:127], a[172:175], a[156:159], a[124:127]\n" \
"v_accvgpr_read_b32 v200, a32\n" \
"v_accvgpr_read_b32 v201, a33\n" \
"v_mfma_f32_16x16x32_bf16 a[124:127], a[236:239], a[220:223], a[124:127]\n" \
"v_accvgpr_read_b32 v202, a34\n" \
"v_accvgpr_read_b32 v203, a35\n" \
"v_mfma_f32_16x16x32_bf16 a[120:123], a[168:171], a[156:159], a[120:123]\n" \
"s_add_u32 s88, s41, 0x1000\n" \
"buffer_store_dwordx4 v[196:199], v20, s[80:83], s88 offen offset:0 sc1\n" \
"v_mfma_f32_16x16x32_bf16 a[120:123], a[232:235], a[220:223], a[120:123]\n" \
"v_accvgpr_read_b32 v180, a36\n" \
"v_accvgpr_read_b32 v181, a37\n" \
"v_mfma_f32_16x16x32_bf16 a[116:119], a[164:167], a[156:159], a[116:119]\n" \
"v_accvgpr_read_b32 v182, a38\n" \
"v_accvgpr_read_b32 v183, a39\n" \
"v_mfma_f32_16x16x32_bf16 a[116:119], a[228:231], a[220:223], a[116:119]\n" \
"v_cvt_pk_bf16_f32 v200, v200, v201\n" \
"v_cvt_pk_bf16_f32 v201, v202, v203\n" \
"v_mfma_f32_16x16x32_bf16 a[112:115], a[160:163], a[156:159], a[112:115]\n" \
"v_cvt_pk_bf16_f32 v202, v180, v181\n" \
"v_cvt_pk_bf16_f32 v203, v182, v183\n" \
"v_mfma_f32_16x16x32_bf16 a[112:115], a[224:227], a[220:223], a[112:115]\n" \
"v_accvgpr_read_b32 v204, a40\n" \
"v_accvgpr_read_b32 v205, a41\n" \
"s_waitcnt vmcnt(12) lgkmcnt(0)\n" \
"s_barrier\n" \
"v_mov_b32_e32 v36, v32\n" \
"v_xor_b32_e32 v32, 0x0, v48\n" \
"v_mov_b32_e32 v37, v33\n" \
"v_xor_b32_e32 v33, 0x20, v48\n" \
"v_mov_b32_e32 v38, v34\n" \
"v_xor_b32_e32 v34, 0x40, v48\n" \
"v_mov_b32_e64 v39, v35\n" \
"v_xor_b32_e32 v35, 0x60, v48\n" \
"v_add_u32_e32 v22, s44, v28\n" \
"v_add_u32_e32 v28, s43, v28\n" \
"v_add_u32_e32 v23, s44, v29\n" \
"v_add_u32_e32 v29, s43, v29\n" \
"v_mfma_f32_16x16x32_bf16 v[52:55], a[176:179], a[128:131], v[52:55]\n" \
"s_add_u32 m0, s40, 0x8000\n" \
"s_add_u32 s85, s39, 0x2000\n" \
"v_mfma_f32_16x16x32_bf16 v[52:55], a[240:243], a[192:195], v[52:55]\n" \
"buffer_load_dwordx4 v20, s[80:83], s85 offen sc1 lds\n" \
"v_mfma_f32_16x16x32_bf16 v[56:59], a[180:183], a[128:131], v[56:59]\n" \
"ds_read_b64_tr_b16 a[160:161], v32 offset:0\n" \
"v_accvgpr_read_b32 v206, a42\n" \
"v_mfma_f32_16x16x32_bf16 v[56:59], a[244:247], a[192:195], v[56:59]\n" \
"s_add_u32 m0, s40, 0x9000\n" \
"s_add_u32 s85, s39, 0x3000\n" \
"v_mfma_f32_16x16x32_bf16 v[60:63], a[184:187], a[128:131], v[60:63]\n" \
"buffer_load_dwordx4 v20, s[80:83], s85 offen sc1 lds\n" \
"v_mfma_f32_16x16x32_bf16 v[60:63], a[248:251], a[192:195], v[60:63]\n" \
"ds_read_b64_tr_b16 a[162:163], v32 offset:512\n" \
"v_accvgpr_read_b32 v207, a43\n" \
"v_mfma_f32_16x16x32_bf16 v[64:67], a[188:191], a[128:131], v[64:67]\n" \
"ds_read_b64_tr_b16 a[224:225], v32 offset:4096\n" \
"v_accvgpr_read_b32 v180, a44\n" \
"v_mfma_f32_16x16x32_bf16 v[64:67], a[252:255], a[192:195], v[64:67]\n" \
"ds_read_b128 a[128:131], v28 offset:0\n" \
"v_accvgpr_read_b32 v181, a45\n" \
"v_mfma_f32_16x16x32_bf16 v[80:83], a[188:191], a[132:135], v[80:83]\n" \
"ds_read_b128 a[192:195], v29 offset:0\n" \
"v_accvgpr_read_b32 v182, a46\n" \
"v_mfma_f32_16x16x32_bf16 v[80:83], a[252:255], a[196:199], v[80:83]\n" \
"s_add_u32 m0, s40, 0xb000\n" \
"s_movk_i32 s84, 0x80\n" \
"v_mfma_f32_16x16x32_bf16 v[76:79], a[184:187], a[132:135], v[76:79]\n" \
"buffer_load_dwordx4 v21, s[76:79], s84 offen offset:0 lds\n" \
"v_mfma_f32_16x16x32_bf16 v[76:79], a[248:251], a[196:199], v[76:79]\n" \
"s_add_u32 m0, s30, 0x8000\n" \
"s_movk_i32 s84, 0x80\n" \
"v_mfma_f32_16x16x32_bf16 v[72:75], a[180:183], a[132:135], v[72:75]\n" \
"buffer_load_dwordx4 v24, s[76:79], s84 offen offset:0 lds\n" \
"v_mfma_f32_16x16x32_bf16 v[72:75], a[244:247], a[196:199], v[72:75]\n" \
"ds_read_b64_tr_b16 a[226:227], v32 offset:4608\n" \
"v_accvgpr_read_b32 v183, a47\n" \
"v_mfma_f32_16x16x32_bf16 v[68:71], a[176:179], a[132:135], v[68:71]\n" \
"ds_read_b64_tr_b16 a[164:165], v33 offset:0\n" \
"v_cvt_pk_bf16_f32 v204, v204, v205\n" \
"v_mfma_f32_16x16x32_bf16 v[68:71], a[240:243], a[196:199], v[68:71]\n" \
"ds_read_b128 a[132:135], v28 offset:2048\n" \
"v_cvt_pk_bf16_f32 v205, v206, v207\n" \
"v_mfma_f32_16x16x32_bf16 v[84:87], a[176:179], a[136:139], v[84:87]\n" \
"ds_read_b128 a[196:199], v29 offset:2048\n" \
"v_cvt_pk_bf16_f32 v206, v180, v181\n" \
"v_mfma_f32_16x16x32_bf16 v[84:87], a[240:243], a[200:203], v[84:87]\n" \
"s_movk_i32 s84, 0x2080\n" \
"v_mfma_f32_16x16x32_bf16 v[88:91], a[180:183], a[136:139], v[88:91]\n" \
"buffer_load_dwordx4 v25, s[76:79], s84 offen offset:1024 lds\n" \
"v_mfma_f32_16x16x32_bf16 v[88:91], a[244:247], a[200:203], v[88:91]\n" \
"s_movk_i32 s84, 0x4080\n" \
"v_mfma_f32_16x16x32_bf16 v[92:95], a[184:187], a[136:139], v[92:95]\n" \
"buffer_load_dwordx4 v24, s[76:79], s84 offen offset:2048 lds\n" \
"v_mfma_f32_16x16x32_bf16 v[92:95], a[248:251], a[200:203], v[92:95]\n" \
"ds_read_b64_tr_b16 a[166:167], v33 offset:512\n" \
"v_cvt_pk_bf16_f32 v207, v182, v183\n" \
"v_mfma_f32_16x16x32_bf16 v[96:99], a[188:191], a[136:139], v[96:99]\n" \
"ds_read_b64_tr_b16 a[228:229], v33 offset:4096\n" \
"v_accvgpr_read_b32 v208, a56\n" \
"v_mfma_f32_16x16x32_bf16 v[96:99], a[252:255], a[200:203], v[96:99]\n" \
"ds_read_b128 a[136:139], v28 offset:4096\n" \
"v_accvgpr_read_b32 v209, a57\n" \
"v_mfma_f32_16x16x32_bf16 v[112:115], a[188:191], a[140:143], v[112:115]\n" \
"ds_read_b128 a[200:203], v29 offset:4096\n" \
"v_accvgpr_read_b32 v210, a58\n" \
"v_mfma_f32_16x16x32_bf16 v[112:115], a[252:255], a[204:207], v[112:115]\n" \
"s_movk_i32 s84, 0x6080\n" \
"v_mfma_f32_16x16x32_bf16 v[108:111], a[184:187], a[140:143], v[108:111]\n" \
"buffer_load_dwordx4 v25, s[76:79], s84 offen offset:3072 lds\n" \
"v_mfma_f32_16x16x32_bf16 v[108:111], a[248:251], a[204:207], v[108:111]\n" \
"s_add_u32 m0, s31, 0x8000\n" \
"s_mov_b32 s84, 0x80000\n" \
"v_mfma_f32_16x16x32_bf16 v[104:107], a[180:183], a[140:143], v[104:107]\n" \
"buffer_load_dwordx4 v26, s[68:71], s84 offen offset:0 lds\n" \
"v_mfma_f32_16x16x32_bf16 v[104:107], a[244:247], a[204:207], v[104:107]\n" \
"s_addk_i32 s84, 0x7c00\n" \
"v_mfma_f32_16x16x32_bf16 v[100:103], a[176:179], a[140:143], v[100:103]\n" \
"buffer_load_dwordx4 v26, s[68:71], s84 offen offset:1024 lds\n" \
"v_mfma_f32_16x16x32_bf16 v[100:103], a[240:243], a[204:207], v[100:103]\n" \
"ds_read_b128 a[140:143], v28 offset:6144\n" \
"v_accvgpr_read_b32 v211, a59\n" \
"v_mfma_f32_16x16x32_bf16 v[116:119], a[176:179], a[144:147], v[116:119]\n" \
"ds_read_b128 a[204:207], v29 offset:6144\n" \
"v_accvgpr_read_b32 v180, a60\n" \
"v_mfma_f32_16x16x32_bf16 v[116:119], a[240:243], a[208:211], v[116:119]\n" \
"s_addk_i32 s84, 0x7c00\n" \
"v_mfma_f32_16x16x32_bf16 v[120:123], a[180:183], a[144:147], v[120:123]\n" \
"buffer_load_dwordx4 v27, s[68:71], s84 offen offset:2048 lds\n" \
"v_mfma_f32_16x16x32_bf16 v[120:123], a[244:247], a[208:211], v[120:123]\n" \
"ds_read_b64_tr_b16 a[230:231], v33 offset:4608\n" \
"v_accvgpr_read_b32 v181, a61\n" \
"v_mfma_f32_16x16x32_bf16 v[124:127], a[184:187], a[144:147], v[124:127]\n" \
"s_addk_i32 s84, 0x7c00\n" \
"v_mfma_f32_16x16x32_bf16 v[124:127], a[248:251], a[208:211], v[124:127]\n" \
"buffer_load_dwordx4 v27, s[68:71], s84 offen offset:3072 lds\n" \
"v_mfma_f32_16x16x32_bf16 v[128:131], a[188:191], a[144:147], v[128:131]\n" \
"ds_read_b64_tr_b16 a[172:173], v35 offset:0\n" \
"v_accvgpr_read_b32 v182, a62\n" \
"v_mfma_f32_16x16x32_bf16 v[128:131], a[252:255], a[208:211], v[128:131]\n" \
"ds_read_b128 a[144:147], v28 offset:8192\n" \
"v_accvgpr_read_b32 v183, a63\n" \
"v_mfma_f32_16x16x32_bf16 v[144:147], a[188:191], a[148:151], v[144:147]\n" \
"ds_read_b128 a[208:211], v29 offset:8192\n" \
"v_cvt_pk_bf16_f32 v208, v208, v209\n" \
"v_mfma_f32_16x16x32_bf16 v[144:147], a[252:255], a[212:215], v[144:147]\n" \
"s_add_u32 m0, s30, 0x9000\n" \
"s_mov_b32 s84, 0x9080\n" \
"v_mfma_f32_16x16x32_bf16 v[140:143], a[184:187], a[148:151], v[140:143]\n" \
"buffer_load_dwordx4 v24, s[76:79], s84 offen offset:0 lds\n" \
"v_mfma_f32_16x16x32_bf16 v[140:143], a[248:251], a[212:215], v[140:143]\n" \
"ds_read_b64_tr_b16 a[174:175], v35 offset:512\n" \
"v_cvt_pk_bf16_f32 v209, v210, v211\n" \
"v_mfma_f32_16x16x32_bf16 v[136:139], a[180:183], a[148:151], v[136:139]\n" \
"s_addk_i32 s84, 0x2000\n" \
"s_nop 0\n" \
"v_mfma_f32_16x16x32_bf16 v[136:139], a[244:247], a[212:215], v[136:139]\n" \
"buffer_load_dwordx4 v25, s[76:79], s84 offen offset:1024 lds\n" \
"v_mfma_f32_16x16x32_bf16 v[132:135], a[176:179], a[148:151], v[132:135]\n" \
"ds_read_b64_tr_b16 a[236:237], v35 offset:4096\n" \
"v_cvt_pk_bf16_f32 v210, v180, v181\n" \
"v_mfma_f32_16x16x32_bf16 v[132:135], a[240:243], a[212:215], v[132:135]\n" \
"ds_read_b128 a[148:151], v28 offset:10240\n" \
"v_cvt_pk_bf16_f32 v211, v182, v183\n" \
"v_mfma_f32_16x16x32_bf16 v[148:151], a[176:179], a[152:155], v[148:151]\n" \
"ds_read_b128 a[212:215], v29 offset:10240\n" \
"v_accvgpr_read_b32 v212, a48\n" \
"v_mfma_f32_16x16x32_bf16 v[148:151], a[240:243], a[216:219], v[148:151]\n" \
"ds_read_b64_tr_b16 a[238:239], v35 offset:4608\n" \
"v_accvgpr_read_b32 v213, a49\n" \
"v_mfma_f32_16x16x32_bf16 v[152:155], a[180:183], a[152:155], v[152:155]\n" \
"ds_read_b64_tr_b16 a[168:169], v34 offset:0\n" \
"v_accvgpr_read_b32 v214, a50\n" \
"v_mfma_f32_16x16x32_bf16 v[152:155], a[244:247], a[216:219], v[152:155]\n" \
"ds_read_b64_tr_b16 a[170:171], v34 offset:512\n" \
"v_accvgpr_read_b32 v215, a51\n" \
"v_mfma_f32_16x16x32_bf16 v[156:159], a[184:187], a[152:155], v[156:159]\n" \
"ds_read_b64_tr_b16 a[232:233], v34 offset:4096\n" \
"v_accvgpr_read_b32 v180, a52\n" \
"v_mfma_f32_16x16x32_bf16 v[156:159], a[248:251], a[216:219], v[156:159]\n" \
"ds_read_b64_tr_b16 a[234:235], v34 offset:4608\n" \
"v_accvgpr_read_b32 v181, a53\n" \
"v_mfma_f32_16x16x32_bf16 v[160:163], a[188:191], a[152:155], v[160:163]\n" \
"ds_read_b128 v[248:251], v22 offset:0\n" \
"v_accvgpr_read_b32 v182, a54\n" \
"v_mfma_f32_16x16x32_bf16 v[160:163], a[252:255], a[216:219], v[160:163]\n" \
"ds_read_b128 a[152:155], v28 offset:12288\n" \
"v_accvgpr_read_b32 v183, a55\n" \
"v_mfma_f32_16x16x32_bf16 v[176:179], a[188:191], a[156:159], v[176:179]\n" \
"ds_read_b128 a[216:219], v29 offset:12288\n" \
"v_cvt_pk_bf16_f32 v212, v212, v213\n" \
"v_mfma_f32_16x16x32_bf16 v[176:179], a[252:255], a[220:223], v[176:179]\n" \
"ds_read_b128 v[48:51], v23 offset:0\n" \
"v_cvt_pk_bf16_f32 v213, v214, v215\n" \
"v_mfma_f32_16x16x32_bf16 v[172:175], a[184:187], a[156:159], v[172:175]\n" \
"v_cvt_pk_bf16_f32 v214, v180, v181\n" \
"v_cvt_pk_bf16_f32 v215, v182, v183\n" \
"v_mfma_f32_16x16x32_bf16 v[172:175], a[248:251], a[220:223], v[172:175]\n" \
"v_accvgpr_read_b32 v216, a64\n" \
"v_accvgpr_read_b32 v217, a65\n" \
"v_mfma_f32_16x16x32_bf16 v[168:171], a[180:183], a[156:159], v[168:171]\n" \
"v_accvgpr_read_b32 v218, a66\n" \
"v_accvgpr_read_b32 v219, a67\n" \
"v_mfma_f32_16x16x32_bf16 v[168:171], a[244:247], a[220:223], v[168:171]\n" \
"v_accvgpr_read_b32 v180, a68\n" \
"v_accvgpr_read_b32 v181, a69\n" \
"v_mfma_f32_16x16x32_bf16 v[164:167], a[176:179], a[156:159], v[164:167]\n" \
"v_accvgpr_read_b32 v182, a70\n" \
"v_accvgpr_read_b32 v183, a71\n" \
"v_mfma_f32_16x16x32_bf16 v[164:167], a[240:243], a[220:223], v[164:167]\n" \
"ds_read_b128 a[156:159], v28 offset:14336\n" \
"ds_read_b128 a[220:223], v29 offset:14336\n" \
"s_waitcnt lgkmcnt(15)\n" \
"v_mfma_f32_16x16x32_bf16 a[0:3], a[160:163], a[128:131], 0\n" \
"s_addk_i32 s84, 0x2000\n" \
"v_cvt_pk_bf16_f32 v216, v216, v217\n" \
"v_mfma_f32_16x16x32_bf16 a[0:3], a[224:227], a[192:195], a[0:3]\n" \
"buffer_load_dwordx4 v24, s[76:79], s84 offen offset:2048 lds\n" \
"v_cvt_pk_bf16_f32 v217, v218, v219\n" \
"v_mfma_f32_16x16x32_bf16 a[4:7], a[164:167], a[128:131], 0\n" \
"ds_read_b64_tr_b16 a[176:177], v36 offset:0\n" \
"v_cvt_pk_bf16_f32 v218, v180, v181\n" \
"v_mfma_f32_16x16x32_bf16 a[4:7], a[228:231], a[192:195], a[4:7]\n" \
"s_addk_i32 s84, 0x2000\n" \
"v_cvt_pk_bf16_f32 v219, v182, v183\n" \
"s_waitcnt lgkmcnt(15)\n" \
"v_mfma_f32_16x16x32_bf16 a[12:15], a[172:175], a[128:131], 0\n" \
"buffer_load_dwordx4 v25, s[76:79], s84 offen offset:3072 lds\n" \
"v_accvgpr_read_b32 v220, a80\n" \
"s_waitcnt lgkmcnt(11)\n" \
"v_mfma_f32_16x16x32_bf16 a[12:15], a[236:239], a[192:195], a[12:15]\n" \
"ds_read_b64_tr_b16 a[178:179], v36 offset:1024\n" \
"v_accvgpr_read_b32 v221, a81\n" \
"s_waitcnt lgkmcnt(10)\n" \
"v_mfma_f32_16x16x32_bf16 a[8:11], a[128:131], a[168:171], 0\n" \
"ds_read_b64_tr_b16 a[240:241], v36 offset:8192\n" \
"v_accvgpr_read_b32 v222, a82\n" \
"s_waitcnt lgkmcnt(9)\n" \
"s_nop 0\n" \
"v_mfma_f32_16x16x32_bf16 a[8:11], a[192:195], a[232:235], a[8:11]\n" \
"ds_read_b64_tr_b16 a[242:243], v36 offset:9216\n" \
"v_accvgpr_read_b32 v223, a83\n" \
"v_mfma_f32_16x16x32_bf16 a[24:27], a[132:135], a[168:171], 0\n" \
"ds_read_b64_tr_b16 a[180:181], v37 offset:0\n" \
"v_accvgpr_read_b32 v180, a84\n" \
"v_mfma_f32_16x16x32_bf16 a[24:27], a[196:199], a[232:235], a[24:27]\n" \
"ds_read_b64_tr_b16 a[182:183], v37 offset:1024\n" \
"v_accvgpr_read_b32 v181, a85\n" \
"v_mfma_f32_16x16x32_bf16 a[28:31], a[172:175], a[132:135], 0\n" \
"ds_read_b64_tr_b16 a[244:245], v37 offset:8192\n" \
"v_accvgpr_read_b32 v182, a86\n" \
"v_mfma_f32_16x16x32_bf16 a[28:31], a[236:239], a[196:199], a[28:31]\n" \
"s_add_u32 s89, s41, 0x2000\n" \
"buffer_store_dwordx4 v[200:203], v20, s[80:83], s89 offen offset:0 sc1\n" \
"v_mfma_f32_16x16x32_bf16 a[20:23], a[164:167], a[132:135], 0\n" \
"ds_read_b64_tr_b16 a[246:247], v37 offset:9216\n" \
"v_accvgpr_read_b32 v183, a87\n" \
"v_mfma_f32_16x16x32_bf16 a[20:23], a[228:231], a[196:199], a[20:23]\n" \
"ds_read_b64_tr_b16 a[184:185], v38 offset:0\n" \
"v_cvt_pk_bf16_f32 v220, v220, v221\n" \
"v_mfma_f32_16x16x32_bf16 a[16:19], a[160:163], a[132:135], 0\n" \
"ds_read_b64_tr_b16 a[186:187], v38 offset:1024\n" \
"v_cvt_pk_bf16_f32 v221, v222, v223\n" \
"v_mfma_f32_16x16x32_bf16 a[16:19], a[224:227], a[196:199], a[16:19]\n" \
"v_cvt_pk_bf16_f32 v222, v180, v181\n" \
"v_cvt_pk_bf16_f32 v223, v182, v183\n" \
"v_cvt_pk_bf16_f32 v224, v52, v53\n" \
"v_mfma_f32_16x16x32_bf16 a[32:35], a[160:163], a[136:139], 0\n" \
"s_add_u32 s88, s41, 0x2000\n" \
"buffer_store_dwordx4 v[204:207], v20, s[80:83], s88 offen offset:1024 sc1\n" \
".fill (L_pb + 20192 - .) / 4, 4, 0xBF800000\n" \
"L_pp5:\n" \
"s_cbranch_execz L_pret\n" \
"s_nop 0\n" \
"v_mfma_f32_16x16x32_bf16 a[32:35], a[224:227], a[200:203], a[32:35]\n" \
"ds_read_b64_tr_b16 a[248:249], v38 offset:8192\n" \
"v_cvt_pk_bf16_f32 v225, v54, v55\n" \
"v_mfma_f32_16x16x32_bf16 a[36:39], a[164:167], a[136:139], 0\n" \
"ds_read_b64_tr_b16 a[250:251], v38 offset:9216\n" \
"v_cvt_pk_bf16_f32 v226, v56, v57\n" \
"v_mfma_f32_16x16x32_bf16 a[36:39], a[228:231], a[200:203], a[36:39]\n" \
"ds_read_b64_tr_b16 a[188:189], v39 offset:0\n" \
"v_cvt_pk_bf16_f32 v227, v58, v59\n" \
"v_mfma_f32_16x16x32_bf16 a[44:47], a[172:175], a[136:139], 0\n" \
"v_cvt_pk_bf16_f32 v228, v60, v61\n" \
"v_cvt_pk_bf16_f32 v229, v62, v63\n" \
"v_cvt_pk_bf16_f32 v230, v64, v65\n" \
"v_mfma_f32_16x16x32_bf16 a[44:47], a[236:239], a[200:203], a[44:47]\n" \
"ds_read_b64_tr_b16 a[190:191], v39 offset:1024\n" \
"v_cvt_pk_bf16_f32 v231, v66, v67\n" \
"v_mfma_f32_16x16x32_bf16 a[40:43], a[136:139], a[168:171], 0\n" \
"s_add_u32 s89, s41, 0x3000\n" \
"buffer_store_dwordx4 v[208:211], v20, s[80:83], s89 offen offset:1024 sc1\n" \
"v_mfma_f32_16x16x32_bf16 a[40:43], a[200:203], a[232:235], a[40:43]\n" \
"ds_read_b64_tr_b16 a[252:253], v39 offset:8192\n" \
"v_cvt_pk_bf16_f32 v232, v76, v77\n" \
"v_mfma_f32_16x16x32_bf16 a[56:59], a[140:143], a[168:171], 0\n" \
"ds_read_b64_tr_b16 a[254:255], v39 offset:9216\n" \
"v_cvt_pk_bf16_f32 v233, v78, v79\n" \
"v_mfma_f32_16x16x32_bf16 a[56:59], a[204:207], a[232:235], a[56:59]\n" \
"v_cvt_pk_bf16_f32 v234, v80, v81\n" \
"v_cvt_pk_bf16_f32 v235, v82, v83\n" \
"v_cvt_pk_bf16_f32 v236, v68, v69\n" \
"v_mfma_f32_16x16x32_bf16 a[60:63], a[172:175], a[140:143], 0\n" \
"v_cvt_pk_bf16_f32 v237, v70, v71\n" \
"v_cvt_pk_bf16_f32 v238, v72, v73\n" \
"v_cvt_pk_bf16_f32 v239, v74, v75\n" \
"v_mfma_f32_16x16x32_bf16 a[60:63], a[236:239], a[204:207], a[60:63]\n" \
"s_add_u32 s88, s41, 0x3000\n" \
"buffer_store_dwordx4 v[212:215], v20, s[80:83], s88 offen offset:0 sc1\n" \
"v_mfma_f32_16x16x32_bf16 a[52:55], a[164:167], a[140:143], 0\n" \
"v_cvt_pk_bf16_f32 v240, v84, v85\n" \
"v_cvt_pk_bf16_f32 v241, v86, v87\n" \
"v_cvt_pk_bf16_f32 v242, v88, v89\n" \
"v_mfma_f32_16x16x32_bf16 a[52:55], a[228:231], a[204:207], a[52:55]\n" \
"v_cvt_pk_bf16_f32 v243, v90, v91\n" \
"v_cvt_pk_bf16_f32 v244, v92, v93\n" \
"v_cvt_pk_bf16_f32 v245, v94, v95\n" \
"v_mfma_f32_16x16x32_bf16 a[48:51], a[160:163], a[140:143], 0\n" \
"v_cvt_pk_bf16_f32 v246, v96, v97\n" \
"v_cvt_pk_bf16_f32 v247, v98, v99\n" \
"v_cvt_pk_bf16_f32 v184, v108, v109\n" \
"v_mfma_f32_16x16x32_bf16 a[48:51], a[224:227], a[204:207], a[48:51]\n" \
"v_cvt_pk_bf16_f32 v185, v110, v111\n" \
"v_cvt_pk_bf16_f32 v186, v112, v113\n" \
"v_cvt_pk_bf16_f32 v187, v114, v115\n" \
"s_waitcnt lgkmcnt(15)\n" \
"s_nop 0\n" \
"v_mfma_f32_16x16x32_bf16 a[64:67], a[164:167], v[248:251], 0\n" \
"v_cvt_pk_bf16_f32 v188, v100, v101\n" \
"v_cvt_pk_bf16_f32 v189, v102, v103\n" \
"v_cvt_pk_bf16_f32 v190, v104, v105\n" \
"v_mfma_f32_16x16x32_bf16 a[64:67], a[228:231], v[48:51], a[64:67]\n" \
"v_cvt_pk_bf16_f32 v191, v106, v107\n" \
"v_cvt_pk_bf16_f32 v192, v116, v117\n" \
"v_cvt_pk_bf16_f32 v193, v118, v119\n" \
"v_mfma_f32_16x16x32_bf16 a[80:83], a[172:175], v[248:251], 0\n" \
"v_cvt_pk_bf16_f32 v194, v120, v121\n" \
"v_cvt_pk_bf16_f32 v195, v122, v123\n" \
"v_cvt_pk_bf16_f32 v196, v124, v125\n" \
"v_mfma_f32_16x16x32_bf16 a[80:83], a[236:239], v[48:51], a[80:83]\n" \
"v_cvt_pk_bf16_f32 v197, v126, v127\n" \
"v_cvt_pk_bf16_f32 v198, v128, v129\n" \
"v_cvt_pk_bf16_f32 v199, v130, v131\n" \
"v_mfma_f32_16x16x32_bf16 a[68:71], v[248:251], a[168:171], 0\n" \
"v_cvt_pk_bf16_f32 v252, v140, v141\n" \
"v_cvt_pk_bf16_f32 v253, v142, v143\n" \
"v_cvt_pk_bf16_f32 v254, v144, v145\n" \
"v_mfma_f32_16x16x32_bf16 a[68:71], v[48:51], a[232:235], a[68:71]\n" \
"v_cvt_pk_bf16_f32 v255, v146, v147\n" \
"v_accvgpr_write_b32 a84, v252\n" \
"v_accvgpr_write_b32 a85, v253\n" \
"s_waitcnt vmcnt(4) lgkmcnt(0)\n" \
"s_barrier\n" \
"v_mfma_f32_16x16x32_bf16 v[52:55], a[176:179], a[128:131], 0\n" \
"s_add_u32 m0, s40, 0x0\n" \
"s_nop 0\n" \
"s_add_u32 s85, s39, 0x4000\n" \
"v_accvgpr_write_b32 a86, v254\n" \
"v_mfma_f32_16x16x32_bf16 v[52:55], a[240:243], a[192:195], v[52:55]\n" \
"buffer_load_dwordx4 v20, s[80:83], s85 offen sc1 lds\n" \
"v_accvgpr_write_b32 a87, v255\n" \
"v_mfma_f32_16x16x32_bf16 v[56:59], a[180:183], a[128:131], 0\n" \
"ds_read_b64_tr_b16 a[160:161], v32 offset:32768\n" \
"v_cvt_pk_bf16_f32 v252, v132, v133\n" \
"v_mfma_f32_16x16x32_bf16 v[56:59], a[244:247], a[192:195], v[56:59]\n" \
"ds_read_b64_tr_b16 a[162:163], v32 offset:33280\n" \
"v_cvt_pk_bf16_f32 v253, v134, v135\n" \
"v_mfma_f32_16x16x32_bf16 v[60:63], a[184:187], a[128:131], 0\n" \
"s_add_u32 m0, s40, 0x1000\n" \
"s_add_u32 s85, s39, 0x5000\n" \
"v_cvt_pk_bf16_f32 v254, v136, v137\n" \
"v_mfma_f32_16x16x32_bf16 v[60:63], a[248:251], a[192:195], v[60:63]\n" \
"buffer_load_dwordx4 v20, s[80:83], s85 offen sc1 lds\n" \
"v_cvt_pk_bf16_f32 v255, v138, v139\n" \
"v_mfma_f32_16x16x32_bf16 v[64:67], a[188:191], a[128:131], 0\n" \
"ds_read_b64_tr_b16 a[224:225], v32 offset:36864\n" \
"v_cvt_pk_bf16_f32 v200, v148, v149\n" \
"v_mfma_f32_16x16x32_bf16 v[64:67], a[252:255], a[192:195], v[64:67]\n" \
"ds_read_b128 a[128:131], v28 offset:32768\n" \
"v_cvt_pk_bf16_f32 v201, v150, v151\n" \
"v_mfma_f32_16x16x32_bf16 v[80:83], a[188:191], a[132:135], 0\n" \
"ds_read_b128 a[192:195], v29 offset:32768\n" \
"v_cvt_pk_bf16_f32 v202, v152, v153\n" \
"v_mfma_f32_16x16x32_bf16 v[80:83], a[252:255], a[196:199], v[80:83]\n" \
"s_add_u32 m0, s40, 0x3000\n" \
"s_movk_i32 s84, 0x100\n" \
"s_nop 0\n" \
"v_cvt_pk_bf16_f32 v203, v154, v155\n" \
"v_mfma_f32_16x16x32_bf16 v[76:79], a[184:187], a[132:135], 0\n" \
"buffer_load_dwordx4 v21, s[76:79], s84 offen offset:0 lds\n" \
"v_cvt_pk_bf16_f32 v204, v156, v157\n" \
"v_mfma_f32_16x16x32_bf16 v[76:79], a[248:251], a[196:199], v[76:79]\n" \
"ds_read_b64_tr_b16 a[226:227], v32 offset:37376\n" \
"v_cvt_pk_bf16_f32 v205, v158, v159\n" \
"v_mfma_f32_16x16x32_bf16 v[72:75], a[180:183], a[132:135], 0\n" \
"ds_read_b64_tr_b16 a[164:165], v33 offset:32768\n" \
"v_cvt_pk_bf16_f32 v206, v160, v161\n" \
"v_mfma_f32_16x16x32_bf16 v[72:75], a[244:247], a[196:199], v[72:75]\n" \
"v_cvt_pk_bf16_f32 v207, v162, v163\n" \
"v_cvt_pk_bf16_f32 v208, v172, v173\n" \
"v_cvt_pk_bf16_f32 v209, v174, v175\n" \
"v_mfma_f32_16x16x32_bf16 v[68:71], a[176:179], a[132:135], 0\n" \
"ds_read_b64_tr_b16 a[166:167], v33 offset:33280\n" \
"v_cvt_pk_bf16_f32 v210, v176, v177\n" \
"v_mfma_f32_16x16x32_bf16 v[68:71], a[240:243], a[196:199], v[68:71]\n" \
"ds_read_b128 a[132:135], v28 offset:34816\n" \
"v_cvt_pk_bf16_f32 v211, v178, v179\n" \
"v_mfma_f32_16x16x32_bf16 v[84:87], a[176:179], a[136:139], 0\n" \
"ds_read_b128 a[196:199], v29 offset:34816\n" \
"v_cvt_pk_bf16_f32 v212, v164, v165\n" \
"v_mfma_f32_16x16x32_bf16 v[84:87], a[240:243], a[200:203], v[84:87]\n" \
"s_add_u32 m0, s31, 0x0\n" \
"s_mov_b32 s84, 0x100000\n" \
"v_cvt_pk_bf16_f32 v213, v166, v167\n" \
"v_mfma_f32_16x16x32_bf16 v[88:91], a[180:183], a[136:139], 0\n" \
"buffer_load_dwordx4 v26, s[68:71], s84 offen offset:0 lds\n" \
"v_cvt_pk_bf16_f32 v214, v168, v169\n" \
"v_mfma_f32_16x16x32_bf16 v[88:91], a[244:247], a[200:203], v[88:91]\n" \
"s_add_u32 s89, s41, 0x0\n" \
"buffer_store_dwordx4 v[224:227], v20, s[80:83], s89 offen offset:2048 sc1\n" \
"v_mfma_f32_16x16x32_bf16 v[92:95], a[184:187], a[136:139], 0\n" \
"ds_read_b64_tr_b16 a[228:229], v33 offset:36864\n" \
"v_cvt_pk_bf16_f32 v215, v170, v171\n" \
"v_mfma_f32_16x16x32_bf16 v[92:95], a[248:251], a[200:203], v[92:95]\n" \
"ds_read_b64_tr_b16 a[230:231], v33 offset:37376\n" \
"v_mfma_f32_16x16x32_bf16 v[96:99], a[188:191], a[136:139], 0\n" \
"v_mfma_f32_16x16x32_bf16 v[96:99], a[252:255], a[200:203], v[96:99]\n" \
"ds_read_b128 a[136:139], v28 offset:36864\n" \
"v_mfma_f32_16x16x32_bf16 v[112:115], a[188:191], a[140:143], 0\n" \
"ds_read_b128 a[200:203], v29 offset:36864\n" \
"v_mfma_f32_16x16x32_bf16 v[112:115], a[252:255], a[204:207], v[112:115]\n" \
"s_addk_i32 s84, 0x7c00\n" \
"v_mfma_f32_16x16x32_bf16 v[108:111], a[184:187], a[140:143], 0\n" \
"buffer_load_dwordx4 v26, s[68:71], s84 offen offset:1024 lds\n" \
"v_mfma_f32_16x16x32_bf16 v[108:111], a[248:251], a[204:207], v[108:111]\n" \
"ds_read_b64_tr_b16 a[172:173], v35 offset:32768\n" \
"v_mfma_f32_16x16x32_bf16 v[104:107], a[180:183], a[140:143], 0\n" \
"s_addk_i32 s84, 0x7c00\n" \
"v_mfma_f32_16x16x32_bf16 v[104:107], a[244:247], a[204:207], v[104:107]\n" \
"buffer_load_dwordx4 v27, s[68:71], s84 offen offset:2048 lds\n" \
"v_mfma_f32_16x16x32_bf16 v[100:103], a[176:179], a[140:143], 0\n" \
"ds_read_b64_tr_b16 a[174:175], v35 offset:33280\n" \
"v_mfma_f32_16x16x32_bf16 v[100:103], a[240:243], a[204:207], v[100:103]\n" \
"ds_read_b128 a[140:143], v28 offset:38912\n" \
"v_mfma_f32_16x16x32_bf16 v[116:119], a[176:179], a[144:147], 0\n" \
"ds_read_b128 a[204:207], v29 offset:38912\n" \
"v_mfma_f32_16x16x32_bf16 v[116:119], a[240:243], a[208:211], v[116:119]\n" \
"s_addk_i32 s84, 0x7c00\n" \
"v_mfma_f32_16x16x32_bf16 v[120:123], a[180:183], a[144:147], 0\n" \
"buffer_load_dwordx4 v27, s[68:71], s84 offen offset:3072 lds\n" \
"v_mfma_f32_16x16x32_bf16 v[120:123], a[244:247], a[208:211], v[120:123]\n" \
"ds_read_b64_tr_b16 a[236:237], v35 offset:36864\n" \
"v_mfma_f32_16x16x32_bf16 v[124:127], a[184:187], a[144:147], 0\n" \
"ds_read_b64_tr_b16 a[238:239], v35 offset:37376\n" \
"v_mfma_f32_16x16x32_bf16 v[124:127], a[248:251], a[208:211], v[124:127]\n" \
"s_add_u32 m0, s30, 0x0\n" \
"s_movk_i32 s84, 0x180\n" \
"v_mfma_f32_16x16x32_bf16 v[128:131], a[188:191], a[144:147], 0\n" \
"buffer_load_dwordx4 v24, s[76:79], s84 offen offset:0 lds\n" \
"v_mfma_f32_16x16x32_bf16 v[128:131], a[252:255], a[208:211], v[128:131]\n" \
"ds_read_b128 a[144:147], v28 offset:40960\n" \
"v_mfma_f32_16x16x32_bf16 v[144:147], a[188:191], a[148:151], 0\n" \
"ds_read_b128 a[208:211], v29 offset:40960\n" \
"v_mfma_f32_16x16x32_bf16 v[144:147], a[252:255], a[212:215], v[144:147]\n" \
"ds_read_b64_tr_b16 a[168:169], v34 offset:32768\n" \
"v_mfma_f32_16x16x32_bf16 v[140:143], a[184:187], a[148:151], 0\n" \
"s_movk_i32 s84, 0x2180\n" \
"v_mfma_f32_16x16x32_bf16 v[140:143], a[248:251], a[212:215], v[140:143]\n" \
"buffer_load_dwordx4 v25, s[76:79], s84 offen offset:1024 lds\n" \
"v_mfma_f32_16x16x32_bf16 v[136:139], a[180:183], a[148:151], 0\n" \
"ds_read_b64_tr_b16 a[170:171], v34 offset:33280\n" \
"v_mfma_f32_16x16x32_bf16 v[136:139], a[244:247], a[212:215], v[136:139]\n" \
"ds_read_b64_tr_b16 a[232:233], v34 offset:36864\n" \
"v_mfma_f32_16x16x32_bf16 v[132:135], a[176:179], a[148:151], 0\n" \
"ds_read_b64_tr_b16 a[234:235], v34 offset:37376\n" \
"v_mfma_f32_16x16x32_bf16 v[132:135], a[240:243], a[212:215], v[132:135]\n" \
"ds_read_b128 a[148:151], v28 offset:43008\n" \
"v_mfma_f32_16x16x32_bf16 v[148:151], a[176:179], a[152:155], 0\n" \
"ds_read_b128 a[212:215], v29 offset:43008\n" \
"v_mfma_f32_16x16x32_bf16 v[148:151], a[240:243], a[216:219], v[148:151]\n" \
"s_movk_i32 s84, 0x4180\n" \
"v_mfma_f32_16x16x32_bf16 v[152:155], a[180:183], a[152:155], 0\n" \
"buffer_load_dwordx4 v24, s[76:79], s84 offen offset:2048 lds\n" \
"v_mfma_f32_16x16x32_bf16 v[152:155], a[244:247], a[216:219], v[152:155]\n" \
"s_movk_i32 s84, 0x6180\n" \
"v_mfma_f32_16x16x32_bf16 v[156:159], a[184:187], a[152:155], 0\n" \
"buffer_load_dwordx4 v25, s[76:79], s84 offen offset:3072 lds\n" \
"v_mfma_f32_16x16x32_bf16 v[156:159], a[248:251], a[216:219], v[156:159]\n" \
"ds_read_b128 v[248:251], v22 offset:32768\n" \
"v_mfma_f32_16x16x32_bf16 v[160:163], a[188:191], a[152:155], 0\n" \
"ds_read_b128 v[48:51], v23 offset:32768\n" \
"v_mfma_f32_16x16x32_bf16 v[160:163], a[252:255], a[216:219], v[160:163]\n" \
"ds_read_b128 a[152:155], v28 offset:45056\n" \
"v_mfma_f32_16x16x32_bf16 v[176:179], a[188:191], a[156:159], 0\n" \
"ds_read_b128 a[216:219], v29 offset:45056\n" \
"v_mfma_f32_16x16x32_bf16 v[176:179], a[252:255], a[220:223], v[176:179]\n" \
"v_mfma_f32_16x16x32_bf16 v[172:175], a[184:187], a[156:159], 0\n" \
"v_mfma_f32_16x16x32_bf16 v[172:175], a[248:251], a[220:223], v[172:175]\n" \
"v_mfma_f32_16x16x32_bf16 v[168:171], a[180:183], a[156:159], 0\n" \
"v_mfma_f32_16x16x32_bf16 v[168:171], a[244:247], a[220:223], v[168:171]\n" \
"v_mfma_f32_16x16x32_bf16 v[164:167], a[176:179], a[156:159], 0\n" \
"v_mfma_f32_16x16x32_bf16 v[164:167], a[240:243], a[220:223], v[164:167]\n" \
"ds_read_b128 a[156:159], v28 offset:47104\n" \
"ds_read_b128 a[220:223], v29 offset:47104\n" \
"s_waitcnt lgkmcnt(15)\n" \
"v_mfma_f32_16x16x32_bf16 a[0:3], a[160:163], a[128:131], a[0:3]\n" \
"s_add_u32 m0, s30, 0x1000\n" \
"s_addk_i32 s84, 0x3000\n" \
"v_mfma_f32_16x16x32_bf16 a[0:3], a[224:227], a[192:195], a[0:3]\n" \
"buffer_load_dwordx4 v24, s[76:79], s84 offen offset:0 lds\n" \
"v_mfma_f32_16x16x32_bf16 a[4:7], a[164:167], a[128:131], a[4:7]\n" \
"ds_read_b64_tr_b16 a[176:177], v36 offset:32768\n" \
"v_mov_b32_e64 v11, 0\n" \
"v_mfma_f32_16x16x32_bf16 a[4:7], a[228:231], a[192:195], a[4:7]\n" \
"ds_read_b64_tr_b16 a[178:179], v36 offset:33792\n" \
"s_and_b32 s6, s2, 7\n" \
"v_mfma_f32_16x16x32_bf16 a[12:15], a[172:175], a[128:131], a[12:15]\n" \
"ds_read_b64_tr_b16 a[240:241], v36 offset:40960\n" \
"s_lshl_b32 s7, s3, 1\n" \
"s_waitcnt lgkmcnt(15)\n" \
"v_mfma_f32_16x16x32_bf16 a[12:15], a[236:239], a[192:195], a[12:15]\n" \
"s_add_u32 s88, s41, 0x0\n" \
"buffer_store_dwordx4 v[228:231], v20, s[80:83], s88 offen offset:3072 sc1\n" \
"s_waitcnt lgkmcnt(13)\n" \
"v_mfma_f32_16x16x32_bf16 a[8:11], a[128:131], a[168:171], a[8:11]\n" \
"ds_read_b64_tr_b16 a[242:243], v36 offset:41984\n" \
"s_lshr_b32 s4, s2, 5\n" \
"s_waitcnt lgkmcnt(12)\n" \
"v_mfma_f32_16x16x32_bf16 a[8:11], a[192:195], a[232:235], a[8:11]\n" \
"ds_read_b64_tr_b16 a[180:181], v37 offset:32768\n" \
"s_lshl_b32 s5, s6, 2\n" \
"v_mfma_f32_16x16x32_bf16 a[24:27], a[132:135], a[168:171], a[24:27]\n" \
"ds_read_b64_tr_b16 a[182:183], v37 offset:33792\n" \
"s_bfe_u32 s8, s2, 0x20003\n" \
"v_mfma_f32_16x16x32_bf16 a[24:27], a[196:199], a[232:235], a[24:27]\n" \
"ds_read_b64_tr_b16 a[244:245], v37 offset:40960\n" \
"s_and_b32 s7, s7, 4\n" \
"v_mfma_f32_16x16x32_bf16 a[28:31], a[172:175], a[132:135], a[28:31]\n" \
"s_and_b32 s9, s3, 1\n" \
"s_or_b32 s13, s5, s8\n" \
"v_mfma_f32_16x16x32_bf16 a[28:31], a[236:239], a[196:199], a[28:31]\n" \
"s_addk_i32 s84, 0x2000\n" \
"v_mfma_f32_16x16x32_bf16 a[20:23], a[164:167], a[132:135], a[20:23]\n" \
"buffer_load_dwordx4 v25, s[76:79], s84 offen offset:1024 lds\n" \
"v_mfma_f32_16x16x32_bf16 a[20:23], a[228:231], a[196:199], a[20:23]\n" \
"ds_read_b64_tr_b16 a[246:247], v37 offset:41984\n" \
"s_or_b32 s9, s7, s9\n" \
"v_mfma_f32_16x16x32_bf16 a[16:19], a[160:163], a[132:135], a[16:19]\n" \
"ds_read_b64_tr_b16 a[184:185], v38 offset:32768\n" \
"s_mul_i32 s7, s4, 0xa00\n" \
"v_mfma_f32_16x16x32_bf16 a[16:19], a[224:227], a[196:199], a[16:19]\n" \
"ds_read_b64_tr_b16 a[186:187], v38 offset:33792\n" \
"s_add_i32 s7, s7, 0x5bc1000\n" \
"v_mfma_f32_16x16x32_bf16 a[32:35], a[160:163], a[136:139], a[32:35]\n" \
"s_addk_i32 s84, 0x2000\n" \
"v_mfma_f32_16x16x32_bf16 a[32:35], a[224:227], a[200:203], a[32:35]\n" \
"buffer_load_dwordx4 v24, s[76:79], s84 offen offset:2048 lds\n" \
"v_mfma_f32_16x16x32_bf16 a[36:39], a[164:167], a[136:139], a[36:39]\n" \
"ds_read_b64_tr_b16 a[248:249], v38 offset:40960\n" \
"s_lshl_b32 s10, s13, 2\n" \
"v_mfma_f32_16x16x32_bf16 a[36:39], a[228:231], a[200:203], a[36:39]\n" \
"ds_read_b64_tr_b16 a[250:251], v38 offset:41984\n" \
"s_or_b32 s10, s10, s7\n" \
"v_mfma_f32_16x16x32_bf16 a[44:47], a[172:175], a[136:139], a[44:47]\n" \
"ds_read_b64_tr_b16 a[188:189], v39 offset:32768\n" \
"v_writelane_b32 v11, s10, 0\n" \
"v_mfma_f32_16x16x32_bf16 a[44:47], a[236:239], a[200:203], a[44:47]\n" \
"ds_read_b64_tr_b16 a[190:191], v39 offset:33792\n" \
"s_lshl_b32 s14, s4, 3\n" \
"v_mfma_f32_16x16x32_bf16 a[40:43], a[136:139], a[168:171], a[40:43]\n" \
"s_add_u32 s89, s41, 0x1000\n" \
"buffer_store_dwordx4 v[232:235], v20, s[80:83], s89 offen offset:3072 sc1\n" \
"v_mfma_f32_16x16x32_bf16 a[40:43], a[200:203], a[232:235], a[40:43]\n" \
"ds_read_b64_tr_b16 a[252:253], v39 offset:40960\n" \
"v_writelane_b32 v11, s7, 1\n" \
"v_mfma_f32_16x16x32_bf16 a[56:59], a[140:143], a[168:171], a[56:59]\n" \
"ds_read_b64_tr_b16 a[254:255], v39 offset:41984\n" \
"s_or_b32 s7, s14, s6\n" \
"v_mfma_f32_16x16x32_bf16 a[56:59], a[204:207], a[232:235], a[56:59]\n" \
"s_lshl_b32 s7, s7, 10\n" \
"s_lshl_b32 s10, s3, 8\n" \
"v_mfma_f32_16x16x32_bf16 a[60:63], a[172:175], a[140:143], a[60:63]\n" \
"s_or_b32 s7, s7, s10\n" \
"s_or_b32 s10, s7, 0x5c00200\n" \
"v_mfma_f32_16x16x32_bf16 a[60:63], a[236:239], a[204:207], a[60:63]\n" \
"s_addk_i32 s84, 0x2000\n" \
"v_mfma_f32_16x16x32_bf16 a[52:55], a[164:167], a[140:143], a[52:55]\n" \
"buffer_load_dwordx4 v25, s[76:79], s84 offen offset:3072 lds\n" \
"v_mfma_f32_16x16x32_bf16 a[52:55], a[228:231], a[204:207], a[52:55]\n" \
"v_writelane_b32 v11, s10, 2\n" \
"s_lshl_b32 s10, s8, 2\n" \
"v_mfma_f32_16x16x32_bf16 a[48:51], a[160:163], a[140:143], a[48:51]\n" \
"s_or_b32 s11, s10, s7\n" \
"s_or_b32 s11, s11, 0x5c00000\n" \
"v_mfma_f32_16x16x32_bf16 a[48:51], a[224:227], a[204:207], a[48:51]\n" \
"v_writelane_b32 v11, s11, 3\n" \
"s_or_b32 s7, s7, 0x5c00080\n" \
"s_waitcnt lgkmcnt(15)\n" \
"v_mfma_f32_16x16x32_bf16 a[64:67], a[164:167], v[248:251], a[64:67]\n" \
"v_writelane_b32 v11, s7, 4\n" \
"s_or_b32 s7, s7, s10\n" \
"v_mfma_f32_16x16x32_bf16 a[64:67], a[228:231], v[48:51], a[64:67]\n" \
"s_add_i32 s10, s3, s2\n" \
"v_writelane_b32 v11, s7, 5\n" \
"v_mfma_f32_16x16x32_bf16 a[80:83], a[172:175], v[248:251], a[80:83]\n" \
"s_mov_b32 s7, 2.0\n" \
"s_mov_b32 s11, 0x5bc0000\n" \
"v_mfma_f32_16x16x32_bf16 a[80:83], a[236:239], v[48:51], a[80:83]\n" \
"s_cmp_lg_u32 s10, 0\n" \
"s_cselect_b32 s10, s7, s11\n" \
"v_mfma_f32_16x16x32_bf16 a[68:71], v[248:251], a[168:171], a[68:71]\n" \
"s_lshr_b32 s15, s2, 3\n" \
"v_writelane_b32 v11, s10, 54\n" \
"v_mfma_f32_16x16x32_bf16 a[68:71], v[48:51], a[232:235], a[68:71]\n" \
"s_waitcnt vmcnt(10) lgkmcnt(0)\n" \
"s_barrier\n" \
"v_xor_b32_e32 v28, 0x20000, v28\n" \
"v_xor_b32_e32 v29, 0x20000, v29\n" \
"v_mfma_f32_16x16x32_bf16 v[52:55], a[176:179], a[128:131], v[52:55]\n" \
"s_add_u32 m0, s40, 0x8000\n" \
"s_add_u32 s85, s39, 0x6000\n" \
"v_mfma_f32_16x16x32_bf16 v[52:55], a[240:243], a[192:195], v[52:55]\n" \
"buffer_load_dwordx4 v20, s[80:83], s85 offen sc1 lds\n" \
"v_mfma_f32_16x16x32_bf16 v[56:59], a[180:183], a[128:131], v[56:59]\n" \
"ds_read_b64_tr_b16 a[160:161], v32 offset:0\n" \
"s_lshl_b32 s10, s6, 5\n" \
"v_mfma_f32_16x16x32_bf16 v[56:59], a[244:247], a[192:195], v[56:59]\n" \
"ds_read_b64_tr_b16 a[162:163], v32 offset:512\n" \
"s_min_u32 s16, s3, 2\n" \
"s_nop 0\n" \
"v_mfma_f32_16x16x32_bf16 v[60:63], a[184:187], a[128:131], v[60:63]\n" \
"s_add_u32 m0, s40, 0x9000\n" \
"s_add_u32 s85, s39, 0x7000\n" \
"v_mfma_f32_16x16x32_bf16 v[60:63], a[248:251], a[192:195], v[60:63]\n" \
"buffer_load_dwordx4 v20, s[80:83], s85 offen sc1 lds\n" \
"v_mfma_f32_16x16x32_bf16 v[64:67], a[188:191], a[128:131], v[64:67]\n" \
"ds_read_b64_tr_b16 a[224:225], v32 offset:4096\n" \
"s_mul_i32 s12, s8, 0x120\n" \
"v_mfma_f32_16x16x32_bf16 v[64:67], a[252:255], a[192:195], v[64:67]\n" \
"ds_read_b128 a[128:131], v28 offset:0\n" \
"v_mfma_f32_16x16x32_bf16 v[80:83], a[188:191], a[132:135], v[80:83]\n" \
"ds_read_b128 a[192:195], v29 offset:0\n" \
"v_mfma_f32_16x16x32_bf16 v[80:83], a[252:255], a[196:199], v[80:83]\n" \
"s_add_u32 s88, s41, 0x1000\n" \
"buffer_store_dwordx4 v[236:239], v20, s[80:83], s88 offen offset:2048 sc1\n" \
"v_mfma_f32_16x16x32_bf16 v[76:79], a[184:187], a[132:135], v[76:79]\n" \
"ds_read_b64_tr_b16 a[226:227], v32 offset:4608\n" \
"v_mfma_f32_16x16x32_bf16 v[76:79], a[248:251], a[196:199], v[76:79]\n" \
"ds_read_b64_tr_b16 a[164:165], v33 offset:0\n" \
"v_mfma_f32_16x16x32_bf16 v[72:75], a[180:183], a[132:135], v[72:75]\n" \
"s_add_u32 m0, s40, 0xb000\n" \
"s_movk_i32 s84, 0x180\n" \
"s_nop 0\n" \
"v_mfma_f32_16x16x32_bf16 v[72:75], a[244:247], a[196:199], v[72:75]\n" \
"buffer_load_dwordx4 v21, s[76:79], s84 offen offset:0 lds\n" \
"v_mfma_f32_16x16x32_bf16 v[68:71], a[176:179], a[132:135], v[68:71]\n" \
"ds_read_b64_tr_b16 a[166:167], v33 offset:512\n" \
"v_mfma_f32_16x16x32_bf16 v[68:71], a[240:243], a[196:199], v[68:71]\n" \
"ds_read_b128 a[132:135], v28 offset:2048\n" \
"v_mfma_f32_16x16x32_bf16 v[84:87], a[176:179], a[136:139], v[84:87]\n" \
"ds_read_b128 a[196:199], v29 offset:2048\n" \
"v_mfma_f32_16x16x32_bf16 v[84:87], a[240:243], a[200:203], v[84:87]\n" \
"s_add_u32 m0, s31, 0x8000\n" \
"s_mov_b32 s84, 0x180000\n" \
"v_mfma_f32_16x16x32_bf16 v[88:91], a[180:183], a[136:139], v[88:91]\n" \
"buffer_load_dwordx4 v26, s[68:71], s84 offen offset:0 lds\n" \
"v_mfma_f32_16x16x32_bf16 v[88:91], a[244:247], a[200:203], v[88:91]\n" \
"ds_read_b64_tr_b16 a[228:229], v33 offset:4096\n" \
"v_mfma_f32_16x16x32_bf16 v[92:95], a[184:187], a[136:139], v[92:95]\n" \
"ds_read_b64_tr_b16 a[230:231], v33 offset:4608\n" \
"v_mfma_f32_16x16x32_bf16 v[92:95], a[248:251], a[200:203], v[92:95]\n" \
"s_addk_i32 s84, 0x7c00\n" \
"v_mfma_f32_16x16x32_bf16 v[96:99], a[188:191], a[136:139], v[96:99]\n" \
"buffer_load_dwordx4 v26, s[68:71], s84 offen offset:1024 lds\n" \
"v_mfma_f32_16x16x32_bf16 v[96:99], a[252:255], a[200:203], v[96:99]\n" \
"ds_read_b128 a[136:139], v28 offset:4096\n" \
"v_mfma_f32_16x16x32_bf16 v[112:115], a[188:191], a[140:143], v[112:115]\n" \
"ds_read_b128 a[200:203], v29 offset:4096\n" \
"v_mfma_f32_16x16x32_bf16 v[112:115], a[252:255], a[204:207], v[112:115]\n" \
"s_addk_i32 s84, 0x7c00\n" \
"v_mfma_f32_16x16x32_bf16 v[108:111], a[184:187], a[140:143], v[108:111]\n" \
"buffer_load_dwordx4 v27, s[68:71], s84 offen offset:2048 lds\n" \
"v_mfma_f32_16x16x32_bf16 v[108:111], a[248:251], a[204:207], v[108:111]\n" \
"ds_read_b64_tr_b16 a[172:173], v35 offset:0\n" \
"v_mfma_f32_16x16x32_bf16 v[104:107], a[180:183], a[140:143], v[104:107]\n" \
"ds_read_b64_tr_b16 a[174:175], v35 offset:512\n" \
"v_mfma_f32_16x16x32_bf16 v[104:107], a[244:247], a[204:207], v[104:107]\n" \
"ds_read_b64_tr_b16 a[236:237], v35 offset:4096\n" \
"v_mfma_f32_16x16x32_bf16 v[100:103], a[176:179], a[140:143], v[100:103]\n" \
"s_add_u32 s11, s22, s12\n" \
"s_addc_u32 s17, s23, 0\n" \
"v_mfma_f32_16x16x32_bf16 v[100:103], a[240:243], a[204:207], v[100:103]\n" \
"ds_read_b128 a[140:143], v28 offset:6144\n" \
"v_mfma_f32_16x16x32_bf16 v[116:119], a[176:179], a[144:147], v[116:119]\n" \
"ds_read_b128 a[204:207], v29 offset:6144\n" \
"v_mfma_f32_16x16x32_bf16 v[116:119], a[240:243], a[208:211], v[116:119]\n" \
"s_addk_i32 s84, 0x7c00\n" \
"v_mfma_f32_16x16x32_bf16 v[120:123], a[180:183], a[144:147], v[120:123]\n" \
"buffer_load_dwordx4 v27, s[68:71], s84 offen offset:3072 lds\n" \
"v_mfma_f32_16x16x32_bf16 v[120:123], a[244:247], a[208:211], v[120:123]\n" \
"s_add_u32 m0, s30, 0x8000\n" \
"s_movk_i32 s84, 0x200\n" \
"v_mfma_f32_16x16x32_bf16 v[124:127], a[184:187], a[144:147], v[124:127]\n" \
"buffer_load_dwordx4 v24, s[76:79], s84 offen offset:0 lds\n" \
"v_mfma_f32_16x16x32_bf16 v[124:127], a[248:251], a[208:211], v[124:127]\n" \
"ds_read_b64_tr_b16 a[238:239], v35 offset:4608\n" \
"v_mfma_f32_16x16x32_bf16 v[128:131], a[188:191], a[144:147], v[128:131]\n" \
"s_add_u32 s89, s41, 0x2000\n" \
"buffer_store_dwordx4 v[240:243], v20, s[80:83], s89 offen offset:2048 sc1\n" \
"v_mfma_f32_16x16x32_bf16 v[128:131], a[252:255], a[208:211], v[128:131]\n" \
"ds_read_b128 a[144:147], v28 offset:8192\n" \
"v_mfma_f32_16x16x32_bf16 v[144:147], a[188:191], a[148:151], v[144:147]\n" \
"ds_read_b128 a[208:211], v29 offset:8192\n" \
"v_mfma_f32_16x16x32_bf16 v[144:147], a[252:255], a[212:215], v[144:147]\n" \
"ds_read_b64_tr_b16 a[168:169], v34 offset:0\n" \
"v_mfma_f32_16x16x32_bf16 v[140:143], a[184:187], a[148:151], v[140:143]\n" \
"ds_read_b64_tr_b16 a[170:171], v34 offset:512\n" \
"v_mfma_f32_16x16x32_bf16 v[140:143], a[248:251], a[212:215], v[140:143]\n" \
"s_movk_i32 s84, 0x2200\n" \
"s_nop 0\n" \
"v_mfma_f32_16x16x32_bf16 v[136:139], a[180:183], a[148:151], v[136:139]\n" \
"buffer_load_dwordx4 v25, s[76:79], s84 offen offset:1024 lds\n" \
"v_mfma_f32_16x16x32_bf16 v[136:139], a[244:247], a[212:215], v[136:139]\n" \
"ds_read_b64_tr_b16 a[232:233], v34 offset:4096\n" \
"v_mfma_f32_16x16x32_bf16 v[132:135], a[176:179], a[148:151], v[132:135]\n" \
"ds_read_b64_tr_b16 a[234:235], v34 offset:4608\n" \
"v_mfma_f32_16x16x32_bf16 v[132:135], a[240:243], a[212:215], v[132:135]\n" \
"ds_read_b128 a[148:151], v28 offset:10240\n" \
"v_mfma_f32_16x16x32_bf16 v[148:151], a[176:179], a[152:155], v[148:151]\n" \
"ds_read_b128 a[212:215], v29 offset:10240\n" \
"v_mfma_f32_16x16x32_bf16 v[148:151], a[240:243], a[216:219], v[148:151]\n" \
"s_movk_i32 s84, 0x4200\n" \
"v_mfma_f32_16x16x32_bf16 v[152:155], a[180:183], a[152:155], v[152:155]\n" \
"buffer_load_dwordx4 v24, s[76:79], s84 offen offset:2048 lds\n" \
"v_mfma_f32_16x16x32_bf16 v[152:155], a[244:247], a[216:219], v[152:155]\n" \
"s_movk_i32 s84, 0x6200\n" \
"v_mfma_f32_16x16x32_bf16 v[156:159], a[184:187], a[152:155], v[156:159]\n" \
"buffer_load_dwordx4 v25, s[76:79], s84 offen offset:3072 lds\n" \
"v_mfma_f32_16x16x32_bf16 v[156:159], a[248:251], a[216:219], v[156:159]\n" \
"ds_read_b128 v[248:251], v22 offset:0\n" \
"v_mfma_f32_16x16x32_bf16 v[160:163], a[188:191], a[152:155], v[160:163]\n" \
"ds_read_b128 v[48:51], v23 offset:0\n" \
"v_mfma_f32_16x16x32_bf16 v[160:163], a[252:255], a[216:219], v[160:163]\n" \
"ds_read_b128 a[152:155], v28 offset:12288\n" \
"v_mfma_f32_16x16x32_bf16 v[176:179], a[188:191], a[156:159], v[176:179]\n" \
"ds_read_b128 a[216:219], v29 offset:12288\n" \
"v_mfma_f32_16x16x32_bf16 v[176:179], a[252:255], a[220:223], v[176:179]\n" \
"s_add_u32 s18, s24, s12\n" \
"v_writelane_b32 v11, s11, 9\n" \
"s_addc_u32 s19, s25, 0\n" \
"v_mfma_f32_16x16x32_bf16 v[172:175], a[184:187], a[156:159], v[172:175]\n" \
"s_and_b32 s20, s17, 0xffff\n" \
"v_writelane_b32 v11, s20, 10\n" \
"v_mfma_f32_16x16x32_bf16 v[172:175], a[248:251], a[220:223], v[172:175]\n" \
"s_and_b32 s20, s19, 0xffff\n" \
"v_writelane_b32 v11, s18, 11\n" \
"v_mfma_f32_16x16x32_bf16 v[168:171], a[180:183], a[156:159], v[168:171]\n" \
"s_cmpk_lt_u32 s3, 3\n" \
"s_cselect_b32 s11, s11, s18\n" \
"v_mfma_f32_16x16x32_bf16 v[168:171], a[244:247], a[220:223], v[168:171]\n" \
"s_movk_i32 s18, 0xfff\n" \
"v_writelane_b32 v11, s20, 12\n" \
"v_mfma_f32_16x16x32_bf16 v[164:167], a[176:179], a[156:159], v[164:167]\n" \
"s_and_b32 s20, s29, 0xffff\n" \
"v_writelane_b32 v11, s28, 13\n" \
"v_mfma_f32_16x16x32_bf16 v[164:167], a[240:243], a[220:223], v[164:167]\n" \
"ds_read_b128 a[156:159], v28 offset:14336\n" \
"ds_read_b128 a[220:223], v29 offset:14336\n" \
"s_waitcnt lgkmcnt(15)\n" \
"v_mfma_f32_16x16x32_bf16 a[0:3], a[160:163], a[128:131], a[0:3]\n" \
"s_add_u32 m0, s30, 0x9000\n" \
"s_addk_i32 s84, 0x3000\n" \
"v_mfma_f32_16x16x32_bf16 a[0:3], a[224:227], a[192:195], a[0:3]\n" \
"buffer_load_dwordx4 v24, s[76:79], s84 offen offset:0 lds\n" \
"v_mfma_f32_16x16x32_bf16 a[4:7], a[164:167], a[128:131], a[4:7]\n" \
"ds_read_b64_tr_b16 a[176:177], v36 offset:0\n" \
"v_writelane_b32 v11, s20, 14\n" \
"v_mfma_f32_16x16x32_bf16 a[4:7], a[228:231], a[192:195], a[4:7]\n" \
"ds_read_b64_tr_b16 a[178:179], v36 offset:1024\n" \
"v_writelane_b32 v11, s11, 15\n" \
"v_mfma_f32_16x16x32_bf16 a[12:15], a[172:175], a[128:131], a[12:15]\n" \
"ds_read_b64_tr_b16 a[240:241], v36 offset:8192\n" \
"s_waitcnt lgkmcnt(15)\n" \
"v_mfma_f32_16x16x32_bf16 a[12:15], a[236:239], a[192:195], a[12:15]\n" \
"s_add_u32 s88, s41, 0x2000\n" \
"buffer_store_dwordx4 v[244:247], v20, s[80:83], s88 offen offset:3072 sc1\n" \
"s_waitcnt lgkmcnt(13)\n" \
"v_mfma_f32_16x16x32_bf16 a[8:11], a[128:131], a[168:171], a[8:11]\n" \
"ds_read_b64_tr_b16 a[242:243], v36 offset:9216\n" \
"s_waitcnt lgkmcnt(12)\n" \
"v_mfma_f32_16x16x32_bf16 a[8:11], a[192:195], a[232:235], a[8:11]\n" \
"ds_read_b64_tr_b16 a[180:181], v37 offset:0\n" \
"v_mfma_f32_16x16x32_bf16 a[24:27], a[132:135], a[168:171], a[24:27]\n" \
"ds_read_b64_tr_b16 a[182:183], v37 offset:1024\n" \
"v_mfma_f32_16x16x32_bf16 a[24:27], a[196:199], a[232:235], a[24:27]\n" \
"ds_read_b64_tr_b16 a[244:245], v37 offset:8192\n" \
"v_mfma_f32_16x16x32_bf16 a[28:31], a[172:175], a[132:135], a[28:31]\n" \
"s_cmpk_lt_u32 s3, 3\n" \
"s_cselect_b32 s11, s17, s19\n" \
"v_mfma_f32_16x16x32_bf16 a[28:31], a[236:239], a[196:199], a[28:31]\n" \
"s_addk_i32 s84, 0x2000\n" \
"s_nop 0\n" \
"v_mfma_f32_16x16x32_bf16 a[20:23], a[164:167], a[132:135], a[20:23]\n" \
"buffer_load_dwordx4 v25, s[76:79], s84 offen offset:1024 lds\n" \
"v_mfma_f32_16x16x32_bf16 a[20:23], a[228:231], a[196:199], a[20:23]\n" \
"ds_read_b64_tr_b16 a[246:247], v37 offset:9216\n" \
"s_and_b32 s11, s11, 0xffff\n" \
"v_mfma_f32_16x16x32_bf16 a[16:19], a[160:163], a[132:135], a[16:19]\n" \
"ds_read_b64_tr_b16 a[184:185], v38 offset:0\n" \
"v_writelane_b32 v11, s11, 16\n" \
"v_mfma_f32_16x16x32_bf16 a[16:19], a[224:227], a[196:199], a[16:19]\n" \
"ds_read_b64_tr_b16 a[186:187], v38 offset:1024\n" \
"s_mul_i32 s11, s6, 0x48\n" \
"v_mfma_f32_16x16x32_bf16 a[32:35], a[160:163], a[136:139], a[32:35]\n" \
"s_addk_i32 s84, 0x2000\n" \
"s_nop 0\n" \
"v_mfma_f32_16x16x32_bf16 a[32:35], a[224:227], a[200:203], a[32:35]\n" \
"buffer_load_dwordx4 v24, s[76:79], s84 offen offset:2048 lds\n" \
"v_mfma_f32_16x16x32_bf16 a[36:39], a[164:167], a[136:139], a[36:39]\n" \
"ds_read_b64_tr_b16 a[248:249], v38 offset:8192\n" \
"s_add_i32 s17, s11, 0x47\n" \
"v_mfma_f32_16x16x32_bf16 a[36:39], a[228:231], a[200:203], a[36:39]\n" \
"ds_read_b64_tr_b16 a[250:251], v38 offset:9216\n" \
"v_mfma_f32_16x16x32_bf16 a[44:47], a[172:175], a[136:139], a[44:47]\n" \
"ds_read_b64_tr_b16 a[188:189], v39 offset:0\n" \
"v_mfma_f32_16x16x32_bf16 a[44:47], a[236:239], a[200:203], a[44:47]\n" \
".fill (L_pb + 24288 - .) / 4, 4, 0xBF800000\n" \
"L_pp6:\n" \
"s_cbranch_execz L_pret\n" \
"ds_read_b64_tr_b16 a[190:191], v39 offset:1024\n" \
"v_mfma_f32_16x16x32_bf16 a[40:43], a[136:139], a[168:171], a[40:43]\n" \
"s_add_u32 s89, s41, 0x3000\n" \
"buffer_store_dwordx4 v[184:187], v20, s[80:83], s89 offen offset:3072 sc1\n" \
"v_mfma_f32_16x16x32_bf16 a[40:43], a[200:203], a[232:235], a[40:43]\n" \
"ds_read_b64_tr_b16 a[252:253], v39 offset:8192\n" \
"v_mfma_f32_16x16x32_bf16 a[56:59], a[140:143], a[168:171], a[56:59]\n" \
"ds_read_b64_tr_b16 a[254:255], v39 offset:9216\n" \
"v_mfma_f32_16x16x32_bf16 a[56:59], a[204:207], a[232:235], a[56:59]\n" \
"s_cmpk_lt_u32 s3, 3\n" \
"s_cselect_b32 s17, s17, s18\n" \
"v_mfma_f32_16x16x32_bf16 a[60:63], a[172:175], a[140:143], a[60:63]\n" \
"s_mulk_i32 s17, 0x480\n" \
"s_addk_i32 s17, 0x120\n" \
"v_mfma_f32_16x16x32_bf16 a[60:63], a[236:239], a[204:207], a[60:63]\n" \
"s_addk_i32 s84, 0x2000\n" \
"v_mfma_f32_16x16x32_bf16 a[52:55], a[164:167], a[140:143], a[52:55]\n" \
"buffer_load_dwordx4 v25, s[76:79], s84 offen offset:3072 lds\n" \
"v_mfma_f32_16x16x32_bf16 a[52:55], a[228:231], a[204:207], a[52:55]\n" \
"v_writelane_b32 v11, s17, 17\n" \
"s_lshl_b32 s17, s3, 5\n" \
"v_mfma_f32_16x16x32_bf16 a[48:51], a[160:163], a[140:143], a[48:51]\n" \
"s_add_i32 s11, s11, s17\n" \
"s_mul_i32 s17, s11, 0x480\n" \
"v_mfma_f32_16x16x32_bf16 a[48:51], a[224:227], a[204:207], a[48:51]\n" \
"s_mul_i32 s11, s13, 0x24000\n" \
"s_add_i32 s18, s11, 0x1b000\n" \
"s_waitcnt lgkmcnt(15)\n" \
"v_mfma_f32_16x16x32_bf16 a[64:67], a[164:167], v[248:251], a[64:67]\n" \
"s_cmpk_lt_u32 s3, 3\n" \
"s_cselect_b32 s17, s17, s18\n" \
"v_mfma_f32_16x16x32_bf16 a[64:67], a[228:231], v[48:51], a[64:67]\n" \
"s_mov_b32 s18, 0x8000\n" \
"v_writelane_b32 v11, s17, 18\n" \
"v_mfma_f32_16x16x32_bf16 a[80:83], a[172:175], v[248:251], a[80:83]\n" \
"s_mul_i32 s17, s16, 0x2400\n" \
"s_mul_i32 s16, s16, 0x9000\n" \
"v_mfma_f32_16x16x32_bf16 a[80:83], a[236:239], v[48:51], a[80:83]\n" \
"s_bitset1_b32 s17, 17\n" \
"v_writelane_b32 v11, s17, 19\n" \
"v_mfma_f32_16x16x32_bf16 a[68:71], v[248:251], a[168:171], a[68:71]\n" \
"s_add_i32 s16, s11, s16\n" \
"v_writelane_b32 v11, s16, 26\n" \
"v_mfma_f32_16x16x32_bf16 a[68:71], v[48:51], a[232:235], a[68:71]\n" \
"s_waitcnt vmcnt(11) lgkmcnt(0)\n" \
"s_barrier\n" \
"v_xor_b32_e32 v28, 0x20000, v28\n" \
"v_xor_b32_e32 v29, 0x20000, v29\n" \
"v_mfma_f32_16x16x32_bf16 v[52:55], a[176:179], a[128:131], v[52:55]\n" \
"s_add_u32 m0, s40, 0x0\n" \
"s_add_u32 s85, s39, 0x8000\n" \
"v_mfma_f32_16x16x32_bf16 v[52:55], a[240:243], a[192:195], v[52:55]\n" \
"buffer_load_dwordx4 v20, s[80:83], s85 offen sc1 lds\n" \
"v_mfma_f32_16x16x32_bf16 v[56:59], a[180:183], a[128:131], v[56:59]\n" \
"ds_read_b64_tr_b16 a[160:161], v32 offset:32768\n" \
"s_mul_i32 s16, s3, 0x2400\n" \
"v_mfma_f32_16x16x32_bf16 v[56:59], a[244:247], a[192:195], v[56:59]\n" \
"ds_read_b64_tr_b16 a[162:163], v32 offset:33280\n" \
"s_add_i32 s17, s16, 0xa400\n" \
"v_mfma_f32_16x16x32_bf16 v[60:63], a[184:187], a[128:131], v[60:63]\n" \
"s_add_u32 m0, s40, 0x1000\n" \
"s_add_u32 s85, s39, 0x9000\n" \
"v_mfma_f32_16x16x32_bf16 v[60:63], a[248:251], a[192:195], v[60:63]\n" \
"buffer_load_dwordx4 v20, s[80:83], s85 offen sc1 lds\n" \
"v_mfma_f32_16x16x32_bf16 v[64:67], a[188:191], a[128:131], v[64:67]\n" \
"ds_read_b64_tr_b16 a[224:225], v32 offset:36864\n" \
"v_mfma_f32_16x16x32_bf16 v[64:67], a[252:255], a[192:195], v[64:67]\n" \
"ds_read_b128 a[128:131], v28 offset:0\n" \
"v_mfma_f32_16x16x32_bf16 v[80:83], a[188:191], a[132:135], v[80:83]\n" \
"ds_read_b128 a[192:195], v29 offset:0\n" \
"v_mfma_f32_16x16x32_bf16 v[80:83], a[252:255], a[196:199], v[80:83]\n" \
"s_add_u32 s88, s41, 0x3000\n" \
"buffer_store_dwordx4 v[188:191], v20, s[80:83], s88 offen offset:2048 sc1\n" \
"v_mfma_f32_16x16x32_bf16 v[76:79], a[184:187], a[132:135], v[76:79]\n" \
"ds_read_b64_tr_b16 a[226:227], v32 offset:37376\n" \
"v_mfma_f32_16x16x32_bf16 v[76:79], a[248:251], a[196:199], v[76:79]\n" \
"ds_read_b64_tr_b16 a[164:165], v33 offset:32768\n" \
"v_mfma_f32_16x16x32_bf16 v[72:75], a[180:183], a[132:135], v[72:75]\n" \
"s_add_u32 m0, s40, 0x3000\n" \
"s_movk_i32 s84, 0x200\n" \
"v_mfma_f32_16x16x32_bf16 v[72:75], a[244:247], a[196:199], v[72:75]\n" \
"buffer_load_dwordx4 v21, s[76:79], s84 offen offset:0 lds\n" \
"v_mfma_f32_16x16x32_bf16 v[68:71], a[176:179], a[132:135], v[68:71]\n" \
"ds_read_b64_tr_b16 a[166:167], v33 offset:33280\n" \
"v_mfma_f32_16x16x32_bf16 v[68:71], a[240:243], a[196:199], v[68:71]\n" \
"ds_read_b128 a[132:135], v28 offset:2048\n" \
"v_mfma_f32_16x16x32_bf16 v[84:87], a[176:179], a[136:139], v[84:87]\n" \
"ds_read_b128 a[196:199], v29 offset:2048\n" \
"v_mfma_f32_16x16x32_bf16 v[84:87], a[240:243], a[200:203], v[84:87]\n" \
"s_add_u32 m0, s31, 0x0\n" \
"s_mov_b32 s84, 0x200000\n" \
"v_mfma_f32_16x16x32_bf16 v[88:91], a[180:183], a[136:139], v[88:91]\n" \
"buffer_load_dwordx4 v26, s[68:71], s84 offen offset:0 lds\n" \
"v_mfma_f32_16x16x32_bf16 v[88:91], a[244:247], a[200:203], v[88:91]\n" \
"ds_read_b64_tr_b16 a[228:229], v33 offset:36864\n" \
"v_mfma_f32_16x16x32_bf16 v[92:95], a[184:187], a[136:139], v[92:95]\n" \
"ds_read_b64_tr_b16 a[230:231], v33 offset:37376\n" \
"v_mfma_f32_16x16x32_bf16 v[92:95], a[248:251], a[200:203], v[92:95]\n" \
"s_addk_i32 s84, 0x7c00\n" \
"v_mfma_f32_16x16x32_bf16 v[96:99], a[188:191], a[136:139], v[96:99]\n" \
"buffer_load_dwordx4 v26, s[68:71], s84 offen offset:1024 lds\n" \
"v_mfma_f32_16x16x32_bf16 v[96:99], a[252:255], a[200:203], v[96:99]\n" \
"ds_read_b128 a[136:139], v28 offset:4096\n" \
"v_mfma_f32_16x16x32_bf16 v[112:115], a[188:191], a[140:143], v[112:115]\n" \
"ds_read_b128 a[200:203], v29 offset:4096\n" \
"v_mfma_f32_16x16x32_bf16 v[112:115], a[252:255], a[204:207], v[112:115]\n" \
"s_addk_i32 s84, 0x7c00\n" \
"v_mfma_f32_16x16x32_bf16 v[108:111], a[184:187], a[140:143], v[108:111]\n" \
"buffer_load_dwordx4 v27, s[68:71], s84 offen offset:2048 lds\n" \
"v_mfma_f32_16x16x32_bf16 v[108:111], a[248:251], a[204:207], v[108:111]\n" \
"ds_read_b64_tr_b16 a[172:173], v35 offset:32768\n" \
"v_mfma_f32_16x16x32_bf16 v[104:107], a[180:183], a[140:143], v[104:107]\n" \
"ds_read_b64_tr_b16 a[174:175], v35 offset:33280\n" \
"v_mfma_f32_16x16x32_bf16 v[104:107], a[244:247], a[204:207], v[104:107]\n" \
"ds_read_b64_tr_b16 a[236:237], v35 offset:36864\n" \
"v_mfma_f32_16x16x32_bf16 v[100:103], a[176:179], a[140:143], v[100:103]\n" \
"s_cmpk_lt_u32 s3, 3\n" \
"s_cselect_b32 s17, s17, s18\n" \
"v_mfma_f32_16x16x32_bf16 v[100:103], a[240:243], a[204:207], v[100:103]\n" \
"ds_read_b128 a[140:143], v28 offset:6144\n" \
"s_mov_b32 s18, 0x1b000\n" \
"v_mfma_f32_16x16x32_bf16 v[116:119], a[176:179], a[144:147], v[116:119]\n" \
"ds_read_b128 a[204:207], v29 offset:6144\n" \
"s_xor_b32 s19, s3, 2\n" \
"v_mfma_f32_16x16x32_bf16 v[116:119], a[240:243], a[208:211], v[116:119]\n" \
"s_addk_i32 s84, 0x7c00\n" \
"v_mfma_f32_16x16x32_bf16 v[120:123], a[180:183], a[144:147], v[120:123]\n" \
"buffer_load_dwordx4 v27, s[68:71], s84 offen offset:3072 lds\n" \
"v_mfma_f32_16x16x32_bf16 v[120:123], a[244:247], a[208:211], v[120:123]\n" \
"s_add_u32 m0, s30, 0x20000\n" \
"s_movk_i32 s84, 0x280\n" \
"v_mfma_f32_16x16x32_bf16 v[124:127], a[184:187], a[144:147], v[124:127]\n" \
"buffer_load_dwordx4 v24, s[76:79], s84 offen offset:0 lds\n" \
"v_mfma_f32_16x16x32_bf16 v[124:127], a[248:251], a[208:211], v[124:127]\n" \
"ds_read_b64_tr_b16 a[238:239], v35 offset:37376\n" \
"v_mfma_f32_16x16x32_bf16 v[128:131], a[188:191], a[144:147], v[128:131]\n" \
"s_add_u32 s89, s42, 0x0\n" \
"buffer_store_dwordx4 v[216:219], v20, s[80:83], s89 offen offset:0 sc1\n" \
"v_mfma_f32_16x16x32_bf16 v[128:131], a[252:255], a[208:211], v[128:131]\n" \
"ds_read_b128 a[144:147], v28 offset:8192\n" \
"v_mfma_f32_16x16x32_bf16 v[144:147], a[188:191], a[148:151], v[144:147]\n" \
"ds_read_b128 a[208:211], v29 offset:8192\n" \
"v_mfma_f32_16x16x32_bf16 v[144:147], a[252:255], a[212:215], v[144:147]\n" \
"ds_read_b64_tr_b16 a[168:169], v34 offset:32768\n" \
"v_mfma_f32_16x16x32_bf16 v[140:143], a[184:187], a[148:151], v[140:143]\n" \
"ds_read_b64_tr_b16 a[170:171], v34 offset:33280\n" \
"v_mfma_f32_16x16x32_bf16 v[140:143], a[248:251], a[212:215], v[140:143]\n" \
"s_movk_i32 s84, 0x2280\n" \
"s_nop 0\n" \
"v_mfma_f32_16x16x32_bf16 v[136:139], a[180:183], a[148:151], v[136:139]\n" \
"buffer_load_dwordx4 v25, s[76:79], s84 offen offset:1024 lds\n" \
"v_mfma_f32_16x16x32_bf16 v[136:139], a[244:247], a[212:215], v[136:139]\n" \
"ds_read_b64_tr_b16 a[232:233], v34 offset:36864\n" \
"v_mfma_f32_16x16x32_bf16 v[132:135], a[176:179], a[148:151], v[132:135]\n" \
"ds_read_b64_tr_b16 a[234:235], v34 offset:37376\n" \
"v_mfma_f32_16x16x32_bf16 v[132:135], a[240:243], a[212:215], v[132:135]\n" \
"ds_read_b128 a[148:151], v28 offset:10240\n" \
"v_mfma_f32_16x16x32_bf16 v[148:151], a[176:179], a[152:155], v[148:151]\n" \
"ds_read_b128 a[212:215], v29 offset:10240\n" \
"v_mfma_f32_16x16x32_bf16 v[148:151], a[240:243], a[216:219], v[148:151]\n" \
"s_movk_i32 s84, 0x4280\n" \
"v_mfma_f32_16x16x32_bf16 v[152:155], a[180:183], a[152:155], v[152:155]\n" \
"buffer_load_dwordx4 v24, s[76:79], s84 offen offset:2048 lds\n" \
"v_mfma_f32_16x16x32_bf16 v[152:155], a[244:247], a[216:219], v[152:155]\n" \
"s_movk_i32 s84, 0x6280\n" \
"v_mfma_f32_16x16x32_bf16 v[156:159], a[184:187], a[152:155], v[156:159]\n" \
"buffer_load_dwordx4 v25, s[76:79], s84 offen offset:3072 lds\n" \
"v_mfma_f32_16x16x32_bf16 v[156:159], a[248:251], a[216:219], v[156:159]\n" \
"ds_read_b128 v[248:251], v22 offset:32768\n" \
"v_mfma_f32_16x16x32_bf16 v[160:163], a[188:191], a[152:155], v[160:163]\n" \
"ds_read_b128 v[48:51], v23 offset:32768\n" \
"v_mfma_f32_16x16x32_bf16 v[160:163], a[252:255], a[216:219], v[160:163]\n" \
"ds_read_b128 a[152:155], v28 offset:12288\n" \
"v_mfma_f32_16x16x32_bf16 v[176:179], a[188:191], a[156:159], v[176:179]\n" \
"ds_read_b128 a[216:219], v29 offset:12288\n" \
"v_mfma_f32_16x16x32_bf16 v[176:179], a[252:255], a[220:223], v[176:179]\n" \
"s_cmp_lg_u32 s19, 0\n" \
"s_cselect_b32 s18, s17, s18\n" \
"v_mfma_f32_16x16x32_bf16 v[172:175], a[184:187], a[156:159], v[172:175]\n" \
"v_writelane_b32 v11, s17, 20\n" \
"s_or_b32 s17, s16, 0x10000\n" \
"v_mfma_f32_16x16x32_bf16 v[172:175], a[248:251], a[220:223], v[172:175]\n" \
"v_writelane_b32 v11, s18, 21\n" \
"s_add_i32 s18, s16, 0x19000\n" \
"v_mfma_f32_16x16x32_bf16 v[168:171], a[180:183], a[156:159], v[168:171]\n" \
"v_writelane_b32 v11, s17, 22\n" \
"s_mov_b32 s17, 0\n" \
"v_mfma_f32_16x16x32_bf16 v[168:171], a[244:247], a[220:223], v[168:171]\n" \
"s_cmpk_lt_u32 s3, 3\n" \
"s_cselect_b32 s18, s18, s17\n" \
"v_mfma_f32_16x16x32_bf16 v[164:167], a[176:179], a[156:159], v[164:167]\n" \
"s_bitset1_b32 s16, 17\n" \
"v_writelane_b32 v11, s18, 23\n" \
"v_mfma_f32_16x16x32_bf16 v[164:167], a[240:243], a[220:223], v[164:167]\n" \
"ds_read_b128 a[156:159], v28 offset:14336\n" \
"ds_read_b128 a[220:223], v29 offset:14336\n" \
"s_waitcnt lgkmcnt(15)\n" \
"v_mfma_f32_16x16x32_bf16 a[0:3], a[160:163], a[128:131], a[0:3]\n" \
"s_add_u32 m0, s30, 0x21000\n" \
"s_addk_i32 s84, 0x3000\n" \
"v_mfma_f32_16x16x32_bf16 a[0:3], a[224:227], a[192:195], a[0:3]\n" \
"buffer_load_dwordx4 v24, s[76:79], s84 offen offset:0 lds\n" \
"v_mfma_f32_16x16x32_bf16 a[4:7], a[164:167], a[128:131], a[4:7]\n" \
"ds_read_b64_tr_b16 a[176:177], v36 offset:32768\n" \
"s_movk_i32 s18, 0x2400\n" \
"v_mfma_f32_16x16x32_bf16 a[4:7], a[228:231], a[192:195], a[4:7]\n" \
"ds_read_b64_tr_b16 a[178:179], v36 offset:33792\n" \
"v_mfma_f32_16x16x32_bf16 a[12:15], a[172:175], a[128:131], a[12:15]\n" \
"ds_read_b64_tr_b16 a[240:241], v36 offset:40960\n" \
"s_waitcnt lgkmcnt(15)\n" \
"v_mfma_f32_16x16x32_bf16 a[12:15], a[236:239], a[192:195], a[12:15]\n" \
"s_add_u32 s88, s42, 0x1000\n" \
"buffer_store_dwordx4 v[220:223], v20, s[80:83], s88 offen offset:0 sc1\n" \
"s_waitcnt lgkmcnt(13)\n" \
"v_mfma_f32_16x16x32_bf16 a[8:11], a[128:131], a[168:171], a[8:11]\n" \
"ds_read_b64_tr_b16 a[242:243], v36 offset:41984\n" \
"s_waitcnt lgkmcnt(12)\n" \
"v_mfma_f32_16x16x32_bf16 a[8:11], a[192:195], a[232:235], a[8:11]\n" \
"ds_read_b64_tr_b16 a[180:181], v37 offset:32768\n" \
"v_mfma_f32_16x16x32_bf16 a[24:27], a[132:135], a[168:171], a[24:27]\n" \
"ds_read_b64_tr_b16 a[182:183], v37 offset:33792\n" \
"v_mfma_f32_16x16x32_bf16 a[24:27], a[196:199], a[232:235], a[24:27]\n" \
"ds_read_b64_tr_b16 a[244:245], v37 offset:40960\n" \
"v_mfma_f32_16x16x32_bf16 a[28:31], a[172:175], a[132:135], a[28:31]\n" \
"s_cmpk_lt_u32 s3, 3\n" \
"s_cselect_b32 s16, s16, s18\n" \
"v_mfma_f32_16x16x32_bf16 a[28:31], a[236:239], a[196:199], a[28:31]\n" \
"s_addk_i32 s84, 0x2000\n" \
"v_mfma_f32_16x16x32_bf16 a[20:23], a[164:167], a[132:135], a[20:23]\n" \
"buffer_load_dwordx4 v25, s[76:79], s84 offen offset:1024 lds\n" \
"v_mfma_f32_16x16x32_bf16 a[20:23], a[228:231], a[196:199], a[20:23]\n" \
"ds_read_b64_tr_b16 a[246:247], v37 offset:41984\n" \
"s_mul_i32 s19, s3, 0x9000\n" \
"v_mfma_f32_16x16x32_bf16 a[16:19], a[160:163], a[132:135], a[16:19]\n" \
"ds_read_b64_tr_b16 a[184:185], v38 offset:32768\n" \
"v_writelane_b32 v11, s16, 24\n" \
"v_mfma_f32_16x16x32_bf16 a[16:19], a[224:227], a[196:199], a[16:19]\n" \
"ds_read_b64_tr_b16 a[186:187], v38 offset:33792\n" \
"s_lshl_b32 s16, s3, 10\n" \
"v_mfma_f32_16x16x32_bf16 a[32:35], a[160:163], a[136:139], a[32:35]\n" \
"s_addk_i32 s84, 0x2000\n" \
"v_mfma_f32_16x16x32_bf16 a[32:35], a[224:227], a[200:203], a[32:35]\n" \
"buffer_load_dwordx4 v24, s[76:79], s84 offen offset:2048 lds\n" \
"v_mfma_f32_16x16x32_bf16 a[36:39], a[164:167], a[136:139], a[36:39]\n" \
"ds_read_b64_tr_b16 a[248:249], v38 offset:40960\n" \
"s_addk_i32 s16, 0x4800\n" \
"v_mfma_f32_16x16x32_bf16 a[36:39], a[228:231], a[200:203], a[36:39]\n" \
"ds_read_b64_tr_b16 a[250:251], v38 offset:41984\n" \
"v_writelane_b32 v11, s16, 25\n" \
"v_mfma_f32_16x16x32_bf16 a[44:47], a[172:175], a[136:139], a[44:47]\n" \
"ds_read_b64_tr_b16 a[188:189], v39 offset:32768\n" \
"s_add_i32 s16, s15, 1\n" \
"v_mfma_f32_16x16x32_bf16 a[44:47], a[236:239], a[200:203], a[44:47]\n" \
"ds_read_b64_tr_b16 a[190:191], v39 offset:33792\n" \
"s_and_b32 s16, s16, 3\n" \
"v_mfma_f32_16x16x32_bf16 a[40:43], a[136:139], a[168:171], a[40:43]\n" \
"s_add_u32 s89, s42, 0x0\n" \
"buffer_store_dwordx4 v[192:195], v20, s[80:83], s89 offen offset:2048 sc1\n" \
"v_mfma_f32_16x16x32_bf16 a[40:43], a[200:203], a[232:235], a[40:43]\n" \
"ds_read_b64_tr_b16 a[252:253], v39 offset:40960\n" \
"s_or_b32 s16, s16, s5\n" \
"v_mfma_f32_16x16x32_bf16 a[56:59], a[140:143], a[168:171], a[56:59]\n" \
"ds_read_b64_tr_b16 a[254:255], v39 offset:41984\n" \
"s_mul_i32 s18, s16, 0x24000\n" \
"v_mfma_f32_16x16x32_bf16 a[56:59], a[204:207], a[232:235], a[56:59]\n" \
"s_add_i32 s18, s18, s19\n" \
"s_xor_b32 s13, s13, 2\n" \
"v_mfma_f32_16x16x32_bf16 a[60:63], a[172:175], a[140:143], a[60:63]\n" \
"s_add_i32 s15, s15, -1\n" \
"v_writelane_b32 v11, s18, 27\n" \
"v_mfma_f32_16x16x32_bf16 a[60:63], a[236:239], a[204:207], a[60:63]\n" \
"s_addk_i32 s84, 0x2000\n" \
"v_mfma_f32_16x16x32_bf16 a[52:55], a[164:167], a[140:143], a[52:55]\n" \
"buffer_load_dwordx4 v25, s[76:79], s84 offen offset:3072 lds\n" \
"v_mfma_f32_16x16x32_bf16 a[52:55], a[228:231], a[204:207], a[52:55]\n" \
"s_mul_i32 s18, s13, 0x24000\n" \
"s_and_b32 s15, s15, 3\n" \
"v_mfma_f32_16x16x32_bf16 a[48:51], a[160:163], a[140:143], a[48:51]\n" \
"s_add_i32 s18, s18, s19\n" \
"s_or_b32 s15, s15, s5\n" \
"v_mfma_f32_16x16x32_bf16 a[48:51], a[224:227], a[204:207], a[48:51]\n" \
"v_writelane_b32 v11, s18, 28\n" \
"s_mul_i32 s18, s15, 0x24000\n" \
"s_waitcnt lgkmcnt(15)\n" \
"v_mfma_f32_16x16x32_bf16 a[64:67], a[164:167], v[248:251], a[64:67]\n" \
"s_add_i32 s18, s18, s19\n" \
"v_writelane_b32 v11, s18, 29\n" \
"v_mfma_f32_16x16x32_bf16 a[64:67], a[228:231], v[48:51], a[64:67]\n" \
"s_or_b32 s18, s9, s14\n" \
"s_mul_i32 s18, s18, 0x24800\n" \
"v_mfma_f32_16x16x32_bf16 a[80:83], a[172:175], v[248:251], a[80:83]\n" \
"s_lshl_b32 s16, s16, 12\n" \
"s_add_i32 s19, s18, 0x3004800\n" \
"v_mfma_f32_16x16x32_bf16 a[80:83], a[236:239], v[48:51], a[80:83]\n" \
"s_add_i32 s20, s19, s16\n" \
"v_writelane_b32 v11, s20, 30\n" \
"v_mfma_f32_16x16x32_bf16 a[68:71], v[248:251], a[168:171], a[68:71]\n" \
"s_lshl_b32 s13, s13, 12\n" \
"s_lshl_b32 s15, s15, 12\n" \
"v_mfma_f32_16x16x32_bf16 a[68:71], v[48:51], a[232:235], a[68:71]\n" \
"s_waitcnt vmcnt(11) lgkmcnt(0)\n" \
"s_barrier\n" \
"v_mfma_f32_16x16x32_bf16 v[52:55], a[176:179], a[128:131], v[52:55]\n" \
"s_add_u32 m0, s40, 0x8000\n" \
"s_add_u32 s85, s39, 0xa000\n" \
"v_mfma_f32_16x16x32_bf16 v[52:55], a[240:243], a[192:195], v[52:55]\n" \
"buffer_load_dwordx4 v20, s[80:83], s85 offen sc1 lds\n" \
"v_mfma_f32_16x16x32_bf16 v[56:59], a[180:183], a[128:131], v[56:59]\n" \
"ds_read_b64_tr_b16 a[160:161], v32 offset:0\n" \
"v_accvgpr_read_b32 v224, a120\n" \
"v_mfma_f32_16x16x32_bf16 v[56:59], a[244:247], a[192:195], v[56:59]\n" \
"ds_read_b64_tr_b16 a[162:163], v32 offset:512\n" \
"v_accvgpr_read_b32 v225, a121\n" \
"v_mfma_f32_16x16x32_bf16 v[60:63], a[184:187], a[128:131], v[60:63]\n" \
"s_add_u32 m0, s40, 0x9000\n" \
"s_add_u32 s85, s39, 0xb000\n" \
"v_mfma_f32_16x16x32_bf16 v[60:63], a[248:251], a[192:195], v[60:63]\n" \
"buffer_load_dwordx4 v20, s[80:83], s85 offen sc1 lds\n" \
"v_mfma_f32_16x16x32_bf16 v[64:67], a[188:191], a[128:131], v[64:67]\n" \
"ds_read_b64_tr_b16 a[224:225], v32 offset:4096\n" \
"v_accvgpr_read_b32 v226, a122\n" \
"v_mfma_f32_16x16x32_bf16 v[64:67], a[252:255], a[192:195], v[64:67]\n" \
"ds_read_b128 a[128:131], v28 offset:32768\n" \
"v_accvgpr_read_b32 v227, a123\n" \
"v_mfma_f32_16x16x32_bf16 v[80:83], a[188:191], a[132:135], v[80:83]\n" \
"ds_read_b128 a[192:195], v29 offset:32768\n" \
"v_accvgpr_read_b32 v180, a124\n" \
"v_mfma_f32_16x16x32_bf16 v[80:83], a[252:255], a[196:199], v[80:83]\n" \
"s_add_u32 s88, s42, 0x0\n" \
"buffer_store_dwordx4 v[196:199], v20, s[80:83], s88 offen offset:3072 sc1\n" \
"v_mfma_f32_16x16x32_bf16 v[76:79], a[184:187], a[132:135], v[76:79]\n" \
"ds_read_b64_tr_b16 a[226:227], v32 offset:4608\n" \
"v_accvgpr_read_b32 v181, a125\n" \
"v_mfma_f32_16x16x32_bf16 v[76:79], a[248:251], a[196:199], v[76:79]\n" \
"ds_read_b64_tr_b16 a[164:165], v33 offset:0\n" \
"v_accvgpr_read_b32 v182, a126\n" \
"v_mfma_f32_16x16x32_bf16 v[72:75], a[180:183], a[132:135], v[72:75]\n" \
"s_add_u32 m0, s40, 0xb000\n" \
"s_movk_i32 s84, 0x280\n" \
"v_mfma_f32_16x16x32_bf16 v[72:75], a[244:247], a[196:199], v[72:75]\n" \
"buffer_load_dwordx4 v21, s[76:79], s84 offen offset:0 lds\n" \
"v_mfma_f32_16x16x32_bf16 v[68:71], a[176:179], a[132:135], v[68:71]\n" \
"ds_read_b64_tr_b16 a[166:167], v33 offset:512\n" \
"v_accvgpr_read_b32 v183, a127\n" \
"v_mfma_f32_16x16x32_bf16 v[68:71], a[240:243], a[196:199], v[68:71]\n" \
"ds_read_b128 a[132:135], v28 offset:34816\n" \
"v_cvt_pk_bf16_f32 v224, v224, v225\n" \
"v_mfma_f32_16x16x32_bf16 v[84:87], a[176:179], a[136:139], v[84:87]\n" \
"ds_read_b128 a[196:199], v29 offset:34816\n" \
"v_cvt_pk_bf16_f32 v225, v226, v227\n" \
"v_mfma_f32_16x16x32_bf16 v[84:87], a[240:243], a[200:203], v[84:87]\n" \
"s_add_u32 m0, s31, 0x8000\n" \
"s_mov_b32 s84, 0x280000\n" \
"v_mfma_f32_16x16x32_bf16 v[88:91], a[180:183], a[136:139], v[88:91]\n" \
"buffer_load_dwordx4 v26, s[68:71], s84 offen offset:0 lds\n" \
"v_mfma_f32_16x16x32_bf16 v[88:91], a[244:247], a[200:203], v[88:91]\n" \
"ds_read_b64_tr_b16 a[228:229], v33 offset:4096\n" \
"v_cvt_pk_bf16_f32 v226, v180, v181\n" \
"v_mfma_f32_16x16x32_bf16 v[92:95], a[184:187], a[136:139], v[92:95]\n" \
"ds_read_b64_tr_b16 a[230:231], v33 offset:4608\n" \
"v_cvt_pk_bf16_f32 v227, v182, v183\n" \
"v_mfma_f32_16x16x32_bf16 v[92:95], a[248:251], a[200:203], v[92:95]\n" \
"s_addk_i32 s84, 0x7c00\n" \
"v_mfma_f32_16x16x32_bf16 v[96:99], a[188:191], a[136:139], v[96:99]\n" \
"buffer_load_dwordx4 v26, s[68:71], s84 offen offset:1024 lds\n" \
"v_mfma_f32_16x16x32_bf16 v[96:99], a[252:255], a[200:203], v[96:99]\n" \
"ds_read_b128 a[136:139], v28 offset:36864\n" \
"v_accvgpr_read_b32 v220, a112\n" \
"v_mfma_f32_16x16x32_bf16 v[112:115], a[188:191], a[140:143], v[112:115]\n" \
"ds_read_b128 a[200:203], v29 offset:36864\n" \
"v_accvgpr_read_b32 v221, a113\n" \
"v_mfma_f32_16x16x32_bf16 v[112:115], a[252:255], a[204:207], v[112:115]\n" \
"s_addk_i32 s84, 0x7c00\n" \
"v_mfma_f32_16x16x32_bf16 v[108:111], a[184:187], a[140:143], v[108:111]\n" \
"buffer_load_dwordx4 v27, s[68:71], s84 offen offset:2048 lds\n" \
"v_mfma_f32_16x16x32_bf16 v[108:111], a[248:251], a[204:207], v[108:111]\n" \
"ds_read_b64_tr_b16 a[172:173], v35 offset:0\n" \
"v_accvgpr_read_b32 v222, a114\n" \
"v_mfma_f32_16x16x32_bf16 v[104:107], a[180:183], a[140:143], v[104:107]\n" \
"ds_read_b64_tr_b16 a[174:175], v35 offset:512\n" \
"v_accvgpr_read_b32 v223, a115\n" \
"v_mfma_f32_16x16x32_bf16 v[104:107], a[244:247], a[204:207], v[104:107]\n" \
"ds_read_b64_tr_b16 a[236:237], v35 offset:4096\n" \
"v_accvgpr_read_b32 v180, a116\n" \
"v_mfma_f32_16x16x32_bf16 v[100:103], a[176:179], a[140:143], v[100:103]\n" \
"v_accvgpr_read_b32 v181, a117\n" \
"v_accvgpr_read_b32 v182, a118\n" \
"v_mfma_f32_16x16x32_bf16 v[100:103], a[240:243], a[204:207], v[100:103]\n" \
"ds_read_b128 a[140:143], v28 offset:38912\n" \
"v_accvgpr_read_b32 v183, a119\n" \
"v_mfma_f32_16x16x32_bf16 v[116:119], a[176:179], a[144:147], v[116:119]\n" \
"ds_read_b128 a[204:207], v29 offset:38912\n" \
"v_cvt_pk_bf16_f32 v220, v220, v221\n" \
"v_mfma_f32_16x16x32_bf16 v[116:119], a[240:243], a[208:211], v[116:119]\n" \
"s_addk_i32 s84, 0x7c00\n" \
"v_mfma_f32_16x16x32_bf16 v[120:123], a[180:183], a[144:147], v[120:123]\n" \
"buffer_load_dwordx4 v27, s[68:71], s84 offen offset:3072 lds\n" \
"v_mfma_f32_16x16x32_bf16 v[120:123], a[244:247], a[208:211], v[120:123]\n" \
"s_add_u32 m0, s30, 0x0\n" \
"s_movk_i32 s84, 0x300\n" \
"s_nop 0\n" \
"v_mfma_f32_16x16x32_bf16 v[124:127], a[184:187], a[144:147], v[124:127]\n" \
"buffer_load_dwordx4 v24, s[76:79], s84 offen offset:0 lds\n" \
"v_mfma_f32_16x16x32_bf16 v[124:127], a[248:251], a[208:211], v[124:127]\n" \
"ds_read_b64_tr_b16 a[238:239], v35 offset:4608\n" \
"v_cvt_pk_bf16_f32 v221, v222, v223\n" \
"v_mfma_f32_16x16x32_bf16 v[128:131], a[188:191], a[144:147], v[128:131]\n" \
"s_add_u32 s89, s42, 0x1000\n" \
"buffer_store_dwordx4 a[84:87], v20, s[80:83], s89 offen offset:3072 sc1\n" \
"v_mfma_f32_16x16x32_bf16 v[128:131], a[252:255], a[208:211], v[128:131]\n" \
"ds_read_b128 a[144:147], v28 offset:40960\n" \
"v_cvt_pk_bf16_f32 v222, v180, v181\n" \
"v_mfma_f32_16x16x32_bf16 v[144:147], a[188:191], a[148:151], v[144:147]\n" \
"ds_read_b128 a[208:211], v29 offset:40960\n" \
"v_cvt_pk_bf16_f32 v223, v182, v183\n" \
"v_mfma_f32_16x16x32_bf16 v[144:147], a[252:255], a[212:215], v[144:147]\n" \
"ds_read_b64_tr_b16 a[168:169], v34 offset:0\n" \
"v_accvgpr_read_b32 v216, a104\n" \
"v_mfma_f32_16x16x32_bf16 v[140:143], a[184:187], a[148:151], v[140:143]\n" \
"ds_read_b64_tr_b16 a[170:171], v34 offset:512\n" \
"v_accvgpr_read_b32 v217, a105\n" \
"v_mfma_f32_16x16x32_bf16 v[140:143], a[248:251], a[212:215], v[140:143]\n" \
"s_movk_i32 s84, 0x2300\n" \
"s_nop 0\n" \
"v_mfma_f32_16x16x32_bf16 v[136:139], a[180:183], a[148:151], v[136:139]\n" \
"buffer_load_dwordx4 v25, s[76:79], s84 offen offset:1024 lds\n" \
"v_mfma_f32_16x16x32_bf16 v[136:139], a[244:247], a[212:215], v[136:139]\n" \
"ds_read_b64_tr_b16 a[232:233], v34 offset:4096\n" \
"v_accvgpr_read_b32 v218, a106\n" \
"v_mfma_f32_16x16x32_bf16 v[132:135], a[176:179], a[148:151], v[132:135]\n" \
"ds_read_b64_tr_b16 a[234:235], v34 offset:4608\n" \
"v_accvgpr_read_b32 v219, a107\n" \
"v_mfma_f32_16x16x32_bf16 v[132:135], a[240:243], a[212:215], v[132:135]\n" \
"ds_read_b128 a[148:151], v28 offset:43008\n" \
"v_accvgpr_read_b32 v180, a108\n" \
"v_mfma_f32_16x16x32_bf16 v[148:151], a[176:179], a[152:155], v[148:151]\n" \
"ds_read_b128 a[212:215], v29 offset:43008\n" \
"v_accvgpr_read_b32 v181, a109\n" \
"v_mfma_f32_16x16x32_bf16 v[148:151], a[240:243], a[216:219], v[148:151]\n" \
"s_movk_i32 s84, 0x4300\n" \
"v_mfma_f32_16x16x32_bf16 v[152:155], a[180:183], a[152:155], v[152:155]\n" \
"buffer_load_dwordx4 v24, s[76:79], s84 offen offset:2048 lds\n" \
"v_mfma_f32_16x16x32_bf16 v[152:155], a[244:247], a[216:219], v[152:155]\n" \
"s_movk_i32 s84, 0x6300\n" \
"v_mfma_f32_16x16x32_bf16 v[156:159], a[184:187], a[152:155], v[156:159]\n" \
"buffer_load_dwordx4 v25, s[76:79], s84 offen offset:3072 lds\n" \
"v_mfma_f32_16x16x32_bf16 v[156:159], a[248:251], a[216:219], v[156:159]\n" \
"ds_read_b128 v[248:251], v22 offset:0\n" \
"v_accvgpr_read_b32 v182, a110\n" \
"v_mfma_f32_16x16x32_bf16 v[160:163], a[188:191], a[152:155], v[160:163]\n" \
"ds_read_b128 v[48:51], v23 offset:0\n" \
"v_accvgpr_read_b32 v183, a111\n" \
"v_mfma_f32_16x16x32_bf16 v[160:163], a[252:255], a[216:219], v[160:163]\n" \
"ds_read_b128 a[152:155], v28 offset:45056\n" \
"v_cvt_pk_bf16_f32 v216, v216, v217\n" \
"v_mfma_f32_16x16x32_bf16 v[176:179], a[188:191], a[156:159], v[176:179]\n" \
"ds_read_b128 a[216:219], v29 offset:45056\n" \
"v_cvt_pk_bf16_f32 v217, v218, v219\n" \
"v_mfma_f32_16x16x32_bf16 v[176:179], a[252:255], a[220:223], v[176:179]\n" \
"v_cvt_pk_bf16_f32 v218, v180, v181\n" \
"v_cvt_pk_bf16_f32 v219, v182, v183\n" \
"v_mfma_f32_16x16x32_bf16 v[172:175], a[184:187], a[156:159], v[172:175]\n" \
"v_accvgpr_read_b32 v192, a96\n" \
"v_accvgpr_read_b32 v193, a97\n" \
"v_mfma_f32_16x16x32_bf16 v[172:175], a[248:251], a[220:223], v[172:175]\n" \
"v_accvgpr_read_b32 v194, a98\n" \
"v_accvgpr_read_b32 v195, a99\n" \
"v_mfma_f32_16x16x32_bf16 v[168:171], a[180:183], a[156:159], v[168:171]\n" \
"v_accvgpr_read_b32 v180, a100\n" \
"v_accvgpr_read_b32 v181, a101\n" \
"v_mfma_f32_16x16x32_bf16 v[168:171], a[244:247], a[220:223], v[168:171]\n" \
"v_accvgpr_read_b32 v182, a102\n" \
"v_accvgpr_read_b32 v183, a103\n" \
"v_mfma_f32_16x16x32_bf16 v[164:167], a[176:179], a[156:159], v[164:167]\n" \
"v_cvt_pk_bf16_f32 v192, v192, v193\n" \
"v_cvt_pk_bf16_f32 v193, v194, v195\n" \
"v_mfma_f32_16x16x32_bf16 v[164:167], a[240:243], a[220:223], v[164:167]\n" \
"ds_read_b128 a[156:159], v28 offset:47104\n" \
"ds_read_b128 a[220:223], v29 offset:47104\n" \
"s_waitcnt lgkmcnt(15)\n" \
"v_mfma_f32_16x16x32_bf16 a[0:3], a[160:163], a[128:131], a[0:3]\n" \
"s_add_u32 m0, s30, 0x1000\n" \
"s_addk_i32 s84, 0x3000\n" \
"v_mfma_f32_16x16x32_bf16 a[0:3], a[224:227], a[192:195], a[0:3]\n" \
"buffer_load_dwordx4 v24, s[76:79], s84 offen offset:0 lds\n" \
"v_mfma_f32_16x16x32_bf16 a[4:7], a[164:167], a[128:131], a[4:7]\n" \
"ds_read_b64_tr_b16 a[176:177], v36 offset:0\n" \
"v_cvt_pk_bf16_f32 v194, v180, v181\n" \
"v_mfma_f32_16x16x32_bf16 a[4:7], a[228:231], a[192:195], a[4:7]\n" \
"ds_read_b64_tr_b16 a[178:179], v36 offset:1024\n" \
"v_cvt_pk_bf16_f32 v195, v182, v183\n" \
"v_mfma_f32_16x16x32_bf16 a[12:15], a[172:175], a[128:131], a[12:15]\n" \
"ds_read_b64_tr_b16 a[240:241], v36 offset:8192\n" \
"v_accvgpr_read_b32 v188, a88\n" \
"s_waitcnt lgkmcnt(15)\n" \
"v_mfma_f32_16x16x32_bf16 a[12:15], a[236:239], a[192:195], a[12:15]\n" \
"s_add_u32 s88, s42, 0x1000\n" \
"buffer_store_dwordx4 v[252:255], v20, s[80:83], s88 offen offset:2048 sc1\n" \
"s_waitcnt lgkmcnt(13)\n" \
"v_mfma_f32_16x16x32_bf16 a[8:11], a[128:131], a[168:171], a[8:11]\n" \
"ds_read_b64_tr_b16 a[242:243], v36 offset:9216\n" \
"v_accvgpr_read_b32 v189, a89\n" \
"s_waitcnt lgkmcnt(12)\n" \
"s_nop 0\n" \
"v_mfma_f32_16x16x32_bf16 a[8:11], a[192:195], a[232:235], a[8:11]\n" \
"ds_read_b64_tr_b16 a[180:181], v37 offset:0\n" \
"v_accvgpr_read_b32 v190, a90\n" \
"v_mfma_f32_16x16x32_bf16 a[24:27], a[132:135], a[168:171], a[24:27]\n" \
"ds_read_b64_tr_b16 a[182:183], v37 offset:1024\n" \
"v_accvgpr_read_b32 v191, a91\n" \
"v_mfma_f32_16x16x32_bf16 a[24:27], a[196:199], a[232:235], a[24:27]\n" \
"ds_read_b64_tr_b16 a[244:245], v37 offset:8192\n" \
"v_accvgpr_read_b32 v180, a92\n" \
"v_mfma_f32_16x16x32_bf16 a[28:31], a[172:175], a[132:135], a[28:31]\n" \
"v_accvgpr_read_b32 v181, a93\n" \
"v_accvgpr_read_b32 v182, a94\n" \
"v_mfma_f32_16x16x32_bf16 a[28:31], a[236:239], a[196:199], a[28:31]\n" \
"s_addk_i32 s84, 0x2000\n" \
"s_nop 0\n" \
"v_mfma_f32_16x16x32_bf16 a[20:23], a[164:167], a[132:135], a[20:23]\n" \
"buffer_load_dwordx4 v25, s[76:79], s84 offen offset:1024 lds\n" \
"v_mfma_f32_16x16x32_bf16 a[20:23], a[228:231], a[196:199], a[20:23]\n" \
"ds_read_b64_tr_b16 a[246:247], v37 offset:9216\n" \
"v_accvgpr_read_b32 v183, a95\n" \
"v_mfma_f32_16x16x32_bf16 a[16:19], a[160:163], a[132:135], a[16:19]\n" \
"ds_read_b64_tr_b16 a[184:185], v38 offset:0\n" \
"v_cvt_pk_bf16_f32 v188, v188, v189\n" \
"v_mfma_f32_16x16x32_bf16 a[16:19], a[224:227], a[196:199], a[16:19]\n" \
"ds_read_b64_tr_b16 a[186:187], v38 offset:1024\n" \
"v_cvt_pk_bf16_f32 v189, v190, v191\n" \
"v_mfma_f32_16x16x32_bf16 a[32:35], a[160:163], a[136:139], a[32:35]\n" \
"s_addk_i32 s84, 0x2000\n" \
"s_nop 0\n" \
"v_mfma_f32_16x16x32_bf16 a[32:35], a[224:227], a[200:203], a[32:35]\n" \
"buffer_load_dwordx4 v24, s[76:79], s84 offen offset:2048 lds\n" \
"v_mfma_f32_16x16x32_bf16 a[36:39], a[164:167], a[136:139], a[36:39]\n" \
"ds_read_b64_tr_b16 a[248:249], v38 offset:8192\n" \
"v_cvt_pk_bf16_f32 v190, v180, v181\n" \
"v_mfma_f32_16x16x32_bf16 a[36:39], a[228:231], a[200:203], a[36:39]\n" \
"ds_read_b64_tr_b16 a[250:251], v38 offset:9216\n" \
"v_cvt_pk_bf16_f32 v191, v182, v183\n" \
"v_mfma_f32_16x16x32_bf16 a[44:47], a[172:175], a[136:139], a[44:47]\n" \
"ds_read_b64_tr_b16 a[188:189], v39 offset:0\n" \
"v_accvgpr_read_b32 v184, a72\n" \
"v_mfma_f32_16x16x32_bf16 a[44:47], a[236:239], a[200:203], a[44:47]\n" \
"ds_read_b64_tr_b16 a[190:191], v39 offset:1024\n" \
"v_accvgpr_read_b32 v185, a73\n" \
"v_mfma_f32_16x16x32_bf16 a[40:43], a[136:139], a[168:171], a[40:43]\n" \
"s_add_u32 s89, s42, 0x2000\n" \
"buffer_store_dwordx4 v[200:203], v20, s[80:83], s89 offen offset:2048 sc1\n" \
"v_mfma_f32_16x16x32_bf16 a[40:43], a[200:203], a[232:235], a[40:43]\n" \
"ds_read_b64_tr_b16 a[252:253], v39 offset:8192\n" \
"v_accvgpr_read_b32 v186, a74\n" \
"v_mfma_f32_16x16x32_bf16 a[56:59], a[140:143], a[168:171], a[56:59]\n" \
"ds_read_b64_tr_b16 a[254:255], v39 offset:9216\n" \
"v_accvgpr_read_b32 v187, a75\n" \
".fill (L_pb + 28384 - .) / 4, 4, 0xBF800000\n" \
"L_pp7:\n" \
"s_cbranch_execz L_pret\n" \
"v_mfma_f32_16x16x32_bf16 a[56:59], a[204:207], a[232:235], a[56:59]\n" \
"v_accvgpr_read_b32 v180, a76\n" \
"v_accvgpr_read_b32 v181, a77\n" \
"v_mfma_f32_16x16x32_bf16 a[60:63], a[172:175], a[140:143], a[60:63]\n" \
"v_accvgpr_read_b32 v182, a78\n" \
"v_accvgpr_read_b32 v183, a79\n" \
"v_mfma_f32_16x16x32_bf16 a[60:63], a[236:239], a[204:207], a[60:63]\n" \
"s_addk_i32 s84, 0x2000\n" \
"v_mfma_f32_16x16x32_bf16 a[52:55], a[164:167], a[140:143], a[52:55]\n" \
"buffer_load_dwordx4 v25, s[76:79], s84 offen offset:3072 lds\n" \
"v_mfma_f32_16x16x32_bf16 a[52:55], a[228:231], a[204:207], a[52:55]\n" \
"v_cvt_pk_bf16_f32 v184, v184, v185\n" \
"v_cvt_pk_bf16_f32 v185, v186, v187\n" \
"v_mfma_f32_16x16x32_bf16 a[48:51], a[160:163], a[140:143], a[48:51]\n" \
"v_cvt_pk_bf16_f32 v186, v180, v181\n" \
"v_cvt_pk_bf16_f32 v187, v182, v183\n" \
"v_mfma_f32_16x16x32_bf16 a[48:51], a[224:227], a[204:207], a[48:51]\n" \
"s_sub_i32 s14, s9, s14\n" \
"s_add_i32 s20, s19, s13\n" \
"s_waitcnt lgkmcnt(15)\n" \
"v_mfma_f32_16x16x32_bf16 a[64:67], a[164:167], v[248:251], a[64:67]\n" \
"v_writelane_b32 v11, s20, 31\n" \
"s_add_i32 s19, s19, s15\n" \
"v_mfma_f32_16x16x32_bf16 a[64:67], a[228:231], v[48:51], a[64:67]\n" \
"s_mul_i32 s14, s14, 0x24800\n" \
"v_writelane_b32 v11, s19, 32\n" \
"v_mfma_f32_16x16x32_bf16 a[80:83], a[172:175], v[248:251], a[80:83]\n" \
"s_add_i32 s19, s14, 0x4120800\n" \
"s_add_i32 s16, s19, s16\n" \
"v_mfma_f32_16x16x32_bf16 a[80:83], a[236:239], v[48:51], a[80:83]\n" \
"v_writelane_b32 v11, s16, 33\n" \
"s_add_i32 s13, s19, s13\n" \
"v_mfma_f32_16x16x32_bf16 a[68:71], v[248:251], a[168:171], a[68:71]\n" \
"v_writelane_b32 v11, s13, 34\n" \
"s_add_i32 s19, s19, s15\n" \
"v_mfma_f32_16x16x32_bf16 a[68:71], v[48:51], a[232:235], a[68:71]\n" \
"s_waitcnt vmcnt(11) lgkmcnt(0)\n" \
"s_barrier\n" \
"v_mov_b32_e32 v45, s62\n" \
"s_mov_b64 exec, 1\n" \
"buffer_store_dword v45, off, s[80:83], s37 offset:0 sc1\n" \
"s_mov_b64 exec, -1\n" \
"v_xor_b32_e32 v28, 0x20000, v28\n" \
"v_xor_b32_e32 v29, 0x20000, v29\n" \
"v_mfma_f32_16x16x32_bf16 v[52:55], a[176:179], a[128:131], v[52:55]\n" \
"s_add_u32 m0, s40, 0x0\n" \
"s_add_u32 s85, s39, 0xc000\n" \
"v_mfma_f32_16x16x32_bf16 v[52:55], a[240:243], a[192:195], v[52:55]\n" \
"buffer_load_dwordx4 v20, s[80:83], s85 offen sc1 lds\n" \
"v_mfma_f32_16x16x32_bf16 v[56:59], a[180:183], a[128:131], v[56:59]\n" \
"ds_read_b64_tr_b16 a[160:161], v32 offset:32768\n" \
"s_or_b32 s13, s18, 0x3000000\n" \
"v_mfma_f32_16x16x32_bf16 v[56:59], a[244:247], a[192:195], v[56:59]\n" \
"ds_read_b64_tr_b16 a[162:163], v32 offset:33280\n" \
"s_lshl_b32 s15, s6, 11\n" \
"v_mfma_f32_16x16x32_bf16 v[60:63], a[184:187], a[128:131], v[60:63]\n" \
"s_add_u32 m0, s40, 0x1000\n" \
"s_add_u32 s85, s39, 0xd000\n" \
"v_mfma_f32_16x16x32_bf16 v[60:63], a[248:251], a[192:195], v[60:63]\n" \
"buffer_load_dwordx4 v20, s[80:83], s85 offen sc1 lds\n" \
"v_mfma_f32_16x16x32_bf16 v[64:67], a[188:191], a[128:131], v[64:67]\n" \
"ds_read_b64_tr_b16 a[224:225], v32 offset:36864\n" \
"v_writelane_b32 v11, s19, 35\n" \
"v_mfma_f32_16x16x32_bf16 v[64:67], a[252:255], a[192:195], v[64:67]\n" \
"ds_read_b128 a[128:131], v28 offset:0\n" \
"s_add_i32 s16, s13, s15\n" \
"v_mfma_f32_16x16x32_bf16 v[80:83], a[188:191], a[132:135], v[80:83]\n" \
"ds_read_b128 a[192:195], v29 offset:0\n" \
"v_writelane_b32 v11, s16, 36\n" \
"v_mfma_f32_16x16x32_bf16 v[80:83], a[252:255], a[196:199], v[80:83]\n" \
"s_add_u32 m0, s40, 0x3000\n" \
"s_movk_i32 s84, 0x300\n" \
"v_mfma_f32_16x16x32_bf16 v[76:79], a[184:187], a[132:135], v[76:79]\n" \
"buffer_load_dwordx4 v21, s[76:79], s84 offen offset:0 lds\n" \
"v_mfma_f32_16x16x32_bf16 v[76:79], a[248:251], a[196:199], v[76:79]\n" \
"ds_read_b64_tr_b16 a[226:227], v32 offset:37376\n" \
"s_add_i32 s16, s14, 0x411c000\n" \
"v_mfma_f32_16x16x32_bf16 v[72:75], a[180:183], a[132:135], v[72:75]\n" \
"ds_read_b64_tr_b16 a[164:165], v33 offset:32768\n" \
"s_add_i32 s19, s16, s15\n" \
"s_nop 0\n" \
"v_mfma_f32_16x16x32_bf16 v[72:75], a[244:247], a[196:199], v[72:75]\n" \
"v_writelane_b32 v11, s19, 37\n" \
"s_add_i32 s18, s18, 0x3004000\n" \
"v_mfma_f32_16x16x32_bf16 v[68:71], a[176:179], a[132:135], v[68:71]\n" \
"ds_read_b64_tr_b16 a[166:167], v33 offset:33280\n" \
"v_writelane_b32 v11, s18, 38\n" \
"v_mfma_f32_16x16x32_bf16 v[68:71], a[240:243], a[196:199], v[68:71]\n" \
"ds_read_b128 a[132:135], v28 offset:2048\n" \
"s_add_i32 s14, s14, 0x4120000\n" \
"v_mfma_f32_16x16x32_bf16 v[84:87], a[176:179], a[136:139], v[84:87]\n" \
"ds_read_b128 a[196:199], v29 offset:2048\n" \
"v_writelane_b32 v11, s14, 39\n" \
"v_mfma_f32_16x16x32_bf16 v[84:87], a[240:243], a[200:203], v[84:87]\n" \
"s_add_u32 m0, s31, 0x0\n" \
"s_mov_b32 s84, 0x300000\n" \
"v_mfma_f32_16x16x32_bf16 v[88:91], a[180:183], a[136:139], v[88:91]\n" \
"buffer_load_dwordx4 v26, s[68:71], s84 offen offset:0 lds\n" \
"v_mfma_f32_16x16x32_bf16 v[88:91], a[244:247], a[200:203], v[88:91]\n" \
"s_add_u32 s88, s42, 0x2000\n" \
"buffer_store_dwordx4 v[204:207], v20, s[80:83], s88 offen offset:3072 sc1\n" \
"v_mfma_f32_16x16x32_bf16 v[92:95], a[184:187], a[136:139], v[92:95]\n" \
"ds_read_b64_tr_b16 a[228:229], v33 offset:36864\n" \
"s_lshl_b32 s14, s4, 2\n" \
"v_mfma_f32_16x16x32_bf16 v[92:95], a[248:251], a[200:203], v[92:95]\n" \
"ds_read_b64_tr_b16 a[230:231], v33 offset:37376\n" \
"s_or_b32 s8, s14, s8\n" \
"v_mfma_f32_16x16x32_bf16 v[96:99], a[188:191], a[136:139], v[96:99]\n" \
"s_mul_i32 s8, s8, 0x90000\n" \
"s_lshl_b32 s14, s3, 14\n" \
"v_mfma_f32_16x16x32_bf16 v[96:99], a[252:255], a[200:203], v[96:99]\n" \
"ds_read_b128 a[136:139], v28 offset:4096\n" \
"s_add_i32 s8, s8, s14\n" \
"v_mfma_f32_16x16x32_bf16 v[112:115], a[188:191], a[140:143], v[112:115]\n" \
"ds_read_b128 a[200:203], v29 offset:4096\n" \
"s_lshl_b32 s9, s9, 4\n" \
"v_mfma_f32_16x16x32_bf16 v[112:115], a[252:255], a[204:207], v[112:115]\n" \
"s_addk_i32 s84, 0x7c00\n" \
"v_mfma_f32_16x16x32_bf16 v[108:111], a[184:187], a[140:143], v[108:111]\n" \
"buffer_load_dwordx4 v26, s[68:71], s84 offen offset:1024 lds\n" \
"v_mfma_f32_16x16x32_bf16 v[108:111], a[248:251], a[204:207], v[108:111]\n" \
"ds_read_b64_tr_b16 a[172:173], v35 offset:32768\n" \
"s_lshl_b32 s14, s4, 7\n" \
"v_mfma_f32_16x16x32_bf16 v[104:107], a[180:183], a[140:143], v[104:107]\n" \
"s_addk_i32 s84, 0x7c00\n" \
"v_mfma_f32_16x16x32_bf16 v[104:107], a[244:247], a[204:207], v[104:107]\n" \
"buffer_load_dwordx4 v27, s[68:71], s84 offen offset:2048 lds\n" \
"v_mfma_f32_16x16x32_bf16 v[100:103], a[176:179], a[140:143], v[100:103]\n" \
"ds_read_b64_tr_b16 a[174:175], v35 offset:33280\n" \
"s_or_b32 s18, s9, s14\n" \
"v_mfma_f32_16x16x32_bf16 v[100:103], a[240:243], a[204:207], v[100:103]\n" \
"ds_read_b128 a[140:143], v28 offset:6144\n" \
"s_sub_i32 s9, s9, s14\n" \
"v_mfma_f32_16x16x32_bf16 v[116:119], a[176:179], a[144:147], v[116:119]\n" \
"ds_read_b128 a[204:207], v29 offset:6144\n" \
"s_mulk_i32 s18, 0x480\n" \
"v_mfma_f32_16x16x32_bf16 v[116:119], a[240:243], a[208:211], v[116:119]\n" \
"s_addk_i32 s84, 0x7c00\n" \
"v_mfma_f32_16x16x32_bf16 v[120:123], a[180:183], a[144:147], v[120:123]\n" \
"buffer_load_dwordx4 v27, s[68:71], s84 offen offset:3072 lds\n" \
"v_mfma_f32_16x16x32_bf16 v[120:123], a[244:247], a[208:211], v[120:123]\n" \
"ds_read_b64_tr_b16 a[236:237], v35 offset:36864\n" \
"s_mulk_i32 s9, 0x480\n" \
"v_mfma_f32_16x16x32_bf16 v[124:127], a[184:187], a[144:147], v[124:127]\n" \
"ds_read_b64_tr_b16 a[238:239], v35 offset:37376\n" \
"s_or_b32 s14, s18, s12\n" \
"v_mfma_f32_16x16x32_bf16 v[124:127], a[248:251], a[208:211], v[124:127]\n" \
"s_add_u32 m0, s30, 0x8000\n" \
"s_movk_i32 s84, 0x380\n" \
"v_mfma_f32_16x16x32_bf16 v[128:131], a[188:191], a[144:147], v[128:131]\n" \
"buffer_load_dwordx4 v24, s[76:79], s84 offen offset:0 lds\n" \
"v_mfma_f32_16x16x32_bf16 v[128:131], a[252:255], a[208:211], v[128:131]\n" \
"ds_read_b128 a[144:147], v28 offset:8192\n" \
"s_or_b32 s9, s9, s12\n" \
"v_mfma_f32_16x16x32_bf16 v[144:147], a[188:191], a[148:151], v[144:147]\n" \
"ds_read_b128 a[208:211], v29 offset:8192\n" \
"s_and_b32 s12, s2, 1\n" \
"v_mfma_f32_16x16x32_bf16 v[144:147], a[252:255], a[212:215], v[144:147]\n" \
"ds_read_b64_tr_b16 a[168:169], v34 offset:32768\n" \
"v_mfma_f32_16x16x32_bf16 v[140:143], a[184:187], a[148:151], v[140:143]\n" \
"s_movk_i32 s84, 0x2380\n" \
"s_nop 0\n" \
"v_mfma_f32_16x16x32_bf16 v[140:143], a[248:251], a[212:215], v[140:143]\n" \
"buffer_load_dwordx4 v25, s[76:79], s84 offen offset:1024 lds\n" \
"v_mfma_f32_16x16x32_bf16 v[136:139], a[180:183], a[148:151], v[136:139]\n" \
"ds_read_b64_tr_b16 a[170:171], v34 offset:33280\n" \
"v_mfma_f32_16x16x32_bf16 v[136:139], a[244:247], a[212:215], v[136:139]\n" \
"ds_read_b64_tr_b16 a[232:233], v34 offset:36864\n" \
"v_mfma_f32_16x16x32_bf16 v[132:135], a[176:179], a[148:151], v[132:135]\n" \
"ds_read_b64_tr_b16 a[234:235], v34 offset:37376\n" \
"v_mfma_f32_16x16x32_bf16 v[132:135], a[240:243], a[212:215], v[132:135]\n" \
"ds_read_b128 a[148:151], v28 offset:10240\n" \
"v_mfma_f32_16x16x32_bf16 v[148:151], a[176:179], a[152:155], v[148:151]\n" \
"ds_read_b128 a[212:215], v29 offset:10240\n" \
"v_mfma_f32_16x16x32_bf16 v[148:151], a[240:243], a[216:219], v[148:151]\n" \
"s_movk_i32 s84, 0x4380\n" \
"v_mfma_f32_16x16x32_bf16 v[152:155], a[180:183], a[152:155], v[152:155]\n" \
"buffer_load_dwordx4 v24, s[76:79], s84 offen offset:2048 lds\n" \
"v_mfma_f32_16x16x32_bf16 v[152:155], a[244:247], a[216:219], v[152:155]\n" \
"s_movk_i32 s84, 0x6380\n" \
"v_mfma_f32_16x16x32_bf16 v[156:159], a[184:187], a[152:155], v[156:159]\n" \
"buffer_load_dwordx4 v25, s[76:79], s84 offen offset:3072 lds\n" \
"v_mfma_f32_16x16x32_bf16 v[156:159], a[248:251], a[216:219], v[156:159]\n" \
"ds_read_b128 v[248:251], v22 offset:32768\n" \
"v_mfma_f32_16x16x32_bf16 v[160:163], a[188:191], a[152:155], v[160:163]\n" \
"ds_read_b128 v[48:51], v23 offset:32768\n" \
"v_mfma_f32_16x16x32_bf16 v[160:163], a[252:255], a[216:219], v[160:163]\n" \
"ds_read_b128 a[152:155], v28 offset:12288\n" \
"v_mfma_f32_16x16x32_bf16 v[176:179], a[188:191], a[156:159], v[176:179]\n" \
"ds_read_b128 a[216:219], v29 offset:12288\n" \
"v_mfma_f32_16x16x32_bf16 v[176:179], a[252:255], a[220:223], v[176:179]\n" \
"s_cmp_lg_u32 s12, 0\n" \
"s_cselect_b32 s13, s16, s13\n" \
"v_mfma_f32_16x16x32_bf16 v[172:175], a[184:187], a[156:159], v[172:175]\n" \
"s_bfe_u32 s16, s2, 0x20001\n" \
"s_addk_i32 s13, 0x4000\n" \
"v_mfma_f32_16x16x32_bf16 v[172:175], a[248:251], a[220:223], v[172:175]\n" \
"s_xor_b32 s16, s16, s3\n" \
"v_mfma_f32_16x16x32_bf16 v[168:171], a[180:183], a[156:159], v[168:171]\n" \
"s_cmp_lg_u32 s16, 0\n" \
"s_cselect_b32 s13, s7, s13\n" \
"v_mfma_f32_16x16x32_bf16 v[168:171], a[244:247], a[220:223], v[168:171]\n" \
"s_lshl_b32 s6, s6, 16\n" \
"v_writelane_b32 v11, s13, 40\n" \
"v_mfma_f32_16x16x32_bf16 v[164:167], a[176:179], a[156:159], v[164:167]\n" \
"s_add_i32 s13, s8, 0x4240000\n" \
"s_add_i32 s6, s13, s6\n" \
"v_mfma_f32_16x16x32_bf16 v[164:167], a[240:243], a[220:223], v[164:167]\n" \
"ds_read_b128 a[156:159], v28 offset:14336\n" \
"ds_read_b128 a[220:223], v29 offset:14336\n" \
"s_waitcnt lgkmcnt(15)\n" \
"v_mfma_f32_16x16x32_bf16 a[0:3], a[160:163], a[128:131], a[0:3]\n" \
"s_add_u32 m0, s30, 0x9000\n" \
"s_addk_i32 s84, 0x3000\n" \
"v_mfma_f32_16x16x32_bf16 a[0:3], a[224:227], a[192:195], a[0:3]\n" \
"buffer_load_dwordx4 v24, s[76:79], s84 offen offset:0 lds\n" \
"v_mfma_f32_16x16x32_bf16 a[4:7], a[164:167], a[128:131], a[4:7]\n" \
"ds_read_b64_tr_b16 a[176:177], v36 offset:32768\n" \
"v_writelane_b32 v11, s6, 41\n" \
"v_mfma_f32_16x16x32_bf16 a[4:7], a[228:231], a[192:195], a[4:7]\n" \
"ds_read_b64_tr_b16 a[178:179], v36 offset:33792\n" \
"s_add_i32 s8, s8, 0x42c0000\n" \
"v_mfma_f32_16x16x32_bf16 a[12:15], a[172:175], a[128:131], a[12:15]\n" \
"ds_read_b64_tr_b16 a[240:241], v36 offset:40960\n" \
"s_lshl_b32 s6, s12, 10\n" \
"s_waitcnt lgkmcnt(15)\n" \
"v_mfma_f32_16x16x32_bf16 a[12:15], a[236:239], a[192:195], a[12:15]\n" \
"s_add_u32 s89, s42, 0x3000\n" \
"buffer_store_dwordx4 v[208:211], v20, s[80:83], s89 offen offset:3072 sc1\n" \
"s_waitcnt lgkmcnt(13)\n" \
"v_mfma_f32_16x16x32_bf16 a[8:11], a[128:131], a[168:171], a[8:11]\n" \
"ds_read_b64_tr_b16 a[242:243], v36 offset:41984\n" \
"s_or_b32 s6, s8, s6\n" \
"s_waitcnt lgkmcnt(12)\n" \
"v_mfma_f32_16x16x32_bf16 a[8:11], a[192:195], a[232:235], a[8:11]\n" \
"ds_read_b64_tr_b16 a[180:181], v37 offset:32768\n" \
"v_mfma_f32_16x16x32_bf16 a[24:27], a[132:135], a[168:171], a[24:27]\n" \
"ds_read_b64_tr_b16 a[182:183], v37 offset:33792\n" \
"v_mfma_f32_16x16x32_bf16 a[24:27], a[196:199], a[232:235], a[24:27]\n" \
"ds_read_b64_tr_b16 a[244:245], v37 offset:40960\n" \
"v_mfma_f32_16x16x32_bf16 a[28:31], a[172:175], a[132:135], a[28:31]\n" \
"s_cmp_lg_u32 s16, 0\n" \
"s_cselect_b32 s6, s7, s6\n" \
"v_mfma_f32_16x16x32_bf16 a[28:31], a[236:239], a[196:199], a[28:31]\n" \
"s_addk_i32 s84, 0x2000\n" \
"v_mfma_f32_16x16x32_bf16 a[20:23], a[164:167], a[132:135], a[20:23]\n" \
"buffer_load_dwordx4 v25, s[76:79], s84 offen offset:1024 lds\n" \
"v_mfma_f32_16x16x32_bf16 a[20:23], a[228:231], a[196:199], a[20:23]\n" \
"ds_read_b64_tr_b16 a[246:247], v37 offset:41984\n" \
"s_mulk_i32 s4, 0x4800\n" \
"v_mfma_f32_16x16x32_bf16 a[16:19], a[160:163], a[132:135], a[16:19]\n" \
"ds_read_b64_tr_b16 a[184:185], v38 offset:32768\n" \
"v_writelane_b32 v11, s6, 42\n" \
"v_mfma_f32_16x16x32_bf16 a[16:19], a[224:227], a[196:199], a[16:19]\n" \
"ds_read_b64_tr_b16 a[186:187], v38 offset:33792\n" \
"s_add_i32 s6, s14, s10\n" \
"v_mfma_f32_16x16x32_bf16 a[32:35], a[160:163], a[136:139], a[32:35]\n" \
"s_addk_i32 s84, 0x2000\n" \
"v_mfma_f32_16x16x32_bf16 a[32:35], a[224:227], a[200:203], a[32:35]\n" \
"buffer_load_dwordx4 v24, s[76:79], s84 offen offset:2048 lds\n" \
"v_mfma_f32_16x16x32_bf16 a[36:39], a[164:167], a[136:139], a[36:39]\n" \
"ds_read_b64_tr_b16 a[248:249], v38 offset:40960\n" \
"v_writelane_b32 v11, s6, 43\n" \
"v_mfma_f32_16x16x32_bf16 a[36:39], a[228:231], a[200:203], a[36:39]\n" \
"ds_read_b64_tr_b16 a[250:251], v38 offset:41984\n" \
"s_add_i32 s6, s9, 0x21c000\n" \
"v_mfma_f32_16x16x32_bf16 a[44:47], a[172:175], a[136:139], a[44:47]\n" \
"ds_read_b64_tr_b16 a[188:189], v39 offset:32768\n" \
"s_add_i32 s9, s6, s10\n" \
"v_mfma_f32_16x16x32_bf16 a[44:47], a[236:239], a[200:203], a[44:47]\n" \
"ds_read_b64_tr_b16 a[190:191], v39 offset:33792\n" \
"v_mfma_f32_16x16x32_bf16 a[40:43], a[136:139], a[168:171], a[40:43]\n" \
"s_add_u32 s88, s42, 0x3000\n" \
"buffer_store_dwordx4 v[212:215], v20, s[80:83], s88 offen offset:2048 sc1\n" \
"v_mfma_f32_16x16x32_bf16 a[40:43], a[200:203], a[232:235], a[40:43]\n" \
"ds_read_b64_tr_b16 a[252:253], v39 offset:40960\n" \
"v_mfma_f32_16x16x32_bf16 a[56:59], a[140:143], a[168:171], a[56:59]\n" \
"ds_read_b64_tr_b16 a[254:255], v39 offset:41984\n" \
"v_mfma_f32_16x16x32_bf16 a[56:59], a[204:207], a[232:235], a[56:59]\n" \
"s_cmp_lg_u32 s12, 0\n" \
"s_cselect_b32 s6, s6, s14\n" \
"v_mfma_f32_16x16x32_bf16 a[60:63], a[172:175], a[140:143], a[60:63]\n" \
"s_addk_i32 s6, 0x100\n" \
"v_writelane_b32 v11, s9, 44\n" \
"v_mfma_f32_16x16x32_bf16 a[60:63], a[236:239], a[204:207], a[60:63]\n" \
"s_addk_i32 s84, 0x2000\n" \
"v_mfma_f32_16x16x32_bf16 a[52:55], a[164:167], a[140:143], a[52:55]\n" \
"buffer_load_dwordx4 v25, s[76:79], s84 offen offset:3072 lds\n" \
"v_mfma_f32_16x16x32_bf16 a[52:55], a[228:231], a[204:207], a[52:55]\n" \
"s_cmp_lg_u32 s16, 0\n" \
"s_cselect_b32 s6, s7, s6\n" \
"v_mfma_f32_16x16x32_bf16 a[48:51], a[160:163], a[140:143], a[48:51]\n" \
"v_writelane_b32 v11, s6, 45\n" \
"s_or_b32 s6, s8, s15\n" \
"v_mfma_f32_16x16x32_bf16 a[48:51], a[224:227], a[204:207], a[48:51]\n" \
"v_writelane_b32 v11, s6, 53\n" \
"s_lshl_b32 s6, s3, 17\n" \
"s_waitcnt lgkmcnt(15)\n" \
"v_mfma_f32_16x16x32_bf16 a[64:67], a[164:167], v[248:251], a[64:67]\n" \
"s_add_i32 s6, s13, s6\n" \
"s_or_b32 s5, s6, s5\n" \
"v_mfma_f32_16x16x32_bf16 a[64:67], a[228:231], v[48:51], a[64:67]\n" \
"s_or_b32 s5, s5, 0x3840\n" \
"v_writelane_b32 v11, s5, 6\n" \
"v_mfma_f32_16x16x32_bf16 a[80:83], a[172:175], v[248:251], a[80:83]\n" \
"s_mov_b32 s5, 0xff0000\n" \
"v_mfma_f32_16x16x32_bf16 a[80:83], a[236:239], v[48:51], a[80:83]\n" \
"s_cmp_lg_u32 s16, 0\n" \
"s_cselect_b32 s5, s17, s5\n" \
"v_mfma_f32_16x16x32_bf16 a[68:71], v[248:251], a[168:171], a[68:71]\n" \
"v_writelane_b32 v11, s5, 7\n" \
"s_mul_i32 s5, s3, 0x1200\n" \
"v_mfma_f32_16x16x32_bf16 a[68:71], v[48:51], a[232:235], a[68:71]\n" \
"s_waitcnt vmcnt(10) lgkmcnt(0)\n" \
"s_barrier\n" \
"v_xor_b32_e32 v28, 0x20000, v28\n" \
"v_xor_b32_e32 v29, 0x20000, v29\n" \
"v_mfma_f32_16x16x32_bf16 v[52:55], a[176:179], a[128:131], v[52:55]\n" \
"s_add_u32 m0, s40, 0x8000\n" \
"s_add_u32 s85, s39, 0xe000\n" \
"v_mfma_f32_16x16x32_bf16 v[52:55], a[240:243], a[192:195], v[52:55]\n" \
"buffer_load_dwordx4 v20, s[80:83], s85 offen sc1 lds\n" \
"v_mfma_f32_16x16x32_bf16 v[56:59], a[180:183], a[128:131], v[56:59]\n" \
"ds_read_b64_tr_b16 a[160:161], v32 offset:0\n" \
"s_add_i32 s4, s5, s4\n" \
"v_mfma_f32_16x16x32_bf16 v[56:59], a[244:247], a[192:195], v[56:59]\n" \
"ds_read_b64_tr_b16 a[162:163], v32 offset:512\n" \
"s_add_i32 s4, s4, s11\n" \
"v_mfma_f32_16x16x32_bf16 v[60:63], a[184:187], a[128:131], v[60:63]\n" \
"s_add_u32 m0, s40, 0x9000\n" \
"s_add_u32 s85, s39, 0xf000\n" \
"v_mfma_f32_16x16x32_bf16 v[60:63], a[248:251], a[192:195], v[60:63]\n" \
"buffer_load_dwordx4 v20, s[80:83], s85 offen sc1 lds\n" \
"v_mfma_f32_16x16x32_bf16 v[64:67], a[188:191], a[128:131], v[64:67]\n" \
"ds_read_b64_tr_b16 a[224:225], v32 offset:4096\n" \
"v_writelane_b32 v11, s4, 8\n" \
"v_mfma_f32_16x16x32_bf16 v[64:67], a[252:255], a[192:195], v[64:67]\n" \
"ds_read_b128 a[128:131], v28 offset:0\n" \
"s_lshl_b32 s4, s2, 16\n" \
"v_mfma_f32_16x16x32_bf16 v[80:83], a[188:191], a[132:135], v[80:83]\n" \
"ds_read_b128 a[192:195], v29 offset:0\n" \
"s_add_i32 s5, s4, 0x10000\n" \
"v_mfma_f32_16x16x32_bf16 v[80:83], a[252:255], a[196:199], v[80:83]\n" \
"s_add_u32 m0, s40, 0xb000\n" \
"s_movk_i32 s84, 0x380\n" \
"v_mfma_f32_16x16x32_bf16 v[76:79], a[184:187], a[132:135], v[76:79]\n" \
"buffer_load_dwordx4 v21, s[76:79], s84 offen offset:0 lds\n" \
"v_mfma_f32_16x16x32_bf16 v[76:79], a[248:251], a[196:199], v[76:79]\n" \
"ds_read_b64_tr_b16 a[226:227], v32 offset:4608\n" \
"s_and_b32 s5, s5, 0x70000\n" \
"v_mfma_f32_16x16x32_bf16 v[72:75], a[180:183], a[132:135], v[72:75]\n" \
"ds_read_b64_tr_b16 a[164:165], v33 offset:0\n" \
"s_add_i32 s5, s13, s5\n" \
"v_mfma_f32_16x16x32_bf16 v[72:75], a[244:247], a[196:199], v[72:75]\n" \
"v_writelane_b32 v11, s5, 46\n" \
"s_add_i32 s5, s4, 0x20000\n" \
"v_mfma_f32_16x16x32_bf16 v[68:71], a[176:179], a[132:135], v[68:71]\n" \
"ds_read_b64_tr_b16 a[166:167], v33 offset:512\n" \
"s_and_b32 s5, s5, 0x70000\n" \
"v_mfma_f32_16x16x32_bf16 v[68:71], a[240:243], a[196:199], v[68:71]\n" \
"ds_read_b128 a[132:135], v28 offset:2048\n" \
"s_add_i32 s5, s13, s5\n" \
"v_mfma_f32_16x16x32_bf16 v[84:87], a[176:179], a[136:139], v[84:87]\n" \
"ds_read_b128 a[196:199], v29 offset:2048\n" \
"s_bitset1_b32 s5, 11\n" \
"v_mfma_f32_16x16x32_bf16 v[84:87], a[240:243], a[200:203], v[84:87]\n" \
"s_add_u32 m0, s31, 0x8000\n" \
"s_mov_b32 s84, 0x380000\n" \
"v_mfma_f32_16x16x32_bf16 v[88:91], a[180:183], a[136:139], v[88:91]\n" \
"buffer_load_dwordx4 v26, s[68:71], s84 offen offset:0 lds\n" \
"v_mfma_f32_16x16x32_bf16 v[88:91], a[244:247], a[200:203], v[88:91]\n" \
"s_add_u32 s89, s42, 0x0\n" \
"buffer_store_dwordx4 v[184:187], v20, s[80:83], s89 offen offset:1024 sc1\n" \
"v_mfma_f32_16x16x32_bf16 v[92:95], a[184:187], a[136:139], v[92:95]\n" \
"ds_read_b64_tr_b16 a[228:229], v33 offset:4096\n" \
"v_writelane_b32 v11, s5, 47\n" \
"v_mfma_f32_16x16x32_bf16 v[92:95], a[248:251], a[200:203], v[92:95]\n" \
"ds_read_b64_tr_b16 a[230:231], v33 offset:4608\n" \
"s_add_i32 s5, s4, 0x30000\n" \
"v_mfma_f32_16x16x32_bf16 v[96:99], a[188:191], a[136:139], v[96:99]\n" \
"s_and_b32 s5, s5, 0x70000\n" \
"s_add_i32 s5, s13, s5\n" \
"v_mfma_f32_16x16x32_bf16 v[96:99], a[252:255], a[200:203], v[96:99]\n" \
"ds_read_b128 a[136:139], v28 offset:4096\n" \
"s_bitset1_b32 s5, 12\n" \
"v_mfma_f32_16x16x32_bf16 v[112:115], a[188:191], a[140:143], v[112:115]\n" \
"ds_read_b128 a[200:203], v29 offset:4096\n" \
"v_writelane_b32 v11, s5, 48\n" \
"v_mfma_f32_16x16x32_bf16 v[112:115], a[252:255], a[204:207], v[112:115]\n" \
"s_addk_i32 s84, 0x7c00\n" \
"v_mfma_f32_16x16x32_bf16 v[108:111], a[184:187], a[140:143], v[108:111]\n" \
"buffer_load_dwordx4 v26, s[68:71], s84 offen offset:1024 lds\n" \
"v_mfma_f32_16x16x32_bf16 v[108:111], a[248:251], a[204:207], v[108:111]\n" \
"ds_read_b64_tr_b16 a[172:173], v35 offset:0\n" \
"s_and_b32 s5, s4, 0x70000\n" \
"v_mfma_f32_16x16x32_bf16 v[104:107], a[180:183], a[140:143], v[104:107]\n" \
"s_addk_i32 s84, 0x7c00\n" \
"v_mfma_f32_16x16x32_bf16 v[104:107], a[244:247], a[204:207], v[104:107]\n" \
"buffer_load_dwordx4 v27, s[68:71], s84 offen offset:2048 lds\n" \
"v_mfma_f32_16x16x32_bf16 v[100:103], a[176:179], a[140:143], v[100:103]\n" \
"ds_read_b64_tr_b16 a[174:175], v35 offset:512\n" \
"s_xor_b32 s5, s5, 0x40000\n" \
"v_mfma_f32_16x16x32_bf16 v[100:103], a[240:243], a[204:207], v[100:103]\n" \
"ds_read_b128 a[140:143], v28 offset:6144\n" \
"s_add_i32 s5, s13, s5\n" \
"v_mfma_f32_16x16x32_bf16 v[116:119], a[176:179], a[144:147], v[116:119]\n" \
"ds_read_b128 a[204:207], v29 offset:6144\n" \
"s_or_b32 s5, s5, 0x1800\n" \
"v_mfma_f32_16x16x32_bf16 v[116:119], a[240:243], a[208:211], v[116:119]\n" \
"s_addk_i32 s84, 0x7c00\n" \
"v_mfma_f32_16x16x32_bf16 v[120:123], a[180:183], a[144:147], v[120:123]\n" \
"buffer_load_dwordx4 v27, s[68:71], s84 offen offset:3072 lds\n" \
"v_mfma_f32_16x16x32_bf16 v[120:123], a[244:247], a[208:211], v[120:123]\n" \
"ds_read_b64_tr_b16 a[236:237], v35 offset:4096\n" \
"v_writelane_b32 v11, s5, 49\n" \
"v_mfma_f32_16x16x32_bf16 v[124:127], a[184:187], a[144:147], v[124:127]\n" \
"ds_read_b64_tr_b16 a[238:239], v35 offset:4608\n" \
"s_add_i32 s5, s4, 0x50000\n" \
"v_mfma_f32_16x16x32_bf16 v[124:127], a[248:251], a[208:211], v[124:127]\n" \
"s_add_u32 m0, s30, 0x20000\n" \
"s_movk_i32 s84, 0x400\n" \
"v_mfma_f32_16x16x32_bf16 v[128:131], a[188:191], a[144:147], v[128:131]\n" \
"buffer_load_dwordx4 v24, s[76:79], s84 offen offset:0 lds\n" \
"v_mfma_f32_16x16x32_bf16 v[128:131], a[252:255], a[208:211], v[128:131]\n" \
"ds_read_b128 a[144:147], v28 offset:8192\n" \
"s_and_b32 s5, s5, 0x70000\n" \
"v_mfma_f32_16x16x32_bf16 v[144:147], a[188:191], a[148:151], v[144:147]\n" \
"ds_read_b128 a[208:211], v29 offset:8192\n" \
"s_add_i32 s5, s13, s5\n" \
"v_mfma_f32_16x16x32_bf16 v[144:147], a[252:255], a[212:215], v[144:147]\n" \
"ds_read_b64_tr_b16 a[168:169], v34 offset:0\n" \
"s_bitset1_b32 s5, 13\n" \
"v_mfma_f32_16x16x32_bf16 v[140:143], a[184:187], a[148:151], v[140:143]\n" \
"s_movk_i32 s84, 0x2400\n" \
"s_nop 0\n" \
"v_mfma_f32_16x16x32_bf16 v[140:143], a[248:251], a[212:215], v[140:143]\n" \
"buffer_load_dwordx4 v25, s[76:79], s84 offen offset:1024 lds\n" \
"v_mfma_f32_16x16x32_bf16 v[136:139], a[180:183], a[148:151], v[136:139]\n" \
"ds_read_b64_tr_b16 a[170:171], v34 offset:512\n" \
"v_writelane_b32 v11, s5, 50\n" \
"v_mfma_f32_16x16x32_bf16 v[136:139], a[244:247], a[212:215], v[136:139]\n" \
"ds_read_b64_tr_b16 a[232:233], v34 offset:4096\n" \
"s_add_i32 s5, s4, 0x60000\n" \
"v_mfma_f32_16x16x32_bf16 v[132:135], a[176:179], a[148:151], v[132:135]\n" \
"ds_read_b64_tr_b16 a[234:235], v34 offset:4608\n" \
"s_and_b32 s5, s5, 0x70000\n" \
"v_mfma_f32_16x16x32_bf16 v[132:135], a[240:243], a[212:215], v[132:135]\n" \
"ds_read_b128 a[148:151], v28 offset:10240\n" \
"s_add_i32 s4, s4, 0x70000\n" \
"v_mfma_f32_16x16x32_bf16 v[148:151], a[176:179], a[152:155], v[148:151]\n" \
"ds_read_b128 a[212:215], v29 offset:10240\n" \
"s_add_i32 s5, s13, s5\n" \
"v_mfma_f32_16x16x32_bf16 v[148:151], a[240:243], a[216:219], v[148:151]\n" \
"s_movk_i32 s84, 0x4400\n" \
"v_mfma_f32_16x16x32_bf16 v[152:155], a[180:183], a[152:155], v[152:155]\n" \
"buffer_load_dwordx4 v24, s[76:79], s84 offen offset:2048 lds\n" \
"v_mfma_f32_16x16x32_bf16 v[152:155], a[244:247], a[216:219], v[152:155]\n" \
"s_movk_i32 s84, 0x6400\n" \
"s_nop 0\n" \
"v_mfma_f32_16x16x32_bf16 v[156:159], a[184:187], a[152:155], v[156:159]\n" \
"buffer_load_dwordx4 v25, s[76:79], s84 offen offset:3072 lds\n" \
"v_mfma_f32_16x16x32_bf16 v[156:159], a[248:251], a[216:219], v[156:159]\n" \
"ds_read_b128 v[248:251], v22 offset:0\n" \
"s_and_b32 s4, s4, 0x70000\n" \
"v_mfma_f32_16x16x32_bf16 v[160:163], a[188:191], a[152:155], v[160:163]\n" \
"ds_read_b128 v[48:51], v23 offset:0\n" \
"s_or_b32 s5, s5, 0x2800\n" \
"v_mfma_f32_16x16x32_bf16 v[160:163], a[252:255], a[216:219], v[160:163]\n" \
"ds_read_b128 a[152:155], v28 offset:12288\n" \
"v_writelane_b32 v11, s5, 51\n" \
"v_mfma_f32_16x16x32_bf16 v[176:179], a[188:191], a[156:159], v[176:179]\n" \
"ds_read_b128 a[216:219], v29 offset:12288\n" \
"s_add_i32 s13, s13, s4\n" \
"v_mfma_f32_16x16x32_bf16 v[176:179], a[252:255], a[220:223], v[176:179]\n" \
"s_or_b32 s4, s13, 0x3000\n" \
"v_writelane_b32 v11, s4, 52\n" \
"v_mfma_f32_16x16x32_bf16 v[172:175], a[184:187], a[156:159], v[172:175]\n" \
"s_movk_i32 s11, 0x4000\n" \
"s_lshl_b32 s4, s2, 2\n" \
"v_mfma_f32_16x16x32_bf16 v[172:175], a[248:251], a[220:223], v[172:175]\n" \
"s_and_b32 s4, s4, 28\n" \
"s_bfe_u32 s5, s2, 0x20003\n" \
"v_mfma_f32_16x16x32_bf16 v[168:171], a[180:183], a[156:159], v[168:171]\n" \
"s_or_b32 s4, s4, s5\n" \
"s_lshr_b32 s5, s2, 5\n" \
"v_mfma_f32_16x16x32_bf16 v[168:171], a[244:247], a[220:223], v[168:171]\n" \
"s_lshr_b32 s19, s3, 1\n" \
"s_lshl_b32 s18, s5, 1\n" \
"v_mfma_f32_16x16x32_bf16 v[164:167], a[176:179], a[156:159], v[164:167]\n" \
"s_lshl_b32 s6, s4, 20\n" \
"s_lshl_b32 s7, s4, 8\n" \
"v_mfma_f32_16x16x32_bf16 v[164:167], a[240:243], a[220:223], v[164:167]\n" \
"ds_read_b128 a[156:159], v28 offset:14336\n" \
"ds_read_b128 a[220:223], v29 offset:14336\n" \
"s_waitcnt lgkmcnt(15)\n" \
"v_mfma_f32_16x16x32_bf16 a[0:3], a[160:163], a[128:131], a[0:3]\n" \
"s_add_u32 m0, s30, 0x21000\n" \
"s_addk_i32 s84, 0x3000\n" \
"v_mfma_f32_16x16x32_bf16 a[0:3], a[224:227], a[192:195], a[0:3]\n" \
"buffer_load_dwordx4 v24, s[76:79], s84 offen offset:0 lds\n" \
"v_mfma_f32_16x16x32_bf16 a[4:7], a[164:167], a[128:131], a[4:7]\n" \
"ds_read_b64_tr_b16 a[176:177], v36 offset:0\n" \
"s_and_b32 s13, s3, 1\n" \
"v_mfma_f32_16x16x32_bf16 a[4:7], a[228:231], a[192:195], a[4:7]\n" \
"ds_read_b64_tr_b16 a[178:179], v36 offset:1024\n" \
"s_add_i32 s14, s18, s19\n" \
"v_mfma_f32_16x16x32_bf16 a[12:15], a[172:175], a[128:131], a[12:15]\n" \
"ds_read_b64_tr_b16 a[240:241], v36 offset:8192\n" \
"s_waitcnt lgkmcnt(15)\n" \
"v_mfma_f32_16x16x32_bf16 a[12:15], a[236:239], a[192:195], a[12:15]\n" \
"s_add_u32 s88, s42, 0x1000\n" \
"buffer_store_dwordx4 v[188:191], v20, s[80:83], s88 offen offset:1024 sc1\n" \
"s_waitcnt lgkmcnt(13)\n" \
"v_mfma_f32_16x16x32_bf16 a[8:11], a[128:131], a[168:171], a[8:11]\n" \
"ds_read_b64_tr_b16 a[242:243], v36 offset:9216\n" \
"s_waitcnt lgkmcnt(12)\n" \
"v_mfma_f32_16x16x32_bf16 a[8:11], a[192:195], a[232:235], a[8:11]\n" \
"ds_read_b64_tr_b16 a[180:181], v37 offset:0\n" \
"v_mfma_f32_16x16x32_bf16 a[24:27], a[132:135], a[168:171], a[24:27]\n" \
"ds_read_b64_tr_b16 a[182:183], v37 offset:1024\n" \
"v_mfma_f32_16x16x32_bf16 a[24:27], a[196:199], a[232:235], a[24:27]\n" \
"ds_read_b64_tr_b16 a[244:245], v37 offset:8192\n" \
"v_mfma_f32_16x16x32_bf16 a[28:31], a[172:175], a[132:135], a[28:31]\n" \
"s_add_u32 s6, s26, s6\n" \
"s_addc_u32 s9, s27, 0\n" \
"v_mfma_f32_16x16x32_bf16 a[28:31], a[236:239], a[196:199], a[28:31]\n" \
"s_addk_i32 s84, 0x2000\n" \
"v_mfma_f32_16x16x32_bf16 a[20:23], a[164:167], a[132:135], a[20:23]\n" \
"buffer_load_dwordx4 v25, s[76:79], s84 offen offset:1024 lds\n" \
"v_mfma_f32_16x16x32_bf16 a[20:23], a[228:231], a[196:199], a[20:23]\n" \
"ds_read_b64_tr_b16 a[246:247], v37 offset:9216\n" \
"v_mfma_f32_16x16x32_bf16 a[16:19], a[160:163], a[132:135], a[16:19]\n" \
"ds_read_b64_tr_b16 a[184:185], v38 offset:0\n" \
"v_mfma_f32_16x16x32_bf16 a[16:19], a[224:227], a[196:199], a[16:19]\n" \
"ds_read_b64_tr_b16 a[186:187], v38 offset:1024\n" \
"v_mfma_f32_16x16x32_bf16 a[32:35], a[160:163], a[136:139], a[32:35]\n" \
"s_addk_i32 s84, 0x2000\n" \
"s_nop 0\n" \
"v_mfma_f32_16x16x32_bf16 a[32:35], a[224:227], a[200:203], a[32:35]\n" \
"buffer_load_dwordx4 v24, s[76:79], s84 offen offset:2048 lds\n" \
"v_mfma_f32_16x16x32_bf16 a[36:39], a[164:167], a[136:139], a[36:39]\n" \
"ds_read_b64_tr_b16 a[248:249], v38 offset:8192\n" \
"v_mfma_f32_16x16x32_bf16 a[36:39], a[228:231], a[200:203], a[36:39]\n" \
"ds_read_b64_tr_b16 a[250:251], v38 offset:9216\n" \
"v_mfma_f32_16x16x32_bf16 a[44:47], a[172:175], a[136:139], a[44:47]\n" \
"ds_read_b64_tr_b16 a[188:189], v39 offset:0\n" \
"v_mfma_f32_16x16x32_bf16 a[44:47], a[236:239], a[200:203], a[44:47]\n" \
"ds_read_b64_tr_b16 a[190:191], v39 offset:1024\n" \
"v_mfma_f32_16x16x32_bf16 a[40:43], a[136:139], a[168:171], a[40:43]\n" \
"s_add_u32 s89, s42, 0x2000\n" \
"buffer_store_dwordx4 v[192:195], v20, s[80:83], s89 offen offset:0 sc1\n" \
"v_mfma_f32_16x16x32_bf16 a[40:43], a[200:203], a[232:235], a[40:43]\n" \
"ds_read_b64_tr_b16 a[252:253], v39 offset:8192\n" \
"v_mfma_f32_16x16x32_bf16 a[56:59], a[140:143], a[168:171], a[56:59]\n" \
"ds_read_b64_tr_b16 a[254:255], v39 offset:9216\n" \
"v_mfma_f32_16x16x32_bf16 a[56:59], a[204:207], a[232:235], a[56:59]\n" \
"s_add_u32 s8, s6, 0x1000000\n" \
"s_addc_u32 s6, s9, 0\n" \
"v_mfma_f32_16x16x32_bf16 a[60:63], a[172:175], a[140:143], a[60:63]\n" \
"s_and_b32 s9, s6, 0xffff\n" \
"s_lshl_b32 s6, s5, 16\n" \
"v_mfma_f32_16x16x32_bf16 a[60:63], a[236:239], a[204:207], a[60:63]\n" \
"s_addk_i32 s84, 0x2000\n" \
"v_mfma_f32_16x16x32_bf16 a[52:55], a[164:167], a[140:143], a[52:55]\n" \
"buffer_load_dwordx4 v25, s[76:79], s84 offen offset:3072 lds\n" \
"v_mfma_f32_16x16x32_bf16 a[52:55], a[228:231], a[204:207], a[52:55]\n" \
"s_sub_i32 s10, 0x100000, s6\n" \
"s_mov_b32 s6, 0x8000\n" \
"v_mfma_f32_16x16x32_bf16 a[48:51], a[160:163], a[140:143], a[48:51]\n" \
"s_cmp_lg_u32 s5, 0\n" \
"s_cselect_b32 s17, s11, s6\n" \
"v_mfma_f32_16x16x32_bf16 a[48:51], a[224:227], a[204:207], a[48:51]\n" \
"s_lshl_b32 s6, s3, 1\n" \
"s_lshl_b32 s5, s5, 3\n" \
"s_waitcnt lgkmcnt(15)\n" \
"v_mfma_f32_16x16x32_bf16 a[64:67], a[164:167], v[248:251], a[64:67]\n" \
"s_and_b32 s6, s6, 4\n" \
"s_or_b32 s12, s5, s6\n" \
".fill (L_pb + 32480 - .) / 4, 4, 0xBF800000\n" \
"L_pp8:\n" \
"s_cbranch_execz L_pret\n" \
"v_mfma_f32_16x16x32_bf16 a[64:67], a[228:231], v[48:51], a[64:67]\n" \
"s_sub_i32 s5, s13, s5\n" \
"s_or_b32 s5, s5, s6\n" \
"v_mfma_f32_16x16x32_bf16 a[80:83], a[172:175], v[248:251], a[80:83]\n" \
"s_or_b32 s6, s12, s13\n" \
"s_lshl_b32 s4, s4, 2\n" \
"v_mfma_f32_16x16x32_bf16 a[80:83], a[236:239], v[48:51], a[80:83]\n" \
"s_mulk_i32 s6, 0x92\n" \
"s_add_i32 s4, s4, 18\n" \
"v_mfma_f32_16x16x32_bf16 a[68:71], v[248:251], a[168:171], a[68:71]\n" \
"s_mulk_i32 s5, 0x92\n" \
"s_add_i32 s6, s4, s6\n" \
"v_mfma_f32_16x16x32_bf16 a[68:71], v[48:51], a[232:235], a[68:71]\n" \
"s_waitcnt vmcnt(10) lgkmcnt(0)\n" \
"s_barrier\n" \
"v_mfma_f32_16x16x32_bf16 v[52:55], a[176:179], a[128:131], v[52:55]\n" \
"s_add_u32 m0, s40, 0x0\n" \
"s_add_u32 s85, s39, 0x10000\n" \
"v_mfma_f32_16x16x32_bf16 v[52:55], a[240:243], a[192:195], v[52:55]\n" \
"buffer_load_dwordx4 v20, s[80:83], s85 offen sc1 lds\n" \
"v_mfma_f32_16x16x32_bf16 v[56:59], a[180:183], a[128:131], v[56:59]\n" \
"ds_read_b64_tr_b16 a[160:161], v32 offset:32768\n" \
"s_add_i32 s5, s5, s4\n" \
"v_mfma_f32_16x16x32_bf16 v[56:59], a[244:247], a[192:195], v[56:59]\n" \
"ds_read_b64_tr_b16 a[162:163], v32 offset:33280\n" \
"s_lshl_b32 s4, s3, 11\n" \
"v_mfma_f32_16x16x32_bf16 v[60:63], a[184:187], a[128:131], v[60:63]\n" \
"s_lshl_b32 s15, s13, 12\n" \
"s_lshl_b32 s16, s19, 14\n" \
"v_mfma_f32_16x16x32_bf16 v[60:63], a[248:251], a[192:195], v[60:63]\n" \
"ds_read_b64_tr_b16 a[224:225], v32 offset:36864\n" \
"s_mul_i32 s19, s19, 0xe000\n" \
"v_mfma_f32_16x16x32_bf16 v[64:67], a[188:191], a[128:131], v[64:67]\n" \
"s_mov_b32 s11, 0x20000\n" \
"s_or_b32 s12, s4, 0xc000\n" \
"v_mfma_f32_16x16x32_bf16 v[64:67], a[252:255], a[192:195], v[64:67]\n" \
"ds_read_b128 a[128:131], v28 offset:32768\n" \
"s_lshl_b32 s4, s3, 8\n" \
"v_mfma_f32_16x16x32_bf16 v[80:83], a[188:191], a[132:135], v[80:83]\n" \
"ds_read_b128 a[192:195], v29 offset:32768\n" \
"s_or_b32 s22, s15, s16\n" \
"v_mfma_f32_16x16x32_bf16 v[80:83], a[252:255], a[196:199], v[80:83]\n" \
"s_add_u32 m0, s40, 0x1000\n" \
"s_add_u32 s85, s39, 0x11000\n" \
"v_mfma_f32_16x16x32_bf16 v[76:79], a[184:187], a[132:135], v[76:79]\n" \
"buffer_load_dwordx4 v20, s[80:83], s85 offen sc1 lds\n" \
"v_mfma_f32_16x16x32_bf16 v[76:79], a[248:251], a[196:199], v[76:79]\n" \
"ds_read_b64_tr_b16 a[226:227], v32 offset:37376\n" \
"s_or_b32 s23, s19, s15\n" \
"v_mfma_f32_16x16x32_bf16 v[72:75], a[180:183], a[132:135], v[72:75]\n" \
"ds_read_b64_tr_b16 a[164:165], v33 offset:32768\n" \
"s_lshl_b32 s60, s6, 10\n" \
"v_mfma_f32_16x16x32_bf16 v[72:75], a[244:247], a[196:199], v[72:75]\n" \
"s_lshl_b32 s61, s5, 10\n" \
"s_lshl_b32 s14, s14, 15\n" \
"v_mfma_f32_16x16x32_bf16 v[68:71], a[176:179], a[132:135], v[68:71]\n" \
"ds_read_b64_tr_b16 a[166:167], v33 offset:33280\n" \
"s_or_b32 s20, s4, 0x26c00\n" \
"v_mfma_f32_16x16x32_bf16 v[68:71], a[240:243], a[196:199], v[68:71]\n" \
"ds_read_b128 a[132:135], v28 offset:34816\n" \
"s_or_b32 s13, s22, 0x8000\n" \
"v_mfma_f32_16x16x32_bf16 v[84:87], a[176:179], a[136:139], v[84:87]\n" \
"ds_read_b128 a[196:199], v29 offset:34816\n" \
"s_add_i32 s22, s22, 0x14000\n" \
"v_mfma_f32_16x16x32_bf16 v[84:87], a[240:243], a[200:203], v[84:87]\n" \
"s_add_u32 m0, s40, 0x3000\n" \
"s_movk_i32 s84, 0x400\n" \
"v_mfma_f32_16x16x32_bf16 v[88:91], a[180:183], a[136:139], v[88:91]\n" \
"buffer_load_dwordx4 v21, s[76:79], s84 offen offset:0 lds\n" \
"v_mfma_f32_16x16x32_bf16 v[88:91], a[244:247], a[200:203], v[88:91]\n" \
"ds_read_b64_tr_b16 a[228:229], v33 offset:36864\n" \
"s_bitset1_b32 s23, 16\n" \
"v_mfma_f32_16x16x32_bf16 v[92:95], a[184:187], a[136:139], v[92:95]\n" \
"ds_read_b64_tr_b16 a[230:231], v33 offset:37376\n" \
"s_or_b32 s24, s7, 0x5be8020\n" \
"v_mfma_f32_16x16x32_bf16 v[92:95], a[248:251], a[200:203], v[92:95]\n" \
"s_add_u32 s88, s42, 0x2000\n" \
"buffer_store_dwordx4 v[216:219], v20, s[80:83], s88 offen offset:1024 sc1\n" \
"v_mfma_f32_16x16x32_bf16 v[96:99], a[188:191], a[136:139], v[96:99]\n" \
"s_and_b32 s57, s27, 0xffff\n" \
"s_mov_b32 s58, 0x6000000\n" \
"v_mfma_f32_16x16x32_bf16 v[96:99], a[252:255], a[200:203], v[96:99]\n" \
"ds_read_b128 a[136:139], v28 offset:36864\n" \
"s_mov_b32 s56, s26\n" \
"v_mfma_f32_16x16x32_bf16 v[112:115], a[188:191], a[140:143], v[112:115]\n" \
"ds_read_b128 a[200:203], v29 offset:36864\n" \
"s_mov_b32 s59, s11\n" \
"v_mfma_f32_16x16x32_bf16 v[112:115], a[252:255], a[204:207], v[112:115]\n" \
"ds_read_b64_tr_b16 a[172:173], v35 offset:32768\n" \
"s_add_i32 s60, s60, 0x3000000\n" \
"v_mfma_f32_16x16x32_bf16 v[108:111], a[184:187], a[140:143], v[108:111]\n" \
"ds_read_b64_tr_b16 a[174:175], v35 offset:33280\n" \
"s_add_i32 s61, s61, 0x411c000\n" \
"v_mfma_f32_16x16x32_bf16 v[108:111], a[248:251], a[204:207], v[108:111]\n" \
"s_sub_i32 s19, 14, s18\n" \
"s_mov_b32 s21, 0x71800000\n" \
"v_mfma_f32_16x16x32_bf16 v[104:107], a[180:183], a[140:143], v[104:107]\n" \
"s_add_u32 m0, s31, 0x0\n" \
"s_mov_b32 s84, 0x400000\n" \
"v_mfma_f32_16x16x32_bf16 v[104:107], a[244:247], a[204:207], v[104:107]\n" \
"buffer_load_dwordx4 v26, s[68:71], s84 offen offset:0 lds\n" \
"v_mfma_f32_16x16x32_bf16 v[100:103], a[176:179], a[140:143], v[100:103]\n" \
"ds_read_b64_tr_b16 a[236:237], v35 offset:36864\n" \
"s_mov_b32 s4, L_gsw-L_gtr\n" \
"v_mfma_f32_16x16x32_bf16 v[100:103], a[240:243], a[204:207], v[100:103]\n" \
"ds_read_b128 a[140:143], v28 offset:38912\n" \
"v_mfma_f32_16x16x32_bf16 v[116:119], a[176:179], a[144:147], v[116:119]\n" \
"ds_read_b128 a[204:207], v29 offset:38912\n" \
"v_mfma_f32_16x16x32_bf16 v[116:119], a[240:243], a[208:211], v[116:119]\n" \
"ds_read_b64_tr_b16 a[238:239], v35 offset:37376\n" \
"v_mfma_f32_16x16x32_bf16 v[120:123], a[180:183], a[144:147], v[120:123]\n" \
"ds_read_b64_tr_b16 a[168:169], v34 offset:32768\n" \
"v_mfma_f32_16x16x32_bf16 v[120:123], a[244:247], a[208:211], v[120:123]\n" \
"s_addk_i32 s84, 0x7c00\n" \
"v_mfma_f32_16x16x32_bf16 v[124:127], a[184:187], a[144:147], v[124:127]\n" \
"buffer_load_dwordx4 v26, s[68:71], s84 offen offset:1024 lds\n" \
"v_mfma_f32_16x16x32_bf16 v[124:127], a[248:251], a[208:211], v[124:127]\n" \
"ds_read_b64_tr_b16 a[170:171], v34 offset:33280\n" \
"v_mfma_f32_16x16x32_bf16 v[128:131], a[188:191], a[144:147], v[128:131]\n" \
"s_cmp_eq_u32 s19, 0\n" \
"s_cselect_b32 s4, L_gs7-L_gtr, s4\n" \
"v_mfma_f32_16x16x32_bf16 v[128:131], a[252:255], a[208:211], v[128:131]\n" \
"ds_read_b128 a[144:147], v28 offset:40960\n" \
"v_mfma_f32_16x16x32_bf16 v[144:147], a[188:191], a[148:151], v[144:147]\n" \
"ds_read_b128 a[208:211], v29 offset:40960\n" \
"v_mfma_f32_16x16x32_bf16 v[144:147], a[252:255], a[212:215], v[144:147]\n" \
"ds_read_b64_tr_b16 a[232:233], v34 offset:36864\n" \
"v_mfma_f32_16x16x32_bf16 v[140:143], a[184:187], a[148:151], v[140:143]\n" \
"s_addk_i32 s84, 0x7c00\n" \
"v_mfma_f32_16x16x32_bf16 v[140:143], a[248:251], a[212:215], v[140:143]\n" \
"buffer_load_dwordx4 v27, s[68:71], s84 offen offset:2048 lds\n" \
"v_mfma_f32_16x16x32_bf16 v[136:139], a[180:183], a[148:151], v[136:139]\n" \
"ds_read_b64_tr_b16 a[234:235], v34 offset:37376\n" \
"v_mfma_f32_16x16x32_bf16 v[136:139], a[244:247], a[212:215], v[136:139]\n" \
"s_cmp_eq_u32 s16, 0\n" \
"s_cselect_b32 s4, 0, s4\n" \
"v_mfma_f32_16x16x32_bf16 v[132:135], a[176:179], a[148:151], v[132:135]\n" \
"ds_read_b128 v[248:251], v22 offset:32768\n" \
"v_mfma_f32_16x16x32_bf16 v[132:135], a[240:243], a[212:215], v[132:135]\n" \
"ds_read_b128 a[148:151], v28 offset:43008\n" \
"v_mfma_f32_16x16x32_bf16 v[148:151], a[176:179], a[152:155], v[148:151]\n" \
"ds_read_b128 a[212:215], v29 offset:43008\n" \
"v_mfma_f32_16x16x32_bf16 v[148:151], a[240:243], a[216:219], v[148:151]\n" \
"s_addk_i32 s84, 0x7c00\n" \
"v_mfma_f32_16x16x32_bf16 v[152:155], a[180:183], a[152:155], v[152:155]\n" \
"buffer_load_dwordx4 v27, s[68:71], s84 offen offset:3072 lds\n" \
"v_mfma_f32_16x16x32_bf16 v[152:155], a[244:247], a[216:219], v[152:155]\n" \
"ds_read_b128 v[48:51], v23 offset:32768\n" \
"v_mfma_f32_16x16x32_bf16 v[156:159], a[184:187], a[152:155], v[156:159]\n" \
"s_getpc_b64 s[28:29]\n" \
"L_gp0_%=:\n" \
"s_add_u32 s28, s28, L_gtr-L_gp0_%=\n" \
"s_addc_u32 s29, s29, 0\n" \
"v_mfma_f32_16x16x32_bf16 v[156:159], a[248:251], a[216:219], v[156:159]\n" \
"s_add_u32 s28, s28, s4\n" \
"s_addc_u32 s29, s29, 0\n" \
"v_mfma_f32_16x16x32_bf16 v[160:163], a[188:191], a[152:155], v[160:163]\n" \
"v_mfma_f32_16x16x32_bf16 v[160:163], a[252:255], a[216:219], v[160:163]\n" \
"ds_read_b128 a[152:155], v28 offset:45056\n" \
"v_mfma_f32_16x16x32_bf16 v[176:179], a[188:191], a[156:159], v[176:179]\n" \
"ds_read_b128 a[216:219], v29 offset:45056\n" \
"v_mfma_f32_16x16x32_bf16 v[176:179], a[252:255], a[220:223], v[176:179]\n" \
"v_mfma_f32_16x16x32_bf16 v[172:175], a[184:187], a[156:159], v[172:175]\n" \
"v_mfma_f32_16x16x32_bf16 v[172:175], a[248:251], a[220:223], v[172:175]\n" \
"v_mfma_f32_16x16x32_bf16 v[168:171], a[180:183], a[156:159], v[168:171]\n" \
"v_mfma_f32_16x16x32_bf16 v[168:171], a[244:247], a[220:223], v[168:171]\n" \
"v_mfma_f32_16x16x32_bf16 v[164:167], a[176:179], a[156:159], v[164:167]\n" \
"v_mfma_f32_16x16x32_bf16 v[164:167], a[240:243], a[220:223], v[164:167]\n" \
"ds_read_b128 a[156:159], v28 offset:47104\n" \
"ds_read_b128 a[220:223], v29 offset:47104\n" \
"s_waitcnt lgkmcnt(15)\n" \
"s_nop 0\n" \
"v_mfma_f32_16x16x32_bf16 a[0:3], a[160:163], a[128:131], a[0:3]\n" \
"s_add_u32 s89, s42, 0x3000\n" \
"buffer_store_dwordx4 v[220:223], v20, s[80:83], s89 offen offset:0 sc1\n" \
"v_mfma_f32_16x16x32_bf16 a[0:3], a[224:227], a[192:195], a[0:3]\n" \
"ds_read_b64_tr_b16 a[176:177], v36 offset:32768\n" \
"v_mfma_f32_16x16x32_bf16 a[4:7], a[164:167], a[128:131], a[4:7]\n" \
"ds_read_b64_tr_b16 a[178:179], v36 offset:33792\n" \
"v_mfma_f32_16x16x32_bf16 a[4:7], a[228:231], a[192:195], a[4:7]\n" \
"ds_read_b64_tr_b16 a[240:241], v36 offset:40960\n" \
"v_mfma_f32_16x16x32_bf16 a[12:15], a[172:175], a[128:131], a[12:15]\n" \
"ds_read_b64_tr_b16 a[242:243], v36 offset:41984\n" \
"s_waitcnt lgkmcnt(15)\n" \
"v_mfma_f32_16x16x32_bf16 a[12:15], a[236:239], a[192:195], a[12:15]\n" \
"v_mfma_f32_16x16x32_bf16 a[8:11], a[128:131], a[168:171], a[8:11]\n" \
"ds_read_b64_tr_b16 a[180:181], v37 offset:32768\n" \
"s_waitcnt lgkmcnt(13)\n" \
"v_mfma_f32_16x16x32_bf16 a[8:11], a[192:195], a[232:235], a[8:11]\n" \
"v_mfma_f32_16x16x32_bf16 a[24:27], a[132:135], a[168:171], a[24:27]\n" \
"ds_read_b64_tr_b16 a[182:183], v37 offset:33792\n" \
"v_mfma_f32_16x16x32_bf16 a[24:27], a[196:199], a[232:235], a[24:27]\n" \
"ds_read_b64_tr_b16 a[244:245], v37 offset:40960\n" \
"v_mfma_f32_16x16x32_bf16 a[28:31], a[172:175], a[132:135], a[28:31]\n" \
"v_mfma_f32_16x16x32_bf16 a[28:31], a[236:239], a[196:199], a[28:31]\n" \
"ds_read_b64_tr_b16 a[246:247], v37 offset:41984\n" \
"v_mfma_f32_16x16x32_bf16 a[20:23], a[164:167], a[132:135], a[20:23]\n" \
"v_mfma_f32_16x16x32_bf16 a[20:23], a[228:231], a[196:199], a[20:23]\n" \
"ds_read_b64_tr_b16 a[184:185], v38 offset:32768\n" \
"v_mfma_f32_16x16x32_bf16 a[16:19], a[160:163], a[132:135], a[16:19]\n" \
"ds_read_b64_tr_b16 a[186:187], v38 offset:33792\n" \
"v_mfma_f32_16x16x32_bf16 a[16:19], a[224:227], a[196:199], a[16:19]\n" \
"v_mfma_f32_16x16x32_bf16 a[32:35], a[160:163], a[136:139], a[32:35]\n" \
"s_add_u32 s88, s42, 0x3000\n" \
"buffer_store_dwordx4 v[224:227], v20, s[80:83], s88 offen offset:1024 sc1\n" \
"v_mfma_f32_16x16x32_bf16 a[32:35], a[224:227], a[200:203], a[32:35]\n" \
"ds_read_b64_tr_b16 a[248:249], v38 offset:40960\n" \
"v_mfma_f32_16x16x32_bf16 a[36:39], a[164:167], a[136:139], a[36:39]\n" \
"v_lshlrev_b32_e32 v45, 3, v20\n" \
"v_add_u32_e32 v45, s30, v45\n" \
"v_mfma_f32_16x16x32_bf16 a[36:39], a[228:231], a[200:203], a[36:39]\n" \
"s_getpc_b64 s[86:87]\n" \
"L_ct0_%=:\n" \
"s_add_u32 s86, s86, L_ap0-L_ct0_%=\n" \
"s_addc_u32 s87, s87, 0\n" \
"s_add_u32 m0, s40, 0x2000\n" \
"v_mfma_f32_16x16x32_bf16 a[44:47], a[172:175], a[136:139], a[44:47]\n" \
"global_load_lds_dword v45, s[86:87]\n" \
"v_mfma_f32_16x16x32_bf16 a[44:47], a[236:239], a[200:203], a[44:47]\n" \
"ds_read_b64_tr_b16 a[250:251], v38 offset:41984\n" \
"v_mfma_f32_16x16x32_bf16 a[40:43], a[136:139], a[168:171], a[40:43]\n" \
"ds_read_b64_tr_b16 a[188:189], v39 offset:32768\n" \
"v_mfma_f32_16x16x32_bf16 a[40:43], a[200:203], a[232:235], a[40:43]\n" \
"ds_read_b64_tr_b16 a[190:191], v39 offset:33792\n" \
"v_mfma_f32_16x16x32_bf16 a[56:59], a[140:143], a[168:171], a[56:59]\n" \
"ds_read_b64_tr_b16 a[252:253], v39 offset:40960\n" \
"v_mfma_f32_16x16x32_bf16 a[56:59], a[204:207], a[232:235], a[56:59]\n" \
"ds_read_b64_tr_b16 a[254:255], v39 offset:41984\n" \
"v_mfma_f32_16x16x32_bf16 a[60:63], a[172:175], a[140:143], a[60:63]\n" \
"v_mfma_f32_16x16x32_bf16 a[60:63], a[236:239], a[204:207], a[60:63]\n" \
"v_mfma_f32_16x16x32_bf16 a[52:55], a[164:167], a[140:143], a[52:55]\n" \
"v_mfma_f32_16x16x32_bf16 a[52:55], a[228:231], a[204:207], a[52:55]\n" \
"v_mfma_f32_16x16x32_bf16 a[48:51], a[160:163], a[140:143], a[48:51]\n" \
"v_mfma_f32_16x16x32_bf16 a[48:51], a[224:227], a[204:207], a[48:51]\n" \
"s_waitcnt lgkmcnt(15)\n" \
"v_mfma_f32_16x16x32_bf16 a[64:67], a[164:167], v[248:251], a[64:67]\n" \
"v_mfma_f32_16x16x32_bf16 a[64:67], a[228:231], v[48:51], a[64:67]\n" \
"v_mfma_f32_16x16x32_bf16 a[80:83], a[172:175], v[248:251], a[80:83]\n" \
"v_mfma_f32_16x16x32_bf16 a[80:83], a[236:239], v[48:51], a[80:83]\n" \
"v_mfma_f32_16x16x32_bf16 a[68:71], v[248:251], a[168:171], a[68:71]\n" \
"v_mfma_f32_16x16x32_bf16 a[68:71], v[48:51], a[232:235], a[68:71]\n" \
"s_waitcnt vmcnt(3) lgkmcnt(0)\n" \
"s_barrier\n" \
"s_nop 0\n" \
"v_xor_b32_e32 v28, 0x20000, v28\n" \
"v_xor_b32_e32 v29, 0x20000, v29\n" \
"v_mfma_f32_16x16x32_bf16 v[52:55], a[176:179], a[128:131], v[52:55]\n" \
"ds_read_b64_tr_b16 a[160:161], v32 offset:0\n" \
"v_accvgpr_read_b32 v184, a0\n" \
"v_mfma_f32_16x16x32_bf16 v[52:55], a[240:243], a[192:195], v[52:55]\n" \
"v_accvgpr_read_b32 v185, a1\n" \
"v_accvgpr_read_b32 v186, a2\n" \
"v_mfma_f32_16x16x32_bf16 v[56:59], a[180:183], a[128:131], v[56:59]\n" \
"ds_read_b64_tr_b16 a[162:163], v32 offset:512\n" \
"v_accvgpr_read_b32 v187, a3\n" \
"v_mfma_f32_16x16x32_bf16 v[56:59], a[244:247], a[192:195], v[56:59]\n" \
"v_accvgpr_read_b32 v188, a4\n" \
"v_accvgpr_read_b32 v189, a5\n" \
"v_mfma_f32_16x16x32_bf16 v[60:63], a[184:187], a[128:131], v[60:63]\n" \
"v_accvgpr_read_b32 v190, a6\n" \
"v_accvgpr_read_b32 v191, a7\n" \
"v_mfma_f32_16x16x32_bf16 v[60:63], a[248:251], a[192:195], v[60:63]\n" \
"ds_read_b64_tr_b16 a[224:225], v32 offset:4096\n" \
"v_accvgpr_read_b32 v196, a12\n" \
"v_mfma_f32_16x16x32_bf16 v[64:67], a[188:191], a[128:131], v[64:67]\n" \
"v_accvgpr_read_b32 v197, a13\n" \
"v_accvgpr_read_b32 v198, a14\n" \
"v_mfma_f32_16x16x32_bf16 v[64:67], a[252:255], a[192:195], v[64:67]\n" \
"ds_read_b128 a[128:131], v28 offset:0\n" \
"v_accvgpr_read_b32 v199, a15\n" \
"v_mfma_f32_16x16x32_bf16 v[80:83], a[188:191], a[132:135], v[80:83]\n" \
"ds_read_b128 a[192:195], v29 offset:0\n" \
"v_accvgpr_read_b32 v192, a8\n" \
"v_mfma_f32_16x16x32_bf16 v[80:83], a[252:255], a[196:199], v[80:83]\n" \
"ds_read_b64_tr_b16 a[226:227], v32 offset:4608\n" \
"v_accvgpr_read_b32 v193, a9\n" \
"v_mfma_f32_16x16x32_bf16 v[76:79], a[184:187], a[132:135], v[76:79]\n" \
"v_accvgpr_read_b32 v194, a10\n" \
"v_accvgpr_read_b32 v195, a11\n" \
"v_mfma_f32_16x16x32_bf16 v[76:79], a[248:251], a[196:199], v[76:79]\n" \
"ds_read_b64_tr_b16 a[164:165], v33 offset:0\n" \
"v_accvgpr_read_b32 v208, a24\n" \
"v_mfma_f32_16x16x32_bf16 v[72:75], a[180:183], a[132:135], v[72:75]\n" \
"v_accvgpr_read_b32 v209, a25\n" \
"v_accvgpr_read_b32 v210, a26\n" \
"v_mfma_f32_16x16x32_bf16 v[72:75], a[244:247], a[196:199], v[72:75]\n" \
"v_accvgpr_read_b32 v211, a27\n" \
"v_accvgpr_read_b32 v212, a28\n" \
"v_mfma_f32_16x16x32_bf16 v[68:71], a[176:179], a[132:135], v[68:71]\n" \
"ds_read_b64_tr_b16 a[166:167], v33 offset:512\n" \
"v_accvgpr_read_b32 v213, a29\n" \
"v_mfma_f32_16x16x32_bf16 v[68:71], a[240:243], a[196:199], v[68:71]\n" \
"ds_read_b128 a[132:135], v28 offset:2048\n" \
"v_accvgpr_read_b32 v214, a30\n" \
"v_mfma_f32_16x16x32_bf16 v[84:87], a[176:179], a[136:139], v[84:87]\n" \
"ds_read_b128 a[196:199], v29 offset:2048\n" \
"v_accvgpr_read_b32 v215, a31\n" \
"v_mfma_f32_16x16x32_bf16 v[84:87], a[240:243], a[200:203], v[84:87]\n" \
"ds_read_b64_tr_b16 a[228:229], v33 offset:4096\n" \
"v_accvgpr_read_b32 v204, a20\n" \
"v_mfma_f32_16x16x32_bf16 v[88:91], a[180:183], a[136:139], v[88:91]\n" \
"v_accvgpr_read_b32 v205, a21\n" \
"v_accvgpr_read_b32 v206, a22\n" \
"v_mfma_f32_16x16x32_bf16 v[88:91], a[244:247], a[200:203], v[88:91]\n" \
"v_accvgpr_read_b32 v207, a23\n" \
"v_accvgpr_read_b32 v200, a16\n" \
"v_mfma_f32_16x16x32_bf16 v[92:95], a[184:187], a[136:139], v[92:95]\n" \
"ds_read_b64_tr_b16 a[230:231], v33 offset:4608\n" \
"v_accvgpr_read_b32 v201, a17\n" \
"v_mfma_f32_16x16x32_bf16 v[92:95], a[248:251], a[200:203], v[92:95]\n" \
"v_accvgpr_read_b32 v202, a18\n" \
"v_accvgpr_read_b32 v203, a19\n" \
"v_mfma_f32_16x16x32_bf16 v[96:99], a[188:191], a[136:139], v[96:99]\n" \
"v_accvgpr_read_b32 v216, a32\n" \
"v_accvgpr_read_b32 v217, a33\n" \
"v_mfma_f32_16x16x32_bf16 v[96:99], a[252:255], a[200:203], v[96:99]\n" \
"ds_read_b128 a[136:139], v28 offset:4096\n" \
"v_accvgpr_read_b32 v218, a34\n" \
"v_mfma_f32_16x16x32_bf16 v[112:115], a[188:191], a[140:143], v[112:115]\n" \
"ds_read_b128 a[200:203], v29 offset:4096\n" \
"v_accvgpr_read_b32 v219, a35\n" \
"v_mfma_f32_16x16x32_bf16 v[112:115], a[252:255], a[204:207], v[112:115]\n" \
"ds_read_b64_tr_b16 a[172:173], v35 offset:0\n" \
"v_accvgpr_read_b32 v220, a36\n" \
"v_mfma_f32_16x16x32_bf16 v[108:111], a[184:187], a[140:143], v[108:111]\n" \
"ds_read_b64_tr_b16 a[174:175], v35 offset:512\n" \
"v_accvgpr_read_b32 v221, a37\n" \
"v_mfma_f32_16x16x32_bf16 v[108:111], a[248:251], a[204:207], v[108:111]\n" \
"v_accvgpr_read_b32 v222, a38\n" \
"v_accvgpr_read_b32 v223, a39\n" \
"v_mfma_f32_16x16x32_bf16 v[104:107], a[180:183], a[140:143], v[104:107]\n" \
"ds_read_b64_tr_b16 a[236:237], v35 offset:4096\n" \
"v_accvgpr_read_b32 v228, a44\n" \
"v_mfma_f32_16x16x32_bf16 v[104:107], a[244:247], a[204:207], v[104:107]\n" \
"v_accvgpr_read_b32 v229, a45\n" \
"v_accvgpr_read_b32 v230, a46\n" \
"v_mfma_f32_16x16x32_bf16 v[100:103], a[176:179], a[140:143], v[100:103]\n" \
"v_accvgpr_read_b32 v231, a47\n" \
"v_accvgpr_read_b32 v224, a40\n" \
"v_mfma_f32_16x16x32_bf16 v[100:103], a[240:243], a[204:207], v[100:103]\n" \
"ds_read_b128 a[140:143], v28 offset:6144\n" \
"v_accvgpr_read_b32 v225, a41\n" \
"v_mfma_f32_16x16x32_bf16 v[116:119], a[176:179], a[144:147], v[116:119]\n" \
"ds_read_b128 a[204:207], v29 offset:6144\n" \
"v_accvgpr_read_b32 v226, a42\n" \
"v_mfma_f32_16x16x32_bf16 v[116:119], a[240:243], a[208:211], v[116:119]\n" \
"ds_read_b64_tr_b16 a[238:239], v35 offset:4608\n" \
"v_accvgpr_read_b32 v227, a43\n" \
"v_mfma_f32_16x16x32_bf16 v[120:123], a[180:183], a[144:147], v[120:123]\n" \
"ds_read_b64_tr_b16 a[168:169], v34 offset:0\n" \
"v_accvgpr_read_b32 v240, a56\n" \
"v_mfma_f32_16x16x32_bf16 v[120:123], a[244:247], a[208:211], v[120:123]\n" \
"v_accvgpr_read_b32 v241, a57\n" \
"v_accvgpr_read_b32 v242, a58\n" \
"v_mfma_f32_16x16x32_bf16 v[124:127], a[184:187], a[144:147], v[124:127]\n" \
"v_accvgpr_read_b32 v243, a59\n" \
"v_accvgpr_read_b32 v244, a60\n" \
"v_mfma_f32_16x16x32_bf16 v[124:127], a[248:251], a[208:211], v[124:127]\n" \
"ds_read_b64_tr_b16 a[170:171], v34 offset:512\n" \
"v_accvgpr_read_b32 v245, a61\n" \
"v_mfma_f32_16x16x32_bf16 v[128:131], a[188:191], a[144:147], v[128:131]\n" \
"v_accvgpr_read_b32 v246, a62\n" \
"v_accvgpr_read_b32 v247, a63\n" \
"v_mfma_f32_16x16x32_bf16 v[128:131], a[252:255], a[208:211], v[128:131]\n" \
"ds_read_b128 a[144:147], v28 offset:8192\n" \
"v_accvgpr_read_b32 v236, a52\n" \
"v_mfma_f32_16x16x32_bf16 v[144:147], a[188:191], a[148:151], v[144:147]\n" \
"ds_read_b128 a[208:211], v29 offset:8192\n" \
"v_accvgpr_read_b32 v237, a53\n" \
"v_mfma_f32_16x16x32_bf16 v[144:147], a[252:255], a[212:215], v[144:147]\n" \
"ds_read_b64_tr_b16 a[232:233], v34 offset:4096\n" \
"v_accvgpr_read_b32 v238, a54\n" \
"v_mfma_f32_16x16x32_bf16 v[140:143], a[184:187], a[148:151], v[140:143]\n" \
"v_accvgpr_read_b32 v239, a55\n" \
"v_accvgpr_read_b32 v232, a48\n" \
"v_mfma_f32_16x16x32_bf16 v[140:143], a[248:251], a[212:215], v[140:143]\n" \
"ds_read_b64_tr_b16 a[234:235], v34 offset:4608\n" \
"v_accvgpr_read_b32 v233, a49\n" \
"v_mfma_f32_16x16x32_bf16 v[136:139], a[180:183], a[148:151], v[136:139]\n" \
"v_accvgpr_read_b32 v234, a50\n" \
"v_accvgpr_read_b32 v235, a51\n" \
"v_mfma_f32_16x16x32_bf16 v[136:139], a[244:247], a[212:215], v[136:139]\n" \
"v_accvgpr_read_b32 v180, a64\n" \
"v_accvgpr_read_b32 v181, a65\n" \
"v_mfma_f32_16x16x32_bf16 v[132:135], a[176:179], a[148:151], v[132:135]\n" \
"ds_read_b128 v[248:251], v22 offset:0\n" \
"v_accvgpr_read_b32 v182, a66\n" \
"v_mfma_f32_16x16x32_bf16 v[132:135], a[240:243], a[212:215], v[132:135]\n" \
"ds_read_b128 a[148:151], v28 offset:10240\n" \
"v_accvgpr_read_b32 v183, a67\n" \
"v_mfma_f32_16x16x32_bf16 v[148:151], a[176:179], a[152:155], v[148:151]\n" \
"ds_read_b128 a[212:215], v29 offset:10240\n" \
"v_accvgpr_read_b32 v252, a68\n" \
"v_mfma_f32_16x16x32_bf16 v[148:151], a[240:243], a[216:219], v[148:151]\n" \
"ds_read_b128 v[48:51], v23 offset:0\n" \
"v_accvgpr_read_b32 v253, a69\n" \
"v_mfma_f32_16x16x32_bf16 v[152:155], a[180:183], a[152:155], v[152:155]\n" \
"v_accvgpr_read_b32 v254, a70\n" \
"v_accvgpr_read_b32 v255, a71\n" \
"v_mfma_f32_16x16x32_bf16 v[152:155], a[244:247], a[216:219], v[152:155]\n" \
"v_lshrrev_b32_e64 v24, 9, v20\n" \
"v_and_b32_e32 v26, 0x1ff, v20\n" \
"v_mfma_f32_16x16x32_bf16 v[156:159], a[184:187], a[152:155], v[156:159]\n" \
"v_lshl_add_u32 v24, v24, 3, v26\n" \
"v_lshrrev_b32_e64 v25, 8, v20\n" \
"v_mfma_f32_16x16x32_bf16 v[156:159], a[248:251], a[216:219], v[156:159]\n" \
"v_mul_u32_u24_e32 v25, 0x24700, v25\n" \
"v_add_u32_e64 v25, v25, v20\n" \
"v_mfma_f32_16x16x32_bf16 v[160:163], a[188:191], a[152:155], v[160:163]\n" \
"v_lshrrev_b32_e32 v26, 2, v20\n" \
"v_add_u32_e32 v26, s45, v26\n" \
"v_mfma_f32_16x16x32_bf16 v[160:163], a[252:255], a[216:219], v[160:163]\n" \
"ds_read_b128 a[152:155], v28 offset:12288\n" \
"v_add_u32_e64 v27, s46, v20\n" \
"v_mfma_f32_16x16x32_bf16 v[176:179], a[188:191], a[156:159], v[176:179]\n" \
"ds_read_b128 a[216:219], v29 offset:12288\n" \
"v_add_u32_e64 v21, s43, v27\n" \
"v_mfma_f32_16x16x32_bf16 v[176:179], a[252:255], a[220:223], v[176:179]\n" \
"v_mfma_f32_16x16x32_bf16 v[172:175], a[184:187], a[156:159], v[172:175]\n" \
"v_mfma_f32_16x16x32_bf16 v[172:175], a[248:251], a[220:223], v[172:175]\n" \
"v_mfma_f32_16x16x32_bf16 v[168:171], a[180:183], a[156:159], v[168:171]\n" \
"v_mfma_f32_16x16x32_bf16 v[168:171], a[244:247], a[220:223], v[168:171]\n" \
"v_mfma_f32_16x16x32_bf16 v[164:167], a[176:179], a[156:159], v[164:167]\n" \
"v_mfma_f32_16x16x32_bf16 v[164:167], a[240:243], a[220:223], v[164:167]\n" \
"ds_read_b128 a[156:159], v28 offset:14336\n" \
"ds_read_b128 a[220:223], v29 offset:14336\n" \
"s_waitcnt lgkmcnt(15)\n" \
"s_nop 0\n" \
"v_mfma_f32_16x16x32_bf16 v[184:187], a[160:163], a[128:131], v[184:187]\n" \
"v_bfe_u32 v44, v20, 2, 5\n" \
"ds_read_b64_tr_b16 a[176:177], v36 offset:0\n" \
"v_mfma_f32_16x16x32_bf16 v[184:187], a[224:227], a[192:195], v[184:187]\n" \
"buffer_load_dword v44, v44, s[80:83], s38 offen offset:0 sc1\n" \
"v_mfma_f32_16x16x32_bf16 v[188:191], a[164:167], a[128:131], v[188:191]\n" \
"ds_read_b64_tr_b16 a[178:179], v36 offset:1024\n" \
"v_mfma_f32_16x16x32_bf16 v[188:191], a[228:231], a[192:195], v[188:191]\n" \
"ds_read_b64_tr_b16 a[240:241], v36 offset:8192\n" \
"v_mfma_f32_16x16x32_bf16 v[196:199], a[172:175], a[128:131], v[196:199]\n" \
"ds_read_b64_tr_b16 a[242:243], v36 offset:9216\n" \
"s_waitcnt lgkmcnt(15)\n" \
"v_mfma_f32_16x16x32_bf16 v[196:199], a[236:239], a[192:195], v[196:199]\n" \
"v_mfma_f32_16x16x32_bf16 v[192:195], a[128:131], a[168:171], v[192:195]\n" \
"ds_read_b64_tr_b16 a[180:181], v37 offset:0\n" \
"s_waitcnt lgkmcnt(13)\n" \
"v_mfma_f32_16x16x32_bf16 v[192:195], a[192:195], a[232:235], v[192:195]\n" \
"v_mfma_f32_16x16x32_bf16 v[208:211], a[132:135], a[168:171], v[208:211]\n" \
"ds_read_b64_tr_b16 a[182:183], v37 offset:1024\n" \
"v_mfma_f32_16x16x32_bf16 v[208:211], a[196:199], a[232:235], v[208:211]\n" \
"ds_read_b64_tr_b16 a[244:245], v37 offset:8192\n" \
"v_mfma_f32_16x16x32_bf16 v[212:215], a[172:175], a[132:135], v[212:215]\n" \
"v_mfma_f32_16x16x32_bf16 v[212:215], a[236:239], a[196:199], v[212:215]\n" \
"ds_read_b64_tr_b16 a[246:247], v37 offset:9216\n" \
"v_cvt_pk_bf16_f32 v188, v188, v189\n" \
"v_mfma_f32_16x16x32_bf16 v[204:207], a[164:167], a[132:135], v[204:207]\n" \
"v_cvt_pk_bf16_f32 v189, v190, v191\n" \
"v_cvt_pk_bf16_f32 v190, v198, v199\n" \
"v_mfma_f32_16x16x32_bf16 v[204:207], a[228:231], a[196:199], v[204:207]\n" \
"ds_read_b64_tr_b16 a[184:185], v38 offset:0\n" \
"v_mov_b32_e32 v191, 0x3f80\n" \
"v_mfma_f32_16x16x32_bf16 v[200:203], a[160:163], a[132:135], v[200:203]\n" \
"ds_read_b64_tr_b16 a[186:187], v38 offset:1024\n" \
"v_cvt_pk_bf16_f32 v184, v184, v185\n" \
"v_mfma_f32_16x16x32_bf16 v[200:203], a[224:227], a[196:199], v[200:203]\n" \
"v_cvt_pk_bf16_f32 v185, v186, v187\n" \
"v_cvt_pk_bf16_f32 v186, v196, v197\n" \
"v_mfma_f32_16x16x32_bf16 v[216:219], a[160:163], a[136:139], v[216:219]\n" \
"ds_read_b64_tr_b16 a[248:249], v38 offset:8192\n" \
"v_mov_b32_e64 v187, 0\n" \
"v_mfma_f32_16x16x32_bf16 v[216:219], a[224:227], a[200:203], v[216:219]\n" \
"ds_read_b64_tr_b16 a[250:251], v38 offset:9216\n" \
"v_mfma_f32_16x16x32_bf16 v[220:223], a[164:167], a[136:139], v[220:223]\n" \
"v_mfma_f32_16x16x32_bf16 v[220:223], a[228:231], a[200:203], v[220:223]\n" \
"ds_read_b64_tr_b16 a[188:189], v39 offset:0\n" \
"v_cvt_pk_bf16_f32 v204, v204, v205\n" \
"v_mfma_f32_16x16x32_bf16 v[228:231], a[172:175], a[136:139], v[228:231]\n" \
"v_cvt_pk_bf16_f32 v205, v206, v207\n" \
"v_cvt_pk_bf16_f32 v206, v214, v215\n" \
"v_mfma_f32_16x16x32_bf16 v[228:231], a[236:239], a[200:203], v[228:231]\n" \
"ds_read_b64_tr_b16 a[190:191], v39 offset:1024\n" \
"v_mov_b32_e32 v207, 0x3f80\n" \
"v_mfma_f32_16x16x32_bf16 v[224:227], a[136:139], a[168:171], v[224:227]\n" \
"ds_read_b64_tr_b16 a[252:253], v39 offset:8192\n" \
"v_cvt_pk_bf16_f32 v200, v200, v201\n" \
"v_mfma_f32_16x16x32_bf16 v[224:227], a[200:203], a[232:235], v[224:227]\n" \
"v_cvt_pk_bf16_f32 v201, v202, v203\n" \
"v_cvt_pk_bf16_f32 v202, v212, v213\n" \
"v_mfma_f32_16x16x32_bf16 v[240:243], a[140:143], a[168:171], v[240:243]\n" \
"ds_read_b64_tr_b16 a[254:255], v39 offset:9216\n" \
"v_mov_b32_e64 v203, 0\n" \
"v_mfma_f32_16x16x32_bf16 v[240:243], a[204:207], a[232:235], v[240:243]\n" \
"v_mfma_f32_16x16x32_bf16 v[244:247], a[172:175], a[140:143], v[244:247]\n" \
"v_mfma_f32_16x16x32_bf16 v[244:247], a[236:239], a[204:207], v[244:247]\n" \
"v_cvt_pk_bf16_f32 v220, v220, v221\n" \
"v_cvt_pk_bf16_f32 v221, v222, v223\n" \
"v_mfma_f32_16x16x32_bf16 v[236:239], a[164:167], a[140:143], v[236:239]\n" \
"v_cvt_pk_bf16_f32 v222, v230, v231\n" \
"v_mov_b32_e32 v223, 0x3f80\n" \
"v_mfma_f32_16x16x32_bf16 v[236:239], a[228:231], a[204:207], v[236:239]\n" \
"v_cvt_pk_bf16_f32 v216, v216, v217\n" \
"v_cvt_pk_bf16_f32 v217, v218, v219\n" \
"v_mfma_f32_16x16x32_bf16 v[232:235], a[160:163], a[140:143], v[232:235]\n" \
"v_cvt_pk_bf16_f32 v218, v228, v229\n" \
"v_mov_b32_e64 v219, 0\n" \
"v_mfma_f32_16x16x32_bf16 v[232:235], a[224:227], a[204:207], v[232:235]\n" \
"s_waitcnt lgkmcnt(15)\n" \
"s_nop 0\n" \
"v_mfma_f32_16x16x32_bf16 v[180:183], a[164:167], v[248:251], v[180:183]\n" \
"v_mfma_f32_16x16x32_bf16 v[180:183], a[228:231], v[48:51], v[180:183]\n" \
"v_mfma_f32_16x16x32_bf16 a[80:83], a[172:175], v[248:251], a[80:83]\n" \
"v_mfma_f32_16x16x32_bf16 a[80:83], a[236:239], v[48:51], a[80:83]\n" \
"v_cvt_pk_bf16_f32 v236, v236, v237\n" \
"v_cvt_pk_bf16_f32 v237, v238, v239\n" \
"v_mfma_f32_16x16x32_bf16 v[252:255], v[248:251], a[168:171], v[252:255]\n" \
"v_cvt_pk_bf16_f32 v238, v246, v247\n" \
"v_mov_b32_e32 v239, 0x3f80\n" \
"v_mfma_f32_16x16x32_bf16 v[252:255], v[48:51], a[232:235], v[252:255]\n" \
"v_cvt_pk_bf16_f32 v232, v232, v233\n" \
"v_cvt_pk_bf16_f32 v233, v234, v235\n" \
"s_waitcnt vmcnt(0) lgkmcnt(0)\n" \
"s_barrier\n" \
"v_mov_b32_e32 v45, s62\n" \
"s_mov_b64 exec, 1\n" \
"buffer_store_dword v45, off, s[80:83], s37 offset:32 sc1\n" \
"s_mov_b64 exec, -1\n" \
"v_cmp_ne_u32_e32 vcc, s62, v44\n" \
"s_nop 4\n" \
"s_cbranch_vccnz L_poll160_%=\n" \
"L_pollr160_%=:\n" \
"v_mfma_f32_16x16x32_bf16 v[52:55], a[176:179], a[128:131], v[52:55]\n" \
"s_mov_b32 s73, s50\n" \
"s_sub_u32 m0, s31, 0x10000\n" \
"v_mfma_f32_16x16x32_bf16 v[52:55], a[240:243], a[192:195], v[52:55]\n" \
"buffer_load_dwordx4 v20, s[80:83], s73 offen offset:0 sc1 lds\n" \
"v_mfma_f32_16x16x32_bf16 v[56:59], a[180:183], a[128:131], v[56:59]\n" \
"s_add_u32 s74, s50, 0x4000\n" \
"s_add_u32 s75, s50, 0x8000\n" \
"v_mfma_f32_16x16x32_bf16 v[56:59], a[244:247], a[192:195], v[56:59]\n" \
"s_mov_b32 s70, 0x3e0293ee\n" \
"s_mov_b32 s76, s52\n" \
"v_mfma_f32_16x16x32_bf16 v[60:63], a[184:187], a[128:131], v[60:63]\n" \
"v_mul_f32_e32 v52, s70, v52\n" \
"v_mul_f32_e32 v53, s70, v53\n" \
"v_mfma_f32_16x16x32_bf16 v[60:63], a[248:251], a[192:195], v[60:63]\n" \
"buffer_load_dwordx4 v20, s[80:83], s73 offen offset:1024 sc1 lds\n" \
"v_mfma_f32_16x16x32_bf16 v[64:67], a[188:191], a[128:131], v[64:67]\n" \
"v_mul_f32_e32 v54, s70, v54\n" \
"v_mul_f32_e32 v55, s70, v55\n" \
"v_mfma_f32_16x16x32_bf16 v[64:67], a[252:255], a[192:195], v[64:67]\n" \
"v_cvt_pk_bf16_f32 v52, v52, v53\n" \
"v_mul_f32_e32 v56, s70, v56\n" \
".fill (L_pb + 36576 - .) / 4, 4, 0xBF800000\n" \
"L_pp9:\n" \
"s_cbranch_execz L_pret\n" \
"v_mfma_f32_16x16x32_bf16 v[80:83], a[188:191], a[132:135], v[80:83]\n" \
"v_mul_f32_e32 v60, s70, v60\n" \
"v_mul_f32_e64 v61, s70, v61\n" \
"v_mfma_f32_16x16x32_bf16 v[80:83], a[252:255], a[196:199], v[80:83]\n" \
"buffer_load_dwordx4 v20, s[80:83], s73 offen offset:2048 sc1 lds\n" \
"v_mfma_f32_16x16x32_bf16 v[76:79], a[184:187], a[132:135], v[76:79]\n" \
"v_mul_f32_e32 v62, s70, v62\n" \
"v_mul_f32_e32 v63, s70, v63\n" \
"v_mfma_f32_16x16x32_bf16 v[76:79], a[248:251], a[196:199], v[76:79]\n" \
"v_cvt_pk_bf16_f32 v60, v60, v61\n" \
"v_mul_f32_e64 v57, s70, v57\n" \
"v_mfma_f32_16x16x32_bf16 v[72:75], a[180:183], a[132:135], v[72:75]\n" \
"v_mul_f32_e32 v58, s70, v58\n" \
"v_mul_f32_e32 v59, s70, v59\n" \
"v_mfma_f32_16x16x32_bf16 v[72:75], a[244:247], a[196:199], v[72:75]\n" \
"buffer_load_dwordx4 v20, s[80:83], s73 offen offset:3072 sc1 lds\n" \
"v_mfma_f32_16x16x32_bf16 v[68:71], a[176:179], a[132:135], v[68:71]\n" \
"s_sub_u32 m0, s31, s51\n" \
"v_cvt_pk_bf16_f32 v53, v54, v55\n" \
"v_mfma_f32_16x16x32_bf16 v[68:71], a[240:243], a[196:199], v[68:71]\n" \
"v_mul_f32_e32 v64, s70, v64\n" \
"v_mul_f32_e64 v65, s70, v65\n" \
"v_mfma_f32_16x16x32_bf16 v[84:87], a[176:179], a[136:139], v[84:87]\n" \
"v_mul_f32_e32 v66, s70, v66\n" \
"v_mul_f32_e32 v67, s70, v67\n" \
"v_mfma_f32_16x16x32_bf16 v[84:87], a[240:243], a[200:203], v[84:87]\n" \
"buffer_load_dwordx4 v20, s[80:83], s74 offen offset:0 sc1 lds\n" \
"v_mfma_f32_16x16x32_bf16 v[88:91], a[180:183], a[136:139], v[88:91]\n" \
"v_cvt_pk_bf16_f32 v61, v62, v63\n" \
"v_cvt_pk_bf16_f32 v54, v56, v57\n" \
"v_mfma_f32_16x16x32_bf16 v[88:91], a[244:247], a[200:203], v[88:91]\n" \
"v_cvt_pk_bf16_f32 v55, v58, v59\n" \
"v_cvt_pk_bf16_f32 v62, v64, v65\n" \
"v_mfma_f32_16x16x32_bf16 v[92:95], a[184:187], a[136:139], v[92:95]\n" \
"v_cvt_pk_bf16_f32 v63, v66, v67\n" \
"v_mul_f32_e64 v68, s70, v68\n" \
"v_mfma_f32_16x16x32_bf16 v[92:95], a[248:251], a[200:203], v[92:95]\n" \
"buffer_load_dwordx4 v20, s[80:83], s74 offen offset:1024 sc1 lds\n" \
"v_mfma_f32_16x16x32_bf16 v[96:99], a[188:191], a[136:139], v[96:99]\n" \
"ds_write_b128 v27, v[52:55] offset:0\n" \
"v_mfma_f32_16x16x32_bf16 v[96:99], a[252:255], a[200:203], v[96:99]\n" \
"v_mul_f32_e32 v69, s70, v69\n" \
"v_mul_f32_e32 v76, s70, v76\n" \
"v_mfma_f32_16x16x32_bf16 v[112:115], a[188:191], a[140:143], v[112:115]\n" \
"v_mul_f32_e32 v77, s70, v77\n" \
"v_mul_f32_e32 v70, s70, v70\n" \
"v_mfma_f32_16x16x32_bf16 v[112:115], a[252:255], a[204:207], v[112:115]\n" \
"buffer_load_dwordx4 v20, s[80:83], s74 offen offset:2048 sc1 lds\n" \
"v_mfma_f32_16x16x32_bf16 v[108:111], a[184:187], a[140:143], v[108:111]\n" \
"ds_write_b128 v27, v[60:63] offset:1024\n" \
"v_mfma_f32_16x16x32_bf16 v[108:111], a[248:251], a[204:207], v[108:111]\n" \
"v_mul_f32_e64 v71, s70, v71\n" \
"v_cvt_pk_bf16_f32 v68, v68, v69\n" \
"v_mfma_f32_16x16x32_bf16 v[104:107], a[180:183], a[140:143], v[104:107]\n" \
"v_mul_f32_e32 v78, s70, v78\n" \
"v_mul_f32_e32 v79, s70, v79\n" \
"v_mfma_f32_16x16x32_bf16 v[104:107], a[244:247], a[204:207], v[104:107]\n" \
"buffer_load_dwordx4 v20, s[80:83], s74 offen offset:3072 sc1 lds\n" \
"v_mfma_f32_16x16x32_bf16 v[100:103], a[176:179], a[140:143], v[100:103]\n" \
"s_sub_u32 m0, s31, 0x4000\n" \
"v_cvt_pk_bf16_f32 v76, v76, v77\n" \
"v_mfma_f32_16x16x32_bf16 v[100:103], a[240:243], a[204:207], v[100:103]\n" \
"v_mul_f32_e32 v72, s70, v72\n" \
"v_mul_f32_e32 v73, s70, v73\n" \
"v_mfma_f32_16x16x32_bf16 v[116:119], a[176:179], a[144:147], v[116:119]\n" \
"v_mul_f32_e32 v74, s70, v74\n" \
"v_mul_f32_e32 v75, s70, v75\n" \
"v_mfma_f32_16x16x32_bf16 v[116:119], a[240:243], a[208:211], v[116:119]\n" \
"buffer_load_dwordx4 v20, s[80:83], s75 offen offset:0 sc1 lds\n" \
"v_mfma_f32_16x16x32_bf16 v[120:123], a[180:183], a[144:147], v[120:123]\n" \
"v_cvt_pk_bf16_f32 v69, v70, v71\n" \
"v_mul_f32_e64 v80, s70, v80\n" \
"v_mfma_f32_16x16x32_bf16 v[120:123], a[244:247], a[208:211], v[120:123]\n" \
"v_mul_f32_e32 v81, s70, v81\n" \
"v_mul_f32_e32 v82, s70, v82\n" \
"v_mfma_f32_16x16x32_bf16 v[124:127], a[184:187], a[144:147], v[124:127]\n" \
"v_mul_f32_e64 v83, s70, v83\n" \
"v_cvt_pk_bf16_f32 v77, v78, v79\n" \
"v_mfma_f32_16x16x32_bf16 v[124:127], a[248:251], a[208:211], v[124:127]\n" \
"buffer_load_dwordx4 v20, s[80:83], s75 offen offset:1024 sc1 lds\n" \
"v_mfma_f32_16x16x32_bf16 v[128:131], a[188:191], a[144:147], v[128:131]\n" \
"v_cvt_pk_bf16_f32 v70, v72, v73\n" \
"v_cvt_pk_bf16_f32 v71, v74, v75\n" \
"v_mfma_f32_16x16x32_bf16 v[128:131], a[252:255], a[208:211], v[128:131]\n" \
"v_cvt_pk_bf16_f32 v78, v80, v81\n" \
"v_cvt_pk_bf16_f32 v79, v82, v83\n" \
"v_mfma_f32_16x16x32_bf16 v[144:147], a[188:191], a[148:151], v[144:147]\n" \
"ds_write_b128 v27, v[68:71] offset:4096\n" \
"v_mfma_f32_16x16x32_bf16 v[144:147], a[252:255], a[212:215], v[144:147]\n" \
"buffer_load_dwordx4 v20, s[80:83], s75 offen offset:2048 sc1 lds\n" \
"v_mfma_f32_16x16x32_bf16 v[140:143], a[184:187], a[148:151], v[140:143]\n" \
"v_cvt_pk_bf16_f32 v180, v180, v181\n" \
"v_accvgpr_read_b32 v50, a82\n" \
"v_mfma_f32_16x16x32_bf16 v[140:143], a[248:251], a[212:215], v[140:143]\n" \
"ds_write_b128 v27, v[76:79] offset:5120\n" \
"v_mfma_f32_16x16x32_bf16 v[136:139], a[180:183], a[148:151], v[136:139]\n" \
"v_accvgpr_read_b32 v51, a83\n" \
"v_cvt_pk_bf16_f32 v181, v182, v183\n" \
"v_mfma_f32_16x16x32_bf16 v[136:139], a[244:247], a[212:215], v[136:139]\n" \
"buffer_load_dwordx4 v20, s[80:83], s75 offen offset:3072 sc1 lds\n" \
"v_mfma_f32_16x16x32_bf16 v[132:135], a[176:179], a[148:151], v[132:135]\n" \
"s_mov_b32 m0, s53\n" \
"v_cvt_pk_bf16_f32 v182, v50, v51\n" \
"v_mfma_f32_16x16x32_bf16 v[132:135], a[240:243], a[212:215], v[132:135]\n" \
"v_mov_b32_e32 v183, 0x3f80\n" \
"v_cvt_pk_bf16_f32 v234, v244, v245\n" \
"v_mfma_f32_16x16x32_bf16 v[148:151], a[176:179], a[152:155], v[148:151]\n" \
"v_mov_b32_e32 v235, 0\n" \
"v_cvt_pk_bf16_f32 v54, v192, v193\n" \
"v_mfma_f32_16x16x32_bf16 v[148:151], a[240:243], a[216:219], v[148:151]\n" \
"buffer_load_dwordx4 v20, s[80:83], s76 offen offset:0 sc1 lds\n" \
"v_mfma_f32_16x16x32_bf16 v[152:155], a[180:183], a[152:155], v[152:155]\n" \
"v_cvt_pk_bf16_f32 v55, v194, v195\n" \
"v_cvt_pk_bf16_f32 v56, v208, v209\n" \
"v_mfma_f32_16x16x32_bf16 v[152:155], a[244:247], a[216:219], v[152:155]\n" \
"v_cvt_pk_bf16_f32 v57, v210, v211\n" \
"v_cvt_pk_bf16_f32 v58, v224, v225\n" \
"v_cvt_pk_bf16_f32 v59, v226, v227\n" \
"v_cvt_pk_bf16_f32 v60, v240, v241\n" \
"v_cvt_pk_bf16_f32 v61, v242, v243\n" \
"v_mfma_f32_16x16x32_bf16 v[32:35], v[180:183], v[184:187], 0\n" \
"buffer_load_dwordx4 v20, s[80:83], s76 offen offset:1024 sc1 lds\n" \
"v_mfma_f32_16x16x32_bf16 v[156:159], a[184:187], a[152:155], v[156:159]\n" \
"v_mfma_f32_16x16x32_bf16 v[192:195], v[188:191], v[184:187], 0\n" \
"v_mfma_f32_16x16x32_bf16 v[208:211], v[204:207], v[200:203], 0\n" \
"v_mfma_f32_16x16x32_bf16 v[36:39], v[188:191], v[200:203], 0\n" \
"v_mfma_f32_16x16x32_bf16 v[224:227], v[220:223], v[216:219], 0\n" \
"v_mfma_f32_16x16x32_bf16 v[248:251], v[204:207], v[216:219], 0\n" \
"v_mfma_f32_16x16x32_bf16 v[240:243], v[236:239], v[232:235], 0\n" \
"v_mfma_f32_16x16x32_bf16 v[64:67], v[220:223], v[232:235], 0\n" \
"s_mov_b32 s64, 0xaaaaaaaa\n" \
"s_mov_b32 s65, 0xaaaaaaaa\n" \
"v_mfma_f32_16x16x32_bf16 v[156:159], a[248:251], a[216:219], v[156:159]\n" \
"v_cndmask_b32_e64 v32, v32, v33, s[64:65]\n" \
"v_cndmask_b32_e64 v34, v34, v35, s[64:65]\n" \
"v_mfma_f32_16x16x32_bf16 v[160:163], a[188:191], a[152:155], v[160:163]\n" \
"s_mov_b32 s66, 0xfff0ffff\n" \
"s_mov_b32 s67, 0xf000ff00\n" \
"s_mov_b32 s68, 0xffc0fffc\n" \
"s_mov_b32 s69, 0xc000fc00\n" \
"v_cndmask_b32_e64 v192, v192, v193, s[64:65]\n" \
"v_add_f32_e64 v32, s47, v32\n" \
"v_cndmask_b32_e64 v194, v194, v195, s[64:65]\n" \
"v_add_f32_e64 v34, s47, v34\n" \
"v_cndmask_b32_e64 v208, v208, v209, s[64:65]\n" \
"v_cndmask_b32_e64 v36, v36, v37, s[64:65]\n" \
"v_cndmask_b32_e64 v210, v210, v211, s[64:65]\n" \
"v_cndmask_b32_e64 v38, v38, v39, s[64:65]\n" \
"v_cndmask_b32_e64 v224, v224, v225, s[64:65]\n" \
"v_cndmask_b32_e64 v248, v248, v249, s[64:65]\n" \
"v_cndmask_b32_e64 v226, v226, v227, s[64:65]\n" \
"v_cndmask_b32_e64 v250, v250, v251, s[64:65]\n" \
"v_cndmask_b32_e64 v240, v240, v241, s[64:65]\n" \
"v_cndmask_b32_e64 v64, v64, v65, s[64:65]\n" \
"v_cndmask_b32_e64 v242, v242, v243, s[64:65]\n" \
"v_cndmask_b32_e64 v66, v66, v67, s[64:65]\n" \
"v_cndmask_b32_e64 v192, v32, v192, s[66:67]\n" \
"v_cndmask_b32_e64 v194, v34, v194, s[68:69]\n" \
"v_cndmask_b32_e64 v208, v36, v208, s[66:67]\n" \
"v_cndmask_b32_e64 v210, v38, v210, s[68:69]\n" \
"v_cndmask_b32_e64 v224, v248, v224, s[66:67]\n" \
"v_cndmask_b32_e64 v226, v250, v226, s[68:69]\n" \
"v_cndmask_b32_e64 v240, v64, v240, s[66:67]\n" \
"v_cndmask_b32_e64 v242, v66, v242, s[68:69]\n" \
"v_mfma_f32_16x16x32_bf16 v[160:163], a[252:255], a[216:219], v[160:163]\n" \
"v_max_f32_e32 v193, v192, v194\n" \
"v_max_f32_e32 v209, v208, v210\n" \
"v_max_f32_e32 v225, v224, v226\n" \
"v_max_f32_e32 v241, v240, v242\n" \
"v_mul_f32_e32 v84, s70, v84\n" \
"v_permlane32_swap_b32_e32 v193, v225\n" \
"v_permlane32_swap_b32_e32 v209, v241\n" \
"v_mul_f32_e32 v85, s70, v85\n" \
"v_mul_f32_e32 v92, s70, v92\n" \
"v_mul_f32_e32 v93, s70, v93\n" \
"v_permlane32_swap_b32_e32 v196, v228\n" \
"v_permlane32_swap_b32_e32 v212, v244\n" \
"v_max_f32_e32 v193, v193, v225\n" \
"v_max_f32_e32 v209, v209, v241\n" \
"v_accvgpr_read_b32 v48, a80\n" \
"v_accvgpr_read_b32 v49, a81\n" \
"v_mul_f32_e32 v86, s70, v86\n" \
"v_mul_f32_e32 v87, s70, v87\n" \
"v_cvt_pk_bf16_f32 v84, v84, v85\n" \
"v_mul_f32_e32 v94, s70, v94\n" \
"v_mul_f32_e32 v95, s70, v95\n" \
"v_cvt_pk_bf16_f32 v92, v92, v93\n" \
"v_permlane16_swap_b32_e32 v228, v244\n" \
"v_permlane16_swap_b32_e32 v193, v209\n" \
"v_mfma_f32_16x16x32_bf16 v[176:179], a[188:191], a[156:159], v[176:179]\n" \
"s_mov_b32 s72, 0x3eae1a93\n" \
"v_mul_f32_e32 v88, s70, v88\n" \
"v_mul_f32_e32 v89, s70, v89\n" \
"v_mul_f32_e32 v90, s70, v90\n" \
"v_mul_f32_e32 v91, s70, v91\n" \
"v_cvt_pk_bf16_f32 v85, v86, v87\n" \
"v_mul_f32_e32 v96, s70, v96\n" \
"v_mul_f32_e32 v97, s70, v97\n" \
"v_mul_f32_e32 v98, s70, v98\n" \
"v_mul_f32_e32 v99, s70, v99\n" \
"v_cvt_pk_bf16_f32 v93, v94, v95\n" \
"v_mul_f32_e32 v45, 0xbfb8aa3b, v228\n" \
"v_mfma_f32_16x16x16_bf16 v[48:51], v[182:183], v[46:47], 0\n" \
"v_mfma_f32_16x16x16_bf16 v[196:199], v[190:191], v[46:47], 0\n" \
"v_mfma_f32_16x16x16_bf16 v[212:215], v[206:207], v[46:47], 0\n" \
"v_max_f32_e64 v193, v193, v209\n" \
"v_cvt_pk_bf16_f32 v86, v88, v89\n" \
"v_cvt_pk_bf16_f32 v87, v90, v91\n" \
"v_cvt_pk_bf16_f32 v94, v96, v97\n" \
"v_cvt_pk_bf16_f32 v95, v98, v99\n" \
"v_mfma_f32_16x16x16_bf16 v[228:231], v[222:223], v[46:47], 0\n" \
"v_mul_f32_e32 v193, s72, v193\n" \
"v_mov_b32_e32 v209, v193\n" \
"ds_write_b128 v27, v[84:87] offset:8192\n" \
"v_mfma_f32_16x16x16_bf16 v[244:247], v[238:239], v[46:47], 0\n" \
"v_permlane16_swap_b32_e32 v193, v209\n" \
"s_mov_b32 s71, 0x3e0293ee\n" \
"v_mfma_f32_16x16x32_bf16 v[176:179], a[252:255], a[220:223], v[176:179]\n" \
"v_mov_b32_e32 v225, v193\n" \
"v_mov_b32_e32 v241, v209\n" \
"v_mul_f32_e32 v100, s70, v100\n" \
"v_mul_f32_e64 v101, s70, v101\n" \
"ds_write_b128 v27, v[92:95] offset:9216\n" \
"v_mul_f32_e32 v108, s70, v108\n" \
"v_mul_f32_e32 v109, s70, v109\n" \
"v_pk_mul_f32 v[116:117], v[116:117], s[70:71]\n" \
"v_pk_mul_f32 v[124:125], v[124:125], s[70:71]\n" \
"v_pk_mul_f32 v[132:133], v[132:133], s[70:71]\n" \
"v_pk_mul_f32 v[140:141], v[140:141], s[70:71]\n" \
"v_pk_mul_f32 v[148:149], v[148:149], s[70:71]\n" \
"v_pk_mul_f32 v[156:157], v[156:157], s[70:71]\n" \
"v_permlane32_swap_b32_e32 v193, v225\n" \
"v_permlane32_swap_b32_e32 v209, v241\n" \
"v_mul_f32_e32 v102, s70, v102\n" \
"v_mul_f32_e32 v103, s70, v103\n" \
"v_cvt_pk_bf16_f32 v100, v100, v101\n" \
"v_mul_f32_e32 v110, s70, v110\n" \
"v_mul_f32_e32 v111, s70, v111\n" \
"v_cvt_pk_bf16_f32 v108, v108, v109\n" \
"v_pk_mul_f32 v[118:119], v[118:119], s[70:71]\n" \
"v_cvt_pk_bf16_f32 v116, v116, v117\n" \
"v_pk_mul_f32 v[126:127], v[126:127], s[70:71]\n" \
"v_cvt_pk_bf16_f32 v124, v124, v125\n" \
"v_pk_mul_f32 v[134:135], v[134:135], s[70:71]\n" \
"v_cvt_pk_bf16_f32 v132, v132, v133\n" \
"v_pk_mul_f32 v[142:143], v[142:143], s[70:71]\n" \
"v_cvt_pk_bf16_f32 v140, v140, v141\n" \
"v_pk_mul_f32 v[150:151], v[150:151], s[70:71]\n" \
"v_cvt_pk_bf16_f32 v148, v148, v149\n" \
"v_pk_mul_f32 v[158:159], v[158:159], s[70:71]\n" \
"v_cvt_pk_bf16_f32 v156, v156, v157\n" \
"v_fma_f32 v192, v192, s72, -v193\n" \
"v_fma_f32 v194, v194, s72, -v193\n" \
"v_fma_f32 v208, v208, s72, -v209\n" \
"v_fma_f32 v210, v210, s72, -v209\n" \
"v_mul_f32_e32 v104, s70, v104\n" \
"v_mul_f32_e32 v105, s70, v105\n" \
"v_mul_f32_e32 v106, s70, v106\n" \
"v_mul_f32_e32 v107, s70, v107\n" \
"v_cvt_pk_bf16_f32 v101, v102, v103\n" \
"v_mul_f32_e32 v112, s70, v112\n" \
"v_mul_f32_e32 v113, s70, v113\n" \
"v_mul_f32_e32 v114, s70, v114\n" \
"v_mul_f32_e32 v115, s70, v115\n" \
"v_cvt_pk_bf16_f32 v109, v110, v111\n" \
"v_pk_mul_f32 v[120:121], v[120:121], s[70:71]\n" \
"v_pk_mul_f32 v[122:123], v[122:123], s[70:71]\n" \
"v_cvt_pk_bf16_f32 v117, v118, v119\n" \
"v_pk_mul_f32 v[128:129], v[128:129], s[70:71]\n" \
"v_pk_mul_f32 v[130:131], v[130:131], s[70:71]\n" \
"v_cvt_pk_bf16_f32 v125, v126, v127\n" \
"v_pk_mul_f32 v[136:137], v[136:137], s[70:71]\n" \
"v_pk_mul_f32 v[138:139], v[138:139], s[70:71]\n" \
"v_cvt_pk_bf16_f32 v133, v134, v135\n" \
"v_pk_mul_f32 v[144:145], v[144:145], s[70:71]\n" \
"v_pk_mul_f32 v[146:147], v[146:147], s[70:71]\n" \
"v_cvt_pk_bf16_f32 v141, v142, v143\n" \
"v_pk_mul_f32 v[152:153], v[152:153], s[70:71]\n" \
"v_pk_mul_f32 v[154:155], v[154:155], s[70:71]\n" \
"v_cvt_pk_bf16_f32 v149, v150, v151\n" \
"v_pk_mul_f32 v[160:161], v[160:161], s[70:71]\n" \
"v_pk_mul_f32 v[162:163], v[162:163], s[70:71]\n" \
"v_cvt_pk_bf16_f32 v157, v158, v159\n" \
"v_pk_mul_f32 v[176:177], v[176:177], s[70:71]\n" \
"v_pk_mul_f32 v[178:179], v[178:179], s[70:71]\n" \
"v_exp_f32_e32 v192, v192\n" \
"v_exp_f32_e32 v194, v194\n" \
"v_exp_f32_e32 v208, v208\n" \
"v_exp_f32_e32 v210, v210\n" \
"v_fma_f32 v224, v224, s72, -v225\n" \
"v_fma_f32 v226, v226, s72, -v225\n" \
"v_mfma_f32_16x16x32_bf16 v[172:175], a[184:187], a[156:159], v[172:175]\n" \
"v_cvt_pk_bf16_f32 v102, v104, v105\n" \
"v_cvt_pk_bf16_f32 v103, v106, v107\n" \
"v_cvt_pk_bf16_f32 v110, v112, v113\n" \
"v_cvt_pk_bf16_f32 v111, v114, v115\n" \
"v_cvt_pk_bf16_f32 v118, v120, v121\n" \
"v_cvt_pk_bf16_f32 v119, v122, v123\n" \
"v_cvt_pk_bf16_f32 v126, v128, v129\n" \
"v_cvt_pk_bf16_f32 v127, v130, v131\n" \
"v_cvt_pk_bf16_f32 v134, v136, v137\n" \
"v_cvt_pk_bf16_f32 v135, v138, v139\n" \
"v_cvt_pk_bf16_f32 v142, v144, v145\n" \
"v_cvt_pk_bf16_f32 v143, v146, v147\n" \
"v_cvt_pk_bf16_f32 v150, v152, v153\n" \
"v_cvt_pk_bf16_f32 v151, v154, v155\n" \
"v_cvt_pk_bf16_f32 v158, v160, v161\n" \
"v_cvt_pk_bf16_f32 v159, v162, v163\n" \
"v_exp_f32_e32 v224, v224\n" \
"v_exp_f32_e32 v226, v226\n" \
"v_fma_f32 v240, v240, s72, -v241\n" \
"v_fma_f32 v242, v242, s72, -v241\n" \
"ds_write_b128 v27, v[100:103] offset:12288\n" \
"v_cvt_pk_bf16_f32 v192, v192, v194\n" \
"v_cvt_pk_bf16_f32 v208, v208, v210\n" \
"v_exp_f32_e32 v240, v240\n" \
"v_exp_f32_e32 v242, v242\n" \
"v_cvt_pk_bf16_f32 v68, v48, v49\n" \
"v_cvt_pk_bf16_f32 v69, v50, v51\n" \
"v_cvt_pk_bf16_f32 v70, v196, v197\n" \
"v_cvt_pk_bf16_f32 v71, v198, v199\n" \
"ds_write_b128 v27, v[108:111] offset:13312\n" \
"v_cvt_pk_bf16_f32 v72, v212, v213\n" \
"v_cvt_pk_bf16_f32 v73, v214, v215\n" \
"v_perm_b32 v184, v192, v192, v40\n" \
"v_perm_b32 v185, v192, v192, v41\n" \
"v_perm_b32 v186, v192, v192, v42\n" \
"v_perm_b32 v187, v192, v192, v43\n" \
"v_perm_b32 v200, v208, v208, v40\n" \
"v_perm_b32 v201, v208, v208, v41\n" \
"v_perm_b32 v202, v208, v208, v42\n" \
"v_perm_b32 v203, v208, v208, v43\n" \
"ds_write_b128 v21, v[116:119] offset:16384\n" \
"v_cvt_pk_bf16_f32 v224, v224, v226\n" \
"v_cvt_pk_bf16_f32 v74, v228, v229\n" \
"v_cvt_pk_bf16_f32 v75, v230, v231\n" \
"v_mfma_f32_16x16x32_bf16 v[32:35], v[68:71], v[184:187], 0\n" \
"v_mfma_f32_16x16x32_bf16 v[36:39], v[70:73], v[200:203], 0\n" \
"v_perm_b32 v216, v224, v224, v40\n" \
"v_perm_b32 v217, v224, v224, v41\n" \
"ds_write_b128 v21, v[124:127] offset:17408\n" \
"v_perm_b32 v218, v224, v224, v42\n" \
"v_perm_b32 v219, v224, v224, v43\n" \
"v_cvt_pk_bf16_f32 v240, v240, v242\n" \
"v_mfma_f32_16x16x32_bf16 v[172:175], a[248:251], a[220:223], v[172:175]\n" \
"v_cvt_pk_bf16_f32 v76, v244, v245\n" \
"v_cvt_pk_bf16_f32 v77, v246, v247\n" \
"v_mfma_f32_16x16x32_bf16 v[248:251], v[72:75], v[216:219], 0\n" \
"v_perm_b32 v232, v240, v240, v40\n" \
"v_perm_b32 v233, v240, v240, v41\n" \
"ds_write_b128 v21, v[132:135] offset:20480\n" \
"v_perm_b32 v234, v240, v240, v42\n" \
"v_perm_b32 v235, v240, v240, v43\n" \
"v_pk_mul_f32 v[172:173], v[172:173], s[70:71]\n" \
"v_pk_mul_f32 v[174:175], v[174:175], s[70:71]\n" \
"v_cvt_pk_bf16_f32 v172, v172, v173\n" \
"v_cvt_pk_bf16_f32 v173, v174, v175\n" \
"v_cvt_pk_bf16_f32 v174, v176, v177\n" \
"v_cvt_pk_bf16_f32 v175, v178, v179\n" \
"v_mfma_f32_16x16x32_bf16 v[64:67], v[74:77], v[232:235], 0\n" \
"ds_write_b128 v21, v[140:143] offset:21504\n" \
"v_mfma_f32_16x16x32_bf16 v[168:171], a[180:183], a[156:159], v[168:171]\n" \
"v_cvt_pk_bf16_f32 v52, v252, v253\n" \
"v_cvt_pk_bf16_f32 v53, v254, v255\n" \
"v_mfma_f32_16x16x32_bf16 v[168:171], a[244:247], a[220:223], v[168:171]\n" \
"v_exp_f32_e64 v45, v45\n" \
"v_mfma_f32_16x16x32_bf16 v[192:195], v[52:55], v[184:187], 0\n" \
"ds_write_b128 v21, v[148:151] offset:24576\n" \
"v_mfma_f32_16x16x32_bf16 v[208:211], v[54:57], v[200:203], 0\n" \
"v_mfma_f32_16x16x32_bf16 v[224:227], v[56:59], v[216:219], 0\n" \
"v_mfma_f32_16x16x32_bf16 v[240:243], v[58:61], v[232:235], 0\n" \
"ds_write_b128 v21, v[156:159] offset:25600\n" \
"v_mov_b32_dpp v34, v38 quad_perm:[0,1,2,3] row_mask:0x2 bank_mask:0xf\n" \
"v_pk_mul_f32 v[168:169], v[168:169], s[70:71]\n" \
"v_pk_mul_f32 v[170:171], v[170:171], s[70:71]\n" \
"v_mov_b32_dpp v34, v250 quad_perm:[0,1,2,3] row_mask:0x4 bank_mask:0xf\n" \
"v_add_f32_e64 v45, 1.0, v45\n" \
"v_mfma_f32_16x16x32_bf16 v[164:167], a[176:179], a[156:159], v[164:167]\n" \
"v_mov_b32_dpp v34, v66 quad_perm:[0,1,2,3] row_mask:0x8 bank_mask:0xf\n" \
"v_mov_b32_dpp v32, v36 quad_perm:[0,1,2,3] row_mask:0x2 bank_mask:0xf\n" \
"v_rcp_f32_e64 v45, v45\n" \
"ds_write_b128 v21, v[172:175] offset:29696\n" \
"v_mov_b32_dpp v33, v37 quad_perm:[0,1,2,3] row_mask:0x2 bank_mask:0xf\n" \
"v_rcp_f32_e64 v34, v34\n" \
"v_mov_b32_dpp v32, v248 quad_perm:[0,1,2,3] row_mask:0x4 bank_mask:0xf\n" \
"v_mov_b32_dpp v33, v249 quad_perm:[0,1,2,3] row_mask:0x4 bank_mask:0xf\n" \
"v_mul_f32_e64 v34, v45, v34\n" \
"v_mov_b32_dpp v32, v64 quad_perm:[0,1,2,3] row_mask:0x8 bank_mask:0xf\n" \
"v_mov_b32_dpp v33, v65 quad_perm:[0,1,2,3] row_mask:0x8 bank_mask:0xf\n" \
"v_mul_f32_e32 v32, v34, v32\n" \
"v_mul_f32_e32 v33, v34, v33\n" \
"v_mov_b32_e64 v38, v34\n" \
"v_mfma_f32_16x16x32_bf16 v[164:167], a[240:243], a[220:223], v[164:167]\n" \
"v_sub_f32_e32 v44, 1.0, v45\n" \
"v_permlane16_swap_b32_e32 v34, v38\n" \
"v_mov_b32_e32 v250, v34\n" \
"v_mov_b32_e32 v66, v38\n" \
"v_cvt_pk_bf16_f32 v32, v32, v33\n" \
"v_permlane32_swap_b32_e32 v34, v250\n" \
"v_permlane32_swap_b32_e32 v38, v66\n" \
"v_mul_f32_e64 v192, v34, v192\n" \
"v_pk_mul_f32 v[164:165], v[164:165], s[70:71]\n" \
"v_pk_mul_f32 v[166:167], v[166:167], s[70:71]\n" \
"v_cvt_pk_bf16_f32 v164, v164, v165\n" \
"v_cvt_pk_bf16_f32 v165, v166, v167\n" \
"v_cvt_pk_bf16_f32 v166, v168, v169\n" \
"v_cvt_pk_bf16_f32 v167, v170, v171\n" \
"ds_write_b128 v21, v[164:167] offset:28672\n" \
"v_mul_f32_e32 v193, v34, v193\n" \
"v_mul_f32_e32 v208, v38, v208\n" \
"v_mul_f32_e32 v209, v38, v209\n" \
"v_mul_f32_e32 v224, v250, v224\n" \
"v_mul_f32_e32 v225, v250, v225\n" \
"v_mul_f32_e32 v240, v66, v240\n" \
"v_mul_f32_e32 v241, v66, v241\n" \
"v_mul_f32_e32 v194, v34, v194\n" \
"v_mul_f32_e64 v195, v34, v195\n" \
"v_cvt_pk_bf16_f32 v192, v192, v193\n" \
"v_mul_f32_e32 v210, v38, v210\n" \
"v_mul_f32_e32 v211, v38, v211\n" \
"v_cvt_pk_bf16_f32 v208, v208, v209\n" \
"v_mul_f32_e32 v226, v250, v226\n" \
"v_mul_f32_e32 v227, v250, v227\n" \
"v_cvt_pk_bf16_f32 v224, v224, v225\n" \
"v_mul_f32_e32 v242, v66, v242\n" \
"v_mul_f32_e32 v243, v66, v243\n" \
"v_cvt_pk_bf16_f32 v240, v240, v241\n" \
"v_cvt_pk_bf16_f32 v193, v194, v195\n" \
"s_add_u32 s84, s48, 0x0\n" \
"s_nop 0\n" \
"v_cvt_pk_bf16_f32 v209, v210, v211\n" \
"s_add_u32 s85, s48, 0x24800\n" \
"v_cvt_pk_bf16_f32 v225, v226, v227\n" \
"s_add_u32 s86, s48, 0x49000\n" \
"v_cvt_pk_bf16_f32 v241, v242, v243\n" \
"s_add_u32 s87, s48, 0x6d800\n" \
"ds_write_b32 v26, v44\n" \
"buffer_store_dword v32, v25, s[80:83], s49 offen sc1\n" \
"buffer_store_dwordx2 v[192:193], v24, s[80:83], s84 offen sc1\n" \
"buffer_store_dwordx2 v[208:209], v24, s[80:83], s85 offen sc1\n" \
"buffer_store_dwordx2 v[224:225], v24, s[80:83], s86 offen sc1\n" \
"buffer_store_dwordx2 v[240:241], v24, s[80:83], s87 offen sc1\n" \
"s_branch L_end_%=\n" \
"L_poll60_%=:\n" \
"s_mov_b32 s84, 0\n" \
"L_pl60_%=:\n" \
"s_sleep 1\n" \
"v_bfe_u32 v44, v20, 2, 5\n" \
"buffer_load_dword v44, v44, s[80:83], s38 offen offset:128 sc1\n" \
"s_waitcnt vmcnt(0)\n" \
"v_cmp_ne_u32_e32 vcc, s62, v44\n" \
"s_add_u32 s84, s84, 1\n" \
"s_nop 3\n" \
"s_cbranch_vccz L_pollr60_%=\n" \
"s_cmp_gt_u32 s84, 0x400000\n" \
"s_cbranch_scc0 L_pl60_%=\n" \
"s_branch L_pollr60_%=\n" \
"L_poll160_%=:\n" \
"s_mov_b32 s84, 0\n" \
"L_pl160_%=:\n" \
"s_sleep 1\n" \
"v_bfe_u32 v44, v20, 2, 5\n" \
"buffer_load_dword v44, v44, s[80:83], s38 offen offset:0 sc1\n" \
"s_waitcnt vmcnt(0)\n" \
"v_cmp_ne_u32_e32 vcc, s62, v44\n" \
"s_add_u32 s84, s84, 1\n" \
"s_nop 3\n" \
"s_cbranch_vccz L_pollr160_%=\n" \
"s_cmp_gt_u32 s84, 0x400000\n" \
"s_cbranch_scc0 L_pl160_%=\n" \
"s_branch L_pollr160_%=\n" \
".fill (L_pb + 42548 - .) / 4, 4, 0xBF800000\n" \
"L_w1_%=:\n" \
"s_waitcnt vmcnt(19)\n" \
"s_barrier\n" \
"s_nop 0\n" \
"ds_read_b64_tr_b16 a[160:161], v32 offset:0\n" \
"ds_read_b64_tr_b16 a[162:163], v32 offset:1024\n" \
"ds_read_b128 a[128:131], v28 offset:16384\n" \
"ds_read_b64_tr_b16 a[224:225], v32 offset:8192\n" \
"ds_read_b64_tr_b16 a[226:227], v32 offset:9216\n" \
"ds_read_b128 a[192:195], v29 offset:16384\n" \
"ds_read_b128 a[132:135], v28 offset:18432\n" \
"ds_read_b128 a[196:199], v29 offset:18432\n" \
"ds_read_b64_tr_b16 a[164:165], v33 offset:0\n" \
"ds_read_b64_tr_b16 a[166:167], v33 offset:1024\n" \
"ds_read_b64_tr_b16 a[228:229], v33 offset:8192\n" \
"ds_read_b64_tr_b16 a[230:231], v33 offset:9216\n" \
"ds_read_b128 a[136:139], v28 offset:20480\n" \
"ds_read_b128 a[200:203], v29 offset:20480\n" \
"s_waitcnt lgkmcnt(11)\n" \
"v_mfma_f32_16x16x32_bf16 a[0:3], a[128:131], a[160:163], 0\n" \
"ds_read_b64_tr_b16 a[168:169], v34 offset:0\n" \
"s_movk_i32 s5, 0xd80\n" \
"s_waitcnt lgkmcnt(9)\n" \
"v_mfma_f32_16x16x32_bf16 a[0:3], a[192:195], a[224:227], a[0:3]\n" \
"ds_read_b64_tr_b16 a[170:171], v34 offset:1024\n" \
"s_lshr_b32 s6, s2, 5\n" \
"s_waitcnt lgkmcnt(9)\n" \
"v_mfma_f32_16x16x32_bf16 a[16:19], a[132:135], a[160:163], 0\n" \
"ds_read_b64_tr_b16 a[232:233], v34 offset:8192\n" \
"v_lshrrev_b32_e32 v212, 4, v20\n" \
"s_waitcnt lgkmcnt(9)\n" \
"v_mfma_f32_16x16x32_bf16 a[16:19], a[196:199], a[224:227], a[16:19]\n" \
"ds_read_b64_tr_b16 a[234:235], v34 offset:9216\n" \
"v_lshl_or_b32 v215, s3, 6, v212\n" \
"s_waitcnt lgkmcnt(8)\n" \
"v_mfma_f32_16x16x32_bf16 a[20:23], a[132:135], a[164:167], 0\n" \
"ds_read_b128 a[140:143], v28 offset:22528\n" \
"v_mul_u32_u24_e32 v216, 0x2aab, v215\n" \
"s_waitcnt lgkmcnt(7)\n" \
"v_mfma_f32_16x16x32_bf16 a[20:23], a[196:199], a[228:231], a[20:23]\n" \
"ds_read_b128 a[204:207], v29 offset:22528\n" \
"v_lshrrev_b32_e32 v216, 16, v216\n" \
"v_mfma_f32_16x16x32_bf16 a[4:7], a[128:131], a[164:167], 0\n" \
"ds_read_b64_tr_b16 a[172:173], v35 offset:0\n" \
"v_mad_i32_i24 v217, v216, -6, v215\n" \
"v_mfma_f32_16x16x32_bf16 a[4:7], a[192:195], a[228:231], a[4:7]\n" \
"ds_read_b64_tr_b16 a[174:175], v35 offset:1024\n" \
"v_add_u32_e32 v218, 0xfffffe50, v215\n" \
"s_waitcnt lgkmcnt(9)\n" \
"v_mfma_f32_16x16x32_bf16 a[32:35], a[136:139], a[160:163], 0\n" \
"ds_read_b64_tr_b16 a[236:237], v35 offset:8192\n" \
"s_mul_i32 s35, s6, 0x48\n" \
"s_waitcnt lgkmcnt(9)\n" \
"v_mfma_f32_16x16x32_bf16 a[32:35], a[200:203], a[224:227], a[32:35]\n" \
"ds_read_b64_tr_b16 a[238:239], v35 offset:9216\n" \
"v_lshrrev_b32_e64 v219, 1, v217\n" \
"v_mfma_f32_16x16x32_bf16 a[36:39], a[136:139], a[164:167], 0\n" \
"ds_read_b128 a[144:147], v28 offset:0\n" \
"v_add_u32_e64 v218, s35, v216\n" \
"v_mfma_f32_16x16x32_bf16 a[36:39], a[200:203], a[228:231], a[36:39]\n" \
"ds_read_b128 a[208:211], v29 offset:0\n" \
"v_mul_u32_u24_e32 v219, 0x480, v219\n" \
"s_waitcnt lgkmcnt(10)\n" \
"v_mfma_f32_16x16x32_bf16 a[40:43], a[136:139], a[168:171], 0\n" \
"ds_read_b128 a[148:151], v28 offset:2048\n" \
"v_and_b32_e32 v214, 16, v20\n" \
"s_waitcnt lgkmcnt(9)\n" \
"v_mfma_f32_16x16x32_bf16 a[40:43], a[200:203], a[232:235], a[40:43]\n" \
"ds_read_b128 a[212:215], v29 offset:2048\n" \
"v_mad_u32_u24 v219, v218, s5, v219\n" \
"v_mfma_f32_16x16x32_bf16 a[24:27], a[132:135], a[168:171], 0\n" \
"ds_read_b128 a[152:155], v28 offset:4096\n" \
"v_or_b32_e32 v192, v219, v214\n" \
"v_mfma_f32_16x16x32_bf16 a[24:27], a[196:199], a[232:235], a[24:27]\n" \
"ds_read_b128 a[216:219], v29 offset:4096\n" \
"v_mov_b32_e64 v220, 2.0\n" \
"v_mfma_f32_16x16x32_bf16 a[8:11], a[128:131], a[168:171], 0\n" \
"ds_read_b128 a[156:159], v28 offset:6144\n" \
"v_mov_b32_e64 v222, 7\n" \
"v_mfma_f32_16x16x32_bf16 a[8:11], a[192:195], a[232:235], a[8:11]\n" \
"ds_read_b128 a[220:223], v29 offset:6144\n" \
"s_mov_b32 m0, s32\n" \
"s_waitcnt lgkmcnt(13)\n" \
"v_mfma_f32_16x16x32_bf16 a[48:51], a[140:143], a[160:163], 0\n" \
"s_mov_b32 exec_lo, 0xff000000\n" \
"s_mov_b32 exec_hi, 0x0\n" \
"global_load_lds_dword v[184:185], off\n" \
"s_mov_b64 exec, -1\n" \
"s_waitcnt lgkmcnt(12)\n" \
"v_mfma_f32_16x16x32_bf16 a[48:51], a[204:207], a[224:227], a[48:51]\n" \
"ds_read_b64_tr_b16 a[176:177], v36 offset:0\n" \
"v_lshlrev_b32_e32 v220, 7, v218\n" \
"v_mfma_f32_16x16x32_bf16 a[52:55], a[140:143], a[164:167], 0\n" \
"ds_read_b64_tr_b16 a[178:179], v36 offset:1024\n" \
"v_lshrrev_b32_e64 v218, 1, v218\n" \
"v_mfma_f32_16x16x32_bf16 a[52:55], a[204:207], a[228:231], a[52:55]\n" \
"s_mov_b32 m0, s32\n" \
"ds_read_b64_tr_b16 a[240:241], v36 offset:8192\n" \
"v_mfma_f32_16x16x32_bf16 a[56:59], a[140:143], a[168:171], 0\n" \
"s_mov_b32 exec_lo, 0xffff\n" \
"s_mov_b32 exec_hi, 0x0\n" \
"global_load_lds_dword v[186:187], off\n" \
"s_mov_b64 exec, -1\n" \
"v_mfma_f32_16x16x32_bf16 a[56:59], a[204:207], a[232:235], a[56:59]\n" \
"ds_read_b64_tr_b16 a[242:243], v36 offset:9216\n" \
"v_and_b32_e32 v218, 4, v218\n" \
"s_waitcnt lgkmcnt(14)\n" \
"v_mfma_f32_16x16x32_bf16 a[60:63], a[140:143], a[172:175], 0\n" \
"ds_read_b64_tr_b16 a[180:181], v37 offset:0\n" \
"v_and_b32_e32 v216, 2, v216\n" \
"s_waitcnt lgkmcnt(13)\n" \
"s_nop 0\n" \
"v_mfma_f32_16x16x32_bf16 a[60:63], a[204:207], a[236:239], a[60:63]\n" \
"ds_read_b64_tr_b16 a[182:183], v37 offset:1024\n" \
"s_mul_i32 s7, s6, 0x2400\n" \
"v_mfma_f32_16x16x32_bf16 a[44:47], a[136:139], a[172:175], 0\n" \
"ds_read_b64_tr_b16 a[244:245], v37 offset:8192\n" \
"s_sub_i32 s34, 0x22000, s7\n" \
"v_mfma_f32_16x16x32_bf16 a[44:47], a[200:203], a[236:239], a[44:47]\n" \
"ds_read_b64_tr_b16 a[246:247], v37 offset:9216\n" \
"v_bitop3_b32 v216, v217, v218, v216 bitop3:0x1e\n" \
"v_mfma_f32_16x16x32_bf16 a[28:31], a[132:135], a[172:175], 0\n" \
"ds_read_b64_tr_b16 a[184:185], v38 offset:0\n" \
"v_or_b32_e32 v213, 0x24400, v20\n" \
"v_mfma_f32_16x16x32_bf16 a[28:31], a[196:199], a[236:239], a[28:31]\n" \
"ds_read_b64_tr_b16 a[186:187], v38 offset:1024\n" \
"v_lshlrev_b32_e64 v216, 4, v216\n" \
"v_mfma_f32_16x16x32_bf16 a[12:15], a[128:131], a[172:175], 0\n" \
"v_add3_u32 v196, v220, s34, v216\n" \
"v_mov_b32_e64 v216, v213\n" \
"v_mfma_f32_16x16x32_bf16 a[12:15], a[192:195], a[236:239], a[12:15]\n" \
"s_movk_i32 s36, 0x1b0\n" \
"v_or_b32_e32 v216, 0x100, v215\n" \
"s_waitcnt lgkmcnt(15)\n" \
"v_mfma_f32_16x16x32_bf16 a[76:79], a[144:147], a[172:175], 0\n" \
"ds_read_b64_tr_b16 a[248:249], v38 offset:8192\n" \
"v_mul_u32_u24_e32 v217, 0x2aab, v216\n" \
"v_mfma_f32_16x16x32_bf16 a[76:79], a[208:211], a[236:239], a[76:79]\n" \
"v_add_u32_e32 v215, 0xffffff50, v215\n" \
"v_lshrrev_b32_e64 v217, 16, v217\n" \
"v_mfma_f32_16x16x32_bf16 a[72:75], a[144:147], a[168:171], 0\n" \
"v_mov_b32_e64 v220, v215\n" \
"v_mad_i32_i24 v218, v217, -6, v216\n" \
"v_mfma_f32_16x16x32_bf16 a[72:75], a[208:211], a[232:235], a[72:75]\n" \
"ds_read_b64_tr_b16 a[250:251], v38 offset:9216\n" \
"v_cmp_gt_u32_e64 vcc, s36, v216\n" \
"v_mfma_f32_16x16x32_bf16 a[68:71], a[144:147], a[164:167], 0\n" \
"v_mov_b32_e32 v221, 7\n" \
"v_lshrrev_b32_e32 v222, 1, v218\n" \
"v_mfma_f32_16x16x32_bf16 a[68:71], a[208:211], a[228:231], a[68:71]\n" \
"v_cndmask_b32_e32 v217, v220, v217, vcc\n" \
"s_movk_i32 s4, 0x48\n" \
"v_mfma_f32_16x16x32_bf16 a[64:67], a[144:147], a[160:163], 0\n" \
"v_add_u32_e64 v220, s35, v217\n" \
"v_mul_u32_u24_e32 v222, 0x480, v222\n" \
"v_mfma_f32_16x16x32_bf16 a[64:67], a[208:211], a[224:227], a[64:67]\n" \
"ds_read_b64_tr_b16 a[188:189], v39 offset:0\n" \
"v_mad_u32_u24 v222, v220, s5, v222\n" \
"v_mfma_f32_16x16x32_bf16 a[80:83], a[148:151], a[160:163], 0\n" \
"v_cmp_gt_u32_e64 s[4:5], s4, v215\n" \
"v_cndmask_b32_e32 v215, v221, v218, vcc\n" \
"s_waitcnt lgkmcnt(15)\n" \
"v_mfma_f32_16x16x32_bf16 a[80:83], a[212:215], a[224:227], a[80:83]\n" \
"v_lshrrev_b32_e32 v218, 1, v220\n" \
"v_or_b32_e32 v214, v222, v214\n" \
"v_mfma_f32_16x16x32_bf16 a[84:87], a[148:151], a[164:167], 0\n" \
"ds_read_b64_tr_b16 a[190:191], v39 offset:1024\n" \
"v_mov_b32_e64 v222, 2.0\n" \
"v_mfma_f32_16x16x32_bf16 a[84:87], a[212:215], a[228:231], a[84:87]\n" \
"v_and_b32_e32 v218, 4, v218\n" \
"v_and_b32_e32 v217, 2, v217\n" \
"v_mfma_f32_16x16x32_bf16 a[88:91], a[148:151], a[168:171], 0\n" \
"v_bitop3_b32 v215, v215, v218, v217 bitop3:0x1e\n" \
"v_lshlrev_b32_e64 v223, 1, v220\n" \
"v_mfma_f32_16x16x32_bf16 a[88:91], a[212:215], a[232:235], a[88:91]\n" \
"ds_read_b64_tr_b16 a[252:253], v39 offset:8192\n" \
"v_mov_b32_e64 v224, 2.0\n" \
"v_mfma_f32_16x16x32_bf16 a[92:95], a[148:151], a[172:175], 0\n" \
"v_cndmask_b32_e32 v193, v222, v214, vcc\n" \
"v_lshlrev_b32_e32 v214, 7, v220\n" \
"v_mfma_f32_16x16x32_bf16 a[92:95], a[212:215], a[236:239], a[92:95]\n" \
"v_lshlrev_b32_e32 v215, 4, v215\n" \
"s_mul_i32 s39, s3, 18\n" \
"v_mfma_f32_16x16x32_bf16 a[108:111], a[152:155], a[172:175], 0\n" \
"ds_read_b64_tr_b16 a[254:255], v39 offset:9216\n" \
"v_add3_u32 v214, v214, s34, v215\n" \
"v_mfma_f32_16x16x32_bf16 a[108:111], a[216:219], a[236:239], a[108:111]\n" \
"v_cndmask_b32_e64 v195, v224, v223, s[4:5]\n" \
"s_movk_i32 s4, 0x1f8\n" \
"v_mfma_f32_16x16x32_bf16 a[104:107], a[152:155], a[168:171], 0\n" \
"v_mov_b32_e32 v215, v213\n" \
"s_add_i32 s35, s35, s39\n" \
"v_mfma_f32_16x16x32_bf16 a[104:107], a[216:219], a[232:235], a[104:107]\n" \
"v_cmp_gt_u32_e32 vcc, s4, v216\n" \
"v_add_u32_e64 v212, s35, v212\n" \
"v_mfma_f32_16x16x32_bf16 a[100:103], a[152:155], a[164:167], 0\n" \
"v_mov_b32_e32 v219, 2.0\n" \
"v_cndmask_b32_e32 v197, v215, v214, vcc\n" \
"v_mfma_f32_16x16x32_bf16 a[100:103], a[216:219], a[228:231], a[100:103]\n" \
"s_movk_i32 s35, 0x120\n" \
"v_mul_u32_u24_e32 v214, 0xd80, v212\n" \
"v_mfma_f32_16x16x32_bf16 a[96:99], a[152:155], a[160:163], 0\n" \
"v_cmp_gt_u32_e32 vcc, s35, v20\n" \
"v_lshrrev_b32_e64 v215, 1, v212\n" \
"v_mfma_f32_16x16x32_bf16 a[96:99], a[216:219], a[224:227], a[96:99]\n" \
"s_and_b32 s4, s2, 7\n" \
"v_cndmask_b32_e32 v194, v219, v214, vcc\n" \
"s_waitcnt lgkmcnt(15)\n" \
"v_mfma_f32_16x16x32_bf16 a[112:115], a[156:159], a[160:163], 0\n" \
"v_lshlrev_b32_e32 v214, 7, v212\n" \
"v_lshrrev_b32_e64 v212, 2, v212\n" \
"v_mfma_f32_16x16x32_bf16 a[112:115], a[220:223], a[224:227], a[112:115]\n" \
"v_and_b32_e64 v212, 2, v212\n" \
"v_and_or_b32 v212, v215, 1, v212\n" \
"v_mfma_f32_16x16x32_bf16 a[116:119], a[156:159], a[164:167], 0\n" \
"s_lshl_b32 s5, s4, 2\n" \
"s_bfe_u32 s38, s2, 0x20003\n" \
"v_mfma_f32_16x16x32_bf16 a[116:119], a[220:223], a[228:231], a[116:119]\n" \
"v_lshlrev_b32_e32 v212, 5, v212\n" \
"s_movk_i32 s35, 0x60\n" \
"v_mfma_f32_16x16x32_bf16 a[120:123], a[156:159], a[168:171], 0\n" \
"s_or_b32 s5, s5, s38\n" \
"v_bitop3_b32 v212, v212, s35, v214 bitop3:0x36\n" \
"v_mfma_f32_16x16x32_bf16 a[120:123], a[220:223], a[232:235], a[120:123]\n" \
"s_lshr_b32 s36, s2, 3\n" \
"v_add_u32_e32 v212, s34, v212\n" \
"v_mfma_f32_16x16x32_bf16 a[124:127], a[156:159], a[172:175], 0\n" \
"s_mul_i32 s34, s5, 0x12000\n" \
"s_or_b32 s40, s34, 0x6000000\n" \
"v_mfma_f32_16x16x32_bf16 a[124:127], a[220:223], a[236:239], a[124:127]\n" \
"s_waitcnt vmcnt(5) lgkmcnt(0)\n" \
"s_barrier\n" \
"v_mfma_f32_16x16x32_bf16 v[52:55], a[128:131], a[176:179], 0\n" \
"s_add_u32 m0, s30, 0x0\n" \
"s_movk_i32 s84, 0x100\n" \
"v_mfma_f32_16x16x32_bf16 v[52:55], a[192:195], a[240:243], v[52:55]\n" \
"buffer_load_dwordx4 v22, s[64:67], s84 offen offset:0 lds\n" \
"v_mfma_f32_16x16x32_bf16 v[56:59], a[128:131], a[180:183], 0\n" \
"ds_read_b64_tr_b16 a[160:161], v32 offset:32768\n" \
"s_mul_i32 s34, s36, 0x90000\n" \
"v_mfma_f32_16x16x32_bf16 v[56:59], a[192:195], a[244:247], v[56:59]\n" \
"s_movk_i32 s84, 0x1d00\n" \
"v_mfma_f32_16x16x32_bf16 v[60:63], a[128:131], a[184:187], 0\n" \
"buffer_load_dwordx4 v23, s[64:67], s84 offen offset:1024 lds\n" \
"v_mfma_f32_16x16x32_bf16 v[60:63], a[192:195], a[248:251], v[60:63]\n" \
"ds_read_b64_tr_b16 a[162:163], v32 offset:33792\n" \
"s_lshl_b32 s36, s3, 14\n" \
"v_mfma_f32_16x16x32_bf16 v[64:67], a[128:131], a[188:191], 0\n" \
"ds_read_b64_tr_b16 a[224:225], v32 offset:40960\n" \
"s_add_i32 s34, s34, s36\n" \
"v_mfma_f32_16x16x32_bf16 v[64:67], a[192:195], a[252:255], v[64:67]\n" \
"ds_read_b128 a[128:131], v28 offset:49152\n" \
"s_lshl_b32 s38, s5, 8\n" \
"v_mfma_f32_16x16x32_bf16 v[80:83], a[132:135], a[188:191], 0\n" \
"ds_read_b128 a[192:195], v29 offset:49152\n" \
"s_add_i32 s41, s34, 0x4240000\n" \
"v_mfma_f32_16x16x32_bf16 v[80:83], a[196:199], a[252:255], v[80:83]\n" \
"s_movk_i32 s84, 0x3900\n" \
"v_mfma_f32_16x16x32_bf16 v[76:79], a[132:135], a[184:187], 0\n" \
"buffer_load_dwordx4 v22, s[64:67], s84 offen offset:2048 lds\n" \
"v_mfma_f32_16x16x32_bf16 v[76:79], a[196:199], a[248:251], v[76:79]\n" \
"s_movk_i32 s84, 0x5500\n" \
"v_mfma_f32_16x16x32_bf16 v[72:75], a[132:135], a[180:183], 0\n" \
"buffer_load_dwordx4 v23, s[64:67], s84 offen offset:3072 lds\n" \
"v_mfma_f32_16x16x32_bf16 v[72:75], a[196:199], a[244:247], v[72:75]\n" \
"s_add_u32 m0, s31, 0x0\n" \
"s_mov_b32 s84, 0x100000\n" \
"v_mfma_f32_16x16x32_bf16 v[68:71], a[132:135], a[176:179], 0\n" \
"buffer_load_dwordx4 v26, s[68:71], s84 offen offset:0 lds\n" \
"v_mfma_f32_16x16x32_bf16 v[68:71], a[196:199], a[240:243], v[68:71]\n" \
"ds_read_b128 a[132:135], v28 offset:51200\n" \
"s_mul_i32 s34, s5, 36\n" \
"v_mfma_f32_16x16x32_bf16 v[84:87], a[136:139], a[176:179], 0\n" \
"ds_read_b128 a[196:199], v29 offset:51200\n" \
"s_add_i32 s5, s7, s40\n" \
"v_mfma_f32_16x16x32_bf16 v[84:87], a[200:203], a[240:243], v[84:87]\n" \
"s_addk_i32 s84, 0x7c00\n" \
"v_mfma_f32_16x16x32_bf16 v[88:91], a[136:139], a[180:183], 0\n" \
"buffer_load_dwordx4 v26, s[68:71], s84 offen offset:1024 lds\n" \
"v_mfma_f32_16x16x32_bf16 v[88:91], a[200:203], a[244:247], v[88:91]\n" \
"ds_read_b64_tr_b16 a[226:227], v32 offset:41984\n" \
"s_addk_i32 s5, 0x2000\n" \
"v_mfma_f32_16x16x32_bf16 v[92:95], a[136:139], a[184:187], 0\n" \
"s_addk_i32 s84, 0x7c00\n" \
"v_mfma_f32_16x16x32_bf16 v[92:95], a[200:203], a[248:251], v[92:95]\n" \
"buffer_load_dwordx4 v27, s[68:71], s84 offen offset:2048 lds\n" \
"v_mfma_f32_16x16x32_bf16 v[96:99], a[136:139], a[188:191], 0\n" \
"ds_read_b64_tr_b16 a[164:165], v33 offset:32768\n" \
"s_mov_b32 s37, 2.0\n" \
"v_mfma_f32_16x16x32_bf16 v[96:99], a[200:203], a[252:255], v[96:99]\n" \
"ds_read_b128 a[136:139], v28 offset:53248\n" \
"s_or_b32 s38, s38, 0x5be8000\n" \
"v_mfma_f32_16x16x32_bf16 v[112:115], a[140:143], a[188:191], 0\n" \
"ds_read_b128 a[200:203], v29 offset:53248\n" \
"v_mfma_f32_16x16x32_bf16 v[112:115], a[204:207], a[252:255], v[112:115]\n" \
"s_addk_i32 s84, 0x7c00\n" \
"v_mfma_f32_16x16x32_bf16 v[108:111], a[140:143], a[184:187], 0\n" \
"buffer_load_dwordx4 v27, s[68:71], s84 offen offset:3072 lds\n" \
"v_mfma_f32_16x16x32_bf16 v[108:111], a[204:207], a[248:251], v[108:111]\n" \
"s_add_u32 m0, s30, 0x1000\n" \
"s_mov_b32 s84, 0x8100\n" \
"v_mfma_f32_16x16x32_bf16 v[104:107], a[140:143], a[180:183], 0\n" \
"buffer_load_dwordx4 v22, s[64:67], s84 offen offset:0 lds\n" \
"v_mfma_f32_16x16x32_bf16 v[104:107], a[204:207], a[244:247], v[104:107]\n" \
"ds_read_b64_tr_b16 a[166:167], v33 offset:33792\n" \
"v_mfma_f32_16x16x32_bf16 v[100:103], a[140:143], a[176:179], 0\n" \
"ds_read_b64_tr_b16 a[228:229], v33 offset:40960\n" \
"v_mfma_f32_16x16x32_bf16 v[100:103], a[204:207], a[240:243], v[100:103]\n" \
"ds_read_b128 a[140:143], v28 offset:55296\n" \
"v_mfma_f32_16x16x32_bf16 v[116:119], a[144:147], a[176:179], 0\n" \
"ds_read_b128 a[204:207], v29 offset:55296\n" \
"v_mfma_f32_16x16x32_bf16 v[116:119], a[208:211], a[240:243], v[116:119]\n" \
"s_addk_i32 s84, 0x1c00\n" \
"v_mfma_f32_16x16x32_bf16 v[120:123], a[144:147], a[180:183], 0\n" \
"buffer_load_dwordx4 v23, s[64:67], s84 offen offset:1024 lds\n" \
"v_mfma_f32_16x16x32_bf16 v[120:123], a[208:211], a[244:247], v[120:123]\n" \
"s_addk_i32 s84, 0x1c00\n" \
"v_mfma_f32_16x16x32_bf16 v[124:127], a[144:147], a[184:187], 0\n" \
"buffer_load_dwordx4 v22, s[64:67], s84 offen offset:2048 lds\n" \
"v_mfma_f32_16x16x32_bf16 v[124:127], a[208:211], a[248:251], v[124:127]\n" \
"s_addk_i32 s84, 0x1c00\n" \
"s_nop 0\n" \
"v_mfma_f32_16x16x32_bf16 v[128:131], a[144:147], a[188:191], 0\n" \
".fill (L_pb + 44768 - .) / 4, 4, 0xBF800000\n" \
"L_pp11:\n" \
"s_cbranch_execz L_pret\n" \
"s_nop 0\n" \
"buffer_load_dwordx4 v23, s[64:67], s84 offen offset:3072 lds\n" \
"v_mfma_f32_16x16x32_bf16 v[128:131], a[208:211], a[252:255], v[128:131]\n" \
"ds_read_b128 a[144:147], v28 offset:32768\n" \
"v_mfma_f32_16x16x32_bf16 v[144:147], a[148:151], a[188:191], 0\n" \
"ds_read_b128 a[208:211], v29 offset:32768\n" \
"v_mfma_f32_16x16x32_bf16 v[144:147], a[212:215], a[252:255], v[144:147]\n" \
"s_add_u32 m0, s31, 0x4000\n" \
"s_mov_b32 s84, 0x100000\n" \
"v_mfma_f32_16x16x32_bf16 v[140:143], a[148:151], a[184:187], 0\n" \
"buffer_load_dwordx4 v26, s[72:75], s84 offen offset:0 lds\n" \
"v_mfma_f32_16x16x32_bf16 v[140:143], a[212:215], a[248:251], v[140:143]\n" \
"ds_read_b64_tr_b16 a[230:231], v33 offset:41984\n" \
"v_mfma_f32_16x16x32_bf16 v[136:139], a[148:151], a[180:183], 0\n" \
"s_addk_i32 s84, 0x7c00\n" \
"s_nop 0\n" \
"v_mfma_f32_16x16x32_bf16 v[136:139], a[212:215], a[244:247], v[136:139]\n" \
"buffer_load_dwordx4 v26, s[72:75], s84 offen offset:1024 lds\n" \
"v_mfma_f32_16x16x32_bf16 v[132:135], a[148:151], a[176:179], 0\n" \
"ds_read_b64_tr_b16 a[168:169], v34 offset:32768\n" \
"v_mfma_f32_16x16x32_bf16 v[132:135], a[212:215], a[240:243], v[132:135]\n" \
"ds_read_b128 a[148:151], v28 offset:34816\n" \
"v_mfma_f32_16x16x32_bf16 v[148:151], a[152:155], a[176:179], 0\n" \
"ds_read_b128 a[212:215], v29 offset:34816\n" \
"v_mfma_f32_16x16x32_bf16 v[148:151], a[216:219], a[240:243], v[148:151]\n" \
"ds_read_b64_tr_b16 a[170:171], v34 offset:33792\n" \
"v_mfma_f32_16x16x32_bf16 v[152:155], a[152:155], a[180:183], 0\n" \
"ds_read_b64_tr_b16 a[232:233], v34 offset:40960\n" \
"v_mfma_f32_16x16x32_bf16 v[152:155], a[216:219], a[244:247], v[152:155]\n" \
"ds_read_b64_tr_b16 a[234:235], v34 offset:41984\n" \
"v_mfma_f32_16x16x32_bf16 v[156:159], a[152:155], a[184:187], 0\n" \
"ds_read_b64_tr_b16 a[172:173], v35 offset:32768\n" \
"v_mfma_f32_16x16x32_bf16 v[156:159], a[216:219], a[248:251], v[156:159]\n" \
"ds_read_b64_tr_b16 a[174:175], v35 offset:33792\n" \
"v_mfma_f32_16x16x32_bf16 v[160:163], a[152:155], a[188:191], 0\n" \
"ds_read_b64_tr_b16 a[236:237], v35 offset:40960\n" \
"v_mfma_f32_16x16x32_bf16 v[160:163], a[216:219], a[252:255], v[160:163]\n" \
"ds_read_b128 a[152:155], v28 offset:36864\n" \
"v_mfma_f32_16x16x32_bf16 v[176:179], a[156:159], a[188:191], 0\n" \
"ds_read_b128 a[216:219], v29 offset:36864\n" \
"v_mfma_f32_16x16x32_bf16 v[176:179], a[220:223], a[252:255], v[176:179]\n" \
"ds_read_b64_tr_b16 a[238:239], v35 offset:41984\n" \
"v_mfma_f32_16x16x32_bf16 v[172:175], a[156:159], a[184:187], 0\n" \
"s_cmp_lg_u32 s3, 0\n" \
"s_cselect_b32 s36, s37, s5\n" \
"v_mfma_f32_16x16x32_bf16 v[172:175], a[220:223], a[248:251], v[172:175]\n" \
"s_lshl_b32 s5, s6, 2\n" \
"s_or_b32 s5, s38, s5\n" \
"v_mfma_f32_16x16x32_bf16 v[168:171], a[156:159], a[180:183], 0\n" \
"s_lshl_b32 s35, s3, 10\n" \
"v_mfma_f32_16x16x32_bf16 v[168:171], a[220:223], a[244:247], v[168:171]\n" \
"s_cmpk_lt_u32 s3, 3\n" \
"s_cselect_b32 s37, s37, s5\n" \
"v_mfma_f32_16x16x32_bf16 v[164:167], a[156:159], a[176:179], 0\n" \
"s_lshl_b32 s5, s4, 16\n" \
"s_lshl_b32 s4, s4, 11\n" \
"v_mfma_f32_16x16x32_bf16 v[164:167], a[220:223], a[240:243], v[164:167]\n" \
"ds_read_b128 a[156:159], v28 offset:38912\n" \
"ds_read_b128 a[220:223], v29 offset:38912\n" \
"s_waitcnt lgkmcnt(15)\n" \
"v_mfma_f32_16x16x32_bf16 a[0:3], a[128:131], a[160:163], a[0:3]\n" \
"s_addk_i32 s84, 0x7c00\n" \
"v_mfma_f32_16x16x32_bf16 a[0:3], a[192:195], a[224:227], a[0:3]\n" \
"buffer_load_dwordx4 v27, s[72:75], s84 offen offset:2048 lds\n" \
"v_mfma_f32_16x16x32_bf16 a[4:7], a[128:131], a[164:167], a[4:7]\n" \
"ds_read_b64_tr_b16 a[176:177], v36 offset:32768\n" \
"v_cndmask_b32_e32 v198, v213, v212, vcc\n" \
"s_waitcnt lgkmcnt(15)\n" \
"v_mfma_f32_16x16x32_bf16 a[4:7], a[192:195], a[228:231], a[4:7]\n" \
"s_addk_i32 s84, 0x7c00\n" \
"s_waitcnt lgkmcnt(11)\n" \
"v_mfma_f32_16x16x32_bf16 a[8:11], a[128:131], a[168:171], a[8:11]\n" \
"buffer_load_dwordx4 v27, s[72:75], s84 offen offset:3072 lds\n" \
"s_waitcnt lgkmcnt(9)\n" \
"v_mfma_f32_16x16x32_bf16 a[8:11], a[192:195], a[232:235], a[8:11]\n" \
"ds_read_b64_tr_b16 a[178:179], v36 offset:33792\n" \
"v_or_b32_e32 v212, s35, v20\n" \
"s_waitcnt lgkmcnt(8)\n" \
"v_mfma_f32_16x16x32_bf16 a[12:15], a[128:131], a[172:175], a[12:15]\n" \
"ds_read_b64_tr_b16 a[240:241], v36 offset:40960\n" \
"s_or_b32 s39, s40, s35\n" \
"s_waitcnt lgkmcnt(5)\n" \
"v_mfma_f32_16x16x32_bf16 a[12:15], a[192:195], a[236:239], a[12:15]\n" \
"s_or_b32 s55, s41, s4\n" \
"v_or_b32_e32 v199, 0x22000, v212\n" \
"v_mfma_f32_16x16x32_bf16 a[28:31], a[132:135], a[172:175], a[28:31]\n" \
"s_add_i32 s35, s39, s7\n" \
"s_add_i32 s54, s41, s5\n" \
"v_mfma_f32_16x16x32_bf16 a[28:31], a[196:199], a[236:239], a[28:31]\n" \
"ds_read_b64_tr_b16 a[242:243], v36 offset:41984\n" \
"s_add_i32 s55, s55, 0x80000\n" \
"v_mfma_f32_16x16x32_bf16 a[24:27], a[132:135], a[168:171], a[24:27]\n" \
"s_lshr_b32 s4, s2, 1\n" \
"s_and_b32 s4, s4, 0x7c\n" \
"v_mfma_f32_16x16x32_bf16 a[24:27], a[196:199], a[232:235], a[24:27]\n" \
"v_mov_b32_e32 v213, s13\n" \
"s_or_b32 s4, s4, s3\n" \
"v_mfma_f32_16x16x32_bf16 a[20:23], a[132:135], a[164:167], a[20:23]\n" \
"v_lshrrev_b32_e64 v212, 4, v20\n" \
"v_mad_u64_u32 v[214:215], s[4:5], s4, 36, v[212:213]\n" \
"v_mfma_f32_16x16x32_bf16 a[20:23], a[196:199], a[228:231], a[20:23]\n" \
"ds_read_b64_tr_b16 a[180:181], v37 offset:32768\n" \
"v_mov_b32_e64 v217, s9\n" \
"v_mfma_f32_16x16x32_bf16 a[16:19], a[132:135], a[160:163], a[16:19]\n" \
"s_movk_i32 s4, 0x240\n" \
"v_cmp_gt_u32_e32 vcc, s4, v20\n" \
"v_mfma_f32_16x16x32_bf16 a[16:19], a[196:199], a[224:227], a[16:19]\n" \
"s_lshl_b32 s4, s2, 3\n" \
"s_and_b32 s4, s4, 56\n" \
"v_mfma_f32_16x16x32_bf16 a[32:35], a[136:139], a[160:163], a[32:35]\n" \
"s_mov_b32 m0, s32\n" \
"ds_read_b64_tr_b16 a[182:183], v37 offset:33792\n" \
"v_mfma_f32_16x16x32_bf16 a[32:35], a[200:203], a[224:227], a[32:35]\n" \
"s_mov_b32 exec_lo, 0x0\n" \
"s_mov_b32 exec_hi, 0xff\n" \
"global_load_lds_dword v[184:185], off\n" \
"s_mov_b64 exec, -1\n" \
"v_mfma_f32_16x16x32_bf16 a[36:39], a[136:139], a[164:167], a[36:39]\n" \
"v_cndmask_b32_e32 v217, v217, v213, vcc\n" \
"v_lshlrev_b32_e64 v213, 3, v214\n" \
"v_mfma_f32_16x16x32_bf16 a[36:39], a[200:203], a[228:231], a[36:39]\n" \
"ds_read_b64_tr_b16 a[244:245], v37 offset:40960\n" \
"v_and_b32_e32 v213, 0x1ffc0, v213\n" \
"v_mfma_f32_16x16x32_bf16 a[40:43], a[136:139], a[168:171], a[40:43]\n" \
"v_and_b32_e64 v214, 7, v214\n" \
"v_or3_b32 v213, v214, s4, v213\n" \
"v_mfma_f32_16x16x32_bf16 a[40:43], a[200:203], a[232:235], a[40:43]\n" \
"s_mov_b32 m0, s32\n" \
"s_lshl_b32 s4, s2, 2\n" \
"v_mfma_f32_16x16x32_bf16 a[44:47], a[136:139], a[172:175], a[44:47]\n" \
"s_mov_b32 exec_lo, 0xffff0000\n" \
"s_mov_b32 exec_hi, 0x0\n" \
"global_load_lds_dword v[186:187], off\n" \
"s_mov_b64 exec, -1\n" \
"v_mfma_f32_16x16x32_bf16 a[44:47], a[200:203], a[236:239], a[44:47]\n" \
"ds_read_b64_tr_b16 a[246:247], v37 offset:41984\n" \
"s_or_b32 s4, s4, s3\n" \
"v_mfma_f32_16x16x32_bf16 a[60:63], a[140:143], a[172:175], a[60:63]\n" \
"s_mul_i32 s4, s4, 18\n" \
"v_min_u32_e64 v212, 53, v212\n" \
"v_mfma_f32_16x16x32_bf16 a[60:63], a[204:207], a[236:239], a[60:63]\n" \
"s_sub_i32 s4, s4, 36\n" \
"v_add_u32_e32 v212, s4, v212\n" \
"v_mfma_f32_16x16x32_bf16 a[56:59], a[140:143], a[168:171], a[56:59]\n" \
"ds_read_b64_tr_b16 a[184:185], v38 offset:32768\n" \
"v_mov_b32_e64 v215, s12\n" \
"v_mfma_f32_16x16x32_bf16 a[56:59], a[204:207], a[232:235], a[56:59]\n" \
"v_mov_b32_e32 v216, s8\n" \
"v_cndmask_b32_e32 v212, v212, v213, vcc\n" \
"v_mfma_f32_16x16x32_bf16 a[52:55], a[140:143], a[164:167], a[52:55]\n" \
"v_cndmask_b32_e32 v216, v216, v215, vcc\n" \
"v_lshlrev_b32_e32 v212, 7, v212\n" \
"v_mfma_f32_16x16x32_bf16 a[52:55], a[204:207], a[228:231], a[52:55]\n" \
"ds_read_b64_tr_b16 a[186:187], v38 offset:33792\n" \
"v_mov_b32_e64 v213, 0\n" \
"v_mfma_f32_16x16x32_bf16 a[48:51], a[140:143], a[160:163], a[48:51]\n" \
"v_lshl_add_u64 v[188:189], v[216:217], 0, v[212:213]\n" \
"v_mov_b32_e32 v215, 0xffff\n" \
"v_mfma_f32_16x16x32_bf16 a[48:51], a[204:207], a[224:227], a[48:51]\n" \
"s_and_b32 s4, s3, 1\n" \
"s_lshl_b32 s5, s4, 4\n" \
"v_mfma_f32_16x16x32_bf16 a[64:67], a[144:147], a[160:163], a[64:67]\n" \
"ds_read_b64_tr_b16 a[248:249], v38 offset:40960\n" \
"v_lshrrev_b32_e64 v212, 4, v20\n" \
"v_mfma_f32_16x16x32_bf16 a[64:67], a[208:211], a[224:227], a[64:67]\n" \
"v_lshrrev_b32_e32 v218, 6, v20\n" \
"v_and_b32_e32 v219, 12, v218\n" \
"v_mfma_f32_16x16x32_bf16 a[68:71], a[144:147], a[164:167], a[68:71]\n" \
"v_and_or_b32 v214, v212, 15, s5\n" \
"v_cmp_gt_u32_e64 vcc, v219, v214\n" \
"v_mfma_f32_16x16x32_bf16 a[68:71], a[208:211], a[228:231], a[68:71]\n" \
"ds_read_b64_tr_b16 a[250:251], v38 offset:41984\n" \
"v_mov_b32_e32 v220, 0xffff0000\n" \
"v_mfma_f32_16x16x32_bf16 a[72:75], a[144:147], a[168:171], a[72:75]\n" \
"s_lshl_b32 s5, s2, 2\n" \
"v_cndmask_b32_e64 v217, v215, 0, vcc\n" \
"v_mfma_f32_16x16x32_bf16 a[72:75], a[208:211], a[232:235], a[72:75]\n" \
"v_cmp_lt_u32_e32 vcc, v219, v214\n" \
"v_mov_b32_e64 v213, s23\n" \
"v_mfma_f32_16x16x32_bf16 a[76:79], a[144:147], a[172:175], a[76:79]\n" \
"s_or_b32 s5, s5, s3\n" \
"v_cndmask_b32_e32 v221, 0, v220, vcc\n" \
"v_mfma_f32_16x16x32_bf16 a[76:79], a[208:211], a[236:239], a[76:79]\n" \
"ds_read_b64_tr_b16 a[188:189], v39 offset:32768\n" \
"v_or_b32_e64 v12, v217, v221\n" \
"v_mfma_f32_16x16x32_bf16 a[92:95], a[148:151], a[172:175], a[92:95]\n" \
"v_or_b32_e32 v217, 2, v219\n" \
"v_cmp_gt_u32_e32 vcc, v217, v214\n" \
"v_mfma_f32_16x16x32_bf16 a[92:95], a[212:215], a[236:239], a[92:95]\n" \
"v_mov_b32_e32 v216, s25\n" \
"s_movk_i32 s40, 0xf400\n" \
"v_mfma_f32_16x16x32_bf16 a[88:91], a[148:151], a[168:171], a[88:91]\n" \
"ds_read_b64_tr_b16 a[190:191], v39 offset:33792\n" \
"v_cndmask_b32_e64 v221, v215, 0, vcc\n" \
"v_mfma_f32_16x16x32_bf16 a[88:91], a[212:215], a[232:235], a[88:91]\n" \
"v_cmp_lt_u32_e32 vcc, v217, v214\n" \
"s_lshl_b32 s4, s4, 6\n" \
"v_mfma_f32_16x16x32_bf16 a[84:87], a[148:151], a[164:167], a[84:87]\n" \
"v_cndmask_b32_e32 v217, 0, v220, vcc\n" \
"v_or_b32_e32 v13, v221, v217\n" \
"v_mfma_f32_16x16x32_bf16 a[84:87], a[212:215], a[228:231], a[84:87]\n" \
"ds_read_b64_tr_b16 a[252:253], v39 offset:40960\n" \
"v_or_b32_e64 v217, 16, v219\n" \
"v_mfma_f32_16x16x32_bf16 a[80:83], a[148:151], a[160:163], a[80:83]\n" \
"v_cmp_gt_u32_e32 vcc, v217, v214\n" \
"s_nop 1\n" \
"v_cndmask_b32_e64 v221, v215, 0, vcc\n" \
"v_mfma_f32_16x16x32_bf16 a[80:83], a[212:215], a[224:227], a[80:83]\n" \
"v_cmp_lt_u32_e32 vcc, v217, v214\n" \
"s_nop 1\n" \
"v_cndmask_b32_e64 v217, 0, v220, vcc\n" \
"v_mfma_f32_16x16x32_bf16 a[96:99], a[152:155], a[160:163], a[96:99]\n" \
"ds_read_b64_tr_b16 a[254:255], v39 offset:41984\n" \
"v_or_b32_e64 v14, v221, v217\n" \
"v_mfma_f32_16x16x32_bf16 a[96:99], a[216:219], a[224:227], a[96:99]\n" \
"v_or_b32_e32 v217, 18, v219\n" \
"v_cmp_gt_u32_e32 vcc, v217, v214\n" \
"v_mfma_f32_16x16x32_bf16 a[100:103], a[152:155], a[164:167], a[100:103]\n" \
"s_nop 0\n" \
"v_cndmask_b32_e64 v215, v215, 0, vcc\n" \
"v_cmp_lt_u32_e32 vcc, v217, v214\n" \
"v_mfma_f32_16x16x32_bf16 a[100:103], a[216:219], a[228:231], a[100:103]\n" \
"s_nop 0\n" \
"v_cndmask_b32_e32 v214, 0, v220, vcc\n" \
"v_or_b32_e64 v15, v215, v214\n" \
"v_mfma_f32_16x16x32_bf16 a[104:107], a[152:155], a[168:171], a[104:107]\n" \
"v_mad_u64_u32 v[214:215], s[6:7], s5, 42, v[212:213]\n" \
"s_movk_i32 s5, 0x1440\n" \
"v_mfma_f32_16x16x32_bf16 a[104:107], a[216:219], a[232:235], a[104:107]\n" \
"v_cmp_gt_u32_e32 vcc, s5, v214\n" \
"v_min_u32_e32 v217, 0xa43f, v214\n" \
"v_mfma_f32_16x16x32_bf16 a[108:111], a[152:155], a[172:175], a[108:111]\n" \
"v_mov_b32_e32 v214, s24\n" \
"v_cndmask_b32_e32 v215, v216, v213, vcc\n" \
"v_mfma_f32_16x16x32_bf16 a[108:111], a[216:219], a[236:239], a[108:111]\n" \
"v_mov_b32_e32 v213, s22\n" \
"s_movk_i32 s6, 0xffee\n" \
"s_waitcnt lgkmcnt(15)\n" \
"v_mfma_f32_16x16x32_bf16 a[124:127], a[156:159], a[172:175], a[124:127]\n" \
"v_cndmask_b32_e32 v214, v214, v213, vcc\n" \
"v_add_u32_e32 v213, 0xffffebc0, v217\n" \
"v_mfma_f32_16x16x32_bf16 a[124:127], a[220:223], a[236:239], a[124:127]\n" \
"s_lshl_b32 s5, s2, 5\n" \
"v_cndmask_b32_e32 v213, v213, v217, vcc\n" \
"v_mfma_f32_16x16x32_bf16 a[120:123], a[156:159], a[168:171], a[120:123]\n" \
"v_lshlrev_b32_e64 v216, 7, v213\n" \
"v_mul_u32_u24_e32 v213, 0xe39, v212\n" \
"v_mfma_f32_16x16x32_bf16 a[120:123], a[220:223], a[232:235], a[120:123]\n" \
"v_mov_b32_e32 v217, 0\n" \
"v_lshrrev_b32_e32 v213, 16, v213\n" \
"v_mfma_f32_16x16x32_bf16 a[116:119], a[156:159], a[164:167], a[116:119]\n" \
"v_lshl_add_u64 v[16:17], v[214:215], 0, v[216:217]\n" \
"v_mad_i32_i24 v214, v213, s6, v212\n" \
"v_mfma_f32_16x16x32_bf16 a[116:119], a[220:223], a[228:231], a[116:119]\n" \
"s_add_i32 s5, s5, 32\n" \
"v_lshlrev_b32_e32 v215, 4, v214\n" \
"v_mfma_f32_16x16x32_bf16 a[112:115], a[156:159], a[160:163], a[112:115]\n" \
"v_add_u32_e64 v216, s5, v215\n" \
"v_and_b32_e32 v216, 0xf0, v216\n" \
"v_mfma_f32_16x16x32_bf16 a[112:115], a[220:223], a[224:227], a[112:115]\n" \
"s_waitcnt vmcnt(20)\n" \
"v_readfirstlane_b32 s62, v180\n" \
"s_nop 3\n" \
"s_add_u32 s62, s62, 0x9E3779B1\n" \
"s_waitcnt vmcnt(2) lgkmcnt(0)\n" \
"s_barrier\n" \
"v_mfma_f32_16x16x32_bf16 v[52:55], a[128:131], a[176:179], v[52:55]\n" \
"s_add_u32 m0, s30, 0x8000\n" \
"s_movk_i32 s84, 0x180\n" \
"v_mfma_f32_16x16x32_bf16 v[52:55], a[192:195], a[240:243], v[52:55]\n" \
"buffer_load_dwordx4 v22, s[64:67], s84 offen offset:0 lds\n" \
"v_mfma_f32_16x16x32_bf16 v[56:59], a[128:131], a[180:183], v[56:59]\n" \
"ds_read_b64_tr_b16 a[160:161], v32 offset:0\n" \
"v_cmp_gt_u32_e64 vcc, 16, v214\n" \
"v_mfma_f32_16x16x32_bf16 v[56:59], a[192:195], a[244:247], v[56:59]\n" \
"s_movk_i32 s84, 0x1d80\n" \
"v_mfma_f32_16x16x32_bf16 v[60:63], a[128:131], a[184:187], v[60:63]\n" \
"buffer_load_dwordx4 v23, s[64:67], s84 offen offset:1024 lds\n" \
"v_mfma_f32_16x16x32_bf16 v[60:63], a[192:195], a[248:251], v[60:63]\n" \
"ds_read_b64_tr_b16 a[162:163], v32 offset:1024\n" \
"s_movk_i32 s7, 0xf800\n" \
"v_mfma_f32_16x16x32_bf16 v[64:67], a[128:131], a[188:191], v[64:67]\n" \
"ds_read_b64_tr_b16 a[224:225], v32 offset:8192\n" \
"v_cndmask_b32_e64 v214, v215, v216, vcc\n" \
"v_mfma_f32_16x16x32_bf16 v[64:67], a[192:195], a[252:255], v[64:67]\n" \
"ds_read_b128 a[128:131], v28 offset:16384\n" \
"v_or_b32_e64 v215, 64, v212\n" \
"v_mfma_f32_16x16x32_bf16 v[80:83], a[132:135], a[188:191], v[80:83]\n" \
"ds_read_b128 a[192:195], v29 offset:16384\n" \
"v_mul_u32_u24_e32 v216, 0xe39, v215\n" \
"v_mfma_f32_16x16x32_bf16 v[80:83], a[196:199], a[252:255], v[80:83]\n" \
"s_movk_i32 s84, 0x3980\n" \
"v_mfma_f32_16x16x32_bf16 v[76:79], a[132:135], a[184:187], v[76:79]\n" \
"buffer_load_dwordx4 v22, s[64:67], s84 offen offset:2048 lds\n" \
"v_mfma_f32_16x16x32_bf16 v[76:79], a[196:199], a[248:251], v[76:79]\n" \
"s_movk_i32 s84, 0x5580\n" \
"v_mfma_f32_16x16x32_bf16 v[72:75], a[132:135], a[180:183], v[72:75]\n" \
"buffer_load_dwordx4 v23, s[64:67], s84 offen offset:3072 lds\n" \
"v_mfma_f32_16x16x32_bf16 v[72:75], a[196:199], a[244:247], v[72:75]\n" \
"s_add_u32 m0, s31, 0x8000\n" \
"s_mov_b32 s84, 0x180000\n" \
"v_mfma_f32_16x16x32_bf16 v[68:71], a[132:135], a[176:179], v[68:71]\n" \
"buffer_load_dwordx4 v26, s[68:71], s84 offen offset:0 lds\n" \
"v_mfma_f32_16x16x32_bf16 v[68:71], a[196:199], a[240:243], v[68:71]\n" \
"ds_read_b128 a[132:135], v28 offset:18432\n" \
"v_lshrrev_b32_e64 v216, 16, v216\n" \
"v_mfma_f32_16x16x32_bf16 v[84:87], a[136:139], a[176:179], v[84:87]\n" \
"ds_read_b128 a[196:199], v29 offset:18432\n" \
"v_mad_i32_i24 v215, v216, s6, v215\n" \
"v_mfma_f32_16x16x32_bf16 v[84:87], a[200:203], a[240:243], v[84:87]\n" \
"s_addk_i32 s84, 0x7c00\n" \
"v_mfma_f32_16x16x32_bf16 v[88:91], a[136:139], a[180:183], v[88:91]\n" \
"buffer_load_dwordx4 v26, s[68:71], s84 offen offset:1024 lds\n" \
"v_mfma_f32_16x16x32_bf16 v[88:91], a[200:203], a[244:247], v[88:91]\n" \
"ds_read_b64_tr_b16 a[226:227], v32 offset:9216\n" \
"v_lshlrev_b32_e32 v217, 4, v215\n" \
"v_mfma_f32_16x16x32_bf16 v[92:95], a[136:139], a[184:187], v[92:95]\n" \
"s_addk_i32 s84, 0x7c00\n" \
"v_mfma_f32_16x16x32_bf16 v[92:95], a[200:203], a[248:251], v[92:95]\n" \
"buffer_load_dwordx4 v27, s[68:71], s84 offen offset:2048 lds\n" \
"v_mfma_f32_16x16x32_bf16 v[96:99], a[136:139], a[188:191], v[96:99]\n" \
"ds_read_b64_tr_b16 a[164:165], v33 offset:0\n" \
"v_add_u32_e32 v220, s5, v217\n" \
"v_mfma_f32_16x16x32_bf16 v[96:99], a[200:203], a[252:255], v[96:99]\n" \
"ds_read_b128 a[136:139], v28 offset:20480\n" \
"v_and_b32_e32 v220, 0xf0, v220\n" \
"v_mfma_f32_16x16x32_bf16 v[112:115], a[140:143], a[188:191], v[112:115]\n" \
"ds_read_b128 a[200:203], v29 offset:20480\n" \
"v_cmp_gt_u32_e64 vcc, 16, v215\n" \
"v_mfma_f32_16x16x32_bf16 v[112:115], a[204:207], a[252:255], v[112:115]\n" \
"s_addk_i32 s84, 0x7c00\n" \
"v_mfma_f32_16x16x32_bf16 v[108:111], a[140:143], a[184:187], v[108:111]\n" \
"buffer_load_dwordx4 v27, s[68:71], s84 offen offset:3072 lds\n" \
"v_mfma_f32_16x16x32_bf16 v[108:111], a[204:207], a[248:251], v[108:111]\n" \
"s_add_u32 m0, s30, 0x9000\n" \
"s_mov_b32 s84, 0x8180\n" \
"v_mfma_f32_16x16x32_bf16 v[104:107], a[140:143], a[180:183], v[104:107]\n" \
"buffer_load_dwordx4 v22, s[64:67], s84 offen offset:0 lds\n" \
"v_mfma_f32_16x16x32_bf16 v[104:107], a[204:207], a[244:247], v[104:107]\n" \
"ds_read_b64_tr_b16 a[166:167], v33 offset:1024\n" \
"v_cndmask_b32_e32 v215, v217, v220, vcc\n" \
"v_mfma_f32_16x16x32_bf16 v[100:103], a[140:143], a[176:179], v[100:103]\n" \
"ds_read_b64_tr_b16 a[228:229], v33 offset:8192\n" \
"v_or_b32_e32 v217, 0x80, v212\n" \
"v_mfma_f32_16x16x32_bf16 v[100:103], a[204:207], a[240:243], v[100:103]\n" \
"ds_read_b128 a[140:143], v28 offset:22528\n" \
"v_mul_u32_u24_e32 v220, 0xe39, v217\n" \
"v_mfma_f32_16x16x32_bf16 v[116:119], a[144:147], a[176:179], v[116:119]\n" \
"ds_read_b128 a[204:207], v29 offset:22528\n" \
"v_lshrrev_b32_e64 v220, 16, v220\n" \
"v_mfma_f32_16x16x32_bf16 v[116:119], a[208:211], a[240:243], v[116:119]\n" \
"s_addk_i32 s84, 0x1c00\n" \
"v_mfma_f32_16x16x32_bf16 v[120:123], a[144:147], a[180:183], v[120:123]\n" \
"buffer_load_dwordx4 v23, s[64:67], s84 offen offset:1024 lds\n" \
"v_mfma_f32_16x16x32_bf16 v[120:123], a[208:211], a[244:247], v[120:123]\n" \
"s_addk_i32 s84, 0x1c00\n" \
"v_mfma_f32_16x16x32_bf16 v[124:127], a[144:147], a[184:187], v[124:127]\n" \
"buffer_load_dwordx4 v22, s[64:67], s84 offen offset:2048 lds\n" \
"v_mfma_f32_16x16x32_bf16 v[124:127], a[208:211], a[248:251], v[124:127]\n" \
"s_addk_i32 s84, 0x1c00\n" \
"v_mfma_f32_16x16x32_bf16 v[128:131], a[144:147], a[188:191], v[128:131]\n" \
"buffer_load_dwordx4 v23, s[64:67], s84 offen offset:3072 lds\n" \
"v_mfma_f32_16x16x32_bf16 v[128:131], a[208:211], a[252:255], v[128:131]\n" \
"ds_read_b128 a[144:147], v28 offset:0\n" \
"v_mad_i32_i24 v217, v220, s6, v217\n" \
"v_mfma_f32_16x16x32_bf16 v[144:147], a[148:151], a[188:191], v[144:147]\n" \
"ds_read_b128 a[208:211], v29 offset:0\n" \
"v_lshlrev_b32_e32 v221, 4, v217\n" \
"v_mfma_f32_16x16x32_bf16 v[144:147], a[212:215], a[252:255], v[144:147]\n" \
"s_add_u32 m0, s31, 0xc000\n" \
"s_mov_b32 s84, 0x180000\n" \
"v_mfma_f32_16x16x32_bf16 v[140:143], a[148:151], a[184:187], v[140:143]\n" \
"buffer_load_dwordx4 v26, s[72:75], s84 offen offset:0 lds\n" \
"v_mfma_f32_16x16x32_bf16 v[140:143], a[212:215], a[248:251], v[140:143]\n" \
"ds_read_b64_tr_b16 a[230:231], v33 offset:9216\n" \
"v_add_u32_e64 v222, s5, v221\n" \
"v_mfma_f32_16x16x32_bf16 v[136:139], a[148:151], a[180:183], v[136:139]\n" \
"s_addk_i32 s84, 0x7c00\n" \
"v_mfma_f32_16x16x32_bf16 v[136:139], a[212:215], a[244:247], v[136:139]\n" \
"buffer_load_dwordx4 v26, s[72:75], s84 offen offset:1024 lds\n" \
"v_mfma_f32_16x16x32_bf16 v[132:135], a[148:151], a[176:179], v[132:135]\n" \
"ds_read_b64_tr_b16 a[168:169], v34 offset:0\n" \
"v_and_b32_e32 v222, 0xf0, v222\n" \
"v_mfma_f32_16x16x32_bf16 v[132:135], a[212:215], a[240:243], v[132:135]\n" \
"ds_read_b128 a[148:151], v28 offset:2048\n" \
"v_cmp_gt_u32_e32 vcc, 16, v217\n" \
"v_mfma_f32_16x16x32_bf16 v[148:151], a[152:155], a[176:179], v[148:151]\n" \
"ds_read_b128 a[212:215], v29 offset:2048\n" \
"v_cndmask_b32_e64 v217, v221, v222, vcc\n" \
"v_mfma_f32_16x16x32_bf16 v[148:151], a[216:219], a[240:243], v[148:151]\n" \
"ds_read_b64_tr_b16 a[170:171], v34 offset:1024\n" \
"v_or_b32_e32 v221, 0xc0, v212\n" \
"v_mfma_f32_16x16x32_bf16 v[152:155], a[152:155], a[180:183], v[152:155]\n" \
"ds_read_b64_tr_b16 a[232:233], v34 offset:8192\n" \
"v_mul_u32_u24_e32 v222, 0xe39, v221\n" \
"v_mfma_f32_16x16x32_bf16 v[152:155], a[216:219], a[244:247], v[152:155]\n" \
"ds_read_b64_tr_b16 a[234:235], v34 offset:9216\n" \
"v_lshrrev_b32_e64 v222, 16, v222\n" \
"v_mfma_f32_16x16x32_bf16 v[156:159], a[152:155], a[184:187], v[156:159]\n" \
"ds_read_b64_tr_b16 a[172:173], v35 offset:0\n" \
"v_mad_i32_i24 v221, v222, s6, v221\n" \
"v_mfma_f32_16x16x32_bf16 v[156:159], a[216:219], a[248:251], v[156:159]\n" \
"ds_read_b64_tr_b16 a[174:175], v35 offset:1024\n" \
"v_lshlrev_b32_e64 v223, 4, v221\n" \
"v_mfma_f32_16x16x32_bf16 v[160:163], a[152:155], a[188:191], v[160:163]\n" \
"ds_read_b64_tr_b16 a[236:237], v35 offset:8192\n" \
"v_add_u32_e64 v224, s5, v223\n" \
"v_mfma_f32_16x16x32_bf16 v[160:163], a[216:219], a[252:255], v[160:163]\n" \
"ds_read_b128 a[152:155], v28 offset:4096\n" \
"v_and_b32_e32 v224, 0xf0, v224\n" \
"v_mfma_f32_16x16x32_bf16 v[176:179], a[156:159], a[188:191], v[176:179]\n" \
"ds_read_b128 a[216:219], v29 offset:4096\n" \
"v_cmp_gt_u32_e64 vcc, 16, v221\n" \
"v_mfma_f32_16x16x32_bf16 v[176:179], a[220:223], a[252:255], v[176:179]\n" \
"ds_read_b64_tr_b16 a[238:239], v35 offset:9216\n" \
"v_cndmask_b32_e64 v221, v223, v224, vcc\n" \
"v_mfma_f32_16x16x32_bf16 v[172:175], a[156:159], a[184:187], v[172:175]\n" \
"v_or_b32_e32 v223, 0x100, v212\n" \
"v_mul_u32_u24_e32 v224, 0xe39, v223\n" \
"v_mfma_f32_16x16x32_bf16 v[172:175], a[220:223], a[248:251], v[172:175]\n" \
"v_lshrrev_b32_e64 v224, 16, v224\n" \
"v_mad_i32_i24 v223, v224, s6, v223\n" \
"v_mfma_f32_16x16x32_bf16 v[168:171], a[156:159], a[180:183], v[168:171]\n" \
"v_lshlrev_b32_e32 v225, 4, v223\n" \
"v_add_u32_e32 v226, s5, v225\n" \
"v_mfma_f32_16x16x32_bf16 v[168:171], a[220:223], a[244:247], v[168:171]\n" \
"v_and_b32_e32 v226, 0xf0, v226\n" \
"v_cmp_gt_u32_e64 vcc, 16, v223\n" \
"v_mfma_f32_16x16x32_bf16 v[164:167], a[156:159], a[176:179], v[164:167]\n" \
"s_nop 0\n" \
"v_cndmask_b32_e32 v223, v225, v226, vcc\n" \
"v_or_b32_e32 v225, 0x140, v212\n" \
"v_mfma_f32_16x16x32_bf16 v[164:167], a[220:223], a[240:243], v[164:167]\n" \
"ds_read_b128 a[156:159], v28 offset:6144\n" \
"ds_read_b128 a[220:223], v29 offset:6144\n" \
"s_waitcnt lgkmcnt(15)\n" \
"v_mfma_f32_16x16x32_bf16 a[0:3], a[128:131], a[160:163], a[0:3]\n" \
"s_addk_i32 s84, 0x7c00\n" \
"v_mfma_f32_16x16x32_bf16 a[0:3], a[192:195], a[224:227], a[0:3]\n" \
"buffer_load_dwordx4 v27, s[72:75], s84 offen offset:2048 lds\n" \
"v_mfma_f32_16x16x32_bf16 a[4:7], a[128:131], a[164:167], a[4:7]\n" \
"ds_read_b64_tr_b16 a[176:177], v36 offset:0\n" \
"v_mul_u32_u24_e32 v226, 0xe39, v225\n" \
"s_waitcnt lgkmcnt(15)\n" \
"v_mfma_f32_16x16x32_bf16 a[4:7], a[192:195], a[228:231], a[4:7]\n" \
"s_addk_i32 s84, 0x7c00\n" \
"s_waitcnt lgkmcnt(11)\n" \
"v_mfma_f32_16x16x32_bf16 a[8:11], a[128:131], a[168:171], a[8:11]\n" \
"buffer_load_dwordx4 v27, s[72:75], s84 offen offset:3072 lds\n" \
"s_waitcnt lgkmcnt(9)\n" \
"v_mfma_f32_16x16x32_bf16 a[8:11], a[192:195], a[232:235], a[8:11]\n" \
"ds_read_b64_tr_b16 a[178:179], v36 offset:1024\n" \
"v_lshrrev_b32_e32 v226, 16, v226\n" \
"s_waitcnt lgkmcnt(8)\n" \
"v_mfma_f32_16x16x32_bf16 a[12:15], a[128:131], a[172:175], a[12:15]\n" \
"ds_read_b64_tr_b16 a[240:241], v36 offset:8192\n" \
"v_mad_i32_i24 v225, v226, s6, v225\n" \
"s_waitcnt lgkmcnt(5)\n" \
"v_mfma_f32_16x16x32_bf16 a[12:15], a[192:195], a[236:239], a[12:15]\n" \
"v_lshlrev_b32_e32 v227, 4, v225\n" \
"v_add_u32_e64 v228, s5, v227\n" \
"v_mfma_f32_16x16x32_bf16 a[28:31], a[132:135], a[172:175], a[28:31]\n" \
"v_and_b32_e32 v228, 0xf0, v228\n" \
"v_cmp_gt_u32_e64 vcc, 16, v225\n" \
"v_mfma_f32_16x16x32_bf16 a[28:31], a[196:199], a[236:239], a[28:31]\n" \
"ds_read_b64_tr_b16 a[242:243], v36 offset:9216\n" \
"v_cndmask_b32_e64 v225, v227, v228, vcc\n" \
"v_mfma_f32_16x16x32_bf16 a[24:27], a[132:135], a[168:171], a[24:27]\n" \
"buffer_load_dwordx4 v[200:203], v192, s[76:79], s34 offen offset:0\n" \
"v_mfma_f32_16x16x32_bf16 a[24:27], a[196:199], a[232:235], a[24:27]\n" \
"v_or_b32_e32 v227, 0x180, v212\n" \
"v_mul_u32_u24_e32 v228, 0xe39, v227\n" \
"v_mfma_f32_16x16x32_bf16 a[20:23], a[132:135], a[164:167], a[20:23]\n" \
"v_lshrrev_b32_e64 v228, 16, v228\n" \
"v_mad_i32_i24 v227, v228, s6, v227\n" \
"v_mfma_f32_16x16x32_bf16 a[20:23], a[196:199], a[228:231], a[20:23]\n" \
"ds_read_b64_tr_b16 a[180:181], v37 offset:0\n" \
"v_lshlrev_b32_e64 v229, 4, v227\n" \
"v_mfma_f32_16x16x32_bf16 a[16:19], a[132:135], a[160:163], a[16:19]\n" \
"v_add_u32_e64 v230, s5, v229\n" \
"v_and_b32_e32 v230, 0xf0, v230\n" \
"v_mfma_f32_16x16x32_bf16 a[16:19], a[196:199], a[224:227], a[16:19]\n" \
"v_cmp_gt_u32_e32 vcc, 16, v227\n" \
"s_nop 1\n" \
"v_cndmask_b32_e64 v227, v229, v230, vcc\n" \
"v_mfma_f32_16x16x32_bf16 a[32:35], a[136:139], a[160:163], a[32:35]\n" \
"buffer_load_dwordx4 v[204:207], v193, s[76:79], s34 offen offset:0\n" \
"v_mfma_f32_16x16x32_bf16 a[32:35], a[200:203], a[224:227], a[32:35]\n" \
"s_mov_b32 m0, s32\n" \
"ds_read_b64_tr_b16 a[182:183], v37 offset:1024\n" \
"v_mfma_f32_16x16x32_bf16 a[36:39], a[136:139], a[164:167], a[36:39]\n" \
"s_mov_b32 exec_lo, 0x0\n" \
"s_mov_b32 exec_hi, 0xff00\n" \
"global_load_lds_dword v[184:185], off\n" \
"s_mov_b64 exec, -1\n" \
"v_mfma_f32_16x16x32_bf16 a[36:39], a[200:203], a[228:231], a[36:39]\n" \
"ds_read_b64_tr_b16 a[244:245], v37 offset:8192\n" \
"v_or_b32_e32 v229, 0x1c0, v212\n" \
"v_mfma_f32_16x16x32_bf16 a[40:43], a[136:139], a[168:171], a[40:43]\n" \
"v_mul_u32_u24_e32 v230, 0xe39, v229\n" \
"v_lshrrev_b32_e32 v230, 16, v230\n" \
"v_mfma_f32_16x16x32_bf16 a[40:43], a[200:203], a[232:235], a[40:43]\n" \
"buffer_load_dword v208, v194, s[76:79], s34 offen offset:32\n" \
"v_mfma_f32_16x16x32_bf16 a[44:47], a[136:139], a[172:175], a[44:47]\n" \
"ds_read_b64_tr_b16 a[246:247], v37 offset:9216\n" \
"v_mad_i32_i24 v229, v230, s6, v229\n" \
"v_mfma_f32_16x16x32_bf16 a[44:47], a[200:203], a[236:239], a[44:47]\n" \
"v_lshlrev_b32_e32 v231, 4, v229\n" \
"v_add_u32_e32 v232, s5, v231\n" \
"v_mfma_f32_16x16x32_bf16 a[60:63], a[140:143], a[172:175], a[60:63]\n" \
"v_and_b32_e32 v232, 0xf0, v232\n" \
"v_cmp_gt_u32_e64 vcc, 16, v229\n" \
"v_mfma_f32_16x16x32_bf16 a[60:63], a[204:207], a[236:239], a[60:63]\n" \
"s_nop 0\n" \
"v_cndmask_b32_e32 v229, v231, v232, vcc\n" \
"v_or_b32_e32 v231, 0x200, v212\n" \
"v_mfma_f32_16x16x32_bf16 a[56:59], a[140:143], a[168:171], a[56:59]\n" \
"buffer_load_dword v209, v194, s[76:79], s34 offen offset:1184\n" \
"v_mfma_f32_16x16x32_bf16 a[56:59], a[204:207], a[232:235], a[56:59]\n" \
"ds_read_b64_tr_b16 a[184:185], v38 offset:0\n" \
"v_mul_u32_u24_e32 v232, 0xe39, v231\n" \
"v_mfma_f32_16x16x32_bf16 a[52:55], a[140:143], a[164:167], a[52:55]\n" \
"v_lshrrev_b32_e64 v232, 16, v232\n" \
"v_mad_i32_i24 v231, v232, s6, v231\n" \
"v_mfma_f32_16x16x32_bf16 a[52:55], a[204:207], a[228:231], a[52:55]\n" \
"ds_read_b64_tr_b16 a[186:187], v38 offset:1024\n" \
"v_lshlrev_b32_e64 v233, 4, v231\n" \
"v_mfma_f32_16x16x32_bf16 a[48:51], a[140:143], a[160:163], a[48:51]\n" \
"v_add_u32_e32 v234, s5, v233\n" \
"s_movk_i32 s5, 0x480\n" \
"v_mfma_f32_16x16x32_bf16 a[48:51], a[204:207], a[224:227], a[48:51]\n" \
"v_mad_u32_u24 v0, v213, s5, v214\n" \
"v_mul_u32_u24_e32 v213, 0x480, v216\n" \
"v_mfma_f32_16x16x32_bf16 a[64:67], a[144:147], a[160:163], a[64:67]\n" \
"buffer_load_dword v211, v194, s[76:79], s34 offen offset:2336\n" \
"v_mfma_f32_16x16x32_bf16 a[64:67], a[208:211], a[224:227], a[64:67]\n" \
"ds_read_b64_tr_b16 a[248:249], v38 offset:8192\n" \
"s_movk_i32 s6, 0xfc00\n" \
"v_mfma_f32_16x16x32_bf16 a[68:71], a[144:147], a[164:167], a[68:71]\n" \
"v_add3_u32 v1, v213, v215, s6\n" \
"v_mul_u32_u24_e32 v213, 0x480, v220\n" \
"v_mfma_f32_16x16x32_bf16 a[68:71], a[208:211], a[228:231], a[68:71]\n" \
"ds_read_b64_tr_b16 a[250:251], v38 offset:9216\n" \
"v_add3_u32 v2, v213, v217, s7\n" \
"v_mfma_f32_16x16x32_bf16 a[72:75], a[144:147], a[168:171], a[72:75]\n" \
"v_mul_u32_u24_e32 v213, 0x480, v222\n" \
"v_add3_u32 v3, v213, v221, s40\n" \
"v_mfma_f32_16x16x32_bf16 a[72:75], a[208:211], a[232:235], a[72:75]\n" \
"s_mov_b32 s76, s20\n" \
"s_and_b32 s77, s21, 0xffff\n" \
"v_mfma_f32_16x16x32_bf16 a[76:79], a[144:147], a[172:175], a[76:79]\n" \
"s_mov_b32 s78, 0x480\n" \
"v_mul_u32_u24_e32 v213, 0x480, v226\n" \
"v_mfma_f32_16x16x32_bf16 a[76:79], a[208:211], a[236:239], a[76:79]\n" \
"buffer_load_ushort v210, v195, s[76:79], 0 offen\n" \
"v_mfma_f32_16x16x32_bf16 a[92:95], a[148:151], a[172:175], a[92:95]\n" \
"ds_read_b64_tr_b16 a[188:189], v39 offset:0\n" \
"v_add3_u32 v5, v213, v225, s6\n" \
"v_mfma_f32_16x16x32_bf16 a[92:95], a[212:215], a[236:239], a[92:95]\n" \
"v_mul_u32_u24_e32 v213, 0x480, v228\n" \
"v_add3_u32 v6, v213, v227, s7\n" \
"v_mfma_f32_16x16x32_bf16 a[88:91], a[148:151], a[168:171], a[88:91]\n" \
"s_mov_b32 s76, s8\n" \
"s_and_b32 s77, s9, 0xffff\n" \
"v_mfma_f32_16x16x32_bf16 a[88:91], a[212:215], a[232:235], a[88:91]\n" \
"s_mov_b32 s78, 0x240000\n" \
"ds_read_b64_tr_b16 a[190:191], v39 offset:1024\n" \
"v_mfma_f32_16x16x32_bf16 a[84:87], a[148:151], a[164:167], a[84:87]\n" \
"v_mul_u32_u24_e32 v213, 0x480, v230\n" \
"v_add3_u32 v7, v213, v229, s40\n" \
"v_mfma_f32_16x16x32_bf16 a[84:87], a[212:215], a[228:231], a[84:87]\n" \
"ds_read_b64_tr_b16 a[252:253], v39 offset:8192\n" \
"s_lshl_b32 s7, s3, 3\n" \
"v_mfma_f32_16x16x32_bf16 a[80:83], a[148:151], a[160:163], a[80:83]\n" \
"s_lshl_b32 s40, s3, 1\n" \
"s_and_b32 s7, s7, 16\n" \
"v_mfma_f32_16x16x32_bf16 a[80:83], a[212:215], a[224:227], a[80:83]\n" \
"s_and_b32 s40, s40, 2\n" \
"s_and_b32 s6, s2, 7\n" \
"v_mfma_f32_16x16x32_bf16 a[96:99], a[152:155], a[160:163], a[96:99]\n" \
"ds_read_b64_tr_b16 a[254:255], v39 offset:9216\n" \
"v_bfe_u32 v213, v20, 7, 1\n" \
"v_mfma_f32_16x16x32_bf16 a[96:99], a[216:219], a[224:227], a[96:99]\n" \
"s_or_b32 s7, s40, s7\n" \
".fill (L_pb + 48864 - .) / 4, 4, 0xBF800000\n" \
"L_pp12:\n" \
"s_cbranch_execz L_pret\n" \
"v_and_b32_e32 v234, 0xf0, v234\n" \
"v_mfma_f32_16x16x32_bf16 a[100:103], a[152:155], a[164:167], a[100:103]\n" \
"v_cmp_gt_u32_e32 vcc, 16, v231\n" \
"v_or3_b32 v213, s7, v213, v219\n" \
"v_mfma_f32_16x16x32_bf16 a[100:103], a[216:219], a[228:231], a[100:103]\n" \
"v_and_b32_e32 v214, 32, v20\n" \
"s_lshl_b32 s6, s6, 5\n" \
"v_mfma_f32_16x16x32_bf16 a[104:107], a[152:155], a[168:171], a[104:107]\n" \
"v_cndmask_b32_e32 v231, v233, v234, vcc\n" \
"v_mul_u32_u24_e32 v213, 18, v213\n" \
"v_mfma_f32_16x16x32_bf16 a[104:107], a[216:219], a[232:235], a[104:107]\n" \
"v_mov_b32_e32 v215, 0x100\n" \
"v_mov_b32_e64 v216, s6\n" \
"v_mfma_f32_16x16x32_bf16 a[108:111], a[152:155], a[172:175], a[108:111]\n" \
"v_cmp_eq_u32_e64 vcc, 0, v214\n" \
"v_and_or_b32 v213, v218, 1, v213\n" \
"v_mfma_f32_16x16x32_bf16 a[108:111], a[216:219], a[236:239], a[108:111]\n" \
"v_lshlrev_b32_e32 v212, 3, v212\n" \
"v_cndmask_b32_e32 v214, v215, v216, vcc\n" \
"s_waitcnt lgkmcnt(15)\n" \
"v_mfma_f32_16x16x32_bf16 a[124:127], a[156:159], a[172:175], a[124:127]\n" \
"v_mad_u32_u24 v213, v213, s5, v214\n" \
"v_and_or_b32 v213, v20, 16, v213\n" \
"v_mfma_f32_16x16x32_bf16 a[124:127], a[220:223], a[236:239], a[124:127]\n" \
"v_add_u32_e32 v9, 0x4800, v213\n" \
"v_mul_u32_u24_e32 v213, 0x120, v218\n" \
"v_mfma_f32_16x16x32_bf16 a[120:123], a[156:159], a[168:171], a[120:123]\n" \
"v_and_or_b32 v10, v212, 24, v213\n" \
"v_lshrrev_b32_e32 v213, 2, v20\n" \
"v_mfma_f32_16x16x32_bf16 a[120:123], a[220:223], a[232:235], a[120:123]\n" \
"v_and_or_b32 v213, v213, 60, s4\n" \
"v_lshl_or_b32 v212, s3, 9, v212\n" \
"v_mfma_f32_16x16x32_bf16 a[116:119], a[156:159], a[164:167], a[116:119]\n" \
"v_mad_u32_u24 v4, v224, s5, v223\n" \
"v_mad_u32_u24 v8, v232, s5, v231\n" \
"v_mfma_f32_16x16x32_bf16 a[116:119], a[220:223], a[228:231], a[116:119]\n" \
"v_or_b32_e32 v18, 0x27000, v213\n" \
"v_or_b32_e32 v19, 0x27800, v212\n" \
"v_mfma_f32_16x16x32_bf16 a[112:115], a[156:159], a[160:163], a[112:115]\n" \
"v_mov_b32_e32 v215, 0x1000c0c\n" \
"v_mov_b32_e32 v216, 0xc0c0100\n" \
"v_mfma_f32_16x16x32_bf16 a[112:115], a[220:223], a[224:227], a[112:115]\n" \
"s_waitcnt vmcnt(7) lgkmcnt(0)\n" \
"s_barrier\n" \
"v_mfma_f32_16x16x32_bf16 v[52:55], a[128:131], a[176:179], v[52:55]\n" \
"s_add_u32 m0, s30, 0x0\n" \
"s_movk_i32 s84, 0x200\n" \
"v_mfma_f32_16x16x32_bf16 v[52:55], a[192:195], a[240:243], v[52:55]\n" \
"buffer_load_dwordx4 v22, s[64:67], s84 offen offset:0 lds\n" \
"v_mfma_f32_16x16x32_bf16 v[56:59], a[128:131], a[180:183], v[56:59]\n" \
"ds_read_b64_tr_b16 a[160:161], v32 offset:32768\n" \
"v_bfe_u32 v214, v20, 4, 1\n" \
"v_mfma_f32_16x16x32_bf16 v[56:59], a[192:195], a[244:247], v[56:59]\n" \
"s_movk_i32 s84, 0x1e00\n" \
"v_mfma_f32_16x16x32_bf16 v[60:63], a[128:131], a[184:187], v[60:63]\n" \
"buffer_load_dwordx4 v23, s[64:67], s84 offen offset:1024 lds\n" \
"v_mfma_f32_16x16x32_bf16 v[60:63], a[192:195], a[248:251], v[60:63]\n" \
"ds_read_b64_tr_b16 a[162:163], v32 offset:33792\n" \
"v_lshrrev_b32_e32 v212, 8, v20\n" \
"v_mfma_f32_16x16x32_bf16 v[64:67], a[128:131], a[188:191], v[64:67]\n" \
"ds_read_b64_tr_b16 a[224:225], v32 offset:40960\n" \
"v_cmp_eq_u32_e64 vcc, 0, v214\n" \
"v_mfma_f32_16x16x32_bf16 v[64:67], a[192:195], a[252:255], v[64:67]\n" \
"ds_read_b128 a[128:131], v28 offset:49152\n" \
"v_bfe_u32 v213, v20, 4, 4\n" \
"v_mfma_f32_16x16x32_bf16 v[80:83], a[132:135], a[188:191], v[80:83]\n" \
"ds_read_b128 a[192:195], v29 offset:49152\n" \
"v_mov_b32_e32 v217, 0xc0c0302\n" \
"v_mfma_f32_16x16x32_bf16 v[80:83], a[196:199], a[252:255], v[80:83]\n" \
"s_movk_i32 s84, 0x3a00\n" \
"v_mfma_f32_16x16x32_bf16 v[76:79], a[132:135], a[184:187], v[76:79]\n" \
"buffer_load_dwordx4 v22, s[64:67], s84 offen offset:2048 lds\n" \
"v_mfma_f32_16x16x32_bf16 v[76:79], a[196:199], a[248:251], v[76:79]\n" \
"s_movk_i32 s84, 0x5600\n" \
"v_mfma_f32_16x16x32_bf16 v[72:75], a[132:135], a[180:183], v[72:75]\n" \
"buffer_load_dwordx4 v23, s[64:67], s84 offen offset:3072 lds\n" \
"v_mfma_f32_16x16x32_bf16 v[72:75], a[196:199], a[244:247], v[72:75]\n" \
"s_add_u32 m0, s31, 0x0\n" \
"s_mov_b32 s84, 0x200000\n" \
"v_mfma_f32_16x16x32_bf16 v[68:71], a[132:135], a[176:179], v[68:71]\n" \
"buffer_load_dwordx4 v26, s[68:71], s84 offen offset:0 lds\n" \
"v_mfma_f32_16x16x32_bf16 v[68:71], a[196:199], a[240:243], v[68:71]\n" \
"ds_read_b128 a[132:135], v28 offset:51200\n" \
"v_cndmask_b32_e32 v215, v215, v216, vcc\n" \
"v_mfma_f32_16x16x32_bf16 v[84:87], a[136:139], a[176:179], v[84:87]\n" \
"ds_read_b128 a[196:199], v29 offset:51200\n" \
"v_mov_b32_e32 v216, 0x3020c0c\n" \
"v_mfma_f32_16x16x32_bf16 v[84:87], a[200:203], a[240:243], v[84:87]\n" \
"s_addk_i32 s84, 0x7c00\n" \
"v_mfma_f32_16x16x32_bf16 v[88:91], a[136:139], a[180:183], v[88:91]\n" \
"buffer_load_dwordx4 v26, s[68:71], s84 offen offset:1024 lds\n" \
"v_mfma_f32_16x16x32_bf16 v[88:91], a[200:203], a[244:247], v[88:91]\n" \
"ds_read_b64_tr_b16 a[226:227], v32 offset:41984\n" \
"v_lshl_or_b32 v214, v212, 2, v214\n" \
"v_mfma_f32_16x16x32_bf16 v[92:95], a[136:139], a[184:187], v[92:95]\n" \
"s_addk_i32 s84, 0x7c00\n" \
"v_mfma_f32_16x16x32_bf16 v[92:95], a[200:203], a[248:251], v[92:95]\n" \
"buffer_load_dwordx4 v27, s[68:71], s84 offen offset:2048 lds\n" \
"v_mfma_f32_16x16x32_bf16 v[96:99], a[136:139], a[188:191], v[96:99]\n" \
"ds_read_b64_tr_b16 a[164:165], v33 offset:32768\n" \
"v_cndmask_b32_e64 v216, v216, v217, vcc\n" \
"v_mfma_f32_16x16x32_bf16 v[96:99], a[200:203], a[252:255], v[96:99]\n" \
"ds_read_b128 a[136:139], v28 offset:53248\n" \
"v_mov_b32_e32 v217, 0xc0c0c0c\n" \
"v_mfma_f32_16x16x32_bf16 v[112:115], a[140:143], a[188:191], v[112:115]\n" \
"ds_read_b128 a[200:203], v29 offset:53248\n" \
"v_cmp_gt_u32_e64 vcc, v214, v213\n" \
"v_mfma_f32_16x16x32_bf16 v[112:115], a[204:207], a[252:255], v[112:115]\n" \
"s_addk_i32 s84, 0x7c00\n" \
"v_mfma_f32_16x16x32_bf16 v[108:111], a[140:143], a[184:187], v[108:111]\n" \
"buffer_load_dwordx4 v27, s[68:71], s84 offen offset:3072 lds\n" \
"v_mfma_f32_16x16x32_bf16 v[108:111], a[204:207], a[248:251], v[108:111]\n" \
"s_add_u32 m0, s30, 0x1000\n" \
"s_mov_b32 s84, 0x8200\n" \
"v_mfma_f32_16x16x32_bf16 v[104:107], a[140:143], a[180:183], v[104:107]\n" \
"buffer_load_dwordx4 v22, s[64:67], s84 offen offset:0 lds\n" \
"v_mfma_f32_16x16x32_bf16 v[104:107], a[204:207], a[244:247], v[104:107]\n" \
"ds_read_b64_tr_b16 a[166:167], v33 offset:33792\n" \
"v_or_b32_e32 v214, 2, v214\n" \
"v_mfma_f32_16x16x32_bf16 v[100:103], a[140:143], a[176:179], v[100:103]\n" \
"ds_read_b64_tr_b16 a[228:229], v33 offset:40960\n" \
"s_lshr_b32 s4, s2, 5\n" \
"v_mfma_f32_16x16x32_bf16 v[100:103], a[204:207], a[240:243], v[100:103]\n" \
"ds_read_b128 a[140:143], v28 offset:55296\n" \
"v_cndmask_b32_e32 v40, v217, v215, vcc\n" \
"v_mfma_f32_16x16x32_bf16 v[116:119], a[144:147], a[176:179], v[116:119]\n" \
"ds_read_b128 a[204:207], v29 offset:55296\n" \
"v_cndmask_b32_e64 v42, v215, v217, vcc\n" \
"v_mfma_f32_16x16x32_bf16 v[116:119], a[208:211], a[240:243], v[116:119]\n" \
"s_addk_i32 s84, 0x1c00\n" \
"v_mfma_f32_16x16x32_bf16 v[120:123], a[144:147], a[180:183], v[120:123]\n" \
"buffer_load_dwordx4 v23, s[64:67], s84 offen offset:1024 lds\n" \
"v_mfma_f32_16x16x32_bf16 v[120:123], a[208:211], a[244:247], v[120:123]\n" \
"s_addk_i32 s84, 0x1c00\n" \
"v_mfma_f32_16x16x32_bf16 v[124:127], a[144:147], a[184:187], v[124:127]\n" \
"buffer_load_dwordx4 v22, s[64:67], s84 offen offset:2048 lds\n" \
"v_mfma_f32_16x16x32_bf16 v[124:127], a[208:211], a[248:251], v[124:127]\n" \
"s_addk_i32 s84, 0x1c00\n" \
"v_mfma_f32_16x16x32_bf16 v[128:131], a[144:147], a[188:191], v[128:131]\n" \
"buffer_load_dwordx4 v23, s[64:67], s84 offen offset:3072 lds\n" \
"v_mfma_f32_16x16x32_bf16 v[128:131], a[208:211], a[252:255], v[128:131]\n" \
"ds_read_b128 a[144:147], v28 offset:32768\n" \
"v_cmp_gt_u32_e32 vcc, v214, v213\n" \
"v_mfma_f32_16x16x32_bf16 v[144:147], a[148:151], a[188:191], v[144:147]\n" \
"ds_read_b128 a[208:211], v29 offset:32768\n" \
"v_and_b32_e64 v214, 16, v20\n" \
"v_mfma_f32_16x16x32_bf16 v[144:147], a[212:215], a[252:255], v[144:147]\n" \
"s_add_u32 m0, s31, 0x4000\n" \
"s_mov_b32 s84, 0x200000\n" \
"v_mfma_f32_16x16x32_bf16 v[140:143], a[148:151], a[184:187], v[140:143]\n" \
"buffer_load_dwordx4 v26, s[72:75], s84 offen offset:0 lds\n" \
"v_mfma_f32_16x16x32_bf16 v[140:143], a[212:215], a[248:251], v[140:143]\n" \
"ds_read_b64_tr_b16 a[230:231], v33 offset:41984\n" \
"s_movk_i32 s42, 0x3f80\n" \
"v_mfma_f32_16x16x32_bf16 v[136:139], a[148:151], a[180:183], v[136:139]\n" \
"s_addk_i32 s84, 0x7c00\n" \
"v_mfma_f32_16x16x32_bf16 v[136:139], a[212:215], a[244:247], v[136:139]\n" \
"buffer_load_dwordx4 v26, s[72:75], s84 offen offset:1024 lds\n" \
"v_mfma_f32_16x16x32_bf16 v[132:135], a[148:151], a[176:179], v[132:135]\n" \
"ds_read_b64_tr_b16 a[168:169], v34 offset:32768\n" \
"s_xor_b32 s5, s4, 15\n" \
"s_nop 0\n" \
"v_mfma_f32_16x16x32_bf16 v[132:135], a[212:215], a[240:243], v[132:135]\n" \
"ds_read_b128 a[148:151], v28 offset:34816\n" \
"v_mfma_f32_16x16x32_bf16 v[148:151], a[152:155], a[176:179], v[148:151]\n" \
"ds_read_b128 a[212:215], v29 offset:34816\n" \
"v_mfma_f32_16x16x32_bf16 v[148:151], a[216:219], a[240:243], v[148:151]\n" \
"ds_read_b64_tr_b16 a[170:171], v34 offset:33792\n" \
"v_mfma_f32_16x16x32_bf16 v[152:155], a[152:155], a[180:183], v[152:155]\n" \
"ds_read_b64_tr_b16 a[232:233], v34 offset:40960\n" \
"v_mfma_f32_16x16x32_bf16 v[152:155], a[216:219], a[244:247], v[152:155]\n" \
"ds_read_b64_tr_b16 a[234:235], v34 offset:41984\n" \
"v_mfma_f32_16x16x32_bf16 v[156:159], a[152:155], a[184:187], v[156:159]\n" \
"ds_read_b64_tr_b16 a[172:173], v35 offset:32768\n" \
"v_mfma_f32_16x16x32_bf16 v[156:159], a[216:219], a[248:251], v[156:159]\n" \
"ds_read_b64_tr_b16 a[174:175], v35 offset:33792\n" \
"v_mfma_f32_16x16x32_bf16 v[160:163], a[152:155], a[188:191], v[160:163]\n" \
"ds_read_b64_tr_b16 a[236:237], v35 offset:40960\n" \
"v_mfma_f32_16x16x32_bf16 v[160:163], a[216:219], a[252:255], v[160:163]\n" \
"ds_read_b128 a[152:155], v28 offset:36864\n" \
"v_mfma_f32_16x16x32_bf16 v[176:179], a[156:159], a[188:191], v[176:179]\n" \
"ds_read_b128 a[216:219], v29 offset:36864\n" \
"v_mfma_f32_16x16x32_bf16 v[176:179], a[220:223], a[252:255], v[176:179]\n" \
"ds_read_b64_tr_b16 a[238:239], v35 offset:41984\n" \
"v_mfma_f32_16x16x32_bf16 v[172:175], a[156:159], a[184:187], v[172:175]\n" \
"s_cmpk_lt_u32 s3, 2\n" \
"s_cselect_b32 s6, s5, s4\n" \
"v_mfma_f32_16x16x32_bf16 v[172:175], a[220:223], a[248:251], v[172:175]\n" \
"s_lshl_b32 s7, s6, 7\n" \
"v_and_b32_e32 v213, 0x320, v20\n" \
"v_mfma_f32_16x16x32_bf16 v[168:171], a[156:159], a[180:183], v[168:171]\n" \
"s_movk_i32 s41, 0x100\n" \
"v_lshlrev_b32_e64 v214, v214, s42\n" \
"v_mfma_f32_16x16x32_bf16 v[168:171], a[220:223], a[244:247], v[168:171]\n" \
"v_mov_b32_e32 v215, 0\n" \
"s_add_i32 s40, s7, -16\n" \
"v_mfma_f32_16x16x32_bf16 v[164:167], a[156:159], a[176:179], v[164:167]\n" \
"v_cndmask_b32_e32 v41, v217, v216, vcc\n" \
"v_cndmask_b32_e32 v43, v216, v217, vcc\n" \
"v_mfma_f32_16x16x32_bf16 v[164:167], a[220:223], a[240:243], v[164:167]\n" \
"ds_read_b128 a[156:159], v28 offset:38912\n" \
"ds_read_b128 a[220:223], v29 offset:38912\n" \
"s_waitcnt lgkmcnt(15)\n" \
"v_mfma_f32_16x16x32_bf16 a[0:3], a[128:131], a[160:163], a[0:3]\n" \
"s_addk_i32 s84, 0x7c00\n" \
"v_mfma_f32_16x16x32_bf16 a[0:3], a[192:195], a[224:227], a[0:3]\n" \
"buffer_load_dwordx4 v27, s[72:75], s84 offen offset:2048 lds\n" \
"v_mfma_f32_16x16x32_bf16 a[4:7], a[128:131], a[164:167], a[4:7]\n" \
"ds_read_b64_tr_b16 a[176:177], v36 offset:32768\n" \
"v_cmp_eq_u32_e32 vcc, s41, v213\n" \
"s_waitcnt lgkmcnt(15)\n" \
"v_mfma_f32_16x16x32_bf16 a[4:7], a[192:195], a[228:231], a[4:7]\n" \
"s_addk_i32 s84, 0x7c00\n" \
"s_waitcnt lgkmcnt(11)\n" \
"v_mfma_f32_16x16x32_bf16 a[8:11], a[128:131], a[168:171], a[8:11]\n" \
"buffer_load_dwordx4 v27, s[72:75], s84 offen offset:3072 lds\n" \
"s_waitcnt lgkmcnt(9)\n" \
"v_mfma_f32_16x16x32_bf16 a[8:11], a[192:195], a[232:235], a[8:11]\n" \
"ds_read_b64_tr_b16 a[178:179], v36 offset:33792\n" \
"s_mov_b32 s41, 0\n" \
"s_waitcnt lgkmcnt(8)\n" \
"v_mfma_f32_16x16x32_bf16 a[12:15], a[128:131], a[172:175], a[12:15]\n" \
"ds_read_b64_tr_b16 a[240:241], v36 offset:40960\n" \
"s_and_b32 s43, s3, 1\n" \
"s_waitcnt lgkmcnt(5)\n" \
"v_mfma_f32_16x16x32_bf16 a[12:15], a[192:195], a[236:239], a[12:15]\n" \
"v_cndmask_b32_e32 v46, v215, v214, vcc\n" \
"v_and_b32_e32 v213, 0x330, v20\n" \
"v_mfma_f32_16x16x32_bf16 a[28:31], a[132:135], a[172:175], a[28:31]\n" \
"s_cmp_lg_u32 s7, 0\n" \
"s_cselect_b32 s40, s40, s41\n" \
"v_mfma_f32_16x16x32_bf16 a[28:31], a[196:199], a[236:239], a[28:31]\n" \
"ds_read_b64_tr_b16 a[242:243], v36 offset:41984\n" \
"s_mov_b32 s42, 0xff800000\n" \
"v_mfma_f32_16x16x32_bf16 a[24:27], a[132:135], a[168:171], a[24:27]\n" \
"s_cmp_lg_u32 s7, 0\n" \
"s_cselect_b32 s47, s41, s42\n" \
"v_mfma_f32_16x16x32_bf16 a[24:27], a[196:199], a[232:235], a[24:27]\n" \
"s_lshl_b32 s41, s43, 6\n" \
"v_lshrrev_b32_e32 v215, 6, v20\n" \
"v_mfma_f32_16x16x32_bf16 a[20:23], a[132:135], a[164:167], a[20:23]\n" \
"v_mov_b32_e32 v214, 0x3f80\n" \
"v_cmp_eq_u32_e64 vcc, 32, v213\n" \
"v_mfma_f32_16x16x32_bf16 a[20:23], a[196:199], a[228:231], a[20:23]\n" \
"ds_read_b64_tr_b16 a[180:181], v37 offset:32768\n" \
"s_or_b32 s7, s41, s7\n" \
"v_mfma_f32_16x16x32_bf16 a[16:19], a[132:135], a[160:163], a[16:19]\n" \
"v_lshrrev_b32_e32 v213, 7, v20\n" \
"v_bfe_u32 v216, v20, 6, 2\n" \
"v_mfma_f32_16x16x32_bf16 a[16:19], a[196:199], a[224:227], a[16:19]\n" \
"v_lshrrev_b32_e64 v219, 1, v20\n" \
"v_bfe_u32 v215, v215, 1, 1\n" \
"v_mfma_f32_16x16x32_bf16 a[32:35], a[136:139], a[160:163], a[32:35]\n" \
"s_waitcnt vmcnt(16)\n" \
"v_or_b32_e32 v204, v210, v204\n" \
"v_or_b32_e64 v206, v210, v206\n" \
"v_mfma_f32_16x16x32_bf16 a[32:35], a[200:203], a[224:227], a[32:35]\n" \
"v_mov_b32_e64 v210, 0\n" \
"s_add_u32 s88, s35, 0x1000\n" \
"v_mfma_f32_16x16x32_bf16 a[36:39], a[136:139], a[164:167], a[36:39]\n" \
"ds_read_b64_tr_b16 a[182:183], v37 offset:33792\n" \
"v_cndmask_b32_e64 v47, 0, v214, vcc\n" \
"v_mfma_f32_16x16x32_bf16 a[36:39], a[200:203], a[228:231], a[36:39]\n" \
"ds_read_b64_tr_b16 a[244:245], v37 offset:40960\n" \
"v_or_b32_e64 v214, s7, v213\n" \
"v_mfma_f32_16x16x32_bf16 a[40:43], a[136:139], a[168:171], a[40:43]\n" \
"s_mov_b32 m0, s32\n" \
"v_lshl_or_b32 v217, s43, 3, v213\n" \
"v_mfma_f32_16x16x32_bf16 a[40:43], a[200:203], a[232:235], a[40:43]\n" \
"s_mov_b32 exec_lo, 0xffffffff\n" \
"s_mov_b32 exec_hi, 0x0\n" \
"global_load_lds_dword v[188:189], off\n" \
"s_mov_b64 exec, -1\n" \
"v_mfma_f32_16x16x32_bf16 a[44:47], a[136:139], a[172:175], a[44:47]\n" \
"ds_read_b64_tr_b16 a[246:247], v37 offset:41984\n" \
"v_lshlrev_b32_e64 v218, 10, v212\n" \
"v_mfma_f32_16x16x32_bf16 a[44:47], a[200:203], a[236:239], a[44:47]\n" \
"v_lshlrev_b32_e32 v216, 7, v216\n" \
"v_and_b32_e32 v219, 24, v219\n" \
"v_mfma_f32_16x16x32_bf16 a[60:63], a[140:143], a[172:175], a[60:63]\n" \
"ds_write_b128 v196, v[200:203]\n" \
"v_mfma_f32_16x16x32_bf16 a[60:63], a[204:207], a[236:239], a[60:63]\n" \
"v_and_or_b32 v213, v213, 2, v215\n" \
"v_add_u32_e64 v217, s40, v217\n" \
"v_mfma_f32_16x16x32_bf16 a[56:59], a[140:143], a[168:171], a[56:59]\n" \
"ds_read_b64_tr_b16 a[184:185], v38 offset:32768\n" \
"v_or3_b32 v216, v216, v218, v219\n" \
"v_mfma_f32_16x16x32_bf16 a[56:59], a[204:207], a[232:235], a[56:59]\n" \
"v_lshlrev_b32_e64 v213, 5, v213\n" \
"s_mov_b32 s40, 0x14000\n" \
"v_mfma_f32_16x16x32_bf16 a[52:55], a[140:143], a[164:167], a[52:55]\n" \
"s_lshl_b32 s7, s2, 2\n" \
"v_or3_b32 v48, v216, v213, s40\n" \
"v_mfma_f32_16x16x32_bf16 a[52:55], a[204:207], a[228:231], a[52:55]\n" \
"ds_read_b64_tr_b16 a[186:187], v38 offset:33792\n" \
"s_lshl_b32 s40, s43, 2\n" \
"v_mfma_f32_16x16x32_bf16 a[48:51], a[140:143], a[160:163], a[48:51]\n" \
"s_lshl_b32 s6, s6, 3\n" \
"v_or_b32_e32 v213, s40, v212\n" \
"v_mfma_f32_16x16x32_bf16 a[48:51], a[204:207], a[224:227], a[48:51]\n" \
"s_or_b32 s6, s6, s40\n" \
"s_and_b32 s40, s7, 0x7c\n" \
"v_mfma_f32_16x16x32_bf16 a[64:67], a[144:147], a[160:163], a[64:67]\n" \
"ds_write_b128 v197, v[204:207]\n" \
"v_mfma_f32_16x16x32_bf16 a[64:67], a[208:211], a[224:227], a[64:67]\n" \
"ds_read_b64_tr_b16 a[248:249], v38 offset:40960\n" \
"s_or_b32 s40, s40, s3\n" \
"v_mfma_f32_16x16x32_bf16 a[68:71], a[144:147], a[164:167], a[68:71]\n" \
"s_cmpk_lt_u32 s40, 64\n" \
"s_cselect_b32 s41, s4, s5\n" \
"v_mfma_f32_16x16x32_bf16 a[68:71], a[208:211], a[228:231], a[68:71]\n" \
"ds_read_b64_tr_b16 a[250:251], v38 offset:41984\n" \
"s_lshl_b32 s40, s40, 1\n" \
"v_mfma_f32_16x16x32_bf16 a[72:75], a[144:147], a[168:171], a[72:75]\n" \
"s_lshl_b32 s41, s41, 7\n" \
"s_and_b32 s40, s40, 0x7e\n" \
"v_mfma_f32_16x16x32_bf16 a[72:75], a[208:211], a[232:235], a[72:75]\n" \
"s_or_b32 s40, s41, s40\n" \
"s_mulk_i32 s40, 0x480\n" \
"v_mfma_f32_16x16x32_bf16 a[76:79], a[144:147], a[172:175], a[76:79]\n" \
"v_add_u32_e64 v49, s40, v20\n" \
"s_mov_b32 s40, 0xc000\n" \
"v_mfma_f32_16x16x32_bf16 a[76:79], a[208:211], a[236:239], a[76:79]\n" \
"ds_read_b64_tr_b16 a[188:189], v39 offset:32768\n" \
"s_and_b32 s7, s7, 28\n" \
"v_mfma_f32_16x16x32_bf16 a[92:95], a[148:151], a[172:175], a[92:95]\n" \
"ds_write_b128 v198, v[208:211]\n" \
"v_mfma_f32_16x16x32_bf16 a[92:95], a[212:215], a[236:239], a[92:95]\n" \
"s_bfe_u32 s45, s2, 0x20003\n" \
"s_mov_b32 s41, 0x8000\n" \
"v_mfma_f32_16x16x32_bf16 a[88:91], a[148:151], a[168:171], a[88:91]\n" \
"ds_read_b64_tr_b16 a[190:191], v39 offset:33792\n" \
"v_mfma_f32_16x16x32_bf16 a[88:91], a[212:215], a[232:235], a[88:91]\n" \
"s_cmp_lg_u32 s4, 0\n" \
"s_cselect_b32 s51, s40, s41\n" \
"v_mfma_f32_16x16x32_bf16 a[84:87], a[148:151], a[164:167], a[84:87]\n" \
"s_movk_i32 s40, 0x4000\n" \
"s_lshr_b32 s44, s3, 1\n" \
"v_mfma_f32_16x16x32_bf16 a[84:87], a[212:215], a[228:231], a[84:87]\n" \
"ds_read_b64_tr_b16 a[252:253], v39 offset:40960\n" \
"s_or_b32 s46, s7, s45\n" \
"v_mfma_f32_16x16x32_bf16 a[80:83], a[148:151], a[160:163], a[80:83]\n" \
"s_cmp_lg_u32 s4, 0\n" \
"s_cselect_b32 s53, s41, s40\n" \
"v_mfma_f32_16x16x32_bf16 a[80:83], a[212:215], a[224:227], a[80:83]\n" \
"s_lshl_b32 s40, s44, 15\n" \
"s_lshl_b32 s41, s43, 14\n" \
"v_mfma_f32_16x16x32_bf16 a[96:99], a[152:155], a[160:163], a[96:99]\n" \
"ds_read_b64_tr_b16 a[254:255], v39 offset:41984\n" \
"s_lshl_b32 s48, s46, 20\n" \
"v_mfma_f32_16x16x32_bf16 a[96:99], a[216:219], a[224:227], a[96:99]\n" \
"s_or_b32 s41, s40, s41\n" \
"s_add_i32 s49, s48, 0x1000000\n" \
"v_mfma_f32_16x16x32_bf16 a[100:103], a[152:155], a[164:167], a[100:103]\n" \
"s_or_b32 s42, s49, s41\n" \
"s_lshl_b32 s4, s4, 16\n" \
"v_mfma_f32_16x16x32_bf16 a[100:103], a[216:219], a[228:231], a[100:103]\n" \
"s_or_b32 s41, s42, s4\n" \
"s_lshl_b32 s4, s5, 16\n" \
"v_mfma_f32_16x16x32_bf16 a[104:107], a[152:155], a[168:171], a[104:107]\n" \
"s_mul_i32 s5, s6, 0x24800\n" \
"s_lshl_b32 s6, s46, 9\n" \
"v_mfma_f32_16x16x32_bf16 a[104:107], a[216:219], a[232:235], a[104:107]\n" \
"s_lshl_b32 s7, s7, 6\n" \
"s_add_i32 s6, s5, s6\n" \
"v_mfma_f32_16x16x32_bf16 a[108:111], a[152:155], a[172:175], a[108:111]\n" \
"s_or_b32 s5, s5, s7\n" \
"s_lshl_b32 s7, s45, 2\n" \
"v_mfma_f32_16x16x32_bf16 a[108:111], a[216:219], a[236:239], a[108:111]\n" \
"s_or_b32 s5, s5, s7\n" \
"s_lshl_b32 s7, s3, 12\n" \
"s_waitcnt lgkmcnt(15)\n" \
"v_mfma_f32_16x16x32_bf16 a[124:127], a[156:159], a[172:175], a[124:127]\n" \
"s_or_b32 s50, s49, s7\n" \
"s_lshl_b32 s7, s3, 11\n" \
"v_mfma_f32_16x16x32_bf16 a[124:127], a[220:223], a[236:239], a[124:127]\n" \
"v_lshrrev_b32_e64 v215, 4, v20\n" \
"v_bfe_u32 v216, v20, 4, 3\n" \
"v_mfma_f32_16x16x32_bf16 a[120:123], a[156:159], a[168:171], a[120:123]\n" \
"s_or_b32 s42, s42, s4\n" \
"s_lshl_b32 s4, s43, 11\n" \
"v_mfma_f32_16x16x32_bf16 a[120:123], a[220:223], a[232:235], a[120:123]\n" \
"s_add_i32 s52, s48, s7\n" \
"s_add_i32 s53, s53, s7\n" \
"v_mfma_f32_16x16x32_bf16 a[116:119], a[156:159], a[164:167], a[116:119]\n" \
"v_mul_u32_u24_e32 v214, 0x480, v214\n" \
"v_bitop3_b32 v218, v212, v215, 7 bitop3:0x78\n" \
"v_mfma_f32_16x16x32_bf16 a[116:119], a[220:223], a[228:231], a[116:119]\n" \
"v_bitop3_b32 v212, v212, v216, 4 bitop3:0x36\n" \
"s_lshl_b32 s7, s3, 10\n" \
"v_mfma_f32_16x16x32_bf16 a[112:115], a[156:159], a[160:163], a[112:115]\n" \
"s_or_b32 s4, s40, s4\n" \
"v_lshl_or_b32 v25, v212, 4, v214\n" \
"v_mfma_f32_16x16x32_bf16 a[112:115], a[220:223], a[224:227], a[112:115]\n" \
"s_waitcnt vmcnt(1) lgkmcnt(0)\n" \
"s_barrier\n" \
"v_mfma_f32_16x16x32_bf16 v[52:55], a[128:131], a[176:179], v[52:55]\n" \
"s_add_u32 m0, s30, 0x8000\n" \
"s_movk_i32 s84, 0x280\n" \
"v_mfma_f32_16x16x32_bf16 v[52:55], a[192:195], a[240:243], v[52:55]\n" \
"buffer_load_dwordx4 v22, s[64:67], s84 offen offset:0 lds\n" \
"v_mfma_f32_16x16x32_bf16 v[56:59], a[128:131], a[180:183], v[56:59]\n" \
"s_movk_i32 s84, 0x1e80\n" \
"v_mfma_f32_16x16x32_bf16 v[56:59], a[192:195], a[244:247], v[56:59]\n" \
"buffer_load_dwordx4 v23, s[64:67], s84 offen offset:1024 lds\n" \
"v_mfma_f32_16x16x32_bf16 v[60:63], a[128:131], a[184:187], v[60:63]\n" \
"ds_read_b64_tr_b16 a[160:161], v32 offset:0\n" \
"v_mul_u32_u24_e32 v212, 0x480, v217\n" \
"v_mfma_f32_16x16x32_bf16 v[60:63], a[192:195], a[248:251], v[60:63]\n" \
"s_movk_i32 s84, 0x3a80\n" \
"v_mfma_f32_16x16x32_bf16 v[64:67], a[128:131], a[188:191], v[64:67]\n" \
"buffer_load_dwordx4 v22, s[64:67], s84 offen offset:2048 lds\n" \
"v_mfma_f32_16x16x32_bf16 v[64:67], a[192:195], a[252:255], v[64:67]\n" \
"ds_read_b128 a[128:131], v28 offset:16384\n" \
"v_bitop3_b32 v213, v215, v213, 7 bitop3:0x6c\n" \
"v_mfma_f32_16x16x32_bf16 v[80:83], a[132:135], a[188:191], v[80:83]\n" \
"ds_read_b128 a[192:195], v29 offset:16384\n" \
"s_or_b32 s40, s7, 0x14000\n" \
"v_mfma_f32_16x16x32_bf16 v[80:83], a[196:199], a[252:255], v[80:83]\n" \
"s_movk_i32 s84, 0x5680\n" \
"v_mfma_f32_16x16x32_bf16 v[76:79], a[132:135], a[184:187], v[76:79]\n" \
"buffer_load_dwordx4 v23, s[64:67], s84 offen offset:3072 lds\n" \
"v_mfma_f32_16x16x32_bf16 v[76:79], a[196:199], a[248:251], v[76:79]\n" \
"s_add_u32 m0, s31, 0x8000\n" \
"s_mov_b32 s84, 0x280000\n" \
"v_mfma_f32_16x16x32_bf16 v[72:75], a[132:135], a[180:183], v[72:75]\n" \
"buffer_load_dwordx4 v26, s[68:71], s84 offen offset:0 lds\n" \
"v_mfma_f32_16x16x32_bf16 v[72:75], a[196:199], a[244:247], v[72:75]\n" \
"ds_read_b64_tr_b16 a[162:163], v32 offset:1024\n" \
"s_lshl_b32 s43, s44, 13\n" \
"v_mfma_f32_16x16x32_bf16 v[68:71], a[132:135], a[176:179], v[68:71]\n" \
"ds_read_b64_tr_b16 a[224:225], v32 offset:8192\n" \
"s_mulk_i32 s44, 0xe800\n" \
"v_mfma_f32_16x16x32_bf16 v[68:71], a[196:199], a[240:243], v[68:71]\n" \
"ds_read_b128 a[132:135], v28 offset:18432\n" \
"s_lshl_b32 s7, s3, 8\n" \
"v_mfma_f32_16x16x32_bf16 v[84:87], a[136:139], a[176:179], v[84:87]\n" \
"ds_read_b128 a[196:199], v29 offset:18432\n" \
"v_lshl_or_b32 v24, v218, 4, v214\n" \
"v_mfma_f32_16x16x32_bf16 v[84:87], a[200:203], a[240:243], v[84:87]\n" \
"s_addk_i32 s84, 0x7c00\n" \
"v_mfma_f32_16x16x32_bf16 v[88:91], a[136:139], a[180:183], v[88:91]\n" \
"buffer_load_dwordx4 v26, s[68:71], s84 offen offset:1024 lds\n" \
"v_mfma_f32_16x16x32_bf16 v[88:91], a[200:203], a[244:247], v[88:91]\n" \
"s_addk_i32 s84, 0x7c00\n" \
"v_mfma_f32_16x16x32_bf16 v[92:95], a[136:139], a[184:187], v[92:95]\n" \
"buffer_load_dwordx4 v27, s[68:71], s84 offen offset:2048 lds\n" \
"v_mfma_f32_16x16x32_bf16 v[92:95], a[200:203], a[248:251], v[92:95]\n" \
"s_addk_i32 s84, 0x7c00\n" \
"v_mfma_f32_16x16x32_bf16 v[96:99], a[136:139], a[188:191], v[96:99]\n" \
"buffer_load_dwordx4 v27, s[68:71], s84 offen offset:3072 lds\n" \
"v_mfma_f32_16x16x32_bf16 v[96:99], a[200:203], a[252:255], v[96:99]\n" \
"ds_read_b128 a[136:139], v28 offset:20480\n" \
"v_lshl_or_b32 v21, v213, 4, v212\n" \
"v_mfma_f32_16x16x32_bf16 v[112:115], a[140:143], a[188:191], v[112:115]\n" \
"ds_read_b128 a[200:203], v29 offset:20480\n" \
"s_add_i32 s44, s44, 0x17000\n" \
"v_mfma_f32_16x16x32_bf16 v[112:115], a[204:207], a[252:255], v[112:115]\n" \
"s_add_u32 m0, s30, 0x9000\n" \
"s_mov_b32 s84, 0x8280\n" \
"v_mfma_f32_16x16x32_bf16 v[108:111], a[140:143], a[184:187], v[108:111]\n" \
"buffer_load_dwordx4 v22, s[64:67], s84 offen offset:0 lds\n" \
"v_mfma_f32_16x16x32_bf16 v[108:111], a[204:207], a[248:251], v[108:111]\n" \
"s_addk_i32 s84, 0x1c00\n" \
"v_mfma_f32_16x16x32_bf16 v[104:107], a[140:143], a[180:183], v[104:107]\n" \
"buffer_load_dwordx4 v23, s[64:67], s84 offen offset:1024 lds\n" \
"v_mfma_f32_16x16x32_bf16 v[104:107], a[204:207], a[244:247], v[104:107]\n" \
"s_addk_i32 s84, 0x1c00\n" \
"v_mfma_f32_16x16x32_bf16 v[100:103], a[140:143], a[176:179], v[100:103]\n" \
"buffer_load_dwordx4 v22, s[64:67], s84 offen offset:2048 lds\n" \
"v_mfma_f32_16x16x32_bf16 v[100:103], a[204:207], a[240:243], v[100:103]\n" \
"ds_read_b128 a[140:143], v28 offset:22528\n" \
"s_or_b32 s45, s7, 0x27000\n" \
"v_mfma_f32_16x16x32_bf16 v[116:119], a[144:147], a[176:179], v[116:119]\n" \
"ds_read_b128 a[204:207], v29 offset:22528\n" \
"s_or_b32 s46, s4, 0x10000\n" \
"v_mfma_f32_16x16x32_bf16 v[116:119], a[208:211], a[240:243], v[116:119]\n" \
"s_addk_i32 s84, 0x1c00\n" \
"v_mfma_f32_16x16x32_bf16 v[120:123], a[144:147], a[180:183], v[120:123]\n" \
"buffer_load_dwordx4 v23, s[64:67], s84 offen offset:3072 lds\n" \
"v_mfma_f32_16x16x32_bf16 v[120:123], a[208:211], a[244:247], v[120:123]\n" \
"s_add_u32 m0, s31, 0xc000\n" \
"s_mov_b32 s84, 0x280000\n" \
"v_mfma_f32_16x16x32_bf16 v[124:127], a[144:147], a[184:187], v[124:127]\n" \
"buffer_load_dwordx4 v26, s[72:75], s84 offen offset:0 lds\n" \
"v_mfma_f32_16x16x32_bf16 v[124:127], a[208:211], a[248:251], v[124:127]\n" \
"s_addk_i32 s84, 0x7c00\n" \
"v_mfma_f32_16x16x32_bf16 v[128:131], a[144:147], a[188:191], v[128:131]\n" \
"buffer_load_dwordx4 v26, s[72:75], s84 offen offset:1024 lds\n" \
"v_mfma_f32_16x16x32_bf16 v[128:131], a[208:211], a[252:255], v[128:131]\n" \
"ds_read_b128 a[144:147], v28 offset:0\n" \
"s_add_i32 s48, s6, 0x3000000\n" \
"v_mfma_f32_16x16x32_bf16 v[144:147], a[148:151], a[188:191], v[144:147]\n" \
"ds_read_b128 a[208:211], v29 offset:0\n" \
"s_add_i32 s49, s5, 0x3004000\n" \
"v_mfma_f32_16x16x32_bf16 v[144:147], a[212:215], a[252:255], v[144:147]\n" \
"ds_read_b128 v[200:203], v199 offset:0\n" \
"s_add_i32 s52, s52, 0x1010000\n" \
"v_mfma_f32_16x16x32_bf16 v[140:143], a[148:151], a[184:187], v[140:143]\n" \
"ds_read_b128 v[204:207], v199 offset:4096\n" \
"v_mfma_f32_16x16x32_bf16 v[140:143], a[212:215], a[248:251], v[140:143]\n" \
"ds_read_b64_tr_b16 a[226:227], v32 offset:9216\n" \
"v_mfma_f32_16x16x32_bf16 v[136:139], a[148:151], a[180:183], v[136:139]\n" \
"ds_read_b64_tr_b16 a[164:165], v33 offset:0\n" \
"v_mfma_f32_16x16x32_bf16 v[136:139], a[212:215], a[244:247], v[136:139]\n" \
"ds_read_b128 v[208:211], v199 offset:8192\n" \
"v_mfma_f32_16x16x32_bf16 v[132:135], a[148:151], a[176:179], v[132:135]\n" \
"ds_read_b64_tr_b16 a[166:167], v33 offset:1024\n" \
"v_mfma_f32_16x16x32_bf16 v[132:135], a[212:215], a[240:243], v[132:135]\n" \
"ds_read_b128 a[148:151], v28 offset:2048\n" \
"v_mfma_f32_16x16x32_bf16 v[148:151], a[152:155], a[176:179], v[148:151]\n" \
"ds_read_b128 a[212:215], v29 offset:2048\n" \
"v_mfma_f32_16x16x32_bf16 v[148:151], a[216:219], a[240:243], v[148:151]\n" \
"ds_read_b64_tr_b16 a[228:229], v33 offset:8192\n" \
"v_mfma_f32_16x16x32_bf16 v[152:155], a[152:155], a[180:183], v[152:155]\n" \
"ds_read_b64_tr_b16 a[230:231], v33 offset:9216\n" \
"v_mfma_f32_16x16x32_bf16 v[152:155], a[216:219], a[244:247], v[152:155]\n" \
"ds_read_b64_tr_b16 a[168:169], v34 offset:0\n" \
"v_mfma_f32_16x16x32_bf16 v[156:159], a[152:155], a[184:187], v[156:159]\n" \
"ds_read_b64_tr_b16 a[170:171], v34 offset:1024\n" \
"v_mfma_f32_16x16x32_bf16 v[156:159], a[216:219], a[248:251], v[156:159]\n" \
"ds_read_b64_tr_b16 a[232:233], v34 offset:8192\n" \
"v_mfma_f32_16x16x32_bf16 v[160:163], a[152:155], a[188:191], v[160:163]\n" \
"ds_read_b64_tr_b16 a[234:235], v34 offset:9216\n" \
"v_mfma_f32_16x16x32_bf16 v[160:163], a[216:219], a[252:255], v[160:163]\n" \
"ds_read_b128 a[152:155], v28 offset:4096\n" \
"v_mfma_f32_16x16x32_bf16 v[176:179], a[156:159], a[188:191], v[176:179]\n" \
"ds_read_b128 a[216:219], v29 offset:4096\n" \
"v_mfma_f32_16x16x32_bf16 v[176:179], a[220:223], a[252:255], v[176:179]\n" \
"ds_read_b64_tr_b16 a[172:173], v35 offset:0\n" \
"v_mfma_f32_16x16x32_bf16 v[172:175], a[156:159], a[184:187], v[172:175]\n" \
"ds_read_b64_tr_b16 a[174:175], v35 offset:1024\n" \
"v_mfma_f32_16x16x32_bf16 v[172:175], a[220:223], a[248:251], v[172:175]\n" \
"ds_read_b64_tr_b16 a[236:237], v35 offset:8192\n" \
"v_mfma_f32_16x16x32_bf16 v[168:171], a[156:159], a[180:183], v[168:171]\n" \
"ds_read_b64_tr_b16 a[238:239], v35 offset:9216\n" \
"v_mfma_f32_16x16x32_bf16 v[168:171], a[220:223], a[244:247], v[168:171]\n" \
"v_mfma_f32_16x16x32_bf16 v[164:167], a[156:159], a[176:179], v[164:167]\n" \
"v_mfma_f32_16x16x32_bf16 v[164:167], a[220:223], a[240:243], v[164:167]\n" \
"ds_read_b128 a[156:159], v28 offset:6144\n" \
"ds_read_b128 a[220:223], v29 offset:6144\n" \
"s_waitcnt lgkmcnt(15)\n" \
"v_mfma_f32_16x16x32_bf16 a[0:3], a[128:131], a[160:163], a[0:3]\n" \
"s_addk_i32 s84, 0x7c00\n" \
"v_mfma_f32_16x16x32_bf16 a[0:3], a[192:195], a[224:227], a[0:3]\n" \
"buffer_load_dwordx4 v27, s[72:75], s84 offen offset:2048 lds\n" \
"v_mfma_f32_16x16x32_bf16 a[4:7], a[128:131], a[164:167], a[4:7]\n" \
"ds_read_b64_tr_b16 a[176:177], v36 offset:0\n" \
"s_waitcnt lgkmcnt(13)\n" \
"v_mfma_f32_16x16x32_bf16 a[4:7], a[192:195], a[228:231], a[4:7]\n" \
"s_addk_i32 s84, 0x7c00\n" \
"s_waitcnt lgkmcnt(11)\n" \
"v_mfma_f32_16x16x32_bf16 a[8:11], a[128:131], a[168:171], a[8:11]\n" \
"buffer_load_dwordx4 v27, s[72:75], s84 offen offset:3072 lds\n" \
"s_waitcnt lgkmcnt(9)\n" \
"v_mfma_f32_16x16x32_bf16 a[8:11], a[192:195], a[232:235], a[8:11]\n" \
"ds_read_b64_tr_b16 a[178:179], v36 offset:1024\n" \
"s_waitcnt lgkmcnt(6)\n" \
"v_mfma_f32_16x16x32_bf16 a[12:15], a[128:131], a[172:175], a[12:15]\n" \
"ds_read_b64_tr_b16 a[240:241], v36 offset:8192\n" \
"s_waitcnt lgkmcnt(5)\n" \
"v_mfma_f32_16x16x32_bf16 a[12:15], a[192:195], a[236:239], a[12:15]\n" \
"v_mfma_f32_16x16x32_bf16 a[28:31], a[132:135], a[172:175], a[28:31]\n" \
"v_mfma_f32_16x16x32_bf16 a[28:31], a[196:199], a[236:239], a[28:31]\n" \
"ds_read_b64_tr_b16 a[242:243], v36 offset:9216\n" \
"v_mfma_f32_16x16x32_bf16 a[24:27], a[132:135], a[168:171], a[24:27]\n" \
"v_mfma_f32_16x16x32_bf16 a[24:27], a[196:199], a[232:235], a[24:27]\n" \
"v_mfma_f32_16x16x32_bf16 a[20:23], a[132:135], a[164:167], a[20:23]\n" \
"v_mfma_f32_16x16x32_bf16 a[20:23], a[196:199], a[228:231], a[20:23]\n" \
"ds_read_b64_tr_b16 a[180:181], v37 offset:0\n" \
"v_mfma_f32_16x16x32_bf16 a[16:19], a[132:135], a[160:163], a[16:19]\n" \
"v_mfma_f32_16x16x32_bf16 a[16:19], a[196:199], a[224:227], a[16:19]\n" \
"v_mfma_f32_16x16x32_bf16 a[32:35], a[136:139], a[160:163], a[32:35]\n" \
"ds_read_b64_tr_b16 a[182:183], v37 offset:1024\n" \
"v_mfma_f32_16x16x32_bf16 a[32:35], a[200:203], a[224:227], a[32:35]\n" \
"v_mfma_f32_16x16x32_bf16 a[36:39], a[136:139], a[164:167], a[36:39]\n" \
"v_mfma_f32_16x16x32_bf16 a[36:39], a[200:203], a[228:231], a[36:39]\n" \
"buffer_store_dwordx4 v[200:203], v20, s[80:83], s35 offen sc1\n" \
"v_mfma_f32_16x16x32_bf16 a[40:43], a[136:139], a[168:171], a[40:43]\n" \
"s_mov_b32 m0, s32\n" \
".fill (L_pb + 52960 - .) / 4, 4, 0xBF800000\n" \
"L_pp13:\n" \
"s_cbranch_execz L_pret\n" \
"ds_read_b64_tr_b16 a[244:245], v37 offset:8192\n" \
"v_mfma_f32_16x16x32_bf16 a[40:43], a[200:203], a[232:235], a[40:43]\n" \
"s_mov_b32 exec_lo, 0x0\n" \
"s_mov_b32 exec_hi, 0xffffffff\n" \
"global_load_lds_dword v[188:189], off\n" \
"s_mov_b64 exec, -1\n" \
"v_mfma_f32_16x16x32_bf16 a[44:47], a[136:139], a[172:175], a[44:47]\n" \
"ds_read_b64_tr_b16 a[246:247], v37 offset:9216\n" \
"v_mfma_f32_16x16x32_bf16 a[44:47], a[200:203], a[236:239], a[44:47]\n" \
"v_mfma_f32_16x16x32_bf16 a[60:63], a[140:143], a[172:175], a[60:63]\n" \
"v_mfma_f32_16x16x32_bf16 a[60:63], a[204:207], a[236:239], a[60:63]\n" \
"v_mfma_f32_16x16x32_bf16 a[56:59], a[140:143], a[168:171], a[56:59]\n" \
"ds_read_b64_tr_b16 a[184:185], v38 offset:0\n" \
"v_mfma_f32_16x16x32_bf16 a[56:59], a[204:207], a[232:235], a[56:59]\n" \
"v_mfma_f32_16x16x32_bf16 a[52:55], a[140:143], a[164:167], a[52:55]\n" \
"buffer_store_dwordx4 v[204:207], v20, s[80:83], s88 offen sc1\n" \
"v_mfma_f32_16x16x32_bf16 a[52:55], a[204:207], a[228:231], a[52:55]\n" \
"ds_read_b64_tr_b16 a[186:187], v38 offset:1024\n" \
"v_mfma_f32_16x16x32_bf16 a[48:51], a[140:143], a[160:163], a[48:51]\n" \
"v_mfma_f32_16x16x32_bf16 a[48:51], a[204:207], a[224:227], a[48:51]\n" \
"v_mfma_f32_16x16x32_bf16 a[64:67], a[144:147], a[160:163], a[64:67]\n" \
"ds_read_b64_tr_b16 a[248:249], v38 offset:8192\n" \
"v_mfma_f32_16x16x32_bf16 a[64:67], a[208:211], a[224:227], a[64:67]\n" \
"v_mfma_f32_16x16x32_bf16 a[68:71], a[144:147], a[164:167], a[68:71]\n" \
"v_mfma_f32_16x16x32_bf16 a[68:71], a[208:211], a[228:231], a[68:71]\n" \
"ds_read_b64_tr_b16 a[250:251], v38 offset:9216\n" \
"v_mfma_f32_16x16x32_bf16 a[72:75], a[144:147], a[168:171], a[72:75]\n" \
"v_mfma_f32_16x16x32_bf16 a[72:75], a[208:211], a[232:235], a[72:75]\n" \
"v_mfma_f32_16x16x32_bf16 a[76:79], a[144:147], a[172:175], a[76:79]\n" \
"buffer_store_dwordx4 v[208:211], v20, s[80:83], s36 offen sc1\n" \
"v_mfma_f32_16x16x32_bf16 a[76:79], a[208:211], a[236:239], a[76:79]\n" \
"ds_read_b64_tr_b16 a[188:189], v39 offset:0\n" \
"v_mfma_f32_16x16x32_bf16 a[92:95], a[148:151], a[172:175], a[92:95]\n" \
"v_mfma_f32_16x16x32_bf16 a[92:95], a[212:215], a[236:239], a[92:95]\n" \
"v_mfma_f32_16x16x32_bf16 a[88:91], a[148:151], a[168:171], a[88:91]\n" \
"ds_read_b64_tr_b16 a[190:191], v39 offset:1024\n" \
"v_mfma_f32_16x16x32_bf16 a[88:91], a[212:215], a[232:235], a[88:91]\n" \
"v_mfma_f32_16x16x32_bf16 a[84:87], a[148:151], a[164:167], a[84:87]\n" \
"v_mfma_f32_16x16x32_bf16 a[84:87], a[212:215], a[228:231], a[84:87]\n" \
"ds_read_b64_tr_b16 a[252:253], v39 offset:8192\n" \
"v_mfma_f32_16x16x32_bf16 a[80:83], a[148:151], a[160:163], a[80:83]\n" \
"v_mfma_f32_16x16x32_bf16 a[80:83], a[212:215], a[224:227], a[80:83]\n" \
"v_mfma_f32_16x16x32_bf16 a[96:99], a[152:155], a[160:163], a[96:99]\n" \
"ds_read_b64_tr_b16 a[254:255], v39 offset:9216\n" \
"v_mfma_f32_16x16x32_bf16 a[96:99], a[216:219], a[224:227], a[96:99]\n" \
"v_mfma_f32_16x16x32_bf16 a[100:103], a[152:155], a[164:167], a[100:103]\n" \
"v_mfma_f32_16x16x32_bf16 a[100:103], a[216:219], a[228:231], a[100:103]\n" \
"v_mfma_f32_16x16x32_bf16 a[104:107], a[152:155], a[168:171], a[104:107]\n" \
"v_mfma_f32_16x16x32_bf16 a[104:107], a[216:219], a[232:235], a[104:107]\n" \
"v_mfma_f32_16x16x32_bf16 a[108:111], a[152:155], a[172:175], a[108:111]\n" \
"v_mfma_f32_16x16x32_bf16 a[108:111], a[216:219], a[236:239], a[108:111]\n" \
"s_waitcnt lgkmcnt(15)\n" \
"v_mfma_f32_16x16x32_bf16 a[124:127], a[156:159], a[172:175], a[124:127]\n" \
"v_mfma_f32_16x16x32_bf16 a[124:127], a[220:223], a[236:239], a[124:127]\n" \
"v_mfma_f32_16x16x32_bf16 a[120:123], a[156:159], a[168:171], a[120:123]\n" \
"v_mfma_f32_16x16x32_bf16 a[120:123], a[220:223], a[232:235], a[120:123]\n" \
"v_mfma_f32_16x16x32_bf16 a[116:119], a[156:159], a[164:167], a[116:119]\n" \
"v_mfma_f32_16x16x32_bf16 a[116:119], a[220:223], a[228:231], a[116:119]\n" \
"v_mfma_f32_16x16x32_bf16 a[112:115], a[156:159], a[160:163], a[112:115]\n" \
"v_mfma_f32_16x16x32_bf16 a[112:115], a[220:223], a[224:227], a[112:115]\n" \
"s_waitcnt vmcnt(4) lgkmcnt(0)\n" \
"s_barrier\n" \
"v_mfma_f32_16x16x32_bf16 v[52:55], a[128:131], a[176:179], v[52:55]\n" \
"s_add_u32 m0, s30, 0x0\n" \
"s_movk_i32 s84, 0x300\n" \
"v_mfma_f32_16x16x32_bf16 v[52:55], a[192:195], a[240:243], v[52:55]\n" \
"buffer_load_dwordx4 v22, s[64:67], s84 offen offset:0 lds\n" \
"v_mfma_f32_16x16x32_bf16 v[56:59], a[128:131], a[180:183], v[56:59]\n" \
"ds_read_b64_tr_b16 a[160:161], v32 offset:32768\n" \
"v_mfma_f32_16x16x32_bf16 v[56:59], a[192:195], a[244:247], v[56:59]\n" \
"s_movk_i32 s84, 0x1f00\n" \
"v_mfma_f32_16x16x32_bf16 v[60:63], a[128:131], a[184:187], v[60:63]\n" \
"buffer_load_dwordx4 v23, s[64:67], s84 offen offset:1024 lds\n" \
"v_mfma_f32_16x16x32_bf16 v[60:63], a[192:195], a[248:251], v[60:63]\n" \
"ds_read_b64_tr_b16 a[162:163], v32 offset:33792\n" \
"v_mfma_f32_16x16x32_bf16 v[64:67], a[128:131], a[188:191], v[64:67]\n" \
"ds_read_b64_tr_b16 a[224:225], v32 offset:40960\n" \
"v_mfma_f32_16x16x32_bf16 v[64:67], a[192:195], a[252:255], v[64:67]\n" \
"ds_read_b128 a[128:131], v28 offset:49152\n" \
"v_mfma_f32_16x16x32_bf16 v[80:83], a[132:135], a[188:191], v[80:83]\n" \
"ds_read_b128 a[192:195], v29 offset:49152\n" \
"v_mfma_f32_16x16x32_bf16 v[80:83], a[196:199], a[252:255], v[80:83]\n" \
"s_movk_i32 s84, 0x3b00\n" \
"v_mfma_f32_16x16x32_bf16 v[76:79], a[132:135], a[184:187], v[76:79]\n" \
"buffer_load_dwordx4 v22, s[64:67], s84 offen offset:2048 lds\n" \
"v_mfma_f32_16x16x32_bf16 v[76:79], a[196:199], a[248:251], v[76:79]\n" \
"s_movk_i32 s84, 0x5700\n" \
"v_mfma_f32_16x16x32_bf16 v[72:75], a[132:135], a[180:183], v[72:75]\n" \
"buffer_load_dwordx4 v23, s[64:67], s84 offen offset:3072 lds\n" \
"v_mfma_f32_16x16x32_bf16 v[72:75], a[196:199], a[244:247], v[72:75]\n" \
"s_add_u32 m0, s31, 0x0\n" \
"s_mov_b32 s84, 0x300000\n" \
"v_mfma_f32_16x16x32_bf16 v[68:71], a[132:135], a[176:179], v[68:71]\n" \
"buffer_load_dwordx4 v26, s[68:71], s84 offen offset:0 lds\n" \
"v_mfma_f32_16x16x32_bf16 v[68:71], a[196:199], a[240:243], v[68:71]\n" \
"ds_read_b128 a[132:135], v28 offset:51200\n" \
"v_mfma_f32_16x16x32_bf16 v[84:87], a[136:139], a[176:179], v[84:87]\n" \
"ds_read_b128 a[196:199], v29 offset:51200\n" \
"v_mfma_f32_16x16x32_bf16 v[84:87], a[200:203], a[240:243], v[84:87]\n" \
"s_addk_i32 s84, 0x7c00\n" \
"v_mfma_f32_16x16x32_bf16 v[88:91], a[136:139], a[180:183], v[88:91]\n" \
"buffer_load_dwordx4 v26, s[68:71], s84 offen offset:1024 lds\n" \
"v_mfma_f32_16x16x32_bf16 v[88:91], a[200:203], a[244:247], v[88:91]\n" \
"ds_read_b64_tr_b16 a[226:227], v32 offset:41984\n" \
"v_mfma_f32_16x16x32_bf16 v[92:95], a[136:139], a[184:187], v[92:95]\n" \
"s_addk_i32 s84, 0x7c00\n" \
"v_mfma_f32_16x16x32_bf16 v[92:95], a[200:203], a[248:251], v[92:95]\n" \
"buffer_load_dwordx4 v27, s[68:71], s84 offen offset:2048 lds\n" \
"v_mfma_f32_16x16x32_bf16 v[96:99], a[136:139], a[188:191], v[96:99]\n" \
"ds_read_b64_tr_b16 a[164:165], v33 offset:32768\n" \
"v_mfma_f32_16x16x32_bf16 v[96:99], a[200:203], a[252:255], v[96:99]\n" \
"ds_read_b128 a[136:139], v28 offset:53248\n" \
"v_mfma_f32_16x16x32_bf16 v[112:115], a[140:143], a[188:191], v[112:115]\n" \
"ds_read_b128 a[200:203], v29 offset:53248\n" \
"v_mfma_f32_16x16x32_bf16 v[112:115], a[204:207], a[252:255], v[112:115]\n" \
"s_addk_i32 s84, 0x7c00\n" \
"v_mfma_f32_16x16x32_bf16 v[108:111], a[140:143], a[184:187], v[108:111]\n" \
"buffer_load_dwordx4 v27, s[68:71], s84 offen offset:3072 lds\n" \
"v_mfma_f32_16x16x32_bf16 v[108:111], a[204:207], a[248:251], v[108:111]\n" \
"s_add_u32 m0, s30, 0x1000\n" \
"s_mov_b32 s84, 0x8300\n" \
"v_mfma_f32_16x16x32_bf16 v[104:107], a[140:143], a[180:183], v[104:107]\n" \
"buffer_load_dwordx4 v22, s[64:67], s84 offen offset:0 lds\n" \
"v_mfma_f32_16x16x32_bf16 v[104:107], a[204:207], a[244:247], v[104:107]\n" \
"ds_read_b64_tr_b16 a[166:167], v33 offset:33792\n" \
"v_mfma_f32_16x16x32_bf16 v[100:103], a[140:143], a[176:179], v[100:103]\n" \
"ds_read_b64_tr_b16 a[228:229], v33 offset:40960\n" \
"v_mfma_f32_16x16x32_bf16 v[100:103], a[204:207], a[240:243], v[100:103]\n" \
"ds_read_b128 a[140:143], v28 offset:55296\n" \
"v_mfma_f32_16x16x32_bf16 v[116:119], a[144:147], a[176:179], v[116:119]\n" \
"ds_read_b128 a[204:207], v29 offset:55296\n" \
"v_mfma_f32_16x16x32_bf16 v[116:119], a[208:211], a[240:243], v[116:119]\n" \
"s_addk_i32 s84, 0x1c00\n" \
"v_mfma_f32_16x16x32_bf16 v[120:123], a[144:147], a[180:183], v[120:123]\n" \
"buffer_load_dwordx4 v23, s[64:67], s84 offen offset:1024 lds\n" \
"v_mfma_f32_16x16x32_bf16 v[120:123], a[208:211], a[244:247], v[120:123]\n" \
"s_addk_i32 s84, 0x1c00\n" \
"v_mfma_f32_16x16x32_bf16 v[124:127], a[144:147], a[184:187], v[124:127]\n" \
"buffer_load_dwordx4 v22, s[64:67], s84 offen offset:2048 lds\n" \
"v_mfma_f32_16x16x32_bf16 v[124:127], a[208:211], a[248:251], v[124:127]\n" \
"s_addk_i32 s84, 0x1c00\n" \
"s_nop 0\n" \
"v_mfma_f32_16x16x32_bf16 v[128:131], a[144:147], a[188:191], v[128:131]\n" \
"buffer_load_dwordx4 v23, s[64:67], s84 offen offset:3072 lds\n" \
"v_mfma_f32_16x16x32_bf16 v[128:131], a[208:211], a[252:255], v[128:131]\n" \
"ds_read_b128 a[144:147], v28 offset:32768\n" \
"v_mfma_f32_16x16x32_bf16 v[144:147], a[148:151], a[188:191], v[144:147]\n" \
"ds_read_b128 a[208:211], v29 offset:32768\n" \
"v_mfma_f32_16x16x32_bf16 v[144:147], a[212:215], a[252:255], v[144:147]\n" \
"s_add_u32 m0, s31, 0x4000\n" \
"s_mov_b32 s84, 0x300000\n" \
"v_mfma_f32_16x16x32_bf16 v[140:143], a[148:151], a[184:187], v[140:143]\n" \
"buffer_load_dwordx4 v26, s[72:75], s84 offen offset:0 lds\n" \
"v_mfma_f32_16x16x32_bf16 v[140:143], a[212:215], a[248:251], v[140:143]\n" \
"ds_read_b64_tr_b16 a[230:231], v33 offset:41984\n" \
"v_mfma_f32_16x16x32_bf16 v[136:139], a[148:151], a[180:183], v[136:139]\n" \
"s_addk_i32 s84, 0x7c00\n" \
"s_nop 0\n" \
"v_mfma_f32_16x16x32_bf16 v[136:139], a[212:215], a[244:247], v[136:139]\n" \
"buffer_load_dwordx4 v26, s[72:75], s84 offen offset:1024 lds\n" \
"v_mfma_f32_16x16x32_bf16 v[132:135], a[148:151], a[176:179], v[132:135]\n" \
"ds_read_b64_tr_b16 a[168:169], v34 offset:32768\n" \
"v_mfma_f32_16x16x32_bf16 v[132:135], a[212:215], a[240:243], v[132:135]\n" \
"ds_read_b128 a[148:151], v28 offset:34816\n" \
"v_mfma_f32_16x16x32_bf16 v[148:151], a[152:155], a[176:179], v[148:151]\n" \
"ds_read_b128 a[212:215], v29 offset:34816\n" \
"v_mfma_f32_16x16x32_bf16 v[148:151], a[216:219], a[240:243], v[148:151]\n" \
"ds_read_b64_tr_b16 a[170:171], v34 offset:33792\n" \
"v_mfma_f32_16x16x32_bf16 v[152:155], a[152:155], a[180:183], v[152:155]\n" \
"ds_read_b64_tr_b16 a[232:233], v34 offset:40960\n" \
"v_mfma_f32_16x16x32_bf16 v[152:155], a[216:219], a[244:247], v[152:155]\n" \
"ds_read_b64_tr_b16 a[234:235], v34 offset:41984\n" \
"v_mfma_f32_16x16x32_bf16 v[156:159], a[152:155], a[184:187], v[156:159]\n" \
"ds_read_b64_tr_b16 a[172:173], v35 offset:32768\n" \
"v_mfma_f32_16x16x32_bf16 v[156:159], a[216:219], a[248:251], v[156:159]\n" \
"ds_read_b64_tr_b16 a[174:175], v35 offset:33792\n" \
"v_mfma_f32_16x16x32_bf16 v[160:163], a[152:155], a[188:191], v[160:163]\n" \
"ds_read_b64_tr_b16 a[236:237], v35 offset:40960\n" \
"v_mfma_f32_16x16x32_bf16 v[160:163], a[216:219], a[252:255], v[160:163]\n" \
"ds_read_b128 a[152:155], v28 offset:36864\n" \
"v_mfma_f32_16x16x32_bf16 v[176:179], a[156:159], a[188:191], v[176:179]\n" \
"ds_read_b128 a[216:219], v29 offset:36864\n" \
"v_mfma_f32_16x16x32_bf16 v[176:179], a[220:223], a[252:255], v[176:179]\n" \
"ds_read_b64_tr_b16 a[238:239], v35 offset:41984\n" \
"v_mfma_f32_16x16x32_bf16 v[172:175], a[156:159], a[184:187], v[172:175]\n" \
"v_mfma_f32_16x16x32_bf16 v[172:175], a[220:223], a[248:251], v[172:175]\n" \
"v_mfma_f32_16x16x32_bf16 v[168:171], a[156:159], a[180:183], v[168:171]\n" \
"v_mfma_f32_16x16x32_bf16 v[168:171], a[220:223], a[244:247], v[168:171]\n" \
"v_mfma_f32_16x16x32_bf16 v[164:167], a[156:159], a[176:179], v[164:167]\n" \
"v_mfma_f32_16x16x32_bf16 v[164:167], a[220:223], a[240:243], v[164:167]\n" \
"ds_read_b128 a[156:159], v28 offset:38912\n" \
"ds_read_b128 a[220:223], v29 offset:38912\n" \
"s_waitcnt lgkmcnt(15)\n" \
"v_mfma_f32_16x16x32_bf16 a[0:3], a[128:131], a[160:163], a[0:3]\n" \
"s_addk_i32 s84, 0x7c00\n" \
"v_mfma_f32_16x16x32_bf16 a[0:3], a[192:195], a[224:227], a[0:3]\n" \
"buffer_load_dwordx4 v27, s[72:75], s84 offen offset:2048 lds\n" \
"v_mfma_f32_16x16x32_bf16 a[4:7], a[128:131], a[164:167], a[4:7]\n" \
"ds_read_b64_tr_b16 a[176:177], v36 offset:32768\n" \
"s_waitcnt lgkmcnt(15)\n" \
"v_mfma_f32_16x16x32_bf16 a[4:7], a[192:195], a[228:231], a[4:7]\n" \
"s_addk_i32 s84, 0x7c00\n" \
"s_waitcnt lgkmcnt(11)\n" \
"v_mfma_f32_16x16x32_bf16 a[8:11], a[128:131], a[168:171], a[8:11]\n" \
"buffer_load_dwordx4 v27, s[72:75], s84 offen offset:3072 lds\n" \
"s_waitcnt lgkmcnt(9)\n" \
"v_mfma_f32_16x16x32_bf16 a[8:11], a[192:195], a[232:235], a[8:11]\n" \
"ds_read_b64_tr_b16 a[178:179], v36 offset:33792\n" \
"s_waitcnt lgkmcnt(8)\n" \
"v_mfma_f32_16x16x32_bf16 a[12:15], a[128:131], a[172:175], a[12:15]\n" \
"ds_read_b64_tr_b16 a[240:241], v36 offset:40960\n" \
"s_waitcnt lgkmcnt(5)\n" \
"v_mfma_f32_16x16x32_bf16 a[12:15], a[192:195], a[236:239], a[12:15]\n" \
"v_mfma_f32_16x16x32_bf16 a[28:31], a[132:135], a[172:175], a[28:31]\n" \
"v_mfma_f32_16x16x32_bf16 a[28:31], a[196:199], a[236:239], a[28:31]\n" \
"ds_read_b64_tr_b16 a[242:243], v36 offset:41984\n" \
"v_mfma_f32_16x16x32_bf16 a[24:27], a[132:135], a[168:171], a[24:27]\n" \
"v_mfma_f32_16x16x32_bf16 a[24:27], a[196:199], a[232:235], a[24:27]\n" \
"v_mfma_f32_16x16x32_bf16 a[20:23], a[132:135], a[164:167], a[20:23]\n" \
"v_mfma_f32_16x16x32_bf16 a[20:23], a[196:199], a[228:231], a[20:23]\n" \
"ds_read_b64_tr_b16 a[180:181], v37 offset:32768\n" \
"v_mfma_f32_16x16x32_bf16 a[16:19], a[132:135], a[160:163], a[16:19]\n" \
"v_mfma_f32_16x16x32_bf16 a[16:19], a[196:199], a[224:227], a[16:19]\n" \
"v_mfma_f32_16x16x32_bf16 a[32:35], a[136:139], a[160:163], a[32:35]\n" \
"ds_read_b64_tr_b16 a[182:183], v37 offset:33792\n" \
"v_mfma_f32_16x16x32_bf16 a[32:35], a[200:203], a[224:227], a[32:35]\n" \
"v_mfma_f32_16x16x32_bf16 a[36:39], a[136:139], a[164:167], a[36:39]\n" \
"v_lshlrev_b32_e32 v45, 3, v20\n" \
"v_add_u32_e32 v45, s31, v45\n" \
"v_mfma_f32_16x16x32_bf16 a[36:39], a[200:203], a[228:231], a[36:39]\n" \
"s_getpc_b64 s[86:87]\n" \
"s_sub_u32 s86, s86, 0x10800\n" \
"s_subb_u32 s87, s87, 0\n" \
"s_mov_b32 m0, s32\n" \
"s_nop 0\n" \
"v_mfma_f32_16x16x32_bf16 a[40:43], a[136:139], a[168:171], a[40:43]\n" \
"global_load_lds_dword v45, s[86:87]\n" \
"v_mfma_f32_16x16x32_bf16 a[40:43], a[200:203], a[232:235], a[40:43]\n" \
"ds_read_b64_tr_b16 a[244:245], v37 offset:40960\n" \
"v_mfma_f32_16x16x32_bf16 a[44:47], a[136:139], a[172:175], a[44:47]\n" \
"ds_read_b64_tr_b16 a[246:247], v37 offset:41984\n" \
"v_mfma_f32_16x16x32_bf16 a[44:47], a[200:203], a[236:239], a[44:47]\n" \
"v_mfma_f32_16x16x32_bf16 a[60:63], a[140:143], a[172:175], a[60:63]\n" \
"v_mfma_f32_16x16x32_bf16 a[60:63], a[204:207], a[236:239], a[60:63]\n" \
"v_mfma_f32_16x16x32_bf16 a[56:59], a[140:143], a[168:171], a[56:59]\n" \
"ds_read_b64_tr_b16 a[184:185], v38 offset:32768\n" \
"v_mfma_f32_16x16x32_bf16 a[56:59], a[204:207], a[232:235], a[56:59]\n" \
"v_mfma_f32_16x16x32_bf16 a[52:55], a[140:143], a[164:167], a[52:55]\n" \
"v_mfma_f32_16x16x32_bf16 a[52:55], a[204:207], a[228:231], a[52:55]\n" \
"ds_read_b64_tr_b16 a[186:187], v38 offset:33792\n" \
"v_mfma_f32_16x16x32_bf16 a[48:51], a[140:143], a[160:163], a[48:51]\n" \
"v_mfma_f32_16x16x32_bf16 a[48:51], a[204:207], a[224:227], a[48:51]\n" \
"v_mfma_f32_16x16x32_bf16 a[64:67], a[144:147], a[160:163], a[64:67]\n" \
"ds_read_b64_tr_b16 a[248:249], v38 offset:40960\n" \
"v_mfma_f32_16x16x32_bf16 a[64:67], a[208:211], a[224:227], a[64:67]\n" \
"v_mfma_f32_16x16x32_bf16 a[68:71], a[144:147], a[164:167], a[68:71]\n" \
"v_mfma_f32_16x16x32_bf16 a[68:71], a[208:211], a[228:231], a[68:71]\n" \
"ds_read_b64_tr_b16 a[250:251], v38 offset:41984\n" \
"v_mfma_f32_16x16x32_bf16 a[72:75], a[144:147], a[168:171], a[72:75]\n" \
"v_mfma_f32_16x16x32_bf16 a[72:75], a[208:211], a[232:235], a[72:75]\n" \
"v_mfma_f32_16x16x32_bf16 a[76:79], a[144:147], a[172:175], a[76:79]\n" \
"v_mfma_f32_16x16x32_bf16 a[76:79], a[208:211], a[236:239], a[76:79]\n" \
"ds_read_b64_tr_b16 a[188:189], v39 offset:32768\n" \
"v_mfma_f32_16x16x32_bf16 a[92:95], a[148:151], a[172:175], a[92:95]\n" \
"v_mfma_f32_16x16x32_bf16 a[92:95], a[212:215], a[236:239], a[92:95]\n" \
"v_mfma_f32_16x16x32_bf16 a[88:91], a[148:151], a[168:171], a[88:91]\n" \
"ds_read_b64_tr_b16 a[190:191], v39 offset:33792\n" \
"v_mfma_f32_16x16x32_bf16 a[88:91], a[212:215], a[232:235], a[88:91]\n" \
"v_mfma_f32_16x16x32_bf16 a[84:87], a[148:151], a[164:167], a[84:87]\n" \
"v_mfma_f32_16x16x32_bf16 a[84:87], a[212:215], a[228:231], a[84:87]\n" \
"ds_read_b64_tr_b16 a[252:253], v39 offset:40960\n" \
"v_mfma_f32_16x16x32_bf16 a[80:83], a[148:151], a[160:163], a[80:83]\n" \
"v_mfma_f32_16x16x32_bf16 a[80:83], a[212:215], a[224:227], a[80:83]\n" \
"v_mfma_f32_16x16x32_bf16 a[96:99], a[152:155], a[160:163], a[96:99]\n" \
"ds_read_b64_tr_b16 a[254:255], v39 offset:41984\n" \
"v_mfma_f32_16x16x32_bf16 a[96:99], a[216:219], a[224:227], a[96:99]\n" \
"v_mfma_f32_16x16x32_bf16 a[100:103], a[152:155], a[164:167], a[100:103]\n" \
"v_mfma_f32_16x16x32_bf16 a[100:103], a[216:219], a[228:231], a[100:103]\n" \
"v_mfma_f32_16x16x32_bf16 a[104:107], a[152:155], a[168:171], a[104:107]\n" \
"v_mfma_f32_16x16x32_bf16 a[104:107], a[216:219], a[232:235], a[104:107]\n" \
"v_mfma_f32_16x16x32_bf16 a[108:111], a[152:155], a[172:175], a[108:111]\n" \
"v_mfma_f32_16x16x32_bf16 a[108:111], a[216:219], a[236:239], a[108:111]\n" \
"s_waitcnt lgkmcnt(15)\n" \
"v_mfma_f32_16x16x32_bf16 a[124:127], a[156:159], a[172:175], a[124:127]\n" \
"v_mfma_f32_16x16x32_bf16 a[124:127], a[220:223], a[236:239], a[124:127]\n" \
"v_mfma_f32_16x16x32_bf16 a[120:123], a[156:159], a[168:171], a[120:123]\n" \
"v_mfma_f32_16x16x32_bf16 a[120:123], a[220:223], a[232:235], a[120:123]\n" \
"v_mfma_f32_16x16x32_bf16 a[116:119], a[156:159], a[164:167], a[116:119]\n" \
"v_mfma_f32_16x16x32_bf16 a[116:119], a[220:223], a[228:231], a[116:119]\n" \
"v_mfma_f32_16x16x32_bf16 a[112:115], a[156:159], a[160:163], a[112:115]\n" \
"v_mfma_f32_16x16x32_bf16 a[112:115], a[220:223], a[224:227], a[112:115]\n" \
"s_waitcnt vmcnt(1) lgkmcnt(0)\n" \
"s_barrier\n" \
"v_mov_b32_e32 v45, s62\n" \
"s_mov_b64 exec, 1\n" \
"buffer_store_dword v45, off, s[80:83], s37 offset:128 sc1\n" \
"s_mov_b64 exec, -1\n" \
"v_mfma_f32_16x16x32_bf16 v[52:55], a[128:131], a[176:179], v[52:55]\n" \
"s_add_u32 m0, s30, 0x8000\n" \
"s_movk_i32 s84, 0x380\n" \
"v_mfma_f32_16x16x32_bf16 v[52:55], a[192:195], a[240:243], v[52:55]\n" \
"buffer_load_dwordx4 v22, s[64:67], s84 offen offset:0 lds\n" \
"v_mfma_f32_16x16x32_bf16 v[56:59], a[128:131], a[180:183], v[56:59]\n" \
"ds_read_b64_tr_b16 a[160:161], v32 offset:0\n" \
"v_mfma_f32_16x16x32_bf16 v[56:59], a[192:195], a[244:247], v[56:59]\n" \
"s_movk_i32 s84, 0x1f80\n" \
"v_mfma_f32_16x16x32_bf16 v[60:63], a[128:131], a[184:187], v[60:63]\n" \
"buffer_load_dwordx4 v23, s[64:67], s84 offen offset:1024 lds\n" \
"v_mfma_f32_16x16x32_bf16 v[60:63], a[192:195], a[248:251], v[60:63]\n" \
"ds_read_b64_tr_b16 a[162:163], v32 offset:1024\n" \
"v_mfma_f32_16x16x32_bf16 v[64:67], a[128:131], a[188:191], v[64:67]\n" \
"ds_read_b64_tr_b16 a[224:225], v32 offset:8192\n" \
"v_mfma_f32_16x16x32_bf16 v[64:67], a[192:195], a[252:255], v[64:67]\n" \
"ds_read_b128 a[128:131], v28 offset:16384\n" \
"v_mfma_f32_16x16x32_bf16 v[80:83], a[132:135], a[188:191], v[80:83]\n" \
"ds_read_b128 a[192:195], v29 offset:16384\n" \
"v_mfma_f32_16x16x32_bf16 v[80:83], a[196:199], a[252:255], v[80:83]\n" \
"s_movk_i32 s84, 0x3b80\n" \
"v_mfma_f32_16x16x32_bf16 v[76:79], a[132:135], a[184:187], v[76:79]\n" \
"buffer_load_dwordx4 v22, s[64:67], s84 offen offset:2048 lds\n" \
"v_mfma_f32_16x16x32_bf16 v[76:79], a[196:199], a[248:251], v[76:79]\n" \
"s_movk_i32 s84, 0x5780\n" \
"v_mfma_f32_16x16x32_bf16 v[72:75], a[132:135], a[180:183], v[72:75]\n" \
"buffer_load_dwordx4 v23, s[64:67], s84 offen offset:3072 lds\n" \
"v_mfma_f32_16x16x32_bf16 v[72:75], a[196:199], a[244:247], v[72:75]\n" \
"s_add_u32 m0, s31, 0x8000\n" \
"s_mov_b32 s84, 0x380000\n" \
"v_mfma_f32_16x16x32_bf16 v[68:71], a[132:135], a[176:179], v[68:71]\n" \
"buffer_load_dwordx4 v26, s[68:71], s84 offen offset:0 lds\n" \
"v_mfma_f32_16x16x32_bf16 v[68:71], a[196:199], a[240:243], v[68:71]\n" \
"ds_read_b128 a[132:135], v28 offset:18432\n" \
"v_mfma_f32_16x16x32_bf16 v[84:87], a[136:139], a[176:179], v[84:87]\n" \
"ds_read_b128 a[196:199], v29 offset:18432\n" \
"v_mfma_f32_16x16x32_bf16 v[84:87], a[200:203], a[240:243], v[84:87]\n" \
"s_addk_i32 s84, 0x7c00\n" \
"v_mfma_f32_16x16x32_bf16 v[88:91], a[136:139], a[180:183], v[88:91]\n" \
"buffer_load_dwordx4 v26, s[68:71], s84 offen offset:1024 lds\n" \
"v_mfma_f32_16x16x32_bf16 v[88:91], a[200:203], a[244:247], v[88:91]\n" \
"ds_read_b64_tr_b16 a[226:227], v32 offset:9216\n" \
"v_mfma_f32_16x16x32_bf16 v[92:95], a[136:139], a[184:187], v[92:95]\n" \
"s_addk_i32 s84, 0x7c00\n" \
"v_mfma_f32_16x16x32_bf16 v[92:95], a[200:203], a[248:251], v[92:95]\n" \
"buffer_load_dwordx4 v27, s[68:71], s84 offen offset:2048 lds\n" \
"v_mfma_f32_16x16x32_bf16 v[96:99], a[136:139], a[188:191], v[96:99]\n" \
"ds_read_b64_tr_b16 a[164:165], v33 offset:0\n" \
"v_mfma_f32_16x16x32_bf16 v[96:99], a[200:203], a[252:255], v[96:99]\n" \
"ds_read_b128 a[136:139], v28 offset:20480\n" \
"v_mfma_f32_16x16x32_bf16 v[112:115], a[140:143], a[188:191], v[112:115]\n" \
"ds_read_b128 a[200:203], v29 offset:20480\n" \
"v_mfma_f32_16x16x32_bf16 v[112:115], a[204:207], a[252:255], v[112:115]\n" \
"s_addk_i32 s84, 0x7c00\n" \
"s_nop 0\n" \
"v_mfma_f32_16x16x32_bf16 v[108:111], a[140:143], a[184:187], v[108:111]\n" \
"buffer_load_dwordx4 v27, s[68:71], s84 offen offset:3072 lds\n" \
"v_mfma_f32_16x16x32_bf16 v[108:111], a[204:207], a[248:251], v[108:111]\n" \
"s_add_u32 m0, s30, 0x9000\n" \
"s_mov_b32 s84, 0x8380\n" \
"v_mfma_f32_16x16x32_bf16 v[104:107], a[140:143], a[180:183], v[104:107]\n" \
"buffer_load_dwordx4 v22, s[64:67], s84 offen offset:0 lds\n" \
"v_mfma_f32_16x16x32_bf16 v[104:107], a[204:207], a[244:247], v[104:107]\n" \
"ds_read_b64_tr_b16 a[166:167], v33 offset:1024\n" \
"v_mfma_f32_16x16x32_bf16 v[100:103], a[140:143], a[176:179], v[100:103]\n" \
"ds_read_b64_tr_b16 a[228:229], v33 offset:8192\n" \
"v_mfma_f32_16x16x32_bf16 v[100:103], a[204:207], a[240:243], v[100:103]\n" \
"ds_read_b128 a[140:143], v28 offset:22528\n" \
"v_mfma_f32_16x16x32_bf16 v[116:119], a[144:147], a[176:179], v[116:119]\n" \
"ds_read_b128 a[204:207], v29 offset:22528\n" \
"v_mfma_f32_16x16x32_bf16 v[116:119], a[208:211], a[240:243], v[116:119]\n" \
"s_addk_i32 s84, 0x1c00\n" \
"v_mfma_f32_16x16x32_bf16 v[120:123], a[144:147], a[180:183], v[120:123]\n" \
"buffer_load_dwordx4 v23, s[64:67], s84 offen offset:1024 lds\n" \
"v_mfma_f32_16x16x32_bf16 v[120:123], a[208:211], a[244:247], v[120:123]\n" \
"s_addk_i32 s84, 0x1c00\n" \
"v_mfma_f32_16x16x32_bf16 v[124:127], a[144:147], a[184:187], v[124:127]\n" \
"buffer_load_dwordx4 v22, s[64:67], s84 offen offset:2048 lds\n" \
"v_mfma_f32_16x16x32_bf16 v[124:127], a[208:211], a[248:251], v[124:127]\n" \
"s_addk_i32 s84, 0x1c00\n" \
"s_nop 0\n" \
"v_mfma_f32_16x16x32_bf16 v[128:131], a[144:147], a[188:191], v[128:131]\n" \
"buffer_load_dwordx4 v23, s[64:67], s84 offen offset:3072 lds\n" \
"v_mfma_f32_16x16x32_bf16 v[128:131], a[208:211], a[252:255], v[128:131]\n" \
"ds_read_b128 a[144:147], v28 offset:0\n" \
"v_mfma_f32_16x16x32_bf16 v[144:147], a[148:151], a[188:191], v[144:147]\n" \
"ds_read_b128 a[208:211], v29 offset:0\n" \
"v_mfma_f32_16x16x32_bf16 v[144:147], a[212:215], a[252:255], v[144:147]\n" \
"s_add_u32 m0, s31, 0xc000\n" \
"s_mov_b32 s84, 0x380000\n" \
"v_mfma_f32_16x16x32_bf16 v[140:143], a[148:151], a[184:187], v[140:143]\n" \
"buffer_load_dwordx4 v26, s[72:75], s84 offen offset:0 lds\n" \
"v_mfma_f32_16x16x32_bf16 v[140:143], a[212:215], a[248:251], v[140:143]\n" \
"ds_read_b64_tr_b16 a[230:231], v33 offset:9216\n" \
"v_mfma_f32_16x16x32_bf16 v[136:139], a[148:151], a[180:183], v[136:139]\n" \
"s_addk_i32 s84, 0x7c00\n" \
"s_nop 0\n" \
"v_mfma_f32_16x16x32_bf16 v[136:139], a[212:215], a[244:247], v[136:139]\n" \
"buffer_load_dwordx4 v26, s[72:75], s84 offen offset:1024 lds\n" \
"v_mfma_f32_16x16x32_bf16 v[132:135], a[148:151], a[176:179], v[132:135]\n" \
"ds_read_b64_tr_b16 a[168:169], v34 offset:0\n" \
"v_mfma_f32_16x16x32_bf16 v[132:135], a[212:215], a[240:243], v[132:135]\n" \
"ds_read_b128 a[148:151], v28 offset:2048\n" \
"v_mfma_f32_16x16x32_bf16 v[148:151], a[152:155], a[176:179], v[148:151]\n" \
"ds_read_b128 a[212:215], v29 offset:2048\n" \
"v_mfma_f32_16x16x32_bf16 v[148:151], a[216:219], a[240:243], v[148:151]\n" \
"ds_read_b64_tr_b16 a[170:171], v34 offset:1024\n" \
"v_mfma_f32_16x16x32_bf16 v[152:155], a[152:155], a[180:183], v[152:155]\n" \
"ds_read_b64_tr_b16 a[232:233], v34 offset:8192\n" \
"v_mfma_f32_16x16x32_bf16 v[152:155], a[216:219], a[244:247], v[152:155]\n" \
"ds_read_b64_tr_b16 a[234:235], v34 offset:9216\n" \
"v_mfma_f32_16x16x32_bf16 v[156:159], a[152:155], a[184:187], v[156:159]\n" \
"ds_read_b64_tr_b16 a[172:173], v35 offset:0\n" \
"v_mfma_f32_16x16x32_bf16 v[156:159], a[216:219], a[248:251], v[156:159]\n" \
"ds_read_b64_tr_b16 a[174:175], v35 offset:1024\n" \
"v_mfma_f32_16x16x32_bf16 v[160:163], a[152:155], a[188:191], v[160:163]\n" \
"ds_read_b64_tr_b16 a[236:237], v35 offset:8192\n" \
"v_mfma_f32_16x16x32_bf16 v[160:163], a[216:219], a[252:255], v[160:163]\n" \
"ds_read_b128 a[152:155], v28 offset:4096\n" \
"v_mfma_f32_16x16x32_bf16 v[176:179], a[156:159], a[188:191], v[176:179]\n" \
"ds_read_b128 a[216:219], v29 offset:4096\n" \
"v_mfma_f32_16x16x32_bf16 v[176:179], a[220:223], a[252:255], v[176:179]\n" \
"ds_read_b64_tr_b16 a[238:239], v35 offset:9216\n" \
"v_mfma_f32_16x16x32_bf16 v[172:175], a[156:159], a[184:187], v[172:175]\n" \
"v_mfma_f32_16x16x32_bf16 v[172:175], a[220:223], a[248:251], v[172:175]\n" \
"v_mfma_f32_16x16x32_bf16 v[168:171], a[156:159], a[180:183], v[168:171]\n" \
"v_mfma_f32_16x16x32_bf16 v[168:171], a[220:223], a[244:247], v[168:171]\n" \
"v_mfma_f32_16x16x32_bf16 v[164:167], a[156:159], a[176:179], v[164:167]\n" \
"v_mfma_f32_16x16x32_bf16 v[164:167], a[220:223], a[240:243], v[164:167]\n" \
"ds_read_b128 a[156:159], v28 offset:6144\n" \
"ds_read_b128 a[220:223], v29 offset:6144\n" \
"s_waitcnt lgkmcnt(15)\n" \
"v_mfma_f32_16x16x32_bf16 a[0:3], a[128:131], a[160:163], a[0:3]\n" \
"s_addk_i32 s84, 0x7c00\n" \
"v_mfma_f32_16x16x32_bf16 a[0:3], a[192:195], a[224:227], a[0:3]\n" \
"buffer_load_dwordx4 v27, s[72:75], s84 offen offset:2048 lds\n" \
"v_mfma_f32_16x16x32_bf16 a[4:7], a[128:131], a[164:167], a[4:7]\n" \
"ds_read_b64_tr_b16 a[176:177], v36 offset:0\n" \
"s_waitcnt lgkmcnt(15)\n" \
"v_mfma_f32_16x16x32_bf16 a[4:7], a[192:195], a[228:231], a[4:7]\n" \
"s_addk_i32 s84, 0x7c00\n" \
"s_waitcnt lgkmcnt(11)\n" \
"v_mfma_f32_16x16x32_bf16 a[8:11], a[128:131], a[168:171], a[8:11]\n" \
"buffer_load_dwordx4 v27, s[72:75], s84 offen offset:3072 lds\n" \
"s_waitcnt lgkmcnt(9)\n" \
"v_mfma_f32_16x16x32_bf16 a[8:11], a[192:195], a[232:235], a[8:11]\n" \
"ds_read_b64_tr_b16 a[178:179], v36 offset:1024\n" \
"s_waitcnt lgkmcnt(8)\n" \
"v_mfma_f32_16x16x32_bf16 a[12:15], a[128:131], a[172:175], a[12:15]\n" \
"ds_read_b64_tr_b16 a[240:241], v36 offset:8192\n" \
"s_waitcnt lgkmcnt(5)\n" \
"v_mfma_f32_16x16x32_bf16 a[12:15], a[192:195], a[236:239], a[12:15]\n" \
"v_mfma_f32_16x16x32_bf16 a[28:31], a[132:135], a[172:175], a[28:31]\n" \
"v_mfma_f32_16x16x32_bf16 a[28:31], a[196:199], a[236:239], a[28:31]\n" \
"ds_read_b64_tr_b16 a[242:243], v36 offset:9216\n" \
"v_mfma_f32_16x16x32_bf16 a[24:27], a[132:135], a[168:171], a[24:27]\n" \
"v_mfma_f32_16x16x32_bf16 a[24:27], a[196:199], a[232:235], a[24:27]\n" \
"v_mfma_f32_16x16x32_bf16 a[20:23], a[132:135], a[164:167], a[20:23]\n" \
"v_mfma_f32_16x16x32_bf16 a[20:23], a[196:199], a[228:231], a[20:23]\n" \
"ds_read_b64_tr_b16 a[180:181], v37 offset:0\n" \
"v_mfma_f32_16x16x32_bf16 a[16:19], a[132:135], a[160:163], a[16:19]\n" \
"v_mfma_f32_16x16x32_bf16 a[16:19], a[196:199], a[224:227], a[16:19]\n" \
"v_mfma_f32_16x16x32_bf16 a[32:35], a[136:139], a[160:163], a[32:35]\n" \
"v_bfe_u32 v44, v20, 2, 5\n" \
"ds_read_b64_tr_b16 a[182:183], v37 offset:1024\n" \
"v_mfma_f32_16x16x32_bf16 a[32:35], a[200:203], a[224:227], a[32:35]\n" \
"buffer_load_dword v44, v44, s[80:83], s38 offen offset:128 sc1\n" \
"v_mfma_f32_16x16x32_bf16 a[36:39], a[136:139], a[164:167], a[36:39]\n" \
"v_mfma_f32_16x16x32_bf16 a[36:39], a[200:203], a[228:231], a[36:39]\n" \
"ds_read_b64_tr_b16 a[244:245], v37 offset:8192\n" \
"v_mfma_f32_16x16x32_bf16 a[40:43], a[136:139], a[168:171], a[40:43]\n" \
"v_mfma_f32_16x16x32_bf16 a[40:43], a[200:203], a[232:235], a[40:43]\n" \
"v_mfma_f32_16x16x32_bf16 a[44:47], a[136:139], a[172:175], a[44:47]\n" \
"ds_read_b64_tr_b16 a[246:247], v37 offset:9216\n" \
"v_mfma_f32_16x16x32_bf16 a[44:47], a[200:203], a[236:239], a[44:47]\n" \
"v_mfma_f32_16x16x32_bf16 a[60:63], a[140:143], a[172:175], a[60:63]\n" \
"global_store_dwordx4 v49, v[0:3], s[28:29] offset:0 sc1\n" \
"v_mfma_f32_16x16x32_bf16 a[60:63], a[204:207], a[236:239], a[60:63]\n" \
"v_mfma_f32_16x16x32_bf16 a[56:59], a[140:143], a[168:171], a[56:59]\n" \
"ds_read_b64_tr_b16 a[184:185], v38 offset:0\n" \
"v_mfma_f32_16x16x32_bf16 a[56:59], a[204:207], a[232:235], a[56:59]\n" \
"v_mfma_f32_16x16x32_bf16 a[52:55], a[140:143], a[164:167], a[52:55]\n" \
"v_mfma_f32_16x16x32_bf16 a[52:55], a[204:207], a[228:231], a[52:55]\n" \
"ds_read_b64_tr_b16 a[186:187], v38 offset:1024\n" \
"v_mfma_f32_16x16x32_bf16 a[48:51], a[140:143], a[160:163], a[48:51]\n" \
"v_mfma_f32_16x16x32_bf16 a[48:51], a[204:207], a[224:227], a[48:51]\n" \
"v_mfma_f32_16x16x32_bf16 a[64:67], a[144:147], a[160:163], a[64:67]\n" \
"global_store_dwordx4 v49, v[0:3], s[28:29] offset:1024 sc1\n" \
"v_mfma_f32_16x16x32_bf16 a[64:67], a[208:211], a[224:227], a[64:67]\n" \
"ds_read_b64_tr_b16 a[248:249], v38 offset:8192\n" \
"v_mfma_f32_16x16x32_bf16 a[68:71], a[144:147], a[164:167], a[68:71]\n" \
"v_mfma_f32_16x16x32_bf16 a[68:71], a[208:211], a[228:231], a[68:71]\n" \
"ds_read_b64_tr_b16 a[250:251], v38 offset:9216\n" \
"v_mfma_f32_16x16x32_bf16 a[72:75], a[144:147], a[168:171], a[72:75]\n" \
"v_mfma_f32_16x16x32_bf16 a[72:75], a[208:211], a[232:235], a[72:75]\n" \
"v_mfma_f32_16x16x32_bf16 a[76:79], a[144:147], a[172:175], a[76:79]\n" \
"v_mfma_f32_16x16x32_bf16 a[76:79], a[208:211], a[236:239], a[76:79]\n" \
"ds_read_b64_tr_b16 a[188:189], v39 offset:0\n" \
"v_mfma_f32_16x16x32_bf16 a[92:95], a[148:151], a[172:175], a[92:95]\n" \
"s_mov_b64 exec, 0xffff\n" \
"global_store_dwordx4 v49, v[0:3], s[28:29] offset:2048 sc1\n" \
"s_mov_b64 exec, -1\n" \
"s_nop 0\n" \
"v_mfma_f32_16x16x32_bf16 a[92:95], a[212:215], a[236:239], a[92:95]\n" \
"v_mfma_f32_16x16x32_bf16 a[88:91], a[148:151], a[168:171], a[88:91]\n" \
"ds_read_b64_tr_b16 a[190:191], v39 offset:1024\n" \
"v_mfma_f32_16x16x32_bf16 a[88:91], a[212:215], a[232:235], a[88:91]\n" \
"v_mfma_f32_16x16x32_bf16 a[84:87], a[148:151], a[164:167], a[84:87]\n" \
"v_mfma_f32_16x16x32_bf16 a[84:87], a[212:215], a[228:231], a[84:87]\n" \
"ds_read_b64_tr_b16 a[252:253], v39 offset:8192\n" \
"v_mfma_f32_16x16x32_bf16 a[80:83], a[148:151], a[160:163], a[80:83]\n" \
"v_mfma_f32_16x16x32_bf16 a[80:83], a[212:215], a[224:227], a[80:83]\n" \
"v_mfma_f32_16x16x32_bf16 a[96:99], a[152:155], a[160:163], a[96:99]\n" \
"ds_read_b64_tr_b16 a[254:255], v39 offset:9216\n" \
"v_mfma_f32_16x16x32_bf16 a[96:99], a[216:219], a[224:227], a[96:99]\n" \
"v_mfma_f32_16x16x32_bf16 a[100:103], a[152:155], a[164:167], a[100:103]\n" \
"v_mfma_f32_16x16x32_bf16 a[100:103], a[216:219], a[228:231], a[100:103]\n" \
"v_mfma_f32_16x16x32_bf16 a[104:107], a[152:155], a[168:171], a[104:107]\n" \
"v_mfma_f32_16x16x32_bf16 a[104:107], a[216:219], a[232:235], a[104:107]\n" \
"v_mfma_f32_16x16x32_bf16 a[108:111], a[152:155], a[172:175], a[108:111]\n" \
"v_mfma_f32_16x16x32_bf16 a[108:111], a[216:219], a[236:239], a[108:111]\n" \
"s_waitcnt lgkmcnt(15)\n" \
"v_mfma_f32_16x16x32_bf16 a[124:127], a[156:159], a[172:175], a[124:127]\n" \
"v_mfma_f32_16x16x32_bf16 a[124:127], a[220:223], a[236:239], a[124:127]\n" \
"v_mfma_f32_16x16x32_bf16 a[120:123], a[156:159], a[168:171], a[120:123]\n" \
"v_mfma_f32_16x16x32_bf16 a[120:123], a[220:223], a[232:235], a[120:123]\n" \
"v_mfma_f32_16x16x32_bf16 a[116:119], a[156:159], a[164:167], a[116:119]\n" \
"v_mfma_f32_16x16x32_bf16 a[116:119], a[220:223], a[228:231], a[116:119]\n" \
"v_mfma_f32_16x16x32_bf16 a[112:115], a[156:159], a[160:163], a[112:115]\n" \
"v_mfma_f32_16x16x32_bf16 a[112:115], a[220:223], a[224:227], a[112:115]\n" \
"s_waitcnt vmcnt(3) lgkmcnt(0)\n" \
"s_barrier\n" \
"v_cmp_ne_u32_e32 vcc, s62, v44\n" \
"s_nop 4\n" \
"s_cbranch_vccnz L_poll61_%=\n" \
"L_pollr61_%=:\n" \
"s_mov_b32 s68, s12\n" \
"s_and_b32 s69, s13, 0xffff\n" \
"s_mov_b32 s70, 0x480000\n" \
"s_mov_b32 s71, 0x20000\n" \
"v_mfma_f32_16x16x32_bf16 v[52:55], a[128:131], a[176:179], v[52:55]\n" \
"s_add_u32 m0, s40, 0x0\n" \
"s_add_u32 s85, s39, 0x0\n" \
"s_nop 0\n" \
"v_mfma_f32_16x16x32_bf16 v[52:55], a[192:195], a[240:243], v[52:55]\n" \
"buffer_load_dwordx4 v20, s[80:83], s85 offen sc1 lds\n" \
"v_mfma_f32_16x16x32_bf16 v[56:59], a[128:131], a[180:183], v[56:59]\n" \
"ds_read_b64_tr_b16 a[160:161], v32 offset:32768\n" \
"v_mfma_f32_16x16x32_bf16 v[56:59], a[192:195], a[244:247], v[56:59]\n" \
"s_add_u32 m0, s40, 0x1000\n" \
"s_add_u32 s85, s39, 0x1000\n" \
"v_mfma_f32_16x16x32_bf16 v[60:63], a[128:131], a[184:187], v[60:63]\n" \
"buffer_load_dwordx4 v20, s[80:83], s85 offen sc1 lds\n" \
".fill (L_pb + 57056 - .) / 4, 4, 0xBF800000\n" \
"L_pp14:\n" \
"s_cbranch_execz L_pret\n" \
"v_mfma_f32_16x16x32_bf16 v[60:63], a[192:195], a[248:251], v[60:63]\n" \
"ds_read_b64_tr_b16 a[162:163], v32 offset:33792\n" \
"v_mfma_f32_16x16x32_bf16 v[64:67], a[128:131], a[188:191], v[64:67]\n" \
"ds_read_b64_tr_b16 a[224:225], v32 offset:40960\n" \
"v_mfma_f32_16x16x32_bf16 v[64:67], a[192:195], a[252:255], v[64:67]\n" \
"ds_read_b128 a[128:131], v28 offset:49152\n" \
"v_mfma_f32_16x16x32_bf16 v[80:83], a[132:135], a[188:191], v[80:83]\n" \
"ds_read_b128 a[192:195], v29 offset:49152\n" \
"v_mfma_f32_16x16x32_bf16 v[80:83], a[196:199], a[252:255], v[80:83]\n" \
"s_add_u32 m0, s40, 0x3000\n" \
"s_movk_i32 s84, 0x0\n" \
"v_mfma_f32_16x16x32_bf16 v[76:79], a[132:135], a[184:187], v[76:79]\n" \
"buffer_load_dwordx4 v21, s[76:79], s84 offen offset:0 lds\n" \
"v_mfma_f32_16x16x32_bf16 v[76:79], a[196:199], a[248:251], v[76:79]\n" \
"s_add_u32 m0, s30, 0x0\n" \
"s_movk_i32 s84, 0x0\n" \
"v_mfma_f32_16x16x32_bf16 v[72:75], a[132:135], a[180:183], v[72:75]\n" \
"buffer_load_dwordx4 v24, s[76:79], s84 offen offset:0 lds\n" \
"v_mfma_f32_16x16x32_bf16 v[72:75], a[196:199], a[244:247], v[72:75]\n" \
"ds_read_b64_tr_b16 a[226:227], v32 offset:41984\n" \
"v_mfma_f32_16x16x32_bf16 v[68:71], a[132:135], a[176:179], v[68:71]\n" \
"ds_read_b64_tr_b16 a[164:165], v33 offset:32768\n" \
"v_mfma_f32_16x16x32_bf16 v[68:71], a[196:199], a[240:243], v[68:71]\n" \
"ds_read_b128 a[132:135], v28 offset:51200\n" \
"v_mfma_f32_16x16x32_bf16 v[84:87], a[136:139], a[176:179], v[84:87]\n" \
"ds_read_b128 a[196:199], v29 offset:51200\n" \
"v_mfma_f32_16x16x32_bf16 v[84:87], a[200:203], a[240:243], v[84:87]\n" \
"s_movk_i32 s84, 0x2000\n" \
"v_mfma_f32_16x16x32_bf16 v[88:91], a[136:139], a[180:183], v[88:91]\n" \
"buffer_load_dwordx4 v25, s[76:79], s84 offen offset:1024 lds\n" \
"v_mfma_f32_16x16x32_bf16 v[88:91], a[200:203], a[244:247], v[88:91]\n" \
"s_movk_i32 s84, 0x4000\n" \
"v_mfma_f32_16x16x32_bf16 v[92:95], a[136:139], a[184:187], v[92:95]\n" \
"buffer_load_dwordx4 v24, s[76:79], s84 offen offset:2048 lds\n" \
"v_mfma_f32_16x16x32_bf16 v[92:95], a[200:203], a[248:251], v[92:95]\n" \
"ds_read_b64_tr_b16 a[166:167], v33 offset:33792\n" \
"v_mfma_f32_16x16x32_bf16 v[96:99], a[136:139], a[188:191], v[96:99]\n" \
"ds_read_b64_tr_b16 a[228:229], v33 offset:40960\n" \
"v_mfma_f32_16x16x32_bf16 v[96:99], a[200:203], a[252:255], v[96:99]\n" \
"ds_read_b128 a[136:139], v28 offset:53248\n" \
"v_mfma_f32_16x16x32_bf16 v[112:115], a[140:143], a[188:191], v[112:115]\n" \
"ds_read_b128 a[200:203], v29 offset:53248\n" \
"v_mfma_f32_16x16x32_bf16 v[112:115], a[204:207], a[252:255], v[112:115]\n" \
"s_movk_i32 s84, 0x6000\n" \
"v_mfma_f32_16x16x32_bf16 v[108:111], a[140:143], a[184:187], v[108:111]\n" \
"buffer_load_dwordx4 v25, s[76:79], s84 offen offset:3072 lds\n" \
"v_mfma_f32_16x16x32_bf16 v[108:111], a[204:207], a[248:251], v[108:111]\n" \
"s_add_u32 m0, s31, 0x0\n" \
"s_movk_i32 s84, 0x0\n" \
"v_mfma_f32_16x16x32_bf16 v[104:107], a[140:143], a[180:183], v[104:107]\n" \
"buffer_load_dwordx4 v26, s[68:71], s84 offen offset:0 lds\n" \
"v_mfma_f32_16x16x32_bf16 v[104:107], a[204:207], a[244:247], v[104:107]\n" \
"s_movk_i32 s84, 0x7c00\n" \
"v_mfma_f32_16x16x32_bf16 v[100:103], a[140:143], a[176:179], v[100:103]\n" \
"buffer_load_dwordx4 v26, s[68:71], s84 offen offset:1024 lds\n" \
"v_mfma_f32_16x16x32_bf16 v[100:103], a[204:207], a[240:243], v[100:103]\n" \
"ds_read_b128 a[140:143], v28 offset:55296\n" \
"v_mfma_f32_16x16x32_bf16 v[116:119], a[144:147], a[176:179], v[116:119]\n" \
"ds_read_b128 a[204:207], v29 offset:55296\n" \
"v_mfma_f32_16x16x32_bf16 v[116:119], a[208:211], a[240:243], v[116:119]\n" \
"s_addk_i32 s84, 0x7c00\n" \
"v_mfma_f32_16x16x32_bf16 v[120:123], a[144:147], a[180:183], v[120:123]\n" \
"buffer_load_dwordx4 v27, s[68:71], s84 offen offset:2048 lds\n" \
"v_mfma_f32_16x16x32_bf16 v[120:123], a[208:211], a[244:247], v[120:123]\n" \
"ds_read_b64_tr_b16 a[230:231], v33 offset:41984\n" \
"v_mfma_f32_16x16x32_bf16 v[124:127], a[144:147], a[184:187], v[124:127]\n" \
"s_addk_i32 s84, 0x7c00\n" \
"v_mfma_f32_16x16x32_bf16 v[124:127], a[208:211], a[248:251], v[124:127]\n" \
"buffer_load_dwordx4 v27, s[68:71], s84 offen offset:3072 lds\n" \
"v_mfma_f32_16x16x32_bf16 v[128:131], a[144:147], a[188:191], v[128:131]\n" \
"ds_read_b64_tr_b16 a[168:169], v34 offset:32768\n" \
"v_mfma_f32_16x16x32_bf16 v[128:131], a[208:211], a[252:255], v[128:131]\n" \
"ds_read_b128 a[144:147], v28 offset:32768\n" \
"v_mfma_f32_16x16x32_bf16 v[144:147], a[148:151], a[188:191], v[144:147]\n" \
"ds_read_b128 a[208:211], v29 offset:32768\n" \
"v_mfma_f32_16x16x32_bf16 v[144:147], a[212:215], a[252:255], v[144:147]\n" \
"s_add_u32 m0, s30, 0x1000\n" \
"s_mov_b32 s84, 0x9000\n" \
"v_mfma_f32_16x16x32_bf16 v[140:143], a[148:151], a[184:187], v[140:143]\n" \
"buffer_load_dwordx4 v24, s[76:79], s84 offen offset:0 lds\n" \
"v_mfma_f32_16x16x32_bf16 v[140:143], a[212:215], a[248:251], v[140:143]\n" \
"ds_read_b64_tr_b16 a[170:171], v34 offset:33792\n" \
"v_mfma_f32_16x16x32_bf16 v[136:139], a[148:151], a[180:183], v[136:139]\n" \
"s_addk_i32 s84, 0x2000\n" \
"s_nop 0\n" \
"v_mfma_f32_16x16x32_bf16 v[136:139], a[212:215], a[244:247], v[136:139]\n" \
"buffer_load_dwordx4 v25, s[76:79], s84 offen offset:1024 lds\n" \
"v_mfma_f32_16x16x32_bf16 v[132:135], a[148:151], a[176:179], v[132:135]\n" \
"ds_read_b64_tr_b16 a[232:233], v34 offset:40960\n" \
"v_mfma_f32_16x16x32_bf16 v[132:135], a[212:215], a[240:243], v[132:135]\n" \
"ds_read_b128 a[148:151], v28 offset:34816\n" \
"v_mfma_f32_16x16x32_bf16 v[148:151], a[152:155], a[176:179], v[148:151]\n" \
"ds_read_b128 a[212:215], v29 offset:34816\n" \
"v_mfma_f32_16x16x32_bf16 v[148:151], a[216:219], a[240:243], v[148:151]\n" \
"ds_read_b64_tr_b16 a[234:235], v34 offset:41984\n" \
"v_mfma_f32_16x16x32_bf16 v[152:155], a[152:155], a[180:183], v[152:155]\n" \
"ds_read_b64_tr_b16 a[172:173], v35 offset:32768\n" \
"v_mfma_f32_16x16x32_bf16 v[152:155], a[216:219], a[244:247], v[152:155]\n" \
"ds_read_b64_tr_b16 a[174:175], v35 offset:33792\n" \
"v_mfma_f32_16x16x32_bf16 v[156:159], a[152:155], a[184:187], v[156:159]\n" \
"ds_read_b64_tr_b16 a[236:237], v35 offset:40960\n" \
"v_mfma_f32_16x16x32_bf16 v[156:159], a[216:219], a[248:251], v[156:159]\n" \
"ds_read_b64_tr_b16 a[238:239], v35 offset:41984\n" \
"v_mfma_f32_16x16x32_bf16 v[160:163], a[152:155], a[188:191], v[160:163]\n" \
"v_mfma_f32_16x16x32_bf16 v[160:163], a[216:219], a[252:255], v[160:163]\n" \
"ds_read_b128 a[152:155], v28 offset:36864\n" \
"v_mfma_f32_16x16x32_bf16 v[176:179], a[156:159], a[188:191], v[176:179]\n" \
"ds_read_b128 a[216:219], v29 offset:36864\n" \
"v_mfma_f32_16x16x32_bf16 v[176:179], a[220:223], a[252:255], v[176:179]\n" \
"v_mfma_f32_16x16x32_bf16 v[172:175], a[156:159], a[184:187], v[172:175]\n" \
"v_mfma_f32_16x16x32_bf16 v[172:175], a[220:223], a[248:251], v[172:175]\n" \
"v_mfma_f32_16x16x32_bf16 v[168:171], a[156:159], a[180:183], v[168:171]\n" \
"v_mfma_f32_16x16x32_bf16 v[168:171], a[220:223], a[244:247], v[168:171]\n" \
"v_mfma_f32_16x16x32_bf16 v[164:167], a[156:159], a[176:179], v[164:167]\n" \
"v_mfma_f32_16x16x32_bf16 v[164:167], a[220:223], a[240:243], v[164:167]\n" \
"ds_read_b128 a[156:159], v28 offset:38912\n" \
"ds_read_b128 a[220:223], v29 offset:38912\n" \
"s_waitcnt lgkmcnt(15)\n" \
"v_mfma_f32_16x16x32_bf16 a[0:3], a[128:131], a[160:163], a[0:3]\n" \
"s_addk_i32 s84, 0x2000\n" \
"v_mfma_f32_16x16x32_bf16 a[0:3], a[192:195], a[224:227], a[0:3]\n" \
"buffer_load_dwordx4 v24, s[76:79], s84 offen offset:2048 lds\n" \
"v_mfma_f32_16x16x32_bf16 a[4:7], a[128:131], a[164:167], a[4:7]\n" \
"ds_read_b64_tr_b16 a[176:177], v36 offset:32768\n" \
"v_mfma_f32_16x16x32_bf16 a[4:7], a[192:195], a[228:231], a[4:7]\n" \
"s_addk_i32 s84, 0x2000\n" \
"s_waitcnt lgkmcnt(13)\n" \
"v_mfma_f32_16x16x32_bf16 a[8:11], a[128:131], a[168:171], a[8:11]\n" \
"buffer_load_dwordx4 v25, s[76:79], s84 offen offset:3072 lds\n" \
"s_waitcnt lgkmcnt(9)\n" \
"v_mfma_f32_16x16x32_bf16 a[8:11], a[192:195], a[232:235], a[8:11]\n" \
"ds_read_b64_tr_b16 a[178:179], v36 offset:33792\n" \
"s_waitcnt lgkmcnt(8)\n" \
"v_mfma_f32_16x16x32_bf16 a[12:15], a[128:131], a[172:175], a[12:15]\n" \
"ds_read_b64_tr_b16 a[240:241], v36 offset:40960\n" \
"s_waitcnt lgkmcnt(7)\n" \
"v_mfma_f32_16x16x32_bf16 a[12:15], a[192:195], a[236:239], a[12:15]\n" \
"v_mfma_f32_16x16x32_bf16 a[28:31], a[132:135], a[172:175], a[28:31]\n" \
"v_mfma_f32_16x16x32_bf16 a[28:31], a[196:199], a[236:239], a[28:31]\n" \
"s_add_u32 m0, s30, 0x20000\n" \
"s_movk_i32 s84, 0x100\n" \
"v_mfma_f32_16x16x32_bf16 a[24:27], a[132:135], a[168:171], a[24:27]\n" \
"buffer_load_dwordx4 v24, s[76:79], s84 offen offset:0 lds\n" \
"v_mfma_f32_16x16x32_bf16 a[24:27], a[196:199], a[232:235], a[24:27]\n" \
"ds_read_b64_tr_b16 a[242:243], v36 offset:41984\n" \
"v_mfma_f32_16x16x32_bf16 a[20:23], a[132:135], a[164:167], a[20:23]\n" \
"v_accvgpr_read_b32 v184, a12\n" \
"v_accvgpr_read_b32 v185, a13\n" \
"v_mfma_f32_16x16x32_bf16 a[20:23], a[196:199], a[228:231], a[20:23]\n" \
"ds_read_b64_tr_b16 a[180:181], v37 offset:32768\n" \
"v_accvgpr_read_b32 v186, a14\n" \
"v_mfma_f32_16x16x32_bf16 a[16:19], a[132:135], a[160:163], a[16:19]\n" \
"s_movk_i32 s84, 0x2100\n" \
"s_nop 0\n" \
"v_mfma_f32_16x16x32_bf16 a[16:19], a[196:199], a[224:227], a[16:19]\n" \
"buffer_load_dwordx4 v25, s[76:79], s84 offen offset:1024 lds\n" \
"v_mfma_f32_16x16x32_bf16 a[32:35], a[136:139], a[160:163], a[32:35]\n" \
"ds_read_b64_tr_b16 a[182:183], v37 offset:33792\n" \
"v_accvgpr_read_b32 v187, a15\n" \
"v_mfma_f32_16x16x32_bf16 a[32:35], a[200:203], a[224:227], a[32:35]\n" \
"v_accvgpr_read_b32 v180, a28\n" \
"v_accvgpr_read_b32 v181, a29\n" \
"v_mfma_f32_16x16x32_bf16 a[36:39], a[136:139], a[164:167], a[36:39]\n" \
"v_accvgpr_read_b32 v182, a30\n" \
"v_accvgpr_read_b32 v183, a31\n" \
"v_mfma_f32_16x16x32_bf16 a[36:39], a[200:203], a[228:231], a[36:39]\n" \
"s_movk_i32 s84, 0x4100\n" \
"s_nop 0\n" \
"v_mfma_f32_16x16x32_bf16 a[40:43], a[136:139], a[168:171], a[40:43]\n" \
"buffer_load_dwordx4 v24, s[76:79], s84 offen offset:2048 lds\n" \
"v_mfma_f32_16x16x32_bf16 a[40:43], a[200:203], a[232:235], a[40:43]\n" \
"ds_read_b64_tr_b16 a[244:245], v37 offset:40960\n" \
"v_cvt_pk_bf16_f32 v184, v184, v185\n" \
"v_mfma_f32_16x16x32_bf16 a[44:47], a[136:139], a[172:175], a[44:47]\n" \
"ds_read_b64_tr_b16 a[246:247], v37 offset:41984\n" \
"v_cvt_pk_bf16_f32 v185, v186, v187\n" \
"v_mfma_f32_16x16x32_bf16 a[44:47], a[200:203], a[236:239], a[44:47]\n" \
"v_cvt_pk_bf16_f32 v186, v180, v181\n" \
"v_cvt_pk_bf16_f32 v187, v182, v183\n" \
"v_mfma_f32_16x16x32_bf16 a[60:63], a[140:143], a[172:175], a[60:63]\n" \
"s_movk_i32 s84, 0x6100\n" \
"v_mfma_f32_16x16x32_bf16 a[60:63], a[204:207], a[236:239], a[60:63]\n" \
"buffer_load_dwordx4 v25, s[76:79], s84 offen offset:3072 lds\n" \
"v_mfma_f32_16x16x32_bf16 a[56:59], a[140:143], a[168:171], a[56:59]\n" \
"ds_read_b64_tr_b16 a[184:185], v38 offset:32768\n" \
"v_accvgpr_read_b32 v188, a8\n" \
"v_mfma_f32_16x16x32_bf16 a[56:59], a[204:207], a[232:235], a[56:59]\n" \
"v_accvgpr_read_b32 v189, a9\n" \
"v_accvgpr_read_b32 v190, a10\n" \
"v_mfma_f32_16x16x32_bf16 a[52:55], a[140:143], a[164:167], a[52:55]\n" \
"s_add_u32 m0, s30, 0x21000\n" \
"s_addk_i32 s84, 0x3000\n" \
"v_mfma_f32_16x16x32_bf16 a[52:55], a[204:207], a[228:231], a[52:55]\n" \
"buffer_load_dwordx4 v24, s[76:79], s84 offen offset:0 lds\n" \
"v_mfma_f32_16x16x32_bf16 a[48:51], a[140:143], a[160:163], a[48:51]\n" \
"ds_read_b64_tr_b16 a[186:187], v38 offset:33792\n" \
"v_accvgpr_read_b32 v191, a11\n" \
"v_mfma_f32_16x16x32_bf16 a[48:51], a[204:207], a[224:227], a[48:51]\n" \
"v_accvgpr_read_b32 v180, a24\n" \
"v_accvgpr_read_b32 v181, a25\n" \
"v_mfma_f32_16x16x32_bf16 a[64:67], a[144:147], a[160:163], a[64:67]\n" \
"ds_read_b64_tr_b16 a[248:249], v38 offset:40960\n" \
"v_accvgpr_read_b32 v182, a26\n" \
"v_mfma_f32_16x16x32_bf16 a[64:67], a[208:211], a[224:227], a[64:67]\n" \
"s_addk_i32 s84, 0x2000\n" \
"s_nop 0\n" \
"v_mfma_f32_16x16x32_bf16 a[68:71], a[144:147], a[164:167], a[68:71]\n" \
"buffer_load_dwordx4 v25, s[76:79], s84 offen offset:1024 lds\n" \
"v_mfma_f32_16x16x32_bf16 a[68:71], a[208:211], a[228:231], a[68:71]\n" \
"ds_read_b64_tr_b16 a[250:251], v38 offset:41984\n" \
"v_accvgpr_read_b32 v183, a27\n" \
"v_mfma_f32_16x16x32_bf16 a[72:75], a[144:147], a[168:171], a[72:75]\n" \
"v_cvt_pk_bf16_f32 v188, v188, v189\n" \
"v_cvt_pk_bf16_f32 v189, v190, v191\n" \
"v_mfma_f32_16x16x32_bf16 a[72:75], a[208:211], a[232:235], a[72:75]\n" \
"v_cvt_pk_bf16_f32 v190, v180, v181\n" \
"v_cvt_pk_bf16_f32 v191, v182, v183\n" \
"v_mfma_f32_16x16x32_bf16 a[76:79], a[144:147], a[172:175], a[76:79]\n" \
"s_addk_i32 s84, 0x2000\n" \
"v_mfma_f32_16x16x32_bf16 a[76:79], a[208:211], a[236:239], a[76:79]\n" \
"buffer_load_dwordx4 v24, s[76:79], s84 offen offset:2048 lds\n" \
"v_mfma_f32_16x16x32_bf16 a[92:95], a[148:151], a[172:175], a[92:95]\n" \
"s_add_u32 s89, s41, 0x0\n" \
"buffer_store_dwordx4 v[184:187], v20, s[80:83], s89 offen offset:3072 sc1\n" \
"v_mfma_f32_16x16x32_bf16 a[92:95], a[212:215], a[236:239], a[92:95]\n" \
"ds_read_b64_tr_b16 a[188:189], v39 offset:32768\n" \
"v_accvgpr_read_b32 v192, a4\n" \
"v_mfma_f32_16x16x32_bf16 a[88:91], a[148:151], a[168:171], a[88:91]\n" \
"ds_read_b64_tr_b16 a[190:191], v39 offset:33792\n" \
"v_accvgpr_read_b32 v193, a5\n" \
"v_mfma_f32_16x16x32_bf16 a[88:91], a[212:215], a[232:235], a[88:91]\n" \
"s_addk_i32 s84, 0x2000\n" \
"v_mfma_f32_16x16x32_bf16 a[84:87], a[148:151], a[164:167], a[84:87]\n" \
"buffer_load_dwordx4 v25, s[76:79], s84 offen offset:3072 lds\n" \
"v_mfma_f32_16x16x32_bf16 a[84:87], a[212:215], a[228:231], a[84:87]\n" \
"ds_read_b64_tr_b16 a[252:253], v39 offset:40960\n" \
"v_accvgpr_read_b32 v194, a6\n" \
"v_mfma_f32_16x16x32_bf16 a[80:83], a[148:151], a[160:163], a[80:83]\n" \
"s_add_u32 s88, s41, 0x0\n" \
"buffer_store_dwordx4 v[188:191], v20, s[80:83], s88 offen offset:2048 sc1\n" \
"v_mfma_f32_16x16x32_bf16 a[80:83], a[212:215], a[224:227], a[80:83]\n" \
"v_accvgpr_read_b32 v195, a7\n" \
"v_accvgpr_read_b32 v180, a20\n" \
"s_waitcnt lgkmcnt(15)\n" \
"s_nop 0\n" \
"v_mfma_f32_16x16x32_bf16 a[96:99], a[152:155], a[160:163], a[96:99]\n" \
"ds_read_b64_tr_b16 a[254:255], v39 offset:41984\n" \
"v_accvgpr_read_b32 v181, a21\n" \
"v_mfma_f32_16x16x32_bf16 a[96:99], a[216:219], a[224:227], a[96:99]\n" \
"v_accvgpr_read_b32 v182, a22\n" \
"v_accvgpr_read_b32 v183, a23\n" \
"v_mfma_f32_16x16x32_bf16 a[100:103], a[152:155], a[164:167], a[100:103]\n" \
"v_cvt_pk_bf16_f32 v192, v192, v193\n" \
"v_cvt_pk_bf16_f32 v193, v194, v195\n" \
"v_mfma_f32_16x16x32_bf16 a[100:103], a[216:219], a[228:231], a[100:103]\n" \
"v_cvt_pk_bf16_f32 v194, v180, v181\n" \
"v_cvt_pk_bf16_f32 v195, v182, v183\n" \
"v_mfma_f32_16x16x32_bf16 a[104:107], a[152:155], a[168:171], a[104:107]\n" \
"s_add_u32 s89, s41, 0x0\n" \
"s_nop 0\n" \
"buffer_store_dwordx4 v[192:195], v20, s[80:83], s89 offen offset:1024 sc1\n" \
"v_mfma_f32_16x16x32_bf16 a[104:107], a[216:219], a[232:235], a[104:107]\n" \
"v_accvgpr_read_b32 v196, a0\n" \
"v_accvgpr_read_b32 v197, a1\n" \
"v_mfma_f32_16x16x32_bf16 a[108:111], a[152:155], a[172:175], a[108:111]\n" \
"v_accvgpr_read_b32 v198, a2\n" \
"v_accvgpr_read_b32 v199, a3\n" \
"v_mfma_f32_16x16x32_bf16 a[108:111], a[216:219], a[236:239], a[108:111]\n" \
"v_accvgpr_read_b32 v180, a16\n" \
"v_accvgpr_read_b32 v181, a17\n" \
"v_mfma_f32_16x16x32_bf16 a[124:127], a[156:159], a[172:175], a[124:127]\n" \
"v_accvgpr_read_b32 v182, a18\n" \
"v_accvgpr_read_b32 v183, a19\n" \
"v_mfma_f32_16x16x32_bf16 a[124:127], a[220:223], a[236:239], a[124:127]\n" \
"v_cvt_pk_bf16_f32 v196, v196, v197\n" \
"v_cvt_pk_bf16_f32 v197, v198, v199\n" \
"v_mfma_f32_16x16x32_bf16 a[120:123], a[156:159], a[168:171], a[120:123]\n" \
"v_cvt_pk_bf16_f32 v198, v180, v181\n" \
"v_cvt_pk_bf16_f32 v199, v182, v183\n" \
"s_add_u32 s88, s41, 0x0\n" \
"s_nop 0\n" \
"buffer_store_dwordx4 v[196:199], v20, s[80:83], s88 offen offset:0 sc1\n" \
"v_mfma_f32_16x16x32_bf16 a[120:123], a[220:223], a[232:235], a[120:123]\n" \
"v_accvgpr_read_b32 v200, a44\n" \
"v_accvgpr_read_b32 v201, a45\n" \
"v_mfma_f32_16x16x32_bf16 a[116:119], a[156:159], a[164:167], a[116:119]\n" \
"v_accvgpr_read_b32 v202, a46\n" \
"v_accvgpr_read_b32 v203, a47\n" \
"v_mfma_f32_16x16x32_bf16 a[116:119], a[220:223], a[228:231], a[116:119]\n" \
"v_accvgpr_read_b32 v180, a60\n" \
"v_accvgpr_read_b32 v181, a61\n" \
"v_mfma_f32_16x16x32_bf16 a[112:115], a[156:159], a[160:163], a[112:115]\n" \
"v_accvgpr_read_b32 v182, a62\n" \
"v_accvgpr_read_b32 v183, a63\n" \
"v_mfma_f32_16x16x32_bf16 a[112:115], a[220:223], a[224:227], a[112:115]\n" \
"v_cvt_pk_bf16_f32 v200, v200, v201\n" \
"v_cvt_pk_bf16_f32 v201, v202, v203\n" \
"s_waitcnt vmcnt(12) lgkmcnt(0)\n" \
"s_barrier\n" \
"v_xor_b32_e32 v36, 0x4000, v36\n" \
"v_xor_b32_e64 v32, 0x0, v48\n" \
"v_xor_b32_e32 v37, 0x4000, v37\n" \
"v_xor_b32_e64 v33, 0x20, v48\n" \
"v_xor_b32_e32 v38, 0x4000, v38\n" \
"v_xor_b32_e64 v34, 0x40, v48\n" \
"v_xor_b32_e32 v39, 0x4000, v39\n" \
"v_xor_b32_e32 v35, 0x60, v48\n" \
"v_add_u32_e32 v28, s43, v28\n" \
"v_add_u32_e32 v29, s43, v29\n" \
"v_mfma_f32_16x16x32_bf16 v[116:119], a[144:147], a[176:179], v[116:119]\n" \
"s_add_u32 m0, s40, 0x8000\n" \
"s_add_u32 s85, s39, 0x2000\n" \
"v_mfma_f32_16x16x32_bf16 v[116:119], a[208:211], a[240:243], v[116:119]\n" \
"buffer_load_dwordx4 v20, s[80:83], s85 offen sc1 lds\n" \
"v_mfma_f32_16x16x32_bf16 v[120:123], a[144:147], a[180:183], v[120:123]\n" \
"ds_read_b64_tr_b16 a[160:161], v32 offset:0\n" \
"v_cvt_pk_bf16_f32 v202, v180, v181\n" \
"v_mfma_f32_16x16x32_bf16 v[120:123], a[208:211], a[244:247], v[120:123]\n" \
"s_add_u32 m0, s40, 0x9000\n" \
"s_add_u32 s85, s39, 0x3000\n" \
"v_mfma_f32_16x16x32_bf16 v[124:127], a[144:147], a[184:187], v[124:127]\n" \
"buffer_load_dwordx4 v20, s[80:83], s85 offen sc1 lds\n" \
"v_mfma_f32_16x16x32_bf16 v[124:127], a[208:211], a[248:251], v[124:127]\n" \
"ds_read_b64_tr_b16 a[162:163], v32 offset:512\n" \
"v_cvt_pk_bf16_f32 v203, v182, v183\n" \
"v_mfma_f32_16x16x32_bf16 v[128:131], a[144:147], a[188:191], v[128:131]\n" \
"ds_read_b64_tr_b16 a[224:225], v32 offset:4096\n" \
"v_accvgpr_read_b32 v204, a40\n" \
"v_mfma_f32_16x16x32_bf16 v[128:131], a[208:211], a[252:255], v[128:131]\n" \
"ds_read_b128 a[144:147], v28 offset:8192\n" \
"v_accvgpr_read_b32 v205, a41\n" \
"v_mfma_f32_16x16x32_bf16 v[144:147], a[148:151], a[188:191], v[144:147]\n" \
"ds_read_b128 a[208:211], v29 offset:8192\n" \
"v_accvgpr_read_b32 v206, a42\n" \
"v_mfma_f32_16x16x32_bf16 v[144:147], a[212:215], a[252:255], v[144:147]\n" \
"s_add_u32 m0, s40, 0xb000\n" \
"s_movk_i32 s84, 0x80\n" \
"v_mfma_f32_16x16x32_bf16 v[140:143], a[148:151], a[184:187], v[140:143]\n" \
"buffer_load_dwordx4 v21, s[76:79], s84 offen offset:0 lds\n" \
"v_mfma_f32_16x16x32_bf16 v[140:143], a[212:215], a[248:251], v[140:143]\n" \
"s_add_u32 m0, s30, 0x8000\n" \
"s_movk_i32 s84, 0x80\n" \
"v_mfma_f32_16x16x32_bf16 v[136:139], a[148:151], a[180:183], v[136:139]\n" \
"buffer_load_dwordx4 v24, s[76:79], s84 offen offset:0 lds\n" \
"v_mfma_f32_16x16x32_bf16 v[136:139], a[212:215], a[244:247], v[136:139]\n" \
"ds_read_b64_tr_b16 a[226:227], v32 offset:4608\n" \
"v_accvgpr_read_b32 v207, a43\n" \
"v_mfma_f32_16x16x32_bf16 v[132:135], a[148:151], a[176:179], v[132:135]\n" \
"ds_read_b64_tr_b16 a[164:165], v33 offset:0\n" \
"v_accvgpr_read_b32 v180, a56\n" \
"v_mfma_f32_16x16x32_bf16 v[132:135], a[212:215], a[240:243], v[132:135]\n" \
"ds_read_b128 a[148:151], v28 offset:10240\n" \
"v_accvgpr_read_b32 v181, a57\n" \
"v_mfma_f32_16x16x32_bf16 v[148:151], a[152:155], a[176:179], v[148:151]\n" \
"ds_read_b128 a[212:215], v29 offset:10240\n" \
"v_accvgpr_read_b32 v182, a58\n" \
"v_mfma_f32_16x16x32_bf16 v[148:151], a[216:219], a[240:243], v[148:151]\n" \
"s_movk_i32 s84, 0x2080\n" \
"v_mfma_f32_16x16x32_bf16 v[152:155], a[152:155], a[180:183], v[152:155]\n" \
"buffer_load_dwordx4 v25, s[76:79], s84 offen offset:1024 lds\n" \
"v_mfma_f32_16x16x32_bf16 v[152:155], a[216:219], a[244:247], v[152:155]\n" \
"s_movk_i32 s84, 0x4080\n" \
"v_mfma_f32_16x16x32_bf16 v[156:159], a[152:155], a[184:187], v[156:159]\n" \
"buffer_load_dwordx4 v24, s[76:79], s84 offen offset:2048 lds\n" \
"v_mfma_f32_16x16x32_bf16 v[156:159], a[216:219], a[248:251], v[156:159]\n" \
"ds_read_b64_tr_b16 a[166:167], v33 offset:512\n" \
"v_accvgpr_read_b32 v183, a59\n" \
"v_mfma_f32_16x16x32_bf16 v[160:163], a[152:155], a[188:191], v[160:163]\n" \
"ds_read_b64_tr_b16 a[228:229], v33 offset:4096\n" \
"v_cvt_pk_bf16_f32 v204, v204, v205\n" \
"v_mfma_f32_16x16x32_bf16 v[160:163], a[216:219], a[252:255], v[160:163]\n" \
"ds_read_b128 a[152:155], v28 offset:12288\n" \
"v_cvt_pk_bf16_f32 v205, v206, v207\n" \
"v_mfma_f32_16x16x32_bf16 v[176:179], a[156:159], a[188:191], v[176:179]\n" \
"ds_read_b128 a[216:219], v29 offset:12288\n" \
"v_cvt_pk_bf16_f32 v206, v180, v181\n" \
"v_mfma_f32_16x16x32_bf16 v[176:179], a[220:223], a[252:255], v[176:179]\n" \
"s_movk_i32 s84, 0x6080\n" \
"v_mfma_f32_16x16x32_bf16 v[172:175], a[156:159], a[184:187], v[172:175]\n" \
"buffer_load_dwordx4 v25, s[76:79], s84 offen offset:3072 lds\n" \
"v_mfma_f32_16x16x32_bf16 v[172:175], a[220:223], a[248:251], v[172:175]\n" \
"s_add_u32 m0, s31, 0x8000\n" \
"s_mov_b32 s84, 0x80000\n" \
"v_mfma_f32_16x16x32_bf16 v[168:171], a[156:159], a[180:183], v[168:171]\n" \
"buffer_load_dwordx4 v26, s[68:71], s84 offen offset:0 lds\n" \
"v_mfma_f32_16x16x32_bf16 v[168:171], a[220:223], a[244:247], v[168:171]\n" \
"s_addk_i32 s84, 0x7c00\n" \
"v_mfma_f32_16x16x32_bf16 v[164:167], a[156:159], a[176:179], v[164:167]\n" \
"buffer_load_dwordx4 v26, s[68:71], s84 offen offset:1024 lds\n" \
"v_mfma_f32_16x16x32_bf16 v[164:167], a[220:223], a[240:243], v[164:167]\n" \
"ds_read_b128 a[156:159], v28 offset:14336\n" \
"v_cvt_pk_bf16_f32 v207, v182, v183\n" \
"v_mfma_f32_16x16x32_bf16 v[84:87], a[136:139], a[176:179], v[84:87]\n" \
"ds_read_b128 a[220:223], v29 offset:14336\n" \
"v_accvgpr_read_b32 v208, a36\n" \
"v_mfma_f32_16x16x32_bf16 v[84:87], a[200:203], a[240:243], v[84:87]\n" \
"s_addk_i32 s84, 0x7c00\n" \
"v_mfma_f32_16x16x32_bf16 v[88:91], a[136:139], a[180:183], v[88:91]\n" \
"buffer_load_dwordx4 v27, s[68:71], s84 offen offset:2048 lds\n" \
"v_mfma_f32_16x16x32_bf16 v[88:91], a[200:203], a[244:247], v[88:91]\n" \
"ds_read_b64_tr_b16 a[230:231], v33 offset:4608\n" \
"v_accvgpr_read_b32 v209, a37\n" \
"v_mfma_f32_16x16x32_bf16 v[92:95], a[136:139], a[184:187], v[92:95]\n" \
"s_addk_i32 s84, 0x7c00\n" \
"v_mfma_f32_16x16x32_bf16 v[92:95], a[200:203], a[248:251], v[92:95]\n" \
"buffer_load_dwordx4 v27, s[68:71], s84 offen offset:3072 lds\n" \
"v_mfma_f32_16x16x32_bf16 v[96:99], a[136:139], a[188:191], v[96:99]\n" \
"ds_read_b64_tr_b16 a[172:173], v35 offset:0\n" \
"v_accvgpr_read_b32 v210, a38\n" \
"v_mfma_f32_16x16x32_bf16 v[96:99], a[200:203], a[252:255], v[96:99]\n" \
"ds_read_b128 a[136:139], v28 offset:4096\n" \
"v_accvgpr_read_b32 v211, a39\n" \
"v_mfma_f32_16x16x32_bf16 v[112:115], a[140:143], a[188:191], v[112:115]\n" \
"ds_read_b128 a[200:203], v29 offset:4096\n" \
"v_accvgpr_read_b32 v180, a52\n" \
"v_mfma_f32_16x16x32_bf16 v[112:115], a[204:207], a[252:255], v[112:115]\n" \
"s_add_u32 m0, s30, 0x9000\n" \
"s_mov_b32 s84, 0x9080\n" \
"v_mfma_f32_16x16x32_bf16 v[108:111], a[140:143], a[184:187], v[108:111]\n" \
"buffer_load_dwordx4 v24, s[76:79], s84 offen offset:0 lds\n" \
"v_mfma_f32_16x16x32_bf16 v[108:111], a[204:207], a[248:251], v[108:111]\n" \
"ds_read_b64_tr_b16 a[174:175], v35 offset:512\n" \
"v_accvgpr_read_b32 v181, a53\n" \
"v_mfma_f32_16x16x32_bf16 v[104:107], a[140:143], a[180:183], v[104:107]\n" \
"s_addk_i32 s84, 0x2000\n" \
"s_nop 0\n" \
"v_mfma_f32_16x16x32_bf16 v[104:107], a[204:207], a[244:247], v[104:107]\n" \
"buffer_load_dwordx4 v25, s[76:79], s84 offen offset:1024 lds\n" \
"v_mfma_f32_16x16x32_bf16 v[100:103], a[140:143], a[176:179], v[100:103]\n" \
"ds_read_b64_tr_b16 a[236:237], v35 offset:4096\n" \
"v_accvgpr_read_b32 v182, a54\n" \
"v_mfma_f32_16x16x32_bf16 v[100:103], a[204:207], a[240:243], v[100:103]\n" \
"ds_read_b128 a[140:143], v28 offset:6144\n" \
"v_accvgpr_read_b32 v183, a55\n" \
"v_mfma_f32_16x16x32_bf16 v[52:55], a[128:131], a[176:179], v[52:55]\n" \
"ds_read_b128 a[204:207], v29 offset:6144\n" \
"v_cvt_pk_bf16_f32 v208, v208, v209\n" \
"v_mfma_f32_16x16x32_bf16 v[52:55], a[192:195], a[240:243], v[52:55]\n" \
"ds_read_b64_tr_b16 a[238:239], v35 offset:4608\n" \
"v_cvt_pk_bf16_f32 v209, v210, v211\n" \
"v_mfma_f32_16x16x32_bf16 v[56:59], a[128:131], a[180:183], v[56:59]\n" \
"ds_read_b64_tr_b16 a[168:169], v34 offset:0\n" \
"v_cvt_pk_bf16_f32 v210, v180, v181\n" \
"v_mfma_f32_16x16x32_bf16 v[56:59], a[192:195], a[244:247], v[56:59]\n" \
"ds_read_b64_tr_b16 a[170:171], v34 offset:512\n" \
"v_cvt_pk_bf16_f32 v211, v182, v183\n" \
"v_mfma_f32_16x16x32_bf16 v[60:63], a[128:131], a[184:187], v[60:63]\n" \
"ds_read_b64_tr_b16 a[232:233], v34 offset:4096\n" \
"v_accvgpr_read_b32 v212, a32\n" \
"v_mfma_f32_16x16x32_bf16 v[60:63], a[192:195], a[248:251], v[60:63]\n" \
"ds_read_b64_tr_b16 a[234:235], v34 offset:4608\n" \
"v_accvgpr_read_b32 v213, a33\n" \
"v_mfma_f32_16x16x32_bf16 v[64:67], a[128:131], a[188:191], v[64:67]\n" \
"v_accvgpr_read_b32 v214, a34\n" \
"v_accvgpr_read_b32 v215, a35\n" \
"v_mfma_f32_16x16x32_bf16 v[64:67], a[192:195], a[252:255], v[64:67]\n" \
"ds_read_b128 a[128:131], v28 offset:0\n" \
"v_accvgpr_read_b32 v180, a48\n" \
"v_mfma_f32_16x16x32_bf16 v[80:83], a[132:135], a[188:191], v[80:83]\n" \
"ds_read_b128 a[192:195], v29 offset:0\n" \
"v_accvgpr_read_b32 v181, a49\n" \
"v_mfma_f32_16x16x32_bf16 v[80:83], a[196:199], a[252:255], v[80:83]\n" \
"v_accvgpr_read_b32 v182, a50\n" \
"v_accvgpr_read_b32 v183, a51\n" \
"v_mfma_f32_16x16x32_bf16 v[76:79], a[132:135], a[184:187], v[76:79]\n" \
"v_cvt_pk_bf16_f32 v212, v212, v213\n" \
"v_cvt_pk_bf16_f32 v213, v214, v215\n" \
"v_mfma_f32_16x16x32_bf16 v[76:79], a[196:199], a[248:251], v[76:79]\n" \
"v_cvt_pk_bf16_f32 v214, v180, v181\n" \
"v_cvt_pk_bf16_f32 v215, v182, v183\n" \
"v_mfma_f32_16x16x32_bf16 v[72:75], a[132:135], a[180:183], v[72:75]\n" \
"v_accvgpr_read_b32 v216, a68\n" \
"v_accvgpr_read_b32 v217, a69\n" \
"v_mfma_f32_16x16x32_bf16 v[72:75], a[196:199], a[244:247], v[72:75]\n" \
"v_accvgpr_read_b32 v218, a70\n" \
"v_accvgpr_read_b32 v219, a71\n" \
"v_mfma_f32_16x16x32_bf16 v[68:71], a[132:135], a[176:179], v[68:71]\n" \
"v_accvgpr_read_b32 v180, a84\n" \
"v_accvgpr_read_b32 v181, a85\n" \
"v_mfma_f32_16x16x32_bf16 v[68:71], a[196:199], a[240:243], v[68:71]\n" \
"ds_read_b128 a[132:135], v28 offset:2048\n" \
"ds_read_b128 a[196:199], v29 offset:2048\n" \
"s_waitcnt lgkmcnt(15)\n" \
"v_mfma_f32_16x16x32_bf16 a[0:3], a[160:163], a[144:147], 0\n" \
"s_addk_i32 s84, 0x2000\n" \
"v_accvgpr_read_b32 v182, a86\n" \
"v_mfma_f32_16x16x32_bf16 a[0:3], a[224:227], a[208:211], a[0:3]\n" \
"buffer_load_dwordx4 v24, s[76:79], s84 offen offset:2048 lds\n" \
"v_accvgpr_read_b32 v183, a87\n" \
"v_mfma_f32_16x16x32_bf16 a[4:7], a[164:167], a[144:147], 0\n" \
"ds_read_b64_tr_b16 a[176:177], v36 offset:0\n" \
"v_cvt_pk_bf16_f32 v216, v216, v217\n" \
"v_mfma_f32_16x16x32_bf16 a[4:7], a[228:231], a[208:211], a[4:7]\n" \
"s_addk_i32 s84, 0x2000\n" \
"v_cvt_pk_bf16_f32 v217, v218, v219\n" \
"s_waitcnt lgkmcnt(13)\n" \
"v_mfma_f32_16x16x32_bf16 a[12:15], a[172:175], a[144:147], 0\n" \
"buffer_load_dwordx4 v25, s[76:79], s84 offen offset:3072 lds\n" \
"v_cvt_pk_bf16_f32 v218, v180, v181\n" \
"s_waitcnt lgkmcnt(9)\n" \
"v_mfma_f32_16x16x32_bf16 a[12:15], a[236:239], a[208:211], a[12:15]\n" \
"ds_read_b64_tr_b16 a[178:179], v36 offset:1024\n" \
"v_cvt_pk_bf16_f32 v219, v182, v183\n" \
"s_waitcnt lgkmcnt(8)\n" \
"v_mfma_f32_16x16x32_bf16 a[8:11], a[144:147], a[168:171], 0\n" \
"ds_read_b64_tr_b16 a[240:241], v36 offset:8192\n" \
"v_accvgpr_read_b32 v220, a64\n" \
"s_waitcnt lgkmcnt(7)\n" \
"s_nop 0\n" \
"v_mfma_f32_16x16x32_bf16 a[8:11], a[208:211], a[232:235], a[8:11]\n" \
"ds_read_b64_tr_b16 a[242:243], v36 offset:9216\n" \
"v_accvgpr_read_b32 v221, a65\n" \
"v_mfma_f32_16x16x32_bf16 a[24:27], a[148:151], a[168:171], 0\n" \
"ds_read_b64_tr_b16 a[180:181], v37 offset:0\n" \
"v_accvgpr_read_b32 v222, a66\n" \
"v_mfma_f32_16x16x32_bf16 a[24:27], a[212:215], a[232:235], a[24:27]\n" \
"ds_read_b64_tr_b16 a[182:183], v37 offset:1024\n" \
"v_accvgpr_read_b32 v223, a67\n" \
"v_mfma_f32_16x16x32_bf16 a[28:31], a[172:175], a[148:151], 0\n" \
"ds_read_b64_tr_b16 a[244:245], v37 offset:8192\n" \
"v_accvgpr_read_b32 v180, a80\n" \
"v_mfma_f32_16x16x32_bf16 a[28:31], a[236:239], a[212:215], a[28:31]\n" \
"s_add_u32 s89, s41, 0x2000\n" \
"buffer_store_dwordx4 v[200:203], v20, s[80:83], s89 offen offset:3072 sc1\n" \
"v_mfma_f32_16x16x32_bf16 a[20:23], a[164:167], a[148:151], 0\n" \
"ds_read_b64_tr_b16 a[246:247], v37 offset:9216\n" \
"v_accvgpr_read_b32 v181, a81\n" \
"v_mfma_f32_16x16x32_bf16 a[20:23], a[228:231], a[212:215], a[20:23]\n" \
"ds_read_b64_tr_b16 a[184:185], v38 offset:0\n" \
"v_accvgpr_read_b32 v182, a82\n" \
"v_mfma_f32_16x16x32_bf16 a[16:19], a[160:163], a[148:151], 0\n" \
"ds_read_b64_tr_b16 a[186:187], v38 offset:1024\n" \
"v_accvgpr_read_b32 v183, a83\n" \
"v_mfma_f32_16x16x32_bf16 a[16:19], a[224:227], a[212:215], a[16:19]\n" \
"v_cvt_pk_bf16_f32 v220, v220, v221\n" \
"v_cvt_pk_bf16_f32 v221, v222, v223\n" \
"v_cvt_pk_bf16_f32 v222, v180, v181\n" \
"v_mfma_f32_16x16x32_bf16 a[32:35], a[160:163], a[152:155], 0\n" \
"s_add_u32 s88, s41, 0x2000\n" \
"buffer_store_dwordx4 v[204:207], v20, s[80:83], s88 offen offset:2048 sc1\n" \
"v_mfma_f32_16x16x32_bf16 a[32:35], a[224:227], a[216:219], a[32:35]\n" \
"ds_read_b64_tr_b16 a[248:249], v38 offset:8192\n" \
".fill (L_pb + 61152 - .) / 4, 4, 0xBF800000\n" \
"L_pp15:\n" \
"s_cbranch_execz L_pret\n" \
"s_nop 0\n" \
"v_cvt_pk_bf16_f32 v223, v182, v183\n" \
"v_mfma_f32_16x16x32_bf16 a[36:39], a[164:167], a[152:155], 0\n" \
"ds_read_b64_tr_b16 a[250:251], v38 offset:9216\n" \
"v_cvt_pk_bf16_f32 v224, v128, v129\n" \
"v_mfma_f32_16x16x32_bf16 a[36:39], a[228:231], a[216:219], a[36:39]\n" \
"ds_read_b64_tr_b16 a[188:189], v39 offset:0\n" \
"v_cvt_pk_bf16_f32 v225, v130, v131\n" \
"v_mfma_f32_16x16x32_bf16 a[44:47], a[172:175], a[152:155], 0\n" \
"v_cvt_pk_bf16_f32 v226, v144, v145\n" \
"v_cvt_pk_bf16_f32 v227, v146, v147\n" \
"v_cvt_pk_bf16_f32 v228, v124, v125\n" \
"v_mfma_f32_16x16x32_bf16 a[44:47], a[236:239], a[216:219], a[44:47]\n" \
"ds_read_b64_tr_b16 a[190:191], v39 offset:1024\n" \
"v_cvt_pk_bf16_f32 v229, v126, v127\n" \
"v_mfma_f32_16x16x32_bf16 a[40:43], a[152:155], a[168:171], 0\n" \
"s_add_u32 s89, s41, 0x2000\n" \
"buffer_store_dwordx4 v[208:211], v20, s[80:83], s89 offen offset:1024 sc1\n" \
"v_mfma_f32_16x16x32_bf16 a[40:43], a[216:219], a[232:235], a[40:43]\n" \
"ds_read_b64_tr_b16 a[252:253], v39 offset:8192\n" \
"v_cvt_pk_bf16_f32 v230, v140, v141\n" \
"v_mfma_f32_16x16x32_bf16 a[56:59], a[156:159], a[168:171], 0\n" \
"ds_read_b64_tr_b16 a[254:255], v39 offset:9216\n" \
"v_cvt_pk_bf16_f32 v231, v142, v143\n" \
"v_mfma_f32_16x16x32_bf16 a[56:59], a[220:223], a[232:235], a[56:59]\n" \
"v_cvt_pk_bf16_f32 v232, v120, v121\n" \
"v_cvt_pk_bf16_f32 v233, v122, v123\n" \
"v_cvt_pk_bf16_f32 v234, v136, v137\n" \
"v_mfma_f32_16x16x32_bf16 a[60:63], a[172:175], a[156:159], 0\n" \
"v_cvt_pk_bf16_f32 v235, v138, v139\n" \
"v_cvt_pk_bf16_f32 v236, v116, v117\n" \
"v_cvt_pk_bf16_f32 v237, v118, v119\n" \
"v_mfma_f32_16x16x32_bf16 a[60:63], a[236:239], a[220:223], a[60:63]\n" \
"s_add_u32 s88, s41, 0x2000\n" \
"buffer_store_dwordx4 v[212:215], v20, s[80:83], s88 offen offset:0 sc1\n" \
"v_mfma_f32_16x16x32_bf16 a[52:55], a[164:167], a[156:159], 0\n" \
"v_cvt_pk_bf16_f32 v238, v132, v133\n" \
"v_cvt_pk_bf16_f32 v239, v134, v135\n" \
"v_cvt_pk_bf16_f32 v240, v160, v161\n" \
"v_mfma_f32_16x16x32_bf16 a[52:55], a[228:231], a[220:223], a[52:55]\n" \
"v_cvt_pk_bf16_f32 v241, v162, v163\n" \
"v_cvt_pk_bf16_f32 v242, v176, v177\n" \
"v_cvt_pk_bf16_f32 v243, v178, v179\n" \
"v_mfma_f32_16x16x32_bf16 a[48:51], a[160:163], a[156:159], 0\n" \
"v_cvt_pk_bf16_f32 v244, v156, v157\n" \
"v_cvt_pk_bf16_f32 v245, v158, v159\n" \
"v_cvt_pk_bf16_f32 v246, v172, v173\n" \
"v_mfma_f32_16x16x32_bf16 a[48:51], a[224:227], a[220:223], a[48:51]\n" \
"v_cvt_pk_bf16_f32 v247, v174, v175\n" \
"v_cvt_pk_bf16_f32 v184, v152, v153\n" \
"v_cvt_pk_bf16_f32 v185, v154, v155\n" \
"v_mfma_f32_16x16x32_bf16 a[64:67], a[164:167], a[140:143], 0\n" \
"v_cvt_pk_bf16_f32 v186, v168, v169\n" \
"v_cvt_pk_bf16_f32 v187, v170, v171\n" \
"v_cvt_pk_bf16_f32 v188, v148, v149\n" \
"v_mfma_f32_16x16x32_bf16 a[64:67], a[228:231], a[204:207], a[64:67]\n" \
"v_cvt_pk_bf16_f32 v189, v150, v151\n" \
"v_cvt_pk_bf16_f32 v190, v164, v165\n" \
"v_cvt_pk_bf16_f32 v191, v166, v167\n" \
"v_mfma_f32_16x16x32_bf16 a[80:83], a[172:175], a[140:143], 0\n" \
"v_cvt_pk_bf16_f32 v192, v96, v97\n" \
"v_cvt_pk_bf16_f32 v193, v98, v99\n" \
"v_cvt_pk_bf16_f32 v194, v112, v113\n" \
"v_mfma_f32_16x16x32_bf16 a[80:83], a[236:239], a[204:207], a[80:83]\n" \
"v_cvt_pk_bf16_f32 v195, v114, v115\n" \
"v_cvt_pk_bf16_f32 v196, v92, v93\n" \
"v_cvt_pk_bf16_f32 v197, v94, v95\n" \
"v_mfma_f32_16x16x32_bf16 a[68:71], a[140:143], a[168:171], 0\n" \
"v_cvt_pk_bf16_f32 v198, v108, v109\n" \
"v_cvt_pk_bf16_f32 v199, v110, v111\n" \
"v_cvt_pk_bf16_f32 v252, v88, v89\n" \
"v_mfma_f32_16x16x32_bf16 a[68:71], a[204:207], a[232:235], a[68:71]\n" \
"v_cvt_pk_bf16_f32 v253, v90, v91\n" \
"v_cvt_pk_bf16_f32 v254, v104, v105\n" \
"v_cvt_pk_bf16_f32 v255, v106, v107\n" \
"s_waitcnt vmcnt(4) lgkmcnt(0)\n" \
"s_barrier\n" \
"v_mfma_f32_16x16x32_bf16 v[116:119], a[176:179], a[144:147], 0\n" \
"s_add_u32 m0, s40, 0x0\n" \
"s_nop 0\n" \
"s_add_u32 s85, s39, 0x4000\n" \
"v_accvgpr_write_b32 a84, v252\n" \
"v_mfma_f32_16x16x32_bf16 v[116:119], a[240:243], a[208:211], v[116:119]\n" \
"buffer_load_dwordx4 v20, s[80:83], s85 offen sc1 lds\n" \
"v_accvgpr_write_b32 a85, v253\n" \
"v_mfma_f32_16x16x32_bf16 v[120:123], a[180:183], a[144:147], 0\n" \
"ds_read_b64_tr_b16 a[160:161], v32 offset:32768\n" \
"v_accvgpr_write_b32 a86, v254\n" \
"v_mfma_f32_16x16x32_bf16 v[120:123], a[244:247], a[208:211], v[120:123]\n" \
"ds_read_b64_tr_b16 a[162:163], v32 offset:33280\n" \
"v_accvgpr_write_b32 a87, v255\n" \
"v_mfma_f32_16x16x32_bf16 v[124:127], a[184:187], a[144:147], 0\n" \
"s_add_u32 m0, s40, 0x1000\n" \
"s_add_u32 s85, s39, 0x5000\n" \
"v_cvt_pk_bf16_f32 v252, v84, v85\n" \
"v_mfma_f32_16x16x32_bf16 v[124:127], a[248:251], a[208:211], v[124:127]\n" \
"buffer_load_dwordx4 v20, s[80:83], s85 offen sc1 lds\n" \
"v_cvt_pk_bf16_f32 v253, v86, v87\n" \
"v_mfma_f32_16x16x32_bf16 v[128:131], a[188:191], a[144:147], 0\n" \
"ds_read_b64_tr_b16 a[224:225], v32 offset:36864\n" \
"v_cvt_pk_bf16_f32 v254, v100, v101\n" \
"v_mfma_f32_16x16x32_bf16 v[128:131], a[252:255], a[208:211], v[128:131]\n" \
"ds_read_b128 a[144:147], v28 offset:40960\n" \
"v_cvt_pk_bf16_f32 v255, v102, v103\n" \
"v_mfma_f32_16x16x32_bf16 v[144:147], a[188:191], a[148:151], 0\n" \
"ds_read_b128 a[208:211], v29 offset:40960\n" \
"v_cvt_pk_bf16_f32 v200, v64, v65\n" \
"v_mfma_f32_16x16x32_bf16 v[144:147], a[252:255], a[212:215], v[144:147]\n" \
"s_add_u32 m0, s40, 0x3000\n" \
"s_movk_i32 s84, 0x100\n" \
"s_nop 0\n" \
"v_cvt_pk_bf16_f32 v201, v66, v67\n" \
"v_mfma_f32_16x16x32_bf16 v[140:143], a[184:187], a[148:151], 0\n" \
"buffer_load_dwordx4 v21, s[76:79], s84 offen offset:0 lds\n" \
"v_cvt_pk_bf16_f32 v202, v80, v81\n" \
"v_mfma_f32_16x16x32_bf16 v[140:143], a[248:251], a[212:215], v[140:143]\n" \
"ds_read_b64_tr_b16 a[226:227], v32 offset:37376\n" \
"v_cvt_pk_bf16_f32 v203, v82, v83\n" \
"v_mfma_f32_16x16x32_bf16 v[136:139], a[180:183], a[148:151], 0\n" \
"v_cvt_pk_bf16_f32 v204, v60, v61\n" \
"v_cvt_pk_bf16_f32 v205, v62, v63\n" \
"v_cvt_pk_bf16_f32 v206, v76, v77\n" \
"v_mfma_f32_16x16x32_bf16 v[136:139], a[244:247], a[212:215], v[136:139]\n" \
"ds_read_b64_tr_b16 a[164:165], v33 offset:32768\n" \
"v_cvt_pk_bf16_f32 v207, v78, v79\n" \
"v_mfma_f32_16x16x32_bf16 v[132:135], a[176:179], a[148:151], 0\n" \
"v_cvt_pk_bf16_f32 v208, v56, v57\n" \
"v_cvt_pk_bf16_f32 v209, v58, v59\n" \
"v_cvt_pk_bf16_f32 v210, v72, v73\n" \
"v_mfma_f32_16x16x32_bf16 v[132:135], a[240:243], a[212:215], v[132:135]\n" \
"ds_read_b128 a[148:151], v28 offset:43008\n" \
"v_cvt_pk_bf16_f32 v211, v74, v75\n" \
"v_mfma_f32_16x16x32_bf16 v[148:151], a[176:179], a[152:155], 0\n" \
"ds_read_b128 a[212:215], v29 offset:43008\n" \
"v_cvt_pk_bf16_f32 v212, v52, v53\n" \
"v_mfma_f32_16x16x32_bf16 v[148:151], a[240:243], a[216:219], v[148:151]\n" \
"s_add_u32 m0, s31, 0x0\n" \
"s_mov_b32 s84, 0x100000\n" \
"v_cvt_pk_bf16_f32 v213, v54, v55\n" \
"v_mfma_f32_16x16x32_bf16 v[152:155], a[180:183], a[152:155], 0\n" \
"buffer_load_dwordx4 v26, s[68:71], s84 offen offset:0 lds\n" \
"v_cvt_pk_bf16_f32 v214, v68, v69\n" \
"v_mfma_f32_16x16x32_bf16 v[152:155], a[244:247], a[216:219], v[152:155]\n" \
"s_addk_i32 s84, 0x7c00\n" \
"v_cvt_pk_bf16_f32 v215, v70, v71\n" \
"v_mfma_f32_16x16x32_bf16 v[156:159], a[184:187], a[152:155], 0\n" \
"buffer_load_dwordx4 v26, s[68:71], s84 offen offset:1024 lds\n" \
"v_mfma_f32_16x16x32_bf16 v[156:159], a[248:251], a[216:219], v[156:159]\n" \
"ds_read_b64_tr_b16 a[166:167], v33 offset:33280\n" \
"v_mfma_f32_16x16x32_bf16 v[160:163], a[188:191], a[152:155], 0\n" \
"ds_read_b64_tr_b16 a[228:229], v33 offset:36864\n" \
"v_mfma_f32_16x16x32_bf16 v[160:163], a[252:255], a[216:219], v[160:163]\n" \
"ds_read_b128 a[152:155], v28 offset:45056\n" \
"v_mfma_f32_16x16x32_bf16 v[176:179], a[188:191], a[156:159], 0\n" \
"ds_read_b128 a[216:219], v29 offset:45056\n" \
"v_mfma_f32_16x16x32_bf16 v[176:179], a[252:255], a[220:223], v[176:179]\n" \
"s_addk_i32 s84, 0x7c00\n" \
"v_mfma_f32_16x16x32_bf16 v[172:175], a[184:187], a[156:159], 0\n" \
"buffer_load_dwordx4 v27, s[68:71], s84 offen offset:2048 lds\n" \
"v_mfma_f32_16x16x32_bf16 v[172:175], a[248:251], a[220:223], v[172:175]\n" \
"ds_read_b64_tr_b16 a[230:231], v33 offset:37376\n" \
"v_mfma_f32_16x16x32_bf16 v[168:171], a[180:183], a[156:159], 0\n" \
"s_addk_i32 s84, 0x7c00\n" \
"v_mfma_f32_16x16x32_bf16 v[168:171], a[244:247], a[220:223], v[168:171]\n" \
"buffer_load_dwordx4 v27, s[68:71], s84 offen offset:3072 lds\n" \
"v_mfma_f32_16x16x32_bf16 v[164:167], a[176:179], a[156:159], 0\n" \
"ds_read_b64_tr_b16 a[172:173], v35 offset:32768\n" \
"v_mfma_f32_16x16x32_bf16 v[164:167], a[240:243], a[220:223], v[164:167]\n" \
"ds_read_b128 a[156:159], v28 offset:47104\n" \
"v_mfma_f32_16x16x32_bf16 v[84:87], a[176:179], a[136:139], 0\n" \
"ds_read_b128 a[220:223], v29 offset:47104\n" \
"v_mfma_f32_16x16x32_bf16 v[84:87], a[240:243], a[200:203], v[84:87]\n" \
"s_add_u32 s89, s41, 0x3000\n" \
"buffer_store_dwordx4 v[192:195], v20, s[80:83], s89 offen offset:3072 sc1\n" \
"v_mfma_f32_16x16x32_bf16 v[88:91], a[180:183], a[136:139], 0\n" \
"ds_read_b64_tr_b16 a[174:175], v35 offset:33280\n" \
"v_mfma_f32_16x16x32_bf16 v[88:91], a[244:247], a[200:203], v[88:91]\n" \
"ds_read_b64_tr_b16 a[236:237], v35 offset:36864\n" \
"v_mfma_f32_16x16x32_bf16 v[92:95], a[184:187], a[136:139], 0\n" \
"ds_read_b64_tr_b16 a[238:239], v35 offset:37376\n" \
"v_mfma_f32_16x16x32_bf16 v[92:95], a[248:251], a[200:203], v[92:95]\n" \
"s_add_u32 m0, s30, 0x0\n" \
"s_movk_i32 s84, 0x180\n" \
"v_mfma_f32_16x16x32_bf16 v[96:99], a[188:191], a[136:139], 0\n" \
"buffer_load_dwordx4 v24, s[76:79], s84 offen offset:0 lds\n" \
"v_mfma_f32_16x16x32_bf16 v[96:99], a[252:255], a[200:203], v[96:99]\n" \
"ds_read_b128 a[136:139], v28 offset:36864\n" \
"v_mfma_f32_16x16x32_bf16 v[112:115], a[188:191], a[140:143], 0\n" \
"ds_read_b128 a[200:203], v29 offset:36864\n" \
"v_mfma_f32_16x16x32_bf16 v[112:115], a[252:255], a[204:207], v[112:115]\n" \
"ds_read_b64_tr_b16 a[168:169], v34 offset:32768\n" \
"v_mfma_f32_16x16x32_bf16 v[108:111], a[184:187], a[140:143], 0\n" \
"s_movk_i32 s84, 0x2180\n" \
"s_nop 0\n" \
"v_mfma_f32_16x16x32_bf16 v[108:111], a[248:251], a[204:207], v[108:111]\n" \
"buffer_load_dwordx4 v25, s[76:79], s84 offen offset:1024 lds\n" \
"v_mfma_f32_16x16x32_bf16 v[104:107], a[180:183], a[140:143], 0\n" \
"ds_read_b64_tr_b16 a[170:171], v34 offset:33280\n" \
"v_mfma_f32_16x16x32_bf16 v[104:107], a[244:247], a[204:207], v[104:107]\n" \
"ds_read_b64_tr_b16 a[232:233], v34 offset:36864\n" \
"v_mfma_f32_16x16x32_bf16 v[100:103], a[176:179], a[140:143], 0\n" \
"v_mfma_f32_16x16x32_bf16 v[100:103], a[240:243], a[204:207], v[100:103]\n" \
"ds_read_b128 a[140:143], v28 offset:38912\n" \
"v_mfma_f32_16x16x32_bf16 v[52:55], a[176:179], a[128:131], 0\n" \
"ds_read_b128 a[204:207], v29 offset:38912\n" \
"v_mfma_f32_16x16x32_bf16 v[52:55], a[240:243], a[192:195], v[52:55]\n" \
"s_movk_i32 s84, 0x4180\n" \
"v_mfma_f32_16x16x32_bf16 v[56:59], a[180:183], a[128:131], 0\n" \
"buffer_load_dwordx4 v24, s[76:79], s84 offen offset:2048 lds\n" \
"v_mfma_f32_16x16x32_bf16 v[56:59], a[244:247], a[192:195], v[56:59]\n" \
"s_movk_i32 s84, 0x6180\n" \
"v_mfma_f32_16x16x32_bf16 v[60:63], a[184:187], a[128:131], 0\n" \
"buffer_load_dwordx4 v25, s[76:79], s84 offen offset:3072 lds\n" \
"v_mfma_f32_16x16x32_bf16 v[60:63], a[248:251], a[192:195], v[60:63]\n" \
"ds_read_b64_tr_b16 a[234:235], v34 offset:37376\n" \
"v_mfma_f32_16x16x32_bf16 v[64:67], a[188:191], a[128:131], 0\n" \
"v_mfma_f32_16x16x32_bf16 v[64:67], a[252:255], a[192:195], v[64:67]\n" \
"ds_read_b128 a[128:131], v28 offset:32768\n" \
"v_mfma_f32_16x16x32_bf16 v[80:83], a[188:191], a[132:135], 0\n" \
"ds_read_b128 a[192:195], v29 offset:32768\n" \
"v_mfma_f32_16x16x32_bf16 v[80:83], a[252:255], a[196:199], v[80:83]\n" \
"v_mfma_f32_16x16x32_bf16 v[76:79], a[184:187], a[132:135], 0\n" \
"v_mfma_f32_16x16x32_bf16 v[76:79], a[248:251], a[196:199], v[76:79]\n" \
"v_mfma_f32_16x16x32_bf16 v[72:75], a[180:183], a[132:135], 0\n" \
"v_mfma_f32_16x16x32_bf16 v[72:75], a[244:247], a[196:199], v[72:75]\n" \
"v_mfma_f32_16x16x32_bf16 v[68:71], a[176:179], a[132:135], 0\n" \
"v_mfma_f32_16x16x32_bf16 v[68:71], a[240:243], a[196:199], v[68:71]\n" \
"ds_read_b128 a[132:135], v28 offset:34816\n" \
"ds_read_b128 a[196:199], v29 offset:34816\n" \
"s_waitcnt lgkmcnt(15)\n" \
"v_mfma_f32_16x16x32_bf16 a[0:3], a[160:163], a[144:147], a[0:3]\n" \
"s_add_u32 m0, s30, 0x1000\n" \
"s_addk_i32 s84, 0x3000\n" \
"v_mfma_f32_16x16x32_bf16 a[0:3], a[224:227], a[208:211], a[0:3]\n" \
"buffer_load_dwordx4 v24, s[76:79], s84 offen offset:0 lds\n" \
"v_mfma_f32_16x16x32_bf16 a[4:7], a[164:167], a[144:147], a[4:7]\n" \
"ds_read_b64_tr_b16 a[176:177], v36 offset:32768\n" \
"v_mov_b32_e64 v11, 0\n" \
"v_mfma_f32_16x16x32_bf16 a[4:7], a[228:231], a[208:211], a[4:7]\n" \
"ds_read_b64_tr_b16 a[178:179], v36 offset:33792\n" \
"s_and_b32 s6, s2, 7\n" \
"s_waitcnt lgkmcnt(15)\n" \
"v_mfma_f32_16x16x32_bf16 a[12:15], a[172:175], a[144:147], a[12:15]\n" \
"ds_read_b64_tr_b16 a[240:241], v36 offset:40960\n" \
"s_lshl_b32 s7, s3, 1\n" \
"s_waitcnt lgkmcnt(15)\n" \
"v_mfma_f32_16x16x32_bf16 a[12:15], a[236:239], a[208:211], a[12:15]\n" \
"s_add_u32 s88, s41, 0x3000\n" \
"buffer_store_dwordx4 v[196:199], v20, s[80:83], s88 offen offset:2048 sc1\n" \
"s_waitcnt lgkmcnt(11)\n" \
"v_mfma_f32_16x16x32_bf16 a[8:11], a[144:147], a[168:171], a[8:11]\n" \
"ds_read_b64_tr_b16 a[242:243], v36 offset:41984\n" \
"s_lshr_b32 s4, s2, 5\n" \
"s_waitcnt lgkmcnt(8)\n" \
"v_mfma_f32_16x16x32_bf16 a[8:11], a[208:211], a[232:235], a[8:11]\n" \
"ds_read_b64_tr_b16 a[180:181], v37 offset:32768\n" \
"s_lshl_b32 s5, s6, 2\n" \
"v_mfma_f32_16x16x32_bf16 a[24:27], a[148:151], a[168:171], a[24:27]\n" \
"ds_read_b64_tr_b16 a[182:183], v37 offset:33792\n" \
"s_bfe_u32 s8, s2, 0x20003\n" \
"v_mfma_f32_16x16x32_bf16 a[24:27], a[212:215], a[232:235], a[24:27]\n" \
"ds_read_b64_tr_b16 a[244:245], v37 offset:40960\n" \
"s_and_b32 s7, s7, 4\n" \
"v_mfma_f32_16x16x32_bf16 a[28:31], a[172:175], a[148:151], a[28:31]\n" \
"s_and_b32 s9, s3, 1\n" \
"s_or_b32 s13, s5, s8\n" \
"v_mfma_f32_16x16x32_bf16 a[28:31], a[236:239], a[212:215], a[28:31]\n" \
"s_addk_i32 s84, 0x2000\n" \
"v_mfma_f32_16x16x32_bf16 a[20:23], a[164:167], a[148:151], a[20:23]\n" \
"buffer_load_dwordx4 v25, s[76:79], s84 offen offset:1024 lds\n" \
"v_mfma_f32_16x16x32_bf16 a[20:23], a[228:231], a[212:215], a[20:23]\n" \
"ds_read_b64_tr_b16 a[246:247], v37 offset:41984\n" \
"s_or_b32 s9, s7, s9\n" \
"v_mfma_f32_16x16x32_bf16 a[16:19], a[160:163], a[148:151], a[16:19]\n" \
"ds_read_b64_tr_b16 a[184:185], v38 offset:32768\n" \
"s_mul_i32 s7, s4, 0xa00\n" \
"v_mfma_f32_16x16x32_bf16 a[16:19], a[224:227], a[212:215], a[16:19]\n" \
"ds_read_b64_tr_b16 a[186:187], v38 offset:33792\n" \
"s_add_i32 s7, s7, 0x5bc1000\n" \
"v_mfma_f32_16x16x32_bf16 a[32:35], a[160:163], a[152:155], a[32:35]\n" \
"s_addk_i32 s84, 0x2000\n" \
"v_mfma_f32_16x16x32_bf16 a[32:35], a[224:227], a[216:219], a[32:35]\n" \
"buffer_load_dwordx4 v24, s[76:79], s84 offen offset:2048 lds\n" \
"v_mfma_f32_16x16x32_bf16 a[36:39], a[164:167], a[152:155], a[36:39]\n" \
"ds_read_b64_tr_b16 a[248:249], v38 offset:40960\n" \
"s_lshl_b32 s10, s13, 2\n" \
"v_mfma_f32_16x16x32_bf16 a[36:39], a[228:231], a[216:219], a[36:39]\n" \
"ds_read_b64_tr_b16 a[250:251], v38 offset:41984\n" \
"s_or_b32 s10, s10, s7\n" \
"v_mfma_f32_16x16x32_bf16 a[44:47], a[172:175], a[152:155], a[44:47]\n" \
"ds_read_b64_tr_b16 a[188:189], v39 offset:32768\n" \
"v_writelane_b32 v11, s10, 0\n" \
"v_mfma_f32_16x16x32_bf16 a[44:47], a[236:239], a[216:219], a[44:47]\n" \
"ds_read_b64_tr_b16 a[190:191], v39 offset:33792\n" \
"s_lshl_b32 s14, s4, 3\n" \
"v_mfma_f32_16x16x32_bf16 a[40:43], a[152:155], a[168:171], a[40:43]\n" \
"s_add_u32 s89, s41, 0x3000\n" \
"buffer_store_dwordx4 a[84:87], v20, s[80:83], s89 offen offset:1024 sc1\n" \
"v_mfma_f32_16x16x32_bf16 a[40:43], a[216:219], a[232:235], a[40:43]\n" \
"ds_read_b64_tr_b16 a[252:253], v39 offset:40960\n" \
"v_writelane_b32 v11, s7, 1\n" \
"v_mfma_f32_16x16x32_bf16 a[56:59], a[156:159], a[168:171], a[56:59]\n" \
"ds_read_b64_tr_b16 a[254:255], v39 offset:41984\n" \
"s_or_b32 s7, s14, s6\n" \
"v_mfma_f32_16x16x32_bf16 a[56:59], a[220:223], a[232:235], a[56:59]\n" \
"s_lshl_b32 s7, s7, 10\n" \
"s_lshl_b32 s10, s3, 8\n" \
"v_mfma_f32_16x16x32_bf16 a[60:63], a[172:175], a[156:159], a[60:63]\n" \
"s_or_b32 s7, s7, s10\n" \
"s_or_b32 s10, s7, 0x5c00200\n" \
"v_mfma_f32_16x16x32_bf16 a[60:63], a[236:239], a[220:223], a[60:63]\n" \
"s_addk_i32 s84, 0x2000\n" \
"v_mfma_f32_16x16x32_bf16 a[52:55], a[164:167], a[156:159], a[52:55]\n" \
"buffer_load_dwordx4 v25, s[76:79], s84 offen offset:3072 lds\n" \
"v_mfma_f32_16x16x32_bf16 a[52:55], a[228:231], a[220:223], a[52:55]\n" \
"v_writelane_b32 v11, s10, 2\n" \
"s_lshl_b32 s10, s8, 2\n" \
"v_mfma_f32_16x16x32_bf16 a[48:51], a[160:163], a[156:159], a[48:51]\n" \
"s_or_b32 s11, s10, s7\n" \
"s_or_b32 s11, s11, 0x5c00000\n" \
"v_mfma_f32_16x16x32_bf16 a[48:51], a[224:227], a[220:223], a[48:51]\n" \
"v_writelane_b32 v11, s11, 3\n" \
"s_or_b32 s7, s7, 0x5c00080\n" \
"v_mfma_f32_16x16x32_bf16 a[64:67], a[164:167], a[140:143], a[64:67]\n" \
"v_writelane_b32 v11, s7, 4\n" \
"s_or_b32 s7, s7, s10\n" \
"v_mfma_f32_16x16x32_bf16 a[64:67], a[228:231], a[204:207], a[64:67]\n" \
"s_add_i32 s10, s3, s2\n" \
"v_writelane_b32 v11, s7, 5\n" \
"v_mfma_f32_16x16x32_bf16 a[80:83], a[172:175], a[140:143], a[80:83]\n" \
"s_mov_b32 s7, 2.0\n" \
"s_mov_b32 s11, 0x5bc0000\n" \
"v_mfma_f32_16x16x32_bf16 a[80:83], a[236:239], a[204:207], a[80:83]\n" \
"s_cmp_lg_u32 s10, 0\n" \
"s_cselect_b32 s10, s7, s11\n" \
"v_mfma_f32_16x16x32_bf16 a[68:71], a[140:143], a[168:171], a[68:71]\n" \
"s_lshr_b32 s15, s2, 3\n" \
"v_writelane_b32 v11, s10, 54\n" \
"v_mfma_f32_16x16x32_bf16 a[68:71], a[204:207], a[232:235], a[68:71]\n" \
"s_waitcnt vmcnt(11) lgkmcnt(0)\n" \
"s_barrier\n" \
"v_xor_b32_e32 v28, 0x20000, v28\n" \
"v_xor_b32_e32 v29, 0x20000, v29\n" \
"v_mfma_f32_16x16x32_bf16 v[116:119], a[176:179], a[144:147], v[116:119]\n" \
"s_add_u32 m0, s40, 0x8000\n" \
"s_add_u32 s85, s39, 0x6000\n" \
"v_mfma_f32_16x16x32_bf16 v[116:119], a[240:243], a[208:211], v[116:119]\n" \
"buffer_load_dwordx4 v20, s[80:83], s85 offen sc1 lds\n" \
"v_mfma_f32_16x16x32_bf16 v[120:123], a[180:183], a[144:147], v[120:123]\n" \
"ds_read_b64_tr_b16 a[160:161], v32 offset:0\n" \
"s_lshl_b32 s10, s6, 5\n" \
"v_mfma_f32_16x16x32_bf16 v[120:123], a[244:247], a[208:211], v[120:123]\n" \
"ds_read_b64_tr_b16 a[162:163], v32 offset:512\n" \
"s_min_u32 s16, s3, 2\n" \
"v_mfma_f32_16x16x32_bf16 v[124:127], a[184:187], a[144:147], v[124:127]\n" \
"s_add_u32 m0, s40, 0x9000\n" \
"s_add_u32 s85, s39, 0x7000\n" \
"v_mfma_f32_16x16x32_bf16 v[124:127], a[248:251], a[208:211], v[124:127]\n" \
"buffer_load_dwordx4 v20, s[80:83], s85 offen sc1 lds\n" \
"v_mfma_f32_16x16x32_bf16 v[128:131], a[188:191], a[144:147], v[128:131]\n" \
"ds_read_b64_tr_b16 a[224:225], v32 offset:4096\n" \
"s_mul_i32 s12, s8, 0x120\n" \
"v_mfma_f32_16x16x32_bf16 v[128:131], a[252:255], a[208:211], v[128:131]\n" \
"ds_read_b128 a[144:147], v28 offset:8192\n" \
"v_mfma_f32_16x16x32_bf16 v[144:147], a[188:191], a[148:151], v[144:147]\n" \
"ds_read_b128 a[208:211], v29 offset:8192\n" \
"v_mfma_f32_16x16x32_bf16 v[144:147], a[252:255], a[212:215], v[144:147]\n" \
"s_add_u32 m0, s40, 0xb000\n" \
"s_movk_i32 s84, 0x180\n" \
"v_mfma_f32_16x16x32_bf16 v[140:143], a[184:187], a[148:151], v[140:143]\n" \
"buffer_load_dwordx4 v21, s[76:79], s84 offen offset:0 lds\n" \
"v_mfma_f32_16x16x32_bf16 v[140:143], a[248:251], a[212:215], v[140:143]\n" \
"ds_read_b64_tr_b16 a[226:227], v32 offset:4608\n" \
"v_mfma_f32_16x16x32_bf16 v[136:139], a[180:183], a[148:151], v[136:139]\n" \
"s_add_u32 s88, s41, 0x3000\n" \
"buffer_store_dwordx4 v[252:255], v20, s[80:83], s88 offen offset:0 sc1\n" \
"v_mfma_f32_16x16x32_bf16 v[136:139], a[244:247], a[212:215], v[136:139]\n" \
"ds_read_b64_tr_b16 a[164:165], v33 offset:0\n" \
"v_mfma_f32_16x16x32_bf16 v[132:135], a[176:179], a[148:151], v[132:135]\n" \
"s_add_u32 s11, s22, s12\n" \
"s_addc_u32 s17, s23, 0\n" \
"s_nop 0\n" \
"v_mfma_f32_16x16x32_bf16 v[132:135], a[240:243], a[212:215], v[132:135]\n" \
"ds_read_b128 a[148:151], v28 offset:10240\n" \
"v_mfma_f32_16x16x32_bf16 v[148:151], a[176:179], a[152:155], v[148:151]\n" \
"ds_read_b128 a[212:215], v29 offset:10240\n" \
"v_mfma_f32_16x16x32_bf16 v[148:151], a[240:243], a[216:219], v[148:151]\n" \
"s_add_u32 m0, s31, 0x8000\n" \
"s_mov_b32 s84, 0x180000\n" \
"v_mfma_f32_16x16x32_bf16 v[152:155], a[180:183], a[152:155], v[152:155]\n" \
"buffer_load_dwordx4 v26, s[68:71], s84 offen offset:0 lds\n" \
"v_mfma_f32_16x16x32_bf16 v[152:155], a[244:247], a[216:219], v[152:155]\n" \
"ds_read_b64_tr_b16 a[166:167], v33 offset:512\n" \
"v_mfma_f32_16x16x32_bf16 v[156:159], a[184:187], a[152:155], v[156:159]\n" \
"ds_read_b64_tr_b16 a[228:229], v33 offset:4096\n" \
"v_mfma_f32_16x16x32_bf16 v[156:159], a[248:251], a[216:219], v[156:159]\n" \
"s_addk_i32 s84, 0x7c00\n" \
"v_mfma_f32_16x16x32_bf16 v[160:163], a[188:191], a[152:155], v[160:163]\n" \
"buffer_load_dwordx4 v26, s[68:71], s84 offen offset:1024 lds\n" \
"v_mfma_f32_16x16x32_bf16 v[160:163], a[252:255], a[216:219], v[160:163]\n" \
"ds_read_b128 a[152:155], v28 offset:12288\n" \
"v_mfma_f32_16x16x32_bf16 v[176:179], a[188:191], a[156:159], v[176:179]\n" \
"ds_read_b128 a[216:219], v29 offset:12288\n" \
"v_mfma_f32_16x16x32_bf16 v[176:179], a[252:255], a[220:223], v[176:179]\n" \
"s_addk_i32 s84, 0x7c00\n" \
"v_mfma_f32_16x16x32_bf16 v[172:175], a[184:187], a[156:159], v[172:175]\n" \
"buffer_load_dwordx4 v27, s[68:71], s84 offen offset:2048 lds\n" \
"v_mfma_f32_16x16x32_bf16 v[172:175], a[248:251], a[220:223], v[172:175]\n" \
"ds_read_b64_tr_b16 a[230:231], v33 offset:4608\n" \
"v_mfma_f32_16x16x32_bf16 v[168:171], a[180:183], a[156:159], v[168:171]\n" \
"ds_read_b64_tr_b16 a[172:173], v35 offset:0\n" \
"v_mfma_f32_16x16x32_bf16 v[168:171], a[244:247], a[220:223], v[168:171]\n" \
"ds_read_b64_tr_b16 a[174:175], v35 offset:512\n" \
"v_mfma_f32_16x16x32_bf16 v[164:167], a[176:179], a[156:159], v[164:167]\n" \
"s_add_u32 s18, s24, s12\n" \
"v_writelane_b32 v11, s11, 9\n" \
"s_addc_u32 s19, s25, 0\n" \
"v_mfma_f32_16x16x32_bf16 v[164:167], a[240:243], a[220:223], v[164:167]\n" \
"ds_read_b128 a[156:159], v28 offset:14336\n" \
"s_and_b32 s20, s17, 0xffff\n" \
"v_mfma_f32_16x16x32_bf16 v[84:87], a[176:179], a[136:139], v[84:87]\n" \
"ds_read_b128 a[220:223], v29 offset:14336\n" \
"v_writelane_b32 v11, s20, 10\n" \
"v_mfma_f32_16x16x32_bf16 v[84:87], a[240:243], a[200:203], v[84:87]\n" \
"s_addk_i32 s84, 0x7c00\n" \
"v_mfma_f32_16x16x32_bf16 v[88:91], a[180:183], a[136:139], v[88:91]\n" \
"buffer_load_dwordx4 v27, s[68:71], s84 offen offset:3072 lds\n" \
"v_mfma_f32_16x16x32_bf16 v[88:91], a[244:247], a[200:203], v[88:91]\n" \
"s_add_u32 m0, s30, 0x8000\n" \
"s_movk_i32 s84, 0x200\n" \
"v_mfma_f32_16x16x32_bf16 v[92:95], a[184:187], a[136:139], v[92:95]\n" \
"buffer_load_dwordx4 v24, s[76:79], s84 offen offset:0 lds\n" \
"v_mfma_f32_16x16x32_bf16 v[92:95], a[248:251], a[200:203], v[92:95]\n" \
"ds_read_b64_tr_b16 a[236:237], v35 offset:4096\n" \
"s_and_b32 s20, s19, 0xffff\n" \
"v_mfma_f32_16x16x32_bf16 v[96:99], a[188:191], a[136:139], v[96:99]\n" \
"ds_read_b64_tr_b16 a[238:239], v35 offset:4608\n" \
"v_writelane_b32 v11, s18, 11\n" \
"v_mfma_f32_16x16x32_bf16 v[96:99], a[252:255], a[200:203], v[96:99]\n" \
"ds_read_b128 a[136:139], v28 offset:4096\n" \
"v_mfma_f32_16x16x32_bf16 v[112:115], a[188:191], a[140:143], v[112:115]\n" \
"ds_read_b128 a[200:203], v29 offset:4096\n" \
"v_mfma_f32_16x16x32_bf16 v[112:115], a[252:255], a[204:207], v[112:115]\n" \
"s_movk_i32 s84, 0x2200\n" \
"s_nop 0\n" \
"v_mfma_f32_16x16x32_bf16 v[108:111], a[184:187], a[140:143], v[108:111]\n" \
"buffer_load_dwordx4 v25, s[76:79], s84 offen offset:1024 lds\n" \
"v_mfma_f32_16x16x32_bf16 v[108:111], a[248:251], a[204:207], v[108:111]\n" \
"s_add_u32 s89, s41, 0x1000\n" \
"buffer_store_dwordx4 v[200:203], v20, s[80:83], s89 offen offset:3072 sc1\n" \
"v_mfma_f32_16x16x32_bf16 v[104:107], a[180:183], a[140:143], v[104:107]\n" \
"ds_read_b64_tr_b16 a[168:169], v34 offset:0\n" \
"v_mfma_f32_16x16x32_bf16 v[104:107], a[244:247], a[204:207], v[104:107]\n" \
"ds_read_b64_tr_b16 a[170:171], v34 offset:512\n" \
"v_mfma_f32_16x16x32_bf16 v[100:103], a[176:179], a[140:143], v[100:103]\n" \
"ds_read_b64_tr_b16 a[232:233], v34 offset:4096\n" \
"v_mfma_f32_16x16x32_bf16 v[100:103], a[240:243], a[204:207], v[100:103]\n" \
"ds_read_b128 a[140:143], v28 offset:6144\n" \
"v_mfma_f32_16x16x32_bf16 v[52:55], a[176:179], a[128:131], v[52:55]\n" \
"ds_read_b128 a[204:207], v29 offset:6144\n" \
"v_mfma_f32_16x16x32_bf16 v[52:55], a[240:243], a[192:195], v[52:55]\n" \
"s_movk_i32 s84, 0x4200\n" \
"v_mfma_f32_16x16x32_bf16 v[56:59], a[180:183], a[128:131], v[56:59]\n" \
"buffer_load_dwordx4 v24, s[76:79], s84 offen offset:2048 lds\n" \
"v_mfma_f32_16x16x32_bf16 v[56:59], a[244:247], a[192:195], v[56:59]\n" \
"s_movk_i32 s84, 0x6200\n" \
"v_mfma_f32_16x16x32_bf16 v[60:63], a[184:187], a[128:131], v[60:63]\n" \
"buffer_load_dwordx4 v25, s[76:79], s84 offen offset:3072 lds\n" \
"v_mfma_f32_16x16x32_bf16 v[60:63], a[248:251], a[192:195], v[60:63]\n" \
"ds_read_b64_tr_b16 a[234:235], v34 offset:4608\n" \
"v_mfma_f32_16x16x32_bf16 v[64:67], a[188:191], a[128:131], v[64:67]\n" \
"s_cmpk_lt_u32 s3, 3\n" \
"s_cselect_b32 s11, s11, s18\n" \
"v_mfma_f32_16x16x32_bf16 v[64:67], a[252:255], a[192:195], v[64:67]\n" \
"ds_read_b128 a[128:131], v28 offset:0\n" \
"s_movk_i32 s18, 0xfff\n" \
"v_mfma_f32_16x16x32_bf16 v[80:83], a[188:191], a[132:135], v[80:83]\n" \
"ds_read_b128 a[192:195], v29 offset:0\n" \
"v_writelane_b32 v11, s20, 12\n" \
"v_mfma_f32_16x16x32_bf16 v[80:83], a[252:255], a[196:199], v[80:83]\n" \
"s_and_b32 s20, s29, 0xffff\n" \
"v_writelane_b32 v11, s28, 13\n" \
"v_mfma_f32_16x16x32_bf16 v[76:79], a[184:187], a[132:135], v[76:79]\n" \
"v_writelane_b32 v11, s20, 14\n" \
"v_writelane_b32 v11, s11, 15\n" \
"v_mfma_f32_16x16x32_bf16 v[76:79], a[248:251], a[196:199], v[76:79]\n" \
"s_cmpk_lt_u32 s3, 3\n" \
"s_cselect_b32 s11, s17, s19\n" \
"v_mfma_f32_16x16x32_bf16 v[72:75], a[180:183], a[132:135], v[72:75]\n" \
"s_and_b32 s11, s11, 0xffff\n" \
"v_writelane_b32 v11, s11, 16\n" \
"v_mfma_f32_16x16x32_bf16 v[72:75], a[244:247], a[196:199], v[72:75]\n" \
"s_mul_i32 s11, s6, 0x48\n" \
"s_add_i32 s17, s11, 0x47\n" \
"v_mfma_f32_16x16x32_bf16 v[68:71], a[176:179], a[132:135], v[68:71]\n" \
"s_cmpk_lt_u32 s3, 3\n" \
"s_cselect_b32 s17, s17, s18\n" \
"v_mfma_f32_16x16x32_bf16 v[68:71], a[240:243], a[196:199], v[68:71]\n" \
"ds_read_b128 a[132:135], v28 offset:2048\n" \
"ds_read_b128 a[196:199], v29 offset:2048\n" \
"s_waitcnt lgkmcnt(15)\n" \
"v_mfma_f32_16x16x32_bf16 a[0:3], a[160:163], a[144:147], a[0:3]\n" \
"s_add_u32 m0, s30, 0x9000\n" \
"s_addk_i32 s84, 0x3000\n" \
"v_mfma_f32_16x16x32_bf16 a[0:3], a[224:227], a[208:211], a[0:3]\n" \
"buffer_load_dwordx4 v24, s[76:79], s84 offen offset:0 lds\n" \
"v_mfma_f32_16x16x32_bf16 a[4:7], a[164:167], a[144:147], a[4:7]\n" \
"ds_read_b64_tr_b16 a[176:177], v36 offset:0\n" \
"s_mulk_i32 s17, 0x480\n" \
"v_mfma_f32_16x16x32_bf16 a[4:7], a[228:231], a[208:211], a[4:7]\n" \
"ds_read_b64_tr_b16 a[178:179], v36 offset:1024\n" \
"s_addk_i32 s17, 0x120\n" \
"v_mfma_f32_16x16x32_bf16 a[12:15], a[172:175], a[144:147], a[12:15]\n" \
"ds_read_b64_tr_b16 a[240:241], v36 offset:8192\n" \
"v_writelane_b32 v11, s17, 17\n" \
"s_waitcnt lgkmcnt(15)\n" \
"v_mfma_f32_16x16x32_bf16 a[12:15], a[236:239], a[208:211], a[12:15]\n" \
"s_add_u32 s88, s41, 0x1000\n" \
"buffer_store_dwordx4 v[204:207], v20, s[80:83], s88 offen offset:2048 sc1\n" \
"s_waitcnt lgkmcnt(11)\n" \
"v_mfma_f32_16x16x32_bf16 a[8:11], a[144:147], a[168:171], a[8:11]\n" \
"ds_read_b64_tr_b16 a[242:243], v36 offset:9216\n" \
"s_lshl_b32 s17, s3, 5\n" \
"s_waitcnt lgkmcnt(8)\n" \
"v_mfma_f32_16x16x32_bf16 a[8:11], a[208:211], a[232:235], a[8:11]\n" \
"ds_read_b64_tr_b16 a[180:181], v37 offset:0\n" \
"s_add_i32 s11, s11, s17\n" \
"v_mfma_f32_16x16x32_bf16 a[24:27], a[148:151], a[168:171], a[24:27]\n" \
"ds_read_b64_tr_b16 a[182:183], v37 offset:1024\n" \
"s_mul_i32 s17, s11, 0x480\n" \
"v_mfma_f32_16x16x32_bf16 a[24:27], a[212:215], a[232:235], a[24:27]\n" \
"ds_read_b64_tr_b16 a[244:245], v37 offset:8192\n" \
"s_mul_i32 s11, s13, 0x24000\n" \
"v_mfma_f32_16x16x32_bf16 a[28:31], a[172:175], a[148:151], a[28:31]\n" \
"s_add_i32 s18, s11, 0x1b000\n" \
"v_mfma_f32_16x16x32_bf16 a[28:31], a[236:239], a[212:215], a[28:31]\n" \
"s_addk_i32 s84, 0x2000\n" \
"v_mfma_f32_16x16x32_bf16 a[20:23], a[164:167], a[148:151], a[20:23]\n" \
"buffer_load_dwordx4 v25, s[76:79], s84 offen offset:1024 lds\n" \
"v_mfma_f32_16x16x32_bf16 a[20:23], a[228:231], a[212:215], a[20:23]\n" \
"ds_read_b64_tr_b16 a[246:247], v37 offset:9216\n" \
"v_mfma_f32_16x16x32_bf16 a[16:19], a[160:163], a[148:151], a[16:19]\n" \
"ds_read_b64_tr_b16 a[184:185], v38 offset:0\n" \
"v_mfma_f32_16x16x32_bf16 a[16:19], a[224:227], a[212:215], a[16:19]\n" \
"ds_read_b64_tr_b16 a[186:187], v38 offset:1024\n" \
"v_mfma_f32_16x16x32_bf16 a[32:35], a[160:163], a[152:155], a[32:35]\n" \
"s_addk_i32 s84, 0x2000\n" \
"v_mfma_f32_16x16x32_bf16 a[32:35], a[224:227], a[216:219], a[32:35]\n" \
"buffer_load_dwordx4 v24, s[76:79], s84 offen offset:2048 lds\n" \
"v_mfma_f32_16x16x32_bf16 a[36:39], a[164:167], a[152:155], a[36:39]\n" \
"ds_read_b64_tr_b16 a[248:249], v38 offset:8192\n" \
"v_mfma_f32_16x16x32_bf16 a[36:39], a[228:231], a[216:219], a[36:39]\n" \
"ds_read_b64_tr_b16 a[250:251], v38 offset:9216\n" \
"v_mfma_f32_16x16x32_bf16 a[44:47], a[172:175], a[152:155], a[44:47]\n" \
".fill (L_pb + 65248 - .) / 4, 4, 0xBF800000\n" \
"L_pp16:\n" \
"s_cbranch_execz L_pret\n" \
"s_nop 0\n" \
"ds_read_b64_tr_b16 a[188:189], v39 offset:0\n" \
"v_mfma_f32_16x16x32_bf16 a[44:47], a[236:239], a[216:219], a[44:47]\n" \
"ds_read_b64_tr_b16 a[190:191], v39 offset:1024\n" \
"v_mfma_f32_16x16x32_bf16 a[40:43], a[152:155], a[168:171], a[40:43]\n" \
"s_add_u32 s89, s41, 0x1000\n" \
"buffer_store_dwordx4 v[208:211], v20, s[80:83], s89 offen offset:1024 sc1\n" \
"v_mfma_f32_16x16x32_bf16 a[40:43], a[216:219], a[232:235], a[40:43]\n" \
"ds_read_b64_tr_b16 a[252:253], v39 offset:8192\n" \
"v_mfma_f32_16x16x32_bf16 a[56:59], a[156:159], a[168:171], a[56:59]\n" \
"ds_read_b64_tr_b16 a[254:255], v39 offset:9216\n" \
"v_mfma_f32_16x16x32_bf16 a[56:59], a[220:223], a[232:235], a[56:59]\n" \
"s_cmpk_lt_u32 s3, 3\n" \
"s_cselect_b32 s17, s17, s18\n" \
"v_mfma_f32_16x16x32_bf16 a[60:63], a[172:175], a[156:159], a[60:63]\n" \
"s_mov_b32 s18, 0x8000\n" \
"v_writelane_b32 v11, s17, 18\n" \
"v_mfma_f32_16x16x32_bf16 a[60:63], a[236:239], a[220:223], a[60:63]\n" \
"s_addk_i32 s84, 0x2000\n" \
"v_mfma_f32_16x16x32_bf16 a[52:55], a[164:167], a[156:159], a[52:55]\n" \
"buffer_load_dwordx4 v25, s[76:79], s84 offen offset:3072 lds\n" \
"v_mfma_f32_16x16x32_bf16 a[52:55], a[228:231], a[220:223], a[52:55]\n" \
"s_mul_i32 s17, s16, 0x2400\n" \
"s_mul_i32 s16, s16, 0x9000\n" \
"v_mfma_f32_16x16x32_bf16 a[48:51], a[160:163], a[156:159], a[48:51]\n" \
"s_bitset1_b32 s17, 17\n" \
"v_writelane_b32 v11, s17, 19\n" \
"v_mfma_f32_16x16x32_bf16 a[48:51], a[224:227], a[220:223], a[48:51]\n" \
"s_add_i32 s16, s11, s16\n" \
"v_writelane_b32 v11, s16, 26\n" \
"v_mfma_f32_16x16x32_bf16 a[64:67], a[164:167], a[140:143], a[64:67]\n" \
"s_mul_i32 s16, s3, 0x2400\n" \
"s_add_i32 s17, s16, 0xa400\n" \
"v_mfma_f32_16x16x32_bf16 a[64:67], a[228:231], a[204:207], a[64:67]\n" \
"s_cmpk_lt_u32 s3, 3\n" \
"s_cselect_b32 s17, s17, s18\n" \
"v_mfma_f32_16x16x32_bf16 a[80:83], a[172:175], a[140:143], a[80:83]\n" \
"s_mov_b32 s18, 0x1b000\n" \
"s_xor_b32 s19, s3, 2\n" \
"v_mfma_f32_16x16x32_bf16 a[80:83], a[236:239], a[204:207], a[80:83]\n" \
"s_cmp_lg_u32 s19, 0\n" \
"s_cselect_b32 s18, s17, s18\n" \
"v_mfma_f32_16x16x32_bf16 a[68:71], a[140:143], a[168:171], a[68:71]\n" \
"v_writelane_b32 v11, s17, 20\n" \
"s_or_b32 s17, s16, 0x10000\n" \
"v_mfma_f32_16x16x32_bf16 a[68:71], a[204:207], a[232:235], a[68:71]\n" \
"s_waitcnt vmcnt(11) lgkmcnt(0)\n" \
"s_barrier\n" \
"v_xor_b32_e32 v28, 0x20000, v28\n" \
"v_xor_b32_e32 v29, 0x20000, v29\n" \
"v_mfma_f32_16x16x32_bf16 v[116:119], a[176:179], a[144:147], v[116:119]\n" \
"s_add_u32 m0, s40, 0x0\n" \
"s_nop 0\n" \
"s_add_u32 s85, s39, 0x8000\n" \
"v_mfma_f32_16x16x32_bf16 v[116:119], a[240:243], a[208:211], v[116:119]\n" \
"buffer_load_dwordx4 v20, s[80:83], s85 offen sc1 lds\n" \
"v_mfma_f32_16x16x32_bf16 v[120:123], a[180:183], a[144:147], v[120:123]\n" \
"ds_read_b64_tr_b16 a[160:161], v32 offset:32768\n" \
"v_writelane_b32 v11, s18, 21\n" \
"v_mfma_f32_16x16x32_bf16 v[120:123], a[244:247], a[208:211], v[120:123]\n" \
"ds_read_b64_tr_b16 a[162:163], v32 offset:33280\n" \
"s_add_i32 s18, s16, 0x19000\n" \
"v_mfma_f32_16x16x32_bf16 v[124:127], a[184:187], a[144:147], v[124:127]\n" \
"s_add_u32 m0, s40, 0x1000\n" \
"s_add_u32 s85, s39, 0x9000\n" \
"v_mfma_f32_16x16x32_bf16 v[124:127], a[248:251], a[208:211], v[124:127]\n" \
"buffer_load_dwordx4 v20, s[80:83], s85 offen sc1 lds\n" \
"v_mfma_f32_16x16x32_bf16 v[128:131], a[188:191], a[144:147], v[128:131]\n" \
"ds_read_b64_tr_b16 a[224:225], v32 offset:36864\n" \
"v_writelane_b32 v11, s17, 22\n" \
"v_mfma_f32_16x16x32_bf16 v[128:131], a[252:255], a[208:211], v[128:131]\n" \
"ds_read_b128 a[144:147], v28 offset:8192\n" \
"s_mov_b32 s17, 0\n" \
"v_mfma_f32_16x16x32_bf16 v[144:147], a[188:191], a[148:151], v[144:147]\n" \
"ds_read_b128 a[208:211], v29 offset:8192\n" \
"v_mfma_f32_16x16x32_bf16 v[144:147], a[252:255], a[212:215], v[144:147]\n" \
"s_add_u32 m0, s40, 0x3000\n" \
"s_movk_i32 s84, 0x200\n" \
"v_mfma_f32_16x16x32_bf16 v[140:143], a[184:187], a[148:151], v[140:143]\n" \
"buffer_load_dwordx4 v21, s[76:79], s84 offen offset:0 lds\n" \
"v_mfma_f32_16x16x32_bf16 v[140:143], a[248:251], a[212:215], v[140:143]\n" \
"ds_read_b64_tr_b16 a[226:227], v32 offset:37376\n" \
"v_mfma_f32_16x16x32_bf16 v[136:139], a[180:183], a[148:151], v[136:139]\n" \
"s_add_u32 s88, s41, 0x1000\n" \
"buffer_store_dwordx4 v[212:215], v20, s[80:83], s88 offen offset:0 sc1\n" \
"v_mfma_f32_16x16x32_bf16 v[136:139], a[244:247], a[212:215], v[136:139]\n" \
"ds_read_b64_tr_b16 a[164:165], v33 offset:32768\n" \
"v_mfma_f32_16x16x32_bf16 v[132:135], a[176:179], a[148:151], v[132:135]\n" \
"s_cmpk_lt_u32 s3, 3\n" \
"s_cselect_b32 s18, s18, s17\n" \
"v_mfma_f32_16x16x32_bf16 v[132:135], a[240:243], a[212:215], v[132:135]\n" \
"ds_read_b128 a[148:151], v28 offset:10240\n" \
"s_bitset1_b32 s16, 17\n" \
"v_mfma_f32_16x16x32_bf16 v[148:151], a[176:179], a[152:155], v[148:151]\n" \
"ds_read_b128 a[212:215], v29 offset:10240\n" \
"v_writelane_b32 v11, s18, 23\n" \
"v_mfma_f32_16x16x32_bf16 v[148:151], a[240:243], a[216:219], v[148:151]\n" \
"s_add_u32 m0, s31, 0x0\n" \
"s_mov_b32 s84, 0x200000\n" \
"v_mfma_f32_16x16x32_bf16 v[152:155], a[180:183], a[152:155], v[152:155]\n" \
"buffer_load_dwordx4 v26, s[68:71], s84 offen offset:0 lds\n" \
"v_mfma_f32_16x16x32_bf16 v[152:155], a[244:247], a[216:219], v[152:155]\n" \
"ds_read_b64_tr_b16 a[166:167], v33 offset:33280\n" \
"s_movk_i32 s18, 0x2400\n" \
"v_mfma_f32_16x16x32_bf16 v[156:159], a[184:187], a[152:155], v[156:159]\n" \
"ds_read_b64_tr_b16 a[228:229], v33 offset:36864\n" \
"v_mfma_f32_16x16x32_bf16 v[156:159], a[248:251], a[216:219], v[156:159]\n" \
"s_addk_i32 s84, 0x7c00\n" \
"v_mfma_f32_16x16x32_bf16 v[160:163], a[188:191], a[152:155], v[160:163]\n" \
"buffer_load_dwordx4 v26, s[68:71], s84 offen offset:1024 lds\n" \
"v_mfma_f32_16x16x32_bf16 v[160:163], a[252:255], a[216:219], v[160:163]\n" \
"ds_read_b128 a[152:155], v28 offset:12288\n" \
"v_mfma_f32_16x16x32_bf16 v[176:179], a[188:191], a[156:159], v[176:179]\n" \
"ds_read_b128 a[216:219], v29 offset:12288\n" \
"v_mfma_f32_16x16x32_bf16 v[176:179], a[252:255], a[220:223], v[176:179]\n" \
"s_addk_i32 s84, 0x7c00\n" \
"v_mfma_f32_16x16x32_bf16 v[172:175], a[184:187], a[156:159], v[172:175]\n" \
"buffer_load_dwordx4 v27, s[68:71], s84 offen offset:2048 lds\n" \
"v_mfma_f32_16x16x32_bf16 v[172:175], a[248:251], a[220:223], v[172:175]\n" \
"ds_read_b64_tr_b16 a[230:231], v33 offset:37376\n" \
"v_mfma_f32_16x16x32_bf16 v[168:171], a[180:183], a[156:159], v[168:171]\n" \
"ds_read_b64_tr_b16 a[172:173], v35 offset:32768\n" \
"v_mfma_f32_16x16x32_bf16 v[168:171], a[244:247], a[220:223], v[168:171]\n" \
"ds_read_b64_tr_b16 a[174:175], v35 offset:33280\n" \
"v_mfma_f32_16x16x32_bf16 v[164:167], a[176:179], a[156:159], v[164:167]\n" \
"s_cmpk_lt_u32 s3, 3\n" \
"s_cselect_b32 s16, s16, s18\n" \
"v_mfma_f32_16x16x32_bf16 v[164:167], a[240:243], a[220:223], v[164:167]\n" \
"ds_read_b128 a[156:159], v28 offset:14336\n" \
"s_mul_i32 s19, s3, 0x9000\n" \
"v_mfma_f32_16x16x32_bf16 v[84:87], a[176:179], a[136:139], v[84:87]\n" \
"ds_read_b128 a[220:223], v29 offset:14336\n" \
"v_writelane_b32 v11, s16, 24\n" \
"v_mfma_f32_16x16x32_bf16 v[84:87], a[240:243], a[200:203], v[84:87]\n" \
"s_addk_i32 s84, 0x7c00\n" \
"v_mfma_f32_16x16x32_bf16 v[88:91], a[180:183], a[136:139], v[88:91]\n" \
"buffer_load_dwordx4 v27, s[68:71], s84 offen offset:3072 lds\n" \
"v_mfma_f32_16x16x32_bf16 v[88:91], a[244:247], a[200:203], v[88:91]\n" \
"s_add_u32 m0, s30, 0x20000\n" \
"s_movk_i32 s84, 0x280\n" \
"v_mfma_f32_16x16x32_bf16 v[92:95], a[184:187], a[136:139], v[92:95]\n" \
"buffer_load_dwordx4 v24, s[76:79], s84 offen offset:0 lds\n" \
"v_mfma_f32_16x16x32_bf16 v[92:95], a[248:251], a[200:203], v[92:95]\n" \
"ds_read_b64_tr_b16 a[236:237], v35 offset:36864\n" \
"s_lshl_b32 s16, s3, 10\n" \
"v_mfma_f32_16x16x32_bf16 v[96:99], a[188:191], a[136:139], v[96:99]\n" \
"ds_read_b64_tr_b16 a[238:239], v35 offset:37376\n" \
"s_addk_i32 s16, 0x4800\n" \
"v_mfma_f32_16x16x32_bf16 v[96:99], a[252:255], a[200:203], v[96:99]\n" \
"ds_read_b128 a[136:139], v28 offset:4096\n" \
"v_writelane_b32 v11, s16, 25\n" \
"v_mfma_f32_16x16x32_bf16 v[112:115], a[188:191], a[140:143], v[112:115]\n" \
"ds_read_b128 a[200:203], v29 offset:4096\n" \
"s_add_i32 s16, s15, 1\n" \
"v_mfma_f32_16x16x32_bf16 v[112:115], a[252:255], a[204:207], v[112:115]\n" \
"s_movk_i32 s84, 0x2280\n" \
"v_mfma_f32_16x16x32_bf16 v[108:111], a[184:187], a[140:143], v[108:111]\n" \
"buffer_load_dwordx4 v25, s[76:79], s84 offen offset:1024 lds\n" \
"v_mfma_f32_16x16x32_bf16 v[108:111], a[248:251], a[204:207], v[108:111]\n" \
"s_add_u32 s89, s42, 0x0\n" \
"buffer_store_dwordx4 v[216:219], v20, s[80:83], s89 offen offset:1024 sc1\n" \
"v_mfma_f32_16x16x32_bf16 v[104:107], a[180:183], a[140:143], v[104:107]\n" \
"ds_read_b64_tr_b16 a[168:169], v34 offset:32768\n" \
"s_and_b32 s16, s16, 3\n" \
"v_mfma_f32_16x16x32_bf16 v[104:107], a[244:247], a[204:207], v[104:107]\n" \
"ds_read_b64_tr_b16 a[170:171], v34 offset:33280\n" \
"s_or_b32 s16, s16, s5\n" \
"v_mfma_f32_16x16x32_bf16 v[100:103], a[176:179], a[140:143], v[100:103]\n" \
"ds_read_b64_tr_b16 a[232:233], v34 offset:36864\n" \
"s_mul_i32 s18, s16, 0x24000\n" \
"v_mfma_f32_16x16x32_bf16 v[100:103], a[240:243], a[204:207], v[100:103]\n" \
"ds_read_b128 a[140:143], v28 offset:6144\n" \
"s_add_i32 s18, s18, s19\n" \
"v_mfma_f32_16x16x32_bf16 v[52:55], a[176:179], a[128:131], v[52:55]\n" \
"ds_read_b128 a[204:207], v29 offset:6144\n" \
"s_xor_b32 s13, s13, 2\n" \
"v_mfma_f32_16x16x32_bf16 v[52:55], a[240:243], a[192:195], v[52:55]\n" \
"s_movk_i32 s84, 0x4280\n" \
"v_mfma_f32_16x16x32_bf16 v[56:59], a[180:183], a[128:131], v[56:59]\n" \
"buffer_load_dwordx4 v24, s[76:79], s84 offen offset:2048 lds\n" \
"v_mfma_f32_16x16x32_bf16 v[56:59], a[244:247], a[192:195], v[56:59]\n" \
"s_movk_i32 s84, 0x6280\n" \
"v_mfma_f32_16x16x32_bf16 v[60:63], a[184:187], a[128:131], v[60:63]\n" \
"buffer_load_dwordx4 v25, s[76:79], s84 offen offset:3072 lds\n" \
"v_mfma_f32_16x16x32_bf16 v[60:63], a[248:251], a[192:195], v[60:63]\n" \
"ds_read_b64_tr_b16 a[234:235], v34 offset:37376\n" \
"s_add_i32 s15, s15, -1\n" \
"v_mfma_f32_16x16x32_bf16 v[64:67], a[188:191], a[128:131], v[64:67]\n" \
"v_writelane_b32 v11, s18, 27\n" \
"s_mul_i32 s18, s13, 0x24000\n" \
"v_mfma_f32_16x16x32_bf16 v[64:67], a[252:255], a[192:195], v[64:67]\n" \
"ds_read_b128 a[128:131], v28 offset:0\n" \
"s_and_b32 s15, s15, 3\n" \
"v_mfma_f32_16x16x32_bf16 v[80:83], a[188:191], a[132:135], v[80:83]\n" \
"ds_read_b128 a[192:195], v29 offset:0\n" \
"s_add_i32 s18, s18, s19\n" \
"v_mfma_f32_16x16x32_bf16 v[80:83], a[252:255], a[196:199], v[80:83]\n" \
"s_or_b32 s15, s15, s5\n" \
"v_writelane_b32 v11, s18, 28\n" \
"v_mfma_f32_16x16x32_bf16 v[76:79], a[184:187], a[132:135], v[76:79]\n" \
"s_mul_i32 s18, s15, 0x24000\n" \
"s_add_i32 s18, s18, s19\n" \
"v_mfma_f32_16x16x32_bf16 v[76:79], a[248:251], a[196:199], v[76:79]\n" \
"v_writelane_b32 v11, s18, 29\n" \
"s_or_b32 s18, s9, s14\n" \
"v_mfma_f32_16x16x32_bf16 v[72:75], a[180:183], a[132:135], v[72:75]\n" \
"s_mul_i32 s18, s18, 0x24800\n" \
"s_lshl_b32 s16, s16, 12\n" \
"v_mfma_f32_16x16x32_bf16 v[72:75], a[244:247], a[196:199], v[72:75]\n" \
"s_add_i32 s19, s18, 0x3004800\n" \
"s_add_i32 s20, s19, s16\n" \
"v_mfma_f32_16x16x32_bf16 v[68:71], a[176:179], a[132:135], v[68:71]\n" \
"v_writelane_b32 v11, s20, 30\n" \
"s_lshl_b32 s13, s13, 12\n" \
"v_mfma_f32_16x16x32_bf16 v[68:71], a[240:243], a[196:199], v[68:71]\n" \
"ds_read_b128 a[132:135], v28 offset:2048\n" \
"ds_read_b128 a[196:199], v29 offset:2048\n" \
"s_waitcnt lgkmcnt(15)\n" \
"v_mfma_f32_16x16x32_bf16 a[0:3], a[160:163], a[144:147], a[0:3]\n" \
"s_add_u32 m0, s30, 0x21000\n" \
"s_addk_i32 s84, 0x3000\n" \
"v_mfma_f32_16x16x32_bf16 a[0:3], a[224:227], a[208:211], a[0:3]\n" \
"buffer_load_dwordx4 v24, s[76:79], s84 offen offset:0 lds\n" \
"v_mfma_f32_16x16x32_bf16 a[4:7], a[164:167], a[144:147], a[4:7]\n" \
"ds_read_b64_tr_b16 a[176:177], v36 offset:32768\n" \
"s_lshl_b32 s15, s15, 12\n" \
"v_mfma_f32_16x16x32_bf16 a[4:7], a[228:231], a[208:211], a[4:7]\n" \
"ds_read_b64_tr_b16 a[178:179], v36 offset:33792\n" \
"s_sub_i32 s14, s9, s14\n" \
"v_mfma_f32_16x16x32_bf16 a[12:15], a[172:175], a[144:147], a[12:15]\n" \
"ds_read_b64_tr_b16 a[240:241], v36 offset:40960\n" \
"s_add_i32 s20, s19, s13\n" \
"s_waitcnt lgkmcnt(15)\n" \
"v_mfma_f32_16x16x32_bf16 a[12:15], a[236:239], a[208:211], a[12:15]\n" \
"s_add_u32 s88, s42, 0x0\n" \
"buffer_store_dwordx4 v[220:223], v20, s[80:83], s88 offen offset:0 sc1\n" \
"s_waitcnt lgkmcnt(11)\n" \
"v_mfma_f32_16x16x32_bf16 a[8:11], a[144:147], a[168:171], a[8:11]\n" \
"ds_read_b64_tr_b16 a[242:243], v36 offset:41984\n" \
"v_writelane_b32 v11, s20, 31\n" \
"s_waitcnt lgkmcnt(8)\n" \
"v_mfma_f32_16x16x32_bf16 a[8:11], a[208:211], a[232:235], a[8:11]\n" \
"ds_read_b64_tr_b16 a[180:181], v37 offset:32768\n" \
"s_add_i32 s19, s19, s15\n" \
"v_mfma_f32_16x16x32_bf16 a[24:27], a[148:151], a[168:171], a[24:27]\n" \
"ds_read_b64_tr_b16 a[182:183], v37 offset:33792\n" \
"s_mul_i32 s14, s14, 0x24800\n" \
"v_mfma_f32_16x16x32_bf16 a[24:27], a[212:215], a[232:235], a[24:27]\n" \
"ds_read_b64_tr_b16 a[244:245], v37 offset:40960\n" \
"v_writelane_b32 v11, s19, 32\n" \
"v_mfma_f32_16x16x32_bf16 a[28:31], a[172:175], a[148:151], a[28:31]\n" \
"s_add_i32 s19, s14, 0x4120800\n" \
"s_add_i32 s16, s19, s16\n" \
"v_mfma_f32_16x16x32_bf16 a[28:31], a[236:239], a[212:215], a[28:31]\n" \
"s_addk_i32 s84, 0x2000\n" \
"v_mfma_f32_16x16x32_bf16 a[20:23], a[164:167], a[148:151], a[20:23]\n" \
"buffer_load_dwordx4 v25, s[76:79], s84 offen offset:1024 lds\n" \
"v_mfma_f32_16x16x32_bf16 a[20:23], a[228:231], a[212:215], a[20:23]\n" \
"ds_read_b64_tr_b16 a[246:247], v37 offset:41984\n" \
"v_writelane_b32 v11, s16, 33\n" \
"v_mfma_f32_16x16x32_bf16 a[16:19], a[160:163], a[148:151], a[16:19]\n" \
"ds_read_b64_tr_b16 a[184:185], v38 offset:32768\n" \
"s_add_i32 s13, s19, s13\n" \
"v_mfma_f32_16x16x32_bf16 a[16:19], a[224:227], a[212:215], a[16:19]\n" \
"ds_read_b64_tr_b16 a[186:187], v38 offset:33792\n" \
"v_writelane_b32 v11, s13, 34\n" \
"v_mfma_f32_16x16x32_bf16 a[32:35], a[160:163], a[152:155], a[32:35]\n" \
"s_addk_i32 s84, 0x2000\n" \
"v_mfma_f32_16x16x32_bf16 a[32:35], a[224:227], a[216:219], a[32:35]\n" \
"buffer_load_dwordx4 v24, s[76:79], s84 offen offset:2048 lds\n" \
"v_mfma_f32_16x16x32_bf16 a[36:39], a[164:167], a[152:155], a[36:39]\n" \
"ds_read_b64_tr_b16 a[248:249], v38 offset:40960\n" \
"s_add_i32 s19, s19, s15\n" \
"v_mfma_f32_16x16x32_bf16 a[36:39], a[228:231], a[216:219], a[36:39]\n" \
"ds_read_b64_tr_b16 a[250:251], v38 offset:41984\n" \
"s_or_b32 s13, s18, 0x3000000\n" \
"v_mfma_f32_16x16x32_bf16 a[44:47], a[172:175], a[152:155], a[44:47]\n" \
"ds_read_b64_tr_b16 a[188:189], v39 offset:32768\n" \
"s_lshl_b32 s15, s6, 11\n" \
"v_mfma_f32_16x16x32_bf16 a[44:47], a[236:239], a[216:219], a[44:47]\n" \
"ds_read_b64_tr_b16 a[190:191], v39 offset:33792\n" \
"v_writelane_b32 v11, s19, 35\n" \
"v_mfma_f32_16x16x32_bf16 a[40:43], a[152:155], a[168:171], a[40:43]\n" \
"s_add_u32 s89, s42, 0x1000\n" \
"buffer_store_dwordx4 v[224:227], v20, s[80:83], s89 offen offset:3072 sc1\n" \
"v_mfma_f32_16x16x32_bf16 a[40:43], a[216:219], a[232:235], a[40:43]\n" \
"ds_read_b64_tr_b16 a[252:253], v39 offset:40960\n" \
"s_add_i32 s16, s13, s15\n" \
"v_mfma_f32_16x16x32_bf16 a[56:59], a[156:159], a[168:171], a[56:59]\n" \
"ds_read_b64_tr_b16 a[254:255], v39 offset:41984\n" \
"v_writelane_b32 v11, s16, 36\n" \
"v_mfma_f32_16x16x32_bf16 a[56:59], a[220:223], a[232:235], a[56:59]\n" \
"s_add_i32 s16, s14, 0x411c000\n" \
"s_add_i32 s19, s16, s15\n" \
"v_mfma_f32_16x16x32_bf16 a[60:63], a[172:175], a[156:159], a[60:63]\n" \
"v_writelane_b32 v11, s19, 37\n" \
"s_add_i32 s18, s18, 0x3004000\n" \
"v_mfma_f32_16x16x32_bf16 a[60:63], a[236:239], a[220:223], a[60:63]\n" \
"s_addk_i32 s84, 0x2000\n" \
"v_mfma_f32_16x16x32_bf16 a[52:55], a[164:167], a[156:159], a[52:55]\n" \
"buffer_load_dwordx4 v25, s[76:79], s84 offen offset:3072 lds\n" \
"v_mfma_f32_16x16x32_bf16 a[52:55], a[228:231], a[220:223], a[52:55]\n" \
"v_writelane_b32 v11, s18, 38\n" \
"s_add_i32 s14, s14, 0x4120000\n" \
"v_mfma_f32_16x16x32_bf16 a[48:51], a[160:163], a[156:159], a[48:51]\n" \
"v_writelane_b32 v11, s14, 39\n" \
"s_lshl_b32 s14, s4, 2\n" \
"v_mfma_f32_16x16x32_bf16 a[48:51], a[224:227], a[220:223], a[48:51]\n" \
"s_or_b32 s8, s14, s8\n" \
"s_mul_i32 s8, s8, 0x90000\n" \
"v_mfma_f32_16x16x32_bf16 a[64:67], a[164:167], a[140:143], a[64:67]\n" \
"s_lshl_b32 s14, s3, 14\n" \
"s_add_i32 s8, s8, s14\n" \
"v_mfma_f32_16x16x32_bf16 a[64:67], a[228:231], a[204:207], a[64:67]\n" \
"s_lshl_b32 s9, s9, 4\n" \
"s_lshl_b32 s14, s4, 7\n" \
"v_mfma_f32_16x16x32_bf16 a[80:83], a[172:175], a[140:143], a[80:83]\n" \
"s_or_b32 s18, s9, s14\n" \
"s_sub_i32 s9, s9, s14\n" \
"v_mfma_f32_16x16x32_bf16 a[80:83], a[236:239], a[204:207], a[80:83]\n" \
"s_mulk_i32 s18, 0x480\n" \
"s_mulk_i32 s9, 0x480\n" \
"v_mfma_f32_16x16x32_bf16 a[68:71], a[140:143], a[168:171], a[68:71]\n" \
"s_or_b32 s14, s18, s12\n" \
"s_or_b32 s9, s9, s12\n" \
"v_mfma_f32_16x16x32_bf16 a[68:71], a[204:207], a[232:235], a[68:71]\n" \
"s_waitcnt vmcnt(11) lgkmcnt(0)\n" \
"s_barrier\n" \
"v_mfma_f32_16x16x32_bf16 v[116:119], a[176:179], a[144:147], v[116:119]\n" \
"s_add_u32 m0, s40, 0x8000\n" \
"s_add_u32 s85, s39, 0xa000\n" \
"v_mfma_f32_16x16x32_bf16 v[116:119], a[240:243], a[208:211], v[116:119]\n" \
"buffer_load_dwordx4 v20, s[80:83], s85 offen sc1 lds\n" \
"v_mfma_f32_16x16x32_bf16 v[120:123], a[180:183], a[144:147], v[120:123]\n" \
"ds_read_b64_tr_b16 a[160:161], v32 offset:0\n" \
"v_accvgpr_read_b32 v212, a108\n" \
"v_mfma_f32_16x16x32_bf16 v[120:123], a[244:247], a[208:211], v[120:123]\n" \
"ds_read_b64_tr_b16 a[162:163], v32 offset:512\n" \
"v_accvgpr_read_b32 v213, a109\n" \
"v_mfma_f32_16x16x32_bf16 v[124:127], a[184:187], a[144:147], v[124:127]\n" \
"s_add_u32 m0, s40, 0x9000\n" \
"s_add_u32 s85, s39, 0xb000\n" \
"v_mfma_f32_16x16x32_bf16 v[124:127], a[248:251], a[208:211], v[124:127]\n" \
"buffer_load_dwordx4 v20, s[80:83], s85 offen sc1 lds\n" \
"v_mfma_f32_16x16x32_bf16 v[128:131], a[188:191], a[144:147], v[128:131]\n" \
"ds_read_b64_tr_b16 a[224:225], v32 offset:4096\n" \
"v_accvgpr_read_b32 v214, a110\n" \
"v_mfma_f32_16x16x32_bf16 v[128:131], a[252:255], a[208:211], v[128:131]\n" \
"ds_read_b128 a[144:147], v28 offset:40960\n" \
"v_accvgpr_read_b32 v215, a111\n" \
"v_mfma_f32_16x16x32_bf16 v[144:147], a[188:191], a[148:151], v[144:147]\n" \
"ds_read_b128 a[208:211], v29 offset:40960\n" \
"v_accvgpr_read_b32 v180, a124\n" \
"v_mfma_f32_16x16x32_bf16 v[144:147], a[252:255], a[212:215], v[144:147]\n" \
"s_add_u32 m0, s40, 0xb000\n" \
"s_movk_i32 s84, 0x280\n" \
"s_nop 0\n" \
"v_mfma_f32_16x16x32_bf16 v[140:143], a[184:187], a[148:151], v[140:143]\n" \
"buffer_load_dwordx4 v21, s[76:79], s84 offen offset:0 lds\n" \
"v_mfma_f32_16x16x32_bf16 v[140:143], a[248:251], a[212:215], v[140:143]\n" \
"ds_read_b64_tr_b16 a[226:227], v32 offset:4608\n" \
"v_accvgpr_read_b32 v181, a125\n" \
"v_mfma_f32_16x16x32_bf16 v[136:139], a[180:183], a[148:151], v[136:139]\n" \
"s_add_u32 s88, s42, 0x1000\n" \
"buffer_store_dwordx4 v[228:231], v20, s[80:83], s88 offen offset:2048 sc1\n" \
"v_mfma_f32_16x16x32_bf16 v[136:139], a[244:247], a[212:215], v[136:139]\n" \
"ds_read_b64_tr_b16 a[164:165], v33 offset:0\n" \
"v_accvgpr_read_b32 v182, a126\n" \
"v_mfma_f32_16x16x32_bf16 v[132:135], a[176:179], a[148:151], v[132:135]\n" \
"v_accvgpr_read_b32 v183, a127\n" \
"v_cvt_pk_bf16_f32 v212, v212, v213\n" \
"v_mfma_f32_16x16x32_bf16 v[132:135], a[240:243], a[212:215], v[132:135]\n" \
"ds_read_b128 a[148:151], v28 offset:43008\n" \
"v_cvt_pk_bf16_f32 v213, v214, v215\n" \
"v_mfma_f32_16x16x32_bf16 v[148:151], a[176:179], a[152:155], v[148:151]\n" \
"ds_read_b128 a[212:215], v29 offset:43008\n" \
"v_cvt_pk_bf16_f32 v214, v180, v181\n" \
"v_mfma_f32_16x16x32_bf16 v[148:151], a[240:243], a[216:219], v[148:151]\n" \
"s_add_u32 m0, s31, 0x8000\n" \
"s_mov_b32 s84, 0x280000\n" \
"v_mfma_f32_16x16x32_bf16 v[152:155], a[180:183], a[152:155], v[152:155]\n" \
"buffer_load_dwordx4 v26, s[68:71], s84 offen offset:0 lds\n" \
"v_mfma_f32_16x16x32_bf16 v[152:155], a[244:247], a[216:219], v[152:155]\n" \
"ds_read_b64_tr_b16 a[166:167], v33 offset:512\n" \
"v_cvt_pk_bf16_f32 v215, v182, v183\n" \
"v_mfma_f32_16x16x32_bf16 v[156:159], a[184:187], a[152:155], v[156:159]\n" \
"ds_read_b64_tr_b16 a[228:229], v33 offset:4096\n" \
"v_accvgpr_read_b32 v208, a104\n" \
"v_mfma_f32_16x16x32_bf16 v[156:159], a[248:251], a[216:219], v[156:159]\n" \
"s_addk_i32 s84, 0x7c00\n" \
"v_mfma_f32_16x16x32_bf16 v[160:163], a[188:191], a[152:155], v[160:163]\n" \
"buffer_load_dwordx4 v26, s[68:71], s84 offen offset:1024 lds\n" \
"v_mfma_f32_16x16x32_bf16 v[160:163], a[252:255], a[216:219], v[160:163]\n" \
"ds_read_b128 a[152:155], v28 offset:45056\n" \
"v_accvgpr_read_b32 v209, a105\n" \
"v_mfma_f32_16x16x32_bf16 v[176:179], a[188:191], a[156:159], v[176:179]\n" \
"ds_read_b128 a[216:219], v29 offset:45056\n" \
"v_accvgpr_read_b32 v210, a106\n" \
"v_mfma_f32_16x16x32_bf16 v[176:179], a[252:255], a[220:223], v[176:179]\n" \
"s_addk_i32 s84, 0x7c00\n" \
"v_mfma_f32_16x16x32_bf16 v[172:175], a[184:187], a[156:159], v[172:175]\n" \
"buffer_load_dwordx4 v27, s[68:71], s84 offen offset:2048 lds\n" \
"v_mfma_f32_16x16x32_bf16 v[172:175], a[248:251], a[220:223], v[172:175]\n" \
"ds_read_b64_tr_b16 a[230:231], v33 offset:4608\n" \
"v_accvgpr_read_b32 v211, a107\n" \
"v_mfma_f32_16x16x32_bf16 v[168:171], a[180:183], a[156:159], v[168:171]\n" \
"ds_read_b64_tr_b16 a[172:173], v35 offset:0\n" \
"v_accvgpr_read_b32 v180, a120\n" \
"v_mfma_f32_16x16x32_bf16 v[168:171], a[244:247], a[220:223], v[168:171]\n" \
"ds_read_b64_tr_b16 a[174:175], v35 offset:512\n" \
"v_accvgpr_read_b32 v181, a121\n" \
"v_mfma_f32_16x16x32_bf16 v[164:167], a[176:179], a[156:159], v[164:167]\n" \
"v_accvgpr_read_b32 v182, a122\n" \
"v_accvgpr_read_b32 v183, a123\n" \
"v_mfma_f32_16x16x32_bf16 v[164:167], a[240:243], a[220:223], v[164:167]\n" \
"ds_read_b128 a[156:159], v28 offset:47104\n" \
"v_cvt_pk_bf16_f32 v208, v208, v209\n" \
"v_mfma_f32_16x16x32_bf16 v[84:87], a[176:179], a[136:139], v[84:87]\n" \
"ds_read_b128 a[220:223], v29 offset:47104\n" \
"v_cvt_pk_bf16_f32 v209, v210, v211\n" \
"v_mfma_f32_16x16x32_bf16 v[84:87], a[240:243], a[200:203], v[84:87]\n" \
"s_addk_i32 s84, 0x7c00\n" \
"v_mfma_f32_16x16x32_bf16 v[88:91], a[180:183], a[136:139], v[88:91]\n" \
"buffer_load_dwordx4 v27, s[68:71], s84 offen offset:3072 lds\n" \
"v_mfma_f32_16x16x32_bf16 v[88:91], a[244:247], a[200:203], v[88:91]\n" \
"s_add_u32 m0, s30, 0x0\n" \
"s_movk_i32 s84, 0x300\n" \
"s_nop 0\n" \
"v_mfma_f32_16x16x32_bf16 v[92:95], a[184:187], a[136:139], v[92:95]\n" \
"buffer_load_dwordx4 v24, s[76:79], s84 offen offset:0 lds\n" \
"v_mfma_f32_16x16x32_bf16 v[92:95], a[248:251], a[200:203], v[92:95]\n" \
"ds_read_b64_tr_b16 a[236:237], v35 offset:4096\n" \
"v_cvt_pk_bf16_f32 v210, v180, v181\n" \
"v_mfma_f32_16x16x32_bf16 v[96:99], a[188:191], a[136:139], v[96:99]\n" \
"ds_read_b64_tr_b16 a[238:239], v35 offset:4608\n" \
"v_cvt_pk_bf16_f32 v211, v182, v183\n" \
"v_mfma_f32_16x16x32_bf16 v[96:99], a[252:255], a[200:203], v[96:99]\n" \
"ds_read_b128 a[136:139], v28 offset:36864\n" \
"v_accvgpr_read_b32 v204, a100\n" \
"v_mfma_f32_16x16x32_bf16 v[112:115], a[188:191], a[140:143], v[112:115]\n" \
"ds_read_b128 a[200:203], v29 offset:36864\n" \
"v_accvgpr_read_b32 v205, a101\n" \
"v_mfma_f32_16x16x32_bf16 v[112:115], a[252:255], a[204:207], v[112:115]\n" \
"s_movk_i32 s84, 0x2300\n" \
"s_nop 0\n" \
"v_mfma_f32_16x16x32_bf16 v[108:111], a[184:187], a[140:143], v[108:111]\n" \
"buffer_load_dwordx4 v25, s[76:79], s84 offen offset:1024 lds\n" \
"v_mfma_f32_16x16x32_bf16 v[108:111], a[248:251], a[204:207], v[108:111]\n" \
"s_add_u32 s89, s42, 0x1000\n" \
"buffer_store_dwordx4 v[232:235], v20, s[80:83], s89 offen offset:1024 sc1\n" \
"v_mfma_f32_16x16x32_bf16 v[104:107], a[180:183], a[140:143], v[104:107]\n" \
"ds_read_b64_tr_b16 a[168:169], v34 offset:0\n" \
"v_accvgpr_read_b32 v206, a102\n" \
"v_mfma_f32_16x16x32_bf16 v[104:107], a[244:247], a[204:207], v[104:107]\n" \
"ds_read_b64_tr_b16 a[170:171], v34 offset:512\n" \
"v_accvgpr_read_b32 v207, a103\n" \
"v_mfma_f32_16x16x32_bf16 v[100:103], a[176:179], a[140:143], v[100:103]\n" \
"ds_read_b64_tr_b16 a[232:233], v34 offset:4096\n" \
"v_accvgpr_read_b32 v180, a116\n" \
"v_mfma_f32_16x16x32_bf16 v[100:103], a[240:243], a[204:207], v[100:103]\n" \
"ds_read_b128 a[140:143], v28 offset:38912\n" \
"v_accvgpr_read_b32 v181, a117\n" \
"v_mfma_f32_16x16x32_bf16 v[52:55], a[176:179], a[128:131], v[52:55]\n" \
"ds_read_b128 a[204:207], v29 offset:38912\n" \
"v_accvgpr_read_b32 v182, a118\n" \
"v_mfma_f32_16x16x32_bf16 v[52:55], a[240:243], a[192:195], v[52:55]\n" \
"s_movk_i32 s84, 0x4300\n" \
"v_mfma_f32_16x16x32_bf16 v[56:59], a[180:183], a[128:131], v[56:59]\n" \
"buffer_load_dwordx4 v24, s[76:79], s84 offen offset:2048 lds\n" \
"v_mfma_f32_16x16x32_bf16 v[56:59], a[244:247], a[192:195], v[56:59]\n" \
"s_movk_i32 s84, 0x6300\n" \
"v_mfma_f32_16x16x32_bf16 v[60:63], a[184:187], a[128:131], v[60:63]\n" \
"buffer_load_dwordx4 v25, s[76:79], s84 offen offset:3072 lds\n" \
"v_mfma_f32_16x16x32_bf16 v[60:63], a[248:251], a[192:195], v[60:63]\n" \
"ds_read_b64_tr_b16 a[234:235], v34 offset:4608\n" \
"v_accvgpr_read_b32 v183, a119\n" \
"v_mfma_f32_16x16x32_bf16 v[64:67], a[188:191], a[128:131], v[64:67]\n" \
"v_cvt_pk_bf16_f32 v204, v204, v205\n" \
"v_cvt_pk_bf16_f32 v205, v206, v207\n" \
"v_mfma_f32_16x16x32_bf16 v[64:67], a[252:255], a[192:195], v[64:67]\n" \
"ds_read_b128 a[128:131], v28 offset:32768\n" \
"v_cvt_pk_bf16_f32 v206, v180, v181\n" \
"v_mfma_f32_16x16x32_bf16 v[80:83], a[188:191], a[132:135], v[80:83]\n" \
"ds_read_b128 a[192:195], v29 offset:32768\n" \
"v_cvt_pk_bf16_f32 v207, v182, v183\n" \
"v_mfma_f32_16x16x32_bf16 v[80:83], a[252:255], a[196:199], v[80:83]\n" \
"v_accvgpr_read_b32 v200, a96\n" \
"v_accvgpr_read_b32 v201, a97\n" \
"v_mfma_f32_16x16x32_bf16 v[76:79], a[184:187], a[132:135], v[76:79]\n" \
"v_accvgpr_read_b32 v202, a98\n" \
"v_accvgpr_read_b32 v203, a99\n" \
"v_mfma_f32_16x16x32_bf16 v[76:79], a[248:251], a[196:199], v[76:79]\n" \
"v_accvgpr_read_b32 v180, a112\n" \
"v_accvgpr_read_b32 v181, a113\n" \
"v_mfma_f32_16x16x32_bf16 v[72:75], a[180:183], a[132:135], v[72:75]\n" \
"v_accvgpr_read_b32 v182, a114\n" \
"v_accvgpr_read_b32 v183, a115\n" \
"v_mfma_f32_16x16x32_bf16 v[72:75], a[244:247], a[196:199], v[72:75]\n" \
"v_cvt_pk_bf16_f32 v200, v200, v201\n" \
"v_cvt_pk_bf16_f32 v201, v202, v203\n" \
"v_mfma_f32_16x16x32_bf16 v[68:71], a[176:179], a[132:135], v[68:71]\n" \
"v_cvt_pk_bf16_f32 v202, v180, v181\n" \
"v_cvt_pk_bf16_f32 v203, v182, v183\n" \
"v_mfma_f32_16x16x32_bf16 v[68:71], a[240:243], a[196:199], v[68:71]\n" \
"ds_read_b128 a[132:135], v28 offset:34816\n" \
"ds_read_b128 a[196:199], v29 offset:34816\n" \
"s_waitcnt lgkmcnt(15)\n" \
"v_mfma_f32_16x16x32_bf16 a[0:3], a[160:163], a[144:147], a[0:3]\n" \
"s_add_u32 m0, s30, 0x1000\n" \
"s_addk_i32 s84, 0x3000\n" \
"v_mfma_f32_16x16x32_bf16 a[0:3], a[224:227], a[208:211], a[0:3]\n" \
"buffer_load_dwordx4 v24, s[76:79], s84 offen offset:0 lds\n" \
"v_mfma_f32_16x16x32_bf16 a[4:7], a[164:167], a[144:147], a[4:7]\n" \
"ds_read_b64_tr_b16 a[176:177], v36 offset:0\n" \
"v_accvgpr_read_b32 v196, a76\n" \
"v_mfma_f32_16x16x32_bf16 a[4:7], a[228:231], a[208:211], a[4:7]\n" \
"ds_read_b64_tr_b16 a[178:179], v36 offset:1024\n" \
"v_accvgpr_read_b32 v197, a77\n" \
"v_mfma_f32_16x16x32_bf16 a[12:15], a[172:175], a[144:147], a[12:15]\n" \
"ds_read_b64_tr_b16 a[240:241], v36 offset:8192\n" \
"v_accvgpr_read_b32 v198, a78\n" \
"s_waitcnt lgkmcnt(15)\n" \
"v_mfma_f32_16x16x32_bf16 a[12:15], a[236:239], a[208:211], a[12:15]\n" \
"s_add_u32 s88, s42, 0x1000\n" \
"buffer_store_dwordx4 v[236:239], v20, s[80:83], s88 offen offset:0 sc1\n" \
"s_waitcnt lgkmcnt(11)\n" \
"v_mfma_f32_16x16x32_bf16 a[8:11], a[144:147], a[168:171], a[8:11]\n" \
"ds_read_b64_tr_b16 a[242:243], v36 offset:9216\n" \
"v_accvgpr_read_b32 v199, a79\n" \
"s_waitcnt lgkmcnt(8)\n" \
"s_nop 0\n" \
"v_mfma_f32_16x16x32_bf16 a[8:11], a[208:211], a[232:235], a[8:11]\n" \
"ds_read_b64_tr_b16 a[180:181], v37 offset:0\n" \
"v_accvgpr_read_b32 v180, a92\n" \
"v_mfma_f32_16x16x32_bf16 a[24:27], a[148:151], a[168:171], a[24:27]\n" \
"ds_read_b64_tr_b16 a[182:183], v37 offset:1024\n" \
"v_accvgpr_read_b32 v181, a93\n" \
"v_mfma_f32_16x16x32_bf16 a[24:27], a[212:215], a[232:235], a[24:27]\n" \
"ds_read_b64_tr_b16 a[244:245], v37 offset:8192\n" \
"v_accvgpr_read_b32 v182, a94\n" \
"v_mfma_f32_16x16x32_bf16 a[28:31], a[172:175], a[148:151], a[28:31]\n" \
"v_accvgpr_read_b32 v183, a95\n" \
"v_cvt_pk_bf16_f32 v196, v196, v197\n" \
"v_mfma_f32_16x16x32_bf16 a[28:31], a[236:239], a[212:215], a[28:31]\n" \
"s_addk_i32 s84, 0x2000\n" \
"s_nop 0\n" \
"v_mfma_f32_16x16x32_bf16 a[20:23], a[164:167], a[148:151], a[20:23]\n" \
"buffer_load_dwordx4 v25, s[76:79], s84 offen offset:1024 lds\n" \
"v_mfma_f32_16x16x32_bf16 a[20:23], a[228:231], a[212:215], a[20:23]\n" \
"ds_read_b64_tr_b16 a[246:247], v37 offset:9216\n" \
"v_cvt_pk_bf16_f32 v197, v198, v199\n" \
"v_mfma_f32_16x16x32_bf16 a[16:19], a[160:163], a[148:151], a[16:19]\n" \
"ds_read_b64_tr_b16 a[184:185], v38 offset:0\n" \
"v_cvt_pk_bf16_f32 v198, v180, v181\n" \
"v_mfma_f32_16x16x32_bf16 a[16:19], a[224:227], a[212:215], a[16:19]\n" \
"ds_read_b64_tr_b16 a[186:187], v38 offset:1024\n" \
"v_cvt_pk_bf16_f32 v199, v182, v183\n" \
"v_mfma_f32_16x16x32_bf16 a[32:35], a[160:163], a[152:155], a[32:35]\n" \
"s_addk_i32 s84, 0x2000\n" \
"s_nop 0\n" \
"v_mfma_f32_16x16x32_bf16 a[32:35], a[224:227], a[216:219], a[32:35]\n" \
"buffer_load_dwordx4 v24, s[76:79], s84 offen offset:2048 lds\n" \
"v_mfma_f32_16x16x32_bf16 a[36:39], a[164:167], a[152:155], a[36:39]\n" \
".fill (L_pb + 69344 - .) / 4, 4, 0xBF800000\n" \
"L_pp17:\n" \
"s_cbranch_execz L_pret\n" \
"s_nop 0\n" \
"ds_read_b64_tr_b16 a[248:249], v38 offset:8192\n" \
"v_accvgpr_read_b32 v192, a72\n" \
"v_mfma_f32_16x16x32_bf16 a[36:39], a[228:231], a[216:219], a[36:39]\n" \
"ds_read_b64_tr_b16 a[250:251], v38 offset:9216\n" \
"v_accvgpr_read_b32 v193, a73\n" \
"v_mfma_f32_16x16x32_bf16 a[44:47], a[172:175], a[152:155], a[44:47]\n" \
"ds_read_b64_tr_b16 a[188:189], v39 offset:0\n" \
"v_accvgpr_read_b32 v194, a74\n" \
"v_mfma_f32_16x16x32_bf16 a[44:47], a[236:239], a[216:219], a[44:47]\n" \
"ds_read_b64_tr_b16 a[190:191], v39 offset:1024\n" \
"v_accvgpr_read_b32 v195, a75\n" \
"v_mfma_f32_16x16x32_bf16 a[40:43], a[152:155], a[168:171], a[40:43]\n" \
"s_add_u32 s89, s42, 0x3000\n" \
"buffer_store_dwordx4 v[240:243], v20, s[80:83], s89 offen offset:3072 sc1\n" \
"v_mfma_f32_16x16x32_bf16 a[40:43], a[216:219], a[232:235], a[40:43]\n" \
"ds_read_b64_tr_b16 a[252:253], v39 offset:8192\n" \
"v_accvgpr_read_b32 v180, a88\n" \
"v_mfma_f32_16x16x32_bf16 a[56:59], a[156:159], a[168:171], a[56:59]\n" \
"ds_read_b64_tr_b16 a[254:255], v39 offset:9216\n" \
"v_accvgpr_read_b32 v181, a89\n" \
"v_mfma_f32_16x16x32_bf16 a[56:59], a[220:223], a[232:235], a[56:59]\n" \
"v_accvgpr_read_b32 v182, a90\n" \
"v_accvgpr_read_b32 v183, a91\n" \
"v_mfma_f32_16x16x32_bf16 a[60:63], a[172:175], a[156:159], a[60:63]\n" \
"v_cvt_pk_bf16_f32 v192, v192, v193\n" \
"v_cvt_pk_bf16_f32 v193, v194, v195\n" \
"v_mfma_f32_16x16x32_bf16 a[60:63], a[236:239], a[220:223], a[60:63]\n" \
"s_addk_i32 s84, 0x2000\n" \
"v_mfma_f32_16x16x32_bf16 a[52:55], a[164:167], a[156:159], a[52:55]\n" \
"buffer_load_dwordx4 v25, s[76:79], s84 offen offset:3072 lds\n" \
"v_mfma_f32_16x16x32_bf16 a[52:55], a[228:231], a[220:223], a[52:55]\n" \
"v_cvt_pk_bf16_f32 v194, v180, v181\n" \
"v_cvt_pk_bf16_f32 v195, v182, v183\n" \
"v_mfma_f32_16x16x32_bf16 a[48:51], a[160:163], a[156:159], a[48:51]\n" \
"s_and_b32 s12, s2, 1\n" \
"v_mfma_f32_16x16x32_bf16 a[48:51], a[224:227], a[220:223], a[48:51]\n" \
"s_cmp_lg_u32 s12, 0\n" \
"s_cselect_b32 s13, s16, s13\n" \
"v_mfma_f32_16x16x32_bf16 a[64:67], a[164:167], a[140:143], a[64:67]\n" \
"s_bfe_u32 s16, s2, 0x20001\n" \
"s_addk_i32 s13, 0x4000\n" \
"v_mfma_f32_16x16x32_bf16 a[64:67], a[228:231], a[204:207], a[64:67]\n" \
"s_xor_b32 s16, s16, s3\n" \
"v_mfma_f32_16x16x32_bf16 a[80:83], a[172:175], a[140:143], a[80:83]\n" \
"s_cmp_lg_u32 s16, 0\n" \
"s_cselect_b32 s13, s7, s13\n" \
"v_mfma_f32_16x16x32_bf16 a[80:83], a[236:239], a[204:207], a[80:83]\n" \
"s_lshl_b32 s6, s6, 16\n" \
"v_writelane_b32 v11, s13, 40\n" \
"v_mfma_f32_16x16x32_bf16 a[68:71], a[140:143], a[168:171], a[68:71]\n" \
"s_add_i32 s13, s8, 0x4240000\n" \
"s_add_i32 s6, s13, s6\n" \
"v_mfma_f32_16x16x32_bf16 a[68:71], a[204:207], a[232:235], a[68:71]\n" \
"s_waitcnt vmcnt(11) lgkmcnt(0)\n" \
"s_barrier\n" \
"v_mov_b32_e32 v45, s62\n" \
"s_mov_b64 exec, 1\n" \
"buffer_store_dword v45, off, s[80:83], s37 offset:0 sc1\n" \
"s_mov_b64 exec, -1\n" \
"v_xor_b32_e32 v28, 0x20000, v28\n" \
"v_xor_b32_e32 v29, 0x20000, v29\n" \
"v_mfma_f32_16x16x32_bf16 v[116:119], a[176:179], a[144:147], v[116:119]\n" \
"s_add_u32 m0, s40, 0x0\n" \
"s_add_u32 s85, s39, 0xc000\n" \
"v_mfma_f32_16x16x32_bf16 v[116:119], a[240:243], a[208:211], v[116:119]\n" \
"buffer_load_dwordx4 v20, s[80:83], s85 offen sc1 lds\n" \
"v_mfma_f32_16x16x32_bf16 v[120:123], a[180:183], a[144:147], v[120:123]\n" \
"ds_read_b64_tr_b16 a[160:161], v32 offset:32768\n" \
"v_writelane_b32 v11, s6, 41\n" \
"v_mfma_f32_16x16x32_bf16 v[120:123], a[244:247], a[208:211], v[120:123]\n" \
"ds_read_b64_tr_b16 a[162:163], v32 offset:33280\n" \
"s_add_i32 s8, s8, 0x42c0000\n" \
"v_mfma_f32_16x16x32_bf16 v[124:127], a[184:187], a[144:147], v[124:127]\n" \
"s_add_u32 m0, s40, 0x1000\n" \
"s_add_u32 s85, s39, 0xd000\n" \
"v_mfma_f32_16x16x32_bf16 v[124:127], a[248:251], a[208:211], v[124:127]\n" \
"buffer_load_dwordx4 v20, s[80:83], s85 offen sc1 lds\n" \
"v_mfma_f32_16x16x32_bf16 v[128:131], a[188:191], a[144:147], v[128:131]\n" \
"ds_read_b64_tr_b16 a[224:225], v32 offset:36864\n" \
"s_lshl_b32 s6, s12, 10\n" \
"v_mfma_f32_16x16x32_bf16 v[128:131], a[252:255], a[208:211], v[128:131]\n" \
"ds_read_b128 a[144:147], v28 offset:8192\n" \
"s_or_b32 s6, s8, s6\n" \
"v_mfma_f32_16x16x32_bf16 v[144:147], a[188:191], a[148:151], v[144:147]\n" \
"ds_read_b128 a[208:211], v29 offset:8192\n" \
"v_mfma_f32_16x16x32_bf16 v[144:147], a[252:255], a[212:215], v[144:147]\n" \
"s_add_u32 m0, s40, 0x3000\n" \
"s_movk_i32 s84, 0x300\n" \
"v_mfma_f32_16x16x32_bf16 v[140:143], a[184:187], a[148:151], v[140:143]\n" \
"buffer_load_dwordx4 v21, s[76:79], s84 offen offset:0 lds\n" \
"v_mfma_f32_16x16x32_bf16 v[140:143], a[248:251], a[212:215], v[140:143]\n" \
"ds_read_b64_tr_b16 a[226:227], v32 offset:37376\n" \
"v_mfma_f32_16x16x32_bf16 v[136:139], a[180:183], a[148:151], v[136:139]\n" \
"s_cmp_lg_u32 s16, 0\n" \
"s_cselect_b32 s6, s7, s6\n" \
"v_mfma_f32_16x16x32_bf16 v[136:139], a[244:247], a[212:215], v[136:139]\n" \
"ds_read_b64_tr_b16 a[164:165], v33 offset:32768\n" \
"s_mulk_i32 s4, 0x4800\n" \
"v_mfma_f32_16x16x32_bf16 v[132:135], a[176:179], a[148:151], v[132:135]\n" \
"v_writelane_b32 v11, s6, 42\n" \
"s_add_i32 s6, s14, s10\n" \
"v_mfma_f32_16x16x32_bf16 v[132:135], a[240:243], a[212:215], v[132:135]\n" \
"ds_read_b128 a[148:151], v28 offset:10240\n" \
"v_writelane_b32 v11, s6, 43\n" \
"v_mfma_f32_16x16x32_bf16 v[148:151], a[176:179], a[152:155], v[148:151]\n" \
"ds_read_b128 a[212:215], v29 offset:10240\n" \
"s_add_i32 s6, s9, 0x21c000\n" \
"v_mfma_f32_16x16x32_bf16 v[148:151], a[240:243], a[216:219], v[148:151]\n" \
"s_add_u32 m0, s31, 0x0\n" \
"s_mov_b32 s84, 0x300000\n" \
"v_mfma_f32_16x16x32_bf16 v[152:155], a[180:183], a[152:155], v[152:155]\n" \
"buffer_load_dwordx4 v26, s[68:71], s84 offen offset:0 lds\n" \
"v_mfma_f32_16x16x32_bf16 v[152:155], a[244:247], a[216:219], v[152:155]\n" \
"s_addk_i32 s84, 0x7c00\n" \
"v_mfma_f32_16x16x32_bf16 v[156:159], a[184:187], a[152:155], v[156:159]\n" \
"buffer_load_dwordx4 v26, s[68:71], s84 offen offset:1024 lds\n" \
"v_mfma_f32_16x16x32_bf16 v[156:159], a[248:251], a[216:219], v[156:159]\n" \
"ds_read_b64_tr_b16 a[166:167], v33 offset:33280\n" \
"s_add_i32 s9, s6, s10\n" \
"v_mfma_f32_16x16x32_bf16 v[160:163], a[188:191], a[152:155], v[160:163]\n" \
"ds_read_b64_tr_b16 a[228:229], v33 offset:36864\n" \
"v_mfma_f32_16x16x32_bf16 v[160:163], a[252:255], a[216:219], v[160:163]\n" \
"ds_read_b128 a[152:155], v28 offset:12288\n" \
"v_mfma_f32_16x16x32_bf16 v[176:179], a[188:191], a[156:159], v[176:179]\n" \
"ds_read_b128 a[216:219], v29 offset:12288\n" \
"v_mfma_f32_16x16x32_bf16 v[176:179], a[252:255], a[220:223], v[176:179]\n" \
"s_addk_i32 s84, 0x7c00\n" \
"v_mfma_f32_16x16x32_bf16 v[172:175], a[184:187], a[156:159], v[172:175]\n" \
"buffer_load_dwordx4 v27, s[68:71], s84 offen offset:2048 lds\n" \
"v_mfma_f32_16x16x32_bf16 v[172:175], a[248:251], a[220:223], v[172:175]\n" \
"ds_read_b64_tr_b16 a[230:231], v33 offset:37376\n" \
"v_mfma_f32_16x16x32_bf16 v[168:171], a[180:183], a[156:159], v[168:171]\n" \
"s_addk_i32 s84, 0x7c00\n" \
"v_mfma_f32_16x16x32_bf16 v[168:171], a[244:247], a[220:223], v[168:171]\n" \
"buffer_load_dwordx4 v27, s[68:71], s84 offen offset:3072 lds\n" \
"v_mfma_f32_16x16x32_bf16 v[164:167], a[176:179], a[156:159], v[164:167]\n" \
"ds_read_b64_tr_b16 a[172:173], v35 offset:32768\n" \
"v_mfma_f32_16x16x32_bf16 v[164:167], a[240:243], a[220:223], v[164:167]\n" \
"ds_read_b128 a[156:159], v28 offset:14336\n" \
"v_mfma_f32_16x16x32_bf16 v[84:87], a[176:179], a[136:139], v[84:87]\n" \
"ds_read_b128 a[220:223], v29 offset:14336\n" \
"v_mfma_f32_16x16x32_bf16 v[84:87], a[240:243], a[200:203], v[84:87]\n" \
"s_add_u32 s88, s42, 0x3000\n" \
"buffer_store_dwordx4 v[244:247], v20, s[80:83], s88 offen offset:2048 sc1\n" \
"v_mfma_f32_16x16x32_bf16 v[88:91], a[180:183], a[136:139], v[88:91]\n" \
"ds_read_b64_tr_b16 a[174:175], v35 offset:33280\n" \
"v_mfma_f32_16x16x32_bf16 v[88:91], a[244:247], a[200:203], v[88:91]\n" \
"ds_read_b64_tr_b16 a[236:237], v35 offset:36864\n" \
"v_mfma_f32_16x16x32_bf16 v[92:95], a[184:187], a[136:139], v[92:95]\n" \
"ds_read_b64_tr_b16 a[238:239], v35 offset:37376\n" \
"v_mfma_f32_16x16x32_bf16 v[92:95], a[248:251], a[200:203], v[92:95]\n" \
"s_add_u32 m0, s30, 0x8000\n" \
"s_movk_i32 s84, 0x380\n" \
"v_mfma_f32_16x16x32_bf16 v[96:99], a[188:191], a[136:139], v[96:99]\n" \
"buffer_load_dwordx4 v24, s[76:79], s84 offen offset:0 lds\n" \
"v_mfma_f32_16x16x32_bf16 v[96:99], a[252:255], a[200:203], v[96:99]\n" \
"ds_read_b128 a[136:139], v28 offset:4096\n" \
"v_mfma_f32_16x16x32_bf16 v[112:115], a[188:191], a[140:143], v[112:115]\n" \
"ds_read_b128 a[200:203], v29 offset:4096\n" \
"v_mfma_f32_16x16x32_bf16 v[112:115], a[252:255], a[204:207], v[112:115]\n" \
"ds_read_b64_tr_b16 a[168:169], v34 offset:32768\n" \
"v_mfma_f32_16x16x32_bf16 v[108:111], a[184:187], a[140:143], v[108:111]\n" \
"s_movk_i32 s84, 0x2380\n" \
"v_mfma_f32_16x16x32_bf16 v[108:111], a[248:251], a[204:207], v[108:111]\n" \
"buffer_load_dwordx4 v25, s[76:79], s84 offen offset:1024 lds\n" \
"v_mfma_f32_16x16x32_bf16 v[104:107], a[180:183], a[140:143], v[104:107]\n" \
"ds_read_b64_tr_b16 a[170:171], v34 offset:33280\n" \
"v_mfma_f32_16x16x32_bf16 v[104:107], a[244:247], a[204:207], v[104:107]\n" \
"ds_read_b64_tr_b16 a[232:233], v34 offset:36864\n" \
"v_mfma_f32_16x16x32_bf16 v[100:103], a[176:179], a[140:143], v[100:103]\n" \
"s_cmp_lg_u32 s12, 0\n" \
"s_cselect_b32 s6, s6, s14\n" \
"v_mfma_f32_16x16x32_bf16 v[100:103], a[240:243], a[204:207], v[100:103]\n" \
"ds_read_b128 a[140:143], v28 offset:6144\n" \
"s_addk_i32 s6, 0x100\n" \
"v_mfma_f32_16x16x32_bf16 v[52:55], a[176:179], a[128:131], v[52:55]\n" \
"ds_read_b128 a[204:207], v29 offset:6144\n" \
"v_writelane_b32 v11, s9, 44\n" \
"v_mfma_f32_16x16x32_bf16 v[52:55], a[240:243], a[192:195], v[52:55]\n" \
"s_movk_i32 s84, 0x4380\n" \
"v_mfma_f32_16x16x32_bf16 v[56:59], a[180:183], a[128:131], v[56:59]\n" \
"buffer_load_dwordx4 v24, s[76:79], s84 offen offset:2048 lds\n" \
"v_mfma_f32_16x16x32_bf16 v[56:59], a[244:247], a[192:195], v[56:59]\n" \
"s_movk_i32 s84, 0x6380\n" \
"v_mfma_f32_16x16x32_bf16 v[60:63], a[184:187], a[128:131], v[60:63]\n" \
"buffer_load_dwordx4 v25, s[76:79], s84 offen offset:3072 lds\n" \
"v_mfma_f32_16x16x32_bf16 v[60:63], a[248:251], a[192:195], v[60:63]\n" \
"ds_read_b64_tr_b16 a[234:235], v34 offset:37376\n" \
"v_mfma_f32_16x16x32_bf16 v[64:67], a[188:191], a[128:131], v[64:67]\n" \
"s_cmp_lg_u32 s16, 0\n" \
"s_cselect_b32 s6, s7, s6\n" \
"v_mfma_f32_16x16x32_bf16 v[64:67], a[252:255], a[192:195], v[64:67]\n" \
"ds_read_b128 a[128:131], v28 offset:0\n" \
"v_writelane_b32 v11, s6, 45\n" \
"v_mfma_f32_16x16x32_bf16 v[80:83], a[188:191], a[132:135], v[80:83]\n" \
"ds_read_b128 a[192:195], v29 offset:0\n" \
"s_or_b32 s6, s8, s15\n" \
"v_mfma_f32_16x16x32_bf16 v[80:83], a[252:255], a[196:199], v[80:83]\n" \
"v_writelane_b32 v11, s6, 53\n" \
"s_lshl_b32 s6, s3, 17\n" \
"v_mfma_f32_16x16x32_bf16 v[76:79], a[184:187], a[132:135], v[76:79]\n" \
"s_add_i32 s6, s13, s6\n" \
"s_or_b32 s5, s6, s5\n" \
"v_mfma_f32_16x16x32_bf16 v[76:79], a[248:251], a[196:199], v[76:79]\n" \
"s_or_b32 s5, s5, 0x3840\n" \
"v_writelane_b32 v11, s5, 6\n" \
"v_mfma_f32_16x16x32_bf16 v[72:75], a[180:183], a[132:135], v[72:75]\n" \
"s_mov_b32 s5, 0xff0000\n" \
"v_mfma_f32_16x16x32_bf16 v[72:75], a[244:247], a[196:199], v[72:75]\n" \
"s_cmp_lg_u32 s16, 0\n" \
"s_cselect_b32 s5, s17, s5\n" \
"v_mfma_f32_16x16x32_bf16 v[68:71], a[176:179], a[132:135], v[68:71]\n" \
"v_writelane_b32 v11, s5, 7\n" \
"s_mul_i32 s5, s3, 0x1200\n" \
"v_mfma_f32_16x16x32_bf16 v[68:71], a[240:243], a[196:199], v[68:71]\n" \
"ds_read_b128 a[132:135], v28 offset:2048\n" \
"ds_read_b128 a[196:199], v29 offset:2048\n" \
"s_waitcnt lgkmcnt(15)\n" \
"v_mfma_f32_16x16x32_bf16 a[0:3], a[160:163], a[144:147], a[0:3]\n" \
"s_add_u32 m0, s30, 0x9000\n" \
"s_addk_i32 s84, 0x3000\n" \
"v_mfma_f32_16x16x32_bf16 a[0:3], a[224:227], a[208:211], a[0:3]\n" \
"buffer_load_dwordx4 v24, s[76:79], s84 offen offset:0 lds\n" \
"v_mfma_f32_16x16x32_bf16 a[4:7], a[164:167], a[144:147], a[4:7]\n" \
"ds_read_b64_tr_b16 a[176:177], v36 offset:32768\n" \
"s_add_i32 s4, s5, s4\n" \
"v_mfma_f32_16x16x32_bf16 a[4:7], a[228:231], a[208:211], a[4:7]\n" \
"ds_read_b64_tr_b16 a[178:179], v36 offset:33792\n" \
"s_add_i32 s4, s4, s11\n" \
"s_waitcnt lgkmcnt(15)\n" \
"v_mfma_f32_16x16x32_bf16 a[12:15], a[172:175], a[144:147], a[12:15]\n" \
"ds_read_b64_tr_b16 a[240:241], v36 offset:40960\n" \
"v_writelane_b32 v11, s4, 8\n" \
"s_waitcnt lgkmcnt(15)\n" \
"v_mfma_f32_16x16x32_bf16 a[12:15], a[236:239], a[208:211], a[12:15]\n" \
"s_add_u32 s89, s42, 0x3000\n" \
"buffer_store_dwordx4 v[184:187], v20, s[80:83], s89 offen offset:1024 sc1\n" \
"s_waitcnt lgkmcnt(11)\n" \
"v_mfma_f32_16x16x32_bf16 a[8:11], a[144:147], a[168:171], a[8:11]\n" \
"ds_read_b64_tr_b16 a[242:243], v36 offset:41984\n" \
"s_lshl_b32 s4, s2, 16\n" \
"s_waitcnt lgkmcnt(8)\n" \
"v_mfma_f32_16x16x32_bf16 a[8:11], a[208:211], a[232:235], a[8:11]\n" \
"ds_read_b64_tr_b16 a[180:181], v37 offset:32768\n" \
"s_add_i32 s5, s4, 0x10000\n" \
"v_mfma_f32_16x16x32_bf16 a[24:27], a[148:151], a[168:171], a[24:27]\n" \
"ds_read_b64_tr_b16 a[182:183], v37 offset:33792\n" \
"s_and_b32 s5, s5, 0x70000\n" \
"v_mfma_f32_16x16x32_bf16 a[24:27], a[212:215], a[232:235], a[24:27]\n" \
"ds_read_b64_tr_b16 a[244:245], v37 offset:40960\n" \
"s_add_i32 s5, s13, s5\n" \
"v_mfma_f32_16x16x32_bf16 a[28:31], a[172:175], a[148:151], a[28:31]\n" \
"v_writelane_b32 v11, s5, 46\n" \
"s_add_i32 s5, s4, 0x20000\n" \
"v_mfma_f32_16x16x32_bf16 a[28:31], a[236:239], a[212:215], a[28:31]\n" \
"s_addk_i32 s84, 0x2000\n" \
"v_mfma_f32_16x16x32_bf16 a[20:23], a[164:167], a[148:151], a[20:23]\n" \
"buffer_load_dwordx4 v25, s[76:79], s84 offen offset:1024 lds\n" \
"v_mfma_f32_16x16x32_bf16 a[20:23], a[228:231], a[212:215], a[20:23]\n" \
"ds_read_b64_tr_b16 a[246:247], v37 offset:41984\n" \
"s_and_b32 s5, s5, 0x70000\n" \
"v_mfma_f32_16x16x32_bf16 a[16:19], a[160:163], a[148:151], a[16:19]\n" \
"ds_read_b64_tr_b16 a[184:185], v38 offset:32768\n" \
"s_add_i32 s5, s13, s5\n" \
"v_mfma_f32_16x16x32_bf16 a[16:19], a[224:227], a[212:215], a[16:19]\n" \
"ds_read_b64_tr_b16 a[186:187], v38 offset:33792\n" \
"s_bitset1_b32 s5, 11\n" \
"v_mfma_f32_16x16x32_bf16 a[32:35], a[160:163], a[152:155], a[32:35]\n" \
"s_addk_i32 s84, 0x2000\n" \
"s_nop 0\n" \
"v_mfma_f32_16x16x32_bf16 a[32:35], a[224:227], a[216:219], a[32:35]\n" \
"buffer_load_dwordx4 v24, s[76:79], s84 offen offset:2048 lds\n" \
"v_mfma_f32_16x16x32_bf16 a[36:39], a[164:167], a[152:155], a[36:39]\n" \
"ds_read_b64_tr_b16 a[248:249], v38 offset:40960\n" \
"v_writelane_b32 v11, s5, 47\n" \
"v_mfma_f32_16x16x32_bf16 a[36:39], a[228:231], a[216:219], a[36:39]\n" \
"ds_read_b64_tr_b16 a[250:251], v38 offset:41984\n" \
"s_add_i32 s5, s4, 0x30000\n" \
"v_mfma_f32_16x16x32_bf16 a[44:47], a[172:175], a[152:155], a[44:47]\n" \
"ds_read_b64_tr_b16 a[188:189], v39 offset:32768\n" \
"s_and_b32 s5, s5, 0x70000\n" \
"v_mfma_f32_16x16x32_bf16 a[44:47], a[236:239], a[216:219], a[44:47]\n" \
"ds_read_b64_tr_b16 a[190:191], v39 offset:33792\n" \
"s_add_i32 s5, s13, s5\n" \
"v_mfma_f32_16x16x32_bf16 a[40:43], a[152:155], a[168:171], a[40:43]\n" \
"s_add_u32 s88, s42, 0x3000\n" \
"buffer_store_dwordx4 v[188:191], v20, s[80:83], s88 offen offset:0 sc1\n" \
"v_mfma_f32_16x16x32_bf16 a[40:43], a[216:219], a[232:235], a[40:43]\n" \
"ds_read_b64_tr_b16 a[252:253], v39 offset:40960\n" \
"s_bitset1_b32 s5, 12\n" \
"v_mfma_f32_16x16x32_bf16 a[56:59], a[156:159], a[168:171], a[56:59]\n" \
"ds_read_b64_tr_b16 a[254:255], v39 offset:41984\n" \
"v_writelane_b32 v11, s5, 48\n" \
"v_mfma_f32_16x16x32_bf16 a[56:59], a[220:223], a[232:235], a[56:59]\n" \
"s_and_b32 s5, s4, 0x70000\n" \
"s_xor_b32 s5, s5, 0x40000\n" \
"v_mfma_f32_16x16x32_bf16 a[60:63], a[172:175], a[156:159], a[60:63]\n" \
"s_add_i32 s5, s13, s5\n" \
"s_or_b32 s5, s5, 0x1800\n" \
"v_mfma_f32_16x16x32_bf16 a[60:63], a[236:239], a[220:223], a[60:63]\n" \
"s_addk_i32 s84, 0x2000\n" \
"v_mfma_f32_16x16x32_bf16 a[52:55], a[164:167], a[156:159], a[52:55]\n" \
"buffer_load_dwordx4 v25, s[76:79], s84 offen offset:3072 lds\n" \
"v_mfma_f32_16x16x32_bf16 a[52:55], a[228:231], a[220:223], a[52:55]\n" \
"v_writelane_b32 v11, s5, 49\n" \
"s_add_i32 s5, s4, 0x50000\n" \
"v_mfma_f32_16x16x32_bf16 a[48:51], a[160:163], a[156:159], a[48:51]\n" \
"s_and_b32 s5, s5, 0x70000\n" \
"s_add_i32 s5, s13, s5\n" \
"v_mfma_f32_16x16x32_bf16 a[48:51], a[224:227], a[220:223], a[48:51]\n" \
"s_bitset1_b32 s5, 13\n" \
"v_writelane_b32 v11, s5, 50\n" \
"v_mfma_f32_16x16x32_bf16 a[64:67], a[164:167], a[140:143], a[64:67]\n" \
"s_add_i32 s5, s4, 0x60000\n" \
"s_and_b32 s5, s5, 0x70000\n" \
"v_mfma_f32_16x16x32_bf16 a[64:67], a[228:231], a[204:207], a[64:67]\n" \
"s_add_i32 s4, s4, 0x70000\n" \
"s_add_i32 s5, s13, s5\n" \
"v_mfma_f32_16x16x32_bf16 a[80:83], a[172:175], a[140:143], a[80:83]\n" \
"s_and_b32 s4, s4, 0x70000\n" \
"s_or_b32 s5, s5, 0x2800\n" \
"v_mfma_f32_16x16x32_bf16 a[80:83], a[236:239], a[204:207], a[80:83]\n" \
"v_writelane_b32 v11, s5, 51\n" \
"s_add_i32 s13, s13, s4\n" \
"v_mfma_f32_16x16x32_bf16 a[68:71], a[140:143], a[168:171], a[68:71]\n" \
"s_or_b32 s4, s13, 0x3000\n" \
"v_writelane_b32 v11, s4, 52\n" \
"v_mfma_f32_16x16x32_bf16 a[68:71], a[204:207], a[232:235], a[68:71]\n" \
"s_waitcnt vmcnt(11) lgkmcnt(0)\n" \
"s_barrier\n" \
"v_xor_b32_e32 v28, 0x20000, v28\n" \
"v_xor_b32_e32 v29, 0x20000, v29\n" \
"v_mfma_f32_16x16x32_bf16 v[116:119], a[176:179], a[144:147], v[116:119]\n" \
"s_add_u32 m0, s40, 0x8000\n" \
"s_add_u32 s85, s39, 0xe000\n" \
"v_mfma_f32_16x16x32_bf16 v[116:119], a[240:243], a[208:211], v[116:119]\n" \
"buffer_load_dwordx4 v20, s[80:83], s85 offen sc1 lds\n" \
"v_mfma_f32_16x16x32_bf16 v[120:123], a[180:183], a[144:147], v[120:123]\n" \
"ds_read_b64_tr_b16 a[160:161], v32 offset:0\n" \
"s_movk_i32 s11, 0x4000\n" \
"v_mfma_f32_16x16x32_bf16 v[120:123], a[244:247], a[208:211], v[120:123]\n" \
"ds_read_b64_tr_b16 a[162:163], v32 offset:512\n" \
"s_lshl_b32 s4, s2, 2\n" \
"v_mfma_f32_16x16x32_bf16 v[124:127], a[184:187], a[144:147], v[124:127]\n" \
"s_add_u32 m0, s40, 0x9000\n" \
"s_add_u32 s85, s39, 0xf000\n" \
"v_mfma_f32_16x16x32_bf16 v[124:127], a[248:251], a[208:211], v[124:127]\n" \
"buffer_load_dwordx4 v20, s[80:83], s85 offen sc1 lds\n" \
"v_mfma_f32_16x16x32_bf16 v[128:131], a[188:191], a[144:147], v[128:131]\n" \
"ds_read_b64_tr_b16 a[224:225], v32 offset:4096\n" \
"s_and_b32 s4, s4, 28\n" \
"v_mfma_f32_16x16x32_bf16 v[128:131], a[252:255], a[208:211], v[128:131]\n" \
"ds_read_b128 a[144:147], v28 offset:8192\n" \
"s_bfe_u32 s5, s2, 0x20003\n" \
"v_mfma_f32_16x16x32_bf16 v[144:147], a[188:191], a[148:151], v[144:147]\n" \
"ds_read_b128 a[208:211], v29 offset:8192\n" \
"s_or_b32 s4, s4, s5\n" \
"v_mfma_f32_16x16x32_bf16 v[144:147], a[252:255], a[212:215], v[144:147]\n" \
"s_add_u32 m0, s40, 0xb000\n" \
"s_movk_i32 s84, 0x380\n" \
"v_mfma_f32_16x16x32_bf16 v[140:143], a[184:187], a[148:151], v[140:143]\n" \
"buffer_load_dwordx4 v21, s[76:79], s84 offen offset:0 lds\n" \
"v_mfma_f32_16x16x32_bf16 v[140:143], a[248:251], a[212:215], v[140:143]\n" \
"ds_read_b64_tr_b16 a[226:227], v32 offset:4608\n" \
"s_lshr_b32 s5, s2, 5\n" \
"v_mfma_f32_16x16x32_bf16 v[136:139], a[180:183], a[148:151], v[136:139]\n" \
"s_lshr_b32 s19, s3, 1\n" \
"s_lshl_b32 s18, s5, 1\n" \
"v_mfma_f32_16x16x32_bf16 v[136:139], a[244:247], a[212:215], v[136:139]\n" \
"ds_read_b64_tr_b16 a[164:165], v33 offset:0\n" \
"s_lshl_b32 s6, s4, 20\n" \
"v_mfma_f32_16x16x32_bf16 v[132:135], a[176:179], a[148:151], v[132:135]\n" \
"s_lshl_b32 s7, s4, 8\n" \
"s_and_b32 s13, s3, 1\n" \
"v_mfma_f32_16x16x32_bf16 v[132:135], a[240:243], a[212:215], v[132:135]\n" \
"ds_read_b128 a[148:151], v28 offset:10240\n" \
"s_add_i32 s14, s18, s19\n" \
"v_mfma_f32_16x16x32_bf16 v[148:151], a[176:179], a[152:155], v[148:151]\n" \
"ds_read_b128 a[212:215], v29 offset:10240\n" \
"v_mfma_f32_16x16x32_bf16 v[148:151], a[240:243], a[216:219], v[148:151]\n" \
"s_add_u32 m0, s31, 0x8000\n" \
"s_mov_b32 s84, 0x380000\n" \
"v_mfma_f32_16x16x32_bf16 v[152:155], a[180:183], a[152:155], v[152:155]\n" \
"buffer_load_dwordx4 v26, s[68:71], s84 offen offset:0 lds\n" \
"v_mfma_f32_16x16x32_bf16 v[152:155], a[244:247], a[216:219], v[152:155]\n" \
"s_addk_i32 s84, 0x7c00\n" \
"s_nop 0\n" \
"v_mfma_f32_16x16x32_bf16 v[156:159], a[184:187], a[152:155], v[156:159]\n" \
"buffer_load_dwordx4 v26, s[68:71], s84 offen offset:1024 lds\n" \
"v_mfma_f32_16x16x32_bf16 v[156:159], a[248:251], a[216:219], v[156:159]\n" \
"ds_read_b64_tr_b16 a[166:167], v33 offset:512\n" \
"v_mfma_f32_16x16x32_bf16 v[160:163], a[188:191], a[152:155], v[160:163]\n" \
"ds_read_b64_tr_b16 a[228:229], v33 offset:4096\n" \
"v_mfma_f32_16x16x32_bf16 v[160:163], a[252:255], a[216:219], v[160:163]\n" \
"ds_read_b128 a[152:155], v28 offset:12288\n" \
"v_mfma_f32_16x16x32_bf16 v[176:179], a[188:191], a[156:159], v[176:179]\n" \
"ds_read_b128 a[216:219], v29 offset:12288\n" \
"v_mfma_f32_16x16x32_bf16 v[176:179], a[252:255], a[220:223], v[176:179]\n" \
"s_addk_i32 s84, 0x7c00\n" \
"v_mfma_f32_16x16x32_bf16 v[172:175], a[184:187], a[156:159], v[172:175]\n" \
"buffer_load_dwordx4 v27, s[68:71], s84 offen offset:2048 lds\n" \
"v_mfma_f32_16x16x32_bf16 v[172:175], a[248:251], a[220:223], v[172:175]\n" \
"ds_read_b64_tr_b16 a[230:231], v33 offset:4608\n" \
"v_mfma_f32_16x16x32_bf16 v[168:171], a[180:183], a[156:159], v[168:171]\n" \
"s_addk_i32 s84, 0x7c00\n" \
"v_mfma_f32_16x16x32_bf16 v[168:171], a[244:247], a[220:223], v[168:171]\n" \
"buffer_load_dwordx4 v27, s[68:71], s84 offen offset:3072 lds\n" \
"v_mfma_f32_16x16x32_bf16 v[164:167], a[176:179], a[156:159], v[164:167]\n" \
"ds_read_b64_tr_b16 a[172:173], v35 offset:0\n" \
"v_mfma_f32_16x16x32_bf16 v[164:167], a[240:243], a[220:223], v[164:167]\n" \
"ds_read_b128 a[156:159], v28 offset:14336\n" \
"v_mfma_f32_16x16x32_bf16 v[84:87], a[176:179], a[136:139], v[84:87]\n" \
"ds_read_b128 a[220:223], v29 offset:14336\n" \
"v_mfma_f32_16x16x32_bf16 v[84:87], a[240:243], a[200:203], v[84:87]\n" \
"s_add_u32 s89, s42, 0x0\n" \
"buffer_store_dwordx4 v[192:195], v20, s[80:83], s89 offen offset:2048 sc1\n" \
"v_mfma_f32_16x16x32_bf16 v[88:91], a[180:183], a[136:139], v[88:91]\n" \
"ds_read_b64_tr_b16 a[174:175], v35 offset:512\n" \
"v_mfma_f32_16x16x32_bf16 v[88:91], a[244:247], a[200:203], v[88:91]\n" \
"ds_read_b64_tr_b16 a[236:237], v35 offset:4096\n" \
"v_mfma_f32_16x16x32_bf16 v[92:95], a[184:187], a[136:139], v[92:95]\n" \
"ds_read_b64_tr_b16 a[238:239], v35 offset:4608\n" \
"v_mfma_f32_16x16x32_bf16 v[92:95], a[248:251], a[200:203], v[92:95]\n" \
"s_add_u32 m0, s30, 0x20000\n" \
"s_movk_i32 s84, 0x400\n" \
"v_mfma_f32_16x16x32_bf16 v[96:99], a[188:191], a[136:139], v[96:99]\n" \
"buffer_load_dwordx4 v24, s[76:79], s84 offen offset:0 lds\n" \
"v_mfma_f32_16x16x32_bf16 v[96:99], a[252:255], a[200:203], v[96:99]\n" \
"ds_read_b128 a[136:139], v28 offset:4096\n" \
"v_mfma_f32_16x16x32_bf16 v[112:115], a[188:191], a[140:143], v[112:115]\n" \
"ds_read_b128 a[200:203], v29 offset:4096\n" \
"v_mfma_f32_16x16x32_bf16 v[112:115], a[252:255], a[204:207], v[112:115]\n" \
"ds_read_b64_tr_b16 a[168:169], v34 offset:0\n" \
"v_mfma_f32_16x16x32_bf16 v[108:111], a[184:187], a[140:143], v[108:111]\n" \
"s_movk_i32 s84, 0x2400\n" \
"v_mfma_f32_16x16x32_bf16 v[108:111], a[248:251], a[204:207], v[108:111]\n" \
"buffer_load_dwordx4 v25, s[76:79], s84 offen offset:1024 lds\n" \
"v_mfma_f32_16x16x32_bf16 v[104:107], a[180:183], a[140:143], v[104:107]\n" \
"ds_read_b64_tr_b16 a[170:171], v34 offset:512\n" \
"v_mfma_f32_16x16x32_bf16 v[104:107], a[244:247], a[204:207], v[104:107]\n" \
"ds_read_b64_tr_b16 a[232:233], v34 offset:4096\n" \
"v_mfma_f32_16x16x32_bf16 v[100:103], a[176:179], a[140:143], v[100:103]\n" \
"s_add_u32 s6, s26, s6\n" \
"s_addc_u32 s9, s27, 0\n" \
"v_mfma_f32_16x16x32_bf16 v[100:103], a[240:243], a[204:207], v[100:103]\n" \
"ds_read_b128 a[140:143], v28 offset:6144\n" \
"v_mfma_f32_16x16x32_bf16 v[52:55], a[176:179], a[128:131], v[52:55]\n" \
"ds_read_b128 a[204:207], v29 offset:6144\n" \
"v_mfma_f32_16x16x32_bf16 v[52:55], a[240:243], a[192:195], v[52:55]\n" \
"s_movk_i32 s84, 0x4400\n" \
"v_mfma_f32_16x16x32_bf16 v[56:59], a[180:183], a[128:131], v[56:59]\n" \
"buffer_load_dwordx4 v24, s[76:79], s84 offen offset:2048 lds\n" \
"v_mfma_f32_16x16x32_bf16 v[56:59], a[244:247], a[192:195], v[56:59]\n" \
"s_movk_i32 s84, 0x6400\n" \
"v_mfma_f32_16x16x32_bf16 v[60:63], a[184:187], a[128:131], v[60:63]\n" \
"buffer_load_dwordx4 v25, s[76:79], s84 offen offset:3072 lds\n" \
"v_mfma_f32_16x16x32_bf16 v[60:63], a[248:251], a[192:195], v[60:63]\n" \
"ds_read_b64_tr_b16 a[234:235], v34 offset:4608\n" \
"v_mfma_f32_16x16x32_bf16 v[64:67], a[188:191], a[128:131], v[64:67]\n" \
"s_add_u32 s8, s6, 0x1000000\n" \
"s_addc_u32 s6, s9, 0\n" \
"v_mfma_f32_16x16x32_bf16 v[64:67], a[252:255], a[192:195], v[64:67]\n" \
"ds_read_b128 a[128:131], v28 offset:0\n" \
"s_and_b32 s9, s6, 0xffff\n" \
"v_mfma_f32_16x16x32_bf16 v[80:83], a[188:191], a[132:135], v[80:83]\n" \
"ds_read_b128 a[192:195], v29 offset:0\n" \
"s_lshl_b32 s6, s5, 16\n" \
"v_mfma_f32_16x16x32_bf16 v[80:83], a[252:255], a[196:199], v[80:83]\n" \
"s_sub_i32 s10, 0x100000, s6\n" \
"s_mov_b32 s6, 0x8000\n" \
"v_mfma_f32_16x16x32_bf16 v[76:79], a[184:187], a[132:135], v[76:79]\n" \
"s_cmp_lg_u32 s5, 0\n" \
"s_cselect_b32 s17, s11, s6\n" \
"v_mfma_f32_16x16x32_bf16 v[76:79], a[248:251], a[196:199], v[76:79]\n" \
"s_lshl_b32 s6, s3, 1\n" \
"s_lshl_b32 s5, s5, 3\n" \
"v_mfma_f32_16x16x32_bf16 v[72:75], a[180:183], a[132:135], v[72:75]\n" \
"s_and_b32 s6, s6, 4\n" \
"s_or_b32 s12, s5, s6\n" \
"v_mfma_f32_16x16x32_bf16 v[72:75], a[244:247], a[196:199], v[72:75]\n" \
"s_sub_i32 s5, s13, s5\n" \
"s_or_b32 s5, s5, s6\n" \
"v_mfma_f32_16x16x32_bf16 v[68:71], a[176:179], a[132:135], v[68:71]\n" \
"s_or_b32 s6, s12, s13\n" \
"s_lshl_b32 s4, s4, 2\n" \
"v_mfma_f32_16x16x32_bf16 v[68:71], a[240:243], a[196:199], v[68:71]\n" \
"ds_read_b128 a[132:135], v28 offset:2048\n" \
"ds_read_b128 a[196:199], v29 offset:2048\n" \
"s_waitcnt lgkmcnt(15)\n" \
"v_mfma_f32_16x16x32_bf16 a[0:3], a[160:163], a[144:147], a[0:3]\n" \
"s_add_u32 m0, s30, 0x21000\n" \
"s_addk_i32 s84, 0x3000\n" \
"v_mfma_f32_16x16x32_bf16 a[0:3], a[224:227], a[208:211], a[0:3]\n" \
"buffer_load_dwordx4 v24, s[76:79], s84 offen offset:0 lds\n" \
"v_mfma_f32_16x16x32_bf16 a[4:7], a[164:167], a[144:147], a[4:7]\n" \
"ds_read_b64_tr_b16 a[176:177], v36 offset:0\n" \
"s_mulk_i32 s6, 0x92\n" \
"v_mfma_f32_16x16x32_bf16 a[4:7], a[228:231], a[208:211], a[4:7]\n" \
"ds_read_b64_tr_b16 a[178:179], v36 offset:1024\n" \
"s_add_i32 s4, s4, 18\n" \
"s_waitcnt lgkmcnt(15)\n" \
"v_mfma_f32_16x16x32_bf16 a[12:15], a[172:175], a[144:147], a[12:15]\n" \
"ds_read_b64_tr_b16 a[240:241], v36 offset:8192\n" \
"s_mulk_i32 s5, 0x92\n" \
"s_waitcnt lgkmcnt(15)\n" \
"v_mfma_f32_16x16x32_bf16 a[12:15], a[236:239], a[208:211], a[12:15]\n" \
"s_add_u32 s88, s42, 0x0\n" \
"buffer_store_dwordx4 v[196:199], v20, s[80:83], s88 offen offset:3072 sc1\n" \
"s_waitcnt lgkmcnt(11)\n" \
"v_mfma_f32_16x16x32_bf16 a[8:11], a[144:147], a[168:171], a[8:11]\n" \
"ds_read_b64_tr_b16 a[242:243], v36 offset:9216\n" \
"s_add_i32 s6, s4, s6\n" \
"s_waitcnt lgkmcnt(8)\n" \
"v_mfma_f32_16x16x32_bf16 a[8:11], a[208:211], a[232:235], a[8:11]\n" \
"ds_read_b64_tr_b16 a[180:181], v37 offset:0\n" \
"s_add_i32 s5, s5, s4\n" \
"v_mfma_f32_16x16x32_bf16 a[24:27], a[148:151], a[168:171], a[24:27]\n" \
"ds_read_b64_tr_b16 a[182:183], v37 offset:1024\n" \
"s_lshl_b32 s4, s3, 11\n" \
"v_mfma_f32_16x16x32_bf16 a[24:27], a[212:215], a[232:235], a[24:27]\n" \
"ds_read_b64_tr_b16 a[244:245], v37 offset:8192\n" \
"s_lshl_b32 s15, s13, 12\n" \
"v_mfma_f32_16x16x32_bf16 a[28:31], a[172:175], a[148:151], a[28:31]\n" \
"s_lshl_b32 s16, s19, 14\n" \
"s_mul_i32 s19, s19, 0xe000\n" \
"v_mfma_f32_16x16x32_bf16 a[28:31], a[236:239], a[212:215], a[28:31]\n" \
"s_addk_i32 s84, 0x2000\n" \
"v_mfma_f32_16x16x32_bf16 a[20:23], a[164:167], a[148:151], a[20:23]\n" \
"buffer_load_dwordx4 v25, s[76:79], s84 offen offset:1024 lds\n" \
"v_mfma_f32_16x16x32_bf16 a[20:23], a[228:231], a[212:215], a[20:23]\n" \
"ds_read_b64_tr_b16 a[246:247], v37 offset:9216\n" \
"s_mov_b32 s11, 0x20000\n" \
"v_mfma_f32_16x16x32_bf16 a[16:19], a[160:163], a[148:151], a[16:19]\n" \
"ds_read_b64_tr_b16 a[184:185], v38 offset:0\n" \
"s_or_b32 s12, s4, 0xc000\n" \
"v_mfma_f32_16x16x32_bf16 a[16:19], a[224:227], a[212:215], a[16:19]\n" \
"ds_read_b64_tr_b16 a[186:187], v38 offset:1024\n" \
"s_lshl_b32 s4, s3, 8\n" \
"v_mfma_f32_16x16x32_bf16 a[32:35], a[160:163], a[152:155], a[32:35]\n" \
"s_addk_i32 s84, 0x2000\n" \
"v_mfma_f32_16x16x32_bf16 a[32:35], a[224:227], a[216:219], a[32:35]\n" \
"buffer_load_dwordx4 v24, s[76:79], s84 offen offset:2048 lds\n" \
"v_mfma_f32_16x16x32_bf16 a[36:39], a[164:167], a[152:155], a[36:39]\n" \
"ds_read_b64_tr_b16 a[248:249], v38 offset:8192\n" \
"s_or_b32 s22, s15, s16\n" \
"v_mfma_f32_16x16x32_bf16 a[36:39], a[228:231], a[216:219], a[36:39]\n" \
"ds_read_b64_tr_b16 a[250:251], v38 offset:9216\n" \
"s_or_b32 s23, s19, s15\n" \
"v_mfma_f32_16x16x32_bf16 a[44:47], a[172:175], a[152:155], a[44:47]\n" \
"ds_read_b64_tr_b16 a[188:189], v39 offset:0\n" \
"s_lshl_b32 s60, s6, 10\n" \
"v_mfma_f32_16x16x32_bf16 a[44:47], a[236:239], a[216:219], a[44:47]\n" \
"ds_read_b64_tr_b16 a[190:191], v39 offset:1024\n" \
"s_lshl_b32 s61, s5, 10\n" \
"v_mfma_f32_16x16x32_bf16 a[40:43], a[152:155], a[168:171], a[40:43]\n" \
"s_add_u32 s89, s42, 0x2000\n" \
"buffer_store_dwordx4 v[200:203], v20, s[80:83], s89 offen offset:0 sc1\n" \
"v_mfma_f32_16x16x32_bf16 a[40:43], a[216:219], a[232:235], a[40:43]\n" \
"ds_read_b64_tr_b16 a[252:253], v39 offset:8192\n" \
"s_lshl_b32 s14, s14, 15\n" \
"v_mfma_f32_16x16x32_bf16 a[56:59], a[156:159], a[168:171], a[56:59]\n" \
"ds_read_b64_tr_b16 a[254:255], v39 offset:9216\n" \
"s_or_b32 s20, s4, 0x26c00\n" \
"v_mfma_f32_16x16x32_bf16 a[56:59], a[220:223], a[232:235], a[56:59]\n" \
"s_or_b32 s13, s22, 0x8000\n" \
"s_add_i32 s22, s22, 0x14000\n" \
"v_mfma_f32_16x16x32_bf16 a[60:63], a[172:175], a[156:159], a[60:63]\n" \
"s_bitset1_b32 s23, 16\n" \
"s_or_b32 s24, s7, 0x5be8020\n" \
"v_mfma_f32_16x16x32_bf16 a[60:63], a[236:239], a[220:223], a[60:63]\n" \
"s_addk_i32 s84, 0x2000\n" \
"v_mfma_f32_16x16x32_bf16 a[52:55], a[164:167], a[156:159], a[52:55]\n" \
"buffer_load_dwordx4 v25, s[76:79], s84 offen offset:3072 lds\n" \
"v_mfma_f32_16x16x32_bf16 a[52:55], a[228:231], a[220:223], a[52:55]\n" \
"s_and_b32 s57, s27, 0xffff\n" \
".fill (L_pb + 73440 - .) / 4, 4, 0xBF800000\n" \
"L_pp18:\n" \
"s_cbranch_execz L_pret\n" \
"s_mov_b32 s58, 0x6000000\n" \
"v_mfma_f32_16x16x32_bf16 a[48:51], a[160:163], a[156:159], a[48:51]\n" \
"s_mov_b32 s56, s26\n" \
"s_mov_b32 s59, s11\n" \
"v_mfma_f32_16x16x32_bf16 a[48:51], a[224:227], a[220:223], a[48:51]\n" \
"s_add_i32 s60, s60, 0x3000000\n" \
"s_add_i32 s61, s61, 0x411c000\n" \
"v_mfma_f32_16x16x32_bf16 a[64:67], a[164:167], a[140:143], a[64:67]\n" \
"s_sub_i32 s19, 14, s18\n" \
"s_mov_b32 s21, 0x71800000\n" \
"v_mfma_f32_16x16x32_bf16 a[64:67], a[228:231], a[204:207], a[64:67]\n" \
"s_mov_b32 s4, L_gsw-L_gtr\n" \
"v_mfma_f32_16x16x32_bf16 a[80:83], a[172:175], a[140:143], a[80:83]\n" \
"s_cmp_eq_u32 s19, 0\n" \
"s_cselect_b32 s4, L_gs7-L_gtr, s4\n" \
"v_mfma_f32_16x16x32_bf16 a[80:83], a[236:239], a[204:207], a[80:83]\n" \
"s_cmp_eq_u32 s16, 0\n" \
"s_cselect_b32 s4, 0, s4\n" \
"v_mfma_f32_16x16x32_bf16 a[68:71], a[140:143], a[168:171], a[68:71]\n" \
"s_getpc_b64 s[28:29]\n" \
"L_gp1_%=:\n" \
"s_add_u32 s28, s28, L_gtr-L_gp1_%=\n" \
"s_addc_u32 s29, s29, 0\n" \
"v_mfma_f32_16x16x32_bf16 a[68:71], a[204:207], a[232:235], a[68:71]\n" \
"s_waitcnt vmcnt(11) lgkmcnt(0)\n" \
"s_barrier\n" \
"v_mfma_f32_16x16x32_bf16 v[116:119], a[176:179], a[144:147], v[116:119]\n" \
"s_add_u32 m0, s40, 0x0\n" \
"s_add_u32 s85, s39, 0x10000\n" \
"v_mfma_f32_16x16x32_bf16 v[116:119], a[240:243], a[208:211], v[116:119]\n" \
"buffer_load_dwordx4 v20, s[80:83], s85 offen sc1 lds\n" \
"v_mfma_f32_16x16x32_bf16 v[120:123], a[180:183], a[144:147], v[120:123]\n" \
"ds_read_b64_tr_b16 a[160:161], v32 offset:32768\n" \
"v_mfma_f32_16x16x32_bf16 v[120:123], a[244:247], a[208:211], v[120:123]\n" \
"ds_read_b64_tr_b16 a[162:163], v32 offset:33280\n" \
"v_mfma_f32_16x16x32_bf16 v[124:127], a[184:187], a[144:147], v[124:127]\n" \
"s_add_u32 s28, s28, s4\n" \
"s_addc_u32 s29, s29, 0\n" \
"v_mfma_f32_16x16x32_bf16 v[124:127], a[248:251], a[208:211], v[124:127]\n" \
"v_mfma_f32_16x16x32_bf16 v[128:131], a[188:191], a[144:147], v[128:131]\n" \
"ds_read_b64_tr_b16 a[224:225], v32 offset:36864\n" \
"v_mfma_f32_16x16x32_bf16 v[128:131], a[252:255], a[208:211], v[128:131]\n" \
"ds_read_b128 a[144:147], v28 offset:40960\n" \
"v_mfma_f32_16x16x32_bf16 v[144:147], a[188:191], a[148:151], v[144:147]\n" \
"ds_read_b128 a[208:211], v29 offset:40960\n" \
"v_mfma_f32_16x16x32_bf16 v[144:147], a[252:255], a[212:215], v[144:147]\n" \
"s_add_u32 m0, s40, 0x1000\n" \
"s_add_u32 s85, s39, 0x11000\n" \
"v_mfma_f32_16x16x32_bf16 v[140:143], a[184:187], a[148:151], v[140:143]\n" \
"buffer_load_dwordx4 v20, s[80:83], s85 offen sc1 lds\n" \
"v_mfma_f32_16x16x32_bf16 v[140:143], a[248:251], a[212:215], v[140:143]\n" \
"ds_read_b64_tr_b16 a[226:227], v32 offset:37376\n" \
"v_mfma_f32_16x16x32_bf16 v[136:139], a[180:183], a[148:151], v[136:139]\n" \
"v_mfma_f32_16x16x32_bf16 v[136:139], a[244:247], a[212:215], v[136:139]\n" \
"ds_read_b64_tr_b16 a[164:165], v33 offset:32768\n" \
"v_mfma_f32_16x16x32_bf16 v[132:135], a[176:179], a[148:151], v[132:135]\n" \
"v_mfma_f32_16x16x32_bf16 v[132:135], a[240:243], a[212:215], v[132:135]\n" \
"ds_read_b128 a[148:151], v28 offset:43008\n" \
"v_mfma_f32_16x16x32_bf16 v[148:151], a[176:179], a[152:155], v[148:151]\n" \
"ds_read_b128 a[212:215], v29 offset:43008\n" \
"v_mfma_f32_16x16x32_bf16 v[148:151], a[240:243], a[216:219], v[148:151]\n" \
"s_add_u32 m0, s40, 0x3000\n" \
"s_movk_i32 s84, 0x400\n" \
"v_mfma_f32_16x16x32_bf16 v[152:155], a[180:183], a[152:155], v[152:155]\n" \
"buffer_load_dwordx4 v21, s[76:79], s84 offen offset:0 lds\n" \
"v_mfma_f32_16x16x32_bf16 v[152:155], a[244:247], a[216:219], v[152:155]\n" \
"ds_read_b64_tr_b16 a[166:167], v33 offset:33280\n" \
"v_mfma_f32_16x16x32_bf16 v[156:159], a[184:187], a[152:155], v[156:159]\n" \
"ds_read_b64_tr_b16 a[228:229], v33 offset:36864\n" \
"v_mfma_f32_16x16x32_bf16 v[156:159], a[248:251], a[216:219], v[156:159]\n" \
"s_add_u32 m0, s31, 0x0\n" \
"s_mov_b32 s84, 0x400000\n" \
"v_mfma_f32_16x16x32_bf16 v[160:163], a[188:191], a[152:155], v[160:163]\n" \
"buffer_load_dwordx4 v26, s[68:71], s84 offen offset:0 lds\n" \
"v_mfma_f32_16x16x32_bf16 v[160:163], a[252:255], a[216:219], v[160:163]\n" \
"ds_read_b128 a[152:155], v28 offset:45056\n" \
"v_mfma_f32_16x16x32_bf16 v[176:179], a[188:191], a[156:159], v[176:179]\n" \
"ds_read_b128 a[216:219], v29 offset:45056\n" \
"v_mfma_f32_16x16x32_bf16 v[176:179], a[252:255], a[220:223], v[176:179]\n" \
"ds_read_b64_tr_b16 a[230:231], v33 offset:37376\n" \
"v_mfma_f32_16x16x32_bf16 v[172:175], a[184:187], a[156:159], v[172:175]\n" \
"ds_read_b64_tr_b16 a[172:173], v35 offset:32768\n" \
"v_mfma_f32_16x16x32_bf16 v[172:175], a[248:251], a[220:223], v[172:175]\n" \
"v_mfma_f32_16x16x32_bf16 v[168:171], a[180:183], a[156:159], v[168:171]\n" \
"s_add_u32 s88, s42, 0x2000\n" \
"buffer_store_dwordx4 v[204:207], v20, s[80:83], s88 offen offset:1024 sc1\n" \
"v_mfma_f32_16x16x32_bf16 v[168:171], a[244:247], a[220:223], v[168:171]\n" \
"ds_read_b64_tr_b16 a[174:175], v35 offset:33280\n" \
"v_mfma_f32_16x16x32_bf16 v[164:167], a[176:179], a[156:159], v[164:167]\n" \
"v_mfma_f32_16x16x32_bf16 v[164:167], a[240:243], a[220:223], v[164:167]\n" \
"ds_read_b128 a[156:159], v28 offset:47104\n" \
"v_mfma_f32_16x16x32_bf16 v[84:87], a[176:179], a[136:139], v[84:87]\n" \
"ds_read_b128 a[220:223], v29 offset:47104\n" \
"v_mfma_f32_16x16x32_bf16 v[84:87], a[240:243], a[200:203], v[84:87]\n" \
"ds_read_b64_tr_b16 a[236:237], v35 offset:36864\n" \
"v_mfma_f32_16x16x32_bf16 v[88:91], a[180:183], a[136:139], v[88:91]\n" \
"v_mfma_f32_16x16x32_bf16 v[88:91], a[244:247], a[200:203], v[88:91]\n" \
"s_addk_i32 s84, 0x7c00\n" \
"s_nop 0\n" \
"v_mfma_f32_16x16x32_bf16 v[92:95], a[184:187], a[136:139], v[92:95]\n" \
"buffer_load_dwordx4 v26, s[68:71], s84 offen offset:1024 lds\n" \
"v_mfma_f32_16x16x32_bf16 v[92:95], a[248:251], a[200:203], v[92:95]\n" \
"ds_read_b64_tr_b16 a[238:239], v35 offset:37376\n" \
"v_mfma_f32_16x16x32_bf16 v[96:99], a[188:191], a[136:139], v[96:99]\n" \
"v_mfma_f32_16x16x32_bf16 v[96:99], a[252:255], a[200:203], v[96:99]\n" \
"ds_read_b128 a[136:139], v28 offset:36864\n" \
"v_mfma_f32_16x16x32_bf16 v[112:115], a[188:191], a[140:143], v[112:115]\n" \
"ds_read_b128 a[200:203], v29 offset:36864\n" \
"v_mfma_f32_16x16x32_bf16 v[112:115], a[252:255], a[204:207], v[112:115]\n" \
"ds_read_b64_tr_b16 a[168:169], v34 offset:32768\n" \
"v_mfma_f32_16x16x32_bf16 v[108:111], a[184:187], a[140:143], v[108:111]\n" \
"s_addk_i32 s84, 0x7c00\n" \
"s_nop 0\n" \
"v_mfma_f32_16x16x32_bf16 v[108:111], a[248:251], a[204:207], v[108:111]\n" \
"buffer_load_dwordx4 v27, s[68:71], s84 offen offset:2048 lds\n" \
"v_mfma_f32_16x16x32_bf16 v[104:107], a[180:183], a[140:143], v[104:107]\n" \
"ds_read_b64_tr_b16 a[170:171], v34 offset:33280\n" \
"v_mfma_f32_16x16x32_bf16 v[104:107], a[244:247], a[204:207], v[104:107]\n" \
"ds_read_b64_tr_b16 a[232:233], v34 offset:36864\n" \
"v_mfma_f32_16x16x32_bf16 v[100:103], a[176:179], a[140:143], v[100:103]\n" \
"v_mfma_f32_16x16x32_bf16 v[100:103], a[240:243], a[204:207], v[100:103]\n" \
"ds_read_b128 a[140:143], v28 offset:38912\n" \
"v_mfma_f32_16x16x32_bf16 v[52:55], a[176:179], a[128:131], v[52:55]\n" \
"ds_read_b128 a[204:207], v29 offset:38912\n" \
"v_mfma_f32_16x16x32_bf16 v[52:55], a[240:243], a[192:195], v[52:55]\n" \
"s_addk_i32 s84, 0x7c00\n" \
"s_nop 0\n" \
"v_mfma_f32_16x16x32_bf16 v[56:59], a[180:183], a[128:131], v[56:59]\n" \
"buffer_load_dwordx4 v27, s[68:71], s84 offen offset:3072 lds\n" \
"v_mfma_f32_16x16x32_bf16 v[56:59], a[244:247], a[192:195], v[56:59]\n" \
"ds_read_b64_tr_b16 a[234:235], v34 offset:37376\n" \
"v_mfma_f32_16x16x32_bf16 v[60:63], a[184:187], a[128:131], v[60:63]\n" \
"v_mfma_f32_16x16x32_bf16 v[60:63], a[248:251], a[192:195], v[60:63]\n" \
"v_mfma_f32_16x16x32_bf16 v[64:67], a[188:191], a[128:131], v[64:67]\n" \
"v_mfma_f32_16x16x32_bf16 v[64:67], a[252:255], a[192:195], v[64:67]\n" \
"ds_read_b128 a[128:131], v28 offset:32768\n" \
"v_mfma_f32_16x16x32_bf16 v[80:83], a[188:191], a[132:135], v[80:83]\n" \
"ds_read_b128 a[192:195], v29 offset:32768\n" \
"v_mfma_f32_16x16x32_bf16 v[80:83], a[252:255], a[196:199], v[80:83]\n" \
"v_mfma_f32_16x16x32_bf16 v[76:79], a[184:187], a[132:135], v[76:79]\n" \
"v_mfma_f32_16x16x32_bf16 v[76:79], a[248:251], a[196:199], v[76:79]\n" \
"v_mfma_f32_16x16x32_bf16 v[72:75], a[180:183], a[132:135], v[72:75]\n" \
"v_mfma_f32_16x16x32_bf16 v[72:75], a[244:247], a[196:199], v[72:75]\n" \
"v_mfma_f32_16x16x32_bf16 v[68:71], a[176:179], a[132:135], v[68:71]\n" \
"v_mfma_f32_16x16x32_bf16 v[68:71], a[240:243], a[196:199], v[68:71]\n" \
"ds_read_b128 a[132:135], v28 offset:34816\n" \
"ds_read_b128 a[196:199], v29 offset:34816\n" \
"s_waitcnt lgkmcnt(15)\n" \
"s_nop 0\n" \
"v_mfma_f32_16x16x32_bf16 a[0:3], a[160:163], a[144:147], a[0:3]\n" \
"s_add_u32 s89, s42, 0x2000\n" \
"buffer_store_dwordx4 v[208:211], v20, s[80:83], s89 offen offset:2048 sc1\n" \
"v_mfma_f32_16x16x32_bf16 a[0:3], a[224:227], a[208:211], a[0:3]\n" \
"ds_read_b64_tr_b16 a[176:177], v36 offset:32768\n" \
"v_mfma_f32_16x16x32_bf16 a[4:7], a[164:167], a[144:147], a[4:7]\n" \
"ds_read_b64_tr_b16 a[178:179], v36 offset:33792\n" \
"v_mfma_f32_16x16x32_bf16 a[4:7], a[228:231], a[208:211], a[4:7]\n" \
"ds_read_b64_tr_b16 a[240:241], v36 offset:40960\n" \
"v_mfma_f32_16x16x32_bf16 a[12:15], a[172:175], a[144:147], a[12:15]\n" \
"ds_read_b64_tr_b16 a[242:243], v36 offset:41984\n" \
"s_waitcnt lgkmcnt(15)\n" \
"v_mfma_f32_16x16x32_bf16 a[12:15], a[236:239], a[208:211], a[12:15]\n" \
"s_waitcnt lgkmcnt(12)\n" \
"v_mfma_f32_16x16x32_bf16 a[8:11], a[144:147], a[168:171], a[8:11]\n" \
"ds_read_b64_tr_b16 a[180:181], v37 offset:32768\n" \
"s_waitcnt lgkmcnt(9)\n" \
"s_nop 0\n" \
"v_mfma_f32_16x16x32_bf16 a[8:11], a[208:211], a[232:235], a[8:11]\n" \
"v_mfma_f32_16x16x32_bf16 a[24:27], a[148:151], a[168:171], a[24:27]\n" \
"ds_read_b64_tr_b16 a[182:183], v37 offset:33792\n" \
"v_mfma_f32_16x16x32_bf16 a[24:27], a[212:215], a[232:235], a[24:27]\n" \
"ds_read_b64_tr_b16 a[244:245], v37 offset:40960\n" \
"v_mfma_f32_16x16x32_bf16 a[28:31], a[172:175], a[148:151], a[28:31]\n" \
"v_mfma_f32_16x16x32_bf16 a[28:31], a[236:239], a[212:215], a[28:31]\n" \
"ds_read_b64_tr_b16 a[246:247], v37 offset:41984\n" \
"v_mfma_f32_16x16x32_bf16 a[20:23], a[164:167], a[148:151], a[20:23]\n" \
"v_mfma_f32_16x16x32_bf16 a[20:23], a[228:231], a[212:215], a[20:23]\n" \
"ds_read_b64_tr_b16 a[184:185], v38 offset:32768\n" \
"v_mfma_f32_16x16x32_bf16 a[16:19], a[160:163], a[148:151], a[16:19]\n" \
"ds_read_b64_tr_b16 a[186:187], v38 offset:33792\n" \
"v_mfma_f32_16x16x32_bf16 a[16:19], a[224:227], a[212:215], a[16:19]\n" \
"v_mfma_f32_16x16x32_bf16 a[32:35], a[160:163], a[152:155], a[32:35]\n" \
"s_add_u32 s88, s42, 0x2000\n" \
"buffer_store_dwordx4 v[212:215], v20, s[80:83], s88 offen offset:3072 sc1\n" \
"v_mfma_f32_16x16x32_bf16 a[32:35], a[224:227], a[216:219], a[32:35]\n" \
"ds_read_b64_tr_b16 a[248:249], v38 offset:40960\n" \
"v_mfma_f32_16x16x32_bf16 a[36:39], a[164:167], a[152:155], a[36:39]\n" \
"v_lshlrev_b32_e32 v45, 3, v20\n" \
"v_add_u32_e32 v45, s30, v45\n" \
"v_mfma_f32_16x16x32_bf16 a[36:39], a[228:231], a[216:219], a[36:39]\n" \
"s_getpc_b64 s[86:87]\n" \
"L_ct1_%=:\n" \
"s_add_u32 s86, s86, L_ap0-L_ct1_%=\n" \
"s_addc_u32 s87, s87, 0\n" \
"s_add_u32 m0, s40, 0x2000\n" \
"v_mfma_f32_16x16x32_bf16 a[44:47], a[172:175], a[152:155], a[44:47]\n" \
"global_load_lds_dword v45, s[86:87]\n" \
"v_mfma_f32_16x16x32_bf16 a[44:47], a[236:239], a[216:219], a[44:47]\n" \
"ds_read_b64_tr_b16 a[250:251], v38 offset:41984\n" \
"v_mfma_f32_16x16x32_bf16 a[40:43], a[152:155], a[168:171], a[40:43]\n" \
"ds_read_b64_tr_b16 a[188:189], v39 offset:32768\n" \
"v_mfma_f32_16x16x32_bf16 a[40:43], a[216:219], a[232:235], a[40:43]\n" \
"ds_read_b64_tr_b16 a[190:191], v39 offset:33792\n" \
"v_mfma_f32_16x16x32_bf16 a[56:59], a[156:159], a[168:171], a[56:59]\n" \
"ds_read_b64_tr_b16 a[252:253], v39 offset:40960\n" \
"v_mfma_f32_16x16x32_bf16 a[56:59], a[220:223], a[232:235], a[56:59]\n" \
"ds_read_b64_tr_b16 a[254:255], v39 offset:41984\n" \
"v_mfma_f32_16x16x32_bf16 a[60:63], a[172:175], a[156:159], a[60:63]\n" \
"v_mfma_f32_16x16x32_bf16 a[60:63], a[236:239], a[220:223], a[60:63]\n" \
"v_mfma_f32_16x16x32_bf16 a[52:55], a[164:167], a[156:159], a[52:55]\n" \
"v_mfma_f32_16x16x32_bf16 a[52:55], a[228:231], a[220:223], a[52:55]\n" \
"v_mfma_f32_16x16x32_bf16 a[48:51], a[160:163], a[156:159], a[48:51]\n" \
"v_mfma_f32_16x16x32_bf16 a[48:51], a[224:227], a[220:223], a[48:51]\n" \
"v_mfma_f32_16x16x32_bf16 a[64:67], a[164:167], a[140:143], a[64:67]\n" \
"v_mfma_f32_16x16x32_bf16 a[64:67], a[228:231], a[204:207], a[64:67]\n" \
"v_mfma_f32_16x16x32_bf16 a[80:83], a[172:175], a[140:143], a[80:83]\n" \
"v_mfma_f32_16x16x32_bf16 a[80:83], a[236:239], a[204:207], a[80:83]\n" \
"v_mfma_f32_16x16x32_bf16 a[68:71], a[140:143], a[168:171], a[68:71]\n" \
"v_mfma_f32_16x16x32_bf16 a[68:71], a[204:207], a[232:235], a[68:71]\n" \
"s_waitcnt vmcnt(3) lgkmcnt(0)\n" \
"s_barrier\n" \
"v_xor_b32_e32 v28, 0x20000, v28\n" \
"v_xor_b32_e32 v29, 0x20000, v29\n" \
"v_mfma_f32_16x16x32_bf16 v[116:119], a[176:179], a[144:147], v[116:119]\n" \
"ds_read_b64_tr_b16 a[160:161], v32 offset:0\n" \
"v_accvgpr_read_b32 v184, a0\n" \
"v_mfma_f32_16x16x32_bf16 v[116:119], a[240:243], a[208:211], v[116:119]\n" \
"v_accvgpr_read_b32 v185, a1\n" \
"v_accvgpr_read_b32 v186, a2\n" \
"v_mfma_f32_16x16x32_bf16 v[120:123], a[180:183], a[144:147], v[120:123]\n" \
"v_accvgpr_read_b32 v187, a3\n" \
"v_accvgpr_read_b32 v188, a4\n" \
"v_mfma_f32_16x16x32_bf16 v[120:123], a[244:247], a[208:211], v[120:123]\n" \
"ds_read_b64_tr_b16 a[162:163], v32 offset:512\n" \
"v_accvgpr_read_b32 v189, a5\n" \
"v_mfma_f32_16x16x32_bf16 v[124:127], a[184:187], a[144:147], v[124:127]\n" \
"v_accvgpr_read_b32 v190, a6\n" \
"v_accvgpr_read_b32 v191, a7\n" \
"v_mfma_f32_16x16x32_bf16 v[124:127], a[248:251], a[208:211], v[124:127]\n" \
"v_accvgpr_read_b32 v196, a12\n" \
"v_accvgpr_read_b32 v197, a13\n" \
"v_mfma_f32_16x16x32_bf16 v[128:131], a[188:191], a[144:147], v[128:131]\n" \
"ds_read_b64_tr_b16 a[224:225], v32 offset:4096\n" \
"v_accvgpr_read_b32 v198, a14\n" \
"v_mfma_f32_16x16x32_bf16 v[128:131], a[252:255], a[208:211], v[128:131]\n" \
"ds_read_b128 a[144:147], v28 offset:8192\n" \
"v_accvgpr_read_b32 v199, a15\n" \
"v_mfma_f32_16x16x32_bf16 v[144:147], a[188:191], a[148:151], v[144:147]\n" \
"ds_read_b128 a[208:211], v29 offset:8192\n" \
"v_accvgpr_read_b32 v192, a8\n" \
"v_mfma_f32_16x16x32_bf16 v[144:147], a[252:255], a[212:215], v[144:147]\n" \
"ds_read_b64_tr_b16 a[226:227], v32 offset:4608\n" \
"v_accvgpr_read_b32 v193, a9\n" \
"v_mfma_f32_16x16x32_bf16 v[140:143], a[184:187], a[148:151], v[140:143]\n" \
"v_accvgpr_read_b32 v194, a10\n" \
"v_accvgpr_read_b32 v195, a11\n" \
"v_mfma_f32_16x16x32_bf16 v[140:143], a[248:251], a[212:215], v[140:143]\n" \
"v_accvgpr_read_b32 v208, a24\n" \
"v_accvgpr_read_b32 v209, a25\n" \
"v_mfma_f32_16x16x32_bf16 v[136:139], a[180:183], a[148:151], v[136:139]\n" \
"v_accvgpr_read_b32 v210, a26\n" \
"v_accvgpr_read_b32 v211, a27\n" \
"v_mfma_f32_16x16x32_bf16 v[136:139], a[244:247], a[212:215], v[136:139]\n" \
"ds_read_b64_tr_b16 a[164:165], v33 offset:0\n" \
"v_accvgpr_read_b32 v212, a28\n" \
"v_mfma_f32_16x16x32_bf16 v[132:135], a[176:179], a[148:151], v[132:135]\n" \
"v_accvgpr_read_b32 v213, a29\n" \
"v_accvgpr_read_b32 v214, a30\n" \
"v_mfma_f32_16x16x32_bf16 v[132:135], a[240:243], a[212:215], v[132:135]\n" \
"ds_read_b128 a[148:151], v28 offset:10240\n" \
"v_accvgpr_read_b32 v215, a31\n" \
"v_mfma_f32_16x16x32_bf16 v[148:151], a[176:179], a[152:155], v[148:151]\n" \
"ds_read_b128 a[212:215], v29 offset:10240\n" \
"v_accvgpr_read_b32 v204, a20\n" \
"v_mfma_f32_16x16x32_bf16 v[148:151], a[240:243], a[216:219], v[148:151]\n" \
"ds_read_b64_tr_b16 a[166:167], v33 offset:512\n" \
"v_accvgpr_read_b32 v205, a21\n" \
"v_mfma_f32_16x16x32_bf16 v[152:155], a[180:183], a[152:155], v[152:155]\n" \
"v_accvgpr_read_b32 v206, a22\n" \
"v_accvgpr_read_b32 v207, a23\n" \
"v_mfma_f32_16x16x32_bf16 v[152:155], a[244:247], a[216:219], v[152:155]\n" \
"ds_read_b64_tr_b16 a[228:229], v33 offset:4096\n" \
"v_accvgpr_read_b32 v200, a16\n" \
"v_mfma_f32_16x16x32_bf16 v[156:159], a[184:187], a[152:155], v[156:159]\n" \
"v_accvgpr_read_b32 v201, a17\n" \
"v_accvgpr_read_b32 v202, a18\n" \
"v_mfma_f32_16x16x32_bf16 v[156:159], a[248:251], a[216:219], v[156:159]\n" \
"v_accvgpr_read_b32 v203, a19\n" \
"v_accvgpr_read_b32 v216, a32\n" \
"v_mfma_f32_16x16x32_bf16 v[160:163], a[188:191], a[152:155], v[160:163]\n" \
"ds_read_b64_tr_b16 a[230:231], v33 offset:4608\n" \
"v_accvgpr_read_b32 v217, a33\n" \
"v_mfma_f32_16x16x32_bf16 v[160:163], a[252:255], a[216:219], v[160:163]\n" \
"ds_read_b128 a[152:155], v28 offset:12288\n" \
"v_accvgpr_read_b32 v218, a34\n" \
"v_mfma_f32_16x16x32_bf16 v[176:179], a[188:191], a[156:159], v[176:179]\n" \
"ds_read_b128 a[216:219], v29 offset:12288\n" \
"v_accvgpr_read_b32 v219, a35\n" \
"v_mfma_f32_16x16x32_bf16 v[176:179], a[252:255], a[220:223], v[176:179]\n" \
"v_accvgpr_read_b32 v220, a36\n" \
"v_accvgpr_read_b32 v221, a37\n" \
"v_mfma_f32_16x16x32_bf16 v[172:175], a[184:187], a[156:159], v[172:175]\n" \
"ds_read_b64_tr_b16 a[172:173], v35 offset:0\n" \
"v_accvgpr_read_b32 v222, a38\n" \
"v_mfma_f32_16x16x32_bf16 v[172:175], a[248:251], a[220:223], v[172:175]\n" \
"v_accvgpr_read_b32 v223, a39\n" \
"v_accvgpr_read_b32 v228, a44\n" \
"v_mfma_f32_16x16x32_bf16 v[168:171], a[180:183], a[156:159], v[168:171]\n" \
"v_accvgpr_read_b32 v229, a45\n" \
"v_accvgpr_read_b32 v230, a46\n" \
"v_mfma_f32_16x16x32_bf16 v[168:171], a[244:247], a[220:223], v[168:171]\n" \
"ds_read_b64_tr_b16 a[174:175], v35 offset:512\n" \
"v_accvgpr_read_b32 v231, a47\n" \
"v_mfma_f32_16x16x32_bf16 v[164:167], a[176:179], a[156:159], v[164:167]\n" \
"v_accvgpr_read_b32 v224, a40\n" \
"v_accvgpr_read_b32 v225, a41\n" \
"v_mfma_f32_16x16x32_bf16 v[164:167], a[240:243], a[220:223], v[164:167]\n" \
"ds_read_b128 a[156:159], v28 offset:14336\n" \
"v_accvgpr_read_b32 v226, a42\n" \
"v_mfma_f32_16x16x32_bf16 v[84:87], a[176:179], a[136:139], v[84:87]\n" \
"ds_read_b128 a[220:223], v29 offset:14336\n" \
"v_accvgpr_read_b32 v227, a43\n" \
"v_mfma_f32_16x16x32_bf16 v[84:87], a[240:243], a[200:203], v[84:87]\n" \
"ds_read_b64_tr_b16 a[236:237], v35 offset:4096\n" \
"v_accvgpr_read_b32 v240, a56\n" \
"v_mfma_f32_16x16x32_bf16 v[88:91], a[180:183], a[136:139], v[88:91]\n" \
"v_accvgpr_read_b32 v241, a57\n" \
"v_accvgpr_read_b32 v242, a58\n" \
"v_mfma_f32_16x16x32_bf16 v[88:91], a[244:247], a[200:203], v[88:91]\n" \
"ds_read_b64_tr_b16 a[238:239], v35 offset:4608\n" \
"v_accvgpr_read_b32 v243, a59\n" \
"v_mfma_f32_16x16x32_bf16 v[92:95], a[184:187], a[136:139], v[92:95]\n" \
"v_accvgpr_read_b32 v244, a60\n" \
"v_accvgpr_read_b32 v245, a61\n" \
"v_mfma_f32_16x16x32_bf16 v[92:95], a[248:251], a[200:203], v[92:95]\n" \
"v_accvgpr_read_b32 v246, a62\n" \
"v_accvgpr_read_b32 v247, a63\n" \
"v_mfma_f32_16x16x32_bf16 v[96:99], a[188:191], a[136:139], v[96:99]\n" \
"v_accvgpr_read_b32 v236, a52\n" \
"v_accvgpr_read_b32 v237, a53\n" \
"v_mfma_f32_16x16x32_bf16 v[96:99], a[252:255], a[200:203], v[96:99]\n" \
"ds_read_b128 a[136:139], v28 offset:4096\n" \
"v_accvgpr_read_b32 v238, a54\n" \
"v_mfma_f32_16x16x32_bf16 v[112:115], a[188:191], a[140:143], v[112:115]\n" \
"ds_read_b128 a[200:203], v29 offset:4096\n" \
"v_accvgpr_read_b32 v239, a55\n" \
"v_mfma_f32_16x16x32_bf16 v[112:115], a[252:255], a[204:207], v[112:115]\n" \
"ds_read_b64_tr_b16 a[168:169], v34 offset:0\n" \
"v_accvgpr_read_b32 v232, a48\n" \
"v_mfma_f32_16x16x32_bf16 v[108:111], a[184:187], a[140:143], v[108:111]\n" \
"ds_read_b64_tr_b16 a[170:171], v34 offset:512\n" \
"v_accvgpr_read_b32 v233, a49\n" \
"v_mfma_f32_16x16x32_bf16 v[108:111], a[248:251], a[204:207], v[108:111]\n" \
"v_accvgpr_read_b32 v234, a50\n" \
"v_accvgpr_read_b32 v235, a51\n" \
"v_mfma_f32_16x16x32_bf16 v[104:107], a[180:183], a[140:143], v[104:107]\n" \
"v_accvgpr_read_b32 v180, a64\n" \
"v_accvgpr_read_b32 v181, a65\n" \
"v_mfma_f32_16x16x32_bf16 v[104:107], a[244:247], a[204:207], v[104:107]\n" \
"ds_read_b64_tr_b16 a[232:233], v34 offset:4096\n" \
"v_accvgpr_read_b32 v182, a66\n" \
"v_mfma_f32_16x16x32_bf16 v[100:103], a[176:179], a[140:143], v[100:103]\n" \
"v_accvgpr_read_b32 v183, a67\n" \
"v_accvgpr_read_b32 v48, a80\n" \
"v_mfma_f32_16x16x32_bf16 v[100:103], a[240:243], a[204:207], v[100:103]\n" \
"ds_read_b128 a[140:143], v28 offset:6144\n" \
"v_accvgpr_read_b32 v49, a81\n" \
"v_mfma_f32_16x16x32_bf16 v[52:55], a[176:179], a[128:131], v[52:55]\n" \
"ds_read_b128 a[204:207], v29 offset:6144\n" \
"v_accvgpr_read_b32 v50, a82\n" \
"v_mfma_f32_16x16x32_bf16 v[52:55], a[240:243], a[192:195], v[52:55]\n" \
"ds_read_b64_tr_b16 a[234:235], v34 offset:4608\n" \
"v_accvgpr_read_b32 v51, a83\n" \
"v_mfma_f32_16x16x32_bf16 v[56:59], a[180:183], a[128:131], v[56:59]\n" \
"v_accvgpr_read_b32 v252, a68\n" \
"v_accvgpr_read_b32 v253, a69\n" \
"v_mfma_f32_16x16x32_bf16 v[56:59], a[244:247], a[192:195], v[56:59]\n" \
"v_accvgpr_read_b32 v254, a70\n" \
"v_accvgpr_read_b32 v255, a71\n" \
"v_mfma_f32_16x16x32_bf16 v[60:63], a[184:187], a[128:131], v[60:63]\n" \
"v_lshrrev_b32_e64 v24, 9, v20\n" \
"v_and_b32_e32 v26, 0x1ff, v20\n" \
"v_mfma_f32_16x16x32_bf16 v[60:63], a[248:251], a[192:195], v[60:63]\n" \
"v_lshl_add_u32 v24, v24, 3, v26\n" \
"v_lshrrev_b32_e64 v25, 8, v20\n" \
"v_mfma_f32_16x16x32_bf16 v[64:67], a[188:191], a[128:131], v[64:67]\n" \
"v_mul_u32_u24_e32 v25, 0x24700, v25\n" \
"v_add_u32_e64 v25, v25, v20\n" \
"v_mfma_f32_16x16x32_bf16 v[64:67], a[252:255], a[192:195], v[64:67]\n" \
"ds_read_b128 a[128:131], v28 offset:0\n" \
"v_lshrrev_b32_e64 v26, 2, v20\n" \
"v_mfma_f32_16x16x32_bf16 v[80:83], a[188:191], a[132:135], v[80:83]\n" \
"ds_read_b128 a[192:195], v29 offset:0\n" \
"v_add_u32_e64 v26, s45, v26\n" \
"v_mfma_f32_16x16x32_bf16 v[80:83], a[252:255], a[196:199], v[80:83]\n" \
"v_add_u32_e32 v27, s46, v20\n" \
"v_add_u32_e32 v21, s43, v27\n" \
"v_mfma_f32_16x16x32_bf16 v[76:79], a[184:187], a[132:135], v[76:79]\n" \
"v_mfma_f32_16x16x32_bf16 v[76:79], a[248:251], a[196:199], v[76:79]\n" \
"v_mfma_f32_16x16x32_bf16 v[72:75], a[180:183], a[132:135], v[72:75]\n" \
"v_mfma_f32_16x16x32_bf16 v[72:75], a[244:247], a[196:199], v[72:75]\n" \
"v_mfma_f32_16x16x32_bf16 v[68:71], a[176:179], a[132:135], v[68:71]\n" \
"v_mfma_f32_16x16x32_bf16 v[68:71], a[240:243], a[196:199], v[68:71]\n" \
"ds_read_b128 a[132:135], v28 offset:2048\n" \
"ds_read_b128 a[196:199], v29 offset:2048\n" \
"s_waitcnt lgkmcnt(15)\n" \
"s_nop 0\n" \
"v_mfma_f32_16x16x32_bf16 v[184:187], a[160:163], a[144:147], v[184:187]\n" \
"v_bfe_u32 v44, v20, 2, 5\n" \
"ds_read_b64_tr_b16 a[176:177], v36 offset:0\n" \
"v_mfma_f32_16x16x32_bf16 v[184:187], a[224:227], a[208:211], v[184:187]\n" \
"buffer_load_dword v44, v44, s[80:83], s38 offen offset:0 sc1\n" \
"v_mfma_f32_16x16x32_bf16 v[188:191], a[164:167], a[144:147], v[188:191]\n" \
"ds_read_b64_tr_b16 a[178:179], v36 offset:1024\n" \
"v_mfma_f32_16x16x32_bf16 v[188:191], a[228:231], a[208:211], v[188:191]\n" \
"ds_read_b64_tr_b16 a[240:241], v36 offset:8192\n" \
"v_mfma_f32_16x16x32_bf16 v[196:199], a[172:175], a[144:147], v[196:199]\n" \
"ds_read_b64_tr_b16 a[242:243], v36 offset:9216\n" \
"s_waitcnt lgkmcnt(15)\n" \
"v_mfma_f32_16x16x32_bf16 v[196:199], a[236:239], a[208:211], v[196:199]\n" \
"s_waitcnt lgkmcnt(12)\n" \
"v_mfma_f32_16x16x32_bf16 v[192:195], a[144:147], a[168:171], v[192:195]\n" \
"ds_read_b64_tr_b16 a[180:181], v37 offset:0\n" \
"s_waitcnt lgkmcnt(9)\n" \
"s_nop 0\n" \
"v_mfma_f32_16x16x32_bf16 v[192:195], a[208:211], a[232:235], v[192:195]\n" \
"v_mfma_f32_16x16x32_bf16 v[208:211], a[148:151], a[168:171], v[208:211]\n" \
"ds_read_b64_tr_b16 a[182:183], v37 offset:1024\n" \
"v_mfma_f32_16x16x32_bf16 v[208:211], a[212:215], a[232:235], v[208:211]\n" \
"ds_read_b64_tr_b16 a[244:245], v37 offset:8192\n" \
"v_mfma_f32_16x16x32_bf16 v[212:215], a[172:175], a[148:151], v[212:215]\n" \
"v_mfma_f32_16x16x32_bf16 v[212:215], a[236:239], a[212:215], v[212:215]\n" \
"ds_read_b64_tr_b16 a[246:247], v37 offset:9216\n" \
"v_cvt_pk_bf16_f32 v188, v188, v189\n" \
"v_mfma_f32_16x16x32_bf16 v[204:207], a[164:167], a[148:151], v[204:207]\n" \
"v_cvt_pk_bf16_f32 v189, v190, v191\n" \
"v_cvt_pk_bf16_f32 v190, v198, v199\n" \
"v_mfma_f32_16x16x32_bf16 v[204:207], a[228:231], a[212:215], v[204:207]\n" \
"ds_read_b64_tr_b16 a[184:185], v38 offset:0\n" \
"v_mov_b32_e32 v191, 0x3f80\n" \
"v_mfma_f32_16x16x32_bf16 v[200:203], a[160:163], a[148:151], v[200:203]\n" \
"ds_read_b64_tr_b16 a[186:187], v38 offset:1024\n" \
"v_cvt_pk_bf16_f32 v184, v184, v185\n" \
"v_mfma_f32_16x16x32_bf16 v[200:203], a[224:227], a[212:215], v[200:203]\n" \
"v_cvt_pk_bf16_f32 v185, v186, v187\n" \
"v_cvt_pk_bf16_f32 v186, v196, v197\n" \
"v_mfma_f32_16x16x32_bf16 v[216:219], a[160:163], a[152:155], v[216:219]\n" \
"ds_read_b64_tr_b16 a[248:249], v38 offset:8192\n" \
"v_mov_b32_e64 v187, 0\n" \
"v_mfma_f32_16x16x32_bf16 v[216:219], a[224:227], a[216:219], v[216:219]\n" \
"ds_read_b64_tr_b16 a[250:251], v38 offset:9216\n" \
"v_mfma_f32_16x16x32_bf16 v[220:223], a[164:167], a[152:155], v[220:223]\n" \
"v_mfma_f32_16x16x32_bf16 v[220:223], a[228:231], a[216:219], v[220:223]\n" \
"ds_read_b64_tr_b16 a[188:189], v39 offset:0\n" \
"v_cvt_pk_bf16_f32 v204, v204, v205\n" \
"v_mfma_f32_16x16x32_bf16 v[228:231], a[172:175], a[152:155], v[228:231]\n" \
"v_cvt_pk_bf16_f32 v205, v206, v207\n" \
"v_cvt_pk_bf16_f32 v206, v214, v215\n" \
"v_mfma_f32_16x16x32_bf16 v[228:231], a[236:239], a[216:219], v[228:231]\n" \
"ds_read_b64_tr_b16 a[190:191], v39 offset:1024\n" \
"v_mov_b32_e32 v207, 0x3f80\n" \
"v_mfma_f32_16x16x32_bf16 v[224:227], a[152:155], a[168:171], v[224:227]\n" \
"ds_read_b64_tr_b16 a[252:253], v39 offset:8192\n" \
"v_cvt_pk_bf16_f32 v200, v200, v201\n" \
"v_mfma_f32_16x16x32_bf16 v[224:227], a[216:219], a[232:235], v[224:227]\n" \
"v_cvt_pk_bf16_f32 v201, v202, v203\n" \
"v_cvt_pk_bf16_f32 v202, v212, v213\n" \
"v_mfma_f32_16x16x32_bf16 v[240:243], a[156:159], a[168:171], v[240:243]\n" \
"ds_read_b64_tr_b16 a[254:255], v39 offset:9216\n" \
"v_mov_b32_e64 v203, 0\n" \
"v_mfma_f32_16x16x32_bf16 v[240:243], a[220:223], a[232:235], v[240:243]\n" \
"v_mfma_f32_16x16x32_bf16 v[244:247], a[172:175], a[156:159], v[244:247]\n" \
"v_mfma_f32_16x16x32_bf16 v[244:247], a[236:239], a[220:223], v[244:247]\n" \
"v_cvt_pk_bf16_f32 v220, v220, v221\n" \
"v_cvt_pk_bf16_f32 v221, v222, v223\n" \
"v_mfma_f32_16x16x32_bf16 v[236:239], a[164:167], a[156:159], v[236:239]\n" \
"v_cvt_pk_bf16_f32 v222, v230, v231\n" \
"v_mov_b32_e32 v223, 0x3f80\n" \
"v_mfma_f32_16x16x32_bf16 v[236:239], a[228:231], a[220:223], v[236:239]\n" \
"v_cvt_pk_bf16_f32 v216, v216, v217\n" \
"v_cvt_pk_bf16_f32 v217, v218, v219\n" \
"v_mfma_f32_16x16x32_bf16 v[232:235], a[160:163], a[156:159], v[232:235]\n" \
"v_cvt_pk_bf16_f32 v218, v228, v229\n" \
"v_mov_b32_e64 v219, 0\n" \
"v_mfma_f32_16x16x32_bf16 v[232:235], a[224:227], a[220:223], v[232:235]\n" \
"v_mfma_f32_16x16x32_bf16 v[180:183], a[164:167], a[140:143], v[180:183]\n" \
"v_mfma_f32_16x16x32_bf16 v[180:183], a[228:231], a[204:207], v[180:183]\n" \
"v_mfma_f32_16x16x32_bf16 v[48:51], a[172:175], a[140:143], v[48:51]\n" \
"v_mfma_f32_16x16x32_bf16 v[48:51], a[236:239], a[204:207], v[48:51]\n" \
"v_cvt_pk_bf16_f32 v236, v236, v237\n" \
"v_cvt_pk_bf16_f32 v237, v238, v239\n" \
"v_mfma_f32_16x16x32_bf16 v[252:255], a[140:143], a[168:171], v[252:255]\n" \
"v_cvt_pk_bf16_f32 v238, v246, v247\n" \
"v_mov_b32_e32 v239, 0x3f80\n" \
"v_mfma_f32_16x16x32_bf16 v[252:255], a[204:207], a[232:235], v[252:255]\n" \
"v_cvt_pk_bf16_f32 v232, v232, v233\n" \
"v_cvt_pk_bf16_f32 v233, v234, v235\n" \
"s_waitcnt vmcnt(0) lgkmcnt(0)\n" \
"s_barrier\n" \
"v_mov_b32_e32 v45, s62\n" \
"s_mov_b64 exec, 1\n" \
"buffer_store_dword v45, off, s[80:83], s37 offset:32 sc1\n" \
"s_mov_b64 exec, -1\n" \
"v_cmp_ne_u32_e32 vcc, s62, v44\n" \
"s_nop 4\n" \
"s_cbranch_vccnz L_poll161_%=\n" \
"L_pollr161_%=:\n" \
"v_mfma_f32_16x16x32_bf16 v[116:119], a[176:179], a[144:147], v[116:119]\n" \
"s_mov_b32 s73, s50\n" \
"s_sub_u32 m0, s31, 0x10000\n" \
"v_mfma_f32_16x16x32_bf16 v[116:119], a[240:243], a[208:211], v[116:119]\n" \
"buffer_load_dwordx4 v20, s[80:83], s73 offen offset:0 sc1 lds\n" \
"v_mfma_f32_16x16x32_bf16 v[120:123], a[180:183], a[144:147], v[120:123]\n" \
"s_add_u32 s74, s50, 0x4000\n" \
"s_add_u32 s75, s50, 0x8000\n" \
"v_mfma_f32_16x16x32_bf16 v[120:123], a[244:247], a[208:211], v[120:123]\n" \
"s_mov_b32 s70, 0x3e0293ee\n" \
"s_mov_b32 s76, s52\n" \
"v_mfma_f32_16x16x32_bf16 v[124:127], a[184:187], a[144:147], v[124:127]\n" \
"v_mul_f32_e32 v116, s70, v116\n" \
"v_mul_f32_e32 v117, s70, v117\n" \
"v_mfma_f32_16x16x32_bf16 v[124:127], a[248:251], a[208:211], v[124:127]\n" \
"buffer_load_dwordx4 v20, s[80:83], s73 offen offset:1024 sc1 lds\n" \
"v_mfma_f32_16x16x32_bf16 v[128:131], a[188:191], a[144:147], v[128:131]\n" \
"v_mul_f32_e32 v118, s70, v118\n" \
"v_mul_f32_e32 v119, s70, v119\n" \
"v_mfma_f32_16x16x32_bf16 v[128:131], a[252:255], a[208:211], v[128:131]\n" \
"v_cvt_pk_bf16_f32 v116, v116, v117\n" \
"v_mul_f32_e64 v120, s70, v120\n" \
"v_mfma_f32_16x16x32_bf16 v[144:147], a[188:191], a[148:151], v[144:147]\n" \
"v_mul_f32_e32 v124, s70, v124\n" \
"v_mul_f32_e32 v125, s70, v125\n" \
"v_mfma_f32_16x16x32_bf16 v[144:147], a[252:255], a[212:215], v[144:147]\n" \
"buffer_load_dwordx4 v20, s[80:83], s73 offen offset:2048 sc1 lds\n" \
"v_mfma_f32_16x16x32_bf16 v[140:143], a[184:187], a[148:151], v[140:143]\n" \
"v_mul_f32_e32 v126, s70, v126\n" \
"v_mul_f32_e32 v127, s70, v127\n" \
"v_mfma_f32_16x16x32_bf16 v[140:143], a[248:251], a[212:215], v[140:143]\n" \
"v_cvt_pk_bf16_f32 v124, v124, v125\n" \
"v_mul_f32_e64 v121, s70, v121\n" \
"v_mfma_f32_16x16x32_bf16 v[136:139], a[180:183], a[148:151], v[136:139]\n" \
"v_mul_f32_e32 v122, s70, v122\n" \
"v_mul_f32_e32 v123, s70, v123\n" \
"v_mfma_f32_16x16x32_bf16 v[136:139], a[244:247], a[212:215], v[136:139]\n" \
"buffer_load_dwordx4 v20, s[80:83], s73 offen offset:3072 sc1 lds\n" \
".fill (L_pb + 77536 - .) / 4, 4, 0xBF800000\n" \
"L_pp19:\n" \
"s_cbranch_execz L_pret\n" \
"v_mfma_f32_16x16x32_bf16 v[132:135], a[176:179], a[148:151], v[132:135]\n" \
"s_sub_u32 m0, s31, s51\n" \
"v_cvt_pk_bf16_f32 v117, v118, v119\n" \
"v_mfma_f32_16x16x32_bf16 v[132:135], a[240:243], a[212:215], v[132:135]\n" \
"v_mul_f32_e32 v128, s70, v128\n" \
"v_mul_f32_e32 v129, s70, v129\n" \
"v_mfma_f32_16x16x32_bf16 v[148:151], a[176:179], a[152:155], v[148:151]\n" \
"v_mul_f32_e32 v130, s70, v130\n" \
"v_mul_f32_e32 v131, s70, v131\n" \
"v_mfma_f32_16x16x32_bf16 v[148:151], a[240:243], a[216:219], v[148:151]\n" \
"buffer_load_dwordx4 v20, s[80:83], s74 offen offset:0 sc1 lds\n" \
"v_mfma_f32_16x16x32_bf16 v[152:155], a[180:183], a[152:155], v[152:155]\n" \
"v_cvt_pk_bf16_f32 v125, v126, v127\n" \
"v_cvt_pk_bf16_f32 v118, v120, v121\n" \
"v_mfma_f32_16x16x32_bf16 v[152:155], a[244:247], a[216:219], v[152:155]\n" \
"v_cvt_pk_bf16_f32 v119, v122, v123\n" \
"v_cvt_pk_bf16_f32 v126, v128, v129\n" \
"v_mfma_f32_16x16x32_bf16 v[156:159], a[184:187], a[152:155], v[156:159]\n" \
"v_cvt_pk_bf16_f32 v127, v130, v131\n" \
"v_mul_f32_e64 v132, s70, v132\n" \
"v_mfma_f32_16x16x32_bf16 v[156:159], a[248:251], a[216:219], v[156:159]\n" \
"buffer_load_dwordx4 v20, s[80:83], s74 offen offset:1024 sc1 lds\n" \
"v_mfma_f32_16x16x32_bf16 v[160:163], a[188:191], a[152:155], v[160:163]\n" \
"ds_write_b128 v21, v[116:119] offset:16384\n" \
"v_mfma_f32_16x16x32_bf16 v[160:163], a[252:255], a[216:219], v[160:163]\n" \
"v_mul_f32_e32 v133, s70, v133\n" \
"v_mul_f32_e32 v140, s70, v140\n" \
"v_mfma_f32_16x16x32_bf16 v[176:179], a[188:191], a[156:159], v[176:179]\n" \
"v_mul_f32_e32 v141, s70, v141\n" \
"v_mul_f32_e32 v134, s70, v134\n" \
"v_mfma_f32_16x16x32_bf16 v[176:179], a[252:255], a[220:223], v[176:179]\n" \
"buffer_load_dwordx4 v20, s[80:83], s74 offen offset:2048 sc1 lds\n" \
"v_mfma_f32_16x16x32_bf16 v[172:175], a[184:187], a[156:159], v[172:175]\n" \
"ds_write_b128 v21, v[124:127] offset:17408\n" \
"v_mfma_f32_16x16x32_bf16 v[172:175], a[248:251], a[220:223], v[172:175]\n" \
"v_mul_f32_e64 v135, s70, v135\n" \
"v_cvt_pk_bf16_f32 v132, v132, v133\n" \
"v_mfma_f32_16x16x32_bf16 v[168:171], a[180:183], a[156:159], v[168:171]\n" \
"v_mul_f32_e32 v142, s70, v142\n" \
"v_mul_f32_e32 v143, s70, v143\n" \
"v_mfma_f32_16x16x32_bf16 v[168:171], a[244:247], a[220:223], v[168:171]\n" \
"buffer_load_dwordx4 v20, s[80:83], s74 offen offset:3072 sc1 lds\n" \
"v_mfma_f32_16x16x32_bf16 v[164:167], a[176:179], a[156:159], v[164:167]\n" \
"s_sub_u32 m0, s31, 0x4000\n" \
"v_cvt_pk_bf16_f32 v140, v140, v141\n" \
"v_mfma_f32_16x16x32_bf16 v[164:167], a[240:243], a[220:223], v[164:167]\n" \
"v_mul_f32_e32 v136, s70, v136\n" \
"v_mul_f32_e32 v137, s70, v137\n" \
"v_mfma_f32_16x16x32_bf16 v[84:87], a[176:179], a[136:139], v[84:87]\n" \
"v_mul_f32_e32 v138, s70, v138\n" \
"v_mul_f32_e32 v139, s70, v139\n" \
"v_mfma_f32_16x16x32_bf16 v[84:87], a[240:243], a[200:203], v[84:87]\n" \
"buffer_load_dwordx4 v20, s[80:83], s75 offen offset:0 sc1 lds\n" \
"v_mfma_f32_16x16x32_bf16 v[88:91], a[180:183], a[136:139], v[88:91]\n" \
"v_cvt_pk_bf16_f32 v133, v134, v135\n" \
"v_mul_f32_e64 v144, s70, v144\n" \
"v_mfma_f32_16x16x32_bf16 v[88:91], a[244:247], a[200:203], v[88:91]\n" \
"v_mul_f32_e32 v145, s70, v145\n" \
"v_mul_f32_e32 v146, s70, v146\n" \
"v_mfma_f32_16x16x32_bf16 v[92:95], a[184:187], a[136:139], v[92:95]\n" \
"v_mul_f32_e64 v147, s70, v147\n" \
"v_cvt_pk_bf16_f32 v141, v142, v143\n" \
"v_mfma_f32_16x16x32_bf16 v[92:95], a[248:251], a[200:203], v[92:95]\n" \
"buffer_load_dwordx4 v20, s[80:83], s75 offen offset:1024 sc1 lds\n" \
"v_mfma_f32_16x16x32_bf16 v[96:99], a[188:191], a[136:139], v[96:99]\n" \
"v_cvt_pk_bf16_f32 v134, v136, v137\n" \
"v_cvt_pk_bf16_f32 v135, v138, v139\n" \
"v_mfma_f32_16x16x32_bf16 v[96:99], a[252:255], a[200:203], v[96:99]\n" \
"v_cvt_pk_bf16_f32 v142, v144, v145\n" \
"v_cvt_pk_bf16_f32 v143, v146, v147\n" \
"v_mfma_f32_16x16x32_bf16 v[112:115], a[188:191], a[140:143], v[112:115]\n" \
"ds_write_b128 v21, v[132:135] offset:20480\n" \
"v_mfma_f32_16x16x32_bf16 v[112:115], a[252:255], a[204:207], v[112:115]\n" \
"buffer_load_dwordx4 v20, s[80:83], s75 offen offset:2048 sc1 lds\n" \
"v_mfma_f32_16x16x32_bf16 v[108:111], a[184:187], a[140:143], v[108:111]\n" \
"v_cvt_pk_bf16_f32 v180, v180, v181\n" \
"v_cvt_pk_bf16_f32 v181, v182, v183\n" \
"v_mfma_f32_16x16x32_bf16 v[108:111], a[248:251], a[204:207], v[108:111]\n" \
"ds_write_b128 v21, v[140:143] offset:21504\n" \
"v_mfma_f32_16x16x32_bf16 v[104:107], a[180:183], a[140:143], v[104:107]\n" \
"v_cvt_pk_bf16_f32 v234, v244, v245\n" \
"v_mov_b32_e64 v235, 0\n" \
"v_mfma_f32_16x16x32_bf16 v[104:107], a[244:247], a[204:207], v[104:107]\n" \
"buffer_load_dwordx4 v20, s[80:83], s75 offen offset:3072 sc1 lds\n" \
"v_mfma_f32_16x16x32_bf16 v[100:103], a[176:179], a[140:143], v[100:103]\n" \
"s_mov_b32 m0, s53\n" \
"s_nop 0\n" \
"v_cvt_pk_bf16_f32 v182, v50, v51\n" \
"v_mfma_f32_16x16x32_bf16 v[100:103], a[240:243], a[204:207], v[100:103]\n" \
"v_mov_b32_e32 v183, 0x3f80\n" \
"v_cvt_pk_bf16_f32 v118, v192, v193\n" \
"v_mfma_f32_16x16x32_bf16 v[52:55], a[176:179], a[128:131], v[52:55]\n" \
"v_cvt_pk_bf16_f32 v119, v194, v195\n" \
"v_cvt_pk_bf16_f32 v120, v208, v209\n" \
"v_mfma_f32_16x16x32_bf16 v[52:55], a[240:243], a[192:195], v[52:55]\n" \
"buffer_load_dwordx4 v20, s[80:83], s76 offen offset:0 sc1 lds\n" \
"v_mfma_f32_16x16x32_bf16 v[56:59], a[180:183], a[128:131], v[56:59]\n" \
"v_cvt_pk_bf16_f32 v121, v210, v211\n" \
"v_cvt_pk_bf16_f32 v122, v224, v225\n" \
"v_mfma_f32_16x16x32_bf16 v[56:59], a[244:247], a[192:195], v[56:59]\n" \
"v_cvt_pk_bf16_f32 v123, v226, v227\n" \
"v_cvt_pk_bf16_f32 v124, v240, v241\n" \
"v_cvt_pk_bf16_f32 v125, v242, v243\n" \
"v_mfma_f32_16x16x32_bf16 v[60:63], a[184:187], a[128:131], v[60:63]\n" \
"v_mfma_f32_16x16x32_bf16 v[192:195], v[188:191], v[184:187], 0\n" \
"buffer_load_dwordx4 v20, s[80:83], s76 offen offset:1024 sc1 lds\n" \
"v_mfma_f32_16x16x32_bf16 v[32:35], v[180:183], v[184:187], 0\n" \
"v_mfma_f32_16x16x32_bf16 v[208:211], v[204:207], v[200:203], 0\n" \
"v_mfma_f32_16x16x32_bf16 v[36:39], v[188:191], v[200:203], 0\n" \
"v_mfma_f32_16x16x32_bf16 v[224:227], v[220:223], v[216:219], 0\n" \
"v_mfma_f32_16x16x32_bf16 v[248:251], v[204:207], v[216:219], 0\n" \
"v_mfma_f32_16x16x32_bf16 v[240:243], v[236:239], v[232:235], 0\n" \
"v_mfma_f32_16x16x32_bf16 v[128:131], v[220:223], v[232:235], 0\n" \
"s_mov_b32 s64, 0xaaaaaaaa\n" \
"s_mov_b32 s65, 0xaaaaaaaa\n" \
"v_mfma_f32_16x16x32_bf16 v[60:63], a[248:251], a[192:195], v[60:63]\n" \
"s_mov_b32 s66, 0xfff0ffff\n" \
"s_mov_b32 s67, 0xf000ff00\n" \
"v_mfma_f32_16x16x32_bf16 v[64:67], a[188:191], a[128:131], v[64:67]\n" \
"s_mov_b32 s68, 0xffc0fffc\n" \
"s_mov_b32 s69, 0xc000fc00\n" \
"v_cndmask_b32_e64 v192, v192, v193, s[64:65]\n" \
"v_cndmask_b32_e64 v32, v32, v33, s[64:65]\n" \
"v_cndmask_b32_e64 v194, v194, v195, s[64:65]\n" \
"v_cndmask_b32_e64 v34, v34, v35, s[64:65]\n" \
"v_cndmask_b32_e64 v208, v208, v209, s[64:65]\n" \
"v_cndmask_b32_e64 v36, v36, v37, s[64:65]\n" \
"v_cndmask_b32_e64 v210, v210, v211, s[64:65]\n" \
"v_cndmask_b32_e64 v38, v38, v39, s[64:65]\n" \
"v_cndmask_b32_e64 v224, v224, v225, s[64:65]\n" \
"v_cndmask_b32_e64 v248, v248, v249, s[64:65]\n" \
"v_cndmask_b32_e64 v226, v226, v227, s[64:65]\n" \
"v_cndmask_b32_e64 v250, v250, v251, s[64:65]\n" \
"v_cndmask_b32_e64 v240, v240, v241, s[64:65]\n" \
"v_cndmask_b32_e64 v128, v128, v129, s[64:65]\n" \
"v_cndmask_b32_e64 v242, v242, v243, s[64:65]\n" \
"v_cndmask_b32_e64 v130, v130, v131, s[64:65]\n" \
"v_cndmask_b32_e64 v192, v32, v192, s[66:67]\n" \
"v_cndmask_b32_e64 v194, v34, v194, s[68:69]\n" \
"v_cndmask_b32_e64 v208, v36, v208, s[66:67]\n" \
"v_cndmask_b32_e64 v210, v38, v210, s[68:69]\n" \
"v_cndmask_b32_e64 v224, v248, v224, s[66:67]\n" \
"v_cndmask_b32_e64 v226, v250, v226, s[68:69]\n" \
"v_cndmask_b32_e64 v240, v128, v240, s[66:67]\n" \
"v_cndmask_b32_e64 v242, v130, v242, s[68:69]\n" \
"v_mfma_f32_16x16x32_bf16 v[64:67], a[252:255], a[192:195], v[64:67]\n" \
"v_max_f32_e32 v193, v192, v194\n" \
"v_max_f32_e32 v209, v208, v210\n" \
"v_max_f32_e32 v225, v224, v226\n" \
"v_max_f32_e32 v241, v240, v242\n" \
"v_mul_f32_e32 v148, s70, v148\n" \
"v_permlane32_swap_b32_e32 v193, v225\n" \
"v_permlane32_swap_b32_e32 v209, v241\n" \
"v_mul_f32_e32 v149, s70, v149\n" \
"v_mul_f32_e32 v156, s70, v156\n" \
"v_mul_f32_e32 v157, s70, v157\n" \
"v_permlane32_swap_b32_e32 v196, v228\n" \
"v_permlane32_swap_b32_e32 v212, v244\n" \
"v_max_f32_e32 v193, v193, v225\n" \
"v_max_f32_e32 v209, v209, v241\n" \
"v_mul_f32_e32 v150, s70, v150\n" \
"v_mul_f32_e32 v151, s70, v151\n" \
"v_cvt_pk_bf16_f32 v148, v148, v149\n" \
"v_mul_f32_e32 v158, s70, v158\n" \
"v_mul_f32_e32 v159, s70, v159\n" \
"v_cvt_pk_bf16_f32 v156, v156, v157\n" \
"v_permlane16_swap_b32_e32 v228, v244\n" \
"v_permlane16_swap_b32_e32 v193, v209\n" \
"v_mfma_f32_16x16x32_bf16 v[80:83], a[188:191], a[132:135], v[80:83]\n" \
"s_mov_b32 s72, 0x3eae1a93\n" \
"v_mul_f32_e32 v152, s70, v152\n" \
"v_mul_f32_e32 v153, s70, v153\n" \
"v_mul_f32_e32 v154, s70, v154\n" \
"v_mul_f32_e32 v155, s70, v155\n" \
"v_cvt_pk_bf16_f32 v149, v150, v151\n" \
"v_mul_f32_e32 v160, s70, v160\n" \
"v_mul_f32_e32 v161, s70, v161\n" \
"v_mul_f32_e32 v162, s70, v162\n" \
"v_mul_f32_e32 v163, s70, v163\n" \
"v_cvt_pk_bf16_f32 v157, v158, v159\n" \
"v_mul_f32_e32 v45, 0xbfb8aa3b, v228\n" \
"v_mfma_f32_16x16x16_bf16 v[48:51], v[182:183], v[46:47], 0\n" \
"v_mfma_f32_16x16x16_bf16 v[196:199], v[190:191], v[46:47], 0\n" \
"v_mfma_f32_16x16x16_bf16 v[212:215], v[206:207], v[46:47], 0\n" \
"v_max_f32_e64 v193, v193, v209\n" \
"v_cvt_pk_bf16_f32 v150, v152, v153\n" \
"v_cvt_pk_bf16_f32 v151, v154, v155\n" \
"v_cvt_pk_bf16_f32 v158, v160, v161\n" \
"v_cvt_pk_bf16_f32 v159, v162, v163\n" \
"v_mfma_f32_16x16x16_bf16 v[228:231], v[222:223], v[46:47], 0\n" \
"v_mul_f32_e32 v193, s72, v193\n" \
"v_mov_b32_e32 v209, v193\n" \
"ds_write_b128 v21, v[148:151] offset:24576\n" \
"v_mfma_f32_16x16x16_bf16 v[244:247], v[238:239], v[46:47], 0\n" \
"v_permlane16_swap_b32_e32 v193, v209\n" \
"s_mov_b32 s71, 0x3e0293ee\n" \
"v_mfma_f32_16x16x32_bf16 v[80:83], a[252:255], a[196:199], v[80:83]\n" \
"v_mov_b32_e32 v225, v193\n" \
"v_mov_b32_e32 v241, v209\n" \
"v_mul_f32_e32 v164, s70, v164\n" \
"v_mul_f32_e64 v165, s70, v165\n" \
"ds_write_b128 v21, v[156:159] offset:25600\n" \
"v_mul_f32_e32 v172, s70, v172\n" \
"v_mul_f32_e32 v173, s70, v173\n" \
"v_pk_mul_f32 v[84:85], v[84:85], s[70:71]\n" \
"v_pk_mul_f32 v[92:93], v[92:93], s[70:71]\n" \
"v_pk_mul_f32 v[100:101], v[100:101], s[70:71]\n" \
"v_pk_mul_f32 v[108:109], v[108:109], s[70:71]\n" \
"v_pk_mul_f32 v[52:53], v[52:53], s[70:71]\n" \
"v_pk_mul_f32 v[60:61], v[60:61], s[70:71]\n" \
"v_permlane32_swap_b32_e32 v193, v225\n" \
"v_permlane32_swap_b32_e32 v209, v241\n" \
"v_mul_f32_e32 v166, s70, v166\n" \
"v_mul_f32_e32 v167, s70, v167\n" \
"v_cvt_pk_bf16_f32 v164, v164, v165\n" \
"v_mul_f32_e32 v174, s70, v174\n" \
"v_mul_f32_e32 v175, s70, v175\n" \
"v_cvt_pk_bf16_f32 v172, v172, v173\n" \
"v_pk_mul_f32 v[86:87], v[86:87], s[70:71]\n" \
"v_cvt_pk_bf16_f32 v84, v84, v85\n" \
"v_pk_mul_f32 v[94:95], v[94:95], s[70:71]\n" \
"v_cvt_pk_bf16_f32 v92, v92, v93\n" \
"v_pk_mul_f32 v[102:103], v[102:103], s[70:71]\n" \
"v_cvt_pk_bf16_f32 v100, v100, v101\n" \
"v_pk_mul_f32 v[110:111], v[110:111], s[70:71]\n" \
"v_cvt_pk_bf16_f32 v108, v108, v109\n" \
"v_pk_mul_f32 v[54:55], v[54:55], s[70:71]\n" \
"v_cvt_pk_bf16_f32 v52, v52, v53\n" \
"v_pk_mul_f32 v[62:63], v[62:63], s[70:71]\n" \
"v_cvt_pk_bf16_f32 v60, v60, v61\n" \
"v_fma_f32 v192, v192, s72, -v193\n" \
"v_fma_f32 v194, v194, s72, -v193\n" \
"v_fma_f32 v208, v208, s72, -v209\n" \
"v_fma_f32 v210, v210, s72, -v209\n" \
"v_mul_f32_e32 v168, s70, v168\n" \
"v_mul_f32_e32 v169, s70, v169\n" \
"v_mul_f32_e32 v170, s70, v170\n" \
"v_mul_f32_e32 v171, s70, v171\n" \
"v_cvt_pk_bf16_f32 v165, v166, v167\n" \
"v_mul_f32_e32 v176, s70, v176\n" \
"v_mul_f32_e32 v177, s70, v177\n" \
"v_mul_f32_e32 v178, s70, v178\n" \
"v_mul_f32_e32 v179, s70, v179\n" \
"v_cvt_pk_bf16_f32 v173, v174, v175\n" \
"v_pk_mul_f32 v[88:89], v[88:89], s[70:71]\n" \
"v_pk_mul_f32 v[90:91], v[90:91], s[70:71]\n" \
"v_cvt_pk_bf16_f32 v85, v86, v87\n" \
"v_pk_mul_f32 v[96:97], v[96:97], s[70:71]\n" \
"v_pk_mul_f32 v[98:99], v[98:99], s[70:71]\n" \
"v_cvt_pk_bf16_f32 v93, v94, v95\n" \
"v_pk_mul_f32 v[104:105], v[104:105], s[70:71]\n" \
"v_pk_mul_f32 v[106:107], v[106:107], s[70:71]\n" \
"v_cvt_pk_bf16_f32 v101, v102, v103\n" \
"v_pk_mul_f32 v[112:113], v[112:113], s[70:71]\n" \
"v_pk_mul_f32 v[114:115], v[114:115], s[70:71]\n" \
"v_cvt_pk_bf16_f32 v109, v110, v111\n" \
"v_pk_mul_f32 v[56:57], v[56:57], s[70:71]\n" \
"v_pk_mul_f32 v[58:59], v[58:59], s[70:71]\n" \
"v_cvt_pk_bf16_f32 v53, v54, v55\n" \
"v_pk_mul_f32 v[64:65], v[64:65], s[70:71]\n" \
"v_pk_mul_f32 v[66:67], v[66:67], s[70:71]\n" \
"v_cvt_pk_bf16_f32 v61, v62, v63\n" \
"v_pk_mul_f32 v[80:81], v[80:81], s[70:71]\n" \
"v_pk_mul_f32 v[82:83], v[82:83], s[70:71]\n" \
"v_exp_f32_e32 v192, v192\n" \
"v_exp_f32_e32 v194, v194\n" \
"v_exp_f32_e32 v208, v208\n" \
"v_exp_f32_e32 v210, v210\n" \
"v_fma_f32 v224, v224, s72, -v225\n" \
"v_fma_f32 v226, v226, s72, -v225\n" \
"v_mfma_f32_16x16x32_bf16 v[76:79], a[184:187], a[132:135], v[76:79]\n" \
"v_cvt_pk_bf16_f32 v166, v168, v169\n" \
"v_cvt_pk_bf16_f32 v167, v170, v171\n" \
"v_cvt_pk_bf16_f32 v174, v176, v177\n" \
"v_cvt_pk_bf16_f32 v175, v178, v179\n" \
"v_cvt_pk_bf16_f32 v86, v88, v89\n" \
"v_cvt_pk_bf16_f32 v87, v90, v91\n" \
"v_cvt_pk_bf16_f32 v94, v96, v97\n" \
"v_cvt_pk_bf16_f32 v95, v98, v99\n" \
"v_cvt_pk_bf16_f32 v102, v104, v105\n" \
"v_cvt_pk_bf16_f32 v103, v106, v107\n" \
"v_cvt_pk_bf16_f32 v110, v112, v113\n" \
"v_cvt_pk_bf16_f32 v111, v114, v115\n" \
"v_cvt_pk_bf16_f32 v54, v56, v57\n" \
"v_cvt_pk_bf16_f32 v55, v58, v59\n" \
"v_cvt_pk_bf16_f32 v62, v64, v65\n" \
"v_cvt_pk_bf16_f32 v63, v66, v67\n" \
"v_exp_f32_e32 v224, v224\n" \
"v_exp_f32_e32 v226, v226\n" \
"v_fma_f32 v240, v240, s72, -v241\n" \
"v_fma_f32 v242, v242, s72, -v241\n" \
"ds_write_b128 v21, v[164:167] offset:28672\n" \
"v_cvt_pk_bf16_f32 v192, v192, v194\n" \
"v_cvt_pk_bf16_f32 v208, v208, v210\n" \
"v_exp_f32_e32 v240, v240\n" \
"v_exp_f32_e32 v242, v242\n" \
"v_cvt_pk_bf16_f32 v132, v48, v49\n" \
"v_cvt_pk_bf16_f32 v133, v50, v51\n" \
"v_cvt_pk_bf16_f32 v134, v196, v197\n" \
"v_cvt_pk_bf16_f32 v135, v198, v199\n" \
"ds_write_b128 v21, v[172:175] offset:29696\n" \
"v_cvt_pk_bf16_f32 v136, v212, v213\n" \
"v_cvt_pk_bf16_f32 v137, v214, v215\n" \
"v_perm_b32 v184, v192, v192, v40\n" \
"v_perm_b32 v185, v192, v192, v41\n" \
"v_perm_b32 v186, v192, v192, v42\n" \
"v_perm_b32 v187, v192, v192, v43\n" \
"v_perm_b32 v200, v208, v208, v40\n" \
"v_perm_b32 v201, v208, v208, v41\n" \
"v_perm_b32 v202, v208, v208, v42\n" \
"v_perm_b32 v203, v208, v208, v43\n" \
"ds_write_b128 v27, v[84:87] offset:8192\n" \
"v_cvt_pk_bf16_f32 v224, v224, v226\n" \
"v_cvt_pk_bf16_f32 v138, v228, v229\n" \
"v_cvt_pk_bf16_f32 v139, v230, v231\n" \
"v_mfma_f32_16x16x32_bf16 v[32:35], v[132:135], v[184:187], 0\n" \
"v_mfma_f32_16x16x32_bf16 v[36:39], v[134:137], v[200:203], 0\n" \
"v_perm_b32 v216, v224, v224, v40\n" \
"v_perm_b32 v217, v224, v224, v41\n" \
"ds_write_b128 v27, v[92:95] offset:9216\n" \
"v_perm_b32 v218, v224, v224, v42\n" \
"v_perm_b32 v219, v224, v224, v43\n" \
"v_cvt_pk_bf16_f32 v240, v240, v242\n" \
"v_mfma_f32_16x16x32_bf16 v[76:79], a[248:251], a[196:199], v[76:79]\n" \
"v_cvt_pk_bf16_f32 v140, v244, v245\n" \
"v_cvt_pk_bf16_f32 v141, v246, v247\n" \
"v_mfma_f32_16x16x32_bf16 v[248:251], v[136:139], v[216:219], 0\n" \
"v_perm_b32 v232, v240, v240, v40\n" \
"v_perm_b32 v233, v240, v240, v41\n" \
"ds_write_b128 v27, v[100:103] offset:12288\n" \
"v_perm_b32 v234, v240, v240, v42\n" \
"v_perm_b32 v235, v240, v240, v43\n" \
"v_pk_mul_f32 v[76:77], v[76:77], s[70:71]\n" \
"v_pk_mul_f32 v[78:79], v[78:79], s[70:71]\n" \
"v_cvt_pk_bf16_f32 v76, v76, v77\n" \
"v_cvt_pk_bf16_f32 v77, v78, v79\n" \
"v_cvt_pk_bf16_f32 v78, v80, v81\n" \
"v_cvt_pk_bf16_f32 v79, v82, v83\n" \
"v_mfma_f32_16x16x32_bf16 v[128:131], v[138:141], v[232:235], 0\n" \
"ds_write_b128 v27, v[108:111] offset:13312\n" \
"v_mfma_f32_16x16x32_bf16 v[72:75], a[180:183], a[132:135], v[72:75]\n" \
"v_cvt_pk_bf16_f32 v116, v252, v253\n" \
"v_cvt_pk_bf16_f32 v117, v254, v255\n" \
"v_mfma_f32_16x16x32_bf16 v[72:75], a[244:247], a[196:199], v[72:75]\n" \
"v_exp_f32_e64 v45, v45\n" \
"v_mfma_f32_16x16x32_bf16 v[192:195], v[116:119], v[184:187], 0\n" \
"ds_write_b128 v27, v[52:55] offset:0\n" \
"v_mfma_f32_16x16x32_bf16 v[208:211], v[118:121], v[200:203], 0\n" \
"v_mfma_f32_16x16x32_bf16 v[224:227], v[120:123], v[216:219], 0\n" \
"v_mfma_f32_16x16x32_bf16 v[240:243], v[122:125], v[232:235], 0\n" \
"ds_write_b128 v27, v[60:63] offset:1024\n" \
"v_mov_b32_dpp v34, v38 quad_perm:[0,1,2,3] row_mask:0x2 bank_mask:0xf\n" \
"v_pk_mul_f32 v[72:73], v[72:73], s[70:71]\n" \
"v_pk_mul_f32 v[74:75], v[74:75], s[70:71]\n" \
"v_mov_b32_dpp v34, v250 quad_perm:[0,1,2,3] row_mask:0x4 bank_mask:0xf\n" \
"v_add_f32_e64 v45, 1.0, v45\n" \
"v_mfma_f32_16x16x32_bf16 v[68:71], a[176:179], a[132:135], v[68:71]\n" \
"v_mov_b32_dpp v34, v130 quad_perm:[0,1,2,3] row_mask:0x8 bank_mask:0xf\n" \
"v_mov_b32_dpp v32, v36 quad_perm:[0,1,2,3] row_mask:0x2 bank_mask:0xf\n" \
"v_rcp_f32_e64 v45, v45\n" \
"ds_write_b128 v27, v[76:79] offset:5120\n" \
"v_mov_b32_dpp v33, v37 quad_perm:[0,1,2,3] row_mask:0x2 bank_mask:0xf\n" \
"v_rcp_f32_e64 v34, v34\n" \
"v_mov_b32_dpp v32, v248 quad_perm:[0,1,2,3] row_mask:0x4 bank_mask:0xf\n" \
"v_mov_b32_dpp v33, v249 quad_perm:[0,1,2,3] row_mask:0x4 bank_mask:0xf\n" \
"v_mul_f32_e64 v34, v45, v34\n" \
"v_mov_b32_dpp v32, v128 quad_perm:[0,1,2,3] row_mask:0x8 bank_mask:0xf\n" \
"v_mov_b32_dpp v33, v129 quad_perm:[0,1,2,3] row_mask:0x8 bank_mask:0xf\n" \
"v_mul_f32_e32 v32, v34, v32\n" \
"v_mul_f32_e32 v33, v34, v33\n" \
"v_mov_b32_e64 v38, v34\n" \
"v_mfma_f32_16x16x32_bf16 v[68:71], a[240:243], a[196:199], v[68:71]\n" \
"v_sub_f32_e32 v44, 1.0, v45\n" \
"v_permlane16_swap_b32_e32 v34, v38\n" \
"v_mov_b32_e32 v250, v34\n" \
"v_mov_b32_e32 v130, v38\n" \
"v_cvt_pk_bf16_f32 v32, v32, v33\n" \
"v_permlane32_swap_b32_e32 v34, v250\n" \
"v_permlane32_swap_b32_e32 v38, v130\n" \
"v_mul_f32_e64 v192, v34, v192\n" \
"v_pk_mul_f32 v[68:69], v[68:69], s[70:71]\n" \
"v_pk_mul_f32 v[70:71], v[70:71], s[70:71]\n" \
"v_cvt_pk_bf16_f32 v68, v68, v69\n" \
"v_cvt_pk_bf16_f32 v69, v70, v71\n" \
"v_cvt_pk_bf16_f32 v70, v72, v73\n" \
"v_cvt_pk_bf16_f32 v71, v74, v75\n" \
"ds_write_b128 v27, v[68:71] offset:4096\n" \
"v_mul_f32_e32 v193, v34, v193\n" \
"v_mul_f32_e32 v208, v38, v208\n" \
"v_mul_f32_e32 v209, v38, v209\n" \
"v_mul_f32_e32 v224, v250, v224\n" \
"v_mul_f32_e32 v225, v250, v225\n" \
"v_mul_f32_e32 v240, v130, v240\n" \
"v_mul_f32_e32 v241, v130, v241\n" \
"v_mul_f32_e32 v194, v34, v194\n" \
"v_mul_f32_e64 v195, v34, v195\n" \
"v_cvt_pk_bf16_f32 v192, v192, v193\n" \
"v_mul_f32_e32 v210, v38, v210\n" \
"v_mul_f32_e32 v211, v38, v211\n" \
"v_cvt_pk_bf16_f32 v208, v208, v209\n" \
"v_mul_f32_e32 v226, v250, v226\n" \
"v_mul_f32_e32 v227, v250, v227\n" \
"v_cvt_pk_bf16_f32 v224, v224, v225\n" \
"v_mul_f32_e32 v242, v130, v242\n" \
"v_mul_f32_e32 v243, v130, v243\n" \
"v_cvt_pk_bf16_f32 v240, v240, v241\n" \
"v_cvt_pk_bf16_f32 v193, v194, v195\n" \
"s_add_u32 s84, s48, 0x0\n" \
"s_nop 0\n" \
"v_cvt_pk_bf16_f32 v209, v210, v211\n" \
"s_add_u32 s85, s48, 0x24800\n" \
"v_cvt_pk_bf16_f32 v225, v226, v227\n" \
"s_add_u32 s86, s48, 0x49000\n" \
"v_cvt_pk_bf16_f32 v241, v242, v243\n" \
"s_add_u32 s87, s48, 0x6d800\n" \
"ds_write_b32 v26, v44\n" \
"buffer_store_dword v32, v25, s[80:83], s49 offen sc1\n" \
"buffer_store_dwordx2 v[192:193], v24, s[80:83], s84 offen sc1\n" \
"buffer_store_dwordx2 v[208:209], v24, s[80:83], s85 offen sc1\n" \
"buffer_store_dwordx2 v[224:225], v24, s[80:83], s86 offen sc1\n" \
"buffer_store_dwordx2 v[240:241], v24, s[80:83], s87 offen sc1\n" \
"s_branch L_end_%=\n" \
"L_poll61_%=:\n" \
"s_mov_b32 s84, 0\n" \
"L_pl61_%=:\n" \
"s_sleep 1\n" \
"v_bfe_u32 v44, v20, 2, 5\n" \
"buffer_load_dword v44, v44, s[80:83], s38 offen offset:128 sc1\n" \
"s_waitcnt vmcnt(0)\n" \
"v_cmp_ne_u32_e32 vcc, s62, v44\n" \
"s_add_u32 s84, s84, 1\n" \
"s_nop 3\n" \
"s_cbranch_vccz L_pollr61_%=\n" \
"s_cmp_gt_u32 s84, 0x400000\n" \
"s_cbranch_scc0 L_pl61_%=\n" \
"s_branch L_pollr61_%=\n" \
"L_poll161_%=:\n" \
"s_mov_b32 s84, 0\n" \
"L_pl161_%=:\n" \
"s_sleep 1\n" \
"v_bfe_u32 v44, v20, 2, 5\n" \
"buffer_load_dword v44, v44, s[80:83], s38 offen offset:0 sc1\n" \
"s_waitcnt vmcnt(0)\n" \
"v_cmp_ne_u32_e32 vcc, s62, v44\n" \
"s_add_u32 s84, s84, 1\n" \
"s_nop 3\n" \
"s_cbranch_vccz L_pollr161_%=\n" \
"s_cmp_gt_u32 s84, 0x400000\n" \
"s_cbranch_scc0 L_pl161_%=\n" \
"s_branch L_pollr161_%=\n" \
"L_end_%=:\n"
#define PROJ_VCLOB "v21","v24","v25","v28","v29","v32","v33","v34","v35","v36","v37","v38","v39","v40","v41","v42","v43","v44","v45","v46","v47","v48","v49","v50","v51","v52","v53","v54","v55","v56","v57","v58","v59","v60","v61","v62","v63","v64","v65","v66","v67","v68","v69","v70","v71","v72","v73","v74","v75","v76","v77","v78","v79","v80","v81","v82","v83","v84","v85","v86","v87","v88","v89","v90","v91","v92","v93","v94","v95","v96","v97","v98","v99","v100","v101","v102","v103","v104","v105","v106","v107","v108","v109","v110","v111","v112","v113","v114","v115","v116","v117","v118","v119","v120","v121","v122","v123","v124","v125","v126","v127","v128","v129","v130","v131","v132","v133","v134","v135","v136","v137","v138","v139","v140","v141","v142","v143","v144","v145","v146","v147","v148","v149","v150","v151","v152","v153","v154","v155","v156","v157","v158","v159","v160","v161","v162","v163","v164","v165","v166","v167","v168","v169","v170","v171","v172","v173","v174","v175","v176","v177","v178","v179","v180","v181","v182","v183","v184","v185","v186","v187","v188","v189","v190","v192","v193","v194","v195","v196","v197","v198","v199","v200","v201","v202","v203","v204","v205","v206","v207","v208","v209","v210","v211","v212","v213","v214","v215","v216","v217","v218","v219","v220","v221","v222","v223","v224","v225","v226","v227","v228","v229","v230","v231","v232","v233","v234","v235","v236","v237","v238","v239","v240","v241","v242","v243","v244","v245","v246","v247","v248","v249","v250","v251","v252","v253","v254","v255"
#define PROJ_SCLOB "s4","s5","s6","s7","s32","s33","s34","s35","s36","s37","s38","s39","s40","s41","s42","s43","s44","s45","s46","s47","s48","s49","s50","s51","s52","s53","s54","s55","s63","s64","s65","s66","s67","s68","s69","s70","s71","s72","s73","s74","s75","s76","s77","s78","s79","s80","s81","s82","s83","s84","s85","s86","s87","s88","s89","s90","s91","s92","s93","s94","s95","s96","s97","s98","s99","s100","s101"

typedef const u16* const __attribute__((address_space(4)))* kaptr;
__device__ __forceinline__ void proj_phase(kaptr& ka, char*& ws, const unsigned wid, u32x4 (&vp)[5], unsigned& vl, u32x4 (&sa)[5], u32x2 s24, u32x2& s28, unsigned tdum, u32x4& srdA, u32x2& sd, unsigned& tag
    ) {
    const unsigned b = blockIdx.x, h = (b & 7) * 4 + ((b >> 3) & 3), mt = b >> 5;
    const unsigned rb = (wid < 2u ? 15u - mt : mt) * 128u;
    const unsigned lane = threadIdx.x & 63u, wc = wid & 1u;
    u32x2 vAc, vB;
#pragma unroll
    for (int par = 0; par < 2; ++par) {
        const unsigned lr = lane >> 3, cc = (lane & 7u) ^ (4u * par + (lr >> 1)), r0 = rb + wc * 64u + lr;
        vAc[par] = r0 * (DC * 2) + cc * 16u;
        const unsigned br = lane >> 4, p16 = lane & 15u, sc = (((p16 >> 1) ^ (br | (par << 2))) << 1) | (p16 & 1u);
        vB[par] = (16u * wid + br) * (NBIG * 2) + h * (HD * 2) + sc * 16u;
    }
    vl = lane * 16u;
    u32x2 s30 = {wid * 8192u, 65536u + wid * 4096u};
    asm volatile(PROJ_ASM
                 : "={s62}"(tag), "={s[56:59]}"(srdA), "={s[60:61]}"(sd), "+{s[8:11]}"(sa[0]), "+{s[12:15]}"(sa[1]), "+{s[16:19]}"(sa[2]), "+{s[20:23]}"(sa[3]), "+{s[24:25]}"(s24), "+{s[28:29]}"(s28), "+{s[30:31]}"(s30),
                   "={v[0:3]}"(vp[0]), "={v[4:7]}"(vp[1]), "={v[8:11]}"(vp[2]), "={v[12:15]}"(vp[3]), "={v[16:19]}"(vp[4]), "+{v[22:23]}"(vAc), "+{v[26:27]}"(vB), "+{v191}"(tdum), "+{s[26:27]}"(ws), "+{s[0:1]}"(ka)
                 : "{s2}"(b), "{s3}"(wid), "{v20}"(vl)
                 : "memory", "m0", "vcc", "scc", PROJ_VCLOB, PROJ_SCLOB, ACLOB_LO, ACLOB_HI);
    sa[4] = (u32x4){s24[0], 0u, 0u, 0u};
}

namespace att {
#define DI __device__ __forceinline__
typedef __bf16 bfx8 __attribute__((ext_vector_type(8)));
DI unsigned rfl(unsigned x) { return __builtin_amdgcn_readfirstlane(x); }
DI f32x4 mfma(u32x4 a, u32x4 b, f32x4 c) { return __builtin_amdgcn_mfma_f32_16x16x32_bf16(__builtin_bit_cast(bfx8, a), __builtin_bit_cast(bfx8, b), c, 0, 0, 0); }
typedef __bf16 bfx2 __attribute__((ext_vector_type(2)));
typedef float f32x2 __attribute__((ext_vector_type(2)));
DI unsigned cvtpk2(float lo, float hi) { f32x2 v = {lo, hi}; bfx2 b = __builtin_convertvector(v, bfx2); return __builtin_bit_cast(unsigned, b); }
DI void att_exact(const u16* __restrict__ x, const u16* __restrict__ wq, char* __restrict__ ws, unsigned h, unsigned r0, float g1) {
    const unsigned lane = lane_id(), g = lane >> 4, c = lane & 15u;
    f32x4 o[8];
#pragma unroll
    for (int n = 0; n < 8; ++n) o[n] = (f32x4){0.f, 0.f, 0.f, 0.f};
#pragma unroll 1
    for (unsigned kk = 0; kk < (unsigned)DM; kk += 32u) {
        const unsigned k = kk + 8u * g;
        const u32x4 xf = *reinterpret_cast<const u32x4*>(x + (r0 + c) * DM + k);
#pragma unroll
        for (int n = 0; n < 8; ++n) {
            const u16* w = wq + k * NBIG + h * HD + n * 16 + c; u32x4 wf;
#pragma unroll
            for (int e = 0; e < 4; ++e) wf[e] = (unsigned)w[2 * e * NBIG] | ((unsigned)w[(2 * e + 1) * NBIG] << 16);
            o[n] = mfma(wf, xf, o[n]);
        }
    }
    u32x4 q[4];
#pragma unroll
    for (int p = 0; p < 4; ++p) {
        const f32x4 a = o[2 * p] * 0.12751743082f, b = o[2 * p + 1] * 0.12751743082f;
        q[p] = (u32x4){cvtpk2(a[0], a[1]), cvtpk2(a[2], a[3]), cvtpk2(b[0], b[1]), cvtpk2(b[2], b[3])};
    }
#pragma unroll
    for (int n = 0; n < 8; ++n) o[n] = (f32x4){0.f, 0.f, 0.f, 0.f};
    const __amdgpu_buffer_rsrc_t srd = __builtin_amdgcn_make_buffer_rsrc((void*)(ws + WS_K + (size_t)h * 1048576), (short)0, 1048576, 0x00020000);
    float m = -3.0e38f, l = 0.f;
#pragma unroll 1
    for (unsigned k0 = 0; k0 <= r0 + 15u; k0 += 32u) {
        const unsigned tb = (k0 >> 6) * 32768u + (k0 & 32u) * 256u;
        f32x4 s[2]; float mx = m;
#pragma unroll
        for (int kt = 0; kt < 2; ++kt) {
            s[kt] = (f32x4){0.f, 0.f, 0.f, 0.f};
#pragma unroll
            for (int p = 0; p < 4; ++p) s[kt] = mfma(__builtin_amdgcn_raw_buffer_load_b128(srd, lane * 16u + (kt * 4 + p) * 1024u, tb, 16), q[p], s[kt]);
#pragma unroll
            for (int e = 0; e < 4; ++e) { if (k0 + 16u * kt + 4u * g + e > r0 + c) s[kt][e] = -3.0e38f; mx = fmaxf(mx, s[kt][e]); }
        }
        mx = fmaxf(mx, __shfl_xor(mx, 16)); mx = fmaxf(mx, __shfl_xor(mx, 32));
        const float al = __builtin_amdgcn_exp2f(m - mx); m = mx;
        u32x4 pf;
#pragma unroll
        for (int e = 0; e < 4; ++e) pf[e] = cvtpk2(__builtin_amdgcn_exp2f(s[e >> 1][2 * (e & 1)] - m), __builtin_amdgcn_exp2f(s[e >> 1][2 * (e & 1) + 1] - m));
        l *= al;
#pragma unroll
        for (int e = 0; e < 4; ++e) l += __uint_as_float(pf[e] << 16) + __uint_as_float(pf[e] & 0xffff0000u);
#pragma unroll
        for (int n = 0; n < 8; ++n) o[n] = mfma(__builtin_amdgcn_raw_buffer_load_b128(srd, lane * 16u + 16384u + n * 1024u, tb, 16), pf, o[n] * al);
    }
    l += __shfl_xor(l, 16); l += __shfl_xor(l, 32);
    const float f = g1 / l;
    const __amdgpu_buffer_rsrc_t srdA = __builtin_amdgcn_make_buffer_rsrc((void*)(ws + WS_AF), (short)0, 128 * KS_TOT * 1024, 0x00020000);
#pragma unroll
    for (int p = 0; p < 4; ++p) {
        const f32x4 a = o[2 * p] * f, b = o[2 * p + 1] * f;
        __builtin_amdgcn_raw_buffer_store_b128((u32x4){cvtpk2(a[0], a[1]), cvtpk2(a[2], a[3]), cvtpk2(b[0], b[1]), cvtpk2(b[2], b[3])}, srdA, lane * 16u + p * 1024u, ((r0 >> 4) * KS_TOT + 18u + 4u * h) * 1024u, 16);
    }
}
}

#define ATT_ASM \
"s_branch L_ago_%=\n" \
".p2align 12\n" \
"L_ab:\n" \
".fill (L_ab + 0 - .) / 4, 4, 0xBF800000\n" \
"L_ap0:\n" \
"s_setpc_b64 s[88:89]\n" \
"L_ago_%=:\n" \
"s_mov_b32 s101, 0\n" \
"s_waitcnt vmcnt(15) lgkmcnt(0)\n" \
"s_barrier\n" \
"v_accvgpr_write_b32 a216, 0\n" \
"v_accvgpr_write_b32 a217, 0\n" \
"v_accvgpr_write_b32 a218, 0\n" \
"v_accvgpr_write_b32 a219, 0\n" \
"v_bfe_u32 v61, v20, 2, 5\n" \
"s_mov_b32 s96, s24\n" \
"v_mfma_f32_32x32x16_bf16 v[128:143], a[216:219], a[216:219], 0\n" \
"buffer_load_dword v57, v61, s[56:59], s96 offen sc1\n" \
"v_and_b32_e32 v63, 0x300, v20\n" \
"ds_read_b128 v[96:99], v63 offset:0\n" \
"v_add_u32_e32 v58, s22, v20\n" \
"ds_read_b128 a[0:3], v58 offset:0\n" \
"v_mfma_f32_32x32x16_bf16 v[144:159], a[216:219], a[216:219], 0\n" \
"ds_read_b128 a[16:19], v58 offset:8192\n" \
"v_add_u32_e32 v59, s23, v20\n" \
"ds_read_b128 a[32:35], v59 offset:0\n" \
"ds_read_b128 a[48:51], v59 offset:8192\n" \
"ds_read_b128 v[100:103], v63 offset:1024\n" \
"ds_read_b128 a[4:7], v58 offset:1024\n" \
"v_mfma_f32_32x32x16_bf16 v[160:175], a[216:219], a[216:219], 0\n" \
"ds_read_b128 a[20:23], v58 offset:9216\n" \
"ds_read_b128 a[36:39], v59 offset:1024\n" \
"ds_read_b128 a[52:55], v59 offset:9216\n" \
"s_sub_u32 s67, s12, 0xc000\n" \
"ds_read_b128 v[104:107], v63 offset:2048\n" \
"s_mov_b32 s65, s12\n" \
"v_mfma_f32_32x32x16_bf16 v[176:191], a[216:219], a[216:219], 0\n" \
"v_add_u32_e32 v55, s67, v20\n" \
"v_add_u32_e32 v52, 0xc000, v55\n" \
"s_xor_b32 m0, s65, 0x10000\n" \
"ds_read_b128 a[8:11], v58 offset:2048\n" \
"buffer_load_dwordx4 v52, s[8:11], 0 offen offset:0 sc1 lds\n" \
"v_mfma_f32_32x32x16_bf16 v[192:207], a[216:219], a[216:219], 0\n" \
"ds_read_b128 a[24:27], v58 offset:10240\n" \
"ds_read_b128 a[40:43], v59 offset:2048\n" \
"ds_read_b128 a[56:59], v59 offset:10240\n" \
"ds_read_b128 v[108:111], v63 offset:3072\n" \
"buffer_load_dwordx4 v52, s[8:11], 0 offen offset:1024 sc1 lds\n" \
"v_mfma_f32_32x32x16_bf16 v[208:223], a[216:219], a[216:219], 0\n" \
"ds_read_b128 a[12:15], v58 offset:3072\n" \
"ds_read_b128 a[28:31], v58 offset:11264\n" \
"ds_read_b128 a[44:47], v59 offset:3072\n" \
"ds_read_b128 a[60:63], v59 offset:11264\n" \
"s_waitcnt lgkmcnt(15)\n" \
"v_xor_b32_e32 v96, 0x80008000, v96\n" \
"v_mfma_f32_32x32x16_bf16 v[224:239], a[216:219], a[216:219], 0\n" \
"v_xor_b32_e32 v97, 0x80008000, v97\n" \
"v_xor_b32_e32 v98, 0x80008000, v98\n" \
"v_xor_b32_e32 v99, 0x80008000, v99\n" \
"ds_read_b128 a[160:163], v20 offset:0\n" \
"ds_read_b128 a[164:167], v20 offset:1024\n" \
"s_waitcnt lgkmcnt(15)\n" \
"v_xor_b32_e32 v100, 0x80008000, v100\n" \
"v_mfma_f32_32x32x16_bf16 v[240:255], a[216:219], a[216:219], 0\n" \
"v_xor_b32_e32 v101, 0x80008000, v101\n" \
"v_xor_b32_e32 v102, 0x80008000, v102\n" \
"v_xor_b32_e32 v103, 0x80008000, v103\n" \
"ds_read_b128 a[168:171], v20 offset:2048\n" \
"ds_read_b128 a[172:175], v20 offset:3072\n" \
"ds_read_b128 a[188:191], v20 offset:7168\n" \
"v_mfma_f32_16x16x32_bf16 a[192:195], a[216:219], a[216:219], 0\n" \
"ds_read_b128 a[184:187], v20 offset:6144\n" \
"s_waitcnt lgkmcnt(15)\n" \
"v_xor_b32_e32 v104, 0x80008000, v104\n" \
"v_mfma_f32_16x16x32_bf16 a[196:199], a[216:219], a[216:219], 0\n" \
"v_xor_b32_e32 v105, 0x80008000, v105\n" \
"v_xor_b32_e32 v106, 0x80008000, v106\n" \
"v_mfma_f32_16x16x32_bf16 v[32:35], v[96:99], a[0:3], 0\n" \
"v_xor_b32_e32 v107, 0x80008000, v107\n" \
"ds_read_b128 a[180:183], v20 offset:5120\n" \
"v_mfma_f32_16x16x32_bf16 v[36:39], v[96:99], a[16:19], 0\n" \
"ds_read_b128 a[176:179], v20 offset:4096\n" \
"s_waitcnt lgkmcnt(12)\n" \
"v_xor_b32_e32 v108, 0x80008000, v108\n" \
"v_mfma_f32_16x16x32_bf16 v[40:43], v[96:99], a[32:35], 0\n" \
"v_xor_b32_e32 v109, 0x80008000, v109\n" \
"v_xor_b32_e32 v110, 0x80008000, v110\n" \
"v_mfma_f32_16x16x32_bf16 v[44:47], v[96:99], a[48:51], 0\n" \
"v_xor_b32_e32 v111, 0x80008000, v111\n" \
"v_add_u32_e32 v60, s15, v20\n" \
"v_mfma_f32_16x16x32_bf16 v[32:35], v[100:103], a[4:7], v[32:35]\n" \
"v_add_u32_e32 v60, 0x10000, v60\n" \
"ds_read_b128 a[128:131], v20 offset:8192\n" \
"v_mfma_f32_16x16x32_bf16 v[36:39], v[100:103], a[20:23], v[36:39]\n" \
"ds_read_b128 a[144:147], v20 offset:12288\n" \
"ds_read_b128 a[132:135], v20 offset:9216\n" \
"v_mfma_f32_16x16x32_bf16 v[40:43], v[100:103], a[36:39], v[40:43]\n" \
"ds_read_b128 a[148:151], v20 offset:13312\n" \
"ds_read_b128 a[136:139], v20 offset:10240\n" \
"v_mfma_f32_16x16x32_bf16 v[44:47], v[100:103], a[52:55], v[44:47]\n" \
"ds_read_b128 a[152:155], v20 offset:14336\n" \
"ds_read_b128 a[140:143], v20 offset:11264\n" \
"v_mfma_f32_16x16x32_bf16 v[32:35], v[104:107], a[8:11], v[32:35]\n" \
"ds_read_b128 a[156:159], v20 offset:15360\n" \
"ds_read_b128 a[64:67], v60 offset:16384\n" \
"v_mfma_f32_16x16x32_bf16 v[36:39], v[104:107], a[24:27], v[36:39]\n" \
"ds_read_b128 a[68:71], v60 offset:17408\n" \
"ds_read_b128 a[72:75], v60 offset:18432\n" \
"v_mfma_f32_16x16x32_bf16 v[40:43], v[104:107], a[40:43], v[40:43]\n" \
"ds_read_b128 a[76:79], v60 offset:19456\n" \
"ds_read_b128 a[80:83], v60 offset:24576\n" \
"v_mfma_f32_16x16x32_bf16 v[44:47], v[104:107], a[56:59], v[44:47]\n" \
"ds_read_b128 a[84:87], v60 offset:25600\n" \
"s_waitcnt lgkmcnt(15)\n" \
"v_mfma_f32_16x16x32_bf16 v[32:35], v[108:111], a[12:15], v[32:35]\n" \
"ds_read_b128 a[88:91], v60 offset:26624\n" \
"ds_read_b128 a[92:95], v60 offset:27648\n" \
"v_mfma_f32_16x16x32_bf16 v[36:39], v[108:111], a[28:31], v[36:39]\n" \
"ds_read_b128 a[96:99], v60 offset:0\n" \
"ds_read_b128 a[100:103], v60 offset:1024\n" \
"v_mfma_f32_16x16x32_bf16 v[40:43], v[108:111], a[44:47], v[40:43]\n" \
"ds_read_b128 a[104:107], v60 offset:2048\n" \
"ds_read_b128 a[108:111], v60 offset:3072\n" \
"v_mfma_f32_16x16x32_bf16 v[44:47], v[108:111], a[60:63], v[44:47]\n" \
"ds_read_b128 a[112:115], v60 offset:8192\n" \
"ds_read_b128 a[116:119], v60 offset:9216\n" \
"v_mfma_f32_16x16x32_bf16 v[64:67], a[160:163], a[0:3], v[32:35]\n" \
"ds_read_b128 a[120:123], v60 offset:10240\n" \
"ds_read_b128 a[124:127], v60 offset:11264\n" \
"v_mfma_f32_16x16x32_bf16 v[64:67], a[164:167], a[4:7], v[64:67]\n" \
"v_bfe_u32 v26, v20, 8, 2\n" \
"s_mov_b32 m0, s20\n" \
"v_mfma_f32_16x16x32_bf16 v[64:67], a[168:171], a[8:11], v[64:67]\n" \
"v_bfe_u32 v25, v20, 5, 3\n" \
"v_lshlrev_b32_e32 v26, 1, v26\n" \
"v_mfma_f32_16x16x32_bf16 v[64:67], a[172:175], a[12:15], v[64:67]\n" \
"global_load_lds_dword v[16:17], off\n" \
"v_mfma_f32_16x16x32_bf16 v[68:71], a[188:191], a[12:15], v[32:35]\n" \
"v_sub_u32_e32 v25, v25, v26\n" \
"v_and_b32_e32 v26, 16, v20\n" \
"v_mfma_f32_16x16x32_bf16 v[68:71], a[184:187], a[8:11], v[68:71]\n" \
"v_mov_b32_e32 v27, 0x3f80\n" \
"v_lshlrev_b32_e32 v26, v26, v27\n" \
"v_mfma_f32_16x16x32_bf16 v[68:71], a[180:183], a[4:7], v[68:71]\n" \
"v_mov_b32_e32 v54, 0x3f803f80\n" \
"s_waitcnt lgkmcnt(15)\n" \
"v_mfma_f32_16x16x32_bf16 v[68:71], a[176:179], a[0:3], v[68:71]\n" \
"s_mov_b32 s84, 0x33333333\n" \
"s_mov_b32 s85, 0xcccccccc\n" \
"v_mfma_f32_16x16x32_bf16 v[76:79], a[176:179], a[16:19], v[36:39]\n" \
"s_mov_b32 s86, 0x11111111\n" \
"s_mov_b32 s87, 0x11111111\n" \
"v_mfma_f32_16x16x32_bf16 v[76:79], a[180:183], a[20:23], v[76:79]\n" \
"s_mov_b32 s88, 0x44444444\n" \
"s_mov_b32 s89, 0x44444444\n" \
"v_mfma_f32_16x16x32_bf16 v[76:79], a[184:187], a[24:27], v[76:79]\n" \
"v_exp_f32_e32 v70, v70\n" \
"v_mfma_f32_16x16x32_bf16 v[76:79], a[188:191], a[28:31], v[76:79]\n" \
"v_exp_f32_e32 v71, v71\n" \
"v_mfma_f32_16x16x32_bf16 v[72:75], a[172:175], a[28:31], v[36:39]\n" \
"v_exp_f32_e32 v68, v68\n" \
"v_mfma_f32_16x16x32_bf16 v[72:75], a[168:171], a[24:27], v[72:75]\n" \
"v_exp_f32_e32 v69, v69\n" \
"v_mfma_f32_16x16x32_bf16 v[72:75], a[164:167], a[20:23], v[72:75]\n" \
"v_bfe_i32 v21, v20, 4, 1\n" \
"v_cvt_pk_bf16_f32 v71, v70, v71\n" \
"v_mfma_f32_16x16x32_bf16 v[72:75], a[160:163], a[16:19], v[72:75]\n" \
"v_exp_f32_e32 v66, v66\n" \
"v_mfma_f32_16x16x32_bf16 v[80:83], a[160:163], a[32:35], v[40:43]\n" \
"v_exp_f32_e32 v67, v67\n" \
"v_mfma_f32_16x16x32_bf16 v[80:83], a[164:167], a[36:39], v[80:83]\n" \
"v_exp_f32_e32 v64, v64\n" \
"v_mfma_f32_16x16x32_bf16 v[80:83], a[168:171], a[40:43], v[80:83]\n" \
"v_exp_f32_e32 v65, v65\n" \
"v_mfma_f32_16x16x32_bf16 v[80:83], a[172:175], a[44:47], v[80:83]\n" \
"v_exp_f32_e32 v72, v72\n" \
"v_mfma_f32_16x16x32_bf16 v[84:87], a[188:191], a[44:47], v[40:43]\n" \
"s_waitcnt vmcnt(3)\n" \
"v_cmp_ne_u32_e32 vcc, s62, v57\n" \
"v_mfma_f32_16x16x32_bf16 v[84:87], a[184:187], a[40:43], v[84:87]\n" \
"v_exp_f32_e32 v73, v73\n" \
"v_mfma_f32_16x16x32_bf16 v[84:87], a[180:183], a[36:39], v[84:87]\n" \
"v_exp_f32_e32 v74, v74\n" \
"v_mfma_f32_16x16x32_bf16 v[84:87], a[176:179], a[32:35], v[84:87]\n" \
"s_cbranch_vccnz L_skb_%=\n" \
"L_rkb_%=:\n" \
"v_cmp_eq_u32_e32 vcc, 0, v25\n" \
"v_mfma_f32_16x16x32_bf16 v[92:95], a[176:179], a[48:51], v[44:47]\n" \
"v_exp_f32_e32 v75, v75\n" \
"v_mfma_f32_16x16x32_bf16 v[92:95], a[180:183], a[52:55], v[92:95]\n" \
"v_cndmask_b32_e32 v28, 0, v26, vcc\n" \
"v_cvt_pk_bf16_f32 v72, v72, v73\n" \
"v_mfma_f32_16x16x32_bf16 v[92:95], a[184:187], a[56:59], v[92:95]\n" \
"v_exp_f32_e32 v76, v76\n" \
"v_mfma_f32_16x16x32_bf16 v[92:95], a[188:191], a[60:63], v[92:95]\n" \
"v_exp_f32_e32 v77, v77\n" \
"v_mfma_f32_16x16x32_bf16 v[88:91], a[172:175], a[60:63], v[44:47]\n" \
"v_exp_f32_e32 v78, v78\n" \
"v_mfma_f32_16x16x32_bf16 v[88:91], a[168:171], a[56:59], v[88:91]\n" \
"v_exp_f32_e32 v79, v79\n" \
"v_mfma_f32_16x16x32_bf16 v[88:91], a[164:167], a[52:55], v[88:91]\n" \
"v_cndmask_b32_e64 v22, 0, v54, s[84:85]\n" \
"v_cndmask_b32_e64 v23, 0, v54, s[86:87]\n" \
"v_mfma_f32_16x16x32_bf16 v[88:91], a[160:163], a[48:51], v[88:91]\n" \
"v_cndmask_b32_e64 v24, 0, v54, s[88:89]\n" \
"v_and_b32_e32 v21, 0xaaaaaaaa, v21\n" \
"s_waitcnt vmcnt(4) lgkmcnt(0)\n" \
"s_barrier\n" \
"v_cmp_eq_u32_e32 vcc, 1, v25\n" \
"v_cvt_pk_bf16_f32 v70, v68, v69\n" \
"v_cvt_pk_bf16_f32 v73, v74, v75\n" \
"v_mov_b32_e32 v62, 0\n" \
"v_accvgpr_write_b32 a208, 0\n" \
"v_accvgpr_write_b32 a209, 0\n" \
"v_accvgpr_write_b32 a210, 0\n" \
"v_accvgpr_write_b32 a211, 0\n" \
"v_accvgpr_write_b32 a200, v22\n" \
"v_accvgpr_write_b32 a201, v22\n" \
"v_accvgpr_write_b32 a202, v22\n" \
"v_accvgpr_write_b32 a203, v22\n" \
"v_accvgpr_write_b32 a204, v23\n" \
"v_accvgpr_write_b32 a205, v23\n" \
"v_accvgpr_write_b32 a206, v23\n" \
"v_accvgpr_write_b32 a207, v23\n" \
"v_accvgpr_write_b32 a212, v24\n" \
"v_accvgpr_write_b32 a213, v24\n" \
"v_accvgpr_write_b32 a214, v24\n" \
"v_accvgpr_write_b32 a215, v24\n" \
"v_or_b32_e32 v21, 0x44444444, v21\n" \
"s_mov_b32 s64, 0x8000\n" \
"v_add_u32_e32 v50, 0x6000, v55\n" \
"v_add_u32_e32 v51, 0xa000, v55\n" \
"v_add_u32_e32 v53, 0x10000, v55\n" \
"v_add_u32_e32 v48, 0xc000, v20\n" \
"v_add_u32_e32 v49, s17, v20\n" \
"v_cndmask_b32_e32 v29, 0, v26, vcc\n" \
"v_accvgpr_write_b32 a220, v96\n" \
"v_accvgpr_write_b32 a221, v97\n" \
"v_accvgpr_write_b32 a222, v98\n" \
"v_accvgpr_write_b32 a223, v99\n" \
"v_accvgpr_write_b32 a224, v100\n" \
"v_accvgpr_write_b32 a225, v101\n" \
"v_accvgpr_write_b32 a226, v102\n" \
"v_accvgpr_write_b32 a227, v103\n" \
"v_accvgpr_write_b32 a228, v104\n" \
"v_accvgpr_write_b32 a229, v105\n" \
"v_accvgpr_write_b32 a230, v106\n" \
"v_accvgpr_write_b32 a231, v107\n" \
"v_accvgpr_write_b32 a232, v108\n" \
"v_accvgpr_write_b32 a233, v109\n" \
"v_accvgpr_write_b32 a234, v110\n" \
"v_accvgpr_write_b32 a235, v111\n" \
"v_cvt_pk_bf16_f32 v69, v66, v67\n" \
"v_cvt_pk_bf16_f32 v68, v64, v65\n" \
"v_cvt_pk_bf16_f32 v74, v76, v77\n" \
"v_cvt_pk_bf16_f32 v75, v78, v79\n" \
"s_sub_u32 s66, s18, 1\n" \
"s_cbranch_scc1 L_tr_%=\n" \
".fill (L_ab + 1856 - .) / 4, 4, 0xBF800000\n" \
"L_1_%=:\n" \
"v_mfma_f32_16x16x32_bf16 v[96:99], a[128:131], a[0:3], v[32:35]\n" \
"s_xor_b32 m0, s65, 0x12000\n" \
"ds_read_b128 a[160:163], v49 offset:0\n" \
"v_mfma_f32_16x16x32_bf16 v[96:99], a[132:135], a[4:7], v[96:99]\n" \
"buffer_load_dwordx4 v50, s[8:11], s64 offen offset:0 sc1 lds\n" \
"v_mfma_f32_16x16x32_bf16 v[96:99], a[136:139], a[8:11], v[96:99]\n" \
"ds_read_b128 a[164:167], v49 offset:1024\n" \
"ds_read_b128 a[168:171], v49 offset:2048\n" \
"v_mfma_f32_16x16x32_bf16 v[96:99], a[140:143], a[12:15], v[96:99]\n" \
"ds_read_b128 a[172:175], v49 offset:3072\n" \
"ds_read_b128 a[176:179], v49 offset:4096\n" \
"v_mfma_f32_16x16x32_bf16 v[100:103], a[156:159], a[12:15], v[32:35]\n" \
"ds_read_b128 a[180:183], v49 offset:5120\n" \
"ds_read_b128 a[184:187], v49 offset:6144\n" \
"v_mfma_f32_16x16x32_bf16 v[100:103], a[152:155], a[8:11], v[100:103]\n" \
"v_exp_f32_e64 v86, v86\n" \
"v_mfma_f32_16x16x32_bf16 v[100:103], a[148:151], a[4:7], v[100:103]\n" \
"v_exp_f32_e64 v87, v87\n" \
"v_mfma_f32_16x16x32_bf16 v[100:103], a[144:147], a[0:3], v[100:103]\n" \
"ds_read_b128 a[188:191], v49 offset:7168\n" \
"v_cvt_pk_bf16_f32 v87, v86, v87\n" \
"v_mfma_f32_16x16x32_bf16 v[108:111], a[144:147], a[16:19], v[36:39]\n" \
"v_exp_f32_e64 v84, v84\n" \
"v_mfma_f32_16x16x32_bf16 v[108:111], a[148:151], a[20:23], v[108:111]\n" \
"v_exp_f32_e64 v85, v85\n" \
"v_mfma_f32_16x16x32_bf16 v[108:111], a[152:155], a[24:27], v[108:111]\n" \
"v_exp_f32_e64 v82, v82\n" \
"v_mfma_f32_16x16x32_bf16 v[108:111], a[156:159], a[28:31], v[108:111]\n" \
"v_exp_f32_e64 v83, v83\n" \
"v_mfma_f32_16x16x32_bf16 v[104:107], a[140:143], a[28:31], v[36:39]\n" \
"v_exp_f32_e64 v80, v80\n" \
"v_mfma_f32_16x16x32_bf16 v[104:107], a[136:139], a[24:27], v[104:107]\n" \
"v_exp_f32_e64 v81, v81\n" \
"v_mfma_f32_16x16x32_bf16 v[104:107], a[132:135], a[20:23], v[104:107]\n" \
"v_exp_f32_e64 v88, v88\n" \
"v_mfma_f32_16x16x32_bf16 v[104:107], a[128:131], a[16:19], v[104:107]\n" \
"buffer_load_dwordx4 v50, s[8:11], s64 offen offset:1024 sc1 lds\n" \
"v_mfma_f32_16x16x32_bf16 v[112:115], a[128:131], a[32:35], v[40:43]\n" \
"s_xor_b32 m0, s65, 0x6000\n" \
"v_cvt_pk_bf16_f32 v86, v84, v85\n" \
"v_mfma_f32_16x16x32_bf16 v[112:115], a[132:135], a[36:39], v[112:115]\n" \
"v_cvt_pk_bf16_f32 v85, v82, v83\n" \
"v_cvt_pk_bf16_f32 v84, v80, v81\n" \
"v_mfma_f32_16x16x32_bf16 v[112:115], a[136:139], a[40:43], v[112:115]\n" \
"v_exp_f32_e64 v89, v89\n" \
"v_mfma_f32_16x16x32_bf16 v[112:115], a[140:143], a[44:47], v[112:115]\n" \
"v_exp_f32_e64 v90, v90\n" \
"v_mfma_f32_16x16x32_bf16 v[116:119], a[156:159], a[44:47], v[40:43]\n" \
"v_exp_f32_e64 v91, v91\n" \
"v_mfma_f32_16x16x32_bf16 v[116:119], a[152:155], a[40:43], v[116:119]\n" \
"v_exp_f32_e64 v92, v92\n" \
"v_mfma_f32_16x16x32_bf16 v[116:119], a[148:151], a[36:39], v[116:119]\n" \
"v_exp_f32_e64 v93, v93\n" \
"v_mfma_f32_16x16x32_bf16 v[116:119], a[144:147], a[32:35], v[116:119]\n" \
"v_exp_f32_e64 v94, v94\n" \
"v_mfma_f32_16x16x32_bf16 v[124:127], a[144:147], a[48:51], v[44:47]\n" \
"v_cvt_pk_bf16_f32 v88, v88, v89\n" \
"v_cvt_pk_bf16_f32 v89, v90, v91\n" \
"v_mfma_f32_16x16x32_bf16 v[124:127], a[148:151], a[52:55], v[124:127]\n" \
"v_exp_f32_e64 v95, v95\n" \
"v_mfma_f32_16x16x32_bf16 v[124:127], a[152:155], a[56:59], v[124:127]\n" \
"v_cvt_pk_bf16_f32 v90, v92, v93\n" \
"v_cvt_pk_bf16_f32 v91, v94, v95\n" \
"v_mfma_f32_16x16x32_bf16 v[124:127], a[156:159], a[60:63], v[124:127]\n" \
"ds_read_b128 a[144:147], v48 offset:4096\n" \
"ds_read_b128 a[148:151], v48 offset:5120\n" \
"v_mfma_f32_16x16x32_bf16 v[120:123], a[140:143], a[60:63], v[44:47]\n" \
"ds_read_b128 a[152:155], v48 offset:6144\n" \
"ds_read_b128 a[140:143], v48 offset:3072\n" \
"v_mfma_f32_16x16x32_bf16 v[120:123], a[136:139], a[56:59], v[120:123]\n" \
"ds_read_b128 a[136:139], v48 offset:2048\n" \
"ds_read_b128 a[156:159], v48 offset:7168\n" \
"v_mfma_f32_16x16x32_bf16 v[120:123], a[132:135], a[52:55], v[120:123]\n" \
"buffer_load_dwordx4 v51, s[8:11], s64 offen offset:0 sc1 lds\n" \
"v_mfma_f32_16x16x32_bf16 v[120:123], a[128:131], a[48:51], v[120:123]\n" \
"ds_read_b128 a[128:131], v48 offset:0\n" \
"s_waitcnt lgkmcnt(7)\n" \
"v_mfma_f32_16x16x32_bf16 v[128:131], a[160:163], v[68:71], v[128:131]\n" \
"v_exp_f32_e32 v102, v102\n" \
"v_mfma_f32_16x16x32_bf16 v[160:163], a[160:163], v[72:75], v[160:163]\n" \
"v_exp_f32_e64 v103, v103\n" \
"v_mfma_f32_16x16x32_bf16 v[132:135], a[164:167], v[68:71], v[132:135]\n" \
"ds_read_b128 a[132:135], v48 offset:1024\n" \
"v_cvt_pk_bf16_f32 v103, v102, v103\n" \
"v_mfma_f32_16x16x32_bf16 v[164:167], a[164:167], v[72:75], v[164:167]\n" \
"v_exp_f32_e64 v100, v100\n" \
"v_mfma_f32_16x16x32_bf16 v[136:139], a[168:171], v[68:71], v[136:139]\n" \
"v_exp_f32_e64 v101, v101\n" \
"v_mfma_f32_16x16x32_bf16 v[168:171], a[168:171], v[72:75], v[168:171]\n" \
"v_exp_f32_e64 v98, v98\n" \
"v_mfma_f32_16x16x32_bf16 v[140:143], a[172:175], v[68:71], v[140:143]\n" \
"v_exp_f32_e64 v99, v99\n" \
"v_mfma_f32_16x16x32_bf16 v[172:175], a[172:175], v[72:75], v[172:175]\n" \
"v_exp_f32_e64 v96, v96\n" \
"v_mfma_f32_16x16x32_bf16 v[144:147], a[176:179], v[68:71], v[144:147]\n" \
"v_exp_f32_e64 v97, v97\n" \
"v_mfma_f32_16x16x32_bf16 v[176:179], a[176:179], v[72:75], v[176:179]\n" \
"v_exp_f32_e64 v104, v104\n" \
"v_mfma_f32_16x16x32_bf16 v[148:151], a[180:183], v[68:71], v[148:151]\n" \
"v_exp_f32_e64 v105, v105\n" \
"v_mfma_f32_16x16x32_bf16 v[180:183], a[180:183], v[72:75], v[180:183]\n" \
"v_cvt_pk_bf16_f32 v102, v100, v101\n" \
"v_cvt_pk_bf16_f32 v101, v98, v99\n" \
"v_mfma_f32_16x16x32_bf16 v[152:155], a[184:187], v[68:71], v[152:155]\n" \
"v_cvt_pk_bf16_f32 v100, v96, v97\n" \
"v_cvt_pk_bf16_f32 v104, v104, v105\n" \
"v_mfma_f32_16x16x32_bf16 v[184:187], a[184:187], v[72:75], v[184:187]\n" \
"buffer_load_dwordx4 v51, s[8:11], s64 offen offset:1024 sc1 lds\n" \
"v_mfma_f32_16x16x32_bf16 v[156:159], a[188:191], v[68:71], v[156:159]\n" \
"v_exp_f32_e64 v106, v106\n" \
"v_mfma_f32_16x16x32_bf16 v[188:191], a[188:191], v[72:75], v[188:191]\n" \
"v_exp_f32_e64 v107, v107\n" \
"v_smfmac_f32_16x16x64_bf16 a[192:195], a[200:203], v[68:75], v21\n" \
"v_exp_f32_e64 v108, v108\n" \
"v_mfma_f32_16x16x32_bf16 v[220:223], a[188:191], v[84:87], v[220:223]\n" \
"v_exp_f32_e64 v109, v109\n" \
"v_mfma_f32_16x16x32_bf16 v[252:255], a[188:191], v[88:91], v[252:255]\n" \
"v_exp_f32_e64 v110, v110\n" \
"v_mfma_f32_16x16x32_bf16 v[216:219], a[184:187], v[84:87], v[216:219]\n" \
"v_exp_f32_e64 v111, v111\n" \
"v_mfma_f32_16x16x32_bf16 v[248:251], a[184:187], v[88:91], v[248:251]\n" \
"v_exp_f32_e64 v118, v118\n" \
"v_mfma_f32_16x16x32_bf16 v[212:215], a[180:183], v[84:87], v[212:215]\n" \
"v_exp_f32_e64 v119, v119\n" \
"v_mfma_f32_16x16x32_bf16 v[244:247], a[180:183], v[88:91], v[244:247]\n" \
"v_cvt_pk_bf16_f32 v105, v106, v107\n" \
"v_cvt_pk_bf16_f32 v106, v108, v109\n" \
"v_mfma_f32_16x16x32_bf16 v[208:211], a[176:179], v[84:87], v[208:211]\n" \
"v_cvt_pk_bf16_f32 v107, v110, v111\n" \
"v_cvt_pk_bf16_f32 v119, v118, v119\n" \
"v_mfma_f32_16x16x32_bf16 v[240:243], a[176:179], v[88:91], v[240:243]\n" \
"v_exp_f32_e64 v116, v116\n" \
"v_mfma_f32_16x16x32_bf16 v[204:207], a[172:175], v[84:87], v[204:207]\n" \
"v_exp_f32_e64 v117, v117\n" \
"v_mfma_f32_16x16x32_bf16 v[236:239], a[172:175], v[88:91], v[236:239]\n" \
"v_exp_f32_e64 v114, v114\n" \
"v_mfma_f32_16x16x32_bf16 v[200:203], a[168:171], v[84:87], v[200:203]\n" \
"v_exp_f32_e64 v115, v115\n" \
"v_mfma_f32_16x16x32_bf16 v[232:235], a[168:171], v[88:91], v[232:235]\n" \
"v_exp_f32_e64 v112, v112\n" \
"v_mfma_f32_16x16x32_bf16 v[196:199], a[164:167], v[84:87], v[196:199]\n" \
"v_exp_f32_e64 v113, v113\n" \
"v_mfma_f32_16x16x32_bf16 v[228:231], a[164:167], v[88:91], v[228:231]\n" \
"v_exp_f32_e64 v120, v120\n" \
"v_mfma_f32_16x16x32_bf16 v[192:195], a[160:163], v[84:87], v[192:195]\n" \
"v_exp_f32_e64 v121, v121\n" \
"v_mfma_f32_16x16x32_bf16 v[224:227], a[160:163], v[88:91], v[224:227]\n" \
"v_cvt_pk_bf16_f32 v118, v116, v117\n" \
"v_cvt_pk_bf16_f32 v117, v114, v115\n" \
"v_smfmac_f32_16x16x64_bf16 a[196:199], a[200:203], v[84:91], v21\n" \
"v_cvt_pk_bf16_f32 v116, v112, v113\n" \
"v_cvt_pk_bf16_f32 v120, v120, v121\n" \
"s_waitcnt vmcnt(4) lgkmcnt(0)\n" \
"s_barrier\n" \
"v_mfma_f32_16x16x32_bf16 v[64:67], a[128:131], a[0:3], v[32:35]\n" \
"s_xor_b32 m0, s65, 0x14000\n" \
"ds_read_b128 a[160:163], v49 offset:8192\n" \
"v_mfma_f32_16x16x32_bf16 v[64:67], a[132:135], a[4:7], v[64:67]\n" \
"buffer_load_dwordx4 v52, s[8:11], s64 offen offset:0 sc1 lds\n" \
"v_mfma_f32_16x16x32_bf16 v[64:67], a[136:139], a[8:11], v[64:67]\n" \
"ds_read_b128 a[164:167], v49 offset:9216\n" \
"ds_read_b128 a[168:171], v49 offset:10240\n" \
"v_mfma_f32_16x16x32_bf16 v[64:67], a[140:143], a[12:15], v[64:67]\n" \
"ds_read_b128 a[172:175], v49 offset:11264\n" \
"ds_read_b128 a[176:179], v49 offset:12288\n" \
"v_mfma_f32_16x16x32_bf16 v[68:71], a[156:159], a[12:15], v[32:35]\n" \
"ds_read_b128 a[180:183], v49 offset:13312\n" \
"ds_read_b128 a[184:187], v49 offset:14336\n" \
"v_mfma_f32_16x16x32_bf16 v[68:71], a[152:155], a[8:11], v[68:71]\n" \
"v_exp_f32_e64 v122, v122\n" \
"v_mfma_f32_16x16x32_bf16 v[68:71], a[148:151], a[4:7], v[68:71]\n" \
"v_exp_f32_e64 v123, v123\n" \
"v_mfma_f32_16x16x32_bf16 v[68:71], a[144:147], a[0:3], v[68:71]\n" \
"ds_read_b128 a[188:191], v49 offset:15360\n" \
"v_cvt_pk_bf16_f32 v121, v122, v123\n" \
"v_mfma_f32_16x16x32_bf16 v[76:79], a[144:147], a[16:19], v[36:39]\n" \
"v_exp_f32_e64 v124, v124\n" \
"v_mfma_f32_16x16x32_bf16 v[76:79], a[148:151], a[20:23], v[76:79]\n" \
"v_exp_f32_e64 v125, v125\n" \
"v_mfma_f32_16x16x32_bf16 v[76:79], a[152:155], a[24:27], v[76:79]\n" \
"v_exp_f32_e64 v126, v126\n" \
"v_mfma_f32_16x16x32_bf16 v[76:79], a[156:159], a[28:31], v[76:79]\n" \
"v_exp_f32_e64 v127, v127\n" \
"v_mfma_f32_16x16x32_bf16 v[72:75], a[140:143], a[28:31], v[36:39]\n" \
"v_cvt_pk_bf16_f32 v122, v124, v125\n" \
"v_cvt_pk_bf16_f32 v123, v126, v127\n" \
"v_mfma_f32_16x16x32_bf16 v[72:75], a[136:139], a[24:27], v[72:75]\n" \
"v_exp_f32_e64 v70, v70\n" \
"v_mfma_f32_16x16x32_bf16 v[72:75], a[132:135], a[20:23], v[72:75]\n" \
"v_exp_f32_e64 v71, v71\n" \
"v_mfma_f32_16x16x32_bf16 v[72:75], a[128:131], a[16:19], v[72:75]\n" \
"buffer_load_dwordx4 v52, s[8:11], s64 offen offset:1024 sc1 lds\n" \
"v_mfma_f32_16x16x32_bf16 v[80:83], a[128:131], a[32:35], v[40:43]\n" \
"s_mov_b32 m0, s65\n" \
"v_cvt_pk_bf16_f32 v71, v70, v71\n" \
"v_mfma_f32_16x16x32_bf16 v[80:83], a[132:135], a[36:39], v[80:83]\n" \
"v_exp_f32_e32 v68, v68\n" \
"v_mfma_f32_16x16x32_bf16 v[80:83], a[136:139], a[40:43], v[80:83]\n" \
"v_exp_f32_e64 v69, v69\n" \
"v_mfma_f32_16x16x32_bf16 v[80:83], a[140:143], a[44:47], v[80:83]\n" \
"v_exp_f32_e64 v72, v72\n" \
"v_mfma_f32_16x16x32_bf16 v[84:87], a[156:159], a[44:47], v[40:43]\n" \
"v_exp_f32_e64 v73, v73\n" \
"v_mfma_f32_16x16x32_bf16 v[84:87], a[152:155], a[40:43], v[84:87]\n" \
"v_exp_f32_e64 v74, v74\n" \
"v_mfma_f32_16x16x32_bf16 v[84:87], a[148:151], a[36:39], v[84:87]\n" \
"v_exp_f32_e64 v75, v75\n" \
"v_mfma_f32_16x16x32_bf16 v[84:87], a[144:147], a[32:35], v[84:87]\n" \
"v_exp_f32_e64 v66, v66\n" \
"v_mfma_f32_16x16x32_bf16 v[92:95], a[144:147], a[48:51], v[44:47]\n" \
"ds_read_b128 a[144:147], v48 offset:12288\n" \
"v_cvt_pk_bf16_f32 v72, v72, v73\n" \
"v_mfma_f32_16x16x32_bf16 v[92:95], a[148:151], a[52:55], v[92:95]\n" \
"ds_read_b128 a[148:151], v48 offset:13312\n" \
"v_cvt_pk_bf16_f32 v70, v68, v69\n" \
"v_mfma_f32_16x16x32_bf16 v[92:95], a[152:155], a[56:59], v[92:95]\n" \
"ds_read_b128 a[152:155], v48 offset:14336\n" \
"v_cvt_pk_bf16_f32 v73, v74, v75\n" \
"v_mfma_f32_16x16x32_bf16 v[92:95], a[156:159], a[60:63], v[92:95]\n" \
"ds_read_b128 a[156:159], v48 offset:15360\n" \
"v_add_u32_e32 v49, 0x10000, v48\n" \
"v_mfma_f32_16x16x32_bf16 v[88:91], a[140:143], a[60:63], v[44:47]\n" \
"ds_read_b128 a[140:143], v48 offset:11264\n" \
"s_xor_b32 s65, s65, 0x4000\n" \
"v_mfma_f32_16x16x32_bf16 v[88:91], a[136:139], a[56:59], v[88:91]\n" \
"v_exp_f32_e64 v67, v67\n" \
"v_mfma_f32_16x16x32_bf16 v[88:91], a[132:135], a[52:55], v[88:91]\n" \
"buffer_load_dwordx4 v53, s[8:11], s64 offen offset:0 sc1 lds\n" \
"v_mfma_f32_16x16x32_bf16 v[88:91], a[128:131], a[48:51], v[88:91]\n" \
"ds_read_b128 a[128:131], v48 offset:8192\n" \
"s_waitcnt lgkmcnt(6)\n" \
"v_mfma_f32_16x16x32_bf16 v[128:131], a[160:163], v[100:103], v[128:131]\n" \
"ds_read_b128 a[132:135], v48 offset:9216\n" \
"ds_read_b128 a[136:139], v48 offset:10240\n" \
"v_mfma_f32_16x16x32_bf16 v[160:163], a[160:163], v[104:107], v[160:163]\n" \
"v_exp_f32_e32 v64, v64\n" \
"v_mfma_f32_16x16x32_bf16 v[132:135], a[164:167], v[100:103], v[132:135]\n" \
"v_exp_f32_e64 v65, v65\n" \
"v_mfma_f32_16x16x32_bf16 v[164:167], a[164:167], v[104:107], v[164:167]\n" \
"v_exp_f32_e64 v76, v76\n" \
"v_mfma_f32_16x16x32_bf16 v[136:139], a[168:171], v[100:103], v[136:139]\n" \
"v_exp_f32_e64 v77, v77\n" \
"v_mfma_f32_16x16x32_bf16 v[168:171], a[168:171], v[104:107], v[168:171]\n" \
"v_exp_f32_e64 v78, v78\n" \
"v_mfma_f32_16x16x32_bf16 v[140:143], a[172:175], v[100:103], v[140:143]\n" \
"v_exp_f32_e64 v79, v79\n" \
"v_mfma_f32_16x16x32_bf16 v[172:175], a[172:175], v[104:107], v[172:175]\n" \
"v_cvt_pk_bf16_f32 v69, v66, v67\n" \
"v_cvt_pk_bf16_f32 v68, v64, v65\n" \
"v_mfma_f32_16x16x32_bf16 v[144:147], a[176:179], v[100:103], v[144:147]\n" \
"v_cvt_pk_bf16_f32 v74, v76, v77\n" \
"v_cvt_pk_bf16_f32 v75, v78, v79\n" \
"v_mfma_f32_16x16x32_bf16 v[176:179], a[176:179], v[104:107], v[176:179]\n" \
"v_xor_b32_e32 v48, 0x4000, v48\n" \
"v_mfma_f32_16x16x32_bf16 v[148:151], a[180:183], v[100:103], v[148:151]\n" \
"v_mfma_f32_16x16x32_bf16 v[180:183], a[180:183], v[104:107], v[180:183]\n" \
"v_mfma_f32_16x16x32_bf16 v[152:155], a[184:187], v[100:103], v[152:155]\n" \
"v_mfma_f32_16x16x32_bf16 v[184:187], a[184:187], v[104:107], v[184:187]\n" \
"buffer_load_dwordx4 v53, s[8:11], s64 offen offset:1024 sc1 lds\n" \
"v_mfma_f32_16x16x32_bf16 v[156:159], a[188:191], v[100:103], v[156:159]\n" \
"s_add_u32 s64, s64, 0x8000\n" \
"s_sub_u32 s66, s66, 1\n" \
"s_nop 0\n" \
"v_mfma_f32_16x16x32_bf16 v[188:191], a[188:191], v[104:107], v[188:191]\n" \
"v_smfmac_f32_16x16x64_bf16 a[192:195], a[200:203], v[100:107], v21\n" \
"v_mfma_f32_16x16x32_bf16 v[220:223], a[188:191], v[116:119], v[220:223]\n" \
"v_mfma_f32_16x16x32_bf16 v[252:255], a[188:191], v[120:123], v[252:255]\n" \
"v_mfma_f32_16x16x32_bf16 v[216:219], a[184:187], v[116:119], v[216:219]\n" \
"v_mfma_f32_16x16x32_bf16 v[248:251], a[184:187], v[120:123], v[248:251]\n" \
"v_mfma_f32_16x16x32_bf16 v[212:215], a[180:183], v[116:119], v[212:215]\n" \
"v_mfma_f32_16x16x32_bf16 v[244:247], a[180:183], v[120:123], v[244:247]\n" \
"v_mfma_f32_16x16x32_bf16 v[208:211], a[176:179], v[116:119], v[208:211]\n" \
"v_mfma_f32_16x16x32_bf16 v[240:243], a[176:179], v[120:123], v[240:243]\n" \
"v_mfma_f32_16x16x32_bf16 v[204:207], a[172:175], v[116:119], v[204:207]\n" \
"v_mfma_f32_16x16x32_bf16 v[236:239], a[172:175], v[120:123], v[236:239]\n" \
"v_mfma_f32_16x16x32_bf16 v[200:203], a[168:171], v[116:119], v[200:203]\n" \
"v_mfma_f32_16x16x32_bf16 v[232:235], a[168:171], v[120:123], v[232:235]\n" \
"v_mfma_f32_16x16x32_bf16 v[196:199], a[164:167], v[116:119], v[196:199]\n" \
"v_mfma_f32_16x16x32_bf16 v[228:231], a[164:167], v[120:123], v[228:231]\n" \
"v_mfma_f32_16x16x32_bf16 v[192:195], a[160:163], v[116:119], v[192:195]\n" \
"v_mfma_f32_16x16x32_bf16 v[224:227], a[160:163], v[120:123], v[224:227]\n" \
"v_smfmac_f32_16x16x64_bf16 a[196:199], a[200:203], v[116:123], v21\n" \
"s_waitcnt vmcnt(4) lgkmcnt(0)\n" \
"s_barrier\n" \
"s_cbranch_scc0 L_1_%=\n" \
"L_tr_%=:\n" \
"s_setpc_b64 s[28:29]\n" \
".fill (L_ab + 4096 - .) / 4, 4, 0xBF800000\n" \
".fill (L_ab + 4096 - .) / 4, 4, 0xBF800000\n" \
"L_ap1:\n" \
"s_setpc_b64 s[88:89]\n" \
"L_gtr:\n" \
"v_mfma_f32_16x16x32_bf16 v[96:99], a[128:131], a[0:3], v[32:35]\n" \
"v_mov_b32_e32 v56, s62\n" \
"v_readlane_b32 s68, v11, 0\n" \
"v_mfma_f32_16x16x32_bf16 v[96:99], a[132:135], a[4:7], v[96:99]\n" \
"s_xor_b32 m0, s65, 0x12000\n" \
"s_mov_b32 s65, s13\n" \
"v_mfma_f32_16x16x32_bf16 v[96:99], a[136:139], a[8:11], v[96:99]\n" \
"buffer_load_dwordx4 v50, s[8:11], s64 offen offset:0 sc1 lds\n" \
"v_mfma_f32_16x16x32_bf16 v[96:99], a[140:143], a[12:15], v[96:99]\n" \
"v_add_u32_e32 v63, s15, v20\n" \
"s_add_u32 s67, s14, 40960\n" \
"v_mfma_f32_16x16x32_bf16 v[100:103], a[156:159], a[12:15], v[32:35]\n" \
"buffer_load_dwordx4 v50, s[8:11], s64 offen offset:1024 sc1 lds\n" \
"s_mov_b64 exec, 1\n" \
"buffer_store_dword v56, off, s[56:59], s68 offset:0 sc1\n" \
"s_mov_b64 exec, -1\n" \
"v_mfma_f32_16x16x32_bf16 v[100:103], a[152:155], a[8:11], v[100:103]\n" \
"v_add_u32_e32 v50, 0x6000, v63\n" \
"s_xor_b32 m0, s65, 0x8000\n" \
"v_mfma_f32_16x16x32_bf16 v[100:103], a[148:151], a[4:7], v[100:103]\n" \
"ds_read_b128 a[160:163], v49 offset:0\n" \
"ds_read_b128 a[164:167], v49 offset:1024\n" \
"v_mfma_f32_16x16x32_bf16 v[100:103], a[144:147], a[0:3], v[100:103]\n" \
"ds_read_b128 a[168:171], v49 offset:2048\n" \
"ds_read_b128 a[172:175], v49 offset:3072\n" \
"v_mfma_f32_16x16x32_bf16 v[108:111], a[144:147], a[16:19], v[36:39]\n" \
"buffer_load_dwordx4 v50, s[8:11], s67 offen offset:0 sc1 lds\n" \
"v_mfma_f32_16x16x32_bf16 v[108:111], a[148:151], a[20:23], v[108:111]\n" \
"ds_read_b128 a[176:179], v49 offset:4096\n" \
"ds_read_b128 a[180:183], v49 offset:5120\n" \
"v_mfma_f32_16x16x32_bf16 v[108:111], a[152:155], a[24:27], v[108:111]\n" \
"ds_read_b128 a[184:187], v49 offset:6144\n" \
"ds_read_b128 a[188:191], v49 offset:7168\n" \
"v_mfma_f32_16x16x32_bf16 v[108:111], a[156:159], a[28:31], v[108:111]\n" \
"s_mov_b32 s64, s14\n" \
"v_add_u32_e32 v51, 0x12000, v63\n" \
"v_mfma_f32_16x16x32_bf16 v[104:107], a[140:143], a[28:31], v[36:39]\n" \
"buffer_load_dwordx4 v50, s[8:11], s67 offen offset:1024 sc1 lds\n" \
"v_mfma_f32_16x16x32_bf16 v[104:107], a[136:139], a[24:27], v[104:107]\n" \
"v_exp_f32_e32 v86, v86\n" \
"v_mfma_f32_16x16x32_bf16 v[104:107], a[132:135], a[20:23], v[104:107]\n" \
"v_exp_f32_e32 v87, v87\n" \
"v_mfma_f32_16x16x32_bf16 v[104:107], a[128:131], a[16:19], v[104:107]\n" \
"v_exp_f32_e32 v84, v84\n" \
"v_mfma_f32_16x16x32_bf16 v[112:115], a[128:131], a[32:35], v[40:43]\n" \
"v_exp_f32_e32 v85, v85\n" \
"v_mfma_f32_16x16x32_bf16 v[112:115], a[132:135], a[36:39], v[112:115]\n" \
"buffer_load_dwordx4 v50, s[8:11], s67 offen offset:2048 sc1 lds\n" \
"v_mfma_f32_16x16x32_bf16 v[112:115], a[136:139], a[40:43], v[112:115]\n" \
"v_cvt_pk_bf16_f32 v87, v86, v87\n" \
"v_cvt_pk_bf16_f32 v86, v84, v85\n" \
"v_mfma_f32_16x16x32_bf16 v[112:115], a[140:143], a[44:47], v[112:115]\n" \
"v_exp_f32_e32 v82, v82\n" \
"v_mfma_f32_16x16x32_bf16 v[116:119], a[156:159], a[44:47], v[40:43]\n" \
"v_exp_f32_e32 v83, v83\n" \
"v_mfma_f32_16x16x32_bf16 v[116:119], a[152:155], a[40:43], v[116:119]\n" \
"buffer_load_dwordx4 v50, s[8:11], s67 offen offset:3072 sc1 lds\n" \
"v_mfma_f32_16x16x32_bf16 v[116:119], a[148:151], a[36:39], v[116:119]\n" \
"s_xor_b32 m0, s65, 0xa000\n" \
"v_cvt_pk_bf16_f32 v85, v82, v83\n" \
"v_mfma_f32_16x16x32_bf16 v[116:119], a[144:147], a[32:35], v[116:119]\n" \
"v_exp_f32_e32 v80, v80\n" \
"v_mfma_f32_16x16x32_bf16 v[124:127], a[144:147], a[48:51], v[44:47]\n" \
"v_exp_f32_e32 v81, v81\n" \
"v_mfma_f32_16x16x32_bf16 v[124:127], a[148:151], a[52:55], v[124:127]\n" \
"buffer_load_dwordx4 v51, s[8:11], s64 offen offset:0 sc1 lds\n" \
"v_mfma_f32_16x16x32_bf16 v[124:127], a[152:155], a[56:59], v[124:127]\n" \
"v_cvt_pk_bf16_f32 v84, v80, v81\n" \
"v_readlane_b32 s93, v11, 19\n" \
"v_mfma_f32_16x16x32_bf16 v[124:127], a[156:159], a[60:63], v[124:127]\n" \
"v_exp_f32_e32 v88, v88\n" \
"v_mfma_f32_16x16x32_bf16 v[120:123], a[140:143], a[60:63], v[44:47]\n" \
"v_exp_f32_e32 v89, v89\n" \
"v_mfma_f32_16x16x32_bf16 v[120:123], a[136:139], a[56:59], v[120:123]\n" \
"v_exp_f32_e32 v90, v90\n" \
"v_mfma_f32_16x16x32_bf16 v[120:123], a[132:135], a[52:55], v[120:123]\n" \
"buffer_load_dwordx4 v51, s[8:11], s64 offen offset:1024 sc1 lds\n" \
"v_mfma_f32_16x16x32_bf16 v[120:123], a[128:131], a[48:51], v[120:123]\n" \
"v_cvt_pk_bf16_f32 v88, v88, v89\n" \
"s_waitcnt lgkmcnt(0)\n" \
"v_mfma_f32_16x16x32_bf16 v[128:131], a[160:163], v[68:71], v[128:131]\n" \
"v_exp_f32_e32 v91, v91\n" \
"v_mfma_f32_16x16x32_bf16 v[160:163], a[160:163], v[72:75], v[160:163]\n" \
"v_exp_f32_e32 v92, v92\n" \
"v_mfma_f32_16x16x32_bf16 v[132:135], a[164:167], v[68:71], v[132:135]\n" \
"buffer_load_dwordx4 v51, s[8:11], s64 offen offset:2048 sc1 lds\n" \
"v_mfma_f32_16x16x32_bf16 v[164:167], a[164:167], v[72:75], v[164:167]\n" \
"v_exp_f32_e32 v93, v93\n" \
"v_mfma_f32_16x16x32_bf16 v[136:139], a[168:171], v[68:71], v[136:139]\n" \
"v_exp_f32_e32 v94, v94\n" \
"v_mfma_f32_16x16x32_bf16 v[168:171], a[168:171], v[72:75], v[168:171]\n" \
"v_exp_f32_e32 v95, v95\n" \
"v_mfma_f32_16x16x32_bf16 v[140:143], a[172:175], v[68:71], v[140:143]\n" \
"buffer_load_dwordx4 v51, s[8:11], s64 offen offset:3072 sc1 lds\n" \
"v_mfma_f32_16x16x32_bf16 v[172:175], a[172:175], v[72:75], v[172:175]\n" \
"v_cvt_pk_bf16_f32 v89, v90, v91\n" \
"v_cvt_pk_bf16_f32 v90, v92, v93\n" \
"v_mfma_f32_16x16x32_bf16 v[144:147], a[176:179], v[68:71], v[144:147]\n" \
"v_cvt_pk_bf16_f32 v91, v94, v95\n" \
"v_readlane_b32 s80, v11, 11\n" \
"v_mfma_f32_16x16x32_bf16 v[176:179], a[176:179], v[72:75], v[176:179]\n" \
"v_readlane_b32 s81, v11, 12\n" \
"s_mov_b32 s82, 0x47fca0\n" \
"v_mfma_f32_16x16x32_bf16 v[148:151], a[180:183], v[68:71], v[148:151]\n" \
"s_mov_b32 s83, 0x20000\n" \
"v_readlane_b32 s92, v11, 26\n" \
"v_mfma_f32_16x16x32_bf16 v[180:183], a[180:183], v[72:75], v[180:183]\n" \
"s_add_u32 m0, s93, 0x0\n" \
"ds_read_b128 a[128:131], v48 offset:0\n" \
"v_mfma_f32_16x16x32_bf16 v[152:155], a[184:187], v[68:71], v[152:155]\n" \
"ds_read_b128 a[144:147], v48 offset:4096\n" \
"ds_read_b128 a[132:135], v48 offset:1024\n" \
"v_mfma_f32_16x16x32_bf16 v[184:187], a[184:187], v[72:75], v[184:187]\n" \
"buffer_load_dwordx4 v0, s[80:83], s92 offen offset:0 lds\n" \
"v_mfma_f32_16x16x32_bf16 v[156:159], a[188:191], v[68:71], v[156:159]\n" \
"ds_read_b128 a[148:151], v48 offset:5120\n" \
"ds_read_b128 a[136:139], v48 offset:2048\n" \
"v_mfma_f32_16x16x32_bf16 v[188:191], a[188:191], v[72:75], v[188:191]\n" \
"ds_read_b128 a[152:155], v48 offset:6144\n" \
"ds_read_b128 a[140:143], v48 offset:3072\n" \
"v_smfmac_f32_16x16x64_bf16 a[192:195], a[200:203], v[68:75], v21\n" \
"ds_read_b128 a[156:159], v48 offset:7168\n" \
"v_add_u32_e32 v52, 0x14000, v63\n" \
"v_mfma_f32_16x16x32_bf16 v[220:223], a[188:191], v[84:87], v[220:223]\n" \
"buffer_load_dwordx4 v1, s[80:83], s92 offen offset:1024 lds\n" \
"v_mfma_f32_16x16x32_bf16 v[252:255], a[188:191], v[88:91], v[252:255]\n" \
"v_exp_f32_e32 v102, v102\n" \
"v_mfma_f32_16x16x32_bf16 v[216:219], a[184:187], v[84:87], v[216:219]\n" \
"buffer_load_dwordx4 v2, s[80:83], s92 offen offset:2048 lds\n" \
"v_mfma_f32_16x16x32_bf16 v[248:251], a[184:187], v[88:91], v[248:251]\n" \
"v_exp_f32_e32 v103, v103\n" \
"v_mfma_f32_16x16x32_bf16 v[212:215], a[180:183], v[84:87], v[212:215]\n" \
"v_add_u32_e32 v53, 0x20000, v63\n" \
"v_cvt_pk_bf16_f32 v103, v102, v103\n" \
"v_mfma_f32_16x16x32_bf16 v[244:247], a[180:183], v[88:91], v[244:247]\n" \
"v_exp_f32_e32 v100, v100\n" \
"v_mfma_f32_16x16x32_bf16 v[208:211], a[176:179], v[84:87], v[208:211]\n" \
"buffer_load_dwordx4 v3, s[80:83], s92 offen offset:3072 lds\n" \
"v_mfma_f32_16x16x32_bf16 v[240:243], a[176:179], v[88:91], v[240:243]\n" \
"v_exp_f32_e32 v101, v101\n" \
"v_mfma_f32_16x16x32_bf16 v[204:207], a[172:175], v[84:87], v[204:207]\n" \
"v_exp_f32_e32 v98, v98\n" \
"v_mfma_f32_16x16x32_bf16 v[236:239], a[172:175], v[88:91], v[236:239]\n" \
"v_exp_f32_e32 v99, v99\n" \
"v_mfma_f32_16x16x32_bf16 v[200:203], a[168:171], v[84:87], v[200:203]\n" \
"v_exp_f32_e32 v96, v96\n" \
"v_mfma_f32_16x16x32_bf16 v[232:235], a[168:171], v[88:91], v[232:235]\n" \
"v_exp_f32_e32 v97, v97\n" \
"v_mfma_f32_16x16x32_bf16 v[196:199], a[164:167], v[84:87], v[196:199]\n" \
"v_exp_f32_e32 v104, v104\n" \
"v_mfma_f32_16x16x32_bf16 v[228:231], a[164:167], v[88:91], v[228:231]\n" \
"v_exp_f32_e32 v105, v105\n" \
"v_mfma_f32_16x16x32_bf16 v[192:195], a[160:163], v[84:87], v[192:195]\n" \
"v_cvt_pk_bf16_f32 v102, v100, v101\n" \
"v_cvt_pk_bf16_f32 v101, v98, v99\n" \
"v_mfma_f32_16x16x32_bf16 v[224:227], a[160:163], v[88:91], v[224:227]\n" \
"v_cvt_pk_bf16_f32 v100, v96, v97\n" \
"v_cvt_pk_bf16_f32 v104, v104, v105\n" \
"v_smfmac_f32_16x16x64_bf16 a[196:199], a[200:203], v[84:91], v21\n" \
"v_exp_f32_e32 v106, v106\n" \
"s_waitcnt vmcnt(15) lgkmcnt(0)\n" \
"s_barrier\n" \
"v_mfma_f32_16x16x32_bf16 v[64:67], a[128:131], a[0:3], v[32:35]\n" \
"s_xor_b32 m0, s65, 0x18000\n" \
"ds_read_b128 a[160:163], v49 offset:8192\n" \
"v_mfma_f32_16x16x32_bf16 v[64:67], a[132:135], a[4:7], v[64:67]\n" \
"buffer_load_dwordx4 v52, s[8:11], s64 offen offset:0 sc1 lds\n" \
"v_mfma_f32_16x16x32_bf16 v[64:67], a[136:139], a[8:11], v[64:67]\n" \
"ds_read_b128 a[164:167], v49 offset:9216\n" \
"ds_read_b128 a[168:171], v49 offset:10240\n" \
"v_mfma_f32_16x16x32_bf16 v[64:67], a[140:143], a[12:15], v[64:67]\n" \
"ds_read_b128 a[172:175], v49 offset:11264\n" \
"ds_read_b128 a[176:179], v49 offset:12288\n" \
"v_mfma_f32_16x16x32_bf16 v[68:71], a[156:159], a[12:15], v[32:35]\n" \
"buffer_load_dwordx4 v52, s[8:11], s64 offen offset:1024 sc1 lds\n" \
"v_mfma_f32_16x16x32_bf16 v[68:71], a[152:155], a[8:11], v[68:71]\n" \
"ds_read_b128 a[180:183], v49 offset:13312\n" \
"ds_read_b128 a[184:187], v49 offset:14336\n" \
"v_mfma_f32_16x16x32_bf16 v[68:71], a[148:151], a[4:7], v[68:71]\n" \
"v_exp_f32_e32 v107, v107\n" \
"v_mfma_f32_16x16x32_bf16 v[68:71], a[144:147], a[0:3], v[68:71]\n" \
"v_exp_f32_e32 v108, v108\n" \
"v_mfma_f32_16x16x32_bf16 v[76:79], a[144:147], a[16:19], v[36:39]\n" \
"v_exp_f32_e32 v109, v109\n" \
"v_mfma_f32_16x16x32_bf16 v[76:79], a[148:151], a[20:23], v[76:79]\n" \
"buffer_load_dwordx4 v52, s[8:11], s64 offen offset:2048 sc1 lds\n" \
"v_mfma_f32_16x16x32_bf16 v[76:79], a[152:155], a[24:27], v[76:79]\n" \
"v_exp_f32_e32 v110, v110\n" \
"v_mfma_f32_16x16x32_bf16 v[76:79], a[156:159], a[28:31], v[76:79]\n" \
"v_exp_f32_e32 v111, v111\n" \
"v_mfma_f32_16x16x32_bf16 v[72:75], a[140:143], a[28:31], v[36:39]\n" \
"v_cvt_pk_bf16_f32 v105, v106, v107\n" \
"v_cvt_pk_bf16_f32 v106, v108, v109\n" \
"v_mfma_f32_16x16x32_bf16 v[72:75], a[136:139], a[24:27], v[72:75]\n" \
"buffer_load_dwordx4 v52, s[8:11], s64 offen offset:3072 sc1 lds\n" \
"v_mfma_f32_16x16x32_bf16 v[72:75], a[132:135], a[20:23], v[72:75]\n" \
"s_mov_b32 m0, s65\n" \
"v_cvt_pk_bf16_f32 v107, v110, v111\n" \
"v_mfma_f32_16x16x32_bf16 v[72:75], a[128:131], a[16:19], v[72:75]\n" \
"ds_read_b128 a[188:191], v49 offset:15360\n" \
"v_add_u32_e32 v49, 0x10000, v48\n" \
"v_mfma_f32_16x16x32_bf16 v[80:83], a[128:131], a[32:35], v[40:43]\n" \
"v_exp_f32_e32 v118, v118\n" \
"v_mfma_f32_16x16x32_bf16 v[80:83], a[132:135], a[36:39], v[80:83]\n" \
"v_exp_f32_e32 v119, v119\n" \
"v_mfma_f32_16x16x32_bf16 v[80:83], a[136:139], a[40:43], v[80:83]\n" \
"buffer_load_dwordx4 v53, s[8:11], s64 offen offset:0 sc1 lds\n" \
"v_mfma_f32_16x16x32_bf16 v[80:83], a[140:143], a[44:47], v[80:83]\n" \
"v_exp_f32_e32 v116, v116\n" \
"v_mfma_f32_16x16x32_bf16 v[84:87], a[156:159], a[44:47], v[40:43]\n" \
"v_exp_f32_e32 v117, v117\n" \
"v_mfma_f32_16x16x32_bf16 v[84:87], a[152:155], a[40:43], v[84:87]\n" \
"v_cvt_pk_bf16_f32 v119, v118, v119\n" \
"v_cvt_pk_bf16_f32 v118, v116, v117\n" \
"v_mfma_f32_16x16x32_bf16 v[84:87], a[148:151], a[36:39], v[84:87]\n" \
"v_exp_f32_e32 v114, v114\n" \
"v_mfma_f32_16x16x32_bf16 v[84:87], a[144:147], a[32:35], v[84:87]\n" \
"buffer_load_dwordx4 v53, s[8:11], s64 offen offset:1024 sc1 lds\n" \
"v_mfma_f32_16x16x32_bf16 v[92:95], a[144:147], a[48:51], v[44:47]\n" \
"v_exp_f32_e32 v115, v115\n" \
"v_mfma_f32_16x16x32_bf16 v[92:95], a[148:151], a[52:55], v[92:95]\n" \
"v_exp_f32_e32 v112, v112\n" \
"v_mfma_f32_16x16x32_bf16 v[92:95], a[152:155], a[56:59], v[92:95]\n" \
"v_exp_f32_e32 v113, v113\n" \
"v_mfma_f32_16x16x32_bf16 v[92:95], a[156:159], a[60:63], v[92:95]\n" \
"buffer_load_dwordx4 v53, s[8:11], s64 offen offset:2048 sc1 lds\n" \
"v_mfma_f32_16x16x32_bf16 v[88:91], a[140:143], a[60:63], v[44:47]\n" \
"v_cvt_pk_bf16_f32 v117, v114, v115\n" \
"v_cvt_pk_bf16_f32 v116, v112, v113\n" \
"v_mfma_f32_16x16x32_bf16 v[88:91], a[136:139], a[56:59], v[88:91]\n" \
"v_exp_f32_e32 v120, v120\n" \
"v_mfma_f32_16x16x32_bf16 v[88:91], a[132:135], a[52:55], v[88:91]\n" \
"v_exp_f32_e32 v121, v121\n" \
"v_mfma_f32_16x16x32_bf16 v[88:91], a[128:131], a[48:51], v[88:91]\n" \
"v_cvt_pk_bf16_f32 v120, v120, v121\n" \
"s_waitcnt lgkmcnt(0)\n" \
"v_mfma_f32_16x16x32_bf16 v[128:131], a[160:163], v[100:103], v[128:131]\n" \
"buffer_load_dwordx4 v53, s[8:11], s64 offen offset:3072 sc1 lds\n" \
"v_mfma_f32_16x16x32_bf16 v[160:163], a[160:163], v[104:107], v[160:163]\n" \
"s_add_u32 m0, s93, 0x1000\n" \
"ds_read_b128 a[128:131], v48 offset:8192\n" \
"v_mfma_f32_16x16x32_bf16 v[132:135], a[164:167], v[100:103], v[132:135]\n" \
"v_exp_f32_e32 v122, v122\n" \
"v_mfma_f32_16x16x32_bf16 v[164:167], a[164:167], v[104:107], v[164:167]\n" \
"v_exp_f32_e32 v123, v123\n" \
"v_mfma_f32_16x16x32_bf16 v[136:139], a[168:171], v[100:103], v[136:139]\n" \
"buffer_load_dwordx4 v4, s[80:83], s92 offen offset:0 lds\n" \
"v_mfma_f32_16x16x32_bf16 v[168:171], a[168:171], v[104:107], v[168:171]\n" \
"v_exp_f32_e32 v124, v124\n" \
"v_mfma_f32_16x16x32_bf16 v[140:143], a[172:175], v[100:103], v[140:143]\n" \
"v_exp_f32_e32 v125, v125\n" \
"v_mfma_f32_16x16x32_bf16 v[172:175], a[172:175], v[104:107], v[172:175]\n" \
"v_exp_f32_e32 v126, v126\n" \
"v_mfma_f32_16x16x32_bf16 v[144:147], a[176:179], v[100:103], v[144:147]\n" \
"v_exp_f32_e32 v127, v127\n" \
"v_mfma_f32_16x16x32_bf16 v[176:179], a[176:179], v[104:107], v[176:179]\n" \
"v_cvt_pk_bf16_f32 v121, v122, v123\n" \
"v_cvt_pk_bf16_f32 v122, v124, v125\n" \
"v_mfma_f32_16x16x32_bf16 v[148:151], a[180:183], v[100:103], v[148:151]\n" \
"buffer_load_dwordx4 v5, s[80:83], s92 offen offset:1024 lds\n" \
"v_mfma_f32_16x16x32_bf16 v[180:183], a[180:183], v[104:107], v[180:183]\n" \
"v_cvt_pk_bf16_f32 v123, v126, v127\n" \
"ds_read_b128 a[144:147], v48 offset:12288\n" \
"v_mfma_f32_16x16x32_bf16 v[152:155], a[184:187], v[100:103], v[152:155]\n" \
"ds_read_b128 a[132:135], v48 offset:9216\n" \
"ds_read_b128 a[148:151], v48 offset:13312\n" \
"v_mfma_f32_16x16x32_bf16 v[184:187], a[184:187], v[104:107], v[184:187]\n" \
"ds_read_b128 a[136:139], v48 offset:10240\n" \
"ds_read_b128 a[152:155], v48 offset:14336\n" \
"v_mfma_f32_16x16x32_bf16 v[156:159], a[188:191], v[100:103], v[156:159]\n" \
"ds_read_b128 a[140:143], v48 offset:11264\n" \
"ds_read_b128 a[156:159], v48 offset:15360\n" \
"v_mfma_f32_16x16x32_bf16 v[188:191], a[188:191], v[104:107], v[188:191]\n" \
"buffer_load_dwordx4 v6, s[80:83], s92 offen offset:2048 lds\n" \
"v_smfmac_f32_16x16x64_bf16 a[192:195], a[200:203], v[100:107], v21\n" \
"v_exp_f32_e32 v70, v70\n" \
"v_mfma_f32_16x16x32_bf16 v[220:223], a[188:191], v[116:119], v[220:223]\n" \
"v_exp_f32_e32 v71, v71\n" \
"v_mfma_f32_16x16x32_bf16 v[252:255], a[188:191], v[120:123], v[252:255]\n" \
"buffer_load_dwordx4 v7, s[80:83], s92 offen offset:3072 lds\n" \
"v_mfma_f32_16x16x32_bf16 v[216:219], a[184:187], v[116:119], v[216:219]\n" \
"s_add_u32 m0, s93, 0x2000\n" \
"v_cvt_pk_bf16_f32 v71, v70, v71\n" \
"v_mfma_f32_16x16x32_bf16 v[248:251], a[184:187], v[120:123], v[248:251]\n" \
"v_exp_f32_e32 v72, v72\n" \
"v_mfma_f32_16x16x32_bf16 v[212:215], a[180:183], v[116:119], v[212:215]\n" \
"v_exp_f32_e32 v73, v73\n" \
"v_mfma_f32_16x16x32_bf16 v[244:247], a[180:183], v[120:123], v[244:247]\n" \
"v_exp_f32_e32 v68, v68\n" \
"v_mfma_f32_16x16x32_bf16 v[208:211], a[176:179], v[116:119], v[208:211]\n" \
"buffer_load_dwordx4 v8, s[80:83], s92 offen offset:0 lds\n" \
"v_mfma_f32_16x16x32_bf16 v[240:243], a[176:179], v[120:123], v[240:243]\n" \
"v_exp_f32_e32 v69, v69\n" \
"v_mfma_f32_16x16x32_bf16 v[204:207], a[172:175], v[116:119], v[204:207]\n" \
"v_exp_f32_e32 v74, v74\n" \
"v_mfma_f32_16x16x32_bf16 v[236:239], a[172:175], v[120:123], v[236:239]\n" \
"v_exp_f32_e32 v75, v75\n" \
"v_mfma_f32_16x16x32_bf16 v[200:203], a[168:171], v[116:119], v[200:203]\n" \
"v_exp_f32_e32 v66, v66\n" \
"v_mfma_f32_16x16x32_bf16 v[232:235], a[168:171], v[120:123], v[232:235]\n" \
"v_exp_f32_e32 v67, v67\n" \
"v_mfma_f32_16x16x32_bf16 v[196:199], a[164:167], v[116:119], v[196:199]\n" \
"v_exp_f32_e32 v64, v64\n" \
"v_mfma_f32_16x16x32_bf16 v[228:231], a[164:167], v[120:123], v[228:231]\n" \
"v_exp_f32_e32 v65, v65\n" \
"v_mfma_f32_16x16x32_bf16 v[192:195], a[160:163], v[116:119], v[192:195]\n" \
"v_cvt_pk_bf16_f32 v72, v72, v73\n" \
"v_cvt_pk_bf16_f32 v70, v68, v69\n" \
"v_mfma_f32_16x16x32_bf16 v[224:227], a[160:163], v[120:123], v[224:227]\n" \
"v_exp_f32_e32 v76, v76\n" \
"v_smfmac_f32_16x16x64_bf16 a[196:199], a[200:203], v[116:123], v21\n" \
"v_exp_f32_e32 v77, v77\n" \
"s_waitcnt vmcnt(13) lgkmcnt(0)\n" \
"s_barrier\n" \
"v_exp_f32_e32 v78, v78\n" \
"v_exp_f32_e32 v79, v79\n" \
"v_cvt_pk_bf16_f32 v73, v74, v75\n" \
"v_cvt_pk_bf16_f32 v69, v66, v67\n" \
"v_cvt_pk_bf16_f32 v68, v64, v65\n" \
"v_cvt_pk_bf16_f32 v74, v76, v77\n" \
"v_cvt_pk_bf16_f32 v75, v78, v79\n" \
"v_add_u32_e32 v48, s16, v20\n" \
"s_xor_b32 s65, s65, 0x8000\n" \
"s_add_u32 s64, s64, 0x10000\n" \
"s_getpc_b64 s[86:87]\n" \
"L_hpt_%=:\n" \
"s_add_u32 s86, s86, L_ap8-L_hpt_%=\n" \
"s_addc_u32 s87, s87, 0\n" \
"s_add_u32 s86, s86, s15\n" \
"s_addc_u32 s87, s87, 0\n" \
"s_mov_b64 exec, 0\n" \
"s_swappc_b64 s[88:89], s[86:87]\n" \
"s_mov_b64 exec, -1\n" \
"s_sub_u32 s66, s19, 1\n" \
"s_cbranch_scc1 L_la_%=\n" \
".fill (L_ab + 6464 - .) / 4, 4, 0xBF800000\n" \
"L_2_%=:\n" \
"v_mfma_f32_16x16x32_bf16 v[96:99], a[128:131], a[64:67], v[32:35]\n" \
"s_xor_b32 m0, s65, 0x12000\n" \
"ds_read_b128 a[160:163], v49 offset:0\n" \
"v_mfma_f32_16x16x32_bf16 v[96:99], a[132:135], a[68:71], v[96:99]\n" \
"buffer_load_dwordx4 v50, s[8:11], s64 offen offset:0 sc1 lds\n" \
"v_mfma_f32_16x16x32_bf16 v[96:99], a[136:139], a[72:75], v[96:99]\n" \
"ds_read_b128 a[164:167], v49 offset:1024\n" \
"ds_read_b128 a[168:171], v49 offset:2048\n" \
"v_mfma_f32_16x16x32_bf16 v[96:99], a[140:143], a[76:79], v[96:99]\n" \
"ds_read_b128 a[172:175], v49 offset:3072\n" \
"ds_read_b128 a[176:179], v49 offset:4096\n" \
"v_mfma_f32_16x16x32_bf16 v[100:103], a[156:159], a[76:79], v[32:35]\n" \
"ds_read_b128 a[180:183], v49 offset:5120\n" \
"ds_read_b128 a[184:187], v49 offset:6144\n" \
"v_mfma_f32_16x16x32_bf16 v[100:103], a[152:155], a[72:75], v[100:103]\n" \
"v_exp_f32_e64 v86, v86\n" \
"v_mfma_f32_16x16x32_bf16 v[100:103], a[148:151], a[68:71], v[100:103]\n" \
"v_exp_f32_e64 v87, v87\n" \
"v_mfma_f32_16x16x32_bf16 v[100:103], a[144:147], a[64:67], v[100:103]\n" \
"buffer_load_dwordx4 v50, s[8:11], s64 offen offset:1024 sc1 lds\n" \
"v_mfma_f32_16x16x32_bf16 v[108:111], a[144:147], a[80:83], v[36:39]\n" \
"ds_read_b128 a[188:191], v49 offset:7168\n" \
"v_cvt_pk_bf16_f32 v87, v86, v87\n" \
"v_mfma_f32_16x16x32_bf16 v[108:111], a[148:151], a[84:87], v[108:111]\n" \
"v_exp_f32_e64 v84, v84\n" \
"v_mfma_f32_16x16x32_bf16 v[108:111], a[152:155], a[88:91], v[108:111]\n" \
"v_exp_f32_e64 v85, v85\n" \
"v_mfma_f32_16x16x32_bf16 v[108:111], a[156:159], a[92:95], v[108:111]\n" \
"v_exp_f32_e64 v82, v82\n" \
"v_mfma_f32_16x16x32_bf16 v[104:107], a[140:143], a[92:95], v[36:39]\n" \
"v_exp_f32_e64 v83, v83\n" \
"v_mfma_f32_16x16x32_bf16 v[104:107], a[136:139], a[88:91], v[104:107]\n" \
"v_exp_f32_e64 v80, v80\n" \
"v_mfma_f32_16x16x32_bf16 v[104:107], a[132:135], a[84:87], v[104:107]\n" \
"v_exp_f32_e64 v81, v81\n" \
"v_mfma_f32_16x16x32_bf16 v[104:107], a[128:131], a[80:83], v[104:107]\n" \
"buffer_load_dwordx4 v50, s[8:11], s64 offen offset:2048 sc1 lds\n" \
"v_mfma_f32_16x16x32_bf16 v[112:115], a[128:131], a[96:99], v[40:43]\n" \
"v_exp_f32_e64 v88, v88\n" \
"v_mfma_f32_16x16x32_bf16 v[112:115], a[132:135], a[100:103], v[112:115]\n" \
"v_exp_f32_e64 v89, v89\n" \
"v_mfma_f32_16x16x32_bf16 v[112:115], a[136:139], a[104:107], v[112:115]\n" \
"v_cvt_pk_bf16_f32 v86, v84, v85\n" \
"v_cvt_pk_bf16_f32 v85, v82, v83\n" \
"v_mfma_f32_16x16x32_bf16 v[112:115], a[140:143], a[108:111], v[112:115]\n" \
"v_cvt_pk_bf16_f32 v84, v80, v81\n" \
"v_cvt_pk_bf16_f32 v88, v88, v89\n" \
"v_mfma_f32_16x16x32_bf16 v[116:119], a[156:159], a[108:111], v[40:43]\n" \
"v_exp_f32_e64 v90, v90\n" \
"v_mfma_f32_16x16x32_bf16 v[116:119], a[152:155], a[104:107], v[116:119]\n" \
"v_exp_f32_e64 v91, v91\n" \
"v_mfma_f32_16x16x32_bf16 v[116:119], a[148:151], a[100:103], v[116:119]\n" \
"buffer_load_dwordx4 v50, s[8:11], s64 offen offset:3072 sc1 lds\n" \
"v_mfma_f32_16x16x32_bf16 v[116:119], a[144:147], a[96:99], v[116:119]\n" \
"s_xor_b32 m0, s65, 0xa000\n" \
"v_cvt_pk_bf16_f32 v89, v90, v91\n" \
"v_mfma_f32_16x16x32_bf16 v[124:127], a[144:147], a[112:115], v[44:47]\n" \
"v_exp_f32_e64 v92, v92\n" \
"v_mfma_f32_16x16x32_bf16 v[124:127], a[148:151], a[116:119], v[124:127]\n" \
"v_exp_f32_e64 v93, v93\n" \
"v_mfma_f32_16x16x32_bf16 v[124:127], a[152:155], a[120:123], v[124:127]\n" \
"v_exp_f32_e64 v94, v94\n" \
"v_mfma_f32_16x16x32_bf16 v[124:127], a[156:159], a[124:127], v[124:127]\n" \
"v_exp_f32_e64 v95, v95\n" \
"v_mfma_f32_16x16x32_bf16 v[120:123], a[140:143], a[124:127], v[44:47]\n" \
"v_cvt_pk_bf16_f32 v90, v92, v93\n" \
"v_cvt_pk_bf16_f32 v91, v94, v95\n" \
"v_mfma_f32_16x16x32_bf16 v[120:123], a[136:139], a[120:123], v[120:123]\n" \
"ds_read_b128 a[144:147], v48 offset:4096\n" \
"ds_read_b128 a[148:151], v48 offset:5120\n" \
"v_mfma_f32_16x16x32_bf16 v[120:123], a[132:135], a[116:119], v[120:123]\n" \
"buffer_load_dwordx4 v51, s[8:11], s64 offen offset:0 sc1 lds\n" \
"v_mfma_f32_16x16x32_bf16 v[120:123], a[128:131], a[112:115], v[120:123]\n" \
"ds_read_b128 a[128:131], v48 offset:0\n" \
"s_waitcnt lgkmcnt(3)\n" \
"v_mfma_f32_16x16x32_bf16 v[128:131], a[160:163], v[68:71], v[128:131]\n" \
"ds_read_b128 a[132:135], v48 offset:1024\n" \
"ds_read_b128 a[136:139], v48 offset:2048\n" \
"v_mfma_f32_16x16x32_bf16 v[160:163], a[160:163], v[72:75], v[160:163]\n" \
"ds_read_b128 a[152:155], v48 offset:6144\n" \
"ds_read_b128 a[140:143], v48 offset:3072\n" \
"v_mfma_f32_16x16x32_bf16 v[132:135], a[164:167], v[68:71], v[132:135]\n" \
"v_exp_f32_e32 v102, v102\n" \
"v_mfma_f32_16x16x32_bf16 v[164:167], a[164:167], v[72:75], v[164:167]\n" \
"v_exp_f32_e64 v103, v103\n" \
"v_mfma_f32_16x16x32_bf16 v[136:139], a[168:171], v[68:71], v[136:139]\n" \
"ds_read_b128 a[156:159], v48 offset:7168\n" \
"v_cvt_pk_bf16_f32 v103, v102, v103\n" \
"v_mfma_f32_16x16x32_bf16 v[168:171], a[168:171], v[72:75], v[168:171]\n" \
"buffer_load_dwordx4 v51, s[8:11], s64 offen offset:1024 sc1 lds\n" \
"v_mfma_f32_16x16x32_bf16 v[140:143], a[172:175], v[68:71], v[140:143]\n" \
"v_exp_f32_e64 v100, v100\n" \
"v_mfma_f32_16x16x32_bf16 v[172:175], a[172:175], v[72:75], v[172:175]\n" \
"v_exp_f32_e64 v101, v101\n" \
"v_mfma_f32_16x16x32_bf16 v[144:147], a[176:179], v[68:71], v[144:147]\n" \
"v_exp_f32_e64 v98, v98\n" \
"v_mfma_f32_16x16x32_bf16 v[176:179], a[176:179], v[72:75], v[176:179]\n" \
"v_exp_f32_e64 v99, v99\n" \
"v_mfma_f32_16x16x32_bf16 v[148:151], a[180:183], v[68:71], v[148:151]\n" \
"v_exp_f32_e64 v96, v96\n" \
"v_mfma_f32_16x16x32_bf16 v[180:183], a[180:183], v[72:75], v[180:183]\n" \
"v_exp_f32_e64 v97, v97\n" \
"v_mfma_f32_16x16x32_bf16 v[152:155], a[184:187], v[68:71], v[152:155]\n" \
"v_exp_f32_e64 v104, v104\n" \
"v_mfma_f32_16x16x32_bf16 v[184:187], a[184:187], v[72:75], v[184:187]\n" \
"buffer_load_dwordx4 v51, s[8:11], s64 offen offset:2048 sc1 lds\n" \
"v_mfma_f32_16x16x32_bf16 v[156:159], a[188:191], v[68:71], v[156:159]\n" \
"v_exp_f32_e64 v105, v105\n" \
"v_mfma_f32_16x16x32_bf16 v[188:191], a[188:191], v[72:75], v[188:191]\n" \
"v_cvt_pk_bf16_f32 v102, v100, v101\n" \
"v_cvt_pk_bf16_f32 v101, v98, v99\n" \
"v_smfmac_f32_16x16x64_bf16 a[192:195], a[200:203], v[68:75], v21\n" \
"v_cvt_pk_bf16_f32 v100, v96, v97\n" \
"v_cvt_pk_bf16_f32 v104, v104, v105\n" \
"v_mfma_f32_16x16x32_bf16 v[220:223], a[188:191], v[84:87], v[220:223]\n" \
"v_exp_f32_e64 v106, v106\n" \
"v_mfma_f32_16x16x32_bf16 v[252:255], a[188:191], v[88:91], v[252:255]\n" \
"v_exp_f32_e64 v107, v107\n" \
"v_mfma_f32_16x16x32_bf16 v[216:219], a[184:187], v[84:87], v[216:219]\n" \
"v_exp_f32_e64 v108, v108\n" \
"v_mfma_f32_16x16x32_bf16 v[248:251], a[184:187], v[88:91], v[248:251]\n" \
"buffer_load_dwordx4 v51, s[8:11], s64 offen offset:3072 sc1 lds\n" \
"v_mfma_f32_16x16x32_bf16 v[212:215], a[180:183], v[84:87], v[212:215]\n" \
"v_exp_f32_e64 v109, v109\n" \
"v_mfma_f32_16x16x32_bf16 v[244:247], a[180:183], v[88:91], v[244:247]\n" \
"v_exp_f32_e64 v110, v110\n" \
"v_mfma_f32_16x16x32_bf16 v[208:211], a[176:179], v[84:87], v[208:211]\n" \
"v_exp_f32_e64 v111, v111\n" \
"v_mfma_f32_16x16x32_bf16 v[240:243], a[176:179], v[88:91], v[240:243]\n" \
"v_exp_f32_e64 v118, v118\n" \
"v_mfma_f32_16x16x32_bf16 v[204:207], a[172:175], v[84:87], v[204:207]\n" \
"v_exp_f32_e64 v119, v119\n" \
"v_mfma_f32_16x16x32_bf16 v[236:239], a[172:175], v[88:91], v[236:239]\n" \
"v_cvt_pk_bf16_f32 v105, v106, v107\n" \
"v_cvt_pk_bf16_f32 v106, v108, v109\n" \
"v_mfma_f32_16x16x32_bf16 v[200:203], a[168:171], v[84:87], v[200:203]\n" \
"v_cvt_pk_bf16_f32 v107, v110, v111\n" \
"v_cvt_pk_bf16_f32 v119, v118, v119\n" \
"v_mfma_f32_16x16x32_bf16 v[232:235], a[168:171], v[88:91], v[232:235]\n" \
"v_exp_f32_e64 v116, v116\n" \
"v_mfma_f32_16x16x32_bf16 v[196:199], a[164:167], v[84:87], v[196:199]\n" \
"v_exp_f32_e64 v117, v117\n" \
"v_mfma_f32_16x16x32_bf16 v[228:231], a[164:167], v[88:91], v[228:231]\n" \
"v_exp_f32_e64 v114, v114\n" \
"v_mfma_f32_16x16x32_bf16 v[192:195], a[160:163], v[84:87], v[192:195]\n" \
"v_exp_f32_e64 v115, v115\n" \
"v_mfma_f32_16x16x32_bf16 v[224:227], a[160:163], v[88:91], v[224:227]\n" \
"v_exp_f32_e64 v112, v112\n" \
"v_smfmac_f32_16x16x64_bf16 a[196:199], a[200:203], v[84:91], v21\n" \
"v_exp_f32_e32 v113, v113\n" \
"s_waitcnt vmcnt(8) lgkmcnt(0)\n" \
"s_barrier\n" \
"s_nop 0\n" \
"v_mfma_f32_16x16x32_bf16 v[64:67], a[128:131], a[64:67], v[32:35]\n" \
"s_xor_b32 m0, s65, 0x18000\n" \
"ds_read_b128 a[160:163], v49 offset:8192\n" \
"v_mfma_f32_16x16x32_bf16 v[64:67], a[132:135], a[68:71], v[64:67]\n" \
"buffer_load_dwordx4 v52, s[8:11], s64 offen offset:0 sc1 lds\n" \
"v_mfma_f32_16x16x32_bf16 v[64:67], a[136:139], a[72:75], v[64:67]\n" \
"ds_read_b128 a[164:167], v49 offset:9216\n" \
"ds_read_b128 a[168:171], v49 offset:10240\n" \
"v_mfma_f32_16x16x32_bf16 v[64:67], a[140:143], a[76:79], v[64:67]\n" \
"ds_read_b128 a[172:175], v49 offset:11264\n" \
"ds_read_b128 a[176:179], v49 offset:12288\n" \
"v_mfma_f32_16x16x32_bf16 v[68:71], a[156:159], a[76:79], v[32:35]\n" \
"ds_read_b128 a[180:183], v49 offset:13312\n" \
"ds_read_b128 a[184:187], v49 offset:14336\n" \
"v_mfma_f32_16x16x32_bf16 v[68:71], a[152:155], a[72:75], v[68:71]\n" \
"ds_read_b128 a[188:191], v49 offset:15360\n" \
"v_cvt_pk_bf16_f32 v118, v116, v117\n" \
"v_mfma_f32_16x16x32_bf16 v[68:71], a[148:151], a[68:71], v[68:71]\n" \
"v_cvt_pk_bf16_f32 v117, v114, v115\n" \
"v_cvt_pk_bf16_f32 v116, v112, v113\n" \
"v_mfma_f32_16x16x32_bf16 v[68:71], a[144:147], a[64:67], v[68:71]\n" \
"buffer_load_dwordx4 v52, s[8:11], s64 offen offset:1024 sc1 lds\n" \
"v_mfma_f32_16x16x32_bf16 v[76:79], a[144:147], a[80:83], v[36:39]\n" \
"v_exp_f32_e64 v120, v120\n" \
"v_mfma_f32_16x16x32_bf16 v[76:79], a[148:151], a[84:87], v[76:79]\n" \
"v_exp_f32_e64 v121, v121\n" \
"v_mfma_f32_16x16x32_bf16 v[76:79], a[152:155], a[88:91], v[76:79]\n" \
"v_exp_f32_e64 v122, v122\n" \
"v_mfma_f32_16x16x32_bf16 v[76:79], a[156:159], a[92:95], v[76:79]\n" \
"v_exp_f32_e64 v123, v123\n" \
"v_mfma_f32_16x16x32_bf16 v[72:75], a[140:143], a[92:95], v[36:39]\n" \
"v_cvt_pk_bf16_f32 v120, v120, v121\n" \
"v_cvt_pk_bf16_f32 v121, v122, v123\n" \
"v_mfma_f32_16x16x32_bf16 v[72:75], a[136:139], a[88:91], v[72:75]\n" \
"v_exp_f32_e64 v124, v124\n" \
"v_mfma_f32_16x16x32_bf16 v[72:75], a[132:135], a[84:87], v[72:75]\n" \
"v_exp_f32_e64 v125, v125\n" \
"v_mfma_f32_16x16x32_bf16 v[72:75], a[128:131], a[80:83], v[72:75]\n" \
"buffer_load_dwordx4 v52, s[8:11], s64 offen offset:2048 sc1 lds\n" \
"v_mfma_f32_16x16x32_bf16 v[80:83], a[128:131], a[96:99], v[40:43]\n" \
"v_exp_f32_e64 v126, v126\n" \
"v_mfma_f32_16x16x32_bf16 v[80:83], a[132:135], a[100:103], v[80:83]\n" \
"v_exp_f32_e64 v127, v127\n" \
"v_mfma_f32_16x16x32_bf16 v[80:83], a[136:139], a[104:107], v[80:83]\n" \
"v_cvt_pk_bf16_f32 v122, v124, v125\n" \
"v_cvt_pk_bf16_f32 v123, v126, v127\n" \
"v_mfma_f32_16x16x32_bf16 v[80:83], a[140:143], a[108:111], v[80:83]\n" \
"v_exp_f32_e64 v70, v70\n" \
"v_mfma_f32_16x16x32_bf16 v[84:87], a[156:159], a[108:111], v[40:43]\n" \
"v_exp_f32_e64 v71, v71\n" \
"v_mfma_f32_16x16x32_bf16 v[84:87], a[152:155], a[104:107], v[84:87]\n" \
"v_exp_f32_e64 v72, v72\n" \
"v_mfma_f32_16x16x32_bf16 v[84:87], a[148:151], a[100:103], v[84:87]\n" \
"buffer_load_dwordx4 v52, s[8:11], s64 offen offset:3072 sc1 lds\n" \
"v_mfma_f32_16x16x32_bf16 v[84:87], a[144:147], a[96:99], v[84:87]\n" \
"s_mov_b32 m0, s65\n" \
"v_cvt_pk_bf16_f32 v71, v70, v71\n" \
"v_mfma_f32_16x16x32_bf16 v[92:95], a[144:147], a[112:115], v[44:47]\n" \
"ds_read_b128 a[144:147], v48 offset:12288\n" \
"v_add_u32_e32 v49, 0x10000, v48\n" \
"v_mfma_f32_16x16x32_bf16 v[92:95], a[148:151], a[116:119], v[92:95]\n" \
"ds_read_b128 a[148:151], v48 offset:13312\n" \
"s_xor_b32 s65, s65, 0x8000\n" \
"v_mfma_f32_16x16x32_bf16 v[92:95], a[152:155], a[120:123], v[92:95]\n" \
"v_exp_f32_e32 v73, v73\n" \
"v_mfma_f32_16x16x32_bf16 v[92:95], a[156:159], a[124:127], v[92:95]\n" \
"ds_read_b128 a[152:155], v48 offset:14336\n" \
".fill (L_ab + 8192 - .) / 4, 4, 0xBF800000\n" \
"L_ap2:\n" \
"s_cbranch_execz L_ap0\n" \
"ds_read_b128 a[156:159], v48 offset:15360\n" \
"v_mfma_f32_16x16x32_bf16 v[88:91], a[140:143], a[124:127], v[44:47]\n" \
"ds_read_b128 a[140:143], v48 offset:11264\n" \
"v_cvt_pk_bf16_f32 v72, v72, v73\n" \
"v_mfma_f32_16x16x32_bf16 v[88:91], a[136:139], a[120:123], v[88:91]\n" \
"v_exp_f32_e32 v68, v68\n" \
"v_mfma_f32_16x16x32_bf16 v[88:91], a[132:135], a[116:119], v[88:91]\n" \
"buffer_load_dwordx4 v53, s[8:11], s64 offen offset:0 sc1 lds\n" \
"v_mfma_f32_16x16x32_bf16 v[88:91], a[128:131], a[112:115], v[88:91]\n" \
"ds_read_b128 a[128:131], v48 offset:8192\n" \
"s_waitcnt lgkmcnt(6)\n" \
"v_mfma_f32_16x16x32_bf16 v[128:131], a[160:163], v[100:103], v[128:131]\n" \
"ds_read_b128 a[132:135], v48 offset:9216\n" \
"ds_read_b128 a[136:139], v48 offset:10240\n" \
"v_mfma_f32_16x16x32_bf16 v[160:163], a[160:163], v[104:107], v[160:163]\n" \
"v_exp_f32_e32 v69, v69\n" \
"v_mfma_f32_16x16x32_bf16 v[132:135], a[164:167], v[100:103], v[132:135]\n" \
"v_exp_f32_e64 v74, v74\n" \
"v_mfma_f32_16x16x32_bf16 v[164:167], a[164:167], v[104:107], v[164:167]\n" \
"v_exp_f32_e64 v75, v75\n" \
"v_mfma_f32_16x16x32_bf16 v[136:139], a[168:171], v[100:103], v[136:139]\n" \
"v_exp_f32_e64 v66, v66\n" \
"v_mfma_f32_16x16x32_bf16 v[168:171], a[168:171], v[104:107], v[168:171]\n" \
"buffer_load_dwordx4 v53, s[8:11], s64 offen offset:1024 sc1 lds\n" \
"v_mfma_f32_16x16x32_bf16 v[140:143], a[172:175], v[100:103], v[140:143]\n" \
"v_exp_f32_e64 v67, v67\n" \
"v_mfma_f32_16x16x32_bf16 v[172:175], a[172:175], v[104:107], v[172:175]\n" \
"v_exp_f32_e64 v64, v64\n" \
"v_mfma_f32_16x16x32_bf16 v[144:147], a[176:179], v[100:103], v[144:147]\n" \
"v_exp_f32_e64 v65, v65\n" \
"v_mfma_f32_16x16x32_bf16 v[176:179], a[176:179], v[104:107], v[176:179]\n" \
"v_exp_f32_e64 v76, v76\n" \
"v_mfma_f32_16x16x32_bf16 v[148:151], a[180:183], v[100:103], v[148:151]\n" \
"v_exp_f32_e64 v77, v77\n" \
"v_mfma_f32_16x16x32_bf16 v[180:183], a[180:183], v[104:107], v[180:183]\n" \
"v_exp_f32_e64 v78, v78\n" \
"v_mfma_f32_16x16x32_bf16 v[152:155], a[184:187], v[100:103], v[152:155]\n" \
"v_exp_f32_e64 v79, v79\n" \
"v_mfma_f32_16x16x32_bf16 v[184:187], a[184:187], v[104:107], v[184:187]\n" \
"buffer_load_dwordx4 v53, s[8:11], s64 offen offset:2048 sc1 lds\n" \
"v_mfma_f32_16x16x32_bf16 v[156:159], a[188:191], v[100:103], v[156:159]\n" \
"v_cvt_pk_bf16_f32 v70, v68, v69\n" \
"v_cvt_pk_bf16_f32 v73, v74, v75\n" \
"v_mfma_f32_16x16x32_bf16 v[188:191], a[188:191], v[104:107], v[188:191]\n" \
"v_cvt_pk_bf16_f32 v69, v66, v67\n" \
"v_cvt_pk_bf16_f32 v68, v64, v65\n" \
"v_smfmac_f32_16x16x64_bf16 a[192:195], a[200:203], v[100:107], v21\n" \
"v_cvt_pk_bf16_f32 v74, v76, v77\n" \
"v_cvt_pk_bf16_f32 v75, v78, v79\n" \
"v_mfma_f32_16x16x32_bf16 v[220:223], a[188:191], v[116:119], v[220:223]\n" \
"v_xor_b32_e32 v48, 0x8000, v48\n" \
"v_mfma_f32_16x16x32_bf16 v[252:255], a[188:191], v[120:123], v[252:255]\n" \
"v_mfma_f32_16x16x32_bf16 v[216:219], a[184:187], v[116:119], v[216:219]\n" \
"v_mfma_f32_16x16x32_bf16 v[248:251], a[184:187], v[120:123], v[248:251]\n" \
"buffer_load_dwordx4 v53, s[8:11], s64 offen offset:3072 sc1 lds\n" \
"v_mfma_f32_16x16x32_bf16 v[212:215], a[180:183], v[116:119], v[212:215]\n" \
"s_add_u32 s64, s64, 0x10000\n" \
"s_sub_u32 s66, s66, 1\n" \
"s_nop 0\n" \
"v_mfma_f32_16x16x32_bf16 v[244:247], a[180:183], v[120:123], v[244:247]\n" \
"v_mfma_f32_16x16x32_bf16 v[208:211], a[176:179], v[116:119], v[208:211]\n" \
"v_mfma_f32_16x16x32_bf16 v[240:243], a[176:179], v[120:123], v[240:243]\n" \
"v_mfma_f32_16x16x32_bf16 v[204:207], a[172:175], v[116:119], v[204:207]\n" \
"v_mfma_f32_16x16x32_bf16 v[236:239], a[172:175], v[120:123], v[236:239]\n" \
"v_mfma_f32_16x16x32_bf16 v[200:203], a[168:171], v[116:119], v[200:203]\n" \
"v_mfma_f32_16x16x32_bf16 v[232:235], a[168:171], v[120:123], v[232:235]\n" \
"v_mfma_f32_16x16x32_bf16 v[196:199], a[164:167], v[116:119], v[196:199]\n" \
"v_mfma_f32_16x16x32_bf16 v[228:231], a[164:167], v[120:123], v[228:231]\n" \
"v_mfma_f32_16x16x32_bf16 v[192:195], a[160:163], v[116:119], v[192:195]\n" \
"v_mfma_f32_16x16x32_bf16 v[224:227], a[160:163], v[120:123], v[224:227]\n" \
"v_smfmac_f32_16x16x64_bf16 a[196:199], a[200:203], v[116:123], v21\n" \
"s_waitcnt vmcnt(8) lgkmcnt(0)\n" \
"s_barrier\n" \
"s_cbranch_scc0 L_2_%=\n" \
"s_nop 0\n" \
"L_la_%=:\n" \
"v_mfma_f32_16x16x32_bf16 v[96:99], a[128:131], a[64:67], v[32:35]\n" \
"v_readlane_b32 s96, v11, 1\n" \
"v_bfe_u32 v27, v20, 2, 7\n" \
"v_mfma_f32_16x16x32_bf16 v[96:99], a[132:135], a[68:71], v[96:99]\n" \
"s_xor_b32 m0, s65, 0x12000\n" \
"ds_read_b128 a[160:163], v49 offset:0\n" \
"v_mfma_f32_16x16x32_bf16 v[96:99], a[136:139], a[72:75], v[96:99]\n" \
"buffer_load_dword v26, v27, s[56:59], s96 offen sc1\n" \
"v_mfma_f32_16x16x32_bf16 v[96:99], a[140:143], a[76:79], v[96:99]\n" \
"buffer_load_dwordx4 v50, s[8:11], s64 offen offset:0 sc1 lds\n" \
"v_mfma_f32_16x16x32_bf16 v[100:103], a[156:159], a[76:79], v[32:35]\n" \
"ds_read_b128 a[164:167], v49 offset:1024\n" \
"ds_read_b128 a[168:171], v49 offset:2048\n" \
"v_mfma_f32_16x16x32_bf16 v[100:103], a[152:155], a[72:75], v[100:103]\n" \
"ds_read_b128 a[172:175], v49 offset:3072\n" \
"ds_read_b128 a[176:179], v49 offset:4096\n" \
"v_mfma_f32_16x16x32_bf16 v[100:103], a[148:151], a[68:71], v[100:103]\n" \
"ds_read_b128 a[180:183], v49 offset:5120\n" \
"ds_read_b128 a[184:187], v49 offset:6144\n" \
"v_mfma_f32_16x16x32_bf16 v[100:103], a[144:147], a[64:67], v[100:103]\n" \
"ds_read_b128 a[188:191], v49 offset:7168\n" \
"v_readlane_b32 s97, v11, 36\n" \
"v_mfma_f32_16x16x32_bf16 v[108:111], a[144:147], a[80:83], v[36:39]\n" \
"v_exp_f32_e64 v86, v86\n" \
"v_mfma_f32_16x16x32_bf16 v[108:111], a[148:151], a[84:87], v[108:111]\n" \
"v_exp_f32_e64 v87, v87\n" \
"v_mfma_f32_16x16x32_bf16 v[108:111], a[152:155], a[88:91], v[108:111]\n" \
"buffer_load_dwordx4 v50, s[8:11], s64 offen offset:1024 sc1 lds\n" \
"v_mfma_f32_16x16x32_bf16 v[108:111], a[156:159], a[92:95], v[108:111]\n" \
"v_exp_f32_e64 v84, v84\n" \
"v_mfma_f32_16x16x32_bf16 v[104:107], a[140:143], a[92:95], v[36:39]\n" \
"v_exp_f32_e64 v85, v85\n" \
"v_mfma_f32_16x16x32_bf16 v[104:107], a[136:139], a[88:91], v[104:107]\n" \
"v_cvt_pk_bf16_f32 v87, v86, v87\n" \
"v_cvt_pk_bf16_f32 v86, v84, v85\n" \
"v_mfma_f32_16x16x32_bf16 v[104:107], a[132:135], a[84:87], v[104:107]\n" \
"v_exp_f32_e64 v82, v82\n" \
"v_mfma_f32_16x16x32_bf16 v[104:107], a[128:131], a[80:83], v[104:107]\n" \
"v_exp_f32_e64 v83, v83\n" \
"v_mfma_f32_16x16x32_bf16 v[112:115], a[128:131], a[96:99], v[40:43]\n" \
"v_exp_f32_e64 v80, v80\n" \
"v_mfma_f32_16x16x32_bf16 v[112:115], a[132:135], a[100:103], v[112:115]\n" \
"v_exp_f32_e64 v81, v81\n" \
"v_mfma_f32_16x16x32_bf16 v[112:115], a[136:139], a[104:107], v[112:115]\n" \
"buffer_load_dwordx4 v50, s[8:11], s64 offen offset:2048 sc1 lds\n" \
"v_mfma_f32_16x16x32_bf16 v[112:115], a[140:143], a[108:111], v[112:115]\n" \
"v_cvt_pk_bf16_f32 v85, v82, v83\n" \
"v_cvt_pk_bf16_f32 v84, v80, v81\n" \
"v_mfma_f32_16x16x32_bf16 v[116:119], a[156:159], a[108:111], v[40:43]\n" \
"v_exp_f32_e64 v88, v88\n" \
"v_mfma_f32_16x16x32_bf16 v[116:119], a[152:155], a[104:107], v[116:119]\n" \
"v_exp_f32_e64 v89, v89\n" \
"v_mfma_f32_16x16x32_bf16 v[116:119], a[148:151], a[100:103], v[116:119]\n" \
"v_exp_f32_e64 v90, v90\n" \
"v_mfma_f32_16x16x32_bf16 v[116:119], a[144:147], a[96:99], v[116:119]\n" \
"v_exp_f32_e64 v91, v91\n" \
"v_mfma_f32_16x16x32_bf16 v[124:127], a[144:147], a[112:115], v[44:47]\n" \
"v_cvt_pk_bf16_f32 v88, v88, v89\n" \
"v_cvt_pk_bf16_f32 v89, v90, v91\n" \
"v_mfma_f32_16x16x32_bf16 v[124:127], a[148:151], a[116:119], v[124:127]\n" \
"buffer_load_dwordx4 v50, s[8:11], s64 offen offset:3072 sc1 lds\n" \
"v_mfma_f32_16x16x32_bf16 v[124:127], a[152:155], a[120:123], v[124:127]\n" \
"v_exp_f32_e64 v92, v92\n" \
"v_mfma_f32_16x16x32_bf16 v[124:127], a[156:159], a[124:127], v[124:127]\n" \
"v_exp_f32_e64 v93, v93\n" \
"v_mfma_f32_16x16x32_bf16 v[120:123], a[140:143], a[124:127], v[44:47]\n" \
"v_exp_f32_e64 v94, v94\n" \
"v_mfma_f32_16x16x32_bf16 v[120:123], a[136:139], a[120:123], v[120:123]\n" \
"v_exp_f32_e64 v95, v95\n" \
"v_mfma_f32_16x16x32_bf16 v[120:123], a[132:135], a[116:119], v[120:123]\n" \
"v_cvt_pk_bf16_f32 v90, v92, v93\n" \
"v_cvt_pk_bf16_f32 v91, v94, v95\n" \
"v_mfma_f32_16x16x32_bf16 v[120:123], a[128:131], a[112:115], v[120:123]\n" \
"ds_read_b128 a[128:131], v48 offset:0\n" \
"s_waitcnt lgkmcnt(1)\n" \
"s_nop 0\n" \
"v_mfma_f32_16x16x32_bf16 v[128:131], a[160:163], v[68:71], v[128:131]\n" \
"ds_read_b128 a[144:147], v48 offset:4096\n" \
"ds_read_b128 a[132:135], v48 offset:1024\n" \
"v_mfma_f32_16x16x32_bf16 v[160:163], a[160:163], v[72:75], v[160:163]\n" \
"ds_read_b128 a[148:151], v48 offset:5120\n" \
"ds_read_b128 a[136:139], v48 offset:2048\n" \
"v_mfma_f32_16x16x32_bf16 v[132:135], a[164:167], v[68:71], v[132:135]\n" \
"ds_read_b128 a[152:155], v48 offset:6144\n" \
"ds_read_b128 a[140:143], v48 offset:3072\n" \
"v_mfma_f32_16x16x32_bf16 v[164:167], a[164:167], v[72:75], v[164:167]\n" \
"ds_read_b128 a[156:159], v48 offset:7168\n" \
"v_add_u32_e32 v17, 0x49000, v20\n" \
"v_mfma_f32_16x16x32_bf16 v[136:139], a[168:171], v[68:71], v[136:139]\n" \
"v_readlane_b32 s98, v11, 37\n" \
"v_readlane_b32 s93, v11, 20\n" \
"v_mfma_f32_16x16x32_bf16 v[168:171], a[168:171], v[72:75], v[168:171]\n" \
"v_readlane_b32 s84, v11, 15\n" \
"v_readlane_b32 s85, v11, 16\n" \
"v_mfma_f32_16x16x32_bf16 v[140:143], a[172:175], v[68:71], v[140:143]\n" \
"v_readlane_b32 s86, v11, 17\n" \
"s_mov_b32 s87, 0x20000\n" \
"v_mfma_f32_16x16x32_bf16 v[172:175], a[172:175], v[72:75], v[172:175]\n" \
"v_readlane_b32 s92, v11, 18\n" \
"v_readlane_b32 s91, v11, 21\n" \
"v_mfma_f32_16x16x32_bf16 v[144:147], a[176:179], v[68:71], v[144:147]\n" \
"v_exp_f32_e64 v102, v102\n" \
"v_mfma_f32_16x16x32_bf16 v[176:179], a[176:179], v[72:75], v[176:179]\n" \
"v_exp_f32_e64 v103, v103\n" \
"v_mfma_f32_16x16x32_bf16 v[148:151], a[180:183], v[68:71], v[148:151]\n" \
"v_exp_f32_e64 v100, v100\n" \
"v_mfma_f32_16x16x32_bf16 v[180:183], a[180:183], v[72:75], v[180:183]\n" \
"v_exp_f32_e64 v101, v101\n" \
"v_mfma_f32_16x16x32_bf16 v[152:155], a[184:187], v[68:71], v[152:155]\n" \
"v_cvt_pk_bf16_f32 v103, v102, v103\n" \
"v_cvt_pk_bf16_f32 v102, v100, v101\n" \
"v_mfma_f32_16x16x32_bf16 v[184:187], a[184:187], v[72:75], v[184:187]\n" \
"v_exp_f32_e64 v98, v98\n" \
"v_mfma_f32_16x16x32_bf16 v[156:159], a[188:191], v[68:71], v[156:159]\n" \
"v_exp_f32_e64 v99, v99\n" \
"v_mfma_f32_16x16x32_bf16 v[188:191], a[188:191], v[72:75], v[188:191]\n" \
"s_waitcnt vmcnt(4)\n" \
"v_cmp_ne_u32_e32 vcc, s62, v26\n" \
"v_smfmac_f32_16x16x64_bf16 a[192:195], a[200:203], v[68:75], v21\n" \
"v_exp_f32_e64 v96, v96\n" \
"v_mfma_f32_16x16x32_bf16 v[220:223], a[188:191], v[84:87], v[220:223]\n" \
"v_exp_f32_e64 v97, v97\n" \
"v_mfma_f32_16x16x32_bf16 v[252:255], a[188:191], v[88:91], v[252:255]\n" \
"s_cbranch_vccnz L_sol_%=\n" \
"L_rol_%=:\n" \
"v_cvt_pk_bf16_f32 v101, v98, v99\n" \
"buffer_load_dwordx4 a[220:223], v20, s[56:59], s97 offen offset:0 sc1\n" \
"v_mfma_f32_16x16x32_bf16 v[216:219], a[184:187], v[84:87], v[216:219]\n" \
"v_cvt_pk_bf16_f32 v100, v96, v97\n" \
"s_xor_b32 s65, s65, 0x8000\n" \
"v_mfma_f32_16x16x32_bf16 v[248:251], a[184:187], v[88:91], v[248:251]\n" \
"v_exp_f32_e32 v104, v104\n" \
"v_mfma_f32_16x16x32_bf16 v[212:215], a[180:183], v[84:87], v[212:215]\n" \
"v_exp_f32_e64 v105, v105\n" \
"v_mfma_f32_16x16x32_bf16 v[244:247], a[180:183], v[88:91], v[244:247]\n" \
"v_exp_f32_e64 v106, v106\n" \
"v_mfma_f32_16x16x32_bf16 v[208:211], a[176:179], v[84:87], v[208:211]\n" \
"v_exp_f32_e64 v107, v107\n" \
"v_mfma_f32_16x16x32_bf16 v[240:243], a[176:179], v[88:91], v[240:243]\n" \
"buffer_load_dwordx4 a[228:231], v17, s[56:59], s97 offen offset:0 sc1\n" \
"v_mfma_f32_16x16x32_bf16 v[204:207], a[172:175], v[84:87], v[204:207]\n" \
"v_cvt_pk_bf16_f32 v104, v104, v105\n" \
"v_cvt_pk_bf16_f32 v105, v106, v107\n" \
"v_mfma_f32_16x16x32_bf16 v[236:239], a[172:175], v[88:91], v[236:239]\n" \
"v_exp_f32_e64 v108, v108\n" \
"v_mfma_f32_16x16x32_bf16 v[200:203], a[168:171], v[84:87], v[200:203]\n" \
"v_exp_f32_e64 v109, v109\n" \
"v_mfma_f32_16x16x32_bf16 v[232:235], a[168:171], v[88:91], v[232:235]\n" \
"v_exp_f32_e64 v110, v110\n" \
"v_mfma_f32_16x16x32_bf16 v[196:199], a[164:167], v[84:87], v[196:199]\n" \
"v_exp_f32_e64 v111, v111\n" \
"v_mfma_f32_16x16x32_bf16 v[228:231], a[164:167], v[88:91], v[228:231]\n" \
"v_cvt_pk_bf16_f32 v106, v108, v109\n" \
"v_cvt_pk_bf16_f32 v107, v110, v111\n" \
"v_mfma_f32_16x16x32_bf16 v[192:195], a[160:163], v[84:87], v[192:195]\n" \
"v_exp_f32_e64 v118, v118\n" \
"v_mfma_f32_16x16x32_bf16 v[224:227], a[160:163], v[88:91], v[224:227]\n" \
"v_exp_f32_e64 v119, v119\n" \
"v_smfmac_f32_16x16x64_bf16 a[196:199], a[200:203], v[84:91], v21\n" \
"buffer_load_dwordx4 a[236:239], v20, s[56:59], s98 offen offset:0 sc1\n" \
"s_waitcnt vmcnt(8) lgkmcnt(0)\n" \
"s_barrier\n" \
"v_mfma_f32_16x16x32_bf16 v[64:67], a[128:131], a[64:67], v[32:35]\n" \
"s_add_u32 m0, s93, 0x0\n" \
"s_nop 0\n" \
"ds_read_b128 a[160:163], v49 offset:8192\n" \
"v_mfma_f32_16x16x32_bf16 v[64:67], a[132:135], a[68:71], v[64:67]\n" \
"buffer_load_dwordx4 v0, s[84:87], s92 offen offset:0 lds\n" \
"v_mfma_f32_16x16x32_bf16 v[64:67], a[136:139], a[72:75], v[64:67]\n" \
"ds_read_b128 a[164:167], v49 offset:9216\n" \
"ds_read_b128 a[168:171], v49 offset:10240\n" \
"v_mfma_f32_16x16x32_bf16 v[64:67], a[140:143], a[76:79], v[64:67]\n" \
"ds_read_b128 a[172:175], v49 offset:11264\n" \
"ds_read_b128 a[176:179], v49 offset:12288\n" \
"v_mfma_f32_16x16x32_bf16 v[68:71], a[156:159], a[76:79], v[32:35]\n" \
"ds_read_b128 a[180:183], v49 offset:13312\n" \
"ds_read_b128 a[184:187], v49 offset:14336\n" \
"v_mfma_f32_16x16x32_bf16 v[68:71], a[152:155], a[72:75], v[68:71]\n" \
"ds_read_b128 a[188:191], v49 offset:15360\n" \
"v_cvt_pk_bf16_f32 v119, v118, v119\n" \
"v_mfma_f32_16x16x32_bf16 v[68:71], a[148:151], a[68:71], v[68:71]\n" \
"buffer_load_dwordx4 v1, s[84:87], s92 offen offset:1024 lds\n" \
"v_mfma_f32_16x16x32_bf16 v[68:71], a[144:147], a[64:67], v[68:71]\n" \
"buffer_load_dwordx4 a[244:247], v17, s[56:59], s98 offen offset:0 sc1\n" \
"v_mfma_f32_16x16x32_bf16 v[76:79], a[144:147], a[80:83], v[36:39]\n" \
"v_exp_f32_e64 v116, v116\n" \
"v_mfma_f32_16x16x32_bf16 v[76:79], a[148:151], a[84:87], v[76:79]\n" \
"v_exp_f32_e64 v117, v117\n" \
"v_mfma_f32_16x16x32_bf16 v[76:79], a[152:155], a[88:91], v[76:79]\n" \
"v_exp_f32_e64 v114, v114\n" \
"v_mfma_f32_16x16x32_bf16 v[76:79], a[156:159], a[92:95], v[76:79]\n" \
"v_exp_f32_e64 v115, v115\n" \
"v_mfma_f32_16x16x32_bf16 v[72:75], a[140:143], a[92:95], v[36:39]\n" \
"v_exp_f32_e64 v112, v112\n" \
"v_mfma_f32_16x16x32_bf16 v[72:75], a[136:139], a[88:91], v[72:75]\n" \
"buffer_load_dwordx4 v2, s[84:87], s92 offen offset:2048 lds\n" \
"v_mfma_f32_16x16x32_bf16 v[72:75], a[132:135], a[84:87], v[72:75]\n" \
"v_exp_f32_e64 v113, v113\n" \
"v_mfma_f32_16x16x32_bf16 v[72:75], a[128:131], a[80:83], v[72:75]\n" \
"v_cvt_pk_bf16_f32 v118, v116, v117\n" \
"v_cvt_pk_bf16_f32 v117, v114, v115\n" \
"v_mfma_f32_16x16x32_bf16 v[80:83], a[128:131], a[96:99], v[40:43]\n" \
"buffer_load_dwordx4 a[224:227], v20, s[56:59], s97 offen offset:1024 sc1\n" \
"v_mfma_f32_16x16x32_bf16 v[80:83], a[132:135], a[100:103], v[80:83]\n" \
"v_cvt_pk_bf16_f32 v116, v112, v113\n" \
"v_add_u32_e32 v49, 0x10000, v48\n" \
"v_mfma_f32_16x16x32_bf16 v[80:83], a[136:139], a[104:107], v[80:83]\n" \
"v_exp_f32_e64 v120, v120\n" \
"v_mfma_f32_16x16x32_bf16 v[80:83], a[140:143], a[108:111], v[80:83]\n" \
"v_exp_f32_e64 v121, v121\n" \
"v_mfma_f32_16x16x32_bf16 v[84:87], a[156:159], a[108:111], v[40:43]\n" \
"buffer_load_dwordx4 v3, s[84:87], s92 offen offset:3072 lds\n" \
"v_mfma_f32_16x16x32_bf16 v[84:87], a[152:155], a[104:107], v[84:87]\n" \
"s_add_u32 m0, s91, 0x1000\n" \
"v_cvt_pk_bf16_f32 v120, v120, v121\n" \
"v_mfma_f32_16x16x32_bf16 v[84:87], a[148:151], a[100:103], v[84:87]\n" \
"v_exp_f32_e64 v122, v122\n" \
"v_mfma_f32_16x16x32_bf16 v[84:87], a[144:147], a[96:99], v[84:87]\n" \
"v_exp_f32_e64 v123, v123\n" \
"v_mfma_f32_16x16x32_bf16 v[92:95], a[144:147], a[112:115], v[44:47]\n" \
"v_exp_f32_e64 v124, v124\n" \
"v_mfma_f32_16x16x32_bf16 v[92:95], a[148:151], a[116:119], v[92:95]\n" \
"buffer_load_dwordx4 a[232:235], v17, s[56:59], s97 offen offset:1024 sc1\n" \
"v_mfma_f32_16x16x32_bf16 v[92:95], a[152:155], a[120:123], v[92:95]\n" \
"buffer_load_dwordx4 v4, s[84:87], s92 offen offset:0 lds\n" \
"v_mfma_f32_16x16x32_bf16 v[92:95], a[156:159], a[124:127], v[92:95]\n" \
"v_exp_f32_e64 v125, v125\n" \
"v_mfma_f32_16x16x32_bf16 v[88:91], a[140:143], a[124:127], v[44:47]\n" \
"v_exp_f32_e64 v126, v126\n" \
"v_mfma_f32_16x16x32_bf16 v[88:91], a[136:139], a[120:123], v[88:91]\n" \
"v_exp_f32_e64 v127, v127\n" \
"v_mfma_f32_16x16x32_bf16 v[88:91], a[132:135], a[116:119], v[88:91]\n" \
"v_cvt_pk_bf16_f32 v121, v122, v123\n" \
"v_cvt_pk_bf16_f32 v122, v124, v125\n" \
"v_mfma_f32_16x16x32_bf16 v[88:91], a[128:131], a[112:115], v[88:91]\n" \
"v_cvt_pk_bf16_f32 v123, v126, v127\n" \
"s_waitcnt lgkmcnt(0)\n" \
"s_nop 0\n" \
"v_mfma_f32_16x16x32_bf16 v[128:131], a[160:163], v[100:103], v[128:131]\n" \
"ds_read_b128 a[128:131], v48 offset:8192\n" \
"ds_read_b128 a[144:147], v48 offset:12288\n" \
"v_mfma_f32_16x16x32_bf16 v[160:163], a[160:163], v[104:107], v[160:163]\n" \
"buffer_load_dwordx4 v5, s[84:87], s92 offen offset:1024 lds\n" \
"v_mfma_f32_16x16x32_bf16 v[132:135], a[164:167], v[100:103], v[132:135]\n" \
"buffer_load_dwordx4 a[240:243], v20, s[56:59], s98 offen offset:1024 sc1\n" \
"v_mfma_f32_16x16x32_bf16 v[164:167], a[164:167], v[104:107], v[164:167]\n" \
"ds_read_b128 a[132:135], v48 offset:9216\n" \
"ds_read_b128 a[148:151], v48 offset:13312\n" \
"v_mfma_f32_16x16x32_bf16 v[136:139], a[168:171], v[100:103], v[136:139]\n" \
"ds_read_b128 a[136:139], v48 offset:10240\n" \
"ds_read_b128 a[152:155], v48 offset:14336\n" \
"v_mfma_f32_16x16x32_bf16 v[168:171], a[168:171], v[104:107], v[168:171]\n" \
"ds_read_b128 a[140:143], v48 offset:11264\n" \
"ds_read_b128 a[156:159], v48 offset:15360\n" \
"v_mfma_f32_16x16x32_bf16 v[140:143], a[172:175], v[100:103], v[140:143]\n" \
"v_exp_f32_e64 v70, v70\n" \
"v_mfma_f32_16x16x32_bf16 v[172:175], a[172:175], v[104:107], v[172:175]\n" \
"v_exp_f32_e64 v71, v71\n" \
"v_mfma_f32_16x16x32_bf16 v[144:147], a[176:179], v[100:103], v[144:147]\n" \
"buffer_load_dwordx4 v6, s[84:87], s92 offen offset:2048 lds\n" \
"v_mfma_f32_16x16x32_bf16 v[176:179], a[176:179], v[104:107], v[176:179]\n" \
"v_exp_f32_e64 v72, v72\n" \
"v_mfma_f32_16x16x32_bf16 v[148:151], a[180:183], v[100:103], v[148:151]\n" \
"v_exp_f32_e64 v73, v73\n" \
"v_mfma_f32_16x16x32_bf16 v[180:183], a[180:183], v[104:107], v[180:183]\n" \
"buffer_load_dwordx4 a[248:251], v17, s[56:59], s98 offen offset:1024 sc1\n" \
"v_mfma_f32_16x16x32_bf16 v[152:155], a[184:187], v[100:103], v[152:155]\n" \
"v_exp_f32_e64 v68, v68\n" \
"v_mfma_f32_16x16x32_bf16 v[184:187], a[184:187], v[104:107], v[184:187]\n" \
"v_exp_f32_e64 v69, v69\n" \
"v_mfma_f32_16x16x32_bf16 v[156:159], a[188:191], v[100:103], v[156:159]\n" \
"buffer_load_dwordx4 v7, s[84:87], s92 offen offset:3072 lds\n" \
"v_mfma_f32_16x16x32_bf16 v[188:191], a[188:191], v[104:107], v[188:191]\n" \
"s_add_u32 m0, s91, 0x2000\n" \
"v_cvt_pk_bf16_f32 v71, v70, v71\n" \
"v_smfmac_f32_16x16x64_bf16 a[192:195], a[200:203], v[100:107], v21\n" \
"v_exp_f32_e64 v74, v74\n" \
"v_mfma_f32_16x16x32_bf16 v[220:223], a[188:191], v[116:119], v[220:223]\n" \
"v_exp_f32_e64 v75, v75\n" \
"v_mfma_f32_16x16x32_bf16 v[252:255], a[188:191], v[120:123], v[252:255]\n" \
"v_exp_f32_e64 v66, v66\n" \
"v_mfma_f32_16x16x32_bf16 v[216:219], a[184:187], v[116:119], v[216:219]\n" \
"v_exp_f32_e64 v67, v67\n" \
"v_mfma_f32_16x16x32_bf16 v[248:251], a[184:187], v[120:123], v[248:251]\n" \
"v_exp_f32_e64 v64, v64\n" \
"v_mfma_f32_16x16x32_bf16 v[212:215], a[180:183], v[116:119], v[212:215]\n" \
"buffer_load_dwordx4 v8, s[84:87], s92 offen offset:0 lds\n" \
"v_mfma_f32_16x16x32_bf16 v[244:247], a[180:183], v[120:123], v[244:247]\n" \
"v_exp_f32_e64 v65, v65\n" \
"v_mfma_f32_16x16x32_bf16 v[208:211], a[176:179], v[116:119], v[208:211]\n" \
"v_cvt_pk_bf16_f32 v72, v72, v73\n" \
"v_cvt_pk_bf16_f32 v70, v68, v69\n" \
"v_mfma_f32_16x16x32_bf16 v[240:243], a[176:179], v[120:123], v[240:243]\n" \
"v_exp_f32_e64 v76, v76\n" \
"v_mfma_f32_16x16x32_bf16 v[204:207], a[172:175], v[116:119], v[204:207]\n" \
"v_exp_f32_e64 v77, v77\n" \
"v_mfma_f32_16x16x32_bf16 v[236:239], a[172:175], v[120:123], v[236:239]\n" \
"v_exp_f32_e64 v78, v78\n" \
"v_mfma_f32_16x16x32_bf16 v[200:203], a[168:171], v[116:119], v[200:203]\n" \
"v_exp_f32_e64 v79, v79\n" \
"v_mfma_f32_16x16x32_bf16 v[232:235], a[168:171], v[120:123], v[232:235]\n" \
"v_cvt_pk_bf16_f32 v73, v74, v75\n" \
"v_cvt_pk_bf16_f32 v69, v66, v67\n" \
"v_mfma_f32_16x16x32_bf16 v[196:199], a[164:167], v[116:119], v[196:199]\n" \
"v_cvt_pk_bf16_f32 v68, v64, v65\n" \
"v_cvt_pk_bf16_f32 v74, v76, v77\n" \
"v_mfma_f32_16x16x32_bf16 v[228:231], a[164:167], v[120:123], v[228:231]\n" \
"v_cvt_pk_bf16_f32 v75, v78, v79\n" \
"v_xor_b32_e32 v48, 0x8000, v48\n" \
"v_mfma_f32_16x16x32_bf16 v[192:195], a[160:163], v[116:119], v[192:195]\n" \
"s_add_u32 s64, s64, 0x10000\n" \
"v_mfma_f32_16x16x32_bf16 v[224:227], a[160:163], v[120:123], v[224:227]\n" \
"v_smfmac_f32_16x16x64_bf16 a[196:199], a[200:203], v[116:123], v21\n" \
"s_waitcnt vmcnt(14) lgkmcnt(0)\n" \
"s_barrier\n" \
"s_cmp_lg_u32 s16, 0\n" \
"s_cbranch_scc1 L_f1_%=\n" \
"ds_read_b128 a[160:163], v49 offset:0\n" \
"ds_read_b128 a[164:167], v49 offset:1024\n" \
"ds_read_b128 a[168:171], v49 offset:2048\n" \
"ds_read_b128 a[172:175], v49 offset:3072\n" \
"ds_read_b128 a[176:179], v49 offset:4096\n" \
"ds_read_b128 a[180:183], v49 offset:5120\n" \
"ds_read_b128 a[184:187], v49 offset:6144\n" \
"ds_read_b128 a[188:191], v49 offset:7168\n" \
"v_add_u32_e32 v54, 0x49000, v20\n" \
"buffer_load_dwordx4 a[96:99], v20, s[56:59], s60 offen offset:0 sc1\n" \
"v_add_u32_e32 v50, s15, v20\n" \
"v_add_u32_e32 v51, 0x10000, v50\n" \
"ds_read_b32 v60, v18 offset:0\n" \
"ds_read_b32 v61, v18 offset:128\n" \
"ds_read_b128 a[0:3], v49 offset:8192\n" \
"ds_read_b128 a[4:7], v49 offset:9216\n" \
"ds_read_b128 a[8:11], v49 offset:10240\n" \
"ds_read_b128 a[12:15], v49 offset:11264\n" \
"ds_read_b128 a[16:19], v49 offset:12288\n" \
"ds_read_b128 a[20:23], v49 offset:13312\n" \
"ds_read_b128 a[24:27], v49 offset:14336\n" \
"ds_read_b128 a[28:31], v49 offset:15360\n" \
"v_exp_f32_e32 v86, v86\n" \
"v_exp_f32_e32 v87, v87\n" \
"v_exp_f32_e32 v84, v84\n" \
"v_exp_f32_e32 v85, v85\n" \
"v_cvt_pk_bf16_f32 v87, v86, v87\n" \
"buffer_load_dwordx4 a[100:103], v20, s[56:59], s60 offen offset:1024 sc1\n" \
"v_exp_f32_e32 v82, v82\n" \
"v_exp_f32_e32 v83, v83\n" \
"v_exp_f32_e32 v80, v80\n" \
"v_exp_f32_e32 v81, v81\n" \
"s_waitcnt lgkmcnt(15)\n" \
"v_mfma_f32_16x16x32_bf16 v[128:131], a[160:163], v[68:71], v[128:131]\n" \
"v_cvt_pk_bf16_f32 v86, v84, v85\n" \
"v_cvt_pk_bf16_f32 v85, v82, v83\n" \
"v_mfma_f32_16x16x32_bf16 v[160:163], a[160:163], v[72:75], v[160:163]\n" \
"v_cvt_pk_bf16_f32 v84, v80, v81\n" \
"v_and_b32_e32 v84, v12, v84\n" \
"v_mfma_f32_16x16x32_bf16 v[132:135], a[164:167], v[68:71], v[132:135]\n" \
"v_and_b32_e32 v85, v13, v85\n" \
"v_and_b32_e32 v86, v14, v86\n" \
"v_mfma_f32_16x16x32_bf16 v[164:167], a[164:167], v[72:75], v[164:167]\n" \
"buffer_load_dwordx4 a[104:107], v20, s[56:59], s60 offen offset:2048 sc1\n" \
"v_mfma_f32_16x16x32_bf16 v[136:139], a[168:171], v[68:71], v[136:139]\n" \
"v_cvt_pk_bf16_f32 v128, v128, v129\n" \
"v_cvt_pk_bf16_f32 v160, v160, v161\n" \
"v_mfma_f32_16x16x32_bf16 v[168:171], a[168:171], v[72:75], v[168:171]\n" \
"v_cvt_pk_bf16_f32 v129, v130, v131\n" \
"s_waitcnt lgkmcnt(10)\n" \
"v_mfma_f32_16x16x32_bf16 v[140:143], a[172:175], v[68:71], v[140:143]\n" \
"v_cvt_pk_bf16_f32 v161, v162, v163\n" \
"v_cvt_pk_bf16_f32 v130, v132, v133\n" \
"v_mfma_f32_16x16x32_bf16 v[172:175], a[172:175], v[72:75], v[172:175]\n" \
"v_cvt_pk_bf16_f32 v131, v134, v135\n" \
"v_cvt_pk_bf16_f32 v162, v164, v165\n" \
"v_mfma_f32_16x16x32_bf16 v[144:147], a[176:179], v[68:71], v[144:147]\n" \
"v_cvt_pk_bf16_f32 v163, v166, v167\n" \
"v_cvt_pk_bf16_f32 v132, v136, v137\n" \
"v_mfma_f32_16x16x32_bf16 v[176:179], a[176:179], v[72:75], v[176:179]\n" \
"buffer_load_dwordx4 a[108:111], v20, s[56:59], s60 offen offset:3072 sc1\n" \
"v_mfma_f32_16x16x32_bf16 v[148:151], a[180:183], v[68:71], v[148:151]\n" \
"v_cvt_pk_bf16_f32 v133, v138, v139\n" \
"v_cvt_pk_bf16_f32 v134, v140, v141\n" \
"v_mfma_f32_16x16x32_bf16 v[180:183], a[180:183], v[72:75], v[180:183]\n" \
"v_cvt_pk_bf16_f32 v135, v142, v143\n" \
"v_cvt_pk_bf16_f32 v164, v168, v169\n" \
"v_mfma_f32_16x16x32_bf16 v[152:155], a[184:187], v[68:71], v[152:155]\n" \
"v_cvt_pk_bf16_f32 v165, v170, v171\n" \
"v_cvt_pk_bf16_f32 v166, v172, v173\n" \
"v_mfma_f32_16x16x32_bf16 v[184:187], a[184:187], v[72:75], v[184:187]\n" \
"v_cvt_pk_bf16_f32 v167, v174, v175\n" \
"v_cvt_pk_bf16_f32 v136, v144, v145\n" \
"v_mfma_f32_16x16x32_bf16 v[156:159], a[188:191], v[68:71], v[156:159]\n" \
"v_cvt_pk_bf16_f32 v137, v146, v147\n" \
"v_cvt_pk_bf16_f32 v138, v148, v149\n" \
"v_mfma_f32_16x16x32_bf16 v[188:191], a[188:191], v[72:75], v[188:191]\n" \
"v_cvt_pk_bf16_f32 v139, v150, v151\n" \
"v_cvt_pk_bf16_f32 v140, v152, v153\n" \
"v_smfmac_f32_16x16x64_bf16 a[192:195], a[200:203], v[68:75], v21\n" \
"v_cvt_pk_bf16_f32 v141, v154, v155\n" \
"v_cvt_pk_bf16_f32 v168, v176, v177\n" \
"v_mfma_f32_16x16x32_bf16 v[120:123], a[128:131], a[112:115], v[44:47]\n" \
"v_cvt_pk_bf16_f32 v142, v156, v157\n" \
"v_cvt_pk_bf16_f32 v143, v158, v159\n" \
"v_mfma_f32_16x16x32_bf16 v[120:123], a[132:135], a[116:119], v[120:123]\n" \
"v_cvt_pk_bf16_f32 v169, v178, v179\n" \
"v_cvt_pk_bf16_f32 v170, v180, v181\n" \
"v_cvt_pk_bf16_f32 v171, v182, v183\n" \
"v_cvt_pk_bf16_f32 v172, v184, v185\n" \
"v_cvt_pk_bf16_f32 v173, v186, v187\n" \
"v_cvt_pk_bf16_f32 v174, v188, v189\n" \
"v_cvt_pk_bf16_f32 v175, v190, v191\n" \
"ds_write_b128 v50, v[128:131] offset:0\n" \
"v_mfma_f32_16x16x32_bf16 v[120:123], a[136:139], a[120:123], v[120:123]\n" \
"v_and_b32_e32 v87, v15, v87\n" \
"v_accvgpr_read_b32 v40, a192\n" \
"v_mfma_f32_16x16x32_bf16 v[120:123], a[140:143], a[124:127], v[120:123]\n" \
"ds_write_b128 v50, v[132:135] offset:1024\n" \
"v_mfma_f32_16x16x32_bf16 v[124:127], a[156:159], a[124:127], v[44:47]\n" \
"buffer_load_dwordx4 a[124:127], v54, s[56:59], s60 offen offset:3072 sc1\n" \
"v_mfma_f32_16x16x32_bf16 v[124:127], a[152:155], a[120:123], v[124:127]\n" \
"ds_write_b128 v50, v[136:139] offset:2048\n" \
"v_mfma_f32_16x16x32_bf16 v[124:127], a[148:151], a[116:119], v[124:127]\n" \
"v_accvgpr_read_b32 v22, a193\n" \
"v_accvgpr_read_b32 v41, a194\n" \
"v_mfma_f32_16x16x32_bf16 v[124:127], a[144:147], a[112:115], v[124:127]\n" \
"ds_write_b128 v50, v[140:143] offset:3072\n" \
"v_mfma_f32_16x16x32_bf16 a[196:199], a[204:207], v[84:87], a[196:199]\n" \
"v_accvgpr_read_b32 v23, a195\n" \
"v_add_f32_e32 v40, v40, v22\n" \
"v_mfma_f32_16x16x32_bf16 v[192:195], a[160:163], v[84:87], v[192:195]\n" \
"ds_write_b128 v51, v[160:163] offset:32768\n" \
"v_mfma_f32_16x16x32_bf16 v[196:199], a[164:167], v[84:87], v[196:199]\n" \
"buffer_load_dwordx4 a[112:115], v54, s[56:59], s60 offen offset:0 sc1\n" \
"v_mfma_f32_16x16x32_bf16 v[200:203], a[168:171], v[84:87], v[200:203]\n" \
"ds_write_b128 v51, v[164:167] offset:33792\n" \
"v_add_f32_e32 v41, v41, v23\n" \
"ds_write_b64 v19, v[40:41]\n" \
"v_mfma_f32_16x16x32_bf16 v[204:207], a[172:175], v[84:87], v[204:207]\n" \
"v_lshl_add_u32 v52, s15, 2, v20\n" \
"v_add_u32_e32 v52, 0x1a000, v52\n" \
"ds_write_b128 v51, v[168:171] offset:34816\n" \
"v_mfma_f32_16x16x32_bf16 v[208:211], a[176:179], v[84:87], v[208:211]\n" \
"v_exp_f32_e32 v88, v88\n" \
"v_mfma_f32_16x16x32_bf16 v[212:215], a[180:183], v[84:87], v[212:215]\n" \
"ds_write_b128 v51, v[172:175] offset:35840\n" \
"v_mfma_f32_16x16x32_bf16 v[216:219], a[184:187], v[84:87], v[216:219]\n" \
"buffer_load_dwordx4 a[116:119], v54, s[56:59], s60 offen offset:1024 sc1\n" \
"v_mfma_f32_16x16x32_bf16 v[220:223], a[188:191], v[84:87], v[220:223]\n" \
"v_exp_f32_e32 v89, v89\n" \
"v_exp_f32_e32 v90, v90\n" \
"v_exp_f32_e32 v91, v91\n" \
"v_cvt_pk_bf16_f32 v88, v88, v89\n" \
"v_exp_f32_e32 v92, v92\n" \
"v_exp_f32_e32 v93, v93\n" \
"v_exp_f32_e32 v94, v94\n" \
"v_exp_f32_e32 v95, v95\n" \
"v_cvt_pk_bf16_f32 v89, v90, v91\n" \
"v_cvt_pk_bf16_f32 v90, v92, v93\n" \
"v_cvt_pk_bf16_f32 v91, v94, v95\n" \
"v_xor_b32_e32 v63, 0x400, v19\n" \
"buffer_load_dwordx4 a[120:123], v54, s[56:59], s60 offen offset:2048 sc1\n" \
"v_exp_f32_e32 v120, v120\n" \
"v_exp_f32_e32 v121, v121\n" \
"v_exp_f32_e32 v122, v122\n" \
"v_exp_f32_e32 v123, v123\n" \
".fill (L_ab + 12288 - .) / 4, 4, 0xBF800000\n" \
"L_ap3:\n" \
"s_cbranch_execz L_ap0\n" \
"v_cvt_pk_bf16_f32 v120, v120, v121\n" \
"v_exp_f32_e32 v124, v124\n" \
"v_exp_f32_e32 v125, v125\n" \
"s_waitcnt lgkmcnt(0)\n" \
"s_barrier\n" \
"ds_read_b128 v[144:147], v52 offset:0\n" \
"ds_read_b128 v[148:151], v52 offset:1024\n" \
"ds_read_b128 v[152:155], v52 offset:2048\n" \
"ds_read_b128 v[156:159], v52 offset:3072\n" \
"ds_read_b128 v[176:179], v52 offset:4096\n" \
"ds_read_b128 v[180:183], v52 offset:5120\n" \
"ds_read_b128 v[184:187], v52 offset:6144\n" \
"ds_read_b128 v[188:191], v52 offset:7168\n" \
"v_mfma_f32_16x16x32_bf16 a[196:199], a[212:215], v[88:91], a[196:199]\n" \
"ds_read_b64 v[42:43], v63\n" \
"v_cvt_pk_bf16_f32 v121, v122, v123\n" \
"v_mfma_f32_16x16x32_bf16 v[224:227], a[160:163], v[88:91], v[224:227]\n" \
"v_exp_f32_e32 v126, v126\n" \
"v_mfma_f32_16x16x32_bf16 v[228:231], a[164:167], v[88:91], v[228:231]\n" \
"v_exp_f32_e32 v127, v127\n" \
"v_mfma_f32_16x16x32_bf16 v[232:235], a[168:171], v[88:91], v[232:235]\n" \
"v_cvt_pk_bf16_f32 v122, v124, v125\n" \
"v_cvt_pk_bf16_f32 v123, v126, v127\n" \
"v_mfma_f32_16x16x32_bf16 v[236:239], a[172:175], v[88:91], v[236:239]\n" \
"v_and_b32_e32 v120, v12, v120\n" \
"v_and_b32_e32 v121, v13, v121\n" \
"v_mfma_f32_16x16x32_bf16 v[240:243], a[176:179], v[88:91], v[240:243]\n" \
"v_and_b32_e32 v122, v14, v122\n" \
"v_and_b32_e32 v123, v15, v123\n" \
"v_mfma_f32_16x16x32_bf16 v[244:247], a[180:183], v[88:91], v[244:247]\n" \
"v_readlane_b32 s93, v11, 22\n" \
"v_readlane_b32 s92, v11, 27\n" \
"v_mfma_f32_16x16x32_bf16 v[248:251], a[184:187], v[88:91], v[248:251]\n" \
"s_add_u32 m0, s93, 0x0\n" \
"v_mov_b32_e32 v130, v28\n" \
"v_mfma_f32_16x16x32_bf16 v[252:255], a[188:191], v[88:91], v[252:255]\n" \
"v_mov_b32_e32 v131, v29\n" \
"v_mov_b32_e32 v132, 0\n" \
"v_mov_b32_e32 v133, 0\n" \
"v_mov_b32_e32 v128, 0\n" \
"v_mov_b32_e32 v129, 0\n" \
"s_waitcnt vmcnt(8) lgkmcnt(1)\n" \
"s_barrier\n" \
"v_mfma_f32_16x16x32_bf16 a[196:199], a[212:215], v[120:123], a[196:199]\n" \
"buffer_load_dwordx4 v0, s[80:83], s92 offen offset:0 lds\n" \
"v_mfma_f32_16x16x32_bf16 v[224:227], a[0:3], v[120:123], v[224:227]\n" \
"v_mfma_f32_16x16x32_bf16 v[228:231], a[4:7], v[120:123], v[228:231]\n" \
"v_mfma_f32_16x16x32_bf16 v[232:235], a[8:11], v[120:123], v[232:235]\n" \
"v_mfma_f32_16x16x32_bf16 v[236:239], a[12:15], v[120:123], v[236:239]\n" \
"v_mfma_f32_16x16x32_bf16 v[240:243], a[16:19], v[120:123], v[240:243]\n" \
"v_mfma_f32_16x16x32_bf16 v[244:247], a[20:23], v[120:123], v[244:247]\n" \
"buffer_load_dwordx4 v1, s[80:83], s92 offen offset:1024 lds\n" \
"v_mfma_f32_16x16x32_bf16 v[248:251], a[24:27], v[120:123], v[248:251]\n" \
"v_accvgpr_read_b32 v56, a196\n" \
"v_accvgpr_read_b32 v24, a197\n" \
"v_mfma_f32_16x16x32_bf16 v[252:255], a[28:31], v[120:123], v[252:255]\n" \
"v_add_f32_e32 v56, v56, v24\n" \
"s_waitcnt lgkmcnt(0)\n" \
"v_add_f32_e32 v56, v56, v42\n" \
"v_mfma_f32_16x16x32_bf16 v[192:195], v[130:133], v[144:147], v[192:195]\n" \
"v_max_f32_e32 v62, v62, v56\n" \
"v_accvgpr_read_b32 v57, a198\n" \
"v_mfma_f32_16x16x32_bf16 v[196:199], v[128:131], v[144:147], v[196:199]\n" \
"v_rcp_f32_e32 v56, v56\n" \
"v_mfma_f32_16x16x32_bf16 v[200:203], v[130:133], v[148:151], v[200:203]\n" \
"v_mul_f32_e32 v56, v56, v60\n" \
"v_accvgpr_read_b32 v25, a199\n" \
"buffer_load_dwordx4 v2, s[80:83], s92 offen offset:2048 lds\n" \
"v_mfma_f32_16x16x32_bf16 v[204:207], v[128:131], v[148:151], v[204:207]\n" \
"v_mul_f32_e32 v192, v56, v192\n" \
"v_mul_f32_e32 v193, v56, v193\n" \
"v_mfma_f32_16x16x32_bf16 v[208:211], v[130:133], v[152:155], v[208:211]\n" \
"v_mul_f32_e32 v194, v56, v194\n" \
"v_mul_f32_e32 v195, v56, v195\n" \
"v_mfma_f32_16x16x32_bf16 v[212:215], v[128:131], v[152:155], v[212:215]\n" \
"v_mul_f32_e32 v196, v56, v196\n" \
"v_mul_f32_e32 v197, v56, v197\n" \
"v_mfma_f32_16x16x32_bf16 v[216:219], v[130:133], v[156:159], v[216:219]\n" \
"v_mul_f32_e32 v198, v56, v198\n" \
"v_mul_f32_e32 v199, v56, v199\n" \
"v_mfma_f32_16x16x32_bf16 v[220:223], v[128:131], v[156:159], v[220:223]\n" \
"v_cvt_pk_bf16_f32 v64, v192, v193\n" \
"v_cvt_pk_bf16_f32 v65, v194, v195\n" \
"v_mfma_f32_16x16x32_bf16 v[224:227], v[130:133], v[176:179], v[224:227]\n" \
"buffer_load_dwordx4 v3, s[80:83], s92 offen offset:3072 lds\n" \
"s_add_u32 m0, s93, 0x1000\n" \
"v_mfma_f32_16x16x32_bf16 v[228:231], v[128:131], v[176:179], v[228:231]\n" \
"v_cvt_pk_bf16_f32 v66, v196, v197\n" \
"v_cvt_pk_bf16_f32 v67, v198, v199\n" \
"v_mfma_f32_16x16x32_bf16 v[232:235], v[130:133], v[180:183], v[232:235]\n" \
"v_mul_f32_e32 v200, v56, v200\n" \
"v_mul_f32_e32 v201, v56, v201\n" \
"v_mfma_f32_16x16x32_bf16 v[236:239], v[128:131], v[180:183], v[236:239]\n" \
"v_mfma_f32_16x16x32_bf16 a[208:211], a[216:219], v[64:67], a[208:211]\n" \
"v_mul_f32_e32 v202, v56, v202\n" \
"v_mul_f32_e32 v203, v56, v203\n" \
"v_mfma_f32_16x16x32_bf16 v[240:243], v[130:133], v[184:187], v[240:243]\n" \
"v_mul_f32_e32 v204, v56, v204\n" \
"v_mul_f32_e32 v205, v56, v205\n" \
"buffer_load_dwordx4 v4, s[80:83], s92 offen offset:0 lds\n" \
"v_mul_f32_e32 v206, v56, v206\n" \
"v_mul_f32_e32 v207, v56, v207\n" \
"v_add_f32_e32 v57, v57, v25\n" \
"v_cvt_pk_bf16_f32 v68, v200, v201\n" \
"v_cvt_pk_bf16_f32 v69, v202, v203\n" \
"v_cvt_pk_bf16_f32 v70, v204, v205\n" \
"v_cvt_pk_bf16_f32 v71, v206, v207\n" \
"v_add_f32_e32 v57, v57, v43\n" \
"v_mul_f32_e32 v208, v56, v208\n" \
"v_mfma_f32_16x16x32_bf16 a[208:211], a[216:219], v[68:71], a[208:211]\n" \
"v_mul_f32_e32 v209, v56, v209\n" \
"v_mul_f32_e32 v210, v56, v210\n" \
"v_mul_f32_e32 v211, v56, v211\n" \
"v_mul_f32_e32 v212, v56, v212\n" \
"v_mul_f32_e32 v213, v56, v213\n" \
"v_mul_f32_e32 v214, v56, v214\n" \
"v_mul_f32_e32 v215, v56, v215\n" \
"v_max_f32_e32 v62, v62, v57\n" \
"v_cvt_pk_bf16_f32 v72, v208, v209\n" \
"v_cvt_pk_bf16_f32 v73, v210, v211\n" \
"buffer_load_dwordx4 v5, s[80:83], s92 offen offset:1024 lds\n" \
"v_cvt_pk_bf16_f32 v74, v212, v213\n" \
"v_cvt_pk_bf16_f32 v75, v214, v215\n" \
"v_rcp_f32_e32 v57, v57\n" \
"v_mfma_f32_16x16x32_bf16 v[244:247], v[128:131], v[184:187], v[244:247]\n" \
"v_mfma_f32_16x16x32_bf16 a[208:211], a[216:219], v[72:75], a[208:211]\n" \
"v_mul_f32_e32 v216, v56, v216\n" \
"v_mul_f32_e32 v217, v56, v217\n" \
"v_mul_f32_e32 v218, v56, v218\n" \
"v_mul_f32_e32 v219, v56, v219\n" \
"v_mul_f32_e32 v220, v56, v220\n" \
"v_mul_f32_e32 v221, v56, v221\n" \
"v_mul_f32_e32 v222, v56, v222\n" \
"v_mul_f32_e32 v223, v56, v223\n" \
"v_cvt_pk_bf16_f32 v76, v216, v217\n" \
"v_cvt_pk_bf16_f32 v77, v218, v219\n" \
"v_cvt_pk_bf16_f32 v78, v220, v221\n" \
"buffer_load_dwordx4 v6, s[80:83], s92 offen offset:2048 lds\n" \
"v_cvt_pk_bf16_f32 v79, v222, v223\n" \
"v_mul_f32_e32 v57, v57, v61\n" \
"v_mul_f32_e32 v224, v57, v224\n" \
"v_mfma_f32_16x16x32_bf16 a[208:211], a[216:219], v[76:79], a[208:211]\n" \
"v_mul_f32_e32 v225, v57, v225\n" \
"v_mul_f32_e32 v226, v57, v226\n" \
"v_mul_f32_e32 v227, v57, v227\n" \
"v_mul_f32_e32 v228, v57, v228\n" \
"v_mul_f32_e32 v229, v57, v229\n" \
"v_mul_f32_e32 v230, v57, v230\n" \
"v_mul_f32_e32 v231, v57, v231\n" \
"v_cvt_pk_bf16_f32 v80, v224, v225\n" \
"v_cvt_pk_bf16_f32 v81, v226, v227\n" \
"v_cvt_pk_bf16_f32 v82, v228, v229\n" \
"v_cvt_pk_bf16_f32 v83, v230, v231\n" \
"v_mfma_f32_16x16x32_bf16 v[248:251], v[130:133], v[188:191], v[248:251]\n" \
"v_mul_f32_e32 v232, v57, v232\n" \
"v_mul_f32_e32 v233, v57, v233\n" \
"v_mfma_f32_16x16x32_bf16 a[208:211], a[216:219], v[80:83], a[208:211]\n" \
"buffer_load_dwordx4 v7, s[80:83], s92 offen offset:3072 lds\n" \
"s_add_u32 m0, s93, 0x2000\n" \
"v_mul_f32_e32 v234, v57, v234\n" \
"v_mul_f32_e32 v235, v57, v235\n" \
"v_mul_f32_e32 v236, v57, v236\n" \
"v_mul_f32_e32 v237, v57, v237\n" \
"v_mul_f32_e32 v238, v57, v238\n" \
"v_mul_f32_e32 v239, v57, v239\n" \
"v_cvt_pk_bf16_f32 v84, v232, v233\n" \
"v_cvt_pk_bf16_f32 v85, v234, v235\n" \
"v_cvt_pk_bf16_f32 v86, v236, v237\n" \
"v_cvt_pk_bf16_f32 v87, v238, v239\n" \
"v_mul_f32_e32 v240, v57, v240\n" \
"v_mul_f32_e32 v241, v57, v241\n" \
"v_mfma_f32_16x16x32_bf16 a[208:211], a[216:219], v[84:87], a[208:211]\n" \
"v_mul_f32_e32 v242, v57, v242\n" \
"v_mul_f32_e32 v243, v57, v243\n" \
"v_mul_f32_e32 v244, v57, v244\n" \
"v_mul_f32_e32 v245, v57, v245\n" \
"v_mul_f32_e32 v246, v57, v246\n" \
"v_mul_f32_e32 v247, v57, v247\n" \
"buffer_load_dwordx4 v8, s[80:83], s92 offen offset:0 lds\n" \
"v_cvt_pk_bf16_f32 v96, v240, v241\n" \
"v_cvt_pk_bf16_f32 v97, v242, v243\n" \
"v_cvt_pk_bf16_f32 v98, v244, v245\n" \
"v_cvt_pk_bf16_f32 v99, v246, v247\n" \
"v_mfma_f32_16x16x32_bf16 v[252:255], v[128:131], v[188:191], v[252:255]\n" \
"v_mul_f32_e32 v248, v57, v248\n" \
"v_mfma_f32_16x16x32_bf16 a[208:211], a[216:219], v[96:99], a[208:211]\n" \
"v_mul_f32_e32 v249, v57, v249\n" \
"v_mul_f32_e32 v250, v57, v250\n" \
"v_mul_f32_e32 v251, v57, v251\n" \
"v_cvt_pk_bf16_f32 v100, v248, v249\n" \
"v_cvt_pk_bf16_f32 v101, v250, v251\n" \
"v_cmp_lt_f32_e64 s[72:73], s21, v62\n" \
"v_mul_f32_e32 v252, v57, v252\n" \
"v_mul_f32_e32 v253, v57, v253\n" \
"v_mul_f32_e32 v254, v57, v254\n" \
"v_mul_f32_e32 v255, v57, v255\n" \
"v_cvt_pk_bf16_f32 v102, v252, v253\n" \
"v_cvt_pk_bf16_f32 v103, v254, v255\n" \
"s_nop 1\n" \
"v_mfma_f32_16x16x32_bf16 a[208:211], a[216:219], v[100:103], a[208:211]\n" \
"s_nop 7\n" \
"v_accvgpr_read_b32 v63, a208\n" \
"v_cmp_u_f32_e32 vcc, v63, v63\n" \
"s_nop 0\n" \
"s_or_b64 vcc, s[72:73], vcc\n" \
"s_cbranch_scc0 L_out_%=\n" \
"s_branch L_end_%=\n" \
"L_skb_%=:\n" \
"s_sleep 1\n" \
"buffer_load_dword v57, v61, s[56:59], s96 offen sc1\n" \
"s_add_u32 s101, s101, 1\n" \
"s_waitcnt vmcnt(0)\n" \
"v_cmp_ne_u32_e32 vcc, s62, v57\n" \
"s_cmp_gt_u32 s101, 0x100000\n" \
"s_cbranch_scc1 L_rkb_%=\n" \
"s_nop 2\n" \
"s_cbranch_vccnz L_skb_%=\n" \
"s_branch L_rkb_%=\n" \
"L_sol_%=:\n" \
"s_sleep 1\n" \
"buffer_load_dword v26, v27, s[56:59], s96 offen sc1\n" \
"s_add_u32 s101, s101, 1\n" \
"s_waitcnt vmcnt(0)\n" \
"v_cmp_ne_u32_e32 vcc, s62, v26\n" \
"s_cmp_gt_u32 s101, 0x100000\n" \
"s_cbranch_scc1 L_rol_%=\n" \
"s_nop 2\n" \
"s_cbranch_vccnz L_sol_%=\n" \
"s_branch L_rol_%=\n" \
"L_sos_%=:\n" \
"s_sleep 1\n" \
"buffer_load_dword v26, v27, s[56:59], s96 offen sc1\n" \
"s_add_u32 s101, s101, 1\n" \
"s_waitcnt vmcnt(0)\n" \
"v_cmp_ne_u32_e32 vcc, s62, v26\n" \
"s_cmp_gt_u32 s101, 0x100000\n" \
"s_cbranch_scc1 L_ros_%=\n" \
"s_nop 2\n" \
"s_cbranch_vccnz L_sos_%=\n" \
"s_branch L_ros_%=\n" \
"L_sa_%=:\n" \
"s_sleep 1\n" \
"buffer_load_dword v26, v38, s[56:59], s64 offen sc1\n" \
"s_add_u32 s101, s101, 1\n" \
"s_waitcnt vmcnt(0)\n" \
"v_cmp_ne_u32_e32 vcc, s62, v26\n" \
"s_cmp_gt_u32 s101, 0x100000\n" \
"s_cbranch_scc1 L_ra_%=\n" \
"s_nop 2\n" \
"s_cbranch_vccnz L_sa_%=\n" \
"s_branch L_ra_%=\n" \
"L_sb_%=:\n" \
"s_sleep 1\n" \
"buffer_load_dword v47, v38, s[56:59], s71 offen sc1\n" \
"s_add_u32 s101, s101, 1\n" \
"s_waitcnt vmcnt(0)\n" \
"v_cmp_ne_u32_e32 vcc, s62, v47\n" \
"s_cmp_gt_u32 s101, 0x100000\n" \
"s_cbranch_scc1 L_rb_%=\n" \
"s_nop 2\n" \
"s_cbranch_vccnz L_sb_%=\n" \
"s_branch L_rb_%=\n" \
"L_sx0_%=:\n" \
"s_sleep 1\n" \
"buffer_load_dword v37, v40, s[56:59], s74 offen sc1\n" \
"s_add_u32 s101, s101, 1\n" \
"s_waitcnt vmcnt(0)\n" \
"v_cmp_eq_u32_e64 s[98:99], s62, v37\n" \
"s_cmp_gt_u32 s101, 0x100000\n" \
"s_cbranch_scc1 L_rx0_%=\n" \
"s_or_b32 s75, 0x7f, s100\n" \
"s_andn2_b32 s75, s75, s98\n" \
"s_cbranch_scc1 L_sx0_%=\n" \
"s_branch L_rx0_%=\n" \
"L_sx1_%=:\n" \
"s_sleep 1\n" \
"buffer_load_dword v37, v40, s[56:59], s74 offen sc1\n" \
"s_add_u32 s101, s101, 1\n" \
"s_waitcnt vmcnt(0)\n" \
"v_cmp_eq_u32_e64 s[98:99], s62, v37\n" \
"s_cmp_gt_u32 s101, 0x100000\n" \
"s_cbranch_scc1 L_rx1_%=\n" \
"s_andn2_b32 s75, 0x100, s98\n" \
"s_cbranch_scc1 L_sx1_%=\n" \
"s_branch L_rx1_%=\n" \
"L_sx2_%=:\n" \
"s_sleep 1\n" \
"buffer_load_dword v37, v40, s[56:59], s74 offen sc1\n" \
"s_add_u32 s101, s101, 1\n" \
"s_waitcnt vmcnt(0)\n" \
"v_cmp_eq_u32_e64 s[98:99], s62, v37\n" \
"s_cmp_gt_u32 s101, 0x100000\n" \
"s_cbranch_scc1 L_rx2_%=\n" \
"s_andn2_b32 s75, 0x200, s98\n" \
"s_cbranch_scc1 L_sx2_%=\n" \
"s_branch L_rx2_%=\n" \
"L_sx3_%=:\n" \
"s_sleep 1\n" \
"buffer_load_dword v37, v40, s[56:59], s74 offen sc1\n" \
"s_add_u32 s101, s101, 1\n" \
"s_waitcnt vmcnt(0)\n" \
"v_cmp_eq_u32_e64 s[98:99], s62, v37\n" \
"s_cmp_gt_u32 s101, 0x100000\n" \
"s_cbranch_scc1 L_rx3_%=\n" \
"s_andn2_b32 s75, 0x400, s98\n" \
"s_cbranch_scc1 L_sx3_%=\n" \
"s_branch L_rx3_%=\n" \
"L_sx4_%=:\n" \
"s_sleep 1\n" \
"buffer_load_dword v37, v40, s[56:59], s74 offen sc1\n" \
"s_add_u32 s101, s101, 1\n" \
"s_waitcnt vmcnt(0)\n" \
"v_cmp_eq_u32_e64 s[98:99], s62, v37\n" \
"s_cmp_gt_u32 s101, 0x100000\n" \
"s_cbranch_scc1 L_rx4_%=\n" \
"s_andn2_b32 s75, 0x800, s98\n" \
"s_cbranch_scc1 L_sx4_%=\n" \
"s_branch L_rx4_%=\n" \
"L_sx5_%=:\n" \
"s_sleep 1\n" \
"buffer_load_dword v37, v40, s[56:59], s74 offen sc1\n" \
"s_add_u32 s101, s101, 1\n" \
"s_waitcnt vmcnt(0)\n" \
"v_cmp_eq_u32_e64 s[98:99], s62, v37\n" \
"s_cmp_gt_u32 s101, 0x100000\n" \
"s_cbranch_scc1 L_rx5_%=\n" \
"s_andn2_b32 s75, 0x1000, s98\n" \
"s_cbranch_scc1 L_sx5_%=\n" \
"s_branch L_rx5_%=\n" \
"L_sx6_%=:\n" \
"s_sleep 1\n" \
"buffer_load_dword v37, v40, s[56:59], s74 offen sc1\n" \
"s_add_u32 s101, s101, 1\n" \
"s_waitcnt vmcnt(0)\n" \
"v_cmp_eq_u32_e64 s[98:99], s62, v37\n" \
"s_cmp_gt_u32 s101, 0x100000\n" \
"s_cbranch_scc1 L_rx6_%=\n" \
"s_andn2_b32 s75, 0x2000, s98\n" \
"s_cbranch_scc1 L_sx6_%=\n" \
"s_branch L_rx6_%=\n" \
"L_sx7_%=:\n" \
"s_sleep 1\n" \
"buffer_load_dword v37, v40, s[56:59], s74 offen sc1\n" \
"s_add_u32 s101, s101, 1\n" \
"s_waitcnt vmcnt(0)\n" \
"v_cmp_eq_u32_e64 s[98:99], s62, v37\n" \
"s_cmp_gt_u32 s101, 0x100000\n" \
"s_cbranch_scc1 L_rx7_%=\n" \
"s_andn2_b32 s75, 0x4000, s98\n" \
"s_cbranch_scc1 L_sx7_%=\n" \
"s_branch L_rx7_%=\n" \
"L_att_stub:\n" \
"s_mov_b32 s101, 0\n" \
"v_add_u32_e32 v32, 0x49000, v20\n" \
"v_readlane_b32 s96, v11, 36\n" \
"v_readlane_b32 s97, v11, 37\n" \
"buffer_load_dwordx4 a[96:99], v20, s[56:59], s60 offen offset:0 sc1\n" \
"buffer_load_dwordx4 a[112:115], v32, s[56:59], s60 offen offset:0 sc1\n" \
"buffer_load_dwordx4 a[100:103], v20, s[56:59], s60 offen offset:1024 sc1\n" \
"buffer_load_dwordx4 a[116:119], v32, s[56:59], s60 offen offset:1024 sc1\n" \
"buffer_load_dwordx4 a[104:107], v20, s[56:59], s60 offen offset:2048 sc1\n" \
"buffer_load_dwordx4 a[120:123], v32, s[56:59], s60 offen offset:2048 sc1\n" \
"buffer_load_dwordx4 a[108:111], v20, s[56:59], s60 offen offset:3072 sc1\n" \
"buffer_load_dwordx4 a[124:127], v32, s[56:59], s60 offen offset:3072 sc1\n" \
"buffer_load_dwordx4 a[220:223], v20, s[56:59], s96 offen offset:0 sc1\n" \
"buffer_load_dwordx4 a[228:231], v32, s[56:59], s96 offen offset:0 sc1\n" \
"buffer_load_dwordx4 a[236:239], v20, s[56:59], s97 offen offset:0 sc1\n" \
"buffer_load_dwordx4 a[244:247], v32, s[56:59], s97 offen offset:0 sc1\n" \
"buffer_load_dwordx4 a[224:227], v20, s[56:59], s96 offen offset:1024 sc1\n" \
"buffer_load_dwordx4 a[232:235], v32, s[56:59], s96 offen offset:1024 sc1\n" \
"buffer_load_dwordx4 a[240:243], v20, s[56:59], s97 offen offset:1024 sc1\n" \
"buffer_load_dwordx4 a[248:251], v32, s[56:59], s97 offen offset:1024 sc1\n" \
"buffer_load_dwordx4 v[64:67], v20, s[56:59], s61 offen offset:0 sc1\n" \
"buffer_load_dwordx4 v[68:71], v20, s[56:59], s61 offen offset:1024 sc1\n" \
"buffer_load_dwordx4 v[72:75], v20, s[56:59], s61 offen offset:2048 sc1\n" \
"buffer_load_dwordx4 v[76:79], v20, s[56:59], s61 offen offset:3072 sc1\n" \
"buffer_load_dwordx4 v[80:83], v32, s[56:59], s61 offen offset:0 sc1\n" \
"buffer_load_dwordx4 v[84:87], v32, s[56:59], s61 offen offset:1024 sc1\n" \
"buffer_load_dwordx4 v[96:99], v32, s[56:59], s61 offen offset:2048 sc1\n" \
"buffer_load_dwordx4 v[100:103], v32, s[56:59], s61 offen offset:3072 sc1\n" \
"v_not_b32_e32 v26, s62\n" \
"s_waitcnt vmcnt(0)\n" \
"s_branch L_out_%=\n" \
".fill (L_ab + 16384 - .) / 4, 4, 0xBF800000\n" \
".fill (L_ab + 16384 - .) / 4, 4, 0xBF800000\n" \
"L_ap4:\n" \
"s_setpc_b64 s[88:89]\n" \
"L_gsw:\n" \
"v_mfma_f32_16x16x32_bf16 v[104:107], a[128:131], a[16:19], v[36:39]\n" \
"s_add_u32 s68, s60, 0xfff6e000\n" \
"s_add_u32 s69, s60, 0xfffb7000\n" \
"s_add_u32 s70, s60, 0x0\n" \
"s_add_u32 s71, s60, 0x49000\n" \
"v_mfma_f32_16x16x32_bf16 v[104:107], a[132:135], a[20:23], v[104:107]\n" \
"s_xor_b32 m0, s65, 0x12000\n" \
"ds_read_b128 a[160:163], v49 offset:0\n" \
"v_mfma_f32_16x16x32_bf16 v[104:107], a[136:139], a[24:27], v[104:107]\n" \
"buffer_load_dwordx4 v50, s[8:11], s64 offen offset:0 sc1 lds\n" \
"v_mfma_f32_16x16x32_bf16 v[104:107], a[140:143], a[28:31], v[104:107]\n" \
"ds_read_b128 a[164:167], v49 offset:1024\n" \
"ds_read_b128 a[168:171], v49 offset:2048\n" \
"v_mfma_f32_16x16x32_bf16 v[108:111], a[156:159], a[28:31], v[36:39]\n" \
"ds_read_b128 a[172:175], v49 offset:3072\n" \
"ds_read_b128 a[176:179], v49 offset:4096\n" \
"v_mfma_f32_16x16x32_bf16 v[108:111], a[152:155], a[24:27], v[108:111]\n" \
"buffer_load_dwordx4 v50, s[8:11], s64 offen offset:1024 sc1 lds\n" \
"v_mfma_f32_16x16x32_bf16 v[108:111], a[148:151], a[20:23], v[108:111]\n" \
"s_mov_b32 s65, s13\n" \
"v_add_u32_e32 v63, s15, v20\n" \
"v_mfma_f32_16x16x32_bf16 v[108:111], a[144:147], a[16:19], v[108:111]\n" \
"s_add_u32 s67, s14, 40960\n" \
"v_add_u32_e32 v50, 0x6000, v63\n" \
"v_mfma_f32_16x16x32_bf16 v[116:119], a[144:147], a[32:35], v[40:43]\n" \
"s_xor_b32 m0, s65, 0x8000\n" \
"ds_read_b128 a[180:183], v49 offset:5120\n" \
"v_mfma_f32_16x16x32_bf16 v[116:119], a[148:151], a[36:39], v[116:119]\n" \
"ds_read_b128 a[184:187], v49 offset:6144\n" \
"v_and_b32_e32 v68, v12, v68\n" \
"v_mfma_f32_16x16x32_bf16 v[116:119], a[152:155], a[40:43], v[116:119]\n" \
"v_and_b32_e32 v69, v13, v69\n" \
"v_and_b32_e32 v70, v14, v70\n" \
"v_mfma_f32_16x16x32_bf16 v[116:119], a[156:159], a[44:47], v[116:119]\n" \
"buffer_load_dwordx4 v50, s[8:11], s67 offen offset:0 sc1 lds\n" \
"v_mfma_f32_16x16x32_bf16 v[112:115], a[140:143], a[44:47], v[40:43]\n" \
"v_and_b32_e32 v71, v15, v71\n" \
"ds_read_b128 a[188:191], v49 offset:7168\n" \
"v_mfma_f32_16x16x32_bf16 v[112:115], a[136:139], a[40:43], v[112:115]\n" \
"v_exp_f32_e32 v86, v86\n" \
"v_mfma_f32_16x16x32_bf16 v[112:115], a[132:135], a[36:39], v[112:115]\n" \
"v_exp_f32_e32 v87, v87\n" \
"v_mfma_f32_16x16x32_bf16 v[112:115], a[128:131], a[32:35], v[112:115]\n" \
"v_exp_f32_e32 v84, v84\n" \
"v_mfma_f32_16x16x32_bf16 v[120:123], a[128:131], a[48:51], v[44:47]\n" \
"buffer_load_dwordx4 v50, s[8:11], s67 offen offset:1024 sc1 lds\n" \
"v_mfma_f32_16x16x32_bf16 v[120:123], a[132:135], a[52:55], v[120:123]\n" \
"v_exp_f32_e32 v85, v85\n" \
"v_mfma_f32_16x16x32_bf16 v[120:123], a[136:139], a[56:59], v[120:123]\n" \
"v_cvt_pk_bf16_f32 v87, v86, v87\n" \
"v_cvt_pk_bf16_f32 v86, v84, v85\n" \
"v_mfma_f32_16x16x32_bf16 v[120:123], a[140:143], a[60:63], v[120:123]\n" \
"v_exp_f32_e32 v82, v82\n" \
"v_mfma_f32_16x16x32_bf16 v[124:127], a[156:159], a[60:63], v[44:47]\n" \
"v_exp_f32_e32 v83, v83\n" \
"v_mfma_f32_16x16x32_bf16 v[124:127], a[152:155], a[56:59], v[124:127]\n" \
"v_exp_f32_e32 v80, v80\n" \
"v_mfma_f32_16x16x32_bf16 v[124:127], a[148:151], a[52:55], v[124:127]\n" \
"buffer_load_dwordx4 v50, s[8:11], s67 offen offset:2048 sc1 lds\n" \
"v_mfma_f32_16x16x32_bf16 v[124:127], a[144:147], a[48:51], v[124:127]\n" \
"v_cvt_pk_bf16_f32 v85, v82, v83\n" \
"s_waitcnt lgkmcnt(1)\n" \
"v_mfma_f32_16x16x32_bf16 v[128:131], a[160:163], v[68:71], v[128:131]\n" \
"v_exp_f32_e32 v81, v81\n" \
"v_mfma_f32_16x16x32_bf16 v[160:163], a[160:163], v[72:75], v[160:163]\n" \
"v_cvt_pk_bf16_f32 v84, v80, v81\n" \
"s_mov_b32 s64, s14\n" \
"v_mfma_f32_16x16x32_bf16 v[132:135], a[164:167], v[68:71], v[132:135]\n" \
"v_exp_f32_e32 v88, v88\n" \
"v_mfma_f32_16x16x32_bf16 v[164:167], a[164:167], v[72:75], v[164:167]\n" \
"v_exp_f32_e32 v89, v89\n" \
"v_mfma_f32_16x16x32_bf16 v[136:139], a[168:171], v[68:71], v[136:139]\n" \
"buffer_load_dwordx4 v50, s[8:11], s67 offen offset:3072 sc1 lds\n" \
"v_mfma_f32_16x16x32_bf16 v[168:171], a[168:171], v[72:75], v[168:171]\n" \
"v_exp_f32_e32 v90, v90\n" \
"v_mfma_f32_16x16x32_bf16 v[140:143], a[172:175], v[68:71], v[140:143]\n" \
"v_exp_f32_e32 v91, v91\n" \
"v_mfma_f32_16x16x32_bf16 v[172:175], a[172:175], v[72:75], v[172:175]\n" \
"v_cvt_pk_bf16_f32 v88, v88, v89\n" \
"v_cvt_pk_bf16_f32 v89, v90, v91\n" \
"v_mfma_f32_16x16x32_bf16 v[144:147], a[176:179], v[68:71], v[144:147]\n" \
"v_exp_f32_e32 v92, v92\n" \
"v_mfma_f32_16x16x32_bf16 v[176:179], a[176:179], v[72:75], v[176:179]\n" \
"v_exp_f32_e32 v93, v93\n" \
"v_mfma_f32_16x16x32_bf16 v[148:151], a[180:183], v[68:71], v[148:151]\n" \
"v_exp_f32_e32 v94, v94\n" \
"v_mfma_f32_16x16x32_bf16 v[180:183], a[180:183], v[72:75], v[180:183]\n" \
"v_exp_f32_e32 v95, v95\n" \
"v_mfma_f32_16x16x32_bf16 v[152:155], a[184:187], v[68:71], v[152:155]\n" \
"v_cvt_pk_bf16_f32 v90, v92, v93\n" \
"v_cvt_pk_bf16_f32 v91, v94, v95\n" \
"v_mfma_f32_16x16x32_bf16 v[184:187], a[184:187], v[72:75], v[184:187]\n" \
"v_add_u32_e32 v51, 0x12000, v63\n" \
"s_waitcnt lgkmcnt(0)\n" \
"v_mfma_f32_16x16x32_bf16 v[156:159], a[188:191], v[68:71], v[156:159]\n" \
"s_xor_b32 m0, s65, 0xa000\n" \
"ds_read_b32 v58, v18 offset:512\n" \
"v_mfma_f32_16x16x32_bf16 v[188:191], a[188:191], v[72:75], v[188:191]\n" \
"buffer_load_dwordx4 v51, s[8:11], s64 offen offset:0 sc1 lds\n" \
"v_smfmac_f32_16x16x64_bf16 a[192:195], a[200:203], v[68:75], v21\n" \
"buffer_load_dwordx4 v51, s[8:11], s64 offen offset:1024 sc1 lds\n" \
"v_mfma_f32_16x16x32_bf16 v[220:223], a[188:191], v[84:87], v[220:223]\n" \
"ds_read_b32 v59, v18 offset:640\n" \
"ds_read_b32 v60, v18 offset:768\n" \
"v_mfma_f32_16x16x32_bf16 v[252:255], a[188:191], v[88:91], v[252:255]\n" \
"ds_read_b32 v61, v18 offset:896\n" \
"ds_read_b128 a[128:131], v48 offset:0\n" \
"v_mfma_f32_16x16x32_bf16 v[216:219], a[184:187], v[84:87], v[216:219]\n" \
"ds_read_b128 a[144:147], v48 offset:4096\n" \
"ds_read_b128 a[132:135], v48 offset:1024\n" \
"v_mfma_f32_16x16x32_bf16 v[248:251], a[184:187], v[88:91], v[248:251]\n" \
"ds_read_b128 a[148:151], v48 offset:5120\n" \
"ds_read_b128 a[136:139], v48 offset:2048\n" \
"v_mfma_f32_16x16x32_bf16 v[212:215], a[180:183], v[84:87], v[212:215]\n" \
"ds_read_b128 a[152:155], v48 offset:6144\n" \
"ds_read_b128 a[140:143], v48 offset:3072\n" \
"v_mfma_f32_16x16x32_bf16 v[244:247], a[180:183], v[88:91], v[244:247]\n" \
"ds_read_b128 a[156:159], v48 offset:7168\n" \
"v_add_u32_e32 v52, 0x14000, v63\n" \
"v_mfma_f32_16x16x32_bf16 v[208:211], a[176:179], v[84:87], v[208:211]\n" \
"buffer_load_dwordx4 v51, s[8:11], s64 offen offset:2048 sc1 lds\n" \
"v_mfma_f32_16x16x32_bf16 v[240:243], a[176:179], v[88:91], v[240:243]\n" \
"v_exp_f32_e32 v104, v104\n" \
"v_mfma_f32_16x16x32_bf16 v[204:207], a[172:175], v[84:87], v[204:207]\n" \
"v_exp_f32_e32 v105, v105\n" \
"v_mfma_f32_16x16x32_bf16 v[236:239], a[172:175], v[88:91], v[236:239]\n" \
"buffer_load_dwordx4 v51, s[8:11], s64 offen offset:3072 sc1 lds\n" \
"v_mfma_f32_16x16x32_bf16 v[200:203], a[168:171], v[84:87], v[200:203]\n" \
"v_exp_f32_e32 v106, v106\n" \
"v_mfma_f32_16x16x32_bf16 v[232:235], a[168:171], v[88:91], v[232:235]\n" \
"v_exp_f32_e32 v107, v107\n" \
"v_mfma_f32_16x16x32_bf16 v[196:199], a[164:167], v[84:87], v[196:199]\n" \
"v_cvt_pk_bf16_f32 v104, v104, v105\n" \
"v_cvt_pk_bf16_f32 v105, v106, v107\n" \
"v_mfma_f32_16x16x32_bf16 v[228:231], a[164:167], v[88:91], v[228:231]\n" \
"v_exp_f32_e32 v108, v108\n" \
"v_mfma_f32_16x16x32_bf16 v[192:195], a[160:163], v[84:87], v[192:195]\n" \
"v_exp_f32_e32 v109, v109\n" \
"v_mfma_f32_16x16x32_bf16 v[224:227], a[160:163], v[88:91], v[224:227]\n" \
"v_exp_f32_e32 v110, v110\n" \
"v_smfmac_f32_16x16x64_bf16 a[196:199], a[200:203], v[84:91], v21\n" \
"v_exp_f32_e32 v111, v111\n" \
"v_cvt_pk_bf16_f32 v106, v108, v109\n" \
"v_cvt_pk_bf16_f32 v107, v110, v111\n" \
"v_and_b32_e32 v104, v12, v104\n" \
"v_and_b32_e32 v105, v13, v105\n" \
"v_and_b32_e32 v106, v14, v106\n" \
"v_and_b32_e32 v107, v15, v107\n" \
"v_accvgpr_read_b32 v54, a192\n" \
"v_accvgpr_read_b32 v22, a193\n" \
"v_add_u32_e32 v53, 0x20000, v63\n" \
"v_exp_f32_e32 v118, v118\n" \
"v_exp_f32_e32 v119, v119\n" \
"v_exp_f32_e32 v116, v116\n" \
"v_exp_f32_e32 v117, v117\n" \
"v_cvt_pk_bf16_f32 v119, v118, v119\n" \
"v_exp_f32_e32 v114, v114\n" \
"v_exp_f32_e32 v115, v115\n" \
"v_exp_f32_e32 v112, v112\n" \
"v_exp_f32_e32 v113, v113\n" \
"v_cvt_pk_bf16_f32 v118, v116, v117\n" \
"v_cvt_pk_bf16_f32 v117, v114, v115\n" \
"v_cvt_pk_bf16_f32 v116, v112, v113\n" \
"v_readlane_b32 s93, v11, 19\n" \
"s_waitcnt vmcnt(10) lgkmcnt(0)\n" \
"s_barrier\n" \
"v_mfma_f32_16x16x32_bf16 v[80:83], a[128:131], a[32:35], v[40:43]\n" \
"ds_read_b128 a[160:163], v49 offset:8192\n" \
"ds_read_b128 a[164:167], v49 offset:9216\n" \
"v_mfma_f32_16x16x32_bf16 v[80:83], a[132:135], a[36:39], v[80:83]\n" \
"ds_read_b128 a[168:171], v49 offset:10240\n" \
"s_xor_b32 m0, s65, 0x18000\n" \
"v_mfma_f32_16x16x32_bf16 v[80:83], a[136:139], a[40:43], v[80:83]\n" \
"buffer_load_dwordx4 v52, s[8:11], s64 offen offset:0 sc1 lds\n" \
"v_mfma_f32_16x16x32_bf16 v[80:83], a[140:143], a[44:47], v[80:83]\n" \
"ds_read_b128 a[172:175], v49 offset:11264\n" \
"ds_read_b128 a[176:179], v49 offset:12288\n" \
"v_mfma_f32_16x16x32_bf16 v[84:87], a[156:159], a[44:47], v[40:43]\n" \
"ds_read_b128 a[180:183], v49 offset:13312\n" \
"ds_read_b128 a[184:187], v49 offset:14336\n" \
"v_mfma_f32_16x16x32_bf16 v[84:87], a[152:155], a[40:43], v[84:87]\n" \
"buffer_load_dwordx4 v52, s[8:11], s64 offen offset:1024 sc1 lds\n" \
"v_mfma_f32_16x16x32_bf16 v[84:87], a[148:151], a[36:39], v[84:87]\n" \
"ds_read_b128 a[188:191], v49 offset:15360\n" \
"v_readlane_b32 s80, v11, 11\n" \
"v_mfma_f32_16x16x32_bf16 v[84:87], a[144:147], a[32:35], v[84:87]\n" \
"buffer_load_dwordx4 v52, s[8:11], s64 offen offset:2048 sc1 lds\n" \
"v_mfma_f32_16x16x32_bf16 v[92:95], a[144:147], a[48:51], v[44:47]\n" \
"v_exp_f32_e32 v120, v120\n" \
"v_mfma_f32_16x16x32_bf16 v[92:95], a[148:151], a[52:55], v[92:95]\n" \
"v_exp_f32_e32 v121, v121\n" \
"v_mfma_f32_16x16x32_bf16 v[92:95], a[152:155], a[56:59], v[92:95]\n" \
"buffer_load_dwordx4 v52, s[8:11], s64 offen offset:3072 sc1 lds\n" \
"v_mfma_f32_16x16x32_bf16 v[92:95], a[156:159], a[60:63], v[92:95]\n" \
"s_mov_b32 m0, s65\n" \
"v_cvt_pk_bf16_f32 v120, v120, v121\n" \
"v_mfma_f32_16x16x32_bf16 v[88:91], a[140:143], a[60:63], v[44:47]\n" \
"v_exp_f32_e32 v122, v122\n" \
"v_mfma_f32_16x16x32_bf16 v[88:91], a[136:139], a[56:59], v[88:91]\n" \
"buffer_load_dwordx4 v53, s[8:11], s64 offen offset:0 sc1 lds\n" \
"v_mfma_f32_16x16x32_bf16 v[88:91], a[132:135], a[52:55], v[88:91]\n" \
"v_exp_f32_e32 v123, v123\n" \
"v_mfma_f32_16x16x32_bf16 v[88:91], a[128:131], a[48:51], v[88:91]\n" \
"v_exp_f32_e32 v124, v124\n" \
"v_mfma_f32_16x16x32_bf16 a[192:195], a[212:215], v[104:107], a[192:195]\n" \
"v_cvt_pk_bf16_f32 v121, v122, v123\n" \
"s_waitcnt lgkmcnt(5)\n" \
"v_mfma_f32_16x16x32_bf16 v[160:163], a[160:163], v[104:107], v[160:163]\n" \
"buffer_load_dwordx4 v53, s[8:11], s64 offen offset:1024 sc1 lds\n" \
"v_mfma_f32_16x16x32_bf16 v[164:167], a[164:167], v[104:107], v[164:167]\n" \
"v_exp_f32_e32 v125, v125\n" \
"v_mfma_f32_16x16x32_bf16 v[168:171], a[168:171], v[104:107], v[168:171]\n" \
"v_cvt_pk_bf16_f32 v122, v124, v125\n" \
"s_waitcnt lgkmcnt(1)\n" \
"v_mfma_f32_16x16x32_bf16 v[172:175], a[172:175], v[104:107], v[172:175]\n" \
"v_exp_f32_e32 v126, v126\n" \
"v_mfma_f32_16x16x32_bf16 v[176:179], a[176:179], v[104:107], v[176:179]\n" \
"v_exp_f32_e32 v127, v127\n" \
"v_mfma_f32_16x16x32_bf16 v[180:183], a[180:183], v[104:107], v[180:183]\n" \
"v_cvt_pk_bf16_f32 v123, v126, v127\n" \
"v_readlane_b32 s81, v11, 12\n" \
"v_mfma_f32_16x16x32_bf16 v[184:187], a[184:187], v[104:107], v[184:187]\n" \
"s_mov_b32 s82, 0x47fca0\n" \
"s_waitcnt lgkmcnt(0)\n" \
"v_mfma_f32_16x16x32_bf16 v[188:191], a[188:191], v[104:107], v[188:191]\n" \
"buffer_load_dwordx4 v53, s[8:11], s64 offen offset:2048 sc1 lds\n" \
"v_mfma_f32_16x16x32_bf16 v[220:223], a[188:191], v[116:119], v[220:223]\n" \
"buffer_load_dwordx4 v53, s[8:11], s64 offen offset:3072 sc1 lds\n" \
"v_mfma_f32_16x16x32_bf16 v[252:255], a[188:191], v[120:123], v[252:255]\n" \
"s_mov_b32 s83, 0x20000\n" \
"v_readlane_b32 s92, v11, 26\n" \
"v_mfma_f32_16x16x32_bf16 v[216:219], a[184:187], v[116:119], v[216:219]\n" \
"s_add_u32 m0, s93, 0x0\n" \
"ds_read_b128 a[128:131], v48 offset:8192\n" \
"v_mfma_f32_16x16x32_bf16 v[248:251], a[184:187], v[120:123], v[248:251]\n" \
"ds_read_b128 a[144:147], v48 offset:12288\n" \
"ds_read_b128 a[132:135], v48 offset:9216\n" \
"v_mfma_f32_16x16x32_bf16 v[212:215], a[180:183], v[116:119], v[212:215]\n" \
"buffer_load_dwordx4 v0, s[80:83], s92 offen offset:0 lds\n" \
"v_mfma_f32_16x16x32_bf16 v[244:247], a[180:183], v[120:123], v[244:247]\n" \
"buffer_load_dwordx4 v1, s[80:83], s92 offen offset:1024 lds\n" \
"v_mfma_f32_16x16x32_bf16 v[208:211], a[176:179], v[116:119], v[208:211]\n" \
"ds_read_b128 a[148:151], v48 offset:13312\n" \
"ds_read_b128 a[136:139], v48 offset:10240\n" \
"v_mfma_f32_16x16x32_bf16 v[240:243], a[176:179], v[120:123], v[240:243]\n" \
"ds_read_b128 a[152:155], v48 offset:14336\n" \
"ds_read_b128 a[140:143], v48 offset:11264\n" \
"v_mfma_f32_16x16x32_bf16 v[204:207], a[172:175], v[116:119], v[204:207]\n" \
"buffer_load_dwordx4 v2, s[80:83], s92 offen offset:2048 lds\n" \
"v_mfma_f32_16x16x32_bf16 v[236:239], a[172:175], v[120:123], v[236:239]\n" \
"ds_read_b128 a[156:159], v48 offset:15360\n" \
"s_xor_b32 s65, s65, 0x8000\n" \
"v_mfma_f32_16x16x32_bf16 v[200:203], a[168:171], v[116:119], v[200:203]\n" \
"s_add_u32 s64, s64, 0x10000\n" \
"v_add_u32_e32 v49, 0x10000, v48\n" \
"v_mfma_f32_16x16x32_bf16 v[232:235], a[168:171], v[120:123], v[232:235]\n" \
"buffer_load_dwordx4 v3, s[80:83], s92 offen offset:3072 lds\n" \
"v_mfma_f32_16x16x32_bf16 v[196:199], a[164:167], v[116:119], v[196:199]\n" \
"v_exp_f32_e32 v86, v86\n" \
"v_mfma_f32_16x16x32_bf16 v[228:231], a[164:167], v[120:123], v[228:231]\n" \
"v_exp_f32_e32 v87, v87\n" \
"v_mfma_f32_16x16x32_bf16 v[192:195], a[160:163], v[116:119], v[192:195]\n" \
"v_exp_f32_e32 v84, v84\n" \
"v_mfma_f32_16x16x32_bf16 v[224:227], a[160:163], v[120:123], v[224:227]\n" \
"v_exp_f32_e32 v85, v85\n" \
"v_smfmac_f32_16x16x64_bf16 a[196:199], a[200:203], v[116:123], v21\n" \
"v_exp_f32_e32 v88, v88\n" \
"v_exp_f32_e32 v89, v89\n" \
"v_cvt_pk_bf16_f32 v87, v86, v87\n" \
"v_exp_f32_e32 v82, v82\n" \
"v_exp_f32_e32 v83, v83\n" \
"v_exp_f32_e32 v80, v80\n" \
"v_exp_f32_e32 v81, v81\n" \
"v_exp_f32_e32 v90, v90\n" \
"v_exp_f32_e32 v91, v91\n" \
"v_cvt_pk_bf16_f32 v86, v84, v85\n" \
"v_cvt_pk_bf16_f32 v88, v88, v89\n" \
"v_exp_f32_e32 v92, v92\n" \
"v_exp_f32_e32 v93, v93\n" \
"v_exp_f32_e32 v94, v94\n" \
"v_exp_f32_e32 v95, v95\n" \
"v_cvt_pk_bf16_f32 v85, v82, v83\n" \
"v_cvt_pk_bf16_f32 v84, v80, v81\n" \
"v_cvt_pk_bf16_f32 v89, v90, v91\n" \
"v_and_b32_e32 v84, v12, v84\n" \
"v_and_b32_e32 v85, v13, v85\n" \
"v_and_b32_e32 v86, v14, v86\n" \
"v_and_b32_e32 v87, v15, v87\n" \
"v_cvt_pk_bf16_f32 v90, v92, v93\n" \
"v_cvt_pk_bf16_f32 v91, v94, v95\n" \
"v_add_u32_e32 v48, s16, v20\n" \
"v_add_f32_e32 v54, v54, v22\n" \
"v_accvgpr_read_b32 v55, a194\n" \
"v_accvgpr_read_b32 v23, a195\n" \
"v_mfma_f32_16x16x32_bf16 a[192:195], a[216:219], a[216:219], 0\n" \
"v_max_f32_e32 v62, v62, v54\n" \
"v_add_f32_e32 v55, v55, v23\n" \
"v_max_f32_e32 v62, v62, v55\n" \
"v_rcp_f32_e32 v54, v54\n" \
"v_rcp_f32_e32 v55, v55\n" \
"v_mul_f32_e32 v54, v54, v58\n" \
"v_mul_f32_e32 v55, v55, v59\n" \
"v_mul_f32_e32 v128, v54, v128\n" \
"v_mul_f32_e32 v129, v54, v129\n" \
"v_mul_f32_e32 v136, v54, v136\n" \
"v_mul_f32_e32 v137, v54, v137\n" \
"v_mul_f32_e32 v144, v54, v144\n" \
"v_mul_f32_e32 v145, v54, v145\n" \
"v_mul_f32_e32 v152, v54, v152\n" \
"v_mul_f32_e32 v153, v54, v153\n" \
"v_mul_f32_e32 v160, v55, v160\n" \
"v_mul_f32_e32 v161, v55, v161\n" \
"v_mul_f32_e32 v168, v55, v168\n" \
"v_mul_f32_e32 v169, v55, v169\n" \
"v_mul_f32_e32 v176, v55, v176\n" \
"v_mul_f32_e32 v177, v55, v177\n" \
"v_mul_f32_e32 v184, v55, v184\n" \
"v_mul_f32_e32 v185, v55, v185\n" \
"v_cvt_pk_bf16_f32 v128, v128, v129\n" \
"v_mul_f32_e32 v130, v54, v130\n" \
"v_mul_f32_e32 v131, v54, v131\n" \
"v_cvt_pk_bf16_f32 v136, v136, v137\n" \
"v_mul_f32_e32 v138, v54, v138\n" \
"v_mul_f32_e32 v139, v54, v139\n" \
"v_cvt_pk_bf16_f32 v144, v144, v145\n" \
"v_mul_f32_e32 v146, v54, v146\n" \
"v_mul_f32_e32 v147, v54, v147\n" \
"v_cvt_pk_bf16_f32 v152, v152, v153\n" \
"v_mul_f32_e32 v154, v54, v154\n" \
"v_mul_f32_e32 v155, v54, v155\n" \
"v_cvt_pk_bf16_f32 v160, v160, v161\n" \
"v_mul_f32_e32 v162, v55, v162\n" \
"v_mul_f32_e32 v163, v55, v163\n" \
"v_cvt_pk_bf16_f32 v168, v168, v169\n" \
"v_mul_f32_e32 v170, v55, v170\n" \
"v_mul_f32_e32 v171, v55, v171\n" \
"v_cvt_pk_bf16_f32 v176, v176, v177\n" \
"v_mul_f32_e32 v178, v55, v178\n" \
"v_mul_f32_e32 v179, v55, v179\n" \
"v_cvt_pk_bf16_f32 v184, v184, v185\n" \
"v_mul_f32_e32 v186, v55, v186\n" \
"v_mul_f32_e32 v187, v55, v187\n" \
"v_cvt_pk_bf16_f32 v129, v130, v131\n" \
"v_mul_f32_e32 v132, v54, v132\n" \
"v_mul_f32_e32 v133, v54, v133\n" \
"v_mul_f32_e32 v134, v54, v134\n" \
"v_mul_f32_e32 v135, v54, v135\n" \
"v_cvt_pk_bf16_f32 v137, v138, v139\n" \
"v_mul_f32_e32 v140, v54, v140\n" \
"s_waitcnt vmcnt(12) lgkmcnt(0)\n" \
"s_barrier\n" \
"v_mfma_f32_16x16x32_bf16 v[120:123], a[128:131], a[48:51], v[44:47]\n" \
"s_xor_b32 m0, s65, 0x12000\n" \
"ds_read_b128 a[188:191], v49 offset:7168\n" \
"v_mfma_f32_16x16x32_bf16 v[120:123], a[132:135], a[52:55], v[120:123]\n" \
"buffer_load_dwordx4 v50, s[8:11], s64 offen offset:0 sc1 lds\n" \
"v_mfma_f32_16x16x32_bf16 v[120:123], a[136:139], a[56:59], v[120:123]\n" \
"ds_read_b128 a[184:187], v49 offset:6144\n" \
"ds_read_b128 a[180:183], v49 offset:5120\n" \
"v_mfma_f32_16x16x32_bf16 v[120:123], a[140:143], a[60:63], v[120:123]\n" \
"buffer_load_dwordx4 v50, s[8:11], s64 offen offset:1024 sc1 lds\n" \
"v_mfma_f32_16x16x32_bf16 v[124:127], a[156:159], a[60:63], v[44:47]\n" \
"ds_read_b128 a[176:179], v49 offset:4096\n" \
"ds_read_b128 a[172:175], v49 offset:3072\n" \
"v_mfma_f32_16x16x32_bf16 v[124:127], a[152:155], a[56:59], v[124:127]\n" \
"ds_read_b128 a[168:171], v49 offset:2048\n" \
"ds_read_b128 a[164:167], v49 offset:1024\n" \
"v_mfma_f32_16x16x32_bf16 v[124:127], a[148:151], a[52:55], v[124:127]\n" \
"ds_read_b128 a[160:163], v49 offset:0\n" \
"ds_read_b128 a[0:3], v49 offset:8192\n" \
"v_mfma_f32_16x16x32_bf16 v[124:127], a[144:147], a[48:51], v[124:127]\n" \
"buffer_load_dwordx4 v50, s[8:11], s64 offen offset:2048 sc1 lds\n" \
"v_mfma_f32_16x16x32_bf16 v[32:35], a[220:223], a[64:67], 0\n" \
"ds_read_b128 a[4:7], v49 offset:9216\n" \
"ds_read_b128 a[8:11], v49 offset:10240\n" \
"v_mfma_f32_16x16x32_bf16 v[36:39], a[220:223], a[80:83], 0\n" \
"ds_read_b128 a[12:15], v49 offset:11264\n" \
"ds_read_b128 a[16:19], v49 offset:12288\n" \
"v_mfma_f32_16x16x32_bf16 v[40:43], a[220:223], a[96:99], 0\n" \
"ds_read_b128 a[20:23], v49 offset:13312\n" \
"ds_read_b128 a[24:27], v49 offset:14336\n" \
"v_mfma_f32_16x16x32_bf16 v[44:47], a[220:223], a[112:115], 0\n" \
"buffer_load_dwordx4 v50, s[8:11], s64 offen offset:3072 sc1 lds\n" \
"v_mfma_f32_16x16x32_bf16 v[32:35], a[224:227], a[68:71], v[32:35]\n" \
"s_xor_b32 m0, s65, 0xa000\n" \
"ds_read_b128 a[28:31], v49 offset:15360\n" \
"v_mfma_f32_16x16x32_bf16 v[36:39], a[224:227], a[84:87], v[36:39]\n" \
"v_exp_f32_e32 v120, v120\n" \
"v_mfma_f32_16x16x32_bf16 v[40:43], a[224:227], a[100:103], v[40:43]\n" \
"buffer_load_dwordx4 v51, s[8:11], s64 offen offset:0 sc1 lds\n" \
"v_mfma_f32_16x16x32_bf16 v[44:47], a[224:227], a[116:119], v[44:47]\n" \
"v_exp_f32_e32 v121, v121\n" \
"v_mfma_f32_16x16x32_bf16 v[32:35], a[228:231], a[72:75], v[32:35]\n" \
"v_exp_f32_e32 v122, v122\n" \
"v_mfma_f32_16x16x32_bf16 v[36:39], a[228:231], a[88:91], v[36:39]\n" \
"v_exp_f32_e32 v123, v123\n" \
"v_mfma_f32_16x16x32_bf16 v[40:43], a[228:231], a[104:107], v[40:43]\n" \
"buffer_load_dwordx4 v51, s[8:11], s64 offen offset:1024 sc1 lds\n" \
"v_mfma_f32_16x16x32_bf16 v[44:47], a[228:231], a[120:123], v[44:47]\n" \
"v_cvt_pk_bf16_f32 v120, v120, v121\n" \
"v_cvt_pk_bf16_f32 v121, v122, v123\n" \
"v_mfma_f32_16x16x32_bf16 v[32:35], a[232:235], a[76:79], v[32:35]\n" \
"v_exp_f32_e32 v124, v124\n" \
"v_mfma_f32_16x16x32_bf16 v[36:39], a[232:235], a[92:95], v[36:39]\n" \
"v_exp_f32_e32 v125, v125\n" \
"v_mfma_f32_16x16x32_bf16 v[40:43], a[232:235], a[108:111], v[40:43]\n" \
"buffer_load_dwordx4 v51, s[8:11], s64 offen offset:2048 sc1 lds\n" \
"v_mfma_f32_16x16x32_bf16 v[44:47], a[232:235], a[124:127], v[44:47]\n" \
"v_exp_f32_e32 v126, v126\n" \
"v_smfmac_f32_16x16x64_bf16 a[196:199], a[200:203], v[84:91], v21\n" \
"v_cvt_pk_bf16_f32 v122, v124, v125\n" \
"s_waitcnt lgkmcnt(1)\n" \
"v_mfma_f32_16x16x32_bf16 v[220:223], a[188:191], v[84:87], v[220:223]\n" \
"buffer_load_dwordx4 v51, s[8:11], s64 offen offset:3072 sc1 lds\n" \
"v_mfma_f32_16x16x32_bf16 v[252:255], a[188:191], v[88:91], v[252:255]\n" \
"s_add_u32 m0, s93, 0x1000\n" \
"v_and_b32_e32 v120, v12, v120\n" \
"v_mfma_f32_16x16x32_bf16 v[216:219], a[184:187], v[84:87], v[216:219]\n" \
"v_exp_f32_e32 v127, v127\n" \
"v_mfma_f32_16x16x32_bf16 v[248:251], a[184:187], v[88:91], v[248:251]\n" \
"v_cvt_pk_bf16_f32 v123, v126, v127\n" \
"v_and_b32_e32 v121, v13, v121\n" \
"v_mfma_f32_16x16x32_bf16 v[212:215], a[180:183], v[84:87], v[212:215]\n" \
"buffer_load_dwordx4 v4, s[80:83], s92 offen offset:0 lds\n" \
"v_mfma_f32_16x16x32_bf16 v[244:247], a[180:183], v[88:91], v[244:247]\n" \
"v_and_b32_e32 v122, v14, v122\n" \
"v_and_b32_e32 v123, v15, v123\n" \
"v_mfma_f32_16x16x32_bf16 v[208:211], a[176:179], v[84:87], v[208:211]\n" \
"v_accvgpr_read_b32 v56, a196\n" \
"v_accvgpr_read_b32 v24, a197\n" \
"v_mfma_f32_16x16x32_bf16 v[240:243], a[176:179], v[88:91], v[240:243]\n" \
"ds_read_b128 a[128:131], v48 offset:0\n" \
"ds_read_b128 a[144:147], v48 offset:4096\n" \
"v_mfma_f32_16x16x32_bf16 v[204:207], a[172:175], v[84:87], v[204:207]\n" \
"buffer_load_dwordx4 v5, s[80:83], s92 offen offset:1024 lds\n" \
"v_mfma_f32_16x16x32_bf16 v[236:239], a[172:175], v[88:91], v[236:239]\n" \
"ds_read_b128 a[132:135], v48 offset:1024\n" \
"ds_read_b128 a[148:151], v48 offset:5120\n" \
"v_mfma_f32_16x16x32_bf16 v[200:203], a[168:171], v[84:87], v[200:203]\n" \
"ds_read_b128 a[136:139], v48 offset:2048\n" \
"ds_read_b128 a[152:155], v48 offset:6144\n" \
"v_mfma_f32_16x16x32_bf16 v[232:235], a[168:171], v[88:91], v[232:235]\n" \
"buffer_load_dwordx4 v6, s[80:83], s92 offen offset:2048 lds\n" \
"v_mfma_f32_16x16x32_bf16 v[196:199], a[164:167], v[84:87], v[196:199]\n" \
"ds_read_b128 a[140:143], v48 offset:3072\n" \
"ds_read_b128 a[156:159], v48 offset:7168\n" \
"v_mfma_f32_16x16x32_bf16 v[228:231], a[164:167], v[88:91], v[228:231]\n" \
"v_mul_f32_e32 v141, v54, v141\n" \
"v_mul_f32_e32 v142, v54, v142\n" \
"v_mfma_f32_16x16x32_bf16 v[192:195], a[160:163], v[84:87], v[192:195]\n" \
"v_mul_f32_e32 v143, v54, v143\n" \
"v_cvt_pk_bf16_f32 v145, v146, v147\n" \
"v_mfma_f32_16x16x32_bf16 v[224:227], a[160:163], v[88:91], v[224:227]\n" \
"buffer_load_dwordx4 v7, s[80:83], s92 offen offset:3072 lds\n" \
"v_mfma_f32_16x16x32_bf16 a[196:199], a[212:215], v[120:123], a[196:199]\n" \
"v_mul_f32_e32 v148, v54, v148\n" \
"v_mul_f32_e32 v149, v54, v149\n" \
"v_mfma_f32_16x16x32_bf16 v[224:227], a[0:3], v[120:123], v[224:227]\n" \
"v_mul_f32_e32 v150, v54, v150\n" \
"v_mul_f32_e32 v151, v54, v151\n" \
"v_mfma_f32_16x16x32_bf16 v[228:231], a[4:7], v[120:123], v[228:231]\n" \
"v_cvt_pk_bf16_f32 v153, v154, v155\n" \
"v_mul_f32_e32 v156, v54, v156\n" \
"v_mfma_f32_16x16x32_bf16 v[232:235], a[8:11], v[120:123], v[232:235]\n" \
"v_mul_f32_e32 v157, v54, v157\n" \
"v_mul_f32_e32 v158, v54, v158\n" \
"v_mfma_f32_16x16x32_bf16 v[236:239], a[12:15], v[120:123], v[236:239]\n" \
"v_mul_f32_e32 v159, v54, v159\n" \
"v_cvt_pk_bf16_f32 v161, v162, v163\n" \
"v_mfma_f32_16x16x32_bf16 v[240:243], a[16:19], v[120:123], v[240:243]\n" \
"v_mul_f32_e32 v164, v55, v164\n" \
"v_mul_f32_e32 v165, v55, v165\n" \
"v_mfma_f32_16x16x32_bf16 v[244:247], a[20:23], v[120:123], v[244:247]\n" \
"v_mul_f32_e32 v166, v55, v166\n" \
"v_mul_f32_e32 v167, v55, v167\n" \
"v_mfma_f32_16x16x32_bf16 v[248:251], a[24:27], v[120:123], v[248:251]\n" \
"v_cvt_pk_bf16_f32 v169, v170, v171\n" \
"s_waitcnt lgkmcnt(4)\n" \
"v_mfma_f32_16x16x32_bf16 v[252:255], a[28:31], v[120:123], v[252:255]\n" \
"v_mul_f32_e32 v172, v55, v172\n" \
"v_mul_f32_e32 v173, v55, v173\n" \
"v_mul_f32_e32 v174, v55, v174\n" \
"v_mul_f32_e32 v175, v55, v175\n" \
"v_cvt_pk_bf16_f32 v177, v178, v179\n" \
"v_mul_f32_e32 v180, v55, v180\n" \
"v_mul_f32_e32 v181, v55, v181\n" \
"v_mul_f32_e32 v182, v55, v182\n" \
"v_mul_f32_e32 v183, v55, v183\n" \
"v_cvt_pk_bf16_f32 v185, v186, v187\n" \
"v_mul_f32_e32 v188, v55, v188\n" \
"v_mul_f32_e32 v189, v55, v189\n" \
"v_mul_f32_e32 v190, v55, v190\n" \
"v_mul_f32_e32 v191, v55, v191\n" \
"v_cvt_pk_bf16_f32 v130, v132, v133\n" \
"v_cvt_pk_bf16_f32 v131, v134, v135\n" \
"buffer_store_dwordx4 v[128:131], v20, s[56:59], s68 offen offset:0 sc1\n" \
"v_cvt_pk_bf16_f32 v138, v140, v141\n" \
"v_cvt_pk_bf16_f32 v139, v142, v143\n" \
"buffer_store_dwordx4 v[136:139], v20, s[56:59], s68 offen offset:1024 sc1\n" \
"v_cvt_pk_bf16_f32 v146, v148, v149\n" \
"v_cvt_pk_bf16_f32 v147, v150, v151\n" \
"buffer_store_dwordx4 v[144:147], v20, s[56:59], s68 offen offset:2048 sc1\n" \
"v_cvt_pk_bf16_f32 v154, v156, v157\n" \
"v_cvt_pk_bf16_f32 v155, v158, v159\n" \
"buffer_store_dwordx4 v[152:155], v20, s[56:59], s68 offen offset:3072 sc1\n" \
"v_cvt_pk_bf16_f32 v162, v164, v165\n" \
"v_cvt_pk_bf16_f32 v163, v166, v167\n" \
"buffer_store_dwordx4 v[160:163], v20, s[56:59], s69 offen offset:0 sc1\n" \
"v_cvt_pk_bf16_f32 v170, v172, v173\n" \
"v_cvt_pk_bf16_f32 v171, v174, v175\n" \
"buffer_store_dwordx4 v[168:171], v20, s[56:59], s69 offen offset:1024 sc1\n" \
"v_cvt_pk_bf16_f32 v178, v180, v181\n" \
"v_cvt_pk_bf16_f32 v179, v182, v183\n" \
"buffer_store_dwordx4 v[176:179], v20, s[56:59], s69 offen offset:2048 sc1\n" \
"v_cvt_pk_bf16_f32 v186, v188, v189\n" \
"v_cvt_pk_bf16_f32 v187, v190, v191\n" \
"buffer_store_dwordx4 v[184:187], v20, s[56:59], s69 offen offset:3072 sc1\n" \
"s_add_u32 m0, s93, 0x2000\n" \
"v_mfma_f32_16x16x32_bf16 a[208:211], a[216:219], v[128:131], a[208:211]\n" \
"buffer_load_dwordx4 v8, s[80:83], s92 offen offset:0 lds\n" \
"v_mfma_f32_16x16x32_bf16 a[208:211], a[216:219], v[136:139], a[208:211]\n" \
"v_mfma_f32_32x32x16_bf16 v[128:143], a[216:219], a[216:219], 0\n" \
"v_mfma_f32_16x16x32_bf16 a[208:211], a[216:219], v[144:147], a[208:211]\n" \
"v_mfma_f32_16x16x32_bf16 a[208:211], a[216:219], v[152:155], a[208:211]\n" \
"v_mfma_f32_32x32x16_bf16 v[144:159], a[216:219], a[216:219], 0\n" \
"v_add_f32_e32 v56, v56, v24\n" \
"v_mfma_f32_16x16x32_bf16 a[208:211], a[216:219], v[160:163], a[208:211]\n" \
"v_max_f32_e32 v62, v62, v56\n" \
"v_accvgpr_read_b32 v57, a198\n" \
"s_waitcnt vmcnt(21) lgkmcnt(0)\n" \
"s_barrier\n" \
"v_mfma_f32_16x16x32_bf16 v[64:67], a[128:131], a[64:67], v[32:35]\n" \
"s_xor_b32 m0, s65, 0x18000\n" \
"v_accvgpr_read_b32 v25, a199\n" \
"v_mfma_f32_16x16x32_bf16 a[196:199], a[216:219], a[216:219], 0\n" \
"buffer_load_dwordx4 v52, s[8:11], s64 offen offset:0 sc1 lds\n" \
"v_mfma_f32_16x16x32_bf16 v[64:67], a[132:135], a[68:71], v[64:67]\n" \
"v_rcp_f32_e32 v56, v56\n" \
"v_mfma_f32_16x16x32_bf16 v[64:67], a[136:139], a[72:75], v[64:67]\n" \
"v_mfma_f32_16x16x32_bf16 a[208:211], a[216:219], v[168:171], a[208:211]\n" \
"v_mfma_f32_32x32x16_bf16 v[160:175], a[216:219], a[216:219], 0\n" \
"v_mul_f32_e32 v56, v56, v60\n" \
"v_mfma_f32_16x16x32_bf16 a[208:211], a[216:219], v[176:179], a[208:211]\n" \
"v_mul_f32_e32 v192, v56, v192\n" \
"v_mul_f32_e32 v193, v56, v193\n" \
"v_mfma_f32_16x16x32_bf16 v[64:67], a[140:143], a[76:79], v[64:67]\n" \
"buffer_load_dwordx4 v52, s[8:11], s64 offen offset:1024 sc1 lds\n" \
"v_mfma_f32_16x16x32_bf16 v[68:71], a[156:159], a[76:79], v[32:35]\n" \
"v_cvt_pk_bf16_f32 v192, v192, v193\n" \
"v_mul_f32_e32 v194, v56, v194\n" \
"v_mfma_f32_16x16x32_bf16 v[68:71], a[152:155], a[72:75], v[68:71]\n" \
"v_mul_f32_e32 v195, v56, v195\n" \
"v_cvt_pk_bf16_f32 v193, v194, v195\n" \
"v_mfma_f32_16x16x32_bf16 v[68:71], a[148:151], a[68:71], v[68:71]\n" \
"v_mfma_f32_16x16x32_bf16 a[208:211], a[216:219], v[184:187], a[208:211]\n" \
"v_mfma_f32_32x32x16_bf16 v[176:191], a[216:219], a[216:219], 0\n" \
"v_mul_f32_e32 v196, v56, v196\n" \
"v_mul_f32_e32 v197, v56, v197\n" \
"v_mul_f32_e32 v198, v56, v198\n" \
"v_mul_f32_e32 v199, v56, v199\n" \
"v_mul_f32_e32 v200, v56, v200\n" \
"v_mul_f32_e32 v201, v56, v201\n" \
"v_mfma_f32_16x16x32_bf16 v[68:71], a[144:147], a[64:67], v[68:71]\n" \
"buffer_load_dwordx4 v52, s[8:11], s64 offen offset:2048 sc1 lds\n" \
"v_mfma_f32_16x16x32_bf16 v[76:79], a[144:147], a[80:83], v[36:39]\n" \
"v_cvt_pk_bf16_f32 v194, v196, v197\n" \
"v_cvt_pk_bf16_f32 v195, v198, v199\n" \
"buffer_store_dwordx4 v[192:195], v20, s[56:59], s70 offen offset:0 sc1\n" \
"v_mfma_f32_16x16x32_bf16 v[76:79], a[148:151], a[84:87], v[76:79]\n" \
"v_cvt_pk_bf16_f32 v200, v200, v201\n" \
"v_mul_f32_e32 v202, v56, v202\n" \
"v_mfma_f32_16x16x32_bf16 v[76:79], a[152:155], a[88:91], v[76:79]\n" \
"v_mul_f32_e32 v203, v56, v203\n" \
"v_add_f32_e32 v57, v57, v25\n" \
"v_mfma_f32_16x16x32_bf16 v[76:79], a[156:159], a[92:95], v[76:79]\n" \
"buffer_load_dwordx4 v52, s[8:11], s64 offen offset:3072 sc1 lds\n" \
"v_mfma_f32_16x16x32_bf16 v[72:75], a[140:143], a[92:95], v[36:39]\n" \
"s_mov_b32 m0, s65\n" \
"v_cvt_pk_bf16_f32 v201, v202, v203\n" \
"v_mfma_f32_16x16x32_bf16 v[72:75], a[136:139], a[88:91], v[72:75]\n" \
"v_mfma_f32_16x16x32_bf16 a[208:211], a[216:219], v[192:195], a[208:211]\n" \
"v_mul_f32_e32 v204, v56, v204\n" \
"v_mul_f32_e32 v205, v56, v205\n" \
"v_mfma_f32_16x16x32_bf16 v[72:75], a[132:135], a[84:87], v[72:75]\n" \
"v_mul_f32_e32 v206, v56, v206\n" \
"v_mul_f32_e32 v207, v56, v207\n" \
"v_mfma_f32_16x16x32_bf16 v[72:75], a[128:131], a[80:83], v[72:75]\n" \
"buffer_load_dwordx4 v53, s[8:11], s64 offen offset:0 sc1 lds\n" \
"v_mfma_f32_16x16x32_bf16 v[80:83], a[128:131], a[96:99], v[40:43]\n" \
"v_mul_f32_e32 v208, v56, v208\n" \
"v_mul_f32_e32 v209, v56, v209\n" \
"v_mfma_f32_16x16x32_bf16 v[80:83], a[132:135], a[100:103], v[80:83]\n" \
"v_max_f32_e32 v62, v62, v57\n" \
"v_cvt_pk_bf16_f32 v202, v204, v205\n" \
"v_mfma_f32_16x16x32_bf16 v[80:83], a[136:139], a[104:107], v[80:83]\n" \
"buffer_load_dwordx4 v53, s[8:11], s64 offen offset:1024 sc1 lds\n" \
"v_mfma_f32_16x16x32_bf16 v[80:83], a[140:143], a[108:111], v[80:83]\n" \
"v_cvt_pk_bf16_f32 v203, v206, v207\n" \
"v_cvt_pk_bf16_f32 v208, v208, v209\n" \
"buffer_store_dwordx4 v[200:203], v20, s[56:59], s70 offen offset:1024 sc1\n" \
"v_mfma_f32_16x16x32_bf16 v[84:87], a[156:159], a[108:111], v[40:43]\n" \
"v_mul_f32_e32 v210, v56, v210\n" \
"v_mul_f32_e32 v211, v56, v211\n" \
"v_mfma_f32_16x16x32_bf16 v[84:87], a[152:155], a[104:107], v[84:87]\n" \
"v_rcp_f32_e32 v57, v57\n" \
"v_mfma_f32_16x16x32_bf16 v[84:87], a[148:151], a[100:103], v[84:87]\n" \
"buffer_load_dwordx4 v53, s[8:11], s64 offen offset:2048 sc1 lds\n" \
"v_mfma_f32_16x16x32_bf16 v[84:87], a[144:147], a[96:99], v[84:87]\n" \
"v_mfma_f32_16x16x32_bf16 a[208:211], a[216:219], v[200:203], a[208:211]\n" \
"v_mfma_f32_32x32x16_bf16 v[192:207], a[216:219], a[216:219], 0\n" \
"v_cvt_pk_bf16_f32 v209, v210, v211\n" \
"v_mul_f32_e32 v212, v56, v212\n" \
".fill (L_ab + 20480 - .) / 4, 4, 0xBF800000\n" \
"L_ap5:\n" \
"s_cbranch_execz L_ap0\n" \
"v_mul_f32_e32 v213, v56, v213\n" \
"v_mul_f32_e32 v214, v56, v214\n" \
"v_mul_f32_e32 v215, v56, v215\n" \
"v_mul_f32_e32 v216, v56, v216\n" \
"v_mfma_f32_16x16x32_bf16 v[92:95], a[144:147], a[112:115], v[44:47]\n" \
"ds_read_b128 a[144:147], v48 offset:12288\n" \
"v_mul_f32_e32 v217, v56, v217\n" \
"v_mfma_f32_16x16x32_bf16 v[92:95], a[148:151], a[116:119], v[92:95]\n" \
"ds_read_b128 a[148:151], v48 offset:13312\n" \
"v_cvt_pk_bf16_f32 v210, v212, v213\n" \
"v_mfma_f32_16x16x32_bf16 v[92:95], a[152:155], a[120:123], v[92:95]\n" \
"ds_read_b128 a[152:155], v48 offset:14336\n" \
"v_cvt_pk_bf16_f32 v211, v214, v215\n" \
"buffer_store_dwordx4 v[208:211], v20, s[56:59], s70 offen offset:2048 sc1\n" \
"v_mfma_f32_16x16x32_bf16 v[92:95], a[156:159], a[124:127], v[92:95]\n" \
"ds_read_b128 a[156:159], v48 offset:15360\n" \
"v_cvt_pk_bf16_f32 v216, v216, v217\n" \
"v_mfma_f32_16x16x32_bf16 v[88:91], a[140:143], a[124:127], v[44:47]\n" \
"ds_read_b128 a[140:143], v48 offset:11264\n" \
"v_mul_f32_e32 v218, v56, v218\n" \
"v_mfma_f32_16x16x32_bf16 v[88:91], a[136:139], a[120:123], v[88:91]\n" \
"ds_read_b128 a[136:139], v48 offset:10240\n" \
"v_mul_f32_e32 v219, v56, v219\n" \
"v_mfma_f32_16x16x32_bf16 v[88:91], a[132:135], a[116:119], v[88:91]\n" \
"ds_read_b128 a[132:135], v48 offset:9216\n" \
"v_mul_f32_e32 v57, v57, v61\n" \
"v_mfma_f32_16x16x32_bf16 v[88:91], a[128:131], a[112:115], v[88:91]\n" \
"ds_read_b128 a[128:131], v48 offset:8192\n" \
"v_mfma_f32_16x16x32_bf16 a[208:211], a[216:219], v[208:211], a[208:211]\n" \
"v_cvt_pk_bf16_f32 v217, v218, v219\n" \
"v_mul_f32_e32 v220, v56, v220\n" \
"v_mul_f32_e32 v221, v56, v221\n" \
"v_mul_f32_e32 v222, v56, v222\n" \
"v_mul_f32_e32 v223, v56, v223\n" \
"v_mul_f32_e32 v224, v57, v224\n" \
"v_mul_f32_e32 v225, v57, v225\n" \
"v_cvt_pk_bf16_f32 v218, v220, v221\n" \
"v_cvt_pk_bf16_f32 v219, v222, v223\n" \
"buffer_store_dwordx4 v[216:219], v20, s[56:59], s70 offen offset:3072 sc1\n" \
"v_cvt_pk_bf16_f32 v224, v224, v225\n" \
"v_mul_f32_e32 v226, v57, v226\n" \
"v_mul_f32_e32 v227, v57, v227\n" \
"v_mfma_f32_16x16x32_bf16 a[208:211], a[216:219], v[216:219], a[208:211]\n" \
"v_mfma_f32_32x32x16_bf16 v[208:223], a[216:219], a[216:219], 0\n" \
"v_cvt_pk_bf16_f32 v225, v226, v227\n" \
"v_mul_f32_e32 v228, v57, v228\n" \
"v_mul_f32_e32 v229, v57, v229\n" \
"v_mul_f32_e32 v230, v57, v230\n" \
"v_mul_f32_e32 v231, v57, v231\n" \
"v_mul_f32_e32 v232, v57, v232\n" \
"v_mul_f32_e32 v233, v57, v233\n" \
"v_cvt_pk_bf16_f32 v226, v228, v229\n" \
"v_cvt_pk_bf16_f32 v227, v230, v231\n" \
"buffer_store_dwordx4 v[224:227], v20, s[56:59], s71 offen offset:0 sc1\n" \
"v_cvt_pk_bf16_f32 v232, v232, v233\n" \
"v_mul_f32_e32 v234, v57, v234\n" \
"v_mul_f32_e32 v235, v57, v235\n" \
"v_mfma_f32_16x16x32_bf16 a[208:211], a[216:219], v[224:227], a[208:211]\n" \
"v_cvt_pk_bf16_f32 v233, v234, v235\n" \
"v_mul_f32_e32 v236, v57, v236\n" \
"v_mul_f32_e32 v237, v57, v237\n" \
"v_mul_f32_e32 v238, v57, v238\n" \
"v_mul_f32_e32 v239, v57, v239\n" \
"v_mul_f32_e32 v240, v57, v240\n" \
"v_mul_f32_e32 v241, v57, v241\n" \
"v_cvt_pk_bf16_f32 v234, v236, v237\n" \
"v_cvt_pk_bf16_f32 v235, v238, v239\n" \
"buffer_store_dwordx4 v[232:235], v20, s[56:59], s71 offen offset:1024 sc1\n" \
"v_cvt_pk_bf16_f32 v240, v240, v241\n" \
"v_mul_f32_e32 v242, v57, v242\n" \
"v_mul_f32_e32 v243, v57, v243\n" \
"v_mfma_f32_16x16x32_bf16 a[208:211], a[216:219], v[232:235], a[208:211]\n" \
"v_mfma_f32_32x32x16_bf16 v[224:239], a[216:219], a[216:219], 0\n" \
"v_cvt_pk_bf16_f32 v241, v242, v243\n" \
"v_mul_f32_e32 v244, v57, v244\n" \
"v_mul_f32_e32 v245, v57, v245\n" \
"v_mul_f32_e32 v246, v57, v246\n" \
"v_mul_f32_e32 v247, v57, v247\n" \
"v_mul_f32_e32 v248, v57, v248\n" \
"v_mul_f32_e32 v249, v57, v249\n" \
"v_cvt_pk_bf16_f32 v242, v244, v245\n" \
"v_cvt_pk_bf16_f32 v243, v246, v247\n" \
"buffer_store_dwordx4 v[240:243], v20, s[56:59], s71 offen offset:2048 sc1\n" \
"v_cvt_pk_bf16_f32 v248, v248, v249\n" \
"v_mul_f32_e32 v250, v57, v250\n" \
"v_mul_f32_e32 v251, v57, v251\n" \
"v_mfma_f32_16x16x32_bf16 a[208:211], a[216:219], v[240:243], a[208:211]\n" \
"v_cvt_pk_bf16_f32 v249, v250, v251\n" \
"v_mul_f32_e32 v252, v57, v252\n" \
"v_mul_f32_e32 v253, v57, v253\n" \
"v_mul_f32_e32 v254, v57, v254\n" \
"v_mul_f32_e32 v255, v57, v255\n" \
"v_cvt_pk_bf16_f32 v250, v252, v253\n" \
"v_cvt_pk_bf16_f32 v251, v254, v255\n" \
"buffer_store_dwordx4 v[248:251], v20, s[56:59], s71 offen offset:3072 sc1\n" \
"buffer_load_dwordx4 v53, s[8:11], s64 offen offset:3072 sc1 lds\n" \
"s_waitcnt vmcnt(16) lgkmcnt(0)\n" \
"s_barrier\n" \
"v_mfma_f32_16x16x32_bf16 a[208:211], a[216:219], v[248:251], a[208:211]\n" \
"v_mfma_f32_32x32x16_bf16 v[240:255], a[216:219], a[216:219], 0\n" \
"v_exp_f32_e32 v70, v70\n" \
"v_exp_f32_e32 v71, v71\n" \
"v_exp_f32_e32 v72, v72\n" \
"v_exp_f32_e32 v73, v73\n" \
"s_add_u32 s64, s64, 0x10000\n" \
"v_cmp_lt_f32_e64 s[72:73], s21, v62\n" \
"v_readlane_b32 s95, v11, 3\n" \
"v_accvgpr_read_b32 v63, a208\n" \
"v_cmp_u_f32_e32 vcc, v63, v63\n" \
"v_exp_f32_e32 v68, v68\n" \
"v_exp_f32_e32 v69, v69\n" \
"v_exp_f32_e32 v74, v74\n" \
"v_exp_f32_e32 v75, v75\n" \
"s_or_b64 vcc, s[72:73], vcc\n" \
"s_cselect_b32 s95, 0x40000000, s95\n" \
"s_cselect_b32 s94, 0x7f800000, 0\n" \
"v_cvt_pk_bf16_f32 v71, v70, v71\n" \
"v_exp_f32_e32 v66, v66\n" \
"v_exp_f32_e32 v67, v67\n" \
"v_exp_f32_e32 v64, v64\n" \
"v_exp_f32_e32 v65, v65\n" \
"v_cvt_pk_bf16_f32 v72, v72, v73\n" \
"v_exp_f32_e32 v76, v76\n" \
"v_exp_f32_e32 v77, v77\n" \
"v_exp_f32_e32 v78, v78\n" \
"v_exp_f32_e32 v79, v79\n" \
"v_cvt_pk_bf16_f32 v70, v68, v69\n" \
"v_cvt_pk_bf16_f32 v73, v74, v75\n" \
"v_add_u32_e32 v49, 0x10000, v48\n" \
"s_xor_b32 s65, s65, 0x8000\n" \
"v_cvt_pk_bf16_f32 v69, v66, v67\n" \
"v_cvt_pk_bf16_f32 v68, v64, v65\n" \
"v_cvt_pk_bf16_f32 v74, v76, v77\n" \
"v_cvt_pk_bf16_f32 v75, v78, v79\n" \
"v_xor_b32_e32 v48, 0x8000, v48\n" \
"s_sub_u32 s66, s19, 2\n" \
"s_branch L_2_%=\n" \
"L_f1_%=:\n" \
"ds_read_b128 a[160:163], v49 offset:0\n" \
"ds_read_b128 a[164:167], v49 offset:1024\n" \
"ds_read_b128 a[168:171], v49 offset:2048\n" \
"v_mfma_f32_16x16x32_bf16 v[104:107], a[128:131], a[80:83], v[36:39]\n" \
"v_cvt_pk_bf16_f32 v192, v192, v193\n" \
"v_cvt_pk_bf16_f32 v224, v224, v225\n" \
"v_cvt_pk_bf16_f32 v193, v194, v195\n" \
"v_cvt_pk_bf16_f32 v225, v226, v227\n" \
"ds_read_b128 a[172:175], v49 offset:3072\n" \
"v_cvt_pk_bf16_f32 v194, v196, v197\n" \
"v_cvt_pk_bf16_f32 v195, v198, v199\n" \
"v_cvt_pk_bf16_f32 v226, v228, v229\n" \
"v_cvt_pk_bf16_f32 v227, v230, v231\n" \
"v_mfma_f32_16x16x32_bf16 v[104:107], a[132:135], a[84:87], v[104:107]\n" \
"v_lshl_add_u32 v50, s15, 2, v20\n" \
"v_cvt_pk_bf16_f32 v196, v200, v201\n" \
"v_cvt_pk_bf16_f32 v197, v202, v203\n" \
"v_cvt_pk_bf16_f32 v198, v204, v205\n" \
"v_cvt_pk_bf16_f32 v199, v206, v207\n" \
"v_cvt_pk_bf16_f32 v228, v232, v233\n" \
"v_cvt_pk_bf16_f32 v229, v234, v235\n" \
"v_cvt_pk_bf16_f32 v230, v236, v237\n" \
"v_cvt_pk_bf16_f32 v231, v238, v239\n" \
"v_add_u32_e32 v50, 0x1a000, v50\n" \
"v_mov_b32_e32 v61, s62\n" \
"v_cvt_pk_bf16_f32 v200, v208, v209\n" \
"v_cvt_pk_bf16_f32 v201, v210, v211\n" \
"v_cvt_pk_bf16_f32 v202, v212, v213\n" \
"v_cvt_pk_bf16_f32 v203, v214, v215\n" \
"v_cvt_pk_bf16_f32 v204, v216, v217\n" \
"v_cvt_pk_bf16_f32 v205, v218, v219\n" \
"v_cvt_pk_bf16_f32 v206, v220, v221\n" \
"v_cvt_pk_bf16_f32 v207, v222, v223\n" \
"v_cvt_pk_bf16_f32 v232, v240, v241\n" \
"v_cvt_pk_bf16_f32 v233, v242, v243\n" \
"v_cvt_pk_bf16_f32 v234, v244, v245\n" \
"v_cvt_pk_bf16_f32 v235, v246, v247\n" \
"v_cvt_pk_bf16_f32 v236, v248, v249\n" \
"v_cvt_pk_bf16_f32 v237, v250, v251\n" \
"v_cvt_pk_bf16_f32 v238, v252, v253\n" \
"v_cvt_pk_bf16_f32 v239, v254, v255\n" \
"ds_write_b128 v50, v[192:195] offset:0\n" \
"v_mfma_f32_16x16x32_bf16 v[104:107], a[136:139], a[88:91], v[104:107]\n" \
"ds_read_b128 a[176:179], v49 offset:4096\n" \
"v_add_u32_e32 v56, 0x49000, v20\n" \
"v_mfma_f32_16x16x32_bf16 v[104:107], a[140:143], a[92:95], v[104:107]\n" \
"ds_write_b128 v50, v[196:199] offset:1024\n" \
"v_mfma_f32_16x16x32_bf16 v[108:111], a[156:159], a[92:95], v[36:39]\n" \
"ds_read_b128 a[180:183], v49 offset:5120\n" \
"ds_read_b128 a[184:187], v49 offset:6144\n" \
"v_mfma_f32_16x16x32_bf16 v[108:111], a[152:155], a[88:91], v[108:111]\n" \
"ds_write_b128 v50, v[200:203] offset:2048\n" \
"v_mfma_f32_16x16x32_bf16 v[108:111], a[148:151], a[84:87], v[108:111]\n" \
"v_and_b32_e32 v68, v12, v68\n" \
"v_and_b32_e32 v69, v13, v69\n" \
"v_mfma_f32_16x16x32_bf16 v[108:111], a[144:147], a[80:83], v[108:111]\n" \
"ds_write_b128 v50, v[204:207] offset:3072\n" \
"v_and_b32_e32 v70, v14, v70\n" \
"v_and_b32_e32 v71, v15, v71\n" \
"ds_read_b128 a[188:191], v49 offset:7168\n" \
"ds_read_b128 a[0:3], v20 offset:8192\n" \
"s_waitcnt lgkmcnt(9)\n" \
"v_mfma_f32_16x16x32_bf16 v[128:131], a[160:163], v[68:71], v[128:131]\n" \
"ds_write_b128 v50, v[224:227] offset:4096\n" \
"v_mfma_f32_16x16x32_bf16 v[160:163], a[160:163], v[72:75], v[160:163]\n" \
"ds_read_b128 a[4:7], v20 offset:9216\n" \
"ds_read_b128 a[8:11], v20 offset:10240\n" \
"v_mfma_f32_16x16x32_bf16 v[132:135], a[164:167], v[68:71], v[132:135]\n" \
"ds_write_b128 v50, v[228:231] offset:5120\n" \
"v_mfma_f32_16x16x32_bf16 v[164:167], a[164:167], v[72:75], v[164:167]\n" \
"ds_read_b128 a[12:15], v20 offset:11264\n" \
"ds_read_b128 a[28:31], v20 offset:15360\n" \
"v_mfma_f32_16x16x32_bf16 v[136:139], a[168:171], v[68:71], v[136:139]\n" \
"ds_write_b128 v50, v[232:235] offset:6144\n" \
"v_mfma_f32_16x16x32_bf16 v[168:171], a[168:171], v[72:75], v[168:171]\n" \
"ds_read_b128 a[24:27], v20 offset:14336\n" \
"ds_read_b128 a[20:23], v20 offset:13312\n" \
"v_mfma_f32_16x16x32_bf16 v[140:143], a[172:175], v[68:71], v[140:143]\n" \
"ds_write_b128 v50, v[236:239] offset:7168\n" \
"v_mfma_f32_16x16x32_bf16 v[172:175], a[172:175], v[72:75], v[172:175]\n" \
"ds_read_b128 a[16:19], v20 offset:12288\n" \
"s_waitcnt lgkmcnt(15)\n" \
"v_mfma_f32_16x16x32_bf16 v[144:147], a[176:179], v[68:71], v[144:147]\n" \
"v_xor_b32_e32 v48, 0x4000, v49\n" \
"ds_read_b32 v58, v18 offset:256\n" \
"v_mfma_f32_16x16x32_bf16 v[176:179], a[176:179], v[72:75], v[176:179]\n" \
"ds_read_b32 v59, v18 offset:384\n" \
"ds_read_b128 a[32:35], v48 offset:8192\n" \
"v_mfma_f32_16x16x32_bf16 v[148:151], a[180:183], v[68:71], v[148:151]\n" \
"ds_read_b128 a[36:39], v48 offset:9216\n" \
"ds_read_b128 a[40:43], v48 offset:10240\n" \
"v_mfma_f32_16x16x32_bf16 v[180:183], a[180:183], v[72:75], v[180:183]\n" \
"ds_read_b128 a[44:47], v48 offset:11264\n" \
"ds_read_b128 a[48:51], v48 offset:12288\n" \
"v_mfma_f32_16x16x32_bf16 v[152:155], a[184:187], v[68:71], v[152:155]\n" \
"ds_read_b128 a[52:55], v48 offset:13312\n" \
"ds_read_b128 a[56:59], v48 offset:14336\n" \
"v_mfma_f32_16x16x32_bf16 v[184:187], a[184:187], v[72:75], v[184:187]\n" \
"ds_read_b128 a[60:63], v48 offset:15360\n" \
"s_waitcnt lgkmcnt(15)\n" \
"v_mfma_f32_16x16x32_bf16 v[156:159], a[188:191], v[68:71], v[156:159]\n" \
"ds_read_b128 a[128:131], v49 offset:8192\n" \
"ds_read_b128 a[132:135], v49 offset:9216\n" \
"v_mfma_f32_16x16x32_bf16 v[188:191], a[188:191], v[72:75], v[188:191]\n" \
"ds_read_b128 a[136:139], v49 offset:10240\n" \
"ds_read_b128 a[140:143], v49 offset:11264\n" \
"v_smfmac_f32_16x16x64_bf16 a[192:195], a[200:203], v[68:75], v21\n" \
"ds_read_b128 a[144:147], v49 offset:12288\n" \
"ds_read_b128 a[148:151], v49 offset:13312\n" \
"v_mfma_f32_16x16x32_bf16 v[112:115], a[0:3], a[64:67], v[32:35]\n" \
"ds_read_b128 a[152:155], v49 offset:14336\n" \
"ds_read_b128 a[156:159], v49 offset:15360\n" \
"v_mfma_f32_16x16x32_bf16 v[112:115], a[4:7], a[68:71], v[112:115]\n" \
"v_accvgpr_read_b32 v40, a196\n" \
"v_accvgpr_read_b32 v24, a197\n" \
"v_mfma_f32_16x16x32_bf16 v[112:115], a[8:11], a[72:75], v[112:115]\n" \
"v_accvgpr_read_b32 v41, a198\n" \
"v_accvgpr_read_b32 v25, a199\n" \
"v_mfma_f32_16x16x32_bf16 v[112:115], a[12:15], a[76:79], v[112:115]\n" \
"v_add_f32_e32 v40, v40, v24\n" \
"v_add_f32_e32 v41, v41, v25\n" \
"v_mfma_f32_16x16x32_bf16 v[116:119], a[28:31], a[76:79], v[32:35]\n" \
"v_add_f32_e32 v40, s94, v40\n" \
"s_waitcnt lgkmcnt(15)\n" \
"v_mfma_f32_16x16x32_bf16 v[116:119], a[24:27], a[72:75], v[116:119]\n" \
"v_add_f32_e32 v41, s94, v41\n" \
"ds_write_b64 v19, v[40:41]\n" \
"v_mfma_f32_16x16x32_bf16 v[116:119], a[20:23], a[68:71], v[116:119]\n" \
"v_add_u32_e32 v52, s15, v20\n" \
"v_add_u32_e32 v51, 0x10000, v52\n" \
"v_mfma_f32_16x16x32_bf16 v[116:119], a[16:19], a[64:67], v[116:119]\n" \
"v_exp_f32_e32 v104, v104\n" \
"v_mfma_f32_16x16x32_bf16 v[124:127], a[16:19], a[80:83], v[36:39]\n" \
"v_exp_f32_e32 v105, v105\n" \
"v_mfma_f32_16x16x32_bf16 v[124:127], a[20:23], a[84:87], v[124:127]\n" \
"v_exp_f32_e32 v106, v106\n" \
"v_mfma_f32_16x16x32_bf16 v[124:127], a[24:27], a[88:91], v[124:127]\n" \
"v_exp_f32_e32 v107, v107\n" \
"v_mfma_f32_16x16x32_bf16 v[124:127], a[28:31], a[92:95], v[124:127]\n" \
"v_cvt_pk_bf16_f32 v104, v104, v105\n" \
"v_cvt_pk_bf16_f32 v105, v106, v107\n" \
"v_mfma_f32_16x16x32_bf16 v[120:123], a[12:15], a[92:95], v[36:39]\n" \
"v_exp_f32_e32 v108, v108\n" \
"v_mfma_f32_16x16x32_bf16 v[120:123], a[8:11], a[88:91], v[120:123]\n" \
"v_exp_f32_e32 v109, v109\n" \
"v_mfma_f32_16x16x32_bf16 v[120:123], a[4:7], a[84:87], v[120:123]\n" \
"v_exp_f32_e32 v110, v110\n" \
"v_mfma_f32_16x16x32_bf16 v[120:123], a[0:3], a[80:83], v[120:123]\n" \
"v_exp_f32_e32 v111, v111\n" \
"s_waitcnt lgkmcnt(0)\n" \
"s_barrier\n" \
"ds_read_b128 v[208:211], v52 offset:0\n" \
"ds_read_b128 v[212:215], v52 offset:1024\n" \
"ds_read_b128 v[216:219], v52 offset:2048\n" \
"ds_read_b128 v[220:223], v52 offset:3072\n" \
"ds_read_b128 v[240:243], v51 offset:32768\n" \
"ds_read_b128 v[244:247], v51 offset:33792\n" \
"ds_read_b128 v[248:251], v51 offset:34816\n" \
"ds_read_b128 v[252:255], v51 offset:35840\n" \
"v_cvt_pk_bf16_f32 v106, v108, v109\n" \
"v_cvt_pk_bf16_f32 v107, v110, v111\n" \
"v_and_b32_e32 v104, v12, v104\n" \
"v_and_b32_e32 v105, v13, v105\n" \
"v_and_b32_e32 v106, v14, v106\n" \
"s_waitcnt vmcnt(0)\n" \
"s_mov_b64 exec, 1\n" \
"buffer_store_dword v61, off, s[56:59], s95 offset:0 sc1\n" \
"s_mov_b64 exec, -1\n" \
"v_and_b32_e32 v107, v15, v107\n" \
"v_xor_b32_e32 v63, 0x400, v19\n" \
"ds_read_b64 v[42:43], v63\n" \
"v_mfma_f32_16x16x32_bf16 a[192:195], a[212:215], v[104:107], a[192:195]\n" \
"v_mov_b32_e32 v194, v28\n" \
"v_mov_b32_e32 v195, v29\n" \
"v_mfma_f32_16x16x32_bf16 v[160:163], a[128:131], v[104:107], v[160:163]\n" \
"v_mov_b32_e32 v196, 0\n" \
"v_mov_b32_e32 v197, 0\n" \
"v_mfma_f32_16x16x32_bf16 v[164:167], a[132:135], v[104:107], v[164:167]\n" \
"v_mov_b32_e32 v192, 0\n" \
"v_mov_b32_e32 v193, 0\n" \
"v_mfma_f32_16x16x32_bf16 v[168:171], a[136:139], v[104:107], v[168:171]\n" \
"v_readlane_b32 s93, v11, 22\n" \
"v_readlane_b32 s92, v11, 27\n" \
"v_mfma_f32_16x16x32_bf16 v[172:175], a[140:143], v[104:107], v[172:175]\n" \
"buffer_load_dwordx4 a[96:99], v20, s[56:59], s60 offen offset:0 sc1\n" \
"v_mfma_f32_16x16x32_bf16 v[176:179], a[144:147], v[104:107], v[176:179]\n" \
"v_exp_f32_e32 v118, v118\n" \
"v_mfma_f32_16x16x32_bf16 v[180:183], a[148:151], v[104:107], v[180:183]\n" \
"v_exp_f32_e32 v119, v119\n" \
"v_mfma_f32_16x16x32_bf16 v[184:187], a[152:155], v[104:107], v[184:187]\n" \
"s_add_u32 m0, s93, 0x0\n" \
"v_cvt_pk_bf16_f32 v119, v118, v119\n" \
"v_mfma_f32_16x16x32_bf16 v[188:191], a[156:159], v[104:107], v[188:191]\n" \
"v_exp_f32_e32 v120, v120\n" \
"s_waitcnt lgkmcnt(1)\n" \
"s_barrier\n" \
"buffer_load_dwordx4 a[112:115], v56, s[56:59], s60 offen offset:0 sc1\n" \
"v_mfma_f32_16x16x32_bf16 v[128:131], v[194:197], v[208:211], v[128:131]\n" \
"v_exp_f32_e32 v121, v121\n" \
"v_mfma_f32_16x16x32_bf16 v[132:135], v[192:195], v[208:211], v[132:135]\n" \
"v_exp_f32_e32 v116, v116\n" \
"v_mfma_f32_16x16x32_bf16 v[136:139], v[194:197], v[212:215], v[136:139]\n" \
"v_exp_f32_e32 v117, v117\n" \
"v_mfma_f32_16x16x32_bf16 v[140:143], v[192:195], v[212:215], v[140:143]\n" \
"v_exp_f32_e32 v122, v122\n" \
"v_mfma_f32_16x16x32_bf16 v[144:147], v[194:197], v[216:219], v[144:147]\n" \
"v_exp_f32_e32 v123, v123\n" \
"v_mfma_f32_16x16x32_bf16 v[148:151], v[192:195], v[216:219], v[148:151]\n" \
"buffer_load_dwordx4 a[100:103], v20, s[56:59], s60 offen offset:1024 sc1\n" \
"v_mfma_f32_16x16x32_bf16 v[152:155], v[194:197], v[220:223], v[152:155]\n" \
"v_exp_f32_e32 v114, v114\n" \
"v_mfma_f32_16x16x32_bf16 v[156:159], v[192:195], v[220:223], v[156:159]\n" \
"v_exp_f32_e32 v115, v115\n" \
"v_mfma_f32_16x16x32_bf16 v[160:163], v[194:197], v[240:243], v[160:163]\n" \
"v_exp_f32_e32 v112, v112\n" \
"v_mfma_f32_16x16x32_bf16 v[164:167], v[192:195], v[240:243], v[164:167]\n" \
"v_exp_f32_e32 v113, v113\n" \
"v_mfma_f32_16x16x32_bf16 v[168:171], v[194:197], v[244:247], v[168:171]\n" \
"v_cvt_pk_bf16_f32 v120, v120, v121\n" \
"v_cvt_pk_bf16_f32 v118, v116, v117\n" \
"v_mfma_f32_16x16x32_bf16 v[172:175], v[192:195], v[244:247], v[172:175]\n" \
"buffer_load_dwordx4 a[116:119], v56, s[56:59], s60 offen offset:1024 sc1\n" \
"v_mfma_f32_16x16x32_bf16 v[176:179], v[194:197], v[248:251], v[176:179]\n" \
"v_exp_f32_e32 v124, v124\n" \
"v_mfma_f32_16x16x32_bf16 v[180:183], v[192:195], v[248:251], v[180:183]\n" \
"v_exp_f32_e32 v125, v125\n" \
"v_mfma_f32_16x16x32_bf16 v[184:187], v[194:197], v[252:255], v[184:187]\n" \
"v_exp_f32_e32 v126, v126\n" \
"v_mfma_f32_16x16x32_bf16 v[188:191], v[192:195], v[252:255], v[188:191]\n" \
"v_exp_f32_e32 v127, v127\n" \
"v_cvt_pk_bf16_f32 v121, v122, v123\n" \
"v_cvt_pk_bf16_f32 v117, v114, v115\n" \
"v_cvt_pk_bf16_f32 v116, v112, v113\n" \
"v_cvt_pk_bf16_f32 v122, v124, v125\n" \
"v_cvt_pk_bf16_f32 v123, v126, v127\n" \
"buffer_load_dwordx4 a[104:107], v20, s[56:59], s60 offen offset:2048 sc1\n" \
"s_nop 0\n" \
"v_smfmac_f32_16x16x64_bf16 a[192:195], a[200:203], v[116:123], v21\n" \
"v_mfma_f32_16x16x32_bf16 v[128:131], a[32:35], v[116:119], v[128:131]\n" \
"v_mfma_f32_16x16x32_bf16 v[160:163], a[32:35], v[120:123], v[160:163]\n" \
"v_mfma_f32_16x16x32_bf16 v[132:135], a[36:39], v[116:119], v[132:135]\n" \
"v_mfma_f32_16x16x32_bf16 v[164:167], a[36:39], v[120:123], v[164:167]\n" \
"v_mfma_f32_16x16x32_bf16 v[136:139], a[40:43], v[116:119], v[136:139]\n" \
"buffer_load_dwordx4 a[120:123], v56, s[56:59], s60 offen offset:2048 sc1\n" \
"v_mfma_f32_16x16x32_bf16 v[168:171], a[40:43], v[120:123], v[168:171]\n" \
"v_mfma_f32_16x16x32_bf16 v[140:143], a[44:47], v[116:119], v[140:143]\n" \
"v_accvgpr_read_b32 v54, a192\n" \
"v_accvgpr_read_b32 v22, a193\n" \
"v_mfma_f32_16x16x32_bf16 v[172:175], a[44:47], v[120:123], v[172:175]\n" \
"v_add_f32_e32 v54, v54, v22\n" \
"s_waitcnt lgkmcnt(0)\n" \
"v_add_f32_e32 v54, v54, v42\n" \
"v_mfma_f32_16x16x32_bf16 v[144:147], a[48:51], v[116:119], v[144:147]\n" \
"v_max_f32_e32 v62, v62, v54\n" \
"v_accvgpr_read_b32 v55, a194\n" \
"v_mfma_f32_16x16x32_bf16 v[176:179], a[48:51], v[120:123], v[176:179]\n" \
"v_rcp_f32_e32 v54, v54\n" \
"buffer_load_dwordx4 a[108:111], v20, s[56:59], s60 offen offset:3072 sc1\n" \
"v_mfma_f32_16x16x32_bf16 v[148:151], a[52:55], v[116:119], v[148:151]\n" \
"v_mul_f32_e32 v54, v54, v58\n" \
"v_mul_f32_e32 v128, v54, v128\n" \
"v_mfma_f32_16x16x32_bf16 v[180:183], a[52:55], v[120:123], v[180:183]\n" \
"v_mul_f32_e32 v129, v54, v129\n" \
"v_mul_f32_e32 v130, v54, v130\n" \
"v_mul_f32_e32 v131, v54, v131\n" \
"v_mul_f32_e32 v132, v54, v132\n" \
"v_mul_f32_e32 v133, v54, v133\n" \
"v_mul_f32_e32 v134, v54, v134\n" \
"v_mul_f32_e32 v135, v54, v135\n" \
"v_cvt_pk_bf16_f32 v64, v128, v129\n" \
"v_cvt_pk_bf16_f32 v65, v130, v131\n" \
"v_cvt_pk_bf16_f32 v66, v132, v133\n" \
"v_cvt_pk_bf16_f32 v67, v134, v135\n" \
"v_accvgpr_read_b32 v23, a195\n" \
"v_mfma_f32_16x16x32_bf16 v[152:155], a[56:59], v[116:119], v[152:155]\n" \
"v_mfma_f32_16x16x32_bf16 a[208:211], a[216:219], v[64:67], a[208:211]\n" \
"buffer_load_dwordx4 a[124:127], v56, s[56:59], s60 offen offset:3072 sc1\n" \
"v_mul_f32_e32 v136, v54, v136\n" \
"v_mul_f32_e32 v137, v54, v137\n" \
"v_mul_f32_e32 v138, v54, v138\n" \
"v_mul_f32_e32 v139, v54, v139\n" \
"v_mul_f32_e32 v140, v54, v140\n" \
"v_mul_f32_e32 v141, v54, v141\n" \
"v_mul_f32_e32 v142, v54, v142\n" \
"v_mul_f32_e32 v143, v54, v143\n" \
"v_add_f32_e32 v55, v55, v23\n" \
"v_cvt_pk_bf16_f32 v68, v136, v137\n" \
"v_cvt_pk_bf16_f32 v69, v138, v139\n" \
"v_cvt_pk_bf16_f32 v70, v140, v141\n" \
"v_cvt_pk_bf16_f32 v71, v142, v143\n" \
"v_add_f32_e32 v55, v55, v43\n" \
"v_mul_f32_e32 v144, v54, v144\n" \
"v_mfma_f32_16x16x32_bf16 a[208:211], a[216:219], v[68:71], a[208:211]\n" \
"v_mul_f32_e32 v145, v54, v145\n" \
"v_mul_f32_e32 v146, v54, v146\n" \
"v_mul_f32_e32 v147, v54, v147\n" \
"v_mul_f32_e32 v148, v54, v148\n" \
"buffer_load_dwordx4 v0, s[80:83], s92 offen offset:0 lds\n" \
"v_mul_f32_e32 v149, v54, v149\n" \
"v_mul_f32_e32 v150, v54, v150\n" \
"v_mul_f32_e32 v151, v54, v151\n" \
"v_max_f32_e32 v62, v62, v55\n" \
"v_cvt_pk_bf16_f32 v72, v144, v145\n" \
"v_cvt_pk_bf16_f32 v73, v146, v147\n" \
"v_cvt_pk_bf16_f32 v74, v148, v149\n" \
"v_cvt_pk_bf16_f32 v75, v150, v151\n" \
"v_rcp_f32_e32 v55, v55\n" \
"v_mfma_f32_16x16x32_bf16 v[184:187], a[56:59], v[120:123], v[184:187]\n" \
"v_mfma_f32_16x16x32_bf16 a[208:211], a[216:219], v[72:75], a[208:211]\n" \
"v_mul_f32_e32 v152, v54, v152\n" \
"v_mul_f32_e32 v153, v54, v153\n" \
"v_mul_f32_e32 v154, v54, v154\n" \
"v_mul_f32_e32 v155, v54, v155\n" \
"v_cvt_pk_bf16_f32 v76, v152, v153\n" \
"buffer_load_dwordx4 v1, s[80:83], s92 offen offset:1024 lds\n" \
"v_cvt_pk_bf16_f32 v77, v154, v155\n" \
"v_mul_f32_e32 v55, v55, v59\n" \
"v_mul_f32_e32 v160, v55, v160\n" \
"v_mul_f32_e32 v161, v55, v161\n" \
"v_mul_f32_e32 v162, v55, v162\n" \
"v_mul_f32_e32 v163, v55, v163\n" \
"v_mul_f32_e32 v164, v55, v164\n" \
"v_mul_f32_e32 v165, v55, v165\n" \
"v_mul_f32_e32 v166, v55, v166\n" \
"v_mul_f32_e32 v167, v55, v167\n" \
"v_cvt_pk_bf16_f32 v80, v160, v161\n" \
"v_cvt_pk_bf16_f32 v81, v162, v163\n" \
"v_cvt_pk_bf16_f32 v82, v164, v165\n" \
"v_cvt_pk_bf16_f32 v83, v166, v167\n" \
"v_mfma_f32_16x16x32_bf16 v[156:159], a[60:63], v[116:119], v[156:159]\n" \
"v_mul_f32_e32 v168, v55, v168\n" \
"v_mul_f32_e32 v169, v55, v169\n" \
"v_mul_f32_e32 v170, v55, v170\n" \
"v_mul_f32_e32 v171, v55, v171\n" \
"v_mul_f32_e32 v172, v55, v172\n" \
"buffer_load_dwordx4 v2, s[80:83], s92 offen offset:2048 lds\n" \
"v_mul_f32_e32 v173, v55, v173\n" \
"v_mul_f32_e32 v174, v55, v174\n" \
"v_mul_f32_e32 v156, v54, v156\n" \
"v_mul_f32_e32 v157, v54, v157\n" \
"v_mul_f32_e32 v158, v54, v158\n" \
"v_mul_f32_e32 v159, v54, v159\n" \
"v_cvt_pk_bf16_f32 v78, v156, v157\n" \
"v_cvt_pk_bf16_f32 v79, v158, v159\n" \
"v_mul_f32_e32 v175, v55, v175\n" \
"v_cvt_pk_bf16_f32 v84, v168, v169\n" \
"v_mfma_f32_16x16x32_bf16 a[208:211], a[216:219], v[76:79], a[208:211]\n" \
"v_mfma_f32_16x16x32_bf16 a[208:211], a[216:219], v[80:83], a[208:211]\n" \
"v_cvt_pk_bf16_f32 v85, v170, v171\n" \
"v_cvt_pk_bf16_f32 v86, v172, v173\n" \
"v_cvt_pk_bf16_f32 v87, v174, v175\n" \
"v_mul_f32_e32 v176, v55, v176\n" \
"v_mul_f32_e32 v177, v55, v177\n" \
"buffer_load_dwordx4 v3, s[80:83], s92 offen offset:3072 lds\n" \
"s_add_u32 m0, s93, 0x1000\n" \
"v_mfma_f32_16x16x32_bf16 a[208:211], a[216:219], v[84:87], a[208:211]\n" \
"v_mul_f32_e32 v178, v55, v178\n" \
"v_mul_f32_e32 v179, v55, v179\n" \
"v_mul_f32_e32 v180, v55, v180\n" \
"v_mul_f32_e32 v181, v55, v181\n" \
"v_mul_f32_e32 v182, v55, v182\n" \
"v_mul_f32_e32 v183, v55, v183\n" \
"v_cvt_pk_bf16_f32 v96, v176, v177\n" \
"v_cvt_pk_bf16_f32 v97, v178, v179\n" \
"v_cvt_pk_bf16_f32 v98, v180, v181\n" \
"v_cvt_pk_bf16_f32 v99, v182, v183\n" \
"v_mfma_f32_16x16x32_bf16 v[188:191], a[60:63], v[120:123], v[188:191]\n" \
"v_mul_f32_e32 v184, v55, v184\n" \
"v_mfma_f32_16x16x32_bf16 a[208:211], a[216:219], v[96:99], a[208:211]\n" \
"v_mul_f32_e32 v185, v55, v185\n" \
"v_mul_f32_e32 v186, v55, v186\n" \
"buffer_load_dwordx4 v4, s[80:83], s92 offen offset:0 lds\n" \
"v_mul_f32_e32 v187, v55, v187\n" \
"v_cvt_pk_bf16_f32 v100, v184, v185\n" \
"v_cvt_pk_bf16_f32 v101, v186, v187\n" \
"v_mul_f32_e32 v188, v55, v188\n" \
"v_mul_f32_e32 v189, v55, v189\n" \
"v_mul_f32_e32 v190, v55, v190\n" \
"v_mul_f32_e32 v191, v55, v191\n" \
"v_cvt_pk_bf16_f32 v102, v188, v189\n" \
"v_cvt_pk_bf16_f32 v103, v190, v191\n" \
"v_cmp_lt_f32_e64 s[72:73], s21, v62\n" \
"buffer_load_dwordx4 v5, s[80:83], s92 offen offset:1024 lds\n" \
"v_mfma_f32_16x16x32_bf16 a[208:211], a[216:219], v[100:103], a[208:211]\n" \
"buffer_load_dwordx4 v6, s[80:83], s92 offen offset:2048 lds\n" \
"buffer_load_dwordx4 v7, s[80:83], s92 offen offset:3072 lds\n" \
"s_add_u32 m0, s93, 0x2000\n" \
"s_nop 0\n" \
"buffer_load_dwordx4 v8, s[80:83], s92 offen offset:0 lds\n" \
"s_nop 2\n" \
"v_accvgpr_read_b32 v63, a208\n" \
"v_cmp_u_f32_e32 vcc, v63, v63\n" \
"s_nop 0\n" \
"s_or_b64 vcc, s[72:73], vcc\n" \
"s_cbranch_scc0 L_out_%=\n" \
"s_branch L_end_%=\n" \
".fill (L_ab + 24576 - .) / 4, 4, 0xBF800000\n" \
".fill (L_ab + 24576 - .) / 4, 4, 0xBF800000\n" \
"L_ap6:\n" \
"s_setpc_b64 s[88:89]\n" \
"L_gs7:\n" \
"v_mfma_f32_16x16x32_bf16 v[104:107], a[128:131], a[16:19], v[36:39]\n" \
"s_add_u32 s68, s60, 0xfff6e000\n" \
"s_add_u32 s69, s60, 0xfffb7000\n" \
"s_add_u32 s70, s60, 0x0\n" \
"s_add_u32 s71, s60, 0x49000\n" \
"v_mfma_f32_16x16x32_bf16 v[104:107], a[132:135], a[20:23], v[104:107]\n" \
"s_xor_b32 m0, s65, 0x12000\n" \
"ds_read_b128 a[160:163], v49 offset:0\n" \
"v_mfma_f32_16x16x32_bf16 v[104:107], a[136:139], a[24:27], v[104:107]\n" \
"buffer_load_dwordx4 v50, s[8:11], s64 offen offset:0 sc1 lds\n" \
"v_mfma_f32_16x16x32_bf16 v[104:107], a[140:143], a[28:31], v[104:107]\n" \
"ds_read_b128 a[164:167], v49 offset:1024\n" \
"ds_read_b128 a[168:171], v49 offset:2048\n" \
"v_mfma_f32_16x16x32_bf16 v[108:111], a[156:159], a[28:31], v[36:39]\n" \
"ds_read_b128 a[172:175], v49 offset:3072\n" \
"ds_read_b128 a[176:179], v49 offset:4096\n" \
"v_mfma_f32_16x16x32_bf16 v[108:111], a[152:155], a[24:27], v[108:111]\n" \
"s_mov_b32 s65, s13\n" \
"v_add_u32_e32 v63, s15, v20\n" \
"v_mfma_f32_16x16x32_bf16 v[108:111], a[148:151], a[20:23], v[108:111]\n" \
"buffer_load_dwordx4 v50, s[8:11], s64 offen offset:1024 sc1 lds\n" \
"v_mfma_f32_16x16x32_bf16 v[108:111], a[144:147], a[16:19], v[108:111]\n" \
"s_add_u32 s67, s14, 40960\n" \
"v_add_u32_e32 v50, 0x6000, v63\n" \
"v_mfma_f32_16x16x32_bf16 v[116:119], a[144:147], a[32:35], v[40:43]\n" \
"s_xor_b32 m0, s65, 0x8000\n" \
"ds_read_b128 a[180:183], v49 offset:5120\n" \
"v_mfma_f32_16x16x32_bf16 v[116:119], a[148:151], a[36:39], v[116:119]\n" \
"ds_read_b128 a[184:187], v49 offset:6144\n" \
"v_and_b32_e32 v68, v12, v68\n" \
"v_mfma_f32_16x16x32_bf16 v[116:119], a[152:155], a[40:43], v[116:119]\n" \
"v_and_b32_e32 v69, v13, v69\n" \
"v_and_b32_e32 v70, v14, v70\n" \
"v_mfma_f32_16x16x32_bf16 v[116:119], a[156:159], a[44:47], v[116:119]\n" \
"buffer_load_dwordx4 v50, s[8:11], s67 offen offset:0 sc1 lds\n" \
"v_mfma_f32_16x16x32_bf16 v[112:115], a[140:143], a[44:47], v[40:43]\n" \
"v_and_b32_e32 v71, v15, v71\n" \
"ds_read_b128 a[188:191], v49 offset:7168\n" \
"v_mfma_f32_16x16x32_bf16 v[112:115], a[136:139], a[40:43], v[112:115]\n" \
"v_exp_f32_e32 v86, v86\n" \
"v_mfma_f32_16x16x32_bf16 v[112:115], a[132:135], a[36:39], v[112:115]\n" \
"v_exp_f32_e32 v87, v87\n" \
"v_mfma_f32_16x16x32_bf16 v[112:115], a[128:131], a[32:35], v[112:115]\n" \
"v_exp_f32_e32 v84, v84\n" \
"v_mfma_f32_16x16x32_bf16 v[120:123], a[128:131], a[48:51], v[44:47]\n" \
"buffer_load_dwordx4 v50, s[8:11], s67 offen offset:1024 sc1 lds\n" \
"v_mfma_f32_16x16x32_bf16 v[120:123], a[132:135], a[52:55], v[120:123]\n" \
"v_exp_f32_e32 v85, v85\n" \
"v_mfma_f32_16x16x32_bf16 v[120:123], a[136:139], a[56:59], v[120:123]\n" \
"v_cvt_pk_bf16_f32 v87, v86, v87\n" \
"v_cvt_pk_bf16_f32 v86, v84, v85\n" \
"v_mfma_f32_16x16x32_bf16 v[120:123], a[140:143], a[60:63], v[120:123]\n" \
"v_exp_f32_e32 v82, v82\n" \
"v_mfma_f32_16x16x32_bf16 v[124:127], a[156:159], a[60:63], v[44:47]\n" \
"v_exp_f32_e32 v83, v83\n" \
"v_mfma_f32_16x16x32_bf16 v[124:127], a[152:155], a[56:59], v[124:127]\n" \
"v_exp_f32_e32 v80, v80\n" \
"v_mfma_f32_16x16x32_bf16 v[124:127], a[148:151], a[52:55], v[124:127]\n" \
"buffer_load_dwordx4 v50, s[8:11], s67 offen offset:2048 sc1 lds\n" \
"v_mfma_f32_16x16x32_bf16 v[124:127], a[144:147], a[48:51], v[124:127]\n" \
"v_cvt_pk_bf16_f32 v85, v82, v83\n" \
"s_waitcnt lgkmcnt(1)\n" \
"v_mfma_f32_16x16x32_bf16 v[128:131], a[160:163], v[68:71], v[128:131]\n" \
"v_exp_f32_e32 v81, v81\n" \
"v_mfma_f32_16x16x32_bf16 v[160:163], a[160:163], v[72:75], v[160:163]\n" \
"v_cvt_pk_bf16_f32 v84, v80, v81\n" \
"s_mov_b32 s64, s14\n" \
"v_mfma_f32_16x16x32_bf16 v[132:135], a[164:167], v[68:71], v[132:135]\n" \
"v_exp_f32_e32 v88, v88\n" \
"v_mfma_f32_16x16x32_bf16 v[164:167], a[164:167], v[72:75], v[164:167]\n" \
"v_exp_f32_e32 v89, v89\n" \
"v_mfma_f32_16x16x32_bf16 v[136:139], a[168:171], v[68:71], v[136:139]\n" \
"buffer_load_dwordx4 v50, s[8:11], s67 offen offset:3072 sc1 lds\n" \
"v_mfma_f32_16x16x32_bf16 v[168:171], a[168:171], v[72:75], v[168:171]\n" \
"v_exp_f32_e32 v90, v90\n" \
"v_mfma_f32_16x16x32_bf16 v[140:143], a[172:175], v[68:71], v[140:143]\n" \
"v_exp_f32_e32 v91, v91\n" \
"v_mfma_f32_16x16x32_bf16 v[172:175], a[172:175], v[72:75], v[172:175]\n" \
"v_cvt_pk_bf16_f32 v88, v88, v89\n" \
"v_cvt_pk_bf16_f32 v89, v90, v91\n" \
"v_mfma_f32_16x16x32_bf16 v[144:147], a[176:179], v[68:71], v[144:147]\n" \
"v_exp_f32_e32 v92, v92\n" \
"v_mfma_f32_16x16x32_bf16 v[176:179], a[176:179], v[72:75], v[176:179]\n" \
"v_exp_f32_e32 v93, v93\n" \
"v_mfma_f32_16x16x32_bf16 v[148:151], a[180:183], v[68:71], v[148:151]\n" \
"v_exp_f32_e32 v94, v94\n" \
"v_mfma_f32_16x16x32_bf16 v[180:183], a[180:183], v[72:75], v[180:183]\n" \
"v_exp_f32_e32 v95, v95\n" \
"v_mfma_f32_16x16x32_bf16 v[152:155], a[184:187], v[68:71], v[152:155]\n" \
"v_cvt_pk_bf16_f32 v90, v92, v93\n" \
"v_cvt_pk_bf16_f32 v91, v94, v95\n" \
"v_mfma_f32_16x16x32_bf16 v[184:187], a[184:187], v[72:75], v[184:187]\n" \
"v_add_u32_e32 v51, 0x12000, v63\n" \
"s_waitcnt lgkmcnt(0)\n" \
"v_mfma_f32_16x16x32_bf16 v[156:159], a[188:191], v[68:71], v[156:159]\n" \
"s_xor_b32 m0, s65, 0xa000\n" \
"ds_read_b32 v58, v18 offset:512\n" \
"v_mfma_f32_16x16x32_bf16 v[188:191], a[188:191], v[72:75], v[188:191]\n" \
"buffer_load_dwordx4 v51, s[8:11], s64 offen offset:0 sc1 lds\n" \
"v_smfmac_f32_16x16x64_bf16 a[192:195], a[200:203], v[68:75], v21\n" \
"ds_read_b32 v59, v18 offset:640\n" \
"ds_read_b32 v60, v18 offset:768\n" \
"v_mfma_f32_16x16x32_bf16 v[220:223], a[188:191], v[84:87], v[220:223]\n" \
"ds_read_b32 v61, v18 offset:896\n" \
"ds_read_b128 a[128:131], v48 offset:0\n" \
"v_mfma_f32_16x16x32_bf16 v[252:255], a[188:191], v[88:91], v[252:255]\n" \
"ds_read_b128 a[144:147], v48 offset:4096\n" \
"ds_read_b128 a[132:135], v48 offset:1024\n" \
"v_mfma_f32_16x16x32_bf16 v[216:219], a[184:187], v[84:87], v[216:219]\n" \
"buffer_load_dwordx4 v51, s[8:11], s64 offen offset:1024 sc1 lds\n" \
"v_mfma_f32_16x16x32_bf16 v[248:251], a[184:187], v[88:91], v[248:251]\n" \
"ds_read_b128 a[148:151], v48 offset:5120\n" \
"ds_read_b128 a[136:139], v48 offset:2048\n" \
"v_mfma_f32_16x16x32_bf16 v[212:215], a[180:183], v[84:87], v[212:215]\n" \
"ds_read_b128 a[152:155], v48 offset:6144\n" \
"ds_read_b128 a[140:143], v48 offset:3072\n" \
"v_mfma_f32_16x16x32_bf16 v[244:247], a[180:183], v[88:91], v[244:247]\n" \
"ds_read_b128 a[156:159], v48 offset:7168\n" \
"v_add_u32_e32 v52, 0x14000, v63\n" \
"v_mfma_f32_16x16x32_bf16 v[208:211], a[176:179], v[84:87], v[208:211]\n" \
"buffer_load_dwordx4 v51, s[8:11], s64 offen offset:2048 sc1 lds\n" \
"v_mfma_f32_16x16x32_bf16 v[240:243], a[176:179], v[88:91], v[240:243]\n" \
"v_exp_f32_e32 v104, v104\n" \
"v_mfma_f32_16x16x32_bf16 v[204:207], a[172:175], v[84:87], v[204:207]\n" \
"v_exp_f32_e32 v105, v105\n" \
"v_mfma_f32_16x16x32_bf16 v[236:239], a[172:175], v[88:91], v[236:239]\n" \
"v_exp_f32_e32 v106, v106\n" \
"v_mfma_f32_16x16x32_bf16 v[200:203], a[168:171], v[84:87], v[200:203]\n" \
"buffer_load_dwordx4 v51, s[8:11], s64 offen offset:3072 sc1 lds\n" \
"v_mfma_f32_16x16x32_bf16 v[232:235], a[168:171], v[88:91], v[232:235]\n" \
"v_exp_f32_e32 v107, v107\n" \
"v_mfma_f32_16x16x32_bf16 v[196:199], a[164:167], v[84:87], v[196:199]\n" \
"v_cvt_pk_bf16_f32 v104, v104, v105\n" \
"v_cvt_pk_bf16_f32 v105, v106, v107\n" \
"v_mfma_f32_16x16x32_bf16 v[228:231], a[164:167], v[88:91], v[228:231]\n" \
"v_exp_f32_e32 v108, v108\n" \
"v_mfma_f32_16x16x32_bf16 v[192:195], a[160:163], v[84:87], v[192:195]\n" \
"v_exp_f32_e32 v109, v109\n" \
"v_mfma_f32_16x16x32_bf16 v[224:227], a[160:163], v[88:91], v[224:227]\n" \
"v_exp_f32_e32 v110, v110\n" \
"v_smfmac_f32_16x16x64_bf16 a[196:199], a[200:203], v[84:91], v21\n" \
"v_exp_f32_e32 v111, v111\n" \
"v_cvt_pk_bf16_f32 v106, v108, v109\n" \
"v_cvt_pk_bf16_f32 v107, v110, v111\n" \
"v_and_b32_e32 v104, v12, v104\n" \
"v_and_b32_e32 v105, v13, v105\n" \
"v_and_b32_e32 v106, v14, v106\n" \
"v_and_b32_e32 v107, v15, v107\n" \
"v_accvgpr_read_b32 v54, a192\n" \
"v_accvgpr_read_b32 v22, a193\n" \
"v_readlane_b32 s93, v11, 19\n" \
"v_readlane_b32 s80, v11, 11\n" \
"v_readlane_b32 s81, v11, 12\n" \
"s_mov_b32 s82, 0x47fca0\n" \
"s_mov_b32 s83, 0x20000\n" \
"v_readlane_b32 s92, v11, 26\n" \
"v_exp_f32_e32 v118, v118\n" \
"v_exp_f32_e32 v119, v119\n" \
"v_exp_f32_e32 v116, v116\n" \
"v_exp_f32_e32 v117, v117\n" \
"v_cvt_pk_bf16_f32 v119, v118, v119\n" \
"v_exp_f32_e32 v114, v114\n" \
"v_exp_f32_e32 v115, v115\n" \
"v_exp_f32_e32 v112, v112\n" \
"v_cvt_pk_bf16_f32 v118, v116, v117\n" \
"s_waitcnt vmcnt(10) lgkmcnt(0)\n" \
"s_barrier\n" \
"v_mfma_f32_16x16x32_bf16 v[80:83], a[128:131], a[32:35], v[40:43]\n" \
"ds_read_b128 a[160:163], v49 offset:8192\n" \
"ds_read_b128 a[164:167], v49 offset:9216\n" \
"v_mfma_f32_16x16x32_bf16 v[80:83], a[132:135], a[36:39], v[80:83]\n" \
"ds_read_b128 a[168:171], v49 offset:10240\n" \
"s_xor_b32 m0, s65, 0x18000\n" \
"v_mfma_f32_16x16x32_bf16 v[80:83], a[136:139], a[40:43], v[80:83]\n" \
"buffer_load_dwordx4 v52, s[8:11], s64 offen offset:0 sc1 lds\n" \
"v_mfma_f32_16x16x32_bf16 v[80:83], a[140:143], a[44:47], v[80:83]\n" \
"ds_read_b128 a[172:175], v49 offset:11264\n" \
"ds_read_b128 a[176:179], v49 offset:12288\n" \
"v_mfma_f32_16x16x32_bf16 v[84:87], a[156:159], a[44:47], v[40:43]\n" \
"ds_read_b128 a[180:183], v49 offset:13312\n" \
"ds_read_b128 a[184:187], v49 offset:14336\n" \
"v_mfma_f32_16x16x32_bf16 v[84:87], a[152:155], a[40:43], v[84:87]\n" \
"ds_read_b128 a[188:191], v49 offset:15360\n" \
"v_cvt_pk_bf16_f32 v117, v114, v115\n" \
"v_mfma_f32_16x16x32_bf16 v[84:87], a[148:151], a[36:39], v[84:87]\n" \
"buffer_load_dwordx4 v52, s[8:11], s64 offen offset:1024 sc1 lds\n" \
"v_mfma_f32_16x16x32_bf16 v[84:87], a[144:147], a[32:35], v[84:87]\n" \
"v_exp_f32_e32 v113, v113\n" \
"v_mfma_f32_16x16x32_bf16 v[92:95], a[144:147], a[48:51], v[44:47]\n" \
"v_cvt_pk_bf16_f32 v116, v112, v113\n" \
"ds_read_b128 a[144:147], v48 offset:12288\n" \
"v_mfma_f32_16x16x32_bf16 v[92:95], a[148:151], a[52:55], v[92:95]\n" \
"v_exp_f32_e32 v120, v120\n" \
"v_mfma_f32_16x16x32_bf16 v[92:95], a[152:155], a[56:59], v[92:95]\n" \
"buffer_load_dwordx4 v52, s[8:11], s64 offen offset:2048 sc1 lds\n" \
"v_mfma_f32_16x16x32_bf16 v[92:95], a[156:159], a[60:63], v[92:95]\n" \
"v_exp_f32_e32 v121, v121\n" \
"v_mfma_f32_16x16x32_bf16 v[88:91], a[140:143], a[60:63], v[44:47]\n" \
"v_exp_f32_e32 v122, v122\n" \
"v_mfma_f32_16x16x32_bf16 v[88:91], a[136:139], a[56:59], v[88:91]\n" \
"v_exp_f32_e32 v123, v123\n" \
"v_mfma_f32_16x16x32_bf16 v[88:91], a[132:135], a[52:55], v[88:91]\n" \
"buffer_load_dwordx4 v52, s[8:11], s64 offen offset:3072 sc1 lds\n" \
"v_mfma_f32_16x16x32_bf16 v[88:91], a[128:131], a[48:51], v[88:91]\n" \
"s_add_u32 m0, s93, 0x0\n" \
"v_cvt_pk_bf16_f32 v120, v120, v121\n" \
"v_mfma_f32_16x16x32_bf16 a[192:195], a[212:215], v[104:107], a[192:195]\n" \
"v_cvt_pk_bf16_f32 v121, v122, v123\n" \
"s_waitcnt lgkmcnt(6)\n" \
"v_mfma_f32_16x16x32_bf16 v[160:163], a[160:163], v[104:107], v[160:163]\n" \
"v_exp_f32_e32 v124, v124\n" \
"v_mfma_f32_16x16x32_bf16 v[164:167], a[164:167], v[104:107], v[164:167]\n" \
"buffer_load_dwordx4 v0, s[80:83], s92 offen offset:0 lds\n" \
"v_mfma_f32_16x16x32_bf16 v[168:171], a[168:171], v[104:107], v[168:171]\n" \
"ds_read_b128 a[128:131], v48 offset:8192\n" \
"s_waitcnt lgkmcnt(2)\n" \
"v_mfma_f32_16x16x32_bf16 v[172:175], a[172:175], v[104:107], v[172:175]\n" \
"v_exp_f32_e32 v125, v125\n" \
"v_mfma_f32_16x16x32_bf16 v[176:179], a[176:179], v[104:107], v[176:179]\n" \
"v_exp_f32_e32 v126, v126\n" \
"buffer_load_dwordx4 v1, s[80:83], s92 offen offset:1024 lds\n" \
"v_mfma_f32_16x16x32_bf16 v[180:183], a[180:183], v[104:107], v[180:183]\n" \
"v_exp_f32_e32 v127, v127\n" \
"v_mfma_f32_16x16x32_bf16 v[184:187], a[184:187], v[104:107], v[184:187]\n" \
"v_cvt_pk_bf16_f32 v122, v124, v125\n" \
"v_cvt_pk_bf16_f32 v123, v126, v127\n" \
"v_mfma_f32_16x16x32_bf16 v[188:191], a[188:191], v[104:107], v[188:191]\n" \
"ds_read_b128 a[132:135], v48 offset:9216\n" \
"ds_read_b128 a[148:151], v48 offset:13312\n" \
"buffer_load_dwordx4 v2, s[80:83], s92 offen offset:2048 lds\n" \
"v_mfma_f32_16x16x32_bf16 v[220:223], a[188:191], v[116:119], v[220:223]\n" \
"ds_read_b128 a[136:139], v48 offset:10240\n" \
"ds_read_b128 a[152:155], v48 offset:14336\n" \
"v_mfma_f32_16x16x32_bf16 v[252:255], a[188:191], v[120:123], v[252:255]\n" \
"ds_read_b128 a[140:143], v48 offset:11264\n" \
"ds_read_b128 a[156:159], v48 offset:15360\n" \
"v_mfma_f32_16x16x32_bf16 v[216:219], a[184:187], v[116:119], v[216:219]\n" \
"v_readlane_b32 s96, v11, 1\n" \
"v_bfe_u32 v27, v20, 2, 7\n" \
"buffer_load_dwordx4 v3, s[80:83], s92 offen offset:3072 lds\n" \
"v_mfma_f32_16x16x32_bf16 v[248:251], a[184:187], v[120:123], v[248:251]\n" \
"s_add_u32 m0, s93, 0x1000\n" \
"v_add_f32_e32 v54, v54, v22\n" \
"v_mfma_f32_16x16x32_bf16 v[212:215], a[180:183], v[116:119], v[212:215]\n" \
"v_max_f32_e32 v62, v62, v54\n" \
"v_add_u32_e32 v49, 0x10000, v48\n" \
"v_mfma_f32_16x16x32_bf16 v[244:247], a[180:183], v[120:123], v[244:247]\n" \
"v_rcp_f32_e32 v54, v54\n" \
"buffer_load_dwordx4 v4, s[80:83], s92 offen offset:0 lds\n" \
"v_mfma_f32_16x16x32_bf16 v[208:211], a[176:179], v[116:119], v[208:211]\n" \
"v_mul_f32_e32 v54, v54, v58\n" \
"v_mul_f32_e32 v128, v54, v128\n" \
"v_mfma_f32_16x16x32_bf16 v[240:243], a[176:179], v[120:123], v[240:243]\n" \
"v_mul_f32_e32 v129, v54, v129\n" \
"v_mul_f32_e32 v136, v54, v136\n" \
"v_mfma_f32_16x16x32_bf16 v[204:207], a[172:175], v[116:119], v[204:207]\n" \
"v_mul_f32_e32 v137, v54, v137\n" \
"v_mul_f32_e32 v144, v54, v144\n" \
"buffer_load_dwordx4 v5, s[80:83], s92 offen offset:1024 lds\n" \
"v_mfma_f32_16x16x32_bf16 v[236:239], a[172:175], v[120:123], v[236:239]\n" \
"v_mul_f32_e32 v145, v54, v145\n" \
"v_mul_f32_e32 v152, v54, v152\n" \
"v_mfma_f32_16x16x32_bf16 v[200:203], a[168:171], v[116:119], v[200:203]\n" \
"v_mul_f32_e32 v153, v54, v153\n" \
"v_cvt_pk_bf16_f32 v128, v128, v129\n" \
"v_mfma_f32_16x16x32_bf16 v[232:235], a[168:171], v[120:123], v[232:235]\n" \
"v_mul_f32_e32 v130, v54, v130\n" \
"v_mul_f32_e32 v131, v54, v131\n" \
"buffer_load_dwordx4 v6, s[80:83], s92 offen offset:2048 lds\n" \
"v_mfma_f32_16x16x32_bf16 v[196:199], a[164:167], v[116:119], v[196:199]\n" \
"v_cvt_pk_bf16_f32 v136, v136, v137\n" \
"v_mul_f32_e32 v138, v54, v138\n" \
"v_mfma_f32_16x16x32_bf16 v[228:231], a[164:167], v[120:123], v[228:231]\n" \
"v_mul_f32_e32 v139, v54, v139\n" \
"v_cvt_pk_bf16_f32 v144, v144, v145\n" \
"v_mfma_f32_16x16x32_bf16 v[192:195], a[160:163], v[116:119], v[192:195]\n" \
"v_mul_f32_e32 v146, v54, v146\n" \
"v_mul_f32_e32 v147, v54, v147\n" \
"buffer_load_dwordx4 v7, s[80:83], s92 offen offset:3072 lds\n" \
"v_mfma_f32_16x16x32_bf16 v[224:227], a[160:163], v[120:123], v[224:227]\n" \
"v_cvt_pk_bf16_f32 v152, v152, v153\n" \
"v_mul_f32_e32 v154, v54, v154\n" \
"v_smfmac_f32_16x16x64_bf16 a[196:199], a[200:203], v[116:123], v21\n" \
"v_mul_f32_e32 v155, v54, v155\n" \
"v_cvt_pk_bf16_f32 v129, v130, v131\n" \
"v_mul_f32_e32 v132, v54, v132\n" \
"v_mul_f32_e32 v133, v54, v133\n" \
"v_mul_f32_e32 v134, v54, v134\n" \
"v_mul_f32_e32 v135, v54, v135\n" \
"v_cvt_pk_bf16_f32 v137, v138, v139\n" \
"v_mul_f32_e32 v140, v54, v140\n" \
"v_mul_f32_e32 v141, v54, v141\n" \
"v_mul_f32_e32 v142, v54, v142\n" \
"v_mul_f32_e32 v143, v54, v143\n" \
"v_cvt_pk_bf16_f32 v145, v146, v147\n" \
"v_mul_f32_e32 v148, v54, v148\n" \
"v_mul_f32_e32 v149, v54, v149\n" \
"v_mul_f32_e32 v150, v54, v150\n" \
"v_mul_f32_e32 v151, v54, v151\n" \
"v_cvt_pk_bf16_f32 v153, v154, v155\n" \
"v_mul_f32_e32 v156, v54, v156\n" \
"v_mul_f32_e32 v157, v54, v157\n" \
"v_mul_f32_e32 v158, v54, v158\n" \
"v_mul_f32_e32 v159, v54, v159\n" \
"v_cvt_pk_bf16_f32 v130, v132, v133\n" \
"v_cvt_pk_bf16_f32 v131, v134, v135\n" \
"buffer_store_dwordx4 v[128:131], v20, s[56:59], s68 offen offset:0 sc1\n" \
"v_cvt_pk_bf16_f32 v138, v140, v141\n" \
"v_cvt_pk_bf16_f32 v139, v142, v143\n" \
"v_cvt_pk_bf16_f32 v146, v148, v149\n" \
"v_cvt_pk_bf16_f32 v147, v150, v151\n" \
"v_cvt_pk_bf16_f32 v154, v156, v157\n" \
"v_cvt_pk_bf16_f32 v155, v158, v159\n" \
"s_add_u32 m0, s93, 0x2000\n" \
"s_xor_b32 s65, s65, 0x8000\n" \
"s_add_u32 s64, s64, 0x10000\n" \
"v_exp_f32_e32 v86, v86\n" \
"buffer_store_dwordx4 v[136:139], v20, s[56:59], s68 offen offset:1024 sc1\n" \
"v_exp_f32_e32 v87, v87\n" \
"v_exp_f32_e32 v84, v84\n" \
"v_exp_f32_e32 v85, v85\n" \
"v_exp_f32_e32 v88, v88\n" \
"v_exp_f32_e32 v89, v89\n" \
"v_cvt_pk_bf16_f32 v87, v86, v87\n" \
"buffer_store_dwordx4 v[144:147], v20, s[56:59], s68 offen offset:2048 sc1\n" \
"v_exp_f32_e32 v82, v82\n" \
"v_exp_f32_e32 v83, v83\n" \
"v_exp_f32_e32 v80, v80\n" \
"v_exp_f32_e32 v81, v81\n" \
"v_exp_f32_e32 v90, v90\n" \
"v_exp_f32_e32 v91, v91\n" \
"buffer_store_dwordx4 v[152:155], v20, s[56:59], s68 offen offset:3072 sc1\n" \
"v_cvt_pk_bf16_f32 v86, v84, v85\n" \
"v_cvt_pk_bf16_f32 v88, v88, v89\n" \
"v_exp_f32_e32 v92, v92\n" \
"v_exp_f32_e32 v93, v93\n" \
"v_exp_f32_e32 v94, v94\n" \
"v_exp_f32_e32 v95, v95\n" \
"v_cvt_pk_bf16_f32 v85, v82, v83\n" \
"buffer_load_dwordx4 v8, s[80:83], s92 offen offset:0 lds\n" \
"v_cvt_pk_bf16_f32 v84, v80, v81\n" \
"v_cvt_pk_bf16_f32 v89, v90, v91\n" \
"v_and_b32_e32 v84, v12, v84\n" \
"v_and_b32_e32 v85, v13, v85\n" \
"v_and_b32_e32 v86, v14, v86\n" \
"v_and_b32_e32 v87, v15, v87\n" \
"s_waitcnt vmcnt(17) lgkmcnt(0)\n" \
"s_barrier\n" \
"v_mfma_f32_16x16x32_bf16 v[120:123], a[128:131], a[48:51], v[44:47]\n" \
"buffer_load_dword v26, v27, s[56:59], s96 offen sc1\n" \
"v_mfma_f32_16x16x32_bf16 v[120:123], a[132:135], a[52:55], v[120:123]\n" \
"ds_read_b128 a[188:191], v49 offset:7168\n" \
"ds_read_b128 a[184:187], v49 offset:6144\n" \
"v_mfma_f32_16x16x32_bf16 v[120:123], a[136:139], a[56:59], v[120:123]\n" \
"s_xor_b32 m0, s65, 0x12000\n" \
"ds_read_b128 a[180:183], v49 offset:5120\n" \
"v_mfma_f32_16x16x32_bf16 v[120:123], a[140:143], a[60:63], v[120:123]\n" \
"ds_read_b128 a[176:179], v49 offset:4096\n" \
"ds_read_b128 a[172:175], v49 offset:3072\n" \
"v_mfma_f32_16x16x32_bf16 v[124:127], a[156:159], a[60:63], v[44:47]\n" \
"buffer_load_dwordx4 v50, s[8:11], s64 offen offset:0 sc1 lds\n" \
"v_mfma_f32_16x16x32_bf16 v[124:127], a[152:155], a[56:59], v[124:127]\n" \
"ds_read_b128 a[168:171], v49 offset:2048\n" \
"v_cvt_pk_bf16_f32 v90, v92, v93\n" \
"v_mfma_f32_16x16x32_bf16 v[124:127], a[148:151], a[52:55], v[124:127]\n" \
"v_cvt_pk_bf16_f32 v91, v94, v95\n" \
"ds_read_b128 a[164:167], v49 offset:1024\n" \
"v_mfma_f32_16x16x32_bf16 v[124:127], a[144:147], a[48:51], v[124:127]\n" \
"ds_read_b128 a[160:163], v49 offset:0\n" \
"ds_read_b128 a[0:3], v49 offset:8192\n" \
"v_mfma_f32_16x16x32_bf16 v[32:35], a[220:223], a[64:67], 0\n" \
"buffer_load_dwordx4 v50, s[8:11], s64 offen offset:1024 sc1 lds\n" \
"v_mfma_f32_16x16x32_bf16 v[36:39], a[220:223], a[80:83], 0\n" \
"ds_read_b128 a[4:7], v49 offset:9216\n" \
"ds_read_b128 a[8:11], v49 offset:10240\n" \
"v_mfma_f32_16x16x32_bf16 v[32:35], a[224:227], a[68:71], v[32:35]\n" \
"ds_read_b128 a[12:15], v49 offset:11264\n" \
"ds_read_b128 a[16:19], v49 offset:12288\n" \
"v_mfma_f32_16x16x32_bf16 v[36:39], a[224:227], a[84:87], v[36:39]\n" \
"ds_read_b128 a[20:23], v49 offset:13312\n" \
"ds_read_b128 a[24:27], v49 offset:14336\n" \
"v_mfma_f32_16x16x32_bf16 v[32:35], a[228:231], a[72:75], v[32:35]\n" \
"buffer_load_dwordx4 v50, s[8:11], s64 offen offset:2048 sc1 lds\n" \
"v_mfma_f32_16x16x32_bf16 v[36:39], a[228:231], a[88:91], v[36:39]\n" \
"ds_read_b128 a[28:31], v49 offset:15360\n" \
"v_readlane_b32 s97, v11, 36\n" \
"v_mfma_f32_16x16x32_bf16 v[32:35], a[232:235], a[76:79], v[32:35]\n" \
"v_exp_f32_e32 v120, v120\n" \
"v_mfma_f32_16x16x32_bf16 v[36:39], a[232:235], a[92:95], v[36:39]\n" \
"v_exp_f32_e32 v121, v121\n" \
"v_smfmac_f32_16x16x64_bf16 a[196:199], a[200:203], v[84:91], v21\n" \
"v_cvt_pk_bf16_f32 v120, v120, v121\n" \
"s_waitcnt lgkmcnt(13)\n" \
"v_mfma_f32_16x16x32_bf16 v[220:223], a[188:191], v[84:87], v[220:223]\n" \
"buffer_load_dwordx4 v50, s[8:11], s64 offen offset:3072 sc1 lds\n" \
"v_mfma_f32_16x16x32_bf16 v[252:255], a[188:191], v[88:91], v[252:255]\n" \
"v_exp_f32_e32 v122, v122\n" \
"v_mfma_f32_16x16x32_bf16 v[216:219], a[184:187], v[84:87], v[216:219]\n" \
"v_exp_f32_e32 v123, v123\n" \
"v_mfma_f32_16x16x32_bf16 v[248:251], a[184:187], v[88:91], v[248:251]\n" \
"v_exp_f32_e32 v124, v124\n" \
"v_mfma_f32_16x16x32_bf16 v[212:215], a[180:183], v[84:87], v[212:215]\n" \
"v_exp_f32_e32 v125, v125\n" \
"v_mfma_f32_16x16x32_bf16 v[244:247], a[180:183], v[88:91], v[244:247]\n" \
"v_cvt_pk_bf16_f32 v121, v122, v123\n" \
"s_waitcnt lgkmcnt(7)\n" \
"v_mfma_f32_16x16x32_bf16 v[208:211], a[176:179], v[84:87], v[208:211]\n" \
"v_exp_f32_e32 v126, v126\n" \
"v_mfma_f32_16x16x32_bf16 v[240:243], a[176:179], v[88:91], v[240:243]\n" \
"v_exp_f32_e32 v127, v127\n" \
"v_mfma_f32_16x16x32_bf16 v[204:207], a[172:175], v[84:87], v[204:207]\n" \
"v_cvt_pk_bf16_f32 v122, v124, v125\n" \
"v_cvt_pk_bf16_f32 v123, v126, v127\n" \
"v_mfma_f32_16x16x32_bf16 v[236:239], a[172:175], v[88:91], v[236:239]\n" \
"v_and_b32_e32 v120, v12, v120\n" \
"v_and_b32_e32 v121, v13, v121\n" \
"v_mfma_f32_16x16x32_bf16 v[200:203], a[168:171], v[84:87], v[200:203]\n" \
"v_and_b32_e32 v122, v14, v122\n" \
"v_and_b32_e32 v123, v15, v123\n" \
"v_mfma_f32_16x16x32_bf16 v[232:235], a[168:171], v[88:91], v[232:235]\n" \
"v_accvgpr_read_b32 v56, a196\n" \
"v_accvgpr_read_b32 v24, a197\n" \
"v_mfma_f32_16x16x32_bf16 v[196:199], a[164:167], v[84:87], v[196:199]\n" \
"v_add_u32_e32 v17, 0x49000, v20\n" \
"v_readlane_b32 s98, v11, 37\n" \
"v_mfma_f32_16x16x32_bf16 v[228:231], a[164:167], v[88:91], v[228:231]\n" \
"v_add_u32_e32 v48, s16, v20\n" \
"ds_read_b128 a[128:131], v48 offset:0\n" \
"v_mfma_f32_16x16x32_bf16 v[192:195], a[160:163], v[84:87], v[192:195]\n" \
"ds_read_b128 a[144:147], v48 offset:4096\n" \
"ds_read_b128 a[132:135], v48 offset:1024\n" \
"v_mfma_f32_16x16x32_bf16 v[224:227], a[160:163], v[88:91], v[224:227]\n" \
"ds_read_b128 a[148:151], v48 offset:5120\n" \
"ds_read_b128 a[136:139], v48 offset:2048\n" \
"v_mfma_f32_16x16x32_bf16 a[196:199], a[212:215], v[120:123], a[196:199]\n" \
"ds_read_b128 a[152:155], v48 offset:6144\n" \
"ds_read_b128 a[140:143], v48 offset:3072\n" \
"v_mfma_f32_16x16x32_bf16 v[224:227], a[0:3], v[120:123], v[224:227]\n" \
"ds_read_b128 a[156:159], v48 offset:7168\n" \
"s_waitcnt lgkmcnt(8)\n" \
"v_mfma_f32_16x16x32_bf16 v[228:231], a[4:7], v[120:123], v[228:231]\n" \
"v_accvgpr_read_b32 v55, a194\n" \
"v_accvgpr_read_b32 v23, a195\n" \
"v_mfma_f32_16x16x32_bf16 a[192:195], a[216:219], a[216:219], 0\n" \
"v_add_f32_e32 v55, v55, v23\n" \
"v_max_f32_e32 v62, v62, v55\n" \
"v_mfma_f32_16x16x32_bf16 v[232:235], a[8:11], v[120:123], v[232:235]\n" \
"v_rcp_f32_e32 v55, v55\n" \
"v_mfma_f32_16x16x32_bf16 v[236:239], a[12:15], v[120:123], v[236:239]\n" \
"v_mul_f32_e32 v55, v55, v59\n" \
"v_mul_f32_e32 v160, v55, v160\n" \
"v_mfma_f32_16x16x32_bf16 v[240:243], a[16:19], v[120:123], v[240:243]\n" \
"v_mul_f32_e32 v161, v55, v161\n" \
"v_mul_f32_e32 v168, v55, v168\n" \
"v_mfma_f32_16x16x32_bf16 v[244:247], a[20:23], v[120:123], v[244:247]\n" \
"v_mul_f32_e32 v169, v55, v169\n" \
"v_mul_f32_e32 v176, v55, v176\n" \
"v_mfma_f32_16x16x32_bf16 v[248:251], a[24:27], v[120:123], v[248:251]\n" \
"v_mul_f32_e32 v177, v55, v177\n" \
"v_mul_f32_e32 v184, v55, v184\n" \
"v_mfma_f32_16x16x32_bf16 v[252:255], a[28:31], v[120:123], v[252:255]\n" \
"v_mul_f32_e32 v185, v55, v185\n" \
"v_cvt_pk_bf16_f32 v160, v160, v161\n" \
"v_mul_f32_e32 v162, v55, v162\n" \
"v_mul_f32_e32 v163, v55, v163\n" \
"v_cvt_pk_bf16_f32 v168, v168, v169\n" \
"v_mul_f32_e32 v170, v55, v170\n" \
"v_mul_f32_e32 v171, v55, v171\n" \
"v_cvt_pk_bf16_f32 v176, v176, v177\n" \
"v_mul_f32_e32 v178, v55, v178\n" \
"s_waitcnt vmcnt(4)\n" \
"v_cmp_ne_u32_e32 vcc, s62, v26\n" \
"v_mul_f32_e32 v179, v55, v179\n" \
"v_cvt_pk_bf16_f32 v184, v184, v185\n" \
"v_mul_f32_e32 v186, v55, v186\n" \
"v_mul_f32_e32 v187, v55, v187\n" \
"v_cvt_pk_bf16_f32 v161, v162, v163\n" \
"s_cbranch_vccnz L_sos_%=\n" \
"L_ros_%=:\n" \
"buffer_load_dwordx4 a[220:223], v20, s[56:59], s97 offen offset:0 sc1\n" \
"v_mul_f32_e32 v164, v55, v164\n" \
"v_mul_f32_e32 v165, v55, v165\n" \
"v_mul_f32_e32 v166, v55, v166\n" \
"v_mul_f32_e32 v167, v55, v167\n" \
"v_cvt_pk_bf16_f32 v169, v170, v171\n" \
"v_mul_f32_e32 v172, v55, v172\n" \
"v_mul_f32_e32 v173, v55, v173\n" \
"v_mul_f32_e32 v174, v55, v174\n" \
"v_mul_f32_e32 v175, v55, v175\n" \
"v_cvt_pk_bf16_f32 v177, v178, v179\n" \
"v_mul_f32_e32 v180, v55, v180\n" \
"buffer_load_dwordx4 a[228:231], v17, s[56:59], s97 offen offset:0 sc1\n" \
"v_mul_f32_e32 v181, v55, v181\n" \
"v_mul_f32_e32 v182, v55, v182\n" \
"v_mul_f32_e32 v183, v55, v183\n" \
"v_cvt_pk_bf16_f32 v185, v186, v187\n" \
"v_mul_f32_e32 v188, v55, v188\n" \
"v_mul_f32_e32 v189, v55, v189\n" \
"v_mul_f32_e32 v190, v55, v190\n" \
"v_mul_f32_e32 v191, v55, v191\n" \
"v_cvt_pk_bf16_f32 v162, v164, v165\n" \
"v_cvt_pk_bf16_f32 v163, v166, v167\n" \
"v_cvt_pk_bf16_f32 v170, v172, v173\n" \
"buffer_store_dwordx4 v[160:163], v20, s[56:59], s69 offen offset:0 sc1\n" \
"v_cvt_pk_bf16_f32 v171, v174, v175\n" \
"v_cvt_pk_bf16_f32 v178, v180, v181\n" \
"v_cvt_pk_bf16_f32 v179, v182, v183\n" \
"v_cvt_pk_bf16_f32 v186, v188, v189\n" \
"v_cvt_pk_bf16_f32 v187, v190, v191\n" \
"v_readlane_b32 s93, v11, 20\n" \
"v_readlane_b32 s84, v11, 15\n" \
"v_readlane_b32 s85, v11, 16\n" \
"v_readlane_b32 s86, v11, 17\n" \
"s_mov_b32 s87, 0x20000\n" \
"v_readlane_b32 s92, v11, 18\n" \
"buffer_store_dwordx4 v[168:171], v20, s[56:59], s69 offen offset:1024 sc1\n" \
"v_readlane_b32 s91, v11, 21\n" \
"v_mfma_f32_16x16x32_bf16 a[208:211], a[216:219], v[128:131], a[208:211]\n" \
"v_mfma_f32_16x16x32_bf16 a[208:211], a[216:219], v[136:139], a[208:211]\n" \
"v_mfma_f32_32x32x16_bf16 v[128:143], a[216:219], a[216:219], 0\n" \
"buffer_store_dwordx4 v[176:179], v20, s[56:59], s69 offen offset:2048 sc1\n" \
"v_mfma_f32_16x16x32_bf16 a[208:211], a[216:219], v[144:147], a[208:211]\n" \
"v_mfma_f32_16x16x32_bf16 a[208:211], a[216:219], v[152:155], a[208:211]\n" \
"v_mfma_f32_32x32x16_bf16 v[144:159], a[216:219], a[216:219], 0\n" \
"buffer_store_dwordx4 v[184:187], v20, s[56:59], s69 offen offset:3072 sc1\n" \
"v_add_f32_e32 v56, v56, v24\n" \
"v_mfma_f32_16x16x32_bf16 a[208:211], a[216:219], v[160:163], a[208:211]\n" \
"v_max_f32_e32 v62, v62, v56\n" \
"v_rcp_f32_e32 v56, v56\n" \
"v_mfma_f32_16x16x32_bf16 a[208:211], a[216:219], v[168:171], a[208:211]\n" \
"v_mfma_f32_32x32x16_bf16 v[160:175], a[216:219], a[216:219], 0\n" \
"buffer_load_dwordx4 a[236:239], v20, s[56:59], s98 offen offset:0 sc1\n" \
"v_mul_f32_e32 v56, v56, v60\n" \
"v_mfma_f32_16x16x32_bf16 a[208:211], a[216:219], v[176:179], a[208:211]\n" \
"v_mul_f32_e32 v192, v56, v192\n" \
"v_mul_f32_e32 v193, v56, v193\n" \
"v_cvt_pk_bf16_f32 v192, v192, v193\n" \
"v_mul_f32_e32 v194, v56, v194\n" \
"v_mul_f32_e32 v195, v56, v195\n" \
"buffer_load_dwordx4 a[244:247], v17, s[56:59], s98 offen offset:0 sc1\n" \
"v_mfma_f32_16x16x32_bf16 a[208:211], a[216:219], v[184:187], a[208:211]\n" \
"v_mfma_f32_32x32x16_bf16 v[176:191], a[216:219], a[216:219], 0\n" \
"v_cvt_pk_bf16_f32 v193, v194, v195\n" \
"v_mul_f32_e32 v196, v56, v196\n" \
"v_mul_f32_e32 v197, v56, v197\n" \
"v_mul_f32_e32 v198, v56, v198\n" \
"v_mul_f32_e32 v199, v56, v199\n" \
"buffer_load_dwordx4 a[224:227], v20, s[56:59], s97 offen offset:1024 sc1\n" \
"v_mul_f32_e32 v200, v56, v200\n" \
"v_mul_f32_e32 v201, v56, v201\n" \
"v_accvgpr_read_b32 v57, a198\n" \
"v_accvgpr_read_b32 v25, a199\n" \
"v_mfma_f32_16x16x32_bf16 a[196:199], a[216:219], a[216:219], 0\n" \
"v_cvt_pk_bf16_f32 v194, v196, v197\n" \
"v_cvt_pk_bf16_f32 v195, v198, v199\n" \
"v_cvt_pk_bf16_f32 v200, v200, v201\n" \
"v_mul_f32_e32 v202, v56, v202\n" \
"v_mul_f32_e32 v203, v56, v203\n" \
"buffer_store_dwordx4 v[192:195], v20, s[56:59], s70 offen offset:0 sc1\n" \
"v_add_f32_e32 v57, v57, v25\n" \
"v_mfma_f32_16x16x32_bf16 a[208:211], a[216:219], v[192:195], a[208:211]\n" \
"v_cvt_pk_bf16_f32 v201, v202, v203\n" \
"v_mul_f32_e32 v204, v56, v204\n" \
"v_mul_f32_e32 v205, v56, v205\n" \
"v_mul_f32_e32 v206, v56, v206\n" \
"v_mul_f32_e32 v207, v56, v207\n" \
"v_mul_f32_e32 v208, v56, v208\n" \
"v_mul_f32_e32 v209, v56, v209\n" \
"v_max_f32_e32 v62, v62, v57\n" \
"buffer_load_dwordx4 a[232:235], v17, s[56:59], s97 offen offset:1024 sc1\n" \
"s_waitcnt vmcnt(16) lgkmcnt(0)\n" \
"s_barrier\n" \
"v_mfma_f32_16x16x32_bf16 v[64:67], a[128:131], a[64:67], v[32:35]\n" \
"s_add_u32 m0, s93, 0x0\n" \
"v_cvt_pk_bf16_f32 v202, v204, v205\n" \
"v_mfma_f32_16x16x32_bf16 v[64:67], a[132:135], a[68:71], v[64:67]\n" \
"buffer_load_dwordx4 a[240:243], v20, s[56:59], s98 offen offset:1024 sc1\n" \
"v_mfma_f32_16x16x32_bf16 v[64:67], a[136:139], a[72:75], v[64:67]\n" \
"v_cvt_pk_bf16_f32 v203, v206, v207\n" \
"v_cvt_pk_bf16_f32 v208, v208, v209\n" \
"v_mfma_f32_16x16x32_bf16 v[64:67], a[140:143], a[76:79], v[64:67]\n" \
"v_mul_f32_e32 v210, v56, v210\n" \
"v_mul_f32_e32 v211, v56, v211\n" \
"v_mfma_f32_16x16x32_bf16 v[68:71], a[156:159], a[76:79], v[32:35]\n" \
"v_rcp_f32_e32 v57, v57\n" \
"buffer_store_dwordx4 v[200:203], v20, s[56:59], s70 offen offset:1024 sc1\n" \
"v_mfma_f32_16x16x32_bf16 v[68:71], a[152:155], a[72:75], v[68:71]\n" \
"v_mfma_f32_16x16x32_bf16 a[208:211], a[216:219], v[200:203], a[208:211]\n" \
"v_mfma_f32_32x32x16_bf16 v[192:207], a[216:219], a[216:219], 0\n" \
"v_cvt_pk_bf16_f32 v209, v210, v211\n" \
"buffer_load_dwordx4 a[248:251], v17, s[56:59], s98 offen offset:1024 sc1\n" \
"v_mul_f32_e32 v212, v56, v212\n" \
"v_mul_f32_e32 v213, v56, v213\n" \
"v_mul_f32_e32 v214, v56, v214\n" \
"v_mfma_f32_16x16x32_bf16 v[68:71], a[148:151], a[68:71], v[68:71]\n" \
"v_mul_f32_e32 v215, v56, v215\n" \
"v_mul_f32_e32 v216, v56, v216\n" \
"v_mfma_f32_16x16x32_bf16 v[68:71], a[144:147], a[64:67], v[68:71]\n" \
"v_mul_f32_e32 v217, v56, v217\n" \
"v_cvt_pk_bf16_f32 v210, v212, v213\n" \
"buffer_load_dwordx4 v0, s[84:87], s92 offen offset:0 lds\n" \
"v_mfma_f32_16x16x32_bf16 v[76:79], a[144:147], a[80:83], v[36:39]\n" \
"ds_read_b128 a[144:147], v48 offset:12288\n" \
"v_cvt_pk_bf16_f32 v211, v214, v215\n" \
"v_mfma_f32_16x16x32_bf16 v[76:79], a[148:151], a[84:87], v[76:79]\n" \
"ds_read_b128 a[148:151], v48 offset:13312\n" \
"v_cvt_pk_bf16_f32 v216, v216, v217\n" \
"v_mfma_f32_16x16x32_bf16 v[76:79], a[152:155], a[88:91], v[76:79]\n" \
"ds_read_b128 a[152:155], v48 offset:14336\n" \
"v_mul_f32_e32 v218, v56, v218\n" \
"buffer_store_dwordx4 v[208:211], v20, s[56:59], s70 offen offset:2048 sc1\n" \
"v_mfma_f32_16x16x32_bf16 v[76:79], a[156:159], a[92:95], v[76:79]\n" \
"ds_read_b128 a[156:159], v48 offset:15360\n" \
"v_mul_f32_e32 v219, v56, v219\n" \
"v_mfma_f32_16x16x32_bf16 v[72:75], a[140:143], a[92:95], v[36:39]\n" \
"ds_read_b128 a[140:143], v48 offset:11264\n" \
"v_mul_f32_e32 v57, v57, v61\n" \
"v_mfma_f32_16x16x32_bf16 v[72:75], a[136:139], a[88:91], v[72:75]\n" \
".fill (L_ab + 28672 - .) / 4, 4, 0xBF800000\n" \
"L_ap7:\n" \
"s_cbranch_execz L_ap0\n" \
"ds_read_b128 a[136:139], v48 offset:10240\n" \
"v_cvt_pk_bf16_f32 v217, v218, v219\n" \
"buffer_load_dwordx4 v1, s[84:87], s92 offen offset:1024 lds\n" \
"v_mfma_f32_16x16x32_bf16 v[72:75], a[132:135], a[84:87], v[72:75]\n" \
"ds_read_b128 a[132:135], v48 offset:9216\n" \
"v_mul_f32_e32 v220, v56, v220\n" \
"v_mfma_f32_16x16x32_bf16 v[72:75], a[128:131], a[80:83], v[72:75]\n" \
"ds_read_b128 a[128:131], v48 offset:8192\n" \
"v_mfma_f32_16x16x32_bf16 a[208:211], a[216:219], v[208:211], a[208:211]\n" \
"v_mul_f32_e32 v221, v56, v221\n" \
"buffer_load_dwordx4 v2, s[84:87], s92 offen offset:2048 lds\n" \
"v_mul_f32_e32 v222, v56, v222\n" \
"v_mul_f32_e32 v223, v56, v223\n" \
"v_mul_f32_e32 v224, v57, v224\n" \
"v_mul_f32_e32 v225, v57, v225\n" \
"v_cvt_pk_bf16_f32 v218, v220, v221\n" \
"v_cvt_pk_bf16_f32 v219, v222, v223\n" \
"v_cvt_pk_bf16_f32 v224, v224, v225\n" \
"v_mul_f32_e32 v226, v57, v226\n" \
"v_mul_f32_e32 v227, v57, v227\n" \
"v_mfma_f32_16x16x32_bf16 a[208:211], a[216:219], v[216:219], a[208:211]\n" \
"buffer_store_dwordx4 v[216:219], v20, s[56:59], s70 offen offset:3072 sc1\n" \
"v_cvt_pk_bf16_f32 v225, v226, v227\n" \
"v_mul_f32_e32 v228, v57, v228\n" \
"v_mfma_f32_32x32x16_bf16 v[208:223], a[216:219], a[216:219], 0\n" \
"v_mul_f32_e32 v229, v57, v229\n" \
"v_mul_f32_e32 v230, v57, v230\n" \
"v_mul_f32_e32 v231, v57, v231\n" \
"v_mul_f32_e32 v232, v57, v232\n" \
"v_mul_f32_e32 v233, v57, v233\n" \
"v_cvt_pk_bf16_f32 v226, v228, v229\n" \
"v_cvt_pk_bf16_f32 v227, v230, v231\n" \
"buffer_store_dwordx4 v[224:227], v20, s[56:59], s71 offen offset:0 sc1\n" \
"v_cvt_pk_bf16_f32 v232, v232, v233\n" \
"v_mul_f32_e32 v234, v57, v234\n" \
"v_mul_f32_e32 v235, v57, v235\n" \
"v_mfma_f32_16x16x32_bf16 a[208:211], a[216:219], v[224:227], a[208:211]\n" \
"v_cvt_pk_bf16_f32 v233, v234, v235\n" \
"v_mul_f32_e32 v236, v57, v236\n" \
"v_mul_f32_e32 v237, v57, v237\n" \
"v_mul_f32_e32 v238, v57, v238\n" \
"v_mul_f32_e32 v239, v57, v239\n" \
"v_mul_f32_e32 v240, v57, v240\n" \
"buffer_load_dwordx4 v3, s[84:87], s92 offen offset:3072 lds\n" \
"s_add_u32 m0, s91, 0x1000\n" \
"v_mul_f32_e32 v241, v57, v241\n" \
"v_cvt_pk_bf16_f32 v234, v236, v237\n" \
"v_cvt_pk_bf16_f32 v235, v238, v239\n" \
"v_cvt_pk_bf16_f32 v240, v240, v241\n" \
"v_mul_f32_e32 v242, v57, v242\n" \
"v_mul_f32_e32 v243, v57, v243\n" \
"v_mfma_f32_16x16x32_bf16 a[208:211], a[216:219], v[232:235], a[208:211]\n" \
"v_cvt_pk_bf16_f32 v241, v242, v243\n" \
"v_mul_f32_e32 v244, v57, v244\n" \
"buffer_store_dwordx4 v[232:235], v20, s[56:59], s71 offen offset:1024 sc1\n" \
"v_mul_f32_e32 v245, v57, v245\n" \
"v_mul_f32_e32 v246, v57, v246\n" \
"v_mfma_f32_32x32x16_bf16 v[224:239], a[216:219], a[216:219], 0\n" \
"v_mul_f32_e32 v247, v57, v247\n" \
"v_mul_f32_e32 v248, v57, v248\n" \
"v_mul_f32_e32 v249, v57, v249\n" \
"v_cvt_pk_bf16_f32 v242, v244, v245\n" \
"v_cvt_pk_bf16_f32 v243, v246, v247\n" \
"v_cvt_pk_bf16_f32 v248, v248, v249\n" \
"v_mul_f32_e32 v250, v57, v250\n" \
"buffer_store_dwordx4 v[240:243], v20, s[56:59], s71 offen offset:2048 sc1\n" \
"v_mul_f32_e32 v251, v57, v251\n" \
"v_mfma_f32_16x16x32_bf16 a[208:211], a[216:219], v[240:243], a[208:211]\n" \
"v_cvt_pk_bf16_f32 v249, v250, v251\n" \
"v_mul_f32_e32 v252, v57, v252\n" \
"v_mul_f32_e32 v253, v57, v253\n" \
"v_mul_f32_e32 v254, v57, v254\n" \
"v_mul_f32_e32 v255, v57, v255\n" \
"v_cvt_pk_bf16_f32 v250, v252, v253\n" \
"v_cvt_pk_bf16_f32 v251, v254, v255\n" \
"v_add_u32_e32 v53, 0x20000, v63\n" \
"buffer_store_dwordx4 v[248:251], v20, s[56:59], s71 offen offset:3072 sc1\n" \
"v_mfma_f32_16x16x32_bf16 a[208:211], a[216:219], v[248:251], a[208:211]\n" \
"v_exp_f32_e32 v70, v70\n" \
"v_mfma_f32_32x32x16_bf16 v[240:255], a[216:219], a[216:219], 0\n" \
"v_exp_f32_e32 v71, v71\n" \
"v_exp_f32_e32 v72, v72\n" \
"v_exp_f32_e32 v73, v73\n" \
"buffer_load_dwordx4 v4, s[84:87], s92 offen offset:0 lds\n" \
"s_add_u32 s64, s64, 0x10000\n" \
"v_cmp_lt_f32_e64 s[72:73], s21, v62\n" \
"v_accvgpr_read_b32 v63, a208\n" \
"v_cmp_u_f32_e32 vcc, v63, v63\n" \
"v_readlane_b32 s95, v11, 3\n" \
"v_exp_f32_e32 v68, v68\n" \
"v_exp_f32_e32 v69, v69\n" \
"v_exp_f32_e32 v74, v74\n" \
"buffer_load_dwordx4 v5, s[84:87], s92 offen offset:1024 lds\n" \
"v_exp_f32_e32 v75, v75\n" \
"s_or_b64 vcc, s[72:73], vcc\n" \
"s_cselect_b32 s95, 0x40000000, s95\n" \
"s_cselect_b32 s94, 0x7f800000, 0\n" \
"v_cvt_pk_bf16_f32 v71, v70, v71\n" \
"v_exp_f32_e32 v66, v66\n" \
"v_exp_f32_e32 v67, v67\n" \
"v_exp_f32_e32 v64, v64\n" \
"buffer_load_dwordx4 v6, s[84:87], s92 offen offset:2048 lds\n" \
"v_exp_f32_e32 v65, v65\n" \
"v_cvt_pk_bf16_f32 v72, v72, v73\n" \
"v_exp_f32_e32 v76, v76\n" \
"v_exp_f32_e32 v77, v77\n" \
"v_exp_f32_e32 v78, v78\n" \
"v_exp_f32_e32 v79, v79\n" \
"buffer_load_dwordx4 v7, s[84:87], s92 offen offset:3072 lds\n" \
"s_add_u32 m0, s91, 0x2000\n" \
"v_cvt_pk_bf16_f32 v70, v68, v69\n" \
"v_cvt_pk_bf16_f32 v73, v74, v75\n" \
"v_add_u32_e32 v49, 0x10000, v48\n" \
"s_xor_b32 s65, s65, 0x8000\n" \
"v_cvt_pk_bf16_f32 v69, v66, v67\n" \
"v_cvt_pk_bf16_f32 v68, v64, v65\n" \
"v_cvt_pk_bf16_f32 v74, v76, v77\n" \
"v_cvt_pk_bf16_f32 v75, v78, v79\n" \
"v_xor_b32_e32 v48, 0x8000, v48\n" \
"s_sub_u32 s66, s19, 2\n" \
"buffer_load_dwordx4 v8, s[84:87], s92 offen offset:0 lds\n" \
"s_waitcnt vmcnt(18) lgkmcnt(0)\n" \
"s_barrier\n" \
"s_branch L_f1_%=\n" \
".fill (L_ab + 32768 - .) / 4, 4, 0xBF800000\n" \
".fill (L_ab + 32768 - .) / 4, 4, 0xBF800000\n" \
"L_ap8:\n" \
"s_setpc_b64 s[88:89]\n" \
"L_out_%=:\n" \
"v_bfe_u32 v38, v20, 2, 4\n" \
"v_readlane_b32 s64, v11, 2\n" \
"v_add_u32_e32 v32, 0x49000, v20\n" \
"v_add_u32_e32 v34, 0x20000, v10\n" \
"ds_read_b64_tr_b16 a[0:1], v34 offset:0\n" \
"ds_read_b64_tr_b16 a[2:3], v34 offset:4608\n" \
"ds_read_b64_tr_b16 a[4:5], v34 offset:32\n" \
"buffer_load_dword v26, v38, s[56:59], s64 offen sc1\n" \
"ds_read_b64_tr_b16 a[6:7], v34 offset:4640\n" \
"ds_read_b64_tr_b16 a[8:9], v34 offset:64\n" \
"ds_read_b64_tr_b16 a[10:11], v34 offset:4672\n" \
"ds_read_b64_tr_b16 a[12:13], v34 offset:96\n" \
"buffer_store_dwordx4 v[64:67], v20, s[56:59], s61 offen offset:0 sc1\n" \
"ds_read_b64_tr_b16 a[14:15], v34 offset:4704\n" \
"v_mov_b32_e32 v39, s62\n" \
"v_readlane_b32 s70, v11, 5\n" \
"v_readlane_b32 s71, v11, 4\n" \
"buffer_store_dwordx4 v[68:71], v20, s[56:59], s61 offen offset:1024 sc1\n" \
"ds_read_b64_tr_b16 a[16:17], v34 offset:128\n" \
"ds_read_b64_tr_b16 a[18:19], v34 offset:4736\n" \
"ds_read_b64_tr_b16 a[20:21], v34 offset:160\n" \
"ds_read_b64_tr_b16 a[22:23], v34 offset:4768\n" \
"buffer_store_dwordx4 v[72:75], v20, s[56:59], s61 offen offset:2048 sc1\n" \
"ds_read_b64_tr_b16 a[24:25], v34 offset:192\n" \
"ds_read_b64_tr_b16 a[26:27], v34 offset:4800\n" \
"ds_read_b64_tr_b16 a[28:29], v34 offset:224\n" \
"ds_read_b64_tr_b16 a[30:31], v34 offset:4832\n" \
"buffer_store_dwordx4 v[76:79], v20, s[56:59], s61 offen offset:3072 sc1\n" \
"ds_read_b64_tr_b16 a[192:193], v34 offset:256\n" \
"ds_read_b64_tr_b16 a[194:195], v34 offset:4864\n" \
"ds_read_b64_tr_b16 a[196:197], v34 offset:9216\n" \
"ds_read_b64_tr_b16 a[198:199], v34 offset:13824\n" \
"buffer_store_dwordx4 v[80:83], v32, s[56:59], s61 offen offset:0 sc1\n" \
"ds_read_b64_tr_b16 a[200:201], v34 offset:9248\n" \
"ds_read_b64_tr_b16 a[202:203], v34 offset:13856\n" \
"ds_read_b64_tr_b16 a[204:205], v34 offset:9280\n" \
"ds_read_b64_tr_b16 a[206:207], v34 offset:13888\n" \
"buffer_store_dwordx4 v[84:87], v32, s[56:59], s61 offen offset:1024 sc1\n" \
"ds_read_b64_tr_b16 a[208:209], v34 offset:9312\n" \
"s_waitcnt vmcnt(16) lgkmcnt(15)\n" \
"v_mfma_f32_16x16x32_bf16 v[112:115], a[0:3], a[96:99], 0\n" \
"buffer_store_dwordx4 v[96:99], v32, s[56:59], s61 offen offset:2048 sc1\n" \
"v_mfma_f32_16x16x32_bf16 v[116:119], a[0:3], a[112:115], 0\n" \
"ds_read_b64_tr_b16 a[210:211], v34 offset:13920\n" \
"ds_read_b64_tr_b16 a[212:213], v34 offset:9344\n" \
"buffer_store_dwordx4 v[100:103], v32, s[56:59], s61 offen offset:3072 sc1\n" \
"v_mfma_f32_16x16x32_bf16 v[184:187], a[0:3], v[64:67], 0\n" \
"ds_read_b64_tr_b16 a[214:215], v34 offset:13952\n" \
"ds_read_b64_tr_b16 a[216:217], v34 offset:9376\n" \
"v_mfma_f32_16x16x32_bf16 v[188:191], a[0:3], v[80:83], 0\n" \
"ds_read_b64_tr_b16 a[218:219], v34 offset:13984\n" \
"ds_read_b64_tr_b16 a[0:1], v34 offset:9408\n" \
"v_mfma_f32_16x16x32_bf16 v[120:123], a[4:7], a[96:99], 0\n" \
"ds_read_b64_tr_b16 a[2:3], v34 offset:14016\n" \
"v_readlane_b32 s65, v11, 30\n" \
"v_mfma_f32_16x16x32_bf16 v[124:127], a[4:7], a[112:115], 0\n" \
"v_lshrrev_b32_e32 v44, 9, v20\n" \
"v_mul_u32_u24_e32 v44, 0xb40, v44\n" \
"v_mfma_f32_16x16x32_bf16 v[192:195], a[4:7], v[64:67], 0\n" \
"v_add_u32_e32 v35, v44, v10\n" \
"v_readlane_b32 s67, v11, 23\n" \
"v_mfma_f32_16x16x32_bf16 v[196:199], a[4:7], v[80:83], 0\n" \
"ds_read_b64_tr_b16 a[4:5], v34 offset:9440\n" \
"ds_read_b64_tr_b16 a[6:7], v34 offset:14048\n" \
"v_mfma_f32_16x16x32_bf16 v[128:131], a[8:11], a[96:99], 0\n" \
"v_readlane_b32 s80, v11, 11\n" \
"v_readlane_b32 s81, v11, 12\n" \
"v_mfma_f32_16x16x32_bf16 v[132:135], a[8:11], a[112:115], 0\n" \
"s_mov_b32 s82, 0x47fca0\n" \
"s_mov_b32 s83, 0x20000\n" \
"v_mfma_f32_16x16x32_bf16 v[200:203], a[8:11], v[64:67], 0\n" \
"v_readlane_b32 s66, v11, 28\n" \
"s_add_u32 m0, s67, 0x0\n" \
"v_mfma_f32_16x16x32_bf16 v[204:207], a[8:11], v[80:83], 0\n" \
"ds_read_b64_tr_b16 a[8:9], v34 offset:9472\n" \
"ds_read_b64_tr_b16 a[10:11], v34 offset:14080\n" \
"v_mfma_f32_16x16x32_bf16 v[136:139], a[12:15], a[96:99], 0\n" \
"buffer_load_dwordx4 v0, s[80:83], s66 offen offset:0 lds\n" \
"v_mfma_f32_16x16x32_bf16 v[140:143], a[12:15], a[112:115], 0\n" \
"v_readlane_b32 s92, v11, 24\n" \
"v_readlane_b32 s73, v11, 29\n" \
"v_mfma_f32_16x16x32_bf16 v[208:211], a[12:15], v[64:67], 0\n" \
"v_readlane_b32 s94, v11, 25\n" \
"v_readlane_b32 s84, v11, 9\n" \
"v_mfma_f32_16x16x32_bf16 v[212:215], a[12:15], v[80:83], 0\n" \
"ds_read_b64_tr_b16 a[12:13], v34 offset:18432\n" \
"ds_read_b64_tr_b16 a[14:15], v34 offset:23040\n" \
"v_mfma_f32_16x16x32_bf16 v[144:147], a[16:19], a[96:99], 0\n" \
"v_readlane_b32 s85, v11, 10\n" \
"s_mov_b32 s86, 0xa1ca0\n" \
"v_mfma_f32_16x16x32_bf16 v[148:151], a[16:19], a[112:115], 0\n" \
"buffer_load_dwordx4 v1, s[80:83], s66 offen offset:1024 lds\n" \
"v_mfma_f32_16x16x32_bf16 v[216:219], a[16:19], v[64:67], 0\n" \
"s_mov_b32 s87, 0x20000\n" \
"v_readlane_b32 s68, v11, 31\n" \
"v_mfma_f32_16x16x32_bf16 v[220:223], a[16:19], v[80:83], 0\n" \
"ds_read_b64_tr_b16 a[16:17], v34 offset:18464\n" \
"s_waitcnt lgkmcnt(7)\n" \
"v_mfma_f32_16x16x32_bf16 v[152:155], a[20:23], a[96:99], 0\n" \
"ds_read_b64_tr_b16 a[18:19], v34 offset:23072\n" \
"v_add_u32_e32 v33, 0x10000, v10\n" \
"v_mfma_f32_16x16x32_bf16 v[156:159], a[20:23], a[112:115], 0\n" \
"v_readlane_b32 s69, v11, 32\n" \
"v_readlane_b32 s72, v11, 34\n" \
"v_mfma_f32_16x16x32_bf16 v[224:227], a[20:23], v[64:67], 0\n" \
"buffer_load_dwordx4 v2, s[80:83], s66 offen offset:2048 lds\n" \
"v_mfma_f32_16x16x32_bf16 v[228:231], a[20:23], v[80:83], 0\n" \
"ds_read_b64_tr_b16 a[20:21], v34 offset:18496\n" \
"ds_read_b64_tr_b16 a[22:23], v34 offset:23104\n" \
"v_mfma_f32_16x16x32_bf16 v[160:163], a[24:27], a[96:99], 0\n" \
"v_readlane_b32 s93, v11, 35\n" \
"v_lshlrev_b32_e32 v41, 12, v20\n" \
"v_mfma_f32_16x16x32_bf16 v[164:167], a[24:27], a[112:115], 0\n" \
"v_readlane_b32 s95, v11, 38\n" \
"v_readlane_b32 s96, v11, 39\n" \
"v_mfma_f32_16x16x32_bf16 v[232:235], a[24:27], v[64:67], 0\n" \
"v_readlane_b32 s63, v11, 41\n" \
"v_bfe_u32 v40, v20, 2, 7\n" \
"v_mfma_f32_16x16x32_bf16 v[236:239], a[24:27], v[80:83], 0\n" \
"ds_read_b64_tr_b16 a[24:25], v34 offset:18528\n" \
"ds_read_b64_tr_b16 a[26:27], v34 offset:23136\n" \
"v_mfma_f32_16x16x32_bf16 v[168:171], a[28:31], a[96:99], 0\n" \
"buffer_load_dwordx4 v3, s[80:83], s66 offen offset:3072 lds\n" \
"v_mfma_f32_16x16x32_bf16 v[172:175], a[28:31], a[112:115], 0\n" \
"s_add_u32 m0, s67, 0x1000\n" \
"s_add_u32 s74, s63, 0x3800\n" \
"v_mfma_f32_16x16x32_bf16 v[240:243], a[28:31], v[64:67], 0\n" \
"v_readlane_b32 s100, v11, 7\n" \
"v_readlane_b32 s97, v11, 40\n" \
"v_mfma_f32_16x16x32_bf16 v[244:247], a[28:31], v[80:83], 0\n" \
"ds_read_b64_tr_b16 a[28:29], v34 offset:18560\n" \
"ds_read_b64_tr_b16 a[30:31], v34 offset:23168\n" \
"v_mfma_f32_16x16x32_bf16 v[176:179], a[192:195], a[96:99], 0\n" \
"v_bfe_u32 v44, v20, 4, 4\n" \
"v_bfe_u32 v45, v20, 8, 2\n" \
"v_mfma_f32_16x16x32_bf16 v[180:183], a[192:195], a[112:115], 0\n" \
"buffer_load_dwordx4 v4, s[80:83], s66 offen offset:0 lds\n" \
"v_mfma_f32_16x16x32_bf16 v[248:251], a[192:195], v[64:67], 0\n" \
"v_mul_u32_u24_e32 v42, 0x480, v44\n" \
"v_lshl_add_u32 v42, v45, 3, v42\n" \
"v_mfma_f32_16x16x32_bf16 v[252:255], a[192:195], v[80:83], 0\n" \
"ds_read_b64_tr_b16 a[192:193], v34 offset:18592\n" \
"ds_read_b64_tr_b16 a[194:195], v34 offset:23200\n" \
"v_mfma_f32_16x16x32_bf16 v[112:115], a[196:199], a[100:103], v[112:115]\n" \
"v_lshrrev_b32_e32 v44, 1, v44\n" \
"v_lshlrev_b32_e32 v45, 1, v45\n" \
"v_mfma_f32_16x16x32_bf16 v[116:119], a[196:199], a[116:119], v[116:119]\n" \
"v_sub_u32_e32 v44, v44, v45\n" \
"v_and_b32_e32 v45, 16, v20\n" \
"v_mfma_f32_16x16x32_bf16 v[184:187], a[196:199], v[68:71], v[184:187]\n" \
"buffer_load_dwordx4 v5, s[80:83], s66 offen offset:1024 lds\n" \
"v_mfma_f32_16x16x32_bf16 v[188:191], a[196:199], v[84:87], v[188:191]\n" \
"ds_read_b64_tr_b16 a[196:197], v34 offset:18624\n" \
"ds_read_b64_tr_b16 a[198:199], v34 offset:23232\n" \
"v_mfma_f32_16x16x32_bf16 v[120:123], a[200:203], a[100:103], v[120:123]\n" \
"v_mov_b32_e32 v46, 0x3f80\n" \
"v_lshlrev_b32_e32 v45, v45, v46\n" \
"v_mfma_f32_16x16x32_bf16 v[124:127], a[200:203], a[116:119], v[124:127]\n" \
"v_mov_b32_e32 v52, 0\n" \
"v_mov_b32_e32 v53, 0\n" \
"v_mfma_f32_16x16x32_bf16 v[192:195], a[200:203], v[68:71], v[192:195]\n" \
"s_waitcnt vmcnt(14)\n" \
"v_cmp_ne_u32_e32 vcc, s62, v26\n" \
"v_mfma_f32_16x16x32_bf16 v[196:199], a[200:203], v[84:87], v[196:199]\n" \
"ds_read_b64_tr_b16 a[200:201], v34 offset:18656\n" \
"ds_read_b64_tr_b16 a[202:203], v34 offset:23264\n" \
"v_mfma_f32_16x16x32_bf16 v[128:131], a[204:207], a[100:103], v[128:131]\n" \
"buffer_load_dwordx4 v6, s[80:83], s66 offen offset:2048 lds\n" \
"v_mfma_f32_16x16x32_bf16 v[132:135], a[204:207], a[116:119], v[132:135]\n" \
"s_cbranch_vccnz L_sa_%=\n" \
"L_ra_%=:\n" \
"v_readlane_b32 s64, v11, 33\n" \
"v_mfma_f32_16x16x32_bf16 v[200:203], a[204:207], v[68:71], v[200:203]\n" \
"v_mov_b32_e32 v48, 0\n" \
"v_mov_b32_e32 v49, 0\n" \
"v_mfma_f32_16x16x32_bf16 v[204:207], a[204:207], v[84:87], v[204:207]\n" \
"ds_read_b64_tr_b16 a[204:205], v34 offset:18688\n" \
"ds_read_b64_tr_b16 a[206:207], v34 offset:23296\n" \
"v_mfma_f32_16x16x32_bf16 v[136:139], a[208:211], a[100:103], v[136:139]\n" \
"v_add_u32_e32 v43, 0x9000, v42\n" \
"v_readlane_b32 s88, v11, 13\n" \
"v_mfma_f32_16x16x32_bf16 v[140:143], a[208:211], a[116:119], v[140:143]\n" \
"buffer_load_dwordx4 a[64:67], v20, s[56:59], s65 offen offset:0 sc1\n" \
"v_mfma_f32_16x16x32_bf16 v[208:211], a[208:211], v[68:71], v[208:211]\n" \
"v_readlane_b32 s89, v11, 14\n" \
"s_mov_b32 s90, 0x240000\n" \
"v_mfma_f32_16x16x32_bf16 v[212:215], a[208:211], v[84:87], v[212:215]\n" \
"s_mov_b32 s91, 0x20000\n" \
"v_mov_b32_e32 v46, s62\n" \
"v_mfma_f32_16x16x32_bf16 v[144:147], a[212:215], a[100:103], v[144:147]\n" \
"v_mfma_f32_16x16x32_bf16 v[148:151], a[212:215], a[116:119], v[148:151]\n" \
"v_mfma_f32_16x16x32_bf16 v[216:219], a[212:215], v[68:71], v[216:219]\n" \
"buffer_load_dwordx4 a[80:83], v32, s[56:59], s65 offen offset:0 sc1\n" \
"v_mfma_f32_16x16x32_bf16 v[220:223], a[212:215], v[84:87], v[220:223]\n" \
"v_mfma_f32_16x16x32_bf16 v[152:155], a[216:219], a[100:103], v[152:155]\n" \
"v_mfma_f32_16x16x32_bf16 v[156:159], a[216:219], a[116:119], v[156:159]\n" \
"v_mfma_f32_16x16x32_bf16 v[224:227], a[216:219], v[68:71], v[224:227]\n" \
"v_mfma_f32_16x16x32_bf16 v[228:231], a[216:219], v[84:87], v[228:231]\n" \
"buffer_load_dwordx4 a[68:71], v20, s[56:59], s65 offen offset:1024 sc1\n" \
"v_mfma_f32_16x16x32_bf16 v[160:163], a[0:3], a[100:103], v[160:163]\n" \
"v_mfma_f32_16x16x32_bf16 v[164:167], a[0:3], a[116:119], v[164:167]\n" \
"v_mfma_f32_16x16x32_bf16 v[232:235], a[0:3], v[68:71], v[232:235]\n" \
"v_mfma_f32_16x16x32_bf16 v[236:239], a[0:3], v[84:87], v[236:239]\n" \
"s_waitcnt lgkmcnt(0)\n" \
"v_mfma_f32_16x16x32_bf16 v[168:171], a[4:7], a[100:103], v[168:171]\n" \
"buffer_load_dwordx4 a[84:87], v32, s[56:59], s65 offen offset:1024 sc1\n" \
"v_mfma_f32_16x16x32_bf16 v[172:175], a[4:7], a[116:119], v[172:175]\n" \
"v_mfma_f32_16x16x32_bf16 v[240:243], a[4:7], v[68:71], v[240:243]\n" \
"v_mfma_f32_16x16x32_bf16 v[244:247], a[4:7], v[84:87], v[244:247]\n" \
"v_mfma_f32_16x16x32_bf16 v[176:179], a[8:11], a[100:103], v[176:179]\n" \
"v_mfma_f32_16x16x32_bf16 v[180:183], a[8:11], a[116:119], v[180:183]\n" \
"buffer_load_dwordx4 a[72:75], v20, s[56:59], s65 offen offset:2048 sc1\n" \
"v_mfma_f32_16x16x32_bf16 v[248:251], a[8:11], v[68:71], v[248:251]\n" \
"v_mfma_f32_16x16x32_bf16 v[252:255], a[8:11], v[84:87], v[252:255]\n" \
"v_mfma_f32_16x16x32_bf16 v[112:115], a[12:15], a[104:107], v[112:115]\n" \
"v_mfma_f32_16x16x32_bf16 v[116:119], a[12:15], a[120:123], v[116:119]\n" \
"v_mfma_f32_16x16x32_bf16 v[184:187], a[12:15], v[72:75], v[184:187]\n" \
"buffer_load_dwordx4 a[88:91], v32, s[56:59], s65 offen offset:2048 sc1\n" \
"v_mfma_f32_16x16x32_bf16 v[188:191], a[12:15], v[96:99], v[188:191]\n" \
"v_mfma_f32_16x16x32_bf16 v[120:123], a[16:19], a[104:107], v[120:123]\n" \
"v_mfma_f32_16x16x32_bf16 v[124:127], a[16:19], a[120:123], v[124:127]\n" \
"v_mfma_f32_16x16x32_bf16 v[192:195], a[16:19], v[72:75], v[192:195]\n" \
"v_mfma_f32_16x16x32_bf16 v[196:199], a[16:19], v[96:99], v[196:199]\n" \
"buffer_load_dwordx4 v7, s[80:83], s66 offen offset:3072 lds\n" \
"v_mfma_f32_16x16x32_bf16 v[128:131], a[20:23], a[104:107], v[128:131]\n" \
"s_add_u32 m0, s67, 0x2000\n" \
"v_readlane_b32 s67, v11, 48\n" \
"v_mfma_f32_16x16x32_bf16 v[132:135], a[20:23], a[120:123], v[132:135]\n" \
"v_mfma_f32_16x16x32_bf16 v[200:203], a[20:23], v[72:75], v[200:203]\n" \
"v_mfma_f32_16x16x32_bf16 v[204:207], a[20:23], v[96:99], v[204:207]\n" \
"s_barrier\n" \
"v_mfma_f32_16x16x32_bf16 v[136:139], a[24:27], a[104:107], v[136:139]\n" \
"ds_read_b64_tr_b16 a[208:209], v10 offset:32768\n" \
"ds_read_b64_tr_b16 a[210:211], v10 offset:37376\n" \
"v_mfma_f32_16x16x32_bf16 v[140:143], a[24:27], a[120:123], v[140:143]\n" \
"ds_read_b64_tr_b16 a[212:213], v10 offset:32800\n" \
"ds_read_b64_tr_b16 a[214:215], v10 offset:37408\n" \
"v_mfma_f32_16x16x32_bf16 v[208:211], a[24:27], v[72:75], v[208:211]\n" \
"ds_read_b64_tr_b16 a[216:217], v10 offset:32832\n" \
"ds_read_b64_tr_b16 a[218:219], v10 offset:37440\n" \
"v_mfma_f32_16x16x32_bf16 v[212:215], a[24:27], v[96:99], v[212:215]\n" \
"ds_read_b64_tr_b16 a[0:1], v10 offset:32864\n" \
"ds_read_b64_tr_b16 a[2:3], v10 offset:37472\n" \
"v_mfma_f32_16x16x32_bf16 v[144:147], a[28:31], a[104:107], v[144:147]\n" \
"ds_read_b64_tr_b16 a[4:5], v10 offset:32896\n" \
"ds_read_b64_tr_b16 a[6:7], v10 offset:37504\n" \
"v_mfma_f32_16x16x32_bf16 v[148:151], a[28:31], a[120:123], v[148:151]\n" \
"ds_read_b64_tr_b16 a[8:9], v10 offset:32928\n" \
"ds_read_b64_tr_b16 a[10:11], v10 offset:37536\n" \
"v_mfma_f32_16x16x32_bf16 v[216:219], a[28:31], v[72:75], v[216:219]\n" \
"ds_read_b64_tr_b16 a[12:13], v10 offset:32960\n" \
"ds_read_b64_tr_b16 a[14:15], v10 offset:37568\n" \
"v_mfma_f32_16x16x32_bf16 v[220:223], a[28:31], v[96:99], v[220:223]\n" \
"ds_read_b64_tr_b16 a[16:17], v10 offset:32992\n" \
"ds_read_b64_tr_b16 a[18:19], v10 offset:37600\n" \
"v_mfma_f32_16x16x32_bf16 v[152:155], a[192:195], a[104:107], v[152:155]\n" \
"ds_read_b64_tr_b16 a[20:21], v10 offset:33024\n" \
"ds_read_b64_tr_b16 a[22:23], v10 offset:37632\n" \
"v_mfma_f32_16x16x32_bf16 v[156:159], a[192:195], a[120:123], v[156:159]\n" \
"ds_read_b64_tr_b16 a[24:25], v35 offset:41984\n" \
"ds_read_b64_tr_b16 a[26:27], v35 offset:44288\n" \
"v_mfma_f32_16x16x32_bf16 v[224:227], a[192:195], v[72:75], v[224:227]\n" \
"ds_read_b64_tr_b16 a[28:29], v35 offset:42016\n" \
"ds_read_b64_tr_b16 a[30:31], v35 offset:44320\n" \
"v_mfma_f32_16x16x32_bf16 v[228:231], a[192:195], v[96:99], v[228:231]\n" \
"ds_read_b64_tr_b16 a[192:193], v35 offset:42048\n" \
"ds_read_b64_tr_b16 a[194:195], v35 offset:44352\n" \
"v_mfma_f32_16x16x32_bf16 v[160:163], a[196:199], a[104:107], v[160:163]\n" \
"buffer_load_dwordx4 v8, s[80:83], s66 offen offset:0 lds\n" \
"v_mfma_f32_16x16x32_bf16 v[164:167], a[196:199], a[120:123], v[164:167]\n" \
"s_add_u32 m0, s92, 0x0\n" \
"v_readlane_b32 s66, v11, 47\n" \
"v_mfma_f32_16x16x32_bf16 v[232:235], a[196:199], v[72:75], v[232:235]\n" \
"v_mfma_f32_16x16x32_bf16 v[236:239], a[196:199], v[96:99], v[236:239]\n" \
"ds_read_b64_tr_b16 a[196:197], v35 offset:42080\n" \
"ds_read_b64_tr_b16 a[198:199], v35 offset:44384\n" \
"v_mfma_f32_16x16x32_bf16 v[168:171], a[200:203], a[104:107], v[168:171]\n" \
"v_mfma_f32_16x16x32_bf16 v[172:175], a[200:203], a[120:123], v[172:175]\n" \
"buffer_load_dwordx4 v0, s[80:83], s73 offen offset:0 lds\n" \
"v_mfma_f32_16x16x32_bf16 v[240:243], a[200:203], v[72:75], v[240:243]\n" \
"v_mfma_f32_16x16x32_bf16 v[244:247], a[200:203], v[96:99], v[244:247]\n" \
"ds_read_b64_tr_b16 a[200:201], v35 offset:42112\n" \
"ds_read_b64_tr_b16 a[202:203], v35 offset:44416\n" \
"v_mfma_f32_16x16x32_bf16 v[176:179], a[204:207], a[104:107], v[176:179]\n" \
"v_mfma_f32_16x16x32_bf16 v[180:183], a[204:207], a[120:123], v[180:183]\n" \
"v_mfma_f32_16x16x32_bf16 v[248:251], a[204:207], v[72:75], v[248:251]\n" \
"buffer_load_dwordx4 v1, s[80:83], s73 offen offset:1024 lds\n" \
"v_mfma_f32_16x16x32_bf16 v[252:255], a[204:207], v[96:99], v[252:255]\n" \
"ds_read_b64_tr_b16 a[204:205], v35 offset:42144\n" \
"s_waitcnt lgkmcnt(9)\n" \
"v_mfma_f32_16x16x32_bf16 v[112:115], a[208:211], a[108:111], v[112:115]\n" \
"ds_read_b64_tr_b16 a[206:207], v35 offset:44448\n" \
"v_mfma_f32_16x16x32_bf16 v[116:119], a[208:211], a[124:127], v[116:119]\n" \
"v_mfma_f32_16x16x32_bf16 v[184:187], a[208:211], v[76:79], v[184:187]\n" \
"v_mfma_f32_16x16x32_bf16 v[188:191], a[208:211], v[100:103], v[188:191]\n" \
"ds_read_b64_tr_b16 a[208:209], v35 offset:42176\n" \
"ds_read_b64_tr_b16 a[210:211], v35 offset:44480\n" \
"v_mfma_f32_16x16x32_bf16 v[120:123], a[212:215], a[108:111], v[120:123]\n" \
"buffer_load_dwordx4 v2, s[80:83], s73 offen offset:2048 lds\n" \
"v_mfma_f32_16x16x32_bf16 v[124:127], a[212:215], a[124:127], v[124:127]\n" \
"v_mfma_f32_16x16x32_bf16 v[192:195], a[212:215], v[76:79], v[192:195]\n" \
"s_waitcnt vmcnt(18)\n" \
"s_mov_b64 exec, 1\n" \
"buffer_store_dword v39, off, s[56:59], s70 offset:0 sc1\n" \
"s_mov_b64 exec, -1\n" \
"v_mfma_f32_16x16x32_bf16 v[196:199], a[212:215], v[100:103], v[196:199]\n" \
"ds_read_b64_tr_b16 a[212:213], v35 offset:42208\n" \
"ds_read_b64_tr_b16 a[214:215], v35 offset:44512\n" \
"v_mfma_f32_16x16x32_bf16 v[128:131], a[216:219], a[108:111], v[128:131]\n" \
"v_readlane_b32 s70, v11, 51\n" \
"v_mfma_f32_16x16x32_bf16 v[132:135], a[216:219], a[124:127], v[132:135]\n" \
"v_mfma_f32_16x16x32_bf16 v[200:203], a[216:219], v[76:79], v[200:203]\n" \
"buffer_load_dwordx4 v3, s[80:83], s73 offen offset:3072 lds\n" \
"v_mfma_f32_16x16x32_bf16 v[204:207], a[216:219], v[100:103], v[204:207]\n" \
"ds_read_b64_tr_b16 a[216:217], v35 offset:42240\n" \
"ds_read_b64_tr_b16 a[218:219], v35 offset:44544\n" \
"v_mfma_f32_16x16x32_bf16 v[136:139], a[0:3], a[108:111], v[136:139]\n" \
"s_add_u32 m0, s92, 0x1000\n" \
"v_mfma_f32_16x16x32_bf16 v[140:143], a[0:3], a[124:127], v[140:143]\n" \
"v_mfma_f32_16x16x32_bf16 v[208:211], a[0:3], v[76:79], v[208:211]\n" \
"v_mfma_f32_16x16x32_bf16 v[212:215], a[0:3], v[100:103], v[212:215]\n" \
"ds_read_b64_tr_b16 a[0:1], v35 offset:52352\n" \
"ds_read_b64_tr_b16 a[2:3], v35 offset:54656\n" \
"v_mfma_f32_16x16x32_bf16 v[144:147], a[4:7], a[108:111], v[144:147]\n" \
"buffer_load_dwordx4 v4, s[80:83], s73 offen offset:0 lds\n" \
"v_mfma_f32_16x16x32_bf16 v[148:151], a[4:7], a[124:127], v[148:151]\n" \
"v_mfma_f32_16x16x32_bf16 v[216:219], a[4:7], v[76:79], v[216:219]\n" \
"v_mfma_f32_16x16x32_bf16 v[220:223], a[4:7], v[100:103], v[220:223]\n" \
"ds_read_b64_tr_b16 a[4:5], v35 offset:52384\n" \
"ds_read_b64_tr_b16 a[6:7], v35 offset:54688\n" \
"v_mfma_f32_16x16x32_bf16 v[152:155], a[8:11], a[108:111], v[152:155]\n" \
"v_mfma_f32_16x16x32_bf16 v[156:159], a[8:11], a[124:127], v[156:159]\n" \
"buffer_load_dwordx4 v5, s[80:83], s73 offen offset:1024 lds\n" \
"v_mfma_f32_16x16x32_bf16 v[224:227], a[8:11], v[76:79], v[224:227]\n" \
"v_mfma_f32_16x16x32_bf16 v[228:231], a[8:11], v[100:103], v[228:231]\n" \
"ds_read_b64_tr_b16 a[8:9], v35 offset:52416\n" \
"ds_read_b64_tr_b16 a[10:11], v35 offset:54720\n" \
"v_mfma_f32_16x16x32_bf16 v[160:163], a[12:15], a[108:111], v[160:163]\n" \
"v_mfma_f32_16x16x32_bf16 v[164:167], a[12:15], a[124:127], v[164:167]\n" \
"v_mfma_f32_16x16x32_bf16 v[232:235], a[12:15], v[76:79], v[232:235]\n" \
"buffer_load_dwordx4 v6, s[80:83], s73 offen offset:2048 lds\n" \
"v_mfma_f32_16x16x32_bf16 v[236:239], a[12:15], v[100:103], v[236:239]\n" \
"ds_read_b64_tr_b16 a[12:13], v35 offset:52448\n" \
"ds_read_b64_tr_b16 a[14:15], v35 offset:54752\n" \
"v_mfma_f32_16x16x32_bf16 v[168:171], a[16:19], a[108:111], v[168:171]\n" \
"v_mfma_f32_16x16x32_bf16 v[172:175], a[16:19], a[124:127], v[172:175]\n" \
"v_mfma_f32_16x16x32_bf16 v[240:243], a[16:19], v[76:79], v[240:243]\n" \
"v_mfma_f32_16x16x32_bf16 v[244:247], a[16:19], v[100:103], v[244:247]\n" \
"buffer_load_dwordx4 v7, s[80:83], s73 offen offset:3072 lds\n" \
"v_mfma_f32_16x16x32_bf16 v[176:179], a[20:23], a[108:111], v[176:179]\n" \
"s_add_u32 m0, s92, 0x2000\n" \
"ds_read_b64_tr_b16 a[16:17], v35 offset:52480\n" \
"v_mfma_f32_16x16x32_bf16 v[180:183], a[20:23], a[124:127], v[180:183]\n" \
"ds_read_b64_tr_b16 a[18:19], v35 offset:54784\n" \
"v_readlane_b32 s92, v11, 46\n" \
"v_mfma_f32_16x16x32_bf16 v[248:251], a[20:23], v[76:79], v[248:251]\n" \
"v_mfma_f32_16x16x32_bf16 v[252:255], a[20:23], v[100:103], v[252:255]\n" \
"ds_read_b64_tr_b16 a[20:21], v35 offset:52512\n" \
"ds_read_b64_tr_b16 a[22:23], v35 offset:54816\n" \
"v_mfma_f32_16x16x32_bf16 v[112:115], a[24:27], a[220:223], v[112:115]\n" \
"buffer_load_dwordx4 v8, s[80:83], s73 offen offset:0 lds\n" \
"v_mfma_f32_16x16x32_bf16 v[116:119], a[24:27], a[228:231], v[116:119]\n" \
"s_mov_b32 m0, s94\n" \
"v_readlane_b32 s94, v11, 48\n" \
"v_mfma_f32_16x16x32_bf16 v[184:187], a[24:27], a[236:239], v[184:187]\n" \
"v_readlane_b32 s73, v11, 6\n" \
"v_mfma_f32_16x16x32_bf16 v[188:191], a[24:27], a[244:247], v[188:191]\n" \
"ds_read_b64_tr_b16 a[24:25], v35 offset:52544\n" \
"s_waitcnt lgkmcnt(7)\n" \
"v_mfma_f32_16x16x32_bf16 v[120:123], a[28:31], a[220:223], v[120:123]\n" \
"ds_read_b64_tr_b16 a[26:27], v35 offset:54848\n" \
"buffer_load_dword v47, v38, s[56:59], s71 offen sc1\n" \
"v_mfma_f32_16x16x32_bf16 v[124:127], a[28:31], a[228:231], v[124:127]\n" \
"v_mfma_f32_16x16x32_bf16 v[192:195], a[28:31], a[236:239], v[192:195]\n" \
"v_mfma_f32_16x16x32_bf16 v[196:199], a[28:31], a[244:247], v[196:199]\n" \
"ds_read_b64_tr_b16 a[28:29], v35 offset:52576\n" \
"ds_read_b64_tr_b16 a[30:31], v35 offset:54880\n" \
"v_mfma_f32_16x16x32_bf16 v[128:131], a[192:195], a[220:223], v[128:131]\n" \
"v_mfma_f32_16x16x32_bf16 v[132:135], a[192:195], a[228:231], v[132:135]\n" \
"buffer_load_dwordx4 v9, s[84:87], 0 offen lds\n" \
"v_mfma_f32_16x16x32_bf16 v[200:203], a[192:195], a[236:239], v[200:203]\n" \
"v_mfma_f32_16x16x32_bf16 v[204:207], a[192:195], a[244:247], v[204:207]\n" \
"ds_read_b64_tr_b16 a[192:193], v35 offset:52608\n" \
"ds_read_b64_tr_b16 a[194:195], v35 offset:54912\n" \
"v_mfma_f32_16x16x32_bf16 v[136:139], a[196:199], a[220:223], v[136:139]\n" \
"v_mfma_f32_16x16x32_bf16 v[140:143], a[196:199], a[228:231], v[140:143]\n" \
"v_mfma_f32_16x16x32_bf16 v[208:211], a[196:199], a[236:239], v[208:211]\n" \
"buffer_load_dwordx4 a[76:79], v20, s[56:59], s65 offen offset:3072 sc1\n" \
"v_mfma_f32_16x16x32_bf16 v[212:215], a[196:199], a[244:247], v[212:215]\n" \
"v_mfma_f32_16x16x32_bf16 v[144:147], a[200:203], a[220:223], v[144:147]\n" \
"v_mfma_f32_16x16x32_bf16 v[148:151], a[200:203], a[228:231], v[148:151]\n" \
"v_mfma_f32_16x16x32_bf16 v[216:219], a[200:203], a[236:239], v[216:219]\n" \
"v_mfma_f32_16x16x32_bf16 v[220:223], a[200:203], a[244:247], v[220:223]\n" \
"buffer_load_dwordx4 a[92:95], v32, s[56:59], s65 offen offset:3072 sc1\n" \
"v_mfma_f32_16x16x32_bf16 v[152:155], a[204:207], a[220:223], v[152:155]\n" \
"v_readlane_b32 s65, v11, 46\n" \
"v_mfma_f32_16x16x32_bf16 v[156:159], a[204:207], a[228:231], v[156:159]\n" \
"v_mfma_f32_16x16x32_bf16 v[224:227], a[204:207], a[236:239], v[224:227]\n" \
"v_mfma_f32_16x16x32_bf16 v[228:231], a[204:207], a[244:247], v[228:231]\n" \
"v_mfma_f32_16x16x32_bf16 v[160:163], a[208:211], a[220:223], v[160:163]\n" \
"buffer_load_dwordx4 a[128:131], v20, s[56:59], s68 offen offset:0 sc1\n" \
"v_mfma_f32_16x16x32_bf16 v[164:167], a[208:211], a[228:231], v[164:167]\n" \
"v_mfma_f32_16x16x32_bf16 v[232:235], a[208:211], a[236:239], v[232:235]\n" \
"v_mfma_f32_16x16x32_bf16 v[236:239], a[208:211], a[244:247], v[236:239]\n" \
"v_mfma_f32_16x16x32_bf16 v[168:171], a[212:215], a[220:223], v[168:171]\n" \
"v_mfma_f32_16x16x32_bf16 v[172:175], a[212:215], a[228:231], v[172:175]\n" \
"buffer_load_dwordx4 a[144:147], v32, s[56:59], s68 offen offset:0 sc1\n" \
"v_mfma_f32_16x16x32_bf16 v[240:243], a[212:215], a[236:239], v[240:243]\n" \
"v_mfma_f32_16x16x32_bf16 v[244:247], a[212:215], a[244:247], v[244:247]\n" \
"v_mfma_f32_16x16x32_bf16 v[176:179], a[216:219], a[220:223], v[176:179]\n" \
"v_mfma_f32_16x16x32_bf16 v[180:183], a[216:219], a[228:231], v[180:183]\n" \
"v_mfma_f32_16x16x32_bf16 v[248:251], a[216:219], a[236:239], v[248:251]\n" \
"buffer_load_dwordx4 a[132:135], v20, s[56:59], s68 offen offset:1024 sc1\n" \
"v_mfma_f32_16x16x32_bf16 v[252:255], a[216:219], a[244:247], v[252:255]\n" \
"v_mfma_f32_16x16x32_bf16 v[112:115], a[0:3], a[224:227], v[112:115]\n" \
"v_mfma_f32_16x16x32_bf16 v[116:119], a[0:3], a[232:235], v[116:119]\n" \
"v_mfma_f32_16x16x32_bf16 v[184:187], a[0:3], a[240:243], v[184:187]\n" \
"v_mfma_f32_16x16x32_bf16 v[188:191], a[0:3], a[248:251], v[188:191]\n" \
"buffer_load_dwordx4 a[148:151], v32, s[56:59], s68 offen offset:1024 sc1\n" \
"v_mfma_f32_16x16x32_bf16 v[120:123], a[4:7], a[224:227], v[120:123]\n" \
"v_mfma_f32_16x16x32_bf16 v[124:127], a[4:7], a[232:235], v[124:127]\n" \
"v_mfma_f32_16x16x32_bf16 v[192:195], a[4:7], a[240:243], v[192:195]\n" \
"v_mfma_f32_16x16x32_bf16 v[196:199], a[4:7], a[248:251], v[196:199]\n" \
"v_mfma_f32_16x16x32_bf16 v[128:131], a[8:11], a[224:227], v[128:131]\n" \
"buffer_load_dwordx4 a[136:139], v20, s[56:59], s68 offen offset:2048 sc1\n" \
"v_mfma_f32_16x16x32_bf16 v[132:135], a[8:11], a[232:235], v[132:135]\n" \
"v_mfma_f32_16x16x32_bf16 v[200:203], a[8:11], a[240:243], v[200:203]\n" \
"v_mfma_f32_16x16x32_bf16 v[204:207], a[8:11], a[248:251], v[204:207]\n" \
"s_waitcnt lgkmcnt(0)\n" \
"v_mfma_f32_16x16x32_bf16 v[136:139], a[12:15], a[224:227], v[136:139]\n" \
"v_mfma_f32_16x16x32_bf16 v[140:143], a[12:15], a[232:235], v[140:143]\n" \
"buffer_load_dwordx4 a[152:155], v32, s[56:59], s68 offen offset:2048 sc1\n" \
"v_mfma_f32_16x16x32_bf16 v[208:211], a[12:15], a[240:243], v[208:211]\n" \
"v_mfma_f32_16x16x32_bf16 v[212:215], a[12:15], a[248:251], v[212:215]\n" \
"s_barrier\n" \
"v_mfma_f32_16x16x32_bf16 v[144:147], a[16:19], a[224:227], v[144:147]\n" \
"ds_read_b64_tr_b16 a[196:197], v33 offset:0\n" \
"ds_read_b64_tr_b16 a[198:199], v33 offset:4608\n" \
"v_mfma_f32_16x16x32_bf16 v[148:151], a[16:19], a[232:235], v[148:151]\n" \
"buffer_load_dwordx4 a[140:143], v20, s[56:59], s68 offen offset:3072 sc1\n" \
"v_mfma_f32_16x16x32_bf16 v[216:219], a[16:19], a[240:243], v[216:219]\n" \
"ds_read_b64_tr_b16 a[200:201], v33 offset:32\n" \
"ds_read_b64_tr_b16 a[202:203], v33 offset:4640\n" \
"v_mfma_f32_16x16x32_bf16 v[220:223], a[16:19], a[248:251], v[220:223]\n" \
"ds_read_b64_tr_b16 a[204:205], v33 offset:64\n" \
"ds_read_b64_tr_b16 a[206:207], v33 offset:4672\n" \
"v_mfma_f32_16x16x32_bf16 v[152:155], a[20:23], a[224:227], v[152:155]\n" \
"ds_read_b64_tr_b16 a[208:209], v33 offset:96\n" \
"ds_read_b64_tr_b16 a[210:211], v33 offset:4704\n" \
"v_mfma_f32_16x16x32_bf16 v[156:159], a[20:23], a[232:235], v[156:159]\n" \
"ds_read_b64_tr_b16 a[212:213], v33 offset:128\n" \
"ds_read_b64_tr_b16 a[214:215], v33 offset:4736\n" \
"v_mfma_f32_16x16x32_bf16 v[224:227], a[20:23], a[240:243], v[224:227]\n" \
"buffer_load_dwordx4 a[156:159], v32, s[56:59], s68 offen offset:3072 sc1\n" \
"v_mfma_f32_16x16x32_bf16 v[228:231], a[20:23], a[248:251], v[228:231]\n" \
"ds_read_b64_tr_b16 a[216:217], v33 offset:160\n" \
"ds_read_b64_tr_b16 a[218:219], v33 offset:4768\n" \
"v_mfma_f32_16x16x32_bf16 v[160:163], a[24:27], a[224:227], v[160:163]\n" \
"ds_read_b64_tr_b16 a[0:1], v33 offset:192\n" \
"ds_read_b64_tr_b16 a[2:3], v33 offset:4800\n" \
"v_mfma_f32_16x16x32_bf16 v[164:167], a[24:27], a[232:235], v[164:167]\n" \
"ds_read_b64_tr_b16 a[4:5], v33 offset:224\n" \
"ds_read_b64_tr_b16 a[6:7], v33 offset:4832\n" \
"v_mfma_f32_16x16x32_bf16 v[232:235], a[24:27], a[240:243], v[232:235]\n" \
"ds_read_b64_tr_b16 a[8:9], v33 offset:256\n" \
"ds_read_b64_tr_b16 a[10:11], v33 offset:4864\n" \
"buffer_load_dwordx4 a[32:35], v20, s[56:59], s69 offen offset:0 sc1\n" \
"v_mfma_f32_16x16x32_bf16 v[236:239], a[24:27], a[248:251], v[236:239]\n" \
"ds_read_b64_tr_b16 a[12:13], v33 offset:9216\n" \
"ds_read_b64_tr_b16 a[14:15], v33 offset:13824\n" \
"v_mfma_f32_16x16x32_bf16 v[168:171], a[28:31], a[224:227], v[168:171]\n" \
"ds_read_b64_tr_b16 a[16:17], v33 offset:9248\n" \
"ds_read_b64_tr_b16 a[18:19], v33 offset:13856\n" \
"v_mfma_f32_16x16x32_bf16 v[172:175], a[28:31], a[232:235], v[172:175]\n" \
"ds_read_b64_tr_b16 a[20:21], v33 offset:9280\n" \
"ds_read_b64_tr_b16 a[22:23], v33 offset:13888\n" \
"v_mfma_f32_16x16x32_bf16 v[240:243], a[28:31], a[240:243], v[240:243]\n" \
"ds_read_b64_tr_b16 a[24:25], v33 offset:9312\n" \
"ds_read_b64_tr_b16 a[26:27], v33 offset:13920\n" \
"buffer_load_dwordx4 a[48:51], v32, s[56:59], s69 offen offset:0 sc1\n" \
"v_mfma_f32_16x16x32_bf16 v[244:247], a[28:31], a[248:251], v[244:247]\n" \
"ds_read_b64_tr_b16 a[28:29], v33 offset:9344\n" \
"ds_read_b64_tr_b16 a[30:31], v33 offset:13952\n" \
"v_mfma_f32_16x16x32_bf16 v[176:179], a[192:195], a[224:227], v[176:179]\n" \
"v_readlane_b32 s68, v11, 49\n" \
"s_waitcnt vmcnt(13)\n" \
"v_cmp_ne_u32_e32 vcc, s62, v47\n" \
"v_mfma_f32_16x16x32_bf16 v[180:183], a[192:195], a[232:235], v[180:183]\n" \
"v_mfma_f32_16x16x32_bf16 v[248:251], a[192:195], a[240:243], v[248:251]\n" \
"buffer_load_dwordx4 a[36:39], v20, s[56:59], s69 offen offset:1024 sc1\n" \
"v_mfma_f32_16x16x32_bf16 v[252:255], a[192:195], a[248:251], v[252:255]\n" \
"ds_read_b64_tr_b16 a[192:193], v33 offset:9376\n" \
"s_waitcnt lgkmcnt(15)\n" \
"v_mfma_f32_16x16x32_bf16 v[112:115], a[196:199], a[64:67], v[112:115]\n" \
"ds_read_b64_tr_b16 a[194:195], v33 offset:13984\n" \
"s_cbranch_vccnz L_sb_%=\n" \
"L_rb_%=:\n" \
"v_mfma_f32_16x16x32_bf16 v[116:119], a[196:199], a[80:83], v[116:119]\n" \
"ds_read_b64_tr_b16 a[196:197], v33 offset:9408\n" \
"ds_read_b64_tr_b16 a[198:199], v33 offset:14016\n" \
"v_mfma_f32_16x16x32_bf16 v[120:123], a[200:203], a[64:67], v[120:123]\n" \
"v_readlane_b32 s71, v11, 52\n" \
"v_cmp_eq_u32_e32 vcc, 0, v44\n" \
"buffer_load_dwordx4 a[52:55], v32, s[56:59], s69 offen offset:1024 sc1\n" \
"v_mfma_f32_16x16x32_bf16 v[124:127], a[200:203], a[80:83], v[124:127]\n" \
"ds_read_b64_tr_b16 a[200:201], v33 offset:9440\n" \
"ds_read_b64_tr_b16 a[202:203], v33 offset:14048\n" \
"v_mfma_f32_16x16x32_bf16 v[128:131], a[204:207], a[64:67], v[128:131]\n" \
"v_cndmask_b32_e32 v50, 0, v45, vcc\n" \
"v_cmp_eq_u32_e32 vcc, 1, v44\n" \
"v_mfma_f32_16x16x32_bf16 v[132:135], a[204:207], a[80:83], v[132:135]\n" \
"ds_read_b64_tr_b16 a[204:205], v33 offset:9472\n" \
"ds_read_b64_tr_b16 a[206:207], v33 offset:14080\n" \
"v_mfma_f32_16x16x32_bf16 v[136:139], a[208:211], a[64:67], v[136:139]\n" \
"v_cndmask_b32_e32 v51, 0, v45, vcc\n" \
"v_bfe_u32 v45, v20, 1, 5\n" \
"buffer_load_dwordx4 a[40:43], v20, s[56:59], s69 offen offset:2048 sc1\n" \
"v_mfma_f32_16x16x32_bf16 v[140:143], a[208:211], a[80:83], v[140:143]\n" \
"ds_read_b64_tr_b16 a[208:209], v33 offset:18432\n" \
"ds_read_b64_tr_b16 a[210:211], v33 offset:23040\n" \
"v_mfma_f32_16x16x32_bf16 v[144:147], a[212:215], a[64:67], v[144:147]\n" \
"v_and_b32_e32 v45, 24, v45\n" \
"v_and_b32_e32 v44, 0x3c0, v20\n" \
"v_mfma_f32_16x16x32_bf16 v[148:151], a[212:215], a[80:83], v[148:151]\n" \
"ds_read_b64_tr_b16 a[212:213], v33 offset:18464\n" \
"ds_read_b64_tr_b16 a[214:215], v33 offset:23072\n" \
"v_mfma_f32_16x16x32_bf16 v[152:155], a[216:219], a[64:67], v[152:155]\n" \
"v_add_u32_e32 v45, 0x4800, v45\n" \
"v_add_u32_e32 v36, v44, v45\n" \
"buffer_load_dwordx4 a[56:59], v32, s[56:59], s69 offen offset:2048 sc1\n" \
"v_mfma_f32_16x16x32_bf16 v[156:159], a[216:219], a[80:83], v[156:159]\n" \
"ds_read_b64_tr_b16 a[216:217], v33 offset:18496\n" \
"ds_read_b64_tr_b16 a[218:219], v33 offset:23104\n" \
"v_mfma_f32_16x16x32_bf16 v[160:163], a[0:3], a[64:67], v[160:163]\n" \
".fill (L_ab + 36864 - .) / 4, 4, 0xBF800000\n" \
"L_ap9:\n" \
"s_cbranch_execz L_ap0\n" \
"v_mfma_f32_16x16x32_bf16 v[164:167], a[0:3], a[80:83], v[164:167]\n" \
"ds_read_b64_tr_b16 a[0:1], v33 offset:18528\n" \
"s_waitcnt lgkmcnt(13)\n" \
"v_mfma_f32_16x16x32_bf16 v[168:171], a[4:7], a[64:67], v[168:171]\n" \
"ds_read_b64_tr_b16 a[2:3], v33 offset:23136\n" \
"buffer_load_dwordx4 a[44:47], v20, s[56:59], s69 offen offset:3072 sc1\n" \
"v_mfma_f32_16x16x32_bf16 v[172:175], a[4:7], a[80:83], v[172:175]\n" \
"ds_read_b64_tr_b16 a[4:5], v33 offset:18560\n" \
"ds_read_b64_tr_b16 a[6:7], v33 offset:23168\n" \
"v_mfma_f32_16x16x32_bf16 v[176:179], a[8:11], a[64:67], v[176:179]\n" \
"v_mfma_f32_16x16x32_bf16 v[180:183], a[8:11], a[80:83], v[180:183]\n" \
"ds_read_b64_tr_b16 a[8:9], v33 offset:18592\n" \
"ds_read_b64_tr_b16 a[10:11], v33 offset:23200\n" \
"v_mfma_f32_16x16x32_bf16 v[112:115], a[12:15], a[68:71], v[112:115]\n" \
"v_mfma_f32_16x16x32_bf16 v[116:119], a[12:15], a[84:87], v[116:119]\n" \
"buffer_load_dwordx4 a[60:63], v32, s[56:59], s69 offen offset:3072 sc1\n" \
"v_mfma_f32_16x16x32_bf16 v[120:123], a[16:19], a[68:71], v[120:123]\n" \
"ds_read_b64_tr_b16 a[12:13], v33 offset:18624\n" \
"ds_read_b64_tr_b16 a[14:15], v33 offset:23232\n" \
"v_mfma_f32_16x16x32_bf16 v[124:127], a[16:19], a[84:87], v[124:127]\n" \
"ds_read_b64_tr_b16 a[16:17], v33 offset:18656\n" \
"ds_read_b64_tr_b16 a[18:19], v33 offset:23264\n" \
"v_mfma_f32_16x16x32_bf16 v[128:131], a[20:23], a[68:71], v[128:131]\n" \
"v_readlane_b32 s69, v11, 50\n" \
"v_mfma_f32_16x16x32_bf16 v[132:135], a[20:23], a[84:87], v[132:135]\n" \
"ds_read_b64_tr_b16 a[20:21], v33 offset:18688\n" \
"ds_read_b64_tr_b16 a[22:23], v33 offset:23296\n" \
"v_mfma_f32_16x16x32_bf16 v[136:139], a[24:27], a[68:71], v[136:139]\n" \
"buffer_load_dwordx4 a[220:223], v20, s[56:59], s64 offen offset:0 sc1\n" \
"v_mfma_f32_16x16x32_bf16 v[140:143], a[24:27], a[84:87], v[140:143]\n" \
"ds_read_b64_tr_b16 a[24:25], v33 offset:27648\n" \
"ds_read_b64_tr_b16 a[26:27], v33 offset:32256\n" \
"v_mfma_f32_16x16x32_bf16 v[144:147], a[28:31], a[68:71], v[144:147]\n" \
"v_mfma_f32_16x16x32_bf16 v[148:151], a[28:31], a[84:87], v[148:151]\n" \
"ds_read_b64_tr_b16 a[28:29], v33 offset:27680\n" \
"ds_read_b64_tr_b16 a[30:31], v33 offset:32288\n" \
"v_mfma_f32_16x16x32_bf16 v[152:155], a[192:195], a[68:71], v[152:155]\n" \
"v_mfma_f32_16x16x32_bf16 v[156:159], a[192:195], a[84:87], v[156:159]\n" \
"ds_read_b64_tr_b16 a[192:193], v33 offset:27712\n" \
"s_waitcnt lgkmcnt(13)\n" \
"v_mfma_f32_16x16x32_bf16 v[160:163], a[196:199], a[68:71], v[160:163]\n" \
"buffer_load_dwordx4 a[236:239], v32, s[56:59], s64 offen offset:0 sc1\n" \
"v_mfma_f32_16x16x32_bf16 v[164:167], a[196:199], a[84:87], v[164:167]\n" \
"ds_read_b64_tr_b16 a[194:195], v33 offset:32320\n" \
"ds_read_b64_tr_b16 a[196:197], v33 offset:27744\n" \
"v_mfma_f32_16x16x32_bf16 v[168:171], a[200:203], a[68:71], v[168:171]\n" \
"ds_read_b64_tr_b16 a[198:199], v33 offset:32352\n" \
"v_mfma_f32_16x16x32_bf16 v[172:175], a[200:203], a[84:87], v[172:175]\n" \
"ds_read_b64_tr_b16 a[200:201], v33 offset:27776\n" \
"ds_read_b64_tr_b16 a[202:203], v33 offset:32384\n" \
"v_mfma_f32_16x16x32_bf16 v[176:179], a[204:207], a[68:71], v[176:179]\n" \
"v_mfma_f32_16x16x32_bf16 v[180:183], a[204:207], a[84:87], v[180:183]\n" \
"ds_read_b64_tr_b16 a[204:205], v33 offset:27808\n" \
"ds_read_b64_tr_b16 a[206:207], v33 offset:32416\n" \
"v_mfma_f32_16x16x32_bf16 v[112:115], a[208:211], a[72:75], v[112:115]\n" \
"buffer_load_dwordx4 a[224:227], v20, s[56:59], s64 offen offset:1024 sc1\n" \
"v_mfma_f32_16x16x32_bf16 v[116:119], a[208:211], a[88:91], v[116:119]\n" \
"ds_read_b64_tr_b16 a[208:209], v33 offset:27840\n" \
"ds_read_b64_tr_b16 a[210:211], v33 offset:32448\n" \
"v_mfma_f32_16x16x32_bf16 v[120:123], a[212:215], a[72:75], v[120:123]\n" \
"v_mfma_f32_16x16x32_bf16 v[124:127], a[212:215], a[88:91], v[124:127]\n" \
"ds_read_b64_tr_b16 a[212:213], v33 offset:27872\n" \
"ds_read_b64_tr_b16 a[214:215], v33 offset:32480\n" \
"v_mfma_f32_16x16x32_bf16 v[128:131], a[216:219], a[72:75], v[128:131]\n" \
"v_mfma_f32_16x16x32_bf16 v[132:135], a[216:219], a[88:91], v[132:135]\n" \
"ds_read_b64_tr_b16 a[216:217], v33 offset:27904\n" \
"ds_read_b64_tr_b16 a[218:219], v33 offset:32512\n" \
"v_mfma_f32_16x16x32_bf16 v[136:139], a[0:3], a[72:75], v[136:139]\n" \
"buffer_load_dwordx4 a[240:243], v32, s[56:59], s64 offen offset:1024 sc1\n" \
"v_mfma_f32_16x16x32_bf16 v[140:143], a[0:3], a[88:91], v[140:143]\n" \
"v_mfma_f32_16x16x32_bf16 v[144:147], a[4:7], a[72:75], v[144:147]\n" \
"v_mfma_f32_16x16x32_bf16 v[148:151], a[4:7], a[88:91], v[148:151]\n" \
"s_waitcnt lgkmcnt(12)\n" \
"v_mfma_f32_16x16x32_bf16 v[152:155], a[8:11], a[72:75], v[152:155]\n" \
"v_mfma_f32_16x16x32_bf16 v[156:159], a[8:11], a[88:91], v[156:159]\n" \
"buffer_load_dwordx4 a[228:231], v20, s[56:59], s64 offen offset:2048 sc1\n" \
"v_mfma_f32_16x16x32_bf16 v[160:163], a[12:15], a[72:75], v[160:163]\n" \
"v_mfma_f32_16x16x32_bf16 v[164:167], a[12:15], a[88:91], v[164:167]\n" \
"v_mfma_f32_16x16x32_bf16 v[168:171], a[16:19], a[72:75], v[168:171]\n" \
"v_mfma_f32_16x16x32_bf16 v[172:175], a[16:19], a[88:91], v[172:175]\n" \
"v_mfma_f32_16x16x32_bf16 v[176:179], a[20:23], a[72:75], v[176:179]\n" \
"buffer_load_dwordx4 a[244:247], v32, s[56:59], s64 offen offset:2048 sc1\n" \
"v_mfma_f32_16x16x32_bf16 v[180:183], a[20:23], a[88:91], v[180:183]\n" \
"s_waitcnt vmcnt(24)\n" \
"s_barrier\n" \
"s_waitcnt vmcnt(23)\n" \
"v_mfma_f32_16x16x32_bf16 v[112:115], a[24:27], a[76:79], v[112:115]\n" \
"ds_read_b64_tr_b16 a[0:1], v33 offset:36864\n" \
"s_waitcnt vmcnt(22)\n" \
"v_mfma_f32_16x16x32_bf16 v[116:119], a[24:27], a[92:95], v[116:119]\n" \
"ds_read_b64_tr_b16 a[2:3], v33 offset:41472\n" \
"ds_read_b64_tr_b16 a[4:5], v33 offset:46080\n" \
"v_mfma_f32_16x16x32_bf16 v[120:123], a[28:31], a[76:79], v[120:123]\n" \
"ds_read_b64_tr_b16 a[6:7], v33 offset:50688\n" \
"ds_read_b64_tr_b16 a[8:9], v33 offset:55296\n" \
"v_mfma_f32_16x16x32_bf16 v[124:127], a[28:31], a[92:95], v[124:127]\n" \
"ds_read_b64_tr_b16 a[10:11], v33 offset:59904\n" \
"ds_read_b64_tr_b16 a[12:13], v10 offset:0\n" \
"v_mfma_f32_16x16x32_bf16 v[128:131], a[192:195], a[76:79], v[128:131]\n" \
"ds_read_b64_tr_b16 a[14:15], v10 offset:4608\n" \
"ds_read_b64_tr_b16 a[16:17], v34 offset:0\n" \
"v_mfma_f32_16x16x32_bf16 v[132:135], a[192:195], a[92:95], v[132:135]\n" \
"ds_read_b64_tr_b16 a[18:19], v34 offset:4608\n" \
"s_waitcnt lgkmcnt(10)\n" \
"v_mfma_f32_16x16x32_bf16 v[136:139], a[196:199], a[76:79], v[136:139]\n" \
"ds_read_b64_tr_b16 a[20:21], v34 offset:9216\n" \
"ds_read_b64_tr_b16 a[22:23], v34 offset:13824\n" \
"v_mfma_f32_16x16x32_bf16 v[140:143], a[196:199], a[92:95], v[140:143]\n" \
"ds_read_b64_tr_b16 a[24:25], v34 offset:18432\n" \
"ds_read_b64_tr_b16 a[26:27], v34 offset:23040\n" \
"v_mfma_f32_16x16x32_bf16 v[144:147], a[200:203], a[76:79], v[144:147]\n" \
"ds_read_b64_tr_b16 a[28:29], v10 offset:9216\n" \
"ds_read_b64_tr_b16 a[30:31], v10 offset:13824\n" \
"v_mfma_f32_16x16x32_bf16 v[148:151], a[200:203], a[92:95], v[148:151]\n" \
"ds_read_b64_tr_b16 a[192:193], v33 offset:36896\n" \
"ds_read_b64_tr_b16 a[194:195], v33 offset:41504\n" \
"v_mfma_f32_16x16x32_bf16 v[152:155], a[204:207], a[76:79], v[152:155]\n" \
"ds_read_b64_tr_b16 a[196:197], v33 offset:46112\n" \
"ds_read_b64_tr_b16 a[198:199], v33 offset:50720\n" \
"v_mfma_f32_16x16x32_bf16 v[156:159], a[204:207], a[92:95], v[156:159]\n" \
"ds_read_b64_tr_b16 a[200:201], v33 offset:55328\n" \
"ds_read_b64_tr_b16 a[202:203], v33 offset:59936\n" \
"v_mfma_f32_16x16x32_bf16 v[160:163], a[208:211], a[76:79], v[160:163]\n" \
"ds_read_b64_tr_b16 a[204:205], v10 offset:32\n" \
"ds_read_b64_tr_b16 a[206:207], v10 offset:4640\n" \
"v_mfma_f32_16x16x32_bf16 v[164:167], a[208:211], a[92:95], v[164:167]\n" \
"ds_read_b64_tr_b16 a[208:209], v34 offset:32\n" \
"ds_read_b64_tr_b16 a[210:211], v34 offset:4640\n" \
"v_mfma_f32_16x16x32_bf16 v[168:171], a[212:215], a[76:79], v[168:171]\n" \
"buffer_load_dwordx4 a[232:235], v20, s[56:59], s64 offen offset:3072 sc1\n" \
"v_mfma_f32_16x16x32_bf16 v[172:175], a[212:215], a[92:95], v[172:175]\n" \
"ds_read_b64_tr_b16 a[212:213], v34 offset:9248\n" \
"ds_read_b64_tr_b16 a[214:215], v34 offset:13856\n" \
"v_mfma_f32_16x16x32_bf16 v[176:179], a[216:219], a[76:79], v[176:179]\n" \
"v_mfma_f32_16x16x32_bf16 v[180:183], a[216:219], a[92:95], v[180:183]\n" \
"ds_read_b64_tr_b16 a[216:217], v34 offset:18464\n" \
"s_waitcnt vmcnt(22) lgkmcnt(15)\n" \
"v_mfma_f32_16x16x32_bf16 v[112:115], a[0:3], a[128:131], v[112:115]\n" \
"ds_read_b64_tr_b16 a[218:219], v34 offset:23072\n" \
"s_waitcnt vmcnt(21)\n" \
"v_mfma_f32_16x16x32_bf16 v[116:119], a[0:3], a[144:147], v[116:119]\n" \
"ds_read_b64_tr_b16 a[0:1], v10 offset:9248\n" \
"s_waitcnt vmcnt(20)\n" \
"v_mfma_f32_16x16x32_bf16 v[112:115], a[4:7], a[132:135], v[112:115]\n" \
"ds_read_b64_tr_b16 a[2:3], v10 offset:13856\n" \
"s_waitcnt vmcnt(19)\n" \
"v_mfma_f32_16x16x32_bf16 v[116:119], a[4:7], a[148:151], v[116:119]\n" \
"ds_read_b64_tr_b16 a[4:5], v33 offset:36928\n" \
"s_waitcnt vmcnt(18)\n" \
"v_mfma_f32_16x16x32_bf16 v[112:115], a[8:11], a[136:139], v[112:115]\n" \
"ds_read_b64_tr_b16 a[6:7], v33 offset:41536\n" \
"s_waitcnt vmcnt(17)\n" \
"v_mfma_f32_16x16x32_bf16 v[116:119], a[8:11], a[152:155], v[116:119]\n" \
"ds_read_b64_tr_b16 a[8:9], v33 offset:46144\n" \
"s_waitcnt vmcnt(16)\n" \
"v_mfma_f32_16x16x32_bf16 v[112:115], a[12:15], a[140:143], v[112:115]\n" \
"ds_read_b64_tr_b16 a[10:11], v33 offset:50752\n" \
"s_waitcnt vmcnt(15)\n" \
"v_mfma_f32_16x16x32_bf16 v[116:119], a[12:15], a[156:159], v[116:119]\n" \
"ds_read_b64_tr_b16 a[12:13], v33 offset:55360\n" \
"ds_read_b64_tr_b16 a[14:15], v33 offset:59968\n" \
"buffer_load_dwordx4 a[248:251], v32, s[56:59], s64 offen offset:3072 sc1\n" \
"s_waitcnt vmcnt(15)\n" \
"v_mfma_f32_16x16x32_bf16 v[112:115], a[16:19], a[32:35], v[112:115]\n" \
"buffer_load_dwordx4 a[160:163], v20, s[56:59], s72 offen offset:0 sc1\n" \
"s_waitcnt vmcnt(15)\n" \
"v_mfma_f32_16x16x32_bf16 v[116:119], a[16:19], a[48:51], v[116:119]\n" \
"ds_read_b64_tr_b16 a[16:17], v10 offset:64\n" \
"ds_read_b64_tr_b16 a[18:19], v10 offset:4672\n" \
"buffer_load_dwordx4 a[176:179], v32, s[56:59], s72 offen offset:0 sc1\n" \
"s_waitcnt vmcnt(15)\n" \
"v_mfma_f32_16x16x32_bf16 v[112:115], a[20:23], a[36:39], v[112:115]\n" \
"buffer_load_dwordx4 a[164:167], v20, s[56:59], s72 offen offset:1024 sc1\n" \
"s_waitcnt vmcnt(15)\n" \
"v_mfma_f32_16x16x32_bf16 v[116:119], a[20:23], a[52:55], v[116:119]\n" \
"ds_read_b64_tr_b16 a[20:21], v34 offset:64\n" \
"ds_read_b64_tr_b16 a[22:23], v34 offset:4672\n" \
"buffer_load_dwordx4 a[180:183], v32, s[56:59], s72 offen offset:1024 sc1\n" \
"s_waitcnt vmcnt(15)\n" \
"v_mfma_f32_16x16x32_bf16 v[112:115], a[24:27], a[40:43], v[112:115]\n" \
"buffer_load_dwordx4 a[168:171], v20, s[56:59], s72 offen offset:2048 sc1\n" \
"s_waitcnt vmcnt(15)\n" \
"v_mfma_f32_16x16x32_bf16 v[116:119], a[24:27], a[56:59], v[116:119]\n" \
"ds_read_b64_tr_b16 a[24:25], v34 offset:9280\n" \
"ds_read_b64_tr_b16 a[26:27], v34 offset:13888\n" \
"buffer_load_dwordx4 a[184:187], v32, s[56:59], s72 offen offset:2048 sc1\n" \
"s_waitcnt vmcnt(15) lgkmcnt(4)\n" \
"v_mfma_f32_16x16x32_bf16 v[112:115], a[28:31], a[44:47], v[112:115]\n" \
"buffer_load_dwordx4 a[172:175], v20, s[56:59], s72 offen offset:3072 sc1\n" \
"s_waitcnt vmcnt(15)\n" \
"v_mfma_f32_16x16x32_bf16 v[116:119], a[28:31], a[60:63], v[116:119]\n" \
"ds_read_b64_tr_b16 a[28:29], v34 offset:18496\n" \
"ds_read_b64_tr_b16 a[30:31], v34 offset:23104\n" \
"v_mfma_f32_16x16x32_bf16 v[120:123], a[192:195], a[128:131], v[120:123]\n" \
"buffer_load_dwordx4 a[188:191], v32, s[56:59], s72 offen offset:3072 sc1\n" \
"v_mfma_f32_16x16x32_bf16 v[124:127], a[192:195], a[144:147], v[124:127]\n" \
"v_cvt_pk_bf16_f32 v112, v112, v113\n" \
"v_cvt_pk_bf16_f32 v113, v114, v115\n" \
"v_mfma_f32_16x16x32_bf16 v[120:123], a[196:199], a[132:135], v[120:123]\n" \
"v_cvt_pk_bf16_f32 v114, v116, v117\n" \
"v_cvt_pk_bf16_f32 v115, v118, v119\n" \
"buffer_store_dwordx4 v[112:115], v20, s[56:59], s65 offen offset:0 sc1\n" \
"s_add_u32 s65, s65, 0x3800\n" \
"v_mfma_f32_16x16x32_bf16 v[124:127], a[196:199], a[148:151], v[124:127]\n" \
"ds_read_b64_tr_b16 a[192:193], v10 offset:9280\n" \
"ds_read_b64_tr_b16 a[194:195], v10 offset:13888\n" \
"v_mfma_f32_16x16x32_bf16 v[120:123], a[200:203], a[136:139], v[120:123]\n" \
"ds_read_b64_tr_b16 a[196:197], v33 offset:36960\n" \
"ds_read_b64_tr_b16 a[198:199], v33 offset:41568\n" \
"v_mfma_f32_16x16x32_bf16 v[124:127], a[200:203], a[152:155], v[124:127]\n" \
"ds_read_b64_tr_b16 a[200:201], v33 offset:46176\n" \
"ds_read_b64_tr_b16 a[202:203], v33 offset:50784\n" \
"v_mfma_f32_16x16x32_bf16 v[120:123], a[204:207], a[140:143], v[120:123]\n" \
"buffer_load_dwordx4 a[96:99], v20, s[56:59], s93 offen offset:0 sc1\n" \
"v_mfma_f32_16x16x32_bf16 v[124:127], a[204:207], a[156:159], v[124:127]\n" \
"ds_read_b64_tr_b16 a[204:205], v33 offset:55392\n" \
"ds_read_b64_tr_b16 a[206:207], v33 offset:60000\n" \
"v_mfma_f32_16x16x32_bf16 v[120:123], a[208:211], a[32:35], v[120:123]\n" \
"v_readlane_b32 s72, v11, 53\n" \
"v_mfma_f32_16x16x32_bf16 v[124:127], a[208:211], a[48:51], v[124:127]\n" \
"ds_read_b64_tr_b16 a[208:209], v10 offset:96\n" \
"ds_read_b64_tr_b16 a[210:211], v10 offset:4704\n" \
"v_mfma_f32_16x16x32_bf16 v[120:123], a[212:215], a[36:39], v[120:123]\n" \
"v_mfma_f32_16x16x32_bf16 v[124:127], a[212:215], a[52:55], v[124:127]\n" \
"ds_read_b64_tr_b16 a[212:213], v34 offset:96\n" \
"ds_read_b64_tr_b16 a[214:215], v34 offset:4704\n" \
"v_mfma_f32_16x16x32_bf16 v[120:123], a[216:219], a[40:43], v[120:123]\n" \
"buffer_load_dwordx4 a[112:115], v32, s[56:59], s93 offen offset:0 sc1\n" \
"v_mfma_f32_16x16x32_bf16 v[124:127], a[216:219], a[56:59], v[124:127]\n" \
"ds_read_b64_tr_b16 a[216:217], v34 offset:9312\n" \
"ds_read_b64_tr_b16 a[218:219], v34 offset:13920\n" \
"v_mfma_f32_16x16x32_bf16 v[120:123], a[0:3], a[44:47], v[120:123]\n" \
"v_mfma_f32_16x16x32_bf16 v[124:127], a[0:3], a[60:63], v[124:127]\n" \
"ds_read_b64_tr_b16 a[0:1], v34 offset:18528\n" \
"ds_read_b64_tr_b16 a[2:3], v34 offset:23136\n" \
"v_mfma_f32_16x16x32_bf16 v[128:131], a[4:7], a[128:131], v[128:131]\n" \
"v_mfma_f32_16x16x32_bf16 v[132:135], a[4:7], a[144:147], v[132:135]\n" \
"buffer_load_dwordx4 a[100:103], v20, s[56:59], s93 offen offset:1024 sc1\n" \
"v_mfma_f32_16x16x32_bf16 v[128:131], a[8:11], a[132:135], v[128:131]\n" \
"ds_read_b64_tr_b16 a[4:5], v10 offset:9312\n" \
"v_cvt_pk_bf16_f32 v120, v120, v121\n" \
"v_cvt_pk_bf16_f32 v121, v122, v123\n" \
"v_cvt_pk_bf16_f32 v122, v124, v125\n" \
"v_cvt_pk_bf16_f32 v123, v126, v127\n" \
"v_mfma_f32_16x16x32_bf16 v[132:135], a[8:11], a[148:151], v[132:135]\n" \
"buffer_store_dwordx4 v[120:123], v20, s[56:59], s66 offen offset:0 sc1\n" \
"s_add_u32 s66, s66, 0x3000\n" \
"v_mfma_f32_16x16x32_bf16 v[128:131], a[12:15], a[136:139], v[128:131]\n" \
"ds_read_b64_tr_b16 a[6:7], v10 offset:13920\n" \
"ds_read_b64_tr_b16 a[8:9], v33 offset:36992\n" \
"v_mfma_f32_16x16x32_bf16 v[132:135], a[12:15], a[152:155], v[132:135]\n" \
"ds_read_b64_tr_b16 a[10:11], v33 offset:41600\n" \
"ds_read_b64_tr_b16 a[12:13], v33 offset:46208\n" \
"v_mfma_f32_16x16x32_bf16 v[128:131], a[16:19], a[140:143], v[128:131]\n" \
"ds_read_b64_tr_b16 a[14:15], v33 offset:50816\n" \
"v_mfma_f32_16x16x32_bf16 v[132:135], a[16:19], a[156:159], v[132:135]\n" \
"ds_read_b64_tr_b16 a[16:17], v33 offset:55424\n" \
"s_waitcnt lgkmcnt(13)\n" \
"v_mfma_f32_16x16x32_bf16 v[128:131], a[20:23], a[32:35], v[128:131]\n" \
"buffer_load_dwordx4 a[116:119], v32, s[56:59], s93 offen offset:1024 sc1\n" \
"v_mfma_f32_16x16x32_bf16 v[132:135], a[20:23], a[48:51], v[132:135]\n" \
"ds_read_b64_tr_b16 a[18:19], v33 offset:60032\n" \
"ds_read_b64_tr_b16 a[20:21], v10 offset:128\n" \
"v_mfma_f32_16x16x32_bf16 v[128:131], a[24:27], a[36:39], v[128:131]\n" \
"ds_read_b64_tr_b16 a[22:23], v10 offset:4736\n" \
"v_mfma_f32_16x16x32_bf16 v[132:135], a[24:27], a[52:55], v[132:135]\n" \
"ds_read_b64_tr_b16 a[24:25], v34 offset:128\n" \
"ds_read_b64_tr_b16 a[26:27], v34 offset:4736\n" \
"v_mfma_f32_16x16x32_bf16 v[128:131], a[28:31], a[40:43], v[128:131]\n" \
"v_mfma_f32_16x16x32_bf16 v[132:135], a[28:31], a[56:59], v[132:135]\n" \
"buffer_load_dwordx4 a[104:107], v20, s[56:59], s93 offen offset:2048 sc1\n" \
"v_mfma_f32_16x16x32_bf16 v[128:131], a[192:195], a[44:47], v[128:131]\n" \
"ds_read_b64_tr_b16 a[28:29], v34 offset:9344\n" \
"ds_read_b64_tr_b16 a[30:31], v34 offset:13952\n" \
"v_mfma_f32_16x16x32_bf16 v[132:135], a[192:195], a[60:63], v[132:135]\n" \
"ds_read_b64_tr_b16 a[192:193], v34 offset:18560\n" \
"ds_read_b64_tr_b16 a[194:195], v34 offset:23168\n" \
"v_mfma_f32_16x16x32_bf16 v[136:139], a[196:199], a[128:131], v[136:139]\n" \
"v_mfma_f32_16x16x32_bf16 v[140:143], a[196:199], a[144:147], v[140:143]\n" \
"ds_read_b64_tr_b16 a[196:197], v10 offset:9344\n" \
"v_cvt_pk_bf16_f32 v128, v128, v129\n" \
"v_cvt_pk_bf16_f32 v129, v130, v131\n" \
"v_mfma_f32_16x16x32_bf16 v[136:139], a[200:203], a[132:135], v[136:139]\n" \
"v_cvt_pk_bf16_f32 v130, v132, v133\n" \
"v_cvt_pk_bf16_f32 v131, v134, v135\n" \
"buffer_store_dwordx4 v[128:131], v20, s[56:59], s67 offen offset:0 sc1\n" \
"s_add_u32 s67, s67, 0x2800\n" \
"v_mfma_f32_16x16x32_bf16 v[140:143], a[200:203], a[148:151], v[140:143]\n" \
"ds_read_b64_tr_b16 a[198:199], v10 offset:13952\n" \
"ds_read_b64_tr_b16 a[200:201], v33 offset:37024\n" \
"v_mfma_f32_16x16x32_bf16 v[136:139], a[204:207], a[136:139], v[136:139]\n" \
"ds_read_b64_tr_b16 a[202:203], v33 offset:41632\n" \
"v_mfma_f32_16x16x32_bf16 v[140:143], a[204:207], a[152:155], v[140:143]\n" \
"ds_read_b64_tr_b16 a[204:205], v33 offset:46240\n" \
"ds_read_b64_tr_b16 a[206:207], v33 offset:50848\n" \
"v_mfma_f32_16x16x32_bf16 v[136:139], a[208:211], a[140:143], v[136:139]\n" \
"buffer_load_dwordx4 a[120:123], v32, s[56:59], s93 offen offset:2048 sc1\n" \
"v_mfma_f32_16x16x32_bf16 v[140:143], a[208:211], a[156:159], v[140:143]\n" \
"ds_read_b64_tr_b16 a[208:209], v33 offset:55456\n" \
"s_waitcnt lgkmcnt(13)\n" \
"v_mfma_f32_16x16x32_bf16 v[136:139], a[212:215], a[32:35], v[136:139]\n" \
"ds_read_b64_tr_b16 a[210:211], v33 offset:60064\n" \
"v_mfma_f32_16x16x32_bf16 v[140:143], a[212:215], a[48:51], v[140:143]\n" \
"ds_read_b64_tr_b16 a[212:213], v10 offset:160\n" \
"ds_read_b64_tr_b16 a[214:215], v10 offset:4768\n" \
"v_mfma_f32_16x16x32_bf16 v[136:139], a[216:219], a[36:39], v[136:139]\n" \
"v_mfma_f32_16x16x32_bf16 v[140:143], a[216:219], a[52:55], v[140:143]\n" \
"buffer_load_dwordx4 a[108:111], v20, s[56:59], s93 offen offset:3072 sc1\n" \
"v_mfma_f32_16x16x32_bf16 v[136:139], a[0:3], a[40:43], v[136:139]\n" \
"ds_read_b64_tr_b16 a[216:217], v34 offset:160\n" \
"ds_read_b64_tr_b16 a[218:219], v34 offset:4768\n" \
"v_mfma_f32_16x16x32_bf16 v[140:143], a[0:3], a[56:59], v[140:143]\n" \
"ds_read_b64_tr_b16 a[0:1], v34 offset:9376\n" \
"ds_read_b64_tr_b16 a[2:3], v34 offset:13984\n" \
"v_mfma_f32_16x16x32_bf16 v[136:139], a[4:7], a[44:47], v[136:139]\n" \
"v_mfma_f32_16x16x32_bf16 v[140:143], a[4:7], a[60:63], v[140:143]\n" \
"ds_read_b64_tr_b16 a[4:5], v34 offset:18592\n" \
"ds_read_b64_tr_b16 a[6:7], v34 offset:23200\n" \
"buffer_load_dwordx4 a[124:127], v32, s[56:59], s93 offen offset:3072 sc1\n" \
"v_mfma_f32_16x16x32_bf16 v[144:147], a[8:11], a[128:131], v[144:147]\n" \
"v_readlane_b32 s93, v11, 47\n" \
"v_mfma_f32_16x16x32_bf16 v[148:151], a[8:11], a[144:147], v[148:151]\n" \
"ds_read_b64_tr_b16 a[8:9], v10 offset:9376\n" \
"v_cvt_pk_bf16_f32 v136, v136, v137\n" \
"v_cvt_pk_bf16_f32 v137, v138, v139\n" \
"v_cvt_pk_bf16_f32 v138, v140, v141\n" \
"v_cvt_pk_bf16_f32 v139, v142, v143\n" \
"buffer_store_dwordx4 v[136:139], v20, s[56:59], s68 offen offset:0 sc1\n" \
"s_add_u32 s68, s68, 0x2000\n" \
"v_mfma_f32_16x16x32_bf16 v[144:147], a[12:15], a[132:135], v[144:147]\n" \
"ds_read_b64_tr_b16 a[10:11], v10 offset:13984\n" \
"v_mfma_f32_16x16x32_bf16 v[148:151], a[12:15], a[148:151], v[148:151]\n" \
"ds_read_b64_tr_b16 a[12:13], v33 offset:37056\n" \
"ds_read_b64_tr_b16 a[14:15], v33 offset:41664\n" \
"v_mfma_f32_16x16x32_bf16 v[144:147], a[16:19], a[136:139], v[144:147]\n" \
"v_mfma_f32_16x16x32_bf16 v[148:151], a[16:19], a[152:155], v[148:151]\n" \
"ds_read_b64_tr_b16 a[16:17], v33 offset:46272\n" \
"ds_read_b64_tr_b16 a[18:19], v33 offset:50880\n" \
"v_mfma_f32_16x16x32_bf16 v[144:147], a[20:23], a[140:143], v[144:147]\n" \
"buffer_load_dwordx4 v[64:67], v20, s[56:59], s95 offen offset:0 sc1\n" \
"v_mfma_f32_16x16x32_bf16 v[148:151], a[20:23], a[156:159], v[148:151]\n" \
"ds_read_b64_tr_b16 a[20:21], v33 offset:55488\n" \
"s_waitcnt lgkmcnt(13)\n" \
"v_mfma_f32_16x16x32_bf16 v[144:147], a[24:27], a[32:35], v[144:147]\n" \
"ds_read_b64_tr_b16 a[22:23], v33 offset:60096\n" \
"v_mfma_f32_16x16x32_bf16 v[148:151], a[24:27], a[48:51], v[148:151]\n" \
"ds_read_b64_tr_b16 a[24:25], v10 offset:192\n" \
"ds_read_b64_tr_b16 a[26:27], v10 offset:4800\n" \
"v_mfma_f32_16x16x32_bf16 v[144:147], a[28:31], a[36:39], v[144:147]\n" \
"v_mfma_f32_16x16x32_bf16 v[148:151], a[28:31], a[52:55], v[148:151]\n" \
"ds_read_b64_tr_b16 a[28:29], v34 offset:192\n" \
"ds_read_b64_tr_b16 a[30:31], v34 offset:4800\n" \
"v_mfma_f32_16x16x32_bf16 v[144:147], a[192:195], a[40:43], v[144:147]\n" \
"buffer_load_dwordx4 v[68:71], v20, s[56:59], s95 offen offset:1024 sc1\n" \
"v_mfma_f32_16x16x32_bf16 v[148:151], a[192:195], a[56:59], v[148:151]\n" \
"ds_read_b64_tr_b16 a[192:193], v34 offset:9408\n" \
"ds_read_b64_tr_b16 a[194:195], v34 offset:14016\n" \
"v_mfma_f32_16x16x32_bf16 v[144:147], a[196:199], a[44:47], v[144:147]\n" \
"v_mfma_f32_16x16x32_bf16 v[148:151], a[196:199], a[60:63], v[148:151]\n" \
"ds_read_b64_tr_b16 a[196:197], v34 offset:18624\n" \
"ds_read_b64_tr_b16 a[198:199], v34 offset:23232\n" \
"v_mfma_f32_16x16x32_bf16 v[152:155], a[200:203], a[128:131], v[152:155]\n" \
"v_mfma_f32_16x16x32_bf16 v[156:159], a[200:203], a[144:147], v[156:159]\n" \
"ds_read_b64_tr_b16 a[200:201], v10 offset:9408\n" \
"ds_read_b64_tr_b16 a[202:203], v10 offset:14016\n" \
"v_mfma_f32_16x16x32_bf16 v[152:155], a[204:207], a[132:135], v[152:155]\n" \
"v_cvt_pk_bf16_f32 v144, v144, v145\n" \
"v_cvt_pk_bf16_f32 v145, v146, v147\n" \
"v_cvt_pk_bf16_f32 v146, v148, v149\n" \
"v_cvt_pk_bf16_f32 v147, v150, v151\n" \
"buffer_store_dwordx4 v[144:147], v20, s[56:59], s69 offen offset:0 sc1\n" \
"s_add_u32 s69, s69, 0x1800\n" \
"v_mfma_f32_16x16x32_bf16 v[156:159], a[204:207], a[148:151], v[156:159]\n" \
"ds_read_b64_tr_b16 a[204:205], v33 offset:256\n" \
"ds_read_b64_tr_b16 a[206:207], v33 offset:4864\n" \
"v_mfma_f32_16x16x32_bf16 v[152:155], a[208:211], a[136:139], v[152:155]\n" \
"v_mfma_f32_16x16x32_bf16 v[156:159], a[208:211], a[152:155], v[156:159]\n" \
"ds_read_b64_tr_b16 a[208:209], v33 offset:9472\n" \
"ds_read_b64_tr_b16 a[210:211], v33 offset:14080\n" \
"v_mfma_f32_16x16x32_bf16 v[152:155], a[212:215], a[140:143], v[152:155]\n" \
"buffer_load_dwordx4 v[72:75], v32, s[56:59], s95 offen offset:0 sc1\n" \
"v_mfma_f32_16x16x32_bf16 v[156:159], a[212:215], a[156:159], v[156:159]\n" \
"ds_read_b64_tr_b16 a[212:213], v33 offset:18688\n" \
"s_waitcnt lgkmcnt(13)\n" \
"v_mfma_f32_16x16x32_bf16 v[152:155], a[216:219], a[32:35], v[152:155]\n" \
"ds_read_b64_tr_b16 a[214:215], v33 offset:23296\n" \
"v_mfma_f32_16x16x32_bf16 v[156:159], a[216:219], a[48:51], v[156:159]\n" \
"ds_read_b64_tr_b16 a[216:217], v33 offset:27904\n" \
"ds_read_b64_tr_b16 a[218:219], v33 offset:32512\n" \
"v_mfma_f32_16x16x32_bf16 v[152:155], a[0:3], a[36:39], v[152:155]\n" \
"v_mfma_f32_16x16x32_bf16 v[156:159], a[0:3], a[52:55], v[156:159]\n" \
"ds_read_b64_tr_b16 a[0:1], v33 offset:37120\n" \
"ds_read_b64_tr_b16 a[2:3], v33 offset:41728\n" \
"v_mfma_f32_16x16x32_bf16 v[152:155], a[4:7], a[40:43], v[152:155]\n" \
"buffer_load_dwordx4 v[76:79], v32, s[56:59], s95 offen offset:1024 sc1\n" \
"v_mfma_f32_16x16x32_bf16 v[156:159], a[4:7], a[56:59], v[156:159]\n" \
"ds_read_b64_tr_b16 a[4:5], v33 offset:46336\n" \
"ds_read_b64_tr_b16 a[6:7], v33 offset:50944\n" \
"v_mfma_f32_16x16x32_bf16 v[152:155], a[8:11], a[44:47], v[152:155]\n" \
"v_readlane_b32 s95, v11, 49\n" \
"v_mfma_f32_16x16x32_bf16 v[156:159], a[8:11], a[60:63], v[156:159]\n" \
"ds_read_b64_tr_b16 a[8:9], v33 offset:55552\n" \
"ds_read_b64_tr_b16 a[10:11], v33 offset:60160\n" \
"v_mfma_f32_16x16x32_bf16 v[160:163], a[12:15], a[128:131], v[160:163]\n" \
"v_mfma_f32_16x16x32_bf16 v[164:167], a[12:15], a[144:147], v[164:167]\n" \
"ds_read_b64_tr_b16 a[12:13], v10 offset:256\n" \
"ds_read_b64_tr_b16 a[14:15], v10 offset:4864\n" \
"v_cvt_pk_bf16_f32 v152, v152, v153\n" \
"v_cvt_pk_bf16_f32 v153, v154, v155\n" \
"v_cvt_pk_bf16_f32 v154, v156, v157\n" \
"v_cvt_pk_bf16_f32 v155, v158, v159\n" \
"buffer_store_dwordx4 v[152:155], v20, s[56:59], s70 offen offset:0 sc1\n" \
"s_add_u32 s70, s70, 0x1000\n" \
"v_mfma_f32_16x16x32_bf16 v[160:163], a[16:19], a[132:135], v[160:163]\n" \
"v_mfma_f32_16x16x32_bf16 v[164:167], a[16:19], a[148:151], v[164:167]\n" \
"ds_read_b64_tr_b16 a[16:17], v34 offset:256\n" \
"ds_read_b64_tr_b16 a[18:19], v34 offset:4864\n" \
"v_mfma_f32_16x16x32_bf16 v[160:163], a[20:23], a[136:139], v[160:163]\n" \
"v_mfma_f32_16x16x32_bf16 v[164:167], a[20:23], a[152:155], v[164:167]\n" \
"ds_read_b64_tr_b16 a[20:21], v34 offset:9472\n" \
"ds_read_b64_tr_b16 a[22:23], v34 offset:14080\n" \
"v_mfma_f32_16x16x32_bf16 v[160:163], a[24:27], a[140:143], v[160:163]\n" \
"buffer_load_dwordx4 v[80:83], v20, s[56:59], s96 offen offset:0 sc1\n" \
"v_mfma_f32_16x16x32_bf16 v[164:167], a[24:27], a[156:159], v[164:167]\n" \
"ds_read_b64_tr_b16 a[24:25], v34 offset:18688\n" \
"s_waitcnt lgkmcnt(12)\n" \
"v_mfma_f32_16x16x32_bf16 v[160:163], a[28:31], a[32:35], v[160:163]\n" \
"ds_read_b64_tr_b16 a[26:27], v34 offset:23296\n" \
"v_mfma_f32_16x16x32_bf16 v[164:167], a[28:31], a[48:51], v[164:167]\n" \
"ds_read_b64_tr_b16 a[28:29], v10 offset:9472\n" \
"ds_read_b64_tr_b16 a[30:31], v10 offset:14080\n" \
"s_waitcnt vmcnt(18)\n" \
"s_mov_b64 exec, 1\n" \
"buffer_store_dword v39, off, s[56:59], s65 offset:0 sc1\n" \
"s_mov_b64 exec, -1\n" \
"v_mfma_f32_16x16x32_bf16 v[160:163], a[192:195], a[36:39], v[160:163]\n" \
"v_mfma_f32_16x16x32_bf16 v[164:167], a[192:195], a[52:55], v[164:167]\n" \
"ds_read_b64_tr_b16 a[192:193], v33 offset:0\n" \
"ds_read_b64_tr_b16 a[194:195], v33 offset:4608\n" \
"v_mfma_f32_16x16x32_bf16 v[160:163], a[196:199], a[40:43], v[160:163]\n" \
"v_mfma_f32_16x16x32_bf16 v[164:167], a[196:199], a[56:59], v[164:167]\n" \
"ds_read_b64_tr_b16 a[196:197], v33 offset:9216\n" \
"ds_read_b64_tr_b16 a[198:199], v33 offset:13824\n" \
"v_mfma_f32_16x16x32_bf16 v[160:163], a[200:203], a[44:47], v[160:163]\n" \
"buffer_load_dwordx4 v[84:87], v20, s[56:59], s96 offen offset:1024 sc1\n" \
"v_mfma_f32_16x16x32_bf16 v[164:167], a[200:203], a[60:63], v[164:167]\n" \
"ds_read_b64_tr_b16 a[200:201], v33 offset:18432\n" \
"ds_read_b64_tr_b16 a[202:203], v33 offset:23040\n" \
"v_mfma_f32_16x16x32_bf16 v[248:251], a[204:207], a[220:223], v[248:251]\n" \
"v_mfma_f32_16x16x32_bf16 v[252:255], a[204:207], a[236:239], v[252:255]\n" \
"ds_read_b64_tr_b16 a[204:205], v33 offset:27648\n" \
"ds_read_b64_tr_b16 a[206:207], v33 offset:32256\n" \
"v_cvt_pk_bf16_f32 v160, v160, v161\n" \
"v_cvt_pk_bf16_f32 v161, v162, v163\n" \
"v_cvt_pk_bf16_f32 v162, v164, v165\n" \
"v_cvt_pk_bf16_f32 v163, v166, v167\n" \
"buffer_store_dwordx4 v[160:163], v20, s[56:59], s71 offen offset:0 sc1\n" \
"s_add_u32 s71, s71, 0x800\n" \
"v_mfma_f32_16x16x32_bf16 v[248:251], a[208:211], a[224:227], v[248:251]\n" \
"v_mfma_f32_16x16x32_bf16 v[252:255], a[208:211], a[240:243], v[252:255]\n" \
"ds_read_b64_tr_b16 a[208:209], v33 offset:36864\n" \
"ds_read_b64_tr_b16 a[210:211], v33 offset:41472\n" \
"v_mfma_f32_16x16x32_bf16 v[248:251], a[212:215], a[228:231], v[248:251]\n" \
"v_mfma_f32_16x16x32_bf16 v[252:255], a[212:215], a[244:247], v[252:255]\n" \
"ds_read_b64_tr_b16 a[212:213], v33 offset:46080\n" \
"ds_read_b64_tr_b16 a[214:215], v33 offset:50688\n" \
"v_mfma_f32_16x16x32_bf16 v[248:251], a[216:219], a[232:235], v[248:251]\n" \
"buffer_load_dwordx4 v[88:91], v32, s[56:59], s96 offen offset:0 sc1\n" \
"v_mfma_f32_16x16x32_bf16 v[252:255], a[216:219], a[248:251], v[252:255]\n" \
"ds_read_b64_tr_b16 a[216:217], v33 offset:55296\n" \
"s_waitcnt lgkmcnt(12)\n" \
"v_mfma_f32_16x16x32_bf16 v[176:179], a[0:3], a[128:131], v[176:179]\n" \
"ds_read_b64_tr_b16 a[218:219], v33 offset:59904\n" \
"s_waitcnt vmcnt(18)\n" \
"s_mov_b64 exec, 1\n" \
"buffer_store_dword v39, off, s[56:59], s66 offset:4 sc1\n" \
"s_mov_b64 exec, -1\n" \
"v_mfma_f32_16x16x32_bf16 v[180:183], a[0:3], a[144:147], v[180:183]\n" \
"v_mfma_f32_16x16x32_bf16 v[248:251], a[0:3], a[160:163], v[248:251]\n" \
"v_mfma_f32_16x16x32_bf16 v[252:255], a[0:3], a[176:179], v[252:255]\n" \
"ds_read_b64_tr_b16 a[0:1], v10 offset:0\n" \
"ds_read_b64_tr_b16 a[2:3], v10 offset:4608\n" \
"v_mfma_f32_16x16x32_bf16 v[176:179], a[4:7], a[132:135], v[176:179]\n" \
"buffer_load_dwordx4 v[92:95], v32, s[56:59], s96 offen offset:1024 sc1\n" \
"v_mfma_f32_16x16x32_bf16 v[180:183], a[4:7], a[148:151], v[180:183]\n" \
"v_readlane_b32 s96, v11, 50\n" \
"v_mfma_f32_16x16x32_bf16 v[248:251], a[4:7], a[164:167], v[248:251]\n" \
"v_mfma_f32_16x16x32_bf16 v[252:255], a[4:7], a[180:183], v[252:255]\n" \
"ds_read_b64_tr_b16 a[4:5], v34 offset:0\n" \
"ds_read_b64_tr_b16 a[6:7], v34 offset:4608\n" \
"v_mfma_f32_16x16x32_bf16 v[176:179], a[8:11], a[136:139], v[176:179]\n" \
"v_mfma_f32_16x16x32_bf16 v[180:183], a[8:11], a[152:155], v[180:183]\n" \
"buffer_load_dwordx4 v[96:99], v20, s[56:59], s97 offen offset:0 sc1\n" \
"v_mfma_f32_16x16x32_bf16 v[248:251], a[8:11], a[168:171], v[248:251]\n" \
"v_mfma_f32_16x16x32_bf16 v[252:255], a[8:11], a[184:187], v[252:255]\n" \
"ds_read_b64_tr_b16 a[8:9], v34 offset:9216\n" \
"ds_read_b64_tr_b16 a[10:11], v34 offset:13824\n" \
"v_mfma_f32_16x16x32_bf16 v[176:179], a[12:15], a[140:143], v[176:179]\n" \
"v_mfma_f32_16x16x32_bf16 v[180:183], a[12:15], a[156:159], v[180:183]\n" \
"v_mfma_f32_16x16x32_bf16 v[248:251], a[12:15], a[172:175], v[248:251]\n" \
"buffer_load_dwordx4 v[104:107], v32, s[56:59], s97 offen offset:0 sc1\n" \
"v_mfma_f32_16x16x32_bf16 v[252:255], a[12:15], a[188:191], v[252:255]\n" \
"ds_read_b64_tr_b16 a[12:13], v34 offset:18432\n" \
"ds_read_b64_tr_b16 a[14:15], v34 offset:23040\n" \
"s_waitcnt vmcnt(19)\n" \
"s_mov_b64 exec, 1\n" \
"buffer_store_dword v39, off, s[56:59], s67 offset:8 sc1\n" \
"s_mov_b64 exec, -1\n" \
"v_mfma_f32_16x16x32_bf16 v[176:179], a[16:19], a[32:35], v[176:179]\n" \
"v_mfma_f32_16x16x32_bf16 v[180:183], a[16:19], a[48:51], v[180:183]\n" \
"v_mfma_f32_16x16x32_bf16 v[248:251], a[16:19], a[96:99], v[248:251]\n" \
"v_mfma_f32_16x16x32_bf16 v[252:255], a[16:19], a[112:115], v[252:255]\n" \
"ds_read_b64_tr_b16 a[16:17], v10 offset:9216\n" \
"ds_read_b64_tr_b16 a[18:19], v10 offset:13824\n" \
"v_mfma_f32_16x16x32_bf16 v[176:179], a[20:23], a[36:39], v[176:179]\n" \
"buffer_load_dwordx4 v[100:103], v20, s[56:59], s97 offen offset:1024 sc1\n" \
"v_mfma_f32_16x16x32_bf16 v[180:183], a[20:23], a[52:55], v[180:183]\n" \
"v_mfma_f32_16x16x32_bf16 v[248:251], a[20:23], a[100:103], v[248:251]\n" \
"v_mfma_f32_16x16x32_bf16 v[252:255], a[20:23], a[116:119], v[252:255]\n" \
"ds_read_b64_tr_b16 a[20:21], v33 offset:32\n" \
"ds_read_b64_tr_b16 a[22:23], v33 offset:4640\n" \
"v_mfma_f32_16x16x32_bf16 v[176:179], a[24:27], a[40:43], v[176:179]\n" \
"v_mfma_f32_16x16x32_bf16 v[180:183], a[24:27], a[56:59], v[180:183]\n" \
"buffer_load_dwordx4 v[108:111], v32, s[56:59], s97 offen offset:1024 sc1\n" \
"v_mfma_f32_16x16x32_bf16 v[248:251], a[24:27], a[104:107], v[248:251]\n" \
"s_waitcnt vmcnt(21)\n" \
"v_mfma_f32_16x16x32_bf16 v[252:255], a[24:27], a[120:123], v[252:255]\n" \
"ds_read_b64_tr_b16 a[24:25], v33 offset:9248\n" \
"ds_read_b64_tr_b16 a[26:27], v33 offset:13856\n" \
"v_mfma_f32_16x16x32_bf16 v[176:179], a[28:31], a[44:47], v[176:179]\n" \
"v_mfma_f32_16x16x32_bf16 v[180:183], a[28:31], a[60:63], v[180:183]\n" \
"s_waitcnt vmcnt(20)\n" \
".fill (L_ab + 40960 - .) / 4, 4, 0xBF800000\n" \
"L_ap10:\n" \
"s_cbranch_execz L_ap0\n" \
"v_mfma_f32_16x16x32_bf16 v[248:251], a[28:31], a[108:111], v[248:251]\n" \
"s_waitcnt vmcnt(19)\n" \
"v_mfma_f32_16x16x32_bf16 v[252:255], a[28:31], a[124:127], v[252:255]\n" \
"ds_read_b64_tr_b16 a[28:29], v33 offset:18464\n" \
"s_waitcnt vmcnt(18)\n" \
"s_mov_b64 exec, 1\n" \
"buffer_store_dword v39, off, s[56:59], s68 offset:12 sc1\n" \
"s_mov_b64 exec, -1\n" \
"v_cvt_pk_bf16_f32 v176, v176, v177\n" \
"v_cvt_pk_bf16_f32 v248, v248, v249\n" \
"v_cvt_pk_bf16_f32 v177, v178, v179\n" \
"v_cvt_pk_bf16_f32 v249, v250, v251\n" \
"v_cvt_pk_bf16_f32 v178, v180, v181\n" \
"v_cvt_pk_bf16_f32 v179, v182, v183\n" \
"v_cvt_pk_bf16_f32 v250, v252, v253\n" \
"v_cvt_pk_bf16_f32 v251, v254, v255\n" \
"buffer_store_dwordx4 v[176:179], v20, s[56:59], s72 offen offset:0 sc1\n" \
"s_waitcnt lgkmcnt(5)\n" \
"v_mfma_f32_16x16x32_bf16 v[184:187], a[192:195], a[220:223], v[184:187]\n" \
"ds_read_b64_tr_b16 a[30:31], v33 offset:23072\n" \
"v_mfma_f32_16x16x32_bf16 v[188:191], a[192:195], a[236:239], v[188:191]\n" \
"ds_read_b64_tr_b16 a[192:193], v33 offset:27680\n" \
"ds_read_b64_tr_b16 a[194:195], v33 offset:32288\n" \
"buffer_store_dwordx4 v[248:251], v20, s[56:59], s72 offen offset:1024 sc1\n" \
"v_mfma_f32_16x16x32_bf16 v[184:187], a[196:199], a[224:227], v[184:187]\n" \
"v_readlane_b32 s72, v11, 52\n" \
"v_mfma_f32_16x16x32_bf16 v[188:191], a[196:199], a[240:243], v[188:191]\n" \
"ds_read_b64_tr_b16 a[196:197], v33 offset:36896\n" \
"ds_read_b64_tr_b16 a[198:199], v33 offset:41504\n" \
"v_mfma_f32_16x16x32_bf16 v[184:187], a[200:203], a[228:231], v[184:187]\n" \
"v_mfma_f32_16x16x32_bf16 v[188:191], a[200:203], a[244:247], v[188:191]\n" \
"ds_read_b64_tr_b16 a[200:201], v33 offset:46112\n" \
"ds_read_b64_tr_b16 a[202:203], v33 offset:50720\n" \
"v_mfma_f32_16x16x32_bf16 v[184:187], a[204:207], a[232:235], v[184:187]\n" \
"v_mfma_f32_16x16x32_bf16 v[188:191], a[204:207], a[248:251], v[188:191]\n" \
"ds_read_b64_tr_b16 a[204:205], v33 offset:55328\n" \
"ds_read_b64_tr_b16 a[206:207], v33 offset:59936\n" \
"v_mfma_f32_16x16x32_bf16 v[184:187], a[208:211], a[160:163], v[184:187]\n" \
"v_mfma_f32_16x16x32_bf16 v[188:191], a[208:211], a[176:179], v[188:191]\n" \
"ds_read_b64_tr_b16 a[208:209], v10 offset:32\n" \
"ds_read_b64_tr_b16 a[210:211], v10 offset:4640\n" \
"v_mfma_f32_16x16x32_bf16 v[184:187], a[212:215], a[164:167], v[184:187]\n" \
"v_mfma_f32_16x16x32_bf16 v[188:191], a[212:215], a[180:183], v[188:191]\n" \
"ds_read_b64_tr_b16 a[212:213], v34 offset:32\n" \
"ds_read_b64_tr_b16 a[214:215], v34 offset:4640\n" \
"v_mfma_f32_16x16x32_bf16 v[184:187], a[216:219], a[168:171], v[184:187]\n" \
"v_mfma_f32_16x16x32_bf16 v[188:191], a[216:219], a[184:187], v[188:191]\n" \
"ds_read_b64_tr_b16 a[216:217], v34 offset:9248\n" \
"ds_read_b64_tr_b16 a[218:219], v34 offset:13856\n" \
"s_waitcnt vmcnt(18)\n" \
"s_mov_b64 exec, 1\n" \
"buffer_store_dword v39, off, s[56:59], s69 offset:16 sc1\n" \
"s_mov_b64 exec, -1\n" \
"v_mfma_f32_16x16x32_bf16 v[184:187], a[0:3], a[172:175], v[184:187]\n" \
"v_mfma_f32_16x16x32_bf16 v[188:191], a[0:3], a[188:191], v[188:191]\n" \
"ds_read_b64_tr_b16 a[0:1], v34 offset:18464\n" \
"ds_read_b64_tr_b16 a[2:3], v34 offset:23072\n" \
"v_mfma_f32_16x16x32_bf16 v[184:187], a[4:7], a[96:99], v[184:187]\n" \
"v_mfma_f32_16x16x32_bf16 v[188:191], a[4:7], a[112:115], v[188:191]\n" \
"ds_read_b64_tr_b16 a[4:5], v10 offset:9248\n" \
"ds_read_b64_tr_b16 a[6:7], v10 offset:13856\n" \
"v_mfma_f32_16x16x32_bf16 v[184:187], a[8:11], a[100:103], v[184:187]\n" \
"v_mfma_f32_16x16x32_bf16 v[188:191], a[8:11], a[116:119], v[188:191]\n" \
"ds_read_b64_tr_b16 a[8:9], v33 offset:64\n" \
"ds_read_b64_tr_b16 a[10:11], v33 offset:4672\n" \
"v_mfma_f32_16x16x32_bf16 v[184:187], a[12:15], a[104:107], v[184:187]\n" \
"v_mfma_f32_16x16x32_bf16 v[188:191], a[12:15], a[120:123], v[188:191]\n" \
"ds_read_b64_tr_b16 a[12:13], v33 offset:9280\n" \
"ds_read_b64_tr_b16 a[14:15], v33 offset:13888\n" \
"v_mfma_f32_16x16x32_bf16 v[184:187], a[16:19], a[108:111], v[184:187]\n" \
"v_mfma_f32_16x16x32_bf16 v[188:191], a[16:19], a[124:127], v[188:191]\n" \
"ds_read_b64_tr_b16 a[16:17], v33 offset:18496\n" \
"s_waitcnt lgkmcnt(13)\n" \
"v_mfma_f32_16x16x32_bf16 v[192:195], a[20:23], a[220:223], v[192:195]\n" \
"ds_read_b64_tr_b16 a[18:19], v33 offset:23104\n" \
"v_mfma_f32_16x16x32_bf16 v[196:199], a[20:23], a[236:239], v[196:199]\n" \
"ds_read_b64_tr_b16 a[20:21], v33 offset:27712\n" \
"ds_read_b64_tr_b16 a[22:23], v33 offset:32320\n" \
"v_cvt_pk_bf16_f32 v184, v184, v185\n" \
"v_cvt_pk_bf16_f32 v185, v186, v187\n" \
"v_cvt_pk_bf16_f32 v186, v188, v189\n" \
"v_cvt_pk_bf16_f32 v187, v190, v191\n" \
"buffer_store_dwordx4 v[184:187], v20, s[56:59], s92 offen offset:1024 sc1\n" \
"s_add_u32 s92, s92, 0x3800\n" \
"v_mfma_f32_16x16x32_bf16 v[192:195], a[24:27], a[224:227], v[192:195]\n" \
"v_mfma_f32_16x16x32_bf16 v[196:199], a[24:27], a[240:243], v[196:199]\n" \
"ds_read_b64_tr_b16 a[24:25], v33 offset:36928\n" \
"ds_read_b64_tr_b16 a[26:27], v33 offset:41536\n" \
"s_waitcnt vmcnt(17)\n" \
"s_mov_b64 exec, 1\n" \
"buffer_store_dword v39, off, s[56:59], s70 offset:20 sc1\n" \
"s_mov_b64 exec, -1\n" \
"v_mfma_f32_16x16x32_bf16 v[192:195], a[28:31], a[228:231], v[192:195]\n" \
"v_readlane_b32 s70, v11, 42\n" \
"v_mfma_f32_16x16x32_bf16 v[196:199], a[28:31], a[244:247], v[196:199]\n" \
"ds_read_b64_tr_b16 a[28:29], v33 offset:46144\n" \
"ds_read_b64_tr_b16 a[30:31], v33 offset:50752\n" \
"v_mfma_f32_16x16x32_bf16 v[192:195], a[192:195], a[232:235], v[192:195]\n" \
"v_mfma_f32_16x16x32_bf16 v[196:199], a[192:195], a[248:251], v[196:199]\n" \
"ds_read_b64_tr_b16 a[192:193], v33 offset:55360\n" \
"ds_read_b64_tr_b16 a[194:195], v33 offset:59968\n" \
"v_mfma_f32_16x16x32_bf16 v[192:195], a[196:199], a[160:163], v[192:195]\n" \
"v_mfma_f32_16x16x32_bf16 v[196:199], a[196:199], a[176:179], v[196:199]\n" \
"ds_read_b64_tr_b16 a[196:197], v10 offset:64\n" \
"ds_read_b64_tr_b16 a[198:199], v10 offset:4672\n" \
"v_mfma_f32_16x16x32_bf16 v[192:195], a[200:203], a[164:167], v[192:195]\n" \
"v_mfma_f32_16x16x32_bf16 v[196:199], a[200:203], a[180:183], v[196:199]\n" \
"ds_read_b64_tr_b16 a[200:201], v34 offset:64\n" \
"ds_read_b64_tr_b16 a[202:203], v34 offset:4672\n" \
"v_mfma_f32_16x16x32_bf16 v[192:195], a[204:207], a[168:171], v[192:195]\n" \
"v_mfma_f32_16x16x32_bf16 v[196:199], a[204:207], a[184:187], v[196:199]\n" \
"ds_read_b64_tr_b16 a[204:205], v34 offset:9280\n" \
"ds_read_b64_tr_b16 a[206:207], v34 offset:13888\n" \
"v_mfma_f32_16x16x32_bf16 v[192:195], a[208:211], a[172:175], v[192:195]\n" \
"v_mfma_f32_16x16x32_bf16 v[196:199], a[208:211], a[188:191], v[196:199]\n" \
"ds_read_b64_tr_b16 a[208:209], v34 offset:18496\n" \
"s_waitcnt lgkmcnt(13)\n" \
"v_mfma_f32_16x16x32_bf16 v[192:195], a[212:215], a[96:99], v[192:195]\n" \
"ds_read_b64_tr_b16 a[210:211], v34 offset:23104\n" \
"v_mfma_f32_16x16x32_bf16 v[196:199], a[212:215], a[112:115], v[196:199]\n" \
"ds_read_b64_tr_b16 a[212:213], v10 offset:9280\n" \
"ds_read_b64_tr_b16 a[214:215], v10 offset:13888\n" \
"v_mfma_f32_16x16x32_bf16 v[192:195], a[216:219], a[100:103], v[192:195]\n" \
"v_mfma_f32_16x16x32_bf16 v[196:199], a[216:219], a[116:119], v[196:199]\n" \
"ds_read_b64_tr_b16 a[216:217], v33 offset:96\n" \
"ds_read_b64_tr_b16 a[218:219], v33 offset:4704\n" \
"v_mfma_f32_16x16x32_bf16 v[192:195], a[0:3], a[104:107], v[192:195]\n" \
"v_mfma_f32_16x16x32_bf16 v[196:199], a[0:3], a[120:123], v[196:199]\n" \
"ds_read_b64_tr_b16 a[0:1], v33 offset:9312\n" \
"ds_read_b64_tr_b16 a[2:3], v33 offset:13920\n" \
"s_waitcnt vmcnt(14)\n" \
"s_mov_b64 exec, 1\n" \
"buffer_store_dword v39, off, s[56:59], s71 offset:24 sc1\n" \
"s_mov_b64 exec, -1\n" \
"v_mfma_f32_16x16x32_bf16 v[192:195], a[4:7], a[108:111], v[192:195]\n" \
"v_readlane_b32 s71, v11, 51\n" \
"v_mfma_f32_16x16x32_bf16 v[196:199], a[4:7], a[124:127], v[196:199]\n" \
"ds_read_b64_tr_b16 a[4:5], v33 offset:18528\n" \
"ds_read_b64_tr_b16 a[6:7], v33 offset:23136\n" \
"v_mfma_f32_16x16x32_bf16 v[200:203], a[8:11], a[220:223], v[200:203]\n" \
"v_mfma_f32_16x16x32_bf16 v[204:207], a[8:11], a[236:239], v[204:207]\n" \
"ds_read_b64_tr_b16 a[8:9], v33 offset:27744\n" \
"ds_read_b64_tr_b16 a[10:11], v33 offset:32352\n" \
"v_cvt_pk_bf16_f32 v192, v192, v193\n" \
"v_cvt_pk_bf16_f32 v193, v194, v195\n" \
"v_cvt_pk_bf16_f32 v194, v196, v197\n" \
"v_cvt_pk_bf16_f32 v195, v198, v199\n" \
"buffer_store_dwordx4 v[192:195], v20, s[56:59], s93 offen offset:1024 sc1\n" \
"s_add_u32 s93, s93, 0x3000\n" \
"v_mfma_f32_16x16x32_bf16 v[200:203], a[12:15], a[224:227], v[200:203]\n" \
"v_mfma_f32_16x16x32_bf16 v[204:207], a[12:15], a[240:243], v[204:207]\n" \
"ds_read_b64_tr_b16 a[12:13], v33 offset:36960\n" \
"ds_read_b64_tr_b16 a[14:15], v33 offset:41568\n" \
"v_mfma_f32_16x16x32_bf16 v[200:203], a[16:19], a[228:231], v[200:203]\n" \
"v_mfma_f32_16x16x32_bf16 v[204:207], a[16:19], a[244:247], v[204:207]\n" \
"ds_read_b64_tr_b16 a[16:17], v33 offset:46176\n" \
"ds_read_b64_tr_b16 a[18:19], v33 offset:50784\n" \
"v_mfma_f32_16x16x32_bf16 v[200:203], a[20:23], a[232:235], v[200:203]\n" \
"v_mfma_f32_16x16x32_bf16 v[204:207], a[20:23], a[248:251], v[204:207]\n" \
"ds_read_b64_tr_b16 a[20:21], v33 offset:55392\n" \
"s_waitcnt lgkmcnt(11)\n" \
"v_mfma_f32_16x16x32_bf16 v[200:203], a[24:27], a[160:163], v[200:203]\n" \
"ds_read_b64_tr_b16 a[22:23], v33 offset:60000\n" \
"v_mfma_f32_16x16x32_bf16 v[204:207], a[24:27], a[176:179], v[204:207]\n" \
"ds_read_b64_tr_b16 a[24:25], v10 offset:96\n" \
"ds_read_b64_tr_b16 a[26:27], v10 offset:4704\n" \
"v_mfma_f32_16x16x32_bf16 v[200:203], a[28:31], a[164:167], v[200:203]\n" \
"v_mfma_f32_16x16x32_bf16 v[204:207], a[28:31], a[180:183], v[204:207]\n" \
"ds_read_b64_tr_b16 a[28:29], v34 offset:96\n" \
"ds_read_b64_tr_b16 a[30:31], v34 offset:4704\n" \
"v_mfma_f32_16x16x32_bf16 v[200:203], a[192:195], a[168:171], v[200:203]\n" \
"v_mfma_f32_16x16x32_bf16 v[204:207], a[192:195], a[184:187], v[204:207]\n" \
"ds_read_b64_tr_b16 a[192:193], v34 offset:9312\n" \
"ds_read_b64_tr_b16 a[194:195], v34 offset:13920\n" \
"v_mfma_f32_16x16x32_bf16 v[200:203], a[196:199], a[172:175], v[200:203]\n" \
"v_mfma_f32_16x16x32_bf16 v[204:207], a[196:199], a[188:191], v[204:207]\n" \
"ds_read_b64_tr_b16 a[196:197], v34 offset:18528\n" \
"ds_read_b64_tr_b16 a[198:199], v34 offset:23136\n" \
"v_mfma_f32_16x16x32_bf16 v[200:203], a[200:203], a[96:99], v[200:203]\n" \
"v_mfma_f32_16x16x32_bf16 v[204:207], a[200:203], a[112:115], v[204:207]\n" \
"ds_read_b64_tr_b16 a[200:201], v10 offset:9312\n" \
"ds_read_b64_tr_b16 a[202:203], v10 offset:13920\n" \
"v_mfma_f32_16x16x32_bf16 v[200:203], a[204:207], a[100:103], v[200:203]\n" \
"v_mfma_f32_16x16x32_bf16 v[204:207], a[204:207], a[116:119], v[204:207]\n" \
"ds_read_b64_tr_b16 a[204:205], v33 offset:128\n" \
"ds_read_b64_tr_b16 a[206:207], v33 offset:4736\n" \
"v_mfma_f32_16x16x32_bf16 v[200:203], a[208:211], a[104:107], v[200:203]\n" \
"v_mfma_f32_16x16x32_bf16 v[204:207], a[208:211], a[120:123], v[204:207]\n" \
"ds_read_b64_tr_b16 a[208:209], v33 offset:9344\n" \
"ds_read_b64_tr_b16 a[210:211], v33 offset:13952\n" \
"v_mfma_f32_16x16x32_bf16 v[200:203], a[212:215], a[108:111], v[200:203]\n" \
"v_mfma_f32_16x16x32_bf16 v[204:207], a[212:215], a[124:127], v[204:207]\n" \
"ds_read_b64_tr_b16 a[212:213], v33 offset:18560\n" \
"ds_read_b64_tr_b16 a[214:215], v33 offset:23168\n" \
"v_mfma_f32_16x16x32_bf16 v[208:211], a[216:219], a[220:223], v[208:211]\n" \
"v_mfma_f32_16x16x32_bf16 v[212:215], a[216:219], a[236:239], v[212:215]\n" \
"ds_read_b64_tr_b16 a[216:217], v33 offset:27776\n" \
"s_waitcnt lgkmcnt(13)\n" \
"v_mfma_f32_16x16x32_bf16 v[208:211], a[0:3], a[224:227], v[208:211]\n" \
"v_cvt_pk_bf16_f32 v200, v200, v201\n" \
"v_cvt_pk_bf16_f32 v201, v202, v203\n" \
"v_cvt_pk_bf16_f32 v202, v204, v205\n" \
"v_cvt_pk_bf16_f32 v203, v206, v207\n" \
"buffer_store_dwordx4 v[200:203], v20, s[56:59], s94 offen offset:1024 sc1\n" \
"s_add_u32 s94, s94, 0x2800\n" \
"v_mfma_f32_16x16x32_bf16 v[212:215], a[0:3], a[240:243], v[212:215]\n" \
"ds_read_b64_tr_b16 a[218:219], v33 offset:32384\n" \
"ds_read_b64_tr_b16 a[0:1], v33 offset:36992\n" \
"v_mfma_f32_16x16x32_bf16 v[208:211], a[4:7], a[228:231], v[208:211]\n" \
"ds_read_b64_tr_b16 a[2:3], v33 offset:41600\n" \
"v_mfma_f32_16x16x32_bf16 v[212:215], a[4:7], a[244:247], v[212:215]\n" \
"ds_read_b64_tr_b16 a[4:5], v33 offset:46208\n" \
"ds_read_b64_tr_b16 a[6:7], v33 offset:50816\n" \
"v_mfma_f32_16x16x32_bf16 v[208:211], a[8:11], a[232:235], v[208:211]\n" \
"v_mfma_f32_16x16x32_bf16 v[212:215], a[8:11], a[248:251], v[212:215]\n" \
"ds_read_b64_tr_b16 a[8:9], v33 offset:55424\n" \
"ds_read_b64_tr_b16 a[10:11], v33 offset:60032\n" \
"v_mfma_f32_16x16x32_bf16 v[208:211], a[12:15], a[160:163], v[208:211]\n" \
"v_mfma_f32_16x16x32_bf16 v[212:215], a[12:15], a[176:179], v[212:215]\n" \
"ds_read_b64_tr_b16 a[12:13], v10 offset:128\n" \
"ds_read_b64_tr_b16 a[14:15], v10 offset:4736\n" \
"v_mfma_f32_16x16x32_bf16 v[208:211], a[16:19], a[164:167], v[208:211]\n" \
"v_mfma_f32_16x16x32_bf16 v[212:215], a[16:19], a[180:183], v[212:215]\n" \
"ds_read_b64_tr_b16 a[16:17], v34 offset:128\n" \
"ds_read_b64_tr_b16 a[18:19], v34 offset:4736\n" \
"v_mfma_f32_16x16x32_bf16 v[208:211], a[20:23], a[168:171], v[208:211]\n" \
"v_mfma_f32_16x16x32_bf16 v[212:215], a[20:23], a[184:187], v[212:215]\n" \
"ds_read_b64_tr_b16 a[20:21], v34 offset:9344\n" \
"ds_read_b64_tr_b16 a[22:23], v34 offset:13952\n" \
"v_mfma_f32_16x16x32_bf16 v[208:211], a[24:27], a[172:175], v[208:211]\n" \
"v_mfma_f32_16x16x32_bf16 v[212:215], a[24:27], a[188:191], v[212:215]\n" \
"ds_read_b64_tr_b16 a[24:25], v34 offset:18560\n" \
"ds_read_b64_tr_b16 a[26:27], v34 offset:23168\n" \
"s_waitcnt vmcnt(6)\n" \
"s_mov_b64 exec, 3\n" \
"buffer_store_dword v39, v41, s[56:59], s73 offen sc1\n" \
"s_mov_b64 exec, -1\n" \
"v_mfma_f32_16x16x32_bf16 v[208:211], a[28:31], a[96:99], v[208:211]\n" \
"v_mfma_f32_16x16x32_bf16 v[212:215], a[28:31], a[112:115], v[212:215]\n" \
"ds_read_b64_tr_b16 a[28:29], v10 offset:9344\n" \
"s_waitcnt lgkmcnt(13)\n" \
"v_mfma_f32_16x16x32_bf16 v[208:211], a[192:195], a[100:103], v[208:211]\n" \
"ds_read_b64_tr_b16 a[30:31], v10 offset:13952\n" \
"v_mfma_f32_16x16x32_bf16 v[212:215], a[192:195], a[116:119], v[212:215]\n" \
"ds_read_b64_tr_b16 a[192:193], v33 offset:160\n" \
"ds_read_b64_tr_b16 a[194:195], v33 offset:4768\n" \
"v_mfma_f32_16x16x32_bf16 v[208:211], a[196:199], a[104:107], v[208:211]\n" \
"v_mfma_f32_16x16x32_bf16 v[212:215], a[196:199], a[120:123], v[212:215]\n" \
"ds_read_b64_tr_b16 a[196:197], v33 offset:9376\n" \
"ds_read_b64_tr_b16 a[198:199], v33 offset:13984\n" \
"v_mfma_f32_16x16x32_bf16 v[208:211], a[200:203], a[108:111], v[208:211]\n" \
"v_mfma_f32_16x16x32_bf16 v[212:215], a[200:203], a[124:127], v[212:215]\n" \
"ds_read_b64_tr_b16 a[200:201], v33 offset:18592\n" \
"ds_read_b64_tr_b16 a[202:203], v33 offset:23200\n" \
"v_mfma_f32_16x16x32_bf16 v[216:219], a[204:207], a[220:223], v[216:219]\n" \
"v_mfma_f32_16x16x32_bf16 v[220:223], a[204:207], a[236:239], v[220:223]\n" \
"ds_read_b64_tr_b16 a[204:205], v33 offset:27808\n" \
"ds_read_b64_tr_b16 a[206:207], v33 offset:32416\n" \
"v_mfma_f32_16x16x32_bf16 v[216:219], a[208:211], a[224:227], v[216:219]\n" \
"v_cvt_pk_bf16_f32 v208, v208, v209\n" \
"v_cvt_pk_bf16_f32 v209, v210, v211\n" \
"v_cvt_pk_bf16_f32 v210, v212, v213\n" \
"v_cvt_pk_bf16_f32 v211, v214, v215\n" \
"buffer_store_dwordx4 v[208:211], v20, s[56:59], s95 offen offset:1024 sc1\n" \
"s_add_u32 s95, s95, 0x2000\n" \
"v_mfma_f32_16x16x32_bf16 v[220:223], a[208:211], a[240:243], v[220:223]\n" \
"ds_read_b64_tr_b16 a[208:209], v33 offset:37024\n" \
"ds_read_b64_tr_b16 a[210:211], v33 offset:41632\n" \
"v_mfma_f32_16x16x32_bf16 v[216:219], a[212:215], a[228:231], v[216:219]\n" \
"v_mfma_f32_16x16x32_bf16 v[220:223], a[212:215], a[244:247], v[220:223]\n" \
"ds_read_b64_tr_b16 a[212:213], v33 offset:46240\n" \
"ds_read_b64_tr_b16 a[214:215], v33 offset:50848\n" \
"v_mfma_f32_16x16x32_bf16 v[216:219], a[216:219], a[232:235], v[216:219]\n" \
"v_mfma_f32_16x16x32_bf16 v[220:223], a[216:219], a[248:251], v[220:223]\n" \
"ds_read_b64_tr_b16 a[216:217], v33 offset:55456\n" \
"ds_read_b64_tr_b16 a[218:219], v33 offset:60064\n" \
"v_mfma_f32_16x16x32_bf16 v[216:219], a[0:3], a[160:163], v[216:219]\n" \
"v_mfma_f32_16x16x32_bf16 v[220:223], a[0:3], a[176:179], v[220:223]\n" \
"ds_read_b64_tr_b16 a[0:1], v10 offset:160\n" \
"s_waitcnt lgkmcnt(13)\n" \
"v_mfma_f32_16x16x32_bf16 v[216:219], a[4:7], a[164:167], v[216:219]\n" \
"ds_read_b64_tr_b16 a[2:3], v10 offset:4768\n" \
"v_mfma_f32_16x16x32_bf16 v[220:223], a[4:7], a[180:183], v[220:223]\n" \
"ds_read_b64_tr_b16 a[4:5], v34 offset:160\n" \
"ds_read_b64_tr_b16 a[6:7], v34 offset:4768\n" \
"v_mfma_f32_16x16x32_bf16 v[216:219], a[8:11], a[168:171], v[216:219]\n" \
"v_mfma_f32_16x16x32_bf16 v[220:223], a[8:11], a[184:187], v[220:223]\n" \
"ds_read_b64_tr_b16 a[8:9], v34 offset:9376\n" \
"ds_read_b64_tr_b16 a[10:11], v34 offset:13984\n" \
"v_mfma_f32_16x16x32_bf16 v[216:219], a[12:15], a[172:175], v[216:219]\n" \
"v_mfma_f32_16x16x32_bf16 v[220:223], a[12:15], a[188:191], v[220:223]\n" \
"ds_read_b64_tr_b16 a[12:13], v34 offset:18592\n" \
"ds_read_b64_tr_b16 a[14:15], v34 offset:23200\n" \
"s_waitcnt vmcnt(6)\n" \
"s_mov_b64 exec, 1\n" \
"buffer_store_dword v39, off, s[56:59], s92 offset:32 sc1\n" \
"s_mov_b64 exec, -1\n" \
"v_mfma_f32_16x16x32_bf16 v[216:219], a[16:19], a[96:99], v[216:219]\n" \
"v_mfma_f32_16x16x32_bf16 v[220:223], a[16:19], a[112:115], v[220:223]\n" \
"ds_read_b64_tr_b16 a[16:17], v10 offset:9376\n" \
"ds_read_b64_tr_b16 a[18:19], v10 offset:13984\n" \
"v_mfma_f32_16x16x32_bf16 v[216:219], a[20:23], a[100:103], v[216:219]\n" \
"v_mfma_f32_16x16x32_bf16 v[220:223], a[20:23], a[116:119], v[220:223]\n" \
"ds_read_b64_tr_b16 a[20:21], v33 offset:192\n" \
"ds_read_b64_tr_b16 a[22:23], v33 offset:4800\n" \
"v_mfma_f32_16x16x32_bf16 v[216:219], a[24:27], a[104:107], v[216:219]\n" \
"v_mfma_f32_16x16x32_bf16 v[220:223], a[24:27], a[120:123], v[220:223]\n" \
"ds_read_b64_tr_b16 a[24:25], v33 offset:9408\n" \
"ds_read_b64_tr_b16 a[26:27], v33 offset:14016\n" \
"v_mfma_f32_16x16x32_bf16 v[216:219], a[28:31], a[108:111], v[216:219]\n" \
"v_mfma_f32_16x16x32_bf16 v[220:223], a[28:31], a[124:127], v[220:223]\n" \
"buffer_load_dword v37, v40, s[56:59], s74 offen sc1\n" \
"v_mfma_f32_16x16x32_bf16 v[224:227], a[192:195], a[220:223], v[224:227]\n" \
"ds_read_b64_tr_b16 a[28:29], v33 offset:18624\n" \
"ds_read_b64_tr_b16 a[30:31], v33 offset:23232\n" \
"v_mfma_f32_16x16x32_bf16 v[228:231], a[192:195], a[236:239], v[228:231]\n" \
"ds_read_b64_tr_b16 a[192:193], v33 offset:27840\n" \
"s_waitcnt lgkmcnt(13)\n" \
"v_mfma_f32_16x16x32_bf16 v[224:227], a[196:199], a[224:227], v[224:227]\n" \
"v_cvt_pk_bf16_f32 v216, v216, v217\n" \
"v_cvt_pk_bf16_f32 v217, v218, v219\n" \
"v_cvt_pk_bf16_f32 v218, v220, v221\n" \
"v_cvt_pk_bf16_f32 v219, v222, v223\n" \
"buffer_store_dwordx4 v[216:219], v20, s[56:59], s96 offen offset:1024 sc1\n" \
"s_add_u32 s96, s96, 0x1800\n" \
"v_mfma_f32_16x16x32_bf16 v[228:231], a[196:199], a[240:243], v[228:231]\n" \
"ds_read_b64_tr_b16 a[194:195], v33 offset:32448\n" \
"ds_read_b64_tr_b16 a[196:197], v33 offset:37056\n" \
"v_mfma_f32_16x16x32_bf16 v[224:227], a[200:203], a[228:231], v[224:227]\n" \
"ds_read_b64_tr_b16 a[198:199], v33 offset:41664\n" \
"v_mfma_f32_16x16x32_bf16 v[228:231], a[200:203], a[244:247], v[228:231]\n" \
"ds_read_b64_tr_b16 a[200:201], v33 offset:46272\n" \
"ds_read_b64_tr_b16 a[202:203], v33 offset:50880\n" \
"v_mfma_f32_16x16x32_bf16 v[224:227], a[204:207], a[232:235], v[224:227]\n" \
"v_mfma_f32_16x16x32_bf16 v[228:231], a[204:207], a[248:251], v[228:231]\n" \
"ds_read_b64_tr_b16 a[204:205], v33 offset:55488\n" \
"ds_read_b64_tr_b16 a[206:207], v33 offset:60096\n" \
"v_mfma_f32_16x16x32_bf16 v[224:227], a[208:211], a[160:163], v[224:227]\n" \
"v_mfma_f32_16x16x32_bf16 v[228:231], a[208:211], a[176:179], v[228:231]\n" \
"ds_read_b64_tr_b16 a[208:209], v10 offset:192\n" \
"ds_read_b64_tr_b16 a[210:211], v10 offset:4800\n" \
"v_mfma_f32_16x16x32_bf16 v[224:227], a[212:215], a[164:167], v[224:227]\n" \
"v_mfma_f32_16x16x32_bf16 v[228:231], a[212:215], a[180:183], v[228:231]\n" \
"ds_read_b64_tr_b16 a[212:213], v34 offset:192\n" \
"ds_read_b64_tr_b16 a[214:215], v34 offset:4800\n" \
"v_mfma_f32_16x16x32_bf16 v[224:227], a[216:219], a[168:171], v[224:227]\n" \
"v_mfma_f32_16x16x32_bf16 v[228:231], a[216:219], a[184:187], v[228:231]\n" \
"ds_read_b64_tr_b16 a[216:217], v34 offset:9408\n" \
"ds_read_b64_tr_b16 a[218:219], v34 offset:14016\n" \
"v_mfma_f32_16x16x32_bf16 v[224:227], a[0:3], a[172:175], v[224:227]\n" \
"v_mfma_f32_16x16x32_bf16 v[228:231], a[0:3], a[188:191], v[228:231]\n" \
"ds_read_b64_tr_b16 a[0:1], v34 offset:18624\n" \
"ds_read_b64_tr_b16 a[2:3], v34 offset:23232\n" \
"v_mfma_f32_16x16x32_bf16 v[224:227], a[4:7], a[96:99], v[224:227]\n" \
"v_mfma_f32_16x16x32_bf16 v[228:231], a[4:7], a[112:115], v[228:231]\n" \
"ds_read_b64_tr_b16 a[4:5], v10 offset:9408\n" \
"s_waitcnt vmcnt(6)\n" \
"s_mov_b64 exec, 1\n" \
"buffer_store_dword v39, off, s[56:59], s93 offset:36 sc1\n" \
"s_mov_b64 exec, -1\n" \
"s_waitcnt lgkmcnt(13)\n" \
"v_mfma_f32_16x16x32_bf16 v[224:227], a[8:11], a[100:103], v[224:227]\n" \
"ds_read_b64_tr_b16 a[6:7], v10 offset:14016\n" \
"v_mfma_f32_16x16x32_bf16 v[228:231], a[8:11], a[116:119], v[228:231]\n" \
"ds_read_b64_tr_b16 a[8:9], v33 offset:224\n" \
"ds_read_b64_tr_b16 a[10:11], v33 offset:4832\n" \
"v_mfma_f32_16x16x32_bf16 v[224:227], a[12:15], a[104:107], v[224:227]\n" \
"v_mfma_f32_16x16x32_bf16 v[228:231], a[12:15], a[120:123], v[228:231]\n" \
"ds_read_b64_tr_b16 a[12:13], v33 offset:9440\n" \
"ds_read_b64_tr_b16 a[14:15], v33 offset:14048\n" \
"v_mfma_f32_16x16x32_bf16 v[224:227], a[16:19], a[108:111], v[224:227]\n" \
"v_mfma_f32_16x16x32_bf16 v[228:231], a[16:19], a[124:127], v[228:231]\n" \
"ds_read_b64_tr_b16 a[16:17], v33 offset:18656\n" \
"ds_read_b64_tr_b16 a[18:19], v33 offset:23264\n" \
"v_mfma_f32_16x16x32_bf16 v[232:235], a[20:23], a[220:223], v[232:235]\n" \
"v_mfma_f32_16x16x32_bf16 v[236:239], a[20:23], a[236:239], v[236:239]\n" \
"ds_read_b64_tr_b16 a[20:21], v33 offset:27872\n" \
"ds_read_b64_tr_b16 a[22:23], v33 offset:32480\n" \
"v_mfma_f32_16x16x32_bf16 v[232:235], a[24:27], a[224:227], v[232:235]\n" \
"v_cvt_pk_bf16_f32 v224, v224, v225\n" \
"v_cvt_pk_bf16_f32 v225, v226, v227\n" \
"v_cvt_pk_bf16_f32 v226, v228, v229\n" \
"v_cvt_pk_bf16_f32 v227, v230, v231\n" \
"buffer_store_dwordx4 v[224:227], v20, s[56:59], s71 offen offset:1024 sc1\n" \
"v_mfma_f32_16x16x32_bf16 v[236:239], a[24:27], a[240:243], v[236:239]\n" \
"ds_read_b64_tr_b16 a[24:25], v33 offset:37088\n" \
"ds_read_b64_tr_b16 a[26:27], v33 offset:41696\n" \
"v_mfma_f32_16x16x32_bf16 v[232:235], a[28:31], a[228:231], v[232:235]\n" \
"v_mfma_f32_16x16x32_bf16 v[236:239], a[28:31], a[244:247], v[236:239]\n" \
"ds_read_b64_tr_b16 a[28:29], v33 offset:46304\n" \
"ds_read_b64_tr_b16 a[30:31], v33 offset:50912\n" \
"v_mfma_f32_16x16x32_bf16 v[232:235], a[192:195], a[232:235], v[232:235]\n" \
"v_mfma_f32_16x16x32_bf16 v[236:239], a[192:195], a[248:251], v[236:239]\n" \
"ds_read_b64_tr_b16 a[192:193], v33 offset:55520\n" \
"ds_read_b64_tr_b16 a[194:195], v33 offset:60128\n" \
"v_mfma_f32_16x16x32_bf16 v[232:235], a[196:199], a[160:163], v[232:235]\n" \
"v_mfma_f32_16x16x32_bf16 v[236:239], a[196:199], a[176:179], v[236:239]\n" \
"ds_read_b64_tr_b16 a[196:197], v10 offset:224\n" \
"s_waitcnt lgkmcnt(13)\n" \
"v_mfma_f32_16x16x32_bf16 v[232:235], a[200:203], a[164:167], v[232:235]\n" \
"ds_read_b64_tr_b16 a[198:199], v10 offset:4832\n" \
"v_mfma_f32_16x16x32_bf16 v[236:239], a[200:203], a[180:183], v[236:239]\n" \
"ds_read_b64_tr_b16 a[200:201], v34 offset:224\n" \
"ds_read_b64_tr_b16 a[202:203], v34 offset:4832\n" \
"v_mfma_f32_16x16x32_bf16 v[232:235], a[204:207], a[168:171], v[232:235]\n" \
"v_mfma_f32_16x16x32_bf16 v[236:239], a[204:207], a[184:187], v[236:239]\n" \
"ds_read_b64_tr_b16 a[204:205], v34 offset:9440\n" \
"ds_read_b64_tr_b16 a[206:207], v34 offset:14048\n" \
"v_mfma_f32_16x16x32_bf16 v[232:235], a[208:211], a[172:175], v[232:235]\n" \
"v_mfma_f32_16x16x32_bf16 v[236:239], a[208:211], a[188:191], v[236:239]\n" \
"ds_read_b64_tr_b16 a[208:209], v34 offset:18656\n" \
"ds_read_b64_tr_b16 a[210:211], v34 offset:23264\n" \
"v_mfma_f32_16x16x32_bf16 v[232:235], a[212:215], a[96:99], v[232:235]\n" \
"v_mfma_f32_16x16x32_bf16 v[236:239], a[212:215], a[112:115], v[236:239]\n" \
"ds_read_b64_tr_b16 a[212:213], v10 offset:9440\n" \
"ds_read_b64_tr_b16 a[214:215], v10 offset:14048\n" \
"s_waitcnt vmcnt(7)\n" \
"s_mov_b64 exec, 1\n" \
"buffer_store_dword v39, off, s[56:59], s94 offset:40 sc1\n" \
"s_mov_b64 exec, -1\n" \
"v_mfma_f32_16x16x32_bf16 v[232:235], a[216:219], a[100:103], v[232:235]\n" \
"v_mfma_f32_16x16x32_bf16 v[236:239], a[216:219], a[116:119], v[236:239]\n" \
"ds_read_b64_tr_b16 a[216:217], v36 offset:0\n" \
"ds_read_b64_tr_b16 a[218:219], v36 offset:1024\n" \
"v_mfma_f32_16x16x32_bf16 v[232:235], a[0:3], a[104:107], v[232:235]\n" \
"v_mfma_f32_16x16x32_bf16 v[236:239], a[0:3], a[120:123], v[236:239]\n" \
"ds_read_b64_tr_b16 a[0:1], v36 offset:2048\n" \
"ds_read_b64_tr_b16 a[2:3], v36 offset:3072\n" \
"v_mfma_f32_16x16x32_bf16 v[232:235], a[4:7], a[108:111], v[232:235]\n" \
"v_mfma_f32_16x16x32_bf16 v[236:239], a[4:7], a[124:127], v[236:239]\n" \
"ds_read_b64_tr_b16 a[4:5], v36 offset:32\n" \
"ds_read_b64_tr_b16 a[6:7], v36 offset:1056\n" \
"v_mfma_f32_16x16x32_bf16 v[240:243], a[8:11], a[220:223], v[240:243]\n" \
"v_mfma_f32_16x16x32_bf16 v[244:247], a[8:11], a[236:239], v[244:247]\n" \
"ds_read_b64_tr_b16 a[8:9], v36 offset:2080\n" \
"s_waitcnt lgkmcnt(13)\n" \
"v_mfma_f32_16x16x32_bf16 v[240:243], a[12:15], a[224:227], v[240:243]\n" \
"v_cvt_pk_bf16_f32 v232, v232, v233\n" \
"v_cvt_pk_bf16_f32 v233, v234, v235\n" \
"v_cvt_pk_bf16_f32 v234, v236, v237\n" \
"v_cvt_pk_bf16_f32 v235, v238, v239\n" \
"buffer_store_dwordx4 v[232:235], v20, s[56:59], s72 offen offset:1024 sc1\n" \
"s_waitcnt vmcnt(5)\n" \
"v_cmp_eq_u32_e64 s[98:99], s62, v37\n" \
"s_mov_b64 exec, 1\n" \
"buffer_store_dword v39, off, s[56:59], s95 offset:44 sc1\n" \
"s_mov_b64 exec, -1\n" \
"v_mfma_f32_16x16x32_bf16 v[244:247], a[12:15], a[240:243], v[244:247]\n" \
"ds_read_b64_tr_b16 a[10:11], v36 offset:3104\n" \
"s_or_b32 s75, 0x7f, s100\n" \
"s_andn2_b32 s75, s75, s98\n" \
"s_cbranch_scc1 L_sx0_%=\n" \
"L_rx0_%=:\n" \
"buffer_load_dword v37, v40, s[56:59], s74 offen sc1\n" \
"s_add_u32 s97, s63, 0x0\n" \
"s_add_u32 s64, s63, 0x800\n" \
"s_add_u32 s65, s63, 0x1000\n" \
"s_add_u32 s66, s63, 0x1800\n" \
"s_add_u32 s67, s63, 0x2000\n" \
"s_add_u32 s68, s63, 0x2800\n" \
"s_add_u32 s69, s63, 0x3000\n" \
"s_add_u32 s71, s71, 0x1000\n" \
"s_add_u32 s72, s72, 0x800\n" \
"v_mfma_f32_16x16x32_bf16 v[240:243], a[16:19], a[228:231], v[240:243]\n" \
"v_mfma_f32_16x16x32_bf16 v[244:247], a[16:19], a[244:247], v[244:247]\n" \
"buffer_load_dwordx4 v[116:119], v20, s[56:59], s97 offen offset:0 sc1\n" \
"v_mfma_f32_16x16x32_bf16 v[240:243], a[20:23], a[232:235], v[240:243]\n" \
"v_mfma_f32_16x16x32_bf16 v[244:247], a[20:23], a[248:251], v[244:247]\n" \
"v_mfma_f32_16x16x32_bf16 v[168:171], a[24:27], a[128:131], v[168:171]\n" \
"v_mfma_f32_16x16x32_bf16 v[172:175], a[24:27], a[144:147], v[172:175]\n" \
"buffer_load_dwordx4 v[124:127], v20, s[56:59], s64 offen offset:0 sc1\n" \
"v_mfma_f32_16x16x32_bf16 v[240:243], a[24:27], a[160:163], v[240:243]\n" \
"v_readlane_b32 s64, v11, 54\n" \
"v_mfma_f32_16x16x32_bf16 v[244:247], a[24:27], a[176:179], v[244:247]\n" \
"v_mfma_f32_16x16x32_bf16 v[168:171], a[28:31], a[132:135], v[168:171]\n" \
"v_mfma_f32_16x16x32_bf16 v[172:175], a[28:31], a[148:151], v[172:175]\n" \
"buffer_load_dwordx4 v[132:135], v20, s[56:59], s65 offen offset:0 sc1\n" \
"v_mfma_f32_16x16x32_bf16 v[240:243], a[28:31], a[164:167], v[240:243]\n" \
"v_readlane_b32 s65, v11, 45\n" \
"v_mfma_f32_16x16x32_bf16 v[244:247], a[28:31], a[180:183], v[244:247]\n" \
"v_mfma_f32_16x16x32_bf16 v[168:171], a[192:195], a[136:139], v[168:171]\n" \
"v_mfma_f32_16x16x32_bf16 v[172:175], a[192:195], a[152:155], v[172:175]\n" \
"buffer_load_dwordx4 v[140:143], v20, s[56:59], s66 offen offset:0 sc1\n" \
"v_mfma_f32_16x16x32_bf16 v[240:243], a[192:195], a[168:171], v[240:243]\n" \
"v_readlane_b32 s66, v11, 43\n" \
"v_mfma_f32_16x16x32_bf16 v[244:247], a[192:195], a[184:187], v[244:247]\n" \
"v_mfma_f32_16x16x32_bf16 v[168:171], a[196:199], a[140:143], v[168:171]\n" \
"v_mfma_f32_16x16x32_bf16 v[172:175], a[196:199], a[156:159], v[172:175]\n" \
"buffer_load_dwordx4 v[148:151], v20, s[56:59], s67 offen offset:0 sc1\n" \
"v_mfma_f32_16x16x32_bf16 v[240:243], a[196:199], a[172:175], v[240:243]\n" \
"v_readlane_b32 s67, v11, 44\n" \
"v_mfma_f32_16x16x32_bf16 v[244:247], a[196:199], a[188:191], v[244:247]\n" \
"v_mfma_f32_16x16x32_bf16 v[168:171], a[200:203], a[32:35], v[168:171]\n" \
"v_mfma_f32_16x16x32_bf16 v[172:175], a[200:203], a[48:51], v[172:175]\n" \
"buffer_load_dwordx4 v[156:159], v20, s[56:59], s68 offen offset:0 sc1\n" \
"v_mfma_f32_16x16x32_bf16 v[240:243], a[200:203], a[96:99], v[240:243]\n" \
"v_mfma_f32_16x16x32_bf16 v[244:247], a[200:203], a[112:115], v[244:247]\n" \
"s_waitcnt lgkmcnt(0)\n" \
"v_mfma_f32_16x16x32_bf16 v[168:171], a[204:207], a[36:39], v[168:171]\n" \
"v_mfma_f32_16x16x32_bf16 v[172:175], a[204:207], a[52:55], v[172:175]\n" \
"buffer_load_dwordx4 v[164:167], v20, s[56:59], s69 offen offset:0 sc1\n" \
"v_mfma_f32_16x16x32_bf16 v[240:243], a[204:207], a[100:103], v[240:243]\n" \
"v_mfma_f32_16x16x32_bf16 v[244:247], a[204:207], a[116:119], v[244:247]\n" \
"v_mfma_f32_16x16x32_bf16 v[168:171], a[208:211], a[40:43], v[168:171]\n" \
"v_mfma_f32_16x16x32_bf16 v[172:175], a[208:211], a[56:59], v[172:175]\n" \
"buffer_load_dwordx4 v[112:115], v20, s[56:59], s70 offen offset:0 sc1\n" \
"v_mfma_f32_16x16x32_bf16 v[240:243], a[208:211], a[104:107], v[240:243]\n" \
"v_mfma_f32_16x16x32_bf16 v[244:247], a[208:211], a[120:123], v[244:247]\n" \
"v_mfma_f32_16x16x32_bf16 v[168:171], a[212:215], a[44:47], v[168:171]\n" \
"v_mfma_f32_16x16x32_bf16 v[172:175], a[212:215], a[60:63], v[172:175]\n" \
"buffer_load_dwordx4 v[184:187], v20, s[56:59], s70 offen offset:2048 sc1\n" \
"s_add_u32 s70, s70, 0x1000\n" \
"v_mfma_f32_16x16x32_bf16 v[240:243], a[212:215], a[108:111], v[240:243]\n" \
"v_mfma_f32_16x16x32_bf16 v[244:247], a[212:215], a[124:127], v[244:247]\n" \
"s_waitcnt vmcnt(15)\n" \
"s_mov_b64 exec, 1\n" \
"buffer_store_dword v39, off, s[56:59], s96 offset:48 sc1\n" \
"s_mov_b64 exec, -1\n" \
"v_mfma_f32_16x16x32_bf16 v[168:171], a[216:219], v[64:67], v[168:171]\n" \
"v_mfma_f32_16x16x32_bf16 v[172:175], a[216:219], v[72:75], v[172:175]\n" \
"v_mfma_f32_16x16x32_bf16 v[240:243], a[216:219], v[80:83], v[240:243]\n" \
"v_mfma_f32_16x16x32_bf16 v[244:247], a[216:219], v[88:91], v[244:247]\n" \
"buffer_load_dwordx4 v[120:123], v20, s[56:59], s70 offen offset:0 sc1\n" \
"v_mfma_f32_16x16x32_bf16 v[168:171], a[0:3], v[68:71], v[168:171]\n" \
"v_mfma_f32_16x16x32_bf16 v[172:175], a[0:3], v[76:79], v[172:175]\n" \
"v_mfma_f32_16x16x32_bf16 v[240:243], a[0:3], v[84:87], v[240:243]\n" \
"v_mfma_f32_16x16x32_bf16 v[244:247], a[0:3], v[92:95], v[244:247]\n" \
"buffer_load_dwordx4 v[192:195], v20, s[56:59], s70 offen offset:2048 sc1\n" \
"s_add_u32 s70, s70, 0x1000\n" \
"v_mfma_f32_16x16x32_bf16 v[56:59], a[4:7], v[96:99], 0\n" \
"v_mfma_f32_16x16x32_bf16 v[60:63], a[4:7], v[104:107], 0\n" \
"v_mfma_f32_16x16x32_bf16 v[56:59], a[8:11], v[100:103], v[56:59]\n" \
"v_mfma_f32_16x16x32_bf16 v[60:63], a[8:11], v[108:111], v[60:63]\n" \
"buffer_load_dwordx4 v[128:131], v20, s[56:59], s70 offen offset:0 sc1\n" \
"buffer_load_dwordx4 v[200:203], v20, s[56:59], s70 offen offset:2048 sc1\n" \
".fill (L_ab + 45056 - .) / 4, 4, 0xBF800000\n" \
"L_ap11:\n" \
"s_cbranch_execz L_ap0\n" \
"s_add_u32 s70, s70, 0x1000\n" \
"buffer_load_dwordx4 v[176:179], v20, s[56:59], s70 offen offset:0 sc1\n" \
"buffer_load_dwordx4 v[248:251], v20, s[56:59], s70 offen offset:2048 sc1\n" \
"s_waitcnt vmcnt(15)\n" \
"v_mfma_f32_16x16x32_bf16 v[168:171], v[50:53], v[116:119], v[168:171]\n" \
"s_mov_b64 exec, 1\n" \
"buffer_store_dword v39, off, s[56:59], s71 offset:52 sc1\n" \
"s_mov_b64 exec, -1\n" \
"s_mov_b64 exec, 1\n" \
"buffer_store_dword v39, off, s[56:59], s72 offset:56 sc1\n" \
"s_mov_b64 exec, -1\n" \
"v_cmp_eq_u32_e64 s[98:99], s62, v37\n" \
"v_mfma_f32_16x16x32_bf16 v[172:175], v[48:51], v[116:119], v[172:175]\n" \
"s_andn2_b32 s75, 0x100, s98\n" \
"s_cbranch_scc1 L_sx1_%=\n" \
"L_rx1_%=:\n" \
"s_add_u32 s73, s63, 0x0\n" \
"s_waitcnt vmcnt(16)\n" \
"v_mfma_f32_16x16x32_bf16 v[168:171], v[50:53], v[124:127], v[168:171]\n" \
"v_mfma_f32_16x16x32_bf16 v[172:175], v[48:51], v[124:127], v[172:175]\n" \
"buffer_load_dwordx4 v[188:191], v20, s[56:59], s73 offen offset:1024 sc1\n" \
"s_andn2_b32 s75, 0x200, s98\n" \
"s_cbranch_scc1 L_sx2_%=\n" \
"L_rx2_%=:\n" \
"s_add_u32 s92, s63, 0x800\n" \
"s_waitcnt vmcnt(16)\n" \
"v_mfma_f32_16x16x32_bf16 v[168:171], v[50:53], v[132:135], v[168:171]\n" \
"v_mfma_f32_16x16x32_bf16 v[172:175], v[48:51], v[132:135], v[172:175]\n" \
"buffer_load_dwordx4 v[196:199], v20, s[56:59], s92 offen offset:1024 sc1\n" \
"s_andn2_b32 s75, 0x400, s98\n" \
"s_cbranch_scc1 L_sx3_%=\n" \
"L_rx3_%=:\n" \
"s_add_u32 s93, s63, 0x1000\n" \
"s_waitcnt vmcnt(16)\n" \
"v_mfma_f32_16x16x32_bf16 v[168:171], v[50:53], v[140:143], v[168:171]\n" \
"v_mfma_f32_16x16x32_bf16 v[172:175], v[48:51], v[140:143], v[172:175]\n" \
"buffer_load_dwordx4 v[204:207], v20, s[56:59], s93 offen offset:1024 sc1\n" \
"s_andn2_b32 s75, 0x800, s98\n" \
"s_cbranch_scc1 L_sx4_%=\n" \
"L_rx4_%=:\n" \
"s_add_u32 s94, s63, 0x1800\n" \
"s_waitcnt vmcnt(16)\n" \
"v_mfma_f32_16x16x32_bf16 v[168:171], v[50:53], v[148:151], v[168:171]\n" \
"v_mfma_f32_16x16x32_bf16 v[172:175], v[48:51], v[148:151], v[172:175]\n" \
"buffer_load_dwordx4 v[212:215], v20, s[56:59], s94 offen offset:1024 sc1\n" \
"s_andn2_b32 s75, 0x1000, s98\n" \
"s_cbranch_scc1 L_sx5_%=\n" \
"L_rx5_%=:\n" \
"s_add_u32 s95, s63, 0x2000\n" \
"s_waitcnt vmcnt(16)\n" \
"v_mfma_f32_16x16x32_bf16 v[168:171], v[50:53], v[156:159], v[168:171]\n" \
"v_mfma_f32_16x16x32_bf16 v[172:175], v[48:51], v[156:159], v[172:175]\n" \
"buffer_load_dwordx4 v[220:223], v20, s[56:59], s95 offen offset:1024 sc1\n" \
"s_andn2_b32 s75, 0x2000, s98\n" \
"s_cbranch_scc1 L_sx6_%=\n" \
"L_rx6_%=:\n" \
"s_add_u32 s96, s63, 0x2800\n" \
"s_waitcnt vmcnt(16)\n" \
"v_mfma_f32_16x16x32_bf16 v[168:171], v[50:53], v[164:167], v[168:171]\n" \
"v_mfma_f32_16x16x32_bf16 v[172:175], v[48:51], v[164:167], v[172:175]\n" \
"buffer_load_dwordx4 v[228:231], v20, s[56:59], s96 offen offset:1024 sc1\n" \
"s_andn2_b32 s75, 0x4000, s98\n" \
"s_cbranch_scc1 L_sx7_%=\n" \
"L_rx7_%=:\n" \
"s_add_u32 s97, s63, 0x3000\n" \
"s_waitcnt vmcnt(16)\n" \
"v_mfma_f32_16x16x32_bf16 v[56:59], v[50:53], v[112:115], v[56:59]\n" \
"v_mfma_f32_16x16x32_bf16 v[60:63], v[48:51], v[112:115], v[60:63]\n" \
"buffer_load_dwordx4 v[236:239], v20, s[56:59], s97 offen offset:1024 sc1\n" \
"v_cvt_pk_bf16_f32 v168, v168, v169\n" \
"v_cvt_pk_bf16_f32 v172, v172, v173\n" \
"v_cvt_pk_bf16_f32 v169, v170, v171\n" \
"v_cvt_pk_bf16_f32 v173, v174, v175\n" \
"buffer_store_dwordx2 v[168:169], v42, s[88:91], s66 offen sc1\n" \
"buffer_store_dwordx2 v[172:173], v43, s[88:91], s66 offen sc1\n" \
"s_waitcnt vmcnt(18)\n" \
"v_mfma_f32_16x16x32_bf16 v[56:59], v[50:53], v[184:187], v[56:59]\n" \
"v_mfma_f32_16x16x32_bf16 v[60:63], v[48:51], v[184:187], v[60:63]\n" \
"s_waitcnt vmcnt(16)\n" \
"v_mfma_f32_16x16x32_bf16 v[56:59], v[50:53], v[120:123], v[56:59]\n" \
"v_mfma_f32_16x16x32_bf16 v[60:63], v[48:51], v[120:123], v[60:63]\n" \
"s_waitcnt vmcnt(15)\n" \
"v_mfma_f32_16x16x32_bf16 v[56:59], v[50:53], v[192:195], v[56:59]\n" \
"v_mfma_f32_16x16x32_bf16 v[60:63], v[48:51], v[192:195], v[60:63]\n" \
"s_waitcnt vmcnt(14)\n" \
"v_mfma_f32_16x16x32_bf16 v[56:59], v[50:53], v[128:131], v[56:59]\n" \
"v_mfma_f32_16x16x32_bf16 v[60:63], v[48:51], v[128:131], v[60:63]\n" \
"s_waitcnt vmcnt(13)\n" \
"v_mfma_f32_16x16x32_bf16 v[56:59], v[50:53], v[200:203], v[56:59]\n" \
"v_mfma_f32_16x16x32_bf16 v[60:63], v[48:51], v[200:203], v[60:63]\n" \
"s_waitcnt vmcnt(12)\n" \
"v_mfma_f32_16x16x32_bf16 v[56:59], v[50:53], v[176:179], v[56:59]\n" \
"v_mfma_f32_16x16x32_bf16 v[60:63], v[48:51], v[176:179], v[60:63]\n" \
"s_waitcnt vmcnt(11)\n" \
"v_mfma_f32_16x16x32_bf16 v[56:59], v[50:53], v[248:251], v[56:59]\n" \
"v_mfma_f32_16x16x32_bf16 v[60:63], v[48:51], v[248:251], v[60:63]\n" \
"s_mov_b64 exec, 1\n" \
"buffer_store_dword v46, off, s[56:59], s64 offset:0 sc1\n" \
"s_mov_b64 exec, -1\n" \
"s_waitcnt vmcnt(9)\n" \
"v_mfma_f32_16x16x32_bf16 v[240:243], v[50:53], v[188:191], v[240:243]\n" \
"v_mfma_f32_16x16x32_bf16 v[244:247], v[48:51], v[188:191], v[244:247]\n" \
"s_waitcnt vmcnt(8)\n" \
"v_mfma_f32_16x16x32_bf16 v[240:243], v[50:53], v[196:199], v[240:243]\n" \
"v_cvt_pk_bf16_f32 v56, v56, v57\n" \
"v_cvt_pk_bf16_f32 v60, v60, v61\n" \
"v_mfma_f32_16x16x32_bf16 v[244:247], v[48:51], v[196:199], v[244:247]\n" \
"v_cvt_pk_bf16_f32 v57, v58, v59\n" \
"v_cvt_pk_bf16_f32 v61, v62, v63\n" \
"buffer_store_dwordx2 v[56:57], v42, s[88:91], s65 offen sc1\n" \
"buffer_store_dwordx2 v[60:61], v43, s[88:91], s65 offen sc1\n" \
"s_waitcnt vmcnt(9)\n" \
"v_mfma_f32_16x16x32_bf16 v[240:243], v[50:53], v[204:207], v[240:243]\n" \
"v_mfma_f32_16x16x32_bf16 v[244:247], v[48:51], v[204:207], v[244:247]\n" \
"s_waitcnt vmcnt(8)\n" \
"v_mfma_f32_16x16x32_bf16 v[240:243], v[50:53], v[212:215], v[240:243]\n" \
"v_mfma_f32_16x16x32_bf16 v[244:247], v[48:51], v[212:215], v[244:247]\n" \
"s_waitcnt vmcnt(7)\n" \
"v_mfma_f32_16x16x32_bf16 v[240:243], v[50:53], v[220:223], v[240:243]\n" \
"v_mfma_f32_16x16x32_bf16 v[244:247], v[48:51], v[220:223], v[244:247]\n" \
"s_waitcnt vmcnt(6)\n" \
"v_mfma_f32_16x16x32_bf16 v[240:243], v[50:53], v[228:231], v[240:243]\n" \
"v_mfma_f32_16x16x32_bf16 v[244:247], v[48:51], v[228:231], v[244:247]\n" \
"s_waitcnt vmcnt(5)\n" \
"v_mfma_f32_16x16x32_bf16 v[240:243], v[50:53], v[236:239], v[240:243]\n" \
"v_mfma_f32_16x16x32_bf16 v[244:247], v[48:51], v[236:239], v[244:247]\n" \
"s_nop 6\n" \
"v_cvt_pk_bf16_f32 v240, v240, v241\n" \
"v_cvt_pk_bf16_f32 v244, v244, v245\n" \
"v_cvt_pk_bf16_f32 v241, v242, v243\n" \
"v_cvt_pk_bf16_f32 v245, v246, v247\n" \
"buffer_store_dwordx2 v[240:241], v42, s[88:91], s67 offen sc1\n" \
"buffer_store_dwordx2 v[244:245], v43, s[88:91], s67 offen sc1\n"
#define ATT_VCLOB "v21","v22","v23","v24","v25","v26","v27","v28","v29","v32","v33","v34","v35","v36","v37","v38","v39","v40","v41","v42","v43","v44","v45","v46","v47","v48","v49","v50","v51","v52","v53","v54","v55","v56","v57","v58","v59","v60","v61","v62","v63","v64","v65","v66","v67","v68","v69","v70","v71","v72","v73","v74","v75","v76","v77","v78","v79","v80","v81","v82","v83","v84","v85","v86","v87","v88","v89","v90","v91","v92","v93","v94","v95","v96","v97","v98","v99","v100","v101","v102","v103","v104","v105","v106","v107","v108","v109","v110","v111","v112","v113","v114","v115","v116","v117","v118","v119","v120","v121","v122","v123","v124","v125","v126","v127","v128","v129","v130","v131","v132","v133","v134","v135","v136","v137","v138","v139","v140","v141","v142","v143","v144","v145","v146","v147","v148","v149","v150","v151","v152","v153","v154","v155","v156","v157","v158","v159","v160","v161","v162","v163","v164","v165","v166","v167","v168","v169","v170","v171","v172","v173","v174","v175","v176","v177","v178","v179","v180","v181","v182","v183","v184","v185","v186","v187","v188","v189","v190","v191","v192","v193","v194","v195","v196","v197","v198","v199","v200","v201","v202","v203","v204","v205","v206","v207","v208","v209","v210","v211","v212","v213","v214","v215","v216","v217","v218","v219","v220","v221","v222","v223","v224","v225","v226","v227","v228","v229","v230","v231","v232","v233","v234","v235","v236","v237","v238","v239","v240","v241","v242","v243","v244","v245","v246","v247","v248","v249","v250","v251","v252","v253","v254","v255"
#define ATT_SCLOB "s63","s64","s65","s66","s67","s68","s69","s70","s71","s72","s73","s74","s75","s76","s77","s78","s79","s80","s81","s82","s83","s84","s85","s86","s87","s88","s89","s90","s91","s92","s93","s95","s96","s97","s98","s99","s100","s101"
#define ATT_WTCH 0
#define ATT_PEEL 1
#define ATT_PEEL_FOW 0
enum { T_fo, T_fj, T_fa, T_fas, T_fb, T_fbs, T_f8, T_mk8, T_wt, T_wol, T_woh, T_wbl, T_wbh, T_outl, T_outh, T_r1l, T_r1h, T_r1n, T_r1s, T_m0, T_m1, T_m1b, T_mw, T_m3, T_m4, T_ml, T_w0, T_w1, T_w2, T_w3, T_A1, T_A2, T_A3, T_B1, T_B2, T_B3, T_Ao, T_Bo, T_Al, T_Bl, T_l8, T_xr, T_x8r, T_oA, T_oB, T_o8, T_x0, T_x1, T_x2, T_x3, T_x4, T_x5, T_x6, T_x8, T_ep };

__device__ __forceinline__ void out_consts(u32x4 (&vp)[3], const u16* wo, const u16* wob, const u16* out, const unsigned b, const unsigned w, const unsigned lane) {
    const unsigned sp = b & 7u, nt = (b >> 3) & 3u, j = b >> 5, head = 4u * sp + nt, OOR = 0x40000000u;
    unsigned vw[9];
#pragma unroll
    for (unsigned i = 0; i < 9u; ++i) {
        const unsigned q = 64u * i + lane, row = __umul24(q, 3641u) >> 16, c = q - __umul24(row, 18u);
        vw[i] = __umul24(row, DM * 2) + (c < 16u ? (c * 16u + sp * 32u + 32u) & 255u : c * 16u) - (i & 3u) * 1024u;
    }
    const unsigned row = 16u * w + (lane >> 2), r9 = __umul24(16u * (row >> 5) + (row & 12u) + 2u * ((row >> 4) & 1u) + ((row >> 1) & 1u), 18u) + 16u + (row & 1u);
    unsigned vwl = __umul24(r9, DM * 2) + ((lane & 2u) ? 256u : sp * 32u) + (lane & 1u) * 16u, vrd = __umul24(lane >> 2, 288u) + (lane & 3u) * 8u;
    unsigned tab = 0;
#define TB(n, v) asm("v_writelane_b32 %0, %1, %2" : "+v"(tab) : "s"((unsigned)(v)), "n"((int)T_##n))
    const unsigned FJ = (unsigned)WS_F + (F_J + 640u * j) * 4u, mt = 2u * (w & 2u) + (w & 1u), w2 = w < 2u ? w : 2u, hw = head * 147456u;
    const unsigned FR = (unsigned)WS_F + F_R * 4u + (8u * j + sp) * 1024u;
    TB(fo, FJ + (ATT_PEEL_FOW ? 16u * head + 4u * w : 4u * head)); TB(fj, FJ); TB(fa, FR + (w | 2u) * 256u); TB(fas, FR + w * 256u + nt * 4u);
    TB(fb, FR + 128u + w * 256u); TB(fbs, FR + 128u + w * 256u + nt * 4u);
    TB(ep, ssel(b + w, OOR, (unsigned)WS_F + F_EPOCH * 4u));
    const u16* pwo = wo + nt * 144u; const u16* pwb = wob + nt * 144u;
    TB(wol, PLO(pwo)); TB(woh, PHI(pwo) & 0xffffu); TB(wbl, PLO(pwb)); TB(wbh, PHI(pwb) & 0xffffu); TB(outl, PLO(out)); TB(outh, PHI(out) & 0xffffu); TB(r1l, sslt<3>(w, PLO(pwo), PLO(pwb))); TB(r1h, sslt<3>(w, PHI(pwo), PHI(pwb)) & 0xffffu);
    TB(r1n, sslt<3>(w, 72u * sp + 71u, 4095u) * 1152u + 288u); TB(r1s, sslt<3>(w, (72u * sp + 32u * w) * 1152u, hw + 110592u));
    TB(m0, 131072u + 9216u * w2); TB(w0, hw + 36864u * w2); TB(m1, ATT_PEEL ? sslt<3>(w, 41984u + 9216u * w, 32768u) : 24576u + 9216u * w); TB(m1b, ssel(w ^ 2u, sslt<3>(w, 41984u + 9216u * w, 32768u), 114688u - 4096u)); TB(mw, 65536u + 9216u * w); TB(m3, sslt<3>(w, 102400u + 9216u * w, 0u)); TB(m4, sslt<3>(w, 131072u + 9216u * w, 9216u)); TB(ml, (ATT_PEEL ? 18432u : 61440u) + 1024u * w);
    const unsigned tA = (unsigned)WS_AF + (8u * j + mt) * (KS_TOT * 1024u), tB = (unsigned)WS_AF + (120u - 8u * j + mt) * (KS_TOT * 1024u);
#define HK(k) (4u * sp + ((nt + k) & 3u))
    TB(w1, HK(1u) * 147456u + w * 36864u); TB(w2, HK(2u) * 147456u + w * 36864u); TB(w3, HK(3u) * 147456u + w * 36864u);
    TB(A1, tA + 18432u + HK(1u) * 4096u); TB(A2, tA + 18432u + HK(2u) * 4096u); TB(A3, tA + 18432u + HK(3u) * 4096u);
    TB(B1, tB + 18432u + HK(1u) * 4096u); TB(B2, tB + 18432u + HK(2u) * 4096u); TB(B3, tB + 18432u + HK(3u) * 4096u);
    TB(Ao, tA + sp * 2048u); TB(Bo, tB + sp * 2048u); TB(Al, tA + 16384u); TB(Bl, tB + 16384u);
    const unsigned nred = w ^ (sp >> 1), blk = sp & 1u;
    const unsigned xg = (unsigned)WS_X + (4u * j + nt) * 589824u + w * 16384u, oA = (128u * j + 16u * mt) * 1152u + nt * 288u, oB = (1920u - 128u * j + 16u * mt) * 1152u + nt * 288u;
    TB(l8, ssel(nred, OOR, ssel(blk, tB, tA) + 16384u)); TB(xr, xg + sp * 65536u); TB(x8r, ssel(nred, OOR, xg + 524288u + (sp & 1u) * 1024u));
    TB(oA, oA + sp * 32u); TB(oB, oB + sp * 32u); TB(o8, ssel(nred, OOR, ssel(blk, oB, oA) + 256u)); TB(x8, xg + 524288u + sp * 2048u);
    TB(f8, xg + w * 131072u + 14400u + sp * 4u); TB(mk8, ssel(nred, 0u, 0xff0000u)); TB(wt, hw + j * 18432u + w * 4608u);
#define XI(i) TB(x##i, xg + ((sp + 1u + i) & 7u) * 65536u + i * 2048u)
    XI(0); XI(1); XI(2); XI(3); XI(4); XI(5); XI(6);
#undef XI
#undef HK
#undef TB
    vp[0] = (u32x4){vw[0], vw[1], vw[2], vw[3]}; vp[1] = (u32x4){vw[4], vw[5], vw[6], vw[7]}; vp[2] = (u32x4){vw[8], vwl, vrd, tab};
}
__global__ __launch_bounds__(256, 1) __attribute__((aligned(4096))) void fused_kernel(const u16* __restrict__ x, const u16* __restrict__ ckv, const u16* __restrict__ wq, const u16* __restrict__ wuk,
                                                       const u16* __restrict__ wuv, const u16* __restrict__ wqkv, const u16* __restrict__ gw,
                                                       const u16* __restrict__ wo, const u16* __restrict__ wob, char* __restrict__ ws_, u16* __restrict__ out) {
    using namespace att;
    extern __shared__ __attribute__((aligned(16))) char lds[];
    u32x4 sa[5]; u32x2 s24, s28; char* ws; unsigned tdum; kaptr ka = (kaptr)__builtin_amdgcn_kernarg_segment_ptr();
    asm volatile("s_load_dwordx8 s[8:15], %[ka], 0x0\n\ts_load_dwordx8 s[16:23], %[ka], 0x20\n\ts_getpc_b64 s[4:5]\n\tv_lshlrev_b32_e32 v191, 7, %[t]\n\ts_bfe_u32 s6, %[b], 0x20003\n\t"
                 "v_lshl_add_u32 v191, s6, 15, v191\n\tv_min_u32_e32 v191, %[e], v191\n\tglobal_load_dword v191, v191, s[4:5]\n\ts_load_dwordx4 s[24:27], %[ka], 0x40\n\ts_load_dwordx2 s[28:29], %[ka], 0x50"
                 : "={s[8:11]}"(sa[0]), "={s[12:15]}"(sa[1]), "={s[16:19]}"(sa[2]), "={s[20:23]}"(sa[3]), "={s[24:25]}"(s24), "={s[26:27]}"(ws), "={s[28:29]}"(s28), "={v191}"(tdum), [ka] "+{s[0:1]}"(ka)
                 : [t] "v"(threadIdx.x), [b] "s"(blockIdx.x), [e] "i"(CT_END) : "s4", "s5", "s6", "scc");
    SBAR();
    const unsigned wid = rfl(threadIdx.x >> 6);
    unsigned tag, vl;
    const int b = blockIdx.x;
    u32x4 vp[5], srdA; u32x2 sd;
    proj_phase(ka, ws, wid, vp, vl, sa, s24, s28, tdum, srdA, sd, tag
    );
    unsigned* const flags = (unsigned*)(ws + WS_F);
    RTS(2);
#define AST(L) do {} while (0)
#define ATT_TAIL "s_endpgm\n"
    unsigned abad;
    asm volatile(ATT_ASM ATT_TAIL "L_end_%=:\n s_nop 1"
                 : "={s94}"(abad)
                 : "{v[0:3]}"(vp[0]), "{v[4:7]}"(vp[1]), "{v[8:11]}"(vp[2]), "{v[12:15]}"(vp[3]), "{v[16:19]}"(vp[4]), "{v20}"(vl), "{s[56:59]}"(srdA), "{s[60:61]}"(sd), "{s62}"(tag),
                   "{s[8:11]}"(sa[0]), "{s[12:15]}"(sa[1]), "{s[16:19]}"(sa[2]), "{s[20:23]}"(sa[3]), "{s24}"(sa[4][0]), "{s3}"(wid), "{s[28:29]}"(s28)
                 : "memory", "m0", "vcc", "scc", ATT_VCLOB, ATT_SCLOB, ACLOB_LO, ACLOB_HI);
    {
        unsigned b2 = b, w2 = wid;
        asm volatile("s_waitcnt vmcnt(0)" : "+s"(ka), "+s"(b2), "+s"(w2), "+s"(ws) :: "memory");
        const unsigned j2 = b2 >> 5, p2 = w2 & 1u, h2 = (b2 & 7u) * 4u + ((b2 >> 3) & 3u);
#pragma unroll 1
        for (unsigned i = w2 & 2u; i < ((w2 & 2u) ? (abad ? 8u : 4u) : 2u); ++i)
            att_exact(ka[0], ka[2], ws, h2, (i < 4u ? 1920u - 128u * j2 : 128u * j2 - 128u) + 32u * i + 16u * p2, *reinterpret_cast<const float*>(lds + LDS_G + (32u * i + 16u * p2 + (lane_id() & 15u)) * 4u));
        asm volatile("s_waitcnt vmcnt(0)" ::: "memory");
        unsigned* fa = (unsigned*)(ws + WS_F) + F_R + (8u * j2 + (b2 & 7u)) * 256u + (2u + p2) * 64u + ((b2 >> 3) & 3u);
        if (w2 & 2u) st_flag(fa, tag);
        wait_flags(fa, ld_flag(fa), tag);
        const unsigned l2 = lane_id(); u32x4 c[3]; out_consts(c, ka[7], ka[8], ka[10], b2, w2, l2);
        asm volatile("s_waitcnt vmcnt(0) lgkmcnt(0)\n\ts_nop 7\n\ts_branch L_att_stub" :: "{v[0:3]}"(c[0]), "{v[4:7]}"(c[1]), "{v[8:11]}"(c[2]), "{v20}"(l2 * 16u), "{s[56:59]}"(SRDA), "{s60}"(SD(8u * j2, h2, w2)),
                     "{s61}"(SD(120u - 8u * j2, h2, w2)), "{s62}"(tag) : "memory");
    }
    RTS(5);
}

extern "C" void kernel_launch(void* const* d_in, const int* in_sizes, int n_in, void* d_out, int out_size, void* d_ws, size_t ws_size,
                              hipStream_t stream) {
    (void)hipFuncSetAttribute((const void*)fused_kernel, hipFuncAttributeMaxDynamicSharedMemorySize, att::LDS_BYTES);
    fused_kernel<<<dim3(256), 256, att::LDS_BYTES, stream>>>((const u16*)d_in[0], (const u16*)d_in[4], (const u16*)d_in[5], (const u16*)d_in[6], (const u16*)d_in[7], (const u16*)d_in[1],
                                                             (const u16*)d_in[9], (const u16*)d_in[2], (const u16*)d_in[8], (char*)d_ws, (u16*)d_out);
}
```

```cpp
#include <hip/hip_runtime.h>
#include <hip/hip_bf16.h>
#include <stdint.h>
#include <utility>

typedef short bf16x8 __attribute__((ext_vector_type(8)));
typedef short s16x4 __attribute__((ext_vector_type(4)));
typedef float f32x16 __attribute__((ext_vector_type(16)));
typedef float f32x4 __attribute__((ext_vector_type(4)));
typedef unsigned u32x4 __attribute__((ext_vector_type(4)));
typedef unsigned u32x2 __attribute__((ext_vector_type(2)));
typedef unsigned short u16;

constexpr int S = 2048, DM = 576, NH = 32, HD = 128, DC = 512, NQKV = 1728, DHA = 18;
constexpr int NBIG = NH * HD;
constexpr int KS_TOT = 146;

#define SBAR() __builtin_amdgcn_sched_barrier(0)
constexpr size_t WS_Q = 0, WS_K = WS_Q + (size_t)S * NBIG * 2, WS_V = WS_K + (size_t)S * NBIG * 2, WS_AF = WS_V + (size_t)S * NBIG * 2, WS_X = WS_AF + (size_t)128 * KS_TOT * 1024,
                 WS_P = WS_X + (size_t)64 * 16 * 9216, WS_F = WS_P + (size_t)128 * (4 * 32 * 256 + 1024) * 4;
constexpr size_t WS_L = (size_t)96 << 20;
static_assert(WS_F == 96206848, "workspace map is baked into the generated asm");
constexpr int F_EPOCH = 0, F_J = 1024, F_ST = 8192, F_KV = 40960, F_R = 65536;
constexpr unsigned TAG_STEP = 0x9E3779B1u;
__device__ __forceinline__ unsigned ld_flag(const unsigned* p) { return __hip_atomic_load(p, __ATOMIC_RELAXED, __HIP_MEMORY_SCOPE_AGENT); }
__device__ __forceinline__ void st_flag(unsigned* p, unsigned v) { __hip_atomic_store(p, v, __ATOMIC_RELAXED, __HIP_MEMORY_SCOPE_AGENT); }
__device__ __forceinline__ unsigned ld_flag_async(const unsigned* p) { unsigned v; asm volatile("global_load_dword %0, %1, off sc1" : "=v"(v) : "v"(p) : "memory"); return v; }
#define WAITV(v, N) asm volatile("s_waitcnt vmcnt(" #N ")" : "+v"(v) :: "memory")
__device__ __forceinline__ void wait_flags(const unsigned* p, unsigned v, unsigned tag) {
    if (__builtin_expect(__builtin_amdgcn_ballot_w64(v != tag) != 0ull, 0)) {
        unsigned spins = 0;
        do { __builtin_amdgcn_s_sleep(1); } while (__builtin_amdgcn_ballot_w64(ld_flag(p) != tag) != 0ull && ++spins < (1u << 22));
    }
    asm volatile("" ::: "memory");
}

__device__ __forceinline__ unsigned cvtpk(float lo, float hi) {
    unsigned r; asm volatile("v_cvt_pk_bf16_f32 %0, %1, %2" : "=v"(r) : "v"(lo), "v"(hi)); return r;
}
__device__ __forceinline__ float bf2f(u16 v) { return __uint_as_float(((unsigned)v) << 16); }
__device__ __forceinline__ int crow(int r, int hi) { return (r & 3) + 8 * (r >> 2) + 4 * hi; }

typedef __attribute__((address_space(3))) s16x4* lds_s16x4_p;
__device__ __forceinline__ s16x4 trrd(unsigned addr) { return __builtin_amdgcn_ds_read_tr16_b64_v4i16((lds_s16x4_p)addr); }

__device__ __forceinline__ unsigned lane_id() { unsigned l; asm volatile("v_mbcnt_lo_u32_b32 %0, -1, 0\n\tv_mbcnt_hi_u32_b32 %0, -1, %0" : "=v"(l)); return l; }
#define RTS(slot) do {} while (0)
#define CT_END 0x1f300
#define ACLOB_LO "a0","a1","a2","a3","a4","a5","a6","a7","a8","a9","a10","a11","a12","a13","a14","a15","a16","a17","a18","a19","a20","a21","a22","a23","a24","a25","a26","a27","a28","a29","a30","a31","a32","a33","a34","a35","a36","a37","a38","a39","a40","a41","a42","a43","a44","a45","a46","a47","a48","a49","a50","a51","a52","a53","a54","a55","a56","a57","a58","a59","a60","a61","a62","a63","a64","a65","a66","a67","a68","a69","a70","a71","a72","a73","a74","a75","a76","a77","a78","a79","a80","a81","a82","a83","a84","a85","a86","a87","a88","a89","a90","a91","a92","a93","a94","a95","a96","a97","a98","a99","a100","a101","a102","a103","a104","a105","a106","a107","a108","a109","a110","a111","a112","a113","a114","a115","a116","a117","a118","a119","a120","a121","a122","a123","a124","a125","a126","a127"
#define ACLOB_HI "a128","a129","a130","a131","a132","a133","a134","a135","a136","a137","a138","a139","a140","a141","a142","a143","a144","a145","a146","a147","a148","a149","a150","a151","a152","a153","a154","a155","a156","a157","a158","a159","a160","a161","a162","a163","a164","a165","a166","a167","a168","a169","a170","a171","a172","a173","a174","a175","a176","a177","a178","a179","a180","a181","a182","a183","a184","a185","a186","a187","a188","a189","a190","a191","a192","a193","a194","a195","a196","a197","a198","a199","a200","a201","a202","a203","a204","a205","a206","a207","a208","a209","a210","a211","a212","a213","a214","a215","a216","a217","a218","a219","a220","a221","a222","a223","a224","a225","a226","a227","a228","a229","a230","a231","a232","a233","a234","a235","a236","a237","a238","a239","a240","a241","a242","a243","a244","a245","a246","a247","a248","a249","a250","a251","a252","a253","a254","a255"
#define PLO(p) ((unsigned)(size_t)(p))
#define PHI(p) ((unsigned)((size_t)(p) >> 32))
__device__ __forceinline__ unsigned vsel(bool c, unsigned a, unsigned b) { asm("" : "+v"(a), "+v"(b)); return c ? a : b; }
__device__ __forceinline__ unsigned ssel(unsigned c, unsigned a, unsigned b) { unsigned r; asm("s_cmp_lg_u32 %1, 0\n\ts_cselect_b32 %0, %2, %3" : "=s"(r) : "s"(c), "s"(a), "s"(b) : "scc"); return r; }
template <int N> __device__ __forceinline__ unsigned sslt(unsigned c, unsigned a, unsigned b) { unsigned r; asm("s_cmpk_lt_u32 %1, %4\n\ts_cselect_b32 %0, %2, %3" : "=s"(r) : "s"(c), "s"(a), "s"(b), "n"(N) : "scc"); return r; }
namespace att {
constexpr int LDS_BYTES = 163840;
constexpr unsigned LDS_G = 159744u, LDS_L = 161792u;
}
#define SD(J8, H, W) ((unsigned)WS_AF + (((J8) + 2u * ((W) & 2u) + ((W) & 1u)) * KS_TOT + 18u + 4u * (H)) * 1024u)
#define SRDA (u32x4){PLO(ws), PHI(ws) & 0xffffu, (unsigned)WS_L, 0x00020000u}
#define PROJ_ASM \
".p2align 3\n" \
"L_pb:\n" \
"s_waitcnt lgkmcnt(0)\n" \
"s_mov_b32 s64, s10\n" \
"s_and_b32 s65, s11, 0xffff\n" \
"s_mov_b32 s66, 0x200000\n" \
"s_mov_b32 s67, 0x20000\n" \
"s_mov_b32 s68, s14\n" \
"s_and_b32 s69, s15, 0xffff\n" \
"s_mov_b32 s70, 0x400000\n" \
"s_mov_b32 s71, 0x20000\n" \
"s_mov_b32 s72, s16\n" \
"s_and_b32 s73, s17, 0xffff\n" \
"s_mov_b32 s74, 0x400000\n" \
"s_mov_b32 s75, 0x20000\n" \
"s_add_u32 m0, s30, 0x0\n" \
"s_movk_i32 s84, 0x0\n" \
"buffer_load_dwordx4 v22, s[64:67], s84 offen offset:0 lds\n" \
"s_movk_i32 s84, 0x1c00\n" \
"buffer_load_dwordx4 v23, s[64:67], s84 offen offset:1024 lds\n" \
"s_movk_i32 s84, 0x3800\n" \
"buffer_load_dwordx4 v22, s[64:67], s84 offen offset:2048 lds\n" \
"s_movk_i32 s84, 0x5400\n" \
"buffer_load_dwordx4 v23, s[64:67], s84 offen offset:3072 lds\n" \
"s_add_u32 m0, s31, 0x0\n" \
"s_movk_i32 s84, 0x0\n" \
"buffer_load_dwordx4 v26, s[68:71], s84 offen offset:0 lds\n" \
"s_movk_i32 s84, 0x7c00\n" \
"buffer_load_dwordx4 v26, s[68:71], s84 offen offset:1024 lds\n" \
"s_addk_i32 s84, 0x7c00\n" \
"buffer_load_dwordx4 v27, s[68:71], s84 offen offset:2048 lds\n" \
"s_addk_i32 s84, 0x7c00\n" \
"buffer_load_dwordx4 v27, s[68:71], s84 offen offset:3072 lds\n" \
"s_movk_i32 s7, 0x300\n" \
"s_lshr_b32 s4, s2, 1\n" \
"s_and_b32 s4, s4, 60\n" \
"s_or_b32 s4, s4, s3\n" \
"v_lshrrev_b32_e32 v212, 4, v20\n" \
"v_min_u32_e32 v212, 59, v212\n" \
"s_cmpk_lt_u32 s2, 0x80\n" \
"s_cselect_b32 s6, s14, s16\n" \
"v_subrev_u32_e32 v214, 48, v212\n" \
"v_mov_b32_e32 v212, s10\n" \
"v_mov_b32_e32 v215, s6\n" \
"s_cmpk_lt_u32 s2, 0x80\n" \
"s_cselect_b32 s5, s15, s17\n" \
"v_cmp_gt_u32_e32 vcc, s7, v20\n" \
"v_mov_b32_e64 v213, s11\n" \
"v_bfe_u32 v216, v20, 4, 3\n" \
"v_cndmask_b32_e32 v212, v212, v215, vcc\n" \
"v_mov_b32_e32 v215, s5\n" \
"s_lshl_b32 s5, s3, 4\n" \
"v_cndmask_b32_e32 v213, v213, v215, vcc\n" \
"v_lshlrev_b32_e64 v215, 5, v20\n" \
"v_and_b32_e32 v215, 0x7000, v215\n" \
"v_lshl_add_u32 v215, s4, 6, v215\n" \
"s_lshl_b32 s4, s2, 3\n" \
"s_and_b32 s4, s4, 56\n" \
"v_or3_b32 v215, v215, s4, v216\n" \
"s_lshl_b32 s4, s2, 6\n" \
"s_or_b32 s6, s5, s4\n" \
"v_cmp_gt_u32_e64 s[4:5], 6, v214\n" \
"v_add_u32_e32 v215, 0x2000, v215\n" \
"s_nop 0\n" \
"v_cndmask_b32_e64 v216, 4, 2, s[4:5]\n" \
"v_add3_u32 v214, s6, v214, v216\n" \
"s_lshl_b32 s4, s3, 9\n" \
"v_cndmask_b32_e32 v214, v214, v215, vcc\n" \
"v_lshlrev_b32_e32 v214, 7, v214\n" \
"v_mov_b32_e64 v215, 0\n" \
"v_lshl_add_u64 v[184:185], v[212:213], 0, v[214:215]\n" \
"s_or_b32 s32, s4, 0x25000\n" \
"s_add_u32 m0, s30, 0x1000\n" \
"s_mov_b32 s84, 0x8000\n" \
"buffer_load_dwordx4 v22, s[64:67], s84 offen offset:0 lds\n" \
"s_and_b32 s33, s3, 1\n" \
"v_lshrrev_b32_e32 v212, 4, v20\n" \
"v_lshrrev_b32_e64 v214, 8, v20\n" \
"v_bfe_u32 v212, v212, 1, 3\n" \
"v_lshlrev_b32_e32 v215, 2, v20\n" \
"s_addk_i32 s84, 0x1c00\n" \
"buffer_load_dwordx4 v23, s[64:67], s84 offen offset:1024 lds\n" \
"v_lshrrev_b32_e64 v216, 1, v20\n" \
"v_bfe_u32 v213, v20, 4, 4\n" \
"s_lshl_b32 s4, s3, 5\n" \
"v_xor_b32_e32 v212, v212, v214\n" \
"v_lshlrev_b32_e32 v214, 11, v214\n" \
"s_addk_i32 s84, 0x1c00\n" \
"buffer_load_dwordx4 v22, s[64:67], s84 offen offset:2048 lds\n" \
"v_and_b32_e32 v215, 0x300, v215\n" \
"v_and_b32_e64 v217, 24, v216\n" \
"v_and_or_b32 v213, s4, 64, v213\n" \
"v_or3_b32 v214, v215, v214, v217\n" \
"s_lshl_b32 s4, s33, 14\n" \
"s_addk_i32 s84, 0x1c00\n" \
"buffer_load_dwordx4 v23, s[64:67], s84 offen offset:3072 lds\n" \
"v_and_b32_e32 v215, 0xe0, v216\n" \
"v_lshlrev_b32_e32 v213, 7, v213\n" \
"v_lshlrev_b32_e32 v212, 4, v212\n" \
"v_or3_b32 v214, v214, s4, v215\n" \
"v_or_b32_e64 v28, v212, v213\n" \
"s_add_u32 m0, s31, 0x4000\n" \
"s_movk_i32 s84, 0x0\n" \
"buffer_load_dwordx4 v26, s[72:75], s84 offen offset:0 lds\n" \
"v_add_u32_e32 v32, 0x10000, v214\n" \
"v_bitop3_b32 v29, v212, 64, v213 bitop3:0x36\n" \
"v_xor_b32_e32 v33, 0x20, v32\n" \
"v_xor_b32_e64 v34, 0x40, v32\n" \
"v_xor_b32_e32 v35, 0x60, v32\n" \
"s_movk_i32 s84, 0x7c00\n" \
"buffer_load_dwordx4 v26, s[72:75], s84 offen offset:1024 lds\n" \
"v_xor_b32_e32 v36, 0x80, v32\n" \
"v_xor_b32_e32 v37, 0xa0, v32\n" \
"v_xor_b32_e32 v38, 0xc0, v32\n" \
"v_xor_b32_e32 v39, 0xe0, v32\n" \
"s_mov_b32 s80, s26\n" \
"s_addk_i32 s84, 0x7c00\n" \
"buffer_load_dwordx4 v27, s[72:75], s84 offen offset:2048 lds\n" \
"s_and_b32 s81, s27, 0xffff\n" \
"s_mov_b32 s82, 0x10000000\n" \
"s_mov_b32 s83, 0x20000\n" \
"s_mov_b32 s76, s18\n" \
"s_and_b32 s77, s19, 0xffff\n" \
"s_addk_i32 s84, 0x7c00\n" \
"buffer_load_dwordx4 v27, s[72:75], s84 offen offset:3072 lds\n" \
"s_mov_b32 s78, 0x1e6000\n" \
"s_mov_b32 s79, 0x20000\n" \
"s_branch L_pgo_%=\n" \
"L_pret:\n" \
"s_setpc_b64 s[88:89]\n" \
"L_pgo_%=:\n" \
"s_mul_i32 s88, s33, 0xa000\n" \
"s_lshl_b32 s89, s3, 11\n" \
"s_and_b32 s89, s89, 0x1000\n" \
"s_add_u32 s88, s88, s89\n" \
"s_getpc_b64 s[86:87]\n" \
"L_hpa_%=:\n" \
"s_add_u32 s86, s86, L_pp1-L_hpa_%=\n" \
"s_addc_u32 s87, s87, 0\n" \
"s_add_u32 s86, s86, s88\n" \
"s_addc_u32 s87, s87, 0\n" \
"s_add_u32 m0, s30, 0x8000\n" \
"s_movk_i32 s84, 0x80\n" \
"buffer_load_dwordx4 v22, s[64:67], s84 offen offset:0 lds\n" \
"s_lshr_b32 s4, s2, 1\n" \
"s_and_b32 s4, s4, 0x7c\n" \
"v_bfe_u32 v212, v20, 4, 5\n" \
"s_movk_i32 s84, 0x1c80\n" \
"buffer_load_dwordx4 v23, s[64:67], s84 offen offset:1024 lds\n" \
"s_or_b32 s4, s4, s3\n" \
"v_min_u32_e32 v212, 26, v212\n" \
"v_mad_u64_u32 v[212:213], s[4:5], s4, 27, v[212:213]\n" \
"s_movk_i32 s84, 0x3880\n" \
"buffer_load_dwordx4 v22, s[64:67], s84 offen offset:2048 lds\n" \
"v_mul_u32_u24_e32 v213, 0xaaab, v212\n" \
"v_lshrrev_b32_e32 v213, 18, v213\n" \
"s_mov_b32 s4, 0x1fffffa\n" \
"s_movk_i32 s84, 0x5480\n" \
"buffer_load_dwordx4 v23, s[64:67], s84 offen offset:3072 lds\n" \
"v_mad_u64_u32 v[214:215], s[4:5], v213, s4, v[212:213]\n" \
"s_and_b32 s6, s2, 7\n" \
"v_mul_u32_u24_e32 v213, 27, v213\n" \
"s_mov_b64 exec, 0\n" \
"s_swappc_b64 s[88:89], s[86:87]\n" \
"s_mov_b64 exec, -1\n" \
"s_add_u32 s86, s86, 0x2000\n" \
"s_addc_u32 s87, s87, 0\n" \
"s_add_u32 m0, s31, 0x8000\n" \
"s_mov_b32 s84, 0x80000\n" \
"buffer_load_dwordx4 v26, s[68:71], s84 offen offset:0 lds\n" \
"v_lshrrev_b32_e32 v214, 1, v214\n" \
"v_and_b32_e32 v212, 1, v212\n" \
"v_mul_u32_u24_e32 v214, 9, v214\n" \
"s_addk_i32 s84, 0x7c00\n" \
"buffer_load_dwordx4 v26, s[68:71], s84 offen offset:1024 lds\n" \
"v_add3_u32 v212, v212, s6, v213\n" \
"v_add_lshl_u32 v212, v212, v214, 7\n" \
"v_mov_b32_e32 v213, 0\n" \
"s_mov_b64 exec, 0\n" \
"s_swappc_b64 s[88:89], s[86:87]\n" \
"s_mov_b64 exec, -1\n" \
"s_add_u32 s86, s86, 0x2000\n" \
"s_addc_u32 s87, s87, 0\n" \
"s_addk_i32 s84, 0x7c00\n" \
"buffer_load_dwordx4 v27, s[68:71], s84 offen offset:2048 lds\n" \
"v_lshl_add_u64 v[186:187], s[18:19], 0, v[212:213]\n" \
"s_lshl_b32 s35, s2, 10\n" \
"s_lshr_b32 s37, s2, 1\n" \
"s_addk_i32 s84, 0x7c00\n" \
"buffer_load_dwordx4 v27, s[68:71], s84 offen offset:3072 lds\n" \
"s_mul_i32 s37, s37, 0x24800\n" \
"s_and_b32 s35, s35, 0x400\n" \
"s_lshl_b32 s36, s3, 8\n" \
"s_mov_b64 exec, 0\n" \
"s_swappc_b64 s[88:89], s[86:87]\n" \
"s_mov_b64 exec, -1\n" \
"s_add_u32 s86, s86, 0x2000\n" \
"s_addc_u32 s87, s87, 0\n" \
"s_add_u32 m0, s30, 0x9000\n" \
"s_mov_b32 s84, 0x8080\n" \
"buffer_load_dwordx4 v22, s[64:67], s84 offen offset:0 lds\n" \
"s_or_b32 s35, s37, s35\n" \
"s_and_b32 s4, s2, 7\n" \
"s_lshr_b32 s6, s2, 5\n" \
"s_addk_i32 s84, 0x1c00\n" \
"buffer_load_dwordx4 v23, s[64:67], s84 offen offset:1024 lds\n" \
"s_lshl_b32 s34, s2, 5\n" \
"s_add_i32 s35, s35, s36\n" \
"s_lshl_b32 s7, s4, 10\n" \
"s_addk_i32 s84, 0x1c00\n" \
"buffer_load_dwordx4 v22, s[64:67], s84 offen offset:2048 lds\n" \
"s_and_b32 s34, s34, 0x300\n" \
"s_add_i32 s35, s35, 0x3004000\n" \
"s_lshl_b32 s37, s6, 13\n" \
"s_mov_b64 exec, 0\n" \
"s_swappc_b64 s[88:89], s[86:87]\n" \
"s_mov_b64 exec, -1\n" \
"s_andn2_b32 s85, 2, s3\n" \
"s_lshl_b32 s85, s85, 12\n" \
"s_add_u32 s86, s86, s85\n" \
"s_addc_u32 s87, s87, 0\n" \
"s_addk_i32 s84, 0x1c00\n" \
"buffer_load_dwordx4 v23, s[64:67], s84 offen offset:3072 lds\n" \
"s_or_b32 s34, s7, s34\n" \
"s_or_b32 s7, s37, s7\n" \
"v_mov_b32_e64 v213, s35\n" \
"s_add_u32 m0, s31, 0xc000\n" \
"s_mov_b32 s84, 0x80000\n" \
"buffer_load_dwordx4 v26, s[72:75], s84 offen offset:0 lds\n" \
"s_or_b32 s7, s7, s36\n" \
"s_lshr_b32 s5, s2, 3\n" \
"s_or_b32 s7, s7, 0x5c00000\n" \
"s_mov_b64 exec, 0\n" \
"s_swappc_b64 s[88:89], s[86:87]\n" \
"s_mov_b64 exec, -1\n" \
"s_addk_i32 s84, 0x7c00\n" \
"buffer_load_dwordx4 v26, s[72:75], s84 offen offset:1024 lds\n" \
"v_writelane_b32 v213, s7, 2\n" \
"s_mul_i32 s5, s5, 0x90000\n" \
"v_writelane_b32 v213, s7, 3\n" \
"s_addk_i32 s84, 0x7c00\n" \
"buffer_load_dwordx4 v27, s[72:75], s84 offen offset:2048 lds\n" \
"s_lshl_b32 s7, s3, 14\n" \
"s_lshl_b32 s4, s4, 16\n" \
"s_add_i32 s5, s5, s7\n" \
"s_addk_i32 s84, 0x7c00\n" \
"buffer_load_dwordx4 v27, s[72:75], s84 offen offset:3072 lds\n" \
"s_or_b32 s34, s34, 0x5be8000\n" \
"v_writelane_b32 v213, s34, 4\n" \
"s_add_i32 s4, s5, s4\n" \
"v_writelane_b32 v213, s34, 5\n" \
"s_add_i32 s4, s4, 0x4243800\n" \
"v_lshlrev_b32_e64 v212, 3, v20\n" \
"v_and_b32_e32 v212, 0x80, v212\n" \
"v_writelane_b32 v213, s4, 6\n" \
"s_mul_i32 s4, s6, 0xa00\n" \
"s_add_i32 s4, s4, 0x5bc0f80\n" \
"v_writelane_b32 v213, s4, 7\n" \
"v_add_u32_e32 v49, v213, v212\n" \
"s_mov_b32 m0, s32\n" \
"s_mov_b32 exec_lo, 0xffffff\n" \
"s_mov_b32 exec_hi, 0xfff0000\n" \
"global_load_lds_dword v[184:185], off\n" \
"s_mov_b64 exec, -1\n" \
"s_mov_b32 m0, s32\n" \
"s_mov_b64 exec, 0xff\n" \
"buffer_load_dword v49, s[80:83], 0 offen lds\n" \
"s_mov_b64 exec, -1\n" \
"s_mov_b32 s84, 0x5bc0000\n" \
"buffer_load_dword v180, off, s[80:83], s84 sc1\n" \
"s_cmp_lg_u32 s33, 0\n" \
"s_cbranch_scc1 L_w1_%=\n" \
"L_w0_%=:\n" \
"s_waitcnt vmcnt(19)\n" \
"s_barrier\n" \
"s_nop 0\n" \
"ds_read_b64_tr_b16 a[160:161], v32 offset:0\n" \
"ds_read_b64_tr_b16 a[162:163], v32 offset:1024\n" \
"ds_read_b128 a[128:131], v28 offset:16384\n" \
"ds_read_b64_tr_b16 a[224:225], v32 offset:8192\n" \
"ds_read_b64_tr_b16 a[226:227], v32 offset:9216\n" \
"ds_read_b128 a[192:195], v29 offset:16384\n" \
"ds_read_b128 a[132:135], v28 offset:18432\n" \
"ds_read_b128 a[196:199], v29 offset:18432\n" \
"ds_read_b64_tr_b16 a[164:165], v33 offset:0\n" \
"ds_read_b64_tr_b16 a[166:167], v33 offset:1024\n" \
"ds_read_b64_tr_b16 a[228:229], v33 offset:8192\n" \
"ds_read_b64_tr_b16 a[230:231], v33 offset:9216\n" \
"ds_read_b128 a[136:139], v28 offset:20480\n" \
"ds_read_b128 a[200:203], v29 offset:20480\n" \
"s_waitcnt lgkmcnt(11)\n" \
"v_mfma_f32_16x16x32_bf16 a[0:3], a[160:163], a[128:131], 0\n" \
"ds_read_b64_tr_b16 a[168:169], v34 offset:0\n" \
"s_movk_i32 s5, 0xd80\n" \
"s_waitcnt lgkmcnt(9)\n" \
"v_mfma_f32_16x16x32_bf16 a[0:3], a[224:227], a[192:195], a[0:3]\n" \
"ds_read_b64_tr_b16 a[170:171], v34 offset:1024\n" \
"s_lshr_b32 s6, s2, 5\n" \
"s_waitcnt lgkmcnt(9)\n" \
"v_mfma_f32_16x16x32_bf16 a[16:19], a[160:163], a[132:135], 0\n" \
"ds_read_b64_tr_b16 a[232:233], v34 offset:8192\n" \
"v_lshrrev_b32_e32 v212, 4, v20\n" \
"s_waitcnt lgkmcnt(9)\n" \
"v_mfma_f32_16x16x32_bf16 a[16:19], a[224:227], a[196:199], a[16:19]\n" \
"ds_read_b64_tr_b16 a[234:235], v34 offset:9216\n" \
"v_lshl_or_b32 v215, s3, 6, v212\n" \
"s_waitcnt lgkmcnt(8)\n" \
"v_mfma_f32_16x16x32_bf16 a[20:23], a[164:167], a[132:135], 0\n" \
"ds_read_b128 a[140:143], v28 offset:22528\n" \
"v_mul_u32_u24_e32 v216, 0x2aab, v215\n" \
"s_waitcnt lgkmcnt(7)\n" \
"v_mfma_f32_16x16x32_bf16 a[20:23], a[228:231], a[196:199], a[20:23]\n" \
"ds_read_b128 a[204:207], v29 offset:22528\n" \
"v_lshrrev_b32_e32 v216, 16, v216\n" \
"v_mfma_f32_16x16x32_bf16 a[4:7], a[164:167], a[128:131], 0\n" \
"ds_read_b64_tr_b16 a[172:173], v35 offset:0\n" \
"v_mad_i32_i24 v217, v216, -6, v215\n" \
"v_mfma_f32_16x16x32_bf16 a[4:7], a[228:231], a[192:195], a[4:7]\n" \
"ds_read_b64_tr_b16 a[174:175], v35 offset:1024\n" \
"v_add_u32_e32 v218, 0xfffffe50, v215\n" \
"s_waitcnt lgkmcnt(9)\n" \
"v_mfma_f32_16x16x32_bf16 a[32:35], a[160:163], a[136:139], 0\n" \
"ds_read_b64_tr_b16 a[236:237], v35 offset:8192\n" \
"s_mul_i32 s35, s6, 0x48\n" \
"s_waitcnt lgkmcnt(9)\n" \
"v_mfma_f32_16x16x32_bf16 a[32:35], a[224:227], a[200:203], a[32:35]\n" \
"ds_read_b64_tr_b16 a[238:239], v35 offset:9216\n" \
"v_lshrrev_b32_e64 v219, 1, v217\n" \
"v_mfma_f32_16x16x32_bf16 a[36:39], a[164:167], a[136:139], 0\n" \
"ds_read_b128 a[144:147], v28 offset:0\n" \
"v_add_u32_e64 v218, s35, v216\n" \
"v_mfma_f32_16x16x32_bf16 a[36:39], a[228:231], a[200:203], a[36:39]\n" \
"ds_read_b128 a[208:211], v29 offset:0\n" \
"v_mul_u32_u24_e32 v219, 0x480, v219\n" \
"s_waitcnt lgkmcnt(10)\n" \
"v_mfma_f32_16x16x32_bf16 a[40:43], a[168:171], a[136:139], 0\n" \
"ds_read_b128 a[148:151], v28 offset:2048\n" \
"v_and_b32_e32 v214, 16, v20\n" \
"s_waitcnt lgkmcnt(9)\n" \
"v_mfma_f32_16x16x32_bf16 a[40:43], a[232:235], a[200:203], a[40:43]\n" \
"ds_read_b128 a[212:215], v29 offset:2048\n" \
"v_mad_u32_u24 v219, v218, s5, v219\n" \
"v_mfma_f32_16x16x32_bf16 a[24:27], a[168:171], a[132:135], 0\n" \
"ds_read_b128 a[152:155], v28 offset:4096\n" \
"v_or_b32_e32 v192, v219, v214\n" \
"v_mfma_f32_16x16x32_bf16 a[24:27], a[232:235], a[196:199], a[24:27]\n" \
"ds_read_b128 a[216:219], v29 offset:4096\n" \
"v_mov_b32_e64 v220, 2.0\n" \
"v_mfma_f32_16x16x32_bf16 a[8:11], a[168:171], a[128:131], 0\n" \
"ds_read_b128 a[156:159], v28 offset:6144\n" \
"v_mov_b32_e64 v222, 7\n" \
"v_mfma_f32_16x16x32_bf16 a[8:11], a[232:235], a[192:195], a[8:11]\n" \
"ds_read_b128 a[220:223], v29 offset:6144\n" \
"s_mov_b32 m0, s32\n" \
"s_waitcnt lgkmcnt(13)\n" \
"v_mfma_f32_16x16x32_bf16 a[48:51], a[160:163], a[140:143], 0\n" \
"s_mov_b32 exec_lo, 0xff000000\n" \
"s_mov_b32 exec_hi, 0x0\n" \
"global_load_lds_dword v[184:185], off\n" \
"s_mov_b64 exec, -1\n" \
"s_waitcnt lgkmcnt(12)\n" \
"v_mfma_f32_16x16x32_bf16 a[48:51], a[224:227], a[204:207], a[48:51]\n" \
"ds_read_b64_tr_b16 a[176:177], v36 offset:0\n" \
"v_lshlrev_b32_e32 v220, 7, v218\n" \
"v_mfma_f32_16x16x32_bf16 a[52:55], a[164:167], a[140:143], 0\n" \
"ds_read_b64_tr_b16 a[178:179], v36 offset:1024\n" \
"v_lshrrev_b32_e64 v218, 1, v218\n" \
"v_mfma_f32_16x16x32_bf16 a[52:55], a[228:231], a[204:207], a[52:55]\n" \
"s_mov_b32 m0, s32\n" \
"ds_read_b64_tr_b16 a[240:241], v36 offset:8192\n" \
"v_mfma_f32_16x16x32_bf16 a[56:59], a[168:171], a[140:143], 0\n" \
"s_mov_b32 exec_lo, 0xffff\n" \
"s_mov_b32 exec_hi, 0x0\n" \
"global_load_lds_dword v[186:187], off\n" \
"s_mov_b64 exec, -1\n" \
"v_mfma_f32_16x16x32_bf16 a[56:59], a[232:235], a[204:207], a[56:59]\n" \
"ds_read_b64_tr_b16 a[242:243], v36 offset:9216\n" \
"v_and_b32_e32 v218, 4, v218\n" \
"s_waitcnt lgkmcnt(14)\n" \
"v_mfma_f32_16x16x32_bf16 a[60:63], a[172:175], a[140:143], 0\n" \
"ds_read_b64_tr_b16 a[180:181], v37 offset:0\n" \
"v_and_b32_e32 v216, 2, v216\n" \
"s_waitcnt lgkmcnt(13)\n" \
"s_nop 0\n" \
"v_mfma_f32_16x16x32_bf16 a[60:63], a[236:239], a[204:207], a[60:63]\n" \
"ds_read_b64_tr_b16 a[182:183], v37 offset:1024\n" \
"s_mul_i32 s7, s6, 0x2400\n" \
"v_mfma_f32_16x16x32_bf16 a[44:47], a[172:175], a[136:139], 0\n" \
"ds_read_b64_tr_b16 a[244:245], v37 offset:8192\n" \
"s_sub_i32 s34, 0x22000, s7\n" \
"v_mfma_f32_16x16x32_bf16 a[44:47], a[236:239], a[200:203], a[44:47]\n" \
"ds_read_b64_tr_b16 a[246:247], v37 offset:9216\n" \
"v_bitop3_b32 v216, v217, v218, v216 bitop3:0x1e\n" \
"v_mfma_f32_16x16x32_bf16 a[28:31], a[172:175], a[132:135], 0\n" \
"ds_read_b64_tr_b16 a[184:185], v38 offset:0\n" \
"v_or_b32_e32 v213, 0x24400, v20\n" \
"v_mfma_f32_16x16x32_bf16 a[28:31], a[236:239], a[196:199], a[28:31]\n" \
"ds_read_b64_tr_b16 a[186:187], v38 offset:1024\n" \
"v_lshlrev_b32_e64 v216, 4, v216\n" \
"v_mfma_f32_16x16x32_bf16 a[12:15], a[172:175], a[128:131], 0\n" \
"v_add3_u32 v196, v220, s34, v216\n" \
"v_mov_b32_e64 v216, v213\n" \
"v_mfma_f32_16x16x32_bf16 a[12:15], a[236:239], a[192:195], a[12:15]\n" \
"s_movk_i32 s36, 0x1b0\n" \
"v_or_b32_e32 v216, 0x100, v215\n" \
"s_waitcnt lgkmcnt(15)\n" \
"v_mfma_f32_16x16x32_bf16 a[76:79], a[172:175], a[144:147], 0\n" \
"ds_read_b64_tr_b16 a[248:249], v38 offset:8192\n" \
"v_mul_u32_u24_e32 v217, 0x2aab, v216\n" \
"v_mfma_f32_16x16x32_bf16 a[76:79], a[236:239], a[208:211], a[76:79]\n" \
"v_add_u32_e32 v215, 0xffffff50, v215\n" \
"v_lshrrev_b32_e64 v217, 16, v217\n" \
"v_mfma_f32_16x16x32_bf16 a[72:75], a[168:171], a[144:147], 0\n" \
"v_mov_b32_e64 v220, v215\n" \
"v_mad_i32_i24 v218, v217, -6, v216\n" \
"v_mfma_f32_16x16x32_bf16 a[72:75], a[232:235], a[208:211], a[72:75]\n" \
"ds_read_b64_tr_b16 a[250:251], v38 offset:9216\n" \
"v_cmp_gt_u32_e64 vcc, s36, v216\n" \
"v_mfma_f32_16x16x32_bf16 a[68:71], a[164:167], a[144:147], 0\n" \
"v_mov_b32_e32 v221, 7\n" \
"v_lshrrev_b32_e32 v222, 1, v218\n" \
"v_mfma_f32_16x16x32_bf16 a[68:71], a[228:231], a[208:211], a[68:71]\n" \
"v_cndmask_b32_e32 v217, v220, v217, vcc\n" \
"s_movk_i32 s4, 0x48\n" \
"v_mfma_f32_16x16x32_bf16 a[64:67], a[160:163], a[144:147], 0\n" \
"v_add_u32_e64 v220, s35, v217\n" \
"v_mul_u32_u24_e32 v222, 0x480, v222\n" \
"v_mfma_f32_16x16x32_bf16 a[64:67], a[224:227], a[208:211], a[64:67]\n" \
"ds_read_b64_tr_b16 a[188:189], v39 offset:0\n" \
"v_mad_u32_u24 v222, v220, s5, v222\n" \
"v_mfma_f32_16x16x32_bf16 a[80:83], a[160:163], a[148:151], 0\n" \
"v_cmp_gt_u32_e64 s[4:5], s4, v215\n" \
"v_cndmask_b32_e32 v215, v221, v218, vcc\n" \
"s_waitcnt lgkmcnt(15)\n" \
"v_mfma_f32_16x16x32_bf16 a[80:83], a[224:227], a[212:215], a[80:83]\n" \
"v_lshrrev_b32_e32 v218, 1, v220\n" \
"v_or_b32_e32 v214, v222, v214\n" \
"v_mfma_f32_16x16x32_bf16 a[84:87], a[164:167], a[148:151], 0\n" \
"ds_read_b64_tr_b16 a[190:191], v39 offset:1024\n" \
"v_mov_b32_e64 v222, 2.0\n" \
"v_mfma_f32_16x16x32_bf16 a[84:87], a[228:231], a[212:215], a[84:87]\n" \
"v_and_b32_e32 v218, 4, v218\n" \
"v_and_b32_e32 v217, 2, v217\n" \
"v_mfma_f32_16x16x32_bf16 a[88:91], a[168:171], a[148:151], 0\n" \
"v_bitop3_b32 v215, v215, v218, v217 bitop3:0x1e\n" \
"v_lshlrev_b32_e64 v223, 1, v220\n" \
"v_mfma_f32_16x16x32_bf16 a[88:91], a[232:235], a[212:215], a[88:91]\n" \
"ds_read_b64_tr_b16 a[252:253], v39 offset:8192\n" \
"v_mov_b32_e64 v224, 2.0\n" \
"v_mfma_f32_16x16x32_bf16 a[92:95], a[172:175], a[148:151], 0\n" \
"v_cndmask_b32_e32 v193, v222, v214, vcc\n" \
"v_lshlrev_b32_e32 v214, 7, v220\n" \
"v_mfma_f32_16x16x32_bf16 a[92:95], a[236:239], a[212:215], a[92:95]\n" \
"v_lshlrev_b32_e32 v215, 4, v215\n" \
"s_mul_i32 s39, s3, 18\n" \
"v_mfma_f32_16x16x32_bf16 a[108:111], a[172:175], a[152:155], 0\n" \
"ds_read_b64_tr_b16 a[254:255], v39 offset:9216\n" \
"v_add3_u32 v214, v214, s34, v215\n" \
"v_mfma_f32_16x16x32_bf16 a[108:111], a[236:239], a[216:219], a[108:111]\n" \
"v_cndmask_b32_e64 v195, v224, v223, s[4:5]\n" \
"s_movk_i32 s4, 0x1f8\n" \
"v_mfma_f32_16x16x32_bf16 a[104:107], a[168:171], a[152:155], 0\n" \
"v_mov_b32_e32 v215, v213\n" \
"s_add_i32 s35, s35, s39\n" \
"v_mfma_f32_16x16x32_bf16 a[104:107], a[232:235], a[216:219], a[104:107]\n" \
"v_cmp_gt_u32_e32 vcc, s4, v216\n" \
"v_add_u32_e64 v212, s35, v212\n" \
"v_mfma_f32_16x16x32_bf16 a[100:103], a[164:167], a[152:155], 0\n" \
"v_mov_b32_e32 v219, 2.0\n" \
"v_cndmask_b32_e32 v197, v215, v214, vcc\n" \
"v_mfma_f32_16x16x32_bf16 a[100:103], a[228:231], a[216:219], a[100:103]\n" \
"s_movk_i32 s35, 0x120\n" \
"v_mul_u32_u24_e32 v214, 0xd80, v212\n" \
"v_mfma_f32_16x16x32_bf16 a[96:99], a[160:163], a[152:155], 0\n" \
"v_cmp_gt_u32_e32 vcc, s35, v20\n" \
"v_lshrrev_b32_e64 v215, 1, v212\n" \
"v_mfma_f32_16x16x32_bf16 a[96:99], a[224:227], a[216:219], a[96:99]\n" \
"s_and_b32 s4, s2, 7\n" \
"v_cndmask_b32_e32 v194, v219, v214, vcc\n" \
"s_waitcnt lgkmcnt(15)\n" \
"v_mfma_f32_16x16x32_bf16 a[112:115], a[160:163], a[156:159], 0\n" \
"v_lshlrev_b32_e32 v214, 7, v212\n" \
"v_lshrrev_b32_e64 v212, 2, v212\n" \
"v_mfma_f32_16x16x32_bf16 a[112:115], a[224:227], a[220:223], a[112:115]\n" \
"v_and_b32_e64 v212, 2, v212\n" \
"v_and_or_b32 v212, v215, 1, v212\n" \
"v_mfma_f32_16x16x32_bf16 a[116:119], a[164:167], a[156:159], 0\n" \
"s_lshl_b32 s5, s4, 2\n" \
"s_bfe_u32 s38, s2, 0x20003\n" \
"v_mfma_f32_16x16x32_bf16 a[116:119], a[228:231], a[220:223], a[116:119]\n" \
"v_lshlrev_b32_e32 v212, 5, v212\n" \
"s_movk_i32 s35, 0x60\n" \
"v_mfma_f32_16x16x32_bf16 a[120:123], a[168:171], a[156:159], 0\n" \
"s_or_b32 s5, s5, s38\n" \
"v_bitop3_b32 v212, v212, s35, v214 bitop3:0x36\n" \
"v_mfma_f32_16x16x32_bf16 a[120:123], a[232:235], a[220:223], a[120:123]\n" \
"s_lshr_b32 s36, s2, 3\n" \
"v_add_u32_e32 v212, s34, v212\n" \
"v_mfma_f32_16x16x32_bf16 a[124:127], a[172:175], a[156:159], 0\n" \
"s_mul_i32 s34, s5, 0x12000\n" \
"s_or_b32 s40, s34, 0x6000000\n" \
"v_mfma_f32_16x16x32_bf16 a[124:127], a[236:239], a[220:223], a[124:127]\n" \
"s_waitcnt vmcnt(5) lgkmcnt(0)\n" \
"s_barrier\n" \
"v_mfma_f32_16x16x32_bf16 v[52:55], a[176:179], a[128:131], 0\n" \
"s_add_u32 m0, s30, 0x0\n" \
"s_movk_i32 s84, 0x100\n" \
"v_mfma_f32_16x16x32_bf16 v[52:55], a[240:243], a[192:195], v[52:55]\n" \
"buffer_load_dwordx4 v22, s[64:67], s84 offen offset:0 lds\n" \
"v_mfma_f32_16x16x32_bf16 v[56:59], a[180:183], a[128:131], 0\n" \
"ds_read_b64_tr_b16 a[160:161], v32 offset:32768\n" \
"s_mul_i32 s34, s36, 0x90000\n" \
"v_mfma_f32_16x16x32_bf16 v[56:59], a[244:247], a[192:195], v[56:59]\n" \
"s_movk_i32 s84, 0x1d00\n" \
"v_mfma_f32_16x16x32_bf16 v[60:63], a[184:187], a[128:131], 0\n" \
"buffer_load_dwordx4 v23, s[64:67], s84 offen offset:1024 lds\n" \
"v_mfma_f32_16x16x32_bf16 v[60:63], a[248:251], a[192:195], v[60:63]\n" \
"ds_read_b64_tr_b16 a[162:163], v32 offset:33792\n" \
"s_lshl_b32 s36, s3, 14\n" \
"v_mfma_f32_16x16x32_bf16 v[64:67], a[188:191], a[128:131], 0\n" \
"ds_read_b64_tr_b16 a[224:225], v32 offset:40960\n" \
"s_add_i32 s34, s34, s36\n" \
"v_mfma_f32_16x16x32_bf16 v[64:67], a[252:255], a[192:195], v[64:67]\n" \
"ds_read_b128 a[128:131], v28 offset:49152\n" \
"s_lshl_b32 s38, s5, 8\n" \
"v_mfma_f32_16x16x32_bf16 v[80:83], a[188:191], a[132:135], 0\n" \
"ds_read_b128 a[192:195], v29 offset:49152\n" \
"s_add_i32 s41, s34, 0x4240000\n" \
"v_mfma_f32_16x16x32_bf16 v[80:83], a[252:255], a[196:199], v[80:83]\n" \
"s_movk_i32 s84, 0x3900\n" \
"v_mfma_f32_16x16x32_bf16 v[76:79], a[184:187], a[132:135], 0\n" \
"buffer_load_dwordx4 v22, s[64:67], s84 offen offset:2048 lds\n" \
"v_mfma_f32_16x16x32_bf16 v[76:79], a[248:251], a[196:199], v[76:79]\n" \
"s_movk_i32 s84, 0x5500\n" \
"v_mfma_f32_16x16x32_bf16 v[72:75], a[180:183], a[132:135], 0\n" \
"buffer_load_dwordx4 v23, s[64:67], s84 offen offset:3072 lds\n" \
"v_mfma_f32_16x16x32_bf16 v[72:75], a[244:247], a[196:199], v[72:75]\n" \
"s_add_u32 m0, s31, 0x0\n" \
"s_mov_b32 s84, 0x100000\n" \
"v_mfma_f32_16x16x32_bf16 v[68:71], a[176:179], a[132:135], 0\n" \
"buffer_load_dwordx4 v26, s[68:71], s84 offen offset:0 lds\n" \
"v_mfma_f32_16x16x32_bf16 v[68:71], a[240:243], a[196:199], v[68:71]\n" \
"ds_read_b128 a[132:135], v28 offset:51200\n" \
"s_mul_i32 s34, s5, 36\n" \
"v_mfma_f32_16x16x32_bf16 v[84:87], a[176:179], a[136:139], 0\n" \
"ds_read_b128 a[196:199], v29 offset:51200\n" \
"s_add_i32 s5, s7, s40\n" \
"v_mfma_f32_16x16x32_bf16 v[84:87], a[240:243], a[200:203], v[84:87]\n" \
"s_addk_i32 s84, 0x7c00\n" \
"v_mfma_f32_16x16x32_bf16 v[88:91], a[180:183], a[136:139], 0\n" \
"buffer_load_dwordx4 v26, s[68:71], s84 offen offset:1024 lds\n" \
"v_mfma_f32_16x16x32_bf16 v[88:91], a[244:247], a[200:203], v[88:91]\n" \
"ds_read_b64_tr_b16 a[226:227], v32 offset:41984\n" \
"s_addk_i32 s5, 0x2000\n" \
"v_mfma_f32_16x16x32_bf16 v[92:95], a[184:187], a[136:139], 0\n" \
"s_addk_i32 s84, 0x7c00\n" \
"v_mfma_f32_16x16x32_bf16 v[92:95], a[248:251], a[200:203], v[92:95]\n" \
"buffer_load_dwordx4 v27, s[68:71], s84 offen offset:2048 lds\n" \
"v_mfma_f32_16x16x32_bf16 v[96:99], a[188:191], a[136:139], 0\n" \
"ds_read_b64_tr_b16 a[164:165], v33 offset:32768\n" \
"s_mov_b32 s37, 2.0\n" \
"v_mfma_f32_16x16x32_bf16 v[96:99], a[252:255], a[200:203], v[96:99]\n" \
"ds_read_b128 a[136:139], v28 offset:53248\n" \
"s_or_b32 s38, s38, 0x5be8000\n" \
"v_mfma_f32_16x16x32_bf16 v[112:115], a[188:191], a[140:143], 0\n" \
"ds_read_b128 a[200:203], v29 offset:53248\n" \
"v_mfma_f32_16x16x32_bf16 v[112:115], a[252:255], a[204:207], v[112:115]\n" \
"s_addk_i32 s84, 0x7c00\n" \
"v_mfma_f32_16x16x32_bf16 v[108:111], a[184:187], a[140:143], 0\n" \
"buffer_load_dwordx4 v27, s[68:71], s84 offen offset:3072 lds\n" \
"v_mfma_f32_16x16x32_bf16 v[108:111], a[248:251], a[204:207], v[108:111]\n" \
"s_add_u32 m0, s30, 0x1000\n" \
"s_mov_b32 s84, 0x8100\n" \
"v_mfma_f32_16x16x32_bf16 v[104:107], a[180:183], a[140:143], 0\n" \
"buffer_load_dwordx4 v22, s[64:67], s84 offen offset:0 lds\n" \
"v_mfma_f32_16x16x32_bf16 v[104:107], a[244:247], a[204:207], v[104:107]\n" \
"ds_read_b64_tr_b16 a[166:167], v33 offset:33792\n" \
"v_mfma_f32_16x16x32_bf16 v[100:103], a[176:179], a[140:143], 0\n" \
"ds_read_b64_tr_b16 a[228:229], v33 offset:40960\n" \
"v_mfma_f32_16x16x32_bf16 v[100:103], a[240:243], a[204:207], v[100:103]\n" \
"ds_read_b128 a[140:143], v28 offset:55296\n" \
"v_mfma_f32_16x16x32_bf16 v[116:119], a[176:179], a[144:147], 0\n" \
"ds_read_b128 a[204:207], v29 offset:55296\n" \
"v_mfma_f32_16x16x32_bf16 v[116:119], a[240:243], a[208:211], v[116:119]\n" \
"s_addk_i32 s84, 0x1c00\n" \
"v_mfma_f32_16x16x32_bf16 v[120:123], a[180:183], a[144:147], 0\n" \
"buffer_load_dwordx4 v23, s[64:67], s84 offen offset:1024 lds\n" \
"v_mfma_f32_16x16x32_bf16 v[120:123], a[244:247], a[208:211], v[120:123]\n" \
"s_addk_i32 s84, 0x1c00\n" \
"v_mfma_f32_16x16x32_bf16 v[124:127], a[184:187], a[144:147], 0\n" \
"buffer_load_dwordx4 v22, s[64:67], s84 offen offset:2048 lds\n" \
"v_mfma_f32_16x16x32_bf16 v[124:127], a[248:251], a[208:211], v[124:127]\n" \
"s_addk_i32 s84, 0x1c00\n" \
"s_nop 0\n" \
"v_mfma_f32_16x16x32_bf16 v[128:131], a[188:191], a[144:147], 0\n" \
".fill (L_pb + 3808 - .) / 4, 4, 0xBF800000\n" \
"L_pp1:\n" \
"s_cbranch_execz L_pret\n" \
"s_nop 0\n" \
"buffer_load_dwordx4 v23, s[64:67], s84 offen offset:3072 lds\n" \
"v_mfma_f32_16x16x32_bf16 v[128:131], a[252:255], a[208:211], v[128:131]\n" \
"ds_read_b128 a[144:147], v28 offset:32768\n" \
"v_mfma_f32_16x16x32_bf16 v[144:147], a[188:191], a[148:151], 0\n" \
"ds_read_b128 a[208:211], v29 offset:32768\n" \
"v_mfma_f32_16x16x32_bf16 v[144:147], a[252:255], a[212:215], v[144:147]\n" \
"s_add_u32 m0, s31, 0x4000\n" \
"s_mov_b32 s84, 0x100000\n" \
"v_mfma_f32_16x16x32_bf16 v[140:143], a[184:187], a[148:151], 0\n" \
"buffer_load_dwordx4 v26, s[72:75], s84 offen offset:0 lds\n" \
"v_mfma_f32_16x16x32_bf16 v[140:143], a[248:251], a[212:215], v[140:143]\n" \
"ds_read_b64_tr_b16 a[230:231], v33 offset:41984\n" \
"v_mfma_f32_16x16x32_bf16 v[136:139], a[180:183], a[148:151], 0\n" \
"s_addk_i32 s84, 0x7c00\n" \
"s_nop 0\n" \
"v_mfma_f32_16x16x32_bf16 v[136:139], a[244:247], a[212:215], v[136:139]\n" \
"buffer_load_dwordx4 v26, s[72:75], s84 offen offset:1024 lds\n" \
"v_mfma_f32_16x16x32_bf16 v[132:135], a[176:179], a[148:151], 0\n" \
"ds_read_b64_tr_b16 a[168:169], v34 offset:32768\n" \
"v_mfma_f32_16x16x32_bf16 v[132:135], a[240:243], a[212:215], v[132:135]\n" \
"ds_read_b128 a[148:151], v28 offset:34816\n" \
"v_mfma_f32_16x16x32_bf16 v[148:151], a[176:179], a[152:155], 0\n" \
"ds_read_b128 a[212:215], v29 offset:34816\n" \
"v_mfma_f32_16x16x32_bf16 v[148:151], a[240:243], a[216:219], v[148:151]\n" \
"ds_read_b64_tr_b16 a[170:171], v34 offset:33792\n" \
"v_mfma_f32_16x16x32_bf16 v[152:155], a[180:183], a[152:155], 0\n" \
"ds_read_b64_tr_b16 a[232:233], v34 offset:40960\n" \
"v_mfma_f32_16x16x32_bf16 v[152:155], a[244:247], a[216:219], v[152:155]\n" \
"ds_read_b64_tr_b16 a[234:235], v34 offset:41984\n" \
"v_mfma_f32_16x16x32_bf16 v[156:159], a[184:187], a[152:155], 0\n" \
"ds_read_b64_tr_b16 a[172:173], v35 offset:32768\n" \
"v_mfma_f32_16x16x32_bf16 v[156:159], a[248:251], a[216:219], v[156:159]\n" \
"ds_read_b64_tr_b16 a[174:175], v35 offset:33792\n" \
"v_mfma_f32_16x16x32_bf16 v[160:163], a[188:191], a[152:155], 0\n" \
"ds_read_b64_tr_b16 a[236:237], v35 offset:40960\n" \
"v_mfma_f32_16x16x32_bf16 v[160:163], a[252:255], a[216:219], v[160:163]\n" \
"ds_read_b128 a[152:155], v28 offset:36864\n" \
"v_mfma_f32_16x16x32_bf16 v[176:179], a[188:191], a[156:159], 0\n" \
"ds_read_b128 a[216:219], v29 offset:36864\n" \
"v_mfma_f32_16x16x32_bf16 v[176:179], a[252:255], a[220:223], v[176:179]\n" \
"ds_read_b64_tr_b16 a[238:239], v35 offset:41984\n" \
"v_mfma_f32_16x16x32_bf16 v[172:175], a[184:187], a[156:159], 0\n" \
"s_cmp_lg_u32 s3, 0\n" \
"s_cselect_b32 s36, s37, s5\n" \
"v_mfma_f32_16x16x32_bf16 v[172:175], a[248:251], a[220:223], v[172:175]\n" \
"s_lshl_b32 s5, s6, 2\n" \
"s_or_b32 s5, s38, s5\n" \
"v_mfma_f32_16x16x32_bf16 v[168:171], a[180:183], a[156:159], 0\n" \
"s_lshl_b32 s35, s3, 10\n" \
"v_mfma_f32_16x16x32_bf16 v[168:171], a[244:247], a[220:223], v[168:171]\n" \
"s_cmpk_lt_u32 s3, 3\n" \
"s_cselect_b32 s37, s37, s5\n" \
"v_mfma_f32_16x16x32_bf16 v[164:167], a[176:179], a[156:159], 0\n" \
"s_lshl_b32 s5, s4, 16\n" \
"s_lshl_b32 s4, s4, 11\n" \
"v_mfma_f32_16x16x32_bf16 v[164:167], a[240:243], a[220:223], v[164:167]\n" \
"ds_read_b128 a[156:159], v28 offset:38912\n" \
"ds_read_b128 a[220:223], v29 offset:38912\n" \
"s_waitcnt lgkmcnt(15)\n" \
"v_mfma_f32_16x16x32_bf16 a[0:3], a[160:163], a[128:131], a[0:3]\n" \
"s_addk_i32 s84, 0x7c00\n" \
"v_mfma_f32_16x16x32_bf16 a[0:3], a[224:227], a[192:195], a[0:3]\n" \
"buffer_load_dwordx4 v27, s[72:75], s84 offen offset:2048 lds\n" \
"v_mfma_f32_16x16x32_bf16 a[4:7], a[164:167], a[128:131], a[4:7]\n" \
"ds_read_b64_tr_b16 a[176:177], v36 offset:32768\n" \
"v_cndmask_b32_e32 v198, v213, v212, vcc\n" \
"s_waitcnt lgkmcnt(15)\n" \
"v_mfma_f32_16x16x32_bf16 a[4:7], a[228:231], a[192:195], a[4:7]\n" \
"s_addk_i32 s84, 0x7c00\n" \
"s_waitcnt lgkmcnt(11)\n" \
"v_mfma_f32_16x16x32_bf16 a[8:11], a[168:171], a[128:131], a[8:11]\n" \
"buffer_load_dwordx4 v27, s[72:75], s84 offen offset:3072 lds\n" \
"s_waitcnt lgkmcnt(9)\n" \
"v_mfma_f32_16x16x32_bf16 a[8:11], a[232:235], a[192:195], a[8:11]\n" \
"ds_read_b64_tr_b16 a[178:179], v36 offset:33792\n" \
"v_or_b32_e32 v212, s35, v20\n" \
"s_waitcnt lgkmcnt(8)\n" \
"v_mfma_f32_16x16x32_bf16 a[12:15], a[172:175], a[128:131], a[12:15]\n" \
"ds_read_b64_tr_b16 a[240:241], v36 offset:40960\n" \
"s_or_b32 s39, s40, s35\n" \
"s_waitcnt lgkmcnt(5)\n" \
"v_mfma_f32_16x16x32_bf16 a[12:15], a[236:239], a[192:195], a[12:15]\n" \
"s_or_b32 s55, s41, s4\n" \
"v_or_b32_e32 v199, 0x22000, v212\n" \
"v_mfma_f32_16x16x32_bf16 a[28:31], a[172:175], a[132:135], a[28:31]\n" \
"s_add_i32 s35, s39, s7\n" \
"s_add_i32 s54, s41, s5\n" \
"v_mfma_f32_16x16x32_bf16 a[28:31], a[236:239], a[196:199], a[28:31]\n" \
"ds_read_b64_tr_b16 a[242:243], v36 offset:41984\n" \
"s_add_i32 s55, s55, 0x80000\n" \
"v_mfma_f32_16x16x32_bf16 a[24:27], a[168:171], a[132:135], a[24:27]\n" \
"s_lshr_b32 s4, s2, 1\n" \
"s_and_b32 s4, s4, 0x7c\n" \
"v_mfma_f32_16x16x32_bf16 a[24:27], a[232:235], a[196:199], a[24:27]\n" \
"v_mov_b32_e32 v213, s13\n" \
"s_or_b32 s4, s4, s3\n" \
"v_mfma_f32_16x16x32_bf16 a[20:23], a[164:167], a[132:135], a[20:23]\n" \
"v_lshrrev_b32_e64 v212, 4, v20\n" \
"v_mad_u64_u32 v[214:215], s[4:5], s4, 36, v[212:213]\n" \
"v_mfma_f32_16x16x32_bf16 a[20:23], a[228:231], a[196:199], a[20:23]\n" \
"ds_read_b64_tr_b16 a[180:181], v37 offset:32768\n" \
"v_mov_b32_e64 v217, s9\n" \
"v_mfma_f32_16x16x32_bf16 a[16:19], a[160:163], a[132:135], a[16:19]\n" \
"s_movk_i32 s4, 0x240\n" \
"v_cmp_gt_u32_e32 vcc, s4, v20\n" \
"v_mfma_f32_16x16x32_bf16 a[16:19], a[224:227], a[196:199], a[16:19]\n" \
"s_lshl_b32 s4, s2, 3\n" \
"s_and_b32 s4, s4, 56\n" \
"v_mfma_f32_16x16x32_bf16 a[32:35], a[160:163], a[136:139], a[32:35]\n" \
"s_mov_b32 m0, s32\n" \
"ds_read_b64_tr_b16 a[182:183], v37 offset:33792\n" \
"v_mfma_f32_16x16x32_bf16 a[32:35], a[224:227], a[200:203], a[32:35]\n" \
"s_mov_b32 exec_lo, 0x0\n" \
"s_mov_b32 exec_hi, 0xff\n" \
"global_load_lds_dword v[184:185], off\n" \
"s_mov_b64 exec, -1\n" \
"v_mfma_f32_16x16x32_bf16 a[36:39], a[164:167], a[136:139], a[36:39]\n" \
"v_cndmask_b32_e32 v217, v217, v213, vcc\n" \
"v_lshlrev_b32_e64 v213, 3, v214\n" \
"v_mfma_f32_16x16x32_bf16 a[36:39], a[228:231], a[200:203], a[36:39]\n" \
"ds_read_b64_tr_b16 a[244:245], v37 offset:40960\n" \
"v_and_b32_e32 v213, 0x1ffc0, v213\n" \
"v_mfma_f32_16x16x32_bf16 a[40:43], a[168:171], a[136:139], a[40:43]\n" \
"v_and_b32_e64 v214, 7, v214\n" \
"v_or3_b32 v213, v214, s4, v213\n" \
"v_mfma_f32_16x16x32_bf16 a[40:43], a[232:235], a[200:203], a[40:43]\n" \
"s_mov_b32 m0, s32\n" \
"s_lshl_b32 s4, s2, 2\n" \
"v_mfma_f32_16x16x32_bf16 a[44:47], a[172:175], a[136:139], a[44:47]\n" \
"s_mov_b32 exec_lo, 0xffff0000\n" \
"s_mov_b32 exec_hi, 0x0\n" \
"global_load_lds_dword v[186:187], off\n" \
"s_mov_b64 exec, -1\n" \
"v_mfma_f32_16x16x32_bf16 a[44:47], a[236:239], a[200:203], a[44:47]\n" \
"ds_read_b64_tr_b16 a[246:247], v37 offset:41984\n" \
"s_or_b32 s4, s4, s3\n" \
"v_mfma_f32_16x16x32_bf16 a[60:63], a[172:175], a[140:143], a[60:63]\n" \
"s_mul_i32 s4, s4, 18\n" \
"v_min_u32_e64 v212, 53, v212\n" \
"v_mfma_f32_16x16x32_bf16 a[60:63], a[236:239], a[204:207], a[60:63]\n" \
"s_sub_i32 s4, s4, 36\n" \
"v_add_u32_e32 v212, s4, v212\n" \
"v_mfma_f32_16x16x32_bf16 a[56:59], a[168:171], a[140:143], a[56:59]\n" \
"ds_read_b64_tr_b16 a[184:185], v38 offset:32768\n" \
"v_mov_b32_e64 v215, s12\n" \
"v_mfma_f32_16x16x32_bf16 a[56:59], a[232:235], a[204:207], a[56:59]\n" \
"v_mov_b32_e32 v216, s8\n" \
"v_cndmask_b32_e32 v212, v212, v213, vcc\n" \
"v_mfma_f32_16x16x32_bf16 a[52:55], a[164:167], a[140:143], a[52:55]\n" \
"v_cndmask_b32_e32 v216, v216, v215, vcc\n" \
"v_lshlrev_b32_e32 v212, 7, v212\n" \
"v_mfma_f32_16x16x32_bf16 a[52:55], a[228:231], a[204:207], a[52:55]\n" \
"ds_read_b64_tr_b16 a[186:187], v38 offset:33792\n" \
"v_mov_b32_e64 v213, 0\n" \
"v_mfma_f32_16x16x32_bf16 a[48:51], a[160:163], a[140:143], a[48:51]\n" \
"v_lshl_add_u64 v[188:189], v[216:217], 0, v[212:213]\n" \
"v_mov_b32_e32 v215, 0xffff\n" \
"v_mfma_f32_16x16x32_bf16 a[48:51], a[224:227], a[204:207], a[48:51]\n" \
"s_and_b32 s4, s3, 1\n" \
"s_lshl_b32 s5, s4, 4\n" \
"v_mfma_f32_16x16x32_bf16 a[64:67], a[160:163], a[144:147], a[64:67]\n" \
"ds_read_b64_tr_b16 a[248:249], v38 offset:40960\n" \
"v_lshrrev_b32_e64 v212, 4, v20\n" \
"v_mfma_f32_16x16x32_bf16 a[64:67], a[224:227], a[208:211], a[64:67]\n" \
"v_lshrrev_b32_e32 v218, 6, v20\n" \
"v_and_b32_e32 v219, 12, v218\n" \
"v_mfma_f32_16x16x32_bf16 a[68:71], a[164:167], a[144:147], a[68:71]\n" \
"v_and_or_b32 v214, v212, 15, s5\n" \
"v_cmp_gt_u32_e64 vcc, v219, v214\n" \
"v_mfma_f32_16x16x32_bf16 a[68:71], a[228:231], a[208:211], a[68:71]\n" \
"ds_read_b64_tr_b16 a[250:251], v38 offset:41984\n" \
"v_mov_b32_e32 v220, 0xffff0000\n" \
"v_mfma_f32_16x16x32_bf16 a[72:75], a[168:171], a[144:147], a[72:75]\n" \
"s_lshl_b32 s5, s2, 2\n" \
"v_cndmask_b32_e64 v217, v215, 0, vcc\n" \
"v_mfma_f32_16x16x32_bf16 a[72:75], a[232:235], a[208:211], a[72:75]\n" \
"v_cmp_lt_u32_e32 vcc, v219, v214\n" \
"v_mov_b32_e64 v213, s23\n" \
"v_mfma_f32_16x16x32_bf16 a[76:79], a[172:175], a[144:147], a[76:79]\n" \
"s_or_b32 s5, s5, s3\n" \
"v_cndmask_b32_e32 v221, 0, v220, vcc\n" \
"v_mfma_f32_16x16x32_bf16 a[76:79], a[236:239], a[208:211], a[76:79]\n" \
"ds_read_b64_tr_b16 a[188:189], v39 offset:32768\n" \
"v_or_b32_e64 v12, v217, v221\n" \
"v_mfma_f32_16x16x32_bf16 a[92:95], a[172:175], a[148:151], a[92:95]\n" \
"v_or_b32_e32 v217, 2, v219\n" \
"v_cmp_gt_u32_e32 vcc, v217, v214\n" \
"v_mfma_f32_16x16x32_bf16 a[92:95], a[236:239], a[212:215], a[92:95]\n" \
"v_mov_b32_e32 v216, s25\n" \
"s_movk_i32 s40, 0xf400\n" \
"v_mfma_f32_16x16x32_bf16 a[88:91], a[168:171], a[148:151], a[88:91]\n" \
"ds_read_b64_tr_b16 a[190:191], v39 offset:33792\n" \
"v_cndmask_b32_e64 v221, v215, 0, vcc\n" \
"v_mfma_f32_16x16x32_bf16 a[88:91], a[232:235], a[212:215], a[88:91]\n" \
"v_cmp_lt_u32_e32 vcc, v217, v214\n" \
"s_lshl_b32 s4, s4, 6\n" \
"v_mfma_f32_16x16x32_bf16 a[84:87], a[164:167], a[148:151], a[84:87]\n" \
"v_cndmask_b32_e32 v217, 0, v220, vcc\n" \
"v_or_b32_e32 v13, v221, v217\n" \
"v_mfma_f32_16x16x32_bf16 a[84:87], a[228:231], a[212:215], a[84:87]\n" \
"ds_read_b64_tr_b16 a[252:253], v39 offset:40960\n" \
"v_or_b32_e64 v217, 16, v219\n" \
"v_mfma_f32_16x16x32_bf16 a[80:83], a[160:163], a[148:151], a[80:83]\n" \
"v_cmp_gt_u32_e32 vcc, v217, v214\n" \
"s_nop 1\n" \
"v_cndmask_b32_e64 v221, v215, 0, vcc\n" \
"v_mfma_f32_16x16x32_bf16 a[80:83], a[224:227], a[212:215], a[80:83]\n" \
"v_cmp_lt_u32_e32 vcc, v217, v214\n" \
"s_nop 1\n" \
"v_cndmask_b32_e64 v217, 0, v220, vcc\n" \
"v_mfma_f32_16x16x32_bf16 a[96:99], a[160:163], a[152:155], a[96:99]\n" \
"ds_read_b64_tr_b16 a[254:255], v39 offset:41984\n" \
"v_or_b32_e64 v14, v221, v217\n" \
"v_mfma_f32_16x16x32_bf16 a[96:99], a[224:227], a[216:219], a[96:99]\n" \
"v_or_b32_e32 v217, 18, v219\n" \
"v_cmp_gt_u32_e32 vcc, v217, v214\n" \
"v_mfma_f32_16x16x32_bf16 a[100:103], a[164:167], a[152:155], a[100:103]\n" \
"s_nop 0\n" \
"v_cndmask_b32_e64 v215, v215, 0, vcc\n" \
"v_cmp_lt_u32_e32 vcc, v217, v214\n" \
"v_mfma_f32_16x16x32_bf16 a[100:103], a[228:231], a[216:219], a[100:103]\n" \
"s_nop 0\n" \
"v_cndmask_b32_e32 v214, 0, v220, vcc\n" \
"v_or_b32_e64 v15, v215, v214\n" \
"v_mfma_f32_16x16x32_bf16 a[104:107], a[168:171], a[152:155], a[104:107]\n" \
"v_mad_u64_u32 v[214:215], s[6:7], s5, 42, v[212:213]\n" \
"s_movk_i32 s5, 0x1440\n" \
"v_mfma_f32_16x16x32_bf16 a[104:107], a[232:235], a[216:219], a[104:107]\n" \
"v_cmp_gt_u32_e32 vcc, s5, v214\n" \
"v_min_u32_e32 v217, 0xa43f, v214\n" \
"v_mfma_f32_16x16x32_bf16 a[108:111], a[172:175], a[152:155], a[108:111]\n" \
"v_mov_b32_e32 v214, s24\n" \
"v_cndmask_b32_e32 v215, v216, v213, vcc\n" \
"v_mfma_f32_16x16x32_bf16 a[108:111], a[236:239], a[216:219], a[108:111]\n" \
"v_mov_b32_e32 v213, s22\n" \
"s_movk_i32 s6, 0xffee\n" \
"s_waitcnt lgkmcnt(15)\n" \
"v_mfma_f32_16x16x32_bf16 a[124:127], a[172:175], a[156:159], a[124:127]\n" \
"v_cndmask_b32_e32 v214, v214, v213, vcc\n" \
"v_add_u32_e32 v213, 0xffffebc0, v217\n" \
"v_mfma_f32_16x16x32_bf16 a[124:127], a[236:239], a[220:223], a[124:127]\n" \
"s_lshl_b32 s5, s2, 5\n" \
"v_cndmask_b32_e32 v213, v213, v217, vcc\n" \
"v_mfma_f32_16x16x32_bf16 a[120:123], a[168:171], a[156:159], a[120:123]\n" \
"v_lshlrev_b32_e64 v216, 7, v213\n" \
"v_mul_u32_u24_e32 v213, 0xe39, v212\n" \
"v_mfma_f32_16x16x32_bf16 a[120:123], a[232:235], a[220:223], a[120:123]\n" \
"v_mov_b32_e32 v217, 0\n" \
"v_lshrrev_b32_e32 v213, 16, v213\n" \
"v_mfma_f32_16x16x32_bf16 a[116:119], a[164:167], a[156:159], a[116:119]\n" \
"v_lshl_add_u64 v[16:17], v[214:215], 0, v[216:217]\n" \
"v_mad_i32_i24 v214, v213, s6, v212\n" \
"v_mfma_f32_16x16x32_bf16 a[116:119], a[228:231], a[220:223], a[116:119]\n" \
"s_add_i32 s5, s5, 32\n" \
"v_lshlrev_b32_e32 v215, 4, v214\n" \
"v_mfma_f32_16x16x32_bf16 a[112:115], a[160:163], a[156:159], a[112:115]\n" \
"v_add_u32_e64 v216, s5, v215\n" \
"v_and_b32_e32 v216, 0xf0, v216\n" \
"v_mfma_f32_16x16x32_bf16 a[112:115], a[224:227], a[220:223], a[112:115]\n" \
"s_waitcnt vmcnt(20)\n" \
"v_readfirstlane_b32 s62, v180\n" \
"s_nop 3\n" \
"s_add_u32 s62, s62, 0x9E3779B1\n" \
"s_waitcnt vmcnt(2) lgkmcnt(0)\n" \
"s_barrier\n" \
"v_mfma_f32_16x16x32_bf16 v[52:55], a[176:179], a[128:131], v[52:55]\n" \
"s_add_u32 m0, s30, 0x8000\n" \
"s_movk_i32 s84, 0x180\n" \
"v_mfma_f32_16x16x32_bf16 v[52:55], a[240:243], a[192:195], v[52:55]\n" \
"buffer_load_dwordx4 v22, s[64:67], s84 offen offset:0 lds\n" \
"v_mfma_f32_16x16x32_bf16 v[56:59], a[180:183], a[128:131], v[56:59]\n" \
"ds_read_b64_tr_b16 a[160:161], v32 offset:0\n" \
"v_cmp_gt_u32_e64 vcc, 16, v214\n" \
"v_mfma_f32_16x16x32_bf16 v[56:59], a[244:247], a[192:195], v[56:59]\n" \
"s_movk_i32 s84, 0x1d80\n" \
"v_mfma_f32_16x16x32_bf16 v[60:63], a[184:187], a[128:131], v[60:63]\n" \
"buffer_load_dwordx4 v23, s[64:67], s84 offen offset:1024 lds\n" \
"v_mfma_f32_16x16x32_bf16 v[60:63], a[248:251], a[192:195], v[60:63]\n" \
"ds_read_b64_tr_b16 a[162:163], v32 offset:1024\n" \
"s_movk_i32 s7, 0xf800\n" \
"v_mfma_f32_16x16x32_bf16 v[64:67], a[188:191], a[128:131], v[64:67]\n" \
"ds_read_b64_tr_b16 a[224:225], v32 offset:8192\n" \
"v_cndmask_b32_e64 v214, v215, v216, vcc\n" \
"v_mfma_f32_16x16x32_bf16 v[64:67], a[252:255], a[192:195], v[64:67]\n" \
"ds_read_b128 a[128:131], v28 offset:16384\n" \
"v_or_b32_e64 v215, 64, v212\n" \
"v_mfma_f32_16x16x32_bf16 v[80:83], a[188:191], a[132:135], v[80:83]\n" \
"ds_read_b128 a[192:195], v29 offset:16384\n" \
"v_mul_u32_u24_e32 v216, 0xe39, v215\n" \
"v_mfma_f32_16x16x32_bf16 v[80:83], a[252:255], a[196:199], v[80:83]\n" \
"s_movk_i32 s84, 0x3980\n" \
"v_mfma_f32_16x16x32_bf16 v[76:79], a[184:187], a[132:135], v[76:79]\n" \
"buffer_load_dwordx4 v22, s[64:67], s84 offen offset:2048 lds\n" \
"v_mfma_f32_16x16x32_bf16 v[76:79], a[248:251], a[196:199], v[76:79]\n" \
"s_movk_i32 s84, 0x5580\n" \
"v_mfma_f32_16x16x32_bf16 v[72:75], a[180:183], a[132:135], v[72:75]\n" \
"buffer_load_dwordx4 v23, s[64:67], s84 offen offset:3072 lds\n" \
"v_mfma_f32_16x16x32_bf16 v[72:75], a[244:247], a[196:199], v[72:75]\n" \
"s_add_u32 m0, s31, 0x8000\n" \
"s_mov_b32 s84, 0x180000\n" \
"v_mfma_f32_16x16x32_bf16 v[68:71], a[176:179], a[132:135], v[68:71]\n" \
"buffer_load_dwordx4 v26, s[68:71], s84 offen offset:0 lds\n" \
"v_mfma_f32_16x16x32_bf16 v[68:71], a[240:243], a[196:199], v[68:71]\n" \
"ds_read_b128 a[132:135], v28 offset:18432\n" \
"v_lshrrev_b32_e64 v216, 16, v216\n" \
"v_mfma_f32_16x16x32_bf16 v[84:87], a[176:179], a[136:139], v[84:87]\n" \
"ds_read_b128 a[196:199], v29 offset:18432\n" \
"v_mad_i32_i24 v215, v216, s6, v215\n" \
"v_mfma_f32_16x16x32_bf16 v[84:87], a[240:243], a[200:203], v[84:87]\n" \
"s_addk_i32 s84, 0x7c00\n" \
"v_mfma_f32_16x16x32_bf16 v[88:91], a[180:183], a[136:139], v[88:91]\n" \
"buffer_load_dwordx4 v26, s[68:71], s84 offen offset:1024 lds\n" \
"v_mfma_f32_16x16x32_bf16 v[88:91], a[244:247], a[200:203], v[88:91]\n" \
"ds_read_b64_tr_b16 a[226:227], v32 offset:9216\n" \
"v_lshlrev_b32_e32 v217, 4, v215\n" \
"v_mfma_f32_16x16x32_bf16 v[92:95], a[184:187], a[136:139], v[92:95]\n" \
"s_addk_i32 s84, 0x7c00\n" \
"v_mfma_f32_16x16x32_bf16 v[92:95], a[248:251], a[200:203], v[92:95]\n" \
"buffer_load_dwordx4 v27, s[68:71], s84 offen offset:2048 lds\n" \
"v_mfma_f32_16x16x32_bf16 v[96:99], a[188:191], a[136:139], v[96:99]\n" \
"ds_read_b64_tr_b16 a[164:165], v33 offset:0\n" \
"v_add_u32_e32 v220, s5, v217\n" \
"v_mfma_f32_16x16x32_bf16 v[96:99], a[252:255], a[200:203], v[96:99]\n" \
"ds_read_b128 a[136:139], v28 offset:20480\n" \
"v_and_b32_e32 v220, 0xf0, v220\n" \
"v_mfma_f32_16x16x32_bf16 v[112:115], a[188:191], a[140:143], v[112:115]\n" \
"ds_read_b128 a[200:203], v29 offset:20480\n" \
"v_cmp_gt_u32_e64 vcc, 16, v215\n" \
"v_mfma_f32_16x16x32_bf16 v[112:115], a[252:255], a[204:207], v[112:115]\n" \
"s_addk_i32 s84, 0x7c00\n" \
"v_mfma_f32_16x16x32_bf16 v[108:111], a[184:187], a[140:143], v[108:111]\n" \
"buffer_load_dwordx4 v27, s[68:71], s84 offen offset:3072 lds\n" \
"v_mfma_f32_16x16x32_bf16 v[108:111], a[248:251], a[204:207], v[108:111]\n" \
"s_add_u32 m0, s30, 0x9000\n" \
"s_mov_b32 s84, 0x8180\n" \
"v_mfma_f32_16x16x32_bf16 v[104:107], a[180:183], a[140:143], v[104:107]\n" \
"buffer_load_dwordx4 v22, s[64:67], s84 offen offset:0 lds\n" \
"v_mfma_f32_16x16x32_bf16 v[104:107], a[244:247], a[204:207], v[104:107]\n" \
"ds_read_b64_tr_b16 a[166:167], v33 offset:1024\n" \
"v_cndmask_b32_e32 v215, v217, v220, vcc\n" \
"v_mfma_f32_16x16x32_bf16 v[100:103], a[176:179], a[140:143], v[100:103]\n" \
"ds_read_b64_tr_b16 a[228:229], v33 offset:8192\n" \
"v_or_b32_e32 v217, 0x80, v212\n" \
"v_mfma_f32_16x16x32_bf16 v[100:103], a[240:243], a[204:207], v[100:103]\n" \
"ds_read_b128 a[140:143], v28 offset:22528\n" \
"v_mul_u32_u24_e32 v220, 0xe39, v217\n" \
"v_mfma_f32_16x16x32_bf16 v[116:119], a[176:179], a[144:147], v[116:119]\n" \
"ds_read_b128 a[204:207], v29 offset:22528\n" \
"v_lshrrev_b32_e64 v220, 16, v220\n" \
"v_mfma_f32_16x16x32_bf16 v[116:119], a[240:243], a[208:211], v[116:119]\n" \
"s_addk_i32 s84, 0x1c00\n" \
"v_mfma_f32_16x16x32_bf16 v[120:123], a[180:183], a[144:147], v[120:123]\n" \
"buffer_load_dwordx4 v23, s[64:67], s84 offen offset:1024 lds\n" \
"v_mfma_f32_16x16x32_bf16 v[120:123], a[244:247], a[208:211], v[120:123]\n" \
"s_addk_i32 s84, 0x1c00\n" \
"v_mfma_f32_16x16x32_bf16 v[124:127], a[184:187], a[144:147], v[124:127]\n" \
"buffer_load_dwordx4 v22, s[64:67], s84 offen offset:2048 lds\n" \
"v_mfma_f32_16x16x32_bf16 v[124:127], a[248:251], a[208:211], v[124:127]\n" \
"s_addk_i32 s84, 0x1c00\n" \
"v_mfma_f32_16x16x32_bf16 v[128:131], a[188:191], a[144:147], v[128:131]\n" \
"buffer_load_dwordx4 v23, s[64:67], s84 offen offset:3072 lds\n" \
"v_mfma_f32_16x16x32_bf16 v[128:131], a[252:255], a[208:211], v[128:131]\n" \
"ds_read_b128 a[144:147], v28 offset:0\n" \
"v_mad_i32_i24 v217, v220, s6, v217\n" \
"v_mfma_f32_16x16x32_bf16 v[144:147], a[188:191], a[148:151], v[144:147]\n" \
"ds_read_b128 a[208:211], v29 offset:0\n" \
"v_lshlrev_b32_e32 v221, 4, v217\n" \
"v_mfma_f32_16x16x32_bf16 v[144:147], a[252:255], a[212:215], v[144:147]\n" \
"s_add_u32 m0, s31, 0xc000\n" \
"s_mov_b32 s84, 0x180000\n" \
"v_mfma_f32_16x16x32_bf16 v[140:143], a[184:187], a[148:151], v[140:143]\n" \
"buffer_load_dwordx4 v26, s[72:75], s84 offen offset:0 lds\n" \
"v_mfma_f32_16x16x32_bf16 v[140:143], a[248:251], a[212:215], v[140:143]\n" \
"ds_read_b64_tr_b16 a[230:231], v33 offset:9216\n" \
"v_add_u32_e64 v222, s5, v221\n" \
"v_mfma_f32_16x16x32_bf16 v[136:139], a[180:183], a[148:151], v[136:139]\n" \
"s_addk_i32 s84, 0x7c00\n" \
"v_mfma_f32_16x16x32_bf16 v[136:139], a[244:247], a[212:215], v[136:139]\n" \
"buffer_load_dwordx4 v26, s[72:75], s84 offen offset:1024 lds\n" \
"v_mfma_f32_16x16x32_bf16 v[132:135], a[176:179], a[148:151], v[132:135]\n" \
"ds_read_b64_tr_b16 a[168:169], v34 offset:0\n" \
"v_and_b32_e32 v222, 0xf0, v222\n" \
"v_mfma_f32_16x16x32_bf16 v[132:135], a[240:243], a[212:215], v[132:135]\n" \
"ds_read_b128 a[148:151], v28 offset:2048\n" \
"v_cmp_gt_u32_e32 vcc, 16, v217\n" \
"v_mfma_f32_16x16x32_bf16 v[148:151], a[176:179], a[152:155], v[148:151]\n" \
"ds_read_b128 a[212:215], v29 offset:2048\n" \
"v_cndmask_b32_e64 v217, v221, v222, vcc\n" \
"v_mfma_f32_16x16x32_bf16 v[148:151], a[240:243], a[216:219], v[148:151]\n" \
"ds_read_b64_tr_b16 a[170:171], v34 offset:1024\n" \
"v_or_b32_e32 v221, 0xc0, v212\n" \
"v_mfma_f32_16x16x32_bf16 v[152:155], a[180:183], a[152:155], v[152:155]\n" \
"ds_read_b64_tr_b16 a[232:233], v34 offset:8192\n" \
"v_mul_u32_u24_e32 v222, 0xe39, v221\n" \
"v_mfma_f32_16x16x32_bf16 v[152:155], a[244:247], a[216:219], v[152:155]\n" \
"ds_read_b64_tr_b16 a[234:235], v34 offset:9216\n" \
"v_lshrrev_b32_e64 v222, 16, v222\n" \
"v_mfma_f32_16x16x32_bf16 v[156:159], a[184:187], a[152:155], v[156:159]\n" \
"ds_read_b64_tr_b16 a[172:173], v35 offset:0\n" \
"v_mad_i32_i24 v221, v222, s6, v221\n" \
"v_mfma_f32_16x16x32_bf16 v[156:159], a[248:251], a[216:219], v[156:159]\n" \
"ds_read_b64_tr_b16 a[174:175], v35 offset:1024\n" \
"v_lshlrev_b32_e64 v223, 4, v221\n" \
"v_mfma_f32_16x16x32_bf16 v[160:163], a[188:191], a[152:155], v[160:163]\n" \
"ds_read_b64_tr_b16 a[236:237], v35 offset:8192\n" \
"v_add_u32_e64 v224, s5, v223\n" \
"v_mfma_f32_16x16x32_bf16 v[160:163], a[252:255], a[216:219], v[160:163]\n" \
"ds_read_b128 a[152:155], v28 offset:4096\n" \
"v_and_b32_e32 v224, 0xf0, v224\n" \
"v_mfma_f32_16x16x32_bf16 v[176:179], a[188:191], a[156:159], v[176:179]\n" \
"ds_read_b128 a[216:219], v29 offset:4096\n" \
"v_cmp_gt_u32_e64 vcc, 16, v221\n" \
"v_mfma_f32_16x16x32_bf16 v[176:179], a[252:255], a[220:223], v[176:179]\n" \
"ds_read_b64_tr_b16 a[238:239], v35 offset:9216\n" \
"v_cndmask_b32_e64 v221, v223, v224, vcc\n" \
"v_mfma_f32_16x16x32_bf16 v[172:175], a[184:187], a[156:159], v[172:175]\n" \
"v_or_b32_e32 v223, 0x100, v212\n" \
"v_mul_u32_u24_e32 v224, 0xe39, v223\n" \
"v_mfma_f32_16x16x32_bf16 v[172:175], a[248:251], a[220:223], v[172:175]\n" \
"v_lshrrev_b32_e64 v224, 16, v224\n" \
"v_mad_i32_i24 v223, v224, s6, v223\n" \
"v_mfma_f32_16x16x32_bf16 v[168:171], a[180:183], a[156:159], v[168:171]\n" \
"v_lshlrev_b32_e32 v225, 4, v223\n" \
"v_add_u32_e32 v226, s5, v225\n" \
"v_mfma_f32_16x16x32_bf16 v[168:171], a[244:247], a[220:223], v[168:171]\n" \
"v_and_b32_e32 v226, 0xf0, v226\n" \
"v_cmp_gt_u32_e64 vcc, 16, v223\n" \
"v_mfma_f32_16x16x32_bf16 v[164:167], a[176:179], a[156:159], v[164:167]\n" \
"s_nop 0\n" \
"v_cndmask_b32_e32 v223, v225, v226, vcc\n" \
"v_or_b32_e32 v225, 0x140, v212\n" \
"v_mfma_f32_16x16x32_bf16 v[164:167], a[240:243], a[220:223], v[164:167]\n" \
"ds_read_b128 a[156:159], v28 offset:6144\n" \
"ds_read_b128 a[220:223], v29 offset:6144\n" \
"s_waitcnt lgkmcnt(15)\n" \
"v_mfma_f32_16x16x32_bf16 a[0:3], a[160:163], a[128:131], a[0:3]\n" \
"s_addk_i32 s84, 0x7c00\n" \
"v_mfma_f32_16x16x32_bf16 a[0:3], a[224:227], a[192:195], a[0:3]\n" \
"buffer_load_dwordx4 v27, s[72:75], s84 offen offset:2048 lds\n" \
"v_mfma_f32_16x16x32_bf16 a[4:7], a[164:167], a[128:131], a[4:7]\n" \
"ds_read_b64_tr_b16 a[176:177], v36 offset:0\n" \
"v_mul_u32_u24_e32 v226, 0xe39, v225\n" \
"s_waitcnt lgkmcnt(15)\n" \
"v_mfma_f32_16x16x32_bf16 a[4:7], a[228:231], a[192:195], a[4:7]\n" \
"s_addk_i32 s84, 0x7c00\n" \
"s_waitcnt lgkmcnt(11)\n" \
"v_mfma_f32_16x16x32_bf16 a[8:11], a[168:171], a[128:131], a[8:11]\n" \
"buffer_load_dwordx4 v27, s[72:75], s84 offen offset:3072 lds\n" \
"s_waitcnt lgkmcnt(9)\n" \
"v_mfma_f32_16x16x32_bf16 a[8:11], a[232:235], a[192:195], a[8:11]\n" \
"ds_read_b64_tr_b16 a[178:179], v36 offset:1024\n" \
"v_lshrrev_b32_e32 v226, 16, v226\n" \
"s_waitcnt lgkmcnt(8)\n" \
"v_mfma_f32_16x16x32_bf16 a[12:15], a[172:175], a[128:131], a[12:15]\n" \
"ds_read_b64_tr_b16 a[240:241], v36 offset:8192\n" \
"v_mad_i32_i24 v225, v226, s6, v225\n" \
"s_waitcnt lgkmcnt(5)\n" \
"v_mfma_f32_16x16x32_bf16 a[12:15], a[236:239], a[192:195], a[12:15]\n" \
"v_lshlrev_b32_e32 v227, 4, v225\n" \
"v_add_u32_e64 v228, s5, v227\n" \
"v_mfma_f32_16x16x32_bf16 a[28:31], a[172:175], a[132:135], a[28:31]\n" \
"v_and_b32_e32 v228, 0xf0, v228\n" \
"v_cmp_gt_u32_e64 vcc, 16, v225\n" \
"v_mfma_f32_16x16x32_bf16 a[28:31], a[236:239], a[196:199], a[28:31]\n" \
"ds_read_b64_tr_b16 a[242:243], v36 offset:9216\n" \
"v_cndmask_b32_e64 v225, v227, v228, vcc\n" \
"v_mfma_f32_16x16x32_bf16 a[24:27], a[168:171], a[132:135], a[24:27]\n" \
"buffer_load_dwordx4 v[200:203], v192, s[76:79], s34 offen offset:0\n" \
"v_mfma_f32_16x16x32_bf16 a[24:27], a[232:235], a[196:199], a[24:27]\n" \
"v_or_b32_e32 v227, 0x180, v212\n" \
"v_mul_u32_u24_e32 v228, 0xe39, v227\n" \
"v_mfma_f32_16x16x32_bf16 a[20:23], a[164:167], a[132:135], a[20:23]\n" \
"v_lshrrev_b32_e64 v228, 16, v228\n" \
"v_mad_i32_i24 v227, v228, s6, v227\n" \
"v_mfma_f32_16x16x32_bf16 a[20:23], a[228:231], a[196:199], a[20:23]\n" \
"ds_read_b64_tr_b16 a[180:181], v37 offset:0\n" \
"v_lshlrev_b32_e64 v229, 4, v227\n" \
"v_mfma_f32_16x16x32_bf16 a[16:19], a[160:163], a[132:135], a[16:19]\n" \
"v_add_u32_e64 v230, s5, v229\n" \
"v_and_b32_e32 v230, 0xf0, v230\n" \
"v_mfma_f32_16x16x32_bf16 a[16:19], a[224:227], a[196:199], a[16:19]\n" \
"v_cmp_gt_u32_e32 vcc, 16, v227\n" \
"s_nop 1\n" \
"v_cndmask_b32_e64 v227, v229, v230, vcc\n" \
"v_mfma_f32_16x16x32_bf16 a[32:35], a[160:163], a[136:139], a[32:35]\n" \
"buffer_load_dwordx4 v[204:207], v193, s[76:79], s34 offen offset:0\n" \
"v_mfma_f32_16x16x32_bf16 a[32:35], a[224:227], a[200:203], a[32:35]\n" \
"s_mov_b32 m0, s32\n" \
"ds_read_b64_tr_b16 a[182:183], v37 offset:1024\n" \
"v_mfma_f32_16x16x32_bf16 a[36:39], a[164:167], a[136:139], a[36:39]\n" \
"s_mov_b32 exec_lo, 0x0\n" \
"s_mov_b32 exec_hi, 0xff00\n" \
"global_load_lds_dword v[184:185], off\n" \
"s_mov_b64 exec, -1\n" \
"v_mfma_f32_16x16x32_bf16 a[36:39], a[228:231], a[200:203], a[36:39]\n" \
"ds_read_b64_tr_b16 a[244:245], v37 offset:8192\n" \
"v_or_b32_e32 v229, 0x1c0, v212\n" \
"v_mfma_f32_16x16x32_bf16 a[40:43], a[168:171], a[136:139], a[40:43]\n" \
"v_mul_u32_u24_e32 v230, 0xe39, v229\n" \
"v_lshrrev_b32_e32 v230, 16, v230\n" \
"v_mfma_f32_16x16x32_bf16 a[40:43], a[232:235], a[200:203], a[40:43]\n" \
"buffer_load_dword v208, v194, s[76:79], s34 offen offset:32\n" \
"v_mfma_f32_16x16x32_bf16 a[44:47], a[172:175], a[136:139], a[44:47]\n" \
"ds_read_b64_tr_b16 a[246:247], v37 offset:9216\n" \
"v_mad_i32_i24 v229, v230, s6, v229\n" \
"v_mfma_f32_16x16x32_bf16 a[44:47], a[236:239], a[200:203], a[44:47]\n" \
"v_lshlrev_b32_e32 v231, 4, v229\n" \
"v_add_u32_e32 v232, s5, v231\n" \
"v_mfma_f32_16x16x32_bf16 a[60:63], a[172:175], a[140:143], a[60:63]\n" \
"v_and_b32_e32 v232, 0xf0, v232\n" \
"v_cmp_gt_u32_e64 vcc, 16, v229\n" \
"v_mfma_f32_16x16x32_bf16 a[60:63], a[236:239], a[204:207], a[60:63]\n" \
"s_nop 0\n" \
"v_cndmask_b32_e32 v229, v231, v232, vcc\n" \
"v_or_b32_e32 v231, 0x200, v212\n" \
"v_mfma_f32_16x16x32_bf16 a[56:59], a[168:171], a[140:143], a[56:59]\n" \
"buffer_load_dword v209, v194, s[76:79], s34 offen offset:1184\n" \
"v_mfma_f32_16x16x32_bf16 a[56:59], a[232:235], a[204:207], a[56:59]\n" \
"ds_read_b64_tr_b16 a[184:185], v38 offset:0\n" \
"v_mul_u32_u24_e32 v232, 0xe39, v231\n" \
"v_mfma_f32_16x16x32_bf16 a[52:55], a[164:167], a[140:143], a[52:55]\n" \
"v_lshrrev_b32_e64 v232, 16, v232\n" \
"v_mad_i32_i24 v231, v232, s6, v231\n" \
"v_mfma_f32_16x16x32_bf16 a[52:55], a[228:231], a[204:207], a[52:55]\n" \
"ds_read_b64_tr_b16 a[186:187], v38 offset:1024\n" \
"v_lshlrev_b32_e64 v233, 4, v231\n" \
"v_mfma_f32_16x16x32_bf16 a[48:51], a[160:163], a[140:143], a[48:51]\n" \
"v_add_u32_e32 v234, s5, v233\n" \
"s_movk_i32 s5, 0x480\n" \
"v_mfma_f32_16x16x32_bf16 a[48:51], a[224:227], a[204:207], a[48:51]\n" \
"v_mad_u32_u24 v0, v213, s5, v214\n" \
"v_mul_u32_u24_e32 v213, 0x480, v216\n" \
"v_mfma_f32_16x16x32_bf16 a[64:67], a[160:163], a[144:147], a[64:67]\n" \
"buffer_load_dword v211, v194, s[76:79], s34 offen offset:2336\n" \
"v_mfma_f32_16x16x32_bf16 a[64:67], a[224:227], a[208:211], a[64:67]\n" \
"ds_read_b64_tr_b16 a[248:249], v38 offset:8192\n" \
"s_movk_i32 s6, 0xfc00\n" \
"v_mfma_f32_16x16x32_bf16 a[68:71], a[164:167], a[144:147], a[68:71]\n" \
"v_add3_u32 v1, v213, v215, s6\n" \
"v_mul_u32_u24_e32 v213, 0x480, v220\n" \
"v_mfma_f32_16x16x32_bf16 a[68:71], a[228:231], a[208:211], a[68:71]\n" \
"ds_read_b64_tr_b16 a[250:251], v38 offset:9216\n" \
"v_add3_u32 v2, v213, v217, s7\n" \
"v_mfma_f32_16x16x32_bf16 a[72:75], a[168:171], a[144:147], a[72:75]\n" \
"v_mul_u32_u24_e32 v213, 0x480, v222\n" \
"v_add3_u32 v3, v213, v221, s40\n" \
"v_mfma_f32_16x16x32_bf16 a[72:75], a[232:235], a[208:211], a[72:75]\n" \
"s_mov_b32 s76, s20\n" \
"s_and_b32 s77, s21, 0xffff\n" \
"v_mfma_f32_16x16x32_bf16 a[76:79], a[172:175], a[144:147], a[76:79]\n" \
"s_mov_b32 s78, 0x480\n" \
"v_mul_u32_u24_e32 v213, 0x480, v226\n" \
"v_mfma_f32_16x16x32_bf16 a[76:79], a[236:239], a[208:211], a[76:79]\n" \
"buffer_load_ushort v210, v195, s[76:79], 0 offen\n" \
"v_mfma_f32_16x16x32_bf16 a[92:95], a[172:175], a[148:151], a[92:95]\n" \
"ds_read_b64_tr_b16 a[188:189], v39 offset:0\n" \
"v_add3_u32 v5, v213, v225, s6\n" \
"v_mfma_f32_16x16x32_bf16 a[92:95], a[236:239], a[212:215], a[92:95]\n" \
"v_mul_u32_u24_e32 v213, 0x480, v228\n" \
"v_add3_u32 v6, v213, v227, s7\n" \
"v_mfma_f32_16x16x32_bf16 a[88:91], a[168:171], a[148:151], a[88:91]\n" \
"s_mov_b32 s76, s8\n" \
"s_and_b32 s77, s9, 0xffff\n" \
"v_mfma_f32_16x16x32_bf16 a[88:91], a[232:235], a[212:215], a[88:91]\n" \
"s_mov_b32 s78, 0x240000\n" \
"ds_read_b64_tr_b16 a[190:191], v39 offset:1024\n" \
"v_mfma_f32_16x16x32_bf16 a[84:87], a[164:167], a[148:151], a[84:87]\n" \
"v_mul_u32_u24_e32 v213, 0x480, v230\n" \
"v_add3_u32 v7, v213, v229, s40\n" \
"v_mfma_f32_16x16x32_bf16 a[84:87], a[228:231], a[212:215], a[84:87]\n" \
"ds_read_b64_tr_b16 a[252:253], v39 offset:8192\n" \
"s_lshl_b32 s7, s3, 3\n" \
"v_mfma_f32_16x16x32_bf16 a[80:83], a[160:163], a[148:151], a[80:83]\n" \
"s_lshl_b32 s40, s3, 1\n" \
"s_and_b32 s7, s7, 16\n" \
"v_mfma_f32_16x16x32_bf16 a[80:83], a[224:227], a[212:215], a[80:83]\n" \
"s_and_b32 s40, s40, 2\n" \
"s_and_b32 s6, s2, 7\n" \
"v_mfma_f32_16x16x32_bf16 a[96:99], a[160:163], a[152:155], a[96:99]\n" \
"ds_read_b64_tr_b16 a[254:255], v39 offset:9216\n" \
"v_bfe_u32 v213, v20, 7, 1\n" \
"v_mfma_f32_16x16x32_bf16 a[96:99], a[224:227], a[216:219], a[96:99]\n" \
"s_or_b32 s7, s40, s7\n" \
".fill (L_pb + 7904 - .) / 4, 4, 0xBF800000\n" \
"L_pp2:\n" \
"s_cbranch_execz L_pret\n" \
"v_and_b32_e32 v234, 0xf0, v234\n" \
"v_mfma_f32_16x16x32_bf16 a[100:103], a[164:167], a[152:155], a[100:103]\n" \
"v_cmp_gt_u32_e32 vcc, 16, v231\n" \
"v_or3_b32 v213, s7, v213, v219\n" \
"v_mfma_f32_16x16x32_bf16 a[100:103], a[228:231], a[216:219], a[100:103]\n" \
"v_and_b32_e32 v214, 32, v20\n" \
"s_lshl_b32 s6, s6, 5\n" \
"v_mfma_f32_16x16x32_bf16 a[104:107], a[168:171], a[152:155], a[104:107]\n" \
"v_cndmask_b32_e32 v231, v233, v234, vcc\n" \
"v_mul_u32_u24_e32 v213, 18, v213\n" \
"v_mfma_f32_16x16x32_bf16 a[104:107], a[232:235], a[216:219], a[104:107]\n" \
"v_mov_b32_e32 v215, 0x100\n" \
"v_mov_b32_e64 v216, s6\n" \
"v_mfma_f32_16x16x32_bf16 a[108:111], a[172:175], a[152:155], a[108:111]\n" \
"v_cmp_eq_u32_e64 vcc, 0, v214\n" \
"v_and_or_b32 v213, v218, 1, v213\n" \
"v_mfma_f32_16x16x32_bf16 a[108:111], a[236:239], a[216:219], a[108:111]\n" \
"v_lshlrev_b32_e32 v212, 3, v212\n" \
"v_cndmask_b32_e32 v214, v215, v216, vcc\n" \
"s_waitcnt lgkmcnt(15)\n" \
"v_mfma_f32_16x16x32_bf16 a[124:127], a[172:175], a[156:159], a[124:127]\n" \
"v_mad_u32_u24 v213, v213, s5, v214\n" \
"v_and_or_b32 v213, v20, 16, v213\n" \
"v_mfma_f32_16x16x32_bf16 a[124:127], a[236:239], a[220:223], a[124:127]\n" \
"v_add_u32_e32 v9, 0x4800, v213\n" \
"v_mul_u32_u24_e32 v213, 0x120, v218\n" \
"v_mfma_f32_16x16x32_bf16 a[120:123], a[168:171], a[156:159], a[120:123]\n" \
"v_and_or_b32 v10, v212, 24, v213\n" \
"v_lshrrev_b32_e32 v213, 2, v20\n" \
"v_mfma_f32_16x16x32_bf16 a[120:123], a[232:235], a[220:223], a[120:123]\n" \
"v_and_or_b32 v213, v213, 60, s4\n" \
"v_lshl_or_b32 v212, s3, 9, v212\n" \
"v_mfma_f32_16x16x32_bf16 a[116:119], a[164:167], a[156:159], a[116:119]\n" \
"v_mad_u32_u24 v4, v224, s5, v223\n" \
"v_mad_u32_u24 v8, v232, s5, v231\n" \
"v_mfma_f32_16x16x32_bf16 a[116:119], a[228:231], a[220:223], a[116:119]\n" \
"v_or_b32_e32 v18, 0x27000, v213\n" \
"v_or_b32_e32 v19, 0x27800, v212\n" \
"v_mfma_f32_16x16x32_bf16 a[112:115], a[160:163], a[156:159], a[112:115]\n" \
"v_mov_b32_e32 v215, 0x1000c0c\n" \
"v_mov_b32_e32 v216, 0xc0c0100\n" \
"v_mfma_f32_16x16x32_bf16 a[112:115], a[224:227], a[220:223], a[112:115]\n" \
"s_waitcnt vmcnt(7) lgkmcnt(0)\n" \
"s_barrier\n" \
"v_mfma_f32_16x16x32_bf16 v[52:55], a[176:179], a[128:131], v[52:55]\n" \
"s_add_u32 m0, s30, 0x0\n" \
"s_movk_i32 s84, 0x200\n" \
"v_mfma_f32_16x16x32_bf16 v[52:55], a[240:243], a[192:195], v[52:55]\n" \
"buffer_load_dwordx4 v22, s[64:67], s84 offen offset:0 lds\n" \
"v_mfma_f32_16x16x32_bf16 v[56:59], a[180:183], a[128:131], v[56:59]\n" \
"ds_read_b64_tr_b16 a[160:161], v32 offset:32768\n" \
"v_bfe_u32 v214, v20, 4, 1\n" \
"v_mfma_f32_16x16x32_bf16 v[56:59], a[244:247], a[192:195], v[56:59]\n" \
"s_movk_i32 s84, 0x1e00\n" \
"v_mfma_f32_16x16x32_bf16 v[60:63], a[184:187], a[128:131], v[60:63]\n" \
"buffer_load_dwordx4 v23, s[64:67], s84 offen offset:1024 lds\n" \
"v_mfma_f32_16x16x32_bf16 v[60:63], a[248:251], a[192:195], v[60:63]\n" \
"ds_read_b64_tr_b16 a[162:163], v32 offset:33792\n" \
"v_lshrrev_b32_e32 v212, 8, v20\n" \
"v_mfma_f32_16x16x32_bf16 v[64:67], a[188:191], a[128:131], v[64:67]\n" \
"ds_read_b64_tr_b16 a[224:225], v32 offset:40960\n" \
"v_cmp_eq_u32_e64 vcc, 0, v214\n" \
"v_mfma_f32_16x16x32_bf16 v[64:67], a[252:255], a[192:195], v[64:67]\n" \
"ds_read_b128 a[128:131], v28 offset:49152\n" \
"v_bfe_u32 v213, v20, 4, 4\n" \
"v_mfma_f32_16x16x32_bf16 v[80:83], a[188:191], a[132:135], v[80:83]\n" \
"ds_read_b128 a[192:195], v29 offset:49152\n" \
"v_mov_b32_e32 v217, 0xc0c0302\n" \
"v_mfma_f32_16x16x32_bf16 v[80:83], a[252:255], a[196:199], v[80:83]\n" \
"s_movk_i32 s84, 0x3a00\n" \
"v_mfma_f32_16x16x32_bf16 v[76:79], a[184:187], a[132:135], v[76:79]\n" \
"buffer_load_dwordx4 v22, s[64:67], s84 offen offset:2048 lds\n" \
"v_mfma_f32_16x16x32_bf16 v[76:79], a[248:251], a[196:199], v[76:79]\n" \
"s_movk_i32 s84, 0x5600\n" \
"v_mfma_f32_16x16x32_bf16 v[72:75], a[180:183], a[132:135], v[72:75]\n" \
"buffer_load_dwordx4 v23, s[64:67], s84 offen offset:3072 lds\n" \
"v_mfma_f32_16x16x32_bf16 v[72:75], a[244:247], a[196:199], v[72:75]\n" \
"s_add_u32 m0, s31, 0x0\n" \
"s_mov_b32 s84, 0x200000\n" \
"v_mfma_f32_16x16x32_bf16 v[68:71], a[176:179], a[132:135], v[68:71]\n" \
"buffer_load_dwordx4 v26, s[68:71], s84 offen offset:0 lds\n" \
"v_mfma_f32_16x16x32_bf16 v[68:71], a[240:243], a[196:199], v[68:71]\n" \
"ds_read_b128 a[132:135], v28 offset:51200\n" \
"v_cndmask_b32_e32 v215, v215, v216, vcc\n" \
"v_mfma_f32_16x16x32_bf16 v[84:87], a[176:179], a[136:139], v[84:87]\n" \
"ds_read_b128 a[196:199], v29 offset:51200\n" \
"v_mov_b32_e32 v216, 0x3020c0c\n" \
"v_mfma_f32_16x16x32_bf16 v[84:87], a[240:243], a[200:203], v[84:87]\n" \
"s_addk_i32 s84, 0x7c00\n" \
"v_mfma_f32_16x16x32_bf16 v[88:91], a[180:183], a[136:139], v[88:91]\n" \
"buffer_load_dwordx4 v26, s[68:71], s84 offen offset:1024 lds\n" \
"v_mfma_f32_16x16x32_bf16 v[88:91], a[244:247], a[200:203], v[88:91]\n" \
"ds_read_b64_tr_b16 a[226:227], v32 offset:41984\n" \
"v_lshl_or_b32 v214, v212, 2, v214\n" \
"v_mfma_f32_16x16x32_bf16 v[92:95], a[184:187], a[136:139], v[92:95]\n" \
"s_addk_i32 s84, 0x7c00\n" \
"v_mfma_f32_16x16x32_bf16 v[92:95], a[248:251], a[200:203], v[92:95]\n" \
"buffer_load_dwordx4 v27, s[68:71], s84 offen offset:2048 lds\n" \
"v_mfma_f32_16x16x32_bf16 v[96:99], a[188:191], a[136:139], v[96:99]\n" \
"ds_read_b64_tr_b16 a[164:165], v33 offset:32768\n" \
"v_cndmask_b32_e64 v216, v216, v217, vcc\n" \
"v_mfma_f32_16x16x32_bf16 v[96:99], a[252:255], a[200:203], v[96:99]\n" \
"ds_read_b128 a[136:139], v28 offset:53248\n" \
"v_mov_b32_e32 v217, 0xc0c0c0c\n" \
"v_mfma_f32_16x16x32_bf16 v[112:115], a[188:191], a[140:143], v[112:115]\n" \
"ds_read_b128 a[200:203], v29 offset:53248\n" \
"v_cmp_gt_u32_e64 vcc, v214, v213\n" \
"v_mfma_f32_16x16x32_bf16 v[112:115], a[252:255], a[204:207], v[112:115]\n" \
"s_addk_i32 s84, 0x7c00\n" \
"v_mfma_f32_16x16x32_bf16 v[108:111], a[184:187], a[140:143], v[108:111]\n" \
"buffer_load_dwordx4 v27, s[68:71], s84 offen offset:3072 lds\n" \
"v_mfma_f32_16x16x32_bf16 v[108:111], a[248:251], a[204:207], v[108:111]\n" \
"s_add_u32 m0, s30, 0x1000\n" \
"s_mov_b32 s84, 0x8200\n" \
"v_mfma_f32_16x16x32_bf16 v[104:107], a[180:183], a[140:143], v[104:107]\n" \
"buffer_load_dwordx4 v22, s[64:67], s84 offen offset:0 lds\n" \
"v_mfma_f32_16x16x32_bf16 v[104:107], a[244:247], a[204:207], v[104:107]\n" \
"ds_read_b64_tr_b16 a[166:167], v33 offset:33792\n" \
"v_or_b32_e32 v214, 2, v214\n" \
"v_mfma_f32_16x16x32_bf16 v[100:103], a[176:179], a[140:143], v[100:103]\n" \
"ds_read_b64_tr_b16 a[228:229], v33 offset:40960\n" \
"s_lshr_b32 s4, s2, 5\n" \
"v_mfma_f32_16x16x32_bf16 v[100:103], a[240:243], a[204:207], v[100:103]\n" \
"ds_read_b128 a[140:143], v28 offset:55296\n" \
"v_cndmask_b32_e32 v40, v217, v215, vcc\n" \
"v_mfma_f32_16x16x32_bf16 v[116:119], a[176:179], a[144:147], v[116:119]\n" \
"ds_read_b128 a[204:207], v29 offset:55296\n" \
"v_cndmask_b32_e64 v42, v215, v217, vcc\n" \
"v_mfma_f32_16x16x32_bf16 v[116:119], a[240:243], a[208:211], v[116:119]\n" \
"s_addk_i32 s84, 0x1c00\n" \
"v_mfma_f32_16x16x32_bf16 v[120:123], a[180:183], a[144:147], v[120:123]\n" \
"buffer_load_dwordx4 v23, s[64:67], s84 offen offset:1024 lds\n" \
"v_mfma_f32_16x16x32_bf16 v[120:123], a[244:247], a[208:211], v[120:123]\n" \
"s_addk_i32 s84, 0x1c00\n" \
"v_mfma_f32_16x16x32_bf16 v[124:127], a[184:187], a[144:147], v[124:127]\n" \
"buffer_load_dwordx4 v22, s[64:67], s84 offen offset:2048 lds\n" \
"v_mfma_f32_16x16x32_bf16 v[124:127], a[248:251], a[208:211], v[124:127]\n" \
"s_addk_i32 s84, 0x1c00\n" \
"v_mfma_f32_16x16x32_bf16 v[128:131], a[188:191], a[144:147], v[128:131]\n" \
"buffer_load_dwordx4 v23, s[64:67], s84 offen offset:3072 lds\n" \
"v_mfma_f32_16x16x32_bf16 v[128:131], a[252:255], a[208:211], v[128:131]\n" \
"ds_read_b128 a[144:147], v28 offset:32768\n" \
"v_cmp_gt_u32_e32 vcc, v214, v213\n" \
"v_mfma_f32_16x16x32_bf16 v[144:147], a[188:191], a[148:151], v[144:147]\n" \
"ds_read_b128 a[208:211], v29 offset:32768\n" \
"v_and_b32_e64 v214, 16, v20\n" \
"v_mfma_f32_16x16x32_bf16 v[144:147], a[252:255], a[212:215], v[144:147]\n" \
"s_add_u32 m0, s31, 0x4000\n" \
"s_mov_b32 s84, 0x200000\n" \
"v_mfma_f32_16x16x32_bf16 v[140:143], a[184:187], a[148:151], v[140:143]\n" \
"buffer_load_dwordx4 v26, s[72:75], s84 offen offset:0 lds\n" \
"v_mfma_f32_16x16x32_bf16 v[140:143], a[248:251], a[212:215], v[140:143]\n" \
"ds_read_b64_tr_b16 a[230:231], v33 offset:41984\n" \
"s_movk_i32 s42, 0x3f80\n" \
"v_mfma_f32_16x16x32_bf16 v[136:139], a[180:183], a[148:151], v[136:139]\n" \
"s_addk_i32 s84, 0x7c00\n" \
"v_mfma_f32_16x16x32_bf16 v[136:139], a[244:247], a[212:215], v[136:139]\n" \
"buffer_load_dwordx4 v26, s[72:75], s84 offen offset:1024 lds\n" \
"v_mfma_f32_16x16x32_bf16 v[132:135], a[176:179], a[148:151], v[132:135]\n" \
"ds_read_b64_tr_b16 a[168:169], v34 offset:32768\n" \
"s_xor_b32 s5, s4, 15\n" \
"s_nop 0\n" \
"v_mfma_f32_16x16x32_bf16 v[132:135], a[240:243], a[212:215], v[132:135]\n" \
"ds_read_b128 a[148:151], v28 offset:34816\n" \
"v_mfma_f32_16x16x32_bf16 v[148:151], a[176:179], a[152:155], v[148:151]\n" \
"ds_read_b128 a[212:215], v29 offset:34816\n" \
"v_mfma_f32_16x16x32_bf16 v[148:151], a[240:243], a[216:219], v[148:151]\n" \
"ds_read_b64_tr_b16 a[170:171], v34 offset:33792\n" \
"v_mfma_f32_16x16x32_bf16 v[152:155], a[180:183], a[152:155], v[152:155]\n" \
"ds_read_b64_tr_b16 a[232:233], v34 offset:40960\n" \
"v_mfma_f32_16x16x32_bf16 v[152:155], a[244:247], a[216:219], v[152:155]\n" \
"ds_read_b64_tr_b16 a[234:235], v34 offset:41984\n" \
"v_mfma_f32_16x16x32_bf16 v[156:159], a[184:187], a[152:155], v[156:159]\n" \
"ds_read_b64_tr_b16 a[172:173], v35 offset:32768\n" \
"v_mfma_f32_16x16x32_bf16 v[156:159], a[248:251], a[216:219], v[156:159]\n" \
"ds_read_b64_tr_b16 a[174:175], v35 offset:33792\n" \
"v_mfma_f32_16x16x32_bf16 v[160:163], a[188:191], a[152:155], v[160:163]\n" \
"ds_read_b64_tr_b16 a[236:237], v35 offset:40960\n" \
"v_mfma_f32_16x16x32_bf16 v[160:163], a[252:255], a[216:219], v[160:163]\n" \
"ds_read_b128 a[152:155], v28 offset:36864\n" \
"v_mfma_f32_16x16x32_bf16 v[176:179], a[188:191], a[156:159], v[176:179]\n" \
"ds_read_b128 a[216:219], v29 offset:36864\n" \
"v_mfma_f32_16x16x32_bf16 v[176:179], a[252:255], a[220:223], v[176:179]\n" \
"ds_read_b64_tr_b16 a[238:239], v35 offset:41984\n" \
"v_mfma_f32_16x16x32_bf16 v[172:175], a[184:187], a[156:159], v[172:175]\n" \
"s_cmpk_lt_u32 s3, 2\n" \
"s_cselect_b32 s6, s5, s4\n" \
"v_mfma_f32_16x16x32_bf16 v[172:175], a[248:251], a[220:223], v[172:175]\n" \
"s_lshl_b32 s7, s6, 7\n" \
"v_and_b32_e32 v213, 0x320, v20\n" \
"v_mfma_f32_16x16x32_bf16 v[168:171], a[180:183], a[156:159], v[168:171]\n" \
"s_movk_i32 s41, 0x100\n" \
"v_lshlrev_b32_e64 v214, v214, s42\n" \
"v_mfma_f32_16x16x32_bf16 v[168:171], a[244:247], a[220:223], v[168:171]\n" \
"v_mov_b32_e32 v215, 0\n" \
"s_add_i32 s40, s7, -16\n" \
"v_mfma_f32_16x16x32_bf16 v[164:167], a[176:179], a[156:159], v[164:167]\n" \
"v_cndmask_b32_e32 v41, v217, v216, vcc\n" \
"v_cndmask_b32_e32 v43, v216, v217, vcc\n" \
"v_mfma_f32_16x16x32_bf16 v[164:167], a[240:243], a[220:223], v[164:167]\n" \
"ds_read_b128 a[156:159], v28 offset:38912\n" \
"ds_read_b128 a[220:223], v29 offset:38912\n" \
"s_waitcnt lgkmcnt(15)\n" \
"v_mfma_f32_16x16x32_bf16 a[0:3], a[160:163], a[128:131], a[0:3]\n" \
"s_addk_i32 s84, 0x7c00\n" \
"v_mfma_f32_16x16x32_bf16 a[0:3], a[224:227], a[192:195], a[0:3]\n" \
"buffer_load_dwordx4 v27, s[72:75], s84 offen offset:2048 lds\n" \
"v_mfma_f32_16x16x32_bf16 a[4:7], a[164:167], a[128:131], a[4:7]\n" \
"ds_read_b64_tr_b16 a[176:177], v36 offset:32768\n" \
"v_cmp_eq_u32_e32 vcc, s41, v213\n" \
"s_waitcnt lgkmcnt(15)\n" \
"v_mfma_f32_16x16x32_bf16 a[4:7], a[228:231], a[192:195], a[4:7]\n" \
"s_addk_i32 s84, 0x7c00\n" \
"s_waitcnt lgkmcnt(11)\n" \
"v_mfma_f32_16x16x32_bf16 a[8:11], a[168:171], a[128:131], a[8:11]\n" \
"buffer_load_dwordx4 v27, s[72:75], s84 offen offset:3072 lds\n" \
"s_waitcnt lgkmcnt(9)\n" \
"v_mfma_f32_16x16x32_bf16 a[8:11], a[232:235], a[192:195], a[8:11]\n" \
"ds_read_b64_tr_b16 a[178:179], v36 offset:33792\n" \
"s_mov_b32 s41, 0\n" \
"s_waitcnt lgkmcnt(8)\n" \
"v_mfma_f32_16x16x32_bf16 a[12:15], a[172:175], a[128:131], a[12:15]\n" \
"ds_read_b64_tr_b16 a[240:241], v36 offset:40960\n" \
"s_and_b32 s43, s3, 1\n" \
"s_waitcnt lgkmcnt(5)\n" \
"v_mfma_f32_16x16x32_bf16 a[12:15], a[236:239], a[192:195], a[12:15]\n" \
"v_cndmask_b32_e32 v46, v215, v214, vcc\n" \
"v_and_b32_e32 v213, 0x330, v20\n" \
"v_mfma_f32_16x16x32_bf16 a[28:31], a[172:175], a[132:135], a[28:31]\n" \
"s_cmp_lg_u32 s7, 0\n" \
"s_cselect_b32 s40, s40, s41\n" \
"v_mfma_f32_16x16x32_bf16 a[28:31], a[236:239], a[196:199], a[28:31]\n" \
"ds_read_b64_tr_b16 a[242:243], v36 offset:41984\n" \
"s_mov_b32 s42, 0xff800000\n" \
"v_mfma_f32_16x16x32_bf16 a[24:27], a[168:171], a[132:135], a[24:27]\n" \
"s_cmp_lg_u32 s7, 0\n" \
"s_cselect_b32 s47, s41, s42\n" \
"v_mfma_f32_16x16x32_bf16 a[24:27], a[232:235], a[196:199], a[24:27]\n" \
"s_lshl_b32 s41, s43, 6\n" \
"v_lshrrev_b32_e32 v215, 6, v20\n" \
"v_mfma_f32_16x16x32_bf16 a[20:23], a[164:167], a[132:135], a[20:23]\n" \
"v_mov_b32_e32 v214, 0x3f80\n" \
"v_cmp_eq_u32_e64 vcc, 32, v213\n" \
"v_mfma_f32_16x16x32_bf16 a[20:23], a[228:231], a[196:199], a[20:23]\n" \
"ds_read_b64_tr_b16 a[180:181], v37 offset:32768\n" \
"s_or_b32 s7, s41, s7\n" \
"v_mfma_f32_16x16x32_bf16 a[16:19], a[160:163], a[132:135], a[16:19]\n" \
"v_lshrrev_b32_e32 v213, 7, v20\n" \
"v_bfe_u32 v216, v20, 6, 2\n" \
"v_mfma_f32_16x16x32_bf16 a[16:19], a[224:227], a[196:199], a[16:19]\n" \
"v_lshrrev_b32_e64 v219, 1, v20\n" \
"v_bfe_u32 v215, v215, 1, 1\n" \
"v_mfma_f32_16x16x32_bf16 a[32:35], a[160:163], a[136:139], a[32:35]\n" \
"s_waitcnt vmcnt(16)\n" \
"v_or_b32_e32 v204, v210, v204\n" \
"v_or_b32_e64 v206, v210, v206\n" \
"v_mfma_f32_16x16x32_bf16 a[32:35], a[224:227], a[200:203], a[32:35]\n" \
"v_mov_b32_e64 v210, 0\n" \
"s_add_u32 s88, s35, 0x1000\n" \
"v_mfma_f32_16x16x32_bf16 a[36:39], a[164:167], a[136:139], a[36:39]\n" \
"ds_read_b64_tr_b16 a[182:183], v37 offset:33792\n" \
"v_cndmask_b32_e64 v47, 0, v214, vcc\n" \
"v_mfma_f32_16x16x32_bf16 a[36:39], a[228:231], a[200:203], a[36:39]\n" \
"ds_read_b64_tr_b16 a[244:245], v37 offset:40960\n" \
"v_or_b32_e64 v214, s7, v213\n" \
"v_mfma_f32_16x16x32_bf16 a[40:43], a[168:171], a[136:139], a[40:43]\n" \
"s_mov_b32 m0, s32\n" \
"v_lshl_or_b32 v217, s43, 3, v213\n" \
"v_mfma_f32_16x16x32_bf16 a[40:43], a[232:235], a[200:203], a[40:43]\n" \
"s_mov_b32 exec_lo, 0xffffffff\n" \
"s_mov_b32 exec_hi, 0x0\n" \
"global_load_lds_dword v[188:189], off\n" \
"s_mov_b64 exec, -1\n" \
"v_mfma_f32_16x16x32_bf16 a[44:47], a[172:175], a[136:139], a[44:47]\n" \
"ds_read_b64_tr_b16 a[246:247], v37 offset:41984\n" \
"v_lshlrev_b32_e64 v218, 10, v212\n" \
"v_mfma_f32_16x16x32_bf16 a[44:47], a[236:239], a[200:203], a[44:47]\n" \
"v_lshlrev_b32_e32 v216, 7, v216\n" \
"v_and_b32_e32 v219, 24, v219\n" \
"v_mfma_f32_16x16x32_bf16 a[60:63], a[172:175], a[140:143], a[60:63]\n" \
"ds_write_b128 v196, v[200:203]\n" \
"v_mfma_f32_16x16x32_bf16 a[60:63], a[236:239], a[204:207], a[60:63]\n" \
"v_and_or_b32 v213, v213, 2, v215\n" \
"v_add_u32_e64 v217, s40, v217\n" \
"v_mfma_f32_16x16x32_bf16 a[56:59], a[168:171], a[140:143], a[56:59]\n" \
"ds_read_b64_tr_b16 a[184:185], v38 offset:32768\n" \
"v_or3_b32 v216, v216, v218, v219\n" \
"v_mfma_f32_16x16x32_bf16 a[56:59], a[232:235], a[204:207], a[56:59]\n" \
"v_lshlrev_b32_e64 v213, 5, v213\n" \
"s_mov_b32 s40, 0x14000\n" \
"v_mfma_f32_16x16x32_bf16 a[52:55], a[164:167], a[140:143], a[52:55]\n" \
"s_lshl_b32 s7, s2, 2\n" \
"v_or3_b32 v48, v216, v213, s40\n" \
"v_mfma_f32_16x16x32_bf16 a[52:55], a[228:231], a[204:207], a[52:55]\n" \
"ds_read_b64_tr_b16 a[186:187], v38 offset:33792\n" \
"s_lshl_b32 s40, s43, 2\n" \
"v_mfma_f32_16x16x32_bf16 a[48:51], a[160:163], a[140:143], a[48:51]\n" \
"s_lshl_b32 s6, s6, 3\n" \
"v_or_b32_e32 v213, s40, v212\n" \
"v_mfma_f32_16x16x32_bf16 a[48:51], a[224:227], a[204:207], a[48:51]\n" \
"s_or_b32 s6, s6, s40\n" \
"s_and_b32 s40, s7, 0x7c\n" \
"v_mfma_f32_16x16x32_bf16 a[64:67], a[160:163], a[144:147], a[64:67]\n" \
"ds_write_b128 v197, v[204:207]\n" \
"v_mfma_f32_16x16x32_bf16 a[64:67], a[224:227], a[208:211], a[64:67]\n" \
"ds_read_b64_tr_b16 a[248:249], v38 offset:40960\n" \
"s_or_b32 s40, s40, s3\n" \
"v_mfma_f32_16x16x32_bf16 a[68:71], a[164:167], a[144:147], a[68:71]\n" \
"s_cmpk_lt_u32 s40, 64\n" \
"s_cselect_b32 s41, s4, s5\n" \
"v_mfma_f32_16x16x32_bf16 a[68:71], a[228:231], a[208:211], a[68:71]\n" \
"ds_read_b64_tr_b16 a[250:251], v38 offset:41984\n" \
"s_lshl_b32 s40, s40, 1\n" \
"v_mfma_f32_16x16x32_bf16 a[72:75], a[168:171], a[144:147], a[72:75]\n" \
"s_lshl_b32 s41, s41, 7\n" \
"s_and_b32 s40, s40, 0x7e\n" \
"v_mfma_f32_16x16x32_bf16 a[72:75], a[232:235], a[208:211], a[72:75]\n" \
"s_or_b32 s40, s41, s40\n" \
"s_mulk_i32 s40, 0x480\n" \
"v_mfma_f32_16x16x32_bf16 a[76:79], a[172:175], a[144:147], a[76:79]\n" \
"v_add_u32_e64 v49, s40, v20\n" \
"s_mov_b32 s40, 0xc000\n" \
"v_mfma_f32_16x16x32_bf16 a[76:79], a[236:239], a[208:211], a[76:79]\n" \
"ds_read_b64_tr_b16 a[188:189], v39 offset:32768\n" \
"s_and_b32 s7, s7, 28\n" \
"v_mfma_f32_16x16x32_bf16 a[92:95], a[172:175], a[148:151], a[92:95]\n" \
"ds_write_b128 v198, v[208:211]\n" \
"v_mfma_f32_16x16x32_bf16 a[92:95], a[236:239], a[212:215], a[92:95]\n" \
"s_bfe_u32 s45, s2, 0x20003\n" \
"s_mov_b32 s41, 0x8000\n" \
"v_mfma_f32_16x16x32_bf16 a[88:91], a[168:171], a[148:151], a[88:91]\n" \
"ds_read_b64_tr_b16 a[190:191], v39 offset:33792\n" \
"v_mfma_f32_16x16x32_bf16 a[88:91], a[232:235], a[212:215], a[88:91]\n" \
"s_cmp_lg_u32 s4, 0\n" \
"s_cselect_b32 s51, s40, s41\n" \
"v_mfma_f32_16x16x32_bf16 a[84:87], a[164:167], a[148:151], a[84:87]\n" \
"s_movk_i32 s40, 0x4000\n" \
"s_lshr_b32 s44, s3, 1\n" \
"v_mfma_f32_16x16x32_bf16 a[84:87], a[228:231], a[212:215], a[84:87]\n" \
"ds_read_b64_tr_b16 a[252:253], v39 offset:40960\n" \
"s_or_b32 s46, s7, s45\n" \
"v_mfma_f32_16x16x32_bf16 a[80:83], a[160:163], a[148:151], a[80:83]\n" \
"s_cmp_lg_u32 s4, 0\n" \
"s_cselect_b32 s53, s41, s40\n" \
"v_mfma_f32_16x16x32_bf16 a[80:83], a[224:227], a[212:215], a[80:83]\n" \
"s_lshl_b32 s40, s44, 15\n" \
"s_lshl_b32 s41, s43, 14\n" \
"v_mfma_f32_16x16x32_bf16 a[96:99], a[160:163], a[152:155], a[96:99]\n" \
"ds_read_b64_tr_b16 a[254:255], v39 offset:41984\n" \
"s_lshl_b32 s48, s46, 20\n" \
"v_mfma_f32_16x16x32_bf16 a[96:99], a[224:227], a[216:219], a[96:99]\n" \
"s_or_b32 s41, s40, s41\n" \
"s_add_i32 s49, s48, 0x1000000\n" \
"v_mfma_f32_16x16x32_bf16 a[100:103], a[164:167], a[152:155], a[100:103]\n" \
"s_or_b32 s42, s49, s41\n" \
"s_lshl_b32 s4, s4, 16\n" \
"v_mfma_f32_16x16x32_bf16 a[100:103], a[228:231], a[216:219], a[100:103]\n" \
"s_or_b32 s41, s42, s4\n" \
"s_lshl_b32 s4, s5, 16\n" \
"v_mfma_f32_16x16x32_bf16 a[104:107], a[168:171], a[152:155], a[104:107]\n" \
"s_mul_i32 s5, s6, 0x24800\n" \
"s_lshl_b32 s6, s46, 9\n" \
"v_mfma_f32_16x16x32_bf16 a[104:107], a[232:235], a[216:219], a[104:107]\n" \
"s_lshl_b32 s7, s7, 6\n" \
"s_add_i32 s6, s5, s6\n" \
"v_mfma_f32_16x16x32_bf16 a[108:111], a[172:175], a[152:155], a[108:111]\n" \
"s_or_b32 s5, s5, s7\n" \
"s_lshl_b32 s7, s45, 2\n" \
"v_mfma_f32_16x16x32_bf16 a[108:111], a[236:239], a[216:219], a[108:111]\n" \
"s_or_b32 s5, s5, s7\n" \
"s_lshl_b32 s7, s3, 12\n" \
"s_waitcnt lgkmcnt(15)\n" \
"v_mfma_f32_16x16x32_bf16 a[124:127], a[172:175], a[156:159], a[124:127]\n" \
"s_or_b32 s50, s49, s7\n" \
"s_lshl_b32 s7, s3, 11\n" \
"v_mfma_f32_16x16x32_bf16 a[124:127], a[236:239], a[220:223], a[124:127]\n" \
"v_lshrrev_b32_e64 v215, 4, v20\n" \
"v_bfe_u32 v216, v20, 4, 3\n" \
"v_mfma_f32_16x16x32_bf16 a[120:123], a[168:171], a[156:159], a[120:123]\n" \
"s_or_b32 s42, s42, s4\n" \
"s_lshl_b32 s4, s43, 11\n" \
"v_mfma_f32_16x16x32_bf16 a[120:123], a[232:235], a[220:223], a[120:123]\n" \
"s_add_i32 s52, s48, s7\n" \
"s_add_i32 s53, s53, s7\n" \
"v_mfma_f32_16x16x32_bf16 a[116:119], a[164:167], a[156:159], a[116:119]\n" \
"v_mul_u32_u24_e32 v214, 0x480, v214\n" \
"v_bitop3_b32 v218, v212, v215, 7 bitop3:0x78\n" \
"v_mfma_f32_16x16x32_bf16 a[116:119], a[228:231], a[220:223], a[116:119]\n" \
"v_bitop3_b32 v212, v212, v216, 4 bitop3:0x36\n" \
"s_lshl_b32 s7, s3, 10\n" \
"v_mfma_f32_16x16x32_bf16 a[112:115], a[160:163], a[156:159], a[112:115]\n" \
"s_or_b32 s4, s40, s4\n" \
"v_lshl_or_b32 v25, v212, 4, v214\n" \
"v_mfma_f32_16x16x32_bf16 a[112:115], a[224:227], a[220:223], a[112:115]\n" \
"s_waitcnt vmcnt(1) lgkmcnt(0)\n" \
"s_barrier\n" \
"v_mfma_f32_16x16x32_bf16 v[52:55], a[176:179], a[128:131], v[52:55]\n" \
"s_add_u32 m0, s30, 0x8000\n" \
"s_movk_i32 s84, 0x280\n" \
"v_mfma_f32_16x16x32_bf16 v[52:55], a[240:243], a[192:195], v[52:55]\n" \
"buffer_load_dwordx4 v22, s[64:67], s84 offen offset:0 lds\n" \
"v_mfma_f32_16x16x32_bf16 v[56:59], a[180:183], a[128:131], v[56:59]\n" \
"s_movk_i32 s84, 0x1e80\n" \
"v_mfma_f32_16x16x32_bf16 v[56:59], a[244:247], a[192:195], v[56:59]\n" \
"buffer_load_dwordx4 v23, s[64:67], s84 offen offset:1024 lds\n" \
"v_mfma_f32_16x16x32_bf16 v[60:63], a[184:187], a[128:131], v[60:63]\n" \
"ds_read_b64_tr_b16 a[160:161], v32 offset:0\n" \
"v_mul_u32_u24_e32 v212, 0x480, v217\n" \
"v_mfma_f32_16x16x32_bf16 v[60:63], a[248:251], a[192:195], v[60:63]\n" \
"s_movk_i32 s84, 0x3a80\n" \
"v_mfma_f32_16x16x32_bf16 v[64:67], a[188:191], a[128:131], v[64:67]\n" \
"buffer_load_dwordx4 v22, s[64:67], s84 offen offset:2048 lds\n" \
"v_mfma_f32_16x16x32_bf16 v[64:67], a[252:255], a[192:195], v[64:67]\n" \
"ds_read_b128 a[128:131], v28 offset:16384\n" \
"v_bitop3_b32 v213, v215, v213, 7 bitop3:0x6c\n" \
"v_mfma_f32_16x16x32_bf16 v[80:83], a[188:191], a[132:135], v[80:83]\n" \
"ds_read_b128 a[192:195], v29 offset:16384\n" \
"s_or_b32 s40, s7, 0x14000\n" \
"v_mfma_f32_16x16x32_bf16 v[80:83], a[252:255], a[196:199], v[80:83]\n" \
"s_movk_i32 s84, 0x5680\n" \
"v_mfma_f32_16x16x32_bf16 v[76:79], a[184:187], a[132:135], v[76:79]\n" \
"buffer_load_dwordx4 v23, s[64:67], s84 offen offset:3072 lds\n" \
"v_mfma_f32_16x16x32_bf16 v[76:79], a[248:251], a[196:199], v[76:79]\n" \
"s_add_u32 m0, s31, 0x8000\n" \
"s_mov_b32 s84, 0x280000\n" \
"v_mfma_f32_16x16x32_bf16 v[72:75], a[180:183], a[132:135], v[72:75]\n" \
"buffer_load_dwordx4 v26, s[68:71], s84 offen offset:0 lds\n" \
"v_mfma_f32_16x16x32_bf16 v[72:75], a[244:247], a[196:199], v[72:75]\n" \
"ds_read_b64_tr_b16 a[162:163], v32 offset:1024\n" \
"s_lshl_b32 s43, s44, 13\n" \
"v_mfma_f32_16x16x32_bf16 v[68:71], a[176:179], a[132:135], v[68:71]\n" \
"ds_read_b64_tr_b16 a[224:225], v32 offset:8192\n" \
"s_mulk_i32 s44, 0xe800\n" \
"v_mfma_f32_16x16x32_bf16 v[68:71], a[240:243], a[196:199], v[68:71]\n" \
"ds_read_b128 a[132:135], v28 offset:18432\n" \
"s_lshl_b32 s7, s3, 8\n" \
"v_mfma_f32_16x16x32_bf16 v[84:87], a[176:179], a[136:139], v[84:87]\n" \
"ds_read_b128 a[196:199], v29 offset:18432\n" \
"v_lshl_or_b32 v24, v218, 4, v214\n" \
"v_mfma_f32_16x16x32_bf16 v[84:87], a[240:243], a[200:203], v[84:87]\n" \
"s_addk_i32 s84, 0x7c00\n" \
"v_mfma_f32_16x16x32_bf16 v[88:91], a[180:183], a[136:139], v[88:91]\n" \
"buffer_load_dwordx4 v26, s[68:71], s84 offen offset:1024 lds\n" \
"v_mfma_f32_16x16x32_bf16 v[88:91], a[244:247], a[200:203], v[88:91]\n" \
"s_addk_i32 s84, 0x7c00\n" \
"v_mfma_f32_16x16x32_bf16 v[92:95], a[184:187], a[136:139], v[92:95]\n" \
"buffer_load_dwordx4 v27, s[68:71], s84 offen offset:2048 lds\n" \
"v_mfma_f32_16x16x32_bf16 v[92:95], a[248:251], a[200:203], v[92:95]\n" \
"s_addk_i32 s84, 0x7c00\n" \
"v_mfma_f32_16x16x32_bf16 v[96:99], a[188:191], a[136:139], v[96:99]\n" \
"buffer_load_dwordx4 v27, s[68:71], s84 offen offset:3072 lds\n" \
"v_mfma_f32_16x16x32_bf16 v[96:99], a[252:255], a[200:203], v[96:99]\n" \
"ds_read_b128 a[136:139], v28 offset:20480\n" \
"v_lshl_or_b32 v21, v213, 4, v212\n" \
"v_mfma_f32_16x16x32_bf16 v[112:115], a[188:191], a[140:143], v[112:115]\n" \
"ds_read_b128 a[200:203], v29 offset:20480\n" \
"s_add_i32 s44, s44, 0x17000\n" \
"v_mfma_f32_16x16x32_bf16 v[112:115], a[252:255], a[204:207], v[112:115]\n" \
"s_add_u32 m0, s30, 0x9000\n" \
"s_mov_b32 s84, 0x8280\n" \
"v_mfma_f32_16x16x32_bf16 v[108:111], a[184:187], a[140:143], v[108:111]\n" \
"buffer_load_dwordx4 v22, s[64:67], s84 offen offset:0 lds\n" \
"v_mfma_f32_16x16x32_bf16 v[108:111], a[248:251], a[204:207], v[108:111]\n" \
"s_addk_i32 s84, 0x1c00\n" \
"v_mfma_f32_16x16x32_bf16 v[104:107], a[180:183], a[140:143], v[104:107]\n" \
"buffer_load_dwordx4 v23, s[64:67], s84 offen offset:1024 lds\n" \
"v_mfma_f32_16x16x32_bf16 v[104:107], a[244:247], a[204:207], v[104:107]\n" \
"s_addk_i32 s84, 0x1c00\n" \
"v_mfma_f32_16x16x32_bf16 v[100:103], a[176:179], a[140:143], v[100:103]\n" \
"buffer_load_dwordx4 v22, s[64:67], s84 offen offset:2048 lds\n" \
"v_mfma_f32_16x16x32_bf16 v[100:103], a[240:243], a[204:207], v[100:103]\n" \
"ds_read_b128 a[140:143], v28 offset:22528\n" \
"s_or_b32 s45, s7, 0x27000\n" \
"v_mfma_f32_16x16x32_bf16 v[116:119], a[176:179], a[144:147], v[116:119]\n" \
"ds_read_b128 a[204:207], v29 offset:22528\n" \
"s_or_b32 s46, s4, 0x10000\n" \
"v_mfma_f32_16x16x32_bf16 v[116:119], a[240:243], a[208:211], v[116:119]\n" \
"s_addk_i32 s84, 0x1c00\n" \
"v_mfma_f32_16x16x32_bf16 v[120:123], a[180:183], a[144:147], v[120:123]\n" \
"buffer_load_dwordx4 v23, s[64:67], s84 offen offset:3072 lds\n" \
"v_mfma_f32_16x16x32_bf16 v[120:123], a[244:247], a[208:211], v[120:123]\n" \
"s_add_u32 m0, s31, 0xc000\n" \
"s_mov_b32 s84, 0x280000\n" \
"v_mfma_f32_16x16x32_bf16 v[124:127], a[184:187], a[144:147], v[124:127]\n" \
"buffer_load_dwordx4 v26, s[72:75], s84 offen offset:0 lds\n" \
"v_mfma_f32_16x16x32_bf16 v[124:127], a[248:251], a[208:211], v[124:127]\n" \
"s_addk_i32 s84, 0x7c00\n" \
"v_mfma_f32_16x16x32_bf16 v[128:131], a[188:191], a[144:147], v[128:131]\n" \
"buffer_load_dwordx4 v26, s[72:75], s84 offen offset:1024 lds\n" \
"v_mfma_f32_16x16x32_bf16 v[128:131], a[252:255], a[208:211], v[128:131]\n" \
"ds_read_b128 a[144:147], v28 offset:0\n" \
"s_add_i32 s48, s6, 0x3000000\n" \
"v_mfma_f32_16x16x32_bf16 v[144:147], a[188:191], a[148:151], v[144:147]\n" \
"ds_read_b128 a[208:211], v29 offset:0\n" \
"s_add_i32 s49, s5, 0x3004000\n" \
"v_mfma_f32_16x16x32_bf16 v[144:147], a[252:255], a[212:215], v[144:147]\n" \
"ds_read_b128 v[200:203], v199 offset:0\n" \
"s_add_i32 s52, s52, 0x1010000\n" \
"v_mfma_f32_16x16x32_bf16 v[140:143], a[184:187], a[148:151], v[140:143]\n" \
"ds_read_b128 v[204:207], v199 offset:4096\n" \
"v_mfma_f32_16x16x32_bf16 v[140:143], a[248:251], a[212:215], v[140:143]\n" \
"ds_read_b64_tr_b16 a[226:227], v32 offset:9216\n" \
"v_mfma_f32_16x16x32_bf16 v[136:139], a[180:183], a[148:151], v[136:139]\n" \
"ds_read_b64_tr_b16 a[164:165], v33 offset:0\n" \
"v_mfma_f32_16x16x32_bf16 v[136:139], a[244:247], a[212:215], v[136:139]\n" \
"ds_read_b128 v[208:211], v199 offset:8192\n" \
"v_mfma_f32_16x16x32_bf16 v[132:135], a[176:179], a[148:151], v[132:135]\n" \
"ds_read_b64_tr_b16 a[166:167], v33 offset:1024\n" \
"v_mfma_f32_16x16x32_bf16 v[132:135], a[240:243], a[212:215], v[132:135]\n" \
"ds_read_b128 a[148:151], v28 offset:2048\n" \
"v_mfma_f32_16x16x32_bf16 v[148:151], a[176:179], a[152:155], v[148:151]\n" \
"ds_read_b128 a[212:215], v29 offset:2048\n" \
"v_mfma_f32_16x16x32_bf16 v[148:151], a[240:243], a[216:219], v[148:151]\n" \
"ds_read_b64_tr_b16 a[228:229], v33 offset:8192\n" \
"v_mfma_f32_16x16x32_bf16 v[152:155], a[180:183], a[152:155], v[152:155]\n" \
"ds_read_b64_tr_b16 a[230:231], v33 offset:9216\n" \
"v_mfma_f32_16x16x32_bf16 v[152:155], a[244:247], a[216:219], v[152:155]\n" \
"ds_read_b64_tr_b16 a[168:169], v34 offset:0\n" \
"v_mfma_f32_16x16x32_bf16 v[156:159], a[184:187], a[152:155], v[156:159]\n" \
"ds_read_b64_tr_b16 a[170:171], v34 offset:1024\n" \
"v_mfma_f32_16x16x32_bf16 v[156:159], a[248:251], a[216:219], v[156:159]\n" \
"ds_read_b64_tr_b16 a[232:233], v34 offset:8192\n" \
"v_mfma_f32_16x16x32_bf16 v[160:163], a[188:191], a[152:155], v[160:163]\n" \
"ds_read_b64_tr_b16 a[234:235], v34 offset:9216\n" \
"v_mfma_f32_16x16x32_bf16 v[160:163], a[252:255], a[216:219], v[160:163]\n" \
"ds_read_b128 a[152:155], v28 offset:4096\n" \
"v_mfma_f32_16x16x32_bf16 v[176:179], a[188:191], a[156:159], v[176:179]\n" \
"ds_read_b128 a[216:219], v29 offset:4096\n" \
"v_mfma_f32_16x16x32_bf16 v[176:179], a[252:255], a[220:223], v[176:179]\n" \
"ds_read_b64_tr_b16 a[172:173], v35 offset:0\n" \
"v_mfma_f32_16x16x32_bf16 v[172:175], a[184:187], a[156:159], v[172:175]\n" \
"ds_read_b64_tr_b16 a[174:175], v35 offset:1024\n" \
"v_mfma_f32_16x16x32_bf16 v[172:175], a[248:251], a[220:223], v[172:175]\n" \
"ds_read_b64_tr_b16 a[236:237], v35 offset:8192\n" \
"v_mfma_f32_16x16x32_bf16 v[168:171], a[180:183], a[156:159], v[168:171]\n" \
"ds_read_b64_tr_b16 a[238:239], v35 offset:9216\n" \
"v_mfma_f32_16x16x32_bf16 v[168:171], a[244:247], a[220:223], v[168:171]\n" \
"v_mfma_f32_16x16x32_bf16 v[164:167], a[176:179], a[156:159], v[164:167]\n" \
"v_mfma_f32_16x16x32_bf16 v[164:167], a[240:243], a[220:223], v[164:167]\n" \
"ds_read_b128 a[156:159], v28 offset:6144\n" \
"ds_read_b128 a[220:223], v29 offset:6144\n" \
"s_waitcnt lgkmcnt(15)\n" \
"v_mfma_f32_16x16x32_bf16 a[0:3], a[160:163], a[128:131], a[0:3]\n" \
"s_addk_i32 s84, 0x7c00\n" \
"v_mfma_f32_16x16x32_bf16 a[0:3], a[224:227], a[192:195], a[0:3]\n" \
"buffer_load_dwordx4 v27, s[72:75], s84 offen offset:2048 lds\n" \
"v_mfma_f32_16x16x32_bf16 a[4:7], a[164:167], a[128:131], a[4:7]\n" \
"ds_read_b64_tr_b16 a[176:177], v36 offset:0\n" \
"s_waitcnt lgkmcnt(13)\n" \
"v_mfma_f32_16x16x32_bf16 a[4:7], a[228:231], a[192:195], a[4:7]\n" \
"s_addk_i32 s84, 0x7c00\n" \
"s_waitcnt lgkmcnt(11)\n" \
"v_mfma_f32_16x16x32_bf16 a[8:11], a[168:171], a[128:131], a[8:11]\n" \
"buffer_load_dwordx4 v27, s[72:75], s84 offen offset:3072 lds\n" \
"s_waitcnt lgkmcnt(9)\n" \
"v_mfma_f32_16x16x32_bf16 a[8:11], a[232:235], a[192:195], a[8:11]\n" \
"ds_read_b64_tr_b16 a[178:179], v36 offset:1024\n" \
"s_waitcnt lgkmcnt(6)\n" \
"v_mfma_f32_16x16x32_bf16 a[12:15], a[172:175], a[128:131], a[12:15]\n" \
"ds_read_b64_tr_b16 a[240:241], v36 offset:8192\n" \
"s_waitcnt lgkmcnt(5)\n" \
"v_mfma_f32_16x16x32_bf16 a[12:15], a[236:239], a[192:195], a[12:15]\n" \
"v_mfma_f32_16x16x32_bf16 a[28:31], a[172:175], a[132:135], a[28:31]\n" \
"v_mfma_f32_16x16x32_bf16 a[28:31], a[236:239], a[196:199], a[28:31]\n" \
"ds_read_b64_tr_b16 a[242:243], v36 offset:9216\n" \
"v_mfma_f32_16x16x32_bf16 a[24:27], a[168:171], a[132:135], a[24:27]\n" \
"v_mfma_f32_16x16x32_bf16 a[24:27], a[232:235], a[196:199], a[24:27]\n" \
"v_mfma_f32_16x16x32_bf16 a[20:23], a[164:167], a[132:135], a[20:23]\n" \
"v_mfma_f32_16x16x32_bf16 a[20:23], a[228:231], a[196:199], a[20:23]\n" \
"ds_read_b64_tr_b16 a[180:181], v37 offset:0\n" \
"v_mfma_f32_16x16x32_bf16 a[16:19], a[160:163], a[132:135], a[16:19]\n" \
"v_mfma_f32_16x16x32_bf16 a[16:19], a[224:227], a[196:199], a[16:19]\n" \
"v_mfma_f32_16x16x32_bf16 a[32:35], a[160:163], a[136:139], a[32:35]\n" \
"ds_read_b64_tr_b16 a[182:183], v37 offset:1024\n" \
"v_mfma_f32_16x16x32_bf16 a[32:35], a[224:227], a[200:203], a[32:35]\n" \
"v_mfma_f32_16x16x32_bf16 a[36:39], a[164:167], a[136:139], a[36:39]\n" \
"v_mfma_f32_16x16x32_bf16 a[36:39], a[228:231], a[200:203], a[36:39]\n" \
"buffer_store_dwordx4 v[200:203], v20, s[80:83], s35 offen sc1\n" \
"v_mfma_f32_16x16x32_bf16 a[40:43], a[168:171], a[136:139], a[40:43]\n" \
"s_mov_b32 m0, s32\n" \
".fill (L_pb + 12000 - .) / 4, 4, 0xBF800000\n" \
"L_pp3:\n" \
"s_cbranch_execz L_pret\n" \
"ds_read_b64_tr_b16 a[244:245], v37 offset:8192\n" \
"v_mfma_f32_16x16x32_bf16 a[40:43], a[232:235], a[200:203], a[40:43]\n" \
"s_mov_b32 exec_lo, 0x0\n" \
"s_mov_b32 exec_hi, 0xffffffff\n" \
"global_load_lds_dword v[188:189], off\n" \
"s_mov_b64 exec, -1\n" \
"v_mfma_f32_16x16x32_bf16 a[44:47], a[172:175], a[136:139], a[44:47]\n" \
"ds_read_b64_tr_b16 a[246:247], v37 offset:9216\n" \
"v_mfma_f32_16x16x32_bf16 a[44:47], a[236:239], a[200:203], a[44:47]\n" \
"v_mfma_f32_16x16x32_bf16 a[60:63], a[172:175], a[140:143], a[60:63]\n" \
"v_mfma_f32_16x16x32_bf16 a[60:63], a[236:239], a[204:207], a[60:63]\n" \
"v_mfma_f32_16x16x32_bf16 a[56:59], a[168:171], a[140:143], a[56:59]\n" \
"ds_read_b64_tr_b16 a[184:185], v38 offset:0\n" \
"v_mfma_f32_16x16x32_bf16 a[56:59], a[232:235], a[204:207], a[56:59]\n" \
"v_mfma_f32_16x16x32_bf16 a[52:55], a[164:167], a[140:143], a[52:55]\n" \
"buffer_store_dwordx4 v[204:207], v20, s[80:83], s88 offen sc1\n" \
"v_mfma_f32_16x16x32_bf16 a[52:55], a[228:231], a[204:207], a[52:55]\n" \
"ds_read_b64_tr_b16 a[186:187], v38 offset:1024\n" \
"v_mfma_f32_16x16x32_bf16 a[48:51], a[160:163], a[140:143], a[48:51]\n" \
"v_mfma_f32_16x16x32_bf16 a[48:51], a[224:227], a[204:207], a[48:51]\n" \
"v_mfma_f32_16x16x32_bf16 a[64:67], a[160:163], a[144:147], a[64:67]\n" \
"ds_read_b64_tr_b16 a[248:249], v38 offset:8192\n" \
"v_mfma_f32_16x16x32_bf16 a[64:67], a[224:227], a[208:211], a[64:67]\n" \
"v_mfma_f32_16x16x32_bf16 a[68:71], a[164:167], a[144:147], a[68:71]\n" \
"v_mfma_f32_16x16x32_bf16 a[68:71], a[228:231], a[208:211], a[68:71]\n" \
"ds_read_b64_tr_b16 a[250:251], v38 offset:9216\n" \
"v_mfma_f32_16x16x32_bf16 a[72:75], a[168:171], a[144:147], a[72:75]\n" \
"v_mfma_f32_16x16x32_bf16 a[72:75], a[232:235], a[208:211], a[72:75]\n" \
"v_mfma_f32_16x16x32_bf16 a[76:79], a[172:175], a[144:147], a[76:79]\n" \
"buffer_store_dwordx4 v[208:211], v20, s[80:83], s36 offen sc1\n" \
"v_mfma_f32_16x16x32_bf16 a[76:79], a[236:239], a[208:211], a[76:79]\n" \
"ds_read_b64_tr_b16 a[188:189], v39 offset:0\n" \
"v_mfma_f32_16x16x32_bf16 a[92:95], a[172:175], a[148:151], a[92:95]\n" \
"v_mfma_f32_16x16x32_bf16 a[92:95], a[236:239], a[212:215], a[92:95]\n" \
"v_mfma_f32_16x16x32_bf16 a[88:91], a[168:171], a[148:151], a[88:91]\n" \
"ds_read_b64_tr_b16 a[190:191], v39 offset:1024\n" \
"v_mfma_f32_16x16x32_bf16 a[88:91], a[232:235], a[212:215], a[88:91]\n" \
"v_mfma_f32_16x16x32_bf16 a[84:87], a[164:167], a[148:151], a[84:87]\n" \
"v_mfma_f32_16x16x32_bf16 a[84:87], a[228:231], a[212:215], a[84:87]\n" \
"ds_read_b64_tr_b16 a[252:253], v39 offset:8192\n" \
"v_mfma_f32_16x16x32_bf16 a[80:83], a[160:163], a[148:151], a[80:83]\n" \
"v_mfma_f32_16x16x32_bf16 a[80:83], a[224:227], a[212:215], a[80:83]\n" \
"v_mfma_f32_16x16x32_bf16 a[96:99], a[160:163], a[152:155], a[96:99]\n" \
"ds_read_b64_tr_b16 a[254:255], v39 offset:9216\n" \
"v_mfma_f32_16x16x32_bf16 a[96:99], a[224:227], a[216:219], a[96:99]\n" \
"v_mfma_f32_16x16x32_bf16 a[100:103], a[164:167], a[152:155], a[100:103]\n" \
"v_mfma_f32_16x16x32_bf16 a[100:103], a[228:231], a[216:219], a[100:103]\n" \
"v_mfma_f32_16x16x32_bf16 a[104:107], a[168:171], a[152:155], a[104:107]\n" \
"v_mfma_f32_16x16x32_bf16 a[104:107], a[232:235], a[216:219], a[104:107]\n" \
"v_mfma_f32_16x16x32_bf16 a[108:111], a[172:175], a[152:155], a[108:111]\n" \
"v_mfma_f32_16x16x32_bf16 a[108:111], a[236:239], a[216:219], a[108:111]\n" \
"s_waitcnt lgkmcnt(15)\n" \
"v_mfma_f32_16x16x32_bf16 a[124:127], a[172:175], a[156:159], a[124:127]\n" \
"v_mfma_f32_16x16x32_bf16 a[124:127], a[236:239], a[220:223], a[124:127]\n" \
"v_mfma_f32_16x16x32_bf16 a[120:123], a[168:171], a[156:159], a[120:123]\n" \
"v_mfma_f32_16x16x32_bf16 a[120:123], a[232:235], a[220:223], a[120:123]\n" \
"v_mfma_f32_16x16x32_bf16 a[116:119], a[164:167], a[156:159], a[116:119]\n" \
"v_mfma_f32_16x16x32_bf16 a[116:119], a[228:231], a[220:223], a[116:119]\n" \
"v_mfma_f32_16x16x32_bf16 a[112:115], a[160:163], a[156:159], a[112:115]\n" \
"v_mfma_f32_16x16x32_bf16 a[112:115], a[224:227], a[220:223], a[112:115]\n" \
"s_waitcnt vmcnt(4) lgkmcnt(0)\n" \
"s_barrier\n" \
"v_mfma_f32_16x16x32_bf16 v[52:55], a[176:179], a[128:131], v[52:55]\n" \
"s_add_u32 m0, s30, 0x0\n" \
"s_movk_i32 s84, 0x300\n" \
"v_mfma_f32_16x16x32_bf16 v[52:55], a[240:243], a[192:195], v[52:55]\n" \
"buffer_load_dwordx4 v22, s[64:67], s84 offen offset:0 lds\n" \
"v_mfma_f32_16x16x32_bf16 v[56:59], a[180:183], a[128:131], v[56:59]\n" \
"ds_read_b64_tr_b16 a[160:161], v32 offset:32768\n" \
"v_mfma_f32_16x16x32_bf16 v[56:59], a[244:247], a[192:195], v[56:59]\n" \
"s_movk_i32 s84, 0x1f00\n" \
"v_mfma_f32_16x16x32_bf16 v[60:63], a[184:187], a[128:131], v[60:63]\n" \
"buffer_load_dwordx4 v23, s[64:67], s84 offen offset:1024 lds\n" \
"v_mfma_f32_16x16x32_bf16 v[60:63], a[248:251], a[192:195], v[60:63]\n" \
"ds_read_b64_tr_b16 a[162:163], v32 offset:33792\n" \
"v_mfma_f32_16x16x32_bf16 v[64:67], a[188:191], a[128:131], v[64:67]\n" \
"ds_read_b64_tr_b16 a[224:225], v32 offset:40960\n" \
"v_mfma_f32_16x16x32_bf16 v[64:67], a[252:255], a[192:195], v[64:67]\n" \
"ds_read_b128 a[128:131], v28 offset:49152\n" \
"v_mfma_f32_16x16x32_bf16 v[80:83], a[188:191], a[132:135], v[80:83]\n" \
"ds_read_b128 a[192:195], v29 offset:49152\n" \
"v_mfma_f32_16x16x32_bf16 v[80:83], a[252:255], a[196:199], v[80:83]\n" \
"s_movk_i32 s84, 0x3b00\n" \
"v_mfma_f32_16x16x32_bf16 v[76:79], a[184:187], a[132:135], v[76:79]\n" \
"buffer_load_dwordx4 v22, s[64:67], s84 offen offset:2048 lds\n" \
"v_mfma_f32_16x16x32_bf16 v[76:79], a[248:251], a[196:199], v[76:79]\n" \
"s_movk_i32 s84, 0x5700\n" \
"v_mfma_f32_16x16x32_bf16 v[72:75], a[180:183], a[132:135], v[72:75]\n" \
"buffer_load_dwordx4 v23, s[64:67], s84 offen offset:3072 lds\n" \
"v_mfma_f32_16x16x32_bf16 v[72:75], a[244:247], a[196:199], v[72:75]\n" \
"s_add_u32 m0, s31, 0x0\n" \
"s_mov_b32 s84, 0x300000\n" \
"v_mfma_f32_16x16x32_bf16 v[68:71], a[176:179], a[132:135], v[68:71]\n" \
"buffer_load_dwordx4 v26, s[68:71], s84 offen offset:0 lds\n" \
"v_mfma_f32_16x16x32_bf16 v[68:71], a[240:243], a[196:199], v[68:71]\n" \
"ds_read_b128 a[132:135], v28 offset:51200\n" \
"v_mfma_f32_16x16x32_bf16 v[84:87], a[176:179], a[136:139], v[84:87]\n" \
"ds_read_b128 a[196:199], v29 offset:51200\n" \
"v_mfma_f32_16x16x32_bf16 v[84:87], a[240:243], a[200:203], v[84:87]\n" \
"s_addk_i32 s84, 0x7c00\n" \
"v_mfma_f32_16x16x32_bf16 v[88:91], a[180:183], a[136:139], v[88:91]\n" \
"buffer_load_dwordx4 v26, s[68:71], s84 offen offset:1024 lds\n" \
"v_mfma_f32_16x16x32_bf16 v[88:91], a[244:247], a[200:203], v[88:91]\n" \
"ds_read_b64_tr_b16 a[226:227], v32 offset:41984\n" \
"v_mfma_f32_16x16x32_bf16 v[92:95], a[184:187], a[136:139], v[92:95]\n" \
"s_addk_i32 s84, 0x7c00\n" \
"v_mfma_f32_16x16x32_bf16 v[92:95], a[248:251], a[200:203], v[92:95]\n" \
"buffer_load_dwordx4 v27, s[68:71], s84 offen offset:2048 lds\n" \
"v_mfma_f32_16x16x32_bf16 v[96:99], a[188:191], a[136:139], v[96:99]\n" \
"ds_read_b64_tr_b16 a[164:165], v33 offset:32768\n" \
"v_mfma_f32_16x16x32_bf16 v[96:99], a[252:255], a[200:203], v[96:99]\n" \
"ds_read_b128 a[136:139], v28 offset:53248\n" \
"v_mfma_f32_16x16x32_bf16 v[112:115], a[188:191], a[140:143], v[112:115]\n" \
"ds_read_b128 a[200:203], v29 offset:53248\n" \
"v_mfma_f32_16x16x32_bf16 v[112:115], a[252:255], a[204:207], v[112:115]\n" \
"s_addk_i32 s84, 0x7c00\n" \
"v_mfma_f32_16x16x32_bf16 v[108:111], a[184:187], a[140:143], v[108:111]\n" \
"buffer_load_dwordx4 v27, s[68:71], s84 offen offset:3072 lds\n" \
"v_mfma_f32_16x16x32_bf16 v[108:111], a[248:251], a[204:207], v[108:111]\n" \
"s_add_u32 m0, s30, 0x1000\n" \
"s_mov_b32 s84, 0x8300\n" \
"v_mfma_f32_16x16x32_bf16 v[104:107], a[180:183], a[140:143], v[104:107]\n" \
"buffer_load_dwordx4 v22, s[64:67], s84 offen offset:0 lds\n" \
"v_mfma_f32_16x16x32_bf16 v[104:107], a[244:247], a[204:207], v[104:107]\n" \
"ds_read_b64_tr_b16 a[166:167], v33 offset:33792\n" \
"v_mfma_f32_16x16x32_bf16 v[100:103], a[176:179], a[140:143], v[100:103]\n" \
"ds_read_b64_tr_b16 a[228:229], v33 offset:40960\n" \
"v_mfma_f32_16x16x32_bf16 v[100:103], a[240:243], a[204:207], v[100:103]\n" \
"ds_read_b128 a[140:143], v28 offset:55296\n" \
"v_mfma_f32_16x16x32_bf16 v[116:119], a[176:179], a[144:147], v[116:119]\n" \
"ds_read_b128 a[204:207], v29 offset:55296\n" \
"v_mfma_f32_16x16x32_bf16 v[116:119], a[240:243], a[208:211], v[116:119]\n" \
"s_addk_i32 s84, 0x1c00\n" \
"v_mfma_f32_16x16x32_bf16 v[120:123], a[180:183], a[144:147], v[120:123]\n" \
"buffer_load_dwordx4 v23, s[64:67], s84 offen offset:1024 lds\n" \
"v_mfma_f32_16x16x32_bf16 v[120:123], a[244:247], a[208:211], v[120:123]\n" \
"s_addk_i32 s84, 0x1c00\n" \
"v_mfma_f32_16x16x32_bf16 v[124:127], a[184:187], a[144:147], v[124:127]\n" \
"buffer_load_dwordx4 v22, s[64:67], s84 offen offset:2048 lds\n" \
"v_mfma_f32_16x16x32_bf16 v[124:127], a[248:251], a[208:211], v[124:127]\n" \
"s_addk_i32 s84, 0x1c00\n" \
"s_nop 0\n" \
"v_mfma_f32_16x16x32_bf16 v[128:131], a[188:191], a[144:147], v[128:131]\n" \
"buffer_load_dwordx4 v23, s[64:67], s84 offen offset:3072 lds\n" \
"v_mfma_f32_16x16x32_bf16 v[128:131], a[252:255], a[208:211], v[128:131]\n" \
"ds_read_b128 a[144:147], v28 offset:32768\n" \
"v_mfma_f32_16x16x32_bf16 v[144:147], a[188:191], a[148:151], v[144:147]\n" \
"ds_read_b128 a[208:211], v29 offset:32768\n" \
"v_mfma_f32_16x16x32_bf16 v[144:147], a[252:255], a[212:215], v[144:147]\n" \
"s_add_u32 m0, s31, 0x4000\n" \
"s_mov_b32 s84, 0x300000\n" \
"v_mfma_f32_16x16x32_bf16 v[140:143], a[184:187], a[148:151], v[140:143]\n" \
"buffer_load_dwordx4 v26, s[72:75], s84 offen offset:0 lds\n" \
"v_mfma_f32_16x16x32_bf16 v[140:143], a[248:251], a[212:215], v[140:143]\n" \
"ds_read_b64_tr_b16 a[230:231], v33 offset:41984\n" \
"v_mfma_f32_16x16x32_bf16 v[136:139], a[180:183], a[148:151], v[136:139]\n" \
"s_addk_i32 s84, 0x7c00\n" \
"s_nop 0\n" \
"v_mfma_f32_16x16x32_bf16 v[136:139], a[244:247], a[212:215], v[136:139]\n" \
"buffer_load_dwordx4 v26, s[72:75], s84 offen offset:1024 lds\n" \
"v_mfma_f32_16x16x32_bf16 v[132:135], a[176:179], a[148:151], v[132:135]\n" \
"ds_read_b64_tr_b16 a[168:169], v34 offset:32768\n" \
"v_mfma_f32_16x16x32_bf16 v[132:135], a[240:243], a[212:215], v[132:135]\n" \
"ds_read_b128 a[148:151], v28 offset:34816\n" \
"v_mfma_f32_16x16x32_bf16 v[148:151], a[176:179], a[152:155], v[148:151]\n" \
"ds_read_b128 a[212:215], v29 offset:34816\n" \
"v_mfma_f32_16x16x32_bf16 v[148:151], a[240:243], a[216:219], v[148:151]\n" \
"ds_read_b64_tr_b16 a[170:171], v34 offset:33792\n" \
"v_mfma_f32_16x16x32_bf16 v[152:155], a[180:183], a[152:155], v[152:155]\n" \
"ds_read_b64_tr_b16 a[232:233], v34 offset:40960\n" \
"v_mfma_f32_16x16x32_bf16 v[152:155], a[244:247], a[216:219], v[152:155]\n" \
"ds_read_b64_tr_b16 a[234:235], v34 offset:41984\n" \
"v_mfma_f32_16x16x32_bf16 v[156:159], a[184:187], a[152:155], v[156:159]\n" \
"ds_read_b64_tr_b16 a[172:173], v35 offset:32768\n" \
"v_mfma_f32_16x16x32_bf16 v[156:159], a[248:251], a[216:219], v[156:159]\n" \
"ds_read_b64_tr_b16 a[174:175], v35 offset:33792\n" \
"v_mfma_f32_16x16x32_bf16 v[160:163], a[188:191], a[152:155], v[160:163]\n" \
"ds_read_b64_tr_b16 a[236:237], v35 offset:40960\n" \
"v_mfma_f32_16x16x32_bf16 v[160:163], a[252:255], a[216:219], v[160:163]\n" \
"ds_read_b128 a[152:155], v28 offset:36864\n" \
"v_mfma_f32_16x16x32_bf16 v[176:179], a[188:191], a[156:159], v[176:179]\n" \
"ds_read_b128 a[216:219], v29 offset:36864\n" \
"v_mfma_f32_16x16x32_bf16 v[176:179], a[252:255], a[220:223], v[176:179]\n" \
"ds_read_b64_tr_b16 a[238:239], v35 offset:41984\n" \
"v_mfma_f32_16x16x32_bf16 v[172:175], a[184:187], a[156:159], v[172:175]\n" \
"v_mfma_f32_16x16x32_bf16 v[172:175], a[248:251], a[220:223], v[172:175]\n" \
"v_mfma_f32_16x16x32_bf16 v[168:171], a[180:183], a[156:159], v[168:171]\n" \
"v_mfma_f32_16x16x32_bf16 v[168:171], a[244:247], a[220:223], v[168:171]\n" \
"v_mfma_f32_16x16x32_bf16 v[164:167], a[176:179], a[156:159], v[164:167]\n" \
"v_mfma_f32_16x16x32_bf16 v[164:167], a[240:243], a[220:223], v[164:167]\n" \
"ds_read_b128 a[156:159], v28 offset:38912\n" \
"ds_read_b128 a[220:223], v29 offset:38912\n" \
"s_waitcnt lgkmcnt(15)\n" \
"v_mfma_f32_16x16x32_bf16 a[0:3], a[160:163], a[128:131], a[0:3]\n" \
"s_addk_i32 s84, 0x7c00\n" \
"v_mfma_f32_16x16x32_bf16 a[0:3], a[224:227], a[192:195], a[0:3]\n" \
"buffer_load_dwordx4 v27, s[72:75], s84 offen offset:2048 lds\n" \
"v_mfma_f32_16x16x32_bf16 a[4:7], a[164:167], a[128:131], a[4:7]\n" \
"ds_read_b64_tr_b16 a[176:177], v36 offset:32768\n" \
"s_waitcnt lgkmcnt(15)\n" \
"v_mfma_f32_16x16x32_bf16 a[4:7], a[228:231], a[192:195], a[4:7]\n" \
"s_addk_i32 s84, 0x7c00\n" \
"s_waitcnt lgkmcnt(11)\n" \
"v_mfma_f32_16x16x32_bf16 a[8:11], a[168:171], a[128:131], a[8:11]\n" \
"buffer_load_dwordx4 v27, s[72:75], s84 offen offset:3072 lds\n" \
"s_waitcnt lgkmcnt(9)\n" \
"v_mfma_f32_16x16x32_bf16 a[8:11], a[232:235], a[192:195], a[8:11]\n" \
"ds_read_b64_tr_b16 a[178:179], v36 offset:33792\n" \
"s_waitcnt lgkmcnt(8)\n" \
"v_mfma_f32_16x16x32_bf16 a[12:15], a[172:175], a[128:131], a[12:15]\n" \
"ds_read_b64_tr_b16 a[240:241], v36 offset:40960\n" \
"s_waitcnt lgkmcnt(5)\n" \
"v_mfma_f32_16x16x32_bf16 a[12:15], a[236:239], a[192:195], a[12:15]\n" \
"v_mfma_f32_16x16x32_bf16 a[28:31], a[172:175], a[132:135], a[28:31]\n" \
"v_mfma_f32_16x16x32_bf16 a[28:31], a[236:239], a[196:199], a[28:31]\n" \
"ds_read_b64_tr_b16 a[242:243], v36 offset:41984\n" \
"v_mfma_f32_16x16x32_bf16 a[24:27], a[168:171], a[132:135], a[24:27]\n" \
"v_mfma_f32_16x16x32_bf16 a[24:27], a[232:235], a[196:199], a[24:27]\n" \
"v_mfma_f32_16x16x32_bf16 a[20:23], a[164:167], a[132:135], a[20:23]\n" \
"v_mfma_f32_16x16x32_bf16 a[20:23], a[228:231], a[196:199], a[20:23]\n" \
"ds_read_b64_tr_b16 a[180:181], v37 offset:32768\n" \
"v_mfma_f32_16x16x32_bf16 a[16:19], a[160:163], a[132:135], a[16:19]\n" \
"v_mfma_f32_16x16x32_bf16 a[16:19], a[224:227], a[196:199], a[16:19]\n" \
"v_mfma_f32_16x16x32_bf16 a[32:35], a[160:163], a[136:139], a[32:35]\n" \
"ds_read_b64_tr_b16 a[182:183], v37 offset:33792\n" \
"v_mfma_f32_16x16x32_bf16 a[32:35], a[224:227], a[200:203], a[32:35]\n" \
"v_mfma_f32_16x16x32_bf16 a[36:39], a[164:167], a[136:139], a[36:39]\n" \
"v_lshlrev_b32_e32 v45, 3, v20\n" \
"v_add_u32_e32 v45, s31, v45\n" \
"v_mfma_f32_16x16x32_bf16 a[36:39], a[228:231], a[200:203], a[36:39]\n" \
"s_getpc_b64 s[86:87]\n" \
"s_sub_u32 s86, s86, 0xf800\n" \
"s_subb_u32 s87, s87, 0\n" \
"s_mov_b32 m0, s32\n" \
"s_nop 0\n" \
"v_mfma_f32_16x16x32_bf16 a[40:43], a[168:171], a[136:139], a[40:43]\n" \
"global_load_lds_dword v45, s[86:87]\n" \
"v_mfma_f32_16x16x32_bf16 a[40:43], a[232:235], a[200:203], a[40:43]\n" \
"ds_read_b64_tr_b16 a[244:245], v37 offset:40960\n" \
"v_mfma_f32_16x16x32_bf16 a[44:47], a[172:175], a[136:139], a[44:47]\n" \
"ds_read_b64_tr_b16 a[246:247], v37 offset:41984\n" \
"v_mfma_f32_16x16x32_bf16 a[44:47], a[236:239], a[200:203], a[44:47]\n" \
"v_mfma_f32_16x16x32_bf16 a[60:63], a[172:175], a[140:143], a[60:63]\n" \
"v_mfma_f32_16x16x32_bf16 a[60:63], a[236:239], a[204:207], a[60:63]\n" \
"v_mfma_f32_16x16x32_bf16 a[56:59], a[168:171], a[140:143], a[56:59]\n" \
"ds_read_b64_tr_b16 a[184:185], v38 offset:32768\n" \
"v_mfma_f32_16x16x32_bf16 a[56:59], a[232:235], a[204:207], a[56:59]\n" \
"v_mfma_f32_16x16x32_bf16 a[52:55], a[164:167], a[140:143], a[52:55]\n" \
"v_mfma_f32_16x16x32_bf16 a[52:55], a[228:231], a[204:207], a[52:55]\n" \
"ds_read_b64_tr_b16 a[186:187], v38 offset:33792\n" \
"v_mfma_f32_16x16x32_bf16 a[48:51], a[160:163], a[140:143], a[48:51]\n" \
"v_mfma_f32_16x16x32_bf16 a[48:51], a[224:227], a[204:207], a[48:51]\n" \
"v_mfma_f32_16x16x32_bf16 a[64:67], a[160:163], a[144:147], a[64:67]\n" \
"ds_read_b64_tr_b16 a[248:249], v38 offset:40960\n" \
"v_mfma_f32_16x16x32_bf16 a[64:67], a[224:227], a[208:211], a[64:67]\n" \
"v_mfma_f32_16x16x32_bf16 a[68:71], a[164:167], a[144:147], a[68:71]\n" \
"v_mfma_f32_16x16x32_bf16 a[68:71], a[228:231], a[208:211], a[68:71]\n" \
"ds_read_b64_tr_b16 a[250:251], v38 offset:41984\n" \
"v_mfma_f32_16x16x32_bf16 a[72:75], a[168:171], a[144:147], a[72:75]\n" \
"v_mfma_f32_16x16x32_bf16 a[72:75], a[232:235], a[208:211], a[72:75]\n" \
"v_mfma_f32_16x16x32_bf16 a[76:79], a[172:175], a[144:147], a[76:79]\n" \
"v_mfma_f32_16x16x32_bf16 a[76:79], a[236:239], a[208:211], a[76:79]\n" \
"ds_read_b64_tr_b16 a[188:189], v39 offset:32768\n" \
"v_mfma_f32_16x16x32_bf16 a[92:95], a[172:175], a[148:151], a[92:95]\n" \
"v_mfma_f32_16x16x32_bf16 a[92:95], a[236:239], a[212:215], a[92:95]\n" \
"v_mfma_f32_16x16x32_bf16 a[88:91], a[168:171], a[148:151], a[88:91]\n" \
"ds_read_b64_tr_b16 a[190:191], v39 offset:33792\n" \
"v_mfma_f32_16x16x32_bf16 a[88:91], a[232:235], a[212:215], a[88:91]\n" \
"v_mfma_f32_16x16x32_bf16 a[84:87], a[164:167], a[148:151], a[84:87]\n" \
"v_mfma_f32_16x16x32_bf16 a[84:87], a[228:231], a[212:215], a[84:87]\n" \
"ds_read_b64_tr_b16 a[252:253], v39 offset:40960\n" \
"v_mfma_f32_16x16x32_bf16 a[80:83], a[160:163], a[148:151], a[80:83]\n" \
"v_mfma_f32_16x16x32_bf16 a[80:83], a[224:227], a[212:215], a[80:83]\n" \
"v_mfma_f32_16x16x32_bf16 a[96:99], a[160:163], a[152:155], a[96:99]\n" \
"ds_read_b64_tr_b16 a[254:255], v39 offset:41984\n" \
"v_mfma_f32_16x16x32_bf16 a[96:99], a[224:227], a[216:219], a[96:99]\n" \
"v_mfma_f32_16x16x32_bf16 a[100:103], a[164:167], a[152:155], a[100:103]\n" \
"v_mfma_f32_16x16x32_bf16 a[100:103], a[228:231], a[216:219], a[100:103]\n" \
"v_mfma_f32_16x16x32_bf16 a[104:107], a[168:171], a[152:155], a[104:107]\n" \
"v_mfma_f32_16x16x32_bf16 a[104:107], a[232:235], a[216:219], a[104:107]\n" \
"v_mfma_f32_16x16x32_bf16 a[108:111], a[172:175], a[152:155], a[108:111]\n" \
"v_mfma_f32_16x16x32_bf16 a[108:111], a[236:239], a[216:219], a[108:111]\n" \
"s_waitcnt lgkmcnt(15)\n" \
"v_mfma_f32_16x16x32_bf16 a[124:127], a[172:175], a[156:159], a[124:127]\n" \
"v_mfma_f32_16x16x32_bf16 a[124:127], a[236:239], a[220:223], a[124:127]\n" \
"v_mfma_f32_16x16x32_bf16 a[120:123], a[168:171], a[156:159], a[120:123]\n" \
"v_mfma_f32_16x16x32_bf16 a[120:123], a[232:235], a[220:223], a[120:123]\n" \
"v_mfma_f32_16x16x32_bf16 a[116:119], a[164:167], a[156:159], a[116:119]\n" \
"v_mfma_f32_16x16x32_bf16 a[116:119], a[228:231], a[220:223], a[116:119]\n" \
"v_mfma_f32_16x16x32_bf16 a[112:115], a[160:163], a[156:159], a[112:115]\n" \
"v_mfma_f32_16x16x32_bf16 a[112:115], a[224:227], a[220:223], a[112:115]\n" \
"s_waitcnt vmcnt(1) lgkmcnt(0)\n" \
"s_barrier\n" \
"v_mov_b32_e32 v45, s62\n" \
"s_mov_b64 exec, 1\n" \
"buffer_store_dword v45, off, s[80:83], s37 offset:128 sc1\n" \
"s_mov_b64 exec, -1\n" \
"v_mfma_f32_16x16x32_bf16 v[52:55], a[176:179], a[128:131], v[52:55]\n" \
"s_add_u32 m0, s30, 0x8000\n" \
"s_movk_i32 s84, 0x380\n" \
"v_mfma_f32_16x16x32_bf16 v[52:55], a[240:243], a[192:195], v[52:55]\n" \
"buffer_load_dwordx4 v22, s[64:67], s84 offen offset:0 lds\n" \
"v_mfma_f32_16x16x32_bf16 v[56:59], a[180:183], a[128:131], v[56:59]\n" \
"ds_read_b64_tr_b16 a[160:161], v32 offset:0\n" \
"v_mfma_f32_16x16x32_bf16 v[56:59], a[244:247], a[192:195], v[56:59]\n" \
"s_movk_i32 s84, 0x1f80\n" \
"v_mfma_f32_16x16x32_bf16 v[60:63], a[184:187], a[128:131], v[60:63]\n" \
"buffer_load_dwordx4 v23, s[64:67], s84 offen offset:1024 lds\n" \
"v_mfma_f32_16x16x32_bf16 v[60:63], a[248:251], a[192:195], v[60:63]\n" \
"ds_read_b64_tr_b16 a[162:163], v32 offset:1024\n" \
"v_mfma_f32_16x16x32_bf16 v[64:67], a[188:191], a[128:131], v[64:67]\n" \
"ds_read_b64_tr_b16 a[224:225], v32 offset:8192\n" \
"v_mfma_f32_16x16x32_bf16 v[64:67], a[252:255], a[192:195], v[64:67]\n" \
"ds_read_b128 a[128:131], v28 offset:16384\n" \
"v_mfma_f32_16x16x32_bf16 v[80:83], a[188:191], a[132:135], v[80:83]\n" \
"ds_read_b128 a[192:195], v29 offset:16384\n" \
"v_mfma_f32_16x16x32_bf16 v[80:83], a[252:255], a[196:199], v[80:83]\n" \
"s_movk_i32 s84, 0x3b80\n" \
"v_mfma_f32_16x16x32_bf16 v[76:79], a[184:187], a[132:135], v[76:79]\n" \
"buffer_load_dwordx4 v22, s[64:67], s84 offen offset:2048 lds\n" \
"v_mfma_f32_16x16x32_bf16 v[76:79], a[248:251], a[196:199], v[76:79]\n" \
"s_movk_i32 s84, 0x5780\n" \
"v_mfma_f32_16x16x32_bf16 v[72:75], a[180:183], a[132:135], v[72:75]\n" \
"buffer_load_dwordx4 v23, s[64:67], s84 offen offset:3072 lds\n" \
"v_mfma_f32_16x16x32_bf16 v[72:75], a[244:247], a[196:199], v[72:75]\n" \
"s_add_u32 m0, s31, 0x8000\n" \
"s_mov_b32 s84, 0x380000\n" \
"v_mfma_f32_16x16x32_bf16 v[68:71], a[176:179], a[132:135], v[68:71]\n" \
"buffer_load_dwordx4 v26, s[68:71], s84 offen offset:0 lds\n" \
"v_mfma_f32_16x16x32_bf16 v[68:71], a[240:243], a[196:199], v[68:71]\n" \
"ds_read_b128 a[132:135], v28 offset:18432\n" \
"v_mfma_f32_16x16x32_bf16 v[84:87], a[176:179], a[136:139], v[84:87]\n" \
"ds_read_b128 a[196:199], v29 offset:18432\n" \
"v_mfma_f32_16x16x32_bf16 v[84:87], a[240:243], a[200:203], v[84:87]\n" \
"s_addk_i32 s84, 0x7c00\n" \
"v_mfma_f32_16x16x32_bf16 v[88:91], a[180:183], a[136:139], v[88:91]\n" \
"buffer_load_dwordx4 v26, s[68:71], s84 offen offset:1024 lds\n" \
"v_mfma_f32_16x16x32_bf16 v[88:91], a[244:247], a[200:203], v[88:91]\n" \
"ds_read_b64_tr_b16 a[226:227], v32 offset:9216\n" \
"v_mfma_f32_16x16x32_bf16 v[92:95], a[184:187], a[136:139], v[92:95]\n" \
"s_addk_i32 s84, 0x7c00\n" \
"v_mfma_f32_16x16x32_bf16 v[92:95], a[248:251], a[200:203], v[92:95]\n" \
"buffer_load_dwordx4 v27, s[68:71], s84 offen offset:2048 lds\n" \
"v_mfma_f32_16x16x32_bf16 v[96:99], a[188:191], a[136:139], v[96:99]\n" \
"ds_read_b64_tr_b16 a[164:165], v33 offset:0\n" \
"v_mfma_f32_16x16x32_bf16 v[96:99], a[252:255], a[200:203], v[96:99]\n" \
"ds_read_b128 a[136:139], v28 offset:20480\n" \
"v_mfma_f32_16x16x32_bf16 v[112:115], a[188:191], a[140:143], v[112:115]\n" \
"ds_read_b128 a[200:203], v29 offset:20480\n" \
"v_mfma_f32_16x16x32_bf16 v[112:115], a[252:255], a[204:207], v[112:115]\n" \
"s_addk_i32 s84, 0x7c00\n" \
"s_nop 0\n" \
"v_mfma_f32_16x16x32_bf16 v[108:111], a[184:187], a[140:143], v[108:111]\n" \
"buffer_load_dwordx4 v27, s[68:71], s84 offen offset:3072 lds\n" \
"v_mfma_f32_16x16x32_bf16 v[108:111], a[248:251], a[204:207], v[108:111]\n" \
"s_add_u32 m0, s30, 0x9000\n" \
"s_mov_b32 s84, 0x8380\n" \
"v_mfma_f32_16x16x32_bf16 v[104:107], a[180:183], a[140:143], v[104:107]\n" \
"buffer_load_dwordx4 v22, s[64:67], s84 offen offset:0 lds\n" \
"v_mfma_f32_16x16x32_bf16 v[104:107], a[244:247], a[204:207], v[104:107]\n" \
"ds_read_b64_tr_b16 a[166:167], v33 offset:1024\n" \
"v_mfma_f32_16x16x32_bf16 v[100:103], a[176:179], a[140:143], v[100:103]\n" \
"ds_read_b64_tr_b16 a[228:229], v33 offset:8192\n" \
"v_mfma_f32_16x16x32_bf16 v[100:103], a[240:243], a[204:207], v[100:103]\n" \
"ds_read_b128 a[140:143], v28 offset:22528\n" \
"v_mfma_f32_16x16x32_bf16 v[116:119], a[176:179], a[144:147], v[116:119]\n" \
"ds_read_b128 a[204:207], v29 offset:22528\n" \
"v_mfma_f32_16x16x32_bf16 v[116:119], a[240:243], a[208:211], v[116:119]\n" \
"s_addk_i32 s84, 0x1c00\n" \
"v_mfma_f32_16x16x32_bf16 v[120:123], a[180:183], a[144:147], v[120:123]\n" \
"buffer_load_dwordx4 v23, s[64:67], s84 offen offset:1024 lds\n" \
"v_mfma_f32_16x16x32_bf16 v[120:123], a[244:247], a[208:211], v[120:123]\n" \
"s_addk_i32 s84, 0x1c00\n" \
"v_mfma_f32_16x16x32_bf16 v[124:127], a[184:187], a[144:147], v[124:127]\n" \
"buffer_load_dwordx4 v22, s[64:67], s84 offen offset:2048 lds\n" \
"v_mfma_f32_16x16x32_bf16 v[124:127], a[248:251], a[208:211], v[124:127]\n" \
"s_addk_i32 s84, 0x1c00\n" \
"s_nop 0\n" \
"v_mfma_f32_16x16x32_bf16 v[128:131], a[188:191], a[144:147], v[128:131]\n" \
"buffer_load_dwordx4 v23, s[64:67], s84 offen offset:3072 lds\n" \
"v_mfma_f32_16x16x32_bf16 v[128:131], a[252:255], a[208:211], v[128:131]\n" \
"ds_read_b128 a[144:147], v28 offset:0\n" \
"v_mfma_f32_16x16x32_bf16 v[144:147], a[188:191], a[148:151], v[144:147]\n" \
"ds_read_b128 a[208:211], v29 offset:0\n" \
"v_mfma_f32_16x16x32_bf16 v[144:147], a[252:255], a[212:215], v[144:147]\n" \
"s_add_u32 m0, s31, 0xc000\n" \
"s_mov_b32 s84, 0x380000\n" \
"v_mfma_f32_16x16x32_bf16 v[140:143], a[184:187], a[148:151], v[140:143]\n" \
"buffer_load_dwordx4 v26, s[72:75], s84 offen offset:0 lds\n" \
"v_mfma_f32_16x16x32_bf16 v[140:143], a[248:251], a[212:215], v[140:143]\n" \
"ds_read_b64_tr_b16 a[230:231], v33 offset:9216\n" \
"v_mfma_f32_16x16x32_bf16 v[136:139], a[180:183], a[148:151], v[136:139]\n" \
"s_addk_i32 s84, 0x7c00\n" \
"s_nop 0\n" \
"v_mfma_f32_16x16x32_bf16 v[136:139], a[244:247], a[212:215], v[136:139]\n" \
"buffer_load_dwordx4 v26, s[72:75], s84 offen offset:1024 lds\n" \
"v_mfma_f32_16x16x32_bf16 v[132:135], a[176:179], a[148:151], v[132:135]\n" \
"ds_read_b64_tr_b16 a[168:169], v34 offset:0\n" \
"v_mfma_f32_16x16x32_bf16 v[132:135], a[240:243], a[212:215], v[132:135]\n" \
"ds_read_b128 a[148:151], v28 offset:2048\n" \
"v_mfma_f32_16x16x32_bf16 v[148:151], a[176:179], a[152:155], v[148:151]\n" \
"ds_read_b128 a[212:215], v29 offset:2048\n" \
"v_mfma_f32_16x16x32_bf16 v[148:151], a[240:243], a[216:219], v[148:151]\n" \
"ds_read_b64_tr_b16 a[170:171], v34 offset:1024\n" \
"v_mfma_f32_16x16x32_bf16 v[152:155], a[180:183], a[152:155], v[152:155]\n" \
"ds_read_b64_tr_b16 a[232:233], v34 offset:8192\n" \
"v_mfma_f32_16x16x32_bf16 v[152:155], a[244:247], a[216:219], v[152:155]\n" \
"ds_read_b64_tr_b16 a[234:235], v34 offset:9216\n" \
"v_mfma_f32_16x16x32_bf16 v[156:159], a[184:187], a[152:155], v[156:159]\n" \
"ds_read_b64_tr_b16 a[172:173], v35 offset:0\n" \
"v_mfma_f32_16x16x32_bf16 v[156:159], a[248:251], a[216:219], v[156:159]\n" \
"ds_read_b64_tr_b16 a[174:175], v35 offset:1024\n" \
"v_mfma_f32_16x16x32_bf16 v[160:163], a[188:191], a[152:155], v[160:163]\n" \
"ds_read_b64_tr_b16 a[236:237], v35 offset:8192\n" \
"v_mfma_f32_16x16x32_bf16 v[160:163], a[252:255], a[216:219], v[160:163]\n" \
"ds_read_b128 a[152:155], v28 offset:4096\n" \
"v_mfma_f32_16x16x32_bf16 v[176:179], a[188:191], a[156:159], v[176:179]\n" \
"ds_read_b128 a[216:219], v29 offset:4096\n" \
"v_mfma_f32_16x16x32_bf16 v[176:179], a[252:255], a[220:223], v[176:179]\n" \
"ds_read_b64_tr_b16 a[238:239], v35 offset:9216\n" \
"v_mfma_f32_16x16x32_bf16 v[172:175], a[184:187], a[156:159], v[172:175]\n" \
"v_mfma_f32_16x16x32_bf16 v[172:175], a[248:251], a[220:223], v[172:175]\n" \
"v_mfma_f32_16x16x32_bf16 v[168:171], a[180:183], a[156:159], v[168:171]\n" \
"v_mfma_f32_16x16x32_bf16 v[168:171], a[244:247], a[220:223], v[168:171]\n" \
"v_mfma_f32_16x16x32_bf16 v[164:167], a[176:179], a[156:159], v[164:167]\n" \
"v_mfma_f32_16x16x32_bf16 v[164:167], a[240:243], a[220:223], v[164:167]\n" \
"ds_read_b128 a[156:159], v28 offset:6144\n" \
"ds_read_b128 a[220:223], v29 offset:6144\n" \
"s_waitcnt lgkmcnt(15)\n" \
"v_mfma_f32_16x16x32_bf16 a[0:3], a[160:163], a[128:131], a[0:3]\n" \
"s_addk_i32 s84, 0x7c00\n" \
"v_mfma_f32_16x16x32_bf16 a[0:3], a[224:227], a[192:195], a[0:3]\n" \
"buffer_load_dwordx4 v27, s[72:75], s84 offen offset:2048 lds\n" \
"v_mfma_f32_16x16x32_bf16 a[4:7], a[164:167], a[128:131], a[4:7]\n" \
"ds_read_b64_tr_b16 a[176:177], v36 offset:0\n" \
"s_waitcnt lgkmcnt(15)\n" \
"v_mfma_f32_16x16x32_bf16 a[4:7], a[228:231], a[192:195], a[4:7]\n" \
"s_addk_i32 s84, 0x7c00\n" \
"s_waitcnt lgkmcnt(11)\n" \
"v_mfma_f32_16x16x32_bf16 a[8:11], a[168:171], a[128:131], a[8:11]\n" \
"buffer_load_dwordx4 v27, s[72:75], s84 offen offset:3072 lds\n" \
"s_waitcnt lgkmcnt(9)\n" \
"v_mfma_f32_16x16x32_bf16 a[8:11], a[232:235], a[192:195], a[8:11]\n" \
"ds_read_b64_tr_b16 a[178:179], v36 offset:1024\n" \
"s_waitcnt lgkmcnt(8)\n" \
"v_mfma_f32_16x16x32_bf16 a[12:15], a[172:175], a[128:131], a[12:15]\n" \
"ds_read_b64_tr_b16 a[240:241], v36 offset:8192\n" \
"s_waitcnt lgkmcnt(5)\n" \
"v_mfma_f32_16x16x32_bf16 a[12:15], a[236:239], a[192:195], a[12:15]\n" \
"v_mfma_f32_16x16x32_bf16 a[28:31], a[172:175], a[132:135], a[28:31]\n" \
"v_mfma_f32_16x16x32_bf16 a[28:31], a[236:239], a[196:199], a[28:31]\n" \
"ds_read_b64_tr_b16 a[242:243], v36 offset:9216\n" \
"v_mfma_f32_16x16x32_bf16 a[24:27], a[168:171], a[132:135], a[24:27]\n" \
"v_mfma_f32_16x16x32_bf16 a[24:27], a[232:235], a[196:199], a[24:27]\n" \
"v_mfma_f32_16x16x32_bf16 a[20:23], a[164:167], a[132:135], a[20:23]\n" \
"v_mfma_f32_16x16x32_bf16 a[20:23], a[228:231], a[196:199], a[20:23]\n" \
"ds_read_b64_tr_b16 a[180:181], v37 offset:0\n" \
"v_mfma_f32_16x16x32_bf16 a[16:19], a[160:163], a[132:135], a[16:19]\n" \
"v_mfma_f32_16x16x32_bf16 a[16:19], a[224:227], a[196:199], a[16:19]\n" \
"v_mfma_f32_16x16x32_bf16 a[32:35], a[160:163], a[136:139], a[32:35]\n" \
"v_bfe_u32 v44, v20, 2, 5\n" \
"ds_read_b64_tr_b16 a[182:183], v37 offset:1024\n" \
"v_mfma_f32_16x16x32_bf16 a[32:35], a[224:227], a[200:203], a[32:35]\n" \
"buffer_load_dword v44, v44, s[80:83], s38 offen offset:128 sc1\n" \
"v_mfma_f32_16x16x32_bf16 a[36:39], a[164:167], a[136:139], a[36:39]\n" \
"v_mfma_f32_16x16x32_bf16 a[36:39], a[228:231], a[200:203], a[36:39]\n" \
"ds_read_b64_tr_b16 a[244:245], v37 offset:8192\n" \
"v_mfma_f32_16x16x32_bf16 a[40:43], a[168:171], a[136:139], a[40:43]\n" \
"v_mfma_f32_16x16x32_bf16 a[40:43], a[232:235], a[200:203], a[40:43]\n" \
"v_mfma_f32_16x16x32_bf16 a[44:47], a[172:175], a[136:139], a[44:47]\n" \
"ds_read_b64_tr_b16 a[246:247], v37 offset:9216\n" \
"v_mfma_f32_16x16x32_bf16 a[44:47], a[236:239], a[200:203], a[44:47]\n" \
"v_mfma_f32_16x16x32_bf16 a[60:63], a[172:175], a[140:143], a[60:63]\n" \
"global_store_dwordx4 v49, v[0:3], s[28:29] offset:0 sc1\n" \
"v_mfma_f32_16x16x32_bf16 a[60:63], a[236:239], a[204:207], a[60:63]\n" \
"v_mfma_f32_16x16x32_bf16 a[56:59], a[168:171], a[140:143], a[56:59]\n" \
"ds_read_b64_tr_b16 a[184:185], v38 offset:0\n" \
"v_mfma_f32_16x16x32_bf16 a[56:59], a[232:235], a[204:207], a[56:59]\n" \
"v_mfma_f32_16x16x32_bf16 a[52:55], a[164:167], a[140:143], a[52:55]\n" \
"v_mfma_f32_16x16x32_bf16 a[52:55], a[228:231], a[204:207], a[52:55]\n" \
"ds_read_b64_tr_b16 a[186:187], v38 offset:1024\n" \
"v_mfma_f32_16x16x32_bf16 a[48:51], a[160:163], a[140:143], a[48:51]\n" \
"v_mfma_f32_16x16x32_bf16 a[48:51], a[224:227], a[204:207], a[48:51]\n" \
"v_mfma_f32_16x16x32_bf16 a[64:67], a[160:163], a[144:147], a[64:67]\n" \
"global_store_dwordx4 v49, v[0:3], s[28:29] offset:1024 sc1\n" \
"v_mfma_f32_16x16x32_bf16 a[64:67], a[224:227], a[208:211], a[64:67]\n" \
"ds_read_b64_tr_b16 a[248:249], v38 offset:8192\n" \
"v_mfma_f32_16x16x32_bf16 a[68:71], a[164:167], a[144:147], a[68:71]\n" \
"v_mfma_f32_16x16x32_bf16 a[68:71], a[228:231], a[208:211], a[68:71]\n" \
"ds_read_b64_tr_b16 a[250:251], v38 offset:9216\n" \
"v_mfma_f32_16x16x32_bf16 a[72:75], a[168:171], a[144:147], a[72:75]\n" \
"v_mfma_f32_16x16x32_bf16 a[72:75], a[232:235], a[208:211], a[72:75]\n" \
"v_mfma_f32_16x16x32_bf16 a[76:79], a[172:175], a[144:147], a[76:79]\n" \
"v_mfma_f32_16x16x32_bf16 a[76:79], a[236:239], a[208:211], a[76:79]\n" \
"ds_read_b64_tr_b16 a[188:189], v39 offset:0\n" \
"v_mfma_f32_16x16x32_bf16 a[92:95], a[172:175], a[148:151], a[92:95]\n" \
"s_mov_b64 exec, 0xffff\n" \
"global_store_dwordx4 v49, v[0:3], s[28:29] offset:2048 sc1\n" \
"s_mov_b64 exec, -1\n" \
"s_nop 0\n" \
"v_mfma_f32_16x16x32_bf16 a[92:95], a[236:239], a[212:215], a[92:95]\n" \
"v_mfma_f32_16x16x32_bf16 a[88:91], a[168:171], a[148:151], a[88:91]\n" \
"ds_read_b64_tr_b16 a[190:191], v39 offset:1024\n" \
"v_mfma_f32_16x16x32_bf16 a[88:91], a[232:235], a[212:215], a[88:91]\n" \
"v_mfma_f32_16x16x32_bf16 a[84:87], a[164:167], a[148:151], a[84:87]\n" \
"v_mfma_f32_16x16x32_bf16 a[84:87], a[228:231], a[212:215], a[84:87]\n" \
"ds_read_b64_tr_b16 a[252:253], v39 offset:8192\n" \
"v_mfma_f32_16x16x32_bf16 a[80:83], a[160:163], a[148:151], a[80:83]\n" \
"v_mfma_f32_16x16x32_bf16 a[80:83], a[224:227], a[212:215], a[80:83]\n" \
"v_mfma_f32_16x16x32_bf16 a[96:99], a[160:163], a[152:155], a[96:99]\n" \
"ds_read_b64_tr_b16 a[254:255], v39 offset:9216\n" \
"v_mfma_f32_16x16x32_bf16 a[96:99], a[224:227], a[216:219], a[96:99]\n" \
"v_mfma_f32_16x16x32_bf16 a[100:103], a[164:167], a[152:155], a[100:103]\n" \
"v_mfma_f32_16x16x32_bf16 a[100:103], a[228:231], a[216:219], a[100:103]\n" \
"v_mfma_f32_16x16x32_bf16 a[104:107], a[168:171], a[152:155], a[104:107]\n" \
"v_mfma_f32_16x16x32_bf16 a[104:107], a[232:235], a[216:219], a[104:107]\n" \
"v_mfma_f32_16x16x32_bf16 a[108:111], a[172:175], a[152:155], a[108:111]\n" \
"v_mfma_f32_16x16x32_bf16 a[108:111], a[236:239], a[216:219], a[108:111]\n" \
"s_waitcnt lgkmcnt(15)\n" \
"v_mfma_f32_16x16x32_bf16 a[124:127], a[172:175], a[156:159], a[124:127]\n" \
"v_mfma_f32_16x16x32_bf16 a[124:127], a[236:239], a[220:223], a[124:127]\n" \
"v_mfma_f32_16x16x32_bf16 a[120:123], a[168:171], a[156:159], a[120:123]\n" \
"v_mfma_f32_16x16x32_bf16 a[120:123], a[232:235], a[220:223], a[120:123]\n" \
"v_mfma_f32_16x16x32_bf16 a[116:119], a[164:167], a[156:159], a[116:119]\n" \
"v_mfma_f32_16x16x32_bf16 a[116:119], a[228:231], a[220:223], a[116:119]\n" \
"v_mfma_f32_16x16x32_bf16 a[112:115], a[160:163], a[156:159], a[112:115]\n" \
"v_mfma_f32_16x16x32_bf16 a[112:115], a[224:227], a[220:223], a[112:115]\n" \
"s_waitcnt vmcnt(3) lgkmcnt(0)\n" \
"s_barrier\n" \
"v_cmp_ne_u32_e32 vcc, s62, v44\n" \
"s_nop 4\n" \
"s_cbranch_vccnz L_poll60_%=\n" \
"L_pollr60_%=:\n" \
"s_mov_b32 s68, s12\n" \
"s_and_b32 s69, s13, 0xffff\n" \
"s_mov_b32 s70, 0x480000\n" \
"s_mov_b32 s71, 0x20000\n" \
"v_mfma_f32_16x16x32_bf16 v[52:55], a[176:179], a[128:131], v[52:55]\n" \
"s_add_u32 m0, s40, 0x0\n" \
"s_add_u32 s85, s39, 0x0\n" \
"s_nop 0\n" \
"v_mfma_f32_16x16x32_bf16 v[52:55], a[240:243], a[192:195], v[52:55]\n" \
"buffer_load_dwordx4 v20, s[80:83], s85 offen sc1 lds\n" \
"v_mfma_f32_16x16x32_bf16 v[56:59], a[180:183], a[128:131], v[56:59]\n" \
"ds_read_b64_tr_b16 a[160:161], v32 offset:32768\n" \
"v_mfma_f32_16x16x32_bf16 v[56:59], a[244:247], a[192:195], v[56:59]\n" \
"s_add_u32 m0, s40, 0x1000\n" \
"s_add_u32 s85, s39, 0x1000\n" \
"v_mfma_f32_16x16x32_bf16 v[60:63], a[184:187], a[128:131], v[60:63]\n" \
"buffer_load_dwordx4 v20, s[80:83], s85 offen sc1 lds\n" \
".fill (L_pb + 16096 - .) / 4, 4, 0xBF800000\n" \
"L_pp4:\n" \
"s_cbranch_execz L_pret\n" \
"v_mfma_f32_16x16x32_bf16 v[60:63], a[248:251], a[192:195], v[60:63]\n" \
"ds_read_b64_tr_b16 a[162:163], v32 offset:33792\n" \
"v_mfma_f32_16x16x32_bf16 v[64:67], a[188:191], a[128:131], v[64:67]\n" \
"ds_read_b64_tr_b16 a[224:225], v32 offset:40960\n" \
"v_mfma_f32_16x16x32_bf16 v[64:67], a[252:255], a[192:195], v[64:67]\n" \
"ds_read_b128 a[128:131], v28 offset:49152\n" \
"v_mfma_f32_16x16x32_bf16 v[80:83], a[188:191], a[132:135], v[80:83]\n" \
"ds_read_b128 a[192:195], v29 offset:49152\n" \
"v_mfma_f32_16x16x32_bf16 v[80:83], a[252:255], a[196:199], v[80:83]\n" \
"s_add_u32 m0, s40, 0x3000\n" \
"s_movk_i32 s84, 0x0\n" \
"v_mfma_f32_16x16x32_bf16 v[76:79], a[184:187], a[132:135], v[76:79]\n" \
"buffer_load_dwordx4 v21, s[76:79], s84 offen offset:0 lds\n" \
"v_mfma_f32_16x16x32_bf16 v[76:79], a[248:251], a[196:199], v[76:79]\n" \
"s_add_u32 m0, s30, 0x0\n" \
"s_movk_i32 s84, 0x0\n" \
"v_mfma_f32_16x16x32_bf16 v[72:75], a[180:183], a[132:135], v[72:75]\n" \
"buffer_load_dwordx4 v24, s[76:79], s84 offen offset:0 lds\n" \
"v_mfma_f32_16x16x32_bf16 v[72:75], a[244:247], a[196:199], v[72:75]\n" \
"ds_read_b64_tr_b16 a[226:227], v32 offset:41984\n" \
"v_mfma_f32_16x16x32_bf16 v[68:71], a[176:179], a[132:135], v[68:71]\n" \
"ds_read_b64_tr_b16 a[164:165], v33 offset:32768\n" \
"v_mfma_f32_16x16x32_bf16 v[68:71], a[240:243], a[196:199], v[68:71]\n" \
"ds_read_b128 a[132:135], v28 offset:51200\n" \
"v_mfma_f32_16x16x32_bf16 v[84:87], a[176:179], a[136:139], v[84:87]\n" \
"ds_read_b128 a[196:199], v29 offset:51200\n" \
"v_mfma_f32_16x16x32_bf16 v[84:87], a[240:243], a[200:203], v[84:87]\n" \
"s_movk_i32 s84, 0x2000\n" \
"v_mfma_f32_16x16x32_bf16 v[88:91], a[180:183], a[136:139], v[88:91]\n" \
"buffer_load_dwordx4 v25, s[76:79], s84 offen offset:1024 lds\n" \
"v_mfma_f32_16x16x32_bf16 v[88:91], a[244:247], a[200:203], v[88:91]\n" \
"s_movk_i32 s84, 0x4000\n" \
"v_mfma_f32_16x16x32_bf16 v[92:95], a[184:187], a[136:139], v[92:95]\n" \
"buffer_load_dwordx4 v24, s[76:79], s84 offen offset:2048 lds\n" \
"v_mfma_f32_16x16x32_bf16 v[92:95], a[248:251], a[200:203], v[92:95]\n" \
"ds_read_b64_tr_b16 a[166:167], v33 offset:33792\n" \
"v_mfma_f32_16x16x32_bf16 v[96:99], a[188:191], a[136:139], v[96:99]\n" \
"ds_read_b64_tr_b16 a[228:229], v33 offset:40960\n" \
"v_mfma_f32_16x16x32_bf16 v[96:99], a[252:255], a[200:203], v[96:99]\n" \
"ds_read_b128 a[136:139], v28 offset:53248\n" \
"v_mfma_f32_16x16x32_bf16 v[112:115], a[188:191], a[140:143], v[112:115]\n" \
"ds_read_b128 a[200:203], v29 offset:53248\n" \
"v_mfma_f32_16x16x32_bf16 v[112:115], a[252:255], a[204:207], v[112:115]\n" \
"s_movk_i32 s84, 0x6000\n" \
"v_mfma_f32_16x16x32_bf16 v[108:111], a[184:187], a[140:143], v[108:111]\n" \
"buffer_load_dwordx4 v25, s[76:79], s84 offen offset:3072 lds\n" \
"v_mfma_f32_16x16x32_bf16 v[108:111], a[248:251], a[204:207], v[108:111]\n" \
"s_add_u32 m0, s31, 0x0\n" \
"s_movk_i32 s84, 0x0\n" \
"v_mfma_f32_16x16x32_bf16 v[104:107], a[180:183], a[140:143], v[104:107]\n" \
"buffer_load_dwordx4 v26, s[68:71], s84 offen offset:0 lds\n" \
"v_mfma_f32_16x16x32_bf16 v[104:107], a[244:247], a[204:207], v[104:107]\n" \
"s_movk_i32 s84, 0x7c00\n" \
"v_mfma_f32_16x16x32_bf16 v[100:103], a[176:179], a[140:143], v[100:103]\n" \
"buffer_load_dwordx4 v26, s[68:71], s84 offen offset:1024 lds\n" \
"v_mfma_f32_16x16x32_bf16 v[100:103], a[240:243], a[204:207], v[100:103]\n" \
"ds_read_b128 a[140:143], v28 offset:55296\n" \
"v_mfma_f32_16x16x32_bf16 v[116:119], a[176:179], a[144:147], v[116:119]\n" \
"ds_read_b128 a[204:207], v29 offset:55296\n" \
"v_mfma_f32_16x16x32_bf16 v[116:119], a[240:243], a[208:211], v[116:119]\n" \
"s_addk_i32 s84, 0x7c00\n" \
"v_mfma_f32_16x16x32_bf16 v[120:123], a[180:183], a[144:147], v[120:123]\n" \
"buffer_load_dwordx4 v27, s[68:71], s84 offen offset:2048 lds\n" \
"v_mfma_f32_16x16x32_bf16 v[120:123], a[244:247], a[208:211], v[120:123]\n" \
"ds_read_b64_tr_b16 a[230:231], v33 offset:41984\n" \
"v_mfma_f32_16x16x32_bf16 v[124:127], a[184:187], a[144:147], v[124:127]\n" \
"s_addk_i32 s84, 0x7c00\n" \
"v_mfma_f32_16x16x32_bf16 v[124:127], a[248:251], a[208:211], v[124:127]\n" \
"buffer_load_dwordx4 v27, s[68:71], s84 offen offset:3072 lds\n" \
"v_mfma_f32_16x16x32_bf16 v[128:131], a[188:191], a[144:147], v[128:131]\n" \
"ds_read_b64_tr_b16 a[168:169], v34 offset:32768\n" \
"v_mfma_f32_16x16x32_bf16 v[128:131], a[252:255], a[208:211], v[128:131]\n" \
"ds_read_b128 a[144:147], v28 offset:32768\n" \
"v_mfma_f32_16x16x32_bf16 v[144:147], a[188:191], a[148:151], v[144:147]\n" \
"ds_read_b128 a[208:211], v29 offset:32768\n" \
"v_mfma_f32_16x16x32_bf16 v[144:147], a[252:255], a[212:215], v[144:147]\n" \
"s_add_u32 m0, s30, 0x1000\n" \
"s_mov_b32 s84, 0x9000\n" \
"v_mfma_f32_16x16x32_bf16 v[140:143], a[184:187], a[148:151], v[140:143]\n" \
"buffer_load_dwordx4 v24, s[76:79], s84 offen offset:0 lds\n" \
"v_mfma_f32_16x16x32_bf16 v[140:143], a[248:251], a[212:215], v[140:143]\n" \
"ds_read_b64_tr_b16 a[170:171], v34 offset:33792\n" \
"v_mfma_f32_16x16x32_bf16 v[136:139], a[180:183], a[148:151], v[136:139]\n" \
"s_addk_i32 s84, 0x2000\n" \
"s_nop 0\n" \
"v_mfma_f32_16x16x32_bf16 v[136:139], a[244:247], a[212:215], v[136:139]\n" \
"buffer_load_dwordx4 v25, s[76:79], s84 offen offset:1024 lds\n" \
"v_mfma_f32_16x16x32_bf16 v[132:135], a[176:179], a[148:151], v[132:135]\n" \
"ds_read_b64_tr_b16 a[232:233], v34 offset:40960\n" \
"v_mfma_f32_16x16x32_bf16 v[132:135], a[240:243], a[212:215], v[132:135]\n" \
"ds_read_b128 a[148:151], v28 offset:34816\n" \
"v_mfma_f32_16x16x32_bf16 v[148:151], a[176:179], a[152:155], v[148:151]\n" \
"ds_read_b128 a[212:215], v29 offset:34816\n" \
"v_mfma_f32_16x16x32_bf16 v[148:151], a[240:243], a[216:219], v[148:151]\n" \
"ds_read_b64_tr_b16 a[234:235], v34 offset:41984\n" \
"v_mfma_f32_16x16x32_bf16 v[152:155], a[180:183], a[152:155], v[152:155]\n" \
"ds_read_b64_tr_b16 a[172:173], v35 offset:32768\n" \
"v_mfma_f32_16x16x32_bf16 v[152:155], a[244:247], a[216:219], v[152:155]\n" \
"ds_read_b64_tr_b16 a[174:175], v35 offset:33792\n" \
"v_mfma_f32_16x16x32_bf16 v[156:159], a[184:187], a[152:155], v[156:159]\n" \
"ds_read_b64_tr_b16 a[236:237], v35 offset:40960\n" \
"v_mfma_f32_16x16x32_bf16 v[156:159], a[248:251], a[216:219], v[156:159]\n" \
"ds_read_b64_tr_b16 a[238:239], v35 offset:41984\n" \
"v_mfma_f32_16x16x32_bf16 v[160:163], a[188:191], a[152:155], v[160:163]\n" \
"v_mfma_f32_16x16x32_bf16 v[160:163], a[252:255], a[216:219], v[160:163]\n" \
"ds_read_b128 a[152:155], v28 offset:36864\n" \
"v_mfma_f32_16x16x32_bf16 v[176:179], a[188:191], a[156:159], v[176:179]\n" \
"ds_read_b128 a[216:219], v29 offset:36864\n" \
"v_mfma_f32_16x16x32_bf16 v[176:179], a[252:255], a[220:223], v[176:179]\n" \
"v_mfma_f32_16x16x32_bf16 v[172:175], a[184:187], a[156:159], v[172:175]\n" \
"v_mfma_f32_16x16x32_bf16 v[172:175], a[248:251], a[220:223], v[172:175]\n" \
"v_mfma_f32_16x16x32_bf16 v[168:171], a[180:183], a[156:159], v[168:171]\n" \
"v_mfma_f32_16x16x32_bf16 v[168:171], a[244:247], a[220:223], v[168:171]\n" \
"v_mfma_f32_16x16x32_bf16 v[164:167], a[176:179], a[156:159], v[164:167]\n" \
"v_mfma_f32_16x16x32_bf16 v[164:167], a[240:243], a[220:223], v[164:167]\n" \
"ds_read_b128 a[156:159], v28 offset:38912\n" \
"ds_read_b128 a[220:223], v29 offset:38912\n" \
"s_waitcnt lgkmcnt(15)\n" \
"v_mfma_f32_16x16x32_bf16 a[0:3], a[160:163], a[128:131], a[0:3]\n" \
"s_addk_i32 s84, 0x2000\n" \
"v_mfma_f32_16x16x32_bf16 a[0:3], a[224:227], a[192:195], a[0:3]\n" \
"buffer_load_dwordx4 v24, s[76:79], s84 offen offset:2048 lds\n" \
"v_mfma_f32_16x16x32_bf16 a[4:7], a[164:167], a[128:131], a[4:7]\n" \
"ds_read_b64_tr_b16 a[176:177], v36 offset:32768\n" \
"v_mfma_f32_16x16x32_bf16 a[4:7], a[228:231], a[192:195], a[4:7]\n" \
"s_addk_i32 s84, 0x2000\n" \
"s_waitcnt lgkmcnt(13)\n" \
"v_mfma_f32_16x16x32_bf16 a[8:11], a[168:171], a[128:131], a[8:11]\n" \
"buffer_load_dwordx4 v25, s[76:79], s84 offen offset:3072 lds\n" \
"s_waitcnt lgkmcnt(9)\n" \
"v_mfma_f32_16x16x32_bf16 a[8:11], a[232:235], a[192:195], a[8:11]\n" \
"ds_read_b64_tr_b16 a[178:179], v36 offset:33792\n" \
"s_waitcnt lgkmcnt(8)\n" \
"v_mfma_f32_16x16x32_bf16 a[12:15], a[172:175], a[128:131], a[12:15]\n" \
"ds_read_b64_tr_b16 a[240:241], v36 offset:40960\n" \
"v_accvgpr_read_b32 v184, a0\n" \
"s_waitcnt lgkmcnt(7)\n" \
"v_mfma_f32_16x16x32_bf16 a[12:15], a[236:239], a[192:195], a[12:15]\n" \
"v_accvgpr_read_b32 v185, a1\n" \
"v_accvgpr_read_b32 v186, a2\n" \
"v_mfma_f32_16x16x32_bf16 a[28:31], a[172:175], a[132:135], a[28:31]\n" \
"v_accvgpr_read_b32 v187, a3\n" \
"v_accvgpr_read_b32 v180, a4\n" \
"v_mfma_f32_16x16x32_bf16 a[28:31], a[236:239], a[196:199], a[28:31]\n" \
"s_add_u32 m0, s30, 0x20000\n" \
"s_movk_i32 s84, 0x100\n" \
"v_mfma_f32_16x16x32_bf16 a[24:27], a[168:171], a[132:135], a[24:27]\n" \
"buffer_load_dwordx4 v24, s[76:79], s84 offen offset:0 lds\n" \
"v_mfma_f32_16x16x32_bf16 a[24:27], a[232:235], a[196:199], a[24:27]\n" \
"ds_read_b64_tr_b16 a[242:243], v36 offset:41984\n" \
"v_accvgpr_read_b32 v181, a5\n" \
"v_mfma_f32_16x16x32_bf16 a[20:23], a[164:167], a[132:135], a[20:23]\n" \
"v_accvgpr_read_b32 v182, a6\n" \
"v_accvgpr_read_b32 v183, a7\n" \
"v_mfma_f32_16x16x32_bf16 a[20:23], a[228:231], a[196:199], a[20:23]\n" \
"ds_read_b64_tr_b16 a[180:181], v37 offset:32768\n" \
"v_cvt_pk_bf16_f32 v184, v184, v185\n" \
"v_mfma_f32_16x16x32_bf16 a[16:19], a[160:163], a[132:135], a[16:19]\n" \
"s_movk_i32 s84, 0x2100\n" \
"s_nop 0\n" \
"v_mfma_f32_16x16x32_bf16 a[16:19], a[224:227], a[196:199], a[16:19]\n" \
"buffer_load_dwordx4 v25, s[76:79], s84 offen offset:1024 lds\n" \
"v_mfma_f32_16x16x32_bf16 a[32:35], a[160:163], a[136:139], a[32:35]\n" \
"ds_read_b64_tr_b16 a[182:183], v37 offset:33792\n" \
"v_cvt_pk_bf16_f32 v185, v186, v187\n" \
"v_mfma_f32_16x16x32_bf16 a[32:35], a[224:227], a[200:203], a[32:35]\n" \
"v_cvt_pk_bf16_f32 v186, v180, v181\n" \
"v_cvt_pk_bf16_f32 v187, v182, v183\n" \
"v_mfma_f32_16x16x32_bf16 a[36:39], a[164:167], a[136:139], a[36:39]\n" \
"v_accvgpr_read_b32 v188, a8\n" \
"v_accvgpr_read_b32 v189, a9\n" \
"v_mfma_f32_16x16x32_bf16 a[36:39], a[228:231], a[200:203], a[36:39]\n" \
"s_movk_i32 s84, 0x4100\n" \
"s_nop 0\n" \
"v_mfma_f32_16x16x32_bf16 a[40:43], a[168:171], a[136:139], a[40:43]\n" \
"buffer_load_dwordx4 v24, s[76:79], s84 offen offset:2048 lds\n" \
"v_mfma_f32_16x16x32_bf16 a[40:43], a[232:235], a[200:203], a[40:43]\n" \
"ds_read_b64_tr_b16 a[244:245], v37 offset:40960\n" \
"v_accvgpr_read_b32 v190, a10\n" \
"v_mfma_f32_16x16x32_bf16 a[44:47], a[172:175], a[136:139], a[44:47]\n" \
"ds_read_b64_tr_b16 a[246:247], v37 offset:41984\n" \
"v_accvgpr_read_b32 v191, a11\n" \
"v_mfma_f32_16x16x32_bf16 a[44:47], a[236:239], a[200:203], a[44:47]\n" \
"v_accvgpr_read_b32 v180, a12\n" \
"v_accvgpr_read_b32 v181, a13\n" \
"v_mfma_f32_16x16x32_bf16 a[60:63], a[172:175], a[140:143], a[60:63]\n" \
"s_movk_i32 s84, 0x6100\n" \
"v_mfma_f32_16x16x32_bf16 a[60:63], a[236:239], a[204:207], a[60:63]\n" \
"buffer_load_dwordx4 v25, s[76:79], s84 offen offset:3072 lds\n" \
"v_mfma_f32_16x16x32_bf16 a[56:59], a[168:171], a[140:143], a[56:59]\n" \
"ds_read_b64_tr_b16 a[184:185], v38 offset:32768\n" \
"v_accvgpr_read_b32 v182, a14\n" \
"v_mfma_f32_16x16x32_bf16 a[56:59], a[232:235], a[204:207], a[56:59]\n" \
"v_accvgpr_read_b32 v183, a15\n" \
"v_cvt_pk_bf16_f32 v188, v188, v189\n" \
"v_mfma_f32_16x16x32_bf16 a[52:55], a[164:167], a[140:143], a[52:55]\n" \
"s_add_u32 m0, s30, 0x21000\n" \
"s_addk_i32 s84, 0x3000\n" \
"v_mfma_f32_16x16x32_bf16 a[52:55], a[228:231], a[204:207], a[52:55]\n" \
"buffer_load_dwordx4 v24, s[76:79], s84 offen offset:0 lds\n" \
"v_mfma_f32_16x16x32_bf16 a[48:51], a[160:163], a[140:143], a[48:51]\n" \
"ds_read_b64_tr_b16 a[186:187], v38 offset:33792\n" \
"v_cvt_pk_bf16_f32 v189, v190, v191\n" \
"v_mfma_f32_16x16x32_bf16 a[48:51], a[224:227], a[204:207], a[48:51]\n" \
"v_cvt_pk_bf16_f32 v190, v180, v181\n" \
"v_cvt_pk_bf16_f32 v191, v182, v183\n" \
"v_mfma_f32_16x16x32_bf16 a[64:67], a[160:163], a[144:147], a[64:67]\n" \
"ds_read_b64_tr_b16 a[248:249], v38 offset:40960\n" \
"v_accvgpr_read_b32 v192, a24\n" \
"v_mfma_f32_16x16x32_bf16 a[64:67], a[224:227], a[208:211], a[64:67]\n" \
"s_addk_i32 s84, 0x2000\n" \
"s_nop 0\n" \
"v_mfma_f32_16x16x32_bf16 a[68:71], a[164:167], a[144:147], a[68:71]\n" \
"buffer_load_dwordx4 v25, s[76:79], s84 offen offset:1024 lds\n" \
"v_mfma_f32_16x16x32_bf16 a[68:71], a[228:231], a[208:211], a[68:71]\n" \
"ds_read_b64_tr_b16 a[250:251], v38 offset:41984\n" \
"v_accvgpr_read_b32 v193, a25\n" \
"v_mfma_f32_16x16x32_bf16 a[72:75], a[168:171], a[144:147], a[72:75]\n" \
"v_accvgpr_read_b32 v194, a26\n" \
"v_accvgpr_read_b32 v195, a27\n" \
"v_mfma_f32_16x16x32_bf16 a[72:75], a[232:235], a[208:211], a[72:75]\n" \
"v_accvgpr_read_b32 v180, a28\n" \
"v_accvgpr_read_b32 v181, a29\n" \
"v_mfma_f32_16x16x32_bf16 a[76:79], a[172:175], a[144:147], a[76:79]\n" \
"s_addk_i32 s84, 0x2000\n" \
"v_mfma_f32_16x16x32_bf16 a[76:79], a[236:239], a[208:211], a[76:79]\n" \
"buffer_load_dwordx4 v24, s[76:79], s84 offen offset:2048 lds\n" \
"v_mfma_f32_16x16x32_bf16 a[92:95], a[172:175], a[148:151], a[92:95]\n" \
"s_add_u32 s89, s41, 0x0\n" \
"buffer_store_dwordx4 v[184:187], v20, s[80:83], s89 offen offset:0 sc1\n" \
"v_mfma_f32_16x16x32_bf16 a[92:95], a[236:239], a[212:215], a[92:95]\n" \
"ds_read_b64_tr_b16 a[188:189], v39 offset:32768\n" \
"v_accvgpr_read_b32 v182, a30\n" \
"v_mfma_f32_16x16x32_bf16 a[88:91], a[168:171], a[148:151], a[88:91]\n" \
"ds_read_b64_tr_b16 a[190:191], v39 offset:33792\n" \
"v_accvgpr_read_b32 v183, a31\n" \
"v_mfma_f32_16x16x32_bf16 a[88:91], a[232:235], a[212:215], a[88:91]\n" \
"s_addk_i32 s84, 0x2000\n" \
"v_mfma_f32_16x16x32_bf16 a[84:87], a[164:167], a[148:151], a[84:87]\n" \
"buffer_load_dwordx4 v25, s[76:79], s84 offen offset:3072 lds\n" \
"v_mfma_f32_16x16x32_bf16 a[84:87], a[228:231], a[212:215], a[84:87]\n" \
"ds_read_b64_tr_b16 a[252:253], v39 offset:40960\n" \
"v_cvt_pk_bf16_f32 v192, v192, v193\n" \
"v_mfma_f32_16x16x32_bf16 a[80:83], a[160:163], a[148:151], a[80:83]\n" \
"s_add_u32 s88, s41, 0x0\n" \
"buffer_store_dwordx4 v[188:191], v20, s[80:83], s88 offen offset:1024 sc1\n" \
"v_mfma_f32_16x16x32_bf16 a[80:83], a[224:227], a[212:215], a[80:83]\n" \
"v_cvt_pk_bf16_f32 v193, v194, v195\n" \
"v_cvt_pk_bf16_f32 v194, v180, v181\n" \
"s_waitcnt lgkmcnt(15)\n" \
"s_nop 0\n" \
"v_mfma_f32_16x16x32_bf16 a[96:99], a[160:163], a[152:155], a[96:99]\n" \
"ds_read_b64_tr_b16 a[254:255], v39 offset:41984\n" \
"v_cvt_pk_bf16_f32 v195, v182, v183\n" \
"v_mfma_f32_16x16x32_bf16 a[96:99], a[224:227], a[216:219], a[96:99]\n" \
"v_accvgpr_read_b32 v196, a16\n" \
"v_accvgpr_read_b32 v197, a17\n" \
"v_mfma_f32_16x16x32_bf16 a[100:103], a[164:167], a[152:155], a[100:103]\n" \
"v_accvgpr_read_b32 v198, a18\n" \
"v_accvgpr_read_b32 v199, a19\n" \
"v_mfma_f32_16x16x32_bf16 a[100:103], a[228:231], a[216:219], a[100:103]\n" \
"v_accvgpr_read_b32 v180, a20\n" \
"v_accvgpr_read_b32 v181, a21\n" \
"v_mfma_f32_16x16x32_bf16 a[104:107], a[168:171], a[152:155], a[104:107]\n" \
"s_add_u32 s89, s41, 0x1000\n" \
"buffer_store_dwordx4 v[192:195], v20, s[80:83], s89 offen offset:1024 sc1\n" \
"v_mfma_f32_16x16x32_bf16 a[104:107], a[232:235], a[216:219], a[104:107]\n" \
"v_accvgpr_read_b32 v182, a22\n" \
"v_accvgpr_read_b32 v183, a23\n" \
"v_mfma_f32_16x16x32_bf16 a[108:111], a[172:175], a[152:155], a[108:111]\n" \
"v_cvt_pk_bf16_f32 v196, v196, v197\n" \
"v_cvt_pk_bf16_f32 v197, v198, v199\n" \
"v_mfma_f32_16x16x32_bf16 a[108:111], a[236:239], a[216:219], a[108:111]\n" \
"v_cvt_pk_bf16_f32 v198, v180, v181\n" \
"v_cvt_pk_bf16_f32 v199, v182, v183\n" \
"v_mfma_f32_16x16x32_bf16 a[124:127], a[172:175], a[156:159], a[124:127]\n" \
"v_accvgpr_read_b32 v200, a32\n" \
"v_accvgpr_read_b32 v201, a33\n" \
"v_mfma_f32_16x16x32_bf16 a[124:127], a[236:239], a[220:223], a[124:127]\n" \
"v_accvgpr_read_b32 v202, a34\n" \
"v_accvgpr_read_b32 v203, a35\n" \
"v_mfma_f32_16x16x32_bf16 a[120:123], a[168:171], a[156:159], a[120:123]\n" \
"s_add_u32 s88, s41, 0x1000\n" \
"buffer_store_dwordx4 v[196:199], v20, s[80:83], s88 offen offset:0 sc1\n" \
"v_mfma_f32_16x16x32_bf16 a[120:123], a[232:235], a[220:223], a[120:123]\n" \
"v_accvgpr_read_b32 v180, a36\n" \
"v_accvgpr_read_b32 v181, a37\n" \
"v_mfma_f32_16x16x32_bf16 a[116:119], a[164:167], a[156:159], a[116:119]\n" \
"v_accvgpr_read_b32 v182, a38\n" \
"v_accvgpr_read_b32 v183, a39\n" \
"v_mfma_f32_16x16x32_bf16 a[116:119], a[228:231], a[220:223], a[116:119]\n" \
"v_cvt_pk_bf16_f32 v200, v200, v201\n" \
"v_cvt_pk_bf16_f32 v201, v202, v203\n" \
"v_mfma_f32_16x16x32_bf16 a[112:115], a[160:163], a[156:159], a[112:115]\n" \
"v_cvt_pk_bf16_f32 v202, v180, v181\n" \
"v_cvt_pk_bf16_f32 v203, v182, v183\n" \
"v_mfma_f32_16x16x32_bf16 a[112:115], a[224:227], a[220:223], a[112:115]\n" \
"v_accvgpr_read_b32 v204, a40\n" \
"v_accvgpr_read_b32 v205, a41\n" \
"s_waitcnt vmcnt(12) lgkmcnt(0)\n" \
"s_barrier\n" \
"v_mov_b32_e32 v36, v32\n" \
"v_xor_b32_e32 v32, 0x0, v48\n" \
"v_mov_b32_e32 v37, v33\n" \
"v_xor_b32_e32 v33, 0x20, v48\n" \
"v_mov_b32_e32 v38, v34\n" \
"v_xor_b32_e32 v34, 0x40, v48\n" \
"v_mov_b32_e64 v39, v35\n" \
"v_xor_b32_e32 v35, 0x60, v48\n" \
"v_add_u32_e32 v22, s44, v28\n" \
"v_add_u32_e32 v28, s43, v28\n" \
"v_add_u32_e32 v23, s44, v29\n" \
"v_add_u32_e32 v29, s43, v29\n" \
"v_mfma_f32_16x16x32_bf16 v[52:55], a[176:179], a[128:131], v[52:55]\n" \
"s_add_u32 m0, s40, 0x8000\n" \
"s_add_u32 s85, s39, 0x2000\n" \
"v_mfma_f32_16x16x32_bf16 v[52:55], a[240:243], a[192:195], v[52:55]\n" \
"buffer_load_dwordx4 v20, s[80:83], s85 offen sc1 lds\n" \
"v_mfma_f32_16x16x32_bf16 v[56:59], a[180:183], a[128:131], v[56:59]\n" \
"ds_read_b64_tr_b16 a[160:161], v32 offset:0\n" \
"v_accvgpr_read_b32 v206, a42\n" \
"v_mfma_f32_16x16x32_bf16 v[56:59], a[244:247], a[192:195], v[56:59]\n" \
"s_add_u32 m0, s40, 0x9000\n" \
"s_add_u32 s85, s39, 0x3000\n" \
"v_mfma_f32_16x16x32_bf16 v[60:63], a[184:187], a[128:131], v[60:63]\n" \
"buffer_load_dwordx4 v20, s[80:83], s85 offen sc1 lds\n" \
"v_mfma_f32_16x16x32_bf16 v[60:63], a[248:251], a[192:195], v[60:63]\n" \
"ds_read_b64_tr_b16 a[162:163], v32 offset:512\n" \
"v_accvgpr_read_b32 v207, a43\n" \
"v_mfma_f32_16x16x32_bf16 v[64:67], a[188:191], a[128:131], v[64:67]\n" \
"ds_read_b64_tr_b16 a[224:225], v32 offset:4096\n" \
"v_accvgpr_read_b32 v180, a44\n" \
"v_mfma_f32_16x16x32_bf16 v[64:67], a[252:255], a[192:195], v[64:67]\n" \
"ds_read_b128 a[128:131], v28 offset:0\n" \
"v_accvgpr_read_b32 v181, a45\n" \
"v_mfma_f32_16x16x32_bf16 v[80:83], a[188:191], a[132:135], v[80:83]\n" \
"ds_read_b128 a[192:195], v29 offset:0\n" \
"v_accvgpr_read_b32 v182, a46\n" \
"v_mfma_f32_16x16x32_bf16 v[80:83], a[252:255], a[196:199], v[80:83]\n" \
"s_add_u32 m0, s40, 0xb000\n" \
"s_movk_i32 s84, 0x80\n" \
"v_mfma_f32_16x16x32_bf16 v[76:79], a[184:187], a[132:135], v[76:79]\n" \
"buffer_load_dwordx4 v21, s[76:79], s84 offen offset:0 lds\n" \
"v_mfma_f32_16x16x32_bf16 v[76:79], a[248:251], a[196:199], v[76:79]\n" \
"s_add_u32 m0, s30, 0x8000\n" \
"s_movk_i32 s84, 0x80\n" \
"v_mfma_f32_16x16x32_bf16 v[72:75], a[180:183], a[132:135], v[72:75]\n" \
"buffer_load_dwordx4 v24, s[76:79], s84 offen offset:0 lds\n" \
"v_mfma_f32_16x16x32_bf16 v[72:75], a[244:247], a[196:199], v[72:75]\n" \
"ds_read_b64_tr_b16 a[226:227], v32 offset:4608\n" \
"v_accvgpr_read_b32 v183, a47\n" \
"v_mfma_f32_16x16x32_bf16 v[68:71], a[176:179], a[132:135], v[68:71]\n" \
"ds_read_b64_tr_b16 a[164:165], v33 offset:0\n" \
"v_cvt_pk_bf16_f32 v204, v204, v205\n" \
"v_mfma_f32_16x16x32_bf16 v[68:71], a[240:243], a[196:199], v[68:71]\n" \
"ds_read_b128 a[132:135], v28 offset:2048\n" \
"v_cvt_pk_bf16_f32 v205, v206, v207\n" \
"v_mfma_f32_16x16x32_bf16 v[84:87], a[176:179], a[136:139], v[84:87]\n" \
"ds_read_b128 a[196:199], v29 offset:2048\n" \
"v_cvt_pk_bf16_f32 v206, v180, v181\n" \
"v_mfma_f32_16x16x32_bf16 v[84:87], a[240:243], a[200:203], v[84:87]\n" \
"s_movk_i32 s84, 0x2080\n" \
"v_mfma_f32_16x16x32_bf16 v[88:91], a[180:183], a[136:139], v[88:91]\n" \
"buffer_load_dwordx4 v25, s[76:79], s84 offen offset:1024 lds\n" \
"v_mfma_f32_16x16x32_bf16 v[88:91], a[244:247], a[200:203], v[88:91]\n" \
"s_movk_i32 s84, 0x4080\n" \
"v_mfma_f32_16x16x32_bf16 v[92:95], a[184:187], a[136:139], v[92:95]\n" \
"buffer_load_dwordx4 v24, s[76:79], s84 offen offset:2048 lds\n" \
"v_mfma_f32_16x16x32_bf16 v[92:95], a[248:251], a[200:203], v[92:95]\n" \
"ds_read_b64_tr_b16 a[166:167], v33 offset:512\n" \
"v_cvt_pk_bf16_f32 v207, v182, v183\n" \
"v_mfma_f32_16x16x32_bf16 v[96:99], a[188:191], a[136:139], v[96:99]\n" \
"ds_read_b64_tr_b16 a[228:229], v33 offset:4096\n" \
"v_accvgpr_read_b32 v208, a56\n" \
"v_mfma_f32_16x16x32_bf16 v[96:99], a[252:255], a[200:203], v[96:99]\n" \
"ds_read_b128 a[136:139], v28 offset:4096\n" \
"v_accvgpr_read_b32 v209, a57\n" \
"v_mfma_f32_16x16x32_bf16 v[112:115], a[188:191], a[140:143], v[112:115]\n" \
"ds_read_b128 a[200:203], v29 offset:4096\n" \
"v_accvgpr_read_b32 v210, a58\n" \
"v_mfma_f32_16x16x32_bf16 v[112:115], a[252:255], a[204:207], v[112:115]\n" \
"s_movk_i32 s84, 0x6080\n" \
"v_mfma_f32_16x16x32_bf16 v[108:111], a[184:187], a[140:143], v[108:111]\n" \
"buffer_load_dwordx4 v25, s[76:79], s84 offen offset:3072 lds\n" \
"v_mfma_f32_16x16x32_bf16 v[108:111], a[248:251], a[204:207], v[108:111]\n" \
"s_add_u32 m0, s31, 0x8000\n" \
"s_mov_b32 s84, 0x80000\n" \
"v_mfma_f32_16x16x32_bf16 v[104:107], a[180:183], a[140:143], v[104:107]\n" \
"buffer_load_dwordx4 v26, s[68:71], s84 offen offset:0 lds\n" \
"v_mfma_f32_16x16x32_bf16 v[104:107], a[244:247], a[204:207], v[104:107]\n" \
"s_addk_i32 s84, 0x7c00\n" \
"v_mfma_f32_16x16x32_bf16 v[100:103], a[176:179], a[140:143], v[100:103]\n" \
"buffer_load_dwordx4 v26, s[68:71], s84 offen offset:1024 lds\n" \
"v_mfma_f32_16x16x32_bf16 v[100:103], a[240:243], a[204:207], v[100:103]\n" \
"ds_read_b128 a[140:143], v28 offset:6144\n" \
"v_accvgpr_read_b32 v211, a59\n" \
"v_mfma_f32_16x16x32_bf16 v[116:119], a[176:179], a[144:147], v[116:119]\n" \
"ds_read_b128 a[204:207], v29 offset:6144\n" \
"v_accvgpr_read_b32 v180, a60\n" \
"v_mfma_f32_16x16x32_bf16 v[116:119], a[240:243], a[208:211], v[116:119]\n" \
"s_addk_i32 s84, 0x7c00\n" \
"v_mfma_f32_16x16x32_bf16 v[120:123], a[180:183], a[144:147], v[120:123]\n" \
"buffer_load_dwordx4 v27, s[68:71], s84 offen offset:2048 lds\n" \
"v_mfma_f32_16x16x32_bf16 v[120:123], a[244:247], a[208:211], v[120:123]\n" \
"ds_read_b64_tr_b16 a[230:231], v33 offset:4608\n" \
"v_accvgpr_read_b32 v181, a61\n" \
"v_mfma_f32_16x16x32_bf16 v[124:127], a[184:187], a[144:147], v[124:127]\n" \
"s_addk_i32 s84, 0x7c00\n" \
"v_mfma_f32_16x16x32_bf16 v[124:127], a[248:251], a[208:211], v[124:127]\n" \
"buffer_load_dwordx4 v27, s[68:71], s84 offen offset:3072 lds\n" \
"v_mfma_f32_16x16x32_bf16 v[128:131], a[188:191], a[144:147], v[128:131]\n" \
"ds_read_b64_tr_b16 a[172:173], v35 offset:0\n" \
"v_accvgpr_read_b32 v182, a62\n" \
"v_mfma_f32_16x16x32_bf16 v[128:131], a[252:255], a[208:211], v[128:131]\n" \
"ds_read_b128 a[144:147], v28 offset:8192\n" \
"v_accvgpr_read_b32 v183, a63\n" \
"v_mfma_f32_16x16x32_bf16 v[144:147], a[188:191], a[148:151], v[144:147]\n" \
"ds_read_b128 a[208:211], v29 offset:8192\n" \
"v_cvt_pk_bf16_f32 v208, v208, v209\n" \
"v_mfma_f32_16x16x32_bf16 v[144:147], a[252:255], a[212:215], v[144:147]\n" \
"s_add_u32 m0, s30, 0x9000\n" \
"s_mov_b32 s84, 0x9080\n" \
"v_mfma_f32_16x16x32_bf16 v[140:143], a[184:187], a[148:151], v[140:143]\n" \
"buffer_load_dwordx4 v24, s[76:79], s84 offen offset:0 lds\n" \
"v_mfma_f32_16x16x32_bf16 v[140:143], a[248:251], a[212:215], v[140:143]\n" \
"ds_read_b64_tr_b16 a[174:175], v35 offset:512\n" \
"v_cvt_pk_bf16_f32 v209, v210, v211\n" \
"v_mfma_f32_16x16x32_bf16 v[136:139], a[180:183], a[148:151], v[136:139]\n" \
"s_addk_i32 s84, 0x2000\n" \
"s_nop 0\n" \
"v_mfma_f32_16x16x32_bf16 v[136:139], a[244:247], a[212:215], v[136:139]\n" \
"buffer_load_dwordx4 v25, s[76:79], s84 offen offset:1024 lds\n" \
"v_mfma_f32_16x16x32_bf16 v[132:135], a[176:179], a[148:151], v[132:135]\n" \
"ds_read_b64_tr_b16 a[236:237], v35 offset:4096\n" \
"v_cvt_pk_bf16_f32 v210, v180, v181\n" \
"v_mfma_f32_16x16x32_bf16 v[132:135], a[240:243], a[212:215], v[132:135]\n" \
"ds_read_b128 a[148:151], v28 offset:10240\n" \
"v_cvt_pk_bf16_f32 v211, v182, v183\n" \
"v_mfma_f32_16x16x32_bf16 v[148:151], a[176:179], a[152:155], v[148:151]\n" \
"ds_read_b128 a[212:215], v29 offset:10240\n" \
"v_accvgpr_read_b32 v212, a48\n" \
"v_mfma_f32_16x16x32_bf16 v[148:151], a[240:243], a[216:219], v[148:151]\n" \
"ds_read_b64_tr_b16 a[238:239], v35 offset:4608\n" \
"v_accvgpr_read_b32 v213, a49\n" \
"v_mfma_f32_16x16x32_bf16 v[152:155], a[180:183], a[152:155], v[152:155]\n" \
"ds_read_b64_tr_b16 a[168:169], v34 offset:0\n" \
"v_accvgpr_read_b32 v214, a50\n" \
"v_mfma_f32_16x16x32_bf16 v[152:155], a[244:247], a[216:219], v[152:155]\n" \
"ds_read_b64_tr_b16 a[170:171], v34 offset:512\n" \
"v_accvgpr_read_b32 v215, a51\n" \
"v_mfma_f32_16x16x32_bf16 v[156:159], a[184:187], a[152:155], v[156:159]\n" \
"ds_read_b64_tr_b16 a[232:233], v34 offset:4096\n" \
"v_accvgpr_read_b32 v180, a52\n" \
"v_mfma_f32_16x16x32_bf16 v[156:159], a[248:251], a[216:219], v[156:159]\n" \
"ds_read_b64_tr_b16 a[234:235], v34 offset:4608\n" \
"v_accvgpr_read_b32 v181, a53\n" \
"v_mfma_f32_16x16x32_bf16 v[160:163], a[188:191], a[152:155], v[160:163]\n" \
"ds_read_b128 v[248:251], v22 offset:0\n" \
"v_accvgpr_read_b32 v182, a54\n" \
"v_mfma_f32_16x16x32_bf16 v[160:163], a[252:255], a[216:219], v[160:163]\n" \
"ds_read_b128 a[152:155], v28 offset:12288\n" \
"v_accvgpr_read_b32 v183, a55\n" \
"v_mfma_f32_16x16x32_bf16 v[176:179], a[188:191], a[156:159], v[176:179]\n" \
"ds_read_b128 a[216:219], v29 offset:12288\n" \
"v_cvt_pk_bf16_f32 v212, v212, v213\n" \
"v_mfma_f32_16x16x32_bf16 v[176:179], a[252:255], a[220:223], v[176:179]\n" \
"ds_read_b128 v[48:51], v23 offset:0\n" \
"v_cvt_pk_bf16_f32 v213, v214, v215\n" \
"v_mfma_f32_16x16x32_bf16 v[172:175], a[184:187], a[156:159], v[172:175]\n" \
"v_cvt_pk_bf16_f32 v214, v180, v181\n" \
"v_cvt_pk_bf16_f32 v215, v182, v183\n" \
"v_mfma_f32_16x16x32_bf16 v[172:175], a[248:251], a[220:223], v[172:175]\n" \
"v_accvgpr_read_b32 v216, a64\n" \
"v_accvgpr_read_b32 v217, a65\n" \
"v_mfma_f32_16x16x32_bf16 v[168:171], a[180:183], a[156:159], v[168:171]\n" \
"v_accvgpr_read_b32 v218, a66\n" \
"v_accvgpr_read_b32 v219, a67\n" \
"v_mfma_f32_16x16x32_bf16 v[168:171], a[244:247], a[220:223], v[168:171]\n" \
"v_accvgpr_read_b32 v180, a68\n" \
"v_accvgpr_read_b32 v181, a69\n" \
"v_mfma_f32_16x16x32_bf16 v[164:167], a[176:179], a[156:159], v[164:167]\n" \
"v_accvgpr_read_b32 v182, a70\n" \
"v_accvgpr_read_b32 v183, a71\n" \
"v_mfma_f32_16x16x32_bf16 v[164:167], a[240:243], a[220:223], v[164:167]\n" \
"ds_read_b128 a[156:159], v28 offset:14336\n" \
"ds_read_b128 a[220:223], v29 offset:14336\n" \
"s_waitcnt lgkmcnt(15)\n" \
"v_mfma_f32_16x16x32_bf16 a[0:3], a[160:163], a[128:131], 0\n" \
"s_addk_i32 s84, 0x2000\n" \
"v_cvt_pk_bf16_f32 v216, v216, v217\n" \
"v_mfma_f32_16x16x32_bf16 a[0:3], a[224:227], a[192:195], a[0:3]\n" \
"buffer_load_dwordx4 v24, s[76:79], s84 offen offset:2048 lds\n" \
"v_cvt_pk_bf16_f32 v217, v218, v219\n" \
"v_mfma_f32_16x16x32_bf16 a[4:7], a[164:167], a[128:131], 0\n" \
"ds_read_b64_tr_b16 a[176:177], v36 offset:0\n" \
"v_cvt_pk_bf16_f32 v218, v180, v181\n" \
"v_mfma_f32_16x16x32_bf16 a[4:7], a[228:231], a[192:195], a[4:7]\n" \
"s_addk_i32 s84, 0x2000\n" \
"v_cvt_pk_bf16_f32 v219, v182, v183\n" \
"s_waitcnt lgkmcnt(15)\n" \
"v_mfma_f32_16x16x32_bf16 a[12:15], a[172:175], a[128:131], 0\n" \
"buffer_load_dwordx4 v25, s[76:79], s84 offen offset:3072 lds\n" \
"v_accvgpr_read_b32 v220, a80\n" \
"s_waitcnt lgkmcnt(11)\n" \
"v_mfma_f32_16x16x32_bf16 a[12:15], a[236:239], a[192:195], a[12:15]\n" \
"ds_read_b64_tr_b16 a[178:179], v36 offset:1024\n" \
"v_accvgpr_read_b32 v221, a81\n" \
"s_waitcnt lgkmcnt(10)\n" \
"v_mfma_f32_16x16x32_bf16 a[8:11], a[128:131], a[168:171], 0\n" \
"ds_read_b64_tr_b16 a[240:241], v36 offset:8192\n" \
"v_accvgpr_read_b32 v222, a82\n" \
"s_waitcnt lgkmcnt(9)\n" \
"s_nop 0\n" \
"v_mfma_f32_16x16x32_bf16 a[8:11], a[192:195], a[232:235], a[8:11]\n" \
"ds_read_b64_tr_b16 a[242:243], v36 offset:9216\n" \
"v_accvgpr_read_b32 v223, a83\n" \
"v_mfma_f32_16x16x32_bf16 a[24:27], a[132:135], a[168:171], 0\n" \
"ds_read_b64_tr_b16 a[180:181], v37 offset:0\n" \
"v_accvgpr_read_b32 v180, a84\n" \
"v_mfma_f32_16x16x32_bf16 a[24:27], a[196:199], a[232:235], a[24:27]\n" \
"ds_read_b64_tr_b16 a[182:183], v37 offset:1024\n" \
"v_accvgpr_read_b32 v181, a85\n" \
"v_mfma_f32_16x16x32_bf16 a[28:31], a[172:175], a[132:135], 0\n" \
"ds_read_b64_tr_b16 a[244:245], v37 offset:8192\n" \
"v_accvgpr_read_b32 v182, a86\n" \
"v_mfma_f32_16x16x32_bf16 a[28:31], a[236:239], a[196:199], a[28:31]\n" \
"s_add_u32 s89, s41, 0x2000\n" \
"buffer_store_dwordx4 v[200:203], v20, s[80:83], s89 offen offset:0 sc1\n" \
"v_mfma_f32_16x16x32_bf16 a[20:23], a[164:167], a[132:135], 0\n" \
"ds_read_b64_tr_b16 a[246:247], v37 offset:9216\n" \
"v_accvgpr_read_b32 v183, a87\n" \
"v_mfma_f32_16x16x32_bf16 a[20:23], a[228:231], a[196:199], a[20:23]\n" \
"ds_read_b64_tr_b16 a[184:185], v38 offset:0\n" \
"v_cvt_pk_bf16_f32 v220, v220, v221\n" \
"v_mfma_f32_16x16x32_bf16 a[16:19], a[160:163], a[132:135], 0\n" \
"ds_read_b64_tr_b16 a[186:187], v38 offset:1024\n" \
"v_cvt_pk_bf16_f32 v221, v222, v223\n" \
"v_mfma_f32_16x16x32_bf16 a[16:19], a[224:227], a[196:199], a[16:19]\n" \
"v_cvt_pk_bf16_f32 v222, v180, v181\n" \
"v_cvt_pk_bf16_f32 v223, v182, v183\n" \
"v_cvt_pk_bf16_f32 v224, v52, v53\n" \
"v_mfma_f32_16x16x32_bf16 a[32:35], a[160:163], a[136:139], 0\n" \
"s_add_u32 s88, s41, 0x2000\n" \
"buffer_store_dwordx4 v[204:207], v20, s[80:83], s88 offen offset:1024 sc1\n" \
".fill (L_pb + 20192 - .) / 4, 4, 0xBF800000\n" \
"L_pp5:\n" \
"s_cbranch_execz L_pret\n" \
"s_nop 0\n" \
"v_mfma_f32_16x16x32_bf16 a[32:35], a[224:227], a[200:203], a[32:35]\n" \
"ds_read_b64_tr_b16 a[248:249], v38 offset:8192\n" \
"v_cvt_pk_bf16_f32 v225, v54, v55\n" \
"v_mfma_f32_16x16x32_bf16 a[36:39], a[164:167], a[136:139], 0\n" \
"ds_read_b64_tr_b16 a[250:251], v38 offset:9216\n" \
"v_cvt_pk_bf16_f32 v226, v56, v57\n" \
"v_mfma_f32_16x16x32_bf16 a[36:39], a[228:231], a[200:203], a[36:39]\n" \
"ds_read_b64_tr_b16 a[188:189], v39 offset:0\n" \
"v_cvt_pk_bf16_f32 v227, v58, v59\n" \
"v_mfma_f32_16x16x32_bf16 a[44:47], a[172:175], a[136:139], 0\n" \
"v_cvt_pk_bf16_f32 v228, v60, v61\n" \
"v_cvt_pk_bf16_f32 v229, v62, v63\n" \
"v_cvt_pk_bf16_f32 v230, v64, v65\n" \
"v_mfma_f32_16x16x32_bf16 a[44:47], a[236:239], a[200:203], a[44:47]\n" \
"ds_read_b64_tr_b16 a[190:191], v39 offset:1024\n" \
"v_cvt_pk_bf16_f32 v231, v66, v67\n" \
"v_mfma_f32_16x16x32_bf16 a[40:43], a[136:139], a[168:171], 0\n" \
"s_add_u32 s89, s41, 0x3000\n" \
"buffer_store_dwordx4 v[208:211], v20, s[80:83], s89 offen offset:1024 sc1\n" \
"v_mfma_f32_16x16x32_bf16 a[40:43], a[200:203], a[232:235], a[40:43]\n" \
"ds_read_b64_tr_b16 a[252:253], v39 offset:8192\n" \
"v_cvt_pk_bf16_f32 v232, v76, v77\n" \
"v_mfma_f32_16x16x32_bf16 a[56:59], a[140:143], a[168:171], 0\n" \
"ds_read_b64_tr_b16 a[254:255], v39 offset:9216\n" \
"v_cvt_pk_bf16_f32 v233, v78, v79\n" \
"v_mfma_f32_16x16x32_bf16 a[56:59], a[204:207], a[232:235], a[56:59]\n" \
"v_cvt_pk_bf16_f32 v234, v80, v81\n" \
"v_cvt_pk_bf16_f32 v235, v82, v83\n" \
"v_cvt_pk_bf16_f32 v236, v68, v69\n" \
"v_mfma_f32_16x16x32_bf16 a[60:63], a[172:175], a[140:143], 0\n" \
"v_cvt_pk_bf16_f32 v237, v70, v71\n" \
"v_cvt_pk_bf16_f32 v238, v72, v73\n" \
"v_cvt_pk_bf16_f32 v239, v74, v75\n" \
"v_mfma_f32_16x16x32_bf16 a[60:63], a[236:239], a[204:207], a[60:63]\n" \
"s_add_u32 s88, s41, 0x3000\n" \
"buffer_store_dwordx4 v[212:215], v20, s[80:83], s88 offen offset:0 sc1\n" \
"v_mfma_f32_16x16x32_bf16 a[52:55], a[164:167], a[140:143], 0\n" \
"v_cvt_pk_bf16_f32 v240, v84, v85\n" \
"v_cvt_pk_bf16_f32 v241, v86, v87\n" \
"v_cvt_pk_bf16_f32 v242, v88, v89\n" \
"v_mfma_f32_16x16x32_bf16 a[52:55], a[228:231], a[204:207], a[52:55]\n" \
"v_cvt_pk_bf16_f32 v243, v90, v91\n" \
"v_cvt_pk_bf16_f32 v244, v92, v93\n" \
"v_cvt_pk_bf16_f32 v245, v94, v95\n" \
"v_mfma_f32_16x16x32_bf16 a[48:51], a[160:163], a[140:143], 0\n" \
"v_cvt_pk_bf16_f32 v246, v96, v97\n" \
"v_cvt_pk_bf16_f32 v247, v98, v99\n" \
"v_cvt_pk_bf16_f32 v184, v108, v109\n" \
"v_mfma_f32_16x16x32_bf16 a[48:51], a[224:227], a[204:207], a[48:51]\n" \
"v_cvt_pk_bf16_f32 v185, v110, v111\n" \
"v_cvt_pk_bf16_f32 v186, v112, v113\n" \
"v_cvt_pk_bf16_f32 v187, v114, v115\n" \
"s_waitcnt lgkmcnt(15)\n" \
"s_nop 0\n" \
"v_mfma_f32_16x16x32_bf16 a[64:67], a[164:167], v[248:251], 0\n" \
"v_cvt_pk_bf16_f32 v188, v100, v101\n" \
"v_cvt_pk_bf16_f32 v189, v102, v103\n" \
"v_cvt_pk_bf16_f32 v190, v104, v105\n" \
"v_mfma_f32_16x16x32_bf16 a[64:67], a[228:231], v[48:51], a[64:67]\n" \
"v_cvt_pk_bf16_f32 v191, v106, v107\n" \
"v_cvt_pk_bf16_f32 v192, v116, v117\n" \
"v_cvt_pk_bf16_f32 v193, v118, v119\n" \
"v_mfma_f32_16x16x32_bf16 a[80:83], a[172:175], v[248:251], 0\n" \
"v_cvt_pk_bf16_f32 v194, v120, v121\n" \
"v_cvt_pk_bf16_f32 v195, v122, v123\n" \
"v_cvt_pk_bf16_f32 v196, v124, v125\n" \
"v_mfma_f32_16x16x32_bf16 a[80:83], a[236:239], v[48:51], a[80:83]\n" \
"v_cvt_pk_bf16_f32 v197, v126, v127\n" \
"v_cvt_pk_bf16_f32 v198, v128, v129\n" \
"v_cvt_pk_bf16_f32 v199, v130, v131\n" \
"v_mfma_f32_16x16x32_bf16 a[68:71], v[248:251], a[168:171], 0\n" \
"v_cvt_pk_bf16_f32 v252, v140, v141\n" \
"v_cvt_pk_bf16_f32 v253, v142, v143\n" \
"v_cvt_pk_bf16_f32 v254, v144, v145\n" \
"v_mfma_f32_16x16x32_bf16 a[68:71], v[48:51], a[232:235], a[68:71]\n" \
"v_cvt_pk_bf16_f32 v255, v146, v147\n" \
"v_accvgpr_write_b32 a84, v252\n" \
"v_accvgpr_write_b32 a85, v253\n" \
"s_waitcnt vmcnt(4) lgkmcnt(0)\n" \
"s_barrier\n" \
"v_mfma_f32_16x16x32_bf16 v[52:55], a[176:179], a[128:131], 0\n" \
"s_add_u32 m0, s40, 0x0\n" \
"s_nop 0\n" \
"s_add_u32 s85, s39, 0x4000\n" \
"v_accvgpr_write_b32 a86, v254\n" \
"v_mfma_f32_16x16x32_bf16 v[52:55], a[240:243], a[192:195], v[52:55]\n" \
"buffer_load_dwordx4 v20, s[80:83], s85 offen sc1 lds\n" \
"v_accvgpr_write_b32 a87, v255\n" \
"v_mfma_f32_16x16x32_bf16 v[56:59], a[180:183], a[128:131], 0\n" \
"ds_read_b64_tr_b16 a[160:161], v32 offset:32768\n" \
"v_cvt_pk_bf16_f32 v252, v132, v133\n" \
"v_mfma_f32_16x16x32_bf16 v[56:59], a[244:247], a[192:195], v[56:59]\n" \
"ds_read_b64_tr_b16 a[162:163], v32 offset:33280\n" \
"v_cvt_pk_bf16_f32 v253, v134, v135\n" \
"v_mfma_f32_16x16x32_bf16 v[60:63], a[184:187], a[128:131], 0\n" \
"s_add_u32 m0, s40, 0x1000\n" \
"s_add_u32 s85, s39, 0x5000\n" \
"v_cvt_pk_bf16_f32 v254, v136, v137\n" \
"v_mfma_f32_16x16x32_bf16 v[60:63], a[248:251], a[192:195], v[60:63]\n" \
"buffer_load_dwordx4 v20, s[80:83], s85 offen sc1 lds\n" \
"v_cvt_pk_bf16_f32 v255, v138, v139\n" \
"v_mfma_f32_16x16x32_bf16 v[64:67], a[188:191], a[128:131], 0\n" \
"ds_read_b64_tr_b16 a[224:225], v32 offset:36864\n" \
"v_cvt_pk_bf16_f32 v200, v148, v149\n" \
"v_mfma_f32_16x16x32_bf16 v[64:67], a[252:255], a[192:195], v[64:67]\n" \
"ds_read_b128 a[128:131], v28 offset:32768\n" \
"v_cvt_pk_bf16_f32 v201, v150, v151\n" \
"v_mfma_f32_16x16x32_bf16 v[80:83], a[188:191], a[132:135], 0\n" \
"ds_read_b128 a[192:195], v29 offset:32768\n" \
"v_cvt_pk_bf16_f32 v202, v152, v153\n" \
"v_mfma_f32_16x16x32_bf16 v[80:83], a[252:255], a[196:199], v[80:83]\n" \
"s_add_u32 m0, s40, 0x3000\n" \
"s_movk_i32 s84, 0x100\n" \
"s_nop 0\n" \
"v_cvt_pk_bf16_f32 v203, v154, v155\n" \
"v_mfma_f32_16x16x32_bf16 v[76:79], a[184:187], a[132:135], 0\n" \
"buffer_load_dwordx4 v21, s[76:79], s84 offen offset:0 lds\n" \
"v_cvt_pk_bf16_f32 v204, v156, v157\n" \
"v_mfma_f32_16x16x32_bf16 v[76:79], a[248:251], a[196:199], v[76:79]\n" \
"ds_read_b64_tr_b16 a[226:227], v32 offset:37376\n" \
"v_cvt_pk_bf16_f32 v205, v158, v159\n" \
"v_mfma_f32_16x16x32_bf16 v[72:75], a[180:183], a[132:135], 0\n" \
"ds_read_b64_tr_b16 a[164:165], v33 offset:32768\n" \
"v_cvt_pk_bf16_f32 v206, v160, v161\n" \
"v_mfma_f32_16x16x32_bf16 v[72:75], a[244:247], a[196:199], v[72:75]\n" \
"v_cvt_pk_bf16_f32 v207, v162, v163\n" \
"v_cvt_pk_bf16_f32 v208, v172, v173\n" \
"v_cvt_pk_bf16_f32 v209, v174, v175\n" \
"v_mfma_f32_16x16x32_bf16 v[68:71], a[176:179], a[132:135], 0\n" \
"ds_read_b64_tr_b16 a[166:167], v33 offset:33280\n" \
"v_cvt_pk_bf16_f32 v210, v176, v177\n" \
"v_mfma_f32_16x16x32_bf16 v[68:71], a[240:243], a[196:199], v[68:71]\n" \
"ds_read_b128 a[132:135], v28 offset:34816\n" \
"v_cvt_pk_bf16_f32 v211, v178, v179\n" \
"v_mfma_f32_16x16x32_bf16 v[84:87], a[176:179], a[136:139], 0\n" \
"ds_read_b128 a[196:199], v29 offset:34816\n" \
"v_cvt_pk_bf16_f32 v212, v164, v165\n" \
"v_mfma_f32_16x16x32_bf16 v[84:87], a[240:243], a[200:203], v[84:87]\n" \
"s_add_u32 m0, s31, 0x0\n" \
"s_mov_b32 s84, 0x100000\n" \
"v_cvt_pk_bf16_f32 v213, v166, v167\n" \
"v_mfma_f32_16x16x32_bf16 v[88:91], a[180:183], a[136:139], 0\n" \
"buffer_load_dwordx4 v26, s[68:71], s84 offen offset:0 lds\n" \
"v_cvt_pk_bf16_f32 v214, v168, v169\n" \
"v_mfma_f32_16x16x32_bf16 v[88:91], a[244:247], a[200:203], v[88:91]\n" \
"s_add_u32 s89, s41, 0x0\n" \
"buffer_store_dwordx4 v[224:227], v20, s[80:83], s89 offen offset:2048 sc1\n" \
"v_mfma_f32_16x16x32_bf16 v[92:95], a[184:187], a[136:139], 0\n" \
"ds_read_b64_tr_b16 a[228:229], v33 offset:36864\n" \
"v_cvt_pk_bf16_f32 v215, v170, v171\n" \
"v_mfma_f32_16x16x32_bf16 v[92:95], a[248:251], a[200:203], v[92:95]\n" \
"ds_read_b64_tr_b16 a[230:231], v33 offset:37376\n" \
"v_mfma_f32_16x16x32_bf16 v[96:99], a[188:191], a[136:139], 0\n" \
"v_mfma_f32_16x16x32_bf16 v[96:99], a[252:255], a[200:203], v[96:99]\n" \
"ds_read_b128 a[136:139], v28 offset:36864\n" \
"v_mfma_f32_16x16x32_bf16 v[112:115], a[188:191], a[140:143], 0\n" \
"ds_read_b128 a[200:203], v29 offset:36864\n" \
"v_mfma_f32_16x16x32_bf16 v[112:115], a[252:255], a[204:207], v[112:115]\n" \
"s_addk_i32 s84, 0x7c00\n" \
"v_mfma_f32_16x16x32_bf16 v[108:111], a[184:187], a[140:143], 0\n" \
"buffer_load_dwordx4 v26, s[68:71], s84 offen offset:1024 lds\n" \
"v_mfma_f32_16x16x32_bf16 v[108:111], a[248:251], a[204:207], v[108:111]\n" \
"ds_read_b64_tr_b16 a[172:173], v35 offset:32768\n" \
"v_mfma_f32_16x16x32_bf16 v[104:107], a[180:183], a[140:143], 0\n" \
"s_addk_i32 s84, 0x7c00\n" \
"v_mfma_f32_16x16x32_bf16 v[104:107], a[244:247], a[204:207], v[104:107]\n" \
"buffer_load_dwordx4 v27, s[68:71], s84 offen offset:2048 lds\n" \
"v_mfma_f32_16x16x32_bf16 v[100:103], a[176:179], a[140:143], 0\n" \
"ds_read_b64_tr_b16 a[174:175], v35 offset:33280\n" \
"v_mfma_f32_16x16x32_bf16 v[100:103], a[240:243], a[204:207], v[100:103]\n" \
"ds_read_b128 a[140:143], v28 offset:38912\n" \
"v_mfma_f32_16x16x32_bf16 v[116:119], a[176:179], a[144:147], 0\n" \
"ds_read_b128 a[204:207], v29 offset:38912\n" \
"v_mfma_f32_16x16x32_bf16 v[116:119], a[240:243], a[208:211], v[116:119]\n" \
"s_addk_i32 s84, 0x7c00\n" \
"v_mfma_f32_16x16x32_bf16 v[120:123], a[180:183], a[144:147], 0\n" \
"buffer_load_dwordx4 v27, s[68:71], s84 offen offset:3072 lds\n" \
"v_mfma_f32_16x16x32_bf16 v[120:123], a[244:247], a[208:211], v[120:123]\n" \
"ds_read_b64_tr_b16 a[236:237], v35 offset:36864\n" \
"v_mfma_f32_16x16x32_bf16 v[124:127], a[184:187], a[144:147], 0\n" \
"ds_read_b64_tr_b16 a[238:239], v35 offset:37376\n" \
"v_mfma_f32_16x16x32_bf16 v[124:127], a[248:251], a[208:211], v[124:127]\n" \
"s_add_u32 m0, s30, 0x0\n" \
"s_movk_i32 s84, 0x180\n" \
"v_mfma_f32_16x16x32_bf16 v[128:131], a[188:191], a[144:147], 0\n" \
"buffer_load_dwordx4 v24, s[76:79], s84 offen offset:0 lds\n" \
"v_mfma_f32_16x16x32_bf16 v[128:131], a[252:255], a[208:211], v[128:131]\n" \
"ds_read_b128 a[144:147], v28 offset:40960\n" \
"v_mfma_f32_16x16x32_bf16 v[144:147], a[188:191], a[148:151], 0\n" \
"ds_read_b128 a[208:211], v29 offset:40960\n" \
"v_mfma_f32_16x16x32_bf16 v[144:147], a[252:255], a[212:215], v[144:147]\n" \
"ds_read_b64_tr_b16 a[168:169], v34 offset:32768\n" \
"v_mfma_f32_16x16x32_bf16 v[140:143], a[184:187], a[148:151], 0\n" \
"s_movk_i32 s84, 0x2180\n" \
"v_mfma_f32_16x16x32_bf16 v[140:143], a[248:251], a[212:215], v[140:143]\n" \
"buffer_load_dwordx4 v25, s[76:79], s84 offen offset:1024 lds\n" \
"v_mfma_f32_16x16x32_bf16 v[136:139], a[180:183], a[148:151], 0\n" \
"ds_read_b64_tr_b16 a[170:171], v34 offset:33280\n" \
"v_mfma_f32_16x16x32_bf16 v[136:139], a[244:247], a[212:215], v[136:139]\n" \
"ds_read_b64_tr_b16 a[232:233], v34 offset:36864\n" \
"v_mfma_f32_16x16x32_bf16 v[132:135], a[176:179], a[148:151], 0\n" \
"ds_read_b64_tr_b16 a[234:235], v34 offset:37376\n" \
"v_mfma_f32_16x16x32_bf16 v[132:135], a[240:243], a[212:215], v[132:135]\n" \
"ds_read_b128 a[148:151], v28 offset:43008\n" \
"v_mfma_f32_16x16x32_bf16 v[148:151], a[176:179], a[152:155], 0\n" \
"ds_read_b128 a[212:215], v29 offset:43008\n" \
"v_mfma_f32_16x16x32_bf16 v[148:151], a[240:243], a[216:219], v[148:151]\n" \
"s_movk_i32 s84, 0x4180\n" \
"v_mfma_f32_16x16x32_bf16 v[152:155], a[180:183], a[152:155], 0\n" \
"buffer_load_dwordx4 v24, s[76:79], s84 offen offset:2048 lds\n" \
"v_mfma_f32_16x16x32_bf16 v[152:155], a[244:247], a[216:219], v[152:155]\n" \
"s_movk_i32 s84, 0x6180\n" \
"v_mfma_f32_16x16x32_bf16 v[156:159], a[184:187], a[152:155], 0\n" \
"buffer_load_dwordx4 v25, s[76:79], s84 offen offset:3072 lds\n" \
"v_mfma_f32_16x16x32_bf16 v[156:159], a[248:251], a[216:219], v[156:159]\n" \
"ds_read_b128 v[248:251], v22 offset:32768\n" \
"v_mfma_f32_16x16x32_bf16 v[160:163], a[188:191], a[152:155], 0\n" \
"ds_read_b128 v[48:51], v23 offset:32768\n" \
"v_mfma_f32_16x16x32_bf16 v[160:163], a[252:255], a[216:219], v[160:163]\n" \
"ds_read_b128 a[152:155], v28 offset:45056\n" \
"v_mfma_f32_16x16x32_bf16 v[176:179], a[188:191], a[156:159], 0\n" \
"ds_read_b128 a[216:219], v29 offset:45056\n" \
"v_mfma_f32_16x16x32_bf16 v[176:179], a[252:255], a[220:223], v[176:179]\n" \
"v_mfma_f32_16x16x32_bf16 v[172:175], a[184:187], a[156:159], 0\n" \
"v_mfma_f32_16x16x32_bf16 v[172:175], a[248:251], a[220:223], v[172:175]\n" \
"v_mfma_f32_16x16x32_bf16 v[168:171], a[180:183], a[156:159], 0\n" \
"v_mfma_f32_16x16x32_bf16 v[168:171], a[244:247], a[220:223], v[168:171]\n" \
"v_mfma_f32_16x16x32_bf16 v[164:167], a[176:179], a[156:159], 0\n" \
"v_mfma_f32_16x16x32_bf16 v[164:167], a[240:243], a[220:223], v[164:167]\n" \
"ds_read_b128 a[156:159], v28 offset:47104\n" \
"ds_read_b128 a[220:223], v29 offset:47104\n" \
"s_waitcnt lgkmcnt(15)\n" \
"v_mfma_f32_16x16x32_bf16 a[0:3], a[160:163], a[128:131], a[0:3]\n" \
"s_add_u32 m0, s30, 0x1000\n" \
"s_addk_i32 s84, 0x3000\n" \
"v_mfma_f32_16x16x32_bf16 a[0:3], a[224:227], a[192:195], a[0:3]\n" \
"buffer_load_dwordx4 v24, s[76:79], s84 offen offset:0 lds\n" \
"v_mfma_f32_16x16x32_bf16 a[4:7], a[164:167], a[128:131], a[4:7]\n" \
"ds_read_b64_tr_b16 a[176:177], v36 offset:32768\n" \
"v_mov_b32_e64 v11, 0\n" \
"v_mfma_f32_16x16x32_bf16 a[4:7], a[228:231], a[192:195], a[4:7]\n" \
"ds_read_b64_tr_b16 a[178:179], v36 offset:33792\n" \
"s_and_b32 s6, s2, 7\n" \
"v_mfma_f32_16x16x32_bf16 a[12:15], a[172:175], a[128:131], a[12:15]\n" \
"ds_read_b64_tr_b16 a[240:241], v36 offset:40960\n" \
"s_lshl_b32 s7, s3, 1\n" \
"s_waitcnt lgkmcnt(15)\n" \
"v_mfma_f32_16x16x32_bf16 a[12:15], a[236:239], a[192:195], a[12:15]\n" \
"s_add_u32 s88, s41, 0x0\n" \
"buffer_store_dwordx4 v[228:231], v20, s[80:83], s88 offen offset:3072 sc1\n" \
"s_waitcnt lgkmcnt(13)\n" \
"v_mfma_f32_16x16x32_bf16 a[8:11], a[128:131], a[168:171], a[8:11]\n" \
"ds_read_b64_tr_b16 a[242:243], v36 offset:41984\n" \
"s_lshr_b32 s4, s2, 5\n" \
"s_waitcnt lgkmcnt(12)\n" \
"v_mfma_f32_16x16x32_bf16 a[8:11], a[192:195], a[232:235], a[8:11]\n" \
"ds_read_b64_tr_b16 a[180:181], v37 offset:32768\n" \
"s_lshl_b32 s5, s6, 2\n" \
"v_mfma_f32_16x16x32_bf16 a[24:27], a[132:135], a[168:171], a[24:27]\n" \
"ds_read_b64_tr_b16 a[182:183], v37 offset:33792\n" \
"s_bfe_u32 s8, s2, 0x20003\n" \
"v_mfma_f32_16x16x32_bf16 a[24:27], a[196:199], a[232:235], a[24:27]\n" \
"ds_read_b64_tr_b16 a[244:245], v37 offset:40960\n" \
"s_and_b32 s7, s7, 4\n" \
"v_mfma_f32_16x16x32_bf16 a[28:31], a[172:175], a[132:135], a[28:31]\n" \
"s_and_b32 s9, s3, 1\n" \
"s_or_b32 s13, s5, s8\n" \
"v_mfma_f32_16x16x32_bf16 a[28:31], a[236:239], a[196:199], a[28:31]\n" \
"s_addk_i32 s84, 0x2000\n" \
"v_mfma_f32_16x16x32_bf16 a[20:23], a[164:167], a[132:135], a[20:23]\n" \
"buffer_load_dwordx4 v25, s[76:79], s84 offen offset:1024 lds\n" \
"v_mfma_f32_16x16x32_bf16 a[20:23], a[228:231], a[196:199], a[20:23]\n" \
"ds_read_b64_tr_b16 a[246:247], v37 offset:41984\n" \
"s_or_b32 s9, s7, s9\n" \
"v_mfma_f32_16x16x32_bf16 a[16:19], a[160:163], a[132:135], a[16:19]\n" \
"ds_read_b64_tr_b16 a[184:185], v38 offset:32768\n" \
"s_mul_i32 s7, s4, 0xa00\n" \
"v_mfma_f32_16x16x32_bf16 a[16:19], a[224:227], a[196:199], a[16:19]\n" \
"ds_read_b64_tr_b16 a[186:187], v38 offset:33792\n" \
"s_add_i32 s7, s7, 0x5bc1000\n" \
"v_mfma_f32_16x16x32_bf16 a[32:35], a[160:163], a[136:139], a[32:35]\n" \
"s_addk_i32 s84, 0x2000\n" \
"v_mfma_f32_16x16x32_bf16 a[32:35], a[224:227], a[200:203], a[32:35]\n" \
"buffer_load_dwordx4 v24, s[76:79], s84 offen offset:2048 lds\n" \
"v_mfma_f32_16x16x32_bf16 a[36:39], a[164:167], a[136:139], a[36:39]\n" \
"ds_read_b64_tr_b16 a[248:249], v38 offset:40960\n" \
"s_lshl_b32 s10, s13, 2\n" \
"v_mfma_f32_16x16x32_bf16 a[36:39], a[228:231], a[200:203], a[36:39]\n" \
"ds_read_b64_tr_b16 a[250:251], v38 offset:41984\n" \
"s_or_b32 s10, s10, s7\n" \
"v_mfma_f32_16x16x32_bf16 a[44:47], a[172:175], a[136:139], a[44:47]\n" \
"ds_read_b64_tr_b16 a[188:189], v39 offset:32768\n" \
"v_writelane_b32 v11, s10, 0\n" \
"v_mfma_f32_16x16x32_bf16 a[44:47], a[236:239], a[200:203], a[44:47]\n" \
"ds_read_b64_tr_b16 a[190:191], v39 offset:33792\n" \
"s_lshl_b32 s14, s4, 3\n" \
"v_mfma_f32_16x16x32_bf16 a[40:43], a[136:139], a[168:171], a[40:43]\n" \
"s_add_u32 s89, s41, 0x1000\n" \
"buffer_store_dwordx4 v[232:235], v20, s[80:83], s89 offen offset:3072 sc1\n" \
"v_mfma_f32_16x16x32_bf16 a[40:43], a[200:203], a[232:235], a[40:43]\n" \
"ds_read_b64_tr_b16 a[252:253], v39 offset:40960\n" \
"v_writelane_b32 v11, s7, 1\n" \
"v_mfma_f32_16x16x32_bf16 a[56:59], a[140:143], a[168:171], a[56:59]\n" \
"ds_read_b64_tr_b16 a[254:255], v39 offset:41984\n" \
"s_or_b32 s7, s14, s6\n" \
"v_mfma_f32_16x16x32_bf16 a[56:59], a[204:207], a[232:235], a[56:59]\n" \
"s_lshl_b32 s7, s7, 10\n" \
"s_lshl_b32 s10, s3, 8\n" \
"v_mfma_f32_16x16x32_bf16 a[60:63], a[172:175], a[140:143], a[60:63]\n" \
"s_or_b32 s7, s7, s10\n" \
"s_or_b32 s10, s7, 0x5c00200\n" \
"v_mfma_f32_16x16x32_bf16 a[60:63], a[236:239], a[204:207], a[60:63]\n" \
"s_addk_i32 s84, 0x2000\n" \
"v_mfma_f32_16x16x32_bf16 a[52:55], a[164:167], a[140:143], a[52:55]\n" \
"buffer_load_dwordx4 v25, s[76:79], s84 offen offset:3072 lds\n" \
"v_mfma_f32_16x16x32_bf16 a[52:55], a[228:231], a[204:207], a[52:55]\n" \
"v_writelane_b32 v11, s10, 2\n" \
"s_lshl_b32 s10, s8, 2\n" \
"v_mfma_f32_16x16x32_bf16 a[48:51], a[160:163], a[140:143], a[48:51]\n" \
"s_or_b32 s11, s10, s7\n" \
"s_or_b32 s11, s11, 0x5c00000\n" \
"v_mfma_f32_16x16x32_bf16 a[48:51], a[224:227], a[204:207], a[48:51]\n" \
"v_writelane_b32 v11, s11, 3\n" \
"s_or_b32 s7, s7, 0x5c00080\n" \
"s_waitcnt lgkmcnt(15)\n" \
"v_mfma_f32_16x16x32_bf16 a[64:67], a[164:167], v[248:251], a[64:67]\n" \
"v_writelane_b32 v11, s7, 4\n" \
"s_or_b32 s7, s7, s10\n" \
"v_mfma_f32_16x16x32_bf16 a[64:67], a[228:231], v[48:51], a[64:67]\n" \
"s_add_i32 s10, s3, s2\n" \
"v_writelane_b32 v11, s7, 5\n" \
"v_mfma_f32_16x16x32_bf16 a[80:83], a[172:175], v[248:251], a[80:83]\n" \
"s_mov_b32 s7, 2.0\n" \
"s_mov_b32 s11, 0x5bc0000\n" \
"v_mfma_f32_16x16x32_bf16 a[80:83], a[236:239], v[48:51], a[80:83]\n" \
"s_cmp_lg_u32 s10, 0\n" \
"s_cselect_b32 s10, s7, s11\n" \
"v_mfma_f32_16x16x32_bf16 a[68:71], v[248:251], a[168:171], a[68:71]\n" \
"s_lshr_b32 s15, s2, 3\n" \
"v_writelane_b32 v11, s10, 54\n" \
"v_mfma_f32_16x16x32_bf16 a[68:71], v[48:51], a[232:235], a[68:71]\n" \
"s_waitcnt vmcnt(10) lgkmcnt(0)\n" \
"s_barrier\n" \
"v_xor_b32_e32 v28, 0x20000, v28\n" \
"v_xor_b32_e32 v29, 0x20000, v29\n" \
"v_mfma_f32_16x16x32_bf16 v[52:55], a[176:179], a[128:131], v[52:55]\n" \
"s_add_u32 m0, s40, 0x8000\n" \
"s_add_u32 s85, s39, 0x6000\n" \
"v_mfma_f32_16x16x32_bf16 v[52:55], a[240:243], a[192:195], v[52:55]\n" \
"buffer_load_dwordx4 v20, s[80:83], s85 offen sc1 lds\n" \
"v_mfma_f32_16x16x32_bf16 v[56:59], a[180:183], a[128:131], v[56:59]\n" \
"ds_read_b64_tr_b16 a[160:161], v32 offset:0\n" \
"s_lshl_b32 s10, s6, 5\n" \
"v_mfma_f32_16x16x32_bf16 v[56:59], a[244:247], a[192:195], v[56:59]\n" \
"ds_read_b64_tr_b16 a[162:163], v32 offset:512\n" \
"s_min_u32 s16, s3, 2\n" \
"s_nop 0\n" \
"v_mfma_f32_16x16x32_bf16 v[60:63], a[184:187], a[128:131], v[60:63]\n" \
"s_add_u32 m0, s40, 0x9000\n" \
"s_add_u32 s85, s39, 0x7000\n" \
"v_mfma_f32_16x16x32_bf16 v[60:63], a[248:251], a[192:195], v[60:63]\n" \
"buffer_load_dwordx4 v20, s[80:83], s85 offen sc1 lds\n" \
"v_mfma_f32_16x16x32_bf16 v[64:67], a[188:191], a[128:131], v[64:67]\n" \
"ds_read_b64_tr_b16 a[224:225], v32 offset:4096\n" \
"s_mul_i32 s12, s8, 0x120\n" \
"v_mfma_f32_16x16x32_bf16 v[64:67], a[252:255], a[192:195], v[64:67]\n" \
"ds_read_b128 a[128:131], v28 offset:0\n" \
"v_mfma_f32_16x16x32_bf16 v[80:83], a[188:191], a[132:135], v[80:83]\n" \
"ds_read_b128 a[192:195], v29 offset:0\n" \
"v_mfma_f32_16x16x32_bf16 v[80:83], a[252:255], a[196:199], v[80:83]\n" \
"s_add_u32 s88, s41, 0x1000\n" \
"buffer_store_dwordx4 v[236:239], v20, s[80:83], s88 offen offset:2048 sc1\n" \
"v_mfma_f32_16x16x32_bf16 v[76:79], a[184:187], a[132:135], v[76:79]\n" \
"ds_read_b64_tr_b16 a[226:227], v32 offset:4608\n" \
"v_mfma_f32_16x16x32_bf16 v[76:79], a[248:251], a[196:199], v[76:79]\n" \
"ds_read_b64_tr_b16 a[164:165], v33 offset:0\n" \
"v_mfma_f32_16x16x32_bf16 v[72:75], a[180:183], a[132:135], v[72:75]\n" \
"s_add_u32 m0, s40, 0xb000\n" \
"s_movk_i32 s84, 0x180\n" \
"s_nop 0\n" \
"v_mfma_f32_16x16x32_bf16 v[72:75], a[244:247], a[196:199], v[72:75]\n" \
"buffer_load_dwordx4 v21, s[76:79], s84 offen offset:0 lds\n" \
"v_mfma_f32_16x16x32_bf16 v[68:71], a[176:179], a[132:135], v[68:71]\n" \
"ds_read_b64_tr_b16 a[166:167], v33 offset:512\n" \
"v_mfma_f32_16x16x32_bf16 v[68:71], a[240:243], a[196:199], v[68:71]\n" \
"ds_read_b128 a[132:135], v28 offset:2048\n" \
"v_mfma_f32_16x16x32_bf16 v[84:87], a[176:179], a[136:139], v[84:87]\n" \
"ds_read_b128 a[196:199], v29 offset:2048\n" \
"v_mfma_f32_16x16x32_bf16 v[84:87], a[240:243], a[200:203], v[84:87]\n" \
"s_add_u32 m0, s31, 0x8000\n" \
"s_mov_b32 s84, 0x180000\n" \
"v_mfma_f32_16x16x32_bf16 v[88:91], a[180:183], a[136:139], v[88:91]\n" \
"buffer_load_dwordx4 v26, s[68:71], s84 offen offset:0 lds\n" \
"v_mfma_f32_16x16x32_bf16 v[88:91], a[244:247], a[200:203], v[88:91]\n" \
"ds_read_b64_tr_b16 a[228:229], v33 offset:4096\n" \
"v_mfma_f32_16x16x32_bf16 v[92:95], a[184:187], a[136:139], v[92:95]\n" \
"ds_read_b64_tr_b16 a[230:231], v33 offset:4608\n" \
"v_mfma_f32_16x16x32_bf16 v[92:95], a[248:251], a[200:203], v[92:95]\n" \
"s_addk_i32 s84, 0x7c00\n" \
"v_mfma_f32_16x16x32_bf16 v[96:99], a[188:191], a[136:139], v[96:99]\n" \
"buffer_load_dwordx4 v26, s[68:71], s84 offen offset:1024 lds\n" \
"v_mfma_f32_16x16x32_bf16 v[96:99], a[252:255], a[200:203], v[96:99]\n" \
"ds_read_b128 a[136:139], v28 offset:4096\n" \
"v_mfma_f32_16x16x32_bf16 v[112:115], a[188:191], a[140:143], v[112:115]\n" \
"ds_read_b128 a[200:203], v29 offset:4096\n" \
"v_mfma_f32_16x16x32_bf16 v[112:115], a[252:255], a[204:207], v[112:115]\n" \
"s_addk_i32 s84, 0x7c00\n" \
"v_mfma_f32_16x16x32_bf16 v[108:111], a[184:187], a[140:143], v[108:111]\n" \
"buffer_load_dwordx4 v27, s[68:71], s84 offen offset:2048 lds\n" \
"v_mfma_f32_16x16x32_bf16 v[108:111], a[248:251], a[204:207], v[108:111]\n" \
"ds_read_b64_tr_b16 a[172:173], v35 offset:0\n" \
"v_mfma_f32_16x16x32_bf16 v[104:107], a[180:183], a[140:143], v[104:107]\n" \
"ds_read_b64_tr_b16 a[174:175], v35 offset:512\n" \
"v_mfma_f32_16x16x32_bf16 v[104:107], a[244:247], a[204:207], v[104:107]\n" \
"ds_read_b64_tr_b16 a[236:237], v35 offset:4096\n" \
"v_mfma_f32_16x16x32_bf16 v[100:103], a[176:179], a[140:143], v[100:103]\n" \
"s_add_u32 s11, s22, s12\n" \
"s_addc_u32 s17, s23, 0\n" \
"v_mfma_f32_16x16x32_bf16 v[100:103], a[240:243], a[204:207], v[100:103]\n" \
"ds_read_b128 a[140:143], v28 offset:6144\n" \
"v_mfma_f32_16x16x32_bf16 v[116:119], a[176:179], a[144:147], v[116:119]\n" \
"ds_read_b128 a[204:207], v29 offset:6144\n" \
"v_mfma_f32_16x16x32_bf16 v[116:119], a[240:243], a[208:211], v[116:119]\n" \
"s_addk_i32 s84, 0x7c00\n" \
"v_mfma_f32_16x16x32_bf16 v[120:123], a[180:183], a[144:147], v[120:123]\n" \
"buffer_load_dwordx4 v27, s[68:71], s84 offen offset:3072 lds\n" \
"v_mfma_f32_16x16x32_bf16 v[120:123], a[244:247], a[208:211], v[120:123]\n" \
"s_add_u32 m0, s30, 0x8000\n" \
"s_movk_i32 s84, 0x200\n" \
"v_mfma_f32_16x16x32_bf16 v[124:127], a[184:187], a[144:147], v[124:127]\n" \
"buffer_load_dwordx4 v24, s[76:79], s84 offen offset:0 lds\n" \
"v_mfma_f32_16x16x32_bf16 v[124:127], a[248:251], a[208:211], v[124:127]\n" \
"ds_read_b64_tr_b16 a[238:239], v35 offset:4608\n" \
"v_mfma_f32_16x16x32_bf16 v[128:131], a[188:191], a[144:147], v[128:131]\n" \
"s_add_u32 s89, s41, 0x2000\n" \
"buffer_store_dwordx4 v[240:243], v20, s[80:83], s89 offen offset:2048 sc1\n" \
"v_mfma_f32_16x16x32_bf16 v[128:131], a[252:255], a[208:211], v[128:131]\n" \
"ds_read_b128 a[144:147], v28 offset:8192\n" \
"v_mfma_f32_16x16x32_bf16 v[144:147], a[188:191], a[148:151], v[144:147]\n" \
"ds_read_b128 a[208:211], v29 offset:8192\n" \
"v_mfma_f32_16x16x32_bf16 v[144:147], a[252:255], a[212:215], v[144:147]\n" \
"ds_read_b64_tr_b16 a[168:169], v34 offset:0\n" \
"v_mfma_f32_16x16x32_bf16 v[140:143], a[184:187], a[148:151], v[140:143]\n" \
"ds_read_b64_tr_b16 a[170:171], v34 offset:512\n" \
"v_mfma_f32_16x16x32_bf16 v[140:143], a[248:251], a[212:215], v[140:143]\n" \
"s_movk_i32 s84, 0x2200\n" \
"s_nop 0\n" \
"v_mfma_f32_16x16x32_bf16 v[136:139], a[180:183], a[148:151], v[136:139]\n" \
"buffer_load_dwordx4 v25, s[76:79], s84 offen offset:1024 lds\n" \
"v_mfma_f32_16x16x32_bf16 v[136:139], a[244:247], a[212:215], v[136:139]\n" \
"ds_read_b64_tr_b16 a[232:233], v34 offset:4096\n" \
"v_mfma_f32_16x16x32_bf16 v[132:135], a[176:179], a[148:151], v[132:135]\n" \
"ds_read_b64_tr_b16 a[234:235], v34 offset:4608\n" \
"v_mfma_f32_16x16x32_bf16 v[132:135], a[240:243], a[212:215], v[132:135]\n" \
"ds_read_b128 a[148:151], v28 offset:10240\n" \
"v_mfma_f32_16x16x32_bf16 v[148:151], a[176:179], a[152:155], v[148:151]\n" \
"ds_read_b128 a[212:215], v29 offset:10240\n" \
"v_mfma_f32_16x16x32_bf16 v[148:151], a[240:243], a[216:219], v[148:151]\n" \
"s_movk_i32 s84, 0x4200\n" \
"v_mfma_f32_16x16x32_bf16 v[152:155], a[180:183], a[152:155], v[152:155]\n" \
"buffer_load_dwordx4 v24, s[76:79], s84 offen offset:2048 lds\n" \
"v_mfma_f32_16x16x32_bf16 v[152:155], a[244:247], a[216:219], v[152:155]\n" \
"s_movk_i32 s84, 0x6200\n" \
"v_mfma_f32_16x16x32_bf16 v[156:159], a[184:187], a[152:155], v[156:159]\n" \
"buffer_load_dwordx4 v25, s[76:79], s84 offen offset:3072 lds\n" \
"v_mfma_f32_16x16x32_bf16 v[156:159], a[248:251], a[216:219], v[156:159]\n" \
"ds_read_b128 v[248:251], v22 offset:0\n" \
"v_mfma_f32_16x16x32_bf16 v[160:163], a[188:191], a[152:155], v[160:163]\n" \
"ds_read_b128 v[48:51], v23 offset:0\n" \
"v_mfma_f32_16x16x32_bf16 v[160:163], a[252:255], a[216:219], v[160:163]\n" \
"ds_read_b128 a[152:155], v28 offset:12288\n" \
"v_mfma_f32_16x16x32_bf16 v[176:179], a[188:191], a[156:159], v[176:179]\n" \
"ds_read_b128 a[216:219], v29 offset:12288\n" \
"v_mfma_f32_16x16x32_bf16 v[176:179], a[252:255], a[220:223], v[176:179]\n" \
"s_add_u32 s18, s24, s12\n" \
"v_writelane_b32 v11, s11, 9\n" \
"s_addc_u32 s19, s25, 0\n" \
"v_mfma_f32_16x16x32_bf16 v[172:175], a[184:187], a[156:159], v[172:175]\n" \
"s_and_b32 s20, s17, 0xffff\n" \
"v_writelane_b32 v11, s20, 10\n" \
"v_mfma_f32_16x16x32_bf16 v[172:175], a[248:251], a[220:223], v[172:175]\n" \
"s_and_b32 s20, s19, 0xffff\n" \
"v_writelane_b32 v11, s18, 11\n" \
"v_mfma_f32_16x16x32_bf16 v[168:171], a[180:183], a[156:159], v[168:171]\n" \
"s_cmpk_lt_u32 s3, 3\n" \
"s_cselect_b32 s11, s11, s18\n" \
"v_mfma_f32_16x16x32_bf16 v[168:171], a[244:247], a[220:223], v[168:171]\n" \
"s_movk_i32 s18, 0xfff\n" \
"v_writelane_b32 v11, s20, 12\n" \
"v_mfma_f32_16x16x32_bf16 v[164:167], a[176:179], a[156:159], v[164:167]\n" \
"s_and_b32 s20, s29, 0xffff\n" \
"v_writelane_b32 v11, s28, 13\n" \
"v_mfma_f32_16x16x32_bf16 v[164:167], a[240:243], a[220:223], v[164:167]\n" \
"ds_read_b128 a[156:159], v28 offset:14336\n" \
"ds_read_b128 a[220:223], v29 offset:14336\n" \
"s_waitcnt lgkmcnt(15)\n" \
"v_mfma_f32_16x16x32_bf16 a[0:3], a[160:163], a[128:131], a[0:3]\n" \
"s_add_u32 m0, s30, 0x9000\n" \
"s_addk_i32 s84, 0x3000\n" \
"v_mfma_f32_16x16x32_bf16 a[0:3], a[224:227], a[192:195], a[0:3]\n" \
"buffer_load_dwordx4 v24, s[76:79], s84 offen offset:0 lds\n" \
"v_mfma_f32_16x16x32_bf16 a[4:7], a[164:167], a[128:131], a[4:7]\n" \
"ds_read_b64_tr_b16 a[176:177], v36 offset:0\n" \
"v_writelane_b32 v11, s20, 14\n" \
"v_mfma_f32_16x16x32_bf16 a[4:7], a[228:231], a[192:195], a[4:7]\n" \
"ds_read_b64_tr_b16 a[178:179], v36 offset:1024\n" \
"v_writelane_b32 v11, s11, 15\n" \
"v_mfma_f32_16x16x32_bf16 a[12:15], a[172:175], a[128:131], a[12:15]\n" \
"ds_read_b64_tr_b16 a[240:241], v36 offset:8192\n" \
"s_waitcnt lgkmcnt(15)\n" \
"v_mfma_f32_16x16x32_bf16 a[12:15], a[236:239], a[192:195], a[12:15]\n" \
"s_add_u32 s88, s41, 0x2000\n" \
"buffer_store_dwordx4 v[244:247], v20, s[80:83], s88 offen offset:3072 sc1\n" \
"s_waitcnt lgkmcnt(13)\n" \
"v_mfma_f32_16x16x32_bf16 a[8:11], a[128:131], a[168:171], a[8:11]\n" \
"ds_read_b64_tr_b16 a[242:243], v36 offset:9216\n" \
"s_waitcnt lgkmcnt(12)\n" \
"v_mfma_f32_16x16x32_bf16 a[8:11], a[192:195], a[232:235], a[8:11]\n" \
"ds_read_b64_tr_b16 a[180:181], v37 offset:0\n" \
"v_mfma_f32_16x16x32_bf16 a[24:27], a[132:135], a[168:171], a[24:27]\n" \
"ds_read_b64_tr_b16 a[182:183], v37 offset:1024\n" \
"v_mfma_f32_16x16x32_bf16 a[24:27], a[196:199], a[232:235], a[24:27]\n" \
"ds_read_b64_tr_b16 a[244:245], v37 offset:8192\n" \
"v_mfma_f32_16x16x32_bf16 a[28:31], a[172:175], a[132:135], a[28:31]\n" \
"s_cmpk_lt_u32 s3, 3\n" \
"s_cselect_b32 s11, s17, s19\n" \
"v_mfma_f32_16x16x32_bf16 a[28:31], a[236:239], a[196:199], a[28:31]\n" \
"s_addk_i32 s84, 0x2000\n" \
"s_nop 0\n" \
"v_mfma_f32_16x16x32_bf16 a[20:23], a[164:167], a[132:135], a[20:23]\n" \
"buffer_load_dwordx4 v25, s[76:79], s84 offen offset:1024 lds\n" \
"v_mfma_f32_16x16x32_bf16 a[20:23], a[228:231], a[196:199], a[20:23]\n" \
"ds_read_b64_tr_b16 a[246:247], v37 offset:9216\n" \
"s_and_b32 s11, s11, 0xffff\n" \
"v_mfma_f32_16x16x32_bf16 a[16:19], a[160:163], a[132:135], a[16:19]\n" \
"ds_read_b64_tr_b16 a[184:185], v38 offset:0\n" \
"v_writelane_b32 v11, s11, 16\n" \
"v_mfma_f32_16x16x32_bf16 a[16:19], a[224:227], a[196:199], a[16:19]\n" \
"ds_read_b64_tr_b16 a[186:187], v38 offset:1024\n" \
"s_mul_i32 s11, s6, 0x48\n" \
"v_mfma_f32_16x16x32_bf16 a[32:35], a[160:163], a[136:139], a[32:35]\n" \
"s_addk_i32 s84, 0x2000\n" \
"s_nop 0\n" \
"v_mfma_f32_16x16x32_bf16 a[32:35], a[224:227], a[200:203], a[32:35]\n" \
"buffer_load_dwordx4 v24, s[76:79], s84 offen offset:2048 lds\n" \
"v_mfma_f32_16x16x32_bf16 a[36:39], a[164:167], a[136:139], a[36:39]\n" \
"ds_read_b64_tr_b16 a[248:249], v38 offset:8192\n" \
"s_add_i32 s17, s11, 0x47\n" \
"v_mfma_f32_16x16x32_bf16 a[36:39], a[228:231], a[200:203], a[36:39]\n" \
"ds_read_b64_tr_b16 a[250:251], v38 offset:9216\n" \
"v_mfma_f32_16x16x32_bf16 a[44:47], a[172:175], a[136:139], a[44:47]\n" \
"ds_read_b64_tr_b16 a[188:189], v39 offset:0\n" \
"v_mfma_f32_16x16x32_bf16 a[44:47], a[236:239], a[200:203], a[44:47]\n" \
".fill (L_pb + 24288 - .) / 4, 4, 0xBF800000\n" \
"L_pp6:\n" \
"s_cbranch_execz L_pret\n" \
"ds_read_b64_tr_b16 a[190:191], v39 offset:1024\n" \
"v_mfma_f32_16x16x32_bf16 a[40:43], a[136:139], a[168:171], a[40:43]\n" \
"s_add_u32 s89, s41, 0x3000\n" \
"buffer_store_dwordx4 v[184:187], v20, s[80:83], s89 offen offset:3072 sc1\n" \
"v_mfma_f32_16x16x32_bf16 a[40:43], a[200:203], a[232:235], a[40:43]\n" \
"ds_read_b64_tr_b16 a[252:253], v39 offset:8192\n" \
"v_mfma_f32_16x16x32_bf16 a[56:59], a[140:143], a[168:171], a[56:59]\n" \
"ds_read_b64_tr_b16 a[254:255], v39 offset:9216\n" \
"v_mfma_f32_16x16x32_bf16 a[56:59], a[204:207], a[232:235], a[56:59]\n" \
"s_cmpk_lt_u32 s3, 3\n" \
"s_cselect_b32 s17, s17, s18\n" \
"v_mfma_f32_16x16x32_bf16 a[60:63], a[172:175], a[140:143], a[60:63]\n" \
"s_mulk_i32 s17, 0x480\n" \
"s_addk_i32 s17, 0x120\n" \
"v_mfma_f32_16x16x32_bf16 a[60:63], a[236:239], a[204:207], a[60:63]\n" \
"s_addk_i32 s84, 0x2000\n" \
"v_mfma_f32_16x16x32_bf16 a[52:55], a[164:167], a[140:143], a[52:55]\n" \
"buffer_load_dwordx4 v25, s[76:79], s84 offen offset:3072 lds\n" \
"v_mfma_f32_16x16x32_bf16 a[52:55], a[228:231], a[204:207], a[52:55]\n" \
"v_writelane_b32 v11, s17, 17\n" \
"s_lshl_b32 s17, s3, 5\n" \
"v_mfma_f32_16x16x32_bf16 a[48:51], a[160:163], a[140:143], a[48:51]\n" \
"s_add_i32 s11, s11, s17\n" \
"s_mul_i32 s17, s11, 0x480\n" \
"v_mfma_f32_16x16x32_bf16 a[48:51], a[224:227], a[204:207], a[48:51]\n" \
"s_mul_i32 s11, s13, 0x24000\n" \
"s_add_i32 s18, s11, 0x1b000\n" \
"s_waitcnt lgkmcnt(15)\n" \
"v_mfma_f32_16x16x32_bf16 a[64:67], a[164:167], v[248:251], a[64:67]\n" \
"s_cmpk_lt_u32 s3, 3\n" \
"s_cselect_b32 s17, s17, s18\n" \
"v_mfma_f32_16x16x32_bf16 a[64:67], a[228:231], v[48:51], a[64:67]\n" \
"s_mov_b32 s18, 0x8000\n" \
"v_writelane_b32 v11, s17, 18\n" \
"v_mfma_f32_16x16x32_bf16 a[80:83], a[172:175], v[248:251], a[80:83]\n" \
"s_mul_i32 s17, s16, 0x2400\n" \
"s_mul_i32 s16, s16, 0x9000\n" \
"v_mfma_f32_16x16x32_bf16 a[80:83], a[236:239], v[48:51], a[80:83]\n" \
"s_bitset1_b32 s17, 17\n" \
"v_writelane_b32 v11, s17, 19\n" \
"v_mfma_f32_16x16x32_bf16 a[68:71], v[248:251], a[168:171], a[68:71]\n" \
"s_add_i32 s16, s11, s16\n" \
"v_writelane_b32 v11, s16, 26\n" \
"v_mfma_f32_16x16x32_bf16 a[68:71], v[48:51], a[232:235], a[68:71]\n" \
"s_waitcnt vmcnt(11) lgkmcnt(0)\n" \
"s_barrier\n" \
"v_xor_b32_e32 v28, 0x20000, v28\n" \
"v_xor_b32_e32 v29, 0x20000, v29\n" \
"v_mfma_f32_16x16x32_bf16 v[52:55], a[176:179], a[128:131], v[52:55]\n" \
"s_add_u32 m0, s40, 0x0\n" \
"s_add_u32 s85, s39, 0x8000\n" \
"v_mfma_f32_16x16x32_bf16 v[52:55], a[240:243], a[192:195], v[52:55]\n" \
"buffer_load_dwordx4 v20, s[80:83], s85 offen sc1 lds\n" \
"v_mfma_f32_16x16x32_bf16 v[56:59], a[180:183], a[128:131], v[56:59]\n" \
"ds_read_b64_tr_b16 a[160:161], v32 offset:32768\n" \
"s_mul_i32 s16, s3, 0x2400\n" \
"v_mfma_f32_16x16x32_bf16 v[56:59], a[244:247], a[192:195], v[56:59]\n" \
"ds_read_b64_tr_b16 a[162:163], v32 offset:33280\n" \
"s_add_i32 s17, s16, 0xa400\n" \
"v_mfma_f32_16x16x32_bf16 v[60:63], a[184:187], a[128:131], v[60:63]\n" \
"s_add_u32 m0, s40, 0x1000\n" \
"s_add_u32 s85, s39, 0x9000\n" \
"v_mfma_f32_16x16x32_bf16 v[60:63], a[248:251], a[192:195], v[60:63]\n" \
"buffer_load_dwordx4 v20, s[80:83], s85 offen sc1 lds\n" \
"v_mfma_f32_16x16x32_bf16 v[64:67], a[188:191], a[128:131], v[64:67]\n" \
"ds_read_b64_tr_b16 a[224:225], v32 offset:36864\n" \
"v_mfma_f32_16x16x32_bf16 v[64:67], a[252:255], a[192:195], v[64:67]\n" \
"ds_read_b128 a[128:131], v28 offset:0\n" \
"v_mfma_f32_16x16x32_bf16 v[80:83], a[188:191], a[132:135], v[80:83]\n" \
"ds_read_b128 a[192:195], v29 offset:0\n" \
"v_mfma_f32_16x16x32_bf16 v[80:83], a[252:255], a[196:199], v[80:83]\n" \
"s_add_u32 s88, s41, 0x3000\n" \
"buffer_store_dwordx4 v[188:191], v20, s[80:83], s88 offen offset:2048 sc1\n" \
"v_mfma_f32_16x16x32_bf16 v[76:79], a[184:187], a[132:135], v[76:79]\n" \
"ds_read_b64_tr_b16 a[226:227], v32 offset:37376\n" \
"v_mfma_f32_16x16x32_bf16 v[76:79], a[248:251], a[196:199], v[76:79]\n" \
"ds_read_b64_tr_b16 a[164:165], v33 offset:32768\n" \
"v_mfma_f32_16x16x32_bf16 v[72:75], a[180:183], a[132:135], v[72:75]\n" \
"s_add_u32 m0, s40, 0x3000\n" \
"s_movk_i32 s84, 0x200\n" \
"v_mfma_f32_16x16x32_bf16 v[72:75], a[244:247], a[196:199], v[72:75]\n" \
"buffer_load_dwordx4 v21, s[76:79], s84 offen offset:0 lds\n" \
"v_mfma_f32_16x16x32_bf16 v[68:71], a[176:179], a[132:135], v[68:71]\n" \
"ds_read_b64_tr_b16 a[166:167], v33 offset:33280\n" \
"v_mfma_f32_16x16x32_bf16 v[68:71], a[240:243], a[196:199], v[68:71]\n" \
"ds_read_b128 a[132:135], v28 offset:2048\n" \
"v_mfma_f32_16x16x32_bf16 v[84:87], a[176:179], a[136:139], v[84:87]\n" \
"ds_read_b128 a[196:199], v29 offset:2048\n" \
"v_mfma_f32_16x16x32_bf16 v[84:87], a[240:243], a[200:203], v[84:87]\n" \
"s_add_u32 m0, s31, 0x0\n" \
"s_mov_b32 s84, 0x200000\n" \
"v_mfma_f32_16x16x32_bf16 v[88:91], a[180:183], a[136:139], v[88:91]\n" \
"buffer_load_dwordx4 v26, s[68:71], s84 offen offset:0 lds\n" \
"v_mfma_f32_16x16x32_bf16 v[88:91], a[244:247], a[200:203], v[88:91]\n" \
"ds_read_b64_tr_b16 a[228:229], v33 offset:36864\n" \
"v_mfma_f32_16x16x32_bf16 v[92:95], a[184:187], a[136:139], v[92:95]\n" \
"ds_read_b64_tr_b16 a[230:231], v33 offset:37376\n" \
"v_mfma_f32_16x16x32_bf16 v[92:95], a[248:251], a[200:203], v[92:95]\n" \
"s_addk_i32 s84, 0x7c00\n" \
"v_mfma_f32_16x16x32_bf16 v[96:99], a[188:191], a[136:139], v[96:99]\n" \
"buffer_load_dwordx4 v26, s[68:71], s84 offen offset:1024 lds\n" \
"v_mfma_f32_16x16x32_bf16 v[96:99], a[252:255], a[200:203], v[96:99]\n" \
"ds_read_b128 a[136:139], v28 offset:4096\n" \
"v_mfma_f32_16x16x32_bf16 v[112:115], a[188:191], a[140:143], v[112:115]\n" \
"ds_read_b128 a[200:203], v29 offset:4096\n" \
"v_mfma_f32_16x16x32_bf16 v[112:115], a[252:255], a[204:207], v[112:115]\n" \
"s_addk_i32 s84, 0x7c00\n" \
"v_mfma_f32_16x16x32_bf16 v[108:111], a[184:187], a[140:143], v[108:111]\n" \
"buffer_load_dwordx4 v27, s[68:71], s84 offen offset:2048 lds\n" \
"v_mfma_f32_16x16x32_bf16 v[108:111], a[248:251], a[204:207], v[108:111]\n" \
"ds_read_b64_tr_b16 a[172:173], v35 offset:32768\n" \
"v_mfma_f32_16x16x32_bf16 v[104:107], a[180:183], a[140:143], v[104:107]\n" \
"ds_read_b64_tr_b16 a[174:175], v35 offset:33280\n" \
"v_mfma_f32_16x16x32_bf16 v[104:107], a[244:247], a[204:207], v[104:107]\n" \
"ds_read_b64_tr_b16 a[236:237], v35 offset:36864\n" \
"v_mfma_f32_16x16x32_bf16 v[100:103], a[176:179], a[140:143], v[100:103]\n" \
"s_cmpk_lt_u32 s3, 3\n" \
"s_cselect_b32 s17, s17, s18\n" \
"v_mfma_f32_16x16x32_bf16 v[100:103], a[240:243], a[204:207], v[100:103]\n" \
"ds_read_b128 a[140:143], v28 offset:6144\n" \
"s_mov_b32 s18, 0x1b000\n" \
"v_mfma_f32_16x16x32_bf16 v[116:119], a[176:179], a[144:147], v[116:119]\n" \
"ds_read_b128 a[204:207], v29 offset:6144\n" \
"s_xor_b32 s19, s3, 2\n" \
"v_mfma_f32_16x16x32_bf16 v[116:119], a[240:243], a[208:211], v[116:119]\n" \
"s_addk_i32 s84, 0x7c00\n" \
"v_mfma_f32_16x16x32_bf16 v[120:123], a[180:183], a[144:147], v[120:123]\n" \
"buffer_load_dwordx4 v27, s[68:71], s84 offen offset:3072 lds\n" \
"v_mfma_f32_16x16x32_bf16 v[120:123], a[244:247], a[208:211], v[120:123]\n" \
"s_add_u32 m0, s30, 0x20000\n" \
"s_movk_i32 s84, 0x280\n" \
"v_mfma_f32_16x16x32_bf16 v[124:127], a[184:187], a[144:147], v[124:127]\n" \
"buffer_load_dwordx4 v24, s[76:79], s84 offen offset:0 lds\n" \
"v_mfma_f32_16x16x32_bf16 v[124:127], a[248:251], a[208:211], v[124:127]\n" \
"ds_read_b64_tr_b16 a[238:239], v35 offset:37376\n" \
"v_mfma_f32_16x16x32_bf16 v[128:131], a[188:191], a[144:147], v[128:131]\n" \
"s_add_u32 s89, s42, 0x0\n" \
"buffer_store_dwordx4 v[216:219], v20, s[80:83], s89 offen offset:0 sc1\n" \
"v_mfma_f32_16x16x32_bf16 v[128:131], a[252:255], a[208:211], v[128:131]\n" \
"ds_read_b128 a[144:147], v28 offset:8192\n" \
"v_mfma_f32_16x16x32_bf16 v[144:147], a[188:191], a[148:151], v[144:147]\n" \
"ds_read_b128 a[208:211], v29 offset:8192\n" \
"v_mfma_f32_16x16x32_bf16 v[144:147], a[252:255], a[212:215], v[144:147]\n" \
"ds_read_b64_tr_b16 a[168:169], v34 offset:32768\n" \
"v_mfma_f32_16x16x32_bf16 v[140:143], a[184:187], a[148:151], v[140:143]\n" \
"ds_read_b64_tr_b16 a[170:171], v34 offset:33280\n" \
"v_mfma_f32_16x16x32_bf16 v[140:143], a[248:251], a[212:215], v[140:143]\n" \
"s_movk_i32 s84, 0x2280\n" \
"s_nop 0\n" \
"v_mfma_f32_16x16x32_bf16 v[136:139], a[180:183], a[148:151], v[136:139]\n" \
"buffer_load_dwordx4 v25, s[76:79], s84 offen offset:1024 lds\n" \
"v_mfma_f32_16x16x32_bf16 v[136:139], a[244:247], a[212:215], v[136:139]\n" \
"ds_read_b64_tr_b16 a[232:233], v34 offset:36864\n" \
"v_mfma_f32_16x16x32_bf16 v[132:135], a[176:179], a[148:151], v[132:135]\n" \
"ds_read_b64_tr_b16 a[234:235], v34 offset:37376\n" \
"v_mfma_f32_16x16x32_bf16 v[132:135], a[240:243], a[212:215], v[132:135]\n" \
"ds_read_b128 a[148:151], v28 offset:10240\n" \
"v_mfma_f32_16x16x32_bf16 v[148:151], a[176:179], a[152:155], v[148:151]\n" \
"ds_read_b128 a[212:215], v29 offset:10240\n" \
"v_mfma_f32_16x16x32_bf16 v[148:151], a[240:243], a[216:219], v[148:151]\n" \
"s_movk_i32 s84, 0x4280\n" \
"v_mfma_f32_16x16x32_bf16 v[152:155], a[180:183], a[152:155], v[152:155]\n" \
"buffer_load_dwordx4 v24, s[76:79], s84 offen offset:2048 lds\n" \
"v_mfma_f32_16x16x32_bf16 v[152:155], a[244:247], a[216:219], v[152:155]\n" \
"s_movk_i32 s84, 0x6280\n" \
"v_mfma_f32_16x16x32_bf16 v[156:159], a[184:187], a[152:155], v[156:159]\n" \
"buffer_load_dwordx4 v25, s[76:79], s84 offen offset:3072 lds\n" \
"v_mfma_f32_16x16x32_bf16 v[156:159], a[248:251], a[216:219], v[156:159]\n" \
"ds_read_b128 v[248:251], v22 offset:32768\n" \
"v_mfma_f32_16x16x32_bf16 v[160:163], a[188:191], a[152:155], v[160:163]\n" \
"ds_read_b128 v[48:51], v23 offset:32768\n" \
"v_mfma_f32_16x16x32_bf16 v[160:163], a[252:255], a[216:219], v[160:163]\n" \
"ds_read_b128 a[152:155], v28 offset:12288\n" \
"v_mfma_f32_16x16x32_bf16 v[176:179], a[188:191], a[156:159], v[176:179]\n" \
"ds_read_b128 a[216:219], v29 offset:12288\n" \
"v_mfma_f32_16x16x32_bf16 v[176:179], a[252:255], a[220:223], v[176:179]\n" \
"s_cmp_lg_u32 s19, 0\n" \
"s_cselect_b32 s18, s17, s18\n" \
"v_mfma_f32_16x16x32_bf16 v[172:175], a[184:187], a[156:159], v[172:175]\n" \
"v_writelane_b32 v11, s17, 20\n" \
"s_or_b32 s17, s16, 0x10000\n" \
"v_mfma_f32_16x16x32_bf16 v[172:175], a[248:251], a[220:223], v[172:175]\n" \
"v_writelane_b32 v11, s18, 21\n" \
"s_add_i32 s18, s16, 0x19000\n" \
"v_mfma_f32_16x16x32_bf16 v[168:171], a[180:183], a[156:159], v[168:171]\n" \
"v_writelane_b32 v11, s17, 22\n" \
"s_mov_b32 s17, 0\n" \
"v_mfma_f32_16x16x32_bf16 v[168:171], a[244:247], a[220:223], v[168:171]\n" \
"s_cmpk_lt_u32 s3, 3\n" \
"s_cselect_b32 s18, s18, s17\n" \
"v_mfma_f32_16x16x32_bf16 v[164:167], a[176:179], a[156:159], v[164:167]\n" \
"s_bitset1_b32 s16, 17\n" \
"v_writelane_b32 v11, s18, 23\n" \
"v_mfma_f32_16x16x32_bf16 v[164:167], a[240:243], a[220:223], v[164:167]\n" \
"ds_read_b128 a[156:159], v28 offset:14336\n" \
"ds_read_b128 a[220:223], v29 offset:14336\n" \
"s_waitcnt lgkmcnt(15)\n" \
"v_mfma_f32_16x16x32_bf16 a[0:3], a[160:163], a[128:131], a[0:3]\n" \
"s_add_u32 m0, s30, 0x21000\n" \
"s_addk_i32 s84, 0x3000\n" \
"v_mfma_f32_16x16x32_bf16 a[0:3], a[224:227], a[192:195], a[0:3]\n" \
"buffer_load_dwordx4 v24, s[76:79], s84 offen offset:0 lds\n" \
"v_mfma_f32_16x16x32_bf16 a[4:7], a[164:167], a[128:131], a[4:7]\n" \
"ds_read_b64_tr_b16 a[176:177], v36 offset:32768\n" \
"s_movk_i32 s18, 0x2400\n" \
"v_mfma_f32_16x16x32_bf16 a[4:7], a[228:231], a[192:195], a[4:7]\n" \
"ds_read_b64_tr_b16 a[178:179], v36 offset:33792\n" \
"v_mfma_f32_16x16x32_bf16 a[12:15], a[172:175], a[128:131], a[12:15]\n" \
"ds_read_b64_tr_b16 a[240:241], v36 offset:40960\n" \
"s_waitcnt lgkmcnt(15)\n" \
"v_mfma_f32_16x16x32_bf16 a[12:15], a[236:239], a[192:195], a[12:15]\n" \
"s_add_u32 s88, s42, 0x1000\n" \
"buffer_store_dwordx4 v[220:223], v20, s[80:83], s88 offen offset:0 sc1\n" \
"s_waitcnt lgkmcnt(13)\n" \
"v_mfma_f32_16x16x32_bf16 a[8:11], a[128:131], a[168:171], a[8:11]\n" \
"ds_read_b64_tr_b16 a[242:243], v36 offset:41984\n" \
"s_waitcnt lgkmcnt(12)\n" \
"v_mfma_f32_16x16x32_bf16 a[8:11], a[192:195], a[232:235], a[8:11]\n" \
"ds_read_b64_tr_b16 a[180:181], v37 offset:32768\n" \
"v_mfma_f32_16x16x32_bf16 a[24:27], a[132:135], a[168:171], a[24:27]\n" \
"ds_read_b64_tr_b16 a[182:183], v37 offset:33792\n" \
"v_mfma_f32_16x16x32_bf16 a[24:27], a[196:199], a[232:235], a[24:27]\n" \
"ds_read_b64_tr_b16 a[244:245], v37 offset:40960\n" \
"v_mfma_f32_16x16x32_bf16 a[28:31], a[172:175], a[132:135], a[28:31]\n" \
"s_cmpk_lt_u32 s3, 3\n" \
"s_cselect_b32 s16, s16, s18\n" \
"v_mfma_f32_16x16x32_bf16 a[28:31], a[236:239], a[196:199], a[28:31]\n" \
"s_addk_i32 s84, 0x2000\n" \
"v_mfma_f32_16x16x32_bf16 a[20:23], a[164:167], a[132:135], a[20:23]\n" \
"buffer_load_dwordx4 v25, s[76:79], s84 offen offset:1024 lds\n" \
"v_mfma_f32_16x16x32_bf16 a[20:23], a[228:231], a[196:199], a[20:23]\n" \
"ds_read_b64_tr_b16 a[246:247], v37 offset:41984\n" \
"s_mul_i32 s19, s3, 0x9000\n" \
"v_mfma_f32_16x16x32_bf16 a[16:19], a[160:163], a[132:135], a[16:19]\n" \
"ds_read_b64_tr_b16 a[184:185], v38 offset:32768\n" \
"v_writelane_b32 v11, s16, 24\n" \
"v_mfma_f32_16x16x32_bf16 a[16:19], a[224:227], a[196:199], a[16:19]\n" \
"ds_read_b64_tr_b16 a[186:187], v38 offset:33792\n" \
"s_lshl_b32 s16, s3, 10\n" \
"v_mfma_f32_16x16x32_bf16 a[32:35], a[160:163], a[136:139], a[32:35]\n" \
"s_addk_i32 s84, 0x2000\n" \
"v_mfma_f32_16x16x32_bf16 a[32:35], a[224:227], a[200:203], a[32:35]\n" \
"buffer_load_dwordx4 v24, s[76:79], s84 offen offset:2048 lds\n" \
"v_mfma_f32_16x16x32_bf16 a[36:39], a[164:167], a[136:139], a[36:39]\n" \
"ds_read_b64_tr_b16 a[248:249], v38 offset:40960\n" \
"s_addk_i32 s16, 0x4800\n" \
"v_mfma_f32_16x16x32_bf16 a[36:39], a[228:231], a[200:203], a[36:39]\n" \
"ds_read_b64_tr_b16 a[250:251], v38 offset:41984\n" \
"v_writelane_b32 v11, s16, 25\n" \
"v_mfma_f32_16x16x32_bf16 a[44:47], a[172:175], a[136:139], a[44:47]\n" \
"ds_read_b64_tr_b16 a[188:189], v39 offset:32768\n" \
"s_add_i32 s16, s15, 1\n" \
"v_mfma_f32_16x16x32_bf16 a[44:47], a[236:239], a[200:203], a[44:47]\n" \
"ds_read_b64_tr_b16 a[190:191], v39 offset:33792\n" \
"s_and_b32 s16, s16, 3\n" \
"v_mfma_f32_16x16x32_bf16 a[40:43], a[136:139], a[168:171], a[40:43]\n" \
"s_add_u32 s89, s42, 0x0\n" \
"buffer_store_dwordx4 v[192:195], v20, s[80:83], s89 offen offset:2048 sc1\n" \
"v_mfma_f32_16x16x32_bf16 a[40:43], a[200:203], a[232:235], a[40:43]\n" \
"ds_read_b64_tr_b16 a[252:253], v39 offset:40960\n" \
"s_or_b32 s16, s16, s5\n" \
"v_mfma_f32_16x16x32_bf16 a[56:59], a[140:143], a[168:171], a[56:59]\n" \
"ds_read_b64_tr_b16 a[254:255], v39 offset:41984\n" \
"s_mul_i32 s18, s16, 0x24000\n" \
"v_mfma_f32_16x16x32_bf16 a[56:59], a[204:207], a[232:235], a[56:59]\n" \
"s_add_i32 s18, s18, s19\n" \
"s_xor_b32 s13, s13, 2\n" \
"v_mfma_f32_16x16x32_bf16 a[60:63], a[172:175], a[140:143], a[60:63]\n" \
"s_add_i32 s15, s15, -1\n" \
"v_writelane_b32 v11, s18, 27\n" \
"v_mfma_f32_16x16x32_bf16 a[60:63], a[236:239], a[204:207], a[60:63]\n" \
"s_addk_i32 s84, 0x2000\n" \
"v_mfma_f32_16x16x32_bf16 a[52:55], a[164:167], a[140:143], a[52:55]\n" \
"buffer_load_dwordx4 v25, s[76:79], s84 offen offset:3072 lds\n" \
"v_mfma_f32_16x16x32_bf16 a[52:55], a[228:231], a[204:207], a[52:55]\n" \
"s_mul_i32 s18, s13, 0x24000\n" \
"s_and_b32 s15, s15, 3\n" \
"v_mfma_f32_16x16x32_bf16 a[48:51], a[160:163], a[140:143], a[48:51]\n" \
"s_add_i32 s18, s18, s19\n" \
"s_or_b32 s15, s15, s5\n" \
"v_mfma_f32_16x16x32_bf16 a[48:51], a[224:227], a[204:207], a[48:51]\n" \
"v_writelane_b32 v11, s18, 28\n" \
"s_mul_i32 s18, s15, 0x24000\n" \
"s_waitcnt lgkmcnt(15)\n" \
"v_mfma_f32_16x16x32_bf16 a[64:67], a[164:167], v[248:251], a[64:67]\n" \
"s_add_i32 s18, s18, s19\n" \
"v_writelane_b32 v11, s18, 29\n" \
"v_mfma_f32_16x16x32_bf16 a[64:67], a[228:231], v[48:51], a[64:67]\n" \
"s_or_b32 s18, s9, s14\n" \
"s_mul_i32 s18, s18, 0x24800\n" \
"v_mfma_f32_16x16x32_bf16 a[80:83], a[172:175], v[248:251], a[80:83]\n" \
"s_lshl_b32 s16, s16, 12\n" \
"s_add_i32 s19, s18, 0x3004800\n" \
"v_mfma_f32_16x16x32_bf16 a[80:83], a[236:239], v[48:51], a[80:83]\n" \
"s_add_i32 s20, s19, s16\n" \
"v_writelane_b32 v11, s20, 30\n" \
"v_mfma_f32_16x16x32_bf16 a[68:71], v[248:251], a[168:171], a[68:71]\n" \
"s_lshl_b32 s13, s13, 12\n" \
"s_lshl_b32 s15, s15, 12\n" \
"v_mfma_f32_16x16x32_bf16 a[68:71], v[48:51], a[232:235], a[68:71]\n" \
"s_waitcnt vmcnt(11) lgkmcnt(0)\n" \
"s_barrier\n" \
"v_mfma_f32_16x16x32_bf16 v[52:55], a[176:179], a[128:131], v[52:55]\n" \
"s_add_u32 m0, s40, 0x8000\n" \
"s_add_u32 s85, s39, 0xa000\n" \
"v_mfma_f32_16x16x32_bf16 v[52:55], a[240:243], a[192:195], v[52:55]\n" \
"buffer_load_dwordx4 v20, s[80:83], s85 offen sc1 lds\n" \
"v_mfma_f32_16x16x32_bf16 v[56:59], a[180:183], a[128:131], v[56:59]\n" \
"ds_read_b64_tr_b16 a[160:161], v32 offset:0\n" \
"v_accvgpr_read_b32 v224, a120\n" \
"v_mfma_f32_16x16x32_bf16 v[56:59], a[244:247], a[192:195], v[56:59]\n" \
"ds_read_b64_tr_b16 a[162:163], v32 offset:512\n" \
"v_accvgpr_read_b32 v225, a121\n" \
"v_mfma_f32_16x16x32_bf16 v[60:63], a[184:187], a[128:131], v[60:63]\n" \
"s_add_u32 m0, s40, 0x9000\n" \
"s_add_u32 s85, s39, 0xb000\n" \
"v_mfma_f32_16x16x32_bf16 v[60:63], a[248:251], a[192:195], v[60:63]\n" \
"buffer_load_dwordx4 v20, s[80:83], s85 offen sc1 lds\n" \
"v_mfma_f32_16x16x32_bf16 v[64:67], a[188:191], a[128:131], v[64:67]\n" \
"ds_read_b64_tr_b16 a[224:225], v32 offset:4096\n" \
"v_accvgpr_read_b32 v226, a122\n" \
"v_mfma_f32_16x16x32_bf16 v[64:67], a[252:255], a[192:195], v[64:67]\n" \
"ds_read_b128 a[128:131], v28 offset:32768\n" \
"v_accvgpr_read_b32 v227, a123\n" \
"v_mfma_f32_16x16x32_bf16 v[80:83], a[188:191], a[132:135], v[80:83]\n" \
"ds_read_b128 a[192:195], v29 offset:32768\n" \
"v_accvgpr_read_b32 v180, a124\n" \
"v_mfma_f32_16x16x32_bf16 v[80:83], a[252:255], a[196:199], v[80:83]\n" \
"s_add_u32 s88, s42, 0x0\n" \
"buffer_store_dwordx4 v[196:199], v20, s[80:83], s88 offen offset:3072 sc1\n" \
"v_mfma_f32_16x16x32_bf16 v[76:79], a[184:187], a[132:135], v[76:79]\n" \
"ds_read_b64_tr_b16 a[226:227], v32 offset:4608\n" \
"v_accvgpr_read_b32 v181, a125\n" \
"v_mfma_f32_16x16x32_bf16 v[76:79], a[248:251], a[196:199], v[76:79]\n" \
"ds_read_b64_tr_b16 a[164:165], v33 offset:0\n" \
"v_accvgpr_read_b32 v182, a126\n" \
"v_mfma_f32_16x16x32_bf16 v[72:75], a[180:183], a[132:135], v[72:75]\n" \
"s_add_u32 m0, s40, 0xb000\n" \
"s_movk_i32 s84, 0x280\n" \
"v_mfma_f32_16x16x32_bf16 v[72:75], a[244:247], a[196:199], v[72:75]\n" \
"buffer_load_dwordx4 v21, s[76:79], s84 offen offset:0 lds\n" \
"v_mfma_f32_16x16x32_bf16 v[68:71], a[176:179], a[132:135], v[68:71]\n" \
"ds_read_b64_tr_b16 a[166:167], v33 offset:512\n" \
"v_accvgpr_read_b32 v183, a127\n" \
"v_mfma_f32_16x16x32_bf16 v[68:71], a[240:243], a[196:199], v[68:71]\n" \
"ds_read_b128 a[132:135], v28 offset:34816\n" \
"v_cvt_pk_bf16_f32 v224, v224, v225\n" \
"v_mfma_f32_16x16x32_bf16 v[84:87], a[176:179], a[136:139], v[84:87]\n" \
"ds_read_b128 a[196:199], v29 offset:34816\n" \
"v_cvt_pk_bf16_f32 v225, v226, v227\n" \
"v_mfma_f32_16x16x32_bf16 v[84:87], a[240:243], a[200:203], v[84:87]\n" \
"s_add_u32 m0, s31, 0x8000\n" \
"s_mov_b32 s84, 0x280000\n" \
"v_mfma_f32_16x16x32_bf16 v[88:91], a[180:183], a[136:139], v[88:91]\n" \
"buffer_load_dwordx4 v26, s[68:71], s84 offen offset:0 lds\n" \
"v_mfma_f32_16x16x32_bf16 v[88:91], a[244:247], a[200:203], v[88:91]\n" \
"ds_read_b64_tr_b16 a[228:229], v33 offset:4096\n" \
"v_cvt_pk_bf16_f32 v226, v180, v181\n" \
"v_mfma_f32_16x16x32_bf16 v[92:95], a[184:187], a[136:139], v[92:95]\n" \
"ds_read_b64_tr_b16 a[230:231], v33 offset:4608\n" \
"v_cvt_pk_bf16_f32 v227, v182, v183\n" \
"v_mfma_f32_16x16x32_bf16 v[92:95], a[248:251], a[200:203], v[92:95]\n" \
"s_addk_i32 s84, 0x7c00\n" \
"v_mfma_f32_16x16x32_bf16 v[96:99], a[188:191], a[136:139], v[96:99]\n" \
"buffer_load_dwordx4 v26, s[68:71], s84 offen offset:1024 lds\n" \
"v_mfma_f32_16x16x32_bf16 v[96:99], a[252:255], a[200:203], v[96:99]\n" \
"ds_read_b128 a[136:139], v28 offset:36864\n" \
"v_accvgpr_read_b32 v220, a112\n" \
"v_mfma_f32_16x16x32_bf16 v[112:115], a[188:191], a[140:143], v[112:115]\n" \
"ds_read_b128 a[200:203], v29 offset:36864\n" \
"v_accvgpr_read_b32 v221, a113\n" \
"v_mfma_f32_16x16x32_bf16 v[112:115], a[252:255], a[204:207], v[112:115]\n" \
"s_addk_i32 s84, 0x7c00\n" \
"v_mfma_f32_16x16x32_bf16 v[108:111], a[184:187], a[140:143], v[108:111]\n" \
"buffer_load_dwordx4 v27, s[68:71], s84 offen offset:2048 lds\n" \
"v_mfma_f32_16x16x32_bf16 v[108:111], a[248:251], a[204:207], v[108:111]\n" \
"ds_read_b64_tr_b16 a[172:173], v35 offset:0\n" \
"v_accvgpr_read_b32 v222, a114\n" \
"v_mfma_f32_16x16x32_bf16 v[104:107], a[180:183], a[140:143], v[104:107]\n" \
"ds_read_b64_tr_b16 a[174:175], v35 offset:512\n" \
"v_accvgpr_read_b32 v223, a115\n" \
"v_mfma_f32_16x16x32_bf16 v[104:107], a[244:247], a[204:207], v[104:107]\n" \
"ds_read_b64_tr_b16 a[236:237], v35 offset:4096\n" \
"v_accvgpr_read_b32 v180, a116\n" \
"v_mfma_f32_16x16x32_bf16 v[100:103], a[176:179], a[140:143], v[100:103]\n" \
"v_accvgpr_read_b32 v181, a117\n" \
"v_accvgpr_read_b32 v182, a118\n" \
"v_mfma_f32_16x16x32_bf16 v[100:103], a[240:243], a[204:207], v[100:103]\n" \
"ds_read_b128 a[140:143], v28 offset:38912\n" \
"v_accvgpr_read_b32 v183, a119\n" \
"v_mfma_f32_16x16x32_bf16 v[116:119], a[176:179], a[144:147], v[116:119]\n" \
"ds_read_b128 a[204:207], v29 offset:38912\n" \
"v_cvt_pk_bf16_f32 v220, v220, v221\n" \
"v_mfma_f32_16x16x32_bf16 v[116:119], a[240:243], a[208:211], v[116:119]\n" \
"s_addk_i32 s84, 0x7c00\n" \
"v_mfma_f32_16x16x32_bf16 v[120:123], a[180:183], a[144:147], v[120:123]\n" \
"buffer_load_dwordx4 v27, s[68:71], s84 offen offset:3072 lds\n" \
"v_mfma_f32_16x16x32_bf16 v[120:123], a[244:247], a[208:211], v[120:123]\n" \
"s_add_u32 m0, s30, 0x0\n" \
"s_movk_i32 s84, 0x300\n" \
"s_nop 0\n" \
"v_mfma_f32_16x16x32_bf16 v[124:127], a[184:187], a[144:147], v[124:127]\n" \
"buffer_load_dwordx4 v24, s[76:79], s84 offen offset:0 lds\n" \
"v_mfma_f32_16x16x32_bf16 v[124:127], a[248:251], a[208:211], v[124:127]\n" \
"ds_read_b64_tr_b16 a[238:239], v35 offset:4608\n" \
"v_cvt_pk_bf16_f32 v221, v222, v223\n" \
"v_mfma_f32_16x16x32_bf16 v[128:131], a[188:191], a[144:147], v[128:131]\n" \
"s_add_u32 s89, s42, 0x1000\n" \
"buffer_store_dwordx4 a[84:87], v20, s[80:83], s89 offen offset:3072 sc1\n" \
"v_mfma_f32_16x16x32_bf16 v[128:131], a[252:255], a[208:211], v[128:131]\n" \
"ds_read_b128 a[144:147], v28 offset:40960\n" \
"v_cvt_pk_bf16_f32 v222, v180, v181\n" \
"v_mfma_f32_16x16x32_bf16 v[144:147], a[188:191], a[148:151], v[144:147]\n" \
"ds_read_b128 a[208:211], v29 offset:40960\n" \
"v_cvt_pk_bf16_f32 v223, v182, v183\n" \
"v_mfma_f32_16x16x32_bf16 v[144:147], a[252:255], a[212:215], v[144:147]\n" \
"ds_read_b64_tr_b16 a[168:169], v34 offset:0\n" \
"v_accvgpr_read_b32 v216, a104\n" \
"v_mfma_f32_16x16x32_bf16 v[140:143], a[184:187], a[148:151], v[140:143]\n" \
"ds_read_b64_tr_b16 a[170:171], v34 offset:512\n" \
"v_accvgpr_read_b32 v217, a105\n" \
"v_mfma_f32_16x16x32_bf16 v[140:143], a[248:251], a[212:215], v[140:143]\n" \
"s_movk_i32 s84, 0x2300\n" \
"s_nop 0\n" \
"v_mfma_f32_16x16x32_bf16 v[136:139], a[180:183], a[148:151], v[136:139]\n" \
"buffer_load_dwordx4 v25, s[76:79], s84 offen offset:1024 lds\n" \
"v_mfma_f32_16x16x32_bf16 v[136:139], a[244:247], a[212:215], v[136:139]\n" \
"ds_read_b64_tr_b16 a[232:233], v34 offset:4096\n" \
"v_accvgpr_read_b32 v218, a106\n" \
"v_mfma_f32_16x16x32_bf16 v[132:135], a[176:179], a[148:151], v[132:135]\n" \
"ds_read_b64_tr_b16 a[234:235], v34 offset:4608\n" \
"v_accvgpr_read_b32 v219, a107\n" \
"v_mfma_f32_16x16x32_bf16 v[132:135], a[240:243], a[212:215], v[132:135]\n" \
"ds_read_b128 a[148:151], v28 offset:43008\n" \
"v_accvgpr_read_b32 v180, a108\n" \
"v_mfma_f32_16x16x32_bf16 v[148:151], a[176:179], a[152:155], v[148:151]\n" \
"ds_read_b128 a[212:215], v29 offset:43008\n" \
"v_accvgpr_read_b32 v181, a109\n" \
"v_mfma_f32_16x16x32_bf16 v[148:151], a[240:243], a[216:219], v[148:151]\n" \
"s_movk_i32 s84, 0x4300\n" \
"v_mfma_f32_16x16x32_bf16 v[152:155], a[180:183], a[152:155], v[152:155]\n" \
"buffer_load_dwordx4 v24, s[76:79], s84 offen offset:2048 lds\n" \
"v_mfma_f32_16x16x32_bf16 v[152:155], a[244:247], a[216:219], v[152:155]\n" \
"s_movk_i32 s84, 0x6300\n" \
"v_mfma_f32_16x16x32_bf16 v[156:159], a[184:187], a[152:155], v[156:159]\n" \
"buffer_load_dwordx4 v25, s[76:79], s84 offen offset:3072 lds\n" \
"v_mfma_f32_16x16x32_bf16 v[156:159], a[248:251], a[216:219], v[156:159]\n" \
"ds_read_b128 v[248:251], v22 offset:0\n" \
"v_accvgpr_read_b32 v182, a110\n" \
"v_mfma_f32_16x16x32_bf16 v[160:163], a[188:191], a[152:155], v[160:163]\n" \
"ds_read_b128 v[48:51], v23 offset:0\n" \
"v_accvgpr_read_b32 v183, a111\n" \
"v_mfma_f32_16x16x32_bf16 v[160:163], a[252:255], a[216:219], v[160:163]\n" \
"ds_read_b128 a[152:155], v28 offset:45056\n" \
"v_cvt_pk_bf16_f32 v216, v216, v217\n" \
"v_mfma_f32_16x16x32_bf16 v[176:179], a[188:191], a[156:159], v[176:179]\n" \
"ds_read_b128 a[216:219], v29 offset:45056\n" \
"v_cvt_pk_bf16_f32 v217, v218, v219\n" \
"v_mfma_f32_16x16x32_bf16 v[176:179], a[252:255], a[220:223], v[176:179]\n" \
"v_cvt_pk_bf16_f32 v218, v180, v181\n" \
"v_cvt_pk_bf16_f32 v219, v182, v183\n" \
"v_mfma_f32_16x16x32_bf16 v[172:175], a[184:187], a[156:159], v[172:175]\n" \
"v_accvgpr_read_b32 v192, a96\n" \
"v_accvgpr_read_b32 v193, a97\n" \
"v_mfma_f32_16x16x32_bf16 v[172:175], a[248:251], a[220:223], v[172:175]\n" \
"v_accvgpr_read_b32 v194, a98\n" \
"v_accvgpr_read_b32 v195, a99\n" \
"v_mfma_f32_16x16x32_bf16 v[168:171], a[180:183], a[156:159], v[168:171]\n" \
"v_accvgpr_read_b32 v180, a100\n" \
"v_accvgpr_read_b32 v181, a101\n" \
"v_mfma_f32_16x16x32_bf16 v[168:171], a[244:247], a[220:223], v[168:171]\n" \
"v_accvgpr_read_b32 v182, a102\n" \
"v_accvgpr_read_b32 v183, a103\n" \
"v_mfma_f32_16x16x32_bf16 v[164:167], a[176:179], a[156:159], v[164:167]\n" \
"v_cvt_pk_bf16_f32 v192, v192, v193\n" \
"v_cvt_pk_bf16_f32 v193, v194, v195\n" \
"v_mfma_f32_16x16x32_bf16 v[164:167], a[240:243], a[220:223], v[164:167]\n" \
"ds_read_b128 a[156:159], v28 offset:47104\n" \
"ds_read_b128 a[220:223], v29 offset:47104\n" \
"s_waitcnt lgkmcnt(15)\n" \
"v_mfma_f32_16x16x32_bf16 a[0:3], a[160:163], a[128:131], a[0:3]\n" \
"s_add_u32 m0, s30, 0x1000\n" \
"s_addk_i32 s84, 0x3000\n" \
"v_mfma_f32_16x16x32_bf16 a[0:3], a[224:227], a[192:195], a[0:3]\n" \
"buffer_load_dwordx4 v24, s[76:79], s84 offen offset:0 lds\n" \
"v_mfma_f32_16x16x32_bf16 a[4:7], a[164:167], a[128:131], a[4:7]\n" \
"ds_read_b64_tr_b16 a[176:177], v36 offset:0\n" \
"v_cvt_pk_bf16_f32 v194, v180, v181\n" \
"v_mfma_f32_16x16x32_bf16 a[4:7], a[228:231], a[192:195], a[4:7]\n" \
"ds_read_b64_tr_b16 a[178:179], v36 offset:1024\n" \
"v_cvt_pk_bf16_f32 v195, v182, v183\n" \
"v_mfma_f32_16x16x32_bf16 a[12:15], a[172:175], a[128:131], a[12:15]\n" \
"ds_read_b64_tr_b16 a[240:241], v36 offset:8192\n" \
"v_accvgpr_read_b32 v188, a88\n" \
"s_waitcnt lgkmcnt(15)\n" \
"v_mfma_f32_16x16x32_bf16 a[12:15], a[236:239], a[192:195], a[12:15]\n" \
"s_add_u32 s88, s42, 0x1000\n" \
"buffer_store_dwordx4 v[252:255], v20, s[80:83], s88 offen offset:2048 sc1\n" \
"s_waitcnt lgkmcnt(13)\n" \
"v_mfma_f32_16x16x32_bf16 a[8:11], a[128:131], a[168:171], a[8:11]\n" \
"ds_read_b64_tr_b16 a[242:243], v36 offset:9216\n" \
"v_accvgpr_read_b32 v189, a89\n" \
"s_waitcnt lgkmcnt(12)\n" \
"s_nop 0\n" \
"v_mfma_f32_16x16x32_bf16 a[8:11], a[192:195], a[232:235], a[8:11]\n" \
"ds_read_b64_tr_b16 a[180:181], v37 offset:0\n" \
"v_accvgpr_read_b32 v190, a90\n" \
"v_mfma_f32_16x16x32_bf16 a[24:27], a[132:135], a[168:171], a[24:27]\n" \
"ds_read_b64_tr_b16 a[182:183], v37 offset:1024\n" \
"v_accvgpr_read_b32 v191, a91\n" \
"v_mfma_f32_16x16x32_bf16 a[24:27], a[196:199], a[232:235], a[24:27]\n" \
"ds_read_b64_tr_b16 a[244:245], v37 offset:8192\n" \
"v_accvgpr_read_b32 v180, a92\n" \
"v_mfma_f32_16x16x32_bf16 a[28:31], a[172:175], a[132:135], a[28:31]\n" \
"v_accvgpr_read_b32 v181, a93\n" \
"v_accvgpr_read_b32 v182, a94\n" \
"v_mfma_f32_16x16x32_bf16 a[28:31], a[236:239], a[196:199], a[28:31]\n" \
"s_addk_i32 s84, 0x2000\n" \
"s_nop 0\n" \
"v_mfma_f32_16x16x32_bf16 a[20:23], a[164:167], a[132:135], a[20:23]\n" \
"buffer_load_dwordx4 v25, s[76:79], s84 offen offset:1024 lds\n" \
"v_mfma_f32_16x16x32_bf16 a[20:23], a[228:231], a[196:199], a[20:23]\n" \
"ds_read_b64_tr_b16 a[246:247], v37 offset:9216\n" \
"v_accvgpr_read_b32 v183, a95\n" \
"v_mfma_f32_16x16x32_bf16 a[16:19], a[160:163], a[132:135], a[16:19]\n" \
"ds_read_b64_tr_b16 a[184:185], v38 offset:0\n" \
"v_cvt_pk_bf16_f32 v188, v188, v189\n" \
"v_mfma_f32_16x16x32_bf16 a[16:19], a[224:227], a[196:199], a[16:19]\n" \
"ds_read_b64_tr_b16 a[186:187], v38 offset:1024\n" \
"v_cvt_pk_bf16_f32 v189, v190, v191\n" \
"v_mfma_f32_16x16x32_bf16 a[32:35], a[160:163], a[136:139], a[32:35]\n" \
"s_addk_i32 s84, 0x2000\n" \
"s_nop 0\n" \
"v_mfma_f32_16x16x32_bf16 a[32:35], a[224:227], a[200:203], a[32:35]\n" \
"buffer_load_dwordx4 v24, s[76:79], s84 offen offset:2048 lds\n" \
"v_mfma_f32_16x16x32_bf16 a[36:39], a[164:167], a[136:139], a[36:39]\n" \
"ds_read_b64_tr_b16 a[248:249], v38 offset:8192\n" \
"v_cvt_pk_bf16_f32 v190, v180, v181\n" \
"v_mfma_f32_16x16x32_bf16 a[36:39], a[228:231], a[200:203], a[36:39]\n" \
"ds_read_b64_tr_b16 a[250:251], v38 offset:9216\n" \
"v_cvt_pk_bf16_f32 v191, v182, v183\n" \
"v_mfma_f32_16x16x32_bf16 a[44:47], a[172:175], a[136:139], a[44:47]\n" \
"ds_read_b64_tr_b16 a[188:189], v39 offset:0\n" \
"v_accvgpr_read_b32 v184, a72\n" \
"v_mfma_f32_16x16x32_bf16 a[44:47], a[236:239], a[200:203], a[44:47]\n" \
"ds_read_b64_tr_b16 a[190:191], v39 offset:1024\n" \
"v_accvgpr_read_b32 v185, a73\n" \
"v_mfma_f32_16x16x32_bf16 a[40:43], a[136:139], a[168:171], a[40:43]\n" \
"s_add_u32 s89, s42, 0x2000\n" \
"buffer_store_dwordx4 v[200:203], v20, s[80:83], s89 offen offset:2048 sc1\n" \
"v_mfma_f32_16x16x32_bf16 a[40:43], a[200:203], a[232:235], a[40:43]\n" \
"ds_read_b64_tr_b16 a[252:253], v39 offset:8192\n" \
"v_accvgpr_read_b32 v186, a74\n" \
"v_mfma_f32_16x16x32_bf16 a[56:59], a[140:143], a[168:171], a[56:59]\n" \
"ds_read_b64_tr_b16 a[254:255], v39 offset:9216\n" \
"v_accvgpr_read_b32 v187, a75\n" \
".fill (L_pb + 28384 - .) / 4, 4, 0xBF800000\n" \
"L_pp7:\n" \
"s_cbranch_execz L_pret\n" \
"v_mfma_f32_16x16x32_bf16 a[56:59], a[204:207], a[232:235], a[56:59]\n" \
"v_accvgpr_read_b32 v180, a76\n" \
"v_accvgpr_read_b32 v181, a77\n" \
"v_mfma_f32_16x16x32_bf16 a[60:63], a[172:175], a[140:143], a[60:63]\n" \
"v_accvgpr_read_b32 v182, a78\n" \
"v_accvgpr_read_b32 v183, a79\n" \
"v_mfma_f32_16x16x32_bf16 a[60:63], a[236:239], a[204:207], a[60:63]\n" \
"s_addk_i32 s84, 0x2000\n" \
"v_mfma_f32_16x16x32_bf16 a[52:55], a[164:167], a[140:143], a[52:55]\n" \
"buffer_load_dwordx4 v25, s[76:79], s84 offen offset:3072 lds\n" \
"v_mfma_f32_16x16x32_bf16 a[52:55], a[228:231], a[204:207], a[52:55]\n" \
"v_cvt_pk_bf16_f32 v184, v184, v185\n" \
"v_cvt_pk_bf16_f32 v185, v186, v187\n" \
"v_mfma_f32_16x16x32_bf16 a[48:51], a[160:163], a[140:143], a[48:51]\n" \
"v_cvt_pk_bf16_f32 v186, v180, v181\n" \
"v_cvt_pk_bf16_f32 v187, v182, v183\n" \
"v_mfma_f32_16x16x32_bf16 a[48:51], a[224:227], a[204:207], a[48:51]\n" \
"s_sub_i32 s14, s9, s14\n" \
"s_add_i32 s20, s19, s13\n" \
"s_waitcnt lgkmcnt(15)\n" \
"v_mfma_f32_16x16x32_bf16 a[64:67], a[164:167], v[248:251], a[64:67]\n" \
"v_writelane_b32 v11, s20, 31\n" \
"s_add_i32 s19, s19, s15\n" \
"v_mfma_f32_16x16x32_bf16 a[64:67], a[228:231], v[48:51], a[64:67]\n" \
"s_mul_i32 s14, s14, 0x24800\n" \
"v_writelane_b32 v11, s19, 32\n" \
"v_mfma_f32_16x16x32_bf16 a[80:83], a[172:175], v[248:251], a[80:83]\n" \
"s_add_i32 s19, s14, 0x4120800\n" \
"s_add_i32 s16, s19, s16\n" \
"v_mfma_f32_16x16x32_bf16 a[80:83], a[236:239], v[48:51], a[80:83]\n" \
"v_writelane_b32 v11, s16, 33\n" \
"s_add_i32 s13, s19, s13\n" \
"v_mfma_f32_16x16x32_bf16 a[68:71], v[248:251], a[168:171], a[68:71]\n" \
"v_writelane_b32 v11, s13, 34\n" \
"s_add_i32 s19, s19, s15\n" \
"v_mfma_f32_16x16x32_bf16 a[68:71], v[48:51], a[232:235], a[68:71]\n" \
"s_waitcnt vmcnt(11) lgkmcnt(0)\n" \
"s_barrier\n" \
"v_mov_b32_e32 v45, s62\n" \
"s_mov_b64 exec, 1\n" \
"buffer_store_dword v45, off, s[80:83], s37 offset:0 sc1\n" \
"s_mov_b64 exec, -1\n" \
"v_xor_b32_e32 v28, 0x20000, v28\n" \
"v_xor_b32_e32 v29, 0x20000, v29\n" \
"v_mfma_f32_16x16x32_bf16 v[52:55], a[176:179], a[128:131], v[52:55]\n" \
"s_add_u32 m0, s40, 0x0\n" \
"s_add_u32 s85, s39, 0xc000\n" \
"v_mfma_f32_16x16x32_bf16 v[52:55], a[240:243], a[192:195], v[52:55]\n" \
"buffer_load_dwordx4 v20, s[80:83], s85 offen sc1 lds\n" \
"v_mfma_f32_16x16x32_bf16 v[56:59], a[180:183], a[128:131], v[56:59]\n" \
"ds_read_b64_tr_b16 a[160:161], v32 offset:32768\n" \
"s_or_b32 s13, s18, 0x3000000\n" \
"v_mfma_f32_16x16x32_bf16 v[56:59], a[244:247], a[192:195], v[56:59]\n" \
"ds_read_b64_tr_b16 a[162:163], v32 offset:33280\n" \
"s_lshl_b32 s15, s6, 11\n" \
"v_mfma_f32_16x16x32_bf16 v[60:63], a[184:187], a[128:131], v[60:63]\n" \
"s_add_u32 m0, s40, 0x1000\n" \
"s_add_u32 s85, s39, 0xd000\n" \
"v_mfma_f32_16x16x32_bf16 v[60:63], a[248:251], a[192:195], v[60:63]\n" \
"buffer_load_dwordx4 v20, s[80:83], s85 offen sc1 lds\n" \
"v_mfma_f32_16x16x32_bf16 v[64:67], a[188:191], a[128:131], v[64:67]\n" \
"ds_read_b64_tr_b16 a[224:225], v32 offset:36864\n" \
"v_writelane_b32 v11, s19, 35\n" \
"v_mfma_f32_16x16x32_bf16 v[64:67], a[252:255], a[192:195], v[64:67]\n" \
"ds_read_b128 a[128:131], v28 offset:0\n" \
"s_add_i32 s16, s13, s15\n" \
"v_mfma_f32_16x16x32_bf16 v[80:83], a[188:191], a[132:135], v[80:83]\n" \
"ds_read_b128 a[192:195], v29 offset:0\n" \
"v_writelane_b32 v11, s16, 36\n" \
"v_mfma_f32_16x16x32_bf16 v[80:83], a[252:255], a[196:199], v[80:83]\n" \
"s_add_u32 m0, s40, 0x3000\n" \
"s_movk_i32 s84, 0x300\n" \
"v_mfma_f32_16x16x32_bf16 v[76:79], a[184:187], a[132:135], v[76:79]\n" \
"buffer_load_dwordx4 v21, s[76:79], s84 offen offset:0 lds\n" \
"v_mfma_f32_16x16x32_bf16 v[76:79], a[248:251], a[196:199], v[76:79]\n" \
"ds_read_b64_tr_b16 a[226:227], v32 offset:37376\n" \
"s_add_i32 s16, s14, 0x411c000\n" \
"v_mfma_f32_16x16x32_bf16 v[72:75], a[180:183], a[132:135], v[72:75]\n" \
"ds_read_b64_tr_b16 a[164:165], v33 offset:32768\n" \
"s_add_i32 s19, s16, s15\n" \
"s_nop 0\n" \
"v_mfma_f32_16x16x32_bf16 v[72:75], a[244:247], a[196:199], v[72:75]\n" \
"v_writelane_b32 v11, s19, 37\n" \
"s_add_i32 s18, s18, 0x3004000\n" \
"v_mfma_f32_16x16x32_bf16 v[68:71], a[176:179], a[132:135], v[68:71]\n" \
"ds_read_b64_tr_b16 a[166:167], v33 offset:33280\n" \
"v_writelane_b32 v11, s18, 38\n" \
"v_mfma_f32_16x16x32_bf16 v[68:71], a[240:243], a[196:199], v[68:71]\n" \
"ds_read_b128 a[132:135], v28 offset:2048\n" \
"s_add_i32 s14, s14, 0x4120000\n" \
"v_mfma_f32_16x16x32_bf16 v[84:87], a[176:179], a[136:139], v[84:87]\n" \
"ds_read_b128 a[196:199], v29 offset:2048\n" \
"v_writelane_b32 v11, s14, 39\n" \
"v_mfma_f32_16x16x32_bf16 v[84:87], a[240:243], a[200:203], v[84:87]\n" \
"s_add_u32 m0, s31, 0x0\n" \
"s_mov_b32 s84, 0x300000\n" \
"v_mfma_f32_16x16x32_bf16 v[88:91], a[180:183], a[136:139], v[88:91]\n" \
"buffer_load_dwordx4 v26, s[68:71], s84 offen offset:0 lds\n" \
"v_mfma_f32_16x16x32_bf16 v[88:91], a[244:247], a[200:203], v[88:91]\n" \
"s_add_u32 s88, s42, 0x2000\n" \
"buffer_store_dwordx4 v[204:207], v20, s[80:83], s88 offen offset:3072 sc1\n" \
"v_mfma_f32_16x16x32_bf16 v[92:95], a[184:187], a[136:139], v[92:95]\n" \
"ds_read_b64_tr_b16 a[228:229], v33 offset:36864\n" \
"s_lshl_b32 s14, s4, 2\n" \
"v_mfma_f32_16x16x32_bf16 v[92:95], a[248:251], a[200:203], v[92:95]\n" \
"ds_read_b64_tr_b16 a[230:231], v33 offset:37376\n" \
"s_or_b32 s8, s14, s8\n" \
"v_mfma_f32_16x16x32_bf16 v[96:99], a[188:191], a[136:139], v[96:99]\n" \
"s_mul_i32 s8, s8, 0x90000\n" \
"s_lshl_b32 s14, s3, 14\n" \
"v_mfma_f32_16x16x32_bf16 v[96:99], a[252:255], a[200:203], v[96:99]\n" \
"ds_read_b128 a[136:139], v28 offset:4096\n" \
"s_add_i32 s8, s8, s14\n" \
"v_mfma_f32_16x16x32_bf16 v[112:115], a[188:191], a[140:143], v[112:115]\n" \
"ds_read_b128 a[200:203], v29 offset:4096\n" \
"s_lshl_b32 s9, s9, 4\n" \
"v_mfma_f32_16x16x32_bf16 v[112:115], a[252:255], a[204:207], v[112:115]\n" \
"s_addk_i32 s84, 0x7c00\n" \
"v_mfma_f32_16x16x32_bf16 v[108:111], a[184:187], a[140:143], v[108:111]\n" \
"buffer_load_dwordx4 v26, s[68:71], s84 offen offset:1024 lds\n" \
"v_mfma_f32_16x16x32_bf16 v[108:111], a[248:251], a[204:207], v[108:111]\n" \
"ds_read_b64_tr_b16 a[172:173], v35 offset:32768\n" \
"s_lshl_b32 s14, s4, 7\n" \
"v_mfma_f32_16x16x32_bf16 v[104:107], a[180:183], a[140:143], v[104:107]\n" \
"s_addk_i32 s84, 0x7c00\n" \
"v_mfma_f32_16x16x32_bf16 v[104:107], a[244:247], a[204:207], v[104:107]\n" \
"buffer_load_dwordx4 v27, s[68:71], s84 offen offset:2048 lds\n" \
"v_mfma_f32_16x16x32_bf16 v[100:103], a[176:179], a[140:143], v[100:103]\n" \
"ds_read_b64_tr_b16 a[174:175], v35 offset:33280\n" \
"s_or_b32 s18, s9, s14\n" \
"v_mfma_f32_16x16x32_bf16 v[100:103], a[240:243], a[204:207], v[100:103]\n" \
"ds_read_b128 a[140:143], v28 offset:6144\n" \
"s_sub_i32 s9, s9, s14\n" \
"v_mfma_f32_16x16x32_bf16 v[116:119], a[176:179], a[144:147], v[116:119]\n" \
"ds_read_b128 a[204:207], v29 offset:6144\n" \
"s_mulk_i32 s18, 0x480\n" \
"v_mfma_f32_16x16x32_bf16 v[116:119], a[240:243], a[208:211], v[116:119]\n" \
"s_addk_i32 s84, 0x7c00\n" \
"v_mfma_f32_16x16x32_bf16 v[120:123], a[180:183], a[144:147], v[120:123]\n" \
"buffer_load_dwordx4 v27, s[68:71], s84 offen offset:3072 lds\n" \
"v_mfma_f32_16x16x32_bf16 v[120:123], a[244:247], a[208:211], v[120:123]\n" \
"ds_read_b64_tr_b16 a[236:237], v35 offset:36864\n" \
"s_mulk_i32 s9, 0x480\n" \
"v_mfma_f32_16x16x32_bf16 v[124:127], a[184:187], a[144:147], v[124:127]\n" \
"ds_read_b64_tr_b16 a[238:239], v35 offset:37376\n" \
"s_or_b32 s14, s18, s12\n" \
"v_mfma_f32_16x16x32_bf16 v[124:127], a[248:251], a[208:211], v[124:127]\n" \
"s_add_u32 m0, s30, 0x8000\n" \
"s_movk_i32 s84, 0x380\n" \
"v_mfma_f32_16x16x32_bf16 v[128:131], a[188:191], a[144:147], v[128:131]\n" \
"buffer_load_dwordx4 v24, s[76:79], s84 offen offset:0 lds\n" \
"v_mfma_f32_16x16x32_bf16 v[128:131], a[252:255], a[208:211], v[128:131]\n" \
"ds_read_b128 a[144:147], v28 offset:8192\n" \
"s_or_b32 s9, s9, s12\n" \
"v_mfma_f32_16x16x32_bf16 v[144:147], a[188:191], a[148:151], v[144:147]\n" \
"ds_read_b128 a[208:211], v29 offset:8192\n" \
"s_and_b32 s12, s2, 1\n" \
"v_mfma_f32_16x16x32_bf16 v[144:147], a[252:255], a[212:215], v[144:147]\n" \
"ds_read_b64_tr_b16 a[168:169], v34 offset:32768\n" \
"v_mfma_f32_16x16x32_bf16 v[140:143], a[184:187], a[148:151], v[140:143]\n" \
"s_movk_i32 s84, 0x2380\n" \
"s_nop 0\n" \
"v_mfma_f32_16x16x32_bf16 v[140:143], a[248:251], a[212:215], v[140:143]\n" \
"buffer_load_dwordx4 v25, s[76:79], s84 offen offset:1024 lds\n" \
"v_mfma_f32_16x16x32_bf16 v[136:139], a[180:183], a[148:151], v[136:139]\n" \
"ds_read_b64_tr_b16 a[170:171], v34 offset:33280\n" \
"v_mfma_f32_16x16x32_bf16 v[136:139], a[244:247], a[212:215], v[136:139]\n" \
"ds_read_b64_tr_b16 a[232:233], v34 offset:36864\n" \
"v_mfma_f32_16x16x32_bf16 v[132:135], a[176:179], a[148:151], v[132:135]\n" \
"ds_read_b64_tr_b16 a[234:235], v34 offset:37376\n" \
"v_mfma_f32_16x16x32_bf16 v[132:135], a[240:243], a[212:215], v[132:135]\n" \
"ds_read_b128 a[148:151], v28 offset:10240\n" \
"v_mfma_f32_16x16x32_bf16 v[148:151], a[176:179], a[152:155], v[148:151]\n" \
"ds_read_b128 a[212:215], v29 offset:10240\n" \
"v_mfma_f32_16x16x32_bf16 v[148:151], a[240:243], a[216:219], v[148:151]\n" \
"s_movk_i32 s84, 0x4380\n" \
"v_mfma_f32_16x16x32_bf16 v[152:155], a[180:183], a[152:155], v[152:155]\n" \
"buffer_load_dwordx4 v24, s[76:79], s84 offen offset:2048 lds\n" \
"v_mfma_f32_16x16x32_bf16 v[152:155], a[244:247], a[216:219], v[152:155]\n" \
"s_movk_i32 s84, 0x6380\n" \
"v_mfma_f32_16x16x32_bf16 v[156:159], a[184:187], a[152:155], v[156:159]\n" \
"buffer_load_dwordx4 v25, s[76:79], s84 offen offset:3072 lds\n" \
"v_mfma_f32_16x16x32_bf16 v[156:159], a[248:251], a[216:219], v[156:159]\n" \
"ds_read_b128 v[248:251], v22 offset:32768\n" \
"v_mfma_f32_16x16x32_bf16 v[160:163], a[188:191], a[152:155], v[160:163]\n" \
"ds_read_b128 v[48:51], v23 offset:32768\n" \
"v_mfma_f32_16x16x32_bf16 v[160:163], a[252:255], a[216:219], v[160:163]\n" \
"ds_read_b128 a[152:155], v28 offset:12288\n" \
"v_mfma_f32_16x16x32_bf16 v[176:179], a[188:191], a[156:159], v[176:179]\n" \
"ds_read_b128 a[216:219], v29 offset:12288\n" \
"v_mfma_f32_16x16x32_bf16 v[176:179], a[252:255], a[220:223], v[176:179]\n" \
"s_cmp_lg_u32 s12, 0\n" \
"s_cselect_b32 s13, s16, s13\n" \
"v_mfma_f32_16x16x32_bf16 v[172:175], a[184:187], a[156:159], v[172:175]\n" \
"s_bfe_u32 s16, s2, 0x20001\n" \
"s_addk_i32 s13, 0x4000\n" \
"v_mfma_f32_16x16x32_bf16 v[172:175], a[248:251], a[220:223], v[172:175]\n" \
"s_xor_b32 s16, s16, s3\n" \
"v_mfma_f32_16x16x32_bf16 v[168:171], a[180:183], a[156:159], v[168:171]\n" \
"s_cmp_lg_u32 s16, 0\n" \
"s_cselect_b32 s13, s7, s13\n" \
"v_mfma_f32_16x16x32_bf16 v[168:171], a[244:247], a[220:223], v[168:171]\n" \
"s_lshl_b32 s6, s6, 16\n" \
"v_writelane_b32 v11, s13, 40\n" \
"v_mfma_f32_16x16x32_bf16 v[164:167], a[176:179], a[156:159], v[164:167]\n" \
"s_add_i32 s13, s8, 0x4240000\n" \
"s_add_i32 s6, s13, s6\n" \
"v_mfma_f32_16x16x32_bf16 v[164:167], a[240:243], a[220:223], v[164:167]\n" \
"ds_read_b128 a[156:159], v28 offset:14336\n" \
"ds_read_b128 a[220:223], v29 offset:14336\n" \
"s_waitcnt lgkmcnt(15)\n" \
"v_mfma_f32_16x16x32_bf16 a[0:3], a[160:163], a[128:131], a[0:3]\n" \
"s_add_u32 m0, s30, 0x9000\n" \
"s_addk_i32 s84, 0x3000\n" \
"v_mfma_f32_16x16x32_bf16 a[0:3], a[224:227], a[192:195], a[0:3]\n" \
"buffer_load_dwordx4 v24, s[76:79], s84 offen offset:0 lds\n" \
"v_mfma_f32_16x16x32_bf16 a[4:7], a[164:167], a[128:131], a[4:7]\n" \
"ds_read_b64_tr_b16 a[176:177], v36 offset:32768\n" \
"v_writelane_b32 v11, s6, 41\n" \
"v_mfma_f32_16x16x32_bf16 a[4:7], a[228:231], a[192:195], a[4:7]\n" \
"ds_read_b64_tr_b16 a[178:179], v36 offset:33792\n" \
"s_add_i32 s8, s8, 0x42c0000\n" \
"v_mfma_f32_16x16x32_bf16 a[12:15], a[172:175], a[128:131], a[12:15]\n" \
"ds_read_b64_tr_b16 a[240:241], v36 offset:40960\n" \
"s_lshl_b32 s6, s12, 10\n" \
"s_waitcnt lgkmcnt(15)\n" \
"v_mfma_f32_16x16x32_bf16 a[12:15], a[236:239], a[192:195], a[12:15]\n" \
"s_add_u32 s89, s42, 0x3000\n" \
"buffer_store_dwordx4 v[208:211], v20, s[80:83], s89 offen offset:3072 sc1\n" \
"s_waitcnt lgkmcnt(13)\n" \
"v_mfma_f32_16x16x32_bf16 a[8:11], a[128:131], a[168:171], a[8:11]\n" \
"ds_read_b64_tr_b16 a[242:243], v36 offset:41984\n" \
"s_or_b32 s6, s8, s6\n" \
"s_waitcnt lgkmcnt(12)\n" \
"v_mfma_f32_16x16x32_bf16 a[8:11], a[192:195], a[232:235], a[8:11]\n" \
"ds_read_b64_tr_b16 a[180:181], v37 offset:32768\n" \
"v_mfma_f32_16x16x32_bf16 a[24:27], a[132:135], a[168:171], a[24:27]\n" \
"ds_read_b64_tr_b16 a[182:183], v37 offset:33792\n" \
"v_mfma_f32_16x16x32_bf16 a[24:27], a[196:199], a[232:235], a[24:27]\n" \
"ds_read_b64_tr_b16 a[244:245], v37 offset:40960\n" \
"v_mfma_f32_16x16x32_bf16 a[28:31], a[172:175], a[132:135], a[28:31]\n" \
"s_cmp_lg_u32 s16, 0\n" \
"s_cselect_b32 s6, s7, s6\n" \
"v_mfma_f32_16x16x32_bf16 a[28:31], a[236:239], a[196:199], a[28:31]\n" \
"s_addk_i32 s84, 0x2000\n" \
"v_mfma_f32_16x16x32_bf16 a[20:23], a[164:167], a[132:135], a[20:23]\n" \
"buffer_load_dwordx4 v25, s[76:79], s84 offen offset:1024 lds\n" \
"v_mfma_f32_16x16x32_bf16 a[20:23], a[228:231], a[196:199], a[20:23]\n" \
"ds_read_b64_tr_b16 a[246:247], v37 offset:41984\n" \
"s_mulk_i32 s4, 0x4800\n" \
"v_mfma_f32_16x16x32_bf16 a[16:19], a[160:163], a[132:135], a[16:19]\n" \
"ds_read_b64_tr_b16 a[184:185], v38 offset:32768\n" \
"v_writelane_b32 v11, s6, 42\n" \
"v_mfma_f32_16x16x32_bf16 a[16:19], a[224:227], a[196:199], a[16:19]\n" \
"ds_read_b64_tr_b16 a[186:187], v38 offset:33792\n" \
"s_add_i32 s6, s14, s10\n" \
"v_mfma_f32_16x16x32_bf16 a[32:35], a[160:163], a[136:139], a[32:35]\n" \
"s_addk_i32 s84, 0x2000\n" \
"v_mfma_f32_16x16x32_bf16 a[32:35], a[224:227], a[200:203], a[32:35]\n" \
"buffer_load_dwordx4 v24, s[76:79], s84 offen offset:2048 lds\n" \
"v_mfma_f32_16x16x32_bf16 a[36:39], a[164:167], a[136:139], a[36:39]\n" \
"ds_read_b64_tr_b16 a[248:249], v38 offset:40960\n" \
"v_writelane_b32 v11, s6, 43\n" \
"v_mfma_f32_16x16x32_bf16 a[36:39], a[228:231], a[200:203], a[36:39]\n" \
"ds_read_b64_tr_b16 a[250:251], v38 offset:41984\n" \
"s_add_i32 s6, s9, 0x21c000\n" \
"v_mfma_f32_16x16x32_bf16 a[44:47], a[172:175], a[136:139], a[44:47]\n" \
"ds_read_b64_tr_b16 a[188:189], v39 offset:32768\n" \
"s_add_i32 s9, s6, s10\n" \
"v_mfma_f32_16x16x32_bf16 a[44:47], a[236:239], a[200:203], a[44:47]\n" \
"ds_read_b64_tr_b16 a[190:191], v39 offset:33792\n" \
"v_mfma_f32_16x16x32_bf16 a[40:43], a[136:139], a[168:171], a[40:43]\n" \
"s_add_u32 s88, s42, 0x3000\n" \
"buffer_store_dwordx4 v[212:215], v20, s[80:83], s88 offen offset:2048 sc1\n" \
"v_mfma_f32_16x16x32_bf16 a[40:43], a[200:203], a[232:235], a[40:43]\n" \
"ds_read_b64_tr_b16 a[252:253], v39 offset:40960\n" \
"v_mfma_f32_16x16x32_bf16 a[56:59], a[140:143], a[168:171], a[56:59]\n" \
"ds_read_b64_tr_b16 a[254:255], v39 offset:41984\n" \
"v_mfma_f32_16x16x32_bf16 a[56:59], a[204:207], a[232:235], a[56:59]\n" \
"s_cmp_lg_u32 s12, 0\n" \
"s_cselect_b32 s6, s6, s14\n" \
"v_mfma_f32_16x16x32_bf16 a[60:63], a[172:175], a[140:143], a[60:63]\n" \
"s_addk_i32 s6, 0x100\n" \
"v_writelane_b32 v11, s9, 44\n" \
"v_mfma_f32_16x16x32_bf16 a[60:63], a[236:239], a[204:207], a[60:63]\n" \
"s_addk_i32 s84, 0x2000\n" \
"v_mfma_f32_16x16x32_bf16 a[52:55], a[164:167], a[140:143], a[52:55]\n" \
"buffer_load_dwordx4 v25, s[76:79], s84 offen offset:3072 lds\n" \
"v_mfma_f32_16x16x32_bf16 a[52:55], a[228:231], a[204:207], a[52:55]\n" \
"s_cmp_lg_u32 s16, 0\n" \
"s_cselect_b32 s6, s7, s6\n" \
"v_mfma_f32_16x16x32_bf16 a[48:51], a[160:163], a[140:143], a[48:51]\n" \
"v_writelane_b32 v11, s6, 45\n" \
"s_or_b32 s6, s8, s15\n" \
"v_mfma_f32_16x16x32_bf16 a[48:51], a[224:227], a[204:207], a[48:51]\n" \
"v_writelane_b32 v11, s6, 53\n" \
"s_lshl_b32 s6, s3, 17\n" \
"s_waitcnt lgkmcnt(15)\n" \
"v_mfma_f32_16x16x32_bf16 a[64:67], a[164:167], v[248:251], a[64:67]\n" \
"s_add_i32 s6, s13, s6\n" \
"s_or_b32 s5, s6, s5\n" \
"v_mfma_f32_16x16x32_bf16 a[64:67], a[228:231], v[48:51], a[64:67]\n" \
"s_or_b32 s5, s5, 0x3840\n" \
"v_writelane_b32 v11, s5, 6\n" \
"v_mfma_f32_16x16x32_bf16 a[80:83], a[172:175], v[248:251], a[80:83]\n" \
"s_mov_b32 s5, 0xff0000\n" \
"v_mfma_f32_16x16x32_bf16 a[80:83], a[236:239], v[48:51], a[80:83]\n" \
"s_cmp_lg_u32 s16, 0\n" \
"s_cselect_b32 s5, s17, s5\n" \
"v_mfma_f32_16x16x32_bf16 a[68:71], v[248:251], a[168:171], a[68:71]\n" \
"v_writelane_b32 v11, s5, 7\n" \
"s_mul_i32 s5, s3, 0x1200\n" \
"v_mfma_f32_16x16x32_bf16 a[68:71], v[48:51], a[232:235], a[68:71]\n" \
"s_waitcnt vmcnt(10) lgkmcnt(0)\n" \
"s_barrier\n" \
"v_xor_b32_e32 v28, 0x20000, v28\n" \
"v_xor_b32_e32 v29, 0x20000, v29\n" \
"v_mfma_f32_16x16x32_bf16 v[52:55], a[176:179], a[128:131], v[52:55]\n" \
"s_add_u32 m0, s40, 0x8000\n" \
"s_add_u32 s85, s39, 0xe000\n" \
"v_mfma_f32_16x16x32_bf16 v[52:55], a[240:243], a[192:195], v[52:55]\n" \
"buffer_load_dwordx4 v20, s[80:83], s85 offen sc1 lds\n" \
"v_mfma_f32_16x16x32_bf16 v[56:59], a[180:183], a[128:131], v[56:59]\n" \
"ds_read_b64_tr_b16 a[160:161], v32 offset:0\n" \
"s_add_i32 s4, s5, s4\n" \
"v_mfma_f32_16x16x32_bf16 v[56:59], a[244:247], a[192:195], v[56:59]\n" \
"ds_read_b64_tr_b16 a[162:163], v32 offset:512\n" \
"s_add_i32 s4, s4, s11\n" \
"v_mfma_f32_16x16x32_bf16 v[60:63], a[184:187], a[128:131], v[60:63]\n" \
"s_add_u32 m0, s40, 0x9000\n" \
"s_add_u32 s85, s39, 0xf000\n" \
"v_mfma_f32_16x16x32_bf16 v[60:63], a[248:251], a[192:195], v[60:63]\n" \
"buffer_load_dwordx4 v20, s[80:83], s85 offen sc1 lds\n" \
"v_mfma_f32_16x16x32_bf16 v[64:67], a[188:191], a[128:131], v[64:67]\n" \
"ds_read_b64_tr_b16 a[224:225], v32 offset:4096\n" \
"v_writelane_b32 v11, s4, 8\n" \
"v_mfma_f32_16x16x32_bf16 v[64:67], a[252:255], a[192:195], v[64:67]\n" \
"ds_read_b128 a[128:131], v28 offset:0\n" \
"s_lshl_b32 s4, s2, 16\n" \
"v_mfma_f32_16x16x32_bf16 v[80:83], a[188:191], a[132:135], v[80:83]\n" \
"ds_read_b128 a[192:195], v29 offset:0\n" \
"s_add_i32 s5, s4, 0x10000\n" \
"v_mfma_f32_16x16x32_bf16 v[80:83], a[252:255], a[196:199], v[80:83]\n" \
"s_add_u32 m0, s40, 0xb000\n" \
"s_movk_i32 s84, 0x380\n" \
"v_mfma_f32_16x16x32_bf16 v[76:79], a[184:187], a[132:135], v[76:79]\n" \
"buffer_load_dwordx4 v21, s[76:79], s84 offen offset:0 lds\n" \
"v_mfma_f32_16x16x32_bf16 v[76:79], a[248:251], a[196:199], v[76:79]\n" \
"ds_read_b64_tr_b16 a[226:227], v32 offset:4608\n" \
"s_and_b32 s5, s5, 0x70000\n" \
"v_mfma_f32_16x16x32_bf16 v[72:75], a[180:183], a[132:135], v[72:75]\n" \
"ds_read_b64_tr_b16 a[164:165], v33 offset:0\n" \
"s_add_i32 s5, s13, s5\n" \
"v_mfma_f32_16x16x32_bf16 v[72:75], a[244:247], a[196:199], v[72:75]\n" \
"v_writelane_b32 v11, s5, 46\n" \
"s_add_i32 s5, s4, 0x20000\n" \
"v_mfma_f32_16x16x32_bf16 v[68:71], a[176:179], a[132:135], v[68:71]\n" \
"ds_read_b64_tr_b16 a[166:167], v33 offset:512\n" \
"s_and_b32 s5, s5, 0x70000\n" \
"v_mfma_f32_16x16x32_bf16 v[68:71], a[240:243], a[196:199], v[68:71]\n" \
"ds_read_b128 a[132:135], v28 offset:2048\n" \
"s_add_i32 s5, s13, s5\n" \
"v_mfma_f32_16x16x32_bf16 v[84:87], a[176:179], a[136:139], v[84:87]\n" \
"ds_read_b128 a[196:199], v29 offset:2048\n" \
"s_bitset1_b32 s5, 11\n" \
"v_mfma_f32_16x16x32_bf16 v[84:87], a[240:243], a[200:203], v[84:87]\n" \
"s_add_u32 m0, s31, 0x8000\n" \
"s_mov_b32 s84, 0x380000\n" \
"v_mfma_f32_16x16x32_bf16 v[88:91], a[180:183], a[136:139], v[88:91]\n" \
"buffer_load_dwordx4 v26, s[68:71], s84 offen offset:0 lds\n" \
"v_mfma_f32_16x16x32_bf16 v[88:91], a[244:247], a[200:203], v[88:91]\n" \
"s_add_u32 s89, s42, 0x0\n" \
"buffer_store_dwordx4 v[184:187], v20, s[80:83], s89 offen offset:1024 sc1\n" \
"v_mfma_f32_16x16x32_bf16 v[92:95], a[184:187], a[136:139], v[92:95]\n" \
"ds_read_b64_tr_b16 a[228:229], v33 offset:4096\n" \
"v_writelane_b32 v11, s5, 47\n" \
"v_mfma_f32_16x16x32_bf16 v[92:95], a[248:251], a[200:203], v[92:95]\n" \
"ds_read_b64_tr_b16 a[230:231], v33 offset:4608\n" \
"s_add_i32 s5, s4, 0x30000\n" \
"v_mfma_f32_16x16x32_bf16 v[96:99], a[188:191], a[136:139], v[96:99]\n" \
"s_and_b32 s5, s5, 0x70000\n" \
"s_add_i32 s5, s13, s5\n" \
"v_mfma_f32_16x16x32_bf16 v[96:99], a[252:255], a[200:203], v[96:99]\n" \
"ds_read_b128 a[136:139], v28 offset:4096\n" \
"s_bitset1_b32 s5, 12\n" \
"v_mfma_f32_16x16x32_bf16 v[112:115], a[188:191], a[140:143], v[112:115]\n" \
"ds_read_b128 a[200:203], v29 offset:4096\n" \
"v_writelane_b32 v11, s5, 48\n" \
"v_mfma_f32_16x16x32_bf16 v[112:115], a[252:255], a[204:207], v[112:115]\n" \
"s_addk_i32 s84, 0x7c00\n" \
"v_mfma_f32_16x16x32_bf16 v[108:111], a[184:187], a[140:143], v[108:111]\n" \
"buffer_load_dwordx4 v26, s[68:71], s84 offen offset:1024 lds\n" \
"v_mfma_f32_16x16x32_bf16 v[108:111], a[248:251], a[204:207], v[108:111]\n" \
"ds_read_b64_tr_b16 a[172:173], v35 offset:0\n" \
"s_and_b32 s5, s4, 0x70000\n" \
"v_mfma_f32_16x16x32_bf16 v[104:107], a[180:183], a[140:143], v[104:107]\n" \
"s_addk_i32 s84, 0x7c00\n" \
"v_mfma_f32_16x16x32_bf16 v[104:107], a[244:247], a[204:207], v[104:107]\n" \
"buffer_load_dwordx4 v27, s[68:71], s84 offen offset:2048 lds\n" \
"v_mfma_f32_16x16x32_bf16 v[100:103], a[176:179], a[140:143], v[100:103]\n" \
"ds_read_b64_tr_b16 a[174:175], v35 offset:512\n" \
"s_xor_b32 s5, s5, 0x40000\n" \
"v_mfma_f32_16x16x32_bf16 v[100:103], a[240:243], a[204:207], v[100:103]\n" \
"ds_read_b128 a[140:143], v28 offset:6144\n" \
"s_add_i32 s5, s13, s5\n" \
"v_mfma_f32_16x16x32_bf16 v[116:119], a[176:179], a[144:147], v[116:119]\n" \
"ds_read_b128 a[204:207], v29 offset:6144\n" \
"s_or_b32 s5, s5, 0x1800\n" \
"v_mfma_f32_16x16x32_bf16 v[116:119], a[240:243], a[208:211], v[116:119]\n" \
"s_addk_i32 s84, 0x7c00\n" \
"v_mfma_f32_16x16x32_bf16 v[120:123], a[180:183], a[144:147], v[120:123]\n" \
"buffer_load_dwordx4 v27, s[68:71], s84 offen offset:3072 lds\n" \
"v_mfma_f32_16x16x32_bf16 v[120:123], a[244:247], a[208:211], v[120:123]\n" \
"ds_read_b64_tr_b16 a[236:237], v35 offset:4096\n" \
"v_writelane_b32 v11, s5, 49\n" \
"v_mfma_f32_16x16x32_bf16 v[124:127], a[184:187], a[144:147], v[124:127]\n" \
"ds_read_b64_tr_b16 a[238:239], v35 offset:4608\n" \
"s_add_i32 s5, s4, 0x50000\n" \
"v_mfma_f32_16x16x32_bf16 v[124:127], a[248:251], a[208:211], v[124:127]\n" \
"s_add_u32 m0, s30, 0x20000\n" \
"s_movk_i32 s84, 0x400\n" \
"v_mfma_f32_16x16x32_bf16 v[128:131], a[188:191], a[144:147], v[128:131]\n" \
"buffer_load_dwordx4 v24, s[76:79], s84 offen offset:0 lds\n" \
"v_mfma_f32_16x16x32_bf16 v[128:131], a[252:255], a[208:211], v[128:131]\n" \
"ds_read_b128 a[144:147], v28 offset:8192\n" \
"s_and_b32 s5, s5, 0x70000\n" \
"v_mfma_f32_16x16x32_bf16 v[144:147], a[188:191], a[148:151], v[144:147]\n" \
"ds_read_b128 a[208:211], v29 offset:8192\n" \
"s_add_i32 s5, s13, s5\n" \
"v_mfma_f32_16x16x32_bf16 v[144:147], a[252:255], a[212:215], v[144:147]\n" \
"ds_read_b64_tr_b16 a[168:169], v34 offset:0\n" \
"s_bitset1_b32 s5, 13\n" \
"v_mfma_f32_16x16x32_bf16 v[140:143], a[184:187], a[148:151], v[140:143]\n" \
"s_movk_i32 s84, 0x2400\n" \
"s_nop 0\n" \
"v_mfma_f32_16x16x32_bf16 v[140:143], a[248:251], a[212:215], v[140:143]\n" \
"buffer_load_dwordx4 v25, s[76:79], s84 offen offset:1024 lds\n" \
"v_mfma_f32_16x16x32_bf16 v[136:139], a[180:183], a[148:151], v[136:139]\n" \
"ds_read_b64_tr_b16 a[170:171], v34 offset:512\n" \
"v_writelane_b32 v11, s5, 50\n" \
"v_mfma_f32_16x16x32_bf16 v[136:139], a[244:247], a[212:215], v[136:139]\n" \
"ds_read_b64_tr_b16 a[232:233], v34 offset:4096\n" \
"s_add_i32 s5, s4, 0x60000\n" \
"v_mfma_f32_16x16x32_bf16 v[132:135], a[176:179], a[148:151], v[132:135]\n" \
"ds_read_b64_tr_b16 a[234:235], v34 offset:4608\n" \
"s_and_b32 s5, s5, 0x70000\n" \
"v_mfma_f32_16x16x32_bf16 v[132:135], a[240:243], a[212:215], v[132:135]\n" \
"ds_read_b128 a[148:151], v28 offset:10240\n" \
"s_add_i32 s4, s4, 0x70000\n" \
"v_mfma_f32_16x16x32_bf16 v[148:151], a[176:179], a[152:155], v[148:151]\n" \
"ds_read_b128 a[212:215], v29 offset:10240\n" \
"s_add_i32 s5, s13, s5\n" \
"v_mfma_f32_16x16x32_bf16 v[148:151], a[240:243], a[216:219], v[148:151]\n" \
"s_movk_i32 s84, 0x4400\n" \
"v_mfma_f32_16x16x32_bf16 v[152:155], a[180:183], a[152:155], v[152:155]\n" \
"buffer_load_dwordx4 v24, s[76:79], s84 offen offset:2048 lds\n" \
"v_mfma_f32_16x16x32_bf16 v[152:155], a[244:247], a[216:219], v[152:155]\n" \
"s_movk_i32 s84, 0x6400\n" \
"s_nop 0\n" \
"v_mfma_f32_16x16x32_bf16 v[156:159], a[184:187], a[152:155], v[156:159]\n" \
"buffer_load_dwordx4 v25, s[76:79], s84 offen offset:3072 lds\n" \
"v_mfma_f32_16x16x32_bf16 v[156:159], a[248:251], a[216:219], v[156:159]\n" \
"ds_read_b128 v[248:251], v22 offset:0\n" \
"s_and_b32 s4, s4, 0x70000\n" \
"v_mfma_f32_16x16x32_bf16 v[160:163], a[188:191], a[152:155], v[160:163]\n" \
"ds_read_b128 v[48:51], v23 offset:0\n" \
"s_or_b32 s5, s5, 0x2800\n" \
"v_mfma_f32_16x16x32_bf16 v[160:163], a[252:255], a[216:219], v[160:163]\n" \
"ds_read_b128 a[152:155], v28 offset:12288\n" \
"v_writelane_b32 v11, s5, 51\n" \
"v_mfma_f32_16x16x32_bf16 v[176:179], a[188:191], a[156:159], v[176:179]\n" \
"ds_read_b128 a[216:219], v29 offset:12288\n" \
"s_add_i32 s13, s13, s4\n" \
"v_mfma_f32_16x16x32_bf16 v[176:179], a[252:255], a[220:223], v[176:179]\n" \
"s_or_b32 s4, s13, 0x3000\n" \
"v_writelane_b32 v11, s4, 52\n" \
"v_mfma_f32_16x16x32_bf16 v[172:175], a[184:187], a[156:159], v[172:175]\n" \
"s_movk_i32 s11, 0x4000\n" \
"s_lshl_b32 s4, s2, 2\n" \
"v_mfma_f32_16x16x32_bf16 v[172:175], a[248:251], a[220:223], v[172:175]\n" \
"s_and_b32 s4, s4, 28\n" \
"s_bfe_u32 s5, s2, 0x20003\n" \
"v_mfma_f32_16x16x32_bf16 v[168:171], a[180:183], a[156:159], v[168:171]\n" \
"s_or_b32 s4, s4, s5\n" \
"s_lshr_b32 s5, s2, 5\n" \
"v_mfma_f32_16x16x32_bf16 v[168:171], a[244:247], a[220:223], v[168:171]\n" \
"s_lshr_b32 s19, s3, 1\n" \
"s_lshl_b32 s18, s5, 1\n" \
"v_mfma_f32_16x16x32_bf16 v[164:167], a[176:179], a[156:159], v[164:167]\n" \
"s_lshl_b32 s6, s4, 20\n" \
"s_lshl_b32 s7, s4, 8\n" \
"v_mfma_f32_16x16x32_bf16 v[164:167], a[240:243], a[220:223], v[164:167]\n" \
"ds_read_b128 a[156:159], v28 offset:14336\n" \
"ds_read_b128 a[220:223], v29 offset:14336\n" \
"s_waitcnt lgkmcnt(15)\n" \
"v_mfma_f32_16x16x32_bf16 a[0:3], a[160:163], a[128:131], a[0:3]\n" \
"s_add_u32 m0, s30, 0x21000\n" \
"s_addk_i32 s84, 0x3000\n" \
"v_mfma_f32_16x16x32_bf16 a[0:3], a[224:227], a[192:195], a[0:3]\n" \
"buffer_load_dwordx4 v24, s[76:79], s84 offen offset:0 lds\n" \
"v_mfma_f32_16x16x32_bf16 a[4:7], a[164:167], a[128:131], a[4:7]\n" \
"ds_read_b64_tr_b16 a[176:177], v36 offset:0\n" \
"s_and_b32 s13, s3, 1\n" \
"v_mfma_f32_16x16x32_bf16 a[4:7], a[228:231], a[192:195], a[4:7]\n" \
"ds_read_b64_tr_b16 a[178:179], v36 offset:1024\n" \
"s_add_i32 s14, s18, s19\n" \
"v_mfma_f32_16x16x32_bf16 a[12:15], a[172:175], a[128:131], a[12:15]\n" \
"ds_read_b64_tr_b16 a[240:241], v36 offset:8192\n" \
"s_waitcnt lgkmcnt(15)\n" \
"v_mfma_f32_16x16x32_bf16 a[12:15], a[236:239], a[192:195], a[12:15]\n" \
"s_add_u32 s88, s42, 0x1000\n" \
"buffer_store_dwordx4 v[188:191], v20, s[80:83], s88 offen offset:1024 sc1\n" \
"s_waitcnt lgkmcnt(13)\n" \
"v_mfma_f32_16x16x32_bf16 a[8:11], a[128:131], a[168:171], a[8:11]\n" \
"ds_read_b64_tr_b16 a[242:243], v36 offset:9216\n" \
"s_waitcnt lgkmcnt(12)\n" \
"v_mfma_f32_16x16x32_bf16 a[8:11], a[192:195], a[232:235], a[8:11]\n" \
"ds_read_b64_tr_b16 a[180:181], v37 offset:0\n" \
"v_mfma_f32_16x16x32_bf16 a[24:27], a[132:135], a[168:171], a[24:27]\n" \
"ds_read_b64_tr_b16 a[182:183], v37 offset:1024\n" \
"v_mfma_f32_16x16x32_bf16 a[24:27], a[196:199], a[232:235], a[24:27]\n" \
"ds_read_b64_tr_b16 a[244:245], v37 offset:8192\n" \
"v_mfma_f32_16x16x32_bf16 a[28:31], a[172:175], a[132:135], a[28:31]\n" \
"s_add_u32 s6, s26, s6\n" \
"s_addc_u32 s9, s27, 0\n" \
"v_mfma_f32_16x16x32_bf16 a[28:31], a[236:239], a[196:199], a[28:31]\n" \
"s_addk_i32 s84, 0x2000\n" \
"v_mfma_f32_16x16x32_bf16 a[20:23], a[164:167], a[132:135], a[20:23]\n" \
"buffer_load_dwordx4 v25, s[76:79], s84 offen offset:1024 lds\n" \
"v_mfma_f32_16x16x32_bf16 a[20:23], a[228:231], a[196:199], a[20:23]\n" \
"ds_read_b64_tr_b16 a[246:247], v37 offset:9216\n" \
"v_mfma_f32_16x16x32_bf16 a[16:19], a[160:163], a[132:135], a[16:19]\n" \
"ds_read_b64_tr_b16 a[184:185], v38 offset:0\n" \
"v_mfma_f32_16x16x32_bf16 a[16:19], a[224:227], a[196:199], a[16:19]\n" \
"ds_read_b64_tr_b16 a[186:187], v38 offset:1024\n" \
"v_mfma_f32_16x16x32_bf16 a[32:35], a[160:163], a[136:139], a[32:35]\n" \
"s_addk_i32 s84, 0x2000\n" \
"s_nop 0\n" \
"v_mfma_f32_16x16x32_bf16 a[32:35], a[224:227], a[200:203], a[32:35]\n" \
"buffer_load_dwordx4 v24, s[76:79], s84 offen offset:2048 lds\n" \
"v_mfma_f32_16x16x32_bf16 a[36:39], a[164:167], a[136:139], a[36:39]\n" \
"ds_read_b64_tr_b16 a[248:249], v38 offset:8192\n" \
"v_mfma_f32_16x16x32_bf16 a[36:39], a[228:231], a[200:203], a[36:39]\n" \
"ds_read_b64_tr_b16 a[250:251], v38 offset:9216\n" \
"v_mfma_f32_16x16x32_bf16 a[44:47], a[172:175], a[136:139], a[44:47]\n" \
"ds_read_b64_tr_b16 a[188:189], v39 offset:0\n" \
"v_mfma_f32_16x16x32_bf16 a[44:47], a[236:239], a[200:203], a[44:47]\n" \
"ds_read_b64_tr_b16 a[190:191], v39 offset:1024\n" \
"v_mfma_f32_16x16x32_bf16 a[40:43], a[136:139], a[168:171], a[40:43]\n" \
"s_add_u32 s89, s42, 0x2000\n" \
"buffer_store_dwordx4 v[192:195], v20, s[80:83], s89 offen offset:0 sc1\n" \
"v_mfma_f32_16x16x32_bf16 a[40:43], a[200:203], a[232:235], a[40:43]\n" \
"ds_read_b64_tr_b16 a[252:253], v39 offset:8192\n" \
"v_mfma_f32_16x16x32_bf16 a[56:59], a[140:143], a[168:171], a[56:59]\n" \
"ds_read_b64_tr_b16 a[254:255], v39 offset:9216\n" \
"v_mfma_f32_16x16x32_bf16 a[56:59], a[204:207], a[232:235], a[56:59]\n" \
"s_add_u32 s8, s6, 0x1000000\n" \
"s_addc_u32 s6, s9, 0\n" \
"v_mfma_f32_16x16x32_bf16 a[60:63], a[172:175], a[140:143], a[60:63]\n" \
"s_and_b32 s9, s6, 0xffff\n" \
"s_lshl_b32 s6, s5, 16\n" \
"v_mfma_f32_16x16x32_bf16 a[60:63], a[236:239], a[204:207], a[60:63]\n" \
"s_addk_i32 s84, 0x2000\n" \
"v_mfma_f32_16x16x32_bf16 a[52:55], a[164:167], a[140:143], a[52:55]\n" \
"buffer_load_dwordx4 v25, s[76:79], s84 offen offset:3072 lds\n" \
"v_mfma_f32_16x16x32_bf16 a[52:55], a[228:231], a[204:207], a[52:55]\n" \
"s_sub_i32 s10, 0x100000, s6\n" \
"s_mov_b32 s6, 0x8000\n" \
"v_mfma_f32_16x16x32_bf16 a[48:51], a[160:163], a[140:143], a[48:51]\n" \
"s_cmp_lg_u32 s5, 0\n" \
"s_cselect_b32 s17, s11, s6\n" \
"v_mfma_f32_16x16x32_bf16 a[48:51], a[224:227], a[204:207], a[48:51]\n" \
"s_lshl_b32 s6, s3, 1\n" \
"s_lshl_b32 s5, s5, 3\n" \
"s_waitcnt lgkmcnt(15)\n" \
"v_mfma_f32_16x16x32_bf16 a[64:67], a[164:167], v[248:251], a[64:67]\n" \
"s_and_b32 s6, s6, 4\n" \
"s_or_b32 s12, s5, s6\n" \
".fill (L_pb + 32480 - .) / 4, 4, 0xBF800000\n" \
"L_pp8:\n" \
"s_cbranch_execz L_pret\n" \
"v_mfma_f32_16x16x32_bf16 a[64:67], a[228:231], v[48:51], a[64:67]\n" \
"s_sub_i32 s5, s13, s5\n" \
"s_or_b32 s5, s5, s6\n" \
"v_mfma_f32_16x16x32_bf16 a[80:83], a[172:175], v[248:251], a[80:83]\n" \
"s_or_b32 s6, s12, s13\n" \
"s_lshl_b32 s4, s4, 2\n" \
"v_mfma_f32_16x16x32_bf16 a[80:83], a[236:239], v[48:51], a[80:83]\n" \
"s_mulk_i32 s6, 0x92\n" \
"s_add_i32 s4, s4, 18\n" \
"v_mfma_f32_16x16x32_bf16 a[68:71], v[248:251], a[168:171], a[68:71]\n" \
"s_mulk_i32 s5, 0x92\n" \
"s_add_i32 s6, s4, s6\n" \
"v_mfma_f32_16x16x32_bf16 a[68:71], v[48:51], a[232:235], a[68:71]\n" \
"s_waitcnt vmcnt(10) lgkmcnt(0)\n" \
"s_barrier\n" \
"v_mfma_f32_16x16x32_bf16 v[52:55], a[176:179], a[128:131], v[52:55]\n" \
"s_add_u32 m0, s40, 0x0\n" \
"s_add_u32 s85, s39, 0x10000\n" \
"v_mfma_f32_16x16x32_bf16 v[52:55], a[240:243], a[192:195], v[52:55]\n" \
"buffer_load_dwordx4 v20, s[80:83], s85 offen sc1 lds\n" \
"v_mfma_f32_16x16x32_bf16 v[56:59], a[180:183], a[128:131], v[56:59]\n" \
"ds_read_b64_tr_b16 a[160:161], v32 offset:32768\n" \
"s_add_i32 s5, s5, s4\n" \
"v_mfma_f32_16x16x32_bf16 v[56:59], a[244:247], a[192:195], v[56:59]\n" \
"ds_read_b64_tr_b16 a[162:163], v32 offset:33280\n" \
"s_lshl_b32 s4, s3, 11\n" \
"v_mfma_f32_16x16x32_bf16 v[60:63], a[184:187], a[128:131], v[60:63]\n" \
"s_lshl_b32 s15, s13, 12\n" \
"s_lshl_b32 s16, s19, 14\n" \
"v_mfma_f32_16x16x32_bf16 v[60:63], a[248:251], a[192:195], v[60:63]\n" \
"ds_read_b64_tr_b16 a[224:225], v32 offset:36864\n" \
"s_mul_i32 s19, s19, 0xe000\n" \
"v_mfma_f32_16x16x32_bf16 v[64:67], a[188:191], a[128:131], v[64:67]\n" \
"s_mov_b32 s11, 0x20000\n" \
"s_or_b32 s12, s4, 0xc000\n" \
"v_mfma_f32_16x16x32_bf16 v[64:67], a[252:255], a[192:195], v[64:67]\n" \
"ds_read_b128 a[128:131], v28 offset:32768\n" \
"s_lshl_b32 s4, s3, 8\n" \
"v_mfma_f32_16x16x32_bf16 v[80:83], a[188:191], a[132:135], v[80:83]\n" \
"ds_read_b128 a[192:195], v29 offset:32768\n" \
"s_or_b32 s22, s15, s16\n" \
"v_mfma_f32_16x16x32_bf16 v[80:83], a[252:255], a[196:199], v[80:83]\n" \
"s_add_u32 m0, s40, 0x1000\n" \
"s_add_u32 s85, s39, 0x11000\n" \
"v_mfma_f32_16x16x32_bf16 v[76:79], a[184:187], a[132:135], v[76:79]\n" \
"buffer_load_dwordx4 v20, s[80:83], s85 offen sc1 lds\n" \
"v_mfma_f32_16x16x32_bf16 v[76:79], a[248:251], a[196:199], v[76:79]\n" \
"ds_read_b64_tr_b16 a[226:227], v32 offset:37376\n" \
"s_or_b32 s23, s19, s15\n" \
"v_mfma_f32_16x16x32_bf16 v[72:75], a[180:183], a[132:135], v[72:75]\n" \
"ds_read_b64_tr_b16 a[164:165], v33 offset:32768\n" \
"s_lshl_b32 s60, s6, 10\n" \
"v_mfma_f32_16x16x32_bf16 v[72:75], a[244:247], a[196:199], v[72:75]\n" \
"s_lshl_b32 s61, s5, 10\n" \
"s_lshl_b32 s14, s14, 15\n" \
"v_mfma_f32_16x16x32_bf16 v[68:71], a[176:179], a[132:135], v[68:71]\n" \
"ds_read_b64_tr_b16 a[166:167], v33 offset:33280\n" \
"s_or_b32 s20, s4, 0x26c00\n" \
"v_mfma_f32_16x16x32_bf16 v[68:71], a[240:243], a[196:199], v[68:71]\n" \
"ds_read_b128 a[132:135], v28 offset:34816\n" \
"s_or_b32 s13, s22, 0x8000\n" \
"v_mfma_f32_16x16x32_bf16 v[84:87], a[176:179], a[136:139], v[84:87]\n" \
"ds_read_b128 a[196:199], v29 offset:34816\n" \
"s_add_i32 s22, s22, 0x14000\n" \
"v_mfma_f32_16x16x32_bf16 v[84:87], a[240:243], a[200:203], v[84:87]\n" \
"s_add_u32 m0, s40, 0x3000\n" \
"s_movk_i32 s84, 0x400\n" \
"v_mfma_f32_16x16x32_bf16 v[88:91], a[180:183], a[136:139], v[88:91]\n" \
"buffer_load_dwordx4 v21, s[76:79], s84 offen offset:0 lds\n" \
"v_mfma_f32_16x16x32_bf16 v[88:91], a[244:247], a[200:203], v[88:91]\n" \
"ds_read_b64_tr_b16 a[228:229], v33 offset:36864\n" \
"s_bitset1_b32 s23, 16\n" \
"v_mfma_f32_16x16x32_bf16 v[92:95], a[184:187], a[136:139], v[92:95]\n" \
"ds_read_b64_tr_b16 a[230:231], v33 offset:37376\n" \
"s_or_b32 s24, s7, 0x5be8020\n" \
"v_mfma_f32_16x16x32_bf16 v[92:95], a[248:251], a[200:203], v[92:95]\n" \
"s_add_u32 s88, s42, 0x2000\n" \
"buffer_store_dwordx4 v[216:219], v20, s[80:83], s88 offen offset:1024 sc1\n" \
"v_mfma_f32_16x16x32_bf16 v[96:99], a[188:191], a[136:139], v[96:99]\n" \
"s_and_b32 s57, s27, 0xffff\n" \
"s_mov_b32 s58, 0x6000000\n" \
"v_mfma_f32_16x16x32_bf16 v[96:99], a[252:255], a[200:203], v[96:99]\n" \
"ds_read_b128 a[136:139], v28 offset:36864\n" \
"s_mov_b32 s56, s26\n" \
"v_mfma_f32_16x16x32_bf16 v[112:115], a[188:191], a[140:143], v[112:115]\n" \
"ds_read_b128 a[200:203], v29 offset:36864\n" \
"s_mov_b32 s59, s11\n" \
"v_mfma_f32_16x16x32_bf16 v[112:115], a[252:255], a[204:207], v[112:115]\n" \
"ds_read_b64_tr_b16 a[172:173], v35 offset:32768\n" \
"s_add_i32 s60, s60, 0x3000000\n" \
"v_mfma_f32_16x16x32_bf16 v[108:111], a[184:187], a[140:143], v[108:111]\n" \
"ds_read_b64_tr_b16 a[174:175], v35 offset:33280\n" \
"s_add_i32 s61, s61, 0x411c000\n" \
"v_mfma_f32_16x16x32_bf16 v[108:111], a[248:251], a[204:207], v[108:111]\n" \
"s_sub_i32 s19, 14, s18\n" \
"s_mov_b32 s21, 0x71800000\n" \
"v_mfma_f32_16x16x32_bf16 v[104:107], a[180:183], a[140:143], v[104:107]\n" \
"s_add_u32 m0, s31, 0x0\n" \
"s_mov_b32 s84, 0x400000\n" \
"v_mfma_f32_16x16x32_bf16 v[104:107], a[244:247], a[204:207], v[104:107]\n" \
"buffer_load_dwordx4 v26, s[68:71], s84 offen offset:0 lds\n" \
"v_mfma_f32_16x16x32_bf16 v[100:103], a[176:179], a[140:143], v[100:103]\n" \
"ds_read_b64_tr_b16 a[236:237], v35 offset:36864\n" \
"s_mov_b32 s4, L_gsw-L_gtr\n" \
"v_mfma_f32_16x16x32_bf16 v[100:103], a[240:243], a[204:207], v[100:103]\n" \
"ds_read_b128 a[140:143], v28 offset:38912\n" \
"v_mfma_f32_16x16x32_bf16 v[116:119], a[176:179], a[144:147], v[116:119]\n" \
"ds_read_b128 a[204:207], v29 offset:38912\n" \
"v_mfma_f32_16x16x32_bf16 v[116:119], a[240:243], a[208:211], v[116:119]\n" \
"ds_read_b64_tr_b16 a[238:239], v35 offset:37376\n" \
"v_mfma_f32_16x16x32_bf16 v[120:123], a[180:183], a[144:147], v[120:123]\n" \
"ds_read_b64_tr_b16 a[168:169], v34 offset:32768\n" \
"v_mfma_f32_16x16x32_bf16 v[120:123], a[244:247], a[208:211], v[120:123]\n" \
"s_addk_i32 s84, 0x7c00\n" \
"v_mfma_f32_16x16x32_bf16 v[124:127], a[184:187], a[144:147], v[124:127]\n" \
"buffer_load_dwordx4 v26, s[68:71], s84 offen offset:1024 lds\n" \
"v_mfma_f32_16x16x32_bf16 v[124:127], a[248:251], a[208:211], v[124:127]\n" \
"ds_read_b64_tr_b16 a[170:171], v34 offset:33280\n" \
"v_mfma_f32_16x16x32_bf16 v[128:131], a[188:191], a[144:147], v[128:131]\n" \
"s_cmp_eq_u32 s19, 0\n" \
"s_cselect_b32 s4, L_gs7-L_gtr, s4\n" \
"v_mfma_f32_16x16x32_bf16 v[128:131], a[252:255], a[208:211], v[128:131]\n" \
"ds_read_b128 a[144:147], v28 offset:40960\n" \
"v_mfma_f32_16x16x32_bf16 v[144:147], a[188:191], a[148:151], v[144:147]\n" \
"ds_read_b128 a[208:211], v29 offset:40960\n" \
"v_mfma_f32_16x16x32_bf16 v[144:147], a[252:255], a[212:215], v[144:147]\n" \
"ds_read_b64_tr_b16 a[232:233], v34 offset:36864\n" \
"v_mfma_f32_16x16x32_bf16 v[140:143], a[184:187], a[148:151], v[140:143]\n" \
"s_addk_i32 s84, 0x7c00\n" \
"v_mfma_f32_16x16x32_bf16 v[140:143], a[248:251], a[212:215], v[140:143]\n" \
"buffer_load_dwordx4 v27, s[68:71], s84 offen offset:2048 lds\n" \
"v_mfma_f32_16x16x32_bf16 v[136:139], a[180:183], a[148:151], v[136:139]\n" \
"ds_read_b64_tr_b16 a[234:235], v34 offset:37376\n" \
"v_mfma_f32_16x16x32_bf16 v[136:139], a[244:247], a[212:215], v[136:139]\n" \
"s_cmp_eq_u32 s16, 0\n" \
"s_cselect_b32 s4, 0, s4\n" \
"v_mfma_f32_16x16x32_bf16 v[132:135], a[176:179], a[148:151], v[132:135]\n" \
"ds_read_b128 v[248:251], v22 offset:32768\n" \
"v_mfma_f32_16x16x32_bf16 v[132:135], a[240:243], a[212:215], v[132:135]\n" \
"ds_read_b128 a[148:151], v28 offset:43008\n" \
"v_mfma_f32_16x16x32_bf16 v[148:151], a[176:179], a[152:155], v[148:151]\n" \
"ds_read_b128 a[212:215], v29 offset:43008\n" \
"v_mfma_f32_16x16x32_bf16 v[148:151], a[240:243], a[216:219], v[148:151]\n" \
"s_addk_i32 s84, 0x7c00\n" \
"v_mfma_f32_16x16x32_bf16 v[152:155], a[180:183], a[152:155], v[152:155]\n" \
"buffer_load_dwordx4 v27, s[68:71], s84 offen offset:3072 lds\n" \
"v_mfma_f32_16x16x32_bf16 v[152:155], a[244:247], a[216:219], v[152:155]\n" \
"ds_read_b128 v[48:51], v23 offset:32768\n" \
"v_mfma_f32_16x16x32_bf16 v[156:159], a[184:187], a[152:155], v[156:159]\n" \
"s_getpc_b64 s[28:29]\n" \
"L_gp0_%=:\n" \
"s_add_u32 s28, s28, L_gtr-L_gp0_%=\n" \
"s_addc_u32 s29, s29, 0\n" \
"v_mfma_f32_16x16x32_bf16 v[156:159], a[248:251], a[216:219], v[156:159]\n" \
"s_add_u32 s28, s28, s4\n" \
"s_addc_u32 s29, s29, 0\n" \
"v_mfma_f32_16x16x32_bf16 v[160:163], a[188:191], a[152:155], v[160:163]\n" \
"v_mfma_f32_16x16x32_bf16 v[160:163], a[252:255], a[216:219], v[160:163]\n" \
"ds_read_b128 a[152:155], v28 offset:45056\n" \
"v_mfma_f32_16x16x32_bf16 v[176:179], a[188:191], a[156:159], v[176:179]\n" \
"ds_read_b128 a[216:219], v29 offset:45056\n" \
"v_mfma_f32_16x16x32_bf16 v[176:179], a[252:255], a[220:223], v[176:179]\n" \
"v_mfma_f32_16x16x32_bf16 v[172:175], a[184:187], a[156:159], v[172:175]\n" \
"v_mfma_f32_16x16x32_bf16 v[172:175], a[248:251], a[220:223], v[172:175]\n" \
"v_mfma_f32_16x16x32_bf16 v[168:171], a[180:183], a[156:159], v[168:171]\n" \
"v_mfma_f32_16x16x32_bf16 v[168:171], a[244:247], a[220:223], v[168:171]\n" \
"v_mfma_f32_16x16x32_bf16 v[164:167], a[176:179], a[156:159], v[164:167]\n" \
"v_mfma_f32_16x16x32_bf16 v[164:167], a[240:243], a[220:223], v[164:167]\n" \
"ds_read_b128 a[156:159], v28 offset:47104\n" \
"ds_read_b128 a[220:223], v29 offset:47104\n" \
"s_waitcnt lgkmcnt(15)\n" \
"s_nop 0\n" \
"v_mfma_f32_16x16x32_bf16 a[0:3], a[160:163], a[128:131], a[0:3]\n" \
"s_add_u32 s89, s42, 0x3000\n" \
"buffer_store_dwordx4 v[220:223], v20, s[80:83], s89 offen offset:0 sc1\n" \
"v_mfma_f32_16x16x32_bf16 a[0:3], a[224:227], a[192:195], a[0:3]\n" \
"ds_read_b64_tr_b16 a[176:177], v36 offset:32768\n" \
"v_mfma_f32_16x16x32_bf16 a[4:7], a[164:167], a[128:131], a[4:7]\n" \
"ds_read_b64_tr_b16 a[178:179], v36 offset:33792\n" \
"v_mfma_f32_16x16x32_bf16 a[4:7], a[228:231], a[192:195], a[4:7]\n" \
"ds_read_b64_tr_b16 a[240:241], v36 offset:40960\n" \
"v_mfma_f32_16x16x32_bf16 a[12:15], a[172:175], a[128:131], a[12:15]\n" \
"ds_read_b64_tr_b16 a[242:243], v36 offset:41984\n" \
"s_waitcnt lgkmcnt(15)\n" \
"v_mfma_f32_16x16x32_bf16 a[12:15], a[236:239], a[192:195], a[12:15]\n" \
"v_mfma_f32_16x16x32_bf16 a[8:11], a[128:131], a[168:171], a[8:11]\n" \
"ds_read_b64_tr_b16 a[180:181], v37 offset:32768\n" \
"s_waitcnt lgkmcnt(13)\n" \
"v_mfma_f32_16x16x32_bf16 a[8:11], a[192:195], a[232:235], a[8:11]\n" \
"v_mfma_f32_16x16x32_bf16 a[24:27], a[132:135], a[168:171], a[24:27]\n" \
"ds_read_b64_tr_b16 a[182:183], v37 offset:33792\n" \
"v_mfma_f32_16x16x32_bf16 a[24:27], a[196:199], a[232:235], a[24:27]\n" \
"ds_read_b64_tr_b16 a[244:245], v37 offset:40960\n" \
"v_mfma_f32_16x16x32_bf16 a[28:31], a[172:175], a[132:135], a[28:31]\n" \
"v_mfma_f32_16x16x32_bf16 a[28:31], a[236:239], a[196:199], a[28:31]\n" \
"ds_read_b64_tr_b16 a[246:247], v37 offset:41984\n" \
"v_mfma_f32_16x16x32_bf16 a[20:23], a[164:167], a[132:135], a[20:23]\n" \
"v_mfma_f32_16x16x32_bf16 a[20:23], a[228:231], a[196:199], a[20:23]\n" \
"ds_read_b64_tr_b16 a[184:185], v38 offset:32768\n" \
"v_mfma_f32_16x16x32_bf16 a[16:19], a[160:163], a[132:135], a[16:19]\n" \
"ds_read_b64_tr_b16 a[186:187], v38 offset:33792\n" \
"v_mfma_f32_16x16x32_bf16 a[16:19], a[224:227], a[196:199], a[16:19]\n" \
"v_mfma_f32_16x16x32_bf16 a[32:35], a[160:163], a[136:139], a[32:35]\n" \
"s_add_u32 s88, s42, 0x3000\n" \
"buffer_store_dwordx4 v[224:227], v20, s[80:83], s88 offen offset:1024 sc1\n" \
"v_mfma_f32_16x16x32_bf16 a[32:35], a[224:227], a[200:203], a[32:35]\n" \
"ds_read_b64_tr_b16 a[248:249], v38 offset:40960\n" \
"v_mfma_f32_16x16x32_bf16 a[36:39], a[164:167], a[136:139], a[36:39]\n" \
"v_lshlrev_b32_e32 v45, 3, v20\n" \
"v_add_u32_e32 v45, s30, v45\n" \
"v_mfma_f32_16x16x32_bf16 a[36:39], a[228:231], a[200:203], a[36:39]\n" \
"s_getpc_b64 s[86:87]\n" \
"L_ct0_%=:\n" \
"s_add_u32 s86, s86, L_ap0-L_ct0_%=\n" \
"s_addc_u32 s87, s87, 0\n" \
"s_add_u32 m0, s40, 0x2000\n" \
"v_mfma_f32_16x16x32_bf16 a[44:47], a[172:175], a[136:139], a[44:47]\n" \
"global_load_lds_dword v45, s[86:87]\n" \
"v_mfma_f32_16x16x32_bf16 a[44:47], a[236:239], a[200:203], a[44:47]\n" \
"ds_read_b64_tr_b16 a[250:251], v38 offset:41984\n" \
"v_mfma_f32_16x16x32_bf16 a[40:43], a[136:139], a[168:171], a[40:43]\n" \
"ds_read_b64_tr_b16 a[188:189], v39 offset:32768\n" \
"v_mfma_f32_16x16x32_bf16 a[40:43], a[200:203], a[232:235], a[40:43]\n" \
"ds_read_b64_tr_b16 a[190:191], v39 offset:33792\n" \
"v_mfma_f32_16x16x32_bf16 a[56:59], a[140:143], a[168:171], a[56:59]\n" \
"ds_read_b64_tr_b16 a[252:253], v39 offset:40960\n" \
"v_mfma_f32_16x16x32_bf16 a[56:59], a[204:207], a[232:235], a[56:59]\n" \
"ds_read_b64_tr_b16 a[254:255], v39 offset:41984\n" \
"v_mfma_f32_16x16x32_bf16 a[60:63], a[172:175], a[140:143], a[60:63]\n" \
"v_mfma_f32_16x16x32_bf16 a[60:63], a[236:239], a[204:207], a[60:63]\n" \
"v_mfma_f32_16x16x32_bf16 a[52:55], a[164:167], a[140:143], a[52:55]\n" \
"v_mfma_f32_16x16x32_bf16 a[52:55], a[228:231], a[204:207], a[52:55]\n" \
"v_mfma_f32_16x16x32_bf16 a[48:51], a[160:163], a[140:143], a[48:51]\n" \
"v_mfma_f32_16x16x32_bf16 a[48:51], a[224:227], a[204:207], a[48:51]\n" \
"s_waitcnt lgkmcnt(15)\n" \
"v_mfma_f32_16x16x32_bf16 a[64:67], a[164:167], v[248:251], a[64:67]\n" \
"v_mfma_f32_16x16x32_bf16 a[64:67], a[228:231], v[48:51], a[64:67]\n" \
"v_mfma_f32_16x16x32_bf16 a[80:83], a[172:175], v[248:251], a[80:83]\n" \
"v_mfma_f32_16x16x32_bf16 a[80:83], a[236:239], v[48:51], a[80:83]\n" \
"v_mfma_f32_16x16x32_bf16 a[68:71], v[248:251], a[168:171], a[68:71]\n" \
"v_mfma_f32_16x16x32_bf16 a[68:71], v[48:51], a[232:235], a[68:71]\n" \
"s_waitcnt vmcnt(3) lgkmcnt(0)\n" \
"s_barrier\n" \
"s_nop 0\n" \
"v_xor_b32_e32 v28, 0x20000, v28\n" \
"v_xor_b32_e32 v29, 0x20000, v29\n" \
"v_mfma_f32_16x16x32_bf16 v[52:55], a[176:179], a[128:131], v[52:55]\n" \
"ds_read_b64_tr_b16 a[160:161], v32 offset:0\n" \
"v_accvgpr_read_b32 v184, a0\n" \
"v_mfma_f32_16x16x32_bf16 v[52:55], a[240:243], a[192:195], v[52:55]\n" \
"v_accvgpr_read_b32 v185, a1\n" \
"v_accvgpr_read_b32 v186, a2\n" \
"v_mfma_f32_16x16x32_bf16 v[56:59], a[180:183], a[128:131], v[56:59]\n" \
"ds_read_b64_tr_b16 a[162:163], v32 offset:512\n" \
"v_accvgpr_read_b32 v187, a3\n" \
"v_mfma_f32_16x16x32_bf16 v[56:59], a[244:247], a[192:195], v[56:59]\n" \
"v_accvgpr_read_b32 v188, a4\n" \
"v_accvgpr_read_b32 v189, a5\n" \
"v_mfma_f32_16x16x32_bf16 v[60:63], a[184:187], a[128:131], v[60:63]\n" \
"v_accvgpr_read_b32 v190, a6\n" \
"v_accvgpr_read_b32 v191, a7\n" \
"v_mfma_f32_16x16x32_bf16 v[60:63], a[248:251], a[192:195], v[60:63]\n" \
"ds_read_b64_tr_b16 a[224:225], v32 offset:4096\n" \
"v_accvgpr_read_b32 v196, a12\n" \
"v_mfma_f32_16x16x32_bf16 v[64:67], a[188:191], a[128:131], v[64:67]\n" \
"v_accvgpr_read_b32 v197, a13\n" \
"v_accvgpr_read_b32 v198, a14\n" \
"v_mfma_f32_16x16x32_bf16 v[64:67], a[252:255], a[192:195], v[64:67]\n" \
"ds_read_b128 a[128:131], v28 offset:0\n" \
"v_accvgpr_read_b32 v199, a15\n" \
"v_mfma_f32_16x16x32_bf16 v[80:83], a[188:191], a[132:135], v[80:83]\n" \
"ds_read_b128 a[192:195], v29 offset:0\n" \
"v_accvgpr_read_b32 v192, a8\n" \
"v_mfma_f32_16x16x32_bf16 v[80:83], a[252:255], a[196:199], v[80:83]\n" \
"ds_read_b64_tr_b16 a[226:227], v32 offset:4608\n" \
"v_accvgpr_read_b32 v193, a9\n" \
"v_mfma_f32_16x16x32_bf16 v[76:79], a[184:187], a[132:135], v[76:79]\n" \
"v_accvgpr_read_b32 v194, a10\n" \
"v_accvgpr_read_b32 v195, a11\n" \
"v_mfma_f32_16x16x32_bf16 v[76:79], a[248:251], a[196:199], v[76:79]\n" \
"ds_read_b64_tr_b16 a[164:165], v33 offset:0\n" \
"v_accvgpr_read_b32 v208, a24\n" \
"v_mfma_f32_16x16x32_bf16 v[72:75], a[180:183], a[132:135], v[72:75]\n" \
"v_accvgpr_read_b32 v209, a25\n" \
"v_accvgpr_read_b32 v210, a26\n" \
"v_mfma_f32_16x16x32_bf16 v[72:75], a[244:247], a[196:199], v[72:75]\n" \
"v_accvgpr_read_b32 v211, a27\n" \
"v_accvgpr_read_b32 v212, a28\n" \
"v_mfma_f32_16x16x32_bf16 v[68:71], a[176:179], a[132:135], v[68:71]\n" \
"ds_read_b64_tr_b16 a[166:167], v33 offset:512\n" \
"v_accvgpr_read_b32 v213, a29\n" \
"v_mfma_f32_16x16x32_bf16 v[68:71], a[240:243], a[196:199], v[68:71]\n" \
"ds_read_b128 a[132:135], v28 offset:2048\n" \
"v_accvgpr_read_b32 v214, a30\n" \
"v_mfma_f32_16x16x32_bf16 v[84:87], a[176:179], a[136:139], v[84:87]\n" \
"ds_read_b128 a[196:199], v29 offset:2048\n" \
"v_accvgpr_read_b32 v215, a31\n" \
"v_mfma_f32_16x16x32_bf16 v[84:87], a[240:243], a[200:203], v[84:87]\n" \
"ds_read_b64_tr_b16 a[228:229], v33 offset:4096\n" \
"v_accvgpr_read_b32 v204, a20\n" \
"v_mfma_f32_16x16x32_bf16 v[88:91], a[180:183], a[136:139], v[88:91]\n" \
"v_accvgpr_read_b32 v205, a21\n" \
"v_accvgpr_read_b32 v206, a22\n" \
"v_mfma_f32_16x16x32_bf16 v[88:91], a[244:247], a[200:203], v[88:91]\n" \
"v_accvgpr_read_b32 v207, a23\n" \
"v_accvgpr_read_b32 v200, a16\n" \
"v_mfma_f32_16x16x32_bf16 v[92:95], a[184:187], a[136:139], v[92:95]\n" \
"ds_read_b64_tr_b16 a[230:231], v33 offset:4608\n" \
"v_accvgpr_read_b32 v201, a17\n" \
"v_mfma_f32_16x16x32_bf16 v[92:95], a[248:251], a[200:203], v[92:95]\n" \
"v_accvgpr_read_b32 v202, a18\n" \
"v_accvgpr_read_b32 v203, a19\n" \
"v_mfma_f32_16x16x32_bf16 v[96:99], a[188:191], a[136:139], v[96:99]\n" \
"v_accvgpr_read_b32 v216, a32\n" \
"v_accvgpr_read_b32 v217, a33\n" \
"v_mfma_f32_16x16x32_bf16 v[96:99], a[252:255], a[200:203], v[96:99]\n" \
"ds_read_b128 a[136:139], v28 offset:4096\n" \
"v_accvgpr_read_b32 v218, a34\n" \
"v_mfma_f32_16x16x32_bf16 v[112:115], a[188:191], a[140:143], v[112:115]\n" \
"ds_read_b128 a[200:203], v29 offset:4096\n" \
"v_accvgpr_read_b32 v219, a35\n" \
"v_mfma_f32_16x16x32_bf16 v[112:115], a[252:255], a[204:207], v[112:115]\n" \
"ds_read_b64_tr_b16 a[172:173], v35 offset:0\n" \
"v_accvgpr_read_b32 v220, a36\n" \
"v_mfma_f32_16x16x32_bf16 v[108:111], a[184:187], a[140:143], v[108:111]\n" \
"ds_read_b64_tr_b16 a[174:175], v35 offset:512\n" \
"v_accvgpr_read_b32 v221, a37\n" \
"v_mfma_f32_16x16x32_bf16 v[108:111], a[248:251], a[204:207], v[108:111]\n" \
"v_accvgpr_read_b32 v222, a38\n" \
"v_accvgpr_read_b32 v223, a39\n" \
"v_mfma_f32_16x16x32_bf16 v[104:107], a[180:183], a[140:143], v[104:107]\n" \
"ds_read_b64_tr_b16 a[236:237], v35 offset:4096\n" \
"v_accvgpr_read_b32 v228, a44\n" \
"v_mfma_f32_16x16x32_bf16 v[104:107], a[244:247], a[204:207], v[104:107]\n" \
"v_accvgpr_read_b32 v229, a45\n" \
"v_accvgpr_read_b32 v230, a46\n" \
"v_mfma_f32_16x16x32_bf16 v[100:103], a[176:179], a[140:143], v[100:103]\n" \
"v_accvgpr_read_b32 v231, a47\n" \
"v_accvgpr_read_b32 v224, a40\n" \
"v_mfma_f32_16x16x32_bf16 v[100:103], a[240:243], a[204:207], v[100:103]\n" \
"ds_read_b128 a[140:143], v28 offset:6144\n" \
"v_accvgpr_read_b32 v225, a41\n" \
"v_mfma_f32_16x16x32_bf16 v[116:119], a[176:179], a[144:147], v[116:119]\n" \
"ds_read_b128 a[204:207], v29 offset:6144\n" \
"v_accvgpr_read_b32 v226, a42\n" \
"v_mfma_f32_16x16x32_bf16 v[116:119], a[240:243], a[208:211], v[116:119]\n" \
"ds_read_b64_tr_b16 a[238:239], v35 offset:4608\n" \
"v_accvgpr_read_b32 v227, a43\n" \
"v_mfma_f32_16x16x32_bf16 v[120:123], a[180:183], a[144:147], v[120:123]\n" \
"ds_read_b64_tr_b16 a[168:169], v34 offset:0\n" \
"v_accvgpr_read_b32 v240, a56\n" \
"v_mfma_f32_16x16x32_bf16 v[120:123], a[244:247], a[208:211], v[120:123]\n" \
"v_accvgpr_read_b32 v241, a57\n" \
"v_accvgpr_read_b32 v242, a58\n" \
"v_mfma_f32_16x16x32_bf16 v[124:127], a[184:187], a[144:147], v[124:127]\n" \
"v_accvgpr_read_b32 v243, a59\n" \
"v_accvgpr_read_b32 v244, a60\n" \
"v_mfma_f32_16x16x32_bf16 v[124:127], a[248:251], a[208:211], v[124:127]\n" \
"ds_read_b64_tr_b16 a[170:171], v34 offset:512\n" \
"v_accvgpr_read_b32 v245, a61\n" \
"v_mfma_f32_16x16x32_bf16 v[128:131], a[188:191], a[144:147], v[128:131]\n" \
"v_accvgpr_read_b32 v246, a62\n" \
"v_accvgpr_read_b32 v247, a63\n" \
"v_mfma_f32_16x16x32_bf16 v[128:131], a[252:255], a[208:211], v[128:131]\n" \
"ds_read_b128 a[144:147], v28 offset:8192\n" \
"v_accvgpr_read_b32 v236, a52\n" \
"v_mfma_f32_16x16x32_bf16 v[144:147], a[188:191], a[148:151], v[144:147]\n" \
"ds_read_b128 a[208:211], v29 offset:8192\n" \
"v_accvgpr_read_b32 v237, a53\n" \
"v_mfma_f32_16x16x32_bf16 v[144:147], a[252:255], a[212:215], v[144:147]\n" \
"ds_read_b64_tr_b16 a[232:233], v34 offset:4096\n" \
"v_accvgpr_read_b32 v238, a54\n" \
"v_mfma_f32_16x16x32_bf16 v[140:143], a[184:187], a[148:151], v[140:143]\n" \
"v_accvgpr_read_b32 v239, a55\n" \
"v_accvgpr_read_b32 v232, a48\n" \
"v_mfma_f32_16x16x32_bf16 v[140:143], a[248:251], a[212:215], v[140:143]\n" \
"ds_read_b64_tr_b16 a[234:235], v34 offset:4608\n" \
"v_accvgpr_read_b32 v233, a49\n" \
"v_mfma_f32_16x16x32_bf16 v[136:139], a[180:183], a[148:151], v[136:139]\n" \
"v_accvgpr_read_b32 v234, a50\n" \
"v_accvgpr_read_b32 v235, a51\n" \
"v_mfma_f32_16x16x32_bf16 v[136:139], a[244:247], a[212:215], v[136:139]\n" \
"v_accvgpr_read_b32 v180, a64\n" \
"v_accvgpr_read_b32 v181, a65\n" \
"v_mfma_f32_16x16x32_bf16 v[132:135], a[176:179], a[148:151], v[132:135]\n" \
"ds_read_b128 v[248:251], v22 offset:0\n" \
"v_accvgpr_read_b32 v182, a66\n" \
"v_mfma_f32_16x16x32_bf16 v[132:135], a[240:243], a[212:215], v[132:135]\n" \
"ds_read_b128 a[148:151], v28 offset:10240\n" \
"v_accvgpr_read_b32 v183, a67\n" \
"v_mfma_f32_16x16x32_bf16 v[148:151], a[176:179], a[152:155], v[148:151]\n" \
"ds_read_b128 a[212:215], v29 offset:10240\n" \
"v_accvgpr_read_b32 v252, a68\n" \
"v_mfma_f32_16x16x32_bf16 v[148:151], a[240:243], a[216:219], v[148:151]\n" \
"ds_read_b128 v[48:51], v23 offset:0\n" \
"v_accvgpr_read_b32 v253, a69\n" \
"v_mfma_f32_16x16x32_bf16 v[152:155], a[180:183], a[152:155], v[152:155]\n" \
"v_accvgpr_read_b32 v254, a70\n" \
"v_accvgpr_read_b32 v255, a71\n" \
"v_mfma_f32_16x16x32_bf16 v[152:155], a[244:247], a[216:219], v[152:155]\n" \
"v_lshrrev_b32_e64 v24, 9, v20\n" \
"v_and_b32_e32 v26, 0x1ff, v20\n" \
"v_mfma_f32_16x16x32_bf16 v[156:159], a[184:187], a[152:155], v[156:159]\n" \
"v_lshl_add_u32 v24, v24, 3, v26\n" \
"v_lshrrev_b32_e64 v25, 8, v20\n" \
"v_mfma_f32_16x16x32_bf16 v[156:159], a[248:251], a[216:219], v[156:159]\n" \
"v_mul_u32_u24_e32 v25, 0x24700, v25\n" \
"v_add_u32_e64 v25, v25, v20\n" \
"v_mfma_f32_16x16x32_bf16 v[160:163], a[188:191], a[152:155], v[160:163]\n" \
"v_lshrrev_b32_e32 v26, 2, v20\n" \
"v_add_u32_e32 v26, s45, v26\n" \
"v_mfma_f32_16x16x32_bf16 v[160:163], a[252:255], a[216:219], v[160:163]\n" \
"ds_read_b128 a[152:155], v28 offset:12288\n" \
"v_add_u32_e64 v27, s46, v20\n" \
"v_mfma_f32_16x16x32_bf16 v[176:179], a[188:191], a[156:159], v[176:179]\n" \
"ds_read_b128 a[216:219], v29 offset:12288\n" \
"v_add_u32_e64 v21, s43, v27\n" \
"v_mfma_f32_16x16x32_bf16 v[176:179], a[252:255], a[220:223], v[176:179]\n" \
"v_mfma_f32_16x16x32_bf16 v[172:175], a[184:187], a[156:159], v[172:175]\n" \
"v_mfma_f32_16x16x32_bf16 v[172:175], a[248:251], a[220:223], v[172:175]\n" \
"v_mfma_f32_16x16x32_bf16 v[168:171], a[180:183], a[156:159], v[168:171]\n" \
"v_mfma_f32_16x16x32_bf16 v[168:171], a[244:247], a[220:223], v[168:171]\n" \
"v_mfma_f32_16x16x32_bf16 v[164:167], a[176:179], a[156:159], v[164:167]\n" \
"v_mfma_f32_16x16x32_bf16 v[164:167], a[240:243], a[220:223], v[164:167]\n" \
"ds_read_b128 a[156:159], v28 offset:14336\n" \
"ds_read_b128 a[220:223], v29 offset:14336\n" \
"s_waitcnt lgkmcnt(15)\n" \
"s_nop 0\n" \
"v_mfma_f32_16x16x32_bf16 v[184:187], a[160:163], a[128:131], v[184:187]\n" \
"v_bfe_u32 v44, v20, 2, 5\n" \
"ds_read_b64_tr_b16 a[176:177], v36 offset:0\n" \
"v_mfma_f32_16x16x32_bf16 v[184:187], a[224:227], a[192:195], v[184:187]\n" \
"buffer_load_dword v44, v44, s[80:83], s38 offen offset:0 sc1\n" \
"v_mfma_f32_16x16x32_bf16 v[188:191], a[164:167], a[128:131], v[188:191]\n" \
"ds_read_b64_tr_b16 a[178:179], v36 offset:1024\n" \
"v_mfma_f32_16x16x32_bf16 v[188:191], a[228:231], a[192:195], v[188:191]\n" \
"ds_read_b64_tr_b16 a[240:241], v36 offset:8192\n" \
"v_mfma_f32_16x16x32_bf16 v[196:199], a[172:175], a[128:131], v[196:199]\n" \
"ds_read_b64_tr_b16 a[242:243], v36 offset:9216\n" \
"s_waitcnt lgkmcnt(15)\n" \
"v_mfma_f32_16x16x32_bf16 v[196:199], a[236:239], a[192:195], v[196:199]\n" \
"v_mfma_f32_16x16x32_bf16 v[192:195], a[128:131], a[168:171], v[192:195]\n" \
"ds_read_b64_tr_b16 a[180:181], v37 offset:0\n" \
"s_waitcnt lgkmcnt(13)\n" \
"v_mfma_f32_16x16x32_bf16 v[192:195], a[192:195], a[232:235], v[192:195]\n" \
"v_mfma_f32_16x16x32_bf16 v[208:211], a[132:135], a[168:171], v[208:211]\n" \
"ds_read_b64_tr_b16 a[182:183], v37 offset:1024\n" \
"v_mfma_f32_16x16x32_bf16 v[208:211], a[196:199], a[232:235], v[208:211]\n" \
"ds_read_b64_tr_b16 a[244:245], v37 offset:8192\n" \
"v_mfma_f32_16x16x32_bf16 v[212:215], a[172:175], a[132:135], v[212:215]\n" \
"v_mfma_f32_16x16x32_bf16 v[212:215], a[236:239], a[196:199], v[212:215]\n" \
"ds_read_b64_tr_b16 a[246:247], v37 offset:9216\n" \
"v_cvt_pk_bf16_f32 v188, v188, v189\n" \
"v_mfma_f32_16x16x32_bf16 v[204:207], a[164:167], a[132:135], v[204:207]\n" \
"v_cvt_pk_bf16_f32 v189, v190, v191\n" \
"v_cvt_pk_bf16_f32 v190, v198, v199\n" \
"v_mfma_f32_16x16x32_bf16 v[204:207], a[228:231], a[196:199], v[204:207]\n" \
"ds_read_b64_tr_b16 a[184:185], v38 offset:0\n" \
"v_mov_b32_e32 v191, 0x3f80\n" \
"v_mfma_f32_16x16x32_bf16 v[200:203], a[160:163], a[132:135], v[200:203]\n" \
"ds_read_b64_tr_b16 a[186:187], v38 offset:1024\n" \
"v_cvt_pk_bf16_f32 v184, v184, v185\n" \
"v_mfma_f32_16x16x32_bf16 v[200:203], a[224:227], a[196:199], v[200:203]\n" \
"v_cvt_pk_bf16_f32 v185, v186, v187\n" \
"v_cvt_pk_bf16_f32 v186, v196, v197\n" \
"v_mfma_f32_16x16x32_bf16 v[216:219], a[160:163], a[136:139], v[216:219]\n" \
"ds_read_b64_tr_b16 a[248:249], v38 offset:8192\n" \
"v_mov_b32_e64 v187, 0\n" \
"v_mfma_f32_16x16x32_bf16 v[216:219], a[224:227], a[200:203], v[216:219]\n" \
"ds_read_b64_tr_b16 a[250:251], v38 offset:9216\n" \
"v_mfma_f32_16x16x32_bf16 v[220:223], a[164:167], a[136:139], v[220:223]\n" \
"v_mfma_f32_16x16x32_bf16 v[220:223], a[228:231], a[200:203], v[220:223]\n" \
"ds_read_b64_tr_b16 a[188:189], v39 offset:0\n" \
"v_cvt_pk_bf16_f32 v204, v204, v205\n" \
"v_mfma_f32_16x16x32_bf16 v[228:231], a[172:175], a[136:139], v[228:231]\n" \
"v_cvt_pk_bf16_f32 v205, v206, v207\n" \
"v_cvt_pk_bf16_f32 v206, v214, v215\n" \
"v_mfma_f32_16x16x32_bf16 v[228:231], a[236:239], a[200:203], v[228:231]\n" \
"ds_read_b64_tr_b16 a[190:191], v39 offset:1024\n" \
"v_mov_b32_e32 v207, 0x3f80\n" \
"v_mfma_f32_16x16x32_bf16 v[224:227], a[136:139], a[168:171], v[224:227]\n" \
"ds_read_b64_tr_b16 a[252:253], v39 offset:8192\n" \
"v_cvt_pk_bf16_f32 v200, v200, v201\n" \
"v_mfma_f32_16x16x32_bf16 v[224:227], a[200:203], a[232:235], v[224:227]\n" \
"v_cvt_pk_bf16_f32 v201, v202, v203\n" \
"v_cvt_pk_bf16_f32 v202, v212, v213\n" \
"v_mfma_f32_16x16x32_bf16 v[240:243], a[140:143], a[168:171], v[240:243]\n" \
"ds_read_b64_tr_b16 a[254:255], v39 offset:9216\n" \
"v_mov_b32_e64 v203, 0\n" \
"v_mfma_f32_16x16x32_bf16 v[240:243], a[204:207], a[232:235], v[240:243]\n" \
"v_mfma_f32_16x16x32_bf16 v[244:247], a[172:175], a[140:143], v[244:247]\n" \
"v_mfma_f32_16x16x32_bf16 v[244:247], a[236:239], a[204:207], v[244:247]\n" \
"v_cvt_pk_bf16_f32 v220, v220, v221\n" \
"v_cvt_pk_bf16_f32 v221, v222, v223\n" \
"v_mfma_f32_16x16x32_bf16 v[236:239], a[164:167], a[140:143], v[236:239]\n" \
"v_cvt_pk_bf16_f32 v222, v230, v231\n" \
"v_mov_b32_e32 v223, 0x3f80\n" \
"v_mfma_f32_16x16x32_bf16 v[236:239], a[228:231], a[204:207], v[236:239]\n" \
"v_cvt_pk_bf16_f32 v216, v216, v217\n" \
"v_cvt_pk_bf16_f32 v217, v218, v219\n" \
"v_mfma_f32_16x16x32_bf16 v[232:235], a[160:163], a[140:143], v[232:235]\n" \
"v_cvt_pk_bf16_f32 v218, v228, v229\n" \
"v_mov_b32_e64 v219, 0\n" \
"v_mfma_f32_16x16x32_bf16 v[232:235], a[224:227], a[204:207], v[232:235]\n" \
"s_waitcnt lgkmcnt(15)\n" \
"s_nop 0\n" \
"v_mfma_f32_16x16x32_bf16 v[180:183], a[164:167], v[248:251], v[180:183]\n" \
"v_mfma_f32_16x16x32_bf16 v[180:183], a[228:231], v[48:51], v[180:183]\n" \
"v_mfma_f32_16x16x32_bf16 a[80:83], a[172:175], v[248:251], a[80:83]\n" \
"v_mfma_f32_16x16x32_bf16 a[80:83], a[236:239], v[48:51], a[80:83]\n" \
"v_cvt_pk_bf16_f32 v236, v236, v237\n" \
"v_cvt_pk_bf16_f32 v237, v238, v239\n" \
"v_mfma_f32_16x16x32_bf16 v[252:255], v[248:251], a[168:171], v[252:255]\n" \
"v_cvt_pk_bf16_f32 v238, v246, v247\n" \
"v_mov_b32_e32 v239, 0x3f80\n" \
"v_mfma_f32_16x16x32_bf16 v[252:255], v[48:51], a[232:235], v[252:255]\n" \
"v_cvt_pk_bf16_f32 v232, v232, v233\n" \
"v_cvt_pk_bf16_f32 v233, v234, v235\n" \
"s_waitcnt vmcnt(0) lgkmcnt(0)\n" \
"s_barrier\n" \
"v_mov_b32_e32 v45, s62\n" \
"s_mov_b64 exec, 1\n" \
"buffer_store_dword v45, off, s[80:83], s37 offset:32 sc1\n" \
"s_mov_b64 exec, -1\n" \
"v_cmp_ne_u32_e32 vcc, s62, v44\n" \
"s_nop 4\n" \
"s_cbranch_vccnz L_poll160_%=\n" \
"L_pollr160_%=:\n" \
"v_mfma_f32_16x16x32_bf16 v[52:55], a[176:179], a[128:131], v[52:55]\n" \
"s_mov_b32 s73, s50\n" \
"s_sub_u32 m0, s31, 0x10000\n" \
"v_mfma_f32_16x16x32_bf16 v[52:55], a[240:243], a[192:195], v[52:55]\n" \
"buffer_load_dwordx4 v20, s[80:83], s73 offen offset:0 sc1 lds\n" \
"v_mfma_f32_16x16x32_bf16 v[56:59], a[180:183], a[128:131], v[56:59]\n" \
"s_add_u32 s74, s50, 0x4000\n" \
"s_add_u32 s75, s50, 0x8000\n" \
"v_mfma_f32_16x16x32_bf16 v[56:59], a[244:247], a[192:195], v[56:59]\n" \
"s_mov_b32 s70, 0x3e0293ee\n" \
"s_mov_b32 s76, s52\n" \
"v_mfma_f32_16x16x32_bf16 v[60:63], a[184:187], a[128:131], v[60:63]\n" \
"v_mul_f32_e32 v52, s70, v52\n" \
"v_mul_f32_e32 v53, s70, v53\n" \
"v_mfma_f32_16x16x32_bf16 v[60:63], a[248:251], a[192:195], v[60:63]\n" \
"buffer_load_dwordx4 v20, s[80:83], s73 offen offset:1024 sc1 lds\n" \
"v_mfma_f32_16x16x32_bf16 v[64:67], a[188:191], a[128:131], v[64:67]\n" \
"v_mul_f32_e32 v54, s70, v54\n" \
"v_mul_f32_e32 v55, s70, v55\n" \
"v_mfma_f32_16x16x32_bf16 v[64:67], a[252:255], a[192:195], v[64:67]\n" \
"v_cvt_pk_bf16_f32 v52, v52, v53\n" \
"v_mul_f32_e32 v56, s70, v56\n" \
".fill (L_pb + 36576 - .) / 4, 4, 0xBF800000\n" \
"L_pp9:\n" \
"s_cbranch_execz L_pret\n" \
"v_mfma_f32_16x16x32_bf16 v[80:83], a[188:191], a[132:135], v[80:83]\n" \
"v_mul_f32_e32 v60, s70, v60\n" \
"v_mul_f32_e64 v61, s70, v61\n" \
"v_mfma_f32_16x16x32_bf16 v[80:83], a[252:255], a[196:199], v[80:83]\n" \
"buffer_load_dwordx4 v20, s[80:83], s73 offen offset:2048 sc1 lds\n" \
"v_mfma_f32_16x16x32_bf16 v[76:79], a[184:187], a[132:135], v[76:79]\n" \
"v_mul_f32_e32 v62, s70, v62\n" \
"v_mul_f32_e32 v63, s70, v63\n" \
"v_mfma_f32_16x16x32_bf16 v[76:79], a[248:251], a[196:199], v[76:79]\n" \
"v_cvt_pk_bf16_f32 v60, v60, v61\n" \
"v_mul_f32_e64 v57, s70, v57\n" \
"v_mfma_f32_16x16x32_bf16 v[72:75], a[180:183], a[132:135], v[72:75]\n" \
"v_mul_f32_e32 v58, s70, v58\n" \
"v_mul_f32_e32 v59, s70, v59\n" \
"v_mfma_f32_16x16x32_bf16 v[72:75], a[244:247], a[196:199], v[72:75]\n" \
"buffer_load_dwordx4 v20, s[80:83], s73 offen offset:3072 sc1 lds\n" \
"v_mfma_f32_16x16x32_bf16 v[68:71], a[176:179], a[132:135], v[68:71]\n" \
"s_sub_u32 m0, s31, s51\n" \
"v_cvt_pk_bf16_f32 v53, v54, v55\n" \
"v_mfma_f32_16x16x32_bf16 v[68:71], a[240:243], a[196:199], v[68:71]\n" \
"v_mul_f32_e32 v64, s70, v64\n" \
"v_mul_f32_e64 v65, s70, v65\n" \
"v_mfma_f32_16x16x32_bf16 v[84:87], a[176:179], a[136:139], v[84:87]\n" \
"v_mul_f32_e32 v66, s70, v66\n" \
"v_mul_f32_e32 v67, s70, v67\n" \
"v_mfma_f32_16x16x32_bf16 v[84:87], a[240:243], a[200:203], v[84:87]\n" \
"buffer_load_dwordx4 v20, s[80:83], s74 offen offset:0 sc1 lds\n" \
"v_mfma_f32_16x16x32_bf16 v[88:91], a[180:183], a[136:139], v[88:91]\n" \
"v_cvt_pk_bf16_f32 v61, v62, v63\n" \
"v_cvt_pk_bf16_f32 v54, v56, v57\n" \
"v_mfma_f32_16x16x32_bf16 v[88:91], a[244:247], a[200:203], v[88:91]\n" \
"v_cvt_pk_bf16_f32 v55, v58, v59\n" \
"v_cvt_pk_bf16_f32 v62, v64, v65\n" \
"v_mfma_f32_16x16x32_bf16 v[92:95], a[184:187], a[136:139], v[92:95]\n" \
"v_cvt_pk_bf16_f32 v63, v66, v67\n" \
"v_mul_f32_e64 v68, s70, v68\n" \
"v_mfma_f32_16x16x32_bf16 v[92:95], a[248:251], a[200:203], v[92:95]\n" \
"buffer_load_dwordx4 v20, s[80:83], s74 offen offset:1024 sc1 lds\n" \
"v_mfma_f32_16x16x32_bf16 v[96:99], a[188:191], a[136:139], v[96:99]\n" \
"ds_write_b128 v27, v[52:55] offset:0\n" \
"v_mfma_f32_16x16x32_bf16 v[96:99], a[252:255], a[200:203], v[96:99]\n" \
"v_mul_f32_e32 v69, s70, v69\n" \
"v_mul_f32_e32 v76, s70, v76\n" \
"v_mfma_f32_16x16x32_bf16 v[112:115], a[188:191], a[140:143], v[112:115]\n" \
"v_mul_f32_e32 v77, s70, v77\n" \
"v_mul_f32_e32 v70, s70, v70\n" \
"v_mfma_f32_16x16x32_bf16 v[112:115], a[252:255], a[204:207], v[112:115]\n" \
"buffer_load_dwordx4 v20, s[80:83], s74 offen offset:2048 sc1 lds\n" \
"v_mfma_f32_16x16x32_bf16 v[108:111], a[184:187], a[140:143], v[108:111]\n" \
"ds_write_b128 v27, v[60:63] offset:1024\n" \
"v_mfma_f32_16x16x32_bf16 v[108:111], a[248:251], a[204:207], v[108:111]\n" \
"v_mul_f32_e64 v71, s70, v71\n" \
"v_cvt_pk_bf16_f32 v68, v68, v69\n" \
"v_mfma_f32_16x16x32_bf16 v[104:107], a[180:183], a[140:143], v[104:107]\n" \
"v_mul_f32_e32 v78, s70, v78\n" \
"v_mul_f32_e32 v79, s70, v79\n" \
"v_mfma_f32_16x16x32_bf16 v[104:107], a[244:247], a[204:207], v[104:107]\n" \
"buffer_load_dwordx4 v20, s[80:83], s74 offen offset:3072 sc1 lds\n" \
"v_mfma_f32_16x16x32_bf16 v[100:103], a[176:179], a[140:143], v[100:103]\n" \
"s_sub_u32 m0, s31, 0x4000\n" \
"v_cvt_pk_bf16_f32 v76, v76, v77\n" \
"v_mfma_f32_16x16x32_bf16 v[100:103], a[240:243], a[204:207], v[100:103]\n" \
"v_mul_f32_e32 v72, s70, v72\n" \
"v_mul_f32_e32 v73, s70, v73\n" \
"v_mfma_f32_16x16x32_bf16 v[116:119], a[176:179], a[144:147], v[116:119]\n" \
"v_mul_f32_e32 v74, s70, v74\n" \
"v_mul_f32_e32 v75, s70, v75\n" \
"v_mfma_f32_16x16x32_bf16 v[116:119], a[240:243], a[208:211], v[116:119]\n" \
"buffer_load_dwordx4 v20, s[80:83], s75 offen offset:0 sc1 lds\n" \
"v_mfma_f32_16x16x32_bf16 v[120:123], a[180:183], a[144:147], v[120:123]\n" \
"v_cvt_pk_bf16_f32 v69, v70, v71\n" \
"v_mul_f32_e64 v80, s70, v80\n" \
"v_mfma_f32_16x16x32_bf16 v[120:123], a[244:247], a[208:211], v[120:123]\n" \
"v_mul_f32_e32 v81, s70, v81\n" \
"v_mul_f32_e32 v82, s70, v82\n" \
"v_mfma_f32_16x16x32_bf16 v[124:127], a[184:187], a[144:147], v[124:127]\n" \
"v_mul_f32_e64 v83, s70, v83\n" \
"v_cvt_pk_bf16_f32 v77, v78, v79\n" \
"v_mfma_f32_16x16x32_bf16 v[124:127], a[248:251], a[208:211], v[124:127]\n" \
"buffer_load_dwordx4 v20, s[80:83], s75 offen offset:1024 sc1 lds\n" \
"v_mfma_f32_16x16x32_bf16 v[128:131], a[188:191], a[144:147], v[128:131]\n" \
"v_cvt_pk_bf16_f32 v70, v72, v73\n" \
"v_cvt_pk_bf16_f32 v71, v74, v75\n" \
"v_mfma_f32_16x16x32_bf16 v[128:131], a[252:255], a[208:211], v[128:131]\n" \
"v_cvt_pk_bf16_f32 v78, v80, v81\n" \
"v_cvt_pk_bf16_f32 v79, v82, v83\n" \
"v_mfma_f32_16x16x32_bf16 v[144:147], a[188:191], a[148:151], v[144:147]\n" \
"ds_write_b128 v27, v[68:71] offset:4096\n" \
"v_mfma_f32_16x16x32_bf16 v[144:147], a[252:255], a[212:215], v[144:147]\n" \
"buffer_load_dwordx4 v20, s[80:83], s75 offen offset:2048 sc1 lds\n" \
"v_mfma_f32_16x16x32_bf16 v[140:143], a[184:187], a[148:151], v[140:143]\n" \
"v_cvt_pk_bf16_f32 v180, v180, v181\n" \
"v_accvgpr_read_b32 v50, a82\n" \
"v_mfma_f32_16x16x32_bf16 v[140:143], a[248:251], a[212:215], v[140:143]\n" \
"ds_write_b128 v27, v[76:79] offset:5120\n" \
"v_mfma_f32_16x16x32_bf16 v[136:139], a[180:183], a[148:151], v[136:139]\n" \
"v_accvgpr_read_b32 v51, a83\n" \
"v_cvt_pk_bf16_f32 v181, v182, v183\n" \
"v_mfma_f32_16x16x32_bf16 v[136:139], a[244:247], a[212:215], v[136:139]\n" \
"buffer_load_dwordx4 v20, s[80:83], s75 offen offset:3072 sc1 lds\n" \
"v_mfma_f32_16x16x32_bf16 v[132:135], a[176:179], a[148:151], v[132:135]\n" \
"s_mov_b32 m0, s53\n" \
"v_cvt_pk_bf16_f32 v182, v50, v51\n" \
"v_mfma_f32_16x16x32_bf16 v[132:135], a[240:243], a[212:215], v[132:135]\n" \
"v_mov_b32_e32 v183, 0x3f80\n" \
"v_cvt_pk_bf16_f32 v234, v244, v245\n" \
"v_mfma_f32_16x16x32_bf16 v[148:151], a[176:179], a[152:155], v[148:151]\n" \
"v_mov_b32_e32 v235, 0\n" \
"v_cvt_pk_bf16_f32 v54, v192, v193\n" \
"v_mfma_f32_16x16x32_bf16 v[148:151], a[240:243], a[216:219], v[148:151]\n" \
"buffer_load_dwordx4 v20, s[80:83], s76 offen offset:0 sc1 lds\n" \
"v_mfma_f32_16x16x32_bf16 v[152:155], a[180:183], a[152:155], v[152:155]\n" \
"v_cvt_pk_bf16_f32 v55, v194, v195\n" \
"v_cvt_pk_bf16_f32 v56, v208, v209\n" \
"v_mfma_f32_16x16x32_bf16 v[152:155], a[244:247], a[216:219], v[152:155]\n" \
"v_cvt_pk_bf16_f32 v57, v210, v211\n" \
"v_cvt_pk_bf16_f32 v58, v224, v225\n" \
"v_cvt_pk_bf16_f32 v59, v226, v227\n" \
"v_cvt_pk_bf16_f32 v60, v240, v241\n" \
"v_cvt_pk_bf16_f32 v61, v242, v243\n" \
"v_mfma_f32_16x16x32_bf16 v[32:35], v[180:183], v[184:187], 0\n" \
"buffer_load_dwordx4 v20, s[80:83], s76 offen offset:1024 sc1 lds\n" \
"v_mfma_f32_16x16x32_bf16 v[156:159], a[184:187], a[152:155], v[156:159]\n" \
"v_mfma_f32_16x16x32_bf16 v[192:195], v[188:191], v[184:187], 0\n" \
"v_mfma_f32_16x16x32_bf16 v[208:211], v[204:207], v[200:203], 0\n" \
"v_mfma_f32_16x16x32_bf16 v[36:39], v[188:191], v[200:203], 0\n" \
"v_mfma_f32_16x16x32_bf16 v[224:227], v[220:223], v[216:219], 0\n" \
"v_mfma_f32_16x16x32_bf16 v[248:251], v[204:207], v[216:219], 0\n" \
"v_mfma_f32_16x16x32_bf16 v[240:243], v[236:239], v[232:235], 0\n" \
"v_mfma_f32_16x16x32_bf16 v[64:67], v[220:223], v[232:235], 0\n" \
"s_mov_b32 s64, 0xaaaaaaaa\n" \
"s_mov_b32 s65, 0xaaaaaaaa\n" \
"v_mfma_f32_16x16x32_bf16 v[156:159], a[248:251], a[216:219], v[156:159]\n" \
"v_cndmask_b32_e64 v32, v32, v33, s[64:65]\n" \
"v_cndmask_b32_e64 v34, v34, v35, s[64:65]\n" \
"v_mfma_f32_16x16x32_bf16 v[160:163], a[188:191], a[152:155], v[160:163]\n" \
"s_mov_b32 s66, 0xfff0ffff\n" \
"s_mov_b32 s67, 0xf000ff00\n" \
"s_mov_b32 s68, 0xffc0fffc\n" \
"s_mov_b32 s69, 0xc000fc00\n" \
"v_cndmask_b32_e64 v192, v192, v193, s[64:65]\n" \
"v_add_f32_e64 v32, s47, v32\n" \
"v_cndmask_b32_e64 v194, v194, v195, s[64:65]\n" \
"v_add_f32_e64 v34, s47, v34\n" \
"v_cndmask_b32_e64 v208, v208, v209, s[64:65]\n" \
"v_cndmask_b32_e64 v36, v36, v37, s[64:65]\n" \
"v_cndmask_b32_e64 v210, v210, v211, s[64:65]\n" \
"v_cndmask_b32_e64 v38, v38, v39, s[64:65]\n" \
"v_cndmask_b32_e64 v224, v224, v225, s[64:65]\n" \
"v_cndmask_b32_e64 v248, v248, v249, s[64:65]\n" \
"v_cndmask_b32_e64 v226, v226, v227, s[64:65]\n" \
"v_cndmask_b32_e64 v250, v250, v251, s[64:65]\n" \
"v_cndmask_b32_e64 v240, v240, v241, s[64:65]\n" \
"v_cndmask_b32_e64 v64, v64, v65, s[64:65]\n" \
"v_cndmask_b32_e64 v242, v242, v243, s[64:65]\n" \
"v_cndmask_b32_e64 v66, v66, v67, s[64:65]\n" \
"v_cndmask_b32_e64 v192, v32, v192, s[66:67]\n" \
"v_cndmask_b32_e64 v194, v34, v194, s[68:69]\n" \
"v_cndmask_b32_e64 v208, v36, v208, s[66:67]\n" \
"v_cndmask_b32_e64 v210, v38, v210, s[68:69]\n" \
"v_cndmask_b32_e64 v224, v248, v224, s[66:67]\n" \
"v_cndmask_b32_e64 v226, v250, v226, s[68:69]\n" \
"v_cndmask_b32_e64 v240, v64, v240, s[66:67]\n" \
"v_cndmask_b32_e64 v242, v66, v242, s[68:69]\n" \
"v_mfma_f32_16x16x32_bf16 v[160:163], a[252:255], a[216:219], v[160:163]\n" \
"v_max_f32_e32 v193, v192, v194\n" \
"v_max_f32_e32 v209, v208, v210\n" \
"v_max_f32_e32 v225, v224, v226\n" \
"v_max_f32_e32 v241, v240, v242\n" \
"v_mul_f32_e32 v84, s70, v84\n" \
"v_permlane32_swap_b32_e32 v193, v225\n" \
"v_permlane32_swap_b32_e32 v209, v241\n" \
"v_mul_f32_e32 v85, s70, v85\n" \
"v_mul_f32_e32 v92, s70, v92\n" \
"v_mul_f32_e32 v93, s70, v93\n" \
"v_permlane32_swap_b32_e32 v196, v228\n" \
"v_permlane32_swap_b32_e32 v212, v244\n" \
"v_max_f32_e32 v193, v193, v225\n" \
"v_max_f32_e32 v209, v209, v241\n" \
"v_accvgpr_read_b32 v48, a80\n" \
"v_accvgpr_read_b32 v49, a81\n" \
"v_mul_f32_e32 v86, s70, v86\n" \
"v_mul_f32_e32 v87, s70, v87\n" \
"v_cvt_pk_bf16_f32 v84, v84, v85\n" \
"v_mul_f32_e32 v94, s70, v94\n" \
"v_mul_f32_e32 v95, s70, v95\n" \
"v_cvt_pk_bf16_f32 v92, v92, v93\n" \
"v_permlane16_swap_b32_e32 v228, v244\n" \
"v_permlane16_swap_b32_e32 v193, v209\n" \
"v_mfma_f32_16x16x32_bf16 v[176:179], a[188:191], a[156:159], v[176:179]\n" \
"s_mov_b32 s72, 0x3eae1a93\n" \
"v_mul_f32_e32 v88, s70, v88\n" \
"v_mul_f32_e32 v89, s70, v89\n" \
"v_mul_f32_e32 v90, s70, v90\n" \
"v_mul_f32_e32 v91, s70, v91\n" \
"v_cvt_pk_bf16_f32 v85, v86, v87\n" \
"v_mul_f32_e32 v96, s70, v96\n" \
"v_mul_f32_e32 v97, s70, v97\n" \
"v_mul_f32_e32 v98, s70, v98\n" \
"v_mul_f32_e32 v99, s70, v99\n" \
"v_cvt_pk_bf16_f32 v93, v94, v95\n" \
"v_mul_f32_e32 v45, 0xbfb8aa3b, v228\n" \
"v_mfma_f32_16x16x16_bf16 v[48:51], v[182:183], v[46:47], 0\n" \
"v_mfma_f32_16x16x16_bf16 v[196:199], v[190:191], v[46:47], 0\n" \
"v_mfma_f32_16x16x16_bf16 v[212:215], v[206:207], v[46:47], 0\n" \
"v_max_f32_e64 v193, v193, v209\n" \
"v_cvt_pk_bf16_f32 v86, v88, v89\n" \
"v_cvt_pk_bf16_f32 v87, v90, v91\n" \
"v_cvt_pk_bf16_f32 v94, v96, v97\n" \
"v_cvt_pk_bf16_f32 v95, v98, v99\n" \
"v_mfma_f32_16x16x16_bf16 v[228:231], v[222:223], v[46:47], 0\n" \
"v_mul_f32_e32 v193, s72, v193\n" \
"v_mov_b32_e32 v209, v193\n" \
"ds_write_b128 v27, v[84:87] offset:8192\n" \
"v_mfma_f32_16x16x16_bf16 v[244:247], v[238:239], v[46:47], 0\n" \
"v_permlane16_swap_b32_e32 v193, v209\n" \
"s_mov_b32 s71, 0x3e0293ee\n" \
"v_mfma_f32_16x16x32_bf16 v[176:179], a[252:255], a[220:223], v[176:179]\n" \
"v_mov_b32_e32 v225, v193\n" \
"v_mov_b32_e32 v241, v209\n" \
"v_mul_f32_e32 v100, s70, v100\n" \
"v_mul_f32_e64 v101, s70, v101\n" \
"ds_write_b128 v27, v[92:95] offset:9216\n" \
"v_mul_f32_e32 v108, s70, v108\n" \
"v_mul_f32_e32 v109, s70, v109\n" \
"v_pk_mul_f32 v[116:117], v[116:117], s[70:71]\n" \
"v_pk_mul_f32 v[124:125], v[124:125], s[70:71]\n" \
"v_pk_mul_f32 v[132:133], v[132:133], s[70:71]\n" \
"v_pk_mul_f32 v[140:141], v[140:141], s[70:71]\n" \
"v_pk_mul_f32 v[148:149], v[148:149], s[70:71]\n" \
"v_pk_mul_f32 v[156:157], v[156:157], s[70:71]\n" \
"v_permlane32_swap_b32_e32 v193, v225\n" \
"v_permlane32_swap_b32_e32 v209, v241\n" \
"v_mul_f32_e32 v102, s70, v102\n" \
"v_mul_f32_e32 v103, s70, v103\n" \
"v_cvt_pk_bf16_f32 v100, v100, v101\n" \
"v_mul_f32_e32 v110, s70, v110\n" \
"v_mul_f32_e32 v111, s70, v111\n" \
"v_cvt_pk_bf16_f32 v108, v108, v109\n" \
"v_pk_mul_f32 v[118:119], v[118:119], s[70:71]\n" \
"v_cvt_pk_bf16_f32 v116, v116, v117\n" \
"v_pk_mul_f32 v[126:127], v[126:127], s[70:71]\n" \
"v_cvt_pk_bf16_f32 v124, v124, v125\n" \
"v_pk_mul_f32 v[134:135], v[134:135], s[70:71]\n" \
"v_cvt_pk_bf16_f32 v132, v132, v133\n" \
"v_pk_mul_f32 v[142:143], v[142:143], s[70:71]\n" \
"v_cvt_pk_bf16_f32 v140, v140, v141\n" \
"v_pk_mul_f32 v[150:151], v[150:151], s[70:71]\n" \
"v_cvt_pk_bf16_f32 v148, v148, v149\n" \
"v_pk_mul_f32 v[158:159], v[158:159], s[70:71]\n" \
"v_cvt_pk_bf16_f32 v156, v156, v157\n" \
"v_fma_f32 v192, v192, s72, -v193\n" \
"v_fma_f32 v194, v194, s72, -v193\n" \
"v_fma_f32 v208, v208, s72, -v209\n" \
"v_fma_f32 v210, v210, s72, -v209\n" \
"v_mul_f32_e32 v104, s70, v104\n" \
"v_mul_f32_e32 v105, s70, v105\n" \
"v_mul_f32_e32 v106, s70, v106\n" \
"v_mul_f32_e32 v107, s70, v107\n" \
"v_cvt_pk_bf16_f32 v101, v102, v103\n" \
"v_mul_f32_e32 v112, s70, v112\n" \
"v_mul_f32_e32 v113, s70, v113\n" \
"v_mul_f32_e32 v114, s70, v114\n" \
"v_mul_f32_e32 v115, s70, v115\n" \
"v_cvt_pk_bf16_f32 v109, v110, v111\n" \
"v_pk_mul_f32 v[120:121], v[120:121], s[70:71]\n" \
"v_pk_mul_f32 v[122:123], v[122:123], s[70:71]\n" \
"v_cvt_pk_bf16_f32 v117, v118, v119\n" \
"v_pk_mul_f32 v[128:129], v[128:129], s[70:71]\n" \
"v_pk_mul_f32 v[130:131], v[130:131], s[70:71]\n" \
"v_cvt_pk_bf16_f32 v125, v126, v127\n" \
"v_pk_mul_f32 v[136:137], v[136:137], s[70:71]\n" \
"v_pk_mul_f32 v[138:139], v[138:139], s[70:71]\n" \
"v_cvt_pk_bf16_f32 v133, v134, v135\n" \
"v_pk_mul_f32 v[144:145], v[144:145], s[70:71]\n" \
"v_pk_mul_f32 v[146:147], v[146:147], s[70:71]\n" \
"v_cvt_pk_bf16_f32 v141, v142, v143\n" \
"v_pk_mul_f32 v[152:153], v[152:153], s[70:71]\n" \
"v_pk_mul_f32 v[154:155], v[154:155], s[70:71]\n" \
"v_cvt_pk_bf16_f32 v149, v150, v151\n" \
"v_pk_mul_f32 v[160:161], v[160:161], s[70:71]\n" \
"v_pk_mul_f32 v[162:163], v[162:163], s[70:71]\n" \
"v_cvt_pk_bf16_f32 v157, v158, v159\n" \
"v_pk_mul_f32 v[176:177], v[176:177], s[70:71]\n" \
"v_pk_mul_f32 v[178:179], v[178:179], s[70:71]\n" \
"v_exp_f32_e32 v192, v192\n" \
"v_exp_f32_e32 v194, v194\n" \
"v_exp_f32_e32 v208, v208\n" \
"v_exp_f32_e32 v210, v210\n" \
"v_fma_f32 v224, v224, s72, -v225\n" \
"v_fma_f32 v226, v226, s72, -v225\n" \
"v_mfma_f32_16x16x32_bf16 v[172:175], a[184:187], a[156:159], v[172:175]\n" \
"v_cvt_pk_bf16_f32 v102, v104, v105\n" \
"v_cvt_pk_bf16_f32 v103, v106, v107\n" \
"v_cvt_pk_bf16_f32 v110, v112, v113\n" \
"v_cvt_pk_bf16_f32 v111, v114, v115\n" \
"v_cvt_pk_bf16_f32 v118, v120, v121\n" \
"v_cvt_pk_bf16_f32 v119, v122, v123\n" \
"v_cvt_pk_bf16_f32 v126, v128, v129\n" \
"v_cvt_pk_bf16_f32 v127, v130, v131\n" \
"v_cvt_pk_bf16_f32 v134, v136, v137\n" \
"v_cvt_pk_bf16_f32 v135, v138, v139\n" \
"v_cvt_pk_bf16_f32 v142, v144, v145\n" \
"v_cvt_pk_bf16_f32 v143, v146, v147\n" \
"v_cvt_pk_bf16_f32 v150, v152, v153\n" \
"v_cvt_pk_bf16_f32 v151, v154, v155\n" \
"v_cvt_pk_bf16_f32 v158, v160, v161\n" \
"v_cvt_pk_bf16_f32 v159, v162, v163\n" \
"v_exp_f32_e32 v224, v224\n" \
"v_exp_f32_e32 v226, v226\n" \
"v_fma_f32 v240, v240, s72, -v241\n" \
"v_fma_f32 v242, v242, s72, -v241\n" \
"ds_write_b128 v27, v[100:103] offset:12288\n" \
"v_cvt_pk_bf16_f32 v192, v192, v194\n" \
"v_cvt_pk_bf16_f32 v208, v208, v210\n" \
"v_exp_f32_e32 v240, v240\n" \
"v_exp_f32_e32 v242, v242\n" \
"v_cvt_pk_bf16_f32 v68, v48, v49\n" \
"v_cvt_pk_bf16_f32 v69, v50, v51\n" \
"v_cvt_pk_bf16_f32 v70, v196, v197\n" \
"v_cvt_pk_bf16_f32 v71, v198, v199\n" \
"ds_write_b128 v27, v[108:111] offset:13312\n" \
"v_cvt_pk_bf16_f32 v72, v212, v213\n" \
"v_cvt_pk_bf16_f32 v73, v214, v215\n" \
"v_perm_b32 v184, v192, v192, v40\n" \
"v_perm_b32 v185, v192, v192, v41\n" \
"v_perm_b32 v186, v192, v192, v42\n" \
"v_perm_b32 v187, v192, v192, v43\n" \
"v_perm_b32 v200, v208, v208, v40\n" \
"v_perm_b32 v201, v208, v208, v41\n" \
"v_perm_b32 v202, v208, v208, v42\n" \
"v_perm_b32 v203, v208, v208, v43\n" \
"ds_write_b128 v21, v[116:119] offset:16384\n" \
"v_cvt_pk_bf16_f32 v224, v224, v226\n" \
"v_cvt_pk_bf16_f32 v74, v228, v229\n" \
"v_cvt_pk_bf16_f32 v75, v230, v231\n" \
"v_mfma_f32_16x16x32_bf16 v[32:35], v[68:71], v[184:187], 0\n" \
"v_mfma_f32_16x16x32_bf16 v[36:39], v[70:73], v[200:203], 0\n" \
"v_perm_b32 v216, v224, v224, v40\n" \
"v_perm_b32 v217, v224, v224, v41\n" \
"ds_write_b128 v21, v[124:127] offset:17408\n" \
"v_perm_b32 v218, v224, v224, v42\n" \
"v_perm_b32 v219, v224, v224, v43\n" \
"v_cvt_pk_bf16_f32 v240, v240, v242\n" \
"v_mfma_f32_16x16x32_bf16 v[172:175], a[248:251], a[220:223], v[172:175]\n" \
"v_cvt_pk_bf16_f32 v76, v244, v245\n" \
"v_cvt_pk_bf16_f32 v77, v246, v247\n" \
"v_mfma_f32_16x16x32_bf16 v[248:251], v[72:75], v[216:219], 0\n" \
"v_perm_b32 v232, v240, v240, v40\n" \
"v_perm_b32 v233, v240, v240, v41\n" \
"ds_write_b128 v21, v[132:135] offset:20480\n" \
"v_perm_b32 v234, v240, v240, v42\n" \
"v_perm_b32 v235, v240, v240, v43\n" \
"v_pk_mul_f32 v[172:173], v[172:173], s[70:71]\n" \
"v_pk_mul_f32 v[174:175], v[174:175], s[70:71]\n" \
"v_cvt_pk_bf16_f32 v172, v172, v173\n" \
"v_cvt_pk_bf16_f32 v173, v174, v175\n" \
"v_cvt_pk_bf16_f32 v174, v176, v177\n" \
"v_cvt_pk_bf16_f32 v175, v178, v179\n" \
"v_mfma_f32_16x16x32_bf16 v[64:67], v[74:77], v[232:235], 0\n" \
"ds_write_b128 v21, v[140:143] offset:21504\n" \
"v_mfma_f32_16x16x32_bf16 v[168:171], a[180:183], a[156:159], v[168:171]\n" \
"v_cvt_pk_bf16_f32 v52, v252, v253\n" \
"v_cvt_pk_bf16_f32 v53, v254, v255\n" \
"v_mfma_f32_16x16x32_bf16 v[168:171], a[244:247], a[220:223], v[168:171]\n" \
"v_exp_f32_e64 v45, v45\n" \
"v_mfma_f32_16x16x32_bf16 v[192:195], v[52:55], v[184:187], 0\n" \
"ds_write_b128 v21, v[148:151] offset:24576\n" \
"v_mfma_f32_16x16x32_bf16 v[208:211], v[54:57], v[200:203], 0\n" \
"v_mfma_f32_16x16x32_bf16 v[224:227], v[56:59], v[216:219], 0\n" \
"v_mfma_f32_16x16x32_bf16 v[240:243], v[58:61], v[232:235], 0\n" \
"ds_write_b128 v21, v[156:159] offset:25600\n" \
"v_mov_b32_dpp v34, v38 quad_perm:[0,1,2,3] row_mask:0x2 bank_mask:0xf\n" \
"v_pk_mul_f32 v[168:169], v[168:169], s[70:71]\n" \
"v_pk_mul_f32 v[170:171], v[170:171], s[70:71]\n" \
"v_mov_b32_dpp v34, v250 quad_perm:[0,1,2,3] row_mask:0x4 bank_mask:0xf\n" \
"v_add_f32_e64 v45, 1.0, v45\n" \
"v_mfma_f32_16x16x32_bf16 v[164:167], a[176:179], a[156:159], v[164:167]\n" \
"v_mov_b32_dpp v34, v66 quad_perm:[0,1,2,3] row_mask:0x8 bank_mask:0xf\n" \
"v_mov_b32_dpp v32, v36 quad_perm:[0,1,2,3] row_mask:0x2 bank_mask:0xf\n" \
"v_rcp_f32_e64 v45, v45\n" \
"ds_write_b128 v21, v[172:175] offset:29696\n" \
"v_mov_b32_dpp v33, v37 quad_perm:[0,1,2,3] row_mask:0x2 bank_mask:0xf\n" \
"v_rcp_f32_e64 v34, v34\n" \
"v_mov_b32_dpp v32, v248 quad_perm:[0,1,2,3] row_mask:0x4 bank_mask:0xf\n" \
"v_mov_b32_dpp v33, v249 quad_perm:[0,1,2,3] row_mask:0x4 bank_mask:0xf\n" \
"v_mul_f32_e64 v34, v45, v34\n" \
"v_mov_b32_dpp v32, v64 quad_perm:[0,1,2,3] row_mask:0x8 bank_mask:0xf\n" \
"v_mov_b32_dpp v33, v65 quad_perm:[0,1,2,3] row_mask:0x8 bank_mask:0xf\n" \
"v_mul_f32_e32 v32, v34, v32\n" \
"v_mul_f32_e32 v33, v34, v33\n" \
"v_mov_b32_e64 v38, v34\n" \
"v_mfma_f32_16x16x32_bf16 v[164:167], a[240:243], a[220:223], v[164:167]\n" \
"v_sub_f32_e32 v44, 1.0, v45\n" \
"v_permlane16_swap_b32_e32 v34, v38\n" \
"v_mov_b32_e32 v250, v34\n" \
"v_mov_b32_e32 v66, v38\n" \
"v_cvt_pk_bf16_f32 v32, v32, v33\n" \
"v_permlane32_swap_b32_e32 v34, v250\n" \
"v_permlane32_swap_b32_e32 v38, v66\n" \
"v_mul_f32_e64 v192, v34, v192\n" \
"v_pk_mul_f32 v[164:165], v[164:165], s[70:71]\n" \
"v_pk_mul_f32 v[166:167], v[166:167], s[70:71]\n" \
"v_cvt_pk_bf16_f32 v164, v164, v165\n" \
"v_cvt_pk_bf16_f32 v165, v166, v167\n" \
"v_cvt_pk_bf16_f32 v166, v168, v169\n" \
"v_cvt_pk_bf16_f32 v167, v170, v171\n" \
"ds_write_b128 v21, v[164:167] offset:28672\n" \
"v_mul_f32_e32 v193, v34, v193\n" \
"v_mul_f32_e32 v208, v38, v208\n" \
"v_mul_f32_e32 v209, v38, v209\n" \
"v_mul_f32_e32 v224, v250, v224\n" \
"v_mul_f32_e32 v225, v250, v225\n" \
"v_mul_f32_e32 v240, v66, v240\n" \
"v_mul_f32_e32 v241, v66, v241\n" \
"v_mul_f32_e32 v194, v34, v194\n" \
"v_mul_f32_e64 v195, v34, v195\n" \
"v_cvt_pk_bf16_f32 v192, v192, v193\n" \
"v_mul_f32_e32 v210, v38, v210\n" \
"v_mul_f32_e32 v211, v38, v211\n" \
"v_cvt_pk_bf16_f32 v208, v208, v209\n" \
"v_mul_f32_e32 v226, v250, v226\n" \
"v_mul_f32_e32 v227, v250, v227\n" \
"v_cvt_pk_bf16_f32 v224, v224, v225\n" \
"v_mul_f32_e32 v242, v66, v242\n" \
"v_mul_f32_e32 v243, v66, v243\n" \
"v_cvt_pk_bf16_f32 v240, v240, v241\n" \
"v_cvt_pk_bf16_f32 v193, v194, v195\n" \
"s_add_u32 s84, s48, 0x0\n" \
"s_nop 0\n" \
"v_cvt_pk_bf16_f32 v209, v210, v211\n" \
"s_add_u32 s85, s48, 0x24800\n" \
"v_cvt_pk_bf16_f32 v225, v226, v227\n" \
"s_add_u32 s86, s48, 0x49000\n" \
"v_cvt_pk_bf16_f32 v241, v242, v243\n" \
"s_add_u32 s87, s48, 0x6d800\n" \
"ds_write_b32 v26, v44\n" \
"buffer_store_dword v32, v25, s[80:83], s49 offen sc1\n" \
"buffer_store_dwordx2 v[192:193], v24, s[80:83], s84 offen sc1\n" \
"buffer_store_dwordx2 v[208:209], v24, s[80:83], s85 offen sc1\n" \
"buffer_store_dwordx2 v[224:225], v24, s[80:83], s86 offen sc1\n" \
"buffer_store_dwordx2 v[240:241], v24, s[80:83], s87 offen sc1\n" \
"s_branch L_end_%=\n" \
"L_poll60_%=:\n" \
"s_mov_b32 s84, 0\n" \
"L_pl60_%=:\n" \
"s_sleep 1\n" \
"v_bfe_u32 v44, v20, 2, 5\n" \
"buffer_load_dword v44, v44, s[80:83], s38 offen offset:128 sc1\n" \
"s_waitcnt vmcnt(0)\n" \
"v_cmp_ne_u32_e32 vcc, s62, v44\n" \
"s_add_u32 s84, s84, 1\n" \
"s_nop 3\n" \
"s_cbranch_vccz L_pollr60_%=\n" \
"s_cmp_gt_u32 s84, 0x400000\n" \
"s_cbranch_scc0 L_pl60_%=\n" \
"s_branch L_pollr60_%=\n" \
"L_poll160_%=:\n" \
"s_mov_b32 s84, 0\n" \
"L_pl160_%=:\n" \
"s_sleep 1\n" \
"v_bfe_u32 v44, v20, 2, 5\n" \
"buffer_load_dword v44, v44, s[80:83], s38 offen offset:0 sc1\n" \
"s_waitcnt vmcnt(0)\n" \
"v_cmp_ne_u32_e32 vcc, s62, v44\n" \
"s_add_u32 s84, s84, 1\n" \
"s_nop 3\n" \
"s_cbranch_vccz L_pollr160_%=\n" \
"s_cmp_gt_u32 s84, 0x400000\n" \
"s_cbranch_scc0 L_pl160_%=\n" \
"s_branch L_pollr160_%=\n" \
".fill (L_pb + 42548 - .) / 4, 4, 0xBF800000\n" \
"L_w1_%=:\n" \
"s_waitcnt vmcnt(19)\n" \
"s_barrier\n" \
"s_nop 0\n" \
"ds_read_b64_tr_b16 a[160:161], v32 offset:0\n" \
"ds_read_b64_tr_b16 a[162:163], v32 offset:1024\n" \
"ds_read_b128 a[128:131], v28 offset:16384\n" \
"ds_read_b64_tr_b16 a[224:225], v32 offset:8192\n" \
"ds_read_b64_tr_b16 a[226:227], v32 offset:9216\n" \
"ds_read_b128 a[192:195], v29 offset:16384\n" \
"ds_read_b128 a[132:135], v28 offset:18432\n" \
"ds_read_b128 a[196:199], v29 offset:18432\n" \
"ds_read_b64_tr_b16 a[164:165], v33 offset:0\n" \
"ds_read_b64_tr_b16 a[166:167], v33 offset:1024\n" \
"ds_read_b64_tr_b16 a[228:229], v33 offset:8192\n" \
"ds_read_b64_tr_b16 a[230:231], v33 offset:9216\n" \
"ds_read_b128 a[136:139], v28 offset:20480\n" \
"ds_read_b128 a[200:203], v29 offset:20480\n" \
"s_waitcnt lgkmcnt(11)\n" \
"v_mfma_f32_16x16x32_bf16 a[0:3], a[128:131], a[160:163], 0\n" \
"ds_read_b64_tr_b16 a[168:169], v34 offset:0\n" \
"s_movk_i32 s5, 0xd80\n" \
"s_waitcnt lgkmcnt(9)\n" \
"v_mfma_f32_16x16x32_bf16 a[0:3], a[192:195], a[224:227], a[0:3]\n" \
"ds_read_b64_tr_b16 a[170:171], v34 offset:1024\n" \
"s_lshr_b32 s6, s2, 5\n" \
"s_waitcnt lgkmcnt(9)\n" \
"v_mfma_f32_16x16x32_bf16 a[16:19], a[132:135], a[160:163], 0\n" \
"ds_read_b64_tr_b16 a[232:233], v34 offset:8192\n" \
"v_lshrrev_b32_e32 v212, 4, v20\n" \
"s_waitcnt lgkmcnt(9)\n" \
"v_mfma_f32_16x16x32_bf16 a[16:19], a[196:199], a[224:227], a[16:19]\n" \
"ds_read_b64_tr_b16 a[234:235], v34 offset:9216\n" \
"v_lshl_or_b32 v215, s3, 6, v212\n" \
"s_waitcnt lgkmcnt(8)\n" \
"v_mfma_f32_16x16x32_bf16 a[20:23], a[132:135], a[164:167], 0\n" \
"ds_read_b128 a[140:143], v28 offset:22528\n" \
"v_mul_u32_u24_e32 v216, 0x2aab, v215\n" \
"s_waitcnt lgkmcnt(7)\n" \
"v_mfma_f32_16x16x32_bf16 a[20:23], a[196:199], a[228:231], a[20:23]\n" \
"ds_read_b128 a[204:207], v29 offset:22528\n" \
"v_lshrrev_b32_e32 v216, 16, v216\n" \
"v_mfma_f32_16x16x32_bf16 a[4:7], a[128:131], a[164:167], 0\n" \
"ds_read_b64_tr_b16 a[172:173], v35 offset:0\n" \
"v_mad_i32_i24 v217, v216, -6, v215\n" \
"v_mfma_f32_16x16x32_bf16 a[4:7], a[192:195], a[228:231], a[4:7]\n" \
"ds_read_b64_tr_b16 a[174:175], v35 offset:1024\n" \
"v_add_u32_e32 v218, 0xfffffe50, v215\n" \
"s_waitcnt lgkmcnt(9)\n" \
"v_mfma_f32_16x16x32_bf16 a[32:35], a[136:139], a[160:163], 0\n" \
"ds_read_b64_tr_b16 a[236:237], v35 offset:8192\n" \
"s_mul_i32 s35, s6, 0x48\n" \
"s_waitcnt lgkmcnt(9)\n" \
"v_mfma_f32_16x16x32_bf16 a[32:35], a[200:203], a[224:227], a[32:35]\n" \
"ds_read_b64_tr_b16 a[238:239], v35 offset:9216\n" \
"v_lshrrev_b32_e64 v219, 1, v217\n" \
"v_mfma_f32_16x16x32_bf16 a[36:39], a[136:139], a[164:167], 0\n" \
"ds_read_b128 a[144:147], v28 offset:0\n" \
"v_add_u32_e64 v218, s35, v216\n" \
"v_mfma_f32_16x16x32_bf16 a[36:39], a[200:203], a[228:231], a[36:39]\n" \
"ds_read_b128 a[208:211], v29 offset:0\n" \
"v_mul_u32_u24_e32 v219, 0x480, v219\n" \
"s_waitcnt lgkmcnt(10)\n" \
"v_mfma_f32_16x16x32_bf16 a[40:43], a[136:139], a[168:171], 0\n" \
"ds_read_b128 a[148:151], v28 offset:2048\n" \
"v_and_b32_e32 v214, 16, v20\n" \
"s_waitcnt lgkmcnt(9)\n" \
"v_mfma_f32_16x16x32_bf16 a[40:43], a[200:203], a[232:235], a[40:43]\n" \
"ds_read_b128 a[212:215], v29 offset:2048\n" \
"v_mad_u32_u24 v219, v218, s5, v219\n" \
"v_mfma_f32_16x16x32_bf16 a[24:27], a[132:135], a[168:171], 0\n" \
"ds_read_b128 a[152:155], v28 offset:4096\n" \
"v_or_b32_e32 v192, v219, v214\n" \
"v_mfma_f32_16x16x32_bf16 a[24:27], a[196:199], a[232:235], a[24:27]\n" \
"ds_read_b128 a[216:219], v29 offset:4096\n" \
"v_mov_b32_e64 v220, 2.0\n" \
"v_mfma_f32_16x16x32_bf16 a[8:11], a[128:131], a[168:171], 0\n" \
"ds_read_b128 a[156:159], v28 offset:6144\n" \
"v_mov_b32_e64 v222, 7\n" \
"v_mfma_f32_16x16x32_bf16 a[8:11], a[192:195], a[232:235], a[8:11]\n" \
"ds_read_b128 a[220:223], v29 offset:6144\n" \
"s_mov_b32 m0, s32\n" \
"s_waitcnt lgkmcnt(13)\n" \
"v_mfma_f32_16x16x32_bf16 a[48:51], a[140:143], a[160:163], 0\n" \
"s_mov_b32 exec_lo, 0xff000000\n" \
"s_mov_b32 exec_hi, 0x0\n" \
"global_load_lds_dword v[184:185], off\n" \
"s_mov_b64 exec, -1\n" \
"s_waitcnt lgkmcnt(12)\n" \
"v_mfma_f32_16x16x32_bf16 a[48:51], a[204:207], a[224:227], a[48:51]\n" \
"ds_read_b64_tr_b16 a[176:177], v36 offset:0\n" \
"v_lshlrev_b32_e32 v220, 7, v218\n" \
"v_mfma_f32_16x16x32_bf16 a[52:55], a[140:143], a[164:167], 0\n" \
"ds_read_b64_tr_b16 a[178:179], v36 offset:1024\n" \
"v_lshrrev_b32_e64 v218, 1, v218\n" \
"v_mfma_f32_16x16x32_bf16 a[52:55], a[204:207], a[228:231], a[52:55]\n" \
"s_mov_b32 m0, s32\n" \
"ds_read_b64_tr_b16 a[240:241], v36 offset:8192\n" \
"v_mfma_f32_16x16x32_bf16 a[56:59], a[140:143], a[168:171], 0\n" \
"s_mov_b32 exec_lo, 0xffff\n" \
"s_mov_b32 exec_hi, 0x0\n" \
"global_load_lds_dword v[186:187], off\n" \
"s_mov_b64 exec, -1\n" \
"v_mfma_f32_16x16x32_bf16 a[56:59], a[204:207], a[232:235], a[56:59]\n" \
"ds_read_b64_tr_b16 a[242:243], v36 offset:9216\n" \
"v_and_b32_e32 v218, 4, v218\n" \
"s_waitcnt lgkmcnt(14)\n" \
"v_mfma_f32_16x16x32_bf16 a[60:63], a[140:143], a[172:175], 0\n" \
"ds_read_b64_tr_b16 a[180:181], v37 offset:0\n" \
"v_and_b32_e32 v216, 2, v216\n" \
"s_waitcnt lgkmcnt(13)\n" \
"s_nop 0\n" \
"v_mfma_f32_16x16x32_bf16 a[60:63], a[204:207], a[236:239], a[60:63]\n" \
"ds_read_b64_tr_b16 a[182:183], v37 offset:1024\n" \
"s_mul_i32 s7, s6, 0x2400\n" \
"v_mfma_f32_16x16x32_bf16 a[44:47], a[136:139], a[172:175], 0\n" \
"ds_read_b64_tr_b16 a[244:245], v37 offset:8192\n" \
"s_sub_i32 s34, 0x22000, s7\n" \
"v_mfma_f32_16x16x32_bf16 a[44:47], a[200:203], a[236:239], a[44:47]\n" \
"ds_read_b64_tr_b16 a[246:247], v37 offset:9216\n" \
"v_bitop3_b32 v216, v217, v218, v216 bitop3:0x1e\n" \
"v_mfma_f32_16x16x32_bf16 a[28:31], a[132:135], a[172:175], 0\n" \
"ds_read_b64_tr_b16 a[184:185], v38 offset:0\n" \
"v_or_b32_e32 v213, 0x24400, v20\n" \
"v_mfma_f32_16x16x32_bf16 a[28:31], a[196:199], a[236:239], a[28:31]\n" \
"ds_read_b64_tr_b16 a[186:187], v38 offset:1024\n" \
"v_lshlrev_b32_e64 v216, 4, v216\n" \
"v_mfma_f32_16x16x32_bf16 a[12:15], a[128:131], a[172:175], 0\n" \
"v_add3_u32 v196, v220, s34, v216\n" \
"v_mov_b32_e64 v216, v213\n" \
"v_mfma_f32_16x16x32_bf16 a[12:15], a[192:195], a[236:239], a[12:15]\n" \
"s_movk_i32 s36, 0x1b0\n" \
"v_or_b32_e32 v216, 0x100, v215\n" \
"s_waitcnt lgkmcnt(15)\n" \
"v_mfma_f32_16x16x32_bf16 a[76:79], a[144:147], a[172:175], 0\n" \
"ds_read_b64_tr_b16 a[248:249], v38 offset:8192\n" \
"v_mul_u32_u24_e32 v217, 0x2aab, v216\n" \
"v_mfma_f32_16x16x32_bf16 a[76:79], a[208:211], a[236:239], a[76:79]\n" \
"v_add_u32_e32 v215, 0xffffff50, v215\n" \
"v_lshrrev_b32_e64 v217, 16, v217\n" \
"v_mfma_f32_16x16x32_bf16 a[72:75], a[144:147], a[168:171], 0\n" \
"v_mov_b32_e64 v220, v215\n" \
"v_mad_i32_i24 v218, v217, -6, v216\n" \
"v_mfma_f32_16x16x32_bf16 a[72:75], a[208:211], a[232:235], a[72:75]\n" \
"ds_read_b64_tr_b16 a[250:251], v38 offset:9216\n" \
"v_cmp_gt_u32_e64 vcc, s36, v216\n" \
"v_mfma_f32_16x16x32_bf16 a[68:71], a[144:147], a[164:167], 0\n" \
"v_mov_b32_e32 v221, 7\n" \
"v_lshrrev_b32_e32 v222, 1, v218\n" \
"v_mfma_f32_16x16x32_bf16 a[68:71], a[208:211], a[228:231], a[68:71]\n" \
"v_cndmask_b32_e32 v217, v220, v217, vcc\n" \
"s_movk_i32 s4, 0x48\n" \
"v_mfma_f32_16x16x32_bf16 a[64:67], a[144:147], a[160:163], 0\n" \
"v_add_u32_e64 v220, s35, v217\n" \
"v_mul_u32_u24_e32 v222, 0x480, v222\n" \
"v_mfma_f32_16x16x32_bf16 a[64:67], a[208:211], a[224:227], a[64:67]\n" \
"ds_read_b64_tr_b16 a[188:189], v39 offset:0\n" \
"v_mad_u32_u24 v222, v220, s5, v222\n" \
"v_mfma_f32_16x16x32_bf16 a[80:83], a[148:151], a[160:163], 0\n" \
"v_cmp_gt_u32_e64 s[4:5], s4, v215\n" \
"v_cndmask_b32_e32 v215, v221, v218, vcc\n" \
"s_waitcnt lgkmcnt(15)\n" \
"v_mfma_f32_16x16x32_bf16 a[80:83], a[212:215], a[224:227], a[80:83]\n" \
"v_lshrrev_b32_e32 v218, 1, v220\n" \
"v_or_b32_e32 v214, v222, v214\n" \
"v_mfma_f32_16x16x32_bf16 a[84:87], a[148:151], a[164:167], 0\n" \
"ds_read_b64_tr_b16 a[190:191], v39 offset:1024\n" \
"v_mov_b32_e64 v222, 2.0\n" \
"v_mfma_f32_16x16x32_bf16 a[84:87], a[212:215], a[228:231], a[84:87]\n" \
"v_and_b32_e32 v218, 4, v218\n" \
"v_and_b32_e32 v217, 2, v217\n" \
"v_mfma_f32_16x16x32_bf16 a[88:91], a[148:151], a[168:171], 0\n" \
"v_bitop3_b32 v215, v215, v218, v217 bitop3:0x1e\n" \
"v_lshlrev_b32_e64 v223, 1, v220\n" \
"v_mfma_f32_16x16x32_bf16 a[88:91], a[212:215], a[232:235], a[88:91]\n" \
"ds_read_b64_tr_b16 a[252:253], v39 offset:8192\n" \
"v_mov_b32_e64 v224, 2.0\n" \
"v_mfma_f32_16x16x32_bf16 a[92:95], a[148:151], a[172:175], 0\n" \
"v_cndmask_b32_e32 v193, v222, v214, vcc\n" \
"v_lshlrev_b32_e32 v214, 7, v220\n" \
"v_mfma_f32_16x16x32_bf16 a[92:95], a[212:215], a[236:239], a[92:95]\n" \
"v_lshlrev_b32_e32 v215, 4, v215\n" \
"s_mul_i32 s39, s3, 18\n" \
"v_mfma_f32_16x16x32_bf16 a[108:111], a[152:155], a[172:175], 0\n" \
"ds_read_b64_tr_b16 a[254:255], v39 offset:9216\n" \
"v_add3_u32 v214, v214, s34, v215\n" \
"v_mfma_f32_16x16x32_bf16 a[108:111], a[216:219], a[236:239], a[108:111]\n" \
"v_cndmask_b32_e64 v195, v224, v223, s[4:5]\n" \
"s_movk_i32 s4, 0x1f8\n" \
"v_mfma_f32_16x16x32_bf16 a[104:107], a[152:155], a[168:171], 0\n" \
"v_mov_b32_e32 v215, v213\n" \
"s_add_i32 s35, s35, s39\n" \
"v_mfma_f32_16x16x32_bf16 a[104:107], a[216:219], a[232:235], a[104:107]\n" \
"v_cmp_gt_u32_e32 vcc, s4, v216\n" \
"v_add_u32_e64 v212, s35, v212\n" \
"v_mfma_f32_16x16x32_bf16 a[100:103], a[152:155], a[164:167], 0\n" \
"v_mov_b32_e32 v219, 2.0\n" \
"v_cndmask_b32_e32 v197, v215, v214, vcc\n" \
"v_mfma_f32_16x16x32_bf16 a[100:103], a[216:219], a[228:231], a[100:103]\n" \
"s_movk_i32 s35, 0x120\n" \
"v_mul_u32_u24_e32 v214, 0xd80, v212\n" \
"v_mfma_f32_16x16x32_bf16 a[96:99], a[152:155], a[160:163], 0\n" \
"v_cmp_gt_u32_e32 vcc, s35, v20\n" \
"v_lshrrev_b32_e64 v215, 1, v212\n" \
"v_mfma_f32_16x16x32_bf16 a[96:99], a[216:219], a[224:227], a[96:99]\n" \
"s_and_b32 s4, s2, 7\n" \
"v_cndmask_b32_e32 v194, v219, v214, vcc\n" \
"s_waitcnt lgkmcnt(15)\n" \
"v_mfma_f32_16x16x32_bf16 a[112:115], a[156:159], a[160:163], 0\n" \
"v_lshlrev_b32_e32 v214, 7, v212\n" \
"v_lshrrev_b32_e64 v212, 2, v212\n" \
"v_mfma_f32_16x16x32_bf16 a[112:115], a[220:223], a[224:227], a[112:115]\n" \
"v_and_b32_e64 v212, 2, v212\n" \
"v_and_or_b32 v212, v215, 1, v212\n" \
"v_mfma_f32_16x16x32_bf16 a[116:119], a[156:159], a[164:167], 0\n" \
"s_lshl_b32 s5, s4, 2\n" \
"s_bfe_u32 s38, s2, 0x20003\n" \
"v_mfma_f32_16x16x32_bf16 a[116:119], a[220:223], a[228:231], a[116:119]\n" \
"v_lshlrev_b32_e32 v212, 5, v212\n" \
"s_movk_i32 s35, 0x60\n" \
"v_mfma_f32_16x16x32_bf16 a[120:123], a[156:159], a[168:171], 0\n" \
"s_or_b32 s5, s5, s38\n" \
"v_bitop3_b32 v212, v212, s35, v214 bitop3:0x36\n" \
"v_mfma_f32_16x16x32_bf16 a[120:123], a[220:223], a[232:235], a[120:123]\n" \
"s_lshr_b32 s36, s2, 3\n" \
"v_add_u32_e32 v212, s34, v212\n" \
"v_mfma_f32_16x16x32_bf16 a[124:127], a[156:159], a[172:175], 0\n" \
"s_mul_i32 s34, s5, 0x12000\n" \
"s_or_b32 s40, s34, 0x6000000\n" \
"v_mfma_f32_16x16x32_bf16 a[124:127], a[220:223], a[236:239], a[124:127]\n" \
"s_waitcnt vmcnt(5) lgkmcnt(0)\n" \
"s_barrier\n" \
"v_mfma_f32_16x16x32_bf16 v[52:55], a[128:131], a[176:179], 0\n" \
"s_add_u32 m0, s30, 0x0\n" \
"s_movk_i32 s84, 0x100\n" \
"v_mfma_f32_16x16x32_bf16 v[52:55], a[192:195], a[240:243], v[52:55]\n" \
"buffer_load_dwordx4 v22, s[64:67], s84 offen offset:0 lds\n" \
"v_mfma_f32_16x16x32_bf16 v[56:59], a[128:131], a[180:183], 0\n" \
"ds_read_b64_tr_b16 a[160:161], v32 offset:32768\n" \
"s_mul_i32 s34, s36, 0x90000\n" \
"v_mfma_f32_16x16x32_bf16 v[56:59], a[192:195], a[244:247], v[56:59]\n" \
"s_movk_i32 s84, 0x1d00\n" \
"v_mfma_f32_16x16x32_bf16 v[60:63], a[128:131], a[184:187], 0\n" \
"buffer_load_dwordx4 v23, s[64:67], s84 offen offset:1024 lds\n" \
"v_mfma_f32_16x16x32_bf16 v[60:63], a[192:195], a[248:251], v[60:63]\n" \
"ds_read_b64_tr_b16 a[162:163], v32 offset:33792\n" \
"s_lshl_b32 s36, s3, 14\n" \
"v_mfma_f32_16x16x32_bf16 v[64:67], a[128:131], a[188:191], 0\n" \
"ds_read_b64_tr_b16 a[224:225], v32 offset:40960\n" \
"s_add_i32 s34, s34, s36\n" \
"v_mfma_f32_16x16x32_bf16 v[64:67], a[192:195], a[252:255], v[64:67]\n" \
"ds_read_b128 a[128:131], v28 offset:49152\n" \
"s_lshl_b32 s38, s5, 8\n" \
"v_mfma_f32_16x16x32_bf16 v[80:83], a[132:135], a[188:191], 0\n" \
"ds_read_b128 a[192:195], v29 offset:49152\n" \
"s_add_i32 s41, s34, 0x4240000\n" \
"v_mfma_f32_16x16x32_bf16 v[80:83], a[196:199], a[252:255], v[80:83]\n" \
"s_movk_i32 s84, 0x3900\n" \
"v_mfma_f32_16x16x32_bf16 v[76:79], a[132:135], a[184:187], 0\n" \
"buffer_load_dwordx4 v22, s[64:67], s84 offen offset:2048 lds\n" \
"v_mfma_f32_16x16x32_bf16 v[76:79], a[196:199], a[248:251], v[76:79]\n" \
"s_movk_i32 s84, 0x5500\n" \
"v_mfma_f32_16x16x32_bf16 v[72:75], a[132:135], a[180:183], 0\n" \
"buffer_load_dwordx4 v23, s[64:67], s84 offen offset:3072 lds\n" \
"v_mfma_f32_16x16x32_bf16 v[72:75], a[196:199], a[244:247], v[72:75]\n" \
"s_add_u32 m0, s31, 0x0\n" \
"s_mov_b32 s84, 0x100000\n" \
"v_mfma_f32_16x16x32_bf16 v[68:71], a[132:135], a[176:179], 0\n" \
"buffer_load_dwordx4 v26, s[68:71], s84 offen offset:0 lds\n" \
"v_mfma_f32_16x16x32_bf16 v[68:71], a[196:199], a[240:243], v[68:71]\n" \
"ds_read_b128 a[132:135], v28 offset:51200\n" \
"s_mul_i32 s34, s5, 36\n" \
"v_mfma_f32_16x16x32_bf16 v[84:87], a[136:139], a[176:179], 0\n" \
"ds_read_b128 a[196:199], v29 offset:51200\n" \
"s_add_i32 s5, s7, s40\n" \
"v_mfma_f32_16x16x32_bf16 v[84:87], a[200:203], a[240:243], v[84:87]\n" \
"s_addk_i32 s84, 0x7c00\n" \
"v_mfma_f32_16x16x32_bf16 v[88:91], a[136:139], a[180:183], 0\n" \
"buffer_load_dwordx4 v26, s[68:71], s84 offen offset:1024 lds\n" \
"v_mfma_f32_16x16x32_bf16 v[88:91], a[200:203], a[244:247], v[88:91]\n" \
"ds_read_b64_tr_b16 a[226:227], v32 offset:41984\n" \
"s_addk_i32 s5, 0x2000\n" \
"v_mfma_f32_16x16x32_bf16 v[92:95], a[136:139], a[184:187], 0\n" \
"s_addk_i32 s84, 0x7c00\n" \
"v_mfma_f32_16x16x32_bf16 v[92:95], a[200:203], a[248:251], v[92:95]\n" \
"buffer_load_dwordx4 v27, s[68:71], s84 offen offset:2048 lds\n" \
"v_mfma_f32_16x16x32_bf16 v[96:99], a[136:139], a[188:191], 0\n" \
"ds_read_b64_tr_b16 a[164:165], v33 offset:32768\n" \
"s_mov_b32 s37, 2.0\n" \
"v_mfma_f32_16x16x32_bf16 v[96:99], a[200:203], a[252:255], v[96:99]\n" \
"ds_read_b128 a[136:139], v28 offset:53248\n" \
"s_or_b32 s38, s38, 0x5be8000\n" \
"v_mfma_f32_16x16x32_bf16 v[112:115], a[140:143], a[188:191], 0\n" \
"ds_read_b128 a[200:203], v29 offset:53248\n" \
"v_mfma_f32_16x16x32_bf16 v[112:115], a[204:207], a[252:255], v[112:115]\n" \
"s_addk_i32 s84, 0x7c00\n" \
"v_mfma_f32_16x16x32_bf16 v[108:111], a[140:143], a[184:187], 0\n" \
"buffer_load_dwordx4 v27, s[68:71], s84 offen offset:3072 lds\n" \
"v_mfma_f32_16x16x32_bf16 v[108:111], a[204:207], a[248:251], v[108:111]\n" \
"s_add_u32 m0, s30, 0x1000\n" \
"s_mov_b32 s84, 0x8100\n" \
"v_mfma_f32_16x16x32_bf16 v[104:107], a[140:143], a[180:183], 0\n" \
"buffer_load_dwordx4 v22, s[64:67], s84 offen offset:0 lds\n" \
"v_mfma_f32_16x16x32_bf16 v[104:107], a[204:207], a[244:247], v[104:107]\n" \
"ds_read_b64_tr_b16 a[166:167], v33 offset:33792\n" \
"v_mfma_f32_16x16x32_bf16 v[100:103], a[140:143], a[176:179], 0\n" \
"ds_read_b64_tr_b16 a[228:229], v33 offset:40960\n" \
"v_mfma_f32_16x16x32_bf16 v[100:103], a[204:207], a[240:243], v[100:103]\n" \
"ds_read_b128 a[140:143], v28 offset:55296\n" \
"v_mfma_f32_16x16x32_bf16 v[116:119], a[144:147], a[176:179], 0\n" \
"ds_read_b128 a[204:207], v29 offset:55296\n" \
"v_mfma_f32_16x16x32_bf16 v[116:119], a[208:211], a[240:243], v[116:119]\n" \
"s_addk_i32 s84, 0x1c00\n" \
"v_mfma_f32_16x16x32_bf16 v[120:123], a[144:147], a[180:183], 0\n" \
"buffer_load_dwordx4 v23, s[64:67], s84 offen offset:1024 lds\n" \
"v_mfma_f32_16x16x32_bf16 v[120:123], a[208:211], a[244:247], v[120:123]\n" \
"s_addk_i32 s84, 0x1c00\n" \
"v_mfma_f32_16x16x32_bf16 v[124:127], a[144:147], a[184:187], 0\n" \
"buffer_load_dwordx4 v22, s[64:67], s84 offen offset:2048 lds\n" \
"v_mfma_f32_16x16x32_bf16 v[124:127], a[208:211], a[248:251], v[124:127]\n" \
"s_addk_i32 s84, 0x1c00\n" \
"s_nop 0\n" \
"v_mfma_f32_16x16x32_bf16 v[128:131], a[144:147], a[188:191], 0\n" \
".fill (L_pb + 44768 - .) / 4, 4, 0xBF800000\n" \
"L_pp11:\n" \
"s_cbranch_execz L_pret\n" \
"s_nop 0\n" \
"buffer_load_dwordx4 v23, s[64:67], s84 offen offset:3072 lds\n" \
"v_mfma_f32_16x16x32_bf16 v[128:131], a[208:211], a[252:255], v[128:131]\n" \
"ds_read_b128 a[144:147], v28 offset:32768\n" \
"v_mfma_f32_16x16x32_bf16 v[144:147], a[148:151], a[188:191], 0\n" \
"ds_read_b128 a[208:211], v29 offset:32768\n" \
"v_mfma_f32_16x16x32_bf16 v[144:147], a[212:215], a[252:255], v[144:147]\n" \
"s_add_u32 m0, s31, 0x4000\n" \
"s_mov_b32 s84, 0x100000\n" \
"v_mfma_f32_16x16x32_bf16 v[140:143], a[148:151], a[184:187], 0\n" \
"buffer_load_dwordx4 v26, s[72:75], s84 offen offset:0 lds\n" \
"v_mfma_f32_16x16x32_bf16 v[140:143], a[212:215], a[248:251], v[140:143]\n" \
"ds_read_b64_tr_b16 a[230:231], v33 offset:41984\n" \
"v_mfma_f32_16x16x32_bf16 v[136:139], a[148:151], a[180:183], 0\n" \
"s_addk_i32 s84, 0x7c00\n" \
"s_nop 0\n" \
"v_mfma_f32_16x16x32_bf16 v[136:139], a[212:215], a[244:247], v[136:139]\n" \
"buffer_load_dwordx4 v26, s[72:75], s84 offen offset:1024 lds\n" \
"v_mfma_f32_16x16x32_bf16 v[132:135], a[148:151], a[176:179], 0\n" \
"ds_read_b64_tr_b16 a[168:169], v34 offset:32768\n" \
"v_mfma_f32_16x16x32_bf16 v[132:135], a[212:215], a[240:243], v[132:135]\n" \
"ds_read_b128 a[148:151], v28 offset:34816\n" \
"v_mfma_f32_16x16x32_bf16 v[148:151], a[152:155], a[176:179], 0\n" \
"ds_read_b128 a[212:215], v29 offset:34816\n" \
"v_mfma_f32_16x16x32_bf16 v[148:151], a[216:219], a[240:243], v[148:151]\n" \
"ds_read_b64_tr_b16 a[170:171], v34 offset:33792\n" \
"v_mfma_f32_16x16x32_bf16 v[152:155], a[152:155], a[180:183], 0\n" \
"ds_read_b64_tr_b16 a[232:233], v34 offset:40960\n" \
"v_mfma_f32_16x16x32_bf16 v[152:155], a[216:219], a[244:247], v[152:155]\n" \
"ds_read_b64_tr_b16 a[234:235], v34 offset:41984\n" \
"v_mfma_f32_16x16x32_bf16 v[156:159], a[152:155], a[184:187], 0\n" \
"ds_read_b64_tr_b16 a[172:173], v35 offset:32768\n" \
"v_mfma_f32_16x16x32_bf16 v[156:159], a[216:219], a[248:251], v[156:159]\n" \
"ds_read_b64_tr_b16 a[174:175], v35 offset:33792\n" \
"v_mfma_f32_16x16x32_bf16 v[160:163], a[152:155], a[188:191], 0\n" \
"ds_read_b64_tr_b16 a[236:237], v35 offset:40960\n" \
"v_mfma_f32_16x16x32_bf16 v[160:163], a[216:219], a[252:255], v[160:163]\n" \
"ds_read_b128 a[152:155], v28 offset:36864\n" \
"v_mfma_f32_16x16x32_bf16 v[176:179], a[156:159], a[188:191], 0\n" \
"ds_read_b128 a[216:219], v29 offset:36864\n" \
"v_mfma_f32_16x16x32_bf16 v[176:179], a[220:223], a[252:255], v[176:179]\n" \
"ds_read_b64_tr_b16 a[238:239], v35 offset:41984\n" \
"v_mfma_f32_16x16x32_bf16 v[172:175], a[156:159], a[184:187], 0\n" \
"s_cmp_lg_u32 s3, 0\n" \
"s_cselect_b32 s36, s37, s5\n" \
"v_mfma_f32_16x16x32_bf16 v[172:175], a[220:223], a[248:251], v[172:175]\n" \
"s_lshl_b32 s5, s6, 2\n" \
"s_or_b32 s5, s38, s5\n" \
"v_mfma_f32_16x16x32_bf16 v[168:171], a[156:159], a[180:183], 0\n" \
"s_lshl_b32 s35, s3, 10\n" \
"v_mfma_f32_16x16x32_bf16 v[168:171], a[220:223], a[244:247], v[168:171]\n" \
"s_cmpk_lt_u32 s3, 3\n" \
"s_cselect_b32 s37, s37, s5\n" \
"v_mfma_f32_16x16x32_bf16 v[164:167], a[156:159], a[176:179], 0\n" \
"s_lshl_b32 s5, s4, 16\n" \
"s_lshl_b32 s4, s4, 11\n" \
"v_mfma_f32_16x16x32_bf16 v[164:167], a[220:223], a[240:243], v[164:167]\n" \
"ds_read_b128 a[156:159], v28 offset:38912\n" \
"ds_read_b128 a[220:223], v29 offset:38912\n" \
"s_waitcnt lgkmcnt(15)\n" \
"v_mfma_f32_16x16x32_bf16 a[0:3], a[128:131], a[160:163], a[0:3]\n" \
"s_addk_i32 s84, 0x7c00\n" \
"v_mfma_f32_16x16x32_bf16 a[0:3], a[192:195], a[224:227], a[0:3]\n" \
"buffer_load_dwordx4 v27, s[72:75], s84 offen offset:2048 lds\n" \
"v_mfma_f32_16x16x32_bf16 a[4:7], a[128:131], a[164:167], a[4:7]\n" \
"ds_read_b64_tr_b16 a[176:177], v36 offset:32768\n" \
"v_cndmask_b32_e32 v198, v213, v212, vcc\n" \
"s_waitcnt lgkmcnt(15)\n" \
"v_mfma_f32_16x16x32_bf16 a[4:7], a[192:195], a[228:231], a[4:7]\n" \
"s_addk_i32 s84, 0x7c00\n" \
"s_waitcnt lgkmcnt(11)\n" \
"v_mfma_f32_16x16x32_bf16 a[8:11], a[128:131], a[168:171], a[8:11]\n" \
"buffer_load_dwordx4 v27, s[72:75], s84 offen offset:3072 lds\n" \
"s_waitcnt lgkmcnt(9)\n" \
"v_mfma_f32_16x16x32_bf16 a[8:11], a[192:195], a[232:235], a[8:11]\n" \
"ds_read_b64_tr_b16 a[178:179], v36 offset:33792\n" \
"v_or_b32_e32 v212, s35, v20\n" \
"s_waitcnt lgkmcnt(8)\n" \
"v_mfma_f32_16x16x32_bf16 a[12:15], a[128:131], a[172:175], a[12:15]\n" \
"ds_read_b64_tr_b16 a[240:241], v36 offset:40960\n" \
"s_or_b32 s39, s40, s35\n" \
"s_waitcnt lgkmcnt(5)\n" \
"v_mfma_f32_16x16x32_bf16 a[12:15], a[192:195], a[236:239], a[12:15]\n" \
"s_or_b32 s55, s41, s4\n" \
"v_or_b32_e32 v199, 0x22000, v212\n" \
"v_mfma_f32_16x16x32_bf16 a[28:31], a[132:135], a[172:175], a[28:31]\n" \
"s_add_i32 s35, s39, s7\n" \
"s_add_i32 s54, s41, s5\n" \
"v_mfma_f32_16x16x32_bf16 a[28:31], a[196:199], a[236:239], a[28:31]\n" \
"ds_read_b64_tr_b16 a[242:243], v36 offset:41984\n" \
"s_add_i32 s55, s55, 0x80000\n" \
"v_mfma_f32_16x16x32_bf16 a[24:27], a[132:135], a[168:171], a[24:27]\n" \
"s_lshr_b32 s4, s2, 1\n" \
"s_and_b32 s4, s4, 0x7c\n" \
"v_mfma_f32_16x16x32_bf16 a[24:27], a[196:199], a[232:235], a[24:27]\n" \
"v_mov_b32_e32 v213, s13\n" \
"s_or_b32 s4, s4, s3\n" \
"v_mfma_f32_16x16x32_bf16 a[20:23], a[132:135], a[164:167], a[20:23]\n" \
"v_lshrrev_b32_e64 v212, 4, v20\n" \
"v_mad_u64_u32 v[214:215], s[4:5], s4, 36, v[212:213]\n" \
"v_mfma_f32_16x16x32_bf16 a[20:23], a[196:199], a[228:231], a[20:23]\n" \
"ds_read_b64_tr_b16 a[180:181], v37 offset:32768\n" \
"v_mov_b32_e64 v217, s9\n" \
"v_mfma_f32_16x16x32_bf16 a[16:19], a[132:135], a[160:163], a[16:19]\n" \
"s_movk_i32 s4, 0x240\n" \
"v_cmp_gt_u32_e32 vcc, s4, v20\n" \
"v_mfma_f32_16x16x32_bf16 a[16:19], a[196:199], a[224:227], a[16:19]\n" \
"s_lshl_b32 s4, s2, 3\n" \
"s_and_b32 s4, s4, 56\n" \
"v_mfma_f32_16x16x32_bf16 a[32:35], a[136:139], a[160:163], a[32:35]\n" \
"s_mov_b32 m0, s32\n" \
"ds_read_b64_tr_b16 a[182:183], v37 offset:33792\n" \
"v_mfma_f32_16x16x32_bf16 a[32:35], a[200:203], a[224:227], a[32:35]\n" \
"s_mov_b32 exec_lo, 0x0\n" \
"s_mov_b32 exec_hi, 0xff\n" \
"global_load_lds_dword v[184:185], off\n" \
"s_mov_b64 exec, -1\n" \
"v_mfma_f32_16x16x32_bf16 a[36:39], a[136:139], a[164:167], a[36:39]\n" \
"v_cndmask_b32_e32 v217, v217, v213, vcc\n" \
"v_lshlrev_b32_e64 v213, 3, v214\n" \
"v_mfma_f32_16x16x32_bf16 a[36:39], a[200:203], a[228:231], a[36:39]\n" \
"ds_read_b64_tr_b16 a[244:245], v37 offset:40960\n" \
"v_and_b32_e32 v213, 0x1ffc0, v213\n" \
"v_mfma_f32_16x16x32_bf16 a[40:43], a[136:139], a[168:171], a[40:43]\n" \
"v_and_b32_e64 v214, 7, v214\n" \
"v_or3_b32 v213, v214, s4, v213\n" \
"v_mfma_f32_16x16x32_bf16 a[40:43], a[200:203], a[232:235], a[40:43]\n" \
"s_mov_b32 m0, s32\n" \
"s_lshl_b32 s4, s2, 2\n" \
"v_mfma_f32_16x16x32_bf16 a[44:47], a[136:139], a[172:175], a[44:47]\n" \
"s_mov_b32 exec_lo, 0xffff0000\n" \
"s_mov_b32 exec_hi, 0x0\n" \
"global_load_lds_dword v[186:187], off\n" \
"s_mov_b64 exec, -1\n" \
"v_mfma_f32_16x16x32_bf16 a[44:47], a[200:203], a[236:239], a[44:47]\n" \
"ds_read_b64_tr_b16 a[246:247], v37 offset:41984\n" \
"s_or_b32 s4, s4, s3\n" \
"v_mfma_f32_16x16x32_bf16 a[60:63], a[140:143], a[172:175], a[60:63]\n" \
"s_mul_i32 s4, s4, 18\n" \
"v_min_u32_e64 v212, 53, v212\n" \
"v_mfma_f32_16x16x32_bf16 a[60:63], a[204:207], a[236:239], a[60:63]\n" \
"s_sub_i32 s4, s4, 36\n" \
"v_add_u32_e32 v212, s4, v212\n" \
"v_mfma_f32_16x16x32_bf16 a[56:59], a[140:143], a[168:171], a[56:59]\n" \
"ds_read_b64_tr_b16 a[184:185], v38 offset:32768\n" \
"v_mov_b32_e64 v215, s12\n" \
"v_mfma_f32_16x16x32_bf16 a[56:59], a[204:207], a[232:235], a[56:59]\n" \
"v_mov_b32_e32 v216, s8\n" \
"v_cndmask_b32_e32 v212, v212, v213, vcc\n" \
"v_mfma_f32_16x16x32_bf16 a[52:55], a[140:143], a[164:167], a[52:55]\n" \
"v_cndmask_b32_e32 v216, v216, v215, vcc\n" \
"v_lshlrev_b32_e32 v212, 7, v212\n" \
"v_mfma_f32_16x16x32_bf16 a[52:55], a[204:207], a[228:231], a[52:55]\n" \
"ds_read_b64_tr_b16 a[186:187], v38 offset:33792\n" \
"v_mov_b32_e64 v213, 0\n" \
"v_mfma_f32_16x16x32_bf16 a[48:51], a[140:143], a[160:163], a[48:51]\n" \
"v_lshl_add_u64 v[188:189], v[216:217], 0, v[212:213]\n" \
"v_mov_b32_e32 v215, 0xffff\n" \
"v_mfma_f32_16x16x32_bf16 a[48:51], a[204:207], a[224:227], a[48:51]\n" \
"s_and_b32 s4, s3, 1\n" \
"s_lshl_b32 s5, s4, 4\n" \
"v_mfma_f32_16x16x32_bf16 a[64:67], a[144:147], a[160:163], a[64:67]\n" \
"ds_read_b64_tr_b16 a[248:249], v38 offset:40960\n" \
"v_lshrrev_b32_e64 v212, 4, v20\n" \
"v_mfma_f32_16x16x32_bf16 a[64:67], a[208:211], a[224:227], a[64:67]\n" \
"v_lshrrev_b32_e32 v218, 6, v20\n" \
"v_and_b32_e32 v219, 12, v218\n" \
"v_mfma_f32_16x16x32_bf16 a[68:71], a[144:147], a[164:167], a[68:71]\n" \
"v_and_or_b32 v214, v212, 15, s5\n" \
"v_cmp_gt_u32_e64 vcc, v219, v214\n" \
"v_mfma_f32_16x16x32_bf16 a[68:71], a[208:211], a[228:231], a[68:71]\n" \
"ds_read_b64_tr_b16 a[250:251], v38 offset:41984\n" \
"v_mov_b32_e32 v220, 0xffff0000\n" \
"v_mfma_f32_16x16x32_bf16 a[72:75], a[144:147], a[168:171], a[72:75]\n" \
"s_lshl_b32 s5, s2, 2\n" \
"v_cndmask_b32_e64 v217, v215, 0, vcc\n" \
"v_mfma_f32_16x16x32_bf16 a[72:75], a[208:211], a[232:235], a[72:75]\n" \
"v_cmp_lt_u32_e32 vcc, v219, v214\n" \
"v_mov_b32_e64 v213, s23\n" \
"v_mfma_f32_16x16x32_bf16 a[76:79], a[144:147], a[172:175], a[76:79]\n" \
"s_or_b32 s5, s5, s3\n" \
"v_cndmask_b32_e32 v221, 0, v220, vcc\n" \
"v_mfma_f32_16x16x32_bf16 a[76:79], a[208:211], a[236:239], a[76:79]\n" \
"ds_read_b64_tr_b16 a[188:189], v39 offset:32768\n" \
"v_or_b32_e64 v12, v217, v221\n" \
"v_mfma_f32_16x16x32_bf16 a[92:95], a[148:151], a[172:175], a[92:95]\n" \
"v_or_b32_e32 v217, 2, v219\n" \
"v_cmp_gt_u32_e32 vcc, v217, v214\n" \
"v_mfma_f32_16x16x32_bf16 a[92:95], a[212:215], a[236:239], a[92:95]\n" \
"v_mov_b32_e32 v216, s25\n" \
"s_movk_i32 s40, 0xf400\n" \
"v_mfma_f32_16x16x32_bf16 a[88:91], a[148:151], a[168:171], a[88:91]\n" \
"ds_read_b64_tr_b16 a[190:191], v39 offset:33792\n" \
"v_cndmask_b32_e64 v221, v215, 0, vcc\n" \
"v_mfma_f32_16x16x32_bf16 a[88:91], a[212:215], a[232:235], a[88:91]\n" \
"v_cmp_lt_u32_e32 vcc, v217, v214\n" \
"s_lshl_b32 s4, s4, 6\n" \
"v_mfma_f32_16x16x32_bf16 a[84:87], a[148:151], a[164:167], a[84:87]\n" \
"v_cndmask_b32_e32 v217, 0, v220, vcc\n" \
"v_or_b32_e32 v13, v221, v217\n" \
"v_mfma_f32_16x16x32_bf16 a[84:87], a[212:215], a[228:231], a[84:87]\n" \
"ds_read_b64_tr_b16 a[252:253], v39 offset:40960\n" \
"v_or_b32_e64 v217, 16, v219\n" \
"v_mfma_f32_16x16x32_bf16 a[80:83], a[148:151], a[160:163], a[80:83]\n" \
"v_cmp_gt_u32_e32 vcc, v217, v214\n" \
"s_nop 1\n" \
"v_cndmask_b32_e64 v221, v215, 0, vcc\n" \
"v_mfma_f32_16x16x32_bf16 a[80:83], a[212:215], a[224:227], a[80:83]\n" \
"v_cmp_lt_u32_e32 vcc, v217, v214\n" \
"s_nop 1\n" \
"v_cndmask_b32_e64 v217, 0, v220, vcc\n" \
"v_mfma_f32_16x16x32_bf16 a[96:99], a[152:155], a[160:163], a[96:99]\n" \
"ds_read_b64_tr_b16 a[254:255], v39 offset:41984\n" \
"v_or_b32_e64 v14, v221, v217\n" \
"v_mfma_f32_16x16x32_bf16 a[96:99], a[216:219], a[224:227], a[96:99]\n" \
"v_or_b32_e32 v217, 18, v219\n" \
"v_cmp_gt_u32_e32 vcc, v217, v214\n" \
"v_mfma_f32_16x16x32_bf16 a[100:103], a[152:155], a[164:167], a[100:103]\n" \
"s_nop 0\n" \
"v_cndmask_b32_e64 v215, v215, 0, vcc\n" \
"v_cmp_lt_u32_e32 vcc, v217, v214\n" \
"v_mfma_f32_16x16x32_bf16 a[100:103], a[216:219], a[228:231], a[100:103]\n" \
"s_nop 0\n" \
"v_cndmask_b32_e32 v214, 0, v220, vcc\n" \
"v_or_b32_e64 v15, v215, v214\n" \
"v_mfma_f32_16x16x32_bf16 a[104:107], a[152:155], a[168:171], a[104:107]\n" \
"v_mad_u64_u32 v[214:215], s[6:7], s5, 42, v[212:213]\n" \
"s_movk_i32 s5, 0x1440\n" \
"v_mfma_f32_16x16x32_bf16 a[104:107], a[216:219], a[232:235], a[104:107]\n" \
"v_cmp_gt_u32_e32 vcc, s5, v214\n" \
"v_min_u32_e32 v217, 0xa43f, v214\n" \
"v_mfma_f32_16x16x32_bf16 a[108:111], a[152:155], a[172:175], a[108:111]\n" \
"v_mov_b32_e32 v214, s24\n" \
"v_cndmask_b32_e32 v215, v216, v213, vcc\n" \
"v_mfma_f32_16x16x32_bf16 a[108:111], a[216:219], a[236:239], a[108:111]\n" \
"v_mov_b32_e32 v213, s22\n" \
"s_movk_i32 s6, 0xffee\n" \
"s_waitcnt lgkmcnt(15)\n" \
"v_mfma_f32_16x16x32_bf16 a[124:127], a[156:159], a[172:175], a[124:127]\n" \
"v_cndmask_b32_e32 v214, v214, v213, vcc\n" \
"v_add_u32_e32 v213, 0xffffebc0, v217\n" \
"v_mfma_f32_16x16x32_bf16 a[124:127], a[220:223], a[236:239], a[124:127]\n" \
"s_lshl_b32 s5, s2, 5\n" \
"v_cndmask_b32_e32 v213, v213, v217, vcc\n" \
"v_mfma_f32_16x16x32_bf16 a[120:123], a[156:159], a[168:171], a[120:123]\n" \
"v_lshlrev_b32_e64 v216, 7, v213\n" \
"v_mul_u32_u24_e32 v213, 0xe39, v212\n" \
"v_mfma_f32_16x16x32_bf16 a[120:123], a[220:223], a[232:235], a[120:123]\n" \
"v_mov_b32_e32 v217, 0\n" \
"v_lshrrev_b32_e32 v213, 16, v213\n" \
"v_mfma_f32_16x16x32_bf16 a[116:119], a[156:159], a[164:167], a[116:119]\n" \
"v_lshl_add_u64 v[16:17], v[214:215], 0, v[216:217]\n" \
"v_mad_i32_i24 v214, v213, s6, v212\n" \
"v_mfma_f32_16x16x32_bf16 a[116:119], a[220:223], a[228:231], a[116:119]\n" \
"s_add_i32 s5, s5, 32\n" \
"v_lshlrev_b32_e32 v215, 4, v214\n" \
"v_mfma_f32_16x16x32_bf16 a[112:115], a[156:159], a[160:163], a[112:115]\n" \
"v_add_u32_e64 v216, s5, v215\n" \
"v_and_b32_e32 v216, 0xf0, v216\n" \
"v_mfma_f32_16x16x32_bf16 a[112:115], a[220:223], a[224:227], a[112:115]\n" \
"s_waitcnt vmcnt(20)\n" \
"v_readfirstlane_b32 s62, v180\n" \
"s_nop 3\n" \
"s_add_u32 s62, s62, 0x9E3779B1\n" \
"s_waitcnt vmcnt(2) lgkmcnt(0)\n" \
"s_barrier\n" \
"v_mfma_f32_16x16x32_bf16 v[52:55], a[128:131], a[176:179], v[52:55]\n" \
"s_add_u32 m0, s30, 0x8000\n" \
"s_movk_i32 s84, 0x180\n" \
"v_mfma_f32_16x16x32_bf16 v[52:55], a[192:195], a[240:243], v[52:55]\n" \
"buffer_load_dwordx4 v22, s[64:67], s84 offen offset:0 lds\n" \
"v_mfma_f32_16x16x32_bf16 v[56:59], a[128:131], a[180:183], v[56:59]\n" \
"ds_read_b64_tr_b16 a[160:161], v32 offset:0\n" \
"v_cmp_gt_u32_e64 vcc, 16, v214\n" \
"v_mfma_f32_16x16x32_bf16 v[56:59], a[192:195], a[244:247], v[56:59]\n" \
"s_movk_i32 s84, 0x1d80\n" \
"v_mfma_f32_16x16x32_bf16 v[60:63], a[128:131], a[184:187], v[60:63]\n" \
"buffer_load_dwordx4 v23, s[64:67], s84 offen offset:1024 lds\n" \
"v_mfma_f32_16x16x32_bf16 v[60:63], a[192:195], a[248:251], v[60:63]\n" \
"ds_read_b64_tr_b16 a[162:163], v32 offset:1024\n" \
"s_movk_i32 s7, 0xf800\n" \
"v_mfma_f32_16x16x32_bf16 v[64:67], a[128:131], a[188:191], v[64:67]\n" \
"ds_read_b64_tr_b16 a[224:225], v32 offset:8192\n" \
"v_cndmask_b32_e64 v214, v215, v216, vcc\n" \
"v_mfma_f32_16x16x32_bf16 v[64:67], a[192:195], a[252:255], v[64:67]\n" \
"ds_read_b128 a[128:131], v28 offset:16384\n" \
"v_or_b32_e64 v215, 64, v212\n" \
"v_mfma_f32_16x16x32_bf16 v[80:83], a[132:135], a[188:191], v[80:83]\n" \
"ds_read_b128 a[192:195], v29 offset:16384\n" \
"v_mul_u32_u24_e32 v216, 0xe39, v215\n" \
"v_mfma_f32_16x16x32_bf16 v[80:83], a[196:199], a[252:255], v[80:83]\n" \
"s_movk_i32 s84, 0x3980\n" \
"v_mfma_f32_16x16x32_bf16 v[76:79], a[132:135], a[184:187], v[76:79]\n" \
"buffer_load_dwordx4 v22, s[64:67], s84 offen offset:2048 lds\n" \
"v_mfma_f32_16x16x32_bf16 v[76:79], a[196:199], a[248:251], v[76:79]\n" \
"s_movk_i32 s84, 0x5580\n" \
"v_mfma_f32_16x16x32_bf16 v[72:75], a[132:135], a[180:183], v[72:75]\n" \
"buffer_load_dwordx4 v23, s[64:67], s84 offen offset:3072 lds\n" \
"v_mfma_f32_16x16x32_bf16 v[72:75], a[196:199], a[244:247], v[72:75]\n" \
"s_add_u32 m0, s31, 0x8000\n" \
"s_mov_b32 s84, 0x180000\n" \
"v_mfma_f32_16x16x32_bf16 v[68:71], a[132:135], a[176:179], v[68:71]\n" \
"buffer_load_dwordx4 v26, s[68:71], s84 offen offset:0 lds\n" \
"v_mfma_f32_16x16x32_bf16 v[68:71], a[196:199], a[240:243], v[68:71]\n" \
"ds_read_b128 a[132:135], v28 offset:18432\n" \
"v_lshrrev_b32_e64 v216, 16, v216\n" \
"v_mfma_f32_16x16x32_bf16 v[84:87], a[136:139], a[176:179], v[84:87]\n" \
"ds_read_b128 a[196:199], v29 offset:18432\n" \
"v_mad_i32_i24 v215, v216, s6, v215\n" \
"v_mfma_f32_16x16x32_bf16 v[84:87], a[200:203], a[240:243], v[84:87]\n" \
"s_addk_i32 s84, 0x7c00\n" \
"v_mfma_f32_16x16x32_bf16 v[88:91], a[136:139], a[180:183], v[88:91]\n" \
"buffer_load_dwordx4 v26, s[68:71], s84 offen offset:1024 lds\n" \
"v_mfma_f32_16x16x32_bf16 v[88:91], a[200:203], a[244:247], v[88:91]\n" \
"ds_read_b64_tr_b16 a[226:227], v32 offset:9216\n" \
"v_lshlrev_b32_e32 v217, 4, v215\n" \
"v_mfma_f32_16x16x32_bf16 v[92:95], a[136:139], a[184:187], v[92:95]\n" \
"s_addk_i32 s84, 0x7c00\n" \
"v_mfma_f32_16x16x32_bf16 v[92:95], a[200:203], a[248:251], v[92:95]\n" \
"buffer_load_dwordx4 v27, s[68:71], s84 offen offset:2048 lds\n" \
"v_mfma_f32_16x16x32_bf16 v[96:99], a[136:139], a[188:191], v[96:99]\n" \
"ds_read_b64_tr_b16 a[164:165], v33 offset:0\n" \
"v_add_u32_e32 v220, s5, v217\n" \
"v_mfma_f32_16x16x32_bf16 v[96:99], a[200:203], a[252:255], v[96:99]\n" \
"ds_read_b128 a[136:139], v28 offset:20480\n" \
"v_and_b32_e32 v220, 0xf0, v220\n" \
"v_mfma_f32_16x16x32_bf16 v[112:115], a[140:143], a[188:191], v[112:115]\n" \
"ds_read_b128 a[200:203], v29 offset:20480\n" \
"v_cmp_gt_u32_e64 vcc, 16, v215\n" \
"v_mfma_f32_16x16x32_bf16 v[112:115], a[204:207], a[252:255], v[112:115]\n" \
"s_addk_i32 s84, 0x7c00\n" \
"v_mfma_f32_16x16x32_bf16 v[108:111], a[140:143], a[184:187], v[108:111]\n" \
"buffer_load_dwordx4 v27, s[68:71], s84 offen offset:3072 lds\n" \
"v_mfma_f32_16x16x32_bf16 v[108:111], a[204:207], a[248:251], v[108:111]\n" \
"s_add_u32 m0, s30, 0x9000\n" \
"s_mov_b32 s84, 0x8180\n" \
"v_mfma_f32_16x16x32_bf16 v[104:107], a[140:143], a[180:183], v[104:107]\n" \
"buffer_load_dwordx4 v22, s[64:67], s84 offen offset:0 lds\n" \
"v_mfma_f32_16x16x32_bf16 v[104:107], a[204:207], a[244:247], v[104:107]\n" \
"ds_read_b64_tr_b16 a[166:167], v33 offset:1024\n" \
"v_cndmask_b32_e32 v215, v217, v220, vcc\n" \
"v_mfma_f32_16x16x32_bf16 v[100:103], a[140:143], a[176:179], v[100:103]\n" \
"ds_read_b64_tr_b16 a[228:229], v33 offset:8192\n" \
"v_or_b32_e32 v217, 0x80, v212\n" \
"v_mfma_f32_16x16x32_bf16 v[100:103], a[204:207], a[240:243], v[100:103]\n" \
"ds_read_b128 a[140:143], v28 offset:22528\n" \
"v_mul_u32_u24_e32 v220, 0xe39, v217\n" \
"v_mfma_f32_16x16x32_bf16 v[116:119], a[144:147], a[176:179], v[116:119]\n" \
"ds_read_b128 a[204:207], v29 offset:22528\n" \
"v_lshrrev_b32_e64 v220, 16, v220\n" \
"v_mfma_f32_16x16x32_bf16 v[116:119], a[208:211], a[240:243], v[116:119]\n" \
"s_addk_i32 s84, 0x1c00\n" \
"v_mfma_f32_16x16x32_bf16 v[120:123], a[144:147], a[180:183], v[120:123]\n" \
"buffer_load_dwordx4 v23, s[64:67], s84 offen offset:1024 lds\n" \
"v_mfma_f32_16x16x32_bf16 v[120:123], a[208:211], a[244:247], v[120:123]\n" \
"s_addk_i32 s84, 0x1c00\n" \
"v_mfma_f32_16x16x32_bf16 v[124:127], a[144:147], a[184:187], v[124:127]\n" \
"buffer_load_dwordx4 v22, s[64:67], s84 offen offset:2048 lds\n" \
"v_mfma_f32_16x16x32_bf16 v[124:127], a[208:211], a[248:251], v[124:127]\n" \
"s_addk_i32 s84, 0x1c00\n" \
"v_mfma_f32_16x16x32_bf16 v[128:131], a[144:147], a[188:191], v[128:131]\n" \
"buffer_load_dwordx4 v23, s[64:67], s84 offen offset:3072 lds\n" \
"v_mfma_f32_16x16x32_bf16 v[128:131], a[208:211], a[252:255], v[128:131]\n" \
"ds_read_b128 a[144:147], v28 offset:0\n" \
"v_mad_i32_i24 v217, v220, s6, v217\n" \
"v_mfma_f32_16x16x32_bf16 v[144:147], a[148:151], a[188:191], v[144:147]\n" \
"ds_read_b128 a[208:211], v29 offset:0\n" \
"v_lshlrev_b32_e32 v221, 4, v217\n" \
"v_mfma_f32_16x16x32_bf16 v[144:147], a[212:215], a[252:255], v[144:147]\n" \
"s_add_u32 m0, s31, 0xc000\n" \
"s_mov_b32 s84, 0x180000\n" \
"v_mfma_f32_16x16x32_bf16 v[140:143], a[148:151], a[184:187], v[140:143]\n" \
"buffer_load_dwordx4 v26, s[72:75], s84 offen offset:0 lds\n" \
"v_mfma_f32_16x16x32_bf16 v[140:143], a[212:215], a[248:251], v[140:143]\n" \
"ds_read_b64_tr_b16 a[230:231], v33 offset:9216\n" \
"v_add_u32_e64 v222, s5, v221\n" \
"v_mfma_f32_16x16x32_bf16 v[136:139], a[148:151], a[180:183], v[136:139]\n" \
"s_addk_i32 s84, 0x7c00\n" \
"v_mfma_f32_16x16x32_bf16 v[136:139], a[212:215], a[244:247], v[136:139]\n" \
"buffer_load_dwordx4 v26, s[72:75], s84 offen offset:1024 lds\n" \
"v_mfma_f32_16x16x32_bf16 v[132:135], a[148:151], a[176:179], v[132:135]\n" \
"ds_read_b64_tr_b16 a[168:169], v34 offset:0\n" \
"v_and_b32_e32 v222, 0xf0, v222\n" \
"v_mfma_f32_16x16x32_bf16 v[132:135], a[212:215], a[240:243], v[132:135]\n" \
"ds_read_b128 a[148:151], v28 offset:2048\n" \
"v_cmp_gt_u32_e32 vcc, 16, v217\n" \
"v_mfma_f32_16x16x32_bf16 v[148:151], a[152:155], a[176:179], v[148:151]\n" \
"ds_read_b128 a[212:215], v29 offset:2048\n" \
"v_cndmask_b32_e64 v217, v221, v222, vcc\n" \
"v_mfma_f32_16x16x32_bf16 v[148:151], a[216:219], a[240:243], v[148:151]\n" \
"ds_read_b64_tr_b16 a[170:171], v34 offset:1024\n" \
"v_or_b32_e32 v221, 0xc0, v212\n" \
"v_mfma_f32_16x16x32_bf16 v[152:155], a[152:155], a[180:183], v[152:155]\n" \
"ds_read_b64_tr_b16 a[232:233], v34 offset:8192\n" \
"v_mul_u32_u24_e32 v222, 0xe39, v221\n" \
"v_mfma_f32_16x16x32_bf16 v[152:155], a[216:219], a[244:247], v[152:155]\n" \
"ds_read_b64_tr_b16 a[234:235], v34 offset:9216\n" \
"v_lshrrev_b32_e64 v222, 16, v222\n" \
"v_mfma_f32_16x16x32_bf16 v[156:159], a[152:155], a[184:187], v[156:159]\n" \
"ds_read_b64_tr_b16 a[172:173], v35 offset:0\n" \
"v_mad_i32_i24 v221, v222, s6, v221\n" \
"v_mfma_f32_16x16x32_bf16 v[156:159], a[216:219], a[248:251], v[156:159]\n" \
"ds_read_b64_tr_b16 a[174:175], v35 offset:1024\n" \
"v_lshlrev_b32_e64 v223, 4, v221\n" \
"v_mfma_f32_16x16x32_bf16 v[160:163], a[152:155], a[188:191], v[160:163]\n" \
"ds_read_b64_tr_b16 a[236:237], v35 offset:8192\n" \
"v_add_u32_e64 v224, s5, v223\n" \
"v_mfma_f32_16x16x32_bf16 v[160:163], a[216:219], a[252:255], v[160:163]\n" \
"ds_read_b128 a[152:155], v28 offset:4096\n" \
"v_and_b32_e32 v224, 0xf0, v224\n" \
"v_mfma_f32_16x16x32_bf16 v[176:179], a[156:159], a[188:191], v[176:179]\n" \
"ds_read_b128 a[216:219], v29 offset:4096\n" \
"v_cmp_gt_u32_e64 vcc, 16, v221\n" \
"v_mfma_f32_16x16x32_bf16 v[176:179], a[220:223], a[252:255], v[176:179]\n" \
"ds_read_b64_tr_b16 a[238:239], v35 offset:9216\n" \
"v_cndmask_b32_e64 v221, v223, v224, vcc\n" \
"v_mfma_f32_16x16x32_bf16 v[172:175], a[156:159], a[184:187], v[172:175]\n" \
"v_or_b32_e32 v223, 0x100, v212\n" \
"v_mul_u32_u24_e32 v224, 0xe39, v223\n" \
"v_mfma_f32_16x16x32_bf16 v[172:175], a[220:223], a[248:251], v[172:175]\n" \
"v_lshrrev_b32_e64 v224, 16, v224\n" \
"v_mad_i32_i24 v223, v224, s6, v223\n" \
"v_mfma_f32_16x16x32_bf16 v[168:171], a[156:159], a[180:183], v[168:171]\n" \
"v_lshlrev_b32_e32 v225, 4, v223\n" \
"v_add_u32_e32 v226, s5, v225\n" \
"v_mfma_f32_16x16x32_bf16 v[168:171], a[220:223], a[244:247], v[168:171]\n" \
"v_and_b32_e32 v226, 0xf0, v226\n" \
"v_cmp_gt_u32_e64 vcc, 16, v223\n" \
"v_mfma_f32_16x16x32_bf16 v[164:167], a[156:159], a[176:179], v[164:167]\n" \
"s_nop 0\n" \
"v_cndmask_b32_e32 v223, v225, v226, vcc\n" \
"v_or_b32_e32 v225, 0x140, v212\n" \
"v_mfma_f32_16x16x32_bf16 v[164:167], a[220:223], a[240:243], v[164:167]\n" \
"ds_read_b128 a[156:159], v28 offset:6144\n" \
"ds_read_b128 a[220:223], v29 offset:6144\n" \
"s_waitcnt lgkmcnt(15)\n" \
"v_mfma_f32_16x16x32_bf16 a[0:3], a[128:131], a[160:163], a[0:3]\n" \
"s_addk_i32 s84, 0x7c00\n" \
"v_mfma_f32_16x16x32_bf16 a[0:3], a[192:195], a[224:227], a[0:3]\n" \
"buffer_load_dwordx4 v27, s[72:75], s84 offen offset:2048 lds\n" \
"v_mfma_f32_16x16x32_bf16 a[4:7], a[128:131], a[164:167], a[4:7]\n" \
"ds_read_b64_tr_b16 a[176:177], v36 offset:0\n" \
"v_mul_u32_u24_e32 v226, 0xe39, v225\n" \
"s_waitcnt lgkmcnt(15)\n" \
"v_mfma_f32_16x16x32_bf16 a[4:7], a[192:195], a[228:231], a[4:7]\n" \
"s_addk_i32 s84, 0x7c00\n" \
"s_waitcnt lgkmcnt(11)\n" \
"v_mfma_f32_16x16x32_bf16 a[8:11], a[128:131], a[168:171], a[8:11]\n" \
"buffer_load_dwordx4 v27, s[72:75], s84 offen offset:3072 lds\n" \
"s_waitcnt lgkmcnt(9)\n" \
"v_mfma_f32_16x16x32_bf16 a[8:11], a[192:195], a[232:235], a[8:11]\n" \
"ds_read_b64_tr_b16 a[178:179], v36 offset:1024\n" \
"v_lshrrev_b32_e32 v226, 16, v226\n" \
"s_waitcnt lgkmcnt(8)\n" \
"v_mfma_f32_16x16x32_bf16 a[12:15], a[128:131], a[172:175], a[12:15]\n" \
"ds_read_b64_tr_b16 a[240:241], v36 offset:8192\n" \
"v_mad_i32_i24 v225, v226, s6, v225\n" \
"s_waitcnt lgkmcnt(5)\n" \
"v_mfma_f32_16x16x32_bf16 a[12:15], a[192:195], a[236:239], a[12:15]\n" \
"v_lshlrev_b32_e32 v227, 4, v225\n" \
"v_add_u32_e64 v228, s5, v227\n" \
"v_mfma_f32_16x16x32_bf16 a[28:31], a[132:135], a[172:175], a[28:31]\n" \
"v_and_b32_e32 v228, 0xf0, v228\n" \
"v_cmp_gt_u32_e64 vcc, 16, v225\n" \
"v_mfma_f32_16x16x32_bf16 a[28:31], a[196:199], a[236:239], a[28:31]\n" \
"ds_read_b64_tr_b16 a[242:243], v36 offset:9216\n" \
"v_cndmask_b32_e64 v225, v227, v228, vcc\n" \
"v_mfma_f32_16x16x32_bf16 a[24:27], a[132:135], a[168:171], a[24:27]\n" \
"buffer_load_dwordx4 v[200:203], v192, s[76:79], s34 offen offset:0\n" \
"v_mfma_f32_16x16x32_bf16 a[24:27], a[196:199], a[232:235], a[24:27]\n" \
"v_or_b32_e32 v227, 0x180, v212\n" \
"v_mul_u32_u24_e32 v228, 0xe39, v227\n" \
"v_mfma_f32_16x16x32_bf16 a[20:23], a[132:135], a[164:167], a[20:23]\n" \
"v_lshrrev_b32_e64 v228, 16, v228\n" \
"v_mad_i32_i24 v227, v228, s6, v227\n" \
"v_mfma_f32_16x16x32_bf16 a[20:23], a[196:199], a[228:231], a[20:23]\n" \
"ds_read_b64_tr_b16 a[180:181], v37 offset:0\n" \
"v_lshlrev_b32_e64 v229, 4, v227\n" \
"v_mfma_f32_16x16x32_bf16 a[16:19], a[132:135], a[160:163], a[16:19]\n" \
"v_add_u32_e64 v230, s5, v229\n" \
"v_and_b32_e32 v230, 0xf0, v230\n" \
"v_mfma_f32_16x16x32_bf16 a[16:19], a[196:199], a[224:227], a[16:19]\n" \
"v_cmp_gt_u32_e32 vcc, 16, v227\n" \
"s_nop 1\n" \
"v_cndmask_b32_e64 v227, v229, v230, vcc\n" \
"v_mfma_f32_16x16x32_bf16 a[32:35], a[136:139], a[160:163], a[32:35]\n" \
"buffer_load_dwordx4 v[204:207], v193, s[76:79], s34 offen offset:0\n" \
"v_mfma_f32_16x16x32_bf16 a[32:35], a[200:203], a[224:227], a[32:35]\n" \
"s_mov_b32 m0, s32\n" \
"ds_read_b64_tr_b16 a[182:183], v37 offset:1024\n" \
"v_mfma_f32_16x16x32_bf16 a[36:39], a[136:139], a[164:167], a[36:39]\n" \
"s_mov_b32 exec_lo, 0x0\n" \
"s_mov_b32 exec_hi, 0xff00\n" \
"global_load_lds_dword v[184:185], off\n" \
"s_mov_b64 exec, -1\n" \
"v_mfma_f32_16x16x32_bf16 a[36:39], a[200:203], a[228:231], a[36:39]\n" \
"ds_read_b64_tr_b16 a[244:245], v37 offset:8192\n" \
"v_or_b32_e32 v229, 0x1c0, v212\n" \
"v_mfma_f32_16x16x32_bf16 a[40:43], a[136:139], a[168:171], a[40:43]\n" \
"v_mul_u32_u24_e32 v230, 0xe39, v229\n" \
"v_lshrrev_b32_e32 v230, 16, v230\n" \
"v_mfma_f32_16x16x32_bf16 a[40:43], a[200:203], a[232:235], a[40:43]\n" \
"buffer_load_dword v208, v194, s[76:79], s34 offen offset:32\n" \
"v_mfma_f32_16x16x32_bf16 a[44:47], a[136:139], a[172:175], a[44:47]\n" \
"ds_read_b64_tr_b16 a[246:247], v37 offset:9216\n" \
"v_mad_i32_i24 v229, v230, s6, v229\n" \
"v_mfma_f32_16x16x32_bf16 a[44:47], a[200:203], a[236:239], a[44:47]\n" \
"v_lshlrev_b32_e32 v231, 4, v229\n" \
"v_add_u32_e32 v232, s5, v231\n" \
"v_mfma_f32_16x16x32_bf16 a[60:63], a[140:143], a[172:175], a[60:63]\n" \
"v_and_b32_e32 v232, 0xf0, v232\n" \
"v_cmp_gt_u32_e64 vcc, 16, v229\n" \
"v_mfma_f32_16x16x32_bf16 a[60:63], a[204:207], a[236:239], a[60:63]\n" \
"s_nop 0\n" \
"v_cndmask_b32_e32 v229, v231, v232, vcc\n" \
"v_or_b32_e32 v231, 0x200, v212\n" \
"v_mfma_f32_16x16x32_bf16 a[56:59], a[140:143], a[168:171], a[56:59]\n" \
"buffer_load_dword v209, v194, s[76:79], s34 offen offset:1184\n" \
"v_mfma_f32_16x16x32_bf16 a[56:59], a[204:207], a[232:235], a[56:59]\n" \
"ds_read_b64_tr_b16 a[184:185], v38 offset:0\n" \
"v_mul_u32_u24_e32 v232, 0xe39, v231\n" \
"v_mfma_f32_16x16x32_bf16 a[52:55], a[140:143], a[164:167], a[52:55]\n" \
"v_lshrrev_b32_e64 v232, 16, v232\n" \
"v_mad_i32_i24 v231, v232, s6, v231\n" \
"v_mfma_f32_16x16x32_bf16 a[52:55], a[204:207], a[228:231], a[52:55]\n" \
"ds_read_b64_tr_b16 a[186:187], v38 offset:1024\n" \
"v_lshlrev_b32_e64 v233, 4, v231\n" \
"v_mfma_f32_16x16x32_bf16 a[48:51], a[140:143], a[160:163], a[48:51]\n" \
"v_add_u32_e32 v234, s5, v233\n" \
"s_movk_i32 s5, 0x480\n" \
"v_mfma_f32_16x16x32_bf16 a[48:51], a[204:207], a[224:227], a[48:51]\n" \
"v_mad_u32_u24 v0, v213, s5, v214\n" \
"v_mul_u32_u24_e32 v213, 0x480, v216\n" \
"v_mfma_f32_16x16x32_bf16 a[64:67], a[144:147], a[160:163], a[64:67]\n" \
"buffer_load_dword v211, v194, s[76:79], s34 offen offset:2336\n" \
"v_mfma_f32_16x16x32_bf16 a[64:67], a[208:211], a[224:227], a[64:67]\n" \
"ds_read_b64_tr_b16 a[248:249], v38 offset:8192\n" \
"s_movk_i32 s6, 0xfc00\n" \
"v_mfma_f32_16x16x32_bf16 a[68:71], a[144:147], a[164:167], a[68:71]\n" \
"v_add3_u32 v1, v213, v215, s6\n" \
"v_mul_u32_u24_e32 v213, 0x480, v220\n" \
"v_mfma_f32_16x16x32_bf16 a[68:71], a[208:211], a[228:231], a[68:71]\n" \
"ds_read_b64_tr_b16 a[250:251], v38 offset:9216\n" \
"v_add3_u32 v2, v213, v217, s7\n" \
"v_mfma_f32_16x16x32_bf16 a[72:75], a[144:147], a[168:171], a[72:75]\n" \
"v_mul_u32_u24_e32 v213, 0x480, v222\n" \
"v_add3_u32 v3, v213, v221, s40\n" \
"v_mfma_f32_16x16x32_bf16 a[72:75], a[208:211], a[232:235], a[72:75]\n" \
"s_mov_b32 s76, s20\n" \
"s_and_b32 s77, s21, 0xffff\n" \
"v_mfma_f32_16x16x32_bf16 a[76:79], a[144:147], a[172:175], a[76:79]\n" \
"s_mov_b32 s78, 0x480\n" \
"v_mul_u32_u24_e32 v213, 0x480, v226\n" \
"v_mfma_f32_16x16x32_bf16 a[76:79], a[208:211], a[236:239], a[76:79]\n" \
"buffer_load_ushort v210, v195, s[76:79], 0 offen\n" \
"v_mfma_f32_16x16x32_bf16 a[92:95], a[148:151], a[172:175], a[92:95]\n" \
"ds_read_b64_tr_b16 a[188:189], v39 offset:0\n" \
"v_add3_u32 v5, v213, v225, s6\n" \
"v_mfma_f32_16x16x32_bf16 a[92:95], a[212:215], a[236:239], a[92:95]\n" \
"v_mul_u32_u24_e32 v213, 0x480, v228\n" \
"v_add3_u32 v6, v213, v227, s7\n" \
"v_mfma_f32_16x16x32_bf16 a[88:91], a[148:151], a[168:171], a[88:91]\n" \
"s_mov_b32 s76, s8\n" \
"s_and_b32 s77, s9, 0xffff\n" \
"v_mfma_f32_16x16x32_bf16 a[88:91], a[212:215], a[232:235], a[88:91]\n" \
"s_mov_b32 s78, 0x240000\n" \
"ds_read_b64_tr_b16 a[190:191], v39 offset:1024\n" \
"v_mfma_f32_16x16x32_bf16 a[84:87], a[148:151], a[164:167], a[84:87]\n" \
"v_mul_u32_u24_e32 v213, 0x480, v230\n" \
"v_add3_u32 v7, v213, v229, s40\n" \
"v_mfma_f32_16x16x32_bf16 a[84:87], a[212:215], a[228:231], a[84:87]\n" \
"ds_read_b64_tr_b16 a[252:253], v39 offset:8192\n" \
"s_lshl_b32 s7, s3, 3\n" \
"v_mfma_f32_16x16x32_bf16 a[80:83], a[148:151], a[160:163], a[80:83]\n" \
"s_lshl_b32 s40, s3, 1\n" \
"s_and_b32 s7, s7, 16\n" \
"v_mfma_f32_16x16x32_bf16 a[80:83], a[212:215], a[224:227], a[80:83]\n" \
"s_and_b32 s40, s40, 2\n" \
"s_and_b32 s6, s2, 7\n" \
"v_mfma_f32_16x16x32_bf16 a[96:99], a[152:155], a[160:163], a[96:99]\n" \
"ds_read_b64_tr_b16 a[254:255], v39 offset:9216\n" \
"v_bfe_u32 v213, v20, 7, 1\n" \
"v_mfma_f32_16x16x32_bf16 a[96:99], a[216:219], a[224:227], a[96:99]\n" \
"s_or_b32 s7, s40, s7\n" \
".fill (L_pb + 48864 - .) / 4, 4, 0xBF800000\n" \
"L_pp12:\n" \
"s_cbranch_execz L_pret\n" \
"v_and_b32_e32 v234, 0xf0, v234\n" \
"v_mfma_f32_16x16x32_bf16 a[100:103], a[152:155], a[164:167], a[100:103]\n" \
"v_cmp_gt_u32_e32 vcc, 16, v231\n" \
"v_or3_b32 v213, s7, v213, v219\n" \
"v_mfma_f32_16x16x32_bf16 a[100:103], a[216:219], a[228:231], a[100:103]\n" \
"v_and_b32_e32 v214, 32, v20\n" \
"s_lshl_b32 s6, s6, 5\n" \
"v_mfma_f32_16x16x32_bf16 a[104:107], a[152:155], a[168:171], a[104:107]\n" \
"v_cndmask_b32_e32 v231, v233, v234, vcc\n" \
"v_mul_u32_u24_e32 v213, 18, v213\n" \
"v_mfma_f32_16x16x32_bf16 a[104:107], a[216:219], a[232:235], a[104:107]\n" \
"v_mov_b32_e32 v215, 0x100\n" \
"v_mov_b32_e64 v216, s6\n" \
"v_mfma_f32_16x16x32_bf16 a[108:111], a[152:155], a[172:175], a[108:111]\n" \
"v_cmp_eq_u32_e64 vcc, 0, v214\n" \
"v_and_or_b32 v213, v218, 1, v213\n" \
"v_mfma_f32_16x16x32_bf16 a[108:111], a[216:219], a[236:239], a[108:111]\n" \
"v_lshlrev_b32_e32 v212, 3, v212\n" \
"v_cndmask_b32_e32 v214, v215, v216, vcc\n" \
"s_waitcnt lgkmcnt(15)\n" \
"v_mfma_f32_16x16x32_bf16 a[124:127], a[156:159], a[172:175], a[124:127]\n" \
"v_mad_u32_u24 v213, v213, s5, v214\n" \
"v_and_or_b32 v213, v20, 16, v213\n" \
"v_mfma_f32_16x16x32_bf16 a[124:127], a[220:223], a[236:239], a[124:127]\n" \
"v_add_u32_e32 v9, 0x4800, v213\n" \
"v_mul_u32_u24_e32 v213, 0x120, v218\n" \
"v_mfma_f32_16x16x32_bf16 a[120:123], a[156:159], a[168:171], a[120:123]\n" \
"v_and_or_b32 v10, v212, 24, v213\n" \
"v_lshrrev_b32_e32 v213, 2, v20\n" \
"v_mfma_f32_16x16x32_bf16 a[120:123], a[220:223], a[232:235], a[120:123]\n" \
"v_and_or_b32 v213, v213, 60, s4\n" \
"v_lshl_or_b32 v212, s3, 9, v212\n" \
"v_mfma_f32_16x16x32_bf16 a[116:119], a[156:159], a[164:167], a[116:119]\n" \
"v_mad_u32_u24 v4, v224, s5, v223\n" \
"v_mad_u32_u24 v8, v232, s5, v231\n" \
"v_mfma_f32_16x16x32_bf16 a[116:119], a[220:223], a[228:231], a[116:119]\n" \
"v_or_b32_e32 v18, 0x27000, v213\n" \
"v_or_b32_e32 v19, 0x27800, v212\n" \
"v_mfma_f32_16x16x32_bf16 a[112:115], a[156:159], a[160:163], a[112:115]\n" \
"v_mov_b32_e32 v215, 0x1000c0c\n" \
"v_mov_b32_e32 v216, 0xc0c0100\n" \
"v_mfma_f32_16x16x32_bf16 a[112:115], a[220:223], a[224:227], a[112:115]\n" \
"s_waitcnt vmcnt(7) lgkmcnt(0)\n" \
"s_barrier\n" \
"v_mfma_f32_16x16x32_bf16 v[52:55], a[128:131], a[176:179], v[52:55]\n" \
"s_add_u32 m0, s30, 0x0\n" \
"s_movk_i32 s84, 0x200\n" \
"v_mfma_f32_16x16x32_bf16 v[52:55], a[192:195], a[240:243], v[52:55]\n" \
"buffer_load_dwordx4 v22, s[64:67], s84 offen offset:0 lds\n" \
"v_mfma_f32_16x16x32_bf16 v[56:59], a[128:131], a[180:183], v[56:59]\n" \
"ds_read_b64_tr_b16 a[160:161], v32 offset:32768\n" \
"v_bfe_u32 v214, v20, 4, 1\n" \
"v_mfma_f32_16x16x32_bf16 v[56:59], a[192:195], a[244:247], v[56:59]\n" \
"s_movk_i32 s84, 0x1e00\n" \
"v_mfma_f32_16x16x32_bf16 v[60:63], a[128:131], a[184:187], v[60:63]\n" \
"buffer_load_dwordx4 v23, s[64:67], s84 offen offset:1024 lds\n" \
"v_mfma_f32_16x16x32_bf16 v[60:63], a[192:195], a[248:251], v[60:63]\n" \
"ds_read_b64_tr_b16 a[162:163], v32 offset:33792\n" \
"v_lshrrev_b32_e32 v212, 8, v20\n" \
"v_mfma_f32_16x16x32_bf16 v[64:67], a[128:131], a[188:191], v[64:67]\n" \
"ds_read_b64_tr_b16 a[224:225], v32 offset:40960\n" \
"v_cmp_eq_u32_e64 vcc, 0, v214\n" \
"v_mfma_f32_16x16x32_bf16 v[64:67], a[192:195], a[252:255], v[64:67]\n" \
"ds_read_b128 a[128:131], v28 offset:49152\n" \
"v_bfe_u32 v213, v20, 4, 4\n" \
"v_mfma_f32_16x16x32_bf16 v[80:83], a[132:135], a[188:191], v[80:83]\n" \
"ds_read_b128 a[192:195], v29 offset:49152\n" \
"v_mov_b32_e32 v217, 0xc0c0302\n" \
"v_mfma_f32_16x16x32_bf16 v[80:83], a[196:199], a[252:255], v[80:83]\n" \
"s_movk_i32 s84, 0x3a00\n" \
"v_mfma_f32_16x16x32_bf16 v[76:79], a[132:135], a[184:187], v[76:79]\n" \
"buffer_load_dwordx4 v22, s[64:67], s84 offen offset:2048 lds\n" \
"v_mfma_f32_16x16x32_bf16 v[76:79], a[196:199], a[248:251], v[76:79]\n" \
"s_movk_i32 s84, 0x5600\n" \
"v_mfma_f32_16x16x32_bf16 v[72:75], a[132:135], a[180:183], v[72:75]\n" \
"buffer_load_dwordx4 v23, s[64:67], s84 offen offset:3072 lds\n" \
"v_mfma_f32_16x16x32_bf16 v[72:75], a[196:199], a[244:247], v[72:75]\n" \
"s_add_u32 m0, s31, 0x0\n" \
"s_mov_b32 s84, 0x200000\n" \
"v_mfma_f32_16x16x32_bf16 v[68:71], a[132:135], a[176:179], v[68:71]\n" \
"buffer_load_dwordx4 v26, s[68:71], s84 offen offset:0 lds\n" \
"v_mfma_f32_16x16x32_bf16 v[68:71], a[196:199], a[240:243], v[68:71]\n" \
"ds_read_b128 a[132:135], v28 offset:51200\n" \
"v_cndmask_b32_e32 v215, v215, v216, vcc\n" \
"v_mfma_f32_16x16x32_bf16 v[84:87], a[136:139], a[176:179], v[84:87]\n" \
"ds_read_b128 a[196:199], v29 offset:51200\n" \
"v_mov_b32_e32 v216, 0x3020c0c\n" \
"v_mfma_f32_16x16x32_bf16 v[84:87], a[200:203], a[240:243], v[84:87]\n" \
"s_addk_i32 s84, 0x7c00\n" \
"v_mfma_f32_16x16x32_bf16 v[88:91], a[136:139], a[180:183], v[88:91]\n" \
"buffer_load_dwordx4 v26, s[68:71], s84 offen offset:1024 lds\n" \
"v_mfma_f32_16x16x32_bf16 v[88:91], a[200:203], a[244:247], v[88:91]\n" \
"ds_read_b64_tr_b16 a[226:227], v32 offset:41984\n" \
"v_lshl_or_b32 v214, v212, 2, v214\n" \
"v_mfma_f32_16x16x32_bf16 v[92:95], a[136:139], a[184:187], v[92:95]\n" \
"s_addk_i32 s84, 0x7c00\n" \
"v_mfma_f32_16x16x32_bf16 v[92:95], a[200:203], a[248:251], v[92:95]\n" \
"buffer_load_dwordx4 v27, s[68:71], s84 offen offset:2048 lds\n" \
"v_mfma_f32_16x16x32_bf16 v[96:99], a[136:139], a[188:191], v[96:99]\n" \
"ds_read_b64_tr_b16 a[164:165], v33 offset:32768\n" \
"v_cndmask_b32_e64 v216, v216, v217, vcc\n" \
"v_mfma_f32_16x16x32_bf16 v[96:99], a[200:203], a[252:255], v[96:99]\n" \
"ds_read_b128 a[136:139], v28 offset:53248\n" \
"v_mov_b32_e32 v217, 0xc0c0c0c\n" \
"v_mfma_f32_16x16x32_bf16 v[112:115], a[140:143], a[188:191], v[112:115]\n" \
"ds_read_b128 a[200:203], v29 offset:53248\n" \
"v_cmp_gt_u32_e64 vcc, v214, v213\n" \
"v_mfma_f32_16x16x32_bf16 v[112:115], a[204:207], a[252:255], v[112:115]\n" \
"s_addk_i32 s84, 0x7c00\n" \
"v_mfma_f32_16x16x32_bf16 v[108:111], a[140:143], a[184:187], v[108:111]\n" \
"buffer_load_dwordx4 v27, s[68:71], s84 offen offset:3072 lds\n" \
"v_mfma_f32_16x16x32_bf16 v[108:111], a[204:207], a[248:251], v[108:111]\n" \
"s_add_u32 m0, s30, 0x1000\n" \
"s_mov_b32 s84, 0x8200\n" \
"v_mfma_f32_16x16x32_bf16 v[104:107], a[140:143], a[180:183], v[104:107]\n" \
"buffer_load_dwordx4 v22, s[64:67], s84 offen offset:0 lds\n" \
"v_mfma_f32_16x16x32_bf16 v[104:107], a[204:207], a[244:247], v[104:107]\n" \
"ds_read_b64_tr_b16 a[166:167], v33 offset:33792\n" \
"v_or_b32_e32 v214, 2, v214\n" \
"v_mfma_f32_16x16x32_bf16 v[100:103], a[140:143], a[176:179], v[100:103]\n" \
"ds_read_b64_tr_b16 a[228:229], v33 offset:40960\n" \
"s_lshr_b32 s4, s2, 5\n" \
"v_mfma_f32_16x16x32_bf16 v[100:103], a[204:207], a[240:243], v[100:103]\n" \
"ds_read_b128 a[140:143], v28 offset:55296\n" \
"v_cndmask_b32_e32 v40, v217, v215, vcc\n" \
"v_mfma_f32_16x16x32_bf16 v[116:119], a[144:147], a[176:179], v[116:119]\n" \
"ds_read_b128 a[204:207], v29 offset:55296\n" \
"v_cndmask_b32_e64 v42, v215, v217, vcc\n" \
"v_mfma_f32_16x16x32_bf16 v[116:119], a[208:211], a[240:243], v[116:119]\n" \
"s_addk_i32 s84, 0x1c00\n" \
"v_mfma_f32_16x16x32_bf16 v[120:123], a[144:147], a[180:183], v[120:123]\n" \
"buffer_load_dwordx4 v23, s[64:67], s84 offen offset:1024 lds\n" \
"v_mfma_f32_16x16x32_bf16 v[120:123], a[208:211], a[244:247], v[120:123]\n" \
"s_addk_i32 s84, 0x1c00\n" \
"v_mfma_f32_16x16x32_bf16 v[124:127], a[144:147], a[184:187], v[124:127]\n" \
"buffer_load_dwordx4 v22, s[64:67], s84 offen offset:2048 lds\n" \
"v_mfma_f32_16x16x32_bf16 v[124:127], a[208:211], a[248:251], v[124:127]\n" \
"s_addk_i32 s84, 0x1c00\n" \
"v_mfma_f32_16x16x32_bf16 v[128:131], a[144:147], a[188:191], v[128:131]\n" \
"buffer_load_dwordx4 v23, s[64:67], s84 offen offset:3072 lds\n" \
"v_mfma_f32_16x16x32_bf16 v[128:131], a[208:211], a[252:255], v[128:131]\n" \
"ds_read_b128 a[144:147], v28 offset:32768\n" \
"v_cmp_gt_u32_e32 vcc, v214, v213\n" \
"v_mfma_f32_16x16x32_bf16 v[144:147], a[148:151], a[188:191], v[144:147]\n" \
"ds_read_b128 a[208:211], v29 offset:32768\n" \
"v_and_b32_e64 v214, 16, v20\n" \
"v_mfma_f32_16x16x32_bf16 v[144:147], a[212:215], a[252:255], v[144:147]\n" \
"s_add_u32 m0, s31, 0x4000\n" \
"s_mov_b32 s84, 0x200000\n" \
"v_mfma_f32_16x16x32_bf16 v[140:143], a[148:151], a[184:187], v[140:143]\n" \
"buffer_load_dwordx4 v26, s[72:75], s84 offen offset:0 lds\n" \
"v_mfma_f32_16x16x32_bf16 v[140:143], a[212:215], a[248:251], v[140:143]\n" \
"ds_read_b64_tr_b16 a[230:231], v33 offset:41984\n" \
"s_movk_i32 s42, 0x3f80\n" \
"v_mfma_f32_16x16x32_bf16 v[136:139], a[148:151], a[180:183], v[136:139]\n" \
"s_addk_i32 s84, 0x7c00\n" \
"v_mfma_f32_16x16x32_bf16 v[136:139], a[212:215], a[244:247], v[136:139]\n" \
"buffer_load_dwordx4 v26, s[72:75], s84 offen offset:1024 lds\n" \
"v_mfma_f32_16x16x32_bf16 v[132:135], a[148:151], a[176:179], v[132:135]\n" \
"ds_read_b64_tr_b16 a[168:169], v34 offset:32768\n" \
"s_xor_b32 s5, s4, 15\n" \
"s_nop 0\n" \
"v_mfma_f32_16x16x32_bf16 v[132:135], a[212:215], a[240:243], v[132:135]\n" \
"ds_read_b128 a[148:151], v28 offset:34816\n" \
"v_mfma_f32_16x16x32_bf16 v[148:151], a[152:155], a[176:179], v[148:151]\n" \
"ds_read_b128 a[212:215], v29 offset:34816\n" \
"v_mfma_f32_16x16x32_bf16 v[148:151], a[216:219], a[240:243], v[148:151]\n" \
"ds_read_b64_tr_b16 a[170:171], v34 offset:33792\n" \
"v_mfma_f32_16x16x32_bf16 v[152:155], a[152:155], a[180:183], v[152:155]\n" \
"ds_read_b64_tr_b16 a[232:233], v34 offset:40960\n" \
"v_mfma_f32_16x16x32_bf16 v[152:155], a[216:219], a[244:247], v[152:155]\n" \
"ds_read_b64_tr_b16 a[234:235], v34 offset:41984\n" \
"v_mfma_f32_16x16x32_bf16 v[156:159], a[152:155], a[184:187], v[156:159]\n" \
"ds_read_b64_tr_b16 a[172:173], v35 offset:32768\n" \
"v_mfma_f32_16x16x32_bf16 v[156:159], a[216:219], a[248:251], v[156:159]\n" \
"ds_read_b64_tr_b16 a[174:175], v35 offset:33792\n" \
"v_mfma_f32_16x16x32_bf16 v[160:163], a[152:155], a[188:191], v[160:163]\n" \
"ds_read_b64_tr_b16 a[236:237], v35 offset:40960\n" \
"v_mfma_f32_16x16x32_bf16 v[160:163], a[216:219], a[252:255], v[160:163]\n" \
"ds_read_b128 a[152:155], v28 offset:36864\n" \
"v_mfma_f32_16x16x32_bf16 v[176:179], a[156:159], a[188:191], v[176:179]\n" \
"ds_read_b128 a[216:219], v29 offset:36864\n" \
"v_mfma_f32_16x16x32_bf16 v[176:179], a[220:223], a[252:255], v[176:179]\n" \
"ds_read_b64_tr_b16 a[238:239], v35 offset:41984\n" \
"v_mfma_f32_16x16x32_bf16 v[172:175], a[156:159], a[184:187], v[172:175]\n" \
"s_cmpk_lt_u32 s3, 2\n" \
"s_cselect_b32 s6, s5, s4\n" \
"v_mfma_f32_16x16x32_bf16 v[172:175], a[220:223], a[248:251], v[172:175]\n" \
"s_lshl_b32 s7, s6, 7\n" \
"v_and_b32_e32 v213, 0x320, v20\n" \
"v_mfma_f32_16x16x32_bf16 v[168:171], a[156:159], a[180:183], v[168:171]\n" \
"s_movk_i32 s41, 0x100\n" \
"v_lshlrev_b32_e64 v214, v214, s42\n" \
"v_mfma_f32_16x16x32_bf16 v[168:171], a[220:223], a[244:247], v[168:171]\n" \
"v_mov_b32_e32 v215, 0\n" \
"s_add_i32 s40, s7, -16\n" \
"v_mfma_f32_16x16x32_bf16 v[164:167], a[156:159], a[176:179], v[164:167]\n" \
"v_cndmask_b32_e32 v41, v217, v216, vcc\n" \
"v_cndmask_b32_e32 v43, v216, v217, vcc\n" \
"v_mfma_f32_16x16x32_bf16 v[164:167], a[220:223], a[240:243], v[164:167]\n" \
"ds_read_b128 a[156:159], v28 offset:38912\n" \
"ds_read_b128 a[220:223], v29 offset:38912\n" \
"s_waitcnt lgkmcnt(15)\n" \
"v_mfma_f32_16x16x32_bf16 a[0:3], a[128:131], a[160:163], a[0:3]\n" \
"s_addk_i32 s84, 0x7c00\n" \
"v_mfma_f32_16x16x32_bf16 a[0:3], a[192:195], a[224:227], a[0:3]\n" \
"buffer_load_dwordx4 v27, s[72:75], s84 offen offset:2048 lds\n" \
"v_mfma_f32_16x16x32_bf16 a[4:7], a[128:131], a[164:167], a[4:7]\n" \
"ds_read_b64_tr_b16 a[176:177], v36 offset:32768\n" \
"v_cmp_eq_u32_e32 vcc, s41, v213\n" \
"s_waitcnt lgkmcnt(15)\n" \
"v_mfma_f32_16x16x32_bf16 a[4:7], a[192:195], a[228:231], a[4:7]\n" \
"s_addk_i32 s84, 0x7c00\n" \
"s_waitcnt lgkmcnt(11)\n" \
"v_mfma_f32_16x16x32_bf16 a[8:11], a[128:131], a[168:171], a[8:11]\n" \
"buffer_load_dwordx4 v27, s[72:75], s84 offen offset:3072 lds\n" \
"s_waitcnt lgkmcnt(9)\n" \
"v_mfma_f32_16x16x32_bf16 a[8:11], a[192:195], a[232:235], a[8:11]\n" \
"ds_read_b64_tr_b16 a[178:179], v36 offset:33792\n" \
"s_mov_b32 s41, 0\n" \
"s_waitcnt lgkmcnt(8)\n" \
"v_mfma_f32_16x16x32_bf16 a[12:15], a[128:131], a[172:175], a[12:15]\n" \
"ds_read_b64_tr_b16 a[240:241], v36 offset:40960\n" \
"s_and_b32 s43, s3, 1\n" \
"s_waitcnt lgkmcnt(5)\n" \
"v_mfma_f32_16x16x32_bf16 a[12:15], a[192:195], a[236:239], a[12:15]\n" \
"v_cndmask_b32_e32 v46, v215, v214, vcc\n" \
"v_and_b32_e32 v213, 0x330, v20\n" \
"v_mfma_f32_16x16x32_bf16 a[28:31], a[132:135], a[172:175], a[28:31]\n" \
"s_cmp_lg_u32 s7, 0\n" \
"s_cselect_b32 s40, s40, s41\n" \
"v_mfma_f32_16x16x32_bf16 a[28:31], a[196:199], a[236:239], a[28:31]\n" \
"ds_read_b64_tr_b16 a[242:243], v36 offset:41984\n" \
"s_mov_b32 s42, 0xff800000\n" \
"v_mfma_f32_16x16x32_bf16 a[24:27], a[132:135], a[168:171], a[24:27]\n" \
"s_cmp_lg_u32 s7, 0\n" \
"s_cselect_b32 s47, s41, s42\n" \
"v_mfma_f32_16x16x32_bf16 a[24:27], a[196:199], a[232:235], a[24:27]\n" \
"s_lshl_b32 s41, s43, 6\n" \
"v_lshrrev_b32_e32 v215, 6, v20\n" \
"v_mfma_f32_16x16x32_bf16 a[20:23], a[132:135], a[164:167], a[20:23]\n" \
"v_mov_b32_e32 v214, 0x3f80\n" \
"v_cmp_eq_u32_e64 vcc, 32, v213\n" \
"v_mfma_f32_16x16x32_bf16 a[20:23], a[196:199], a[228:231], a[20:23]\n" \
"ds_read_b64_tr_b16 a[180:181], v37 offset:32768\n" \
"s_or_b32 s7, s41, s7\n" \
"v_mfma_f32_16x16x32_bf16 a[16:19], a[132:135], a[160:163], a[16:19]\n" \
"v_lshrrev_b32_e32 v213, 7, v20\n" \
"v_bfe_u32 v216, v20, 6, 2\n" \
"v_mfma_f32_16x16x32_bf16 a[16:19], a[196:199], a[224:227], a[16:19]\n" \
"v_lshrrev_b32_e64 v219, 1, v20\n" \
"v_bfe_u32 v215, v215, 1, 1\n" \
"v_mfma_f32_16x16x32_bf16 a[32:35], a[136:139], a[160:163], a[32:35]\n" \
"s_waitcnt vmcnt(16)\n" \
"v_or_b32_e32 v204, v210, v204\n" \
"v_or_b32_e64 v206, v210, v206\n" \
"v_mfma_f32_16x16x32_bf16 a[32:35], a[200:203], a[224:227], a[32:35]\n" \
"v_mov_b32_e64 v210, 0\n" \
"s_add_u32 s88, s35, 0x1000\n" \
"v_mfma_f32_16x16x32_bf16 a[36:39], a[136:139], a[164:167], a[36:39]\n" \
"ds_read_b64_tr_b16 a[182:183], v37 offset:33792\n" \
"v_cndmask_b32_e64 v47, 0, v214, vcc\n" \
"v_mfma_f32_16x16x32_bf16 a[36:39], a[200:203], a[228:231], a[36:39]\n" \
"ds_read_b64_tr_b16 a[244:245], v37 offset:40960\n" \
"v_or_b32_e64 v214, s7, v213\n" \
"v_mfma_f32_16x16x32_bf16 a[40:43], a[136:139], a[168:171], a[40:43]\n" \
"s_mov_b32 m0, s32\n" \
"v_lshl_or_b32 v217, s43, 3, v213\n" \
"v_mfma_f32_16x16x32_bf16 a[40:43], a[200:203], a[232:235], a[40:43]\n" \
"s_mov_b32 exec_lo, 0xffffffff\n" \
"s_mov_b32 exec_hi, 0x0\n" \
"global_load_lds_dword v[188:189], off\n" \
"s_mov_b64 exec, -1\n" \
"v_mfma_f32_16x16x32_bf16 a[44:47], a[136:139], a[172:175], a[44:47]\n" \
"ds_read_b64_tr_b16 a[246:247], v37 offset:41984\n" \
"v_lshlrev_b32_e64 v218, 10, v212\n" \
"v_mfma_f32_16x16x32_bf16 a[44:47], a[200:203], a[236:239], a[44:47]\n" \
"v_lshlrev_b32_e32 v216, 7, v216\n" \
"v_and_b32_e32 v219, 24, v219\n" \
"v_mfma_f32_16x16x32_bf16 a[60:63], a[140:143], a[172:175], a[60:63]\n" \
"ds_write_b128 v196, v[200:203]\n" \
"v_mfma_f32_16x16x32_bf16 a[60:63], a[204:207], a[236:239], a[60:63]\n" \
"v_and_or_b32 v213, v213, 2, v215\n" \
"v_add_u32_e64 v217, s40, v217\n" \
"v_mfma_f32_16x16x32_bf16 a[56:59], a[140:143], a[168:171], a[56:59]\n" \
"ds_read_b64_tr_b16 a[184:185], v38 offset:32768\n" \
"v_or3_b32 v216, v216, v218, v219\n" \
"v_mfma_f32_16x16x32_bf16 a[56:59], a[204:207], a[232:235], a[56:59]\n" \
"v_lshlrev_b32_e64 v213, 5, v213\n" \
"s_mov_b32 s40, 0x14000\n" \
"v_mfma_f32_16x16x32_bf16 a[52:55], a[140:143], a[164:167], a[52:55]\n" \
"s_lshl_b32 s7, s2, 2\n" \
"v_or3_b32 v48, v216, v213, s40\n" \
"v_mfma_f32_16x16x32_bf16 a[52:55], a[204:207], a[228:231], a[52:55]\n" \
"ds_read_b64_tr_b16 a[186:187], v38 offset:33792\n" \
"s_lshl_b32 s40, s43, 2\n" \
"v_mfma_f32_16x16x32_bf16 a[48:51], a[140:143], a[160:163], a[48:51]\n" \
"s_lshl_b32 s6, s6, 3\n" \
"v_or_b32_e32 v213, s40, v212\n" \
"v_mfma_f32_16x16x32_bf16 a[48:51], a[204:207], a[224:227], a[48:51]\n" \
"s_or_b32 s6, s6, s40\n" \
"s_and_b32 s40, s7, 0x7c\n" \
"v_mfma_f32_16x16x32_bf16 a[64:67], a[144:147], a[160:163], a[64:67]\n" \
"ds_write_b128 v197, v[204:207]\n" \
"v_mfma_f32_16x16x32_bf16 a[64:67], a[208:211], a[224:227], a[64:67]\n" \
"ds_read_b64_tr_b16 a[248:249], v38 offset:40960\n" \
"s_or_b32 s40, s40, s3\n" \
"v_mfma_f32_16x16x32_bf16 a[68:71], a[144:147], a[164:167], a[68:71]\n" \
"s_cmpk_lt_u32 s40, 64\n" \
"s_cselect_b32 s41, s4, s5\n" \
"v_mfma_f32_16x16x32_bf16 a[68:71], a[208:211], a[228:231], a[68:71]\n" \
"ds_read_b64_tr_b16 a[250:251], v38 offset:41984\n" \
"s_lshl_b32 s40, s40, 1\n" \
"v_mfma_f32_16x16x32_bf16 a[72:75], a[144:147], a[168:171], a[72:75]\n" \
"s_lshl_b32 s41, s41, 7\n" \
"s_and_b32 s40, s40, 0x7e\n" \
"v_mfma_f32_16x16x32_bf16 a[72:75], a[208:211], a[232:235], a[72:75]\n" \
"s_or_b32 s40, s41, s40\n" \
"s_mulk_i32 s40, 0x480\n" \
"v_mfma_f32_16x16x32_bf16 a[76:79], a[144:147], a[172:175], a[76:79]\n" \
"v_add_u32_e64 v49, s40, v20\n" \
"s_mov_b32 s40, 0xc000\n" \
"v_mfma_f32_16x16x32_bf16 a[76:79], a[208:211], a[236:239], a[76:79]\n" \
"ds_read_b64_tr_b16 a[188:189], v39 offset:32768\n" \
"s_and_b32 s7, s7, 28\n" \
"v_mfma_f32_16x16x32_bf16 a[92:95], a[148:151], a[172:175], a[92:95]\n" \
"ds_write_b128 v198, v[208:211]\n" \
"v_mfma_f32_16x16x32_bf16 a[92:95], a[212:215], a[236:239], a[92:95]\n" \
"s_bfe_u32 s45, s2, 0x20003\n" \
"s_mov_b32 s41, 0x8000\n" \
"v_mfma_f32_16x16x32_bf16 a[88:91], a[148:151], a[168:171], a[88:91]\n" \
"ds_read_b64_tr_b16 a[190:191], v39 offset:33792\n" \
"v_mfma_f32_16x16x32_bf16 a[88:91], a[212:215], a[232:235], a[88:91]\n" \
"s_cmp_lg_u32 s4, 0\n" \
"s_cselect_b32 s51, s40, s41\n" \
"v_mfma_f32_16x16x32_bf16 a[84:87], a[148:151], a[164:167], a[84:87]\n" \
"s_movk_i32 s40, 0x4000\n" \
"s_lshr_b32 s44, s3, 1\n" \
"v_mfma_f32_16x16x32_bf16 a[84:87], a[212:215], a[228:231], a[84:87]\n" \
"ds_read_b64_tr_b16 a[252:253], v39 offset:40960\n" \
"s_or_b32 s46, s7, s45\n" \
"v_mfma_f32_16x16x32_bf16 a[80:83], a[148:151], a[160:163], a[80:83]\n" \
"s_cmp_lg_u32 s4, 0\n" \
"s_cselect_b32 s53, s41, s40\n" \
"v_mfma_f32_16x16x32_bf16 a[80:83], a[212:215], a[224:227], a[80:83]\n" \
"s_lshl_b32 s40, s44, 15\n" \
"s_lshl_b32 s41, s43, 14\n" \
"v_mfma_f32_16x16x32_bf16 a[96:99], a[152:155], a[160:163], a[96:99]\n" \
"ds_read_b64_tr_b16 a[254:255], v39 offset:41984\n" \
"s_lshl_b32 s48, s46, 20\n" \
"v_mfma_f32_16x16x32_bf16 a[96:99], a[216:219], a[224:227], a[96:99]\n" \
"s_or_b32 s41, s40, s41\n" \
"s_add_i32 s49, s48, 0x1000000\n" \
"v_mfma_f32_16x16x32_bf16 a[100:103], a[152:155], a[164:167], a[100:103]\n" \
"s_or_b32 s42, s49, s41\n" \
"s_lshl_b32 s4, s4, 16\n" \
"v_mfma_f32_16x16x32_bf16 a[100:103], a[216:219], a[228:231], a[100:103]\n" \
"s_or_b32 s41, s42, s4\n" \
"s_lshl_b32 s4, s5, 16\n" \
"v_mfma_f32_16x16x32_bf16 a[104:107], a[152:155], a[168:171], a[104:107]\n" \
"s_mul_i32 s5, s6, 0x24800\n" \
"s_lshl_b32 s6, s46, 9\n" \
"v_mfma_f32_16x16x32_bf16 a[104:107], a[216:219], a[232:235], a[104:107]\n" \
"s_lshl_b32 s7, s7, 6\n" \
"s_add_i32 s6, s5, s6\n" \
"v_mfma_f32_16x16x32_bf16 a[108:111], a[152:155], a[172:175], a[108:111]\n" \
"s_or_b32 s5, s5, s7\n" \
"s_lshl_b32 s7, s45, 2\n" \
"v_mfma_f32_16x16x32_bf16 a[108:111], a[216:219], a[236:239], a[108:111]\n" \
"s_or_b32 s5, s5, s7\n" \
"s_lshl_b32 s7, s3, 12\n" \
"s_waitcnt lgkmcnt(15)\n" \
"v_mfma_f32_16x16x32_bf16 a[124:127], a[156:159], a[172:175], a[124:127]\n" \
"s_or_b32 s50, s49, s7\n" \
"s_lshl_b32 s7, s3, 11\n" \
"v_mfma_f32_16x16x32_bf16 a[124:127], a[220:223], a[236:239], a[124:127]\n" \
"v_lshrrev_b32_e64 v215, 4, v20\n" \
"v_bfe_u32 v216, v20, 4, 3\n" \
"v_mfma_f32_16x16x32_bf16 a[120:123], a[156:159], a[168:171], a[120:123]\n" \
"s_or_b32 s42, s42, s4\n" \
"s_lshl_b32 s4, s43, 11\n" \
"v_mfma_f32_16x16x32_bf16 a[120:123], a[220:223], a[232:235], a[120:123]\n" \
"s_add_i32 s52, s48, s7\n" \
"s_add_i32 s53, s53, s7\n" \
"v_mfma_f32_16x16x32_bf16 a[116:119], a[156:159], a[164:167], a[116:119]\n" \
"v_mul_u32_u24_e32 v214, 0x480, v214\n" \
"v_bitop3_b32 v218, v212, v215, 7 bitop3:0x78\n" \
"v_mfma_f32_16x16x32_bf16 a[116:119], a[220:223], a[228:231], a[116:119]\n" \
"v_bitop3_b32 v212, v212, v216, 4 bitop3:0x36\n" \
"s_lshl_b32 s7, s3, 10\n" \
"v_mfma_f32_16x16x32_bf16 a[112:115], a[156:159], a[160:163], a[112:115]\n" \
"s_or_b32 s4, s40, s4\n" \
"v_lshl_or_b32 v25, v212, 4, v214\n" \
"v_mfma_f32_16x16x32_bf16 a[112:115], a[220:223], a[224:227], a[112:115]\n" \
"s_waitcnt vmcnt(1) lgkmcnt(0)\n" \
"s_barrier\n" \
"v_mfma_f32_16x16x32_bf16 v[52:55], a[128:131], a[176:179], v[52:55]\n" \
"s_add_u32 m0, s30, 0x8000\n" \
"s_movk_i32 s84, 0x280\n" \
"v_mfma_f32_16x16x32_bf16 v[52:55], a[192:195], a[240:243], v[52:55]\n" \
"buffer_load_dwordx4 v22, s[64:67], s84 offen offset:0 lds\n" \
"v_mfma_f32_16x16x32_bf16 v[56:59], a[128:131], a[180:183], v[56:59]\n" \
"s_movk_i32 s84, 0x1e80\n" \
"v_mfma_f32_16x16x32_bf16 v[56:59], a[192:195], a[244:247], v[56:59]\n" \
"buffer_load_dwordx4 v23, s[64:67], s84 offen offset:1024 lds\n" \
"v_mfma_f32_16x16x32_bf16 v[60:63], a[128:131], a[184:187], v[60:63]\n" \
"ds_read_b64_tr_b16 a[160:161], v32 offset:0\n" \
"v_mul_u32_u24_e32 v212, 0x480, v217\n" \
"v_mfma_f32_16x16x32_bf16 v[60:63], a[192:195], a[248:251], v[60:63]\n" \
"s_movk_i32 s84, 0x3a80\n" \
"v_mfma_f32_16x16x32_bf16 v[64:67], a[128:131], a[188:191], v[64:67]\n" \
"buffer_load_dwordx4 v22, s[64:67], s84 offen offset:2048 lds\n" \
"v_mfma_f32_16x16x32_bf16 v[64:67], a[192:195], a[252:255], v[64:67]\n" \
"ds_read_b128 a[128:131], v28 offset:16384\n" \
"v_bitop3_b32 v213, v215, v213, 7 bitop3:0x6c\n" \
"v_mfma_f32_16x16x32_bf16 v[80:83], a[132:135], a[188:191], v[80:83]\n" \
"ds_read_b128 a[192:195], v29 offset:16384\n" \
"s_or_b32 s40, s7, 0x14000\n" \
"v_mfma_f32_16x16x32_bf16 v[80:83], a[196:199], a[252:255], v[80:83]\n" \
"s_movk_i32 s84, 0x5680\n" \
"v_mfma_f32_16x16x32_bf16 v[76:79], a[132:135], a[184:187], v[76:79]\n" \
"buffer_load_dwordx4 v23, s[64:67], s84 offen offset:3072 lds\n" \
"v_mfma_f32_16x16x32_bf16 v[76:79], a[196:199], a[248:251], v[76:79]\n" \
"s_add_u32 m0, s31, 0x8000\n" \
"s_mov_b32 s84, 0x280000\n" \
"v_mfma_f32_16x16x32_bf16 v[72:75], a[132:135], a[180:183], v[72:75]\n" \
"buffer_load_dwordx4 v26, s[68:71], s84 offen offset:0 lds\n" \
"v_mfma_f32_16x16x32_bf16 v[72:75], a[196:199], a[244:247], v[72:75]\n" \
"ds_read_b64_tr_b16 a[162:163], v32 offset:1024\n" \
"s_lshl_b32 s43, s44, 13\n" \
"v_mfma_f32_16x16x32_bf16 v[68:71], a[132:135], a[176:179], v[68:71]\n" \
"ds_read_b64_tr_b16 a[224:225], v32 offset:8192\n" \
"s_mulk_i32 s44, 0xe800\n" \
"v_mfma_f32_16x16x32_bf16 v[68:71], a[196:199], a[240:243], v[68:71]\n" \
"ds_read_b128 a[132:135], v28 offset:18432\n" \
"s_lshl_b32 s7, s3, 8\n" \
"v_mfma_f32_16x16x32_bf16 v[84:87], a[136:139], a[176:179], v[84:87]\n" \
"ds_read_b128 a[196:199], v29 offset:18432\n" \
"v_lshl_or_b32 v24, v218, 4, v214\n" \
"v_mfma_f32_16x16x32_bf16 v[84:87], a[200:203], a[240:243], v[84:87]\n" \
"s_addk_i32 s84, 0x7c00\n" \
"v_mfma_f32_16x16x32_bf16 v[88:91], a[136:139], a[180:183], v[88:91]\n" \
"buffer_load_dwordx4 v26, s[68:71], s84 offen offset:1024 lds\n" \
"v_mfma_f32_16x16x32_bf16 v[88:91], a[200:203], a[244:247], v[88:91]\n" \
"s_addk_i32 s84, 0x7c00\n" \
"v_mfma_f32_16x16x32_bf16 v[92:95], a[136:139], a[184:187], v[92:95]\n" \
"buffer_load_dwordx4 v27, s[68:71], s84 offen offset:2048 lds\n" \
"v_mfma_f32_16x16x32_bf16 v[92:95], a[200:203], a[248:251], v[92:95]\n" \
"s_addk_i32 s84, 0x7c00\n" \
"v_mfma_f32_16x16x32_bf16 v[96:99], a[136:139], a[188:191], v[96:99]\n" \
"buffer_load_dwordx4 v27, s[68:71], s84 offen offset:3072 lds\n" \
"v_mfma_f32_16x16x32_bf16 v[96:99], a[200:203], a[252:255], v[96:99]\n" \
"ds_read_b128 a[136:139], v28 offset:20480\n" \
"v_lshl_or_b32 v21, v213, 4, v212\n" \
"v_mfma_f32_16x16x32_bf16 v[112:115], a[140:143], a[188:191], v[112:115]\n" \
"ds_read_b128 a[200:203], v29 offset:20480\n" \
"s_add_i32 s44, s44, 0x17000\n" \
"v_mfma_f32_16x16x32_bf16 v[112:115], a[204:207], a[252:255], v[112:115]\n" \
"s_add_u32 m0, s30, 0x9000\n" \
"s_mov_b32 s84, 0x8280\n" \
"v_mfma_f32_16x16x32_bf16 v[108:111], a[140:143], a[184:187], v[108:111]\n" \
"buffer_load_dwordx4 v22, s[64:67], s84 offen offset:0 lds\n" \
"v_mfma_f32_16x16x32_bf16 v[108:111], a[204:207], a[248:251], v[108:111]\n" \
"s_addk_i32 s84, 0x1c00\n" \
"v_mfma_f32_16x16x32_bf16 v[104:107], a[140:143], a[180:183], v[104:107]\n" \
"buffer_load_dwordx4 v23, s[64:67], s84 offen offset:1024 lds\n" \
"v_mfma_f32_16x16x32_bf16 v[104:107], a[204:207], a[244:247], v[104:107]\n" \
"s_addk_i32 s84, 0x1c00\n" \
"v_mfma_f32_16x16x32_bf16 v[100:103], a[140:143], a[176:179], v[100:103]\n" \
"buffer_load_dwordx4 v22, s[64:67], s84 offen offset:2048 lds\n" \
"v_mfma_f32_16x16x32_bf16 v[100:103], a[204:207], a[240:243], v[100:103]\n" \
"ds_read_b128 a[140:143], v28 offset:22528\n" \
"s_or_b32 s45, s7, 0x27000\n" \
"v_mfma_f32_16x16x32_bf16 v[116:119], a[144:147], a[176:179], v[116:119]\n" \
"ds_read_b128 a[204:207], v29 offset:22528\n" \
"s_or_b32 s46, s4, 0x10000\n" \
"v_mfma_f32_16x16x32_bf16 v[116:119], a[208:211], a[240:243], v[116:119]\n" \
"s_addk_i32 s84, 0x1c00\n" \
"v_mfma_f32_16x16x32_bf16 v[120:123], a[144:147], a[180:183], v[120:123]\n" \
"buffer_load_dwordx4 v23, s[64:67], s84 offen offset:3072 lds\n" \
"v_mfma_f32_16x16x32_bf16 v[120:123], a[208:211], a[244:247], v[120:123]\n" \
"s_add_u32 m0, s31, 0xc000\n" \
"s_mov_b32 s84, 0x280000\n" \
"v_mfma_f32_16x16x32_bf16 v[124:127], a[144:147], a[184:187], v[124:127]\n" \
"buffer_load_dwordx4 v26, s[72:75], s84 offen offset:0 lds\n" \
"v_mfma_f32_16x16x32_bf16 v[124:127], a[208:211], a[248:251], v[124:127]\n" \
"s_addk_i32 s84, 0x7c00\n" \
"v_mfma_f32_16x16x32_bf16 v[128:131], a[144:147], a[188:191], v[128:131]\n" \
"buffer_load_dwordx4 v26, s[72:75], s84 offen offset:1024 lds\n" \
"v_mfma_f32_16x16x32_bf16 v[128:131], a[208:211], a[252:255], v[128:131]\n" \
"ds_read_b128 a[144:147], v28 offset:0\n" \
"s_add_i32 s48, s6, 0x3000000\n" \
"v_mfma_f32_16x16x32_bf16 v[144:147], a[148:151], a[188:191], v[144:147]\n" \
"ds_read_b128 a[208:211], v29 offset:0\n" \
"s_add_i32 s49, s5, 0x3004000\n" \
"v_mfma_f32_16x16x32_bf16 v[144:147], a[212:215], a[252:255], v[144:147]\n" \
"ds_read_b128 v[200:203], v199 offset:0\n" \
"s_add_i32 s52, s52, 0x1010000\n" \
"v_mfma_f32_16x16x32_bf16 v[140:143], a[148:151], a[184:187], v[140:143]\n" \
"ds_read_b128 v[204:207], v199 offset:4096\n" \
"v_mfma_f32_16x16x32_bf16 v[140:143], a[212:215], a[248:251], v[140:143]\n" \
"ds_read_b64_tr_b16 a[226:227], v32 offset:9216\n" \
"v_mfma_f32_16x16x32_bf16 v[136:139], a[148:151], a[180:183], v[136:139]\n" \
"ds_read_b64_tr_b16 a[164:165], v33 offset:0\n" \
"v_mfma_f32_16x16x32_bf16 v[136:139], a[212:215], a[244:247], v[136:139]\n" \
"ds_read_b128 v[208:211], v199 offset:8192\n" \
"v_mfma_f32_16x16x32_bf16 v[132:135], a[148:151], a[176:179], v[132:135]\n" \
"ds_read_b64_tr_b16 a[166:167], v33 offset:1024\n" \
"v_mfma_f32_16x16x32_bf16 v[132:135], a[212:215], a[240:243], v[132:135]\n" \
"ds_read_b128 a[148:151], v28 offset:2048\n" \
"v_mfma_f32_16x16x32_bf16 v[148:151], a[152:155], a[176:179], v[148:151]\n" \
"ds_read_b128 a[212:215], v29 offset:2048\n" \
"v_mfma_f32_16x16x32_bf16 v[148:151], a[216:219], a[240:243], v[148:151]\n" \
"ds_read_b64_tr_b16 a[228:229], v33 offset:8192\n" \
"v_mfma_f32_16x16x32_bf16 v[152:155], a[152:155], a[180:183], v[152:155]\n" \
"ds_read_b64_tr_b16 a[230:231], v33 offset:9216\n" \
"v_mfma_f32_16x16x32_bf16 v[152:155], a[216:219], a[244:247], v[152:155]\n" \
"ds_read_b64_tr_b16 a[168:169], v34 offset:0\n" \
"v_mfma_f32_16x16x32_bf16 v[156:159], a[152:155], a[184:187], v[156:159]\n" \
"ds_read_b64_tr_b16 a[170:171], v34 offset:1024\n" \
"v_mfma_f32_16x16x32_bf16 v[156:159], a[216:219], a[248:251], v[156:159]\n" \
"ds_read_b64_tr_b16 a[232:233], v34 offset:8192\n" \
"v_mfma_f32_16x16x32_bf16 v[160:163], a[152:155], a[188:191], v[160:163]\n" \
"ds_read_b64_tr_b16 a[234:235], v34 offset:9216\n" \
"v_mfma_f32_16x16x32_bf16 v[160:163], a[216:219], a[252:255], v[160:163]\n" \
"ds_read_b128 a[152:155], v28 offset:4096\n" \
"v_mfma_f32_16x16x32_bf16 v[176:179], a[156:159], a[188:191], v[176:179]\n" \
"ds_read_b128 a[216:219], v29 offset:4096\n" \
"v_mfma_f32_16x16x32_bf16 v[176:179], a[220:223], a[252:255], v[176:179]\n" \
"ds_read_b64_tr_b16 a[172:173], v35 offset:0\n" \
"v_mfma_f32_16x16x32_bf16 v[172:175], a[156:159], a[184:187], v[172:175]\n" \
"ds_read_b64_tr_b16 a[174:175], v35 offset:1024\n" \
"v_mfma_f32_16x16x32_bf16 v[172:175], a[220:223], a[248:251], v[172:175]\n" \
"ds_read_b64_tr_b16 a[236:237], v35 offset:8192\n" \
"v_mfma_f32_16x16x32_bf16 v[168:171], a[156:159], a[180:183], v[168:171]\n" \
"ds_read_b64_tr_b16 a[238:239], v35 offset:9216\n" \
"v_mfma_f32_16x16x32_bf16 v[168:171], a[220:223], a[244:247], v[168:171]\n" \
"v_mfma_f32_16x16x32_bf16 v[164:167], a[156:159], a[176:179], v[164:167]\n" \
"v_mfma_f32_16x16x32_bf16 v[164:167], a[220:223], a[240:243], v[164:167]\n" \
"ds_read_b128 a[156:159], v28 offset:6144\n" \
"ds_read_b128 a[220:223], v29 offset:6144\n" \
"s_waitcnt lgkmcnt(15)\n" \
"v_mfma_f32_16x16x32_bf16 a[0:3], a[128:131], a[160:163], a[0:3]\n" \
"s_addk_i32 s84, 0x7c00\n" \
"v_mfma_f32_16x16x32_bf16 a[0:3], a[192:195], a[224:227], a[0:3]\n" \
"buffer_load_dwordx4 v27, s[72:75], s84 offen offset:2048 lds\n" \
"v_mfma_f32_16x16x32_bf16 a[4:7], a[128:131], a[164:167], a[4:7]\n" \
"ds_read_b64_tr_b16 a[176:177], v36 offset:0\n" \
"s_waitcnt lgkmcnt(13)\n" \
"v_mfma_f32_16x16x32_bf16 a[4:7], a[192:195], a[228:231], a[4:7]\n" \
"s_addk_i32 s84, 0x7c00\n" \
"s_waitcnt lgkmcnt(11)\n" \
"v_mfma_f32_16x16x32_bf16 a[8:11], a[128:131], a[168:171], a[8:11]\n" \
"buffer_load_dwordx4 v27, s[72:75], s84 offen offset:3072 lds\n" \
"s_waitcnt lgkmcnt(9)\n" \
"v_mfma_f32_16x16x32_bf16 a[8:11], a[192:195], a[232:235], a[8:11]\n" \
"ds_read_b64_tr_b16 a[178:179], v36 offset:1024\n" \
"s_waitcnt lgkmcnt(6)\n" \
"v_mfma_f32_16x16x32_bf16 a[12:15], a[128:131], a[172:175], a[12:15]\n" \
"ds_read_b64_tr_b16 a[240:241], v36 offset:8192\n" \
"s_waitcnt lgkmcnt(5)\n" \
"v_mfma_f32_16x16x32_bf16 a[12:15], a[192:195], a[236:239], a[12:15]\n" \
"v_mfma_f32_16x16x32_bf16 a[28:31], a[132:135], a[172:175], a[28:31]\n" \
"v_mfma_f32_16x16x32_bf16 a[28:31], a[196:199], a[236:239], a[28:31]\n" \
"ds_read_b64_tr_b16 a[242:243], v36 offset:9216\n" \
"v_mfma_f32_16x16x32_bf16 a[24:27], a[132:135], a[168:171], a[24:27]\n" \
"v_mfma_f32_16x16x32_bf16 a[24:27], a[196:199], a[232:235], a[24:27]\n" \
"v_mfma_f32_16x16x32_bf16 a[20:23], a[132:135], a[164:167], a[20:23]\n" \
"v_mfma_f32_16x16x32_bf16 a[20:23], a[196:199], a[228:231], a[20:23]\n" \
"ds_read_b64_tr_b16 a[180:181], v37 offset:0\n" \
"v_mfma_f32_16x16x32_bf16 a[16:19], a[132:135], a[160:163], a[16:19]\n" \
"v_mfma_f32_16x16x32_bf16 a[16:19], a[196:199], a[224:227], a[16:19]\n" \
"v_mfma_f32_16x16x32_bf16 a[32:35], a[136:139], a[160:163], a[32:35]\n" \
"ds_read_b64_tr_b16 a[182:183], v37 offset:1024\n" \
"v_mfma_f32_16x16x32_bf16 a[32:35], a[200:203], a[224:227], a[32:35]\n" \
"v_mfma_f32_16x16x32_bf16 a[36:39], a[136:139], a[164:167], a[36:39]\n" \
"v_mfma_f32_16x16x32_bf16 a[36:39], a[200:203], a[228:231], a[36:39]\n" \
"buffer_store_dwordx4 v[200:203], v20, s[80:83], s35 offen sc1\n" \
"v_mfma_f32_16x16x32_bf16 a[40:43], a[136:139], a[168:171], a[40:43]\n" \
"s_mov_b32 m0, s32\n" \
".fill (L_pb + 52960 - .) / 4, 4, 0xBF800000\n" \
"L_pp13:\n" \
"s_cbranch_execz L_pret\n" \
"ds_read_b64_tr_b16 a[244:245], v37 offset:8192\n" \
"v_mfma_f32_16x16x32_bf16 a[40:43], a[200:203], a[232:235], a[40:43]\n" \
"s_mov_b32 exec_lo, 0x0\n" \
"s_mov_b32 exec_hi, 0xffffffff\n" \
"global_load_lds_dword v[188:189], off\n" \
"s_mov_b64 exec, -1\n" \
"v_mfma_f32_16x16x32_bf16 a[44:47], a[136:139], a[172:175], a[44:47]\n" \
"ds_read_b64_tr_b16 a[246:247], v37 offset:9216\n" \
"v_mfma_f32_16x16x32_bf16 a[44:47], a[200:203], a[236:239], a[44:47]\n" \
"v_mfma_f32_16x16x32_bf16 a[60:63], a[140:143], a[172:175], a[60:63]\n" \
"v_mfma_f32_16x16x32_bf16 a[60:63], a[204:207], a[236:239], a[60:63]\n" \
"v_mfma_f32_16x16x32_bf16 a[56:59], a[140:143], a[168:171], a[56:59]\n" \
"ds_read_b64_tr_b16 a[184:185], v38 offset:0\n" \
"v_mfma_f32_16x16x32_bf16 a[56:59], a[204:207], a[232:235], a[56:59]\n" \
"v_mfma_f32_16x16x32_bf16 a[52:55], a[140:143], a[164:167], a[52:55]\n" \
"buffer_store_dwordx4 v[204:207], v20, s[80:83], s88 offen sc1\n" \
"v_mfma_f32_16x16x32_bf16 a[52:55], a[204:207], a[228:231], a[52:55]\n" \
"ds_read_b64_tr_b16 a[186:187], v38 offset:1024\n" \
"v_mfma_f32_16x16x32_bf16 a[48:51], a[140:143], a[160:163], a[48:51]\n" \
"v_mfma_f32_16x16x32_bf16 a[48:51], a[204:207], a[224:227], a[48:51]\n" \
"v_mfma_f32_16x16x32_bf16 a[64:67], a[144:147], a[160:163], a[64:67]\n" \
"ds_read_b64_tr_b16 a[248:249], v38 offset:8192\n" \
"v_mfma_f32_16x16x32_bf16 a[64:67], a[208:211], a[224:227], a[64:67]\n" \
"v_mfma_f32_16x16x32_bf16 a[68:71], a[144:147], a[164:167], a[68:71]\n" \
"v_mfma_f32_16x16x32_bf16 a[68:71], a[208:211], a[228:231], a[68:71]\n" \
"ds_read_b64_tr_b16 a[250:251], v38 offset:9216\n" \
"v_mfma_f32_16x16x32_bf16 a[72:75], a[144:147], a[168:171], a[72:75]\n" \
"v_mfma_f32_16x16x32_bf16 a[72:75], a[208:211], a[232:235], a[72:75]\n" \
"v_mfma_f32_16x16x32_bf16 a[76:79], a[144:147], a[172:175], a[76:79]\n" \
"buffer_store_dwordx4 v[208:211], v20, s[80:83], s36 offen sc1\n" \
"v_mfma_f32_16x16x32_bf16 a[76:79], a[208:211], a[236:239], a[76:79]\n" \
"ds_read_b64_tr_b16 a[188:189], v39 offset:0\n" \
"v_mfma_f32_16x16x32_bf16 a[92:95], a[148:151], a[172:175], a[92:95]\n" \
"v_mfma_f32_16x16x32_bf16 a[92:95], a[212:215], a[236:239], a[92:95]\n" \
"v_mfma_f32_16x16x32_bf16 a[88:91], a[148:151], a[168:171], a[88:91]\n" \
"ds_read_b64_tr_b16 a[190:191], v39 offset:1024\n" \
"v_mfma_f32_16x16x32_bf16 a[88:91], a[212:215], a[232:235], a[88:91]\n" \
"v_mfma_f32_16x16x32_bf16 a[84:87], a[148:151], a[164:167], a[84:87]\n" \
"v_mfma_f32_16x16x32_bf16 a[84:87], a[212:215], a[228:231], a[84:87]\n" \
"ds_read_b64_tr_b16 a[252:253], v39 offset:8192\n" \
"v_mfma_f32_16x16x32_bf16 a[80:83], a[148:151], a[160:163], a[80:83]\n" \
"v_mfma_f32_16x16x32_bf16 a[80:83], a[212:215], a[224:227], a[80:83]\n" \
"v_mfma_f32_16x16x32_bf16 a[96:99], a[152:155], a[160:163], a[96:99]\n" \
"ds_read_b64_tr_b16 a[254:255], v39 offset:9216\n" \
"v_mfma_f32_16x16x32_bf16 a[96:99], a[216:219], a[224:227], a[96:99]\n" \
"v_mfma_f32_16x16x32_bf16 a[100:103], a[152:155], a[164:167], a[100:103]\n" \
"v_mfma_f32_16x16x32_bf16 a[100:103], a[216:219], a[228:231], a[100:103]\n" \
"v_mfma_f32_16x16x32_bf16 a[104:107], a[152:155], a[168:171], a[104:107]\n" \
"v_mfma_f32_16x16x32_bf16 a[104:107], a[216:219], a[232:235], a[104:107]\n" \
"v_mfma_f32_16x16x32_bf16 a[108:111], a[152:155], a[172:175], a[108:111]\n" \
"v_mfma_f32_16x16x32_bf16 a[108:111], a[216:219], a[236:239], a[108:111]\n" \
"s_waitcnt lgkmcnt(15)\n" \
"v_mfma_f32_16x16x32_bf16 a[124:127], a[156:159], a[172:175], a[124:127]\n" \
"v_mfma_f32_16x16x32_bf16 a[124:127], a[220:223], a[236:239], a[124:127]\n" \
"v_mfma_f32_16x16x32_bf16 a[120:123], a[156:159], a[168:171], a[120:123]\n" \
"v_mfma_f32_16x16x32_bf16 a[120:123], a[220:223], a[232:235], a[120:123]\n" \
"v_mfma_f32_16x16x32_bf16 a[116:119], a[156:159], a[164:167], a[116:119]\n" \
"v_mfma_f32_16x16x32_bf16 a[116:119], a[220:223], a[228:231], a[116:119]\n" \
"v_mfma_f32_16x16x32_bf16 a[112:115], a[156:159], a[160:163], a[112:115]\n" \
"v_mfma_f32_16x16x32_bf16 a[112:115], a[220:223], a[224:227], a[112:115]\n" \
"s_waitcnt vmcnt(4) lgkmcnt(0)\n" \
"s_barrier\n" \
"v_mfma_f32_16x16x32_bf16 v[52:55], a[128:131], a[176:179], v[52:55]\n" \
"s_add_u32 m0, s30, 0x0\n" \
"s_movk_i32 s84, 0x300\n" \
"v_mfma_f32_16x16x32_bf16 v[52:55], a[192:195], a[240:243], v[52:55]\n" \
"buffer_load_dwordx4 v22, s[64:67], s84 offen offset:0 lds\n" \
"v_mfma_f32_16x16x32_bf16 v[56:59], a[128:131], a[180:183], v[56:59]\n" \
"ds_read_b64_tr_b16 a[160:161], v32 offset:32768\n" \
"v_mfma_f32_16x16x32_bf16 v[56:59], a[192:195], a[244:247], v[56:59]\n" \
"s_movk_i32 s84, 0x1f00\n" \
"v_mfma_f32_16x16x32_bf16 v[60:63], a[128:131], a[184:187], v[60:63]\n" \
"buffer_load_dwordx4 v23, s[64:67], s84 offen offset:1024 lds\n" \
"v_mfma_f32_16x16x32_bf16 v[60:63], a[192:195], a[248:251], v[60:63]\n" \
"ds_read_b64_tr_b16 a[162:163], v32 offset:33792\n" \
"v_mfma_f32_16x16x32_bf16 v[64:67], a[128:131], a[188:191], v[64:67]\n" \
"ds_read_b64_tr_b16 a[224:225], v32 offset:40960\n" \
"v_mfma_f32_16x16x32_bf16 v[64:67], a[192:195], a[252:255], v[64:67]\n" \
"ds_read_b128 a[128:131], v28 offset:49152\n" \
"v_mfma_f32_16x16x32_bf16 v[80:83], a[132:135], a[188:191], v[80:83]\n" \
"ds_read_b128 a[192:195], v29 offset:49152\n" \
"v_mfma_f32_16x16x32_bf16 v[80:83], a[196:199], a[252:255], v[80:83]\n" \
"s_movk_i32 s84, 0x3b00\n" \
"v_mfma_f32_16x16x32_bf16 v[76:79], a[132:135], a[184:187], v[76:79]\n" \
"buffer_load_dwordx4 v22, s[64:67], s84 offen offset:2048 lds\n" \
"v_mfma_f32_16x16x32_bf16 v[76:79], a[196:199], a[248:251], v[76:79]\n" \
"s_movk_i32 s84, 0x5700\n" \
"v_mfma_f32_16x16x32_bf16 v[72:75], a[132:135], a[180:183], v[72:75]\n" \
"buffer_load_dwordx4 v23, s[64:67], s84 offen offset:3072 lds\n" \
"v_mfma_f32_16x16x32_bf16 v[72:75], a[196:199], a[244:247], v[72:75]\n" \
"s_add_u32 m0, s31, 0x0\n" \
"s_mov_b32 s84, 0x300000\n" \
"v_mfma_f32_16x16x32_bf16 v[68:71], a[132:135], a[176:179], v[68:71]\n" \
"buffer_load_dwordx4 v26, s[68:71], s84 offen offset:0 lds\n" \
"v_mfma_f32_16x16x32_bf16 v[68:71], a[196:199], a[240:243], v[68:71]\n" \
"ds_read_b128 a[132:135], v28 offset:51200\n" \
"v_mfma_f32_16x16x32_bf16 v[84:87], a[136:139], a[176:179], v[84:87]\n" \
"ds_read_b128 a[196:199], v29 offset:51200\n" \
"v_mfma_f32_16x16x32_bf16 v[84:87], a[200:203], a[240:243], v[84:87]\n" \
"s_addk_i32 s84, 0x7c00\n" \
"v_mfma_f32_16x16x32_bf16 v[88:91], a[136:139], a[180:183], v[88:91]\n" \
"buffer_load_dwordx4 v26, s[68:71], s84 offen offset:1024 lds\n" \
"v_mfma_f32_16x16x32_bf16 v[88:91], a[200:203], a[244:247], v[88:91]\n" \
"ds_read_b64_tr_b16 a[226:227], v32 offset:41984\n" \
"v_mfma_f32_16x16x32_bf16 v[92:95], a[136:139], a[184:187], v[92:95]\n" \
"s_addk_i32 s84, 0x7c00\n" \
"v_mfma_f32_16x16x32_bf16 v[92:95], a[200:203], a[248:251], v[92:95]\n" \
"buffer_load_dwordx4 v27, s[68:71], s84 offen offset:2048 lds\n" \
"v_mfma_f32_16x16x32_bf16 v[96:99], a[136:139], a[188:191], v[96:99]\n" \
"ds_read_b64_tr_b16 a[164:165], v33 offset:32768\n" \
"v_mfma_f32_16x16x32_bf16 v[96:99], a[200:203], a[252:255], v[96:99]\n" \
"ds_read_b128 a[136:139], v28 offset:53248\n" \
"v_mfma_f32_16x16x32_bf16 v[112:115], a[140:143], a[188:191], v[112:115]\n" \
"ds_read_b128 a[200:203], v29 offset:53248\n" \
"v_mfma_f32_16x16x32_bf16 v[112:115], a[204:207], a[252:255], v[112:115]\n" \
"s_addk_i32 s84, 0x7c00\n" \
"v_mfma_f32_16x16x32_bf16 v[108:111], a[140:143], a[184:187], v[108:111]\n" \
"buffer_load_dwordx4 v27, s[68:71], s84 offen offset:3072 lds\n" \
"v_mfma_f32_16x16x32_bf16 v[108:111], a[204:207], a[248:251], v[108:111]\n" \
"s_add_u32 m0, s30, 0x1000\n" \
"s_mov_b32 s84, 0x8300\n" \
"v_mfma_f32_16x16x32_bf16 v[104:107], a[140:143], a[180:183], v[104:107]\n" \
"buffer_load_dwordx4 v22, s[64:67], s84 offen offset:0 lds\n" \
"v_mfma_f32_16x16x32_bf16 v[104:107], a[204:207], a[244:247], v[104:107]\n" \
"ds_read_b64_tr_b16 a[166:167], v33 offset:33792\n" \
"v_mfma_f32_16x16x32_bf16 v[100:103], a[140:143], a[176:179], v[100:103]\n" \
"ds_read_b64_tr_b16 a[228:229], v33 offset:40960\n" \
"v_mfma_f32_16x16x32_bf16 v[100:103], a[204:207], a[240:243], v[100:103]\n" \
"ds_read_b128 a[140:143], v28 offset:55296\n" \
"v_mfma_f32_16x16x32_bf16 v[116:119], a[144:147], a[176:179], v[116:119]\n" \
"ds_read_b128 a[204:207], v29 offset:55296\n" \
"v_mfma_f32_16x16x32_bf16 v[116:119], a[208:211], a[240:243], v[116:119]\n" \
"s_addk_i32 s84, 0x1c00\n" \
"v_mfma_f32_16x16x32_bf16 v[120:123], a[144:147], a[180:183], v[120:123]\n" \
"buffer_load_dwordx4 v23, s[64:67], s84 offen offset:1024 lds\n" \
"v_mfma_f32_16x16x32_bf16 v[120:123], a[208:211], a[244:247], v[120:123]\n" \
"s_addk_i32 s84, 0x1c00\n" \
"v_mfma_f32_16x16x32_bf16 v[124:127], a[144:147], a[184:187], v[124:127]\n" \
"buffer_load_dwordx4 v22, s[64:67], s84 offen offset:2048 lds\n" \
"v_mfma_f32_16x16x32_bf16 v[124:127], a[208:211], a[248:251], v[124:127]\n" \
"s_addk_i32 s84, 0x1c00\n" \
"s_nop 0\n" \
"v_mfma_f32_16x16x32_bf16 v[128:131], a[144:147], a[188:191], v[128:131]\n" \
"buffer_load_dwordx4 v23, s[64:67], s84 offen offset:3072 lds\n" \
"v_mfma_f32_16x16x32_bf16 v[128:131], a[208:211], a[252:255], v[128:131]\n" \
"ds_read_b128 a[144:147], v28 offset:32768\n" \
"v_mfma_f32_16x16x32_bf16 v[144:147], a[148:151], a[188:191], v[144:147]\n" \
"ds_read_b128 a[208:211], v29 offset:32768\n" \
"v_mfma_f32_16x16x32_bf16 v[144:147], a[212:215], a[252:255], v[144:147]\n" \
"s_add_u32 m0, s31, 0x4000\n" \
"s_mov_b32 s84, 0x300000\n" \
"v_mfma_f32_16x16x32_bf16 v[140:143], a[148:151], a[184:187], v[140:143]\n" \
"buffer_load_dwordx4 v26, s[72:75], s84 offen offset:0 lds\n" \
"v_mfma_f32_16x16x32_bf16 v[140:143], a[212:215], a[248:251], v[140:143]\n" \
"ds_read_b64_tr_b16 a[230:231], v33 offset:41984\n" \
"v_mfma_f32_16x16x32_bf16 v[136:139], a[148:151], a[180:183], v[136:139]\n" \
"s_addk_i32 s84, 0x7c00\n" \
"s_nop 0\n" \
"v_mfma_f32_16x16x32_bf16 v[136:139], a[212:215], a[244:247], v[136:139]\n" \
"buffer_load_dwordx4 v26, s[72:75], s84 offen offset:1024 lds\n" \
"v_mfma_f32_16x16x32_bf16 v[132:135], a[148:151], a[176:179], v[132:135]\n" \
"ds_read_b64_tr_b16 a[168:169], v34 offset:32768\n" \
"v_mfma_f32_16x16x32_bf16 v[132:135], a[212:215], a[240:243], v[132:135]\n" \
"ds_read_b128 a[148:151], v28 offset:34816\n" \
"v_mfma_f32_16x16x32_bf16 v[148:151], a[152:155], a[176:179], v[148:151]\n" \
"ds_read_b128 a[212:215], v29 offset:34816\n" \
"v_mfma_f32_16x16x32_bf16 v[148:151], a[216:219], a[240:243], v[148:151]\n" \
"ds_read_b64_tr_b16 a[170:171], v34 offset:33792\n" \
"v_mfma_f32_16x16x32_bf16 v[152:155], a[152:155], a[180:183], v[152:155]\n" \
"ds_read_b64_tr_b16 a[232:233], v34 offset:40960\n" \
"v_mfma_f32_16x16x32_bf16 v[152:155], a[216:219], a[244:247], v[152:155]\n" \
"ds_read_b64_tr_b16 a[234:235], v34 offset:41984\n" \
"v_mfma_f32_16x16x32_bf16 v[156:159], a[152:155], a[184:187], v[156:159]\n" \
"ds_read_b64_tr_b16 a[172:173], v35 offset:32768\n" \
"v_mfma_f32_16x16x32_bf16 v[156:159], a[216:219], a[248:251], v[156:159]\n" \
"ds_read_b64_tr_b16 a[174:175], v35 offset:33792\n" \
"v_mfma_f32_16x16x32_bf16 v[160:163], a[152:155], a[188:191], v[160:163]\n" \
"ds_read_b64_tr_b16 a[236:237], v35 offset:40960\n" \
"v_mfma_f32_16x16x32_bf16 v[160:163], a[216:219], a[252:255], v[160:163]\n" \
"ds_read_b128 a[152:155], v28 offset:36864\n" \
"v_mfma_f32_16x16x32_bf16 v[176:179], a[156:159], a[188:191], v[176:179]\n" \
"ds_read_b128 a[216:219], v29 offset:36864\n" \
"v_mfma_f32_16x16x32_bf16 v[176:179], a[220:223], a[252:255], v[176:179]\n" \
"ds_read_b64_tr_b16 a[238:239], v35 offset:41984\n" \
"v_mfma_f32_16x16x32_bf16 v[172:175], a[156:159], a[184:187], v[172:175]\n" \
"v_mfma_f32_16x16x32_bf16 v[172:175], a[220:223], a[248:251], v[172:175]\n" \
"v_mfma_f32_16x16x32_bf16 v[168:171], a[156:159], a[180:183], v[168:171]\n" \
"v_mfma_f32_16x16x32_bf16 v[168:171], a[220:223], a[244:247], v[168:171]\n" \
"v_mfma_f32_16x16x32_bf16 v[164:167], a[156:159], a[176:179], v[164:167]\n" \
"v_mfma_f32_16x16x32_bf16 v[164:167], a[220:223], a[240:243], v[164:167]\n" \
"ds_read_b128 a[156:159], v28 offset:38912\n" \
"ds_read_b128 a[220:223], v29 offset:38912\n" \
"s_waitcnt lgkmcnt(15)\n" \
"v_mfma_f32_16x16x32_bf16 a[0:3], a[128:131], a[160:163], a[0:3]\n" \
"s_addk_i32 s84, 0x7c00\n" \
"v_mfma_f32_16x16x32_bf16 a[0:3], a[192:195], a[224:227], a[0:3]\n" \
"buffer_load_dwordx4 v27, s[72:75], s84 offen offset:2048 lds\n" \
"v_mfma_f32_16x16x32_bf16 a[4:7], a[128:131], a[164:167], a[4:7]\n" \
"ds_read_b64_tr_b16 a[176:177], v36 offset:32768\n" \
"s_waitcnt lgkmcnt(15)\n" \
"v_mfma_f32_16x16x32_bf16 a[4:7], a[192:195], a[228:231], a[4:7]\n" \
"s_addk_i32 s84, 0x7c00\n" \
"s_waitcnt lgkmcnt(11)\n" \
"v_mfma_f32_16x16x32_bf16 a[8:11], a[128:131], a[168:171], a[8:11]\n" \
"buffer_load_dwordx4 v27, s[72:75], s84 offen offset:3072 lds\n" \
"s_waitcnt lgkmcnt(9)\n" \
"v_mfma_f32_16x16x32_bf16 a[8:11], a[192:195], a[232:235], a[8:11]\n" \
"ds_read_b64_tr_b16 a[178:179], v36 offset:33792\n" \
"s_waitcnt lgkmcnt(8)\n" \
"v_mfma_f32_16x16x32_bf16 a[12:15], a[128:131], a[172:175], a[12:15]\n" \
"ds_read_b64_tr_b16 a[240:241], v36 offset:40960\n" \
"s_waitcnt lgkmcnt(5)\n" \
"v_mfma_f32_16x16x32_bf16 a[12:15], a[192:195], a[236:239], a[12:15]\n" \
"v_mfma_f32_16x16x32_bf16 a[28:31], a[132:135], a[172:175], a[28:31]\n" \
"v_mfma_f32_16x16x32_bf16 a[28:31], a[196:199], a[236:239], a[28:31]\n" \
"ds_read_b64_tr_b16 a[242:243], v36 offset:41984\n" \
"v_mfma_f32_16x16x32_bf16 a[24:27], a[132:135], a[168:171], a[24:27]\n" \
"v_mfma_f32_16x16x32_bf16 a[24:27], a[196:199], a[232:235], a[24:27]\n" \
"v_mfma_f32_16x16x32_bf16 a[20:23], a[132:135], a[164:167], a[20:23]\n" \
"v_mfma_f32_16x16x32_bf16 a[20:23], a[196:199], a[228:231], a[20:23]\n" \
"ds_read_b64_tr_b16 a[180:181], v37 offset:32768\n" \
"v_mfma_f32_16x16x32_bf16 a[16:19], a[132:135], a[160:163], a[16:19]\n" \
"v_mfma_f32_16x16x32_bf16 a[16:19], a[196:199], a[224:227], a[16:19]\n" \
"v_mfma_f32_16x16x32_bf16 a[32:35], a[136:139], a[160:163], a[32:35]\n" \
"ds_read_b64_tr_b16 a[182:183], v37 offset:33792\n" \
"v_mfma_f32_16x16x32_bf16 a[32:35], a[200:203], a[224:227], a[32:35]\n" \
"v_mfma_f32_16x16x32_bf16 a[36:39], a[136:139], a[164:167], a[36:39]\n" \
"v_lshlrev_b32_e32 v45, 3, v20\n" \
"v_add_u32_e32 v45, s31, v45\n" \
"v_mfma_f32_16x16x32_bf16 a[36:39], a[200:203], a[228:231], a[36:39]\n" \
"s_getpc_b64 s[86:87]\n" \
"s_sub_u32 s86, s86, 0x10800\n" \
"s_subb_u32 s87, s87, 0\n" \
"s_mov_b32 m0, s32\n" \
"s_nop 0\n" \
"v_mfma_f32_16x16x32_bf16 a[40:43], a[136:139], a[168:171], a[40:43]\n" \
"global_load_lds_dword v45, s[86:87]\n" \
"v_mfma_f32_16x16x32_bf16 a[40:43], a[200:203], a[232:235], a[40:43]\n" \
"ds_read_b64_tr_b16 a[244:245], v37 offset:40960\n" \
"v_mfma_f32_16x16x32_bf16 a[44:47], a[136:139], a[172:175], a[44:47]\n" \
"ds_read_b64_tr_b16 a[246:247], v37 offset:41984\n" \
"v_mfma_f32_16x16x32_bf16 a[44:47], a[200:203], a[236:239], a[44:47]\n" \
"v_mfma_f32_16x16x32_bf16 a[60:63], a[140:143], a[172:175], a[60:63]\n" \
"v_mfma_f32_16x16x32_bf16 a[60:63], a[204:207], a[236:239], a[60:63]\n" \
"v_mfma_f32_16x16x32_bf16 a[56:59], a[140:143], a[168:171], a[56:59]\n" \
"ds_read_b64_tr_b16 a[184:185], v38 offset:32768\n" \
"v_mfma_f32_16x16x32_bf16 a[56:59], a[204:207], a[232:235], a[56:59]\n" \
"v_mfma_f32_16x16x32_bf16 a[52:55], a[140:143], a[164:167], a[52:55]\n" \
"v_mfma_f32_16x16x32_bf16 a[52:55], a[204:207], a[228:231], a[52:55]\n" \
"ds_read_b64_tr_b16 a[186:187], v38 offset:33792\n" \
"v_mfma_f32_16x16x32_bf16 a[48:51], a[140:143], a[160:163], a[48:51]\n" \
"v_mfma_f32_16x16x32_bf16 a[48:51], a[204:207], a[224:227], a[48:51]\n" \
"v_mfma_f32_16x16x32_bf16 a[64:67], a[144:147], a[160:163], a[64:67]\n" \
"ds_read_b64_tr_b16 a[248:249], v38 offset:40960\n" \
"v_mfma_f32_16x16x32_bf16 a[64:67], a[208:211], a[224:227], a[64:67]\n" \
"v_mfma_f32_16x16x32_bf16 a[68:71], a[144:147], a[164:167], a[68:71]\n" \
"v_mfma_f32_16x16x32_bf16 a[68:71], a[208:211], a[228:231], a[68:71]\n" \
"ds_read_b64_tr_b16 a[250:251], v38 offset:41984\n" \
"v_mfma_f32_16x16x32_bf16 a[72:75], a[144:147], a[168:171], a[72:75]\n" \
"v_mfma_f32_16x16x32_bf16 a[72:75], a[208:211], a[232:235], a[72:75]\n" \
"v_mfma_f32_16x16x32_bf16 a[76:79], a[144:147], a[172:175], a[76:79]\n" \
"v_mfma_f32_16x16x32_bf16 a[76:79], a[208:211], a[236:239], a[76:79]\n" \
"ds_read_b64_tr_b16 a[188:189], v39 offset:32768\n" \
"v_mfma_f32_16x16x32_bf16 a[92:95], a[148:151], a[172:175], a[92:95]\n" \
"v_mfma_f32_16x16x32_bf16 a[92:95], a[212:215], a[236:239], a[92:95]\n" \
"v_mfma_f32_16x16x32_bf16 a[88:91], a[148:151], a[168:171], a[88:91]\n" \
"ds_read_b64_tr_b16 a[190:191], v39 offset:33792\n" \
"v_mfma_f32_16x16x32_bf16 a[88:91], a[212:215], a[232:235], a[88:91]\n" \
"v_mfma_f32_16x16x32_bf16 a[84:87], a[148:151], a[164:167], a[84:87]\n" \
"v_mfma_f32_16x16x32_bf16 a[84:87], a[212:215], a[228:231], a[84:87]\n" \
"ds_read_b64_tr_b16 a[252:253], v39 offset:40960\n" \
"v_mfma_f32_16x16x32_bf16 a[80:83], a[148:151], a[160:163], a[80:83]\n" \
"v_mfma_f32_16x16x32_bf16 a[80:83], a[212:215], a[224:227], a[80:83]\n" \
"v_mfma_f32_16x16x32_bf16 a[96:99], a[152:155], a[160:163], a[96:99]\n" \
"ds_read_b64_tr_b16 a[254:255], v39 offset:41984\n" \
"v_mfma_f32_16x16x32_bf16 a[96:99], a[216:219], a[224:227], a[96:99]\n" \
"v_mfma_f32_16x16x32_bf16 a[100:103], a[152:155], a[164:167], a[100:103]\n" \
"v_mfma_f32_16x16x32_bf16 a[100:103], a[216:219], a[228:231], a[100:103]\n" \
"v_mfma_f32_16x16x32_bf16 a[104:107], a[152:155], a[168:171], a[104:107]\n" \
"v_mfma_f32_16x16x32_bf16 a[104:107], a[216:219], a[232:235], a[104:107]\n" \
"v_mfma_f32_16x16x32_bf16 a[108:111], a[152:155], a[172:175], a[108:111]\n" \
"v_mfma_f32_16x16x32_bf16 a[108:111], a[216:219], a[236:239], a[108:111]\n" \
"s_waitcnt lgkmcnt(15)\n" \
"v_mfma_f32_16x16x32_bf16 a[124:127], a[156:159], a[172:175], a[124:127]\n" \
"v_mfma_f32_16x16x32_bf16 a[124:127], a[220:223], a[236:239], a[124:127]\n" \
"v_mfma_f32_16x16x32_bf16 a[120:123], a[156:159], a[168:171], a[120:123]\n" \
"v_mfma_f32_16x16x32_bf16 a[120:123], a[220:223], a[232:235], a[120:123]\n" \
"v_mfma_f32_16x16x32_bf16 a[116:119], a[156:159], a[164:167], a[116:119]\n" \
"v_mfma_f32_16x16x32_bf16 a[116:119], a[220:223], a[228:231], a[116:119]\n" \
"v_mfma_f32_16x16x32_bf16 a[112:115], a[156:159], a[160:163], a[112:115]\n" \
"v_mfma_f32_16x16x32_bf16 a[112:115], a[220:223], a[224:227], a[112:115]\n" \
"s_waitcnt vmcnt(1) lgkmcnt(0)\n" \
"s_barrier\n" \
"v_mov_b32_e32 v45, s62\n" \
"s_mov_b64 exec, 1\n" \
"buffer_store_dword v45, off, s[80:83], s37 offset:128 sc1\n" \
"s_mov_b64 exec, -1\n" \
"v_mfma_f32_16x16x32_bf16 v[52:55], a[128:131], a[176:179], v[52:55]\n" \
"s_add_u32 m0, s30, 0x8000\n" \
"s_movk_i32 s84, 0x380\n" \
"v_mfma_f32_16x16x32_bf16 v[52:55], a[192:195], a[240:243], v[52:55]\n" \
"buffer_load_dwordx4 v22, s[64:67], s84 offen offset:0 lds\n" \
"v_mfma_f32_16x16x32_bf16 v[56:59], a[128:131], a[180:183], v[56:59]\n" \
"ds_read_b64_tr_b16 a[160:161], v32 offset:0\n" \
"v_mfma_f32_16x16x32_bf16 v[56:59], a[192:195], a[244:247], v[56:59]\n" \
"s_movk_i32 s84, 0x1f80\n" \
"v_mfma_f32_16x16x32_bf16 v[60:63], a[128:131], a[184:187], v[60:63]\n" \
"buffer_load_dwordx4 v23, s[64:67], s84 offen offset:1024 lds\n" \
"v_mfma_f32_16x16x32_bf16 v[60:63], a[192:195], a[248:251], v[60:63]\n" \
"ds_read_b64_tr_b16 a[162:163], v32 offset:1024\n" \
"v_mfma_f32_16x16x32_bf16 v[64:67], a[128:131], a[188:191], v[64:67]\n" \
"ds_read_b64_tr_b16 a[224:225], v32 offset:8192\n" \
"v_mfma_f32_16x16x32_bf16 v[64:67], a[192:195], a[252:255], v[64:67]\n" \
"ds_read_b128 a[128:131], v28 offset:16384\n" \
"v_mfma_f32_16x16x32_bf16 v[80:83], a[132:135], a[188:191], v[80:83]\n" \
"ds_read_b128 a[192:195], v29 offset:16384\n" \
"v_mfma_f32_16x16x32_bf16 v[80:83], a[196:199], a[252:255], v[80:83]\n" \
"s_movk_i32 s84, 0x3b80\n" \
"v_mfma_f32_16x16x32_bf16 v[76:79], a[132:135], a[184:187], v[76:79]\n" \
"buffer_load_dwordx4 v22, s[64:67], s84 offen offset:2048 lds\n" \
"v_mfma_f32_16x16x32_bf16 v[76:79], a[196:199], a[248:251], v[76:79]\n" \
"s_movk_i32 s84, 0x5780\n" \
"v_mfma_f32_16x16x32_bf16 v[72:75], a[132:135], a[180:183], v[72:75]\n" \
"buffer_load_dwordx4 v23, s[64:67], s84 offen offset:3072 lds\n" \
"v_mfma_f32_16x16x32_bf16 v[72:75], a[196:199], a[244:247], v[72:75]\n" \
"s_add_u32 m0, s31, 0x8000\n" \
"s_mov_b32 s84, 0x380000\n" \
"v_mfma_f32_16x16x32_bf16 v[68:71], a[132:135], a[176:179], v[68:71]\n" \
"buffer_load_dwordx4 v26, s[68:71], s84 offen offset:0 lds\n" \
"v_mfma_f32_16x16x32_bf16 v[68:71], a[196:199], a[240:243], v[68:71]\n" \
"ds_read_b128 a[132:135], v28 offset:18432\n" \
"v_mfma_f32_16x16x32_bf16 v[84:87], a[136:139], a[176:179], v[84:87]\n" \
"ds_read_b128 a[196:199], v29 offset:18432\n" \
"v_mfma_f32_16x16x32_bf16 v[84:87], a[200:203], a[240:243], v[84:87]\n" \
"s_addk_i32 s84, 0x7c00\n" \
"v_mfma_f32_16x16x32_bf16 v[88:91], a[136:139], a[180:183], v[88:91]\n" \
"buffer_load_dwordx4 v26, s[68:71], s84 offen offset:1024 lds\n" \
"v_mfma_f32_16x16x32_bf16 v[88:91], a[200:203], a[244:247], v[88:91]\n" \
"ds_read_b64_tr_b16 a[226:227], v32 offset:9216\n" \
"v_mfma_f32_16x16x32_bf16 v[92:95], a[136:139], a[184:187], v[92:95]\n" \
"s_addk_i32 s84, 0x7c00\n" \
"v_mfma_f32_16x16x32_bf16 v[92:95], a[200:203], a[248:251], v[92:95]\n" \
"buffer_load_dwordx4 v27, s[68:71], s84 offen offset:2048 lds\n" \
"v_mfma_f32_16x16x32_bf16 v[96:99], a[136:139], a[188:191], v[96:99]\n" \
"ds_read_b64_tr_b16 a[164:165], v33 offset:0\n" \
"v_mfma_f32_16x16x32_bf16 v[96:99], a[200:203], a[252:255], v[96:99]\n" \
"ds_read_b128 a[136:139], v28 offset:20480\n" \
"v_mfma_f32_16x16x32_bf16 v[112:115], a[140:143], a[188:191], v[112:115]\n" \
"ds_read_b128 a[200:203], v29 offset:20480\n" \
"v_mfma_f32_16x16x32_bf16 v[112:115], a[204:207], a[252:255], v[112:115]\n" \
"s_addk_i32 s84, 0x7c00\n" \
"s_nop 0\n" \
"v_mfma_f32_16x16x32_bf16 v[108:111], a[140:143], a[184:187], v[108:111]\n" \
"buffer_load_dwordx4 v27, s[68:71], s84 offen offset:3072 lds\n" \
"v_mfma_f32_16x16x32_bf16 v[108:111], a[204:207], a[248:251], v[108:111]\n" \
"s_add_u32 m0, s30, 0x9000\n" \
"s_mov_b32 s84, 0x8380\n" \
"v_mfma_f32_16x16x32_bf16 v[104:107], a[140:143], a[180:183], v[104:107]\n" \
"buffer_load_dwordx4 v22, s[64:67], s84 offen offset:0 lds\n" \
"v_mfma_f32_16x16x32_bf16 v[104:107], a[204:207], a[244:247], v[104:107]\n" \
"ds_read_b64_tr_b16 a[166:167], v33 offset:1024\n" \
"v_mfma_f32_16x16x32_bf16 v[100:103], a[140:143], a[176:179], v[100:103]\n" \
"ds_read_b64_tr_b16 a[228:229], v33 offset:8192\n" \
"v_mfma_f32_16x16x32_bf16 v[100:103], a[204:207], a[240:243], v[100:103]\n" \
"ds_read_b128 a[140:143], v28 offset:22528\n" \
"v_mfma_f32_16x16x32_bf16 v[116:119], a[144:147], a[176:179], v[116:119]\n" \
"ds_read_b128 a[204:207], v29 offset:22528\n" \
"v_mfma_f32_16x16x32_bf16 v[116:119], a[208:211], a[240:243], v[116:119]\n" \
"s_addk_i32 s84, 0x1c00\n" \
"v_mfma_f32_16x16x32_bf16 v[120:123], a[144:147], a[180:183], v[120:123]\n" \
"buffer_load_dwordx4 v23, s[64:67], s84 offen offset:1024 lds\n" \
"v_mfma_f32_16x16x32_bf16 v[120:123], a[208:211], a[244:247], v[120:123]\n" \
"s_addk_i32 s84, 0x1c00\n" \
"v_mfma_f32_16x16x32_bf16 v[124:127], a[144:147], a[184:187], v[124:127]\n" \
"buffer_load_dwordx4 v22, s[64:67], s84 offen offset:2048 lds\n" \
"v_mfma_f32_16x16x32_bf16 v[124:127], a[208:211], a[248:251], v[124:127]\n" \
"s_addk_i32 s84, 0x1c00\n" \
"s_nop 0\n" \
"v_mfma_f32_16x16x32_bf16 v[128:131], a[144:147], a[188:191], v[128:131]\n" \
"buffer_load_dwordx4 v23, s[64:67], s84 offen offset:3072 lds\n" \
"v_mfma_f32_16x16x32_bf16 v[128:131], a[208:211], a[252:255], v[128:131]\n" \
"ds_read_b128 a[144:147], v28 offset:0\n" \
"v_mfma_f32_16x16x32_bf16 v[144:147], a[148:151], a[188:191], v[144:147]\n" \
"ds_read_b128 a[208:211], v29 offset:0\n" \
"v_mfma_f32_16x16x32_bf16 v[144:147], a[212:215], a[252:255], v[144:147]\n" \
"s_add_u32 m0, s31, 0xc000\n" \
"s_mov_b32 s84, 0x380000\n" \
"v_mfma_f32_16x16x32_bf16 v[140:143], a[148:151], a[184:187], v[140:143]\n" \
"buffer_load_dwordx4 v26, s[72:75], s84 offen offset:0 lds\n" \
"v_mfma_f32_16x16x32_bf16 v[140:143], a[212:215], a[248:251], v[140:143]\n" \
"ds_read_b64_tr_b16 a[230:231], v33 offset:9216\n" \
"v_mfma_f32_16x16x32_bf16 v[136:139], a[148:151], a[180:183], v[136:139]\n" \
"s_addk_i32 s84, 0x7c00\n" \
"s_nop 0\n" \
"v_mfma_f32_16x16x32_bf16 v[136:139], a[212:215], a[244:247], v[136:139]\n" \
"buffer_load_dwordx4 v26, s[72:75], s84 offen offset:1024 lds\n" \
"v_mfma_f32_16x16x32_bf16 v[132:135], a[148:151], a[176:179], v[132:135]\n" \
"ds_read_b64_tr_b16 a[168:169], v34 offset:0\n" \
"v_mfma_f32_16x16x32_bf16 v[132:135], a[212:215], a[240:243], v[132:135]\n" \
"ds_read_b128 a[148:151], v28 offset:2048\n" \
"v_mfma_f32_16x16x32_bf16 v[148:151], a[152:155], a[176:179], v[148:151]\n" \
"ds_read_b128 a[212:215], v29 offset:2048\n" \
"v_mfma_f32_16x16x32_bf16 v[148:151], a[216:219], a[240:243], v[148:151]\n" \
"ds_read_b64_tr_b16 a[170:171], v34 offset:1024\n" \
"v_mfma_f32_16x16x32_bf16 v[152:155], a[152:155], a[180:183], v[152:155]\n" \
"ds_read_b64_tr_b16 a[232:233], v34 offset:8192\n" \
"v_mfma_f32_16x16x32_bf16 v[152:155], a[216:219], a[244:247], v[152:155]\n" \
"ds_read_b64_tr_b16 a[234:235], v34 offset:9216\n" \
"v_mfma_f32_16x16x32_bf16 v[156:159], a[152:155], a[184:187], v[156:159]\n" \
"ds_read_b64_tr_b16 a[172:173], v35 offset:0\n" \
"v_mfma_f32_16x16x32_bf16 v[156:159], a[216:219], a[248:251], v[156:159]\n" \
"ds_read_b64_tr_b16 a[174:175], v35 offset:1024\n" \
"v_mfma_f32_16x16x32_bf16 v[160:163], a[152:155], a[188:191], v[160:163]\n" \
"ds_read_b64_tr_b16 a[236:237], v35 offset:8192\n" \
"v_mfma_f32_16x16x32_bf16 v[160:163], a[216:219], a[252:255], v[160:163]\n" \
"ds_read_b128 a[152:155], v28 offset:4096\n" \
"v_mfma_f32_16x16x32_bf16 v[176:179], a[156:159], a[188:191], v[176:179]\n" \
"ds_read_b128 a[216:219], v29 offset:4096\n" \
"v_mfma_f32_16x16x32_bf16 v[176:179], a[220:223], a[252:255], v[176:179]\n" \
"ds_read_b64_tr_b16 a[238:239], v35 offset:9216\n" \
"v_mfma_f32_16x16x32_bf16 v[172:175], a[156:159], a[184:187], v[172:175]\n" \
"v_mfma_f32_16x16x32_bf16 v[172:175], a[220:223], a[248:251], v[172:175]\n" \
"v_mfma_f32_16x16x32_bf16 v[168:171], a[156:159], a[180:183], v[168:171]\n" \
"v_mfma_f32_16x16x32_bf16 v[168:171], a[220:223], a[244:247], v[168:171]\n" \
"v_mfma_f32_16x16x32_bf16 v[164:167], a[156:159], a[176:179], v[164:167]\n" \
"v_mfma_f32_16x16x32_bf16 v[164:167], a[220:223], a[240:243], v[164:167]\n" \
"ds_read_b128 a[156:159], v28 offset:6144\n" \
"ds_read_b128 a[220:223], v29 offset:6144\n" \
"s_waitcnt lgkmcnt(15)\n" \
"v_mfma_f32_16x16x32_bf16 a[0:3], a[128:131], a[160:163], a[0:3]\n" \
"s_addk_i32 s84, 0x7c00\n" \
"v_mfma_f32_16x16x32_bf16 a[0:3], a[192:195], a[224:227], a[0:3]\n" \
"buffer_load_dwordx4 v27, s[72:75], s84 offen offset:2048 lds\n" \
"v_mfma_f32_16x16x32_bf16 a[4:7], a[128:131], a[164:167], a[4:7]\n" \
"ds_read_b64_tr_b16 a[176:177], v36 offset:0\n" \
"s_waitcnt lgkmcnt(15)\n" \
"v_mfma_f32_16x16x32_bf16 a[4:7], a[192:195], a[228:231], a[4:7]\n" \
"s_addk_i32 s84, 0x7c00\n" \
"s_waitcnt lgkmcnt(11)\n" \
"v_mfma_f32_16x16x32_bf16 a[8:11], a[128:131], a[168:171], a[8:11]\n" \
"buffer_load_dwordx4 v27, s[72:75], s84 offen offset:3072 lds\n" \
"s_waitcnt lgkmcnt(9)\n" \
"v_mfma_f32_16x16x32_bf16 a[8:11], a[192:195], a[232:235], a[8:11]\n" \
"ds_read_b64_tr_b16 a[178:179], v36 offset:1024\n" \
"s_waitcnt lgkmcnt(8)\n" \
"v_mfma_f32_16x16x32_bf16 a[12:15], a[128:131], a[172:175], a[12:15]\n" \
"ds_read_b64_tr_b16 a[240:241], v36 offset:8192\n" \
"s_waitcnt lgkmcnt(5)\n" \
"v_mfma_f32_16x16x32_bf16 a[12:15], a[192:195], a[236:239], a[12:15]\n" \
"v_mfma_f32_16x16x32_bf16 a[28:31], a[132:135], a[172:175], a[28:31]\n" \
"v_mfma_f32_16x16x32_bf16 a[28:31], a[196:199], a[236:239], a[28:31]\n" \
"ds_read_b64_tr_b16 a[242:243], v36 offset:9216\n" \
"v_mfma_f32_16x16x32_bf16 a[24:27], a[132:135], a[168:171], a[24:27]\n" \
"v_mfma_f32_16x16x32_bf16 a[24:27], a[196:199], a[232:235], a[24:27]\n" \
"v_mfma_f32_16x16x32_bf16 a[20:23], a[132:135], a[164:167], a[20:23]\n" \
"v_mfma_f32_16x16x32_bf16 a[20:23], a[196:199], a[228:231], a[20:23]\n" \
"ds_read_b64_tr_b16 a[180:181], v37 offset:0\n" \
"v_mfma_f32_16x16x32_bf16 a[16:19], a[132:135], a[160:163], a[16:19]\n" \
"v_mfma_f32_16x16x32_bf16 a[16:19], a[196:199], a[224:227], a[16:19]\n" \
"v_mfma_f32_16x16x32_bf16 a[32:35], a[136:139], a[160:163], a[32:35]\n" \
"v_bfe_u32 v44, v20, 2, 5\n" \
"ds_read_b64_tr_b16 a[182:183], v37 offset:1024\n" \
"v_mfma_f32_16x16x32_bf16 a[32:35], a[200:203], a[224:227], a[32:35]\n" \
"buffer_load_dword v44, v44, s[80:83], s38 offen offset:128 sc1\n" \
"v_mfma_f32_16x16x32_bf16 a[36:39], a[136:139], a[164:167], a[36:39]\n" \
"v_mfma_f32_16x16x32_bf16 a[36:39], a[200:203], a[228:231], a[36:39]\n" \
"ds_read_b64_tr_b16 a[244:245], v37 offset:8192\n" \
"v_mfma_f32_16x16x32_bf16 a[40:43], a[136:139], a[168:171], a[40:43]\n" \
"v_mfma_f32_16x16x32_bf16 a[40:43], a[200:203], a[232:235], a[40:43]\n" \
"v_mfma_f32_16x16x32_bf16 a[44:47], a[136:139], a[172:175], a[44:47]\n" \
"ds_read_b64_tr_b16 a[246:247], v37 offset:9216\n" \
"v_mfma_f32_16x16x32_bf16 a[44:47], a[200:203], a[236:239], a[44:47]\n" \
"v_mfma_f32_16x16x32_bf16 a[60:63], a[140:143], a[172:175], a[60:63]\n" \
"global_store_dwordx4 v49, v[0:3], s[28:29] offset:0 sc1\n" \
"v_mfma_f32_16x16x32_bf16 a[60:63], a[204:207], a[236:239], a[60:63]\n" \
"v_mfma_f32_16x16x32_bf16 a[56:59], a[140:143], a[168:171], a[56:59]\n" \
"ds_read_b64_tr_b16 a[184:185], v38 offset:0\n" \
"v_mfma_f32_16x16x32_bf16 a[56:59], a[204:207], a[232:235], a[56:59]\n" \
"v_mfma_f32_16x16x32_bf16 a[52:55], a[140:143], a[164:167], a[52:55]\n" \
"v_mfma_f32_16x16x32_bf16 a[52:55], a[204:207], a[228:231], a[52:55]\n" \
"ds_read_b64_tr_b16 a[186:187], v38 offset:1024\n" \
"v_mfma_f32_16x16x32_bf16 a[48:51], a[140:143], a[160:163], a[48:51]\n" \
"v_mfma_f32_16x16x32_bf16 a[48:51], a[204:207], a[224:227], a[48:51]\n" \
"v_mfma_f32_16x16x32_bf16 a[64:67], a[144:147], a[160:163], a[64:67]\n" \
"global_store_dwordx4 v49, v[0:3], s[28:29] offset:1024 sc1\n" \
"v_mfma_f32_16x16x32_bf16 a[64:67], a[208:211], a[224:227], a[64:67]\n" \
"ds_read_b64_tr_b16 a[248:249], v38 offset:8192\n" \
"v_mfma_f32_16x16x32_bf16 a[68:71], a[144:147], a[164:167], a[68:71]\n" \
"v_mfma_f32_16x16x32_bf16 a[68:71], a[208:211], a[228:231], a[68:71]\n" \
"ds_read_b64_tr_b16 a[250:251], v38 offset:9216\n" \
"v_mfma_f32_16x16x32_bf16 a[72:75], a[144:147], a[168:171], a[72:75]\n" \
"v_mfma_f32_16x16x32_bf16 a[72:75], a[208:211], a[232:235], a[72:75]\n" \
"v_mfma_f32_16x16x32_bf16 a[76:79], a[144:147], a[172:175], a[76:79]\n" \
"v_mfma_f32_16x16x32_bf16 a[76:79], a[208:211], a[236:239], a[76:79]\n" \
"ds_read_b64_tr_b16 a[188:189], v39 offset:0\n" \
"v_mfma_f32_16x16x32_bf16 a[92:95], a[148:151], a[172:175], a[92:95]\n" \
"s_mov_b64 exec, 0xffff\n" \
"global_store_dwordx4 v49, v[0:3], s[28:29] offset:2048 sc1\n" \
"s_mov_b64 exec, -1\n" \
"s_nop 0\n" \
"v_mfma_f32_16x16x32_bf16 a[92:95], a[212:215], a[236:239], a[92:95]\n" \
"v_mfma_f32_16x16x32_bf16 a[88:91], a[148:151], a[168:171], a[88:91]\n" \
"ds_read_b64_tr_b16 a[190:191], v39 offset:1024\n" \
"v_mfma_f32_16x16x32_bf16 a[88:91], a[212:215], a[232:235], a[88:91]\n" \
"v_mfma_f32_16x16x32_bf16 a[84:87], a[148:151], a[164:167], a[84:87]\n" \
"v_mfma_f32_16x16x32_bf16 a[84:87], a[212:215], a[228:231], a[84:87]\n" \
"ds_read_b64_tr_b16 a[252:253], v39 offset:8192\n" \
"v_mfma_f32_16x16x32_bf16 a[80:83], a[148:151], a[160:163], a[80:83]\n" \
"v_mfma_f32_16x16x32_bf16 a[80:83], a[212:215], a[224:227], a[80:83]\n" \
"v_mfma_f32_16x16x32_bf16 a[96:99], a[152:155], a[160:163], a[96:99]\n" \
"ds_read_b64_tr_b16 a[254:255], v39 offset:9216\n" \
"v_mfma_f32_16x16x32_bf16 a[96:99], a[216:219], a[224:227], a[96:99]\n" \
"v_mfma_f32_16x16x32_bf16 a[100:103], a[152:155], a[164:167], a[100:103]\n" \
"v_mfma_f32_16x16x32_bf16 a[100:103], a[216:219], a[228:231], a[100:103]\n" \
"v_mfma_f32_16x16x32_bf16 a[104:107], a[152:155], a[168:171], a[104:107]\n" \
"v_mfma_f32_16x16x32_bf16 a[104:107], a[216:219], a[232:235], a[104:107]\n" \
"v_mfma_f32_16x16x32_bf16 a[108:111], a[152:155], a[172:175], a[108:111]\n" \
"v_mfma_f32_16x16x32_bf16 a[108:111], a[216:219], a[236:239], a[108:111]\n" \
"s_waitcnt lgkmcnt(15)\n" \
"v_mfma_f32_16x16x32_bf16 a[124:127], a[156:159], a[172:175], a[124:127]\n" \
"v_mfma_f32_16x16x32_bf16 a[124:127], a[220:223], a[236:239], a[124:127]\n" \
"v_mfma_f32_16x16x32_bf16 a[120:123], a[156:159], a[168:171], a[120:123]\n" \
"v_mfma_f32_16x16x32_bf16 a[120:123], a[220:223], a[232:235], a[120:123]\n" \
"v_mfma_f32_16x16x32_bf16 a[116:119], a[156:159], a[164:167], a[116:119]\n" \
"v_mfma_f32_16x16x32_bf16 a[116:119], a[220:223], a[228:231], a[116:119]\n" \
"v_mfma_f32_16x16x32_bf16 a[112:115], a[156:159], a[160:163], a[112:115]\n" \
"v_mfma_f32_16x16x32_bf16 a[112:115], a[220:223], a[224:227], a[112:115]\n" \
"s_waitcnt vmcnt(3) lgkmcnt(0)\n" \
"s_barrier\n" \
"v_cmp_ne_u32_e32 vcc, s62, v44\n" \
"s_nop 4\n" \
"s_cbranch_vccnz L_poll61_%=\n" \
"L_pollr61_%=:\n" \
"s_mov_b32 s68, s12\n" \
"s_and_b32 s69, s13, 0xffff\n" \
"s_mov_b32 s70, 0x480000\n" \
"s_mov_b32 s71, 0x20000\n" \
"v_mfma_f32_16x16x32_bf16 v[52:55], a[128:131], a[176:179], v[52:55]\n" \
"s_add_u32 m0, s40, 0x0\n" \
"s_add_u32 s85, s39, 0x0\n" \
"s_nop 0\n" \
"v_mfma_f32_16x16x32_bf16 v[52:55], a[192:195], a[240:243], v[52:55]\n" \
"buffer_load_dwordx4 v20, s[80:83], s85 offen sc1 lds\n" \
"v_mfma_f32_16x16x32_bf16 v[56:59], a[128:131], a[180:183], v[56:59]\n" \
"ds_read_b64_tr_b16 a[160:161], v32 offset:32768\n" \
"v_mfma_f32_16x16x32_bf16 v[56:59], a[192:195], a[244:247], v[56:59]\n" \
"s_add_u32 m0, s40, 0x1000\n" \
"s_add_u32 s85, s39, 0x1000\n" \
"v_mfma_f32_16x16x32_bf16 v[60:63], a[128:131], a[184:187], v[60:63]\n" \
"buffer_load_dwordx4 v20, s[80:83], s85 offen sc1 lds\n" \
".fill (L_pb + 57056 - .) / 4, 4, 0xBF800000\n" \
"L_pp14:\n" \
"s_cbranch_execz L_pret\n" \
"v_mfma_f32_16x16x32_bf16 v[60:63], a[192:195], a[248:251], v[60:63]\n" \
"ds_read_b64_tr_b16 a[162:163], v32 offset:33792\n" \
"v_mfma_f32_16x16x32_bf16 v[64:67], a[128:131], a[188:191], v[64:67]\n" \
"ds_read_b64_tr_b16 a[224:225], v32 offset:40960\n" \
"v_mfma_f32_16x16x32_bf16 v[64:67], a[192:195], a[252:255], v[64:67]\n" \
"ds_read_b128 a[128:131], v28 offset:49152\n" \
"v_mfma_f32_16x16x32_bf16 v[80:83], a[132:135], a[188:191], v[80:83]\n" \
"ds_read_b128 a[192:195], v29 offset:49152\n" \
"v_mfma_f32_16x16x32_bf16 v[80:83], a[196:199], a[252:255], v[80:83]\n" \
"s_add_u32 m0, s40, 0x3000\n" \
"s_movk_i32 s84, 0x0\n" \
"v_mfma_f32_16x16x32_bf16 v[76:79], a[132:135], a[184:187], v[76:79]\n" \
"buffer_load_dwordx4 v21, s[76:79], s84 offen offset:0 lds\n" \
"v_mfma_f32_16x16x32_bf16 v[76:79], a[196:199], a[248:251], v[76:79]\n" \
"s_add_u32 m0, s30, 0x0\n" \
"s_movk_i32 s84, 0x0\n" \
"v_mfma_f32_16x16x32_bf16 v[72:75], a[132:135], a[180:183], v[72:75]\n" \
"buffer_load_dwordx4 v24, s[76:79], s84 offen offset:0 lds\n" \
"v_mfma_f32_16x16x32_bf16 v[72:75], a[196:199], a[244:247], v[72:75]\n" \
"ds_read_b64_tr_b16 a[226:227], v32 offset:41984\n" \
"v_mfma_f32_16x16x32_bf16 v[68:71], a[132:135], a[176:179], v[68:71]\n" \
"ds_read_b64_tr_b16 a[164:165], v33 offset:32768\n" \
"v_mfma_f32_16x16x32_bf16 v[68:71], a[196:199], a[240:243], v[68:71]\n" \
"ds_read_b128 a[132:135], v28 offset:51200\n" \
"v_mfma_f32_16x16x32_bf16 v[84:87], a[136:139], a[176:179], v[84:87]\n" \
"ds_read_b128 a[196:199], v29 offset:51200\n" \
"v_mfma_f32_16x16x32_bf16 v[84:87], a[200:203], a[240:243], v[84:87]\n" \
"s_movk_i32 s84, 0x2000\n" \
"v_mfma_f32_16x16x32_bf16 v[88:91], a[136:139], a[180:183], v[88:91]\n" \
"buffer_load_dwordx4 v25, s[76:79], s84 offen offset:1024 lds\n" \
"v_mfma_f32_16x16x32_bf16 v[88:91], a[200:203], a[244:247], v[88:91]\n" \
"s_movk_i32 s84, 0x4000\n" \
"v_mfma_f32_16x16x32_bf16 v[92:95], a[136:139], a[184:187], v[92:95]\n" \
"buffer_load_dwordx4 v24, s[76:79], s84 offen offset:2048 lds\n" \
"v_mfma_f32_16x16x32_bf16 v[92:95], a[200:203], a[248:251], v[92:95]\n" \
"ds_read_b64_tr_b16 a[166:167], v33 offset:33792\n" \
"v_mfma_f32_16x16x32_bf16 v[96:99], a[136:139], a[188:191], v[96:99]\n" \
"ds_read_b64_tr_b16 a[228:229], v33 offset:40960\n" \
"v_mfma_f32_16x16x32_bf16 v[96:99], a[200:203], a[252:255], v[96:99]\n" \
"ds_read_b128 a[136:139], v28 offset:53248\n" \
"v_mfma_f32_16x16x32_bf16 v[112:115], a[140:143], a[188:191], v[112:115]\n" \
"ds_read_b128 a[200:203], v29 offset:53248\n" \
"v_mfma_f32_16x16x32_bf16 v[112:115], a[204:207], a[252:255], v[112:115]\n" \
"s_movk_i32 s84, 0x6000\n" \
"v_mfma_f32_16x16x32_bf16 v[108:111], a[140:143], a[184:187], v[108:111]\n" \
"buffer_load_dwordx4 v25, s[76:79], s84 offen offset:3072 lds\n" \
"v_mfma_f32_16x16x32_bf16 v[108:111], a[204:207], a[248:251], v[108:111]\n" \
"s_add_u32 m0, s31, 0x0\n" \
"s_movk_i32 s84, 0x0\n" \
"v_mfma_f32_16x16x32_bf16 v[104:107], a[140:143], a[180:183], v[104:107]\n" \
"buffer_load_dwordx4 v26, s[68:71], s84 offen offset:0 lds\n" \
"v_mfma_f32_16x16x32_bf16 v[104:107], a[204:207], a[244:247], v[104:107]\n" \
"s_movk_i32 s84, 0x7c00\n" \
"v_mfma_f32_16x16x32_bf16 v[100:103], a[140:143], a[176:179], v[100:103]\n" \
"buffer_load_dwordx4 v26, s[68:71], s84 offen offset:1024 lds\n" \
"v_mfma_f32_16x16x32_bf16 v[100:103], a[204:207], a[240:243], v[100:103]\n" \
"ds_read_b128 a[140:143], v28 offset:55296\n" \
"v_mfma_f32_16x16x32_bf16 v[116:119], a[144:147], a[176:179], v[116:119]\n" \
"ds_read_b128 a[204:207], v29 offset:55296\n" \
"v_mfma_f32_16x16x32_bf16 v[116:119], a[208:211], a[240:243], v[116:119]\n" \
"s_addk_i32 s84, 0x7c00\n" \
"v_mfma_f32_16x16x32_bf16 v[120:123], a[144:147], a[180:183], v[120:123]\n" \
"buffer_load_dwordx4 v27, s[68:71], s84 offen offset:2048 lds\n" \
"v_mfma_f32_16x16x32_bf16 v[120:123], a[208:211], a[244:247], v[120:123]\n" \
"ds_read_b64_tr_b16 a[230:231], v33 offset:41984\n" \
"v_mfma_f32_16x16x32_bf16 v[124:127], a[144:147], a[184:187], v[124:127]\n" \
"s_addk_i32 s84, 0x7c00\n" \
"v_mfma_f32_16x16x32_bf16 v[124:127], a[208:211], a[248:251], v[124:127]\n" \
"buffer_load_dwordx4 v27, s[68:71], s84 offen offset:3072 lds\n" \
"v_mfma_f32_16x16x32_bf16 v[128:131], a[144:147], a[188:191], v[128:131]\n" \
"ds_read_b64_tr_b16 a[168:169], v34 offset:32768\n" \
"v_mfma_f32_16x16x32_bf16 v[128:131], a[208:211], a[252:255], v[128:131]\n" \
"ds_read_b128 a[144:147], v28 offset:32768\n" \
"v_mfma_f32_16x16x32_bf16 v[144:147], a[148:151], a[188:191], v[144:147]\n" \
"ds_read_b128 a[208:211], v29 offset:32768\n" \
"v_mfma_f32_16x16x32_bf16 v[144:147], a[212:215], a[252:255], v[144:147]\n" \
"s_add_u32 m0, s30, 0x1000\n" \
"s_mov_b32 s84, 0x9000\n" \
"v_mfma_f32_16x16x32_bf16 v[140:143], a[148:151], a[184:187], v[140:143]\n" \
"buffer_load_dwordx4 v24, s[76:79], s84 offen offset:0 lds\n" \
"v_mfma_f32_16x16x32_bf16 v[140:143], a[212:215], a[248:251], v[140:143]\n" \
"ds_read_b64_tr_b16 a[170:171], v34 offset:33792\n" \
"v_mfma_f32_16x16x32_bf16 v[136:139], a[148:151], a[180:183], v[136:139]\n" \
"s_addk_i32 s84, 0x2000\n" \
"s_nop 0\n" \
"v_mfma_f32_16x16x32_bf16 v[136:139], a[212:215], a[244:247], v[136:139]\n" \
"buffer_load_dwordx4 v25, s[76:79], s84 offen offset:1024 lds\n" \
"v_mfma_f32_16x16x32_bf16 v[132:135], a[148:151], a[176:179], v[132:135]\n" \
"ds_read_b64_tr_b16 a[232:233], v34 offset:40960\n" \
"v_mfma_f32_16x16x32_bf16 v[132:135], a[212:215], a[240:243], v[132:135]\n" \
"ds_read_b128 a[148:151], v28 offset:34816\n" \
"v_mfma_f32_16x16x32_bf16 v[148:151], a[152:155], a[176:179], v[148:151]\n" \
"ds_read_b128 a[212:215], v29 offset:34816\n" \
"v_mfma_f32_16x16x32_bf16 v[148:151], a[216:219], a[240:243], v[148:151]\n" \
"ds_read_b64_tr_b16 a[234:235], v34 offset:41984\n" \
"v_mfma_f32_16x16x32_bf16 v[152:155], a[152:155], a[180:183], v[152:155]\n" \
"ds_read_b64_tr_b16 a[172:173], v35 offset:32768\n" \
"v_mfma_f32_16x16x32_bf16 v[152:155], a[216:219], a[244:247], v[152:155]\n" \
"ds_read_b64_tr_b16 a[174:175], v35 offset:33792\n" \
"v_mfma_f32_16x16x32_bf16 v[156:159], a[152:155], a[184:187], v[156:159]\n" \
"ds_read_b64_tr_b16 a[236:237], v35 offset:40960\n" \
"v_mfma_f32_16x16x32_bf16 v[156:159], a[216:219], a[248:251], v[156:159]\n" \
"ds_read_b64_tr_b16 a[238:239], v35 offset:41984\n" \
"v_mfma_f32_16x16x32_bf16 v[160:163], a[152:155], a[188:191], v[160:163]\n" \
"v_mfma_f32_16x16x32_bf16 v[160:163], a[216:219], a[252:255], v[160:163]\n" \
"ds_read_b128 a[152:155], v28 offset:36864\n" \
"v_mfma_f32_16x16x32_bf16 v[176:179], a[156:159], a[188:191], v[176:179]\n" \
"ds_read_b128 a[216:219], v29 offset:36864\n" \
"v_mfma_f32_16x16x32_bf16 v[176:179], a[220:223], a[252:255], v[176:179]\n" \
"v_mfma_f32_16x16x32_bf16 v[172:175], a[156:159], a[184:187], v[172:175]\n" \
"v_mfma_f32_16x16x32_bf16 v[172:175], a[220:223], a[248:251], v[172:175]\n" \
"v_mfma_f32_16x16x32_bf16 v[168:171], a[156:159], a[180:183], v[168:171]\n" \
"v_mfma_f32_16x16x32_bf16 v[168:171], a[220:223], a[244:247], v[168:171]\n" \
"v_mfma_f32_16x16x32_bf16 v[164:167], a[156:159], a[176:179], v[164:167]\n" \
"v_mfma_f32_16x16x32_bf16 v[164:167], a[220:223], a[240:243], v[164:167]\n" \
"ds_read_b128 a[156:159], v28 offset:38912\n" \
"ds_read_b128 a[220:223], v29 offset:38912\n" \
"s_waitcnt lgkmcnt(15)\n" \
"v_mfma_f32_16x16x32_bf16 a[0:3], a[128:131], a[160:163], a[0:3]\n" \
"s_addk_i32 s84, 0x2000\n" \
"v_mfma_f32_16x16x32_bf16 a[0:3], a[192:195], a[224:227], a[0:3]\n" \
"buffer_load_dwordx4 v24, s[76:79], s84 offen offset:2048 lds\n" \
"v_mfma_f32_16x16x32_bf16 a[4:7], a[128:131], a[164:167], a[4:7]\n" \
"ds_read_b64_tr_b16 a[176:177], v36 offset:32768\n" \
"v_mfma_f32_16x16x32_bf16 a[4:7], a[192:195], a[228:231], a[4:7]\n" \
"s_addk_i32 s84, 0x2000\n" \
"s_waitcnt lgkmcnt(13)\n" \
"v_mfma_f32_16x16x32_bf16 a[8:11], a[128:131], a[168:171], a[8:11]\n" \
"buffer_load_dwordx4 v25, s[76:79], s84 offen offset:3072 lds\n" \
"s_waitcnt lgkmcnt(9)\n" \
"v_mfma_f32_16x16x32_bf16 a[8:11], a[192:195], a[232:235], a[8:11]\n" \
"ds_read_b64_tr_b16 a[178:179], v36 offset:33792\n" \
"s_waitcnt lgkmcnt(8)\n" \
"v_mfma_f32_16x16x32_bf16 a[12:15], a[128:131], a[172:175], a[12:15]\n" \
"ds_read_b64_tr_b16 a[240:241], v36 offset:40960\n" \
"s_waitcnt lgkmcnt(7)\n" \
"v_mfma_f32_16x16x32_bf16 a[12:15], a[192:195], a[236:239], a[12:15]\n" \
"v_mfma_f32_16x16x32_bf16 a[28:31], a[132:135], a[172:175], a[28:31]\n" \
"v_mfma_f32_16x16x32_bf16 a[28:31], a[196:199], a[236:239], a[28:31]\n" \
"s_add_u32 m0, s30, 0x20000\n" \
"s_movk_i32 s84, 0x100\n" \
"v_mfma_f32_16x16x32_bf16 a[24:27], a[132:135], a[168:171], a[24:27]\n" \
"buffer_load_dwordx4 v24, s[76:79], s84 offen offset:0 lds\n" \
"v_mfma_f32_16x16x32_bf16 a[24:27], a[196:199], a[232:235], a[24:27]\n" \
"ds_read_b64_tr_b16 a[242:243], v36 offset:41984\n" \
"v_mfma_f32_16x16x32_bf16 a[20:23], a[132:135], a[164:167], a[20:23]\n" \
"v_accvgpr_read_b32 v184, a12\n" \
"v_accvgpr_read_b32 v185, a13\n" \
"v_mfma_f32_16x16x32_bf16 a[20:23], a[196:199], a[228:231], a[20:23]\n" \
"ds_read_b64_tr_b16 a[180:181], v37 offset:32768\n" \
"v_accvgpr_read_b32 v186, a14\n" \
"v_mfma_f32_16x16x32_bf16 a[16:19], a[132:135], a[160:163], a[16:19]\n" \
"s_movk_i32 s84, 0x2100\n" \
"s_nop 0\n" \
"v_mfma_f32_16x16x32_bf16 a[16:19], a[196:199], a[224:227], a[16:19]\n" \
"buffer_load_dwordx4 v25, s[76:79], s84 offen offset:1024 lds\n" \
"v_mfma_f32_16x16x32_bf16 a[32:35], a[136:139], a[160:163], a[32:35]\n" \
"ds_read_b64_tr_b16 a[182:183], v37 offset:33792\n" \
"v_accvgpr_read_b32 v187, a15\n" \
"v_mfma_f32_16x16x32_bf16 a[32:35], a[200:203], a[224:227], a[32:35]\n" \
"v_accvgpr_read_b32 v180, a28\n" \
"v_accvgpr_read_b32 v181, a29\n" \
"v_mfma_f32_16x16x32_bf16 a[36:39], a[136:139], a[164:167], a[36:39]\n" \
"v_accvgpr_read_b32 v182, a30\n" \
"v_accvgpr_read_b32 v183, a31\n" \
"v_mfma_f32_16x16x32_bf16 a[36:39], a[200:203], a[228:231], a[36:39]\n" \
"s_movk_i32 s84, 0x4100\n" \
"s_nop 0\n" \
"v_mfma_f32_16x16x32_bf16 a[40:43], a[136:139], a[168:171], a[40:43]\n" \
"buffer_load_dwordx4 v24, s[76:79], s84 offen offset:2048 lds\n" \
"v_mfma_f32_16x16x32_bf16 a[40:43], a[200:203], a[232:235], a[40:43]\n" \
"ds_read_b64_tr_b16 a[244:245], v37 offset:40960\n" \
"v_cvt_pk_bf16_f32 v184, v184, v185\n" \
"v_mfma_f32_16x16x32_bf16 a[44:47], a[136:139], a[172:175], a[44:47]\n" \
"ds_read_b64_tr_b16 a[246:247], v37 offset:41984\n" \
"v_cvt_pk_bf16_f32 v185, v186, v187\n" \
"v_mfma_f32_16x16x32_bf16 a[44:47], a[200:203], a[236:239], a[44:47]\n" \
"v_cvt_pk_bf16_f32 v186, v180, v181\n" \
"v_cvt_pk_bf16_f32 v187, v182, v183\n" \
"v_mfma_f32_16x16x32_bf16 a[60:63], a[140:143], a[172:175], a[60:63]\n" \
"s_movk_i32 s84, 0x6100\n" \
"v_mfma_f32_16x16x32_bf16 a[60:63], a[204:207], a[236:239], a[60:63]\n" \
"buffer_load_dwordx4 v25, s[76:79], s84 offen offset:3072 lds\n" \
"v_mfma_f32_16x16x32_bf16 a[56:59], a[140:143], a[168:171], a[56:59]\n" \
"ds_read_b64_tr_b16 a[184:185], v38 offset:32768\n" \
"v_accvgpr_read_b32 v188, a8\n" \
"v_mfma_f32_16x16x32_bf16 a[56:59], a[204:207], a[232:235], a[56:59]\n" \
"v_accvgpr_read_b32 v189, a9\n" \
"v_accvgpr_read_b32 v190, a10\n" \
"v_mfma_f32_16x16x32_bf16 a[52:55], a[140:143], a[164:167], a[52:55]\n" \
"s_add_u32 m0, s30, 0x21000\n" \
"s_addk_i32 s84, 0x3000\n" \
"v_mfma_f32_16x16x32_bf16 a[52:55], a[204:207], a[228:231], a[52:55]\n" \
"buffer_load_dwordx4 v24, s[76:79], s84 offen offset:0 lds\n" \
"v_mfma_f32_16x16x32_bf16 a[48:51], a[140:143], a[160:163], a[48:51]\n" \
"ds_read_b64_tr_b16 a[186:187], v38 offset:33792\n" \
"v_accvgpr_read_b32 v191, a11\n" \
"v_mfma_f32_16x16x32_bf16 a[48:51], a[204:207], a[224:227], a[48:51]\n" \
"v_accvgpr_read_b32 v180, a24\n" \
"v_accvgpr_read_b32 v181, a25\n" \
"v_mfma_f32_16x16x32_bf16 a[64:67], a[144:147], a[160:163], a[64:67]\n" \
"ds_read_b64_tr_b16 a[248:249], v38 offset:40960\n" \
"v_accvgpr_read_b32 v182, a26\n" \
"v_mfma_f32_16x16x32_bf16 a[64:67], a[208:211], a[224:227], a[64:67]\n" \
"s_addk_i32 s84, 0x2000\n" \
"s_nop 0\n" \
"v_mfma_f32_16x16x32_bf16 a[68:71], a[144:147], a[164:167], a[68:71]\n" \
"buffer_load_dwordx4 v25, s[76:79], s84 offen offset:1024 lds\n" \
"v_mfma_f32_16x16x32_bf16 a[68:71], a[208:211], a[228:231], a[68:71]\n" \
"ds_read_b64_tr_b16 a[250:251], v38 offset:41984\n" \
"v_accvgpr_read_b32 v183, a27\n" \
"v_mfma_f32_16x16x32_bf16 a[72:75], a[144:147], a[168:171], a[72:75]\n" \
"v_cvt_pk_bf16_f32 v188, v188, v189\n" \
"v_cvt_pk_bf16_f32 v189, v190, v191\n" \
"v_mfma_f32_16x16x32_bf16 a[72:75], a[208:211], a[232:235], a[72:75]\n" \
"v_cvt_pk_bf16_f32 v190, v180, v181\n" \
"v_cvt_pk_bf16_f32 v191, v182, v183\n" \
"v_mfma_f32_16x16x32_bf16 a[76:79], a[144:147], a[172:175], a[76:79]\n" \
"s_addk_i32 s84, 0x2000\n" \
"v_mfma_f32_16x16x32_bf16 a[76:79], a[208:211], a[236:239], a[76:79]\n" \
"buffer_load_dwordx4 v24, s[76:79], s84 offen offset:2048 lds\n" \
"v_mfma_f32_16x16x32_bf16 a[92:95], a[148:151], a[172:175], a[92:95]\n" \
"s_add_u32 s89, s41, 0x0\n" \
"buffer_store_dwordx4 v[184:187], v20, s[80:83], s89 offen offset:3072 sc1\n" \
"v_mfma_f32_16x16x32_bf16 a[92:95], a[212:215], a[236:239], a[92:95]\n" \
"ds_read_b64_tr_b16 a[188:189], v39 offset:32768\n" \
"v_accvgpr_read_b32 v192, a4\n" \
"v_mfma_f32_16x16x32_bf16 a[88:91], a[148:151], a[168:171], a[88:91]\n" \
"ds_read_b64_tr_b16 a[190:191], v39 offset:33792\n" \
"v_accvgpr_read_b32 v193, a5\n" \
"v_mfma_f32_16x16x32_bf16 a[88:91], a[212:215], a[232:235], a[88:91]\n" \
"s_addk_i32 s84, 0x2000\n" \
"v_mfma_f32_16x16x32_bf16 a[84:87], a[148:151], a[164:167], a[84:87]\n" \
"buffer_load_dwordx4 v25, s[76:79], s84 offen offset:3072 lds\n" \
"v_mfma_f32_16x16x32_bf16 a[84:87], a[212:215], a[228:231], a[84:87]\n" \
"ds_read_b64_tr_b16 a[252:253], v39 offset:40960\n" \
"v_accvgpr_read_b32 v194, a6\n" \
"v_mfma_f32_16x16x32_bf16 a[80:83], a[148:151], a[160:163], a[80:83]\n" \
"s_add_u32 s88, s41, 0x0\n" \
"buffer_store_dwordx4 v[188:191], v20, s[80:83], s88 offen offset:2048 sc1\n" \
"v_mfma_f32_16x16x32_bf16 a[80:83], a[212:215], a[224:227], a[80:83]\n" \
"v_accvgpr_read_b32 v195, a7\n" \
"v_accvgpr_read_b32 v180, a20\n" \
"s_waitcnt lgkmcnt(15)\n" \
"s_nop 0\n" \
"v_mfma_f32_16x16x32_bf16 a[96:99], a[152:155], a[160:163], a[96:99]\n" \
"ds_read_b64_tr_b16 a[254:255], v39 offset:41984\n" \
"v_accvgpr_read_b32 v181, a21\n" \
"v_mfma_f32_16x16x32_bf16 a[96:99], a[216:219], a[224:227], a[96:99]\n" \
"v_accvgpr_read_b32 v182, a22\n" \
"v_accvgpr_read_b32 v183, a23\n" \
"v_mfma_f32_16x16x32_bf16 a[100:103], a[152:155], a[164:167], a[100:103]\n" \
"v_cvt_pk_bf16_f32 v192, v192, v193\n" \
"v_cvt_pk_bf16_f32 v193, v194, v195\n" \
"v_mfma_f32_16x16x32_bf16 a[100:103], a[216:219], a[228:231], a[100:103]\n" \
"v_cvt_pk_bf16_f32 v194, v180, v181\n" \
"v_cvt_pk_bf16_f32 v195, v182, v183\n" \
"v_mfma_f32_16x16x32_bf16 a[104:107], a[152:155], a[168:171], a[104:107]\n" \
"s_add_u32 s89, s41, 0x0\n" \
"s_nop 0\n" \
"buffer_store_dwordx4 v[192:195], v20, s[80:83], s89 offen offset:1024 sc1\n" \
"v_mfma_f32_16x16x32_bf16 a[104:107], a[216:219], a[232:235], a[104:107]\n" \
"v_accvgpr_read_b32 v196, a0\n" \
"v_accvgpr_read_b32 v197, a1\n" \
"v_mfma_f32_16x16x32_bf16 a[108:111], a[152:155], a[172:175], a[108:111]\n" \
"v_accvgpr_read_b32 v198, a2\n" \
"v_accvgpr_read_b32 v199, a3\n" \
"v_mfma_f32_16x16x32_bf16 a[108:111], a[216:219], a[236:239], a[108:111]\n" \
"v_accvgpr_read_b32 v180, a16\n" \
"v_accvgpr_read_b32 v181, a17\n" \
"v_mfma_f32_16x16x32_bf16 a[124:127], a[156:159], a[172:175], a[124:127]\n" \
"v_accvgpr_read_b32 v182, a18\n" \
"v_accvgpr_read_b32 v183, a19\n" \
"v_mfma_f32_16x16x32_bf16 a[124:127], a[220:223], a[236:239], a[124:127]\n" \
"v_cvt_pk_bf16_f32 v196, v196, v197\n" \
"v_cvt_pk_bf16_f32 v197, v198, v199\n" \
"v_mfma_f32_16x16x32_bf16 a[120:123], a[156:159], a[168:171], a[120:123]\n" \
"v_cvt_pk_bf16_f32 v198, v180, v181\n" \
"v_cvt_pk_bf16_f32 v199, v182, v183\n" \
"s_add_u32 s88, s41, 0x0\n" \
"s_nop 0\n" \
"buffer_store_dwordx4 v[196:199], v20, s[80:83], s88 offen offset:0 sc1\n" \
"v_mfma_f32_16x16x32_bf16 a[120:123], a[220:223], a[232:235], a[120:123]\n" \
"v_accvgpr_read_b32 v200, a44\n" \
"v_accvgpr_read_b32 v201, a45\n" \
"v_mfma_f32_16x16x32_bf16 a[116:119], a[156:159], a[164:167], a[116:119]\n" \
"v_accvgpr_read_b32 v202, a46\n" \
"v_accvgpr_read_b32 v203, a47\n" \
"v_mfma_f32_16x16x32_bf16 a[116:119], a[220:223], a[228:231], a[116:119]\n" \
"v_accvgpr_read_b32 v180, a60\n" \
"v_accvgpr_read_b32 v181, a61\n" \
"v_mfma_f32_16x16x32_bf16 a[112:115], a[156:159], a[160:163], a[112:115]\n" \
"v_accvgpr_read_b32 v182, a62\n" \
"v_accvgpr_read_b32 v183, a63\n" \
"v_mfma_f32_16x16x32_bf16 a[112:115], a[220:223], a[224:227], a[112:115]\n" \
"v_cvt_pk_bf16_f32 v200, v200, v201\n" \
"v_cvt_pk_bf16_f32 v201, v202, v203\n" \
"s_waitcnt vmcnt(12) lgkmcnt(0)\n" \
"s_barrier\n" \
"v_xor_b32_e32 v36, 0x4000, v36\n" \
"v_xor_b32_e64 v32, 0x0, v48\n" \
"v_xor_b32_e32 v37, 0x4000, v37\n" \
"v_xor_b32_e64 v33, 0x20, v48\n" \
"v_xor_b32_e32 v38, 0x4000, v38\n" \
"v_xor_b32_e64 v34, 0x40, v48\n" \
"v_xor_b32_e32 v39, 0x4000, v39\n" \
"v_xor_b32_e32 v35, 0x60, v48\n" \
"v_add_u32_e32 v28, s43, v28\n" \
"v_add_u32_e32 v29, s43, v29\n" \
"v_mfma_f32_16x16x32_bf16 v[116:119], a[144:147], a[176:179], v[116:119]\n" \
"s_add_u32 m0, s40, 0x8000\n" \
"s_add_u32 s85, s39, 0x2000\n" \
"v_mfma_f32_16x16x32_bf16 v[116:119], a[208:211], a[240:243], v[116:119]\n" \
"buffer_load_dwordx4 v20, s[80:83], s85 offen sc1 lds\n" \
"v_mfma_f32_16x16x32_bf16 v[120:123], a[144:147], a[180:183], v[120:123]\n" \
"ds_read_b64_tr_b16 a[160:161], v32 offset:0\n" \
"v_cvt_pk_bf16_f32 v202, v180, v181\n" \
"v_mfma_f32_16x16x32_bf16 v[120:123], a[208:211], a[244:247], v[120:123]\n" \
"s_add_u32 m0, s40, 0x9000\n" \
"s_add_u32 s85, s39, 0x3000\n" \
"v_mfma_f32_16x16x32_bf16 v[124:127], a[144:147], a[184:187], v[124:127]\n" \
"buffer_load_dwordx4 v20, s[80:83], s85 offen sc1 lds\n" \
"v_mfma_f32_16x16x32_bf16 v[124:127], a[208:211], a[248:251], v[124:127]\n" \
"ds_read_b64_tr_b16 a[162:163], v32 offset:512\n" \
"v_cvt_pk_bf16_f32 v203, v182, v183\n" \
"v_mfma_f32_16x16x32_bf16 v[128:131], a[144:147], a[188:191], v[128:131]\n" \
"ds_read_b64_tr_b16 a[224:225], v32 offset:4096\n" \
"v_accvgpr_read_b32 v204, a40\n" \
"v_mfma_f32_16x16x32_bf16 v[128:131], a[208:211], a[252:255], v[128:131]\n" \
"ds_read_b128 a[144:147], v28 offset:8192\n" \
"v_accvgpr_read_b32 v205, a41\n" \
"v_mfma_f32_16x16x32_bf16 v[144:147], a[148:151], a[188:191], v[144:147]\n" \
"ds_read_b128 a[208:211], v29 offset:8192\n" \
"v_accvgpr_read_b32 v206, a42\n" \
"v_mfma_f32_16x16x32_bf16 v[144:147], a[212:215], a[252:255], v[144:147]\n" \
"s_add_u32 m0, s40, 0xb000\n" \
"s_movk_i32 s84, 0x80\n" \
"v_mfma_f32_16x16x32_bf16 v[140:143], a[148:151], a[184:187], v[140:143]\n" \
"buffer_load_dwordx4 v21, s[76:79], s84 offen offset:0 lds\n" \
"v_mfma_f32_16x16x32_bf16 v[140:143], a[212:215], a[248:251], v[140:143]\n" \
"s_add_u32 m0, s30, 0x8000\n" \
"s_movk_i32 s84, 0x80\n" \
"v_mfma_f32_16x16x32_bf16 v[136:139], a[148:151], a[180:183], v[136:139]\n" \
"buffer_load_dwordx4 v24, s[76:79], s84 offen offset:0 lds\n" \
"v_mfma_f32_16x16x32_bf16 v[136:139], a[212:215], a[244:247], v[136:139]\n" \
"ds_read_b64_tr_b16 a[226:227], v32 offset:4608\n" \
"v_accvgpr_read_b32 v207, a43\n" \
"v_mfma_f32_16x16x32_bf16 v[132:135], a[148:151], a[176:179], v[132:135]\n" \
"ds_read_b64_tr_b16 a[164:165], v33 offset:0\n" \
"v_accvgpr_read_b32 v180, a56\n" \
"v_mfma_f32_16x16x32_bf16 v[132:135], a[212:215], a[240:243], v[132:135]\n" \
"ds_read_b128 a[148:151], v28 offset:10240\n" \
"v_accvgpr_read_b32 v181, a57\n" \
"v_mfma_f32_16x16x32_bf16 v[148:151], a[152:155], a[176:179], v[148:151]\n" \
"ds_read_b128 a[212:215], v29 offset:10240\n" \
"v_accvgpr_read_b32 v182, a58\n" \
"v_mfma_f32_16x16x32_bf16 v[148:151], a[216:219], a[240:243], v[148:151]\n" \
"s_movk_i32 s84, 0x2080\n" \
"v_mfma_f32_16x16x32_bf16 v[152:155], a[152:155], a[180:183], v[152:155]\n" \
"buffer_load_dwordx4 v25, s[76:79], s84 offen offset:1024 lds\n" \
"v_mfma_f32_16x16x32_bf16 v[152:155], a[216:219], a[244:247], v[152:155]\n" \
"s_movk_i32 s84, 0x4080\n" \
"v_mfma_f32_16x16x32_bf16 v[156:159], a[152:155], a[184:187], v[156:159]\n" \
"buffer_load_dwordx4 v24, s[76:79], s84 offen offset:2048 lds\n" \
"v_mfma_f32_16x16x32_bf16 v[156:159], a[216:219], a[248:251], v[156:159]\n" \
"ds_read_b64_tr_b16 a[166:167], v33 offset:512\n" \
"v_accvgpr_read_b32 v183, a59\n" \
"v_mfma_f32_16x16x32_bf16 v[160:163], a[152:155], a[188:191], v[160:163]\n" \
"ds_read_b64_tr_b16 a[228:229], v33 offset:4096\n" \
"v_cvt_pk_bf16_f32 v204, v204, v205\n" \
"v_mfma_f32_16x16x32_bf16 v[160:163], a[216:219], a[252:255], v[160:163]\n" \
"ds_read_b128 a[152:155], v28 offset:12288\n" \
"v_cvt_pk_bf16_f32 v205, v206, v207\n" \
"v_mfma_f32_16x16x32_bf16 v[176:179], a[156:159], a[188:191], v[176:179]\n" \
"ds_read_b128 a[216:219], v29 offset:12288\n" \
"v_cvt_pk_bf16_f32 v206, v180, v181\n" \
"v_mfma_f32_16x16x32_bf16 v[176:179], a[220:223], a[252:255], v[176:179]\n" \
"s_movk_i32 s84, 0x6080\n" \
"v_mfma_f32_16x16x32_bf16 v[172:175], a[156:159], a[184:187], v[172:175]\n" \
"buffer_load_dwordx4 v25, s[76:79], s84 offen offset:3072 lds\n" \
"v_mfma_f32_16x16x32_bf16 v[172:175], a[220:223], a[248:251], v[172:175]\n" \
"s_add_u32 m0, s31, 0x8000\n" \
"s_mov_b32 s84, 0x80000\n" \
"v_mfma_f32_16x16x32_bf16 v[168:171], a[156:159], a[180:183], v[168:171]\n" \
"buffer_load_dwordx4 v26, s[68:71], s84 offen offset:0 lds\n" \
"v_mfma_f32_16x16x32_bf16 v[168:171], a[220:223], a[244:247], v[168:171]\n" \
"s_addk_i32 s84, 0x7c00\n" \
"v_mfma_f32_16x16x32_bf16 v[164:167], a[156:159], a[176:179], v[164:167]\n" \
"buffer_load_dwordx4 v26, s[68:71], s84 offen offset:1024 lds\n" \
"v_mfma_f32_16x16x32_bf16 v[164:167], a[220:223], a[240:243], v[164:167]\n" \
"ds_read_b128 a[156:159], v28 offset:14336\n" \
"v_cvt_pk_bf16_f32 v207, v182, v183\n" \
"v_mfma_f32_16x16x32_bf16 v[84:87], a[136:139], a[176:179], v[84:87]\n" \
"ds_read_b128 a[220:223], v29 offset:14336\n" \
"v_accvgpr_read_b32 v208, a36\n" \
"v_mfma_f32_16x16x32_bf16 v[84:87], a[200:203], a[240:243], v[84:87]\n" \
"s_addk_i32 s84, 0x7c00\n" \
"v_mfma_f32_16x16x32_bf16 v[88:91], a[136:139], a[180:183], v[88:91]\n" \
"buffer_load_dwordx4 v27, s[68:71], s84 offen offset:2048 lds\n" \
"v_mfma_f32_16x16x32_bf16 v[88:91], a[200:203], a[244:247], v[88:91]\n" \
"ds_read_b64_tr_b16 a[230:231], v33 offset:4608\n" \
"v_accvgpr_read_b32 v209, a37\n" \
"v_mfma_f32_16x16x32_bf16 v[92:95], a[136:139], a[184:187], v[92:95]\n" \
"s_addk_i32 s84, 0x7c00\n" \
"v_mfma_f32_16x16x32_bf16 v[92:95], a[200:203], a[248:251], v[92:95]\n" \
"buffer_load_dwordx4 v27, s[68:71], s84 offen offset:3072 lds\n" \
"v_mfma_f32_16x16x32_bf16 v[96:99], a[136:139], a[188:191], v[96:99]\n" \
"ds_read_b64_tr_b16 a[172:173], v35 offset:0\n" \
"v_accvgpr_read_b32 v210, a38\n" \
"v_mfma_f32_16x16x32_bf16 v[96:99], a[200:203], a[252:255], v[96:99]\n" \
"ds_read_b128 a[136:139], v28 offset:4096\n" \
"v_accvgpr_read_b32 v211, a39\n" \
"v_mfma_f32_16x16x32_bf16 v[112:115], a[140:143], a[188:191], v[112:115]\n" \
"ds_read_b128 a[200:203], v29 offset:4096\n" \
"v_accvgpr_read_b32 v180, a52\n" \
"v_mfma_f32_16x16x32_bf16 v[112:115], a[204:207], a[252:255], v[112:115]\n" \
"s_add_u32 m0, s30, 0x9000\n" \
"s_mov_b32 s84, 0x9080\n" \
"v_mfma_f32_16x16x32_bf16 v[108:111], a[140:143], a[184:187], v[108:111]\n" \
"buffer_load_dwordx4 v24, s[76:79], s84 offen offset:0 lds\n" \
"v_mfma_f32_16x16x32_bf16 v[108:111], a[204:207], a[248:251], v[108:111]\n" \
"ds_read_b64_tr_b16 a[174:175], v35 offset:512\n" \
"v_accvgpr_read_b32 v181, a53\n" \
"v_mfma_f32_16x16x32_bf16 v[104:107], a[140:143], a[180:183], v[104:107]\n" \
"s_addk_i32 s84, 0x2000\n" \
"s_nop 0\n" \
"v_mfma_f32_16x16x32_bf16 v[104:107], a[204:207], a[244:247], v[104:107]\n" \
"buffer_load_dwordx4 v25, s[76:79], s84 offen offset:1024 lds\n" \
"v_mfma_f32_16x16x32_bf16 v[100:103], a[140:143], a[176:179], v[100:103]\n" \
"ds_read_b64_tr_b16 a[236:237], v35 offset:4096\n" \
"v_accvgpr_read_b32 v182, a54\n" \
"v_mfma_f32_16x16x32_bf16 v[100:103], a[204:207], a[240:243], v[100:103]\n" \
"ds_read_b128 a[140:143], v28 offset:6144\n" \
"v_accvgpr_read_b32 v183, a55\n" \
"v_mfma_f32_16x16x32_bf16 v[52:55], a[128:131], a[176:179], v[52:55]\n" \
"ds_read_b128 a[204:207], v29 offset:6144\n" \
"v_cvt_pk_bf16_f32 v208, v208, v209\n" \
"v_mfma_f32_16x16x32_bf16 v[52:55], a[192:195], a[240:243], v[52:55]\n" \
"ds_read_b64_tr_b16 a[238:239], v35 offset:4608\n" \
"v_cvt_pk_bf16_f32 v209, v210, v211\n" \
"v_mfma_f32_16x16x32_bf16 v[56:59], a[128:131], a[180:183], v[56:59]\n" \
"ds_read_b64_tr_b16 a[168:169], v34 offset:0\n" \
"v_cvt_pk_bf16_f32 v210, v180, v181\n" \
"v_mfma_f32_16x16x32_bf16 v[56:59], a[192:195], a[244:247], v[56:59]\n" \
"ds_read_b64_tr_b16 a[170:171], v34 offset:512\n" \
"v_cvt_pk_bf16_f32 v211, v182, v183\n" \
"v_mfma_f32_16x16x32_bf16 v[60:63], a[128:131], a[184:187], v[60:63]\n" \
"ds_read_b64_tr_b16 a[232:233], v34 offset:4096\n" \
"v_accvgpr_read_b32 v212, a32\n" \
"v_mfma_f32_16x16x32_bf16 v[60:63], a[192:195], a[248:251], v[60:63]\n" \
"ds_read_b64_tr_b16 a[234:235], v34 offset:4608\n" \
"v_accvgpr_read_b32 v213, a33\n" \
"v_mfma_f32_16x16x32_bf16 v[64:67], a[128:131], a[188:191], v[64:67]\n" \
"v_accvgpr_read_b32 v214, a34\n" \
"v_accvgpr_read_b32 v215, a35\n" \
"v_mfma_f32_16x16x32_bf16 v[64:67], a[192:195], a[252:255], v[64:67]\n" \
"ds_read_b128 a[128:131], v28 offset:0\n" \
"v_accvgpr_read_b32 v180, a48\n" \
"v_mfma_f32_16x16x32_bf16 v[80:83], a[132:135], a[188:191], v[80:83]\n" \
"ds_read_b128 a[192:195], v29 offset:0\n" \
"v_accvgpr_read_b32 v181, a49\n" \
"v_mfma_f32_16x16x32_bf16 v[80:83], a[196:199], a[252:255], v[80:83]\n" \
"v_accvgpr_read_b32 v182, a50\n" \
"v_accvgpr_read_b32 v183, a51\n" \
"v_mfma_f32_16x16x32_bf16 v[76:79], a[132:135], a[184:187], v[76:79]\n" \
"v_cvt_pk_bf16_f32 v212, v212, v213\n" \
"v_cvt_pk_bf16_f32 v213, v214, v215\n" \
"v_mfma_f32_16x16x32_bf16 v[76:79], a[196:199], a[248:251], v[76:79]\n" \
"v_cvt_pk_bf16_f32 v214, v180, v181\n" \
"v_cvt_pk_bf16_f32 v215, v182, v183\n" \
"v_mfma_f32_16x16x32_bf16 v[72:75], a[132:135], a[180:183], v[72:75]\n" \
"v_accvgpr_read_b32 v216, a68\n" \
"v_accvgpr_read_b32 v217, a69\n" \
"v_mfma_f32_16x16x32_bf16 v[72:75], a[196:199], a[244:247], v[72:75]\n" \
"v_accvgpr_read_b32 v218, a70\n" \
"v_accvgpr_read_b32 v219, a71\n" \
"v_mfma_f32_16x16x32_bf16 v[68:71], a[132:135], a[176:179], v[68:71]\n" \
"v_accvgpr_read_b32 v180, a84\n" \
"v_accvgpr_read_b32 v181, a85\n" \
"v_mfma_f32_16x16x32_bf16 v[68:71], a[196:199], a[240:243], v[68:71]\n" \
"ds_read_b128 a[132:135], v28 offset:2048\n" \
"ds_read_b128 a[196:199], v29 offset:2048\n" \
"s_waitcnt lgkmcnt(15)\n" \
"v_mfma_f32_16x16x32_bf16 a[0:3], a[160:163], a[144:147], 0\n" \
"s_addk_i32 s84, 0x2000\n" \
"v_accvgpr_read_b32 v182, a86\n" \
"v_mfma_f32_16x16x32_bf16 a[0:3], a[224:227], a[208:211], a[0:3]\n" \
"buffer_load_dwordx4 v24, s[76:79], s84 offen offset:2048 lds\n" \
"v_accvgpr_read_b32 v183, a87\n" \
"v_mfma_f32_16x16x32_bf16 a[4:7], a[164:167], a[144:147], 0\n" \
"ds_read_b64_tr_b16 a[176:177], v36 offset:0\n" \
"v_cvt_pk_bf16_f32 v216, v216, v217\n" \
"v_mfma_f32_16x16x32_bf16 a[4:7], a[228:231], a[208:211], a[4:7]\n" \
"s_addk_i32 s84, 0x2000\n" \
"v_cvt_pk_bf16_f32 v217, v218, v219\n" \
"s_waitcnt lgkmcnt(13)\n" \
"v_mfma_f32_16x16x32_bf16 a[12:15], a[172:175], a[144:147], 0\n" \
"buffer_load_dwordx4 v25, s[76:79], s84 offen offset:3072 lds\n" \
"v_cvt_pk_bf16_f32 v218, v180, v181\n" \
"s_waitcnt lgkmcnt(9)\n" \
"v_mfma_f32_16x16x32_bf16 a[12:15], a[236:239], a[208:211], a[12:15]\n" \
"ds_read_b64_tr_b16 a[178:179], v36 offset:1024\n" \
"v_cvt_pk_bf16_f32 v219, v182, v183\n" \
"s_waitcnt lgkmcnt(8)\n" \
"v_mfma_f32_16x16x32_bf16 a[8:11], a[144:147], a[168:171], 0\n" \
"ds_read_b64_tr_b16 a[240:241], v36 offset:8192\n" \
"v_accvgpr_read_b32 v220, a64\n" \
"s_waitcnt lgkmcnt(7)\n" \
"s_nop 0\n" \
"v_mfma_f32_16x16x32_bf16 a[8:11], a[208:211], a[232:235], a[8:11]\n" \
"ds_read_b64_tr_b16 a[242:243], v36 offset:9216\n" \
"v_accvgpr_read_b32 v221, a65\n" \
"v_mfma_f32_16x16x32_bf16 a[24:27], a[148:151], a[168:171], 0\n" \
"ds_read_b64_tr_b16 a[180:181], v37 offset:0\n" \
"v_accvgpr_read_b32 v222, a66\n" \
"v_mfma_f32_16x16x32_bf16 a[24:27], a[212:215], a[232:235], a[24:27]\n" \
"ds_read_b64_tr_b16 a[182:183], v37 offset:1024\n" \
"v_accvgpr_read_b32 v223, a67\n" \
"v_mfma_f32_16x16x32_bf16 a[28:31], a[172:175], a[148:151], 0\n" \
"ds_read_b64_tr_b16 a[244:245], v37 offset:8192\n" \
"v_accvgpr_read_b32 v180, a80\n" \
"v_mfma_f32_16x16x32_bf16 a[28:31], a[236:239], a[212:215], a[28:31]\n" \
"s_add_u32 s89, s41, 0x2000\n" \
"buffer_store_dwordx4 v[200:203], v20, s[80:83], s89 offen offset:3072 sc1\n" \
"v_mfma_f32_16x16x32_bf16 a[20:23], a[164:167], a[148:151], 0\n" \
"ds_read_b64_tr_b16 a[246:247], v37 offset:9216\n" \
"v_accvgpr_read_b32 v181, a81\n" \
"v_mfma_f32_16x16x32_bf16 a[20:23], a[228:231], a[212:215], a[20:23]\n" \
"ds_read_b64_tr_b16 a[184:185], v38 offset:0\n" \
"v_accvgpr_read_b32 v182, a82\n" \
"v_mfma_f32_16x16x32_bf16 a[16:19], a[160:163], a[148:151], 0\n" \
"ds_read_b64_tr_b16 a[186:187], v38 offset:1024\n" \
"v_accvgpr_read_b32 v183, a83\n" \
"v_mfma_f32_16x16x32_bf16 a[16:19], a[224:227], a[212:215], a[16:19]\n" \
"v_cvt_pk_bf16_f32 v220, v220, v221\n" \
"v_cvt_pk_bf16_f32 v221, v222, v223\n" \
"v_cvt_pk_bf16_f32 v222, v180, v181\n" \
"v_mfma_f32_16x16x32_bf16 a[32:35], a[160:163], a[152:155], 0\n" \
"s_add_u32 s88, s41, 0x2000\n" \
"buffer_store_dwordx4 v[204:207], v20, s[80:83], s88 offen offset:2048 sc1\n" \
"v_mfma_f32_16x16x32_bf16 a[32:35], a[224:227], a[216:219], a[32:35]\n" \
"ds_read_b64_tr_b16 a[248:249], v38 offset:8192\n" \
".fill (L_pb + 61152 - .) / 4, 4, 0xBF800000\n" \
"L_pp15:\n" \
"s_cbranch_execz L_pret\n" \
"s_nop 0\n" \
"v_cvt_pk_bf16_f32 v223, v182, v183\n" \
"v_mfma_f32_16x16x32_bf16 a[36:39], a[164:167], a[152:155], 0\n" \
"ds_read_b64_tr_b16 a[250:251], v38 offset:9216\n" \
"v_cvt_pk_bf16_f32 v224, v128, v129\n" \
"v_mfma_f32_16x16x32_bf16 a[36:39], a[228:231], a[216:219], a[36:39]\n" \
"ds_read_b64_tr_b16 a[188:189], v39 offset:0\n" \
"v_cvt_pk_bf16_f32 v225, v130, v131\n" \
"v_mfma_f32_16x16x32_bf16 a[44:47], a[172:175], a[152:155], 0\n" \
"v_cvt_pk_bf16_f32 v226, v144, v145\n" \
"v_cvt_pk_bf16_f32 v227, v146, v147\n" \
"v_cvt_pk_bf16_f32 v228, v124, v125\n" \
"v_mfma_f32_16x16x32_bf16 a[44:47], a[236:239], a[216:219], a[44:47]\n" \
"ds_read_b64_tr_b16 a[190:191], v39 offset:1024\n" \
"v_cvt_pk_bf16_f32 v229, v126, v127\n" \
"v_mfma_f32_16x16x32_bf16 a[40:43], a[152:155], a[168:171], 0\n" \
"s_add_u32 s89, s41, 0x2000\n" \
"buffer_store_dwordx4 v[208:211], v20, s[80:83], s89 offen offset:1024 sc1\n" \
"v_mfma_f32_16x16x32_bf16 a[40:43], a[216:219], a[232:235], a[40:43]\n" \
"ds_read_b64_tr_b16 a[252:253], v39 offset:8192\n" \
"v_cvt_pk_bf16_f32 v230, v140, v141\n" \
"v_mfma_f32_16x16x32_bf16 a[56:59], a[156:159], a[168:171], 0\n" \
"ds_read_b64_tr_b16 a[254:255], v39 offset:9216\n" \
"v_cvt_pk_bf16_f32 v231, v142, v143\n" \
"v_mfma_f32_16x16x32_bf16 a[56:59], a[220:223], a[232:235], a[56:59]\n" \
"v_cvt_pk_bf16_f32 v232, v120, v121\n" \
"v_cvt_pk_bf16_f32 v233, v122, v123\n" \
"v_cvt_pk_bf16_f32 v234, v136, v137\n" \
"v_mfma_f32_16x16x32_bf16 a[60:63], a[172:175], a[156:159], 0\n" \
"v_cvt_pk_bf16_f32 v235, v138, v139\n" \
"v_cvt_pk_bf16_f32 v236, v116, v117\n" \
"v_cvt_pk_bf16_f32 v237, v118, v119\n" \
"v_mfma_f32_16x16x32_bf16 a[60:63], a[236:239], a[220:223], a[60:63]\n" \
"s_add_u32 s88, s41, 0x2000\n" \
"buffer_store_dwordx4 v[212:215], v20, s[80:83], s88 offen offset:0 sc1\n" \
"v_mfma_f32_16x16x32_bf16 a[52:55], a[164:167], a[156:159], 0\n" \
"v_cvt_pk_bf16_f32 v238, v132, v133\n" \
"v_cvt_pk_bf16_f32 v239, v134, v135\n" \
"v_cvt_pk_bf16_f32 v240, v160, v161\n" \
"v_mfma_f32_16x16x32_bf16 a[52:55], a[228:231], a[220:223], a[52:55]\n" \
"v_cvt_pk_bf16_f32 v241, v162, v163\n" \
"v_cvt_pk_bf16_f32 v242, v176, v177\n" \
"v_cvt_pk_bf16_f32 v243, v178, v179\n" \
"v_mfma_f32_16x16x32_bf16 a[48:51], a[160:163], a[156:159], 0\n" \
"v_cvt_pk_bf16_f32 v244, v156, v157\n" \
"v_cvt_pk_bf16_f32 v245, v158, v159\n" \
"v_cvt_pk_bf16_f32 v246, v172, v173\n" \
"v_mfma_f32_16x16x32_bf16 a[48:51], a[224:227], a[220:223], a[48:51]\n" \
"v_cvt_pk_bf16_f32 v247, v174, v175\n" \
"v_cvt_pk_bf16_f32 v184, v152, v153\n" \
"v_cvt_pk_bf16_f32 v185, v154, v155\n" \
"v_mfma_f32_16x16x32_bf16 a[64:67], a[164:167], a[140:143], 0\n" \
"v_cvt_pk_bf16_f32 v186, v168, v169\n" \
"v_cvt_pk_bf16_f32 v187, v170, v171\n" \
"v_cvt_pk_bf16_f32 v188, v148, v149\n" \
"v_mfma_f32_16x16x32_bf16 a[64:67], a[228:231], a[204:207], a[64:67]\n" \
"v_cvt_pk_bf16_f32 v189, v150, v151\n" \
"v_cvt_pk_bf16_f32 v190, v164, v165\n" \
"v_cvt_pk_bf16_f32 v191, v166, v167\n" \
"v_mfma_f32_16x16x32_bf16 a[80:83], a[172:175], a[140:143], 0\n" \
"v_cvt_pk_bf16_f32 v192, v96, v97\n" \
"v_cvt_pk_bf16_f32 v193, v98, v99\n" \
"v_cvt_pk_bf16_f32 v194, v112, v113\n" \
"v_mfma_f32_16x16x32_bf16 a[80:83], a[236:239], a[204:207], a[80:83]\n" \
"v_cvt_pk_bf16_f32 v195, v114, v115\n" \
"v_cvt_pk_bf16_f32 v196, v92, v93\n" \
"v_cvt_pk_bf16_f32 v197, v94, v95\n" \
"v_mfma_f32_16x16x32_bf16 a[68:71], a[140:143], a[168:171], 0\n" \
"v_cvt_pk_bf16_f32 v198, v108, v109\n" \
"v_cvt_pk_bf16_f32 v199, v110, v111\n" \
"v_cvt_pk_bf16_f32 v252, v88, v89\n" \
"v_mfma_f32_16x16x32_bf16 a[68:71], a[204:207], a[232:235], a[68:71]\n" \
"v_cvt_pk_bf16_f32 v253, v90, v91\n" \
"v_cvt_pk_bf16_f32 v254, v104, v105\n" \
"v_cvt_pk_bf16_f32 v255, v106, v107\n" \
"s_waitcnt vmcnt(4) lgkmcnt(0)\n" \
"s_barrier\n" \
"v_mfma_f32_16x16x32_bf16 v[116:119], a[176:179], a[144:147], 0\n" \
"s_add_u32 m0, s40, 0x0\n" \
"s_nop 0\n" \
"s_add_u32 s85, s39, 0x4000\n" \
"v_accvgpr_write_b32 a84, v252\n" \
"v_mfma_f32_16x16x32_bf16 v[116:119], a[240:243], a[208:211], v[116:119]\n" \
"buffer_load_dwordx4 v20, s[80:83], s85 offen sc1 lds\n" \
"v_accvgpr_write_b32 a85, v253\n" \
"v_mfma_f32_16x16x32_bf16 v[120:123], a[180:183], a[144:147], 0\n" \
"ds_read_b64_tr_b16 a[160:161], v32 offset:32768\n" \
"v_accvgpr_write_b32 a86, v254\n" \
"v_mfma_f32_16x16x32_bf16 v[120:123], a[244:247], a[208:211], v[120:123]\n" \
"ds_read_b64_tr_b16 a[162:163], v32 offset:33280\n" \
"v_accvgpr_write_b32 a87, v255\n" \
"v_mfma_f32_16x16x32_bf16 v[124:127], a[184:187], a[144:147], 0\n" \
"s_add_u32 m0, s40, 0x1000\n" \
"s_add_u32 s85, s39, 0x5000\n" \
"v_cvt_pk_bf16_f32 v252, v84, v85\n" \
"v_mfma_f32_16x16x32_bf16 v[124:127], a[248:251], a[208:211], v[124:127]\n" \
"buffer_load_dwordx4 v20, s[80:83], s85 offen sc1 lds\n" \
"v_cvt_pk_bf16_f32 v253, v86, v87\n" \
"v_mfma_f32_16x16x32_bf16 v[128:131], a[188:191], a[144:147], 0\n" \
"ds_read_b64_tr_b16 a[224:225], v32 offset:36864\n" \
"v_cvt_pk_bf16_f32 v254, v100, v101\n" \
"v_mfma_f32_16x16x32_bf16 v[128:131], a[252:255], a[208:211], v[128:131]\n" \
"ds_read_b128 a[144:147], v28 offset:40960\n" \
"v_cvt_pk_bf16_f32 v255, v102, v103\n" \
"v_mfma_f32_16x16x32_bf16 v[144:147], a[188:191], a[148:151], 0\n" \
"ds_read_b128 a[208:211], v29 offset:40960\n" \
"v_cvt_pk_bf16_f32 v200, v64, v65\n" \
"v_mfma_f32_16x16x32_bf16 v[144:147], a[252:255], a[212:215], v[144:147]\n" \
"s_add_u32 m0, s40, 0x3000\n" \
"s_movk_i32 s84, 0x100\n" \
"s_nop 0\n" \
"v_cvt_pk_bf16_f32 v201, v66, v67\n" \
"v_mfma_f32_16x16x32_bf16 v[140:143], a[184:187], a[148:151], 0\n" \
"buffer_load_dwordx4 v21, s[76:79], s84 offen offset:0 lds\n" \
"v_cvt_pk_bf16_f32 v202, v80, v81\n" \
"v_mfma_f32_16x16x32_bf16 v[140:143], a[248:251], a[212:215], v[140:143]\n" \
"ds_read_b64_tr_b16 a[226:227], v32 offset:37376\n" \
"v_cvt_pk_bf16_f32 v203, v82, v83\n" \
"v_mfma_f32_16x16x32_bf16 v[136:139], a[180:183], a[148:151], 0\n" \
"v_cvt_pk_bf16_f32 v204, v60, v61\n" \
"v_cvt_pk_bf16_f32 v205, v62, v63\n" \
"v_cvt_pk_bf16_f32 v206, v76, v77\n" \
"v_mfma_f32_16x16x32_bf16 v[136:139], a[244:247], a[212:215], v[136:139]\n" \
"ds_read_b64_tr_b16 a[164:165], v33 offset:32768\n" \
"v_cvt_pk_bf16_f32 v207, v78, v79\n" \
"v_mfma_f32_16x16x32_bf16 v[132:135], a[176:179], a[148:151], 0\n" \
"v_cvt_pk_bf16_f32 v208, v56, v57\n" \
"v_cvt_pk_bf16_f32 v209, v58, v59\n" \
"v_cvt_pk_bf16_f32 v210, v72, v73\n" \
"v_mfma_f32_16x16x32_bf16 v[132:135], a[240:243], a[212:215], v[132:135]\n" \
"ds_read_b128 a[148:151], v28 offset:43008\n" \
"v_cvt_pk_bf16_f32 v211, v74, v75\n" \
"v_mfma_f32_16x16x32_bf16 v[148:151], a[176:179], a[152:155], 0\n" \
"ds_read_b128 a[212:215], v29 offset:43008\n" \
"v_cvt_pk_bf16_f32 v212, v52, v53\n" \
"v_mfma_f32_16x16x32_bf16 v[148:151], a[240:243], a[216:219], v[148:151]\n" \
"s_add_u32 m0, s31, 0x0\n" \
"s_mov_b32 s84, 0x100000\n" \
"v_cvt_pk_bf16_f32 v213, v54, v55\n" \
"v_mfma_f32_16x16x32_bf16 v[152:155], a[180:183], a[152:155], 0\n" \
"buffer_load_dwordx4 v26, s[68:71], s84 offen offset:0 lds\n" \
"v_cvt_pk_bf16_f32 v214, v68, v69\n" \
"v_mfma_f32_16x16x32_bf16 v[152:155], a[244:247], a[216:219], v[152:155]\n" \
"s_addk_i32 s84, 0x7c00\n" \
"v_cvt_pk_bf16_f32 v215, v70, v71\n" \
"v_mfma_f32_16x16x32_bf16 v[156:159], a[184:187], a[152:155], 0\n" \
"buffer_load_dwordx4 v26, s[68:71], s84 offen offset:1024 lds\n" \
"v_mfma_f32_16x16x32_bf16 v[156:159], a[248:251], a[216:219], v[156:159]\n" \
"ds_read_b64_tr_b16 a[166:167], v33 offset:33280\n" \
"v_mfma_f32_16x16x32_bf16 v[160:163], a[188:191], a[152:155], 0\n" \
"ds_read_b64_tr_b16 a[228:229], v33 offset:36864\n" \
"v_mfma_f32_16x16x32_bf16 v[160:163], a[252:255], a[216:219], v[160:163]\n" \
"ds_read_b128 a[152:155], v28 offset:45056\n" \
"v_mfma_f32_16x16x32_bf16 v[176:179], a[188:191], a[156:159], 0\n" \
"ds_read_b128 a[216:219], v29 offset:45056\n" \
"v_mfma_f32_16x16x32_bf16 v[176:179], a[252:255], a[220:223], v[176:179]\n" \
"s_addk_i32 s84, 0x7c00\n" \
"v_mfma_f32_16x16x32_bf16 v[172:175], a[184:187], a[156:159], 0\n" \
"buffer_load_dwordx4 v27, s[68:71], s84 offen offset:2048 lds\n" \
"v_mfma_f32_16x16x32_bf16 v[172:175], a[248:251], a[220:223], v[172:175]\n" \
"ds_read_b64_tr_b16 a[230:231], v33 offset:37376\n" \
"v_mfma_f32_16x16x32_bf16 v[168:171], a[180:183], a[156:159], 0\n" \
"s_addk_i32 s84, 0x7c00\n" \
"v_mfma_f32_16x16x32_bf16 v[168:171], a[244:247], a[220:223], v[168:171]\n" \
"buffer_load_dwordx4 v27, s[68:71], s84 offen offset:3072 lds\n" \
"v_mfma_f32_16x16x32_bf16 v[164:167], a[176:179], a[156:159], 0\n" \
"ds_read_b64_tr_b16 a[172:173], v35 offset:32768\n" \
"v_mfma_f32_16x16x32_bf16 v[164:167], a[240:243], a[220:223], v[164:167]\n" \
"ds_read_b128 a[156:159], v28 offset:47104\n" \
"v_mfma_f32_16x16x32_bf16 v[84:87], a[176:179], a[136:139], 0\n" \
"ds_read_b128 a[220:223], v29 offset:47104\n" \
"v_mfma_f32_16x16x32_bf16 v[84:87], a[240:243], a[200:203], v[84:87]\n" \
"s_add_u32 s89, s41, 0x3000\n" \
"buffer_store_dwordx4 v[192:195], v20, s[80:83], s89 offen offset:3072 sc1\n" \
"v_mfma_f32_16x16x32_bf16 v[88:91], a[180:183], a[136:139], 0\n" \
"ds_read_b64_tr_b16 a[174:175], v35 offset:33280\n" \
"v_mfma_f32_16x16x32_bf16 v[88:91], a[244:247], a[200:203], v[88:91]\n" \
"ds_read_b64_tr_b16 a[236:237], v35 offset:36864\n" \
"v_mfma_f32_16x16x32_bf16 v[92:95], a[184:187], a[136:139], 0\n" \
"ds_read_b64_tr_b16 a[238:239], v35 offset:37376\n" \
"v_mfma_f32_16x16x32_bf16 v[92:95], a[248:251], a[200:203], v[92:95]\n" \
"s_add_u32 m0, s30, 0x0\n" \
"s_movk_i32 s84, 0x180\n" \
"v_mfma_f32_16x16x32_bf16 v[96:99], a[188:191], a[136:139], 0\n" \
"buffer_load_dwordx4 v24, s[76:79], s84 offen offset:0 lds\n" \
"v_mfma_f32_16x16x32_bf16 v[96:99], a[252:255], a[200:203], v[96:99]\n" \
"ds_read_b128 a[136:139], v28 offset:36864\n" \
"v_mfma_f32_16x16x32_bf16 v[112:115], a[188:191], a[140:143], 0\n" \
"ds_read_b128 a[200:203], v29 offset:36864\n" \
"v_mfma_f32_16x16x32_bf16 v[112:115], a[252:255], a[204:207], v[112:115]\n" \
"ds_read_b64_tr_b16 a[168:169], v34 offset:32768\n" \
"v_mfma_f32_16x16x32_bf16 v[108:111], a[184:187], a[140:143], 0\n" \
"s_movk_i32 s84, 0x2180\n" \
"s_nop 0\n" \
"v_mfma_f32_16x16x32_bf16 v[108:111], a[248:251], a[204:207], v[108:111]\n" \
"buffer_load_dwordx4 v25, s[76:79], s84 offen offset:1024 lds\n" \
"v_mfma_f32_16x16x32_bf16 v[104:107], a[180:183], a[140:143], 0\n" \
"ds_read_b64_tr_b16 a[170:171], v34 offset:33280\n" \
"v_mfma_f32_16x16x32_bf16 v[104:107], a[244:247], a[204:207], v[104:107]\n" \
"ds_read_b64_tr_b16 a[232:233], v34 offset:36864\n" \
"v_mfma_f32_16x16x32_bf16 v[100:103], a[176:179], a[140:143], 0\n" \
"v_mfma_f32_16x16x32_bf16 v[100:103], a[240:243], a[204:207], v[100:103]\n" \
"ds_read_b128 a[140:143], v28 offset:38912\n" \
"v_mfma_f32_16x16x32_bf16 v[52:55], a[176:179], a[128:131], 0\n" \
"ds_read_b128 a[204:207], v29 offset:38912\n" \
"v_mfma_f32_16x16x32_bf16 v[52:55], a[240:243], a[192:195], v[52:55]\n" \
"s_movk_i32 s84, 0x4180\n" \
"v_mfma_f32_16x16x32_bf16 v[56:59], a[180:183], a[128:131], 0\n" \
"buffer_load_dwordx4 v24, s[76:79], s84 offen offset:2048 lds\n" \
"v_mfma_f32_16x16x32_bf16 v[56:59], a[244:247], a[192:195], v[56:59]\n" \
"s_movk_i32 s84, 0x6180\n" \
"v_mfma_f32_16x16x32_bf16 v[60:63], a[184:187], a[128:131], 0\n" \
"buffer_load_dwordx4 v25, s[76:79], s84 offen offset:3072 lds\n" \
"v_mfma_f32_16x16x32_bf16 v[60:63], a[248:251], a[192:195], v[60:63]\n" \
"ds_read_b64_tr_b16 a[234:235], v34 offset:37376\n" \
"v_mfma_f32_16x16x32_bf16 v[64:67], a[188:191], a[128:131], 0\n" \
"v_mfma_f32_16x16x32_bf16 v[64:67], a[252:255], a[192:195], v[64:67]\n" \
"ds_read_b128 a[128:131], v28 offset:32768\n" \
"v_mfma_f32_16x16x32_bf16 v[80:83], a[188:191], a[132:135], 0\n" \
"ds_read_b128 a[192:195], v29 offset:32768\n" \
"v_mfma_f32_16x16x32_bf16 v[80:83], a[252:255], a[196:199], v[80:83]\n" \
"v_mfma_f32_16x16x32_bf16 v[76:79], a[184:187], a[132:135], 0\n" \
"v_mfma_f32_16x16x32_bf16 v[76:79], a[248:251], a[196:199], v[76:79]\n" \
"v_mfma_f32_16x16x32_bf16 v[72:75], a[180:183], a[132:135], 0\n" \
"v_mfma_f32_16x16x32_bf16 v[72:75], a[244:247], a[196:199], v[72:75]\n" \
"v_mfma_f32_16x16x32_bf16 v[68:71], a[176:179], a[132:135], 0\n" \
"v_mfma_f32_16x16x32_bf16 v[68:71], a[240:243], a[196:199], v[68:71]\n" \
"ds_read_b128 a[132:135], v28 offset:34816\n" \
"ds_read_b128 a[196:199], v29 offset:34816\n" \
"s_waitcnt lgkmcnt(15)\n" \
"v_mfma_f32_16x16x32_bf16 a[0:3], a[160:163], a[144:147], a[0:3]\n" \
"s_add_u32 m0, s30, 0x1000\n" \
"s_addk_i32 s84, 0x3000\n" \
"v_mfma_f32_16x16x32_bf16 a[0:3], a[224:227], a[208:211], a[0:3]\n" \
"buffer_load_dwordx4 v24, s[76:79], s84 offen offset:0 lds\n" \
"v_mfma_f32_16x16x32_bf16 a[4:7], a[164:167], a[144:147], a[4:7]\n" \
"ds_read_b64_tr_b16 a[176:177], v36 offset:32768\n" \
"v_mov_b32_e64 v11, 0\n" \
"v_mfma_f32_16x16x32_bf16 a[4:7], a[228:231], a[208:211], a[4:7]\n" \
"ds_read_b64_tr_b16 a[178:179], v36 offset:33792\n" \
"s_and_b32 s6, s2, 7\n" \
"s_waitcnt lgkmcnt(15)\n" \
"v_mfma_f32_16x16x32_bf16 a[12:15], a[172:175], a[144:147], a[12:15]\n" \
"ds_read_b64_tr_b16 a[240:241], v36 offset:40960\n" \
"s_lshl_b32 s7, s3, 1\n" \
"s_waitcnt lgkmcnt(15)\n" \
"v_mfma_f32_16x16x32_bf16 a[12:15], a[236:239], a[208:211], a[12:15]\n" \
"s_add_u32 s88, s41, 0x3000\n" \
"buffer_store_dwordx4 v[196:199], v20, s[80:83], s88 offen offset:2048 sc1\n" \
"s_waitcnt lgkmcnt(11)\n" \
"v_mfma_f32_16x16x32_bf16 a[8:11], a[144:147], a[168:171], a[8:11]\n" \
"ds_read_b64_tr_b16 a[242:243], v36 offset:41984\n" \
"s_lshr_b32 s4, s2, 5\n" \
"s_waitcnt lgkmcnt(8)\n" \
"v_mfma_f32_16x16x32_bf16 a[8:11], a[208:211], a[232:235], a[8:11]\n" \
"ds_read_b64_tr_b16 a[180:181], v37 offset:32768\n" \
"s_lshl_b32 s5, s6, 2\n" \
"v_mfma_f32_16x16x32_bf16 a[24:27], a[148:151], a[168:171], a[24:27]\n" \
"ds_read_b64_tr_b16 a[182:183], v37 offset:33792\n" \
"s_bfe_u32 s8, s2, 0x20003\n" \
"v_mfma_f32_16x16x32_bf16 a[24:27], a[212:215], a[232:235], a[24:27]\n" \
"ds_read_b64_tr_b16 a[244:245], v37 offset:40960\n" \
"s_and_b32 s7, s7, 4\n" \
"v_mfma_f32_16x16x32_bf16 a[28:31], a[172:175], a[148:151], a[28:31]\n" \
"s_and_b32 s9, s3, 1\n" \
"s_or_b32 s13, s5, s8\n" \
"v_mfma_f32_16x16x32_bf16 a[28:31], a[236:239], a[212:215], a[28:31]\n" \
"s_addk_i32 s84, 0x2000\n" \
"v_mfma_f32_16x16x32_bf16 a[20:23], a[164:167], a[148:151], a[20:23]\n" \
"buffer_load_dwordx4 v25, s[76:79], s84 offen offset:1024 lds\n" \
"v_mfma_f32_16x16x32_bf16 a[20:23], a[228:231], a[212:215], a[20:23]\n" \
"ds_read_b64_tr_b16 a[246:247], v37 offset:41984\n" \
"s_or_b32 s9, s7, s9\n" \
"v_mfma_f32_16x16x32_bf16 a[16:19], a[160:163], a[148:151], a[16:19]\n" \
"ds_read_b64_tr_b16 a[184:185], v38 offset:32768\n" \
"s_mul_i32 s7, s4, 0xa00\n" \
"v_mfma_f32_16x16x32_bf16 a[16:19], a[224:227], a[212:215], a[16:19]\n" \
"ds_read_b64_tr_b16 a[186:187], v38 offset:33792\n" \
"s_add_i32 s7, s7, 0x5bc1000\n" \
"v_mfma_f32_16x16x32_bf16 a[32:35], a[160:163], a[152:155], a[32:35]\n" \
"s_addk_i32 s84, 0x2000\n" \
"v_mfma_f32_16x16x32_bf16 a[32:35], a[224:227], a[216:219], a[32:35]\n" \
"buffer_load_dwordx4 v24, s[76:79], s84 offen offset:2048 lds\n" \
"v_mfma_f32_16x16x32_bf16 a[36:39], a[164:167], a[152:155], a[36:39]\n" \
"ds_read_b64_tr_b16 a[248:249], v38 offset:40960\n" \
"s_lshl_b32 s10, s13, 2\n" \
"v_mfma_f32_16x16x32_bf16 a[36:39], a[228:231], a[216:219], a[36:39]\n" \
"ds_read_b64_tr_b16 a[250:251], v38 offset:41984\n" \
"s_or_b32 s10, s10, s7\n" \
"v_mfma_f32_16x16x32_bf16 a[44:47], a[172:175], a[152:155], a[44:47]\n" \
"ds_read_b64_tr_b16 a[188:189], v39 offset:32768\n" \
"v_writelane_b32 v11, s10, 0\n" \
"v_mfma_f32_16x16x32_bf16 a[44:47], a[236:239], a[216:219], a[44:47]\n" \
"ds_read_b64_tr_b16 a[190:191], v39 offset:33792\n" \
"s_lshl_b32 s14, s4, 3\n" \
"v_mfma_f32_16x16x32_bf16 a[40:43], a[152:155], a[168:171], a[40:43]\n" \
"s_add_u32 s89, s41, 0x3000\n" \
"buffer_store_dwordx4 a[84:87], v20, s[80:83], s89 offen offset:1024 sc1\n" \
"v_mfma_f32_16x16x32_bf16 a[40:43], a[216:219], a[232:235], a[40:43]\n" \
"ds_read_b64_tr_b16 a[252:253], v39 offset:40960\n" \
"v_writelane_b32 v11, s7, 1\n" \
"v_mfma_f32_16x16x32_bf16 a[56:59], a[156:159], a[168:171], a[56:59]\n" \
"ds_read_b64_tr_b16 a[254:255], v39 offset:41984\n" \
"s_or_b32 s7, s14, s6\n" \
"v_mfma_f32_16x16x32_bf16 a[56:59], a[220:223], a[232:235], a[56:59]\n" \
"s_lshl_b32 s7, s7, 10\n" \
"s_lshl_b32 s10, s3, 8\n" \
"v_mfma_f32_16x16x32_bf16 a[60:63], a[172:175], a[156:159], a[60:63]\n" \
"s_or_b32 s7, s7, s10\n" \
"s_or_b32 s10, s7, 0x5c00200\n" \
"v_mfma_f32_16x16x32_bf16 a[60:63], a[236:239], a[220:223], a[60:63]\n" \
"s_addk_i32 s84, 0x2000\n" \
"v_mfma_f32_16x16x32_bf16 a[52:55], a[164:167], a[156:159], a[52:55]\n" \
"buffer_load_dwordx4 v25, s[76:79], s84 offen offset:3072 lds\n" \
"v_mfma_f32_16x16x32_bf16 a[52:55], a[228:231], a[220:223], a[52:55]\n" \
"v_writelane_b32 v11, s10, 2\n" \
"s_lshl_b32 s10, s8, 2\n" \
"v_mfma_f32_16x16x32_bf16 a[48:51], a[160:163], a[156:159], a[48:51]\n" \
"s_or_b32 s11, s10, s7\n" \
"s_or_b32 s11, s11, 0x5c00000\n" \
"v_mfma_f32_16x16x32_bf16 a[48:51], a[224:227], a[220:223], a[48:51]\n" \
"v_writelane_b32 v11, s11, 3\n" \
"s_or_b32 s7, s7, 0x5c00080\n" \
"v_mfma_f32_16x16x32_bf16 a[64:67], a[164:167], a[140:143], a[64:67]\n" \
"v_writelane_b32 v11, s7, 4\n" \
"s_or_b32 s7, s7, s10\n" \
"v_mfma_f32_16x16x32_bf16 a[64:67], a[228:231], a[204:207], a[64:67]\n" \
"s_add_i32 s10, s3, s2\n" \
"v_writelane_b32 v11, s7, 5\n" \
"v_mfma_f32_16x16x32_bf16 a[80:83], a[172:175], a[140:143], a[80:83]\n" \
"s_mov_b32 s7, 2.0\n" \
"s_mov_b32 s11, 0x5bc0000\n" \
"v_mfma_f32_16x16x32_bf16 a[80:83], a[236:239], a[204:207], a[80:83]\n" \
"s_cmp_lg_u32 s10, 0\n" \
"s_cselect_b32 s10, s7, s11\n" \
"v_mfma_f32_16x16x32_bf16 a[68:71], a[140:143], a[168:171], a[68:71]\n" \
"s_lshr_b32 s15, s2, 3\n" \
"v_writelane_b32 v11, s10, 54\n" \
"v_mfma_f32_16x16x32_bf16 a[68:71], a[204:207], a[232:235], a[68:71]\n" \
"s_waitcnt vmcnt(11) lgkmcnt(0)\n" \
"s_barrier\n" \
"v_xor_b32_e32 v28, 0x20000, v28\n" \
"v_xor_b32_e32 v29, 0x20000, v29\n" \
"v_mfma_f32_16x16x32_bf16 v[116:119], a[176:179], a[144:147], v[116:119]\n" \
"s_add_u32 m0, s40, 0x8000\n" \
"s_add_u32 s85, s39, 0x6000\n" \
"v_mfma_f32_16x16x32_bf16 v[116:119], a[240:243], a[208:211], v[116:119]\n" \
"buffer_load_dwordx4 v20, s[80:83], s85 offen sc1 lds\n" \
"v_mfma_f32_16x16x32_bf16 v[120:123], a[180:183], a[144:147], v[120:123]\n" \
"ds_read_b64_tr_b16 a[160:161], v32 offset:0\n" \
"s_lshl_b32 s10, s6, 5\n" \
"v_mfma_f32_16x16x32_bf16 v[120:123], a[244:247], a[208:211], v[120:123]\n" \
"ds_read_b64_tr_b16 a[162:163], v32 offset:512\n" \
"s_min_u32 s16, s3, 2\n" \
"v_mfma_f32_16x16x32_bf16 v[124:127], a[184:187], a[144:147], v[124:127]\n" \
"s_add_u32 m0, s40, 0x9000\n" \
"s_add_u32 s85, s39, 0x7000\n" \
"v_mfma_f32_16x16x32_bf16 v[124:127], a[248:251], a[208:211], v[124:127]\n" \
"buffer_load_dwordx4 v20, s[80:83], s85 offen sc1 lds\n" \
"v_mfma_f32_16x16x32_bf16 v[128:131], a[188:191], a[144:147], v[128:131]\n" \
"ds_read_b64_tr_b16 a[224:225], v32 offset:4096\n" \
"s_mul_i32 s12, s8, 0x120\n" \
"v_mfma_f32_16x16x32_bf16 v[128:131], a[252:255], a[208:211], v[128:131]\n" \
"ds_read_b128 a[144:147], v28 offset:8192\n" \
"v_mfma_f32_16x16x32_bf16 v[144:147], a[188:191], a[148:151], v[144:147]\n" \
"ds_read_b128 a[208:211], v29 offset:8192\n" \
"v_mfma_f32_16x16x32_bf16 v[144:147], a[252:255], a[212:215], v[144:147]\n" \
"s_add_u32 m0, s40, 0xb000\n" \
"s_movk_i32 s84, 0x180\n" \
"v_mfma_f32_16x16x32_bf16 v[140:143], a[184:187], a[148:151], v[140:143]\n" \
"buffer_load_dwordx4 v21, s[76:79], s84 offen offset:0 lds\n" \
"v_mfma_f32_16x16x32_bf16 v[140:143], a[248:251], a[212:215], v[140:143]\n" \
"ds_read_b64_tr_b16 a[226:227], v32 offset:4608\n" \
"v_mfma_f32_16x16x32_bf16 v[136:139], a[180:183], a[148:151], v[136:139]\n" \
"s_add_u32 s88, s41, 0x3000\n" \
"buffer_store_dwordx4 v[252:255], v20, s[80:83], s88 offen offset:0 sc1\n" \
"v_mfma_f32_16x16x32_bf16 v[136:139], a[244:247], a[212:215], v[136:139]\n" \
"ds_read_b64_tr_b16 a[164:165], v33 offset:0\n" \
"v_mfma_f32_16x16x32_bf16 v[132:135], a[176:179], a[148:151], v[132:135]\n" \
"s_add_u32 s11, s22, s12\n" \
"s_addc_u32 s17, s23, 0\n" \
"s_nop 0\n" \
"v_mfma_f32_16x16x32_bf16 v[132:135], a[240:243], a[212:215], v[132:135]\n" \
"ds_read_b128 a[148:151], v28 offset:10240\n" \
"v_mfma_f32_16x16x32_bf16 v[148:151], a[176:179], a[152:155], v[148:151]\n" \
"ds_read_b128 a[212:215], v29 offset:10240\n" \
"v_mfma_f32_16x16x32_bf16 v[148:151], a[240:243], a[216:219], v[148:151]\n" \
"s_add_u32 m0, s31, 0x8000\n" \
"s_mov_b32 s84, 0x180000\n" \
"v_mfma_f32_16x16x32_bf16 v[152:155], a[180:183], a[152:155], v[152:155]\n" \
"buffer_load_dwordx4 v26, s[68:71], s84 offen offset:0 lds\n" \
"v_mfma_f32_16x16x32_bf16 v[152:155], a[244:247], a[216:219], v[152:155]\n" \
"ds_read_b64_tr_b16 a[166:167], v33 offset:512\n" \
"v_mfma_f32_16x16x32_bf16 v[156:159], a[184:187], a[152:155], v[156:159]\n" \
"ds_read_b64_tr_b16 a[228:229], v33 offset:4096\n" \
"v_mfma_f32_16x16x32_bf16 v[156:159], a[248:251], a[216:219], v[156:159]\n" \
"s_addk_i32 s84, 0x7c00\n" \
"v_mfma_f32_16x16x32_bf16 v[160:163], a[188:191], a[152:155], v[160:163]\n" \
"buffer_load_dwordx4 v26, s[68:71], s84 offen offset:1024 lds\n" \
"v_mfma_f32_16x16x32_bf16 v[160:163], a[252:255], a[216:219], v[160:163]\n" \
"ds_read_b128 a[152:155], v28 offset:12288\n" \
"v_mfma_f32_16x16x32_bf16 v[176:179], a[188:191], a[156:159], v[176:179]\n" \
"ds_read_b128 a[216:219], v29 offset:12288\n" \
"v_mfma_f32_16x16x32_bf16 v[176:179], a[252:255], a[220:223], v[176:179]\n" \
"s_addk_i32 s84, 0x7c00\n" \
"v_mfma_f32_16x16x32_bf16 v[172:175], a[184:187], a[156:159], v[172:175]\n" \
"buffer_load_dwordx4 v27, s[68:71], s84 offen offset:2048 lds\n" \
"v_mfma_f32_16x16x32_bf16 v[172:175], a[248:251], a[220:223], v[172:175]\n" \
"ds_read_b64_tr_b16 a[230:231], v33 offset:4608\n" \
"v_mfma_f32_16x16x32_bf16 v[168:171], a[180:183], a[156:159], v[168:171]\n" \
"ds_read_b64_tr_b16 a[172:173], v35 offset:0\n" \
"v_mfma_f32_16x16x32_bf16 v[168:171], a[244:247], a[220:223], v[168:171]\n" \
"ds_read_b64_tr_b16 a[174:175], v35 offset:512\n" \
"v_mfma_f32_16x16x32_bf16 v[164:167], a[176:179], a[156:159], v[164:167]\n" \
"s_add_u32 s18, s24, s12\n" \
"v_writelane_b32 v11, s11, 9\n" \
"s_addc_u32 s19, s25, 0\n" \
"v_mfma_f32_16x16x32_bf16 v[164:167], a[240:243], a[220:223], v[164:167]\n" \
"ds_read_b128 a[156:159], v28 offset:14336\n" \
"s_and_b32 s20, s17, 0xffff\n" \
"v_mfma_f32_16x16x32_bf16 v[84:87], a[176:179], a[136:139], v[84:87]\n" \
"ds_read_b128 a[220:223], v29 offset:14336\n" \
"v_writelane_b32 v11, s20, 10\n" \
"v_mfma_f32_16x16x32_bf16 v[84:87], a[240:243], a[200:203], v[84:87]\n" \
"s_addk_i32 s84, 0x7c00\n" \
"v_mfma_f32_16x16x32_bf16 v[88:91], a[180:183], a[136:139], v[88:91]\n" \
"buffer_load_dwordx4 v27, s[68:71], s84 offen offset:3072 lds\n" \
"v_mfma_f32_16x16x32_bf16 v[88:91], a[244:247], a[200:203], v[88:91]\n" \
"s_add_u32 m0, s30, 0x8000\n" \
"s_movk_i32 s84, 0x200\n" \
"v_mfma_f32_16x16x32_bf16 v[92:95], a[184:187], a[136:139], v[92:95]\n" \
"buffer_load_dwordx4 v24, s[76:79], s84 offen offset:0 lds\n" \
"v_mfma_f32_16x16x32_bf16 v[92:95], a[248:251], a[200:203], v[92:95]\n" \
"ds_read_b64_tr_b16 a[236:237], v35 offset:4096\n" \
"s_and_b32 s20, s19, 0xffff\n" \
"v_mfma_f32_16x16x32_bf16 v[96:99], a[188:191], a[136:139], v[96:99]\n" \
"ds_read_b64_tr_b16 a[238:239], v35 offset:4608\n" \
"v_writelane_b32 v11, s18, 11\n" \
"v_mfma_f32_16x16x32_bf16 v[96:99], a[252:255], a[200:203], v[96:99]\n" \
"ds_read_b128 a[136:139], v28 offset:4096\n" \
"v_mfma_f32_16x16x32_bf16 v[112:115], a[188:191], a[140:143], v[112:115]\n" \
"ds_read_b128 a[200:203], v29 offset:4096\n" \
"v_mfma_f32_16x16x32_bf16 v[112:115], a[252:255], a[204:207], v[112:115]\n" \
"s_movk_i32 s84, 0x2200\n" \
"s_nop 0\n" \
"v_mfma_f32_16x16x32_bf16 v[108:111], a[184:187], a[140:143], v[108:111]\n" \
"buffer_load_dwordx4 v25, s[76:79], s84 offen offset:1024 lds\n" \
"v_mfma_f32_16x16x32_bf16 v[108:111], a[248:251], a[204:207], v[108:111]\n" \
"s_add_u32 s89, s41, 0x1000\n" \
"buffer_store_dwordx4 v[200:203], v20, s[80:83], s89 offen offset:3072 sc1\n" \
"v_mfma_f32_16x16x32_bf16 v[104:107], a[180:183], a[140:143], v[104:107]\n" \
"ds_read_b64_tr_b16 a[168:169], v34 offset:0\n" \
"v_mfma_f32_16x16x32_bf16 v[104:107], a[244:247], a[204:207], v[104:107]\n" \
"ds_read_b64_tr_b16 a[170:171], v34 offset:512\n" \
"v_mfma_f32_16x16x32_bf16 v[100:103], a[176:179], a[140:143], v[100:103]\n" \
"ds_read_b64_tr_b16 a[232:233], v34 offset:4096\n" \
"v_mfma_f32_16x16x32_bf16 v[100:103], a[240:243], a[204:207], v[100:103]\n" \
"ds_read_b128 a[140:143], v28 offset:6144\n" \
"v_mfma_f32_16x16x32_bf16 v[52:55], a[176:179], a[128:131], v[52:55]\n" \
"ds_read_b128 a[204:207], v29 offset:6144\n" \
"v_mfma_f32_16x16x32_bf16 v[52:55], a[240:243], a[192:195], v[52:55]\n" \
"s_movk_i32 s84, 0x4200\n" \
"v_mfma_f32_16x16x32_bf16 v[56:59], a[180:183], a[128:131], v[56:59]\n" \
"buffer_load_dwordx4 v24, s[76:79], s84 offen offset:2048 lds\n" \
"v_mfma_f32_16x16x32_bf16 v[56:59], a[244:247], a[192:195], v[56:59]\n" \
"s_movk_i32 s84, 0x6200\n" \
"v_mfma_f32_16x16x32_bf16 v[60:63], a[184:187], a[128:131], v[60:63]\n" \
"buffer_load_dwordx4 v25, s[76:79], s84 offen offset:3072 lds\n" \
"v_mfma_f32_16x16x32_bf16 v[60:63], a[248:251], a[192:195], v[60:63]\n" \
"ds_read_b64_tr_b16 a[234:235], v34 offset:4608\n" \
"v_mfma_f32_16x16x32_bf16 v[64:67], a[188:191], a[128:131], v[64:67]\n" \
"s_cmpk_lt_u32 s3, 3\n" \
"s_cselect_b32 s11, s11, s18\n" \
"v_mfma_f32_16x16x32_bf16 v[64:67], a[252:255], a[192:195], v[64:67]\n" \
"ds_read_b128 a[128:131], v28 offset:0\n" \
"s_movk_i32 s18, 0xfff\n" \
"v_mfma_f32_16x16x32_bf16 v[80:83], a[188:191], a[132:135], v[80:83]\n" \
"ds_read_b128 a[192:195], v29 offset:0\n" \
"v_writelane_b32 v11, s20, 12\n" \
"v_mfma_f32_16x16x32_bf16 v[80:83], a[252:255], a[196:199], v[80:83]\n" \
"s_and_b32 s20, s29, 0xffff\n" \
"v_writelane_b32 v11, s28, 13\n" \
"v_mfma_f32_16x16x32_bf16 v[76:79], a[184:187], a[132:135], v[76:79]\n" \
"v_writelane_b32 v11, s20, 14\n" \
"v_writelane_b32 v11, s11, 15\n" \
"v_mfma_f32_16x16x32_bf16 v[76:79], a[248:251], a[196:199], v[76:79]\n" \
"s_cmpk_lt_u32 s3, 3\n" \
"s_cselect_b32 s11, s17, s19\n" \
"v_mfma_f32_16x16x32_bf16 v[72:75], a[180:183], a[132:135], v[72:75]\n" \
"s_and_b32 s11, s11, 0xffff\n" \
"v_writelane_b32 v11, s11, 16\n" \
"v_mfma_f32_16x16x32_bf16 v[72:75], a[244:247], a[196:199], v[72:75]\n" \
"s_mul_i32 s11, s6, 0x48\n" \
"s_add_i32 s17, s11, 0x47\n" \
"v_mfma_f32_16x16x32_bf16 v[68:71], a[176:179], a[132:135], v[68:71]\n" \
"s_cmpk_lt_u32 s3, 3\n" \
"s_cselect_b32 s17, s17, s18\n" \
"v_mfma_f32_16x16x32_bf16 v[68:71], a[240:243], a[196:199], v[68:71]\n" \
"ds_read_b128 a[132:135], v28 offset:2048\n" \
"ds_read_b128 a[196:199], v29 offset:2048\n" \
"s_waitcnt lgkmcnt(15)\n" \
"v_mfma_f32_16x16x32_bf16 a[0:3], a[160:163], a[144:147], a[0:3]\n" \
"s_add_u32 m0, s30, 0x9000\n" \
"s_addk_i32 s84, 0x3000\n" \
"v_mfma_f32_16x16x32_bf16 a[0:3], a[224:227], a[208:211], a[0:3]\n" \
"buffer_load_dwordx4 v24, s[76:79], s84 offen offset:0 lds\n" \
"v_mfma_f32_16x16x32_bf16 a[4:7], a[164:167], a[144:147], a[4:7]\n" \
"ds_read_b64_tr_b16 a[176:177], v36 offset:0\n" \
"s_mulk_i32 s17, 0x480\n" \
"v_mfma_f32_16x16x32_bf16 a[4:7], a[228:231], a[208:211], a[4:7]\n" \
"ds_read_b64_tr_b16 a[178:179], v36 offset:1024\n" \
"s_addk_i32 s17, 0x120\n" \
"v_mfma_f32_16x16x32_bf16 a[12:15], a[172:175], a[144:147], a[12:15]\n" \
"ds_read_b64_tr_b16 a[240:241], v36 offset:8192\n" \
"v_writelane_b32 v11, s17, 17\n" \
"s_waitcnt lgkmcnt(15)\n" \
"v_mfma_f32_16x16x32_bf16 a[12:15], a[236:239], a[208:211], a[12:15]\n" \
"s_add_u32 s88, s41, 0x1000\n" \
"buffer_store_dwordx4 v[204:207], v20, s[80:83], s88 offen offset:2048 sc1\n" \
"s_waitcnt lgkmcnt(11)\n" \
"v_mfma_f32_16x16x32_bf16 a[8:11], a[144:147], a[168:171], a[8:11]\n" \
"ds_read_b64_tr_b16 a[242:243], v36 offset:9216\n" \
"s_lshl_b32 s17, s3, 5\n" \
"s_waitcnt lgkmcnt(8)\n" \
"v_mfma_f32_16x16x32_bf16 a[8:11], a[208:211], a[232:235], a[8:11]\n" \
"ds_read_b64_tr_b16 a[180:181], v37 offset:0\n" \
"s_add_i32 s11, s11, s17\n" \
"v_mfma_f32_16x16x32_bf16 a[24:27], a[148:151], a[168:171], a[24:27]\n" \
"ds_read_b64_tr_b16 a[182:183], v37 offset:1024\n" \
"s_mul_i32 s17, s11, 0x480\n" \
"v_mfma_f32_16x16x32_bf16 a[24:27], a[212:215], a[232:235], a[24:27]\n" \
"ds_read_b64_tr_b16 a[244:245], v37 offset:8192\n" \
"s_mul_i32 s11, s13, 0x24000\n" \
"v_mfma_f32_16x16x32_bf16 a[28:31], a[172:175], a[148:151], a[28:31]\n" \
"s_add_i32 s18, s11, 0x1b000\n" \
"v_mfma_f32_16x16x32_bf16 a[28:31], a[236:239], a[212:215], a[28:31]\n" \
"s_addk_i32 s84, 0x2000\n" \
"v_mfma_f32_16x16x32_bf16 a[20:23], a[164:167], a[148:151], a[20:23]\n" \
"buffer_load_dwordx4 v25, s[76:79], s84 offen offset:1024 lds\n" \
"v_mfma_f32_16x16x32_bf16 a[20:23], a[228:231], a[212:215], a[20:23]\n" \
"ds_read_b64_tr_b16 a[246:247], v37 offset:9216\n" \
"v_mfma_f32_16x16x32_bf16 a[16:19], a[160:163], a[148:151], a[16:19]\n" \
"ds_read_b64_tr_b16 a[184:185], v38 offset:0\n" \
"v_mfma_f32_16x16x32_bf16 a[16:19], a[224:227], a[212:215], a[16:19]\n" \
"ds_read_b64_tr_b16 a[186:187], v38 offset:1024\n" \
"v_mfma_f32_16x16x32_bf16 a[32:35], a[160:163], a[152:155], a[32:35]\n" \
"s_addk_i32 s84, 0x2000\n" \
"v_mfma_f32_16x16x32_bf16 a[32:35], a[224:227], a[216:219], a[32:35]\n" \
"buffer_load_dwordx4 v24, s[76:79], s84 offen offset:2048 lds\n" \
"v_mfma_f32_16x16x32_bf16 a[36:39], a[164:167], a[152:155], a[36:39]\n" \
"ds_read_b64_tr_b16 a[248:249], v38 offset:8192\n" \
"v_mfma_f32_16x16x32_bf16 a[36:39], a[228:231], a[216:219], a[36:39]\n" \
"ds_read_b64_tr_b16 a[250:251], v38 offset:9216\n" \
"v_mfma_f32_16x16x32_bf16 a[44:47], a[172:175], a[152:155], a[44:47]\n" \
".fill (L_pb + 65248 - .) / 4, 4, 0xBF800000\n" \
"L_pp16:\n" \
"s_cbranch_execz L_pret\n" \
"s_nop 0\n" \
"ds_read_b64_tr_b16 a[188:189], v39 offset:0\n" \
"v_mfma_f32_16x16x32_bf16 a[44:47], a[236:239], a[216:219], a[44:47]\n" \
"ds_read_b64_tr_b16 a[190:191], v39 offset:1024\n" \
"v_mfma_f32_16x16x32_bf16 a[40:43], a[152:155], a[168:171], a[40:43]\n" \
"s_add_u32 s89, s41, 0x1000\n" \
"buffer_store_dwordx4 v[208:211], v20, s[80:83], s89 offen offset:1024 sc1\n" \
"v_mfma_f32_16x16x32_bf16 a[40:43], a[216:219], a[232:235], a[40:43]\n" \
"ds_read_b64_tr_b16 a[252:253], v39 offset:8192\n" \
"v_mfma_f32_16x16x32_bf16 a[56:59], a[156:159], a[168:171], a[56:59]\n" \
"ds_read_b64_tr_b16 a[254:255], v39 offset:9216\n" \
"v_mfma_f32_16x16x32_bf16 a[56:59], a[220:223], a[232:235], a[56:59]\n" \
"s_cmpk_lt_u32 s3, 3\n" \
"s_cselect_b32 s17, s17, s18\n" \
"v_mfma_f32_16x16x32_bf16 a[60:63], a[172:175], a[156:159], a[60:63]\n" \
"s_mov_b32 s18, 0x8000\n" \
"v_writelane_b32 v11, s17, 18\n" \
"v_mfma_f32_16x16x32_bf16 a[60:63], a[236:239], a[220:223], a[60:63]\n" \
"s_addk_i32 s84, 0x2000\n" \
"v_mfma_f32_16x16x32_bf16 a[52:55], a[164:167], a[156:159], a[52:55]\n" \
"buffer_load_dwordx4 v25, s[76:79], s84 offen offset:3072 lds\n" \
"v_mfma_f32_16x16x32_bf16 a[52:55], a[228:231], a[220:223], a[52:55]\n" \
"s_mul_i32 s17, s16, 0x2400\n" \
"s_mul_i32 s16, s16, 0x9000\n" \
"v_mfma_f32_16x16x32_bf16 a[48:51], a[160:163], a[156:159], a[48:51]\n" \
"s_bitset1_b32 s17, 17\n" \
"v_writelane_b32 v11, s17, 19\n" \
"v_mfma_f32_16x16x32_bf16 a[48:51], a[224:227], a[220:223], a[48:51]\n" \
"s_add_i32 s16, s11, s16\n" \
"v_writelane_b32 v11, s16, 26\n" \
"v_mfma_f32_16x16x32_bf16 a[64:67], a[164:167], a[140:143], a[64:67]\n" \
"s_mul_i32 s16, s3, 0x2400\n" \
"s_add_i32 s17, s16, 0xa400\n" \
"v_mfma_f32_16x16x32_bf16 a[64:67], a[228:231], a[204:207], a[64:67]\n" \
"s_cmpk_lt_u32 s3, 3\n" \
"s_cselect_b32 s17, s17, s18\n" \
"v_mfma_f32_16x16x32_bf16 a[80:83], a[172:175], a[140:143], a[80:83]\n" \
"s_mov_b32 s18, 0x1b000\n" \
"s_xor_b32 s19, s3, 2\n" \
"v_mfma_f32_16x16x32_bf16 a[80:83], a[236:239], a[204:207], a[80:83]\n" \
"s_cmp_lg_u32 s19, 0\n" \
"s_cselect_b32 s18, s17, s18\n" \
"v_mfma_f32_16x16x32_bf16 a[68:71], a[140:143], a[168:171], a[68:71]\n" \
"v_writelane_b32 v11, s17, 20\n" \
"s_or_b32 s17, s16, 0x10000\n" \
"v_mfma_f32_16x16x32_bf16 a[68:71], a[204:207], a[232:235], a[68:71]\n" \
"s_waitcnt vmcnt(11) lgkmcnt(0)\n" \
"s_barrier\n" \
"v_xor_b32_e32 v28, 0x20000, v28\n" \
"v_xor_b32_e32 v29, 0x20000, v29\n" \
"v_mfma_f32_16x16x32_bf16 v[116:119], a[176:179], a[144:147], v[116:119]\n" \
"s_add_u32 m0, s40, 0x0\n" \
"s_nop 0\n" \
"s_add_u32 s85, s39, 0x8000\n" \
"v_mfma_f32_16x16x32_bf16 v[116:119], a[240:243], a[208:211], v[116:119]\n" \
"buffer_load_dwordx4 v20, s[80:83], s85 offen sc1 lds\n" \
"v_mfma_f32_16x16x32_bf16 v[120:123], a[180:183], a[144:147], v[120:123]\n" \
"ds_read_b64_tr_b16 a[160:161], v32 offset:32768\n" \
"v_writelane_b32 v11, s18, 21\n" \
"v_mfma_f32_16x16x32_bf16 v[120:123], a[244:247], a[208:211], v[120:123]\n" \
"ds_read_b64_tr_b16 a[162:163], v32 offset:33280\n" \
"s_add_i32 s18, s16, 0x19000\n" \
"v_mfma_f32_16x16x32_bf16 v[124:127], a[184:187], a[144:147], v[124:127]\n" \
"s_add_u32 m0, s40, 0x1000\n" \
"s_add_u32 s85, s39, 0x9000\n" \
"v_mfma_f32_16x16x32_bf16 v[124:127], a[248:251], a[208:211], v[124:127]\n" \
"buffer_load_dwordx4 v20, s[80:83], s85 offen sc1 lds\n" \
"v_mfma_f32_16x16x32_bf16 v[128:131], a[188:191], a[144:147], v[128:131]\n" \
"ds_read_b64_tr_b16 a[224:225], v32 offset:36864\n" \
"v_writelane_b32 v11, s17, 22\n" \
"v_mfma_f32_16x16x32_bf16 v[128:131], a[252:255], a[208:211], v[128:131]\n" \
"ds_read_b128 a[144:147], v28 offset:8192\n" \
"s_mov_b32 s17, 0\n" \
"v_mfma_f32_16x16x32_bf16 v[144:147], a[188:191], a[148:151], v[144:147]\n" \
"ds_read_b128 a[208:211], v29 offset:8192\n" \
"v_mfma_f32_16x16x32_bf16 v[144:147], a[252:255], a[212:215], v[144:147]\n" \
"s_add_u32 m0, s40, 0x3000\n" \
"s_movk_i32 s84, 0x200\n" \
"v_mfma_f32_16x16x32_bf16 v[140:143], a[184:187], a[148:151], v[140:143]\n" \
"buffer_load_dwordx4 v21, s[76:79], s84 offen offset:0 lds\n" \
"v_mfma_f32_16x16x32_bf16 v[140:143], a[248:251], a[212:215], v[140:143]\n" \
"ds_read_b64_tr_b16 a[226:227], v32 offset:37376\n" \
"v_mfma_f32_16x16x32_bf16 v[136:139], a[180:183], a[148:151], v[136:139]\n" \
"s_add_u32 s88, s41, 0x1000\n" \
"buffer_store_dwordx4 v[212:215], v20, s[80:83], s88 offen offset:0 sc1\n" \
"v_mfma_f32_16x16x32_bf16 v[136:139], a[244:247], a[212:215], v[136:139]\n" \
"ds_read_b64_tr_b16 a[164:165], v33 offset:32768\n" \
"v_mfma_f32_16x16x32_bf16 v[132:135], a[176:179], a[148:151], v[132:135]\n" \
"s_cmpk_lt_u32 s3, 3\n" \
"s_cselect_b32 s18, s18, s17\n" \
"v_mfma_f32_16x16x32_bf16 v[132:135], a[240:243], a[212:215], v[132:135]\n" \
"ds_read_b128 a[148:151], v28 offset:10240\n" \
"s_bitset1_b32 s16, 17\n" \
"v_mfma_f32_16x16x32_bf16 v[148:151], a[176:179], a[152:155], v[148:151]\n" \
"ds_read_b128 a[212:215], v29 offset:10240\n" \
"v_writelane_b32 v11, s18, 23\n" \
"v_mfma_f32_16x16x32_bf16 v[148:151], a[240:243], a[216:219], v[148:151]\n" \
"s_add_u32 m0, s31, 0x0\n" \
"s_mov_b32 s84, 0x200000\n" \
"v_mfma_f32_16x16x32_bf16 v[152:155], a[180:183], a[152:155], v[152:155]\n" \
"buffer_load_dwordx4 v26, s[68:71], s84 offen offset:0 lds\n" \
"v_mfma_f32_16x16x32_bf16 v[152:155], a[244:247], a[216:219], v[152:155]\n" \
"ds_read_b64_tr_b16 a[166:167], v33 offset:33280\n" \
"s_movk_i32 s18, 0x2400\n" \
"v_mfma_f32_16x16x32_bf16 v[156:159], a[184:187], a[152:155], v[156:159]\n" \
"ds_read_b64_tr_b16 a[228:229], v33 offset:36864\n" \
"v_mfma_f32_16x16x32_bf16 v[156:159], a[248:251], a[216:219], v[156:159]\n" \
"s_addk_i32 s84, 0x7c00\n" \
"v_mfma_f32_16x16x32_bf16 v[160:163], a[188:191], a[152:155], v[160:163]\n" \
"buffer_load_dwordx4 v26, s[68:71], s84 offen offset:1024 lds\n" \
"v_mfma_f32_16x16x32_bf16 v[160:163], a[252:255], a[216:219], v[160:163]\n" \
"ds_read_b128 a[152:155], v28 offset:12288\n" \
"v_mfma_f32_16x16x32_bf16 v[176:179], a[188:191], a[156:159], v[176:179]\n" \
"ds_read_b128 a[216:219], v29 offset:12288\n" \
"v_mfma_f32_16x16x32_bf16 v[176:179], a[252:255], a[220:223], v[176:179]\n" \
"s_addk_i32 s84, 0x7c00\n" \
"v_mfma_f32_16x16x32_bf16 v[172:175], a[184:187], a[156:159], v[172:175]\n" \
"buffer_load_dwordx4 v27, s[68:71], s84 offen offset:2048 lds\n" \
"v_mfma_f32_16x16x32_bf16 v[172:175], a[248:251], a[220:223], v[172:175]\n" \
"ds_read_b64_tr_b16 a[230:231], v33 offset:37376\n" \
"v_mfma_f32_16x16x32_bf16 v[168:171], a[180:183], a[156:159], v[168:171]\n" \
"ds_read_b64_tr_b16 a[172:173], v35 offset:32768\n" \
"v_mfma_f32_16x16x32_bf16 v[168:171], a[244:247], a[220:223], v[168:171]\n" \
"ds_read_b64_tr_b16 a[174:175], v35 offset:33280\n" \
"v_mfma_f32_16x16x32_bf16 v[164:167], a[176:179], a[156:159], v[164:167]\n" \
"s_cmpk_lt_u32 s3, 3\n" \
"s_cselect_b32 s16, s16, s18\n" \
"v_mfma_f32_16x16x32_bf16 v[164:167], a[240:243], a[220:223], v[164:167]\n" \
"ds_read_b128 a[156:159], v28 offset:14336\n" \
"s_mul_i32 s19, s3, 0x9000\n" \
"v_mfma_f32_16x16x32_bf16 v[84:87], a[176:179], a[136:139], v[84:87]\n" \
"ds_read_b128 a[220:223], v29 offset:14336\n" \
"v_writelane_b32 v11, s16, 24\n" \
"v_mfma_f32_16x16x32_bf16 v[84:87], a[240:243], a[200:203], v[84:87]\n" \
"s_addk_i32 s84, 0x7c00\n" \
"v_mfma_f32_16x16x32_bf16 v[88:91], a[180:183], a[136:139], v[88:91]\n" \
"buffer_load_dwordx4 v27, s[68:71], s84 offen offset:3072 lds\n" \
"v_mfma_f32_16x16x32_bf16 v[88:91], a[244:247], a[200:203], v[88:91]\n" \
"s_add_u32 m0, s30, 0x20000\n" \
"s_movk_i32 s84, 0x280\n" \
"v_mfma_f32_16x16x32_bf16 v[92:95], a[184:187], a[136:139], v[92:95]\n" \
"buffer_load_dwordx4 v24, s[76:79], s84 offen offset:0 lds\n" \
"v_mfma_f32_16x16x32_bf16 v[92:95], a[248:251], a[200:203], v[92:95]\n" \
"ds_read_b64_tr_b16 a[236:237], v35 offset:36864\n" \
"s_lshl_b32 s16, s3, 10\n" \
"v_mfma_f32_16x16x32_bf16 v[96:99], a[188:191], a[136:139], v[96:99]\n" \
"ds_read_b64_tr_b16 a[238:239], v35 offset:37376\n" \
"s_addk_i32 s16, 0x4800\n" \
"v_mfma_f32_16x16x32_bf16 v[96:99], a[252:255], a[200:203], v[96:99]\n" \
"ds_read_b128 a[136:139], v28 offset:4096\n" \
"v_writelane_b32 v11, s16, 25\n" \
"v_mfma_f32_16x16x32_bf16 v[112:115], a[188:191], a[140:143], v[112:115]\n" \
"ds_read_b128 a[200:203], v29 offset:4096\n" \
"s_add_i32 s16, s15, 1\n" \
"v_mfma_f32_16x16x32_bf16 v[112:115], a[252:255], a[204:207], v[112:115]\n" \
"s_movk_i32 s84, 0x2280\n" \
"v_mfma_f32_16x16x32_bf16 v[108:111], a[184:187], a[140:143], v[108:111]\n" \
"buffer_load_dwordx4 v25, s[76:79], s84 offen offset:1024 lds\n" \
"v_mfma_f32_16x16x32_bf16 v[108:111], a[248:251], a[204:207], v[108:111]\n" \
"s_add_u32 s89, s42, 0x0\n" \
"buffer_store_dwordx4 v[216:219], v20, s[80:83], s89 offen offset:1024 sc1\n" \
"v_mfma_f32_16x16x32_bf16 v[104:107], a[180:183], a[140:143], v[104:107]\n" \
"ds_read_b64_tr_b16 a[168:169], v34 offset:32768\n" \
"s_and_b32 s16, s16, 3\n" \
"v_mfma_f32_16x16x32_bf16 v[104:107], a[244:247], a[204:207], v[104:107]\n" \
"ds_read_b64_tr_b16 a[170:171], v34 offset:33280\n" \
"s_or_b32 s16, s16, s5\n" \
"v_mfma_f32_16x16x32_bf16 v[100:103], a[176:179], a[140:143], v[100:103]\n" \
"ds_read_b64_tr_b16 a[232:233], v34 offset:36864\n" \
"s_mul_i32 s18, s16, 0x24000\n" \
"v_mfma_f32_16x16x32_bf16 v[100:103], a[240:243], a[204:207], v[100:103]\n" \
"ds_read_b128 a[140:143], v28 offset:6144\n" \
"s_add_i32 s18, s18, s19\n" \
"v_mfma_f32_16x16x32_bf16 v[52:55], a[176:179], a[128:131], v[52:55]\n" \
"ds_read_b128 a[204:207], v29 offset:6144\n" \
"s_xor_b32 s13, s13, 2\n" \
"v_mfma_f32_16x16x32_bf16 v[52:55], a[240:243], a[192:195], v[52:55]\n" \
"s_movk_i32 s84, 0x4280\n" \
"v_mfma_f32_16x16x32_bf16 v[56:59], a[180:183], a[128:131], v[56:59]\n" \
"buffer_load_dwordx4 v24, s[76:79], s84 offen offset:2048 lds\n" \
"v_mfma_f32_16x16x32_bf16 v[56:59], a[244:247], a[192:195], v[56:59]\n" \
"s_movk_i32 s84, 0x6280\n" \
"v_mfma_f32_16x16x32_bf16 v[60:63], a[184:187], a[128:131], v[60:63]\n" \
"buffer_load_dwordx4 v25, s[76:79], s84 offen offset:3072 lds\n" \
"v_mfma_f32_16x16x32_bf16 v[60:63], a[248:251], a[192:195], v[60:63]\n" \
"ds_read_b64_tr_b16 a[234:235], v34 offset:37376\n" \
"s_add_i32 s15, s15, -1\n" \
"v_mfma_f32_16x16x32_bf16 v[64:67], a[188:191], a[128:131], v[64:67]\n" \
"v_writelane_b32 v11, s18, 27\n" \
"s_mul_i32 s18, s13, 0x24000\n" \
"v_mfma_f32_16x16x32_bf16 v[64:67], a[252:255], a[192:195], v[64:67]\n" \
"ds_read_b128 a[128:131], v28 offset:0\n" \
"s_and_b32 s15, s15, 3\n" \
"v_mfma_f32_16x16x32_bf16 v[80:83], a[188:191], a[132:135], v[80:83]\n" \
"ds_read_b128 a[192:195], v29 offset:0\n" \
"s_add_i32 s18, s18, s19\n" \
"v_mfma_f32_16x16x32_bf16 v[80:83], a[252:255], a[196:199], v[80:83]\n" \
"s_or_b32 s15, s15, s5\n" \
"v_writelane_b32 v11, s18, 28\n" \
"v_mfma_f32_16x16x32_bf16 v[76:79], a[184:187], a[132:135], v[76:79]\n" \
"s_mul_i32 s18, s15, 0x24000\n" \
"s_add_i32 s18, s18, s19\n" \
"v_mfma_f32_16x16x32_bf16 v[76:79], a[248:251], a[196:199], v[76:79]\n" \
"v_writelane_b32 v11, s18, 29\n" \
"s_or_b32 s18, s9, s14\n" \
"v_mfma_f32_16x16x32_bf16 v[72:75], a[180:183], a[132:135], v[72:75]\n" \
"s_mul_i32 s18, s18, 0x24800\n" \
"s_lshl_b32 s16, s16, 12\n" \
"v_mfma_f32_16x16x32_bf16 v[72:75], a[244:247], a[196:199], v[72:75]\n" \
"s_add_i32 s19, s18, 0x3004800\n" \
"s_add_i32 s20, s19, s16\n" \
"v_mfma_f32_16x16x32_bf16 v[68:71], a[176:179], a[132:135], v[68:71]\n" \
"v_writelane_b32 v11, s20, 30\n" \
"s_lshl_b32 s13, s13, 12\n" \
"v_mfma_f32_16x16x32_bf16 v[68:71], a[240:243], a[196:199], v[68:71]\n" \
"ds_read_b128 a[132:135], v28 offset:2048\n" \
"ds_read_b128 a[196:199], v29 offset:2048\n" \
"s_waitcnt lgkmcnt(15)\n" \
"v_mfma_f32_16x16x32_bf16 a[0:3], a[160:163], a[144:147], a[0:3]\n" \
"s_add_u32 m0, s30, 0x21000\n" \
"s_addk_i32 s84, 0x3000\n" \
"v_mfma_f32_16x16x32_bf16 a[0:3], a[224:227], a[208:211], a[0:3]\n" \
"buffer_load_dwordx4 v24, s[76:79], s84 offen offset:0 lds\n" \
"v_mfma_f32_16x16x32_bf16 a[4:7], a[164:167], a[144:147], a[4:7]\n" \
"ds_read_b64_tr_b16 a[176:177], v36 offset:32768\n" \
"s_lshl_b32 s15, s15, 12\n" \
"v_mfma_f32_16x16x32_bf16 a[4:7], a[228:231], a[208:211], a[4:7]\n" \
"ds_read_b64_tr_b16 a[178:179], v36 offset:33792\n" \
"s_sub_i32 s14, s9, s14\n" \
"v_mfma_f32_16x16x32_bf16 a[12:15], a[172:175], a[144:147], a[12:15]\n" \
"ds_read_b64_tr_b16 a[240:241], v36 offset:40960\n" \
"s_add_i32 s20, s19, s13\n" \
"s_waitcnt lgkmcnt(15)\n" \
"v_mfma_f32_16x16x32_bf16 a[12:15], a[236:239], a[208:211], a[12:15]\n" \
"s_add_u32 s88, s42, 0x0\n" \
"buffer_store_dwordx4 v[220:223], v20, s[80:83], s88 offen offset:0 sc1\n" \
"s_waitcnt lgkmcnt(11)\n" \
"v_mfma_f32_16x16x32_bf16 a[8:11], a[144:147], a[168:171], a[8:11]\n" \
"ds_read_b64_tr_b16 a[242:243], v36 offset:41984\n" \
"v_writelane_b32 v11, s20, 31\n" \
"s_waitcnt lgkmcnt(8)\n" \
"v_mfma_f32_16x16x32_bf16 a[8:11], a[208:211], a[232:235], a[8:11]\n" \
"ds_read_b64_tr_b16 a[180:181], v37 offset:32768\n" \
"s_add_i32 s19, s19, s15\n" \
"v_mfma_f32_16x16x32_bf16 a[24:27], a[148:151], a[168:171], a[24:27]\n" \
"ds_read_b64_tr_b16 a[182:183], v37 offset:33792\n" \
"s_mul_i32 s14, s14, 0x24800\n" \
"v_mfma_f32_16x16x32_bf16 a[24:27], a[212:215], a[232:235], a[24:27]\n" \
"ds_read_b64_tr_b16 a[244:245], v37 offset:40960\n" \
"v_writelane_b32 v11, s19, 32\n" \
"v_mfma_f32_16x16x32_bf16 a[28:31], a[172:175], a[148:151], a[28:31]\n" \
"s_add_i32 s19, s14, 0x4120800\n" \
"s_add_i32 s16, s19, s16\n" \
"v_mfma_f32_16x16x32_bf16 a[28:31], a[236:239], a[212:215], a[28:31]\n" \
"s_addk_i32 s84, 0x2000\n" \
"v_mfma_f32_16x16x32_bf16 a[20:23], a[164:167], a[148:151], a[20:23]\n" \
"buffer_load_dwordx4 v25, s[76:79], s84 offen offset:1024 lds\n" \
"v_mfma_f32_16x16x32_bf16 a[20:23], a[228:231], a[212:215], a[20:23]\n" \
"ds_read_b64_tr_b16 a[246:247], v37 offset:41984\n" \
"v_writelane_b32 v11, s16, 33\n" \
"v_mfma_f32_16x16x32_bf16 a[16:19], a[160:163], a[148:151], a[16:19]\n" \
"ds_read_b64_tr_b16 a[184:185], v38 offset:32768\n" \
"s_add_i32 s13, s19, s13\n" \
"v_mfma_f32_16x16x32_bf16 a[16:19], a[224:227], a[212:215], a[16:19]\n" \
"ds_read_b64_tr_b16 a[186:187], v38 offset:33792\n" \
"v_writelane_b32 v11, s13, 34\n" \
"v_mfma_f32_16x16x32_bf16 a[32:35], a[160:163], a[152:155], a[32:35]\n" \
"s_addk_i32 s84, 0x2000\n" \
"v_mfma_f32_16x16x32_bf16 a[32:35], a[224:227], a[216:219], a[32:35]\n" \
"buffer_load_dwordx4 v24, s[76:79], s84 offen offset:2048 lds\n" \
"v_mfma_f32_16x16x32_bf16 a[36:39], a[164:167], a[152:155], a[36:39]\n" \
"ds_read_b64_tr_b16 a[248:249], v38 offset:40960\n" \
"s_add_i32 s19, s19, s15\n" \
"v_mfma_f32_16x16x32_bf16 a[36:39], a[228:231], a[216:219], a[36:39]\n" \
"ds_read_b64_tr_b16 a[250:251], v38 offset:41984\n" \
"s_or_b32 s13, s18, 0x3000000\n" \
"v_mfma_f32_16x16x32_bf16 a[44:47], a[172:175], a[152:155], a[44:47]\n" \
"ds_read_b64_tr_b16 a[188:189], v39 offset:32768\n" \
"s_lshl_b32 s15, s6, 11\n" \
"v_mfma_f32_16x16x32_bf16 a[44:47], a[236:239], a[216:219], a[44:47]\n" \
"ds_read_b64_tr_b16 a[190:191], v39 offset:33792\n" \
"v_writelane_b32 v11, s19, 35\n" \
"v_mfma_f32_16x16x32_bf16 a[40:43], a[152:155], a[168:171], a[40:43]\n" \
"s_add_u32 s89, s42, 0x1000\n" \
"buffer_store_dwordx4 v[224:227], v20, s[80:83], s89 offen offset:3072 sc1\n" \
"v_mfma_f32_16x16x32_bf16 a[40:43], a[216:219], a[232:235], a[40:43]\n" \
"ds_read_b64_tr_b16 a[252:253], v39 offset:40960\n" \
"s_add_i32 s16, s13, s15\n" \
"v_mfma_f32_16x16x32_bf16 a[56:59], a[156:159], a[168:171], a[56:59]\n" \
"ds_read_b64_tr_b16 a[254:255], v39 offset:41984\n" \
"v_writelane_b32 v11, s16, 36\n" \
"v_mfma_f32_16x16x32_bf16 a[56:59], a[220:223], a[232:235], a[56:59]\n" \
"s_add_i32 s16, s14, 0x411c000\n" \
"s_add_i32 s19, s16, s15\n" \
"v_mfma_f32_16x16x32_bf16 a[60:63], a[172:175], a[156:159], a[60:63]\n" \
"v_writelane_b32 v11, s19, 37\n" \
"s_add_i32 s18, s18, 0x3004000\n" \
"v_mfma_f32_16x16x32_bf16 a[60:63], a[236:239], a[220:223], a[60:63]\n" \
"s_addk_i32 s84, 0x2000\n" \
"v_mfma_f32_16x16x32_bf16 a[52:55], a[164:167], a[156:159], a[52:55]\n" \
"buffer_load_dwordx4 v25, s[76:79], s84 offen offset:3072 lds\n" \
"v_mfma_f32_16x16x32_bf16 a[52:55], a[228:231], a[220:223], a[52:55]\n" \
"v_writelane_b32 v11, s18, 38\n" \
"s_add_i32 s14, s14, 0x4120000\n" \
"v_mfma_f32_16x16x32_bf16 a[48:51], a[160:163], a[156:159], a[48:51]\n" \
"v_writelane_b32 v11, s14, 39\n" \
"s_lshl_b32 s14, s4, 2\n" \
"v_mfma_f32_16x16x32_bf16 a[48:51], a[224:227], a[220:223], a[48:51]\n" \
"s_or_b32 s8, s14, s8\n" \
"s_mul_i32 s8, s8, 0x90000\n" \
"v_mfma_f32_16x16x32_bf16 a[64:67], a[164:167], a[140:143], a[64:67]\n" \
"s_lshl_b32 s14, s3, 14\n" \
"s_add_i32 s8, s8, s14\n" \
"v_mfma_f32_16x16x32_bf16 a[64:67], a[228:231], a[204:207], a[64:67]\n" \
"s_lshl_b32 s9, s9, 4\n" \
"s_lshl_b32 s14, s4, 7\n" \
"v_mfma_f32_16x16x32_bf16 a[80:83], a[172:175], a[140:143], a[80:83]\n" \
"s_or_b32 s18, s9, s14\n" \
"s_sub_i32 s9, s9, s14\n" \
"v_mfma_f32_16x16x32_bf16 a[80:83], a[236:239], a[204:207], a[80:83]\n" \
"s_mulk_i32 s18, 0x480\n" \
"s_mulk_i32 s9, 0x480\n" \
"v_mfma_f32_16x16x32_bf16 a[68:71], a[140:143], a[168:171], a[68:71]\n" \
"s_or_b32 s14, s18, s12\n" \
"s_or_b32 s9, s9, s12\n" \
"v_mfma_f32_16x16x32_bf16 a[68:71], a[204:207], a[232:235], a[68:71]\n" \
"s_waitcnt vmcnt(11) lgkmcnt(0)\n" \
"s_barrier\n" \
"v_mfma_f32_16x16x32_bf16 v[116:119], a[176:179], a[144:147], v[116:119]\n" \
"s_add_u32 m0, s40, 0x8000\n" \
"s_add_u32 s85, s39, 0xa000\n" \
"v_mfma_f32_16x16x32_bf16 v[116:119], a[240:243], a[208:211], v[116:119]\n" \
"buffer_load_dwordx4 v20, s[80:83], s85 offen sc1 lds\n" \
"v_mfma_f32_16x16x32_bf16 v[120:123], a[180:183], a[144:147], v[120:123]\n" \
"ds_read_b64_tr_b16 a[160:161], v32 offset:0\n" \
"v_accvgpr_read_b32 v212, a108\n" \
"v_mfma_f32_16x16x32_bf16 v[120:123], a[244:247], a[208:211], v[120:123]\n" \
"ds_read_b64_tr_b16 a[162:163], v32 offset:512\n" \
"v_accvgpr_read_b32 v213, a109\n" \
"v_mfma_f32_16x16x32_bf16 v[124:127], a[184:187], a[144:147], v[124:127]\n" \
"s_add_u32 m0, s40, 0x9000\n" \
"s_add_u32 s85, s39, 0xb000\n" \
"v_mfma_f32_16x16x32_bf16 v[124:127], a[248:251], a[208:211], v[124:127]\n" \
"buffer_load_dwordx4 v20, s[80:83], s85 offen sc1 lds\n" \
"v_mfma_f32_16x16x32_bf16 v[128:131], a[188:191], a[144:147], v[128:131]\n" \
"ds_read_b64_tr_b16 a[224:225], v32 offset:4096\n" \
"v_accvgpr_read_b32 v214, a110\n" \
"v_mfma_f32_16x16x32_bf16 v[128:131], a[252:255], a[208:211], v[128:131]\n" \
"ds_read_b128 a[144:147], v28 offset:40960\n" \
"v_accvgpr_read_b32 v215, a111\n" \
"v_mfma_f32_16x16x32_bf16 v[144:147], a[188:191], a[148:151], v[144:147]\n" \
"ds_read_b128 a[208:211], v29 offset:40960\n" \
"v_accvgpr_read_b32 v180, a124\n" \
"v_mfma_f32_16x16x32_bf16 v[144:147], a[252:255], a[212:215], v[144:147]\n" \
"s_add_u32 m0, s40, 0xb000\n" \
"s_movk_i32 s84, 0x280\n" \
"s_nop 0\n" \
"v_mfma_f32_16x16x32_bf16 v[140:143], a[184:187], a[148:151], v[140:143]\n" \
"buffer_load_dwordx4 v21, s[76:79], s84 offen offset:0 lds\n" \
"v_mfma_f32_16x16x32_bf16 v[140:143], a[248:251], a[212:215], v[140:143]\n" \
"ds_read_b64_tr_b16 a[226:227], v32 offset:4608\n" \
"v_accvgpr_read_b32 v181, a125\n" \
"v_mfma_f32_16x16x32_bf16 v[136:139], a[180:183], a[148:151], v[136:139]\n" \
"s_add_u32 s88, s42, 0x1000\n" \
"buffer_store_dwordx4 v[228:231], v20, s[80:83], s88 offen offset:2048 sc1\n" \
"v_mfma_f32_16x16x32_bf16 v[136:139], a[244:247], a[212:215], v[136:139]\n" \
"ds_read_b64_tr_b16 a[164:165], v33 offset:0\n" \
"v_accvgpr_read_b32 v182, a126\n" \
"v_mfma_f32_16x16x32_bf16 v[132:135], a[176:179], a[148:151], v[132:135]\n" \
"v_accvgpr_read_b32 v183, a127\n" \
"v_cvt_pk_bf16_f32 v212, v212, v213\n" \
"v_mfma_f32_16x16x32_bf16 v[132:135], a[240:243], a[212:215], v[132:135]\n" \
"ds_read_b128 a[148:151], v28 offset:43008\n" \
"v_cvt_pk_bf16_f32 v213, v214, v215\n" \
"v_mfma_f32_16x16x32_bf16 v[148:151], a[176:179], a[152:155], v[148:151]\n" \
"ds_read_b128 a[212:215], v29 offset:43008\n" \
"v_cvt_pk_bf16_f32 v214, v180, v181\n" \
"v_mfma_f32_16x16x32_bf16 v[148:151], a[240:243], a[216:219], v[148:151]\n" \
"s_add_u32 m0, s31, 0x8000\n" \
"s_mov_b32 s84, 0x280000\n" \
"v_mfma_f32_16x16x32_bf16 v[152:155], a[180:183], a[152:155], v[152:155]\n" \
"buffer_load_dwordx4 v26, s[68:71], s84 offen offset:0 lds\n" \
"v_mfma_f32_16x16x32_bf16 v[152:155], a[244:247], a[216:219], v[152:155]\n" \
"ds_read_b64_tr_b16 a[166:167], v33 offset:512\n" \
"v_cvt_pk_bf16_f32 v215, v182, v183\n" \
"v_mfma_f32_16x16x32_bf16 v[156:159], a[184:187], a[152:155], v[156:159]\n" \
"ds_read_b64_tr_b16 a[228:229], v33 offset:4096\n" \
"v_accvgpr_read_b32 v208, a104\n" \
"v_mfma_f32_16x16x32_bf16 v[156:159], a[248:251], a[216:219], v[156:159]\n" \
"s_addk_i32 s84, 0x7c00\n" \
"v_mfma_f32_16x16x32_bf16 v[160:163], a[188:191], a[152:155], v[160:163]\n" \
"buffer_load_dwordx4 v26, s[68:71], s84 offen offset:1024 lds\n" \
"v_mfma_f32_16x16x32_bf16 v[160:163], a[252:255], a[216:219], v[160:163]\n" \
"ds_read_b128 a[152:155], v28 offset:45056\n" \
"v_accvgpr_read_b32 v209, a105\n" \
"v_mfma_f32_16x16x32_bf16 v[176:179], a[188:191], a[156:159], v[176:179]\n" \
"ds_read_b128 a[216:219], v29 offset:45056\n" \
"v_accvgpr_read_b32 v210, a106\n" \
"v_mfma_f32_16x16x32_bf16 v[176:179], a[252:255], a[220:223], v[176:179]\n" \
"s_addk_i32 s84, 0x7c00\n" \
"v_mfma_f32_16x16x32_bf16 v[172:175], a[184:187], a[156:159], v[172:175]\n" \
"buffer_load_dwordx4 v27, s[68:71], s84 offen offset:2048 lds\n" \
"v_mfma_f32_16x16x32_bf16 v[172:175], a[248:251], a[220:223], v[172:175]\n" \
"ds_read_b64_tr_b16 a[230:231], v33 offset:4608\n" \
"v_accvgpr_read_b32 v211, a107\n" \
"v_mfma_f32_16x16x32_bf16 v[168:171], a[180:183], a[156:159], v[168:171]\n" \
"ds_read_b64_tr_b16 a[172:173], v35 offset:0\n" \
"v_accvgpr_read_b32 v180, a120\n" \
"v_mfma_f32_16x16x32_bf16 v[168:171], a[244:247], a[220:223], v[168:171]\n" \
"ds_read_b64_tr_b16 a[174:175], v35 offset:512\n" \
"v_accvgpr_read_b32 v181, a121\n" \
"v_mfma_f32_16x16x32_bf16 v[164:167], a[176:179], a[156:159], v[164:167]\n" \
"v_accvgpr_read_b32 v182, a122\n" \
"v_accvgpr_read_b32 v183, a123\n" \
"v_mfma_f32_16x16x32_bf16 v[164:167], a[240:243], a[220:223], v[164:167]\n" \
"ds_read_b128 a[156:159], v28 offset:47104\n" \
"v_cvt_pk_bf16_f32 v208, v208, v209\n" \
"v_mfma_f32_16x16x32_bf16 v[84:87], a[176:179], a[136:139], v[84:87]\n" \
"ds_read_b128 a[220:223], v29 offset:47104\n" \
"v_cvt_pk_bf16_f32 v209, v210, v211\n" \
"v_mfma_f32_16x16x32_bf16 v[84:87], a[240:243], a[200:203], v[84:87]\n" \
"s_addk_i32 s84, 0x7c00\n" \
"v_mfma_f32_16x16x32_bf16 v[88:91], a[180:183], a[136:139], v[88:91]\n" \
"buffer_load_dwordx4 v27, s[68:71], s84 offen offset:3072 lds\n" \
"v_mfma_f32_16x16x32_bf16 v[88:91], a[244:247], a[200:203], v[88:91]\n" \
"s_add_u32 m0, s30, 0x0\n" \
"s_movk_i32 s84, 0x300\n" \
"s_nop 0\n" \
"v_mfma_f32_16x16x32_bf16 v[92:95], a[184:187], a[136:139], v[92:95]\n" \
"buffer_load_dwordx4 v24, s[76:79], s84 offen offset:0 lds\n" \
"v_mfma_f32_16x16x32_bf16 v[92:95], a[248:251], a[200:203], v[92:95]\n" \
"ds_read_b64_tr_b16 a[236:237], v35 offset:4096\n" \
"v_cvt_pk_bf16_f32 v210, v180, v181\n" \
"v_mfma_f32_16x16x32_bf16 v[96:99], a[188:191], a[136:139], v[96:99]\n" \
"ds_read_b64_tr_b16 a[238:239], v35 offset:4608\n" \
"v_cvt_pk_bf16_f32 v211, v182, v183\n" \
"v_mfma_f32_16x16x32_bf16 v[96:99], a[252:255], a[200:203], v[96:99]\n" \
"ds_read_b128 a[136:139], v28 offset:36864\n" \
"v_accvgpr_read_b32 v204, a100\n" \
"v_mfma_f32_16x16x32_bf16 v[112:115], a[188:191], a[140:143], v[112:115]\n" \
"ds_read_b128 a[200:203], v29 offset:36864\n" \
"v_accvgpr_read_b32 v205, a101\n" \
"v_mfma_f32_16x16x32_bf16 v[112:115], a[252:255], a[204:207], v[112:115]\n" \
"s_movk_i32 s84, 0x2300\n" \
"s_nop 0\n" \
"v_mfma_f32_16x16x32_bf16 v[108:111], a[184:187], a[140:143], v[108:111]\n" \
"buffer_load_dwordx4 v25, s[76:79], s84 offen offset:1024 lds\n" \
"v_mfma_f32_16x16x32_bf16 v[108:111], a[248:251], a[204:207], v[108:111]\n" \
"s_add_u32 s89, s42, 0x1000\n" \
"buffer_store_dwordx4 v[232:235], v20, s[80:83], s89 offen offset:1024 sc1\n" \
"v_mfma_f32_16x16x32_bf16 v[104:107], a[180:183], a[140:143], v[104:107]\n" \
"ds_read_b64_tr_b16 a[168:169], v34 offset:0\n" \
"v_accvgpr_read_b32 v206, a102\n" \
"v_mfma_f32_16x16x32_bf16 v[104:107], a[244:247], a[204:207], v[104:107]\n" \
"ds_read_b64_tr_b16 a[170:171], v34 offset:512\n" \
"v_accvgpr_read_b32 v207, a103\n" \
"v_mfma_f32_16x16x32_bf16 v[100:103], a[176:179], a[140:143], v[100:103]\n" \
"ds_read_b64_tr_b16 a[232:233], v34 offset:4096\n" \
"v_accvgpr_read_b32 v180, a116\n" \
"v_mfma_f32_16x16x32_bf16 v[100:103], a[240:243], a[204:207], v[100:103]\n" \
"ds_read_b128 a[140:143], v28 offset:38912\n" \
"v_accvgpr_read_b32 v181, a117\n" \
"v_mfma_f32_16x16x32_bf16 v[52:55], a[176:179], a[128:131], v[52:55]\n" \
"ds_read_b128 a[204:207], v29 offset:38912\n" \
"v_accvgpr_read_b32 v182, a118\n" \
"v_mfma_f32_16x16x32_bf16 v[52:55], a[240:243], a[192:195], v[52:55]\n" \
"s_movk_i32 s84, 0x4300\n" \
"v_mfma_f32_16x16x32_bf16 v[56:59], a[180:183], a[128:131], v[56:59]\n" \
"buffer_load_dwordx4 v24, s[76:79], s84 offen offset:2048 lds\n" \
"v_mfma_f32_16x16x32_bf16 v[56:59], a[244:247], a[192:195], v[56:59]\n" \
"s_movk_i32 s84, 0x6300\n" \
"v_mfma_f32_16x16x32_bf16 v[60:63], a[184:187], a[128:131], v[60:63]\n" \
"buffer_load_dwordx4 v25, s[76:79], s84 offen offset:3072 lds\n" \
"v_mfma_f32_16x16x32_bf16 v[60:63], a[248:251], a[192:195], v[60:63]\n" \
"ds_read_b64_tr_b16 a[234:235], v34 offset:4608\n" \
"v_accvgpr_read_b32 v183, a119\n" \
"v_mfma_f32_16x16x32_bf16 v[64:67], a[188:191], a[128:131], v[64:67]\n" \
"v_cvt_pk_bf16_f32 v204, v204, v205\n" \
"v_cvt_pk_bf16_f32 v205, v206, v207\n" \
"v_mfma_f32_16x16x32_bf16 v[64:67], a[252:255], a[192:195], v[64:67]\n" \
"ds_read_b128 a[128:131], v28 offset:32768\n" \
"v_cvt_pk_bf16_f32 v206, v180, v181\n" \
"v_mfma_f32_16x16x32_bf16 v[80:83], a[188:191], a[132:135], v[80:83]\n" \
"ds_read_b128 a[192:195], v29 offset:32768\n" \
"v_cvt_pk_bf16_f32 v207, v182, v183\n" \
"v_mfma_f32_16x16x32_bf16 v[80:83], a[252:255], a[196:199], v[80:83]\n" \
"v_accvgpr_read_b32 v200, a96\n" \
"v_accvgpr_read_b32 v201, a97\n" \
"v_mfma_f32_16x16x32_bf16 v[76:79], a[184:187], a[132:135], v[76:79]\n" \
"v_accvgpr_read_b32 v202, a98\n" \
"v_accvgpr_read_b32 v203, a99\n" \
"v_mfma_f32_16x16x32_bf16 v[76:79], a[248:251], a[196:199], v[76:79]\n" \
"v_accvgpr_read_b32 v180, a112\n" \
"v_accvgpr_read_b32 v181, a113\n" \
"v_mfma_f32_16x16x32_bf16 v[72:75], a[180:183], a[132:135], v[72:75]\n" \
"v_accvgpr_read_b32 v182, a114\n" \
"v_accvgpr_read_b32 v183, a115\n" \
"v_mfma_f32_16x16x32_bf16 v[72:75], a[244:247], a[196:199], v[72:75]\n" \
"v_cvt_pk_bf16_f32 v200, v200, v201\n" \
"v_cvt_pk_bf16_f32 v201, v202, v203\n" \
"v_mfma_f32_16x16x32_bf16 v[68:71], a[176:179], a[132:135], v[68:71]\n" \
"v_cvt_pk_bf16_f32 v202, v180, v181\n" \
"v_cvt_pk_bf16_f32 v203, v182, v183\n" \
"v_mfma_f32_16x16x32_bf16 v[68:71], a[240:243], a[196:199], v[68:71]\n" \
"ds_read_b128 a[132:135], v28 offset:34816\n" \
"ds_read_b128 a[196:199], v29 offset:34816\n" \
"s_waitcnt lgkmcnt(15)\n" \
"v_mfma_f32_16x16x32_bf16 a[0:3], a[160:163], a[144:147], a[0:3]\n" \
"s_add_u32 m0, s30, 0x1000\n" \
"s_addk_i32 s84, 0x3000\n" \
"v_mfma_f32_16x16x32_bf16 a[0:3], a[224:227], a[208:211], a[0:3]\n" \
"buffer_load_dwordx4 v24, s[76:79], s84 offen offset:0 lds\n" \
"v_mfma_f32_16x16x32_bf16 a[4:7], a[164:167], a[144:147], a[4:7]\n" \
"ds_read_b64_tr_b16 a[176:177], v36 offset:0\n" \
"v_accvgpr_read_b32 v196, a76\n" \
"v_mfma_f32_16x16x32_bf16 a[4:7], a[228:231], a[208:211], a[4:7]\n" \
"ds_read_b64_tr_b16 a[178:179], v36 offset:1024\n" \
"v_accvgpr_read_b32 v197, a77\n" \
"v_mfma_f32_16x16x32_bf16 a[12:15], a[172:175], a[144:147], a[12:15]\n" \
"ds_read_b64_tr_b16 a[240:241], v36 offset:8192\n" \
"v_accvgpr_read_b32 v198, a78\n" \
"s_waitcnt lgkmcnt(15)\n" \
"v_mfma_f32_16x16x32_bf16 a[12:15], a[236:239], a[208:211], a[12:15]\n" \
"s_add_u32 s88, s42, 0x1000\n" \
"buffer_store_dwordx4 v[236:239], v20, s[80:83], s88 offen offset:0 sc1\n" \
"s_waitcnt lgkmcnt(11)\n" \
"v_mfma_f32_16x16x32_bf16 a[8:11], a[144:147], a[168:171], a[8:11]\n" \
"ds_read_b64_tr_b16 a[242:243], v36 offset:9216\n" \
"v_accvgpr_read_b32 v199, a79\n" \
"s_waitcnt lgkmcnt(8)\n" \
"s_nop 0\n" \
"v_mfma_f32_16x16x32_bf16 a[8:11], a[208:211], a[232:235], a[8:11]\n" \
"ds_read_b64_tr_b16 a[180:181], v37 offset:0\n" \
"v_accvgpr_read_b32 v180, a92\n" \
"v_mfma_f32_16x16x32_bf16 a[24:27], a[148:151], a[168:171], a[24:27]\n" \
"ds_read_b64_tr_b16 a[182:183], v37 offset:1024\n" \
"v_accvgpr_read_b32 v181, a93\n" \
"v_mfma_f32_16x16x32_bf16 a[24:27], a[212:215], a[232:235], a[24:27]\n" \
"ds_read_b64_tr_b16 a[244:245], v37 offset:8192\n" \
"v_accvgpr_read_b32 v182, a94\n" \
"v_mfma_f32_16x16x32_bf16 a[28:31], a[172:175], a[148:151], a[28:31]\n" \
"v_accvgpr_read_b32 v183, a95\n" \
"v_cvt_pk_bf16_f32 v196, v196, v197\n" \
"v_mfma_f32_16x16x32_bf16 a[28:31], a[236:239], a[212:215], a[28:31]\n" \
"s_addk_i32 s84, 0x2000\n" \
"s_nop 0\n" \
"v_mfma_f32_16x16x32_bf16 a[20:23], a[164:167], a[148:151], a[20:23]\n" \
"buffer_load_dwordx4 v25, s[76:79], s84 offen offset:1024 lds\n" \
"v_mfma_f32_16x16x32_bf16 a[20:23], a[228:231], a[212:215], a[20:23]\n" \
"ds_read_b64_tr_b16 a[246:247], v37 offset:9216\n" \
"v_cvt_pk_bf16_f32 v197, v198, v199\n" \
"v_mfma_f32_16x16x32_bf16 a[16:19], a[160:163], a[148:151], a[16:19]\n" \
"ds_read_b64_tr_b16 a[184:185], v38 offset:0\n" \
"v_cvt_pk_bf16_f32 v198, v180, v181\n" \
"v_mfma_f32_16x16x32_bf16 a[16:19], a[224:227], a[212:215], a[16:19]\n" \
"ds_read_b64_tr_b16 a[186:187], v38 offset:1024\n" \
"v_cvt_pk_bf16_f32 v199, v182, v183\n" \
"v_mfma_f32_16x16x32_bf16 a[32:35], a[160:163], a[152:155], a[32:35]\n" \
"s_addk_i32 s84, 0x2000\n" \
"s_nop 0\n" \
"v_mfma_f32_16x16x32_bf16 a[32:35], a[224:227], a[216:219], a[32:35]\n" \
"buffer_load_dwordx4 v24, s[76:79], s84 offen offset:2048 lds\n" \
"v_mfma_f32_16x16x32_bf16 a[36:39], a[164:167], a[152:155], a[36:39]\n" \
".fill (L_pb + 69344 - .) / 4, 4, 0xBF800000\n" \
"L_pp17:\n" \
"s_cbranch_execz L_pret\n" \
"s_nop 0\n" \
"ds_read_b64_tr_b16 a[248:249], v38 offset:8192\n" \
"v_accvgpr_read_b32 v192, a72\n" \
"v_mfma_f32_16x16x32_bf16 a[36:39], a[228:231], a[216:219], a[36:39]\n" \
"ds_read_b64_tr_b16 a[250:251], v38 offset:9216\n" \
"v_accvgpr_read_b32 v193, a73\n" \
"v_mfma_f32_16x16x32_bf16 a[44:47], a[172:175], a[152:155], a[44:47]\n" \
"ds_read_b64_tr_b16 a[188:189], v39 offset:0\n" \
"v_accvgpr_read_b32 v194, a74\n" \
"v_mfma_f32_16x16x32_bf16 a[44:47], a[236:239], a[216:219], a[44:47]\n" \
"ds_read_b64_tr_b16 a[190:191], v39 offset:1024\n" \
"v_accvgpr_read_b32 v195, a75\n" \
"v_mfma_f32_16x16x32_bf16 a[40:43], a[152:155], a[168:171], a[40:43]\n" \
"s_add_u32 s89, s42, 0x3000\n" \
"buffer_store_dwordx4 v[240:243], v20, s[80:83], s89 offen offset:3072 sc1\n" \
"v_mfma_f32_16x16x32_bf16 a[40:43], a[216:219], a[232:235], a[40:43]\n" \
"ds_read_b64_tr_b16 a[252:253], v39 offset:8192\n" \
"v_accvgpr_read_b32 v180, a88\n" \
"v_mfma_f32_16x16x32_bf16 a[56:59], a[156:159], a[168:171], a[56:59]\n" \
"ds_read_b64_tr_b16 a[254:255], v39 offset:9216\n" \
"v_accvgpr_read_b32 v181, a89\n" \
"v_mfma_f32_16x16x32_bf16 a[56:59], a[220:223], a[232:235], a[56:59]\n" \
"v_accvgpr_read_b32 v182, a90\n" \
"v_accvgpr_read_b32 v183, a91\n" \
"v_mfma_f32_16x16x32_bf16 a[60:63], a[172:175], a[156:159], a[60:63]\n" \
"v_cvt_pk_bf16_f32 v192, v192, v193\n" \
"v_cvt_pk_bf16_f32 v193, v194, v195\n" \
"v_mfma_f32_16x16x32_bf16 a[60:63], a[236:239], a[220:223], a[60:63]\n" \
"s_addk_i32 s84, 0x2000\n" \
"v_mfma_f32_16x16x32_bf16 a[52:55], a[164:167], a[156:159], a[52:55]\n" \
"buffer_load_dwordx4 v25, s[76:79], s84 offen offset:3072 lds\n" \
"v_mfma_f32_16x16x32_bf16 a[52:55], a[228:231], a[220:223], a[52:55]\n" \
"v_cvt_pk_bf16_f32 v194, v180, v181\n" \
"v_cvt_pk_bf16_f32 v195, v182, v183\n" \
"v_mfma_f32_16x16x32_bf16 a[48:51], a[160:163], a[156:159], a[48:51]\n" \
"s_and_b32 s12, s2, 1\n" \
"v_mfma_f32_16x16x32_bf16 a[48:51], a[224:227], a[220:223], a[48:51]\n" \
"s_cmp_lg_u32 s12, 0\n" \
"s_cselect_b32 s13, s16, s13\n" \
"v_mfma_f32_16x16x32_bf16 a[64:67], a[164:167], a[140:143], a[64:67]\n" \
"s_bfe_u32 s16, s2, 0x20001\n" \
"s_addk_i32 s13, 0x4000\n" \
"v_mfma_f32_16x16x32_bf16 a[64:67], a[228:231], a[204:207], a[64:67]\n" \
"s_xor_b32 s16, s16, s3\n" \
"v_mfma_f32_16x16x32_bf16 a[80:83], a[172:175], a[140:143], a[80:83]\n" \
"s_cmp_lg_u32 s16, 0\n" \
"s_cselect_b32 s13, s7, s13\n" \
"v_mfma_f32_16x16x32_bf16 a[80:83], a[236:239], a[204:207], a[80:83]\n" \
"s_lshl_b32 s6, s6, 16\n" \
"v_writelane_b32 v11, s13, 40\n" \
"v_mfma_f32_16x16x32_bf16 a[68:71], a[140:143], a[168:171], a[68:71]\n" \
"s_add_i32 s13, s8, 0x4240000\n" \
"s_add_i32 s6, s13, s6\n" \
"v_mfma_f32_16x16x32_bf16 a[68:71], a[204:207], a[232:235], a[68:71]\n" \
"s_waitcnt vmcnt(11) lgkmcnt(0)\n" \
"s_barrier\n" \
"v_mov_b32_e32 v45, s62\n" \
"s_mov_b64 exec, 1\n" \
"buffer_store_dword v45, off, s[80:83], s37 offset:0 sc1\n" \
"s_mov_b64 exec, -1\n" \
"v_xor_b32_e32 v28, 0x20000, v28\n" \
"v_xor_b32_e32 v29, 0x20000, v29\n" \
"v_mfma_f32_16x16x32_bf16 v[116:119], a[176:179], a[144:147], v[116:119]\n" \
"s_add_u32 m0, s40, 0x0\n" \
"s_add_u32 s85, s39, 0xc000\n" \
"v_mfma_f32_16x16x32_bf16 v[116:119], a[240:243], a[208:211], v[116:119]\n" \
"buffer_load_dwordx4 v20, s[80:83], s85 offen sc1 lds\n" \
"v_mfma_f32_16x16x32_bf16 v[120:123], a[180:183], a[144:147], v[120:123]\n" \
"ds_read_b64_tr_b16 a[160:161], v32 offset:32768\n" \
"v_writelane_b32 v11, s6, 41\n" \
"v_mfma_f32_16x16x32_bf16 v[120:123], a[244:247], a[208:211], v[120:123]\n" \
"ds_read_b64_tr_b16 a[162:163], v32 offset:33280\n" \
"s_add_i32 s8, s8, 0x42c0000\n" \
"v_mfma_f32_16x16x32_bf16 v[124:127], a[184:187], a[144:147], v[124:127]\n" \
"s_add_u32 m0, s40, 0x1000\n" \
"s_add_u32 s85, s39, 0xd000\n" \
"v_mfma_f32_16x16x32_bf16 v[124:127], a[248:251], a[208:211], v[124:127]\n" \
"buffer_load_dwordx4 v20, s[80:83], s85 offen sc1 lds\n" \
"v_mfma_f32_16x16x32_bf16 v[128:131], a[188:191], a[144:147], v[128:131]\n" \
"ds_read_b64_tr_b16 a[224:225], v32 offset:36864\n" \
"s_lshl_b32 s6, s12, 10\n" \
"v_mfma_f32_16x16x32_bf16 v[128:131], a[252:255], a[208:211], v[128:131]\n" \
"ds_read_b128 a[144:147], v28 offset:8192\n" \
"s_or_b32 s6, s8, s6\n" \
"v_mfma_f32_16x16x32_bf16 v[144:147], a[188:191], a[148:151], v[144:147]\n" \
"ds_read_b128 a[208:211], v29 offset:8192\n" \
"v_mfma_f32_16x16x32_bf16 v[144:147], a[252:255], a[212:215], v[144:147]\n" \
"s_add_u32 m0, s40, 0x3000\n" \
"s_movk_i32 s84, 0x300\n" \
"v_mfma_f32_16x16x32_bf16 v[140:143], a[184:187], a[148:151], v[140:143]\n" \
"buffer_load_dwordx4 v21, s[76:79], s84 offen offset:0 lds\n" \
"v_mfma_f32_16x16x32_bf16 v[140:143], a[248:251], a[212:215], v[140:143]\n" \
"ds_read_b64_tr_b16 a[226:227], v32 offset:37376\n" \
"v_mfma_f32_16x16x32_bf16 v[136:139], a[180:183], a[148:151], v[136:139]\n" \
"s_cmp_lg_u32 s16, 0\n" \
"s_cselect_b32 s6, s7, s6\n" \
"v_mfma_f32_16x16x32_bf16 v[136:139], a[244:247], a[212:215], v[136:139]\n" \
"ds_read_b64_tr_b16 a[164:165], v33 offset:32768\n" \
"s_mulk_i32 s4, 0x4800\n" \
"v_mfma_f32_16x16x32_bf16 v[132:135], a[176:179], a[148:151], v[132:135]\n" \
"v_writelane_b32 v11, s6, 42\n" \
"s_add_i32 s6, s14, s10\n" \
"v_mfma_f32_16x16x32_bf16 v[132:135], a[240:243], a[212:215], v[132:135]\n" \
"ds_read_b128 a[148:151], v28 offset:10240\n" \
"v_writelane_b32 v11, s6, 43\n" \
"v_mfma_f32_16x16x32_bf16 v[148:151], a[176:179], a[152:155], v[148:151]\n" \
"ds_read_b128 a[212:215], v29 offset:10240\n" \
"s_add_i32 s6, s9, 0x21c000\n" \
"v_mfma_f32_16x16x32_bf16 v[148:151], a[240:243], a[216:219], v[148:151]\n" \
"s_add_u32 m0, s31, 0x0\n" \
"s_mov_b32 s84, 0x300000\n" \
"v_mfma_f32_16x16x32_bf16 v[152:155], a[180:183], a[152:155], v[152:155]\n" \
"buffer_load_dwordx4 v26, s[68:71], s84 offen offset:0 lds\n" \
"v_mfma_f32_16x16x32_bf16 v[152:155], a[244:247], a[216:219], v[152:155]\n" \
"s_addk_i32 s84, 0x7c00\n" \
"v_mfma_f32_16x16x32_bf16 v[156:159], a[184:187], a[152:155], v[156:159]\n" \
"buffer_load_dwordx4 v26, s[68:71], s84 offen offset:1024 lds\n" \
"v_mfma_f32_16x16x32_bf16 v[156:159], a[248:251], a[216:219], v[156:159]\n" \
"ds_read_b64_tr_b16 a[166:167], v33 offset:33280\n" \
"s_add_i32 s9, s6, s10\n" \
"v_mfma_f32_16x16x32_bf16 v[160:163], a[188:191], a[152:155], v[160:163]\n" \
"ds_read_b64_tr_b16 a[228:229], v33 offset:36864\n" \
"v_mfma_f32_16x16x32_bf16 v[160:163], a[252:255], a[216:219], v[160:163]\n" \
"ds_read_b128 a[152:155], v28 offset:12288\n" \
"v_mfma_f32_16x16x32_bf16 v[176:179], a[188:191], a[156:159], v[176:179]\n" \
"ds_read_b128 a[216:219], v29 offset:12288\n" \
"v_mfma_f32_16x16x32_bf16 v[176:179], a[252:255], a[220:223], v[176:179]\n" \
"s_addk_i32 s84, 0x7c00\n" \
"v_mfma_f32_16x16x32_bf16 v[172:175], a[184:187], a[156:159], v[172:175]\n" \
"buffer_load_dwordx4 v27, s[68:71], s84 offen offset:2048 lds\n" \
"v_mfma_f32_16x16x32_bf16 v[172:175], a[248:251], a[220:223], v[172:175]\n" \
"ds_read_b64_tr_b16 a[230:231], v33 offset:37376\n" \
"v_mfma_f32_16x16x32_bf16 v[168:171], a[180:183], a[156:159], v[168:171]\n" \
"s_addk_i32 s84, 0x7c00\n" \
"v_mfma_f32_16x16x32_bf16 v[168:171], a[244:247], a[220:223], v[168:171]\n" \
"buffer_load_dwordx4 v27, s[68:71], s84 offen offset:3072 lds\n" \
"v_mfma_f32_16x16x32_bf16 v[164:167], a[176:179], a[156:159], v[164:167]\n" \
"ds_read_b64_tr_b16 a[172:173], v35 offset:32768\n" \
"v_mfma_f32_16x16x32_bf16 v[164:167], a[240:243], a[220:223], v[164:167]\n" \
"ds_read_b128 a[156:159], v28 offset:14336\n" \
"v_mfma_f32_16x16x32_bf16 v[84:87], a[176:179], a[136:139], v[84:87]\n" \
"ds_read_b128 a[220:223], v29 offset:14336\n" \
"v_mfma_f32_16x16x32_bf16 v[84:87], a[240:243], a[200:203], v[84:87]\n" \
"s_add_u32 s88, s42, 0x3000\n" \
"buffer_store_dwordx4 v[244:247], v20, s[80:83], s88 offen offset:2048 sc1\n" \
"v_mfma_f32_16x16x32_bf16 v[88:91], a[180:183], a[136:139], v[88:91]\n" \
"ds_read_b64_tr_b16 a[174:175], v35 offset:33280\n" \
"v_mfma_f32_16x16x32_bf16 v[88:91], a[244:247], a[200:203], v[88:91]\n" \
"ds_read_b64_tr_b16 a[236:237], v35 offset:36864\n" \
"v_mfma_f32_16x16x32_bf16 v[92:95], a[184:187], a[136:139], v[92:95]\n" \
"ds_read_b64_tr_b16 a[238:239], v35 offset:37376\n" \
"v_mfma_f32_16x16x32_bf16 v[92:95], a[248:251], a[200:203], v[92:95]\n" \
"s_add_u32 m0, s30, 0x8000\n" \
"s_movk_i32 s84, 0x380\n" \
"v_mfma_f32_16x16x32_bf16 v[96:99], a[188:191], a[136:139], v[96:99]\n" \
"buffer_load_dwordx4 v24, s[76:79], s84 offen offset:0 lds\n" \
"v_mfma_f32_16x16x32_bf16 v[96:99], a[252:255], a[200:203], v[96:99]\n" \
"ds_read_b128 a[136:139], v28 offset:4096\n" \
"v_mfma_f32_16x16x32_bf16 v[112:115], a[188:191], a[140:143], v[112:115]\n" \
"ds_read_b128 a[200:203], v29 offset:4096\n" \
"v_mfma_f32_16x16x32_bf16 v[112:115], a[252:255], a[204:207], v[112:115]\n" \
"ds_read_b64_tr_b16 a[168:169], v34 offset:32768\n" \
"v_mfma_f32_16x16x32_bf16 v[108:111], a[184:187], a[140:143], v[108:111]\n" \
"s_movk_i32 s84, 0x2380\n" \
"v_mfma_f32_16x16x32_bf16 v[108:111], a[248:251], a[204:207], v[108:111]\n" \
"buffer_load_dwordx4 v25, s[76:79], s84 offen offset:1024 lds\n" \
"v_mfma_f32_16x16x32_bf16 v[104:107], a[180:183], a[140:143], v[104:107]\n" \
"ds_read_b64_tr_b16 a[170:171], v34 offset:33280\n" \
"v_mfma_f32_16x16x32_bf16 v[104:107], a[244:247], a[204:207], v[104:107]\n" \
"ds_read_b64_tr_b16 a[232:233], v34 offset:36864\n" \
"v_mfma_f32_16x16x32_bf16 v[100:103], a[176:179], a[140:143], v[100:103]\n" \
"s_cmp_lg_u32 s12, 0\n" \
"s_cselect_b32 s6, s6, s14\n" \
"v_mfma_f32_16x16x32_bf16 v[100:103], a[240:243], a[204:207], v[100:103]\n" \
"ds_read_b128 a[140:143], v28 offset:6144\n" \
"s_addk_i32 s6, 0x100\n" \
"v_mfma_f32_16x16x32_bf16 v[52:55], a[176:179], a[128:131], v[52:55]\n" \
"ds_read_b128 a[204:207], v29 offset:6144\n" \
"v_writelane_b32 v11, s9, 44\n" \
"v_mfma_f32_16x16x32_bf16 v[52:55], a[240:243], a[192:195], v[52:55]\n" \
"s_movk_i32 s84, 0x4380\n" \
"v_mfma_f32_16x16x32_bf16 v[56:59], a[180:183], a[128:131], v[56:59]\n" \
"buffer_load_dwordx4 v24, s[76:79], s84 offen offset:2048 lds\n" \
"v_mfma_f32_16x16x32_bf16 v[56:59], a[244:247], a[192:195], v[56:59]\n" \
"s_movk_i32 s84, 0x6380\n" \
"v_mfma_f32_16x16x32_bf16 v[60:63], a[184:187], a[128:131], v[60:63]\n" \
"buffer_load_dwordx4 v25, s[76:79], s84 offen offset:3072 lds\n" \
"v_mfma_f32_16x16x32_bf16 v[60:63], a[248:251], a[192:195], v[60:63]\n" \
"ds_read_b64_tr_b16 a[234:235], v34 offset:37376\n" \
"v_mfma_f32_16x16x32_bf16 v[64:67], a[188:191], a[128:131], v[64:67]\n" \
"s_cmp_lg_u32 s16, 0\n" \
"s_cselect_b32 s6, s7, s6\n" \
"v_mfma_f32_16x16x32_bf16 v[64:67], a[252:255], a[192:195], v[64:67]\n" \
"ds_read_b128 a[128:131], v28 offset:0\n" \
"v_writelane_b32 v11, s6, 45\n" \
"v_mfma_f32_16x16x32_bf16 v[80:83], a[188:191], a[132:135], v[80:83]\n" \
"ds_read_b128 a[192:195], v29 offset:0\n" \
"s_or_b32 s6, s8, s15\n" \
"v_mfma_f32_16x16x32_bf16 v[80:83], a[252:255], a[196:199], v[80:83]\n" \
"v_writelane_b32 v11, s6, 53\n" \
"s_lshl_b32 s6, s3, 17\n" \
"v_mfma_f32_16x16x32_bf16 v[76:79], a[184:187], a[132:135], v[76:79]\n" \
"s_add_i32 s6, s13, s6\n" \
"s_or_b32 s5, s6, s5\n" \
"v_mfma_f32_16x16x32_bf16 v[76:79], a[248:251], a[196:199], v[76:79]\n" \
"s_or_b32 s5, s5, 0x3840\n" \
"v_writelane_b32 v11, s5, 6\n" \
"v_mfma_f32_16x16x32_bf16 v[72:75], a[180:183], a[132:135], v[72:75]\n" \
"s_mov_b32 s5, 0xff0000\n" \
"v_mfma_f32_16x16x32_bf16 v[72:75], a[244:247], a[196:199], v[72:75]\n" \
"s_cmp_lg_u32 s16, 0\n" \
"s_cselect_b32 s5, s17, s5\n" \
"v_mfma_f32_16x16x32_bf16 v[68:71], a[176:179], a[132:135], v[68:71]\n" \
"v_writelane_b32 v11, s5, 7\n" \
"s_mul_i32 s5, s3, 0x1200\n" \
"v_mfma_f32_16x16x32_bf16 v[68:71], a[240:243], a[196:199], v[68:71]\n" \
"ds_read_b128 a[132:135], v28 offset:2048\n" \
"ds_read_b128 a[196:199], v29 offset:2048\n" \
"s_waitcnt lgkmcnt(15)\n" \
"v_mfma_f32_16x16x32_bf16 a[0:3], a[160:163], a[144:147], a[0:3]\n" \
"s_add_u32 m0, s30, 0x9000\n" \
"s_addk_i32 s84, 0x3000\n" \
"v_mfma_f32_16x16x32_bf16 a[0:3], a[224:227], a[208:211], a[0:3]\n" \
"buffer_load_dwordx4 v24, s[76:79], s84 offen offset:0 lds\n" \
"v_mfma_f32_16x16x32_bf16 a[4:7], a[164:167], a[144:147], a[4:7]\n" \
"ds_read_b64_tr_b16 a[176:177], v36 offset:32768\n" \
"s_add_i32 s4, s5, s4\n" \
"v_mfma_f32_16x16x32_bf16 a[4:7], a[228:231], a[208:211], a[4:7]\n" \
"ds_read_b64_tr_b16 a[178:179], v36 offset:33792\n" \
"s_add_i32 s4, s4, s11\n" \
"s_waitcnt lgkmcnt(15)\n" \
"v_mfma_f32_16x16x32_bf16 a[12:15], a[172:175], a[144:147], a[12:15]\n" \
"ds_read_b64_tr_b16 a[240:241], v36 offset:40960\n" \
"v_writelane_b32 v11, s4, 8\n" \
"s_waitcnt lgkmcnt(15)\n" \
"v_mfma_f32_16x16x32_bf16 a[12:15], a[236:239], a[208:211], a[12:15]\n" \
"s_add_u32 s89, s42, 0x3000\n" \
"buffer_store_dwordx4 v[184:187], v20, s[80:83], s89 offen offset:1024 sc1\n" \
"s_waitcnt lgkmcnt(11)\n" \
"v_mfma_f32_16x16x32_bf16 a[8:11], a[144:147], a[168:171], a[8:11]\n" \
"ds_read_b64_tr_b16 a[242:243], v36 offset:41984\n" \
"s_lshl_b32 s4, s2, 16\n" \
"s_waitcnt lgkmcnt(8)\n" \
"v_mfma_f32_16x16x32_bf16 a[8:11], a[208:211], a[232:235], a[8:11]\n" \
"ds_read_b64_tr_b16 a[180:181], v37 offset:32768\n" \
"s_add_i32 s5, s4, 0x10000\n" \
"v_mfma_f32_16x16x32_bf16 a[24:27], a[148:151], a[168:171], a[24:27]\n" \
"ds_read_b64_tr_b16 a[182:183], v37 offset:33792\n" \
"s_and_b32 s5, s5, 0x70000\n" \
"v_mfma_f32_16x16x32_bf16 a[24:27], a[212:215], a[232:235], a[24:27]\n" \
"ds_read_b64_tr_b16 a[244:245], v37 offset:40960\n" \
"s_add_i32 s5, s13, s5\n" \
"v_mfma_f32_16x16x32_bf16 a[28:31], a[172:175], a[148:151], a[28:31]\n" \
"v_writelane_b32 v11, s5, 46\n" \
"s_add_i32 s5, s4, 0x20000\n" \
"v_mfma_f32_16x16x32_bf16 a[28:31], a[236:239], a[212:215], a[28:31]\n" \
"s_addk_i32 s84, 0x2000\n" \
"v_mfma_f32_16x16x32_bf16 a[20:23], a[164:167], a[148:151], a[20:23]\n" \
"buffer_load_dwordx4 v25, s[76:79], s84 offen offset:1024 lds\n" \
"v_mfma_f32_16x16x32_bf16 a[20:23], a[228:231], a[212:215], a[20:23]\n" \
"ds_read_b64_tr_b16 a[246:247], v37 offset:41984\n" \
"s_and_b32 s5, s5, 0x70000\n" \
"v_mfma_f32_16x16x32_bf16 a[16:19], a[160:163], a[148:151], a[16:19]\n" \
"ds_read_b64_tr_b16 a[184:185], v38 offset:32768\n" \
"s_add_i32 s5, s13, s5\n" \
"v_mfma_f32_16x16x32_bf16 a[16:19], a[224:227], a[212:215], a[16:19]\n" \
"ds_read_b64_tr_b16 a[186:187], v38 offset:33792\n" \
"s_bitset1_b32 s5, 11\n" \
"v_mfma_f32_16x16x32_bf16 a[32:35], a[160:163], a[152:155], a[32:35]\n" \
"s_addk_i32 s84, 0x2000\n" \
"s_nop 0\n" \
"v_mfma_f32_16x16x32_bf16 a[32:35], a[224:227], a[216:219], a[32:35]\n" \
"buffer_load_dwordx4 v24, s[76:79], s84 offen offset:2048 lds\n" \
"v_mfma_f32_16x16x32_bf16 a[36:39], a[164:167], a[152:155], a[36:39]\n" \
"ds_read_b64_tr_b16 a[248:249], v38 offset:40960\n" \
"v_writelane_b32 v11, s5, 47\n" \
"v_mfma_f32_16x16x32_bf16 a[36:39], a[228:231], a[216:219], a[36:39]\n" \
"ds_read_b64_tr_b16 a[250:251], v38 offset:41984\n" \
"s_add_i32 s5, s4, 0x30000\n" \
"v_mfma_f32_16x16x32_bf16 a[44:47], a[172:175], a[152:155], a[44:47]\n" \
"ds_read_b64_tr_b16 a[188:189], v39 offset:32768\n" \
"s_and_b32 s5, s5, 0x70000\n" \
"v_mfma_f32_16x16x32_bf16 a[44:47], a[236:239], a[216:219], a[44:47]\n" \
"ds_read_b64_tr_b16 a[190:191], v39 offset:33792\n" \
"s_add_i32 s5, s13, s5\n" \
"v_mfma_f32_16x16x32_bf16 a[40:43], a[152:155], a[168:171], a[40:43]\n" \
"s_add_u32 s88, s42, 0x3000\n" \
"buffer_store_dwordx4 v[188:191], v20, s[80:83], s88 offen offset:0 sc1\n" \
"v_mfma_f32_16x16x32_bf16 a[40:43], a[216:219], a[232:235], a[40:43]\n" \
"ds_read_b64_tr_b16 a[252:253], v39 offset:40960\n" \
"s_bitset1_b32 s5, 12\n" \
"v_mfma_f32_16x16x32_bf16 a[56:59], a[156:159], a[168:171], a[56:59]\n" \
"ds_read_b64_tr_b16 a[254:255], v39 offset:41984\n" \
"v_writelane_b32 v11, s5, 48\n" \
"v_mfma_f32_16x16x32_bf16 a[56:59], a[220:223], a[232:235], a[56:59]\n" \
"s_and_b32 s5, s4, 0x70000\n" \
"s_xor_b32 s5, s5, 0x40000\n" \
"v_mfma_f32_16x16x32_bf16 a[60:63], a[172:175], a[156:159], a[60:63]\n" \
"s_add_i32 s5, s13, s5\n" \
"s_or_b32 s5, s5, 0x1800\n" \
"v_mfma_f32_16x16x32_bf16 a[60:63], a[236:239], a[220:223], a[60:63]\n" \
"s_addk_i32 s84, 0x2000\n" \
"v_mfma_f32_16x16x32_bf16 a[52:55], a[164:167], a[156:159], a[52:55]\n" \
"buffer_load_dwordx4 v25, s[76:79], s84 offen offset:3072 lds\n" \
"v_mfma_f32_16x16x32_bf16 a[52:55], a[228:231], a[220:223], a[52:55]\n" \
"v_writelane_b32 v11, s5, 49\n" \
"s_add_i32 s5, s4, 0x50000\n" \
"v_mfma_f32_16x16x32_bf16 a[48:51], a[160:163], a[156:159], a[48:51]\n" \
"s_and_b32 s5, s5, 0x70000\n" \
"s_add_i32 s5, s13, s5\n" \
"v_mfma_f32_16x16x32_bf16 a[48:51], a[224:227], a[220:223], a[48:51]\n" \
"s_bitset1_b32 s5, 13\n" \
"v_writelane_b32 v11, s5, 50\n" \
"v_mfma_f32_16x16x32_bf16 a[64:67], a[164:167], a[140:143], a[64:67]\n" \
"s_add_i32 s5, s4, 0x60000\n" \
"s_and_b32 s5, s5, 0x70000\n" \
"v_mfma_f32_16x16x32_bf16 a[64:67], a[228:231], a[204:207], a[64:67]\n" \
"s_add_i32 s4, s4, 0x70000\n" \
"s_add_i32 s5, s13, s5\n" \
"v_mfma_f32_16x16x32_bf16 a[80:83], a[172:175], a[140:143], a[80:83]\n" \
"s_and_b32 s4, s4, 0x70000\n" \
"s_or_b32 s5, s5, 0x2800\n" \
"v_mfma_f32_16x16x32_bf16 a[80:83], a[236:239], a[204:207], a[80:83]\n" \
"v_writelane_b32 v11, s5, 51\n" \
"s_add_i32 s13, s13, s4\n" \
"v_mfma_f32_16x16x32_bf16 a[68:71], a[140:143], a[168:171], a[68:71]\n" \
"s_or_b32 s4, s13, 0x3000\n" \
"v_writelane_b32 v11, s4, 52\n" \
"v_mfma_f32_16x16x32_bf16 a[68:71], a[204:207], a[232:235], a[68:71]\n" \
"s_waitcnt vmcnt(11) lgkmcnt(0)\n" \
"s_barrier\n" \
"v_xor_b32_e32 v28, 0x20000, v28\n" \
"v_xor_b32_e32 v29, 0x20000, v29\n" \
"v_mfma_f32_16x16x32_bf16 v[116:119], a[176:179], a[144:147], v[116:119]\n" \
"s_add_u32 m0, s40, 0x8000\n" \
"s_add_u32 s85, s39, 0xe000\n" \
"v_mfma_f32_16x16x32_bf16 v[116:119], a[240:243], a[208:211], v[116:119]\n" \
"buffer_load_dwordx4 v20, s[80:83], s85 offen sc1 lds\n" \
"v_mfma_f32_16x16x32_bf16 v[120:123], a[180:183], a[144:147], v[120:123]\n" \
"ds_read_b64_tr_b16 a[160:161], v32 offset:0\n" \
"s_movk_i32 s11, 0x4000\n" \
"v_mfma_f32_16x16x32_bf16 v[120:123], a[244:247], a[208:211], v[120:123]\n" \
"ds_read_b64_tr_b16 a[162:163], v32 offset:512\n" \
"s_lshl_b32 s4, s2, 2\n" \
"v_mfma_f32_16x16x32_bf16 v[124:127], a[184:187], a[144:147], v[124:127]\n" \
"s_add_u32 m0, s40, 0x9000\n" \
"s_add_u32 s85, s39, 0xf000\n" \
"v_mfma_f32_16x16x32_bf16 v[124:127], a[248:251], a[208:211], v[124:127]\n" \
"buffer_load_dwordx4 v20, s[80:83], s85 offen sc1 lds\n" \
"v_mfma_f32_16x16x32_bf16 v[128:131], a[188:191], a[144:147], v[128:131]\n" \
"ds_read_b64_tr_b16 a[224:225], v32 offset:4096\n" \
"s_and_b32 s4, s4, 28\n" \
"v_mfma_f32_16x16x32_bf16 v[128:131], a[252:255], a[208:211], v[128:131]\n" \
"ds_read_b128 a[144:147], v28 offset:8192\n" \
"s_bfe_u32 s5, s2, 0x20003\n" \
"v_mfma_f32_16x16x32_bf16 v[144:147], a[188:191], a[148:151], v[144:147]\n" \
"ds_read_b128 a[208:211], v29 offset:8192\n" \
"s_or_b32 s4, s4, s5\n" \
"v_mfma_f32_16x16x32_bf16 v[144:147], a[252:255], a[212:215], v[144:147]\n" \
"s_add_u32 m0, s40, 0xb000\n" \
"s_movk_i32 s84, 0x380\n" \
"v_mfma_f32_16x16x32_bf16 v[140:143], a[184:187], a[148:151], v[140:143]\n" \
"buffer_load_dwordx4 v21, s[76:79], s84 offen offset:0 lds\n" \
"v_mfma_f32_16x16x32_bf16 v[140:143], a[248:251], a[212:215], v[140:143]\n" \
"ds_read_b64_tr_b16 a[226:227], v32 offset:4608\n" \
"s_lshr_b32 s5, s2, 5\n" \
"v_mfma_f32_16x16x32_bf16 v[136:139], a[180:183], a[148:151], v[136:139]\n" \
"s_lshr_b32 s19, s3, 1\n" \
"s_lshl_b32 s18, s5, 1\n" \
"v_mfma_f32_16x16x32_bf16 v[136:139], a[244:247], a[212:215], v[136:139]\n" \
"ds_read_b64_tr_b16 a[164:165], v33 offset:0\n" \
"s_lshl_b32 s6, s4, 20\n" \
"v_mfma_f32_16x16x32_bf16 v[132:135], a[176:179], a[148:151], v[132:135]\n" \
"s_lshl_b32 s7, s4, 8\n" \
"s_and_b32 s13, s3, 1\n" \
"v_mfma_f32_16x16x32_bf16 v[132:135], a[240:243], a[212:215], v[132:135]\n" \
"ds_read_b128 a[148:151], v28 offset:10240\n" \
"s_add_i32 s14, s18, s19\n" \
"v_mfma_f32_16x16x32_bf16 v[148:151], a[176:179], a[152:155], v[148:151]\n" \
"ds_read_b128 a[212:215], v29 offset:10240\n" \
"v_mfma_f32_16x16x32_bf16 v[148:151], a[240:243], a[216:219], v[148:151]\n" \
"s_add_u32 m0, s31, 0x8000\n" \
"s_mov_b32 s84, 0x380000\n" \
"v_mfma_f32_16x16x32_bf16 v[152:155], a[180:183], a[152:155], v[152:155]\n" \
"buffer_load_dwordx4 v26, s[68:71], s84 offen offset:0 lds\n" \
"v_mfma_f32_16x16x32_bf16 v[152:155], a[244:247], a[216:219], v[152:155]\n" \
"s_addk_i32 s84, 0x7c00\n" \
"s_nop 0\n" \
"v_mfma_f32_16x16x32_bf16 v[156:159], a[184:187], a[152:155], v[156:159]\n" \
"buffer_load_dwordx4 v26, s[68:71], s84 offen offset:1024 lds\n" \
"v_mfma_f32_16x16x32_bf16 v[156:159], a[248:251], a[216:219], v[156:159]\n" \
"ds_read_b64_tr_b16 a[166:167], v33 offset:512\n" \
"v_mfma_f32_16x16x32_bf16 v[160:163], a[188:191], a[152:155], v[160:163]\n" \
"ds_read_b64_tr_b16 a[228:229], v33 offset:4096\n" \
"v_mfma_f32_16x16x32_bf16 v[160:163], a[252:255], a[216:219], v[160:163]\n" \
"ds_read_b128 a[152:155], v28 offset:12288\n" \
"v_mfma_f32_16x16x32_bf16 v[176:179], a[188:191], a[156:159], v[176:179]\n" \
"ds_read_b128 a[216:219], v29 offset:12288\n" \
"v_mfma_f32_16x16x32_bf16 v[176:179], a[252:255], a[220:223], v[176:179]\n" \
"s_addk_i32 s84, 0x7c00\n" \
"v_mfma_f32_16x16x32_bf16 v[172:175], a[184:187], a[156:159], v[172:175]\n" \
"buffer_load_dwordx4 v27, s[68:71], s84 offen offset:2048 lds\n" \
"v_mfma_f32_16x16x32_bf16 v[172:175], a[248:251], a[220:223], v[172:175]\n" \
"ds_read_b64_tr_b16 a[230:231], v33 offset:4608\n" \
"v_mfma_f32_16x16x32_bf16 v[168:171], a[180:183], a[156:159], v[168:171]\n" \
"s_addk_i32 s84, 0x7c00\n" \
"v_mfma_f32_16x16x32_bf16 v[168:171], a[244:247], a[220:223], v[168:171]\n" \
"buffer_load_dwordx4 v27, s[68:71], s84 offen offset:3072 lds\n" \
"v_mfma_f32_16x16x32_bf16 v[164:167], a[176:179], a[156:159], v[164:167]\n" \
"ds_read_b64_tr_b16 a[172:173], v35 offset:0\n" \
"v_mfma_f32_16x16x32_bf16 v[164:167], a[240:243], a[220:223], v[164:167]\n" \
"ds_read_b128 a[156:159], v28 offset:14336\n" \
"v_mfma_f32_16x16x32_bf16 v[84:87], a[176:179], a[136:139], v[84:87]\n" \
"ds_read_b128 a[220:223], v29 offset:14336\n" \
"v_mfma_f32_16x16x32_bf16 v[84:87], a[240:243], a[200:203], v[84:87]\n" \
"s_add_u32 s89, s42, 0x0\n" \
"buffer_store_dwordx4 v[192:195], v20, s[80:83], s89 offen offset:2048 sc1\n" \
"v_mfma_f32_16x16x32_bf16 v[88:91], a[180:183], a[136:139], v[88:91]\n" \
"ds_read_b64_tr_b16 a[174:175], v35 offset:512\n" \
"v_mfma_f32_16x16x32_bf16 v[88:91], a[244:247], a[200:203], v[88:91]\n" \
"ds_read_b64_tr_b16 a[236:237], v35 offset:4096\n" \
"v_mfma_f32_16x16x32_bf16 v[92:95], a[184:187], a[136:139], v[92:95]\n" \
"ds_read_b64_tr_b16 a[238:239], v35 offset:4608\n" \
"v_mfma_f32_16x16x32_bf16 v[92:95], a[248:251], a[200:203], v[92:95]\n" \
"s_add_u32 m0, s30, 0x20000\n" \
"s_movk_i32 s84, 0x400\n" \
"v_mfma_f32_16x16x32_bf16 v[96:99], a[188:191], a[136:139], v[96:99]\n" \
"buffer_load_dwordx4 v24, s[76:79], s84 offen offset:0 lds\n" \
"v_mfma_f32_16x16x32_bf16 v[96:99], a[252:255], a[200:203], v[96:99]\n" \
"ds_read_b128 a[136:139], v28 offset:4096\n" \
"v_mfma_f32_16x16x32_bf16 v[112:115], a[188:191], a[140:143], v[112:115]\n" \
"ds_read_b128 a[200:203], v29 offset:4096\n" \
"v_mfma_f32_16x16x32_bf16 v[112:115], a[252:255], a[204:207], v[112:115]\n" \
"ds_read_b64_tr_b16 a[168:169], v34 offset:0\n" \
"v_mfma_f32_16x16x32_bf16 v[108:111], a[184:187], a[140:143], v[108:111]\n" \
"s_movk_i32 s84, 0x2400\n" \
"v_mfma_f32_16x16x32_bf16 v[108:111], a[248:251], a[204:207], v[108:111]\n" \
"buffer_load_dwordx4 v25, s[76:79], s84 offen offset:1024 lds\n" \
"v_mfma_f32_16x16x32_bf16 v[104:107], a[180:183], a[140:143], v[104:107]\n" \
"ds_read_b64_tr_b16 a[170:171], v34 offset:512\n" \
"v_mfma_f32_16x16x32_bf16 v[104:107], a[244:247], a[204:207], v[104:107]\n" \
"ds_read_b64_tr_b16 a[232:233], v34 offset:4096\n" \
"v_mfma_f32_16x16x32_bf16 v[100:103], a[176:179], a[140:143], v[100:103]\n" \
"s_add_u32 s6, s26, s6\n" \
"s_addc_u32 s9, s27, 0\n" \
"v_mfma_f32_16x16x32_bf16 v[100:103], a[240:243], a[204:207], v[100:103]\n" \
"ds_read_b128 a[140:143], v28 offset:6144\n" \
"v_mfma_f32_16x16x32_bf16 v[52:55], a[176:179], a[128:131], v[52:55]\n" \
"ds_read_b128 a[204:207], v29 offset:6144\n" \
"v_mfma_f32_16x16x32_bf16 v[52:55], a[240:243], a[192:195], v[52:55]\n" \
"s_movk_i32 s84, 0x4400\n" \
"v_mfma_f32_16x16x32_bf16 v[56:59], a[180:183], a[128:131], v[56:59]\n" \
"buffer_load_dwordx4 v24, s[76:79], s84 offen offset:2048 lds\n" \
"v_mfma_f32_16x16x32_bf16 v[56:59], a[244:247], a[192:195], v[56:59]\n" \
"s_movk_i32 s84, 0x6400\n" \
"v_mfma_f32_16x16x32_bf16 v[60:63], a[184:187], a[128:131], v[60:63]\n" \
"buffer_load_dwordx4 v25, s[76:79], s84 offen offset:3072 lds\n" \
"v_mfma_f32_16x16x32_bf16 v[60:63], a[248:251], a[192:195], v[60:63]\n" \
"ds_read_b64_tr_b16 a[234:235], v34 offset:4608\n" \
"v_mfma_f32_16x16x32_bf16 v[64:67], a[188:191], a[128:131], v[64:67]\n" \
"s_add_u32 s8, s6, 0x1000000\n" \
"s_addc_u32 s6, s9, 0\n" \
"v_mfma_f32_16x16x32_bf16 v[64:67], a[252:255], a[192:195], v[64:67]\n" \
"ds_read_b128 a[128:131], v28 offset:0\n" \
"s_and_b32 s9, s6, 0xffff\n" \
"v_mfma_f32_16x16x32_bf16 v[80:83], a[188:191], a[132:135], v[80:83]\n" \
"ds_read_b128 a[192:195], v29 offset:0\n" \
"s_lshl_b32 s6, s5, 16\n" \
"v_mfma_f32_16x16x32_bf16 v[80:83], a[252:255], a[196:199], v[80:83]\n" \
"s_sub_i32 s10, 0x100000, s6\n" \
"s_mov_b32 s6, 0x8000\n" \
"v_mfma_f32_16x16x32_bf16 v[76:79], a[184:187], a[132:135], v[76:79]\n" \
"s_cmp_lg_u32 s5, 0\n" \
"s_cselect_b32 s17, s11, s6\n" \
"v_mfma_f32_16x16x32_bf16 v[76:79], a[248:251], a[196:199], v[76:79]\n" \
"s_lshl_b32 s6, s3, 1\n" \
"s_lshl_b32 s5, s5, 3\n" \
"v_mfma_f32_16x16x32_bf16 v[72:75], a[180:183], a[132:135], v[72:75]\n" \
"s_and_b32 s6, s6, 4\n" \
"s_or_b32 s12, s5, s6\n" \
"v_mfma_f32_16x16x32_bf16 v[72:75], a[244:247], a[196:199], v[72:75]\n" \
"s_sub_i32 s5, s13, s5\n" \
"s_or_b32 s5, s5, s6\n" \
"v_mfma_f32_16x16x32_bf16 v[68:71], a[176:179], a[132:135], v[68:71]\n" \
"s_or_b32 s6, s12, s13\n" \
"s_lshl_b32 s4, s4, 2\n" \
"v_mfma_f32_16x16x32_bf16 v[68:71], a[240:243], a[196:199], v[68:71]\n" \
"ds_read_b128 a[132:135], v28 offset:2048\n" \
"ds_read_b128 a[196:199], v29 offset:2048\n" \
"s_waitcnt lgkmcnt(15)\n" \
"v_mfma_f32_16x16x32_bf16 a[0:3], a[160:163], a[144:147], a[0:3]\n" \
"s_add_u32 m0, s30, 0x21000\n" \
"s_addk_i32 s84, 0x3000\n" \
"v_mfma_f32_16x16x32_bf16 a[0:3], a[224:227], a[208:211], a[0:3]\n" \
"buffer_load_dwordx4 v24, s[76:79], s84 offen offset:0 lds\n" \
"v_mfma_f32_16x16x32_bf16 a[4:7], a[164:167], a[144:147], a[4:7]\n" \
"ds_read_b64_tr_b16 a[176:177], v36 offset:0\n" \
"s_mulk_i32 s6, 0x92\n" \
"v_mfma_f32_16x16x32_bf16 a[4:7], a[228:231], a[208:211], a[4:7]\n" \
"ds_read_b64_tr_b16 a[178:179], v36 offset:1024\n" \
"s_add_i32 s4, s4, 18\n" \
"s_waitcnt lgkmcnt(15)\n" \
"v_mfma_f32_16x16x32_bf16 a[12:15], a[172:175], a[144:147], a[12:15]\n" \
"ds_read_b64_tr_b16 a[240:241], v36 offset:8192\n" \
"s_mulk_i32 s5, 0x92\n" \
"s_waitcnt lgkmcnt(15)\n" \
"v_mfma_f32_16x16x32_bf16 a[12:15], a[236:239], a[208:211], a[12:15]\n" \
"s_add_u32 s88, s42, 0x0\n" \
"buffer_store_dwordx4 v[196:199], v20, s[80:83], s88 offen offset:3072 sc1\n" \
"s_waitcnt lgkmcnt(11)\n" \
"v_mfma_f32_16x16x32_bf16 a[8:11], a[144:147], a[168:171], a[8:11]\n" \
"ds_read_b64_tr_b16 a[242:243], v36 offset:9216\n" \
"s_add_i32 s6, s4, s6\n" \
"s_waitcnt lgkmcnt(8)\n" \
"v_mfma_f32_16x16x32_bf16 a[8:11], a[208:211], a[232:235], a[8:11]\n" \
"ds_read_b64_tr_b16 a[180:181], v37 offset:0\n" \
"s_add_i32 s5, s5, s4\n" \
"v_mfma_f32_16x16x32_bf16 a[24:27], a[148:151], a[168:171], a[24:27]\n" \
"ds_read_b64_tr_b16 a[182:183], v37 offset:1024\n" \
"s_lshl_b32 s4, s3, 11\n" \
"v_mfma_f32_16x16x32_bf16 a[24:27], a[212:215], a[232:235], a[24:27]\n" \
"ds_read_b64_tr_b16 a[244:245], v37 offset:8192\n" \
"s_lshl_b32 s15, s13, 12\n" \
"v_mfma_f32_16x16x32_bf16 a[28:31], a[172:175], a[148:151], a[28:31]\n" \
"s_lshl_b32 s16, s19, 14\n" \
"s_mul_i32 s19, s19, 0xe000\n" \
"v_mfma_f32_16x16x32_bf16 a[28:31], a[236:239], a[212:215], a[28:31]\n" \
"s_addk_i32 s84, 0x2000\n" \
"v_mfma_f32_16x16x32_bf16 a[20:23], a[164:167], a[148:151], a[20:23]\n" \
"buffer_load_dwordx4 v25, s[76:79], s84 offen offset:1024 lds\n" \
"v_mfma_f32_16x16x32_bf16 a[20:23], a[228:231], a[212:215], a[20:23]\n" \
"ds_read_b64_tr_b16 a[246:247], v37 offset:9216\n" \
"s_mov_b32 s11, 0x20000\n" \
"v_mfma_f32_16x16x32_bf16 a[16:19], a[160:163], a[148:151], a[16:19]\n" \
"ds_read_b64_tr_b16 a[184:185], v38 offset:0\n" \
"s_or_b32 s12, s4, 0xc000\n" \
"v_mfma_f32_16x16x32_bf16 a[16:19], a[224:227], a[212:215], a[16:19]\n" \
"ds_read_b64_tr_b16 a[186:187], v38 offset:1024\n" \
"s_lshl_b32 s4, s3, 8\n" \
"v_mfma_f32_16x16x32_bf16 a[32:35], a[160:163], a[152:155], a[32:35]\n" \
"s_addk_i32 s84, 0x2000\n" \
"v_mfma_f32_16x16x32_bf16 a[32:35], a[224:227], a[216:219], a[32:35]\n" \
"buffer_load_dwordx4 v24, s[76:79], s84 offen offset:2048 lds\n" \
"v_mfma_f32_16x16x32_bf16 a[36:39], a[164:167], a[152:155], a[36:39]\n" \
"ds_read_b64_tr_b16 a[248:249], v38 offset:8192\n" \
"s_or_b32 s22, s15, s16\n" \
"v_mfma_f32_16x16x32_bf16 a[36:39], a[228:231], a[216:219], a[36:39]\n" \
"ds_read_b64_tr_b16 a[250:251], v38 offset:9216\n" \
"s_or_b32 s23, s19, s15\n" \
"v_mfma_f32_16x16x32_bf16 a[44:47], a[172:175], a[152:155], a[44:47]\n" \
"ds_read_b64_tr_b16 a[188:189], v39 offset:0\n" \
"s_lshl_b32 s60, s6, 10\n" \
"v_mfma_f32_16x16x32_bf16 a[44:47], a[236:239], a[216:219], a[44:47]\n" \
"ds_read_b64_tr_b16 a[190:191], v39 offset:1024\n" \
"s_lshl_b32 s61, s5, 10\n" \
"v_mfma_f32_16x16x32_bf16 a[40:43], a[152:155], a[168:171], a[40:43]\n" \
"s_add_u32 s89, s42, 0x2000\n" \
"buffer_store_dwordx4 v[200:203], v20, s[80:83], s89 offen offset:0 sc1\n" \
"v_mfma_f32_16x16x32_bf16 a[40:43], a[216:219], a[232:235], a[40:43]\n" \
"ds_read_b64_tr_b16 a[252:253], v39 offset:8192\n" \
"s_lshl_b32 s14, s14, 15\n" \
"v_mfma_f32_16x16x32_bf16 a[56:59], a[156:159], a[168:171], a[56:59]\n" \
"ds_read_b64_tr_b16 a[254:255], v39 offset:9216\n" \
"s_or_b32 s20, s4, 0x26c00\n" \
"v_mfma_f32_16x16x32_bf16 a[56:59], a[220:223], a[232:235], a[56:59]\n" \
"s_or_b32 s13, s22, 0x8000\n" \
"s_add_i32 s22, s22, 0x14000\n" \
"v_mfma_f32_16x16x32_bf16 a[60:63], a[172:175], a[156:159], a[60:63]\n" \
"s_bitset1_b32 s23, 16\n" \
"s_or_b32 s24, s7, 0x5be8020\n" \
"v_mfma_f32_16x16x32_bf16 a[60:63], a[236:239], a[220:223], a[60:63]\n" \
"s_addk_i32 s84, 0x2000\n" \
"v_mfma_f32_16x16x32_bf16 a[52:55], a[164:167], a[156:159], a[52:55]\n" \
"buffer_load_dwordx4 v25, s[76:79], s84 offen offset:3072 lds\n" \
"v_mfma_f32_16x16x32_bf16 a[52:55], a[228:231], a[220:223], a[52:55]\n" \
"s_and_b32 s57, s27, 0xffff\n" \
".fill (L_pb + 73440 - .) / 4, 4, 0xBF800000\n" \
"L_pp18:\n" \
"s_cbranch_execz L_pret\n" \
"s_mov_b32 s58, 0x6000000\n" \
"v_mfma_f32_16x16x32_bf16 a[48:51], a[160:163], a[156:159], a[48:51]\n" \
"s_mov_b32 s56, s26\n" \
"s_mov_b32 s59, s11\n" \
"v_mfma_f32_16x16x32_bf16 a[48:51], a[224:227], a[220:223], a[48:51]\n" \
"s_add_i32 s60, s60, 0x3000000\n" \
"s_add_i32 s61, s61, 0x411c000\n" \
"v_mfma_f32_16x16x32_bf16 a[64:67], a[164:167], a[140:143], a[64:67]\n" \
"s_sub_i32 s19, 14, s18\n" \
"s_mov_b32 s21, 0x71800000\n" \
"v_mfma_f32_16x16x32_bf16 a[64:67], a[228:231], a[204:207], a[64:67]\n" \
"s_mov_b32 s4, L_gsw-L_gtr\n" \
"v_mfma_f32_16x16x32_bf16 a[80:83], a[172:175], a[140:143], a[80:83]\n" \
"s_cmp_eq_u32 s19, 0\n" \
"s_cselect_b32 s4, L_gs7-L_gtr, s4\n" \
"v_mfma_f32_16x16x32_bf16 a[80:83], a[236:239], a[204:207], a[80:83]\n" \
"s_cmp_eq_u32 s16, 0\n" \
"s_cselect_b32 s4, 0, s4\n" \
"v_mfma_f32_16x16x32_bf16 a[68:71], a[140:143], a[168:171], a[68:71]\n" \
"s_getpc_b64 s[28:29]\n" \
"L_gp1_%=:\n" \
"s_add_u32 s28, s28, L_gtr-L_gp1_%=\n" \
"s_addc_u32 s29, s29, 0\n" \
"v_mfma_f32_16x16x32_bf16 a[68:71], a[204:207], a[232:235], a[68:71]\n" \
"s_waitcnt vmcnt(11) lgkmcnt(0)\n" \
"s_barrier\n" \
"v_mfma_f32_16x16x32_bf16 v[116:119], a[176:179], a[144:147], v[116:119]\n" \
"s_add_u32 m0, s40, 0x0\n" \
"s_add_u32 s85, s39, 0x10000\n" \
"v_mfma_f32_16x16x32_bf16 v[116:119], a[240:243], a[208:211], v[116:119]\n" \
"buffer_load_dwordx4 v20, s[80:83], s85 offen sc1 lds\n" \
"v_mfma_f32_16x16x32_bf16 v[120:123], a[180:183], a[144:147], v[120:123]\n" \
"ds_read_b64_tr_b16 a[160:161], v32 offset:32768\n" \
"v_mfma_f32_16x16x32_bf16 v[120:123], a[244:247], a[208:211], v[120:123]\n" \
"ds_read_b64_tr_b16 a[162:163], v32 offset:33280\n" \
"v_mfma_f32_16x16x32_bf16 v[124:127], a[184:187], a[144:147], v[124:127]\n" \
"s_add_u32 s28, s28, s4\n" \
"s_addc_u32 s29, s29, 0\n" \
"v_mfma_f32_16x16x32_bf16 v[124:127], a[248:251], a[208:211], v[124:127]\n" \
"v_mfma_f32_16x16x32_bf16 v[128:131], a[188:191], a[144:147], v[128:131]\n" \
"ds_read_b64_tr_b16 a[224:225], v32 offset:36864\n" \
"v_mfma_f32_16x16x32_bf16 v[128:131], a[252:255], a[208:211], v[128:131]\n" \
"ds_read_b128 a[144:147], v28 offset:40960\n" \
"v_mfma_f32_16x16x32_bf16 v[144:147], a[188:191], a[148:151], v[144:147]\n" \
"ds_read_b128 a[208:211], v29 offset:40960\n" \
"v_mfma_f32_16x16x32_bf16 v[144:147], a[252:255], a[212:215], v[144:147]\n" \
"s_add_u32 m0, s40, 0x1000\n" \
"s_add_u32 s85, s39, 0x11000\n" \
"v_mfma_f32_16x16x32_bf16 v[140:143], a[184:187], a[148:151], v[140:143]\n" \
"buffer_load_dwordx4 v20, s[80:83], s85 offen sc1 lds\n" \
"v_mfma_f32_16x16x32_bf16 v[140:143], a[248:251], a[212:215], v[140:143]\n" \
"ds_read_b64_tr_b16 a[226:227], v32 offset:37376\n" \
"v_mfma_f32_16x16x32_bf16 v[136:139], a[180:183], a[148:151], v[136:139]\n" \
"v_mfma_f32_16x16x32_bf16 v[136:139], a[244:247], a[212:215], v[136:139]\n" \
"ds_read_b64_tr_b16 a[164:165], v33 offset:32768\n" \
"v_mfma_f32_16x16x32_bf16 v[132:135], a[176:179], a[148:151], v[132:135]\n" \
"v_mfma_f32_16x16x32_bf16 v[132:135], a[240:243], a[212:215], v[132:135]\n" \
"ds_read_b128 a[148:151], v28 offset:43008\n" \
"v_mfma_f32_16x16x32_bf16 v[148:151], a[176:179], a[152:155], v[148:151]\n" \
"ds_read_b128 a[212:215], v29 offset:43008\n" \
"v_mfma_f32_16x16x32_bf16 v[148:151], a[240:243], a[216:219], v[148:151]\n" \
"s_add_u32 m0, s40, 0x3000\n" \
"s_movk_i32 s84, 0x400\n" \
"v_mfma_f32_16x16x32_bf16 v[152:155], a[180:183], a[152:155], v[152:155]\n" \
"buffer_load_dwordx4 v21, s[76:79], s84 offen offset:0 lds\n" \
"v_mfma_f32_16x16x32_bf16 v[152:155], a[244:247], a[216:219], v[152:155]\n" \
"ds_read_b64_tr_b16 a[166:167], v33 offset:33280\n" \
"v_mfma_f32_16x16x32_bf16 v[156:159], a[184:187], a[152:155], v[156:159]\n" \
"ds_read_b64_tr_b16 a[228:229], v33 offset:36864\n" \
"v_mfma_f32_16x16x32_bf16 v[156:159], a[248:251], a[216:219], v[156:159]\n" \
"s_add_u32 m0, s31, 0x0\n" \
"s_mov_b32 s84, 0x400000\n" \
"v_mfma_f32_16x16x32_bf16 v[160:163], a[188:191], a[152:155], v[160:163]\n" \
"buffer_load_dwordx4 v26, s[68:71], s84 offen offset:0 lds\n" \
"v_mfma_f32_16x16x32_bf16 v[160:163], a[252:255], a[216:219], v[160:163]\n" \
"ds_read_b128 a[152:155], v28 offset:45056\n" \
"v_mfma_f32_16x16x32_bf16 v[176:179], a[188:191], a[156:159], v[176:179]\n" \
"ds_read_b128 a[216:219], v29 offset:45056\n" \
"v_mfma_f32_16x16x32_bf16 v[176:179], a[252:255], a[220:223], v[176:179]\n" \
"ds_read_b64_tr_b16 a[230:231], v33 offset:37376\n" \
"v_mfma_f32_16x16x32_bf16 v[172:175], a[184:187], a[156:159], v[172:175]\n" \
"ds_read_b64_tr_b16 a[172:173], v35 offset:32768\n" \
"v_mfma_f32_16x16x32_bf16 v[172:175], a[248:251], a[220:223], v[172:175]\n" \
"v_mfma_f32_16x16x32_bf16 v[168:171], a[180:183], a[156:159], v[168:171]\n" \
"s_add_u32 s88, s42, 0x2000\n" \
"buffer_store_dwordx4 v[204:207], v20, s[80:83], s88 offen offset:1024 sc1\n" \
"v_mfma_f32_16x16x32_bf16 v[168:171], a[244:247], a[220:223], v[168:171]\n" \
"ds_read_b64_tr_b16 a[174:175], v35 offset:33280\n" \
"v_mfma_f32_16x16x32_bf16 v[164:167], a[176:179], a[156:159], v[164:167]\n" \
"v_mfma_f32_16x16x32_bf16 v[164:167], a[240:243], a[220:223], v[164:167]\n" \
"ds_read_b128 a[156:159], v28 offset:47104\n" \
"v_mfma_f32_16x16x32_bf16 v[84:87], a[176:179], a[136:139], v[84:87]\n" \
"ds_read_b128 a[220:223], v29 offset:47104\n" \
"v_mfma_f32_16x16x32_bf16 v[84:87], a[240:243], a[200:203], v[84:87]\n" \
"ds_read_b64_tr_b16 a[236:237], v35 offset:36864\n" \
"v_mfma_f32_16x16x32_bf16 v[88:91], a[180:183], a[136:139], v[88:91]\n" \
"v_mfma_f32_16x16x32_bf16 v[88:91], a[244:247], a[200:203], v[88:91]\n" \
"s_addk_i32 s84, 0x7c00\n" \
"s_nop 0\n" \
"v_mfma_f32_16x16x32_bf16 v[92:95], a[184:187], a[136:139], v[92:95]\n" \
"buffer_load_dwordx4 v26, s[68:71], s84 offen offset:1024 lds\n" \
"v_mfma_f32_16x16x32_bf16 v[92:95], a[248:251], a[200:203], v[92:95]\n" \
"ds_read_b64_tr_b16 a[238:239], v35 offset:37376\n" \
"v_mfma_f32_16x16x32_bf16 v[96:99], a[188:191], a[136:139], v[96:99]\n" \
"v_mfma_f32_16x16x32_bf16 v[96:99], a[252:255], a[200:203], v[96:99]\n" \
"ds_read_b128 a[136:139], v28 offset:36864\n" \
"v_mfma_f32_16x16x32_bf16 v[112:115], a[188:191], a[140:143], v[112:115]\n" \
"ds_read_b128 a[200:203], v29 offset:36864\n" \
"v_mfma_f32_16x16x32_bf16 v[112:115], a[252:255], a[204:207], v[112:115]\n" \
"ds_read_b64_tr_b16 a[168:169], v34 offset:32768\n" \
"v_mfma_f32_16x16x32_bf16 v[108:111], a[184:187], a[140:143], v[108:111]\n" \
"s_addk_i32 s84, 0x7c00\n" \
"s_nop 0\n" \
"v_mfma_f32_16x16x32_bf16 v[108:111], a[248:251], a[204:207], v[108:111]\n" \
"buffer_load_dwordx4 v27, s[68:71], s84 offen offset:2048 lds\n" \
"v_mfma_f32_16x16x32_bf16 v[104:107], a[180:183], a[140:143], v[104:107]\n" \
"ds_read_b64_tr_b16 a[170:171], v34 offset:33280\n" \
"v_mfma_f32_16x16x32_bf16 v[104:107], a[244:247], a[204:207], v[104:107]\n" \
"ds_read_b64_tr_b16 a[232:233], v34 offset:36864\n" \
"v_mfma_f32_16x16x32_bf16 v[100:103], a[176:179], a[140:143], v[100:103]\n" \
"v_mfma_f32_16x16x32_bf16 v[100:103], a[240:243], a[204:207], v[100:103]\n" \
"ds_read_b128 a[140:143], v28 offset:38912\n" \
"v_mfma_f32_16x16x32_bf16 v[52:55], a[176:179], a[128:131], v[52:55]\n" \
"ds_read_b128 a[204:207], v29 offset:38912\n" \
"v_mfma_f32_16x16x32_bf16 v[52:55], a[240:243], a[192:195], v[52:55]\n" \
"s_addk_i32 s84, 0x7c00\n" \
"s_nop 0\n" \
"v_mfma_f32_16x16x32_bf16 v[56:59], a[180:183], a[128:131], v[56:59]\n" \
"buffer_load_dwordx4 v27, s[68:71], s84 offen offset:3072 lds\n" \
"v_mfma_f32_16x16x32_bf16 v[56:59], a[244:247], a[192:195], v[56:59]\n" \
"ds_read_b64_tr_b16 a[234:235], v34 offset:37376\n" \
"v_mfma_f32_16x16x32_bf16 v[60:63], a[184:187], a[128:131], v[60:63]\n" \
"v_mfma_f32_16x16x32_bf16 v[60:63], a[248:251], a[192:195], v[60:63]\n" \
"v_mfma_f32_16x16x32_bf16 v[64:67], a[188:191], a[128:131], v[64:67]\n" \
"v_mfma_f32_16x16x32_bf16 v[64:67], a[252:255], a[192:195], v[64:67]\n" \
"ds_read_b128 a[128:131], v28 offset:32768\n" \
"v_mfma_f32_16x16x32_bf16 v[80:83], a[188:191], a[132:135], v[80:83]\n" \
"ds_read_b128 a[192:195], v29 offset:32768\n" \
"v_mfma_f32_16x16x32_bf16 v[80:83], a[252:255], a[196:199], v[80:83]\n" \
"v_mfma_f32_16x16x32_bf16 v[76:79], a[184:187], a[132:135], v[76:79]\n" \
"v_mfma_f32_16x16x32_bf16 v[76:79], a[248:251], a[196:199], v[76:79]\n" \
"v_mfma_f32_16x16x32_bf16 v[72:75], a[180:183], a[132:135], v[72:75]\n" \
"v_mfma_f32_16x16x32_bf16 v[72:75], a[244:247], a[196:199], v[72:75]\n" \
"v_mfma_f32_16x16x32_bf16 v[68:71], a[176:179], a[132:135], v[68:71]\n" \
"v_mfma_f32_16x16x32_bf16 v[68:71], a[240:243], a[196:199], v[68:71]\n" \
"ds_read_b128 a[132:135], v28 offset:34816\n" \
"ds_read_b128 a[196:199], v29 offset:34816\n" \
"s_waitcnt lgkmcnt(15)\n" \
"s_nop 0\n" \
"v_mfma_f32_16x16x32_bf16 a[0:3], a[160:163], a[144:147], a[0:3]\n" \
"s_add_u32 s89, s42, 0x2000\n" \
"buffer_store_dwordx4 v[208:211], v20, s[80:83], s89 offen offset:2048 sc1\n" \
"v_mfma_f32_16x16x32_bf16 a[0:3], a[224:227], a[208:211], a[0:3]\n" \
"ds_read_b64_tr_b16 a[176:177], v36 offset:32768\n" \
"v_mfma_f32_16x16x32_bf16 a[4:7], a[164:167], a[144:147], a[4:7]\n" \
"ds_read_b64_tr_b16 a[178:179], v36 offset:33792\n" \
"v_mfma_f32_16x16x32_bf16 a[4:7], a[228:231], a[208:211], a[4:7]\n" \
"ds_read_b64_tr_b16 a[240:241], v36 offset:40960\n" \
"v_mfma_f32_16x16x32_bf16 a[12:15], a[172:175], a[144:147], a[12:15]\n" \
"ds_read_b64_tr_b16 a[242:243], v36 offset:41984\n" \
"s_waitcnt lgkmcnt(15)\n" \
"v_mfma_f32_16x16x32_bf16 a[12:15], a[236:239], a[208:211], a[12:15]\n" \
"s_waitcnt lgkmcnt(12)\n" \
"v_mfma_f32_16x16x32_bf16 a[8:11], a[144:147], a[168:171], a[8:11]\n" \
"ds_read_b64_tr_b16 a[180:181], v37 offset:32768\n" \
"s_waitcnt lgkmcnt(9)\n" \
"s_nop 0\n" \
"v_mfma_f32_16x16x32_bf16 a[8:11], a[208:211], a[232:235], a[8:11]\n" \
"v_mfma_f32_16x16x32_bf16 a[24:27], a[148:151], a[168:171], a[24:27]\n" \
"ds_read_b64_tr_b16 a[182:183], v37 offset:33792\n" \
"v_mfma_f32_16x16x32_bf16 a[24:27], a[212:215], a[232:235], a[24:27]\n" \
"ds_read_b64_tr_b16 a[244:245], v37 offset:40960\n" \
"v_mfma_f32_16x16x32_bf16 a[28:31], a[172:175], a[148:151], a[28:31]\n" \
"v_mfma_f32_16x16x32_bf16 a[28:31], a[236:239], a[212:215], a[28:31]\n" \
"ds_read_b64_tr_b16 a[246:247], v37 offset:41984\n" \
"v_mfma_f32_16x16x32_bf16 a[20:23], a[164:167], a[148:151], a[20:23]\n" \
"v_mfma_f32_16x16x32_bf16 a[20:23], a[228:231], a[212:215], a[20:23]\n" \
"ds_read_b64_tr_b16 a[184:185], v38 offset:32768\n" \
"v_mfma_f32_16x16x32_bf16 a[16:19], a[160:163], a[148:151], a[16:19]\n" \
"ds_read_b64_tr_b16 a[186:187], v38 offset:33792\n" \
"v_mfma_f32_16x16x32_bf16 a[16:19], a[224:227], a[212:215], a[16:19]\n" \
"v_mfma_f32_16x16x32_bf16 a[32:35], a[160:163], a[152:155], a[32:35]\n" \
"s_add_u32 s88, s42, 0x2000\n" \
"buffer_store_dwordx4 v[212:215], v20, s[80:83], s88 offen offset:3072 sc1\n" \
"v_mfma_f32_16x16x32_bf16 a[32:35], a[224:227], a[216:219], a[32:35]\n" \
"ds_read_b64_tr_b16 a[248:249], v38 offset:40960\n" \
"v_mfma_f32_16x16x32_bf16 a[36:39], a[164:167], a[152:155], a[36:39]\n" \
"v_lshlrev_b32_e32 v45, 3, v20\n" \
"v_add_u32_e32 v45, s30, v45\n" \
"v_mfma_f32_16x16x32_bf16 a[36:39], a[228:231], a[216:219], a[36:39]\n" \
"s_getpc_b64 s[86:87]\n" \
"L_ct1_%=:\n" \
"s_add_u32 s86, s86, L_ap0-L_ct1_%=\n" \
"s_addc_u32 s87, s87, 0\n" \
"s_add_u32 m0, s40, 0x2000\n" \
"v_mfma_f32_16x16x32_bf16 a[44:47], a[172:175], a[152:155], a[44:47]\n" \
"global_load_lds_dword v45, s[86:87]\n" \
"v_mfma_f32_16x16x32_bf16 a[44:47], a[236:239], a[216:219], a[44:47]\n" \
"ds_read_b64_tr_b16 a[250:251], v38 offset:41984\n" \
"v_mfma_f32_16x16x32_bf16 a[40:43], a[152:155], a[168:171], a[40:43]\n" \
"ds_read_b64_tr_b16 a[188:189], v39 offset:32768\n" \
"v_mfma_f32_16x16x32_bf16 a[40:43], a[216:219], a[232:235], a[40:43]\n" \
"ds_read_b64_tr_b16 a[190:191], v39 offset:33792\n" \
"v_mfma_f32_16x16x32_bf16 a[56:59], a[156:159], a[168:171], a[56:59]\n" \
"ds_read_b64_tr_b16 a[252:253], v39 offset:40960\n" \
"v_mfma_f32_16x16x32_bf16 a[56:59], a[220:223], a[232:235], a[56:59]\n" \
"ds_read_b64_tr_b16 a[254:255], v39 offset:41984\n" \
"v_mfma_f32_16x16x32_bf16 a[60:63], a[172:175], a[156:159], a[60:63]\n" \
"v_mfma_f32_16x16x32_bf16 a[60:63], a[236:239], a[220:223], a[60:63]\n" \
"v_mfma_f32_16x16x32_bf16 a[52:55], a[164:167], a[156:159], a[52:55]\n" \
"v_mfma_f32_16x16x32_bf16 a[52:55], a[228:231], a[220:223], a[52:55]\n" \
"v_mfma_f32_16x16x32_bf16 a[48:51], a[160:163], a[156:159], a[48:51]\n" \
"v_mfma_f32_16x16x32_bf16 a[48:51], a[224:227], a[220:223], a[48:51]\n" \
"v_mfma_f32_16x16x32_bf16 a[64:67], a[164:167], a[140:143], a[64:67]\n" \
"v_mfma_f32_16x16x32_bf16 a[64:67], a[228:231], a[204:207], a[64:67]\n" \
"v_mfma_f32_16x16x32_bf16 a[80:83], a[172:175], a[140:143], a[80:83]\n" \
"v_mfma_f32_16x16x32_bf16 a[80:83], a[236:239], a[204:207], a[80:83]\n" \
"v_mfma_f32_16x16x32_bf16 a[68:71], a[140:143], a[168:171], a[68:71]\n" \
"v_mfma_f32_16x16x32_bf16 a[68:71], a[204:207], a[232:235], a[68:71]\n" \
"s_waitcnt vmcnt(3) lgkmcnt(0)\n" \
"s_barrier\n" \
"v_xor_b32_e32 v28, 0x20000, v28\n" \
"v_xor_b32_e32 v29, 0x20000, v29\n" \
"v_mfma_f32_16x16x32_bf16 v[116:119], a[176:179], a[144:147], v[116:119]\n" \
"ds_read_b64_tr_b16 a[160:161], v32 offset:0\n" \
"v_accvgpr_read_b32 v184, a0\n" \
"v_mfma_f32_16x16x32_bf16 v[116:119], a[240:243], a[208:211], v[116:119]\n" \
"v_accvgpr_read_b32 v185, a1\n" \
"v_accvgpr_read_b32 v186, a2\n" \
"v_mfma_f32_16x16x32_bf16 v[120:123], a[180:183], a[144:147], v[120:123]\n" \
"v_accvgpr_read_b32 v187, a3\n" \
"v_accvgpr_read_b32 v188, a4\n" \
"v_mfma_f32_16x16x32_bf16 v[120:123], a[244:247], a[208:211], v[120:123]\n" \
"ds_read_b64_tr_b16 a[162:163], v32 offset:512\n" \
"v_accvgpr_read_b32 v189, a5\n" \
"v_mfma_f32_16x16x32_bf16 v[124:127], a[184:187], a[144:147], v[124:127]\n" \
"v_accvgpr_read_b32 v190, a6\n" \
"v_accvgpr_read_b32 v191, a7\n" \
"v_mfma_f32_16x16x32_bf16 v[124:127], a[248:251], a[208:211], v[124:127]\n" \
"v_accvgpr_read_b32 v196, a12\n" \
"v_accvgpr_read_b32 v197, a13\n" \
"v_mfma_f32_16x16x32_bf16 v[128:131], a[188:191], a[144:147], v[128:131]\n" \
"ds_read_b64_tr_b16 a[224:225], v32 offset:4096\n" \
"v_accvgpr_read_b32 v198, a14\n" \
"v_mfma_f32_16x16x32_bf16 v[128:131], a[252:255], a[208:211], v[128:131]\n" \
"ds_read_b128 a[144:147], v28 offset:8192\n" \
"v_accvgpr_read_b32 v199, a15\n" \
"v_mfma_f32_16x16x32_bf16 v[144:147], a[188:191], a[148:151], v[144:147]\n" \
"ds_read_b128 a[208:211], v29 offset:8192\n" \
"v_accvgpr_read_b32 v192, a8\n" \
"v_mfma_f32_16x16x32_bf16 v[144:147], a[252:255], a[212:215], v[144:147]\n" \
"ds_read_b64_tr_b16 a[226:227], v32 offset:4608\n" \
"v_accvgpr_read_b32 v193, a9\n" \
"v_mfma_f32_16x16x32_bf16 v[140:143], a[184:187], a[148:151], v[140:143]\n" \
"v_accvgpr_read_b32 v194, a10\n" \
"v_accvgpr_read_b32 v195, a11\n" \
"v_mfma_f32_16x16x32_bf16 v[140:143], a[248:251], a[212:215], v[140:143]\n" \
"v_accvgpr_read_b32 v208, a24\n" \
"v_accvgpr_read_b32 v209, a25\n" \
"v_mfma_f32_16x16x32_bf16 v[136:139], a[180:183], a[148:151], v[136:139]\n" \
"v_accvgpr_read_b32 v210, a26\n" \
"v_accvgpr_read_b32 v211, a27\n" \
"v_mfma_f32_16x16x32_bf16 v[136:139], a[244:247], a[212:215], v[136:139]\n" \
"ds_read_b64_tr_b16 a[164:165], v33 offset:0\n" \
"v_accvgpr_read_b32 v212, a28\n" \
"v_mfma_f32_16x16x32_bf16 v[132:135], a[176:179], a[148:151], v[132:135]\n" \
"v_accvgpr_read_b32 v213, a29\n" \
"v_accvgpr_read_b32 v214, a30\n" \
"v_mfma_f32_16x16x32_bf16 v[132:135], a[240:243], a[212:215], v[132:135]\n" \
"ds_read_b128 a[148:151], v28 offset:10240\n" \
"v_accvgpr_read_b32 v215, a31\n" \
"v_mfma_f32_16x16x32_bf16 v[148:151], a[176:179], a[152:155], v[148:151]\n" \
"ds_read_b128 a[212:215], v29 offset:10240\n" \
"v_accvgpr_read_b32 v204, a20\n" \
"v_mfma_f32_16x16x32_bf16 v[148:151], a[240:243], a[216:219], v[148:151]\n" \
"ds_read_b64_tr_b16 a[166:167], v33 offset:512\n" \
"v_accvgpr_read_b32 v205, a21\n" \
"v_mfma_f32_16x16x32_bf16 v[152:155], a[180:183], a[152:155], v[152:155]\n" \
"v_accvgpr_read_b32 v206, a22\n" \
"v_accvgpr_read_b32 v207, a23\n" \
"v_mfma_f32_16x16x32_bf16 v[152:155], a[244:247], a[216:219], v[152:155]\n" \
"ds_read_b64_tr_b16 a[228:229], v33 offset:4096\n" \
"v_accvgpr_read_b32 v200, a16\n" \
"v_mfma_f32_16x16x32_bf16 v[156:159], a[184:187], a[152:155], v[156:159]\n" \
"v_accvgpr_read_b32 v201, a17\n" \
"v_accvgpr_read_b32 v202, a18\n" \
"v_mfma_f32_16x16x32_bf16 v[156:159], a[248:251], a[216:219], v[156:159]\n" \
"v_accvgpr_read_b32 v203, a19\n" \
"v_accvgpr_read_b32 v216, a32\n" \
"v_mfma_f32_16x16x32_bf16 v[160:163], a[188:191], a[152:155], v[160:163]\n" \
"ds_read_b64_tr_b16 a[230:231], v33 offset:4608\n" \
"v_accvgpr_read_b32 v217, a33\n" \
"v_mfma_f32_16x16x32_bf16 v[160:163], a[252:255], a[216:219], v[160:163]\n" \
"ds_read_b128 a[152:155], v28 offset:12288\n" \
"v_accvgpr_read_b32 v218, a34\n" \
"v_mfma_f32_16x16x32_bf16 v[176:179], a[188:191], a[156:159], v[176:179]\n" \
"ds_read_b128 a[216:219], v29 offset:12288\n" \
"v_accvgpr_read_b32 v219, a35\n" \
"v_mfma_f32_16x16x32_bf16 v[176:179], a[252:255], a[220:223], v[176:179]\n" \
"v_accvgpr_read_b32 v220, a36\n" \
"v_accvgpr_read_b32 v221, a37\n" \
"v_mfma_f32_16x16x32_bf16 v[172:175], a[184:187], a[156:159], v[172:175]\n" \
"ds_read_b64_tr_b16 a[172:173], v35 offset:0\n" \
"v_accvgpr_read_b32 v222, a38\n" \
"v_mfma_f32_16x16x32_bf16 v[172:175], a[248:251], a[220:223], v[172:175]\n" \
"v_accvgpr_read_b32 v223, a39\n" \
"v_accvgpr_read_b32 v228, a44\n" \
"v_mfma_f32_16x16x32_bf16 v[168:171], a[180:183], a[156:159], v[168:171]\n" \
"v_accvgpr_read_b32 v229, a45\n" \
"v_accvgpr_read_b32 v230, a46\n" \
"v_mfma_f32_16x16x32_bf16 v[168:171], a[244:247], a[220:223], v[168:171]\n" \
"ds_read_b64_tr_b16 a[174:175], v35 offset:512\n" \
"v_accvgpr_read_b32 v231, a47\n" \
"v_mfma_f32_16x16x32_bf16 v[164:167], a[176:179], a[156:159], v[164:167]\n" \
"v_accvgpr_read_b32 v224, a40\n" \
"v_accvgpr_read_b32 v225, a41\n" \
"v_mfma_f32_16x16x32_bf16 v[164:167], a[240:243], a[220:223], v[164:167]\n" \
"ds_read_b128 a[156:159], v28 offset:14336\n" \
"v_accvgpr_read_b32 v226, a42\n" \
"v_mfma_f32_16x16x32_bf16 v[84:87], a[176:179], a[136:139], v[84:87]\n" \
"ds_read_b128 a[220:223], v29 offset:14336\n" \
"v_accvgpr_read_b32 v227, a43\n" \
"v_mfma_f32_16x16x32_bf16 v[84:87], a[240:243], a[200:203], v[84:87]\n" \
"ds_read_b64_tr_b16 a[236:237], v35 offset:4096\n" \
"v_accvgpr_read_b32 v240, a56\n" \
"v_mfma_f32_16x16x32_bf16 v[88:91], a[180:183], a[136:139], v[88:91]\n" \
"v_accvgpr_read_b32 v241, a57\n" \
"v_accvgpr_read_b32 v242, a58\n" \
"v_mfma_f32_16x16x32_bf16 v[88:91], a[244:247], a[200:203], v[88:91]\n" \
"ds_read_b64_tr_b16 a[238:239], v35 offset:4608\n" \
"v_accvgpr_read_b32 v243, a59\n" \
"v_mfma_f32_16x16x32_bf16 v[92:95], a[184:187], a[136:139], v[92:95]\n" \
"v_accvgpr_read_b32 v244, a60\n" \
"v_accvgpr_read_b32 v245, a61\n" \
"v_mfma_f32_16x16x32_bf16 v[92:95], a[248:251], a[200:203], v[92:95]\n" \
"v_accvgpr_read_b32 v246, a62\n" \
"v_accvgpr_read_b32 v247, a63\n" \
"v_mfma_f32_16x16x32_bf16 v[96:99], a[188:191], a[136:139], v[96:99]\n" \
"v_accvgpr_read_b32 v236, a52\n" \
"v_accvgpr_read_b32 v237, a53\n" \
"v_mfma_f32_16x16x32_bf16 v[96:99], a[252:255], a[200:203], v[96:99]\n" \
"ds_read_b128 a[136:139], v28 offset:4096\n" \
"v_accvgpr_read_b32 v238, a54\n" \
"v_mfma_f32_16x16x32_bf16 v[112:115], a[188:191], a[140:143], v[112:115]\n" \
"ds_read_b128 a[200:203], v29 offset:4096\n" \
"v_accvgpr_read_b32 v239, a55\n" \
"v_mfma_f32_16x16x32_bf16 v[112:115], a[252:255], a[204:207], v[112:115]\n" \
"ds_read_b64_tr_b16 a[168:169], v34 offset:0\n" \
"v_accvgpr_read_b32 v232, a48\n" \
"v_mfma_f32_16x16x32_bf16 v[108:111], a[184:187], a[140:143], v[108:111]\n" \
"ds_read_b64_tr_b16 a[170:171], v34 offset:512\n" \
"v_accvgpr_read_b32 v233, a49\n" \
"v_mfma_f32_16x16x32_bf16 v[108:111], a[248:251], a[204:207], v[108:111]\n" \
"v_accvgpr_read_b32 v234, a50\n" \
"v_accvgpr_read_b32 v235, a51\n" \
"v_mfma_f32_16x16x32_bf16 v[104:107], a[180:183], a[140:143], v[104:107]\n" \
"v_accvgpr_read_b32 v180, a64\n" \
"v_accvgpr_read_b32 v181, a65\n" \
"v_mfma_f32_16x16x32_bf16 v[104:107], a[244:247], a[204:207], v[104:107]\n" \
"ds_read_b64_tr_b16 a[232:233], v34 offset:4096\n" \
"v_accvgpr_read_b32 v182, a66\n" \
"v_mfma_f32_16x16x32_bf16 v[100:103], a[176:179], a[140:143], v[100:103]\n" \
"v_accvgpr_read_b32 v183, a67\n" \
"v_accvgpr_read_b32 v48, a80\n" \
"v_mfma_f32_16x16x32_bf16 v[100:103], a[240:243], a[204:207], v[100:103]\n" \
"ds_read_b128 a[140:143], v28 offset:6144\n" \
"v_accvgpr_read_b32 v49, a81\n" \
"v_mfma_f32_16x16x32_bf16 v[52:55], a[176:179], a[128:131], v[52:55]\n" \
"ds_read_b128 a[204:207], v29 offset:6144\n" \
"v_accvgpr_read_b32 v50, a82\n" \
"v_mfma_f32_16x16x32_bf16 v[52:55], a[240:243], a[192:195], v[52:55]\n" \
"ds_read_b64_tr_b16 a[234:235], v34 offset:4608\n" \
"v_accvgpr_read_b32 v51, a83\n" \
"v_mfma_f32_16x16x32_bf16 v[56:59], a[180:183], a[128:131], v[56:59]\n" \
"v_accvgpr_read_b32 v252, a68\n" \
"v_accvgpr_read_b32 v253, a69\n" \
"v_mfma_f32_16x16x32_bf16 v[56:59], a[244:247], a[192:195], v[56:59]\n" \
"v_accvgpr_read_b32 v254, a70\n" \
"v_accvgpr_read_b32 v255, a71\n" \
"v_mfma_f32_16x16x32_bf16 v[60:63], a[184:187], a[128:131], v[60:63]\n" \
"v_lshrrev_b32_e64 v24, 9, v20\n" \
"v_and_b32_e32 v26, 0x1ff, v20\n" \
"v_mfma_f32_16x16x32_bf16 v[60:63], a[248:251], a[192:195], v[60:63]\n" \
"v_lshl_add_u32 v24, v24, 3, v26\n" \
"v_lshrrev_b32_e64 v25, 8, v20\n" \
"v_mfma_f32_16x16x32_bf16 v[64:67], a[188:191], a[128:131], v[64:67]\n" \
"v_mul_u32_u24_e32 v25, 0x24700, v25\n" \
"v_add_u32_e64 v25, v25, v20\n" \
"v_mfma_f32_16x16x32_bf16 v[64:67], a[252:255], a[192:195], v[64:67]\n" \
"ds_read_b128 a[128:131], v28 offset:0\n" \
"v_lshrrev_b32_e64 v26, 2, v20\n" \
"v_mfma_f32_16x16x32_bf16 v[80:83], a[188:191], a[132:135], v[80:83]\n" \
"ds_read_b128 a[192:195], v29 offset:0\n" \
"v_add_u32_e64 v26, s45, v26\n" \
"v_mfma_f32_16x16x32_bf16 v[80:83], a[252:255], a[196:199], v[80:83]\n" \
"v_add_u32_e32 v27, s46, v20\n" \
"v_add_u32_e32 v21, s43, v27\n" \
"v_mfma_f32_16x16x32_bf16 v[76:79], a[184:187], a[132:135], v[76:79]\n" \
"v_mfma_f32_16x16x32_bf16 v[76:79], a[248:251], a[196:199], v[76:79]\n" \
"v_mfma_f32_16x16x32_bf16 v[72:75], a[180:183], a[132:135], v[72:75]\n" \
"v_mfma_f32_16x16x32_bf16 v[72:75], a[244:247], a[196:199], v[72:75]\n" \
"v_mfma_f32_16x16x32_bf16 v[68:71], a[176:179], a[132:135], v[68:71]\n" \
"v_mfma_f32_16x16x32_bf16 v[68:71], a[240:243], a[196:199], v[68:71]\n" \
"ds_read_b128 a[132:135], v28 offset:2048\n" \
"ds_read_b128 a[196:199], v29 offset:2048\n" \
"s_waitcnt lgkmcnt(15)\n" \
"s_nop 0\n" \
"v_mfma_f32_16x16x32_bf16 v[184:187], a[160:163], a[144:147], v[184:187]\n" \
"v_bfe_u32 v44, v20, 2, 5\n" \
"ds_read_b64_tr_b16 a[176:177], v36 offset:0\n" \
"v_mfma_f32_16x16x32_bf16 v[184:187], a[224:227], a[208:211], v[184:187]\n" \
"buffer_load_dword v44, v44, s[80:83], s38 offen offset:0 sc1\n" \
"v_mfma_f32_16x16x32_bf16 v[188:191], a[164:167], a[144:147], v[188:191]\n" \
"ds_read_b64_tr_b16 a[178:179], v36 offset:1024\n" \
"v_mfma_f32_16x16x32_bf16 v[188:191], a[228:231], a[208:211], v[188:191]\n" \
"ds_read_b64_tr_b16 a[240:241], v36 offset:8192\n" \
"v_mfma_f32_16x16x32_bf16 v[196:199], a[172:175], a[144:147], v[196:199]\n" \
"ds_read_b64_tr_b16 a[242:243], v36 offset:9216\n" \
"s_waitcnt lgkmcnt(15)\n" \
"v_mfma_f32_16x16x32_bf16 v[196:199], a[236:239], a[208:211], v[196:199]\n" \
"s_waitcnt lgkmcnt(12)\n" \
"v_mfma_f32_16x16x32_bf16 v[192:195], a[144:147], a[168:171], v[192:195]\n" \
"ds_read_b64_tr_b16 a[180:181], v37 offset:0\n" \
"s_waitcnt lgkmcnt(9)\n" \
"s_nop 0\n" \
"v_mfma_f32_16x16x32_bf16 v[192:195], a[208:211], a[232:235], v[192:195]\n" \
"v_mfma_f32_16x16x32_bf16 v[208:211], a[148:151], a[168:171], v[208:211]\n" \
"ds_read_b64_tr_b16 a[182:183], v37 offset:1024\n" \
"v_mfma_f32_16x16x32_bf16 v[208:211], a[212:215], a[232:235], v[208:211]\n" \
"ds_read_b64_tr_b16 a[244:245], v37 offset:8192\n" \
"v_mfma_f32_16x16x32_bf16 v[212:215], a[172:175], a[148:151], v[212:215]\n" \
"v_mfma_f32_16x16x32_bf16 v[212:215], a[236:239], a[212:215], v[212:215]\n" \
"ds_read_b64_tr_b16 a[246:247], v37 offset:9216\n" \
"v_cvt_pk_bf16_f32 v188, v188, v189\n" \
"v_mfma_f32_16x16x32_bf16 v[204:207], a[164:167], a[148:151], v[204:207]\n" \
"v_cvt_pk_bf16_f32 v189, v190, v191\n" \
"v_cvt_pk_bf16_f32 v190, v198, v199\n" \
"v_mfma_f32_16x16x32_bf16 v[204:207], a[228:231], a[212:215], v[204:207]\n" \
"ds_read_b64_tr_b16 a[184:185], v38 offset:0\n" \
"v_mov_b32_e32 v191, 0x3f80\n" \
"v_mfma_f32_16x16x32_bf16 v[200:203], a[160:163], a[148:151], v[200:203]\n" \
"ds_read_b64_tr_b16 a[186:187], v38 offset:1024\n" \
"v_cvt_pk_bf16_f32 v184, v184, v185\n" \
"v_mfma_f32_16x16x32_bf16 v[200:203], a[224:227], a[212:215], v[200:203]\n" \
"v_cvt_pk_bf16_f32 v185, v186, v187\n" \
"v_cvt_pk_bf16_f32 v186, v196, v197\n" \
"v_mfma_f32_16x16x32_bf16 v[216:219], a[160:163], a[152:155], v[216:219]\n" \
"ds_read_b64_tr_b16 a[248:249], v38 offset:8192\n" \
"v_mov_b32_e64 v187, 0\n" \
"v_mfma_f32_16x16x32_bf16 v[216:219], a[224:227], a[216:219], v[216:219]\n" \
"ds_read_b64_tr_b16 a[250:251], v38 offset:9216\n" \
"v_mfma_f32_16x16x32_bf16 v[220:223], a[164:167], a[152:155], v[220:223]\n" \
"v_mfma_f32_16x16x32_bf16 v[220:223], a[228:231], a[216:219], v[220:223]\n" \
"ds_read_b64_tr_b16 a[188:189], v39 offset:0\n" \
"v_cvt_pk_bf16_f32 v204, v204, v205\n" \
"v_mfma_f32_16x16x32_bf16 v[228:231], a[172:175], a[152:155], v[228:231]\n" \
"v_cvt_pk_bf16_f32 v205, v206, v207\n" \
"v_cvt_pk_bf16_f32 v206, v214, v215\n" \
"v_mfma_f32_16x16x32_bf16 v[228:231], a[236:239], a[216:219], v[228:231]\n" \
"ds_read_b64_tr_b16 a[190:191], v39 offset:1024\n" \
"v_mov_b32_e32 v207, 0x3f80\n" \
"v_mfma_f32_16x16x32_bf16 v[224:227], a[152:155], a[168:171], v[224:227]\n" \
"ds_read_b64_tr_b16 a[252:253], v39 offset:8192\n" \
"v_cvt_pk_bf16_f32 v200, v200, v201\n" \
"v_mfma_f32_16x16x32_bf16 v[224:227], a[216:219], a[232:235], v[224:227]\n" \
"v_cvt_pk_bf16_f32 v201, v202, v203\n" \
"v_cvt_pk_bf16_f32 v202, v212, v213\n" \
"v_mfma_f32_16x16x32_bf16 v[240:243], a[156:159], a[168:171], v[240:243]\n" \
"ds_read_b64_tr_b16 a[254:255], v39 offset:9216\n" \
"v_mov_b32_e64 v203, 0\n" \
"v_mfma_f32_16x16x32_bf16 v[240:243], a[220:223], a[232:235], v[240:243]\n" \
"v_mfma_f32_16x16x32_bf16 v[244:247], a[172:175], a[156:159], v[244:247]\n" \
"v_mfma_f32_16x16x32_bf16 v[244:247], a[236:239], a[220:223], v[244:247]\n" \
"v_cvt_pk_bf16_f32 v220, v220, v221\n" \
"v_cvt_pk_bf16_f32 v221, v222, v223\n" \
"v_mfma_f32_16x16x32_bf16 v[236:239], a[164:167], a[156:159], v[236:239]\n" \
"v_cvt_pk_bf16_f32 v222, v230, v231\n" \
"v_mov_b32_e32 v223, 0x3f80\n" \
"v_mfma_f32_16x16x32_bf16 v[236:239], a[228:231], a[220:223], v[236:239]\n" \
"v_cvt_pk_bf16_f32 v216, v216, v217\n" \
"v_cvt_pk_bf16_f32 v217, v218, v219\n" \
"v_mfma_f32_16x16x32_bf16 v[232:235], a[160:163], a[156:159], v[232:235]\n" \
"v_cvt_pk_bf16_f32 v218, v228, v229\n" \
"v_mov_b32_e64 v219, 0\n" \
"v_mfma_f32_16x16x32_bf16 v[232:235], a[224:227], a[220:223], v[232:235]\n" \
"v_mfma_f32_16x16x32_bf16 v[180:183], a[164:167], a[140:143], v[180:183]\n" \
"v_mfma_f32_16x16x32_bf16 v[180:183], a[228:231], a[204:207], v[180:183]\n" \
"v_mfma_f32_16x16x32_bf16 v[48:51], a[172:175], a[140:143], v[48:51]\n" \
"v_mfma_f32_16x16x32_bf16 v[48:51], a[236:239], a[204:207], v[48:51]\n" \
"v_cvt_pk_bf16_f32 v236, v236, v237\n" \
"v_cvt_pk_bf16_f32 v237, v238, v239\n" \
"v_mfma_f32_16x16x32_bf16 v[252:255], a[140:143], a[168:171], v[252:255]\n" \
"v_cvt_pk_bf16_f32 v238, v246, v247\n" \
"v_mov_b32_e32 v239, 0x3f80\n" \
"v_mfma_f32_16x16x32_bf16 v[252:255], a[204:207], a[232:235], v[252:255]\n" \
"v_cvt_pk_bf16_f32 v232, v232, v233\n" \
"v_cvt_pk_bf16_f32 v233, v234, v235\n" \
"s_waitcnt vmcnt(0) lgkmcnt(0)\n" \
"s_barrier\n" \
"v_mov_b32_e32 v45, s62\n" \
"s_mov_b64 exec, 1\n" \
"buffer_store_dword v45, off, s[80:83], s37 offset:32 sc1\n" \
"s_mov_b64 exec, -1\n" \
"v_cmp_ne_u32_e32 vcc, s62, v44\n" \
"s_nop 4\n" \
"s_cbranch_vccnz L_poll161_%=\n" \
"L_pollr161_%=:\n" \
"v_mfma_f32_16x16x32_bf16 v[116:119], a[176:179], a[144:147], v[116:119]\n" \
"s_mov_b32 s73, s50\n" \
"s_sub_u32 m0, s31, 0x10000\n" \
"v_mfma_f32_16x16x32_bf16 v[116:119], a[240:243], a[208:211], v[116:119]\n" \
"buffer_load_dwordx4 v20, s[80:83], s73 offen offset:0 sc1 lds\n" \
"v_mfma_f32_16x16x32_bf16 v[120:123], a[180:183], a[144:147], v[120:123]\n" \
"s_add_u32 s74, s50, 0x4000\n" \
"s_add_u32 s75, s50, 0x8000\n" \
"v_mfma_f32_16x16x32_bf16 v[120:123], a[244:247], a[208:211], v[120:123]\n" \
"s_mov_b32 s70, 0x3e0293ee\n" \
"s_mov_b32 s76, s52\n" \
"v_mfma_f32_16x16x32_bf16 v[124:127], a[184:187], a[144:147], v[124:127]\n" \
"v_mul_f32_e32 v116, s70, v116\n" \
"v_mul_f32_e32 v117, s70, v117\n" \
"v_mfma_f32_16x16x32_bf16 v[124:127], a[248:251], a[208:211], v[124:127]\n" \
"buffer_load_dwordx4 v20, s[80:83], s73 offen offset:1024 sc1 lds\n" \
"v_mfma_f32_16x16x32_bf16 v[128:131], a[188:191], a[144:147], v[128:131]\n" \
"v_mul_f32_e32 v118, s70, v118\n" \
"v_mul_f32_e32 v119, s70, v119\n" \
"v_mfma_f32_16x16x32_bf16 v[128:131], a[252:255], a[208:211], v[128:131]\n" \
"v_cvt_pk_bf16_f32 v116, v116, v117\n" \
"v_mul_f32_e64 v120, s70, v120\n" \
"v_mfma_f32_16x16x32_bf16 v[144:147], a[188:191], a[148:151], v[144:147]\n" \
"v_mul_f32_e32 v124, s70, v124\n" \
"v_mul_f32_e32 v125, s70, v125\n" \
"v_mfma_f32_16x16x32_bf16 v[144:147], a[252:255], a[212:215], v[144:147]\n" \
"buffer_load_dwordx4 v20, s[80:83], s73 offen offset:2048 sc1 lds\n" \
"v_mfma_f32_16x16x32_bf16 v[140:143], a[184:187], a[148:151], v[140:143]\n" \
"v_mul_f32_e32 v126, s70, v126\n" \
"v_mul_f32_e32 v127, s70, v127\n" \
"v_mfma_f32_16x16x32_bf16 v[140:143], a[248:251], a[212:215], v[140:143]\n" \
"v_cvt_pk_bf16_f32 v124, v124, v125\n" \
"v_mul_f32_e64 v121, s70, v121\n" \
"v_mfma_f32_16x16x32_bf16 v[136:139], a[180:183], a[148:151], v[136:139]\n" \
"v_mul_f32_e32 v122, s70, v122\n" \
"v_mul_f32_e32 v123, s70, v123\n" \
"v_mfma_f32_16x16x32_bf16 v[136:139], a[244:247], a[212:215], v[136:139]\n" \
"buffer_load_dwordx4 v20, s[80:83], s73 offen offset:3072 sc1 lds\n" \
".fill (L_pb + 77536 - .) / 4, 4, 0xBF800000\n" \
"L_pp19:\n" \
"s_cbranch_execz L_pret\n" \
"v_mfma_f32_16x16x32_bf16 v[132:135], a[176:179], a[148:151], v[132:135]\n" \
"s_sub_u32 m0, s31, s51\n" \
"v_cvt_pk_bf16_f32 v117, v118, v119\n" \
"v_mfma_f32_16x16x32_bf16 v[132:135], a[240:243], a[212:215], v[132:135]\n" \
"v_mul_f32_e32 v128, s70, v128\n" \
"v_mul_f32_e32 v129, s70, v129\n" \
"v_mfma_f32_16x16x32_bf16 v[148:151], a[176:179], a[152:155], v[148:151]\n" \
"v_mul_f32_e32 v130, s70, v130\n" \
"v_mul_f32_e32 v131, s70, v131\n" \
"v_mfma_f32_16x16x32_bf16 v[148:151], a[240:243], a[216:219], v[148:151]\n" \
"buffer_load_dwordx4 v20, s[80:83], s74 offen offset:0 sc1 lds\n" \
"v_mfma_f32_16x16x32_bf16 v[152:155], a[180:183], a[152:155], v[152:155]\n" \
"v_cvt_pk_bf16_f32 v125, v126, v127\n" \
"v_cvt_pk_bf16_f32 v118, v120, v121\n" \
"v_mfma_f32_16x16x32_bf16 v[152:155], a[244:247], a[216:219], v[152:155]\n" \
"v_cvt_pk_bf16_f32 v119, v122, v123\n" \
"v_cvt_pk_bf16_f32 v126, v128, v129\n" \
"v_mfma_f32_16x16x32_bf16 v[156:159], a[184:187], a[152:155], v[156:159]\n" \
"v_cvt_pk_bf16_f32 v127, v130, v131\n" \
"v_mul_f32_e64 v132, s70, v132\n" \
"v_mfma_f32_16x16x32_bf16 v[156:159], a[248:251], a[216:219], v[156:159]\n" \
"buffer_load_dwordx4 v20, s[80:83], s74 offen offset:1024 sc1 lds\n" \
"v_mfma_f32_16x16x32_bf16 v[160:163], a[188:191], a[152:155], v[160:163]\n" \
"ds_write_b128 v21, v[116:119] offset:16384\n" \
"v_mfma_f32_16x16x32_bf16 v[160:163], a[252:255], a[216:219], v[160:163]\n" \
"v_mul_f32_e32 v133, s70, v133\n" \
"v_mul_f32_e32 v140, s70, v140\n" \
"v_mfma_f32_16x16x32_bf16 v[176:179], a[188:191], a[156:159], v[176:179]\n" \
"v_mul_f32_e32 v141, s70, v141\n" \
"v_mul_f32_e32 v134, s70, v134\n" \
"v_mfma_f32_16x16x32_bf16 v[176:179], a[252:255], a[220:223], v[176:179]\n" \
"buffer_load_dwordx4 v20, s[80:83], s74 offen offset:2048 sc1 lds\n" \
"v_mfma_f32_16x16x32_bf16 v[172:175], a[184:187], a[156:159], v[172:175]\n" \
"ds_write_b128 v21, v[124:127] offset:17408\n" \
"v_mfma_f32_16x16x32_bf16 v[172:175], a[248:251], a[220:223], v[172:175]\n" \
"v_mul_f32_e64 v135, s70, v135\n" \
"v_cvt_pk_bf16_f32 v132, v132, v133\n" \
"v_mfma_f32_16x16x32_bf16 v[168:171], a[180:183], a[156:159], v[168:171]\n" \
"v_mul_f32_e32 v142, s70, v142\n" \
"v_mul_f32_e32 v143, s70, v143\n" \
"v_mfma_f32_16x16x32_bf16 v[168:171], a[244:247], a[220:223], v[168:171]\n" \
"buffer_load_dwordx4 v20, s[80:83], s74 offen offset:3072 sc1 lds\n" \
"v_mfma_f32_16x16x32_bf16 v[164:167], a[176:179], a[156:159], v[164:167]\n" \
"s_sub_u32 m0, s31, 0x4000\n" \
"v_cvt_pk_bf16_f32 v140, v140, v141\n" \
"v_mfma_f32_16x16x32_bf16 v[164:167], a[240:243], a[220:223], v[164:167]\n" \
"v_mul_f32_e32 v136, s70, v136\n" \
"v_mul_f32_e32 v137, s70, v137\n" \
"v_mfma_f32_16x16x32_bf16 v[84:87], a[176:179], a[136:139], v[84:87]\n" \
"v_mul_f32_e32 v138, s70, v138\n" \
"v_mul_f32_e32 v139, s70, v139\n" \
"v_mfma_f32_16x16x32_bf16 v[84:87], a[240:243], a[200:203], v[84:87]\n" \
"buffer_load_dwordx4 v20, s[80:83], s75 offen offset:0 sc1 lds\n" \
"v_mfma_f32_16x16x32_bf16 v[88:91], a[180:183], a[136:139], v[88:91]\n" \
"v_cvt_pk_bf16_f32 v133, v134, v135\n" \
"v_mul_f32_e64 v144, s70, v144\n" \
"v_mfma_f32_16x16x32_bf16 v[88:91], a[244:247], a[200:203], v[88:91]\n" \
"v_mul_f32_e32 v145, s70, v145\n" \
"v_mul_f32_e32 v146, s70, v146\n" \
"v_mfma_f32_16x16x32_bf16 v[92:95], a[184:187], a[136:139], v[92:95]\n" \
"v_mul_f32_e64 v147, s70, v147\n" \
"v_cvt_pk_bf16_f32 v141, v142, v143\n" \
"v_mfma_f32_16x16x32_bf16 v[92:95], a[248:251], a[200:203], v[92:95]\n" \
"buffer_load_dwordx4 v20, s[80:83], s75 offen offset:1024 sc1 lds\n" \
"v_mfma_f32_16x16x32_bf16 v[96:99], a[188:191], a[136:139], v[96:99]\n" \
"v_cvt_pk_bf16_f32 v134, v136, v137\n" \
"v_cvt_pk_bf16_f32 v135, v138, v139\n" \
"v_mfma_f32_16x16x32_bf16 v[96:99], a[252:255], a[200:203], v[96:99]\n" \
"v_cvt_pk_bf16_f32 v142, v144, v145\n" \
"v_cvt_pk_bf16_f32 v143, v146, v147\n" \
"v_mfma_f32_16x16x32_bf16 v[112:115], a[188:191], a[140:143], v[112:115]\n" \
"ds_write_b128 v21, v[132:135] offset:20480\n" \
"v_mfma_f32_16x16x32_bf16 v[112:115], a[252:255], a[204:207], v[112:115]\n" \
"buffer_load_dwordx4 v20, s[80:83], s75 offen offset:2048 sc1 lds\n" \
"v_mfma_f32_16x16x32_bf16 v[108:111], a[184:187], a[140:143], v[108:111]\n" \
"v_cvt_pk_bf16_f32 v180, v180, v181\n" \
"v_cvt_pk_bf16_f32 v181, v182, v183\n" \
"v_mfma_f32_16x16x32_bf16 v[108:111], a[248:251], a[204:207], v[108:111]\n" \
"ds_write_b128 v21, v[140:143] offset:21504\n" \
"v_mfma_f32_16x16x32_bf16 v[104:107], a[180:183], a[140:143], v[104:107]\n" \
"v_cvt_pk_bf16_f32 v234, v244, v245\n" \
"v_mov_b32_e64 v235, 0\n" \
"v_mfma_f32_16x16x32_bf16 v[104:107], a[244:247], a[204:207], v[104:107]\n" \
"buffer_load_dwordx4 v20, s[80:83], s75 offen offset:3072 sc1 lds\n" \
"v_mfma_f32_16x16x32_bf16 v[100:103], a[176:179], a[140:143], v[100:103]\n" \
"s_mov_b32 m0, s53\n" \
"s_nop 0\n" \
"v_cvt_pk_bf16_f32 v182, v50, v51\n" \
"v_mfma_f32_16x16x32_bf16 v[100:103], a[240:243], a[204:207], v[100:103]\n" \
"v_mov_b32_e32 v183, 0x3f80\n" \
"v_cvt_pk_bf16_f32 v118, v192, v193\n" \
"v_mfma_f32_16x16x32_bf16 v[52:55], a[176:179], a[128:131], v[52:55]\n" \
"v_cvt_pk_bf16_f32 v119, v194, v195\n" \
"v_cvt_pk_bf16_f32 v120, v208, v209\n" \
"v_mfma_f32_16x16x32_bf16 v[52:55], a[240:243], a[192:195], v[52:55]\n" \
"buffer_load_dwordx4 v20, s[80:83], s76 offen offset:0 sc1 lds\n" \
"v_mfma_f32_16x16x32_bf16 v[56:59], a[180:183], a[128:131], v[56:59]\n" \
"v_cvt_pk_bf16_f32 v121, v210, v211\n" \
"v_cvt_pk_bf16_f32 v122, v224, v225\n" \
"v_mfma_f32_16x16x32_bf16 v[56:59], a[244:247], a[192:195], v[56:59]\n" \
"v_cvt_pk_bf16_f32 v123, v226, v227\n" \
"v_cvt_pk_bf16_f32 v124, v240, v241\n" \
"v_cvt_pk_bf16_f32 v125, v242, v243\n" \
"v_mfma_f32_16x16x32_bf16 v[60:63], a[184:187], a[128:131], v[60:63]\n" \
"v_mfma_f32_16x16x32_bf16 v[192:195], v[188:191], v[184:187], 0\n" \
"buffer_load_dwordx4 v20, s[80:83], s76 offen offset:1024 sc1 lds\n" \
"v_mfma_f32_16x16x32_bf16 v[32:35], v[180:183], v[184:187], 0\n" \
"v_mfma_f32_16x16x32_bf16 v[208:211], v[204:207], v[200:203], 0\n" \
"v_mfma_f32_16x16x32_bf16 v[36:39], v[188:191], v[200:203], 0\n" \
"v_mfma_f32_16x16x32_bf16 v[224:227], v[220:223], v[216:219], 0\n" \
"v_mfma_f32_16x16x32_bf16 v[248:251], v[204:207], v[216:219], 0\n" \
"v_mfma_f32_16x16x32_bf16 v[240:243], v[236:239], v[232:235], 0\n" \
"v_mfma_f32_16x16x32_bf16 v[128:131], v[220:223], v[232:235], 0\n" \
"s_mov_b32 s64, 0xaaaaaaaa\n" \
"s_mov_b32 s65, 0xaaaaaaaa\n" \
"v_mfma_f32_16x16x32_bf16 v[60:63], a[248:251], a[192:195], v[60:63]\n" \
"s_mov_b32 s66, 0xfff0ffff\n" \
"s_mov_b32 s67, 0xf000ff00\n" \
"v_mfma_f32_16x16x32_bf16 v[64:67], a[188:191], a[128:131], v[64:67]\n" \
"s_mov_b32 s68, 0xffc0fffc\n" \
"s_mov_b32 s69, 0xc000fc00\n" \
"v_cndmask_b32_e64 v192, v192, v193, s[64:65]\n" \
"v_cndmask_b32_e64 v32, v32, v33, s[64:65]\n" \
"v_cndmask_b32_e64 v194, v194, v195, s[64:65]\n" \
"v_cndmask_b32_e64 v34, v34, v35, s[64:65]\n" \
"v_cndmask_b32_e64 v208, v208, v209, s[64:65]\n" \
"v_cndmask_b32_e64 v36, v36, v37, s[64:65]\n" \
"v_cndmask_b32_e64 v210, v210, v211, s[64:65]\n" \
"v_cndmask_b32_e64 v38, v38, v39, s[64:65]\n" \
"v_cndmask_b32_e64 v224, v224, v225, s[64:65]\n" \
"v_cndmask_b32_e64 v248, v248, v249, s[64:65]\n" \
"v_cndmask_b32_e64 v226, v226, v227, s[64:65]\n" \
"v_cndmask_b32_e64 v250, v250, v251, s[64:65]\n" \
"v_cndmask_b32_e64 v240, v240, v241, s[64:65]\n" \
"v_cndmask_b32_e64 v128, v128, v129, s[64:65]\n" \
"v_cndmask_b32_e64 v242, v242, v243, s[64:65]\n" \
"v_cndmask_b32_e64 v130, v130, v131, s[64:65]\n" \
"v_cndmask_b32_e64 v192, v32, v192, s[66:67]\n" \
"v_cndmask_b32_e64 v194, v34, v194, s[68:69]\n" \
"v_cndmask_b32_e64 v208, v36, v208, s[66:67]\n" \
"v_cndmask_b32_e64 v210, v38, v210, s[68:69]\n" \
"v_cndmask_b32_e64 v224, v248, v224, s[66:67]\n" \
"v_cndmask_b32_e64 v226, v250, v226, s[68:69]\n" \
"v_cndmask_b32_e64 v240, v128, v240, s[66:67]\n" \
"v_cndmask_b32_e64 v242, v130, v242, s[68:69]\n" \
"v_mfma_f32_16x16x32_bf16 v[64:67], a[252:255], a[192:195], v[64:67]\n" \
"v_max_f32_e32 v193, v192, v194\n" \
"v_max_f32_e32 v209, v208, v210\n" \
"v_max_f32_e32 v225, v224, v226\n" \
"v_max_f32_e32 v241, v240, v242\n" \
"v_mul_f32_e32 v148, s70, v148\n" \
"v_permlane32_swap_b32_e32 v193, v225\n" \
"v_permlane32_swap_b32_e32 v209, v241\n" \
"v_mul_f32_e32 v149, s70, v149\n" \
"v_mul_f32_e32 v156, s70, v156\n" \
"v_mul_f32_e32 v157, s70, v157\n" \
"v_permlane32_swap_b32_e32 v196, v228\n" \
"v_permlane32_swap_b32_e32 v212, v244\n" \
"v_max_f32_e32 v193, v193, v225\n" \
"v_max_f32_e32 v209, v209, v241\n" \
"v_mul_f32_e32 v150, s70, v150\n" \
"v_mul_f32_e32 v151, s70, v151\n" \
"v_cvt_pk_bf16_f32 v148, v148, v149\n" \
"v_mul_f32_e32 v158, s70, v158\n" \
"v_mul_f32_e32 v159, s70, v159\n" \
"v_cvt_pk_bf16_f32 v156, v156, v157\n" \
"v_permlane16_swap_b32_e32 v228, v244\n" \
"v_permlane16_swap_b32_e32 v193, v209\n" \
"v_mfma_f32_16x16x32_bf16 v[80:83], a[188:191], a[132:135], v[80:83]\n" \
"s_mov_b32 s72, 0x3eae1a93\n" \
"v_mul_f32_e32 v152, s70, v152\n" \
"v_mul_f32_e32 v153, s70, v153\n" \
"v_mul_f32_e32 v154, s70, v154\n" \
"v_mul_f32_e32 v155, s70, v155\n" \
"v_cvt_pk_bf16_f32 v149, v150, v151\n" \
"v_mul_f32_e32 v160, s70, v160\n" \
"v_mul_f32_e32 v161, s70, v161\n" \
"v_mul_f32_e32 v162, s70, v162\n" \
"v_mul_f32_e32 v163, s70, v163\n" \
"v_cvt_pk_bf16_f32 v157, v158, v159\n" \
"v_mul_f32_e32 v45, 0xbfb8aa3b, v228\n" \
"v_mfma_f32_16x16x16_bf16 v[48:51], v[182:183], v[46:47], 0\n" \
"v_mfma_f32_16x16x16_bf16 v[196:199], v[190:191], v[46:47], 0\n" \
"v_mfma_f32_16x16x16_bf16 v[212:215], v[206:207], v[46:47], 0\n" \
"v_max_f32_e64 v193, v193, v209\n" \
"v_cvt_pk_bf16_f32 v150, v152, v153\n" \
"v_cvt_pk_bf16_f32 v151, v154, v155\n" \
"v_cvt_pk_bf16_f32 v158, v160, v161\n" \
"v_cvt_pk_bf16_f32 v159, v162, v163\n" \
"v_mfma_f32_16x16x16_bf16 v[228:231], v[222:223], v[46:47], 0\n" \
"v_mul_f32_e32 v193, s72, v193\n" \
"v_mov_b32_e32 v209, v193\n" \
"ds_write_b128 v21, v[148:151] offset:24576\n" \
"v_mfma_f32_16x16x16_bf16 v[244:247], v[238:239], v[46:47], 0\n" \
"v_permlane16_swap_b32_e32 v193, v209\n" \
"s_mov_b32 s71, 0x3e0293ee\n" \
"v_mfma_f32_16x16x32_bf16 v[80:83], a[252:255], a[196:199], v[80:83]\n" \
"v_mov_b32_e32 v225, v193\n" \
"v_mov_b32_e32 v241, v209\n" \
"v_mul_f32_e32 v164, s70, v164\n" \
"v_mul_f32_e64 v165, s70, v165\n" \
"ds_write_b128 v21, v[156:159] offset:25600\n" \
"v_mul_f32_e32 v172, s70, v172\n" \
"v_mul_f32_e32 v173, s70, v173\n" \
"v_pk_mul_f32 v[84:85], v[84:85], s[70:71]\n" \
"v_pk_mul_f32 v[92:93], v[92:93], s[70:71]\n" \
"v_pk_mul_f32 v[100:101], v[100:101], s[70:71]\n" \
"v_pk_mul_f32 v[108:109], v[108:109], s[70:71]\n" \
"v_pk_mul_f32 v[52:53], v[52:53], s[70:71]\n" \
"v_pk_mul_f32 v[60:61], v[60:61], s[70:71]\n" \
"v_permlane32_swap_b32_e32 v193, v225\n" \
"v_permlane32_swap_b32_e32 v209, v241\n" \
"v_mul_f32_e32 v166, s70, v166\n" \
"v_mul_f32_e32 v167, s70, v167\n" \
"v_cvt_pk_bf16_f32 v164, v164, v165\n" \
"v_mul_f32_e32 v174, s70, v174\n" \
"v_mul_f32_e32 v175, s70, v175\n" \
"v_cvt_pk_bf16_f32 v172, v172, v173\n" \
"v_pk_mul_f32 v[86:87], v[86:87], s[70:71]\n" \
"v_cvt_pk_bf16_f32 v84, v84, v85\n" \
"v_pk_mul_f32 v[94:95], v[94:95], s[70:71]\n" \
"v_cvt_pk_bf16_f32 v92, v92, v93\n" \
"v_pk_mul_f32 v[102:103], v[102:103], s[70:71]\n" \
"v_cvt_pk_bf16_f32 v100, v100, v101\n" \
"v_pk_mul_f32 v[110:111], v[110:111], s[70:71]\n" \
"v_cvt_pk_bf16_f32 v108, v108, v109\n" \
"v_pk_mul_f32 v[54:55], v[54:55], s[70:71]\n" \
"v_cvt_pk_bf16_f32 v52, v52, v53\n" \
"v_pk_mul_f32 v[62:63], v[62:63], s[70:71]\n" \
"v_cvt_pk_bf16_f32 v60, v60, v61\n" \
"v_fma_f32 v192, v192, s72, -v193\n" \
"v_fma_f32 v194, v194, s72, -v193\n" \
"v_fma_f32 v208, v208, s72, -v209\n" \
"v_fma_f32 v210, v210, s72, -v209\n" \
"v_mul_f32_e32 v168, s70, v168\n" \
"v_mul_f32_e32 v169, s70, v169\n" \
"v_mul_f32_e32 v170, s70, v170\n" \
"v_mul_f32_e32 v171, s70, v171\n" \
"v_cvt_pk_bf16_f32 v165, v166, v167\n" \
"v_mul_f32_e32 v176, s70, v176\n" \
"v_mul_f32_e32 v177, s70, v177\n" \
"v_mul_f32_e32 v178, s70, v178\n" \
"v_mul_f32_e32 v179, s70, v179\n" \
"v_cvt_pk_bf16_f32 v173, v174, v175\n" \
"v_pk_mul_f32 v[88:89], v[88:89], s[70:71]\n" \
"v_pk_mul_f32 v[90:91], v[90:91], s[70:71]\n" \
"v_cvt_pk_bf16_f32 v85, v86, v87\n" \
"v_pk_mul_f32 v[96:97], v[96:97], s[70:71]\n" \
"v_pk_mul_f32 v[98:99], v[98:99], s[70:71]\n" \
"v_cvt_pk_bf16_f32 v93, v94, v95\n" \
"v_pk_mul_f32 v[104:105], v[104:105], s[70:71]\n" \
"v_pk_mul_f32 v[106:107], v[106:107], s[70:71]\n" \
"v_cvt_pk_bf16_f32 v101, v102, v103\n" \
"v_pk_mul_f32 v[112:113], v[112:113], s[70:71]\n" \
"v_pk_mul_f32 v[114:115], v[114:115], s[70:71]\n" \
"v_cvt_pk_bf16_f32 v109, v110, v111\n" \
"v_pk_mul_f32 v[56:57], v[56:57], s[70:71]\n" \
"v_pk_mul_f32 v[58:59], v[58:59], s[70:71]\n" \
"v_cvt_pk_bf16_f32 v53, v54, v55\n" \
"v_pk_mul_f32 v[64:65], v[64:65], s[70:71]\n" \
"v_pk_mul_f32 v[66:67], v[66:67], s[70:71]\n" \
"v_cvt_pk_bf16_f32 v61, v62, v63\n" \
"v_pk_mul_f32 v[80:81], v[80:81], s[70:71]\n" \
"v_pk_mul_f32 v[82:83], v[82:83], s[70:71]\n" \
"v_exp_f32_e32 v192, v192\n" \
"v_exp_f32_e32 v194, v194\n" \
"v_exp_f32_e32 v208, v208\n" \
"v_exp_f32_e32 v210, v210\n" \
"v_fma_f32 v224, v224, s72, -v225\n" \
"v_fma_f32 v226, v226, s72, -v225\n" \
"v_mfma_f32_16x16x32_bf16 v[76:79], a[184:187], a[132:135], v[76:79]\n" \
"v_cvt_pk_bf16_f32 v166, v168, v169\n" \
"v_cvt_pk_bf16_f32 v167, v170, v171\n" \
"v_cvt_pk_bf16_f32 v174, v176, v177\n" \
"v_cvt_pk_bf16_f32 v175, v178, v179\n" \
"v_cvt_pk_bf16_f32 v86, v88, v89\n" \
"v_cvt_pk_bf16_f32 v87, v90, v91\n" \
"v_cvt_pk_bf16_f32 v94, v96, v97\n" \
"v_cvt_pk_bf16_f32 v95, v98, v99\n" \
"v_cvt_pk_bf16_f32 v102, v104, v105\n" \
"v_cvt_pk_bf16_f32 v103, v106, v107\n" \
"v_cvt_pk_bf16_f32 v110, v112, v113\n" \
"v_cvt_pk_bf16_f32 v111, v114, v115\n" \
"v_cvt_pk_bf16_f32 v54, v56, v57\n" \
"v_cvt_pk_bf16_f32 v55, v58, v59\n" \
"v_cvt_pk_bf16_f32 v62, v64, v65\n" \
"v_cvt_pk_bf16_f32 v63, v66, v67\n" \
"v_exp_f32_e32 v224, v224\n" \
"v_exp_f32_e32 v226, v226\n" \
"v_fma_f32 v240, v240, s72, -v241\n" \
"v_fma_f32 v242, v242, s72, -v241\n" \
"ds_write_b128 v21, v[164:167] offset:28672\n" \
"v_cvt_pk_bf16_f32 v192, v192, v194\n" \
"v_cvt_pk_bf16_f32 v208, v208, v210\n" \
"v_exp_f32_e32 v240, v240\n" \
"v_exp_f32_e32 v242, v242\n" \
"v_cvt_pk_bf16_f32 v132, v48, v49\n" \
"v_cvt_pk_bf16_f32 v133, v50, v51\n" \
"v_cvt_pk_bf16_f32 v134, v196, v197\n" \
"v_cvt_pk_bf16_f32 v135, v198, v199\n" \
"ds_write_b128 v21, v[172:175] offset:29696\n" \
"v_cvt_pk_bf16_f32 v136, v212, v213\n" \
"v_cvt_pk_bf16_f32 v137, v214, v215\n" \
"v_perm_b32 v184, v192, v192, v40\n" \
"v_perm_b32 v185, v192, v192, v41\n" \
"v_perm_b32 v186, v192, v192, v42\n" \
"v_perm_b32 v187, v192, v192, v43\n" \
"v_perm_b32 v200, v208, v208, v40\n" \
"v_perm_b32 v201, v208, v208, v41\n" \
"v_perm_b32 v202, v208, v208, v42\n" \
"v_perm_b32 v203, v208, v208, v43\n" \
"ds_write_b128 v27, v[84:87] offset:8192\n" \
"v_cvt_pk_bf16_f32 v224, v224, v226\n" \
"v_cvt_pk_bf16_f32 v138, v228, v229\n" \
"v_cvt_pk_bf16_f32 v139, v230, v231\n" \
"v_mfma_f32_16x16x32_bf16 v[32:35], v[132:135], v[184:187], 0\n" \
"v_mfma_f32_16x16x32_bf16 v[36:39], v[134:137], v[200:203], 0\n" \
"v_perm_b32 v216, v224, v224, v40\n" \
"v_perm_b32 v217, v224, v224, v41\n" \
"ds_write_b128 v27, v[92:95] offset:9216\n" \
"v_perm_b32 v218, v224, v224, v42\n" \
"v_perm_b32 v219, v224, v224, v43\n" \
"v_cvt_pk_bf16_f32 v240, v240, v242\n" \
"v_mfma_f32_16x16x32_bf16 v[76:79], a[248:251], a[196:199], v[76:79]\n" \
"v_cvt_pk_bf16_f32 v140, v244, v245\n" \
"v_cvt_pk_bf16_f32 v141, v246, v247\n" \
"v_mfma_f32_16x16x32_bf16 v[248:251], v[136:139], v[216:219], 0\n" \
"v_perm_b32 v232, v240, v240, v40\n" \
"v_perm_b32 v233, v240, v240, v41\n" \
"ds_write_b128 v27, v[100:103] offset:12288\n" \
"v_perm_b32 v234, v240, v240, v42\n" \
"v_perm_b32 v235, v240, v240, v43\n" \
"v_pk_mul_f32 v[76:77], v[76:77], s[70:71]\n" \
"v_pk_mul_f32 v[78:79], v[78:79], s[70:71]\n" \
"v_cvt_pk_bf16_f32 v76, v76, v77\n" \
"v_cvt_pk_bf16_f32 v77, v78, v79\n" \
"v_cvt_pk_bf16_f32 v78, v80, v81\n" \
"v_cvt_pk_bf16_f32 v79, v82, v83\n" \
"v_mfma_f32_16x16x32_bf16 v[128:131], v[138:141], v[232:235], 0\n" \
"ds_write_b128 v27, v[108:111] offset:13312\n" \
"v_mfma_f32_16x16x32_bf16 v[72:75], a[180:183], a[132:135], v[72:75]\n" \
"v_cvt_pk_bf16_f32 v116, v252, v253\n" \
"v_cvt_pk_bf16_f32 v117, v254, v255\n" \
"v_mfma_f32_16x16x32_bf16 v[72:75], a[244:247], a[196:199], v[72:75]\n" \
"v_exp_f32_e64 v45, v45\n" \
"v_mfma_f32_16x16x32_bf16 v[192:195], v[116:119], v[184:187], 0\n" \
"ds_write_b128 v27, v[52:55] offset:0\n" \
"v_mfma_f32_16x16x32_bf16 v[208:211], v[118:121], v[200:203], 0\n" \
"v_mfma_f32_16x16x32_bf16 v[224:227], v[120:123], v[216:219], 0\n" \
"v_mfma_f32_16x16x32_bf16 v[240:243], v[122:125], v[232:235], 0\n" \
"ds_write_b128 v27, v[60:63] offset:1024\n" \
"v_mov_b32_dpp v34, v38 quad_perm:[0,1,2,3] row_mask:0x2 bank_mask:0xf\n" \
"v_pk_mul_f32 v[72:73], v[72:73], s[70:71]\n" \
"v_pk_mul_f32 v[74:75], v[74:75], s[70:71]\n" \
"v_mov_b32_dpp v34, v250 quad_perm:[0,1,2,3] row_mask:0x4 bank_mask:0xf\n" \
"v_add_f32_e64 v45, 1.0, v45\n" \
"v_mfma_f32_16x16x32_bf16 v[68:71], a[176:179], a[132:135], v[68:71]\n" \
"v_mov_b32_dpp v34, v130 quad_perm:[0,1,2,3] row_mask:0x8 bank_mask:0xf\n" \
"v_mov_b32_dpp v32, v36 quad_perm:[0,1,2,3] row_mask:0x2 bank_mask:0xf\n" \
"v_rcp_f32_e64 v45, v45\n" \
"ds_write_b128 v27, v[76:79] offset:5120\n" \
"v_mov_b32_dpp v33, v37 quad_perm:[0,1,2,3] row_mask:0x2 bank_mask:0xf\n" \
"v_rcp_f32_e64 v34, v34\n" \
"v_mov_b32_dpp v32, v248 quad_perm:[0,1,2,3] row_mask:0x4 bank_mask:0xf\n" \
"v_mov_b32_dpp v33, v249 quad_perm:[0,1,2,3] row_mask:0x4 bank_mask:0xf\n" \
"v_mul_f32_e64 v34, v45, v34\n" \
"v_mov_b32_dpp v32, v128 quad_perm:[0,1,2,3] row_mask:0x8 bank_mask:0xf\n" \
"v_mov_b32_dpp v33, v129 quad_perm:[0,1,2,3] row_mask:0x8 bank_mask:0xf\n" \
"v_mul_f32_e32 v32, v34, v32\n" \
"v_mul_f32_e32 v33, v34, v33\n" \
"v_mov_b32_e64 v38, v34\n" \
"v_mfma_f32_16x16x32_bf16 v[68:71], a[240:243], a[196:199], v[68:71]\n" \
"v_sub_f32_e32 v44, 1.0, v45\n" \
"v_permlane16_swap_b32_e32 v34, v38\n" \
"v_mov_b32_e32 v250, v34\n" \
"v_mov_b32_e32 v130, v38\n" \
"v_cvt_pk_bf16_f32 v32, v32, v33\n" \
"v_permlane32_swap_b32_e32 v34, v250\n" \
"v_permlane32_swap_b32_e32 v38, v130\n" \
"v_mul_f32_e64 v192, v34, v192\n" \
"v_pk_mul_f32 v[68:69], v[68:69], s[70:71]\n" \
"v_pk_mul_f32 v[70:71], v[70:71], s[70:71]\n" \
"v_cvt_pk_bf16_f32 v68, v68, v69\n" \
"v_cvt_pk_bf16_f32 v69, v70, v71\n" \
"v_cvt_pk_bf16_f32 v70, v72, v73\n" \
"v_cvt_pk_bf16_f32 v71, v74, v75\n" \
"ds_write_b128 v27, v[68:71] offset:4096\n" \
"v_mul_f32_e32 v193, v34, v193\n" \
"v_mul_f32_e32 v208, v38, v208\n" \
"v_mul_f32_e32 v209, v38, v209\n" \
"v_mul_f32_e32 v224, v250, v224\n" \
"v_mul_f32_e32 v225, v250, v225\n" \
"v_mul_f32_e32 v240, v130, v240\n" \
"v_mul_f32_e32 v241, v130, v241\n" \
"v_mul_f32_e32 v194, v34, v194\n" \
"v_mul_f32_e64 v195, v34, v195\n" \
"v_cvt_pk_bf16_f32 v192, v192, v193\n" \
"v_mul_f32_e32 v210, v38, v210\n" \
"v_mul_f32_e32 v211, v38, v211\n" \
"v_cvt_pk_bf16_f32 v208, v208, v209\n" \
"v_mul_f32_e32 v226, v250, v226\n" \
"v_mul_f32_e32 v227, v250, v227\n" \
"v_cvt_pk_bf16_f32 v224, v224, v225\n" \
"v_mul_f32_e32 v242, v130, v242\n" \
"v_mul_f32_e32 v243, v130, v243\n" \
"v_cvt_pk_bf16_f32 v240, v240, v241\n" \
"v_cvt_pk_bf16_f32 v193, v194, v195\n" \
"s_add_u32 s84, s48, 0x0\n" \
"s_nop 0\n" \
"v_cvt_pk_bf16_f32 v209, v210, v211\n" \
"s_add_u32 s85, s48, 0x24800\n" \
"v_cvt_pk_bf16_f32 v225, v226, v227\n" \
"s_add_u32 s86, s48, 0x49000\n" \
"v_cvt_pk_bf16_f32 v241, v242, v243\n" \
"s_add_u32 s87, s48, 0x6d800\n" \
"ds_write_b32 v26, v44\n" \
"buffer_store_dword v32, v25, s[80:83], s49 offen sc1\n" \
"buffer_store_dwordx2 v[192:193], v24, s[80:83], s84 offen sc1\n" \
"buffer_store_dwordx2 v[208:209], v24, s[80:83], s85 offen sc1\n" \
"buffer_store_dwordx2 v[224:225], v24, s[80:83], s86 offen sc1\n" \
"buffer_store_dwordx2 v[240:241], v24, s[80:83], s87 offen sc1\n" \
"s_branch L_end_%=\n" \
"L_poll61_%=:\n" \
"s_mov_b32 s84, 0\n" \
"L_pl61_%=:\n" \
"s_sleep 1\n" \
"v_bfe_u32 v44, v20, 2, 5\n" \
"buffer_load_dword v44, v44, s[80:83], s38 offen offset:128 sc1\n" \
"s_waitcnt vmcnt(0)\n" \
"v_cmp_ne_u32_e32 vcc, s62, v44\n" \
"s_add_u32 s84, s84, 1\n" \
"s_nop 3\n" \
"s_cbranch_vccz L_pollr61_%=\n" \
"s_cmp_gt_u32 s84, 0x400000\n" \
"s_cbranch_scc0 L_pl61_%=\n" \
"s_branch L_pollr61_%=\n" \
"L_poll161_%=:\n" \
"s_mov_b32 s84, 0\n" \
"L_pl161_%=:\n" \
"s_sleep 1\n" \
"v_bfe_u32 v44, v20, 2, 5\n" \
"buffer_load_dword v44, v44, s[80:83], s38 offen offset:0 sc1\n" \
"s_waitcnt vmcnt(0)\n" \
"v_cmp_ne_u32_e32 vcc, s62, v44\n" \
"s_add_u32 s84, s84, 1\n" \
"s_nop 3\n" \
"s_cbranch_vccz L_pollr161_%=\n" \
"s_cmp_gt_u32 s84, 0x400000\n" \
"s_cbranch_scc0 L_pl161_%=\n" \
"s_branch L_pollr161_%=\n" \
"L_end_%=:\n"
#define PROJ_VCLOB "v21","v24","v25","v28","v29","v32","v33","v34","v35","v36","v37","v38","v39","v40","v41","v42","v43","v44","v45","v46","v47","v48","v49","v50","v51","v52","v53","v54","v55","v56","v57","v58","v59","v60","v61","v62","v63","v64","v65","v66","v67","v68","v69","v70","v71","v72","v73","v74","v75","v76","v77","v78","v79","v80","v81","v82","v83","v84","v85","v86","v87","v88","v89","v90","v91","v92","v93","v94","v95","v96","v97","v98","v99","v100","v101","v102","v103","v104","v105","v106","v107","v108","v109","v110","v111","v112","v113","v114","v115","v116","v117","v118","v119","v120","v121","v122","v123","v124","v125","v126","v127","v128","v129","v130","v131","v132","v133","v134","v135","v136","v137","v138","v139","v140","v141","v142","v143","v144","v145","v146","v147","v148","v149","v150","v151","v152","v153","v154","v155","v156","v157","v158","v159","v160","v161","v162","v163","v164","v165","v166","v167","v168","v169","v170","v171","v172","v173","v174","v175","v176","v177","v178","v179","v180","v181","v182","v183","v184","v185","v186","v187","v188","v189","v190","v192","v193","v194","v195","v196","v197","v198","v199","v200","v201","v202","v203","v204","v205","v206","v207","v208","v209","v210","v211","v212","v213","v214","v215","v216","v217","v218","v219","v220","v221","v222","v223","v224","v225","v226","v227","v228","v229","v230","v231","v232","v233","v234","v235","v236","v237","v238","v239","v240","v241","v242","v243","v244","v245","v246","v247","v248","v249","v250","v251","v252","v253","v254","v255"
#define PROJ_SCLOB "s4","s5","s6","s7","s32","s33","s34","s35","s36","s37","s38","s39","s40","s41","s42","s43","s44","s45","s46","s47","s48","s49","s50","s51","s52","s53","s54","s55","s63","s64","s65","s66","s67","s68","s69","s70","s71","s72","s73","s74","s75","s76","s77","s78","s79","s80","s81","s82","s83","s84","s85","s86","s87","s88","s89","s90","s91","s92","s93","s94","s95","s96","s97","s98","s99","s100","s101"

typedef const u16* const __attribute__((address_space(4)))* kaptr;
__device__ __forceinline__ void proj_phase(kaptr& ka, char*& ws, const unsigned wid, u32x4 (&vp)[5], unsigned& vl, u32x4 (&sa)[5], u32x2 s24, u32x2& s28, unsigned tdum, u32x4& srdA, u32x2& sd, unsigned& tag
    ) {
    const unsigned b = blockIdx.x, h = (b & 7) * 4 + ((b >> 3) & 3), mt = b >> 5;
    const unsigned rb = (wid < 2u ? 15u - mt : mt) * 128u;
    const unsigned lane = threadIdx.x & 63u, wc = wid & 1u;
    u32x2 vAc, vB;
#pragma unroll
    for (int par = 0; par < 2; ++par) {
        const unsigned lr = lane >> 3, cc = (lane & 7u) ^ (4u * par + (lr >> 1)), r0 = rb + wc * 64u + lr;
        vAc[par] = r0 * (DC * 2) + cc * 16u;
        const unsigned br = lane >> 4, p16 = lane & 15u, sc = (((p16 >> 1) ^ (br | (par << 2))) << 1) | (p16 & 1u);
        vB[par] = (16u * wid + br) * (NBIG * 2) + h * (HD * 2) + sc * 16u;
    }
    vl = lane * 16u;
    u32x2 s30 = {wid * 8192u, 65536u + wid * 4096u};
    asm volatile(PROJ_ASM
                 : "={s62}"(tag), "={s[56:59]}"(srdA), "={s[60:61]}"(sd), "+{s[8:11]}"(sa[0]), "+{s[12:15]}"(sa[1]), "+{s[16:19]}"(sa[2]), "+{s[20:23]}"(sa[3]), "+{s[24:25]}"(s24), "+{s[28:29]}"(s28), "+{s[30:31]}"(s30),
                   "={v[0:3]}"(vp[0]), "={v[4:7]}"(vp[1]), "={v[8:11]}"(vp[2]), "={v[12:15]}"(vp[3]), "={v[16:19]}"(vp[4]), "+{v[22:23]}"(vAc), "+{v[26:27]}"(vB), "+{v191}"(tdum), "+{s[26:27]}"(ws), "+{s[0:1]}"(ka)
                 : "{s2}"(b), "{s3}"(wid), "{v20}"(vl)
                 : "memory", "m0", "vcc", "scc", PROJ_VCLOB, PROJ_SCLOB, ACLOB_LO, ACLOB_HI);
    sa[4] = (u32x4){s24[0], 0u, 0u, 0u};
}

namespace att {
#define DI __device__ __forceinline__
typedef __bf16 bfx8 __attribute__((ext_vector_type(8)));
DI unsigned rfl(unsigned x) { return __builtin_amdgcn_readfirstlane(x); }
DI f32x4 mfma(u32x4 a, u32x4 b, f32x4 c) { return __builtin_amdgcn_mfma_f32_16x16x32_bf16(__builtin_bit_cast(bfx8, a), __builtin_bit_cast(bfx8, b), c, 0, 0, 0); }
typedef __bf16 bfx2 __attribute__((ext_vector_type(2)));
typedef float f32x2 __attribute__((ext_vector_type(2)));
DI unsigned cvtpk2(float lo, float hi) { f32x2 v = {lo, hi}; bfx2 b = __builtin_convertvector(v, bfx2); return __builtin_bit_cast(unsigned, b); }
DI void att_exact(const u16* __restrict__ x, const u16* __restrict__ wq, char* __restrict__ ws, unsigned h, unsigned r0, float g1) {
    const unsigned lane = lane_id(), g = lane >> 4, c = lane & 15u;
    f32x4 o[8];
#pragma unroll
    for (int n = 0; n < 8; ++n) o[n] = (f32x4){0.f, 0.f, 0.f, 0.f};
#pragma unroll 1
    for (unsigned kk = 0; kk < (unsigned)DM; kk += 32u) {
        const unsigned k = kk + 8u * g;
        const u32x4 xf = *reinterpret_cast<const u32x4*>(x + (r0 + c) * DM + k);
#pragma unroll
        for (int n = 0; n < 8; ++n) {
            const u16* w = wq + k * NBIG + h * HD + n * 16 + c; u32x4 wf;
#pragma unroll
            for (int e = 0; e < 4; ++e) wf[e] = (unsigned)w[2 * e * NBIG] | ((unsigned)w[(2 * e + 1) * NBIG] << 16);
            o[n] = mfma(wf, xf, o[n]);
        }
    }
    u32x4 q[4];
#pragma unroll
    for (int p = 0; p < 4; ++p) {
        const f32x4 a = o[2 * p] * 0.12751743082f, b = o[2 * p + 1] * 0.12751743082f;
        q[p] = (u32x4){cvtpk2(a[0], a[1]), cvtpk2(a[2], a[3]), cvtpk2(b[0], b[1]), cvtpk2(b[2], b[3])};
    }
#pragma unroll
    for (int n = 0; n < 8; ++n) o[n] = (f32x4){0.f, 0.f, 0.f, 0.f};
    const __amdgpu_buffer_rsrc_t srd = __builtin_amdgcn_make_buffer_rsrc((void*)(ws + WS_K + (size_t)h * 1048576), (short)0, 1048576, 0x00020000);
    float m = -3.0e38f, l = 0.f;
#pragma unroll 1
    for (unsigned k0 = 0; k0 <= r0 + 15u; k0 += 32u) {
        const unsigned tb = (k0 >> 6) * 32768u + (k0 & 32u) * 256u;
        f32x4 s[2]; float mx = m;
#pragma unroll
        for (int kt = 0; kt < 2; ++kt) {
            s[kt] = (f32x4){0.f, 0.f, 0.f, 0.f};
#pragma unroll
            for (int p = 0; p < 4; ++p) s[kt] = mfma(__builtin_amdgcn_raw_buffer_load_b128(srd, lane * 16u + (kt * 4 + p) * 1024u, tb, 16), q[p], s[kt]);
#pragma unroll
            for (int e = 0; e < 4; ++e) { if (k0 + 16u * kt + 4u * g + e > r0 + c) s[kt][e] = -3.0e38f; mx = fmaxf(mx, s[kt][e]); }
        }
        mx = fmaxf(mx, __shfl_xor(mx, 16)); mx = fmaxf(mx, __shfl_xor(mx, 32));
        const float al = __builtin_amdgcn_exp2f(m - mx); m = mx;
        u32x4 pf;
#pragma unroll
        for (int e = 0; e < 4; ++e) pf[e] = cvtpk2(__builtin_amdgcn_exp2f(s[e >> 1][2 * (e & 1)] - m), __builtin_amdgcn_exp2f(s[e >> 1][2 * (e & 1) + 1] - m));
        l *= al;
#pragma unroll
        for (int e = 0; e < 4; ++e) l += __uint_as_float(pf[e] << 16) + __uint_as_float(pf[e] & 0xffff0000u);
#pragma unroll
        for (int n = 0; n < 8; ++n) o[n] = mfma(__builtin_amdgcn_raw_buffer_load_b128(srd, lane * 16u + 16384u + n * 1024u, tb, 16), pf, o[n] * al);
    }
    l += __shfl_xor(l, 16); l += __shfl_xor(l, 32);
    const float f = g1 / l;
    const __amdgpu_buffer_rsrc_t srdA = __builtin_amdgcn_make_buffer_rsrc((void*)(ws + WS_AF), (short)0, 128 * KS_TOT * 1024, 0x00020000);
#pragma unroll
    for (int p = 0; p < 4; ++p) {
        const f32x4 a = o[2 * p] * f, b = o[2 * p + 1] * f;
        __builtin_amdgcn_raw_buffer_store_b128((u32x4){cvtpk2(a[0], a[1]), cvtpk2(a[2], a[3]), cvtpk2(b[0], b[1]), cvtpk2(b[2], b[3])}, srdA, lane * 16u + p * 1024u, ((r0 >> 4) * KS_TOT + 18u + 4u * h) * 1024u, 16);
    }
}
}

#define ATT_ASM \
"s_branch L_ago_%=\n" \
".p2align 12\n" \
"L_ab:\n" \
".fill (L_ab + 0 - .) / 4, 4, 0xBF800000\n" \
"L_ap0:\n" \
"s_setpc_b64 s[88:89]\n" \
"L_ago_%=:\n" \
"s_mov_b32 s101, 0\n" \
"s_waitcnt vmcnt(15) lgkmcnt(0)\n" \
"s_barrier\n" \
"v_accvgpr_write_b32 a216, 0\n" \
"v_accvgpr_write_b32 a217, 0\n" \
"v_accvgpr_write_b32 a218, 0\n" \
"v_accvgpr_write_b32 a219, 0\n" \
"v_bfe_u32 v61, v20, 2, 5\n" \
"s_mov_b32 s96, s24\n" \
"v_mfma_f32_32x32x16_bf16 v[128:143], a[216:219], a[216:219], 0\n" \
"buffer_load_dword v57, v61, s[56:59], s96 offen sc1\n" \
"v_and_b32_e32 v63, 0x300, v20\n" \
"ds_read_b128 v[96:99], v63 offset:0\n" \
"v_add_u32_e32 v58, s22, v20\n" \
"ds_read_b128 a[0:3], v58 offset:0\n" \
"v_mfma_f32_32x32x16_bf16 v[144:159], a[216:219], a[216:219], 0\n" \
"ds_read_b128 a[16:19], v58 offset:8192\n" \
"v_add_u32_e32 v59, s23, v20\n" \
"ds_read_b128 a[32:35], v59 offset:0\n" \
"ds_read_b128 a[48:51], v59 offset:8192\n" \
"ds_read_b128 v[100:103], v63 offset:1024\n" \
"ds_read_b128 a[4:7], v58 offset:1024\n" \
"v_mfma_f32_32x32x16_bf16 v[160:175], a[216:219], a[216:219], 0\n" \
"ds_read_b128 a[20:23], v58 offset:9216\n" \
"ds_read_b128 a[36:39], v59 offset:1024\n" \
"ds_read_b128 a[52:55], v59 offset:9216\n" \
"s_sub_u32 s67, s12, 0xc000\n" \
"ds_read_b128 v[104:107], v63 offset:2048\n" \
"s_mov_b32 s65, s12\n" \
"v_mfma_f32_32x32x16_bf16 v[176:191], a[216:219], a[216:219], 0\n" \
"v_add_u32_e32 v55, s67, v20\n" \
"v_add_u32_e32 v52, 0xc000, v55\n" \
"s_xor_b32 m0, s65, 0x10000\n" \
"ds_read_b128 a[8:11], v58 offset:2048\n" \
"buffer_load_dwordx4 v52, s[8:11], 0 offen offset:0 sc1 lds\n" \
"v_mfma_f32_32x32x16_bf16 v[192:207], a[216:219], a[216:219], 0\n" \
"ds_read_b128 a[24:27], v58 offset:10240\n" \
"ds_read_b128 a[40:43], v59 offset:2048\n" \
"ds_read_b128 a[56:59], v59 offset:10240\n" \
"ds_read_b128 v[108:111], v63 offset:3072\n" \
"buffer_load_dwordx4 v52, s[8:11], 0 offen offset:1024 sc1 lds\n" \
"v_mfma_f32_32x32x16_bf16 v[208:223], a[216:219], a[216:219], 0\n" \
"ds_read_b128 a[12:15], v58 offset:3072\n" \
"ds_read_b128 a[28:31], v58 offset:11264\n" \
"ds_read_b128 a[44:47], v59 offset:3072\n" \
"ds_read_b128 a[60:63], v59 offset:11264\n" \
"s_waitcnt lgkmcnt(15)\n" \
"v_xor_b32_e32 v96, 0x80008000, v96\n" \
"v_mfma_f32_32x32x16_bf16 v[224:239], a[216:219], a[216:219], 0\n" \
"v_xor_b32_e32 v97, 0x80008000, v97\n" \
"v_xor_b32_e32 v98, 0x80008000, v98\n" \
"v_xor_b32_e32 v99, 0x80008000, v99\n" \
"ds_read_b128 a[160:163], v20 offset:0\n" \
"ds_read_b128 a[164:167], v20 offset:1024\n" \
"s_waitcnt lgkmcnt(15)\n" \
"v_xor_b32_e32 v100, 0x80008000, v100\n" \
"v_mfma_f32_32x32x16_bf16 v[240:255], a[216:219], a[216:219], 0\n" \
"v_xor_b32_e32 v101, 0x80008000, v101\n" \
"v_xor_b32_e32 v102, 0x80008000, v102\n" \
"v_xor_b32_e32 v103, 0x80008000, v103\n" \
"ds_read_b128 a[168:171], v20 offset:2048\n" \
"ds_read_b128 a[172:175], v20 offset:3072\n" \
"ds_read_b128 a[188:191], v20 offset:7168\n" \
"v_mfma_f32_16x16x32_bf16 a[192:195], a[216:219], a[216:219], 0\n" \
"ds_read_b128 a[184:187], v20 offset:6144\n" \
"s_waitcnt lgkmcnt(15)\n" \
"v_xor_b32_e32 v104, 0x80008000, v104\n" \
"v_mfma_f32_16x16x32_bf16 a[196:199], a[216:219], a[216:219], 0\n" \
"v_xor_b32_e32 v105, 0x80008000, v105\n" \
"v_xor_b32_e32 v106, 0x80008000, v106\n" \
"v_mfma_f32_16x16x32_bf16 v[32:35], v[96:99], a[0:3], 0\n" \
"v_xor_b32_e32 v107, 0x80008000, v107\n" \
"ds_read_b128 a[180:183], v20 offset:5120\n" \
"v_mfma_f32_16x16x32_bf16 v[36:39], v[96:99], a[16:19], 0\n" \
"ds_read_b128 a[176:179], v20 offset:4096\n" \
"s_waitcnt lgkmcnt(12)\n" \
"v_xor_b32_e32 v108, 0x80008000, v108\n" \
"v_mfma_f32_16x16x32_bf16 v[40:43], v[96:99], a[32:35], 0\n" \
"v_xor_b32_e32 v109, 0x80008000, v109\n" \
"v_xor_b32_e32 v110, 0x80008000, v110\n" \
"v_mfma_f32_16x16x32_bf16 v[44:47], v[96:99], a[48:51], 0\n" \
"v_xor_b32_e32 v111, 0x80008000, v111\n" \
"v_add_u32_e32 v60, s15, v20\n" \
"v_mfma_f32_16x16x32_bf16 v[32:35], v[100:103], a[4:7], v[32:35]\n" \
"v_add_u32_e32 v60, 0x10000, v60\n" \
"ds_read_b128 a[128:131], v20 offset:8192\n" \
"v_mfma_f32_16x16x32_bf16 v[36:39], v[100:103], a[20:23], v[36:39]\n" \
"ds_read_b128 a[144:147], v20 offset:12288\n" \
"ds_read_b128 a[132:135], v20 offset:9216\n" \
"v_mfma_f32_16x16x32_bf16 v[40:43], v[100:103], a[36:39], v[40:43]\n" \
"ds_read_b128 a[148:151], v20 offset:13312\n" \
"ds_read_b128 a[136:139], v20 offset:10240\n" \
"v_mfma_f32_16x16x32_bf16 v[44:47], v[100:103], a[52:55], v[44:47]\n" \
"ds_read_b128 a[152:155], v20 offset:14336\n" \
"ds_read_b128 a[140:143], v20 offset:11264\n" \
"v_mfma_f32_16x16x32_bf16 v[32:35], v[104:107], a[8:11], v[32:35]\n" \
"ds_read_b128 a[156:159], v20 offset:15360\n" \
"ds_read_b128 a[64:67], v60 offset:16384\n" \
"v_mfma_f32_16x16x32_bf16 v[36:39], v[104:107], a[24:27], v[36:39]\n" \
"ds_read_b128 a[68:71], v60 offset:17408\n" \
"ds_read_b128 a[72:75], v60 offset:18432\n" \
"v_mfma_f32_16x16x32_bf16 v[40:43], v[104:107], a[40:43], v[40:43]\n" \
"ds_read_b128 a[76:79], v60 offset:19456\n" \
"ds_read_b128 a[80:83], v60 offset:24576\n" \
"v_mfma_f32_16x16x32_bf16 v[44:47], v[104:107], a[56:59], v[44:47]\n" \
"ds_read_b128 a[84:87], v60 offset:25600\n" \
"s_waitcnt lgkmcnt(15)\n" \
"v_mfma_f32_16x16x32_bf16 v[32:35], v[108:111], a[12:15], v[32:35]\n" \
"ds_read_b128 a[88:91], v60 offset:26624\n" \
"ds_read_b128 a[92:95], v60 offset:27648\n" \
"v_mfma_f32_16x16x32_bf16 v[36:39], v[108:111], a[28:31], v[36:39]\n" \
"ds_read_b128 a[96:99], v60 offset:0\n" \
"ds_read_b128 a[100:103], v60 offset:1024\n" \
"v_mfma_f32_16x16x32_bf16 v[40:43], v[108:111], a[44:47], v[40:43]\n" \
"ds_read_b128 a[104:107], v60 offset:2048\n" \
"ds_read_b128 a[108:111], v60 offset:3072\n" \
"v_mfma_f32_16x16x32_bf16 v[44:47], v[108:111], a[60:63], v[44:47]\n" \
"ds_read_b128 a[112:115], v60 offset:8192\n" \
"ds_read_b128 a[116:119], v60 offset:9216\n" \
"v_mfma_f32_16x16x32_bf16 v[64:67], a[160:163], a[0:3], v[32:35]\n" \
"ds_read_b128 a[120:123], v60 offset:10240\n" \
"ds_read_b128 a[124:127], v60 offset:11264\n" \
"v_mfma_f32_16x16x32_bf16 v[64:67], a[164:167], a[4:7], v[64:67]\n" \
"v_bfe_u32 v26, v20, 8, 2\n" \
"s_mov_b32 m0, s20\n" \
"v_mfma_f32_16x16x32_bf16 v[64:67], a[168:171], a[8:11], v[64:67]\n" \
"v_bfe_u32 v25, v20, 5, 3\n" \
"v_lshlrev_b32_e32 v26, 1, v26\n" \
"v_mfma_f32_16x16x32_bf16 v[64:67], a[172:175], a[12:15], v[64:67]\n" \
"global_load_lds_dword v[16:17], off\n" \
"v_mfma_f32_16x16x32_bf16 v[68:71], a[188:191], a[12:15], v[32:35]\n" \
"v_sub_u32_e32 v25, v25, v26\n" \
"v_and_b32_e32 v26, 16, v20\n" \
"v_mfma_f32_16x16x32_bf16 v[68:71], a[184:187], a[8:11], v[68:71]\n" \
"v_mov_b32_e32 v27, 0x3f80\n" \
"v_lshlrev_b32_e32 v26, v26, v27\n" \
"v_mfma_f32_16x16x32_bf16 v[68:71], a[180:183], a[4:7], v[68:71]\n" \
"v_mov_b32_e32 v54, 0x3f803f80\n" \
"s_waitcnt lgkmcnt(15)\n" \
"v_mfma_f32_16x16x32_bf16 v[68:71], a[176:179], a[0:3], v[68:71]\n" \
"s_mov_b32 s84, 0x33333333\n" \
"s_mov_b32 s85, 0xcccccccc\n" \
"v_mfma_f32_16x16x32_bf16 v[76:79], a[176:179], a[16:19], v[36:39]\n" \
"s_mov_b32 s86, 0x11111111\n" \
"s_mov_b32 s87, 0x11111111\n" \
"v_mfma_f32_16x16x32_bf16 v[76:79], a[180:183], a[20:23], v[76:79]\n" \
"s_mov_b32 s88, 0x44444444\n" \
"s_mov_b32 s89, 0x44444444\n" \
"v_mfma_f32_16x16x32_bf16 v[76:79], a[184:187], a[24:27], v[76:79]\n" \
"v_exp_f32_e32 v70, v70\n" \
"v_mfma_f32_16x16x32_bf16 v[76:79], a[188:191], a[28:31], v[76:79]\n" \
"v_exp_f32_e32 v71, v71\n" \
"v_mfma_f32_16x16x32_bf16 v[72:75], a[172:175], a[28:31], v[36:39]\n" \
"v_exp_f32_e32 v68, v68\n" \
"v_mfma_f32_16x16x32_bf16 v[72:75], a[168:171], a[24:27], v[72:75]\n" \
"v_exp_f32_e32 v69, v69\n" \
"v_mfma_f32_16x16x32_bf16 v[72:75], a[164:167], a[20:23], v[72:75]\n" \
"v_bfe_i32 v21, v20, 4, 1\n" \
"v_cvt_pk_bf16_f32 v71, v70, v71\n" \
"v_mfma_f32_16x16x32_bf16 v[72:75], a[160:163], a[16:19], v[72:75]\n" \
"v_exp_f32_e32 v66, v66\n" \
"v_mfma_f32_16x16x32_bf16 v[80:83], a[160:163], a[32:35], v[40:43]\n" \
"v_exp_f32_e32 v67, v67\n" \
"v_mfma_f32_16x16x32_bf16 v[80:83], a[164:167], a[36:39], v[80:83]\n" \
"v_exp_f32_e32 v64, v64\n" \
"v_mfma_f32_16x16x32_bf16 v[80:83], a[168:171], a[40:43], v[80:83]\n" \
"v_exp_f32_e32 v65, v65\n" \
"v_mfma_f32_16x16x32_bf16 v[80:83], a[172:175], a[44:47], v[80:83]\n" \
"v_exp_f32_e32 v72, v72\n" \
"v_mfma_f32_16x16x32_bf16 v[84:87], a[188:191], a[44:47], v[40:43]\n" \
"s_waitcnt vmcnt(3)\n" \
"v_cmp_ne_u32_e32 vcc, s62, v57\n" \
"v_mfma_f32_16x16x32_bf16 v[84:87], a[184:187], a[40:43], v[84:87]\n" \
"v_exp_f32_e32 v73, v73\n" \
"v_mfma_f32_16x16x32_bf16 v[84:87], a[180:183], a[36:39], v[84:87]\n" \
"v_exp_f32_e32 v74, v74\n" \
"v_mfma_f32_16x16x32_bf16 v[84:87], a[176:179], a[32:35], v[84:87]\n" \
"s_cbranch_vccnz L_skb_%=\n" \
"L_rkb_%=:\n" \
"v_cmp_eq_u32_e32 vcc, 0, v25\n" \
"v_mfma_f32_16x16x32_bf16 v[92:95], a[176:179], a[48:51], v[44:47]\n" \
"v_exp_f32_e32 v75, v75\n" \
"v_mfma_f32_16x16x32_bf16 v[92:95], a[180:183], a[52:55], v[92:95]\n" \
"v_cndmask_b32_e32 v28, 0, v26, vcc\n" \
"v_cvt_pk_bf16_f32 v72, v72, v73\n" \
"v_mfma_f32_16x16x32_bf16 v[92:95], a[184:187], a[56:59], v[92:95]\n" \
"v_exp_f32_e32 v76, v76\n" \
"v_mfma_f32_16x16x32_bf16 v[92:95], a[188:191], a[60:63], v[92:95]\n" \
"v_exp_f32_e32 v77, v77\n" \
"v_mfma_f32_16x16x32_bf16 v[88:91], a[172:175], a[60:63], v[44:47]\n" \
"v_exp_f32_e32 v78, v78\n" \
"v_mfma_f32_16x16x32_bf16 v[88:91], a[168:171], a[56:59], v[88:91]\n" \
"v_exp_f32_e32 v79, v79\n" \
"v_mfma_f32_16x16x32_bf16 v[88:91], a[164:167], a[52:55], v[88:91]\n" \
"v_cndmask_b32_e64 v22, 0, v54, s[84:85]\n" \
"v_cndmask_b32_e64 v23, 0, v54, s[86:87]\n" \
"v_mfma_f32_16x16x32_bf16 v[88:91], a[160:163], a[48:51], v[88:91]\n" \
"v_cndmask_b32_e64 v24, 0, v54, s[88:89]\n" \
"v_and_b32_e32 v21, 0xaaaaaaaa, v21\n" \
"s_waitcnt vmcnt(4) lgkmcnt(0)\n" \
"s_barrier\n" \
"v_cmp_eq_u32_e32 vcc, 1, v25\n" \
"v_cvt_pk_bf16_f32 v70, v68, v69\n" \
"v_cvt_pk_bf16_f32 v73, v74, v75\n" \
"v_mov_b32_e32 v62, 0\n" \
"v_accvgpr_write_b32 a208, 0\n" \
"v_accvgpr_write_b32 a209, 0\n" \
"v_accvgpr_write_b32 a210, 0\n" \
"v_accvgpr_write_b32 a211, 0\n" \
"v_accvgpr_write_b32 a200, v22\n" \
"v_accvgpr_write_b32 a201, v22\n" \
"v_accvgpr_write_b32 a202, v22\n" \
"v_accvgpr_write_b32 a203, v22\n" \
"v_accvgpr_write_b32 a204, v23\n" \
"v_accvgpr_write_b32 a205, v23\n" \
"v_accvgpr_write_b32 a206, v23\n" \
"v_accvgpr_write_b32 a207, v23\n" \
"v_accvgpr_write_b32 a212, v24\n" \
"v_accvgpr_write_b32 a213, v24\n" \
"v_accvgpr_write_b32 a214, v24\n" \
"v_accvgpr_write_b32 a215, v24\n" \
"v_or_b32_e32 v21, 0x44444444, v21\n" \
"s_mov_b32 s64, 0x8000\n" \
"v_add_u32_e32 v50, 0x6000, v55\n" \
"v_add_u32_e32 v51, 0xa000, v55\n" \
"v_add_u32_e32 v53, 0x10000, v55\n" \
"v_add_u32_e32 v48, 0xc000, v20\n" \
"v_add_u32_e32 v49, s17, v20\n" \
"v_cndmask_b32_e32 v29, 0, v26, vcc\n" \
"v_accvgpr_write_b32 a220, v96\n" \
"v_accvgpr_write_b32 a221, v97\n" \
"v_accvgpr_write_b32 a222, v98\n" \
"v_accvgpr_write_b32 a223, v99\n" \
"v_accvgpr_write_b32 a224, v100\n" \
"v_accvgpr_write_b32 a225, v101\n" \
"v_accvgpr_write_b32 a226, v102\n" \
"v_accvgpr_write_b32 a227, v103\n" \
"v_accvgpr_write_b32 a228, v104\n" \
"v_accvgpr_write_b32 a229, v105\n" \
"v_accvgpr_write_b32 a230, v106\n" \
"v_accvgpr_write_b32 a231, v107\n" \
"v_accvgpr_write_b32 a232, v108\n" \
"v_accvgpr_write_b32 a233, v109\n" \
"v_accvgpr_write_b32 a234, v110\n" \
"v_accvgpr_write_b32 a235, v111\n" \
"v_cvt_pk_bf16_f32 v69, v66, v67\n" \
"v_cvt_pk_bf16_f32 v68, v64, v65\n" \
"v_cvt_pk_bf16_f32 v74, v76, v77\n" \
"v_cvt_pk_bf16_f32 v75, v78, v79\n" \
"s_sub_u32 s66, s18, 1\n" \
"s_cbranch_scc1 L_tr_%=\n" \
".fill (L_ab + 1856 - .) / 4, 4, 0xBF800000\n" \
"L_1_%=:\n" \
"v_mfma_f32_16x16x32_bf16 v[96:99], a[128:131], a[0:3], v[32:35]\n" \
"s_xor_b32 m0, s65, 0x12000\n" \
"ds_read_b128 a[160:163], v49 offset:0\n" \
"v_mfma_f32_16x16x32_bf16 v[96:99], a[132:135], a[4:7], v[96:99]\n" \
"buffer_load_dwordx4 v50, s[8:11], s64 offen offset:0 sc1 lds\n" \
"v_mfma_f32_16x16x32_bf16 v[96:99], a[136:139], a[8:11], v[96:99]\n" \
"ds_read_b128 a[164:167], v49 offset:1024\n" \
"ds_read_b128 a[168:171], v49 offset:2048\n" \
"v_mfma_f32_16x16x32_bf16 v[96:99], a[140:143], a[12:15], v[96:99]\n" \
"ds_read_b128 a[172:175], v49 offset:3072\n" \
"ds_read_b128 a[176:179], v49 offset:4096\n" \
"v_mfma_f32_16x16x32_bf16 v[100:103], a[156:159], a[12:15], v[32:35]\n" \
"ds_read_b128 a[180:183], v49 offset:5120\n" \
"ds_read_b128 a[184:187], v49 offset:6144\n" \
"v_mfma_f32_16x16x32_bf16 v[100:103], a[152:155], a[8:11], v[100:103]\n" \
"v_exp_f32_e64 v86, v86\n" \
"v_mfma_f32_16x16x32_bf16 v[100:103], a[148:151], a[4:7], v[100:103]\n" \
"v_exp_f32_e64 v87, v87\n" \
"v_mfma_f32_16x16x32_bf16 v[100:103], a[144:147], a[0:3], v[100:103]\n" \
"ds_read_b128 a[188:191], v49 offset:7168\n" \
"v_cvt_pk_bf16_f32 v87, v86, v87\n" \
"v_mfma_f32_16x16x32_bf16 v[108:111], a[144:147], a[16:19], v[36:39]\n" \
"v_exp_f32_e64 v84, v84\n" \
"v_mfma_f32_16x16x32_bf16 v[108:111], a[148:151], a[20:23], v[108:111]\n" \
"v_exp_f32_e64 v85, v85\n" \
"v_mfma_f32_16x16x32_bf16 v[108:111], a[152:155], a[24:27], v[108:111]\n" \
"v_exp_f32_e64 v82, v82\n" \
"v_mfma_f32_16x16x32_bf16 v[108:111], a[156:159], a[28:31], v[108:111]\n" \
"v_exp_f32_e64 v83, v83\n" \
"v_mfma_f32_16x16x32_bf16 v[104:107], a[140:143], a[28:31], v[36:39]\n" \
"v_exp_f32_e64 v80, v80\n" \
"v_mfma_f32_16x16x32_bf16 v[104:107], a[136:139], a[24:27], v[104:107]\n" \
"v_exp_f32_e64 v81, v81\n" \
"v_mfma_f32_16x16x32_bf16 v[104:107], a[132:135], a[20:23], v[104:107]\n" \
"v_exp_f32_e64 v88, v88\n" \
"v_mfma_f32_16x16x32_bf16 v[104:107], a[128:131], a[16:19], v[104:107]\n" \
"buffer_load_dwordx4 v50, s[8:11], s64 offen offset:1024 sc1 lds\n" \
"v_mfma_f32_16x16x32_bf16 v[112:115], a[128:131], a[32:35], v[40:43]\n" \
"s_xor_b32 m0, s65, 0x6000\n" \
"v_cvt_pk_bf16_f32 v86, v84, v85\n" \
"v_mfma_f32_16x16x32_bf16 v[112:115], a[132:135], a[36:39], v[112:115]\n" \
"v_cvt_pk_bf16_f32 v85, v82, v83\n" \
"v_cvt_pk_bf16_f32 v84, v80, v81\n" \
"v_mfma_f32_16x16x32_bf16 v[112:115], a[136:139], a[40:43], v[112:115]\n" \
"v_exp_f32_e64 v89, v89\n" \
"v_mfma_f32_16x16x32_bf16 v[112:115], a[140:143], a[44:47], v[112:115]\n" \
"v_exp_f32_e64 v90, v90\n" \
"v_mfma_f32_16x16x32_bf16 v[116:119], a[156:159], a[44:47], v[40:43]\n" \
"v_exp_f32_e64 v91, v91\n" \
"v_mfma_f32_16x16x32_bf16 v[116:119], a[152:155], a[40:43], v[116:119]\n" \
"v_exp_f32_e64 v92, v92\n" \
"v_mfma_f32_16x16x32_bf16 v[116:119], a[148:151], a[36:39], v[116:119]\n" \
"v_exp_f32_e64 v93, v93\n" \
"v_mfma_f32_16x16x32_bf16 v[116:119], a[144:147], a[32:35], v[116:119]\n" \
"v_exp_f32_e64 v94, v94\n" \
"v_mfma_f32_16x16x32_bf16 v[124:127], a[144:147], a[48:51], v[44:47]\n" \
"v_cvt_pk_bf16_f32 v88, v88, v89\n" \
"v_cvt_pk_bf16_f32 v89, v90, v91\n" \
"v_mfma_f32_16x16x32_bf16 v[124:127], a[148:151], a[52:55], v[124:127]\n" \
"v_exp_f32_e64 v95, v95\n" \
"v_mfma_f32_16x16x32_bf16 v[124:127], a[152:155], a[56:59], v[124:127]\n" \
"v_cvt_pk_bf16_f32 v90, v92, v93\n" \
"v_cvt_pk_bf16_f32 v91, v94, v95\n" \
"v_mfma_f32_16x16x32_bf16 v[124:127], a[156:159], a[60:63], v[124:127]\n" \
"ds_read_b128 a[144:147], v48 offset:4096\n" \
"ds_read_b128 a[148:151], v48 offset:5120\n" \
"v_mfma_f32_16x16x32_bf16 v[120:123], a[140:143], a[60:63], v[44:47]\n" \
"ds_read_b128 a[152:155], v48 offset:6144\n" \
"ds_read_b128 a[140:143], v48 offset:3072\n" \
"v_mfma_f32_16x16x32_bf16 v[120:123], a[136:139], a[56:59], v[120:123]\n" \
"ds_read_b128 a[136:139], v48 offset:2048\n" \
"ds_read_b128 a[156:159], v48 offset:7168\n" \
"v_mfma_f32_16x16x32_bf16 v[120:123], a[132:135], a[52:55], v[120:123]\n" \
"buffer_load_dwordx4 v51, s[8:11], s64 offen offset:0 sc1 lds\n" \
"v_mfma_f32_16x16x32_bf16 v[120:123], a[128:131], a[48:51], v[120:123]\n" \
"ds_read_b128 a[128:131], v48 offset:0\n" \
"s_waitcnt lgkmcnt(7)\n" \
"v_mfma_f32_16x16x32_bf16 v[128:131], a[160:163], v[68:71], v[128:131]\n" \
"v_exp_f32_e32 v102, v102\n" \
"v_mfma_f32_16x16x32_bf16 v[160:163], a[160:163], v[72:75], v[160:163]\n" \
"v_exp_f32_e64 v103, v103\n" \
"v_mfma_f32_16x16x32_bf16 v[132:135], a[164:167], v[68:71], v[132:135]\n" \
"ds_read_b128 a[132:135], v48 offset:1024\n" \
"v_cvt_pk_bf16_f32 v103, v102, v103\n" \
"v_mfma_f32_16x16x32_bf16 v[164:167], a[164:167], v[72:75], v[164:167]\n" \
"v_exp_f32_e64 v100, v100\n" \
"v_mfma_f32_16x16x32_bf16 v[136:139], a[168:171], v[68:71], v[136:139]\n" \
"v_exp_f32_e64 v101, v101\n" \
"v_mfma_f32_16x16x32_bf16 v[168:171], a[168:171], v[72:75], v[168:171]\n" \
"v_exp_f32_e64 v98, v98\n" \
"v_mfma_f32_16x16x32_bf16 v[140:143], a[172:175], v[68:71], v[140:143]\n" \
"v_exp_f32_e64 v99, v99\n" \
"v_mfma_f32_16x16x32_bf16 v[172:175], a[172:175], v[72:75], v[172:175]\n" \
"v_exp_f32_e64 v96, v96\n" \
"v_mfma_f32_16x16x32_bf16 v[144:147], a[176:179], v[68:71], v[144:147]\n" \
"v_exp_f32_e64 v97, v97\n" \
"v_mfma_f32_16x16x32_bf16 v[176:179], a[176:179], v[72:75], v[176:179]\n" \
"v_exp_f32_e64 v104, v104\n" \
"v_mfma_f32_16x16x32_bf16 v[148:151], a[180:183], v[68:71], v[148:151]\n" \
"v_exp_f32_e64 v105, v105\n" \
"v_mfma_f32_16x16x32_bf16 v[180:183], a[180:183], v[72:75], v[180:183]\n" \
"v_cvt_pk_bf16_f32 v102, v100, v101\n" \
"v_cvt_pk_bf16_f32 v101, v98, v99\n" \
"v_mfma_f32_16x16x32_bf16 v[152:155], a[184:187], v[68:71], v[152:155]\n" \
"v_cvt_pk_bf16_f32 v100, v96, v97\n" \
"v_cvt_pk_bf16_f32 v104, v104, v105\n" \
"v_mfma_f32_16x16x32_bf16 v[184:187], a[184:187], v[72:75], v[184:187]\n" \
"buffer_load_dwordx4 v51, s[8:11], s64 offen offset:1024 sc1 lds\n" \
"v_mfma_f32_16x16x32_bf16 v[156:159], a[188:191], v[68:71], v[156:159]\n" \
"v_exp_f32_e64 v106, v106\n" \
"v_mfma_f32_16x16x32_bf16 v[188:191], a[188:191], v[72:75], v[188:191]\n" \
"v_exp_f32_e64 v107, v107\n" \
"v_smfmac_f32_16x16x64_bf16 a[192:195], a[200:203], v[68:75], v21\n" \
"v_exp_f32_e64 v108, v108\n" \
"v_mfma_f32_16x16x32_bf16 v[220:223], a[188:191], v[84:87], v[220:223]\n" \
"v_exp_f32_e64 v109, v109\n" \
"v_mfma_f32_16x16x32_bf16 v[252:255], a[188:191], v[88:91], v[252:255]\n" \
"v_exp_f32_e64 v110, v110\n" \
"v_mfma_f32_16x16x32_bf16 v[216:219], a[184:187], v[84:87], v[216:219]\n" \
"v_exp_f32_e64 v111, v111\n" \
"v_mfma_f32_16x16x32_bf16 v[248:251], a[184:187], v[88:91], v[248:251]\n" \
"v_exp_f32_e64 v118, v118\n" \
"v_mfma_f32_16x16x32_bf16 v[212:215], a[180:183], v[84:87], v[212:215]\n" \
"v_exp_f32_e64 v119, v119\n" \
"v_mfma_f32_16x16x32_bf16 v[244:247], a[180:183], v[88:91], v[244:247]\n" \
"v_cvt_pk_bf16_f32 v105, v106, v107\n" \
"v_cvt_pk_bf16_f32 v106, v108, v109\n" \
"v_mfma_f32_16x16x32_bf16 v[208:211], a[176:179], v[84:87], v[208:211]\n" \
"v_cvt_pk_bf16_f32 v107, v110, v111\n" \
"v_cvt_pk_bf16_f32 v119, v118, v119\n" \
"v_mfma_f32_16x16x32_bf16 v[240:243], a[176:179], v[88:91], v[240:243]\n" \
"v_exp_f32_e64 v116, v116\n" \
"v_mfma_f32_16x16x32_bf16 v[204:207], a[172:175], v[84:87], v[204:207]\n" \
"v_exp_f32_e64 v117, v117\n" \
"v_mfma_f32_16x16x32_bf16 v[236:239], a[172:175], v[88:91], v[236:239]\n" \
"v_exp_f32_e64 v114, v114\n" \
"v_mfma_f32_16x16x32_bf16 v[200:203], a[168:171], v[84:87], v[200:203]\n" \
"v_exp_f32_e64 v115, v115\n" \
"v_mfma_f32_16x16x32_bf16 v[232:235], a[168:171], v[88:91], v[232:235]\n" \
"v_exp_f32_e64 v112, v112\n" \
"v_mfma_f32_16x16x32_bf16 v[196:199], a[164:167], v[84:87], v[196:199]\n" \
"v_exp_f32_e64 v113, v113\n" \
"v_mfma_f32_16x16x32_bf16 v[228:231], a[164:167], v[88:91], v[228:231]\n" \
"v_exp_f32_e64 v120, v120\n" \
"v_mfma_f32_16x16x32_bf16 v[192:195], a[160:163], v[84:87], v[192:195]\n" \
"v_exp_f32_e64 v121, v121\n" \
"v_mfma_f32_16x16x32_bf16 v[224:227], a[160:163], v[88:91], v[224:227]\n" \
"v_cvt_pk_bf16_f32 v118, v116, v117\n" \
"v_cvt_pk_bf16_f32 v117, v114, v115\n" \
"v_smfmac_f32_16x16x64_bf16 a[196:199], a[200:203], v[84:91], v21\n" \
"v_cvt_pk_bf16_f32 v116, v112, v113\n" \
"v_cvt_pk_bf16_f32 v120, v120, v121\n" \
"s_waitcnt vmcnt(4) lgkmcnt(0)\n" \
"s_barrier\n" \
"v_mfma_f32_16x16x32_bf16 v[64:67], a[128:131], a[0:3], v[32:35]\n" \
"s_xor_b32 m0, s65, 0x14000\n" \
"ds_read_b128 a[160:163], v49 offset:8192\n" \
"v_mfma_f32_16x16x32_bf16 v[64:67], a[132:135], a[4:7], v[64:67]\n" \
"buffer_load_dwordx4 v52, s[8:11], s64 offen offset:0 sc1 lds\n" \
"v_mfma_f32_16x16x32_bf16 v[64:67], a[136:139], a[8:11], v[64:67]\n" \
"ds_read_b128 a[164:167], v49 offset:9216\n" \
"ds_read_b128 a[168:171], v49 offset:10240\n" \
"v_mfma_f32_16x16x32_bf16 v[64:67], a[140:143], a[12:15], v[64:67]\n" \
"ds_read_b128 a[172:175], v49 offset:11264\n" \
"ds_read_b128 a[176:179], v49 offset:12288\n" \
"v_mfma_f32_16x16x32_bf16 v[68:71], a[156:159], a[12:15], v[32:35]\n" \
"ds_read_b128 a[180:183], v49 offset:13312\n" \
"ds_read_b128 a[184:187], v49 offset:14336\n" \
"v_mfma_f32_16x16x32_bf16 v[68:71], a[152:155], a[8:11], v[68:71]\n" \
"v_exp_f32_e64 v122, v122\n" \
"v_mfma_f32_16x16x32_bf16 v[68:71], a[148:151], a[4:7], v[68:71]\n" \
"v_exp_f32_e64 v123, v123\n" \
"v_mfma_f32_16x16x32_bf16 v[68:71], a[144:147], a[0:3], v[68:71]\n" \
"ds_read_b128 a[188:191], v49 offset:15360\n" \
"v_cvt_pk_bf16_f32 v121, v122, v123\n" \
"v_mfma_f32_16x16x32_bf16 v[76:79], a[144:147], a[16:19], v[36:39]\n" \
"v_exp_f32_e64 v124, v124\n" \
"v_mfma_f32_16x16x32_bf16 v[76:79], a[148:151], a[20:23], v[76:79]\n" \
"v_exp_f32_e64 v125, v125\n" \
"v_mfma_f32_16x16x32_bf16 v[76:79], a[152:155], a[24:27], v[76:79]\n" \
"v_exp_f32_e64 v126, v126\n" \
"v_mfma_f32_16x16x32_bf16 v[76:79], a[156:159], a[28:31], v[76:79]\n" \
"v_exp_f32_e64 v127, v127\n" \
"v_mfma_f32_16x16x32_bf16 v[72:75], a[140:143], a[28:31], v[36:39]\n" \
"v_cvt_pk_bf16_f32 v122, v124, v125\n" \
"v_cvt_pk_bf16_f32 v123, v126, v127\n" \
"v_mfma_f32_16x16x32_bf16 v[72:75], a[136:139], a[24:27], v[72:75]\n" \
"v_exp_f32_e64 v70, v70\n" \
"v_mfma_f32_16x16x32_bf16 v[72:75], a[132:135], a[20:23], v[72:75]\n" \
"v_exp_f32_e64 v71, v71\n" \
"v_mfma_f32_16x16x32_bf16 v[72:75], a[128:131], a[16:19], v[72:75]\n" \
"buffer_load_dwordx4 v52, s[8:11], s64 offen offset:1024 sc1 lds\n" \
"v_mfma_f32_16x16x32_bf16 v[80:83], a[128:131], a[32:35], v[40:43]\n" \
"s_mov_b32 m0, s65\n" \
"v_cvt_pk_bf16_f32 v71, v70, v71\n" \
"v_mfma_f32_16x16x32_bf16 v[80:83], a[132:135], a[36:39], v[80:83]\n" \
"v_exp_f32_e32 v68, v68\n" \
"v_mfma_f32_16x16x32_bf16 v[80:83], a[136:139], a[40:43], v[80:83]\n" \
"v_exp_f32_e64 v69, v69\n" \
"v_mfma_f32_16x16x32_bf16 v[80:83], a[140:143], a[44:47], v[80:83]\n" \
"v_exp_f32_e64 v72, v72\n" \
"v_mfma_f32_16x16x32_bf16 v[84:87], a[156:159], a[44:47], v[40:43]\n" \
"v_exp_f32_e64 v73, v73\n" \
"v_mfma_f32_16x16x32_bf16 v[84:87], a[152:155], a[40:43], v[84:87]\n" \
"v_exp_f32_e64 v74, v74\n" \
"v_mfma_f32_16x16x32_bf16 v[84:87], a[148:151], a[36:39], v[84:87]\n" \
"v_exp_f32_e64 v75, v75\n" \
"v_mfma_f32_16x16x32_bf16 v[84:87], a[144:147], a[32:35], v[84:87]\n" \
"v_exp_f32_e64 v66, v66\n" \
"v_mfma_f32_16x16x32_bf16 v[92:95], a[144:147], a[48:51], v[44:47]\n" \
"ds_read_b128 a[144:147], v48 offset:12288\n" \
"v_cvt_pk_bf16_f32 v72, v72, v73\n" \
"v_mfma_f32_16x16x32_bf16 v[92:95], a[148:151], a[52:55], v[92:95]\n" \
"ds_read_b128 a[148:151], v48 offset:13312\n" \
"v_cvt_pk_bf16_f32 v70, v68, v69\n" \
"v_mfma_f32_16x16x32_bf16 v[92:95], a[152:155], a[56:59], v[92:95]\n" \
"ds_read_b128 a[152:155], v48 offset:14336\n" \
"v_cvt_pk_bf16_f32 v73, v74, v75\n" \
"v_mfma_f32_16x16x32_bf16 v[92:95], a[156:159], a[60:63], v[92:95]\n" \
"ds_read_b128 a[156:159], v48 offset:15360\n" \
"v_add_u32_e32 v49, 0x10000, v48\n" \
"v_mfma_f32_16x16x32_bf16 v[88:91], a[140:143], a[60:63], v[44:47]\n" \
"ds_read_b128 a[140:143], v48 offset:11264\n" \
"s_xor_b32 s65, s65, 0x4000\n" \
"v_mfma_f32_16x16x32_bf16 v[88:91], a[136:139], a[56:59], v[88:91]\n" \
"v_exp_f32_e64 v67, v67\n" \
"v_mfma_f32_16x16x32_bf16 v[88:91], a[132:135], a[52:55], v[88:91]\n" \
"buffer_load_dwordx4 v53, s[8:11], s64 offen offset:0 sc1 lds\n" \
"v_mfma_f32_16x16x32_bf16 v[88:91], a[128:131], a[48:51], v[88:91]\n" \
"ds_read_b128 a[128:131], v48 offset:8192\n" \
"s_waitcnt lgkmcnt(6)\n" \
"v_mfma_f32_16x16x32_bf16 v[128:131], a[160:163], v[100:103], v[128:131]\n" \
"ds_read_b128 a[132:135], v48 offset:9216\n" \
"ds_read_b128 a[136:139], v48 offset:10240\n" \
"v_mfma_f32_16x16x32_bf16 v[160:163], a[160:163], v[104:107], v[160:163]\n" \
"v_exp_f32_e32 v64, v64\n" \
"v_mfma_f32_16x16x32_bf16 v[132:135], a[164:167], v[100:103], v[132:135]\n" \
"v_exp_f32_e64 v65, v65\n" \
"v_mfma_f32_16x16x32_bf16 v[164:167], a[164:167], v[104:107], v[164:167]\n" \
"v_exp_f32_e64 v76, v76\n" \
"v_mfma_f32_16x16x32_bf16 v[136:139], a[168:171], v[100:103], v[136:139]\n" \
"v_exp_f32_e64 v77, v77\n" \
"v_mfma_f32_16x16x32_bf16 v[168:171], a[168:171], v[104:107], v[168:171]\n" \
"v_exp_f32_e64 v78, v78\n" \
"v_mfma_f32_16x16x32_bf16 v[140:143], a[172:175], v[100:103], v[140:143]\n" \
"v_exp_f32_e64 v79, v79\n" \
"v_mfma_f32_16x16x32_bf16 v[172:175], a[172:175], v[104:107], v[172:175]\n" \
"v_cvt_pk_bf16_f32 v69, v66, v67\n" \
"v_cvt_pk_bf16_f32 v68, v64, v65\n" \
"v_mfma_f32_16x16x32_bf16 v[144:147], a[176:179], v[100:103], v[144:147]\n" \
"v_cvt_pk_bf16_f32 v74, v76, v77\n" \
"v_cvt_pk_bf16_f32 v75, v78, v79\n" \
"v_mfma_f32_16x16x32_bf16 v[176:179], a[176:179], v[104:107], v[176:179]\n" \
"v_xor_b32_e32 v48, 0x4000, v48\n" \
"v_mfma_f32_16x16x32_bf16 v[148:151], a[180:183], v[100:103], v[148:151]\n" \
"v_mfma_f32_16x16x32_bf16 v[180:183], a[180:183], v[104:107], v[180:183]\n" \
"v_mfma_f32_16x16x32_bf16 v[152:155], a[184:187], v[100:103], v[152:155]\n" \
"v_mfma_f32_16x16x32_bf16 v[184:187], a[184:187], v[104:107], v[184:187]\n" \
"buffer_load_dwordx4 v53, s[8:11], s64 offen offset:1024 sc1 lds\n" \
"v_mfma_f32_16x16x32_bf16 v[156:159], a[188:191], v[100:103], v[156:159]\n" \
"s_add_u32 s64, s64, 0x8000\n" \
"s_sub_u32 s66, s66, 1\n" \
"s_nop 0\n" \
"v_mfma_f32_16x16x32_bf16 v[188:191], a[188:191], v[104:107], v[188:191]\n" \
"v_smfmac_f32_16x16x64_bf16 a[192:195], a[200:203], v[100:107], v21\n" \
"v_mfma_f32_16x16x32_bf16 v[220:223], a[188:191], v[116:119], v[220:223]\n" \
"v_mfma_f32_16x16x32_bf16 v[252:255], a[188:191], v[120:123], v[252:255]\n" \
"v_mfma_f32_16x16x32_bf16 v[216:219], a[184:187], v[116:119], v[216:219]\n" \
"v_mfma_f32_16x16x32_bf16 v[248:251], a[184:187], v[120:123], v[248:251]\n" \
"v_mfma_f32_16x16x32_bf16 v[212:215], a[180:183], v[116:119], v[212:215]\n" \
"v_mfma_f32_16x16x32_bf16 v[244:247], a[180:183], v[120:123], v[244:247]\n" \
"v_mfma_f32_16x16x32_bf16 v[208:211], a[176:179], v[116:119], v[208:211]\n" \
"v_mfma_f32_16x16x32_bf16 v[240:243], a[176:179], v[120:123], v[240:243]\n" \
"v_mfma_f32_16x16x32_bf16 v[204:207], a[172:175], v[116:119], v[204:207]\n" \
"v_mfma_f32_16x16x32_bf16 v[236:239], a[172:175], v[120:123], v[236:239]\n" \
"v_mfma_f32_16x16x32_bf16 v[200:203], a[168:171], v[116:119], v[200:203]\n" \
"v_mfma_f32_16x16x32_bf16 v[232:235], a[168:171], v[120:123], v[232:235]\n" \
"v_mfma_f32_16x16x32_bf16 v[196:199], a[164:167], v[116:119], v[196:199]\n" \
"v_mfma_f32_16x16x32_bf16 v[228:231], a[164:167], v[120:123], v[228:231]\n" \
"v_mfma_f32_16x16x32_bf16 v[192:195], a[160:163], v[116:119], v[192:195]\n" \
"v_mfma_f32_16x16x32_bf16 v[224:227], a[160:163], v[120:123], v[224:227]\n" \
"v_smfmac_f32_16x16x64_bf16 a[196:199], a[200:203], v[116:123], v21\n" \
"s_waitcnt vmcnt(4) lgkmcnt(0)\n" \
"s_barrier\n" \
"s_cbranch_scc0 L_1_%=\n" \
"L_tr_%=:\n" \
"s_setpc_b64 s[28:29]\n" \
".fill (L_ab + 4096 - .) / 4, 4, 0xBF800000\n" \
".fill (L_ab + 4096 - .) / 4, 4, 0xBF800000\n" \
"L_ap1:\n" \
"s_setpc_b64 s[88:89]\n" \
"L_gtr:\n" \
"v_mfma_f32_16x16x32_bf16 v[96:99], a[128:131], a[0:3], v[32:35]\n" \
"v_mov_b32_e32 v56, s62\n" \
"v_readlane_b32 s68, v11, 0\n" \
"v_mfma_f32_16x16x32_bf16 v[96:99], a[132:135], a[4:7], v[96:99]\n" \
"s_xor_b32 m0, s65, 0x12000\n" \
"s_mov_b32 s65, s13\n" \
"v_mfma_f32_16x16x32_bf16 v[96:99], a[136:139], a[8:11], v[96:99]\n" \
"buffer_load_dwordx4 v50, s[8:11], s64 offen offset:0 sc1 lds\n" \
"v_mfma_f32_16x16x32_bf16 v[96:99], a[140:143], a[12:15], v[96:99]\n" \
"v_add_u32_e32 v63, s15, v20\n" \
"s_add_u32 s67, s14, 40960\n" \
"v_mfma_f32_16x16x32_bf16 v[100:103], a[156:159], a[12:15], v[32:35]\n" \
"buffer_load_dwordx4 v50, s[8:11], s64 offen offset:1024 sc1 lds\n" \
"s_mov_b64 exec, 1\n" \
"buffer_store_dword v56, off, s[56:59], s68 offset:0 sc1\n" \
"s_mov_b64 exec, -1\n" \
"v_mfma_f32_16x16x32_bf16 v[100:103], a[152:155], a[8:11], v[100:103]\n" \
"v_add_u32_e32 v50, 0x6000, v63\n" \
"s_xor_b32 m0, s65, 0x8000\n" \
"v_mfma_f32_16x16x32_bf16 v[100:103], a[148:151], a[4:7], v[100:103]\n" \
"ds_read_b128 a[160:163], v49 offset:0\n" \
"ds_read_b128 a[164:167], v49 offset:1024\n" \
"v_mfma_f32_16x16x32_bf16 v[100:103], a[144:147], a[0:3], v[100:103]\n" \
"ds_read_b128 a[168:171], v49 offset:2048\n" \
"ds_read_b128 a[172:175], v49 offset:3072\n" \
"v_mfma_f32_16x16x32_bf16 v[108:111], a[144:147], a[16:19], v[36:39]\n" \
"buffer_load_dwordx4 v50, s[8:11], s67 offen offset:0 sc1 lds\n" \
"v_mfma_f32_16x16x32_bf16 v[108:111], a[148:151], a[20:23], v[108:111]\n" \
"ds_read_b128 a[176:179], v49 offset:4096\n" \
"ds_read_b128 a[180:183], v49 offset:5120\n" \
"v_mfma_f32_16x16x32_bf16 v[108:111], a[152:155], a[24:27], v[108:111]\n" \
"ds_read_b128 a[184:187], v49 offset:6144\n" \
"ds_read_b128 a[188:191], v49 offset:7168\n" \
"v_mfma_f32_16x16x32_bf16 v[108:111], a[156:159], a[28:31], v[108:111]\n" \
"s_mov_b32 s64, s14\n" \
"v_add_u32_e32 v51, 0x12000, v63\n" \
"v_mfma_f32_16x16x32_bf16 v[104:107], a[140:143], a[28:31], v[36:39]\n" \
"buffer_load_dwordx4 v50, s[8:11], s67 offen offset:1024 sc1 lds\n" \
"v_mfma_f32_16x16x32_bf16 v[104:107], a[136:139], a[24:27], v[104:107]\n" \
"v_exp_f32_e32 v86, v86\n" \
"v_mfma_f32_16x16x32_bf16 v[104:107], a[132:135], a[20:23], v[104:107]\n" \
"v_exp_f32_e32 v87, v87\n" \
"v_mfma_f32_16x16x32_bf16 v[104:107], a[128:131], a[16:19], v[104:107]\n" \
"v_exp_f32_e32 v84, v84\n" \
"v_mfma_f32_16x16x32_bf16 v[112:115], a[128:131], a[32:35], v[40:43]\n" \
"v_exp_f32_e32 v85, v85\n" \
"v_mfma_f32_16x16x32_bf16 v[112:115], a[132:135], a[36:39], v[112:115]\n" \
"buffer_load_dwordx4 v50, s[8:11], s67 offen offset:2048 sc1 lds\n" \
"v_mfma_f32_16x16x32_bf16 v[112:115], a[136:139], a[40:43], v[112:115]\n" \
"v_cvt_pk_bf16_f32 v87, v86, v87\n" \
"v_cvt_pk_bf16_f32 v86, v84, v85\n" \
"v_mfma_f32_16x16x32_bf16 v[112:115], a[140:143], a[44:47], v[112:115]\n" \
"v_exp_f32_e32 v82, v82\n" \
"v_mfma_f32_16x16x32_bf16 v[116:119], a[156:159], a[44:47], v[40:43]\n" \
"v_exp_f32_e32 v83, v83\n" \
"v_mfma_f32_16x16x32_bf16 v[116:119], a[152:155], a[40:43], v[116:119]\n" \
"buffer_load_dwordx4 v50, s[8:11], s67 offen offset:3072 sc1 lds\n" \
"v_mfma_f32_16x16x32_bf16 v[116:119], a[148:151], a[36:39], v[116:119]\n" \
"s_xor_b32 m0, s65, 0xa000\n" \
"v_cvt_pk_bf16_f32 v85, v82, v83\n" \
"v_mfma_f32_16x16x32_bf16 v[116:119], a[144:147], a[32:35], v[116:119]\n" \
"v_exp_f32_e32 v80, v80\n" \
"v_mfma_f32_16x16x32_bf16 v[124:127], a[144:147], a[48:51], v[44:47]\n" \
"v_exp_f32_e32 v81, v81\n" \
"v_mfma_f32_16x16x32_bf16 v[124:127], a[148:151], a[52:55], v[124:127]\n" \
"buffer_load_dwordx4 v51, s[8:11], s64 offen offset:0 sc1 lds\n" \
"v_mfma_f32_16x16x32_bf16 v[124:127], a[152:155], a[56:59], v[124:127]\n" \
"v_cvt_pk_bf16_f32 v84, v80, v81\n" \
"v_readlane_b32 s93, v11, 19\n" \
"v_mfma_f32_16x16x32_bf16 v[124:127], a[156:159], a[60:63], v[124:127]\n" \
"v_exp_f32_e32 v88, v88\n" \
"v_mfma_f32_16x16x32_bf16 v[120:123], a[140:143], a[60:63], v[44:47]\n" \
"v_exp_f32_e32 v89, v89\n" \
"v_mfma_f32_16x16x32_bf16 v[120:123], a[136:139], a[56:59], v[120:123]\n" \
"v_exp_f32_e32 v90, v90\n" \
"v_mfma_f32_16x16x32_bf16 v[120:123], a[132:135], a[52:55], v[120:123]\n" \
"buffer_load_dwordx4 v51, s[8:11], s64 offen offset:1024 sc1 lds\n" \
"v_mfma_f32_16x16x32_bf16 v[120:123], a[128:131], a[48:51], v[120:123]\n" \
"v_cvt_pk_bf16_f32 v88, v88, v89\n" \
"s_waitcnt lgkmcnt(0)\n" \
"v_mfma_f32_16x16x32_bf16 v[128:131], a[160:163], v[68:71], v[128:131]\n" \
"v_exp_f32_e32 v91, v91\n" \
"v_mfma_f32_16x16x32_bf16 v[160:163], a[160:163], v[72:75], v[160:163]\n" \
"v_exp_f32_e32 v92, v92\n" \
"v_mfma_f32_16x16x32_bf16 v[132:135], a[164:167], v[68:71], v[132:135]\n" \
"buffer_load_dwordx4 v51, s[8:11], s64 offen offset:2048 sc1 lds\n" \
"v_mfma_f32_16x16x32_bf16 v[164:167], a[164:167], v[72:75], v[164:167]\n" \
"v_exp_f32_e32 v93, v93\n" \
"v_mfma_f32_16x16x32_bf16 v[136:139], a[168:171], v[68:71], v[136:139]\n" \
"v_exp_f32_e32 v94, v94\n" \
"v_mfma_f32_16x16x32_bf16 v[168:171], a[168:171], v[72:75], v[168:171]\n" \
"v_exp_f32_e32 v95, v95\n" \
"v_mfma_f32_16x16x32_bf16 v[140:143], a[172:175], v[68:71], v[140:143]\n" \
"buffer_load_dwordx4 v51, s[8:11], s64 offen offset:3072 sc1 lds\n" \
"v_mfma_f32_16x16x32_bf16 v[172:175], a[172:175], v[72:75], v[172:175]\n" \
"v_cvt_pk_bf16_f32 v89, v90, v91\n" \
"v_cvt_pk_bf16_f32 v90, v92, v93\n" \
"v_mfma_f32_16x16x32_bf16 v[144:147], a[176:179], v[68:71], v[144:147]\n" \
"v_cvt_pk_bf16_f32 v91, v94, v95\n" \
"v_readlane_b32 s80, v11, 11\n" \
"v_mfma_f32_16x16x32_bf16 v[176:179], a[176:179], v[72:75], v[176:179]\n" \
"v_readlane_b32 s81, v11, 12\n" \
"s_mov_b32 s82, 0x47fca0\n" \
"v_mfma_f32_16x16x32_bf16 v[148:151], a[180:183], v[68:71], v[148:151]\n" \
"s_mov_b32 s83, 0x20000\n" \
"v_readlane_b32 s92, v11, 26\n" \
"v_mfma_f32_16x16x32_bf16 v[180:183], a[180:183], v[72:75], v[180:183]\n" \
"s_add_u32 m0, s93, 0x0\n" \
"ds_read_b128 a[128:131], v48 offset:0\n" \
"v_mfma_f32_16x16x32_bf16 v[152:155], a[184:187], v[68:71], v[152:155]\n" \
"ds_read_b128 a[144:147], v48 offset:4096\n" \
"ds_read_b128 a[132:135], v48 offset:1024\n" \
"v_mfma_f32_16x16x32_bf16 v[184:187], a[184:187], v[72:75], v[184:187]\n" \
"buffer_load_dwordx4 v0, s[80:83], s92 offen offset:0 lds\n" \
"v_mfma_f32_16x16x32_bf16 v[156:159], a[188:191], v[68:71], v[156:159]\n" \
"ds_read_b128 a[148:151], v48 offset:5120\n" \
"ds_read_b128 a[136:139], v48 offset:2048\n" \
"v_mfma_f32_16x16x32_bf16 v[188:191], a[188:191], v[72:75], v[188:191]\n" \
"ds_read_b128 a[152:155], v48 offset:6144\n" \
"ds_read_b128 a[140:143], v48 offset:3072\n" \
"v_smfmac_f32_16x16x64_bf16 a[192:195], a[200:203], v[68:75], v21\n" \
"ds_read_b128 a[156:159], v48 offset:7168\n" \
"v_add_u32_e32 v52, 0x14000, v63\n" \
"v_mfma_f32_16x16x32_bf16 v[220:223], a[188:191], v[84:87], v[220:223]\n" \
"buffer_load_dwordx4 v1, s[80:83], s92 offen offset:1024 lds\n" \
"v_mfma_f32_16x16x32_bf16 v[252:255], a[188:191], v[88:91], v[252:255]\n" \
"v_exp_f32_e32 v102, v102\n" \
"v_mfma_f32_16x16x32_bf16 v[216:219], a[184:187], v[84:87], v[216:219]\n" \
"buffer_load_dwordx4 v2, s[80:83], s92 offen offset:2048 lds\n" \
"v_mfma_f32_16x16x32_bf16 v[248:251], a[184:187], v[88:91], v[248:251]\n" \
"v_exp_f32_e32 v103, v103\n" \
"v_mfma_f32_16x16x32_bf16 v[212:215], a[180:183], v[84:87], v[212:215]\n" \
"v_add_u32_e32 v53, 0x20000, v63\n" \
"v_cvt_pk_bf16_f32 v103, v102, v103\n" \
"v_mfma_f32_16x16x32_bf16 v[244:247], a[180:183], v[88:91], v[244:247]\n" \
"v_exp_f32_e32 v100, v100\n" \
"v_mfma_f32_16x16x32_bf16 v[208:211], a[176:179], v[84:87], v[208:211]\n" \
"buffer_load_dwordx4 v3, s[80:83], s92 offen offset:3072 lds\n" \
"v_mfma_f32_16x16x32_bf16 v[240:243], a[176:179], v[88:91], v[240:243]\n" \
"v_exp_f32_e32 v101, v101\n" \
"v_mfma_f32_16x16x32_bf16 v[204:207], a[172:175], v[84:87], v[204:207]\n" \
"v_exp_f32_e32 v98, v98\n" \
"v_mfma_f32_16x16x32_bf16 v[236:239], a[172:175], v[88:91], v[236:239]\n" \
"v_exp_f32_e32 v99, v99\n" \
"v_mfma_f32_16x16x32_bf16 v[200:203], a[168:171], v[84:87], v[200:203]\n" \
"v_exp_f32_e32 v96, v96\n" \
"v_mfma_f32_16x16x32_bf16 v[232:235], a[168:171], v[88:91], v[232:235]\n" \
"v_exp_f32_e32 v97, v97\n" \
"v_mfma_f32_16x16x32_bf16 v[196:199], a[164:167], v[84:87], v[196:199]\n" \
"v_exp_f32_e32 v104, v104\n" \
"v_mfma_f32_16x16x32_bf16 v[228:231], a[164:167], v[88:91], v[228:231]\n" \
"v_exp_f32_e32 v105, v105\n" \
"v_mfma_f32_16x16x32_bf16 v[192:195], a[160:163], v[84:87], v[192:195]\n" \
"v_cvt_pk_bf16_f32 v102, v100, v101\n" \
"v_cvt_pk_bf16_f32 v101, v98, v99\n" \
"v_mfma_f32_16x16x32_bf16 v[224:227], a[160:163], v[88:91], v[224:227]\n" \
"v_cvt_pk_bf16_f32 v100, v96, v97\n" \
"v_cvt_pk_bf16_f32 v104, v104, v105\n" \
"v_smfmac_f32_16x16x64_bf16 a[196:199], a[200:203], v[84:91], v21\n" \
"v_exp_f32_e32 v106, v106\n" \
"s_waitcnt vmcnt(15) lgkmcnt(0)\n" \
"s_barrier\n" \
"v_mfma_f32_16x16x32_bf16 v[64:67], a[128:131], a[0:3], v[32:35]\n" \
"s_xor_b32 m0, s65, 0x18000\n" \
"ds_read_b128 a[160:163], v49 offset:8192\n" \
"v_mfma_f32_16x16x32_bf16 v[64:67], a[132:135], a[4:7], v[64:67]\n" \
"buffer_load_dwordx4 v52, s[8:11], s64 offen offset:0 sc1 lds\n" \
"v_mfma_f32_16x16x32_bf16 v[64:67], a[136:139], a[8:11], v[64:67]\n" \
"ds_read_b128 a[164:167], v49 offset:9216\n" \
"ds_read_b128 a[168:171], v49 offset:10240\n" \
"v_mfma_f32_16x16x32_bf16 v[64:67], a[140:143], a[12:15], v[64:67]\n" \
"ds_read_b128 a[172:175], v49 offset:11264\n" \
"ds_read_b128 a[176:179], v49 offset:12288\n" \
"v_mfma_f32_16x16x32_bf16 v[68:71], a[156:159], a[12:15], v[32:35]\n" \
"buffer_load_dwordx4 v52, s[8:11], s64 offen offset:1024 sc1 lds\n" \
"v_mfma_f32_16x16x32_bf16 v[68:71], a[152:155], a[8:11], v[68:71]\n" \
"ds_read_b128 a[180:183], v49 offset:13312\n" \
"ds_read_b128 a[184:187], v49 offset:14336\n" \
"v_mfma_f32_16x16x32_bf16 v[68:71], a[148:151], a[4:7], v[68:71]\n" \
"v_exp_f32_e32 v107, v107\n" \
"v_mfma_f32_16x16x32_bf16 v[68:71], a[144:147], a[0:3], v[68:71]\n" \
"v_exp_f32_e32 v108, v108\n" \
"v_mfma_f32_16x16x32_bf16 v[76:79], a[144:147], a[16:19], v[36:39]\n" \
"v_exp_f32_e32 v109, v109\n" \
"v_mfma_f32_16x16x32_bf16 v[76:79], a[148:151], a[20:23], v[76:79]\n" \
"buffer_load_dwordx4 v52, s[8:11], s64 offen offset:2048 sc1 lds\n" \
"v_mfma_f32_16x16x32_bf16 v[76:79], a[152:155], a[24:27], v[76:79]\n" \
"v_exp_f32_e32 v110, v110\n" \
"v_mfma_f32_16x16x32_bf16 v[76:79], a[156:159], a[28:31], v[76:79]\n" \
"v_exp_f32_e32 v111, v111\n" \
"v_mfma_f32_16x16x32_bf16 v[72:75], a[140:143], a[28:31], v[36:39]\n" \
"v_cvt_pk_bf16_f32 v105, v106, v107\n" \
"v_cvt_pk_bf16_f32 v106, v108, v109\n" \
"v_mfma_f32_16x16x32_bf16 v[72:75], a[136:139], a[24:27], v[72:75]\n" \
"buffer_load_dwordx4 v52, s[8:11], s64 offen offset:3072 sc1 lds\n" \
"v_mfma_f32_16x16x32_bf16 v[72:75], a[132:135], a[20:23], v[72:75]\n" \
"s_mov_b32 m0, s65\n" \
"v_cvt_pk_bf16_f32 v107, v110, v111\n" \
"v_mfma_f32_16x16x32_bf16 v[72:75], a[128:131], a[16:19], v[72:75]\n" \
"ds_read_b128 a[188:191], v49 offset:15360\n" \
"v_add_u32_e32 v49, 0x10000, v48\n" \
"v_mfma_f32_16x16x32_bf16 v[80:83], a[128:131], a[32:35], v[40:43]\n" \
"v_exp_f32_e32 v118, v118\n" \
"v_mfma_f32_16x16x32_bf16 v[80:83], a[132:135], a[36:39], v[80:83]\n" \
"v_exp_f32_e32 v119, v119\n" \
"v_mfma_f32_16x16x32_bf16 v[80:83], a[136:139], a[40:43], v[80:83]\n" \
"buffer_load_dwordx4 v53, s[8:11], s64 offen offset:0 sc1 lds\n" \
"v_mfma_f32_16x16x32_bf16 v[80:83], a[140:143], a[44:47], v[80:83]\n" \
"v_exp_f32_e32 v116, v116\n" \
"v_mfma_f32_16x16x32_bf16 v[84:87], a[156:159], a[44:47], v[40:43]\n" \
"v_exp_f32_e32 v117, v117\n" \
"v_mfma_f32_16x16x32_bf16 v[84:87], a[152:155], a[40:43], v[84:87]\n" \
"v_cvt_pk_bf16_f32 v119, v118, v119\n" \
"v_cvt_pk_bf16_f32 v118, v116, v117\n" \
"v_mfma_f32_16x16x32_bf16 v[84:87], a[148:151], a[36:39], v[84:87]\n" \
"v_exp_f32_e32 v114, v114\n" \
"v_mfma_f32_16x16x32_bf16 v[84:87], a[144:147], a[32:35], v[84:87]\n" \
"buffer_load_dwordx4 v53, s[8:11], s64 offen offset:1024 sc1 lds\n" \
"v_mfma_f32_16x16x32_bf16 v[92:95], a[144:147], a[48:51], v[44:47]\n" \
"v_exp_f32_e32 v115, v115\n" \
"v_mfma_f32_16x16x32_bf16 v[92:95], a[148:151], a[52:55], v[92:95]\n" \
"v_exp_f32_e32 v112, v112\n" \
"v_mfma_f32_16x16x32_bf16 v[92:95], a[152:155], a[56:59], v[92:95]\n" \
"v_exp_f32_e32 v113, v113\n" \
"v_mfma_f32_16x16x32_bf16 v[92:95], a[156:159], a[60:63], v[92:95]\n" \
"buffer_load_dwordx4 v53, s[8:11], s64 offen offset:2048 sc1 lds\n" \
"v_mfma_f32_16x16x32_bf16 v[88:91], a[140:143], a[60:63], v[44:47]\n" \
"v_cvt_pk_bf16_f32 v117, v114, v115\n" \
"v_cvt_pk_bf16_f32 v116, v112, v113\n" \
"v_mfma_f32_16x16x32_bf16 v[88:91], a[136:139], a[56:59], v[88:91]\n" \
"v_exp_f32_e32 v120, v120\n" \
"v_mfma_f32_16x16x32_bf16 v[88:91], a[132:135], a[52:55], v[88:91]\n" \
"v_exp_f32_e32 v121, v121\n" \
"v_mfma_f32_16x16x32_bf16 v[88:91], a[128:131], a[48:51], v[88:91]\n" \
"v_cvt_pk_bf16_f32 v120, v120, v121\n" \
"s_waitcnt lgkmcnt(0)\n" \
"v_mfma_f32_16x16x32_bf16 v[128:131], a[160:163], v[100:103], v[128:131]\n" \
"buffer_load_dwordx4 v53, s[8:11], s64 offen offset:3072 sc1 lds\n" \
"v_mfma_f32_16x16x32_bf16 v[160:163], a[160:163], v[104:107], v[160:163]\n" \
"s_add_u32 m0, s93, 0x1000\n" \
"ds_read_b128 a[128:131], v48 offset:8192\n" \
"v_mfma_f32_16x16x32_bf16 v[132:135], a[164:167], v[100:103], v[132:135]\n" \
"v_exp_f32_e32 v122, v122\n" \
"v_mfma_f32_16x16x32_bf16 v[164:167], a[164:167], v[104:107], v[164:167]\n" \
"v_exp_f32_e32 v123, v123\n" \
"v_mfma_f32_16x16x32_bf16 v[136:139], a[168:171], v[100:103], v[136:139]\n" \
"buffer_load_dwordx4 v4, s[80:83], s92 offen offset:0 lds\n" \
"v_mfma_f32_16x16x32_bf16 v[168:171], a[168:171], v[104:107], v[168:171]\n" \
"v_exp_f32_e32 v124, v124\n" \
"v_mfma_f32_16x16x32_bf16 v[140:143], a[172:175], v[100:103], v[140:143]\n" \
"v_exp_f32_e32 v125, v125\n" \
"v_mfma_f32_16x16x32_bf16 v[172:175], a[172:175], v[104:107], v[172:175]\n" \
"v_exp_f32_e32 v126, v126\n" \
"v_mfma_f32_16x16x32_bf16 v[144:147], a[176:179], v[100:103], v[144:147]\n" \
"v_exp_f32_e32 v127, v127\n" \
"v_mfma_f32_16x16x32_bf16 v[176:179], a[176:179], v[104:107], v[176:179]\n" \
"v_cvt_pk_bf16_f32 v121, v122, v123\n" \
"v_cvt_pk_bf16_f32 v122, v124, v125\n" \
"v_mfma_f32_16x16x32_bf16 v[148:151], a[180:183], v[100:103], v[148:151]\n" \
"buffer_load_dwordx4 v5, s[80:83], s92 offen offset:1024 lds\n" \
"v_mfma_f32_16x16x32_bf16 v[180:183], a[180:183], v[104:107], v[180:183]\n" \
"v_cvt_pk_bf16_f32 v123, v126, v127\n" \
"ds_read_b128 a[144:147], v48 offset:12288\n" \
"v_mfma_f32_16x16x32_bf16 v[152:155], a[184:187], v[100:103], v[152:155]\n" \
"ds_read_b128 a[132:135], v48 offset:9216\n" \
"ds_read_b128 a[148:151], v48 offset:13312\n" \
"v_mfma_f32_16x16x32_bf16 v[184:187], a[184:187], v[104:107], v[184:187]\n" \
"ds_read_b128 a[136:139], v48 offset:10240\n" \
"ds_read_b128 a[152:155], v48 offset:14336\n" \
"v_mfma_f32_16x16x32_bf16 v[156:159], a[188:191], v[100:103], v[156:159]\n" \
"ds_read_b128 a[140:143], v48 offset:11264\n" \
"ds_read_b128 a[156:159], v48 offset:15360\n" \
"v_mfma_f32_16x16x32_bf16 v[188:191], a[188:191], v[104:107], v[188:191]\n" \
"buffer_load_dwordx4 v6, s[80:83], s92 offen offset:2048 lds\n" \
"v_smfmac_f32_16x16x64_bf16 a[192:195], a[200:203], v[100:107], v21\n" \
"v_exp_f32_e32 v70, v70\n" \
"v_mfma_f32_16x16x32_bf16 v[220:223], a[188:191], v[116:119], v[220:223]\n" \
"v_exp_f32_e32 v71, v71\n" \
"v_mfma_f32_16x16x32_bf16 v[252:255], a[188:191], v[120:123], v[252:255]\n" \
"buffer_load_dwordx4 v7, s[80:83], s92 offen offset:3072 lds\n" \
"v_mfma_f32_16x16x32_bf16 v[216:219], a[184:187], v[116:119], v[216:219]\n" \
"s_add_u32 m0, s93, 0x2000\n" \
"v_cvt_pk_bf16_f32 v71, v70, v71\n" \
"v_mfma_f32_16x16x32_bf16 v[248:251], a[184:187], v[120:123], v[248:251]\n" \
"v_exp_f32_e32 v72, v72\n" \
"v_mfma_f32_16x16x32_bf16 v[212:215], a[180:183], v[116:119], v[212:215]\n" \
"v_exp_f32_e32 v73, v73\n" \
"v_mfma_f32_16x16x32_bf16 v[244:247], a[180:183], v[120:123], v[244:247]\n" \
"v_exp_f32_e32 v68, v68\n" \
"v_mfma_f32_16x16x32_bf16 v[208:211], a[176:179], v[116:119], v[208:211]\n" \
"buffer_load_dwordx4 v8, s[80:83], s92 offen offset:0 lds\n" \
"v_mfma_f32_16x16x32_bf16 v[240:243], a[176:179], v[120:123], v[240:243]\n" \
"v_exp_f32_e32 v69, v69\n" \
"v_mfma_f32_16x16x32_bf16 v[204:207], a[172:175], v[116:119], v[204:207]\n" \
"v_exp_f32_e32 v74, v74\n" \
"v_mfma_f32_16x16x32_bf16 v[236:239], a[172:175], v[120:123], v[236:239]\n" \
"v_exp_f32_e32 v75, v75\n" \
"v_mfma_f32_16x16x32_bf16 v[200:203], a[168:171], v[116:119], v[200:203]\n" \
"v_exp_f32_e32 v66, v66\n" \
"v_mfma_f32_16x16x32_bf16 v[232:235], a[168:171], v[120:123], v[232:235]\n" \
"v_exp_f32_e32 v67, v67\n" \
"v_mfma_f32_16x16x32_bf16 v[196:199], a[164:167], v[116:119], v[196:199]\n" \
"v_exp_f32_e32 v64, v64\n" \
"v_mfma_f32_16x16x32_bf16 v[228:231], a[164:167], v[120:123], v[228:231]\n" \
"v_exp_f32_e32 v65, v65\n" \
"v_mfma_f32_16x16x32_bf16 v[192:195], a[160:163], v[116:119], v[192:195]\n" \
"v_cvt_pk_bf16_f32 v72, v72, v73\n" \
"v_cvt_pk_bf16_f32 v70, v68, v69\n" \
"v_mfma_f32_16x16x32_bf16 v[224:227], a[160:163], v[120:123], v[224:227]\n" \
"v_exp_f32_e32 v76, v76\n" \
"v_smfmac_f32_16x16x64_bf16 a[196:199], a[200:203], v[116:123], v21\n" \
"v_exp_f32_e32 v77, v77\n" \
"s_waitcnt vmcnt(13) lgkmcnt(0)\n" \
"s_barrier\n" \
"v_exp_f32_e32 v78, v78\n" \
"v_exp_f32_e32 v79, v79\n" \
"v_cvt_pk_bf16_f32 v73, v74, v75\n" \
"v_cvt_pk_bf16_f32 v69, v66, v67\n" \
"v_cvt_pk_bf16_f32 v68, v64, v65\n" \
"v_cvt_pk_bf16_f32 v74, v76, v77\n" \
"v_cvt_pk_bf16_f32 v75, v78, v79\n" \
"v_add_u32_e32 v48, s16, v20\n" \
"s_xor_b32 s65, s65, 0x8000\n" \
"s_add_u32 s64, s64, 0x10000\n" \
"s_getpc_b64 s[86:87]\n" \
"L_hpt_%=:\n" \
"s_add_u32 s86, s86, L_ap8-L_hpt_%=\n" \
"s_addc_u32 s87, s87, 0\n" \
"s_add_u32 s86, s86, s15\n" \
"s_addc_u32 s87, s87, 0\n" \
"s_mov_b64 exec, 0\n" \
"s_swappc_b64 s[88:89], s[86:87]\n" \
"s_mov_b64 exec, -1\n" \
"s_sub_u32 s66, s19, 1\n" \
"s_cbranch_scc1 L_la_%=\n" \
".fill (L_ab + 6464 - .) / 4, 4, 0xBF800000\n" \
"L_2_%=:\n" \
"v_mfma_f32_16x16x32_bf16 v[96:99], a[128:131], a[64:67], v[32:35]\n" \
"s_xor_b32 m0, s65, 0x12000\n" \
"ds_read_b128 a[160:163], v49 offset:0\n" \
"v_mfma_f32_16x16x32_bf16 v[96:99], a[132:135], a[68:71], v[96:99]\n" \
"buffer_load_dwordx4 v50, s[8:11], s64 offen offset:0 sc1 lds\n" \
"v_mfma_f32_16x16x32_bf16 v[96:99], a[136:139], a[72:75], v[96:99]\n" \
"ds_read_b128 a[164:167], v49 offset:1024\n" \
"ds_read_b128 a[168:171], v49 offset:2048\n" \
"v_mfma_f32_16x16x32_bf16 v[96:99], a[140:143], a[76:79], v[96:99]\n" \
"ds_read_b128 a[172:175], v49 offset:3072\n" \
"ds_read_b128 a[176:179], v49 offset:4096\n" \
"v_mfma_f32_16x16x32_bf16 v[100:103], a[156:159], a[76:79], v[32:35]\n" \
"ds_read_b128 a[180:183], v49 offset:5120\n" \
"ds_read_b128 a[184:187], v49 offset:6144\n" \
"v_mfma_f32_16x16x32_bf16 v[100:103], a[152:155], a[72:75], v[100:103]\n" \
"v_exp_f32_e64 v86, v86\n" \
"v_mfma_f32_16x16x32_bf16 v[100:103], a[148:151], a[68:71], v[100:103]\n" \
"v_exp_f32_e64 v87, v87\n" \
"v_mfma_f32_16x16x32_bf16 v[100:103], a[144:147], a[64:67], v[100:103]\n" \
"buffer_load_dwordx4 v50, s[8:11], s64 offen offset:1024 sc1 lds\n" \
"v_mfma_f32_16x16x32_bf16 v[108:111], a[144:147], a[80:83], v[36:39]\n" \
"ds_read_b128 a[188:191], v49 offset:7168\n" \
"v_cvt_pk_bf16_f32 v87, v86, v87\n" \
"v_mfma_f32_16x16x32_bf16 v[108:111], a[148:151], a[84:87], v[108:111]\n" \
"v_exp_f32_e64 v84, v84\n" \
"v_mfma_f32_16x16x32_bf16 v[108:111], a[152:155], a[88:91], v[108:111]\n" \
"v_exp_f32_e64 v85, v85\n" \
"v_mfma_f32_16x16x32_bf16 v[108:111], a[156:159], a[92:95], v[108:111]\n" \
"v_exp_f32_e64 v82, v82\n" \
"v_mfma_f32_16x16x32_bf16 v[104:107], a[140:143], a[92:95], v[36:39]\n" \
"v_exp_f32_e64 v83, v83\n" \
"v_mfma_f32_16x16x32_bf16 v[104:107], a[136:139], a[88:91], v[104:107]\n" \
"v_exp_f32_e64 v80, v80\n" \
"v_mfma_f32_16x16x32_bf16 v[104:107], a[132:135], a[84:87], v[104:107]\n" \
"v_exp_f32_e64 v81, v81\n" \
"v_mfma_f32_16x16x32_bf16 v[104:107], a[128:131], a[80:83], v[104:107]\n" \
"buffer_load_dwordx4 v50, s[8:11], s64 offen offset:2048 sc1 lds\n" \
"v_mfma_f32_16x16x32_bf16 v[112:115], a[128:131], a[96:99], v[40:43]\n" \
"v_exp_f32_e64 v88, v88\n" \
"v_mfma_f32_16x16x32_bf16 v[112:115], a[132:135], a[100:103], v[112:115]\n" \
"v_exp_f32_e64 v89, v89\n" \
"v_mfma_f32_16x16x32_bf16 v[112:115], a[136:139], a[104:107], v[112:115]\n" \
"v_cvt_pk_bf16_f32 v86, v84, v85\n" \
"v_cvt_pk_bf16_f32 v85, v82, v83\n" \
"v_mfma_f32_16x16x32_bf16 v[112:115], a[140:143], a[108:111], v[112:115]\n" \
"v_cvt_pk_bf16_f32 v84, v80, v81\n" \
"v_cvt_pk_bf16_f32 v88, v88, v89\n" \
"v_mfma_f32_16x16x32_bf16 v[116:119], a[156:159], a[108:111], v[40:43]\n" \
"v_exp_f32_e64 v90, v90\n" \
"v_mfma_f32_16x16x32_bf16 v[116:119], a[152:155], a[104:107], v[116:119]\n" \
"v_exp_f32_e64 v91, v91\n" \
"v_mfma_f32_16x16x32_bf16 v[116:119], a[148:151], a[100:103], v[116:119]\n" \
"buffer_load_dwordx4 v50, s[8:11], s64 offen offset:3072 sc1 lds\n" \
"v_mfma_f32_16x16x32_bf16 v[116:119], a[144:147], a[96:99], v[116:119]\n" \
"s_xor_b32 m0, s65, 0xa000\n" \
"v_cvt_pk_bf16_f32 v89, v90, v91\n" \
"v_mfma_f32_16x16x32_bf16 v[124:127], a[144:147], a[112:115], v[44:47]\n" \
"v_exp_f32_e64 v92, v92\n" \
"v_mfma_f32_16x16x32_bf16 v[124:127], a[148:151], a[116:119], v[124:127]\n" \
"v_exp_f32_e64 v93, v93\n" \
"v_mfma_f32_16x16x32_bf16 v[124:127], a[152:155], a[120:123], v[124:127]\n" \
"v_exp_f32_e64 v94, v94\n" \
"v_mfma_f32_16x16x32_bf16 v[124:127], a[156:159], a[124:127], v[124:127]\n" \
"v_exp_f32_e64 v95, v95\n" \
"v_mfma_f32_16x16x32_bf16 v[120:123], a[140:143], a[124:127], v[44:47]\n" \
"v_cvt_pk_bf16_f32 v90, v92, v93\n" \
"v_cvt_pk_bf16_f32 v91, v94, v95\n" \
"v_mfma_f32_16x16x32_bf16 v[120:123], a[136:139], a[120:123], v[120:123]\n" \
"ds_read_b128 a[144:147], v48 offset:4096\n" \
"ds_read_b128 a[148:151], v48 offset:5120\n" \
"v_mfma_f32_16x16x32_bf16 v[120:123], a[132:135], a[116:119], v[120:123]\n" \
"buffer_load_dwordx4 v51, s[8:11], s64 offen offset:0 sc1 lds\n" \
"v_mfma_f32_16x16x32_bf16 v[120:123], a[128:131], a[112:115], v[120:123]\n" \
"ds_read_b128 a[128:131], v48 offset:0\n" \
"s_waitcnt lgkmcnt(3)\n" \
"v_mfma_f32_16x16x32_bf16 v[128:131], a[160:163], v[68:71], v[128:131]\n" \
"ds_read_b128 a[132:135], v48 offset:1024\n" \
"ds_read_b128 a[136:139], v48 offset:2048\n" \
"v_mfma_f32_16x16x32_bf16 v[160:163], a[160:163], v[72:75], v[160:163]\n" \
"ds_read_b128 a[152:155], v48 offset:6144\n" \
"ds_read_b128 a[140:143], v48 offset:3072\n" \
"v_mfma_f32_16x16x32_bf16 v[132:135], a[164:167], v[68:71], v[132:135]\n" \
"v_exp_f32_e32 v102, v102\n" \
"v_mfma_f32_16x16x32_bf16 v[164:167], a[164:167], v[72:75], v[164:167]\n" \
"v_exp_f32_e64 v103, v103\n" \
"v_mfma_f32_16x16x32_bf16 v[136:139], a[168:171], v[68:71], v[136:139]\n" \
"ds_read_b128 a[156:159], v48 offset:7168\n" \
"v_cvt_pk_bf16_f32 v103, v102, v103\n" \
"v_mfma_f32_16x16x32_bf16 v[168:171], a[168:171], v[72:75], v[168:171]\n" \
"buffer_load_dwordx4 v51, s[8:11], s64 offen offset:1024 sc1 lds\n" \
"v_mfma_f32_16x16x32_bf16 v[140:143], a[172:175], v[68:71], v[140:143]\n" \
"v_exp_f32_e64 v100, v100\n" \
"v_mfma_f32_16x16x32_bf16 v[172:175], a[172:175], v[72:75], v[172:175]\n" \
"v_exp_f32_e64 v101, v101\n" \
"v_mfma_f32_16x16x32_bf16 v[144:147], a[176:179], v[68:71], v[144:147]\n" \
"v_exp_f32_e64 v98, v98\n" \
"v_mfma_f32_16x16x32_bf16 v[176:179], a[176:179], v[72:75], v[176:179]\n" \
"v_exp_f32_e64 v99, v99\n" \
"v_mfma_f32_16x16x32_bf16 v[148:151], a[180:183], v[68:71], v[148:151]\n" \
"v_exp_f32_e64 v96, v96\n" \
"v_mfma_f32_16x16x32_bf16 v[180:183], a[180:183], v[72:75], v[180:183]\n" \
"v_exp_f32_e64 v97, v97\n" \
"v_mfma_f32_16x16x32_bf16 v[152:155], a[184:187], v[68:71], v[152:155]\n" \
"v_exp_f32_e64 v104, v104\n" \
"v_mfma_f32_16x16x32_bf16 v[184:187], a[184:187], v[72:75], v[184:187]\n" \
"buffer_load_dwordx4 v51, s[8:11], s64 offen offset:2048 sc1 lds\n" \
"v_mfma_f32_16x16x32_bf16 v[156:159], a[188:191], v[68:71], v[156:159]\n" \
"v_exp_f32_e64 v105, v105\n" \
"v_mfma_f32_16x16x32_bf16 v[188:191], a[188:191], v[72:75], v[188:191]\n" \
"v_cvt_pk_bf16_f32 v102, v100, v101\n" \
"v_cvt_pk_bf16_f32 v101, v98, v99\n" \
"v_smfmac_f32_16x16x64_bf16 a[192:195], a[200:203], v[68:75], v21\n" \
"v_cvt_pk_bf16_f32 v100, v96, v97\n" \
"v_cvt_pk_bf16_f32 v104, v104, v105\n" \
"v_mfma_f32_16x16x32_bf16 v[220:223], a[188:191], v[84:87], v[220:223]\n" \
"v_exp_f32_e64 v106, v106\n" \
"v_mfma_f32_16x16x32_bf16 v[252:255], a[188:191], v[88:91], v[252:255]\n" \
"v_exp_f32_e64 v107, v107\n" \
"v_mfma_f32_16x16x32_bf16 v[216:219], a[184:187], v[84:87], v[216:219]\n" \
"v_exp_f32_e64 v108, v108\n" \
"v_mfma_f32_16x16x32_bf16 v[248:251], a[184:187], v[88:91], v[248:251]\n" \
"buffer_load_dwordx4 v51, s[8:11], s64 offen offset:3072 sc1 lds\n" \
"v_mfma_f32_16x16x32_bf16 v[212:215], a[180:183], v[84:87], v[212:215]\n" \
"v_exp_f32_e64 v109, v109\n" \
"v_mfma_f32_16x16x32_bf16 v[244:247], a[180:183], v[88:91], v[244:247]\n" \
"v_exp_f32_e64 v110, v110\n" \
"v_mfma_f32_16x16x32_bf16 v[208:211], a[176:179], v[84:87], v[208:211]\n" \
"v_exp_f32_e64 v111, v111\n" \
"v_mfma_f32_16x16x32_bf16 v[240:243], a[176:179], v[88:91], v[240:243]\n" \
"v_exp_f32_e64 v118, v118\n" \
"v_mfma_f32_16x16x32_bf16 v[204:207], a[172:175], v[84:87], v[204:207]\n" \
"v_exp_f32_e64 v119, v119\n" \
"v_mfma_f32_16x16x32_bf16 v[236:239], a[172:175], v[88:91], v[236:239]\n" \
"v_cvt_pk_bf16_f32 v105, v106, v107\n" \
"v_cvt_pk_bf16_f32 v106, v108, v109\n" \
"v_mfma_f32_16x16x32_bf16 v[200:203], a[168:171], v[84:87], v[200:203]\n" \
"v_cvt_pk_bf16_f32 v107, v110, v111\n" \
"v_cvt_pk_bf16_f32 v119, v118, v119\n" \
"v_mfma_f32_16x16x32_bf16 v[232:235], a[168:171], v[88:91], v[232:235]\n" \
"v_exp_f32_e64 v116, v116\n" \
"v_mfma_f32_16x16x32_bf16 v[196:199], a[164:167], v[84:87], v[196:199]\n" \
"v_exp_f32_e64 v117, v117\n" \
"v_mfma_f32_16x16x32_bf16 v[228:231], a[164:167], v[88:91], v[228:231]\n" \
"v_exp_f32_e64 v114, v114\n" \
"v_mfma_f32_16x16x32_bf16 v[192:195], a[160:163], v[84:87], v[192:195]\n" \
"v_exp_f32_e64 v115, v115\n" \
"v_mfma_f32_16x16x32_bf16 v[224:227], a[160:163], v[88:91], v[224:227]\n" \
"v_exp_f32_e64 v112, v112\n" \
"v_smfmac_f32_16x16x64_bf16 a[196:199], a[200:203], v[84:91], v21\n" \
"v_exp_f32_e32 v113, v113\n" \
"s_waitcnt vmcnt(8) lgkmcnt(0)\n" \
"s_barrier\n" \
"s_nop 0\n" \
"v_mfma_f32_16x16x32_bf16 v[64:67], a[128:131], a[64:67], v[32:35]\n" \
"s_xor_b32 m0, s65, 0x18000\n" \
"ds_read_b128 a[160:163], v49 offset:8192\n" \
"v_mfma_f32_16x16x32_bf16 v[64:67], a[132:135], a[68:71], v[64:67]\n" \
"buffer_load_dwordx4 v52, s[8:11], s64 offen offset:0 sc1 lds\n" \
"v_mfma_f32_16x16x32_bf16 v[64:67], a[136:139], a[72:75], v[64:67]\n" \
"ds_read_b128 a[164:167], v49 offset:9216\n" \
"ds_read_b128 a[168:171], v49 offset:10240\n" \
"v_mfma_f32_16x16x32_bf16 v[64:67], a[140:143], a[76:79], v[64:67]\n" \
"ds_read_b128 a[172:175], v49 offset:11264\n" \
"ds_read_b128 a[176:179], v49 offset:12288\n" \
"v_mfma_f32_16x16x32_bf16 v[68:71], a[156:159], a[76:79], v[32:35]\n" \
"ds_read_b128 a[180:183], v49 offset:13312\n" \
"ds_read_b128 a[184:187], v49 offset:14336\n" \
"v_mfma_f32_16x16x32_bf16 v[68:71], a[152:155], a[72:75], v[68:71]\n" \
"ds_read_b128 a[188:191], v49 offset:15360\n" \
"v_cvt_pk_bf16_f32 v118, v116, v117\n" \
"v_mfma_f32_16x16x32_bf16 v[68:71], a[148:151], a[68:71], v[68:71]\n" \
"v_cvt_pk_bf16_f32 v117, v114, v115\n" \
"v_cvt_pk_bf16_f32 v116, v112, v113\n" \
"v_mfma_f32_16x16x32_bf16 v[68:71], a[144:147], a[64:67], v[68:71]\n" \
"buffer_load_dwordx4 v52, s[8:11], s64 offen offset:1024 sc1 lds\n" \
"v_mfma_f32_16x16x32_bf16 v[76:79], a[144:147], a[80:83], v[36:39]\n" \
"v_exp_f32_e64 v120, v120\n" \
"v_mfma_f32_16x16x32_bf16 v[76:79], a[148:151], a[84:87], v[76:79]\n" \
"v_exp_f32_e64 v121, v121\n" \
"v_mfma_f32_16x16x32_bf16 v[76:79], a[152:155], a[88:91], v[76:79]\n" \
"v_exp_f32_e64 v122, v122\n" \
"v_mfma_f32_16x16x32_bf16 v[76:79], a[156:159], a[92:95], v[76:79]\n" \
"v_exp_f32_e64 v123, v123\n" \
"v_mfma_f32_16x16x32_bf16 v[72:75], a[140:143], a[92:95], v[36:39]\n" \
"v_cvt_pk_bf16_f32 v120, v120, v121\n" \
"v_cvt_pk_bf16_f32 v121, v122, v123\n" \
"v_mfma_f32_16x16x32_bf16 v[72:75], a[136:139], a[88:91], v[72:75]\n" \
"v_exp_f32_e64 v124, v124\n" \
"v_mfma_f32_16x16x32_bf16 v[72:75], a[132:135], a[84:87], v[72:75]\n" \
"v_exp_f32_e64 v125, v125\n" \
"v_mfma_f32_16x16x32_bf16 v[72:75], a[128:131], a[80:83], v[72:75]\n" \
"buffer_load_dwordx4 v52, s[8:11], s64 offen offset:2048 sc1 lds\n" \
"v_mfma_f32_16x16x32_bf16 v[80:83], a[128:131], a[96:99], v[40:43]\n" \
"v_exp_f32_e64 v126, v126\n" \
"v_mfma_f32_16x16x32_bf16 v[80:83], a[132:135], a[100:103], v[80:83]\n" \
"v_exp_f32_e64 v127, v127\n" \
"v_mfma_f32_16x16x32_bf16 v[80:83], a[136:139], a[104:107], v[80:83]\n" \
"v_cvt_pk_bf16_f32 v122, v124, v125\n" \
"v_cvt_pk_bf16_f32 v123, v126, v127\n" \
"v_mfma_f32_16x16x32_bf16 v[80:83], a[140:143], a[108:111], v[80:83]\n" \
"v_exp_f32_e64 v70, v70\n" \
"v_mfma_f32_16x16x32_bf16 v[84:87], a[156:159], a[108:111], v[40:43]\n" \
"v_exp_f32_e64 v71, v71\n" \
"v_mfma_f32_16x16x32_bf16 v[84:87], a[152:155], a[104:107], v[84:87]\n" \
"v_exp_f32_e64 v72, v72\n" \
"v_mfma_f32_16x16x32_bf16 v[84:87], a[148:151], a[100:103], v[84:87]\n" \
"buffer_load_dwordx4 v52, s[8:11], s64 offen offset:3072 sc1 lds\n" \
"v_mfma_f32_16x16x32_bf16 v[84:87], a[144:147], a[96:99], v[84:87]\n" \
"s_mov_b32 m0, s65\n" \
"v_cvt_pk_bf16_f32 v71, v70, v71\n" \
"v_mfma_f32_16x16x32_bf16 v[92:95], a[144:147], a[112:115], v[44:47]\n" \
"ds_read_b128 a[144:147], v48 offset:12288\n" \
"v_add_u32_e32 v49, 0x10000, v48\n" \
"v_mfma_f32_16x16x32_bf16 v[92:95], a[148:151], a[116:119], v[92:95]\n" \
"ds_read_b128 a[148:151], v48 offset:13312\n" \
"s_xor_b32 s65, s65, 0x8000\n" \
"v_mfma_f32_16x16x32_bf16 v[92:95], a[152:155], a[120:123], v[92:95]\n" \
"v_exp_f32_e32 v73, v73\n" \
"v_mfma_f32_16x16x32_bf16 v[92:95], a[156:159], a[124:127], v[92:95]\n" \
"ds_read_b128 a[152:155], v48 offset:14336\n" \
".fill (L_ab + 8192 - .) / 4, 4, 0xBF800000\n" \
"L_ap2:\n" \
"s_cbranch_execz L_ap0\n" \
"ds_read_b128 a[156:159], v48 offset:15360\n" \
"v_mfma_f32_16x16x32_bf16 v[88:91], a[140:143], a[124:127], v[44:47]\n" \
"ds_read_b128 a[140:143], v48 offset:11264\n" \
"v_cvt_pk_bf16_f32 v72, v72, v73\n" \
"v_mfma_f32_16x16x32_bf16 v[88:91], a[136:139], a[120:123], v[88:91]\n" \
"v_exp_f32_e32 v68, v68\n" \
"v_mfma_f32_16x16x32_bf16 v[88:91], a[132:135], a[116:119], v[88:91]\n" \
"buffer_load_dwordx4 v53, s[8:11], s64 offen offset:0 sc1 lds\n" \
"v_mfma_f32_16x16x32_bf16 v[88:91], a[128:131], a[112:115], v[88:91]\n" \
"ds_read_b128 a[128:131], v48 offset:8192\n" \
"s_waitcnt lgkmcnt(6)\n" \
"v_mfma_f32_16x16x32_bf16 v[128:131], a[160:163], v[100:103], v[128:131]\n" \
"ds_read_b128 a[132:135], v48 offset:9216\n" \
"ds_read_b128 a[136:139], v48 offset:10240\n" \
"v_mfma_f32_16x16x32_bf16 v[160:163], a[160:163], v[104:107], v[160:163]\n" \
"v_exp_f32_e32 v69, v69\n" \
"v_mfma_f32_16x16x32_bf16 v[132:135], a[164:167], v[100:103], v[132:135]\n" \
"v_exp_f32_e64 v74, v74\n" \
"v_mfma_f32_16x16x32_bf16 v[164:167], a[164:167], v[104:107], v[164:167]\n" \
"v_exp_f32_e64 v75, v75\n" \
"v_mfma_f32_16x16x32_bf16 v[136:139], a[168:171], v[100:103], v[136:139]\n" \
"v_exp_f32_e64 v66, v66\n" \
"v_mfma_f32_16x16x32_bf16 v[168:171], a[168:171], v[104:107], v[168:171]\n" \
"buffer_load_dwordx4 v53, s[8:11], s64 offen offset:1024 sc1 lds\n" \
"v_mfma_f32_16x16x32_bf16 v[140:143], a[172:175], v[100:103], v[140:143]\n" \
"v_exp_f32_e64 v67, v67\n" \
"v_mfma_f32_16x16x32_bf16 v[172:175], a[172:175], v[104:107], v[172:175]\n" \
"v_exp_f32_e64 v64, v64\n" \
"v_mfma_f32_16x16x32_bf16 v[144:147], a[176:179], v[100:103], v[144:147]\n" \
"v_exp_f32_e64 v65, v65\n" \
"v_mfma_f32_16x16x32_bf16 v[176:179], a[176:179], v[104:107], v[176:179]\n" \
"v_exp_f32_e64 v76, v76\n" \
"v_mfma_f32_16x16x32_bf16 v[148:151], a[180:183], v[100:103], v[148:151]\n" \
"v_exp_f32_e64 v77, v77\n" \
"v_mfma_f32_16x16x32_bf16 v[180:183], a[180:183], v[104:107], v[180:183]\n" \
"v_exp_f32_e64 v78, v78\n" \
"v_mfma_f32_16x16x32_bf16 v[152:155], a[184:187], v[100:103], v[152:155]\n" \
"v_exp_f32_e64 v79, v79\n" \
"v_mfma_f32_16x16x32_bf16 v[184:187], a[184:187], v[104:107], v[184:187]\n" \
"buffer_load_dwordx4 v53, s[8:11], s64 offen offset:2048 sc1 lds\n" \
"v_mfma_f32_16x16x32_bf16 v[156:159], a[188:191], v[100:103], v[156:159]\n" \
"v_cvt_pk_bf16_f32 v70, v68, v69\n" \
"v_cvt_pk_bf16_f32 v73, v74, v75\n" \
"v_mfma_f32_16x16x32_bf16 v[188:191], a[188:191], v[104:107], v[188:191]\n" \
"v_cvt_pk_bf16_f32 v69, v66, v67\n" \
"v_cvt_pk_bf16_f32 v68, v64, v65\n" \
"v_smfmac_f32_16x16x64_bf16 a[192:195], a[200:203], v[100:107], v21\n" \
"v_cvt_pk_bf16_f32 v74, v76, v77\n" \
"v_cvt_pk_bf16_f32 v75, v78, v79\n" \
"v_mfma_f32_16x16x32_bf16 v[220:223], a[188:191], v[116:119], v[220:223]\n" \
"v_xor_b32_e32 v48, 0x8000, v48\n" \
"v_mfma_f32_16x16x32_bf16 v[252:255], a[188:191], v[120:123], v[252:255]\n" \
"v_mfma_f32_16x16x32_bf16 v[216:219], a[184:187], v[116:119], v[216:219]\n" \
"v_mfma_f32_16x16x32_bf16 v[248:251], a[184:187], v[120:123], v[248:251]\n" \
"buffer_load_dwordx4 v53, s[8:11], s64 offen offset:3072 sc1 lds\n" \
"v_mfma_f32_16x16x32_bf16 v[212:215], a[180:183], v[116:119], v[212:215]\n" \
"s_add_u32 s64, s64, 0x10000\n" \
"s_sub_u32 s66, s66, 1\n" \
"s_nop 0\n" \
"v_mfma_f32_16x16x32_bf16 v[244:247], a[180:183], v[120:123], v[244:247]\n" \
"v_mfma_f32_16x16x32_bf16 v[208:211], a[176:179], v[116:119], v[208:211]\n" \
"v_mfma_f32_16x16x32_bf16 v[240:243], a[176:179], v[120:123], v[240:243]\n" \
"v_mfma_f32_16x16x32_bf16 v[204:207], a[172:175], v[116:119], v[204:207]\n" \
"v_mfma_f32_16x16x32_bf16 v[236:239], a[172:175], v[120:123], v[236:239]\n" \
"v_mfma_f32_16x16x32_bf16 v[200:203], a[168:171], v[116:119], v[200:203]\n" \
"v_mfma_f32_16x16x32_bf16 v[232:235], a[168:171], v[120:123], v[232:235]\n" \
"v_mfma_f32_16x16x32_bf16 v[196:199], a[164:167], v[116:119], v[196:199]\n" \
"v_mfma_f32_16x16x32_bf16 v[228:231], a[164:167], v[120:123], v[228:231]\n" \
"v_mfma_f32_16x16x32_bf16 v[192:195], a[160:163], v[116:119], v[192:195]\n" \
"v_mfma_f32_16x16x32_bf16 v[224:227], a[160:163], v[120:123], v[224:227]\n" \
"v_smfmac_f32_16x16x64_bf16 a[196:199], a[200:203], v[116:123], v21\n" \
"s_waitcnt vmcnt(8) lgkmcnt(0)\n" \
"s_barrier\n" \
"s_cbranch_scc0 L_2_%=\n" \
"s_nop 0\n" \
"L_la_%=:\n" \
"v_mfma_f32_16x16x32_bf16 v[96:99], a[128:131], a[64:67], v[32:35]\n" \
"v_readlane_b32 s96, v11, 1\n" \
"v_bfe_u32 v27, v20, 2, 7\n" \
"v_mfma_f32_16x16x32_bf16 v[96:99], a[132:135], a[68:71], v[96:99]\n" \
"s_xor_b32 m0, s65, 0x12000\n" \
"ds_read_b128 a[160:163], v49 offset:0\n" \
"v_mfma_f32_16x16x32_bf16 v[96:99], a[136:139], a[72:75], v[96:99]\n" \
"buffer_load_dword v26, v27, s[56:59], s96 offen sc1\n" \
"v_mfma_f32_16x16x32_bf16 v[96:99], a[140:143], a[76:79], v[96:99]\n" \
"buffer_load_dwordx4 v50, s[8:11], s64 offen offset:0 sc1 lds\n" \
"v_mfma_f32_16x16x32_bf16 v[100:103], a[156:159], a[76:79], v[32:35]\n" \
"ds_read_b128 a[164:167], v49 offset:1024\n" \
"ds_read_b128 a[168:171], v49 offset:2048\n" \
"v_mfma_f32_16x16x32_bf16 v[100:103], a[152:155], a[72:75], v[100:103]\n" \
"ds_read_b128 a[172:175], v49 offset:3072\n" \
"ds_read_b128 a[176:179], v49 offset:4096\n" \
"v_mfma_f32_16x16x32_bf16 v[100:103], a[148:151], a[68:71], v[100:103]\n" \
"ds_read_b128 a[180:183], v49 offset:5120\n" \
"ds_read_b128 a[184:187], v49 offset:6144\n" \
"v_mfma_f32_16x16x32_bf16 v[100:103], a[144:147], a[64:67], v[100:103]\n" \
"ds_read_b128 a[188:191], v49 offset:7168\n" \
"v_readlane_b32 s97, v11, 36\n" \
"v_mfma_f32_16x16x32_bf16 v[108:111], a[144:147], a[80:83], v[36:39]\n" \
"v_exp_f32_e64 v86, v86\n" \
"v_mfma_f32_16x16x32_bf16 v[108:111], a[148:151], a[84:87], v[108:111]\n" \
"v_exp_f32_e64 v87, v87\n" \
"v_mfma_f32_16x16x32_bf16 v[108:111], a[152:155], a[88:91], v[108:111]\n" \
"buffer_load_dwordx4 v50, s[8:11], s64 offen offset:1024 sc1 lds\n" \
"v_mfma_f32_16x16x32_bf16 v[108:111], a[156:159], a[92:95], v[108:111]\n" \
"v_exp_f32_e64 v84, v84\n" \
"v_mfma_f32_16x16x32_bf16 v[104:107], a[140:143], a[92:95], v[36:39]\n" \
"v_exp_f32_e64 v85, v85\n" \
"v_mfma_f32_16x16x32_bf16 v[104:107], a[136:139], a[88:91], v[104:107]\n" \
"v_cvt_pk_bf16_f32 v87, v86, v87\n" \
"v_cvt_pk_bf16_f32 v86, v84, v85\n" \
"v_mfma_f32_16x16x32_bf16 v[104:107], a[132:135], a[84:87], v[104:107]\n" \
"v_exp_f32_e64 v82, v82\n" \
"v_mfma_f32_16x16x32_bf16 v[104:107], a[128:131], a[80:83], v[104:107]\n" \
"v_exp_f32_e64 v83, v83\n" \
"v_mfma_f32_16x16x32_bf16 v[112:115], a[128:131], a[96:99], v[40:43]\n" \
"v_exp_f32_e64 v80, v80\n" \
"v_mfma_f32_16x16x32_bf16 v[112:115], a[132:135], a[100:103], v[112:115]\n" \
"v_exp_f32_e64 v81, v81\n" \
"v_mfma_f32_16x16x32_bf16 v[112:115], a[136:139], a[104:107], v[112:115]\n" \
"buffer_load_dwordx4 v50, s[8:11], s64 offen offset:2048 sc1 lds\n" \
"v_mfma_f32_16x16x32_bf16 v[112:115], a[140:143], a[108:111], v[112:115]\n" \
"v_cvt_pk_bf16_f32 v85, v82, v83\n" \
"v_cvt_pk_bf16_f32 v84, v80, v81\n" \
"v_mfma_f32_16x16x32_bf16 v[116:119], a[156:159], a[108:111], v[40:43]\n" \
"v_exp_f32_e64 v88, v88\n" \
"v_mfma_f32_16x16x32_bf16 v[116:119], a[152:155], a[104:107], v[116:119]\n" \
"v_exp_f32_e64 v89, v89\n" \
"v_mfma_f32_16x16x32_bf16 v[116:119], a[148:151], a[100:103], v[116:119]\n" \
"v_exp_f32_e64 v90, v90\n" \
"v_mfma_f32_16x16x32_bf16 v[116:119], a[144:147], a[96:99], v[116:119]\n" \
"v_exp_f32_e64 v91, v91\n" \
"v_mfma_f32_16x16x32_bf16 v[124:127], a[144:147], a[112:115], v[44:47]\n" \
"v_cvt_pk_bf16_f32 v88, v88, v89\n" \
"v_cvt_pk_bf16_f32 v89, v90, v91\n" \
"v_mfma_f32_16x16x32_bf16 v[124:127], a[148:151], a[116:119], v[124:127]\n" \
"buffer_load_dwordx4 v50, s[8:11], s64 offen offset:3072 sc1 lds\n" \
"v_mfma_f32_16x16x32_bf16 v[124:127], a[152:155], a[120:123], v[124:127]\n" \
"v_exp_f32_e64 v92, v92\n" \
"v_mfma_f32_16x16x32_bf16 v[124:127], a[156:159], a[124:127], v[124:127]\n" \
"v_exp_f32_e64 v93, v93\n" \
"v_mfma_f32_16x16x32_bf16 v[120:123], a[140:143], a[124:127], v[44:47]\n" \
"v_exp_f32_e64 v94, v94\n" \
"v_mfma_f32_16x16x32_bf16 v[120:123], a[136:139], a[120:123], v[120:123]\n" \
"v_exp_f32_e64 v95, v95\n" \
"v_mfma_f32_16x16x32_bf16 v[120:123], a[132:135], a[116:119], v[120:123]\n" \
"v_cvt_pk_bf16_f32 v90, v92, v93\n" \
"v_cvt_pk_bf16_f32 v91, v94, v95\n" \
"v_mfma_f32_16x16x32_bf16 v[120:123], a[128:131], a[112:115], v[120:123]\n" \
"ds_read_b128 a[128:131], v48 offset:0\n" \
"s_waitcnt lgkmcnt(1)\n" \
"s_nop 0\n" \
"v_mfma_f32_16x16x32_bf16 v[128:131], a[160:163], v[68:71], v[128:131]\n" \
"ds_read_b128 a[144:147], v48 offset:4096\n" \
"ds_read_b128 a[132:135], v48 offset:1024\n" \
"v_mfma_f32_16x16x32_bf16 v[160:163], a[160:163], v[72:75], v[160:163]\n" \
"ds_read_b128 a[148:151], v48 offset:5120\n" \
"ds_read_b128 a[136:139], v48 offset:2048\n" \
"v_mfma_f32_16x16x32_bf16 v[132:135], a[164:167], v[68:71], v[132:135]\n" \
"ds_read_b128 a[152:155], v48 offset:6144\n" \
"ds_read_b128 a[140:143], v48 offset:3072\n" \
"v_mfma_f32_16x16x32_bf16 v[164:167], a[164:167], v[72:75], v[164:167]\n" \
"ds_read_b128 a[156:159], v48 offset:7168\n" \
"v_add_u32_e32 v17, 0x49000, v20\n" \
"v_mfma_f32_16x16x32_bf16 v[136:139], a[168:171], v[68:71], v[136:139]\n" \
"v_readlane_b32 s98, v11, 37\n" \
"v_readlane_b32 s93, v11, 20\n" \
"v_mfma_f32_16x16x32_bf16 v[168:171], a[168:171], v[72:75], v[168:171]\n" \
"v_readlane_b32 s84, v11, 15\n" \
"v_readlane_b32 s85, v11, 16\n" \
"v_mfma_f32_16x16x32_bf16 v[140:143], a[172:175], v[68:71], v[140:143]\n" \
"v_readlane_b32 s86, v11, 17\n" \
"s_mov_b32 s87, 0x20000\n" \
"v_mfma_f32_16x16x32_bf16 v[172:175], a[172:175], v[72:75], v[172:175]\n" \
"v_readlane_b32 s92, v11, 18\n" \
"v_readlane_b32 s91, v11, 21\n" \
"v_mfma_f32_16x16x32_bf16 v[144:147], a[176:179], v[68:71], v[144:147]\n" \
"v_exp_f32_e64 v102, v102\n" \
"v_mfma_f32_16x16x32_bf16 v[176:179], a[176:179], v[72:75], v[176:179]\n" \
"v_exp_f32_e64 v103, v103\n" \
"v_mfma_f32_16x16x32_bf16 v[148:151], a[180:183], v[68:71], v[148:151]\n" \
"v_exp_f32_e64 v100, v100\n" \
"v_mfma_f32_16x16x32_bf16 v[180:183], a[180:183], v[72:75], v[180:183]\n" \
"v_exp_f32_e64 v101, v101\n" \
"v_mfma_f32_16x16x32_bf16 v[152:155], a[184:187], v[68:71], v[152:155]\n" \
"v_cvt_pk_bf16_f32 v103, v102, v103\n" \
"v_cvt_pk_bf16_f32 v102, v100, v101\n" \
"v_mfma_f32_16x16x32_bf16 v[184:187], a[184:187], v[72:75], v[184:187]\n" \
"v_exp_f32_e64 v98, v98\n" \
"v_mfma_f32_16x16x32_bf16 v[156:159], a[188:191], v[68:71], v[156:159]\n" \
"v_exp_f32_e64 v99, v99\n" \
"v_mfma_f32_16x16x32_bf16 v[188:191], a[188:191], v[72:75], v[188:191]\n" \
"s_waitcnt vmcnt(4)\n" \
"v_cmp_ne_u32_e32 vcc, s62, v26\n" \
"v_smfmac_f32_16x16x64_bf16 a[192:195], a[200:203], v[68:75], v21\n" \
"v_exp_f32_e64 v96, v96\n" \
"v_mfma_f32_16x16x32_bf16 v[220:223], a[188:191], v[84:87], v[220:223]\n" \
"v_exp_f32_e64 v97, v97\n" \
"v_mfma_f32_16x16x32_bf16 v[252:255], a[188:191], v[88:91], v[252:255]\n" \
"s_cbranch_vccnz L_sol_%=\n" \
"L_rol_%=:\n" \
"v_cvt_pk_bf16_f32 v101, v98, v99\n" \
"buffer_load_dwordx4 a[220:223], v20, s[56:59], s97 offen offset:0 sc1\n" \
"v_mfma_f32_16x16x32_bf16 v[216:219], a[184:187], v[84:87], v[216:219]\n" \
"v_cvt_pk_bf16_f32 v100, v96, v97\n" \
"s_xor_b32 s65, s65, 0x8000\n" \
"v_mfma_f32_16x16x32_bf16 v[248:251], a[184:187], v[88:91], v[248:251]\n" \
"v_exp_f32_e32 v104, v104\n" \
"v_mfma_f32_16x16x32_bf16 v[212:215], a[180:183], v[84:87], v[212:215]\n" \
"v_exp_f32_e64 v105, v105\n" \
"v_mfma_f32_16x16x32_bf16 v[244:247], a[180:183], v[88:91], v[244:247]\n" \
"v_exp_f32_e64 v106, v106\n" \
"v_mfma_f32_16x16x32_bf16 v[208:211], a[176:179], v[84:87], v[208:211]\n" \
"v_exp_f32_e64 v107, v107\n" \
"v_mfma_f32_16x16x32_bf16 v[240:243], a[176:179], v[88:91], v[240:243]\n" \
"buffer_load_dwordx4 a[228:231], v17, s[56:59], s97 offen offset:0 sc1\n" \
"v_mfma_f32_16x16x32_bf16 v[204:207], a[172:175], v[84:87], v[204:207]\n" \
"v_cvt_pk_bf16_f32 v104, v104, v105\n" \
"v_cvt_pk_bf16_f32 v105, v106, v107\n" \
"v_mfma_f32_16x16x32_bf16 v[236:239], a[172:175], v[88:91], v[236:239]\n" \
"v_exp_f32_e64 v108, v108\n" \
"v_mfma_f32_16x16x32_bf16 v[200:203], a[168:171], v[84:87], v[200:203]\n" \
"v_exp_f32_e64 v109, v109\n" \
"v_mfma_f32_16x16x32_bf16 v[232:235], a[168:171], v[88:91], v[232:235]\n" \
"v_exp_f32_e64 v110, v110\n" \
"v_mfma_f32_16x16x32_bf16 v[196:199], a[164:167], v[84:87], v[196:199]\n" \
"v_exp_f32_e64 v111, v111\n" \
"v_mfma_f32_16x16x32_bf16 v[228:231], a[164:167], v[88:91], v[228:231]\n" \
"v_cvt_pk_bf16_f32 v106, v108, v109\n" \
"v_cvt_pk_bf16_f32 v107, v110, v111\n" \
"v_mfma_f32_16x16x32_bf16 v[192:195], a[160:163], v[84:87], v[192:195]\n" \
"v_exp_f32_e64 v118, v118\n" \
"v_mfma_f32_16x16x32_bf16 v[224:227], a[160:163], v[88:91], v[224:227]\n" \
"v_exp_f32_e64 v119, v119\n" \
"v_smfmac_f32_16x16x64_bf16 a[196:199], a[200:203], v[84:91], v21\n" \
"buffer_load_dwordx4 a[236:239], v20, s[56:59], s98 offen offset:0 sc1\n" \
"s_waitcnt vmcnt(8) lgkmcnt(0)\n" \
"s_barrier\n" \
"v_mfma_f32_16x16x32_bf16 v[64:67], a[128:131], a[64:67], v[32:35]\n" \
"s_add_u32 m0, s93, 0x0\n" \
"s_nop 0\n" \
"ds_read_b128 a[160:163], v49 offset:8192\n" \
"v_mfma_f32_16x16x32_bf16 v[64:67], a[132:135], a[68:71], v[64:67]\n" \
"buffer_load_dwordx4 v0, s[84:87], s92 offen offset:0 lds\n" \
"v_mfma_f32_16x16x32_bf16 v[64:67], a[136:139], a[72:75], v[64:67]\n" \
"ds_read_b128 a[164:167], v49 offset:9216\n" \
"ds_read_b128 a[168:171], v49 offset:10240\n" \
"v_mfma_f32_16x16x32_bf16 v[64:67], a[140:143], a[76:79], v[64:67]\n" \
"ds_read_b128 a[172:175], v49 offset:11264\n" \
"ds_read_b128 a[176:179], v49 offset:12288\n" \
"v_mfma_f32_16x16x32_bf16 v[68:71], a[156:159], a[76:79], v[32:35]\n" \
"ds_read_b128 a[180:183], v49 offset:13312\n" \
"ds_read_b128 a[184:187], v49 offset:14336\n" \
"v_mfma_f32_16x16x32_bf16 v[68:71], a[152:155], a[72:75], v[68:71]\n" \
"ds_read_b128 a[188:191], v49 offset:15360\n" \
"v_cvt_pk_bf16_f32 v119, v118, v119\n" \
"v_mfma_f32_16x16x32_bf16 v[68:71], a[148:151], a[68:71], v[68:71]\n" \
"buffer_load_dwordx4 v1, s[84:87], s92 offen offset:1024 lds\n" \
"v_mfma_f32_16x16x32_bf16 v[68:71], a[144:147], a[64:67], v[68:71]\n" \
"buffer_load_dwordx4 a[244:247], v17, s[56:59], s98 offen offset:0 sc1\n" \
"v_mfma_f32_16x16x32_bf16 v[76:79], a[144:147], a[80:83], v[36:39]\n" \
"v_exp_f32_e64 v116, v116\n" \
"v_mfma_f32_16x16x32_bf16 v[76:79], a[148:151], a[84:87], v[76:79]\n" \
"v_exp_f32_e64 v117, v117\n" \
"v_mfma_f32_16x16x32_bf16 v[76:79], a[152:155], a[88:91], v[76:79]\n" \
"v_exp_f32_e64 v114, v114\n" \
"v_mfma_f32_16x16x32_bf16 v[76:79], a[156:159], a[92:95], v[76:79]\n" \
"v_exp_f32_e64 v115, v115\n" \
"v_mfma_f32_16x16x32_bf16 v[72:75], a[140:143], a[92:95], v[36:39]\n" \
"v_exp_f32_e64 v112, v112\n" \
"v_mfma_f32_16x16x32_bf16 v[72:75], a[136:139], a[88:91], v[72:75]\n" \
"buffer_load_dwordx4 v2, s[84:87], s92 offen offset:2048 lds\n" \
"v_mfma_f32_16x16x32_bf16 v[72:75], a[132:135], a[84:87], v[72:75]\n" \
"v_exp_f32_e64 v113, v113\n" \
"v_mfma_f32_16x16x32_bf16 v[72:75], a[128:131], a[80:83], v[72:75]\n" \
"v_cvt_pk_bf16_f32 v118, v116, v117\n" \
"v_cvt_pk_bf16_f32 v117, v114, v115\n" \
"v_mfma_f32_16x16x32_bf16 v[80:83], a[128:131], a[96:99], v[40:43]\n" \
"buffer_load_dwordx4 a[224:227], v20, s[56:59], s97 offen offset:1024 sc1\n" \
"v_mfma_f32_16x16x32_bf16 v[80:83], a[132:135], a[100:103], v[80:83]\n" \
"v_cvt_pk_bf16_f32 v116, v112, v113\n" \
"v_add_u32_e32 v49, 0x10000, v48\n" \
"v_mfma_f32_16x16x32_bf16 v[80:83], a[136:139], a[104:107], v[80:83]\n" \
"v_exp_f32_e64 v120, v120\n" \
"v_mfma_f32_16x16x32_bf16 v[80:83], a[140:143], a[108:111], v[80:83]\n" \
"v_exp_f32_e64 v121, v121\n" \
"v_mfma_f32_16x16x32_bf16 v[84:87], a[156:159], a[108:111], v[40:43]\n" \
"buffer_load_dwordx4 v3, s[84:87], s92 offen offset:3072 lds\n" \
"v_mfma_f32_16x16x32_bf16 v[84:87], a[152:155], a[104:107], v[84:87]\n" \
"s_add_u32 m0, s91, 0x1000\n" \
"v_cvt_pk_bf16_f32 v120, v120, v121\n" \
"v_mfma_f32_16x16x32_bf16 v[84:87], a[148:151], a[100:103], v[84:87]\n" \
"v_exp_f32_e64 v122, v122\n" \
"v_mfma_f32_16x16x32_bf16 v[84:87], a[144:147], a[96:99], v[84:87]\n" \
"v_exp_f32_e64 v123, v123\n" \
"v_mfma_f32_16x16x32_bf16 v[92:95], a[144:147], a[112:115], v[44:47]\n" \
"v_exp_f32_e64 v124, v124\n" \
"v_mfma_f32_16x16x32_bf16 v[92:95], a[148:151], a[116:119], v[92:95]\n" \
"buffer_load_dwordx4 a[232:235], v17, s[56:59], s97 offen offset:1024 sc1\n" \
"v_mfma_f32_16x16x32_bf16 v[92:95], a[152:155], a[120:123], v[92:95]\n" \
"buffer_load_dwordx4 v4, s[84:87], s92 offen offset:0 lds\n" \
"v_mfma_f32_16x16x32_bf16 v[92:95], a[156:159], a[124:127], v[92:95]\n" \
"v_exp_f32_e64 v125, v125\n" \
"v_mfma_f32_16x16x32_bf16 v[88:91], a[140:143], a[124:127], v[44:47]\n" \
"v_exp_f32_e64 v126, v126\n" \
"v_mfma_f32_16x16x32_bf16 v[88:91], a[136:139], a[120:123], v[88:91]\n" \
"v_exp_f32_e64 v127, v127\n" \
"v_mfma_f32_16x16x32_bf16 v[88:91], a[132:135], a[116:119], v[88:91]\n" \
"v_cvt_pk_bf16_f32 v121, v122, v123\n" \
"v_cvt_pk_bf16_f32 v122, v124, v125\n" \
"v_mfma_f32_16x16x32_bf16 v[88:91], a[128:131], a[112:115], v[88:91]\n" \
"v_cvt_pk_bf16_f32 v123, v126, v127\n" \
"s_waitcnt lgkmcnt(0)\n" \
"s_nop 0\n" \
"v_mfma_f32_16x16x32_bf16 v[128:131], a[160:163], v[100:103], v[128:131]\n" \
"ds_read_b128 a[128:131], v48 offset:8192\n" \
"ds_read_b128 a[144:147], v48 offset:12288\n" \
"v_mfma_f32_16x16x32_bf16 v[160:163], a[160:163], v[104:107], v[160:163]\n" \
"buffer_load_dwordx4 v5, s[84:87], s92 offen offset:1024 lds\n" \
"v_mfma_f32_16x16x32_bf16 v[132:135], a[164:167], v[100:103], v[132:135]\n" \
"buffer_load_dwordx4 a[240:243], v20, s[56:59], s98 offen offset:1024 sc1\n" \
"v_mfma_f32_16x16x32_bf16 v[164:167], a[164:167], v[104:107], v[164:167]\n" \
"ds_read_b128 a[132:135], v48 offset:9216\n" \
"ds_read_b128 a[148:151], v48 offset:13312\n" \
"v_mfma_f32_16x16x32_bf16 v[136:139], a[168:171], v[100:103], v[136:139]\n" \
"ds_read_b128 a[136:139], v48 offset:10240\n" \
"ds_read_b128 a[152:155], v48 offset:14336\n" \
"v_mfma_f32_16x16x32_bf16 v[168:171], a[168:171], v[104:107], v[168:171]\n" \
"ds_read_b128 a[140:143], v48 offset:11264\n" \
"ds_read_b128 a[156:159], v48 offset:15360\n" \
"v_mfma_f32_16x16x32_bf16 v[140:143], a[172:175], v[100:103], v[140:143]\n" \
"v_exp_f32_e64 v70, v70\n" \
"v_mfma_f32_16x16x32_bf16 v[172:175], a[172:175], v[104:107], v[172:175]\n" \
"v_exp_f32_e64 v71, v71\n" \
"v_mfma_f32_16x16x32_bf16 v[144:147], a[176:179], v[100:103], v[144:147]\n" \
"buffer_load_dwordx4 v6, s[84:87], s92 offen offset:2048 lds\n" \
"v_mfma_f32_16x16x32_bf16 v[176:179], a[176:179], v[104:107], v[176:179]\n" \
"v_exp_f32_e64 v72, v72\n" \
"v_mfma_f32_16x16x32_bf16 v[148:151], a[180:183], v[100:103], v[148:151]\n" \
"v_exp_f32_e64 v73, v73\n" \
"v_mfma_f32_16x16x32_bf16 v[180:183], a[180:183], v[104:107], v[180:183]\n" \
"buffer_load_dwordx4 a[248:251], v17, s[56:59], s98 offen offset:1024 sc1\n" \
"v_mfma_f32_16x16x32_bf16 v[152:155], a[184:187], v[100:103], v[152:155]\n" \
"v_exp_f32_e64 v68, v68\n" \
"v_mfma_f32_16x16x32_bf16 v[184:187], a[184:187], v[104:107], v[184:187]\n" \
"v_exp_f32_e64 v69, v69\n" \
"v_mfma_f32_16x16x32_bf16 v[156:159], a[188:191], v[100:103], v[156:159]\n" \
"buffer_load_dwordx4 v7, s[84:87], s92 offen offset:3072 lds\n" \
"v_mfma_f32_16x16x32_bf16 v[188:191], a[188:191], v[104:107], v[188:191]\n" \
"s_add_u32 m0, s91, 0x2000\n" \
"v_cvt_pk_bf16_f32 v71, v70, v71\n" \
"v_smfmac_f32_16x16x64_bf16 a[192:195], a[200:203], v[100:107], v21\n" \
"v_exp_f32_e64 v74, v74\n" \
"v_mfma_f32_16x16x32_bf16 v[220:223], a[188:191], v[116:119], v[220:223]\n" \
"v_exp_f32_e64 v75, v75\n" \
"v_mfma_f32_16x16x32_bf16 v[252:255], a[188:191], v[120:123], v[252:255]\n" \
"v_exp_f32_e64 v66, v66\n" \
"v_mfma_f32_16x16x32_bf16 v[216:219], a[184:187], v[116:119], v[216:219]\n" \
"v_exp_f32_e64 v67, v67\n" \
"v_mfma_f32_16x16x32_bf16 v[248:251], a[184:187], v[120:123], v[248:251]\n" \
"v_exp_f32_e64 v64, v64\n" \
"v_mfma_f32_16x16x32_bf16 v[212:215], a[180:183], v[116:119], v[212:215]\n" \
"buffer_load_dwordx4 v8, s[84:87], s92 offen offset:0 lds\n" \
"v_mfma_f32_16x16x32_bf16 v[244:247], a[180:183], v[120:123], v[244:247]\n" \
"v_exp_f32_e64 v65, v65\n" \
"v_mfma_f32_16x16x32_bf16 v[208:211], a[176:179], v[116:119], v[208:211]\n" \
"v_cvt_pk_bf16_f32 v72, v72, v73\n" \
"v_cvt_pk_bf16_f32 v70, v68, v69\n" \
"v_mfma_f32_16x16x32_bf16 v[240:243], a[176:179], v[120:123], v[240:243]\n" \
"v_exp_f32_e64 v76, v76\n" \
"v_mfma_f32_16x16x32_bf16 v[204:207], a[172:175], v[116:119], v[204:207]\n" \
"v_exp_f32_e64 v77, v77\n" \
"v_mfma_f32_16x16x32_bf16 v[236:239], a[172:175], v[120:123], v[236:239]\n" \
"v_exp_f32_e64 v78, v78\n" \
"v_mfma_f32_16x16x32_bf16 v[200:203], a[168:171], v[116:119], v[200:203]\n" \
"v_exp_f32_e64 v79, v79\n" \
"v_mfma_f32_16x16x32_bf16 v[232:235], a[168:171], v[120:123], v[232:235]\n" \
"v_cvt_pk_bf16_f32 v73, v74, v75\n" \
"v_cvt_pk_bf16_f32 v69, v66, v67\n" \
"v_mfma_f32_16x16x32_bf16 v[196:199], a[164:167], v[116:119], v[196:199]\n" \
"v_cvt_pk_bf16_f32 v68, v64, v65\n" \
"v_cvt_pk_bf16_f32 v74, v76, v77\n" \
"v_mfma_f32_16x16x32_bf16 v[228:231], a[164:167], v[120:123], v[228:231]\n" \
"v_cvt_pk_bf16_f32 v75, v78, v79\n" \
"v_xor_b32_e32 v48, 0x8000, v48\n" \
"v_mfma_f32_16x16x32_bf16 v[192:195], a[160:163], v[116:119], v[192:195]\n" \
"s_add_u32 s64, s64, 0x10000\n" \
"v_mfma_f32_16x16x32_bf16 v[224:227], a[160:163], v[120:123], v[224:227]\n" \
"v_smfmac_f32_16x16x64_bf16 a[196:199], a[200:203], v[116:123], v21\n" \
"s_waitcnt vmcnt(14) lgkmcnt(0)\n" \
"s_barrier\n" \
"s_cmp_lg_u32 s16, 0\n" \
"s_cbranch_scc1 L_f1_%=\n" \
"ds_read_b128 a[160:163], v49 offset:0\n" \
"ds_read_b128 a[164:167], v49 offset:1024\n" \
"ds_read_b128 a[168:171], v49 offset:2048\n" \
"ds_read_b128 a[172:175], v49 offset:3072\n" \
"ds_read_b128 a[176:179], v49 offset:4096\n" \
"ds_read_b128 a[180:183], v49 offset:5120\n" \
"ds_read_b128 a[184:187], v49 offset:6144\n" \
"ds_read_b128 a[188:191], v49 offset:7168\n" \
"v_add_u32_e32 v54, 0x49000, v20\n" \
"buffer_load_dwordx4 a[96:99], v20, s[56:59], s60 offen offset:0 sc1\n" \
"v_add_u32_e32 v50, s15, v20\n" \
"v_add_u32_e32 v51, 0x10000, v50\n" \
"ds_read_b32 v60, v18 offset:0\n" \
"ds_read_b32 v61, v18 offset:128\n" \
"ds_read_b128 a[0:3], v49 offset:8192\n" \
"ds_read_b128 a[4:7], v49 offset:9216\n" \
"ds_read_b128 a[8:11], v49 offset:10240\n" \
"ds_read_b128 a[12:15], v49 offset:11264\n" \
"ds_read_b128 a[16:19], v49 offset:12288\n" \
"ds_read_b128 a[20:23], v49 offset:13312\n" \
"ds_read_b128 a[24:27], v49 offset:14336\n" \
"ds_read_b128 a[28:31], v49 offset:15360\n" \
"v_exp_f32_e32 v86, v86\n" \
"v_exp_f32_e32 v87, v87\n" \
"v_exp_f32_e32 v84, v84\n" \
"v_exp_f32_e32 v85, v85\n" \
"v_cvt_pk_bf16_f32 v87, v86, v87\n" \
"buffer_load_dwordx4 a[100:103], v20, s[56:59], s60 offen offset:1024 sc1\n" \
"v_exp_f32_e32 v82, v82\n" \
"v_exp_f32_e32 v83, v83\n" \
"v_exp_f32_e32 v80, v80\n" \
"v_exp_f32_e32 v81, v81\n" \
"s_waitcnt lgkmcnt(15)\n" \
"v_mfma_f32_16x16x32_bf16 v[128:131], a[160:163], v[68:71], v[128:131]\n" \
"v_cvt_pk_bf16_f32 v86, v84, v85\n" \
"v_cvt_pk_bf16_f32 v85, v82, v83\n" \
"v_mfma_f32_16x16x32_bf16 v[160:163], a[160:163], v[72:75], v[160:163]\n" \
"v_cvt_pk_bf16_f32 v84, v80, v81\n" \
"v_and_b32_e32 v84, v12, v84\n" \
"v_mfma_f32_16x16x32_bf16 v[132:135], a[164:167], v[68:71], v[132:135]\n" \
"v_and_b32_e32 v85, v13, v85\n" \
"v_and_b32_e32 v86, v14, v86\n" \
"v_mfma_f32_16x16x32_bf16 v[164:167], a[164:167], v[72:75], v[164:167]\n" \
"buffer_load_dwordx4 a[104:107], v20, s[56:59], s60 offen offset:2048 sc1\n" \
"v_mfma_f32_16x16x32_bf16 v[136:139], a[168:171], v[68:71], v[136:139]\n" \
"v_cvt_pk_bf16_f32 v128, v128, v129\n" \
"v_cvt_pk_bf16_f32 v160, v160, v161\n" \
"v_mfma_f32_16x16x32_bf16 v[168:171], a[168:171], v[72:75], v[168:171]\n" \
"v_cvt_pk_bf16_f32 v129, v130, v131\n" \
"s_waitcnt lgkmcnt(10)\n" \
"v_mfma_f32_16x16x32_bf16 v[140:143], a[172:175], v[68:71], v[140:143]\n" \
"v_cvt_pk_bf16_f32 v161, v162, v163\n" \
"v_cvt_pk_bf16_f32 v130, v132, v133\n" \
"v_mfma_f32_16x16x32_bf16 v[172:175], a[172:175], v[72:75], v[172:175]\n" \
"v_cvt_pk_bf16_f32 v131, v134, v135\n" \
"v_cvt_pk_bf16_f32 v162, v164, v165\n" \
"v_mfma_f32_16x16x32_bf16 v[144:147], a[176:179], v[68:71], v[144:147]\n" \
"v_cvt_pk_bf16_f32 v163, v166, v167\n" \
"v_cvt_pk_bf16_f32 v132, v136, v137\n" \
"v_mfma_f32_16x16x32_bf16 v[176:179], a[176:179], v[72:75], v[176:179]\n" \
"buffer_load_dwordx4 a[108:111], v20, s[56:59], s60 offen offset:3072 sc1\n" \
"v_mfma_f32_16x16x32_bf16 v[148:151], a[180:183], v[68:71], v[148:151]\n" \
"v_cvt_pk_bf16_f32 v133, v138, v139\n" \
"v_cvt_pk_bf16_f32 v134, v140, v141\n" \
"v_mfma_f32_16x16x32_bf16 v[180:183], a[180:183], v[72:75], v[180:183]\n" \
"v_cvt_pk_bf16_f32 v135, v142, v143\n" \
"v_cvt_pk_bf16_f32 v164, v168, v169\n" \
"v_mfma_f32_16x16x32_bf16 v[152:155], a[184:187], v[68:71], v[152:155]\n" \
"v_cvt_pk_bf16_f32 v165, v170, v171\n" \
"v_cvt_pk_bf16_f32 v166, v172, v173\n" \
"v_mfma_f32_16x16x32_bf16 v[184:187], a[184:187], v[72:75], v[184:187]\n" \
"v_cvt_pk_bf16_f32 v167, v174, v175\n" \
"v_cvt_pk_bf16_f32 v136, v144, v145\n" \
"v_mfma_f32_16x16x32_bf16 v[156:159], a[188:191], v[68:71], v[156:159]\n" \
"v_cvt_pk_bf16_f32 v137, v146, v147\n" \
"v_cvt_pk_bf16_f32 v138, v148, v149\n" \
"v_mfma_f32_16x16x32_bf16 v[188:191], a[188:191], v[72:75], v[188:191]\n" \
"v_cvt_pk_bf16_f32 v139, v150, v151\n" \
"v_cvt_pk_bf16_f32 v140, v152, v153\n" \
"v_smfmac_f32_16x16x64_bf16 a[192:195], a[200:203], v[68:75], v21\n" \
"v_cvt_pk_bf16_f32 v141, v154, v155\n" \
"v_cvt_pk_bf16_f32 v168, v176, v177\n" \
"v_mfma_f32_16x16x32_bf16 v[120:123], a[128:131], a[112:115], v[44:47]\n" \
"v_cvt_pk_bf16_f32 v142, v156, v157\n" \
"v_cvt_pk_bf16_f32 v143, v158, v159\n" \
"v_mfma_f32_16x16x32_bf16 v[120:123], a[132:135], a[116:119], v[120:123]\n" \
"v_cvt_pk_bf16_f32 v169, v178, v179\n" \
"v_cvt_pk_bf16_f32 v170, v180, v181\n" \
"v_cvt_pk_bf16_f32 v171, v182, v183\n" \
"v_cvt_pk_bf16_f32 v172, v184, v185\n" \
"v_cvt_pk_bf16_f32 v173, v186, v187\n" \
"v_cvt_pk_bf16_f32 v174, v188, v189\n" \
"v_cvt_pk_bf16_f32 v175, v190, v191\n" \
"ds_write_b128 v50, v[128:131] offset:0\n" \
"v_mfma_f32_16x16x32_bf16 v[120:123], a[136:139], a[120:123], v[120:123]\n" \
"v_and_b32_e32 v87, v15, v87\n" \
"v_accvgpr_read_b32 v40, a192\n" \
"v_mfma_f32_16x16x32_bf16 v[120:123], a[140:143], a[124:127], v[120:123]\n" \
"ds_write_b128 v50, v[132:135] offset:1024\n" \
"v_mfma_f32_16x16x32_bf16 v[124:127], a[156:159], a[124:127], v[44:47]\n" \
"buffer_load_dwordx4 a[124:127], v54, s[56:59], s60 offen offset:3072 sc1\n" \
"v_mfma_f32_16x16x32_bf16 v[124:127], a[152:155], a[120:123], v[124:127]\n" \
"ds_write_b128 v50, v[136:139] offset:2048\n" \
"v_mfma_f32_16x16x32_bf16 v[124:127], a[148:151], a[116:119], v[124:127]\n" \
"v_accvgpr_read_b32 v22, a193\n" \
"v_accvgpr_read_b32 v41, a194\n" \
"v_mfma_f32_16x16x32_bf16 v[124:127], a[144:147], a[112:115], v[124:127]\n" \
"ds_write_b128 v50, v[140:143] offset:3072\n" \
"v_mfma_f32_16x16x32_bf16 a[196:199], a[204:207], v[84:87], a[196:199]\n" \
"v_accvgpr_read_b32 v23, a195\n" \
"v_add_f32_e32 v40, v40, v22\n" \
"v_mfma_f32_16x16x32_bf16 v[192:195], a[160:163], v[84:87], v[192:195]\n" \
"ds_write_b128 v51, v[160:163] offset:32768\n" \
"v_mfma_f32_16x16x32_bf16 v[196:199], a[164:167], v[84:87], v[196:199]\n" \
"buffer_load_dwordx4 a[112:115], v54, s[56:59], s60 offen offset:0 sc1\n" \
"v_mfma_f32_16x16x32_bf16 v[200:203], a[168:171], v[84:87], v[200:203]\n" \
"ds_write_b128 v51, v[164:167] offset:33792\n" \
"v_add_f32_e32 v41, v41, v23\n" \
"ds_write_b64 v19, v[40:41]\n" \
"v_mfma_f32_16x16x32_bf16 v[204:207], a[172:175], v[84:87], v[204:207]\n" \
"v_lshl_add_u32 v52, s15, 2, v20\n" \
"v_add_u32_e32 v52, 0x1a000, v52\n" \
"ds_write_b128 v51, v[168:171] offset:34816\n" \
"v_mfma_f32_16x16x32_bf16 v[208:211], a[176:179], v[84:87], v[208:211]\n" \
"v_exp_f32_e32 v88, v88\n" \
"v_mfma_f32_16x16x32_bf16 v[212:215], a[180:183], v[84:87], v[212:215]\n" \
"ds_write_b128 v51, v[172:175] offset:35840\n" \
"v_mfma_f32_16x16x32_bf16 v[216:219], a[184:187], v[84:87], v[216:219]\n" \
"buffer_load_dwordx4 a[116:119], v54, s[56:59], s60 offen offset:1024 sc1\n" \
"v_mfma_f32_16x16x32_bf16 v[220:223], a[188:191], v[84:87], v[220:223]\n" \
"v_exp_f32_e32 v89, v89\n" \
"v_exp_f32_e32 v90, v90\n" \
"v_exp_f32_e32 v91, v91\n" \
"v_cvt_pk_bf16_f32 v88, v88, v89\n" \
"v_exp_f32_e32 v92, v92\n" \
"v_exp_f32_e32 v93, v93\n" \
"v_exp_f32_e32 v94, v94\n" \
"v_exp_f32_e32 v95, v95\n" \
"v_cvt_pk_bf16_f32 v89, v90, v91\n" \
"v_cvt_pk_bf16_f32 v90, v92, v93\n" \
"v_cvt_pk_bf16_f32 v91, v94, v95\n" \
"v_xor_b32_e32 v63, 0x400, v19\n" \
"buffer_load_dwordx4 a[120:123], v54, s[56:59], s60 offen offset:2048 sc1\n" \
"v_exp_f32_e32 v120, v120\n" \
"v_exp_f32_e32 v121, v121\n" \
"v_exp_f32_e32 v122, v122\n" \
"v_exp_f32_e32 v123, v123\n" \
".fill (L_ab + 12288 - .) / 4, 4, 0xBF800000\n" \
"L_ap3:\n" \
"s_cbranch_execz L_ap0\n" \
"v_cvt_pk_bf16_f32 v120, v120, v121\n" \
"v_exp_f32_e32 v124, v124\n" \
"v_exp_f32_e32 v125, v125\n" \
"s_waitcnt lgkmcnt(0)\n" \
"s_barrier\n" \
"ds_read_b128 v[144:147], v52 offset:0\n" \
"ds_read_b128 v[148:151], v52 offset:1024\n" \
"ds_read_b128 v[152:155], v52 offset:2048\n" \
"ds_read_b128 v[156:159], v52 offset:3072\n" \
"ds_read_b128 v[176:179], v52 offset:4096\n" \
"ds_read_b128 v[180:183], v52 offset:5120\n" \
"ds_read_b128 v[184:187], v52 offset:6144\n" \
"ds_read_b128 v[188:191], v52 offset:7168\n" \
"v_mfma_f32_16x16x32_bf16 a[196:199], a[212:215], v[88:91], a[196:199]\n" \
"ds_read_b64 v[42:43], v63\n" \
"v_cvt_pk_bf16_f32 v121, v122, v123\n" \
"v_mfma_f32_16x16x32_bf16 v[224:227], a[160:163], v[88:91], v[224:227]\n" \
"v_exp_f32_e32 v126, v126\n" \
"v_mfma_f32_16x16x32_bf16 v[228:231], a[164:167], v[88:91], v[228:231]\n" \
"v_exp_f32_e32 v127, v127\n" \
"v_mfma_f32_16x16x32_bf16 v[232:235], a[168:171], v[88:91], v[232:235]\n" \
"v_cvt_pk_bf16_f32 v122, v124, v125\n" \
"v_cvt_pk_bf16_f32 v123, v126, v127\n" \
"v_mfma_f32_16x16x32_bf16 v[236:239], a[172:175], v[88:91], v[236:239]\n" \
"v_and_b32_e32 v120, v12, v120\n" \
"v_and_b32_e32 v121, v13, v121\n" \
"v_mfma_f32_16x16x32_bf16 v[240:243], a[176:179], v[88:91], v[240:243]\n" \
"v_and_b32_e32 v122, v14, v122\n" \
"v_and_b32_e32 v123, v15, v123\n" \
"v_mfma_f32_16x16x32_bf16 v[244:247], a[180:183], v[88:91], v[244:247]\n" \
"v_readlane_b32 s93, v11, 22\n" \
"v_readlane_b32 s92, v11, 27\n" \
"v_mfma_f32_16x16x32_bf16 v[248:251], a[184:187], v[88:91], v[248:251]\n" \
"s_add_u32 m0, s93, 0x0\n" \
"v_mov_b32_e32 v130, v28\n" \
"v_mfma_f32_16x16x32_bf16 v[252:255], a[188:191], v[88:91], v[252:255]\n" \
"v_mov_b32_e32 v131, v29\n" \
"v_mov_b32_e32 v132, 0\n" \
"v_mov_b32_e32 v133, 0\n" \
"v_mov_b32_e32 v128, 0\n" \
"v_mov_b32_e32 v129, 0\n" \
"s_waitcnt vmcnt(8) lgkmcnt(1)\n" \
"s_barrier\n" \
"v_mfma_f32_16x16x32_bf16 a[196:199], a[212:215], v[120:123], a[196:199]\n" \
"buffer_load_dwordx4 v0, s[80:83], s92 offen offset:0 lds\n" \
"v_mfma_f32_16x16x32_bf16 v[224:227], a[0:3], v[120:123], v[224:227]\n" \
"v_mfma_f32_16x16x32_bf16 v[228:231], a[4:7], v[120:123], v[228:231]\n" \
"v_mfma_f32_16x16x32_bf16 v[232:235], a[8:11], v[120:123], v[232:235]\n" \
"v_mfma_f32_16x16x32_bf16 v[236:239], a[12:15], v[120:123], v[236:239]\n" \
"v_mfma_f32_16x16x32_bf16 v[240:243], a[16:19], v[120:123], v[240:243]\n" \
"v_mfma_f32_16x16x32_bf16 v[244:247], a[20:23], v[120:123], v[244:247]\n" \
"buffer_load_dwordx4 v1, s[80:83], s92 offen offset:1024 lds\n" \
"v_mfma_f32_16x16x32_bf16 v[248:251], a[24:27], v[120:123], v[248:251]\n" \
"v_accvgpr_read_b32 v56, a196\n" \
"v_accvgpr_read_b32 v24, a197\n" \
"v_mfma_f32_16x16x32_bf16 v[252:255], a[28:31], v[120:123], v[252:255]\n" \
"v_add_f32_e32 v56, v56, v24\n" \
"s_waitcnt lgkmcnt(0)\n" \
"v_add_f32_e32 v56, v56, v42\n" \
"v_mfma_f32_16x16x32_bf16 v[192:195], v[130:133], v[144:147], v[192:195]\n" \
"v_max_f32_e32 v62, v62, v56\n" \
"v_accvgpr_read_b32 v57, a198\n" \
"v_mfma_f32_16x16x32_bf16 v[196:199], v[128:131], v[144:147], v[196:199]\n" \
"v_rcp_f32_e32 v56, v56\n" \
"v_mfma_f32_16x16x32_bf16 v[200:203], v[130:133], v[148:151], v[200:203]\n" \
"v_mul_f32_e32 v56, v56, v60\n" \
"v_accvgpr_read_b32 v25, a199\n" \
"buffer_load_dwordx4 v2, s[80:83], s92 offen offset:2048 lds\n" \
"v_mfma_f32_16x16x32_bf16 v[204:207], v[128:131], v[148:151], v[204:207]\n" \
"v_mul_f32_e32 v192, v56, v192\n" \
"v_mul_f32_e32 v193, v56, v193\n" \
"v_mfma_f32_16x16x32_bf16 v[208:211], v[130:133], v[152:155], v[208:211]\n" \
"v_mul_f32_e32 v194, v56, v194\n" \
"v_mul_f32_e32 v195, v56, v195\n" \
"v_mfma_f32_16x16x32_bf16 v[212:215], v[128:131], v[152:155], v[212:215]\n" \
"v_mul_f32_e32 v196, v56, v196\n" \
"v_mul_f32_e32 v197, v56, v197\n" \
"v_mfma_f32_16x16x32_bf16 v[216:219], v[130:133], v[156:159], v[216:219]\n" \
"v_mul_f32_e32 v198, v56, v198\n" \
"v_mul_f32_e32 v199, v56, v199\n" \
"v_mfma_f32_16x16x32_bf16 v[220:223], v[128:131], v[156:159], v[220:223]\n" \
"v_cvt_pk_bf16_f32 v64, v192, v193\n" \
"v_cvt_pk_bf16_f32 v65, v194, v195\n" \
"v_mfma_f32_16x16x32_bf16 v[224:227], v[130:133], v[176:179], v[224:227]\n" \
"buffer_load_dwordx4 v3, s[80:83], s92 offen offset:3072 lds\n" \
"s_add_u32 m0, s93, 0x1000\n" \
"v_mfma_f32_16x16x32_bf16 v[228:231], v[128:131], v[176:179], v[228:231]\n" \
"v_cvt_pk_bf16_f32 v66, v196, v197\n" \
"v_cvt_pk_bf16_f32 v67, v198, v199\n" \
"v_mfma_f32_16x16x32_bf16 v[232:235], v[130:133], v[180:183], v[232:235]\n" \
"v_mul_f32_e32 v200, v56, v200\n" \
"v_mul_f32_e32 v201, v56, v201\n" \
"v_mfma_f32_16x16x32_bf16 v[236:239], v[128:131], v[180:183], v[236:239]\n" \
"v_mfma_f32_16x16x32_bf16 a[208:211], a[216:219], v[64:67], a[208:211]\n" \
"v_mul_f32_e32 v202, v56, v202\n" \
"v_mul_f32_e32 v203, v56, v203\n" \
"v_mfma_f32_16x16x32_bf16 v[240:243], v[130:133], v[184:187], v[240:243]\n" \
"v_mul_f32_e32 v204, v56, v204\n" \
"v_mul_f32_e32 v205, v56, v205\n" \
"buffer_load_dwordx4 v4, s[80:83], s92 offen offset:0 lds\n" \
"v_mul_f32_e32 v206, v56, v206\n" \
"v_mul_f32_e32 v207, v56, v207\n" \
"v_add_f32_e32 v57, v57, v25\n" \
"v_cvt_pk_bf16_f32 v68, v200, v201\n" \
"v_cvt_pk_bf16_f32 v69, v202, v203\n" \
"v_cvt_pk_bf16_f32 v70, v204, v205\n" \
"v_cvt_pk_bf16_f32 v71, v206, v207\n" \
"v_add_f32_e32 v57, v57, v43\n" \
"v_mul_f32_e32 v208, v56, v208\n" \
"v_mfma_f32_16x16x32_bf16 a[208:211], a[216:219], v[68:71], a[208:211]\n" \
"v_mul_f32_e32 v209, v56, v209\n" \
"v_mul_f32_e32 v210, v56, v210\n" \
"v_mul_f32_e32 v211, v56, v211\n" \
"v_mul_f32_e32 v212, v56, v212\n" \
"v_mul_f32_e32 v213, v56, v213\n" \
"v_mul_f32_e32 v214, v56, v214\n" \
"v_mul_f32_e32 v215, v56, v215\n" \
"v_max_f32_e32 v62, v62, v57\n" \
"v_cvt_pk_bf16_f32 v72, v208, v209\n" \
"v_cvt_pk_bf16_f32 v73, v210, v211\n" \
"buffer_load_dwordx4 v5, s[80:83], s92 offen offset:1024 lds\n" \
"v_cvt_pk_bf16_f32 v74, v212, v213\n" \
"v_cvt_pk_bf16_f32 v75, v214, v215\n" \
"v_rcp_f32_e32 v57, v57\n" \
"v_mfma_f32_16x16x32_bf16 v[244:247], v[128:131], v[184:187], v[244:247]\n" \
"v_mfma_f32_16x16x32_bf16 a[208:211], a[216:219], v[72:75], a[208:211]\n" \
"v_mul_f32_e32 v216, v56, v216\n" \
"v_mul_f32_e32 v217, v56, v217\n" \
"v_mul_f32_e32 v218, v56, v218\n" \
"v_mul_f32_e32 v219, v56, v219\n" \
"v_mul_f32_e32 v220, v56, v220\n" \
"v_mul_f32_e32 v221, v56, v221\n" \
"v_mul_f32_e32 v222, v56, v222\n" \
"v_mul_f32_e32 v223, v56, v223\n" \
"v_cvt_pk_bf16_f32 v76, v216, v217\n" \
"v_cvt_pk_bf16_f32 v77, v218, v219\n" \
"v_cvt_pk_bf16_f32 v78, v220, v221\n" \
"buffer_load_dwordx4 v6, s[80:83], s92 offen offset:2048 lds\n" \
"v_cvt_pk_bf16_f32 v79, v222, v223\n" \
"v_mul_f32_e32 v57, v57, v61\n" \
"v_mul_f32_e32 v224, v57, v224\n" \
"v_mfma_f32_16x16x32_bf16 a[208:211], a[216:219], v[76:79], a[208:211]\n" \
"v_mul_f32_e32 v225, v57, v225\n" \
"v_mul_f32_e32 v226, v57, v226\n" \
"v_mul_f32_e32 v227, v57, v227\n" \
"v_mul_f32_e32 v228, v57, v228\n" \
"v_mul_f32_e32 v229, v57, v229\n" \
"v_mul_f32_e32 v230, v57, v230\n" \
"v_mul_f32_e32 v231, v57, v231\n" \
"v_cvt_pk_bf16_f32 v80, v224, v225\n" \
"v_cvt_pk_bf16_f32 v81, v226, v227\n" \
"v_cvt_pk_bf16_f32 v82, v228, v229\n" \
"v_cvt_pk_bf16_f32 v83, v230, v231\n" \
"v_mfma_f32_16x16x32_bf16 v[248:251], v[130:133], v[188:191], v[248:251]\n" \
"v_mul_f32_e32 v232, v57, v232\n" \
"v_mul_f32_e32 v233, v57, v233\n" \
"v_mfma_f32_16x16x32_bf16 a[208:211], a[216:219], v[80:83], a[208:211]\n" \
"buffer_load_dwordx4 v7, s[80:83], s92 offen offset:3072 lds\n" \
"s_add_u32 m0, s93, 0x2000\n" \
"v_mul_f32_e32 v234, v57, v234\n" \
"v_mul_f32_e32 v235, v57, v235\n" \
"v_mul_f32_e32 v236, v57, v236\n" \
"v_mul_f32_e32 v237, v57, v237\n" \
"v_mul_f32_e32 v238, v57, v238\n" \
"v_mul_f32_e32 v239, v57, v239\n" \
"v_cvt_pk_bf16_f32 v84, v232, v233\n" \
"v_cvt_pk_bf16_f32 v85, v234, v235\n" \
"v_cvt_pk_bf16_f32 v86, v236, v237\n" \
"v_cvt_pk_bf16_f32 v87, v238, v239\n" \
"v_mul_f32_e32 v240, v57, v240\n" \
"v_mul_f32_e32 v241, v57, v241\n" \
"v_mfma_f32_16x16x32_bf16 a[208:211], a[216:219], v[84:87], a[208:211]\n" \
"v_mul_f32_e32 v242, v57, v242\n" \
"v_mul_f32_e32 v243, v57, v243\n" \
"v_mul_f32_e32 v244, v57, v244\n" \
"v_mul_f32_e32 v245, v57, v245\n" \
"v_mul_f32_e32 v246, v57, v246\n" \
"v_mul_f32_e32 v247, v57, v247\n" \
"buffer_load_dwordx4 v8, s[80:83], s92 offen offset:0 lds\n" \
"v_cvt_pk_bf16_f32 v96, v240, v241\n" \
"v_cvt_pk_bf16_f32 v97, v242, v243\n" \
"v_cvt_pk_bf16_f32 v98, v244, v245\n" \
"v_cvt_pk_bf16_f32 v99, v246, v247\n" \
"v_mfma_f32_16x16x32_bf16 v[252:255], v[128:131], v[188:191], v[252:255]\n" \
"v_mul_f32_e32 v248, v57, v248\n" \
"v_mfma_f32_16x16x32_bf16 a[208:211], a[216:219], v[96:99], a[208:211]\n" \
"v_mul_f32_e32 v249, v57, v249\n" \
"v_mul_f32_e32 v250, v57, v250\n" \
"v_mul_f32_e32 v251, v57, v251\n" \
"v_cvt_pk_bf16_f32 v100, v248, v249\n" \
"v_cvt_pk_bf16_f32 v101, v250, v251\n" \
"v_cmp_lt_f32_e64 s[72:73], s21, v62\n" \
"v_mul_f32_e32 v252, v57, v252\n" \
"v_mul_f32_e32 v253, v57, v253\n" \
"v_mul_f32_e32 v254, v57, v254\n" \
"v_mul_f32_e32 v255, v57, v255\n" \
"v_cvt_pk_bf16_f32 v102, v252, v253\n" \
"v_cvt_pk_bf16_f32 v103, v254, v255\n" \
"s_nop 1\n" \
"v_mfma_f32_16x16x32_bf16 a[208:211], a[216:219], v[100:103], a[208:211]\n" \
"s_nop 7\n" \
"v_accvgpr_read_b32 v63, a208\n" \
"v_cmp_u_f32_e32 vcc, v63, v63\n" \
"s_nop 0\n" \
"s_or_b64 vcc, s[72:73], vcc\n" \
"s_cbranch_scc0 L_out_%=\n" \
"s_branch L_end_%=\n" \
"L_skb_%=:\n" \
"s_sleep 1\n" \
"buffer_load_dword v57, v61, s[56:59], s96 offen sc1\n" \
"s_add_u32 s101, s101, 1\n" \
"s_waitcnt vmcnt(0)\n" \
"v_cmp_ne_u32_e32 vcc, s62, v57\n" \
"s_cmp_gt_u32 s101, 0x100000\n" \
"s_cbranch_scc1 L_rkb_%=\n" \
"s_nop 2\n" \
"s_cbranch_vccnz L_skb_%=\n" \
"s_branch L_rkb_%=\n" \
"L_sol_%=:\n" \
"s_sleep 1\n" \
"buffer_load_dword v26, v27, s[56:59], s96 offen sc1\n" \
"s_add_u32 s101, s101, 1\n" \
"s_waitcnt vmcnt(0)\n" \
"v_cmp_ne_u32_e32 vcc, s62, v26\n" \
"s_cmp_gt_u32 s101, 0x100000\n" \
"s_cbranch_scc1 L_rol_%=\n" \
"s_nop 2\n" \
"s_cbranch_vccnz L_sol_%=\n" \
"s_branch L_rol_%=\n" \
"L_sos_%=:\n" \
"s_sleep 1\n" \
"buffer_load_dword v26, v27, s[56:59], s96 offen sc1\n" \
"s_add_u32 s101, s101, 1\n" \
"s_waitcnt vmcnt(0)\n" \
"v_cmp_ne_u32_e32 vcc, s62, v26\n" \
"s_cmp_gt_u32 s101, 0x100000\n" \
"s_cbranch_scc1 L_ros_%=\n" \
"s_nop 2\n" \
"s_cbranch_vccnz L_sos_%=\n" \
"s_branch L_ros_%=\n" \
"L_sa_%=:\n" \
"s_sleep 1\n" \
"buffer_load_dword v26, v38, s[56:59], s64 offen sc1\n" \
"s_add_u32 s101, s101, 1\n" \
"s_waitcnt vmcnt(0)\n" \
"v_cmp_ne_u32_e32 vcc, s62, v26\n" \
"s_cmp_gt_u32 s101, 0x100000\n" \
"s_cbranch_scc1 L_ra_%=\n" \
"s_nop 2\n" \
"s_cbranch_vccnz L_sa_%=\n" \
"s_branch L_ra_%=\n" \
"L_sb_%=:\n" \
"s_sleep 1\n" \
"buffer_load_dword v47, v38, s[56:59], s71 offen sc1\n" \
"s_add_u32 s101, s101, 1\n" \
"s_waitcnt vmcnt(0)\n" \
"v_cmp_ne_u32_e32 vcc, s62, v47\n" \
"s_cmp_gt_u32 s101, 0x100000\n" \
"s_cbranch_scc1 L_rb_%=\n" \
"s_nop 2\n" \
"s_cbranch_vccnz L_sb_%=\n" \
"s_branch L_rb_%=\n" \
"L_sna_%=:\n" \
"buffer_load_dword v37, v40, s[56:59], s74 offen sc1\n" \
"s_add_u32 s101, s101, 1\n" \
"s_waitcnt vmcnt(0)\n" \
"v_cmp_eq_u32_e64 s[98:99], s62, v37\n" \
"s_cmp_gt_u32 s101, 0x100000\n" \
"s_cbranch_scc1 L_rna_%=\n" \
"s_andn2_b32 s75, s20, s98\n" \
"s_cbranch_scc0 L_gna_%=\n" \
"s_sleep 1\n" \
"s_branch L_sna_%=\n" \
"L_gna_%=:\n" \
"s_bitcmp1_b32 s20, 0\n" \
"s_cbranch_scc0 L_kna0_%=\n" \
"buffer_load_dwordx4 v[116:119], v20, s[56:59], s12 offen offset:0 sc1\n" \
"L_kna0_%=:\n" \
"s_bitcmp1_b32 s20, 1\n" \
"s_cbranch_scc0 L_kna1_%=\n" \
"buffer_load_dwordx4 v[124:127], v20, s[56:59], s12 offen offset:2048 sc1\n" \
"L_kna1_%=:\n" \
"s_bitcmp1_b32 s20, 2\n" \
"s_cbranch_scc0 L_kna2_%=\n" \
"buffer_load_dwordx4 v[132:135], v20, s[56:59], s13 offen offset:0 sc1\n" \
"L_kna2_%=:\n" \
"s_bitcmp1_b32 s20, 3\n" \
"s_cbranch_scc0 L_kna3_%=\n" \
"buffer_load_dwordx4 v[140:143], v20, s[56:59], s13 offen offset:2048 sc1\n" \
"L_kna3_%=:\n" \
"s_bitcmp1_b32 s20, 4\n" \
"s_cbranch_scc0 L_kna4_%=\n" \
"buffer_load_dwordx4 v[148:151], v20, s[56:59], s14 offen offset:0 sc1\n" \
"L_kna4_%=:\n" \
"s_bitcmp1_b32 s20, 5\n" \
"s_cbranch_scc0 L_kna5_%=\n" \
"buffer_load_dwordx4 v[156:159], v20, s[56:59], s14 offen offset:2048 sc1\n" \
"L_kna5_%=:\n" \
"s_bitcmp1_b32 s20, 6\n" \
"s_cbranch_scc0 L_kna6_%=\n" \
"buffer_load_dwordx4 v[164:167], v20, s[56:59], s15 offen offset:0 sc1\n" \
"L_kna6_%=:\n" \
"s_waitcnt vmcnt(0)\n" \
"s_branch L_rna_%=\n" \
"L_snx_%=:\n" \
"buffer_load_dword v37, v40, s[56:59], s74 offen sc1\n" \
"s_add_u32 s101, s101, 1\n" \
"s_waitcnt vmcnt(0)\n" \
"v_cmp_eq_u32_e64 s[98:99], s62, v37\n" \
"s_cmp_gt_u32 s101, 0x100000\n" \
"s_cbranch_scc1 L_rnx_%=\n" \
"s_andn2_b32 s75, s21, s98\n" \
"s_cbranch_scc0 L_gnx_%=\n" \
"s_sleep 1\n" \
"s_branch L_snx_%=\n" \
"L_gnx_%=:\n" \
"s_bitcmp1_b32 s21, 16\n" \
"s_cbranch_scc0 L_knx16_%=\n" \
"buffer_load_dwordx4 v[112:115], v20, s[56:59], s16 offen offset:0 sc1\n" \
"L_knx16_%=:\n" \
"s_bitcmp1_b32 s21, 17\n" \
"s_cbranch_scc0 L_knx17_%=\n" \
"buffer_load_dwordx4 v[184:187], v20, s[56:59], s16 offen offset:2048 sc1\n" \
"L_knx17_%=:\n" \
"s_bitcmp1_b32 s21, 18\n" \
"s_cbranch_scc0 L_knx18_%=\n" \
"buffer_load_dwordx4 v[120:123], v20, s[56:59], s17 offen offset:0 sc1\n" \
"L_knx18_%=:\n" \
"s_bitcmp1_b32 s21, 19\n" \
"s_cbranch_scc0 L_knx19_%=\n" \
"buffer_load_dwordx4 v[192:195], v20, s[56:59], s17 offen offset:2048 sc1\n" \
"L_knx19_%=:\n" \
"s_bitcmp1_b32 s21, 20\n" \
"s_cbranch_scc0 L_knx20_%=\n" \
"buffer_load_dwordx4 v[128:131], v20, s[56:59], s18 offen offset:0 sc1\n" \
"L_knx20_%=:\n" \
"s_bitcmp1_b32 s21, 21\n" \
"s_cbranch_scc0 L_knx21_%=\n" \
"buffer_load_dwordx4 v[200:203], v20, s[56:59], s18 offen offset:2048 sc1\n" \
"L_knx21_%=:\n" \
"s_bitcmp1_b32 s21, 22\n" \
"s_cbranch_scc0 L_knx22_%=\n" \
"buffer_load_dwordx4 v[176:179], v20, s[56:59], s19 offen offset:0 sc1\n" \
"L_knx22_%=:\n" \
"s_bitcmp1_b32 s21, 23\n" \
"s_cbranch_scc0 L_knx23_%=\n" \
"buffer_load_dwordx4 v[248:251], v20, s[56:59], s19 offen offset:2048 sc1\n" \
"L_knx23_%=:\n" \
"s_bitcmp1_b32 s21, 8\n" \
"s_cbranch_scc0 L_knx8_%=\n" \
"buffer_load_dwordx4 v[188:191], v20, s[56:59], s12 offen offset:1024 sc1\n" \
"L_knx8_%=:\n" \
"s_bitcmp1_b32 s21, 9\n" \
"s_cbranch_scc0 L_knx9_%=\n" \
"buffer_load_dwordx4 v[196:199], v20, s[56:59], s12 offen offset:3072 sc1\n" \
"L_knx9_%=:\n" \
"s_waitcnt vmcnt(0)\n" \
"s_branch L_rnx_%=\n" \
"L_sx0_%=:\n" \
"s_sleep 1\n" \
"buffer_load_dword v37, v40, s[56:59], s74 offen sc1\n" \
"s_add_u32 s101, s101, 1\n" \
"s_waitcnt vmcnt(0)\n" \
"v_cmp_eq_u32_e64 s[98:99], s62, v37\n" \
"s_cmp_gt_u32 s101, 0x100000\n" \
"s_cbranch_scc1 L_rx0_%=\n" \
"s_andn2_b32 s75, 0x400, s98\n" \
"s_cbranch_scc1 L_sx0_%=\n" \
"s_branch L_rx0_%=\n" \
"L_sx1_%=:\n" \
"s_sleep 1\n" \
"buffer_load_dword v37, v40, s[56:59], s74 offen sc1\n" \
"s_add_u32 s101, s101, 1\n" \
"s_waitcnt vmcnt(0)\n" \
"v_cmp_eq_u32_e64 s[98:99], s62, v37\n" \
"s_cmp_gt_u32 s101, 0x100000\n" \
"s_cbranch_scc1 L_rx1_%=\n" \
"s_andn2_b32 s75, 0x800, s98\n" \
"s_cbranch_scc1 L_sx1_%=\n" \
"s_branch L_rx1_%=\n" \
"L_sx2_%=:\n" \
"s_sleep 1\n" \
"buffer_load_dword v37, v40, s[56:59], s74 offen sc1\n" \
"s_add_u32 s101, s101, 1\n" \
"s_waitcnt vmcnt(0)\n" \
"v_cmp_eq_u32_e64 s[98:99], s62, v37\n" \
"s_cmp_gt_u32 s101, 0x100000\n" \
"s_cbranch_scc1 L_rx2_%=\n" \
"s_andn2_b32 s75, 0x1000, s98\n" \
"s_cbranch_scc1 L_sx2_%=\n" \
"s_branch L_rx2_%=\n" \
"L_sx3_%=:\n" \
"s_sleep 1\n" \
"buffer_load_dword v37, v40, s[56:59], s74 offen sc1\n" \
"s_add_u32 s101, s101, 1\n" \
"s_waitcnt vmcnt(0)\n" \
"v_cmp_eq_u32_e64 s[98:99], s62, v37\n" \
"s_cmp_gt_u32 s101, 0x100000\n" \
"s_cbranch_scc1 L_rx3_%=\n" \
"s_andn2_b32 s75, 0x2000, s98\n" \
"s_cbranch_scc1 L_sx3_%=\n" \
"s_branch L_rx3_%=\n" \
"L_sx4_%=:\n" \
"s_sleep 1\n" \
"buffer_load_dword v37, v40, s[56:59], s74 offen sc1\n" \
"s_add_u32 s101, s101, 1\n" \
"s_waitcnt vmcnt(0)\n" \
"v_cmp_eq_u32_e64 s[98:99], s62, v37\n" \
"s_cmp_gt_u32 s101, 0x100000\n" \
"s_cbranch_scc1 L_rx4_%=\n" \
"s_andn2_b32 s75, 0x4000, s98\n" \
"s_cbranch_scc1 L_sx4_%=\n" \
"s_branch L_rx4_%=\n" \
"L_att_stub:\n" \
"s_mov_b32 s101, 0\n" \
"v_add_u32_e32 v32, 0x49000, v20\n" \
"v_readlane_b32 s96, v11, 36\n" \
"v_readlane_b32 s97, v11, 37\n" \
"buffer_load_dwordx4 a[96:99], v20, s[56:59], s60 offen offset:0 sc1\n" \
"buffer_load_dwordx4 a[112:115], v32, s[56:59], s60 offen offset:0 sc1\n" \
"buffer_load_dwordx4 a[100:103], v20, s[56:59], s60 offen offset:1024 sc1\n" \
"buffer_load_dwordx4 a[116:119], v32, s[56:59], s60 offen offset:1024 sc1\n" \
"buffer_load_dwordx4 a[104:107], v20, s[56:59], s60 offen offset:2048 sc1\n" \
"buffer_load_dwordx4 a[120:123], v32, s[56:59], s60 offen offset:2048 sc1\n" \
"buffer_load_dwordx4 a[108:111], v20, s[56:59], s60 offen offset:3072 sc1\n" \
"buffer_load_dwordx4 a[124:127], v32, s[56:59], s60 offen offset:3072 sc1\n" \
"buffer_load_dwordx4 a[220:223], v20, s[56:59], s96 offen offset:0 sc1\n" \
"buffer_load_dwordx4 a[228:231], v32, s[56:59], s96 offen offset:0 sc1\n" \
"buffer_load_dwordx4 a[236:239], v20, s[56:59], s97 offen offset:0 sc1\n" \
"buffer_load_dwordx4 a[244:247], v32, s[56:59], s97 offen offset:0 sc1\n" \
"buffer_load_dwordx4 a[224:227], v20, s[56:59], s96 offen offset:1024 sc1\n" \
"buffer_load_dwordx4 a[232:235], v32, s[56:59], s96 offen offset:1024 sc1\n" \
"buffer_load_dwordx4 a[240:243], v20, s[56:59], s97 offen offset:1024 sc1\n" \
"buffer_load_dwordx4 a[248:251], v32, s[56:59], s97 offen offset:1024 sc1\n" \
"buffer_load_dwordx4 v[64:67], v20, s[56:59], s61 offen offset:0 sc1\n" \
"buffer_load_dwordx4 v[68:71], v20, s[56:59], s61 offen offset:1024 sc1\n" \
"buffer_load_dwordx4 v[72:75], v20, s[56:59], s61 offen offset:2048 sc1\n" \
"buffer_load_dwordx4 v[76:79], v20, s[56:59], s61 offen offset:3072 sc1\n" \
"buffer_load_dwordx4 v[80:83], v32, s[56:59], s61 offen offset:0 sc1\n" \
"buffer_load_dwordx4 v[84:87], v32, s[56:59], s61 offen offset:1024 sc1\n" \
"buffer_load_dwordx4 v[96:99], v32, s[56:59], s61 offen offset:2048 sc1\n" \
"buffer_load_dwordx4 v[100:103], v32, s[56:59], s61 offen offset:3072 sc1\n" \
"v_not_b32_e32 v26, s62\n" \
"s_waitcnt vmcnt(0)\n" \
"s_branch L_out_%=\n" \
".fill (L_ab + 16384 - .) / 4, 4, 0xBF800000\n" \
".fill (L_ab + 16384 - .) / 4, 4, 0xBF800000\n" \
"L_ap4:\n" \
"s_setpc_b64 s[88:89]\n" \
"L_gsw:\n" \
"v_mfma_f32_16x16x32_bf16 v[104:107], a[128:131], a[16:19], v[36:39]\n" \
"s_add_u32 s68, s60, 0xfff6e000\n" \
"s_add_u32 s69, s60, 0xfffb7000\n" \
"s_add_u32 s70, s60, 0x0\n" \
"s_add_u32 s71, s60, 0x49000\n" \
"v_mfma_f32_16x16x32_bf16 v[104:107], a[132:135], a[20:23], v[104:107]\n" \
"s_xor_b32 m0, s65, 0x12000\n" \
"ds_read_b128 a[160:163], v49 offset:0\n" \
"v_mfma_f32_16x16x32_bf16 v[104:107], a[136:139], a[24:27], v[104:107]\n" \
"buffer_load_dwordx4 v50, s[8:11], s64 offen offset:0 sc1 lds\n" \
"v_mfma_f32_16x16x32_bf16 v[104:107], a[140:143], a[28:31], v[104:107]\n" \
"ds_read_b128 a[164:167], v49 offset:1024\n" \
"ds_read_b128 a[168:171], v49 offset:2048\n" \
"v_mfma_f32_16x16x32_bf16 v[108:111], a[156:159], a[28:31], v[36:39]\n" \
"ds_read_b128 a[172:175], v49 offset:3072\n" \
"ds_read_b128 a[176:179], v49 offset:4096\n" \
"v_mfma_f32_16x16x32_bf16 v[108:111], a[152:155], a[24:27], v[108:111]\n" \
"buffer_load_dwordx4 v50, s[8:11], s64 offen offset:1024 sc1 lds\n" \
"v_mfma_f32_16x16x32_bf16 v[108:111], a[148:151], a[20:23], v[108:111]\n" \
"s_mov_b32 s65, s13\n" \
"v_add_u32_e32 v63, s15, v20\n" \
"v_mfma_f32_16x16x32_bf16 v[108:111], a[144:147], a[16:19], v[108:111]\n" \
"s_add_u32 s67, s14, 40960\n" \
"v_add_u32_e32 v50, 0x6000, v63\n" \
"v_mfma_f32_16x16x32_bf16 v[116:119], a[144:147], a[32:35], v[40:43]\n" \
"s_xor_b32 m0, s65, 0x8000\n" \
"ds_read_b128 a[180:183], v49 offset:5120\n" \
"v_mfma_f32_16x16x32_bf16 v[116:119], a[148:151], a[36:39], v[116:119]\n" \
"ds_read_b128 a[184:187], v49 offset:6144\n" \
"v_and_b32_e32 v68, v12, v68\n" \
"v_mfma_f32_16x16x32_bf16 v[116:119], a[152:155], a[40:43], v[116:119]\n" \
"v_and_b32_e32 v69, v13, v69\n" \
"v_and_b32_e32 v70, v14, v70\n" \
"v_mfma_f32_16x16x32_bf16 v[116:119], a[156:159], a[44:47], v[116:119]\n" \
"buffer_load_dwordx4 v50, s[8:11], s67 offen offset:0 sc1 lds\n" \
"v_mfma_f32_16x16x32_bf16 v[112:115], a[140:143], a[44:47], v[40:43]\n" \
"v_and_b32_e32 v71, v15, v71\n" \
"ds_read_b128 a[188:191], v49 offset:7168\n" \
"v_mfma_f32_16x16x32_bf16 v[112:115], a[136:139], a[40:43], v[112:115]\n" \
"v_exp_f32_e32 v86, v86\n" \
"v_mfma_f32_16x16x32_bf16 v[112:115], a[132:135], a[36:39], v[112:115]\n" \
"v_exp_f32_e32 v87, v87\n" \
"v_mfma_f32_16x16x32_bf16 v[112:115], a[128:131], a[32:35], v[112:115]\n" \
"v_exp_f32_e32 v84, v84\n" \
"v_mfma_f32_16x16x32_bf16 v[120:123], a[128:131], a[48:51], v[44:47]\n" \
"buffer_load_dwordx4 v50, s[8:11], s67 offen offset:1024 sc1 lds\n" \
"v_mfma_f32_16x16x32_bf16 v[120:123], a[132:135], a[52:55], v[120:123]\n" \
"v_exp_f32_e32 v85, v85\n" \
"v_mfma_f32_16x16x32_bf16 v[120:123], a[136:139], a[56:59], v[120:123]\n" \
"v_cvt_pk_bf16_f32 v87, v86, v87\n" \
"v_cvt_pk_bf16_f32 v86, v84, v85\n" \
"v_mfma_f32_16x16x32_bf16 v[120:123], a[140:143], a[60:63], v[120:123]\n" \
"v_exp_f32_e32 v82, v82\n" \
"v_mfma_f32_16x16x32_bf16 v[124:127], a[156:159], a[60:63], v[44:47]\n" \
"v_exp_f32_e32 v83, v83\n" \
"v_mfma_f32_16x16x32_bf16 v[124:127], a[152:155], a[56:59], v[124:127]\n" \
"v_exp_f32_e32 v80, v80\n" \
"v_mfma_f32_16x16x32_bf16 v[124:127], a[148:151], a[52:55], v[124:127]\n" \
"buffer_load_dwordx4 v50, s[8:11], s67 offen offset:2048 sc1 lds\n" \
"v_mfma_f32_16x16x32_bf16 v[124:127], a[144:147], a[48:51], v[124:127]\n" \
"v_cvt_pk_bf16_f32 v85, v82, v83\n" \
"s_waitcnt lgkmcnt(1)\n" \
"v_mfma_f32_16x16x32_bf16 v[128:131], a[160:163], v[68:71], v[128:131]\n" \
"v_exp_f32_e32 v81, v81\n" \
"v_mfma_f32_16x16x32_bf16 v[160:163], a[160:163], v[72:75], v[160:163]\n" \
"v_cvt_pk_bf16_f32 v84, v80, v81\n" \
"s_mov_b32 s64, s14\n" \
"v_mfma_f32_16x16x32_bf16 v[132:135], a[164:167], v[68:71], v[132:135]\n" \
"v_exp_f32_e32 v88, v88\n" \
"v_mfma_f32_16x16x32_bf16 v[164:167], a[164:167], v[72:75], v[164:167]\n" \
"v_exp_f32_e32 v89, v89\n" \
"v_mfma_f32_16x16x32_bf16 v[136:139], a[168:171], v[68:71], v[136:139]\n" \
"buffer_load_dwordx4 v50, s[8:11], s67 offen offset:3072 sc1 lds\n" \
"v_mfma_f32_16x16x32_bf16 v[168:171], a[168:171], v[72:75], v[168:171]\n" \
"v_exp_f32_e32 v90, v90\n" \
"v_mfma_f32_16x16x32_bf16 v[140:143], a[172:175], v[68:71], v[140:143]\n" \
"v_exp_f32_e32 v91, v91\n" \
"v_mfma_f32_16x16x32_bf16 v[172:175], a[172:175], v[72:75], v[172:175]\n" \
"v_cvt_pk_bf16_f32 v88, v88, v89\n" \
"v_cvt_pk_bf16_f32 v89, v90, v91\n" \
"v_mfma_f32_16x16x32_bf16 v[144:147], a[176:179], v[68:71], v[144:147]\n" \
"v_exp_f32_e32 v92, v92\n" \
"v_mfma_f32_16x16x32_bf16 v[176:179], a[176:179], v[72:75], v[176:179]\n" \
"v_exp_f32_e32 v93, v93\n" \
"v_mfma_f32_16x16x32_bf16 v[148:151], a[180:183], v[68:71], v[148:151]\n" \
"v_exp_f32_e32 v94, v94\n" \
"v_mfma_f32_16x16x32_bf16 v[180:183], a[180:183], v[72:75], v[180:183]\n" \
"v_exp_f32_e32 v95, v95\n" \
"v_mfma_f32_16x16x32_bf16 v[152:155], a[184:187], v[68:71], v[152:155]\n" \
"v_cvt_pk_bf16_f32 v90, v92, v93\n" \
"v_cvt_pk_bf16_f32 v91, v94, v95\n" \
"v_mfma_f32_16x16x32_bf16 v[184:187], a[184:187], v[72:75], v[184:187]\n" \
"v_add_u32_e32 v51, 0x12000, v63\n" \
"s_waitcnt lgkmcnt(0)\n" \
"v_mfma_f32_16x16x32_bf16 v[156:159], a[188:191], v[68:71], v[156:159]\n" \
"s_xor_b32 m0, s65, 0xa000\n" \
"ds_read_b32 v58, v18 offset:512\n" \
"v_mfma_f32_16x16x32_bf16 v[188:191], a[188:191], v[72:75], v[188:191]\n" \
"buffer_load_dwordx4 v51, s[8:11], s64 offen offset:0 sc1 lds\n" \
"v_smfmac_f32_16x16x64_bf16 a[192:195], a[200:203], v[68:75], v21\n" \
"buffer_load_dwordx4 v51, s[8:11], s64 offen offset:1024 sc1 lds\n" \
"v_mfma_f32_16x16x32_bf16 v[220:223], a[188:191], v[84:87], v[220:223]\n" \
"ds_read_b32 v59, v18 offset:640\n" \
"ds_read_b32 v60, v18 offset:768\n" \
"v_mfma_f32_16x16x32_bf16 v[252:255], a[188:191], v[88:91], v[252:255]\n" \
"ds_read_b32 v61, v18 offset:896\n" \
"ds_read_b128 a[128:131], v48 offset:0\n" \
"v_mfma_f32_16x16x32_bf16 v[216:219], a[184:187], v[84:87], v[216:219]\n" \
"ds_read_b128 a[144:147], v48 offset:4096\n" \
"ds_read_b128 a[132:135], v48 offset:1024\n" \
"v_mfma_f32_16x16x32_bf16 v[248:251], a[184:187], v[88:91], v[248:251]\n" \
"ds_read_b128 a[148:151], v48 offset:5120\n" \
"ds_read_b128 a[136:139], v48 offset:2048\n" \
"v_mfma_f32_16x16x32_bf16 v[212:215], a[180:183], v[84:87], v[212:215]\n" \
"ds_read_b128 a[152:155], v48 offset:6144\n" \
"ds_read_b128 a[140:143], v48 offset:3072\n" \
"v_mfma_f32_16x16x32_bf16 v[244:247], a[180:183], v[88:91], v[244:247]\n" \
"ds_read_b128 a[156:159], v48 offset:7168\n" \
"v_add_u32_e32 v52, 0x14000, v63\n" \
"v_mfma_f32_16x16x32_bf16 v[208:211], a[176:179], v[84:87], v[208:211]\n" \
"buffer_load_dwordx4 v51, s[8:11], s64 offen offset:2048 sc1 lds\n" \
"v_mfma_f32_16x16x32_bf16 v[240:243], a[176:179], v[88:91], v[240:243]\n" \
"v_exp_f32_e32 v104, v104\n" \
"v_mfma_f32_16x16x32_bf16 v[204:207], a[172:175], v[84:87], v[204:207]\n" \
"v_exp_f32_e32 v105, v105\n" \
"v_mfma_f32_16x16x32_bf16 v[236:239], a[172:175], v[88:91], v[236:239]\n" \
"buffer_load_dwordx4 v51, s[8:11], s64 offen offset:3072 sc1 lds\n" \
"v_mfma_f32_16x16x32_bf16 v[200:203], a[168:171], v[84:87], v[200:203]\n" \
"v_exp_f32_e32 v106, v106\n" \
"v_mfma_f32_16x16x32_bf16 v[232:235], a[168:171], v[88:91], v[232:235]\n" \
"v_exp_f32_e32 v107, v107\n" \
"v_mfma_f32_16x16x32_bf16 v[196:199], a[164:167], v[84:87], v[196:199]\n" \
"v_cvt_pk_bf16_f32 v104, v104, v105\n" \
"v_cvt_pk_bf16_f32 v105, v106, v107\n" \
"v_mfma_f32_16x16x32_bf16 v[228:231], a[164:167], v[88:91], v[228:231]\n" \
"v_exp_f32_e32 v108, v108\n" \
"v_mfma_f32_16x16x32_bf16 v[192:195], a[160:163], v[84:87], v[192:195]\n" \
"v_exp_f32_e32 v109, v109\n" \
"v_mfma_f32_16x16x32_bf16 v[224:227], a[160:163], v[88:91], v[224:227]\n" \
"v_exp_f32_e32 v110, v110\n" \
"v_smfmac_f32_16x16x64_bf16 a[196:199], a[200:203], v[84:91], v21\n" \
"v_exp_f32_e32 v111, v111\n" \
"v_cvt_pk_bf16_f32 v106, v108, v109\n" \
"v_cvt_pk_bf16_f32 v107, v110, v111\n" \
"v_and_b32_e32 v104, v12, v104\n" \
"v_and_b32_e32 v105, v13, v105\n" \
"v_and_b32_e32 v106, v14, v106\n" \
"v_and_b32_e32 v107, v15, v107\n" \
"v_accvgpr_read_b32 v54, a192\n" \
"v_accvgpr_read_b32 v22, a193\n" \
"v_add_u32_e32 v53, 0x20000, v63\n" \
"v_exp_f32_e32 v118, v118\n" \
"v_exp_f32_e32 v119, v119\n" \
"v_exp_f32_e32 v116, v116\n" \
"v_exp_f32_e32 v117, v117\n" \
"v_cvt_pk_bf16_f32 v119, v118, v119\n" \
"v_exp_f32_e32 v114, v114\n" \
"v_exp_f32_e32 v115, v115\n" \
"v_exp_f32_e32 v112, v112\n" \
"v_exp_f32_e32 v113, v113\n" \
"v_cvt_pk_bf16_f32 v118, v116, v117\n" \
"v_cvt_pk_bf16_f32 v117, v114, v115\n" \
"v_cvt_pk_bf16_f32 v116, v112, v113\n" \
"v_readlane_b32 s93, v11, 19\n" \
"s_waitcnt vmcnt(10) lgkmcnt(0)\n" \
"s_barrier\n" \
"v_mfma_f32_16x16x32_bf16 v[80:83], a[128:131], a[32:35], v[40:43]\n" \
"ds_read_b128 a[160:163], v49 offset:8192\n" \
"ds_read_b128 a[164:167], v49 offset:9216\n" \
"v_mfma_f32_16x16x32_bf16 v[80:83], a[132:135], a[36:39], v[80:83]\n" \
"ds_read_b128 a[168:171], v49 offset:10240\n" \
"s_xor_b32 m0, s65, 0x18000\n" \
"v_mfma_f32_16x16x32_bf16 v[80:83], a[136:139], a[40:43], v[80:83]\n" \
"buffer_load_dwordx4 v52, s[8:11], s64 offen offset:0 sc1 lds\n" \
"v_mfma_f32_16x16x32_bf16 v[80:83], a[140:143], a[44:47], v[80:83]\n" \
"ds_read_b128 a[172:175], v49 offset:11264\n" \
"ds_read_b128 a[176:179], v49 offset:12288\n" \
"v_mfma_f32_16x16x32_bf16 v[84:87], a[156:159], a[44:47], v[40:43]\n" \
"ds_read_b128 a[180:183], v49 offset:13312\n" \
"ds_read_b128 a[184:187], v49 offset:14336\n" \
"v_mfma_f32_16x16x32_bf16 v[84:87], a[152:155], a[40:43], v[84:87]\n" \
"buffer_load_dwordx4 v52, s[8:11], s64 offen offset:1024 sc1 lds\n" \
"v_mfma_f32_16x16x32_bf16 v[84:87], a[148:151], a[36:39], v[84:87]\n" \
"ds_read_b128 a[188:191], v49 offset:15360\n" \
"v_readlane_b32 s80, v11, 11\n" \
"v_mfma_f32_16x16x32_bf16 v[84:87], a[144:147], a[32:35], v[84:87]\n" \
"buffer_load_dwordx4 v52, s[8:11], s64 offen offset:2048 sc1 lds\n" \
"v_mfma_f32_16x16x32_bf16 v[92:95], a[144:147], a[48:51], v[44:47]\n" \
"v_exp_f32_e32 v120, v120\n" \
"v_mfma_f32_16x16x32_bf16 v[92:95], a[148:151], a[52:55], v[92:95]\n" \
"v_exp_f32_e32 v121, v121\n" \
"v_mfma_f32_16x16x32_bf16 v[92:95], a[152:155], a[56:59], v[92:95]\n" \
"buffer_load_dwordx4 v52, s[8:11], s64 offen offset:3072 sc1 lds\n" \
"v_mfma_f32_16x16x32_bf16 v[92:95], a[156:159], a[60:63], v[92:95]\n" \
"s_mov_b32 m0, s65\n" \
"v_cvt_pk_bf16_f32 v120, v120, v121\n" \
"v_mfma_f32_16x16x32_bf16 v[88:91], a[140:143], a[60:63], v[44:47]\n" \
"v_exp_f32_e32 v122, v122\n" \
"v_mfma_f32_16x16x32_bf16 v[88:91], a[136:139], a[56:59], v[88:91]\n" \
"buffer_load_dwordx4 v53, s[8:11], s64 offen offset:0 sc1 lds\n" \
"v_mfma_f32_16x16x32_bf16 v[88:91], a[132:135], a[52:55], v[88:91]\n" \
"v_exp_f32_e32 v123, v123\n" \
"v_mfma_f32_16x16x32_bf16 v[88:91], a[128:131], a[48:51], v[88:91]\n" \
"v_exp_f32_e32 v124, v124\n" \
"v_mfma_f32_16x16x32_bf16 a[192:195], a[212:215], v[104:107], a[192:195]\n" \
"v_cvt_pk_bf16_f32 v121, v122, v123\n" \
"s_waitcnt lgkmcnt(5)\n" \
"v_mfma_f32_16x16x32_bf16 v[160:163], a[160:163], v[104:107], v[160:163]\n" \
"buffer_load_dwordx4 v53, s[8:11], s64 offen offset:1024 sc1 lds\n" \
"v_mfma_f32_16x16x32_bf16 v[164:167], a[164:167], v[104:107], v[164:167]\n" \
"v_exp_f32_e32 v125, v125\n" \
"v_mfma_f32_16x16x32_bf16 v[168:171], a[168:171], v[104:107], v[168:171]\n" \
"v_cvt_pk_bf16_f32 v122, v124, v125\n" \
"s_waitcnt lgkmcnt(1)\n" \
"v_mfma_f32_16x16x32_bf16 v[172:175], a[172:175], v[104:107], v[172:175]\n" \
"v_exp_f32_e32 v126, v126\n" \
"v_mfma_f32_16x16x32_bf16 v[176:179], a[176:179], v[104:107], v[176:179]\n" \
"v_exp_f32_e32 v127, v127\n" \
"v_mfma_f32_16x16x32_bf16 v[180:183], a[180:183], v[104:107], v[180:183]\n" \
"v_cvt_pk_bf16_f32 v123, v126, v127\n" \
"v_readlane_b32 s81, v11, 12\n" \
"v_mfma_f32_16x16x32_bf16 v[184:187], a[184:187], v[104:107], v[184:187]\n" \
"s_mov_b32 s82, 0x47fca0\n" \
"s_waitcnt lgkmcnt(0)\n" \
"v_mfma_f32_16x16x32_bf16 v[188:191], a[188:191], v[104:107], v[188:191]\n" \
"buffer_load_dwordx4 v53, s[8:11], s64 offen offset:2048 sc1 lds\n" \
"v_mfma_f32_16x16x32_bf16 v[220:223], a[188:191], v[116:119], v[220:223]\n" \
"buffer_load_dwordx4 v53, s[8:11], s64 offen offset:3072 sc1 lds\n" \
"v_mfma_f32_16x16x32_bf16 v[252:255], a[188:191], v[120:123], v[252:255]\n" \
"s_mov_b32 s83, 0x20000\n" \
"v_readlane_b32 s92, v11, 26\n" \
"v_mfma_f32_16x16x32_bf16 v[216:219], a[184:187], v[116:119], v[216:219]\n" \
"s_add_u32 m0, s93, 0x0\n" \
"ds_read_b128 a[128:131], v48 offset:8192\n" \
"v_mfma_f32_16x16x32_bf16 v[248:251], a[184:187], v[120:123], v[248:251]\n" \
"ds_read_b128 a[144:147], v48 offset:12288\n" \
"ds_read_b128 a[132:135], v48 offset:9216\n" \
"v_mfma_f32_16x16x32_bf16 v[212:215], a[180:183], v[116:119], v[212:215]\n" \
"buffer_load_dwordx4 v0, s[80:83], s92 offen offset:0 lds\n" \
"v_mfma_f32_16x16x32_bf16 v[244:247], a[180:183], v[120:123], v[244:247]\n" \
"buffer_load_dwordx4 v1, s[80:83], s92 offen offset:1024 lds\n" \
"v_mfma_f32_16x16x32_bf16 v[208:211], a[176:179], v[116:119], v[208:211]\n" \
"ds_read_b128 a[148:151], v48 offset:13312\n" \
"ds_read_b128 a[136:139], v48 offset:10240\n" \
"v_mfma_f32_16x16x32_bf16 v[240:243], a[176:179], v[120:123], v[240:243]\n" \
"ds_read_b128 a[152:155], v48 offset:14336\n" \
"ds_read_b128 a[140:143], v48 offset:11264\n" \
"v_mfma_f32_16x16x32_bf16 v[204:207], a[172:175], v[116:119], v[204:207]\n" \
"buffer_load_dwordx4 v2, s[80:83], s92 offen offset:2048 lds\n" \
"v_mfma_f32_16x16x32_bf16 v[236:239], a[172:175], v[120:123], v[236:239]\n" \
"ds_read_b128 a[156:159], v48 offset:15360\n" \
"s_xor_b32 s65, s65, 0x8000\n" \
"v_mfma_f32_16x16x32_bf16 v[200:203], a[168:171], v[116:119], v[200:203]\n" \
"s_add_u32 s64, s64, 0x10000\n" \
"v_add_u32_e32 v49, 0x10000, v48\n" \
"v_mfma_f32_16x16x32_bf16 v[232:235], a[168:171], v[120:123], v[232:235]\n" \
"buffer_load_dwordx4 v3, s[80:83], s92 offen offset:3072 lds\n" \
"v_mfma_f32_16x16x32_bf16 v[196:199], a[164:167], v[116:119], v[196:199]\n" \
"v_exp_f32_e32 v86, v86\n" \
"v_mfma_f32_16x16x32_bf16 v[228:231], a[164:167], v[120:123], v[228:231]\n" \
"v_exp_f32_e32 v87, v87\n" \
"v_mfma_f32_16x16x32_bf16 v[192:195], a[160:163], v[116:119], v[192:195]\n" \
"v_exp_f32_e32 v84, v84\n" \
"v_mfma_f32_16x16x32_bf16 v[224:227], a[160:163], v[120:123], v[224:227]\n" \
"v_exp_f32_e32 v85, v85\n" \
"v_smfmac_f32_16x16x64_bf16 a[196:199], a[200:203], v[116:123], v21\n" \
"v_exp_f32_e32 v88, v88\n" \
"v_exp_f32_e32 v89, v89\n" \
"v_cvt_pk_bf16_f32 v87, v86, v87\n" \
"v_exp_f32_e32 v82, v82\n" \
"v_exp_f32_e32 v83, v83\n" \
"v_exp_f32_e32 v80, v80\n" \
"v_exp_f32_e32 v81, v81\n" \
"v_exp_f32_e32 v90, v90\n" \
"v_exp_f32_e32 v91, v91\n" \
"v_cvt_pk_bf16_f32 v86, v84, v85\n" \
"v_cvt_pk_bf16_f32 v88, v88, v89\n" \
"v_exp_f32_e32 v92, v92\n" \
"v_exp_f32_e32 v93, v93\n" \
"v_exp_f32_e32 v94, v94\n" \
"v_exp_f32_e32 v95, v95\n" \
"v_cvt_pk_bf16_f32 v85, v82, v83\n" \
"v_cvt_pk_bf16_f32 v84, v80, v81\n" \
"v_cvt_pk_bf16_f32 v89, v90, v91\n" \
"v_and_b32_e32 v84, v12, v84\n" \
"v_and_b32_e32 v85, v13, v85\n" \
"v_and_b32_e32 v86, v14, v86\n" \
"v_and_b32_e32 v87, v15, v87\n" \
"v_cvt_pk_bf16_f32 v90, v92, v93\n" \
"v_cvt_pk_bf16_f32 v91, v94, v95\n" \
"v_add_u32_e32 v48, s16, v20\n" \
"v_add_f32_e32 v54, v54, v22\n" \
"v_accvgpr_read_b32 v55, a194\n" \
"v_accvgpr_read_b32 v23, a195\n" \
"v_mfma_f32_16x16x32_bf16 a[192:195], a[216:219], a[216:219], 0\n" \
"v_max_f32_e32 v62, v62, v54\n" \
"v_add_f32_e32 v55, v55, v23\n" \
"v_max_f32_e32 v62, v62, v55\n" \
"v_rcp_f32_e32 v54, v54\n" \
"v_rcp_f32_e32 v55, v55\n" \
"v_mul_f32_e32 v54, v54, v58\n" \
"v_mul_f32_e32 v55, v55, v59\n" \
"v_mul_f32_e32 v128, v54, v128\n" \
"v_mul_f32_e32 v129, v54, v129\n" \
"v_mul_f32_e32 v136, v54, v136\n" \
"v_mul_f32_e32 v137, v54, v137\n" \
"v_mul_f32_e32 v144, v54, v144\n" \
"v_mul_f32_e32 v145, v54, v145\n" \
"v_mul_f32_e32 v152, v54, v152\n" \
"v_mul_f32_e32 v153, v54, v153\n" \
"v_mul_f32_e32 v160, v55, v160\n" \
"v_mul_f32_e32 v161, v55, v161\n" \
"v_mul_f32_e32 v168, v55, v168\n" \
"v_mul_f32_e32 v169, v55, v169\n" \
"v_mul_f32_e32 v176, v55, v176\n" \
"v_mul_f32_e32 v177, v55, v177\n" \
"v_mul_f32_e32 v184, v55, v184\n" \
"v_mul_f32_e32 v185, v55, v185\n" \
"v_cvt_pk_bf16_f32 v128, v128, v129\n" \
"v_mul_f32_e32 v130, v54, v130\n" \
"v_mul_f32_e32 v131, v54, v131\n" \
"v_cvt_pk_bf16_f32 v136, v136, v137\n" \
"v_mul_f32_e32 v138, v54, v138\n" \
"v_mul_f32_e32 v139, v54, v139\n" \
"v_cvt_pk_bf16_f32 v144, v144, v145\n" \
"v_mul_f32_e32 v146, v54, v146\n" \
"v_mul_f32_e32 v147, v54, v147\n" \
"v_cvt_pk_bf16_f32 v152, v152, v153\n" \
"v_mul_f32_e32 v154, v54, v154\n" \
"v_mul_f32_e32 v155, v54, v155\n" \
"v_cvt_pk_bf16_f32 v160, v160, v161\n" \
"v_mul_f32_e32 v162, v55, v162\n" \
"v_mul_f32_e32 v163, v55, v163\n" \
"v_cvt_pk_bf16_f32 v168, v168, v169\n" \
"v_mul_f32_e32 v170, v55, v170\n" \
"v_mul_f32_e32 v171, v55, v171\n" \
"v_cvt_pk_bf16_f32 v176, v176, v177\n" \
"v_mul_f32_e32 v178, v55, v178\n" \
"v_mul_f32_e32 v179, v55, v179\n" \
"v_cvt_pk_bf16_f32 v184, v184, v185\n" \
"v_mul_f32_e32 v186, v55, v186\n" \
"v_mul_f32_e32 v187, v55, v187\n" \
"v_cvt_pk_bf16_f32 v129, v130, v131\n" \
"v_mul_f32_e32 v132, v54, v132\n" \
"v_mul_f32_e32 v133, v54, v133\n" \
"v_mul_f32_e32 v134, v54, v134\n" \
"v_mul_f32_e32 v135, v54, v135\n" \
"v_cvt_pk_bf16_f32 v137, v138, v139\n" \
"v_mul_f32_e32 v140, v54, v140\n" \
"s_waitcnt vmcnt(12) lgkmcnt(0)\n" \
"s_barrier\n" \
"v_mfma_f32_16x16x32_bf16 v[120:123], a[128:131], a[48:51], v[44:47]\n" \
"s_xor_b32 m0, s65, 0x12000\n" \
"ds_read_b128 a[188:191], v49 offset:7168\n" \
"v_mfma_f32_16x16x32_bf16 v[120:123], a[132:135], a[52:55], v[120:123]\n" \
"buffer_load_dwordx4 v50, s[8:11], s64 offen offset:0 sc1 lds\n" \
"v_mfma_f32_16x16x32_bf16 v[120:123], a[136:139], a[56:59], v[120:123]\n" \
"ds_read_b128 a[184:187], v49 offset:6144\n" \
"ds_read_b128 a[180:183], v49 offset:5120\n" \
"v_mfma_f32_16x16x32_bf16 v[120:123], a[140:143], a[60:63], v[120:123]\n" \
"buffer_load_dwordx4 v50, s[8:11], s64 offen offset:1024 sc1 lds\n" \
"v_mfma_f32_16x16x32_bf16 v[124:127], a[156:159], a[60:63], v[44:47]\n" \
"ds_read_b128 a[176:179], v49 offset:4096\n" \
"ds_read_b128 a[172:175], v49 offset:3072\n" \
"v_mfma_f32_16x16x32_bf16 v[124:127], a[152:155], a[56:59], v[124:127]\n" \
"ds_read_b128 a[168:171], v49 offset:2048\n" \
"ds_read_b128 a[164:167], v49 offset:1024\n" \
"v_mfma_f32_16x16x32_bf16 v[124:127], a[148:151], a[52:55], v[124:127]\n" \
"ds_read_b128 a[160:163], v49 offset:0\n" \
"ds_read_b128 a[0:3], v49 offset:8192\n" \
"v_mfma_f32_16x16x32_bf16 v[124:127], a[144:147], a[48:51], v[124:127]\n" \
"buffer_load_dwordx4 v50, s[8:11], s64 offen offset:2048 sc1 lds\n" \
"v_mfma_f32_16x16x32_bf16 v[32:35], a[220:223], a[64:67], 0\n" \
"ds_read_b128 a[4:7], v49 offset:9216\n" \
"ds_read_b128 a[8:11], v49 offset:10240\n" \
"v_mfma_f32_16x16x32_bf16 v[36:39], a[220:223], a[80:83], 0\n" \
"ds_read_b128 a[12:15], v49 offset:11264\n" \
"ds_read_b128 a[16:19], v49 offset:12288\n" \
"v_mfma_f32_16x16x32_bf16 v[40:43], a[220:223], a[96:99], 0\n" \
"ds_read_b128 a[20:23], v49 offset:13312\n" \
"ds_read_b128 a[24:27], v49 offset:14336\n" \
"v_mfma_f32_16x16x32_bf16 v[44:47], a[220:223], a[112:115], 0\n" \
"buffer_load_dwordx4 v50, s[8:11], s64 offen offset:3072 sc1 lds\n" \
"v_mfma_f32_16x16x32_bf16 v[32:35], a[224:227], a[68:71], v[32:35]\n" \
"s_xor_b32 m0, s65, 0xa000\n" \
"ds_read_b128 a[28:31], v49 offset:15360\n" \
"v_mfma_f32_16x16x32_bf16 v[36:39], a[224:227], a[84:87], v[36:39]\n" \
"v_exp_f32_e32 v120, v120\n" \
"v_mfma_f32_16x16x32_bf16 v[40:43], a[224:227], a[100:103], v[40:43]\n" \
"buffer_load_dwordx4 v51, s[8:11], s64 offen offset:0 sc1 lds\n" \
"v_mfma_f32_16x16x32_bf16 v[44:47], a[224:227], a[116:119], v[44:47]\n" \
"v_exp_f32_e32 v121, v121\n" \
"v_mfma_f32_16x16x32_bf16 v[32:35], a[228:231], a[72:75], v[32:35]\n" \
"v_exp_f32_e32 v122, v122\n" \
"v_mfma_f32_16x16x32_bf16 v[36:39], a[228:231], a[88:91], v[36:39]\n" \
"v_exp_f32_e32 v123, v123\n" \
"v_mfma_f32_16x16x32_bf16 v[40:43], a[228:231], a[104:107], v[40:43]\n" \
"buffer_load_dwordx4 v51, s[8:11], s64 offen offset:1024 sc1 lds\n" \
"v_mfma_f32_16x16x32_bf16 v[44:47], a[228:231], a[120:123], v[44:47]\n" \
"v_cvt_pk_bf16_f32 v120, v120, v121\n" \
"v_cvt_pk_bf16_f32 v121, v122, v123\n" \
"v_mfma_f32_16x16x32_bf16 v[32:35], a[232:235], a[76:79], v[32:35]\n" \
"v_exp_f32_e32 v124, v124\n" \
"v_mfma_f32_16x16x32_bf16 v[36:39], a[232:235], a[92:95], v[36:39]\n" \
"v_exp_f32_e32 v125, v125\n" \
"v_mfma_f32_16x16x32_bf16 v[40:43], a[232:235], a[108:111], v[40:43]\n" \
"buffer_load_dwordx4 v51, s[8:11], s64 offen offset:2048 sc1 lds\n" \
"v_mfma_f32_16x16x32_bf16 v[44:47], a[232:235], a[124:127], v[44:47]\n" \
"v_exp_f32_e32 v126, v126\n" \
"v_smfmac_f32_16x16x64_bf16 a[196:199], a[200:203], v[84:91], v21\n" \
"v_cvt_pk_bf16_f32 v122, v124, v125\n" \
"s_waitcnt lgkmcnt(1)\n" \
"v_mfma_f32_16x16x32_bf16 v[220:223], a[188:191], v[84:87], v[220:223]\n" \
"buffer_load_dwordx4 v51, s[8:11], s64 offen offset:3072 sc1 lds\n" \
"v_mfma_f32_16x16x32_bf16 v[252:255], a[188:191], v[88:91], v[252:255]\n" \
"s_add_u32 m0, s93, 0x1000\n" \
"v_and_b32_e32 v120, v12, v120\n" \
"v_mfma_f32_16x16x32_bf16 v[216:219], a[184:187], v[84:87], v[216:219]\n" \
"v_exp_f32_e32 v127, v127\n" \
"v_mfma_f32_16x16x32_bf16 v[248:251], a[184:187], v[88:91], v[248:251]\n" \
"v_cvt_pk_bf16_f32 v123, v126, v127\n" \
"v_and_b32_e32 v121, v13, v121\n" \
"v_mfma_f32_16x16x32_bf16 v[212:215], a[180:183], v[84:87], v[212:215]\n" \
"buffer_load_dwordx4 v4, s[80:83], s92 offen offset:0 lds\n" \
"v_mfma_f32_16x16x32_bf16 v[244:247], a[180:183], v[88:91], v[244:247]\n" \
"v_and_b32_e32 v122, v14, v122\n" \
"v_and_b32_e32 v123, v15, v123\n" \
"v_mfma_f32_16x16x32_bf16 v[208:211], a[176:179], v[84:87], v[208:211]\n" \
"v_accvgpr_read_b32 v56, a196\n" \
"v_accvgpr_read_b32 v24, a197\n" \
"v_mfma_f32_16x16x32_bf16 v[240:243], a[176:179], v[88:91], v[240:243]\n" \
"ds_read_b128 a[128:131], v48 offset:0\n" \
"ds_read_b128 a[144:147], v48 offset:4096\n" \
"v_mfma_f32_16x16x32_bf16 v[204:207], a[172:175], v[84:87], v[204:207]\n" \
"buffer_load_dwordx4 v5, s[80:83], s92 offen offset:1024 lds\n" \
"v_mfma_f32_16x16x32_bf16 v[236:239], a[172:175], v[88:91], v[236:239]\n" \
"ds_read_b128 a[132:135], v48 offset:1024\n" \
"ds_read_b128 a[148:151], v48 offset:5120\n" \
"v_mfma_f32_16x16x32_bf16 v[200:203], a[168:171], v[84:87], v[200:203]\n" \
"ds_read_b128 a[136:139], v48 offset:2048\n" \
"ds_read_b128 a[152:155], v48 offset:6144\n" \
"v_mfma_f32_16x16x32_bf16 v[232:235], a[168:171], v[88:91], v[232:235]\n" \
"buffer_load_dwordx4 v6, s[80:83], s92 offen offset:2048 lds\n" \
"v_mfma_f32_16x16x32_bf16 v[196:199], a[164:167], v[84:87], v[196:199]\n" \
"ds_read_b128 a[140:143], v48 offset:3072\n" \
"ds_read_b128 a[156:159], v48 offset:7168\n" \
"v_mfma_f32_16x16x32_bf16 v[228:231], a[164:167], v[88:91], v[228:231]\n" \
"v_mul_f32_e32 v141, v54, v141\n" \
"v_mul_f32_e32 v142, v54, v142\n" \
"v_mfma_f32_16x16x32_bf16 v[192:195], a[160:163], v[84:87], v[192:195]\n" \
"v_mul_f32_e32 v143, v54, v143\n" \
"v_cvt_pk_bf16_f32 v145, v146, v147\n" \
"v_mfma_f32_16x16x32_bf16 v[224:227], a[160:163], v[88:91], v[224:227]\n" \
"buffer_load_dwordx4 v7, s[80:83], s92 offen offset:3072 lds\n" \
"v_mfma_f32_16x16x32_bf16 a[196:199], a[212:215], v[120:123], a[196:199]\n" \
"v_mul_f32_e32 v148, v54, v148\n" \
"v_mul_f32_e32 v149, v54, v149\n" \
"v_mfma_f32_16x16x32_bf16 v[224:227], a[0:3], v[120:123], v[224:227]\n" \
"v_mul_f32_e32 v150, v54, v150\n" \
"v_mul_f32_e32 v151, v54, v151\n" \
"v_mfma_f32_16x16x32_bf16 v[228:231], a[4:7], v[120:123], v[228:231]\n" \
"v_cvt_pk_bf16_f32 v153, v154, v155\n" \
"v_mul_f32_e32 v156, v54, v156\n" \
"v_mfma_f32_16x16x32_bf16 v[232:235], a[8:11], v[120:123], v[232:235]\n" \
"v_mul_f32_e32 v157, v54, v157\n" \
"v_mul_f32_e32 v158, v54, v158\n" \
"v_mfma_f32_16x16x32_bf16 v[236:239], a[12:15], v[120:123], v[236:239]\n" \
"v_mul_f32_e32 v159, v54, v159\n" \
"v_cvt_pk_bf16_f32 v161, v162, v163\n" \
"v_mfma_f32_16x16x32_bf16 v[240:243], a[16:19], v[120:123], v[240:243]\n" \
"v_mul_f32_e32 v164, v55, v164\n" \
"v_mul_f32_e32 v165, v55, v165\n" \
"v_mfma_f32_16x16x32_bf16 v[244:247], a[20:23], v[120:123], v[244:247]\n" \
"v_mul_f32_e32 v166, v55, v166\n" \
"v_mul_f32_e32 v167, v55, v167\n" \
"v_mfma_f32_16x16x32_bf16 v[248:251], a[24:27], v[120:123], v[248:251]\n" \
"v_cvt_pk_bf16_f32 v169, v170, v171\n" \
"s_waitcnt lgkmcnt(4)\n" \
"v_mfma_f32_16x16x32_bf16 v[252:255], a[28:31], v[120:123], v[252:255]\n" \
"v_mul_f32_e32 v172, v55, v172\n" \
"v_mul_f32_e32 v173, v55, v173\n" \
"v_mul_f32_e32 v174, v55, v174\n" \
"v_mul_f32_e32 v175, v55, v175\n" \
"v_cvt_pk_bf16_f32 v177, v178, v179\n" \
"v_mul_f32_e32 v180, v55, v180\n" \
"v_mul_f32_e32 v181, v55, v181\n" \
"v_mul_f32_e32 v182, v55, v182\n" \
"v_mul_f32_e32 v183, v55, v183\n" \
"v_cvt_pk_bf16_f32 v185, v186, v187\n" \
"v_mul_f32_e32 v188, v55, v188\n" \
"v_mul_f32_e32 v189, v55, v189\n" \
"v_mul_f32_e32 v190, v55, v190\n" \
"v_mul_f32_e32 v191, v55, v191\n" \
"v_cvt_pk_bf16_f32 v130, v132, v133\n" \
"v_cvt_pk_bf16_f32 v131, v134, v135\n" \
"buffer_store_dwordx4 v[128:131], v20, s[56:59], s68 offen offset:0 sc1\n" \
"v_cvt_pk_bf16_f32 v138, v140, v141\n" \
"v_cvt_pk_bf16_f32 v139, v142, v143\n" \
"buffer_store_dwordx4 v[136:139], v20, s[56:59], s68 offen offset:1024 sc1\n" \
"v_cvt_pk_bf16_f32 v146, v148, v149\n" \
"v_cvt_pk_bf16_f32 v147, v150, v151\n" \
"buffer_store_dwordx4 v[144:147], v20, s[56:59], s68 offen offset:2048 sc1\n" \
"v_cvt_pk_bf16_f32 v154, v156, v157\n" \
"v_cvt_pk_bf16_f32 v155, v158, v159\n" \
"buffer_store_dwordx4 v[152:155], v20, s[56:59], s68 offen offset:3072 sc1\n" \
"v_cvt_pk_bf16_f32 v162, v164, v165\n" \
"v_cvt_pk_bf16_f32 v163, v166, v167\n" \
"buffer_store_dwordx4 v[160:163], v20, s[56:59], s69 offen offset:0 sc1\n" \
"v_cvt_pk_bf16_f32 v170, v172, v173\n" \
"v_cvt_pk_bf16_f32 v171, v174, v175\n" \
"buffer_store_dwordx4 v[168:171], v20, s[56:59], s69 offen offset:1024 sc1\n" \
"v_cvt_pk_bf16_f32 v178, v180, v181\n" \
"v_cvt_pk_bf16_f32 v179, v182, v183\n" \
"buffer_store_dwordx4 v[176:179], v20, s[56:59], s69 offen offset:2048 sc1\n" \
"v_cvt_pk_bf16_f32 v186, v188, v189\n" \
"v_cvt_pk_bf16_f32 v187, v190, v191\n" \
"buffer_store_dwordx4 v[184:187], v20, s[56:59], s69 offen offset:3072 sc1\n" \
"s_add_u32 m0, s93, 0x2000\n" \
"v_mfma_f32_16x16x32_bf16 a[208:211], a[216:219], v[128:131], a[208:211]\n" \
"buffer_load_dwordx4 v8, s[80:83], s92 offen offset:0 lds\n" \
"v_mfma_f32_16x16x32_bf16 a[208:211], a[216:219], v[136:139], a[208:211]\n" \
"v_mfma_f32_32x32x16_bf16 v[128:143], a[216:219], a[216:219], 0\n" \
"v_mfma_f32_16x16x32_bf16 a[208:211], a[216:219], v[144:147], a[208:211]\n" \
"v_mfma_f32_16x16x32_bf16 a[208:211], a[216:219], v[152:155], a[208:211]\n" \
"v_mfma_f32_32x32x16_bf16 v[144:159], a[216:219], a[216:219], 0\n" \
"v_add_f32_e32 v56, v56, v24\n" \
"v_mfma_f32_16x16x32_bf16 a[208:211], a[216:219], v[160:163], a[208:211]\n" \
"v_max_f32_e32 v62, v62, v56\n" \
"v_accvgpr_read_b32 v57, a198\n" \
"s_waitcnt vmcnt(21) lgkmcnt(0)\n" \
"s_barrier\n" \
"v_mfma_f32_16x16x32_bf16 v[64:67], a[128:131], a[64:67], v[32:35]\n" \
"s_xor_b32 m0, s65, 0x18000\n" \
"v_accvgpr_read_b32 v25, a199\n" \
"v_mfma_f32_16x16x32_bf16 a[196:199], a[216:219], a[216:219], 0\n" \
"buffer_load_dwordx4 v52, s[8:11], s64 offen offset:0 sc1 lds\n" \
"v_mfma_f32_16x16x32_bf16 v[64:67], a[132:135], a[68:71], v[64:67]\n" \
"v_rcp_f32_e32 v56, v56\n" \
"v_mfma_f32_16x16x32_bf16 v[64:67], a[136:139], a[72:75], v[64:67]\n" \
"v_mfma_f32_16x16x32_bf16 a[208:211], a[216:219], v[168:171], a[208:211]\n" \
"v_mfma_f32_32x32x16_bf16 v[160:175], a[216:219], a[216:219], 0\n" \
"v_mul_f32_e32 v56, v56, v60\n" \
"v_mfma_f32_16x16x32_bf16 a[208:211], a[216:219], v[176:179], a[208:211]\n" \
"v_mul_f32_e32 v192, v56, v192\n" \
"v_mul_f32_e32 v193, v56, v193\n" \
"v_mfma_f32_16x16x32_bf16 v[64:67], a[140:143], a[76:79], v[64:67]\n" \
"buffer_load_dwordx4 v52, s[8:11], s64 offen offset:1024 sc1 lds\n" \
"v_mfma_f32_16x16x32_bf16 v[68:71], a[156:159], a[76:79], v[32:35]\n" \
"v_cvt_pk_bf16_f32 v192, v192, v193\n" \
"v_mul_f32_e32 v194, v56, v194\n" \
"v_mfma_f32_16x16x32_bf16 v[68:71], a[152:155], a[72:75], v[68:71]\n" \
"v_mul_f32_e32 v195, v56, v195\n" \
"v_cvt_pk_bf16_f32 v193, v194, v195\n" \
"v_mfma_f32_16x16x32_bf16 v[68:71], a[148:151], a[68:71], v[68:71]\n" \
"v_mfma_f32_16x16x32_bf16 a[208:211], a[216:219], v[184:187], a[208:211]\n" \
"v_mfma_f32_32x32x16_bf16 v[176:191], a[216:219], a[216:219], 0\n" \
"v_mul_f32_e32 v196, v56, v196\n" \
"v_mul_f32_e32 v197, v56, v197\n" \
"v_mul_f32_e32 v198, v56, v198\n" \
"v_mul_f32_e32 v199, v56, v199\n" \
"v_mul_f32_e32 v200, v56, v200\n" \
"v_mul_f32_e32 v201, v56, v201\n" \
"v_mfma_f32_16x16x32_bf16 v[68:71], a[144:147], a[64:67], v[68:71]\n" \
"buffer_load_dwordx4 v52, s[8:11], s64 offen offset:2048 sc1 lds\n" \
"v_mfma_f32_16x16x32_bf16 v[76:79], a[144:147], a[80:83], v[36:39]\n" \
"v_cvt_pk_bf16_f32 v194, v196, v197\n" \
"v_cvt_pk_bf16_f32 v195, v198, v199\n" \
"buffer_store_dwordx4 v[192:195], v20, s[56:59], s70 offen offset:0 sc1\n" \
"v_mfma_f32_16x16x32_bf16 v[76:79], a[148:151], a[84:87], v[76:79]\n" \
"v_cvt_pk_bf16_f32 v200, v200, v201\n" \
"v_mul_f32_e32 v202, v56, v202\n" \
"v_mfma_f32_16x16x32_bf16 v[76:79], a[152:155], a[88:91], v[76:79]\n" \
"v_mul_f32_e32 v203, v56, v203\n" \
"v_add_f32_e32 v57, v57, v25\n" \
"v_mfma_f32_16x16x32_bf16 v[76:79], a[156:159], a[92:95], v[76:79]\n" \
"buffer_load_dwordx4 v52, s[8:11], s64 offen offset:3072 sc1 lds\n" \
"v_mfma_f32_16x16x32_bf16 v[72:75], a[140:143], a[92:95], v[36:39]\n" \
"s_mov_b32 m0, s65\n" \
"v_cvt_pk_bf16_f32 v201, v202, v203\n" \
"v_mfma_f32_16x16x32_bf16 v[72:75], a[136:139], a[88:91], v[72:75]\n" \
"v_mfma_f32_16x16x32_bf16 a[208:211], a[216:219], v[192:195], a[208:211]\n" \
"v_mul_f32_e32 v204, v56, v204\n" \
"v_mul_f32_e32 v205, v56, v205\n" \
"v_mfma_f32_16x16x32_bf16 v[72:75], a[132:135], a[84:87], v[72:75]\n" \
"v_mul_f32_e32 v206, v56, v206\n" \
"v_mul_f32_e32 v207, v56, v207\n" \
"v_mfma_f32_16x16x32_bf16 v[72:75], a[128:131], a[80:83], v[72:75]\n" \
"buffer_load_dwordx4 v53, s[8:11], s64 offen offset:0 sc1 lds\n" \
"v_mfma_f32_16x16x32_bf16 v[80:83], a[128:131], a[96:99], v[40:43]\n" \
"v_mul_f32_e32 v208, v56, v208\n" \
"v_mul_f32_e32 v209, v56, v209\n" \
"v_mfma_f32_16x16x32_bf16 v[80:83], a[132:135], a[100:103], v[80:83]\n" \
"v_max_f32_e32 v62, v62, v57\n" \
"v_cvt_pk_bf16_f32 v202, v204, v205\n" \
"v_mfma_f32_16x16x32_bf16 v[80:83], a[136:139], a[104:107], v[80:83]\n" \
"buffer_load_dwordx4 v53, s[8:11], s64 offen offset:1024 sc1 lds\n" \
"v_mfma_f32_16x16x32_bf16 v[80:83], a[140:143], a[108:111], v[80:83]\n" \
"v_cvt_pk_bf16_f32 v203, v206, v207\n" \
"v_cvt_pk_bf16_f32 v208, v208, v209\n" \
"buffer_store_dwordx4 v[200:203], v20, s[56:59], s70 offen offset:1024 sc1\n" \
"v_mfma_f32_16x16x32_bf16 v[84:87], a[156:159], a[108:111], v[40:43]\n" \
"v_mul_f32_e32 v210, v56, v210\n" \
"v_mul_f32_e32 v211, v56, v211\n" \
"v_mfma_f32_16x16x32_bf16 v[84:87], a[152:155], a[104:107], v[84:87]\n" \
"v_rcp_f32_e32 v57, v57\n" \
"v_mfma_f32_16x16x32_bf16 v[84:87], a[148:151], a[100:103], v[84:87]\n" \
"buffer_load_dwordx4 v53, s[8:11], s64 offen offset:2048 sc1 lds\n" \
"v_mfma_f32_16x16x32_bf16 v[84:87], a[144:147], a[96:99], v[84:87]\n" \
"v_mfma_f32_16x16x32_bf16 a[208:211], a[216:219], v[200:203], a[208:211]\n" \
"v_mfma_f32_32x32x16_bf16 v[192:207], a[216:219], a[216:219], 0\n" \
"v_cvt_pk_bf16_f32 v209, v210, v211\n" \
"v_mul_f32_e32 v212, v56, v212\n" \
".fill (L_ab + 20480 - .) / 4, 4, 0xBF800000\n" \
"L_ap5:\n" \
"s_cbranch_execz L_ap0\n" \
"v_mul_f32_e32 v213, v56, v213\n" \
"v_mul_f32_e32 v214, v56, v214\n" \
"v_mul_f32_e32 v215, v56, v215\n" \
"v_mul_f32_e32 v216, v56, v216\n" \
"v_mfma_f32_16x16x32_bf16 v[92:95], a[144:147], a[112:115], v[44:47]\n" \
"ds_read_b128 a[144:147], v48 offset:12288\n" \
"v_mul_f32_e32 v217, v56, v217\n" \
"v_mfma_f32_16x16x32_bf16 v[92:95], a[148:151], a[116:119], v[92:95]\n" \
"ds_read_b128 a[148:151], v48 offset:13312\n" \
"v_cvt_pk_bf16_f32 v210, v212, v213\n" \
"v_mfma_f32_16x16x32_bf16 v[92:95], a[152:155], a[120:123], v[92:95]\n" \
"ds_read_b128 a[152:155], v48 offset:14336\n" \
"v_cvt_pk_bf16_f32 v211, v214, v215\n" \
"buffer_store_dwordx4 v[208:211], v20, s[56:59], s70 offen offset:2048 sc1\n" \
"v_mfma_f32_16x16x32_bf16 v[92:95], a[156:159], a[124:127], v[92:95]\n" \
"ds_read_b128 a[156:159], v48 offset:15360\n" \
"v_cvt_pk_bf16_f32 v216, v216, v217\n" \
"v_mfma_f32_16x16x32_bf16 v[88:91], a[140:143], a[124:127], v[44:47]\n" \
"ds_read_b128 a[140:143], v48 offset:11264\n" \
"v_mul_f32_e32 v218, v56, v218\n" \
"v_mfma_f32_16x16x32_bf16 v[88:91], a[136:139], a[120:123], v[88:91]\n" \
"ds_read_b128 a[136:139], v48 offset:10240\n" \
"v_mul_f32_e32 v219, v56, v219\n" \
"v_mfma_f32_16x16x32_bf16 v[88:91], a[132:135], a[116:119], v[88:91]\n" \
"ds_read_b128 a[132:135], v48 offset:9216\n" \
"v_mul_f32_e32 v57, v57, v61\n" \
"v_mfma_f32_16x16x32_bf16 v[88:91], a[128:131], a[112:115], v[88:91]\n" \
"ds_read_b128 a[128:131], v48 offset:8192\n" \
"v_mfma_f32_16x16x32_bf16 a[208:211], a[216:219], v[208:211], a[208:211]\n" \
"v_cvt_pk_bf16_f32 v217, v218, v219\n" \
"v_mul_f32_e32 v220, v56, v220\n" \
"v_mul_f32_e32 v221, v56, v221\n" \
"v_mul_f32_e32 v222, v56, v222\n" \
"v_mul_f32_e32 v223, v56, v223\n" \
"v_mul_f32_e32 v224, v57, v224\n" \
"v_mul_f32_e32 v225, v57, v225\n" \
"v_cvt_pk_bf16_f32 v218, v220, v221\n" \
"v_cvt_pk_bf16_f32 v219, v222, v223\n" \
"buffer_store_dwordx4 v[216:219], v20, s[56:59], s70 offen offset:3072 sc1\n" \
"v_cvt_pk_bf16_f32 v224, v224, v225\n" \
"v_mul_f32_e32 v226, v57, v226\n" \
"v_mul_f32_e32 v227, v57, v227\n" \
"v_mfma_f32_16x16x32_bf16 a[208:211], a[216:219], v[216:219], a[208:211]\n" \
"v_mfma_f32_32x32x16_bf16 v[208:223], a[216:219], a[216:219], 0\n" \
"v_cvt_pk_bf16_f32 v225, v226, v227\n" \
"v_mul_f32_e32 v228, v57, v228\n" \
"v_mul_f32_e32 v229, v57, v229\n" \
"v_mul_f32_e32 v230, v57, v230\n" \
"v_mul_f32_e32 v231, v57, v231\n" \
"v_mul_f32_e32 v232, v57, v232\n" \
"v_mul_f32_e32 v233, v57, v233\n" \
"v_cvt_pk_bf16_f32 v226, v228, v229\n" \
"v_cvt_pk_bf16_f32 v227, v230, v231\n" \
"buffer_store_dwordx4 v[224:227], v20, s[56:59], s71 offen offset:0 sc1\n" \
"v_cvt_pk_bf16_f32 v232, v232, v233\n" \
"v_mul_f32_e32 v234, v57, v234\n" \
"v_mul_f32_e32 v235, v57, v235\n" \
"v_mfma_f32_16x16x32_bf16 a[208:211], a[216:219], v[224:227], a[208:211]\n" \
"v_cvt_pk_bf16_f32 v233, v234, v235\n" \
"v_mul_f32_e32 v236, v57, v236\n" \
"v_mul_f32_e32 v237, v57, v237\n" \
"v_mul_f32_e32 v238, v57, v238\n" \
"v_mul_f32_e32 v239, v57, v239\n" \
"v_mul_f32_e32 v240, v57, v240\n" \
"v_mul_f32_e32 v241, v57, v241\n" \
"v_cvt_pk_bf16_f32 v234, v236, v237\n" \
"v_cvt_pk_bf16_f32 v235, v238, v239\n" \
"buffer_store_dwordx4 v[232:235], v20, s[56:59], s71 offen offset:1024 sc1\n" \
"v_cvt_pk_bf16_f32 v240, v240, v241\n" \
"v_mul_f32_e32 v242, v57, v242\n" \
"v_mul_f32_e32 v243, v57, v243\n" \
"v_mfma_f32_16x16x32_bf16 a[208:211], a[216:219], v[232:235], a[208:211]\n" \
"v_mfma_f32_32x32x16_bf16 v[224:239], a[216:219], a[216:219], 0\n" \
"v_cvt_pk_bf16_f32 v241, v242, v243\n" \
"v_mul_f32_e32 v244, v57, v244\n" \
"v_mul_f32_e32 v245, v57, v245\n" \
"v_mul_f32_e32 v246, v57, v246\n" \
"v_mul_f32_e32 v247, v57, v247\n" \
"v_mul_f32_e32 v248, v57, v248\n" \
"v_mul_f32_e32 v249, v57, v249\n" \
"v_cvt_pk_bf16_f32 v242, v244, v245\n" \
"v_cvt_pk_bf16_f32 v243, v246, v247\n" \
"buffer_store_dwordx4 v[240:243], v20, s[56:59], s71 offen offset:2048 sc1\n" \
"v_cvt_pk_bf16_f32 v248, v248, v249\n" \
"v_mul_f32_e32 v250, v57, v250\n" \
"v_mul_f32_e32 v251, v57, v251\n" \
"v_mfma_f32_16x16x32_bf16 a[208:211], a[216:219], v[240:243], a[208:211]\n" \
"v_cvt_pk_bf16_f32 v249, v250, v251\n" \
"v_mul_f32_e32 v252, v57, v252\n" \
"v_mul_f32_e32 v253, v57, v253\n" \
"v_mul_f32_e32 v254, v57, v254\n" \
"v_mul_f32_e32 v255, v57, v255\n" \
"v_cvt_pk_bf16_f32 v250, v252, v253\n" \
"v_cvt_pk_bf16_f32 v251, v254, v255\n" \
"buffer_store_dwordx4 v[248:251], v20, s[56:59], s71 offen offset:3072 sc1\n" \
"buffer_load_dwordx4 v53, s[8:11], s64 offen offset:3072 sc1 lds\n" \
"s_waitcnt vmcnt(16) lgkmcnt(0)\n" \
"s_barrier\n" \
"v_mfma_f32_16x16x32_bf16 a[208:211], a[216:219], v[248:251], a[208:211]\n" \
"v_mfma_f32_32x32x16_bf16 v[240:255], a[216:219], a[216:219], 0\n" \
"v_exp_f32_e32 v70, v70\n" \
"v_exp_f32_e32 v71, v71\n" \
"v_exp_f32_e32 v72, v72\n" \
"v_exp_f32_e32 v73, v73\n" \
"s_add_u32 s64, s64, 0x10000\n" \
"v_cmp_lt_f32_e64 s[72:73], s21, v62\n" \
"v_readlane_b32 s95, v11, 3\n" \
"v_accvgpr_read_b32 v63, a208\n" \
"v_cmp_u_f32_e32 vcc, v63, v63\n" \
"v_exp_f32_e32 v68, v68\n" \
"v_exp_f32_e32 v69, v69\n" \
"v_exp_f32_e32 v74, v74\n" \
"v_exp_f32_e32 v75, v75\n" \
"s_or_b64 vcc, s[72:73], vcc\n" \
"s_cselect_b32 s95, 0x40000000, s95\n" \
"s_cselect_b32 s94, 0x7f800000, 0\n" \
"v_cvt_pk_bf16_f32 v71, v70, v71\n" \
"v_exp_f32_e32 v66, v66\n" \
"v_exp_f32_e32 v67, v67\n" \
"v_exp_f32_e32 v64, v64\n" \
"v_exp_f32_e32 v65, v65\n" \
"v_cvt_pk_bf16_f32 v72, v72, v73\n" \
"v_exp_f32_e32 v76, v76\n" \
"v_exp_f32_e32 v77, v77\n" \
"v_exp_f32_e32 v78, v78\n" \
"v_exp_f32_e32 v79, v79\n" \
"v_cvt_pk_bf16_f32 v70, v68, v69\n" \
"v_cvt_pk_bf16_f32 v73, v74, v75\n" \
"v_add_u32_e32 v49, 0x10000, v48\n" \
"s_xor_b32 s65, s65, 0x8000\n" \
"v_cvt_pk_bf16_f32 v69, v66, v67\n" \
"v_cvt_pk_bf16_f32 v68, v64, v65\n" \
"v_cvt_pk_bf16_f32 v74, v76, v77\n" \
"v_cvt_pk_bf16_f32 v75, v78, v79\n" \
"v_xor_b32_e32 v48, 0x8000, v48\n" \
"s_sub_u32 s66, s19, 2\n" \
"s_branch L_2_%=\n" \
"L_f1_%=:\n" \
"ds_read_b128 a[160:163], v49 offset:0\n" \
"ds_read_b128 a[164:167], v49 offset:1024\n" \
"ds_read_b128 a[168:171], v49 offset:2048\n" \
"v_mfma_f32_16x16x32_bf16 v[104:107], a[128:131], a[80:83], v[36:39]\n" \
"v_cvt_pk_bf16_f32 v192, v192, v193\n" \
"v_cvt_pk_bf16_f32 v224, v224, v225\n" \
"v_cvt_pk_bf16_f32 v193, v194, v195\n" \
"v_cvt_pk_bf16_f32 v225, v226, v227\n" \
"ds_read_b128 a[172:175], v49 offset:3072\n" \
"v_cvt_pk_bf16_f32 v194, v196, v197\n" \
"v_cvt_pk_bf16_f32 v195, v198, v199\n" \
"v_cvt_pk_bf16_f32 v226, v228, v229\n" \
"v_cvt_pk_bf16_f32 v227, v230, v231\n" \
"v_mfma_f32_16x16x32_bf16 v[104:107], a[132:135], a[84:87], v[104:107]\n" \
"v_lshl_add_u32 v50, s15, 2, v20\n" \
"v_cvt_pk_bf16_f32 v196, v200, v201\n" \
"v_cvt_pk_bf16_f32 v197, v202, v203\n" \
"v_cvt_pk_bf16_f32 v198, v204, v205\n" \
"v_cvt_pk_bf16_f32 v199, v206, v207\n" \
"v_cvt_pk_bf16_f32 v228, v232, v233\n" \
"v_cvt_pk_bf16_f32 v229, v234, v235\n" \
"v_cvt_pk_bf16_f32 v230, v236, v237\n" \
"v_cvt_pk_bf16_f32 v231, v238, v239\n" \
"v_add_u32_e32 v50, 0x1a000, v50\n" \
"v_mov_b32_e32 v61, s62\n" \
"v_cvt_pk_bf16_f32 v200, v208, v209\n" \
"v_cvt_pk_bf16_f32 v201, v210, v211\n" \
"v_cvt_pk_bf16_f32 v202, v212, v213\n" \
"v_cvt_pk_bf16_f32 v203, v214, v215\n" \
"v_cvt_pk_bf16_f32 v204, v216, v217\n" \
"v_cvt_pk_bf16_f32 v205, v218, v219\n" \
"v_cvt_pk_bf16_f32 v206, v220, v221\n" \
"v_cvt_pk_bf16_f32 v207, v222, v223\n" \
"v_cvt_pk_bf16_f32 v232, v240, v241\n" \
"v_cvt_pk_bf16_f32 v233, v242, v243\n" \
"v_cvt_pk_bf16_f32 v234, v244, v245\n" \
"v_cvt_pk_bf16_f32 v235, v246, v247\n" \
"v_cvt_pk_bf16_f32 v236, v248, v249\n" \
"v_cvt_pk_bf16_f32 v237, v250, v251\n" \
"v_cvt_pk_bf16_f32 v238, v252, v253\n" \
"v_cvt_pk_bf16_f32 v239, v254, v255\n" \
"ds_write_b128 v50, v[192:195] offset:0\n" \
"v_mfma_f32_16x16x32_bf16 v[104:107], a[136:139], a[88:91], v[104:107]\n" \
"ds_read_b128 a[176:179], v49 offset:4096\n" \
"v_add_u32_e32 v56, 0x49000, v20\n" \
"v_mfma_f32_16x16x32_bf16 v[104:107], a[140:143], a[92:95], v[104:107]\n" \
"ds_write_b128 v50, v[196:199] offset:1024\n" \
"v_mfma_f32_16x16x32_bf16 v[108:111], a[156:159], a[92:95], v[36:39]\n" \
"ds_read_b128 a[180:183], v49 offset:5120\n" \
"ds_read_b128 a[184:187], v49 offset:6144\n" \
"v_mfma_f32_16x16x32_bf16 v[108:111], a[152:155], a[88:91], v[108:111]\n" \
"ds_write_b128 v50, v[200:203] offset:2048\n" \
"v_mfma_f32_16x16x32_bf16 v[108:111], a[148:151], a[84:87], v[108:111]\n" \
"v_and_b32_e32 v68, v12, v68\n" \
"v_and_b32_e32 v69, v13, v69\n" \
"v_mfma_f32_16x16x32_bf16 v[108:111], a[144:147], a[80:83], v[108:111]\n" \
"ds_write_b128 v50, v[204:207] offset:3072\n" \
"v_and_b32_e32 v70, v14, v70\n" \
"v_and_b32_e32 v71, v15, v71\n" \
"ds_read_b128 a[188:191], v49 offset:7168\n" \
"ds_read_b128 a[0:3], v20 offset:8192\n" \
"s_waitcnt lgkmcnt(9)\n" \
"v_mfma_f32_16x16x32_bf16 v[128:131], a[160:163], v[68:71], v[128:131]\n" \
"ds_write_b128 v50, v[224:227] offset:4096\n" \
"v_mfma_f32_16x16x32_bf16 v[160:163], a[160:163], v[72:75], v[160:163]\n" \
"ds_read_b128 a[4:7], v20 offset:9216\n" \
"ds_read_b128 a[8:11], v20 offset:10240\n" \
"v_mfma_f32_16x16x32_bf16 v[132:135], a[164:167], v[68:71], v[132:135]\n" \
"ds_write_b128 v50, v[228:231] offset:5120\n" \
"v_mfma_f32_16x16x32_bf16 v[164:167], a[164:167], v[72:75], v[164:167]\n" \
"ds_read_b128 a[12:15], v20 offset:11264\n" \
"ds_read_b128 a[28:31], v20 offset:15360\n" \
"v_mfma_f32_16x16x32_bf16 v[136:139], a[168:171], v[68:71], v[136:139]\n" \
"ds_write_b128 v50, v[232:235] offset:6144\n" \
"v_mfma_f32_16x16x32_bf16 v[168:171], a[168:171], v[72:75], v[168:171]\n" \
"ds_read_b128 a[24:27], v20 offset:14336\n" \
"ds_read_b128 a[20:23], v20 offset:13312\n" \
"v_mfma_f32_16x16x32_bf16 v[140:143], a[172:175], v[68:71], v[140:143]\n" \
"ds_write_b128 v50, v[236:239] offset:7168\n" \
"v_mfma_f32_16x16x32_bf16 v[172:175], a[172:175], v[72:75], v[172:175]\n" \
"ds_read_b128 a[16:19], v20 offset:12288\n" \
"s_waitcnt lgkmcnt(15)\n" \
"v_mfma_f32_16x16x32_bf16 v[144:147], a[176:179], v[68:71], v[144:147]\n" \
"v_xor_b32_e32 v48, 0x4000, v49\n" \
"ds_read_b32 v58, v18 offset:256\n" \
"v_mfma_f32_16x16x32_bf16 v[176:179], a[176:179], v[72:75], v[176:179]\n" \
"ds_read_b32 v59, v18 offset:384\n" \
"ds_read_b128 a[32:35], v48 offset:8192\n" \
"v_mfma_f32_16x16x32_bf16 v[148:151], a[180:183], v[68:71], v[148:151]\n" \
"ds_read_b128 a[36:39], v48 offset:9216\n" \
"ds_read_b128 a[40:43], v48 offset:10240\n" \
"v_mfma_f32_16x16x32_bf16 v[180:183], a[180:183], v[72:75], v[180:183]\n" \
"ds_read_b128 a[44:47], v48 offset:11264\n" \
"ds_read_b128 a[48:51], v48 offset:12288\n" \
"v_mfma_f32_16x16x32_bf16 v[152:155], a[184:187], v[68:71], v[152:155]\n" \
"ds_read_b128 a[52:55], v48 offset:13312\n" \
"ds_read_b128 a[56:59], v48 offset:14336\n" \
"v_mfma_f32_16x16x32_bf16 v[184:187], a[184:187], v[72:75], v[184:187]\n" \
"ds_read_b128 a[60:63], v48 offset:15360\n" \
"s_waitcnt lgkmcnt(15)\n" \
"v_mfma_f32_16x16x32_bf16 v[156:159], a[188:191], v[68:71], v[156:159]\n" \
"ds_read_b128 a[128:131], v49 offset:8192\n" \
"ds_read_b128 a[132:135], v49 offset:9216\n" \
"v_mfma_f32_16x16x32_bf16 v[188:191], a[188:191], v[72:75], v[188:191]\n" \
"ds_read_b128 a[136:139], v49 offset:10240\n" \
"ds_read_b128 a[140:143], v49 offset:11264\n" \
"v_smfmac_f32_16x16x64_bf16 a[192:195], a[200:203], v[68:75], v21\n" \
"ds_read_b128 a[144:147], v49 offset:12288\n" \
"ds_read_b128 a[148:151], v49 offset:13312\n" \
"v_mfma_f32_16x16x32_bf16 v[112:115], a[0:3], a[64:67], v[32:35]\n" \
"ds_read_b128 a[152:155], v49 offset:14336\n" \
"ds_read_b128 a[156:159], v49 offset:15360\n" \
"v_mfma_f32_16x16x32_bf16 v[112:115], a[4:7], a[68:71], v[112:115]\n" \
"v_accvgpr_read_b32 v40, a196\n" \
"v_accvgpr_read_b32 v24, a197\n" \
"v_mfma_f32_16x16x32_bf16 v[112:115], a[8:11], a[72:75], v[112:115]\n" \
"v_accvgpr_read_b32 v41, a198\n" \
"v_accvgpr_read_b32 v25, a199\n" \
"v_mfma_f32_16x16x32_bf16 v[112:115], a[12:15], a[76:79], v[112:115]\n" \
"v_add_f32_e32 v40, v40, v24\n" \
"v_add_f32_e32 v41, v41, v25\n" \
"v_mfma_f32_16x16x32_bf16 v[116:119], a[28:31], a[76:79], v[32:35]\n" \
"v_add_f32_e32 v40, s94, v40\n" \
"s_waitcnt lgkmcnt(15)\n" \
"v_mfma_f32_16x16x32_bf16 v[116:119], a[24:27], a[72:75], v[116:119]\n" \
"v_add_f32_e32 v41, s94, v41\n" \
"ds_write_b64 v19, v[40:41]\n" \
"v_mfma_f32_16x16x32_bf16 v[116:119], a[20:23], a[68:71], v[116:119]\n" \
"v_add_u32_e32 v52, s15, v20\n" \
"v_add_u32_e32 v51, 0x10000, v52\n" \
"v_mfma_f32_16x16x32_bf16 v[116:119], a[16:19], a[64:67], v[116:119]\n" \
"v_exp_f32_e32 v104, v104\n" \
"v_mfma_f32_16x16x32_bf16 v[124:127], a[16:19], a[80:83], v[36:39]\n" \
"v_exp_f32_e32 v105, v105\n" \
"v_mfma_f32_16x16x32_bf16 v[124:127], a[20:23], a[84:87], v[124:127]\n" \
"v_exp_f32_e32 v106, v106\n" \
"v_mfma_f32_16x16x32_bf16 v[124:127], a[24:27], a[88:91], v[124:127]\n" \
"v_exp_f32_e32 v107, v107\n" \
"v_mfma_f32_16x16x32_bf16 v[124:127], a[28:31], a[92:95], v[124:127]\n" \
"v_cvt_pk_bf16_f32 v104, v104, v105\n" \
"v_cvt_pk_bf16_f32 v105, v106, v107\n" \
"v_mfma_f32_16x16x32_bf16 v[120:123], a[12:15], a[92:95], v[36:39]\n" \
"v_exp_f32_e32 v108, v108\n" \
"v_mfma_f32_16x16x32_bf16 v[120:123], a[8:11], a[88:91], v[120:123]\n" \
"v_exp_f32_e32 v109, v109\n" \
"v_mfma_f32_16x16x32_bf16 v[120:123], a[4:7], a[84:87], v[120:123]\n" \
"v_exp_f32_e32 v110, v110\n" \
"v_mfma_f32_16x16x32_bf16 v[120:123], a[0:3], a[80:83], v[120:123]\n" \
"v_exp_f32_e32 v111, v111\n" \
"s_waitcnt lgkmcnt(0)\n" \
"s_barrier\n" \
"ds_read_b128 v[208:211], v52 offset:0\n" \
"ds_read_b128 v[212:215], v52 offset:1024\n" \
"ds_read_b128 v[216:219], v52 offset:2048\n" \
"ds_read_b128 v[220:223], v52 offset:3072\n" \
"ds_read_b128 v[240:243], v51 offset:32768\n" \
"ds_read_b128 v[244:247], v51 offset:33792\n" \
"ds_read_b128 v[248:251], v51 offset:34816\n" \
"ds_read_b128 v[252:255], v51 offset:35840\n" \
"v_cvt_pk_bf16_f32 v106, v108, v109\n" \
"v_cvt_pk_bf16_f32 v107, v110, v111\n" \
"v_and_b32_e32 v104, v12, v104\n" \
"v_and_b32_e32 v105, v13, v105\n" \
"v_and_b32_e32 v106, v14, v106\n" \
"s_waitcnt vmcnt(0)\n" \
"s_mov_b64 exec, 1\n" \
"buffer_store_dword v61, off, s[56:59], s95 offset:0 sc1\n" \
"s_mov_b64 exec, -1\n" \
"v_and_b32_e32 v107, v15, v107\n" \
"v_xor_b32_e32 v63, 0x400, v19\n" \
"ds_read_b64 v[42:43], v63\n" \
"v_mfma_f32_16x16x32_bf16 a[192:195], a[212:215], v[104:107], a[192:195]\n" \
"v_mov_b32_e32 v194, v28\n" \
"v_mov_b32_e32 v195, v29\n" \
"v_mfma_f32_16x16x32_bf16 v[160:163], a[128:131], v[104:107], v[160:163]\n" \
"v_mov_b32_e32 v196, 0\n" \
"v_mov_b32_e32 v197, 0\n" \
"v_mfma_f32_16x16x32_bf16 v[164:167], a[132:135], v[104:107], v[164:167]\n" \
"v_mov_b32_e32 v192, 0\n" \
"v_mov_b32_e32 v193, 0\n" \
"v_mfma_f32_16x16x32_bf16 v[168:171], a[136:139], v[104:107], v[168:171]\n" \
"v_readlane_b32 s93, v11, 22\n" \
"v_readlane_b32 s92, v11, 27\n" \
"v_mfma_f32_16x16x32_bf16 v[172:175], a[140:143], v[104:107], v[172:175]\n" \
"buffer_load_dwordx4 a[96:99], v20, s[56:59], s60 offen offset:0 sc1\n" \
"v_mfma_f32_16x16x32_bf16 v[176:179], a[144:147], v[104:107], v[176:179]\n" \
"v_exp_f32_e32 v118, v118\n" \
"v_mfma_f32_16x16x32_bf16 v[180:183], a[148:151], v[104:107], v[180:183]\n" \
"v_exp_f32_e32 v119, v119\n" \
"v_mfma_f32_16x16x32_bf16 v[184:187], a[152:155], v[104:107], v[184:187]\n" \
"s_add_u32 m0, s93, 0x0\n" \
"v_cvt_pk_bf16_f32 v119, v118, v119\n" \
"v_mfma_f32_16x16x32_bf16 v[188:191], a[156:159], v[104:107], v[188:191]\n" \
"v_exp_f32_e32 v120, v120\n" \
"s_waitcnt lgkmcnt(1)\n" \
"s_barrier\n" \
"buffer_load_dwordx4 a[112:115], v56, s[56:59], s60 offen offset:0 sc1\n" \
"v_mfma_f32_16x16x32_bf16 v[128:131], v[194:197], v[208:211], v[128:131]\n" \
"v_exp_f32_e32 v121, v121\n" \
"v_mfma_f32_16x16x32_bf16 v[132:135], v[192:195], v[208:211], v[132:135]\n" \
"v_exp_f32_e32 v116, v116\n" \
"v_mfma_f32_16x16x32_bf16 v[136:139], v[194:197], v[212:215], v[136:139]\n" \
"v_exp_f32_e32 v117, v117\n" \
"v_mfma_f32_16x16x32_bf16 v[140:143], v[192:195], v[212:215], v[140:143]\n" \
"v_exp_f32_e32 v122, v122\n" \
"v_mfma_f32_16x16x32_bf16 v[144:147], v[194:197], v[216:219], v[144:147]\n" \
"v_exp_f32_e32 v123, v123\n" \
"v_mfma_f32_16x16x32_bf16 v[148:151], v[192:195], v[216:219], v[148:151]\n" \
"buffer_load_dwordx4 a[100:103], v20, s[56:59], s60 offen offset:1024 sc1\n" \
"v_mfma_f32_16x16x32_bf16 v[152:155], v[194:197], v[220:223], v[152:155]\n" \
"v_exp_f32_e32 v114, v114\n" \
"v_mfma_f32_16x16x32_bf16 v[156:159], v[192:195], v[220:223], v[156:159]\n" \
"v_exp_f32_e32 v115, v115\n" \
"v_mfma_f32_16x16x32_bf16 v[160:163], v[194:197], v[240:243], v[160:163]\n" \
"v_exp_f32_e32 v112, v112\n" \
"v_mfma_f32_16x16x32_bf16 v[164:167], v[192:195], v[240:243], v[164:167]\n" \
"v_exp_f32_e32 v113, v113\n" \
"v_mfma_f32_16x16x32_bf16 v[168:171], v[194:197], v[244:247], v[168:171]\n" \
"v_cvt_pk_bf16_f32 v120, v120, v121\n" \
"v_cvt_pk_bf16_f32 v118, v116, v117\n" \
"v_mfma_f32_16x16x32_bf16 v[172:175], v[192:195], v[244:247], v[172:175]\n" \
"buffer_load_dwordx4 a[116:119], v56, s[56:59], s60 offen offset:1024 sc1\n" \
"v_mfma_f32_16x16x32_bf16 v[176:179], v[194:197], v[248:251], v[176:179]\n" \
"v_exp_f32_e32 v124, v124\n" \
"v_mfma_f32_16x16x32_bf16 v[180:183], v[192:195], v[248:251], v[180:183]\n" \
"v_exp_f32_e32 v125, v125\n" \
"v_mfma_f32_16x16x32_bf16 v[184:187], v[194:197], v[252:255], v[184:187]\n" \
"v_exp_f32_e32 v126, v126\n" \
"v_mfma_f32_16x16x32_bf16 v[188:191], v[192:195], v[252:255], v[188:191]\n" \
"v_exp_f32_e32 v127, v127\n" \
"v_cvt_pk_bf16_f32 v121, v122, v123\n" \
"v_cvt_pk_bf16_f32 v117, v114, v115\n" \
"v_cvt_pk_bf16_f32 v116, v112, v113\n" \
"v_cvt_pk_bf16_f32 v122, v124, v125\n" \
"v_cvt_pk_bf16_f32 v123, v126, v127\n" \
"buffer_load_dwordx4 a[104:107], v20, s[56:59], s60 offen offset:2048 sc1\n" \
"s_nop 0\n" \
"v_smfmac_f32_16x16x64_bf16 a[192:195], a[200:203], v[116:123], v21\n" \
"v_mfma_f32_16x16x32_bf16 v[128:131], a[32:35], v[116:119], v[128:131]\n" \
"v_mfma_f32_16x16x32_bf16 v[160:163], a[32:35], v[120:123], v[160:163]\n" \
"v_mfma_f32_16x16x32_bf16 v[132:135], a[36:39], v[116:119], v[132:135]\n" \
"v_mfma_f32_16x16x32_bf16 v[164:167], a[36:39], v[120:123], v[164:167]\n" \
"v_mfma_f32_16x16x32_bf16 v[136:139], a[40:43], v[116:119], v[136:139]\n" \
"buffer_load_dwordx4 a[120:123], v56, s[56:59], s60 offen offset:2048 sc1\n" \
"v_mfma_f32_16x16x32_bf16 v[168:171], a[40:43], v[120:123], v[168:171]\n" \
"v_mfma_f32_16x16x32_bf16 v[140:143], a[44:47], v[116:119], v[140:143]\n" \
"v_accvgpr_read_b32 v54, a192\n" \
"v_accvgpr_read_b32 v22, a193\n" \
"v_mfma_f32_16x16x32_bf16 v[172:175], a[44:47], v[120:123], v[172:175]\n" \
"v_add_f32_e32 v54, v54, v22\n" \
"s_waitcnt lgkmcnt(0)\n" \
"v_add_f32_e32 v54, v54, v42\n" \
"v_mfma_f32_16x16x32_bf16 v[144:147], a[48:51], v[116:119], v[144:147]\n" \
"v_max_f32_e32 v62, v62, v54\n" \
"v_accvgpr_read_b32 v55, a194\n" \
"v_mfma_f32_16x16x32_bf16 v[176:179], a[48:51], v[120:123], v[176:179]\n" \
"v_rcp_f32_e32 v54, v54\n" \
"buffer_load_dwordx4 a[108:111], v20, s[56:59], s60 offen offset:3072 sc1\n" \
"v_mfma_f32_16x16x32_bf16 v[148:151], a[52:55], v[116:119], v[148:151]\n" \
"v_mul_f32_e32 v54, v54, v58\n" \
"v_mul_f32_e32 v128, v54, v128\n" \
"v_mfma_f32_16x16x32_bf16 v[180:183], a[52:55], v[120:123], v[180:183]\n" \
"v_mul_f32_e32 v129, v54, v129\n" \
"v_mul_f32_e32 v130, v54, v130\n" \
"v_mul_f32_e32 v131, v54, v131\n" \
"v_mul_f32_e32 v132, v54, v132\n" \
"v_mul_f32_e32 v133, v54, v133\n" \
"v_mul_f32_e32 v134, v54, v134\n" \
"v_mul_f32_e32 v135, v54, v135\n" \
"v_cvt_pk_bf16_f32 v64, v128, v129\n" \
"v_cvt_pk_bf16_f32 v65, v130, v131\n" \
"v_cvt_pk_bf16_f32 v66, v132, v133\n" \
"v_cvt_pk_bf16_f32 v67, v134, v135\n" \
"v_accvgpr_read_b32 v23, a195\n" \
"v_mfma_f32_16x16x32_bf16 v[152:155], a[56:59], v[116:119], v[152:155]\n" \
"v_mfma_f32_16x16x32_bf16 a[208:211], a[216:219], v[64:67], a[208:211]\n" \
"buffer_load_dwordx4 a[124:127], v56, s[56:59], s60 offen offset:3072 sc1\n" \
"v_mul_f32_e32 v136, v54, v136\n" \
"v_mul_f32_e32 v137, v54, v137\n" \
"v_mul_f32_e32 v138, v54, v138\n" \
"v_mul_f32_e32 v139, v54, v139\n" \
"v_mul_f32_e32 v140, v54, v140\n" \
"v_mul_f32_e32 v141, v54, v141\n" \
"v_mul_f32_e32 v142, v54, v142\n" \
"v_mul_f32_e32 v143, v54, v143\n" \
"v_add_f32_e32 v55, v55, v23\n" \
"v_cvt_pk_bf16_f32 v68, v136, v137\n" \
"v_cvt_pk_bf16_f32 v69, v138, v139\n" \
"v_cvt_pk_bf16_f32 v70, v140, v141\n" \
"v_cvt_pk_bf16_f32 v71, v142, v143\n" \
"v_add_f32_e32 v55, v55, v43\n" \
"v_mul_f32_e32 v144, v54, v144\n" \
"v_mfma_f32_16x16x32_bf16 a[208:211], a[216:219], v[68:71], a[208:211]\n" \
"v_mul_f32_e32 v145, v54, v145\n" \
"v_mul_f32_e32 v146, v54, v146\n" \
"v_mul_f32_e32 v147, v54, v147\n" \
"v_mul_f32_e32 v148, v54, v148\n" \
"buffer_load_dwordx4 v0, s[80:83], s92 offen offset:0 lds\n" \
"v_mul_f32_e32 v149, v54, v149\n" \
"v_mul_f32_e32 v150, v54, v150\n" \
"v_mul_f32_e32 v151, v54, v151\n" \
"v_max_f32_e32 v62, v62, v55\n" \
"v_cvt_pk_bf16_f32 v72, v144, v145\n" \
"v_cvt_pk_bf16_f32 v73, v146, v147\n" \
"v_cvt_pk_bf16_f32 v74, v148, v149\n" \
"v_cvt_pk_bf16_f32 v75, v150, v151\n" \
"v_rcp_f32_e32 v55, v55\n" \
"v_mfma_f32_16x16x32_bf16 v[184:187], a[56:59], v[120:123], v[184:187]\n" \
"v_mfma_f32_16x16x32_bf16 a[208:211], a[216:219], v[72:75], a[208:211]\n" \
"v_mul_f32_e32 v152, v54, v152\n" \
"v_mul_f32_e32 v153, v54, v153\n" \
"v_mul_f32_e32 v154, v54, v154\n" \
"v_mul_f32_e32 v155, v54, v155\n" \
"v_cvt_pk_bf16_f32 v76, v152, v153\n" \
"buffer_load_dwordx4 v1, s[80:83], s92 offen offset:1024 lds\n" \
"v_cvt_pk_bf16_f32 v77, v154, v155\n" \
"v_mul_f32_e32 v55, v55, v59\n" \
"v_mul_f32_e32 v160, v55, v160\n" \
"v_mul_f32_e32 v161, v55, v161\n" \
"v_mul_f32_e32 v162, v55, v162\n" \
"v_mul_f32_e32 v163, v55, v163\n" \
"v_mul_f32_e32 v164, v55, v164\n" \
"v_mul_f32_e32 v165, v55, v165\n" \
"v_mul_f32_e32 v166, v55, v166\n" \
"v_mul_f32_e32 v167, v55, v167\n" \
"v_cvt_pk_bf16_f32 v80, v160, v161\n" \
"v_cvt_pk_bf16_f32 v81, v162, v163\n" \
"v_cvt_pk_bf16_f32 v82, v164, v165\n" \
"v_cvt_pk_bf16_f32 v83, v166, v167\n" \
"v_mfma_f32_16x16x32_bf16 v[156:159], a[60:63], v[116:119], v[156:159]\n" \
"v_mul_f32_e32 v168, v55, v168\n" \
"v_mul_f32_e32 v169, v55, v169\n" \
"v_mul_f32_e32 v170, v55, v170\n" \
"v_mul_f32_e32 v171, v55, v171\n" \
"v_mul_f32_e32 v172, v55, v172\n" \
"buffer_load_dwordx4 v2, s[80:83], s92 offen offset:2048 lds\n" \
"v_mul_f32_e32 v173, v55, v173\n" \
"v_mul_f32_e32 v174, v55, v174\n" \
"v_mul_f32_e32 v156, v54, v156\n" \
"v_mul_f32_e32 v157, v54, v157\n" \
"v_mul_f32_e32 v158, v54, v158\n" \
"v_mul_f32_e32 v159, v54, v159\n" \
"v_cvt_pk_bf16_f32 v78, v156, v157\n" \
"v_cvt_pk_bf16_f32 v79, v158, v159\n" \
"v_mul_f32_e32 v175, v55, v175\n" \
"v_cvt_pk_bf16_f32 v84, v168, v169\n" \
"v_mfma_f32_16x16x32_bf16 a[208:211], a[216:219], v[76:79], a[208:211]\n" \
"v_mfma_f32_16x16x32_bf16 a[208:211], a[216:219], v[80:83], a[208:211]\n" \
"v_cvt_pk_bf16_f32 v85, v170, v171\n" \
"v_cvt_pk_bf16_f32 v86, v172, v173\n" \
"v_cvt_pk_bf16_f32 v87, v174, v175\n" \
"v_mul_f32_e32 v176, v55, v176\n" \
"v_mul_f32_e32 v177, v55, v177\n" \
"buffer_load_dwordx4 v3, s[80:83], s92 offen offset:3072 lds\n" \
"s_add_u32 m0, s93, 0x1000\n" \
"v_mfma_f32_16x16x32_bf16 a[208:211], a[216:219], v[84:87], a[208:211]\n" \
"v_mul_f32_e32 v178, v55, v178\n" \
"v_mul_f32_e32 v179, v55, v179\n" \
"v_mul_f32_e32 v180, v55, v180\n" \
"v_mul_f32_e32 v181, v55, v181\n" \
"v_mul_f32_e32 v182, v55, v182\n" \
"v_mul_f32_e32 v183, v55, v183\n" \
"v_cvt_pk_bf16_f32 v96, v176, v177\n" \
"v_cvt_pk_bf16_f32 v97, v178, v179\n" \
"v_cvt_pk_bf16_f32 v98, v180, v181\n" \
"v_cvt_pk_bf16_f32 v99, v182, v183\n" \
"v_mfma_f32_16x16x32_bf16 v[188:191], a[60:63], v[120:123], v[188:191]\n" \
"v_mul_f32_e32 v184, v55, v184\n" \
"v_mfma_f32_16x16x32_bf16 a[208:211], a[216:219], v[96:99], a[208:211]\n" \
"v_mul_f32_e32 v185, v55, v185\n" \
"v_mul_f32_e32 v186, v55, v186\n" \
"buffer_load_dwordx4 v4, s[80:83], s92 offen offset:0 lds\n" \
"v_mul_f32_e32 v187, v55, v187\n" \
"v_cvt_pk_bf16_f32 v100, v184, v185\n" \
"v_cvt_pk_bf16_f32 v101, v186, v187\n" \
"v_mul_f32_e32 v188, v55, v188\n" \
"v_mul_f32_e32 v189, v55, v189\n" \
"v_mul_f32_e32 v190, v55, v190\n" \
"v_mul_f32_e32 v191, v55, v191\n" \
"v_cvt_pk_bf16_f32 v102, v188, v189\n" \
"v_cvt_pk_bf16_f32 v103, v190, v191\n" \
"v_cmp_lt_f32_e64 s[72:73], s21, v62\n" \
"buffer_load_dwordx4 v5, s[80:83], s92 offen offset:1024 lds\n" \
"v_mfma_f32_16x16x32_bf16 a[208:211], a[216:219], v[100:103], a[208:211]\n" \
"buffer_load_dwordx4 v6, s[80:83], s92 offen offset:2048 lds\n" \
"buffer_load_dwordx4 v7, s[80:83], s92 offen offset:3072 lds\n" \
"s_add_u32 m0, s93, 0x2000\n" \
"s_nop 0\n" \
"buffer_load_dwordx4 v8, s[80:83], s92 offen offset:0 lds\n" \
"s_nop 2\n" \
"v_accvgpr_read_b32 v63, a208\n" \
"v_cmp_u_f32_e32 vcc, v63, v63\n" \
"s_nop 0\n" \
"s_or_b64 vcc, s[72:73], vcc\n" \
"s_cbranch_scc0 L_out_%=\n" \
"s_branch L_end_%=\n" \
".fill (L_ab + 24576 - .) / 4, 4, 0xBF800000\n" \
".fill (L_ab + 24576 - .) / 4, 4, 0xBF800000\n" \
"L_ap6:\n" \
"s_setpc_b64 s[88:89]\n" \
"L_gs7:\n" \
"v_mfma_f32_16x16x32_bf16 v[104:107], a[128:131], a[16:19], v[36:39]\n" \
"s_add_u32 s68, s60, 0xfff6e000\n" \
"s_add_u32 s69, s60, 0xfffb7000\n" \
"s_add_u32 s70, s60, 0x0\n" \
"s_add_u32 s71, s60, 0x49000\n" \
"v_mfma_f32_16x16x32_bf16 v[104:107], a[132:135], a[20:23], v[104:107]\n" \
"s_xor_b32 m0, s65, 0x12000\n" \
"ds_read_b128 a[160:163], v49 offset:0\n" \
"v_mfma_f32_16x16x32_bf16 v[104:107], a[136:139], a[24:27], v[104:107]\n" \
"buffer_load_dwordx4 v50, s[8:11], s64 offen offset:0 sc1 lds\n" \
"v_mfma_f32_16x16x32_bf16 v[104:107], a[140:143], a[28:31], v[104:107]\n" \
"ds_read_b128 a[164:167], v49 offset:1024\n" \
"ds_read_b128 a[168:171], v49 offset:2048\n" \
"v_mfma_f32_16x16x32_bf16 v[108:111], a[156:159], a[28:31], v[36:39]\n" \
"ds_read_b128 a[172:175], v49 offset:3072\n" \
"ds_read_b128 a[176:179], v49 offset:4096\n" \
"v_mfma_f32_16x16x32_bf16 v[108:111], a[152:155], a[24:27], v[108:111]\n" \
"s_mov_b32 s65, s13\n" \
"v_add_u32_e32 v63, s15, v20\n" \
"v_mfma_f32_16x16x32_bf16 v[108:111], a[148:151], a[20:23], v[108:111]\n" \
"buffer_load_dwordx4 v50, s[8:11], s64 offen offset:1024 sc1 lds\n" \
"v_mfma_f32_16x16x32_bf16 v[108:111], a[144:147], a[16:19], v[108:111]\n" \
"s_add_u32 s67, s14, 40960\n" \
"v_add_u32_e32 v50, 0x6000, v63\n" \
"v_mfma_f32_16x16x32_bf16 v[116:119], a[144:147], a[32:35], v[40:43]\n" \
"s_xor_b32 m0, s65, 0x8000\n" \
"ds_read_b128 a[180:183], v49 offset:5120\n" \
"v_mfma_f32_16x16x32_bf16 v[116:119], a[148:151], a[36:39], v[116:119]\n" \
"ds_read_b128 a[184:187], v49 offset:6144\n" \
"v_and_b32_e32 v68, v12, v68\n" \
"v_mfma_f32_16x16x32_bf16 v[116:119], a[152:155], a[40:43], v[116:119]\n" \
"v_and_b32_e32 v69, v13, v69\n" \
"v_and_b32_e32 v70, v14, v70\n" \
"v_mfma_f32_16x16x32_bf16 v[116:119], a[156:159], a[44:47], v[116:119]\n" \
"buffer_load_dwordx4 v50, s[8:11], s67 offen offset:0 sc1 lds\n" \
"v_mfma_f32_16x16x32_bf16 v[112:115], a[140:143], a[44:47], v[40:43]\n" \
"v_and_b32_e32 v71, v15, v71\n" \
"ds_read_b128 a[188:191], v49 offset:7168\n" \
"v_mfma_f32_16x16x32_bf16 v[112:115], a[136:139], a[40:43], v[112:115]\n" \
"v_exp_f32_e32 v86, v86\n" \
"v_mfma_f32_16x16x32_bf16 v[112:115], a[132:135], a[36:39], v[112:115]\n" \
"v_exp_f32_e32 v87, v87\n" \
"v_mfma_f32_16x16x32_bf16 v[112:115], a[128:131], a[32:35], v[112:115]\n" \
"v_exp_f32_e32 v84, v84\n" \
"v_mfma_f32_16x16x32_bf16 v[120:123], a[128:131], a[48:51], v[44:47]\n" \
"buffer_load_dwordx4 v50, s[8:11], s67 offen offset:1024 sc1 lds\n" \
"v_mfma_f32_16x16x32_bf16 v[120:123], a[132:135], a[52:55], v[120:123]\n" \
"v_exp_f32_e32 v85, v85\n" \
"v_mfma_f32_16x16x32_bf16 v[120:123], a[136:139], a[56:59], v[120:123]\n" \
"v_cvt_pk_bf16_f32 v87, v86, v87\n" \
"v_cvt_pk_bf16_f32 v86, v84, v85\n" \
"v_mfma_f32_16x16x32_bf16 v[120:123], a[140:143], a[60:63], v[120:123]\n" \
"v_exp_f32_e32 v82, v82\n" \
"v_mfma_f32_16x16x32_bf16 v[124:127], a[156:159], a[60:63], v[44:47]\n" \
"v_exp_f32_e32 v83, v83\n" \
"v_mfma_f32_16x16x32_bf16 v[124:127], a[152:155], a[56:59], v[124:127]\n" \
"v_exp_f32_e32 v80, v80\n" \
"v_mfma_f32_16x16x32_bf16 v[124:127], a[148:151], a[52:55], v[124:127]\n" \
"buffer_load_dwordx4 v50, s[8:11], s67 offen offset:2048 sc1 lds\n" \
"v_mfma_f32_16x16x32_bf16 v[124:127], a[144:147], a[48:51], v[124:127]\n" \
"v_cvt_pk_bf16_f32 v85, v82, v83\n" \
"s_waitcnt lgkmcnt(1)\n" \
"v_mfma_f32_16x16x32_bf16 v[128:131], a[160:163], v[68:71], v[128:131]\n" \
"v_exp_f32_e32 v81, v81\n" \
"v_mfma_f32_16x16x32_bf16 v[160:163], a[160:163], v[72:75], v[160:163]\n" \
"v_cvt_pk_bf16_f32 v84, v80, v81\n" \
"s_mov_b32 s64, s14\n" \
"v_mfma_f32_16x16x32_bf16 v[132:135], a[164:167], v[68:71], v[132:135]\n" \
"v_exp_f32_e32 v88, v88\n" \
"v_mfma_f32_16x16x32_bf16 v[164:167], a[164:167], v[72:75], v[164:167]\n" \
"v_exp_f32_e32 v89, v89\n" \
"v_mfma_f32_16x16x32_bf16 v[136:139], a[168:171], v[68:71], v[136:139]\n" \
"buffer_load_dwordx4 v50, s[8:11], s67 offen offset:3072 sc1 lds\n" \
"v_mfma_f32_16x16x32_bf16 v[168:171], a[168:171], v[72:75], v[168:171]\n" \
"v_exp_f32_e32 v90, v90\n" \
"v_mfma_f32_16x16x32_bf16 v[140:143], a[172:175], v[68:71], v[140:143]\n" \
"v_exp_f32_e32 v91, v91\n" \
"v_mfma_f32_16x16x32_bf16 v[172:175], a[172:175], v[72:75], v[172:175]\n" \
"v_cvt_pk_bf16_f32 v88, v88, v89\n" \
"v_cvt_pk_bf16_f32 v89, v90, v91\n" \
"v_mfma_f32_16x16x32_bf16 v[144:147], a[176:179], v[68:71], v[144:147]\n" \
"v_exp_f32_e32 v92, v92\n" \
"v_mfma_f32_16x16x32_bf16 v[176:179], a[176:179], v[72:75], v[176:179]\n" \
"v_exp_f32_e32 v93, v93\n" \
"v_mfma_f32_16x16x32_bf16 v[148:151], a[180:183], v[68:71], v[148:151]\n" \
"v_exp_f32_e32 v94, v94\n" \
"v_mfma_f32_16x16x32_bf16 v[180:183], a[180:183], v[72:75], v[180:183]\n" \
"v_exp_f32_e32 v95, v95\n" \
"v_mfma_f32_16x16x32_bf16 v[152:155], a[184:187], v[68:71], v[152:155]\n" \
"v_cvt_pk_bf16_f32 v90, v92, v93\n" \
"v_cvt_pk_bf16_f32 v91, v94, v95\n" \
"v_mfma_f32_16x16x32_bf16 v[184:187], a[184:187], v[72:75], v[184:187]\n" \
"v_add_u32_e32 v51, 0x12000, v63\n" \
"s_waitcnt lgkmcnt(0)\n" \
"v_mfma_f32_16x16x32_bf16 v[156:159], a[188:191], v[68:71], v[156:159]\n" \
"s_xor_b32 m0, s65, 0xa000\n" \
"ds_read_b32 v58, v18 offset:512\n" \
"v_mfma_f32_16x16x32_bf16 v[188:191], a[188:191], v[72:75], v[188:191]\n" \
"buffer_load_dwordx4 v51, s[8:11], s64 offen offset:0 sc1 lds\n" \
"v_smfmac_f32_16x16x64_bf16 a[192:195], a[200:203], v[68:75], v21\n" \
"ds_read_b32 v59, v18 offset:640\n" \
"ds_read_b32 v60, v18 offset:768\n" \
"v_mfma_f32_16x16x32_bf16 v[220:223], a[188:191], v[84:87], v[220:223]\n" \
"ds_read_b32 v61, v18 offset:896\n" \
"ds_read_b128 a[128:131], v48 offset:0\n" \
"v_mfma_f32_16x16x32_bf16 v[252:255], a[188:191], v[88:91], v[252:255]\n" \
"ds_read_b128 a[144:147], v48 offset:4096\n" \
"ds_read_b128 a[132:135], v48 offset:1024\n" \
"v_mfma_f32_16x16x32_bf16 v[216:219], a[184:187], v[84:87], v[216:219]\n" \
"buffer_load_dwordx4 v51, s[8:11], s64 offen offset:1024 sc1 lds\n" \
"v_mfma_f32_16x16x32_bf16 v[248:251], a[184:187], v[88:91], v[248:251]\n" \
"ds_read_b128 a[148:151], v48 offset:5120\n" \
"ds_read_b128 a[136:139], v48 offset:2048\n" \
"v_mfma_f32_16x16x32_bf16 v[212:215], a[180:183], v[84:87], v[212:215]\n" \
"ds_read_b128 a[152:155], v48 offset:6144\n" \
"ds_read_b128 a[140:143], v48 offset:3072\n" \
"v_mfma_f32_16x16x32_bf16 v[244:247], a[180:183], v[88:91], v[244:247]\n" \
"ds_read_b128 a[156:159], v48 offset:7168\n" \
"v_add_u32_e32 v52, 0x14000, v63\n" \
"v_mfma_f32_16x16x32_bf16 v[208:211], a[176:179], v[84:87], v[208:211]\n" \
"buffer_load_dwordx4 v51, s[8:11], s64 offen offset:2048 sc1 lds\n" \
"v_mfma_f32_16x16x32_bf16 v[240:243], a[176:179], v[88:91], v[240:243]\n" \
"v_exp_f32_e32 v104, v104\n" \
"v_mfma_f32_16x16x32_bf16 v[204:207], a[172:175], v[84:87], v[204:207]\n" \
"v_exp_f32_e32 v105, v105\n" \
"v_mfma_f32_16x16x32_bf16 v[236:239], a[172:175], v[88:91], v[236:239]\n" \
"v_exp_f32_e32 v106, v106\n" \
"v_mfma_f32_16x16x32_bf16 v[200:203], a[168:171], v[84:87], v[200:203]\n" \
"buffer_load_dwordx4 v51, s[8:11], s64 offen offset:3072 sc1 lds\n" \
"v_mfma_f32_16x16x32_bf16 v[232:235], a[168:171], v[88:91], v[232:235]\n" \
"v_exp_f32_e32 v107, v107\n" \
"v_mfma_f32_16x16x32_bf16 v[196:199], a[164:167], v[84:87], v[196:199]\n" \
"v_cvt_pk_bf16_f32 v104, v104, v105\n" \
"v_cvt_pk_bf16_f32 v105, v106, v107\n" \
"v_mfma_f32_16x16x32_bf16 v[228:231], a[164:167], v[88:91], v[228:231]\n" \
"v_exp_f32_e32 v108, v108\n" \
"v_mfma_f32_16x16x32_bf16 v[192:195], a[160:163], v[84:87], v[192:195]\n" \
"v_exp_f32_e32 v109, v109\n" \
"v_mfma_f32_16x16x32_bf16 v[224:227], a[160:163], v[88:91], v[224:227]\n" \
"v_exp_f32_e32 v110, v110\n" \
"v_smfmac_f32_16x16x64_bf16 a[196:199], a[200:203], v[84:91], v21\n" \
"v_exp_f32_e32 v111, v111\n" \
"v_cvt_pk_bf16_f32 v106, v108, v109\n" \
"v_cvt_pk_bf16_f32 v107, v110, v111\n" \
"v_and_b32_e32 v104, v12, v104\n" \
"v_and_b32_e32 v105, v13, v105\n" \
"v_and_b32_e32 v106, v14, v106\n" \
"v_and_b32_e32 v107, v15, v107\n" \
"v_accvgpr_read_b32 v54, a192\n" \
"v_accvgpr_read_b32 v22, a193\n" \
"v_readlane_b32 s93, v11, 19\n" \
"v_readlane_b32 s80, v11, 11\n" \
"v_readlane_b32 s81, v11, 12\n" \
"s_mov_b32 s82, 0x47fca0\n" \
"s_mov_b32 s83, 0x20000\n" \
"v_readlane_b32 s92, v11, 26\n" \
"v_exp_f32_e32 v118, v118\n" \
"v_exp_f32_e32 v119, v119\n" \
"v_exp_f32_e32 v116, v116\n" \
"v_exp_f32_e32 v117, v117\n" \
"v_cvt_pk_bf16_f32 v119, v118, v119\n" \
"v_exp_f32_e32 v114, v114\n" \
"v_exp_f32_e32 v115, v115\n" \
"v_exp_f32_e32 v112, v112\n" \
"v_cvt_pk_bf16_f32 v118, v116, v117\n" \
"s_waitcnt vmcnt(10) lgkmcnt(0)\n" \
"s_barrier\n" \
"v_mfma_f32_16x16x32_bf16 v[80:83], a[128:131], a[32:35], v[40:43]\n" \
"ds_read_b128 a[160:163], v49 offset:8192\n" \
"ds_read_b128 a[164:167], v49 offset:9216\n" \
"v_mfma_f32_16x16x32_bf16 v[80:83], a[132:135], a[36:39], v[80:83]\n" \
"ds_read_b128 a[168:171], v49 offset:10240\n" \
"s_xor_b32 m0, s65, 0x18000\n" \
"v_mfma_f32_16x16x32_bf16 v[80:83], a[136:139], a[40:43], v[80:83]\n" \
"buffer_load_dwordx4 v52, s[8:11], s64 offen offset:0 sc1 lds\n" \
"v_mfma_f32_16x16x32_bf16 v[80:83], a[140:143], a[44:47], v[80:83]\n" \
"ds_read_b128 a[172:175], v49 offset:11264\n" \
"ds_read_b128 a[176:179], v49 offset:12288\n" \
"v_mfma_f32_16x16x32_bf16 v[84:87], a[156:159], a[44:47], v[40:43]\n" \
"ds_read_b128 a[180:183], v49 offset:13312\n" \
"ds_read_b128 a[184:187], v49 offset:14336\n" \
"v_mfma_f32_16x16x32_bf16 v[84:87], a[152:155], a[40:43], v[84:87]\n" \
"ds_read_b128 a[188:191], v49 offset:15360\n" \
"v_cvt_pk_bf16_f32 v117, v114, v115\n" \
"v_mfma_f32_16x16x32_bf16 v[84:87], a[148:151], a[36:39], v[84:87]\n" \
"buffer_load_dwordx4 v52, s[8:11], s64 offen offset:1024 sc1 lds\n" \
"v_mfma_f32_16x16x32_bf16 v[84:87], a[144:147], a[32:35], v[84:87]\n" \
"v_exp_f32_e32 v113, v113\n" \
"v_mfma_f32_16x16x32_bf16 v[92:95], a[144:147], a[48:51], v[44:47]\n" \
"v_cvt_pk_bf16_f32 v116, v112, v113\n" \
"ds_read_b128 a[144:147], v48 offset:12288\n" \
"v_mfma_f32_16x16x32_bf16 v[92:95], a[148:151], a[52:55], v[92:95]\n" \
"v_exp_f32_e32 v120, v120\n" \
"v_mfma_f32_16x16x32_bf16 v[92:95], a[152:155], a[56:59], v[92:95]\n" \
"buffer_load_dwordx4 v52, s[8:11], s64 offen offset:2048 sc1 lds\n" \
"v_mfma_f32_16x16x32_bf16 v[92:95], a[156:159], a[60:63], v[92:95]\n" \
"v_exp_f32_e32 v121, v121\n" \
"v_mfma_f32_16x16x32_bf16 v[88:91], a[140:143], a[60:63], v[44:47]\n" \
"v_exp_f32_e32 v122, v122\n" \
"v_mfma_f32_16x16x32_bf16 v[88:91], a[136:139], a[56:59], v[88:91]\n" \
"v_exp_f32_e32 v123, v123\n" \
"v_mfma_f32_16x16x32_bf16 v[88:91], a[132:135], a[52:55], v[88:91]\n" \
"buffer_load_dwordx4 v52, s[8:11], s64 offen offset:3072 sc1 lds\n" \
"v_mfma_f32_16x16x32_bf16 v[88:91], a[128:131], a[48:51], v[88:91]\n" \
"s_add_u32 m0, s93, 0x0\n" \
"v_cvt_pk_bf16_f32 v120, v120, v121\n" \
"v_mfma_f32_16x16x32_bf16 a[192:195], a[212:215], v[104:107], a[192:195]\n" \
"v_cvt_pk_bf16_f32 v121, v122, v123\n" \
"s_waitcnt lgkmcnt(6)\n" \
"v_mfma_f32_16x16x32_bf16 v[160:163], a[160:163], v[104:107], v[160:163]\n" \
"v_exp_f32_e32 v124, v124\n" \
"v_mfma_f32_16x16x32_bf16 v[164:167], a[164:167], v[104:107], v[164:167]\n" \
"buffer_load_dwordx4 v0, s[80:83], s92 offen offset:0 lds\n" \
"v_mfma_f32_16x16x32_bf16 v[168:171], a[168:171], v[104:107], v[168:171]\n" \
"ds_read_b128 a[128:131], v48 offset:8192\n" \
"s_waitcnt lgkmcnt(2)\n" \
"v_mfma_f32_16x16x32_bf16 v[172:175], a[172:175], v[104:107], v[172:175]\n" \
"v_exp_f32_e32 v125, v125\n" \
"v_mfma_f32_16x16x32_bf16 v[176:179], a[176:179], v[104:107], v[176:179]\n" \
"v_exp_f32_e32 v126, v126\n" \
"buffer_load_dwordx4 v1, s[80:83], s92 offen offset:1024 lds\n" \
"v_mfma_f32_16x16x32_bf16 v[180:183], a[180:183], v[104:107], v[180:183]\n" \
"v_exp_f32_e32 v127, v127\n" \
"v_mfma_f32_16x16x32_bf16 v[184:187], a[184:187], v[104:107], v[184:187]\n" \
"v_cvt_pk_bf16_f32 v122, v124, v125\n" \
"v_cvt_pk_bf16_f32 v123, v126, v127\n" \
"v_mfma_f32_16x16x32_bf16 v[188:191], a[188:191], v[104:107], v[188:191]\n" \
"ds_read_b128 a[132:135], v48 offset:9216\n" \
"ds_read_b128 a[148:151], v48 offset:13312\n" \
"buffer_load_dwordx4 v2, s[80:83], s92 offen offset:2048 lds\n" \
"v_mfma_f32_16x16x32_bf16 v[220:223], a[188:191], v[116:119], v[220:223]\n" \
"ds_read_b128 a[136:139], v48 offset:10240\n" \
"ds_read_b128 a[152:155], v48 offset:14336\n" \
"v_mfma_f32_16x16x32_bf16 v[252:255], a[188:191], v[120:123], v[252:255]\n" \
"ds_read_b128 a[140:143], v48 offset:11264\n" \
"ds_read_b128 a[156:159], v48 offset:15360\n" \
"v_mfma_f32_16x16x32_bf16 v[216:219], a[184:187], v[116:119], v[216:219]\n" \
"v_readlane_b32 s96, v11, 1\n" \
"v_bfe_u32 v27, v20, 2, 7\n" \
"buffer_load_dwordx4 v3, s[80:83], s92 offen offset:3072 lds\n" \
"v_mfma_f32_16x16x32_bf16 v[248:251], a[184:187], v[120:123], v[248:251]\n" \
"s_add_u32 m0, s93, 0x1000\n" \
"v_add_f32_e32 v54, v54, v22\n" \
"v_mfma_f32_16x16x32_bf16 v[212:215], a[180:183], v[116:119], v[212:215]\n" \
"v_max_f32_e32 v62, v62, v54\n" \
"v_add_u32_e32 v49, 0x10000, v48\n" \
"v_mfma_f32_16x16x32_bf16 v[244:247], a[180:183], v[120:123], v[244:247]\n" \
"v_rcp_f32_e32 v54, v54\n" \
"buffer_load_dwordx4 v4, s[80:83], s92 offen offset:0 lds\n" \
"v_mfma_f32_16x16x32_bf16 v[208:211], a[176:179], v[116:119], v[208:211]\n" \
"v_mul_f32_e32 v54, v54, v58\n" \
"v_mul_f32_e32 v128, v54, v128\n" \
"v_mfma_f32_16x16x32_bf16 v[240:243], a[176:179], v[120:123], v[240:243]\n" \
"v_mul_f32_e32 v129, v54, v129\n" \
"v_mul_f32_e32 v136, v54, v136\n" \
"v_mfma_f32_16x16x32_bf16 v[204:207], a[172:175], v[116:119], v[204:207]\n" \
"v_mul_f32_e32 v137, v54, v137\n" \
"v_mul_f32_e32 v144, v54, v144\n" \
"buffer_load_dwordx4 v5, s[80:83], s92 offen offset:1024 lds\n" \
"v_mfma_f32_16x16x32_bf16 v[236:239], a[172:175], v[120:123], v[236:239]\n" \
"v_mul_f32_e32 v145, v54, v145\n" \
"v_mul_f32_e32 v152, v54, v152\n" \
"v_mfma_f32_16x16x32_bf16 v[200:203], a[168:171], v[116:119], v[200:203]\n" \
"v_mul_f32_e32 v153, v54, v153\n" \
"v_cvt_pk_bf16_f32 v128, v128, v129\n" \
"v_mfma_f32_16x16x32_bf16 v[232:235], a[168:171], v[120:123], v[232:235]\n" \
"v_mul_f32_e32 v130, v54, v130\n" \
"v_mul_f32_e32 v131, v54, v131\n" \
"buffer_load_dwordx4 v6, s[80:83], s92 offen offset:2048 lds\n" \
"v_mfma_f32_16x16x32_bf16 v[196:199], a[164:167], v[116:119], v[196:199]\n" \
"v_cvt_pk_bf16_f32 v136, v136, v137\n" \
"v_mul_f32_e32 v138, v54, v138\n" \
"v_mfma_f32_16x16x32_bf16 v[228:231], a[164:167], v[120:123], v[228:231]\n" \
"v_mul_f32_e32 v139, v54, v139\n" \
"v_cvt_pk_bf16_f32 v144, v144, v145\n" \
"v_mfma_f32_16x16x32_bf16 v[192:195], a[160:163], v[116:119], v[192:195]\n" \
"v_mul_f32_e32 v146, v54, v146\n" \
"v_mul_f32_e32 v147, v54, v147\n" \
"buffer_load_dwordx4 v7, s[80:83], s92 offen offset:3072 lds\n" \
"v_mfma_f32_16x16x32_bf16 v[224:227], a[160:163], v[120:123], v[224:227]\n" \
"v_cvt_pk_bf16_f32 v152, v152, v153\n" \
"v_mul_f32_e32 v154, v54, v154\n" \
"v_smfmac_f32_16x16x64_bf16 a[196:199], a[200:203], v[116:123], v21\n" \
"v_mul_f32_e32 v155, v54, v155\n" \
"v_cvt_pk_bf16_f32 v129, v130, v131\n" \
"v_mul_f32_e32 v132, v54, v132\n" \
"v_mul_f32_e32 v133, v54, v133\n" \
"v_mul_f32_e32 v134, v54, v134\n" \
"v_mul_f32_e32 v135, v54, v135\n" \
"v_cvt_pk_bf16_f32 v137, v138, v139\n" \
"v_mul_f32_e32 v140, v54, v140\n" \
"v_mul_f32_e32 v141, v54, v141\n" \
"v_mul_f32_e32 v142, v54, v142\n" \
"v_mul_f32_e32 v143, v54, v143\n" \
"v_cvt_pk_bf16_f32 v145, v146, v147\n" \
"v_mul_f32_e32 v148, v54, v148\n" \
"v_mul_f32_e32 v149, v54, v149\n" \
"v_mul_f32_e32 v150, v54, v150\n" \
"v_mul_f32_e32 v151, v54, v151\n" \
"v_cvt_pk_bf16_f32 v153, v154, v155\n" \
"v_mul_f32_e32 v156, v54, v156\n" \
"v_mul_f32_e32 v157, v54, v157\n" \
"v_mul_f32_e32 v158, v54, v158\n" \
"v_mul_f32_e32 v159, v54, v159\n" \
"v_cvt_pk_bf16_f32 v130, v132, v133\n" \
"v_cvt_pk_bf16_f32 v131, v134, v135\n" \
"buffer_store_dwordx4 v[128:131], v20, s[56:59], s68 offen offset:0 sc1\n" \
"v_cvt_pk_bf16_f32 v138, v140, v141\n" \
"v_cvt_pk_bf16_f32 v139, v142, v143\n" \
"v_cvt_pk_bf16_f32 v146, v148, v149\n" \
"v_cvt_pk_bf16_f32 v147, v150, v151\n" \
"v_cvt_pk_bf16_f32 v154, v156, v157\n" \
"v_cvt_pk_bf16_f32 v155, v158, v159\n" \
"s_add_u32 m0, s93, 0x2000\n" \
"s_xor_b32 s65, s65, 0x8000\n" \
"s_add_u32 s64, s64, 0x10000\n" \
"v_exp_f32_e32 v86, v86\n" \
"buffer_store_dwordx4 v[136:139], v20, s[56:59], s68 offen offset:1024 sc1\n" \
"v_exp_f32_e32 v87, v87\n" \
"v_exp_f32_e32 v84, v84\n" \
"v_exp_f32_e32 v85, v85\n" \
"v_exp_f32_e32 v88, v88\n" \
"v_exp_f32_e32 v89, v89\n" \
"v_cvt_pk_bf16_f32 v87, v86, v87\n" \
"buffer_store_dwordx4 v[144:147], v20, s[56:59], s68 offen offset:2048 sc1\n" \
"v_exp_f32_e32 v82, v82\n" \
"v_exp_f32_e32 v83, v83\n" \
"v_exp_f32_e32 v80, v80\n" \
"v_exp_f32_e32 v81, v81\n" \
"v_exp_f32_e32 v90, v90\n" \
"v_exp_f32_e32 v91, v91\n" \
"buffer_store_dwordx4 v[152:155], v20, s[56:59], s68 offen offset:3072 sc1\n" \
"v_cvt_pk_bf16_f32 v86, v84, v85\n" \
"v_cvt_pk_bf16_f32 v88, v88, v89\n" \
"v_exp_f32_e32 v92, v92\n" \
"v_exp_f32_e32 v93, v93\n" \
"v_exp_f32_e32 v94, v94\n" \
"v_exp_f32_e32 v95, v95\n" \
"v_cvt_pk_bf16_f32 v85, v82, v83\n" \
"buffer_load_dwordx4 v8, s[80:83], s92 offen offset:0 lds\n" \
"v_cvt_pk_bf16_f32 v84, v80, v81\n" \
"v_cvt_pk_bf16_f32 v89, v90, v91\n" \
"v_and_b32_e32 v84, v12, v84\n" \
"v_and_b32_e32 v85, v13, v85\n" \
"v_and_b32_e32 v86, v14, v86\n" \
"v_and_b32_e32 v87, v15, v87\n" \
"s_waitcnt vmcnt(17) lgkmcnt(0)\n" \
"s_barrier\n" \
"v_mfma_f32_16x16x32_bf16 v[120:123], a[128:131], a[48:51], v[44:47]\n" \
"buffer_load_dword v26, v27, s[56:59], s96 offen sc1\n" \
"v_mfma_f32_16x16x32_bf16 v[120:123], a[132:135], a[52:55], v[120:123]\n" \
"ds_read_b128 a[188:191], v49 offset:7168\n" \
"ds_read_b128 a[184:187], v49 offset:6144\n" \
"v_mfma_f32_16x16x32_bf16 v[120:123], a[136:139], a[56:59], v[120:123]\n" \
"s_xor_b32 m0, s65, 0x12000\n" \
"ds_read_b128 a[180:183], v49 offset:5120\n" \
"v_mfma_f32_16x16x32_bf16 v[120:123], a[140:143], a[60:63], v[120:123]\n" \
"ds_read_b128 a[176:179], v49 offset:4096\n" \
"ds_read_b128 a[172:175], v49 offset:3072\n" \
"v_mfma_f32_16x16x32_bf16 v[124:127], a[156:159], a[60:63], v[44:47]\n" \
"buffer_load_dwordx4 v50, s[8:11], s64 offen offset:0 sc1 lds\n" \
"v_mfma_f32_16x16x32_bf16 v[124:127], a[152:155], a[56:59], v[124:127]\n" \
"ds_read_b128 a[168:171], v49 offset:2048\n" \
"v_cvt_pk_bf16_f32 v90, v92, v93\n" \
"v_mfma_f32_16x16x32_bf16 v[124:127], a[148:151], a[52:55], v[124:127]\n" \
"v_cvt_pk_bf16_f32 v91, v94, v95\n" \
"ds_read_b128 a[164:167], v49 offset:1024\n" \
"v_mfma_f32_16x16x32_bf16 v[124:127], a[144:147], a[48:51], v[124:127]\n" \
"ds_read_b128 a[160:163], v49 offset:0\n" \
"ds_read_b128 a[0:3], v49 offset:8192\n" \
"v_mfma_f32_16x16x32_bf16 v[32:35], a[220:223], a[64:67], 0\n" \
"buffer_load_dwordx4 v50, s[8:11], s64 offen offset:1024 sc1 lds\n" \
"v_mfma_f32_16x16x32_bf16 v[36:39], a[220:223], a[80:83], 0\n" \
"ds_read_b128 a[4:7], v49 offset:9216\n" \
"ds_read_b128 a[8:11], v49 offset:10240\n" \
"v_mfma_f32_16x16x32_bf16 v[32:35], a[224:227], a[68:71], v[32:35]\n" \
"ds_read_b128 a[12:15], v49 offset:11264\n" \
"ds_read_b128 a[16:19], v49 offset:12288\n" \
"v_mfma_f32_16x16x32_bf16 v[36:39], a[224:227], a[84:87], v[36:39]\n" \
"ds_read_b128 a[20:23], v49 offset:13312\n" \
"ds_read_b128 a[24:27], v49 offset:14336\n" \
"v_mfma_f32_16x16x32_bf16 v[32:35], a[228:231], a[72:75], v[32:35]\n" \
"buffer_load_dwordx4 v50, s[8:11], s64 offen offset:2048 sc1 lds\n" \
"v_mfma_f32_16x16x32_bf16 v[36:39], a[228:231], a[88:91], v[36:39]\n" \
"ds_read_b128 a[28:31], v49 offset:15360\n" \
"v_readlane_b32 s97, v11, 36\n" \
"v_mfma_f32_16x16x32_bf16 v[32:35], a[232:235], a[76:79], v[32:35]\n" \
"v_exp_f32_e32 v120, v120\n" \
"v_mfma_f32_16x16x32_bf16 v[36:39], a[232:235], a[92:95], v[36:39]\n" \
"v_exp_f32_e32 v121, v121\n" \
"v_smfmac_f32_16x16x64_bf16 a[196:199], a[200:203], v[84:91], v21\n" \
"v_cvt_pk_bf16_f32 v120, v120, v121\n" \
"s_waitcnt lgkmcnt(13)\n" \
"v_mfma_f32_16x16x32_bf16 v[220:223], a[188:191], v[84:87], v[220:223]\n" \
"buffer_load_dwordx4 v50, s[8:11], s64 offen offset:3072 sc1 lds\n" \
"v_mfma_f32_16x16x32_bf16 v[252:255], a[188:191], v[88:91], v[252:255]\n" \
"v_exp_f32_e32 v122, v122\n" \
"v_mfma_f32_16x16x32_bf16 v[216:219], a[184:187], v[84:87], v[216:219]\n" \
"v_exp_f32_e32 v123, v123\n" \
"v_mfma_f32_16x16x32_bf16 v[248:251], a[184:187], v[88:91], v[248:251]\n" \
"v_exp_f32_e32 v124, v124\n" \
"v_mfma_f32_16x16x32_bf16 v[212:215], a[180:183], v[84:87], v[212:215]\n" \
"v_exp_f32_e32 v125, v125\n" \
"v_mfma_f32_16x16x32_bf16 v[244:247], a[180:183], v[88:91], v[244:247]\n" \
"v_cvt_pk_bf16_f32 v121, v122, v123\n" \
"s_waitcnt lgkmcnt(7)\n" \
"v_mfma_f32_16x16x32_bf16 v[208:211], a[176:179], v[84:87], v[208:211]\n" \
"v_exp_f32_e32 v126, v126\n" \
"v_mfma_f32_16x16x32_bf16 v[240:243], a[176:179], v[88:91], v[240:243]\n" \
"v_exp_f32_e32 v127, v127\n" \
"v_mfma_f32_16x16x32_bf16 v[204:207], a[172:175], v[84:87], v[204:207]\n" \
"v_cvt_pk_bf16_f32 v122, v124, v125\n" \
"v_cvt_pk_bf16_f32 v123, v126, v127\n" \
"v_mfma_f32_16x16x32_bf16 v[236:239], a[172:175], v[88:91], v[236:239]\n" \
"v_and_b32_e32 v120, v12, v120\n" \
"v_and_b32_e32 v121, v13, v121\n" \
"v_mfma_f32_16x16x32_bf16 v[200:203], a[168:171], v[84:87], v[200:203]\n" \
"v_and_b32_e32 v122, v14, v122\n" \
"v_and_b32_e32 v123, v15, v123\n" \
"v_mfma_f32_16x16x32_bf16 v[232:235], a[168:171], v[88:91], v[232:235]\n" \
"v_accvgpr_read_b32 v56, a196\n" \
"v_accvgpr_read_b32 v24, a197\n" \
"v_mfma_f32_16x16x32_bf16 v[196:199], a[164:167], v[84:87], v[196:199]\n" \
"v_add_u32_e32 v17, 0x49000, v20\n" \
"v_readlane_b32 s98, v11, 37\n" \
"v_mfma_f32_16x16x32_bf16 v[228:231], a[164:167], v[88:91], v[228:231]\n" \
"v_add_u32_e32 v48, s16, v20\n" \
"ds_read_b128 a[128:131], v48 offset:0\n" \
"v_mfma_f32_16x16x32_bf16 v[192:195], a[160:163], v[84:87], v[192:195]\n" \
"ds_read_b128 a[144:147], v48 offset:4096\n" \
"ds_read_b128 a[132:135], v48 offset:1024\n" \
"v_mfma_f32_16x16x32_bf16 v[224:227], a[160:163], v[88:91], v[224:227]\n" \
"ds_read_b128 a[148:151], v48 offset:5120\n" \
"ds_read_b128 a[136:139], v48 offset:2048\n" \
"v_mfma_f32_16x16x32_bf16 a[196:199], a[212:215], v[120:123], a[196:199]\n" \
"ds_read_b128 a[152:155], v48 offset:6144\n" \
"ds_read_b128 a[140:143], v48 offset:3072\n" \
"v_mfma_f32_16x16x32_bf16 v[224:227], a[0:3], v[120:123], v[224:227]\n" \
"ds_read_b128 a[156:159], v48 offset:7168\n" \
"s_waitcnt lgkmcnt(8)\n" \
"v_mfma_f32_16x16x32_bf16 v[228:231], a[4:7], v[120:123], v[228:231]\n" \
"v_accvgpr_read_b32 v55, a194\n" \
"v_accvgpr_read_b32 v23, a195\n" \
"v_mfma_f32_16x16x32_bf16 a[192:195], a[216:219], a[216:219], 0\n" \
"v_add_f32_e32 v55, v55, v23\n" \
"v_max_f32_e32 v62, v62, v55\n" \
"v_mfma_f32_16x16x32_bf16 v[232:235], a[8:11], v[120:123], v[232:235]\n" \
"v_rcp_f32_e32 v55, v55\n" \
"v_mfma_f32_16x16x32_bf16 v[236:239], a[12:15], v[120:123], v[236:239]\n" \
"v_mul_f32_e32 v55, v55, v59\n" \
"v_mul_f32_e32 v160, v55, v160\n" \
"v_mfma_f32_16x16x32_bf16 v[240:243], a[16:19], v[120:123], v[240:243]\n" \
"v_mul_f32_e32 v161, v55, v161\n" \
"v_mul_f32_e32 v168, v55, v168\n" \
"v_mfma_f32_16x16x32_bf16 v[244:247], a[20:23], v[120:123], v[244:247]\n" \
"v_mul_f32_e32 v169, v55, v169\n" \
"v_mul_f32_e32 v176, v55, v176\n" \
"v_mfma_f32_16x16x32_bf16 v[248:251], a[24:27], v[120:123], v[248:251]\n" \
"v_mul_f32_e32 v177, v55, v177\n" \
"v_mul_f32_e32 v184, v55, v184\n" \
"v_mfma_f32_16x16x32_bf16 v[252:255], a[28:31], v[120:123], v[252:255]\n" \
"v_mul_f32_e32 v185, v55, v185\n" \
"v_cvt_pk_bf16_f32 v160, v160, v161\n" \
"v_mul_f32_e32 v162, v55, v162\n" \
"v_mul_f32_e32 v163, v55, v163\n" \
"v_cvt_pk_bf16_f32 v168, v168, v169\n" \
"v_mul_f32_e32 v170, v55, v170\n" \
"v_mul_f32_e32 v171, v55, v171\n" \
"v_cvt_pk_bf16_f32 v176, v176, v177\n" \
"v_mul_f32_e32 v178, v55, v178\n" \
"s_waitcnt vmcnt(4)\n" \
"v_cmp_ne_u32_e32 vcc, s62, v26\n" \
"v_mul_f32_e32 v179, v55, v179\n" \
"v_cvt_pk_bf16_f32 v184, v184, v185\n" \
"v_mul_f32_e32 v186, v55, v186\n" \
"v_mul_f32_e32 v187, v55, v187\n" \
"v_cvt_pk_bf16_f32 v161, v162, v163\n" \
"s_cbranch_vccnz L_sos_%=\n" \
"L_ros_%=:\n" \
"buffer_load_dwordx4 a[220:223], v20, s[56:59], s97 offen offset:0 sc1\n" \
"v_mul_f32_e32 v164, v55, v164\n" \
"v_mul_f32_e32 v165, v55, v165\n" \
"v_mul_f32_e32 v166, v55, v166\n" \
"v_mul_f32_e32 v167, v55, v167\n" \
"v_cvt_pk_bf16_f32 v169, v170, v171\n" \
"v_mul_f32_e32 v172, v55, v172\n" \
"v_mul_f32_e32 v173, v55, v173\n" \
"v_mul_f32_e32 v174, v55, v174\n" \
"v_mul_f32_e32 v175, v55, v175\n" \
"v_cvt_pk_bf16_f32 v177, v178, v179\n" \
"v_mul_f32_e32 v180, v55, v180\n" \
"buffer_load_dwordx4 a[228:231], v17, s[56:59], s97 offen offset:0 sc1\n" \
"v_mul_f32_e32 v181, v55, v181\n" \
"v_mul_f32_e32 v182, v55, v182\n" \
"v_mul_f32_e32 v183, v55, v183\n" \
"v_cvt_pk_bf16_f32 v185, v186, v187\n" \
"v_mul_f32_e32 v188, v55, v188\n" \
"v_mul_f32_e32 v189, v55, v189\n" \
"v_mul_f32_e32 v190, v55, v190\n" \
"v_mul_f32_e32 v191, v55, v191\n" \
"v_cvt_pk_bf16_f32 v162, v164, v165\n" \
"v_cvt_pk_bf16_f32 v163, v166, v167\n" \
"v_cvt_pk_bf16_f32 v170, v172, v173\n" \
"buffer_store_dwordx4 v[160:163], v20, s[56:59], s69 offen offset:0 sc1\n" \
"v_cvt_pk_bf16_f32 v171, v174, v175\n" \
"v_cvt_pk_bf16_f32 v178, v180, v181\n" \
"v_cvt_pk_bf16_f32 v179, v182, v183\n" \
"v_cvt_pk_bf16_f32 v186, v188, v189\n" \
"v_cvt_pk_bf16_f32 v187, v190, v191\n" \
"v_readlane_b32 s93, v11, 20\n" \
"v_readlane_b32 s84, v11, 15\n" \
"v_readlane_b32 s85, v11, 16\n" \
"v_readlane_b32 s86, v11, 17\n" \
"s_mov_b32 s87, 0x20000\n" \
"v_readlane_b32 s92, v11, 18\n" \
"buffer_store_dwordx4 v[168:171], v20, s[56:59], s69 offen offset:1024 sc1\n" \
"v_readlane_b32 s91, v11, 21\n" \
"v_mfma_f32_16x16x32_bf16 a[208:211], a[216:219], v[128:131], a[208:211]\n" \
"v_mfma_f32_16x16x32_bf16 a[208:211], a[216:219], v[136:139], a[208:211]\n" \
"v_mfma_f32_32x32x16_bf16 v[128:143], a[216:219], a[216:219], 0\n" \
"buffer_store_dwordx4 v[176:179], v20, s[56:59], s69 offen offset:2048 sc1\n" \
"v_mfma_f32_16x16x32_bf16 a[208:211], a[216:219], v[144:147], a[208:211]\n" \
"v_mfma_f32_16x16x32_bf16 a[208:211], a[216:219], v[152:155], a[208:211]\n" \
"v_mfma_f32_32x32x16_bf16 v[144:159], a[216:219], a[216:219], 0\n" \
"buffer_store_dwordx4 v[184:187], v20, s[56:59], s69 offen offset:3072 sc1\n" \
"v_add_f32_e32 v56, v56, v24\n" \
"v_mfma_f32_16x16x32_bf16 a[208:211], a[216:219], v[160:163], a[208:211]\n" \
"v_max_f32_e32 v62, v62, v56\n" \
"v_rcp_f32_e32 v56, v56\n" \
"v_mfma_f32_16x16x32_bf16 a[208:211], a[216:219], v[168:171], a[208:211]\n" \
"v_mfma_f32_32x32x16_bf16 v[160:175], a[216:219], a[216:219], 0\n" \
"buffer_load_dwordx4 a[236:239], v20, s[56:59], s98 offen offset:0 sc1\n" \
"v_mul_f32_e32 v56, v56, v60\n" \
"v_mfma_f32_16x16x32_bf16 a[208:211], a[216:219], v[176:179], a[208:211]\n" \
"v_mul_f32_e32 v192, v56, v192\n" \
"v_mul_f32_e32 v193, v56, v193\n" \
"v_cvt_pk_bf16_f32 v192, v192, v193\n" \
"v_mul_f32_e32 v194, v56, v194\n" \
"v_mul_f32_e32 v195, v56, v195\n" \
"buffer_load_dwordx4 a[244:247], v17, s[56:59], s98 offen offset:0 sc1\n" \
"v_mfma_f32_16x16x32_bf16 a[208:211], a[216:219], v[184:187], a[208:211]\n" \
"v_mfma_f32_32x32x16_bf16 v[176:191], a[216:219], a[216:219], 0\n" \
"v_cvt_pk_bf16_f32 v193, v194, v195\n" \
"v_mul_f32_e32 v196, v56, v196\n" \
"v_mul_f32_e32 v197, v56, v197\n" \
"v_mul_f32_e32 v198, v56, v198\n" \
"v_mul_f32_e32 v199, v56, v199\n" \
"buffer_load_dwordx4 a[224:227], v20, s[56:59], s97 offen offset:1024 sc1\n" \
"v_mul_f32_e32 v200, v56, v200\n" \
"v_mul_f32_e32 v201, v56, v201\n" \
"v_accvgpr_read_b32 v57, a198\n" \
"v_accvgpr_read_b32 v25, a199\n" \
"v_mfma_f32_16x16x32_bf16 a[196:199], a[216:219], a[216:219], 0\n" \
"v_cvt_pk_bf16_f32 v194, v196, v197\n" \
"v_cvt_pk_bf16_f32 v195, v198, v199\n" \
"v_cvt_pk_bf16_f32 v200, v200, v201\n" \
"v_mul_f32_e32 v202, v56, v202\n" \
"v_mul_f32_e32 v203, v56, v203\n" \
"buffer_store_dwordx4 v[192:195], v20, s[56:59], s70 offen offset:0 sc1\n" \
"v_add_f32_e32 v57, v57, v25\n" \
"v_mfma_f32_16x16x32_bf16 a[208:211], a[216:219], v[192:195], a[208:211]\n" \
"v_cvt_pk_bf16_f32 v201, v202, v203\n" \
"v_mul_f32_e32 v204, v56, v204\n" \
"v_mul_f32_e32 v205, v56, v205\n" \
"v_mul_f32_e32 v206, v56, v206\n" \
"v_mul_f32_e32 v207, v56, v207\n" \
"v_mul_f32_e32 v208, v56, v208\n" \
"v_mul_f32_e32 v209, v56, v209\n" \
"v_max_f32_e32 v62, v62, v57\n" \
"buffer_load_dwordx4 a[232:235], v17, s[56:59], s97 offen offset:1024 sc1\n" \
"s_waitcnt vmcnt(16) lgkmcnt(0)\n" \
"s_barrier\n" \
"v_mfma_f32_16x16x32_bf16 v[64:67], a[128:131], a[64:67], v[32:35]\n" \
"s_add_u32 m0, s93, 0x0\n" \
"v_cvt_pk_bf16_f32 v202, v204, v205\n" \
"v_mfma_f32_16x16x32_bf16 v[64:67], a[132:135], a[68:71], v[64:67]\n" \
"buffer_load_dwordx4 a[240:243], v20, s[56:59], s98 offen offset:1024 sc1\n" \
"v_mfma_f32_16x16x32_bf16 v[64:67], a[136:139], a[72:75], v[64:67]\n" \
"v_cvt_pk_bf16_f32 v203, v206, v207\n" \
"v_cvt_pk_bf16_f32 v208, v208, v209\n" \
"v_mfma_f32_16x16x32_bf16 v[64:67], a[140:143], a[76:79], v[64:67]\n" \
"v_mul_f32_e32 v210, v56, v210\n" \
"v_mul_f32_e32 v211, v56, v211\n" \
"v_mfma_f32_16x16x32_bf16 v[68:71], a[156:159], a[76:79], v[32:35]\n" \
"v_rcp_f32_e32 v57, v57\n" \
"buffer_store_dwordx4 v[200:203], v20, s[56:59], s70 offen offset:1024 sc1\n" \
"v_mfma_f32_16x16x32_bf16 v[68:71], a[152:155], a[72:75], v[68:71]\n" \
"v_mfma_f32_16x16x32_bf16 a[208:211], a[216:219], v[200:203], a[208:211]\n" \
"v_mfma_f32_32x32x16_bf16 v[192:207], a[216:219], a[216:219], 0\n" \
"v_cvt_pk_bf16_f32 v209, v210, v211\n" \
"buffer_load_dwordx4 a[248:251], v17, s[56:59], s98 offen offset:1024 sc1\n" \
"v_mul_f32_e32 v212, v56, v212\n" \
"v_mul_f32_e32 v213, v56, v213\n" \
"v_mul_f32_e32 v214, v56, v214\n" \
"v_mfma_f32_16x16x32_bf16 v[68:71], a[148:151], a[68:71], v[68:71]\n" \
"v_mul_f32_e32 v215, v56, v215\n" \
"v_mul_f32_e32 v216, v56, v216\n" \
"v_mfma_f32_16x16x32_bf16 v[68:71], a[144:147], a[64:67], v[68:71]\n" \
"v_mul_f32_e32 v217, v56, v217\n" \
"v_cvt_pk_bf16_f32 v210, v212, v213\n" \
"buffer_load_dwordx4 v0, s[84:87], s92 offen offset:0 lds\n" \
"v_mfma_f32_16x16x32_bf16 v[76:79], a[144:147], a[80:83], v[36:39]\n" \
"ds_read_b128 a[144:147], v48 offset:12288\n" \
"v_cvt_pk_bf16_f32 v211, v214, v215\n" \
"v_mfma_f32_16x16x32_bf16 v[76:79], a[148:151], a[84:87], v[76:79]\n" \
"ds_read_b128 a[148:151], v48 offset:13312\n" \
"v_cvt_pk_bf16_f32 v216, v216, v217\n" \
"v_mfma_f32_16x16x32_bf16 v[76:79], a[152:155], a[88:91], v[76:79]\n" \
"ds_read_b128 a[152:155], v48 offset:14336\n" \
"v_mul_f32_e32 v218, v56, v218\n" \
"buffer_store_dwordx4 v[208:211], v20, s[56:59], s70 offen offset:2048 sc1\n" \
"v_mfma_f32_16x16x32_bf16 v[76:79], a[156:159], a[92:95], v[76:79]\n" \
"ds_read_b128 a[156:159], v48 offset:15360\n" \
"v_mul_f32_e32 v219, v56, v219\n" \
"v_mfma_f32_16x16x32_bf16 v[72:75], a[140:143], a[92:95], v[36:39]\n" \
"ds_read_b128 a[140:143], v48 offset:11264\n" \
"v_mul_f32_e32 v57, v57, v61\n" \
"v_mfma_f32_16x16x32_bf16 v[72:75], a[136:139], a[88:91], v[72:75]\n" \
".fill (L_ab + 28672 - .) / 4, 4, 0xBF800000\n" \
"L_ap7:\n" \
"s_cbranch_execz L_ap0\n" \
"ds_read_b128 a[136:139], v48 offset:10240\n" \
"v_cvt_pk_bf16_f32 v217, v218, v219\n" \
"buffer_load_dwordx4 v1, s[84:87], s92 offen offset:1024 lds\n" \
"v_mfma_f32_16x16x32_bf16 v[72:75], a[132:135], a[84:87], v[72:75]\n" \
"ds_read_b128 a[132:135], v48 offset:9216\n" \
"v_mul_f32_e32 v220, v56, v220\n" \
"v_mfma_f32_16x16x32_bf16 v[72:75], a[128:131], a[80:83], v[72:75]\n" \
"ds_read_b128 a[128:131], v48 offset:8192\n" \
"v_mfma_f32_16x16x32_bf16 a[208:211], a[216:219], v[208:211], a[208:211]\n" \
"v_mul_f32_e32 v221, v56, v221\n" \
"buffer_load_dwordx4 v2, s[84:87], s92 offen offset:2048 lds\n" \
"v_mul_f32_e32 v222, v56, v222\n" \
"v_mul_f32_e32 v223, v56, v223\n" \
"v_mul_f32_e32 v224, v57, v224\n" \
"v_mul_f32_e32 v225, v57, v225\n" \
"v_cvt_pk_bf16_f32 v218, v220, v221\n" \
"v_cvt_pk_bf16_f32 v219, v222, v223\n" \
"v_cvt_pk_bf16_f32 v224, v224, v225\n" \
"v_mul_f32_e32 v226, v57, v226\n" \
"v_mul_f32_e32 v227, v57, v227\n" \
"v_mfma_f32_16x16x32_bf16 a[208:211], a[216:219], v[216:219], a[208:211]\n" \
"buffer_store_dwordx4 v[216:219], v20, s[56:59], s70 offen offset:3072 sc1\n" \
"v_cvt_pk_bf16_f32 v225, v226, v227\n" \
"v_mul_f32_e32 v228, v57, v228\n" \
"v_mfma_f32_32x32x16_bf16 v[208:223], a[216:219], a[216:219], 0\n" \
"v_mul_f32_e32 v229, v57, v229\n" \
"v_mul_f32_e32 v230, v57, v230\n" \
"v_mul_f32_e32 v231, v57, v231\n" \
"v_mul_f32_e32 v232, v57, v232\n" \
"v_mul_f32_e32 v233, v57, v233\n" \
"v_cvt_pk_bf16_f32 v226, v228, v229\n" \
"v_cvt_pk_bf16_f32 v227, v230, v231\n" \
"buffer_store_dwordx4 v[224:227], v20, s[56:59], s71 offen offset:0 sc1\n" \
"v_cvt_pk_bf16_f32 v232, v232, v233\n" \
"v_mul_f32_e32 v234, v57, v234\n" \
"v_mul_f32_e32 v235, v57, v235\n" \
"v_mfma_f32_16x16x32_bf16 a[208:211], a[216:219], v[224:227], a[208:211]\n" \
"v_cvt_pk_bf16_f32 v233, v234, v235\n" \
"v_mul_f32_e32 v236, v57, v236\n" \
"v_mul_f32_e32 v237, v57, v237\n" \
"v_mul_f32_e32 v238, v57, v238\n" \
"v_mul_f32_e32 v239, v57, v239\n" \
"v_mul_f32_e32 v240, v57, v240\n" \
"buffer_load_dwordx4 v3, s[84:87], s92 offen offset:3072 lds\n" \
"s_add_u32 m0, s91, 0x1000\n" \
"v_mul_f32_e32 v241, v57, v241\n" \
"v_cvt_pk_bf16_f32 v234, v236, v237\n" \
"v_cvt_pk_bf16_f32 v235, v238, v239\n" \
"v_cvt_pk_bf16_f32 v240, v240, v241\n" \
"v_mul_f32_e32 v242, v57, v242\n" \
"v_mul_f32_e32 v243, v57, v243\n" \
"v_mfma_f32_16x16x32_bf16 a[208:211], a[216:219], v[232:235], a[208:211]\n" \
"v_cvt_pk_bf16_f32 v241, v242, v243\n" \
"v_mul_f32_e32 v244, v57, v244\n" \
"buffer_store_dwordx4 v[232:235], v20, s[56:59], s71 offen offset:1024 sc1\n" \
"v_mul_f32_e32 v245, v57, v245\n" \
"v_mul_f32_e32 v246, v57, v246\n" \
"v_mfma_f32_32x32x16_bf16 v[224:239], a[216:219], a[216:219], 0\n" \
"v_mul_f32_e32 v247, v57, v247\n" \
"v_mul_f32_e32 v248, v57, v248\n" \
"v_mul_f32_e32 v249, v57, v249\n" \
"v_cvt_pk_bf16_f32 v242, v244, v245\n" \
"v_cvt_pk_bf16_f32 v243, v246, v247\n" \
"v_cvt_pk_bf16_f32 v248, v248, v249\n" \
"v_mul_f32_e32 v250, v57, v250\n" \
"buffer_store_dwordx4 v[240:243], v20, s[56:59], s71 offen offset:2048 sc1\n" \
"v_mul_f32_e32 v251, v57, v251\n" \
"v_mfma_f32_16x16x32_bf16 a[208:211], a[216:219], v[240:243], a[208:211]\n" \
"v_cvt_pk_bf16_f32 v249, v250, v251\n" \
"v_mul_f32_e32 v252, v57, v252\n" \
"v_mul_f32_e32 v253, v57, v253\n" \
"v_mul_f32_e32 v254, v57, v254\n" \
"v_mul_f32_e32 v255, v57, v255\n" \
"v_cvt_pk_bf16_f32 v250, v252, v253\n" \
"v_cvt_pk_bf16_f32 v251, v254, v255\n" \
"v_add_u32_e32 v53, 0x20000, v63\n" \
"buffer_store_dwordx4 v[248:251], v20, s[56:59], s71 offen offset:3072 sc1\n" \
"v_mfma_f32_16x16x32_bf16 a[208:211], a[216:219], v[248:251], a[208:211]\n" \
"v_exp_f32_e32 v70, v70\n" \
"v_mfma_f32_32x32x16_bf16 v[240:255], a[216:219], a[216:219], 0\n" \
"v_exp_f32_e32 v71, v71\n" \
"v_exp_f32_e32 v72, v72\n" \
"v_exp_f32_e32 v73, v73\n" \
"buffer_load_dwordx4 v4, s[84:87], s92 offen offset:0 lds\n" \
"s_add_u32 s64, s64, 0x10000\n" \
"v_cmp_lt_f32_e64 s[72:73], s21, v62\n" \
"v_accvgpr_read_b32 v63, a208\n" \
"v_cmp_u_f32_e32 vcc, v63, v63\n" \
"v_readlane_b32 s95, v11, 3\n" \
"v_exp_f32_e32 v68, v68\n" \
"v_exp_f32_e32 v69, v69\n" \
"v_exp_f32_e32 v74, v74\n" \
"buffer_load_dwordx4 v5, s[84:87], s92 offen offset:1024 lds\n" \
"v_exp_f32_e32 v75, v75\n" \
"s_or_b64 vcc, s[72:73], vcc\n" \
"s_cselect_b32 s95, 0x40000000, s95\n" \
"s_cselect_b32 s94, 0x7f800000, 0\n" \
"v_cvt_pk_bf16_f32 v71, v70, v71\n" \
"v_exp_f32_e32 v66, v66\n" \
"v_exp_f32_e32 v67, v67\n" \
"v_exp_f32_e32 v64, v64\n" \
"buffer_load_dwordx4 v6, s[84:87], s92 offen offset:2048 lds\n" \
"v_exp_f32_e32 v65, v65\n" \
"v_cvt_pk_bf16_f32 v72, v72, v73\n" \
"v_exp_f32_e32 v76, v76\n" \
"v_exp_f32_e32 v77, v77\n" \
"v_exp_f32_e32 v78, v78\n" \
"v_exp_f32_e32 v79, v79\n" \
"buffer_load_dwordx4 v7, s[84:87], s92 offen offset:3072 lds\n" \
"s_add_u32 m0, s91, 0x2000\n" \
"v_cvt_pk_bf16_f32 v70, v68, v69\n" \
"v_cvt_pk_bf16_f32 v73, v74, v75\n" \
"v_add_u32_e32 v49, 0x10000, v48\n" \
"s_xor_b32 s65, s65, 0x8000\n" \
"v_cvt_pk_bf16_f32 v69, v66, v67\n" \
"v_cvt_pk_bf16_f32 v68, v64, v65\n" \
"v_cvt_pk_bf16_f32 v74, v76, v77\n" \
"v_cvt_pk_bf16_f32 v75, v78, v79\n" \
"v_xor_b32_e32 v48, 0x8000, v48\n" \
"s_sub_u32 s66, s19, 2\n" \
"buffer_load_dwordx4 v8, s[84:87], s92 offen offset:0 lds\n" \
"s_waitcnt vmcnt(18) lgkmcnt(0)\n" \
"s_barrier\n" \
"s_branch L_f1_%=\n" \
".fill (L_ab + 32768 - .) / 4, 4, 0xBF800000\n" \
".fill (L_ab + 32768 - .) / 4, 4, 0xBF800000\n" \
"L_ap8:\n" \
"s_setpc_b64 s[88:89]\n" \
"L_out_%=:\n" \
"v_bfe_u32 v38, v20, 2, 4\n" \
"v_readlane_b32 s64, v11, 2\n" \
"v_add_u32_e32 v32, 0x49000, v20\n" \
"v_add_u32_e32 v34, 0x20000, v10\n" \
"ds_read_b64_tr_b16 a[0:1], v34 offset:0\n" \
"ds_read_b64_tr_b16 a[2:3], v34 offset:4608\n" \
"ds_read_b64_tr_b16 a[4:5], v34 offset:32\n" \
"buffer_load_dword v26, v38, s[56:59], s64 offen sc1\n" \
"ds_read_b64_tr_b16 a[6:7], v34 offset:4640\n" \
"ds_read_b64_tr_b16 a[8:9], v34 offset:64\n" \
"ds_read_b64_tr_b16 a[10:11], v34 offset:4672\n" \
"ds_read_b64_tr_b16 a[12:13], v34 offset:96\n" \
"buffer_store_dwordx4 v[64:67], v20, s[56:59], s61 offen offset:0 sc1\n" \
"ds_read_b64_tr_b16 a[14:15], v34 offset:4704\n" \
"v_mov_b32_e32 v39, s62\n" \
"v_readlane_b32 s70, v11, 5\n" \
"v_readlane_b32 s71, v11, 4\n" \
"buffer_store_dwordx4 v[68:71], v20, s[56:59], s61 offen offset:1024 sc1\n" \
"ds_read_b64_tr_b16 a[16:17], v34 offset:128\n" \
"ds_read_b64_tr_b16 a[18:19], v34 offset:4736\n" \
"ds_read_b64_tr_b16 a[20:21], v34 offset:160\n" \
"ds_read_b64_tr_b16 a[22:23], v34 offset:4768\n" \
"buffer_store_dwordx4 v[72:75], v20, s[56:59], s61 offen offset:2048 sc1\n" \
"ds_read_b64_tr_b16 a[24:25], v34 offset:192\n" \
"ds_read_b64_tr_b16 a[26:27], v34 offset:4800\n" \
"ds_read_b64_tr_b16 a[28:29], v34 offset:224\n" \
"ds_read_b64_tr_b16 a[30:31], v34 offset:4832\n" \
"buffer_store_dwordx4 v[76:79], v20, s[56:59], s61 offen offset:3072 sc1\n" \
"ds_read_b64_tr_b16 a[192:193], v34 offset:256\n" \
"ds_read_b64_tr_b16 a[194:195], v34 offset:4864\n" \
"ds_read_b64_tr_b16 a[196:197], v34 offset:9216\n" \
"ds_read_b64_tr_b16 a[198:199], v34 offset:13824\n" \
"buffer_store_dwordx4 v[80:83], v32, s[56:59], s61 offen offset:0 sc1\n" \
"ds_read_b64_tr_b16 a[200:201], v34 offset:9248\n" \
"ds_read_b64_tr_b16 a[202:203], v34 offset:13856\n" \
"ds_read_b64_tr_b16 a[204:205], v34 offset:9280\n" \
"ds_read_b64_tr_b16 a[206:207], v34 offset:13888\n" \
"buffer_store_dwordx4 v[84:87], v32, s[56:59], s61 offen offset:1024 sc1\n" \
"ds_read_b64_tr_b16 a[208:209], v34 offset:9312\n" \
"s_waitcnt vmcnt(16) lgkmcnt(15)\n" \
"v_mfma_f32_16x16x32_bf16 v[112:115], a[0:3], a[96:99], 0\n" \
"buffer_store_dwordx4 v[96:99], v32, s[56:59], s61 offen offset:2048 sc1\n" \
"v_mfma_f32_16x16x32_bf16 v[116:119], a[0:3], a[112:115], 0\n" \
"ds_read_b64_tr_b16 a[210:211], v34 offset:13920\n" \
"ds_read_b64_tr_b16 a[212:213], v34 offset:9344\n" \
"buffer_store_dwordx4 v[100:103], v32, s[56:59], s61 offen offset:3072 sc1\n" \
"v_mfma_f32_16x16x32_bf16 v[184:187], a[0:3], v[64:67], 0\n" \
"ds_read_b64_tr_b16 a[214:215], v34 offset:13952\n" \
"ds_read_b64_tr_b16 a[216:217], v34 offset:9376\n" \
"v_mfma_f32_16x16x32_bf16 v[188:191], a[0:3], v[80:83], 0\n" \
"ds_read_b64_tr_b16 a[218:219], v34 offset:13984\n" \
"ds_read_b64_tr_b16 a[0:1], v34 offset:9408\n" \
"v_mfma_f32_16x16x32_bf16 v[120:123], a[4:7], a[96:99], 0\n" \
"ds_read_b64_tr_b16 a[2:3], v34 offset:14016\n" \
"v_readlane_b32 s65, v11, 30\n" \
"v_mfma_f32_16x16x32_bf16 v[124:127], a[4:7], a[112:115], 0\n" \
"v_lshrrev_b32_e32 v44, 9, v20\n" \
"v_mul_u32_u24_e32 v44, 0xb40, v44\n" \
"v_mfma_f32_16x16x32_bf16 v[192:195], a[4:7], v[64:67], 0\n" \
"v_add_u32_e32 v35, v44, v10\n" \
"v_readlane_b32 s67, v11, 23\n" \
"v_mfma_f32_16x16x32_bf16 v[196:199], a[4:7], v[80:83], 0\n" \
"ds_read_b64_tr_b16 a[4:5], v34 offset:9440\n" \
"ds_read_b64_tr_b16 a[6:7], v34 offset:14048\n" \
"v_mfma_f32_16x16x32_bf16 v[128:131], a[8:11], a[96:99], 0\n" \
"v_readlane_b32 s80, v11, 11\n" \
"v_readlane_b32 s81, v11, 12\n" \
"v_mfma_f32_16x16x32_bf16 v[132:135], a[8:11], a[112:115], 0\n" \
"s_mov_b32 s82, 0x47fca0\n" \
"s_mov_b32 s83, 0x20000\n" \
"v_mfma_f32_16x16x32_bf16 v[200:203], a[8:11], v[64:67], 0\n" \
"v_readlane_b32 s66, v11, 28\n" \
"s_add_u32 m0, s67, 0x0\n" \
"v_mfma_f32_16x16x32_bf16 v[204:207], a[8:11], v[80:83], 0\n" \
"ds_read_b64_tr_b16 a[8:9], v34 offset:9472\n" \
"ds_read_b64_tr_b16 a[10:11], v34 offset:14080\n" \
"v_mfma_f32_16x16x32_bf16 v[136:139], a[12:15], a[96:99], 0\n" \
"buffer_load_dwordx4 v0, s[80:83], s66 offen offset:0 lds\n" \
"v_mfma_f32_16x16x32_bf16 v[140:143], a[12:15], a[112:115], 0\n" \
"v_readlane_b32 s92, v11, 24\n" \
"v_readlane_b32 s73, v11, 29\n" \
"v_mfma_f32_16x16x32_bf16 v[208:211], a[12:15], v[64:67], 0\n" \
"v_readlane_b32 s94, v11, 25\n" \
"v_readlane_b32 s84, v11, 9\n" \
"v_mfma_f32_16x16x32_bf16 v[212:215], a[12:15], v[80:83], 0\n" \
"ds_read_b64_tr_b16 a[12:13], v34 offset:18432\n" \
"ds_read_b64_tr_b16 a[14:15], v34 offset:23040\n" \
"v_mfma_f32_16x16x32_bf16 v[144:147], a[16:19], a[96:99], 0\n" \
"v_readlane_b32 s85, v11, 10\n" \
"s_mov_b32 s86, 0xa1ca0\n" \
"v_mfma_f32_16x16x32_bf16 v[148:151], a[16:19], a[112:115], 0\n" \
"buffer_load_dwordx4 v1, s[80:83], s66 offen offset:1024 lds\n" \
"v_mfma_f32_16x16x32_bf16 v[216:219], a[16:19], v[64:67], 0\n" \
"s_mov_b32 s87, 0x20000\n" \
"v_readlane_b32 s68, v11, 31\n" \
"v_mfma_f32_16x16x32_bf16 v[220:223], a[16:19], v[80:83], 0\n" \
"ds_read_b64_tr_b16 a[16:17], v34 offset:18464\n" \
"s_waitcnt lgkmcnt(7)\n" \
"v_mfma_f32_16x16x32_bf16 v[152:155], a[20:23], a[96:99], 0\n" \
"ds_read_b64_tr_b16 a[18:19], v34 offset:23072\n" \
"v_add_u32_e32 v33, 0x10000, v10\n" \
"v_mfma_f32_16x16x32_bf16 v[156:159], a[20:23], a[112:115], 0\n" \
"v_readlane_b32 s69, v11, 32\n" \
"v_readlane_b32 s72, v11, 34\n" \
"v_mfma_f32_16x16x32_bf16 v[224:227], a[20:23], v[64:67], 0\n" \
"buffer_load_dwordx4 v2, s[80:83], s66 offen offset:2048 lds\n" \
"v_mfma_f32_16x16x32_bf16 v[228:231], a[20:23], v[80:83], 0\n" \
"ds_read_b64_tr_b16 a[20:21], v34 offset:18496\n" \
"ds_read_b64_tr_b16 a[22:23], v34 offset:23104\n" \
"v_mfma_f32_16x16x32_bf16 v[160:163], a[24:27], a[96:99], 0\n" \
"v_readlane_b32 s93, v11, 35\n" \
"v_readlane_b32 s63, v11, 41\n" \
"v_mfma_f32_16x16x32_bf16 v[164:167], a[24:27], a[112:115], 0\n" \
"v_bfe_u32 v40, v20, 2, 7\n" \
"s_add_u32 s74, s63, 0x3800\n" \
"v_mfma_f32_16x16x32_bf16 v[232:235], a[24:27], v[64:67], 0\n" \
"v_lshlrev_b32_e32 v41, 12, v20\n" \
"s_add_u32 s12, s63, 0x0\n" \
"v_mfma_f32_16x16x32_bf16 v[236:239], a[24:27], v[80:83], 0\n" \
"ds_read_b64_tr_b16 a[24:25], v34 offset:18528\n" \
"ds_read_b64_tr_b16 a[26:27], v34 offset:23136\n" \
"v_mfma_f32_16x16x32_bf16 v[168:171], a[28:31], a[96:99], 0\n" \
"buffer_load_dwordx4 v3, s[80:83], s66 offen offset:3072 lds\n" \
"v_mfma_f32_16x16x32_bf16 v[172:175], a[28:31], a[112:115], 0\n" \
"s_add_u32 m0, s67, 0x1000\n" \
"v_readlane_b32 s95, v11, 38\n" \
"v_mfma_f32_16x16x32_bf16 v[240:243], a[28:31], v[64:67], 0\n" \
"v_readlane_b32 s96, v11, 39\n" \
"s_add_u32 s13, s63, 0x1000\n" \
"v_mfma_f32_16x16x32_bf16 v[244:247], a[28:31], v[80:83], 0\n" \
"ds_read_b64_tr_b16 a[28:29], v34 offset:18560\n" \
"ds_read_b64_tr_b16 a[30:31], v34 offset:23168\n" \
"v_mfma_f32_16x16x32_bf16 v[176:179], a[192:195], a[96:99], 0\n" \
"v_readlane_b32 s97, v11, 40\n" \
"s_add_u32 s14, s63, 0x2000\n" \
"v_mfma_f32_16x16x32_bf16 v[180:183], a[192:195], a[112:115], 0\n" \
"buffer_load_dwordx4 v4, s[80:83], s66 offen offset:0 lds\n" \
"v_mfma_f32_16x16x32_bf16 v[248:251], a[192:195], v[64:67], 0\n" \
"s_add_u32 s15, s63, 0x3000\n" \
"v_readlane_b32 s100, v11, 7\n" \
"v_mfma_f32_16x16x32_bf16 v[252:255], a[192:195], v[80:83], 0\n" \
"ds_read_b64_tr_b16 a[192:193], v34 offset:18592\n" \
"ds_read_b64_tr_b16 a[194:195], v34 offset:23200\n" \
"v_mfma_f32_16x16x32_bf16 v[112:115], a[196:199], a[100:103], v[112:115]\n" \
"v_readlane_b32 s16, v11, 42\n" \
"v_bfe_u32 v44, v20, 4, 4\n" \
"v_mfma_f32_16x16x32_bf16 v[116:119], a[196:199], a[116:119], v[116:119]\n" \
"s_add_u32 s17, s16, 0x1000\n" \
"s_add_u32 s18, s16, 0x2000\n" \
"v_mfma_f32_16x16x32_bf16 v[184:187], a[196:199], v[68:71], v[184:187]\n" \
"buffer_load_dwordx4 v5, s[80:83], s66 offen offset:1024 lds\n" \
"v_mfma_f32_16x16x32_bf16 v[188:191], a[196:199], v[84:87], v[188:191]\n" \
"ds_read_b64_tr_b16 a[196:197], v34 offset:18624\n" \
"ds_read_b64_tr_b16 a[198:199], v34 offset:23232\n" \
"v_mfma_f32_16x16x32_bf16 v[120:123], a[200:203], a[100:103], v[120:123]\n" \
"s_add_u32 s19, s16, 0x3000\n" \
"v_bfe_u32 v45, v20, 8, 2\n" \
"v_mfma_f32_16x16x32_bf16 v[124:127], a[200:203], a[116:119], v[124:127]\n" \
"v_mul_u32_u24_e32 v42, 0x480, v44\n" \
"v_lshl_add_u32 v42, v45, 3, v42\n" \
"v_mfma_f32_16x16x32_bf16 v[192:195], a[200:203], v[68:71], v[192:195]\n" \
"s_waitcnt vmcnt(14)\n" \
"v_cmp_ne_u32_e32 vcc, s62, v26\n" \
"v_mfma_f32_16x16x32_bf16 v[196:199], a[200:203], v[84:87], v[196:199]\n" \
"ds_read_b64_tr_b16 a[200:201], v34 offset:18656\n" \
"ds_read_b64_tr_b16 a[202:203], v34 offset:23264\n" \
"v_mfma_f32_16x16x32_bf16 v[128:131], a[204:207], a[100:103], v[128:131]\n" \
"buffer_load_dwordx4 v6, s[80:83], s66 offen offset:2048 lds\n" \
"v_mfma_f32_16x16x32_bf16 v[132:135], a[204:207], a[116:119], v[132:135]\n" \
"s_cbranch_vccnz L_sa_%=\n" \
"L_ra_%=:\n" \
"v_readlane_b32 s64, v11, 33\n" \
"v_mfma_f32_16x16x32_bf16 v[200:203], a[204:207], v[68:71], v[200:203]\n" \
"v_lshrrev_b32_e32 v44, 1, v44\n" \
"v_lshlrev_b32_e32 v45, 1, v45\n" \
"v_mfma_f32_16x16x32_bf16 v[204:207], a[204:207], v[84:87], v[204:207]\n" \
"ds_read_b64_tr_b16 a[204:205], v34 offset:18688\n" \
"ds_read_b64_tr_b16 a[206:207], v34 offset:23296\n" \
"v_mfma_f32_16x16x32_bf16 v[136:139], a[208:211], a[100:103], v[136:139]\n" \
"v_sub_u32_e32 v44, v44, v45\n" \
"v_and_b32_e32 v45, 16, v20\n" \
"v_mfma_f32_16x16x32_bf16 v[140:143], a[208:211], a[116:119], v[140:143]\n" \
"buffer_load_dwordx4 a[64:67], v20, s[56:59], s65 offen offset:0 sc1\n" \
"v_mfma_f32_16x16x32_bf16 v[208:211], a[208:211], v[68:71], v[208:211]\n" \
"v_mov_b32_e32 v46, 0x3f80\n" \
"v_lshlrev_b32_e32 v45, v45, v46\n" \
"v_mfma_f32_16x16x32_bf16 v[212:215], a[208:211], v[84:87], v[212:215]\n" \
"v_mov_b32_e32 v52, 0\n" \
"v_mov_b32_e32 v53, 0\n" \
"v_mfma_f32_16x16x32_bf16 v[144:147], a[212:215], a[100:103], v[144:147]\n" \
"v_mov_b32_e32 v48, 0\n" \
"v_mov_b32_e32 v49, 0\n" \
"v_mfma_f32_16x16x32_bf16 v[148:151], a[212:215], a[116:119], v[148:151]\n" \
"v_add_u32_e32 v43, 0x9000, v42\n" \
"v_readlane_b32 s88, v11, 13\n" \
"v_mfma_f32_16x16x32_bf16 v[216:219], a[212:215], v[68:71], v[216:219]\n" \
"buffer_load_dwordx4 a[80:83], v32, s[56:59], s65 offen offset:0 sc1\n" \
"v_mfma_f32_16x16x32_bf16 v[220:223], a[212:215], v[84:87], v[220:223]\n" \
"v_readlane_b32 s89, v11, 14\n" \
"s_mov_b32 s90, 0x240000\n" \
"v_mfma_f32_16x16x32_bf16 v[152:155], a[216:219], a[100:103], v[152:155]\n" \
"s_mov_b32 s91, 0x20000\n" \
"v_mov_b32_e32 v46, s62\n" \
"v_mfma_f32_16x16x32_bf16 v[156:159], a[216:219], a[116:119], v[156:159]\n" \
"v_mfma_f32_16x16x32_bf16 v[224:227], a[216:219], v[68:71], v[224:227]\n" \
"v_mfma_f32_16x16x32_bf16 v[228:231], a[216:219], v[84:87], v[228:231]\n" \
"buffer_load_dwordx4 a[68:71], v20, s[56:59], s65 offen offset:1024 sc1\n" \
"v_mfma_f32_16x16x32_bf16 v[160:163], a[0:3], a[100:103], v[160:163]\n" \
"v_mfma_f32_16x16x32_bf16 v[164:167], a[0:3], a[116:119], v[164:167]\n" \
"v_mfma_f32_16x16x32_bf16 v[232:235], a[0:3], v[68:71], v[232:235]\n" \
"v_mfma_f32_16x16x32_bf16 v[236:239], a[0:3], v[84:87], v[236:239]\n" \
"s_waitcnt lgkmcnt(0)\n" \
"v_mfma_f32_16x16x32_bf16 v[168:171], a[4:7], a[100:103], v[168:171]\n" \
"buffer_load_dwordx4 a[84:87], v32, s[56:59], s65 offen offset:1024 sc1\n" \
"v_mfma_f32_16x16x32_bf16 v[172:175], a[4:7], a[116:119], v[172:175]\n" \
"v_mfma_f32_16x16x32_bf16 v[240:243], a[4:7], v[68:71], v[240:243]\n" \
"v_mfma_f32_16x16x32_bf16 v[244:247], a[4:7], v[84:87], v[244:247]\n" \
"v_mfma_f32_16x16x32_bf16 v[176:179], a[8:11], a[100:103], v[176:179]\n" \
"v_mfma_f32_16x16x32_bf16 v[180:183], a[8:11], a[116:119], v[180:183]\n" \
"buffer_load_dwordx4 a[72:75], v20, s[56:59], s65 offen offset:2048 sc1\n" \
"v_mfma_f32_16x16x32_bf16 v[248:251], a[8:11], v[68:71], v[248:251]\n" \
"v_mfma_f32_16x16x32_bf16 v[252:255], a[8:11], v[84:87], v[252:255]\n" \
"v_mfma_f32_16x16x32_bf16 v[112:115], a[12:15], a[104:107], v[112:115]\n" \
"v_mfma_f32_16x16x32_bf16 v[116:119], a[12:15], a[120:123], v[116:119]\n" \
"v_mfma_f32_16x16x32_bf16 v[184:187], a[12:15], v[72:75], v[184:187]\n" \
"buffer_load_dwordx4 a[88:91], v32, s[56:59], s65 offen offset:2048 sc1\n" \
"v_mfma_f32_16x16x32_bf16 v[188:191], a[12:15], v[96:99], v[188:191]\n" \
"v_mfma_f32_16x16x32_bf16 v[120:123], a[16:19], a[104:107], v[120:123]\n" \
"v_mfma_f32_16x16x32_bf16 v[124:127], a[16:19], a[120:123], v[124:127]\n" \
"v_mfma_f32_16x16x32_bf16 v[192:195], a[16:19], v[72:75], v[192:195]\n" \
"v_mfma_f32_16x16x32_bf16 v[196:199], a[16:19], v[96:99], v[196:199]\n" \
"buffer_load_dwordx4 v7, s[80:83], s66 offen offset:3072 lds\n" \
"v_mfma_f32_16x16x32_bf16 v[128:131], a[20:23], a[104:107], v[128:131]\n" \
"s_add_u32 m0, s67, 0x2000\n" \
"v_readlane_b32 s67, v11, 48\n" \
"v_mfma_f32_16x16x32_bf16 v[132:135], a[20:23], a[120:123], v[132:135]\n" \
"v_mfma_f32_16x16x32_bf16 v[200:203], a[20:23], v[72:75], v[200:203]\n" \
"v_mfma_f32_16x16x32_bf16 v[204:207], a[20:23], v[96:99], v[204:207]\n" \
"s_barrier\n" \
"v_mfma_f32_16x16x32_bf16 v[136:139], a[24:27], a[104:107], v[136:139]\n" \
"ds_read_b64_tr_b16 a[208:209], v10 offset:32768\n" \
"ds_read_b64_tr_b16 a[210:211], v10 offset:37376\n" \
"v_mfma_f32_16x16x32_bf16 v[140:143], a[24:27], a[120:123], v[140:143]\n" \
"ds_read_b64_tr_b16 a[212:213], v10 offset:32800\n" \
"ds_read_b64_tr_b16 a[214:215], v10 offset:37408\n" \
"v_mfma_f32_16x16x32_bf16 v[208:211], a[24:27], v[72:75], v[208:211]\n" \
"ds_read_b64_tr_b16 a[216:217], v10 offset:32832\n" \
"ds_read_b64_tr_b16 a[218:219], v10 offset:37440\n" \
"v_mfma_f32_16x16x32_bf16 v[212:215], a[24:27], v[96:99], v[212:215]\n" \
"ds_read_b64_tr_b16 a[0:1], v10 offset:32864\n" \
"ds_read_b64_tr_b16 a[2:3], v10 offset:37472\n" \
"v_mfma_f32_16x16x32_bf16 v[144:147], a[28:31], a[104:107], v[144:147]\n" \
"ds_read_b64_tr_b16 a[4:5], v10 offset:32896\n" \
"ds_read_b64_tr_b16 a[6:7], v10 offset:37504\n" \
"v_mfma_f32_16x16x32_bf16 v[148:151], a[28:31], a[120:123], v[148:151]\n" \
"ds_read_b64_tr_b16 a[8:9], v10 offset:32928\n" \
"ds_read_b64_tr_b16 a[10:11], v10 offset:37536\n" \
"v_mfma_f32_16x16x32_bf16 v[216:219], a[28:31], v[72:75], v[216:219]\n" \
"ds_read_b64_tr_b16 a[12:13], v10 offset:32960\n" \
"ds_read_b64_tr_b16 a[14:15], v10 offset:37568\n" \
"v_mfma_f32_16x16x32_bf16 v[220:223], a[28:31], v[96:99], v[220:223]\n" \
"ds_read_b64_tr_b16 a[16:17], v10 offset:32992\n" \
"ds_read_b64_tr_b16 a[18:19], v10 offset:37600\n" \
"v_mfma_f32_16x16x32_bf16 v[152:155], a[192:195], a[104:107], v[152:155]\n" \
"ds_read_b64_tr_b16 a[20:21], v10 offset:33024\n" \
"ds_read_b64_tr_b16 a[22:23], v10 offset:37632\n" \
"v_mfma_f32_16x16x32_bf16 v[156:159], a[192:195], a[120:123], v[156:159]\n" \
"ds_read_b64_tr_b16 a[24:25], v35 offset:41984\n" \
"ds_read_b64_tr_b16 a[26:27], v35 offset:44288\n" \
"v_mfma_f32_16x16x32_bf16 v[224:227], a[192:195], v[72:75], v[224:227]\n" \
"ds_read_b64_tr_b16 a[28:29], v35 offset:42016\n" \
"ds_read_b64_tr_b16 a[30:31], v35 offset:44320\n" \
"v_mfma_f32_16x16x32_bf16 v[228:231], a[192:195], v[96:99], v[228:231]\n" \
"ds_read_b64_tr_b16 a[192:193], v35 offset:42048\n" \
"ds_read_b64_tr_b16 a[194:195], v35 offset:44352\n" \
"v_mfma_f32_16x16x32_bf16 v[160:163], a[196:199], a[104:107], v[160:163]\n" \
"buffer_load_dwordx4 v8, s[80:83], s66 offen offset:0 lds\n" \
"v_mfma_f32_16x16x32_bf16 v[164:167], a[196:199], a[120:123], v[164:167]\n" \
"s_add_u32 m0, s92, 0x0\n" \
"v_readlane_b32 s66, v11, 47\n" \
"v_mfma_f32_16x16x32_bf16 v[232:235], a[196:199], v[72:75], v[232:235]\n" \
"v_mfma_f32_16x16x32_bf16 v[236:239], a[196:199], v[96:99], v[236:239]\n" \
"ds_read_b64_tr_b16 a[196:197], v35 offset:42080\n" \
"ds_read_b64_tr_b16 a[198:199], v35 offset:44384\n" \
"v_mfma_f32_16x16x32_bf16 v[168:171], a[200:203], a[104:107], v[168:171]\n" \
"v_mfma_f32_16x16x32_bf16 v[172:175], a[200:203], a[120:123], v[172:175]\n" \
"buffer_load_dwordx4 v0, s[80:83], s73 offen offset:0 lds\n" \
"v_mfma_f32_16x16x32_bf16 v[240:243], a[200:203], v[72:75], v[240:243]\n" \
"v_mfma_f32_16x16x32_bf16 v[244:247], a[200:203], v[96:99], v[244:247]\n" \
"ds_read_b64_tr_b16 a[200:201], v35 offset:42112\n" \
"ds_read_b64_tr_b16 a[202:203], v35 offset:44416\n" \
"v_mfma_f32_16x16x32_bf16 v[176:179], a[204:207], a[104:107], v[176:179]\n" \
"v_mfma_f32_16x16x32_bf16 v[180:183], a[204:207], a[120:123], v[180:183]\n" \
"v_mfma_f32_16x16x32_bf16 v[248:251], a[204:207], v[72:75], v[248:251]\n" \
"buffer_load_dwordx4 v1, s[80:83], s73 offen offset:1024 lds\n" \
"v_mfma_f32_16x16x32_bf16 v[252:255], a[204:207], v[96:99], v[252:255]\n" \
"ds_read_b64_tr_b16 a[204:205], v35 offset:42144\n" \
"s_waitcnt lgkmcnt(9)\n" \
"v_mfma_f32_16x16x32_bf16 v[112:115], a[208:211], a[108:111], v[112:115]\n" \
"ds_read_b64_tr_b16 a[206:207], v35 offset:44448\n" \
"v_mfma_f32_16x16x32_bf16 v[116:119], a[208:211], a[124:127], v[116:119]\n" \
"v_mfma_f32_16x16x32_bf16 v[184:187], a[208:211], v[76:79], v[184:187]\n" \
"v_mfma_f32_16x16x32_bf16 v[188:191], a[208:211], v[100:103], v[188:191]\n" \
"ds_read_b64_tr_b16 a[208:209], v35 offset:42176\n" \
"ds_read_b64_tr_b16 a[210:211], v35 offset:44480\n" \
"v_mfma_f32_16x16x32_bf16 v[120:123], a[212:215], a[108:111], v[120:123]\n" \
"buffer_load_dwordx4 v2, s[80:83], s73 offen offset:2048 lds\n" \
"v_mfma_f32_16x16x32_bf16 v[124:127], a[212:215], a[124:127], v[124:127]\n" \
"v_mfma_f32_16x16x32_bf16 v[192:195], a[212:215], v[76:79], v[192:195]\n" \
"s_waitcnt vmcnt(18)\n" \
"s_mov_b64 exec, 1\n" \
"buffer_store_dword v39, off, s[56:59], s70 offset:0 sc1\n" \
"s_mov_b64 exec, -1\n" \
"v_mfma_f32_16x16x32_bf16 v[196:199], a[212:215], v[100:103], v[196:199]\n" \
"ds_read_b64_tr_b16 a[212:213], v35 offset:42208\n" \
"ds_read_b64_tr_b16 a[214:215], v35 offset:44512\n" \
"v_mfma_f32_16x16x32_bf16 v[128:131], a[216:219], a[108:111], v[128:131]\n" \
"v_readlane_b32 s70, v11, 51\n" \
"v_mfma_f32_16x16x32_bf16 v[132:135], a[216:219], a[124:127], v[132:135]\n" \
"v_mfma_f32_16x16x32_bf16 v[200:203], a[216:219], v[76:79], v[200:203]\n" \
"buffer_load_dwordx4 v3, s[80:83], s73 offen offset:3072 lds\n" \
"v_mfma_f32_16x16x32_bf16 v[204:207], a[216:219], v[100:103], v[204:207]\n" \
"ds_read_b64_tr_b16 a[216:217], v35 offset:42240\n" \
"ds_read_b64_tr_b16 a[218:219], v35 offset:44544\n" \
"v_mfma_f32_16x16x32_bf16 v[136:139], a[0:3], a[108:111], v[136:139]\n" \
"s_add_u32 m0, s92, 0x1000\n" \
"v_mfma_f32_16x16x32_bf16 v[140:143], a[0:3], a[124:127], v[140:143]\n" \
"v_mfma_f32_16x16x32_bf16 v[208:211], a[0:3], v[76:79], v[208:211]\n" \
"v_mfma_f32_16x16x32_bf16 v[212:215], a[0:3], v[100:103], v[212:215]\n" \
"ds_read_b64_tr_b16 a[0:1], v35 offset:52352\n" \
"ds_read_b64_tr_b16 a[2:3], v35 offset:54656\n" \
"v_mfma_f32_16x16x32_bf16 v[144:147], a[4:7], a[108:111], v[144:147]\n" \
"buffer_load_dwordx4 v4, s[80:83], s73 offen offset:0 lds\n" \
"v_mfma_f32_16x16x32_bf16 v[148:151], a[4:7], a[124:127], v[148:151]\n" \
"v_mfma_f32_16x16x32_bf16 v[216:219], a[4:7], v[76:79], v[216:219]\n" \
"v_mfma_f32_16x16x32_bf16 v[220:223], a[4:7], v[100:103], v[220:223]\n" \
"ds_read_b64_tr_b16 a[4:5], v35 offset:52384\n" \
"ds_read_b64_tr_b16 a[6:7], v35 offset:54688\n" \
"v_mfma_f32_16x16x32_bf16 v[152:155], a[8:11], a[108:111], v[152:155]\n" \
"v_mfma_f32_16x16x32_bf16 v[156:159], a[8:11], a[124:127], v[156:159]\n" \
"buffer_load_dwordx4 v5, s[80:83], s73 offen offset:1024 lds\n" \
"v_mfma_f32_16x16x32_bf16 v[224:227], a[8:11], v[76:79], v[224:227]\n" \
"v_mfma_f32_16x16x32_bf16 v[228:231], a[8:11], v[100:103], v[228:231]\n" \
"ds_read_b64_tr_b16 a[8:9], v35 offset:52416\n" \
"ds_read_b64_tr_b16 a[10:11], v35 offset:54720\n" \
"v_mfma_f32_16x16x32_bf16 v[160:163], a[12:15], a[108:111], v[160:163]\n" \
"v_mfma_f32_16x16x32_bf16 v[164:167], a[12:15], a[124:127], v[164:167]\n" \
"v_mfma_f32_16x16x32_bf16 v[232:235], a[12:15], v[76:79], v[232:235]\n" \
"buffer_load_dwordx4 v6, s[80:83], s73 offen offset:2048 lds\n" \
"v_mfma_f32_16x16x32_bf16 v[236:239], a[12:15], v[100:103], v[236:239]\n" \
"ds_read_b64_tr_b16 a[12:13], v35 offset:52448\n" \
"ds_read_b64_tr_b16 a[14:15], v35 offset:54752\n" \
"v_mfma_f32_16x16x32_bf16 v[168:171], a[16:19], a[108:111], v[168:171]\n" \
"v_mfma_f32_16x16x32_bf16 v[172:175], a[16:19], a[124:127], v[172:175]\n" \
"v_mfma_f32_16x16x32_bf16 v[240:243], a[16:19], v[76:79], v[240:243]\n" \
"v_mfma_f32_16x16x32_bf16 v[244:247], a[16:19], v[100:103], v[244:247]\n" \
"buffer_load_dwordx4 v7, s[80:83], s73 offen offset:3072 lds\n" \
"v_mfma_f32_16x16x32_bf16 v[176:179], a[20:23], a[108:111], v[176:179]\n" \
"s_add_u32 m0, s92, 0x2000\n" \
"ds_read_b64_tr_b16 a[16:17], v35 offset:52480\n" \
"v_mfma_f32_16x16x32_bf16 v[180:183], a[20:23], a[124:127], v[180:183]\n" \
"ds_read_b64_tr_b16 a[18:19], v35 offset:54784\n" \
"v_readlane_b32 s92, v11, 46\n" \
"v_mfma_f32_16x16x32_bf16 v[248:251], a[20:23], v[76:79], v[248:251]\n" \
"v_mfma_f32_16x16x32_bf16 v[252:255], a[20:23], v[100:103], v[252:255]\n" \
"ds_read_b64_tr_b16 a[20:21], v35 offset:52512\n" \
"ds_read_b64_tr_b16 a[22:23], v35 offset:54816\n" \
"v_mfma_f32_16x16x32_bf16 v[112:115], a[24:27], a[220:223], v[112:115]\n" \
"buffer_load_dwordx4 v8, s[80:83], s73 offen offset:0 lds\n" \
"v_mfma_f32_16x16x32_bf16 v[116:119], a[24:27], a[228:231], v[116:119]\n" \
"s_mov_b32 m0, s94\n" \
"v_readlane_b32 s73, v11, 6\n" \
"v_mfma_f32_16x16x32_bf16 v[184:187], a[24:27], a[236:239], v[184:187]\n" \
"v_mfma_f32_16x16x32_bf16 v[188:191], a[24:27], a[244:247], v[188:191]\n" \
"ds_read_b64_tr_b16 a[24:25], v35 offset:52544\n" \
"s_waitcnt lgkmcnt(7)\n" \
"v_mfma_f32_16x16x32_bf16 v[120:123], a[28:31], a[220:223], v[120:123]\n" \
"ds_read_b64_tr_b16 a[26:27], v35 offset:54848\n" \
"buffer_load_dword v47, v38, s[56:59], s71 offen sc1\n" \
"v_mfma_f32_16x16x32_bf16 v[124:127], a[28:31], a[228:231], v[124:127]\n" \
"v_mfma_f32_16x16x32_bf16 v[192:195], a[28:31], a[236:239], v[192:195]\n" \
"v_mfma_f32_16x16x32_bf16 v[196:199], a[28:31], a[244:247], v[196:199]\n" \
"ds_read_b64_tr_b16 a[28:29], v35 offset:52576\n" \
"ds_read_b64_tr_b16 a[30:31], v35 offset:54880\n" \
"v_mfma_f32_16x16x32_bf16 v[128:131], a[192:195], a[220:223], v[128:131]\n" \
"v_mfma_f32_16x16x32_bf16 v[132:135], a[192:195], a[228:231], v[132:135]\n" \
"buffer_load_dwordx4 v9, s[84:87], 0 offen lds\n" \
"v_mfma_f32_16x16x32_bf16 v[200:203], a[192:195], a[236:239], v[200:203]\n" \
"v_mfma_f32_16x16x32_bf16 v[204:207], a[192:195], a[244:247], v[204:207]\n" \
"ds_read_b64_tr_b16 a[192:193], v35 offset:52608\n" \
"ds_read_b64_tr_b16 a[194:195], v35 offset:54912\n" \
"v_mfma_f32_16x16x32_bf16 v[136:139], a[196:199], a[220:223], v[136:139]\n" \
"v_mfma_f32_16x16x32_bf16 v[140:143], a[196:199], a[228:231], v[140:143]\n" \
"v_mfma_f32_16x16x32_bf16 v[208:211], a[196:199], a[236:239], v[208:211]\n" \
"buffer_load_dwordx4 a[76:79], v20, s[56:59], s65 offen offset:3072 sc1\n" \
"v_mfma_f32_16x16x32_bf16 v[212:215], a[196:199], a[244:247], v[212:215]\n" \
"v_mfma_f32_16x16x32_bf16 v[144:147], a[200:203], a[220:223], v[144:147]\n" \
"v_mfma_f32_16x16x32_bf16 v[148:151], a[200:203], a[228:231], v[148:151]\n" \
"v_mfma_f32_16x16x32_bf16 v[216:219], a[200:203], a[236:239], v[216:219]\n" \
"v_mfma_f32_16x16x32_bf16 v[220:223], a[200:203], a[244:247], v[220:223]\n" \
"buffer_load_dwordx4 a[92:95], v32, s[56:59], s65 offen offset:3072 sc1\n" \
"v_mfma_f32_16x16x32_bf16 v[152:155], a[204:207], a[220:223], v[152:155]\n" \
"v_readlane_b32 s65, v11, 46\n" \
"v_mfma_f32_16x16x32_bf16 v[156:159], a[204:207], a[228:231], v[156:159]\n" \
"v_mfma_f32_16x16x32_bf16 v[224:227], a[204:207], a[236:239], v[224:227]\n" \
"v_mfma_f32_16x16x32_bf16 v[228:231], a[204:207], a[244:247], v[228:231]\n" \
"v_mfma_f32_16x16x32_bf16 v[160:163], a[208:211], a[220:223], v[160:163]\n" \
"buffer_load_dwordx4 a[128:131], v20, s[56:59], s68 offen offset:0 sc1\n" \
"v_mfma_f32_16x16x32_bf16 v[164:167], a[208:211], a[228:231], v[164:167]\n" \
"v_mfma_f32_16x16x32_bf16 v[232:235], a[208:211], a[236:239], v[232:235]\n" \
"v_mfma_f32_16x16x32_bf16 v[236:239], a[208:211], a[244:247], v[236:239]\n" \
"v_mfma_f32_16x16x32_bf16 v[168:171], a[212:215], a[220:223], v[168:171]\n" \
"v_mfma_f32_16x16x32_bf16 v[172:175], a[212:215], a[228:231], v[172:175]\n" \
"buffer_load_dwordx4 a[144:147], v32, s[56:59], s68 offen offset:0 sc1\n" \
"v_mfma_f32_16x16x32_bf16 v[240:243], a[212:215], a[236:239], v[240:243]\n" \
"v_mfma_f32_16x16x32_bf16 v[244:247], a[212:215], a[244:247], v[244:247]\n" \
"v_mfma_f32_16x16x32_bf16 v[176:179], a[216:219], a[220:223], v[176:179]\n" \
"v_mfma_f32_16x16x32_bf16 v[180:183], a[216:219], a[228:231], v[180:183]\n" \
"v_mfma_f32_16x16x32_bf16 v[248:251], a[216:219], a[236:239], v[248:251]\n" \
"buffer_load_dwordx4 a[132:135], v20, s[56:59], s68 offen offset:1024 sc1\n" \
"v_mfma_f32_16x16x32_bf16 v[252:255], a[216:219], a[244:247], v[252:255]\n" \
"v_mfma_f32_16x16x32_bf16 v[112:115], a[0:3], a[224:227], v[112:115]\n" \
"v_mfma_f32_16x16x32_bf16 v[116:119], a[0:3], a[232:235], v[116:119]\n" \
"v_mfma_f32_16x16x32_bf16 v[184:187], a[0:3], a[240:243], v[184:187]\n" \
"v_mfma_f32_16x16x32_bf16 v[188:191], a[0:3], a[248:251], v[188:191]\n" \
"buffer_load_dwordx4 a[148:151], v32, s[56:59], s68 offen offset:1024 sc1\n" \
"v_mfma_f32_16x16x32_bf16 v[120:123], a[4:7], a[224:227], v[120:123]\n" \
"v_mfma_f32_16x16x32_bf16 v[124:127], a[4:7], a[232:235], v[124:127]\n" \
"v_mfma_f32_16x16x32_bf16 v[192:195], a[4:7], a[240:243], v[192:195]\n" \
"v_mfma_f32_16x16x32_bf16 v[196:199], a[4:7], a[248:251], v[196:199]\n" \
"v_mfma_f32_16x16x32_bf16 v[128:131], a[8:11], a[224:227], v[128:131]\n" \
"buffer_load_dwordx4 a[136:139], v20, s[56:59], s68 offen offset:2048 sc1\n" \
"v_mfma_f32_16x16x32_bf16 v[132:135], a[8:11], a[232:235], v[132:135]\n" \
"v_mfma_f32_16x16x32_bf16 v[200:203], a[8:11], a[240:243], v[200:203]\n" \
"v_mfma_f32_16x16x32_bf16 v[204:207], a[8:11], a[248:251], v[204:207]\n" \
"s_waitcnt lgkmcnt(0)\n" \
"v_mfma_f32_16x16x32_bf16 v[136:139], a[12:15], a[224:227], v[136:139]\n" \
"v_mfma_f32_16x16x32_bf16 v[140:143], a[12:15], a[232:235], v[140:143]\n" \
"buffer_load_dwordx4 a[152:155], v32, s[56:59], s68 offen offset:2048 sc1\n" \
"v_mfma_f32_16x16x32_bf16 v[208:211], a[12:15], a[240:243], v[208:211]\n" \
"v_mfma_f32_16x16x32_bf16 v[212:215], a[12:15], a[248:251], v[212:215]\n" \
"s_barrier\n" \
"v_mfma_f32_16x16x32_bf16 v[144:147], a[16:19], a[224:227], v[144:147]\n" \
"ds_read_b64_tr_b16 a[196:197], v33 offset:0\n" \
"ds_read_b64_tr_b16 a[198:199], v33 offset:4608\n" \
"v_mfma_f32_16x16x32_bf16 v[148:151], a[16:19], a[232:235], v[148:151]\n" \
"buffer_load_dwordx4 a[140:143], v20, s[56:59], s68 offen offset:3072 sc1\n" \
"v_mfma_f32_16x16x32_bf16 v[216:219], a[16:19], a[240:243], v[216:219]\n" \
"ds_read_b64_tr_b16 a[200:201], v33 offset:32\n" \
"ds_read_b64_tr_b16 a[202:203], v33 offset:4640\n" \
"v_mfma_f32_16x16x32_bf16 v[220:223], a[16:19], a[248:251], v[220:223]\n" \
"ds_read_b64_tr_b16 a[204:205], v33 offset:64\n" \
"ds_read_b64_tr_b16 a[206:207], v33 offset:4672\n" \
"v_mfma_f32_16x16x32_bf16 v[152:155], a[20:23], a[224:227], v[152:155]\n" \
"ds_read_b64_tr_b16 a[208:209], v33 offset:96\n" \
"ds_read_b64_tr_b16 a[210:211], v33 offset:4704\n" \
"v_mfma_f32_16x16x32_bf16 v[156:159], a[20:23], a[232:235], v[156:159]\n" \
"ds_read_b64_tr_b16 a[212:213], v33 offset:128\n" \
"ds_read_b64_tr_b16 a[214:215], v33 offset:4736\n" \
"v_mfma_f32_16x16x32_bf16 v[224:227], a[20:23], a[240:243], v[224:227]\n" \
"buffer_load_dwordx4 a[156:159], v32, s[56:59], s68 offen offset:3072 sc1\n" \
"v_mfma_f32_16x16x32_bf16 v[228:231], a[20:23], a[248:251], v[228:231]\n" \
"ds_read_b64_tr_b16 a[216:217], v33 offset:160\n" \
"ds_read_b64_tr_b16 a[218:219], v33 offset:4768\n" \
"v_mfma_f32_16x16x32_bf16 v[160:163], a[24:27], a[224:227], v[160:163]\n" \
"ds_read_b64_tr_b16 a[0:1], v33 offset:192\n" \
"ds_read_b64_tr_b16 a[2:3], v33 offset:4800\n" \
"v_mfma_f32_16x16x32_bf16 v[164:167], a[24:27], a[232:235], v[164:167]\n" \
"ds_read_b64_tr_b16 a[4:5], v33 offset:224\n" \
"ds_read_b64_tr_b16 a[6:7], v33 offset:4832\n" \
"v_mfma_f32_16x16x32_bf16 v[232:235], a[24:27], a[240:243], v[232:235]\n" \
"ds_read_b64_tr_b16 a[8:9], v33 offset:256\n" \
"ds_read_b64_tr_b16 a[10:11], v33 offset:4864\n" \
"buffer_load_dwordx4 a[32:35], v20, s[56:59], s69 offen offset:0 sc1\n" \
"v_mfma_f32_16x16x32_bf16 v[236:239], a[24:27], a[248:251], v[236:239]\n" \
"ds_read_b64_tr_b16 a[12:13], v33 offset:9216\n" \
"ds_read_b64_tr_b16 a[14:15], v33 offset:13824\n" \
"v_mfma_f32_16x16x32_bf16 v[168:171], a[28:31], a[224:227], v[168:171]\n" \
"ds_read_b64_tr_b16 a[16:17], v33 offset:9248\n" \
"ds_read_b64_tr_b16 a[18:19], v33 offset:13856\n" \
"v_mfma_f32_16x16x32_bf16 v[172:175], a[28:31], a[232:235], v[172:175]\n" \
"ds_read_b64_tr_b16 a[20:21], v33 offset:9280\n" \
"ds_read_b64_tr_b16 a[22:23], v33 offset:13888\n" \
"v_mfma_f32_16x16x32_bf16 v[240:243], a[28:31], a[240:243], v[240:243]\n" \
"ds_read_b64_tr_b16 a[24:25], v33 offset:9312\n" \
"ds_read_b64_tr_b16 a[26:27], v33 offset:13920\n" \
"buffer_load_dwordx4 a[48:51], v32, s[56:59], s69 offen offset:0 sc1\n" \
"v_mfma_f32_16x16x32_bf16 v[244:247], a[28:31], a[248:251], v[244:247]\n" \
"ds_read_b64_tr_b16 a[28:29], v33 offset:9344\n" \
"ds_read_b64_tr_b16 a[30:31], v33 offset:13952\n" \
"v_mfma_f32_16x16x32_bf16 v[176:179], a[192:195], a[224:227], v[176:179]\n" \
"v_readlane_b32 s68, v11, 49\n" \
"s_waitcnt vmcnt(13)\n" \
"v_cmp_ne_u32_e32 vcc, s62, v47\n" \
"v_mfma_f32_16x16x32_bf16 v[180:183], a[192:195], a[232:235], v[180:183]\n" \
"v_mfma_f32_16x16x32_bf16 v[248:251], a[192:195], a[240:243], v[248:251]\n" \
"buffer_load_dwordx4 a[36:39], v20, s[56:59], s69 offen offset:1024 sc1\n" \
"v_mfma_f32_16x16x32_bf16 v[252:255], a[192:195], a[248:251], v[252:255]\n" \
"ds_read_b64_tr_b16 a[192:193], v33 offset:9376\n" \
"s_waitcnt lgkmcnt(15)\n" \
"v_mfma_f32_16x16x32_bf16 v[112:115], a[196:199], a[64:67], v[112:115]\n" \
"ds_read_b64_tr_b16 a[194:195], v33 offset:13984\n" \
"s_cbranch_vccnz L_sb_%=\n" \
"L_rb_%=:\n" \
"v_mfma_f32_16x16x32_bf16 v[116:119], a[196:199], a[80:83], v[116:119]\n" \
"ds_read_b64_tr_b16 a[196:197], v33 offset:9408\n" \
"ds_read_b64_tr_b16 a[198:199], v33 offset:14016\n" \
"v_mfma_f32_16x16x32_bf16 v[120:123], a[200:203], a[64:67], v[120:123]\n" \
"v_readlane_b32 s71, v11, 52\n" \
"v_cmp_eq_u32_e32 vcc, 0, v44\n" \
"buffer_load_dwordx4 a[52:55], v32, s[56:59], s69 offen offset:1024 sc1\n" \
"v_mfma_f32_16x16x32_bf16 v[124:127], a[200:203], a[80:83], v[124:127]\n" \
"ds_read_b64_tr_b16 a[200:201], v33 offset:9440\n" \
"ds_read_b64_tr_b16 a[202:203], v33 offset:14048\n" \
"v_mfma_f32_16x16x32_bf16 v[128:131], a[204:207], a[64:67], v[128:131]\n" \
"v_cndmask_b32_e32 v50, 0, v45, vcc\n" \
"v_cmp_eq_u32_e32 vcc, 1, v44\n" \
"v_mfma_f32_16x16x32_bf16 v[132:135], a[204:207], a[80:83], v[132:135]\n" \
"ds_read_b64_tr_b16 a[204:205], v33 offset:9472\n" \
"ds_read_b64_tr_b16 a[206:207], v33 offset:14080\n" \
"v_mfma_f32_16x16x32_bf16 v[136:139], a[208:211], a[64:67], v[136:139]\n" \
"v_cndmask_b32_e32 v51, 0, v45, vcc\n" \
"v_bfe_u32 v45, v20, 1, 5\n" \
"buffer_load_dwordx4 a[40:43], v20, s[56:59], s69 offen offset:2048 sc1\n" \
"v_mfma_f32_16x16x32_bf16 v[140:143], a[208:211], a[80:83], v[140:143]\n" \
"ds_read_b64_tr_b16 a[208:209], v33 offset:18432\n" \
"ds_read_b64_tr_b16 a[210:211], v33 offset:23040\n" \
"v_mfma_f32_16x16x32_bf16 v[144:147], a[212:215], a[64:67], v[144:147]\n" \
"v_and_b32_e32 v45, 24, v45\n" \
"v_and_b32_e32 v44, 0x3c0, v20\n" \
"v_mfma_f32_16x16x32_bf16 v[148:151], a[212:215], a[80:83], v[148:151]\n" \
"ds_read_b64_tr_b16 a[212:213], v33 offset:18464\n" \
"ds_read_b64_tr_b16 a[214:215], v33 offset:23072\n" \
"v_mfma_f32_16x16x32_bf16 v[152:155], a[216:219], a[64:67], v[152:155]\n" \
".fill (L_ab + 36864 - .) / 4, 4, 0xBF800000\n" \
"L_ap9:\n" \
"s_cbranch_execz L_ap0\n" \
"v_add_u32_e32 v45, 0x4800, v45\n" \
"v_add_u32_e32 v36, v44, v45\n" \
"buffer_load_dwordx4 a[56:59], v32, s[56:59], s69 offen offset:2048 sc1\n" \
"v_mfma_f32_16x16x32_bf16 v[156:159], a[216:219], a[80:83], v[156:159]\n" \
"ds_read_b64_tr_b16 a[216:217], v33 offset:18496\n" \
"ds_read_b64_tr_b16 a[218:219], v33 offset:23104\n" \
"v_mfma_f32_16x16x32_bf16 v[160:163], a[0:3], a[64:67], v[160:163]\n" \
"v_mfma_f32_16x16x32_bf16 v[164:167], a[0:3], a[80:83], v[164:167]\n" \
"ds_read_b64_tr_b16 a[0:1], v33 offset:18528\n" \
"s_waitcnt lgkmcnt(13)\n" \
"v_mfma_f32_16x16x32_bf16 v[168:171], a[4:7], a[64:67], v[168:171]\n" \
"ds_read_b64_tr_b16 a[2:3], v33 offset:23136\n" \
"buffer_load_dwordx4 a[44:47], v20, s[56:59], s69 offen offset:3072 sc1\n" \
"v_mfma_f32_16x16x32_bf16 v[172:175], a[4:7], a[80:83], v[172:175]\n" \
"ds_read_b64_tr_b16 a[4:5], v33 offset:18560\n" \
"ds_read_b64_tr_b16 a[6:7], v33 offset:23168\n" \
"v_mfma_f32_16x16x32_bf16 v[176:179], a[8:11], a[64:67], v[176:179]\n" \
"v_mfma_f32_16x16x32_bf16 v[180:183], a[8:11], a[80:83], v[180:183]\n" \
"ds_read_b64_tr_b16 a[8:9], v33 offset:18592\n" \
"ds_read_b64_tr_b16 a[10:11], v33 offset:23200\n" \
"v_mfma_f32_16x16x32_bf16 v[112:115], a[12:15], a[68:71], v[112:115]\n" \
"v_mfma_f32_16x16x32_bf16 v[116:119], a[12:15], a[84:87], v[116:119]\n" \
"buffer_load_dwordx4 a[60:63], v32, s[56:59], s69 offen offset:3072 sc1\n" \
"v_mfma_f32_16x16x32_bf16 v[120:123], a[16:19], a[68:71], v[120:123]\n" \
"ds_read_b64_tr_b16 a[12:13], v33 offset:18624\n" \
"ds_read_b64_tr_b16 a[14:15], v33 offset:23232\n" \
"v_mfma_f32_16x16x32_bf16 v[124:127], a[16:19], a[84:87], v[124:127]\n" \
"ds_read_b64_tr_b16 a[16:17], v33 offset:18656\n" \
"ds_read_b64_tr_b16 a[18:19], v33 offset:23264\n" \
"v_mfma_f32_16x16x32_bf16 v[128:131], a[20:23], a[68:71], v[128:131]\n" \
"v_readlane_b32 s69, v11, 50\n" \
"v_mfma_f32_16x16x32_bf16 v[132:135], a[20:23], a[84:87], v[132:135]\n" \
"ds_read_b64_tr_b16 a[20:21], v33 offset:18688\n" \
"ds_read_b64_tr_b16 a[22:23], v33 offset:23296\n" \
"v_mfma_f32_16x16x32_bf16 v[136:139], a[24:27], a[68:71], v[136:139]\n" \
"buffer_load_dwordx4 a[220:223], v20, s[56:59], s64 offen offset:0 sc1\n" \
"v_mfma_f32_16x16x32_bf16 v[140:143], a[24:27], a[84:87], v[140:143]\n" \
"ds_read_b64_tr_b16 a[24:25], v33 offset:27648\n" \
"ds_read_b64_tr_b16 a[26:27], v33 offset:32256\n" \
"v_mfma_f32_16x16x32_bf16 v[144:147], a[28:31], a[68:71], v[144:147]\n" \
"v_mfma_f32_16x16x32_bf16 v[148:151], a[28:31], a[84:87], v[148:151]\n" \
"ds_read_b64_tr_b16 a[28:29], v33 offset:27680\n" \
"ds_read_b64_tr_b16 a[30:31], v33 offset:32288\n" \
"v_mfma_f32_16x16x32_bf16 v[152:155], a[192:195], a[68:71], v[152:155]\n" \
"v_mfma_f32_16x16x32_bf16 v[156:159], a[192:195], a[84:87], v[156:159]\n" \
"ds_read_b64_tr_b16 a[192:193], v33 offset:27712\n" \
"s_waitcnt lgkmcnt(13)\n" \
"v_mfma_f32_16x16x32_bf16 v[160:163], a[196:199], a[68:71], v[160:163]\n" \
"buffer_load_dwordx4 a[236:239], v32, s[56:59], s64 offen offset:0 sc1\n" \
"v_mfma_f32_16x16x32_bf16 v[164:167], a[196:199], a[84:87], v[164:167]\n" \
"ds_read_b64_tr_b16 a[194:195], v33 offset:32320\n" \
"ds_read_b64_tr_b16 a[196:197], v33 offset:27744\n" \
"v_mfma_f32_16x16x32_bf16 v[168:171], a[200:203], a[68:71], v[168:171]\n" \
"ds_read_b64_tr_b16 a[198:199], v33 offset:32352\n" \
"v_mfma_f32_16x16x32_bf16 v[172:175], a[200:203], a[84:87], v[172:175]\n" \
"ds_read_b64_tr_b16 a[200:201], v33 offset:27776\n" \
"ds_read_b64_tr_b16 a[202:203], v33 offset:32384\n" \
"v_mfma_f32_16x16x32_bf16 v[176:179], a[204:207], a[68:71], v[176:179]\n" \
"v_mfma_f32_16x16x32_bf16 v[180:183], a[204:207], a[84:87], v[180:183]\n" \
"ds_read_b64_tr_b16 a[204:205], v33 offset:27808\n" \
"ds_read_b64_tr_b16 a[206:207], v33 offset:32416\n" \
"v_mfma_f32_16x16x32_bf16 v[112:115], a[208:211], a[72:75], v[112:115]\n" \
"buffer_load_dwordx4 a[224:227], v20, s[56:59], s64 offen offset:1024 sc1\n" \
"v_mfma_f32_16x16x32_bf16 v[116:119], a[208:211], a[88:91], v[116:119]\n" \
"ds_read_b64_tr_b16 a[208:209], v33 offset:27840\n" \
"ds_read_b64_tr_b16 a[210:211], v33 offset:32448\n" \
"v_mfma_f32_16x16x32_bf16 v[120:123], a[212:215], a[72:75], v[120:123]\n" \
"v_mfma_f32_16x16x32_bf16 v[124:127], a[212:215], a[88:91], v[124:127]\n" \
"ds_read_b64_tr_b16 a[212:213], v33 offset:27872\n" \
"ds_read_b64_tr_b16 a[214:215], v33 offset:32480\n" \
"v_mfma_f32_16x16x32_bf16 v[128:131], a[216:219], a[72:75], v[128:131]\n" \
"v_mfma_f32_16x16x32_bf16 v[132:135], a[216:219], a[88:91], v[132:135]\n" \
"ds_read_b64_tr_b16 a[216:217], v33 offset:27904\n" \
"ds_read_b64_tr_b16 a[218:219], v33 offset:32512\n" \
"v_mfma_f32_16x16x32_bf16 v[136:139], a[0:3], a[72:75], v[136:139]\n" \
"buffer_load_dwordx4 a[240:243], v32, s[56:59], s64 offen offset:1024 sc1\n" \
"v_mfma_f32_16x16x32_bf16 v[140:143], a[0:3], a[88:91], v[140:143]\n" \
"v_mfma_f32_16x16x32_bf16 v[144:147], a[4:7], a[72:75], v[144:147]\n" \
"v_mfma_f32_16x16x32_bf16 v[148:151], a[4:7], a[88:91], v[148:151]\n" \
"s_waitcnt lgkmcnt(12)\n" \
"v_mfma_f32_16x16x32_bf16 v[152:155], a[8:11], a[72:75], v[152:155]\n" \
"v_mfma_f32_16x16x32_bf16 v[156:159], a[8:11], a[88:91], v[156:159]\n" \
"buffer_load_dwordx4 a[228:231], v20, s[56:59], s64 offen offset:2048 sc1\n" \
"v_mfma_f32_16x16x32_bf16 v[160:163], a[12:15], a[72:75], v[160:163]\n" \
"v_mfma_f32_16x16x32_bf16 v[164:167], a[12:15], a[88:91], v[164:167]\n" \
"v_mfma_f32_16x16x32_bf16 v[168:171], a[16:19], a[72:75], v[168:171]\n" \
"v_mfma_f32_16x16x32_bf16 v[172:175], a[16:19], a[88:91], v[172:175]\n" \
"v_mfma_f32_16x16x32_bf16 v[176:179], a[20:23], a[72:75], v[176:179]\n" \
"buffer_load_dwordx4 a[244:247], v32, s[56:59], s64 offen offset:2048 sc1\n" \
"v_mfma_f32_16x16x32_bf16 v[180:183], a[20:23], a[88:91], v[180:183]\n" \
"s_waitcnt vmcnt(24)\n" \
"s_barrier\n" \
"s_waitcnt vmcnt(23)\n" \
"v_mfma_f32_16x16x32_bf16 v[112:115], a[24:27], a[76:79], v[112:115]\n" \
"ds_read_b64_tr_b16 a[0:1], v33 offset:36864\n" \
"s_waitcnt vmcnt(22)\n" \
"v_mfma_f32_16x16x32_bf16 v[116:119], a[24:27], a[92:95], v[116:119]\n" \
"ds_read_b64_tr_b16 a[2:3], v33 offset:41472\n" \
"ds_read_b64_tr_b16 a[4:5], v33 offset:46080\n" \
"v_mfma_f32_16x16x32_bf16 v[120:123], a[28:31], a[76:79], v[120:123]\n" \
"ds_read_b64_tr_b16 a[6:7], v33 offset:50688\n" \
"ds_read_b64_tr_b16 a[8:9], v33 offset:55296\n" \
"v_mfma_f32_16x16x32_bf16 v[124:127], a[28:31], a[92:95], v[124:127]\n" \
"ds_read_b64_tr_b16 a[10:11], v33 offset:59904\n" \
"ds_read_b64_tr_b16 a[12:13], v10 offset:0\n" \
"v_mfma_f32_16x16x32_bf16 v[128:131], a[192:195], a[76:79], v[128:131]\n" \
"ds_read_b64_tr_b16 a[14:15], v10 offset:4608\n" \
"ds_read_b64_tr_b16 a[16:17], v34 offset:0\n" \
"v_mfma_f32_16x16x32_bf16 v[132:135], a[192:195], a[92:95], v[132:135]\n" \
"ds_read_b64_tr_b16 a[18:19], v34 offset:4608\n" \
"s_waitcnt lgkmcnt(10)\n" \
"v_mfma_f32_16x16x32_bf16 v[136:139], a[196:199], a[76:79], v[136:139]\n" \
"ds_read_b64_tr_b16 a[20:21], v34 offset:9216\n" \
"ds_read_b64_tr_b16 a[22:23], v34 offset:13824\n" \
"v_mfma_f32_16x16x32_bf16 v[140:143], a[196:199], a[92:95], v[140:143]\n" \
"ds_read_b64_tr_b16 a[24:25], v34 offset:18432\n" \
"ds_read_b64_tr_b16 a[26:27], v34 offset:23040\n" \
"v_mfma_f32_16x16x32_bf16 v[144:147], a[200:203], a[76:79], v[144:147]\n" \
"ds_read_b64_tr_b16 a[28:29], v10 offset:9216\n" \
"ds_read_b64_tr_b16 a[30:31], v10 offset:13824\n" \
"v_mfma_f32_16x16x32_bf16 v[148:151], a[200:203], a[92:95], v[148:151]\n" \
"ds_read_b64_tr_b16 a[192:193], v33 offset:36896\n" \
"ds_read_b64_tr_b16 a[194:195], v33 offset:41504\n" \
"v_mfma_f32_16x16x32_bf16 v[152:155], a[204:207], a[76:79], v[152:155]\n" \
"ds_read_b64_tr_b16 a[196:197], v33 offset:46112\n" \
"ds_read_b64_tr_b16 a[198:199], v33 offset:50720\n" \
"v_mfma_f32_16x16x32_bf16 v[156:159], a[204:207], a[92:95], v[156:159]\n" \
"ds_read_b64_tr_b16 a[200:201], v33 offset:55328\n" \
"ds_read_b64_tr_b16 a[202:203], v33 offset:59936\n" \
"v_mfma_f32_16x16x32_bf16 v[160:163], a[208:211], a[76:79], v[160:163]\n" \
"ds_read_b64_tr_b16 a[204:205], v10 offset:32\n" \
"ds_read_b64_tr_b16 a[206:207], v10 offset:4640\n" \
"v_mfma_f32_16x16x32_bf16 v[164:167], a[208:211], a[92:95], v[164:167]\n" \
"ds_read_b64_tr_b16 a[208:209], v34 offset:32\n" \
"ds_read_b64_tr_b16 a[210:211], v34 offset:4640\n" \
"v_mfma_f32_16x16x32_bf16 v[168:171], a[212:215], a[76:79], v[168:171]\n" \
"buffer_load_dwordx4 a[232:235], v20, s[56:59], s64 offen offset:3072 sc1\n" \
"v_mfma_f32_16x16x32_bf16 v[172:175], a[212:215], a[92:95], v[172:175]\n" \
"ds_read_b64_tr_b16 a[212:213], v34 offset:9248\n" \
"ds_read_b64_tr_b16 a[214:215], v34 offset:13856\n" \
"v_mfma_f32_16x16x32_bf16 v[176:179], a[216:219], a[76:79], v[176:179]\n" \
"v_mfma_f32_16x16x32_bf16 v[180:183], a[216:219], a[92:95], v[180:183]\n" \
"ds_read_b64_tr_b16 a[216:217], v34 offset:18464\n" \
"s_waitcnt vmcnt(22) lgkmcnt(15)\n" \
"v_mfma_f32_16x16x32_bf16 v[112:115], a[0:3], a[128:131], v[112:115]\n" \
"ds_read_b64_tr_b16 a[218:219], v34 offset:23072\n" \
"s_waitcnt vmcnt(21)\n" \
"v_mfma_f32_16x16x32_bf16 v[116:119], a[0:3], a[144:147], v[116:119]\n" \
"ds_read_b64_tr_b16 a[0:1], v10 offset:9248\n" \
"s_waitcnt vmcnt(20)\n" \
"v_mfma_f32_16x16x32_bf16 v[112:115], a[4:7], a[132:135], v[112:115]\n" \
"ds_read_b64_tr_b16 a[2:3], v10 offset:13856\n" \
"s_waitcnt vmcnt(19)\n" \
"v_mfma_f32_16x16x32_bf16 v[116:119], a[4:7], a[148:151], v[116:119]\n" \
"ds_read_b64_tr_b16 a[4:5], v33 offset:36928\n" \
"s_waitcnt vmcnt(18)\n" \
"v_mfma_f32_16x16x32_bf16 v[112:115], a[8:11], a[136:139], v[112:115]\n" \
"ds_read_b64_tr_b16 a[6:7], v33 offset:41536\n" \
"s_waitcnt vmcnt(17)\n" \
"v_mfma_f32_16x16x32_bf16 v[116:119], a[8:11], a[152:155], v[116:119]\n" \
"ds_read_b64_tr_b16 a[8:9], v33 offset:46144\n" \
"s_waitcnt vmcnt(16)\n" \
"v_mfma_f32_16x16x32_bf16 v[112:115], a[12:15], a[140:143], v[112:115]\n" \
"ds_read_b64_tr_b16 a[10:11], v33 offset:50752\n" \
"s_waitcnt vmcnt(15)\n" \
"v_mfma_f32_16x16x32_bf16 v[116:119], a[12:15], a[156:159], v[116:119]\n" \
"ds_read_b64_tr_b16 a[12:13], v33 offset:55360\n" \
"ds_read_b64_tr_b16 a[14:15], v33 offset:59968\n" \
"buffer_load_dwordx4 a[248:251], v32, s[56:59], s64 offen offset:3072 sc1\n" \
"s_waitcnt vmcnt(15)\n" \
"v_mfma_f32_16x16x32_bf16 v[112:115], a[16:19], a[32:35], v[112:115]\n" \
"buffer_load_dwordx4 a[160:163], v20, s[56:59], s72 offen offset:0 sc1\n" \
"s_waitcnt vmcnt(15)\n" \
"v_mfma_f32_16x16x32_bf16 v[116:119], a[16:19], a[48:51], v[116:119]\n" \
"ds_read_b64_tr_b16 a[16:17], v10 offset:64\n" \
"ds_read_b64_tr_b16 a[18:19], v10 offset:4672\n" \
"buffer_load_dwordx4 a[176:179], v32, s[56:59], s72 offen offset:0 sc1\n" \
"s_waitcnt vmcnt(15)\n" \
"v_mfma_f32_16x16x32_bf16 v[112:115], a[20:23], a[36:39], v[112:115]\n" \
"buffer_load_dwordx4 a[164:167], v20, s[56:59], s72 offen offset:1024 sc1\n" \
"s_waitcnt vmcnt(15)\n" \
"v_mfma_f32_16x16x32_bf16 v[116:119], a[20:23], a[52:55], v[116:119]\n" \
"ds_read_b64_tr_b16 a[20:21], v34 offset:64\n" \
"ds_read_b64_tr_b16 a[22:23], v34 offset:4672\n" \
"buffer_load_dwordx4 a[180:183], v32, s[56:59], s72 offen offset:1024 sc1\n" \
"s_waitcnt vmcnt(15)\n" \
"v_mfma_f32_16x16x32_bf16 v[112:115], a[24:27], a[40:43], v[112:115]\n" \
"buffer_load_dwordx4 a[168:171], v20, s[56:59], s72 offen offset:2048 sc1\n" \
"s_waitcnt vmcnt(15)\n" \
"v_mfma_f32_16x16x32_bf16 v[116:119], a[24:27], a[56:59], v[116:119]\n" \
"ds_read_b64_tr_b16 a[24:25], v34 offset:9280\n" \
"ds_read_b64_tr_b16 a[26:27], v34 offset:13888\n" \
"buffer_load_dwordx4 a[184:187], v32, s[56:59], s72 offen offset:2048 sc1\n" \
"s_waitcnt vmcnt(15) lgkmcnt(4)\n" \
"v_mfma_f32_16x16x32_bf16 v[112:115], a[28:31], a[44:47], v[112:115]\n" \
"buffer_load_dwordx4 a[172:175], v20, s[56:59], s72 offen offset:3072 sc1\n" \
"s_waitcnt vmcnt(15)\n" \
"v_mfma_f32_16x16x32_bf16 v[116:119], a[28:31], a[60:63], v[116:119]\n" \
"ds_read_b64_tr_b16 a[28:29], v34 offset:18496\n" \
"ds_read_b64_tr_b16 a[30:31], v34 offset:23104\n" \
"v_mfma_f32_16x16x32_bf16 v[120:123], a[192:195], a[128:131], v[120:123]\n" \
"buffer_load_dwordx4 a[188:191], v32, s[56:59], s72 offen offset:3072 sc1\n" \
"v_mfma_f32_16x16x32_bf16 v[124:127], a[192:195], a[144:147], v[124:127]\n" \
"v_cvt_pk_bf16_f32 v112, v112, v113\n" \
"v_cvt_pk_bf16_f32 v113, v114, v115\n" \
"v_mfma_f32_16x16x32_bf16 v[120:123], a[196:199], a[132:135], v[120:123]\n" \
"v_cvt_pk_bf16_f32 v114, v116, v117\n" \
"v_cvt_pk_bf16_f32 v115, v118, v119\n" \
"buffer_store_dwordx4 v[112:115], v20, s[56:59], s65 offen offset:0 sc1\n" \
"s_add_u32 s65, s65, 0x3800\n" \
"v_mfma_f32_16x16x32_bf16 v[124:127], a[196:199], a[148:151], v[124:127]\n" \
"ds_read_b64_tr_b16 a[192:193], v10 offset:9280\n" \
"ds_read_b64_tr_b16 a[194:195], v10 offset:13888\n" \
"v_mfma_f32_16x16x32_bf16 v[120:123], a[200:203], a[136:139], v[120:123]\n" \
"ds_read_b64_tr_b16 a[196:197], v33 offset:36960\n" \
"ds_read_b64_tr_b16 a[198:199], v33 offset:41568\n" \
"v_mfma_f32_16x16x32_bf16 v[124:127], a[200:203], a[152:155], v[124:127]\n" \
"ds_read_b64_tr_b16 a[200:201], v33 offset:46176\n" \
"ds_read_b64_tr_b16 a[202:203], v33 offset:50784\n" \
"v_mfma_f32_16x16x32_bf16 v[120:123], a[204:207], a[140:143], v[120:123]\n" \
"buffer_load_dwordx4 a[96:99], v20, s[56:59], s93 offen offset:0 sc1\n" \
"v_mfma_f32_16x16x32_bf16 v[124:127], a[204:207], a[156:159], v[124:127]\n" \
"ds_read_b64_tr_b16 a[204:205], v33 offset:55392\n" \
"ds_read_b64_tr_b16 a[206:207], v33 offset:60000\n" \
"v_mfma_f32_16x16x32_bf16 v[120:123], a[208:211], a[32:35], v[120:123]\n" \
"v_readlane_b32 s72, v11, 53\n" \
"v_mfma_f32_16x16x32_bf16 v[124:127], a[208:211], a[48:51], v[124:127]\n" \
"ds_read_b64_tr_b16 a[208:209], v10 offset:96\n" \
"ds_read_b64_tr_b16 a[210:211], v10 offset:4704\n" \
"v_mfma_f32_16x16x32_bf16 v[120:123], a[212:215], a[36:39], v[120:123]\n" \
"v_mfma_f32_16x16x32_bf16 v[124:127], a[212:215], a[52:55], v[124:127]\n" \
"ds_read_b64_tr_b16 a[212:213], v34 offset:96\n" \
"ds_read_b64_tr_b16 a[214:215], v34 offset:4704\n" \
"v_mfma_f32_16x16x32_bf16 v[120:123], a[216:219], a[40:43], v[120:123]\n" \
"buffer_load_dwordx4 a[112:115], v32, s[56:59], s93 offen offset:0 sc1\n" \
"v_mfma_f32_16x16x32_bf16 v[124:127], a[216:219], a[56:59], v[124:127]\n" \
"ds_read_b64_tr_b16 a[216:217], v34 offset:9312\n" \
"ds_read_b64_tr_b16 a[218:219], v34 offset:13920\n" \
"v_mfma_f32_16x16x32_bf16 v[120:123], a[0:3], a[44:47], v[120:123]\n" \
"v_mfma_f32_16x16x32_bf16 v[124:127], a[0:3], a[60:63], v[124:127]\n" \
"ds_read_b64_tr_b16 a[0:1], v34 offset:18528\n" \
"ds_read_b64_tr_b16 a[2:3], v34 offset:23136\n" \
"v_mfma_f32_16x16x32_bf16 v[128:131], a[4:7], a[128:131], v[128:131]\n" \
"v_mfma_f32_16x16x32_bf16 v[132:135], a[4:7], a[144:147], v[132:135]\n" \
"buffer_load_dwordx4 a[100:103], v20, s[56:59], s93 offen offset:1024 sc1\n" \
"v_mfma_f32_16x16x32_bf16 v[128:131], a[8:11], a[132:135], v[128:131]\n" \
"ds_read_b64_tr_b16 a[4:5], v10 offset:9312\n" \
"v_cvt_pk_bf16_f32 v120, v120, v121\n" \
"v_cvt_pk_bf16_f32 v121, v122, v123\n" \
"v_cvt_pk_bf16_f32 v122, v124, v125\n" \
"v_cvt_pk_bf16_f32 v123, v126, v127\n" \
"v_mfma_f32_16x16x32_bf16 v[132:135], a[8:11], a[148:151], v[132:135]\n" \
"buffer_store_dwordx4 v[120:123], v20, s[56:59], s66 offen offset:0 sc1\n" \
"s_add_u32 s66, s66, 0x3000\n" \
"v_mfma_f32_16x16x32_bf16 v[128:131], a[12:15], a[136:139], v[128:131]\n" \
"ds_read_b64_tr_b16 a[6:7], v10 offset:13920\n" \
"ds_read_b64_tr_b16 a[8:9], v33 offset:36992\n" \
"v_mfma_f32_16x16x32_bf16 v[132:135], a[12:15], a[152:155], v[132:135]\n" \
"ds_read_b64_tr_b16 a[10:11], v33 offset:41600\n" \
"ds_read_b64_tr_b16 a[12:13], v33 offset:46208\n" \
"v_mfma_f32_16x16x32_bf16 v[128:131], a[16:19], a[140:143], v[128:131]\n" \
"ds_read_b64_tr_b16 a[14:15], v33 offset:50816\n" \
"v_mfma_f32_16x16x32_bf16 v[132:135], a[16:19], a[156:159], v[132:135]\n" \
"ds_read_b64_tr_b16 a[16:17], v33 offset:55424\n" \
"s_waitcnt lgkmcnt(13)\n" \
"v_mfma_f32_16x16x32_bf16 v[128:131], a[20:23], a[32:35], v[128:131]\n" \
"buffer_load_dwordx4 a[116:119], v32, s[56:59], s93 offen offset:1024 sc1\n" \
"v_mfma_f32_16x16x32_bf16 v[132:135], a[20:23], a[48:51], v[132:135]\n" \
"ds_read_b64_tr_b16 a[18:19], v33 offset:60032\n" \
"ds_read_b64_tr_b16 a[20:21], v10 offset:128\n" \
"v_mfma_f32_16x16x32_bf16 v[128:131], a[24:27], a[36:39], v[128:131]\n" \
"ds_read_b64_tr_b16 a[22:23], v10 offset:4736\n" \
"v_mfma_f32_16x16x32_bf16 v[132:135], a[24:27], a[52:55], v[132:135]\n" \
"ds_read_b64_tr_b16 a[24:25], v34 offset:128\n" \
"ds_read_b64_tr_b16 a[26:27], v34 offset:4736\n" \
"v_mfma_f32_16x16x32_bf16 v[128:131], a[28:31], a[40:43], v[128:131]\n" \
"v_mfma_f32_16x16x32_bf16 v[132:135], a[28:31], a[56:59], v[132:135]\n" \
"buffer_load_dwordx4 a[104:107], v20, s[56:59], s93 offen offset:2048 sc1\n" \
"v_mfma_f32_16x16x32_bf16 v[128:131], a[192:195], a[44:47], v[128:131]\n" \
"ds_read_b64_tr_b16 a[28:29], v34 offset:9344\n" \
"ds_read_b64_tr_b16 a[30:31], v34 offset:13952\n" \
"v_mfma_f32_16x16x32_bf16 v[132:135], a[192:195], a[60:63], v[132:135]\n" \
"ds_read_b64_tr_b16 a[192:193], v34 offset:18560\n" \
"ds_read_b64_tr_b16 a[194:195], v34 offset:23168\n" \
"v_mfma_f32_16x16x32_bf16 v[136:139], a[196:199], a[128:131], v[136:139]\n" \
"v_mfma_f32_16x16x32_bf16 v[140:143], a[196:199], a[144:147], v[140:143]\n" \
"ds_read_b64_tr_b16 a[196:197], v10 offset:9344\n" \
"v_cvt_pk_bf16_f32 v128, v128, v129\n" \
"v_cvt_pk_bf16_f32 v129, v130, v131\n" \
"v_mfma_f32_16x16x32_bf16 v[136:139], a[200:203], a[132:135], v[136:139]\n" \
"v_cvt_pk_bf16_f32 v130, v132, v133\n" \
"v_cvt_pk_bf16_f32 v131, v134, v135\n" \
"buffer_store_dwordx4 v[128:131], v20, s[56:59], s67 offen offset:0 sc1\n" \
"s_add_u32 s67, s67, 0x2800\n" \
"v_mfma_f32_16x16x32_bf16 v[140:143], a[200:203], a[148:151], v[140:143]\n" \
"ds_read_b64_tr_b16 a[198:199], v10 offset:13952\n" \
"ds_read_b64_tr_b16 a[200:201], v33 offset:37024\n" \
"v_mfma_f32_16x16x32_bf16 v[136:139], a[204:207], a[136:139], v[136:139]\n" \
"ds_read_b64_tr_b16 a[202:203], v33 offset:41632\n" \
"v_mfma_f32_16x16x32_bf16 v[140:143], a[204:207], a[152:155], v[140:143]\n" \
"ds_read_b64_tr_b16 a[204:205], v33 offset:46240\n" \
"ds_read_b64_tr_b16 a[206:207], v33 offset:50848\n" \
"v_mfma_f32_16x16x32_bf16 v[136:139], a[208:211], a[140:143], v[136:139]\n" \
"buffer_load_dwordx4 a[120:123], v32, s[56:59], s93 offen offset:2048 sc1\n" \
"v_mfma_f32_16x16x32_bf16 v[140:143], a[208:211], a[156:159], v[140:143]\n" \
"ds_read_b64_tr_b16 a[208:209], v33 offset:55456\n" \
"s_waitcnt lgkmcnt(13)\n" \
"v_mfma_f32_16x16x32_bf16 v[136:139], a[212:215], a[32:35], v[136:139]\n" \
"ds_read_b64_tr_b16 a[210:211], v33 offset:60064\n" \
"v_mfma_f32_16x16x32_bf16 v[140:143], a[212:215], a[48:51], v[140:143]\n" \
"ds_read_b64_tr_b16 a[212:213], v10 offset:160\n" \
"ds_read_b64_tr_b16 a[214:215], v10 offset:4768\n" \
"v_mfma_f32_16x16x32_bf16 v[136:139], a[216:219], a[36:39], v[136:139]\n" \
"v_mfma_f32_16x16x32_bf16 v[140:143], a[216:219], a[52:55], v[140:143]\n" \
"buffer_load_dwordx4 a[108:111], v20, s[56:59], s93 offen offset:3072 sc1\n" \
"v_mfma_f32_16x16x32_bf16 v[136:139], a[0:3], a[40:43], v[136:139]\n" \
"ds_read_b64_tr_b16 a[216:217], v34 offset:160\n" \
"ds_read_b64_tr_b16 a[218:219], v34 offset:4768\n" \
"v_mfma_f32_16x16x32_bf16 v[140:143], a[0:3], a[56:59], v[140:143]\n" \
"ds_read_b64_tr_b16 a[0:1], v34 offset:9376\n" \
"ds_read_b64_tr_b16 a[2:3], v34 offset:13984\n" \
"v_mfma_f32_16x16x32_bf16 v[136:139], a[4:7], a[44:47], v[136:139]\n" \
"v_mfma_f32_16x16x32_bf16 v[140:143], a[4:7], a[60:63], v[140:143]\n" \
"ds_read_b64_tr_b16 a[4:5], v34 offset:18592\n" \
"ds_read_b64_tr_b16 a[6:7], v34 offset:23200\n" \
"buffer_load_dwordx4 a[124:127], v32, s[56:59], s93 offen offset:3072 sc1\n" \
"v_mfma_f32_16x16x32_bf16 v[144:147], a[8:11], a[128:131], v[144:147]\n" \
"v_mfma_f32_16x16x32_bf16 v[148:151], a[8:11], a[144:147], v[148:151]\n" \
"ds_read_b64_tr_b16 a[8:9], v10 offset:9376\n" \
"ds_read_b64_tr_b16 a[10:11], v10 offset:13984\n" \
"v_cvt_pk_bf16_f32 v136, v136, v137\n" \
"v_cvt_pk_bf16_f32 v137, v138, v139\n" \
"v_cvt_pk_bf16_f32 v138, v140, v141\n" \
"v_cvt_pk_bf16_f32 v139, v142, v143\n" \
"buffer_store_dwordx4 v[136:139], v20, s[56:59], s68 offen offset:0 sc1\n" \
"s_add_u32 s68, s68, 0x2000\n" \
"v_mfma_f32_16x16x32_bf16 v[144:147], a[12:15], a[132:135], v[144:147]\n" \
"v_mfma_f32_16x16x32_bf16 v[148:151], a[12:15], a[148:151], v[148:151]\n" \
"ds_read_b64_tr_b16 a[12:13], v33 offset:37056\n" \
"ds_read_b64_tr_b16 a[14:15], v33 offset:41664\n" \
"v_mfma_f32_16x16x32_bf16 v[144:147], a[16:19], a[136:139], v[144:147]\n" \
"v_mfma_f32_16x16x32_bf16 v[148:151], a[16:19], a[152:155], v[148:151]\n" \
"ds_read_b64_tr_b16 a[16:17], v33 offset:46272\n" \
"ds_read_b64_tr_b16 a[18:19], v33 offset:50880\n" \
"v_mfma_f32_16x16x32_bf16 v[144:147], a[20:23], a[140:143], v[144:147]\n" \
"buffer_load_dwordx4 v[64:67], v20, s[56:59], s95 offen offset:0 sc1\n" \
"v_mfma_f32_16x16x32_bf16 v[148:151], a[20:23], a[156:159], v[148:151]\n" \
"ds_read_b64_tr_b16 a[20:21], v33 offset:55488\n" \
"s_waitcnt lgkmcnt(13)\n" \
"v_mfma_f32_16x16x32_bf16 v[144:147], a[24:27], a[32:35], v[144:147]\n" \
"ds_read_b64_tr_b16 a[22:23], v33 offset:60096\n" \
"v_mfma_f32_16x16x32_bf16 v[148:151], a[24:27], a[48:51], v[148:151]\n" \
"ds_read_b64_tr_b16 a[24:25], v10 offset:192\n" \
"ds_read_b64_tr_b16 a[26:27], v10 offset:4800\n" \
"v_mfma_f32_16x16x32_bf16 v[144:147], a[28:31], a[36:39], v[144:147]\n" \
"v_mfma_f32_16x16x32_bf16 v[148:151], a[28:31], a[52:55], v[148:151]\n" \
"ds_read_b64_tr_b16 a[28:29], v34 offset:192\n" \
"ds_read_b64_tr_b16 a[30:31], v34 offset:4800\n" \
"v_mfma_f32_16x16x32_bf16 v[144:147], a[192:195], a[40:43], v[144:147]\n" \
"buffer_load_dwordx4 v[68:71], v20, s[56:59], s95 offen offset:1024 sc1\n" \
"v_mfma_f32_16x16x32_bf16 v[148:151], a[192:195], a[56:59], v[148:151]\n" \
"ds_read_b64_tr_b16 a[192:193], v34 offset:9408\n" \
"ds_read_b64_tr_b16 a[194:195], v34 offset:14016\n" \
"v_mfma_f32_16x16x32_bf16 v[144:147], a[196:199], a[44:47], v[144:147]\n" \
"v_mfma_f32_16x16x32_bf16 v[148:151], a[196:199], a[60:63], v[148:151]\n" \
"ds_read_b64_tr_b16 a[196:197], v34 offset:18624\n" \
"ds_read_b64_tr_b16 a[198:199], v34 offset:23232\n" \
"v_mfma_f32_16x16x32_bf16 v[152:155], a[200:203], a[128:131], v[152:155]\n" \
"v_mfma_f32_16x16x32_bf16 v[156:159], a[200:203], a[144:147], v[156:159]\n" \
"ds_read_b64_tr_b16 a[200:201], v10 offset:9408\n" \
"ds_read_b64_tr_b16 a[202:203], v10 offset:14016\n" \
"v_mfma_f32_16x16x32_bf16 v[152:155], a[204:207], a[132:135], v[152:155]\n" \
"v_cvt_pk_bf16_f32 v144, v144, v145\n" \
"v_cvt_pk_bf16_f32 v145, v146, v147\n" \
"v_cvt_pk_bf16_f32 v146, v148, v149\n" \
"v_cvt_pk_bf16_f32 v147, v150, v151\n" \
"buffer_store_dwordx4 v[144:147], v20, s[56:59], s69 offen offset:0 sc1\n" \
"s_add_u32 s69, s69, 0x1800\n" \
"v_mfma_f32_16x16x32_bf16 v[156:159], a[204:207], a[148:151], v[156:159]\n" \
"ds_read_b64_tr_b16 a[204:205], v33 offset:256\n" \
"ds_read_b64_tr_b16 a[206:207], v33 offset:4864\n" \
"v_mfma_f32_16x16x32_bf16 v[152:155], a[208:211], a[136:139], v[152:155]\n" \
"v_mfma_f32_16x16x32_bf16 v[156:159], a[208:211], a[152:155], v[156:159]\n" \
"ds_read_b64_tr_b16 a[208:209], v33 offset:9472\n" \
"ds_read_b64_tr_b16 a[210:211], v33 offset:14080\n" \
"v_mfma_f32_16x16x32_bf16 v[152:155], a[212:215], a[140:143], v[152:155]\n" \
"buffer_load_dwordx4 v[72:75], v32, s[56:59], s95 offen offset:0 sc1\n" \
"v_mfma_f32_16x16x32_bf16 v[156:159], a[212:215], a[156:159], v[156:159]\n" \
"ds_read_b64_tr_b16 a[212:213], v33 offset:18688\n" \
"s_waitcnt lgkmcnt(13)\n" \
"v_mfma_f32_16x16x32_bf16 v[152:155], a[216:219], a[32:35], v[152:155]\n" \
"ds_read_b64_tr_b16 a[214:215], v33 offset:23296\n" \
"v_mfma_f32_16x16x32_bf16 v[156:159], a[216:219], a[48:51], v[156:159]\n" \
"ds_read_b64_tr_b16 a[216:217], v33 offset:27904\n" \
"ds_read_b64_tr_b16 a[218:219], v33 offset:32512\n" \
"v_mfma_f32_16x16x32_bf16 v[152:155], a[0:3], a[36:39], v[152:155]\n" \
"v_mfma_f32_16x16x32_bf16 v[156:159], a[0:3], a[52:55], v[156:159]\n" \
"ds_read_b64_tr_b16 a[0:1], v33 offset:37120\n" \
"ds_read_b64_tr_b16 a[2:3], v33 offset:41728\n" \
"v_mfma_f32_16x16x32_bf16 v[152:155], a[4:7], a[40:43], v[152:155]\n" \
"buffer_load_dwordx4 v[76:79], v32, s[56:59], s95 offen offset:1024 sc1\n" \
"v_mfma_f32_16x16x32_bf16 v[156:159], a[4:7], a[56:59], v[156:159]\n" \
"ds_read_b64_tr_b16 a[4:5], v33 offset:46336\n" \
"ds_read_b64_tr_b16 a[6:7], v33 offset:50944\n" \
"v_mfma_f32_16x16x32_bf16 v[152:155], a[8:11], a[44:47], v[152:155]\n" \
"v_mfma_f32_16x16x32_bf16 v[156:159], a[8:11], a[60:63], v[156:159]\n" \
"ds_read_b64_tr_b16 a[8:9], v33 offset:55552\n" \
"ds_read_b64_tr_b16 a[10:11], v33 offset:60160\n" \
"v_mfma_f32_16x16x32_bf16 v[160:163], a[12:15], a[128:131], v[160:163]\n" \
"v_mfma_f32_16x16x32_bf16 v[164:167], a[12:15], a[144:147], v[164:167]\n" \
"ds_read_b64_tr_b16 a[12:13], v10 offset:256\n" \
"ds_read_b64_tr_b16 a[14:15], v10 offset:4864\n" \
"v_mfma_f32_16x16x32_bf16 v[160:163], a[16:19], a[132:135], v[160:163]\n" \
"v_cvt_pk_bf16_f32 v152, v152, v153\n" \
"v_cvt_pk_bf16_f32 v153, v154, v155\n" \
"v_cvt_pk_bf16_f32 v154, v156, v157\n" \
"v_cvt_pk_bf16_f32 v155, v158, v159\n" \
"buffer_store_dwordx4 v[152:155], v20, s[56:59], s70 offen offset:0 sc1\n" \
"s_add_u32 s70, s70, 0x1000\n" \
"v_mfma_f32_16x16x32_bf16 v[164:167], a[16:19], a[148:151], v[164:167]\n" \
"ds_read_b64_tr_b16 a[16:17], v34 offset:256\n" \
"ds_read_b64_tr_b16 a[18:19], v34 offset:4864\n" \
"v_mfma_f32_16x16x32_bf16 v[160:163], a[20:23], a[136:139], v[160:163]\n" \
"v_mfma_f32_16x16x32_bf16 v[164:167], a[20:23], a[152:155], v[164:167]\n" \
"ds_read_b64_tr_b16 a[20:21], v34 offset:9472\n" \
"ds_read_b64_tr_b16 a[22:23], v34 offset:14080\n" \
"v_mfma_f32_16x16x32_bf16 v[160:163], a[24:27], a[140:143], v[160:163]\n" \
"buffer_load_dwordx4 v[80:83], v20, s[56:59], s96 offen offset:0 sc1\n" \
"v_mfma_f32_16x16x32_bf16 v[164:167], a[24:27], a[156:159], v[164:167]\n" \
"ds_read_b64_tr_b16 a[24:25], v34 offset:18688\n" \
"s_waitcnt lgkmcnt(13)\n" \
"v_mfma_f32_16x16x32_bf16 v[160:163], a[28:31], a[32:35], v[160:163]\n" \
"ds_read_b64_tr_b16 a[26:27], v34 offset:23296\n" \
"v_mfma_f32_16x16x32_bf16 v[164:167], a[28:31], a[48:51], v[164:167]\n" \
"ds_read_b64_tr_b16 a[28:29], v10 offset:9472\n" \
"ds_read_b64_tr_b16 a[30:31], v10 offset:14080\n" \
"s_waitcnt vmcnt(18)\n" \
"s_mov_b64 exec, 1\n" \
"buffer_store_dword v39, off, s[56:59], s65 offset:0 sc1\n" \
"s_mov_b64 exec, -1\n" \
"v_mfma_f32_16x16x32_bf16 v[160:163], a[192:195], a[36:39], v[160:163]\n" \
"v_mfma_f32_16x16x32_bf16 v[164:167], a[192:195], a[52:55], v[164:167]\n" \
"ds_read_b64_tr_b16 a[192:193], v33 offset:0\n" \
"ds_read_b64_tr_b16 a[194:195], v33 offset:4608\n" \
"v_mfma_f32_16x16x32_bf16 v[160:163], a[196:199], a[40:43], v[160:163]\n" \
"v_mfma_f32_16x16x32_bf16 v[164:167], a[196:199], a[56:59], v[164:167]\n" \
"ds_read_b64_tr_b16 a[196:197], v33 offset:9216\n" \
"ds_read_b64_tr_b16 a[198:199], v33 offset:13824\n" \
"v_mfma_f32_16x16x32_bf16 v[160:163], a[200:203], a[44:47], v[160:163]\n" \
"buffer_load_dwordx4 v[84:87], v20, s[56:59], s96 offen offset:1024 sc1\n" \
"v_mfma_f32_16x16x32_bf16 v[164:167], a[200:203], a[60:63], v[164:167]\n" \
"ds_read_b64_tr_b16 a[200:201], v33 offset:18432\n" \
"ds_read_b64_tr_b16 a[202:203], v33 offset:23040\n" \
"v_mfma_f32_16x16x32_bf16 v[248:251], a[204:207], a[220:223], v[248:251]\n" \
"v_mfma_f32_16x16x32_bf16 v[252:255], a[204:207], a[236:239], v[252:255]\n" \
"ds_read_b64_tr_b16 a[204:205], v33 offset:27648\n" \
"ds_read_b64_tr_b16 a[206:207], v33 offset:32256\n" \
"v_cvt_pk_bf16_f32 v160, v160, v161\n" \
"v_cvt_pk_bf16_f32 v161, v162, v163\n" \
"v_cvt_pk_bf16_f32 v162, v164, v165\n" \
"v_cvt_pk_bf16_f32 v163, v166, v167\n" \
"buffer_store_dwordx4 v[160:163], v20, s[56:59], s71 offen offset:0 sc1\n" \
"s_add_u32 s71, s71, 0x800\n" \
"v_mfma_f32_16x16x32_bf16 v[248:251], a[208:211], a[224:227], v[248:251]\n" \
"v_mfma_f32_16x16x32_bf16 v[252:255], a[208:211], a[240:243], v[252:255]\n" \
"ds_read_b64_tr_b16 a[208:209], v33 offset:36864\n" \
"ds_read_b64_tr_b16 a[210:211], v33 offset:41472\n" \
"v_mfma_f32_16x16x32_bf16 v[248:251], a[212:215], a[228:231], v[248:251]\n" \
"v_mfma_f32_16x16x32_bf16 v[252:255], a[212:215], a[244:247], v[252:255]\n" \
"ds_read_b64_tr_b16 a[212:213], v33 offset:46080\n" \
"ds_read_b64_tr_b16 a[214:215], v33 offset:50688\n" \
"v_mfma_f32_16x16x32_bf16 v[248:251], a[216:219], a[232:235], v[248:251]\n" \
"buffer_load_dwordx4 v[88:91], v32, s[56:59], s96 offen offset:0 sc1\n" \
"v_mfma_f32_16x16x32_bf16 v[252:255], a[216:219], a[248:251], v[252:255]\n" \
"ds_read_b64_tr_b16 a[216:217], v33 offset:55296\n" \
"s_waitcnt lgkmcnt(12)\n" \
"v_mfma_f32_16x16x32_bf16 v[176:179], a[0:3], a[128:131], v[176:179]\n" \
"ds_read_b64_tr_b16 a[218:219], v33 offset:59904\n" \
"s_waitcnt vmcnt(18)\n" \
"s_mov_b64 exec, 1\n" \
"buffer_store_dword v39, off, s[56:59], s66 offset:4 sc1\n" \
"s_mov_b64 exec, -1\n" \
"v_readlane_b32 s66, v11, 47\n" \
"v_mfma_f32_16x16x32_bf16 v[180:183], a[0:3], a[144:147], v[180:183]\n" \
"v_mfma_f32_16x16x32_bf16 v[248:251], a[0:3], a[160:163], v[248:251]\n" \
"v_mfma_f32_16x16x32_bf16 v[252:255], a[0:3], a[176:179], v[252:255]\n" \
"ds_read_b64_tr_b16 a[0:1], v10 offset:0\n" \
"ds_read_b64_tr_b16 a[2:3], v10 offset:4608\n" \
"v_mfma_f32_16x16x32_bf16 v[176:179], a[4:7], a[132:135], v[176:179]\n" \
"buffer_load_dwordx4 v[92:95], v32, s[56:59], s96 offen offset:1024 sc1\n" \
"v_mfma_f32_16x16x32_bf16 v[180:183], a[4:7], a[148:151], v[180:183]\n" \
"v_mfma_f32_16x16x32_bf16 v[248:251], a[4:7], a[164:167], v[248:251]\n" \
"v_mfma_f32_16x16x32_bf16 v[252:255], a[4:7], a[180:183], v[252:255]\n" \
"ds_read_b64_tr_b16 a[4:5], v34 offset:0\n" \
"ds_read_b64_tr_b16 a[6:7], v34 offset:4608\n" \
"v_mfma_f32_16x16x32_bf16 v[176:179], a[8:11], a[136:139], v[176:179]\n" \
"v_mfma_f32_16x16x32_bf16 v[180:183], a[8:11], a[152:155], v[180:183]\n" \
"buffer_load_dwordx4 v[96:99], v20, s[56:59], s97 offen offset:0 sc1\n" \
"v_mfma_f32_16x16x32_bf16 v[248:251], a[8:11], a[168:171], v[248:251]\n" \
"v_mfma_f32_16x16x32_bf16 v[252:255], a[8:11], a[184:187], v[252:255]\n" \
"ds_read_b64_tr_b16 a[8:9], v34 offset:9216\n" \
"ds_read_b64_tr_b16 a[10:11], v34 offset:13824\n" \
"v_mfma_f32_16x16x32_bf16 v[176:179], a[12:15], a[140:143], v[176:179]\n" \
"v_mfma_f32_16x16x32_bf16 v[180:183], a[12:15], a[156:159], v[180:183]\n" \
"v_mfma_f32_16x16x32_bf16 v[248:251], a[12:15], a[172:175], v[248:251]\n" \
"buffer_load_dwordx4 v[100:103], v20, s[56:59], s97 offen offset:1024 sc1\n" \
"v_mfma_f32_16x16x32_bf16 v[252:255], a[12:15], a[188:191], v[252:255]\n" \
"ds_read_b64_tr_b16 a[12:13], v34 offset:18432\n" \
"ds_read_b64_tr_b16 a[14:15], v34 offset:23040\n" \
"s_waitcnt vmcnt(19)\n" \
"s_mov_b64 exec, 1\n" \
"buffer_store_dword v39, off, s[56:59], s67 offset:8 sc1\n" \
"s_mov_b64 exec, -1\n" \
"v_readlane_b32 s67, v11, 48\n" \
"v_mfma_f32_16x16x32_bf16 v[176:179], a[16:19], a[32:35], v[176:179]\n" \
"v_mfma_f32_16x16x32_bf16 v[180:183], a[16:19], a[48:51], v[180:183]\n" \
"v_mfma_f32_16x16x32_bf16 v[248:251], a[16:19], a[96:99], v[248:251]\n" \
"v_mfma_f32_16x16x32_bf16 v[252:255], a[16:19], a[112:115], v[252:255]\n" \
"ds_read_b64_tr_b16 a[16:17], v10 offset:9216\n" \
"ds_read_b64_tr_b16 a[18:19], v10 offset:13824\n" \
"v_mfma_f32_16x16x32_bf16 v[176:179], a[20:23], a[36:39], v[176:179]\n" \
"buffer_load_dwordx4 v[104:107], v32, s[56:59], s97 offen offset:0 sc1\n" \
"v_mfma_f32_16x16x32_bf16 v[180:183], a[20:23], a[52:55], v[180:183]\n" \
"v_mfma_f32_16x16x32_bf16 v[248:251], a[20:23], a[100:103], v[248:251]\n" \
"v_mfma_f32_16x16x32_bf16 v[252:255], a[20:23], a[116:119], v[252:255]\n" \
"ds_read_b64_tr_b16 a[20:21], v33 offset:32\n" \
"ds_read_b64_tr_b16 a[22:23], v33 offset:4640\n" \
"v_mfma_f32_16x16x32_bf16 v[176:179], a[24:27], a[40:43], v[176:179]\n" \
"v_mfma_f32_16x16x32_bf16 v[180:183], a[24:27], a[56:59], v[180:183]\n" \
"buffer_load_dwordx4 v[108:111], v32, s[56:59], s97 offen offset:1024 sc1\n" \
"v_mfma_f32_16x16x32_bf16 v[248:251], a[24:27], a[104:107], v[248:251]\n" \
"s_waitcnt vmcnt(21)\n" \
".fill (L_ab + 40960 - .) / 4, 4, 0xBF800000\n" \
"L_ap10:\n" \
"s_cbranch_execz L_ap0\n" \
"v_mfma_f32_16x16x32_bf16 v[252:255], a[24:27], a[120:123], v[252:255]\n" \
"ds_read_b64_tr_b16 a[24:25], v33 offset:9248\n" \
"ds_read_b64_tr_b16 a[26:27], v33 offset:13856\n" \
"v_mfma_f32_16x16x32_bf16 v[176:179], a[28:31], a[44:47], v[176:179]\n" \
"v_mfma_f32_16x16x32_bf16 v[180:183], a[28:31], a[60:63], v[180:183]\n" \
"s_waitcnt vmcnt(20)\n" \
"v_mfma_f32_16x16x32_bf16 v[248:251], a[28:31], a[108:111], v[248:251]\n" \
"s_waitcnt vmcnt(19)\n" \
"v_mfma_f32_16x16x32_bf16 v[252:255], a[28:31], a[124:127], v[252:255]\n" \
"ds_read_b64_tr_b16 a[28:29], v33 offset:18464\n" \
"s_waitcnt vmcnt(18)\n" \
"s_mov_b64 exec, 1\n" \
"buffer_store_dword v39, off, s[56:59], s68 offset:12 sc1\n" \
"s_mov_b64 exec, -1\n" \
"v_cvt_pk_bf16_f32 v176, v176, v177\n" \
"v_cvt_pk_bf16_f32 v248, v248, v249\n" \
"v_cvt_pk_bf16_f32 v177, v178, v179\n" \
"v_cvt_pk_bf16_f32 v249, v250, v251\n" \
"v_cvt_pk_bf16_f32 v178, v180, v181\n" \
"v_cvt_pk_bf16_f32 v179, v182, v183\n" \
"v_cvt_pk_bf16_f32 v250, v252, v253\n" \
"v_cvt_pk_bf16_f32 v251, v254, v255\n" \
"buffer_store_dwordx4 v[176:179], v20, s[56:59], s72 offen offset:0 sc1\n" \
"s_waitcnt lgkmcnt(5)\n" \
"v_mfma_f32_16x16x32_bf16 v[184:187], a[192:195], a[220:223], v[184:187]\n" \
"v_readlane_b32 s68, v11, 49\n" \
"ds_read_b64_tr_b16 a[30:31], v33 offset:23072\n" \
"v_mfma_f32_16x16x32_bf16 v[188:191], a[192:195], a[236:239], v[188:191]\n" \
"ds_read_b64_tr_b16 a[192:193], v33 offset:27680\n" \
"ds_read_b64_tr_b16 a[194:195], v33 offset:32288\n" \
"buffer_store_dwordx4 v[248:251], v20, s[56:59], s72 offen offset:1024 sc1\n" \
"v_mfma_f32_16x16x32_bf16 v[184:187], a[196:199], a[224:227], v[184:187]\n" \
"v_mfma_f32_16x16x32_bf16 v[188:191], a[196:199], a[240:243], v[188:191]\n" \
"ds_read_b64_tr_b16 a[196:197], v33 offset:36896\n" \
"ds_read_b64_tr_b16 a[198:199], v33 offset:41504\n" \
"v_mfma_f32_16x16x32_bf16 v[184:187], a[200:203], a[228:231], v[184:187]\n" \
"v_mfma_f32_16x16x32_bf16 v[188:191], a[200:203], a[244:247], v[188:191]\n" \
"ds_read_b64_tr_b16 a[200:201], v33 offset:46112\n" \
"ds_read_b64_tr_b16 a[202:203], v33 offset:50720\n" \
"v_mfma_f32_16x16x32_bf16 v[184:187], a[204:207], a[232:235], v[184:187]\n" \
"v_mfma_f32_16x16x32_bf16 v[188:191], a[204:207], a[248:251], v[188:191]\n" \
"ds_read_b64_tr_b16 a[204:205], v33 offset:55328\n" \
"ds_read_b64_tr_b16 a[206:207], v33 offset:59936\n" \
"v_mfma_f32_16x16x32_bf16 v[184:187], a[208:211], a[160:163], v[184:187]\n" \
"v_mfma_f32_16x16x32_bf16 v[188:191], a[208:211], a[176:179], v[188:191]\n" \
"ds_read_b64_tr_b16 a[208:209], v10 offset:32\n" \
"ds_read_b64_tr_b16 a[210:211], v10 offset:4640\n" \
"v_mfma_f32_16x16x32_bf16 v[184:187], a[212:215], a[164:167], v[184:187]\n" \
"v_mfma_f32_16x16x32_bf16 v[188:191], a[212:215], a[180:183], v[188:191]\n" \
"ds_read_b64_tr_b16 a[212:213], v34 offset:32\n" \
"ds_read_b64_tr_b16 a[214:215], v34 offset:4640\n" \
"v_mfma_f32_16x16x32_bf16 v[184:187], a[216:219], a[168:171], v[184:187]\n" \
"v_mfma_f32_16x16x32_bf16 v[188:191], a[216:219], a[184:187], v[188:191]\n" \
"ds_read_b64_tr_b16 a[216:217], v34 offset:9248\n" \
"ds_read_b64_tr_b16 a[218:219], v34 offset:13856\n" \
"s_waitcnt vmcnt(18)\n" \
"s_mov_b64 exec, 1\n" \
"buffer_store_dword v39, off, s[56:59], s69 offset:16 sc1\n" \
"s_mov_b64 exec, -1\n" \
"v_mfma_f32_16x16x32_bf16 v[184:187], a[0:3], a[172:175], v[184:187]\n" \
"v_readlane_b32 s69, v11, 50\n" \
"v_mfma_f32_16x16x32_bf16 v[188:191], a[0:3], a[188:191], v[188:191]\n" \
"ds_read_b64_tr_b16 a[0:1], v34 offset:18464\n" \
"ds_read_b64_tr_b16 a[2:3], v34 offset:23072\n" \
"v_mfma_f32_16x16x32_bf16 v[184:187], a[4:7], a[96:99], v[184:187]\n" \
"v_mfma_f32_16x16x32_bf16 v[188:191], a[4:7], a[112:115], v[188:191]\n" \
"ds_read_b64_tr_b16 a[4:5], v10 offset:9248\n" \
"ds_read_b64_tr_b16 a[6:7], v10 offset:13856\n" \
"v_mfma_f32_16x16x32_bf16 v[184:187], a[8:11], a[100:103], v[184:187]\n" \
"v_mfma_f32_16x16x32_bf16 v[188:191], a[8:11], a[116:119], v[188:191]\n" \
"ds_read_b64_tr_b16 a[8:9], v33 offset:64\n" \
"ds_read_b64_tr_b16 a[10:11], v33 offset:4672\n" \
"v_mfma_f32_16x16x32_bf16 v[184:187], a[12:15], a[104:107], v[184:187]\n" \
"v_mfma_f32_16x16x32_bf16 v[188:191], a[12:15], a[120:123], v[188:191]\n" \
"ds_read_b64_tr_b16 a[12:13], v33 offset:9280\n" \
"ds_read_b64_tr_b16 a[14:15], v33 offset:13888\n" \
"v_mfma_f32_16x16x32_bf16 v[184:187], a[16:19], a[108:111], v[184:187]\n" \
"v_mfma_f32_16x16x32_bf16 v[188:191], a[16:19], a[124:127], v[188:191]\n" \
"ds_read_b64_tr_b16 a[16:17], v33 offset:18496\n" \
"buffer_load_dword v37, v40, s[56:59], s74 offen sc1\n" \
"s_waitcnt lgkmcnt(12)\n" \
"v_mfma_f32_16x16x32_bf16 v[192:195], a[20:23], a[220:223], v[192:195]\n" \
"ds_read_b64_tr_b16 a[18:19], v33 offset:23104\n" \
"v_mfma_f32_16x16x32_bf16 v[196:199], a[20:23], a[236:239], v[196:199]\n" \
"ds_read_b64_tr_b16 a[20:21], v33 offset:27712\n" \
"v_cvt_pk_bf16_f32 v184, v184, v185\n" \
"v_cvt_pk_bf16_f32 v185, v186, v187\n" \
"v_cvt_pk_bf16_f32 v186, v188, v189\n" \
"v_cvt_pk_bf16_f32 v187, v190, v191\n" \
"buffer_store_dwordx4 v[184:187], v20, s[56:59], s92 offen offset:1024 sc1\n" \
"s_add_u32 s92, s92, 0x3800\n" \
"v_mfma_f32_16x16x32_bf16 v[192:195], a[24:27], a[224:227], v[192:195]\n" \
"ds_read_b64_tr_b16 a[22:23], v33 offset:32320\n" \
"v_mfma_f32_16x16x32_bf16 v[196:199], a[24:27], a[240:243], v[196:199]\n" \
"ds_read_b64_tr_b16 a[24:25], v33 offset:36928\n" \
"ds_read_b64_tr_b16 a[26:27], v33 offset:41536\n" \
"s_waitcnt vmcnt(18)\n" \
"s_mov_b64 exec, 1\n" \
"buffer_store_dword v39, off, s[56:59], s70 offset:20 sc1\n" \
"s_mov_b64 exec, -1\n" \
"v_mfma_f32_16x16x32_bf16 v[192:195], a[28:31], a[228:231], v[192:195]\n" \
"v_mfma_f32_16x16x32_bf16 v[196:199], a[28:31], a[244:247], v[196:199]\n" \
"ds_read_b64_tr_b16 a[28:29], v33 offset:46144\n" \
"ds_read_b64_tr_b16 a[30:31], v33 offset:50752\n" \
"v_mfma_f32_16x16x32_bf16 v[192:195], a[192:195], a[232:235], v[192:195]\n" \
"v_mfma_f32_16x16x32_bf16 v[196:199], a[192:195], a[248:251], v[196:199]\n" \
"ds_read_b64_tr_b16 a[192:193], v33 offset:55360\n" \
"ds_read_b64_tr_b16 a[194:195], v33 offset:59968\n" \
"v_mfma_f32_16x16x32_bf16 v[192:195], a[196:199], a[160:163], v[192:195]\n" \
"v_mfma_f32_16x16x32_bf16 v[196:199], a[196:199], a[176:179], v[196:199]\n" \
"ds_read_b64_tr_b16 a[196:197], v10 offset:64\n" \
"ds_read_b64_tr_b16 a[198:199], v10 offset:4672\n" \
"v_mfma_f32_16x16x32_bf16 v[192:195], a[200:203], a[164:167], v[192:195]\n" \
"v_mfma_f32_16x16x32_bf16 v[196:199], a[200:203], a[180:183], v[196:199]\n" \
"ds_read_b64_tr_b16 a[200:201], v34 offset:64\n" \
"ds_read_b64_tr_b16 a[202:203], v34 offset:4672\n" \
"v_mfma_f32_16x16x32_bf16 v[192:195], a[204:207], a[168:171], v[192:195]\n" \
"v_mfma_f32_16x16x32_bf16 v[196:199], a[204:207], a[184:187], v[196:199]\n" \
"ds_read_b64_tr_b16 a[204:205], v34 offset:9280\n" \
"ds_read_b64_tr_b16 a[206:207], v34 offset:13888\n" \
"v_mfma_f32_16x16x32_bf16 v[192:195], a[208:211], a[172:175], v[192:195]\n" \
"v_mfma_f32_16x16x32_bf16 v[196:199], a[208:211], a[188:191], v[196:199]\n" \
"ds_read_b64_tr_b16 a[208:209], v34 offset:18496\n" \
"s_waitcnt lgkmcnt(13)\n" \
"v_mfma_f32_16x16x32_bf16 v[192:195], a[212:215], a[96:99], v[192:195]\n" \
"ds_read_b64_tr_b16 a[210:211], v34 offset:23104\n" \
"v_mfma_f32_16x16x32_bf16 v[196:199], a[212:215], a[112:115], v[196:199]\n" \
"ds_read_b64_tr_b16 a[212:213], v10 offset:9280\n" \
"ds_read_b64_tr_b16 a[214:215], v10 offset:13888\n" \
"v_mfma_f32_16x16x32_bf16 v[192:195], a[216:219], a[100:103], v[192:195]\n" \
"v_mfma_f32_16x16x32_bf16 v[196:199], a[216:219], a[116:119], v[196:199]\n" \
"ds_read_b64_tr_b16 a[216:217], v33 offset:96\n" \
"ds_read_b64_tr_b16 a[218:219], v33 offset:4704\n" \
"s_waitcnt vmcnt(15)\n" \
"s_mov_b64 exec, 1\n" \
"buffer_store_dword v39, off, s[56:59], s71 offset:24 sc1\n" \
"s_mov_b64 exec, -1\n" \
"v_mfma_f32_16x16x32_bf16 v[192:195], a[0:3], a[104:107], v[192:195]\n" \
"v_mfma_f32_16x16x32_bf16 v[196:199], a[0:3], a[120:123], v[196:199]\n" \
"ds_read_b64_tr_b16 a[0:1], v33 offset:9312\n" \
"ds_read_b64_tr_b16 a[2:3], v33 offset:13920\n" \
"v_mfma_f32_16x16x32_bf16 v[192:195], a[4:7], a[108:111], v[192:195]\n" \
"v_mfma_f32_16x16x32_bf16 v[196:199], a[4:7], a[124:127], v[196:199]\n" \
"ds_read_b64_tr_b16 a[4:5], v33 offset:18528\n" \
"ds_read_b64_tr_b16 a[6:7], v33 offset:23136\n" \
"v_mfma_f32_16x16x32_bf16 v[200:203], a[8:11], a[220:223], v[200:203]\n" \
"v_mfma_f32_16x16x32_bf16 v[204:207], a[8:11], a[236:239], v[204:207]\n" \
"ds_read_b64_tr_b16 a[8:9], v33 offset:27744\n" \
"ds_read_b64_tr_b16 a[10:11], v33 offset:32352\n" \
"v_mfma_f32_16x16x32_bf16 v[200:203], a[12:15], a[224:227], v[200:203]\n" \
"v_cvt_pk_bf16_f32 v192, v192, v193\n" \
"v_cvt_pk_bf16_f32 v193, v194, v195\n" \
"v_cvt_pk_bf16_f32 v194, v196, v197\n" \
"v_cvt_pk_bf16_f32 v195, v198, v199\n" \
"buffer_store_dwordx4 v[192:195], v20, s[56:59], s66 offen offset:1024 sc1\n" \
"v_mfma_f32_16x16x32_bf16 v[204:207], a[12:15], a[240:243], v[204:207]\n" \
"ds_read_b64_tr_b16 a[12:13], v33 offset:36960\n" \
"ds_read_b64_tr_b16 a[14:15], v33 offset:41568\n" \
"v_mfma_f32_16x16x32_bf16 v[200:203], a[16:19], a[228:231], v[200:203]\n" \
"v_mfma_f32_16x16x32_bf16 v[204:207], a[16:19], a[244:247], v[204:207]\n" \
"ds_read_b64_tr_b16 a[16:17], v33 offset:46176\n" \
"ds_read_b64_tr_b16 a[18:19], v33 offset:50784\n" \
"v_mfma_f32_16x16x32_bf16 v[200:203], a[20:23], a[232:235], v[200:203]\n" \
"v_mfma_f32_16x16x32_bf16 v[204:207], a[20:23], a[248:251], v[204:207]\n" \
"ds_read_b64_tr_b16 a[20:21], v33 offset:55392\n" \
"s_waitcnt lgkmcnt(11)\n" \
"v_mfma_f32_16x16x32_bf16 v[200:203], a[24:27], a[160:163], v[200:203]\n" \
"ds_read_b64_tr_b16 a[22:23], v33 offset:60000\n" \
"v_mfma_f32_16x16x32_bf16 v[204:207], a[24:27], a[176:179], v[204:207]\n" \
"ds_read_b64_tr_b16 a[24:25], v10 offset:96\n" \
"ds_read_b64_tr_b16 a[26:27], v10 offset:4704\n" \
"v_mfma_f32_16x16x32_bf16 v[200:203], a[28:31], a[164:167], v[200:203]\n" \
"v_mfma_f32_16x16x32_bf16 v[204:207], a[28:31], a[180:183], v[204:207]\n" \
"ds_read_b64_tr_b16 a[28:29], v34 offset:96\n" \
"ds_read_b64_tr_b16 a[30:31], v34 offset:4704\n" \
"v_mfma_f32_16x16x32_bf16 v[200:203], a[192:195], a[168:171], v[200:203]\n" \
"v_mfma_f32_16x16x32_bf16 v[204:207], a[192:195], a[184:187], v[204:207]\n" \
"ds_read_b64_tr_b16 a[192:193], v34 offset:9312\n" \
"ds_read_b64_tr_b16 a[194:195], v34 offset:13920\n" \
"v_mfma_f32_16x16x32_bf16 v[200:203], a[196:199], a[172:175], v[200:203]\n" \
"v_mfma_f32_16x16x32_bf16 v[204:207], a[196:199], a[188:191], v[204:207]\n" \
"ds_read_b64_tr_b16 a[196:197], v34 offset:18528\n" \
"ds_read_b64_tr_b16 a[198:199], v34 offset:23136\n" \
"v_mfma_f32_16x16x32_bf16 v[200:203], a[200:203], a[96:99], v[200:203]\n" \
"v_mfma_f32_16x16x32_bf16 v[204:207], a[200:203], a[112:115], v[204:207]\n" \
"ds_read_b64_tr_b16 a[200:201], v10 offset:9312\n" \
"ds_read_b64_tr_b16 a[202:203], v10 offset:13920\n" \
"v_mfma_f32_16x16x32_bf16 v[200:203], a[204:207], a[100:103], v[200:203]\n" \
"v_mfma_f32_16x16x32_bf16 v[204:207], a[204:207], a[116:119], v[204:207]\n" \
"ds_read_b64_tr_b16 a[204:205], v33 offset:128\n" \
"ds_read_b64_tr_b16 a[206:207], v33 offset:4736\n" \
"v_mfma_f32_16x16x32_bf16 v[200:203], a[208:211], a[104:107], v[200:203]\n" \
"v_mfma_f32_16x16x32_bf16 v[204:207], a[208:211], a[120:123], v[204:207]\n" \
"ds_read_b64_tr_b16 a[208:209], v33 offset:9344\n" \
"ds_read_b64_tr_b16 a[210:211], v33 offset:13952\n" \
"v_mfma_f32_16x16x32_bf16 v[200:203], a[212:215], a[108:111], v[200:203]\n" \
"v_mfma_f32_16x16x32_bf16 v[204:207], a[212:215], a[124:127], v[204:207]\n" \
"ds_read_b64_tr_b16 a[212:213], v33 offset:18560\n" \
"ds_read_b64_tr_b16 a[214:215], v33 offset:23168\n" \
"v_mfma_f32_16x16x32_bf16 v[208:211], a[216:219], a[220:223], v[208:211]\n" \
"v_mfma_f32_16x16x32_bf16 v[212:215], a[216:219], a[236:239], v[212:215]\n" \
"ds_read_b64_tr_b16 a[216:217], v33 offset:27776\n" \
"s_waitcnt lgkmcnt(13)\n" \
"v_mfma_f32_16x16x32_bf16 v[208:211], a[0:3], a[224:227], v[208:211]\n" \
"v_cvt_pk_bf16_f32 v200, v200, v201\n" \
"v_cvt_pk_bf16_f32 v201, v202, v203\n" \
"v_cvt_pk_bf16_f32 v202, v204, v205\n" \
"v_cvt_pk_bf16_f32 v203, v206, v207\n" \
"buffer_store_dwordx4 v[200:203], v20, s[56:59], s67 offen offset:1024 sc1\n" \
"v_mfma_f32_16x16x32_bf16 v[212:215], a[0:3], a[240:243], v[212:215]\n" \
"ds_read_b64_tr_b16 a[218:219], v33 offset:32384\n" \
"ds_read_b64_tr_b16 a[0:1], v33 offset:36992\n" \
"v_mfma_f32_16x16x32_bf16 v[208:211], a[4:7], a[228:231], v[208:211]\n" \
"ds_read_b64_tr_b16 a[2:3], v33 offset:41600\n" \
"v_mfma_f32_16x16x32_bf16 v[212:215], a[4:7], a[244:247], v[212:215]\n" \
"ds_read_b64_tr_b16 a[4:5], v33 offset:46208\n" \
"ds_read_b64_tr_b16 a[6:7], v33 offset:50816\n" \
"v_mfma_f32_16x16x32_bf16 v[208:211], a[8:11], a[232:235], v[208:211]\n" \
"v_mfma_f32_16x16x32_bf16 v[212:215], a[8:11], a[248:251], v[212:215]\n" \
"ds_read_b64_tr_b16 a[8:9], v33 offset:55424\n" \
"ds_read_b64_tr_b16 a[10:11], v33 offset:60032\n" \
"v_mfma_f32_16x16x32_bf16 v[208:211], a[12:15], a[160:163], v[208:211]\n" \
"v_mfma_f32_16x16x32_bf16 v[212:215], a[12:15], a[176:179], v[212:215]\n" \
"ds_read_b64_tr_b16 a[12:13], v10 offset:128\n" \
"ds_read_b64_tr_b16 a[14:15], v10 offset:4736\n" \
"v_mfma_f32_16x16x32_bf16 v[208:211], a[16:19], a[164:167], v[208:211]\n" \
"s_waitcnt vmcnt(5)\n" \
"v_cmp_eq_u32_e64 s[98:99], s62, v37\n" \
"s_mov_b64 exec, 3\n" \
"buffer_store_dword v39, v41, s[56:59], s73 offen sc1\n" \
"s_mov_b64 exec, -1\n" \
"s_andn2_b32 s75, 0x7f, s98\n" \
"s_mov_b32 s20, s75\n" \
"s_bitcmp1_b32 s20, 0\n" \
"s_cselect_b32 s93, 0x40000000, s12\n" \
"s_bitcmp1_b32 s20, 1\n" \
"s_cselect_b32 s94, 0x40000000, s12\n" \
"s_bitcmp1_b32 s20, 2\n" \
"s_cselect_b32 s95, 0x40000000, s13\n" \
"s_bitcmp1_b32 s20, 3\n" \
"s_cselect_b32 s96, 0x40000000, s13\n" \
"s_bitcmp1_b32 s20, 4\n" \
"s_cselect_b32 s97, 0x40000000, s14\n" \
"s_bitcmp1_b32 s20, 5\n" \
"s_cselect_b32 s64, 0x40000000, s14\n" \
"s_bitcmp1_b32 s20, 6\n" \
"s_cselect_b32 s65, 0x40000000, s15\n" \
"s_add_u32 s66, s66, 0x3000\n" \
"s_add_u32 s67, s67, 0x2800\n" \
"v_mfma_f32_16x16x32_bf16 v[212:215], a[16:19], a[180:183], v[212:215]\n" \
"buffer_load_dwordx4 v[116:119], v20, s[56:59], s93 offen offset:0 sc1\n" \
"v_mfma_f32_16x16x32_bf16 v[208:211], a[20:23], a[168:171], v[208:211]\n" \
"ds_read_b64_tr_b16 a[16:17], v34 offset:128\n" \
"ds_read_b64_tr_b16 a[18:19], v34 offset:4736\n" \
"v_mfma_f32_16x16x32_bf16 v[212:215], a[20:23], a[184:187], v[212:215]\n" \
"ds_read_b64_tr_b16 a[20:21], v34 offset:9344\n" \
"ds_read_b64_tr_b16 a[22:23], v34 offset:13952\n" \
"v_mfma_f32_16x16x32_bf16 v[208:211], a[24:27], a[172:175], v[208:211]\n" \
"v_mfma_f32_16x16x32_bf16 v[212:215], a[24:27], a[188:191], v[212:215]\n" \
"ds_read_b64_tr_b16 a[24:25], v34 offset:18560\n" \
"ds_read_b64_tr_b16 a[26:27], v34 offset:23168\n" \
"v_mfma_f32_16x16x32_bf16 v[208:211], a[28:31], a[96:99], v[208:211]\n" \
"v_mfma_f32_16x16x32_bf16 v[212:215], a[28:31], a[112:115], v[212:215]\n" \
"ds_read_b64_tr_b16 a[28:29], v10 offset:9344\n" \
"s_waitcnt lgkmcnt(9)\n" \
"v_mfma_f32_16x16x32_bf16 v[208:211], a[192:195], a[100:103], v[208:211]\n" \
"ds_read_b64_tr_b16 a[30:31], v10 offset:13952\n" \
"v_mfma_f32_16x16x32_bf16 v[212:215], a[192:195], a[116:119], v[212:215]\n" \
"ds_read_b64_tr_b16 a[192:193], v33 offset:160\n" \
"ds_read_b64_tr_b16 a[194:195], v33 offset:4768\n" \
"v_mfma_f32_16x16x32_bf16 v[208:211], a[196:199], a[104:107], v[208:211]\n" \
"v_mfma_f32_16x16x32_bf16 v[212:215], a[196:199], a[120:123], v[212:215]\n" \
"ds_read_b64_tr_b16 a[196:197], v33 offset:9376\n" \
"ds_read_b64_tr_b16 a[198:199], v33 offset:13984\n" \
"v_mfma_f32_16x16x32_bf16 v[208:211], a[200:203], a[108:111], v[208:211]\n" \
"v_mfma_f32_16x16x32_bf16 v[212:215], a[200:203], a[124:127], v[212:215]\n" \
"ds_read_b64_tr_b16 a[200:201], v33 offset:18592\n" \
"ds_read_b64_tr_b16 a[202:203], v33 offset:23200\n" \
"v_mfma_f32_16x16x32_bf16 v[216:219], a[204:207], a[220:223], v[216:219]\n" \
"v_mfma_f32_16x16x32_bf16 v[220:223], a[204:207], a[236:239], v[220:223]\n" \
"buffer_load_dwordx4 v[124:127], v20, s[56:59], s94 offen offset:2048 sc1\n" \
"v_mfma_f32_16x16x32_bf16 v[216:219], a[208:211], a[224:227], v[216:219]\n" \
"ds_read_b64_tr_b16 a[204:205], v33 offset:27808\n" \
"v_cvt_pk_bf16_f32 v208, v208, v209\n" \
"v_cvt_pk_bf16_f32 v209, v210, v211\n" \
"v_cvt_pk_bf16_f32 v210, v212, v213\n" \
"v_cvt_pk_bf16_f32 v211, v214, v215\n" \
"v_mfma_f32_16x16x32_bf16 v[220:223], a[208:211], a[240:243], v[220:223]\n" \
"buffer_store_dwordx4 v[208:211], v20, s[56:59], s68 offen offset:1024 sc1\n" \
"s_add_u32 s68, s68, 0x2000\n" \
"v_mfma_f32_16x16x32_bf16 v[216:219], a[212:215], a[228:231], v[216:219]\n" \
"ds_read_b64_tr_b16 a[206:207], v33 offset:32416\n" \
"ds_read_b64_tr_b16 a[208:209], v33 offset:37024\n" \
"v_mfma_f32_16x16x32_bf16 v[220:223], a[212:215], a[244:247], v[220:223]\n" \
"ds_read_b64_tr_b16 a[210:211], v33 offset:41632\n" \
"ds_read_b64_tr_b16 a[212:213], v33 offset:46240\n" \
"v_mfma_f32_16x16x32_bf16 v[216:219], a[216:219], a[232:235], v[216:219]\n" \
"ds_read_b64_tr_b16 a[214:215], v33 offset:50848\n" \
"v_mfma_f32_16x16x32_bf16 v[220:223], a[216:219], a[248:251], v[220:223]\n" \
"ds_read_b64_tr_b16 a[216:217], v33 offset:55456\n" \
"ds_read_b64_tr_b16 a[218:219], v33 offset:60064\n" \
"v_mfma_f32_16x16x32_bf16 v[216:219], a[0:3], a[160:163], v[216:219]\n" \
"v_mfma_f32_16x16x32_bf16 v[220:223], a[0:3], a[176:179], v[220:223]\n" \
"ds_read_b64_tr_b16 a[0:1], v10 offset:160\n" \
"ds_read_b64_tr_b16 a[2:3], v10 offset:4768\n" \
"v_mfma_f32_16x16x32_bf16 v[216:219], a[4:7], a[164:167], v[216:219]\n" \
"v_mfma_f32_16x16x32_bf16 v[220:223], a[4:7], a[180:183], v[220:223]\n" \
"ds_read_b64_tr_b16 a[4:5], v34 offset:160\n" \
"ds_read_b64_tr_b16 a[6:7], v34 offset:4768\n" \
"v_mfma_f32_16x16x32_bf16 v[216:219], a[8:11], a[168:171], v[216:219]\n" \
"s_waitcnt vmcnt(8)\n" \
"s_mov_b64 exec, 1\n" \
"buffer_store_dword v39, off, s[56:59], s92 offset:32 sc1\n" \
"s_mov_b64 exec, -1\n" \
"v_mfma_f32_16x16x32_bf16 v[220:223], a[8:11], a[184:187], v[220:223]\n" \
"ds_read_b64_tr_b16 a[8:9], v34 offset:9376\n" \
"s_waitcnt lgkmcnt(13)\n" \
"v_mfma_f32_16x16x32_bf16 v[216:219], a[12:15], a[172:175], v[216:219]\n" \
"ds_read_b64_tr_b16 a[10:11], v34 offset:13984\n" \
"v_mfma_f32_16x16x32_bf16 v[220:223], a[12:15], a[188:191], v[220:223]\n" \
"ds_read_b64_tr_b16 a[12:13], v34 offset:18592\n" \
"ds_read_b64_tr_b16 a[14:15], v34 offset:23200\n" \
"v_mfma_f32_16x16x32_bf16 v[216:219], a[16:19], a[96:99], v[216:219]\n" \
"buffer_load_dwordx4 v[132:135], v20, s[56:59], s95 offen offset:0 sc1\n" \
"v_mfma_f32_16x16x32_bf16 v[220:223], a[16:19], a[112:115], v[220:223]\n" \
"ds_read_b64_tr_b16 a[16:17], v10 offset:9376\n" \
"ds_read_b64_tr_b16 a[18:19], v10 offset:13984\n" \
"v_mfma_f32_16x16x32_bf16 v[216:219], a[20:23], a[100:103], v[216:219]\n" \
"v_mfma_f32_16x16x32_bf16 v[220:223], a[20:23], a[116:119], v[220:223]\n" \
"ds_read_b64_tr_b16 a[20:21], v33 offset:192\n" \
"ds_read_b64_tr_b16 a[22:23], v33 offset:4800\n" \
"v_mfma_f32_16x16x32_bf16 v[216:219], a[24:27], a[104:107], v[216:219]\n" \
"v_mfma_f32_16x16x32_bf16 v[220:223], a[24:27], a[120:123], v[220:223]\n" \
"ds_read_b64_tr_b16 a[24:25], v33 offset:9408\n" \
"ds_read_b64_tr_b16 a[26:27], v33 offset:14016\n" \
"v_mfma_f32_16x16x32_bf16 v[216:219], a[28:31], a[108:111], v[216:219]\n" \
"v_mfma_f32_16x16x32_bf16 v[220:223], a[28:31], a[124:127], v[220:223]\n" \
"buffer_load_dword v37, v40, s[56:59], s74 offen sc1\n" \
"v_mfma_f32_16x16x32_bf16 v[224:227], a[192:195], a[220:223], v[224:227]\n" \
"ds_read_b64_tr_b16 a[28:29], v33 offset:18624\n" \
"ds_read_b64_tr_b16 a[30:31], v33 offset:23232\n" \
"v_mfma_f32_16x16x32_bf16 v[228:231], a[192:195], a[236:239], v[228:231]\n" \
"ds_read_b64_tr_b16 a[192:193], v33 offset:27840\n" \
"ds_read_b64_tr_b16 a[194:195], v33 offset:32448\n" \
"v_cvt_pk_bf16_f32 v216, v216, v217\n" \
"v_cvt_pk_bf16_f32 v217, v218, v219\n" \
"v_cvt_pk_bf16_f32 v218, v220, v221\n" \
"v_cvt_pk_bf16_f32 v219, v222, v223\n" \
"buffer_store_dwordx4 v[216:219], v20, s[56:59], s69 offen offset:1024 sc1\n" \
"s_add_u32 s69, s69, 0x1800\n" \
"v_mfma_f32_16x16x32_bf16 v[224:227], a[196:199], a[224:227], v[224:227]\n" \
"v_mfma_f32_16x16x32_bf16 v[228:231], a[196:199], a[240:243], v[228:231]\n" \
"ds_read_b64_tr_b16 a[196:197], v33 offset:37056\n" \
"ds_read_b64_tr_b16 a[198:199], v33 offset:41664\n" \
"v_mfma_f32_16x16x32_bf16 v[224:227], a[200:203], a[228:231], v[224:227]\n" \
"v_mfma_f32_16x16x32_bf16 v[228:231], a[200:203], a[244:247], v[228:231]\n" \
"ds_read_b64_tr_b16 a[200:201], v33 offset:46272\n" \
"s_waitcnt lgkmcnt(12)\n" \
"v_mfma_f32_16x16x32_bf16 v[224:227], a[204:207], a[232:235], v[224:227]\n" \
"buffer_load_dwordx4 v[140:143], v20, s[56:59], s96 offen offset:2048 sc1\n" \
"v_mfma_f32_16x16x32_bf16 v[228:231], a[204:207], a[248:251], v[228:231]\n" \
"ds_read_b64_tr_b16 a[202:203], v33 offset:50880\n" \
"ds_read_b64_tr_b16 a[204:205], v33 offset:55488\n" \
"v_mfma_f32_16x16x32_bf16 v[224:227], a[208:211], a[160:163], v[224:227]\n" \
"ds_read_b64_tr_b16 a[206:207], v33 offset:60096\n" \
"v_mfma_f32_16x16x32_bf16 v[228:231], a[208:211], a[176:179], v[228:231]\n" \
"ds_read_b64_tr_b16 a[208:209], v10 offset:192\n" \
"ds_read_b64_tr_b16 a[210:211], v10 offset:4800\n" \
"v_mfma_f32_16x16x32_bf16 v[224:227], a[212:215], a[164:167], v[224:227]\n" \
"v_mfma_f32_16x16x32_bf16 v[228:231], a[212:215], a[180:183], v[228:231]\n" \
"ds_read_b64_tr_b16 a[212:213], v34 offset:192\n" \
"ds_read_b64_tr_b16 a[214:215], v34 offset:4800\n" \
"v_mfma_f32_16x16x32_bf16 v[224:227], a[216:219], a[168:171], v[224:227]\n" \
"v_mfma_f32_16x16x32_bf16 v[228:231], a[216:219], a[184:187], v[228:231]\n" \
"ds_read_b64_tr_b16 a[216:217], v34 offset:9408\n" \
"ds_read_b64_tr_b16 a[218:219], v34 offset:14016\n" \
"v_mfma_f32_16x16x32_bf16 v[224:227], a[0:3], a[172:175], v[224:227]\n" \
"v_mfma_f32_16x16x32_bf16 v[228:231], a[0:3], a[188:191], v[228:231]\n" \
"ds_read_b64_tr_b16 a[0:1], v34 offset:18624\n" \
"ds_read_b64_tr_b16 a[2:3], v34 offset:23232\n" \
"s_waitcnt vmcnt(10)\n" \
"s_mov_b64 exec, 1\n" \
"buffer_store_dword v39, off, s[56:59], s66 offset:36 sc1\n" \
"s_mov_b64 exec, -1\n" \
"v_mfma_f32_16x16x32_bf16 v[224:227], a[4:7], a[96:99], v[224:227]\n" \
"v_mfma_f32_16x16x32_bf16 v[228:231], a[4:7], a[112:115], v[228:231]\n" \
"ds_read_b64_tr_b16 a[4:5], v10 offset:9408\n" \
"ds_read_b64_tr_b16 a[6:7], v10 offset:14016\n" \
"v_mfma_f32_16x16x32_bf16 v[224:227], a[8:11], a[100:103], v[224:227]\n" \
"v_mfma_f32_16x16x32_bf16 v[228:231], a[8:11], a[116:119], v[228:231]\n" \
"buffer_load_dwordx4 v[148:151], v20, s[56:59], s97 offen offset:0 sc1\n" \
"v_mfma_f32_16x16x32_bf16 v[224:227], a[12:15], a[104:107], v[224:227]\n" \
"ds_read_b64_tr_b16 a[8:9], v33 offset:224\n" \
"ds_read_b64_tr_b16 a[10:11], v33 offset:4832\n" \
"v_mfma_f32_16x16x32_bf16 v[228:231], a[12:15], a[120:123], v[228:231]\n" \
"ds_read_b64_tr_b16 a[12:13], v33 offset:9440\n" \
"s_waitcnt lgkmcnt(13)\n" \
"v_mfma_f32_16x16x32_bf16 v[224:227], a[16:19], a[108:111], v[224:227]\n" \
"ds_read_b64_tr_b16 a[14:15], v33 offset:14048\n" \
"v_mfma_f32_16x16x32_bf16 v[228:231], a[16:19], a[124:127], v[228:231]\n" \
"ds_read_b64_tr_b16 a[16:17], v33 offset:18656\n" \
"ds_read_b64_tr_b16 a[18:19], v33 offset:23264\n" \
"v_mfma_f32_16x16x32_bf16 v[232:235], a[20:23], a[220:223], v[232:235]\n" \
"v_mfma_f32_16x16x32_bf16 v[236:239], a[20:23], a[236:239], v[236:239]\n" \
"ds_read_b64_tr_b16 a[20:21], v33 offset:27872\n" \
"ds_read_b64_tr_b16 a[22:23], v33 offset:32480\n" \
"v_cvt_pk_bf16_f32 v224, v224, v225\n" \
"v_cvt_pk_bf16_f32 v225, v226, v227\n" \
"v_cvt_pk_bf16_f32 v226, v228, v229\n" \
"v_cvt_pk_bf16_f32 v227, v230, v231\n" \
"v_mfma_f32_16x16x32_bf16 v[232:235], a[24:27], a[224:227], v[232:235]\n" \
"v_mfma_f32_16x16x32_bf16 v[236:239], a[24:27], a[240:243], v[236:239]\n" \
"ds_read_b64_tr_b16 a[24:25], v33 offset:37088\n" \
"ds_read_b64_tr_b16 a[26:27], v33 offset:41696\n" \
"v_mfma_f32_16x16x32_bf16 v[232:235], a[28:31], a[228:231], v[232:235]\n" \
"v_mfma_f32_16x16x32_bf16 v[236:239], a[28:31], a[244:247], v[236:239]\n" \
"ds_read_b64_tr_b16 a[28:29], v33 offset:46304\n" \
"ds_read_b64_tr_b16 a[30:31], v33 offset:50912\n" \
"v_mfma_f32_16x16x32_bf16 v[232:235], a[192:195], a[232:235], v[232:235]\n" \
"v_mfma_f32_16x16x32_bf16 v[236:239], a[192:195], a[248:251], v[236:239]\n" \
"ds_read_b64_tr_b16 a[192:193], v33 offset:55520\n" \
"ds_read_b64_tr_b16 a[194:195], v33 offset:60128\n" \
"v_mfma_f32_16x16x32_bf16 v[232:235], a[196:199], a[160:163], v[232:235]\n" \
"v_mfma_f32_16x16x32_bf16 v[236:239], a[196:199], a[176:179], v[236:239]\n" \
"buffer_load_dwordx4 v[156:159], v20, s[56:59], s64 offen offset:2048 sc1\n" \
"v_readlane_b32 s64, v11, 51\n" \
"v_mfma_f32_16x16x32_bf16 v[232:235], a[200:203], a[164:167], v[232:235]\n" \
"ds_read_b64_tr_b16 a[196:197], v10 offset:224\n" \
"ds_read_b64_tr_b16 a[198:199], v10 offset:4832\n" \
"v_mfma_f32_16x16x32_bf16 v[236:239], a[200:203], a[180:183], v[236:239]\n" \
"ds_read_b64_tr_b16 a[200:201], v34 offset:224\n" \
"ds_read_b64_tr_b16 a[202:203], v34 offset:4832\n" \
"buffer_store_dwordx4 v[224:227], v20, s[56:59], s64 offen offset:1024 sc1\n" \
"v_mfma_f32_16x16x32_bf16 v[232:235], a[204:207], a[168:171], v[232:235]\n" \
"v_mfma_f32_16x16x32_bf16 v[236:239], a[204:207], a[184:187], v[236:239]\n" \
"ds_read_b64_tr_b16 a[204:205], v34 offset:9440\n" \
"s_waitcnt vmcnt(13)\n" \
"s_mov_b64 exec, 1\n" \
"buffer_store_dword v39, off, s[56:59], s67 offset:40 sc1\n" \
"s_mov_b64 exec, -1\n" \
"s_waitcnt lgkmcnt(11)\n" \
"v_mfma_f32_16x16x32_bf16 v[232:235], a[208:211], a[172:175], v[232:235]\n" \
"ds_read_b64_tr_b16 a[206:207], v34 offset:14048\n" \
"v_mfma_f32_16x16x32_bf16 v[236:239], a[208:211], a[188:191], v[236:239]\n" \
"ds_read_b64_tr_b16 a[208:209], v34 offset:18656\n" \
"ds_read_b64_tr_b16 a[210:211], v34 offset:23264\n" \
"v_mfma_f32_16x16x32_bf16 v[232:235], a[212:215], a[96:99], v[232:235]\n" \
"v_mfma_f32_16x16x32_bf16 v[236:239], a[212:215], a[112:115], v[236:239]\n" \
"ds_read_b64_tr_b16 a[212:213], v10 offset:9440\n" \
"ds_read_b64_tr_b16 a[214:215], v10 offset:14048\n" \
"v_mfma_f32_16x16x32_bf16 v[232:235], a[216:219], a[100:103], v[232:235]\n" \
"v_mfma_f32_16x16x32_bf16 v[236:239], a[216:219], a[116:119], v[236:239]\n" \
"ds_read_b64_tr_b16 a[216:217], v36 offset:0\n" \
"ds_read_b64_tr_b16 a[218:219], v36 offset:1024\n" \
"v_mfma_f32_16x16x32_bf16 v[232:235], a[0:3], a[104:107], v[232:235]\n" \
"v_mfma_f32_16x16x32_bf16 v[236:239], a[0:3], a[120:123], v[236:239]\n" \
"ds_read_b64_tr_b16 a[0:1], v36 offset:2048\n" \
"ds_read_b64_tr_b16 a[2:3], v36 offset:3072\n" \
"v_mfma_f32_16x16x32_bf16 v[232:235], a[4:7], a[108:111], v[232:235]\n" \
"v_mfma_f32_16x16x32_bf16 v[236:239], a[4:7], a[124:127], v[236:239]\n" \
"buffer_load_dwordx4 v[164:167], v20, s[56:59], s65 offen offset:0 sc1\n" \
"v_readlane_b32 s65, v11, 52\n" \
"v_mfma_f32_16x16x32_bf16 v[240:243], a[8:11], a[220:223], v[240:243]\n" \
"ds_read_b64_tr_b16 a[4:5], v36 offset:32\n" \
"ds_read_b64_tr_b16 a[6:7], v36 offset:1056\n" \
"v_mfma_f32_16x16x32_bf16 v[244:247], a[8:11], a[236:239], v[244:247]\n" \
"ds_read_b64_tr_b16 a[8:9], v36 offset:2080\n" \
"v_cvt_pk_bf16_f32 v232, v232, v233\n" \
"v_cvt_pk_bf16_f32 v233, v234, v235\n" \
"v_cvt_pk_bf16_f32 v234, v236, v237\n" \
"v_cvt_pk_bf16_f32 v235, v238, v239\n" \
"buffer_store_dwordx4 v[232:235], v20, s[56:59], s65 offen offset:1024 sc1\n" \
"v_mfma_f32_16x16x32_bf16 v[240:243], a[12:15], a[224:227], v[240:243]\n" \
"ds_read_b64_tr_b16 a[10:11], v36 offset:3104\n" \
"v_mfma_f32_16x16x32_bf16 v[244:247], a[12:15], a[240:243], v[244:247]\n" \
"v_mfma_f32_16x16x32_bf16 v[240:243], a[16:19], a[228:231], v[240:243]\n" \
"v_mfma_f32_16x16x32_bf16 v[244:247], a[16:19], a[244:247], v[244:247]\n" \
"v_mfma_f32_16x16x32_bf16 v[240:243], a[20:23], a[232:235], v[240:243]\n" \
"v_mfma_f32_16x16x32_bf16 v[244:247], a[20:23], a[248:251], v[244:247]\n" \
"s_waitcnt lgkmcnt(8)\n" \
"v_mfma_f32_16x16x32_bf16 v[168:171], a[24:27], a[128:131], v[168:171]\n" \
"v_mfma_f32_16x16x32_bf16 v[172:175], a[24:27], a[144:147], v[172:175]\n" \
"v_mfma_f32_16x16x32_bf16 v[240:243], a[24:27], a[160:163], v[240:243]\n" \
"s_waitcnt vmcnt(9)\n" \
"v_cmp_eq_u32_e64 s[98:99], s62, v37\n" \
"buffer_load_dword v37, v40, s[56:59], s74 offen sc1\n" \
"s_mov_b64 exec, 1\n" \
"buffer_store_dword v39, off, s[56:59], s68 offset:44 sc1\n" \
"s_mov_b64 exec, -1\n" \
"s_or_b32 s75, 0x300, s100\n" \
"s_andn2_b32 s75, s75, s98\n" \
"s_mov_b32 s21, s75\n" \
"s_bitcmp1_b32 s21, 16\n" \
"s_cselect_b32 s70, 0x40000000, s16\n" \
"s_bitcmp1_b32 s21, 17\n" \
"s_cselect_b32 s71, 0x40000000, s16\n" \
"s_bitcmp1_b32 s21, 18\n" \
"s_cselect_b32 s72, 0x40000000, s17\n" \
"s_bitcmp1_b32 s21, 19\n" \
"s_cselect_b32 s73, 0x40000000, s17\n" \
"s_bitcmp1_b32 s21, 20\n" \
"s_cselect_b32 s92, 0x40000000, s18\n" \
"s_bitcmp1_b32 s21, 21\n" \
"s_cselect_b32 s93, 0x40000000, s18\n" \
"s_bitcmp1_b32 s21, 22\n" \
"s_cselect_b32 s94, 0x40000000, s19\n" \
"s_bitcmp1_b32 s21, 23\n" \
"s_cselect_b32 s95, 0x40000000, s19\n" \
"s_bitcmp1_b32 s21, 8\n" \
"s_cselect_b32 s96, 0x40000000, s12\n" \
"s_bitcmp1_b32 s21, 9\n" \
"s_cselect_b32 s97, 0x40000000, s12\n" \
"s_add_u32 s64, s64, 0x1000\n" \
"s_add_u32 s65, s65, 0x800\n" \
"v_mfma_f32_16x16x32_bf16 v[244:247], a[24:27], a[176:179], v[244:247]\n" \
"buffer_load_dwordx4 v[112:115], v20, s[56:59], s70 offen offset:0 sc1\n" \
"v_mfma_f32_16x16x32_bf16 v[168:171], a[28:31], a[132:135], v[168:171]\n" \
"v_mfma_f32_16x16x32_bf16 v[172:175], a[28:31], a[148:151], v[172:175]\n" \
"v_mfma_f32_16x16x32_bf16 v[240:243], a[28:31], a[164:167], v[240:243]\n" \
"v_mfma_f32_16x16x32_bf16 v[244:247], a[28:31], a[180:183], v[244:247]\n" \
"buffer_load_dwordx4 v[184:187], v20, s[56:59], s71 offen offset:2048 sc1\n" \
"v_mfma_f32_16x16x32_bf16 v[168:171], a[192:195], a[136:139], v[168:171]\n" \
"v_readlane_b32 s71, v11, 54\n" \
"v_mfma_f32_16x16x32_bf16 v[172:175], a[192:195], a[152:155], v[172:175]\n" \
"v_mfma_f32_16x16x32_bf16 v[240:243], a[192:195], a[168:171], v[240:243]\n" \
"v_mfma_f32_16x16x32_bf16 v[244:247], a[192:195], a[184:187], v[244:247]\n" \
"buffer_load_dwordx4 v[120:123], v20, s[56:59], s72 offen offset:0 sc1\n" \
"v_mfma_f32_16x16x32_bf16 v[168:171], a[196:199], a[140:143], v[168:171]\n" \
"v_readlane_b32 s72, v11, 45\n" \
"v_mfma_f32_16x16x32_bf16 v[172:175], a[196:199], a[156:159], v[172:175]\n" \
"v_mfma_f32_16x16x32_bf16 v[240:243], a[196:199], a[172:175], v[240:243]\n" \
"v_mfma_f32_16x16x32_bf16 v[244:247], a[196:199], a[188:191], v[244:247]\n" \
"buffer_load_dwordx4 v[192:195], v20, s[56:59], s73 offen offset:2048 sc1\n" \
"v_mfma_f32_16x16x32_bf16 v[168:171], a[200:203], a[32:35], v[168:171]\n" \
"v_readlane_b32 s73, v11, 43\n" \
"v_mfma_f32_16x16x32_bf16 v[172:175], a[200:203], a[48:51], v[172:175]\n" \
"v_mfma_f32_16x16x32_bf16 v[240:243], a[200:203], a[96:99], v[240:243]\n" \
"v_mfma_f32_16x16x32_bf16 v[244:247], a[200:203], a[112:115], v[244:247]\n" \
"buffer_load_dwordx4 v[128:131], v20, s[56:59], s92 offen offset:0 sc1\n" \
"v_mfma_f32_16x16x32_bf16 v[168:171], a[204:207], a[36:39], v[168:171]\n" \
"v_readlane_b32 s92, v11, 44\n" \
"v_mfma_f32_16x16x32_bf16 v[172:175], a[204:207], a[52:55], v[172:175]\n" \
"v_mfma_f32_16x16x32_bf16 v[240:243], a[204:207], a[100:103], v[240:243]\n" \
"v_mfma_f32_16x16x32_bf16 v[244:247], a[204:207], a[116:119], v[244:247]\n" \
"buffer_load_dwordx4 v[200:203], v20, s[56:59], s93 offen offset:2048 sc1\n" \
"v_mfma_f32_16x16x32_bf16 v[168:171], a[208:211], a[40:43], v[168:171]\n" \
"v_mfma_f32_16x16x32_bf16 v[172:175], a[208:211], a[56:59], v[172:175]\n" \
"v_mfma_f32_16x16x32_bf16 v[240:243], a[208:211], a[104:107], v[240:243]\n" \
"s_waitcnt vmcnt(16)\n" \
"s_mov_b64 exec, 1\n" \
"buffer_store_dword v39, off, s[56:59], s69 offset:48 sc1\n" \
".fill (L_ab + 45056 - .) / 4, 4, 0xBF800000\n" \
"L_ap11:\n" \
"s_cbranch_execz L_ap0\n" \
"s_mov_b64 exec, -1\n" \
"v_mfma_f32_16x16x32_bf16 v[244:247], a[208:211], a[120:123], v[244:247]\n" \
"v_mfma_f32_16x16x32_bf16 v[168:171], a[212:215], a[44:47], v[168:171]\n" \
"v_mfma_f32_16x16x32_bf16 v[172:175], a[212:215], a[60:63], v[172:175]\n" \
"v_mfma_f32_16x16x32_bf16 v[240:243], a[212:215], a[108:111], v[240:243]\n" \
"buffer_load_dwordx4 v[176:179], v20, s[56:59], s94 offen offset:0 sc1\n" \
"v_mfma_f32_16x16x32_bf16 v[244:247], a[212:215], a[124:127], v[244:247]\n" \
"s_waitcnt lgkmcnt(0)\n" \
"v_mfma_f32_16x16x32_bf16 v[168:171], a[216:219], v[64:67], v[168:171]\n" \
"v_mfma_f32_16x16x32_bf16 v[172:175], a[216:219], v[72:75], v[172:175]\n" \
"v_mfma_f32_16x16x32_bf16 v[240:243], a[216:219], v[80:83], v[240:243]\n" \
"buffer_load_dwordx4 v[248:251], v20, s[56:59], s95 offen offset:2048 sc1\n" \
"v_mfma_f32_16x16x32_bf16 v[244:247], a[216:219], v[88:91], v[244:247]\n" \
"v_mfma_f32_16x16x32_bf16 v[168:171], a[0:3], v[68:71], v[168:171]\n" \
"v_mfma_f32_16x16x32_bf16 v[172:175], a[0:3], v[76:79], v[172:175]\n" \
"v_mfma_f32_16x16x32_bf16 v[240:243], a[0:3], v[84:87], v[240:243]\n" \
"buffer_load_dwordx4 v[188:191], v20, s[56:59], s96 offen offset:1024 sc1\n" \
"v_mfma_f32_16x16x32_bf16 v[244:247], a[0:3], v[92:95], v[244:247]\n" \
"v_mfma_f32_16x16x32_bf16 v[56:59], a[4:7], v[96:99], 0\n" \
"v_mfma_f32_16x16x32_bf16 v[60:63], a[4:7], v[104:107], 0\n" \
"v_mfma_f32_16x16x32_bf16 v[56:59], a[8:11], v[100:103], v[56:59]\n" \
"buffer_load_dwordx4 v[196:199], v20, s[56:59], s97 offen offset:3072 sc1\n" \
"v_mfma_f32_16x16x32_bf16 v[60:63], a[8:11], v[108:111], v[60:63]\n" \
"s_waitcnt vmcnt(16)\n" \
"s_mov_b64 exec, 1\n" \
"buffer_store_dword v39, off, s[56:59], s64 offset:52 sc1\n" \
"s_mov_b64 exec, -1\n" \
"s_waitcnt vmcnt(14)\n" \
"s_mov_b64 exec, 1\n" \
"buffer_store_dword v39, off, s[56:59], s65 offset:56 sc1\n" \
"s_mov_b64 exec, -1\n" \
"s_cmp_lg_u32 s20, 0\n" \
"s_cbranch_scc1 L_sna_%=\n" \
"L_rna_%=:\n" \
"s_cmp_lg_u32 s21, 0\n" \
"s_cbranch_scc1 L_snx_%=\n" \
"L_rnx_%=:\n" \
"s_waitcnt vmcnt(14)\n" \
"v_cmp_eq_u32_e64 s[98:99], s62, v37\n" \
"v_mfma_f32_16x16x32_bf16 v[168:171], v[50:53], v[116:119], v[168:171]\n" \
"s_andn2_b32 s75, 0x400, s98\n" \
"s_cbranch_scc1 L_sx0_%=\n" \
"L_rx0_%=:\n" \
"s_add_u32 s66, s63, 0x1000\n" \
"v_mfma_f32_16x16x32_bf16 v[172:175], v[48:51], v[116:119], v[172:175]\n" \
"buffer_load_dwordx4 v[204:207], v20, s[56:59], s66 offen offset:1024 sc1\n" \
"s_andn2_b32 s75, 0x800, s98\n" \
"s_cbranch_scc1 L_sx1_%=\n" \
"L_rx1_%=:\n" \
"s_add_u32 s67, s63, 0x1800\n" \
"v_mfma_f32_16x16x32_bf16 v[168:171], v[50:53], v[124:127], v[168:171]\n" \
"v_mfma_f32_16x16x32_bf16 v[172:175], v[48:51], v[124:127], v[172:175]\n" \
"v_mfma_f32_16x16x32_bf16 v[168:171], v[50:53], v[132:135], v[168:171]\n" \
"buffer_load_dwordx4 v[212:215], v20, s[56:59], s67 offen offset:1024 sc1\n" \
"s_andn2_b32 s75, 0x1000, s98\n" \
"s_cbranch_scc1 L_sx2_%=\n" \
"L_rx2_%=:\n" \
"s_add_u32 s68, s63, 0x2000\n" \
"v_mfma_f32_16x16x32_bf16 v[172:175], v[48:51], v[132:135], v[172:175]\n" \
"v_mfma_f32_16x16x32_bf16 v[168:171], v[50:53], v[140:143], v[168:171]\n" \
"v_mfma_f32_16x16x32_bf16 v[172:175], v[48:51], v[140:143], v[172:175]\n" \
"buffer_load_dwordx4 v[220:223], v20, s[56:59], s68 offen offset:1024 sc1\n" \
"s_andn2_b32 s75, 0x2000, s98\n" \
"s_cbranch_scc1 L_sx3_%=\n" \
"L_rx3_%=:\n" \
"s_add_u32 s69, s63, 0x2800\n" \
"v_mfma_f32_16x16x32_bf16 v[168:171], v[50:53], v[148:151], v[168:171]\n" \
"v_mfma_f32_16x16x32_bf16 v[172:175], v[48:51], v[148:151], v[172:175]\n" \
"v_mfma_f32_16x16x32_bf16 v[168:171], v[50:53], v[156:159], v[168:171]\n" \
"buffer_load_dwordx4 v[228:231], v20, s[56:59], s69 offen offset:1024 sc1\n" \
"s_andn2_b32 s75, 0x4000, s98\n" \
"s_cbranch_scc1 L_sx4_%=\n" \
"L_rx4_%=:\n" \
"s_add_u32 s70, s63, 0x3000\n" \
"v_mfma_f32_16x16x32_bf16 v[172:175], v[48:51], v[156:159], v[172:175]\n" \
"v_mfma_f32_16x16x32_bf16 v[168:171], v[50:53], v[164:167], v[168:171]\n" \
"v_mfma_f32_16x16x32_bf16 v[172:175], v[48:51], v[164:167], v[172:175]\n" \
"buffer_load_dwordx4 v[236:239], v20, s[56:59], s70 offen offset:1024 sc1\n" \
"s_waitcnt vmcnt(17)\n" \
"v_mfma_f32_16x16x32_bf16 v[56:59], v[50:53], v[112:115], v[56:59]\n" \
"v_mfma_f32_16x16x32_bf16 v[60:63], v[48:51], v[112:115], v[60:63]\n" \
"s_waitcnt vmcnt(16)\n" \
"v_mfma_f32_16x16x32_bf16 v[56:59], v[50:53], v[184:187], v[56:59]\n" \
"v_mfma_f32_16x16x32_bf16 v[60:63], v[48:51], v[184:187], v[60:63]\n" \
"v_cvt_pk_bf16_f32 v168, v168, v169\n" \
"v_cvt_pk_bf16_f32 v172, v172, v173\n" \
"v_cvt_pk_bf16_f32 v169, v170, v171\n" \
"v_cvt_pk_bf16_f32 v173, v174, v175\n" \
"buffer_store_dwordx2 v[168:169], v42, s[88:91], s73 offen sc1\n" \
"buffer_store_dwordx2 v[172:173], v43, s[88:91], s73 offen sc1\n" \
"s_waitcnt vmcnt(17)\n" \
"v_mfma_f32_16x16x32_bf16 v[56:59], v[50:53], v[120:123], v[56:59]\n" \
"v_mfma_f32_16x16x32_bf16 v[60:63], v[48:51], v[120:123], v[60:63]\n" \
"s_waitcnt vmcnt(16)\n" \
"v_mfma_f32_16x16x32_bf16 v[56:59], v[50:53], v[192:195], v[56:59]\n" \
"v_mfma_f32_16x16x32_bf16 v[60:63], v[48:51], v[192:195], v[60:63]\n" \
"s_waitcnt vmcnt(15)\n" \
"v_mfma_f32_16x16x32_bf16 v[56:59], v[50:53], v[128:131], v[56:59]\n" \
"v_mfma_f32_16x16x32_bf16 v[60:63], v[48:51], v[128:131], v[60:63]\n" \
"s_waitcnt vmcnt(14)\n" \
"v_mfma_f32_16x16x32_bf16 v[56:59], v[50:53], v[200:203], v[56:59]\n" \
"v_mfma_f32_16x16x32_bf16 v[60:63], v[48:51], v[200:203], v[60:63]\n" \
"s_waitcnt vmcnt(12)\n" \
"v_mfma_f32_16x16x32_bf16 v[56:59], v[50:53], v[176:179], v[56:59]\n" \
"v_mfma_f32_16x16x32_bf16 v[60:63], v[48:51], v[176:179], v[60:63]\n" \
"s_waitcnt vmcnt(11)\n" \
"v_mfma_f32_16x16x32_bf16 v[56:59], v[50:53], v[248:251], v[56:59]\n" \
"v_mfma_f32_16x16x32_bf16 v[60:63], v[48:51], v[248:251], v[60:63]\n" \
"s_waitcnt vmcnt(10)\n" \
"v_mfma_f32_16x16x32_bf16 v[240:243], v[50:53], v[188:191], v[240:243]\n" \
"v_mfma_f32_16x16x32_bf16 v[244:247], v[48:51], v[188:191], v[244:247]\n" \
"s_waitcnt vmcnt(9)\n" \
"v_mfma_f32_16x16x32_bf16 v[240:243], v[50:53], v[196:199], v[240:243]\n" \
"v_mfma_f32_16x16x32_bf16 v[244:247], v[48:51], v[196:199], v[244:247]\n" \
"s_mov_b64 exec, 1\n" \
"buffer_store_dword v46, off, s[56:59], s71 offset:0 sc1\n" \
"s_mov_b64 exec, -1\n" \
"v_cvt_pk_bf16_f32 v56, v56, v57\n" \
"v_cvt_pk_bf16_f32 v60, v60, v61\n" \
"v_cvt_pk_bf16_f32 v57, v58, v59\n" \
"v_cvt_pk_bf16_f32 v61, v62, v63\n" \
"buffer_store_dwordx2 v[56:57], v42, s[88:91], s72 offen sc1\n" \
"buffer_store_dwordx2 v[60:61], v43, s[88:91], s72 offen sc1\n" \
"s_waitcnt vmcnt(9)\n" \
"v_mfma_f32_16x16x32_bf16 v[240:243], v[50:53], v[204:207], v[240:243]\n" \
"v_mfma_f32_16x16x32_bf16 v[244:247], v[48:51], v[204:207], v[244:247]\n" \
"s_waitcnt vmcnt(8)\n" \
"v_mfma_f32_16x16x32_bf16 v[240:243], v[50:53], v[212:215], v[240:243]\n" \
"v_mfma_f32_16x16x32_bf16 v[244:247], v[48:51], v[212:215], v[244:247]\n" \
"s_waitcnt vmcnt(7)\n" \
"v_mfma_f32_16x16x32_bf16 v[240:243], v[50:53], v[220:223], v[240:243]\n" \
"v_mfma_f32_16x16x32_bf16 v[244:247], v[48:51], v[220:223], v[244:247]\n" \
"s_waitcnt vmcnt(6)\n" \
"v_mfma_f32_16x16x32_bf16 v[240:243], v[50:53], v[228:231], v[240:243]\n" \
"v_mfma_f32_16x16x32_bf16 v[244:247], v[48:51], v[228:231], v[244:247]\n" \
"s_waitcnt vmcnt(5)\n" \
"v_mfma_f32_16x16x32_bf16 v[240:243], v[50:53], v[236:239], v[240:243]\n" \
"v_mfma_f32_16x16x32_bf16 v[244:247], v[48:51], v[236:239], v[244:247]\n" \
"s_nop 6\n" \
"v_cvt_pk_bf16_f32 v240, v240, v241\n" \
"v_cvt_pk_bf16_f32 v244, v244, v245\n" \
"v_cvt_pk_bf16_f32 v241, v242, v243\n" \
"v_cvt_pk_bf16_f32 v245, v246, v247\n" \
"buffer_store_dwordx2 v[240:241], v42, s[88:91], s92 offen sc1\n" \
"buffer_store_dwordx2 v[244:245], v43, s[88:91], s92 offen sc1\n"
#define ATT_VCLOB "v21","v22","v23","v24","v25","v26","v27","v28","v29","v32","v33","v34","v35","v36","v37","v38","v39","v40","v41","v42","v43","v44","v45","v46","v47","v48","v49","v50","v51","v52","v53","v54","v55","v56","v57","v58","v59","v60","v61","v62","v63","v64","v65","v66","v67","v68","v69","v70","v71","v72","v73","v74","v75","v76","v77","v78","v79","v80","v81","v82","v83","v84","v85","v86","v87","v88","v89","v90","v91","v92","v93","v94","v95","v96","v97","v98","v99","v100","v101","v102","v103","v104","v105","v106","v107","v108","v109","v110","v111","v112","v113","v114","v115","v116","v117","v118","v119","v120","v121","v122","v123","v124","v125","v126","v127","v128","v129","v130","v131","v132","v133","v134","v135","v136","v137","v138","v139","v140","v141","v142","v143","v144","v145","v146","v147","v148","v149","v150","v151","v152","v153","v154","v155","v156","v157","v158","v159","v160","v161","v162","v163","v164","v165","v166","v167","v168","v169","v170","v171","v172","v173","v174","v175","v176","v177","v178","v179","v180","v181","v182","v183","v184","v185","v186","v187","v188","v189","v190","v191","v192","v193","v194","v195","v196","v197","v198","v199","v200","v201","v202","v203","v204","v205","v206","v207","v208","v209","v210","v211","v212","v213","v214","v215","v216","v217","v218","v219","v220","v221","v222","v223","v224","v225","v226","v227","v228","v229","v230","v231","v232","v233","v234","v235","v236","v237","v238","v239","v240","v241","v242","v243","v244","v245","v246","v247","v248","v249","v250","v251","v252","v253","v254","v255"
#define ATT_SCLOB "s63","s64","s65","s66","s67","s68","s69","s70","s71","s72","s73","s74","s75","s76","s77","s78","s79","s80","s81","s82","s83","s84","s85","s86","s87","s88","s89","s90","s91","s92","s93","s95","s96","s97","s98","s99","s100","s101"
#define ATT_WTCH 0
#define ATT_PEEL 1
#define ATT_PEEL_FOW 0
enum { T_fo, T_fj, T_fa, T_fas, T_fb, T_fbs, T_f8, T_mk8, T_wt, T_wol, T_woh, T_wbl, T_wbh, T_outl, T_outh, T_r1l, T_r1h, T_r1n, T_r1s, T_m0, T_m1, T_m1b, T_mw, T_m3, T_m4, T_ml, T_w0, T_w1, T_w2, T_w3, T_A1, T_A2, T_A3, T_B1, T_B2, T_B3, T_Ao, T_Bo, T_Al, T_Bl, T_l8, T_xr, T_x8r, T_oA, T_oB, T_o8, T_x0, T_x1, T_x2, T_x3, T_x4, T_x5, T_x6, T_x8, T_ep };

__device__ __forceinline__ void out_consts(u32x4 (&vp)[3], const u16* wo, const u16* wob, const u16* out, const unsigned b, const unsigned w, const unsigned lane) {
    const unsigned sp = b & 7u, nt = (b >> 3) & 3u, j = b >> 5, head = 4u * sp + nt, OOR = 0x40000000u;
    unsigned vw[9];
#pragma unroll
    for (unsigned i = 0; i < 9u; ++i) {
        const unsigned q = 64u * i + lane, row = __umul24(q, 3641u) >> 16, c = q - __umul24(row, 18u);
        vw[i] = __umul24(row, DM * 2) + (c < 16u ? (c * 16u + sp * 32u + 32u) & 255u : c * 16u) - (i & 3u) * 1024u;
    }
    const unsigned row = 16u * w + (lane >> 2), r9 = __umul24(16u * (row >> 5) + (row & 12u) + 2u * ((row >> 4) & 1u) + ((row >> 1) & 1u), 18u) + 16u + (row & 1u);
    unsigned vwl = __umul24(r9, DM * 2) + ((lane & 2u) ? 256u : sp * 32u) + (lane & 1u) * 16u, vrd = __umul24(lane >> 2, 288u) + (lane & 3u) * 8u;
    unsigned tab = 0;
#define TB(n, v) asm("v_writelane_b32 %0, %1, %2" : "+v"(tab) : "s"((unsigned)(v)), "n"((int)T_##n))
    const unsigned FJ = (unsigned)WS_F + (F_J + 640u * j) * 4u, mt = 2u * (w & 2u) + (w & 1u), w2 = w < 2u ? w : 2u, hw = head * 147456u;
    const unsigned FR = (unsigned)WS_F + F_R * 4u + (8u * j + sp) * 1024u;
    TB(fo, FJ + (ATT_PEEL_FOW ? 16u * head + 4u * w : 4u * head)); TB(fj, FJ); TB(fa, FR + (w | 2u) * 256u); TB(fas, FR + w * 256u + nt * 4u);
    TB(fb, FR + 128u + w * 256u); TB(fbs, FR + 128u + w * 256u + nt * 4u);
    TB(ep, ssel(b + w, OOR, (unsigned)WS_F + F_EPOCH * 4u));
    const u16* pwo = wo + nt * 144u; const u16* pwb = wob + nt * 144u;
    TB(wol, PLO(pwo)); TB(woh, PHI(pwo) & 0xffffu); TB(wbl, PLO(pwb)); TB(wbh, PHI(pwb) & 0xffffu); TB(outl, PLO(out)); TB(outh, PHI(out) & 0xffffu); TB(r1l, sslt<3>(w, PLO(pwo), PLO(pwb))); TB(r1h, sslt<3>(w, PHI(pwo), PHI(pwb)) & 0xffffu);
    TB(r1n, sslt<3>(w, 72u * sp + 71u, 4095u) * 1152u + 288u); TB(r1s, sslt<3>(w, (72u * sp + 32u * w) * 1152u, hw + 110592u));
    TB(m0, 131072u + 9216u * w2); TB(w0, hw + 36864u * w2); TB(m1, ATT_PEEL ? sslt<3>(w, 41984u + 9216u * w, 32768u) : 24576u + 9216u * w); TB(m1b, ssel(w ^ 2u, sslt<3>(w, 41984u + 9216u * w, 32768u), 114688u - 4096u)); TB(mw, 65536u + 9216u * w); TB(m3, sslt<3>(w, 102400u + 9216u * w, 0u)); TB(m4, sslt<3>(w, 131072u + 9216u * w, 9216u)); TB(ml, (ATT_PEEL ? 18432u : 61440u) + 1024u * w);
    const unsigned tA = (unsigned)WS_AF + (8u * j + mt) * (KS_TOT * 1024u), tB = (unsigned)WS_AF + (120u - 8u * j + mt) * (KS_TOT * 1024u);
#define HK(k) (4u * sp + ((nt + k) & 3u))
    TB(w1, HK(1u) * 147456u + w * 36864u); TB(w2, HK(2u) * 147456u + w * 36864u); TB(w3, HK(3u) * 147456u + w * 36864u);
    TB(A1, tA + 18432u + HK(1u) * 4096u); TB(A2, tA + 18432u + HK(2u) * 4096u); TB(A3, tA + 18432u + HK(3u) * 4096u);
    TB(B1, tB + 18432u + HK(1u) * 4096u); TB(B2, tB + 18432u + HK(2u) * 4096u); TB(B3, tB + 18432u + HK(3u) * 4096u);
    TB(Ao, tA + sp * 2048u); TB(Bo, tB + sp * 2048u); TB(Al, tA + 16384u); TB(Bl, tB + 16384u);
    const unsigned nred = w ^ (sp >> 1), blk = sp & 1u;
    const unsigned xg = (unsigned)WS_X + (4u * j + nt) * 589824u + w * 16384u, oA = (128u * j + 16u * mt) * 1152u + nt * 288u, oB = (1920u - 128u * j + 16u * mt) * 1152u + nt * 288u;
    TB(l8, ssel(nred, OOR, ssel(blk, tB, tA) + 16384u)); TB(xr, xg + sp * 65536u); TB(x8r, ssel(nred, OOR, xg + 524288u + (sp & 1u) * 1024u));
    TB(oA, oA + sp * 32u); TB(oB, oB + sp * 32u); TB(o8, ssel(nred, OOR, ssel(blk, oB, oA) + 256u)); TB(x8, xg + 524288u + sp * 2048u);
    TB(f8, xg + w * 131072u + 14400u + sp * 4u); TB(mk8, ssel(nred, 0u, 0xff0000u)); TB(wt, hw + j * 18432u + w * 4608u);
#define XI(i) TB(x##i, xg + ((sp + 1u + i) & 7u) * 65536u + i * 2048u)
    XI(0); XI(1); XI(2); XI(3); XI(4); XI(5); XI(6);
#undef XI
#undef HK
#undef TB
    vp[0] = (u32x4){vw[0], vw[1], vw[2], vw[3]}; vp[1] = (u32x4){vw[4], vw[5], vw[6], vw[7]}; vp[2] = (u32x4){vw[8], vwl, vrd, tab};
}
__global__ __launch_bounds__(256, 1) __attribute__((aligned(4096))) void fused_kernel(const u16* __restrict__ x, const u16* __restrict__ ckv, const u16* __restrict__ wq, const u16* __restrict__ wuk,
                                                       const u16* __restrict__ wuv, const u16* __restrict__ wqkv, const u16* __restrict__ gw,
                                                       const u16* __restrict__ wo, const u16* __restrict__ wob, char* __restrict__ ws_, u16* __restrict__ out) {
    using namespace att;
    extern __shared__ __attribute__((aligned(16))) char lds[];
    u32x4 sa[5]; u32x2 s24, s28; char* ws; unsigned tdum; kaptr ka = (kaptr)__builtin_amdgcn_kernarg_segment_ptr();
    asm volatile("s_load_dwordx8 s[8:15], %[ka], 0x0\n\ts_load_dwordx8 s[16:23], %[ka], 0x20\n\ts_getpc_b64 s[4:5]\n\tv_lshlrev_b32_e32 v191, 7, %[t]\n\ts_bfe_u32 s6, %[b], 0x20003\n\t"
                 "v_lshl_add_u32 v191, s6, 15, v191\n\tv_min_u32_e32 v191, %[e], v191\n\tglobal_load_dword v191, v191, s[4:5]\n\ts_load_dwordx4 s[24:27], %[ka], 0x40\n\ts_load_dwordx2 s[28:29], %[ka], 0x50"
                 : "={s[8:11]}"(sa[0]), "={s[12:15]}"(sa[1]), "={s[16:19]}"(sa[2]), "={s[20:23]}"(sa[3]), "={s[24:25]}"(s24), "={s[26:27]}"(ws), "={s[28:29]}"(s28), "={v191}"(tdum), [ka] "+{s[0:1]}"(ka)
                 : [t] "v"(threadIdx.x), [b] "s"(blockIdx.x), [e] "i"(CT_END) : "s4", "s5", "s6", "scc");
    SBAR();
    const unsigned wid = rfl(threadIdx.x >> 6);
    unsigned tag, vl;
    const int b = blockIdx.x;
    u32x4 vp[5], srdA; u32x2 sd;
    proj_phase(ka, ws, wid, vp, vl, sa, s24, s28, tdum, srdA, sd, tag
    );
    unsigned* const flags = (unsigned*)(ws + WS_F);
    RTS(2);
#define AST(L) do {} while (0)
#define ATT_TAIL "s_endpgm\n"
    unsigned abad;
    asm volatile(ATT_ASM ATT_TAIL "L_end_%=:\n s_nop 1"
                 : "={s94}"(abad)
                 : "{v[0:3]}"(vp[0]), "{v[4:7]}"(vp[1]), "{v[8:11]}"(vp[2]), "{v[12:15]}"(vp[3]), "{v[16:19]}"(vp[4]), "{v20}"(vl), "{s[56:59]}"(srdA), "{s[60:61]}"(sd), "{s62}"(tag),
                   "{s[8:11]}"(sa[0]), "{s[12:15]}"(sa[1]), "{s[16:19]}"(sa[2]), "{s[20:23]}"(sa[3]), "{s24}"(sa[4][0]), "{s3}"(wid), "{s[28:29]}"(s28)
                 : "memory", "m0", "vcc", "scc", ATT_VCLOB, ATT_SCLOB, ACLOB_LO, ACLOB_HI);
    {
        unsigned b2 = b, w2 = wid;
        asm volatile("s_waitcnt vmcnt(0)" : "+s"(ka), "+s"(b2), "+s"(w2), "+s"(ws) :: "memory");
        const unsigned j2 = b2 >> 5, p2 = w2 & 1u, h2 = (b2 & 7u) * 4u + ((b2 >> 3) & 3u);
#pragma unroll 1
        for (unsigned i = w2 & 2u; i < ((w2 & 2u) ? (abad ? 8u : 4u) : 2u); ++i)
            att_exact(ka[0], ka[2], ws, h2, (i < 4u ? 1920u - 128u * j2 : 128u * j2 - 128u) + 32u * i + 16u * p2, *reinterpret_cast<const float*>(lds + LDS_G + (32u * i + 16u * p2 + (lane_id() & 15u)) * 4u));
        asm volatile("s_waitcnt vmcnt(0)" ::: "memory");
        unsigned* fa = (unsigned*)(ws + WS_F) + F_R + (8u * j2 + (b2 & 7u)) * 256u + (2u + p2) * 64u + ((b2 >> 3) & 3u);
        if (w2 & 2u) st_flag(fa, tag);
        wait_flags(fa, ld_flag(fa), tag);
        const unsigned l2 = lane_id(); u32x4 c[3]; out_consts(c, ka[7], ka[8], ka[10], b2, w2, l2);
        asm volatile("s_waitcnt vmcnt(0) lgkmcnt(0)\n\ts_nop 7\n\ts_branch L_att_stub" :: "{v[0:3]}"(c[0]), "{v[4:7]}"(c[1]), "{v[8:11]}"(c[2]), "{v20}"(l2 * 16u), "{s[56:59]}"(SRDA), "{s60}"(SD(8u * j2, h2, w2)),
                     "{s61}"(SD(120u - 8u * j2, h2, w2)), "{s62}"(tag) : "memory");
    }
    RTS(5);
}

extern "C" void kernel_launch(void* const* d_in, const int* in_sizes, int n_in, void* d_out, int out_size, void* d_ws, size_t ws_size,
                              hipStream_t stream) {
    (void)hipFuncSetAttribute((const void*)fused_kernel, hipFuncAttributeMaxDynamicSharedMemorySize, att::LDS_BYTES);
    fused_kernel<<<dim3(256), 256, att::LDS_BYTES, stream>>>((const u16*)d_in[0], (const u16*)d_in[4], (const u16*)d_in[5], (const u16*)d_in[6], (const u16*)d_in[7], (const u16*)d_in[1],
                                                             (const u16*)d_in[9], (const u16*)d_in[2], (const u16*)d_in[8], (char*)d_ws, (u16*)d_out);
}
```

```cpp
#include <hip/hip_runtime.h>
#include <hip/hip_bf16.h>
#include <stdint.h>
#include <utility>

typedef short bf16x8 __attribute__((ext_vector_type(8)));
typedef short s16x4 __attribute__((ext_vector_type(4)));
typedef float f32x16 __attribute__((ext_vector_type(16)));
typedef float f32x4 __attribute__((ext_vector_type(4)));
typedef unsigned u32x4 __attribute__((ext_vector_type(4)));
typedef unsigned u32x2 __attribute__((ext_vector_type(2)));
typedef unsigned short u16;

constexpr int S = 2048, DM = 576, NH = 32, HD = 128, DC = 512, NQKV = 1728, DHA = 18;
constexpr int NBIG = NH * HD;
constexpr int KS_TOT = 146;

#define SBAR() __builtin_amdgcn_sched_barrier(0)
constexpr size_t WS_Q = 0, WS_K = WS_Q + (size_t)S * NBIG * 2, WS_V = WS_K + (size_t)S * NBIG * 2, WS_AF = WS_V + (size_t)S * NBIG * 2, WS_X = WS_AF + (size_t)128 * KS_TOT * 1024,
                 WS_P = WS_X + (size_t)64 * 16 * 9216, WS_F = WS_P + (size_t)128 * (4 * 32 * 256 + 1024) * 4;
constexpr size_t WS_L = (size_t)96 << 20;
static_assert(WS_F == 96206848, "workspace map is baked into the generated asm");
constexpr int F_EPOCH = 0, F_J = 1024, F_ST = 8192, F_KV = 40960, F_R = 65536;
constexpr unsigned TAG_STEP = 0x9E3779B1u;
__device__ __forceinline__ unsigned ld_flag(const unsigned* p) { return __hip_atomic_load(p, __ATOMIC_RELAXED, __HIP_MEMORY_SCOPE_AGENT); }
__device__ __forceinline__ void st_flag(unsigned* p, unsigned v) { __hip_atomic_store(p, v, __ATOMIC_RELAXED, __HIP_MEMORY_SCOPE_AGENT); }
__device__ __forceinline__ unsigned ld_flag_async(const unsigned* p) { unsigned v; asm volatile("global_load_dword %0, %1, off sc1" : "=v"(v) : "v"(p) : "memory"); return v; }
#define WAITV(v, N) asm volatile("s_waitcnt vmcnt(" #N ")" : "+v"(v) :: "memory")
__device__ __forceinline__ void wait_flags(const unsigned* p, unsigned v, unsigned tag) {
    if (__builtin_expect(__builtin_amdgcn_ballot_w64(v != tag) != 0ull, 0)) {
        unsigned spins = 0;
        do { __builtin_amdgcn_s_sleep(1); } while (__builtin_amdgcn_ballot_w64(ld_flag(p) != tag) != 0ull && ++spins < (1u << 22));
    }
    asm volatile("" ::: "memory");
}

__device__ __forceinline__ unsigned cvtpk(float lo, float hi) {
    unsigned r; asm volatile("v_cvt_pk_bf16_f32 %0, %1, %2" : "=v"(r) : "v"(lo), "v"(hi)); return r;
}
__device__ __forceinline__ float bf2f(u16 v) { return __uint_as_float(((unsigned)v) << 16); }
__device__ __forceinline__ int crow(int r, int hi) { return (r & 3) + 8 * (r >> 2) + 4 * hi; }

typedef __attribute__((address_space(3))) s16x4* lds_s16x4_p;
__device__ __forceinline__ s16x4 trrd(unsigned addr) { return __builtin_amdgcn_ds_read_tr16_b64_v4i16((lds_s16x4_p)addr); }

__device__ __forceinline__ unsigned lane_id() { unsigned l; asm volatile("v_mbcnt_lo_u32_b32 %0, -1, 0\n\tv_mbcnt_hi_u32_b32 %0, -1, %0" : "=v"(l)); return l; }
#define RTS(slot) do {} while (0)
#define CT_END 0x1f300
#define ACLOB_LO "a0","a1","a2","a3","a4","a5","a6","a7","a8","a9","a10","a11","a12","a13","a14","a15","a16","a17","a18","a19","a20","a21","a22","a23","a24","a25","a26","a27","a28","a29","a30","a31","a32","a33","a34","a35","a36","a37","a38","a39","a40","a41","a42","a43","a44","a45","a46","a47","a48","a49","a50","a51","a52","a53","a54","a55","a56","a57","a58","a59","a60","a61","a62","a63","a64","a65","a66","a67","a68","a69","a70","a71","a72","a73","a74","a75","a76","a77","a78","a79","a80","a81","a82","a83","a84","a85","a86","a87","a88","a89","a90","a91","a92","a93","a94","a95","a96","a97","a98","a99","a100","a101","a102","a103","a104","a105","a106","a107","a108","a109","a110","a111","a112","a113","a114","a115","a116","a117","a118","a119","a120","a121","a122","a123","a124","a125","a126","a127"
#define ACLOB_HI "a128","a129","a130","a131","a132","a133","a134","a135","a136","a137","a138","a139","a140","a141","a142","a143","a144","a145","a146","a147","a148","a149","a150","a151","a152","a153","a154","a155","a156","a157","a158","a159","a160","a161","a162","a163","a164","a165","a166","a167","a168","a169","a170","a171","a172","a173","a174","a175","a176","a177","a178","a179","a180","a181","a182","a183","a184","a185","a186","a187","a188","a189","a190","a191","a192","a193","a194","a195","a196","a197","a198","a199","a200","a201","a202","a203","a204","a205","a206","a207","a208","a209","a210","a211","a212","a213","a214","a215","a216","a217","a218","a219","a220","a221","a222","a223","a224","a225","a226","a227","a228","a229","a230","a231","a232","a233","a234","a235","a236","a237","a238","a239","a240","a241","a242","a243","a244","a245","a246","a247","a248","a249","a250","a251","a252","a253","a254","a255"
#define PLO(p) ((unsigned)(size_t)(p))
#define PHI(p) ((unsigned)((size_t)(p) >> 32))
__device__ __forceinline__ unsigned vsel(bool c, unsigned a, unsigned b) { asm("" : "+v"(a), "+v"(b)); return c ? a : b; }
__device__ __forceinline__ unsigned ssel(unsigned c, unsigned a, unsigned b) { unsigned r; asm("s_cmp_lg_u32 %1, 0\n\ts_cselect_b32 %0, %2, %3" : "=s"(r) : "s"(c), "s"(a), "s"(b) : "scc"); return r; }
template <int N> __device__ __forceinline__ unsigned sslt(unsigned c, unsigned a, unsigned b) { unsigned r; asm("s_cmpk_lt_u32 %1, %4\n\ts_cselect_b32 %0, %2, %3" : "=s"(r) : "s"(c), "s"(a), "s"(b), "n"(N) : "scc"); return r; }
namespace att {
constexpr int LDS_BYTES = 163840;
constexpr unsigned LDS_G = 159744u, LDS_L = 161792u;
}
#define SD(J8, H, W) ((unsigned)WS_AF + (((J8) + 2u * ((W) & 2u) + ((W) & 1u)) * KS_TOT + 18u + 4u * (H)) * 1024u)
#define SRDA (u32x4){PLO(ws), PHI(ws) & 0xffffu, (unsigned)WS_L, 0x00020000u}
#define PROJ_ASM \
".p2align 3\n" \
"L_pb:\n" \
"s_waitcnt lgkmcnt(0)\n" \
"s_mov_b32 s64, s10\n" \
"s_and_b32 s65, s11, 0xffff\n" \
"s_mov_b32 s66, 0x200000\n" \
"s_mov_b32 s67, 0x20000\n" \
"s_mov_b32 s68, s14\n" \
"s_and_b32 s69, s15, 0xffff\n" \
"s_mov_b32 s70, 0x400000\n" \
"s_mov_b32 s71, 0x20000\n" \
"s_mov_b32 s72, s16\n" \
"s_and_b32 s73, s17, 0xffff\n" \
"s_mov_b32 s74, 0x400000\n" \
"s_mov_b32 s75, 0x20000\n" \
"s_add_u32 m0, s30, 0x0\n" \
"s_movk_i32 s84, 0x0\n" \
"buffer_load_dwordx4 v22, s[64:67], s84 offen offset:0 lds\n" \
"s_movk_i32 s84, 0x1c00\n" \
"buffer_load_dwordx4 v23, s[64:67], s84 offen offset:1024 lds\n" \
"s_movk_i32 s84, 0x3800\n" \
"buffer_load_dwordx4 v22, s[64:67], s84 offen offset:2048 lds\n" \
"s_movk_i32 s84, 0x5400\n" \
"buffer_load_dwordx4 v23, s[64:67], s84 offen offset:3072 lds\n" \
"s_add_u32 m0, s31, 0x0\n" \
"s_movk_i32 s84, 0x0\n" \
"buffer_load_dwordx4 v26, s[68:71], s84 offen offset:0 lds\n" \
"s_movk_i32 s84, 0x7c00\n" \
"buffer_load_dwordx4 v26, s[68:71], s84 offen offset:1024 lds\n" \
"s_addk_i32 s84, 0x7c00\n" \
"buffer_load_dwordx4 v27, s[68:71], s84 offen offset:2048 lds\n" \
"s_addk_i32 s84, 0x7c00\n" \
"buffer_load_dwordx4 v27, s[68:71], s84 offen offset:3072 lds\n" \
"s_movk_i32 s7, 0x300\n" \
"s_lshr_b32 s4, s2, 1\n" \
"s_and_b32 s4, s4, 60\n" \
"s_or_b32 s4, s4, s3\n" \
"v_lshrrev_b32_e32 v212, 4, v20\n" \
"v_min_u32_e32 v212, 59, v212\n" \
"s_cmpk_lt_u32 s2, 0x80\n" \
"s_cselect_b32 s6, s14, s16\n" \
"v_subrev_u32_e32 v214, 48, v212\n" \
"v_mov_b32_e32 v212, s10\n" \
"v_mov_b32_e32 v215, s6\n" \
"s_cmpk_lt_u32 s2, 0x80\n" \
"s_cselect_b32 s5, s15, s17\n" \
"v_cmp_gt_u32_e32 vcc, s7, v20\n" \
"v_mov_b32_e64 v213, s11\n" \
"v_bfe_u32 v216, v20, 4, 3\n" \
"v_cndmask_b32_e32 v212, v212, v215, vcc\n" \
"v_mov_b32_e32 v215, s5\n" \
"s_lshl_b32 s5, s3, 4\n" \
"v_cndmask_b32_e32 v213, v213, v215, vcc\n" \
"v_lshlrev_b32_e64 v215, 5, v20\n" \
"v_and_b32_e32 v215, 0x7000, v215\n" \
"v_lshl_add_u32 v215, s4, 6, v215\n" \
"s_lshl_b32 s4, s2, 3\n" \
"s_and_b32 s4, s4, 56\n" \
"v_or3_b32 v215, v215, s4, v216\n" \
"s_lshl_b32 s4, s2, 6\n" \
"s_or_b32 s6, s5, s4\n" \
"v_cmp_gt_u32_e64 s[4:5], 6, v214\n" \
"v_add_u32_e32 v215, 0x2000, v215\n" \
"s_nop 0\n" \
"v_cndmask_b32_e64 v216, 4, 2, s[4:5]\n" \
"v_add3_u32 v214, s6, v214, v216\n" \
"s_lshl_b32 s4, s3, 9\n" \
"v_cndmask_b32_e32 v214, v214, v215, vcc\n" \
"v_lshlrev_b32_e32 v214, 7, v214\n" \
"v_mov_b32_e64 v215, 0\n" \
"v_lshl_add_u64 v[184:185], v[212:213], 0, v[214:215]\n" \
"s_or_b32 s32, s4, 0x25000\n" \
"s_add_u32 m0, s30, 0x1000\n" \
"s_mov_b32 s84, 0x8000\n" \
"buffer_load_dwordx4 v22, s[64:67], s84 offen offset:0 lds\n" \
"s_and_b32 s33, s3, 1\n" \
"v_lshrrev_b32_e32 v212, 4, v20\n" \
"v_lshrrev_b32_e64 v214, 8, v20\n" \
"v_bfe_u32 v212, v212, 1, 3\n" \
"v_lshlrev_b32_e32 v215, 2, v20\n" \
"s_addk_i32 s84, 0x1c00\n" \
"buffer_load_dwordx4 v23, s[64:67], s84 offen offset:1024 lds\n" \
"v_lshrrev_b32_e64 v216, 1, v20\n" \
"v_bfe_u32 v213, v20, 4, 4\n" \
"s_lshl_b32 s4, s3, 5\n" \
"v_xor_b32_e32 v212, v212, v214\n" \
"v_lshlrev_b32_e32 v214, 11, v214\n" \
"s_addk_i32 s84, 0x1c00\n" \
"buffer_load_dwordx4 v22, s[64:67], s84 offen offset:2048 lds\n" \
"v_and_b32_e32 v215, 0x300, v215\n" \
"v_and_b32_e64 v217, 24, v216\n" \
"v_and_or_b32 v213, s4, 64, v213\n" \
"v_or3_b32 v214, v215, v214, v217\n" \
"s_lshl_b32 s4, s33, 14\n" \
"s_addk_i32 s84, 0x1c00\n" \
"buffer_load_dwordx4 v23, s[64:67], s84 offen offset:3072 lds\n" \
"v_and_b32_e32 v215, 0xe0, v216\n" \
"v_lshlrev_b32_e32 v213, 7, v213\n" \
"v_lshlrev_b32_e32 v212, 4, v212\n" \
"v_or3_b32 v214, v214, s4, v215\n" \
"v_or_b32_e64 v28, v212, v213\n" \
"s_add_u32 m0, s31, 0x4000\n" \
"s_movk_i32 s84, 0x0\n" \
"buffer_load_dwordx4 v26, s[72:75], s84 offen offset:0 lds\n" \
"v_add_u32_e32 v32, 0x10000, v214\n" \
"v_bitop3_b32 v29, v212, 64, v213 bitop3:0x36\n" \
"v_xor_b32_e32 v33, 0x20, v32\n" \
"v_xor_b32_e64 v34, 0x40, v32\n" \
"v_xor_b32_e32 v35, 0x60, v32\n" \
"s_movk_i32 s84, 0x7c00\n" \
"buffer_load_dwordx4 v26, s[72:75], s84 offen offset:1024 lds\n" \
"v_xor_b32_e32 v36, 0x80, v32\n" \
"v_xor_b32_e32 v37, 0xa0, v32\n" \
"v_xor_b32_e32 v38, 0xc0, v32\n" \
"v_xor_b32_e32 v39, 0xe0, v32\n" \
"s_mov_b32 s80, s26\n" \
"s_addk_i32 s84, 0x7c00\n" \
"buffer_load_dwordx4 v27, s[72:75], s84 offen offset:2048 lds\n" \
"s_and_b32 s81, s27, 0xffff\n" \
"s_mov_b32 s82, 0x10000000\n" \
"s_mov_b32 s83, 0x20000\n" \
"s_mov_b32 s76, s18\n" \
"s_and_b32 s77, s19, 0xffff\n" \
"s_addk_i32 s84, 0x7c00\n" \
"buffer_load_dwordx4 v27, s[72:75], s84 offen offset:3072 lds\n" \
"s_mov_b32 s78, 0x1e6000\n" \
"s_mov_b32 s79, 0x20000\n" \
"s_branch L_pgo_%=\n" \
"L_pret:\n" \
"s_setpc_b64 s[88:89]\n" \
"L_pgo_%=:\n" \
"s_mul_i32 s88, s33, 0xa000\n" \
"s_lshl_b32 s89, s3, 11\n" \
"s_and_b32 s89, s89, 0x1000\n" \
"s_add_u32 s88, s88, s89\n" \
"s_getpc_b64 s[86:87]\n" \
"L_hpa_%=:\n" \
"s_add_u32 s86, s86, L_pp1-L_hpa_%=\n" \
"s_addc_u32 s87, s87, 0\n" \
"s_add_u32 s86, s86, s88\n" \
"s_addc_u32 s87, s87, 0\n" \
"s_add_u32 m0, s30, 0x8000\n" \
"s_movk_i32 s84, 0x80\n" \
"buffer_load_dwordx4 v22, s[64:67], s84 offen offset:0 lds\n" \
"s_lshr_b32 s4, s2, 1\n" \
"s_and_b32 s4, s4, 0x7c\n" \
"v_bfe_u32 v212, v20, 4, 5\n" \
"s_movk_i32 s84, 0x1c80\n" \
"buffer_load_dwordx4 v23, s[64:67], s84 offen offset:1024 lds\n" \
"s_or_b32 s4, s4, s3\n" \
"v_min_u32_e32 v212, 26, v212\n" \
"v_mad_u64_u32 v[212:213], s[4:5], s4, 27, v[212:213]\n" \
"s_movk_i32 s84, 0x3880\n" \
"buffer_load_dwordx4 v22, s[64:67], s84 offen offset:2048 lds\n" \
"v_mul_u32_u24_e32 v213, 0xaaab, v212\n" \
"v_lshrrev_b32_e32 v213, 18, v213\n" \
"s_mov_b32 s4, 0x1fffffa\n" \
"s_movk_i32 s84, 0x5480\n" \
"buffer_load_dwordx4 v23, s[64:67], s84 offen offset:3072 lds\n" \
"v_mad_u64_u32 v[214:215], s[4:5], v213, s4, v[212:213]\n" \
"s_and_b32 s6, s2, 7\n" \
"v_mul_u32_u24_e32 v213, 27, v213\n" \
"s_mov_b64 exec, 0\n" \
"s_swappc_b64 s[88:89], s[86:87]\n" \
"s_mov_b64 exec, -1\n" \
"s_add_u32 s86, s86, 0x2000\n" \
"s_addc_u32 s87, s87, 0\n" \
"s_add_u32 m0, s31, 0x8000\n" \
"s_mov_b32 s84, 0x80000\n" \
"buffer_load_dwordx4 v26, s[68:71], s84 offen offset:0 lds\n" \
"v_lshrrev_b32_e32 v214, 1, v214\n" \
"v_and_b32_e32 v212, 1, v212\n" \
"v_mul_u32_u24_e32 v214, 9, v214\n" \
"s_addk_i32 s84, 0x7c00\n" \
"buffer_load_dwordx4 v26, s[68:71], s84 offen offset:1024 lds\n" \
"v_add3_u32 v212, v212, s6, v213\n" \
"v_add_lshl_u32 v212, v212, v214, 7\n" \
"v_mov_b32_e32 v213, 0\n" \
"s_mov_b64 exec, 0\n" \
"s_swappc_b64 s[88:89], s[86:87]\n" \
"s_mov_b64 exec, -1\n" \
"s_add_u32 s86, s86, 0x2000\n" \
"s_addc_u32 s87, s87, 0\n" \
"s_addk_i32 s84, 0x7c00\n" \
"buffer_load_dwordx4 v27, s[68:71], s84 offen offset:2048 lds\n" \
"v_lshl_add_u64 v[186:187], s[18:19], 0, v[212:213]\n" \
"s_lshl_b32 s35, s2, 10\n" \
"s_lshr_b32 s37, s2, 1\n" \
"s_addk_i32 s84, 0x7c00\n" \
"buffer_load_dwordx4 v27, s[68:71], s84 offen offset:3072 lds\n" \
"s_mul_i32 s37, s37, 0x24800\n" \
"s_and_b32 s35, s35, 0x400\n" \
"s_lshl_b32 s36, s3, 8\n" \
"s_mov_b64 exec, 0\n" \
"s_swappc_b64 s[88:89], s[86:87]\n" \
"s_mov_b64 exec, -1\n" \
"s_add_u32 s86, s86, 0x2000\n" \
"s_addc_u32 s87, s87, 0\n" \
"s_add_u32 m0, s30, 0x9000\n" \
"s_mov_b32 s84, 0x8080\n" \
"buffer_load_dwordx4 v22, s[64:67], s84 offen offset:0 lds\n" \
"s_or_b32 s35, s37, s35\n" \
"s_and_b32 s4, s2, 7\n" \
"s_lshr_b32 s6, s2, 5\n" \
"s_addk_i32 s84, 0x1c00\n" \
"buffer_load_dwordx4 v23, s[64:67], s84 offen offset:1024 lds\n" \
"s_lshl_b32 s34, s2, 5\n" \
"s_add_i32 s35, s35, s36\n" \
"s_lshl_b32 s7, s4, 10\n" \
"s_addk_i32 s84, 0x1c00\n" \
"buffer_load_dwordx4 v22, s[64:67], s84 offen offset:2048 lds\n" \
"s_and_b32 s34, s34, 0x300\n" \
"s_add_i32 s35, s35, 0x3004000\n" \
"s_lshl_b32 s37, s6, 13\n" \
"s_mov_b64 exec, 0\n" \
"s_swappc_b64 s[88:89], s[86:87]\n" \
"s_mov_b64 exec, -1\n" \
"s_andn2_b32 s85, 2, s3\n" \
"s_lshl_b32 s85, s85, 12\n" \
"s_add_u32 s86, s86, s85\n" \
"s_addc_u32 s87, s87, 0\n" \
"s_addk_i32 s84, 0x1c00\n" \
"buffer_load_dwordx4 v23, s[64:67], s84 offen offset:3072 lds\n" \
"s_or_b32 s34, s7, s34\n" \
"s_or_b32 s7, s37, s7\n" \
"v_mov_b32_e64 v213, s35\n" \
"s_add_u32 m0, s31, 0xc000\n" \
"s_mov_b32 s84, 0x80000\n" \
"buffer_load_dwordx4 v26, s[72:75], s84 offen offset:0 lds\n" \
"s_or_b32 s7, s7, s36\n" \
"s_lshr_b32 s5, s2, 3\n" \
"s_or_b32 s7, s7, 0x5c00000\n" \
"s_mov_b64 exec, 0\n" \
"s_swappc_b64 s[88:89], s[86:87]\n" \
"s_mov_b64 exec, -1\n" \
"s_addk_i32 s84, 0x7c00\n" \
"buffer_load_dwordx4 v26, s[72:75], s84 offen offset:1024 lds\n" \
"v_writelane_b32 v213, s7, 2\n" \
"s_mul_i32 s5, s5, 0x90000\n" \
"v_writelane_b32 v213, s7, 3\n" \
"s_addk_i32 s84, 0x7c00\n" \
"buffer_load_dwordx4 v27, s[72:75], s84 offen offset:2048 lds\n" \
"s_lshl_b32 s7, s3, 14\n" \
"s_lshl_b32 s4, s4, 16\n" \
"s_add_i32 s5, s5, s7\n" \
"s_addk_i32 s84, 0x7c00\n" \
"buffer_load_dwordx4 v27, s[72:75], s84 offen offset:3072 lds\n" \
"s_or_b32 s34, s34, 0x5be8000\n" \
"v_writelane_b32 v213, s34, 4\n" \
"s_add_i32 s4, s5, s4\n" \
"v_writelane_b32 v213, s34, 5\n" \
"s_add_i32 s4, s4, 0x4243800\n" \
"v_lshlrev_b32_e64 v212, 3, v20\n" \
"v_and_b32_e32 v212, 0x80, v212\n" \
"v_writelane_b32 v213, s4, 6\n" \
"s_mul_i32 s4, s6, 0xa00\n" \
"s_add_i32 s4, s4, 0x5bc0f80\n" \
"v_writelane_b32 v213, s4, 7\n" \
"v_add_u32_e32 v49, v213, v212\n" \
"s_mov_b32 m0, s32\n" \
"s_mov_b32 exec_lo, 0xffffff\n" \
"s_mov_b32 exec_hi, 0xfff0000\n" \
"global_load_lds_dword v[184:185], off\n" \
"s_mov_b64 exec, -1\n" \
"s_mov_b32 m0, s32\n" \
"s_mov_b64 exec, 0xff\n" \
"buffer_load_dword v49, s[80:83], 0 offen lds\n" \
"s_mov_b64 exec, -1\n" \
"s_mov_b32 s84, 0x5bc0000\n" \
"buffer_load_dword v180, off, s[80:83], s84 sc1\n" \
"s_cmp_lg_u32 s33, 0\n" \
"s_cbranch_scc1 L_w1_%=\n" \
"L_w0_%=:\n" \
"s_waitcnt vmcnt(19)\n" \
"s_barrier\n" \
"s_nop 0\n" \
"ds_read_b64_tr_b16 a[160:161], v32 offset:0\n" \
"ds_read_b64_tr_b16 a[162:163], v32 offset:1024\n" \
"ds_read_b128 a[128:131], v28 offset:16384\n" \
"ds_read_b64_tr_b16 a[224:225], v32 offset:8192\n" \
"ds_read_b64_tr_b16 a[226:227], v32 offset:9216\n" \
"ds_read_b128 a[192:195], v29 offset:16384\n" \
"ds_read_b128 a[132:135], v28 offset:18432\n" \
"ds_read_b128 a[196:199], v29 offset:18432\n" \
"ds_read_b64_tr_b16 a[164:165], v33 offset:0\n" \
"ds_read_b64_tr_b16 a[166:167], v33 offset:1024\n" \
"ds_read_b64_tr_b16 a[228:229], v33 offset:8192\n" \
"ds_read_b64_tr_b16 a[230:231], v33 offset:9216\n" \
"ds_read_b128 a[136:139], v28 offset:20480\n" \
"ds_read_b128 a[200:203], v29 offset:20480\n" \
"s_waitcnt lgkmcnt(11)\n" \
"v_mfma_f32_16x16x32_bf16 a[0:3], a[160:163], a[128:131], 0\n" \
"ds_read_b64_tr_b16 a[168:169], v34 offset:0\n" \
"s_lshr_b32 s4, s2, 1\n" \
"s_waitcnt lgkmcnt(9)\n" \
"v_mfma_f32_16x16x32_bf16 a[0:3], a[224:227], a[192:195], a[0:3]\n" \
"ds_read_b64_tr_b16 a[170:171], v34 offset:1024\n" \
"s_and_b32 s4, s4, 0x7c\n" \
"s_waitcnt lgkmcnt(9)\n" \
"v_mfma_f32_16x16x32_bf16 a[16:19], a[160:163], a[132:135], 0\n" \
"ds_read_b64_tr_b16 a[232:233], v34 offset:8192\n" \
"v_mov_b32_e32 v213, s13\n" \
"s_waitcnt lgkmcnt(9)\n" \
"v_mfma_f32_16x16x32_bf16 a[16:19], a[224:227], a[196:199], a[16:19]\n" \
"ds_read_b64_tr_b16 a[234:235], v34 offset:9216\n" \
"s_or_b32 s4, s4, s3\n" \
"s_waitcnt lgkmcnt(8)\n" \
"v_mfma_f32_16x16x32_bf16 a[20:23], a[164:167], a[132:135], 0\n" \
"ds_read_b128 a[140:143], v28 offset:22528\n" \
"v_lshrrev_b32_e32 v212, 4, v20\n" \
"s_waitcnt lgkmcnt(7)\n" \
"v_mfma_f32_16x16x32_bf16 a[20:23], a[228:231], a[196:199], a[20:23]\n" \
"ds_read_b128 a[204:207], v29 offset:22528\n" \
"v_mad_u64_u32 v[214:215], s[4:5], s4, 36, v[212:213]\n" \
"v_mfma_f32_16x16x32_bf16 a[4:7], a[164:167], a[128:131], 0\n" \
"ds_read_b64_tr_b16 a[172:173], v35 offset:0\n" \
"v_mov_b32_e64 v217, s9\n" \
"v_mfma_f32_16x16x32_bf16 a[4:7], a[228:231], a[192:195], a[4:7]\n" \
"ds_read_b64_tr_b16 a[174:175], v35 offset:1024\n" \
"s_movk_i32 s4, 0x240\n" \
"s_waitcnt lgkmcnt(9)\n" \
"v_mfma_f32_16x16x32_bf16 a[32:35], a[160:163], a[136:139], 0\n" \
"ds_read_b64_tr_b16 a[236:237], v35 offset:8192\n" \
"v_cmp_gt_u32_e32 vcc, s4, v20\n" \
"s_waitcnt lgkmcnt(9)\n" \
"v_mfma_f32_16x16x32_bf16 a[32:35], a[224:227], a[200:203], a[32:35]\n" \
"ds_read_b64_tr_b16 a[238:239], v35 offset:9216\n" \
"s_lshl_b32 s4, s2, 3\n" \
"v_mfma_f32_16x16x32_bf16 a[36:39], a[164:167], a[136:139], 0\n" \
"ds_read_b128 a[144:147], v28 offset:0\n" \
"s_and_b32 s4, s4, 56\n" \
"v_mfma_f32_16x16x32_bf16 a[36:39], a[228:231], a[200:203], a[36:39]\n" \
"ds_read_b128 a[208:211], v29 offset:0\n" \
"v_cndmask_b32_e32 v217, v217, v213, vcc\n" \
"s_waitcnt lgkmcnt(10)\n" \
"v_mfma_f32_16x16x32_bf16 a[40:43], a[168:171], a[136:139], 0\n" \
"ds_read_b128 a[148:151], v28 offset:2048\n" \
"v_lshlrev_b32_e32 v213, 3, v214\n" \
"s_waitcnt lgkmcnt(9)\n" \
"v_mfma_f32_16x16x32_bf16 a[40:43], a[232:235], a[200:203], a[40:43]\n" \
"ds_read_b128 a[212:215], v29 offset:2048\n" \
"v_and_b32_e32 v213, 0x1ffc0, v213\n" \
"v_mfma_f32_16x16x32_bf16 a[24:27], a[168:171], a[132:135], 0\n" \
"ds_read_b128 a[152:155], v28 offset:4096\n" \
"v_and_b32_e64 v214, 7, v214\n" \
"v_mfma_f32_16x16x32_bf16 a[24:27], a[232:235], a[196:199], a[24:27]\n" \
"ds_read_b128 a[216:219], v29 offset:4096\n" \
"v_or3_b32 v213, v214, s4, v213\n" \
"v_mfma_f32_16x16x32_bf16 a[8:11], a[168:171], a[128:131], 0\n" \
"ds_read_b128 a[156:159], v28 offset:6144\n" \
"s_lshl_b32 s4, s2, 2\n" \
"v_mfma_f32_16x16x32_bf16 a[8:11], a[232:235], a[192:195], a[8:11]\n" \
"ds_read_b128 a[220:223], v29 offset:6144\n" \
"s_mov_b32 m0, s32\n" \
"s_waitcnt lgkmcnt(13)\n" \
"v_mfma_f32_16x16x32_bf16 a[48:51], a[160:163], a[140:143], 0\n" \
"s_mov_b32 exec_lo, 0xff000000\n" \
"s_mov_b32 exec_hi, 0x0\n" \
"global_load_lds_dword v[184:185], off\n" \
"s_mov_b64 exec, -1\n" \
"s_waitcnt lgkmcnt(12)\n" \
"v_mfma_f32_16x16x32_bf16 a[48:51], a[224:227], a[204:207], a[48:51]\n" \
"ds_read_b64_tr_b16 a[176:177], v36 offset:0\n" \
"s_or_b32 s4, s4, s3\n" \
"v_mfma_f32_16x16x32_bf16 a[52:55], a[164:167], a[140:143], 0\n" \
"ds_read_b64_tr_b16 a[178:179], v36 offset:1024\n" \
"s_mul_i32 s4, s4, 18\n" \
"v_mfma_f32_16x16x32_bf16 a[52:55], a[228:231], a[204:207], a[52:55]\n" \
"s_mov_b32 m0, s32\n" \
"ds_read_b64_tr_b16 a[240:241], v36 offset:8192\n" \
"v_mfma_f32_16x16x32_bf16 a[56:59], a[168:171], a[140:143], 0\n" \
"s_mov_b32 exec_lo, 0xffff\n" \
"s_mov_b32 exec_hi, 0x0\n" \
"global_load_lds_dword v[186:187], off\n" \
"s_mov_b64 exec, -1\n" \
"v_mfma_f32_16x16x32_bf16 a[56:59], a[232:235], a[204:207], a[56:59]\n" \
"ds_read_b64_tr_b16 a[242:243], v36 offset:9216\n" \
"v_min_u32_e32 v212, 53, v212\n" \
"s_waitcnt lgkmcnt(14)\n" \
"v_mfma_f32_16x16x32_bf16 a[60:63], a[172:175], a[140:143], 0\n" \
"ds_read_b64_tr_b16 a[180:181], v37 offset:0\n" \
"s_sub_i32 s4, s4, 36\n" \
"s_waitcnt lgkmcnt(13)\n" \
"v_mfma_f32_16x16x32_bf16 a[60:63], a[236:239], a[204:207], a[60:63]\n" \
"ds_read_b64_tr_b16 a[182:183], v37 offset:1024\n" \
"v_add_u32_e32 v212, s4, v212\n" \
"v_mfma_f32_16x16x32_bf16 a[44:47], a[172:175], a[136:139], 0\n" \
"ds_read_b64_tr_b16 a[244:245], v37 offset:8192\n" \
"v_mov_b32_e64 v215, s12\n" \
"v_mfma_f32_16x16x32_bf16 a[44:47], a[236:239], a[200:203], a[44:47]\n" \
"ds_read_b64_tr_b16 a[246:247], v37 offset:9216\n" \
"v_mov_b32_e64 v216, s8\n" \
"v_mfma_f32_16x16x32_bf16 a[28:31], a[172:175], a[132:135], 0\n" \
"ds_read_b64_tr_b16 a[184:185], v38 offset:0\n" \
"v_cndmask_b32_e64 v212, v212, v213, vcc\n" \
"v_mfma_f32_16x16x32_bf16 a[28:31], a[236:239], a[196:199], a[28:31]\n" \
"ds_read_b64_tr_b16 a[186:187], v38 offset:1024\n" \
"v_cndmask_b32_e64 v216, v216, v215, vcc\n" \
"v_mfma_f32_16x16x32_bf16 a[12:15], a[172:175], a[128:131], 0\n" \
"v_lshlrev_b32_e32 v212, 7, v212\n" \
"v_mov_b32_e32 v213, 0\n" \
"v_mfma_f32_16x16x32_bf16 a[12:15], a[236:239], a[192:195], a[12:15]\n" \
"v_lshl_add_u64 v[188:189], v[216:217], 0, v[212:213]\n" \
"s_movk_i32 s5, 0xd80\n" \
"s_waitcnt lgkmcnt(15)\n" \
"v_mfma_f32_16x16x32_bf16 a[76:79], a[172:175], a[144:147], 0\n" \
"ds_read_b64_tr_b16 a[248:249], v38 offset:8192\n" \
"s_lshr_b32 s6, s2, 5\n" \
"v_mfma_f32_16x16x32_bf16 a[76:79], a[236:239], a[208:211], a[76:79]\n" \
"v_lshrrev_b32_e32 v212, 4, v20\n" \
"v_lshl_or_b32 v215, s3, 6, v212\n" \
"v_mfma_f32_16x16x32_bf16 a[72:75], a[168:171], a[144:147], 0\n" \
"v_mul_u32_u24_e32 v216, 0x2aab, v215\n" \
"v_lshrrev_b32_e64 v216, 16, v216\n" \
"v_mfma_f32_16x16x32_bf16 a[72:75], a[232:235], a[208:211], a[72:75]\n" \
"ds_read_b64_tr_b16 a[250:251], v38 offset:9216\n" \
"v_mad_i32_i24 v217, v216, -6, v215\n" \
"v_mfma_f32_16x16x32_bf16 a[68:71], a[164:167], a[144:147], 0\n" \
"v_add_u32_e32 v218, 0xfffffe50, v215\n" \
"s_mul_i32 s35, s6, 0x48\n" \
"v_mfma_f32_16x16x32_bf16 a[68:71], a[228:231], a[208:211], a[68:71]\n" \
"v_lshrrev_b32_e32 v219, 1, v217\n" \
"v_add_u32_e32 v218, s35, v216\n" \
"v_mfma_f32_16x16x32_bf16 a[64:67], a[160:163], a[144:147], 0\n" \
"v_mul_u32_u24_e32 v219, 0x480, v219\n" \
"v_and_b32_e64 v214, 16, v20\n" \
"v_mfma_f32_16x16x32_bf16 a[64:67], a[224:227], a[208:211], a[64:67]\n" \
"ds_read_b64_tr_b16 a[188:189], v39 offset:0\n" \
"v_mad_u32_u24 v219, v218, s5, v219\n" \
"v_mfma_f32_16x16x32_bf16 a[80:83], a[160:163], a[148:151], 0\n" \
"v_or_b32_e32 v192, v219, v214\n" \
"v_mov_b32_e32 v220, 2.0\n" \
"s_waitcnt lgkmcnt(15)\n" \
"v_mfma_f32_16x16x32_bf16 a[80:83], a[224:227], a[212:215], a[80:83]\n" \
"v_mov_b32_e32 v222, 7\n" \
"v_lshlrev_b32_e64 v220, 7, v218\n" \
"v_mfma_f32_16x16x32_bf16 a[84:87], a[164:167], a[148:151], 0\n" \
"ds_read_b64_tr_b16 a[190:191], v39 offset:1024\n" \
"v_lshrrev_b32_e64 v218, 1, v218\n" \
"v_mfma_f32_16x16x32_bf16 a[84:87], a[228:231], a[212:215], a[84:87]\n" \
"v_and_b32_e32 v218, 4, v218\n" \
"v_and_b32_e32 v216, 2, v216\n" \
"v_mfma_f32_16x16x32_bf16 a[88:91], a[168:171], a[148:151], 0\n" \
"s_mul_i32 s7, s6, 0x2400\n" \
"s_sub_i32 s34, 0x22000, s7\n" \
"v_mfma_f32_16x16x32_bf16 a[88:91], a[232:235], a[212:215], a[88:91]\n" \
"ds_read_b64_tr_b16 a[252:253], v39 offset:8192\n" \
"v_bitop3_b32 v216, v217, v218, v216 bitop3:0x1e\n" \
"v_mfma_f32_16x16x32_bf16 a[92:95], a[172:175], a[148:151], 0\n" \
"v_or_b32_e32 v213, 0x24400, v20\n" \
"v_lshlrev_b32_e64 v216, 4, v216\n" \
"v_mfma_f32_16x16x32_bf16 a[92:95], a[236:239], a[212:215], a[92:95]\n" \
"v_add3_u32 v196, v220, s34, v216\n" \
"v_mov_b32_e64 v216, v213\n" \
"v_mfma_f32_16x16x32_bf16 a[108:111], a[172:175], a[152:155], 0\n" \
"ds_read_b64_tr_b16 a[254:255], v39 offset:9216\n" \
"s_movk_i32 s36, 0x1b0\n" \
"v_mfma_f32_16x16x32_bf16 a[108:111], a[236:239], a[216:219], a[108:111]\n" \
"v_or_b32_e32 v216, 0x100, v215\n" \
"v_mul_u32_u24_e32 v217, 0x2aab, v216\n" \
"v_mfma_f32_16x16x32_bf16 a[104:107], a[168:171], a[152:155], 0\n" \
"v_add_u32_e32 v215, 0xffffff50, v215\n" \
"v_lshrrev_b32_e32 v217, 16, v217\n" \
"v_mfma_f32_16x16x32_bf16 a[104:107], a[232:235], a[216:219], a[104:107]\n" \
"v_mov_b32_e64 v220, v215\n" \
"v_mad_i32_i24 v218, v217, -6, v216\n" \
"v_mfma_f32_16x16x32_bf16 a[100:103], a[164:167], a[152:155], 0\n" \
"v_cmp_gt_u32_e32 vcc, s36, v216\n" \
"v_mov_b32_e32 v221, 7\n" \
"v_mfma_f32_16x16x32_bf16 a[100:103], a[228:231], a[216:219], a[100:103]\n" \
"v_lshrrev_b32_e32 v222, 1, v218\n" \
"v_cndmask_b32_e32 v217, v220, v217, vcc\n" \
"v_mfma_f32_16x16x32_bf16 a[96:99], a[160:163], a[152:155], 0\n" \
"s_movk_i32 s4, 0x48\n" \
"v_add_u32_e32 v220, s35, v217\n" \
"v_mfma_f32_16x16x32_bf16 a[96:99], a[224:227], a[216:219], a[96:99]\n" \
"v_mul_u32_u24_e32 v222, 0x480, v222\n" \
"v_mad_u32_u24 v222, v220, s5, v222\n" \
"s_waitcnt lgkmcnt(15)\n" \
"v_mfma_f32_16x16x32_bf16 a[112:115], a[160:163], a[156:159], 0\n" \
"v_cmp_gt_u32_e64 s[4:5], s4, v215\n" \
"v_cndmask_b32_e32 v215, v221, v218, vcc\n" \
"v_mfma_f32_16x16x32_bf16 a[112:115], a[224:227], a[220:223], a[112:115]\n" \
"v_lshrrev_b32_e32 v218, 1, v220\n" \
"v_or_b32_e32 v214, v222, v214\n" \
"v_mfma_f32_16x16x32_bf16 a[116:119], a[164:167], a[156:159], 0\n" \
"v_mov_b32_e32 v222, 2.0\n" \
"v_and_b32_e32 v218, 4, v218\n" \
"v_mfma_f32_16x16x32_bf16 a[116:119], a[228:231], a[220:223], a[116:119]\n" \
"v_and_b32_e64 v217, 2, v217\n" \
"v_bitop3_b32 v215, v215, v218, v217 bitop3:0x1e\n" \
"v_mfma_f32_16x16x32_bf16 a[120:123], a[168:171], a[156:159], 0\n" \
"v_lshlrev_b32_e32 v223, 1, v220\n" \
"v_mov_b32_e32 v224, 2.0\n" \
"v_mfma_f32_16x16x32_bf16 a[120:123], a[232:235], a[220:223], a[120:123]\n" \
"v_cndmask_b32_e32 v193, v222, v214, vcc\n" \
"v_lshlrev_b32_e32 v214, 7, v220\n" \
"v_mfma_f32_16x16x32_bf16 a[124:127], a[172:175], a[156:159], 0\n" \
"v_lshlrev_b32_e32 v215, 4, v215\n" \
"s_mul_i32 s39, s3, 18\n" \
"v_mfma_f32_16x16x32_bf16 a[124:127], a[236:239], a[220:223], a[124:127]\n" \
"s_waitcnt vmcnt(5) lgkmcnt(0)\n" \
"s_barrier\n" \
"v_mfma_f32_16x16x32_bf16 v[52:55], a[176:179], a[128:131], 0\n" \
"s_add_u32 m0, s30, 0x0\n" \
"s_movk_i32 s84, 0x100\n" \
"v_mfma_f32_16x16x32_bf16 v[52:55], a[240:243], a[192:195], v[52:55]\n" \
"buffer_load_dwordx4 v22, s[64:67], s84 offen offset:0 lds\n" \
"v_mfma_f32_16x16x32_bf16 v[56:59], a[180:183], a[128:131], 0\n" \
"ds_read_b64_tr_b16 a[160:161], v32 offset:32768\n" \
"v_add3_u32 v214, v214, s34, v215\n" \
"v_mfma_f32_16x16x32_bf16 v[56:59], a[244:247], a[192:195], v[56:59]\n" \
"s_movk_i32 s84, 0x1d00\n" \
"v_mfma_f32_16x16x32_bf16 v[60:63], a[184:187], a[128:131], 0\n" \
"buffer_load_dwordx4 v23, s[64:67], s84 offen offset:1024 lds\n" \
"v_mfma_f32_16x16x32_bf16 v[60:63], a[248:251], a[192:195], v[60:63]\n" \
"ds_read_b64_tr_b16 a[162:163], v32 offset:33792\n" \
"v_cndmask_b32_e64 v195, v224, v223, s[4:5]\n" \
"v_mfma_f32_16x16x32_bf16 v[64:67], a[188:191], a[128:131], 0\n" \
"ds_read_b64_tr_b16 a[224:225], v32 offset:40960\n" \
"s_movk_i32 s4, 0x1f8\n" \
"v_mfma_f32_16x16x32_bf16 v[64:67], a[252:255], a[192:195], v[64:67]\n" \
"ds_read_b128 a[128:131], v28 offset:49152\n" \
"v_mov_b32_e64 v215, v213\n" \
"v_mfma_f32_16x16x32_bf16 v[80:83], a[188:191], a[132:135], 0\n" \
"ds_read_b128 a[192:195], v29 offset:49152\n" \
"s_add_i32 s35, s35, s39\n" \
"v_mfma_f32_16x16x32_bf16 v[80:83], a[252:255], a[196:199], v[80:83]\n" \
"s_movk_i32 s84, 0x3900\n" \
"v_mfma_f32_16x16x32_bf16 v[76:79], a[184:187], a[132:135], 0\n" \
"buffer_load_dwordx4 v22, s[64:67], s84 offen offset:2048 lds\n" \
"v_mfma_f32_16x16x32_bf16 v[76:79], a[248:251], a[196:199], v[76:79]\n" \
"s_movk_i32 s84, 0x5500\n" \
"v_mfma_f32_16x16x32_bf16 v[72:75], a[180:183], a[132:135], 0\n" \
"buffer_load_dwordx4 v23, s[64:67], s84 offen offset:3072 lds\n" \
"v_mfma_f32_16x16x32_bf16 v[72:75], a[244:247], a[196:199], v[72:75]\n" \
"s_add_u32 m0, s31, 0x0\n" \
"s_mov_b32 s84, 0x100000\n" \
"v_mfma_f32_16x16x32_bf16 v[68:71], a[176:179], a[132:135], 0\n" \
"buffer_load_dwordx4 v26, s[68:71], s84 offen offset:0 lds\n" \
"v_mfma_f32_16x16x32_bf16 v[68:71], a[240:243], a[196:199], v[68:71]\n" \
"ds_read_b128 a[132:135], v28 offset:51200\n" \
"v_cmp_gt_u32_e64 vcc, s4, v216\n" \
"v_mfma_f32_16x16x32_bf16 v[84:87], a[176:179], a[136:139], 0\n" \
"ds_read_b128 a[196:199], v29 offset:51200\n" \
"v_add_u32_e64 v212, s35, v212\n" \
"v_mfma_f32_16x16x32_bf16 v[84:87], a[240:243], a[200:203], v[84:87]\n" \
"s_addk_i32 s84, 0x7c00\n" \
"v_mfma_f32_16x16x32_bf16 v[88:91], a[180:183], a[136:139], 0\n" \
"buffer_load_dwordx4 v26, s[68:71], s84 offen offset:1024 lds\n" \
"v_mfma_f32_16x16x32_bf16 v[88:91], a[244:247], a[200:203], v[88:91]\n" \
"ds_read_b64_tr_b16 a[226:227], v32 offset:41984\n" \
"v_mov_b32_e32 v219, 2.0\n" \
"v_mfma_f32_16x16x32_bf16 v[92:95], a[184:187], a[136:139], 0\n" \
"s_addk_i32 s84, 0x7c00\n" \
"v_mfma_f32_16x16x32_bf16 v[92:95], a[248:251], a[200:203], v[92:95]\n" \
"buffer_load_dwordx4 v27, s[68:71], s84 offen offset:2048 lds\n" \
"v_mfma_f32_16x16x32_bf16 v[96:99], a[188:191], a[136:139], 0\n" \
"ds_read_b64_tr_b16 a[164:165], v33 offset:32768\n" \
"v_cndmask_b32_e32 v197, v215, v214, vcc\n" \
"v_mfma_f32_16x16x32_bf16 v[96:99], a[252:255], a[200:203], v[96:99]\n" \
"ds_read_b128 a[136:139], v28 offset:53248\n" \
"s_movk_i32 s35, 0x120\n" \
"v_mfma_f32_16x16x32_bf16 v[112:115], a[188:191], a[140:143], 0\n" \
"ds_read_b128 a[200:203], v29 offset:53248\n" \
"v_mul_u32_u24_e32 v214, 0xd80, v212\n" \
"v_mfma_f32_16x16x32_bf16 v[112:115], a[252:255], a[204:207], v[112:115]\n" \
"s_addk_i32 s84, 0x7c00\n" \
"v_mfma_f32_16x16x32_bf16 v[108:111], a[184:187], a[140:143], 0\n" \
"buffer_load_dwordx4 v27, s[68:71], s84 offen offset:3072 lds\n" \
"v_mfma_f32_16x16x32_bf16 v[108:111], a[248:251], a[204:207], v[108:111]\n" \
"s_add_u32 m0, s30, 0x1000\n" \
"s_mov_b32 s84, 0x8100\n" \
"v_mfma_f32_16x16x32_bf16 v[104:107], a[180:183], a[140:143], 0\n" \
"buffer_load_dwordx4 v22, s[64:67], s84 offen offset:0 lds\n" \
"v_mfma_f32_16x16x32_bf16 v[104:107], a[244:247], a[204:207], v[104:107]\n" \
"ds_read_b64_tr_b16 a[166:167], v33 offset:33792\n" \
"v_cmp_gt_u32_e64 vcc, s35, v20\n" \
"v_mfma_f32_16x16x32_bf16 v[100:103], a[176:179], a[140:143], 0\n" \
"ds_read_b64_tr_b16 a[228:229], v33 offset:40960\n" \
"v_lshrrev_b32_e64 v215, 1, v212\n" \
"v_mfma_f32_16x16x32_bf16 v[100:103], a[240:243], a[204:207], v[100:103]\n" \
"ds_read_b128 a[140:143], v28 offset:55296\n" \
"s_and_b32 s4, s2, 7\n" \
"v_mfma_f32_16x16x32_bf16 v[116:119], a[176:179], a[144:147], 0\n" \
"ds_read_b128 a[204:207], v29 offset:55296\n" \
"v_cndmask_b32_e32 v194, v219, v214, vcc\n" \
"v_mfma_f32_16x16x32_bf16 v[116:119], a[240:243], a[208:211], v[116:119]\n" \
"s_addk_i32 s84, 0x1c00\n" \
"v_mfma_f32_16x16x32_bf16 v[120:123], a[180:183], a[144:147], 0\n" \
"buffer_load_dwordx4 v23, s[64:67], s84 offen offset:1024 lds\n" \
"v_mfma_f32_16x16x32_bf16 v[120:123], a[244:247], a[208:211], v[120:123]\n" \
"s_addk_i32 s84, 0x1c00\n" \
"v_mfma_f32_16x16x32_bf16 v[124:127], a[184:187], a[144:147], 0\n" \
"buffer_load_dwordx4 v22, s[64:67], s84 offen offset:2048 lds\n" \
"v_mfma_f32_16x16x32_bf16 v[124:127], a[248:251], a[208:211], v[124:127]\n" \
"s_addk_i32 s84, 0x1c00\n" \
".fill (L_pb + 3808 - .) / 4, 4, 0xBF800000\n" \
"L_pp1:\n" \
"s_cbranch_execz L_pret\n" \
"v_mfma_f32_16x16x32_bf16 v[128:131], a[188:191], a[144:147], 0\n" \
"buffer_load_dwordx4 v23, s[64:67], s84 offen offset:3072 lds\n" \
"v_mfma_f32_16x16x32_bf16 v[128:131], a[252:255], a[208:211], v[128:131]\n" \
"ds_read_b128 a[144:147], v28 offset:32768\n" \
"v_lshlrev_b32_e32 v214, 7, v212\n" \
"v_mfma_f32_16x16x32_bf16 v[144:147], a[188:191], a[148:151], 0\n" \
"ds_read_b128 a[208:211], v29 offset:32768\n" \
"v_lshrrev_b32_e64 v212, 2, v212\n" \
"v_mfma_f32_16x16x32_bf16 v[144:147], a[252:255], a[212:215], v[144:147]\n" \
"s_add_u32 m0, s31, 0x4000\n" \
"s_mov_b32 s84, 0x100000\n" \
"v_mfma_f32_16x16x32_bf16 v[140:143], a[184:187], a[148:151], 0\n" \
"buffer_load_dwordx4 v26, s[72:75], s84 offen offset:0 lds\n" \
"v_mfma_f32_16x16x32_bf16 v[140:143], a[248:251], a[212:215], v[140:143]\n" \
"ds_read_b64_tr_b16 a[230:231], v33 offset:41984\n" \
"v_and_b32_e64 v212, 2, v212\n" \
"v_mfma_f32_16x16x32_bf16 v[136:139], a[180:183], a[148:151], 0\n" \
"s_addk_i32 s84, 0x7c00\n" \
"v_mfma_f32_16x16x32_bf16 v[136:139], a[244:247], a[212:215], v[136:139]\n" \
"buffer_load_dwordx4 v26, s[72:75], s84 offen offset:1024 lds\n" \
"v_mfma_f32_16x16x32_bf16 v[132:135], a[176:179], a[148:151], 0\n" \
"ds_read_b64_tr_b16 a[168:169], v34 offset:32768\n" \
"v_and_or_b32 v212, v215, 1, v212\n" \
"v_mfma_f32_16x16x32_bf16 v[132:135], a[240:243], a[212:215], v[132:135]\n" \
"ds_read_b128 a[148:151], v28 offset:34816\n" \
"s_lshl_b32 s5, s4, 2\n" \
"v_mfma_f32_16x16x32_bf16 v[148:151], a[176:179], a[152:155], 0\n" \
"ds_read_b128 a[212:215], v29 offset:34816\n" \
"s_bfe_u32 s38, s2, 0x20003\n" \
"v_mfma_f32_16x16x32_bf16 v[148:151], a[240:243], a[216:219], v[148:151]\n" \
"ds_read_b64_tr_b16 a[170:171], v34 offset:33792\n" \
"v_lshlrev_b32_e64 v212, 5, v212\n" \
"v_mfma_f32_16x16x32_bf16 v[152:155], a[180:183], a[152:155], 0\n" \
"ds_read_b64_tr_b16 a[232:233], v34 offset:40960\n" \
"s_movk_i32 s35, 0x60\n" \
"v_mfma_f32_16x16x32_bf16 v[152:155], a[244:247], a[216:219], v[152:155]\n" \
"ds_read_b64_tr_b16 a[234:235], v34 offset:41984\n" \
"s_or_b32 s5, s5, s38\n" \
"v_mfma_f32_16x16x32_bf16 v[156:159], a[184:187], a[152:155], 0\n" \
"ds_read_b64_tr_b16 a[172:173], v35 offset:32768\n" \
"v_bitop3_b32 v212, v212, s35, v214 bitop3:0x36\n" \
"v_mfma_f32_16x16x32_bf16 v[156:159], a[248:251], a[216:219], v[156:159]\n" \
"ds_read_b64_tr_b16 a[174:175], v35 offset:33792\n" \
"s_lshr_b32 s36, s2, 3\n" \
"v_mfma_f32_16x16x32_bf16 v[160:163], a[188:191], a[152:155], 0\n" \
"ds_read_b64_tr_b16 a[236:237], v35 offset:40960\n" \
"v_add_u32_e32 v212, s34, v212\n" \
"v_mfma_f32_16x16x32_bf16 v[160:163], a[252:255], a[216:219], v[160:163]\n" \
"ds_read_b128 a[152:155], v28 offset:36864\n" \
"s_mul_i32 s34, s5, 0x12000\n" \
"v_mfma_f32_16x16x32_bf16 v[176:179], a[188:191], a[156:159], 0\n" \
"ds_read_b128 a[216:219], v29 offset:36864\n" \
"s_or_b32 s40, s34, 0x6000000\n" \
"v_mfma_f32_16x16x32_bf16 v[176:179], a[252:255], a[220:223], v[176:179]\n" \
"ds_read_b64_tr_b16 a[238:239], v35 offset:41984\n" \
"s_mul_i32 s34, s36, 0x90000\n" \
"v_mfma_f32_16x16x32_bf16 v[172:175], a[184:187], a[156:159], 0\n" \
"s_lshl_b32 s36, s3, 14\n" \
"s_add_i32 s34, s34, s36\n" \
"v_mfma_f32_16x16x32_bf16 v[172:175], a[248:251], a[220:223], v[172:175]\n" \
"s_lshl_b32 s38, s5, 8\n" \
"s_add_i32 s41, s34, 0x4240000\n" \
"v_mfma_f32_16x16x32_bf16 v[168:171], a[180:183], a[156:159], 0\n" \
"s_mul_i32 s34, s5, 36\n" \
"s_add_i32 s5, s7, s40\n" \
"v_mfma_f32_16x16x32_bf16 v[168:171], a[244:247], a[220:223], v[168:171]\n" \
"s_addk_i32 s5, 0x2000\n" \
"s_mov_b32 s37, 2.0\n" \
"v_mfma_f32_16x16x32_bf16 v[164:167], a[176:179], a[156:159], 0\n" \
"s_or_b32 s38, s38, 0x5be8000\n" \
"v_mfma_f32_16x16x32_bf16 v[164:167], a[240:243], a[220:223], v[164:167]\n" \
"ds_read_b128 a[156:159], v28 offset:38912\n" \
"ds_read_b128 a[220:223], v29 offset:38912\n" \
"s_waitcnt lgkmcnt(15)\n" \
"v_mfma_f32_16x16x32_bf16 a[0:3], a[160:163], a[128:131], a[0:3]\n" \
"s_addk_i32 s84, 0x7c00\n" \
"v_mfma_f32_16x16x32_bf16 a[0:3], a[224:227], a[192:195], a[0:3]\n" \
"buffer_load_dwordx4 v27, s[72:75], s84 offen offset:2048 lds\n" \
"v_mfma_f32_16x16x32_bf16 a[4:7], a[164:167], a[128:131], a[4:7]\n" \
"ds_read_b64_tr_b16 a[176:177], v36 offset:32768\n" \
"s_waitcnt lgkmcnt(15)\n" \
"v_mfma_f32_16x16x32_bf16 a[4:7], a[228:231], a[192:195], a[4:7]\n" \
"s_addk_i32 s84, 0x7c00\n" \
"s_waitcnt lgkmcnt(11)\n" \
"v_mfma_f32_16x16x32_bf16 a[8:11], a[168:171], a[128:131], a[8:11]\n" \
"buffer_load_dwordx4 v27, s[72:75], s84 offen offset:3072 lds\n" \
"s_waitcnt lgkmcnt(9)\n" \
"v_mfma_f32_16x16x32_bf16 a[8:11], a[232:235], a[192:195], a[8:11]\n" \
"ds_read_b64_tr_b16 a[178:179], v36 offset:33792\n" \
"s_waitcnt lgkmcnt(8)\n" \
"v_mfma_f32_16x16x32_bf16 a[12:15], a[172:175], a[128:131], a[12:15]\n" \
"ds_read_b64_tr_b16 a[240:241], v36 offset:40960\n" \
"s_waitcnt lgkmcnt(5)\n" \
"v_mfma_f32_16x16x32_bf16 a[12:15], a[236:239], a[192:195], a[12:15]\n" \
"s_cmp_lg_u32 s3, 0\n" \
"s_cselect_b32 s36, s37, s5\n" \
"v_mfma_f32_16x16x32_bf16 a[28:31], a[172:175], a[132:135], a[28:31]\n" \
"s_lshl_b32 s5, s6, 2\n" \
"s_or_b32 s5, s38, s5\n" \
"v_mfma_f32_16x16x32_bf16 a[28:31], a[236:239], a[196:199], a[28:31]\n" \
"ds_read_b64_tr_b16 a[242:243], v36 offset:41984\n" \
"s_lshl_b32 s35, s3, 10\n" \
"v_mfma_f32_16x16x32_bf16 a[24:27], a[168:171], a[132:135], a[24:27]\n" \
"s_cmpk_lt_u32 s3, 3\n" \
"s_cselect_b32 s37, s37, s5\n" \
"v_mfma_f32_16x16x32_bf16 a[24:27], a[232:235], a[196:199], a[24:27]\n" \
"s_lshl_b32 s5, s4, 16\n" \
"s_lshl_b32 s4, s4, 11\n" \
"v_mfma_f32_16x16x32_bf16 a[20:23], a[164:167], a[132:135], a[20:23]\n" \
"v_cndmask_b32_e32 v198, v213, v212, vcc\n" \
"v_or_b32_e32 v212, s35, v20\n" \
"v_mfma_f32_16x16x32_bf16 a[20:23], a[228:231], a[196:199], a[20:23]\n" \
"ds_read_b64_tr_b16 a[180:181], v37 offset:32768\n" \
"s_or_b32 s39, s40, s35\n" \
"v_mfma_f32_16x16x32_bf16 a[16:19], a[160:163], a[132:135], a[16:19]\n" \
"s_or_b32 s55, s41, s4\n" \
"v_or_b32_e32 v199, 0x22000, v212\n" \
"v_mfma_f32_16x16x32_bf16 a[16:19], a[224:227], a[196:199], a[16:19]\n" \
"s_add_i32 s35, s39, s7\n" \
"s_add_i32 s54, s41, s5\n" \
"v_mfma_f32_16x16x32_bf16 a[32:35], a[160:163], a[136:139], a[32:35]\n" \
"s_mov_b32 m0, s32\n" \
"ds_read_b64_tr_b16 a[182:183], v37 offset:33792\n" \
"v_mfma_f32_16x16x32_bf16 a[32:35], a[224:227], a[200:203], a[32:35]\n" \
"s_mov_b32 exec_lo, 0x0\n" \
"s_mov_b32 exec_hi, 0xff\n" \
"global_load_lds_dword v[184:185], off\n" \
"s_mov_b64 exec, -1\n" \
"v_mfma_f32_16x16x32_bf16 a[36:39], a[164:167], a[136:139], a[36:39]\n" \
"s_add_i32 s55, s55, 0x80000\n" \
"v_mov_b32_e32 v215, 0xffff\n" \
"v_mfma_f32_16x16x32_bf16 a[36:39], a[228:231], a[200:203], a[36:39]\n" \
"ds_read_b64_tr_b16 a[244:245], v37 offset:40960\n" \
"s_and_b32 s4, s3, 1\n" \
"v_mfma_f32_16x16x32_bf16 a[40:43], a[168:171], a[136:139], a[40:43]\n" \
"s_lshl_b32 s5, s4, 4\n" \
"v_lshrrev_b32_e32 v212, 4, v20\n" \
"v_mfma_f32_16x16x32_bf16 a[40:43], a[232:235], a[200:203], a[40:43]\n" \
"s_mov_b32 m0, s32\n" \
"v_lshrrev_b32_e32 v218, 6, v20\n" \
"v_mfma_f32_16x16x32_bf16 a[44:47], a[172:175], a[136:139], a[44:47]\n" \
"s_mov_b32 exec_lo, 0xffff0000\n" \
"s_mov_b32 exec_hi, 0x0\n" \
"global_load_lds_dword v[186:187], off\n" \
"s_mov_b64 exec, -1\n" \
"v_mfma_f32_16x16x32_bf16 a[44:47], a[236:239], a[200:203], a[44:47]\n" \
"ds_read_b64_tr_b16 a[246:247], v37 offset:41984\n" \
"v_and_b32_e64 v219, 12, v218\n" \
"v_mfma_f32_16x16x32_bf16 a[60:63], a[172:175], a[140:143], a[60:63]\n" \
"v_and_or_b32 v214, v212, 15, s5\n" \
"v_cmp_gt_u32_e64 vcc, v219, v214\n" \
"v_mfma_f32_16x16x32_bf16 a[60:63], a[236:239], a[204:207], a[60:63]\n" \
"v_mov_b32_e32 v220, 0xffff0000\n" \
"s_lshl_b32 s5, s2, 2\n" \
"v_mfma_f32_16x16x32_bf16 a[56:59], a[168:171], a[140:143], a[56:59]\n" \
"ds_read_b64_tr_b16 a[184:185], v38 offset:32768\n" \
"v_cndmask_b32_e64 v217, v215, 0, vcc\n" \
"v_mfma_f32_16x16x32_bf16 a[56:59], a[232:235], a[204:207], a[56:59]\n" \
"v_cmp_lt_u32_e32 vcc, v219, v214\n" \
"v_mov_b32_e64 v213, s23\n" \
"v_mfma_f32_16x16x32_bf16 a[52:55], a[164:167], a[140:143], a[52:55]\n" \
"s_or_b32 s5, s5, s3\n" \
"v_cndmask_b32_e32 v221, 0, v220, vcc\n" \
"v_mfma_f32_16x16x32_bf16 a[52:55], a[228:231], a[204:207], a[52:55]\n" \
"ds_read_b64_tr_b16 a[186:187], v38 offset:33792\n" \
"v_or_b32_e64 v12, v217, v221\n" \
"v_mfma_f32_16x16x32_bf16 a[48:51], a[160:163], a[140:143], a[48:51]\n" \
"v_or_b32_e32 v217, 2, v219\n" \
"v_cmp_gt_u32_e32 vcc, v217, v214\n" \
"v_mfma_f32_16x16x32_bf16 a[48:51], a[224:227], a[204:207], a[48:51]\n" \
"v_mov_b32_e32 v216, s25\n" \
"s_movk_i32 s40, 0xf400\n" \
"v_mfma_f32_16x16x32_bf16 a[64:67], a[160:163], a[144:147], a[64:67]\n" \
"ds_read_b64_tr_b16 a[248:249], v38 offset:40960\n" \
"v_cndmask_b32_e64 v221, v215, 0, vcc\n" \
"v_mfma_f32_16x16x32_bf16 a[64:67], a[224:227], a[208:211], a[64:67]\n" \
"v_cmp_lt_u32_e32 vcc, v217, v214\n" \
"s_lshl_b32 s4, s4, 6\n" \
"v_mfma_f32_16x16x32_bf16 a[68:71], a[164:167], a[144:147], a[68:71]\n" \
"v_cndmask_b32_e32 v217, 0, v220, vcc\n" \
"v_or_b32_e32 v13, v221, v217\n" \
"v_mfma_f32_16x16x32_bf16 a[68:71], a[228:231], a[208:211], a[68:71]\n" \
"ds_read_b64_tr_b16 a[250:251], v38 offset:41984\n" \
"v_or_b32_e64 v217, 16, v219\n" \
"v_mfma_f32_16x16x32_bf16 a[72:75], a[168:171], a[144:147], a[72:75]\n" \
"v_cmp_gt_u32_e32 vcc, v217, v214\n" \
"s_nop 1\n" \
"v_cndmask_b32_e64 v221, v215, 0, vcc\n" \
"v_mfma_f32_16x16x32_bf16 a[72:75], a[232:235], a[208:211], a[72:75]\n" \
"v_cmp_lt_u32_e32 vcc, v217, v214\n" \
"s_nop 1\n" \
"v_cndmask_b32_e64 v217, 0, v220, vcc\n" \
"v_mfma_f32_16x16x32_bf16 a[76:79], a[172:175], a[144:147], a[76:79]\n" \
"v_or_b32_e32 v14, v221, v217\n" \
"v_or_b32_e32 v217, 18, v219\n" \
"v_mfma_f32_16x16x32_bf16 a[76:79], a[236:239], a[208:211], a[76:79]\n" \
"ds_read_b64_tr_b16 a[188:189], v39 offset:32768\n" \
"v_cmp_gt_u32_e64 vcc, v217, v214\n" \
"v_mfma_f32_16x16x32_bf16 a[92:95], a[172:175], a[148:151], a[92:95]\n" \
"s_nop 0\n" \
"v_cndmask_b32_e64 v215, v215, 0, vcc\n" \
"v_cmp_lt_u32_e32 vcc, v217, v214\n" \
"v_mfma_f32_16x16x32_bf16 a[92:95], a[236:239], a[212:215], a[92:95]\n" \
"s_nop 0\n" \
"v_cndmask_b32_e32 v214, 0, v220, vcc\n" \
"v_or_b32_e64 v15, v215, v214\n" \
"v_mfma_f32_16x16x32_bf16 a[88:91], a[168:171], a[148:151], a[88:91]\n" \
"ds_read_b64_tr_b16 a[190:191], v39 offset:33792\n" \
"v_mad_u64_u32 v[214:215], s[6:7], s5, 42, v[212:213]\n" \
"v_mfma_f32_16x16x32_bf16 a[88:91], a[232:235], a[212:215], a[88:91]\n" \
"s_movk_i32 s5, 0x1440\n" \
"v_cmp_gt_u32_e32 vcc, s5, v214\n" \
"v_mfma_f32_16x16x32_bf16 a[84:87], a[164:167], a[148:151], a[84:87]\n" \
"v_min_u32_e32 v217, 0xa43f, v214\n" \
"v_mov_b32_e64 v214, s24\n" \
"v_mfma_f32_16x16x32_bf16 a[84:87], a[228:231], a[212:215], a[84:87]\n" \
"ds_read_b64_tr_b16 a[252:253], v39 offset:40960\n" \
"v_cndmask_b32_e64 v215, v216, v213, vcc\n" \
"v_mfma_f32_16x16x32_bf16 a[80:83], a[160:163], a[148:151], a[80:83]\n" \
"v_mov_b32_e32 v213, s22\n" \
"s_movk_i32 s6, 0xffee\n" \
"v_mfma_f32_16x16x32_bf16 a[80:83], a[224:227], a[212:215], a[80:83]\n" \
"v_cndmask_b32_e64 v214, v214, v213, vcc\n" \
"v_add_u32_e32 v213, 0xffffebc0, v217\n" \
"v_mfma_f32_16x16x32_bf16 a[96:99], a[160:163], a[152:155], a[96:99]\n" \
"ds_read_b64_tr_b16 a[254:255], v39 offset:41984\n" \
"s_lshl_b32 s5, s2, 5\n" \
"v_mfma_f32_16x16x32_bf16 a[96:99], a[224:227], a[216:219], a[96:99]\n" \
"v_cndmask_b32_e32 v213, v213, v217, vcc\n" \
"v_lshlrev_b32_e64 v216, 7, v213\n" \
"v_mfma_f32_16x16x32_bf16 a[100:103], a[164:167], a[152:155], a[100:103]\n" \
"v_mul_u32_u24_e32 v213, 0xe39, v212\n" \
"v_mov_b32_e64 v217, 0\n" \
"v_mfma_f32_16x16x32_bf16 a[100:103], a[228:231], a[216:219], a[100:103]\n" \
"v_lshrrev_b32_e64 v213, 16, v213\n" \
"v_lshl_add_u64 v[16:17], v[214:215], 0, v[216:217]\n" \
"v_mfma_f32_16x16x32_bf16 a[104:107], a[168:171], a[152:155], a[104:107]\n" \
"v_mad_i32_i24 v214, v213, s6, v212\n" \
"s_add_i32 s5, s5, 32\n" \
"v_mfma_f32_16x16x32_bf16 a[104:107], a[232:235], a[216:219], a[104:107]\n" \
"v_lshlrev_b32_e32 v215, 4, v214\n" \
"v_add_u32_e64 v216, s5, v215\n" \
"v_mfma_f32_16x16x32_bf16 a[108:111], a[172:175], a[152:155], a[108:111]\n" \
"v_and_b32_e32 v216, 0xf0, v216\n" \
"v_cmp_gt_u32_e64 vcc, 16, v214\n" \
"v_mfma_f32_16x16x32_bf16 a[108:111], a[236:239], a[216:219], a[108:111]\n" \
"s_movk_i32 s7, 0xf800\n" \
"v_cndmask_b32_e32 v214, v215, v216, vcc\n" \
"s_waitcnt lgkmcnt(15)\n" \
"v_mfma_f32_16x16x32_bf16 a[124:127], a[172:175], a[156:159], a[124:127]\n" \
"v_or_b32_e32 v215, 64, v212\n" \
"v_mul_u32_u24_e32 v216, 0xe39, v215\n" \
"v_mfma_f32_16x16x32_bf16 a[124:127], a[236:239], a[220:223], a[124:127]\n" \
"v_lshrrev_b32_e64 v216, 16, v216\n" \
"v_mad_i32_i24 v215, v216, s6, v215\n" \
"v_mfma_f32_16x16x32_bf16 a[120:123], a[168:171], a[156:159], a[120:123]\n" \
"v_lshlrev_b32_e32 v217, 4, v215\n" \
"v_add_u32_e32 v220, s5, v217\n" \
"v_mfma_f32_16x16x32_bf16 a[120:123], a[232:235], a[220:223], a[120:123]\n" \
"v_and_b32_e32 v220, 0xf0, v220\n" \
"v_cmp_gt_u32_e64 vcc, 16, v215\n" \
"v_mfma_f32_16x16x32_bf16 a[116:119], a[164:167], a[156:159], a[116:119]\n" \
"s_nop 0\n" \
"v_cndmask_b32_e32 v215, v217, v220, vcc\n" \
"v_or_b32_e32 v217, 0x80, v212\n" \
"v_mfma_f32_16x16x32_bf16 a[116:119], a[228:231], a[220:223], a[116:119]\n" \
"v_mul_u32_u24_e32 v220, 0xe39, v217\n" \
"v_lshrrev_b32_e64 v220, 16, v220\n" \
"v_mfma_f32_16x16x32_bf16 a[112:115], a[160:163], a[156:159], a[112:115]\n" \
"v_mad_i32_i24 v217, v220, s6, v217\n" \
"v_lshlrev_b32_e64 v221, 4, v217\n" \
"v_mfma_f32_16x16x32_bf16 a[112:115], a[224:227], a[220:223], a[112:115]\n" \
"s_waitcnt vmcnt(20)\n" \
"v_readfirstlane_b32 s62, v180\n" \
"s_nop 3\n" \
"s_add_u32 s62, s62, 0x9E3779B1\n" \
"s_waitcnt vmcnt(2) lgkmcnt(0)\n" \
"s_barrier\n" \
"v_mfma_f32_16x16x32_bf16 v[52:55], a[176:179], a[128:131], v[52:55]\n" \
"s_add_u32 m0, s30, 0x8000\n" \
"s_movk_i32 s84, 0x180\n" \
"v_mfma_f32_16x16x32_bf16 v[52:55], a[240:243], a[192:195], v[52:55]\n" \
"buffer_load_dwordx4 v22, s[64:67], s84 offen offset:0 lds\n" \
"v_mfma_f32_16x16x32_bf16 v[56:59], a[180:183], a[128:131], v[56:59]\n" \
"ds_read_b64_tr_b16 a[160:161], v32 offset:0\n" \
"v_add_u32_e64 v222, s5, v221\n" \
"v_mfma_f32_16x16x32_bf16 v[56:59], a[244:247], a[192:195], v[56:59]\n" \
"s_movk_i32 s84, 0x1d80\n" \
"v_mfma_f32_16x16x32_bf16 v[60:63], a[184:187], a[128:131], v[60:63]\n" \
"buffer_load_dwordx4 v23, s[64:67], s84 offen offset:1024 lds\n" \
"v_mfma_f32_16x16x32_bf16 v[60:63], a[248:251], a[192:195], v[60:63]\n" \
"ds_read_b64_tr_b16 a[162:163], v32 offset:1024\n" \
"v_and_b32_e32 v222, 0xf0, v222\n" \
"v_mfma_f32_16x16x32_bf16 v[64:67], a[188:191], a[128:131], v[64:67]\n" \
"ds_read_b64_tr_b16 a[224:225], v32 offset:8192\n" \
"v_cmp_gt_u32_e32 vcc, 16, v217\n" \
"v_mfma_f32_16x16x32_bf16 v[64:67], a[252:255], a[192:195], v[64:67]\n" \
"ds_read_b128 a[128:131], v28 offset:16384\n" \
"v_cndmask_b32_e64 v217, v221, v222, vcc\n" \
"v_mfma_f32_16x16x32_bf16 v[80:83], a[188:191], a[132:135], v[80:83]\n" \
"ds_read_b128 a[192:195], v29 offset:16384\n" \
"v_or_b32_e32 v221, 0xc0, v212\n" \
"v_mfma_f32_16x16x32_bf16 v[80:83], a[252:255], a[196:199], v[80:83]\n" \
"s_movk_i32 s84, 0x3980\n" \
"v_mfma_f32_16x16x32_bf16 v[76:79], a[184:187], a[132:135], v[76:79]\n" \
"buffer_load_dwordx4 v22, s[64:67], s84 offen offset:2048 lds\n" \
"v_mfma_f32_16x16x32_bf16 v[76:79], a[248:251], a[196:199], v[76:79]\n" \
"s_movk_i32 s84, 0x5580\n" \
"v_mfma_f32_16x16x32_bf16 v[72:75], a[180:183], a[132:135], v[72:75]\n" \
"buffer_load_dwordx4 v23, s[64:67], s84 offen offset:3072 lds\n" \
"v_mfma_f32_16x16x32_bf16 v[72:75], a[244:247], a[196:199], v[72:75]\n" \
"s_add_u32 m0, s31, 0x8000\n" \
"s_mov_b32 s84, 0x180000\n" \
"v_mfma_f32_16x16x32_bf16 v[68:71], a[176:179], a[132:135], v[68:71]\n" \
"buffer_load_dwordx4 v26, s[68:71], s84 offen offset:0 lds\n" \
"v_mfma_f32_16x16x32_bf16 v[68:71], a[240:243], a[196:199], v[68:71]\n" \
"ds_read_b128 a[132:135], v28 offset:18432\n" \
"v_mul_u32_u24_e32 v222, 0xe39, v221\n" \
"v_mfma_f32_16x16x32_bf16 v[84:87], a[176:179], a[136:139], v[84:87]\n" \
"ds_read_b128 a[196:199], v29 offset:18432\n" \
"v_lshrrev_b32_e64 v222, 16, v222\n" \
"v_mfma_f32_16x16x32_bf16 v[84:87], a[240:243], a[200:203], v[84:87]\n" \
"s_addk_i32 s84, 0x7c00\n" \
"v_mfma_f32_16x16x32_bf16 v[88:91], a[180:183], a[136:139], v[88:91]\n" \
"buffer_load_dwordx4 v26, s[68:71], s84 offen offset:1024 lds\n" \
"v_mfma_f32_16x16x32_bf16 v[88:91], a[244:247], a[200:203], v[88:91]\n" \
"ds_read_b64_tr_b16 a[226:227], v32 offset:9216\n" \
"v_mad_i32_i24 v221, v222, s6, v221\n" \
"v_mfma_f32_16x16x32_bf16 v[92:95], a[184:187], a[136:139], v[92:95]\n" \
"s_addk_i32 s84, 0x7c00\n" \
"v_mfma_f32_16x16x32_bf16 v[92:95], a[248:251], a[200:203], v[92:95]\n" \
"buffer_load_dwordx4 v27, s[68:71], s84 offen offset:2048 lds\n" \
"v_mfma_f32_16x16x32_bf16 v[96:99], a[188:191], a[136:139], v[96:99]\n" \
"ds_read_b64_tr_b16 a[164:165], v33 offset:0\n" \
"v_lshlrev_b32_e64 v223, 4, v221\n" \
"v_mfma_f32_16x16x32_bf16 v[96:99], a[252:255], a[200:203], v[96:99]\n" \
"ds_read_b128 a[136:139], v28 offset:20480\n" \
"v_add_u32_e64 v224, s5, v223\n" \
"v_mfma_f32_16x16x32_bf16 v[112:115], a[188:191], a[140:143], v[112:115]\n" \
"ds_read_b128 a[200:203], v29 offset:20480\n" \
"v_and_b32_e32 v224, 0xf0, v224\n" \
"v_mfma_f32_16x16x32_bf16 v[112:115], a[252:255], a[204:207], v[112:115]\n" \
"s_addk_i32 s84, 0x7c00\n" \
"v_mfma_f32_16x16x32_bf16 v[108:111], a[184:187], a[140:143], v[108:111]\n" \
"buffer_load_dwordx4 v27, s[68:71], s84 offen offset:3072 lds\n" \
"v_mfma_f32_16x16x32_bf16 v[108:111], a[248:251], a[204:207], v[108:111]\n" \
"s_add_u32 m0, s30, 0x9000\n" \
"s_mov_b32 s84, 0x8180\n" \
"v_mfma_f32_16x16x32_bf16 v[104:107], a[180:183], a[140:143], v[104:107]\n" \
"buffer_load_dwordx4 v22, s[64:67], s84 offen offset:0 lds\n" \
"v_mfma_f32_16x16x32_bf16 v[104:107], a[244:247], a[204:207], v[104:107]\n" \
"ds_read_b64_tr_b16 a[166:167], v33 offset:1024\n" \
"v_cmp_gt_u32_e32 vcc, 16, v221\n" \
"v_mfma_f32_16x16x32_bf16 v[100:103], a[176:179], a[140:143], v[100:103]\n" \
"ds_read_b64_tr_b16 a[228:229], v33 offset:8192\n" \
"v_cndmask_b32_e64 v221, v223, v224, vcc\n" \
"v_mfma_f32_16x16x32_bf16 v[100:103], a[240:243], a[204:207], v[100:103]\n" \
"ds_read_b128 a[140:143], v28 offset:22528\n" \
"v_or_b32_e32 v223, 0x100, v212\n" \
"v_mfma_f32_16x16x32_bf16 v[116:119], a[176:179], a[144:147], v[116:119]\n" \
"ds_read_b128 a[204:207], v29 offset:22528\n" \
"v_mul_u32_u24_e32 v224, 0xe39, v223\n" \
"v_mfma_f32_16x16x32_bf16 v[116:119], a[240:243], a[208:211], v[116:119]\n" \
"s_addk_i32 s84, 0x1c00\n" \
"v_mfma_f32_16x16x32_bf16 v[120:123], a[180:183], a[144:147], v[120:123]\n" \
"buffer_load_dwordx4 v23, s[64:67], s84 offen offset:1024 lds\n" \
"v_mfma_f32_16x16x32_bf16 v[120:123], a[244:247], a[208:211], v[120:123]\n" \
"s_addk_i32 s84, 0x1c00\n" \
"v_mfma_f32_16x16x32_bf16 v[124:127], a[184:187], a[144:147], v[124:127]\n" \
"buffer_load_dwordx4 v22, s[64:67], s84 offen offset:2048 lds\n" \
"v_mfma_f32_16x16x32_bf16 v[124:127], a[248:251], a[208:211], v[124:127]\n" \
"s_addk_i32 s84, 0x1c00\n" \
"v_mfma_f32_16x16x32_bf16 v[128:131], a[188:191], a[144:147], v[128:131]\n" \
"buffer_load_dwordx4 v23, s[64:67], s84 offen offset:3072 lds\n" \
"v_mfma_f32_16x16x32_bf16 v[128:131], a[252:255], a[208:211], v[128:131]\n" \
"ds_read_b128 a[144:147], v28 offset:0\n" \
"v_lshrrev_b32_e32 v224, 16, v224\n" \
"v_mfma_f32_16x16x32_bf16 v[144:147], a[188:191], a[148:151], v[144:147]\n" \
"ds_read_b128 a[208:211], v29 offset:0\n" \
"v_mad_i32_i24 v223, v224, s6, v223\n" \
"v_mfma_f32_16x16x32_bf16 v[144:147], a[252:255], a[212:215], v[144:147]\n" \
"s_add_u32 m0, s31, 0xc000\n" \
"s_mov_b32 s84, 0x180000\n" \
"v_mfma_f32_16x16x32_bf16 v[140:143], a[184:187], a[148:151], v[140:143]\n" \
"buffer_load_dwordx4 v26, s[72:75], s84 offen offset:0 lds\n" \
"v_mfma_f32_16x16x32_bf16 v[140:143], a[248:251], a[212:215], v[140:143]\n" \
"ds_read_b64_tr_b16 a[230:231], v33 offset:9216\n" \
"v_lshlrev_b32_e64 v225, 4, v223\n" \
"v_mfma_f32_16x16x32_bf16 v[136:139], a[180:183], a[148:151], v[136:139]\n" \
"s_addk_i32 s84, 0x7c00\n" \
"v_mfma_f32_16x16x32_bf16 v[136:139], a[244:247], a[212:215], v[136:139]\n" \
"buffer_load_dwordx4 v26, s[72:75], s84 offen offset:1024 lds\n" \
"v_mfma_f32_16x16x32_bf16 v[132:135], a[176:179], a[148:151], v[132:135]\n" \
"ds_read_b64_tr_b16 a[168:169], v34 offset:0\n" \
"v_add_u32_e32 v226, s5, v225\n" \
"v_mfma_f32_16x16x32_bf16 v[132:135], a[240:243], a[212:215], v[132:135]\n" \
"ds_read_b128 a[148:151], v28 offset:2048\n" \
"v_and_b32_e32 v226, 0xf0, v226\n" \
"v_mfma_f32_16x16x32_bf16 v[148:151], a[176:179], a[152:155], v[148:151]\n" \
"ds_read_b128 a[212:215], v29 offset:2048\n" \
"v_cmp_gt_u32_e64 vcc, 16, v223\n" \
"v_mfma_f32_16x16x32_bf16 v[148:151], a[240:243], a[216:219], v[148:151]\n" \
"ds_read_b64_tr_b16 a[170:171], v34 offset:1024\n" \
"v_cndmask_b32_e64 v223, v225, v226, vcc\n" \
"v_mfma_f32_16x16x32_bf16 v[152:155], a[180:183], a[152:155], v[152:155]\n" \
"ds_read_b64_tr_b16 a[232:233], v34 offset:8192\n" \
"v_or_b32_e32 v225, 0x140, v212\n" \
"v_mfma_f32_16x16x32_bf16 v[152:155], a[244:247], a[216:219], v[152:155]\n" \
"ds_read_b64_tr_b16 a[234:235], v34 offset:9216\n" \
"v_mul_u32_u24_e32 v226, 0xe39, v225\n" \
"v_mfma_f32_16x16x32_bf16 v[156:159], a[184:187], a[152:155], v[156:159]\n" \
"ds_read_b64_tr_b16 a[172:173], v35 offset:0\n" \
"v_lshrrev_b32_e64 v226, 16, v226\n" \
"v_mfma_f32_16x16x32_bf16 v[156:159], a[248:251], a[216:219], v[156:159]\n" \
"ds_read_b64_tr_b16 a[174:175], v35 offset:1024\n" \
"v_mad_i32_i24 v225, v226, s6, v225\n" \
"v_mfma_f32_16x16x32_bf16 v[160:163], a[188:191], a[152:155], v[160:163]\n" \
"ds_read_b64_tr_b16 a[236:237], v35 offset:8192\n" \
"v_lshlrev_b32_e64 v227, 4, v225\n" \
"v_mfma_f32_16x16x32_bf16 v[160:163], a[252:255], a[216:219], v[160:163]\n" \
"ds_read_b128 a[152:155], v28 offset:4096\n" \
"v_add_u32_e64 v228, s5, v227\n" \
"v_mfma_f32_16x16x32_bf16 v[176:179], a[188:191], a[156:159], v[176:179]\n" \
"ds_read_b128 a[216:219], v29 offset:4096\n" \
"v_and_b32_e32 v228, 0xf0, v228\n" \
"v_mfma_f32_16x16x32_bf16 v[176:179], a[252:255], a[220:223], v[176:179]\n" \
"ds_read_b64_tr_b16 a[238:239], v35 offset:9216\n" \
"v_cmp_gt_u32_e64 vcc, 16, v225\n" \
"v_mfma_f32_16x16x32_bf16 v[172:175], a[184:187], a[156:159], v[172:175]\n" \
"s_nop 0\n" \
"v_cndmask_b32_e32 v225, v227, v228, vcc\n" \
"v_or_b32_e32 v227, 0x180, v212\n" \
"v_mfma_f32_16x16x32_bf16 v[172:175], a[248:251], a[220:223], v[172:175]\n" \
"v_mul_u32_u24_e32 v228, 0xe39, v227\n" \
"v_lshrrev_b32_e64 v228, 16, v228\n" \
"v_mfma_f32_16x16x32_bf16 v[168:171], a[180:183], a[156:159], v[168:171]\n" \
"v_mad_i32_i24 v227, v228, s6, v227\n" \
"v_lshlrev_b32_e64 v229, 4, v227\n" \
"v_mfma_f32_16x16x32_bf16 v[168:171], a[244:247], a[220:223], v[168:171]\n" \
"v_add_u32_e64 v230, s5, v229\n" \
"v_and_b32_e32 v230, 0xf0, v230\n" \
"v_mfma_f32_16x16x32_bf16 v[164:167], a[176:179], a[156:159], v[164:167]\n" \
"v_cmp_gt_u32_e32 vcc, 16, v227\n" \
"s_nop 1\n" \
"v_cndmask_b32_e64 v227, v229, v230, vcc\n" \
"v_mfma_f32_16x16x32_bf16 v[164:167], a[240:243], a[220:223], v[164:167]\n" \
"ds_read_b128 a[156:159], v28 offset:6144\n" \
"ds_read_b128 a[220:223], v29 offset:6144\n" \
"s_waitcnt lgkmcnt(15)\n" \
"v_mfma_f32_16x16x32_bf16 a[0:3], a[160:163], a[128:131], a[0:3]\n" \
"s_addk_i32 s84, 0x7c00\n" \
"v_mfma_f32_16x16x32_bf16 a[0:3], a[224:227], a[192:195], a[0:3]\n" \
"buffer_load_dwordx4 v27, s[72:75], s84 offen offset:2048 lds\n" \
"v_mfma_f32_16x16x32_bf16 a[4:7], a[164:167], a[128:131], a[4:7]\n" \
"ds_read_b64_tr_b16 a[176:177], v36 offset:0\n" \
"v_or_b32_e32 v229, 0x1c0, v212\n" \
"s_waitcnt lgkmcnt(15)\n" \
"v_mfma_f32_16x16x32_bf16 a[4:7], a[228:231], a[192:195], a[4:7]\n" \
"s_addk_i32 s84, 0x7c00\n" \
"s_waitcnt lgkmcnt(11)\n" \
"v_mfma_f32_16x16x32_bf16 a[8:11], a[168:171], a[128:131], a[8:11]\n" \
"buffer_load_dwordx4 v27, s[72:75], s84 offen offset:3072 lds\n" \
"s_waitcnt lgkmcnt(9)\n" \
"v_mfma_f32_16x16x32_bf16 a[8:11], a[232:235], a[192:195], a[8:11]\n" \
"ds_read_b64_tr_b16 a[178:179], v36 offset:1024\n" \
"v_mul_u32_u24_e32 v230, 0xe39, v229\n" \
"s_waitcnt lgkmcnt(8)\n" \
"v_mfma_f32_16x16x32_bf16 a[12:15], a[172:175], a[128:131], a[12:15]\n" \
"ds_read_b64_tr_b16 a[240:241], v36 offset:8192\n" \
"v_lshrrev_b32_e32 v230, 16, v230\n" \
"s_waitcnt lgkmcnt(5)\n" \
"v_mfma_f32_16x16x32_bf16 a[12:15], a[236:239], a[192:195], a[12:15]\n" \
"v_mad_i32_i24 v229, v230, s6, v229\n" \
"v_lshlrev_b32_e32 v231, 4, v229\n" \
"v_mfma_f32_16x16x32_bf16 a[28:31], a[172:175], a[132:135], a[28:31]\n" \
"v_add_u32_e64 v232, s5, v231\n" \
"v_and_b32_e32 v232, 0xf0, v232\n" \
"v_mfma_f32_16x16x32_bf16 a[28:31], a[236:239], a[196:199], a[28:31]\n" \
"ds_read_b64_tr_b16 a[242:243], v36 offset:9216\n" \
"v_cmp_gt_u32_e64 vcc, 16, v229\n" \
"v_mfma_f32_16x16x32_bf16 a[24:27], a[168:171], a[132:135], a[24:27]\n" \
"buffer_load_dwordx4 v[200:203], v192, s[76:79], s34 offen offset:0\n" \
"v_mfma_f32_16x16x32_bf16 a[24:27], a[232:235], a[196:199], a[24:27]\n" \
"v_cndmask_b32_e64 v229, v231, v232, vcc\n" \
"v_or_b32_e32 v231, 0x200, v212\n" \
"v_mfma_f32_16x16x32_bf16 a[20:23], a[164:167], a[132:135], a[20:23]\n" \
"v_mul_u32_u24_e32 v232, 0xe39, v231\n" \
"v_lshrrev_b32_e64 v232, 16, v232\n" \
"v_mfma_f32_16x16x32_bf16 a[20:23], a[228:231], a[196:199], a[20:23]\n" \
"ds_read_b64_tr_b16 a[180:181], v37 offset:0\n" \
"v_mad_i32_i24 v231, v232, s6, v231\n" \
"v_mfma_f32_16x16x32_bf16 a[16:19], a[160:163], a[132:135], a[16:19]\n" \
"v_lshlrev_b32_e32 v233, 4, v231\n" \
"v_add_u32_e32 v234, s5, v233\n" \
"v_mfma_f32_16x16x32_bf16 a[16:19], a[224:227], a[196:199], a[16:19]\n" \
"s_movk_i32 s5, 0x480\n" \
"v_mad_u32_u24 v0, v213, s5, v214\n" \
"v_mfma_f32_16x16x32_bf16 a[32:35], a[160:163], a[136:139], a[32:35]\n" \
"buffer_load_dwordx4 v[204:207], v193, s[76:79], s34 offen offset:0\n" \
"v_mfma_f32_16x16x32_bf16 a[32:35], a[224:227], a[200:203], a[32:35]\n" \
"s_mov_b32 m0, s32\n" \
"ds_read_b64_tr_b16 a[182:183], v37 offset:1024\n" \
"v_mfma_f32_16x16x32_bf16 a[36:39], a[164:167], a[136:139], a[36:39]\n" \
"s_mov_b32 exec_lo, 0x0\n" \
"s_mov_b32 exec_hi, 0xff00\n" \
"global_load_lds_dword v[184:185], off\n" \
"s_mov_b64 exec, -1\n" \
"v_mfma_f32_16x16x32_bf16 a[36:39], a[228:231], a[200:203], a[36:39]\n" \
"ds_read_b64_tr_b16 a[244:245], v37 offset:8192\n" \
"v_mul_u32_u24_e32 v213, 0x480, v216\n" \
"v_mfma_f32_16x16x32_bf16 a[40:43], a[168:171], a[136:139], a[40:43]\n" \
"s_movk_i32 s6, 0xfc00\n" \
"s_nop 0\n" \
"v_add3_u32 v1, v213, v215, s6\n" \
"v_mfma_f32_16x16x32_bf16 a[40:43], a[232:235], a[200:203], a[40:43]\n" \
"buffer_load_dword v208, v194, s[76:79], s34 offen offset:32\n" \
"v_mfma_f32_16x16x32_bf16 a[44:47], a[172:175], a[136:139], a[44:47]\n" \
"ds_read_b64_tr_b16 a[246:247], v37 offset:9216\n" \
"v_mul_u32_u24_e32 v213, 0x480, v220\n" \
"v_mfma_f32_16x16x32_bf16 a[44:47], a[236:239], a[200:203], a[44:47]\n" \
"v_add3_u32 v2, v213, v217, s7\n" \
"v_mul_u32_u24_e32 v213, 0x480, v222\n" \
"v_mfma_f32_16x16x32_bf16 a[60:63], a[172:175], a[140:143], a[60:63]\n" \
"v_add3_u32 v3, v213, v221, s40\n" \
"v_mul_u32_u24_e32 v213, 0x480, v226\n" \
"v_mfma_f32_16x16x32_bf16 a[60:63], a[236:239], a[204:207], a[60:63]\n" \
"v_add3_u32 v5, v213, v225, s6\n" \
"v_mul_u32_u24_e32 v213, 0x480, v228\n" \
"v_mfma_f32_16x16x32_bf16 a[56:59], a[168:171], a[140:143], a[56:59]\n" \
"buffer_load_dword v209, v194, s[76:79], s34 offen offset:1184\n" \
"v_mfma_f32_16x16x32_bf16 a[56:59], a[232:235], a[204:207], a[56:59]\n" \
"ds_read_b64_tr_b16 a[184:185], v38 offset:0\n" \
"v_add3_u32 v6, v213, v227, s7\n" \
"v_mfma_f32_16x16x32_bf16 a[52:55], a[164:167], a[140:143], a[52:55]\n" \
"v_mul_u32_u24_e32 v213, 0x480, v230\n" \
"v_add3_u32 v7, v213, v229, s40\n" \
"v_mfma_f32_16x16x32_bf16 a[52:55], a[228:231], a[204:207], a[52:55]\n" \
"ds_read_b64_tr_b16 a[186:187], v38 offset:1024\n" \
"s_lshl_b32 s7, s3, 3\n" \
"v_mfma_f32_16x16x32_bf16 a[48:51], a[160:163], a[140:143], a[48:51]\n" \
"s_lshl_b32 s40, s3, 1\n" \
"s_and_b32 s7, s7, 16\n" \
"v_mfma_f32_16x16x32_bf16 a[48:51], a[224:227], a[204:207], a[48:51]\n" \
"s_and_b32 s40, s40, 2\n" \
"s_and_b32 s6, s2, 7\n" \
"v_mfma_f32_16x16x32_bf16 a[64:67], a[160:163], a[144:147], a[64:67]\n" \
"buffer_load_dword v211, v194, s[76:79], s34 offen offset:2336\n" \
"v_mfma_f32_16x16x32_bf16 a[64:67], a[224:227], a[208:211], a[64:67]\n" \
"ds_read_b64_tr_b16 a[248:249], v38 offset:8192\n" \
"v_bfe_u32 v213, v20, 7, 1\n" \
"v_mfma_f32_16x16x32_bf16 a[68:71], a[164:167], a[144:147], a[68:71]\n" \
"s_or_b32 s7, s40, s7\n" \
"v_and_b32_e32 v234, 0xf0, v234\n" \
"v_mfma_f32_16x16x32_bf16 a[68:71], a[228:231], a[208:211], a[68:71]\n" \
"ds_read_b64_tr_b16 a[250:251], v38 offset:9216\n" \
"v_cmp_gt_u32_e64 vcc, 16, v231\n" \
"v_mfma_f32_16x16x32_bf16 a[72:75], a[168:171], a[144:147], a[72:75]\n" \
"v_or3_b32 v213, s7, v213, v219\n" \
"v_and_b32_e64 v214, 32, v20\n" \
"v_mfma_f32_16x16x32_bf16 a[72:75], a[232:235], a[208:211], a[72:75]\n" \
"s_mov_b32 s76, s20\n" \
"s_and_b32 s77, s21, 0xffff\n" \
"v_mfma_f32_16x16x32_bf16 a[76:79], a[172:175], a[144:147], a[76:79]\n" \
"s_mov_b32 s78, 0x480\n" \
"s_lshl_b32 s6, s6, 5\n" \
"v_mfma_f32_16x16x32_bf16 a[76:79], a[236:239], a[208:211], a[76:79]\n" \
"buffer_load_ushort v210, v195, s[76:79], 0 offen\n" \
"v_mfma_f32_16x16x32_bf16 a[92:95], a[172:175], a[148:151], a[92:95]\n" \
"ds_read_b64_tr_b16 a[188:189], v39 offset:0\n" \
"v_cndmask_b32_e64 v231, v233, v234, vcc\n" \
"v_mfma_f32_16x16x32_bf16 a[92:95], a[236:239], a[212:215], a[92:95]\n" \
"v_mul_u32_u24_e64 v213, 18, v213\n" \
"v_mov_b32_e32 v215, 0x100\n" \
"v_mfma_f32_16x16x32_bf16 a[88:91], a[168:171], a[148:151], a[88:91]\n" \
"s_mov_b32 s76, s8\n" \
"s_and_b32 s77, s9, 0xffff\n" \
"v_mfma_f32_16x16x32_bf16 a[88:91], a[232:235], a[212:215], a[88:91]\n" \
"s_mov_b32 s78, 0x240000\n" \
"ds_read_b64_tr_b16 a[190:191], v39 offset:1024\n" \
".fill (L_pb + 7904 - .) / 4, 4, 0xBF800000\n" \
"L_pp2:\n" \
"s_cbranch_execz L_pret\n" \
"v_mfma_f32_16x16x32_bf16 a[84:87], a[164:167], a[148:151], a[84:87]\n" \
"v_mov_b32_e32 v216, s6\n" \
"v_cmp_eq_u32_e64 vcc, 0, v214\n" \
"v_mfma_f32_16x16x32_bf16 a[84:87], a[228:231], a[212:215], a[84:87]\n" \
"ds_read_b64_tr_b16 a[252:253], v39 offset:8192\n" \
"v_and_or_b32 v213, v218, 1, v213\n" \
"v_mfma_f32_16x16x32_bf16 a[80:83], a[160:163], a[148:151], a[80:83]\n" \
"v_lshlrev_b32_e32 v212, 3, v212\n" \
"v_cndmask_b32_e32 v214, v215, v216, vcc\n" \
"v_mfma_f32_16x16x32_bf16 a[80:83], a[224:227], a[212:215], a[80:83]\n" \
"v_mad_u32_u24 v213, v213, s5, v214\n" \
"v_and_or_b32 v213, v20, 16, v213\n" \
"v_mfma_f32_16x16x32_bf16 a[96:99], a[160:163], a[152:155], a[96:99]\n" \
"ds_read_b64_tr_b16 a[254:255], v39 offset:9216\n" \
"v_add_u32_e32 v9, 0x4800, v213\n" \
"v_mfma_f32_16x16x32_bf16 a[96:99], a[224:227], a[216:219], a[96:99]\n" \
"v_mul_u32_u24_e32 v213, 0x120, v218\n" \
"v_and_or_b32 v10, v212, 24, v213\n" \
"v_mfma_f32_16x16x32_bf16 a[100:103], a[164:167], a[152:155], a[100:103]\n" \
"v_lshrrev_b32_e64 v213, 2, v20\n" \
"v_and_or_b32 v213, v213, 60, s4\n" \
"v_mfma_f32_16x16x32_bf16 a[100:103], a[228:231], a[216:219], a[100:103]\n" \
"v_lshl_or_b32 v212, s3, 9, v212\n" \
"v_mad_u32_u24 v4, v224, s5, v223\n" \
"v_mfma_f32_16x16x32_bf16 a[104:107], a[168:171], a[152:155], a[104:107]\n" \
"v_mad_u32_u24 v8, v232, s5, v231\n" \
"v_or_b32_e32 v18, 0x27000, v213\n" \
"v_mfma_f32_16x16x32_bf16 a[104:107], a[232:235], a[216:219], a[104:107]\n" \
"v_or_b32_e32 v19, 0x27800, v212\n" \
"v_mov_b32_e32 v215, 0x1000c0c\n" \
"v_mfma_f32_16x16x32_bf16 a[108:111], a[172:175], a[152:155], a[108:111]\n" \
"v_mov_b32_e32 v216, 0xc0c0100\n" \
"v_bfe_u32 v214, v20, 4, 1\n" \
"v_mfma_f32_16x16x32_bf16 a[108:111], a[236:239], a[216:219], a[108:111]\n" \
"v_lshrrev_b32_e32 v212, 8, v20\n" \
"v_cmp_eq_u32_e32 vcc, 0, v214\n" \
"s_waitcnt lgkmcnt(15)\n" \
"v_mfma_f32_16x16x32_bf16 a[124:127], a[172:175], a[156:159], a[124:127]\n" \
"v_bfe_u32 v213, v20, 4, 4\n" \
"v_mov_b32_e32 v217, 0xc0c0302\n" \
"v_mfma_f32_16x16x32_bf16 a[124:127], a[236:239], a[220:223], a[124:127]\n" \
"v_cndmask_b32_e32 v215, v215, v216, vcc\n" \
"v_mov_b32_e32 v216, 0x3020c0c\n" \
"v_mfma_f32_16x16x32_bf16 a[120:123], a[168:171], a[156:159], a[120:123]\n" \
"v_lshl_or_b32 v214, v212, 2, v214\n" \
"v_cndmask_b32_e64 v216, v216, v217, vcc\n" \
"v_mfma_f32_16x16x32_bf16 a[120:123], a[232:235], a[220:223], a[120:123]\n" \
"v_mov_b32_e32 v217, 0xc0c0c0c\n" \
"v_cmp_gt_u32_e64 vcc, v214, v213\n" \
"v_mfma_f32_16x16x32_bf16 a[116:119], a[164:167], a[156:159], a[116:119]\n" \
"v_or_b32_e32 v214, 2, v214\n" \
"s_lshr_b32 s4, s2, 5\n" \
"v_mfma_f32_16x16x32_bf16 a[116:119], a[228:231], a[220:223], a[116:119]\n" \
"v_cndmask_b32_e32 v40, v217, v215, vcc\n" \
"v_cndmask_b32_e32 v42, v215, v217, vcc\n" \
"v_mfma_f32_16x16x32_bf16 a[112:115], a[160:163], a[156:159], a[112:115]\n" \
"v_cmp_gt_u32_e32 vcc, v214, v213\n" \
"v_and_b32_e32 v214, 16, v20\n" \
"v_mfma_f32_16x16x32_bf16 a[112:115], a[224:227], a[220:223], a[112:115]\n" \
"s_waitcnt vmcnt(7) lgkmcnt(0)\n" \
"s_barrier\n" \
"v_mfma_f32_16x16x32_bf16 v[52:55], a[176:179], a[128:131], v[52:55]\n" \
"s_add_u32 m0, s30, 0x0\n" \
"s_movk_i32 s84, 0x200\n" \
"v_mfma_f32_16x16x32_bf16 v[52:55], a[240:243], a[192:195], v[52:55]\n" \
"buffer_load_dwordx4 v22, s[64:67], s84 offen offset:0 lds\n" \
"v_mfma_f32_16x16x32_bf16 v[56:59], a[180:183], a[128:131], v[56:59]\n" \
"ds_read_b64_tr_b16 a[160:161], v32 offset:32768\n" \
"s_movk_i32 s42, 0x3f80\n" \
"v_mfma_f32_16x16x32_bf16 v[56:59], a[244:247], a[192:195], v[56:59]\n" \
"s_movk_i32 s84, 0x1e00\n" \
"v_mfma_f32_16x16x32_bf16 v[60:63], a[184:187], a[128:131], v[60:63]\n" \
"buffer_load_dwordx4 v23, s[64:67], s84 offen offset:1024 lds\n" \
"v_mfma_f32_16x16x32_bf16 v[60:63], a[248:251], a[192:195], v[60:63]\n" \
"ds_read_b64_tr_b16 a[162:163], v32 offset:33792\n" \
"s_xor_b32 s5, s4, 15\n" \
"v_mfma_f32_16x16x32_bf16 v[64:67], a[188:191], a[128:131], v[64:67]\n" \
"ds_read_b64_tr_b16 a[224:225], v32 offset:40960\n" \
"v_mfma_f32_16x16x32_bf16 v[64:67], a[252:255], a[192:195], v[64:67]\n" \
"ds_read_b128 a[128:131], v28 offset:49152\n" \
"v_mfma_f32_16x16x32_bf16 v[80:83], a[188:191], a[132:135], v[80:83]\n" \
"ds_read_b128 a[192:195], v29 offset:49152\n" \
"v_mfma_f32_16x16x32_bf16 v[80:83], a[252:255], a[196:199], v[80:83]\n" \
"s_movk_i32 s84, 0x3a00\n" \
"v_mfma_f32_16x16x32_bf16 v[76:79], a[184:187], a[132:135], v[76:79]\n" \
"buffer_load_dwordx4 v22, s[64:67], s84 offen offset:2048 lds\n" \
"v_mfma_f32_16x16x32_bf16 v[76:79], a[248:251], a[196:199], v[76:79]\n" \
"s_movk_i32 s84, 0x5600\n" \
"v_mfma_f32_16x16x32_bf16 v[72:75], a[180:183], a[132:135], v[72:75]\n" \
"buffer_load_dwordx4 v23, s[64:67], s84 offen offset:3072 lds\n" \
"v_mfma_f32_16x16x32_bf16 v[72:75], a[244:247], a[196:199], v[72:75]\n" \
"s_add_u32 m0, s31, 0x0\n" \
"s_mov_b32 s84, 0x200000\n" \
"v_mfma_f32_16x16x32_bf16 v[68:71], a[176:179], a[132:135], v[68:71]\n" \
"buffer_load_dwordx4 v26, s[68:71], s84 offen offset:0 lds\n" \
"v_mfma_f32_16x16x32_bf16 v[68:71], a[240:243], a[196:199], v[68:71]\n" \
"ds_read_b128 a[132:135], v28 offset:51200\n" \
"v_mfma_f32_16x16x32_bf16 v[84:87], a[176:179], a[136:139], v[84:87]\n" \
"ds_read_b128 a[196:199], v29 offset:51200\n" \
"v_mfma_f32_16x16x32_bf16 v[84:87], a[240:243], a[200:203], v[84:87]\n" \
"s_addk_i32 s84, 0x7c00\n" \
"v_mfma_f32_16x16x32_bf16 v[88:91], a[180:183], a[136:139], v[88:91]\n" \
"buffer_load_dwordx4 v26, s[68:71], s84 offen offset:1024 lds\n" \
"v_mfma_f32_16x16x32_bf16 v[88:91], a[244:247], a[200:203], v[88:91]\n" \
"ds_read_b64_tr_b16 a[226:227], v32 offset:41984\n" \
"v_mfma_f32_16x16x32_bf16 v[92:95], a[184:187], a[136:139], v[92:95]\n" \
"s_addk_i32 s84, 0x7c00\n" \
"v_mfma_f32_16x16x32_bf16 v[92:95], a[248:251], a[200:203], v[92:95]\n" \
"buffer_load_dwordx4 v27, s[68:71], s84 offen offset:2048 lds\n" \
"v_mfma_f32_16x16x32_bf16 v[96:99], a[188:191], a[136:139], v[96:99]\n" \
"ds_read_b64_tr_b16 a[164:165], v33 offset:32768\n" \
"v_mfma_f32_16x16x32_bf16 v[96:99], a[252:255], a[200:203], v[96:99]\n" \
"ds_read_b128 a[136:139], v28 offset:53248\n" \
"v_mfma_f32_16x16x32_bf16 v[112:115], a[188:191], a[140:143], v[112:115]\n" \
"ds_read_b128 a[200:203], v29 offset:53248\n" \
"v_mfma_f32_16x16x32_bf16 v[112:115], a[252:255], a[204:207], v[112:115]\n" \
"s_addk_i32 s84, 0x7c00\n" \
"s_nop 0\n" \
"v_mfma_f32_16x16x32_bf16 v[108:111], a[184:187], a[140:143], v[108:111]\n" \
"buffer_load_dwordx4 v27, s[68:71], s84 offen offset:3072 lds\n" \
"v_mfma_f32_16x16x32_bf16 v[108:111], a[248:251], a[204:207], v[108:111]\n" \
"s_add_u32 m0, s30, 0x1000\n" \
"s_mov_b32 s84, 0x8200\n" \
"v_mfma_f32_16x16x32_bf16 v[104:107], a[180:183], a[140:143], v[104:107]\n" \
"buffer_load_dwordx4 v22, s[64:67], s84 offen offset:0 lds\n" \
"v_mfma_f32_16x16x32_bf16 v[104:107], a[244:247], a[204:207], v[104:107]\n" \
"ds_read_b64_tr_b16 a[166:167], v33 offset:33792\n" \
"v_mfma_f32_16x16x32_bf16 v[100:103], a[176:179], a[140:143], v[100:103]\n" \
"ds_read_b64_tr_b16 a[228:229], v33 offset:40960\n" \
"v_mfma_f32_16x16x32_bf16 v[100:103], a[240:243], a[204:207], v[100:103]\n" \
"ds_read_b128 a[140:143], v28 offset:55296\n" \
"v_mfma_f32_16x16x32_bf16 v[116:119], a[176:179], a[144:147], v[116:119]\n" \
"ds_read_b128 a[204:207], v29 offset:55296\n" \
"v_mfma_f32_16x16x32_bf16 v[116:119], a[240:243], a[208:211], v[116:119]\n" \
"s_addk_i32 s84, 0x1c00\n" \
"v_mfma_f32_16x16x32_bf16 v[120:123], a[180:183], a[144:147], v[120:123]\n" \
"buffer_load_dwordx4 v23, s[64:67], s84 offen offset:1024 lds\n" \
"v_mfma_f32_16x16x32_bf16 v[120:123], a[244:247], a[208:211], v[120:123]\n" \
"s_addk_i32 s84, 0x1c00\n" \
"v_mfma_f32_16x16x32_bf16 v[124:127], a[184:187], a[144:147], v[124:127]\n" \
"buffer_load_dwordx4 v22, s[64:67], s84 offen offset:2048 lds\n" \
"v_mfma_f32_16x16x32_bf16 v[124:127], a[248:251], a[208:211], v[124:127]\n" \
"s_addk_i32 s84, 0x1c00\n" \
"s_nop 0\n" \
"v_mfma_f32_16x16x32_bf16 v[128:131], a[188:191], a[144:147], v[128:131]\n" \
"buffer_load_dwordx4 v23, s[64:67], s84 offen offset:3072 lds\n" \
"v_mfma_f32_16x16x32_bf16 v[128:131], a[252:255], a[208:211], v[128:131]\n" \
"ds_read_b128 a[144:147], v28 offset:32768\n" \
"v_mfma_f32_16x16x32_bf16 v[144:147], a[188:191], a[148:151], v[144:147]\n" \
"ds_read_b128 a[208:211], v29 offset:32768\n" \
"v_mfma_f32_16x16x32_bf16 v[144:147], a[252:255], a[212:215], v[144:147]\n" \
"s_add_u32 m0, s31, 0x4000\n" \
"s_mov_b32 s84, 0x200000\n" \
"v_mfma_f32_16x16x32_bf16 v[140:143], a[184:187], a[148:151], v[140:143]\n" \
"buffer_load_dwordx4 v26, s[72:75], s84 offen offset:0 lds\n" \
"v_mfma_f32_16x16x32_bf16 v[140:143], a[248:251], a[212:215], v[140:143]\n" \
"ds_read_b64_tr_b16 a[230:231], v33 offset:41984\n" \
"v_mfma_f32_16x16x32_bf16 v[136:139], a[180:183], a[148:151], v[136:139]\n" \
"s_addk_i32 s84, 0x7c00\n" \
"s_nop 0\n" \
"v_mfma_f32_16x16x32_bf16 v[136:139], a[244:247], a[212:215], v[136:139]\n" \
"buffer_load_dwordx4 v26, s[72:75], s84 offen offset:1024 lds\n" \
"v_mfma_f32_16x16x32_bf16 v[132:135], a[176:179], a[148:151], v[132:135]\n" \
"ds_read_b64_tr_b16 a[168:169], v34 offset:32768\n" \
"v_mfma_f32_16x16x32_bf16 v[132:135], a[240:243], a[212:215], v[132:135]\n" \
"ds_read_b128 a[148:151], v28 offset:34816\n" \
"v_mfma_f32_16x16x32_bf16 v[148:151], a[176:179], a[152:155], v[148:151]\n" \
"ds_read_b128 a[212:215], v29 offset:34816\n" \
"v_mfma_f32_16x16x32_bf16 v[148:151], a[240:243], a[216:219], v[148:151]\n" \
"ds_read_b64_tr_b16 a[170:171], v34 offset:33792\n" \
"v_mfma_f32_16x16x32_bf16 v[152:155], a[180:183], a[152:155], v[152:155]\n" \
"ds_read_b64_tr_b16 a[232:233], v34 offset:40960\n" \
"v_mfma_f32_16x16x32_bf16 v[152:155], a[244:247], a[216:219], v[152:155]\n" \
"ds_read_b64_tr_b16 a[234:235], v34 offset:41984\n" \
"v_mfma_f32_16x16x32_bf16 v[156:159], a[184:187], a[152:155], v[156:159]\n" \
"ds_read_b64_tr_b16 a[172:173], v35 offset:32768\n" \
"v_mfma_f32_16x16x32_bf16 v[156:159], a[248:251], a[216:219], v[156:159]\n" \
"ds_read_b64_tr_b16 a[174:175], v35 offset:33792\n" \
"v_mfma_f32_16x16x32_bf16 v[160:163], a[188:191], a[152:155], v[160:163]\n" \
"ds_read_b64_tr_b16 a[236:237], v35 offset:40960\n" \
"v_mfma_f32_16x16x32_bf16 v[160:163], a[252:255], a[216:219], v[160:163]\n" \
"ds_read_b128 a[152:155], v28 offset:36864\n" \
"v_mfma_f32_16x16x32_bf16 v[176:179], a[188:191], a[156:159], v[176:179]\n" \
"ds_read_b128 a[216:219], v29 offset:36864\n" \
"v_mfma_f32_16x16x32_bf16 v[176:179], a[252:255], a[220:223], v[176:179]\n" \
"ds_read_b64_tr_b16 a[238:239], v35 offset:41984\n" \
"v_mfma_f32_16x16x32_bf16 v[172:175], a[184:187], a[156:159], v[172:175]\n" \
"s_cmpk_lt_u32 s3, 2\n" \
"s_cselect_b32 s6, s5, s4\n" \
"v_mfma_f32_16x16x32_bf16 v[172:175], a[248:251], a[220:223], v[172:175]\n" \
"s_lshl_b32 s7, s6, 7\n" \
"v_and_b32_e32 v213, 0x320, v20\n" \
"v_mfma_f32_16x16x32_bf16 v[168:171], a[180:183], a[156:159], v[168:171]\n" \
"s_movk_i32 s41, 0x100\n" \
"v_lshlrev_b32_e64 v214, v214, s42\n" \
"v_mfma_f32_16x16x32_bf16 v[168:171], a[244:247], a[220:223], v[168:171]\n" \
"v_mov_b32_e32 v215, 0\n" \
"s_add_i32 s40, s7, -16\n" \
"v_mfma_f32_16x16x32_bf16 v[164:167], a[176:179], a[156:159], v[164:167]\n" \
"v_cndmask_b32_e32 v41, v217, v216, vcc\n" \
"v_cndmask_b32_e32 v43, v216, v217, vcc\n" \
"v_mfma_f32_16x16x32_bf16 v[164:167], a[240:243], a[220:223], v[164:167]\n" \
"ds_read_b128 a[156:159], v28 offset:38912\n" \
"ds_read_b128 a[220:223], v29 offset:38912\n" \
"s_waitcnt lgkmcnt(15)\n" \
"v_mfma_f32_16x16x32_bf16 a[0:3], a[160:163], a[128:131], a[0:3]\n" \
"s_addk_i32 s84, 0x7c00\n" \
"v_mfma_f32_16x16x32_bf16 a[0:3], a[224:227], a[192:195], a[0:3]\n" \
"buffer_load_dwordx4 v27, s[72:75], s84 offen offset:2048 lds\n" \
"v_mfma_f32_16x16x32_bf16 a[4:7], a[164:167], a[128:131], a[4:7]\n" \
"ds_read_b64_tr_b16 a[176:177], v36 offset:32768\n" \
"v_cmp_eq_u32_e32 vcc, s41, v213\n" \
"s_waitcnt lgkmcnt(15)\n" \
"v_mfma_f32_16x16x32_bf16 a[4:7], a[228:231], a[192:195], a[4:7]\n" \
"s_addk_i32 s84, 0x7c00\n" \
"s_waitcnt lgkmcnt(11)\n" \
"v_mfma_f32_16x16x32_bf16 a[8:11], a[168:171], a[128:131], a[8:11]\n" \
"buffer_load_dwordx4 v27, s[72:75], s84 offen offset:3072 lds\n" \
"s_waitcnt lgkmcnt(9)\n" \
"v_mfma_f32_16x16x32_bf16 a[8:11], a[232:235], a[192:195], a[8:11]\n" \
"ds_read_b64_tr_b16 a[178:179], v36 offset:33792\n" \
"s_mov_b32 s41, 0\n" \
"s_waitcnt lgkmcnt(8)\n" \
"v_mfma_f32_16x16x32_bf16 a[12:15], a[172:175], a[128:131], a[12:15]\n" \
"ds_read_b64_tr_b16 a[240:241], v36 offset:40960\n" \
"s_and_b32 s43, s3, 1\n" \
"s_waitcnt lgkmcnt(5)\n" \
"v_mfma_f32_16x16x32_bf16 a[12:15], a[236:239], a[192:195], a[12:15]\n" \
"v_cndmask_b32_e32 v46, v215, v214, vcc\n" \
"v_and_b32_e32 v213, 0x330, v20\n" \
"v_mfma_f32_16x16x32_bf16 a[28:31], a[172:175], a[132:135], a[28:31]\n" \
"s_cmp_lg_u32 s7, 0\n" \
"s_cselect_b32 s40, s40, s41\n" \
"v_mfma_f32_16x16x32_bf16 a[28:31], a[236:239], a[196:199], a[28:31]\n" \
"ds_read_b64_tr_b16 a[242:243], v36 offset:41984\n" \
"s_mov_b32 s42, 0xff800000\n" \
"v_mfma_f32_16x16x32_bf16 a[24:27], a[168:171], a[132:135], a[24:27]\n" \
"s_cmp_lg_u32 s7, 0\n" \
"s_cselect_b32 s47, s41, s42\n" \
"v_mfma_f32_16x16x32_bf16 a[24:27], a[232:235], a[196:199], a[24:27]\n" \
"s_lshl_b32 s41, s43, 6\n" \
"v_lshrrev_b32_e32 v215, 6, v20\n" \
"v_mfma_f32_16x16x32_bf16 a[20:23], a[164:167], a[132:135], a[20:23]\n" \
"v_mov_b32_e32 v214, 0x3f80\n" \
"v_cmp_eq_u32_e64 vcc, 32, v213\n" \
"v_mfma_f32_16x16x32_bf16 a[20:23], a[228:231], a[196:199], a[20:23]\n" \
"ds_read_b64_tr_b16 a[180:181], v37 offset:32768\n" \
"s_or_b32 s7, s41, s7\n" \
"v_mfma_f32_16x16x32_bf16 a[16:19], a[160:163], a[132:135], a[16:19]\n" \
"v_lshrrev_b32_e32 v213, 7, v20\n" \
"v_bfe_u32 v216, v20, 6, 2\n" \
"v_mfma_f32_16x16x32_bf16 a[16:19], a[224:227], a[196:199], a[16:19]\n" \
"v_lshrrev_b32_e64 v219, 1, v20\n" \
"v_bfe_u32 v215, v215, 1, 1\n" \
"v_mfma_f32_16x16x32_bf16 a[32:35], a[160:163], a[136:139], a[32:35]\n" \
"s_waitcnt vmcnt(16)\n" \
"v_or_b32_e32 v204, v210, v204\n" \
"v_or_b32_e64 v206, v210, v206\n" \
"v_mfma_f32_16x16x32_bf16 a[32:35], a[224:227], a[200:203], a[32:35]\n" \
"v_mov_b32_e64 v210, 0\n" \
"s_add_u32 s88, s35, 0x1000\n" \
"v_mfma_f32_16x16x32_bf16 a[36:39], a[164:167], a[136:139], a[36:39]\n" \
"ds_read_b64_tr_b16 a[182:183], v37 offset:33792\n" \
"v_cndmask_b32_e64 v47, 0, v214, vcc\n" \
"v_mfma_f32_16x16x32_bf16 a[36:39], a[228:231], a[200:203], a[36:39]\n" \
"ds_read_b64_tr_b16 a[244:245], v37 offset:40960\n" \
"v_or_b32_e64 v214, s7, v213\n" \
"v_mfma_f32_16x16x32_bf16 a[40:43], a[168:171], a[136:139], a[40:43]\n" \
"s_mov_b32 m0, s32\n" \
"v_lshl_or_b32 v217, s43, 3, v213\n" \
"v_mfma_f32_16x16x32_bf16 a[40:43], a[232:235], a[200:203], a[40:43]\n" \
"s_mov_b32 exec_lo, 0xffffffff\n" \
"s_mov_b32 exec_hi, 0x0\n" \
"global_load_lds_dword v[188:189], off\n" \
"s_mov_b64 exec, -1\n" \
"v_mfma_f32_16x16x32_bf16 a[44:47], a[172:175], a[136:139], a[44:47]\n" \
"ds_read_b64_tr_b16 a[246:247], v37 offset:41984\n" \
"v_lshlrev_b32_e64 v218, 10, v212\n" \
"v_mfma_f32_16x16x32_bf16 a[44:47], a[236:239], a[200:203], a[44:47]\n" \
"v_lshlrev_b32_e32 v216, 7, v216\n" \
"v_and_b32_e32 v219, 24, v219\n" \
"v_mfma_f32_16x16x32_bf16 a[60:63], a[172:175], a[140:143], a[60:63]\n" \
"ds_write_b128 v196, v[200:203]\n" \
"v_mfma_f32_16x16x32_bf16 a[60:63], a[236:239], a[204:207], a[60:63]\n" \
"v_and_or_b32 v213, v213, 2, v215\n" \
"v_add_u32_e64 v217, s40, v217\n" \
"v_mfma_f32_16x16x32_bf16 a[56:59], a[168:171], a[140:143], a[56:59]\n" \
"ds_read_b64_tr_b16 a[184:185], v38 offset:32768\n" \
"v_or3_b32 v216, v216, v218, v219\n" \
"v_mfma_f32_16x16x32_bf16 a[56:59], a[232:235], a[204:207], a[56:59]\n" \
"v_lshlrev_b32_e64 v213, 5, v213\n" \
"s_mov_b32 s40, 0x14000\n" \
"v_mfma_f32_16x16x32_bf16 a[52:55], a[164:167], a[140:143], a[52:55]\n" \
"s_lshl_b32 s7, s2, 2\n" \
"v_or3_b32 v48, v216, v213, s40\n" \
"v_mfma_f32_16x16x32_bf16 a[52:55], a[228:231], a[204:207], a[52:55]\n" \
"ds_read_b64_tr_b16 a[186:187], v38 offset:33792\n" \
"s_lshl_b32 s40, s43, 2\n" \
"v_mfma_f32_16x16x32_bf16 a[48:51], a[160:163], a[140:143], a[48:51]\n" \
"s_lshl_b32 s6, s6, 3\n" \
"v_or_b32_e32 v213, s40, v212\n" \
"v_mfma_f32_16x16x32_bf16 a[48:51], a[224:227], a[204:207], a[48:51]\n" \
"s_or_b32 s6, s6, s40\n" \
"s_and_b32 s40, s7, 0x7c\n" \
"v_mfma_f32_16x16x32_bf16 a[64:67], a[160:163], a[144:147], a[64:67]\n" \
"ds_write_b128 v197, v[204:207]\n" \
"v_mfma_f32_16x16x32_bf16 a[64:67], a[224:227], a[208:211], a[64:67]\n" \
"ds_read_b64_tr_b16 a[248:249], v38 offset:40960\n" \
"s_or_b32 s40, s40, s3\n" \
"v_mfma_f32_16x16x32_bf16 a[68:71], a[164:167], a[144:147], a[68:71]\n" \
"s_cmpk_lt_u32 s40, 64\n" \
"s_cselect_b32 s41, s4, s5\n" \
"v_mfma_f32_16x16x32_bf16 a[68:71], a[228:231], a[208:211], a[68:71]\n" \
"ds_read_b64_tr_b16 a[250:251], v38 offset:41984\n" \
"s_lshl_b32 s40, s40, 1\n" \
"v_mfma_f32_16x16x32_bf16 a[72:75], a[168:171], a[144:147], a[72:75]\n" \
"s_lshl_b32 s41, s41, 7\n" \
"s_and_b32 s40, s40, 0x7e\n" \
"v_mfma_f32_16x16x32_bf16 a[72:75], a[232:235], a[208:211], a[72:75]\n" \
"s_or_b32 s40, s41, s40\n" \
"s_mulk_i32 s40, 0x480\n" \
"v_mfma_f32_16x16x32_bf16 a[76:79], a[172:175], a[144:147], a[76:79]\n" \
"v_add_u32_e64 v49, s40, v20\n" \
"s_mov_b32 s40, 0xc000\n" \
"v_mfma_f32_16x16x32_bf16 a[76:79], a[236:239], a[208:211], a[76:79]\n" \
"ds_read_b64_tr_b16 a[188:189], v39 offset:32768\n" \
"s_and_b32 s7, s7, 28\n" \
"v_mfma_f32_16x16x32_bf16 a[92:95], a[172:175], a[148:151], a[92:95]\n" \
"ds_write_b128 v198, v[208:211]\n" \
"v_mfma_f32_16x16x32_bf16 a[92:95], a[236:239], a[212:215], a[92:95]\n" \
"s_bfe_u32 s45, s2, 0x20003\n" \
"s_mov_b32 s41, 0x8000\n" \
"v_mfma_f32_16x16x32_bf16 a[88:91], a[168:171], a[148:151], a[88:91]\n" \
"ds_read_b64_tr_b16 a[190:191], v39 offset:33792\n" \
"v_mfma_f32_16x16x32_bf16 a[88:91], a[232:235], a[212:215], a[88:91]\n" \
"s_cmp_lg_u32 s4, 0\n" \
"s_cselect_b32 s51, s40, s41\n" \
"v_mfma_f32_16x16x32_bf16 a[84:87], a[164:167], a[148:151], a[84:87]\n" \
"s_movk_i32 s40, 0x4000\n" \
"s_lshr_b32 s44, s3, 1\n" \
"v_mfma_f32_16x16x32_bf16 a[84:87], a[228:231], a[212:215], a[84:87]\n" \
"ds_read_b64_tr_b16 a[252:253], v39 offset:40960\n" \
"s_or_b32 s46, s7, s45\n" \
"v_mfma_f32_16x16x32_bf16 a[80:83], a[160:163], a[148:151], a[80:83]\n" \
"s_cmp_lg_u32 s4, 0\n" \
"s_cselect_b32 s53, s41, s40\n" \
"v_mfma_f32_16x16x32_bf16 a[80:83], a[224:227], a[212:215], a[80:83]\n" \
"s_lshl_b32 s40, s44, 15\n" \
"s_lshl_b32 s41, s43, 14\n" \
"v_mfma_f32_16x16x32_bf16 a[96:99], a[160:163], a[152:155], a[96:99]\n" \
"ds_read_b64_tr_b16 a[254:255], v39 offset:41984\n" \
"s_lshl_b32 s48, s46, 20\n" \
"v_mfma_f32_16x16x32_bf16 a[96:99], a[224:227], a[216:219], a[96:99]\n" \
"s_or_b32 s41, s40, s41\n" \
"s_add_i32 s49, s48, 0x1000000\n" \
"v_mfma_f32_16x16x32_bf16 a[100:103], a[164:167], a[152:155], a[100:103]\n" \
"s_or_b32 s42, s49, s41\n" \
"s_lshl_b32 s4, s4, 16\n" \
"v_mfma_f32_16x16x32_bf16 a[100:103], a[228:231], a[216:219], a[100:103]\n" \
"s_or_b32 s41, s42, s4\n" \
"s_lshl_b32 s4, s5, 16\n" \
"v_mfma_f32_16x16x32_bf16 a[104:107], a[168:171], a[152:155], a[104:107]\n" \
"s_mul_i32 s5, s6, 0x24800\n" \
"s_lshl_b32 s6, s46, 9\n" \
"v_mfma_f32_16x16x32_bf16 a[104:107], a[232:235], a[216:219], a[104:107]\n" \
"s_lshl_b32 s7, s7, 6\n" \
"s_add_i32 s6, s5, s6\n" \
"v_mfma_f32_16x16x32_bf16 a[108:111], a[172:175], a[152:155], a[108:111]\n" \
"s_or_b32 s5, s5, s7\n" \
"s_lshl_b32 s7, s45, 2\n" \
"v_mfma_f32_16x16x32_bf16 a[108:111], a[236:239], a[216:219], a[108:111]\n" \
"s_or_b32 s5, s5, s7\n" \
"s_lshl_b32 s7, s3, 12\n" \
"s_waitcnt lgkmcnt(15)\n" \
"v_mfma_f32_16x16x32_bf16 a[124:127], a[172:175], a[156:159], a[124:127]\n" \
"s_or_b32 s50, s49, s7\n" \
"s_lshl_b32 s7, s3, 11\n" \
"v_mfma_f32_16x16x32_bf16 a[124:127], a[236:239], a[220:223], a[124:127]\n" \
"v_lshrrev_b32_e64 v215, 4, v20\n" \
"v_bfe_u32 v216, v20, 4, 3\n" \
"v_mfma_f32_16x16x32_bf16 a[120:123], a[168:171], a[156:159], a[120:123]\n" \
"s_or_b32 s42, s42, s4\n" \
"s_lshl_b32 s4, s43, 11\n" \
"v_mfma_f32_16x16x32_bf16 a[120:123], a[232:235], a[220:223], a[120:123]\n" \
"s_add_i32 s52, s48, s7\n" \
"s_add_i32 s53, s53, s7\n" \
"v_mfma_f32_16x16x32_bf16 a[116:119], a[164:167], a[156:159], a[116:119]\n" \
"v_mul_u32_u24_e32 v214, 0x480, v214\n" \
"v_bitop3_b32 v218, v212, v215, 7 bitop3:0x78\n" \
"v_mfma_f32_16x16x32_bf16 a[116:119], a[228:231], a[220:223], a[116:119]\n" \
"v_bitop3_b32 v212, v212, v216, 4 bitop3:0x36\n" \
"s_lshl_b32 s7, s3, 10\n" \
"v_mfma_f32_16x16x32_bf16 a[112:115], a[160:163], a[156:159], a[112:115]\n" \
"s_or_b32 s4, s40, s4\n" \
"v_lshl_or_b32 v25, v212, 4, v214\n" \
"v_mfma_f32_16x16x32_bf16 a[112:115], a[224:227], a[220:223], a[112:115]\n" \
"s_waitcnt vmcnt(1) lgkmcnt(0)\n" \
"s_barrier\n" \
"v_mfma_f32_16x16x32_bf16 v[52:55], a[176:179], a[128:131], v[52:55]\n" \
"s_add_u32 m0, s30, 0x8000\n" \
"s_movk_i32 s84, 0x280\n" \
"v_mfma_f32_16x16x32_bf16 v[52:55], a[240:243], a[192:195], v[52:55]\n" \
"buffer_load_dwordx4 v22, s[64:67], s84 offen offset:0 lds\n" \
"v_mfma_f32_16x16x32_bf16 v[56:59], a[180:183], a[128:131], v[56:59]\n" \
"s_movk_i32 s84, 0x1e80\n" \
"v_mfma_f32_16x16x32_bf16 v[56:59], a[244:247], a[192:195], v[56:59]\n" \
"buffer_load_dwordx4 v23, s[64:67], s84 offen offset:1024 lds\n" \
"v_mfma_f32_16x16x32_bf16 v[60:63], a[184:187], a[128:131], v[60:63]\n" \
"ds_read_b64_tr_b16 a[160:161], v32 offset:0\n" \
"v_mul_u32_u24_e32 v212, 0x480, v217\n" \
"v_mfma_f32_16x16x32_bf16 v[60:63], a[248:251], a[192:195], v[60:63]\n" \
"s_movk_i32 s84, 0x3a80\n" \
"v_mfma_f32_16x16x32_bf16 v[64:67], a[188:191], a[128:131], v[64:67]\n" \
"buffer_load_dwordx4 v22, s[64:67], s84 offen offset:2048 lds\n" \
"v_mfma_f32_16x16x32_bf16 v[64:67], a[252:255], a[192:195], v[64:67]\n" \
"ds_read_b128 a[128:131], v28 offset:16384\n" \
"v_bitop3_b32 v213, v215, v213, 7 bitop3:0x6c\n" \
"v_mfma_f32_16x16x32_bf16 v[80:83], a[188:191], a[132:135], v[80:83]\n" \
"ds_read_b128 a[192:195], v29 offset:16384\n" \
"s_or_b32 s40, s7, 0x14000\n" \
"v_mfma_f32_16x16x32_bf16 v[80:83], a[252:255], a[196:199], v[80:83]\n" \
"s_movk_i32 s84, 0x5680\n" \
"v_mfma_f32_16x16x32_bf16 v[76:79], a[184:187], a[132:135], v[76:79]\n" \
"buffer_load_dwordx4 v23, s[64:67], s84 offen offset:3072 lds\n" \
"v_mfma_f32_16x16x32_bf16 v[76:79], a[248:251], a[196:199], v[76:79]\n" \
"s_add_u32 m0, s31, 0x8000\n" \
"s_mov_b32 s84, 0x280000\n" \
"v_mfma_f32_16x16x32_bf16 v[72:75], a[180:183], a[132:135], v[72:75]\n" \
"buffer_load_dwordx4 v26, s[68:71], s84 offen offset:0 lds\n" \
"v_mfma_f32_16x16x32_bf16 v[72:75], a[244:247], a[196:199], v[72:75]\n" \
"ds_read_b64_tr_b16 a[162:163], v32 offset:1024\n" \
"s_lshl_b32 s43, s44, 13\n" \
"v_mfma_f32_16x16x32_bf16 v[68:71], a[176:179], a[132:135], v[68:71]\n" \
"ds_read_b64_tr_b16 a[224:225], v32 offset:8192\n" \
"s_mulk_i32 s44, 0xe800\n" \
"v_mfma_f32_16x16x32_bf16 v[68:71], a[240:243], a[196:199], v[68:71]\n" \
"ds_read_b128 a[132:135], v28 offset:18432\n" \
"s_lshl_b32 s7, s3, 8\n" \
"v_mfma_f32_16x16x32_bf16 v[84:87], a[176:179], a[136:139], v[84:87]\n" \
"ds_read_b128 a[196:199], v29 offset:18432\n" \
"v_lshl_or_b32 v24, v218, 4, v214\n" \
"v_mfma_f32_16x16x32_bf16 v[84:87], a[240:243], a[200:203], v[84:87]\n" \
"s_addk_i32 s84, 0x7c00\n" \
"v_mfma_f32_16x16x32_bf16 v[88:91], a[180:183], a[136:139], v[88:91]\n" \
"buffer_load_dwordx4 v26, s[68:71], s84 offen offset:1024 lds\n" \
"v_mfma_f32_16x16x32_bf16 v[88:91], a[244:247], a[200:203], v[88:91]\n" \
"s_addk_i32 s84, 0x7c00\n" \
"v_mfma_f32_16x16x32_bf16 v[92:95], a[184:187], a[136:139], v[92:95]\n" \
"buffer_load_dwordx4 v27, s[68:71], s84 offen offset:2048 lds\n" \
"v_mfma_f32_16x16x32_bf16 v[92:95], a[248:251], a[200:203], v[92:95]\n" \
"s_addk_i32 s84, 0x7c00\n" \
"v_mfma_f32_16x16x32_bf16 v[96:99], a[188:191], a[136:139], v[96:99]\n" \
"buffer_load_dwordx4 v27, s[68:71], s84 offen offset:3072 lds\n" \
"v_mfma_f32_16x16x32_bf16 v[96:99], a[252:255], a[200:203], v[96:99]\n" \
"ds_read_b128 a[136:139], v28 offset:20480\n" \
"v_lshl_or_b32 v21, v213, 4, v212\n" \
"v_mfma_f32_16x16x32_bf16 v[112:115], a[188:191], a[140:143], v[112:115]\n" \
"ds_read_b128 a[200:203], v29 offset:20480\n" \
"s_add_i32 s44, s44, 0x17000\n" \
"v_mfma_f32_16x16x32_bf16 v[112:115], a[252:255], a[204:207], v[112:115]\n" \
"s_add_u32 m0, s30, 0x9000\n" \
"s_mov_b32 s84, 0x8280\n" \
"v_mfma_f32_16x16x32_bf16 v[108:111], a[184:187], a[140:143], v[108:111]\n" \
"buffer_load_dwordx4 v22, s[64:67], s84 offen offset:0 lds\n" \
"v_mfma_f32_16x16x32_bf16 v[108:111], a[248:251], a[204:207], v[108:111]\n" \
"s_addk_i32 s84, 0x1c00\n" \
"v_mfma_f32_16x16x32_bf16 v[104:107], a[180:183], a[140:143], v[104:107]\n" \
"buffer_load_dwordx4 v23, s[64:67], s84 offen offset:1024 lds\n" \
"v_mfma_f32_16x16x32_bf16 v[104:107], a[244:247], a[204:207], v[104:107]\n" \
"s_addk_i32 s84, 0x1c00\n" \
"v_mfma_f32_16x16x32_bf16 v[100:103], a[176:179], a[140:143], v[100:103]\n" \
"buffer_load_dwordx4 v22, s[64:67], s84 offen offset:2048 lds\n" \
"v_mfma_f32_16x16x32_bf16 v[100:103], a[240:243], a[204:207], v[100:103]\n" \
"ds_read_b128 a[140:143], v28 offset:22528\n" \
"s_or_b32 s45, s7, 0x27000\n" \
"v_mfma_f32_16x16x32_bf16 v[116:119], a[176:179], a[144:147], v[116:119]\n" \
"ds_read_b128 a[204:207], v29 offset:22528\n" \
"s_or_b32 s46, s4, 0x10000\n" \
"v_mfma_f32_16x16x32_bf16 v[116:119], a[240:243], a[208:211], v[116:119]\n" \
"s_addk_i32 s84, 0x1c00\n" \
"v_mfma_f32_16x16x32_bf16 v[120:123], a[180:183], a[144:147], v[120:123]\n" \
"buffer_load_dwordx4 v23, s[64:67], s84 offen offset:3072 lds\n" \
"v_mfma_f32_16x16x32_bf16 v[120:123], a[244:247], a[208:211], v[120:123]\n" \
"s_add_u32 m0, s31, 0xc000\n" \
"s_mov_b32 s84, 0x280000\n" \
"v_mfma_f32_16x16x32_bf16 v[124:127], a[184:187], a[144:147], v[124:127]\n" \
"buffer_load_dwordx4 v26, s[72:75], s84 offen offset:0 lds\n" \
"v_mfma_f32_16x16x32_bf16 v[124:127], a[248:251], a[208:211], v[124:127]\n" \
"s_addk_i32 s84, 0x7c00\n" \
"v_mfma_f32_16x16x32_bf16 v[128:131], a[188:191], a[144:147], v[128:131]\n" \
"buffer_load_dwordx4 v26, s[72:75], s84 offen offset:1024 lds\n" \
"v_mfma_f32_16x16x32_bf16 v[128:131], a[252:255], a[208:211], v[128:131]\n" \
"ds_read_b128 a[144:147], v28 offset:0\n" \
"s_add_i32 s48, s6, 0x3000000\n" \
"v_mfma_f32_16x16x32_bf16 v[144:147], a[188:191], a[148:151], v[144:147]\n" \
"ds_read_b128 a[208:211], v29 offset:0\n" \
"s_add_i32 s49, s5, 0x3004000\n" \
"v_mfma_f32_16x16x32_bf16 v[144:147], a[252:255], a[212:215], v[144:147]\n" \
"ds_read_b128 v[200:203], v199 offset:0\n" \
"s_add_i32 s52, s52, 0x1010000\n" \
"v_mfma_f32_16x16x32_bf16 v[140:143], a[184:187], a[148:151], v[140:143]\n" \
"ds_read_b128 v[204:207], v199 offset:4096\n" \
"v_mfma_f32_16x16x32_bf16 v[140:143], a[248:251], a[212:215], v[140:143]\n" \
"ds_read_b64_tr_b16 a[226:227], v32 offset:9216\n" \
"v_mfma_f32_16x16x32_bf16 v[136:139], a[180:183], a[148:151], v[136:139]\n" \
"ds_read_b64_tr_b16 a[164:165], v33 offset:0\n" \
"v_mfma_f32_16x16x32_bf16 v[136:139], a[244:247], a[212:215], v[136:139]\n" \
"ds_read_b128 v[208:211], v199 offset:8192\n" \
"v_mfma_f32_16x16x32_bf16 v[132:135], a[176:179], a[148:151], v[132:135]\n" \
"ds_read_b64_tr_b16 a[166:167], v33 offset:1024\n" \
"v_mfma_f32_16x16x32_bf16 v[132:135], a[240:243], a[212:215], v[132:135]\n" \
"ds_read_b128 a[148:151], v28 offset:2048\n" \
"v_mfma_f32_16x16x32_bf16 v[148:151], a[176:179], a[152:155], v[148:151]\n" \
"ds_read_b128 a[212:215], v29 offset:2048\n" \
"v_mfma_f32_16x16x32_bf16 v[148:151], a[240:243], a[216:219], v[148:151]\n" \
"ds_read_b64_tr_b16 a[228:229], v33 offset:8192\n" \
"v_mfma_f32_16x16x32_bf16 v[152:155], a[180:183], a[152:155], v[152:155]\n" \
"ds_read_b64_tr_b16 a[230:231], v33 offset:9216\n" \
"v_mfma_f32_16x16x32_bf16 v[152:155], a[244:247], a[216:219], v[152:155]\n" \
"ds_read_b64_tr_b16 a[168:169], v34 offset:0\n" \
"v_mfma_f32_16x16x32_bf16 v[156:159], a[184:187], a[152:155], v[156:159]\n" \
"ds_read_b64_tr_b16 a[170:171], v34 offset:1024\n" \
"v_mfma_f32_16x16x32_bf16 v[156:159], a[248:251], a[216:219], v[156:159]\n" \
"ds_read_b64_tr_b16 a[232:233], v34 offset:8192\n" \
"v_mfma_f32_16x16x32_bf16 v[160:163], a[188:191], a[152:155], v[160:163]\n" \
"ds_read_b64_tr_b16 a[234:235], v34 offset:9216\n" \
"v_mfma_f32_16x16x32_bf16 v[160:163], a[252:255], a[216:219], v[160:163]\n" \
"ds_read_b128 a[152:155], v28 offset:4096\n" \
"v_mfma_f32_16x16x32_bf16 v[176:179], a[188:191], a[156:159], v[176:179]\n" \
"ds_read_b128 a[216:219], v29 offset:4096\n" \
"v_mfma_f32_16x16x32_bf16 v[176:179], a[252:255], a[220:223], v[176:179]\n" \
"ds_read_b64_tr_b16 a[172:173], v35 offset:0\n" \
"v_mfma_f32_16x16x32_bf16 v[172:175], a[184:187], a[156:159], v[172:175]\n" \
"ds_read_b64_tr_b16 a[174:175], v35 offset:1024\n" \
"v_mfma_f32_16x16x32_bf16 v[172:175], a[248:251], a[220:223], v[172:175]\n" \
"ds_read_b64_tr_b16 a[236:237], v35 offset:8192\n" \
"v_mfma_f32_16x16x32_bf16 v[168:171], a[180:183], a[156:159], v[168:171]\n" \
"ds_read_b64_tr_b16 a[238:239], v35 offset:9216\n" \
"v_mfma_f32_16x16x32_bf16 v[168:171], a[244:247], a[220:223], v[168:171]\n" \
"v_mfma_f32_16x16x32_bf16 v[164:167], a[176:179], a[156:159], v[164:167]\n" \
"v_mfma_f32_16x16x32_bf16 v[164:167], a[240:243], a[220:223], v[164:167]\n" \
"ds_read_b128 a[156:159], v28 offset:6144\n" \
"ds_read_b128 a[220:223], v29 offset:6144\n" \
"s_waitcnt lgkmcnt(15)\n" \
"v_mfma_f32_16x16x32_bf16 a[0:3], a[160:163], a[128:131], a[0:3]\n" \
"s_addk_i32 s84, 0x7c00\n" \
"v_mfma_f32_16x16x32_bf16 a[0:3], a[224:227], a[192:195], a[0:3]\n" \
"buffer_load_dwordx4 v27, s[72:75], s84 offen offset:2048 lds\n" \
"v_mfma_f32_16x16x32_bf16 a[4:7], a[164:167], a[128:131], a[4:7]\n" \
"ds_read_b64_tr_b16 a[176:177], v36 offset:0\n" \
"s_waitcnt lgkmcnt(13)\n" \
"v_mfma_f32_16x16x32_bf16 a[4:7], a[228:231], a[192:195], a[4:7]\n" \
"s_addk_i32 s84, 0x7c00\n" \
"s_waitcnt lgkmcnt(11)\n" \
"v_mfma_f32_16x16x32_bf16 a[8:11], a[168:171], a[128:131], a[8:11]\n" \
"buffer_load_dwordx4 v27, s[72:75], s84 offen offset:3072 lds\n" \
"s_waitcnt lgkmcnt(9)\n" \
"v_mfma_f32_16x16x32_bf16 a[8:11], a[232:235], a[192:195], a[8:11]\n" \
"ds_read_b64_tr_b16 a[178:179], v36 offset:1024\n" \
"s_waitcnt lgkmcnt(6)\n" \
"v_mfma_f32_16x16x32_bf16 a[12:15], a[172:175], a[128:131], a[12:15]\n" \
"ds_read_b64_tr_b16 a[240:241], v36 offset:8192\n" \
"s_waitcnt lgkmcnt(5)\n" \
"v_mfma_f32_16x16x32_bf16 a[12:15], a[236:239], a[192:195], a[12:15]\n" \
"v_mfma_f32_16x16x32_bf16 a[28:31], a[172:175], a[132:135], a[28:31]\n" \
"v_mfma_f32_16x16x32_bf16 a[28:31], a[236:239], a[196:199], a[28:31]\n" \
"ds_read_b64_tr_b16 a[242:243], v36 offset:9216\n" \
"v_mfma_f32_16x16x32_bf16 a[24:27], a[168:171], a[132:135], a[24:27]\n" \
"v_mfma_f32_16x16x32_bf16 a[24:27], a[232:235], a[196:199], a[24:27]\n" \
"v_mfma_f32_16x16x32_bf16 a[20:23], a[164:167], a[132:135], a[20:23]\n" \
"v_mfma_f32_16x16x32_bf16 a[20:23], a[228:231], a[196:199], a[20:23]\n" \
"ds_read_b64_tr_b16 a[180:181], v37 offset:0\n" \
"v_mfma_f32_16x16x32_bf16 a[16:19], a[160:163], a[132:135], a[16:19]\n" \
"v_mfma_f32_16x16x32_bf16 a[16:19], a[224:227], a[196:199], a[16:19]\n" \
"v_mfma_f32_16x16x32_bf16 a[32:35], a[160:163], a[136:139], a[32:35]\n" \
"ds_read_b64_tr_b16 a[182:183], v37 offset:1024\n" \
"v_mfma_f32_16x16x32_bf16 a[32:35], a[224:227], a[200:203], a[32:35]\n" \
"v_mfma_f32_16x16x32_bf16 a[36:39], a[164:167], a[136:139], a[36:39]\n" \
"v_mfma_f32_16x16x32_bf16 a[36:39], a[228:231], a[200:203], a[36:39]\n" \
"buffer_store_dwordx4 v[200:203], v20, s[80:83], s35 offen sc1\n" \
".fill (L_pb + 12000 - .) / 4, 4, 0xBF800000\n" \
"L_pp3:\n" \
"s_cbranch_execz L_pret\n" \
"v_mfma_f32_16x16x32_bf16 a[40:43], a[168:171], a[136:139], a[40:43]\n" \
"s_mov_b32 m0, s32\n" \
"ds_read_b64_tr_b16 a[244:245], v37 offset:8192\n" \
"v_mfma_f32_16x16x32_bf16 a[40:43], a[232:235], a[200:203], a[40:43]\n" \
"s_mov_b32 exec_lo, 0x0\n" \
"s_mov_b32 exec_hi, 0xffffffff\n" \
"global_load_lds_dword v[188:189], off\n" \
"s_mov_b64 exec, -1\n" \
"s_nop 0\n" \
"v_mfma_f32_16x16x32_bf16 a[44:47], a[172:175], a[136:139], a[44:47]\n" \
"ds_read_b64_tr_b16 a[246:247], v37 offset:9216\n" \
"v_mfma_f32_16x16x32_bf16 a[44:47], a[236:239], a[200:203], a[44:47]\n" \
"v_mfma_f32_16x16x32_bf16 a[60:63], a[172:175], a[140:143], a[60:63]\n" \
"v_mfma_f32_16x16x32_bf16 a[60:63], a[236:239], a[204:207], a[60:63]\n" \
"v_mfma_f32_16x16x32_bf16 a[56:59], a[168:171], a[140:143], a[56:59]\n" \
"ds_read_b64_tr_b16 a[184:185], v38 offset:0\n" \
"v_mfma_f32_16x16x32_bf16 a[56:59], a[232:235], a[204:207], a[56:59]\n" \
"v_mfma_f32_16x16x32_bf16 a[52:55], a[164:167], a[140:143], a[52:55]\n" \
"buffer_store_dwordx4 v[204:207], v20, s[80:83], s88 offen sc1\n" \
"v_mfma_f32_16x16x32_bf16 a[52:55], a[228:231], a[204:207], a[52:55]\n" \
"ds_read_b64_tr_b16 a[186:187], v38 offset:1024\n" \
"v_mfma_f32_16x16x32_bf16 a[48:51], a[160:163], a[140:143], a[48:51]\n" \
"v_mfma_f32_16x16x32_bf16 a[48:51], a[224:227], a[204:207], a[48:51]\n" \
"v_mfma_f32_16x16x32_bf16 a[64:67], a[160:163], a[144:147], a[64:67]\n" \
"ds_read_b64_tr_b16 a[248:249], v38 offset:8192\n" \
"v_mfma_f32_16x16x32_bf16 a[64:67], a[224:227], a[208:211], a[64:67]\n" \
"v_mfma_f32_16x16x32_bf16 a[68:71], a[164:167], a[144:147], a[68:71]\n" \
"v_mfma_f32_16x16x32_bf16 a[68:71], a[228:231], a[208:211], a[68:71]\n" \
"ds_read_b64_tr_b16 a[250:251], v38 offset:9216\n" \
"v_mfma_f32_16x16x32_bf16 a[72:75], a[168:171], a[144:147], a[72:75]\n" \
"v_mfma_f32_16x16x32_bf16 a[72:75], a[232:235], a[208:211], a[72:75]\n" \
"v_mfma_f32_16x16x32_bf16 a[76:79], a[172:175], a[144:147], a[76:79]\n" \
"buffer_store_dwordx4 v[208:211], v20, s[80:83], s36 offen sc1\n" \
"v_mfma_f32_16x16x32_bf16 a[76:79], a[236:239], a[208:211], a[76:79]\n" \
"ds_read_b64_tr_b16 a[188:189], v39 offset:0\n" \
"v_mfma_f32_16x16x32_bf16 a[92:95], a[172:175], a[148:151], a[92:95]\n" \
"v_mfma_f32_16x16x32_bf16 a[92:95], a[236:239], a[212:215], a[92:95]\n" \
"v_mfma_f32_16x16x32_bf16 a[88:91], a[168:171], a[148:151], a[88:91]\n" \
"ds_read_b64_tr_b16 a[190:191], v39 offset:1024\n" \
"v_mfma_f32_16x16x32_bf16 a[88:91], a[232:235], a[212:215], a[88:91]\n" \
"v_mfma_f32_16x16x32_bf16 a[84:87], a[164:167], a[148:151], a[84:87]\n" \
"v_mfma_f32_16x16x32_bf16 a[84:87], a[228:231], a[212:215], a[84:87]\n" \
"ds_read_b64_tr_b16 a[252:253], v39 offset:8192\n" \
"v_mfma_f32_16x16x32_bf16 a[80:83], a[160:163], a[148:151], a[80:83]\n" \
"v_mfma_f32_16x16x32_bf16 a[80:83], a[224:227], a[212:215], a[80:83]\n" \
"v_mfma_f32_16x16x32_bf16 a[96:99], a[160:163], a[152:155], a[96:99]\n" \
"ds_read_b64_tr_b16 a[254:255], v39 offset:9216\n" \
"v_mfma_f32_16x16x32_bf16 a[96:99], a[224:227], a[216:219], a[96:99]\n" \
"v_mfma_f32_16x16x32_bf16 a[100:103], a[164:167], a[152:155], a[100:103]\n" \
"v_mfma_f32_16x16x32_bf16 a[100:103], a[228:231], a[216:219], a[100:103]\n" \
"v_mfma_f32_16x16x32_bf16 a[104:107], a[168:171], a[152:155], a[104:107]\n" \
"v_mfma_f32_16x16x32_bf16 a[104:107], a[232:235], a[216:219], a[104:107]\n" \
"v_mfma_f32_16x16x32_bf16 a[108:111], a[172:175], a[152:155], a[108:111]\n" \
"v_mfma_f32_16x16x32_bf16 a[108:111], a[236:239], a[216:219], a[108:111]\n" \
"s_waitcnt lgkmcnt(15)\n" \
"v_mfma_f32_16x16x32_bf16 a[124:127], a[172:175], a[156:159], a[124:127]\n" \
"v_mfma_f32_16x16x32_bf16 a[124:127], a[236:239], a[220:223], a[124:127]\n" \
"v_mfma_f32_16x16x32_bf16 a[120:123], a[168:171], a[156:159], a[120:123]\n" \
"v_mfma_f32_16x16x32_bf16 a[120:123], a[232:235], a[220:223], a[120:123]\n" \
"v_mfma_f32_16x16x32_bf16 a[116:119], a[164:167], a[156:159], a[116:119]\n" \
"v_mfma_f32_16x16x32_bf16 a[116:119], a[228:231], a[220:223], a[116:119]\n" \
"v_mfma_f32_16x16x32_bf16 a[112:115], a[160:163], a[156:159], a[112:115]\n" \
"v_mfma_f32_16x16x32_bf16 a[112:115], a[224:227], a[220:223], a[112:115]\n" \
"s_waitcnt vmcnt(4) lgkmcnt(0)\n" \
"s_barrier\n" \
"v_mfma_f32_16x16x32_bf16 v[52:55], a[176:179], a[128:131], v[52:55]\n" \
"s_add_u32 m0, s30, 0x0\n" \
"s_movk_i32 s84, 0x300\n" \
"v_mfma_f32_16x16x32_bf16 v[52:55], a[240:243], a[192:195], v[52:55]\n" \
"buffer_load_dwordx4 v22, s[64:67], s84 offen offset:0 lds\n" \
"v_mfma_f32_16x16x32_bf16 v[56:59], a[180:183], a[128:131], v[56:59]\n" \
"ds_read_b64_tr_b16 a[160:161], v32 offset:32768\n" \
"v_mfma_f32_16x16x32_bf16 v[56:59], a[244:247], a[192:195], v[56:59]\n" \
"s_movk_i32 s84, 0x1f00\n" \
"v_mfma_f32_16x16x32_bf16 v[60:63], a[184:187], a[128:131], v[60:63]\n" \
"buffer_load_dwordx4 v23, s[64:67], s84 offen offset:1024 lds\n" \
"v_mfma_f32_16x16x32_bf16 v[60:63], a[248:251], a[192:195], v[60:63]\n" \
"ds_read_b64_tr_b16 a[162:163], v32 offset:33792\n" \
"v_mfma_f32_16x16x32_bf16 v[64:67], a[188:191], a[128:131], v[64:67]\n" \
"ds_read_b64_tr_b16 a[224:225], v32 offset:40960\n" \
"v_mfma_f32_16x16x32_bf16 v[64:67], a[252:255], a[192:195], v[64:67]\n" \
"ds_read_b128 a[128:131], v28 offset:49152\n" \
"v_mfma_f32_16x16x32_bf16 v[80:83], a[188:191], a[132:135], v[80:83]\n" \
"ds_read_b128 a[192:195], v29 offset:49152\n" \
"v_mfma_f32_16x16x32_bf16 v[80:83], a[252:255], a[196:199], v[80:83]\n" \
"s_movk_i32 s84, 0x3b00\n" \
"v_mfma_f32_16x16x32_bf16 v[76:79], a[184:187], a[132:135], v[76:79]\n" \
"buffer_load_dwordx4 v22, s[64:67], s84 offen offset:2048 lds\n" \
"v_mfma_f32_16x16x32_bf16 v[76:79], a[248:251], a[196:199], v[76:79]\n" \
"s_movk_i32 s84, 0x5700\n" \
"v_mfma_f32_16x16x32_bf16 v[72:75], a[180:183], a[132:135], v[72:75]\n" \
"buffer_load_dwordx4 v23, s[64:67], s84 offen offset:3072 lds\n" \
"v_mfma_f32_16x16x32_bf16 v[72:75], a[244:247], a[196:199], v[72:75]\n" \
"s_add_u32 m0, s31, 0x0\n" \
"s_mov_b32 s84, 0x300000\n" \
"v_mfma_f32_16x16x32_bf16 v[68:71], a[176:179], a[132:135], v[68:71]\n" \
"buffer_load_dwordx4 v26, s[68:71], s84 offen offset:0 lds\n" \
"v_mfma_f32_16x16x32_bf16 v[68:71], a[240:243], a[196:199], v[68:71]\n" \
"ds_read_b128 a[132:135], v28 offset:51200\n" \
"v_mfma_f32_16x16x32_bf16 v[84:87], a[176:179], a[136:139], v[84:87]\n" \
"ds_read_b128 a[196:199], v29 offset:51200\n" \
"v_mfma_f32_16x16x32_bf16 v[84:87], a[240:243], a[200:203], v[84:87]\n" \
"s_addk_i32 s84, 0x7c00\n" \
"v_mfma_f32_16x16x32_bf16 v[88:91], a[180:183], a[136:139], v[88:91]\n" \
"buffer_load_dwordx4 v26, s[68:71], s84 offen offset:1024 lds\n" \
"v_mfma_f32_16x16x32_bf16 v[88:91], a[244:247], a[200:203], v[88:91]\n" \
"ds_read_b64_tr_b16 a[226:227], v32 offset:41984\n" \
"v_mfma_f32_16x16x32_bf16 v[92:95], a[184:187], a[136:139], v[92:95]\n" \
"s_addk_i32 s84, 0x7c00\n" \
"v_mfma_f32_16x16x32_bf16 v[92:95], a[248:251], a[200:203], v[92:95]\n" \
"buffer_load_dwordx4 v27, s[68:71], s84 offen offset:2048 lds\n" \
"v_mfma_f32_16x16x32_bf16 v[96:99], a[188:191], a[136:139], v[96:99]\n" \
"ds_read_b64_tr_b16 a[164:165], v33 offset:32768\n" \
"v_mfma_f32_16x16x32_bf16 v[96:99], a[252:255], a[200:203], v[96:99]\n" \
"ds_read_b128 a[136:139], v28 offset:53248\n" \
"v_mfma_f32_16x16x32_bf16 v[112:115], a[188:191], a[140:143], v[112:115]\n" \
"ds_read_b128 a[200:203], v29 offset:53248\n" \
"v_mfma_f32_16x16x32_bf16 v[112:115], a[252:255], a[204:207], v[112:115]\n" \
"s_addk_i32 s84, 0x7c00\n" \
"v_mfma_f32_16x16x32_bf16 v[108:111], a[184:187], a[140:143], v[108:111]\n" \
"buffer_load_dwordx4 v27, s[68:71], s84 offen offset:3072 lds\n" \
"v_mfma_f32_16x16x32_bf16 v[108:111], a[248:251], a[204:207], v[108:111]\n" \
"s_add_u32 m0, s30, 0x1000\n" \
"s_mov_b32 s84, 0x8300\n" \
"v_mfma_f32_16x16x32_bf16 v[104:107], a[180:183], a[140:143], v[104:107]\n" \
"buffer_load_dwordx4 v22, s[64:67], s84 offen offset:0 lds\n" \
"v_mfma_f32_16x16x32_bf16 v[104:107], a[244:247], a[204:207], v[104:107]\n" \
"ds_read_b64_tr_b16 a[166:167], v33 offset:33792\n" \
"v_mfma_f32_16x16x32_bf16 v[100:103], a[176:179], a[140:143], v[100:103]\n" \
"ds_read_b64_tr_b16 a[228:229], v33 offset:40960\n" \
"v_mfma_f32_16x16x32_bf16 v[100:103], a[240:243], a[204:207], v[100:103]\n" \
"ds_read_b128 a[140:143], v28 offset:55296\n" \
"v_mfma_f32_16x16x32_bf16 v[116:119], a[176:179], a[144:147], v[116:119]\n" \
"ds_read_b128 a[204:207], v29 offset:55296\n" \
"v_mfma_f32_16x16x32_bf16 v[116:119], a[240:243], a[208:211], v[116:119]\n" \
"s_addk_i32 s84, 0x1c00\n" \
"v_mfma_f32_16x16x32_bf16 v[120:123], a[180:183], a[144:147], v[120:123]\n" \
"buffer_load_dwordx4 v23, s[64:67], s84 offen offset:1024 lds\n" \
"v_mfma_f32_16x16x32_bf16 v[120:123], a[244:247], a[208:211], v[120:123]\n" \
"s_addk_i32 s84, 0x1c00\n" \
"v_mfma_f32_16x16x32_bf16 v[124:127], a[184:187], a[144:147], v[124:127]\n" \
"buffer_load_dwordx4 v22, s[64:67], s84 offen offset:2048 lds\n" \
"v_mfma_f32_16x16x32_bf16 v[124:127], a[248:251], a[208:211], v[124:127]\n" \
"s_addk_i32 s84, 0x1c00\n" \
"s_nop 0\n" \
"v_mfma_f32_16x16x32_bf16 v[128:131], a[188:191], a[144:147], v[128:131]\n" \
"buffer_load_dwordx4 v23, s[64:67], s84 offen offset:3072 lds\n" \
"v_mfma_f32_16x16x32_bf16 v[128:131], a[252:255], a[208:211], v[128:131]\n" \
"ds_read_b128 a[144:147], v28 offset:32768\n" \
"v_mfma_f32_16x16x32_bf16 v[144:147], a[188:191], a[148:151], v[144:147]\n" \
"ds_read_b128 a[208:211], v29 offset:32768\n" \
"v_mfma_f32_16x16x32_bf16 v[144:147], a[252:255], a[212:215], v[144:147]\n" \
"s_add_u32 m0, s31, 0x4000\n" \
"s_mov_b32 s84, 0x300000\n" \
"v_mfma_f32_16x16x32_bf16 v[140:143], a[184:187], a[148:151], v[140:143]\n" \
"buffer_load_dwordx4 v26, s[72:75], s84 offen offset:0 lds\n" \
"v_mfma_f32_16x16x32_bf16 v[140:143], a[248:251], a[212:215], v[140:143]\n" \
"ds_read_b64_tr_b16 a[230:231], v33 offset:41984\n" \
"v_mfma_f32_16x16x32_bf16 v[136:139], a[180:183], a[148:151], v[136:139]\n" \
"s_addk_i32 s84, 0x7c00\n" \
"s_nop 0\n" \
"v_mfma_f32_16x16x32_bf16 v[136:139], a[244:247], a[212:215], v[136:139]\n" \
"buffer_load_dwordx4 v26, s[72:75], s84 offen offset:1024 lds\n" \
"v_mfma_f32_16x16x32_bf16 v[132:135], a[176:179], a[148:151], v[132:135]\n" \
"ds_read_b64_tr_b16 a[168:169], v34 offset:32768\n" \
"v_mfma_f32_16x16x32_bf16 v[132:135], a[240:243], a[212:215], v[132:135]\n" \
"ds_read_b128 a[148:151], v28 offset:34816\n" \
"v_mfma_f32_16x16x32_bf16 v[148:151], a[176:179], a[152:155], v[148:151]\n" \
"ds_read_b128 a[212:215], v29 offset:34816\n" \
"v_mfma_f32_16x16x32_bf16 v[148:151], a[240:243], a[216:219], v[148:151]\n" \
"ds_read_b64_tr_b16 a[170:171], v34 offset:33792\n" \
"v_mfma_f32_16x16x32_bf16 v[152:155], a[180:183], a[152:155], v[152:155]\n" \
"ds_read_b64_tr_b16 a[232:233], v34 offset:40960\n" \
"v_mfma_f32_16x16x32_bf16 v[152:155], a[244:247], a[216:219], v[152:155]\n" \
"ds_read_b64_tr_b16 a[234:235], v34 offset:41984\n" \
"v_mfma_f32_16x16x32_bf16 v[156:159], a[184:187], a[152:155], v[156:159]\n" \
"ds_read_b64_tr_b16 a[172:173], v35 offset:32768\n" \
"v_mfma_f32_16x16x32_bf16 v[156:159], a[248:251], a[216:219], v[156:159]\n" \
"ds_read_b64_tr_b16 a[174:175], v35 offset:33792\n" \
"v_mfma_f32_16x16x32_bf16 v[160:163], a[188:191], a[152:155], v[160:163]\n" \
"ds_read_b64_tr_b16 a[236:237], v35 offset:40960\n" \
"v_mfma_f32_16x16x32_bf16 v[160:163], a[252:255], a[216:219], v[160:163]\n" \
"ds_read_b128 a[152:155], v28 offset:36864\n" \
"v_mfma_f32_16x16x32_bf16 v[176:179], a[188:191], a[156:159], v[176:179]\n" \
"ds_read_b128 a[216:219], v29 offset:36864\n" \
"v_mfma_f32_16x16x32_bf16 v[176:179], a[252:255], a[220:223], v[176:179]\n" \
"ds_read_b64_tr_b16 a[238:239], v35 offset:41984\n" \
"v_mfma_f32_16x16x32_bf16 v[172:175], a[184:187], a[156:159], v[172:175]\n" \
"v_mfma_f32_16x16x32_bf16 v[172:175], a[248:251], a[220:223], v[172:175]\n" \
"v_mfma_f32_16x16x32_bf16 v[168:171], a[180:183], a[156:159], v[168:171]\n" \
"v_mfma_f32_16x16x32_bf16 v[168:171], a[244:247], a[220:223], v[168:171]\n" \
"v_mfma_f32_16x16x32_bf16 v[164:167], a[176:179], a[156:159], v[164:167]\n" \
"v_mfma_f32_16x16x32_bf16 v[164:167], a[240:243], a[220:223], v[164:167]\n" \
"ds_read_b128 a[156:159], v28 offset:38912\n" \
"ds_read_b128 a[220:223], v29 offset:38912\n" \
"s_waitcnt lgkmcnt(15)\n" \
"v_mfma_f32_16x16x32_bf16 a[0:3], a[160:163], a[128:131], a[0:3]\n" \
"s_addk_i32 s84, 0x7c00\n" \
"v_mfma_f32_16x16x32_bf16 a[0:3], a[224:227], a[192:195], a[0:3]\n" \
"buffer_load_dwordx4 v27, s[72:75], s84 offen offset:2048 lds\n" \
"v_mfma_f32_16x16x32_bf16 a[4:7], a[164:167], a[128:131], a[4:7]\n" \
"ds_read_b64_tr_b16 a[176:177], v36 offset:32768\n" \
"s_waitcnt lgkmcnt(15)\n" \
"v_mfma_f32_16x16x32_bf16 a[4:7], a[228:231], a[192:195], a[4:7]\n" \
"s_addk_i32 s84, 0x7c00\n" \
"s_waitcnt lgkmcnt(11)\n" \
"v_mfma_f32_16x16x32_bf16 a[8:11], a[168:171], a[128:131], a[8:11]\n" \
"buffer_load_dwordx4 v27, s[72:75], s84 offen offset:3072 lds\n" \
"s_waitcnt lgkmcnt(9)\n" \
"v_mfma_f32_16x16x32_bf16 a[8:11], a[232:235], a[192:195], a[8:11]\n" \
"ds_read_b64_tr_b16 a[178:179], v36 offset:33792\n" \
"s_waitcnt lgkmcnt(8)\n" \
"v_mfma_f32_16x16x32_bf16 a[12:15], a[172:175], a[128:131], a[12:15]\n" \
"ds_read_b64_tr_b16 a[240:241], v36 offset:40960\n" \
"s_waitcnt lgkmcnt(5)\n" \
"v_mfma_f32_16x16x32_bf16 a[12:15], a[236:239], a[192:195], a[12:15]\n" \
"v_mfma_f32_16x16x32_bf16 a[28:31], a[172:175], a[132:135], a[28:31]\n" \
"v_mfma_f32_16x16x32_bf16 a[28:31], a[236:239], a[196:199], a[28:31]\n" \
"ds_read_b64_tr_b16 a[242:243], v36 offset:41984\n" \
"v_mfma_f32_16x16x32_bf16 a[24:27], a[168:171], a[132:135], a[24:27]\n" \
"v_mfma_f32_16x16x32_bf16 a[24:27], a[232:235], a[196:199], a[24:27]\n" \
"v_mfma_f32_16x16x32_bf16 a[20:23], a[164:167], a[132:135], a[20:23]\n" \
"v_mfma_f32_16x16x32_bf16 a[20:23], a[228:231], a[196:199], a[20:23]\n" \
"ds_read_b64_tr_b16 a[180:181], v37 offset:32768\n" \
"v_mfma_f32_16x16x32_bf16 a[16:19], a[160:163], a[132:135], a[16:19]\n" \
"v_mfma_f32_16x16x32_bf16 a[16:19], a[224:227], a[196:199], a[16:19]\n" \
"v_mfma_f32_16x16x32_bf16 a[32:35], a[160:163], a[136:139], a[32:35]\n" \
"ds_read_b64_tr_b16 a[182:183], v37 offset:33792\n" \
"v_mfma_f32_16x16x32_bf16 a[32:35], a[224:227], a[200:203], a[32:35]\n" \
"v_mfma_f32_16x16x32_bf16 a[36:39], a[164:167], a[136:139], a[36:39]\n" \
"v_lshlrev_b32_e32 v45, 3, v20\n" \
"v_add_u32_e32 v45, s31, v45\n" \
"v_mfma_f32_16x16x32_bf16 a[36:39], a[228:231], a[200:203], a[36:39]\n" \
"s_getpc_b64 s[86:87]\n" \
"s_sub_u32 s86, s86, 0xf800\n" \
"s_subb_u32 s87, s87, 0\n" \
"s_mov_b32 m0, s32\n" \
"s_nop 0\n" \
"v_mfma_f32_16x16x32_bf16 a[40:43], a[168:171], a[136:139], a[40:43]\n" \
"global_load_lds_dword v45, s[86:87]\n" \
"v_mfma_f32_16x16x32_bf16 a[40:43], a[232:235], a[200:203], a[40:43]\n" \
"ds_read_b64_tr_b16 a[244:245], v37 offset:40960\n" \
"v_mfma_f32_16x16x32_bf16 a[44:47], a[172:175], a[136:139], a[44:47]\n" \
"ds_read_b64_tr_b16 a[246:247], v37 offset:41984\n" \
"v_mfma_f32_16x16x32_bf16 a[44:47], a[236:239], a[200:203], a[44:47]\n" \
"v_mfma_f32_16x16x32_bf16 a[60:63], a[172:175], a[140:143], a[60:63]\n" \
"v_mfma_f32_16x16x32_bf16 a[60:63], a[236:239], a[204:207], a[60:63]\n" \
"v_mfma_f32_16x16x32_bf16 a[56:59], a[168:171], a[140:143], a[56:59]\n" \
"ds_read_b64_tr_b16 a[184:185], v38 offset:32768\n" \
"v_mfma_f32_16x16x32_bf16 a[56:59], a[232:235], a[204:207], a[56:59]\n" \
"v_mfma_f32_16x16x32_bf16 a[52:55], a[164:167], a[140:143], a[52:55]\n" \
"v_mfma_f32_16x16x32_bf16 a[52:55], a[228:231], a[204:207], a[52:55]\n" \
"ds_read_b64_tr_b16 a[186:187], v38 offset:33792\n" \
"v_mfma_f32_16x16x32_bf16 a[48:51], a[160:163], a[140:143], a[48:51]\n" \
"v_mfma_f32_16x16x32_bf16 a[48:51], a[224:227], a[204:207], a[48:51]\n" \
"v_mfma_f32_16x16x32_bf16 a[64:67], a[160:163], a[144:147], a[64:67]\n" \
"ds_read_b64_tr_b16 a[248:249], v38 offset:40960\n" \
"v_mfma_f32_16x16x32_bf16 a[64:67], a[224:227], a[208:211], a[64:67]\n" \
"v_mfma_f32_16x16x32_bf16 a[68:71], a[164:167], a[144:147], a[68:71]\n" \
"v_mfma_f32_16x16x32_bf16 a[68:71], a[228:231], a[208:211], a[68:71]\n" \
"ds_read_b64_tr_b16 a[250:251], v38 offset:41984\n" \
"v_mfma_f32_16x16x32_bf16 a[72:75], a[168:171], a[144:147], a[72:75]\n" \
"v_mfma_f32_16x16x32_bf16 a[72:75], a[232:235], a[208:211], a[72:75]\n" \
"v_mfma_f32_16x16x32_bf16 a[76:79], a[172:175], a[144:147], a[76:79]\n" \
"v_mfma_f32_16x16x32_bf16 a[76:79], a[236:239], a[208:211], a[76:79]\n" \
"ds_read_b64_tr_b16 a[188:189], v39 offset:32768\n" \
"v_mfma_f32_16x16x32_bf16 a[92:95], a[172:175], a[148:151], a[92:95]\n" \
"v_mfma_f32_16x16x32_bf16 a[92:95], a[236:239], a[212:215], a[92:95]\n" \
"v_mfma_f32_16x16x32_bf16 a[88:91], a[168:171], a[148:151], a[88:91]\n" \
"ds_read_b64_tr_b16 a[190:191], v39 offset:33792\n" \
"v_mfma_f32_16x16x32_bf16 a[88:91], a[232:235], a[212:215], a[88:91]\n" \
"v_mfma_f32_16x16x32_bf16 a[84:87], a[164:167], a[148:151], a[84:87]\n" \
"v_mfma_f32_16x16x32_bf16 a[84:87], a[228:231], a[212:215], a[84:87]\n" \
"ds_read_b64_tr_b16 a[252:253], v39 offset:40960\n" \
"v_mfma_f32_16x16x32_bf16 a[80:83], a[160:163], a[148:151], a[80:83]\n" \
"v_mfma_f32_16x16x32_bf16 a[80:83], a[224:227], a[212:215], a[80:83]\n" \
"v_mfma_f32_16x16x32_bf16 a[96:99], a[160:163], a[152:155], a[96:99]\n" \
"ds_read_b64_tr_b16 a[254:255], v39 offset:41984\n" \
"v_mfma_f32_16x16x32_bf16 a[96:99], a[224:227], a[216:219], a[96:99]\n" \
"v_mfma_f32_16x16x32_bf16 a[100:103], a[164:167], a[152:155], a[100:103]\n" \
"v_mfma_f32_16x16x32_bf16 a[100:103], a[228:231], a[216:219], a[100:103]\n" \
"v_mfma_f32_16x16x32_bf16 a[104:107], a[168:171], a[152:155], a[104:107]\n" \
"v_mfma_f32_16x16x32_bf16 a[104:107], a[232:235], a[216:219], a[104:107]\n" \
"v_mfma_f32_16x16x32_bf16 a[108:111], a[172:175], a[152:155], a[108:111]\n" \
"v_mfma_f32_16x16x32_bf16 a[108:111], a[236:239], a[216:219], a[108:111]\n" \
"s_waitcnt lgkmcnt(15)\n" \
"v_mfma_f32_16x16x32_bf16 a[124:127], a[172:175], a[156:159], a[124:127]\n" \
"v_mfma_f32_16x16x32_bf16 a[124:127], a[236:239], a[220:223], a[124:127]\n" \
"v_mfma_f32_16x16x32_bf16 a[120:123], a[168:171], a[156:159], a[120:123]\n" \
"v_mfma_f32_16x16x32_bf16 a[120:123], a[232:235], a[220:223], a[120:123]\n" \
"v_mfma_f32_16x16x32_bf16 a[116:119], a[164:167], a[156:159], a[116:119]\n" \
"v_mfma_f32_16x16x32_bf16 a[116:119], a[228:231], a[220:223], a[116:119]\n" \
"v_mfma_f32_16x16x32_bf16 a[112:115], a[160:163], a[156:159], a[112:115]\n" \
"v_mfma_f32_16x16x32_bf16 a[112:115], a[224:227], a[220:223], a[112:115]\n" \
"s_waitcnt vmcnt(1) lgkmcnt(0)\n" \
"s_barrier\n" \
"v_mov_b32_e32 v45, s62\n" \
"s_mov_b64 exec, 1\n" \
"buffer_store_dword v45, off, s[80:83], s37 offset:128 sc1\n" \
"s_mov_b64 exec, -1\n" \
"v_mfma_f32_16x16x32_bf16 v[52:55], a[176:179], a[128:131], v[52:55]\n" \
"s_add_u32 m0, s30, 0x8000\n" \
"s_movk_i32 s84, 0x380\n" \
"v_mfma_f32_16x16x32_bf16 v[52:55], a[240:243], a[192:195], v[52:55]\n" \
"buffer_load_dwordx4 v22, s[64:67], s84 offen offset:0 lds\n" \
"v_mfma_f32_16x16x32_bf16 v[56:59], a[180:183], a[128:131], v[56:59]\n" \
"ds_read_b64_tr_b16 a[160:161], v32 offset:0\n" \
"v_mfma_f32_16x16x32_bf16 v[56:59], a[244:247], a[192:195], v[56:59]\n" \
"s_movk_i32 s84, 0x1f80\n" \
"v_mfma_f32_16x16x32_bf16 v[60:63], a[184:187], a[128:131], v[60:63]\n" \
"buffer_load_dwordx4 v23, s[64:67], s84 offen offset:1024 lds\n" \
"v_mfma_f32_16x16x32_bf16 v[60:63], a[248:251], a[192:195], v[60:63]\n" \
"ds_read_b64_tr_b16 a[162:163], v32 offset:1024\n" \
"v_mfma_f32_16x16x32_bf16 v[64:67], a[188:191], a[128:131], v[64:67]\n" \
"ds_read_b64_tr_b16 a[224:225], v32 offset:8192\n" \
"v_mfma_f32_16x16x32_bf16 v[64:67], a[252:255], a[192:195], v[64:67]\n" \
"ds_read_b128 a[128:131], v28 offset:16384\n" \
"v_mfma_f32_16x16x32_bf16 v[80:83], a[188:191], a[132:135], v[80:83]\n" \
"ds_read_b128 a[192:195], v29 offset:16384\n" \
"v_mfma_f32_16x16x32_bf16 v[80:83], a[252:255], a[196:199], v[80:83]\n" \
"s_movk_i32 s84, 0x3b80\n" \
"v_mfma_f32_16x16x32_bf16 v[76:79], a[184:187], a[132:135], v[76:79]\n" \
"buffer_load_dwordx4 v22, s[64:67], s84 offen offset:2048 lds\n" \
"v_mfma_f32_16x16x32_bf16 v[76:79], a[248:251], a[196:199], v[76:79]\n" \
"s_movk_i32 s84, 0x5780\n" \
"v_mfma_f32_16x16x32_bf16 v[72:75], a[180:183], a[132:135], v[72:75]\n" \
"buffer_load_dwordx4 v23, s[64:67], s84 offen offset:3072 lds\n" \
"v_mfma_f32_16x16x32_bf16 v[72:75], a[244:247], a[196:199], v[72:75]\n" \
"s_add_u32 m0, s31, 0x8000\n" \
"s_mov_b32 s84, 0x380000\n" \
"v_mfma_f32_16x16x32_bf16 v[68:71], a[176:179], a[132:135], v[68:71]\n" \
"buffer_load_dwordx4 v26, s[68:71], s84 offen offset:0 lds\n" \
"v_mfma_f32_16x16x32_bf16 v[68:71], a[240:243], a[196:199], v[68:71]\n" \
"ds_read_b128 a[132:135], v28 offset:18432\n" \
"v_mfma_f32_16x16x32_bf16 v[84:87], a[176:179], a[136:139], v[84:87]\n" \
"ds_read_b128 a[196:199], v29 offset:18432\n" \
"v_mfma_f32_16x16x32_bf16 v[84:87], a[240:243], a[200:203], v[84:87]\n" \
"s_addk_i32 s84, 0x7c00\n" \
"v_mfma_f32_16x16x32_bf16 v[88:91], a[180:183], a[136:139], v[88:91]\n" \
"buffer_load_dwordx4 v26, s[68:71], s84 offen offset:1024 lds\n" \
"v_mfma_f32_16x16x32_bf16 v[88:91], a[244:247], a[200:203], v[88:91]\n" \
"ds_read_b64_tr_b16 a[226:227], v32 offset:9216\n" \
"v_mfma_f32_16x16x32_bf16 v[92:95], a[184:187], a[136:139], v[92:95]\n" \
"s_addk_i32 s84, 0x7c00\n" \
"v_mfma_f32_16x16x32_bf16 v[92:95], a[248:251], a[200:203], v[92:95]\n" \
"buffer_load_dwordx4 v27, s[68:71], s84 offen offset:2048 lds\n" \
"v_mfma_f32_16x16x32_bf16 v[96:99], a[188:191], a[136:139], v[96:99]\n" \
"ds_read_b64_tr_b16 a[164:165], v33 offset:0\n" \
"v_mfma_f32_16x16x32_bf16 v[96:99], a[252:255], a[200:203], v[96:99]\n" \
"ds_read_b128 a[136:139], v28 offset:20480\n" \
"v_mfma_f32_16x16x32_bf16 v[112:115], a[188:191], a[140:143], v[112:115]\n" \
"ds_read_b128 a[200:203], v29 offset:20480\n" \
"v_mfma_f32_16x16x32_bf16 v[112:115], a[252:255], a[204:207], v[112:115]\n" \
"s_addk_i32 s84, 0x7c00\n" \
"s_nop 0\n" \
"v_mfma_f32_16x16x32_bf16 v[108:111], a[184:187], a[140:143], v[108:111]\n" \
"buffer_load_dwordx4 v27, s[68:71], s84 offen offset:3072 lds\n" \
"v_mfma_f32_16x16x32_bf16 v[108:111], a[248:251], a[204:207], v[108:111]\n" \
"s_add_u32 m0, s30, 0x9000\n" \
"s_mov_b32 s84, 0x8380\n" \
"v_mfma_f32_16x16x32_bf16 v[104:107], a[180:183], a[140:143], v[104:107]\n" \
"buffer_load_dwordx4 v22, s[64:67], s84 offen offset:0 lds\n" \
"v_mfma_f32_16x16x32_bf16 v[104:107], a[244:247], a[204:207], v[104:107]\n" \
"ds_read_b64_tr_b16 a[166:167], v33 offset:1024\n" \
"v_mfma_f32_16x16x32_bf16 v[100:103], a[176:179], a[140:143], v[100:103]\n" \
"ds_read_b64_tr_b16 a[228:229], v33 offset:8192\n" \
"v_mfma_f32_16x16x32_bf16 v[100:103], a[240:243], a[204:207], v[100:103]\n" \
"ds_read_b128 a[140:143], v28 offset:22528\n" \
"v_mfma_f32_16x16x32_bf16 v[116:119], a[176:179], a[144:147], v[116:119]\n" \
"ds_read_b128 a[204:207], v29 offset:22528\n" \
"v_mfma_f32_16x16x32_bf16 v[116:119], a[240:243], a[208:211], v[116:119]\n" \
"s_addk_i32 s84, 0x1c00\n" \
"v_mfma_f32_16x16x32_bf16 v[120:123], a[180:183], a[144:147], v[120:123]\n" \
"buffer_load_dwordx4 v23, s[64:67], s84 offen offset:1024 lds\n" \
"v_mfma_f32_16x16x32_bf16 v[120:123], a[244:247], a[208:211], v[120:123]\n" \
"s_addk_i32 s84, 0x1c00\n" \
"v_mfma_f32_16x16x32_bf16 v[124:127], a[184:187], a[144:147], v[124:127]\n" \
"buffer_load_dwordx4 v22, s[64:67], s84 offen offset:2048 lds\n" \
"v_mfma_f32_16x16x32_bf16 v[124:127], a[248:251], a[208:211], v[124:127]\n" \
"s_addk_i32 s84, 0x1c00\n" \
"s_nop 0\n" \
"v_mfma_f32_16x16x32_bf16 v[128:131], a[188:191], a[144:147], v[128:131]\n" \
"buffer_load_dwordx4 v23, s[64:67], s84 offen offset:3072 lds\n" \
"v_mfma_f32_16x16x32_bf16 v[128:131], a[252:255], a[208:211], v[128:131]\n" \
"ds_read_b128 a[144:147], v28 offset:0\n" \
"v_mfma_f32_16x16x32_bf16 v[144:147], a[188:191], a[148:151], v[144:147]\n" \
"ds_read_b128 a[208:211], v29 offset:0\n" \
"v_mfma_f32_16x16x32_bf16 v[144:147], a[252:255], a[212:215], v[144:147]\n" \
"s_add_u32 m0, s31, 0xc000\n" \
"s_mov_b32 s84, 0x380000\n" \
"v_mfma_f32_16x16x32_bf16 v[140:143], a[184:187], a[148:151], v[140:143]\n" \
"buffer_load_dwordx4 v26, s[72:75], s84 offen offset:0 lds\n" \
"v_mfma_f32_16x16x32_bf16 v[140:143], a[248:251], a[212:215], v[140:143]\n" \
"ds_read_b64_tr_b16 a[230:231], v33 offset:9216\n" \
"v_mfma_f32_16x16x32_bf16 v[136:139], a[180:183], a[148:151], v[136:139]\n" \
"s_addk_i32 s84, 0x7c00\n" \
"s_nop 0\n" \
"v_mfma_f32_16x16x32_bf16 v[136:139], a[244:247], a[212:215], v[136:139]\n" \
"buffer_load_dwordx4 v26, s[72:75], s84 offen offset:1024 lds\n" \
"v_mfma_f32_16x16x32_bf16 v[132:135], a[176:179], a[148:151], v[132:135]\n" \
"ds_read_b64_tr_b16 a[168:169], v34 offset:0\n" \
"v_mfma_f32_16x16x32_bf16 v[132:135], a[240:243], a[212:215], v[132:135]\n" \
"ds_read_b128 a[148:151], v28 offset:2048\n" \
"v_mfma_f32_16x16x32_bf16 v[148:151], a[176:179], a[152:155], v[148:151]\n" \
"ds_read_b128 a[212:215], v29 offset:2048\n" \
"v_mfma_f32_16x16x32_bf16 v[148:151], a[240:243], a[216:219], v[148:151]\n" \
"ds_read_b64_tr_b16 a[170:171], v34 offset:1024\n" \
"v_mfma_f32_16x16x32_bf16 v[152:155], a[180:183], a[152:155], v[152:155]\n" \
"ds_read_b64_tr_b16 a[232:233], v34 offset:8192\n" \
"v_mfma_f32_16x16x32_bf16 v[152:155], a[244:247], a[216:219], v[152:155]\n" \
"ds_read_b64_tr_b16 a[234:235], v34 offset:9216\n" \
"v_mfma_f32_16x16x32_bf16 v[156:159], a[184:187], a[152:155], v[156:159]\n" \
"ds_read_b64_tr_b16 a[172:173], v35 offset:0\n" \
"v_mfma_f32_16x16x32_bf16 v[156:159], a[248:251], a[216:219], v[156:159]\n" \
"ds_read_b64_tr_b16 a[174:175], v35 offset:1024\n" \
"v_mfma_f32_16x16x32_bf16 v[160:163], a[188:191], a[152:155], v[160:163]\n" \
"ds_read_b64_tr_b16 a[236:237], v35 offset:8192\n" \
"v_mfma_f32_16x16x32_bf16 v[160:163], a[252:255], a[216:219], v[160:163]\n" \
"ds_read_b128 a[152:155], v28 offset:4096\n" \
"v_mfma_f32_16x16x32_bf16 v[176:179], a[188:191], a[156:159], v[176:179]\n" \
"ds_read_b128 a[216:219], v29 offset:4096\n" \
"v_mfma_f32_16x16x32_bf16 v[176:179], a[252:255], a[220:223], v[176:179]\n" \
"ds_read_b64_tr_b16 a[238:239], v35 offset:9216\n" \
"v_mfma_f32_16x16x32_bf16 v[172:175], a[184:187], a[156:159], v[172:175]\n" \
"v_mfma_f32_16x16x32_bf16 v[172:175], a[248:251], a[220:223], v[172:175]\n" \
"v_mfma_f32_16x16x32_bf16 v[168:171], a[180:183], a[156:159], v[168:171]\n" \
"v_mfma_f32_16x16x32_bf16 v[168:171], a[244:247], a[220:223], v[168:171]\n" \
"v_mfma_f32_16x16x32_bf16 v[164:167], a[176:179], a[156:159], v[164:167]\n" \
"v_mfma_f32_16x16x32_bf16 v[164:167], a[240:243], a[220:223], v[164:167]\n" \
"ds_read_b128 a[156:159], v28 offset:6144\n" \
"ds_read_b128 a[220:223], v29 offset:6144\n" \
"s_waitcnt lgkmcnt(15)\n" \
"v_mfma_f32_16x16x32_bf16 a[0:3], a[160:163], a[128:131], a[0:3]\n" \
"s_addk_i32 s84, 0x7c00\n" \
"v_mfma_f32_16x16x32_bf16 a[0:3], a[224:227], a[192:195], a[0:3]\n" \
"buffer_load_dwordx4 v27, s[72:75], s84 offen offset:2048 lds\n" \
"v_mfma_f32_16x16x32_bf16 a[4:7], a[164:167], a[128:131], a[4:7]\n" \
"ds_read_b64_tr_b16 a[176:177], v36 offset:0\n" \
"s_waitcnt lgkmcnt(15)\n" \
"v_mfma_f32_16x16x32_bf16 a[4:7], a[228:231], a[192:195], a[4:7]\n" \
"s_addk_i32 s84, 0x7c00\n" \
"s_waitcnt lgkmcnt(11)\n" \
"v_mfma_f32_16x16x32_bf16 a[8:11], a[168:171], a[128:131], a[8:11]\n" \
"buffer_load_dwordx4 v27, s[72:75], s84 offen offset:3072 lds\n" \
"s_waitcnt lgkmcnt(9)\n" \
"v_mfma_f32_16x16x32_bf16 a[8:11], a[232:235], a[192:195], a[8:11]\n" \
"ds_read_b64_tr_b16 a[178:179], v36 offset:1024\n" \
"s_waitcnt lgkmcnt(8)\n" \
"v_mfma_f32_16x16x32_bf16 a[12:15], a[172:175], a[128:131], a[12:15]\n" \
"ds_read_b64_tr_b16 a[240:241], v36 offset:8192\n" \
"s_waitcnt lgkmcnt(5)\n" \
"v_mfma_f32_16x16x32_bf16 a[12:15], a[236:239], a[192:195], a[12:15]\n" \
"v_mfma_f32_16x16x32_bf16 a[28:31], a[172:175], a[132:135], a[28:31]\n" \
"v_mfma_f32_16x16x32_bf16 a[28:31], a[236:239], a[196:199], a[28:31]\n" \
"ds_read_b64_tr_b16 a[242:243], v36 offset:9216\n" \
"v_mfma_f32_16x16x32_bf16 a[24:27], a[168:171], a[132:135], a[24:27]\n" \
"v_mfma_f32_16x16x32_bf16 a[24:27], a[232:235], a[196:199], a[24:27]\n" \
"v_mfma_f32_16x16x32_bf16 a[20:23], a[164:167], a[132:135], a[20:23]\n" \
"v_mfma_f32_16x16x32_bf16 a[20:23], a[228:231], a[196:199], a[20:23]\n" \
"ds_read_b64_tr_b16 a[180:181], v37 offset:0\n" \
"v_mfma_f32_16x16x32_bf16 a[16:19], a[160:163], a[132:135], a[16:19]\n" \
"v_mfma_f32_16x16x32_bf16 a[16:19], a[224:227], a[196:199], a[16:19]\n" \
"v_mfma_f32_16x16x32_bf16 a[32:35], a[160:163], a[136:139], a[32:35]\n" \
"v_bfe_u32 v44, v20, 2, 5\n" \
"ds_read_b64_tr_b16 a[182:183], v37 offset:1024\n" \
"v_mfma_f32_16x16x32_bf16 a[32:35], a[224:227], a[200:203], a[32:35]\n" \
"buffer_load_dword v44, v44, s[80:83], s38 offen offset:128 sc1\n" \
"v_mfma_f32_16x16x32_bf16 a[36:39], a[164:167], a[136:139], a[36:39]\n" \
"v_mfma_f32_16x16x32_bf16 a[36:39], a[228:231], a[200:203], a[36:39]\n" \
"ds_read_b64_tr_b16 a[244:245], v37 offset:8192\n" \
"v_mfma_f32_16x16x32_bf16 a[40:43], a[168:171], a[136:139], a[40:43]\n" \
"v_mfma_f32_16x16x32_bf16 a[40:43], a[232:235], a[200:203], a[40:43]\n" \
"v_mfma_f32_16x16x32_bf16 a[44:47], a[172:175], a[136:139], a[44:47]\n" \
"ds_read_b64_tr_b16 a[246:247], v37 offset:9216\n" \
"v_mfma_f32_16x16x32_bf16 a[44:47], a[236:239], a[200:203], a[44:47]\n" \
"v_mfma_f32_16x16x32_bf16 a[60:63], a[172:175], a[140:143], a[60:63]\n" \
"global_store_dwordx4 v49, v[0:3], s[28:29] offset:0 sc1\n" \
"v_mfma_f32_16x16x32_bf16 a[60:63], a[236:239], a[204:207], a[60:63]\n" \
"v_mfma_f32_16x16x32_bf16 a[56:59], a[168:171], a[140:143], a[56:59]\n" \
"ds_read_b64_tr_b16 a[184:185], v38 offset:0\n" \
"v_mfma_f32_16x16x32_bf16 a[56:59], a[232:235], a[204:207], a[56:59]\n" \
"v_mfma_f32_16x16x32_bf16 a[52:55], a[164:167], a[140:143], a[52:55]\n" \
"v_mfma_f32_16x16x32_bf16 a[52:55], a[228:231], a[204:207], a[52:55]\n" \
"ds_read_b64_tr_b16 a[186:187], v38 offset:1024\n" \
"v_mfma_f32_16x16x32_bf16 a[48:51], a[160:163], a[140:143], a[48:51]\n" \
"v_mfma_f32_16x16x32_bf16 a[48:51], a[224:227], a[204:207], a[48:51]\n" \
"v_mfma_f32_16x16x32_bf16 a[64:67], a[160:163], a[144:147], a[64:67]\n" \
"global_store_dwordx4 v49, v[0:3], s[28:29] offset:1024 sc1\n" \
"v_mfma_f32_16x16x32_bf16 a[64:67], a[224:227], a[208:211], a[64:67]\n" \
"ds_read_b64_tr_b16 a[248:249], v38 offset:8192\n" \
"v_mfma_f32_16x16x32_bf16 a[68:71], a[164:167], a[144:147], a[68:71]\n" \
"v_mfma_f32_16x16x32_bf16 a[68:71], a[228:231], a[208:211], a[68:71]\n" \
"ds_read_b64_tr_b16 a[250:251], v38 offset:9216\n" \
"v_mfma_f32_16x16x32_bf16 a[72:75], a[168:171], a[144:147], a[72:75]\n" \
"v_mfma_f32_16x16x32_bf16 a[72:75], a[232:235], a[208:211], a[72:75]\n" \
"v_mfma_f32_16x16x32_bf16 a[76:79], a[172:175], a[144:147], a[76:79]\n" \
"v_mfma_f32_16x16x32_bf16 a[76:79], a[236:239], a[208:211], a[76:79]\n" \
"ds_read_b64_tr_b16 a[188:189], v39 offset:0\n" \
"v_mfma_f32_16x16x32_bf16 a[92:95], a[172:175], a[148:151], a[92:95]\n" \
"s_mov_b64 exec, 0xffff\n" \
"global_store_dwordx4 v49, v[0:3], s[28:29] offset:2048 sc1\n" \
"s_mov_b64 exec, -1\n" \
"s_nop 0\n" \
"v_mfma_f32_16x16x32_bf16 a[92:95], a[236:239], a[212:215], a[92:95]\n" \
"v_mfma_f32_16x16x32_bf16 a[88:91], a[168:171], a[148:151], a[88:91]\n" \
"ds_read_b64_tr_b16 a[190:191], v39 offset:1024\n" \
"v_mfma_f32_16x16x32_bf16 a[88:91], a[232:235], a[212:215], a[88:91]\n" \
"v_mfma_f32_16x16x32_bf16 a[84:87], a[164:167], a[148:151], a[84:87]\n" \
"v_mfma_f32_16x16x32_bf16 a[84:87], a[228:231], a[212:215], a[84:87]\n" \
"ds_read_b64_tr_b16 a[252:253], v39 offset:8192\n" \
"v_mfma_f32_16x16x32_bf16 a[80:83], a[160:163], a[148:151], a[80:83]\n" \
"v_mfma_f32_16x16x32_bf16 a[80:83], a[224:227], a[212:215], a[80:83]\n" \
"v_mfma_f32_16x16x32_bf16 a[96:99], a[160:163], a[152:155], a[96:99]\n" \
"ds_read_b64_tr_b16 a[254:255], v39 offset:9216\n" \
"v_mfma_f32_16x16x32_bf16 a[96:99], a[224:227], a[216:219], a[96:99]\n" \
"v_mfma_f32_16x16x32_bf16 a[100:103], a[164:167], a[152:155], a[100:103]\n" \
"v_mfma_f32_16x16x32_bf16 a[100:103], a[228:231], a[216:219], a[100:103]\n" \
"v_mfma_f32_16x16x32_bf16 a[104:107], a[168:171], a[152:155], a[104:107]\n" \
"v_mfma_f32_16x16x32_bf16 a[104:107], a[232:235], a[216:219], a[104:107]\n" \
"v_mfma_f32_16x16x32_bf16 a[108:111], a[172:175], a[152:155], a[108:111]\n" \
"v_mfma_f32_16x16x32_bf16 a[108:111], a[236:239], a[216:219], a[108:111]\n" \
"s_waitcnt lgkmcnt(15)\n" \
"v_mfma_f32_16x16x32_bf16 a[124:127], a[172:175], a[156:159], a[124:127]\n" \
"v_mfma_f32_16x16x32_bf16 a[124:127], a[236:239], a[220:223], a[124:127]\n" \
"v_mfma_f32_16x16x32_bf16 a[120:123], a[168:171], a[156:159], a[120:123]\n" \
"v_mfma_f32_16x16x32_bf16 a[120:123], a[232:235], a[220:223], a[120:123]\n" \
"v_mfma_f32_16x16x32_bf16 a[116:119], a[164:167], a[156:159], a[116:119]\n" \
"v_mfma_f32_16x16x32_bf16 a[116:119], a[228:231], a[220:223], a[116:119]\n" \
"v_mfma_f32_16x16x32_bf16 a[112:115], a[160:163], a[156:159], a[112:115]\n" \
"v_mfma_f32_16x16x32_bf16 a[112:115], a[224:227], a[220:223], a[112:115]\n" \
"s_waitcnt vmcnt(3) lgkmcnt(0)\n" \
"s_barrier\n" \
"v_cmp_ne_u32_e32 vcc, s62, v44\n" \
"s_nop 4\n" \
"s_cbranch_vccnz L_poll60_%=\n" \
"L_pollr60_%=:\n" \
"s_mov_b32 s68, s12\n" \
"s_and_b32 s69, s13, 0xffff\n" \
"s_mov_b32 s70, 0x480000\n" \
"s_mov_b32 s71, 0x20000\n" \
"v_mfma_f32_16x16x32_bf16 v[52:55], a[176:179], a[128:131], v[52:55]\n" \
"s_add_u32 m0, s40, 0x0\n" \
"s_add_u32 s85, s39, 0x0\n" \
"s_nop 0\n" \
"v_mfma_f32_16x16x32_bf16 v[52:55], a[240:243], a[192:195], v[52:55]\n" \
"buffer_load_dwordx4 v20, s[80:83], s85 offen sc1 lds\n" \
"v_mfma_f32_16x16x32_bf16 v[56:59], a[180:183], a[128:131], v[56:59]\n" \
"ds_read_b64_tr_b16 a[160:161], v32 offset:32768\n" \
"v_mfma_f32_16x16x32_bf16 v[56:59], a[244:247], a[192:195], v[56:59]\n" \
"s_add_u32 m0, s40, 0x1000\n" \
"s_add_u32 s85, s39, 0x1000\n" \
".fill (L_pb + 16096 - .) / 4, 4, 0xBF800000\n" \
"L_pp4:\n" \
"s_cbranch_execz L_pret\n" \
"s_nop 0\n" \
"v_mfma_f32_16x16x32_bf16 v[60:63], a[184:187], a[128:131], v[60:63]\n" \
"buffer_load_dwordx4 v20, s[80:83], s85 offen sc1 lds\n" \
"v_mfma_f32_16x16x32_bf16 v[60:63], a[248:251], a[192:195], v[60:63]\n" \
"ds_read_b64_tr_b16 a[162:163], v32 offset:33792\n" \
"v_mfma_f32_16x16x32_bf16 v[64:67], a[188:191], a[128:131], v[64:67]\n" \
"ds_read_b64_tr_b16 a[224:225], v32 offset:40960\n" \
"v_mfma_f32_16x16x32_bf16 v[64:67], a[252:255], a[192:195], v[64:67]\n" \
"ds_read_b128 a[128:131], v28 offset:49152\n" \
"v_mfma_f32_16x16x32_bf16 v[80:83], a[188:191], a[132:135], v[80:83]\n" \
"ds_read_b128 a[192:195], v29 offset:49152\n" \
"v_mfma_f32_16x16x32_bf16 v[80:83], a[252:255], a[196:199], v[80:83]\n" \
"s_add_u32 m0, s40, 0x3000\n" \
"s_movk_i32 s84, 0x0\n" \
"v_mfma_f32_16x16x32_bf16 v[76:79], a[184:187], a[132:135], v[76:79]\n" \
"buffer_load_dwordx4 v21, s[76:79], s84 offen offset:0 lds\n" \
"v_mfma_f32_16x16x32_bf16 v[76:79], a[248:251], a[196:199], v[76:79]\n" \
"s_add_u32 m0, s30, 0x0\n" \
"s_movk_i32 s84, 0x0\n" \
"s_nop 0\n" \
"v_mfma_f32_16x16x32_bf16 v[72:75], a[180:183], a[132:135], v[72:75]\n" \
"buffer_load_dwordx4 v24, s[76:79], s84 offen offset:0 lds\n" \
"v_mfma_f32_16x16x32_bf16 v[72:75], a[244:247], a[196:199], v[72:75]\n" \
"ds_read_b64_tr_b16 a[226:227], v32 offset:41984\n" \
"v_mfma_f32_16x16x32_bf16 v[68:71], a[176:179], a[132:135], v[68:71]\n" \
"ds_read_b64_tr_b16 a[164:165], v33 offset:32768\n" \
"v_mfma_f32_16x16x32_bf16 v[68:71], a[240:243], a[196:199], v[68:71]\n" \
"ds_read_b128 a[132:135], v28 offset:51200\n" \
"v_mfma_f32_16x16x32_bf16 v[84:87], a[176:179], a[136:139], v[84:87]\n" \
"ds_read_b128 a[196:199], v29 offset:51200\n" \
"v_mfma_f32_16x16x32_bf16 v[84:87], a[240:243], a[200:203], v[84:87]\n" \
"s_movk_i32 s84, 0x2000\n" \
"v_mfma_f32_16x16x32_bf16 v[88:91], a[180:183], a[136:139], v[88:91]\n" \
"buffer_load_dwordx4 v25, s[76:79], s84 offen offset:1024 lds\n" \
"v_mfma_f32_16x16x32_bf16 v[88:91], a[244:247], a[200:203], v[88:91]\n" \
"s_movk_i32 s84, 0x4000\n" \
"v_mfma_f32_16x16x32_bf16 v[92:95], a[184:187], a[136:139], v[92:95]\n" \
"buffer_load_dwordx4 v24, s[76:79], s84 offen offset:2048 lds\n" \
"v_mfma_f32_16x16x32_bf16 v[92:95], a[248:251], a[200:203], v[92:95]\n" \
"ds_read_b64_tr_b16 a[166:167], v33 offset:33792\n" \
"v_mfma_f32_16x16x32_bf16 v[96:99], a[188:191], a[136:139], v[96:99]\n" \
"ds_read_b64_tr_b16 a[228:229], v33 offset:40960\n" \
"v_mfma_f32_16x16x32_bf16 v[96:99], a[252:255], a[200:203], v[96:99]\n" \
"ds_read_b128 a[136:139], v28 offset:53248\n" \
"v_mfma_f32_16x16x32_bf16 v[112:115], a[188:191], a[140:143], v[112:115]\n" \
"ds_read_b128 a[200:203], v29 offset:53248\n" \
"v_mfma_f32_16x16x32_bf16 v[112:115], a[252:255], a[204:207], v[112:115]\n" \
"s_movk_i32 s84, 0x6000\n" \
"v_mfma_f32_16x16x32_bf16 v[108:111], a[184:187], a[140:143], v[108:111]\n" \
"buffer_load_dwordx4 v25, s[76:79], s84 offen offset:3072 lds\n" \
"v_mfma_f32_16x16x32_bf16 v[108:111], a[248:251], a[204:207], v[108:111]\n" \
"s_add_u32 m0, s31, 0x0\n" \
"s_movk_i32 s84, 0x0\n" \
"v_mfma_f32_16x16x32_bf16 v[104:107], a[180:183], a[140:143], v[104:107]\n" \
"buffer_load_dwordx4 v26, s[68:71], s84 offen offset:0 lds\n" \
"v_mfma_f32_16x16x32_bf16 v[104:107], a[244:247], a[204:207], v[104:107]\n" \
"s_movk_i32 s84, 0x7c00\n" \
"v_mfma_f32_16x16x32_bf16 v[100:103], a[176:179], a[140:143], v[100:103]\n" \
"buffer_load_dwordx4 v26, s[68:71], s84 offen offset:1024 lds\n" \
"v_mfma_f32_16x16x32_bf16 v[100:103], a[240:243], a[204:207], v[100:103]\n" \
"ds_read_b128 a[140:143], v28 offset:55296\n" \
"v_mfma_f32_16x16x32_bf16 v[116:119], a[176:179], a[144:147], v[116:119]\n" \
"ds_read_b128 a[204:207], v29 offset:55296\n" \
"v_mfma_f32_16x16x32_bf16 v[116:119], a[240:243], a[208:211], v[116:119]\n" \
"s_addk_i32 s84, 0x7c00\n" \
"v_mfma_f32_16x16x32_bf16 v[120:123], a[180:183], a[144:147], v[120:123]\n" \
"buffer_load_dwordx4 v27, s[68:71], s84 offen offset:2048 lds\n" \
"v_mfma_f32_16x16x32_bf16 v[120:123], a[244:247], a[208:211], v[120:123]\n" \
"ds_read_b64_tr_b16 a[230:231], v33 offset:41984\n" \
"v_mfma_f32_16x16x32_bf16 v[124:127], a[184:187], a[144:147], v[124:127]\n" \
"s_addk_i32 s84, 0x7c00\n" \
"v_mfma_f32_16x16x32_bf16 v[124:127], a[248:251], a[208:211], v[124:127]\n" \
"buffer_load_dwordx4 v27, s[68:71], s84 offen offset:3072 lds\n" \
"v_mfma_f32_16x16x32_bf16 v[128:131], a[188:191], a[144:147], v[128:131]\n" \
"ds_read_b64_tr_b16 a[168:169], v34 offset:32768\n" \
"v_mfma_f32_16x16x32_bf16 v[128:131], a[252:255], a[208:211], v[128:131]\n" \
"ds_read_b128 a[144:147], v28 offset:32768\n" \
"v_mfma_f32_16x16x32_bf16 v[144:147], a[188:191], a[148:151], v[144:147]\n" \
"ds_read_b128 a[208:211], v29 offset:32768\n" \
"v_mfma_f32_16x16x32_bf16 v[144:147], a[252:255], a[212:215], v[144:147]\n" \
"s_add_u32 m0, s30, 0x1000\n" \
"s_mov_b32 s84, 0x9000\n" \
"v_mfma_f32_16x16x32_bf16 v[140:143], a[184:187], a[148:151], v[140:143]\n" \
"buffer_load_dwordx4 v24, s[76:79], s84 offen offset:0 lds\n" \
"v_mfma_f32_16x16x32_bf16 v[140:143], a[248:251], a[212:215], v[140:143]\n" \
"ds_read_b64_tr_b16 a[170:171], v34 offset:33792\n" \
"v_mfma_f32_16x16x32_bf16 v[136:139], a[180:183], a[148:151], v[136:139]\n" \
"s_addk_i32 s84, 0x2000\n" \
"s_nop 0\n" \
"v_mfma_f32_16x16x32_bf16 v[136:139], a[244:247], a[212:215], v[136:139]\n" \
"buffer_load_dwordx4 v25, s[76:79], s84 offen offset:1024 lds\n" \
"v_mfma_f32_16x16x32_bf16 v[132:135], a[176:179], a[148:151], v[132:135]\n" \
"ds_read_b64_tr_b16 a[232:233], v34 offset:40960\n" \
"v_mfma_f32_16x16x32_bf16 v[132:135], a[240:243], a[212:215], v[132:135]\n" \
"ds_read_b128 a[148:151], v28 offset:34816\n" \
"v_mfma_f32_16x16x32_bf16 v[148:151], a[176:179], a[152:155], v[148:151]\n" \
"ds_read_b128 a[212:215], v29 offset:34816\n" \
"v_mfma_f32_16x16x32_bf16 v[148:151], a[240:243], a[216:219], v[148:151]\n" \
"ds_read_b64_tr_b16 a[234:235], v34 offset:41984\n" \
"v_mfma_f32_16x16x32_bf16 v[152:155], a[180:183], a[152:155], v[152:155]\n" \
"ds_read_b64_tr_b16 a[172:173], v35 offset:32768\n" \
"v_mfma_f32_16x16x32_bf16 v[152:155], a[244:247], a[216:219], v[152:155]\n" \
"ds_read_b64_tr_b16 a[174:175], v35 offset:33792\n" \
"v_mfma_f32_16x16x32_bf16 v[156:159], a[184:187], a[152:155], v[156:159]\n" \
"ds_read_b64_tr_b16 a[236:237], v35 offset:40960\n" \
"v_mfma_f32_16x16x32_bf16 v[156:159], a[248:251], a[216:219], v[156:159]\n" \
"ds_read_b64_tr_b16 a[238:239], v35 offset:41984\n" \
"v_mfma_f32_16x16x32_bf16 v[160:163], a[188:191], a[152:155], v[160:163]\n" \
"v_mfma_f32_16x16x32_bf16 v[160:163], a[252:255], a[216:219], v[160:163]\n" \
"ds_read_b128 a[152:155], v28 offset:36864\n" \
"v_mfma_f32_16x16x32_bf16 v[176:179], a[188:191], a[156:159], v[176:179]\n" \
"ds_read_b128 a[216:219], v29 offset:36864\n" \
"v_mfma_f32_16x16x32_bf16 v[176:179], a[252:255], a[220:223], v[176:179]\n" \
"v_mfma_f32_16x16x32_bf16 v[172:175], a[184:187], a[156:159], v[172:175]\n" \
"v_mfma_f32_16x16x32_bf16 v[172:175], a[248:251], a[220:223], v[172:175]\n" \
"v_mfma_f32_16x16x32_bf16 v[168:171], a[180:183], a[156:159], v[168:171]\n" \
"v_mfma_f32_16x16x32_bf16 v[168:171], a[244:247], a[220:223], v[168:171]\n" \
"v_mfma_f32_16x16x32_bf16 v[164:167], a[176:179], a[156:159], v[164:167]\n" \
"v_mfma_f32_16x16x32_bf16 v[164:167], a[240:243], a[220:223], v[164:167]\n" \
"ds_read_b128 a[156:159], v28 offset:38912\n" \
"ds_read_b128 a[220:223], v29 offset:38912\n" \
"s_waitcnt lgkmcnt(15)\n" \
"v_mfma_f32_16x16x32_bf16 a[0:3], a[160:163], a[128:131], a[0:3]\n" \
"s_addk_i32 s84, 0x2000\n" \
"v_mfma_f32_16x16x32_bf16 a[0:3], a[224:227], a[192:195], a[0:3]\n" \
"buffer_load_dwordx4 v24, s[76:79], s84 offen offset:2048 lds\n" \
"v_mfma_f32_16x16x32_bf16 a[4:7], a[164:167], a[128:131], a[4:7]\n" \
"ds_read_b64_tr_b16 a[176:177], v36 offset:32768\n" \
"v_mfma_f32_16x16x32_bf16 a[4:7], a[228:231], a[192:195], a[4:7]\n" \
"s_addk_i32 s84, 0x2000\n" \
"s_waitcnt lgkmcnt(13)\n" \
"v_mfma_f32_16x16x32_bf16 a[8:11], a[168:171], a[128:131], a[8:11]\n" \
"buffer_load_dwordx4 v25, s[76:79], s84 offen offset:3072 lds\n" \
"s_waitcnt lgkmcnt(9)\n" \
"v_mfma_f32_16x16x32_bf16 a[8:11], a[232:235], a[192:195], a[8:11]\n" \
"ds_read_b64_tr_b16 a[178:179], v36 offset:33792\n" \
"s_waitcnt lgkmcnt(8)\n" \
"v_mfma_f32_16x16x32_bf16 a[12:15], a[172:175], a[128:131], a[12:15]\n" \
"ds_read_b64_tr_b16 a[240:241], v36 offset:40960\n" \
"v_accvgpr_read_b32 v184, a0\n" \
"s_waitcnt lgkmcnt(7)\n" \
"v_mfma_f32_16x16x32_bf16 a[12:15], a[236:239], a[192:195], a[12:15]\n" \
"v_accvgpr_read_b32 v185, a1\n" \
"v_accvgpr_read_b32 v186, a2\n" \
"v_mfma_f32_16x16x32_bf16 a[28:31], a[172:175], a[132:135], a[28:31]\n" \
"v_accvgpr_read_b32 v187, a3\n" \
"v_accvgpr_read_b32 v180, a4\n" \
"v_mfma_f32_16x16x32_bf16 a[28:31], a[236:239], a[196:199], a[28:31]\n" \
"s_add_u32 m0, s30, 0x20000\n" \
"s_movk_i32 s84, 0x100\n" \
"v_mfma_f32_16x16x32_bf16 a[24:27], a[168:171], a[132:135], a[24:27]\n" \
"buffer_load_dwordx4 v24, s[76:79], s84 offen offset:0 lds\n" \
"v_mfma_f32_16x16x32_bf16 a[24:27], a[232:235], a[196:199], a[24:27]\n" \
"ds_read_b64_tr_b16 a[242:243], v36 offset:41984\n" \
"v_accvgpr_read_b32 v181, a5\n" \
"v_mfma_f32_16x16x32_bf16 a[20:23], a[164:167], a[132:135], a[20:23]\n" \
"v_accvgpr_read_b32 v182, a6\n" \
"v_accvgpr_read_b32 v183, a7\n" \
"v_mfma_f32_16x16x32_bf16 a[20:23], a[228:231], a[196:199], a[20:23]\n" \
"ds_read_b64_tr_b16 a[180:181], v37 offset:32768\n" \
"v_cvt_pk_bf16_f32 v184, v184, v185\n" \
"v_mfma_f32_16x16x32_bf16 a[16:19], a[160:163], a[132:135], a[16:19]\n" \
"s_movk_i32 s84, 0x2100\n" \
"s_nop 0\n" \
"v_mfma_f32_16x16x32_bf16 a[16:19], a[224:227], a[196:199], a[16:19]\n" \
"buffer_load_dwordx4 v25, s[76:79], s84 offen offset:1024 lds\n" \
"v_mfma_f32_16x16x32_bf16 a[32:35], a[160:163], a[136:139], a[32:35]\n" \
"ds_read_b64_tr_b16 a[182:183], v37 offset:33792\n" \
"v_cvt_pk_bf16_f32 v185, v186, v187\n" \
"v_mfma_f32_16x16x32_bf16 a[32:35], a[224:227], a[200:203], a[32:35]\n" \
"v_cvt_pk_bf16_f32 v186, v180, v181\n" \
"v_cvt_pk_bf16_f32 v187, v182, v183\n" \
"v_mfma_f32_16x16x32_bf16 a[36:39], a[164:167], a[136:139], a[36:39]\n" \
"v_accvgpr_read_b32 v188, a8\n" \
"v_accvgpr_read_b32 v189, a9\n" \
"v_mfma_f32_16x16x32_bf16 a[36:39], a[228:231], a[200:203], a[36:39]\n" \
"s_movk_i32 s84, 0x4100\n" \
"s_nop 0\n" \
"v_mfma_f32_16x16x32_bf16 a[40:43], a[168:171], a[136:139], a[40:43]\n" \
"buffer_load_dwordx4 v24, s[76:79], s84 offen offset:2048 lds\n" \
"v_mfma_f32_16x16x32_bf16 a[40:43], a[232:235], a[200:203], a[40:43]\n" \
"ds_read_b64_tr_b16 a[244:245], v37 offset:40960\n" \
"v_accvgpr_read_b32 v190, a10\n" \
"v_mfma_f32_16x16x32_bf16 a[44:47], a[172:175], a[136:139], a[44:47]\n" \
"ds_read_b64_tr_b16 a[246:247], v37 offset:41984\n" \
"v_accvgpr_read_b32 v191, a11\n" \
"v_mfma_f32_16x16x32_bf16 a[44:47], a[236:239], a[200:203], a[44:47]\n" \
"v_accvgpr_read_b32 v180, a12\n" \
"v_accvgpr_read_b32 v181, a13\n" \
"v_mfma_f32_16x16x32_bf16 a[60:63], a[172:175], a[140:143], a[60:63]\n" \
"s_movk_i32 s84, 0x6100\n" \
"v_mfma_f32_16x16x32_bf16 a[60:63], a[236:239], a[204:207], a[60:63]\n" \
"buffer_load_dwordx4 v25, s[76:79], s84 offen offset:3072 lds\n" \
"v_mfma_f32_16x16x32_bf16 a[56:59], a[168:171], a[140:143], a[56:59]\n" \
"ds_read_b64_tr_b16 a[184:185], v38 offset:32768\n" \
"v_accvgpr_read_b32 v182, a14\n" \
"v_mfma_f32_16x16x32_bf16 a[56:59], a[232:235], a[204:207], a[56:59]\n" \
"v_accvgpr_read_b32 v183, a15\n" \
"v_cvt_pk_bf16_f32 v188, v188, v189\n" \
"v_mfma_f32_16x16x32_bf16 a[52:55], a[164:167], a[140:143], a[52:55]\n" \
"s_add_u32 m0, s30, 0x21000\n" \
"s_addk_i32 s84, 0x3000\n" \
"v_mfma_f32_16x16x32_bf16 a[52:55], a[228:231], a[204:207], a[52:55]\n" \
"buffer_load_dwordx4 v24, s[76:79], s84 offen offset:0 lds\n" \
"v_mfma_f32_16x16x32_bf16 a[48:51], a[160:163], a[140:143], a[48:51]\n" \
"ds_read_b64_tr_b16 a[186:187], v38 offset:33792\n" \
"v_cvt_pk_bf16_f32 v189, v190, v191\n" \
"v_mfma_f32_16x16x32_bf16 a[48:51], a[224:227], a[204:207], a[48:51]\n" \
"v_cvt_pk_bf16_f32 v190, v180, v181\n" \
"v_cvt_pk_bf16_f32 v191, v182, v183\n" \
"v_mfma_f32_16x16x32_bf16 a[64:67], a[160:163], a[144:147], a[64:67]\n" \
"ds_read_b64_tr_b16 a[248:249], v38 offset:40960\n" \
"v_accvgpr_read_b32 v192, a24\n" \
"v_mfma_f32_16x16x32_bf16 a[64:67], a[224:227], a[208:211], a[64:67]\n" \
"s_addk_i32 s84, 0x2000\n" \
"s_nop 0\n" \
"v_mfma_f32_16x16x32_bf16 a[68:71], a[164:167], a[144:147], a[68:71]\n" \
"buffer_load_dwordx4 v25, s[76:79], s84 offen offset:1024 lds\n" \
"v_mfma_f32_16x16x32_bf16 a[68:71], a[228:231], a[208:211], a[68:71]\n" \
"ds_read_b64_tr_b16 a[250:251], v38 offset:41984\n" \
"v_accvgpr_read_b32 v193, a25\n" \
"v_mfma_f32_16x16x32_bf16 a[72:75], a[168:171], a[144:147], a[72:75]\n" \
"v_accvgpr_read_b32 v194, a26\n" \
"v_accvgpr_read_b32 v195, a27\n" \
"v_mfma_f32_16x16x32_bf16 a[72:75], a[232:235], a[208:211], a[72:75]\n" \
"v_accvgpr_read_b32 v180, a28\n" \
"v_accvgpr_read_b32 v181, a29\n" \
"v_mfma_f32_16x16x32_bf16 a[76:79], a[172:175], a[144:147], a[76:79]\n" \
"s_addk_i32 s84, 0x2000\n" \
"v_mfma_f32_16x16x32_bf16 a[76:79], a[236:239], a[208:211], a[76:79]\n" \
"buffer_load_dwordx4 v24, s[76:79], s84 offen offset:2048 lds\n" \
"v_mfma_f32_16x16x32_bf16 a[92:95], a[172:175], a[148:151], a[92:95]\n" \
"s_add_u32 s89, s41, 0x0\n" \
"buffer_store_dwordx4 v[184:187], v20, s[80:83], s89 offen offset:0 sc1\n" \
"v_mfma_f32_16x16x32_bf16 a[92:95], a[236:239], a[212:215], a[92:95]\n" \
"ds_read_b64_tr_b16 a[188:189], v39 offset:32768\n" \
"v_accvgpr_read_b32 v182, a30\n" \
"v_mfma_f32_16x16x32_bf16 a[88:91], a[168:171], a[148:151], a[88:91]\n" \
"ds_read_b64_tr_b16 a[190:191], v39 offset:33792\n" \
"v_accvgpr_read_b32 v183, a31\n" \
"v_mfma_f32_16x16x32_bf16 a[88:91], a[232:235], a[212:215], a[88:91]\n" \
"s_addk_i32 s84, 0x2000\n" \
"v_mfma_f32_16x16x32_bf16 a[84:87], a[164:167], a[148:151], a[84:87]\n" \
"buffer_load_dwordx4 v25, s[76:79], s84 offen offset:3072 lds\n" \
"v_mfma_f32_16x16x32_bf16 a[84:87], a[228:231], a[212:215], a[84:87]\n" \
"ds_read_b64_tr_b16 a[252:253], v39 offset:40960\n" \
"v_cvt_pk_bf16_f32 v192, v192, v193\n" \
"v_mfma_f32_16x16x32_bf16 a[80:83], a[160:163], a[148:151], a[80:83]\n" \
"s_add_u32 s88, s41, 0x0\n" \
"buffer_store_dwordx4 v[188:191], v20, s[80:83], s88 offen offset:1024 sc1\n" \
"v_mfma_f32_16x16x32_bf16 a[80:83], a[224:227], a[212:215], a[80:83]\n" \
"v_cvt_pk_bf16_f32 v193, v194, v195\n" \
"v_cvt_pk_bf16_f32 v194, v180, v181\n" \
"s_waitcnt lgkmcnt(15)\n" \
"s_nop 0\n" \
"v_mfma_f32_16x16x32_bf16 a[96:99], a[160:163], a[152:155], a[96:99]\n" \
"ds_read_b64_tr_b16 a[254:255], v39 offset:41984\n" \
"v_cvt_pk_bf16_f32 v195, v182, v183\n" \
"v_mfma_f32_16x16x32_bf16 a[96:99], a[224:227], a[216:219], a[96:99]\n" \
"v_accvgpr_read_b32 v196, a16\n" \
"v_accvgpr_read_b32 v197, a17\n" \
"v_mfma_f32_16x16x32_bf16 a[100:103], a[164:167], a[152:155], a[100:103]\n" \
"v_accvgpr_read_b32 v198, a18\n" \
"v_accvgpr_read_b32 v199, a19\n" \
"v_mfma_f32_16x16x32_bf16 a[100:103], a[228:231], a[216:219], a[100:103]\n" \
"v_accvgpr_read_b32 v180, a20\n" \
"v_accvgpr_read_b32 v181, a21\n" \
"v_mfma_f32_16x16x32_bf16 a[104:107], a[168:171], a[152:155], a[104:107]\n" \
"s_add_u32 s89, s41, 0x1000\n" \
"buffer_store_dwordx4 v[192:195], v20, s[80:83], s89 offen offset:1024 sc1\n" \
"v_mfma_f32_16x16x32_bf16 a[104:107], a[232:235], a[216:219], a[104:107]\n" \
"v_accvgpr_read_b32 v182, a22\n" \
"v_accvgpr_read_b32 v183, a23\n" \
"v_mfma_f32_16x16x32_bf16 a[108:111], a[172:175], a[152:155], a[108:111]\n" \
"v_cvt_pk_bf16_f32 v196, v196, v197\n" \
"v_cvt_pk_bf16_f32 v197, v198, v199\n" \
"v_mfma_f32_16x16x32_bf16 a[108:111], a[236:239], a[216:219], a[108:111]\n" \
"v_cvt_pk_bf16_f32 v198, v180, v181\n" \
"v_cvt_pk_bf16_f32 v199, v182, v183\n" \
"v_mfma_f32_16x16x32_bf16 a[124:127], a[172:175], a[156:159], a[124:127]\n" \
"v_accvgpr_read_b32 v200, a32\n" \
"v_accvgpr_read_b32 v201, a33\n" \
"v_mfma_f32_16x16x32_bf16 a[124:127], a[236:239], a[220:223], a[124:127]\n" \
"v_accvgpr_read_b32 v202, a34\n" \
"v_accvgpr_read_b32 v203, a35\n" \
"v_mfma_f32_16x16x32_bf16 a[120:123], a[168:171], a[156:159], a[120:123]\n" \
"s_add_u32 s88, s41, 0x1000\n" \
"buffer_store_dwordx4 v[196:199], v20, s[80:83], s88 offen offset:0 sc1\n" \
"v_mfma_f32_16x16x32_bf16 a[120:123], a[232:235], a[220:223], a[120:123]\n" \
"v_accvgpr_read_b32 v180, a36\n" \
"v_accvgpr_read_b32 v181, a37\n" \
"v_mfma_f32_16x16x32_bf16 a[116:119], a[164:167], a[156:159], a[116:119]\n" \
"v_accvgpr_read_b32 v182, a38\n" \
"v_accvgpr_read_b32 v183, a39\n" \
"v_mfma_f32_16x16x32_bf16 a[116:119], a[228:231], a[220:223], a[116:119]\n" \
"v_cvt_pk_bf16_f32 v200, v200, v201\n" \
"v_cvt_pk_bf16_f32 v201, v202, v203\n" \
"v_mfma_f32_16x16x32_bf16 a[112:115], a[160:163], a[156:159], a[112:115]\n" \
"v_cvt_pk_bf16_f32 v202, v180, v181\n" \
"v_cvt_pk_bf16_f32 v203, v182, v183\n" \
"v_mfma_f32_16x16x32_bf16 a[112:115], a[224:227], a[220:223], a[112:115]\n" \
"v_accvgpr_read_b32 v204, a40\n" \
"v_accvgpr_read_b32 v205, a41\n" \
"s_waitcnt vmcnt(12) lgkmcnt(0)\n" \
"s_barrier\n" \
"v_mov_b32_e32 v36, v32\n" \
"v_xor_b32_e32 v32, 0x0, v48\n" \
"v_mov_b32_e32 v37, v33\n" \
"v_xor_b32_e32 v33, 0x20, v48\n" \
"v_mov_b32_e32 v38, v34\n" \
"v_xor_b32_e32 v34, 0x40, v48\n" \
"v_mov_b32_e64 v39, v35\n" \
"v_xor_b32_e32 v35, 0x60, v48\n" \
"v_add_u32_e32 v22, s44, v28\n" \
"v_add_u32_e32 v28, s43, v28\n" \
"v_add_u32_e32 v23, s44, v29\n" \
"v_add_u32_e32 v29, s43, v29\n" \
"v_mfma_f32_16x16x32_bf16 v[52:55], a[176:179], a[128:131], v[52:55]\n" \
"s_add_u32 m0, s40, 0x8000\n" \
"s_add_u32 s85, s39, 0x2000\n" \
"v_mfma_f32_16x16x32_bf16 v[52:55], a[240:243], a[192:195], v[52:55]\n" \
"buffer_load_dwordx4 v20, s[80:83], s85 offen sc1 lds\n" \
"v_mfma_f32_16x16x32_bf16 v[56:59], a[180:183], a[128:131], v[56:59]\n" \
"ds_read_b64_tr_b16 a[160:161], v32 offset:0\n" \
"v_accvgpr_read_b32 v206, a42\n" \
"v_mfma_f32_16x16x32_bf16 v[56:59], a[244:247], a[192:195], v[56:59]\n" \
"s_add_u32 m0, s40, 0x9000\n" \
"s_add_u32 s85, s39, 0x3000\n" \
"v_mfma_f32_16x16x32_bf16 v[60:63], a[184:187], a[128:131], v[60:63]\n" \
"buffer_load_dwordx4 v20, s[80:83], s85 offen sc1 lds\n" \
"v_mfma_f32_16x16x32_bf16 v[60:63], a[248:251], a[192:195], v[60:63]\n" \
"ds_read_b64_tr_b16 a[162:163], v32 offset:512\n" \
"v_accvgpr_read_b32 v207, a43\n" \
"v_mfma_f32_16x16x32_bf16 v[64:67], a[188:191], a[128:131], v[64:67]\n" \
"ds_read_b64_tr_b16 a[224:225], v32 offset:4096\n" \
"v_accvgpr_read_b32 v180, a44\n" \
"v_mfma_f32_16x16x32_bf16 v[64:67], a[252:255], a[192:195], v[64:67]\n" \
"ds_read_b128 a[128:131], v28 offset:0\n" \
"v_accvgpr_read_b32 v181, a45\n" \
"v_mfma_f32_16x16x32_bf16 v[80:83], a[188:191], a[132:135], v[80:83]\n" \
"ds_read_b128 a[192:195], v29 offset:0\n" \
"v_accvgpr_read_b32 v182, a46\n" \
"v_mfma_f32_16x16x32_bf16 v[80:83], a[252:255], a[196:199], v[80:83]\n" \
"s_add_u32 m0, s40, 0xb000\n" \
"s_movk_i32 s84, 0x80\n" \
"v_mfma_f32_16x16x32_bf16 v[76:79], a[184:187], a[132:135], v[76:79]\n" \
"buffer_load_dwordx4 v21, s[76:79], s84 offen offset:0 lds\n" \
"v_mfma_f32_16x16x32_bf16 v[76:79], a[248:251], a[196:199], v[76:79]\n" \
"s_add_u32 m0, s30, 0x8000\n" \
"s_movk_i32 s84, 0x80\n" \
"v_mfma_f32_16x16x32_bf16 v[72:75], a[180:183], a[132:135], v[72:75]\n" \
"buffer_load_dwordx4 v24, s[76:79], s84 offen offset:0 lds\n" \
"v_mfma_f32_16x16x32_bf16 v[72:75], a[244:247], a[196:199], v[72:75]\n" \
"ds_read_b64_tr_b16 a[226:227], v32 offset:4608\n" \
"v_accvgpr_read_b32 v183, a47\n" \
"v_mfma_f32_16x16x32_bf16 v[68:71], a[176:179], a[132:135], v[68:71]\n" \
"ds_read_b64_tr_b16 a[164:165], v33 offset:0\n" \
"v_cvt_pk_bf16_f32 v204, v204, v205\n" \
"v_mfma_f32_16x16x32_bf16 v[68:71], a[240:243], a[196:199], v[68:71]\n" \
"ds_read_b128 a[132:135], v28 offset:2048\n" \
"v_cvt_pk_bf16_f32 v205, v206, v207\n" \
"v_mfma_f32_16x16x32_bf16 v[84:87], a[176:179], a[136:139], v[84:87]\n" \
"ds_read_b128 a[196:199], v29 offset:2048\n" \
"v_cvt_pk_bf16_f32 v206, v180, v181\n" \
"v_mfma_f32_16x16x32_bf16 v[84:87], a[240:243], a[200:203], v[84:87]\n" \
"s_movk_i32 s84, 0x2080\n" \
"v_mfma_f32_16x16x32_bf16 v[88:91], a[180:183], a[136:139], v[88:91]\n" \
"buffer_load_dwordx4 v25, s[76:79], s84 offen offset:1024 lds\n" \
"v_mfma_f32_16x16x32_bf16 v[88:91], a[244:247], a[200:203], v[88:91]\n" \
"s_movk_i32 s84, 0x4080\n" \
"v_mfma_f32_16x16x32_bf16 v[92:95], a[184:187], a[136:139], v[92:95]\n" \
"buffer_load_dwordx4 v24, s[76:79], s84 offen offset:2048 lds\n" \
"v_mfma_f32_16x16x32_bf16 v[92:95], a[248:251], a[200:203], v[92:95]\n" \
"ds_read_b64_tr_b16 a[166:167], v33 offset:512\n" \
"v_cvt_pk_bf16_f32 v207, v182, v183\n" \
"v_mfma_f32_16x16x32_bf16 v[96:99], a[188:191], a[136:139], v[96:99]\n" \
"ds_read_b64_tr_b16 a[228:229], v33 offset:4096\n" \
"v_accvgpr_read_b32 v208, a56\n" \
"v_mfma_f32_16x16x32_bf16 v[96:99], a[252:255], a[200:203], v[96:99]\n" \
"ds_read_b128 a[136:139], v28 offset:4096\n" \
"v_accvgpr_read_b32 v209, a57\n" \
"v_mfma_f32_16x16x32_bf16 v[112:115], a[188:191], a[140:143], v[112:115]\n" \
"ds_read_b128 a[200:203], v29 offset:4096\n" \
"v_accvgpr_read_b32 v210, a58\n" \
"v_mfma_f32_16x16x32_bf16 v[112:115], a[252:255], a[204:207], v[112:115]\n" \
"s_movk_i32 s84, 0x6080\n" \
"v_mfma_f32_16x16x32_bf16 v[108:111], a[184:187], a[140:143], v[108:111]\n" \
"buffer_load_dwordx4 v25, s[76:79], s84 offen offset:3072 lds\n" \
"v_mfma_f32_16x16x32_bf16 v[108:111], a[248:251], a[204:207], v[108:111]\n" \
"s_add_u32 m0, s31, 0x8000\n" \
"s_mov_b32 s84, 0x80000\n" \
"v_mfma_f32_16x16x32_bf16 v[104:107], a[180:183], a[140:143], v[104:107]\n" \
"buffer_load_dwordx4 v26, s[68:71], s84 offen offset:0 lds\n" \
"v_mfma_f32_16x16x32_bf16 v[104:107], a[244:247], a[204:207], v[104:107]\n" \
"s_addk_i32 s84, 0x7c00\n" \
"v_mfma_f32_16x16x32_bf16 v[100:103], a[176:179], a[140:143], v[100:103]\n" \
"buffer_load_dwordx4 v26, s[68:71], s84 offen offset:1024 lds\n" \
"v_mfma_f32_16x16x32_bf16 v[100:103], a[240:243], a[204:207], v[100:103]\n" \
"ds_read_b128 a[140:143], v28 offset:6144\n" \
"v_accvgpr_read_b32 v211, a59\n" \
"v_mfma_f32_16x16x32_bf16 v[116:119], a[176:179], a[144:147], v[116:119]\n" \
"ds_read_b128 a[204:207], v29 offset:6144\n" \
"v_accvgpr_read_b32 v180, a60\n" \
"v_mfma_f32_16x16x32_bf16 v[116:119], a[240:243], a[208:211], v[116:119]\n" \
"s_addk_i32 s84, 0x7c00\n" \
"v_mfma_f32_16x16x32_bf16 v[120:123], a[180:183], a[144:147], v[120:123]\n" \
"buffer_load_dwordx4 v27, s[68:71], s84 offen offset:2048 lds\n" \
"v_mfma_f32_16x16x32_bf16 v[120:123], a[244:247], a[208:211], v[120:123]\n" \
"ds_read_b64_tr_b16 a[230:231], v33 offset:4608\n" \
"v_accvgpr_read_b32 v181, a61\n" \
"v_mfma_f32_16x16x32_bf16 v[124:127], a[184:187], a[144:147], v[124:127]\n" \
"s_addk_i32 s84, 0x7c00\n" \
"v_mfma_f32_16x16x32_bf16 v[124:127], a[248:251], a[208:211], v[124:127]\n" \
"buffer_load_dwordx4 v27, s[68:71], s84 offen offset:3072 lds\n" \
"v_mfma_f32_16x16x32_bf16 v[128:131], a[188:191], a[144:147], v[128:131]\n" \
"ds_read_b64_tr_b16 a[172:173], v35 offset:0\n" \
"v_accvgpr_read_b32 v182, a62\n" \
"v_mfma_f32_16x16x32_bf16 v[128:131], a[252:255], a[208:211], v[128:131]\n" \
"ds_read_b128 a[144:147], v28 offset:8192\n" \
"v_accvgpr_read_b32 v183, a63\n" \
"v_mfma_f32_16x16x32_bf16 v[144:147], a[188:191], a[148:151], v[144:147]\n" \
"ds_read_b128 a[208:211], v29 offset:8192\n" \
"v_cvt_pk_bf16_f32 v208, v208, v209\n" \
"v_mfma_f32_16x16x32_bf16 v[144:147], a[252:255], a[212:215], v[144:147]\n" \
"s_add_u32 m0, s30, 0x9000\n" \
"s_mov_b32 s84, 0x9080\n" \
"v_mfma_f32_16x16x32_bf16 v[140:143], a[184:187], a[148:151], v[140:143]\n" \
"buffer_load_dwordx4 v24, s[76:79], s84 offen offset:0 lds\n" \
"v_mfma_f32_16x16x32_bf16 v[140:143], a[248:251], a[212:215], v[140:143]\n" \
"ds_read_b64_tr_b16 a[174:175], v35 offset:512\n" \
"v_cvt_pk_bf16_f32 v209, v210, v211\n" \
"v_mfma_f32_16x16x32_bf16 v[136:139], a[180:183], a[148:151], v[136:139]\n" \
"s_addk_i32 s84, 0x2000\n" \
"s_nop 0\n" \
"v_mfma_f32_16x16x32_bf16 v[136:139], a[244:247], a[212:215], v[136:139]\n" \
"buffer_load_dwordx4 v25, s[76:79], s84 offen offset:1024 lds\n" \
"v_mfma_f32_16x16x32_bf16 v[132:135], a[176:179], a[148:151], v[132:135]\n" \
"ds_read_b64_tr_b16 a[236:237], v35 offset:4096\n" \
"v_cvt_pk_bf16_f32 v210, v180, v181\n" \
"v_mfma_f32_16x16x32_bf16 v[132:135], a[240:243], a[212:215], v[132:135]\n" \
"ds_read_b128 a[148:151], v28 offset:10240\n" \
"v_cvt_pk_bf16_f32 v211, v182, v183\n" \
"v_mfma_f32_16x16x32_bf16 v[148:151], a[176:179], a[152:155], v[148:151]\n" \
"ds_read_b128 a[212:215], v29 offset:10240\n" \
"v_accvgpr_read_b32 v212, a48\n" \
"v_mfma_f32_16x16x32_bf16 v[148:151], a[240:243], a[216:219], v[148:151]\n" \
"ds_read_b64_tr_b16 a[238:239], v35 offset:4608\n" \
"v_accvgpr_read_b32 v213, a49\n" \
"v_mfma_f32_16x16x32_bf16 v[152:155], a[180:183], a[152:155], v[152:155]\n" \
"ds_read_b64_tr_b16 a[168:169], v34 offset:0\n" \
"v_accvgpr_read_b32 v214, a50\n" \
"v_mfma_f32_16x16x32_bf16 v[152:155], a[244:247], a[216:219], v[152:155]\n" \
"ds_read_b64_tr_b16 a[170:171], v34 offset:512\n" \
"v_accvgpr_read_b32 v215, a51\n" \
"v_mfma_f32_16x16x32_bf16 v[156:159], a[184:187], a[152:155], v[156:159]\n" \
"ds_read_b64_tr_b16 a[232:233], v34 offset:4096\n" \
"v_accvgpr_read_b32 v180, a52\n" \
"v_mfma_f32_16x16x32_bf16 v[156:159], a[248:251], a[216:219], v[156:159]\n" \
"ds_read_b64_tr_b16 a[234:235], v34 offset:4608\n" \
"v_accvgpr_read_b32 v181, a53\n" \
"v_mfma_f32_16x16x32_bf16 v[160:163], a[188:191], a[152:155], v[160:163]\n" \
"ds_read_b128 v[248:251], v22 offset:0\n" \
"v_accvgpr_read_b32 v182, a54\n" \
"v_mfma_f32_16x16x32_bf16 v[160:163], a[252:255], a[216:219], v[160:163]\n" \
"ds_read_b128 a[152:155], v28 offset:12288\n" \
"v_accvgpr_read_b32 v183, a55\n" \
"v_mfma_f32_16x16x32_bf16 v[176:179], a[188:191], a[156:159], v[176:179]\n" \
"ds_read_b128 a[216:219], v29 offset:12288\n" \
"v_cvt_pk_bf16_f32 v212, v212, v213\n" \
"v_mfma_f32_16x16x32_bf16 v[176:179], a[252:255], a[220:223], v[176:179]\n" \
"ds_read_b128 v[48:51], v23 offset:0\n" \
"v_cvt_pk_bf16_f32 v213, v214, v215\n" \
"v_mfma_f32_16x16x32_bf16 v[172:175], a[184:187], a[156:159], v[172:175]\n" \
"v_cvt_pk_bf16_f32 v214, v180, v181\n" \
"v_cvt_pk_bf16_f32 v215, v182, v183\n" \
"v_mfma_f32_16x16x32_bf16 v[172:175], a[248:251], a[220:223], v[172:175]\n" \
"v_accvgpr_read_b32 v216, a64\n" \
"v_accvgpr_read_b32 v217, a65\n" \
"v_mfma_f32_16x16x32_bf16 v[168:171], a[180:183], a[156:159], v[168:171]\n" \
"v_accvgpr_read_b32 v218, a66\n" \
"v_accvgpr_read_b32 v219, a67\n" \
"v_mfma_f32_16x16x32_bf16 v[168:171], a[244:247], a[220:223], v[168:171]\n" \
"v_accvgpr_read_b32 v180, a68\n" \
"v_accvgpr_read_b32 v181, a69\n" \
"v_mfma_f32_16x16x32_bf16 v[164:167], a[176:179], a[156:159], v[164:167]\n" \
"v_accvgpr_read_b32 v182, a70\n" \
"v_accvgpr_read_b32 v183, a71\n" \
"v_mfma_f32_16x16x32_bf16 v[164:167], a[240:243], a[220:223], v[164:167]\n" \
"ds_read_b128 a[156:159], v28 offset:14336\n" \
"ds_read_b128 a[220:223], v29 offset:14336\n" \
"s_waitcnt lgkmcnt(15)\n" \
"v_mfma_f32_16x16x32_bf16 a[0:3], a[160:163], a[128:131], 0\n" \
"s_addk_i32 s84, 0x2000\n" \
"v_cvt_pk_bf16_f32 v216, v216, v217\n" \
"v_mfma_f32_16x16x32_bf16 a[0:3], a[224:227], a[192:195], a[0:3]\n" \
"buffer_load_dwordx4 v24, s[76:79], s84 offen offset:2048 lds\n" \
"v_cvt_pk_bf16_f32 v217, v218, v219\n" \
"v_mfma_f32_16x16x32_bf16 a[4:7], a[164:167], a[128:131], 0\n" \
"ds_read_b64_tr_b16 a[176:177], v36 offset:0\n" \
"v_cvt_pk_bf16_f32 v218, v180, v181\n" \
"v_mfma_f32_16x16x32_bf16 a[4:7], a[228:231], a[192:195], a[4:7]\n" \
"s_addk_i32 s84, 0x2000\n" \
"v_cvt_pk_bf16_f32 v219, v182, v183\n" \
"s_waitcnt lgkmcnt(15)\n" \
"v_mfma_f32_16x16x32_bf16 a[12:15], a[172:175], a[128:131], 0\n" \
"buffer_load_dwordx4 v25, s[76:79], s84 offen offset:3072 lds\n" \
"v_accvgpr_read_b32 v220, a80\n" \
"s_waitcnt lgkmcnt(11)\n" \
"v_mfma_f32_16x16x32_bf16 a[12:15], a[236:239], a[192:195], a[12:15]\n" \
"ds_read_b64_tr_b16 a[178:179], v36 offset:1024\n" \
"v_accvgpr_read_b32 v221, a81\n" \
"s_waitcnt lgkmcnt(10)\n" \
"v_mfma_f32_16x16x32_bf16 a[8:11], a[128:131], a[168:171], 0\n" \
"ds_read_b64_tr_b16 a[240:241], v36 offset:8192\n" \
"v_accvgpr_read_b32 v222, a82\n" \
"s_waitcnt lgkmcnt(9)\n" \
"s_nop 0\n" \
"v_mfma_f32_16x16x32_bf16 a[8:11], a[192:195], a[232:235], a[8:11]\n" \
"ds_read_b64_tr_b16 a[242:243], v36 offset:9216\n" \
"v_accvgpr_read_b32 v223, a83\n" \
"v_mfma_f32_16x16x32_bf16 a[24:27], a[132:135], a[168:171], 0\n" \
"ds_read_b64_tr_b16 a[180:181], v37 offset:0\n" \
"v_accvgpr_read_b32 v180, a84\n" \
"v_mfma_f32_16x16x32_bf16 a[24:27], a[196:199], a[232:235], a[24:27]\n" \
"ds_read_b64_tr_b16 a[182:183], v37 offset:1024\n" \
"v_accvgpr_read_b32 v181, a85\n" \
"v_mfma_f32_16x16x32_bf16 a[28:31], a[172:175], a[132:135], 0\n" \
"ds_read_b64_tr_b16 a[244:245], v37 offset:8192\n" \
"v_accvgpr_read_b32 v182, a86\n" \
"v_mfma_f32_16x16x32_bf16 a[28:31], a[236:239], a[196:199], a[28:31]\n" \
"s_add_u32 s89, s41, 0x2000\n" \
"buffer_store_dwordx4 v[200:203], v20, s[80:83], s89 offen offset:0 sc1\n" \
"v_mfma_f32_16x16x32_bf16 a[20:23], a[164:167], a[132:135], 0\n" \
"ds_read_b64_tr_b16 a[246:247], v37 offset:9216\n" \
"v_accvgpr_read_b32 v183, a87\n" \
"v_mfma_f32_16x16x32_bf16 a[20:23], a[228:231], a[196:199], a[20:23]\n" \
"ds_read_b64_tr_b16 a[184:185], v38 offset:0\n" \
"v_cvt_pk_bf16_f32 v220, v220, v221\n" \
"v_mfma_f32_16x16x32_bf16 a[16:19], a[160:163], a[132:135], 0\n" \
"ds_read_b64_tr_b16 a[186:187], v38 offset:1024\n" \
"v_cvt_pk_bf16_f32 v221, v222, v223\n" \
"v_mfma_f32_16x16x32_bf16 a[16:19], a[224:227], a[196:199], a[16:19]\n" \
"v_cvt_pk_bf16_f32 v222, v180, v181\n" \
"v_cvt_pk_bf16_f32 v223, v182, v183\n" \
"v_cvt_pk_bf16_f32 v224, v52, v53\n" \
".fill (L_pb + 20192 - .) / 4, 4, 0xBF800000\n" \
"L_pp5:\n" \
"s_cbranch_execz L_pret\n" \
"s_nop 0\n" \
"v_mfma_f32_16x16x32_bf16 a[32:35], a[160:163], a[136:139], 0\n" \
"s_add_u32 s88, s41, 0x2000\n" \
"buffer_store_dwordx4 v[204:207], v20, s[80:83], s88 offen offset:1024 sc1\n" \
"v_mfma_f32_16x16x32_bf16 a[32:35], a[224:227], a[200:203], a[32:35]\n" \
"ds_read_b64_tr_b16 a[248:249], v38 offset:8192\n" \
"v_cvt_pk_bf16_f32 v225, v54, v55\n" \
"v_mfma_f32_16x16x32_bf16 a[36:39], a[164:167], a[136:139], 0\n" \
"ds_read_b64_tr_b16 a[250:251], v38 offset:9216\n" \
"v_cvt_pk_bf16_f32 v226, v56, v57\n" \
"v_mfma_f32_16x16x32_bf16 a[36:39], a[228:231], a[200:203], a[36:39]\n" \
"ds_read_b64_tr_b16 a[188:189], v39 offset:0\n" \
"v_cvt_pk_bf16_f32 v227, v58, v59\n" \
"v_mfma_f32_16x16x32_bf16 a[44:47], a[172:175], a[136:139], 0\n" \
"v_cvt_pk_bf16_f32 v228, v60, v61\n" \
"v_cvt_pk_bf16_f32 v229, v62, v63\n" \
"v_cvt_pk_bf16_f32 v230, v64, v65\n" \
"v_mfma_f32_16x16x32_bf16 a[44:47], a[236:239], a[200:203], a[44:47]\n" \
"ds_read_b64_tr_b16 a[190:191], v39 offset:1024\n" \
"v_cvt_pk_bf16_f32 v231, v66, v67\n" \
"v_mfma_f32_16x16x32_bf16 a[40:43], a[136:139], a[168:171], 0\n" \
"s_add_u32 s89, s41, 0x3000\n" \
"buffer_store_dwordx4 v[208:211], v20, s[80:83], s89 offen offset:1024 sc1\n" \
"v_mfma_f32_16x16x32_bf16 a[40:43], a[200:203], a[232:235], a[40:43]\n" \
"ds_read_b64_tr_b16 a[252:253], v39 offset:8192\n" \
"v_cvt_pk_bf16_f32 v232, v76, v77\n" \
"v_mfma_f32_16x16x32_bf16 a[56:59], a[140:143], a[168:171], 0\n" \
"ds_read_b64_tr_b16 a[254:255], v39 offset:9216\n" \
"v_cvt_pk_bf16_f32 v233, v78, v79\n" \
"v_mfma_f32_16x16x32_bf16 a[56:59], a[204:207], a[232:235], a[56:59]\n" \
"v_cvt_pk_bf16_f32 v234, v80, v81\n" \
"v_cvt_pk_bf16_f32 v235, v82, v83\n" \
"v_cvt_pk_bf16_f32 v236, v68, v69\n" \
"v_mfma_f32_16x16x32_bf16 a[60:63], a[172:175], a[140:143], 0\n" \
"v_cvt_pk_bf16_f32 v237, v70, v71\n" \
"v_cvt_pk_bf16_f32 v238, v72, v73\n" \
"v_cvt_pk_bf16_f32 v239, v74, v75\n" \
"v_mfma_f32_16x16x32_bf16 a[60:63], a[236:239], a[204:207], a[60:63]\n" \
"s_add_u32 s88, s41, 0x3000\n" \
"buffer_store_dwordx4 v[212:215], v20, s[80:83], s88 offen offset:0 sc1\n" \
"v_mfma_f32_16x16x32_bf16 a[52:55], a[164:167], a[140:143], 0\n" \
"v_cvt_pk_bf16_f32 v240, v84, v85\n" \
"v_cvt_pk_bf16_f32 v241, v86, v87\n" \
"v_cvt_pk_bf16_f32 v242, v88, v89\n" \
"v_mfma_f32_16x16x32_bf16 a[52:55], a[228:231], a[204:207], a[52:55]\n" \
"v_cvt_pk_bf16_f32 v243, v90, v91\n" \
"v_cvt_pk_bf16_f32 v244, v92, v93\n" \
"v_cvt_pk_bf16_f32 v245, v94, v95\n" \
"v_mfma_f32_16x16x32_bf16 a[48:51], a[160:163], a[140:143], 0\n" \
"v_cvt_pk_bf16_f32 v246, v96, v97\n" \
"v_cvt_pk_bf16_f32 v247, v98, v99\n" \
"v_cvt_pk_bf16_f32 v184, v108, v109\n" \
"v_mfma_f32_16x16x32_bf16 a[48:51], a[224:227], a[204:207], a[48:51]\n" \
"v_cvt_pk_bf16_f32 v185, v110, v111\n" \
"v_cvt_pk_bf16_f32 v186, v112, v113\n" \
"v_cvt_pk_bf16_f32 v187, v114, v115\n" \
"s_waitcnt lgkmcnt(15)\n" \
"s_nop 0\n" \
"v_mfma_f32_16x16x32_bf16 a[64:67], a[164:167], v[248:251], 0\n" \
"v_cvt_pk_bf16_f32 v188, v100, v101\n" \
"v_cvt_pk_bf16_f32 v189, v102, v103\n" \
"v_cvt_pk_bf16_f32 v190, v104, v105\n" \
"v_mfma_f32_16x16x32_bf16 a[64:67], a[228:231], v[48:51], a[64:67]\n" \
"v_cvt_pk_bf16_f32 v191, v106, v107\n" \
"v_cvt_pk_bf16_f32 v192, v116, v117\n" \
"v_cvt_pk_bf16_f32 v193, v118, v119\n" \
"v_mfma_f32_16x16x32_bf16 a[80:83], a[172:175], v[248:251], 0\n" \
"v_cvt_pk_bf16_f32 v194, v120, v121\n" \
"v_cvt_pk_bf16_f32 v195, v122, v123\n" \
"v_cvt_pk_bf16_f32 v196, v124, v125\n" \
"v_mfma_f32_16x16x32_bf16 a[80:83], a[236:239], v[48:51], a[80:83]\n" \
"v_cvt_pk_bf16_f32 v197, v126, v127\n" \
"v_cvt_pk_bf16_f32 v198, v128, v129\n" \
"v_cvt_pk_bf16_f32 v199, v130, v131\n" \
"v_mfma_f32_16x16x32_bf16 a[68:71], v[248:251], a[168:171], 0\n" \
"v_cvt_pk_bf16_f32 v252, v140, v141\n" \
"v_cvt_pk_bf16_f32 v253, v142, v143\n" \
"v_cvt_pk_bf16_f32 v254, v144, v145\n" \
"v_mfma_f32_16x16x32_bf16 a[68:71], v[48:51], a[232:235], a[68:71]\n" \
"v_cvt_pk_bf16_f32 v255, v146, v147\n" \
"v_accvgpr_write_b32 a84, v252\n" \
"v_accvgpr_write_b32 a85, v253\n" \
"s_waitcnt vmcnt(4) lgkmcnt(0)\n" \
"s_barrier\n" \
"v_mfma_f32_16x16x32_bf16 v[52:55], a[176:179], a[128:131], 0\n" \
"s_add_u32 m0, s40, 0x0\n" \
"s_nop 0\n" \
"s_add_u32 s85, s39, 0x4000\n" \
"v_accvgpr_write_b32 a86, v254\n" \
"v_mfma_f32_16x16x32_bf16 v[52:55], a[240:243], a[192:195], v[52:55]\n" \
"buffer_load_dwordx4 v20, s[80:83], s85 offen sc1 lds\n" \
"v_accvgpr_write_b32 a87, v255\n" \
"v_mfma_f32_16x16x32_bf16 v[56:59], a[180:183], a[128:131], 0\n" \
"ds_read_b64_tr_b16 a[160:161], v32 offset:32768\n" \
"v_cvt_pk_bf16_f32 v252, v132, v133\n" \
"v_mfma_f32_16x16x32_bf16 v[56:59], a[244:247], a[192:195], v[56:59]\n" \
"ds_read_b64_tr_b16 a[162:163], v32 offset:33280\n" \
"v_cvt_pk_bf16_f32 v253, v134, v135\n" \
"v_mfma_f32_16x16x32_bf16 v[60:63], a[184:187], a[128:131], 0\n" \
"s_add_u32 m0, s40, 0x1000\n" \
"s_add_u32 s85, s39, 0x5000\n" \
"v_cvt_pk_bf16_f32 v254, v136, v137\n" \
"v_mfma_f32_16x16x32_bf16 v[60:63], a[248:251], a[192:195], v[60:63]\n" \
"buffer_load_dwordx4 v20, s[80:83], s85 offen sc1 lds\n" \
"v_cvt_pk_bf16_f32 v255, v138, v139\n" \
"v_mfma_f32_16x16x32_bf16 v[64:67], a[188:191], a[128:131], 0\n" \
"ds_read_b64_tr_b16 a[224:225], v32 offset:36864\n" \
"v_cvt_pk_bf16_f32 v200, v148, v149\n" \
"v_mfma_f32_16x16x32_bf16 v[64:67], a[252:255], a[192:195], v[64:67]\n" \
"ds_read_b128 a[128:131], v28 offset:32768\n" \
"v_cvt_pk_bf16_f32 v201, v150, v151\n" \
"v_mfma_f32_16x16x32_bf16 v[80:83], a[188:191], a[132:135], 0\n" \
"ds_read_b128 a[192:195], v29 offset:32768\n" \
"v_cvt_pk_bf16_f32 v202, v152, v153\n" \
"v_mfma_f32_16x16x32_bf16 v[80:83], a[252:255], a[196:199], v[80:83]\n" \
"s_add_u32 m0, s40, 0x3000\n" \
"s_movk_i32 s84, 0x100\n" \
"s_nop 0\n" \
"v_cvt_pk_bf16_f32 v203, v154, v155\n" \
"v_mfma_f32_16x16x32_bf16 v[76:79], a[184:187], a[132:135], 0\n" \
"buffer_load_dwordx4 v21, s[76:79], s84 offen offset:0 lds\n" \
"v_cvt_pk_bf16_f32 v204, v156, v157\n" \
"v_mfma_f32_16x16x32_bf16 v[76:79], a[248:251], a[196:199], v[76:79]\n" \
"ds_read_b64_tr_b16 a[226:227], v32 offset:37376\n" \
"v_cvt_pk_bf16_f32 v205, v158, v159\n" \
"v_mfma_f32_16x16x32_bf16 v[72:75], a[180:183], a[132:135], 0\n" \
"ds_read_b64_tr_b16 a[164:165], v33 offset:32768\n" \
"v_cvt_pk_bf16_f32 v206, v160, v161\n" \
"v_mfma_f32_16x16x32_bf16 v[72:75], a[244:247], a[196:199], v[72:75]\n" \
"v_cvt_pk_bf16_f32 v207, v162, v163\n" \
"v_cvt_pk_bf16_f32 v208, v172, v173\n" \
"v_cvt_pk_bf16_f32 v209, v174, v175\n" \
"v_mfma_f32_16x16x32_bf16 v[68:71], a[176:179], a[132:135], 0\n" \
"ds_read_b64_tr_b16 a[166:167], v33 offset:33280\n" \
"v_cvt_pk_bf16_f32 v210, v176, v177\n" \
"v_mfma_f32_16x16x32_bf16 v[68:71], a[240:243], a[196:199], v[68:71]\n" \
"ds_read_b128 a[132:135], v28 offset:34816\n" \
"v_cvt_pk_bf16_f32 v211, v178, v179\n" \
"v_mfma_f32_16x16x32_bf16 v[84:87], a[176:179], a[136:139], 0\n" \
"ds_read_b128 a[196:199], v29 offset:34816\n" \
"v_cvt_pk_bf16_f32 v212, v164, v165\n" \
"v_mfma_f32_16x16x32_bf16 v[84:87], a[240:243], a[200:203], v[84:87]\n" \
"s_add_u32 m0, s31, 0x0\n" \
"s_mov_b32 s84, 0x100000\n" \
"v_cvt_pk_bf16_f32 v213, v166, v167\n" \
"v_mfma_f32_16x16x32_bf16 v[88:91], a[180:183], a[136:139], 0\n" \
"buffer_load_dwordx4 v26, s[68:71], s84 offen offset:0 lds\n" \
"v_cvt_pk_bf16_f32 v214, v168, v169\n" \
"v_mfma_f32_16x16x32_bf16 v[88:91], a[244:247], a[200:203], v[88:91]\n" \
"s_add_u32 s89, s41, 0x0\n" \
"buffer_store_dwordx4 v[224:227], v20, s[80:83], s89 offen offset:2048 sc1\n" \
"v_mfma_f32_16x16x32_bf16 v[92:95], a[184:187], a[136:139], 0\n" \
"ds_read_b64_tr_b16 a[228:229], v33 offset:36864\n" \
"v_cvt_pk_bf16_f32 v215, v170, v171\n" \
"v_mfma_f32_16x16x32_bf16 v[92:95], a[248:251], a[200:203], v[92:95]\n" \
"ds_read_b64_tr_b16 a[230:231], v33 offset:37376\n" \
"v_mfma_f32_16x16x32_bf16 v[96:99], a[188:191], a[136:139], 0\n" \
"v_mfma_f32_16x16x32_bf16 v[96:99], a[252:255], a[200:203], v[96:99]\n" \
"ds_read_b128 a[136:139], v28 offset:36864\n" \
"v_mfma_f32_16x16x32_bf16 v[112:115], a[188:191], a[140:143], 0\n" \
"ds_read_b128 a[200:203], v29 offset:36864\n" \
"v_mfma_f32_16x16x32_bf16 v[112:115], a[252:255], a[204:207], v[112:115]\n" \
"s_addk_i32 s84, 0x7c00\n" \
"v_mfma_f32_16x16x32_bf16 v[108:111], a[184:187], a[140:143], 0\n" \
"buffer_load_dwordx4 v26, s[68:71], s84 offen offset:1024 lds\n" \
"v_mfma_f32_16x16x32_bf16 v[108:111], a[248:251], a[204:207], v[108:111]\n" \
"ds_read_b64_tr_b16 a[172:173], v35 offset:32768\n" \
"v_mfma_f32_16x16x32_bf16 v[104:107], a[180:183], a[140:143], 0\n" \
"s_addk_i32 s84, 0x7c00\n" \
"v_mfma_f32_16x16x32_bf16 v[104:107], a[244:247], a[204:207], v[104:107]\n" \
"buffer_load_dwordx4 v27, s[68:71], s84 offen offset:2048 lds\n" \
"v_mfma_f32_16x16x32_bf16 v[100:103], a[176:179], a[140:143], 0\n" \
"ds_read_b64_tr_b16 a[174:175], v35 offset:33280\n" \
"v_mfma_f32_16x16x32_bf16 v[100:103], a[240:243], a[204:207], v[100:103]\n" \
"ds_read_b128 a[140:143], v28 offset:38912\n" \
"v_mfma_f32_16x16x32_bf16 v[116:119], a[176:179], a[144:147], 0\n" \
"ds_read_b128 a[204:207], v29 offset:38912\n" \
"v_mfma_f32_16x16x32_bf16 v[116:119], a[240:243], a[208:211], v[116:119]\n" \
"s_addk_i32 s84, 0x7c00\n" \
"v_mfma_f32_16x16x32_bf16 v[120:123], a[180:183], a[144:147], 0\n" \
"buffer_load_dwordx4 v27, s[68:71], s84 offen offset:3072 lds\n" \
"v_mfma_f32_16x16x32_bf16 v[120:123], a[244:247], a[208:211], v[120:123]\n" \
"ds_read_b64_tr_b16 a[236:237], v35 offset:36864\n" \
"v_mfma_f32_16x16x32_bf16 v[124:127], a[184:187], a[144:147], 0\n" \
"ds_read_b64_tr_b16 a[238:239], v35 offset:37376\n" \
"v_mfma_f32_16x16x32_bf16 v[124:127], a[248:251], a[208:211], v[124:127]\n" \
"s_add_u32 m0, s30, 0x0\n" \
"s_movk_i32 s84, 0x180\n" \
"v_mfma_f32_16x16x32_bf16 v[128:131], a[188:191], a[144:147], 0\n" \
"buffer_load_dwordx4 v24, s[76:79], s84 offen offset:0 lds\n" \
"v_mfma_f32_16x16x32_bf16 v[128:131], a[252:255], a[208:211], v[128:131]\n" \
"ds_read_b128 a[144:147], v28 offset:40960\n" \
"v_mfma_f32_16x16x32_bf16 v[144:147], a[188:191], a[148:151], 0\n" \
"ds_read_b128 a[208:211], v29 offset:40960\n" \
"v_mfma_f32_16x16x32_bf16 v[144:147], a[252:255], a[212:215], v[144:147]\n" \
"ds_read_b64_tr_b16 a[168:169], v34 offset:32768\n" \
"v_mfma_f32_16x16x32_bf16 v[140:143], a[184:187], a[148:151], 0\n" \
"s_movk_i32 s84, 0x2180\n" \
"v_mfma_f32_16x16x32_bf16 v[140:143], a[248:251], a[212:215], v[140:143]\n" \
"buffer_load_dwordx4 v25, s[76:79], s84 offen offset:1024 lds\n" \
"v_mfma_f32_16x16x32_bf16 v[136:139], a[180:183], a[148:151], 0\n" \
"ds_read_b64_tr_b16 a[170:171], v34 offset:33280\n" \
"v_mfma_f32_16x16x32_bf16 v[136:139], a[244:247], a[212:215], v[136:139]\n" \
"ds_read_b64_tr_b16 a[232:233], v34 offset:36864\n" \
"v_mfma_f32_16x16x32_bf16 v[132:135], a[176:179], a[148:151], 0\n" \
"ds_read_b64_tr_b16 a[234:235], v34 offset:37376\n" \
"v_mfma_f32_16x16x32_bf16 v[132:135], a[240:243], a[212:215], v[132:135]\n" \
"ds_read_b128 a[148:151], v28 offset:43008\n" \
"v_mfma_f32_16x16x32_bf16 v[148:151], a[176:179], a[152:155], 0\n" \
"ds_read_b128 a[212:215], v29 offset:43008\n" \
"v_mfma_f32_16x16x32_bf16 v[148:151], a[240:243], a[216:219], v[148:151]\n" \
"s_movk_i32 s84, 0x4180\n" \
"v_mfma_f32_16x16x32_bf16 v[152:155], a[180:183], a[152:155], 0\n" \
"buffer_load_dwordx4 v24, s[76:79], s84 offen offset:2048 lds\n" \
"v_mfma_f32_16x16x32_bf16 v[152:155], a[244:247], a[216:219], v[152:155]\n" \
"s_movk_i32 s84, 0x6180\n" \
"v_mfma_f32_16x16x32_bf16 v[156:159], a[184:187], a[152:155], 0\n" \
"buffer_load_dwordx4 v25, s[76:79], s84 offen offset:3072 lds\n" \
"v_mfma_f32_16x16x32_bf16 v[156:159], a[248:251], a[216:219], v[156:159]\n" \
"ds_read_b128 v[248:251], v22 offset:32768\n" \
"v_mfma_f32_16x16x32_bf16 v[160:163], a[188:191], a[152:155], 0\n" \
"ds_read_b128 v[48:51], v23 offset:32768\n" \
"v_mfma_f32_16x16x32_bf16 v[160:163], a[252:255], a[216:219], v[160:163]\n" \
"ds_read_b128 a[152:155], v28 offset:45056\n" \
"v_mfma_f32_16x16x32_bf16 v[176:179], a[188:191], a[156:159], 0\n" \
"ds_read_b128 a[216:219], v29 offset:45056\n" \
"v_mfma_f32_16x16x32_bf16 v[176:179], a[252:255], a[220:223], v[176:179]\n" \
"v_mfma_f32_16x16x32_bf16 v[172:175], a[184:187], a[156:159], 0\n" \
"v_mfma_f32_16x16x32_bf16 v[172:175], a[248:251], a[220:223], v[172:175]\n" \
"v_mfma_f32_16x16x32_bf16 v[168:171], a[180:183], a[156:159], 0\n" \
"v_mfma_f32_16x16x32_bf16 v[168:171], a[244:247], a[220:223], v[168:171]\n" \
"v_mfma_f32_16x16x32_bf16 v[164:167], a[176:179], a[156:159], 0\n" \
"v_mfma_f32_16x16x32_bf16 v[164:167], a[240:243], a[220:223], v[164:167]\n" \
"ds_read_b128 a[156:159], v28 offset:47104\n" \
"ds_read_b128 a[220:223], v29 offset:47104\n" \
"s_waitcnt lgkmcnt(15)\n" \
"v_mfma_f32_16x16x32_bf16 a[0:3], a[160:163], a[128:131], a[0:3]\n" \
"s_add_u32 m0, s30, 0x1000\n" \
"s_addk_i32 s84, 0x3000\n" \
"v_mfma_f32_16x16x32_bf16 a[0:3], a[224:227], a[192:195], a[0:3]\n" \
"buffer_load_dwordx4 v24, s[76:79], s84 offen offset:0 lds\n" \
"v_mfma_f32_16x16x32_bf16 a[4:7], a[164:167], a[128:131], a[4:7]\n" \
"ds_read_b64_tr_b16 a[176:177], v36 offset:32768\n" \
"v_mov_b32_e64 v11, 0\n" \
"v_mfma_f32_16x16x32_bf16 a[4:7], a[228:231], a[192:195], a[4:7]\n" \
"ds_read_b64_tr_b16 a[178:179], v36 offset:33792\n" \
"s_and_b32 s6, s2, 7\n" \
"v_mfma_f32_16x16x32_bf16 a[12:15], a[172:175], a[128:131], a[12:15]\n" \
"ds_read_b64_tr_b16 a[240:241], v36 offset:40960\n" \
"s_lshl_b32 s7, s3, 1\n" \
"s_waitcnt lgkmcnt(15)\n" \
"v_mfma_f32_16x16x32_bf16 a[12:15], a[236:239], a[192:195], a[12:15]\n" \
"s_add_u32 s88, s41, 0x0\n" \
"buffer_store_dwordx4 v[228:231], v20, s[80:83], s88 offen offset:3072 sc1\n" \
"s_waitcnt lgkmcnt(13)\n" \
"v_mfma_f32_16x16x32_bf16 a[8:11], a[128:131], a[168:171], a[8:11]\n" \
"ds_read_b64_tr_b16 a[242:243], v36 offset:41984\n" \
"s_lshr_b32 s4, s2, 5\n" \
"s_waitcnt lgkmcnt(12)\n" \
"v_mfma_f32_16x16x32_bf16 a[8:11], a[192:195], a[232:235], a[8:11]\n" \
"ds_read_b64_tr_b16 a[180:181], v37 offset:32768\n" \
"s_lshl_b32 s5, s6, 2\n" \
"v_mfma_f32_16x16x32_bf16 a[24:27], a[132:135], a[168:171], a[24:27]\n" \
"ds_read_b64_tr_b16 a[182:183], v37 offset:33792\n" \
"s_bfe_u32 s8, s2, 0x20003\n" \
"v_mfma_f32_16x16x32_bf16 a[24:27], a[196:199], a[232:235], a[24:27]\n" \
"ds_read_b64_tr_b16 a[244:245], v37 offset:40960\n" \
"s_and_b32 s7, s7, 4\n" \
"v_mfma_f32_16x16x32_bf16 a[28:31], a[172:175], a[132:135], a[28:31]\n" \
"s_and_b32 s9, s3, 1\n" \
"s_or_b32 s13, s5, s8\n" \
"v_mfma_f32_16x16x32_bf16 a[28:31], a[236:239], a[196:199], a[28:31]\n" \
"s_addk_i32 s84, 0x2000\n" \
"v_mfma_f32_16x16x32_bf16 a[20:23], a[164:167], a[132:135], a[20:23]\n" \
"buffer_load_dwordx4 v25, s[76:79], s84 offen offset:1024 lds\n" \
"v_mfma_f32_16x16x32_bf16 a[20:23], a[228:231], a[196:199], a[20:23]\n" \
"ds_read_b64_tr_b16 a[246:247], v37 offset:41984\n" \
"s_or_b32 s9, s7, s9\n" \
"v_mfma_f32_16x16x32_bf16 a[16:19], a[160:163], a[132:135], a[16:19]\n" \
"ds_read_b64_tr_b16 a[184:185], v38 offset:32768\n" \
"s_mul_i32 s7, s4, 0xa00\n" \
"v_mfma_f32_16x16x32_bf16 a[16:19], a[224:227], a[196:199], a[16:19]\n" \
"ds_read_b64_tr_b16 a[186:187], v38 offset:33792\n" \
"s_add_i32 s7, s7, 0x5bc1000\n" \
"v_mfma_f32_16x16x32_bf16 a[32:35], a[160:163], a[136:139], a[32:35]\n" \
"s_addk_i32 s84, 0x2000\n" \
"v_mfma_f32_16x16x32_bf16 a[32:35], a[224:227], a[200:203], a[32:35]\n" \
"buffer_load_dwordx4 v24, s[76:79], s84 offen offset:2048 lds\n" \
"v_mfma_f32_16x16x32_bf16 a[36:39], a[164:167], a[136:139], a[36:39]\n" \
"ds_read_b64_tr_b16 a[248:249], v38 offset:40960\n" \
"s_lshl_b32 s10, s13, 2\n" \
"v_mfma_f32_16x16x32_bf16 a[36:39], a[228:231], a[200:203], a[36:39]\n" \
"ds_read_b64_tr_b16 a[250:251], v38 offset:41984\n" \
"s_or_b32 s10, s10, s7\n" \
"v_mfma_f32_16x16x32_bf16 a[44:47], a[172:175], a[136:139], a[44:47]\n" \
"ds_read_b64_tr_b16 a[188:189], v39 offset:32768\n" \
"v_writelane_b32 v11, s10, 0\n" \
"v_mfma_f32_16x16x32_bf16 a[44:47], a[236:239], a[200:203], a[44:47]\n" \
"ds_read_b64_tr_b16 a[190:191], v39 offset:33792\n" \
"s_lshl_b32 s14, s4, 3\n" \
"v_mfma_f32_16x16x32_bf16 a[40:43], a[136:139], a[168:171], a[40:43]\n" \
"s_add_u32 s89, s41, 0x1000\n" \
"buffer_store_dwordx4 v[232:235], v20, s[80:83], s89 offen offset:3072 sc1\n" \
"v_mfma_f32_16x16x32_bf16 a[40:43], a[200:203], a[232:235], a[40:43]\n" \
"ds_read_b64_tr_b16 a[252:253], v39 offset:40960\n" \
"v_writelane_b32 v11, s7, 1\n" \
"v_mfma_f32_16x16x32_bf16 a[56:59], a[140:143], a[168:171], a[56:59]\n" \
"ds_read_b64_tr_b16 a[254:255], v39 offset:41984\n" \
"s_or_b32 s7, s14, s6\n" \
"v_mfma_f32_16x16x32_bf16 a[56:59], a[204:207], a[232:235], a[56:59]\n" \
"s_lshl_b32 s7, s7, 10\n" \
"s_lshl_b32 s10, s3, 8\n" \
"v_mfma_f32_16x16x32_bf16 a[60:63], a[172:175], a[140:143], a[60:63]\n" \
"s_or_b32 s7, s7, s10\n" \
"s_or_b32 s10, s7, 0x5c00200\n" \
"v_mfma_f32_16x16x32_bf16 a[60:63], a[236:239], a[204:207], a[60:63]\n" \
"s_addk_i32 s84, 0x2000\n" \
"v_mfma_f32_16x16x32_bf16 a[52:55], a[164:167], a[140:143], a[52:55]\n" \
"buffer_load_dwordx4 v25, s[76:79], s84 offen offset:3072 lds\n" \
"v_mfma_f32_16x16x32_bf16 a[52:55], a[228:231], a[204:207], a[52:55]\n" \
"v_writelane_b32 v11, s10, 2\n" \
"s_lshl_b32 s10, s8, 2\n" \
"v_mfma_f32_16x16x32_bf16 a[48:51], a[160:163], a[140:143], a[48:51]\n" \
"s_or_b32 s11, s10, s7\n" \
"s_or_b32 s11, s11, 0x5c00000\n" \
"v_mfma_f32_16x16x32_bf16 a[48:51], a[224:227], a[204:207], a[48:51]\n" \
"v_writelane_b32 v11, s11, 3\n" \
"s_or_b32 s7, s7, 0x5c00080\n" \
"s_waitcnt lgkmcnt(15)\n" \
"v_mfma_f32_16x16x32_bf16 a[64:67], a[164:167], v[248:251], a[64:67]\n" \
"v_writelane_b32 v11, s7, 4\n" \
"s_or_b32 s7, s7, s10\n" \
"v_mfma_f32_16x16x32_bf16 a[64:67], a[228:231], v[48:51], a[64:67]\n" \
"s_add_i32 s10, s3, s2\n" \
"v_writelane_b32 v11, s7, 5\n" \
"v_mfma_f32_16x16x32_bf16 a[80:83], a[172:175], v[248:251], a[80:83]\n" \
"s_mov_b32 s7, 2.0\n" \
"s_mov_b32 s11, 0x5bc0000\n" \
"v_mfma_f32_16x16x32_bf16 a[80:83], a[236:239], v[48:51], a[80:83]\n" \
"s_cmp_lg_u32 s10, 0\n" \
"s_cselect_b32 s10, s7, s11\n" \
"v_mfma_f32_16x16x32_bf16 a[68:71], v[248:251], a[168:171], a[68:71]\n" \
"s_lshr_b32 s15, s2, 3\n" \
"v_writelane_b32 v11, s10, 54\n" \
"v_mfma_f32_16x16x32_bf16 a[68:71], v[48:51], a[232:235], a[68:71]\n" \
"s_waitcnt vmcnt(10) lgkmcnt(0)\n" \
"s_barrier\n" \
"v_xor_b32_e32 v28, 0x20000, v28\n" \
"v_xor_b32_e32 v29, 0x20000, v29\n" \
"v_mfma_f32_16x16x32_bf16 v[52:55], a[176:179], a[128:131], v[52:55]\n" \
"s_add_u32 m0, s40, 0x8000\n" \
"s_add_u32 s85, s39, 0x6000\n" \
"v_mfma_f32_16x16x32_bf16 v[52:55], a[240:243], a[192:195], v[52:55]\n" \
"buffer_load_dwordx4 v20, s[80:83], s85 offen sc1 lds\n" \
"v_mfma_f32_16x16x32_bf16 v[56:59], a[180:183], a[128:131], v[56:59]\n" \
"ds_read_b64_tr_b16 a[160:161], v32 offset:0\n" \
"s_lshl_b32 s10, s6, 5\n" \
"v_mfma_f32_16x16x32_bf16 v[56:59], a[244:247], a[192:195], v[56:59]\n" \
"ds_read_b64_tr_b16 a[162:163], v32 offset:512\n" \
"s_min_u32 s16, s3, 2\n" \
"s_nop 0\n" \
"v_mfma_f32_16x16x32_bf16 v[60:63], a[184:187], a[128:131], v[60:63]\n" \
"s_add_u32 m0, s40, 0x9000\n" \
"s_add_u32 s85, s39, 0x7000\n" \
"v_mfma_f32_16x16x32_bf16 v[60:63], a[248:251], a[192:195], v[60:63]\n" \
"buffer_load_dwordx4 v20, s[80:83], s85 offen sc1 lds\n" \
"v_mfma_f32_16x16x32_bf16 v[64:67], a[188:191], a[128:131], v[64:67]\n" \
"ds_read_b64_tr_b16 a[224:225], v32 offset:4096\n" \
"s_mul_i32 s12, s8, 0x120\n" \
"v_mfma_f32_16x16x32_bf16 v[64:67], a[252:255], a[192:195], v[64:67]\n" \
"ds_read_b128 a[128:131], v28 offset:0\n" \
"v_mfma_f32_16x16x32_bf16 v[80:83], a[188:191], a[132:135], v[80:83]\n" \
"ds_read_b128 a[192:195], v29 offset:0\n" \
"v_mfma_f32_16x16x32_bf16 v[80:83], a[252:255], a[196:199], v[80:83]\n" \
"s_add_u32 s88, s41, 0x1000\n" \
"buffer_store_dwordx4 v[236:239], v20, s[80:83], s88 offen offset:2048 sc1\n" \
"v_mfma_f32_16x16x32_bf16 v[76:79], a[184:187], a[132:135], v[76:79]\n" \
"ds_read_b64_tr_b16 a[226:227], v32 offset:4608\n" \
"v_mfma_f32_16x16x32_bf16 v[76:79], a[248:251], a[196:199], v[76:79]\n" \
"ds_read_b64_tr_b16 a[164:165], v33 offset:0\n" \
"v_mfma_f32_16x16x32_bf16 v[72:75], a[180:183], a[132:135], v[72:75]\n" \
"s_add_u32 m0, s40, 0xb000\n" \
"s_movk_i32 s84, 0x180\n" \
"s_nop 0\n" \
"v_mfma_f32_16x16x32_bf16 v[72:75], a[244:247], a[196:199], v[72:75]\n" \
"buffer_load_dwordx4 v21, s[76:79], s84 offen offset:0 lds\n" \
"v_mfma_f32_16x16x32_bf16 v[68:71], a[176:179], a[132:135], v[68:71]\n" \
"ds_read_b64_tr_b16 a[166:167], v33 offset:512\n" \
"v_mfma_f32_16x16x32_bf16 v[68:71], a[240:243], a[196:199], v[68:71]\n" \
"ds_read_b128 a[132:135], v28 offset:2048\n" \
"v_mfma_f32_16x16x32_bf16 v[84:87], a[176:179], a[136:139], v[84:87]\n" \
"ds_read_b128 a[196:199], v29 offset:2048\n" \
"v_mfma_f32_16x16x32_bf16 v[84:87], a[240:243], a[200:203], v[84:87]\n" \
"s_add_u32 m0, s31, 0x8000\n" \
"s_mov_b32 s84, 0x180000\n" \
"v_mfma_f32_16x16x32_bf16 v[88:91], a[180:183], a[136:139], v[88:91]\n" \
"buffer_load_dwordx4 v26, s[68:71], s84 offen offset:0 lds\n" \
"v_mfma_f32_16x16x32_bf16 v[88:91], a[244:247], a[200:203], v[88:91]\n" \
"ds_read_b64_tr_b16 a[228:229], v33 offset:4096\n" \
"v_mfma_f32_16x16x32_bf16 v[92:95], a[184:187], a[136:139], v[92:95]\n" \
"ds_read_b64_tr_b16 a[230:231], v33 offset:4608\n" \
"v_mfma_f32_16x16x32_bf16 v[92:95], a[248:251], a[200:203], v[92:95]\n" \
"s_addk_i32 s84, 0x7c00\n" \
"v_mfma_f32_16x16x32_bf16 v[96:99], a[188:191], a[136:139], v[96:99]\n" \
"buffer_load_dwordx4 v26, s[68:71], s84 offen offset:1024 lds\n" \
"v_mfma_f32_16x16x32_bf16 v[96:99], a[252:255], a[200:203], v[96:99]\n" \
"ds_read_b128 a[136:139], v28 offset:4096\n" \
"v_mfma_f32_16x16x32_bf16 v[112:115], a[188:191], a[140:143], v[112:115]\n" \
"ds_read_b128 a[200:203], v29 offset:4096\n" \
"v_mfma_f32_16x16x32_bf16 v[112:115], a[252:255], a[204:207], v[112:115]\n" \
"s_addk_i32 s84, 0x7c00\n" \
"v_mfma_f32_16x16x32_bf16 v[108:111], a[184:187], a[140:143], v[108:111]\n" \
"buffer_load_dwordx4 v27, s[68:71], s84 offen offset:2048 lds\n" \
"v_mfma_f32_16x16x32_bf16 v[108:111], a[248:251], a[204:207], v[108:111]\n" \
"ds_read_b64_tr_b16 a[172:173], v35 offset:0\n" \
"v_mfma_f32_16x16x32_bf16 v[104:107], a[180:183], a[140:143], v[104:107]\n" \
"ds_read_b64_tr_b16 a[174:175], v35 offset:512\n" \
"v_mfma_f32_16x16x32_bf16 v[104:107], a[244:247], a[204:207], v[104:107]\n" \
"ds_read_b64_tr_b16 a[236:237], v35 offset:4096\n" \
"v_mfma_f32_16x16x32_bf16 v[100:103], a[176:179], a[140:143], v[100:103]\n" \
"s_add_u32 s11, s22, s12\n" \
"s_addc_u32 s17, s23, 0\n" \
"v_mfma_f32_16x16x32_bf16 v[100:103], a[240:243], a[204:207], v[100:103]\n" \
"ds_read_b128 a[140:143], v28 offset:6144\n" \
"v_mfma_f32_16x16x32_bf16 v[116:119], a[176:179], a[144:147], v[116:119]\n" \
"ds_read_b128 a[204:207], v29 offset:6144\n" \
"v_mfma_f32_16x16x32_bf16 v[116:119], a[240:243], a[208:211], v[116:119]\n" \
"s_addk_i32 s84, 0x7c00\n" \
"v_mfma_f32_16x16x32_bf16 v[120:123], a[180:183], a[144:147], v[120:123]\n" \
"buffer_load_dwordx4 v27, s[68:71], s84 offen offset:3072 lds\n" \
"v_mfma_f32_16x16x32_bf16 v[120:123], a[244:247], a[208:211], v[120:123]\n" \
"s_add_u32 m0, s30, 0x8000\n" \
"s_movk_i32 s84, 0x200\n" \
"v_mfma_f32_16x16x32_bf16 v[124:127], a[184:187], a[144:147], v[124:127]\n" \
"buffer_load_dwordx4 v24, s[76:79], s84 offen offset:0 lds\n" \
"v_mfma_f32_16x16x32_bf16 v[124:127], a[248:251], a[208:211], v[124:127]\n" \
"ds_read_b64_tr_b16 a[238:239], v35 offset:4608\n" \
"v_mfma_f32_16x16x32_bf16 v[128:131], a[188:191], a[144:147], v[128:131]\n" \
"s_add_u32 s89, s41, 0x2000\n" \
"buffer_store_dwordx4 v[240:243], v20, s[80:83], s89 offen offset:2048 sc1\n" \
"v_mfma_f32_16x16x32_bf16 v[128:131], a[252:255], a[208:211], v[128:131]\n" \
"ds_read_b128 a[144:147], v28 offset:8192\n" \
"v_mfma_f32_16x16x32_bf16 v[144:147], a[188:191], a[148:151], v[144:147]\n" \
"ds_read_b128 a[208:211], v29 offset:8192\n" \
"v_mfma_f32_16x16x32_bf16 v[144:147], a[252:255], a[212:215], v[144:147]\n" \
"ds_read_b64_tr_b16 a[168:169], v34 offset:0\n" \
"v_mfma_f32_16x16x32_bf16 v[140:143], a[184:187], a[148:151], v[140:143]\n" \
"ds_read_b64_tr_b16 a[170:171], v34 offset:512\n" \
"v_mfma_f32_16x16x32_bf16 v[140:143], a[248:251], a[212:215], v[140:143]\n" \
"s_movk_i32 s84, 0x2200\n" \
"s_nop 0\n" \
"v_mfma_f32_16x16x32_bf16 v[136:139], a[180:183], a[148:151], v[136:139]\n" \
"buffer_load_dwordx4 v25, s[76:79], s84 offen offset:1024 lds\n" \
"v_mfma_f32_16x16x32_bf16 v[136:139], a[244:247], a[212:215], v[136:139]\n" \
"ds_read_b64_tr_b16 a[232:233], v34 offset:4096\n" \
"v_mfma_f32_16x16x32_bf16 v[132:135], a[176:179], a[148:151], v[132:135]\n" \
"ds_read_b64_tr_b16 a[234:235], v34 offset:4608\n" \
"v_mfma_f32_16x16x32_bf16 v[132:135], a[240:243], a[212:215], v[132:135]\n" \
"ds_read_b128 a[148:151], v28 offset:10240\n" \
"v_mfma_f32_16x16x32_bf16 v[148:151], a[176:179], a[152:155], v[148:151]\n" \
"ds_read_b128 a[212:215], v29 offset:10240\n" \
"v_mfma_f32_16x16x32_bf16 v[148:151], a[240:243], a[216:219], v[148:151]\n" \
"s_movk_i32 s84, 0x4200\n" \
"v_mfma_f32_16x16x32_bf16 v[152:155], a[180:183], a[152:155], v[152:155]\n" \
"buffer_load_dwordx4 v24, s[76:79], s84 offen offset:2048 lds\n" \
"v_mfma_f32_16x16x32_bf16 v[152:155], a[244:247], a[216:219], v[152:155]\n" \
"s_movk_i32 s84, 0x6200\n" \
"v_mfma_f32_16x16x32_bf16 v[156:159], a[184:187], a[152:155], v[156:159]\n" \
"buffer_load_dwordx4 v25, s[76:79], s84 offen offset:3072 lds\n" \
"v_mfma_f32_16x16x32_bf16 v[156:159], a[248:251], a[216:219], v[156:159]\n" \
"ds_read_b128 v[248:251], v22 offset:0\n" \
"v_mfma_f32_16x16x32_bf16 v[160:163], a[188:191], a[152:155], v[160:163]\n" \
"ds_read_b128 v[48:51], v23 offset:0\n" \
"v_mfma_f32_16x16x32_bf16 v[160:163], a[252:255], a[216:219], v[160:163]\n" \
"ds_read_b128 a[152:155], v28 offset:12288\n" \
"v_mfma_f32_16x16x32_bf16 v[176:179], a[188:191], a[156:159], v[176:179]\n" \
"ds_read_b128 a[216:219], v29 offset:12288\n" \
"v_mfma_f32_16x16x32_bf16 v[176:179], a[252:255], a[220:223], v[176:179]\n" \
"s_add_u32 s18, s24, s12\n" \
"v_writelane_b32 v11, s11, 9\n" \
"s_addc_u32 s19, s25, 0\n" \
"v_mfma_f32_16x16x32_bf16 v[172:175], a[184:187], a[156:159], v[172:175]\n" \
"s_and_b32 s20, s17, 0xffff\n" \
"v_writelane_b32 v11, s20, 10\n" \
"v_mfma_f32_16x16x32_bf16 v[172:175], a[248:251], a[220:223], v[172:175]\n" \
"s_and_b32 s20, s19, 0xffff\n" \
"v_writelane_b32 v11, s18, 11\n" \
"v_mfma_f32_16x16x32_bf16 v[168:171], a[180:183], a[156:159], v[168:171]\n" \
"s_cmpk_lt_u32 s3, 3\n" \
"s_cselect_b32 s11, s11, s18\n" \
"v_mfma_f32_16x16x32_bf16 v[168:171], a[244:247], a[220:223], v[168:171]\n" \
"s_movk_i32 s18, 0xfff\n" \
"v_writelane_b32 v11, s20, 12\n" \
"v_mfma_f32_16x16x32_bf16 v[164:167], a[176:179], a[156:159], v[164:167]\n" \
"s_and_b32 s20, s29, 0xffff\n" \
"v_writelane_b32 v11, s28, 13\n" \
"v_mfma_f32_16x16x32_bf16 v[164:167], a[240:243], a[220:223], v[164:167]\n" \
"ds_read_b128 a[156:159], v28 offset:14336\n" \
"ds_read_b128 a[220:223], v29 offset:14336\n" \
"s_waitcnt lgkmcnt(15)\n" \
"v_mfma_f32_16x16x32_bf16 a[0:3], a[160:163], a[128:131], a[0:3]\n" \
"s_add_u32 m0, s30, 0x9000\n" \
"s_addk_i32 s84, 0x3000\n" \
"v_mfma_f32_16x16x32_bf16 a[0:3], a[224:227], a[192:195], a[0:3]\n" \
"buffer_load_dwordx4 v24, s[76:79], s84 offen offset:0 lds\n" \
"v_mfma_f32_16x16x32_bf16 a[4:7], a[164:167], a[128:131], a[4:7]\n" \
"ds_read_b64_tr_b16 a[176:177], v36 offset:0\n" \
"v_writelane_b32 v11, s20, 14\n" \
"v_mfma_f32_16x16x32_bf16 a[4:7], a[228:231], a[192:195], a[4:7]\n" \
"ds_read_b64_tr_b16 a[178:179], v36 offset:1024\n" \
"v_writelane_b32 v11, s11, 15\n" \
"v_mfma_f32_16x16x32_bf16 a[12:15], a[172:175], a[128:131], a[12:15]\n" \
"ds_read_b64_tr_b16 a[240:241], v36 offset:8192\n" \
"s_waitcnt lgkmcnt(15)\n" \
"v_mfma_f32_16x16x32_bf16 a[12:15], a[236:239], a[192:195], a[12:15]\n" \
"s_add_u32 s88, s41, 0x2000\n" \
"buffer_store_dwordx4 v[244:247], v20, s[80:83], s88 offen offset:3072 sc1\n" \
"s_waitcnt lgkmcnt(13)\n" \
"v_mfma_f32_16x16x32_bf16 a[8:11], a[128:131], a[168:171], a[8:11]\n" \
"ds_read_b64_tr_b16 a[242:243], v36 offset:9216\n" \
"s_waitcnt lgkmcnt(12)\n" \
"v_mfma_f32_16x16x32_bf16 a[8:11], a[192:195], a[232:235], a[8:11]\n" \
"ds_read_b64_tr_b16 a[180:181], v37 offset:0\n" \
"v_mfma_f32_16x16x32_bf16 a[24:27], a[132:135], a[168:171], a[24:27]\n" \
"ds_read_b64_tr_b16 a[182:183], v37 offset:1024\n" \
"v_mfma_f32_16x16x32_bf16 a[24:27], a[196:199], a[232:235], a[24:27]\n" \
"ds_read_b64_tr_b16 a[244:245], v37 offset:8192\n" \
"v_mfma_f32_16x16x32_bf16 a[28:31], a[172:175], a[132:135], a[28:31]\n" \
"s_cmpk_lt_u32 s3, 3\n" \
"s_cselect_b32 s11, s17, s19\n" \
"v_mfma_f32_16x16x32_bf16 a[28:31], a[236:239], a[196:199], a[28:31]\n" \
"s_addk_i32 s84, 0x2000\n" \
"s_nop 0\n" \
"v_mfma_f32_16x16x32_bf16 a[20:23], a[164:167], a[132:135], a[20:23]\n" \
"buffer_load_dwordx4 v25, s[76:79], s84 offen offset:1024 lds\n" \
"v_mfma_f32_16x16x32_bf16 a[20:23], a[228:231], a[196:199], a[20:23]\n" \
"ds_read_b64_tr_b16 a[246:247], v37 offset:9216\n" \
"s_and_b32 s11, s11, 0xffff\n" \
"v_mfma_f32_16x16x32_bf16 a[16:19], a[160:163], a[132:135], a[16:19]\n" \
"ds_read_b64_tr_b16 a[184:185], v38 offset:0\n" \
"v_writelane_b32 v11, s11, 16\n" \
"v_mfma_f32_16x16x32_bf16 a[16:19], a[224:227], a[196:199], a[16:19]\n" \
"ds_read_b64_tr_b16 a[186:187], v38 offset:1024\n" \
"s_mul_i32 s11, s6, 0x48\n" \
"v_mfma_f32_16x16x32_bf16 a[32:35], a[160:163], a[136:139], a[32:35]\n" \
"s_addk_i32 s84, 0x2000\n" \
"s_nop 0\n" \
"v_mfma_f32_16x16x32_bf16 a[32:35], a[224:227], a[200:203], a[32:35]\n" \
"buffer_load_dwordx4 v24, s[76:79], s84 offen offset:2048 lds\n" \
"v_mfma_f32_16x16x32_bf16 a[36:39], a[164:167], a[136:139], a[36:39]\n" \
"ds_read_b64_tr_b16 a[248:249], v38 offset:8192\n" \
"s_add_i32 s17, s11, 0x47\n" \
"v_mfma_f32_16x16x32_bf16 a[36:39], a[228:231], a[200:203], a[36:39]\n" \
"ds_read_b64_tr_b16 a[250:251], v38 offset:9216\n" \
".fill (L_pb + 24288 - .) / 4, 4, 0xBF800000\n" \
"L_pp6:\n" \
"s_cbranch_execz L_pret\n" \
"s_nop 0\n" \
"v_mfma_f32_16x16x32_bf16 a[44:47], a[172:175], a[136:139], a[44:47]\n" \
"ds_read_b64_tr_b16 a[188:189], v39 offset:0\n" \
"v_mfma_f32_16x16x32_bf16 a[44:47], a[236:239], a[200:203], a[44:47]\n" \
"ds_read_b64_tr_b16 a[190:191], v39 offset:1024\n" \
"v_mfma_f32_16x16x32_bf16 a[40:43], a[136:139], a[168:171], a[40:43]\n" \
"s_add_u32 s89, s41, 0x3000\n" \
"buffer_store_dwordx4 v[184:187], v20, s[80:83], s89 offen offset:3072 sc1\n" \
"v_mfma_f32_16x16x32_bf16 a[40:43], a[200:203], a[232:235], a[40:43]\n" \
"ds_read_b64_tr_b16 a[252:253], v39 offset:8192\n" \
"v_mfma_f32_16x16x32_bf16 a[56:59], a[140:143], a[168:171], a[56:59]\n" \
"ds_read_b64_tr_b16 a[254:255], v39 offset:9216\n" \
"v_mfma_f32_16x16x32_bf16 a[56:59], a[204:207], a[232:235], a[56:59]\n" \
"s_cmpk_lt_u32 s3, 3\n" \
"s_cselect_b32 s17, s17, s18\n" \
"v_mfma_f32_16x16x32_bf16 a[60:63], a[172:175], a[140:143], a[60:63]\n" \
"s_mulk_i32 s17, 0x480\n" \
"s_addk_i32 s17, 0x120\n" \
"v_mfma_f32_16x16x32_bf16 a[60:63], a[236:239], a[204:207], a[60:63]\n" \
"s_addk_i32 s84, 0x2000\n" \
"v_mfma_f32_16x16x32_bf16 a[52:55], a[164:167], a[140:143], a[52:55]\n" \
"buffer_load_dwordx4 v25, s[76:79], s84 offen offset:3072 lds\n" \
"v_mfma_f32_16x16x32_bf16 a[52:55], a[228:231], a[204:207], a[52:55]\n" \
"v_writelane_b32 v11, s17, 17\n" \
"s_lshl_b32 s17, s3, 5\n" \
"v_mfma_f32_16x16x32_bf16 a[48:51], a[160:163], a[140:143], a[48:51]\n" \
"s_add_i32 s11, s11, s17\n" \
"s_mul_i32 s17, s11, 0x480\n" \
"v_mfma_f32_16x16x32_bf16 a[48:51], a[224:227], a[204:207], a[48:51]\n" \
"s_mul_i32 s11, s13, 0x24000\n" \
"s_add_i32 s18, s11, 0x1b000\n" \
"s_waitcnt lgkmcnt(15)\n" \
"v_mfma_f32_16x16x32_bf16 a[64:67], a[164:167], v[248:251], a[64:67]\n" \
"s_cmpk_lt_u32 s3, 3\n" \
"s_cselect_b32 s17, s17, s18\n" \
"v_mfma_f32_16x16x32_bf16 a[64:67], a[228:231], v[48:51], a[64:67]\n" \
"s_mov_b32 s18, 0x8000\n" \
"v_writelane_b32 v11, s17, 18\n" \
"v_mfma_f32_16x16x32_bf16 a[80:83], a[172:175], v[248:251], a[80:83]\n" \
"s_mul_i32 s17, s16, 0x2400\n" \
"s_mul_i32 s16, s16, 0x9000\n" \
"v_mfma_f32_16x16x32_bf16 a[80:83], a[236:239], v[48:51], a[80:83]\n" \
"s_bitset1_b32 s17, 17\n" \
"v_writelane_b32 v11, s17, 19\n" \
"v_mfma_f32_16x16x32_bf16 a[68:71], v[248:251], a[168:171], a[68:71]\n" \
"s_add_i32 s16, s11, s16\n" \
"v_writelane_b32 v11, s16, 26\n" \
"v_mfma_f32_16x16x32_bf16 a[68:71], v[48:51], a[232:235], a[68:71]\n" \
"s_waitcnt vmcnt(11) lgkmcnt(0)\n" \
"s_barrier\n" \
"v_xor_b32_e32 v28, 0x20000, v28\n" \
"v_xor_b32_e32 v29, 0x20000, v29\n" \
"v_mfma_f32_16x16x32_bf16 v[52:55], a[176:179], a[128:131], v[52:55]\n" \
"s_add_u32 m0, s40, 0x0\n" \
"s_nop 0\n" \
"s_add_u32 s85, s39, 0x8000\n" \
"v_mfma_f32_16x16x32_bf16 v[52:55], a[240:243], a[192:195], v[52:55]\n" \
"buffer_load_dwordx4 v20, s[80:83], s85 offen sc1 lds\n" \
"v_mfma_f32_16x16x32_bf16 v[56:59], a[180:183], a[128:131], v[56:59]\n" \
"ds_read_b64_tr_b16 a[160:161], v32 offset:32768\n" \
"s_mul_i32 s16, s3, 0x2400\n" \
"v_mfma_f32_16x16x32_bf16 v[56:59], a[244:247], a[192:195], v[56:59]\n" \
"ds_read_b64_tr_b16 a[162:163], v32 offset:33280\n" \
"s_add_i32 s17, s16, 0xa400\n" \
"v_mfma_f32_16x16x32_bf16 v[60:63], a[184:187], a[128:131], v[60:63]\n" \
"s_add_u32 m0, s40, 0x1000\n" \
"s_add_u32 s85, s39, 0x9000\n" \
"v_mfma_f32_16x16x32_bf16 v[60:63], a[248:251], a[192:195], v[60:63]\n" \
"buffer_load_dwordx4 v20, s[80:83], s85 offen sc1 lds\n" \
"v_mfma_f32_16x16x32_bf16 v[64:67], a[188:191], a[128:131], v[64:67]\n" \
"ds_read_b64_tr_b16 a[224:225], v32 offset:36864\n" \
"v_mfma_f32_16x16x32_bf16 v[64:67], a[252:255], a[192:195], v[64:67]\n" \
"ds_read_b128 a[128:131], v28 offset:0\n" \
"v_mfma_f32_16x16x32_bf16 v[80:83], a[188:191], a[132:135], v[80:83]\n" \
"ds_read_b128 a[192:195], v29 offset:0\n" \
"v_mfma_f32_16x16x32_bf16 v[80:83], a[252:255], a[196:199], v[80:83]\n" \
"s_add_u32 s88, s41, 0x3000\n" \
"buffer_store_dwordx4 v[188:191], v20, s[80:83], s88 offen offset:2048 sc1\n" \
"v_mfma_f32_16x16x32_bf16 v[76:79], a[184:187], a[132:135], v[76:79]\n" \
"ds_read_b64_tr_b16 a[226:227], v32 offset:37376\n" \
"v_mfma_f32_16x16x32_bf16 v[76:79], a[248:251], a[196:199], v[76:79]\n" \
"ds_read_b64_tr_b16 a[164:165], v33 offset:32768\n" \
"v_mfma_f32_16x16x32_bf16 v[72:75], a[180:183], a[132:135], v[72:75]\n" \
"s_add_u32 m0, s40, 0x3000\n" \
"s_movk_i32 s84, 0x200\n" \
"v_mfma_f32_16x16x32_bf16 v[72:75], a[244:247], a[196:199], v[72:75]\n" \
"buffer_load_dwordx4 v21, s[76:79], s84 offen offset:0 lds\n" \
"v_mfma_f32_16x16x32_bf16 v[68:71], a[176:179], a[132:135], v[68:71]\n" \
"ds_read_b64_tr_b16 a[166:167], v33 offset:33280\n" \
"v_mfma_f32_16x16x32_bf16 v[68:71], a[240:243], a[196:199], v[68:71]\n" \
"ds_read_b128 a[132:135], v28 offset:2048\n" \
"v_mfma_f32_16x16x32_bf16 v[84:87], a[176:179], a[136:139], v[84:87]\n" \
"ds_read_b128 a[196:199], v29 offset:2048\n" \
"v_mfma_f32_16x16x32_bf16 v[84:87], a[240:243], a[200:203], v[84:87]\n" \
"s_add_u32 m0, s31, 0x0\n" \
"s_mov_b32 s84, 0x200000\n" \
"v_mfma_f32_16x16x32_bf16 v[88:91], a[180:183], a[136:139], v[88:91]\n" \
"buffer_load_dwordx4 v26, s[68:71], s84 offen offset:0 lds\n" \
"v_mfma_f32_16x16x32_bf16 v[88:91], a[244:247], a[200:203], v[88:91]\n" \
"ds_read_b64_tr_b16 a[228:229], v33 offset:36864\n" \
"v_mfma_f32_16x16x32_bf16 v[92:95], a[184:187], a[136:139], v[92:95]\n" \
"ds_read_b64_tr_b16 a[230:231], v33 offset:37376\n" \
"v_mfma_f32_16x16x32_bf16 v[92:95], a[248:251], a[200:203], v[92:95]\n" \
"s_addk_i32 s84, 0x7c00\n" \
"v_mfma_f32_16x16x32_bf16 v[96:99], a[188:191], a[136:139], v[96:99]\n" \
"buffer_load_dwordx4 v26, s[68:71], s84 offen offset:1024 lds\n" \
"v_mfma_f32_16x16x32_bf16 v[96:99], a[252:255], a[200:203], v[96:99]\n" \
"ds_read_b128 a[136:139], v28 offset:4096\n" \
"v_mfma_f32_16x16x32_bf16 v[112:115], a[188:191], a[140:143], v[112:115]\n" \
"ds_read_b128 a[200:203], v29 offset:4096\n" \
"v_mfma_f32_16x16x32_bf16 v[112:115], a[252:255], a[204:207], v[112:115]\n" \
"s_addk_i32 s84, 0x7c00\n" \
"v_mfma_f32_16x16x32_bf16 v[108:111], a[184:187], a[140:143], v[108:111]\n" \
"buffer_load_dwordx4 v27, s[68:71], s84 offen offset:2048 lds\n" \
"v_mfma_f32_16x16x32_bf16 v[108:111], a[248:251], a[204:207], v[108:111]\n" \
"ds_read_b64_tr_b16 a[172:173], v35 offset:32768\n" \
"v_mfma_f32_16x16x32_bf16 v[104:107], a[180:183], a[140:143], v[104:107]\n" \
"ds_read_b64_tr_b16 a[174:175], v35 offset:33280\n" \
"v_mfma_f32_16x16x32_bf16 v[104:107], a[244:247], a[204:207], v[104:107]\n" \
"ds_read_b64_tr_b16 a[236:237], v35 offset:36864\n" \
"v_mfma_f32_16x16x32_bf16 v[100:103], a[176:179], a[140:143], v[100:103]\n" \
"s_cmpk_lt_u32 s3, 3\n" \
"s_cselect_b32 s17, s17, s18\n" \
"v_mfma_f32_16x16x32_bf16 v[100:103], a[240:243], a[204:207], v[100:103]\n" \
"ds_read_b128 a[140:143], v28 offset:6144\n" \
"s_mov_b32 s18, 0x1b000\n" \
"v_mfma_f32_16x16x32_bf16 v[116:119], a[176:179], a[144:147], v[116:119]\n" \
"ds_read_b128 a[204:207], v29 offset:6144\n" \
"s_xor_b32 s19, s3, 2\n" \
"v_mfma_f32_16x16x32_bf16 v[116:119], a[240:243], a[208:211], v[116:119]\n" \
"s_addk_i32 s84, 0x7c00\n" \
"v_mfma_f32_16x16x32_bf16 v[120:123], a[180:183], a[144:147], v[120:123]\n" \
"buffer_load_dwordx4 v27, s[68:71], s84 offen offset:3072 lds\n" \
"v_mfma_f32_16x16x32_bf16 v[120:123], a[244:247], a[208:211], v[120:123]\n" \
"s_add_u32 m0, s30, 0x20000\n" \
"s_movk_i32 s84, 0x280\n" \
"v_mfma_f32_16x16x32_bf16 v[124:127], a[184:187], a[144:147], v[124:127]\n" \
"buffer_load_dwordx4 v24, s[76:79], s84 offen offset:0 lds\n" \
"v_mfma_f32_16x16x32_bf16 v[124:127], a[248:251], a[208:211], v[124:127]\n" \
"ds_read_b64_tr_b16 a[238:239], v35 offset:37376\n" \
"v_mfma_f32_16x16x32_bf16 v[128:131], a[188:191], a[144:147], v[128:131]\n" \
"s_add_u32 s89, s42, 0x0\n" \
"buffer_store_dwordx4 v[216:219], v20, s[80:83], s89 offen offset:0 sc1\n" \
"v_mfma_f32_16x16x32_bf16 v[128:131], a[252:255], a[208:211], v[128:131]\n" \
"ds_read_b128 a[144:147], v28 offset:8192\n" \
"v_mfma_f32_16x16x32_bf16 v[144:147], a[188:191], a[148:151], v[144:147]\n" \
"ds_read_b128 a[208:211], v29 offset:8192\n" \
"v_mfma_f32_16x16x32_bf16 v[144:147], a[252:255], a[212:215], v[144:147]\n" \
"ds_read_b64_tr_b16 a[168:169], v34 offset:32768\n" \
"v_mfma_f32_16x16x32_bf16 v[140:143], a[184:187], a[148:151], v[140:143]\n" \
"ds_read_b64_tr_b16 a[170:171], v34 offset:33280\n" \
"v_mfma_f32_16x16x32_bf16 v[140:143], a[248:251], a[212:215], v[140:143]\n" \
"s_movk_i32 s84, 0x2280\n" \
"s_nop 0\n" \
"v_mfma_f32_16x16x32_bf16 v[136:139], a[180:183], a[148:151], v[136:139]\n" \
"buffer_load_dwordx4 v25, s[76:79], s84 offen offset:1024 lds\n" \
"v_mfma_f32_16x16x32_bf16 v[136:139], a[244:247], a[212:215], v[136:139]\n" \
"ds_read_b64_tr_b16 a[232:233], v34 offset:36864\n" \
"v_mfma_f32_16x16x32_bf16 v[132:135], a[176:179], a[148:151], v[132:135]\n" \
"ds_read_b64_tr_b16 a[234:235], v34 offset:37376\n" \
"v_mfma_f32_16x16x32_bf16 v[132:135], a[240:243], a[212:215], v[132:135]\n" \
"ds_read_b128 a[148:151], v28 offset:10240\n" \
"v_mfma_f32_16x16x32_bf16 v[148:151], a[176:179], a[152:155], v[148:151]\n" \
"ds_read_b128 a[212:215], v29 offset:10240\n" \
"v_mfma_f32_16x16x32_bf16 v[148:151], a[240:243], a[216:219], v[148:151]\n" \
"s_movk_i32 s84, 0x4280\n" \
"v_mfma_f32_16x16x32_bf16 v[152:155], a[180:183], a[152:155], v[152:155]\n" \
"buffer_load_dwordx4 v24, s[76:79], s84 offen offset:2048 lds\n" \
"v_mfma_f32_16x16x32_bf16 v[152:155], a[244:247], a[216:219], v[152:155]\n" \
"s_movk_i32 s84, 0x6280\n" \
"v_mfma_f32_16x16x32_bf16 v[156:159], a[184:187], a[152:155], v[156:159]\n" \
"buffer_load_dwordx4 v25, s[76:79], s84 offen offset:3072 lds\n" \
"v_mfma_f32_16x16x32_bf16 v[156:159], a[248:251], a[216:219], v[156:159]\n" \
"ds_read_b128 v[248:251], v22 offset:32768\n" \
"v_mfma_f32_16x16x32_bf16 v[160:163], a[188:191], a[152:155], v[160:163]\n" \
"ds_read_b128 v[48:51], v23 offset:32768\n" \
"v_mfma_f32_16x16x32_bf16 v[160:163], a[252:255], a[216:219], v[160:163]\n" \
"ds_read_b128 a[152:155], v28 offset:12288\n" \
"v_mfma_f32_16x16x32_bf16 v[176:179], a[188:191], a[156:159], v[176:179]\n" \
"ds_read_b128 a[216:219], v29 offset:12288\n" \
"v_mfma_f32_16x16x32_bf16 v[176:179], a[252:255], a[220:223], v[176:179]\n" \
"s_cmp_lg_u32 s19, 0\n" \
"s_cselect_b32 s18, s17, s18\n" \
"v_mfma_f32_16x16x32_bf16 v[172:175], a[184:187], a[156:159], v[172:175]\n" \
"v_writelane_b32 v11, s17, 20\n" \
"s_or_b32 s17, s16, 0x10000\n" \
"v_mfma_f32_16x16x32_bf16 v[172:175], a[248:251], a[220:223], v[172:175]\n" \
"v_writelane_b32 v11, s18, 21\n" \
"s_add_i32 s18, s16, 0x19000\n" \
"v_mfma_f32_16x16x32_bf16 v[168:171], a[180:183], a[156:159], v[168:171]\n" \
"v_writelane_b32 v11, s17, 22\n" \
"s_mov_b32 s17, 0\n" \
"v_mfma_f32_16x16x32_bf16 v[168:171], a[244:247], a[220:223], v[168:171]\n" \
"s_cmpk_lt_u32 s3, 3\n" \
"s_cselect_b32 s18, s18, s17\n" \
"v_mfma_f32_16x16x32_bf16 v[164:167], a[176:179], a[156:159], v[164:167]\n" \
"s_bitset1_b32 s16, 17\n" \
"v_writelane_b32 v11, s18, 23\n" \
"v_mfma_f32_16x16x32_bf16 v[164:167], a[240:243], a[220:223], v[164:167]\n" \
"ds_read_b128 a[156:159], v28 offset:14336\n" \
"ds_read_b128 a[220:223], v29 offset:14336\n" \
"s_waitcnt lgkmcnt(15)\n" \
"v_mfma_f32_16x16x32_bf16 a[0:3], a[160:163], a[128:131], a[0:3]\n" \
"s_add_u32 m0, s30, 0x21000\n" \
"s_addk_i32 s84, 0x3000\n" \
"v_mfma_f32_16x16x32_bf16 a[0:3], a[224:227], a[192:195], a[0:3]\n" \
"buffer_load_dwordx4 v24, s[76:79], s84 offen offset:0 lds\n" \
"v_mfma_f32_16x16x32_bf16 a[4:7], a[164:167], a[128:131], a[4:7]\n" \
"ds_read_b64_tr_b16 a[176:177], v36 offset:32768\n" \
"s_movk_i32 s18, 0x2400\n" \
"v_mfma_f32_16x16x32_bf16 a[4:7], a[228:231], a[192:195], a[4:7]\n" \
"ds_read_b64_tr_b16 a[178:179], v36 offset:33792\n" \
"v_mfma_f32_16x16x32_bf16 a[12:15], a[172:175], a[128:131], a[12:15]\n" \
"ds_read_b64_tr_b16 a[240:241], v36 offset:40960\n" \
"s_waitcnt lgkmcnt(15)\n" \
"v_mfma_f32_16x16x32_bf16 a[12:15], a[236:239], a[192:195], a[12:15]\n" \
"s_add_u32 s88, s42, 0x1000\n" \
"buffer_store_dwordx4 v[220:223], v20, s[80:83], s88 offen offset:0 sc1\n" \
"s_waitcnt lgkmcnt(13)\n" \
"v_mfma_f32_16x16x32_bf16 a[8:11], a[128:131], a[168:171], a[8:11]\n" \
"ds_read_b64_tr_b16 a[242:243], v36 offset:41984\n" \
"s_waitcnt lgkmcnt(12)\n" \
"v_mfma_f32_16x16x32_bf16 a[8:11], a[192:195], a[232:235], a[8:11]\n" \
"ds_read_b64_tr_b16 a[180:181], v37 offset:32768\n" \
"v_mfma_f32_16x16x32_bf16 a[24:27], a[132:135], a[168:171], a[24:27]\n" \
"ds_read_b64_tr_b16 a[182:183], v37 offset:33792\n" \
"v_mfma_f32_16x16x32_bf16 a[24:27], a[196:199], a[232:235], a[24:27]\n" \
"ds_read_b64_tr_b16 a[244:245], v37 offset:40960\n" \
"v_mfma_f32_16x16x32_bf16 a[28:31], a[172:175], a[132:135], a[28:31]\n" \
"s_cmpk_lt_u32 s3, 3\n" \
"s_cselect_b32 s16, s16, s18\n" \
"v_mfma_f32_16x16x32_bf16 a[28:31], a[236:239], a[196:199], a[28:31]\n" \
"s_addk_i32 s84, 0x2000\n" \
"v_mfma_f32_16x16x32_bf16 a[20:23], a[164:167], a[132:135], a[20:23]\n" \
"buffer_load_dwordx4 v25, s[76:79], s84 offen offset:1024 lds\n" \
"v_mfma_f32_16x16x32_bf16 a[20:23], a[228:231], a[196:199], a[20:23]\n" \
"ds_read_b64_tr_b16 a[246:247], v37 offset:41984\n" \
"s_mul_i32 s19, s3, 0x9000\n" \
"v_mfma_f32_16x16x32_bf16 a[16:19], a[160:163], a[132:135], a[16:19]\n" \
"ds_read_b64_tr_b16 a[184:185], v38 offset:32768\n" \
"v_writelane_b32 v11, s16, 24\n" \
"v_mfma_f32_16x16x32_bf16 a[16:19], a[224:227], a[196:199], a[16:19]\n" \
"ds_read_b64_tr_b16 a[186:187], v38 offset:33792\n" \
"s_lshl_b32 s16, s3, 10\n" \
"v_mfma_f32_16x16x32_bf16 a[32:35], a[160:163], a[136:139], a[32:35]\n" \
"s_addk_i32 s84, 0x2000\n" \
"v_mfma_f32_16x16x32_bf16 a[32:35], a[224:227], a[200:203], a[32:35]\n" \
"buffer_load_dwordx4 v24, s[76:79], s84 offen offset:2048 lds\n" \
"v_mfma_f32_16x16x32_bf16 a[36:39], a[164:167], a[136:139], a[36:39]\n" \
"ds_read_b64_tr_b16 a[248:249], v38 offset:40960\n" \
"s_addk_i32 s16, 0x4800\n" \
"v_mfma_f32_16x16x32_bf16 a[36:39], a[228:231], a[200:203], a[36:39]\n" \
"ds_read_b64_tr_b16 a[250:251], v38 offset:41984\n" \
"v_writelane_b32 v11, s16, 25\n" \
"v_mfma_f32_16x16x32_bf16 a[44:47], a[172:175], a[136:139], a[44:47]\n" \
"ds_read_b64_tr_b16 a[188:189], v39 offset:32768\n" \
"s_add_i32 s16, s15, 1\n" \
"v_mfma_f32_16x16x32_bf16 a[44:47], a[236:239], a[200:203], a[44:47]\n" \
"ds_read_b64_tr_b16 a[190:191], v39 offset:33792\n" \
"s_and_b32 s16, s16, 3\n" \
"v_mfma_f32_16x16x32_bf16 a[40:43], a[136:139], a[168:171], a[40:43]\n" \
"s_add_u32 s89, s42, 0x0\n" \
"buffer_store_dwordx4 v[192:195], v20, s[80:83], s89 offen offset:2048 sc1\n" \
"v_mfma_f32_16x16x32_bf16 a[40:43], a[200:203], a[232:235], a[40:43]\n" \
"ds_read_b64_tr_b16 a[252:253], v39 offset:40960\n" \
"s_or_b32 s16, s16, s5\n" \
"v_mfma_f32_16x16x32_bf16 a[56:59], a[140:143], a[168:171], a[56:59]\n" \
"ds_read_b64_tr_b16 a[254:255], v39 offset:41984\n" \
"s_mul_i32 s18, s16, 0x24000\n" \
"v_mfma_f32_16x16x32_bf16 a[56:59], a[204:207], a[232:235], a[56:59]\n" \
"s_add_i32 s18, s18, s19\n" \
"s_xor_b32 s13, s13, 2\n" \
"v_mfma_f32_16x16x32_bf16 a[60:63], a[172:175], a[140:143], a[60:63]\n" \
"s_add_i32 s15, s15, -1\n" \
"v_writelane_b32 v11, s18, 27\n" \
"v_mfma_f32_16x16x32_bf16 a[60:63], a[236:239], a[204:207], a[60:63]\n" \
"s_addk_i32 s84, 0x2000\n" \
"v_mfma_f32_16x16x32_bf16 a[52:55], a[164:167], a[140:143], a[52:55]\n" \
"buffer_load_dwordx4 v25, s[76:79], s84 offen offset:3072 lds\n" \
"v_mfma_f32_16x16x32_bf16 a[52:55], a[228:231], a[204:207], a[52:55]\n" \
"s_mul_i32 s18, s13, 0x24000\n" \
"s_and_b32 s15, s15, 3\n" \
"v_mfma_f32_16x16x32_bf16 a[48:51], a[160:163], a[140:143], a[48:51]\n" \
"s_add_i32 s18, s18, s19\n" \
"s_or_b32 s15, s15, s5\n" \
"v_mfma_f32_16x16x32_bf16 a[48:51], a[224:227], a[204:207], a[48:51]\n" \
"v_writelane_b32 v11, s18, 28\n" \
"s_mul_i32 s18, s15, 0x24000\n" \
"s_waitcnt lgkmcnt(15)\n" \
"v_mfma_f32_16x16x32_bf16 a[64:67], a[164:167], v[248:251], a[64:67]\n" \
"s_add_i32 s18, s18, s19\n" \
"v_writelane_b32 v11, s18, 29\n" \
"v_mfma_f32_16x16x32_bf16 a[64:67], a[228:231], v[48:51], a[64:67]\n" \
"s_or_b32 s18, s9, s14\n" \
"s_mul_i32 s18, s18, 0x24800\n" \
"v_mfma_f32_16x16x32_bf16 a[80:83], a[172:175], v[248:251], a[80:83]\n" \
"s_lshl_b32 s16, s16, 12\n" \
"s_add_i32 s19, s18, 0x3004800\n" \
"v_mfma_f32_16x16x32_bf16 a[80:83], a[236:239], v[48:51], a[80:83]\n" \
"s_add_i32 s20, s19, s16\n" \
"v_writelane_b32 v11, s20, 30\n" \
"v_mfma_f32_16x16x32_bf16 a[68:71], v[248:251], a[168:171], a[68:71]\n" \
"s_lshl_b32 s13, s13, 12\n" \
"s_lshl_b32 s15, s15, 12\n" \
"v_mfma_f32_16x16x32_bf16 a[68:71], v[48:51], a[232:235], a[68:71]\n" \
"s_waitcnt vmcnt(11) lgkmcnt(0)\n" \
"s_barrier\n" \
"v_mfma_f32_16x16x32_bf16 v[52:55], a[176:179], a[128:131], v[52:55]\n" \
"s_add_u32 m0, s40, 0x8000\n" \
"s_add_u32 s85, s39, 0xa000\n" \
"v_mfma_f32_16x16x32_bf16 v[52:55], a[240:243], a[192:195], v[52:55]\n" \
"buffer_load_dwordx4 v20, s[80:83], s85 offen sc1 lds\n" \
"v_mfma_f32_16x16x32_bf16 v[56:59], a[180:183], a[128:131], v[56:59]\n" \
"ds_read_b64_tr_b16 a[160:161], v32 offset:0\n" \
"v_accvgpr_read_b32 v224, a120\n" \
"v_mfma_f32_16x16x32_bf16 v[56:59], a[244:247], a[192:195], v[56:59]\n" \
"ds_read_b64_tr_b16 a[162:163], v32 offset:512\n" \
"v_accvgpr_read_b32 v225, a121\n" \
"v_mfma_f32_16x16x32_bf16 v[60:63], a[184:187], a[128:131], v[60:63]\n" \
"s_add_u32 m0, s40, 0x9000\n" \
"s_add_u32 s85, s39, 0xb000\n" \
"v_mfma_f32_16x16x32_bf16 v[60:63], a[248:251], a[192:195], v[60:63]\n" \
"buffer_load_dwordx4 v20, s[80:83], s85 offen sc1 lds\n" \
"v_mfma_f32_16x16x32_bf16 v[64:67], a[188:191], a[128:131], v[64:67]\n" \
"ds_read_b64_tr_b16 a[224:225], v32 offset:4096\n" \
"v_accvgpr_read_b32 v226, a122\n" \
"v_mfma_f32_16x16x32_bf16 v[64:67], a[252:255], a[192:195], v[64:67]\n" \
"ds_read_b128 a[128:131], v28 offset:32768\n" \
"v_accvgpr_read_b32 v227, a123\n" \
"v_mfma_f32_16x16x32_bf16 v[80:83], a[188:191], a[132:135], v[80:83]\n" \
"ds_read_b128 a[192:195], v29 offset:32768\n" \
"v_accvgpr_read_b32 v180, a124\n" \
"v_mfma_f32_16x16x32_bf16 v[80:83], a[252:255], a[196:199], v[80:83]\n" \
"s_add_u32 s88, s42, 0x0\n" \
"buffer_store_dwordx4 v[196:199], v20, s[80:83], s88 offen offset:3072 sc1\n" \
"v_mfma_f32_16x16x32_bf16 v[76:79], a[184:187], a[132:135], v[76:79]\n" \
"ds_read_b64_tr_b16 a[226:227], v32 offset:4608\n" \
"v_accvgpr_read_b32 v181, a125\n" \
"v_mfma_f32_16x16x32_bf16 v[76:79], a[248:251], a[196:199], v[76:79]\n" \
"ds_read_b64_tr_b16 a[164:165], v33 offset:0\n" \
"v_accvgpr_read_b32 v182, a126\n" \
"v_mfma_f32_16x16x32_bf16 v[72:75], a[180:183], a[132:135], v[72:75]\n" \
"s_add_u32 m0, s40, 0xb000\n" \
"s_movk_i32 s84, 0x280\n" \
"v_mfma_f32_16x16x32_bf16 v[72:75], a[244:247], a[196:199], v[72:75]\n" \
"buffer_load_dwordx4 v21, s[76:79], s84 offen offset:0 lds\n" \
"v_mfma_f32_16x16x32_bf16 v[68:71], a[176:179], a[132:135], v[68:71]\n" \
"ds_read_b64_tr_b16 a[166:167], v33 offset:512\n" \
"v_accvgpr_read_b32 v183, a127\n" \
"v_mfma_f32_16x16x32_bf16 v[68:71], a[240:243], a[196:199], v[68:71]\n" \
"ds_read_b128 a[132:135], v28 offset:34816\n" \
"v_cvt_pk_bf16_f32 v224, v224, v225\n" \
"v_mfma_f32_16x16x32_bf16 v[84:87], a[176:179], a[136:139], v[84:87]\n" \
"ds_read_b128 a[196:199], v29 offset:34816\n" \
"v_cvt_pk_bf16_f32 v225, v226, v227\n" \
"v_mfma_f32_16x16x32_bf16 v[84:87], a[240:243], a[200:203], v[84:87]\n" \
"s_add_u32 m0, s31, 0x8000\n" \
"s_mov_b32 s84, 0x280000\n" \
"v_mfma_f32_16x16x32_bf16 v[88:91], a[180:183], a[136:139], v[88:91]\n" \
"buffer_load_dwordx4 v26, s[68:71], s84 offen offset:0 lds\n" \
"v_mfma_f32_16x16x32_bf16 v[88:91], a[244:247], a[200:203], v[88:91]\n" \
"ds_read_b64_tr_b16 a[228:229], v33 offset:4096\n" \
"v_cvt_pk_bf16_f32 v226, v180, v181\n" \
"v_mfma_f32_16x16x32_bf16 v[92:95], a[184:187], a[136:139], v[92:95]\n" \
"ds_read_b64_tr_b16 a[230:231], v33 offset:4608\n" \
"v_cvt_pk_bf16_f32 v227, v182, v183\n" \
"v_mfma_f32_16x16x32_bf16 v[92:95], a[248:251], a[200:203], v[92:95]\n" \
"s_addk_i32 s84, 0x7c00\n" \
"v_mfma_f32_16x16x32_bf16 v[96:99], a[188:191], a[136:139], v[96:99]\n" \
"buffer_load_dwordx4 v26, s[68:71], s84 offen offset:1024 lds\n" \
"v_mfma_f32_16x16x32_bf16 v[96:99], a[252:255], a[200:203], v[96:99]\n" \
"ds_read_b128 a[136:139], v28 offset:36864\n" \
"v_accvgpr_read_b32 v220, a112\n" \
"v_mfma_f32_16x16x32_bf16 v[112:115], a[188:191], a[140:143], v[112:115]\n" \
"ds_read_b128 a[200:203], v29 offset:36864\n" \
"v_accvgpr_read_b32 v221, a113\n" \
"v_mfma_f32_16x16x32_bf16 v[112:115], a[252:255], a[204:207], v[112:115]\n" \
"s_addk_i32 s84, 0x7c00\n" \
"v_mfma_f32_16x16x32_bf16 v[108:111], a[184:187], a[140:143], v[108:111]\n" \
"buffer_load_dwordx4 v27, s[68:71], s84 offen offset:2048 lds\n" \
"v_mfma_f32_16x16x32_bf16 v[108:111], a[248:251], a[204:207], v[108:111]\n" \
"ds_read_b64_tr_b16 a[172:173], v35 offset:0\n" \
"v_accvgpr_read_b32 v222, a114\n" \
"v_mfma_f32_16x16x32_bf16 v[104:107], a[180:183], a[140:143], v[104:107]\n" \
"ds_read_b64_tr_b16 a[174:175], v35 offset:512\n" \
"v_accvgpr_read_b32 v223, a115\n" \
"v_mfma_f32_16x16x32_bf16 v[104:107], a[244:247], a[204:207], v[104:107]\n" \
"ds_read_b64_tr_b16 a[236:237], v35 offset:4096\n" \
"v_accvgpr_read_b32 v180, a116\n" \
"v_mfma_f32_16x16x32_bf16 v[100:103], a[176:179], a[140:143], v[100:103]\n" \
"v_accvgpr_read_b32 v181, a117\n" \
"v_accvgpr_read_b32 v182, a118\n" \
"v_mfma_f32_16x16x32_bf16 v[100:103], a[240:243], a[204:207], v[100:103]\n" \
"ds_read_b128 a[140:143], v28 offset:38912\n" \
"v_accvgpr_read_b32 v183, a119\n" \
"v_mfma_f32_16x16x32_bf16 v[116:119], a[176:179], a[144:147], v[116:119]\n" \
"ds_read_b128 a[204:207], v29 offset:38912\n" \
"v_cvt_pk_bf16_f32 v220, v220, v221\n" \
"v_mfma_f32_16x16x32_bf16 v[116:119], a[240:243], a[208:211], v[116:119]\n" \
"s_addk_i32 s84, 0x7c00\n" \
"v_mfma_f32_16x16x32_bf16 v[120:123], a[180:183], a[144:147], v[120:123]\n" \
"buffer_load_dwordx4 v27, s[68:71], s84 offen offset:3072 lds\n" \
"v_mfma_f32_16x16x32_bf16 v[120:123], a[244:247], a[208:211], v[120:123]\n" \
"s_add_u32 m0, s30, 0x0\n" \
"s_movk_i32 s84, 0x300\n" \
"s_nop 0\n" \
"v_mfma_f32_16x16x32_bf16 v[124:127], a[184:187], a[144:147], v[124:127]\n" \
"buffer_load_dwordx4 v24, s[76:79], s84 offen offset:0 lds\n" \
"v_mfma_f32_16x16x32_bf16 v[124:127], a[248:251], a[208:211], v[124:127]\n" \
"ds_read_b64_tr_b16 a[238:239], v35 offset:4608\n" \
"v_cvt_pk_bf16_f32 v221, v222, v223\n" \
"v_mfma_f32_16x16x32_bf16 v[128:131], a[188:191], a[144:147], v[128:131]\n" \
"s_add_u32 s89, s42, 0x1000\n" \
"buffer_store_dwordx4 a[84:87], v20, s[80:83], s89 offen offset:3072 sc1\n" \
"v_mfma_f32_16x16x32_bf16 v[128:131], a[252:255], a[208:211], v[128:131]\n" \
"ds_read_b128 a[144:147], v28 offset:40960\n" \
"v_cvt_pk_bf16_f32 v222, v180, v181\n" \
"v_mfma_f32_16x16x32_bf16 v[144:147], a[188:191], a[148:151], v[144:147]\n" \
"ds_read_b128 a[208:211], v29 offset:40960\n" \
"v_cvt_pk_bf16_f32 v223, v182, v183\n" \
"v_mfma_f32_16x16x32_bf16 v[144:147], a[252:255], a[212:215], v[144:147]\n" \
"ds_read_b64_tr_b16 a[168:169], v34 offset:0\n" \
"v_accvgpr_read_b32 v216, a104\n" \
"v_mfma_f32_16x16x32_bf16 v[140:143], a[184:187], a[148:151], v[140:143]\n" \
"ds_read_b64_tr_b16 a[170:171], v34 offset:512\n" \
"v_accvgpr_read_b32 v217, a105\n" \
"v_mfma_f32_16x16x32_bf16 v[140:143], a[248:251], a[212:215], v[140:143]\n" \
"s_movk_i32 s84, 0x2300\n" \
"s_nop 0\n" \
"v_mfma_f32_16x16x32_bf16 v[136:139], a[180:183], a[148:151], v[136:139]\n" \
"buffer_load_dwordx4 v25, s[76:79], s84 offen offset:1024 lds\n" \
"v_mfma_f32_16x16x32_bf16 v[136:139], a[244:247], a[212:215], v[136:139]\n" \
"ds_read_b64_tr_b16 a[232:233], v34 offset:4096\n" \
"v_accvgpr_read_b32 v218, a106\n" \
"v_mfma_f32_16x16x32_bf16 v[132:135], a[176:179], a[148:151], v[132:135]\n" \
"ds_read_b64_tr_b16 a[234:235], v34 offset:4608\n" \
"v_accvgpr_read_b32 v219, a107\n" \
"v_mfma_f32_16x16x32_bf16 v[132:135], a[240:243], a[212:215], v[132:135]\n" \
"ds_read_b128 a[148:151], v28 offset:43008\n" \
"v_accvgpr_read_b32 v180, a108\n" \
"v_mfma_f32_16x16x32_bf16 v[148:151], a[176:179], a[152:155], v[148:151]\n" \
"ds_read_b128 a[212:215], v29 offset:43008\n" \
"v_accvgpr_read_b32 v181, a109\n" \
"v_mfma_f32_16x16x32_bf16 v[148:151], a[240:243], a[216:219], v[148:151]\n" \
"s_movk_i32 s84, 0x4300\n" \
"v_mfma_f32_16x16x32_bf16 v[152:155], a[180:183], a[152:155], v[152:155]\n" \
"buffer_load_dwordx4 v24, s[76:79], s84 offen offset:2048 lds\n" \
"v_mfma_f32_16x16x32_bf16 v[152:155], a[244:247], a[216:219], v[152:155]\n" \
"s_movk_i32 s84, 0x6300\n" \
"v_mfma_f32_16x16x32_bf16 v[156:159], a[184:187], a[152:155], v[156:159]\n" \
"buffer_load_dwordx4 v25, s[76:79], s84 offen offset:3072 lds\n" \
"v_mfma_f32_16x16x32_bf16 v[156:159], a[248:251], a[216:219], v[156:159]\n" \
"ds_read_b128 v[248:251], v22 offset:0\n" \
"v_accvgpr_read_b32 v182, a110\n" \
"v_mfma_f32_16x16x32_bf16 v[160:163], a[188:191], a[152:155], v[160:163]\n" \
"ds_read_b128 v[48:51], v23 offset:0\n" \
"v_accvgpr_read_b32 v183, a111\n" \
"v_mfma_f32_16x16x32_bf16 v[160:163], a[252:255], a[216:219], v[160:163]\n" \
"ds_read_b128 a[152:155], v28 offset:45056\n" \
"v_cvt_pk_bf16_f32 v216, v216, v217\n" \
"v_mfma_f32_16x16x32_bf16 v[176:179], a[188:191], a[156:159], v[176:179]\n" \
"ds_read_b128 a[216:219], v29 offset:45056\n" \
"v_cvt_pk_bf16_f32 v217, v218, v219\n" \
"v_mfma_f32_16x16x32_bf16 v[176:179], a[252:255], a[220:223], v[176:179]\n" \
"v_cvt_pk_bf16_f32 v218, v180, v181\n" \
"v_cvt_pk_bf16_f32 v219, v182, v183\n" \
"v_mfma_f32_16x16x32_bf16 v[172:175], a[184:187], a[156:159], v[172:175]\n" \
"v_accvgpr_read_b32 v192, a96\n" \
"v_accvgpr_read_b32 v193, a97\n" \
"v_mfma_f32_16x16x32_bf16 v[172:175], a[248:251], a[220:223], v[172:175]\n" \
"v_accvgpr_read_b32 v194, a98\n" \
"v_accvgpr_read_b32 v195, a99\n" \
"v_mfma_f32_16x16x32_bf16 v[168:171], a[180:183], a[156:159], v[168:171]\n" \
"v_accvgpr_read_b32 v180, a100\n" \
"v_accvgpr_read_b32 v181, a101\n" \
"v_mfma_f32_16x16x32_bf16 v[168:171], a[244:247], a[220:223], v[168:171]\n" \
"v_accvgpr_read_b32 v182, a102\n" \
"v_accvgpr_read_b32 v183, a103\n" \
"v_mfma_f32_16x16x32_bf16 v[164:167], a[176:179], a[156:159], v[164:167]\n" \
"v_cvt_pk_bf16_f32 v192, v192, v193\n" \
"v_cvt_pk_bf16_f32 v193, v194, v195\n" \
"v_mfma_f32_16x16x32_bf16 v[164:167], a[240:243], a[220:223], v[164:167]\n" \
"ds_read_b128 a[156:159], v28 offset:47104\n" \
"ds_read_b128 a[220:223], v29 offset:47104\n" \
"s_waitcnt lgkmcnt(15)\n" \
"v_mfma_f32_16x16x32_bf16 a[0:3], a[160:163], a[128:131], a[0:3]\n" \
"s_add_u32 m0, s30, 0x1000\n" \
"s_addk_i32 s84, 0x3000\n" \
"v_mfma_f32_16x16x32_bf16 a[0:3], a[224:227], a[192:195], a[0:3]\n" \
"buffer_load_dwordx4 v24, s[76:79], s84 offen offset:0 lds\n" \
"v_mfma_f32_16x16x32_bf16 a[4:7], a[164:167], a[128:131], a[4:7]\n" \
"ds_read_b64_tr_b16 a[176:177], v36 offset:0\n" \
"v_cvt_pk_bf16_f32 v194, v180, v181\n" \
"v_mfma_f32_16x16x32_bf16 a[4:7], a[228:231], a[192:195], a[4:7]\n" \
"ds_read_b64_tr_b16 a[178:179], v36 offset:1024\n" \
"v_cvt_pk_bf16_f32 v195, v182, v183\n" \
"v_mfma_f32_16x16x32_bf16 a[12:15], a[172:175], a[128:131], a[12:15]\n" \
"ds_read_b64_tr_b16 a[240:241], v36 offset:8192\n" \
"v_accvgpr_read_b32 v188, a88\n" \
"s_waitcnt lgkmcnt(15)\n" \
"v_mfma_f32_16x16x32_bf16 a[12:15], a[236:239], a[192:195], a[12:15]\n" \
"s_add_u32 s88, s42, 0x1000\n" \
"buffer_store_dwordx4 v[252:255], v20, s[80:83], s88 offen offset:2048 sc1\n" \
"s_waitcnt lgkmcnt(13)\n" \
"v_mfma_f32_16x16x32_bf16 a[8:11], a[128:131], a[168:171], a[8:11]\n" \
"ds_read_b64_tr_b16 a[242:243], v36 offset:9216\n" \
"v_accvgpr_read_b32 v189, a89\n" \
"s_waitcnt lgkmcnt(12)\n" \
"s_nop 0\n" \
"v_mfma_f32_16x16x32_bf16 a[8:11], a[192:195], a[232:235], a[8:11]\n" \
"ds_read_b64_tr_b16 a[180:181], v37 offset:0\n" \
"v_accvgpr_read_b32 v190, a90\n" \
"v_mfma_f32_16x16x32_bf16 a[24:27], a[132:135], a[168:171], a[24:27]\n" \
"ds_read_b64_tr_b16 a[182:183], v37 offset:1024\n" \
"v_accvgpr_read_b32 v191, a91\n" \
"v_mfma_f32_16x16x32_bf16 a[24:27], a[196:199], a[232:235], a[24:27]\n" \
"ds_read_b64_tr_b16 a[244:245], v37 offset:8192\n" \
"v_accvgpr_read_b32 v180, a92\n" \
"v_mfma_f32_16x16x32_bf16 a[28:31], a[172:175], a[132:135], a[28:31]\n" \
"v_accvgpr_read_b32 v181, a93\n" \
"v_accvgpr_read_b32 v182, a94\n" \
"v_mfma_f32_16x16x32_bf16 a[28:31], a[236:239], a[196:199], a[28:31]\n" \
"s_addk_i32 s84, 0x2000\n" \
"s_nop 0\n" \
"v_mfma_f32_16x16x32_bf16 a[20:23], a[164:167], a[132:135], a[20:23]\n" \
"buffer_load_dwordx4 v25, s[76:79], s84 offen offset:1024 lds\n" \
"v_mfma_f32_16x16x32_bf16 a[20:23], a[228:231], a[196:199], a[20:23]\n" \
"ds_read_b64_tr_b16 a[246:247], v37 offset:9216\n" \
"v_accvgpr_read_b32 v183, a95\n" \
"v_mfma_f32_16x16x32_bf16 a[16:19], a[160:163], a[132:135], a[16:19]\n" \
"ds_read_b64_tr_b16 a[184:185], v38 offset:0\n" \
"v_cvt_pk_bf16_f32 v188, v188, v189\n" \
"v_mfma_f32_16x16x32_bf16 a[16:19], a[224:227], a[196:199], a[16:19]\n" \
"ds_read_b64_tr_b16 a[186:187], v38 offset:1024\n" \
"v_cvt_pk_bf16_f32 v189, v190, v191\n" \
"v_mfma_f32_16x16x32_bf16 a[32:35], a[160:163], a[136:139], a[32:35]\n" \
"s_addk_i32 s84, 0x2000\n" \
"s_nop 0\n" \
"v_mfma_f32_16x16x32_bf16 a[32:35], a[224:227], a[200:203], a[32:35]\n" \
"buffer_load_dwordx4 v24, s[76:79], s84 offen offset:2048 lds\n" \
"v_mfma_f32_16x16x32_bf16 a[36:39], a[164:167], a[136:139], a[36:39]\n" \
"ds_read_b64_tr_b16 a[248:249], v38 offset:8192\n" \
"v_cvt_pk_bf16_f32 v190, v180, v181\n" \
"v_mfma_f32_16x16x32_bf16 a[36:39], a[228:231], a[200:203], a[36:39]\n" \
"ds_read_b64_tr_b16 a[250:251], v38 offset:9216\n" \
"v_cvt_pk_bf16_f32 v191, v182, v183\n" \
"v_mfma_f32_16x16x32_bf16 a[44:47], a[172:175], a[136:139], a[44:47]\n" \
"ds_read_b64_tr_b16 a[188:189], v39 offset:0\n" \
"v_accvgpr_read_b32 v184, a72\n" \
"v_mfma_f32_16x16x32_bf16 a[44:47], a[236:239], a[200:203], a[44:47]\n" \
"ds_read_b64_tr_b16 a[190:191], v39 offset:1024\n" \
"v_accvgpr_read_b32 v185, a73\n" \
"v_mfma_f32_16x16x32_bf16 a[40:43], a[136:139], a[168:171], a[40:43]\n" \
"s_add_u32 s89, s42, 0x2000\n" \
"buffer_store_dwordx4 v[200:203], v20, s[80:83], s89 offen offset:2048 sc1\n" \
"v_mfma_f32_16x16x32_bf16 a[40:43], a[200:203], a[232:235], a[40:43]\n" \
"ds_read_b64_tr_b16 a[252:253], v39 offset:8192\n" \
".fill (L_pb + 28384 - .) / 4, 4, 0xBF800000\n" \
"L_pp7:\n" \
"s_cbranch_execz L_pret\n" \
"s_nop 0\n" \
"v_accvgpr_read_b32 v186, a74\n" \
"v_mfma_f32_16x16x32_bf16 a[56:59], a[140:143], a[168:171], a[56:59]\n" \
"ds_read_b64_tr_b16 a[254:255], v39 offset:9216\n" \
"v_accvgpr_read_b32 v187, a75\n" \
"v_mfma_f32_16x16x32_bf16 a[56:59], a[204:207], a[232:235], a[56:59]\n" \
"v_accvgpr_read_b32 v180, a76\n" \
"v_accvgpr_read_b32 v181, a77\n" \
"v_mfma_f32_16x16x32_bf16 a[60:63], a[172:175], a[140:143], a[60:63]\n" \
"v_accvgpr_read_b32 v182, a78\n" \
"v_accvgpr_read_b32 v183, a79\n" \
"v_mfma_f32_16x16x32_bf16 a[60:63], a[236:239], a[204:207], a[60:63]\n" \
"s_addk_i32 s84, 0x2000\n" \
"v_mfma_f32_16x16x32_bf16 a[52:55], a[164:167], a[140:143], a[52:55]\n" \
"buffer_load_dwordx4 v25, s[76:79], s84 offen offset:3072 lds\n" \
"v_mfma_f32_16x16x32_bf16 a[52:55], a[228:231], a[204:207], a[52:55]\n" \
"v_cvt_pk_bf16_f32 v184, v184, v185\n" \
"v_cvt_pk_bf16_f32 v185, v186, v187\n" \
"v_mfma_f32_16x16x32_bf16 a[48:51], a[160:163], a[140:143], a[48:51]\n" \
"v_cvt_pk_bf16_f32 v186, v180, v181\n" \
"v_cvt_pk_bf16_f32 v187, v182, v183\n" \
"v_mfma_f32_16x16x32_bf16 a[48:51], a[224:227], a[204:207], a[48:51]\n" \
"s_sub_i32 s14, s9, s14\n" \
"s_add_i32 s20, s19, s13\n" \
"s_waitcnt lgkmcnt(15)\n" \
"v_mfma_f32_16x16x32_bf16 a[64:67], a[164:167], v[248:251], a[64:67]\n" \
"v_writelane_b32 v11, s20, 31\n" \
"s_add_i32 s19, s19, s15\n" \
"v_mfma_f32_16x16x32_bf16 a[64:67], a[228:231], v[48:51], a[64:67]\n" \
"s_mul_i32 s14, s14, 0x24800\n" \
"v_writelane_b32 v11, s19, 32\n" \
"v_mfma_f32_16x16x32_bf16 a[80:83], a[172:175], v[248:251], a[80:83]\n" \
"s_add_i32 s19, s14, 0x4120800\n" \
"s_add_i32 s16, s19, s16\n" \
"v_mfma_f32_16x16x32_bf16 a[80:83], a[236:239], v[48:51], a[80:83]\n" \
"v_writelane_b32 v11, s16, 33\n" \
"s_add_i32 s13, s19, s13\n" \
"v_mfma_f32_16x16x32_bf16 a[68:71], v[248:251], a[168:171], a[68:71]\n" \
"v_writelane_b32 v11, s13, 34\n" \
"s_add_i32 s19, s19, s15\n" \
"v_mfma_f32_16x16x32_bf16 a[68:71], v[48:51], a[232:235], a[68:71]\n" \
"s_waitcnt vmcnt(11) lgkmcnt(0)\n" \
"s_barrier\n" \
"v_mov_b32_e32 v45, s62\n" \
"s_mov_b64 exec, 1\n" \
"buffer_store_dword v45, off, s[80:83], s37 offset:0 sc1\n" \
"s_mov_b64 exec, -1\n" \
"v_xor_b32_e32 v28, 0x20000, v28\n" \
"v_xor_b32_e32 v29, 0x20000, v29\n" \
"v_mfma_f32_16x16x32_bf16 v[52:55], a[176:179], a[128:131], v[52:55]\n" \
"s_add_u32 m0, s40, 0x0\n" \
"s_add_u32 s85, s39, 0xc000\n" \
"v_mfma_f32_16x16x32_bf16 v[52:55], a[240:243], a[192:195], v[52:55]\n" \
"buffer_load_dwordx4 v20, s[80:83], s85 offen sc1 lds\n" \
"v_mfma_f32_16x16x32_bf16 v[56:59], a[180:183], a[128:131], v[56:59]\n" \
"ds_read_b64_tr_b16 a[160:161], v32 offset:32768\n" \
"s_or_b32 s13, s18, 0x3000000\n" \
"v_mfma_f32_16x16x32_bf16 v[56:59], a[244:247], a[192:195], v[56:59]\n" \
"ds_read_b64_tr_b16 a[162:163], v32 offset:33280\n" \
"s_lshl_b32 s15, s6, 11\n" \
"v_mfma_f32_16x16x32_bf16 v[60:63], a[184:187], a[128:131], v[60:63]\n" \
"s_add_u32 m0, s40, 0x1000\n" \
"s_add_u32 s85, s39, 0xd000\n" \
"v_mfma_f32_16x16x32_bf16 v[60:63], a[248:251], a[192:195], v[60:63]\n" \
"buffer_load_dwordx4 v20, s[80:83], s85 offen sc1 lds\n" \
"v_mfma_f32_16x16x32_bf16 v[64:67], a[188:191], a[128:131], v[64:67]\n" \
"ds_read_b64_tr_b16 a[224:225], v32 offset:36864\n" \
"v_writelane_b32 v11, s19, 35\n" \
"v_mfma_f32_16x16x32_bf16 v[64:67], a[252:255], a[192:195], v[64:67]\n" \
"ds_read_b128 a[128:131], v28 offset:0\n" \
"s_add_i32 s16, s13, s15\n" \
"v_mfma_f32_16x16x32_bf16 v[80:83], a[188:191], a[132:135], v[80:83]\n" \
"ds_read_b128 a[192:195], v29 offset:0\n" \
"v_writelane_b32 v11, s16, 36\n" \
"v_mfma_f32_16x16x32_bf16 v[80:83], a[252:255], a[196:199], v[80:83]\n" \
"s_add_u32 m0, s40, 0x3000\n" \
"s_movk_i32 s84, 0x300\n" \
"v_mfma_f32_16x16x32_bf16 v[76:79], a[184:187], a[132:135], v[76:79]\n" \
"buffer_load_dwordx4 v21, s[76:79], s84 offen offset:0 lds\n" \
"v_mfma_f32_16x16x32_bf16 v[76:79], a[248:251], a[196:199], v[76:79]\n" \
"ds_read_b64_tr_b16 a[226:227], v32 offset:37376\n" \
"s_add_i32 s16, s14, 0x411c000\n" \
"v_mfma_f32_16x16x32_bf16 v[72:75], a[180:183], a[132:135], v[72:75]\n" \
"ds_read_b64_tr_b16 a[164:165], v33 offset:32768\n" \
"s_add_i32 s19, s16, s15\n" \
"v_mfma_f32_16x16x32_bf16 v[72:75], a[244:247], a[196:199], v[72:75]\n" \
"v_writelane_b32 v11, s19, 37\n" \
"s_add_i32 s18, s18, 0x3004000\n" \
"v_mfma_f32_16x16x32_bf16 v[68:71], a[176:179], a[132:135], v[68:71]\n" \
"ds_read_b64_tr_b16 a[166:167], v33 offset:33280\n" \
"v_writelane_b32 v11, s18, 38\n" \
"v_mfma_f32_16x16x32_bf16 v[68:71], a[240:243], a[196:199], v[68:71]\n" \
"ds_read_b128 a[132:135], v28 offset:2048\n" \
"s_add_i32 s14, s14, 0x4120000\n" \
"v_mfma_f32_16x16x32_bf16 v[84:87], a[176:179], a[136:139], v[84:87]\n" \
"ds_read_b128 a[196:199], v29 offset:2048\n" \
"v_writelane_b32 v11, s14, 39\n" \
"v_mfma_f32_16x16x32_bf16 v[84:87], a[240:243], a[200:203], v[84:87]\n" \
"s_add_u32 m0, s31, 0x0\n" \
"s_mov_b32 s84, 0x300000\n" \
"v_mfma_f32_16x16x32_bf16 v[88:91], a[180:183], a[136:139], v[88:91]\n" \
"buffer_load_dwordx4 v26, s[68:71], s84 offen offset:0 lds\n" \
"v_mfma_f32_16x16x32_bf16 v[88:91], a[244:247], a[200:203], v[88:91]\n" \
"s_add_u32 s88, s42, 0x2000\n" \
"buffer_store_dwordx4 v[204:207], v20, s[80:83], s88 offen offset:3072 sc1\n" \
"v_mfma_f32_16x16x32_bf16 v[92:95], a[184:187], a[136:139], v[92:95]\n" \
"ds_read_b64_tr_b16 a[228:229], v33 offset:36864\n" \
"s_lshl_b32 s14, s4, 2\n" \
"v_mfma_f32_16x16x32_bf16 v[92:95], a[248:251], a[200:203], v[92:95]\n" \
"ds_read_b64_tr_b16 a[230:231], v33 offset:37376\n" \
"s_or_b32 s8, s14, s8\n" \
"v_mfma_f32_16x16x32_bf16 v[96:99], a[188:191], a[136:139], v[96:99]\n" \
"s_mul_i32 s8, s8, 0x90000\n" \
"s_lshl_b32 s14, s3, 14\n" \
"v_mfma_f32_16x16x32_bf16 v[96:99], a[252:255], a[200:203], v[96:99]\n" \
"ds_read_b128 a[136:139], v28 offset:4096\n" \
"s_add_i32 s8, s8, s14\n" \
"v_mfma_f32_16x16x32_bf16 v[112:115], a[188:191], a[140:143], v[112:115]\n" \
"ds_read_b128 a[200:203], v29 offset:4096\n" \
"s_lshl_b32 s9, s9, 4\n" \
"v_mfma_f32_16x16x32_bf16 v[112:115], a[252:255], a[204:207], v[112:115]\n" \
"s_addk_i32 s84, 0x7c00\n" \
"v_mfma_f32_16x16x32_bf16 v[108:111], a[184:187], a[140:143], v[108:111]\n" \
"buffer_load_dwordx4 v26, s[68:71], s84 offen offset:1024 lds\n" \
"v_mfma_f32_16x16x32_bf16 v[108:111], a[248:251], a[204:207], v[108:111]\n" \
"ds_read_b64_tr_b16 a[172:173], v35 offset:32768\n" \
"s_lshl_b32 s14, s4, 7\n" \
"v_mfma_f32_16x16x32_bf16 v[104:107], a[180:183], a[140:143], v[104:107]\n" \
"s_addk_i32 s84, 0x7c00\n" \
"v_mfma_f32_16x16x32_bf16 v[104:107], a[244:247], a[204:207], v[104:107]\n" \
"buffer_load_dwordx4 v27, s[68:71], s84 offen offset:2048 lds\n" \
"v_mfma_f32_16x16x32_bf16 v[100:103], a[176:179], a[140:143], v[100:103]\n" \
"ds_read_b64_tr_b16 a[174:175], v35 offset:33280\n" \
"s_or_b32 s18, s9, s14\n" \
"v_mfma_f32_16x16x32_bf16 v[100:103], a[240:243], a[204:207], v[100:103]\n" \
"ds_read_b128 a[140:143], v28 offset:6144\n" \
"s_sub_i32 s9, s9, s14\n" \
"v_mfma_f32_16x16x32_bf16 v[116:119], a[176:179], a[144:147], v[116:119]\n" \
"ds_read_b128 a[204:207], v29 offset:6144\n" \
"s_mulk_i32 s18, 0x480\n" \
"v_mfma_f32_16x16x32_bf16 v[116:119], a[240:243], a[208:211], v[116:119]\n" \
"s_addk_i32 s84, 0x7c00\n" \
"v_mfma_f32_16x16x32_bf16 v[120:123], a[180:183], a[144:147], v[120:123]\n" \
"buffer_load_dwordx4 v27, s[68:71], s84 offen offset:3072 lds\n" \
"v_mfma_f32_16x16x32_bf16 v[120:123], a[244:247], a[208:211], v[120:123]\n" \
"ds_read_b64_tr_b16 a[236:237], v35 offset:36864\n" \
"s_mulk_i32 s9, 0x480\n" \
"v_mfma_f32_16x16x32_bf16 v[124:127], a[184:187], a[144:147], v[124:127]\n" \
"ds_read_b64_tr_b16 a[238:239], v35 offset:37376\n" \
"s_or_b32 s14, s18, s12\n" \
"v_mfma_f32_16x16x32_bf16 v[124:127], a[248:251], a[208:211], v[124:127]\n" \
"s_add_u32 m0, s30, 0x8000\n" \
"s_movk_i32 s84, 0x380\n" \
"v_mfma_f32_16x16x32_bf16 v[128:131], a[188:191], a[144:147], v[128:131]\n" \
"buffer_load_dwordx4 v24, s[76:79], s84 offen offset:0 lds\n" \
"v_mfma_f32_16x16x32_bf16 v[128:131], a[252:255], a[208:211], v[128:131]\n" \
"ds_read_b128 a[144:147], v28 offset:8192\n" \
"s_or_b32 s9, s9, s12\n" \
"v_mfma_f32_16x16x32_bf16 v[144:147], a[188:191], a[148:151], v[144:147]\n" \
"ds_read_b128 a[208:211], v29 offset:8192\n" \
"s_and_b32 s12, s2, 1\n" \
"v_mfma_f32_16x16x32_bf16 v[144:147], a[252:255], a[212:215], v[144:147]\n" \
"ds_read_b64_tr_b16 a[168:169], v34 offset:32768\n" \
"v_mfma_f32_16x16x32_bf16 v[140:143], a[184:187], a[148:151], v[140:143]\n" \
"s_movk_i32 s84, 0x2380\n" \
"s_nop 0\n" \
"v_mfma_f32_16x16x32_bf16 v[140:143], a[248:251], a[212:215], v[140:143]\n" \
"buffer_load_dwordx4 v25, s[76:79], s84 offen offset:1024 lds\n" \
"v_mfma_f32_16x16x32_bf16 v[136:139], a[180:183], a[148:151], v[136:139]\n" \
"ds_read_b64_tr_b16 a[170:171], v34 offset:33280\n" \
"v_mfma_f32_16x16x32_bf16 v[136:139], a[244:247], a[212:215], v[136:139]\n" \
"ds_read_b64_tr_b16 a[232:233], v34 offset:36864\n" \
"v_mfma_f32_16x16x32_bf16 v[132:135], a[176:179], a[148:151], v[132:135]\n" \
"ds_read_b64_tr_b16 a[234:235], v34 offset:37376\n" \
"v_mfma_f32_16x16x32_bf16 v[132:135], a[240:243], a[212:215], v[132:135]\n" \
"ds_read_b128 a[148:151], v28 offset:10240\n" \
"v_mfma_f32_16x16x32_bf16 v[148:151], a[176:179], a[152:155], v[148:151]\n" \
"ds_read_b128 a[212:215], v29 offset:10240\n" \
"v_mfma_f32_16x16x32_bf16 v[148:151], a[240:243], a[216:219], v[148:151]\n" \
"s_movk_i32 s84, 0x4380\n" \
"v_mfma_f32_16x16x32_bf16 v[152:155], a[180:183], a[152:155], v[152:155]\n" \
"buffer_load_dwordx4 v24, s[76:79], s84 offen offset:2048 lds\n" \
"v_mfma_f32_16x16x32_bf16 v[152:155], a[244:247], a[216:219], v[152:155]\n" \
"s_movk_i32 s84, 0x6380\n" \
"v_mfma_f32_16x16x32_bf16 v[156:159], a[184:187], a[152:155], v[156:159]\n" \
"buffer_load_dwordx4 v25, s[76:79], s84 offen offset:3072 lds\n" \
"v_mfma_f32_16x16x32_bf16 v[156:159], a[248:251], a[216:219], v[156:159]\n" \
"ds_read_b128 v[248:251], v22 offset:32768\n" \
"v_mfma_f32_16x16x32_bf16 v[160:163], a[188:191], a[152:155], v[160:163]\n" \
"ds_read_b128 v[48:51], v23 offset:32768\n" \
"v_mfma_f32_16x16x32_bf16 v[160:163], a[252:255], a[216:219], v[160:163]\n" \
"ds_read_b128 a[152:155], v28 offset:12288\n" \
"v_mfma_f32_16x16x32_bf16 v[176:179], a[188:191], a[156:159], v[176:179]\n" \
"ds_read_b128 a[216:219], v29 offset:12288\n" \
"v_mfma_f32_16x16x32_bf16 v[176:179], a[252:255], a[220:223], v[176:179]\n" \
"s_cmp_lg_u32 s12, 0\n" \
"s_cselect_b32 s13, s16, s13\n" \
"v_mfma_f32_16x16x32_bf16 v[172:175], a[184:187], a[156:159], v[172:175]\n" \
"s_bfe_u32 s16, s2, 0x20001\n" \
"s_addk_i32 s13, 0x4000\n" \
"v_mfma_f32_16x16x32_bf16 v[172:175], a[248:251], a[220:223], v[172:175]\n" \
"s_xor_b32 s16, s16, s3\n" \
"v_mfma_f32_16x16x32_bf16 v[168:171], a[180:183], a[156:159], v[168:171]\n" \
"s_cmp_lg_u32 s16, 0\n" \
"s_cselect_b32 s13, s7, s13\n" \
"v_mfma_f32_16x16x32_bf16 v[168:171], a[244:247], a[220:223], v[168:171]\n" \
"s_lshl_b32 s6, s6, 16\n" \
"v_writelane_b32 v11, s13, 40\n" \
"v_mfma_f32_16x16x32_bf16 v[164:167], a[176:179], a[156:159], v[164:167]\n" \
"s_add_i32 s13, s8, 0x4240000\n" \
"s_add_i32 s6, s13, s6\n" \
"v_mfma_f32_16x16x32_bf16 v[164:167], a[240:243], a[220:223], v[164:167]\n" \
"ds_read_b128 a[156:159], v28 offset:14336\n" \
"ds_read_b128 a[220:223], v29 offset:14336\n" \
"s_waitcnt lgkmcnt(15)\n" \
"v_mfma_f32_16x16x32_bf16 a[0:3], a[160:163], a[128:131], a[0:3]\n" \
"s_add_u32 m0, s30, 0x9000\n" \
"s_addk_i32 s84, 0x3000\n" \
"v_mfma_f32_16x16x32_bf16 a[0:3], a[224:227], a[192:195], a[0:3]\n" \
"buffer_load_dwordx4 v24, s[76:79], s84 offen offset:0 lds\n" \
"v_mfma_f32_16x16x32_bf16 a[4:7], a[164:167], a[128:131], a[4:7]\n" \
"ds_read_b64_tr_b16 a[176:177], v36 offset:32768\n" \
"v_writelane_b32 v11, s6, 41\n" \
"v_mfma_f32_16x16x32_bf16 a[4:7], a[228:231], a[192:195], a[4:7]\n" \
"ds_read_b64_tr_b16 a[178:179], v36 offset:33792\n" \
"s_add_i32 s8, s8, 0x42c0000\n" \
"v_mfma_f32_16x16x32_bf16 a[12:15], a[172:175], a[128:131], a[12:15]\n" \
"ds_read_b64_tr_b16 a[240:241], v36 offset:40960\n" \
"s_lshl_b32 s6, s12, 10\n" \
"s_waitcnt lgkmcnt(15)\n" \
"v_mfma_f32_16x16x32_bf16 a[12:15], a[236:239], a[192:195], a[12:15]\n" \
"s_add_u32 s89, s42, 0x3000\n" \
"buffer_store_dwordx4 v[208:211], v20, s[80:83], s89 offen offset:3072 sc1\n" \
"s_waitcnt lgkmcnt(13)\n" \
"v_mfma_f32_16x16x32_bf16 a[8:11], a[128:131], a[168:171], a[8:11]\n" \
"ds_read_b64_tr_b16 a[242:243], v36 offset:41984\n" \
"s_or_b32 s6, s8, s6\n" \
"s_waitcnt lgkmcnt(12)\n" \
"v_mfma_f32_16x16x32_bf16 a[8:11], a[192:195], a[232:235], a[8:11]\n" \
"ds_read_b64_tr_b16 a[180:181], v37 offset:32768\n" \
"v_mfma_f32_16x16x32_bf16 a[24:27], a[132:135], a[168:171], a[24:27]\n" \
"ds_read_b64_tr_b16 a[182:183], v37 offset:33792\n" \
"v_mfma_f32_16x16x32_bf16 a[24:27], a[196:199], a[232:235], a[24:27]\n" \
"ds_read_b64_tr_b16 a[244:245], v37 offset:40960\n" \
"v_mfma_f32_16x16x32_bf16 a[28:31], a[172:175], a[132:135], a[28:31]\n" \
"s_cmp_lg_u32 s16, 0\n" \
"s_cselect_b32 s6, s7, s6\n" \
"v_mfma_f32_16x16x32_bf16 a[28:31], a[236:239], a[196:199], a[28:31]\n" \
"s_addk_i32 s84, 0x2000\n" \
"v_mfma_f32_16x16x32_bf16 a[20:23], a[164:167], a[132:135], a[20:23]\n" \
"buffer_load_dwordx4 v25, s[76:79], s84 offen offset:1024 lds\n" \
"v_mfma_f32_16x16x32_bf16 a[20:23], a[228:231], a[196:199], a[20:23]\n" \
"ds_read_b64_tr_b16 a[246:247], v37 offset:41984\n" \
"s_mulk_i32 s4, 0x4800\n" \
"v_mfma_f32_16x16x32_bf16 a[16:19], a[160:163], a[132:135], a[16:19]\n" \
"ds_read_b64_tr_b16 a[184:185], v38 offset:32768\n" \
"v_writelane_b32 v11, s6, 42\n" \
"v_mfma_f32_16x16x32_bf16 a[16:19], a[224:227], a[196:199], a[16:19]\n" \
"ds_read_b64_tr_b16 a[186:187], v38 offset:33792\n" \
"s_add_i32 s6, s14, s10\n" \
"v_mfma_f32_16x16x32_bf16 a[32:35], a[160:163], a[136:139], a[32:35]\n" \
"s_addk_i32 s84, 0x2000\n" \
"v_mfma_f32_16x16x32_bf16 a[32:35], a[224:227], a[200:203], a[32:35]\n" \
"buffer_load_dwordx4 v24, s[76:79], s84 offen offset:2048 lds\n" \
"v_mfma_f32_16x16x32_bf16 a[36:39], a[164:167], a[136:139], a[36:39]\n" \
"ds_read_b64_tr_b16 a[248:249], v38 offset:40960\n" \
"v_writelane_b32 v11, s6, 43\n" \
"v_mfma_f32_16x16x32_bf16 a[36:39], a[228:231], a[200:203], a[36:39]\n" \
"ds_read_b64_tr_b16 a[250:251], v38 offset:41984\n" \
"s_add_i32 s6, s9, 0x21c000\n" \
"v_mfma_f32_16x16x32_bf16 a[44:47], a[172:175], a[136:139], a[44:47]\n" \
"ds_read_b64_tr_b16 a[188:189], v39 offset:32768\n" \
"s_add_i32 s9, s6, s10\n" \
"v_mfma_f32_16x16x32_bf16 a[44:47], a[236:239], a[200:203], a[44:47]\n" \
"ds_read_b64_tr_b16 a[190:191], v39 offset:33792\n" \
"v_mfma_f32_16x16x32_bf16 a[40:43], a[136:139], a[168:171], a[40:43]\n" \
"s_add_u32 s88, s42, 0x3000\n" \
"buffer_store_dwordx4 v[212:215], v20, s[80:83], s88 offen offset:2048 sc1\n" \
"v_mfma_f32_16x16x32_bf16 a[40:43], a[200:203], a[232:235], a[40:43]\n" \
"ds_read_b64_tr_b16 a[252:253], v39 offset:40960\n" \
"v_mfma_f32_16x16x32_bf16 a[56:59], a[140:143], a[168:171], a[56:59]\n" \
"ds_read_b64_tr_b16 a[254:255], v39 offset:41984\n" \
"v_mfma_f32_16x16x32_bf16 a[56:59], a[204:207], a[232:235], a[56:59]\n" \
"s_cmp_lg_u32 s12, 0\n" \
"s_cselect_b32 s6, s6, s14\n" \
"v_mfma_f32_16x16x32_bf16 a[60:63], a[172:175], a[140:143], a[60:63]\n" \
"s_addk_i32 s6, 0x100\n" \
"v_writelane_b32 v11, s9, 44\n" \
"v_mfma_f32_16x16x32_bf16 a[60:63], a[236:239], a[204:207], a[60:63]\n" \
"s_addk_i32 s84, 0x2000\n" \
"v_mfma_f32_16x16x32_bf16 a[52:55], a[164:167], a[140:143], a[52:55]\n" \
"buffer_load_dwordx4 v25, s[76:79], s84 offen offset:3072 lds\n" \
"v_mfma_f32_16x16x32_bf16 a[52:55], a[228:231], a[204:207], a[52:55]\n" \
"s_cmp_lg_u32 s16, 0\n" \
"s_cselect_b32 s6, s7, s6\n" \
"v_mfma_f32_16x16x32_bf16 a[48:51], a[160:163], a[140:143], a[48:51]\n" \
"v_writelane_b32 v11, s6, 45\n" \
"s_or_b32 s6, s8, s15\n" \
"v_mfma_f32_16x16x32_bf16 a[48:51], a[224:227], a[204:207], a[48:51]\n" \
"v_writelane_b32 v11, s6, 53\n" \
"s_lshl_b32 s6, s3, 17\n" \
"s_waitcnt lgkmcnt(15)\n" \
"v_mfma_f32_16x16x32_bf16 a[64:67], a[164:167], v[248:251], a[64:67]\n" \
"s_add_i32 s6, s13, s6\n" \
"s_or_b32 s5, s6, s5\n" \
"v_mfma_f32_16x16x32_bf16 a[64:67], a[228:231], v[48:51], a[64:67]\n" \
"s_or_b32 s5, s5, 0x3840\n" \
"v_writelane_b32 v11, s5, 6\n" \
"v_mfma_f32_16x16x32_bf16 a[80:83], a[172:175], v[248:251], a[80:83]\n" \
"s_mov_b32 s5, 0xff0000\n" \
"v_mfma_f32_16x16x32_bf16 a[80:83], a[236:239], v[48:51], a[80:83]\n" \
"s_cmp_lg_u32 s16, 0\n" \
"s_cselect_b32 s5, s17, s5\n" \
"v_mfma_f32_16x16x32_bf16 a[68:71], v[248:251], a[168:171], a[68:71]\n" \
"v_writelane_b32 v11, s5, 7\n" \
"s_mul_i32 s5, s3, 0x1200\n" \
"v_mfma_f32_16x16x32_bf16 a[68:71], v[48:51], a[232:235], a[68:71]\n" \
"s_waitcnt vmcnt(10) lgkmcnt(0)\n" \
"s_barrier\n" \
"v_xor_b32_e32 v28, 0x20000, v28\n" \
"v_xor_b32_e32 v29, 0x20000, v29\n" \
"v_mfma_f32_16x16x32_bf16 v[52:55], a[176:179], a[128:131], v[52:55]\n" \
"s_add_u32 m0, s40, 0x8000\n" \
"s_add_u32 s85, s39, 0xe000\n" \
"v_mfma_f32_16x16x32_bf16 v[52:55], a[240:243], a[192:195], v[52:55]\n" \
"buffer_load_dwordx4 v20, s[80:83], s85 offen sc1 lds\n" \
"v_mfma_f32_16x16x32_bf16 v[56:59], a[180:183], a[128:131], v[56:59]\n" \
"ds_read_b64_tr_b16 a[160:161], v32 offset:0\n" \
"s_add_i32 s4, s5, s4\n" \
"v_mfma_f32_16x16x32_bf16 v[56:59], a[244:247], a[192:195], v[56:59]\n" \
"ds_read_b64_tr_b16 a[162:163], v32 offset:512\n" \
"s_add_i32 s4, s4, s11\n" \
"v_mfma_f32_16x16x32_bf16 v[60:63], a[184:187], a[128:131], v[60:63]\n" \
"s_add_u32 m0, s40, 0x9000\n" \
"s_add_u32 s85, s39, 0xf000\n" \
"v_mfma_f32_16x16x32_bf16 v[60:63], a[248:251], a[192:195], v[60:63]\n" \
"buffer_load_dwordx4 v20, s[80:83], s85 offen sc1 lds\n" \
"v_mfma_f32_16x16x32_bf16 v[64:67], a[188:191], a[128:131], v[64:67]\n" \
"ds_read_b64_tr_b16 a[224:225], v32 offset:4096\n" \
"v_writelane_b32 v11, s4, 8\n" \
"v_mfma_f32_16x16x32_bf16 v[64:67], a[252:255], a[192:195], v[64:67]\n" \
"ds_read_b128 a[128:131], v28 offset:0\n" \
"s_lshl_b32 s4, s2, 16\n" \
"v_mfma_f32_16x16x32_bf16 v[80:83], a[188:191], a[132:135], v[80:83]\n" \
"ds_read_b128 a[192:195], v29 offset:0\n" \
"s_add_i32 s5, s4, 0x10000\n" \
"v_mfma_f32_16x16x32_bf16 v[80:83], a[252:255], a[196:199], v[80:83]\n" \
"s_add_u32 m0, s40, 0xb000\n" \
"s_movk_i32 s84, 0x380\n" \
"v_mfma_f32_16x16x32_bf16 v[76:79], a[184:187], a[132:135], v[76:79]\n" \
"buffer_load_dwordx4 v21, s[76:79], s84 offen offset:0 lds\n" \
"v_mfma_f32_16x16x32_bf16 v[76:79], a[248:251], a[196:199], v[76:79]\n" \
"ds_read_b64_tr_b16 a[226:227], v32 offset:4608\n" \
"s_and_b32 s5, s5, 0x70000\n" \
"v_mfma_f32_16x16x32_bf16 v[72:75], a[180:183], a[132:135], v[72:75]\n" \
"ds_read_b64_tr_b16 a[164:165], v33 offset:0\n" \
"s_add_i32 s5, s13, s5\n" \
"v_mfma_f32_16x16x32_bf16 v[72:75], a[244:247], a[196:199], v[72:75]\n" \
"v_writelane_b32 v11, s5, 46\n" \
"s_add_i32 s5, s4, 0x20000\n" \
"v_mfma_f32_16x16x32_bf16 v[68:71], a[176:179], a[132:135], v[68:71]\n" \
"ds_read_b64_tr_b16 a[166:167], v33 offset:512\n" \
"s_and_b32 s5, s5, 0x70000\n" \
"v_mfma_f32_16x16x32_bf16 v[68:71], a[240:243], a[196:199], v[68:71]\n" \
"ds_read_b128 a[132:135], v28 offset:2048\n" \
"s_add_i32 s5, s13, s5\n" \
"v_mfma_f32_16x16x32_bf16 v[84:87], a[176:179], a[136:139], v[84:87]\n" \
"ds_read_b128 a[196:199], v29 offset:2048\n" \
"s_bitset1_b32 s5, 11\n" \
"v_mfma_f32_16x16x32_bf16 v[84:87], a[240:243], a[200:203], v[84:87]\n" \
"s_add_u32 m0, s31, 0x8000\n" \
"s_mov_b32 s84, 0x380000\n" \
"v_mfma_f32_16x16x32_bf16 v[88:91], a[180:183], a[136:139], v[88:91]\n" \
"buffer_load_dwordx4 v26, s[68:71], s84 offen offset:0 lds\n" \
"v_mfma_f32_16x16x32_bf16 v[88:91], a[244:247], a[200:203], v[88:91]\n" \
"s_add_u32 s89, s42, 0x0\n" \
"buffer_store_dwordx4 v[184:187], v20, s[80:83], s89 offen offset:1024 sc1\n" \
"v_mfma_f32_16x16x32_bf16 v[92:95], a[184:187], a[136:139], v[92:95]\n" \
"ds_read_b64_tr_b16 a[228:229], v33 offset:4096\n" \
"v_writelane_b32 v11, s5, 47\n" \
"v_mfma_f32_16x16x32_bf16 v[92:95], a[248:251], a[200:203], v[92:95]\n" \
"ds_read_b64_tr_b16 a[230:231], v33 offset:4608\n" \
"s_add_i32 s5, s4, 0x30000\n" \
"v_mfma_f32_16x16x32_bf16 v[96:99], a[188:191], a[136:139], v[96:99]\n" \
"s_and_b32 s5, s5, 0x70000\n" \
"s_add_i32 s5, s13, s5\n" \
"v_mfma_f32_16x16x32_bf16 v[96:99], a[252:255], a[200:203], v[96:99]\n" \
"ds_read_b128 a[136:139], v28 offset:4096\n" \
"s_bitset1_b32 s5, 12\n" \
"v_mfma_f32_16x16x32_bf16 v[112:115], a[188:191], a[140:143], v[112:115]\n" \
"ds_read_b128 a[200:203], v29 offset:4096\n" \
"v_writelane_b32 v11, s5, 48\n" \
"v_mfma_f32_16x16x32_bf16 v[112:115], a[252:255], a[204:207], v[112:115]\n" \
"s_addk_i32 s84, 0x7c00\n" \
"v_mfma_f32_16x16x32_bf16 v[108:111], a[184:187], a[140:143], v[108:111]\n" \
"buffer_load_dwordx4 v26, s[68:71], s84 offen offset:1024 lds\n" \
"v_mfma_f32_16x16x32_bf16 v[108:111], a[248:251], a[204:207], v[108:111]\n" \
"ds_read_b64_tr_b16 a[172:173], v35 offset:0\n" \
"s_and_b32 s5, s4, 0x70000\n" \
"v_mfma_f32_16x16x32_bf16 v[104:107], a[180:183], a[140:143], v[104:107]\n" \
"s_addk_i32 s84, 0x7c00\n" \
"v_mfma_f32_16x16x32_bf16 v[104:107], a[244:247], a[204:207], v[104:107]\n" \
"buffer_load_dwordx4 v27, s[68:71], s84 offen offset:2048 lds\n" \
"v_mfma_f32_16x16x32_bf16 v[100:103], a[176:179], a[140:143], v[100:103]\n" \
"ds_read_b64_tr_b16 a[174:175], v35 offset:512\n" \
"s_xor_b32 s5, s5, 0x40000\n" \
"v_mfma_f32_16x16x32_bf16 v[100:103], a[240:243], a[204:207], v[100:103]\n" \
"ds_read_b128 a[140:143], v28 offset:6144\n" \
"s_add_i32 s5, s13, s5\n" \
"v_mfma_f32_16x16x32_bf16 v[116:119], a[176:179], a[144:147], v[116:119]\n" \
"ds_read_b128 a[204:207], v29 offset:6144\n" \
"s_or_b32 s5, s5, 0x1800\n" \
"v_mfma_f32_16x16x32_bf16 v[116:119], a[240:243], a[208:211], v[116:119]\n" \
"s_addk_i32 s84, 0x7c00\n" \
"v_mfma_f32_16x16x32_bf16 v[120:123], a[180:183], a[144:147], v[120:123]\n" \
"buffer_load_dwordx4 v27, s[68:71], s84 offen offset:3072 lds\n" \
"v_mfma_f32_16x16x32_bf16 v[120:123], a[244:247], a[208:211], v[120:123]\n" \
"ds_read_b64_tr_b16 a[236:237], v35 offset:4096\n" \
"v_writelane_b32 v11, s5, 49\n" \
"v_mfma_f32_16x16x32_bf16 v[124:127], a[184:187], a[144:147], v[124:127]\n" \
"ds_read_b64_tr_b16 a[238:239], v35 offset:4608\n" \
"s_add_i32 s5, s4, 0x50000\n" \
"v_mfma_f32_16x16x32_bf16 v[124:127], a[248:251], a[208:211], v[124:127]\n" \
"s_add_u32 m0, s30, 0x20000\n" \
"s_movk_i32 s84, 0x400\n" \
"v_mfma_f32_16x16x32_bf16 v[128:131], a[188:191], a[144:147], v[128:131]\n" \
"buffer_load_dwordx4 v24, s[76:79], s84 offen offset:0 lds\n" \
"v_mfma_f32_16x16x32_bf16 v[128:131], a[252:255], a[208:211], v[128:131]\n" \
"ds_read_b128 a[144:147], v28 offset:8192\n" \
"s_and_b32 s5, s5, 0x70000\n" \
"v_mfma_f32_16x16x32_bf16 v[144:147], a[188:191], a[148:151], v[144:147]\n" \
"ds_read_b128 a[208:211], v29 offset:8192\n" \
"s_add_i32 s5, s13, s5\n" \
"v_mfma_f32_16x16x32_bf16 v[144:147], a[252:255], a[212:215], v[144:147]\n" \
"ds_read_b64_tr_b16 a[168:169], v34 offset:0\n" \
"s_bitset1_b32 s5, 13\n" \
"v_mfma_f32_16x16x32_bf16 v[140:143], a[184:187], a[148:151], v[140:143]\n" \
"s_movk_i32 s84, 0x2400\n" \
"s_nop 0\n" \
"v_mfma_f32_16x16x32_bf16 v[140:143], a[248:251], a[212:215], v[140:143]\n" \
"buffer_load_dwordx4 v25, s[76:79], s84 offen offset:1024 lds\n" \
"v_mfma_f32_16x16x32_bf16 v[136:139], a[180:183], a[148:151], v[136:139]\n" \
"ds_read_b64_tr_b16 a[170:171], v34 offset:512\n" \
"v_writelane_b32 v11, s5, 50\n" \
"v_mfma_f32_16x16x32_bf16 v[136:139], a[244:247], a[212:215], v[136:139]\n" \
"ds_read_b64_tr_b16 a[232:233], v34 offset:4096\n" \
"s_add_i32 s5, s4, 0x60000\n" \
"v_mfma_f32_16x16x32_bf16 v[132:135], a[176:179], a[148:151], v[132:135]\n" \
"ds_read_b64_tr_b16 a[234:235], v34 offset:4608\n" \
"s_and_b32 s5, s5, 0x70000\n" \
"v_mfma_f32_16x16x32_bf16 v[132:135], a[240:243], a[212:215], v[132:135]\n" \
"ds_read_b128 a[148:151], v28 offset:10240\n" \
"s_add_i32 s4, s4, 0x70000\n" \
"v_mfma_f32_16x16x32_bf16 v[148:151], a[176:179], a[152:155], v[148:151]\n" \
"ds_read_b128 a[212:215], v29 offset:10240\n" \
"s_add_i32 s5, s13, s5\n" \
"v_mfma_f32_16x16x32_bf16 v[148:151], a[240:243], a[216:219], v[148:151]\n" \
"s_movk_i32 s84, 0x4400\n" \
"v_mfma_f32_16x16x32_bf16 v[152:155], a[180:183], a[152:155], v[152:155]\n" \
"buffer_load_dwordx4 v24, s[76:79], s84 offen offset:2048 lds\n" \
"v_mfma_f32_16x16x32_bf16 v[152:155], a[244:247], a[216:219], v[152:155]\n" \
"s_movk_i32 s84, 0x6400\n" \
"s_nop 0\n" \
"v_mfma_f32_16x16x32_bf16 v[156:159], a[184:187], a[152:155], v[156:159]\n" \
"buffer_load_dwordx4 v25, s[76:79], s84 offen offset:3072 lds\n" \
"v_mfma_f32_16x16x32_bf16 v[156:159], a[248:251], a[216:219], v[156:159]\n" \
"ds_read_b128 v[248:251], v22 offset:0\n" \
"s_and_b32 s4, s4, 0x70000\n" \
"v_mfma_f32_16x16x32_bf16 v[160:163], a[188:191], a[152:155], v[160:163]\n" \
"ds_read_b128 v[48:51], v23 offset:0\n" \
"s_or_b32 s5, s5, 0x2800\n" \
"v_mfma_f32_16x16x32_bf16 v[160:163], a[252:255], a[216:219], v[160:163]\n" \
"ds_read_b128 a[152:155], v28 offset:12288\n" \
"v_writelane_b32 v11, s5, 51\n" \
"v_mfma_f32_16x16x32_bf16 v[176:179], a[188:191], a[156:159], v[176:179]\n" \
"ds_read_b128 a[216:219], v29 offset:12288\n" \
"s_add_i32 s13, s13, s4\n" \
"v_mfma_f32_16x16x32_bf16 v[176:179], a[252:255], a[220:223], v[176:179]\n" \
"s_or_b32 s4, s13, 0x3000\n" \
"v_writelane_b32 v11, s4, 52\n" \
"v_mfma_f32_16x16x32_bf16 v[172:175], a[184:187], a[156:159], v[172:175]\n" \
"s_movk_i32 s11, 0x4000\n" \
"s_lshl_b32 s4, s2, 2\n" \
"v_mfma_f32_16x16x32_bf16 v[172:175], a[248:251], a[220:223], v[172:175]\n" \
"s_and_b32 s4, s4, 28\n" \
"s_bfe_u32 s5, s2, 0x20003\n" \
"v_mfma_f32_16x16x32_bf16 v[168:171], a[180:183], a[156:159], v[168:171]\n" \
"s_or_b32 s4, s4, s5\n" \
"s_lshr_b32 s5, s2, 5\n" \
"v_mfma_f32_16x16x32_bf16 v[168:171], a[244:247], a[220:223], v[168:171]\n" \
"s_lshr_b32 s19, s3, 1\n" \
"s_lshl_b32 s18, s5, 1\n" \
"v_mfma_f32_16x16x32_bf16 v[164:167], a[176:179], a[156:159], v[164:167]\n" \
"s_lshl_b32 s6, s4, 20\n" \
"s_lshl_b32 s7, s4, 8\n" \
"v_mfma_f32_16x16x32_bf16 v[164:167], a[240:243], a[220:223], v[164:167]\n" \
"ds_read_b128 a[156:159], v28 offset:14336\n" \
"ds_read_b128 a[220:223], v29 offset:14336\n" \
"s_waitcnt lgkmcnt(15)\n" \
"v_mfma_f32_16x16x32_bf16 a[0:3], a[160:163], a[128:131], a[0:3]\n" \
"s_add_u32 m0, s30, 0x21000\n" \
"s_addk_i32 s84, 0x3000\n" \
"v_mfma_f32_16x16x32_bf16 a[0:3], a[224:227], a[192:195], a[0:3]\n" \
"buffer_load_dwordx4 v24, s[76:79], s84 offen offset:0 lds\n" \
"v_mfma_f32_16x16x32_bf16 a[4:7], a[164:167], a[128:131], a[4:7]\n" \
"ds_read_b64_tr_b16 a[176:177], v36 offset:0\n" \
"s_and_b32 s13, s3, 1\n" \
"v_mfma_f32_16x16x32_bf16 a[4:7], a[228:231], a[192:195], a[4:7]\n" \
"ds_read_b64_tr_b16 a[178:179], v36 offset:1024\n" \
"s_add_i32 s14, s18, s19\n" \
"v_mfma_f32_16x16x32_bf16 a[12:15], a[172:175], a[128:131], a[12:15]\n" \
"ds_read_b64_tr_b16 a[240:241], v36 offset:8192\n" \
"s_waitcnt lgkmcnt(15)\n" \
"v_mfma_f32_16x16x32_bf16 a[12:15], a[236:239], a[192:195], a[12:15]\n" \
"s_add_u32 s88, s42, 0x1000\n" \
"buffer_store_dwordx4 v[188:191], v20, s[80:83], s88 offen offset:1024 sc1\n" \
"s_waitcnt lgkmcnt(13)\n" \
"v_mfma_f32_16x16x32_bf16 a[8:11], a[128:131], a[168:171], a[8:11]\n" \
"ds_read_b64_tr_b16 a[242:243], v36 offset:9216\n" \
"s_waitcnt lgkmcnt(12)\n" \
"v_mfma_f32_16x16x32_bf16 a[8:11], a[192:195], a[232:235], a[8:11]\n" \
"ds_read_b64_tr_b16 a[180:181], v37 offset:0\n" \
"v_mfma_f32_16x16x32_bf16 a[24:27], a[132:135], a[168:171], a[24:27]\n" \
"ds_read_b64_tr_b16 a[182:183], v37 offset:1024\n" \
"v_mfma_f32_16x16x32_bf16 a[24:27], a[196:199], a[232:235], a[24:27]\n" \
"ds_read_b64_tr_b16 a[244:245], v37 offset:8192\n" \
"v_mfma_f32_16x16x32_bf16 a[28:31], a[172:175], a[132:135], a[28:31]\n" \
"s_add_u32 s6, s26, s6\n" \
"s_addc_u32 s9, s27, 0\n" \
"v_mfma_f32_16x16x32_bf16 a[28:31], a[236:239], a[196:199], a[28:31]\n" \
"s_addk_i32 s84, 0x2000\n" \
"v_mfma_f32_16x16x32_bf16 a[20:23], a[164:167], a[132:135], a[20:23]\n" \
"buffer_load_dwordx4 v25, s[76:79], s84 offen offset:1024 lds\n" \
"v_mfma_f32_16x16x32_bf16 a[20:23], a[228:231], a[196:199], a[20:23]\n" \
"ds_read_b64_tr_b16 a[246:247], v37 offset:9216\n" \
"v_mfma_f32_16x16x32_bf16 a[16:19], a[160:163], a[132:135], a[16:19]\n" \
"ds_read_b64_tr_b16 a[184:185], v38 offset:0\n" \
"v_mfma_f32_16x16x32_bf16 a[16:19], a[224:227], a[196:199], a[16:19]\n" \
"ds_read_b64_tr_b16 a[186:187], v38 offset:1024\n" \
"v_mfma_f32_16x16x32_bf16 a[32:35], a[160:163], a[136:139], a[32:35]\n" \
"s_addk_i32 s84, 0x2000\n" \
"s_nop 0\n" \
"v_mfma_f32_16x16x32_bf16 a[32:35], a[224:227], a[200:203], a[32:35]\n" \
"buffer_load_dwordx4 v24, s[76:79], s84 offen offset:2048 lds\n" \
"v_mfma_f32_16x16x32_bf16 a[36:39], a[164:167], a[136:139], a[36:39]\n" \
"ds_read_b64_tr_b16 a[248:249], v38 offset:8192\n" \
"v_mfma_f32_16x16x32_bf16 a[36:39], a[228:231], a[200:203], a[36:39]\n" \
"ds_read_b64_tr_b16 a[250:251], v38 offset:9216\n" \
"v_mfma_f32_16x16x32_bf16 a[44:47], a[172:175], a[136:139], a[44:47]\n" \
"ds_read_b64_tr_b16 a[188:189], v39 offset:0\n" \
"v_mfma_f32_16x16x32_bf16 a[44:47], a[236:239], a[200:203], a[44:47]\n" \
"ds_read_b64_tr_b16 a[190:191], v39 offset:1024\n" \
"v_mfma_f32_16x16x32_bf16 a[40:43], a[136:139], a[168:171], a[40:43]\n" \
"s_add_u32 s89, s42, 0x2000\n" \
"buffer_store_dwordx4 v[192:195], v20, s[80:83], s89 offen offset:0 sc1\n" \
"v_mfma_f32_16x16x32_bf16 a[40:43], a[200:203], a[232:235], a[40:43]\n" \
"ds_read_b64_tr_b16 a[252:253], v39 offset:8192\n" \
"v_mfma_f32_16x16x32_bf16 a[56:59], a[140:143], a[168:171], a[56:59]\n" \
"ds_read_b64_tr_b16 a[254:255], v39 offset:9216\n" \
"v_mfma_f32_16x16x32_bf16 a[56:59], a[204:207], a[232:235], a[56:59]\n" \
"s_add_u32 s8, s6, 0x1000000\n" \
"s_addc_u32 s6, s9, 0\n" \
"v_mfma_f32_16x16x32_bf16 a[60:63], a[172:175], a[140:143], a[60:63]\n" \
"s_and_b32 s9, s6, 0xffff\n" \
"s_lshl_b32 s6, s5, 16\n" \
"v_mfma_f32_16x16x32_bf16 a[60:63], a[236:239], a[204:207], a[60:63]\n" \
"s_addk_i32 s84, 0x2000\n" \
"v_mfma_f32_16x16x32_bf16 a[52:55], a[164:167], a[140:143], a[52:55]\n" \
"buffer_load_dwordx4 v25, s[76:79], s84 offen offset:3072 lds\n" \
"v_mfma_f32_16x16x32_bf16 a[52:55], a[228:231], a[204:207], a[52:55]\n" \
"s_sub_i32 s10, 0x100000, s6\n" \
"s_mov_b32 s6, 0x8000\n" \
"v_mfma_f32_16x16x32_bf16 a[48:51], a[160:163], a[140:143], a[48:51]\n" \
"s_cmp_lg_u32 s5, 0\n" \
"s_cselect_b32 s17, s11, s6\n" \
".fill (L_pb + 32480 - .) / 4, 4, 0xBF800000\n" \
"L_pp8:\n" \
"s_cbranch_execz L_pret\n" \
"v_mfma_f32_16x16x32_bf16 a[48:51], a[224:227], a[204:207], a[48:51]\n" \
"s_lshl_b32 s6, s3, 1\n" \
"s_lshl_b32 s5, s5, 3\n" \
"s_waitcnt lgkmcnt(15)\n" \
"v_mfma_f32_16x16x32_bf16 a[64:67], a[164:167], v[248:251], a[64:67]\n" \
"s_and_b32 s6, s6, 4\n" \
"s_or_b32 s12, s5, s6\n" \
"v_mfma_f32_16x16x32_bf16 a[64:67], a[228:231], v[48:51], a[64:67]\n" \
"s_sub_i32 s5, s13, s5\n" \
"s_or_b32 s5, s5, s6\n" \
"v_mfma_f32_16x16x32_bf16 a[80:83], a[172:175], v[248:251], a[80:83]\n" \
"s_or_b32 s6, s12, s13\n" \
"s_lshl_b32 s4, s4, 2\n" \
"v_mfma_f32_16x16x32_bf16 a[80:83], a[236:239], v[48:51], a[80:83]\n" \
"s_mulk_i32 s6, 0x92\n" \
"s_add_i32 s4, s4, 18\n" \
"v_mfma_f32_16x16x32_bf16 a[68:71], v[248:251], a[168:171], a[68:71]\n" \
"s_mulk_i32 s5, 0x92\n" \
"s_add_i32 s6, s4, s6\n" \
"v_mfma_f32_16x16x32_bf16 a[68:71], v[48:51], a[232:235], a[68:71]\n" \
"s_waitcnt vmcnt(10) lgkmcnt(0)\n" \
"s_barrier\n" \
"v_mfma_f32_16x16x32_bf16 v[52:55], a[176:179], a[128:131], v[52:55]\n" \
"s_add_u32 m0, s40, 0x0\n" \
"s_add_u32 s85, s39, 0x10000\n" \
"v_mfma_f32_16x16x32_bf16 v[52:55], a[240:243], a[192:195], v[52:55]\n" \
"buffer_load_dwordx4 v20, s[80:83], s85 offen sc1 lds\n" \
"v_mfma_f32_16x16x32_bf16 v[56:59], a[180:183], a[128:131], v[56:59]\n" \
"ds_read_b64_tr_b16 a[160:161], v32 offset:32768\n" \
"s_add_i32 s5, s5, s4\n" \
"v_mfma_f32_16x16x32_bf16 v[56:59], a[244:247], a[192:195], v[56:59]\n" \
"ds_read_b64_tr_b16 a[162:163], v32 offset:33280\n" \
"s_lshl_b32 s4, s3, 11\n" \
"v_mfma_f32_16x16x32_bf16 v[60:63], a[184:187], a[128:131], v[60:63]\n" \
"s_lshl_b32 s15, s13, 12\n" \
"s_lshl_b32 s16, s19, 14\n" \
"v_mfma_f32_16x16x32_bf16 v[60:63], a[248:251], a[192:195], v[60:63]\n" \
"ds_read_b64_tr_b16 a[224:225], v32 offset:36864\n" \
"s_mul_i32 s19, s19, 0xe000\n" \
"v_mfma_f32_16x16x32_bf16 v[64:67], a[188:191], a[128:131], v[64:67]\n" \
"s_mov_b32 s11, 0x20000\n" \
"s_or_b32 s12, s4, 0xc000\n" \
"v_mfma_f32_16x16x32_bf16 v[64:67], a[252:255], a[192:195], v[64:67]\n" \
"ds_read_b128 a[128:131], v28 offset:32768\n" \
"s_lshl_b32 s4, s3, 8\n" \
"v_mfma_f32_16x16x32_bf16 v[80:83], a[188:191], a[132:135], v[80:83]\n" \
"ds_read_b128 a[192:195], v29 offset:32768\n" \
"s_or_b32 s22, s15, s16\n" \
"v_mfma_f32_16x16x32_bf16 v[80:83], a[252:255], a[196:199], v[80:83]\n" \
"s_add_u32 m0, s40, 0x1000\n" \
"s_add_u32 s85, s39, 0x11000\n" \
"v_mfma_f32_16x16x32_bf16 v[76:79], a[184:187], a[132:135], v[76:79]\n" \
"buffer_load_dwordx4 v20, s[80:83], s85 offen sc1 lds\n" \
"v_mfma_f32_16x16x32_bf16 v[76:79], a[248:251], a[196:199], v[76:79]\n" \
"ds_read_b64_tr_b16 a[226:227], v32 offset:37376\n" \
"s_or_b32 s23, s19, s15\n" \
"v_mfma_f32_16x16x32_bf16 v[72:75], a[180:183], a[132:135], v[72:75]\n" \
"ds_read_b64_tr_b16 a[164:165], v33 offset:32768\n" \
"s_lshl_b32 s60, s6, 10\n" \
"v_mfma_f32_16x16x32_bf16 v[72:75], a[244:247], a[196:199], v[72:75]\n" \
"s_lshl_b32 s61, s5, 10\n" \
"s_lshl_b32 s14, s14, 15\n" \
"s_nop 0\n" \
"v_mfma_f32_16x16x32_bf16 v[68:71], a[176:179], a[132:135], v[68:71]\n" \
"ds_read_b64_tr_b16 a[166:167], v33 offset:33280\n" \
"s_or_b32 s20, s4, 0x26c00\n" \
"v_mfma_f32_16x16x32_bf16 v[68:71], a[240:243], a[196:199], v[68:71]\n" \
"ds_read_b128 a[132:135], v28 offset:34816\n" \
"s_or_b32 s13, s22, 0x8000\n" \
"v_mfma_f32_16x16x32_bf16 v[84:87], a[176:179], a[136:139], v[84:87]\n" \
"ds_read_b128 a[196:199], v29 offset:34816\n" \
"s_add_i32 s22, s22, 0x14000\n" \
"v_mfma_f32_16x16x32_bf16 v[84:87], a[240:243], a[200:203], v[84:87]\n" \
"s_add_u32 m0, s40, 0x3000\n" \
"s_movk_i32 s84, 0x400\n" \
"v_mfma_f32_16x16x32_bf16 v[88:91], a[180:183], a[136:139], v[88:91]\n" \
"buffer_load_dwordx4 v21, s[76:79], s84 offen offset:0 lds\n" \
"v_mfma_f32_16x16x32_bf16 v[88:91], a[244:247], a[200:203], v[88:91]\n" \
"ds_read_b64_tr_b16 a[228:229], v33 offset:36864\n" \
"s_bitset1_b32 s23, 16\n" \
"v_mfma_f32_16x16x32_bf16 v[92:95], a[184:187], a[136:139], v[92:95]\n" \
"ds_read_b64_tr_b16 a[230:231], v33 offset:37376\n" \
"s_or_b32 s24, s7, 0x5be8020\n" \
"v_mfma_f32_16x16x32_bf16 v[92:95], a[248:251], a[200:203], v[92:95]\n" \
"s_add_u32 s88, s42, 0x2000\n" \
"buffer_store_dwordx4 v[216:219], v20, s[80:83], s88 offen offset:1024 sc1\n" \
"v_mfma_f32_16x16x32_bf16 v[96:99], a[188:191], a[136:139], v[96:99]\n" \
"s_and_b32 s57, s27, 0xffff\n" \
"s_mov_b32 s58, 0x6000000\n" \
"v_mfma_f32_16x16x32_bf16 v[96:99], a[252:255], a[200:203], v[96:99]\n" \
"ds_read_b128 a[136:139], v28 offset:36864\n" \
"s_mov_b32 s56, s26\n" \
"v_mfma_f32_16x16x32_bf16 v[112:115], a[188:191], a[140:143], v[112:115]\n" \
"ds_read_b128 a[200:203], v29 offset:36864\n" \
"s_mov_b32 s59, s11\n" \
"v_mfma_f32_16x16x32_bf16 v[112:115], a[252:255], a[204:207], v[112:115]\n" \
"ds_read_b64_tr_b16 a[172:173], v35 offset:32768\n" \
"s_add_i32 s60, s60, 0x3000000\n" \
"v_mfma_f32_16x16x32_bf16 v[108:111], a[184:187], a[140:143], v[108:111]\n" \
"ds_read_b64_tr_b16 a[174:175], v35 offset:33280\n" \
"s_add_i32 s61, s61, 0x411c000\n" \
"v_mfma_f32_16x16x32_bf16 v[108:111], a[248:251], a[204:207], v[108:111]\n" \
"s_sub_i32 s19, 14, s18\n" \
"s_mov_b32 s21, 0x71800000\n" \
"v_mfma_f32_16x16x32_bf16 v[104:107], a[180:183], a[140:143], v[104:107]\n" \
"s_add_u32 m0, s31, 0x0\n" \
"s_mov_b32 s84, 0x400000\n" \
"v_mfma_f32_16x16x32_bf16 v[104:107], a[244:247], a[204:207], v[104:107]\n" \
"buffer_load_dwordx4 v26, s[68:71], s84 offen offset:0 lds\n" \
"v_mfma_f32_16x16x32_bf16 v[100:103], a[176:179], a[140:143], v[100:103]\n" \
"ds_read_b64_tr_b16 a[236:237], v35 offset:36864\n" \
"s_mov_b32 s4, L_gsw-L_gtr\n" \
"v_mfma_f32_16x16x32_bf16 v[100:103], a[240:243], a[204:207], v[100:103]\n" \
"ds_read_b128 a[140:143], v28 offset:38912\n" \
"v_mfma_f32_16x16x32_bf16 v[116:119], a[176:179], a[144:147], v[116:119]\n" \
"ds_read_b128 a[204:207], v29 offset:38912\n" \
"v_mfma_f32_16x16x32_bf16 v[116:119], a[240:243], a[208:211], v[116:119]\n" \
"ds_read_b64_tr_b16 a[238:239], v35 offset:37376\n" \
"v_mfma_f32_16x16x32_bf16 v[120:123], a[180:183], a[144:147], v[120:123]\n" \
"ds_read_b64_tr_b16 a[168:169], v34 offset:32768\n" \
"v_mfma_f32_16x16x32_bf16 v[120:123], a[244:247], a[208:211], v[120:123]\n" \
"s_addk_i32 s84, 0x7c00\n" \
"v_mfma_f32_16x16x32_bf16 v[124:127], a[184:187], a[144:147], v[124:127]\n" \
"buffer_load_dwordx4 v26, s[68:71], s84 offen offset:1024 lds\n" \
"v_mfma_f32_16x16x32_bf16 v[124:127], a[248:251], a[208:211], v[124:127]\n" \
"ds_read_b64_tr_b16 a[170:171], v34 offset:33280\n" \
"v_mfma_f32_16x16x32_bf16 v[128:131], a[188:191], a[144:147], v[128:131]\n" \
"s_cmp_eq_u32 s19, 0\n" \
"s_cselect_b32 s4, L_gs7-L_gtr, s4\n" \
"v_mfma_f32_16x16x32_bf16 v[128:131], a[252:255], a[208:211], v[128:131]\n" \
"ds_read_b128 a[144:147], v28 offset:40960\n" \
"v_mfma_f32_16x16x32_bf16 v[144:147], a[188:191], a[148:151], v[144:147]\n" \
"ds_read_b128 a[208:211], v29 offset:40960\n" \
"v_mfma_f32_16x16x32_bf16 v[144:147], a[252:255], a[212:215], v[144:147]\n" \
"ds_read_b64_tr_b16 a[232:233], v34 offset:36864\n" \
"v_mfma_f32_16x16x32_bf16 v[140:143], a[184:187], a[148:151], v[140:143]\n" \
"s_addk_i32 s84, 0x7c00\n" \
"v_mfma_f32_16x16x32_bf16 v[140:143], a[248:251], a[212:215], v[140:143]\n" \
"buffer_load_dwordx4 v27, s[68:71], s84 offen offset:2048 lds\n" \
"v_mfma_f32_16x16x32_bf16 v[136:139], a[180:183], a[148:151], v[136:139]\n" \
"ds_read_b64_tr_b16 a[234:235], v34 offset:37376\n" \
"v_mfma_f32_16x16x32_bf16 v[136:139], a[244:247], a[212:215], v[136:139]\n" \
"s_cmp_eq_u32 s16, 0\n" \
"s_cselect_b32 s4, 0, s4\n" \
"v_mfma_f32_16x16x32_bf16 v[132:135], a[176:179], a[148:151], v[132:135]\n" \
"ds_read_b128 v[248:251], v22 offset:32768\n" \
"v_mfma_f32_16x16x32_bf16 v[132:135], a[240:243], a[212:215], v[132:135]\n" \
"ds_read_b128 a[148:151], v28 offset:43008\n" \
"v_mfma_f32_16x16x32_bf16 v[148:151], a[176:179], a[152:155], v[148:151]\n" \
"ds_read_b128 a[212:215], v29 offset:43008\n" \
"v_mfma_f32_16x16x32_bf16 v[148:151], a[240:243], a[216:219], v[148:151]\n" \
"s_addk_i32 s84, 0x7c00\n" \
"v_mfma_f32_16x16x32_bf16 v[152:155], a[180:183], a[152:155], v[152:155]\n" \
"buffer_load_dwordx4 v27, s[68:71], s84 offen offset:3072 lds\n" \
"v_mfma_f32_16x16x32_bf16 v[152:155], a[244:247], a[216:219], v[152:155]\n" \
"ds_read_b128 v[48:51], v23 offset:32768\n" \
"v_mfma_f32_16x16x32_bf16 v[156:159], a[184:187], a[152:155], v[156:159]\n" \
"s_getpc_b64 s[28:29]\n" \
"L_gp0_%=:\n" \
"s_add_u32 s28, s28, L_gtr-L_gp0_%=\n" \
"s_addc_u32 s29, s29, 0\n" \
"v_mfma_f32_16x16x32_bf16 v[156:159], a[248:251], a[216:219], v[156:159]\n" \
"s_add_u32 s28, s28, s4\n" \
"s_addc_u32 s29, s29, 0\n" \
"v_mfma_f32_16x16x32_bf16 v[160:163], a[188:191], a[152:155], v[160:163]\n" \
"v_mfma_f32_16x16x32_bf16 v[160:163], a[252:255], a[216:219], v[160:163]\n" \
"ds_read_b128 a[152:155], v28 offset:45056\n" \
"v_mfma_f32_16x16x32_bf16 v[176:179], a[188:191], a[156:159], v[176:179]\n" \
"ds_read_b128 a[216:219], v29 offset:45056\n" \
"v_mfma_f32_16x16x32_bf16 v[176:179], a[252:255], a[220:223], v[176:179]\n" \
"v_mfma_f32_16x16x32_bf16 v[172:175], a[184:187], a[156:159], v[172:175]\n" \
"v_mfma_f32_16x16x32_bf16 v[172:175], a[248:251], a[220:223], v[172:175]\n" \
"v_mfma_f32_16x16x32_bf16 v[168:171], a[180:183], a[156:159], v[168:171]\n" \
"v_mfma_f32_16x16x32_bf16 v[168:171], a[244:247], a[220:223], v[168:171]\n" \
"v_mfma_f32_16x16x32_bf16 v[164:167], a[176:179], a[156:159], v[164:167]\n" \
"v_mfma_f32_16x16x32_bf16 v[164:167], a[240:243], a[220:223], v[164:167]\n" \
"ds_read_b128 a[156:159], v28 offset:47104\n" \
"ds_read_b128 a[220:223], v29 offset:47104\n" \
"s_waitcnt lgkmcnt(15)\n" \
"s_nop 0\n" \
"v_mfma_f32_16x16x32_bf16 a[0:3], a[160:163], a[128:131], a[0:3]\n" \
"s_add_u32 s89, s42, 0x3000\n" \
"buffer_store_dwordx4 v[220:223], v20, s[80:83], s89 offen offset:0 sc1\n" \
"v_mfma_f32_16x16x32_bf16 a[0:3], a[224:227], a[192:195], a[0:3]\n" \
"ds_read_b64_tr_b16 a[176:177], v36 offset:32768\n" \
"v_mfma_f32_16x16x32_bf16 a[4:7], a[164:167], a[128:131], a[4:7]\n" \
"ds_read_b64_tr_b16 a[178:179], v36 offset:33792\n" \
"v_mfma_f32_16x16x32_bf16 a[4:7], a[228:231], a[192:195], a[4:7]\n" \
"ds_read_b64_tr_b16 a[240:241], v36 offset:40960\n" \
"v_mfma_f32_16x16x32_bf16 a[12:15], a[172:175], a[128:131], a[12:15]\n" \
"ds_read_b64_tr_b16 a[242:243], v36 offset:41984\n" \
"s_waitcnt lgkmcnt(15)\n" \
"v_mfma_f32_16x16x32_bf16 a[12:15], a[236:239], a[192:195], a[12:15]\n" \
"v_mfma_f32_16x16x32_bf16 a[8:11], a[128:131], a[168:171], a[8:11]\n" \
"ds_read_b64_tr_b16 a[180:181], v37 offset:32768\n" \
"s_waitcnt lgkmcnt(13)\n" \
"v_mfma_f32_16x16x32_bf16 a[8:11], a[192:195], a[232:235], a[8:11]\n" \
"v_mfma_f32_16x16x32_bf16 a[24:27], a[132:135], a[168:171], a[24:27]\n" \
"ds_read_b64_tr_b16 a[182:183], v37 offset:33792\n" \
"v_mfma_f32_16x16x32_bf16 a[24:27], a[196:199], a[232:235], a[24:27]\n" \
"ds_read_b64_tr_b16 a[244:245], v37 offset:40960\n" \
"v_mfma_f32_16x16x32_bf16 a[28:31], a[172:175], a[132:135], a[28:31]\n" \
"v_mfma_f32_16x16x32_bf16 a[28:31], a[236:239], a[196:199], a[28:31]\n" \
"ds_read_b64_tr_b16 a[246:247], v37 offset:41984\n" \
"v_mfma_f32_16x16x32_bf16 a[20:23], a[164:167], a[132:135], a[20:23]\n" \
"v_mfma_f32_16x16x32_bf16 a[20:23], a[228:231], a[196:199], a[20:23]\n" \
"ds_read_b64_tr_b16 a[184:185], v38 offset:32768\n" \
"v_mfma_f32_16x16x32_bf16 a[16:19], a[160:163], a[132:135], a[16:19]\n" \
"ds_read_b64_tr_b16 a[186:187], v38 offset:33792\n" \
"v_mfma_f32_16x16x32_bf16 a[16:19], a[224:227], a[196:199], a[16:19]\n" \
"v_mfma_f32_16x16x32_bf16 a[32:35], a[160:163], a[136:139], a[32:35]\n" \
"s_add_u32 s88, s42, 0x3000\n" \
"buffer_store_dwordx4 v[224:227], v20, s[80:83], s88 offen offset:1024 sc1\n" \
"v_mfma_f32_16x16x32_bf16 a[32:35], a[224:227], a[200:203], a[32:35]\n" \
"ds_read_b64_tr_b16 a[248:249], v38 offset:40960\n" \
"v_mfma_f32_16x16x32_bf16 a[36:39], a[164:167], a[136:139], a[36:39]\n" \
"v_lshlrev_b32_e32 v45, 3, v20\n" \
"v_add_u32_e32 v45, s30, v45\n" \
"v_mfma_f32_16x16x32_bf16 a[36:39], a[228:231], a[200:203], a[36:39]\n" \
"s_getpc_b64 s[86:87]\n" \
"L_ct0_%=:\n" \
"s_add_u32 s86, s86, L_ap0-L_ct0_%=\n" \
"s_addc_u32 s87, s87, 0\n" \
"s_add_u32 m0, s40, 0x2000\n" \
"v_mfma_f32_16x16x32_bf16 a[44:47], a[172:175], a[136:139], a[44:47]\n" \
"global_load_lds_dword v45, s[86:87]\n" \
"v_mfma_f32_16x16x32_bf16 a[44:47], a[236:239], a[200:203], a[44:47]\n" \
"ds_read_b64_tr_b16 a[250:251], v38 offset:41984\n" \
"v_mfma_f32_16x16x32_bf16 a[40:43], a[136:139], a[168:171], a[40:43]\n" \
"ds_read_b64_tr_b16 a[188:189], v39 offset:32768\n" \
"v_mfma_f32_16x16x32_bf16 a[40:43], a[200:203], a[232:235], a[40:43]\n" \
"ds_read_b64_tr_b16 a[190:191], v39 offset:33792\n" \
"v_mfma_f32_16x16x32_bf16 a[56:59], a[140:143], a[168:171], a[56:59]\n" \
"ds_read_b64_tr_b16 a[252:253], v39 offset:40960\n" \
"v_mfma_f32_16x16x32_bf16 a[56:59], a[204:207], a[232:235], a[56:59]\n" \
"ds_read_b64_tr_b16 a[254:255], v39 offset:41984\n" \
"v_mfma_f32_16x16x32_bf16 a[60:63], a[172:175], a[140:143], a[60:63]\n" \
"v_mfma_f32_16x16x32_bf16 a[60:63], a[236:239], a[204:207], a[60:63]\n" \
"v_mfma_f32_16x16x32_bf16 a[52:55], a[164:167], a[140:143], a[52:55]\n" \
"v_mfma_f32_16x16x32_bf16 a[52:55], a[228:231], a[204:207], a[52:55]\n" \
"v_mfma_f32_16x16x32_bf16 a[48:51], a[160:163], a[140:143], a[48:51]\n" \
"v_mfma_f32_16x16x32_bf16 a[48:51], a[224:227], a[204:207], a[48:51]\n" \
"s_waitcnt lgkmcnt(15)\n" \
"v_mfma_f32_16x16x32_bf16 a[64:67], a[164:167], v[248:251], a[64:67]\n" \
"v_mfma_f32_16x16x32_bf16 a[64:67], a[228:231], v[48:51], a[64:67]\n" \
"v_mfma_f32_16x16x32_bf16 a[80:83], a[172:175], v[248:251], a[80:83]\n" \
"v_mfma_f32_16x16x32_bf16 a[80:83], a[236:239], v[48:51], a[80:83]\n" \
"v_mfma_f32_16x16x32_bf16 a[68:71], v[248:251], a[168:171], a[68:71]\n" \
"v_mfma_f32_16x16x32_bf16 a[68:71], v[48:51], a[232:235], a[68:71]\n" \
"s_waitcnt vmcnt(3) lgkmcnt(0)\n" \
"s_barrier\n" \
"s_nop 0\n" \
"v_xor_b32_e32 v28, 0x20000, v28\n" \
"v_xor_b32_e32 v29, 0x20000, v29\n" \
"v_mfma_f32_16x16x32_bf16 v[52:55], a[176:179], a[128:131], v[52:55]\n" \
"ds_read_b64_tr_b16 a[160:161], v32 offset:0\n" \
"v_accvgpr_read_b32 v184, a0\n" \
"v_mfma_f32_16x16x32_bf16 v[52:55], a[240:243], a[192:195], v[52:55]\n" \
"v_accvgpr_read_b32 v185, a1\n" \
"v_accvgpr_read_b32 v186, a2\n" \
"v_mfma_f32_16x16x32_bf16 v[56:59], a[180:183], a[128:131], v[56:59]\n" \
"ds_read_b64_tr_b16 a[162:163], v32 offset:512\n" \
"v_accvgpr_read_b32 v187, a3\n" \
"v_mfma_f32_16x16x32_bf16 v[56:59], a[244:247], a[192:195], v[56:59]\n" \
"v_accvgpr_read_b32 v188, a4\n" \
"v_accvgpr_read_b32 v189, a5\n" \
"v_mfma_f32_16x16x32_bf16 v[60:63], a[184:187], a[128:131], v[60:63]\n" \
"v_accvgpr_read_b32 v190, a6\n" \
"v_accvgpr_read_b32 v191, a7\n" \
"v_mfma_f32_16x16x32_bf16 v[60:63], a[248:251], a[192:195], v[60:63]\n" \
"ds_read_b64_tr_b16 a[224:225], v32 offset:4096\n" \
"v_accvgpr_read_b32 v196, a12\n" \
"v_mfma_f32_16x16x32_bf16 v[64:67], a[188:191], a[128:131], v[64:67]\n" \
"v_accvgpr_read_b32 v197, a13\n" \
"v_accvgpr_read_b32 v198, a14\n" \
"v_mfma_f32_16x16x32_bf16 v[64:67], a[252:255], a[192:195], v[64:67]\n" \
"ds_read_b128 a[128:131], v28 offset:0\n" \
"v_accvgpr_read_b32 v199, a15\n" \
"v_mfma_f32_16x16x32_bf16 v[80:83], a[188:191], a[132:135], v[80:83]\n" \
"ds_read_b128 a[192:195], v29 offset:0\n" \
"v_accvgpr_read_b32 v192, a8\n" \
"v_mfma_f32_16x16x32_bf16 v[80:83], a[252:255], a[196:199], v[80:83]\n" \
"ds_read_b64_tr_b16 a[226:227], v32 offset:4608\n" \
"v_accvgpr_read_b32 v193, a9\n" \
"v_mfma_f32_16x16x32_bf16 v[76:79], a[184:187], a[132:135], v[76:79]\n" \
"v_accvgpr_read_b32 v194, a10\n" \
"v_accvgpr_read_b32 v195, a11\n" \
"v_mfma_f32_16x16x32_bf16 v[76:79], a[248:251], a[196:199], v[76:79]\n" \
"ds_read_b64_tr_b16 a[164:165], v33 offset:0\n" \
"v_accvgpr_read_b32 v208, a24\n" \
"v_mfma_f32_16x16x32_bf16 v[72:75], a[180:183], a[132:135], v[72:75]\n" \
"v_accvgpr_read_b32 v209, a25\n" \
"v_accvgpr_read_b32 v210, a26\n" \
"v_mfma_f32_16x16x32_bf16 v[72:75], a[244:247], a[196:199], v[72:75]\n" \
"v_accvgpr_read_b32 v211, a27\n" \
"v_accvgpr_read_b32 v212, a28\n" \
"v_mfma_f32_16x16x32_bf16 v[68:71], a[176:179], a[132:135], v[68:71]\n" \
"ds_read_b64_tr_b16 a[166:167], v33 offset:512\n" \
"v_accvgpr_read_b32 v213, a29\n" \
"v_mfma_f32_16x16x32_bf16 v[68:71], a[240:243], a[196:199], v[68:71]\n" \
"ds_read_b128 a[132:135], v28 offset:2048\n" \
"v_accvgpr_read_b32 v214, a30\n" \
"v_mfma_f32_16x16x32_bf16 v[84:87], a[176:179], a[136:139], v[84:87]\n" \
"ds_read_b128 a[196:199], v29 offset:2048\n" \
"v_accvgpr_read_b32 v215, a31\n" \
"v_mfma_f32_16x16x32_bf16 v[84:87], a[240:243], a[200:203], v[84:87]\n" \
"ds_read_b64_tr_b16 a[228:229], v33 offset:4096\n" \
"v_accvgpr_read_b32 v204, a20\n" \
"v_mfma_f32_16x16x32_bf16 v[88:91], a[180:183], a[136:139], v[88:91]\n" \
"v_accvgpr_read_b32 v205, a21\n" \
"v_accvgpr_read_b32 v206, a22\n" \
"v_mfma_f32_16x16x32_bf16 v[88:91], a[244:247], a[200:203], v[88:91]\n" \
"v_accvgpr_read_b32 v207, a23\n" \
"v_accvgpr_read_b32 v200, a16\n" \
"v_mfma_f32_16x16x32_bf16 v[92:95], a[184:187], a[136:139], v[92:95]\n" \
"ds_read_b64_tr_b16 a[230:231], v33 offset:4608\n" \
"v_accvgpr_read_b32 v201, a17\n" \
"v_mfma_f32_16x16x32_bf16 v[92:95], a[248:251], a[200:203], v[92:95]\n" \
"v_accvgpr_read_b32 v202, a18\n" \
"v_accvgpr_read_b32 v203, a19\n" \
"v_mfma_f32_16x16x32_bf16 v[96:99], a[188:191], a[136:139], v[96:99]\n" \
"v_accvgpr_read_b32 v216, a32\n" \
"v_accvgpr_read_b32 v217, a33\n" \
"v_mfma_f32_16x16x32_bf16 v[96:99], a[252:255], a[200:203], v[96:99]\n" \
"ds_read_b128 a[136:139], v28 offset:4096\n" \
"v_accvgpr_read_b32 v218, a34\n" \
"v_mfma_f32_16x16x32_bf16 v[112:115], a[188:191], a[140:143], v[112:115]\n" \
"ds_read_b128 a[200:203], v29 offset:4096\n" \
"v_accvgpr_read_b32 v219, a35\n" \
"v_mfma_f32_16x16x32_bf16 v[112:115], a[252:255], a[204:207], v[112:115]\n" \
"ds_read_b64_tr_b16 a[172:173], v35 offset:0\n" \
"v_accvgpr_read_b32 v220, a36\n" \
"v_mfma_f32_16x16x32_bf16 v[108:111], a[184:187], a[140:143], v[108:111]\n" \
"ds_read_b64_tr_b16 a[174:175], v35 offset:512\n" \
"v_accvgpr_read_b32 v221, a37\n" \
"v_mfma_f32_16x16x32_bf16 v[108:111], a[248:251], a[204:207], v[108:111]\n" \
"v_accvgpr_read_b32 v222, a38\n" \
"v_accvgpr_read_b32 v223, a39\n" \
"v_mfma_f32_16x16x32_bf16 v[104:107], a[180:183], a[140:143], v[104:107]\n" \
"ds_read_b64_tr_b16 a[236:237], v35 offset:4096\n" \
"v_accvgpr_read_b32 v228, a44\n" \
"v_mfma_f32_16x16x32_bf16 v[104:107], a[244:247], a[204:207], v[104:107]\n" \
"v_accvgpr_read_b32 v229, a45\n" \
"v_accvgpr_read_b32 v230, a46\n" \
"v_mfma_f32_16x16x32_bf16 v[100:103], a[176:179], a[140:143], v[100:103]\n" \
"v_accvgpr_read_b32 v231, a47\n" \
"v_accvgpr_read_b32 v224, a40\n" \
"v_mfma_f32_16x16x32_bf16 v[100:103], a[240:243], a[204:207], v[100:103]\n" \
"ds_read_b128 a[140:143], v28 offset:6144\n" \
"v_accvgpr_read_b32 v225, a41\n" \
"v_mfma_f32_16x16x32_bf16 v[116:119], a[176:179], a[144:147], v[116:119]\n" \
"ds_read_b128 a[204:207], v29 offset:6144\n" \
"v_accvgpr_read_b32 v226, a42\n" \
"v_mfma_f32_16x16x32_bf16 v[116:119], a[240:243], a[208:211], v[116:119]\n" \
"ds_read_b64_tr_b16 a[238:239], v35 offset:4608\n" \
"v_accvgpr_read_b32 v227, a43\n" \
"v_mfma_f32_16x16x32_bf16 v[120:123], a[180:183], a[144:147], v[120:123]\n" \
"ds_read_b64_tr_b16 a[168:169], v34 offset:0\n" \
"v_accvgpr_read_b32 v240, a56\n" \
"v_mfma_f32_16x16x32_bf16 v[120:123], a[244:247], a[208:211], v[120:123]\n" \
"v_accvgpr_read_b32 v241, a57\n" \
"v_accvgpr_read_b32 v242, a58\n" \
"v_mfma_f32_16x16x32_bf16 v[124:127], a[184:187], a[144:147], v[124:127]\n" \
"v_accvgpr_read_b32 v243, a59\n" \
"v_accvgpr_read_b32 v244, a60\n" \
"v_mfma_f32_16x16x32_bf16 v[124:127], a[248:251], a[208:211], v[124:127]\n" \
"ds_read_b64_tr_b16 a[170:171], v34 offset:512\n" \
"v_accvgpr_read_b32 v245, a61\n" \
"v_mfma_f32_16x16x32_bf16 v[128:131], a[188:191], a[144:147], v[128:131]\n" \
"v_accvgpr_read_b32 v246, a62\n" \
"v_accvgpr_read_b32 v247, a63\n" \
"v_mfma_f32_16x16x32_bf16 v[128:131], a[252:255], a[208:211], v[128:131]\n" \
"ds_read_b128 a[144:147], v28 offset:8192\n" \
"v_accvgpr_read_b32 v236, a52\n" \
"v_mfma_f32_16x16x32_bf16 v[144:147], a[188:191], a[148:151], v[144:147]\n" \
"ds_read_b128 a[208:211], v29 offset:8192\n" \
"v_accvgpr_read_b32 v237, a53\n" \
"v_mfma_f32_16x16x32_bf16 v[144:147], a[252:255], a[212:215], v[144:147]\n" \
"ds_read_b64_tr_b16 a[232:233], v34 offset:4096\n" \
"v_accvgpr_read_b32 v238, a54\n" \
"v_mfma_f32_16x16x32_bf16 v[140:143], a[184:187], a[148:151], v[140:143]\n" \
"v_accvgpr_read_b32 v239, a55\n" \
"v_accvgpr_read_b32 v232, a48\n" \
"v_mfma_f32_16x16x32_bf16 v[140:143], a[248:251], a[212:215], v[140:143]\n" \
"ds_read_b64_tr_b16 a[234:235], v34 offset:4608\n" \
"v_accvgpr_read_b32 v233, a49\n" \
"v_mfma_f32_16x16x32_bf16 v[136:139], a[180:183], a[148:151], v[136:139]\n" \
"v_accvgpr_read_b32 v234, a50\n" \
"v_accvgpr_read_b32 v235, a51\n" \
"v_mfma_f32_16x16x32_bf16 v[136:139], a[244:247], a[212:215], v[136:139]\n" \
"v_accvgpr_read_b32 v180, a64\n" \
"v_accvgpr_read_b32 v181, a65\n" \
"v_mfma_f32_16x16x32_bf16 v[132:135], a[176:179], a[148:151], v[132:135]\n" \
"ds_read_b128 v[248:251], v22 offset:0\n" \
"v_accvgpr_read_b32 v182, a66\n" \
"v_mfma_f32_16x16x32_bf16 v[132:135], a[240:243], a[212:215], v[132:135]\n" \
"ds_read_b128 a[148:151], v28 offset:10240\n" \
"v_accvgpr_read_b32 v183, a67\n" \
"v_mfma_f32_16x16x32_bf16 v[148:151], a[176:179], a[152:155], v[148:151]\n" \
"ds_read_b128 a[212:215], v29 offset:10240\n" \
"v_accvgpr_read_b32 v252, a68\n" \
"v_mfma_f32_16x16x32_bf16 v[148:151], a[240:243], a[216:219], v[148:151]\n" \
"ds_read_b128 v[48:51], v23 offset:0\n" \
"v_accvgpr_read_b32 v253, a69\n" \
"v_mfma_f32_16x16x32_bf16 v[152:155], a[180:183], a[152:155], v[152:155]\n" \
"v_accvgpr_read_b32 v254, a70\n" \
"v_accvgpr_read_b32 v255, a71\n" \
"v_mfma_f32_16x16x32_bf16 v[152:155], a[244:247], a[216:219], v[152:155]\n" \
"v_lshrrev_b32_e64 v24, 9, v20\n" \
"v_and_b32_e32 v26, 0x1ff, v20\n" \
"v_mfma_f32_16x16x32_bf16 v[156:159], a[184:187], a[152:155], v[156:159]\n" \
"v_lshl_add_u32 v24, v24, 3, v26\n" \
"v_lshrrev_b32_e64 v25, 8, v20\n" \
"v_mfma_f32_16x16x32_bf16 v[156:159], a[248:251], a[216:219], v[156:159]\n" \
"v_mul_u32_u24_e32 v25, 0x24700, v25\n" \
"v_add_u32_e64 v25, v25, v20\n" \
"v_mfma_f32_16x16x32_bf16 v[160:163], a[188:191], a[152:155], v[160:163]\n" \
"v_lshrrev_b32_e32 v26, 2, v20\n" \
"v_add_u32_e32 v26, s45, v26\n" \
"v_mfma_f32_16x16x32_bf16 v[160:163], a[252:255], a[216:219], v[160:163]\n" \
"ds_read_b128 a[152:155], v28 offset:12288\n" \
"v_add_u32_e64 v27, s46, v20\n" \
"v_mfma_f32_16x16x32_bf16 v[176:179], a[188:191], a[156:159], v[176:179]\n" \
"ds_read_b128 a[216:219], v29 offset:12288\n" \
"v_add_u32_e64 v21, s43, v27\n" \
"v_mfma_f32_16x16x32_bf16 v[176:179], a[252:255], a[220:223], v[176:179]\n" \
"v_mfma_f32_16x16x32_bf16 v[172:175], a[184:187], a[156:159], v[172:175]\n" \
"v_mfma_f32_16x16x32_bf16 v[172:175], a[248:251], a[220:223], v[172:175]\n" \
"v_mfma_f32_16x16x32_bf16 v[168:171], a[180:183], a[156:159], v[168:171]\n" \
"v_mfma_f32_16x16x32_bf16 v[168:171], a[244:247], a[220:223], v[168:171]\n" \
"v_mfma_f32_16x16x32_bf16 v[164:167], a[176:179], a[156:159], v[164:167]\n" \
"v_mfma_f32_16x16x32_bf16 v[164:167], a[240:243], a[220:223], v[164:167]\n" \
"ds_read_b128 a[156:159], v28 offset:14336\n" \
"ds_read_b128 a[220:223], v29 offset:14336\n" \
"s_waitcnt lgkmcnt(15)\n" \
"s_nop 0\n" \
"v_mfma_f32_16x16x32_bf16 v[184:187], a[160:163], a[128:131], v[184:187]\n" \
"v_bfe_u32 v44, v20, 2, 5\n" \
"ds_read_b64_tr_b16 a[176:177], v36 offset:0\n" \
"v_mfma_f32_16x16x32_bf16 v[184:187], a[224:227], a[192:195], v[184:187]\n" \
"buffer_load_dword v44, v44, s[80:83], s38 offen offset:0 sc1\n" \
"v_mfma_f32_16x16x32_bf16 v[188:191], a[164:167], a[128:131], v[188:191]\n" \
"ds_read_b64_tr_b16 a[178:179], v36 offset:1024\n" \
"v_mfma_f32_16x16x32_bf16 v[188:191], a[228:231], a[192:195], v[188:191]\n" \
"ds_read_b64_tr_b16 a[240:241], v36 offset:8192\n" \
"v_mfma_f32_16x16x32_bf16 v[196:199], a[172:175], a[128:131], v[196:199]\n" \
"ds_read_b64_tr_b16 a[242:243], v36 offset:9216\n" \
"s_waitcnt lgkmcnt(15)\n" \
"v_mfma_f32_16x16x32_bf16 v[196:199], a[236:239], a[192:195], v[196:199]\n" \
"v_mfma_f32_16x16x32_bf16 v[192:195], a[128:131], a[168:171], v[192:195]\n" \
"ds_read_b64_tr_b16 a[180:181], v37 offset:0\n" \
"s_waitcnt lgkmcnt(13)\n" \
"v_mfma_f32_16x16x32_bf16 v[192:195], a[192:195], a[232:235], v[192:195]\n" \
"v_mfma_f32_16x16x32_bf16 v[208:211], a[132:135], a[168:171], v[208:211]\n" \
"ds_read_b64_tr_b16 a[182:183], v37 offset:1024\n" \
"v_mfma_f32_16x16x32_bf16 v[208:211], a[196:199], a[232:235], v[208:211]\n" \
"ds_read_b64_tr_b16 a[244:245], v37 offset:8192\n" \
"v_mfma_f32_16x16x32_bf16 v[212:215], a[172:175], a[132:135], v[212:215]\n" \
"v_mfma_f32_16x16x32_bf16 v[212:215], a[236:239], a[196:199], v[212:215]\n" \
"ds_read_b64_tr_b16 a[246:247], v37 offset:9216\n" \
"v_cvt_pk_bf16_f32 v188, v188, v189\n" \
"v_mfma_f32_16x16x32_bf16 v[204:207], a[164:167], a[132:135], v[204:207]\n" \
"v_cvt_pk_bf16_f32 v189, v190, v191\n" \
"v_cvt_pk_bf16_f32 v190, v198, v199\n" \
"v_mfma_f32_16x16x32_bf16 v[204:207], a[228:231], a[196:199], v[204:207]\n" \
"ds_read_b64_tr_b16 a[184:185], v38 offset:0\n" \
"v_mov_b32_e32 v191, 0x3f80\n" \
"v_mfma_f32_16x16x32_bf16 v[200:203], a[160:163], a[132:135], v[200:203]\n" \
"ds_read_b64_tr_b16 a[186:187], v38 offset:1024\n" \
"v_cvt_pk_bf16_f32 v184, v184, v185\n" \
"v_mfma_f32_16x16x32_bf16 v[200:203], a[224:227], a[196:199], v[200:203]\n" \
"v_cvt_pk_bf16_f32 v185, v186, v187\n" \
"v_cvt_pk_bf16_f32 v186, v196, v197\n" \
"v_mfma_f32_16x16x32_bf16 v[216:219], a[160:163], a[136:139], v[216:219]\n" \
"ds_read_b64_tr_b16 a[248:249], v38 offset:8192\n" \
"v_mov_b32_e64 v187, 0\n" \
"v_mfma_f32_16x16x32_bf16 v[216:219], a[224:227], a[200:203], v[216:219]\n" \
"ds_read_b64_tr_b16 a[250:251], v38 offset:9216\n" \
"v_mfma_f32_16x16x32_bf16 v[220:223], a[164:167], a[136:139], v[220:223]\n" \
"v_mfma_f32_16x16x32_bf16 v[220:223], a[228:231], a[200:203], v[220:223]\n" \
"ds_read_b64_tr_b16 a[188:189], v39 offset:0\n" \
"v_cvt_pk_bf16_f32 v204, v204, v205\n" \
"v_mfma_f32_16x16x32_bf16 v[228:231], a[172:175], a[136:139], v[228:231]\n" \
"v_cvt_pk_bf16_f32 v205, v206, v207\n" \
"v_cvt_pk_bf16_f32 v206, v214, v215\n" \
"v_mfma_f32_16x16x32_bf16 v[228:231], a[236:239], a[200:203], v[228:231]\n" \
"ds_read_b64_tr_b16 a[190:191], v39 offset:1024\n" \
"v_mov_b32_e32 v207, 0x3f80\n" \
"v_mfma_f32_16x16x32_bf16 v[224:227], a[136:139], a[168:171], v[224:227]\n" \
"ds_read_b64_tr_b16 a[252:253], v39 offset:8192\n" \
"v_cvt_pk_bf16_f32 v200, v200, v201\n" \
"v_mfma_f32_16x16x32_bf16 v[224:227], a[200:203], a[232:235], v[224:227]\n" \
"v_cvt_pk_bf16_f32 v201, v202, v203\n" \
"v_cvt_pk_bf16_f32 v202, v212, v213\n" \
"v_mfma_f32_16x16x32_bf16 v[240:243], a[140:143], a[168:171], v[240:243]\n" \
"ds_read_b64_tr_b16 a[254:255], v39 offset:9216\n" \
"v_mov_b32_e64 v203, 0\n" \
"v_mfma_f32_16x16x32_bf16 v[240:243], a[204:207], a[232:235], v[240:243]\n" \
"v_mfma_f32_16x16x32_bf16 v[244:247], a[172:175], a[140:143], v[244:247]\n" \
"v_mfma_f32_16x16x32_bf16 v[244:247], a[236:239], a[204:207], v[244:247]\n" \
"v_cvt_pk_bf16_f32 v220, v220, v221\n" \
"v_cvt_pk_bf16_f32 v221, v222, v223\n" \
"v_mfma_f32_16x16x32_bf16 v[236:239], a[164:167], a[140:143], v[236:239]\n" \
"v_cvt_pk_bf16_f32 v222, v230, v231\n" \
"v_mov_b32_e32 v223, 0x3f80\n" \
"v_mfma_f32_16x16x32_bf16 v[236:239], a[228:231], a[204:207], v[236:239]\n" \
"v_cvt_pk_bf16_f32 v216, v216, v217\n" \
"v_cvt_pk_bf16_f32 v217, v218, v219\n" \
"v_mfma_f32_16x16x32_bf16 v[232:235], a[160:163], a[140:143], v[232:235]\n" \
"v_cvt_pk_bf16_f32 v218, v228, v229\n" \
"v_mov_b32_e64 v219, 0\n" \
"v_mfma_f32_16x16x32_bf16 v[232:235], a[224:227], a[204:207], v[232:235]\n" \
"s_waitcnt lgkmcnt(15)\n" \
"s_nop 0\n" \
"v_mfma_f32_16x16x32_bf16 v[180:183], a[164:167], v[248:251], v[180:183]\n" \
"v_mfma_f32_16x16x32_bf16 v[180:183], a[228:231], v[48:51], v[180:183]\n" \
"v_mfma_f32_16x16x32_bf16 a[80:83], a[172:175], v[248:251], a[80:83]\n" \
"v_mfma_f32_16x16x32_bf16 a[80:83], a[236:239], v[48:51], a[80:83]\n" \
"v_cvt_pk_bf16_f32 v236, v236, v237\n" \
"v_cvt_pk_bf16_f32 v237, v238, v239\n" \
"v_mfma_f32_16x16x32_bf16 v[252:255], v[248:251], a[168:171], v[252:255]\n" \
"v_cvt_pk_bf16_f32 v238, v246, v247\n" \
"v_mov_b32_e32 v239, 0x3f80\n" \
"v_mfma_f32_16x16x32_bf16 v[252:255], v[48:51], a[232:235], v[252:255]\n" \
"v_cvt_pk_bf16_f32 v232, v232, v233\n" \
"v_cvt_pk_bf16_f32 v233, v234, v235\n" \
"s_waitcnt vmcnt(0) lgkmcnt(0)\n" \
"s_barrier\n" \
"v_mov_b32_e32 v45, s62\n" \
"s_mov_b64 exec, 1\n" \
"buffer_store_dword v45, off, s[80:83], s37 offset:32 sc1\n" \
"s_mov_b64 exec, -1\n" \
"v_cmp_ne_u32_e32 vcc, s62, v44\n" \
"s_nop 4\n" \
"s_cbranch_vccnz L_poll160_%=\n" \
"L_pollr160_%=:\n" \
"v_mfma_f32_16x16x32_bf16 v[52:55], a[176:179], a[128:131], v[52:55]\n" \
"s_mov_b32 s73, s50\n" \
"s_sub_u32 m0, s31, 0x10000\n" \
"v_mfma_f32_16x16x32_bf16 v[52:55], a[240:243], a[192:195], v[52:55]\n" \
"buffer_load_dwordx4 v20, s[80:83], s73 offen offset:0 sc1 lds\n" \
"v_mfma_f32_16x16x32_bf16 v[56:59], a[180:183], a[128:131], v[56:59]\n" \
"s_add_u32 s74, s50, 0x4000\n" \
"s_add_u32 s75, s50, 0x8000\n" \
"v_mfma_f32_16x16x32_bf16 v[56:59], a[244:247], a[192:195], v[56:59]\n" \
"s_mov_b32 s70, 0x3e0293ee\n" \
"s_mov_b32 s76, s52\n" \
"v_mfma_f32_16x16x32_bf16 v[60:63], a[184:187], a[128:131], v[60:63]\n" \
"v_mul_f32_e32 v52, s70, v52\n" \
"v_mul_f32_e32 v53, s70, v53\n" \
"v_mfma_f32_16x16x32_bf16 v[60:63], a[248:251], a[192:195], v[60:63]\n" \
"buffer_load_dwordx4 v20, s[80:83], s73 offen offset:1024 sc1 lds\n" \
".fill (L_pb + 36576 - .) / 4, 4, 0xBF800000\n" \
"L_pp9:\n" \
"s_cbranch_execz L_pret\n" \
"v_mfma_f32_16x16x32_bf16 v[64:67], a[188:191], a[128:131], v[64:67]\n" \
"v_mul_f32_e32 v54, s70, v54\n" \
"v_mul_f32_e64 v55, s70, v55\n" \
"v_mfma_f32_16x16x32_bf16 v[64:67], a[252:255], a[192:195], v[64:67]\n" \
"v_cvt_pk_bf16_f32 v52, v52, v53\n" \
"v_mul_f32_e64 v56, s70, v56\n" \
"v_mfma_f32_16x16x32_bf16 v[80:83], a[188:191], a[132:135], v[80:83]\n" \
"v_mul_f32_e32 v60, s70, v60\n" \
"v_mul_f32_e32 v61, s70, v61\n" \
"v_mfma_f32_16x16x32_bf16 v[80:83], a[252:255], a[196:199], v[80:83]\n" \
"buffer_load_dwordx4 v20, s[80:83], s73 offen offset:2048 sc1 lds\n" \
"v_mfma_f32_16x16x32_bf16 v[76:79], a[184:187], a[132:135], v[76:79]\n" \
"v_mul_f32_e32 v62, s70, v62\n" \
"v_mul_f32_e32 v63, s70, v63\n" \
"v_mfma_f32_16x16x32_bf16 v[76:79], a[248:251], a[196:199], v[76:79]\n" \
"v_cvt_pk_bf16_f32 v60, v60, v61\n" \
"v_mul_f32_e64 v57, s70, v57\n" \
"v_mfma_f32_16x16x32_bf16 v[72:75], a[180:183], a[132:135], v[72:75]\n" \
"v_mul_f32_e32 v58, s70, v58\n" \
"v_mul_f32_e32 v59, s70, v59\n" \
"v_mfma_f32_16x16x32_bf16 v[72:75], a[244:247], a[196:199], v[72:75]\n" \
"buffer_load_dwordx4 v20, s[80:83], s73 offen offset:3072 sc1 lds\n" \
"v_mfma_f32_16x16x32_bf16 v[68:71], a[176:179], a[132:135], v[68:71]\n" \
"s_sub_u32 m0, s31, s51\n" \
"v_cvt_pk_bf16_f32 v53, v54, v55\n" \
"v_mfma_f32_16x16x32_bf16 v[68:71], a[240:243], a[196:199], v[68:71]\n" \
"v_mul_f32_e32 v64, s70, v64\n" \
"v_mul_f32_e64 v65, s70, v65\n" \
"v_mfma_f32_16x16x32_bf16 v[84:87], a[176:179], a[136:139], v[84:87]\n" \
"v_mul_f32_e32 v66, s70, v66\n" \
"v_mul_f32_e32 v67, s70, v67\n" \
"v_mfma_f32_16x16x32_bf16 v[84:87], a[240:243], a[200:203], v[84:87]\n" \
"buffer_load_dwordx4 v20, s[80:83], s74 offen offset:0 sc1 lds\n" \
"v_mfma_f32_16x16x32_bf16 v[88:91], a[180:183], a[136:139], v[88:91]\n" \
"v_cvt_pk_bf16_f32 v61, v62, v63\n" \
"v_cvt_pk_bf16_f32 v54, v56, v57\n" \
"v_mfma_f32_16x16x32_bf16 v[88:91], a[244:247], a[200:203], v[88:91]\n" \
"v_cvt_pk_bf16_f32 v55, v58, v59\n" \
"v_cvt_pk_bf16_f32 v62, v64, v65\n" \
"v_mfma_f32_16x16x32_bf16 v[92:95], a[184:187], a[136:139], v[92:95]\n" \
"v_cvt_pk_bf16_f32 v63, v66, v67\n" \
"v_mul_f32_e64 v68, s70, v68\n" \
"v_mfma_f32_16x16x32_bf16 v[92:95], a[248:251], a[200:203], v[92:95]\n" \
"buffer_load_dwordx4 v20, s[80:83], s74 offen offset:1024 sc1 lds\n" \
"v_mfma_f32_16x16x32_bf16 v[96:99], a[188:191], a[136:139], v[96:99]\n" \
"ds_write_b128 v27, v[52:55] offset:0\n" \
"v_mfma_f32_16x16x32_bf16 v[96:99], a[252:255], a[200:203], v[96:99]\n" \
"v_mul_f32_e32 v69, s70, v69\n" \
"v_mul_f32_e32 v76, s70, v76\n" \
"v_mfma_f32_16x16x32_bf16 v[112:115], a[188:191], a[140:143], v[112:115]\n" \
"v_mul_f32_e32 v77, s70, v77\n" \
"v_mul_f32_e32 v70, s70, v70\n" \
"v_mfma_f32_16x16x32_bf16 v[112:115], a[252:255], a[204:207], v[112:115]\n" \
"buffer_load_dwordx4 v20, s[80:83], s74 offen offset:2048 sc1 lds\n" \
"v_mfma_f32_16x16x32_bf16 v[108:111], a[184:187], a[140:143], v[108:111]\n" \
"ds_write_b128 v27, v[60:63] offset:1024\n" \
"v_mfma_f32_16x16x32_bf16 v[108:111], a[248:251], a[204:207], v[108:111]\n" \
"v_mul_f32_e64 v71, s70, v71\n" \
"v_cvt_pk_bf16_f32 v68, v68, v69\n" \
"v_mfma_f32_16x16x32_bf16 v[104:107], a[180:183], a[140:143], v[104:107]\n" \
"v_mul_f32_e32 v78, s70, v78\n" \
"v_mul_f32_e32 v79, s70, v79\n" \
"v_mfma_f32_16x16x32_bf16 v[104:107], a[244:247], a[204:207], v[104:107]\n" \
"buffer_load_dwordx4 v20, s[80:83], s74 offen offset:3072 sc1 lds\n" \
"v_mfma_f32_16x16x32_bf16 v[100:103], a[176:179], a[140:143], v[100:103]\n" \
"s_sub_u32 m0, s31, 0x4000\n" \
"v_cvt_pk_bf16_f32 v76, v76, v77\n" \
"v_mfma_f32_16x16x32_bf16 v[100:103], a[240:243], a[204:207], v[100:103]\n" \
"v_mul_f32_e32 v72, s70, v72\n" \
"v_mul_f32_e32 v73, s70, v73\n" \
"v_mfma_f32_16x16x32_bf16 v[116:119], a[176:179], a[144:147], v[116:119]\n" \
"v_mul_f32_e32 v74, s70, v74\n" \
"v_mul_f32_e32 v75, s70, v75\n" \
"v_mfma_f32_16x16x32_bf16 v[116:119], a[240:243], a[208:211], v[116:119]\n" \
"buffer_load_dwordx4 v20, s[80:83], s75 offen offset:0 sc1 lds\n" \
"v_mfma_f32_16x16x32_bf16 v[120:123], a[180:183], a[144:147], v[120:123]\n" \
"v_cvt_pk_bf16_f32 v69, v70, v71\n" \
"v_mul_f32_e64 v80, s70, v80\n" \
"v_mfma_f32_16x16x32_bf16 v[120:123], a[244:247], a[208:211], v[120:123]\n" \
"v_mul_f32_e32 v81, s70, v81\n" \
"v_mul_f32_e32 v82, s70, v82\n" \
"v_mfma_f32_16x16x32_bf16 v[124:127], a[184:187], a[144:147], v[124:127]\n" \
"v_mul_f32_e64 v83, s70, v83\n" \
"v_cvt_pk_bf16_f32 v77, v78, v79\n" \
"v_mfma_f32_16x16x32_bf16 v[124:127], a[248:251], a[208:211], v[124:127]\n" \
"buffer_load_dwordx4 v20, s[80:83], s75 offen offset:1024 sc1 lds\n" \
"v_mfma_f32_16x16x32_bf16 v[128:131], a[188:191], a[144:147], v[128:131]\n" \
"v_cvt_pk_bf16_f32 v70, v72, v73\n" \
"v_cvt_pk_bf16_f32 v71, v74, v75\n" \
"v_mfma_f32_16x16x32_bf16 v[128:131], a[252:255], a[208:211], v[128:131]\n" \
"v_cvt_pk_bf16_f32 v78, v80, v81\n" \
"v_cvt_pk_bf16_f32 v79, v82, v83\n" \
"v_mfma_f32_16x16x32_bf16 v[144:147], a[188:191], a[148:151], v[144:147]\n" \
"ds_write_b128 v27, v[68:71] offset:4096\n" \
"v_mfma_f32_16x16x32_bf16 v[144:147], a[252:255], a[212:215], v[144:147]\n" \
"buffer_load_dwordx4 v20, s[80:83], s75 offen offset:2048 sc1 lds\n" \
"v_mfma_f32_16x16x32_bf16 v[140:143], a[184:187], a[148:151], v[140:143]\n" \
"v_cvt_pk_bf16_f32 v180, v180, v181\n" \
"v_accvgpr_read_b32 v50, a82\n" \
"v_mfma_f32_16x16x32_bf16 v[140:143], a[248:251], a[212:215], v[140:143]\n" \
"ds_write_b128 v27, v[76:79] offset:5120\n" \
"v_mfma_f32_16x16x32_bf16 v[136:139], a[180:183], a[148:151], v[136:139]\n" \
"v_accvgpr_read_b32 v51, a83\n" \
"v_cvt_pk_bf16_f32 v181, v182, v183\n" \
"v_mfma_f32_16x16x32_bf16 v[136:139], a[244:247], a[212:215], v[136:139]\n" \
"buffer_load_dwordx4 v20, s[80:83], s75 offen offset:3072 sc1 lds\n" \
"v_mfma_f32_16x16x32_bf16 v[132:135], a[176:179], a[148:151], v[132:135]\n" \
"s_mov_b32 m0, s53\n" \
"v_cvt_pk_bf16_f32 v182, v50, v51\n" \
"v_mfma_f32_16x16x32_bf16 v[132:135], a[240:243], a[212:215], v[132:135]\n" \
"v_mov_b32_e32 v183, 0x3f80\n" \
"v_cvt_pk_bf16_f32 v234, v244, v245\n" \
"v_mfma_f32_16x16x32_bf16 v[148:151], a[176:179], a[152:155], v[148:151]\n" \
"v_mov_b32_e32 v235, 0\n" \
"v_cvt_pk_bf16_f32 v54, v192, v193\n" \
"v_mfma_f32_16x16x32_bf16 v[148:151], a[240:243], a[216:219], v[148:151]\n" \
"buffer_load_dwordx4 v20, s[80:83], s76 offen offset:0 sc1 lds\n" \
"v_mfma_f32_16x16x32_bf16 v[152:155], a[180:183], a[152:155], v[152:155]\n" \
"v_cvt_pk_bf16_f32 v55, v194, v195\n" \
"v_cvt_pk_bf16_f32 v56, v208, v209\n" \
"v_mfma_f32_16x16x32_bf16 v[152:155], a[244:247], a[216:219], v[152:155]\n" \
"v_cvt_pk_bf16_f32 v57, v210, v211\n" \
"v_cvt_pk_bf16_f32 v58, v224, v225\n" \
"v_cvt_pk_bf16_f32 v59, v226, v227\n" \
"v_cvt_pk_bf16_f32 v60, v240, v241\n" \
"v_cvt_pk_bf16_f32 v61, v242, v243\n" \
"v_mfma_f32_16x16x32_bf16 v[32:35], v[180:183], v[184:187], 0\n" \
"buffer_load_dwordx4 v20, s[80:83], s76 offen offset:1024 sc1 lds\n" \
"v_mfma_f32_16x16x32_bf16 v[156:159], a[184:187], a[152:155], v[156:159]\n" \
"v_mfma_f32_16x16x32_bf16 v[192:195], v[188:191], v[184:187], 0\n" \
"v_mfma_f32_16x16x32_bf16 v[208:211], v[204:207], v[200:203], 0\n" \
"v_mfma_f32_16x16x32_bf16 v[36:39], v[188:191], v[200:203], 0\n" \
"v_mfma_f32_16x16x32_bf16 v[224:227], v[220:223], v[216:219], 0\n" \
"v_mfma_f32_16x16x32_bf16 v[248:251], v[204:207], v[216:219], 0\n" \
"v_mfma_f32_16x16x32_bf16 v[240:243], v[236:239], v[232:235], 0\n" \
"v_mfma_f32_16x16x32_bf16 v[64:67], v[220:223], v[232:235], 0\n" \
"s_mov_b32 s64, 0xaaaaaaaa\n" \
"s_mov_b32 s65, 0xaaaaaaaa\n" \
"v_mfma_f32_16x16x32_bf16 v[156:159], a[248:251], a[216:219], v[156:159]\n" \
"v_cndmask_b32_e64 v32, v32, v33, s[64:65]\n" \
"v_cndmask_b32_e64 v34, v34, v35, s[64:65]\n" \
"v_mfma_f32_16x16x32_bf16 v[160:163], a[188:191], a[152:155], v[160:163]\n" \
"s_mov_b32 s66, 0xfff0ffff\n" \
"s_mov_b32 s67, 0xf000ff00\n" \
"s_mov_b32 s68, 0xffc0fffc\n" \
"s_mov_b32 s69, 0xc000fc00\n" \
"v_cndmask_b32_e64 v192, v192, v193, s[64:65]\n" \
"v_add_f32_e64 v32, s47, v32\n" \
"v_cndmask_b32_e64 v194, v194, v195, s[64:65]\n" \
"v_add_f32_e64 v34, s47, v34\n" \
"v_cndmask_b32_e64 v208, v208, v209, s[64:65]\n" \
"v_cndmask_b32_e64 v36, v36, v37, s[64:65]\n" \
"v_cndmask_b32_e64 v210, v210, v211, s[64:65]\n" \
"v_cndmask_b32_e64 v38, v38, v39, s[64:65]\n" \
"v_cndmask_b32_e64 v224, v224, v225, s[64:65]\n" \
"v_cndmask_b32_e64 v248, v248, v249, s[64:65]\n" \
"v_cndmask_b32_e64 v226, v226, v227, s[64:65]\n" \
"v_cndmask_b32_e64 v250, v250, v251, s[64:65]\n" \
"v_cndmask_b32_e64 v240, v240, v241, s[64:65]\n" \
"v_cndmask_b32_e64 v64, v64, v65, s[64:65]\n" \
"v_cndmask_b32_e64 v242, v242, v243, s[64:65]\n" \
"v_cndmask_b32_e64 v66, v66, v67, s[64:65]\n" \
"v_cndmask_b32_e64 v192, v32, v192, s[66:67]\n" \
"v_cndmask_b32_e64 v194, v34, v194, s[68:69]\n" \
"v_cndmask_b32_e64 v208, v36, v208, s[66:67]\n" \
"v_cndmask_b32_e64 v210, v38, v210, s[68:69]\n" \
"v_cndmask_b32_e64 v224, v248, v224, s[66:67]\n" \
"v_cndmask_b32_e64 v226, v250, v226, s[68:69]\n" \
"v_cndmask_b32_e64 v240, v64, v240, s[66:67]\n" \
"v_cndmask_b32_e64 v242, v66, v242, s[68:69]\n" \
"v_mfma_f32_16x16x32_bf16 v[160:163], a[252:255], a[216:219], v[160:163]\n" \
"v_max_f32_e32 v193, v192, v194\n" \
"v_max_f32_e32 v209, v208, v210\n" \
"v_max_f32_e32 v225, v224, v226\n" \
"v_max_f32_e32 v241, v240, v242\n" \
"v_mul_f32_e32 v84, s70, v84\n" \
"v_permlane32_swap_b32_e32 v193, v225\n" \
"v_permlane32_swap_b32_e32 v209, v241\n" \
"v_mul_f32_e32 v85, s70, v85\n" \
"v_mul_f32_e32 v92, s70, v92\n" \
"v_mul_f32_e32 v93, s70, v93\n" \
"v_permlane32_swap_b32_e32 v196, v228\n" \
"v_permlane32_swap_b32_e32 v212, v244\n" \
"v_max_f32_e32 v193, v193, v225\n" \
"v_max_f32_e32 v209, v209, v241\n" \
"v_accvgpr_read_b32 v48, a80\n" \
"v_accvgpr_read_b32 v49, a81\n" \
"v_mul_f32_e32 v86, s70, v86\n" \
"v_mul_f32_e32 v87, s70, v87\n" \
"v_cvt_pk_bf16_f32 v84, v84, v85\n" \
"v_mul_f32_e32 v94, s70, v94\n" \
"v_mul_f32_e32 v95, s70, v95\n" \
"v_cvt_pk_bf16_f32 v92, v92, v93\n" \
"v_permlane16_swap_b32_e32 v228, v244\n" \
"v_permlane16_swap_b32_e32 v193, v209\n" \
"v_mfma_f32_16x16x32_bf16 v[176:179], a[188:191], a[156:159], v[176:179]\n" \
"s_mov_b32 s72, 0x3eae1a93\n" \
"v_mul_f32_e32 v88, s70, v88\n" \
"v_mul_f32_e32 v89, s70, v89\n" \
"v_mul_f32_e32 v90, s70, v90\n" \
"v_mul_f32_e32 v91, s70, v91\n" \
"v_cvt_pk_bf16_f32 v85, v86, v87\n" \
"v_mul_f32_e32 v96, s70, v96\n" \
"v_mul_f32_e32 v97, s70, v97\n" \
"v_mul_f32_e32 v98, s70, v98\n" \
"v_mul_f32_e32 v99, s70, v99\n" \
"v_cvt_pk_bf16_f32 v93, v94, v95\n" \
"v_mul_f32_e32 v45, 0xbfb8aa3b, v228\n" \
"v_mfma_f32_16x16x16_bf16 v[48:51], v[182:183], v[46:47], 0\n" \
"v_mfma_f32_16x16x16_bf16 v[196:199], v[190:191], v[46:47], 0\n" \
"v_mfma_f32_16x16x16_bf16 v[212:215], v[206:207], v[46:47], 0\n" \
"v_max_f32_e64 v193, v193, v209\n" \
"v_cvt_pk_bf16_f32 v86, v88, v89\n" \
"v_cvt_pk_bf16_f32 v87, v90, v91\n" \
"v_cvt_pk_bf16_f32 v94, v96, v97\n" \
"v_cvt_pk_bf16_f32 v95, v98, v99\n" \
"v_mfma_f32_16x16x16_bf16 v[228:231], v[222:223], v[46:47], 0\n" \
"v_mul_f32_e32 v193, s72, v193\n" \
"v_mov_b32_e32 v209, v193\n" \
"ds_write_b128 v27, v[84:87] offset:8192\n" \
"v_mfma_f32_16x16x16_bf16 v[244:247], v[238:239], v[46:47], 0\n" \
"v_permlane16_swap_b32_e32 v193, v209\n" \
"s_mov_b32 s71, 0x3e0293ee\n" \
"v_mfma_f32_16x16x32_bf16 v[176:179], a[252:255], a[220:223], v[176:179]\n" \
"v_mov_b32_e32 v225, v193\n" \
"v_mov_b32_e32 v241, v209\n" \
"v_mul_f32_e32 v100, s70, v100\n" \
"v_mul_f32_e64 v101, s70, v101\n" \
"ds_write_b128 v27, v[92:95] offset:9216\n" \
"v_mul_f32_e32 v108, s70, v108\n" \
"v_mul_f32_e32 v109, s70, v109\n" \
"v_pk_mul_f32 v[116:117], v[116:117], s[70:71]\n" \
"v_pk_mul_f32 v[124:125], v[124:125], s[70:71]\n" \
"v_pk_mul_f32 v[132:133], v[132:133], s[70:71]\n" \
"v_pk_mul_f32 v[140:141], v[140:141], s[70:71]\n" \
"v_pk_mul_f32 v[148:149], v[148:149], s[70:71]\n" \
"v_pk_mul_f32 v[156:157], v[156:157], s[70:71]\n" \
"v_permlane32_swap_b32_e32 v193, v225\n" \
"v_permlane32_swap_b32_e32 v209, v241\n" \
"v_mul_f32_e32 v102, s70, v102\n" \
"v_mul_f32_e32 v103, s70, v103\n" \
"v_cvt_pk_bf16_f32 v100, v100, v101\n" \
"v_mul_f32_e32 v110, s70, v110\n" \
"v_mul_f32_e32 v111, s70, v111\n" \
"v_cvt_pk_bf16_f32 v108, v108, v109\n" \
"v_pk_mul_f32 v[118:119], v[118:119], s[70:71]\n" \
"v_cvt_pk_bf16_f32 v116, v116, v117\n" \
"v_pk_mul_f32 v[126:127], v[126:127], s[70:71]\n" \
"v_cvt_pk_bf16_f32 v124, v124, v125\n" \
"v_pk_mul_f32 v[134:135], v[134:135], s[70:71]\n" \
"v_cvt_pk_bf16_f32 v132, v132, v133\n" \
"v_pk_mul_f32 v[142:143], v[142:143], s[70:71]\n" \
"v_cvt_pk_bf16_f32 v140, v140, v141\n" \
"v_pk_mul_f32 v[150:151], v[150:151], s[70:71]\n" \
"v_cvt_pk_bf16_f32 v148, v148, v149\n" \
"v_pk_mul_f32 v[158:159], v[158:159], s[70:71]\n" \
"v_cvt_pk_bf16_f32 v156, v156, v157\n" \
"v_fma_f32 v192, v192, s72, -v193\n" \
"v_fma_f32 v194, v194, s72, -v193\n" \
"v_fma_f32 v208, v208, s72, -v209\n" \
"v_fma_f32 v210, v210, s72, -v209\n" \
"v_mul_f32_e32 v104, s70, v104\n" \
"v_mul_f32_e32 v105, s70, v105\n" \
"v_mul_f32_e32 v106, s70, v106\n" \
"v_mul_f32_e32 v107, s70, v107\n" \
"v_cvt_pk_bf16_f32 v101, v102, v103\n" \
"v_mul_f32_e32 v112, s70, v112\n" \
"v_mul_f32_e32 v113, s70, v113\n" \
"v_mul_f32_e32 v114, s70, v114\n" \
"v_mul_f32_e32 v115, s70, v115\n" \
"v_cvt_pk_bf16_f32 v109, v110, v111\n" \
"v_pk_mul_f32 v[120:121], v[120:121], s[70:71]\n" \
"v_pk_mul_f32 v[122:123], v[122:123], s[70:71]\n" \
"v_cvt_pk_bf16_f32 v117, v118, v119\n" \
"v_pk_mul_f32 v[128:129], v[128:129], s[70:71]\n" \
"v_pk_mul_f32 v[130:131], v[130:131], s[70:71]\n" \
"v_cvt_pk_bf16_f32 v125, v126, v127\n" \
"v_pk_mul_f32 v[136:137], v[136:137], s[70:71]\n" \
"v_pk_mul_f32 v[138:139], v[138:139], s[70:71]\n" \
"v_cvt_pk_bf16_f32 v133, v134, v135\n" \
"v_pk_mul_f32 v[144:145], v[144:145], s[70:71]\n" \
"v_pk_mul_f32 v[146:147], v[146:147], s[70:71]\n" \
"v_cvt_pk_bf16_f32 v141, v142, v143\n" \
"v_pk_mul_f32 v[152:153], v[152:153], s[70:71]\n" \
"v_pk_mul_f32 v[154:155], v[154:155], s[70:71]\n" \
"v_cvt_pk_bf16_f32 v149, v150, v151\n" \
"v_pk_mul_f32 v[160:161], v[160:161], s[70:71]\n" \
"v_pk_mul_f32 v[162:163], v[162:163], s[70:71]\n" \
"v_cvt_pk_bf16_f32 v157, v158, v159\n" \
"v_pk_mul_f32 v[176:177], v[176:177], s[70:71]\n" \
"v_pk_mul_f32 v[178:179], v[178:179], s[70:71]\n" \
"v_exp_f32_e32 v192, v192\n" \
"v_exp_f32_e32 v194, v194\n" \
"v_exp_f32_e32 v208, v208\n" \
"v_exp_f32_e32 v210, v210\n" \
"v_fma_f32 v224, v224, s72, -v225\n" \
"v_fma_f32 v226, v226, s72, -v225\n" \
"v_mfma_f32_16x16x32_bf16 v[172:175], a[184:187], a[156:159], v[172:175]\n" \
"v_cvt_pk_bf16_f32 v102, v104, v105\n" \
"v_cvt_pk_bf16_f32 v103, v106, v107\n" \
"v_cvt_pk_bf16_f32 v110, v112, v113\n" \
"v_cvt_pk_bf16_f32 v111, v114, v115\n" \
"v_cvt_pk_bf16_f32 v118, v120, v121\n" \
"v_cvt_pk_bf16_f32 v119, v122, v123\n" \
"v_cvt_pk_bf16_f32 v126, v128, v129\n" \
"v_cvt_pk_bf16_f32 v127, v130, v131\n" \
"v_cvt_pk_bf16_f32 v134, v136, v137\n" \
"v_cvt_pk_bf16_f32 v135, v138, v139\n" \
"v_cvt_pk_bf16_f32 v142, v144, v145\n" \
"v_cvt_pk_bf16_f32 v143, v146, v147\n" \
"v_cvt_pk_bf16_f32 v150, v152, v153\n" \
"v_cvt_pk_bf16_f32 v151, v154, v155\n" \
"v_cvt_pk_bf16_f32 v158, v160, v161\n" \
"v_cvt_pk_bf16_f32 v159, v162, v163\n" \
"v_exp_f32_e32 v224, v224\n" \
"v_exp_f32_e32 v226, v226\n" \
"v_fma_f32 v240, v240, s72, -v241\n" \
"v_fma_f32 v242, v242, s72, -v241\n" \
"ds_write_b128 v27, v[100:103] offset:12288\n" \
"v_cvt_pk_bf16_f32 v192, v192, v194\n" \
"v_cvt_pk_bf16_f32 v208, v208, v210\n" \
"v_exp_f32_e32 v240, v240\n" \
"v_exp_f32_e32 v242, v242\n" \
"v_cvt_pk_bf16_f32 v68, v48, v49\n" \
"v_cvt_pk_bf16_f32 v69, v50, v51\n" \
"v_cvt_pk_bf16_f32 v70, v196, v197\n" \
"v_cvt_pk_bf16_f32 v71, v198, v199\n" \
"ds_write_b128 v27, v[108:111] offset:13312\n" \
"v_cvt_pk_bf16_f32 v72, v212, v213\n" \
"v_cvt_pk_bf16_f32 v73, v214, v215\n" \
"v_perm_b32 v184, v192, v192, v40\n" \
"v_perm_b32 v185, v192, v192, v41\n" \
"v_perm_b32 v186, v192, v192, v42\n" \
"v_perm_b32 v187, v192, v192, v43\n" \
"v_perm_b32 v200, v208, v208, v40\n" \
"v_perm_b32 v201, v208, v208, v41\n" \
"v_perm_b32 v202, v208, v208, v42\n" \
"v_perm_b32 v203, v208, v208, v43\n" \
"ds_write_b128 v21, v[116:119] offset:16384\n" \
"v_cvt_pk_bf16_f32 v224, v224, v226\n" \
"v_cvt_pk_bf16_f32 v74, v228, v229\n" \
"v_cvt_pk_bf16_f32 v75, v230, v231\n" \
"v_mfma_f32_16x16x32_bf16 v[32:35], v[68:71], v[184:187], 0\n" \
"v_mfma_f32_16x16x32_bf16 v[36:39], v[70:73], v[200:203], 0\n" \
"v_perm_b32 v216, v224, v224, v40\n" \
"v_perm_b32 v217, v224, v224, v41\n" \
"ds_write_b128 v21, v[124:127] offset:17408\n" \
"v_perm_b32 v218, v224, v224, v42\n" \
"v_perm_b32 v219, v224, v224, v43\n" \
"v_cvt_pk_bf16_f32 v240, v240, v242\n" \
"v_mfma_f32_16x16x32_bf16 v[172:175], a[248:251], a[220:223], v[172:175]\n" \
"v_cvt_pk_bf16_f32 v76, v244, v245\n" \
"v_cvt_pk_bf16_f32 v77, v246, v247\n" \
"v_mfma_f32_16x16x32_bf16 v[248:251], v[72:75], v[216:219], 0\n" \
"v_perm_b32 v232, v240, v240, v40\n" \
"v_perm_b32 v233, v240, v240, v41\n" \
"ds_write_b128 v21, v[132:135] offset:20480\n" \
"v_perm_b32 v234, v240, v240, v42\n" \
"v_perm_b32 v235, v240, v240, v43\n" \
"v_pk_mul_f32 v[172:173], v[172:173], s[70:71]\n" \
"v_pk_mul_f32 v[174:175], v[174:175], s[70:71]\n" \
"v_cvt_pk_bf16_f32 v172, v172, v173\n" \
"v_cvt_pk_bf16_f32 v173, v174, v175\n" \
"v_cvt_pk_bf16_f32 v174, v176, v177\n" \
"v_cvt_pk_bf16_f32 v175, v178, v179\n" \
"v_mfma_f32_16x16x32_bf16 v[64:67], v[74:77], v[232:235], 0\n" \
"ds_write_b128 v21, v[140:143] offset:21504\n" \
"v_mfma_f32_16x16x32_bf16 v[168:171], a[180:183], a[156:159], v[168:171]\n" \
"v_cvt_pk_bf16_f32 v52, v252, v253\n" \
"v_cvt_pk_bf16_f32 v53, v254, v255\n" \
"v_mfma_f32_16x16x32_bf16 v[168:171], a[244:247], a[220:223], v[168:171]\n" \
"v_exp_f32_e64 v45, v45\n" \
"v_mfma_f32_16x16x32_bf16 v[192:195], v[52:55], v[184:187], 0\n" \
"ds_write_b128 v21, v[148:151] offset:24576\n" \
"v_mfma_f32_16x16x32_bf16 v[208:211], v[54:57], v[200:203], 0\n" \
"v_mfma_f32_16x16x32_bf16 v[224:227], v[56:59], v[216:219], 0\n" \
"v_mfma_f32_16x16x32_bf16 v[240:243], v[58:61], v[232:235], 0\n" \
"ds_write_b128 v21, v[156:159] offset:25600\n" \
"v_mov_b32_dpp v34, v38 quad_perm:[0,1,2,3] row_mask:0x2 bank_mask:0xf\n" \
"v_pk_mul_f32 v[168:169], v[168:169], s[70:71]\n" \
"v_pk_mul_f32 v[170:171], v[170:171], s[70:71]\n" \
"v_mov_b32_dpp v34, v250 quad_perm:[0,1,2,3] row_mask:0x4 bank_mask:0xf\n" \
"v_add_f32_e64 v45, 1.0, v45\n" \
"v_mfma_f32_16x16x32_bf16 v[164:167], a[176:179], a[156:159], v[164:167]\n" \
"v_mov_b32_dpp v34, v66 quad_perm:[0,1,2,3] row_mask:0x8 bank_mask:0xf\n" \
"v_mov_b32_dpp v32, v36 quad_perm:[0,1,2,3] row_mask:0x2 bank_mask:0xf\n" \
"v_rcp_f32_e64 v45, v45\n" \
"ds_write_b128 v21, v[172:175] offset:29696\n" \
"v_mov_b32_dpp v33, v37 quad_perm:[0,1,2,3] row_mask:0x2 bank_mask:0xf\n" \
"v_rcp_f32_e64 v34, v34\n" \
"v_mov_b32_dpp v32, v248 quad_perm:[0,1,2,3] row_mask:0x4 bank_mask:0xf\n" \
"v_mov_b32_dpp v33, v249 quad_perm:[0,1,2,3] row_mask:0x4 bank_mask:0xf\n" \
"v_mul_f32_e64 v34, v45, v34\n" \
"v_mov_b32_dpp v32, v64 quad_perm:[0,1,2,3] row_mask:0x8 bank_mask:0xf\n" \
"v_mov_b32_dpp v33, v65 quad_perm:[0,1,2,3] row_mask:0x8 bank_mask:0xf\n" \
"v_mul_f32_e32 v32, v34, v32\n" \
"v_mul_f32_e32 v33, v34, v33\n" \
"v_mov_b32_e64 v38, v34\n" \
"v_mfma_f32_16x16x32_bf16 v[164:167], a[240:243], a[220:223], v[164:167]\n" \
"v_sub_f32_e32 v44, 1.0, v45\n" \
"v_permlane16_swap_b32_e32 v34, v38\n" \
"v_mov_b32_e32 v250, v34\n" \
"v_mov_b32_e32 v66, v38\n" \
"v_cvt_pk_bf16_f32 v32, v32, v33\n" \
"v_permlane32_swap_b32_e32 v34, v250\n" \
"v_permlane32_swap_b32_e32 v38, v66\n" \
"v_mul_f32_e64 v192, v34, v192\n" \
"v_pk_mul_f32 v[164:165], v[164:165], s[70:71]\n" \
"v_pk_mul_f32 v[166:167], v[166:167], s[70:71]\n" \
"v_cvt_pk_bf16_f32 v164, v164, v165\n" \
"v_cvt_pk_bf16_f32 v165, v166, v167\n" \
"v_cvt_pk_bf16_f32 v166, v168, v169\n" \
"v_cvt_pk_bf16_f32 v167, v170, v171\n" \
"ds_write_b128 v21, v[164:167] offset:28672\n" \
"v_mul_f32_e32 v193, v34, v193\n" \
"v_mul_f32_e32 v208, v38, v208\n" \
"v_mul_f32_e32 v209, v38, v209\n" \
"v_mul_f32_e32 v224, v250, v224\n" \
"v_mul_f32_e32 v225, v250, v225\n" \
"v_mul_f32_e32 v240, v66, v240\n" \
"v_mul_f32_e32 v241, v66, v241\n" \
"v_mul_f32_e32 v194, v34, v194\n" \
"v_mul_f32_e64 v195, v34, v195\n" \
"v_cvt_pk_bf16_f32 v192, v192, v193\n" \
"v_mul_f32_e32 v210, v38, v210\n" \
"v_mul_f32_e32 v211, v38, v211\n" \
"v_cvt_pk_bf16_f32 v208, v208, v209\n" \
"v_mul_f32_e32 v226, v250, v226\n" \
"v_mul_f32_e32 v227, v250, v227\n" \
"v_cvt_pk_bf16_f32 v224, v224, v225\n" \
"v_mul_f32_e32 v242, v66, v242\n" \
"v_mul_f32_e32 v243, v66, v243\n" \
"v_cvt_pk_bf16_f32 v240, v240, v241\n" \
"v_cvt_pk_bf16_f32 v193, v194, v195\n" \
"s_add_u32 s84, s48, 0x0\n" \
"s_nop 0\n" \
"v_cvt_pk_bf16_f32 v209, v210, v211\n" \
"s_add_u32 s85, s48, 0x24800\n" \
"v_cvt_pk_bf16_f32 v225, v226, v227\n" \
"s_add_u32 s86, s48, 0x49000\n" \
"v_cvt_pk_bf16_f32 v241, v242, v243\n" \
"s_add_u32 s87, s48, 0x6d800\n" \
"ds_write_b32 v26, v44\n" \
"buffer_store_dword v32, v25, s[80:83], s49 offen sc1\n" \
"buffer_store_dwordx2 v[192:193], v24, s[80:83], s84 offen sc1\n" \
"buffer_store_dwordx2 v[208:209], v24, s[80:83], s85 offen sc1\n" \
"buffer_store_dwordx2 v[224:225], v24, s[80:83], s86 offen sc1\n" \
"buffer_store_dwordx2 v[240:241], v24, s[80:83], s87 offen sc1\n" \
"s_branch L_end_%=\n" \
"L_poll60_%=:\n" \
"s_mov_b32 s84, 0\n" \
"L_pl60_%=:\n" \
"s_sleep 1\n" \
"v_bfe_u32 v44, v20, 2, 5\n" \
"buffer_load_dword v44, v44, s[80:83], s38 offen offset:128 sc1\n" \
"s_waitcnt vmcnt(0)\n" \
"v_cmp_ne_u32_e32 vcc, s62, v44\n" \
"s_add_u32 s84, s84, 1\n" \
"s_nop 3\n" \
"s_cbranch_vccz L_pollr60_%=\n" \
"s_cmp_gt_u32 s84, 0x400000\n" \
"s_cbranch_scc0 L_pl60_%=\n" \
"s_branch L_pollr60_%=\n" \
"L_poll160_%=:\n" \
"s_mov_b32 s84, 0\n" \
"L_pl160_%=:\n" \
"s_sleep 1\n" \
"v_bfe_u32 v44, v20, 2, 5\n" \
"buffer_load_dword v44, v44, s[80:83], s38 offen offset:0 sc1\n" \
"s_waitcnt vmcnt(0)\n" \
"v_cmp_ne_u32_e32 vcc, s62, v44\n" \
"s_add_u32 s84, s84, 1\n" \
"s_nop 3\n" \
"s_cbranch_vccz L_pollr160_%=\n" \
"s_cmp_gt_u32 s84, 0x400000\n" \
"s_cbranch_scc0 L_pl160_%=\n" \
"s_branch L_pollr160_%=\n" \
".fill (L_pb + 42548 - .) / 4, 4, 0xBF800000\n" \
"L_w1_%=:\n" \
"s_waitcnt vmcnt(19)\n" \
"s_barrier\n" \
"s_nop 0\n" \
"ds_read_b64_tr_b16 a[160:161], v32 offset:0\n" \
"ds_read_b64_tr_b16 a[162:163], v32 offset:1024\n" \
"ds_read_b128 a[128:131], v28 offset:16384\n" \
"ds_read_b64_tr_b16 a[224:225], v32 offset:8192\n" \
"ds_read_b64_tr_b16 a[226:227], v32 offset:9216\n" \
"ds_read_b128 a[192:195], v29 offset:16384\n" \
"ds_read_b128 a[132:135], v28 offset:18432\n" \
"ds_read_b128 a[196:199], v29 offset:18432\n" \
"ds_read_b64_tr_b16 a[164:165], v33 offset:0\n" \
"ds_read_b64_tr_b16 a[166:167], v33 offset:1024\n" \
"ds_read_b64_tr_b16 a[228:229], v33 offset:8192\n" \
"ds_read_b64_tr_b16 a[230:231], v33 offset:9216\n" \
"ds_read_b128 a[136:139], v28 offset:20480\n" \
"ds_read_b128 a[200:203], v29 offset:20480\n" \
"s_waitcnt lgkmcnt(11)\n" \
"v_mfma_f32_16x16x32_bf16 a[0:3], a[128:131], a[160:163], 0\n" \
"ds_read_b64_tr_b16 a[168:169], v34 offset:0\n" \
"s_lshr_b32 s4, s2, 1\n" \
"s_waitcnt lgkmcnt(9)\n" \
"v_mfma_f32_16x16x32_bf16 a[0:3], a[192:195], a[224:227], a[0:3]\n" \
"ds_read_b64_tr_b16 a[170:171], v34 offset:1024\n" \
"s_and_b32 s4, s4, 0x7c\n" \
"s_waitcnt lgkmcnt(9)\n" \
"v_mfma_f32_16x16x32_bf16 a[16:19], a[132:135], a[160:163], 0\n" \
"ds_read_b64_tr_b16 a[232:233], v34 offset:8192\n" \
"v_mov_b32_e32 v213, s13\n" \
"s_waitcnt lgkmcnt(9)\n" \
"v_mfma_f32_16x16x32_bf16 a[16:19], a[196:199], a[224:227], a[16:19]\n" \
"ds_read_b64_tr_b16 a[234:235], v34 offset:9216\n" \
"s_or_b32 s4, s4, s3\n" \
"s_waitcnt lgkmcnt(8)\n" \
"v_mfma_f32_16x16x32_bf16 a[20:23], a[132:135], a[164:167], 0\n" \
"ds_read_b128 a[140:143], v28 offset:22528\n" \
"v_lshrrev_b32_e32 v212, 4, v20\n" \
"s_waitcnt lgkmcnt(7)\n" \
"v_mfma_f32_16x16x32_bf16 a[20:23], a[196:199], a[228:231], a[20:23]\n" \
"ds_read_b128 a[204:207], v29 offset:22528\n" \
"v_mad_u64_u32 v[214:215], s[4:5], s4, 36, v[212:213]\n" \
"v_mfma_f32_16x16x32_bf16 a[4:7], a[128:131], a[164:167], 0\n" \
"ds_read_b64_tr_b16 a[172:173], v35 offset:0\n" \
"v_mov_b32_e64 v217, s9\n" \
"v_mfma_f32_16x16x32_bf16 a[4:7], a[192:195], a[228:231], a[4:7]\n" \
"ds_read_b64_tr_b16 a[174:175], v35 offset:1024\n" \
"s_movk_i32 s4, 0x240\n" \
"s_waitcnt lgkmcnt(9)\n" \
"v_mfma_f32_16x16x32_bf16 a[32:35], a[136:139], a[160:163], 0\n" \
"ds_read_b64_tr_b16 a[236:237], v35 offset:8192\n" \
"v_cmp_gt_u32_e32 vcc, s4, v20\n" \
"s_waitcnt lgkmcnt(9)\n" \
"v_mfma_f32_16x16x32_bf16 a[32:35], a[200:203], a[224:227], a[32:35]\n" \
"ds_read_b64_tr_b16 a[238:239], v35 offset:9216\n" \
"s_lshl_b32 s4, s2, 3\n" \
"v_mfma_f32_16x16x32_bf16 a[36:39], a[136:139], a[164:167], 0\n" \
"ds_read_b128 a[144:147], v28 offset:0\n" \
"s_and_b32 s4, s4, 56\n" \
"v_mfma_f32_16x16x32_bf16 a[36:39], a[200:203], a[228:231], a[36:39]\n" \
"ds_read_b128 a[208:211], v29 offset:0\n" \
"v_cndmask_b32_e32 v217, v217, v213, vcc\n" \
"s_waitcnt lgkmcnt(10)\n" \
"v_mfma_f32_16x16x32_bf16 a[40:43], a[136:139], a[168:171], 0\n" \
"ds_read_b128 a[148:151], v28 offset:2048\n" \
"v_lshlrev_b32_e32 v213, 3, v214\n" \
"s_waitcnt lgkmcnt(9)\n" \
"v_mfma_f32_16x16x32_bf16 a[40:43], a[200:203], a[232:235], a[40:43]\n" \
"ds_read_b128 a[212:215], v29 offset:2048\n" \
"v_and_b32_e32 v213, 0x1ffc0, v213\n" \
"v_mfma_f32_16x16x32_bf16 a[24:27], a[132:135], a[168:171], 0\n" \
"ds_read_b128 a[152:155], v28 offset:4096\n" \
"v_and_b32_e64 v214, 7, v214\n" \
"v_mfma_f32_16x16x32_bf16 a[24:27], a[196:199], a[232:235], a[24:27]\n" \
"ds_read_b128 a[216:219], v29 offset:4096\n" \
"v_or3_b32 v213, v214, s4, v213\n" \
"v_mfma_f32_16x16x32_bf16 a[8:11], a[128:131], a[168:171], 0\n" \
"ds_read_b128 a[156:159], v28 offset:6144\n" \
"s_lshl_b32 s4, s2, 2\n" \
"v_mfma_f32_16x16x32_bf16 a[8:11], a[192:195], a[232:235], a[8:11]\n" \
"ds_read_b128 a[220:223], v29 offset:6144\n" \
"s_mov_b32 m0, s32\n" \
"s_waitcnt lgkmcnt(13)\n" \
"v_mfma_f32_16x16x32_bf16 a[48:51], a[140:143], a[160:163], 0\n" \
"s_mov_b32 exec_lo, 0xff000000\n" \
"s_mov_b32 exec_hi, 0x0\n" \
"global_load_lds_dword v[184:185], off\n" \
"s_mov_b64 exec, -1\n" \
"s_waitcnt lgkmcnt(12)\n" \
"v_mfma_f32_16x16x32_bf16 a[48:51], a[204:207], a[224:227], a[48:51]\n" \
"ds_read_b64_tr_b16 a[176:177], v36 offset:0\n" \
"s_or_b32 s4, s4, s3\n" \
"v_mfma_f32_16x16x32_bf16 a[52:55], a[140:143], a[164:167], 0\n" \
"ds_read_b64_tr_b16 a[178:179], v36 offset:1024\n" \
"s_mul_i32 s4, s4, 18\n" \
"v_mfma_f32_16x16x32_bf16 a[52:55], a[204:207], a[228:231], a[52:55]\n" \
"s_mov_b32 m0, s32\n" \
"ds_read_b64_tr_b16 a[240:241], v36 offset:8192\n" \
"v_mfma_f32_16x16x32_bf16 a[56:59], a[140:143], a[168:171], 0\n" \
"s_mov_b32 exec_lo, 0xffff\n" \
"s_mov_b32 exec_hi, 0x0\n" \
"global_load_lds_dword v[186:187], off\n" \
"s_mov_b64 exec, -1\n" \
"v_mfma_f32_16x16x32_bf16 a[56:59], a[204:207], a[232:235], a[56:59]\n" \
"ds_read_b64_tr_b16 a[242:243], v36 offset:9216\n" \
"v_min_u32_e32 v212, 53, v212\n" \
"s_waitcnt lgkmcnt(14)\n" \
"v_mfma_f32_16x16x32_bf16 a[60:63], a[140:143], a[172:175], 0\n" \
"ds_read_b64_tr_b16 a[180:181], v37 offset:0\n" \
"s_sub_i32 s4, s4, 36\n" \
"s_waitcnt lgkmcnt(13)\n" \
"v_mfma_f32_16x16x32_bf16 a[60:63], a[204:207], a[236:239], a[60:63]\n" \
"ds_read_b64_tr_b16 a[182:183], v37 offset:1024\n" \
"v_add_u32_e32 v212, s4, v212\n" \
"v_mfma_f32_16x16x32_bf16 a[44:47], a[136:139], a[172:175], 0\n" \
"ds_read_b64_tr_b16 a[244:245], v37 offset:8192\n" \
"v_mov_b32_e64 v215, s12\n" \
"v_mfma_f32_16x16x32_bf16 a[44:47], a[200:203], a[236:239], a[44:47]\n" \
"ds_read_b64_tr_b16 a[246:247], v37 offset:9216\n" \
"v_mov_b32_e64 v216, s8\n" \
"v_mfma_f32_16x16x32_bf16 a[28:31], a[132:135], a[172:175], 0\n" \
"ds_read_b64_tr_b16 a[184:185], v38 offset:0\n" \
"v_cndmask_b32_e64 v212, v212, v213, vcc\n" \
"v_mfma_f32_16x16x32_bf16 a[28:31], a[196:199], a[236:239], a[28:31]\n" \
"ds_read_b64_tr_b16 a[186:187], v38 offset:1024\n" \
"v_cndmask_b32_e64 v216, v216, v215, vcc\n" \
"v_mfma_f32_16x16x32_bf16 a[12:15], a[128:131], a[172:175], 0\n" \
"v_lshlrev_b32_e32 v212, 7, v212\n" \
"v_mov_b32_e32 v213, 0\n" \
"v_mfma_f32_16x16x32_bf16 a[12:15], a[192:195], a[236:239], a[12:15]\n" \
"v_lshl_add_u64 v[188:189], v[216:217], 0, v[212:213]\n" \
"s_movk_i32 s5, 0xd80\n" \
"s_waitcnt lgkmcnt(15)\n" \
"v_mfma_f32_16x16x32_bf16 a[76:79], a[144:147], a[172:175], 0\n" \
"ds_read_b64_tr_b16 a[248:249], v38 offset:8192\n" \
"s_lshr_b32 s6, s2, 5\n" \
"v_mfma_f32_16x16x32_bf16 a[76:79], a[208:211], a[236:239], a[76:79]\n" \
"v_lshrrev_b32_e32 v212, 4, v20\n" \
"v_lshl_or_b32 v215, s3, 6, v212\n" \
"v_mfma_f32_16x16x32_bf16 a[72:75], a[144:147], a[168:171], 0\n" \
"v_mul_u32_u24_e32 v216, 0x2aab, v215\n" \
"v_lshrrev_b32_e64 v216, 16, v216\n" \
"v_mfma_f32_16x16x32_bf16 a[72:75], a[208:211], a[232:235], a[72:75]\n" \
"ds_read_b64_tr_b16 a[250:251], v38 offset:9216\n" \
"v_mad_i32_i24 v217, v216, -6, v215\n" \
"v_mfma_f32_16x16x32_bf16 a[68:71], a[144:147], a[164:167], 0\n" \
"v_add_u32_e32 v218, 0xfffffe50, v215\n" \
"s_mul_i32 s35, s6, 0x48\n" \
"v_mfma_f32_16x16x32_bf16 a[68:71], a[208:211], a[228:231], a[68:71]\n" \
"v_lshrrev_b32_e32 v219, 1, v217\n" \
"v_add_u32_e32 v218, s35, v216\n" \
"v_mfma_f32_16x16x32_bf16 a[64:67], a[144:147], a[160:163], 0\n" \
"v_mul_u32_u24_e32 v219, 0x480, v219\n" \
"v_and_b32_e64 v214, 16, v20\n" \
"v_mfma_f32_16x16x32_bf16 a[64:67], a[208:211], a[224:227], a[64:67]\n" \
"ds_read_b64_tr_b16 a[188:189], v39 offset:0\n" \
"v_mad_u32_u24 v219, v218, s5, v219\n" \
"v_mfma_f32_16x16x32_bf16 a[80:83], a[148:151], a[160:163], 0\n" \
"v_or_b32_e32 v192, v219, v214\n" \
"v_mov_b32_e32 v220, 2.0\n" \
"s_waitcnt lgkmcnt(15)\n" \
"v_mfma_f32_16x16x32_bf16 a[80:83], a[212:215], a[224:227], a[80:83]\n" \
"v_mov_b32_e32 v222, 7\n" \
"v_lshlrev_b32_e64 v220, 7, v218\n" \
"v_mfma_f32_16x16x32_bf16 a[84:87], a[148:151], a[164:167], 0\n" \
"ds_read_b64_tr_b16 a[190:191], v39 offset:1024\n" \
"v_lshrrev_b32_e64 v218, 1, v218\n" \
"v_mfma_f32_16x16x32_bf16 a[84:87], a[212:215], a[228:231], a[84:87]\n" \
"v_and_b32_e32 v218, 4, v218\n" \
"v_and_b32_e32 v216, 2, v216\n" \
"v_mfma_f32_16x16x32_bf16 a[88:91], a[148:151], a[168:171], 0\n" \
"s_mul_i32 s7, s6, 0x2400\n" \
"s_sub_i32 s34, 0x22000, s7\n" \
"v_mfma_f32_16x16x32_bf16 a[88:91], a[212:215], a[232:235], a[88:91]\n" \
"ds_read_b64_tr_b16 a[252:253], v39 offset:8192\n" \
"v_bitop3_b32 v216, v217, v218, v216 bitop3:0x1e\n" \
"v_mfma_f32_16x16x32_bf16 a[92:95], a[148:151], a[172:175], 0\n" \
"v_or_b32_e32 v213, 0x24400, v20\n" \
"v_lshlrev_b32_e64 v216, 4, v216\n" \
"v_mfma_f32_16x16x32_bf16 a[92:95], a[212:215], a[236:239], a[92:95]\n" \
"v_add3_u32 v196, v220, s34, v216\n" \
"v_mov_b32_e64 v216, v213\n" \
"v_mfma_f32_16x16x32_bf16 a[108:111], a[152:155], a[172:175], 0\n" \
"ds_read_b64_tr_b16 a[254:255], v39 offset:9216\n" \
"s_movk_i32 s36, 0x1b0\n" \
"v_mfma_f32_16x16x32_bf16 a[108:111], a[216:219], a[236:239], a[108:111]\n" \
"v_or_b32_e32 v216, 0x100, v215\n" \
"v_mul_u32_u24_e32 v217, 0x2aab, v216\n" \
"v_mfma_f32_16x16x32_bf16 a[104:107], a[152:155], a[168:171], 0\n" \
"v_add_u32_e32 v215, 0xffffff50, v215\n" \
"v_lshrrev_b32_e32 v217, 16, v217\n" \
"v_mfma_f32_16x16x32_bf16 a[104:107], a[216:219], a[232:235], a[104:107]\n" \
"v_mov_b32_e64 v220, v215\n" \
"v_mad_i32_i24 v218, v217, -6, v216\n" \
"v_mfma_f32_16x16x32_bf16 a[100:103], a[152:155], a[164:167], 0\n" \
"v_cmp_gt_u32_e32 vcc, s36, v216\n" \
"v_mov_b32_e32 v221, 7\n" \
"v_mfma_f32_16x16x32_bf16 a[100:103], a[216:219], a[228:231], a[100:103]\n" \
"v_lshrrev_b32_e32 v222, 1, v218\n" \
"v_cndmask_b32_e32 v217, v220, v217, vcc\n" \
"v_mfma_f32_16x16x32_bf16 a[96:99], a[152:155], a[160:163], 0\n" \
"s_movk_i32 s4, 0x48\n" \
"v_add_u32_e32 v220, s35, v217\n" \
"v_mfma_f32_16x16x32_bf16 a[96:99], a[216:219], a[224:227], a[96:99]\n" \
"v_mul_u32_u24_e32 v222, 0x480, v222\n" \
"v_mad_u32_u24 v222, v220, s5, v222\n" \
"s_waitcnt lgkmcnt(15)\n" \
"v_mfma_f32_16x16x32_bf16 a[112:115], a[156:159], a[160:163], 0\n" \
"v_cmp_gt_u32_e64 s[4:5], s4, v215\n" \
"v_cndmask_b32_e32 v215, v221, v218, vcc\n" \
"v_mfma_f32_16x16x32_bf16 a[112:115], a[220:223], a[224:227], a[112:115]\n" \
"v_lshrrev_b32_e32 v218, 1, v220\n" \
"v_or_b32_e32 v214, v222, v214\n" \
"v_mfma_f32_16x16x32_bf16 a[116:119], a[156:159], a[164:167], 0\n" \
"v_mov_b32_e32 v222, 2.0\n" \
"v_and_b32_e32 v218, 4, v218\n" \
"v_mfma_f32_16x16x32_bf16 a[116:119], a[220:223], a[228:231], a[116:119]\n" \
"v_and_b32_e64 v217, 2, v217\n" \
"v_bitop3_b32 v215, v215, v218, v217 bitop3:0x1e\n" \
"v_mfma_f32_16x16x32_bf16 a[120:123], a[156:159], a[168:171], 0\n" \
"v_lshlrev_b32_e32 v223, 1, v220\n" \
"v_mov_b32_e32 v224, 2.0\n" \
"v_mfma_f32_16x16x32_bf16 a[120:123], a[220:223], a[232:235], a[120:123]\n" \
"v_cndmask_b32_e32 v193, v222, v214, vcc\n" \
"v_lshlrev_b32_e32 v214, 7, v220\n" \
"v_mfma_f32_16x16x32_bf16 a[124:127], a[156:159], a[172:175], 0\n" \
"v_lshlrev_b32_e32 v215, 4, v215\n" \
"s_mul_i32 s39, s3, 18\n" \
"v_mfma_f32_16x16x32_bf16 a[124:127], a[220:223], a[236:239], a[124:127]\n" \
"s_waitcnt vmcnt(5) lgkmcnt(0)\n" \
"s_barrier\n" \
"v_mfma_f32_16x16x32_bf16 v[52:55], a[128:131], a[176:179], 0\n" \
"s_add_u32 m0, s30, 0x0\n" \
"s_movk_i32 s84, 0x100\n" \
"v_mfma_f32_16x16x32_bf16 v[52:55], a[192:195], a[240:243], v[52:55]\n" \
"buffer_load_dwordx4 v22, s[64:67], s84 offen offset:0 lds\n" \
"v_mfma_f32_16x16x32_bf16 v[56:59], a[128:131], a[180:183], 0\n" \
"ds_read_b64_tr_b16 a[160:161], v32 offset:32768\n" \
"v_add3_u32 v214, v214, s34, v215\n" \
"v_mfma_f32_16x16x32_bf16 v[56:59], a[192:195], a[244:247], v[56:59]\n" \
"s_movk_i32 s84, 0x1d00\n" \
"v_mfma_f32_16x16x32_bf16 v[60:63], a[128:131], a[184:187], 0\n" \
"buffer_load_dwordx4 v23, s[64:67], s84 offen offset:1024 lds\n" \
"v_mfma_f32_16x16x32_bf16 v[60:63], a[192:195], a[248:251], v[60:63]\n" \
"ds_read_b64_tr_b16 a[162:163], v32 offset:33792\n" \
"v_cndmask_b32_e64 v195, v224, v223, s[4:5]\n" \
"v_mfma_f32_16x16x32_bf16 v[64:67], a[128:131], a[188:191], 0\n" \
"ds_read_b64_tr_b16 a[224:225], v32 offset:40960\n" \
"s_movk_i32 s4, 0x1f8\n" \
"v_mfma_f32_16x16x32_bf16 v[64:67], a[192:195], a[252:255], v[64:67]\n" \
"ds_read_b128 a[128:131], v28 offset:49152\n" \
"v_mov_b32_e64 v215, v213\n" \
"v_mfma_f32_16x16x32_bf16 v[80:83], a[132:135], a[188:191], 0\n" \
"ds_read_b128 a[192:195], v29 offset:49152\n" \
"s_add_i32 s35, s35, s39\n" \
"v_mfma_f32_16x16x32_bf16 v[80:83], a[196:199], a[252:255], v[80:83]\n" \
"s_movk_i32 s84, 0x3900\n" \
"v_mfma_f32_16x16x32_bf16 v[76:79], a[132:135], a[184:187], 0\n" \
"buffer_load_dwordx4 v22, s[64:67], s84 offen offset:2048 lds\n" \
"v_mfma_f32_16x16x32_bf16 v[76:79], a[196:199], a[248:251], v[76:79]\n" \
"s_movk_i32 s84, 0x5500\n" \
"v_mfma_f32_16x16x32_bf16 v[72:75], a[132:135], a[180:183], 0\n" \
"buffer_load_dwordx4 v23, s[64:67], s84 offen offset:3072 lds\n" \
"v_mfma_f32_16x16x32_bf16 v[72:75], a[196:199], a[244:247], v[72:75]\n" \
"s_add_u32 m0, s31, 0x0\n" \
"s_mov_b32 s84, 0x100000\n" \
"v_mfma_f32_16x16x32_bf16 v[68:71], a[132:135], a[176:179], 0\n" \
"buffer_load_dwordx4 v26, s[68:71], s84 offen offset:0 lds\n" \
"v_mfma_f32_16x16x32_bf16 v[68:71], a[196:199], a[240:243], v[68:71]\n" \
"ds_read_b128 a[132:135], v28 offset:51200\n" \
"v_cmp_gt_u32_e64 vcc, s4, v216\n" \
"v_mfma_f32_16x16x32_bf16 v[84:87], a[136:139], a[176:179], 0\n" \
"ds_read_b128 a[196:199], v29 offset:51200\n" \
"v_add_u32_e64 v212, s35, v212\n" \
"v_mfma_f32_16x16x32_bf16 v[84:87], a[200:203], a[240:243], v[84:87]\n" \
"s_addk_i32 s84, 0x7c00\n" \
"v_mfma_f32_16x16x32_bf16 v[88:91], a[136:139], a[180:183], 0\n" \
"buffer_load_dwordx4 v26, s[68:71], s84 offen offset:1024 lds\n" \
"v_mfma_f32_16x16x32_bf16 v[88:91], a[200:203], a[244:247], v[88:91]\n" \
"ds_read_b64_tr_b16 a[226:227], v32 offset:41984\n" \
"v_mov_b32_e32 v219, 2.0\n" \
"v_mfma_f32_16x16x32_bf16 v[92:95], a[136:139], a[184:187], 0\n" \
"s_addk_i32 s84, 0x7c00\n" \
"v_mfma_f32_16x16x32_bf16 v[92:95], a[200:203], a[248:251], v[92:95]\n" \
"buffer_load_dwordx4 v27, s[68:71], s84 offen offset:2048 lds\n" \
"v_mfma_f32_16x16x32_bf16 v[96:99], a[136:139], a[188:191], 0\n" \
"ds_read_b64_tr_b16 a[164:165], v33 offset:32768\n" \
"v_cndmask_b32_e32 v197, v215, v214, vcc\n" \
"v_mfma_f32_16x16x32_bf16 v[96:99], a[200:203], a[252:255], v[96:99]\n" \
"ds_read_b128 a[136:139], v28 offset:53248\n" \
"s_movk_i32 s35, 0x120\n" \
"v_mfma_f32_16x16x32_bf16 v[112:115], a[140:143], a[188:191], 0\n" \
"ds_read_b128 a[200:203], v29 offset:53248\n" \
"v_mul_u32_u24_e32 v214, 0xd80, v212\n" \
"v_mfma_f32_16x16x32_bf16 v[112:115], a[204:207], a[252:255], v[112:115]\n" \
"s_addk_i32 s84, 0x7c00\n" \
"v_mfma_f32_16x16x32_bf16 v[108:111], a[140:143], a[184:187], 0\n" \
"buffer_load_dwordx4 v27, s[68:71], s84 offen offset:3072 lds\n" \
"v_mfma_f32_16x16x32_bf16 v[108:111], a[204:207], a[248:251], v[108:111]\n" \
"s_add_u32 m0, s30, 0x1000\n" \
"s_mov_b32 s84, 0x8100\n" \
"v_mfma_f32_16x16x32_bf16 v[104:107], a[140:143], a[180:183], 0\n" \
"buffer_load_dwordx4 v22, s[64:67], s84 offen offset:0 lds\n" \
"v_mfma_f32_16x16x32_bf16 v[104:107], a[204:207], a[244:247], v[104:107]\n" \
"ds_read_b64_tr_b16 a[166:167], v33 offset:33792\n" \
"v_cmp_gt_u32_e64 vcc, s35, v20\n" \
"v_mfma_f32_16x16x32_bf16 v[100:103], a[140:143], a[176:179], 0\n" \
"ds_read_b64_tr_b16 a[228:229], v33 offset:40960\n" \
"v_lshrrev_b32_e64 v215, 1, v212\n" \
"v_mfma_f32_16x16x32_bf16 v[100:103], a[204:207], a[240:243], v[100:103]\n" \
"ds_read_b128 a[140:143], v28 offset:55296\n" \
"s_and_b32 s4, s2, 7\n" \
"v_mfma_f32_16x16x32_bf16 v[116:119], a[144:147], a[176:179], 0\n" \
"ds_read_b128 a[204:207], v29 offset:55296\n" \
"v_cndmask_b32_e32 v194, v219, v214, vcc\n" \
"v_mfma_f32_16x16x32_bf16 v[116:119], a[208:211], a[240:243], v[116:119]\n" \
"s_addk_i32 s84, 0x1c00\n" \
"v_mfma_f32_16x16x32_bf16 v[120:123], a[144:147], a[180:183], 0\n" \
"buffer_load_dwordx4 v23, s[64:67], s84 offen offset:1024 lds\n" \
"v_mfma_f32_16x16x32_bf16 v[120:123], a[208:211], a[244:247], v[120:123]\n" \
"s_addk_i32 s84, 0x1c00\n" \
"v_mfma_f32_16x16x32_bf16 v[124:127], a[144:147], a[184:187], 0\n" \
"buffer_load_dwordx4 v22, s[64:67], s84 offen offset:2048 lds\n" \
"v_mfma_f32_16x16x32_bf16 v[124:127], a[208:211], a[248:251], v[124:127]\n" \
"s_addk_i32 s84, 0x1c00\n" \
".fill (L_pb + 44768 - .) / 4, 4, 0xBF800000\n" \
"L_pp11:\n" \
"s_cbranch_execz L_pret\n" \
"v_mfma_f32_16x16x32_bf16 v[128:131], a[144:147], a[188:191], 0\n" \
"buffer_load_dwordx4 v23, s[64:67], s84 offen offset:3072 lds\n" \
"v_mfma_f32_16x16x32_bf16 v[128:131], a[208:211], a[252:255], v[128:131]\n" \
"ds_read_b128 a[144:147], v28 offset:32768\n" \
"v_lshlrev_b32_e32 v214, 7, v212\n" \
"v_mfma_f32_16x16x32_bf16 v[144:147], a[148:151], a[188:191], 0\n" \
"ds_read_b128 a[208:211], v29 offset:32768\n" \
"v_lshrrev_b32_e64 v212, 2, v212\n" \
"v_mfma_f32_16x16x32_bf16 v[144:147], a[212:215], a[252:255], v[144:147]\n" \
"s_add_u32 m0, s31, 0x4000\n" \
"s_mov_b32 s84, 0x100000\n" \
"v_mfma_f32_16x16x32_bf16 v[140:143], a[148:151], a[184:187], 0\n" \
"buffer_load_dwordx4 v26, s[72:75], s84 offen offset:0 lds\n" \
"v_mfma_f32_16x16x32_bf16 v[140:143], a[212:215], a[248:251], v[140:143]\n" \
"ds_read_b64_tr_b16 a[230:231], v33 offset:41984\n" \
"v_and_b32_e64 v212, 2, v212\n" \
"v_mfma_f32_16x16x32_bf16 v[136:139], a[148:151], a[180:183], 0\n" \
"s_addk_i32 s84, 0x7c00\n" \
"v_mfma_f32_16x16x32_bf16 v[136:139], a[212:215], a[244:247], v[136:139]\n" \
"buffer_load_dwordx4 v26, s[72:75], s84 offen offset:1024 lds\n" \
"v_mfma_f32_16x16x32_bf16 v[132:135], a[148:151], a[176:179], 0\n" \
"ds_read_b64_tr_b16 a[168:169], v34 offset:32768\n" \
"v_and_or_b32 v212, v215, 1, v212\n" \
"v_mfma_f32_16x16x32_bf16 v[132:135], a[212:215], a[240:243], v[132:135]\n" \
"ds_read_b128 a[148:151], v28 offset:34816\n" \
"s_lshl_b32 s5, s4, 2\n" \
"v_mfma_f32_16x16x32_bf16 v[148:151], a[152:155], a[176:179], 0\n" \
"ds_read_b128 a[212:215], v29 offset:34816\n" \
"s_bfe_u32 s38, s2, 0x20003\n" \
"v_mfma_f32_16x16x32_bf16 v[148:151], a[216:219], a[240:243], v[148:151]\n" \
"ds_read_b64_tr_b16 a[170:171], v34 offset:33792\n" \
"v_lshlrev_b32_e64 v212, 5, v212\n" \
"v_mfma_f32_16x16x32_bf16 v[152:155], a[152:155], a[180:183], 0\n" \
"ds_read_b64_tr_b16 a[232:233], v34 offset:40960\n" \
"s_movk_i32 s35, 0x60\n" \
"v_mfma_f32_16x16x32_bf16 v[152:155], a[216:219], a[244:247], v[152:155]\n" \
"ds_read_b64_tr_b16 a[234:235], v34 offset:41984\n" \
"s_or_b32 s5, s5, s38\n" \
"v_mfma_f32_16x16x32_bf16 v[156:159], a[152:155], a[184:187], 0\n" \
"ds_read_b64_tr_b16 a[172:173], v35 offset:32768\n" \
"v_bitop3_b32 v212, v212, s35, v214 bitop3:0x36\n" \
"v_mfma_f32_16x16x32_bf16 v[156:159], a[216:219], a[248:251], v[156:159]\n" \
"ds_read_b64_tr_b16 a[174:175], v35 offset:33792\n" \
"s_lshr_b32 s36, s2, 3\n" \
"v_mfma_f32_16x16x32_bf16 v[160:163], a[152:155], a[188:191], 0\n" \
"ds_read_b64_tr_b16 a[236:237], v35 offset:40960\n" \
"v_add_u32_e32 v212, s34, v212\n" \
"v_mfma_f32_16x16x32_bf16 v[160:163], a[216:219], a[252:255], v[160:163]\n" \
"ds_read_b128 a[152:155], v28 offset:36864\n" \
"s_mul_i32 s34, s5, 0x12000\n" \
"v_mfma_f32_16x16x32_bf16 v[176:179], a[156:159], a[188:191], 0\n" \
"ds_read_b128 a[216:219], v29 offset:36864\n" \
"s_or_b32 s40, s34, 0x6000000\n" \
"v_mfma_f32_16x16x32_bf16 v[176:179], a[220:223], a[252:255], v[176:179]\n" \
"ds_read_b64_tr_b16 a[238:239], v35 offset:41984\n" \
"s_mul_i32 s34, s36, 0x90000\n" \
"v_mfma_f32_16x16x32_bf16 v[172:175], a[156:159], a[184:187], 0\n" \
"s_lshl_b32 s36, s3, 14\n" \
"s_add_i32 s34, s34, s36\n" \
"v_mfma_f32_16x16x32_bf16 v[172:175], a[220:223], a[248:251], v[172:175]\n" \
"s_lshl_b32 s38, s5, 8\n" \
"s_add_i32 s41, s34, 0x4240000\n" \
"v_mfma_f32_16x16x32_bf16 v[168:171], a[156:159], a[180:183], 0\n" \
"s_mul_i32 s34, s5, 36\n" \
"s_add_i32 s5, s7, s40\n" \
"v_mfma_f32_16x16x32_bf16 v[168:171], a[220:223], a[244:247], v[168:171]\n" \
"s_addk_i32 s5, 0x2000\n" \
"s_mov_b32 s37, 2.0\n" \
"v_mfma_f32_16x16x32_bf16 v[164:167], a[156:159], a[176:179], 0\n" \
"s_or_b32 s38, s38, 0x5be8000\n" \
"v_mfma_f32_16x16x32_bf16 v[164:167], a[220:223], a[240:243], v[164:167]\n" \
"ds_read_b128 a[156:159], v28 offset:38912\n" \
"ds_read_b128 a[220:223], v29 offset:38912\n" \
"s_waitcnt lgkmcnt(15)\n" \
"v_mfma_f32_16x16x32_bf16 a[0:3], a[128:131], a[160:163], a[0:3]\n" \
"s_addk_i32 s84, 0x7c00\n" \
"v_mfma_f32_16x16x32_bf16 a[0:3], a[192:195], a[224:227], a[0:3]\n" \
"buffer_load_dwordx4 v27, s[72:75], s84 offen offset:2048 lds\n" \
"v_mfma_f32_16x16x32_bf16 a[4:7], a[128:131], a[164:167], a[4:7]\n" \
"ds_read_b64_tr_b16 a[176:177], v36 offset:32768\n" \
"s_waitcnt lgkmcnt(15)\n" \
"v_mfma_f32_16x16x32_bf16 a[4:7], a[192:195], a[228:231], a[4:7]\n" \
"s_addk_i32 s84, 0x7c00\n" \
"s_waitcnt lgkmcnt(11)\n" \
"v_mfma_f32_16x16x32_bf16 a[8:11], a[128:131], a[168:171], a[8:11]\n" \
"buffer_load_dwordx4 v27, s[72:75], s84 offen offset:3072 lds\n" \
"s_waitcnt lgkmcnt(9)\n" \
"v_mfma_f32_16x16x32_bf16 a[8:11], a[192:195], a[232:235], a[8:11]\n" \
"ds_read_b64_tr_b16 a[178:179], v36 offset:33792\n" \
"s_waitcnt lgkmcnt(8)\n" \
"v_mfma_f32_16x16x32_bf16 a[12:15], a[128:131], a[172:175], a[12:15]\n" \
"ds_read_b64_tr_b16 a[240:241], v36 offset:40960\n" \
"s_waitcnt lgkmcnt(5)\n" \
"v_mfma_f32_16x16x32_bf16 a[12:15], a[192:195], a[236:239], a[12:15]\n" \
"s_cmp_lg_u32 s3, 0\n" \
"s_cselect_b32 s36, s37, s5\n" \
"v_mfma_f32_16x16x32_bf16 a[28:31], a[132:135], a[172:175], a[28:31]\n" \
"s_lshl_b32 s5, s6, 2\n" \
"s_or_b32 s5, s38, s5\n" \
"v_mfma_f32_16x16x32_bf16 a[28:31], a[196:199], a[236:239], a[28:31]\n" \
"ds_read_b64_tr_b16 a[242:243], v36 offset:41984\n" \
"s_lshl_b32 s35, s3, 10\n" \
"v_mfma_f32_16x16x32_bf16 a[24:27], a[132:135], a[168:171], a[24:27]\n" \
"s_cmpk_lt_u32 s3, 3\n" \
"s_cselect_b32 s37, s37, s5\n" \
"v_mfma_f32_16x16x32_bf16 a[24:27], a[196:199], a[232:235], a[24:27]\n" \
"s_lshl_b32 s5, s4, 16\n" \
"s_lshl_b32 s4, s4, 11\n" \
"v_mfma_f32_16x16x32_bf16 a[20:23], a[132:135], a[164:167], a[20:23]\n" \
"v_cndmask_b32_e32 v198, v213, v212, vcc\n" \
"v_or_b32_e32 v212, s35, v20\n" \
"v_mfma_f32_16x16x32_bf16 a[20:23], a[196:199], a[228:231], a[20:23]\n" \
"ds_read_b64_tr_b16 a[180:181], v37 offset:32768\n" \
"s_or_b32 s39, s40, s35\n" \
"v_mfma_f32_16x16x32_bf16 a[16:19], a[132:135], a[160:163], a[16:19]\n" \
"s_or_b32 s55, s41, s4\n" \
"v_or_b32_e32 v199, 0x22000, v212\n" \
"v_mfma_f32_16x16x32_bf16 a[16:19], a[196:199], a[224:227], a[16:19]\n" \
"s_add_i32 s35, s39, s7\n" \
"s_add_i32 s54, s41, s5\n" \
"v_mfma_f32_16x16x32_bf16 a[32:35], a[136:139], a[160:163], a[32:35]\n" \
"s_mov_b32 m0, s32\n" \
"ds_read_b64_tr_b16 a[182:183], v37 offset:33792\n" \
"v_mfma_f32_16x16x32_bf16 a[32:35], a[200:203], a[224:227], a[32:35]\n" \
"s_mov_b32 exec_lo, 0x0\n" \
"s_mov_b32 exec_hi, 0xff\n" \
"global_load_lds_dword v[184:185], off\n" \
"s_mov_b64 exec, -1\n" \
"v_mfma_f32_16x16x32_bf16 a[36:39], a[136:139], a[164:167], a[36:39]\n" \
"s_add_i32 s55, s55, 0x80000\n" \
"v_mov_b32_e32 v215, 0xffff\n" \
"v_mfma_f32_16x16x32_bf16 a[36:39], a[200:203], a[228:231], a[36:39]\n" \
"ds_read_b64_tr_b16 a[244:245], v37 offset:40960\n" \
"s_and_b32 s4, s3, 1\n" \
"v_mfma_f32_16x16x32_bf16 a[40:43], a[136:139], a[168:171], a[40:43]\n" \
"s_lshl_b32 s5, s4, 4\n" \
"v_lshrrev_b32_e32 v212, 4, v20\n" \
"v_mfma_f32_16x16x32_bf16 a[40:43], a[200:203], a[232:235], a[40:43]\n" \
"s_mov_b32 m0, s32\n" \
"v_lshrrev_b32_e32 v218, 6, v20\n" \
"v_mfma_f32_16x16x32_bf16 a[44:47], a[136:139], a[172:175], a[44:47]\n" \
"s_mov_b32 exec_lo, 0xffff0000\n" \
"s_mov_b32 exec_hi, 0x0\n" \
"global_load_lds_dword v[186:187], off\n" \
"s_mov_b64 exec, -1\n" \
"v_mfma_f32_16x16x32_bf16 a[44:47], a[200:203], a[236:239], a[44:47]\n" \
"ds_read_b64_tr_b16 a[246:247], v37 offset:41984\n" \
"v_and_b32_e64 v219, 12, v218\n" \
"v_mfma_f32_16x16x32_bf16 a[60:63], a[140:143], a[172:175], a[60:63]\n" \
"v_and_or_b32 v214, v212, 15, s5\n" \
"v_cmp_gt_u32_e64 vcc, v219, v214\n" \
"v_mfma_f32_16x16x32_bf16 a[60:63], a[204:207], a[236:239], a[60:63]\n" \
"v_mov_b32_e32 v220, 0xffff0000\n" \
"s_lshl_b32 s5, s2, 2\n" \
"v_mfma_f32_16x16x32_bf16 a[56:59], a[140:143], a[168:171], a[56:59]\n" \
"ds_read_b64_tr_b16 a[184:185], v38 offset:32768\n" \
"v_cndmask_b32_e64 v217, v215, 0, vcc\n" \
"v_mfma_f32_16x16x32_bf16 a[56:59], a[204:207], a[232:235], a[56:59]\n" \
"v_cmp_lt_u32_e32 vcc, v219, v214\n" \
"v_mov_b32_e64 v213, s23\n" \
"v_mfma_f32_16x16x32_bf16 a[52:55], a[140:143], a[164:167], a[52:55]\n" \
"s_or_b32 s5, s5, s3\n" \
"v_cndmask_b32_e32 v221, 0, v220, vcc\n" \
"v_mfma_f32_16x16x32_bf16 a[52:55], a[204:207], a[228:231], a[52:55]\n" \
"ds_read_b64_tr_b16 a[186:187], v38 offset:33792\n" \
"v_or_b32_e64 v12, v217, v221\n" \
"v_mfma_f32_16x16x32_bf16 a[48:51], a[140:143], a[160:163], a[48:51]\n" \
"v_or_b32_e32 v217, 2, v219\n" \
"v_cmp_gt_u32_e32 vcc, v217, v214\n" \
"v_mfma_f32_16x16x32_bf16 a[48:51], a[204:207], a[224:227], a[48:51]\n" \
"v_mov_b32_e32 v216, s25\n" \
"s_movk_i32 s40, 0xf400\n" \
"v_mfma_f32_16x16x32_bf16 a[64:67], a[144:147], a[160:163], a[64:67]\n" \
"ds_read_b64_tr_b16 a[248:249], v38 offset:40960\n" \
"v_cndmask_b32_e64 v221, v215, 0, vcc\n" \
"v_mfma_f32_16x16x32_bf16 a[64:67], a[208:211], a[224:227], a[64:67]\n" \
"v_cmp_lt_u32_e32 vcc, v217, v214\n" \
"s_lshl_b32 s4, s4, 6\n" \
"v_mfma_f32_16x16x32_bf16 a[68:71], a[144:147], a[164:167], a[68:71]\n" \
"v_cndmask_b32_e32 v217, 0, v220, vcc\n" \
"v_or_b32_e32 v13, v221, v217\n" \
"v_mfma_f32_16x16x32_bf16 a[68:71], a[208:211], a[228:231], a[68:71]\n" \
"ds_read_b64_tr_b16 a[250:251], v38 offset:41984\n" \
"v_or_b32_e64 v217, 16, v219\n" \
"v_mfma_f32_16x16x32_bf16 a[72:75], a[144:147], a[168:171], a[72:75]\n" \
"v_cmp_gt_u32_e32 vcc, v217, v214\n" \
"s_nop 1\n" \
"v_cndmask_b32_e64 v221, v215, 0, vcc\n" \
"v_mfma_f32_16x16x32_bf16 a[72:75], a[208:211], a[232:235], a[72:75]\n" \
"v_cmp_lt_u32_e32 vcc, v217, v214\n" \
"s_nop 1\n" \
"v_cndmask_b32_e64 v217, 0, v220, vcc\n" \
"v_mfma_f32_16x16x32_bf16 a[76:79], a[144:147], a[172:175], a[76:79]\n" \
"v_or_b32_e32 v14, v221, v217\n" \
"v_or_b32_e32 v217, 18, v219\n" \
"v_mfma_f32_16x16x32_bf16 a[76:79], a[208:211], a[236:239], a[76:79]\n" \
"ds_read_b64_tr_b16 a[188:189], v39 offset:32768\n" \
"v_cmp_gt_u32_e64 vcc, v217, v214\n" \
"v_mfma_f32_16x16x32_bf16 a[92:95], a[148:151], a[172:175], a[92:95]\n" \
"s_nop 0\n" \
"v_cndmask_b32_e64 v215, v215, 0, vcc\n" \
"v_cmp_lt_u32_e32 vcc, v217, v214\n" \
"v_mfma_f32_16x16x32_bf16 a[92:95], a[212:215], a[236:239], a[92:95]\n" \
"s_nop 0\n" \
"v_cndmask_b32_e32 v214, 0, v220, vcc\n" \
"v_or_b32_e64 v15, v215, v214\n" \
"v_mfma_f32_16x16x32_bf16 a[88:91], a[148:151], a[168:171], a[88:91]\n" \
"ds_read_b64_tr_b16 a[190:191], v39 offset:33792\n" \
"v_mad_u64_u32 v[214:215], s[6:7], s5, 42, v[212:213]\n" \
"v_mfma_f32_16x16x32_bf16 a[88:91], a[212:215], a[232:235], a[88:91]\n" \
"s_movk_i32 s5, 0x1440\n" \
"v_cmp_gt_u32_e32 vcc, s5, v214\n" \
"v_mfma_f32_16x16x32_bf16 a[84:87], a[148:151], a[164:167], a[84:87]\n" \
"v_min_u32_e32 v217, 0xa43f, v214\n" \
"v_mov_b32_e64 v214, s24\n" \
"v_mfma_f32_16x16x32_bf16 a[84:87], a[212:215], a[228:231], a[84:87]\n" \
"ds_read_b64_tr_b16 a[252:253], v39 offset:40960\n" \
"v_cndmask_b32_e64 v215, v216, v213, vcc\n" \
"v_mfma_f32_16x16x32_bf16 a[80:83], a[148:151], a[160:163], a[80:83]\n" \
"v_mov_b32_e32 v213, s22\n" \
"s_movk_i32 s6, 0xffee\n" \
"v_mfma_f32_16x16x32_bf16 a[80:83], a[212:215], a[224:227], a[80:83]\n" \
"v_cndmask_b32_e64 v214, v214, v213, vcc\n" \
"v_add_u32_e32 v213, 0xffffebc0, v217\n" \
"v_mfma_f32_16x16x32_bf16 a[96:99], a[152:155], a[160:163], a[96:99]\n" \
"ds_read_b64_tr_b16 a[254:255], v39 offset:41984\n" \
"s_lshl_b32 s5, s2, 5\n" \
"v_mfma_f32_16x16x32_bf16 a[96:99], a[216:219], a[224:227], a[96:99]\n" \
"v_cndmask_b32_e32 v213, v213, v217, vcc\n" \
"v_lshlrev_b32_e64 v216, 7, v213\n" \
"v_mfma_f32_16x16x32_bf16 a[100:103], a[152:155], a[164:167], a[100:103]\n" \
"v_mul_u32_u24_e32 v213, 0xe39, v212\n" \
"v_mov_b32_e64 v217, 0\n" \
"v_mfma_f32_16x16x32_bf16 a[100:103], a[216:219], a[228:231], a[100:103]\n" \
"v_lshrrev_b32_e64 v213, 16, v213\n" \
"v_lshl_add_u64 v[16:17], v[214:215], 0, v[216:217]\n" \
"v_mfma_f32_16x16x32_bf16 a[104:107], a[152:155], a[168:171], a[104:107]\n" \
"v_mad_i32_i24 v214, v213, s6, v212\n" \
"s_add_i32 s5, s5, 32\n" \
"v_mfma_f32_16x16x32_bf16 a[104:107], a[216:219], a[232:235], a[104:107]\n" \
"v_lshlrev_b32_e32 v215, 4, v214\n" \
"v_add_u32_e64 v216, s5, v215\n" \
"v_mfma_f32_16x16x32_bf16 a[108:111], a[152:155], a[172:175], a[108:111]\n" \
"v_and_b32_e32 v216, 0xf0, v216\n" \
"v_cmp_gt_u32_e64 vcc, 16, v214\n" \
"v_mfma_f32_16x16x32_bf16 a[108:111], a[216:219], a[236:239], a[108:111]\n" \
"s_movk_i32 s7, 0xf800\n" \
"v_cndmask_b32_e32 v214, v215, v216, vcc\n" \
"s_waitcnt lgkmcnt(15)\n" \
"v_mfma_f32_16x16x32_bf16 a[124:127], a[156:159], a[172:175], a[124:127]\n" \
"v_or_b32_e32 v215, 64, v212\n" \
"v_mul_u32_u24_e32 v216, 0xe39, v215\n" \
"v_mfma_f32_16x16x32_bf16 a[124:127], a[220:223], a[236:239], a[124:127]\n" \
"v_lshrrev_b32_e64 v216, 16, v216\n" \
"v_mad_i32_i24 v215, v216, s6, v215\n" \
"v_mfma_f32_16x16x32_bf16 a[120:123], a[156:159], a[168:171], a[120:123]\n" \
"v_lshlrev_b32_e32 v217, 4, v215\n" \
"v_add_u32_e32 v220, s5, v217\n" \
"v_mfma_f32_16x16x32_bf16 a[120:123], a[220:223], a[232:235], a[120:123]\n" \
"v_and_b32_e32 v220, 0xf0, v220\n" \
"v_cmp_gt_u32_e64 vcc, 16, v215\n" \
"v_mfma_f32_16x16x32_bf16 a[116:119], a[156:159], a[164:167], a[116:119]\n" \
"s_nop 0\n" \
"v_cndmask_b32_e32 v215, v217, v220, vcc\n" \
"v_or_b32_e32 v217, 0x80, v212\n" \
"v_mfma_f32_16x16x32_bf16 a[116:119], a[220:223], a[228:231], a[116:119]\n" \
"v_mul_u32_u24_e32 v220, 0xe39, v217\n" \
"v_lshrrev_b32_e64 v220, 16, v220\n" \
"v_mfma_f32_16x16x32_bf16 a[112:115], a[156:159], a[160:163], a[112:115]\n" \
"v_mad_i32_i24 v217, v220, s6, v217\n" \
"v_lshlrev_b32_e64 v221, 4, v217\n" \
"v_mfma_f32_16x16x32_bf16 a[112:115], a[220:223], a[224:227], a[112:115]\n" \
"s_waitcnt vmcnt(20)\n" \
"v_readfirstlane_b32 s62, v180\n" \
"s_nop 3\n" \
"s_add_u32 s62, s62, 0x9E3779B1\n" \
"s_waitcnt vmcnt(2) lgkmcnt(0)\n" \
"s_barrier\n" \
"v_mfma_f32_16x16x32_bf16 v[52:55], a[128:131], a[176:179], v[52:55]\n" \
"s_add_u32 m0, s30, 0x8000\n" \
"s_movk_i32 s84, 0x180\n" \
"v_mfma_f32_16x16x32_bf16 v[52:55], a[192:195], a[240:243], v[52:55]\n" \
"buffer_load_dwordx4 v22, s[64:67], s84 offen offset:0 lds\n" \
"v_mfma_f32_16x16x32_bf16 v[56:59], a[128:131], a[180:183], v[56:59]\n" \
"ds_read_b64_tr_b16 a[160:161], v32 offset:0\n" \
"v_add_u32_e64 v222, s5, v221\n" \
"v_mfma_f32_16x16x32_bf16 v[56:59], a[192:195], a[244:247], v[56:59]\n" \
"s_movk_i32 s84, 0x1d80\n" \
"v_mfma_f32_16x16x32_bf16 v[60:63], a[128:131], a[184:187], v[60:63]\n" \
"buffer_load_dwordx4 v23, s[64:67], s84 offen offset:1024 lds\n" \
"v_mfma_f32_16x16x32_bf16 v[60:63], a[192:195], a[248:251], v[60:63]\n" \
"ds_read_b64_tr_b16 a[162:163], v32 offset:1024\n" \
"v_and_b32_e32 v222, 0xf0, v222\n" \
"v_mfma_f32_16x16x32_bf16 v[64:67], a[128:131], a[188:191], v[64:67]\n" \
"ds_read_b64_tr_b16 a[224:225], v32 offset:8192\n" \
"v_cmp_gt_u32_e32 vcc, 16, v217\n" \
"v_mfma_f32_16x16x32_bf16 v[64:67], a[192:195], a[252:255], v[64:67]\n" \
"ds_read_b128 a[128:131], v28 offset:16384\n" \
"v_cndmask_b32_e64 v217, v221, v222, vcc\n" \
"v_mfma_f32_16x16x32_bf16 v[80:83], a[132:135], a[188:191], v[80:83]\n" \
"ds_read_b128 a[192:195], v29 offset:16384\n" \
"v_or_b32_e32 v221, 0xc0, v212\n" \
"v_mfma_f32_16x16x32_bf16 v[80:83], a[196:199], a[252:255], v[80:83]\n" \
"s_movk_i32 s84, 0x3980\n" \
"v_mfma_f32_16x16x32_bf16 v[76:79], a[132:135], a[184:187], v[76:79]\n" \
"buffer_load_dwordx4 v22, s[64:67], s84 offen offset:2048 lds\n" \
"v_mfma_f32_16x16x32_bf16 v[76:79], a[196:199], a[248:251], v[76:79]\n" \
"s_movk_i32 s84, 0x5580\n" \
"v_mfma_f32_16x16x32_bf16 v[72:75], a[132:135], a[180:183], v[72:75]\n" \
"buffer_load_dwordx4 v23, s[64:67], s84 offen offset:3072 lds\n" \
"v_mfma_f32_16x16x32_bf16 v[72:75], a[196:199], a[244:247], v[72:75]\n" \
"s_add_u32 m0, s31, 0x8000\n" \
"s_mov_b32 s84, 0x180000\n" \
"v_mfma_f32_16x16x32_bf16 v[68:71], a[132:135], a[176:179], v[68:71]\n" \
"buffer_load_dwordx4 v26, s[68:71], s84 offen offset:0 lds\n" \
"v_mfma_f32_16x16x32_bf16 v[68:71], a[196:199], a[240:243], v[68:71]\n" \
"ds_read_b128 a[132:135], v28 offset:18432\n" \
"v_mul_u32_u24_e32 v222, 0xe39, v221\n" \
"v_mfma_f32_16x16x32_bf16 v[84:87], a[136:139], a[176:179], v[84:87]\n" \
"ds_read_b128 a[196:199], v29 offset:18432\n" \
"v_lshrrev_b32_e64 v222, 16, v222\n" \
"v_mfma_f32_16x16x32_bf16 v[84:87], a[200:203], a[240:243], v[84:87]\n" \
"s_addk_i32 s84, 0x7c00\n" \
"v_mfma_f32_16x16x32_bf16 v[88:91], a[136:139], a[180:183], v[88:91]\n" \
"buffer_load_dwordx4 v26, s[68:71], s84 offen offset:1024 lds\n" \
"v_mfma_f32_16x16x32_bf16 v[88:91], a[200:203], a[244:247], v[88:91]\n" \
"ds_read_b64_tr_b16 a[226:227], v32 offset:9216\n" \
"v_mad_i32_i24 v221, v222, s6, v221\n" \
"v_mfma_f32_16x16x32_bf16 v[92:95], a[136:139], a[184:187], v[92:95]\n" \
"s_addk_i32 s84, 0x7c00\n" \
"v_mfma_f32_16x16x32_bf16 v[92:95], a[200:203], a[248:251], v[92:95]\n" \
"buffer_load_dwordx4 v27, s[68:71], s84 offen offset:2048 lds\n" \
"v_mfma_f32_16x16x32_bf16 v[96:99], a[136:139], a[188:191], v[96:99]\n" \
"ds_read_b64_tr_b16 a[164:165], v33 offset:0\n" \
"v_lshlrev_b32_e64 v223, 4, v221\n" \
"v_mfma_f32_16x16x32_bf16 v[96:99], a[200:203], a[252:255], v[96:99]\n" \
"ds_read_b128 a[136:139], v28 offset:20480\n" \
"v_add_u32_e64 v224, s5, v223\n" \
"v_mfma_f32_16x16x32_bf16 v[112:115], a[140:143], a[188:191], v[112:115]\n" \
"ds_read_b128 a[200:203], v29 offset:20480\n" \
"v_and_b32_e32 v224, 0xf0, v224\n" \
"v_mfma_f32_16x16x32_bf16 v[112:115], a[204:207], a[252:255], v[112:115]\n" \
"s_addk_i32 s84, 0x7c00\n" \
"v_mfma_f32_16x16x32_bf16 v[108:111], a[140:143], a[184:187], v[108:111]\n" \
"buffer_load_dwordx4 v27, s[68:71], s84 offen offset:3072 lds\n" \
"v_mfma_f32_16x16x32_bf16 v[108:111], a[204:207], a[248:251], v[108:111]\n" \
"s_add_u32 m0, s30, 0x9000\n" \
"s_mov_b32 s84, 0x8180\n" \
"v_mfma_f32_16x16x32_bf16 v[104:107], a[140:143], a[180:183], v[104:107]\n" \
"buffer_load_dwordx4 v22, s[64:67], s84 offen offset:0 lds\n" \
"v_mfma_f32_16x16x32_bf16 v[104:107], a[204:207], a[244:247], v[104:107]\n" \
"ds_read_b64_tr_b16 a[166:167], v33 offset:1024\n" \
"v_cmp_gt_u32_e32 vcc, 16, v221\n" \
"v_mfma_f32_16x16x32_bf16 v[100:103], a[140:143], a[176:179], v[100:103]\n" \
"ds_read_b64_tr_b16 a[228:229], v33 offset:8192\n" \
"v_cndmask_b32_e64 v221, v223, v224, vcc\n" \
"v_mfma_f32_16x16x32_bf16 v[100:103], a[204:207], a[240:243], v[100:103]\n" \
"ds_read_b128 a[140:143], v28 offset:22528\n" \
"v_or_b32_e32 v223, 0x100, v212\n" \
"v_mfma_f32_16x16x32_bf16 v[116:119], a[144:147], a[176:179], v[116:119]\n" \
"ds_read_b128 a[204:207], v29 offset:22528\n" \
"v_mul_u32_u24_e32 v224, 0xe39, v223\n" \
"v_mfma_f32_16x16x32_bf16 v[116:119], a[208:211], a[240:243], v[116:119]\n" \
"s_addk_i32 s84, 0x1c00\n" \
"v_mfma_f32_16x16x32_bf16 v[120:123], a[144:147], a[180:183], v[120:123]\n" \
"buffer_load_dwordx4 v23, s[64:67], s84 offen offset:1024 lds\n" \
"v_mfma_f32_16x16x32_bf16 v[120:123], a[208:211], a[244:247], v[120:123]\n" \
"s_addk_i32 s84, 0x1c00\n" \
"v_mfma_f32_16x16x32_bf16 v[124:127], a[144:147], a[184:187], v[124:127]\n" \
"buffer_load_dwordx4 v22, s[64:67], s84 offen offset:2048 lds\n" \
"v_mfma_f32_16x16x32_bf16 v[124:127], a[208:211], a[248:251], v[124:127]\n" \
"s_addk_i32 s84, 0x1c00\n" \
"v_mfma_f32_16x16x32_bf16 v[128:131], a[144:147], a[188:191], v[128:131]\n" \
"buffer_load_dwordx4 v23, s[64:67], s84 offen offset:3072 lds\n" \
"v_mfma_f32_16x16x32_bf16 v[128:131], a[208:211], a[252:255], v[128:131]\n" \
"ds_read_b128 a[144:147], v28 offset:0\n" \
"v_lshrrev_b32_e32 v224, 16, v224\n" \
"v_mfma_f32_16x16x32_bf16 v[144:147], a[148:151], a[188:191], v[144:147]\n" \
"ds_read_b128 a[208:211], v29 offset:0\n" \
"v_mad_i32_i24 v223, v224, s6, v223\n" \
"v_mfma_f32_16x16x32_bf16 v[144:147], a[212:215], a[252:255], v[144:147]\n" \
"s_add_u32 m0, s31, 0xc000\n" \
"s_mov_b32 s84, 0x180000\n" \
"v_mfma_f32_16x16x32_bf16 v[140:143], a[148:151], a[184:187], v[140:143]\n" \
"buffer_load_dwordx4 v26, s[72:75], s84 offen offset:0 lds\n" \
"v_mfma_f32_16x16x32_bf16 v[140:143], a[212:215], a[248:251], v[140:143]\n" \
"ds_read_b64_tr_b16 a[230:231], v33 offset:9216\n" \
"v_lshlrev_b32_e64 v225, 4, v223\n" \
"v_mfma_f32_16x16x32_bf16 v[136:139], a[148:151], a[180:183], v[136:139]\n" \
"s_addk_i32 s84, 0x7c00\n" \
"v_mfma_f32_16x16x32_bf16 v[136:139], a[212:215], a[244:247], v[136:139]\n" \
"buffer_load_dwordx4 v26, s[72:75], s84 offen offset:1024 lds\n" \
"v_mfma_f32_16x16x32_bf16 v[132:135], a[148:151], a[176:179], v[132:135]\n" \
"ds_read_b64_tr_b16 a[168:169], v34 offset:0\n" \
"v_add_u32_e32 v226, s5, v225\n" \
"v_mfma_f32_16x16x32_bf16 v[132:135], a[212:215], a[240:243], v[132:135]\n" \
"ds_read_b128 a[148:151], v28 offset:2048\n" \
"v_and_b32_e32 v226, 0xf0, v226\n" \
"v_mfma_f32_16x16x32_bf16 v[148:151], a[152:155], a[176:179], v[148:151]\n" \
"ds_read_b128 a[212:215], v29 offset:2048\n" \
"v_cmp_gt_u32_e64 vcc, 16, v223\n" \
"v_mfma_f32_16x16x32_bf16 v[148:151], a[216:219], a[240:243], v[148:151]\n" \
"ds_read_b64_tr_b16 a[170:171], v34 offset:1024\n" \
"v_cndmask_b32_e64 v223, v225, v226, vcc\n" \
"v_mfma_f32_16x16x32_bf16 v[152:155], a[152:155], a[180:183], v[152:155]\n" \
"ds_read_b64_tr_b16 a[232:233], v34 offset:8192\n" \
"v_or_b32_e32 v225, 0x140, v212\n" \
"v_mfma_f32_16x16x32_bf16 v[152:155], a[216:219], a[244:247], v[152:155]\n" \
"ds_read_b64_tr_b16 a[234:235], v34 offset:9216\n" \
"v_mul_u32_u24_e32 v226, 0xe39, v225\n" \
"v_mfma_f32_16x16x32_bf16 v[156:159], a[152:155], a[184:187], v[156:159]\n" \
"ds_read_b64_tr_b16 a[172:173], v35 offset:0\n" \
"v_lshrrev_b32_e64 v226, 16, v226\n" \
"v_mfma_f32_16x16x32_bf16 v[156:159], a[216:219], a[248:251], v[156:159]\n" \
"ds_read_b64_tr_b16 a[174:175], v35 offset:1024\n" \
"v_mad_i32_i24 v225, v226, s6, v225\n" \
"v_mfma_f32_16x16x32_bf16 v[160:163], a[152:155], a[188:191], v[160:163]\n" \
"ds_read_b64_tr_b16 a[236:237], v35 offset:8192\n" \
"v_lshlrev_b32_e64 v227, 4, v225\n" \
"v_mfma_f32_16x16x32_bf16 v[160:163], a[216:219], a[252:255], v[160:163]\n" \
"ds_read_b128 a[152:155], v28 offset:4096\n" \
"v_add_u32_e64 v228, s5, v227\n" \
"v_mfma_f32_16x16x32_bf16 v[176:179], a[156:159], a[188:191], v[176:179]\n" \
"ds_read_b128 a[216:219], v29 offset:4096\n" \
"v_and_b32_e32 v228, 0xf0, v228\n" \
"v_mfma_f32_16x16x32_bf16 v[176:179], a[220:223], a[252:255], v[176:179]\n" \
"ds_read_b64_tr_b16 a[238:239], v35 offset:9216\n" \
"v_cmp_gt_u32_e64 vcc, 16, v225\n" \
"v_mfma_f32_16x16x32_bf16 v[172:175], a[156:159], a[184:187], v[172:175]\n" \
"s_nop 0\n" \
"v_cndmask_b32_e32 v225, v227, v228, vcc\n" \
"v_or_b32_e32 v227, 0x180, v212\n" \
"v_mfma_f32_16x16x32_bf16 v[172:175], a[220:223], a[248:251], v[172:175]\n" \
"v_mul_u32_u24_e32 v228, 0xe39, v227\n" \
"v_lshrrev_b32_e64 v228, 16, v228\n" \
"v_mfma_f32_16x16x32_bf16 v[168:171], a[156:159], a[180:183], v[168:171]\n" \
"v_mad_i32_i24 v227, v228, s6, v227\n" \
"v_lshlrev_b32_e64 v229, 4, v227\n" \
"v_mfma_f32_16x16x32_bf16 v[168:171], a[220:223], a[244:247], v[168:171]\n" \
"v_add_u32_e64 v230, s5, v229\n" \
"v_and_b32_e32 v230, 0xf0, v230\n" \
"v_mfma_f32_16x16x32_bf16 v[164:167], a[156:159], a[176:179], v[164:167]\n" \
"v_cmp_gt_u32_e32 vcc, 16, v227\n" \
"s_nop 1\n" \
"v_cndmask_b32_e64 v227, v229, v230, vcc\n" \
"v_mfma_f32_16x16x32_bf16 v[164:167], a[220:223], a[240:243], v[164:167]\n" \
"ds_read_b128 a[156:159], v28 offset:6144\n" \
"ds_read_b128 a[220:223], v29 offset:6144\n" \
"s_waitcnt lgkmcnt(15)\n" \
"v_mfma_f32_16x16x32_bf16 a[0:3], a[128:131], a[160:163], a[0:3]\n" \
"s_addk_i32 s84, 0x7c00\n" \
"v_mfma_f32_16x16x32_bf16 a[0:3], a[192:195], a[224:227], a[0:3]\n" \
"buffer_load_dwordx4 v27, s[72:75], s84 offen offset:2048 lds\n" \
"v_mfma_f32_16x16x32_bf16 a[4:7], a[128:131], a[164:167], a[4:7]\n" \
"ds_read_b64_tr_b16 a[176:177], v36 offset:0\n" \
"v_or_b32_e32 v229, 0x1c0, v212\n" \
"s_waitcnt lgkmcnt(15)\n" \
"v_mfma_f32_16x16x32_bf16 a[4:7], a[192:195], a[228:231], a[4:7]\n" \
"s_addk_i32 s84, 0x7c00\n" \
"s_waitcnt lgkmcnt(11)\n" \
"v_mfma_f32_16x16x32_bf16 a[8:11], a[128:131], a[168:171], a[8:11]\n" \
"buffer_load_dwordx4 v27, s[72:75], s84 offen offset:3072 lds\n" \
"s_waitcnt lgkmcnt(9)\n" \
"v_mfma_f32_16x16x32_bf16 a[8:11], a[192:195], a[232:235], a[8:11]\n" \
"ds_read_b64_tr_b16 a[178:179], v36 offset:1024\n" \
"v_mul_u32_u24_e32 v230, 0xe39, v229\n" \
"s_waitcnt lgkmcnt(8)\n" \
"v_mfma_f32_16x16x32_bf16 a[12:15], a[128:131], a[172:175], a[12:15]\n" \
"ds_read_b64_tr_b16 a[240:241], v36 offset:8192\n" \
"v_lshrrev_b32_e32 v230, 16, v230\n" \
"s_waitcnt lgkmcnt(5)\n" \
"v_mfma_f32_16x16x32_bf16 a[12:15], a[192:195], a[236:239], a[12:15]\n" \
"v_mad_i32_i24 v229, v230, s6, v229\n" \
"v_lshlrev_b32_e32 v231, 4, v229\n" \
"v_mfma_f32_16x16x32_bf16 a[28:31], a[132:135], a[172:175], a[28:31]\n" \
"v_add_u32_e64 v232, s5, v231\n" \
"v_and_b32_e32 v232, 0xf0, v232\n" \
"v_mfma_f32_16x16x32_bf16 a[28:31], a[196:199], a[236:239], a[28:31]\n" \
"ds_read_b64_tr_b16 a[242:243], v36 offset:9216\n" \
"v_cmp_gt_u32_e64 vcc, 16, v229\n" \
"v_mfma_f32_16x16x32_bf16 a[24:27], a[132:135], a[168:171], a[24:27]\n" \
"buffer_load_dwordx4 v[200:203], v192, s[76:79], s34 offen offset:0\n" \
"v_mfma_f32_16x16x32_bf16 a[24:27], a[196:199], a[232:235], a[24:27]\n" \
"v_cndmask_b32_e64 v229, v231, v232, vcc\n" \
"v_or_b32_e32 v231, 0x200, v212\n" \
"v_mfma_f32_16x16x32_bf16 a[20:23], a[132:135], a[164:167], a[20:23]\n" \
"v_mul_u32_u24_e32 v232, 0xe39, v231\n" \
"v_lshrrev_b32_e64 v232, 16, v232\n" \
"v_mfma_f32_16x16x32_bf16 a[20:23], a[196:199], a[228:231], a[20:23]\n" \
"ds_read_b64_tr_b16 a[180:181], v37 offset:0\n" \
"v_mad_i32_i24 v231, v232, s6, v231\n" \
"v_mfma_f32_16x16x32_bf16 a[16:19], a[132:135], a[160:163], a[16:19]\n" \
"v_lshlrev_b32_e32 v233, 4, v231\n" \
"v_add_u32_e32 v234, s5, v233\n" \
"v_mfma_f32_16x16x32_bf16 a[16:19], a[196:199], a[224:227], a[16:19]\n" \
"s_movk_i32 s5, 0x480\n" \
"v_mad_u32_u24 v0, v213, s5, v214\n" \
"v_mfma_f32_16x16x32_bf16 a[32:35], a[136:139], a[160:163], a[32:35]\n" \
"buffer_load_dwordx4 v[204:207], v193, s[76:79], s34 offen offset:0\n" \
"v_mfma_f32_16x16x32_bf16 a[32:35], a[200:203], a[224:227], a[32:35]\n" \
"s_mov_b32 m0, s32\n" \
"ds_read_b64_tr_b16 a[182:183], v37 offset:1024\n" \
"v_mfma_f32_16x16x32_bf16 a[36:39], a[136:139], a[164:167], a[36:39]\n" \
"s_mov_b32 exec_lo, 0x0\n" \
"s_mov_b32 exec_hi, 0xff00\n" \
"global_load_lds_dword v[184:185], off\n" \
"s_mov_b64 exec, -1\n" \
"v_mfma_f32_16x16x32_bf16 a[36:39], a[200:203], a[228:231], a[36:39]\n" \
"ds_read_b64_tr_b16 a[244:245], v37 offset:8192\n" \
"v_mul_u32_u24_e32 v213, 0x480, v216\n" \
"v_mfma_f32_16x16x32_bf16 a[40:43], a[136:139], a[168:171], a[40:43]\n" \
"s_movk_i32 s6, 0xfc00\n" \
"s_nop 0\n" \
"v_add3_u32 v1, v213, v215, s6\n" \
"v_mfma_f32_16x16x32_bf16 a[40:43], a[200:203], a[232:235], a[40:43]\n" \
"buffer_load_dword v208, v194, s[76:79], s34 offen offset:32\n" \
"v_mfma_f32_16x16x32_bf16 a[44:47], a[136:139], a[172:175], a[44:47]\n" \
"ds_read_b64_tr_b16 a[246:247], v37 offset:9216\n" \
"v_mul_u32_u24_e32 v213, 0x480, v220\n" \
"v_mfma_f32_16x16x32_bf16 a[44:47], a[200:203], a[236:239], a[44:47]\n" \
"v_add3_u32 v2, v213, v217, s7\n" \
"v_mul_u32_u24_e32 v213, 0x480, v222\n" \
"v_mfma_f32_16x16x32_bf16 a[60:63], a[140:143], a[172:175], a[60:63]\n" \
"v_add3_u32 v3, v213, v221, s40\n" \
"v_mul_u32_u24_e32 v213, 0x480, v226\n" \
"v_mfma_f32_16x16x32_bf16 a[60:63], a[204:207], a[236:239], a[60:63]\n" \
"v_add3_u32 v5, v213, v225, s6\n" \
"v_mul_u32_u24_e32 v213, 0x480, v228\n" \
"v_mfma_f32_16x16x32_bf16 a[56:59], a[140:143], a[168:171], a[56:59]\n" \
"buffer_load_dword v209, v194, s[76:79], s34 offen offset:1184\n" \
"v_mfma_f32_16x16x32_bf16 a[56:59], a[204:207], a[232:235], a[56:59]\n" \
"ds_read_b64_tr_b16 a[184:185], v38 offset:0\n" \
"v_add3_u32 v6, v213, v227, s7\n" \
"v_mfma_f32_16x16x32_bf16 a[52:55], a[140:143], a[164:167], a[52:55]\n" \
"v_mul_u32_u24_e32 v213, 0x480, v230\n" \
"v_add3_u32 v7, v213, v229, s40\n" \
"v_mfma_f32_16x16x32_bf16 a[52:55], a[204:207], a[228:231], a[52:55]\n" \
"ds_read_b64_tr_b16 a[186:187], v38 offset:1024\n" \
"s_lshl_b32 s7, s3, 3\n" \
"v_mfma_f32_16x16x32_bf16 a[48:51], a[140:143], a[160:163], a[48:51]\n" \
"s_lshl_b32 s40, s3, 1\n" \
"s_and_b32 s7, s7, 16\n" \
"v_mfma_f32_16x16x32_bf16 a[48:51], a[204:207], a[224:227], a[48:51]\n" \
"s_and_b32 s40, s40, 2\n" \
"s_and_b32 s6, s2, 7\n" \
"v_mfma_f32_16x16x32_bf16 a[64:67], a[144:147], a[160:163], a[64:67]\n" \
"buffer_load_dword v211, v194, s[76:79], s34 offen offset:2336\n" \
"v_mfma_f32_16x16x32_bf16 a[64:67], a[208:211], a[224:227], a[64:67]\n" \
"ds_read_b64_tr_b16 a[248:249], v38 offset:8192\n" \
"v_bfe_u32 v213, v20, 7, 1\n" \
"v_mfma_f32_16x16x32_bf16 a[68:71], a[144:147], a[164:167], a[68:71]\n" \
"s_or_b32 s7, s40, s7\n" \
"v_and_b32_e32 v234, 0xf0, v234\n" \
"v_mfma_f32_16x16x32_bf16 a[68:71], a[208:211], a[228:231], a[68:71]\n" \
"ds_read_b64_tr_b16 a[250:251], v38 offset:9216\n" \
"v_cmp_gt_u32_e64 vcc, 16, v231\n" \
"v_mfma_f32_16x16x32_bf16 a[72:75], a[144:147], a[168:171], a[72:75]\n" \
"v_or3_b32 v213, s7, v213, v219\n" \
"v_and_b32_e64 v214, 32, v20\n" \
"v_mfma_f32_16x16x32_bf16 a[72:75], a[208:211], a[232:235], a[72:75]\n" \
"s_mov_b32 s76, s20\n" \
"s_and_b32 s77, s21, 0xffff\n" \
"v_mfma_f32_16x16x32_bf16 a[76:79], a[144:147], a[172:175], a[76:79]\n" \
"s_mov_b32 s78, 0x480\n" \
"s_lshl_b32 s6, s6, 5\n" \
"v_mfma_f32_16x16x32_bf16 a[76:79], a[208:211], a[236:239], a[76:79]\n" \
"buffer_load_ushort v210, v195, s[76:79], 0 offen\n" \
"v_mfma_f32_16x16x32_bf16 a[92:95], a[148:151], a[172:175], a[92:95]\n" \
"ds_read_b64_tr_b16 a[188:189], v39 offset:0\n" \
"v_cndmask_b32_e64 v231, v233, v234, vcc\n" \
"v_mfma_f32_16x16x32_bf16 a[92:95], a[212:215], a[236:239], a[92:95]\n" \
"v_mul_u32_u24_e64 v213, 18, v213\n" \
"v_mov_b32_e32 v215, 0x100\n" \
"v_mfma_f32_16x16x32_bf16 a[88:91], a[148:151], a[168:171], a[88:91]\n" \
"s_mov_b32 s76, s8\n" \
"s_and_b32 s77, s9, 0xffff\n" \
"v_mfma_f32_16x16x32_bf16 a[88:91], a[212:215], a[232:235], a[88:91]\n" \
"s_mov_b32 s78, 0x240000\n" \
"ds_read_b64_tr_b16 a[190:191], v39 offset:1024\n" \
".fill (L_pb + 48864 - .) / 4, 4, 0xBF800000\n" \
"L_pp12:\n" \
"s_cbranch_execz L_pret\n" \
"v_mfma_f32_16x16x32_bf16 a[84:87], a[148:151], a[164:167], a[84:87]\n" \
"v_mov_b32_e32 v216, s6\n" \
"v_cmp_eq_u32_e64 vcc, 0, v214\n" \
"v_mfma_f32_16x16x32_bf16 a[84:87], a[212:215], a[228:231], a[84:87]\n" \
"ds_read_b64_tr_b16 a[252:253], v39 offset:8192\n" \
"v_and_or_b32 v213, v218, 1, v213\n" \
"v_mfma_f32_16x16x32_bf16 a[80:83], a[148:151], a[160:163], a[80:83]\n" \
"v_lshlrev_b32_e32 v212, 3, v212\n" \
"v_cndmask_b32_e32 v214, v215, v216, vcc\n" \
"v_mfma_f32_16x16x32_bf16 a[80:83], a[212:215], a[224:227], a[80:83]\n" \
"v_mad_u32_u24 v213, v213, s5, v214\n" \
"v_and_or_b32 v213, v20, 16, v213\n" \
"v_mfma_f32_16x16x32_bf16 a[96:99], a[152:155], a[160:163], a[96:99]\n" \
"ds_read_b64_tr_b16 a[254:255], v39 offset:9216\n" \
"v_add_u32_e32 v9, 0x4800, v213\n" \
"v_mfma_f32_16x16x32_bf16 a[96:99], a[216:219], a[224:227], a[96:99]\n" \
"v_mul_u32_u24_e32 v213, 0x120, v218\n" \
"v_and_or_b32 v10, v212, 24, v213\n" \
"v_mfma_f32_16x16x32_bf16 a[100:103], a[152:155], a[164:167], a[100:103]\n" \
"v_lshrrev_b32_e64 v213, 2, v20\n" \
"v_and_or_b32 v213, v213, 60, s4\n" \
"v_mfma_f32_16x16x32_bf16 a[100:103], a[216:219], a[228:231], a[100:103]\n" \
"v_lshl_or_b32 v212, s3, 9, v212\n" \
"v_mad_u32_u24 v4, v224, s5, v223\n" \
"v_mfma_f32_16x16x32_bf16 a[104:107], a[152:155], a[168:171], a[104:107]\n" \
"v_mad_u32_u24 v8, v232, s5, v231\n" \
"v_or_b32_e32 v18, 0x27000, v213\n" \
"v_mfma_f32_16x16x32_bf16 a[104:107], a[216:219], a[232:235], a[104:107]\n" \
"v_or_b32_e32 v19, 0x27800, v212\n" \
"v_mov_b32_e32 v215, 0x1000c0c\n" \
"v_mfma_f32_16x16x32_bf16 a[108:111], a[152:155], a[172:175], a[108:111]\n" \
"v_mov_b32_e32 v216, 0xc0c0100\n" \
"v_bfe_u32 v214, v20, 4, 1\n" \
"v_mfma_f32_16x16x32_bf16 a[108:111], a[216:219], a[236:239], a[108:111]\n" \
"v_lshrrev_b32_e32 v212, 8, v20\n" \
"v_cmp_eq_u32_e32 vcc, 0, v214\n" \
"s_waitcnt lgkmcnt(15)\n" \
"v_mfma_f32_16x16x32_bf16 a[124:127], a[156:159], a[172:175], a[124:127]\n" \
"v_bfe_u32 v213, v20, 4, 4\n" \
"v_mov_b32_e32 v217, 0xc0c0302\n" \
"v_mfma_f32_16x16x32_bf16 a[124:127], a[220:223], a[236:239], a[124:127]\n" \
"v_cndmask_b32_e32 v215, v215, v216, vcc\n" \
"v_mov_b32_e32 v216, 0x3020c0c\n" \
"v_mfma_f32_16x16x32_bf16 a[120:123], a[156:159], a[168:171], a[120:123]\n" \
"v_lshl_or_b32 v214, v212, 2, v214\n" \
"v_cndmask_b32_e64 v216, v216, v217, vcc\n" \
"v_mfma_f32_16x16x32_bf16 a[120:123], a[220:223], a[232:235], a[120:123]\n" \
"v_mov_b32_e32 v217, 0xc0c0c0c\n" \
"v_cmp_gt_u32_e64 vcc, v214, v213\n" \
"v_mfma_f32_16x16x32_bf16 a[116:119], a[156:159], a[164:167], a[116:119]\n" \
"v_or_b32_e32 v214, 2, v214\n" \
"s_lshr_b32 s4, s2, 5\n" \
"v_mfma_f32_16x16x32_bf16 a[116:119], a[220:223], a[228:231], a[116:119]\n" \
"v_cndmask_b32_e32 v40, v217, v215, vcc\n" \
"v_cndmask_b32_e32 v42, v215, v217, vcc\n" \
"v_mfma_f32_16x16x32_bf16 a[112:115], a[156:159], a[160:163], a[112:115]\n" \
"v_cmp_gt_u32_e32 vcc, v214, v213\n" \
"v_and_b32_e32 v214, 16, v20\n" \
"v_mfma_f32_16x16x32_bf16 a[112:115], a[220:223], a[224:227], a[112:115]\n" \
"s_waitcnt vmcnt(7) lgkmcnt(0)\n" \
"s_barrier\n" \
"v_mfma_f32_16x16x32_bf16 v[52:55], a[128:131], a[176:179], v[52:55]\n" \
"s_add_u32 m0, s30, 0x0\n" \
"s_movk_i32 s84, 0x200\n" \
"v_mfma_f32_16x16x32_bf16 v[52:55], a[192:195], a[240:243], v[52:55]\n" \
"buffer_load_dwordx4 v22, s[64:67], s84 offen offset:0 lds\n" \
"v_mfma_f32_16x16x32_bf16 v[56:59], a[128:131], a[180:183], v[56:59]\n" \
"ds_read_b64_tr_b16 a[160:161], v32 offset:32768\n" \
"s_movk_i32 s42, 0x3f80\n" \
"v_mfma_f32_16x16x32_bf16 v[56:59], a[192:195], a[244:247], v[56:59]\n" \
"s_movk_i32 s84, 0x1e00\n" \
"v_mfma_f32_16x16x32_bf16 v[60:63], a[128:131], a[184:187], v[60:63]\n" \
"buffer_load_dwordx4 v23, s[64:67], s84 offen offset:1024 lds\n" \
"v_mfma_f32_16x16x32_bf16 v[60:63], a[192:195], a[248:251], v[60:63]\n" \
"ds_read_b64_tr_b16 a[162:163], v32 offset:33792\n" \
"s_xor_b32 s5, s4, 15\n" \
"v_mfma_f32_16x16x32_bf16 v[64:67], a[128:131], a[188:191], v[64:67]\n" \
"ds_read_b64_tr_b16 a[224:225], v32 offset:40960\n" \
"v_mfma_f32_16x16x32_bf16 v[64:67], a[192:195], a[252:255], v[64:67]\n" \
"ds_read_b128 a[128:131], v28 offset:49152\n" \
"v_mfma_f32_16x16x32_bf16 v[80:83], a[132:135], a[188:191], v[80:83]\n" \
"ds_read_b128 a[192:195], v29 offset:49152\n" \
"v_mfma_f32_16x16x32_bf16 v[80:83], a[196:199], a[252:255], v[80:83]\n" \
"s_movk_i32 s84, 0x3a00\n" \
"v_mfma_f32_16x16x32_bf16 v[76:79], a[132:135], a[184:187], v[76:79]\n" \
"buffer_load_dwordx4 v22, s[64:67], s84 offen offset:2048 lds\n" \
"v_mfma_f32_16x16x32_bf16 v[76:79], a[196:199], a[248:251], v[76:79]\n" \
"s_movk_i32 s84, 0x5600\n" \
"v_mfma_f32_16x16x32_bf16 v[72:75], a[132:135], a[180:183], v[72:75]\n" \
"buffer_load_dwordx4 v23, s[64:67], s84 offen offset:3072 lds\n" \
"v_mfma_f32_16x16x32_bf16 v[72:75], a[196:199], a[244:247], v[72:75]\n" \
"s_add_u32 m0, s31, 0x0\n" \
"s_mov_b32 s84, 0x200000\n" \
"v_mfma_f32_16x16x32_bf16 v[68:71], a[132:135], a[176:179], v[68:71]\n" \
"buffer_load_dwordx4 v26, s[68:71], s84 offen offset:0 lds\n" \
"v_mfma_f32_16x16x32_bf16 v[68:71], a[196:199], a[240:243], v[68:71]\n" \
"ds_read_b128 a[132:135], v28 offset:51200\n" \
"v_mfma_f32_16x16x32_bf16 v[84:87], a[136:139], a[176:179], v[84:87]\n" \
"ds_read_b128 a[196:199], v29 offset:51200\n" \
"v_mfma_f32_16x16x32_bf16 v[84:87], a[200:203], a[240:243], v[84:87]\n" \
"s_addk_i32 s84, 0x7c00\n" \
"v_mfma_f32_16x16x32_bf16 v[88:91], a[136:139], a[180:183], v[88:91]\n" \
"buffer_load_dwordx4 v26, s[68:71], s84 offen offset:1024 lds\n" \
"v_mfma_f32_16x16x32_bf16 v[88:91], a[200:203], a[244:247], v[88:91]\n" \
"ds_read_b64_tr_b16 a[226:227], v32 offset:41984\n" \
"v_mfma_f32_16x16x32_bf16 v[92:95], a[136:139], a[184:187], v[92:95]\n" \
"s_addk_i32 s84, 0x7c00\n" \
"v_mfma_f32_16x16x32_bf16 v[92:95], a[200:203], a[248:251], v[92:95]\n" \
"buffer_load_dwordx4 v27, s[68:71], s84 offen offset:2048 lds\n" \
"v_mfma_f32_16x16x32_bf16 v[96:99], a[136:139], a[188:191], v[96:99]\n" \
"ds_read_b64_tr_b16 a[164:165], v33 offset:32768\n" \
"v_mfma_f32_16x16x32_bf16 v[96:99], a[200:203], a[252:255], v[96:99]\n" \
"ds_read_b128 a[136:139], v28 offset:53248\n" \
"v_mfma_f32_16x16x32_bf16 v[112:115], a[140:143], a[188:191], v[112:115]\n" \
"ds_read_b128 a[200:203], v29 offset:53248\n" \
"v_mfma_f32_16x16x32_bf16 v[112:115], a[204:207], a[252:255], v[112:115]\n" \
"s_addk_i32 s84, 0x7c00\n" \
"s_nop 0\n" \
"v_mfma_f32_16x16x32_bf16 v[108:111], a[140:143], a[184:187], v[108:111]\n" \
"buffer_load_dwordx4 v27, s[68:71], s84 offen offset:3072 lds\n" \
"v_mfma_f32_16x16x32_bf16 v[108:111], a[204:207], a[248:251], v[108:111]\n" \
"s_add_u32 m0, s30, 0x1000\n" \
"s_mov_b32 s84, 0x8200\n" \
"v_mfma_f32_16x16x32_bf16 v[104:107], a[140:143], a[180:183], v[104:107]\n" \
"buffer_load_dwordx4 v22, s[64:67], s84 offen offset:0 lds\n" \
"v_mfma_f32_16x16x32_bf16 v[104:107], a[204:207], a[244:247], v[104:107]\n" \
"ds_read_b64_tr_b16 a[166:167], v33 offset:33792\n" \
"v_mfma_f32_16x16x32_bf16 v[100:103], a[140:143], a[176:179], v[100:103]\n" \
"ds_read_b64_tr_b16 a[228:229], v33 offset:40960\n" \
"v_mfma_f32_16x16x32_bf16 v[100:103], a[204:207], a[240:243], v[100:103]\n" \
"ds_read_b128 a[140:143], v28 offset:55296\n" \
"v_mfma_f32_16x16x32_bf16 v[116:119], a[144:147], a[176:179], v[116:119]\n" \
"ds_read_b128 a[204:207], v29 offset:55296\n" \
"v_mfma_f32_16x16x32_bf16 v[116:119], a[208:211], a[240:243], v[116:119]\n" \
"s_addk_i32 s84, 0x1c00\n" \
"v_mfma_f32_16x16x32_bf16 v[120:123], a[144:147], a[180:183], v[120:123]\n" \
"buffer_load_dwordx4 v23, s[64:67], s84 offen offset:1024 lds\n" \
"v_mfma_f32_16x16x32_bf16 v[120:123], a[208:211], a[244:247], v[120:123]\n" \
"s_addk_i32 s84, 0x1c00\n" \
"v_mfma_f32_16x16x32_bf16 v[124:127], a[144:147], a[184:187], v[124:127]\n" \
"buffer_load_dwordx4 v22, s[64:67], s84 offen offset:2048 lds\n" \
"v_mfma_f32_16x16x32_bf16 v[124:127], a[208:211], a[248:251], v[124:127]\n" \
"s_addk_i32 s84, 0x1c00\n" \
"s_nop 0\n" \
"v_mfma_f32_16x16x32_bf16 v[128:131], a[144:147], a[188:191], v[128:131]\n" \
"buffer_load_dwordx4 v23, s[64:67], s84 offen offset:3072 lds\n" \
"v_mfma_f32_16x16x32_bf16 v[128:131], a[208:211], a[252:255], v[128:131]\n" \
"ds_read_b128 a[144:147], v28 offset:32768\n" \
"v_mfma_f32_16x16x32_bf16 v[144:147], a[148:151], a[188:191], v[144:147]\n" \
"ds_read_b128 a[208:211], v29 offset:32768\n" \
"v_mfma_f32_16x16x32_bf16 v[144:147], a[212:215], a[252:255], v[144:147]\n" \
"s_add_u32 m0, s31, 0x4000\n" \
"s_mov_b32 s84, 0x200000\n" \
"v_mfma_f32_16x16x32_bf16 v[140:143], a[148:151], a[184:187], v[140:143]\n" \
"buffer_load_dwordx4 v26, s[72:75], s84 offen offset:0 lds\n" \
"v_mfma_f32_16x16x32_bf16 v[140:143], a[212:215], a[248:251], v[140:143]\n" \
"ds_read_b64_tr_b16 a[230:231], v33 offset:41984\n" \
"v_mfma_f32_16x16x32_bf16 v[136:139], a[148:151], a[180:183], v[136:139]\n" \
"s_addk_i32 s84, 0x7c00\n" \
"s_nop 0\n" \
"v_mfma_f32_16x16x32_bf16 v[136:139], a[212:215], a[244:247], v[136:139]\n" \
"buffer_load_dwordx4 v26, s[72:75], s84 offen offset:1024 lds\n" \
"v_mfma_f32_16x16x32_bf16 v[132:135], a[148:151], a[176:179], v[132:135]\n" \
"ds_read_b64_tr_b16 a[168:169], v34 offset:32768\n" \
"v_mfma_f32_16x16x32_bf16 v[132:135], a[212:215], a[240:243], v[132:135]\n" \
"ds_read_b128 a[148:151], v28 offset:34816\n" \
"v_mfma_f32_16x16x32_bf16 v[148:151], a[152:155], a[176:179], v[148:151]\n" \
"ds_read_b128 a[212:215], v29 offset:34816\n" \
"v_mfma_f32_16x16x32_bf16 v[148:151], a[216:219], a[240:243], v[148:151]\n" \
"ds_read_b64_tr_b16 a[170:171], v34 offset:33792\n" \
"v_mfma_f32_16x16x32_bf16 v[152:155], a[152:155], a[180:183], v[152:155]\n" \
"ds_read_b64_tr_b16 a[232:233], v34 offset:40960\n" \
"v_mfma_f32_16x16x32_bf16 v[152:155], a[216:219], a[244:247], v[152:155]\n" \
"ds_read_b64_tr_b16 a[234:235], v34 offset:41984\n" \
"v_mfma_f32_16x16x32_bf16 v[156:159], a[152:155], a[184:187], v[156:159]\n" \
"ds_read_b64_tr_b16 a[172:173], v35 offset:32768\n" \
"v_mfma_f32_16x16x32_bf16 v[156:159], a[216:219], a[248:251], v[156:159]\n" \
"ds_read_b64_tr_b16 a[174:175], v35 offset:33792\n" \
"v_mfma_f32_16x16x32_bf16 v[160:163], a[152:155], a[188:191], v[160:163]\n" \
"ds_read_b64_tr_b16 a[236:237], v35 offset:40960\n" \
"v_mfma_f32_16x16x32_bf16 v[160:163], a[216:219], a[252:255], v[160:163]\n" \
"ds_read_b128 a[152:155], v28 offset:36864\n" \
"v_mfma_f32_16x16x32_bf16 v[176:179], a[156:159], a[188:191], v[176:179]\n" \
"ds_read_b128 a[216:219], v29 offset:36864\n" \
"v_mfma_f32_16x16x32_bf16 v[176:179], a[220:223], a[252:255], v[176:179]\n" \
"ds_read_b64_tr_b16 a[238:239], v35 offset:41984\n" \
"v_mfma_f32_16x16x32_bf16 v[172:175], a[156:159], a[184:187], v[172:175]\n" \
"s_cmpk_lt_u32 s3, 2\n" \
"s_cselect_b32 s6, s5, s4\n" \
"v_mfma_f32_16x16x32_bf16 v[172:175], a[220:223], a[248:251], v[172:175]\n" \
"s_lshl_b32 s7, s6, 7\n" \
"v_and_b32_e32 v213, 0x320, v20\n" \
"v_mfma_f32_16x16x32_bf16 v[168:171], a[156:159], a[180:183], v[168:171]\n" \
"s_movk_i32 s41, 0x100\n" \
"v_lshlrev_b32_e64 v214, v214, s42\n" \
"v_mfma_f32_16x16x32_bf16 v[168:171], a[220:223], a[244:247], v[168:171]\n" \
"v_mov_b32_e32 v215, 0\n" \
"s_add_i32 s40, s7, -16\n" \
"v_mfma_f32_16x16x32_bf16 v[164:167], a[156:159], a[176:179], v[164:167]\n" \
"v_cndmask_b32_e32 v41, v217, v216, vcc\n" \
"v_cndmask_b32_e32 v43, v216, v217, vcc\n" \
"v_mfma_f32_16x16x32_bf16 v[164:167], a[220:223], a[240:243], v[164:167]\n" \
"ds_read_b128 a[156:159], v28 offset:38912\n" \
"ds_read_b128 a[220:223], v29 offset:38912\n" \
"s_waitcnt lgkmcnt(15)\n" \
"v_mfma_f32_16x16x32_bf16 a[0:3], a[128:131], a[160:163], a[0:3]\n" \
"s_addk_i32 s84, 0x7c00\n" \
"v_mfma_f32_16x16x32_bf16 a[0:3], a[192:195], a[224:227], a[0:3]\n" \
"buffer_load_dwordx4 v27, s[72:75], s84 offen offset:2048 lds\n" \
"v_mfma_f32_16x16x32_bf16 a[4:7], a[128:131], a[164:167], a[4:7]\n" \
"ds_read_b64_tr_b16 a[176:177], v36 offset:32768\n" \
"v_cmp_eq_u32_e32 vcc, s41, v213\n" \
"s_waitcnt lgkmcnt(15)\n" \
"v_mfma_f32_16x16x32_bf16 a[4:7], a[192:195], a[228:231], a[4:7]\n" \
"s_addk_i32 s84, 0x7c00\n" \
"s_waitcnt lgkmcnt(11)\n" \
"v_mfma_f32_16x16x32_bf16 a[8:11], a[128:131], a[168:171], a[8:11]\n" \
"buffer_load_dwordx4 v27, s[72:75], s84 offen offset:3072 lds\n" \
"s_waitcnt lgkmcnt(9)\n" \
"v_mfma_f32_16x16x32_bf16 a[8:11], a[192:195], a[232:235], a[8:11]\n" \
"ds_read_b64_tr_b16 a[178:179], v36 offset:33792\n" \
"s_mov_b32 s41, 0\n" \
"s_waitcnt lgkmcnt(8)\n" \
"v_mfma_f32_16x16x32_bf16 a[12:15], a[128:131], a[172:175], a[12:15]\n" \
"ds_read_b64_tr_b16 a[240:241], v36 offset:40960\n" \
"s_and_b32 s43, s3, 1\n" \
"s_waitcnt lgkmcnt(5)\n" \
"v_mfma_f32_16x16x32_bf16 a[12:15], a[192:195], a[236:239], a[12:15]\n" \
"v_cndmask_b32_e32 v46, v215, v214, vcc\n" \
"v_and_b32_e32 v213, 0x330, v20\n" \
"v_mfma_f32_16x16x32_bf16 a[28:31], a[132:135], a[172:175], a[28:31]\n" \
"s_cmp_lg_u32 s7, 0\n" \
"s_cselect_b32 s40, s40, s41\n" \
"v_mfma_f32_16x16x32_bf16 a[28:31], a[196:199], a[236:239], a[28:31]\n" \
"ds_read_b64_tr_b16 a[242:243], v36 offset:41984\n" \
"s_mov_b32 s42, 0xff800000\n" \
"v_mfma_f32_16x16x32_bf16 a[24:27], a[132:135], a[168:171], a[24:27]\n" \
"s_cmp_lg_u32 s7, 0\n" \
"s_cselect_b32 s47, s41, s42\n" \
"v_mfma_f32_16x16x32_bf16 a[24:27], a[196:199], a[232:235], a[24:27]\n" \
"s_lshl_b32 s41, s43, 6\n" \
"v_lshrrev_b32_e32 v215, 6, v20\n" \
"v_mfma_f32_16x16x32_bf16 a[20:23], a[132:135], a[164:167], a[20:23]\n" \
"v_mov_b32_e32 v214, 0x3f80\n" \
"v_cmp_eq_u32_e64 vcc, 32, v213\n" \
"v_mfma_f32_16x16x32_bf16 a[20:23], a[196:199], a[228:231], a[20:23]\n" \
"ds_read_b64_tr_b16 a[180:181], v37 offset:32768\n" \
"s_or_b32 s7, s41, s7\n" \
"v_mfma_f32_16x16x32_bf16 a[16:19], a[132:135], a[160:163], a[16:19]\n" \
"v_lshrrev_b32_e32 v213, 7, v20\n" \
"v_bfe_u32 v216, v20, 6, 2\n" \
"v_mfma_f32_16x16x32_bf16 a[16:19], a[196:199], a[224:227], a[16:19]\n" \
"v_lshrrev_b32_e64 v219, 1, v20\n" \
"v_bfe_u32 v215, v215, 1, 1\n" \
"v_mfma_f32_16x16x32_bf16 a[32:35], a[136:139], a[160:163], a[32:35]\n" \
"s_waitcnt vmcnt(16)\n" \
"v_or_b32_e32 v204, v210, v204\n" \
"v_or_b32_e64 v206, v210, v206\n" \
"v_mfma_f32_16x16x32_bf16 a[32:35], a[200:203], a[224:227], a[32:35]\n" \
"v_mov_b32_e64 v210, 0\n" \
"s_add_u32 s88, s35, 0x1000\n" \
"v_mfma_f32_16x16x32_bf16 a[36:39], a[136:139], a[164:167], a[36:39]\n" \
"ds_read_b64_tr_b16 a[182:183], v37 offset:33792\n" \
"v_cndmask_b32_e64 v47, 0, v214, vcc\n" \
"v_mfma_f32_16x16x32_bf16 a[36:39], a[200:203], a[228:231], a[36:39]\n" \
"ds_read_b64_tr_b16 a[244:245], v37 offset:40960\n" \
"v_or_b32_e64 v214, s7, v213\n" \
"v_mfma_f32_16x16x32_bf16 a[40:43], a[136:139], a[168:171], a[40:43]\n" \
"s_mov_b32 m0, s32\n" \
"v_lshl_or_b32 v217, s43, 3, v213\n" \
"v_mfma_f32_16x16x32_bf16 a[40:43], a[200:203], a[232:235], a[40:43]\n" \
"s_mov_b32 exec_lo, 0xffffffff\n" \
"s_mov_b32 exec_hi, 0x0\n" \
"global_load_lds_dword v[188:189], off\n" \
"s_mov_b64 exec, -1\n" \
"v_mfma_f32_16x16x32_bf16 a[44:47], a[136:139], a[172:175], a[44:47]\n" \
"ds_read_b64_tr_b16 a[246:247], v37 offset:41984\n" \
"v_lshlrev_b32_e64 v218, 10, v212\n" \
"v_mfma_f32_16x16x32_bf16 a[44:47], a[200:203], a[236:239], a[44:47]\n" \
"v_lshlrev_b32_e32 v216, 7, v216\n" \
"v_and_b32_e32 v219, 24, v219\n" \
"v_mfma_f32_16x16x32_bf16 a[60:63], a[140:143], a[172:175], a[60:63]\n" \
"ds_write_b128 v196, v[200:203]\n" \
"v_mfma_f32_16x16x32_bf16 a[60:63], a[204:207], a[236:239], a[60:63]\n" \
"v_and_or_b32 v213, v213, 2, v215\n" \
"v_add_u32_e64 v217, s40, v217\n" \
"v_mfma_f32_16x16x32_bf16 a[56:59], a[140:143], a[168:171], a[56:59]\n" \
"ds_read_b64_tr_b16 a[184:185], v38 offset:32768\n" \
"v_or3_b32 v216, v216, v218, v219\n" \
"v_mfma_f32_16x16x32_bf16 a[56:59], a[204:207], a[232:235], a[56:59]\n" \
"v_lshlrev_b32_e64 v213, 5, v213\n" \
"s_mov_b32 s40, 0x14000\n" \
"v_mfma_f32_16x16x32_bf16 a[52:55], a[140:143], a[164:167], a[52:55]\n" \
"s_lshl_b32 s7, s2, 2\n" \
"v_or3_b32 v48, v216, v213, s40\n" \
"v_mfma_f32_16x16x32_bf16 a[52:55], a[204:207], a[228:231], a[52:55]\n" \
"ds_read_b64_tr_b16 a[186:187], v38 offset:33792\n" \
"s_lshl_b32 s40, s43, 2\n" \
"v_mfma_f32_16x16x32_bf16 a[48:51], a[140:143], a[160:163], a[48:51]\n" \
"s_lshl_b32 s6, s6, 3\n" \
"v_or_b32_e32 v213, s40, v212\n" \
"v_mfma_f32_16x16x32_bf16 a[48:51], a[204:207], a[224:227], a[48:51]\n" \
"s_or_b32 s6, s6, s40\n" \
"s_and_b32 s40, s7, 0x7c\n" \
"v_mfma_f32_16x16x32_bf16 a[64:67], a[144:147], a[160:163], a[64:67]\n" \
"ds_write_b128 v197, v[204:207]\n" \
"v_mfma_f32_16x16x32_bf16 a[64:67], a[208:211], a[224:227], a[64:67]\n" \
"ds_read_b64_tr_b16 a[248:249], v38 offset:40960\n" \
"s_or_b32 s40, s40, s3\n" \
"v_mfma_f32_16x16x32_bf16 a[68:71], a[144:147], a[164:167], a[68:71]\n" \
"s_cmpk_lt_u32 s40, 64\n" \
"s_cselect_b32 s41, s4, s5\n" \
"v_mfma_f32_16x16x32_bf16 a[68:71], a[208:211], a[228:231], a[68:71]\n" \
"ds_read_b64_tr_b16 a[250:251], v38 offset:41984\n" \
"s_lshl_b32 s40, s40, 1\n" \
"v_mfma_f32_16x16x32_bf16 a[72:75], a[144:147], a[168:171], a[72:75]\n" \
"s_lshl_b32 s41, s41, 7\n" \
"s_and_b32 s40, s40, 0x7e\n" \
"v_mfma_f32_16x16x32_bf16 a[72:75], a[208:211], a[232:235], a[72:75]\n" \
"s_or_b32 s40, s41, s40\n" \
"s_mulk_i32 s40, 0x480\n" \
"v_mfma_f32_16x16x32_bf16 a[76:79], a[144:147], a[172:175], a[76:79]\n" \
"v_add_u32_e64 v49, s40, v20\n" \
"s_mov_b32 s40, 0xc000\n" \
"v_mfma_f32_16x16x32_bf16 a[76:79], a[208:211], a[236:239], a[76:79]\n" \
"ds_read_b64_tr_b16 a[188:189], v39 offset:32768\n" \
"s_and_b32 s7, s7, 28\n" \
"v_mfma_f32_16x16x32_bf16 a[92:95], a[148:151], a[172:175], a[92:95]\n" \
"ds_write_b128 v198, v[208:211]\n" \
"v_mfma_f32_16x16x32_bf16 a[92:95], a[212:215], a[236:239], a[92:95]\n" \
"s_bfe_u32 s45, s2, 0x20003\n" \
"s_mov_b32 s41, 0x8000\n" \
"v_mfma_f32_16x16x32_bf16 a[88:91], a[148:151], a[168:171], a[88:91]\n" \
"ds_read_b64_tr_b16 a[190:191], v39 offset:33792\n" \
"v_mfma_f32_16x16x32_bf16 a[88:91], a[212:215], a[232:235], a[88:91]\n" \
"s_cmp_lg_u32 s4, 0\n" \
"s_cselect_b32 s51, s40, s41\n" \
"v_mfma_f32_16x16x32_bf16 a[84:87], a[148:151], a[164:167], a[84:87]\n" \
"s_movk_i32 s40, 0x4000\n" \
"s_lshr_b32 s44, s3, 1\n" \
"v_mfma_f32_16x16x32_bf16 a[84:87], a[212:215], a[228:231], a[84:87]\n" \
"ds_read_b64_tr_b16 a[252:253], v39 offset:40960\n" \
"s_or_b32 s46, s7, s45\n" \
"v_mfma_f32_16x16x32_bf16 a[80:83], a[148:151], a[160:163], a[80:83]\n" \
"s_cmp_lg_u32 s4, 0\n" \
"s_cselect_b32 s53, s41, s40\n" \
"v_mfma_f32_16x16x32_bf16 a[80:83], a[212:215], a[224:227], a[80:83]\n" \
"s_lshl_b32 s40, s44, 15\n" \
"s_lshl_b32 s41, s43, 14\n" \
"v_mfma_f32_16x16x32_bf16 a[96:99], a[152:155], a[160:163], a[96:99]\n" \
"ds_read_b64_tr_b16 a[254:255], v39 offset:41984\n" \
"s_lshl_b32 s48, s46, 20\n" \
"v_mfma_f32_16x16x32_bf16 a[96:99], a[216:219], a[224:227], a[96:99]\n" \
"s_or_b32 s41, s40, s41\n" \
"s_add_i32 s49, s48, 0x1000000\n" \
"v_mfma_f32_16x16x32_bf16 a[100:103], a[152:155], a[164:167], a[100:103]\n" \
"s_or_b32 s42, s49, s41\n" \
"s_lshl_b32 s4, s4, 16\n" \
"v_mfma_f32_16x16x32_bf16 a[100:103], a[216:219], a[228:231], a[100:103]\n" \
"s_or_b32 s41, s42, s4\n" \
"s_lshl_b32 s4, s5, 16\n" \
"v_mfma_f32_16x16x32_bf16 a[104:107], a[152:155], a[168:171], a[104:107]\n" \
"s_mul_i32 s5, s6, 0x24800\n" \
"s_lshl_b32 s6, s46, 9\n" \
"v_mfma_f32_16x16x32_bf16 a[104:107], a[216:219], a[232:235], a[104:107]\n" \
"s_lshl_b32 s7, s7, 6\n" \
"s_add_i32 s6, s5, s6\n" \
"v_mfma_f32_16x16x32_bf16 a[108:111], a[152:155], a[172:175], a[108:111]\n" \
"s_or_b32 s5, s5, s7\n" \
"s_lshl_b32 s7, s45, 2\n" \
"v_mfma_f32_16x16x32_bf16 a[108:111], a[216:219], a[236:239], a[108:111]\n" \
"s_or_b32 s5, s5, s7\n" \
"s_lshl_b32 s7, s3, 12\n" \
"s_waitcnt lgkmcnt(15)\n" \
"v_mfma_f32_16x16x32_bf16 a[124:127], a[156:159], a[172:175], a[124:127]\n" \
"s_or_b32 s50, s49, s7\n" \
"s_lshl_b32 s7, s3, 11\n" \
"v_mfma_f32_16x16x32_bf16 a[124:127], a[220:223], a[236:239], a[124:127]\n" \
"v_lshrrev_b32_e64 v215, 4, v20\n" \
"v_bfe_u32 v216, v20, 4, 3\n" \
"v_mfma_f32_16x16x32_bf16 a[120:123], a[156:159], a[168:171], a[120:123]\n" \
"s_or_b32 s42, s42, s4\n" \
"s_lshl_b32 s4, s43, 11\n" \
"v_mfma_f32_16x16x32_bf16 a[120:123], a[220:223], a[232:235], a[120:123]\n" \
"s_add_i32 s52, s48, s7\n" \
"s_add_i32 s53, s53, s7\n" \
"v_mfma_f32_16x16x32_bf16 a[116:119], a[156:159], a[164:167], a[116:119]\n" \
"v_mul_u32_u24_e32 v214, 0x480, v214\n" \
"v_bitop3_b32 v218, v212, v215, 7 bitop3:0x78\n" \
"v_mfma_f32_16x16x32_bf16 a[116:119], a[220:223], a[228:231], a[116:119]\n" \
"v_bitop3_b32 v212, v212, v216, 4 bitop3:0x36\n" \
"s_lshl_b32 s7, s3, 10\n" \
"v_mfma_f32_16x16x32_bf16 a[112:115], a[156:159], a[160:163], a[112:115]\n" \
"s_or_b32 s4, s40, s4\n" \
"v_lshl_or_b32 v25, v212, 4, v214\n" \
"v_mfma_f32_16x16x32_bf16 a[112:115], a[220:223], a[224:227], a[112:115]\n" \
"s_waitcnt vmcnt(1) lgkmcnt(0)\n" \
"s_barrier\n" \
"v_mfma_f32_16x16x32_bf16 v[52:55], a[128:131], a[176:179], v[52:55]\n" \
"s_add_u32 m0, s30, 0x8000\n" \
"s_movk_i32 s84, 0x280\n" \
"v_mfma_f32_16x16x32_bf16 v[52:55], a[192:195], a[240:243], v[52:55]\n" \
"buffer_load_dwordx4 v22, s[64:67], s84 offen offset:0 lds\n" \
"v_mfma_f32_16x16x32_bf16 v[56:59], a[128:131], a[180:183], v[56:59]\n" \
"s_movk_i32 s84, 0x1e80\n" \
"v_mfma_f32_16x16x32_bf16 v[56:59], a[192:195], a[244:247], v[56:59]\n" \
"buffer_load_dwordx4 v23, s[64:67], s84 offen offset:1024 lds\n" \
"v_mfma_f32_16x16x32_bf16 v[60:63], a[128:131], a[184:187], v[60:63]\n" \
"ds_read_b64_tr_b16 a[160:161], v32 offset:0\n" \
"v_mul_u32_u24_e32 v212, 0x480, v217\n" \
"v_mfma_f32_16x16x32_bf16 v[60:63], a[192:195], a[248:251], v[60:63]\n" \
"s_movk_i32 s84, 0x3a80\n" \
"v_mfma_f32_16x16x32_bf16 v[64:67], a[128:131], a[188:191], v[64:67]\n" \
"buffer_load_dwordx4 v22, s[64:67], s84 offen offset:2048 lds\n" \
"v_mfma_f32_16x16x32_bf16 v[64:67], a[192:195], a[252:255], v[64:67]\n" \
"ds_read_b128 a[128:131], v28 offset:16384\n" \
"v_bitop3_b32 v213, v215, v213, 7 bitop3:0x6c\n" \
"v_mfma_f32_16x16x32_bf16 v[80:83], a[132:135], a[188:191], v[80:83]\n" \
"ds_read_b128 a[192:195], v29 offset:16384\n" \
"s_or_b32 s40, s7, 0x14000\n" \
"v_mfma_f32_16x16x32_bf16 v[80:83], a[196:199], a[252:255], v[80:83]\n" \
"s_movk_i32 s84, 0x5680\n" \
"v_mfma_f32_16x16x32_bf16 v[76:79], a[132:135], a[184:187], v[76:79]\n" \
"buffer_load_dwordx4 v23, s[64:67], s84 offen offset:3072 lds\n" \
"v_mfma_f32_16x16x32_bf16 v[76:79], a[196:199], a[248:251], v[76:79]\n" \
"s_add_u32 m0, s31, 0x8000\n" \
"s_mov_b32 s84, 0x280000\n" \
"v_mfma_f32_16x16x32_bf16 v[72:75], a[132:135], a[180:183], v[72:75]\n" \
"buffer_load_dwordx4 v26, s[68:71], s84 offen offset:0 lds\n" \
"v_mfma_f32_16x16x32_bf16 v[72:75], a[196:199], a[244:247], v[72:75]\n" \
"ds_read_b64_tr_b16 a[162:163], v32 offset:1024\n" \
"s_lshl_b32 s43, s44, 13\n" \
"v_mfma_f32_16x16x32_bf16 v[68:71], a[132:135], a[176:179], v[68:71]\n" \
"ds_read_b64_tr_b16 a[224:225], v32 offset:8192\n" \
"s_mulk_i32 s44, 0xe800\n" \
"v_mfma_f32_16x16x32_bf16 v[68:71], a[196:199], a[240:243], v[68:71]\n" \
"ds_read_b128 a[132:135], v28 offset:18432\n" \
"s_lshl_b32 s7, s3, 8\n" \
"v_mfma_f32_16x16x32_bf16 v[84:87], a[136:139], a[176:179], v[84:87]\n" \
"ds_read_b128 a[196:199], v29 offset:18432\n" \
"v_lshl_or_b32 v24, v218, 4, v214\n" \
"v_mfma_f32_16x16x32_bf16 v[84:87], a[200:203], a[240:243], v[84:87]\n" \
"s_addk_i32 s84, 0x7c00\n" \
"v_mfma_f32_16x16x32_bf16 v[88:91], a[136:139], a[180:183], v[88:91]\n" \
"buffer_load_dwordx4 v26, s[68:71], s84 offen offset:1024 lds\n" \
"v_mfma_f32_16x16x32_bf16 v[88:91], a[200:203], a[244:247], v[88:91]\n" \
"s_addk_i32 s84, 0x7c00\n" \
"v_mfma_f32_16x16x32_bf16 v[92:95], a[136:139], a[184:187], v[92:95]\n" \
"buffer_load_dwordx4 v27, s[68:71], s84 offen offset:2048 lds\n" \
"v_mfma_f32_16x16x32_bf16 v[92:95], a[200:203], a[248:251], v[92:95]\n" \
"s_addk_i32 s84, 0x7c00\n" \
"v_mfma_f32_16x16x32_bf16 v[96:99], a[136:139], a[188:191], v[96:99]\n" \
"buffer_load_dwordx4 v27, s[68:71], s84 offen offset:3072 lds\n" \
"v_mfma_f32_16x16x32_bf16 v[96:99], a[200:203], a[252:255], v[96:99]\n" \
"ds_read_b128 a[136:139], v28 offset:20480\n" \
"v_lshl_or_b32 v21, v213, 4, v212\n" \
"v_mfma_f32_16x16x32_bf16 v[112:115], a[140:143], a[188:191], v[112:115]\n" \
"ds_read_b128 a[200:203], v29 offset:20480\n" \
"s_add_i32 s44, s44, 0x17000\n" \
"v_mfma_f32_16x16x32_bf16 v[112:115], a[204:207], a[252:255], v[112:115]\n" \
"s_add_u32 m0, s30, 0x9000\n" \
"s_mov_b32 s84, 0x8280\n" \
"v_mfma_f32_16x16x32_bf16 v[108:111], a[140:143], a[184:187], v[108:111]\n" \
"buffer_load_dwordx4 v22, s[64:67], s84 offen offset:0 lds\n" \
"v_mfma_f32_16x16x32_bf16 v[108:111], a[204:207], a[248:251], v[108:111]\n" \
"s_addk_i32 s84, 0x1c00\n" \
"v_mfma_f32_16x16x32_bf16 v[104:107], a[140:143], a[180:183], v[104:107]\n" \
"buffer_load_dwordx4 v23, s[64:67], s84 offen offset:1024 lds\n" \
"v_mfma_f32_16x16x32_bf16 v[104:107], a[204:207], a[244:247], v[104:107]\n" \
"s_addk_i32 s84, 0x1c00\n" \
"v_mfma_f32_16x16x32_bf16 v[100:103], a[140:143], a[176:179], v[100:103]\n" \
"buffer_load_dwordx4 v22, s[64:67], s84 offen offset:2048 lds\n" \
"v_mfma_f32_16x16x32_bf16 v[100:103], a[204:207], a[240:243], v[100:103]\n" \
"ds_read_b128 a[140:143], v28 offset:22528\n" \
"s_or_b32 s45, s7, 0x27000\n" \
"v_mfma_f32_16x16x32_bf16 v[116:119], a[144:147], a[176:179], v[116:119]\n" \
"ds_read_b128 a[204:207], v29 offset:22528\n" \
"s_or_b32 s46, s4, 0x10000\n" \
"v_mfma_f32_16x16x32_bf16 v[116:119], a[208:211], a[240:243], v[116:119]\n" \
"s_addk_i32 s84, 0x1c00\n" \
"v_mfma_f32_16x16x32_bf16 v[120:123], a[144:147], a[180:183], v[120:123]\n" \
"buffer_load_dwordx4 v23, s[64:67], s84 offen offset:3072 lds\n" \
"v_mfma_f32_16x16x32_bf16 v[120:123], a[208:211], a[244:247], v[120:123]\n" \
"s_add_u32 m0, s31, 0xc000\n" \
"s_mov_b32 s84, 0x280000\n" \
"v_mfma_f32_16x16x32_bf16 v[124:127], a[144:147], a[184:187], v[124:127]\n" \
"buffer_load_dwordx4 v26, s[72:75], s84 offen offset:0 lds\n" \
"v_mfma_f32_16x16x32_bf16 v[124:127], a[208:211], a[248:251], v[124:127]\n" \
"s_addk_i32 s84, 0x7c00\n" \
"v_mfma_f32_16x16x32_bf16 v[128:131], a[144:147], a[188:191], v[128:131]\n" \
"buffer_load_dwordx4 v26, s[72:75], s84 offen offset:1024 lds\n" \
"v_mfma_f32_16x16x32_bf16 v[128:131], a[208:211], a[252:255], v[128:131]\n" \
"ds_read_b128 a[144:147], v28 offset:0\n" \
"s_add_i32 s48, s6, 0x3000000\n" \
"v_mfma_f32_16x16x32_bf16 v[144:147], a[148:151], a[188:191], v[144:147]\n" \
"ds_read_b128 a[208:211], v29 offset:0\n" \
"s_add_i32 s49, s5, 0x3004000\n" \
"v_mfma_f32_16x16x32_bf16 v[144:147], a[212:215], a[252:255], v[144:147]\n" \
"ds_read_b128 v[200:203], v199 offset:0\n" \
"s_add_i32 s52, s52, 0x1010000\n" \
"v_mfma_f32_16x16x32_bf16 v[140:143], a[148:151], a[184:187], v[140:143]\n" \
"ds_read_b128 v[204:207], v199 offset:4096\n" \
"v_mfma_f32_16x16x32_bf16 v[140:143], a[212:215], a[248:251], v[140:143]\n" \
"ds_read_b64_tr_b16 a[226:227], v32 offset:9216\n" \
"v_mfma_f32_16x16x32_bf16 v[136:139], a[148:151], a[180:183], v[136:139]\n" \
"ds_read_b64_tr_b16 a[164:165], v33 offset:0\n" \
"v_mfma_f32_16x16x32_bf16 v[136:139], a[212:215], a[244:247], v[136:139]\n" \
"ds_read_b128 v[208:211], v199 offset:8192\n" \
"v_mfma_f32_16x16x32_bf16 v[132:135], a[148:151], a[176:179], v[132:135]\n" \
"ds_read_b64_tr_b16 a[166:167], v33 offset:1024\n" \
"v_mfma_f32_16x16x32_bf16 v[132:135], a[212:215], a[240:243], v[132:135]\n" \
"ds_read_b128 a[148:151], v28 offset:2048\n" \
"v_mfma_f32_16x16x32_bf16 v[148:151], a[152:155], a[176:179], v[148:151]\n" \
"ds_read_b128 a[212:215], v29 offset:2048\n" \
"v_mfma_f32_16x16x32_bf16 v[148:151], a[216:219], a[240:243], v[148:151]\n" \
"ds_read_b64_tr_b16 a[228:229], v33 offset:8192\n" \
"v_mfma_f32_16x16x32_bf16 v[152:155], a[152:155], a[180:183], v[152:155]\n" \
"ds_read_b64_tr_b16 a[230:231], v33 offset:9216\n" \
"v_mfma_f32_16x16x32_bf16 v[152:155], a[216:219], a[244:247], v[152:155]\n" \
"ds_read_b64_tr_b16 a[168:169], v34 offset:0\n" \
"v_mfma_f32_16x16x32_bf16 v[156:159], a[152:155], a[184:187], v[156:159]\n" \
"ds_read_b64_tr_b16 a[170:171], v34 offset:1024\n" \
"v_mfma_f32_16x16x32_bf16 v[156:159], a[216:219], a[248:251], v[156:159]\n" \
"ds_read_b64_tr_b16 a[232:233], v34 offset:8192\n" \
"v_mfma_f32_16x16x32_bf16 v[160:163], a[152:155], a[188:191], v[160:163]\n" \
"ds_read_b64_tr_b16 a[234:235], v34 offset:9216\n" \
"v_mfma_f32_16x16x32_bf16 v[160:163], a[216:219], a[252:255], v[160:163]\n" \
"ds_read_b128 a[152:155], v28 offset:4096\n" \
"v_mfma_f32_16x16x32_bf16 v[176:179], a[156:159], a[188:191], v[176:179]\n" \
"ds_read_b128 a[216:219], v29 offset:4096\n" \
"v_mfma_f32_16x16x32_bf16 v[176:179], a[220:223], a[252:255], v[176:179]\n" \
"ds_read_b64_tr_b16 a[172:173], v35 offset:0\n" \
"v_mfma_f32_16x16x32_bf16 v[172:175], a[156:159], a[184:187], v[172:175]\n" \
"ds_read_b64_tr_b16 a[174:175], v35 offset:1024\n" \
"v_mfma_f32_16x16x32_bf16 v[172:175], a[220:223], a[248:251], v[172:175]\n" \
"ds_read_b64_tr_b16 a[236:237], v35 offset:8192\n" \
"v_mfma_f32_16x16x32_bf16 v[168:171], a[156:159], a[180:183], v[168:171]\n" \
"ds_read_b64_tr_b16 a[238:239], v35 offset:9216\n" \
"v_mfma_f32_16x16x32_bf16 v[168:171], a[220:223], a[244:247], v[168:171]\n" \
"v_mfma_f32_16x16x32_bf16 v[164:167], a[156:159], a[176:179], v[164:167]\n" \
"v_mfma_f32_16x16x32_bf16 v[164:167], a[220:223], a[240:243], v[164:167]\n" \
"ds_read_b128 a[156:159], v28 offset:6144\n" \
"ds_read_b128 a[220:223], v29 offset:6144\n" \
"s_waitcnt lgkmcnt(15)\n" \
"v_mfma_f32_16x16x32_bf16 a[0:3], a[128:131], a[160:163], a[0:3]\n" \
"s_addk_i32 s84, 0x7c00\n" \
"v_mfma_f32_16x16x32_bf16 a[0:3], a[192:195], a[224:227], a[0:3]\n" \
"buffer_load_dwordx4 v27, s[72:75], s84 offen offset:2048 lds\n" \
"v_mfma_f32_16x16x32_bf16 a[4:7], a[128:131], a[164:167], a[4:7]\n" \
"ds_read_b64_tr_b16 a[176:177], v36 offset:0\n" \
"s_waitcnt lgkmcnt(13)\n" \
"v_mfma_f32_16x16x32_bf16 a[4:7], a[192:195], a[228:231], a[4:7]\n" \
"s_addk_i32 s84, 0x7c00\n" \
"s_waitcnt lgkmcnt(11)\n" \
"v_mfma_f32_16x16x32_bf16 a[8:11], a[128:131], a[168:171], a[8:11]\n" \
"buffer_load_dwordx4 v27, s[72:75], s84 offen offset:3072 lds\n" \
"s_waitcnt lgkmcnt(9)\n" \
"v_mfma_f32_16x16x32_bf16 a[8:11], a[192:195], a[232:235], a[8:11]\n" \
"ds_read_b64_tr_b16 a[178:179], v36 offset:1024\n" \
"s_waitcnt lgkmcnt(6)\n" \
"v_mfma_f32_16x16x32_bf16 a[12:15], a[128:131], a[172:175], a[12:15]\n" \
"ds_read_b64_tr_b16 a[240:241], v36 offset:8192\n" \
"s_waitcnt lgkmcnt(5)\n" \
"v_mfma_f32_16x16x32_bf16 a[12:15], a[192:195], a[236:239], a[12:15]\n" \
"v_mfma_f32_16x16x32_bf16 a[28:31], a[132:135], a[172:175], a[28:31]\n" \
"v_mfma_f32_16x16x32_bf16 a[28:31], a[196:199], a[236:239], a[28:31]\n" \
"ds_read_b64_tr_b16 a[242:243], v36 offset:9216\n" \
"v_mfma_f32_16x16x32_bf16 a[24:27], a[132:135], a[168:171], a[24:27]\n" \
"v_mfma_f32_16x16x32_bf16 a[24:27], a[196:199], a[232:235], a[24:27]\n" \
"v_mfma_f32_16x16x32_bf16 a[20:23], a[132:135], a[164:167], a[20:23]\n" \
"v_mfma_f32_16x16x32_bf16 a[20:23], a[196:199], a[228:231], a[20:23]\n" \
"ds_read_b64_tr_b16 a[180:181], v37 offset:0\n" \
"v_mfma_f32_16x16x32_bf16 a[16:19], a[132:135], a[160:163], a[16:19]\n" \
"v_mfma_f32_16x16x32_bf16 a[16:19], a[196:199], a[224:227], a[16:19]\n" \
"v_mfma_f32_16x16x32_bf16 a[32:35], a[136:139], a[160:163], a[32:35]\n" \
"ds_read_b64_tr_b16 a[182:183], v37 offset:1024\n" \
"v_mfma_f32_16x16x32_bf16 a[32:35], a[200:203], a[224:227], a[32:35]\n" \
"v_mfma_f32_16x16x32_bf16 a[36:39], a[136:139], a[164:167], a[36:39]\n" \
"v_mfma_f32_16x16x32_bf16 a[36:39], a[200:203], a[228:231], a[36:39]\n" \
"buffer_store_dwordx4 v[200:203], v20, s[80:83], s35 offen sc1\n" \
".fill (L_pb + 52960 - .) / 4, 4, 0xBF800000\n" \
"L_pp13:\n" \
"s_cbranch_execz L_pret\n" \
"v_mfma_f32_16x16x32_bf16 a[40:43], a[136:139], a[168:171], a[40:43]\n" \
"s_mov_b32 m0, s32\n" \
"ds_read_b64_tr_b16 a[244:245], v37 offset:8192\n" \
"v_mfma_f32_16x16x32_bf16 a[40:43], a[200:203], a[232:235], a[40:43]\n" \
"s_mov_b32 exec_lo, 0x0\n" \
"s_mov_b32 exec_hi, 0xffffffff\n" \
"global_load_lds_dword v[188:189], off\n" \
"s_mov_b64 exec, -1\n" \
"s_nop 0\n" \
"v_mfma_f32_16x16x32_bf16 a[44:47], a[136:139], a[172:175], a[44:47]\n" \
"ds_read_b64_tr_b16 a[246:247], v37 offset:9216\n" \
"v_mfma_f32_16x16x32_bf16 a[44:47], a[200:203], a[236:239], a[44:47]\n" \
"v_mfma_f32_16x16x32_bf16 a[60:63], a[140:143], a[172:175], a[60:63]\n" \
"v_mfma_f32_16x16x32_bf16 a[60:63], a[204:207], a[236:239], a[60:63]\n" \
"v_mfma_f32_16x16x32_bf16 a[56:59], a[140:143], a[168:171], a[56:59]\n" \
"ds_read_b64_tr_b16 a[184:185], v38 offset:0\n" \
"v_mfma_f32_16x16x32_bf16 a[56:59], a[204:207], a[232:235], a[56:59]\n" \
"v_mfma_f32_16x16x32_bf16 a[52:55], a[140:143], a[164:167], a[52:55]\n" \
"buffer_store_dwordx4 v[204:207], v20, s[80:83], s88 offen sc1\n" \
"v_mfma_f32_16x16x32_bf16 a[52:55], a[204:207], a[228:231], a[52:55]\n" \
"ds_read_b64_tr_b16 a[186:187], v38 offset:1024\n" \
"v_mfma_f32_16x16x32_bf16 a[48:51], a[140:143], a[160:163], a[48:51]\n" \
"v_mfma_f32_16x16x32_bf16 a[48:51], a[204:207], a[224:227], a[48:51]\n" \
"v_mfma_f32_16x16x32_bf16 a[64:67], a[144:147], a[160:163], a[64:67]\n" \
"ds_read_b64_tr_b16 a[248:249], v38 offset:8192\n" \
"v_mfma_f32_16x16x32_bf16 a[64:67], a[208:211], a[224:227], a[64:67]\n" \
"v_mfma_f32_16x16x32_bf16 a[68:71], a[144:147], a[164:167], a[68:71]\n" \
"v_mfma_f32_16x16x32_bf16 a[68:71], a[208:211], a[228:231], a[68:71]\n" \
"ds_read_b64_tr_b16 a[250:251], v38 offset:9216\n" \
"v_mfma_f32_16x16x32_bf16 a[72:75], a[144:147], a[168:171], a[72:75]\n" \
"v_mfma_f32_16x16x32_bf16 a[72:75], a[208:211], a[232:235], a[72:75]\n" \
"v_mfma_f32_16x16x32_bf16 a[76:79], a[144:147], a[172:175], a[76:79]\n" \
"buffer_store_dwordx4 v[208:211], v20, s[80:83], s36 offen sc1\n" \
"v_mfma_f32_16x16x32_bf16 a[76:79], a[208:211], a[236:239], a[76:79]\n" \
"ds_read_b64_tr_b16 a[188:189], v39 offset:0\n" \
"v_mfma_f32_16x16x32_bf16 a[92:95], a[148:151], a[172:175], a[92:95]\n" \
"v_mfma_f32_16x16x32_bf16 a[92:95], a[212:215], a[236:239], a[92:95]\n" \
"v_mfma_f32_16x16x32_bf16 a[88:91], a[148:151], a[168:171], a[88:91]\n" \
"ds_read_b64_tr_b16 a[190:191], v39 offset:1024\n" \
"v_mfma_f32_16x16x32_bf16 a[88:91], a[212:215], a[232:235], a[88:91]\n" \
"v_mfma_f32_16x16x32_bf16 a[84:87], a[148:151], a[164:167], a[84:87]\n" \
"v_mfma_f32_16x16x32_bf16 a[84:87], a[212:215], a[228:231], a[84:87]\n" \
"ds_read_b64_tr_b16 a[252:253], v39 offset:8192\n" \
"v_mfma_f32_16x16x32_bf16 a[80:83], a[148:151], a[160:163], a[80:83]\n" \
"v_mfma_f32_16x16x32_bf16 a[80:83], a[212:215], a[224:227], a[80:83]\n" \
"v_mfma_f32_16x16x32_bf16 a[96:99], a[152:155], a[160:163], a[96:99]\n" \
"ds_read_b64_tr_b16 a[254:255], v39 offset:9216\n" \
"v_mfma_f32_16x16x32_bf16 a[96:99], a[216:219], a[224:227], a[96:99]\n" \
"v_mfma_f32_16x16x32_bf16 a[100:103], a[152:155], a[164:167], a[100:103]\n" \
"v_mfma_f32_16x16x32_bf16 a[100:103], a[216:219], a[228:231], a[100:103]\n" \
"v_mfma_f32_16x16x32_bf16 a[104:107], a[152:155], a[168:171], a[104:107]\n" \
"v_mfma_f32_16x16x32_bf16 a[104:107], a[216:219], a[232:235], a[104:107]\n" \
"v_mfma_f32_16x16x32_bf16 a[108:111], a[152:155], a[172:175], a[108:111]\n" \
"v_mfma_f32_16x16x32_bf16 a[108:111], a[216:219], a[236:239], a[108:111]\n" \
"s_waitcnt lgkmcnt(15)\n" \
"v_mfma_f32_16x16x32_bf16 a[124:127], a[156:159], a[172:175], a[124:127]\n" \
"v_mfma_f32_16x16x32_bf16 a[124:127], a[220:223], a[236:239], a[124:127]\n" \
"v_mfma_f32_16x16x32_bf16 a[120:123], a[156:159], a[168:171], a[120:123]\n" \
"v_mfma_f32_16x16x32_bf16 a[120:123], a[220:223], a[232:235], a[120:123]\n" \
"v_mfma_f32_16x16x32_bf16 a[116:119], a[156:159], a[164:167], a[116:119]\n" \
"v_mfma_f32_16x16x32_bf16 a[116:119], a[220:223], a[228:231], a[116:119]\n" \
"v_mfma_f32_16x16x32_bf16 a[112:115], a[156:159], a[160:163], a[112:115]\n" \
"v_mfma_f32_16x16x32_bf16 a[112:115], a[220:223], a[224:227], a[112:115]\n" \
"s_waitcnt vmcnt(4) lgkmcnt(0)\n" \
"s_barrier\n" \
"v_mfma_f32_16x16x32_bf16 v[52:55], a[128:131], a[176:179], v[52:55]\n" \
"s_add_u32 m0, s30, 0x0\n" \
"s_movk_i32 s84, 0x300\n" \
"v_mfma_f32_16x16x32_bf16 v[52:55], a[192:195], a[240:243], v[52:55]\n" \
"buffer_load_dwordx4 v22, s[64:67], s84 offen offset:0 lds\n" \
"v_mfma_f32_16x16x32_bf16 v[56:59], a[128:131], a[180:183], v[56:59]\n" \
"ds_read_b64_tr_b16 a[160:161], v32 offset:32768\n" \
"v_mfma_f32_16x16x32_bf16 v[56:59], a[192:195], a[244:247], v[56:59]\n" \
"s_movk_i32 s84, 0x1f00\n" \
"v_mfma_f32_16x16x32_bf16 v[60:63], a[128:131], a[184:187], v[60:63]\n" \
"buffer_load_dwordx4 v23, s[64:67], s84 offen offset:1024 lds\n" \
"v_mfma_f32_16x16x32_bf16 v[60:63], a[192:195], a[248:251], v[60:63]\n" \
"ds_read_b64_tr_b16 a[162:163], v32 offset:33792\n" \
"v_mfma_f32_16x16x32_bf16 v[64:67], a[128:131], a[188:191], v[64:67]\n" \
"ds_read_b64_tr_b16 a[224:225], v32 offset:40960\n" \
"v_mfma_f32_16x16x32_bf16 v[64:67], a[192:195], a[252:255], v[64:67]\n" \
"ds_read_b128 a[128:131], v28 offset:49152\n" \
"v_mfma_f32_16x16x32_bf16 v[80:83], a[132:135], a[188:191], v[80:83]\n" \
"ds_read_b128 a[192:195], v29 offset:49152\n" \
"v_mfma_f32_16x16x32_bf16 v[80:83], a[196:199], a[252:255], v[80:83]\n" \
"s_movk_i32 s84, 0x3b00\n" \
"v_mfma_f32_16x16x32_bf16 v[76:79], a[132:135], a[184:187], v[76:79]\n" \
"buffer_load_dwordx4 v22, s[64:67], s84 offen offset:2048 lds\n" \
"v_mfma_f32_16x16x32_bf16 v[76:79], a[196:199], a[248:251], v[76:79]\n" \
"s_movk_i32 s84, 0x5700\n" \
"v_mfma_f32_16x16x32_bf16 v[72:75], a[132:135], a[180:183], v[72:75]\n" \
"buffer_load_dwordx4 v23, s[64:67], s84 offen offset:3072 lds\n" \
"v_mfma_f32_16x16x32_bf16 v[72:75], a[196:199], a[244:247], v[72:75]\n" \
"s_add_u32 m0, s31, 0x0\n" \
"s_mov_b32 s84, 0x300000\n" \
"v_mfma_f32_16x16x32_bf16 v[68:71], a[132:135], a[176:179], v[68:71]\n" \
"buffer_load_dwordx4 v26, s[68:71], s84 offen offset:0 lds\n" \
"v_mfma_f32_16x16x32_bf16 v[68:71], a[196:199], a[240:243], v[68:71]\n" \
"ds_read_b128 a[132:135], v28 offset:51200\n" \
"v_mfma_f32_16x16x32_bf16 v[84:87], a[136:139], a[176:179], v[84:87]\n" \
"ds_read_b128 a[196:199], v29 offset:51200\n" \
"v_mfma_f32_16x16x32_bf16 v[84:87], a[200:203], a[240:243], v[84:87]\n" \
"s_addk_i32 s84, 0x7c00\n" \
"v_mfma_f32_16x16x32_bf16 v[88:91], a[136:139], a[180:183], v[88:91]\n" \
"buffer_load_dwordx4 v26, s[68:71], s84 offen offset:1024 lds\n" \
"v_mfma_f32_16x16x32_bf16 v[88:91], a[200:203], a[244:247], v[88:91]\n" \
"ds_read_b64_tr_b16 a[226:227], v32 offset:41984\n" \
"v_mfma_f32_16x16x32_bf16 v[92:95], a[136:139], a[184:187], v[92:95]\n" \
"s_addk_i32 s84, 0x7c00\n" \
"v_mfma_f32_16x16x32_bf16 v[92:95], a[200:203], a[248:251], v[92:95]\n" \
"buffer_load_dwordx4 v27, s[68:71], s84 offen offset:2048 lds\n" \
"v_mfma_f32_16x16x32_bf16 v[96:99], a[136:139], a[188:191], v[96:99]\n" \
"ds_read_b64_tr_b16 a[164:165], v33 offset:32768\n" \
"v_mfma_f32_16x16x32_bf16 v[96:99], a[200:203], a[252:255], v[96:99]\n" \
"ds_read_b128 a[136:139], v28 offset:53248\n" \
"v_mfma_f32_16x16x32_bf16 v[112:115], a[140:143], a[188:191], v[112:115]\n" \
"ds_read_b128 a[200:203], v29 offset:53248\n" \
"v_mfma_f32_16x16x32_bf16 v[112:115], a[204:207], a[252:255], v[112:115]\n" \
"s_addk_i32 s84, 0x7c00\n" \
"v_mfma_f32_16x16x32_bf16 v[108:111], a[140:143], a[184:187], v[108:111]\n" \
"buffer_load_dwordx4 v27, s[68:71], s84 offen offset:3072 lds\n" \
"v_mfma_f32_16x16x32_bf16 v[108:111], a[204:207], a[248:251], v[108:111]\n" \
"s_add_u32 m0, s30, 0x1000\n" \
"s_mov_b32 s84, 0x8300\n" \
"v_mfma_f32_16x16x32_bf16 v[104:107], a[140:143], a[180:183], v[104:107]\n" \
"buffer_load_dwordx4 v22, s[64:67], s84 offen offset:0 lds\n" \
"v_mfma_f32_16x16x32_bf16 v[104:107], a[204:207], a[244:247], v[104:107]\n" \
"ds_read_b64_tr_b16 a[166:167], v33 offset:33792\n" \
"v_mfma_f32_16x16x32_bf16 v[100:103], a[140:143], a[176:179], v[100:103]\n" \
"ds_read_b64_tr_b16 a[228:229], v33 offset:40960\n" \
"v_mfma_f32_16x16x32_bf16 v[100:103], a[204:207], a[240:243], v[100:103]\n" \
"ds_read_b128 a[140:143], v28 offset:55296\n" \
"v_mfma_f32_16x16x32_bf16 v[116:119], a[144:147], a[176:179], v[116:119]\n" \
"ds_read_b128 a[204:207], v29 offset:55296\n" \
"v_mfma_f32_16x16x32_bf16 v[116:119], a[208:211], a[240:243], v[116:119]\n" \
"s_addk_i32 s84, 0x1c00\n" \
"v_mfma_f32_16x16x32_bf16 v[120:123], a[144:147], a[180:183], v[120:123]\n" \
"buffer_load_dwordx4 v23, s[64:67], s84 offen offset:1024 lds\n" \
"v_mfma_f32_16x16x32_bf16 v[120:123], a[208:211], a[244:247], v[120:123]\n" \
"s_addk_i32 s84, 0x1c00\n" \
"v_mfma_f32_16x16x32_bf16 v[124:127], a[144:147], a[184:187], v[124:127]\n" \
"buffer_load_dwordx4 v22, s[64:67], s84 offen offset:2048 lds\n" \
"v_mfma_f32_16x16x32_bf16 v[124:127], a[208:211], a[248:251], v[124:127]\n" \
"s_addk_i32 s84, 0x1c00\n" \
"s_nop 0\n" \
"v_mfma_f32_16x16x32_bf16 v[128:131], a[144:147], a[188:191], v[128:131]\n" \
"buffer_load_dwordx4 v23, s[64:67], s84 offen offset:3072 lds\n" \
"v_mfma_f32_16x16x32_bf16 v[128:131], a[208:211], a[252:255], v[128:131]\n" \
"ds_read_b128 a[144:147], v28 offset:32768\n" \
"v_mfma_f32_16x16x32_bf16 v[144:147], a[148:151], a[188:191], v[144:147]\n" \
"ds_read_b128 a[208:211], v29 offset:32768\n" \
"v_mfma_f32_16x16x32_bf16 v[144:147], a[212:215], a[252:255], v[144:147]\n" \
"s_add_u32 m0, s31, 0x4000\n" \
"s_mov_b32 s84, 0x300000\n" \
"v_mfma_f32_16x16x32_bf16 v[140:143], a[148:151], a[184:187], v[140:143]\n" \
"buffer_load_dwordx4 v26, s[72:75], s84 offen offset:0 lds\n" \
"v_mfma_f32_16x16x32_bf16 v[140:143], a[212:215], a[248:251], v[140:143]\n" \
"ds_read_b64_tr_b16 a[230:231], v33 offset:41984\n" \
"v_mfma_f32_16x16x32_bf16 v[136:139], a[148:151], a[180:183], v[136:139]\n" \
"s_addk_i32 s84, 0x7c00\n" \
"s_nop 0\n" \
"v_mfma_f32_16x16x32_bf16 v[136:139], a[212:215], a[244:247], v[136:139]\n" \
"buffer_load_dwordx4 v26, s[72:75], s84 offen offset:1024 lds\n" \
"v_mfma_f32_16x16x32_bf16 v[132:135], a[148:151], a[176:179], v[132:135]\n" \
"ds_read_b64_tr_b16 a[168:169], v34 offset:32768\n" \
"v_mfma_f32_16x16x32_bf16 v[132:135], a[212:215], a[240:243], v[132:135]\n" \
"ds_read_b128 a[148:151], v28 offset:34816\n" \
"v_mfma_f32_16x16x32_bf16 v[148:151], a[152:155], a[176:179], v[148:151]\n" \
"ds_read_b128 a[212:215], v29 offset:34816\n" \
"v_mfma_f32_16x16x32_bf16 v[148:151], a[216:219], a[240:243], v[148:151]\n" \
"ds_read_b64_tr_b16 a[170:171], v34 offset:33792\n" \
"v_mfma_f32_16x16x32_bf16 v[152:155], a[152:155], a[180:183], v[152:155]\n" \
"ds_read_b64_tr_b16 a[232:233], v34 offset:40960\n" \
"v_mfma_f32_16x16x32_bf16 v[152:155], a[216:219], a[244:247], v[152:155]\n" \
"ds_read_b64_tr_b16 a[234:235], v34 offset:41984\n" \
"v_mfma_f32_16x16x32_bf16 v[156:159], a[152:155], a[184:187], v[156:159]\n" \
"ds_read_b64_tr_b16 a[172:173], v35 offset:32768\n" \
"v_mfma_f32_16x16x32_bf16 v[156:159], a[216:219], a[248:251], v[156:159]\n" \
"ds_read_b64_tr_b16 a[174:175], v35 offset:33792\n" \
"v_mfma_f32_16x16x32_bf16 v[160:163], a[152:155], a[188:191], v[160:163]\n" \
"ds_read_b64_tr_b16 a[236:237], v35 offset:40960\n" \
"v_mfma_f32_16x16x32_bf16 v[160:163], a[216:219], a[252:255], v[160:163]\n" \
"ds_read_b128 a[152:155], v28 offset:36864\n" \
"v_mfma_f32_16x16x32_bf16 v[176:179], a[156:159], a[188:191], v[176:179]\n" \
"ds_read_b128 a[216:219], v29 offset:36864\n" \
"v_mfma_f32_16x16x32_bf16 v[176:179], a[220:223], a[252:255], v[176:179]\n" \
"ds_read_b64_tr_b16 a[238:239], v35 offset:41984\n" \
"v_mfma_f32_16x16x32_bf16 v[172:175], a[156:159], a[184:187], v[172:175]\n" \
"v_mfma_f32_16x16x32_bf16 v[172:175], a[220:223], a[248:251], v[172:175]\n" \
"v_mfma_f32_16x16x32_bf16 v[168:171], a[156:159], a[180:183], v[168:171]\n" \
"v_mfma_f32_16x16x32_bf16 v[168:171], a[220:223], a[244:247], v[168:171]\n" \
"v_mfma_f32_16x16x32_bf16 v[164:167], a[156:159], a[176:179], v[164:167]\n" \
"v_mfma_f32_16x16x32_bf16 v[164:167], a[220:223], a[240:243], v[164:167]\n" \
"ds_read_b128 a[156:159], v28 offset:38912\n" \
"ds_read_b128 a[220:223], v29 offset:38912\n" \
"s_waitcnt lgkmcnt(15)\n" \
"v_mfma_f32_16x16x32_bf16 a[0:3], a[128:131], a[160:163], a[0:3]\n" \
"s_addk_i32 s84, 0x7c00\n" \
"v_mfma_f32_16x16x32_bf16 a[0:3], a[192:195], a[224:227], a[0:3]\n" \
"buffer_load_dwordx4 v27, s[72:75], s84 offen offset:2048 lds\n" \
"v_mfma_f32_16x16x32_bf16 a[4:7], a[128:131], a[164:167], a[4:7]\n" \
"ds_read_b64_tr_b16 a[176:177], v36 offset:32768\n" \
"s_waitcnt lgkmcnt(15)\n" \
"v_mfma_f32_16x16x32_bf16 a[4:7], a[192:195], a[228:231], a[4:7]\n" \
"s_addk_i32 s84, 0x7c00\n" \
"s_waitcnt lgkmcnt(11)\n" \
"v_mfma_f32_16x16x32_bf16 a[8:11], a[128:131], a[168:171], a[8:11]\n" \
"buffer_load_dwordx4 v27, s[72:75], s84 offen offset:3072 lds\n" \
"s_waitcnt lgkmcnt(9)\n" \
"v_mfma_f32_16x16x32_bf16 a[8:11], a[192:195], a[232:235], a[8:11]\n" \
"ds_read_b64_tr_b16 a[178:179], v36 offset:33792\n" \
"s_waitcnt lgkmcnt(8)\n" \
"v_mfma_f32_16x16x32_bf16 a[12:15], a[128:131], a[172:175], a[12:15]\n" \
"ds_read_b64_tr_b16 a[240:241], v36 offset:40960\n" \
"s_waitcnt lgkmcnt(5)\n" \
"v_mfma_f32_16x16x32_bf16 a[12:15], a[192:195], a[236:239], a[12:15]\n" \
"v_mfma_f32_16x16x32_bf16 a[28:31], a[132:135], a[172:175], a[28:31]\n" \
"v_mfma_f32_16x16x32_bf16 a[28:31], a[196:199], a[236:239], a[28:31]\n" \
"ds_read_b64_tr_b16 a[242:243], v36 offset:41984\n" \
"v_mfma_f32_16x16x32_bf16 a[24:27], a[132:135], a[168:171], a[24:27]\n" \
"v_mfma_f32_16x16x32_bf16 a[24:27], a[196:199], a[232:235], a[24:27]\n" \
"v_mfma_f32_16x16x32_bf16 a[20:23], a[132:135], a[164:167], a[20:23]\n" \
"v_mfma_f32_16x16x32_bf16 a[20:23], a[196:199], a[228:231], a[20:23]\n" \
"ds_read_b64_tr_b16 a[180:181], v37 offset:32768\n" \
"v_mfma_f32_16x16x32_bf16 a[16:19], a[132:135], a[160:163], a[16:19]\n" \
"v_mfma_f32_16x16x32_bf16 a[16:19], a[196:199], a[224:227], a[16:19]\n" \
"v_mfma_f32_16x16x32_bf16 a[32:35], a[136:139], a[160:163], a[32:35]\n" \
"ds_read_b64_tr_b16 a[182:183], v37 offset:33792\n" \
"v_mfma_f32_16x16x32_bf16 a[32:35], a[200:203], a[224:227], a[32:35]\n" \
"v_mfma_f32_16x16x32_bf16 a[36:39], a[136:139], a[164:167], a[36:39]\n" \
"v_lshlrev_b32_e32 v45, 3, v20\n" \
"v_add_u32_e32 v45, s31, v45\n" \
"v_mfma_f32_16x16x32_bf16 a[36:39], a[200:203], a[228:231], a[36:39]\n" \
"s_getpc_b64 s[86:87]\n" \
"s_sub_u32 s86, s86, 0x10800\n" \
"s_subb_u32 s87, s87, 0\n" \
"s_mov_b32 m0, s32\n" \
"s_nop 0\n" \
"v_mfma_f32_16x16x32_bf16 a[40:43], a[136:139], a[168:171], a[40:43]\n" \
"global_load_lds_dword v45, s[86:87]\n" \
"v_mfma_f32_16x16x32_bf16 a[40:43], a[200:203], a[232:235], a[40:43]\n" \
"ds_read_b64_tr_b16 a[244:245], v37 offset:40960\n" \
"v_mfma_f32_16x16x32_bf16 a[44:47], a[136:139], a[172:175], a[44:47]\n" \
"ds_read_b64_tr_b16 a[246:247], v37 offset:41984\n" \
"v_mfma_f32_16x16x32_bf16 a[44:47], a[200:203], a[236:239], a[44:47]\n" \
"v_mfma_f32_16x16x32_bf16 a[60:63], a[140:143], a[172:175], a[60:63]\n" \
"v_mfma_f32_16x16x32_bf16 a[60:63], a[204:207], a[236:239], a[60:63]\n" \
"v_mfma_f32_16x16x32_bf16 a[56:59], a[140:143], a[168:171], a[56:59]\n" \
"ds_read_b64_tr_b16 a[184:185], v38 offset:32768\n" \
"v_mfma_f32_16x16x32_bf16 a[56:59], a[204:207], a[232:235], a[56:59]\n" \
"v_mfma_f32_16x16x32_bf16 a[52:55], a[140:143], a[164:167], a[52:55]\n" \
"v_mfma_f32_16x16x32_bf16 a[52:55], a[204:207], a[228:231], a[52:55]\n" \
"ds_read_b64_tr_b16 a[186:187], v38 offset:33792\n" \
"v_mfma_f32_16x16x32_bf16 a[48:51], a[140:143], a[160:163], a[48:51]\n" \
"v_mfma_f32_16x16x32_bf16 a[48:51], a[204:207], a[224:227], a[48:51]\n" \
"v_mfma_f32_16x16x32_bf16 a[64:67], a[144:147], a[160:163], a[64:67]\n" \
"ds_read_b64_tr_b16 a[248:249], v38 offset:40960\n" \
"v_mfma_f32_16x16x32_bf16 a[64:67], a[208:211], a[224:227], a[64:67]\n" \
"v_mfma_f32_16x16x32_bf16 a[68:71], a[144:147], a[164:167], a[68:71]\n" \
"v_mfma_f32_16x16x32_bf16 a[68:71], a[208:211], a[228:231], a[68:71]\n" \
"ds_read_b64_tr_b16 a[250:251], v38 offset:41984\n" \
"v_mfma_f32_16x16x32_bf16 a[72:75], a[144:147], a[168:171], a[72:75]\n" \
"v_mfma_f32_16x16x32_bf16 a[72:75], a[208:211], a[232:235], a[72:75]\n" \
"v_mfma_f32_16x16x32_bf16 a[76:79], a[144:147], a[172:175], a[76:79]\n" \
"v_mfma_f32_16x16x32_bf16 a[76:79], a[208:211], a[236:239], a[76:79]\n" \
"ds_read_b64_tr_b16 a[188:189], v39 offset:32768\n" \
"v_mfma_f32_16x16x32_bf16 a[92:95], a[148:151], a[172:175], a[92:95]\n" \
"v_mfma_f32_16x16x32_bf16 a[92:95], a[212:215], a[236:239], a[92:95]\n" \
"v_mfma_f32_16x16x32_bf16 a[88:91], a[148:151], a[168:171], a[88:91]\n" \
"ds_read_b64_tr_b16 a[190:191], v39 offset:33792\n" \
"v_mfma_f32_16x16x32_bf16 a[88:91], a[212:215], a[232:235], a[88:91]\n" \
"v_mfma_f32_16x16x32_bf16 a[84:87], a[148:151], a[164:167], a[84:87]\n" \
"v_mfma_f32_16x16x32_bf16 a[84:87], a[212:215], a[228:231], a[84:87]\n" \
"ds_read_b64_tr_b16 a[252:253], v39 offset:40960\n" \
"v_mfma_f32_16x16x32_bf16 a[80:83], a[148:151], a[160:163], a[80:83]\n" \
"v_mfma_f32_16x16x32_bf16 a[80:83], a[212:215], a[224:227], a[80:83]\n" \
"v_mfma_f32_16x16x32_bf16 a[96:99], a[152:155], a[160:163], a[96:99]\n" \
"ds_read_b64_tr_b16 a[254:255], v39 offset:41984\n" \
"v_mfma_f32_16x16x32_bf16 a[96:99], a[216:219], a[224:227], a[96:99]\n" \
"v_mfma_f32_16x16x32_bf16 a[100:103], a[152:155], a[164:167], a[100:103]\n" \
"v_mfma_f32_16x16x32_bf16 a[100:103], a[216:219], a[228:231], a[100:103]\n" \
"v_mfma_f32_16x16x32_bf16 a[104:107], a[152:155], a[168:171], a[104:107]\n" \
"v_mfma_f32_16x16x32_bf16 a[104:107], a[216:219], a[232:235], a[104:107]\n" \
"v_mfma_f32_16x16x32_bf16 a[108:111], a[152:155], a[172:175], a[108:111]\n" \
"v_mfma_f32_16x16x32_bf16 a[108:111], a[216:219], a[236:239], a[108:111]\n" \
"s_waitcnt lgkmcnt(15)\n" \
"v_mfma_f32_16x16x32_bf16 a[124:127], a[156:159], a[172:175], a[124:127]\n" \
"v_mfma_f32_16x16x32_bf16 a[124:127], a[220:223], a[236:239], a[124:127]\n" \
"v_mfma_f32_16x16x32_bf16 a[120:123], a[156:159], a[168:171], a[120:123]\n" \
"v_mfma_f32_16x16x32_bf16 a[120:123], a[220:223], a[232:235], a[120:123]\n" \
"v_mfma_f32_16x16x32_bf16 a[116:119], a[156:159], a[164:167], a[116:119]\n" \
"v_mfma_f32_16x16x32_bf16 a[116:119], a[220:223], a[228:231], a[116:119]\n" \
"v_mfma_f32_16x16x32_bf16 a[112:115], a[156:159], a[160:163], a[112:115]\n" \
"v_mfma_f32_16x16x32_bf16 a[112:115], a[220:223], a[224:227], a[112:115]\n" \
"s_waitcnt vmcnt(1) lgkmcnt(0)\n" \
"s_barrier\n" \
"v_mov_b32_e32 v45, s62\n" \
"s_mov_b64 exec, 1\n" \
"buffer_store_dword v45, off, s[80:83], s37 offset:128 sc1\n" \
"s_mov_b64 exec, -1\n" \
"v_mfma_f32_16x16x32_bf16 v[52:55], a[128:131], a[176:179], v[52:55]\n" \
"s_add_u32 m0, s30, 0x8000\n" \
"s_movk_i32 s84, 0x380\n" \
"v_mfma_f32_16x16x32_bf16 v[52:55], a[192:195], a[240:243], v[52:55]\n" \
"buffer_load_dwordx4 v22, s[64:67], s84 offen offset:0 lds\n" \
"v_mfma_f32_16x16x32_bf16 v[56:59], a[128:131], a[180:183], v[56:59]\n" \
"ds_read_b64_tr_b16 a[160:161], v32 offset:0\n" \
"v_mfma_f32_16x16x32_bf16 v[56:59], a[192:195], a[244:247], v[56:59]\n" \
"s_movk_i32 s84, 0x1f80\n" \
"v_mfma_f32_16x16x32_bf16 v[60:63], a[128:131], a[184:187], v[60:63]\n" \
"buffer_load_dwordx4 v23, s[64:67], s84 offen offset:1024 lds\n" \
"v_mfma_f32_16x16x32_bf16 v[60:63], a[192:195], a[248:251], v[60:63]\n" \
"ds_read_b64_tr_b16 a[162:163], v32 offset:1024\n" \
"v_mfma_f32_16x16x32_bf16 v[64:67], a[128:131], a[188:191], v[64:67]\n" \
"ds_read_b64_tr_b16 a[224:225], v32 offset:8192\n" \
"v_mfma_f32_16x16x32_bf16 v[64:67], a[192:195], a[252:255], v[64:67]\n" \
"ds_read_b128 a[128:131], v28 offset:16384\n" \
"v_mfma_f32_16x16x32_bf16 v[80:83], a[132:135], a[188:191], v[80:83]\n" \
"ds_read_b128 a[192:195], v29 offset:16384\n" \
"v_mfma_f32_16x16x32_bf16 v[80:83], a[196:199], a[252:255], v[80:83]\n" \
"s_movk_i32 s84, 0x3b80\n" \
"v_mfma_f32_16x16x32_bf16 v[76:79], a[132:135], a[184:187], v[76:79]\n" \
"buffer_load_dwordx4 v22, s[64:67], s84 offen offset:2048 lds\n" \
"v_mfma_f32_16x16x32_bf16 v[76:79], a[196:199], a[248:251], v[76:79]\n" \
"s_movk_i32 s84, 0x5780\n" \
"v_mfma_f32_16x16x32_bf16 v[72:75], a[132:135], a[180:183], v[72:75]\n" \
"buffer_load_dwordx4 v23, s[64:67], s84 offen offset:3072 lds\n" \
"v_mfma_f32_16x16x32_bf16 v[72:75], a[196:199], a[244:247], v[72:75]\n" \
"s_add_u32 m0, s31, 0x8000\n" \
"s_mov_b32 s84, 0x380000\n" \
"v_mfma_f32_16x16x32_bf16 v[68:71], a[132:135], a[176:179], v[68:71]\n" \
"buffer_load_dwordx4 v26, s[68:71], s84 offen offset:0 lds\n" \
"v_mfma_f32_16x16x32_bf16 v[68:71], a[196:199], a[240:243], v[68:71]\n" \
"ds_read_b128 a[132:135], v28 offset:18432\n" \
"v_mfma_f32_16x16x32_bf16 v[84:87], a[136:139], a[176:179], v[84:87]\n" \
"ds_read_b128 a[196:199], v29 offset:18432\n" \
"v_mfma_f32_16x16x32_bf16 v[84:87], a[200:203], a[240:243], v[84:87]\n" \
"s_addk_i32 s84, 0x7c00\n" \
"v_mfma_f32_16x16x32_bf16 v[88:91], a[136:139], a[180:183], v[88:91]\n" \
"buffer_load_dwordx4 v26, s[68:71], s84 offen offset:1024 lds\n" \
"v_mfma_f32_16x16x32_bf16 v[88:91], a[200:203], a[244:247], v[88:91]\n" \
"ds_read_b64_tr_b16 a[226:227], v32 offset:9216\n" \
"v_mfma_f32_16x16x32_bf16 v[92:95], a[136:139], a[184:187], v[92:95]\n" \
"s_addk_i32 s84, 0x7c00\n" \
"v_mfma_f32_16x16x32_bf16 v[92:95], a[200:203], a[248:251], v[92:95]\n" \
"buffer_load_dwordx4 v27, s[68:71], s84 offen offset:2048 lds\n" \
"v_mfma_f32_16x16x32_bf16 v[96:99], a[136:139], a[188:191], v[96:99]\n" \
"ds_read_b64_tr_b16 a[164:165], v33 offset:0\n" \
"v_mfma_f32_16x16x32_bf16 v[96:99], a[200:203], a[252:255], v[96:99]\n" \
"ds_read_b128 a[136:139], v28 offset:20480\n" \
"v_mfma_f32_16x16x32_bf16 v[112:115], a[140:143], a[188:191], v[112:115]\n" \
"ds_read_b128 a[200:203], v29 offset:20480\n" \
"v_mfma_f32_16x16x32_bf16 v[112:115], a[204:207], a[252:255], v[112:115]\n" \
"s_addk_i32 s84, 0x7c00\n" \
"s_nop 0\n" \
"v_mfma_f32_16x16x32_bf16 v[108:111], a[140:143], a[184:187], v[108:111]\n" \
"buffer_load_dwordx4 v27, s[68:71], s84 offen offset:3072 lds\n" \
"v_mfma_f32_16x16x32_bf16 v[108:111], a[204:207], a[248:251], v[108:111]\n" \
"s_add_u32 m0, s30, 0x9000\n" \
"s_mov_b32 s84, 0x8380\n" \
"v_mfma_f32_16x16x32_bf16 v[104:107], a[140:143], a[180:183], v[104:107]\n" \
"buffer_load_dwordx4 v22, s[64:67], s84 offen offset:0 lds\n" \
"v_mfma_f32_16x16x32_bf16 v[104:107], a[204:207], a[244:247], v[104:107]\n" \
"ds_read_b64_tr_b16 a[166:167], v33 offset:1024\n" \
"v_mfma_f32_16x16x32_bf16 v[100:103], a[140:143], a[176:179], v[100:103]\n" \
"ds_read_b64_tr_b16 a[228:229], v33 offset:8192\n" \
"v_mfma_f32_16x16x32_bf16 v[100:103], a[204:207], a[240:243], v[100:103]\n" \
"ds_read_b128 a[140:143], v28 offset:22528\n" \
"v_mfma_f32_16x16x32_bf16 v[116:119], a[144:147], a[176:179], v[116:119]\n" \
"ds_read_b128 a[204:207], v29 offset:22528\n" \
"v_mfma_f32_16x16x32_bf16 v[116:119], a[208:211], a[240:243], v[116:119]\n" \
"s_addk_i32 s84, 0x1c00\n" \
"v_mfma_f32_16x16x32_bf16 v[120:123], a[144:147], a[180:183], v[120:123]\n" \
"buffer_load_dwordx4 v23, s[64:67], s84 offen offset:1024 lds\n" \
"v_mfma_f32_16x16x32_bf16 v[120:123], a[208:211], a[244:247], v[120:123]\n" \
"s_addk_i32 s84, 0x1c00\n" \
"v_mfma_f32_16x16x32_bf16 v[124:127], a[144:147], a[184:187], v[124:127]\n" \
"buffer_load_dwordx4 v22, s[64:67], s84 offen offset:2048 lds\n" \
"v_mfma_f32_16x16x32_bf16 v[124:127], a[208:211], a[248:251], v[124:127]\n" \
"s_addk_i32 s84, 0x1c00\n" \
"s_nop 0\n" \
"v_mfma_f32_16x16x32_bf16 v[128:131], a[144:147], a[188:191], v[128:131]\n" \
"buffer_load_dwordx4 v23, s[64:67], s84 offen offset:3072 lds\n" \
"v_mfma_f32_16x16x32_bf16 v[128:131], a[208:211], a[252:255], v[128:131]\n" \
"ds_read_b128 a[144:147], v28 offset:0\n" \
"v_mfma_f32_16x16x32_bf16 v[144:147], a[148:151], a[188:191], v[144:147]\n" \
"ds_read_b128 a[208:211], v29 offset:0\n" \
"v_mfma_f32_16x16x32_bf16 v[144:147], a[212:215], a[252:255], v[144:147]\n" \
"s_add_u32 m0, s31, 0xc000\n" \
"s_mov_b32 s84, 0x380000\n" \
"v_mfma_f32_16x16x32_bf16 v[140:143], a[148:151], a[184:187], v[140:143]\n" \
"buffer_load_dwordx4 v26, s[72:75], s84 offen offset:0 lds\n" \
"v_mfma_f32_16x16x32_bf16 v[140:143], a[212:215], a[248:251], v[140:143]\n" \
"ds_read_b64_tr_b16 a[230:231], v33 offset:9216\n" \
"v_mfma_f32_16x16x32_bf16 v[136:139], a[148:151], a[180:183], v[136:139]\n" \
"s_addk_i32 s84, 0x7c00\n" \
"s_nop 0\n" \
"v_mfma_f32_16x16x32_bf16 v[136:139], a[212:215], a[244:247], v[136:139]\n" \
"buffer_load_dwordx4 v26, s[72:75], s84 offen offset:1024 lds\n" \
"v_mfma_f32_16x16x32_bf16 v[132:135], a[148:151], a[176:179], v[132:135]\n" \
"ds_read_b64_tr_b16 a[168:169], v34 offset:0\n" \
"v_mfma_f32_16x16x32_bf16 v[132:135], a[212:215], a[240:243], v[132:135]\n" \
"ds_read_b128 a[148:151], v28 offset:2048\n" \
"v_mfma_f32_16x16x32_bf16 v[148:151], a[152:155], a[176:179], v[148:151]\n" \
"ds_read_b128 a[212:215], v29 offset:2048\n" \
"v_mfma_f32_16x16x32_bf16 v[148:151], a[216:219], a[240:243], v[148:151]\n" \
"ds_read_b64_tr_b16 a[170:171], v34 offset:1024\n" \
"v_mfma_f32_16x16x32_bf16 v[152:155], a[152:155], a[180:183], v[152:155]\n" \
"ds_read_b64_tr_b16 a[232:233], v34 offset:8192\n" \
"v_mfma_f32_16x16x32_bf16 v[152:155], a[216:219], a[244:247], v[152:155]\n" \
"ds_read_b64_tr_b16 a[234:235], v34 offset:9216\n" \
"v_mfma_f32_16x16x32_bf16 v[156:159], a[152:155], a[184:187], v[156:159]\n" \
"ds_read_b64_tr_b16 a[172:173], v35 offset:0\n" \
"v_mfma_f32_16x16x32_bf16 v[156:159], a[216:219], a[248:251], v[156:159]\n" \
"ds_read_b64_tr_b16 a[174:175], v35 offset:1024\n" \
"v_mfma_f32_16x16x32_bf16 v[160:163], a[152:155], a[188:191], v[160:163]\n" \
"ds_read_b64_tr_b16 a[236:237], v35 offset:8192\n" \
"v_mfma_f32_16x16x32_bf16 v[160:163], a[216:219], a[252:255], v[160:163]\n" \
"ds_read_b128 a[152:155], v28 offset:4096\n" \
"v_mfma_f32_16x16x32_bf16 v[176:179], a[156:159], a[188:191], v[176:179]\n" \
"ds_read_b128 a[216:219], v29 offset:4096\n" \
"v_mfma_f32_16x16x32_bf16 v[176:179], a[220:223], a[252:255], v[176:179]\n" \
"ds_read_b64_tr_b16 a[238:239], v35 offset:9216\n" \
"v_mfma_f32_16x16x32_bf16 v[172:175], a[156:159], a[184:187], v[172:175]\n" \
"v_mfma_f32_16x16x32_bf16 v[172:175], a[220:223], a[248:251], v[172:175]\n" \
"v_mfma_f32_16x16x32_bf16 v[168:171], a[156:159], a[180:183], v[168:171]\n" \
"v_mfma_f32_16x16x32_bf16 v[168:171], a[220:223], a[244:247], v[168:171]\n" \
"v_mfma_f32_16x16x32_bf16 v[164:167], a[156:159], a[176:179], v[164:167]\n" \
"v_mfma_f32_16x16x32_bf16 v[164:167], a[220:223], a[240:243], v[164:167]\n" \
"ds_read_b128 a[156:159], v28 offset:6144\n" \
"ds_read_b128 a[220:223], v29 offset:6144\n" \
"s_waitcnt lgkmcnt(15)\n" \
"v_mfma_f32_16x16x32_bf16 a[0:3], a[128:131], a[160:163], a[0:3]\n" \
"s_addk_i32 s84, 0x7c00\n" \
"v_mfma_f32_16x16x32_bf16 a[0:3], a[192:195], a[224:227], a[0:3]\n" \
"buffer_load_dwordx4 v27, s[72:75], s84 offen offset:2048 lds\n" \
"v_mfma_f32_16x16x32_bf16 a[4:7], a[128:131], a[164:167], a[4:7]\n" \
"ds_read_b64_tr_b16 a[176:177], v36 offset:0\n" \
"s_waitcnt lgkmcnt(15)\n" \
"v_mfma_f32_16x16x32_bf16 a[4:7], a[192:195], a[228:231], a[4:7]\n" \
"s_addk_i32 s84, 0x7c00\n" \
"s_waitcnt lgkmcnt(11)\n" \
"v_mfma_f32_16x16x32_bf16 a[8:11], a[128:131], a[168:171], a[8:11]\n" \
"buffer_load_dwordx4 v27, s[72:75], s84 offen offset:3072 lds\n" \
"s_waitcnt lgkmcnt(9)\n" \
"v_mfma_f32_16x16x32_bf16 a[8:11], a[192:195], a[232:235], a[8:11]\n" \
"ds_read_b64_tr_b16 a[178:179], v36 offset:1024\n" \
"s_waitcnt lgkmcnt(8)\n" \
"v_mfma_f32_16x16x32_bf16 a[12:15], a[128:131], a[172:175], a[12:15]\n" \
"ds_read_b64_tr_b16 a[240:241], v36 offset:8192\n" \
"s_waitcnt lgkmcnt(5)\n" \
"v_mfma_f32_16x16x32_bf16 a[12:15], a[192:195], a[236:239], a[12:15]\n" \
"v_mfma_f32_16x16x32_bf16 a[28:31], a[132:135], a[172:175], a[28:31]\n" \
"v_mfma_f32_16x16x32_bf16 a[28:31], a[196:199], a[236:239], a[28:31]\n" \
"ds_read_b64_tr_b16 a[242:243], v36 offset:9216\n" \
"v_mfma_f32_16x16x32_bf16 a[24:27], a[132:135], a[168:171], a[24:27]\n" \
"v_mfma_f32_16x16x32_bf16 a[24:27], a[196:199], a[232:235], a[24:27]\n" \
"v_mfma_f32_16x16x32_bf16 a[20:23], a[132:135], a[164:167], a[20:23]\n" \
"v_mfma_f32_16x16x32_bf16 a[20:23], a[196:199], a[228:231], a[20:23]\n" \
"ds_read_b64_tr_b16 a[180:181], v37 offset:0\n" \
"v_mfma_f32_16x16x32_bf16 a[16:19], a[132:135], a[160:163], a[16:19]\n" \
"v_mfma_f32_16x16x32_bf16 a[16:19], a[196:199], a[224:227], a[16:19]\n" \
"v_mfma_f32_16x16x32_bf16 a[32:35], a[136:139], a[160:163], a[32:35]\n" \
"v_bfe_u32 v44, v20, 2, 5\n" \
"ds_read_b64_tr_b16 a[182:183], v37 offset:1024\n" \
"v_mfma_f32_16x16x32_bf16 a[32:35], a[200:203], a[224:227], a[32:35]\n" \
"buffer_load_dword v44, v44, s[80:83], s38 offen offset:128 sc1\n" \
"v_mfma_f32_16x16x32_bf16 a[36:39], a[136:139], a[164:167], a[36:39]\n" \
"v_mfma_f32_16x16x32_bf16 a[36:39], a[200:203], a[228:231], a[36:39]\n" \
"ds_read_b64_tr_b16 a[244:245], v37 offset:8192\n" \
"v_mfma_f32_16x16x32_bf16 a[40:43], a[136:139], a[168:171], a[40:43]\n" \
"v_mfma_f32_16x16x32_bf16 a[40:43], a[200:203], a[232:235], a[40:43]\n" \
"v_mfma_f32_16x16x32_bf16 a[44:47], a[136:139], a[172:175], a[44:47]\n" \
"ds_read_b64_tr_b16 a[246:247], v37 offset:9216\n" \
"v_mfma_f32_16x16x32_bf16 a[44:47], a[200:203], a[236:239], a[44:47]\n" \
"v_mfma_f32_16x16x32_bf16 a[60:63], a[140:143], a[172:175], a[60:63]\n" \
"global_store_dwordx4 v49, v[0:3], s[28:29] offset:0 sc1\n" \
"v_mfma_f32_16x16x32_bf16 a[60:63], a[204:207], a[236:239], a[60:63]\n" \
"v_mfma_f32_16x16x32_bf16 a[56:59], a[140:143], a[168:171], a[56:59]\n" \
"ds_read_b64_tr_b16 a[184:185], v38 offset:0\n" \
"v_mfma_f32_16x16x32_bf16 a[56:59], a[204:207], a[232:235], a[56:59]\n" \
"v_mfma_f32_16x16x32_bf16 a[52:55], a[140:143], a[164:167], a[52:55]\n" \
"v_mfma_f32_16x16x32_bf16 a[52:55], a[204:207], a[228:231], a[52:55]\n" \
"ds_read_b64_tr_b16 a[186:187], v38 offset:1024\n" \
"v_mfma_f32_16x16x32_bf16 a[48:51], a[140:143], a[160:163], a[48:51]\n" \
"v_mfma_f32_16x16x32_bf16 a[48:51], a[204:207], a[224:227], a[48:51]\n" \
"v_mfma_f32_16x16x32_bf16 a[64:67], a[144:147], a[160:163], a[64:67]\n" \
"global_store_dwordx4 v49, v[0:3], s[28:29] offset:1024 sc1\n" \
"v_mfma_f32_16x16x32_bf16 a[64:67], a[208:211], a[224:227], a[64:67]\n" \
"ds_read_b64_tr_b16 a[248:249], v38 offset:8192\n" \
"v_mfma_f32_16x16x32_bf16 a[68:71], a[144:147], a[164:167], a[68:71]\n" \
"v_mfma_f32_16x16x32_bf16 a[68:71], a[208:211], a[228:231], a[68:71]\n" \
"ds_read_b64_tr_b16 a[250:251], v38 offset:9216\n" \
"v_mfma_f32_16x16x32_bf16 a[72:75], a[144:147], a[168:171], a[72:75]\n" \
"v_mfma_f32_16x16x32_bf16 a[72:75], a[208:211], a[232:235], a[72:75]\n" \
"v_mfma_f32_16x16x32_bf16 a[76:79], a[144:147], a[172:175], a[76:79]\n" \
"v_mfma_f32_16x16x32_bf16 a[76:79], a[208:211], a[236:239], a[76:79]\n" \
"ds_read_b64_tr_b16 a[188:189], v39 offset:0\n" \
"v_mfma_f32_16x16x32_bf16 a[92:95], a[148:151], a[172:175], a[92:95]\n" \
"s_mov_b64 exec, 0xffff\n" \
"global_store_dwordx4 v49, v[0:3], s[28:29] offset:2048 sc1\n" \
"s_mov_b64 exec, -1\n" \
"s_nop 0\n" \
"v_mfma_f32_16x16x32_bf16 a[92:95], a[212:215], a[236:239], a[92:95]\n" \
"v_mfma_f32_16x16x32_bf16 a[88:91], a[148:151], a[168:171], a[88:91]\n" \
"ds_read_b64_tr_b16 a[190:191], v39 offset:1024\n" \
"v_mfma_f32_16x16x32_bf16 a[88:91], a[212:215], a[232:235], a[88:91]\n" \
"v_mfma_f32_16x16x32_bf16 a[84:87], a[148:151], a[164:167], a[84:87]\n" \
"v_mfma_f32_16x16x32_bf16 a[84:87], a[212:215], a[228:231], a[84:87]\n" \
"ds_read_b64_tr_b16 a[252:253], v39 offset:8192\n" \
"v_mfma_f32_16x16x32_bf16 a[80:83], a[148:151], a[160:163], a[80:83]\n" \
"v_mfma_f32_16x16x32_bf16 a[80:83], a[212:215], a[224:227], a[80:83]\n" \
"v_mfma_f32_16x16x32_bf16 a[96:99], a[152:155], a[160:163], a[96:99]\n" \
"ds_read_b64_tr_b16 a[254:255], v39 offset:9216\n" \
"v_mfma_f32_16x16x32_bf16 a[96:99], a[216:219], a[224:227], a[96:99]\n" \
"v_mfma_f32_16x16x32_bf16 a[100:103], a[152:155], a[164:167], a[100:103]\n" \
"v_mfma_f32_16x16x32_bf16 a[100:103], a[216:219], a[228:231], a[100:103]\n" \
"v_mfma_f32_16x16x32_bf16 a[104:107], a[152:155], a[168:171], a[104:107]\n" \
"v_mfma_f32_16x16x32_bf16 a[104:107], a[216:219], a[232:235], a[104:107]\n" \
"v_mfma_f32_16x16x32_bf16 a[108:111], a[152:155], a[172:175], a[108:111]\n" \
"v_mfma_f32_16x16x32_bf16 a[108:111], a[216:219], a[236:239], a[108:111]\n" \
"s_waitcnt lgkmcnt(15)\n" \
"v_mfma_f32_16x16x32_bf16 a[124:127], a[156:159], a[172:175], a[124:127]\n" \
"v_mfma_f32_16x16x32_bf16 a[124:127], a[220:223], a[236:239], a[124:127]\n" \
"v_mfma_f32_16x16x32_bf16 a[120:123], a[156:159], a[168:171], a[120:123]\n" \
"v_mfma_f32_16x16x32_bf16 a[120:123], a[220:223], a[232:235], a[120:123]\n" \
"v_mfma_f32_16x16x32_bf16 a[116:119], a[156:159], a[164:167], a[116:119]\n" \
"v_mfma_f32_16x16x32_bf16 a[116:119], a[220:223], a[228:231], a[116:119]\n" \
"v_mfma_f32_16x16x32_bf16 a[112:115], a[156:159], a[160:163], a[112:115]\n" \
"v_mfma_f32_16x16x32_bf16 a[112:115], a[220:223], a[224:227], a[112:115]\n" \
"s_waitcnt vmcnt(3) lgkmcnt(0)\n" \
"s_barrier\n" \
"v_cmp_ne_u32_e32 vcc, s62, v44\n" \
"s_nop 4\n" \
"s_cbranch_vccnz L_poll61_%=\n" \
"L_pollr61_%=:\n" \
"s_mov_b32 s68, s12\n" \
"s_and_b32 s69, s13, 0xffff\n" \
"s_mov_b32 s70, 0x480000\n" \
"s_mov_b32 s71, 0x20000\n" \
"v_mfma_f32_16x16x32_bf16 v[52:55], a[128:131], a[176:179], v[52:55]\n" \
"s_add_u32 m0, s40, 0x0\n" \
"s_add_u32 s85, s39, 0x0\n" \
"s_nop 0\n" \
"v_mfma_f32_16x16x32_bf16 v[52:55], a[192:195], a[240:243], v[52:55]\n" \
"buffer_load_dwordx4 v20, s[80:83], s85 offen sc1 lds\n" \
"v_mfma_f32_16x16x32_bf16 v[56:59], a[128:131], a[180:183], v[56:59]\n" \
"ds_read_b64_tr_b16 a[160:161], v32 offset:32768\n" \
"v_mfma_f32_16x16x32_bf16 v[56:59], a[192:195], a[244:247], v[56:59]\n" \
"s_add_u32 m0, s40, 0x1000\n" \
"s_add_u32 s85, s39, 0x1000\n" \
".fill (L_pb + 57056 - .) / 4, 4, 0xBF800000\n" \
"L_pp14:\n" \
"s_cbranch_execz L_pret\n" \
"s_nop 0\n" \
"v_mfma_f32_16x16x32_bf16 v[60:63], a[128:131], a[184:187], v[60:63]\n" \
"buffer_load_dwordx4 v20, s[80:83], s85 offen sc1 lds\n" \
"v_mfma_f32_16x16x32_bf16 v[60:63], a[192:195], a[248:251], v[60:63]\n" \
"ds_read_b64_tr_b16 a[162:163], v32 offset:33792\n" \
"v_mfma_f32_16x16x32_bf16 v[64:67], a[128:131], a[188:191], v[64:67]\n" \
"ds_read_b64_tr_b16 a[224:225], v32 offset:40960\n" \
"v_mfma_f32_16x16x32_bf16 v[64:67], a[192:195], a[252:255], v[64:67]\n" \
"ds_read_b128 a[128:131], v28 offset:49152\n" \
"v_mfma_f32_16x16x32_bf16 v[80:83], a[132:135], a[188:191], v[80:83]\n" \
"ds_read_b128 a[192:195], v29 offset:49152\n" \
"v_mfma_f32_16x16x32_bf16 v[80:83], a[196:199], a[252:255], v[80:83]\n" \
"s_add_u32 m0, s40, 0x3000\n" \
"s_movk_i32 s84, 0x0\n" \
"v_mfma_f32_16x16x32_bf16 v[76:79], a[132:135], a[184:187], v[76:79]\n" \
"buffer_load_dwordx4 v21, s[76:79], s84 offen offset:0 lds\n" \
"v_mfma_f32_16x16x32_bf16 v[76:79], a[196:199], a[248:251], v[76:79]\n" \
"s_add_u32 m0, s30, 0x0\n" \
"s_movk_i32 s84, 0x0\n" \
"s_nop 0\n" \
"v_mfma_f32_16x16x32_bf16 v[72:75], a[132:135], a[180:183], v[72:75]\n" \
"buffer_load_dwordx4 v24, s[76:79], s84 offen offset:0 lds\n" \
"v_mfma_f32_16x16x32_bf16 v[72:75], a[196:199], a[244:247], v[72:75]\n" \
"ds_read_b64_tr_b16 a[226:227], v32 offset:41984\n" \
"v_mfma_f32_16x16x32_bf16 v[68:71], a[132:135], a[176:179], v[68:71]\n" \
"ds_read_b64_tr_b16 a[164:165], v33 offset:32768\n" \
"v_mfma_f32_16x16x32_bf16 v[68:71], a[196:199], a[240:243], v[68:71]\n" \
"ds_read_b128 a[132:135], v28 offset:51200\n" \
"v_mfma_f32_16x16x32_bf16 v[84:87], a[136:139], a[176:179], v[84:87]\n" \
"ds_read_b128 a[196:199], v29 offset:51200\n" \
"v_mfma_f32_16x16x32_bf16 v[84:87], a[200:203], a[240:243], v[84:87]\n" \
"s_movk_i32 s84, 0x2000\n" \
"v_mfma_f32_16x16x32_bf16 v[88:91], a[136:139], a[180:183], v[88:91]\n" \
"buffer_load_dwordx4 v25, s[76:79], s84 offen offset:1024 lds\n" \
"v_mfma_f32_16x16x32_bf16 v[88:91], a[200:203], a[244:247], v[88:91]\n" \
"s_movk_i32 s84, 0x4000\n" \
"v_mfma_f32_16x16x32_bf16 v[92:95], a[136:139], a[184:187], v[92:95]\n" \
"buffer_load_dwordx4 v24, s[76:79], s84 offen offset:2048 lds\n" \
"v_mfma_f32_16x16x32_bf16 v[92:95], a[200:203], a[248:251], v[92:95]\n" \
"ds_read_b64_tr_b16 a[166:167], v33 offset:33792\n" \
"v_mfma_f32_16x16x32_bf16 v[96:99], a[136:139], a[188:191], v[96:99]\n" \
"ds_read_b64_tr_b16 a[228:229], v33 offset:40960\n" \
"v_mfma_f32_16x16x32_bf16 v[96:99], a[200:203], a[252:255], v[96:99]\n" \
"ds_read_b128 a[136:139], v28 offset:53248\n" \
"v_mfma_f32_16x16x32_bf16 v[112:115], a[140:143], a[188:191], v[112:115]\n" \
"ds_read_b128 a[200:203], v29 offset:53248\n" \
"v_mfma_f32_16x16x32_bf16 v[112:115], a[204:207], a[252:255], v[112:115]\n" \
"s_movk_i32 s84, 0x6000\n" \
"v_mfma_f32_16x16x32_bf16 v[108:111], a[140:143], a[184:187], v[108:111]\n" \
"buffer_load_dwordx4 v25, s[76:79], s84 offen offset:3072 lds\n" \
"v_mfma_f32_16x16x32_bf16 v[108:111], a[204:207], a[248:251], v[108:111]\n" \
"s_add_u32 m0, s31, 0x0\n" \
"s_movk_i32 s84, 0x0\n" \
"v_mfma_f32_16x16x32_bf16 v[104:107], a[140:143], a[180:183], v[104:107]\n" \
"buffer_load_dwordx4 v26, s[68:71], s84 offen offset:0 lds\n" \
"v_mfma_f32_16x16x32_bf16 v[104:107], a[204:207], a[244:247], v[104:107]\n" \
"s_movk_i32 s84, 0x7c00\n" \
"v_mfma_f32_16x16x32_bf16 v[100:103], a[140:143], a[176:179], v[100:103]\n" \
"buffer_load_dwordx4 v26, s[68:71], s84 offen offset:1024 lds\n" \
"v_mfma_f32_16x16x32_bf16 v[100:103], a[204:207], a[240:243], v[100:103]\n" \
"ds_read_b128 a[140:143], v28 offset:55296\n" \
"v_mfma_f32_16x16x32_bf16 v[116:119], a[144:147], a[176:179], v[116:119]\n" \
"ds_read_b128 a[204:207], v29 offset:55296\n" \
"v_mfma_f32_16x16x32_bf16 v[116:119], a[208:211], a[240:243], v[116:119]\n" \
"s_addk_i32 s84, 0x7c00\n" \
"v_mfma_f32_16x16x32_bf16 v[120:123], a[144:147], a[180:183], v[120:123]\n" \
"buffer_load_dwordx4 v27, s[68:71], s84 offen offset:2048 lds\n" \
"v_mfma_f32_16x16x32_bf16 v[120:123], a[208:211], a[244:247], v[120:123]\n" \
"ds_read_b64_tr_b16 a[230:231], v33 offset:41984\n" \
"v_mfma_f32_16x16x32_bf16 v[124:127], a[144:147], a[184:187], v[124:127]\n" \
"s_addk_i32 s84, 0x7c00\n" \
"v_mfma_f32_16x16x32_bf16 v[124:127], a[208:211], a[248:251], v[124:127]\n" \
"buffer_load_dwordx4 v27, s[68:71], s84 offen offset:3072 lds\n" \
"v_mfma_f32_16x16x32_bf16 v[128:131], a[144:147], a[188:191], v[128:131]\n" \
"ds_read_b64_tr_b16 a[168:169], v34 offset:32768\n" \
"v_mfma_f32_16x16x32_bf16 v[128:131], a[208:211], a[252:255], v[128:131]\n" \
"ds_read_b128 a[144:147], v28 offset:32768\n" \
"v_mfma_f32_16x16x32_bf16 v[144:147], a[148:151], a[188:191], v[144:147]\n" \
"ds_read_b128 a[208:211], v29 offset:32768\n" \
"v_mfma_f32_16x16x32_bf16 v[144:147], a[212:215], a[252:255], v[144:147]\n" \
"s_add_u32 m0, s30, 0x1000\n" \
"s_mov_b32 s84, 0x9000\n" \
"v_mfma_f32_16x16x32_bf16 v[140:143], a[148:151], a[184:187], v[140:143]\n" \
"buffer_load_dwordx4 v24, s[76:79], s84 offen offset:0 lds\n" \
"v_mfma_f32_16x16x32_bf16 v[140:143], a[212:215], a[248:251], v[140:143]\n" \
"ds_read_b64_tr_b16 a[170:171], v34 offset:33792\n" \
"v_mfma_f32_16x16x32_bf16 v[136:139], a[148:151], a[180:183], v[136:139]\n" \
"s_addk_i32 s84, 0x2000\n" \
"s_nop 0\n" \
"v_mfma_f32_16x16x32_bf16 v[136:139], a[212:215], a[244:247], v[136:139]\n" \
"buffer_load_dwordx4 v25, s[76:79], s84 offen offset:1024 lds\n" \
"v_mfma_f32_16x16x32_bf16 v[132:135], a[148:151], a[176:179], v[132:135]\n" \
"ds_read_b64_tr_b16 a[232:233], v34 offset:40960\n" \
"v_mfma_f32_16x16x32_bf16 v[132:135], a[212:215], a[240:243], v[132:135]\n" \
"ds_read_b128 a[148:151], v28 offset:34816\n" \
"v_mfma_f32_16x16x32_bf16 v[148:151], a[152:155], a[176:179], v[148:151]\n" \
"ds_read_b128 a[212:215], v29 offset:34816\n" \
"v_mfma_f32_16x16x32_bf16 v[148:151], a[216:219], a[240:243], v[148:151]\n" \
"ds_read_b64_tr_b16 a[234:235], v34 offset:41984\n" \
"v_mfma_f32_16x16x32_bf16 v[152:155], a[152:155], a[180:183], v[152:155]\n" \
"ds_read_b64_tr_b16 a[172:173], v35 offset:32768\n" \
"v_mfma_f32_16x16x32_bf16 v[152:155], a[216:219], a[244:247], v[152:155]\n" \
"ds_read_b64_tr_b16 a[174:175], v35 offset:33792\n" \
"v_mfma_f32_16x16x32_bf16 v[156:159], a[152:155], a[184:187], v[156:159]\n" \
"ds_read_b64_tr_b16 a[236:237], v35 offset:40960\n" \
"v_mfma_f32_16x16x32_bf16 v[156:159], a[216:219], a[248:251], v[156:159]\n" \
"ds_read_b64_tr_b16 a[238:239], v35 offset:41984\n" \
"v_mfma_f32_16x16x32_bf16 v[160:163], a[152:155], a[188:191], v[160:163]\n" \
"v_mfma_f32_16x16x32_bf16 v[160:163], a[216:219], a[252:255], v[160:163]\n" \
"ds_read_b128 a[152:155], v28 offset:36864\n" \
"v_mfma_f32_16x16x32_bf16 v[176:179], a[156:159], a[188:191], v[176:179]\n" \
"ds_read_b128 a[216:219], v29 offset:36864\n" \
"v_mfma_f32_16x16x32_bf16 v[176:179], a[220:223], a[252:255], v[176:179]\n" \
"v_mfma_f32_16x16x32_bf16 v[172:175], a[156:159], a[184:187], v[172:175]\n" \
"v_mfma_f32_16x16x32_bf16 v[172:175], a[220:223], a[248:251], v[172:175]\n" \
"v_mfma_f32_16x16x32_bf16 v[168:171], a[156:159], a[180:183], v[168:171]\n" \
"v_mfma_f32_16x16x32_bf16 v[168:171], a[220:223], a[244:247], v[168:171]\n" \
"v_mfma_f32_16x16x32_bf16 v[164:167], a[156:159], a[176:179], v[164:167]\n" \
"v_mfma_f32_16x16x32_bf16 v[164:167], a[220:223], a[240:243], v[164:167]\n" \
"ds_read_b128 a[156:159], v28 offset:38912\n" \
"ds_read_b128 a[220:223], v29 offset:38912\n" \
"s_waitcnt lgkmcnt(15)\n" \
"v_mfma_f32_16x16x32_bf16 a[0:3], a[128:131], a[160:163], a[0:3]\n" \
"s_addk_i32 s84, 0x2000\n" \
"v_mfma_f32_16x16x32_bf16 a[0:3], a[192:195], a[224:227], a[0:3]\n" \
"buffer_load_dwordx4 v24, s[76:79], s84 offen offset:2048 lds\n" \
"v_mfma_f32_16x16x32_bf16 a[4:7], a[128:131], a[164:167], a[4:7]\n" \
"ds_read_b64_tr_b16 a[176:177], v36 offset:32768\n" \
"v_mfma_f32_16x16x32_bf16 a[4:7], a[192:195], a[228:231], a[4:7]\n" \
"s_addk_i32 s84, 0x2000\n" \
"s_waitcnt lgkmcnt(13)\n" \
"v_mfma_f32_16x16x32_bf16 a[8:11], a[128:131], a[168:171], a[8:11]\n" \
"buffer_load_dwordx4 v25, s[76:79], s84 offen offset:3072 lds\n" \
"s_waitcnt lgkmcnt(9)\n" \
"v_mfma_f32_16x16x32_bf16 a[8:11], a[192:195], a[232:235], a[8:11]\n" \
"ds_read_b64_tr_b16 a[178:179], v36 offset:33792\n" \
"s_waitcnt lgkmcnt(8)\n" \
"v_mfma_f32_16x16x32_bf16 a[12:15], a[128:131], a[172:175], a[12:15]\n" \
"ds_read_b64_tr_b16 a[240:241], v36 offset:40960\n" \
"s_waitcnt lgkmcnt(7)\n" \
"v_mfma_f32_16x16x32_bf16 a[12:15], a[192:195], a[236:239], a[12:15]\n" \
"v_mfma_f32_16x16x32_bf16 a[28:31], a[132:135], a[172:175], a[28:31]\n" \
"v_mfma_f32_16x16x32_bf16 a[28:31], a[196:199], a[236:239], a[28:31]\n" \
"s_add_u32 m0, s30, 0x20000\n" \
"s_movk_i32 s84, 0x100\n" \
"v_mfma_f32_16x16x32_bf16 a[24:27], a[132:135], a[168:171], a[24:27]\n" \
"buffer_load_dwordx4 v24, s[76:79], s84 offen offset:0 lds\n" \
"v_mfma_f32_16x16x32_bf16 a[24:27], a[196:199], a[232:235], a[24:27]\n" \
"ds_read_b64_tr_b16 a[242:243], v36 offset:41984\n" \
"v_mfma_f32_16x16x32_bf16 a[20:23], a[132:135], a[164:167], a[20:23]\n" \
"v_accvgpr_read_b32 v184, a12\n" \
"v_accvgpr_read_b32 v185, a13\n" \
"v_mfma_f32_16x16x32_bf16 a[20:23], a[196:199], a[228:231], a[20:23]\n" \
"ds_read_b64_tr_b16 a[180:181], v37 offset:32768\n" \
"v_accvgpr_read_b32 v186, a14\n" \
"v_mfma_f32_16x16x32_bf16 a[16:19], a[132:135], a[160:163], a[16:19]\n" \
"s_movk_i32 s84, 0x2100\n" \
"s_nop 0\n" \
"v_mfma_f32_16x16x32_bf16 a[16:19], a[196:199], a[224:227], a[16:19]\n" \
"buffer_load_dwordx4 v25, s[76:79], s84 offen offset:1024 lds\n" \
"v_mfma_f32_16x16x32_bf16 a[32:35], a[136:139], a[160:163], a[32:35]\n" \
"ds_read_b64_tr_b16 a[182:183], v37 offset:33792\n" \
"v_accvgpr_read_b32 v187, a15\n" \
"v_mfma_f32_16x16x32_bf16 a[32:35], a[200:203], a[224:227], a[32:35]\n" \
"v_accvgpr_read_b32 v180, a28\n" \
"v_accvgpr_read_b32 v181, a29\n" \
"v_mfma_f32_16x16x32_bf16 a[36:39], a[136:139], a[164:167], a[36:39]\n" \
"v_accvgpr_read_b32 v182, a30\n" \
"v_accvgpr_read_b32 v183, a31\n" \
"v_mfma_f32_16x16x32_bf16 a[36:39], a[200:203], a[228:231], a[36:39]\n" \
"s_movk_i32 s84, 0x4100\n" \
"s_nop 0\n" \
"v_mfma_f32_16x16x32_bf16 a[40:43], a[136:139], a[168:171], a[40:43]\n" \
"buffer_load_dwordx4 v24, s[76:79], s84 offen offset:2048 lds\n" \
"v_mfma_f32_16x16x32_bf16 a[40:43], a[200:203], a[232:235], a[40:43]\n" \
"ds_read_b64_tr_b16 a[244:245], v37 offset:40960\n" \
"v_cvt_pk_bf16_f32 v184, v184, v185\n" \
"v_mfma_f32_16x16x32_bf16 a[44:47], a[136:139], a[172:175], a[44:47]\n" \
"ds_read_b64_tr_b16 a[246:247], v37 offset:41984\n" \
"v_cvt_pk_bf16_f32 v185, v186, v187\n" \
"v_mfma_f32_16x16x32_bf16 a[44:47], a[200:203], a[236:239], a[44:47]\n" \
"v_cvt_pk_bf16_f32 v186, v180, v181\n" \
"v_cvt_pk_bf16_f32 v187, v182, v183\n" \
"v_mfma_f32_16x16x32_bf16 a[60:63], a[140:143], a[172:175], a[60:63]\n" \
"s_movk_i32 s84, 0x6100\n" \
"v_mfma_f32_16x16x32_bf16 a[60:63], a[204:207], a[236:239], a[60:63]\n" \
"buffer_load_dwordx4 v25, s[76:79], s84 offen offset:3072 lds\n" \
"v_mfma_f32_16x16x32_bf16 a[56:59], a[140:143], a[168:171], a[56:59]\n" \
"ds_read_b64_tr_b16 a[184:185], v38 offset:32768\n" \
"v_accvgpr_read_b32 v188, a8\n" \
"v_mfma_f32_16x16x32_bf16 a[56:59], a[204:207], a[232:235], a[56:59]\n" \
"v_accvgpr_read_b32 v189, a9\n" \
"v_accvgpr_read_b32 v190, a10\n" \
"v_mfma_f32_16x16x32_bf16 a[52:55], a[140:143], a[164:167], a[52:55]\n" \
"s_add_u32 m0, s30, 0x21000\n" \
"s_addk_i32 s84, 0x3000\n" \
"v_mfma_f32_16x16x32_bf16 a[52:55], a[204:207], a[228:231], a[52:55]\n" \
"buffer_load_dwordx4 v24, s[76:79], s84 offen offset:0 lds\n" \
"v_mfma_f32_16x16x32_bf16 a[48:51], a[140:143], a[160:163], a[48:51]\n" \
"ds_read_b64_tr_b16 a[186:187], v38 offset:33792\n" \
"v_accvgpr_read_b32 v191, a11\n" \
"v_mfma_f32_16x16x32_bf16 a[48:51], a[204:207], a[224:227], a[48:51]\n" \
"v_accvgpr_read_b32 v180, a24\n" \
"v_accvgpr_read_b32 v181, a25\n" \
"v_mfma_f32_16x16x32_bf16 a[64:67], a[144:147], a[160:163], a[64:67]\n" \
"ds_read_b64_tr_b16 a[248:249], v38 offset:40960\n" \
"v_accvgpr_read_b32 v182, a26\n" \
"v_mfma_f32_16x16x32_bf16 a[64:67], a[208:211], a[224:227], a[64:67]\n" \
"s_addk_i32 s84, 0x2000\n" \
"s_nop 0\n" \
"v_mfma_f32_16x16x32_bf16 a[68:71], a[144:147], a[164:167], a[68:71]\n" \
"buffer_load_dwordx4 v25, s[76:79], s84 offen offset:1024 lds\n" \
"v_mfma_f32_16x16x32_bf16 a[68:71], a[208:211], a[228:231], a[68:71]\n" \
"ds_read_b64_tr_b16 a[250:251], v38 offset:41984\n" \
"v_accvgpr_read_b32 v183, a27\n" \
"v_mfma_f32_16x16x32_bf16 a[72:75], a[144:147], a[168:171], a[72:75]\n" \
"v_cvt_pk_bf16_f32 v188, v188, v189\n" \
"v_cvt_pk_bf16_f32 v189, v190, v191\n" \
"v_mfma_f32_16x16x32_bf16 a[72:75], a[208:211], a[232:235], a[72:75]\n" \
"v_cvt_pk_bf16_f32 v190, v180, v181\n" \
"v_cvt_pk_bf16_f32 v191, v182, v183\n" \
"v_mfma_f32_16x16x32_bf16 a[76:79], a[144:147], a[172:175], a[76:79]\n" \
"s_addk_i32 s84, 0x2000\n" \
"v_mfma_f32_16x16x32_bf16 a[76:79], a[208:211], a[236:239], a[76:79]\n" \
"buffer_load_dwordx4 v24, s[76:79], s84 offen offset:2048 lds\n" \
"v_mfma_f32_16x16x32_bf16 a[92:95], a[148:151], a[172:175], a[92:95]\n" \
"s_add_u32 s89, s41, 0x0\n" \
"buffer_store_dwordx4 v[184:187], v20, s[80:83], s89 offen offset:3072 sc1\n" \
"v_mfma_f32_16x16x32_bf16 a[92:95], a[212:215], a[236:239], a[92:95]\n" \
"ds_read_b64_tr_b16 a[188:189], v39 offset:32768\n" \
"v_accvgpr_read_b32 v192, a4\n" \
"v_mfma_f32_16x16x32_bf16 a[88:91], a[148:151], a[168:171], a[88:91]\n" \
"ds_read_b64_tr_b16 a[190:191], v39 offset:33792\n" \
"v_accvgpr_read_b32 v193, a5\n" \
"v_mfma_f32_16x16x32_bf16 a[88:91], a[212:215], a[232:235], a[88:91]\n" \
"s_addk_i32 s84, 0x2000\n" \
"v_mfma_f32_16x16x32_bf16 a[84:87], a[148:151], a[164:167], a[84:87]\n" \
"buffer_load_dwordx4 v25, s[76:79], s84 offen offset:3072 lds\n" \
"v_mfma_f32_16x16x32_bf16 a[84:87], a[212:215], a[228:231], a[84:87]\n" \
"ds_read_b64_tr_b16 a[252:253], v39 offset:40960\n" \
"v_accvgpr_read_b32 v194, a6\n" \
"v_mfma_f32_16x16x32_bf16 a[80:83], a[148:151], a[160:163], a[80:83]\n" \
"s_add_u32 s88, s41, 0x0\n" \
"buffer_store_dwordx4 v[188:191], v20, s[80:83], s88 offen offset:2048 sc1\n" \
"v_mfma_f32_16x16x32_bf16 a[80:83], a[212:215], a[224:227], a[80:83]\n" \
"v_accvgpr_read_b32 v195, a7\n" \
"v_accvgpr_read_b32 v180, a20\n" \
"s_waitcnt lgkmcnt(15)\n" \
"s_nop 0\n" \
"v_mfma_f32_16x16x32_bf16 a[96:99], a[152:155], a[160:163], a[96:99]\n" \
"ds_read_b64_tr_b16 a[254:255], v39 offset:41984\n" \
"v_accvgpr_read_b32 v181, a21\n" \
"v_mfma_f32_16x16x32_bf16 a[96:99], a[216:219], a[224:227], a[96:99]\n" \
"v_accvgpr_read_b32 v182, a22\n" \
"v_accvgpr_read_b32 v183, a23\n" \
"v_mfma_f32_16x16x32_bf16 a[100:103], a[152:155], a[164:167], a[100:103]\n" \
"v_cvt_pk_bf16_f32 v192, v192, v193\n" \
"v_cvt_pk_bf16_f32 v193, v194, v195\n" \
"v_mfma_f32_16x16x32_bf16 a[100:103], a[216:219], a[228:231], a[100:103]\n" \
"v_cvt_pk_bf16_f32 v194, v180, v181\n" \
"v_cvt_pk_bf16_f32 v195, v182, v183\n" \
"v_mfma_f32_16x16x32_bf16 a[104:107], a[152:155], a[168:171], a[104:107]\n" \
"s_add_u32 s89, s41, 0x0\n" \
"s_nop 0\n" \
"buffer_store_dwordx4 v[192:195], v20, s[80:83], s89 offen offset:1024 sc1\n" \
"v_mfma_f32_16x16x32_bf16 a[104:107], a[216:219], a[232:235], a[104:107]\n" \
"v_accvgpr_read_b32 v196, a0\n" \
"v_accvgpr_read_b32 v197, a1\n" \
"v_mfma_f32_16x16x32_bf16 a[108:111], a[152:155], a[172:175], a[108:111]\n" \
"v_accvgpr_read_b32 v198, a2\n" \
"v_accvgpr_read_b32 v199, a3\n" \
"v_mfma_f32_16x16x32_bf16 a[108:111], a[216:219], a[236:239], a[108:111]\n" \
"v_accvgpr_read_b32 v180, a16\n" \
"v_accvgpr_read_b32 v181, a17\n" \
"v_mfma_f32_16x16x32_bf16 a[124:127], a[156:159], a[172:175], a[124:127]\n" \
"v_accvgpr_read_b32 v182, a18\n" \
"v_accvgpr_read_b32 v183, a19\n" \
"v_mfma_f32_16x16x32_bf16 a[124:127], a[220:223], a[236:239], a[124:127]\n" \
"v_cvt_pk_bf16_f32 v196, v196, v197\n" \
"v_cvt_pk_bf16_f32 v197, v198, v199\n" \
"v_mfma_f32_16x16x32_bf16 a[120:123], a[156:159], a[168:171], a[120:123]\n" \
"v_cvt_pk_bf16_f32 v198, v180, v181\n" \
"v_cvt_pk_bf16_f32 v199, v182, v183\n" \
"s_add_u32 s88, s41, 0x0\n" \
"s_nop 0\n" \
"buffer_store_dwordx4 v[196:199], v20, s[80:83], s88 offen offset:0 sc1\n" \
"v_mfma_f32_16x16x32_bf16 a[120:123], a[220:223], a[232:235], a[120:123]\n" \
"v_accvgpr_read_b32 v200, a44\n" \
"v_accvgpr_read_b32 v201, a45\n" \
"v_mfma_f32_16x16x32_bf16 a[116:119], a[156:159], a[164:167], a[116:119]\n" \
"v_accvgpr_read_b32 v202, a46\n" \
"v_accvgpr_read_b32 v203, a47\n" \
"v_mfma_f32_16x16x32_bf16 a[116:119], a[220:223], a[228:231], a[116:119]\n" \
"v_accvgpr_read_b32 v180, a60\n" \
"v_accvgpr_read_b32 v181, a61\n" \
"v_mfma_f32_16x16x32_bf16 a[112:115], a[156:159], a[160:163], a[112:115]\n" \
"v_accvgpr_read_b32 v182, a62\n" \
"v_accvgpr_read_b32 v183, a63\n" \
"v_mfma_f32_16x16x32_bf16 a[112:115], a[220:223], a[224:227], a[112:115]\n" \
"v_cvt_pk_bf16_f32 v200, v200, v201\n" \
"v_cvt_pk_bf16_f32 v201, v202, v203\n" \
"s_waitcnt vmcnt(12) lgkmcnt(0)\n" \
"s_barrier\n" \
"v_xor_b32_e32 v36, 0x4000, v36\n" \
"v_xor_b32_e64 v32, 0x0, v48\n" \
"v_xor_b32_e32 v37, 0x4000, v37\n" \
"v_xor_b32_e64 v33, 0x20, v48\n" \
"v_xor_b32_e32 v38, 0x4000, v38\n" \
"v_xor_b32_e64 v34, 0x40, v48\n" \
"v_xor_b32_e32 v39, 0x4000, v39\n" \
"v_xor_b32_e32 v35, 0x60, v48\n" \
"v_add_u32_e32 v28, s43, v28\n" \
"v_add_u32_e32 v29, s43, v29\n" \
"v_mfma_f32_16x16x32_bf16 v[116:119], a[144:147], a[176:179], v[116:119]\n" \
"s_add_u32 m0, s40, 0x8000\n" \
"s_add_u32 s85, s39, 0x2000\n" \
"v_mfma_f32_16x16x32_bf16 v[116:119], a[208:211], a[240:243], v[116:119]\n" \
"buffer_load_dwordx4 v20, s[80:83], s85 offen sc1 lds\n" \
"v_mfma_f32_16x16x32_bf16 v[120:123], a[144:147], a[180:183], v[120:123]\n" \
"ds_read_b64_tr_b16 a[160:161], v32 offset:0\n" \
"v_cvt_pk_bf16_f32 v202, v180, v181\n" \
"v_mfma_f32_16x16x32_bf16 v[120:123], a[208:211], a[244:247], v[120:123]\n" \
"s_add_u32 m0, s40, 0x9000\n" \
"s_add_u32 s85, s39, 0x3000\n" \
"v_mfma_f32_16x16x32_bf16 v[124:127], a[144:147], a[184:187], v[124:127]\n" \
"buffer_load_dwordx4 v20, s[80:83], s85 offen sc1 lds\n" \
"v_mfma_f32_16x16x32_bf16 v[124:127], a[208:211], a[248:251], v[124:127]\n" \
"ds_read_b64_tr_b16 a[162:163], v32 offset:512\n" \
"v_cvt_pk_bf16_f32 v203, v182, v183\n" \
"v_mfma_f32_16x16x32_bf16 v[128:131], a[144:147], a[188:191], v[128:131]\n" \
"ds_read_b64_tr_b16 a[224:225], v32 offset:4096\n" \
"v_accvgpr_read_b32 v204, a40\n" \
"v_mfma_f32_16x16x32_bf16 v[128:131], a[208:211], a[252:255], v[128:131]\n" \
"ds_read_b128 a[144:147], v28 offset:8192\n" \
"v_accvgpr_read_b32 v205, a41\n" \
"v_mfma_f32_16x16x32_bf16 v[144:147], a[148:151], a[188:191], v[144:147]\n" \
"ds_read_b128 a[208:211], v29 offset:8192\n" \
"v_accvgpr_read_b32 v206, a42\n" \
"v_mfma_f32_16x16x32_bf16 v[144:147], a[212:215], a[252:255], v[144:147]\n" \
"s_add_u32 m0, s40, 0xb000\n" \
"s_movk_i32 s84, 0x80\n" \
"v_mfma_f32_16x16x32_bf16 v[140:143], a[148:151], a[184:187], v[140:143]\n" \
"buffer_load_dwordx4 v21, s[76:79], s84 offen offset:0 lds\n" \
"v_mfma_f32_16x16x32_bf16 v[140:143], a[212:215], a[248:251], v[140:143]\n" \
"s_add_u32 m0, s30, 0x8000\n" \
"s_movk_i32 s84, 0x80\n" \
"v_mfma_f32_16x16x32_bf16 v[136:139], a[148:151], a[180:183], v[136:139]\n" \
"buffer_load_dwordx4 v24, s[76:79], s84 offen offset:0 lds\n" \
"v_mfma_f32_16x16x32_bf16 v[136:139], a[212:215], a[244:247], v[136:139]\n" \
"ds_read_b64_tr_b16 a[226:227], v32 offset:4608\n" \
"v_accvgpr_read_b32 v207, a43\n" \
"v_mfma_f32_16x16x32_bf16 v[132:135], a[148:151], a[176:179], v[132:135]\n" \
"ds_read_b64_tr_b16 a[164:165], v33 offset:0\n" \
"v_accvgpr_read_b32 v180, a56\n" \
"v_mfma_f32_16x16x32_bf16 v[132:135], a[212:215], a[240:243], v[132:135]\n" \
"ds_read_b128 a[148:151], v28 offset:10240\n" \
"v_accvgpr_read_b32 v181, a57\n" \
"v_mfma_f32_16x16x32_bf16 v[148:151], a[152:155], a[176:179], v[148:151]\n" \
"ds_read_b128 a[212:215], v29 offset:10240\n" \
"v_accvgpr_read_b32 v182, a58\n" \
"v_mfma_f32_16x16x32_bf16 v[148:151], a[216:219], a[240:243], v[148:151]\n" \
"s_movk_i32 s84, 0x2080\n" \
"v_mfma_f32_16x16x32_bf16 v[152:155], a[152:155], a[180:183], v[152:155]\n" \
"buffer_load_dwordx4 v25, s[76:79], s84 offen offset:1024 lds\n" \
"v_mfma_f32_16x16x32_bf16 v[152:155], a[216:219], a[244:247], v[152:155]\n" \
"s_movk_i32 s84, 0x4080\n" \
"v_mfma_f32_16x16x32_bf16 v[156:159], a[152:155], a[184:187], v[156:159]\n" \
"buffer_load_dwordx4 v24, s[76:79], s84 offen offset:2048 lds\n" \
"v_mfma_f32_16x16x32_bf16 v[156:159], a[216:219], a[248:251], v[156:159]\n" \
"ds_read_b64_tr_b16 a[166:167], v33 offset:512\n" \
"v_accvgpr_read_b32 v183, a59\n" \
"v_mfma_f32_16x16x32_bf16 v[160:163], a[152:155], a[188:191], v[160:163]\n" \
"ds_read_b64_tr_b16 a[228:229], v33 offset:4096\n" \
"v_cvt_pk_bf16_f32 v204, v204, v205\n" \
"v_mfma_f32_16x16x32_bf16 v[160:163], a[216:219], a[252:255], v[160:163]\n" \
"ds_read_b128 a[152:155], v28 offset:12288\n" \
"v_cvt_pk_bf16_f32 v205, v206, v207\n" \
"v_mfma_f32_16x16x32_bf16 v[176:179], a[156:159], a[188:191], v[176:179]\n" \
"ds_read_b128 a[216:219], v29 offset:12288\n" \
"v_cvt_pk_bf16_f32 v206, v180, v181\n" \
"v_mfma_f32_16x16x32_bf16 v[176:179], a[220:223], a[252:255], v[176:179]\n" \
"s_movk_i32 s84, 0x6080\n" \
"v_mfma_f32_16x16x32_bf16 v[172:175], a[156:159], a[184:187], v[172:175]\n" \
"buffer_load_dwordx4 v25, s[76:79], s84 offen offset:3072 lds\n" \
"v_mfma_f32_16x16x32_bf16 v[172:175], a[220:223], a[248:251], v[172:175]\n" \
"s_add_u32 m0, s31, 0x8000\n" \
"s_mov_b32 s84, 0x80000\n" \
"v_mfma_f32_16x16x32_bf16 v[168:171], a[156:159], a[180:183], v[168:171]\n" \
"buffer_load_dwordx4 v26, s[68:71], s84 offen offset:0 lds\n" \
"v_mfma_f32_16x16x32_bf16 v[168:171], a[220:223], a[244:247], v[168:171]\n" \
"s_addk_i32 s84, 0x7c00\n" \
"v_mfma_f32_16x16x32_bf16 v[164:167], a[156:159], a[176:179], v[164:167]\n" \
"buffer_load_dwordx4 v26, s[68:71], s84 offen offset:1024 lds\n" \
"v_mfma_f32_16x16x32_bf16 v[164:167], a[220:223], a[240:243], v[164:167]\n" \
"ds_read_b128 a[156:159], v28 offset:14336\n" \
"v_cvt_pk_bf16_f32 v207, v182, v183\n" \
"v_mfma_f32_16x16x32_bf16 v[84:87], a[136:139], a[176:179], v[84:87]\n" \
"ds_read_b128 a[220:223], v29 offset:14336\n" \
"v_accvgpr_read_b32 v208, a36\n" \
"v_mfma_f32_16x16x32_bf16 v[84:87], a[200:203], a[240:243], v[84:87]\n" \
"s_addk_i32 s84, 0x7c00\n" \
"v_mfma_f32_16x16x32_bf16 v[88:91], a[136:139], a[180:183], v[88:91]\n" \
"buffer_load_dwordx4 v27, s[68:71], s84 offen offset:2048 lds\n" \
"v_mfma_f32_16x16x32_bf16 v[88:91], a[200:203], a[244:247], v[88:91]\n" \
"ds_read_b64_tr_b16 a[230:231], v33 offset:4608\n" \
"v_accvgpr_read_b32 v209, a37\n" \
"v_mfma_f32_16x16x32_bf16 v[92:95], a[136:139], a[184:187], v[92:95]\n" \
"s_addk_i32 s84, 0x7c00\n" \
"v_mfma_f32_16x16x32_bf16 v[92:95], a[200:203], a[248:251], v[92:95]\n" \
"buffer_load_dwordx4 v27, s[68:71], s84 offen offset:3072 lds\n" \
"v_mfma_f32_16x16x32_bf16 v[96:99], a[136:139], a[188:191], v[96:99]\n" \
"ds_read_b64_tr_b16 a[172:173], v35 offset:0\n" \
"v_accvgpr_read_b32 v210, a38\n" \
"v_mfma_f32_16x16x32_bf16 v[96:99], a[200:203], a[252:255], v[96:99]\n" \
"ds_read_b128 a[136:139], v28 offset:4096\n" \
"v_accvgpr_read_b32 v211, a39\n" \
"v_mfma_f32_16x16x32_bf16 v[112:115], a[140:143], a[188:191], v[112:115]\n" \
"ds_read_b128 a[200:203], v29 offset:4096\n" \
"v_accvgpr_read_b32 v180, a52\n" \
"v_mfma_f32_16x16x32_bf16 v[112:115], a[204:207], a[252:255], v[112:115]\n" \
"s_add_u32 m0, s30, 0x9000\n" \
"s_mov_b32 s84, 0x9080\n" \
"v_mfma_f32_16x16x32_bf16 v[108:111], a[140:143], a[184:187], v[108:111]\n" \
"buffer_load_dwordx4 v24, s[76:79], s84 offen offset:0 lds\n" \
"v_mfma_f32_16x16x32_bf16 v[108:111], a[204:207], a[248:251], v[108:111]\n" \
"ds_read_b64_tr_b16 a[174:175], v35 offset:512\n" \
"v_accvgpr_read_b32 v181, a53\n" \
"v_mfma_f32_16x16x32_bf16 v[104:107], a[140:143], a[180:183], v[104:107]\n" \
"s_addk_i32 s84, 0x2000\n" \
"s_nop 0\n" \
"v_mfma_f32_16x16x32_bf16 v[104:107], a[204:207], a[244:247], v[104:107]\n" \
"buffer_load_dwordx4 v25, s[76:79], s84 offen offset:1024 lds\n" \
"v_mfma_f32_16x16x32_bf16 v[100:103], a[140:143], a[176:179], v[100:103]\n" \
"ds_read_b64_tr_b16 a[236:237], v35 offset:4096\n" \
"v_accvgpr_read_b32 v182, a54\n" \
"v_mfma_f32_16x16x32_bf16 v[100:103], a[204:207], a[240:243], v[100:103]\n" \
"ds_read_b128 a[140:143], v28 offset:6144\n" \
"v_accvgpr_read_b32 v183, a55\n" \
"v_mfma_f32_16x16x32_bf16 v[52:55], a[128:131], a[176:179], v[52:55]\n" \
"ds_read_b128 a[204:207], v29 offset:6144\n" \
"v_cvt_pk_bf16_f32 v208, v208, v209\n" \
"v_mfma_f32_16x16x32_bf16 v[52:55], a[192:195], a[240:243], v[52:55]\n" \
"ds_read_b64_tr_b16 a[238:239], v35 offset:4608\n" \
"v_cvt_pk_bf16_f32 v209, v210, v211\n" \
"v_mfma_f32_16x16x32_bf16 v[56:59], a[128:131], a[180:183], v[56:59]\n" \
"ds_read_b64_tr_b16 a[168:169], v34 offset:0\n" \
"v_cvt_pk_bf16_f32 v210, v180, v181\n" \
"v_mfma_f32_16x16x32_bf16 v[56:59], a[192:195], a[244:247], v[56:59]\n" \
"ds_read_b64_tr_b16 a[170:171], v34 offset:512\n" \
"v_cvt_pk_bf16_f32 v211, v182, v183\n" \
"v_mfma_f32_16x16x32_bf16 v[60:63], a[128:131], a[184:187], v[60:63]\n" \
"ds_read_b64_tr_b16 a[232:233], v34 offset:4096\n" \
"v_accvgpr_read_b32 v212, a32\n" \
"v_mfma_f32_16x16x32_bf16 v[60:63], a[192:195], a[248:251], v[60:63]\n" \
"ds_read_b64_tr_b16 a[234:235], v34 offset:4608\n" \
"v_accvgpr_read_b32 v213, a33\n" \
"v_mfma_f32_16x16x32_bf16 v[64:67], a[128:131], a[188:191], v[64:67]\n" \
"v_accvgpr_read_b32 v214, a34\n" \
"v_accvgpr_read_b32 v215, a35\n" \
"v_mfma_f32_16x16x32_bf16 v[64:67], a[192:195], a[252:255], v[64:67]\n" \
"ds_read_b128 a[128:131], v28 offset:0\n" \
"v_accvgpr_read_b32 v180, a48\n" \
"v_mfma_f32_16x16x32_bf16 v[80:83], a[132:135], a[188:191], v[80:83]\n" \
"ds_read_b128 a[192:195], v29 offset:0\n" \
"v_accvgpr_read_b32 v181, a49\n" \
"v_mfma_f32_16x16x32_bf16 v[80:83], a[196:199], a[252:255], v[80:83]\n" \
"v_accvgpr_read_b32 v182, a50\n" \
"v_accvgpr_read_b32 v183, a51\n" \
"v_mfma_f32_16x16x32_bf16 v[76:79], a[132:135], a[184:187], v[76:79]\n" \
"v_cvt_pk_bf16_f32 v212, v212, v213\n" \
"v_cvt_pk_bf16_f32 v213, v214, v215\n" \
"v_mfma_f32_16x16x32_bf16 v[76:79], a[196:199], a[248:251], v[76:79]\n" \
"v_cvt_pk_bf16_f32 v214, v180, v181\n" \
"v_cvt_pk_bf16_f32 v215, v182, v183\n" \
"v_mfma_f32_16x16x32_bf16 v[72:75], a[132:135], a[180:183], v[72:75]\n" \
"v_accvgpr_read_b32 v216, a68\n" \
"v_accvgpr_read_b32 v217, a69\n" \
"v_mfma_f32_16x16x32_bf16 v[72:75], a[196:199], a[244:247], v[72:75]\n" \
"v_accvgpr_read_b32 v218, a70\n" \
"v_accvgpr_read_b32 v219, a71\n" \
"v_mfma_f32_16x16x32_bf16 v[68:71], a[132:135], a[176:179], v[68:71]\n" \
"v_accvgpr_read_b32 v180, a84\n" \
"v_accvgpr_read_b32 v181, a85\n" \
"v_mfma_f32_16x16x32_bf16 v[68:71], a[196:199], a[240:243], v[68:71]\n" \
"ds_read_b128 a[132:135], v28 offset:2048\n" \
"ds_read_b128 a[196:199], v29 offset:2048\n" \
"s_waitcnt lgkmcnt(15)\n" \
"v_mfma_f32_16x16x32_bf16 a[0:3], a[160:163], a[144:147], 0\n" \
"s_addk_i32 s84, 0x2000\n" \
"v_accvgpr_read_b32 v182, a86\n" \
"v_mfma_f32_16x16x32_bf16 a[0:3], a[224:227], a[208:211], a[0:3]\n" \
"buffer_load_dwordx4 v24, s[76:79], s84 offen offset:2048 lds\n" \
"v_accvgpr_read_b32 v183, a87\n" \
"v_mfma_f32_16x16x32_bf16 a[4:7], a[164:167], a[144:147], 0\n" \
"ds_read_b64_tr_b16 a[176:177], v36 offset:0\n" \
"v_cvt_pk_bf16_f32 v216, v216, v217\n" \
"v_mfma_f32_16x16x32_bf16 a[4:7], a[228:231], a[208:211], a[4:7]\n" \
"s_addk_i32 s84, 0x2000\n" \
"v_cvt_pk_bf16_f32 v217, v218, v219\n" \
"s_waitcnt lgkmcnt(13)\n" \
"v_mfma_f32_16x16x32_bf16 a[12:15], a[172:175], a[144:147], 0\n" \
"buffer_load_dwordx4 v25, s[76:79], s84 offen offset:3072 lds\n" \
"v_cvt_pk_bf16_f32 v218, v180, v181\n" \
"s_waitcnt lgkmcnt(9)\n" \
"v_mfma_f32_16x16x32_bf16 a[12:15], a[236:239], a[208:211], a[12:15]\n" \
"ds_read_b64_tr_b16 a[178:179], v36 offset:1024\n" \
"v_cvt_pk_bf16_f32 v219, v182, v183\n" \
"s_waitcnt lgkmcnt(8)\n" \
"v_mfma_f32_16x16x32_bf16 a[8:11], a[144:147], a[168:171], 0\n" \
"ds_read_b64_tr_b16 a[240:241], v36 offset:8192\n" \
"v_accvgpr_read_b32 v220, a64\n" \
"s_waitcnt lgkmcnt(7)\n" \
"s_nop 0\n" \
"v_mfma_f32_16x16x32_bf16 a[8:11], a[208:211], a[232:235], a[8:11]\n" \
"ds_read_b64_tr_b16 a[242:243], v36 offset:9216\n" \
"v_accvgpr_read_b32 v221, a65\n" \
"v_mfma_f32_16x16x32_bf16 a[24:27], a[148:151], a[168:171], 0\n" \
"ds_read_b64_tr_b16 a[180:181], v37 offset:0\n" \
"v_accvgpr_read_b32 v222, a66\n" \
"v_mfma_f32_16x16x32_bf16 a[24:27], a[212:215], a[232:235], a[24:27]\n" \
"ds_read_b64_tr_b16 a[182:183], v37 offset:1024\n" \
"v_accvgpr_read_b32 v223, a67\n" \
"v_mfma_f32_16x16x32_bf16 a[28:31], a[172:175], a[148:151], 0\n" \
"ds_read_b64_tr_b16 a[244:245], v37 offset:8192\n" \
"v_accvgpr_read_b32 v180, a80\n" \
"v_mfma_f32_16x16x32_bf16 a[28:31], a[236:239], a[212:215], a[28:31]\n" \
"s_add_u32 s89, s41, 0x2000\n" \
"buffer_store_dwordx4 v[200:203], v20, s[80:83], s89 offen offset:3072 sc1\n" \
"v_mfma_f32_16x16x32_bf16 a[20:23], a[164:167], a[148:151], 0\n" \
"ds_read_b64_tr_b16 a[246:247], v37 offset:9216\n" \
"v_accvgpr_read_b32 v181, a81\n" \
"v_mfma_f32_16x16x32_bf16 a[20:23], a[228:231], a[212:215], a[20:23]\n" \
"ds_read_b64_tr_b16 a[184:185], v38 offset:0\n" \
"v_accvgpr_read_b32 v182, a82\n" \
"v_mfma_f32_16x16x32_bf16 a[16:19], a[160:163], a[148:151], 0\n" \
"ds_read_b64_tr_b16 a[186:187], v38 offset:1024\n" \
"v_accvgpr_read_b32 v183, a83\n" \
"v_mfma_f32_16x16x32_bf16 a[16:19], a[224:227], a[212:215], a[16:19]\n" \
"v_cvt_pk_bf16_f32 v220, v220, v221\n" \
"v_cvt_pk_bf16_f32 v221, v222, v223\n" \
"v_cvt_pk_bf16_f32 v222, v180, v181\n" \
"v_mfma_f32_16x16x32_bf16 a[32:35], a[160:163], a[152:155], 0\n" \
"s_add_u32 s88, s41, 0x2000\n" \
".fill (L_pb + 61152 - .) / 4, 4, 0xBF800000\n" \
"L_pp15:\n" \
"s_cbranch_execz L_pret\n" \
"s_nop 0\n" \
"buffer_store_dwordx4 v[204:207], v20, s[80:83], s88 offen offset:2048 sc1\n" \
"v_mfma_f32_16x16x32_bf16 a[32:35], a[224:227], a[216:219], a[32:35]\n" \
"ds_read_b64_tr_b16 a[248:249], v38 offset:8192\n" \
"v_cvt_pk_bf16_f32 v223, v182, v183\n" \
"v_mfma_f32_16x16x32_bf16 a[36:39], a[164:167], a[152:155], 0\n" \
"ds_read_b64_tr_b16 a[250:251], v38 offset:9216\n" \
"v_cvt_pk_bf16_f32 v224, v128, v129\n" \
"v_mfma_f32_16x16x32_bf16 a[36:39], a[228:231], a[216:219], a[36:39]\n" \
"ds_read_b64_tr_b16 a[188:189], v39 offset:0\n" \
"v_cvt_pk_bf16_f32 v225, v130, v131\n" \
"v_mfma_f32_16x16x32_bf16 a[44:47], a[172:175], a[152:155], 0\n" \
"v_cvt_pk_bf16_f32 v226, v144, v145\n" \
"v_cvt_pk_bf16_f32 v227, v146, v147\n" \
"v_cvt_pk_bf16_f32 v228, v124, v125\n" \
"v_mfma_f32_16x16x32_bf16 a[44:47], a[236:239], a[216:219], a[44:47]\n" \
"ds_read_b64_tr_b16 a[190:191], v39 offset:1024\n" \
"v_cvt_pk_bf16_f32 v229, v126, v127\n" \
"v_mfma_f32_16x16x32_bf16 a[40:43], a[152:155], a[168:171], 0\n" \
"s_add_u32 s89, s41, 0x2000\n" \
"buffer_store_dwordx4 v[208:211], v20, s[80:83], s89 offen offset:1024 sc1\n" \
"v_mfma_f32_16x16x32_bf16 a[40:43], a[216:219], a[232:235], a[40:43]\n" \
"ds_read_b64_tr_b16 a[252:253], v39 offset:8192\n" \
"v_cvt_pk_bf16_f32 v230, v140, v141\n" \
"v_mfma_f32_16x16x32_bf16 a[56:59], a[156:159], a[168:171], 0\n" \
"ds_read_b64_tr_b16 a[254:255], v39 offset:9216\n" \
"v_cvt_pk_bf16_f32 v231, v142, v143\n" \
"v_mfma_f32_16x16x32_bf16 a[56:59], a[220:223], a[232:235], a[56:59]\n" \
"v_cvt_pk_bf16_f32 v232, v120, v121\n" \
"v_cvt_pk_bf16_f32 v233, v122, v123\n" \
"v_cvt_pk_bf16_f32 v234, v136, v137\n" \
"v_mfma_f32_16x16x32_bf16 a[60:63], a[172:175], a[156:159], 0\n" \
"v_cvt_pk_bf16_f32 v235, v138, v139\n" \
"v_cvt_pk_bf16_f32 v236, v116, v117\n" \
"v_cvt_pk_bf16_f32 v237, v118, v119\n" \
"v_mfma_f32_16x16x32_bf16 a[60:63], a[236:239], a[220:223], a[60:63]\n" \
"s_add_u32 s88, s41, 0x2000\n" \
"buffer_store_dwordx4 v[212:215], v20, s[80:83], s88 offen offset:0 sc1\n" \
"v_mfma_f32_16x16x32_bf16 a[52:55], a[164:167], a[156:159], 0\n" \
"v_cvt_pk_bf16_f32 v238, v132, v133\n" \
"v_cvt_pk_bf16_f32 v239, v134, v135\n" \
"v_cvt_pk_bf16_f32 v240, v160, v161\n" \
"v_mfma_f32_16x16x32_bf16 a[52:55], a[228:231], a[220:223], a[52:55]\n" \
"v_cvt_pk_bf16_f32 v241, v162, v163\n" \
"v_cvt_pk_bf16_f32 v242, v176, v177\n" \
"v_cvt_pk_bf16_f32 v243, v178, v179\n" \
"v_mfma_f32_16x16x32_bf16 a[48:51], a[160:163], a[156:159], 0\n" \
"v_cvt_pk_bf16_f32 v244, v156, v157\n" \
"v_cvt_pk_bf16_f32 v245, v158, v159\n" \
"v_cvt_pk_bf16_f32 v246, v172, v173\n" \
"v_mfma_f32_16x16x32_bf16 a[48:51], a[224:227], a[220:223], a[48:51]\n" \
"v_cvt_pk_bf16_f32 v247, v174, v175\n" \
"v_cvt_pk_bf16_f32 v184, v152, v153\n" \
"v_cvt_pk_bf16_f32 v185, v154, v155\n" \
"v_mfma_f32_16x16x32_bf16 a[64:67], a[164:167], a[140:143], 0\n" \
"v_cvt_pk_bf16_f32 v186, v168, v169\n" \
"v_cvt_pk_bf16_f32 v187, v170, v171\n" \
"v_cvt_pk_bf16_f32 v188, v148, v149\n" \
"v_mfma_f32_16x16x32_bf16 a[64:67], a[228:231], a[204:207], a[64:67]\n" \
"v_cvt_pk_bf16_f32 v189, v150, v151\n" \
"v_cvt_pk_bf16_f32 v190, v164, v165\n" \
"v_cvt_pk_bf16_f32 v191, v166, v167\n" \
"v_mfma_f32_16x16x32_bf16 a[80:83], a[172:175], a[140:143], 0\n" \
"v_cvt_pk_bf16_f32 v192, v96, v97\n" \
"v_cvt_pk_bf16_f32 v193, v98, v99\n" \
"v_cvt_pk_bf16_f32 v194, v112, v113\n" \
"v_mfma_f32_16x16x32_bf16 a[80:83], a[236:239], a[204:207], a[80:83]\n" \
"v_cvt_pk_bf16_f32 v195, v114, v115\n" \
"v_cvt_pk_bf16_f32 v196, v92, v93\n" \
"v_cvt_pk_bf16_f32 v197, v94, v95\n" \
"v_mfma_f32_16x16x32_bf16 a[68:71], a[140:143], a[168:171], 0\n" \
"v_cvt_pk_bf16_f32 v198, v108, v109\n" \
"v_cvt_pk_bf16_f32 v199, v110, v111\n" \
"v_cvt_pk_bf16_f32 v252, v88, v89\n" \
"v_mfma_f32_16x16x32_bf16 a[68:71], a[204:207], a[232:235], a[68:71]\n" \
"v_cvt_pk_bf16_f32 v253, v90, v91\n" \
"v_cvt_pk_bf16_f32 v254, v104, v105\n" \
"v_cvt_pk_bf16_f32 v255, v106, v107\n" \
"s_waitcnt vmcnt(4) lgkmcnt(0)\n" \
"s_barrier\n" \
"v_mfma_f32_16x16x32_bf16 v[116:119], a[176:179], a[144:147], 0\n" \
"s_add_u32 m0, s40, 0x0\n" \
"s_nop 0\n" \
"s_add_u32 s85, s39, 0x4000\n" \
"v_accvgpr_write_b32 a84, v252\n" \
"v_mfma_f32_16x16x32_bf16 v[116:119], a[240:243], a[208:211], v[116:119]\n" \
"buffer_load_dwordx4 v20, s[80:83], s85 offen sc1 lds\n" \
"v_accvgpr_write_b32 a85, v253\n" \
"v_mfma_f32_16x16x32_bf16 v[120:123], a[180:183], a[144:147], 0\n" \
"ds_read_b64_tr_b16 a[160:161], v32 offset:32768\n" \
"v_accvgpr_write_b32 a86, v254\n" \
"v_mfma_f32_16x16x32_bf16 v[120:123], a[244:247], a[208:211], v[120:123]\n" \
"ds_read_b64_tr_b16 a[162:163], v32 offset:33280\n" \
"v_accvgpr_write_b32 a87, v255\n" \
"v_mfma_f32_16x16x32_bf16 v[124:127], a[184:187], a[144:147], 0\n" \
"s_add_u32 m0, s40, 0x1000\n" \
"s_add_u32 s85, s39, 0x5000\n" \
"v_cvt_pk_bf16_f32 v252, v84, v85\n" \
"v_mfma_f32_16x16x32_bf16 v[124:127], a[248:251], a[208:211], v[124:127]\n" \
"buffer_load_dwordx4 v20, s[80:83], s85 offen sc1 lds\n" \
"v_cvt_pk_bf16_f32 v253, v86, v87\n" \
"v_mfma_f32_16x16x32_bf16 v[128:131], a[188:191], a[144:147], 0\n" \
"ds_read_b64_tr_b16 a[224:225], v32 offset:36864\n" \
"v_cvt_pk_bf16_f32 v254, v100, v101\n" \
"v_mfma_f32_16x16x32_bf16 v[128:131], a[252:255], a[208:211], v[128:131]\n" \
"ds_read_b128 a[144:147], v28 offset:40960\n" \
"v_cvt_pk_bf16_f32 v255, v102, v103\n" \
"v_mfma_f32_16x16x32_bf16 v[144:147], a[188:191], a[148:151], 0\n" \
"ds_read_b128 a[208:211], v29 offset:40960\n" \
"v_cvt_pk_bf16_f32 v200, v64, v65\n" \
"v_mfma_f32_16x16x32_bf16 v[144:147], a[252:255], a[212:215], v[144:147]\n" \
"s_add_u32 m0, s40, 0x3000\n" \
"s_movk_i32 s84, 0x100\n" \
"s_nop 0\n" \
"v_cvt_pk_bf16_f32 v201, v66, v67\n" \
"v_mfma_f32_16x16x32_bf16 v[140:143], a[184:187], a[148:151], 0\n" \
"buffer_load_dwordx4 v21, s[76:79], s84 offen offset:0 lds\n" \
"v_cvt_pk_bf16_f32 v202, v80, v81\n" \
"v_mfma_f32_16x16x32_bf16 v[140:143], a[248:251], a[212:215], v[140:143]\n" \
"ds_read_b64_tr_b16 a[226:227], v32 offset:37376\n" \
"v_cvt_pk_bf16_f32 v203, v82, v83\n" \
"v_mfma_f32_16x16x32_bf16 v[136:139], a[180:183], a[148:151], 0\n" \
"v_cvt_pk_bf16_f32 v204, v60, v61\n" \
"v_cvt_pk_bf16_f32 v205, v62, v63\n" \
"v_cvt_pk_bf16_f32 v206, v76, v77\n" \
"v_mfma_f32_16x16x32_bf16 v[136:139], a[244:247], a[212:215], v[136:139]\n" \
"ds_read_b64_tr_b16 a[164:165], v33 offset:32768\n" \
"v_cvt_pk_bf16_f32 v207, v78, v79\n" \
"v_mfma_f32_16x16x32_bf16 v[132:135], a[176:179], a[148:151], 0\n" \
"v_cvt_pk_bf16_f32 v208, v56, v57\n" \
"v_cvt_pk_bf16_f32 v209, v58, v59\n" \
"v_cvt_pk_bf16_f32 v210, v72, v73\n" \
"v_mfma_f32_16x16x32_bf16 v[132:135], a[240:243], a[212:215], v[132:135]\n" \
"ds_read_b128 a[148:151], v28 offset:43008\n" \
"v_cvt_pk_bf16_f32 v211, v74, v75\n" \
"v_mfma_f32_16x16x32_bf16 v[148:151], a[176:179], a[152:155], 0\n" \
"ds_read_b128 a[212:215], v29 offset:43008\n" \
"v_cvt_pk_bf16_f32 v212, v52, v53\n" \
"v_mfma_f32_16x16x32_bf16 v[148:151], a[240:243], a[216:219], v[148:151]\n" \
"s_add_u32 m0, s31, 0x0\n" \
"s_mov_b32 s84, 0x100000\n" \
"v_cvt_pk_bf16_f32 v213, v54, v55\n" \
"v_mfma_f32_16x16x32_bf16 v[152:155], a[180:183], a[152:155], 0\n" \
"buffer_load_dwordx4 v26, s[68:71], s84 offen offset:0 lds\n" \
"v_cvt_pk_bf16_f32 v214, v68, v69\n" \
"v_mfma_f32_16x16x32_bf16 v[152:155], a[244:247], a[216:219], v[152:155]\n" \
"s_addk_i32 s84, 0x7c00\n" \
"v_cvt_pk_bf16_f32 v215, v70, v71\n" \
"v_mfma_f32_16x16x32_bf16 v[156:159], a[184:187], a[152:155], 0\n" \
"buffer_load_dwordx4 v26, s[68:71], s84 offen offset:1024 lds\n" \
"v_mfma_f32_16x16x32_bf16 v[156:159], a[248:251], a[216:219], v[156:159]\n" \
"ds_read_b64_tr_b16 a[166:167], v33 offset:33280\n" \
"v_mfma_f32_16x16x32_bf16 v[160:163], a[188:191], a[152:155], 0\n" \
"ds_read_b64_tr_b16 a[228:229], v33 offset:36864\n" \
"v_mfma_f32_16x16x32_bf16 v[160:163], a[252:255], a[216:219], v[160:163]\n" \
"ds_read_b128 a[152:155], v28 offset:45056\n" \
"v_mfma_f32_16x16x32_bf16 v[176:179], a[188:191], a[156:159], 0\n" \
"ds_read_b128 a[216:219], v29 offset:45056\n" \
"v_mfma_f32_16x16x32_bf16 v[176:179], a[252:255], a[220:223], v[176:179]\n" \
"s_addk_i32 s84, 0x7c00\n" \
"v_mfma_f32_16x16x32_bf16 v[172:175], a[184:187], a[156:159], 0\n" \
"buffer_load_dwordx4 v27, s[68:71], s84 offen offset:2048 lds\n" \
"v_mfma_f32_16x16x32_bf16 v[172:175], a[248:251], a[220:223], v[172:175]\n" \
"ds_read_b64_tr_b16 a[230:231], v33 offset:37376\n" \
"v_mfma_f32_16x16x32_bf16 v[168:171], a[180:183], a[156:159], 0\n" \
"s_addk_i32 s84, 0x7c00\n" \
"v_mfma_f32_16x16x32_bf16 v[168:171], a[244:247], a[220:223], v[168:171]\n" \
"buffer_load_dwordx4 v27, s[68:71], s84 offen offset:3072 lds\n" \
"v_mfma_f32_16x16x32_bf16 v[164:167], a[176:179], a[156:159], 0\n" \
"ds_read_b64_tr_b16 a[172:173], v35 offset:32768\n" \
"v_mfma_f32_16x16x32_bf16 v[164:167], a[240:243], a[220:223], v[164:167]\n" \
"ds_read_b128 a[156:159], v28 offset:47104\n" \
"v_mfma_f32_16x16x32_bf16 v[84:87], a[176:179], a[136:139], 0\n" \
"ds_read_b128 a[220:223], v29 offset:47104\n" \
"v_mfma_f32_16x16x32_bf16 v[84:87], a[240:243], a[200:203], v[84:87]\n" \
"s_add_u32 s89, s41, 0x3000\n" \
"buffer_store_dwordx4 v[192:195], v20, s[80:83], s89 offen offset:3072 sc1\n" \
"v_mfma_f32_16x16x32_bf16 v[88:91], a[180:183], a[136:139], 0\n" \
"ds_read_b64_tr_b16 a[174:175], v35 offset:33280\n" \
"v_mfma_f32_16x16x32_bf16 v[88:91], a[244:247], a[200:203], v[88:91]\n" \
"ds_read_b64_tr_b16 a[236:237], v35 offset:36864\n" \
"v_mfma_f32_16x16x32_bf16 v[92:95], a[184:187], a[136:139], 0\n" \
"ds_read_b64_tr_b16 a[238:239], v35 offset:37376\n" \
"v_mfma_f32_16x16x32_bf16 v[92:95], a[248:251], a[200:203], v[92:95]\n" \
"s_add_u32 m0, s30, 0x0\n" \
"s_movk_i32 s84, 0x180\n" \
"v_mfma_f32_16x16x32_bf16 v[96:99], a[188:191], a[136:139], 0\n" \
"buffer_load_dwordx4 v24, s[76:79], s84 offen offset:0 lds\n" \
"v_mfma_f32_16x16x32_bf16 v[96:99], a[252:255], a[200:203], v[96:99]\n" \
"ds_read_b128 a[136:139], v28 offset:36864\n" \
"v_mfma_f32_16x16x32_bf16 v[112:115], a[188:191], a[140:143], 0\n" \
"ds_read_b128 a[200:203], v29 offset:36864\n" \
"v_mfma_f32_16x16x32_bf16 v[112:115], a[252:255], a[204:207], v[112:115]\n" \
"ds_read_b64_tr_b16 a[168:169], v34 offset:32768\n" \
"v_mfma_f32_16x16x32_bf16 v[108:111], a[184:187], a[140:143], 0\n" \
"s_movk_i32 s84, 0x2180\n" \
"s_nop 0\n" \
"v_mfma_f32_16x16x32_bf16 v[108:111], a[248:251], a[204:207], v[108:111]\n" \
"buffer_load_dwordx4 v25, s[76:79], s84 offen offset:1024 lds\n" \
"v_mfma_f32_16x16x32_bf16 v[104:107], a[180:183], a[140:143], 0\n" \
"ds_read_b64_tr_b16 a[170:171], v34 offset:33280\n" \
"v_mfma_f32_16x16x32_bf16 v[104:107], a[244:247], a[204:207], v[104:107]\n" \
"ds_read_b64_tr_b16 a[232:233], v34 offset:36864\n" \
"v_mfma_f32_16x16x32_bf16 v[100:103], a[176:179], a[140:143], 0\n" \
"v_mfma_f32_16x16x32_bf16 v[100:103], a[240:243], a[204:207], v[100:103]\n" \
"ds_read_b128 a[140:143], v28 offset:38912\n" \
"v_mfma_f32_16x16x32_bf16 v[52:55], a[176:179], a[128:131], 0\n" \
"ds_read_b128 a[204:207], v29 offset:38912\n" \
"v_mfma_f32_16x16x32_bf16 v[52:55], a[240:243], a[192:195], v[52:55]\n" \
"s_movk_i32 s84, 0x4180\n" \
"v_mfma_f32_16x16x32_bf16 v[56:59], a[180:183], a[128:131], 0\n" \
"buffer_load_dwordx4 v24, s[76:79], s84 offen offset:2048 lds\n" \
"v_mfma_f32_16x16x32_bf16 v[56:59], a[244:247], a[192:195], v[56:59]\n" \
"s_movk_i32 s84, 0x6180\n" \
"v_mfma_f32_16x16x32_bf16 v[60:63], a[184:187], a[128:131], 0\n" \
"buffer_load_dwordx4 v25, s[76:79], s84 offen offset:3072 lds\n" \
"v_mfma_f32_16x16x32_bf16 v[60:63], a[248:251], a[192:195], v[60:63]\n" \
"ds_read_b64_tr_b16 a[234:235], v34 offset:37376\n" \
"v_mfma_f32_16x16x32_bf16 v[64:67], a[188:191], a[128:131], 0\n" \
"v_mfma_f32_16x16x32_bf16 v[64:67], a[252:255], a[192:195], v[64:67]\n" \
"ds_read_b128 a[128:131], v28 offset:32768\n" \
"v_mfma_f32_16x16x32_bf16 v[80:83], a[188:191], a[132:135], 0\n" \
"ds_read_b128 a[192:195], v29 offset:32768\n" \
"v_mfma_f32_16x16x32_bf16 v[80:83], a[252:255], a[196:199], v[80:83]\n" \
"v_mfma_f32_16x16x32_bf16 v[76:79], a[184:187], a[132:135], 0\n" \
"v_mfma_f32_16x16x32_bf16 v[76:79], a[248:251], a[196:199], v[76:79]\n" \
"v_mfma_f32_16x16x32_bf16 v[72:75], a[180:183], a[132:135], 0\n" \
"v_mfma_f32_16x16x32_bf16 v[72:75], a[244:247], a[196:199], v[72:75]\n" \
"v_mfma_f32_16x16x32_bf16 v[68:71], a[176:179], a[132:135], 0\n" \
"v_mfma_f32_16x16x32_bf16 v[68:71], a[240:243], a[196:199], v[68:71]\n" \
"ds_read_b128 a[132:135], v28 offset:34816\n" \
"ds_read_b128 a[196:199], v29 offset:34816\n" \
"s_waitcnt lgkmcnt(15)\n" \
"v_mfma_f32_16x16x32_bf16 a[0:3], a[160:163], a[144:147], a[0:3]\n" \
"s_add_u32 m0, s30, 0x1000\n" \
"s_addk_i32 s84, 0x3000\n" \
"v_mfma_f32_16x16x32_bf16 a[0:3], a[224:227], a[208:211], a[0:3]\n" \
"buffer_load_dwordx4 v24, s[76:79], s84 offen offset:0 lds\n" \
"v_mfma_f32_16x16x32_bf16 a[4:7], a[164:167], a[144:147], a[4:7]\n" \
"ds_read_b64_tr_b16 a[176:177], v36 offset:32768\n" \
"v_mov_b32_e64 v11, 0\n" \
"v_mfma_f32_16x16x32_bf16 a[4:7], a[228:231], a[208:211], a[4:7]\n" \
"ds_read_b64_tr_b16 a[178:179], v36 offset:33792\n" \
"s_and_b32 s6, s2, 7\n" \
"s_waitcnt lgkmcnt(15)\n" \
"v_mfma_f32_16x16x32_bf16 a[12:15], a[172:175], a[144:147], a[12:15]\n" \
"ds_read_b64_tr_b16 a[240:241], v36 offset:40960\n" \
"s_lshl_b32 s7, s3, 1\n" \
"s_waitcnt lgkmcnt(15)\n" \
"v_mfma_f32_16x16x32_bf16 a[12:15], a[236:239], a[208:211], a[12:15]\n" \
"s_add_u32 s88, s41, 0x3000\n" \
"buffer_store_dwordx4 v[196:199], v20, s[80:83], s88 offen offset:2048 sc1\n" \
"s_waitcnt lgkmcnt(11)\n" \
"v_mfma_f32_16x16x32_bf16 a[8:11], a[144:147], a[168:171], a[8:11]\n" \
"ds_read_b64_tr_b16 a[242:243], v36 offset:41984\n" \
"s_lshr_b32 s4, s2, 5\n" \
"s_waitcnt lgkmcnt(8)\n" \
"v_mfma_f32_16x16x32_bf16 a[8:11], a[208:211], a[232:235], a[8:11]\n" \
"ds_read_b64_tr_b16 a[180:181], v37 offset:32768\n" \
"s_lshl_b32 s5, s6, 2\n" \
"v_mfma_f32_16x16x32_bf16 a[24:27], a[148:151], a[168:171], a[24:27]\n" \
"ds_read_b64_tr_b16 a[182:183], v37 offset:33792\n" \
"s_bfe_u32 s8, s2, 0x20003\n" \
"v_mfma_f32_16x16x32_bf16 a[24:27], a[212:215], a[232:235], a[24:27]\n" \
"ds_read_b64_tr_b16 a[244:245], v37 offset:40960\n" \
"s_and_b32 s7, s7, 4\n" \
"v_mfma_f32_16x16x32_bf16 a[28:31], a[172:175], a[148:151], a[28:31]\n" \
"s_and_b32 s9, s3, 1\n" \
"s_or_b32 s13, s5, s8\n" \
"v_mfma_f32_16x16x32_bf16 a[28:31], a[236:239], a[212:215], a[28:31]\n" \
"s_addk_i32 s84, 0x2000\n" \
"v_mfma_f32_16x16x32_bf16 a[20:23], a[164:167], a[148:151], a[20:23]\n" \
"buffer_load_dwordx4 v25, s[76:79], s84 offen offset:1024 lds\n" \
"v_mfma_f32_16x16x32_bf16 a[20:23], a[228:231], a[212:215], a[20:23]\n" \
"ds_read_b64_tr_b16 a[246:247], v37 offset:41984\n" \
"s_or_b32 s9, s7, s9\n" \
"v_mfma_f32_16x16x32_bf16 a[16:19], a[160:163], a[148:151], a[16:19]\n" \
"ds_read_b64_tr_b16 a[184:185], v38 offset:32768\n" \
"s_mul_i32 s7, s4, 0xa00\n" \
"v_mfma_f32_16x16x32_bf16 a[16:19], a[224:227], a[212:215], a[16:19]\n" \
"ds_read_b64_tr_b16 a[186:187], v38 offset:33792\n" \
"s_add_i32 s7, s7, 0x5bc1000\n" \
"v_mfma_f32_16x16x32_bf16 a[32:35], a[160:163], a[152:155], a[32:35]\n" \
"s_addk_i32 s84, 0x2000\n" \
"v_mfma_f32_16x16x32_bf16 a[32:35], a[224:227], a[216:219], a[32:35]\n" \
"buffer_load_dwordx4 v24, s[76:79], s84 offen offset:2048 lds\n" \
"v_mfma_f32_16x16x32_bf16 a[36:39], a[164:167], a[152:155], a[36:39]\n" \
"ds_read_b64_tr_b16 a[248:249], v38 offset:40960\n" \
"s_lshl_b32 s10, s13, 2\n" \
"v_mfma_f32_16x16x32_bf16 a[36:39], a[228:231], a[216:219], a[36:39]\n" \
"ds_read_b64_tr_b16 a[250:251], v38 offset:41984\n" \
"s_or_b32 s10, s10, s7\n" \
"v_mfma_f32_16x16x32_bf16 a[44:47], a[172:175], a[152:155], a[44:47]\n" \
"ds_read_b64_tr_b16 a[188:189], v39 offset:32768\n" \
"v_writelane_b32 v11, s10, 0\n" \
"v_mfma_f32_16x16x32_bf16 a[44:47], a[236:239], a[216:219], a[44:47]\n" \
"ds_read_b64_tr_b16 a[190:191], v39 offset:33792\n" \
"s_lshl_b32 s14, s4, 3\n" \
"v_mfma_f32_16x16x32_bf16 a[40:43], a[152:155], a[168:171], a[40:43]\n" \
"s_add_u32 s89, s41, 0x3000\n" \
"buffer_store_dwordx4 a[84:87], v20, s[80:83], s89 offen offset:1024 sc1\n" \
"v_mfma_f32_16x16x32_bf16 a[40:43], a[216:219], a[232:235], a[40:43]\n" \
"ds_read_b64_tr_b16 a[252:253], v39 offset:40960\n" \
"v_writelane_b32 v11, s7, 1\n" \
"v_mfma_f32_16x16x32_bf16 a[56:59], a[156:159], a[168:171], a[56:59]\n" \
"ds_read_b64_tr_b16 a[254:255], v39 offset:41984\n" \
"s_or_b32 s7, s14, s6\n" \
"v_mfma_f32_16x16x32_bf16 a[56:59], a[220:223], a[232:235], a[56:59]\n" \
"s_lshl_b32 s7, s7, 10\n" \
"s_lshl_b32 s10, s3, 8\n" \
"v_mfma_f32_16x16x32_bf16 a[60:63], a[172:175], a[156:159], a[60:63]\n" \
"s_or_b32 s7, s7, s10\n" \
"s_or_b32 s10, s7, 0x5c00200\n" \
"v_mfma_f32_16x16x32_bf16 a[60:63], a[236:239], a[220:223], a[60:63]\n" \
"s_addk_i32 s84, 0x2000\n" \
"v_mfma_f32_16x16x32_bf16 a[52:55], a[164:167], a[156:159], a[52:55]\n" \
"buffer_load_dwordx4 v25, s[76:79], s84 offen offset:3072 lds\n" \
"v_mfma_f32_16x16x32_bf16 a[52:55], a[228:231], a[220:223], a[52:55]\n" \
"v_writelane_b32 v11, s10, 2\n" \
"s_lshl_b32 s10, s8, 2\n" \
"v_mfma_f32_16x16x32_bf16 a[48:51], a[160:163], a[156:159], a[48:51]\n" \
"s_or_b32 s11, s10, s7\n" \
"s_or_b32 s11, s11, 0x5c00000\n" \
"v_mfma_f32_16x16x32_bf16 a[48:51], a[224:227], a[220:223], a[48:51]\n" \
"v_writelane_b32 v11, s11, 3\n" \
"s_or_b32 s7, s7, 0x5c00080\n" \
"v_mfma_f32_16x16x32_bf16 a[64:67], a[164:167], a[140:143], a[64:67]\n" \
"v_writelane_b32 v11, s7, 4\n" \
"s_or_b32 s7, s7, s10\n" \
"v_mfma_f32_16x16x32_bf16 a[64:67], a[228:231], a[204:207], a[64:67]\n" \
"s_add_i32 s10, s3, s2\n" \
"v_writelane_b32 v11, s7, 5\n" \
"v_mfma_f32_16x16x32_bf16 a[80:83], a[172:175], a[140:143], a[80:83]\n" \
"s_mov_b32 s7, 2.0\n" \
"s_mov_b32 s11, 0x5bc0000\n" \
"v_mfma_f32_16x16x32_bf16 a[80:83], a[236:239], a[204:207], a[80:83]\n" \
"s_cmp_lg_u32 s10, 0\n" \
"s_cselect_b32 s10, s7, s11\n" \
"v_mfma_f32_16x16x32_bf16 a[68:71], a[140:143], a[168:171], a[68:71]\n" \
"s_lshr_b32 s15, s2, 3\n" \
"v_writelane_b32 v11, s10, 54\n" \
"v_mfma_f32_16x16x32_bf16 a[68:71], a[204:207], a[232:235], a[68:71]\n" \
"s_waitcnt vmcnt(11) lgkmcnt(0)\n" \
"s_barrier\n" \
"v_xor_b32_e32 v28, 0x20000, v28\n" \
"v_xor_b32_e32 v29, 0x20000, v29\n" \
"v_mfma_f32_16x16x32_bf16 v[116:119], a[176:179], a[144:147], v[116:119]\n" \
"s_add_u32 m0, s40, 0x8000\n" \
"s_add_u32 s85, s39, 0x6000\n" \
"v_mfma_f32_16x16x32_bf16 v[116:119], a[240:243], a[208:211], v[116:119]\n" \
"buffer_load_dwordx4 v20, s[80:83], s85 offen sc1 lds\n" \
"v_mfma_f32_16x16x32_bf16 v[120:123], a[180:183], a[144:147], v[120:123]\n" \
"ds_read_b64_tr_b16 a[160:161], v32 offset:0\n" \
"s_lshl_b32 s10, s6, 5\n" \
"v_mfma_f32_16x16x32_bf16 v[120:123], a[244:247], a[208:211], v[120:123]\n" \
"ds_read_b64_tr_b16 a[162:163], v32 offset:512\n" \
"s_min_u32 s16, s3, 2\n" \
"v_mfma_f32_16x16x32_bf16 v[124:127], a[184:187], a[144:147], v[124:127]\n" \
"s_add_u32 m0, s40, 0x9000\n" \
"s_add_u32 s85, s39, 0x7000\n" \
"v_mfma_f32_16x16x32_bf16 v[124:127], a[248:251], a[208:211], v[124:127]\n" \
"buffer_load_dwordx4 v20, s[80:83], s85 offen sc1 lds\n" \
"v_mfma_f32_16x16x32_bf16 v[128:131], a[188:191], a[144:147], v[128:131]\n" \
"ds_read_b64_tr_b16 a[224:225], v32 offset:4096\n" \
"s_mul_i32 s12, s8, 0x120\n" \
"v_mfma_f32_16x16x32_bf16 v[128:131], a[252:255], a[208:211], v[128:131]\n" \
"ds_read_b128 a[144:147], v28 offset:8192\n" \
"v_mfma_f32_16x16x32_bf16 v[144:147], a[188:191], a[148:151], v[144:147]\n" \
"ds_read_b128 a[208:211], v29 offset:8192\n" \
"v_mfma_f32_16x16x32_bf16 v[144:147], a[252:255], a[212:215], v[144:147]\n" \
"s_add_u32 m0, s40, 0xb000\n" \
"s_movk_i32 s84, 0x180\n" \
"v_mfma_f32_16x16x32_bf16 v[140:143], a[184:187], a[148:151], v[140:143]\n" \
"buffer_load_dwordx4 v21, s[76:79], s84 offen offset:0 lds\n" \
"v_mfma_f32_16x16x32_bf16 v[140:143], a[248:251], a[212:215], v[140:143]\n" \
"ds_read_b64_tr_b16 a[226:227], v32 offset:4608\n" \
"v_mfma_f32_16x16x32_bf16 v[136:139], a[180:183], a[148:151], v[136:139]\n" \
"s_add_u32 s88, s41, 0x3000\n" \
"buffer_store_dwordx4 v[252:255], v20, s[80:83], s88 offen offset:0 sc1\n" \
"v_mfma_f32_16x16x32_bf16 v[136:139], a[244:247], a[212:215], v[136:139]\n" \
"ds_read_b64_tr_b16 a[164:165], v33 offset:0\n" \
"v_mfma_f32_16x16x32_bf16 v[132:135], a[176:179], a[148:151], v[132:135]\n" \
"s_add_u32 s11, s22, s12\n" \
"s_addc_u32 s17, s23, 0\n" \
"s_nop 0\n" \
"v_mfma_f32_16x16x32_bf16 v[132:135], a[240:243], a[212:215], v[132:135]\n" \
"ds_read_b128 a[148:151], v28 offset:10240\n" \
"v_mfma_f32_16x16x32_bf16 v[148:151], a[176:179], a[152:155], v[148:151]\n" \
"ds_read_b128 a[212:215], v29 offset:10240\n" \
"v_mfma_f32_16x16x32_bf16 v[148:151], a[240:243], a[216:219], v[148:151]\n" \
"s_add_u32 m0, s31, 0x8000\n" \
"s_mov_b32 s84, 0x180000\n" \
"v_mfma_f32_16x16x32_bf16 v[152:155], a[180:183], a[152:155], v[152:155]\n" \
"buffer_load_dwordx4 v26, s[68:71], s84 offen offset:0 lds\n" \
"v_mfma_f32_16x16x32_bf16 v[152:155], a[244:247], a[216:219], v[152:155]\n" \
"ds_read_b64_tr_b16 a[166:167], v33 offset:512\n" \
"v_mfma_f32_16x16x32_bf16 v[156:159], a[184:187], a[152:155], v[156:159]\n" \
"ds_read_b64_tr_b16 a[228:229], v33 offset:4096\n" \
"v_mfma_f32_16x16x32_bf16 v[156:159], a[248:251], a[216:219], v[156:159]\n" \
"s_addk_i32 s84, 0x7c00\n" \
"v_mfma_f32_16x16x32_bf16 v[160:163], a[188:191], a[152:155], v[160:163]\n" \
"buffer_load_dwordx4 v26, s[68:71], s84 offen offset:1024 lds\n" \
"v_mfma_f32_16x16x32_bf16 v[160:163], a[252:255], a[216:219], v[160:163]\n" \
"ds_read_b128 a[152:155], v28 offset:12288\n" \
"v_mfma_f32_16x16x32_bf16 v[176:179], a[188:191], a[156:159], v[176:179]\n" \
"ds_read_b128 a[216:219], v29 offset:12288\n" \
"v_mfma_f32_16x16x32_bf16 v[176:179], a[252:255], a[220:223], v[176:179]\n" \
"s_addk_i32 s84, 0x7c00\n" \
"v_mfma_f32_16x16x32_bf16 v[172:175], a[184:187], a[156:159], v[172:175]\n" \
"buffer_load_dwordx4 v27, s[68:71], s84 offen offset:2048 lds\n" \
"v_mfma_f32_16x16x32_bf16 v[172:175], a[248:251], a[220:223], v[172:175]\n" \
"ds_read_b64_tr_b16 a[230:231], v33 offset:4608\n" \
"v_mfma_f32_16x16x32_bf16 v[168:171], a[180:183], a[156:159], v[168:171]\n" \
"ds_read_b64_tr_b16 a[172:173], v35 offset:0\n" \
"v_mfma_f32_16x16x32_bf16 v[168:171], a[244:247], a[220:223], v[168:171]\n" \
"ds_read_b64_tr_b16 a[174:175], v35 offset:512\n" \
"v_mfma_f32_16x16x32_bf16 v[164:167], a[176:179], a[156:159], v[164:167]\n" \
"s_add_u32 s18, s24, s12\n" \
"v_writelane_b32 v11, s11, 9\n" \
"s_addc_u32 s19, s25, 0\n" \
"v_mfma_f32_16x16x32_bf16 v[164:167], a[240:243], a[220:223], v[164:167]\n" \
"ds_read_b128 a[156:159], v28 offset:14336\n" \
"s_and_b32 s20, s17, 0xffff\n" \
"v_mfma_f32_16x16x32_bf16 v[84:87], a[176:179], a[136:139], v[84:87]\n" \
"ds_read_b128 a[220:223], v29 offset:14336\n" \
"v_writelane_b32 v11, s20, 10\n" \
"v_mfma_f32_16x16x32_bf16 v[84:87], a[240:243], a[200:203], v[84:87]\n" \
"s_addk_i32 s84, 0x7c00\n" \
"v_mfma_f32_16x16x32_bf16 v[88:91], a[180:183], a[136:139], v[88:91]\n" \
"buffer_load_dwordx4 v27, s[68:71], s84 offen offset:3072 lds\n" \
"v_mfma_f32_16x16x32_bf16 v[88:91], a[244:247], a[200:203], v[88:91]\n" \
"s_add_u32 m0, s30, 0x8000\n" \
"s_movk_i32 s84, 0x200\n" \
"v_mfma_f32_16x16x32_bf16 v[92:95], a[184:187], a[136:139], v[92:95]\n" \
"buffer_load_dwordx4 v24, s[76:79], s84 offen offset:0 lds\n" \
"v_mfma_f32_16x16x32_bf16 v[92:95], a[248:251], a[200:203], v[92:95]\n" \
"ds_read_b64_tr_b16 a[236:237], v35 offset:4096\n" \
"s_and_b32 s20, s19, 0xffff\n" \
"v_mfma_f32_16x16x32_bf16 v[96:99], a[188:191], a[136:139], v[96:99]\n" \
"ds_read_b64_tr_b16 a[238:239], v35 offset:4608\n" \
"v_writelane_b32 v11, s18, 11\n" \
"v_mfma_f32_16x16x32_bf16 v[96:99], a[252:255], a[200:203], v[96:99]\n" \
"ds_read_b128 a[136:139], v28 offset:4096\n" \
"v_mfma_f32_16x16x32_bf16 v[112:115], a[188:191], a[140:143], v[112:115]\n" \
"ds_read_b128 a[200:203], v29 offset:4096\n" \
"v_mfma_f32_16x16x32_bf16 v[112:115], a[252:255], a[204:207], v[112:115]\n" \
"s_movk_i32 s84, 0x2200\n" \
"s_nop 0\n" \
"v_mfma_f32_16x16x32_bf16 v[108:111], a[184:187], a[140:143], v[108:111]\n" \
"buffer_load_dwordx4 v25, s[76:79], s84 offen offset:1024 lds\n" \
"v_mfma_f32_16x16x32_bf16 v[108:111], a[248:251], a[204:207], v[108:111]\n" \
"s_add_u32 s89, s41, 0x1000\n" \
"buffer_store_dwordx4 v[200:203], v20, s[80:83], s89 offen offset:3072 sc1\n" \
"v_mfma_f32_16x16x32_bf16 v[104:107], a[180:183], a[140:143], v[104:107]\n" \
"ds_read_b64_tr_b16 a[168:169], v34 offset:0\n" \
"v_mfma_f32_16x16x32_bf16 v[104:107], a[244:247], a[204:207], v[104:107]\n" \
"ds_read_b64_tr_b16 a[170:171], v34 offset:512\n" \
"v_mfma_f32_16x16x32_bf16 v[100:103], a[176:179], a[140:143], v[100:103]\n" \
"ds_read_b64_tr_b16 a[232:233], v34 offset:4096\n" \
"v_mfma_f32_16x16x32_bf16 v[100:103], a[240:243], a[204:207], v[100:103]\n" \
"ds_read_b128 a[140:143], v28 offset:6144\n" \
"v_mfma_f32_16x16x32_bf16 v[52:55], a[176:179], a[128:131], v[52:55]\n" \
"ds_read_b128 a[204:207], v29 offset:6144\n" \
"v_mfma_f32_16x16x32_bf16 v[52:55], a[240:243], a[192:195], v[52:55]\n" \
"s_movk_i32 s84, 0x4200\n" \
"v_mfma_f32_16x16x32_bf16 v[56:59], a[180:183], a[128:131], v[56:59]\n" \
"buffer_load_dwordx4 v24, s[76:79], s84 offen offset:2048 lds\n" \
"v_mfma_f32_16x16x32_bf16 v[56:59], a[244:247], a[192:195], v[56:59]\n" \
"s_movk_i32 s84, 0x6200\n" \
"v_mfma_f32_16x16x32_bf16 v[60:63], a[184:187], a[128:131], v[60:63]\n" \
"buffer_load_dwordx4 v25, s[76:79], s84 offen offset:3072 lds\n" \
"v_mfma_f32_16x16x32_bf16 v[60:63], a[248:251], a[192:195], v[60:63]\n" \
"ds_read_b64_tr_b16 a[234:235], v34 offset:4608\n" \
"v_mfma_f32_16x16x32_bf16 v[64:67], a[188:191], a[128:131], v[64:67]\n" \
"s_cmpk_lt_u32 s3, 3\n" \
"s_cselect_b32 s11, s11, s18\n" \
"v_mfma_f32_16x16x32_bf16 v[64:67], a[252:255], a[192:195], v[64:67]\n" \
"ds_read_b128 a[128:131], v28 offset:0\n" \
"s_movk_i32 s18, 0xfff\n" \
"v_mfma_f32_16x16x32_bf16 v[80:83], a[188:191], a[132:135], v[80:83]\n" \
"ds_read_b128 a[192:195], v29 offset:0\n" \
"v_writelane_b32 v11, s20, 12\n" \
"v_mfma_f32_16x16x32_bf16 v[80:83], a[252:255], a[196:199], v[80:83]\n" \
"s_and_b32 s20, s29, 0xffff\n" \
"v_writelane_b32 v11, s28, 13\n" \
"v_mfma_f32_16x16x32_bf16 v[76:79], a[184:187], a[132:135], v[76:79]\n" \
"v_writelane_b32 v11, s20, 14\n" \
"v_writelane_b32 v11, s11, 15\n" \
"v_mfma_f32_16x16x32_bf16 v[76:79], a[248:251], a[196:199], v[76:79]\n" \
"s_cmpk_lt_u32 s3, 3\n" \
"s_cselect_b32 s11, s17, s19\n" \
"v_mfma_f32_16x16x32_bf16 v[72:75], a[180:183], a[132:135], v[72:75]\n" \
"s_and_b32 s11, s11, 0xffff\n" \
"v_writelane_b32 v11, s11, 16\n" \
"v_mfma_f32_16x16x32_bf16 v[72:75], a[244:247], a[196:199], v[72:75]\n" \
"s_mul_i32 s11, s6, 0x48\n" \
"s_add_i32 s17, s11, 0x47\n" \
"v_mfma_f32_16x16x32_bf16 v[68:71], a[176:179], a[132:135], v[68:71]\n" \
"s_cmpk_lt_u32 s3, 3\n" \
"s_cselect_b32 s17, s17, s18\n" \
"v_mfma_f32_16x16x32_bf16 v[68:71], a[240:243], a[196:199], v[68:71]\n" \
"ds_read_b128 a[132:135], v28 offset:2048\n" \
"ds_read_b128 a[196:199], v29 offset:2048\n" \
"s_waitcnt lgkmcnt(15)\n" \
"v_mfma_f32_16x16x32_bf16 a[0:3], a[160:163], a[144:147], a[0:3]\n" \
"s_add_u32 m0, s30, 0x9000\n" \
"s_addk_i32 s84, 0x3000\n" \
"v_mfma_f32_16x16x32_bf16 a[0:3], a[224:227], a[208:211], a[0:3]\n" \
"buffer_load_dwordx4 v24, s[76:79], s84 offen offset:0 lds\n" \
"v_mfma_f32_16x16x32_bf16 a[4:7], a[164:167], a[144:147], a[4:7]\n" \
"ds_read_b64_tr_b16 a[176:177], v36 offset:0\n" \
"s_mulk_i32 s17, 0x480\n" \
"v_mfma_f32_16x16x32_bf16 a[4:7], a[228:231], a[208:211], a[4:7]\n" \
"ds_read_b64_tr_b16 a[178:179], v36 offset:1024\n" \
"s_addk_i32 s17, 0x120\n" \
"v_mfma_f32_16x16x32_bf16 a[12:15], a[172:175], a[144:147], a[12:15]\n" \
"ds_read_b64_tr_b16 a[240:241], v36 offset:8192\n" \
"v_writelane_b32 v11, s17, 17\n" \
"s_waitcnt lgkmcnt(15)\n" \
"v_mfma_f32_16x16x32_bf16 a[12:15], a[236:239], a[208:211], a[12:15]\n" \
"s_add_u32 s88, s41, 0x1000\n" \
"buffer_store_dwordx4 v[204:207], v20, s[80:83], s88 offen offset:2048 sc1\n" \
"s_waitcnt lgkmcnt(11)\n" \
"v_mfma_f32_16x16x32_bf16 a[8:11], a[144:147], a[168:171], a[8:11]\n" \
"ds_read_b64_tr_b16 a[242:243], v36 offset:9216\n" \
"s_lshl_b32 s17, s3, 5\n" \
"s_waitcnt lgkmcnt(8)\n" \
"v_mfma_f32_16x16x32_bf16 a[8:11], a[208:211], a[232:235], a[8:11]\n" \
"ds_read_b64_tr_b16 a[180:181], v37 offset:0\n" \
"s_add_i32 s11, s11, s17\n" \
"v_mfma_f32_16x16x32_bf16 a[24:27], a[148:151], a[168:171], a[24:27]\n" \
"ds_read_b64_tr_b16 a[182:183], v37 offset:1024\n" \
"s_mul_i32 s17, s11, 0x480\n" \
"v_mfma_f32_16x16x32_bf16 a[24:27], a[212:215], a[232:235], a[24:27]\n" \
"ds_read_b64_tr_b16 a[244:245], v37 offset:8192\n" \
"s_mul_i32 s11, s13, 0x24000\n" \
"v_mfma_f32_16x16x32_bf16 a[28:31], a[172:175], a[148:151], a[28:31]\n" \
"s_add_i32 s18, s11, 0x1b000\n" \
"v_mfma_f32_16x16x32_bf16 a[28:31], a[236:239], a[212:215], a[28:31]\n" \
"s_addk_i32 s84, 0x2000\n" \
"v_mfma_f32_16x16x32_bf16 a[20:23], a[164:167], a[148:151], a[20:23]\n" \
"buffer_load_dwordx4 v25, s[76:79], s84 offen offset:1024 lds\n" \
"v_mfma_f32_16x16x32_bf16 a[20:23], a[228:231], a[212:215], a[20:23]\n" \
"ds_read_b64_tr_b16 a[246:247], v37 offset:9216\n" \
"v_mfma_f32_16x16x32_bf16 a[16:19], a[160:163], a[148:151], a[16:19]\n" \
"ds_read_b64_tr_b16 a[184:185], v38 offset:0\n" \
"v_mfma_f32_16x16x32_bf16 a[16:19], a[224:227], a[212:215], a[16:19]\n" \
"ds_read_b64_tr_b16 a[186:187], v38 offset:1024\n" \
"v_mfma_f32_16x16x32_bf16 a[32:35], a[160:163], a[152:155], a[32:35]\n" \
"s_addk_i32 s84, 0x2000\n" \
"v_mfma_f32_16x16x32_bf16 a[32:35], a[224:227], a[216:219], a[32:35]\n" \
"buffer_load_dwordx4 v24, s[76:79], s84 offen offset:2048 lds\n" \
"v_mfma_f32_16x16x32_bf16 a[36:39], a[164:167], a[152:155], a[36:39]\n" \
"ds_read_b64_tr_b16 a[248:249], v38 offset:8192\n" \
".fill (L_pb + 65248 - .) / 4, 4, 0xBF800000\n" \
"L_pp16:\n" \
"s_cbranch_execz L_pret\n" \
"s_nop 0\n" \
"v_mfma_f32_16x16x32_bf16 a[36:39], a[228:231], a[216:219], a[36:39]\n" \
"ds_read_b64_tr_b16 a[250:251], v38 offset:9216\n" \
"v_mfma_f32_16x16x32_bf16 a[44:47], a[172:175], a[152:155], a[44:47]\n" \
"ds_read_b64_tr_b16 a[188:189], v39 offset:0\n" \
"v_mfma_f32_16x16x32_bf16 a[44:47], a[236:239], a[216:219], a[44:47]\n" \
"ds_read_b64_tr_b16 a[190:191], v39 offset:1024\n" \
"v_mfma_f32_16x16x32_bf16 a[40:43], a[152:155], a[168:171], a[40:43]\n" \
"s_add_u32 s89, s41, 0x1000\n" \
"buffer_store_dwordx4 v[208:211], v20, s[80:83], s89 offen offset:1024 sc1\n" \
"v_mfma_f32_16x16x32_bf16 a[40:43], a[216:219], a[232:235], a[40:43]\n" \
"ds_read_b64_tr_b16 a[252:253], v39 offset:8192\n" \
"v_mfma_f32_16x16x32_bf16 a[56:59], a[156:159], a[168:171], a[56:59]\n" \
"ds_read_b64_tr_b16 a[254:255], v39 offset:9216\n" \
"v_mfma_f32_16x16x32_bf16 a[56:59], a[220:223], a[232:235], a[56:59]\n" \
"s_cmpk_lt_u32 s3, 3\n" \
"s_cselect_b32 s17, s17, s18\n" \
"v_mfma_f32_16x16x32_bf16 a[60:63], a[172:175], a[156:159], a[60:63]\n" \
"s_mov_b32 s18, 0x8000\n" \
"v_writelane_b32 v11, s17, 18\n" \
"v_mfma_f32_16x16x32_bf16 a[60:63], a[236:239], a[220:223], a[60:63]\n" \
"s_addk_i32 s84, 0x2000\n" \
"v_mfma_f32_16x16x32_bf16 a[52:55], a[164:167], a[156:159], a[52:55]\n" \
"buffer_load_dwordx4 v25, s[76:79], s84 offen offset:3072 lds\n" \
"v_mfma_f32_16x16x32_bf16 a[52:55], a[228:231], a[220:223], a[52:55]\n" \
"s_mul_i32 s17, s16, 0x2400\n" \
"s_mul_i32 s16, s16, 0x9000\n" \
"v_mfma_f32_16x16x32_bf16 a[48:51], a[160:163], a[156:159], a[48:51]\n" \
"s_bitset1_b32 s17, 17\n" \
"v_writelane_b32 v11, s17, 19\n" \
"v_mfma_f32_16x16x32_bf16 a[48:51], a[224:227], a[220:223], a[48:51]\n" \
"s_add_i32 s16, s11, s16\n" \
"v_writelane_b32 v11, s16, 26\n" \
"v_mfma_f32_16x16x32_bf16 a[64:67], a[164:167], a[140:143], a[64:67]\n" \
"s_mul_i32 s16, s3, 0x2400\n" \
"s_add_i32 s17, s16, 0xa400\n" \
"v_mfma_f32_16x16x32_bf16 a[64:67], a[228:231], a[204:207], a[64:67]\n" \
"s_cmpk_lt_u32 s3, 3\n" \
"s_cselect_b32 s17, s17, s18\n" \
"v_mfma_f32_16x16x32_bf16 a[80:83], a[172:175], a[140:143], a[80:83]\n" \
"s_mov_b32 s18, 0x1b000\n" \
"s_xor_b32 s19, s3, 2\n" \
"v_mfma_f32_16x16x32_bf16 a[80:83], a[236:239], a[204:207], a[80:83]\n" \
"s_cmp_lg_u32 s19, 0\n" \
"s_cselect_b32 s18, s17, s18\n" \
"v_mfma_f32_16x16x32_bf16 a[68:71], a[140:143], a[168:171], a[68:71]\n" \
"v_writelane_b32 v11, s17, 20\n" \
"s_or_b32 s17, s16, 0x10000\n" \
"v_mfma_f32_16x16x32_bf16 a[68:71], a[204:207], a[232:235], a[68:71]\n" \
"s_waitcnt vmcnt(11) lgkmcnt(0)\n" \
"s_barrier\n" \
"v_xor_b32_e32 v28, 0x20000, v28\n" \
"v_xor_b32_e32 v29, 0x20000, v29\n" \
"v_mfma_f32_16x16x32_bf16 v[116:119], a[176:179], a[144:147], v[116:119]\n" \
"s_add_u32 m0, s40, 0x0\n" \
"s_nop 0\n" \
"s_add_u32 s85, s39, 0x8000\n" \
"v_mfma_f32_16x16x32_bf16 v[116:119], a[240:243], a[208:211], v[116:119]\n" \
"buffer_load_dwordx4 v20, s[80:83], s85 offen sc1 lds\n" \
"v_mfma_f32_16x16x32_bf16 v[120:123], a[180:183], a[144:147], v[120:123]\n" \
"ds_read_b64_tr_b16 a[160:161], v32 offset:32768\n" \
"v_writelane_b32 v11, s18, 21\n" \
"v_mfma_f32_16x16x32_bf16 v[120:123], a[244:247], a[208:211], v[120:123]\n" \
"ds_read_b64_tr_b16 a[162:163], v32 offset:33280\n" \
"s_add_i32 s18, s16, 0x19000\n" \
"v_mfma_f32_16x16x32_bf16 v[124:127], a[184:187], a[144:147], v[124:127]\n" \
"s_add_u32 m0, s40, 0x1000\n" \
"s_add_u32 s85, s39, 0x9000\n" \
"v_mfma_f32_16x16x32_bf16 v[124:127], a[248:251], a[208:211], v[124:127]\n" \
"buffer_load_dwordx4 v20, s[80:83], s85 offen sc1 lds\n" \
"v_mfma_f32_16x16x32_bf16 v[128:131], a[188:191], a[144:147], v[128:131]\n" \
"ds_read_b64_tr_b16 a[224:225], v32 offset:36864\n" \
"v_writelane_b32 v11, s17, 22\n" \
"v_mfma_f32_16x16x32_bf16 v[128:131], a[252:255], a[208:211], v[128:131]\n" \
"ds_read_b128 a[144:147], v28 offset:8192\n" \
"s_mov_b32 s17, 0\n" \
"v_mfma_f32_16x16x32_bf16 v[144:147], a[188:191], a[148:151], v[144:147]\n" \
"ds_read_b128 a[208:211], v29 offset:8192\n" \
"v_mfma_f32_16x16x32_bf16 v[144:147], a[252:255], a[212:215], v[144:147]\n" \
"s_add_u32 m0, s40, 0x3000\n" \
"s_movk_i32 s84, 0x200\n" \
"v_mfma_f32_16x16x32_bf16 v[140:143], a[184:187], a[148:151], v[140:143]\n" \
"buffer_load_dwordx4 v21, s[76:79], s84 offen offset:0 lds\n" \
"v_mfma_f32_16x16x32_bf16 v[140:143], a[248:251], a[212:215], v[140:143]\n" \
"ds_read_b64_tr_b16 a[226:227], v32 offset:37376\n" \
"v_mfma_f32_16x16x32_bf16 v[136:139], a[180:183], a[148:151], v[136:139]\n" \
"s_add_u32 s88, s41, 0x1000\n" \
"buffer_store_dwordx4 v[212:215], v20, s[80:83], s88 offen offset:0 sc1\n" \
"v_mfma_f32_16x16x32_bf16 v[136:139], a[244:247], a[212:215], v[136:139]\n" \
"ds_read_b64_tr_b16 a[164:165], v33 offset:32768\n" \
"v_mfma_f32_16x16x32_bf16 v[132:135], a[176:179], a[148:151], v[132:135]\n" \
"s_cmpk_lt_u32 s3, 3\n" \
"s_cselect_b32 s18, s18, s17\n" \
"v_mfma_f32_16x16x32_bf16 v[132:135], a[240:243], a[212:215], v[132:135]\n" \
"ds_read_b128 a[148:151], v28 offset:10240\n" \
"s_bitset1_b32 s16, 17\n" \
"v_mfma_f32_16x16x32_bf16 v[148:151], a[176:179], a[152:155], v[148:151]\n" \
"ds_read_b128 a[212:215], v29 offset:10240\n" \
"v_writelane_b32 v11, s18, 23\n" \
"v_mfma_f32_16x16x32_bf16 v[148:151], a[240:243], a[216:219], v[148:151]\n" \
"s_add_u32 m0, s31, 0x0\n" \
"s_mov_b32 s84, 0x200000\n" \
"v_mfma_f32_16x16x32_bf16 v[152:155], a[180:183], a[152:155], v[152:155]\n" \
"buffer_load_dwordx4 v26, s[68:71], s84 offen offset:0 lds\n" \
"v_mfma_f32_16x16x32_bf16 v[152:155], a[244:247], a[216:219], v[152:155]\n" \
"ds_read_b64_tr_b16 a[166:167], v33 offset:33280\n" \
"s_movk_i32 s18, 0x2400\n" \
"v_mfma_f32_16x16x32_bf16 v[156:159], a[184:187], a[152:155], v[156:159]\n" \
"ds_read_b64_tr_b16 a[228:229], v33 offset:36864\n" \
"v_mfma_f32_16x16x32_bf16 v[156:159], a[248:251], a[216:219], v[156:159]\n" \
"s_addk_i32 s84, 0x7c00\n" \
"v_mfma_f32_16x16x32_bf16 v[160:163], a[188:191], a[152:155], v[160:163]\n" \
"buffer_load_dwordx4 v26, s[68:71], s84 offen offset:1024 lds\n" \
"v_mfma_f32_16x16x32_bf16 v[160:163], a[252:255], a[216:219], v[160:163]\n" \
"ds_read_b128 a[152:155], v28 offset:12288\n" \
"v_mfma_f32_16x16x32_bf16 v[176:179], a[188:191], a[156:159], v[176:179]\n" \
"ds_read_b128 a[216:219], v29 offset:12288\n" \
"v_mfma_f32_16x16x32_bf16 v[176:179], a[252:255], a[220:223], v[176:179]\n" \
"s_addk_i32 s84, 0x7c00\n" \
"v_mfma_f32_16x16x32_bf16 v[172:175], a[184:187], a[156:159], v[172:175]\n" \
"buffer_load_dwordx4 v27, s[68:71], s84 offen offset:2048 lds\n" \
"v_mfma_f32_16x16x32_bf16 v[172:175], a[248:251], a[220:223], v[172:175]\n" \
"ds_read_b64_tr_b16 a[230:231], v33 offset:37376\n" \
"v_mfma_f32_16x16x32_bf16 v[168:171], a[180:183], a[156:159], v[168:171]\n" \
"ds_read_b64_tr_b16 a[172:173], v35 offset:32768\n" \
"v_mfma_f32_16x16x32_bf16 v[168:171], a[244:247], a[220:223], v[168:171]\n" \
"ds_read_b64_tr_b16 a[174:175], v35 offset:33280\n" \
"v_mfma_f32_16x16x32_bf16 v[164:167], a[176:179], a[156:159], v[164:167]\n" \
"s_cmpk_lt_u32 s3, 3\n" \
"s_cselect_b32 s16, s16, s18\n" \
"v_mfma_f32_16x16x32_bf16 v[164:167], a[240:243], a[220:223], v[164:167]\n" \
"ds_read_b128 a[156:159], v28 offset:14336\n" \
"s_mul_i32 s19, s3, 0x9000\n" \
"v_mfma_f32_16x16x32_bf16 v[84:87], a[176:179], a[136:139], v[84:87]\n" \
"ds_read_b128 a[220:223], v29 offset:14336\n" \
"v_writelane_b32 v11, s16, 24\n" \
"v_mfma_f32_16x16x32_bf16 v[84:87], a[240:243], a[200:203], v[84:87]\n" \
"s_addk_i32 s84, 0x7c00\n" \
"v_mfma_f32_16x16x32_bf16 v[88:91], a[180:183], a[136:139], v[88:91]\n" \
"buffer_load_dwordx4 v27, s[68:71], s84 offen offset:3072 lds\n" \
"v_mfma_f32_16x16x32_bf16 v[88:91], a[244:247], a[200:203], v[88:91]\n" \
"s_add_u32 m0, s30, 0x20000\n" \
"s_movk_i32 s84, 0x280\n" \
"v_mfma_f32_16x16x32_bf16 v[92:95], a[184:187], a[136:139], v[92:95]\n" \
"buffer_load_dwordx4 v24, s[76:79], s84 offen offset:0 lds\n" \
"v_mfma_f32_16x16x32_bf16 v[92:95], a[248:251], a[200:203], v[92:95]\n" \
"ds_read_b64_tr_b16 a[236:237], v35 offset:36864\n" \
"s_lshl_b32 s16, s3, 10\n" \
"v_mfma_f32_16x16x32_bf16 v[96:99], a[188:191], a[136:139], v[96:99]\n" \
"ds_read_b64_tr_b16 a[238:239], v35 offset:37376\n" \
"s_addk_i32 s16, 0x4800\n" \
"v_mfma_f32_16x16x32_bf16 v[96:99], a[252:255], a[200:203], v[96:99]\n" \
"ds_read_b128 a[136:139], v28 offset:4096\n" \
"v_writelane_b32 v11, s16, 25\n" \
"v_mfma_f32_16x16x32_bf16 v[112:115], a[188:191], a[140:143], v[112:115]\n" \
"ds_read_b128 a[200:203], v29 offset:4096\n" \
"s_add_i32 s16, s15, 1\n" \
"v_mfma_f32_16x16x32_bf16 v[112:115], a[252:255], a[204:207], v[112:115]\n" \
"s_movk_i32 s84, 0x2280\n" \
"v_mfma_f32_16x16x32_bf16 v[108:111], a[184:187], a[140:143], v[108:111]\n" \
"buffer_load_dwordx4 v25, s[76:79], s84 offen offset:1024 lds\n" \
"v_mfma_f32_16x16x32_bf16 v[108:111], a[248:251], a[204:207], v[108:111]\n" \
"s_add_u32 s89, s42, 0x0\n" \
"buffer_store_dwordx4 v[216:219], v20, s[80:83], s89 offen offset:1024 sc1\n" \
"v_mfma_f32_16x16x32_bf16 v[104:107], a[180:183], a[140:143], v[104:107]\n" \
"ds_read_b64_tr_b16 a[168:169], v34 offset:32768\n" \
"s_and_b32 s16, s16, 3\n" \
"v_mfma_f32_16x16x32_bf16 v[104:107], a[244:247], a[204:207], v[104:107]\n" \
"ds_read_b64_tr_b16 a[170:171], v34 offset:33280\n" \
"s_or_b32 s16, s16, s5\n" \
"v_mfma_f32_16x16x32_bf16 v[100:103], a[176:179], a[140:143], v[100:103]\n" \
"ds_read_b64_tr_b16 a[232:233], v34 offset:36864\n" \
"s_mul_i32 s18, s16, 0x24000\n" \
"v_mfma_f32_16x16x32_bf16 v[100:103], a[240:243], a[204:207], v[100:103]\n" \
"ds_read_b128 a[140:143], v28 offset:6144\n" \
"s_add_i32 s18, s18, s19\n" \
"v_mfma_f32_16x16x32_bf16 v[52:55], a[176:179], a[128:131], v[52:55]\n" \
"ds_read_b128 a[204:207], v29 offset:6144\n" \
"s_xor_b32 s13, s13, 2\n" \
"v_mfma_f32_16x16x32_bf16 v[52:55], a[240:243], a[192:195], v[52:55]\n" \
"s_movk_i32 s84, 0x4280\n" \
"v_mfma_f32_16x16x32_bf16 v[56:59], a[180:183], a[128:131], v[56:59]\n" \
"buffer_load_dwordx4 v24, s[76:79], s84 offen offset:2048 lds\n" \
"v_mfma_f32_16x16x32_bf16 v[56:59], a[244:247], a[192:195], v[56:59]\n" \
"s_movk_i32 s84, 0x6280\n" \
"v_mfma_f32_16x16x32_bf16 v[60:63], a[184:187], a[128:131], v[60:63]\n" \
"buffer_load_dwordx4 v25, s[76:79], s84 offen offset:3072 lds\n" \
"v_mfma_f32_16x16x32_bf16 v[60:63], a[248:251], a[192:195], v[60:63]\n" \
"ds_read_b64_tr_b16 a[234:235], v34 offset:37376\n" \
"s_add_i32 s15, s15, -1\n" \
"v_mfma_f32_16x16x32_bf16 v[64:67], a[188:191], a[128:131], v[64:67]\n" \
"v_writelane_b32 v11, s18, 27\n" \
"s_mul_i32 s18, s13, 0x24000\n" \
"v_mfma_f32_16x16x32_bf16 v[64:67], a[252:255], a[192:195], v[64:67]\n" \
"ds_read_b128 a[128:131], v28 offset:0\n" \
"s_and_b32 s15, s15, 3\n" \
"v_mfma_f32_16x16x32_bf16 v[80:83], a[188:191], a[132:135], v[80:83]\n" \
"ds_read_b128 a[192:195], v29 offset:0\n" \
"s_add_i32 s18, s18, s19\n" \
"v_mfma_f32_16x16x32_bf16 v[80:83], a[252:255], a[196:199], v[80:83]\n" \
"s_or_b32 s15, s15, s5\n" \
"v_writelane_b32 v11, s18, 28\n" \
"v_mfma_f32_16x16x32_bf16 v[76:79], a[184:187], a[132:135], v[76:79]\n" \
"s_mul_i32 s18, s15, 0x24000\n" \
"s_add_i32 s18, s18, s19\n" \
"v_mfma_f32_16x16x32_bf16 v[76:79], a[248:251], a[196:199], v[76:79]\n" \
"v_writelane_b32 v11, s18, 29\n" \
"s_or_b32 s18, s9, s14\n" \
"v_mfma_f32_16x16x32_bf16 v[72:75], a[180:183], a[132:135], v[72:75]\n" \
"s_mul_i32 s18, s18, 0x24800\n" \
"s_lshl_b32 s16, s16, 12\n" \
"v_mfma_f32_16x16x32_bf16 v[72:75], a[244:247], a[196:199], v[72:75]\n" \
"s_add_i32 s19, s18, 0x3004800\n" \
"s_add_i32 s20, s19, s16\n" \
"v_mfma_f32_16x16x32_bf16 v[68:71], a[176:179], a[132:135], v[68:71]\n" \
"v_writelane_b32 v11, s20, 30\n" \
"s_lshl_b32 s13, s13, 12\n" \
"v_mfma_f32_16x16x32_bf16 v[68:71], a[240:243], a[196:199], v[68:71]\n" \
"ds_read_b128 a[132:135], v28 offset:2048\n" \
"ds_read_b128 a[196:199], v29 offset:2048\n" \
"s_waitcnt lgkmcnt(15)\n" \
"v_mfma_f32_16x16x32_bf16 a[0:3], a[160:163], a[144:147], a[0:3]\n" \
"s_add_u32 m0, s30, 0x21000\n" \
"s_addk_i32 s84, 0x3000\n" \
"v_mfma_f32_16x16x32_bf16 a[0:3], a[224:227], a[208:211], a[0:3]\n" \
"buffer_load_dwordx4 v24, s[76:79], s84 offen offset:0 lds\n" \
"v_mfma_f32_16x16x32_bf16 a[4:7], a[164:167], a[144:147], a[4:7]\n" \
"ds_read_b64_tr_b16 a[176:177], v36 offset:32768\n" \
"s_lshl_b32 s15, s15, 12\n" \
"v_mfma_f32_16x16x32_bf16 a[4:7], a[228:231], a[208:211], a[4:7]\n" \
"ds_read_b64_tr_b16 a[178:179], v36 offset:33792\n" \
"s_sub_i32 s14, s9, s14\n" \
"v_mfma_f32_16x16x32_bf16 a[12:15], a[172:175], a[144:147], a[12:15]\n" \
"ds_read_b64_tr_b16 a[240:241], v36 offset:40960\n" \
"s_add_i32 s20, s19, s13\n" \
"s_waitcnt lgkmcnt(15)\n" \
"v_mfma_f32_16x16x32_bf16 a[12:15], a[236:239], a[208:211], a[12:15]\n" \
"s_add_u32 s88, s42, 0x0\n" \
"buffer_store_dwordx4 v[220:223], v20, s[80:83], s88 offen offset:0 sc1\n" \
"s_waitcnt lgkmcnt(11)\n" \
"v_mfma_f32_16x16x32_bf16 a[8:11], a[144:147], a[168:171], a[8:11]\n" \
"ds_read_b64_tr_b16 a[242:243], v36 offset:41984\n" \
"v_writelane_b32 v11, s20, 31\n" \
"s_waitcnt lgkmcnt(8)\n" \
"v_mfma_f32_16x16x32_bf16 a[8:11], a[208:211], a[232:235], a[8:11]\n" \
"ds_read_b64_tr_b16 a[180:181], v37 offset:32768\n" \
"s_add_i32 s19, s19, s15\n" \
"v_mfma_f32_16x16x32_bf16 a[24:27], a[148:151], a[168:171], a[24:27]\n" \
"ds_read_b64_tr_b16 a[182:183], v37 offset:33792\n" \
"s_mul_i32 s14, s14, 0x24800\n" \
"v_mfma_f32_16x16x32_bf16 a[24:27], a[212:215], a[232:235], a[24:27]\n" \
"ds_read_b64_tr_b16 a[244:245], v37 offset:40960\n" \
"v_writelane_b32 v11, s19, 32\n" \
"v_mfma_f32_16x16x32_bf16 a[28:31], a[172:175], a[148:151], a[28:31]\n" \
"s_add_i32 s19, s14, 0x4120800\n" \
"s_add_i32 s16, s19, s16\n" \
"v_mfma_f32_16x16x32_bf16 a[28:31], a[236:239], a[212:215], a[28:31]\n" \
"s_addk_i32 s84, 0x2000\n" \
"v_mfma_f32_16x16x32_bf16 a[20:23], a[164:167], a[148:151], a[20:23]\n" \
"buffer_load_dwordx4 v25, s[76:79], s84 offen offset:1024 lds\n" \
"v_mfma_f32_16x16x32_bf16 a[20:23], a[228:231], a[212:215], a[20:23]\n" \
"ds_read_b64_tr_b16 a[246:247], v37 offset:41984\n" \
"v_writelane_b32 v11, s16, 33\n" \
"v_mfma_f32_16x16x32_bf16 a[16:19], a[160:163], a[148:151], a[16:19]\n" \
"ds_read_b64_tr_b16 a[184:185], v38 offset:32768\n" \
"s_add_i32 s13, s19, s13\n" \
"v_mfma_f32_16x16x32_bf16 a[16:19], a[224:227], a[212:215], a[16:19]\n" \
"ds_read_b64_tr_b16 a[186:187], v38 offset:33792\n" \
"v_writelane_b32 v11, s13, 34\n" \
"v_mfma_f32_16x16x32_bf16 a[32:35], a[160:163], a[152:155], a[32:35]\n" \
"s_addk_i32 s84, 0x2000\n" \
"v_mfma_f32_16x16x32_bf16 a[32:35], a[224:227], a[216:219], a[32:35]\n" \
"buffer_load_dwordx4 v24, s[76:79], s84 offen offset:2048 lds\n" \
"v_mfma_f32_16x16x32_bf16 a[36:39], a[164:167], a[152:155], a[36:39]\n" \
"ds_read_b64_tr_b16 a[248:249], v38 offset:40960\n" \
"s_add_i32 s19, s19, s15\n" \
"v_mfma_f32_16x16x32_bf16 a[36:39], a[228:231], a[216:219], a[36:39]\n" \
"ds_read_b64_tr_b16 a[250:251], v38 offset:41984\n" \
"s_or_b32 s13, s18, 0x3000000\n" \
"v_mfma_f32_16x16x32_bf16 a[44:47], a[172:175], a[152:155], a[44:47]\n" \
"ds_read_b64_tr_b16 a[188:189], v39 offset:32768\n" \
"s_lshl_b32 s15, s6, 11\n" \
"v_mfma_f32_16x16x32_bf16 a[44:47], a[236:239], a[216:219], a[44:47]\n" \
"ds_read_b64_tr_b16 a[190:191], v39 offset:33792\n" \
"v_writelane_b32 v11, s19, 35\n" \
"v_mfma_f32_16x16x32_bf16 a[40:43], a[152:155], a[168:171], a[40:43]\n" \
"s_add_u32 s89, s42, 0x1000\n" \
"buffer_store_dwordx4 v[224:227], v20, s[80:83], s89 offen offset:3072 sc1\n" \
"v_mfma_f32_16x16x32_bf16 a[40:43], a[216:219], a[232:235], a[40:43]\n" \
"ds_read_b64_tr_b16 a[252:253], v39 offset:40960\n" \
"s_add_i32 s16, s13, s15\n" \
"v_mfma_f32_16x16x32_bf16 a[56:59], a[156:159], a[168:171], a[56:59]\n" \
"ds_read_b64_tr_b16 a[254:255], v39 offset:41984\n" \
"v_writelane_b32 v11, s16, 36\n" \
"v_mfma_f32_16x16x32_bf16 a[56:59], a[220:223], a[232:235], a[56:59]\n" \
"s_add_i32 s16, s14, 0x411c000\n" \
"s_add_i32 s19, s16, s15\n" \
"v_mfma_f32_16x16x32_bf16 a[60:63], a[172:175], a[156:159], a[60:63]\n" \
"v_writelane_b32 v11, s19, 37\n" \
"s_add_i32 s18, s18, 0x3004000\n" \
"v_mfma_f32_16x16x32_bf16 a[60:63], a[236:239], a[220:223], a[60:63]\n" \
"s_addk_i32 s84, 0x2000\n" \
"v_mfma_f32_16x16x32_bf16 a[52:55], a[164:167], a[156:159], a[52:55]\n" \
"buffer_load_dwordx4 v25, s[76:79], s84 offen offset:3072 lds\n" \
"v_mfma_f32_16x16x32_bf16 a[52:55], a[228:231], a[220:223], a[52:55]\n" \
"v_writelane_b32 v11, s18, 38\n" \
"s_add_i32 s14, s14, 0x4120000\n" \
"v_mfma_f32_16x16x32_bf16 a[48:51], a[160:163], a[156:159], a[48:51]\n" \
"v_writelane_b32 v11, s14, 39\n" \
"s_lshl_b32 s14, s4, 2\n" \
"v_mfma_f32_16x16x32_bf16 a[48:51], a[224:227], a[220:223], a[48:51]\n" \
"s_or_b32 s8, s14, s8\n" \
"s_mul_i32 s8, s8, 0x90000\n" \
"v_mfma_f32_16x16x32_bf16 a[64:67], a[164:167], a[140:143], a[64:67]\n" \
"s_lshl_b32 s14, s3, 14\n" \
"s_add_i32 s8, s8, s14\n" \
"v_mfma_f32_16x16x32_bf16 a[64:67], a[228:231], a[204:207], a[64:67]\n" \
"s_lshl_b32 s9, s9, 4\n" \
"s_lshl_b32 s14, s4, 7\n" \
"v_mfma_f32_16x16x32_bf16 a[80:83], a[172:175], a[140:143], a[80:83]\n" \
"s_or_b32 s18, s9, s14\n" \
"s_sub_i32 s9, s9, s14\n" \
"v_mfma_f32_16x16x32_bf16 a[80:83], a[236:239], a[204:207], a[80:83]\n" \
"s_mulk_i32 s18, 0x480\n" \
"s_mulk_i32 s9, 0x480\n" \
"v_mfma_f32_16x16x32_bf16 a[68:71], a[140:143], a[168:171], a[68:71]\n" \
"s_or_b32 s14, s18, s12\n" \
"s_or_b32 s9, s9, s12\n" \
"v_mfma_f32_16x16x32_bf16 a[68:71], a[204:207], a[232:235], a[68:71]\n" \
"s_waitcnt vmcnt(11) lgkmcnt(0)\n" \
"s_barrier\n" \
"v_mfma_f32_16x16x32_bf16 v[116:119], a[176:179], a[144:147], v[116:119]\n" \
"s_add_u32 m0, s40, 0x8000\n" \
"s_add_u32 s85, s39, 0xa000\n" \
"v_mfma_f32_16x16x32_bf16 v[116:119], a[240:243], a[208:211], v[116:119]\n" \
"buffer_load_dwordx4 v20, s[80:83], s85 offen sc1 lds\n" \
"v_mfma_f32_16x16x32_bf16 v[120:123], a[180:183], a[144:147], v[120:123]\n" \
"ds_read_b64_tr_b16 a[160:161], v32 offset:0\n" \
"v_accvgpr_read_b32 v212, a108\n" \
"v_mfma_f32_16x16x32_bf16 v[120:123], a[244:247], a[208:211], v[120:123]\n" \
"ds_read_b64_tr_b16 a[162:163], v32 offset:512\n" \
"v_accvgpr_read_b32 v213, a109\n" \
"v_mfma_f32_16x16x32_bf16 v[124:127], a[184:187], a[144:147], v[124:127]\n" \
"s_add_u32 m0, s40, 0x9000\n" \
"s_add_u32 s85, s39, 0xb000\n" \
"v_mfma_f32_16x16x32_bf16 v[124:127], a[248:251], a[208:211], v[124:127]\n" \
"buffer_load_dwordx4 v20, s[80:83], s85 offen sc1 lds\n" \
"v_mfma_f32_16x16x32_bf16 v[128:131], a[188:191], a[144:147], v[128:131]\n" \
"ds_read_b64_tr_b16 a[224:225], v32 offset:4096\n" \
"v_accvgpr_read_b32 v214, a110\n" \
"v_mfma_f32_16x16x32_bf16 v[128:131], a[252:255], a[208:211], v[128:131]\n" \
"ds_read_b128 a[144:147], v28 offset:40960\n" \
"v_accvgpr_read_b32 v215, a111\n" \
"v_mfma_f32_16x16x32_bf16 v[144:147], a[188:191], a[148:151], v[144:147]\n" \
"ds_read_b128 a[208:211], v29 offset:40960\n" \
"v_accvgpr_read_b32 v180, a124\n" \
"v_mfma_f32_16x16x32_bf16 v[144:147], a[252:255], a[212:215], v[144:147]\n" \
"s_add_u32 m0, s40, 0xb000\n" \
"s_movk_i32 s84, 0x280\n" \
"s_nop 0\n" \
"v_mfma_f32_16x16x32_bf16 v[140:143], a[184:187], a[148:151], v[140:143]\n" \
"buffer_load_dwordx4 v21, s[76:79], s84 offen offset:0 lds\n" \
"v_mfma_f32_16x16x32_bf16 v[140:143], a[248:251], a[212:215], v[140:143]\n" \
"ds_read_b64_tr_b16 a[226:227], v32 offset:4608\n" \
"v_accvgpr_read_b32 v181, a125\n" \
"v_mfma_f32_16x16x32_bf16 v[136:139], a[180:183], a[148:151], v[136:139]\n" \
"s_add_u32 s88, s42, 0x1000\n" \
"buffer_store_dwordx4 v[228:231], v20, s[80:83], s88 offen offset:2048 sc1\n" \
"v_mfma_f32_16x16x32_bf16 v[136:139], a[244:247], a[212:215], v[136:139]\n" \
"ds_read_b64_tr_b16 a[164:165], v33 offset:0\n" \
"v_accvgpr_read_b32 v182, a126\n" \
"v_mfma_f32_16x16x32_bf16 v[132:135], a[176:179], a[148:151], v[132:135]\n" \
"v_accvgpr_read_b32 v183, a127\n" \
"v_cvt_pk_bf16_f32 v212, v212, v213\n" \
"v_mfma_f32_16x16x32_bf16 v[132:135], a[240:243], a[212:215], v[132:135]\n" \
"ds_read_b128 a[148:151], v28 offset:43008\n" \
"v_cvt_pk_bf16_f32 v213, v214, v215\n" \
"v_mfma_f32_16x16x32_bf16 v[148:151], a[176:179], a[152:155], v[148:151]\n" \
"ds_read_b128 a[212:215], v29 offset:43008\n" \
"v_cvt_pk_bf16_f32 v214, v180, v181\n" \
"v_mfma_f32_16x16x32_bf16 v[148:151], a[240:243], a[216:219], v[148:151]\n" \
"s_add_u32 m0, s31, 0x8000\n" \
"s_mov_b32 s84, 0x280000\n" \
"v_mfma_f32_16x16x32_bf16 v[152:155], a[180:183], a[152:155], v[152:155]\n" \
"buffer_load_dwordx4 v26, s[68:71], s84 offen offset:0 lds\n" \
"v_mfma_f32_16x16x32_bf16 v[152:155], a[244:247], a[216:219], v[152:155]\n" \
"ds_read_b64_tr_b16 a[166:167], v33 offset:512\n" \
"v_cvt_pk_bf16_f32 v215, v182, v183\n" \
"v_mfma_f32_16x16x32_bf16 v[156:159], a[184:187], a[152:155], v[156:159]\n" \
"ds_read_b64_tr_b16 a[228:229], v33 offset:4096\n" \
"v_accvgpr_read_b32 v208, a104\n" \
"v_mfma_f32_16x16x32_bf16 v[156:159], a[248:251], a[216:219], v[156:159]\n" \
"s_addk_i32 s84, 0x7c00\n" \
"v_mfma_f32_16x16x32_bf16 v[160:163], a[188:191], a[152:155], v[160:163]\n" \
"buffer_load_dwordx4 v26, s[68:71], s84 offen offset:1024 lds\n" \
"v_mfma_f32_16x16x32_bf16 v[160:163], a[252:255], a[216:219], v[160:163]\n" \
"ds_read_b128 a[152:155], v28 offset:45056\n" \
"v_accvgpr_read_b32 v209, a105\n" \
"v_mfma_f32_16x16x32_bf16 v[176:179], a[188:191], a[156:159], v[176:179]\n" \
"ds_read_b128 a[216:219], v29 offset:45056\n" \
"v_accvgpr_read_b32 v210, a106\n" \
"v_mfma_f32_16x16x32_bf16 v[176:179], a[252:255], a[220:223], v[176:179]\n" \
"s_addk_i32 s84, 0x7c00\n" \
"v_mfma_f32_16x16x32_bf16 v[172:175], a[184:187], a[156:159], v[172:175]\n" \
"buffer_load_dwordx4 v27, s[68:71], s84 offen offset:2048 lds\n" \
"v_mfma_f32_16x16x32_bf16 v[172:175], a[248:251], a[220:223], v[172:175]\n" \
"ds_read_b64_tr_b16 a[230:231], v33 offset:4608\n" \
"v_accvgpr_read_b32 v211, a107\n" \
"v_mfma_f32_16x16x32_bf16 v[168:171], a[180:183], a[156:159], v[168:171]\n" \
"ds_read_b64_tr_b16 a[172:173], v35 offset:0\n" \
"v_accvgpr_read_b32 v180, a120\n" \
"v_mfma_f32_16x16x32_bf16 v[168:171], a[244:247], a[220:223], v[168:171]\n" \
"ds_read_b64_tr_b16 a[174:175], v35 offset:512\n" \
"v_accvgpr_read_b32 v181, a121\n" \
"v_mfma_f32_16x16x32_bf16 v[164:167], a[176:179], a[156:159], v[164:167]\n" \
"v_accvgpr_read_b32 v182, a122\n" \
"v_accvgpr_read_b32 v183, a123\n" \
"v_mfma_f32_16x16x32_bf16 v[164:167], a[240:243], a[220:223], v[164:167]\n" \
"ds_read_b128 a[156:159], v28 offset:47104\n" \
"v_cvt_pk_bf16_f32 v208, v208, v209\n" \
"v_mfma_f32_16x16x32_bf16 v[84:87], a[176:179], a[136:139], v[84:87]\n" \
"ds_read_b128 a[220:223], v29 offset:47104\n" \
"v_cvt_pk_bf16_f32 v209, v210, v211\n" \
"v_mfma_f32_16x16x32_bf16 v[84:87], a[240:243], a[200:203], v[84:87]\n" \
"s_addk_i32 s84, 0x7c00\n" \
"v_mfma_f32_16x16x32_bf16 v[88:91], a[180:183], a[136:139], v[88:91]\n" \
"buffer_load_dwordx4 v27, s[68:71], s84 offen offset:3072 lds\n" \
"v_mfma_f32_16x16x32_bf16 v[88:91], a[244:247], a[200:203], v[88:91]\n" \
"s_add_u32 m0, s30, 0x0\n" \
"s_movk_i32 s84, 0x300\n" \
"s_nop 0\n" \
"v_mfma_f32_16x16x32_bf16 v[92:95], a[184:187], a[136:139], v[92:95]\n" \
"buffer_load_dwordx4 v24, s[76:79], s84 offen offset:0 lds\n" \
"v_mfma_f32_16x16x32_bf16 v[92:95], a[248:251], a[200:203], v[92:95]\n" \
"ds_read_b64_tr_b16 a[236:237], v35 offset:4096\n" \
"v_cvt_pk_bf16_f32 v210, v180, v181\n" \
"v_mfma_f32_16x16x32_bf16 v[96:99], a[188:191], a[136:139], v[96:99]\n" \
"ds_read_b64_tr_b16 a[238:239], v35 offset:4608\n" \
"v_cvt_pk_bf16_f32 v211, v182, v183\n" \
"v_mfma_f32_16x16x32_bf16 v[96:99], a[252:255], a[200:203], v[96:99]\n" \
"ds_read_b128 a[136:139], v28 offset:36864\n" \
"v_accvgpr_read_b32 v204, a100\n" \
"v_mfma_f32_16x16x32_bf16 v[112:115], a[188:191], a[140:143], v[112:115]\n" \
"ds_read_b128 a[200:203], v29 offset:36864\n" \
"v_accvgpr_read_b32 v205, a101\n" \
"v_mfma_f32_16x16x32_bf16 v[112:115], a[252:255], a[204:207], v[112:115]\n" \
"s_movk_i32 s84, 0x2300\n" \
"s_nop 0\n" \
"v_mfma_f32_16x16x32_bf16 v[108:111], a[184:187], a[140:143], v[108:111]\n" \
"buffer_load_dwordx4 v25, s[76:79], s84 offen offset:1024 lds\n" \
"v_mfma_f32_16x16x32_bf16 v[108:111], a[248:251], a[204:207], v[108:111]\n" \
"s_add_u32 s89, s42, 0x1000\n" \
"buffer_store_dwordx4 v[232:235], v20, s[80:83], s89 offen offset:1024 sc1\n" \
"v_mfma_f32_16x16x32_bf16 v[104:107], a[180:183], a[140:143], v[104:107]\n" \
"ds_read_b64_tr_b16 a[168:169], v34 offset:0\n" \
"v_accvgpr_read_b32 v206, a102\n" \
"v_mfma_f32_16x16x32_bf16 v[104:107], a[244:247], a[204:207], v[104:107]\n" \
"ds_read_b64_tr_b16 a[170:171], v34 offset:512\n" \
"v_accvgpr_read_b32 v207, a103\n" \
"v_mfma_f32_16x16x32_bf16 v[100:103], a[176:179], a[140:143], v[100:103]\n" \
"ds_read_b64_tr_b16 a[232:233], v34 offset:4096\n" \
"v_accvgpr_read_b32 v180, a116\n" \
"v_mfma_f32_16x16x32_bf16 v[100:103], a[240:243], a[204:207], v[100:103]\n" \
"ds_read_b128 a[140:143], v28 offset:38912\n" \
"v_accvgpr_read_b32 v181, a117\n" \
"v_mfma_f32_16x16x32_bf16 v[52:55], a[176:179], a[128:131], v[52:55]\n" \
"ds_read_b128 a[204:207], v29 offset:38912\n" \
"v_accvgpr_read_b32 v182, a118\n" \
"v_mfma_f32_16x16x32_bf16 v[52:55], a[240:243], a[192:195], v[52:55]\n" \
"s_movk_i32 s84, 0x4300\n" \
"v_mfma_f32_16x16x32_bf16 v[56:59], a[180:183], a[128:131], v[56:59]\n" \
"buffer_load_dwordx4 v24, s[76:79], s84 offen offset:2048 lds\n" \
"v_mfma_f32_16x16x32_bf16 v[56:59], a[244:247], a[192:195], v[56:59]\n" \
"s_movk_i32 s84, 0x6300\n" \
"v_mfma_f32_16x16x32_bf16 v[60:63], a[184:187], a[128:131], v[60:63]\n" \
"buffer_load_dwordx4 v25, s[76:79], s84 offen offset:3072 lds\n" \
"v_mfma_f32_16x16x32_bf16 v[60:63], a[248:251], a[192:195], v[60:63]\n" \
"ds_read_b64_tr_b16 a[234:235], v34 offset:4608\n" \
"v_accvgpr_read_b32 v183, a119\n" \
"v_mfma_f32_16x16x32_bf16 v[64:67], a[188:191], a[128:131], v[64:67]\n" \
"v_cvt_pk_bf16_f32 v204, v204, v205\n" \
"v_cvt_pk_bf16_f32 v205, v206, v207\n" \
"v_mfma_f32_16x16x32_bf16 v[64:67], a[252:255], a[192:195], v[64:67]\n" \
"ds_read_b128 a[128:131], v28 offset:32768\n" \
"v_cvt_pk_bf16_f32 v206, v180, v181\n" \
"v_mfma_f32_16x16x32_bf16 v[80:83], a[188:191], a[132:135], v[80:83]\n" \
"ds_read_b128 a[192:195], v29 offset:32768\n" \
"v_cvt_pk_bf16_f32 v207, v182, v183\n" \
"v_mfma_f32_16x16x32_bf16 v[80:83], a[252:255], a[196:199], v[80:83]\n" \
"v_accvgpr_read_b32 v200, a96\n" \
"v_accvgpr_read_b32 v201, a97\n" \
"v_mfma_f32_16x16x32_bf16 v[76:79], a[184:187], a[132:135], v[76:79]\n" \
"v_accvgpr_read_b32 v202, a98\n" \
"v_accvgpr_read_b32 v203, a99\n" \
"v_mfma_f32_16x16x32_bf16 v[76:79], a[248:251], a[196:199], v[76:79]\n" \
"v_accvgpr_read_b32 v180, a112\n" \
"v_accvgpr_read_b32 v181, a113\n" \
"v_mfma_f32_16x16x32_bf16 v[72:75], a[180:183], a[132:135], v[72:75]\n" \
"v_accvgpr_read_b32 v182, a114\n" \
"v_accvgpr_read_b32 v183, a115\n" \
"v_mfma_f32_16x16x32_bf16 v[72:75], a[244:247], a[196:199], v[72:75]\n" \
"v_cvt_pk_bf16_f32 v200, v200, v201\n" \
"v_cvt_pk_bf16_f32 v201, v202, v203\n" \
"v_mfma_f32_16x16x32_bf16 v[68:71], a[176:179], a[132:135], v[68:71]\n" \
"v_cvt_pk_bf16_f32 v202, v180, v181\n" \
"v_cvt_pk_bf16_f32 v203, v182, v183\n" \
"v_mfma_f32_16x16x32_bf16 v[68:71], a[240:243], a[196:199], v[68:71]\n" \
"ds_read_b128 a[132:135], v28 offset:34816\n" \
"ds_read_b128 a[196:199], v29 offset:34816\n" \
"s_waitcnt lgkmcnt(15)\n" \
"v_mfma_f32_16x16x32_bf16 a[0:3], a[160:163], a[144:147], a[0:3]\n" \
"s_add_u32 m0, s30, 0x1000\n" \
"s_addk_i32 s84, 0x3000\n" \
"v_mfma_f32_16x16x32_bf16 a[0:3], a[224:227], a[208:211], a[0:3]\n" \
"buffer_load_dwordx4 v24, s[76:79], s84 offen offset:0 lds\n" \
"v_mfma_f32_16x16x32_bf16 a[4:7], a[164:167], a[144:147], a[4:7]\n" \
"ds_read_b64_tr_b16 a[176:177], v36 offset:0\n" \
"v_accvgpr_read_b32 v196, a76\n" \
"v_mfma_f32_16x16x32_bf16 a[4:7], a[228:231], a[208:211], a[4:7]\n" \
"ds_read_b64_tr_b16 a[178:179], v36 offset:1024\n" \
"v_accvgpr_read_b32 v197, a77\n" \
"v_mfma_f32_16x16x32_bf16 a[12:15], a[172:175], a[144:147], a[12:15]\n" \
"ds_read_b64_tr_b16 a[240:241], v36 offset:8192\n" \
"v_accvgpr_read_b32 v198, a78\n" \
"s_waitcnt lgkmcnt(15)\n" \
"v_mfma_f32_16x16x32_bf16 a[12:15], a[236:239], a[208:211], a[12:15]\n" \
"s_add_u32 s88, s42, 0x1000\n" \
"buffer_store_dwordx4 v[236:239], v20, s[80:83], s88 offen offset:0 sc1\n" \
"s_waitcnt lgkmcnt(11)\n" \
"v_mfma_f32_16x16x32_bf16 a[8:11], a[144:147], a[168:171], a[8:11]\n" \
"ds_read_b64_tr_b16 a[242:243], v36 offset:9216\n" \
"v_accvgpr_read_b32 v199, a79\n" \
"s_waitcnt lgkmcnt(8)\n" \
"s_nop 0\n" \
"v_mfma_f32_16x16x32_bf16 a[8:11], a[208:211], a[232:235], a[8:11]\n" \
"ds_read_b64_tr_b16 a[180:181], v37 offset:0\n" \
"v_accvgpr_read_b32 v180, a92\n" \
"v_mfma_f32_16x16x32_bf16 a[24:27], a[148:151], a[168:171], a[24:27]\n" \
"ds_read_b64_tr_b16 a[182:183], v37 offset:1024\n" \
"v_accvgpr_read_b32 v181, a93\n" \
"v_mfma_f32_16x16x32_bf16 a[24:27], a[212:215], a[232:235], a[24:27]\n" \
"ds_read_b64_tr_b16 a[244:245], v37 offset:8192\n" \
"v_accvgpr_read_b32 v182, a94\n" \
"v_mfma_f32_16x16x32_bf16 a[28:31], a[172:175], a[148:151], a[28:31]\n" \
"v_accvgpr_read_b32 v183, a95\n" \
"v_cvt_pk_bf16_f32 v196, v196, v197\n" \
"v_mfma_f32_16x16x32_bf16 a[28:31], a[236:239], a[212:215], a[28:31]\n" \
"s_addk_i32 s84, 0x2000\n" \
"s_nop 0\n" \
"v_mfma_f32_16x16x32_bf16 a[20:23], a[164:167], a[148:151], a[20:23]\n" \
"buffer_load_dwordx4 v25, s[76:79], s84 offen offset:1024 lds\n" \
"v_mfma_f32_16x16x32_bf16 a[20:23], a[228:231], a[212:215], a[20:23]\n" \
"ds_read_b64_tr_b16 a[246:247], v37 offset:9216\n" \
"v_cvt_pk_bf16_f32 v197, v198, v199\n" \
"v_mfma_f32_16x16x32_bf16 a[16:19], a[160:163], a[148:151], a[16:19]\n" \
"ds_read_b64_tr_b16 a[184:185], v38 offset:0\n" \
"v_cvt_pk_bf16_f32 v198, v180, v181\n" \
"v_mfma_f32_16x16x32_bf16 a[16:19], a[224:227], a[212:215], a[16:19]\n" \
"ds_read_b64_tr_b16 a[186:187], v38 offset:1024\n" \
"v_cvt_pk_bf16_f32 v199, v182, v183\n" \
"v_mfma_f32_16x16x32_bf16 a[32:35], a[160:163], a[152:155], a[32:35]\n" \
"s_addk_i32 s84, 0x2000\n" \
"s_nop 0\n" \
".fill (L_pb + 69344 - .) / 4, 4, 0xBF800000\n" \
"L_pp17:\n" \
"s_cbranch_execz L_pret\n" \
"s_nop 0\n" \
"v_mfma_f32_16x16x32_bf16 a[32:35], a[224:227], a[216:219], a[32:35]\n" \
"buffer_load_dwordx4 v24, s[76:79], s84 offen offset:2048 lds\n" \
"v_mfma_f32_16x16x32_bf16 a[36:39], a[164:167], a[152:155], a[36:39]\n" \
"ds_read_b64_tr_b16 a[248:249], v38 offset:8192\n" \
"v_accvgpr_read_b32 v192, a72\n" \
"v_mfma_f32_16x16x32_bf16 a[36:39], a[228:231], a[216:219], a[36:39]\n" \
"ds_read_b64_tr_b16 a[250:251], v38 offset:9216\n" \
"v_accvgpr_read_b32 v193, a73\n" \
"v_mfma_f32_16x16x32_bf16 a[44:47], a[172:175], a[152:155], a[44:47]\n" \
"ds_read_b64_tr_b16 a[188:189], v39 offset:0\n" \
"v_accvgpr_read_b32 v194, a74\n" \
"v_mfma_f32_16x16x32_bf16 a[44:47], a[236:239], a[216:219], a[44:47]\n" \
"ds_read_b64_tr_b16 a[190:191], v39 offset:1024\n" \
"v_accvgpr_read_b32 v195, a75\n" \
"v_mfma_f32_16x16x32_bf16 a[40:43], a[152:155], a[168:171], a[40:43]\n" \
"s_add_u32 s89, s42, 0x3000\n" \
"buffer_store_dwordx4 v[240:243], v20, s[80:83], s89 offen offset:3072 sc1\n" \
"v_mfma_f32_16x16x32_bf16 a[40:43], a[216:219], a[232:235], a[40:43]\n" \
"ds_read_b64_tr_b16 a[252:253], v39 offset:8192\n" \
"v_accvgpr_read_b32 v180, a88\n" \
"v_mfma_f32_16x16x32_bf16 a[56:59], a[156:159], a[168:171], a[56:59]\n" \
"ds_read_b64_tr_b16 a[254:255], v39 offset:9216\n" \
"v_accvgpr_read_b32 v181, a89\n" \
"v_mfma_f32_16x16x32_bf16 a[56:59], a[220:223], a[232:235], a[56:59]\n" \
"v_accvgpr_read_b32 v182, a90\n" \
"v_accvgpr_read_b32 v183, a91\n" \
"v_mfma_f32_16x16x32_bf16 a[60:63], a[172:175], a[156:159], a[60:63]\n" \
"v_cvt_pk_bf16_f32 v192, v192, v193\n" \
"v_cvt_pk_bf16_f32 v193, v194, v195\n" \
"v_mfma_f32_16x16x32_bf16 a[60:63], a[236:239], a[220:223], a[60:63]\n" \
"s_addk_i32 s84, 0x2000\n" \
"v_mfma_f32_16x16x32_bf16 a[52:55], a[164:167], a[156:159], a[52:55]\n" \
"buffer_load_dwordx4 v25, s[76:79], s84 offen offset:3072 lds\n" \
"v_mfma_f32_16x16x32_bf16 a[52:55], a[228:231], a[220:223], a[52:55]\n" \
"v_cvt_pk_bf16_f32 v194, v180, v181\n" \
"v_cvt_pk_bf16_f32 v195, v182, v183\n" \
"v_mfma_f32_16x16x32_bf16 a[48:51], a[160:163], a[156:159], a[48:51]\n" \
"s_and_b32 s12, s2, 1\n" \
"v_mfma_f32_16x16x32_bf16 a[48:51], a[224:227], a[220:223], a[48:51]\n" \
"s_cmp_lg_u32 s12, 0\n" \
"s_cselect_b32 s13, s16, s13\n" \
"v_mfma_f32_16x16x32_bf16 a[64:67], a[164:167], a[140:143], a[64:67]\n" \
"s_bfe_u32 s16, s2, 0x20001\n" \
"s_addk_i32 s13, 0x4000\n" \
"v_mfma_f32_16x16x32_bf16 a[64:67], a[228:231], a[204:207], a[64:67]\n" \
"s_xor_b32 s16, s16, s3\n" \
"v_mfma_f32_16x16x32_bf16 a[80:83], a[172:175], a[140:143], a[80:83]\n" \
"s_cmp_lg_u32 s16, 0\n" \
"s_cselect_b32 s13, s7, s13\n" \
"v_mfma_f32_16x16x32_bf16 a[80:83], a[236:239], a[204:207], a[80:83]\n" \
"s_lshl_b32 s6, s6, 16\n" \
"v_writelane_b32 v11, s13, 40\n" \
"v_mfma_f32_16x16x32_bf16 a[68:71], a[140:143], a[168:171], a[68:71]\n" \
"s_add_i32 s13, s8, 0x4240000\n" \
"s_add_i32 s6, s13, s6\n" \
"v_mfma_f32_16x16x32_bf16 a[68:71], a[204:207], a[232:235], a[68:71]\n" \
"s_waitcnt vmcnt(11) lgkmcnt(0)\n" \
"s_barrier\n" \
"v_mov_b32_e32 v45, s62\n" \
"s_mov_b64 exec, 1\n" \
"buffer_store_dword v45, off, s[80:83], s37 offset:0 sc1\n" \
"s_mov_b64 exec, -1\n" \
"v_xor_b32_e32 v28, 0x20000, v28\n" \
"v_xor_b32_e32 v29, 0x20000, v29\n" \
"v_mfma_f32_16x16x32_bf16 v[116:119], a[176:179], a[144:147], v[116:119]\n" \
"s_add_u32 m0, s40, 0x0\n" \
"s_add_u32 s85, s39, 0xc000\n" \
"v_mfma_f32_16x16x32_bf16 v[116:119], a[240:243], a[208:211], v[116:119]\n" \
"buffer_load_dwordx4 v20, s[80:83], s85 offen sc1 lds\n" \
"v_mfma_f32_16x16x32_bf16 v[120:123], a[180:183], a[144:147], v[120:123]\n" \
"ds_read_b64_tr_b16 a[160:161], v32 offset:32768\n" \
"v_writelane_b32 v11, s6, 41\n" \
"v_mfma_f32_16x16x32_bf16 v[120:123], a[244:247], a[208:211], v[120:123]\n" \
"ds_read_b64_tr_b16 a[162:163], v32 offset:33280\n" \
"s_add_i32 s8, s8, 0x42c0000\n" \
"v_mfma_f32_16x16x32_bf16 v[124:127], a[184:187], a[144:147], v[124:127]\n" \
"s_add_u32 m0, s40, 0x1000\n" \
"s_add_u32 s85, s39, 0xd000\n" \
"v_mfma_f32_16x16x32_bf16 v[124:127], a[248:251], a[208:211], v[124:127]\n" \
"buffer_load_dwordx4 v20, s[80:83], s85 offen sc1 lds\n" \
"v_mfma_f32_16x16x32_bf16 v[128:131], a[188:191], a[144:147], v[128:131]\n" \
"ds_read_b64_tr_b16 a[224:225], v32 offset:36864\n" \
"s_lshl_b32 s6, s12, 10\n" \
"v_mfma_f32_16x16x32_bf16 v[128:131], a[252:255], a[208:211], v[128:131]\n" \
"ds_read_b128 a[144:147], v28 offset:8192\n" \
"s_or_b32 s6, s8, s6\n" \
"v_mfma_f32_16x16x32_bf16 v[144:147], a[188:191], a[148:151], v[144:147]\n" \
"ds_read_b128 a[208:211], v29 offset:8192\n" \
"v_mfma_f32_16x16x32_bf16 v[144:147], a[252:255], a[212:215], v[144:147]\n" \
"s_add_u32 m0, s40, 0x3000\n" \
"s_movk_i32 s84, 0x300\n" \
"v_mfma_f32_16x16x32_bf16 v[140:143], a[184:187], a[148:151], v[140:143]\n" \
"buffer_load_dwordx4 v21, s[76:79], s84 offen offset:0 lds\n" \
"v_mfma_f32_16x16x32_bf16 v[140:143], a[248:251], a[212:215], v[140:143]\n" \
"ds_read_b64_tr_b16 a[226:227], v32 offset:37376\n" \
"v_mfma_f32_16x16x32_bf16 v[136:139], a[180:183], a[148:151], v[136:139]\n" \
"s_cmp_lg_u32 s16, 0\n" \
"s_cselect_b32 s6, s7, s6\n" \
"v_mfma_f32_16x16x32_bf16 v[136:139], a[244:247], a[212:215], v[136:139]\n" \
"ds_read_b64_tr_b16 a[164:165], v33 offset:32768\n" \
"s_mulk_i32 s4, 0x4800\n" \
"v_mfma_f32_16x16x32_bf16 v[132:135], a[176:179], a[148:151], v[132:135]\n" \
"v_writelane_b32 v11, s6, 42\n" \
"s_add_i32 s6, s14, s10\n" \
"v_mfma_f32_16x16x32_bf16 v[132:135], a[240:243], a[212:215], v[132:135]\n" \
"ds_read_b128 a[148:151], v28 offset:10240\n" \
"v_writelane_b32 v11, s6, 43\n" \
"v_mfma_f32_16x16x32_bf16 v[148:151], a[176:179], a[152:155], v[148:151]\n" \
"ds_read_b128 a[212:215], v29 offset:10240\n" \
"s_add_i32 s6, s9, 0x21c000\n" \
"v_mfma_f32_16x16x32_bf16 v[148:151], a[240:243], a[216:219], v[148:151]\n" \
"s_add_u32 m0, s31, 0x0\n" \
"s_mov_b32 s84, 0x300000\n" \
"v_mfma_f32_16x16x32_bf16 v[152:155], a[180:183], a[152:155], v[152:155]\n" \
"buffer_load_dwordx4 v26, s[68:71], s84 offen offset:0 lds\n" \
"v_mfma_f32_16x16x32_bf16 v[152:155], a[244:247], a[216:219], v[152:155]\n" \
"s_addk_i32 s84, 0x7c00\n" \
"v_mfma_f32_16x16x32_bf16 v[156:159], a[184:187], a[152:155], v[156:159]\n" \
"buffer_load_dwordx4 v26, s[68:71], s84 offen offset:1024 lds\n" \
"v_mfma_f32_16x16x32_bf16 v[156:159], a[248:251], a[216:219], v[156:159]\n" \
"ds_read_b64_tr_b16 a[166:167], v33 offset:33280\n" \
"s_add_i32 s9, s6, s10\n" \
"v_mfma_f32_16x16x32_bf16 v[160:163], a[188:191], a[152:155], v[160:163]\n" \
"ds_read_b64_tr_b16 a[228:229], v33 offset:36864\n" \
"v_mfma_f32_16x16x32_bf16 v[160:163], a[252:255], a[216:219], v[160:163]\n" \
"ds_read_b128 a[152:155], v28 offset:12288\n" \
"v_mfma_f32_16x16x32_bf16 v[176:179], a[188:191], a[156:159], v[176:179]\n" \
"ds_read_b128 a[216:219], v29 offset:12288\n" \
"v_mfma_f32_16x16x32_bf16 v[176:179], a[252:255], a[220:223], v[176:179]\n" \
"s_addk_i32 s84, 0x7c00\n" \
"v_mfma_f32_16x16x32_bf16 v[172:175], a[184:187], a[156:159], v[172:175]\n" \
"buffer_load_dwordx4 v27, s[68:71], s84 offen offset:2048 lds\n" \
"v_mfma_f32_16x16x32_bf16 v[172:175], a[248:251], a[220:223], v[172:175]\n" \
"ds_read_b64_tr_b16 a[230:231], v33 offset:37376\n" \
"v_mfma_f32_16x16x32_bf16 v[168:171], a[180:183], a[156:159], v[168:171]\n" \
"s_addk_i32 s84, 0x7c00\n" \
"v_mfma_f32_16x16x32_bf16 v[168:171], a[244:247], a[220:223], v[168:171]\n" \
"buffer_load_dwordx4 v27, s[68:71], s84 offen offset:3072 lds\n" \
"v_mfma_f32_16x16x32_bf16 v[164:167], a[176:179], a[156:159], v[164:167]\n" \
"ds_read_b64_tr_b16 a[172:173], v35 offset:32768\n" \
"v_mfma_f32_16x16x32_bf16 v[164:167], a[240:243], a[220:223], v[164:167]\n" \
"ds_read_b128 a[156:159], v28 offset:14336\n" \
"v_mfma_f32_16x16x32_bf16 v[84:87], a[176:179], a[136:139], v[84:87]\n" \
"ds_read_b128 a[220:223], v29 offset:14336\n" \
"v_mfma_f32_16x16x32_bf16 v[84:87], a[240:243], a[200:203], v[84:87]\n" \
"s_add_u32 s88, s42, 0x3000\n" \
"buffer_store_dwordx4 v[244:247], v20, s[80:83], s88 offen offset:2048 sc1\n" \
"v_mfma_f32_16x16x32_bf16 v[88:91], a[180:183], a[136:139], v[88:91]\n" \
"ds_read_b64_tr_b16 a[174:175], v35 offset:33280\n" \
"v_mfma_f32_16x16x32_bf16 v[88:91], a[244:247], a[200:203], v[88:91]\n" \
"ds_read_b64_tr_b16 a[236:237], v35 offset:36864\n" \
"v_mfma_f32_16x16x32_bf16 v[92:95], a[184:187], a[136:139], v[92:95]\n" \
"ds_read_b64_tr_b16 a[238:239], v35 offset:37376\n" \
"v_mfma_f32_16x16x32_bf16 v[92:95], a[248:251], a[200:203], v[92:95]\n" \
"s_add_u32 m0, s30, 0x8000\n" \
"s_movk_i32 s84, 0x380\n" \
"v_mfma_f32_16x16x32_bf16 v[96:99], a[188:191], a[136:139], v[96:99]\n" \
"buffer_load_dwordx4 v24, s[76:79], s84 offen offset:0 lds\n" \
"v_mfma_f32_16x16x32_bf16 v[96:99], a[252:255], a[200:203], v[96:99]\n" \
"ds_read_b128 a[136:139], v28 offset:4096\n" \
"v_mfma_f32_16x16x32_bf16 v[112:115], a[188:191], a[140:143], v[112:115]\n" \
"ds_read_b128 a[200:203], v29 offset:4096\n" \
"v_mfma_f32_16x16x32_bf16 v[112:115], a[252:255], a[204:207], v[112:115]\n" \
"ds_read_b64_tr_b16 a[168:169], v34 offset:32768\n" \
"v_mfma_f32_16x16x32_bf16 v[108:111], a[184:187], a[140:143], v[108:111]\n" \
"s_movk_i32 s84, 0x2380\n" \
"v_mfma_f32_16x16x32_bf16 v[108:111], a[248:251], a[204:207], v[108:111]\n" \
"buffer_load_dwordx4 v25, s[76:79], s84 offen offset:1024 lds\n" \
"v_mfma_f32_16x16x32_bf16 v[104:107], a[180:183], a[140:143], v[104:107]\n" \
"ds_read_b64_tr_b16 a[170:171], v34 offset:33280\n" \
"v_mfma_f32_16x16x32_bf16 v[104:107], a[244:247], a[204:207], v[104:107]\n" \
"ds_read_b64_tr_b16 a[232:233], v34 offset:36864\n" \
"v_mfma_f32_16x16x32_bf16 v[100:103], a[176:179], a[140:143], v[100:103]\n" \
"s_cmp_lg_u32 s12, 0\n" \
"s_cselect_b32 s6, s6, s14\n" \
"v_mfma_f32_16x16x32_bf16 v[100:103], a[240:243], a[204:207], v[100:103]\n" \
"ds_read_b128 a[140:143], v28 offset:6144\n" \
"s_addk_i32 s6, 0x100\n" \
"v_mfma_f32_16x16x32_bf16 v[52:55], a[176:179], a[128:131], v[52:55]\n" \
"ds_read_b128 a[204:207], v29 offset:6144\n" \
"v_writelane_b32 v11, s9, 44\n" \
"v_mfma_f32_16x16x32_bf16 v[52:55], a[240:243], a[192:195], v[52:55]\n" \
"s_movk_i32 s84, 0x4380\n" \
"v_mfma_f32_16x16x32_bf16 v[56:59], a[180:183], a[128:131], v[56:59]\n" \
"buffer_load_dwordx4 v24, s[76:79], s84 offen offset:2048 lds\n" \
"v_mfma_f32_16x16x32_bf16 v[56:59], a[244:247], a[192:195], v[56:59]\n" \
"s_movk_i32 s84, 0x6380\n" \
"v_mfma_f32_16x16x32_bf16 v[60:63], a[184:187], a[128:131], v[60:63]\n" \
"buffer_load_dwordx4 v25, s[76:79], s84 offen offset:3072 lds\n" \
"v_mfma_f32_16x16x32_bf16 v[60:63], a[248:251], a[192:195], v[60:63]\n" \
"ds_read_b64_tr_b16 a[234:235], v34 offset:37376\n" \
"v_mfma_f32_16x16x32_bf16 v[64:67], a[188:191], a[128:131], v[64:67]\n" \
"s_cmp_lg_u32 s16, 0\n" \
"s_cselect_b32 s6, s7, s6\n" \
"v_mfma_f32_16x16x32_bf16 v[64:67], a[252:255], a[192:195], v[64:67]\n" \
"ds_read_b128 a[128:131], v28 offset:0\n" \
"v_writelane_b32 v11, s6, 45\n" \
"v_mfma_f32_16x16x32_bf16 v[80:83], a[188:191], a[132:135], v[80:83]\n" \
"ds_read_b128 a[192:195], v29 offset:0\n" \
"s_or_b32 s6, s8, s15\n" \
"v_mfma_f32_16x16x32_bf16 v[80:83], a[252:255], a[196:199], v[80:83]\n" \
"v_writelane_b32 v11, s6, 53\n" \
"s_lshl_b32 s6, s3, 17\n" \
"v_mfma_f32_16x16x32_bf16 v[76:79], a[184:187], a[132:135], v[76:79]\n" \
"s_add_i32 s6, s13, s6\n" \
"s_or_b32 s5, s6, s5\n" \
"v_mfma_f32_16x16x32_bf16 v[76:79], a[248:251], a[196:199], v[76:79]\n" \
"s_or_b32 s5, s5, 0x3840\n" \
"v_writelane_b32 v11, s5, 6\n" \
"v_mfma_f32_16x16x32_bf16 v[72:75], a[180:183], a[132:135], v[72:75]\n" \
"s_mov_b32 s5, 0xff0000\n" \
"v_mfma_f32_16x16x32_bf16 v[72:75], a[244:247], a[196:199], v[72:75]\n" \
"s_cmp_lg_u32 s16, 0\n" \
"s_cselect_b32 s5, s17, s5\n" \
"v_mfma_f32_16x16x32_bf16 v[68:71], a[176:179], a[132:135], v[68:71]\n" \
"v_writelane_b32 v11, s5, 7\n" \
"s_mul_i32 s5, s3, 0x1200\n" \
"v_mfma_f32_16x16x32_bf16 v[68:71], a[240:243], a[196:199], v[68:71]\n" \
"ds_read_b128 a[132:135], v28 offset:2048\n" \
"ds_read_b128 a[196:199], v29 offset:2048\n" \
"s_waitcnt lgkmcnt(15)\n" \
"v_mfma_f32_16x16x32_bf16 a[0:3], a[160:163], a[144:147], a[0:3]\n" \
"s_add_u32 m0, s30, 0x9000\n" \
"s_addk_i32 s84, 0x3000\n" \
"v_mfma_f32_16x16x32_bf16 a[0:3], a[224:227], a[208:211], a[0:3]\n" \
"buffer_load_dwordx4 v24, s[76:79], s84 offen offset:0 lds\n" \
"v_mfma_f32_16x16x32_bf16 a[4:7], a[164:167], a[144:147], a[4:7]\n" \
"ds_read_b64_tr_b16 a[176:177], v36 offset:32768\n" \
"s_add_i32 s4, s5, s4\n" \
"v_mfma_f32_16x16x32_bf16 a[4:7], a[228:231], a[208:211], a[4:7]\n" \
"ds_read_b64_tr_b16 a[178:179], v36 offset:33792\n" \
"s_add_i32 s4, s4, s11\n" \
"s_waitcnt lgkmcnt(15)\n" \
"v_mfma_f32_16x16x32_bf16 a[12:15], a[172:175], a[144:147], a[12:15]\n" \
"ds_read_b64_tr_b16 a[240:241], v36 offset:40960\n" \
"v_writelane_b32 v11, s4, 8\n" \
"s_waitcnt lgkmcnt(15)\n" \
"v_mfma_f32_16x16x32_bf16 a[12:15], a[236:239], a[208:211], a[12:15]\n" \
"s_add_u32 s89, s42, 0x3000\n" \
"buffer_store_dwordx4 v[184:187], v20, s[80:83], s89 offen offset:1024 sc1\n" \
"s_waitcnt lgkmcnt(11)\n" \
"v_mfma_f32_16x16x32_bf16 a[8:11], a[144:147], a[168:171], a[8:11]\n" \
"ds_read_b64_tr_b16 a[242:243], v36 offset:41984\n" \
"s_lshl_b32 s4, s2, 16\n" \
"s_waitcnt lgkmcnt(8)\n" \
"v_mfma_f32_16x16x32_bf16 a[8:11], a[208:211], a[232:235], a[8:11]\n" \
"ds_read_b64_tr_b16 a[180:181], v37 offset:32768\n" \
"s_add_i32 s5, s4, 0x10000\n" \
"v_mfma_f32_16x16x32_bf16 a[24:27], a[148:151], a[168:171], a[24:27]\n" \
"ds_read_b64_tr_b16 a[182:183], v37 offset:33792\n" \
"s_and_b32 s5, s5, 0x70000\n" \
"v_mfma_f32_16x16x32_bf16 a[24:27], a[212:215], a[232:235], a[24:27]\n" \
"ds_read_b64_tr_b16 a[244:245], v37 offset:40960\n" \
"s_add_i32 s5, s13, s5\n" \
"v_mfma_f32_16x16x32_bf16 a[28:31], a[172:175], a[148:151], a[28:31]\n" \
"v_writelane_b32 v11, s5, 46\n" \
"s_add_i32 s5, s4, 0x20000\n" \
"v_mfma_f32_16x16x32_bf16 a[28:31], a[236:239], a[212:215], a[28:31]\n" \
"s_addk_i32 s84, 0x2000\n" \
"v_mfma_f32_16x16x32_bf16 a[20:23], a[164:167], a[148:151], a[20:23]\n" \
"buffer_load_dwordx4 v25, s[76:79], s84 offen offset:1024 lds\n" \
"v_mfma_f32_16x16x32_bf16 a[20:23], a[228:231], a[212:215], a[20:23]\n" \
"ds_read_b64_tr_b16 a[246:247], v37 offset:41984\n" \
"s_and_b32 s5, s5, 0x70000\n" \
"v_mfma_f32_16x16x32_bf16 a[16:19], a[160:163], a[148:151], a[16:19]\n" \
"ds_read_b64_tr_b16 a[184:185], v38 offset:32768\n" \
"s_add_i32 s5, s13, s5\n" \
"v_mfma_f32_16x16x32_bf16 a[16:19], a[224:227], a[212:215], a[16:19]\n" \
"ds_read_b64_tr_b16 a[186:187], v38 offset:33792\n" \
"s_bitset1_b32 s5, 11\n" \
"v_mfma_f32_16x16x32_bf16 a[32:35], a[160:163], a[152:155], a[32:35]\n" \
"s_addk_i32 s84, 0x2000\n" \
"s_nop 0\n" \
"v_mfma_f32_16x16x32_bf16 a[32:35], a[224:227], a[216:219], a[32:35]\n" \
"buffer_load_dwordx4 v24, s[76:79], s84 offen offset:2048 lds\n" \
"v_mfma_f32_16x16x32_bf16 a[36:39], a[164:167], a[152:155], a[36:39]\n" \
"ds_read_b64_tr_b16 a[248:249], v38 offset:40960\n" \
"v_writelane_b32 v11, s5, 47\n" \
"v_mfma_f32_16x16x32_bf16 a[36:39], a[228:231], a[216:219], a[36:39]\n" \
"ds_read_b64_tr_b16 a[250:251], v38 offset:41984\n" \
"s_add_i32 s5, s4, 0x30000\n" \
"v_mfma_f32_16x16x32_bf16 a[44:47], a[172:175], a[152:155], a[44:47]\n" \
"ds_read_b64_tr_b16 a[188:189], v39 offset:32768\n" \
"s_and_b32 s5, s5, 0x70000\n" \
"v_mfma_f32_16x16x32_bf16 a[44:47], a[236:239], a[216:219], a[44:47]\n" \
"ds_read_b64_tr_b16 a[190:191], v39 offset:33792\n" \
"s_add_i32 s5, s13, s5\n" \
"v_mfma_f32_16x16x32_bf16 a[40:43], a[152:155], a[168:171], a[40:43]\n" \
"s_add_u32 s88, s42, 0x3000\n" \
"buffer_store_dwordx4 v[188:191], v20, s[80:83], s88 offen offset:0 sc1\n" \
"v_mfma_f32_16x16x32_bf16 a[40:43], a[216:219], a[232:235], a[40:43]\n" \
"ds_read_b64_tr_b16 a[252:253], v39 offset:40960\n" \
"s_bitset1_b32 s5, 12\n" \
"v_mfma_f32_16x16x32_bf16 a[56:59], a[156:159], a[168:171], a[56:59]\n" \
"ds_read_b64_tr_b16 a[254:255], v39 offset:41984\n" \
"v_writelane_b32 v11, s5, 48\n" \
"v_mfma_f32_16x16x32_bf16 a[56:59], a[220:223], a[232:235], a[56:59]\n" \
"s_and_b32 s5, s4, 0x70000\n" \
"s_xor_b32 s5, s5, 0x40000\n" \
"v_mfma_f32_16x16x32_bf16 a[60:63], a[172:175], a[156:159], a[60:63]\n" \
"s_add_i32 s5, s13, s5\n" \
"s_or_b32 s5, s5, 0x1800\n" \
"v_mfma_f32_16x16x32_bf16 a[60:63], a[236:239], a[220:223], a[60:63]\n" \
"s_addk_i32 s84, 0x2000\n" \
"v_mfma_f32_16x16x32_bf16 a[52:55], a[164:167], a[156:159], a[52:55]\n" \
"buffer_load_dwordx4 v25, s[76:79], s84 offen offset:3072 lds\n" \
"v_mfma_f32_16x16x32_bf16 a[52:55], a[228:231], a[220:223], a[52:55]\n" \
"v_writelane_b32 v11, s5, 49\n" \
"s_add_i32 s5, s4, 0x50000\n" \
"v_mfma_f32_16x16x32_bf16 a[48:51], a[160:163], a[156:159], a[48:51]\n" \
"s_and_b32 s5, s5, 0x70000\n" \
"s_add_i32 s5, s13, s5\n" \
"v_mfma_f32_16x16x32_bf16 a[48:51], a[224:227], a[220:223], a[48:51]\n" \
"s_bitset1_b32 s5, 13\n" \
"v_writelane_b32 v11, s5, 50\n" \
"v_mfma_f32_16x16x32_bf16 a[64:67], a[164:167], a[140:143], a[64:67]\n" \
"s_add_i32 s5, s4, 0x60000\n" \
"s_and_b32 s5, s5, 0x70000\n" \
"v_mfma_f32_16x16x32_bf16 a[64:67], a[228:231], a[204:207], a[64:67]\n" \
"s_add_i32 s4, s4, 0x70000\n" \
"s_add_i32 s5, s13, s5\n" \
"v_mfma_f32_16x16x32_bf16 a[80:83], a[172:175], a[140:143], a[80:83]\n" \
"s_and_b32 s4, s4, 0x70000\n" \
"s_or_b32 s5, s5, 0x2800\n" \
"v_mfma_f32_16x16x32_bf16 a[80:83], a[236:239], a[204:207], a[80:83]\n" \
"v_writelane_b32 v11, s5, 51\n" \
"s_add_i32 s13, s13, s4\n" \
"v_mfma_f32_16x16x32_bf16 a[68:71], a[140:143], a[168:171], a[68:71]\n" \
"s_or_b32 s4, s13, 0x3000\n" \
"v_writelane_b32 v11, s4, 52\n" \
"v_mfma_f32_16x16x32_bf16 a[68:71], a[204:207], a[232:235], a[68:71]\n" \
"s_waitcnt vmcnt(11) lgkmcnt(0)\n" \
"s_barrier\n" \
"v_xor_b32_e32 v28, 0x20000, v28\n" \
"v_xor_b32_e32 v29, 0x20000, v29\n" \
"v_mfma_f32_16x16x32_bf16 v[116:119], a[176:179], a[144:147], v[116:119]\n" \
"s_add_u32 m0, s40, 0x8000\n" \
"s_add_u32 s85, s39, 0xe000\n" \
"v_mfma_f32_16x16x32_bf16 v[116:119], a[240:243], a[208:211], v[116:119]\n" \
"buffer_load_dwordx4 v20, s[80:83], s85 offen sc1 lds\n" \
"v_mfma_f32_16x16x32_bf16 v[120:123], a[180:183], a[144:147], v[120:123]\n" \
"ds_read_b64_tr_b16 a[160:161], v32 offset:0\n" \
"s_movk_i32 s11, 0x4000\n" \
"v_mfma_f32_16x16x32_bf16 v[120:123], a[244:247], a[208:211], v[120:123]\n" \
"ds_read_b64_tr_b16 a[162:163], v32 offset:512\n" \
"s_lshl_b32 s4, s2, 2\n" \
"v_mfma_f32_16x16x32_bf16 v[124:127], a[184:187], a[144:147], v[124:127]\n" \
"s_add_u32 m0, s40, 0x9000\n" \
"s_add_u32 s85, s39, 0xf000\n" \
"v_mfma_f32_16x16x32_bf16 v[124:127], a[248:251], a[208:211], v[124:127]\n" \
"buffer_load_dwordx4 v20, s[80:83], s85 offen sc1 lds\n" \
"v_mfma_f32_16x16x32_bf16 v[128:131], a[188:191], a[144:147], v[128:131]\n" \
"ds_read_b64_tr_b16 a[224:225], v32 offset:4096\n" \
"s_and_b32 s4, s4, 28\n" \
"v_mfma_f32_16x16x32_bf16 v[128:131], a[252:255], a[208:211], v[128:131]\n" \
"ds_read_b128 a[144:147], v28 offset:8192\n" \
"s_bfe_u32 s5, s2, 0x20003\n" \
"v_mfma_f32_16x16x32_bf16 v[144:147], a[188:191], a[148:151], v[144:147]\n" \
"ds_read_b128 a[208:211], v29 offset:8192\n" \
"s_or_b32 s4, s4, s5\n" \
"v_mfma_f32_16x16x32_bf16 v[144:147], a[252:255], a[212:215], v[144:147]\n" \
"s_add_u32 m0, s40, 0xb000\n" \
"s_movk_i32 s84, 0x380\n" \
"v_mfma_f32_16x16x32_bf16 v[140:143], a[184:187], a[148:151], v[140:143]\n" \
"buffer_load_dwordx4 v21, s[76:79], s84 offen offset:0 lds\n" \
"v_mfma_f32_16x16x32_bf16 v[140:143], a[248:251], a[212:215], v[140:143]\n" \
"ds_read_b64_tr_b16 a[226:227], v32 offset:4608\n" \
"s_lshr_b32 s5, s2, 5\n" \
"v_mfma_f32_16x16x32_bf16 v[136:139], a[180:183], a[148:151], v[136:139]\n" \
"s_lshr_b32 s19, s3, 1\n" \
"s_lshl_b32 s18, s5, 1\n" \
"v_mfma_f32_16x16x32_bf16 v[136:139], a[244:247], a[212:215], v[136:139]\n" \
"ds_read_b64_tr_b16 a[164:165], v33 offset:0\n" \
"s_lshl_b32 s6, s4, 20\n" \
"v_mfma_f32_16x16x32_bf16 v[132:135], a[176:179], a[148:151], v[132:135]\n" \
"s_lshl_b32 s7, s4, 8\n" \
"s_and_b32 s13, s3, 1\n" \
"v_mfma_f32_16x16x32_bf16 v[132:135], a[240:243], a[212:215], v[132:135]\n" \
"ds_read_b128 a[148:151], v28 offset:10240\n" \
"s_add_i32 s14, s18, s19\n" \
"v_mfma_f32_16x16x32_bf16 v[148:151], a[176:179], a[152:155], v[148:151]\n" \
"ds_read_b128 a[212:215], v29 offset:10240\n" \
"v_mfma_f32_16x16x32_bf16 v[148:151], a[240:243], a[216:219], v[148:151]\n" \
"s_add_u32 m0, s31, 0x8000\n" \
"s_mov_b32 s84, 0x380000\n" \
"v_mfma_f32_16x16x32_bf16 v[152:155], a[180:183], a[152:155], v[152:155]\n" \
"buffer_load_dwordx4 v26, s[68:71], s84 offen offset:0 lds\n" \
"v_mfma_f32_16x16x32_bf16 v[152:155], a[244:247], a[216:219], v[152:155]\n" \
"s_addk_i32 s84, 0x7c00\n" \
"s_nop 0\n" \
"v_mfma_f32_16x16x32_bf16 v[156:159], a[184:187], a[152:155], v[156:159]\n" \
"buffer_load_dwordx4 v26, s[68:71], s84 offen offset:1024 lds\n" \
"v_mfma_f32_16x16x32_bf16 v[156:159], a[248:251], a[216:219], v[156:159]\n" \
"ds_read_b64_tr_b16 a[166:167], v33 offset:512\n" \
"v_mfma_f32_16x16x32_bf16 v[160:163], a[188:191], a[152:155], v[160:163]\n" \
"ds_read_b64_tr_b16 a[228:229], v33 offset:4096\n" \
"v_mfma_f32_16x16x32_bf16 v[160:163], a[252:255], a[216:219], v[160:163]\n" \
"ds_read_b128 a[152:155], v28 offset:12288\n" \
"v_mfma_f32_16x16x32_bf16 v[176:179], a[188:191], a[156:159], v[176:179]\n" \
"ds_read_b128 a[216:219], v29 offset:12288\n" \
"v_mfma_f32_16x16x32_bf16 v[176:179], a[252:255], a[220:223], v[176:179]\n" \
"s_addk_i32 s84, 0x7c00\n" \
"v_mfma_f32_16x16x32_bf16 v[172:175], a[184:187], a[156:159], v[172:175]\n" \
"buffer_load_dwordx4 v27, s[68:71], s84 offen offset:2048 lds\n" \
"v_mfma_f32_16x16x32_bf16 v[172:175], a[248:251], a[220:223], v[172:175]\n" \
"ds_read_b64_tr_b16 a[230:231], v33 offset:4608\n" \
"v_mfma_f32_16x16x32_bf16 v[168:171], a[180:183], a[156:159], v[168:171]\n" \
"s_addk_i32 s84, 0x7c00\n" \
"v_mfma_f32_16x16x32_bf16 v[168:171], a[244:247], a[220:223], v[168:171]\n" \
"buffer_load_dwordx4 v27, s[68:71], s84 offen offset:3072 lds\n" \
"v_mfma_f32_16x16x32_bf16 v[164:167], a[176:179], a[156:159], v[164:167]\n" \
"ds_read_b64_tr_b16 a[172:173], v35 offset:0\n" \
"v_mfma_f32_16x16x32_bf16 v[164:167], a[240:243], a[220:223], v[164:167]\n" \
"ds_read_b128 a[156:159], v28 offset:14336\n" \
"v_mfma_f32_16x16x32_bf16 v[84:87], a[176:179], a[136:139], v[84:87]\n" \
"ds_read_b128 a[220:223], v29 offset:14336\n" \
"v_mfma_f32_16x16x32_bf16 v[84:87], a[240:243], a[200:203], v[84:87]\n" \
"s_add_u32 s89, s42, 0x0\n" \
"buffer_store_dwordx4 v[192:195], v20, s[80:83], s89 offen offset:2048 sc1\n" \
"v_mfma_f32_16x16x32_bf16 v[88:91], a[180:183], a[136:139], v[88:91]\n" \
"ds_read_b64_tr_b16 a[174:175], v35 offset:512\n" \
"v_mfma_f32_16x16x32_bf16 v[88:91], a[244:247], a[200:203], v[88:91]\n" \
"ds_read_b64_tr_b16 a[236:237], v35 offset:4096\n" \
"v_mfma_f32_16x16x32_bf16 v[92:95], a[184:187], a[136:139], v[92:95]\n" \
"ds_read_b64_tr_b16 a[238:239], v35 offset:4608\n" \
"v_mfma_f32_16x16x32_bf16 v[92:95], a[248:251], a[200:203], v[92:95]\n" \
"s_add_u32 m0, s30, 0x20000\n" \
"s_movk_i32 s84, 0x400\n" \
"v_mfma_f32_16x16x32_bf16 v[96:99], a[188:191], a[136:139], v[96:99]\n" \
"buffer_load_dwordx4 v24, s[76:79], s84 offen offset:0 lds\n" \
"v_mfma_f32_16x16x32_bf16 v[96:99], a[252:255], a[200:203], v[96:99]\n" \
"ds_read_b128 a[136:139], v28 offset:4096\n" \
"v_mfma_f32_16x16x32_bf16 v[112:115], a[188:191], a[140:143], v[112:115]\n" \
"ds_read_b128 a[200:203], v29 offset:4096\n" \
"v_mfma_f32_16x16x32_bf16 v[112:115], a[252:255], a[204:207], v[112:115]\n" \
"ds_read_b64_tr_b16 a[168:169], v34 offset:0\n" \
"v_mfma_f32_16x16x32_bf16 v[108:111], a[184:187], a[140:143], v[108:111]\n" \
"s_movk_i32 s84, 0x2400\n" \
"v_mfma_f32_16x16x32_bf16 v[108:111], a[248:251], a[204:207], v[108:111]\n" \
"buffer_load_dwordx4 v25, s[76:79], s84 offen offset:1024 lds\n" \
"v_mfma_f32_16x16x32_bf16 v[104:107], a[180:183], a[140:143], v[104:107]\n" \
"ds_read_b64_tr_b16 a[170:171], v34 offset:512\n" \
"v_mfma_f32_16x16x32_bf16 v[104:107], a[244:247], a[204:207], v[104:107]\n" \
"ds_read_b64_tr_b16 a[232:233], v34 offset:4096\n" \
"v_mfma_f32_16x16x32_bf16 v[100:103], a[176:179], a[140:143], v[100:103]\n" \
"s_add_u32 s6, s26, s6\n" \
"s_addc_u32 s9, s27, 0\n" \
"v_mfma_f32_16x16x32_bf16 v[100:103], a[240:243], a[204:207], v[100:103]\n" \
"ds_read_b128 a[140:143], v28 offset:6144\n" \
"v_mfma_f32_16x16x32_bf16 v[52:55], a[176:179], a[128:131], v[52:55]\n" \
"ds_read_b128 a[204:207], v29 offset:6144\n" \
"v_mfma_f32_16x16x32_bf16 v[52:55], a[240:243], a[192:195], v[52:55]\n" \
"s_movk_i32 s84, 0x4400\n" \
"v_mfma_f32_16x16x32_bf16 v[56:59], a[180:183], a[128:131], v[56:59]\n" \
"buffer_load_dwordx4 v24, s[76:79], s84 offen offset:2048 lds\n" \
"v_mfma_f32_16x16x32_bf16 v[56:59], a[244:247], a[192:195], v[56:59]\n" \
"s_movk_i32 s84, 0x6400\n" \
"v_mfma_f32_16x16x32_bf16 v[60:63], a[184:187], a[128:131], v[60:63]\n" \
"buffer_load_dwordx4 v25, s[76:79], s84 offen offset:3072 lds\n" \
"v_mfma_f32_16x16x32_bf16 v[60:63], a[248:251], a[192:195], v[60:63]\n" \
"ds_read_b64_tr_b16 a[234:235], v34 offset:4608\n" \
"v_mfma_f32_16x16x32_bf16 v[64:67], a[188:191], a[128:131], v[64:67]\n" \
"s_add_u32 s8, s6, 0x1000000\n" \
"s_addc_u32 s6, s9, 0\n" \
"v_mfma_f32_16x16x32_bf16 v[64:67], a[252:255], a[192:195], v[64:67]\n" \
"ds_read_b128 a[128:131], v28 offset:0\n" \
"s_and_b32 s9, s6, 0xffff\n" \
"v_mfma_f32_16x16x32_bf16 v[80:83], a[188:191], a[132:135], v[80:83]\n" \
"ds_read_b128 a[192:195], v29 offset:0\n" \
"s_lshl_b32 s6, s5, 16\n" \
"v_mfma_f32_16x16x32_bf16 v[80:83], a[252:255], a[196:199], v[80:83]\n" \
"s_sub_i32 s10, 0x100000, s6\n" \
"s_mov_b32 s6, 0x8000\n" \
"v_mfma_f32_16x16x32_bf16 v[76:79], a[184:187], a[132:135], v[76:79]\n" \
"s_cmp_lg_u32 s5, 0\n" \
"s_cselect_b32 s17, s11, s6\n" \
"v_mfma_f32_16x16x32_bf16 v[76:79], a[248:251], a[196:199], v[76:79]\n" \
"s_lshl_b32 s6, s3, 1\n" \
"s_lshl_b32 s5, s5, 3\n" \
"v_mfma_f32_16x16x32_bf16 v[72:75], a[180:183], a[132:135], v[72:75]\n" \
"s_and_b32 s6, s6, 4\n" \
"s_or_b32 s12, s5, s6\n" \
"v_mfma_f32_16x16x32_bf16 v[72:75], a[244:247], a[196:199], v[72:75]\n" \
"s_sub_i32 s5, s13, s5\n" \
"s_or_b32 s5, s5, s6\n" \
"v_mfma_f32_16x16x32_bf16 v[68:71], a[176:179], a[132:135], v[68:71]\n" \
"s_or_b32 s6, s12, s13\n" \
"s_lshl_b32 s4, s4, 2\n" \
"v_mfma_f32_16x16x32_bf16 v[68:71], a[240:243], a[196:199], v[68:71]\n" \
"ds_read_b128 a[132:135], v28 offset:2048\n" \
"ds_read_b128 a[196:199], v29 offset:2048\n" \
"s_waitcnt lgkmcnt(15)\n" \
"v_mfma_f32_16x16x32_bf16 a[0:3], a[160:163], a[144:147], a[0:3]\n" \
"s_add_u32 m0, s30, 0x21000\n" \
"s_addk_i32 s84, 0x3000\n" \
"v_mfma_f32_16x16x32_bf16 a[0:3], a[224:227], a[208:211], a[0:3]\n" \
"buffer_load_dwordx4 v24, s[76:79], s84 offen offset:0 lds\n" \
"v_mfma_f32_16x16x32_bf16 a[4:7], a[164:167], a[144:147], a[4:7]\n" \
"ds_read_b64_tr_b16 a[176:177], v36 offset:0\n" \
"s_mulk_i32 s6, 0x92\n" \
"v_mfma_f32_16x16x32_bf16 a[4:7], a[228:231], a[208:211], a[4:7]\n" \
"ds_read_b64_tr_b16 a[178:179], v36 offset:1024\n" \
"s_add_i32 s4, s4, 18\n" \
"s_waitcnt lgkmcnt(15)\n" \
"v_mfma_f32_16x16x32_bf16 a[12:15], a[172:175], a[144:147], a[12:15]\n" \
"ds_read_b64_tr_b16 a[240:241], v36 offset:8192\n" \
"s_mulk_i32 s5, 0x92\n" \
"s_waitcnt lgkmcnt(15)\n" \
"v_mfma_f32_16x16x32_bf16 a[12:15], a[236:239], a[208:211], a[12:15]\n" \
"s_add_u32 s88, s42, 0x0\n" \
"buffer_store_dwordx4 v[196:199], v20, s[80:83], s88 offen offset:3072 sc1\n" \
"s_waitcnt lgkmcnt(11)\n" \
"v_mfma_f32_16x16x32_bf16 a[8:11], a[144:147], a[168:171], a[8:11]\n" \
"ds_read_b64_tr_b16 a[242:243], v36 offset:9216\n" \
"s_add_i32 s6, s4, s6\n" \
"s_waitcnt lgkmcnt(8)\n" \
"v_mfma_f32_16x16x32_bf16 a[8:11], a[208:211], a[232:235], a[8:11]\n" \
"ds_read_b64_tr_b16 a[180:181], v37 offset:0\n" \
"s_add_i32 s5, s5, s4\n" \
"v_mfma_f32_16x16x32_bf16 a[24:27], a[148:151], a[168:171], a[24:27]\n" \
"ds_read_b64_tr_b16 a[182:183], v37 offset:1024\n" \
"s_lshl_b32 s4, s3, 11\n" \
"v_mfma_f32_16x16x32_bf16 a[24:27], a[212:215], a[232:235], a[24:27]\n" \
"ds_read_b64_tr_b16 a[244:245], v37 offset:8192\n" \
"s_lshl_b32 s15, s13, 12\n" \
"v_mfma_f32_16x16x32_bf16 a[28:31], a[172:175], a[148:151], a[28:31]\n" \
"s_lshl_b32 s16, s19, 14\n" \
"s_mul_i32 s19, s19, 0xe000\n" \
"v_mfma_f32_16x16x32_bf16 a[28:31], a[236:239], a[212:215], a[28:31]\n" \
"s_addk_i32 s84, 0x2000\n" \
"v_mfma_f32_16x16x32_bf16 a[20:23], a[164:167], a[148:151], a[20:23]\n" \
"buffer_load_dwordx4 v25, s[76:79], s84 offen offset:1024 lds\n" \
"v_mfma_f32_16x16x32_bf16 a[20:23], a[228:231], a[212:215], a[20:23]\n" \
"ds_read_b64_tr_b16 a[246:247], v37 offset:9216\n" \
"s_mov_b32 s11, 0x20000\n" \
"v_mfma_f32_16x16x32_bf16 a[16:19], a[160:163], a[148:151], a[16:19]\n" \
"ds_read_b64_tr_b16 a[184:185], v38 offset:0\n" \
"s_or_b32 s12, s4, 0xc000\n" \
"v_mfma_f32_16x16x32_bf16 a[16:19], a[224:227], a[212:215], a[16:19]\n" \
"ds_read_b64_tr_b16 a[186:187], v38 offset:1024\n" \
"s_lshl_b32 s4, s3, 8\n" \
"v_mfma_f32_16x16x32_bf16 a[32:35], a[160:163], a[152:155], a[32:35]\n" \
"s_addk_i32 s84, 0x2000\n" \
"v_mfma_f32_16x16x32_bf16 a[32:35], a[224:227], a[216:219], a[32:35]\n" \
"buffer_load_dwordx4 v24, s[76:79], s84 offen offset:2048 lds\n" \
"v_mfma_f32_16x16x32_bf16 a[36:39], a[164:167], a[152:155], a[36:39]\n" \
"ds_read_b64_tr_b16 a[248:249], v38 offset:8192\n" \
"s_or_b32 s22, s15, s16\n" \
"v_mfma_f32_16x16x32_bf16 a[36:39], a[228:231], a[216:219], a[36:39]\n" \
"ds_read_b64_tr_b16 a[250:251], v38 offset:9216\n" \
"s_or_b32 s23, s19, s15\n" \
"v_mfma_f32_16x16x32_bf16 a[44:47], a[172:175], a[152:155], a[44:47]\n" \
"ds_read_b64_tr_b16 a[188:189], v39 offset:0\n" \
"s_lshl_b32 s60, s6, 10\n" \
"v_mfma_f32_16x16x32_bf16 a[44:47], a[236:239], a[216:219], a[44:47]\n" \
"ds_read_b64_tr_b16 a[190:191], v39 offset:1024\n" \
"s_lshl_b32 s61, s5, 10\n" \
"v_mfma_f32_16x16x32_bf16 a[40:43], a[152:155], a[168:171], a[40:43]\n" \
"s_add_u32 s89, s42, 0x2000\n" \
"buffer_store_dwordx4 v[200:203], v20, s[80:83], s89 offen offset:0 sc1\n" \
"v_mfma_f32_16x16x32_bf16 a[40:43], a[216:219], a[232:235], a[40:43]\n" \
"ds_read_b64_tr_b16 a[252:253], v39 offset:8192\n" \
"s_lshl_b32 s14, s14, 15\n" \
"v_mfma_f32_16x16x32_bf16 a[56:59], a[156:159], a[168:171], a[56:59]\n" \
"ds_read_b64_tr_b16 a[254:255], v39 offset:9216\n" \
"s_or_b32 s20, s4, 0x26c00\n" \
"v_mfma_f32_16x16x32_bf16 a[56:59], a[220:223], a[232:235], a[56:59]\n" \
"s_or_b32 s13, s22, 0x8000\n" \
"s_add_i32 s22, s22, 0x14000\n" \
"v_mfma_f32_16x16x32_bf16 a[60:63], a[172:175], a[156:159], a[60:63]\n" \
"s_bitset1_b32 s23, 16\n" \
"s_or_b32 s24, s7, 0x5be8020\n" \
"v_mfma_f32_16x16x32_bf16 a[60:63], a[236:239], a[220:223], a[60:63]\n" \
"s_addk_i32 s84, 0x2000\n" \
"v_mfma_f32_16x16x32_bf16 a[52:55], a[164:167], a[156:159], a[52:55]\n" \
".fill (L_pb + 73440 - .) / 4, 4, 0xBF800000\n" \
"L_pp18:\n" \
"s_cbranch_execz L_pret\n" \
"buffer_load_dwordx4 v25, s[76:79], s84 offen offset:3072 lds\n" \
"v_mfma_f32_16x16x32_bf16 a[52:55], a[228:231], a[220:223], a[52:55]\n" \
"s_and_b32 s57, s27, 0xffff\n" \
"s_mov_b32 s58, 0x6000000\n" \
"v_mfma_f32_16x16x32_bf16 a[48:51], a[160:163], a[156:159], a[48:51]\n" \
"s_mov_b32 s56, s26\n" \
"s_mov_b32 s59, s11\n" \
"v_mfma_f32_16x16x32_bf16 a[48:51], a[224:227], a[220:223], a[48:51]\n" \
"s_add_i32 s60, s60, 0x3000000\n" \
"s_add_i32 s61, s61, 0x411c000\n" \
"v_mfma_f32_16x16x32_bf16 a[64:67], a[164:167], a[140:143], a[64:67]\n" \
"s_sub_i32 s19, 14, s18\n" \
"s_mov_b32 s21, 0x71800000\n" \
"v_mfma_f32_16x16x32_bf16 a[64:67], a[228:231], a[204:207], a[64:67]\n" \
"s_mov_b32 s4, L_gsw-L_gtr\n" \
"v_mfma_f32_16x16x32_bf16 a[80:83], a[172:175], a[140:143], a[80:83]\n" \
"s_cmp_eq_u32 s19, 0\n" \
"s_cselect_b32 s4, L_gs7-L_gtr, s4\n" \
"v_mfma_f32_16x16x32_bf16 a[80:83], a[236:239], a[204:207], a[80:83]\n" \
"s_cmp_eq_u32 s16, 0\n" \
"s_cselect_b32 s4, 0, s4\n" \
"v_mfma_f32_16x16x32_bf16 a[68:71], a[140:143], a[168:171], a[68:71]\n" \
"s_getpc_b64 s[28:29]\n" \
"L_gp1_%=:\n" \
"s_add_u32 s28, s28, L_gtr-L_gp1_%=\n" \
"s_addc_u32 s29, s29, 0\n" \
"v_mfma_f32_16x16x32_bf16 a[68:71], a[204:207], a[232:235], a[68:71]\n" \
"s_waitcnt vmcnt(11) lgkmcnt(0)\n" \
"s_barrier\n" \
"v_mfma_f32_16x16x32_bf16 v[116:119], a[176:179], a[144:147], v[116:119]\n" \
"s_add_u32 m0, s40, 0x0\n" \
"s_add_u32 s85, s39, 0x10000\n" \
"v_mfma_f32_16x16x32_bf16 v[116:119], a[240:243], a[208:211], v[116:119]\n" \
"buffer_load_dwordx4 v20, s[80:83], s85 offen sc1 lds\n" \
"v_mfma_f32_16x16x32_bf16 v[120:123], a[180:183], a[144:147], v[120:123]\n" \
"ds_read_b64_tr_b16 a[160:161], v32 offset:32768\n" \
"v_mfma_f32_16x16x32_bf16 v[120:123], a[244:247], a[208:211], v[120:123]\n" \
"ds_read_b64_tr_b16 a[162:163], v32 offset:33280\n" \
"v_mfma_f32_16x16x32_bf16 v[124:127], a[184:187], a[144:147], v[124:127]\n" \
"s_add_u32 s28, s28, s4\n" \
"s_addc_u32 s29, s29, 0\n" \
"v_mfma_f32_16x16x32_bf16 v[124:127], a[248:251], a[208:211], v[124:127]\n" \
"v_mfma_f32_16x16x32_bf16 v[128:131], a[188:191], a[144:147], v[128:131]\n" \
"ds_read_b64_tr_b16 a[224:225], v32 offset:36864\n" \
"v_mfma_f32_16x16x32_bf16 v[128:131], a[252:255], a[208:211], v[128:131]\n" \
"ds_read_b128 a[144:147], v28 offset:40960\n" \
"v_mfma_f32_16x16x32_bf16 v[144:147], a[188:191], a[148:151], v[144:147]\n" \
"ds_read_b128 a[208:211], v29 offset:40960\n" \
"v_mfma_f32_16x16x32_bf16 v[144:147], a[252:255], a[212:215], v[144:147]\n" \
"s_add_u32 m0, s40, 0x1000\n" \
"s_add_u32 s85, s39, 0x11000\n" \
"v_mfma_f32_16x16x32_bf16 v[140:143], a[184:187], a[148:151], v[140:143]\n" \
"buffer_load_dwordx4 v20, s[80:83], s85 offen sc1 lds\n" \
"v_mfma_f32_16x16x32_bf16 v[140:143], a[248:251], a[212:215], v[140:143]\n" \
"ds_read_b64_tr_b16 a[226:227], v32 offset:37376\n" \
"v_mfma_f32_16x16x32_bf16 v[136:139], a[180:183], a[148:151], v[136:139]\n" \
"v_mfma_f32_16x16x32_bf16 v[136:139], a[244:247], a[212:215], v[136:139]\n" \
"ds_read_b64_tr_b16 a[164:165], v33 offset:32768\n" \
"v_mfma_f32_16x16x32_bf16 v[132:135], a[176:179], a[148:151], v[132:135]\n" \
"v_mfma_f32_16x16x32_bf16 v[132:135], a[240:243], a[212:215], v[132:135]\n" \
"ds_read_b128 a[148:151], v28 offset:43008\n" \
"v_mfma_f32_16x16x32_bf16 v[148:151], a[176:179], a[152:155], v[148:151]\n" \
"ds_read_b128 a[212:215], v29 offset:43008\n" \
"v_mfma_f32_16x16x32_bf16 v[148:151], a[240:243], a[216:219], v[148:151]\n" \
"s_add_u32 m0, s40, 0x3000\n" \
"s_movk_i32 s84, 0x400\n" \
"v_mfma_f32_16x16x32_bf16 v[152:155], a[180:183], a[152:155], v[152:155]\n" \
"buffer_load_dwordx4 v21, s[76:79], s84 offen offset:0 lds\n" \
"v_mfma_f32_16x16x32_bf16 v[152:155], a[244:247], a[216:219], v[152:155]\n" \
"ds_read_b64_tr_b16 a[166:167], v33 offset:33280\n" \
"v_mfma_f32_16x16x32_bf16 v[156:159], a[184:187], a[152:155], v[156:159]\n" \
"ds_read_b64_tr_b16 a[228:229], v33 offset:36864\n" \
"v_mfma_f32_16x16x32_bf16 v[156:159], a[248:251], a[216:219], v[156:159]\n" \
"s_add_u32 m0, s31, 0x0\n" \
"s_mov_b32 s84, 0x400000\n" \
"v_mfma_f32_16x16x32_bf16 v[160:163], a[188:191], a[152:155], v[160:163]\n" \
"buffer_load_dwordx4 v26, s[68:71], s84 offen offset:0 lds\n" \
"v_mfma_f32_16x16x32_bf16 v[160:163], a[252:255], a[216:219], v[160:163]\n" \
"ds_read_b128 a[152:155], v28 offset:45056\n" \
"v_mfma_f32_16x16x32_bf16 v[176:179], a[188:191], a[156:159], v[176:179]\n" \
"ds_read_b128 a[216:219], v29 offset:45056\n" \
"v_mfma_f32_16x16x32_bf16 v[176:179], a[252:255], a[220:223], v[176:179]\n" \
"ds_read_b64_tr_b16 a[230:231], v33 offset:37376\n" \
"v_mfma_f32_16x16x32_bf16 v[172:175], a[184:187], a[156:159], v[172:175]\n" \
"ds_read_b64_tr_b16 a[172:173], v35 offset:32768\n" \
"v_mfma_f32_16x16x32_bf16 v[172:175], a[248:251], a[220:223], v[172:175]\n" \
"v_mfma_f32_16x16x32_bf16 v[168:171], a[180:183], a[156:159], v[168:171]\n" \
"s_add_u32 s88, s42, 0x2000\n" \
"buffer_store_dwordx4 v[204:207], v20, s[80:83], s88 offen offset:1024 sc1\n" \
"v_mfma_f32_16x16x32_bf16 v[168:171], a[244:247], a[220:223], v[168:171]\n" \
"ds_read_b64_tr_b16 a[174:175], v35 offset:33280\n" \
"v_mfma_f32_16x16x32_bf16 v[164:167], a[176:179], a[156:159], v[164:167]\n" \
"v_mfma_f32_16x16x32_bf16 v[164:167], a[240:243], a[220:223], v[164:167]\n" \
"ds_read_b128 a[156:159], v28 offset:47104\n" \
"v_mfma_f32_16x16x32_bf16 v[84:87], a[176:179], a[136:139], v[84:87]\n" \
"ds_read_b128 a[220:223], v29 offset:47104\n" \
"v_mfma_f32_16x16x32_bf16 v[84:87], a[240:243], a[200:203], v[84:87]\n" \
"ds_read_b64_tr_b16 a[236:237], v35 offset:36864\n" \
"v_mfma_f32_16x16x32_bf16 v[88:91], a[180:183], a[136:139], v[88:91]\n" \
"v_mfma_f32_16x16x32_bf16 v[88:91], a[244:247], a[200:203], v[88:91]\n" \
"s_addk_i32 s84, 0x7c00\n" \
"s_nop 0\n" \
"v_mfma_f32_16x16x32_bf16 v[92:95], a[184:187], a[136:139], v[92:95]\n" \
"buffer_load_dwordx4 v26, s[68:71], s84 offen offset:1024 lds\n" \
"v_mfma_f32_16x16x32_bf16 v[92:95], a[248:251], a[200:203], v[92:95]\n" \
"ds_read_b64_tr_b16 a[238:239], v35 offset:37376\n" \
"v_mfma_f32_16x16x32_bf16 v[96:99], a[188:191], a[136:139], v[96:99]\n" \
"v_mfma_f32_16x16x32_bf16 v[96:99], a[252:255], a[200:203], v[96:99]\n" \
"ds_read_b128 a[136:139], v28 offset:36864\n" \
"v_mfma_f32_16x16x32_bf16 v[112:115], a[188:191], a[140:143], v[112:115]\n" \
"ds_read_b128 a[200:203], v29 offset:36864\n" \
"v_mfma_f32_16x16x32_bf16 v[112:115], a[252:255], a[204:207], v[112:115]\n" \
"ds_read_b64_tr_b16 a[168:169], v34 offset:32768\n" \
"v_mfma_f32_16x16x32_bf16 v[108:111], a[184:187], a[140:143], v[108:111]\n" \
"s_addk_i32 s84, 0x7c00\n" \
"s_nop 0\n" \
"v_mfma_f32_16x16x32_bf16 v[108:111], a[248:251], a[204:207], v[108:111]\n" \
"buffer_load_dwordx4 v27, s[68:71], s84 offen offset:2048 lds\n" \
"v_mfma_f32_16x16x32_bf16 v[104:107], a[180:183], a[140:143], v[104:107]\n" \
"ds_read_b64_tr_b16 a[170:171], v34 offset:33280\n" \
"v_mfma_f32_16x16x32_bf16 v[104:107], a[244:247], a[204:207], v[104:107]\n" \
"ds_read_b64_tr_b16 a[232:233], v34 offset:36864\n" \
"v_mfma_f32_16x16x32_bf16 v[100:103], a[176:179], a[140:143], v[100:103]\n" \
"v_mfma_f32_16x16x32_bf16 v[100:103], a[240:243], a[204:207], v[100:103]\n" \
"ds_read_b128 a[140:143], v28 offset:38912\n" \
"v_mfma_f32_16x16x32_bf16 v[52:55], a[176:179], a[128:131], v[52:55]\n" \
"ds_read_b128 a[204:207], v29 offset:38912\n" \
"v_mfma_f32_16x16x32_bf16 v[52:55], a[240:243], a[192:195], v[52:55]\n" \
"s_addk_i32 s84, 0x7c00\n" \
"s_nop 0\n" \
"v_mfma_f32_16x16x32_bf16 v[56:59], a[180:183], a[128:131], v[56:59]\n" \
"buffer_load_dwordx4 v27, s[68:71], s84 offen offset:3072 lds\n" \
"v_mfma_f32_16x16x32_bf16 v[56:59], a[244:247], a[192:195], v[56:59]\n" \
"ds_read_b64_tr_b16 a[234:235], v34 offset:37376\n" \
"v_mfma_f32_16x16x32_bf16 v[60:63], a[184:187], a[128:131], v[60:63]\n" \
"v_mfma_f32_16x16x32_bf16 v[60:63], a[248:251], a[192:195], v[60:63]\n" \
"v_mfma_f32_16x16x32_bf16 v[64:67], a[188:191], a[128:131], v[64:67]\n" \
"v_mfma_f32_16x16x32_bf16 v[64:67], a[252:255], a[192:195], v[64:67]\n" \
"ds_read_b128 a[128:131], v28 offset:32768\n" \
"v_mfma_f32_16x16x32_bf16 v[80:83], a[188:191], a[132:135], v[80:83]\n" \
"ds_read_b128 a[192:195], v29 offset:32768\n" \
"v_mfma_f32_16x16x32_bf16 v[80:83], a[252:255], a[196:199], v[80:83]\n" \
"v_mfma_f32_16x16x32_bf16 v[76:79], a[184:187], a[132:135], v[76:79]\n" \
"v_mfma_f32_16x16x32_bf16 v[76:79], a[248:251], a[196:199], v[76:79]\n" \
"v_mfma_f32_16x16x32_bf16 v[72:75], a[180:183], a[132:135], v[72:75]\n" \
"v_mfma_f32_16x16x32_bf16 v[72:75], a[244:247], a[196:199], v[72:75]\n" \
"v_mfma_f32_16x16x32_bf16 v[68:71], a[176:179], a[132:135], v[68:71]\n" \
"v_mfma_f32_16x16x32_bf16 v[68:71], a[240:243], a[196:199], v[68:71]\n" \
"ds_read_b128 a[132:135], v28 offset:34816\n" \
"ds_read_b128 a[196:199], v29 offset:34816\n" \
"s_waitcnt lgkmcnt(15)\n" \
"s_nop 0\n" \
"v_mfma_f32_16x16x32_bf16 a[0:3], a[160:163], a[144:147], a[0:3]\n" \
"s_add_u32 s89, s42, 0x2000\n" \
"buffer_store_dwordx4 v[208:211], v20, s[80:83], s89 offen offset:2048 sc1\n" \
"v_mfma_f32_16x16x32_bf16 a[0:3], a[224:227], a[208:211], a[0:3]\n" \
"ds_read_b64_tr_b16 a[176:177], v36 offset:32768\n" \
"v_mfma_f32_16x16x32_bf16 a[4:7], a[164:167], a[144:147], a[4:7]\n" \
"ds_read_b64_tr_b16 a[178:179], v36 offset:33792\n" \
"v_mfma_f32_16x16x32_bf16 a[4:7], a[228:231], a[208:211], a[4:7]\n" \
"ds_read_b64_tr_b16 a[240:241], v36 offset:40960\n" \
"v_mfma_f32_16x16x32_bf16 a[12:15], a[172:175], a[144:147], a[12:15]\n" \
"ds_read_b64_tr_b16 a[242:243], v36 offset:41984\n" \
"s_waitcnt lgkmcnt(15)\n" \
"v_mfma_f32_16x16x32_bf16 a[12:15], a[236:239], a[208:211], a[12:15]\n" \
"s_waitcnt lgkmcnt(12)\n" \
"v_mfma_f32_16x16x32_bf16 a[8:11], a[144:147], a[168:171], a[8:11]\n" \
"ds_read_b64_tr_b16 a[180:181], v37 offset:32768\n" \
"s_waitcnt lgkmcnt(9)\n" \
"s_nop 0\n" \
"v_mfma_f32_16x16x32_bf16 a[8:11], a[208:211], a[232:235], a[8:11]\n" \
"v_mfma_f32_16x16x32_bf16 a[24:27], a[148:151], a[168:171], a[24:27]\n" \
"ds_read_b64_tr_b16 a[182:183], v37 offset:33792\n" \
"v_mfma_f32_16x16x32_bf16 a[24:27], a[212:215], a[232:235], a[24:27]\n" \
"ds_read_b64_tr_b16 a[244:245], v37 offset:40960\n" \
"v_mfma_f32_16x16x32_bf16 a[28:31], a[172:175], a[148:151], a[28:31]\n" \
"v_mfma_f32_16x16x32_bf16 a[28:31], a[236:239], a[212:215], a[28:31]\n" \
"ds_read_b64_tr_b16 a[246:247], v37 offset:41984\n" \
"v_mfma_f32_16x16x32_bf16 a[20:23], a[164:167], a[148:151], a[20:23]\n" \
"v_mfma_f32_16x16x32_bf16 a[20:23], a[228:231], a[212:215], a[20:23]\n" \
"ds_read_b64_tr_b16 a[184:185], v38 offset:32768\n" \
"v_mfma_f32_16x16x32_bf16 a[16:19], a[160:163], a[148:151], a[16:19]\n" \
"ds_read_b64_tr_b16 a[186:187], v38 offset:33792\n" \
"v_mfma_f32_16x16x32_bf16 a[16:19], a[224:227], a[212:215], a[16:19]\n" \
"v_mfma_f32_16x16x32_bf16 a[32:35], a[160:163], a[152:155], a[32:35]\n" \
"s_add_u32 s88, s42, 0x2000\n" \
"buffer_store_dwordx4 v[212:215], v20, s[80:83], s88 offen offset:3072 sc1\n" \
"v_mfma_f32_16x16x32_bf16 a[32:35], a[224:227], a[216:219], a[32:35]\n" \
"ds_read_b64_tr_b16 a[248:249], v38 offset:40960\n" \
"v_mfma_f32_16x16x32_bf16 a[36:39], a[164:167], a[152:155], a[36:39]\n" \
"v_lshlrev_b32_e32 v45, 3, v20\n" \
"v_add_u32_e32 v45, s30, v45\n" \
"v_mfma_f32_16x16x32_bf16 a[36:39], a[228:231], a[216:219], a[36:39]\n" \
"s_getpc_b64 s[86:87]\n" \
"L_ct1_%=:\n" \
"s_add_u32 s86, s86, L_ap0-L_ct1_%=\n" \
"s_addc_u32 s87, s87, 0\n" \
"s_add_u32 m0, s40, 0x2000\n" \
"v_mfma_f32_16x16x32_bf16 a[44:47], a[172:175], a[152:155], a[44:47]\n" \
"global_load_lds_dword v45, s[86:87]\n" \
"v_mfma_f32_16x16x32_bf16 a[44:47], a[236:239], a[216:219], a[44:47]\n" \
"ds_read_b64_tr_b16 a[250:251], v38 offset:41984\n" \
"v_mfma_f32_16x16x32_bf16 a[40:43], a[152:155], a[168:171], a[40:43]\n" \
"ds_read_b64_tr_b16 a[188:189], v39 offset:32768\n" \
"v_mfma_f32_16x16x32_bf16 a[40:43], a[216:219], a[232:235], a[40:43]\n" \
"ds_read_b64_tr_b16 a[190:191], v39 offset:33792\n" \
"v_mfma_f32_16x16x32_bf16 a[56:59], a[156:159], a[168:171], a[56:59]\n" \
"ds_read_b64_tr_b16 a[252:253], v39 offset:40960\n" \
"v_mfma_f32_16x16x32_bf16 a[56:59], a[220:223], a[232:235], a[56:59]\n" \
"ds_read_b64_tr_b16 a[254:255], v39 offset:41984\n" \
"v_mfma_f32_16x16x32_bf16 a[60:63], a[172:175], a[156:159], a[60:63]\n" \
"v_mfma_f32_16x16x32_bf16 a[60:63], a[236:239], a[220:223], a[60:63]\n" \
"v_mfma_f32_16x16x32_bf16 a[52:55], a[164:167], a[156:159], a[52:55]\n" \
"v_mfma_f32_16x16x32_bf16 a[52:55], a[228:231], a[220:223], a[52:55]\n" \
"v_mfma_f32_16x16x32_bf16 a[48:51], a[160:163], a[156:159], a[48:51]\n" \
"v_mfma_f32_16x16x32_bf16 a[48:51], a[224:227], a[220:223], a[48:51]\n" \
"v_mfma_f32_16x16x32_bf16 a[64:67], a[164:167], a[140:143], a[64:67]\n" \
"v_mfma_f32_16x16x32_bf16 a[64:67], a[228:231], a[204:207], a[64:67]\n" \
"v_mfma_f32_16x16x32_bf16 a[80:83], a[172:175], a[140:143], a[80:83]\n" \
"v_mfma_f32_16x16x32_bf16 a[80:83], a[236:239], a[204:207], a[80:83]\n" \
"v_mfma_f32_16x16x32_bf16 a[68:71], a[140:143], a[168:171], a[68:71]\n" \
"v_mfma_f32_16x16x32_bf16 a[68:71], a[204:207], a[232:235], a[68:71]\n" \
"s_waitcnt vmcnt(3) lgkmcnt(0)\n" \
"s_barrier\n" \
"v_xor_b32_e32 v28, 0x20000, v28\n" \
"v_xor_b32_e32 v29, 0x20000, v29\n" \
"v_mfma_f32_16x16x32_bf16 v[116:119], a[176:179], a[144:147], v[116:119]\n" \
"ds_read_b64_tr_b16 a[160:161], v32 offset:0\n" \
"v_accvgpr_read_b32 v184, a0\n" \
"v_mfma_f32_16x16x32_bf16 v[116:119], a[240:243], a[208:211], v[116:119]\n" \
"v_accvgpr_read_b32 v185, a1\n" \
"v_accvgpr_read_b32 v186, a2\n" \
"v_mfma_f32_16x16x32_bf16 v[120:123], a[180:183], a[144:147], v[120:123]\n" \
"v_accvgpr_read_b32 v187, a3\n" \
"v_accvgpr_read_b32 v188, a4\n" \
"v_mfma_f32_16x16x32_bf16 v[120:123], a[244:247], a[208:211], v[120:123]\n" \
"ds_read_b64_tr_b16 a[162:163], v32 offset:512\n" \
"v_accvgpr_read_b32 v189, a5\n" \
"v_mfma_f32_16x16x32_bf16 v[124:127], a[184:187], a[144:147], v[124:127]\n" \
"v_accvgpr_read_b32 v190, a6\n" \
"v_accvgpr_read_b32 v191, a7\n" \
"v_mfma_f32_16x16x32_bf16 v[124:127], a[248:251], a[208:211], v[124:127]\n" \
"v_accvgpr_read_b32 v196, a12\n" \
"v_accvgpr_read_b32 v197, a13\n" \
"v_mfma_f32_16x16x32_bf16 v[128:131], a[188:191], a[144:147], v[128:131]\n" \
"ds_read_b64_tr_b16 a[224:225], v32 offset:4096\n" \
"v_accvgpr_read_b32 v198, a14\n" \
"v_mfma_f32_16x16x32_bf16 v[128:131], a[252:255], a[208:211], v[128:131]\n" \
"ds_read_b128 a[144:147], v28 offset:8192\n" \
"v_accvgpr_read_b32 v199, a15\n" \
"v_mfma_f32_16x16x32_bf16 v[144:147], a[188:191], a[148:151], v[144:147]\n" \
"ds_read_b128 a[208:211], v29 offset:8192\n" \
"v_accvgpr_read_b32 v192, a8\n" \
"v_mfma_f32_16x16x32_bf16 v[144:147], a[252:255], a[212:215], v[144:147]\n" \
"ds_read_b64_tr_b16 a[226:227], v32 offset:4608\n" \
"v_accvgpr_read_b32 v193, a9\n" \
"v_mfma_f32_16x16x32_bf16 v[140:143], a[184:187], a[148:151], v[140:143]\n" \
"v_accvgpr_read_b32 v194, a10\n" \
"v_accvgpr_read_b32 v195, a11\n" \
"v_mfma_f32_16x16x32_bf16 v[140:143], a[248:251], a[212:215], v[140:143]\n" \
"v_accvgpr_read_b32 v208, a24\n" \
"v_accvgpr_read_b32 v209, a25\n" \
"v_mfma_f32_16x16x32_bf16 v[136:139], a[180:183], a[148:151], v[136:139]\n" \
"v_accvgpr_read_b32 v210, a26\n" \
"v_accvgpr_read_b32 v211, a27\n" \
"v_mfma_f32_16x16x32_bf16 v[136:139], a[244:247], a[212:215], v[136:139]\n" \
"ds_read_b64_tr_b16 a[164:165], v33 offset:0\n" \
"v_accvgpr_read_b32 v212, a28\n" \
"v_mfma_f32_16x16x32_bf16 v[132:135], a[176:179], a[148:151], v[132:135]\n" \
"v_accvgpr_read_b32 v213, a29\n" \
"v_accvgpr_read_b32 v214, a30\n" \
"v_mfma_f32_16x16x32_bf16 v[132:135], a[240:243], a[212:215], v[132:135]\n" \
"ds_read_b128 a[148:151], v28 offset:10240\n" \
"v_accvgpr_read_b32 v215, a31\n" \
"v_mfma_f32_16x16x32_bf16 v[148:151], a[176:179], a[152:155], v[148:151]\n" \
"ds_read_b128 a[212:215], v29 offset:10240\n" \
"v_accvgpr_read_b32 v204, a20\n" \
"v_mfma_f32_16x16x32_bf16 v[148:151], a[240:243], a[216:219], v[148:151]\n" \
"ds_read_b64_tr_b16 a[166:167], v33 offset:512\n" \
"v_accvgpr_read_b32 v205, a21\n" \
"v_mfma_f32_16x16x32_bf16 v[152:155], a[180:183], a[152:155], v[152:155]\n" \
"v_accvgpr_read_b32 v206, a22\n" \
"v_accvgpr_read_b32 v207, a23\n" \
"v_mfma_f32_16x16x32_bf16 v[152:155], a[244:247], a[216:219], v[152:155]\n" \
"ds_read_b64_tr_b16 a[228:229], v33 offset:4096\n" \
"v_accvgpr_read_b32 v200, a16\n" \
"v_mfma_f32_16x16x32_bf16 v[156:159], a[184:187], a[152:155], v[156:159]\n" \
"v_accvgpr_read_b32 v201, a17\n" \
"v_accvgpr_read_b32 v202, a18\n" \
"v_mfma_f32_16x16x32_bf16 v[156:159], a[248:251], a[216:219], v[156:159]\n" \
"v_accvgpr_read_b32 v203, a19\n" \
"v_accvgpr_read_b32 v216, a32\n" \
"v_mfma_f32_16x16x32_bf16 v[160:163], a[188:191], a[152:155], v[160:163]\n" \
"ds_read_b64_tr_b16 a[230:231], v33 offset:4608\n" \
"v_accvgpr_read_b32 v217, a33\n" \
"v_mfma_f32_16x16x32_bf16 v[160:163], a[252:255], a[216:219], v[160:163]\n" \
"ds_read_b128 a[152:155], v28 offset:12288\n" \
"v_accvgpr_read_b32 v218, a34\n" \
"v_mfma_f32_16x16x32_bf16 v[176:179], a[188:191], a[156:159], v[176:179]\n" \
"ds_read_b128 a[216:219], v29 offset:12288\n" \
"v_accvgpr_read_b32 v219, a35\n" \
"v_mfma_f32_16x16x32_bf16 v[176:179], a[252:255], a[220:223], v[176:179]\n" \
"v_accvgpr_read_b32 v220, a36\n" \
"v_accvgpr_read_b32 v221, a37\n" \
"v_mfma_f32_16x16x32_bf16 v[172:175], a[184:187], a[156:159], v[172:175]\n" \
"ds_read_b64_tr_b16 a[172:173], v35 offset:0\n" \
"v_accvgpr_read_b32 v222, a38\n" \
"v_mfma_f32_16x16x32_bf16 v[172:175], a[248:251], a[220:223], v[172:175]\n" \
"v_accvgpr_read_b32 v223, a39\n" \
"v_accvgpr_read_b32 v228, a44\n" \
"v_mfma_f32_16x16x32_bf16 v[168:171], a[180:183], a[156:159], v[168:171]\n" \
"v_accvgpr_read_b32 v229, a45\n" \
"v_accvgpr_read_b32 v230, a46\n" \
"v_mfma_f32_16x16x32_bf16 v[168:171], a[244:247], a[220:223], v[168:171]\n" \
"ds_read_b64_tr_b16 a[174:175], v35 offset:512\n" \
"v_accvgpr_read_b32 v231, a47\n" \
"v_mfma_f32_16x16x32_bf16 v[164:167], a[176:179], a[156:159], v[164:167]\n" \
"v_accvgpr_read_b32 v224, a40\n" \
"v_accvgpr_read_b32 v225, a41\n" \
"v_mfma_f32_16x16x32_bf16 v[164:167], a[240:243], a[220:223], v[164:167]\n" \
"ds_read_b128 a[156:159], v28 offset:14336\n" \
"v_accvgpr_read_b32 v226, a42\n" \
"v_mfma_f32_16x16x32_bf16 v[84:87], a[176:179], a[136:139], v[84:87]\n" \
"ds_read_b128 a[220:223], v29 offset:14336\n" \
"v_accvgpr_read_b32 v227, a43\n" \
"v_mfma_f32_16x16x32_bf16 v[84:87], a[240:243], a[200:203], v[84:87]\n" \
"ds_read_b64_tr_b16 a[236:237], v35 offset:4096\n" \
"v_accvgpr_read_b32 v240, a56\n" \
"v_mfma_f32_16x16x32_bf16 v[88:91], a[180:183], a[136:139], v[88:91]\n" \
"v_accvgpr_read_b32 v241, a57\n" \
"v_accvgpr_read_b32 v242, a58\n" \
"v_mfma_f32_16x16x32_bf16 v[88:91], a[244:247], a[200:203], v[88:91]\n" \
"ds_read_b64_tr_b16 a[238:239], v35 offset:4608\n" \
"v_accvgpr_read_b32 v243, a59\n" \
"v_mfma_f32_16x16x32_bf16 v[92:95], a[184:187], a[136:139], v[92:95]\n" \
"v_accvgpr_read_b32 v244, a60\n" \
"v_accvgpr_read_b32 v245, a61\n" \
"v_mfma_f32_16x16x32_bf16 v[92:95], a[248:251], a[200:203], v[92:95]\n" \
"v_accvgpr_read_b32 v246, a62\n" \
"v_accvgpr_read_b32 v247, a63\n" \
"v_mfma_f32_16x16x32_bf16 v[96:99], a[188:191], a[136:139], v[96:99]\n" \
"v_accvgpr_read_b32 v236, a52\n" \
"v_accvgpr_read_b32 v237, a53\n" \
"v_mfma_f32_16x16x32_bf16 v[96:99], a[252:255], a[200:203], v[96:99]\n" \
"ds_read_b128 a[136:139], v28 offset:4096\n" \
"v_accvgpr_read_b32 v238, a54\n" \
"v_mfma_f32_16x16x32_bf16 v[112:115], a[188:191], a[140:143], v[112:115]\n" \
"ds_read_b128 a[200:203], v29 offset:4096\n" \
"v_accvgpr_read_b32 v239, a55\n" \
"v_mfma_f32_16x16x32_bf16 v[112:115], a[252:255], a[204:207], v[112:115]\n" \
"ds_read_b64_tr_b16 a[168:169], v34 offset:0\n" \
"v_accvgpr_read_b32 v232, a48\n" \
"v_mfma_f32_16x16x32_bf16 v[108:111], a[184:187], a[140:143], v[108:111]\n" \
"ds_read_b64_tr_b16 a[170:171], v34 offset:512\n" \
"v_accvgpr_read_b32 v233, a49\n" \
"v_mfma_f32_16x16x32_bf16 v[108:111], a[248:251], a[204:207], v[108:111]\n" \
"v_accvgpr_read_b32 v234, a50\n" \
"v_accvgpr_read_b32 v235, a51\n" \
"v_mfma_f32_16x16x32_bf16 v[104:107], a[180:183], a[140:143], v[104:107]\n" \
"v_accvgpr_read_b32 v180, a64\n" \
"v_accvgpr_read_b32 v181, a65\n" \
"v_mfma_f32_16x16x32_bf16 v[104:107], a[244:247], a[204:207], v[104:107]\n" \
"ds_read_b64_tr_b16 a[232:233], v34 offset:4096\n" \
"v_accvgpr_read_b32 v182, a66\n" \
"v_mfma_f32_16x16x32_bf16 v[100:103], a[176:179], a[140:143], v[100:103]\n" \
"v_accvgpr_read_b32 v183, a67\n" \
"v_accvgpr_read_b32 v48, a80\n" \
"v_mfma_f32_16x16x32_bf16 v[100:103], a[240:243], a[204:207], v[100:103]\n" \
"ds_read_b128 a[140:143], v28 offset:6144\n" \
"v_accvgpr_read_b32 v49, a81\n" \
"v_mfma_f32_16x16x32_bf16 v[52:55], a[176:179], a[128:131], v[52:55]\n" \
"ds_read_b128 a[204:207], v29 offset:6144\n" \
"v_accvgpr_read_b32 v50, a82\n" \
"v_mfma_f32_16x16x32_bf16 v[52:55], a[240:243], a[192:195], v[52:55]\n" \
"ds_read_b64_tr_b16 a[234:235], v34 offset:4608\n" \
"v_accvgpr_read_b32 v51, a83\n" \
"v_mfma_f32_16x16x32_bf16 v[56:59], a[180:183], a[128:131], v[56:59]\n" \
"v_accvgpr_read_b32 v252, a68\n" \
"v_accvgpr_read_b32 v253, a69\n" \
"v_mfma_f32_16x16x32_bf16 v[56:59], a[244:247], a[192:195], v[56:59]\n" \
"v_accvgpr_read_b32 v254, a70\n" \
"v_accvgpr_read_b32 v255, a71\n" \
"v_mfma_f32_16x16x32_bf16 v[60:63], a[184:187], a[128:131], v[60:63]\n" \
"v_lshrrev_b32_e64 v24, 9, v20\n" \
"v_and_b32_e32 v26, 0x1ff, v20\n" \
"v_mfma_f32_16x16x32_bf16 v[60:63], a[248:251], a[192:195], v[60:63]\n" \
"v_lshl_add_u32 v24, v24, 3, v26\n" \
"v_lshrrev_b32_e64 v25, 8, v20\n" \
"v_mfma_f32_16x16x32_bf16 v[64:67], a[188:191], a[128:131], v[64:67]\n" \
"v_mul_u32_u24_e32 v25, 0x24700, v25\n" \
"v_add_u32_e64 v25, v25, v20\n" \
"v_mfma_f32_16x16x32_bf16 v[64:67], a[252:255], a[192:195], v[64:67]\n" \
"ds_read_b128 a[128:131], v28 offset:0\n" \
"v_lshrrev_b32_e64 v26, 2, v20\n" \
"v_mfma_f32_16x16x32_bf16 v[80:83], a[188:191], a[132:135], v[80:83]\n" \
"ds_read_b128 a[192:195], v29 offset:0\n" \
"v_add_u32_e64 v26, s45, v26\n" \
"v_mfma_f32_16x16x32_bf16 v[80:83], a[252:255], a[196:199], v[80:83]\n" \
"v_add_u32_e32 v27, s46, v20\n" \
"v_add_u32_e32 v21, s43, v27\n" \
"v_mfma_f32_16x16x32_bf16 v[76:79], a[184:187], a[132:135], v[76:79]\n" \
"v_mfma_f32_16x16x32_bf16 v[76:79], a[248:251], a[196:199], v[76:79]\n" \
"v_mfma_f32_16x16x32_bf16 v[72:75], a[180:183], a[132:135], v[72:75]\n" \
"v_mfma_f32_16x16x32_bf16 v[72:75], a[244:247], a[196:199], v[72:75]\n" \
"v_mfma_f32_16x16x32_bf16 v[68:71], a[176:179], a[132:135], v[68:71]\n" \
"v_mfma_f32_16x16x32_bf16 v[68:71], a[240:243], a[196:199], v[68:71]\n" \
"ds_read_b128 a[132:135], v28 offset:2048\n" \
"ds_read_b128 a[196:199], v29 offset:2048\n" \
"s_waitcnt lgkmcnt(15)\n" \
"s_nop 0\n" \
"v_mfma_f32_16x16x32_bf16 v[184:187], a[160:163], a[144:147], v[184:187]\n" \
"v_bfe_u32 v44, v20, 2, 5\n" \
"ds_read_b64_tr_b16 a[176:177], v36 offset:0\n" \
"v_mfma_f32_16x16x32_bf16 v[184:187], a[224:227], a[208:211], v[184:187]\n" \
"buffer_load_dword v44, v44, s[80:83], s38 offen offset:0 sc1\n" \
"v_mfma_f32_16x16x32_bf16 v[188:191], a[164:167], a[144:147], v[188:191]\n" \
"ds_read_b64_tr_b16 a[178:179], v36 offset:1024\n" \
"v_mfma_f32_16x16x32_bf16 v[188:191], a[228:231], a[208:211], v[188:191]\n" \
"ds_read_b64_tr_b16 a[240:241], v36 offset:8192\n" \
"v_mfma_f32_16x16x32_bf16 v[196:199], a[172:175], a[144:147], v[196:199]\n" \
"ds_read_b64_tr_b16 a[242:243], v36 offset:9216\n" \
"s_waitcnt lgkmcnt(15)\n" \
"v_mfma_f32_16x16x32_bf16 v[196:199], a[236:239], a[208:211], v[196:199]\n" \
"s_waitcnt lgkmcnt(12)\n" \
"v_mfma_f32_16x16x32_bf16 v[192:195], a[144:147], a[168:171], v[192:195]\n" \
"ds_read_b64_tr_b16 a[180:181], v37 offset:0\n" \
"s_waitcnt lgkmcnt(9)\n" \
"s_nop 0\n" \
"v_mfma_f32_16x16x32_bf16 v[192:195], a[208:211], a[232:235], v[192:195]\n" \
"v_mfma_f32_16x16x32_bf16 v[208:211], a[148:151], a[168:171], v[208:211]\n" \
"ds_read_b64_tr_b16 a[182:183], v37 offset:1024\n" \
"v_mfma_f32_16x16x32_bf16 v[208:211], a[212:215], a[232:235], v[208:211]\n" \
"ds_read_b64_tr_b16 a[244:245], v37 offset:8192\n" \
"v_mfma_f32_16x16x32_bf16 v[212:215], a[172:175], a[148:151], v[212:215]\n" \
"v_mfma_f32_16x16x32_bf16 v[212:215], a[236:239], a[212:215], v[212:215]\n" \
"ds_read_b64_tr_b16 a[246:247], v37 offset:9216\n" \
"v_cvt_pk_bf16_f32 v188, v188, v189\n" \
"v_mfma_f32_16x16x32_bf16 v[204:207], a[164:167], a[148:151], v[204:207]\n" \
"v_cvt_pk_bf16_f32 v189, v190, v191\n" \
"v_cvt_pk_bf16_f32 v190, v198, v199\n" \
"v_mfma_f32_16x16x32_bf16 v[204:207], a[228:231], a[212:215], v[204:207]\n" \
"ds_read_b64_tr_b16 a[184:185], v38 offset:0\n" \
"v_mov_b32_e32 v191, 0x3f80\n" \
"v_mfma_f32_16x16x32_bf16 v[200:203], a[160:163], a[148:151], v[200:203]\n" \
"ds_read_b64_tr_b16 a[186:187], v38 offset:1024\n" \
"v_cvt_pk_bf16_f32 v184, v184, v185\n" \
"v_mfma_f32_16x16x32_bf16 v[200:203], a[224:227], a[212:215], v[200:203]\n" \
"v_cvt_pk_bf16_f32 v185, v186, v187\n" \
"v_cvt_pk_bf16_f32 v186, v196, v197\n" \
"v_mfma_f32_16x16x32_bf16 v[216:219], a[160:163], a[152:155], v[216:219]\n" \
"ds_read_b64_tr_b16 a[248:249], v38 offset:8192\n" \
"v_mov_b32_e64 v187, 0\n" \
"v_mfma_f32_16x16x32_bf16 v[216:219], a[224:227], a[216:219], v[216:219]\n" \
"ds_read_b64_tr_b16 a[250:251], v38 offset:9216\n" \
"v_mfma_f32_16x16x32_bf16 v[220:223], a[164:167], a[152:155], v[220:223]\n" \
"v_mfma_f32_16x16x32_bf16 v[220:223], a[228:231], a[216:219], v[220:223]\n" \
"ds_read_b64_tr_b16 a[188:189], v39 offset:0\n" \
"v_cvt_pk_bf16_f32 v204, v204, v205\n" \
"v_mfma_f32_16x16x32_bf16 v[228:231], a[172:175], a[152:155], v[228:231]\n" \
"v_cvt_pk_bf16_f32 v205, v206, v207\n" \
"v_cvt_pk_bf16_f32 v206, v214, v215\n" \
"v_mfma_f32_16x16x32_bf16 v[228:231], a[236:239], a[216:219], v[228:231]\n" \
"ds_read_b64_tr_b16 a[190:191], v39 offset:1024\n" \
"v_mov_b32_e32 v207, 0x3f80\n" \
"v_mfma_f32_16x16x32_bf16 v[224:227], a[152:155], a[168:171], v[224:227]\n" \
"ds_read_b64_tr_b16 a[252:253], v39 offset:8192\n" \
"v_cvt_pk_bf16_f32 v200, v200, v201\n" \
"v_mfma_f32_16x16x32_bf16 v[224:227], a[216:219], a[232:235], v[224:227]\n" \
"v_cvt_pk_bf16_f32 v201, v202, v203\n" \
"v_cvt_pk_bf16_f32 v202, v212, v213\n" \
"v_mfma_f32_16x16x32_bf16 v[240:243], a[156:159], a[168:171], v[240:243]\n" \
"ds_read_b64_tr_b16 a[254:255], v39 offset:9216\n" \
"v_mov_b32_e64 v203, 0\n" \
"v_mfma_f32_16x16x32_bf16 v[240:243], a[220:223], a[232:235], v[240:243]\n" \
"v_mfma_f32_16x16x32_bf16 v[244:247], a[172:175], a[156:159], v[244:247]\n" \
"v_mfma_f32_16x16x32_bf16 v[244:247], a[236:239], a[220:223], v[244:247]\n" \
"v_cvt_pk_bf16_f32 v220, v220, v221\n" \
"v_cvt_pk_bf16_f32 v221, v222, v223\n" \
"v_mfma_f32_16x16x32_bf16 v[236:239], a[164:167], a[156:159], v[236:239]\n" \
"v_cvt_pk_bf16_f32 v222, v230, v231\n" \
"v_mov_b32_e32 v223, 0x3f80\n" \
"v_mfma_f32_16x16x32_bf16 v[236:239], a[228:231], a[220:223], v[236:239]\n" \
"v_cvt_pk_bf16_f32 v216, v216, v217\n" \
"v_cvt_pk_bf16_f32 v217, v218, v219\n" \
"v_mfma_f32_16x16x32_bf16 v[232:235], a[160:163], a[156:159], v[232:235]\n" \
"v_cvt_pk_bf16_f32 v218, v228, v229\n" \
"v_mov_b32_e64 v219, 0\n" \
"v_mfma_f32_16x16x32_bf16 v[232:235], a[224:227], a[220:223], v[232:235]\n" \
"v_mfma_f32_16x16x32_bf16 v[180:183], a[164:167], a[140:143], v[180:183]\n" \
"v_mfma_f32_16x16x32_bf16 v[180:183], a[228:231], a[204:207], v[180:183]\n" \
"v_mfma_f32_16x16x32_bf16 v[48:51], a[172:175], a[140:143], v[48:51]\n" \
"v_mfma_f32_16x16x32_bf16 v[48:51], a[236:239], a[204:207], v[48:51]\n" \
"v_cvt_pk_bf16_f32 v236, v236, v237\n" \
"v_cvt_pk_bf16_f32 v237, v238, v239\n" \
"v_mfma_f32_16x16x32_bf16 v[252:255], a[140:143], a[168:171], v[252:255]\n" \
"v_cvt_pk_bf16_f32 v238, v246, v247\n" \
"v_mov_b32_e32 v239, 0x3f80\n" \
"v_mfma_f32_16x16x32_bf16 v[252:255], a[204:207], a[232:235], v[252:255]\n" \
"v_cvt_pk_bf16_f32 v232, v232, v233\n" \
"v_cvt_pk_bf16_f32 v233, v234, v235\n" \
"s_waitcnt vmcnt(0) lgkmcnt(0)\n" \
"s_barrier\n" \
"v_mov_b32_e32 v45, s62\n" \
"s_mov_b64 exec, 1\n" \
"buffer_store_dword v45, off, s[80:83], s37 offset:32 sc1\n" \
"s_mov_b64 exec, -1\n" \
"v_cmp_ne_u32_e32 vcc, s62, v44\n" \
"s_nop 4\n" \
"s_cbranch_vccnz L_poll161_%=\n" \
"L_pollr161_%=:\n" \
"v_mfma_f32_16x16x32_bf16 v[116:119], a[176:179], a[144:147], v[116:119]\n" \
"s_mov_b32 s73, s50\n" \
"s_sub_u32 m0, s31, 0x10000\n" \
"v_mfma_f32_16x16x32_bf16 v[116:119], a[240:243], a[208:211], v[116:119]\n" \
"buffer_load_dwordx4 v20, s[80:83], s73 offen offset:0 sc1 lds\n" \
"v_mfma_f32_16x16x32_bf16 v[120:123], a[180:183], a[144:147], v[120:123]\n" \
"s_add_u32 s74, s50, 0x4000\n" \
"s_add_u32 s75, s50, 0x8000\n" \
"v_mfma_f32_16x16x32_bf16 v[120:123], a[244:247], a[208:211], v[120:123]\n" \
"s_mov_b32 s70, 0x3e0293ee\n" \
"s_mov_b32 s76, s52\n" \
"v_mfma_f32_16x16x32_bf16 v[124:127], a[184:187], a[144:147], v[124:127]\n" \
"v_mul_f32_e32 v116, s70, v116\n" \
"v_mul_f32_e32 v117, s70, v117\n" \
"v_mfma_f32_16x16x32_bf16 v[124:127], a[248:251], a[208:211], v[124:127]\n" \
"buffer_load_dwordx4 v20, s[80:83], s73 offen offset:1024 sc1 lds\n" \
"v_mfma_f32_16x16x32_bf16 v[128:131], a[188:191], a[144:147], v[128:131]\n" \
"v_mul_f32_e32 v118, s70, v118\n" \
"v_mul_f32_e32 v119, s70, v119\n" \
"v_mfma_f32_16x16x32_bf16 v[128:131], a[252:255], a[208:211], v[128:131]\n" \
"v_cvt_pk_bf16_f32 v116, v116, v117\n" \
"v_mul_f32_e64 v120, s70, v120\n" \
"v_mfma_f32_16x16x32_bf16 v[144:147], a[188:191], a[148:151], v[144:147]\n" \
"v_mul_f32_e32 v124, s70, v124\n" \
"v_mul_f32_e32 v125, s70, v125\n" \
"v_mfma_f32_16x16x32_bf16 v[144:147], a[252:255], a[212:215], v[144:147]\n" \
"buffer_load_dwordx4 v20, s[80:83], s73 offen offset:2048 sc1 lds\n" \
"v_mfma_f32_16x16x32_bf16 v[140:143], a[184:187], a[148:151], v[140:143]\n" \
"v_mul_f32_e32 v126, s70, v126\n" \
"v_mul_f32_e32 v127, s70, v127\n" \
"v_mfma_f32_16x16x32_bf16 v[140:143], a[248:251], a[212:215], v[140:143]\n" \
"v_cvt_pk_bf16_f32 v124, v124, v125\n" \
"v_mul_f32_e64 v121, s70, v121\n" \
"v_mfma_f32_16x16x32_bf16 v[136:139], a[180:183], a[148:151], v[136:139]\n" \
".fill (L_pb + 77536 - .) / 4, 4, 0xBF800000\n" \
"L_pp19:\n" \
"s_cbranch_execz L_pret\n" \
"v_mul_f32_e32 v122, s70, v122\n" \
"v_mul_f32_e64 v123, s70, v123\n" \
"v_mfma_f32_16x16x32_bf16 v[136:139], a[244:247], a[212:215], v[136:139]\n" \
"buffer_load_dwordx4 v20, s[80:83], s73 offen offset:3072 sc1 lds\n" \
"v_mfma_f32_16x16x32_bf16 v[132:135], a[176:179], a[148:151], v[132:135]\n" \
"s_sub_u32 m0, s31, s51\n" \
"v_cvt_pk_bf16_f32 v117, v118, v119\n" \
"v_mfma_f32_16x16x32_bf16 v[132:135], a[240:243], a[212:215], v[132:135]\n" \
"v_mul_f32_e32 v128, s70, v128\n" \
"v_mul_f32_e64 v129, s70, v129\n" \
"v_mfma_f32_16x16x32_bf16 v[148:151], a[176:179], a[152:155], v[148:151]\n" \
"v_mul_f32_e32 v130, s70, v130\n" \
"v_mul_f32_e32 v131, s70, v131\n" \
"v_mfma_f32_16x16x32_bf16 v[148:151], a[240:243], a[216:219], v[148:151]\n" \
"buffer_load_dwordx4 v20, s[80:83], s74 offen offset:0 sc1 lds\n" \
"v_mfma_f32_16x16x32_bf16 v[152:155], a[180:183], a[152:155], v[152:155]\n" \
"v_cvt_pk_bf16_f32 v125, v126, v127\n" \
"v_cvt_pk_bf16_f32 v118, v120, v121\n" \
"v_mfma_f32_16x16x32_bf16 v[152:155], a[244:247], a[216:219], v[152:155]\n" \
"v_cvt_pk_bf16_f32 v119, v122, v123\n" \
"v_cvt_pk_bf16_f32 v126, v128, v129\n" \
"v_mfma_f32_16x16x32_bf16 v[156:159], a[184:187], a[152:155], v[156:159]\n" \
"v_cvt_pk_bf16_f32 v127, v130, v131\n" \
"v_mul_f32_e64 v132, s70, v132\n" \
"v_mfma_f32_16x16x32_bf16 v[156:159], a[248:251], a[216:219], v[156:159]\n" \
"buffer_load_dwordx4 v20, s[80:83], s74 offen offset:1024 sc1 lds\n" \
"v_mfma_f32_16x16x32_bf16 v[160:163], a[188:191], a[152:155], v[160:163]\n" \
"ds_write_b128 v21, v[116:119] offset:16384\n" \
"v_mfma_f32_16x16x32_bf16 v[160:163], a[252:255], a[216:219], v[160:163]\n" \
"v_mul_f32_e32 v133, s70, v133\n" \
"v_mul_f32_e32 v140, s70, v140\n" \
"v_mfma_f32_16x16x32_bf16 v[176:179], a[188:191], a[156:159], v[176:179]\n" \
"v_mul_f32_e32 v141, s70, v141\n" \
"v_mul_f32_e32 v134, s70, v134\n" \
"v_mfma_f32_16x16x32_bf16 v[176:179], a[252:255], a[220:223], v[176:179]\n" \
"buffer_load_dwordx4 v20, s[80:83], s74 offen offset:2048 sc1 lds\n" \
"v_mfma_f32_16x16x32_bf16 v[172:175], a[184:187], a[156:159], v[172:175]\n" \
"ds_write_b128 v21, v[124:127] offset:17408\n" \
"v_mfma_f32_16x16x32_bf16 v[172:175], a[248:251], a[220:223], v[172:175]\n" \
"v_mul_f32_e64 v135, s70, v135\n" \
"v_cvt_pk_bf16_f32 v132, v132, v133\n" \
"v_mfma_f32_16x16x32_bf16 v[168:171], a[180:183], a[156:159], v[168:171]\n" \
"v_mul_f32_e32 v142, s70, v142\n" \
"v_mul_f32_e32 v143, s70, v143\n" \
"v_mfma_f32_16x16x32_bf16 v[168:171], a[244:247], a[220:223], v[168:171]\n" \
"buffer_load_dwordx4 v20, s[80:83], s74 offen offset:3072 sc1 lds\n" \
"v_mfma_f32_16x16x32_bf16 v[164:167], a[176:179], a[156:159], v[164:167]\n" \
"s_sub_u32 m0, s31, 0x4000\n" \
"v_cvt_pk_bf16_f32 v140, v140, v141\n" \
"v_mfma_f32_16x16x32_bf16 v[164:167], a[240:243], a[220:223], v[164:167]\n" \
"v_mul_f32_e32 v136, s70, v136\n" \
"v_mul_f32_e32 v137, s70, v137\n" \
"v_mfma_f32_16x16x32_bf16 v[84:87], a[176:179], a[136:139], v[84:87]\n" \
"v_mul_f32_e32 v138, s70, v138\n" \
"v_mul_f32_e32 v139, s70, v139\n" \
"v_mfma_f32_16x16x32_bf16 v[84:87], a[240:243], a[200:203], v[84:87]\n" \
"buffer_load_dwordx4 v20, s[80:83], s75 offen offset:0 sc1 lds\n" \
"v_mfma_f32_16x16x32_bf16 v[88:91], a[180:183], a[136:139], v[88:91]\n" \
"v_cvt_pk_bf16_f32 v133, v134, v135\n" \
"v_mul_f32_e64 v144, s70, v144\n" \
"v_mfma_f32_16x16x32_bf16 v[88:91], a[244:247], a[200:203], v[88:91]\n" \
"v_mul_f32_e32 v145, s70, v145\n" \
"v_mul_f32_e32 v146, s70, v146\n" \
"v_mfma_f32_16x16x32_bf16 v[92:95], a[184:187], a[136:139], v[92:95]\n" \
"v_mul_f32_e64 v147, s70, v147\n" \
"v_cvt_pk_bf16_f32 v141, v142, v143\n" \
"v_mfma_f32_16x16x32_bf16 v[92:95], a[248:251], a[200:203], v[92:95]\n" \
"buffer_load_dwordx4 v20, s[80:83], s75 offen offset:1024 sc1 lds\n" \
"v_mfma_f32_16x16x32_bf16 v[96:99], a[188:191], a[136:139], v[96:99]\n" \
"v_cvt_pk_bf16_f32 v134, v136, v137\n" \
"v_cvt_pk_bf16_f32 v135, v138, v139\n" \
"v_mfma_f32_16x16x32_bf16 v[96:99], a[252:255], a[200:203], v[96:99]\n" \
"v_cvt_pk_bf16_f32 v142, v144, v145\n" \
"v_cvt_pk_bf16_f32 v143, v146, v147\n" \
"v_mfma_f32_16x16x32_bf16 v[112:115], a[188:191], a[140:143], v[112:115]\n" \
"ds_write_b128 v21, v[132:135] offset:20480\n" \
"v_mfma_f32_16x16x32_bf16 v[112:115], a[252:255], a[204:207], v[112:115]\n" \
"buffer_load_dwordx4 v20, s[80:83], s75 offen offset:2048 sc1 lds\n" \
"v_mfma_f32_16x16x32_bf16 v[108:111], a[184:187], a[140:143], v[108:111]\n" \
"v_cvt_pk_bf16_f32 v180, v180, v181\n" \
"v_cvt_pk_bf16_f32 v181, v182, v183\n" \
"v_mfma_f32_16x16x32_bf16 v[108:111], a[248:251], a[204:207], v[108:111]\n" \
"ds_write_b128 v21, v[140:143] offset:21504\n" \
"v_mfma_f32_16x16x32_bf16 v[104:107], a[180:183], a[140:143], v[104:107]\n" \
"v_cvt_pk_bf16_f32 v234, v244, v245\n" \
"v_mov_b32_e64 v235, 0\n" \
"v_mfma_f32_16x16x32_bf16 v[104:107], a[244:247], a[204:207], v[104:107]\n" \
"buffer_load_dwordx4 v20, s[80:83], s75 offen offset:3072 sc1 lds\n" \
"v_mfma_f32_16x16x32_bf16 v[100:103], a[176:179], a[140:143], v[100:103]\n" \
"s_mov_b32 m0, s53\n" \
"s_nop 0\n" \
"v_cvt_pk_bf16_f32 v182, v50, v51\n" \
"v_mfma_f32_16x16x32_bf16 v[100:103], a[240:243], a[204:207], v[100:103]\n" \
"v_mov_b32_e32 v183, 0x3f80\n" \
"v_cvt_pk_bf16_f32 v118, v192, v193\n" \
"v_mfma_f32_16x16x32_bf16 v[52:55], a[176:179], a[128:131], v[52:55]\n" \
"v_cvt_pk_bf16_f32 v119, v194, v195\n" \
"v_cvt_pk_bf16_f32 v120, v208, v209\n" \
"v_mfma_f32_16x16x32_bf16 v[52:55], a[240:243], a[192:195], v[52:55]\n" \
"buffer_load_dwordx4 v20, s[80:83], s76 offen offset:0 sc1 lds\n" \
"v_mfma_f32_16x16x32_bf16 v[56:59], a[180:183], a[128:131], v[56:59]\n" \
"v_cvt_pk_bf16_f32 v121, v210, v211\n" \
"v_cvt_pk_bf16_f32 v122, v224, v225\n" \
"v_mfma_f32_16x16x32_bf16 v[56:59], a[244:247], a[192:195], v[56:59]\n" \
"v_cvt_pk_bf16_f32 v123, v226, v227\n" \
"v_cvt_pk_bf16_f32 v124, v240, v241\n" \
"v_cvt_pk_bf16_f32 v125, v242, v243\n" \
"v_mfma_f32_16x16x32_bf16 v[60:63], a[184:187], a[128:131], v[60:63]\n" \
"v_mfma_f32_16x16x32_bf16 v[192:195], v[188:191], v[184:187], 0\n" \
"buffer_load_dwordx4 v20, s[80:83], s76 offen offset:1024 sc1 lds\n" \
"v_mfma_f32_16x16x32_bf16 v[32:35], v[180:183], v[184:187], 0\n" \
"v_mfma_f32_16x16x32_bf16 v[208:211], v[204:207], v[200:203], 0\n" \
"v_mfma_f32_16x16x32_bf16 v[36:39], v[188:191], v[200:203], 0\n" \
"v_mfma_f32_16x16x32_bf16 v[224:227], v[220:223], v[216:219], 0\n" \
"v_mfma_f32_16x16x32_bf16 v[248:251], v[204:207], v[216:219], 0\n" \
"v_mfma_f32_16x16x32_bf16 v[240:243], v[236:239], v[232:235], 0\n" \
"v_mfma_f32_16x16x32_bf16 v[128:131], v[220:223], v[232:235], 0\n" \
"s_mov_b32 s64, 0xaaaaaaaa\n" \
"s_mov_b32 s65, 0xaaaaaaaa\n" \
"v_mfma_f32_16x16x32_bf16 v[60:63], a[248:251], a[192:195], v[60:63]\n" \
"s_mov_b32 s66, 0xfff0ffff\n" \
"s_mov_b32 s67, 0xf000ff00\n" \
"v_mfma_f32_16x16x32_bf16 v[64:67], a[188:191], a[128:131], v[64:67]\n" \
"s_mov_b32 s68, 0xffc0fffc\n" \
"s_mov_b32 s69, 0xc000fc00\n" \
"v_cndmask_b32_e64 v192, v192, v193, s[64:65]\n" \
"v_cndmask_b32_e64 v32, v32, v33, s[64:65]\n" \
"v_cndmask_b32_e64 v194, v194, v195, s[64:65]\n" \
"v_cndmask_b32_e64 v34, v34, v35, s[64:65]\n" \
"v_cndmask_b32_e64 v208, v208, v209, s[64:65]\n" \
"v_cndmask_b32_e64 v36, v36, v37, s[64:65]\n" \
"v_cndmask_b32_e64 v210, v210, v211, s[64:65]\n" \
"v_cndmask_b32_e64 v38, v38, v39, s[64:65]\n" \
"v_cndmask_b32_e64 v224, v224, v225, s[64:65]\n" \
"v_cndmask_b32_e64 v248, v248, v249, s[64:65]\n" \
"v_cndmask_b32_e64 v226, v226, v227, s[64:65]\n" \
"v_cndmask_b32_e64 v250, v250, v251, s[64:65]\n" \
"v_cndmask_b32_e64 v240, v240, v241, s[64:65]\n" \
"v_cndmask_b32_e64 v128, v128, v129, s[64:65]\n" \
"v_cndmask_b32_e64 v242, v242, v243, s[64:65]\n" \
"v_cndmask_b32_e64 v130, v130, v131, s[64:65]\n" \
"v_cndmask_b32_e64 v192, v32, v192, s[66:67]\n" \
"v_cndmask_b32_e64 v194, v34, v194, s[68:69]\n" \
"v_cndmask_b32_e64 v208, v36, v208, s[66:67]\n" \
"v_cndmask_b32_e64 v210, v38, v210, s[68:69]\n" \
"v_cndmask_b32_e64 v224, v248, v224, s[66:67]\n" \
"v_cndmask_b32_e64 v226, v250, v226, s[68:69]\n" \
"v_cndmask_b32_e64 v240, v128, v240, s[66:67]\n" \
"v_cndmask_b32_e64 v242, v130, v242, s[68:69]\n" \
"v_mfma_f32_16x16x32_bf16 v[64:67], a[252:255], a[192:195], v[64:67]\n" \
"v_max_f32_e32 v193, v192, v194\n" \
"v_max_f32_e32 v209, v208, v210\n" \
"v_max_f32_e32 v225, v224, v226\n" \
"v_max_f32_e32 v241, v240, v242\n" \
"v_mul_f32_e32 v148, s70, v148\n" \
"v_permlane32_swap_b32_e32 v193, v225\n" \
"v_permlane32_swap_b32_e32 v209, v241\n" \
"v_mul_f32_e32 v149, s70, v149\n" \
"v_mul_f32_e32 v156, s70, v156\n" \
"v_mul_f32_e32 v157, s70, v157\n" \
"v_permlane32_swap_b32_e32 v196, v228\n" \
"v_permlane32_swap_b32_e32 v212, v244\n" \
"v_max_f32_e32 v193, v193, v225\n" \
"v_max_f32_e32 v209, v209, v241\n" \
"v_mul_f32_e32 v150, s70, v150\n" \
"v_mul_f32_e32 v151, s70, v151\n" \
"v_cvt_pk_bf16_f32 v148, v148, v149\n" \
"v_mul_f32_e32 v158, s70, v158\n" \
"v_mul_f32_e32 v159, s70, v159\n" \
"v_cvt_pk_bf16_f32 v156, v156, v157\n" \
"v_permlane16_swap_b32_e32 v228, v244\n" \
"v_permlane16_swap_b32_e32 v193, v209\n" \
"v_mfma_f32_16x16x32_bf16 v[80:83], a[188:191], a[132:135], v[80:83]\n" \
"s_mov_b32 s72, 0x3eae1a93\n" \
"v_mul_f32_e32 v152, s70, v152\n" \
"v_mul_f32_e32 v153, s70, v153\n" \
"v_mul_f32_e32 v154, s70, v154\n" \
"v_mul_f32_e32 v155, s70, v155\n" \
"v_cvt_pk_bf16_f32 v149, v150, v151\n" \
"v_mul_f32_e32 v160, s70, v160\n" \
"v_mul_f32_e32 v161, s70, v161\n" \
"v_mul_f32_e32 v162, s70, v162\n" \
"v_mul_f32_e32 v163, s70, v163\n" \
"v_cvt_pk_bf16_f32 v157, v158, v159\n" \
"v_mul_f32_e32 v45, 0xbfb8aa3b, v228\n" \
"v_mfma_f32_16x16x16_bf16 v[48:51], v[182:183], v[46:47], 0\n" \
"v_mfma_f32_16x16x16_bf16 v[196:199], v[190:191], v[46:47], 0\n" \
"v_mfma_f32_16x16x16_bf16 v[212:215], v[206:207], v[46:47], 0\n" \
"v_max_f32_e64 v193, v193, v209\n" \
"v_cvt_pk_bf16_f32 v150, v152, v153\n" \
"v_cvt_pk_bf16_f32 v151, v154, v155\n" \
"v_cvt_pk_bf16_f32 v158, v160, v161\n" \
"v_cvt_pk_bf16_f32 v159, v162, v163\n" \
"v_mfma_f32_16x16x16_bf16 v[228:231], v[222:223], v[46:47], 0\n" \
"v_mul_f32_e32 v193, s72, v193\n" \
"v_mov_b32_e32 v209, v193\n" \
"ds_write_b128 v21, v[148:151] offset:24576\n" \
"v_mfma_f32_16x16x16_bf16 v[244:247], v[238:239], v[46:47], 0\n" \
"v_permlane16_swap_b32_e32 v193, v209\n" \
"s_mov_b32 s71, 0x3e0293ee\n" \
"v_mfma_f32_16x16x32_bf16 v[80:83], a[252:255], a[196:199], v[80:83]\n" \
"v_mov_b32_e32 v225, v193\n" \
"v_mov_b32_e32 v241, v209\n" \
"v_mul_f32_e32 v164, s70, v164\n" \
"v_mul_f32_e64 v165, s70, v165\n" \
"ds_write_b128 v21, v[156:159] offset:25600\n" \
"v_mul_f32_e32 v172, s70, v172\n" \
"v_mul_f32_e32 v173, s70, v173\n" \
"v_pk_mul_f32 v[84:85], v[84:85], s[70:71]\n" \
"v_pk_mul_f32 v[92:93], v[92:93], s[70:71]\n" \
"v_pk_mul_f32 v[100:101], v[100:101], s[70:71]\n" \
"v_pk_mul_f32 v[108:109], v[108:109], s[70:71]\n" \
"v_pk_mul_f32 v[52:53], v[52:53], s[70:71]\n" \
"v_pk_mul_f32 v[60:61], v[60:61], s[70:71]\n" \
"v_permlane32_swap_b32_e32 v193, v225\n" \
"v_permlane32_swap_b32_e32 v209, v241\n" \
"v_mul_f32_e32 v166, s70, v166\n" \
"v_mul_f32_e32 v167, s70, v167\n" \
"v_cvt_pk_bf16_f32 v164, v164, v165\n" \
"v_mul_f32_e32 v174, s70, v174\n" \
"v_mul_f32_e32 v175, s70, v175\n" \
"v_cvt_pk_bf16_f32 v172, v172, v173\n" \
"v_pk_mul_f32 v[86:87], v[86:87], s[70:71]\n" \
"v_cvt_pk_bf16_f32 v84, v84, v85\n" \
"v_pk_mul_f32 v[94:95], v[94:95], s[70:71]\n" \
"v_cvt_pk_bf16_f32 v92, v92, v93\n" \
"v_pk_mul_f32 v[102:103], v[102:103], s[70:71]\n" \
"v_cvt_pk_bf16_f32 v100, v100, v101\n" \
"v_pk_mul_f32 v[110:111], v[110:111], s[70:71]\n" \
"v_cvt_pk_bf16_f32 v108, v108, v109\n" \
"v_pk_mul_f32 v[54:55], v[54:55], s[70:71]\n" \
"v_cvt_pk_bf16_f32 v52, v52, v53\n" \
"v_pk_mul_f32 v[62:63], v[62:63], s[70:71]\n" \
"v_cvt_pk_bf16_f32 v60, v60, v61\n" \
"v_fma_f32 v192, v192, s72, -v193\n" \
"v_fma_f32 v194, v194, s72, -v193\n" \
"v_fma_f32 v208, v208, s72, -v209\n" \
"v_fma_f32 v210, v210, s72, -v209\n" \
"v_mul_f32_e32 v168, s70, v168\n" \
"v_mul_f32_e32 v169, s70, v169\n" \
"v_mul_f32_e32 v170, s70, v170\n" \
"v_mul_f32_e32 v171, s70, v171\n" \
"v_cvt_pk_bf16_f32 v165, v166, v167\n" \
"v_mul_f32_e32 v176, s70, v176\n" \
"v_mul_f32_e32 v177, s70, v177\n" \
"v_mul_f32_e32 v178, s70, v178\n" \
"v_mul_f32_e32 v179, s70, v179\n" \
"v_cvt_pk_bf16_f32 v173, v174, v175\n" \
"v_pk_mul_f32 v[88:89], v[88:89], s[70:71]\n" \
"v_pk_mul_f32 v[90:91], v[90:91], s[70:71]\n" \
"v_cvt_pk_bf16_f32 v85, v86, v87\n" \
"v_pk_mul_f32 v[96:97], v[96:97], s[70:71]\n" \
"v_pk_mul_f32 v[98:99], v[98:99], s[70:71]\n" \
"v_cvt_pk_bf16_f32 v93, v94, v95\n" \
"v_pk_mul_f32 v[104:105], v[104:105], s[70:71]\n" \
"v_pk_mul_f32 v[106:107], v[106:107], s[70:71]\n" \
"v_cvt_pk_bf16_f32 v101, v102, v103\n" \
"v_pk_mul_f32 v[112:113], v[112:113], s[70:71]\n" \
"v_pk_mul_f32 v[114:115], v[114:115], s[70:71]\n" \
"v_cvt_pk_bf16_f32 v109, v110, v111\n" \
"v_pk_mul_f32 v[56:57], v[56:57], s[70:71]\n" \
"v_pk_mul_f32 v[58:59], v[58:59], s[70:71]\n" \
"v_cvt_pk_bf16_f32 v53, v54, v55\n" \
"v_pk_mul_f32 v[64:65], v[64:65], s[70:71]\n" \
"v_pk_mul_f32 v[66:67], v[66:67], s[70:71]\n" \
"v_cvt_pk_bf16_f32 v61, v62, v63\n" \
"v_pk_mul_f32 v[80:81], v[80:81], s[70:71]\n" \
"v_pk_mul_f32 v[82:83], v[82:83], s[70:71]\n" \
"v_exp_f32_e32 v192, v192\n" \
"v_exp_f32_e32 v194, v194\n" \
"v_exp_f32_e32 v208, v208\n" \
"v_exp_f32_e32 v210, v210\n" \
"v_fma_f32 v224, v224, s72, -v225\n" \
"v_fma_f32 v226, v226, s72, -v225\n" \
"v_mfma_f32_16x16x32_bf16 v[76:79], a[184:187], a[132:135], v[76:79]\n" \
"v_cvt_pk_bf16_f32 v166, v168, v169\n" \
"v_cvt_pk_bf16_f32 v167, v170, v171\n" \
"v_cvt_pk_bf16_f32 v174, v176, v177\n" \
"v_cvt_pk_bf16_f32 v175, v178, v179\n" \
"v_cvt_pk_bf16_f32 v86, v88, v89\n" \
"v_cvt_pk_bf16_f32 v87, v90, v91\n" \
"v_cvt_pk_bf16_f32 v94, v96, v97\n" \
"v_cvt_pk_bf16_f32 v95, v98, v99\n" \
"v_cvt_pk_bf16_f32 v102, v104, v105\n" \
"v_cvt_pk_bf16_f32 v103, v106, v107\n" \
"v_cvt_pk_bf16_f32 v110, v112, v113\n" \
"v_cvt_pk_bf16_f32 v111, v114, v115\n" \
"v_cvt_pk_bf16_f32 v54, v56, v57\n" \
"v_cvt_pk_bf16_f32 v55, v58, v59\n" \
"v_cvt_pk_bf16_f32 v62, v64, v65\n" \
"v_cvt_pk_bf16_f32 v63, v66, v67\n" \
"v_exp_f32_e32 v224, v224\n" \
"v_exp_f32_e32 v226, v226\n" \
"v_fma_f32 v240, v240, s72, -v241\n" \
"v_fma_f32 v242, v242, s72, -v241\n" \
"ds_write_b128 v21, v[164:167] offset:28672\n" \
"v_cvt_pk_bf16_f32 v192, v192, v194\n" \
"v_cvt_pk_bf16_f32 v208, v208, v210\n" \
"v_exp_f32_e32 v240, v240\n" \
"v_exp_f32_e32 v242, v242\n" \
"v_cvt_pk_bf16_f32 v132, v48, v49\n" \
"v_cvt_pk_bf16_f32 v133, v50, v51\n" \
"v_cvt_pk_bf16_f32 v134, v196, v197\n" \
"v_cvt_pk_bf16_f32 v135, v198, v199\n" \
"ds_write_b128 v21, v[172:175] offset:29696\n" \
"v_cvt_pk_bf16_f32 v136, v212, v213\n" \
"v_cvt_pk_bf16_f32 v137, v214, v215\n" \
"v_perm_b32 v184, v192, v192, v40\n" \
"v_perm_b32 v185, v192, v192, v41\n" \
"v_perm_b32 v186, v192, v192, v42\n" \
"v_perm_b32 v187, v192, v192, v43\n" \
"v_perm_b32 v200, v208, v208, v40\n" \
"v_perm_b32 v201, v208, v208, v41\n" \
"v_perm_b32 v202, v208, v208, v42\n" \
"v_perm_b32 v203, v208, v208, v43\n" \
"ds_write_b128 v27, v[84:87] offset:8192\n" \
"v_cvt_pk_bf16_f32 v224, v224, v226\n" \
"v_cvt_pk_bf16_f32 v138, v228, v229\n" \
"v_cvt_pk_bf16_f32 v139, v230, v231\n" \
"v_mfma_f32_16x16x32_bf16 v[32:35], v[132:135], v[184:187], 0\n" \
"v_mfma_f32_16x16x32_bf16 v[36:39], v[134:137], v[200:203], 0\n" \
"v_perm_b32 v216, v224, v224, v40\n" \
"v_perm_b32 v217, v224, v224, v41\n" \
"ds_write_b128 v27, v[92:95] offset:9216\n" \
"v_perm_b32 v218, v224, v224, v42\n" \
"v_perm_b32 v219, v224, v224, v43\n" \
"v_cvt_pk_bf16_f32 v240, v240, v242\n" \
"v_mfma_f32_16x16x32_bf16 v[76:79], a[248:251], a[196:199], v[76:79]\n" \
"v_cvt_pk_bf16_f32 v140, v244, v245\n" \
"v_cvt_pk_bf16_f32 v141, v246, v247\n" \
"v_mfma_f32_16x16x32_bf16 v[248:251], v[136:139], v[216:219], 0\n" \
"v_perm_b32 v232, v240, v240, v40\n" \
"v_perm_b32 v233, v240, v240, v41\n" \
"ds_write_b128 v27, v[100:103] offset:12288\n" \
"v_perm_b32 v234, v240, v240, v42\n" \
"v_perm_b32 v235, v240, v240, v43\n" \
"v_pk_mul_f32 v[76:77], v[76:77], s[70:71]\n" \
"v_pk_mul_f32 v[78:79], v[78:79], s[70:71]\n" \
"v_cvt_pk_bf16_f32 v76, v76, v77\n" \
"v_cvt_pk_bf16_f32 v77, v78, v79\n" \
"v_cvt_pk_bf16_f32 v78, v80, v81\n" \
"v_cvt_pk_bf16_f32 v79, v82, v83\n" \
"v_mfma_f32_16x16x32_bf16 v[128:131], v[138:141], v[232:235], 0\n" \
"ds_write_b128 v27, v[108:111] offset:13312\n" \
"v_mfma_f32_16x16x32_bf16 v[72:75], a[180:183], a[132:135], v[72:75]\n" \
"v_cvt_pk_bf16_f32 v116, v252, v253\n" \
"v_cvt_pk_bf16_f32 v117, v254, v255\n" \
"v_mfma_f32_16x16x32_bf16 v[72:75], a[244:247], a[196:199], v[72:75]\n" \
"v_exp_f32_e64 v45, v45\n" \
"v_mfma_f32_16x16x32_bf16 v[192:195], v[116:119], v[184:187], 0\n" \
"ds_write_b128 v27, v[52:55] offset:0\n" \
"v_mfma_f32_16x16x32_bf16 v[208:211], v[118:121], v[200:203], 0\n" \
"v_mfma_f32_16x16x32_bf16 v[224:227], v[120:123], v[216:219], 0\n" \
"v_mfma_f32_16x16x32_bf16 v[240:243], v[122:125], v[232:235], 0\n" \
"ds_write_b128 v27, v[60:63] offset:1024\n" \
"v_mov_b32_dpp v34, v38 quad_perm:[0,1,2,3] row_mask:0x2 bank_mask:0xf\n" \
"v_pk_mul_f32 v[72:73], v[72:73], s[70:71]\n" \
"v_pk_mul_f32 v[74:75], v[74:75], s[70:71]\n" \
"v_mov_b32_dpp v34, v250 quad_perm:[0,1,2,3] row_mask:0x4 bank_mask:0xf\n" \
"v_add_f32_e64 v45, 1.0, v45\n" \
"v_mfma_f32_16x16x32_bf16 v[68:71], a[176:179], a[132:135], v[68:71]\n" \
"v_mov_b32_dpp v34, v130 quad_perm:[0,1,2,3] row_mask:0x8 bank_mask:0xf\n" \
"v_mov_b32_dpp v32, v36 quad_perm:[0,1,2,3] row_mask:0x2 bank_mask:0xf\n" \
"v_rcp_f32_e64 v45, v45\n" \
"ds_write_b128 v27, v[76:79] offset:5120\n" \
"v_mov_b32_dpp v33, v37 quad_perm:[0,1,2,3] row_mask:0x2 bank_mask:0xf\n" \
"v_rcp_f32_e64 v34, v34\n" \
"v_mov_b32_dpp v32, v248 quad_perm:[0,1,2,3] row_mask:0x4 bank_mask:0xf\n" \
"v_mov_b32_dpp v33, v249 quad_perm:[0,1,2,3] row_mask:0x4 bank_mask:0xf\n" \
"v_mul_f32_e64 v34, v45, v34\n" \
"v_mov_b32_dpp v32, v128 quad_perm:[0,1,2,3] row_mask:0x8 bank_mask:0xf\n" \
"v_mov_b32_dpp v33, v129 quad_perm:[0,1,2,3] row_mask:0x8 bank_mask:0xf\n" \
"v_mul_f32_e32 v32, v34, v32\n" \
"v_mul_f32_e32 v33, v34, v33\n" \
"v_mov_b32_e64 v38, v34\n" \
"v_mfma_f32_16x16x32_bf16 v[68:71], a[240:243], a[196:199], v[68:71]\n" \
"v_sub_f32_e32 v44, 1.0, v45\n" \
"v_permlane16_swap_b32_e32 v34, v38\n" \
"v_mov_b32_e32 v250, v34\n" \
"v_mov_b32_e32 v130, v38\n" \
"v_cvt_pk_bf16_f32 v32, v32, v33\n" \
"v_permlane32_swap_b32_e32 v34, v250\n" \
"v_permlane32_swap_b32_e32 v38, v130\n" \
"v_mul_f32_e64 v192, v34, v192\n" \
"v_pk_mul_f32 v[68:69], v[68:69], s[70:71]\n" \
"v_pk_mul_f32 v[70:71], v[70:71], s[70:71]\n" \
"v_cvt_pk_bf16_f32 v68, v68, v69\n" \
"v_cvt_pk_bf16_f32 v69, v70, v71\n" \
"v_cvt_pk_bf16_f32 v70, v72, v73\n" \
"v_cvt_pk_bf16_f32 v71, v74, v75\n" \
"ds_write_b128 v27, v[68:71] offset:4096\n" \
"v_mul_f32_e32 v193, v34, v193\n" \
"v_mul_f32_e32 v208, v38, v208\n" \
"v_mul_f32_e32 v209, v38, v209\n" \
"v_mul_f32_e32 v224, v250, v224\n" \
"v_mul_f32_e32 v225, v250, v225\n" \
"v_mul_f32_e32 v240, v130, v240\n" \
"v_mul_f32_e32 v241, v130, v241\n" \
"v_mul_f32_e32 v194, v34, v194\n" \
"v_mul_f32_e64 v195, v34, v195\n" \
"v_cvt_pk_bf16_f32 v192, v192, v193\n" \
"v_mul_f32_e32 v210, v38, v210\n" \
"v_mul_f32_e32 v211, v38, v211\n" \
"v_cvt_pk_bf16_f32 v208, v208, v209\n" \
"v_mul_f32_e32 v226, v250, v226\n" \
"v_mul_f32_e32 v227, v250, v227\n" \
"v_cvt_pk_bf16_f32 v224, v224, v225\n" \
"v_mul_f32_e32 v242, v130, v242\n" \
"v_mul_f32_e32 v243, v130, v243\n" \
"v_cvt_pk_bf16_f32 v240, v240, v241\n" \
"v_cvt_pk_bf16_f32 v193, v194, v195\n" \
"s_add_u32 s84, s48, 0x0\n" \
"s_nop 0\n" \
"v_cvt_pk_bf16_f32 v209, v210, v211\n" \
"s_add_u32 s85, s48, 0x24800\n" \
"v_cvt_pk_bf16_f32 v225, v226, v227\n" \
"s_add_u32 s86, s48, 0x49000\n" \
"v_cvt_pk_bf16_f32 v241, v242, v243\n" \
"s_add_u32 s87, s48, 0x6d800\n" \
"ds_write_b32 v26, v44\n" \
"buffer_store_dword v32, v25, s[80:83], s49 offen sc1\n" \
"buffer_store_dwordx2 v[192:193], v24, s[80:83], s84 offen sc1\n" \
"buffer_store_dwordx2 v[208:209], v24, s[80:83], s85 offen sc1\n" \
"buffer_store_dwordx2 v[224:225], v24, s[80:83], s86 offen sc1\n" \
"buffer_store_dwordx2 v[240:241], v24, s[80:83], s87 offen sc1\n" \
"s_branch L_end_%=\n" \
"L_poll61_%=:\n" \
"s_mov_b32 s84, 0\n" \
"L_pl61_%=:\n" \
"s_sleep 1\n" \
"v_bfe_u32 v44, v20, 2, 5\n" \
"buffer_load_dword v44, v44, s[80:83], s38 offen offset:128 sc1\n" \
"s_waitcnt vmcnt(0)\n" \
"v_cmp_ne_u32_e32 vcc, s62, v44\n" \
"s_add_u32 s84, s84, 1\n" \
"s_nop 3\n" \
"s_cbranch_vccz L_pollr61_%=\n" \
"s_cmp_gt_u32 s84, 0x400000\n" \
"s_cbranch_scc0 L_pl61_%=\n" \
"s_branch L_pollr61_%=\n" \
"L_poll161_%=:\n" \
"s_mov_b32 s84, 0\n" \
"L_pl161_%=:\n" \
"s_sleep 1\n" \
"v_bfe_u32 v44, v20, 2, 5\n" \
"buffer_load_dword v44, v44, s[80:83], s38 offen offset:0 sc1\n" \
"s_waitcnt vmcnt(0)\n" \
"v_cmp_ne_u32_e32 vcc, s62, v44\n" \
"s_add_u32 s84, s84, 1\n" \
"s_nop 3\n" \
"s_cbranch_vccz L_pollr161_%=\n" \
"s_cmp_gt_u32 s84, 0x400000\n" \
"s_cbranch_scc0 L_pl161_%=\n" \
"s_branch L_pollr161_%=\n" \
"L_end_%=:\n"
#define PROJ_VCLOB "v21","v24","v25","v28","v29","v32","v33","v34","v35","v36","v37","v38","v39","v40","v41","v42","v43","v44","v45","v46","v47","v48","v49","v50","v51","v52","v53","v54","v55","v56","v57","v58","v59","v60","v61","v62","v63","v64","v65","v66","v67","v68","v69","v70","v71","v72","v73","v74","v75","v76","v77","v78","v79","v80","v81","v82","v83","v84","v85","v86","v87","v88","v89","v90","v91","v92","v93","v94","v95","v96","v97","v98","v99","v100","v101","v102","v103","v104","v105","v106","v107","v108","v109","v110","v111","v112","v113","v114","v115","v116","v117","v118","v119","v120","v121","v122","v123","v124","v125","v126","v127","v128","v129","v130","v131","v132","v133","v134","v135","v136","v137","v138","v139","v140","v141","v142","v143","v144","v145","v146","v147","v148","v149","v150","v151","v152","v153","v154","v155","v156","v157","v158","v159","v160","v161","v162","v163","v164","v165","v166","v167","v168","v169","v170","v171","v172","v173","v174","v175","v176","v177","v178","v179","v180","v181","v182","v183","v184","v185","v186","v187","v188","v189","v190","v192","v193","v194","v195","v196","v197","v198","v199","v200","v201","v202","v203","v204","v205","v206","v207","v208","v209","v210","v211","v212","v213","v214","v215","v216","v217","v218","v219","v220","v221","v222","v223","v224","v225","v226","v227","v228","v229","v230","v231","v232","v233","v234","v235","v236","v237","v238","v239","v240","v241","v242","v243","v244","v245","v246","v247","v248","v249","v250","v251","v252","v253","v254","v255"
#define PROJ_SCLOB "s4","s5","s6","s7","s32","s33","s34","s35","s36","s37","s38","s39","s40","s41","s42","s43","s44","s45","s46","s47","s48","s49","s50","s51","s52","s53","s54","s55","s63","s64","s65","s66","s67","s68","s69","s70","s71","s72","s73","s74","s75","s76","s77","s78","s79","s80","s81","s82","s83","s84","s85","s86","s87","s88","s89","s90","s91","s92","s93","s94","s95","s96","s97","s98","s99","s100","s101"

typedef const u16* const __attribute__((address_space(4)))* kaptr;
__device__ __forceinline__ void proj_phase(kaptr& ka, char*& ws, const unsigned wid, u32x4 (&vp)[5], unsigned& vl, u32x4 (&sa)[5], u32x2 s24, u32x2& s28, unsigned tdum, u32x4& srdA, u32x2& sd, unsigned& tag
    ) {
    const unsigned b = blockIdx.x, h = (b & 7) * 4 + ((b >> 3) & 3), mt = b >> 5;
    const unsigned rb = (wid < 2u ? 15u - mt : mt) * 128u;
    const unsigned lane = threadIdx.x & 63u, wc = wid & 1u;
    u32x2 vAc, vB;
#pragma unroll
    for (int par = 0; par < 2; ++par) {
        const unsigned lr = lane >> 3, cc = (lane & 7u) ^ (4u * par + (lr >> 1)), r0 = rb + wc * 64u + lr;
        vAc[par] = r0 * (DC * 2) + cc * 16u;
        const unsigned br = lane >> 4, p16 = lane & 15u, sc = (((p16 >> 1) ^ (br | (par << 2))) << 1) | (p16 & 1u);
        vB[par] = (16u * wid + br) * (NBIG * 2) + h * (HD * 2) + sc * 16u;
    }
    vl = lane * 16u;
    u32x2 s30 = {wid * 8192u, 65536u + wid * 4096u};
    asm volatile(PROJ_ASM
                 : "={s62}"(tag), "={s[56:59]}"(srdA), "={s[60:61]}"(sd), "+{s[8:11]}"(sa[0]), "+{s[12:15]}"(sa[1]), "+{s[16:19]}"(sa[2]), "+{s[20:23]}"(sa[3]), "+{s[24:25]}"(s24), "+{s[28:29]}"(s28), "+{s[30:31]}"(s30),
                   "={v[0:3]}"(vp[0]), "={v[4:7]}"(vp[1]), "={v[8:11]}"(vp[2]), "={v[12:15]}"(vp[3]), "={v[16:19]}"(vp[4]), "+{v[22:23]}"(vAc), "+{v[26:27]}"(vB), "+{v191}"(tdum), "+{s[26:27]}"(ws), "+{s[0:1]}"(ka)
                 : "{s2}"(b), "{s3}"(wid), "{v20}"(vl)
                 : "memory", "m0", "vcc", "scc", PROJ_VCLOB, PROJ_SCLOB, ACLOB_LO, ACLOB_HI);
    sa[4] = (u32x4){s24[0], 0u, 0u, 0u};
}

namespace att {
#define DI __device__ __forceinline__
typedef __bf16 bfx8 __attribute__((ext_vector_type(8)));
DI unsigned rfl(unsigned x) { return __builtin_amdgcn_readfirstlane(x); }
DI f32x4 mfma(u32x4 a, u32x4 b, f32x4 c) { return __builtin_amdgcn_mfma_f32_16x16x32_bf16(__builtin_bit_cast(bfx8, a), __builtin_bit_cast(bfx8, b), c, 0, 0, 0); }
typedef __bf16 bfx2 __attribute__((ext_vector_type(2)));
typedef float f32x2 __attribute__((ext_vector_type(2)));
DI unsigned cvtpk2(float lo, float hi) { f32x2 v = {lo, hi}; bfx2 b = __builtin_convertvector(v, bfx2); return __builtin_bit_cast(unsigned, b); }
DI void att_exact(const u16* __restrict__ x, const u16* __restrict__ wq, char* __restrict__ ws, unsigned h, unsigned r0, float g1) {
    const unsigned lane = lane_id(), g = lane >> 4, c = lane & 15u;
    f32x4 o[8];
#pragma unroll
    for (int n = 0; n < 8; ++n) o[n] = (f32x4){0.f, 0.f, 0.f, 0.f};
#pragma unroll 1
    for (unsigned kk = 0; kk < (unsigned)DM; kk += 32u) {
        const unsigned k = kk + 8u * g;
        const u32x4 xf = *reinterpret_cast<const u32x4*>(x + (r0 + c) * DM + k);
#pragma unroll
        for (int n = 0; n < 8; ++n) {
            const u16* w = wq + k * NBIG + h * HD + n * 16 + c; u32x4 wf;
#pragma unroll
            for (int e = 0; e < 4; ++e) wf[e] = (unsigned)w[2 * e * NBIG] | ((unsigned)w[(2 * e + 1) * NBIG] << 16);
            o[n] = mfma(wf, xf, o[n]);
        }
    }
    u32x4 q[4];
#pragma unroll
    for (int p = 0; p < 4; ++p) {
        const f32x4 a = o[2 * p] * 0.12751743082f, b = o[2 * p + 1] * 0.12751743082f;
        q[p] = (u32x4){cvtpk2(a[0], a[1]), cvtpk2(a[2], a[3]), cvtpk2(b[0], b[1]), cvtpk2(b[2], b[3])};
    }
#pragma unroll
    for (int n = 0; n < 8; ++n) o[n] = (f32x4){0.f, 0.f, 0.f, 0.f};
    const __amdgpu_buffer_rsrc_t srd = __builtin_amdgcn_make_buffer_rsrc((void*)(ws + WS_K + (size_t)h * 1048576), (short)0, 1048576, 0x00020000);
    float m = -3.0e38f, l = 0.f;
#pragma unroll 1
    for (unsigned k0 = 0; k0 <= r0 + 15u; k0 += 32u) {
        const unsigned tb = (k0 >> 6) * 32768u + (k0 & 32u) * 256u;
        f32x4 s[2]; float mx = m;
#pragma unroll
        for (int kt = 0; kt < 2; ++kt) {
            s[kt] = (f32x4){0.f, 0.f, 0.f, 0.f};
#pragma unroll
            for (int p = 0; p < 4; ++p) s[kt] = mfma(__builtin_amdgcn_raw_buffer_load_b128(srd, lane * 16u + (kt * 4 + p) * 1024u, tb, 16), q[p], s[kt]);
#pragma unroll
            for (int e = 0; e < 4; ++e) { if (k0 + 16u * kt + 4u * g + e > r0 + c) s[kt][e] = -3.0e38f; mx = fmaxf(mx, s[kt][e]); }
        }
        mx = fmaxf(mx, __shfl_xor(mx, 16)); mx = fmaxf(mx, __shfl_xor(mx, 32));
        const float al = __builtin_amdgcn_exp2f(m - mx); m = mx;
        u32x4 pf;
#pragma unroll
        for (int e = 0; e < 4; ++e) pf[e] = cvtpk2(__builtin_amdgcn_exp2f(s[e >> 1][2 * (e & 1)] - m), __builtin_amdgcn_exp2f(s[e >> 1][2 * (e & 1) + 1] - m));
        l *= al;
#pragma unroll
        for (int e = 0; e < 4; ++e) l += __uint_as_float(pf[e] << 16) + __uint_as_float(pf[e] & 0xffff0000u);
#pragma unroll
        for (int n = 0; n < 8; ++n) o[n] = mfma(__builtin_amdgcn_raw_buffer_load_b128(srd, lane * 16u + 16384u + n * 1024u, tb, 16), pf, o[n] * al);
    }
    l += __shfl_xor(l, 16); l += __shfl_xor(l, 32);
    const float f = g1 / l;
    const __amdgpu_buffer_rsrc_t srdA = __builtin_amdgcn_make_buffer_rsrc((void*)(ws + WS_AF), (short)0, 128 * KS_TOT * 1024, 0x00020000);
#pragma unroll
    for (int p = 0; p < 4; ++p) {
        const f32x4 a = o[2 * p] * f, b = o[2 * p + 1] * f;
        __builtin_amdgcn_raw_buffer_store_b128((u32x4){cvtpk2(a[0], a[1]), cvtpk2(a[2], a[3]), cvtpk2(b[0], b[1]), cvtpk2(b[2], b[3])}, srdA, lane * 16u + p * 1024u, ((r0 >> 4) * KS_TOT + 18u + 4u * h) * 1024u, 16);
    }
}
}

#define ATT_ASM \
"s_branch L_ago_%=\n" \
".p2align 12\n" \
"L_ab:\n" \
".fill (L_ab + 0 - .) / 4, 4, 0xBF800000\n" \
"L_ap0:\n" \
"s_setpc_b64 s[88:89]\n" \
"L_ago_%=:\n" \
"s_mov_b32 s101, 0\n" \
"s_waitcnt vmcnt(15) lgkmcnt(0)\n" \
"s_barrier\n" \
"v_accvgpr_write_b32 a216, 0\n" \
"v_accvgpr_write_b32 a217, 0\n" \
"v_accvgpr_write_b32 a218, 0\n" \
"v_accvgpr_write_b32 a219, 0\n" \
"v_bfe_u32 v61, v20, 2, 5\n" \
"s_mov_b32 s96, s24\n" \
"v_mfma_f32_32x32x16_bf16 v[128:143], a[216:219], a[216:219], 0\n" \
"buffer_load_dword v57, v61, s[56:59], s96 offen sc1\n" \
"v_and_b32_e32 v63, 0x300, v20\n" \
"ds_read_b128 v[96:99], v63 offset:0\n" \
"v_add_u32_e32 v58, s22, v20\n" \
"ds_read_b128 a[0:3], v58 offset:0\n" \
"v_mfma_f32_32x32x16_bf16 v[144:159], a[216:219], a[216:219], 0\n" \
"ds_read_b128 a[16:19], v58 offset:8192\n" \
"v_add_u32_e32 v59, s23, v20\n" \
"ds_read_b128 a[32:35], v59 offset:0\n" \
"ds_read_b128 a[48:51], v59 offset:8192\n" \
"ds_read_b128 v[100:103], v63 offset:1024\n" \
"ds_read_b128 a[4:7], v58 offset:1024\n" \
"v_mfma_f32_32x32x16_bf16 v[160:175], a[216:219], a[216:219], 0\n" \
"ds_read_b128 a[20:23], v58 offset:9216\n" \
"ds_read_b128 a[36:39], v59 offset:1024\n" \
"ds_read_b128 a[52:55], v59 offset:9216\n" \
"s_sub_u32 s67, s12, 0xc000\n" \
"ds_read_b128 v[104:107], v63 offset:2048\n" \
"s_mov_b32 s65, s12\n" \
"v_mfma_f32_32x32x16_bf16 v[176:191], a[216:219], a[216:219], 0\n" \
"v_add_u32_e32 v55, s67, v20\n" \
"v_add_u32_e32 v52, 0xc000, v55\n" \
"s_xor_b32 m0, s65, 0x10000\n" \
"ds_read_b128 a[8:11], v58 offset:2048\n" \
"buffer_load_dwordx4 v52, s[8:11], 0 offen offset:0 sc1 lds\n" \
"v_mfma_f32_32x32x16_bf16 v[192:207], a[216:219], a[216:219], 0\n" \
"ds_read_b128 a[24:27], v58 offset:10240\n" \
"ds_read_b128 a[40:43], v59 offset:2048\n" \
"ds_read_b128 a[56:59], v59 offset:10240\n" \
"ds_read_b128 v[108:111], v63 offset:3072\n" \
"buffer_load_dwordx4 v52, s[8:11], 0 offen offset:1024 sc1 lds\n" \
"v_mfma_f32_32x32x16_bf16 v[208:223], a[216:219], a[216:219], 0\n" \
"ds_read_b128 a[12:15], v58 offset:3072\n" \
"ds_read_b128 a[28:31], v58 offset:11264\n" \
"ds_read_b128 a[44:47], v59 offset:3072\n" \
"ds_read_b128 a[60:63], v59 offset:11264\n" \
"s_waitcnt lgkmcnt(15)\n" \
"v_xor_b32_e32 v96, 0x80008000, v96\n" \
"v_mfma_f32_32x32x16_bf16 v[224:239], a[216:219], a[216:219], 0\n" \
"v_xor_b32_e32 v97, 0x80008000, v97\n" \
"v_xor_b32_e32 v98, 0x80008000, v98\n" \
"v_xor_b32_e32 v99, 0x80008000, v99\n" \
"ds_read_b128 a[160:163], v20 offset:0\n" \
"ds_read_b128 a[164:167], v20 offset:1024\n" \
"s_waitcnt lgkmcnt(15)\n" \
"v_xor_b32_e32 v100, 0x80008000, v100\n" \
"v_mfma_f32_32x32x16_bf16 v[240:255], a[216:219], a[216:219], 0\n" \
"v_xor_b32_e32 v101, 0x80008000, v101\n" \
"v_xor_b32_e32 v102, 0x80008000, v102\n" \
"v_xor_b32_e32 v103, 0x80008000, v103\n" \
"ds_read_b128 a[168:171], v20 offset:2048\n" \
"ds_read_b128 a[172:175], v20 offset:3072\n" \
"ds_read_b128 a[188:191], v20 offset:7168\n" \
"v_mfma_f32_16x16x32_bf16 a[192:195], a[216:219], a[216:219], 0\n" \
"ds_read_b128 a[184:187], v20 offset:6144\n" \
"s_waitcnt lgkmcnt(15)\n" \
"v_xor_b32_e32 v104, 0x80008000, v104\n" \
"v_mfma_f32_16x16x32_bf16 a[196:199], a[216:219], a[216:219], 0\n" \
"v_xor_b32_e32 v105, 0x80008000, v105\n" \
"v_xor_b32_e32 v106, 0x80008000, v106\n" \
"v_mfma_f32_16x16x32_bf16 v[32:35], v[96:99], a[0:3], 0\n" \
"v_xor_b32_e32 v107, 0x80008000, v107\n" \
"ds_read_b128 a[180:183], v20 offset:5120\n" \
"v_mfma_f32_16x16x32_bf16 v[36:39], v[96:99], a[16:19], 0\n" \
"ds_read_b128 a[176:179], v20 offset:4096\n" \
"s_waitcnt lgkmcnt(12)\n" \
"v_xor_b32_e32 v108, 0x80008000, v108\n" \
"v_mfma_f32_16x16x32_bf16 v[40:43], v[96:99], a[32:35], 0\n" \
"v_xor_b32_e32 v109, 0x80008000, v109\n" \
"v_xor_b32_e32 v110, 0x80008000, v110\n" \
"v_mfma_f32_16x16x32_bf16 v[44:47], v[96:99], a[48:51], 0\n" \
"v_xor_b32_e32 v111, 0x80008000, v111\n" \
"v_add_u32_e32 v60, s15, v20\n" \
"v_mfma_f32_16x16x32_bf16 v[32:35], v[100:103], a[4:7], v[32:35]\n" \
"v_add_u32_e32 v60, 0x10000, v60\n" \
"ds_read_b128 a[128:131], v20 offset:8192\n" \
"v_mfma_f32_16x16x32_bf16 v[36:39], v[100:103], a[20:23], v[36:39]\n" \
"ds_read_b128 a[144:147], v20 offset:12288\n" \
"ds_read_b128 a[132:135], v20 offset:9216\n" \
"v_mfma_f32_16x16x32_bf16 v[40:43], v[100:103], a[36:39], v[40:43]\n" \
"ds_read_b128 a[148:151], v20 offset:13312\n" \
"ds_read_b128 a[136:139], v20 offset:10240\n" \
"v_mfma_f32_16x16x32_bf16 v[44:47], v[100:103], a[52:55], v[44:47]\n" \
"ds_read_b128 a[152:155], v20 offset:14336\n" \
"ds_read_b128 a[140:143], v20 offset:11264\n" \
"v_mfma_f32_16x16x32_bf16 v[32:35], v[104:107], a[8:11], v[32:35]\n" \
"ds_read_b128 a[156:159], v20 offset:15360\n" \
"ds_read_b128 a[64:67], v60 offset:16384\n" \
"v_mfma_f32_16x16x32_bf16 v[36:39], v[104:107], a[24:27], v[36:39]\n" \
"ds_read_b128 a[68:71], v60 offset:17408\n" \
"ds_read_b128 a[72:75], v60 offset:18432\n" \
"v_mfma_f32_16x16x32_bf16 v[40:43], v[104:107], a[40:43], v[40:43]\n" \
"ds_read_b128 a[76:79], v60 offset:19456\n" \
"ds_read_b128 a[80:83], v60 offset:24576\n" \
"v_mfma_f32_16x16x32_bf16 v[44:47], v[104:107], a[56:59], v[44:47]\n" \
"ds_read_b128 a[84:87], v60 offset:25600\n" \
"s_waitcnt lgkmcnt(15)\n" \
"v_mfma_f32_16x16x32_bf16 v[32:35], v[108:111], a[12:15], v[32:35]\n" \
"ds_read_b128 a[88:91], v60 offset:26624\n" \
"ds_read_b128 a[92:95], v60 offset:27648\n" \
"v_mfma_f32_16x16x32_bf16 v[36:39], v[108:111], a[28:31], v[36:39]\n" \
"ds_read_b128 a[96:99], v60 offset:0\n" \
"ds_read_b128 a[100:103], v60 offset:1024\n" \
"v_mfma_f32_16x16x32_bf16 v[40:43], v[108:111], a[44:47], v[40:43]\n" \
"ds_read_b128 a[104:107], v60 offset:2048\n" \
"ds_read_b128 a[108:111], v60 offset:3072\n" \
"v_mfma_f32_16x16x32_bf16 v[44:47], v[108:111], a[60:63], v[44:47]\n" \
"ds_read_b128 a[112:115], v60 offset:8192\n" \
"ds_read_b128 a[116:119], v60 offset:9216\n" \
"v_mfma_f32_16x16x32_bf16 v[64:67], a[160:163], a[0:3], v[32:35]\n" \
"ds_read_b128 a[120:123], v60 offset:10240\n" \
"ds_read_b128 a[124:127], v60 offset:11264\n" \
"v_mfma_f32_16x16x32_bf16 v[64:67], a[164:167], a[4:7], v[64:67]\n" \
"v_bfe_u32 v26, v20, 8, 2\n" \
"s_mov_b32 m0, s20\n" \
"v_mfma_f32_16x16x32_bf16 v[64:67], a[168:171], a[8:11], v[64:67]\n" \
"v_bfe_u32 v25, v20, 5, 3\n" \
"v_lshlrev_b32_e32 v26, 1, v26\n" \
"v_mfma_f32_16x16x32_bf16 v[64:67], a[172:175], a[12:15], v[64:67]\n" \
"global_load_lds_dword v[16:17], off\n" \
"v_mfma_f32_16x16x32_bf16 v[68:71], a[188:191], a[12:15], v[32:35]\n" \
"v_sub_u32_e32 v25, v25, v26\n" \
"v_and_b32_e32 v26, 16, v20\n" \
"v_mfma_f32_16x16x32_bf16 v[68:71], a[184:187], a[8:11], v[68:71]\n" \
"v_mov_b32_e32 v27, 0x3f80\n" \
"v_lshlrev_b32_e32 v26, v26, v27\n" \
"v_mfma_f32_16x16x32_bf16 v[68:71], a[180:183], a[4:7], v[68:71]\n" \
"v_mov_b32_e32 v54, 0x3f803f80\n" \
"s_waitcnt lgkmcnt(15)\n" \
"v_mfma_f32_16x16x32_bf16 v[68:71], a[176:179], a[0:3], v[68:71]\n" \
"s_mov_b32 s84, 0x33333333\n" \
"s_mov_b32 s85, 0xcccccccc\n" \
"v_mfma_f32_16x16x32_bf16 v[76:79], a[176:179], a[16:19], v[36:39]\n" \
"s_mov_b32 s86, 0x11111111\n" \
"s_mov_b32 s87, 0x11111111\n" \
"v_mfma_f32_16x16x32_bf16 v[76:79], a[180:183], a[20:23], v[76:79]\n" \
"s_mov_b32 s88, 0x44444444\n" \
"s_mov_b32 s89, 0x44444444\n" \
"v_mfma_f32_16x16x32_bf16 v[76:79], a[184:187], a[24:27], v[76:79]\n" \
"v_exp_f32_e32 v70, v70\n" \
"v_mfma_f32_16x16x32_bf16 v[76:79], a[188:191], a[28:31], v[76:79]\n" \
"v_exp_f32_e32 v71, v71\n" \
"v_mfma_f32_16x16x32_bf16 v[72:75], a[172:175], a[28:31], v[36:39]\n" \
"v_exp_f32_e32 v68, v68\n" \
"v_mfma_f32_16x16x32_bf16 v[72:75], a[168:171], a[24:27], v[72:75]\n" \
"v_exp_f32_e32 v69, v69\n" \
"v_mfma_f32_16x16x32_bf16 v[72:75], a[164:167], a[20:23], v[72:75]\n" \
"v_bfe_i32 v21, v20, 4, 1\n" \
"v_cvt_pk_bf16_f32 v71, v70, v71\n" \
"v_mfma_f32_16x16x32_bf16 v[72:75], a[160:163], a[16:19], v[72:75]\n" \
"v_exp_f32_e32 v66, v66\n" \
"v_mfma_f32_16x16x32_bf16 v[80:83], a[160:163], a[32:35], v[40:43]\n" \
"v_exp_f32_e32 v67, v67\n" \
"v_mfma_f32_16x16x32_bf16 v[80:83], a[164:167], a[36:39], v[80:83]\n" \
"v_exp_f32_e32 v64, v64\n" \
"v_mfma_f32_16x16x32_bf16 v[80:83], a[168:171], a[40:43], v[80:83]\n" \
"v_exp_f32_e32 v65, v65\n" \
"v_mfma_f32_16x16x32_bf16 v[80:83], a[172:175], a[44:47], v[80:83]\n" \
"v_exp_f32_e32 v72, v72\n" \
"v_mfma_f32_16x16x32_bf16 v[84:87], a[188:191], a[44:47], v[40:43]\n" \
"s_waitcnt vmcnt(3)\n" \
"v_cmp_ne_u32_e32 vcc, s62, v57\n" \
"v_mfma_f32_16x16x32_bf16 v[84:87], a[184:187], a[40:43], v[84:87]\n" \
"v_exp_f32_e32 v73, v73\n" \
"v_mfma_f32_16x16x32_bf16 v[84:87], a[180:183], a[36:39], v[84:87]\n" \
"v_exp_f32_e32 v74, v74\n" \
"v_mfma_f32_16x16x32_bf16 v[84:87], a[176:179], a[32:35], v[84:87]\n" \
"s_cbranch_vccnz L_skb_%=\n" \
"L_rkb_%=:\n" \
"v_cmp_eq_u32_e32 vcc, 0, v25\n" \
"v_mfma_f32_16x16x32_bf16 v[92:95], a[176:179], a[48:51], v[44:47]\n" \
"v_exp_f32_e32 v75, v75\n" \
"v_mfma_f32_16x16x32_bf16 v[92:95], a[180:183], a[52:55], v[92:95]\n" \
"v_cndmask_b32_e32 v28, 0, v26, vcc\n" \
"v_cvt_pk_bf16_f32 v72, v72, v73\n" \
"v_mfma_f32_16x16x32_bf16 v[92:95], a[184:187], a[56:59], v[92:95]\n" \
"v_exp_f32_e32 v76, v76\n" \
"v_mfma_f32_16x16x32_bf16 v[92:95], a[188:191], a[60:63], v[92:95]\n" \
"v_exp_f32_e32 v77, v77\n" \
"v_mfma_f32_16x16x32_bf16 v[88:91], a[172:175], a[60:63], v[44:47]\n" \
"v_exp_f32_e32 v78, v78\n" \
"v_mfma_f32_16x16x32_bf16 v[88:91], a[168:171], a[56:59], v[88:91]\n" \
"v_exp_f32_e32 v79, v79\n" \
"v_mfma_f32_16x16x32_bf16 v[88:91], a[164:167], a[52:55], v[88:91]\n" \
"v_cndmask_b32_e64 v22, 0, v54, s[84:85]\n" \
"v_cndmask_b32_e64 v23, 0, v54, s[86:87]\n" \
"v_mfma_f32_16x16x32_bf16 v[88:91], a[160:163], a[48:51], v[88:91]\n" \
"v_cndmask_b32_e64 v24, 0, v54, s[88:89]\n" \
"v_and_b32_e32 v21, 0xaaaaaaaa, v21\n" \
"s_waitcnt vmcnt(4) lgkmcnt(0)\n" \
"s_barrier\n" \
"v_cmp_eq_u32_e32 vcc, 1, v25\n" \
"v_cvt_pk_bf16_f32 v70, v68, v69\n" \
"v_cvt_pk_bf16_f32 v73, v74, v75\n" \
"v_mov_b32_e32 v62, 0\n" \
"v_accvgpr_write_b32 a208, 0\n" \
"v_accvgpr_write_b32 a209, 0\n" \
"v_accvgpr_write_b32 a210, 0\n" \
"v_accvgpr_write_b32 a211, 0\n" \
"v_accvgpr_write_b32 a200, v22\n" \
"v_accvgpr_write_b32 a201, v22\n" \
"v_accvgpr_write_b32 a202, v22\n" \
"v_accvgpr_write_b32 a203, v22\n" \
"v_accvgpr_write_b32 a204, v23\n" \
"v_accvgpr_write_b32 a205, v23\n" \
"v_accvgpr_write_b32 a206, v23\n" \
"v_accvgpr_write_b32 a207, v23\n" \
"v_accvgpr_write_b32 a212, v24\n" \
"v_accvgpr_write_b32 a213, v24\n" \
"v_accvgpr_write_b32 a214, v24\n" \
"v_accvgpr_write_b32 a215, v24\n" \
"v_or_b32_e32 v21, 0x44444444, v21\n" \
"s_mov_b32 s64, 0x8000\n" \
"v_add_u32_e32 v50, 0x6000, v55\n" \
"v_add_u32_e32 v51, 0xa000, v55\n" \
"v_add_u32_e32 v53, 0x10000, v55\n" \
"v_add_u32_e32 v48, 0xc000, v20\n" \
"v_add_u32_e32 v49, s17, v20\n" \
"v_cndmask_b32_e32 v29, 0, v26, vcc\n" \
"v_accvgpr_write_b32 a220, v96\n" \
"v_accvgpr_write_b32 a221, v97\n" \
"v_accvgpr_write_b32 a222, v98\n" \
"v_accvgpr_write_b32 a223, v99\n" \
"v_accvgpr_write_b32 a224, v100\n" \
"v_accvgpr_write_b32 a225, v101\n" \
"v_accvgpr_write_b32 a226, v102\n" \
"v_accvgpr_write_b32 a227, v103\n" \
"v_accvgpr_write_b32 a228, v104\n" \
"v_accvgpr_write_b32 a229, v105\n" \
"v_accvgpr_write_b32 a230, v106\n" \
"v_accvgpr_write_b32 a231, v107\n" \
"v_accvgpr_write_b32 a232, v108\n" \
"v_accvgpr_write_b32 a233, v109\n" \
"v_accvgpr_write_b32 a234, v110\n" \
"v_accvgpr_write_b32 a235, v111\n" \
"v_cvt_pk_bf16_f32 v69, v66, v67\n" \
"v_cvt_pk_bf16_f32 v68, v64, v65\n" \
"v_cvt_pk_bf16_f32 v74, v76, v77\n" \
"v_cvt_pk_bf16_f32 v75, v78, v79\n" \
"s_sub_u32 s66, s18, 1\n" \
"s_cbranch_scc1 L_tr_%=\n" \
".fill (L_ab + 1856 - .) / 4, 4, 0xBF800000\n" \
"L_1_%=:\n" \
"v_mfma_f32_16x16x32_bf16 v[96:99], a[128:131], a[0:3], v[32:35]\n" \
"s_xor_b32 m0, s65, 0x12000\n" \
"ds_read_b128 a[160:163], v49 offset:0\n" \
"v_mfma_f32_16x16x32_bf16 v[96:99], a[132:135], a[4:7], v[96:99]\n" \
"buffer_load_dwordx4 v50, s[8:11], s64 offen offset:0 sc1 lds\n" \
"v_mfma_f32_16x16x32_bf16 v[96:99], a[136:139], a[8:11], v[96:99]\n" \
"ds_read_b128 a[164:167], v49 offset:1024\n" \
"ds_read_b128 a[168:171], v49 offset:2048\n" \
"v_mfma_f32_16x16x32_bf16 v[96:99], a[140:143], a[12:15], v[96:99]\n" \
"ds_read_b128 a[172:175], v49 offset:3072\n" \
"ds_read_b128 a[176:179], v49 offset:4096\n" \
"v_mfma_f32_16x16x32_bf16 v[100:103], a[156:159], a[12:15], v[32:35]\n" \
"ds_read_b128 a[180:183], v49 offset:5120\n" \
"ds_read_b128 a[184:187], v49 offset:6144\n" \
"v_mfma_f32_16x16x32_bf16 v[100:103], a[152:155], a[8:11], v[100:103]\n" \
"v_exp_f32_e64 v86, v86\n" \
"v_mfma_f32_16x16x32_bf16 v[100:103], a[148:151], a[4:7], v[100:103]\n" \
"v_exp_f32_e64 v87, v87\n" \
"v_mfma_f32_16x16x32_bf16 v[100:103], a[144:147], a[0:3], v[100:103]\n" \
"ds_read_b128 a[188:191], v49 offset:7168\n" \
"v_cvt_pk_bf16_f32 v87, v86, v87\n" \
"v_mfma_f32_16x16x32_bf16 v[108:111], a[144:147], a[16:19], v[36:39]\n" \
"v_exp_f32_e64 v84, v84\n" \
"v_mfma_f32_16x16x32_bf16 v[108:111], a[148:151], a[20:23], v[108:111]\n" \
"v_exp_f32_e64 v85, v85\n" \
"v_mfma_f32_16x16x32_bf16 v[108:111], a[152:155], a[24:27], v[108:111]\n" \
"v_exp_f32_e64 v82, v82\n" \
"v_mfma_f32_16x16x32_bf16 v[108:111], a[156:159], a[28:31], v[108:111]\n" \
"v_exp_f32_e64 v83, v83\n" \
"v_mfma_f32_16x16x32_bf16 v[104:107], a[140:143], a[28:31], v[36:39]\n" \
"v_exp_f32_e64 v80, v80\n" \
"v_mfma_f32_16x16x32_bf16 v[104:107], a[136:139], a[24:27], v[104:107]\n" \
"v_exp_f32_e64 v81, v81\n" \
"v_mfma_f32_16x16x32_bf16 v[104:107], a[132:135], a[20:23], v[104:107]\n" \
"v_exp_f32_e64 v88, v88\n" \
"v_mfma_f32_16x16x32_bf16 v[104:107], a[128:131], a[16:19], v[104:107]\n" \
"buffer_load_dwordx4 v50, s[8:11], s64 offen offset:1024 sc1 lds\n" \
"v_mfma_f32_16x16x32_bf16 v[112:115], a[128:131], a[32:35], v[40:43]\n" \
"s_xor_b32 m0, s65, 0x6000\n" \
"v_cvt_pk_bf16_f32 v86, v84, v85\n" \
"v_mfma_f32_16x16x32_bf16 v[112:115], a[132:135], a[36:39], v[112:115]\n" \
"v_cvt_pk_bf16_f32 v85, v82, v83\n" \
"v_cvt_pk_bf16_f32 v84, v80, v81\n" \
"v_mfma_f32_16x16x32_bf16 v[112:115], a[136:139], a[40:43], v[112:115]\n" \
"v_exp_f32_e64 v89, v89\n" \
"v_mfma_f32_16x16x32_bf16 v[112:115], a[140:143], a[44:47], v[112:115]\n" \
"v_exp_f32_e64 v90, v90\n" \
"v_mfma_f32_16x16x32_bf16 v[116:119], a[156:159], a[44:47], v[40:43]\n" \
"v_exp_f32_e64 v91, v91\n" \
"v_mfma_f32_16x16x32_bf16 v[116:119], a[152:155], a[40:43], v[116:119]\n" \
"v_exp_f32_e64 v92, v92\n" \
"v_mfma_f32_16x16x32_bf16 v[116:119], a[148:151], a[36:39], v[116:119]\n" \
"v_exp_f32_e64 v93, v93\n" \
"v_mfma_f32_16x16x32_bf16 v[116:119], a[144:147], a[32:35], v[116:119]\n" \
"v_exp_f32_e64 v94, v94\n" \
"v_mfma_f32_16x16x32_bf16 v[124:127], a[144:147], a[48:51], v[44:47]\n" \
"v_cvt_pk_bf16_f32 v88, v88, v89\n" \
"v_cvt_pk_bf16_f32 v89, v90, v91\n" \
"v_mfma_f32_16x16x32_bf16 v[124:127], a[148:151], a[52:55], v[124:127]\n" \
"v_exp_f32_e64 v95, v95\n" \
"v_mfma_f32_16x16x32_bf16 v[124:127], a[152:155], a[56:59], v[124:127]\n" \
"v_cvt_pk_bf16_f32 v90, v92, v93\n" \
"v_cvt_pk_bf16_f32 v91, v94, v95\n" \
"v_mfma_f32_16x16x32_bf16 v[124:127], a[156:159], a[60:63], v[124:127]\n" \
"ds_read_b128 a[144:147], v48 offset:4096\n" \
"ds_read_b128 a[148:151], v48 offset:5120\n" \
"v_mfma_f32_16x16x32_bf16 v[120:123], a[140:143], a[60:63], v[44:47]\n" \
"ds_read_b128 a[152:155], v48 offset:6144\n" \
"ds_read_b128 a[140:143], v48 offset:3072\n" \
"v_mfma_f32_16x16x32_bf16 v[120:123], a[136:139], a[56:59], v[120:123]\n" \
"ds_read_b128 a[136:139], v48 offset:2048\n" \
"ds_read_b128 a[156:159], v48 offset:7168\n" \
"v_mfma_f32_16x16x32_bf16 v[120:123], a[132:135], a[52:55], v[120:123]\n" \
"buffer_load_dwordx4 v51, s[8:11], s64 offen offset:0 sc1 lds\n" \
"v_mfma_f32_16x16x32_bf16 v[120:123], a[128:131], a[48:51], v[120:123]\n" \
"ds_read_b128 a[128:131], v48 offset:0\n" \
"s_waitcnt lgkmcnt(7)\n" \
"v_mfma_f32_16x16x32_bf16 v[128:131], a[160:163], v[68:71], v[128:131]\n" \
"v_exp_f32_e32 v102, v102\n" \
"v_mfma_f32_16x16x32_bf16 v[160:163], a[160:163], v[72:75], v[160:163]\n" \
"v_exp_f32_e64 v103, v103\n" \
"v_mfma_f32_16x16x32_bf16 v[132:135], a[164:167], v[68:71], v[132:135]\n" \
"ds_read_b128 a[132:135], v48 offset:1024\n" \
"v_cvt_pk_bf16_f32 v103, v102, v103\n" \
"v_mfma_f32_16x16x32_bf16 v[164:167], a[164:167], v[72:75], v[164:167]\n" \
"v_exp_f32_e64 v100, v100\n" \
"v_mfma_f32_16x16x32_bf16 v[136:139], a[168:171], v[68:71], v[136:139]\n" \
"v_exp_f32_e64 v101, v101\n" \
"v_mfma_f32_16x16x32_bf16 v[168:171], a[168:171], v[72:75], v[168:171]\n" \
"v_exp_f32_e64 v98, v98\n" \
"v_mfma_f32_16x16x32_bf16 v[140:143], a[172:175], v[68:71], v[140:143]\n" \
"v_exp_f32_e64 v99, v99\n" \
"v_mfma_f32_16x16x32_bf16 v[172:175], a[172:175], v[72:75], v[172:175]\n" \
"v_exp_f32_e64 v96, v96\n" \
"v_mfma_f32_16x16x32_bf16 v[144:147], a[176:179], v[68:71], v[144:147]\n" \
"v_exp_f32_e64 v97, v97\n" \
"v_mfma_f32_16x16x32_bf16 v[176:179], a[176:179], v[72:75], v[176:179]\n" \
"v_exp_f32_e64 v104, v104\n" \
"v_mfma_f32_16x16x32_bf16 v[148:151], a[180:183], v[68:71], v[148:151]\n" \
"v_exp_f32_e64 v105, v105\n" \
"v_mfma_f32_16x16x32_bf16 v[180:183], a[180:183], v[72:75], v[180:183]\n" \
"v_cvt_pk_bf16_f32 v102, v100, v101\n" \
"v_cvt_pk_bf16_f32 v101, v98, v99\n" \
"v_mfma_f32_16x16x32_bf16 v[152:155], a[184:187], v[68:71], v[152:155]\n" \
"v_cvt_pk_bf16_f32 v100, v96, v97\n" \
"v_cvt_pk_bf16_f32 v104, v104, v105\n" \
"v_mfma_f32_16x16x32_bf16 v[184:187], a[184:187], v[72:75], v[184:187]\n" \
"buffer_load_dwordx4 v51, s[8:11], s64 offen offset:1024 sc1 lds\n" \
"v_mfma_f32_16x16x32_bf16 v[156:159], a[188:191], v[68:71], v[156:159]\n" \
"v_exp_f32_e64 v106, v106\n" \
"v_mfma_f32_16x16x32_bf16 v[188:191], a[188:191], v[72:75], v[188:191]\n" \
"v_exp_f32_e64 v107, v107\n" \
"v_smfmac_f32_16x16x64_bf16 a[192:195], a[200:203], v[68:75], v21\n" \
"v_exp_f32_e64 v108, v108\n" \
"v_mfma_f32_16x16x32_bf16 v[220:223], a[188:191], v[84:87], v[220:223]\n" \
"v_exp_f32_e64 v109, v109\n" \
"v_mfma_f32_16x16x32_bf16 v[252:255], a[188:191], v[88:91], v[252:255]\n" \
"v_exp_f32_e64 v110, v110\n" \
"v_mfma_f32_16x16x32_bf16 v[216:219], a[184:187], v[84:87], v[216:219]\n" \
"v_exp_f32_e64 v111, v111\n" \
"v_mfma_f32_16x16x32_bf16 v[248:251], a[184:187], v[88:91], v[248:251]\n" \
"v_exp_f32_e64 v118, v118\n" \
"v_mfma_f32_16x16x32_bf16 v[212:215], a[180:183], v[84:87], v[212:215]\n" \
"v_exp_f32_e64 v119, v119\n" \
"v_mfma_f32_16x16x32_bf16 v[244:247], a[180:183], v[88:91], v[244:247]\n" \
"v_cvt_pk_bf16_f32 v105, v106, v107\n" \
"v_cvt_pk_bf16_f32 v106, v108, v109\n" \
"v_mfma_f32_16x16x32_bf16 v[208:211], a[176:179], v[84:87], v[208:211]\n" \
"v_cvt_pk_bf16_f32 v107, v110, v111\n" \
"v_cvt_pk_bf16_f32 v119, v118, v119\n" \
"v_mfma_f32_16x16x32_bf16 v[240:243], a[176:179], v[88:91], v[240:243]\n" \
"v_exp_f32_e64 v116, v116\n" \
"v_mfma_f32_16x16x32_bf16 v[204:207], a[172:175], v[84:87], v[204:207]\n" \
"v_exp_f32_e64 v117, v117\n" \
"v_mfma_f32_16x16x32_bf16 v[236:239], a[172:175], v[88:91], v[236:239]\n" \
"v_exp_f32_e64 v114, v114\n" \
"v_mfma_f32_16x16x32_bf16 v[200:203], a[168:171], v[84:87], v[200:203]\n" \
"v_exp_f32_e64 v115, v115\n" \
"v_mfma_f32_16x16x32_bf16 v[232:235], a[168:171], v[88:91], v[232:235]\n" \
"v_exp_f32_e64 v112, v112\n" \
"v_mfma_f32_16x16x32_bf16 v[196:199], a[164:167], v[84:87], v[196:199]\n" \
"v_exp_f32_e64 v113, v113\n" \
"v_mfma_f32_16x16x32_bf16 v[228:231], a[164:167], v[88:91], v[228:231]\n" \
"v_exp_f32_e64 v120, v120\n" \
"v_mfma_f32_16x16x32_bf16 v[192:195], a[160:163], v[84:87], v[192:195]\n" \
"v_exp_f32_e64 v121, v121\n" \
"v_mfma_f32_16x16x32_bf16 v[224:227], a[160:163], v[88:91], v[224:227]\n" \
"v_cvt_pk_bf16_f32 v118, v116, v117\n" \
"v_cvt_pk_bf16_f32 v117, v114, v115\n" \
"v_smfmac_f32_16x16x64_bf16 a[196:199], a[200:203], v[84:91], v21\n" \
"v_cvt_pk_bf16_f32 v116, v112, v113\n" \
"v_cvt_pk_bf16_f32 v120, v120, v121\n" \
"s_waitcnt vmcnt(4) lgkmcnt(0)\n" \
"s_barrier\n" \
"v_mfma_f32_16x16x32_bf16 v[64:67], a[128:131], a[0:3], v[32:35]\n" \
"s_xor_b32 m0, s65, 0x14000\n" \
"ds_read_b128 a[160:163], v49 offset:8192\n" \
"v_mfma_f32_16x16x32_bf16 v[64:67], a[132:135], a[4:7], v[64:67]\n" \
"buffer_load_dwordx4 v52, s[8:11], s64 offen offset:0 sc1 lds\n" \
"v_mfma_f32_16x16x32_bf16 v[64:67], a[136:139], a[8:11], v[64:67]\n" \
"ds_read_b128 a[164:167], v49 offset:9216\n" \
"ds_read_b128 a[168:171], v49 offset:10240\n" \
"v_mfma_f32_16x16x32_bf16 v[64:67], a[140:143], a[12:15], v[64:67]\n" \
"ds_read_b128 a[172:175], v49 offset:11264\n" \
"ds_read_b128 a[176:179], v49 offset:12288\n" \
"v_mfma_f32_16x16x32_bf16 v[68:71], a[156:159], a[12:15], v[32:35]\n" \
"ds_read_b128 a[180:183], v49 offset:13312\n" \
"ds_read_b128 a[184:187], v49 offset:14336\n" \
"v_mfma_f32_16x16x32_bf16 v[68:71], a[152:155], a[8:11], v[68:71]\n" \
"v_exp_f32_e64 v122, v122\n" \
"v_mfma_f32_16x16x32_bf16 v[68:71], a[148:151], a[4:7], v[68:71]\n" \
"v_exp_f32_e64 v123, v123\n" \
"v_mfma_f32_16x16x32_bf16 v[68:71], a[144:147], a[0:3], v[68:71]\n" \
"ds_read_b128 a[188:191], v49 offset:15360\n" \
"v_cvt_pk_bf16_f32 v121, v122, v123\n" \
"v_mfma_f32_16x16x32_bf16 v[76:79], a[144:147], a[16:19], v[36:39]\n" \
"v_exp_f32_e64 v124, v124\n" \
"v_mfma_f32_16x16x32_bf16 v[76:79], a[148:151], a[20:23], v[76:79]\n" \
"v_exp_f32_e64 v125, v125\n" \
"v_mfma_f32_16x16x32_bf16 v[76:79], a[152:155], a[24:27], v[76:79]\n" \
"v_exp_f32_e64 v126, v126\n" \
"v_mfma_f32_16x16x32_bf16 v[76:79], a[156:159], a[28:31], v[76:79]\n" \
"v_exp_f32_e64 v127, v127\n" \
"v_mfma_f32_16x16x32_bf16 v[72:75], a[140:143], a[28:31], v[36:39]\n" \
"v_cvt_pk_bf16_f32 v122, v124, v125\n" \
"v_cvt_pk_bf16_f32 v123, v126, v127\n" \
"v_mfma_f32_16x16x32_bf16 v[72:75], a[136:139], a[24:27], v[72:75]\n" \
"v_exp_f32_e64 v70, v70\n" \
"v_mfma_f32_16x16x32_bf16 v[72:75], a[132:135], a[20:23], v[72:75]\n" \
"v_exp_f32_e64 v71, v71\n" \
"v_mfma_f32_16x16x32_bf16 v[72:75], a[128:131], a[16:19], v[72:75]\n" \
"buffer_load_dwordx4 v52, s[8:11], s64 offen offset:1024 sc1 lds\n" \
"v_mfma_f32_16x16x32_bf16 v[80:83], a[128:131], a[32:35], v[40:43]\n" \
"s_mov_b32 m0, s65\n" \
"v_cvt_pk_bf16_f32 v71, v70, v71\n" \
"v_mfma_f32_16x16x32_bf16 v[80:83], a[132:135], a[36:39], v[80:83]\n" \
"v_exp_f32_e32 v68, v68\n" \
"v_mfma_f32_16x16x32_bf16 v[80:83], a[136:139], a[40:43], v[80:83]\n" \
"v_exp_f32_e64 v69, v69\n" \
"v_mfma_f32_16x16x32_bf16 v[80:83], a[140:143], a[44:47], v[80:83]\n" \
"v_exp_f32_e64 v72, v72\n" \
"v_mfma_f32_16x16x32_bf16 v[84:87], a[156:159], a[44:47], v[40:43]\n" \
"v_exp_f32_e64 v73, v73\n" \
"v_mfma_f32_16x16x32_bf16 v[84:87], a[152:155], a[40:43], v[84:87]\n" \
"v_exp_f32_e64 v74, v74\n" \
"v_mfma_f32_16x16x32_bf16 v[84:87], a[148:151], a[36:39], v[84:87]\n" \
"v_exp_f32_e64 v75, v75\n" \
"v_mfma_f32_16x16x32_bf16 v[84:87], a[144:147], a[32:35], v[84:87]\n" \
"v_exp_f32_e64 v66, v66\n" \
"v_mfma_f32_16x16x32_bf16 v[92:95], a[144:147], a[48:51], v[44:47]\n" \
"ds_read_b128 a[144:147], v48 offset:12288\n" \
"v_cvt_pk_bf16_f32 v72, v72, v73\n" \
"v_mfma_f32_16x16x32_bf16 v[92:95], a[148:151], a[52:55], v[92:95]\n" \
"ds_read_b128 a[148:151], v48 offset:13312\n" \
"v_cvt_pk_bf16_f32 v70, v68, v69\n" \
"v_mfma_f32_16x16x32_bf16 v[92:95], a[152:155], a[56:59], v[92:95]\n" \
"ds_read_b128 a[152:155], v48 offset:14336\n" \
"v_cvt_pk_bf16_f32 v73, v74, v75\n" \
"v_mfma_f32_16x16x32_bf16 v[92:95], a[156:159], a[60:63], v[92:95]\n" \
"ds_read_b128 a[156:159], v48 offset:15360\n" \
"v_add_u32_e32 v49, 0x10000, v48\n" \
"v_mfma_f32_16x16x32_bf16 v[88:91], a[140:143], a[60:63], v[44:47]\n" \
"ds_read_b128 a[140:143], v48 offset:11264\n" \
"s_xor_b32 s65, s65, 0x4000\n" \
"v_mfma_f32_16x16x32_bf16 v[88:91], a[136:139], a[56:59], v[88:91]\n" \
"v_exp_f32_e64 v67, v67\n" \
"v_mfma_f32_16x16x32_bf16 v[88:91], a[132:135], a[52:55], v[88:91]\n" \
"buffer_load_dwordx4 v53, s[8:11], s64 offen offset:0 sc1 lds\n" \
"v_mfma_f32_16x16x32_bf16 v[88:91], a[128:131], a[48:51], v[88:91]\n" \
"ds_read_b128 a[128:131], v48 offset:8192\n" \
"s_waitcnt lgkmcnt(6)\n" \
"v_mfma_f32_16x16x32_bf16 v[128:131], a[160:163], v[100:103], v[128:131]\n" \
"ds_read_b128 a[132:135], v48 offset:9216\n" \
"ds_read_b128 a[136:139], v48 offset:10240\n" \
"v_mfma_f32_16x16x32_bf16 v[160:163], a[160:163], v[104:107], v[160:163]\n" \
"v_exp_f32_e32 v64, v64\n" \
"v_mfma_f32_16x16x32_bf16 v[132:135], a[164:167], v[100:103], v[132:135]\n" \
"v_exp_f32_e64 v65, v65\n" \
"v_mfma_f32_16x16x32_bf16 v[164:167], a[164:167], v[104:107], v[164:167]\n" \
"v_exp_f32_e64 v76, v76\n" \
"v_mfma_f32_16x16x32_bf16 v[136:139], a[168:171], v[100:103], v[136:139]\n" \
"v_exp_f32_e64 v77, v77\n" \
"v_mfma_f32_16x16x32_bf16 v[168:171], a[168:171], v[104:107], v[168:171]\n" \
"v_exp_f32_e64 v78, v78\n" \
"v_mfma_f32_16x16x32_bf16 v[140:143], a[172:175], v[100:103], v[140:143]\n" \
"v_exp_f32_e64 v79, v79\n" \
"v_mfma_f32_16x16x32_bf16 v[172:175], a[172:175], v[104:107], v[172:175]\n" \
"v_cvt_pk_bf16_f32 v69, v66, v67\n" \
"v_cvt_pk_bf16_f32 v68, v64, v65\n" \
"v_mfma_f32_16x16x32_bf16 v[144:147], a[176:179], v[100:103], v[144:147]\n" \
"v_cvt_pk_bf16_f32 v74, v76, v77\n" \
"v_cvt_pk_bf16_f32 v75, v78, v79\n" \
"v_mfma_f32_16x16x32_bf16 v[176:179], a[176:179], v[104:107], v[176:179]\n" \
"v_xor_b32_e32 v48, 0x4000, v48\n" \
"v_mfma_f32_16x16x32_bf16 v[148:151], a[180:183], v[100:103], v[148:151]\n" \
"v_mfma_f32_16x16x32_bf16 v[180:183], a[180:183], v[104:107], v[180:183]\n" \
"v_mfma_f32_16x16x32_bf16 v[152:155], a[184:187], v[100:103], v[152:155]\n" \
"v_mfma_f32_16x16x32_bf16 v[184:187], a[184:187], v[104:107], v[184:187]\n" \
"buffer_load_dwordx4 v53, s[8:11], s64 offen offset:1024 sc1 lds\n" \
"v_mfma_f32_16x16x32_bf16 v[156:159], a[188:191], v[100:103], v[156:159]\n" \
"s_add_u32 s64, s64, 0x8000\n" \
"s_sub_u32 s66, s66, 1\n" \
"s_nop 0\n" \
"v_mfma_f32_16x16x32_bf16 v[188:191], a[188:191], v[104:107], v[188:191]\n" \
"v_smfmac_f32_16x16x64_bf16 a[192:195], a[200:203], v[100:107], v21\n" \
"v_mfma_f32_16x16x32_bf16 v[220:223], a[188:191], v[116:119], v[220:223]\n" \
"v_mfma_f32_16x16x32_bf16 v[252:255], a[188:191], v[120:123], v[252:255]\n" \
"v_mfma_f32_16x16x32_bf16 v[216:219], a[184:187], v[116:119], v[216:219]\n" \
"v_mfma_f32_16x16x32_bf16 v[248:251], a[184:187], v[120:123], v[248:251]\n" \
"v_mfma_f32_16x16x32_bf16 v[212:215], a[180:183], v[116:119], v[212:215]\n" \
"v_mfma_f32_16x16x32_bf16 v[244:247], a[180:183], v[120:123], v[244:247]\n" \
"v_mfma_f32_16x16x32_bf16 v[208:211], a[176:179], v[116:119], v[208:211]\n" \
"v_mfma_f32_16x16x32_bf16 v[240:243], a[176:179], v[120:123], v[240:243]\n" \
"v_mfma_f32_16x16x32_bf16 v[204:207], a[172:175], v[116:119], v[204:207]\n" \
"v_mfma_f32_16x16x32_bf16 v[236:239], a[172:175], v[120:123], v[236:239]\n" \
"v_mfma_f32_16x16x32_bf16 v[200:203], a[168:171], v[116:119], v[200:203]\n" \
"v_mfma_f32_16x16x32_bf16 v[232:235], a[168:171], v[120:123], v[232:235]\n" \
"v_mfma_f32_16x16x32_bf16 v[196:199], a[164:167], v[116:119], v[196:199]\n" \
"v_mfma_f32_16x16x32_bf16 v[228:231], a[164:167], v[120:123], v[228:231]\n" \
"v_mfma_f32_16x16x32_bf16 v[192:195], a[160:163], v[116:119], v[192:195]\n" \
"v_mfma_f32_16x16x32_bf16 v[224:227], a[160:163], v[120:123], v[224:227]\n" \
"v_smfmac_f32_16x16x64_bf16 a[196:199], a[200:203], v[116:123], v21\n" \
"s_waitcnt vmcnt(4) lgkmcnt(0)\n" \
"s_barrier\n" \
"s_cbranch_scc0 L_1_%=\n" \
"L_tr_%=:\n" \
"s_setpc_b64 s[28:29]\n" \
".fill (L_ab + 4096 - .) / 4, 4, 0xBF800000\n" \
".fill (L_ab + 4096 - .) / 4, 4, 0xBF800000\n" \
"L_ap1:\n" \
"s_setpc_b64 s[88:89]\n" \
"L_gtr:\n" \
"v_mfma_f32_16x16x32_bf16 v[96:99], a[128:131], a[0:3], v[32:35]\n" \
"v_mov_b32_e32 v56, s62\n" \
"v_readlane_b32 s68, v11, 0\n" \
"v_mfma_f32_16x16x32_bf16 v[96:99], a[132:135], a[4:7], v[96:99]\n" \
"s_xor_b32 m0, s65, 0x12000\n" \
"s_mov_b32 s65, s13\n" \
"v_mfma_f32_16x16x32_bf16 v[96:99], a[136:139], a[8:11], v[96:99]\n" \
"buffer_load_dwordx4 v50, s[8:11], s64 offen offset:0 sc1 lds\n" \
"v_mfma_f32_16x16x32_bf16 v[96:99], a[140:143], a[12:15], v[96:99]\n" \
"v_add_u32_e32 v63, s15, v20\n" \
"s_add_u32 s67, s14, 40960\n" \
"v_mfma_f32_16x16x32_bf16 v[100:103], a[156:159], a[12:15], v[32:35]\n" \
"buffer_load_dwordx4 v50, s[8:11], s64 offen offset:1024 sc1 lds\n" \
"s_mov_b64 exec, 1\n" \
"buffer_store_dword v56, off, s[56:59], s68 offset:0 sc1\n" \
"s_mov_b64 exec, -1\n" \
"v_mfma_f32_16x16x32_bf16 v[100:103], a[152:155], a[8:11], v[100:103]\n" \
"v_add_u32_e32 v50, 0x6000, v63\n" \
"s_xor_b32 m0, s65, 0x8000\n" \
"v_mfma_f32_16x16x32_bf16 v[100:103], a[148:151], a[4:7], v[100:103]\n" \
"ds_read_b128 a[160:163], v49 offset:0\n" \
"ds_read_b128 a[164:167], v49 offset:1024\n" \
"v_mfma_f32_16x16x32_bf16 v[100:103], a[144:147], a[0:3], v[100:103]\n" \
"ds_read_b128 a[168:171], v49 offset:2048\n" \
"ds_read_b128 a[172:175], v49 offset:3072\n" \
"v_mfma_f32_16x16x32_bf16 v[108:111], a[144:147], a[16:19], v[36:39]\n" \
"buffer_load_dwordx4 v50, s[8:11], s67 offen offset:0 sc1 lds\n" \
"v_mfma_f32_16x16x32_bf16 v[108:111], a[148:151], a[20:23], v[108:111]\n" \
"ds_read_b128 a[176:179], v49 offset:4096\n" \
"ds_read_b128 a[180:183], v49 offset:5120\n" \
"v_mfma_f32_16x16x32_bf16 v[108:111], a[152:155], a[24:27], v[108:111]\n" \
"ds_read_b128 a[184:187], v49 offset:6144\n" \
"ds_read_b128 a[188:191], v49 offset:7168\n" \
"v_mfma_f32_16x16x32_bf16 v[108:111], a[156:159], a[28:31], v[108:111]\n" \
"s_mov_b32 s64, s14\n" \
"v_add_u32_e32 v51, 0x12000, v63\n" \
"v_mfma_f32_16x16x32_bf16 v[104:107], a[140:143], a[28:31], v[36:39]\n" \
"buffer_load_dwordx4 v50, s[8:11], s67 offen offset:1024 sc1 lds\n" \
"v_mfma_f32_16x16x32_bf16 v[104:107], a[136:139], a[24:27], v[104:107]\n" \
"v_exp_f32_e32 v86, v86\n" \
"v_mfma_f32_16x16x32_bf16 v[104:107], a[132:135], a[20:23], v[104:107]\n" \
"v_exp_f32_e32 v87, v87\n" \
"v_mfma_f32_16x16x32_bf16 v[104:107], a[128:131], a[16:19], v[104:107]\n" \
"v_exp_f32_e32 v84, v84\n" \
"v_mfma_f32_16x16x32_bf16 v[112:115], a[128:131], a[32:35], v[40:43]\n" \
"v_exp_f32_e32 v85, v85\n" \
"v_mfma_f32_16x16x32_bf16 v[112:115], a[132:135], a[36:39], v[112:115]\n" \
"buffer_load_dwordx4 v50, s[8:11], s67 offen offset:2048 sc1 lds\n" \
"v_mfma_f32_16x16x32_bf16 v[112:115], a[136:139], a[40:43], v[112:115]\n" \
"v_cvt_pk_bf16_f32 v87, v86, v87\n" \
"v_cvt_pk_bf16_f32 v86, v84, v85\n" \
"v_mfma_f32_16x16x32_bf16 v[112:115], a[140:143], a[44:47], v[112:115]\n" \
"v_exp_f32_e32 v82, v82\n" \
"v_mfma_f32_16x16x32_bf16 v[116:119], a[156:159], a[44:47], v[40:43]\n" \
"v_exp_f32_e32 v83, v83\n" \
"v_mfma_f32_16x16x32_bf16 v[116:119], a[152:155], a[40:43], v[116:119]\n" \
"buffer_load_dwordx4 v50, s[8:11], s67 offen offset:3072 sc1 lds\n" \
"v_mfma_f32_16x16x32_bf16 v[116:119], a[148:151], a[36:39], v[116:119]\n" \
"s_xor_b32 m0, s65, 0xa000\n" \
"v_cvt_pk_bf16_f32 v85, v82, v83\n" \
"v_mfma_f32_16x16x32_bf16 v[116:119], a[144:147], a[32:35], v[116:119]\n" \
"v_exp_f32_e32 v80, v80\n" \
"v_mfma_f32_16x16x32_bf16 v[124:127], a[144:147], a[48:51], v[44:47]\n" \
"v_exp_f32_e32 v81, v81\n" \
"v_mfma_f32_16x16x32_bf16 v[124:127], a[148:151], a[52:55], v[124:127]\n" \
"buffer_load_dwordx4 v51, s[8:11], s64 offen offset:0 sc1 lds\n" \
"v_mfma_f32_16x16x32_bf16 v[124:127], a[152:155], a[56:59], v[124:127]\n" \
"v_cvt_pk_bf16_f32 v84, v80, v81\n" \
"v_readlane_b32 s93, v11, 19\n" \
"v_mfma_f32_16x16x32_bf16 v[124:127], a[156:159], a[60:63], v[124:127]\n" \
"v_exp_f32_e32 v88, v88\n" \
"v_mfma_f32_16x16x32_bf16 v[120:123], a[140:143], a[60:63], v[44:47]\n" \
"v_exp_f32_e32 v89, v89\n" \
"v_mfma_f32_16x16x32_bf16 v[120:123], a[136:139], a[56:59], v[120:123]\n" \
"v_exp_f32_e32 v90, v90\n" \
"v_mfma_f32_16x16x32_bf16 v[120:123], a[132:135], a[52:55], v[120:123]\n" \
"buffer_load_dwordx4 v51, s[8:11], s64 offen offset:1024 sc1 lds\n" \
"v_mfma_f32_16x16x32_bf16 v[120:123], a[128:131], a[48:51], v[120:123]\n" \
"v_cvt_pk_bf16_f32 v88, v88, v89\n" \
"s_waitcnt lgkmcnt(0)\n" \
"v_mfma_f32_16x16x32_bf16 v[128:131], a[160:163], v[68:71], v[128:131]\n" \
"v_exp_f32_e32 v91, v91\n" \
"v_mfma_f32_16x16x32_bf16 v[160:163], a[160:163], v[72:75], v[160:163]\n" \
"v_exp_f32_e32 v92, v92\n" \
"v_mfma_f32_16x16x32_bf16 v[132:135], a[164:167], v[68:71], v[132:135]\n" \
"buffer_load_dwordx4 v51, s[8:11], s64 offen offset:2048 sc1 lds\n" \
"v_mfma_f32_16x16x32_bf16 v[164:167], a[164:167], v[72:75], v[164:167]\n" \
"v_exp_f32_e32 v93, v93\n" \
"v_mfma_f32_16x16x32_bf16 v[136:139], a[168:171], v[68:71], v[136:139]\n" \
"v_exp_f32_e32 v94, v94\n" \
"v_mfma_f32_16x16x32_bf16 v[168:171], a[168:171], v[72:75], v[168:171]\n" \
"v_exp_f32_e32 v95, v95\n" \
"v_mfma_f32_16x16x32_bf16 v[140:143], a[172:175], v[68:71], v[140:143]\n" \
"buffer_load_dwordx4 v51, s[8:11], s64 offen offset:3072 sc1 lds\n" \
"v_mfma_f32_16x16x32_bf16 v[172:175], a[172:175], v[72:75], v[172:175]\n" \
"v_cvt_pk_bf16_f32 v89, v90, v91\n" \
"v_cvt_pk_bf16_f32 v90, v92, v93\n" \
"v_mfma_f32_16x16x32_bf16 v[144:147], a[176:179], v[68:71], v[144:147]\n" \
"v_cvt_pk_bf16_f32 v91, v94, v95\n" \
"v_readlane_b32 s80, v11, 11\n" \
"v_mfma_f32_16x16x32_bf16 v[176:179], a[176:179], v[72:75], v[176:179]\n" \
"v_readlane_b32 s81, v11, 12\n" \
"s_mov_b32 s82, 0x47fca0\n" \
"v_mfma_f32_16x16x32_bf16 v[148:151], a[180:183], v[68:71], v[148:151]\n" \
"s_mov_b32 s83, 0x20000\n" \
"v_readlane_b32 s92, v11, 26\n" \
"v_mfma_f32_16x16x32_bf16 v[180:183], a[180:183], v[72:75], v[180:183]\n" \
"s_add_u32 m0, s93, 0x0\n" \
"ds_read_b128 a[128:131], v48 offset:0\n" \
"v_mfma_f32_16x16x32_bf16 v[152:155], a[184:187], v[68:71], v[152:155]\n" \
"ds_read_b128 a[144:147], v48 offset:4096\n" \
"ds_read_b128 a[132:135], v48 offset:1024\n" \
"v_mfma_f32_16x16x32_bf16 v[184:187], a[184:187], v[72:75], v[184:187]\n" \
"buffer_load_dwordx4 v0, s[80:83], s92 offen offset:0 lds\n" \
"v_mfma_f32_16x16x32_bf16 v[156:159], a[188:191], v[68:71], v[156:159]\n" \
"ds_read_b128 a[148:151], v48 offset:5120\n" \
"ds_read_b128 a[136:139], v48 offset:2048\n" \
"v_mfma_f32_16x16x32_bf16 v[188:191], a[188:191], v[72:75], v[188:191]\n" \
"ds_read_b128 a[152:155], v48 offset:6144\n" \
"ds_read_b128 a[140:143], v48 offset:3072\n" \
"v_smfmac_f32_16x16x64_bf16 a[192:195], a[200:203], v[68:75], v21\n" \
"ds_read_b128 a[156:159], v48 offset:7168\n" \
"v_add_u32_e32 v52, 0x14000, v63\n" \
"v_mfma_f32_16x16x32_bf16 v[220:223], a[188:191], v[84:87], v[220:223]\n" \
"buffer_load_dwordx4 v1, s[80:83], s92 offen offset:1024 lds\n" \
"v_mfma_f32_16x16x32_bf16 v[252:255], a[188:191], v[88:91], v[252:255]\n" \
"v_exp_f32_e32 v102, v102\n" \
"v_mfma_f32_16x16x32_bf16 v[216:219], a[184:187], v[84:87], v[216:219]\n" \
"buffer_load_dwordx4 v2, s[80:83], s92 offen offset:2048 lds\n" \
"v_mfma_f32_16x16x32_bf16 v[248:251], a[184:187], v[88:91], v[248:251]\n" \
"v_exp_f32_e32 v103, v103\n" \
"v_mfma_f32_16x16x32_bf16 v[212:215], a[180:183], v[84:87], v[212:215]\n" \
"v_add_u32_e32 v53, 0x20000, v63\n" \
"v_cvt_pk_bf16_f32 v103, v102, v103\n" \
"v_mfma_f32_16x16x32_bf16 v[244:247], a[180:183], v[88:91], v[244:247]\n" \
"v_exp_f32_e32 v100, v100\n" \
"v_mfma_f32_16x16x32_bf16 v[208:211], a[176:179], v[84:87], v[208:211]\n" \
"buffer_load_dwordx4 v3, s[80:83], s92 offen offset:3072 lds\n" \
"v_mfma_f32_16x16x32_bf16 v[240:243], a[176:179], v[88:91], v[240:243]\n" \
"v_exp_f32_e32 v101, v101\n" \
"v_mfma_f32_16x16x32_bf16 v[204:207], a[172:175], v[84:87], v[204:207]\n" \
"v_exp_f32_e32 v98, v98\n" \
"v_mfma_f32_16x16x32_bf16 v[236:239], a[172:175], v[88:91], v[236:239]\n" \
"v_exp_f32_e32 v99, v99\n" \
"v_mfma_f32_16x16x32_bf16 v[200:203], a[168:171], v[84:87], v[200:203]\n" \
"v_exp_f32_e32 v96, v96\n" \
"v_mfma_f32_16x16x32_bf16 v[232:235], a[168:171], v[88:91], v[232:235]\n" \
"v_exp_f32_e32 v97, v97\n" \
"v_mfma_f32_16x16x32_bf16 v[196:199], a[164:167], v[84:87], v[196:199]\n" \
"v_exp_f32_e32 v104, v104\n" \
"v_mfma_f32_16x16x32_bf16 v[228:231], a[164:167], v[88:91], v[228:231]\n" \
"v_exp_f32_e32 v105, v105\n" \
"v_mfma_f32_16x16x32_bf16 v[192:195], a[160:163], v[84:87], v[192:195]\n" \
"v_cvt_pk_bf16_f32 v102, v100, v101\n" \
"v_cvt_pk_bf16_f32 v101, v98, v99\n" \
"v_mfma_f32_16x16x32_bf16 v[224:227], a[160:163], v[88:91], v[224:227]\n" \
"v_cvt_pk_bf16_f32 v100, v96, v97\n" \
"v_cvt_pk_bf16_f32 v104, v104, v105\n" \
"v_smfmac_f32_16x16x64_bf16 a[196:199], a[200:203], v[84:91], v21\n" \
"v_exp_f32_e32 v106, v106\n" \
"s_waitcnt vmcnt(15) lgkmcnt(0)\n" \
"s_barrier\n" \
"v_mfma_f32_16x16x32_bf16 v[64:67], a[128:131], a[0:3], v[32:35]\n" \
"s_xor_b32 m0, s65, 0x18000\n" \
"ds_read_b128 a[160:163], v49 offset:8192\n" \
"v_mfma_f32_16x16x32_bf16 v[64:67], a[132:135], a[4:7], v[64:67]\n" \
"buffer_load_dwordx4 v52, s[8:11], s64 offen offset:0 sc1 lds\n" \
"v_mfma_f32_16x16x32_bf16 v[64:67], a[136:139], a[8:11], v[64:67]\n" \
"ds_read_b128 a[164:167], v49 offset:9216\n" \
"ds_read_b128 a[168:171], v49 offset:10240\n" \
"v_mfma_f32_16x16x32_bf16 v[64:67], a[140:143], a[12:15], v[64:67]\n" \
"ds_read_b128 a[172:175], v49 offset:11264\n" \
"ds_read_b128 a[176:179], v49 offset:12288\n" \
"v_mfma_f32_16x16x32_bf16 v[68:71], a[156:159], a[12:15], v[32:35]\n" \
"buffer_load_dwordx4 v52, s[8:11], s64 offen offset:1024 sc1 lds\n" \
"v_mfma_f32_16x16x32_bf16 v[68:71], a[152:155], a[8:11], v[68:71]\n" \
"ds_read_b128 a[180:183], v49 offset:13312\n" \
"ds_read_b128 a[184:187], v49 offset:14336\n" \
"v_mfma_f32_16x16x32_bf16 v[68:71], a[148:151], a[4:7], v[68:71]\n" \
"v_exp_f32_e32 v107, v107\n" \
"v_mfma_f32_16x16x32_bf16 v[68:71], a[144:147], a[0:3], v[68:71]\n" \
"v_exp_f32_e32 v108, v108\n" \
"v_mfma_f32_16x16x32_bf16 v[76:79], a[144:147], a[16:19], v[36:39]\n" \
"v_exp_f32_e32 v109, v109\n" \
"v_mfma_f32_16x16x32_bf16 v[76:79], a[148:151], a[20:23], v[76:79]\n" \
"buffer_load_dwordx4 v52, s[8:11], s64 offen offset:2048 sc1 lds\n" \
"v_mfma_f32_16x16x32_bf16 v[76:79], a[152:155], a[24:27], v[76:79]\n" \
"v_exp_f32_e32 v110, v110\n" \
"v_mfma_f32_16x16x32_bf16 v[76:79], a[156:159], a[28:31], v[76:79]\n" \
"v_exp_f32_e32 v111, v111\n" \
"v_mfma_f32_16x16x32_bf16 v[72:75], a[140:143], a[28:31], v[36:39]\n" \
"v_cvt_pk_bf16_f32 v105, v106, v107\n" \
"v_cvt_pk_bf16_f32 v106, v108, v109\n" \
"v_mfma_f32_16x16x32_bf16 v[72:75], a[136:139], a[24:27], v[72:75]\n" \
"buffer_load_dwordx4 v52, s[8:11], s64 offen offset:3072 sc1 lds\n" \
"v_mfma_f32_16x16x32_bf16 v[72:75], a[132:135], a[20:23], v[72:75]\n" \
"s_mov_b32 m0, s65\n" \
"v_cvt_pk_bf16_f32 v107, v110, v111\n" \
"v_mfma_f32_16x16x32_bf16 v[72:75], a[128:131], a[16:19], v[72:75]\n" \
"ds_read_b128 a[188:191], v49 offset:15360\n" \
"v_add_u32_e32 v49, 0x10000, v48\n" \
"v_mfma_f32_16x16x32_bf16 v[80:83], a[128:131], a[32:35], v[40:43]\n" \
"v_exp_f32_e32 v118, v118\n" \
"v_mfma_f32_16x16x32_bf16 v[80:83], a[132:135], a[36:39], v[80:83]\n" \
"v_exp_f32_e32 v119, v119\n" \
"v_mfma_f32_16x16x32_bf16 v[80:83], a[136:139], a[40:43], v[80:83]\n" \
"buffer_load_dwordx4 v53, s[8:11], s64 offen offset:0 sc1 lds\n" \
"v_mfma_f32_16x16x32_bf16 v[80:83], a[140:143], a[44:47], v[80:83]\n" \
"v_exp_f32_e32 v116, v116\n" \
"v_mfma_f32_16x16x32_bf16 v[84:87], a[156:159], a[44:47], v[40:43]\n" \
"v_exp_f32_e32 v117, v117\n" \
"v_mfma_f32_16x16x32_bf16 v[84:87], a[152:155], a[40:43], v[84:87]\n" \
"v_cvt_pk_bf16_f32 v119, v118, v119\n" \
"v_cvt_pk_bf16_f32 v118, v116, v117\n" \
"v_mfma_f32_16x16x32_bf16 v[84:87], a[148:151], a[36:39], v[84:87]\n" \
"v_exp_f32_e32 v114, v114\n" \
"v_mfma_f32_16x16x32_bf16 v[84:87], a[144:147], a[32:35], v[84:87]\n" \
"buffer_load_dwordx4 v53, s[8:11], s64 offen offset:1024 sc1 lds\n" \
"v_mfma_f32_16x16x32_bf16 v[92:95], a[144:147], a[48:51], v[44:47]\n" \
"v_exp_f32_e32 v115, v115\n" \
"v_mfma_f32_16x16x32_bf16 v[92:95], a[148:151], a[52:55], v[92:95]\n" \
"v_exp_f32_e32 v112, v112\n" \
"v_mfma_f32_16x16x32_bf16 v[92:95], a[152:155], a[56:59], v[92:95]\n" \
"v_exp_f32_e32 v113, v113\n" \
"v_mfma_f32_16x16x32_bf16 v[92:95], a[156:159], a[60:63], v[92:95]\n" \
"buffer_load_dwordx4 v53, s[8:11], s64 offen offset:2048 sc1 lds\n" \
"v_mfma_f32_16x16x32_bf16 v[88:91], a[140:143], a[60:63], v[44:47]\n" \
"v_cvt_pk_bf16_f32 v117, v114, v115\n" \
"v_cvt_pk_bf16_f32 v116, v112, v113\n" \
"v_mfma_f32_16x16x32_bf16 v[88:91], a[136:139], a[56:59], v[88:91]\n" \
"v_exp_f32_e32 v120, v120\n" \
"v_mfma_f32_16x16x32_bf16 v[88:91], a[132:135], a[52:55], v[88:91]\n" \
"v_exp_f32_e32 v121, v121\n" \
"v_mfma_f32_16x16x32_bf16 v[88:91], a[128:131], a[48:51], v[88:91]\n" \
"v_cvt_pk_bf16_f32 v120, v120, v121\n" \
"s_waitcnt lgkmcnt(0)\n" \
"v_mfma_f32_16x16x32_bf16 v[128:131], a[160:163], v[100:103], v[128:131]\n" \
"buffer_load_dwordx4 v53, s[8:11], s64 offen offset:3072 sc1 lds\n" \
"v_mfma_f32_16x16x32_bf16 v[160:163], a[160:163], v[104:107], v[160:163]\n" \
"s_add_u32 m0, s93, 0x1000\n" \
"ds_read_b128 a[128:131], v48 offset:8192\n" \
"v_mfma_f32_16x16x32_bf16 v[132:135], a[164:167], v[100:103], v[132:135]\n" \
"v_exp_f32_e32 v122, v122\n" \
"v_mfma_f32_16x16x32_bf16 v[164:167], a[164:167], v[104:107], v[164:167]\n" \
"v_exp_f32_e32 v123, v123\n" \
"v_mfma_f32_16x16x32_bf16 v[136:139], a[168:171], v[100:103], v[136:139]\n" \
"buffer_load_dwordx4 v4, s[80:83], s92 offen offset:0 lds\n" \
"v_mfma_f32_16x16x32_bf16 v[168:171], a[168:171], v[104:107], v[168:171]\n" \
"v_exp_f32_e32 v124, v124\n" \
"v_mfma_f32_16x16x32_bf16 v[140:143], a[172:175], v[100:103], v[140:143]\n" \
"v_exp_f32_e32 v125, v125\n" \
"v_mfma_f32_16x16x32_bf16 v[172:175], a[172:175], v[104:107], v[172:175]\n" \
"v_exp_f32_e32 v126, v126\n" \
"v_mfma_f32_16x16x32_bf16 v[144:147], a[176:179], v[100:103], v[144:147]\n" \
"v_exp_f32_e32 v127, v127\n" \
"v_mfma_f32_16x16x32_bf16 v[176:179], a[176:179], v[104:107], v[176:179]\n" \
"v_cvt_pk_bf16_f32 v121, v122, v123\n" \
"v_cvt_pk_bf16_f32 v122, v124, v125\n" \
"v_mfma_f32_16x16x32_bf16 v[148:151], a[180:183], v[100:103], v[148:151]\n" \
"buffer_load_dwordx4 v5, s[80:83], s92 offen offset:1024 lds\n" \
"v_mfma_f32_16x16x32_bf16 v[180:183], a[180:183], v[104:107], v[180:183]\n" \
"v_cvt_pk_bf16_f32 v123, v126, v127\n" \
"ds_read_b128 a[144:147], v48 offset:12288\n" \
"v_mfma_f32_16x16x32_bf16 v[152:155], a[184:187], v[100:103], v[152:155]\n" \
"ds_read_b128 a[132:135], v48 offset:9216\n" \
"ds_read_b128 a[148:151], v48 offset:13312\n" \
"v_mfma_f32_16x16x32_bf16 v[184:187], a[184:187], v[104:107], v[184:187]\n" \
"ds_read_b128 a[136:139], v48 offset:10240\n" \
"ds_read_b128 a[152:155], v48 offset:14336\n" \
"v_mfma_f32_16x16x32_bf16 v[156:159], a[188:191], v[100:103], v[156:159]\n" \
"ds_read_b128 a[140:143], v48 offset:11264\n" \
"ds_read_b128 a[156:159], v48 offset:15360\n" \
"v_mfma_f32_16x16x32_bf16 v[188:191], a[188:191], v[104:107], v[188:191]\n" \
"buffer_load_dwordx4 v6, s[80:83], s92 offen offset:2048 lds\n" \
"v_smfmac_f32_16x16x64_bf16 a[192:195], a[200:203], v[100:107], v21\n" \
"v_exp_f32_e32 v70, v70\n" \
"v_mfma_f32_16x16x32_bf16 v[220:223], a[188:191], v[116:119], v[220:223]\n" \
"v_exp_f32_e32 v71, v71\n" \
"v_mfma_f32_16x16x32_bf16 v[252:255], a[188:191], v[120:123], v[252:255]\n" \
"buffer_load_dwordx4 v7, s[80:83], s92 offen offset:3072 lds\n" \
"v_mfma_f32_16x16x32_bf16 v[216:219], a[184:187], v[116:119], v[216:219]\n" \
"s_add_u32 m0, s93, 0x2000\n" \
"v_cvt_pk_bf16_f32 v71, v70, v71\n" \
"v_mfma_f32_16x16x32_bf16 v[248:251], a[184:187], v[120:123], v[248:251]\n" \
"v_exp_f32_e32 v72, v72\n" \
"v_mfma_f32_16x16x32_bf16 v[212:215], a[180:183], v[116:119], v[212:215]\n" \
"v_exp_f32_e32 v73, v73\n" \
"v_mfma_f32_16x16x32_bf16 v[244:247], a[180:183], v[120:123], v[244:247]\n" \
"v_exp_f32_e32 v68, v68\n" \
"v_mfma_f32_16x16x32_bf16 v[208:211], a[176:179], v[116:119], v[208:211]\n" \
"buffer_load_dwordx4 v8, s[80:83], s92 offen offset:0 lds\n" \
"v_mfma_f32_16x16x32_bf16 v[240:243], a[176:179], v[120:123], v[240:243]\n" \
"v_exp_f32_e32 v69, v69\n" \
"v_mfma_f32_16x16x32_bf16 v[204:207], a[172:175], v[116:119], v[204:207]\n" \
"v_exp_f32_e32 v74, v74\n" \
"v_mfma_f32_16x16x32_bf16 v[236:239], a[172:175], v[120:123], v[236:239]\n" \
"v_exp_f32_e32 v75, v75\n" \
"v_mfma_f32_16x16x32_bf16 v[200:203], a[168:171], v[116:119], v[200:203]\n" \
"v_exp_f32_e32 v66, v66\n" \
"v_mfma_f32_16x16x32_bf16 v[232:235], a[168:171], v[120:123], v[232:235]\n" \
"v_exp_f32_e32 v67, v67\n" \
"v_mfma_f32_16x16x32_bf16 v[196:199], a[164:167], v[116:119], v[196:199]\n" \
"v_exp_f32_e32 v64, v64\n" \
"v_mfma_f32_16x16x32_bf16 v[228:231], a[164:167], v[120:123], v[228:231]\n" \
"v_exp_f32_e32 v65, v65\n" \
"v_mfma_f32_16x16x32_bf16 v[192:195], a[160:163], v[116:119], v[192:195]\n" \
"v_cvt_pk_bf16_f32 v72, v72, v73\n" \
"v_cvt_pk_bf16_f32 v70, v68, v69\n" \
"v_mfma_f32_16x16x32_bf16 v[224:227], a[160:163], v[120:123], v[224:227]\n" \
"v_exp_f32_e32 v76, v76\n" \
"v_smfmac_f32_16x16x64_bf16 a[196:199], a[200:203], v[116:123], v21\n" \
"v_exp_f32_e32 v77, v77\n" \
"s_waitcnt vmcnt(13) lgkmcnt(0)\n" \
"s_barrier\n" \
"v_exp_f32_e32 v78, v78\n" \
"v_exp_f32_e32 v79, v79\n" \
"v_cvt_pk_bf16_f32 v73, v74, v75\n" \
"v_cvt_pk_bf16_f32 v69, v66, v67\n" \
"v_cvt_pk_bf16_f32 v68, v64, v65\n" \
"v_cvt_pk_bf16_f32 v74, v76, v77\n" \
"v_cvt_pk_bf16_f32 v75, v78, v79\n" \
"v_add_u32_e32 v48, s16, v20\n" \
"s_xor_b32 s65, s65, 0x8000\n" \
"s_add_u32 s64, s64, 0x10000\n" \
"s_getpc_b64 s[86:87]\n" \
"L_hpt_%=:\n" \
"s_add_u32 s86, s86, L_ap8-L_hpt_%=\n" \
"s_addc_u32 s87, s87, 0\n" \
"s_add_u32 s86, s86, s15\n" \
"s_addc_u32 s87, s87, 0\n" \
"s_mov_b64 exec, 0\n" \
"s_swappc_b64 s[88:89], s[86:87]\n" \
"s_mov_b64 exec, -1\n" \
"s_sub_u32 s66, s19, 1\n" \
"s_cbranch_scc1 L_la_%=\n" \
".fill (L_ab + 6464 - .) / 4, 4, 0xBF800000\n" \
"L_2_%=:\n" \
"v_mfma_f32_16x16x32_bf16 v[96:99], a[128:131], a[64:67], v[32:35]\n" \
"s_xor_b32 m0, s65, 0x12000\n" \
"ds_read_b128 a[160:163], v49 offset:0\n" \
"v_mfma_f32_16x16x32_bf16 v[96:99], a[132:135], a[68:71], v[96:99]\n" \
"buffer_load_dwordx4 v50, s[8:11], s64 offen offset:0 sc1 lds\n" \
"v_mfma_f32_16x16x32_bf16 v[96:99], a[136:139], a[72:75], v[96:99]\n" \
"ds_read_b128 a[164:167], v49 offset:1024\n" \
"ds_read_b128 a[168:171], v49 offset:2048\n" \
"v_mfma_f32_16x16x32_bf16 v[96:99], a[140:143], a[76:79], v[96:99]\n" \
"ds_read_b128 a[172:175], v49 offset:3072\n" \
"ds_read_b128 a[176:179], v49 offset:4096\n" \
"v_mfma_f32_16x16x32_bf16 v[100:103], a[156:159], a[76:79], v[32:35]\n" \
"ds_read_b128 a[180:183], v49 offset:5120\n" \
"ds_read_b128 a[184:187], v49 offset:6144\n" \
"v_mfma_f32_16x16x32_bf16 v[100:103], a[152:155], a[72:75], v[100:103]\n" \
"v_exp_f32_e64 v86, v86\n" \
"v_mfma_f32_16x16x32_bf16 v[100:103], a[148:151], a[68:71], v[100:103]\n" \
"v_exp_f32_e64 v87, v87\n" \
"v_mfma_f32_16x16x32_bf16 v[100:103], a[144:147], a[64:67], v[100:103]\n" \
"buffer_load_dwordx4 v50, s[8:11], s64 offen offset:1024 sc1 lds\n" \
"v_mfma_f32_16x16x32_bf16 v[108:111], a[144:147], a[80:83], v[36:39]\n" \
"ds_read_b128 a[188:191], v49 offset:7168\n" \
"v_cvt_pk_bf16_f32 v87, v86, v87\n" \
"v_mfma_f32_16x16x32_bf16 v[108:111], a[148:151], a[84:87], v[108:111]\n" \
"v_exp_f32_e64 v84, v84\n" \
"v_mfma_f32_16x16x32_bf16 v[108:111], a[152:155], a[88:91], v[108:111]\n" \
"v_exp_f32_e64 v85, v85\n" \
"v_mfma_f32_16x16x32_bf16 v[108:111], a[156:159], a[92:95], v[108:111]\n" \
"v_exp_f32_e64 v82, v82\n" \
"v_mfma_f32_16x16x32_bf16 v[104:107], a[140:143], a[92:95], v[36:39]\n" \
"v_exp_f32_e64 v83, v83\n" \
"v_mfma_f32_16x16x32_bf16 v[104:107], a[136:139], a[88:91], v[104:107]\n" \
"v_exp_f32_e64 v80, v80\n" \
"v_mfma_f32_16x16x32_bf16 v[104:107], a[132:135], a[84:87], v[104:107]\n" \
"v_exp_f32_e64 v81, v81\n" \
"v_mfma_f32_16x16x32_bf16 v[104:107], a[128:131], a[80:83], v[104:107]\n" \
"buffer_load_dwordx4 v50, s[8:11], s64 offen offset:2048 sc1 lds\n" \
"v_mfma_f32_16x16x32_bf16 v[112:115], a[128:131], a[96:99], v[40:43]\n" \
"v_exp_f32_e64 v88, v88\n" \
"v_mfma_f32_16x16x32_bf16 v[112:115], a[132:135], a[100:103], v[112:115]\n" \
"v_exp_f32_e64 v89, v89\n" \
"v_mfma_f32_16x16x32_bf16 v[112:115], a[136:139], a[104:107], v[112:115]\n" \
"v_cvt_pk_bf16_f32 v86, v84, v85\n" \
"v_cvt_pk_bf16_f32 v85, v82, v83\n" \
"v_mfma_f32_16x16x32_bf16 v[112:115], a[140:143], a[108:111], v[112:115]\n" \
"v_cvt_pk_bf16_f32 v84, v80, v81\n" \
"v_cvt_pk_bf16_f32 v88, v88, v89\n" \
"v_mfma_f32_16x16x32_bf16 v[116:119], a[156:159], a[108:111], v[40:43]\n" \
"v_exp_f32_e64 v90, v90\n" \
"v_mfma_f32_16x16x32_bf16 v[116:119], a[152:155], a[104:107], v[116:119]\n" \
"v_exp_f32_e64 v91, v91\n" \
"v_mfma_f32_16x16x32_bf16 v[116:119], a[148:151], a[100:103], v[116:119]\n" \
"buffer_load_dwordx4 v50, s[8:11], s64 offen offset:3072 sc1 lds\n" \
"v_mfma_f32_16x16x32_bf16 v[116:119], a[144:147], a[96:99], v[116:119]\n" \
"s_xor_b32 m0, s65, 0xa000\n" \
"v_cvt_pk_bf16_f32 v89, v90, v91\n" \
"v_mfma_f32_16x16x32_bf16 v[124:127], a[144:147], a[112:115], v[44:47]\n" \
"v_exp_f32_e64 v92, v92\n" \
"v_mfma_f32_16x16x32_bf16 v[124:127], a[148:151], a[116:119], v[124:127]\n" \
"v_exp_f32_e64 v93, v93\n" \
"v_mfma_f32_16x16x32_bf16 v[124:127], a[152:155], a[120:123], v[124:127]\n" \
"v_exp_f32_e64 v94, v94\n" \
"v_mfma_f32_16x16x32_bf16 v[124:127], a[156:159], a[124:127], v[124:127]\n" \
"v_exp_f32_e64 v95, v95\n" \
"v_mfma_f32_16x16x32_bf16 v[120:123], a[140:143], a[124:127], v[44:47]\n" \
"v_cvt_pk_bf16_f32 v90, v92, v93\n" \
"v_cvt_pk_bf16_f32 v91, v94, v95\n" \
"v_mfma_f32_16x16x32_bf16 v[120:123], a[136:139], a[120:123], v[120:123]\n" \
"ds_read_b128 a[144:147], v48 offset:4096\n" \
"ds_read_b128 a[148:151], v48 offset:5120\n" \
"v_mfma_f32_16x16x32_bf16 v[120:123], a[132:135], a[116:119], v[120:123]\n" \
"buffer_load_dwordx4 v51, s[8:11], s64 offen offset:0 sc1 lds\n" \
"v_mfma_f32_16x16x32_bf16 v[120:123], a[128:131], a[112:115], v[120:123]\n" \
"ds_read_b128 a[128:131], v48 offset:0\n" \
"s_waitcnt lgkmcnt(3)\n" \
"v_mfma_f32_16x16x32_bf16 v[128:131], a[160:163], v[68:71], v[128:131]\n" \
"ds_read_b128 a[132:135], v48 offset:1024\n" \
"ds_read_b128 a[136:139], v48 offset:2048\n" \
"v_mfma_f32_16x16x32_bf16 v[160:163], a[160:163], v[72:75], v[160:163]\n" \
"ds_read_b128 a[152:155], v48 offset:6144\n" \
"ds_read_b128 a[140:143], v48 offset:3072\n" \
"v_mfma_f32_16x16x32_bf16 v[132:135], a[164:167], v[68:71], v[132:135]\n" \
"v_exp_f32_e32 v102, v102\n" \
"v_mfma_f32_16x16x32_bf16 v[164:167], a[164:167], v[72:75], v[164:167]\n" \
"v_exp_f32_e64 v103, v103\n" \
"v_mfma_f32_16x16x32_bf16 v[136:139], a[168:171], v[68:71], v[136:139]\n" \
"ds_read_b128 a[156:159], v48 offset:7168\n" \
"v_cvt_pk_bf16_f32 v103, v102, v103\n" \
"v_mfma_f32_16x16x32_bf16 v[168:171], a[168:171], v[72:75], v[168:171]\n" \
"buffer_load_dwordx4 v51, s[8:11], s64 offen offset:1024 sc1 lds\n" \
"v_mfma_f32_16x16x32_bf16 v[140:143], a[172:175], v[68:71], v[140:143]\n" \
"v_exp_f32_e64 v100, v100\n" \
"v_mfma_f32_16x16x32_bf16 v[172:175], a[172:175], v[72:75], v[172:175]\n" \
"v_exp_f32_e64 v101, v101\n" \
"v_mfma_f32_16x16x32_bf16 v[144:147], a[176:179], v[68:71], v[144:147]\n" \
"v_exp_f32_e64 v98, v98\n" \
"v_mfma_f32_16x16x32_bf16 v[176:179], a[176:179], v[72:75], v[176:179]\n" \
"v_exp_f32_e64 v99, v99\n" \
"v_mfma_f32_16x16x32_bf16 v[148:151], a[180:183], v[68:71], v[148:151]\n" \
"v_exp_f32_e64 v96, v96\n" \
"v_mfma_f32_16x16x32_bf16 v[180:183], a[180:183], v[72:75], v[180:183]\n" \
"v_exp_f32_e64 v97, v97\n" \
"v_mfma_f32_16x16x32_bf16 v[152:155], a[184:187], v[68:71], v[152:155]\n" \
"v_exp_f32_e64 v104, v104\n" \
"v_mfma_f32_16x16x32_bf16 v[184:187], a[184:187], v[72:75], v[184:187]\n" \
"buffer_load_dwordx4 v51, s[8:11], s64 offen offset:2048 sc1 lds\n" \
"v_mfma_f32_16x16x32_bf16 v[156:159], a[188:191], v[68:71], v[156:159]\n" \
"v_exp_f32_e64 v105, v105\n" \
"v_mfma_f32_16x16x32_bf16 v[188:191], a[188:191], v[72:75], v[188:191]\n" \
"v_cvt_pk_bf16_f32 v102, v100, v101\n" \
"v_cvt_pk_bf16_f32 v101, v98, v99\n" \
"v_smfmac_f32_16x16x64_bf16 a[192:195], a[200:203], v[68:75], v21\n" \
"v_cvt_pk_bf16_f32 v100, v96, v97\n" \
"v_cvt_pk_bf16_f32 v104, v104, v105\n" \
"v_mfma_f32_16x16x32_bf16 v[220:223], a[188:191], v[84:87], v[220:223]\n" \
"v_exp_f32_e64 v106, v106\n" \
"v_mfma_f32_16x16x32_bf16 v[252:255], a[188:191], v[88:91], v[252:255]\n" \
"v_exp_f32_e64 v107, v107\n" \
"v_mfma_f32_16x16x32_bf16 v[216:219], a[184:187], v[84:87], v[216:219]\n" \
"v_exp_f32_e64 v108, v108\n" \
"v_mfma_f32_16x16x32_bf16 v[248:251], a[184:187], v[88:91], v[248:251]\n" \
"buffer_load_dwordx4 v51, s[8:11], s64 offen offset:3072 sc1 lds\n" \
"v_mfma_f32_16x16x32_bf16 v[212:215], a[180:183], v[84:87], v[212:215]\n" \
"v_exp_f32_e64 v109, v109\n" \
"v_mfma_f32_16x16x32_bf16 v[244:247], a[180:183], v[88:91], v[244:247]\n" \
"v_exp_f32_e64 v110, v110\n" \
"v_mfma_f32_16x16x32_bf16 v[208:211], a[176:179], v[84:87], v[208:211]\n" \
"v_exp_f32_e64 v111, v111\n" \
"v_mfma_f32_16x16x32_bf16 v[240:243], a[176:179], v[88:91], v[240:243]\n" \
"v_exp_f32_e64 v118, v118\n" \
"v_mfma_f32_16x16x32_bf16 v[204:207], a[172:175], v[84:87], v[204:207]\n" \
"v_exp_f32_e64 v119, v119\n" \
"v_mfma_f32_16x16x32_bf16 v[236:239], a[172:175], v[88:91], v[236:239]\n" \
"v_cvt_pk_bf16_f32 v105, v106, v107\n" \
"v_cvt_pk_bf16_f32 v106, v108, v109\n" \
"v_mfma_f32_16x16x32_bf16 v[200:203], a[168:171], v[84:87], v[200:203]\n" \
"v_cvt_pk_bf16_f32 v107, v110, v111\n" \
"v_cvt_pk_bf16_f32 v119, v118, v119\n" \
"v_mfma_f32_16x16x32_bf16 v[232:235], a[168:171], v[88:91], v[232:235]\n" \
"v_exp_f32_e64 v116, v116\n" \
"v_mfma_f32_16x16x32_bf16 v[196:199], a[164:167], v[84:87], v[196:199]\n" \
"v_exp_f32_e64 v117, v117\n" \
"v_mfma_f32_16x16x32_bf16 v[228:231], a[164:167], v[88:91], v[228:231]\n" \
"v_exp_f32_e64 v114, v114\n" \
"v_mfma_f32_16x16x32_bf16 v[192:195], a[160:163], v[84:87], v[192:195]\n" \
"v_exp_f32_e64 v115, v115\n" \
"v_mfma_f32_16x16x32_bf16 v[224:227], a[160:163], v[88:91], v[224:227]\n" \
"v_exp_f32_e64 v112, v112\n" \
"v_smfmac_f32_16x16x64_bf16 a[196:199], a[200:203], v[84:91], v21\n" \
"v_exp_f32_e32 v113, v113\n" \
"s_waitcnt vmcnt(8) lgkmcnt(0)\n" \
"s_barrier\n" \
"s_nop 0\n" \
"v_mfma_f32_16x16x32_bf16 v[64:67], a[128:131], a[64:67], v[32:35]\n" \
"s_xor_b32 m0, s65, 0x18000\n" \
"ds_read_b128 a[160:163], v49 offset:8192\n" \
"v_mfma_f32_16x16x32_bf16 v[64:67], a[132:135], a[68:71], v[64:67]\n" \
"buffer_load_dwordx4 v52, s[8:11], s64 offen offset:0 sc1 lds\n" \
"v_mfma_f32_16x16x32_bf16 v[64:67], a[136:139], a[72:75], v[64:67]\n" \
"ds_read_b128 a[164:167], v49 offset:9216\n" \
"ds_read_b128 a[168:171], v49 offset:10240\n" \
"v_mfma_f32_16x16x32_bf16 v[64:67], a[140:143], a[76:79], v[64:67]\n" \
"ds_read_b128 a[172:175], v49 offset:11264\n" \
"ds_read_b128 a[176:179], v49 offset:12288\n" \
"v_mfma_f32_16x16x32_bf16 v[68:71], a[156:159], a[76:79], v[32:35]\n" \
"ds_read_b128 a[180:183], v49 offset:13312\n" \
"ds_read_b128 a[184:187], v49 offset:14336\n" \
"v_mfma_f32_16x16x32_bf16 v[68:71], a[152:155], a[72:75], v[68:71]\n" \
"ds_read_b128 a[188:191], v49 offset:15360\n" \
"v_cvt_pk_bf16_f32 v118, v116, v117\n" \
"v_mfma_f32_16x16x32_bf16 v[68:71], a[148:151], a[68:71], v[68:71]\n" \
"v_cvt_pk_bf16_f32 v117, v114, v115\n" \
"v_cvt_pk_bf16_f32 v116, v112, v113\n" \
"v_mfma_f32_16x16x32_bf16 v[68:71], a[144:147], a[64:67], v[68:71]\n" \
"buffer_load_dwordx4 v52, s[8:11], s64 offen offset:1024 sc1 lds\n" \
"v_mfma_f32_16x16x32_bf16 v[76:79], a[144:147], a[80:83], v[36:39]\n" \
"v_exp_f32_e64 v120, v120\n" \
"v_mfma_f32_16x16x32_bf16 v[76:79], a[148:151], a[84:87], v[76:79]\n" \
"v_exp_f32_e64 v121, v121\n" \
"v_mfma_f32_16x16x32_bf16 v[76:79], a[152:155], a[88:91], v[76:79]\n" \
"v_exp_f32_e64 v122, v122\n" \
"v_mfma_f32_16x16x32_bf16 v[76:79], a[156:159], a[92:95], v[76:79]\n" \
"v_exp_f32_e64 v123, v123\n" \
"v_mfma_f32_16x16x32_bf16 v[72:75], a[140:143], a[92:95], v[36:39]\n" \
"v_cvt_pk_bf16_f32 v120, v120, v121\n" \
"v_cvt_pk_bf16_f32 v121, v122, v123\n" \
"v_mfma_f32_16x16x32_bf16 v[72:75], a[136:139], a[88:91], v[72:75]\n" \
"v_exp_f32_e64 v124, v124\n" \
"v_mfma_f32_16x16x32_bf16 v[72:75], a[132:135], a[84:87], v[72:75]\n" \
"v_exp_f32_e64 v125, v125\n" \
"v_mfma_f32_16x16x32_bf16 v[72:75], a[128:131], a[80:83], v[72:75]\n" \
"buffer_load_dwordx4 v52, s[8:11], s64 offen offset:2048 sc1 lds\n" \
"v_mfma_f32_16x16x32_bf16 v[80:83], a[128:131], a[96:99], v[40:43]\n" \
"v_exp_f32_e64 v126, v126\n" \
"v_mfma_f32_16x16x32_bf16 v[80:83], a[132:135], a[100:103], v[80:83]\n" \
"v_exp_f32_e64 v127, v127\n" \
"v_mfma_f32_16x16x32_bf16 v[80:83], a[136:139], a[104:107], v[80:83]\n" \
"v_cvt_pk_bf16_f32 v122, v124, v125\n" \
"v_cvt_pk_bf16_f32 v123, v126, v127\n" \
"v_mfma_f32_16x16x32_bf16 v[80:83], a[140:143], a[108:111], v[80:83]\n" \
"v_exp_f32_e64 v70, v70\n" \
"v_mfma_f32_16x16x32_bf16 v[84:87], a[156:159], a[108:111], v[40:43]\n" \
"v_exp_f32_e64 v71, v71\n" \
"v_mfma_f32_16x16x32_bf16 v[84:87], a[152:155], a[104:107], v[84:87]\n" \
"v_exp_f32_e64 v72, v72\n" \
"v_mfma_f32_16x16x32_bf16 v[84:87], a[148:151], a[100:103], v[84:87]\n" \
"buffer_load_dwordx4 v52, s[8:11], s64 offen offset:3072 sc1 lds\n" \
"v_mfma_f32_16x16x32_bf16 v[84:87], a[144:147], a[96:99], v[84:87]\n" \
"s_mov_b32 m0, s65\n" \
"v_cvt_pk_bf16_f32 v71, v70, v71\n" \
"v_mfma_f32_16x16x32_bf16 v[92:95], a[144:147], a[112:115], v[44:47]\n" \
"ds_read_b128 a[144:147], v48 offset:12288\n" \
"v_add_u32_e32 v49, 0x10000, v48\n" \
"v_mfma_f32_16x16x32_bf16 v[92:95], a[148:151], a[116:119], v[92:95]\n" \
"ds_read_b128 a[148:151], v48 offset:13312\n" \
"s_xor_b32 s65, s65, 0x8000\n" \
"v_mfma_f32_16x16x32_bf16 v[92:95], a[152:155], a[120:123], v[92:95]\n" \
"v_exp_f32_e32 v73, v73\n" \
"v_mfma_f32_16x16x32_bf16 v[92:95], a[156:159], a[124:127], v[92:95]\n" \
"ds_read_b128 a[152:155], v48 offset:14336\n" \
".fill (L_ab + 8192 - .) / 4, 4, 0xBF800000\n" \
"L_ap2:\n" \
"s_cbranch_execz L_ap0\n" \
"ds_read_b128 a[156:159], v48 offset:15360\n" \
"v_mfma_f32_16x16x32_bf16 v[88:91], a[140:143], a[124:127], v[44:47]\n" \
"ds_read_b128 a[140:143], v48 offset:11264\n" \
"v_cvt_pk_bf16_f32 v72, v72, v73\n" \
"v_mfma_f32_16x16x32_bf16 v[88:91], a[136:139], a[120:123], v[88:91]\n" \
"v_exp_f32_e32 v68, v68\n" \
"v_mfma_f32_16x16x32_bf16 v[88:91], a[132:135], a[116:119], v[88:91]\n" \
"buffer_load_dwordx4 v53, s[8:11], s64 offen offset:0 sc1 lds\n" \
"v_mfma_f32_16x16x32_bf16 v[88:91], a[128:131], a[112:115], v[88:91]\n" \
"ds_read_b128 a[128:131], v48 offset:8192\n" \
"s_waitcnt lgkmcnt(6)\n" \
"v_mfma_f32_16x16x32_bf16 v[128:131], a[160:163], v[100:103], v[128:131]\n" \
"ds_read_b128 a[132:135], v48 offset:9216\n" \
"ds_read_b128 a[136:139], v48 offset:10240\n" \
"v_mfma_f32_16x16x32_bf16 v[160:163], a[160:163], v[104:107], v[160:163]\n" \
"v_exp_f32_e32 v69, v69\n" \
"v_mfma_f32_16x16x32_bf16 v[132:135], a[164:167], v[100:103], v[132:135]\n" \
"v_exp_f32_e64 v74, v74\n" \
"v_mfma_f32_16x16x32_bf16 v[164:167], a[164:167], v[104:107], v[164:167]\n" \
"v_exp_f32_e64 v75, v75\n" \
"v_mfma_f32_16x16x32_bf16 v[136:139], a[168:171], v[100:103], v[136:139]\n" \
"v_exp_f32_e64 v66, v66\n" \
"v_mfma_f32_16x16x32_bf16 v[168:171], a[168:171], v[104:107], v[168:171]\n" \
"buffer_load_dwordx4 v53, s[8:11], s64 offen offset:1024 sc1 lds\n" \
"v_mfma_f32_16x16x32_bf16 v[140:143], a[172:175], v[100:103], v[140:143]\n" \
"v_exp_f32_e64 v67, v67\n" \
"v_mfma_f32_16x16x32_bf16 v[172:175], a[172:175], v[104:107], v[172:175]\n" \
"v_exp_f32_e64 v64, v64\n" \
"v_mfma_f32_16x16x32_bf16 v[144:147], a[176:179], v[100:103], v[144:147]\n" \
"v_exp_f32_e64 v65, v65\n" \
"v_mfma_f32_16x16x32_bf16 v[176:179], a[176:179], v[104:107], v[176:179]\n" \
"v_exp_f32_e64 v76, v76\n" \
"v_mfma_f32_16x16x32_bf16 v[148:151], a[180:183], v[100:103], v[148:151]\n" \
"v_exp_f32_e64 v77, v77\n" \
"v_mfma_f32_16x16x32_bf16 v[180:183], a[180:183], v[104:107], v[180:183]\n" \
"v_exp_f32_e64 v78, v78\n" \
"v_mfma_f32_16x16x32_bf16 v[152:155], a[184:187], v[100:103], v[152:155]\n" \
"v_exp_f32_e64 v79, v79\n" \
"v_mfma_f32_16x16x32_bf16 v[184:187], a[184:187], v[104:107], v[184:187]\n" \
"buffer_load_dwordx4 v53, s[8:11], s64 offen offset:2048 sc1 lds\n" \
"v_mfma_f32_16x16x32_bf16 v[156:159], a[188:191], v[100:103], v[156:159]\n" \
"v_cvt_pk_bf16_f32 v70, v68, v69\n" \
"v_cvt_pk_bf16_f32 v73, v74, v75\n" \
"v_mfma_f32_16x16x32_bf16 v[188:191], a[188:191], v[104:107], v[188:191]\n" \
"v_cvt_pk_bf16_f32 v69, v66, v67\n" \
"v_cvt_pk_bf16_f32 v68, v64, v65\n" \
"v_smfmac_f32_16x16x64_bf16 a[192:195], a[200:203], v[100:107], v21\n" \
"v_cvt_pk_bf16_f32 v74, v76, v77\n" \
"v_cvt_pk_bf16_f32 v75, v78, v79\n" \
"v_mfma_f32_16x16x32_bf16 v[220:223], a[188:191], v[116:119], v[220:223]\n" \
"v_xor_b32_e32 v48, 0x8000, v48\n" \
"v_mfma_f32_16x16x32_bf16 v[252:255], a[188:191], v[120:123], v[252:255]\n" \
"v_mfma_f32_16x16x32_bf16 v[216:219], a[184:187], v[116:119], v[216:219]\n" \
"v_mfma_f32_16x16x32_bf16 v[248:251], a[184:187], v[120:123], v[248:251]\n" \
"buffer_load_dwordx4 v53, s[8:11], s64 offen offset:3072 sc1 lds\n" \
"v_mfma_f32_16x16x32_bf16 v[212:215], a[180:183], v[116:119], v[212:215]\n" \
"s_add_u32 s64, s64, 0x10000\n" \
"s_sub_u32 s66, s66, 1\n" \
"s_nop 0\n" \
"v_mfma_f32_16x16x32_bf16 v[244:247], a[180:183], v[120:123], v[244:247]\n" \
"v_mfma_f32_16x16x32_bf16 v[208:211], a[176:179], v[116:119], v[208:211]\n" \
"v_mfma_f32_16x16x32_bf16 v[240:243], a[176:179], v[120:123], v[240:243]\n" \
"v_mfma_f32_16x16x32_bf16 v[204:207], a[172:175], v[116:119], v[204:207]\n" \
"v_mfma_f32_16x16x32_bf16 v[236:239], a[172:175], v[120:123], v[236:239]\n" \
"v_mfma_f32_16x16x32_bf16 v[200:203], a[168:171], v[116:119], v[200:203]\n" \
"v_mfma_f32_16x16x32_bf16 v[232:235], a[168:171], v[120:123], v[232:235]\n" \
"v_mfma_f32_16x16x32_bf16 v[196:199], a[164:167], v[116:119], v[196:199]\n" \
"v_mfma_f32_16x16x32_bf16 v[228:231], a[164:167], v[120:123], v[228:231]\n" \
"v_mfma_f32_16x16x32_bf16 v[192:195], a[160:163], v[116:119], v[192:195]\n" \
"v_mfma_f32_16x16x32_bf16 v[224:227], a[160:163], v[120:123], v[224:227]\n" \
"v_smfmac_f32_16x16x64_bf16 a[196:199], a[200:203], v[116:123], v21\n" \
"s_waitcnt vmcnt(8) lgkmcnt(0)\n" \
"s_barrier\n" \
"s_cbranch_scc0 L_2_%=\n" \
"s_nop 0\n" \
"L_la_%=:\n" \
"v_mfma_f32_16x16x32_bf16 v[96:99], a[128:131], a[64:67], v[32:35]\n" \
"v_readlane_b32 s96, v11, 1\n" \
"v_bfe_u32 v27, v20, 2, 7\n" \
"v_mfma_f32_16x16x32_bf16 v[96:99], a[132:135], a[68:71], v[96:99]\n" \
"s_xor_b32 m0, s65, 0x12000\n" \
"ds_read_b128 a[160:163], v49 offset:0\n" \
"v_mfma_f32_16x16x32_bf16 v[96:99], a[136:139], a[72:75], v[96:99]\n" \
"buffer_load_dword v26, v27, s[56:59], s96 offen sc1\n" \
"v_mfma_f32_16x16x32_bf16 v[96:99], a[140:143], a[76:79], v[96:99]\n" \
"buffer_load_dwordx4 v50, s[8:11], s64 offen offset:0 sc1 lds\n" \
"v_mfma_f32_16x16x32_bf16 v[100:103], a[156:159], a[76:79], v[32:35]\n" \
"ds_read_b128 a[164:167], v49 offset:1024\n" \
"ds_read_b128 a[168:171], v49 offset:2048\n" \
"v_mfma_f32_16x16x32_bf16 v[100:103], a[152:155], a[72:75], v[100:103]\n" \
"ds_read_b128 a[172:175], v49 offset:3072\n" \
"ds_read_b128 a[176:179], v49 offset:4096\n" \
"v_mfma_f32_16x16x32_bf16 v[100:103], a[148:151], a[68:71], v[100:103]\n" \
"ds_read_b128 a[180:183], v49 offset:5120\n" \
"ds_read_b128 a[184:187], v49 offset:6144\n" \
"v_mfma_f32_16x16x32_bf16 v[100:103], a[144:147], a[64:67], v[100:103]\n" \
"ds_read_b128 a[188:191], v49 offset:7168\n" \
"v_readlane_b32 s97, v11, 36\n" \
"v_mfma_f32_16x16x32_bf16 v[108:111], a[144:147], a[80:83], v[36:39]\n" \
"v_exp_f32_e64 v86, v86\n" \
"v_mfma_f32_16x16x32_bf16 v[108:111], a[148:151], a[84:87], v[108:111]\n" \
"v_exp_f32_e64 v87, v87\n" \
"v_mfma_f32_16x16x32_bf16 v[108:111], a[152:155], a[88:91], v[108:111]\n" \
"buffer_load_dwordx4 v50, s[8:11], s64 offen offset:1024 sc1 lds\n" \
"v_mfma_f32_16x16x32_bf16 v[108:111], a[156:159], a[92:95], v[108:111]\n" \
"v_exp_f32_e64 v84, v84\n" \
"v_mfma_f32_16x16x32_bf16 v[104:107], a[140:143], a[92:95], v[36:39]\n" \
"v_exp_f32_e64 v85, v85\n" \
"v_mfma_f32_16x16x32_bf16 v[104:107], a[136:139], a[88:91], v[104:107]\n" \
"v_cvt_pk_bf16_f32 v87, v86, v87\n" \
"v_cvt_pk_bf16_f32 v86, v84, v85\n" \
"v_mfma_f32_16x16x32_bf16 v[104:107], a[132:135], a[84:87], v[104:107]\n" \
"v_exp_f32_e64 v82, v82\n" \
"v_mfma_f32_16x16x32_bf16 v[104:107], a[128:131], a[80:83], v[104:107]\n" \
"v_exp_f32_e64 v83, v83\n" \
"v_mfma_f32_16x16x32_bf16 v[112:115], a[128:131], a[96:99], v[40:43]\n" \
"v_exp_f32_e64 v80, v80\n" \
"v_mfma_f32_16x16x32_bf16 v[112:115], a[132:135], a[100:103], v[112:115]\n" \
"v_exp_f32_e64 v81, v81\n" \
"v_mfma_f32_16x16x32_bf16 v[112:115], a[136:139], a[104:107], v[112:115]\n" \
"buffer_load_dwordx4 v50, s[8:11], s64 offen offset:2048 sc1 lds\n" \
"v_mfma_f32_16x16x32_bf16 v[112:115], a[140:143], a[108:111], v[112:115]\n" \
"v_cvt_pk_bf16_f32 v85, v82, v83\n" \
"v_cvt_pk_bf16_f32 v84, v80, v81\n" \
"v_mfma_f32_16x16x32_bf16 v[116:119], a[156:159], a[108:111], v[40:43]\n" \
"v_exp_f32_e64 v88, v88\n" \
"v_mfma_f32_16x16x32_bf16 v[116:119], a[152:155], a[104:107], v[116:119]\n" \
"v_exp_f32_e64 v89, v89\n" \
"v_mfma_f32_16x16x32_bf16 v[116:119], a[148:151], a[100:103], v[116:119]\n" \
"v_exp_f32_e64 v90, v90\n" \
"v_mfma_f32_16x16x32_bf16 v[116:119], a[144:147], a[96:99], v[116:119]\n" \
"v_exp_f32_e64 v91, v91\n" \
"v_mfma_f32_16x16x32_bf16 v[124:127], a[144:147], a[112:115], v[44:47]\n" \
"v_cvt_pk_bf16_f32 v88, v88, v89\n" \
"v_cvt_pk_bf16_f32 v89, v90, v91\n" \
"v_mfma_f32_16x16x32_bf16 v[124:127], a[148:151], a[116:119], v[124:127]\n" \
"buffer_load_dwordx4 v50, s[8:11], s64 offen offset:3072 sc1 lds\n" \
"v_mfma_f32_16x16x32_bf16 v[124:127], a[152:155], a[120:123], v[124:127]\n" \
"v_exp_f32_e64 v92, v92\n" \
"v_mfma_f32_16x16x32_bf16 v[124:127], a[156:159], a[124:127], v[124:127]\n" \
"v_exp_f32_e64 v93, v93\n" \
"v_mfma_f32_16x16x32_bf16 v[120:123], a[140:143], a[124:127], v[44:47]\n" \
"v_exp_f32_e64 v94, v94\n" \
"v_mfma_f32_16x16x32_bf16 v[120:123], a[136:139], a[120:123], v[120:123]\n" \
"v_exp_f32_e64 v95, v95\n" \
"v_mfma_f32_16x16x32_bf16 v[120:123], a[132:135], a[116:119], v[120:123]\n" \
"v_cvt_pk_bf16_f32 v90, v92, v93\n" \
"v_cvt_pk_bf16_f32 v91, v94, v95\n" \
"v_mfma_f32_16x16x32_bf16 v[120:123], a[128:131], a[112:115], v[120:123]\n" \
"ds_read_b128 a[128:131], v48 offset:0\n" \
"s_waitcnt lgkmcnt(1)\n" \
"s_nop 0\n" \
"v_mfma_f32_16x16x32_bf16 v[128:131], a[160:163], v[68:71], v[128:131]\n" \
"ds_read_b128 a[144:147], v48 offset:4096\n" \
"ds_read_b128 a[132:135], v48 offset:1024\n" \
"v_mfma_f32_16x16x32_bf16 v[160:163], a[160:163], v[72:75], v[160:163]\n" \
"ds_read_b128 a[148:151], v48 offset:5120\n" \
"ds_read_b128 a[136:139], v48 offset:2048\n" \
"v_mfma_f32_16x16x32_bf16 v[132:135], a[164:167], v[68:71], v[132:135]\n" \
"ds_read_b128 a[152:155], v48 offset:6144\n" \
"ds_read_b128 a[140:143], v48 offset:3072\n" \
"v_mfma_f32_16x16x32_bf16 v[164:167], a[164:167], v[72:75], v[164:167]\n" \
"ds_read_b128 a[156:159], v48 offset:7168\n" \
"v_add_u32_e32 v17, 0x49000, v20\n" \
"v_mfma_f32_16x16x32_bf16 v[136:139], a[168:171], v[68:71], v[136:139]\n" \
"v_readlane_b32 s98, v11, 37\n" \
"v_readlane_b32 s93, v11, 20\n" \
"v_mfma_f32_16x16x32_bf16 v[168:171], a[168:171], v[72:75], v[168:171]\n" \
"v_readlane_b32 s84, v11, 15\n" \
"v_readlane_b32 s85, v11, 16\n" \
"v_mfma_f32_16x16x32_bf16 v[140:143], a[172:175], v[68:71], v[140:143]\n" \
"v_readlane_b32 s86, v11, 17\n" \
"s_mov_b32 s87, 0x20000\n" \
"v_mfma_f32_16x16x32_bf16 v[172:175], a[172:175], v[72:75], v[172:175]\n" \
"v_readlane_b32 s92, v11, 18\n" \
"v_readlane_b32 s91, v11, 21\n" \
"v_mfma_f32_16x16x32_bf16 v[144:147], a[176:179], v[68:71], v[144:147]\n" \
"v_exp_f32_e64 v102, v102\n" \
"v_mfma_f32_16x16x32_bf16 v[176:179], a[176:179], v[72:75], v[176:179]\n" \
"v_exp_f32_e64 v103, v103\n" \
"v_mfma_f32_16x16x32_bf16 v[148:151], a[180:183], v[68:71], v[148:151]\n" \
"v_exp_f32_e64 v100, v100\n" \
"v_mfma_f32_16x16x32_bf16 v[180:183], a[180:183], v[72:75], v[180:183]\n" \
"v_exp_f32_e64 v101, v101\n" \
"v_mfma_f32_16x16x32_bf16 v[152:155], a[184:187], v[68:71], v[152:155]\n" \
"v_cvt_pk_bf16_f32 v103, v102, v103\n" \
"v_cvt_pk_bf16_f32 v102, v100, v101\n" \
"v_mfma_f32_16x16x32_bf16 v[184:187], a[184:187], v[72:75], v[184:187]\n" \
"v_exp_f32_e64 v98, v98\n" \
"v_mfma_f32_16x16x32_bf16 v[156:159], a[188:191], v[68:71], v[156:159]\n" \
"v_exp_f32_e64 v99, v99\n" \
"v_mfma_f32_16x16x32_bf16 v[188:191], a[188:191], v[72:75], v[188:191]\n" \
"s_waitcnt vmcnt(4)\n" \
"v_cmp_ne_u32_e32 vcc, s62, v26\n" \
"v_smfmac_f32_16x16x64_bf16 a[192:195], a[200:203], v[68:75], v21\n" \
"v_exp_f32_e64 v96, v96\n" \
"v_mfma_f32_16x16x32_bf16 v[220:223], a[188:191], v[84:87], v[220:223]\n" \
"v_exp_f32_e64 v97, v97\n" \
"v_mfma_f32_16x16x32_bf16 v[252:255], a[188:191], v[88:91], v[252:255]\n" \
"s_cbranch_vccnz L_sol_%=\n" \
"L_rol_%=:\n" \
"v_cvt_pk_bf16_f32 v101, v98, v99\n" \
"buffer_load_dwordx4 a[220:223], v20, s[56:59], s97 offen offset:0 sc1\n" \
"v_mfma_f32_16x16x32_bf16 v[216:219], a[184:187], v[84:87], v[216:219]\n" \
"v_cvt_pk_bf16_f32 v100, v96, v97\n" \
"s_xor_b32 s65, s65, 0x8000\n" \
"v_mfma_f32_16x16x32_bf16 v[248:251], a[184:187], v[88:91], v[248:251]\n" \
"v_exp_f32_e32 v104, v104\n" \
"v_mfma_f32_16x16x32_bf16 v[212:215], a[180:183], v[84:87], v[212:215]\n" \
"v_exp_f32_e64 v105, v105\n" \
"v_mfma_f32_16x16x32_bf16 v[244:247], a[180:183], v[88:91], v[244:247]\n" \
"v_exp_f32_e64 v106, v106\n" \
"v_mfma_f32_16x16x32_bf16 v[208:211], a[176:179], v[84:87], v[208:211]\n" \
"v_exp_f32_e64 v107, v107\n" \
"v_mfma_f32_16x16x32_bf16 v[240:243], a[176:179], v[88:91], v[240:243]\n" \
"buffer_load_dwordx4 a[228:231], v17, s[56:59], s97 offen offset:0 sc1\n" \
"v_mfma_f32_16x16x32_bf16 v[204:207], a[172:175], v[84:87], v[204:207]\n" \
"v_cvt_pk_bf16_f32 v104, v104, v105\n" \
"v_cvt_pk_bf16_f32 v105, v106, v107\n" \
"v_mfma_f32_16x16x32_bf16 v[236:239], a[172:175], v[88:91], v[236:239]\n" \
"v_exp_f32_e64 v108, v108\n" \
"v_mfma_f32_16x16x32_bf16 v[200:203], a[168:171], v[84:87], v[200:203]\n" \
"v_exp_f32_e64 v109, v109\n" \
"v_mfma_f32_16x16x32_bf16 v[232:235], a[168:171], v[88:91], v[232:235]\n" \
"v_exp_f32_e64 v110, v110\n" \
"v_mfma_f32_16x16x32_bf16 v[196:199], a[164:167], v[84:87], v[196:199]\n" \
"v_exp_f32_e64 v111, v111\n" \
"v_mfma_f32_16x16x32_bf16 v[228:231], a[164:167], v[88:91], v[228:231]\n" \
"v_cvt_pk_bf16_f32 v106, v108, v109\n" \
"v_cvt_pk_bf16_f32 v107, v110, v111\n" \
"v_mfma_f32_16x16x32_bf16 v[192:195], a[160:163], v[84:87], v[192:195]\n" \
"v_exp_f32_e64 v118, v118\n" \
"v_mfma_f32_16x16x32_bf16 v[224:227], a[160:163], v[88:91], v[224:227]\n" \
"v_exp_f32_e64 v119, v119\n" \
"v_smfmac_f32_16x16x64_bf16 a[196:199], a[200:203], v[84:91], v21\n" \
"buffer_load_dwordx4 a[236:239], v20, s[56:59], s98 offen offset:0 sc1\n" \
"s_waitcnt vmcnt(8) lgkmcnt(0)\n" \
"s_barrier\n" \
"v_mfma_f32_16x16x32_bf16 v[64:67], a[128:131], a[64:67], v[32:35]\n" \
"s_add_u32 m0, s93, 0x0\n" \
"s_nop 0\n" \
"ds_read_b128 a[160:163], v49 offset:8192\n" \
"v_mfma_f32_16x16x32_bf16 v[64:67], a[132:135], a[68:71], v[64:67]\n" \
"buffer_load_dwordx4 v0, s[84:87], s92 offen offset:0 lds\n" \
"v_mfma_f32_16x16x32_bf16 v[64:67], a[136:139], a[72:75], v[64:67]\n" \
"ds_read_b128 a[164:167], v49 offset:9216\n" \
"ds_read_b128 a[168:171], v49 offset:10240\n" \
"v_mfma_f32_16x16x32_bf16 v[64:67], a[140:143], a[76:79], v[64:67]\n" \
"ds_read_b128 a[172:175], v49 offset:11264\n" \
"ds_read_b128 a[176:179], v49 offset:12288\n" \
"v_mfma_f32_16x16x32_bf16 v[68:71], a[156:159], a[76:79], v[32:35]\n" \
"ds_read_b128 a[180:183], v49 offset:13312\n" \
"ds_read_b128 a[184:187], v49 offset:14336\n" \
"v_mfma_f32_16x16x32_bf16 v[68:71], a[152:155], a[72:75], v[68:71]\n" \
"ds_read_b128 a[188:191], v49 offset:15360\n" \
"v_cvt_pk_bf16_f32 v119, v118, v119\n" \
"v_mfma_f32_16x16x32_bf16 v[68:71], a[148:151], a[68:71], v[68:71]\n" \
"buffer_load_dwordx4 v1, s[84:87], s92 offen offset:1024 lds\n" \
"v_mfma_f32_16x16x32_bf16 v[68:71], a[144:147], a[64:67], v[68:71]\n" \
"buffer_load_dwordx4 a[244:247], v17, s[56:59], s98 offen offset:0 sc1\n" \
"v_mfma_f32_16x16x32_bf16 v[76:79], a[144:147], a[80:83], v[36:39]\n" \
"v_exp_f32_e64 v116, v116\n" \
"v_mfma_f32_16x16x32_bf16 v[76:79], a[148:151], a[84:87], v[76:79]\n" \
"v_exp_f32_e64 v117, v117\n" \
"v_mfma_f32_16x16x32_bf16 v[76:79], a[152:155], a[88:91], v[76:79]\n" \
"v_exp_f32_e64 v114, v114\n" \
"v_mfma_f32_16x16x32_bf16 v[76:79], a[156:159], a[92:95], v[76:79]\n" \
"v_exp_f32_e64 v115, v115\n" \
"v_mfma_f32_16x16x32_bf16 v[72:75], a[140:143], a[92:95], v[36:39]\n" \
"v_exp_f32_e64 v112, v112\n" \
"v_mfma_f32_16x16x32_bf16 v[72:75], a[136:139], a[88:91], v[72:75]\n" \
"buffer_load_dwordx4 v2, s[84:87], s92 offen offset:2048 lds\n" \
"v_mfma_f32_16x16x32_bf16 v[72:75], a[132:135], a[84:87], v[72:75]\n" \
"v_exp_f32_e64 v113, v113\n" \
"v_mfma_f32_16x16x32_bf16 v[72:75], a[128:131], a[80:83], v[72:75]\n" \
"v_cvt_pk_bf16_f32 v118, v116, v117\n" \
"v_cvt_pk_bf16_f32 v117, v114, v115\n" \
"v_mfma_f32_16x16x32_bf16 v[80:83], a[128:131], a[96:99], v[40:43]\n" \
"buffer_load_dwordx4 a[224:227], v20, s[56:59], s97 offen offset:1024 sc1\n" \
"v_mfma_f32_16x16x32_bf16 v[80:83], a[132:135], a[100:103], v[80:83]\n" \
"v_cvt_pk_bf16_f32 v116, v112, v113\n" \
"v_add_u32_e32 v49, 0x10000, v48\n" \
"v_mfma_f32_16x16x32_bf16 v[80:83], a[136:139], a[104:107], v[80:83]\n" \
"v_exp_f32_e64 v120, v120\n" \
"v_mfma_f32_16x16x32_bf16 v[80:83], a[140:143], a[108:111], v[80:83]\n" \
"v_exp_f32_e64 v121, v121\n" \
"v_mfma_f32_16x16x32_bf16 v[84:87], a[156:159], a[108:111], v[40:43]\n" \
"buffer_load_dwordx4 v3, s[84:87], s92 offen offset:3072 lds\n" \
"v_mfma_f32_16x16x32_bf16 v[84:87], a[152:155], a[104:107], v[84:87]\n" \
"s_add_u32 m0, s91, 0x1000\n" \
"v_cvt_pk_bf16_f32 v120, v120, v121\n" \
"v_mfma_f32_16x16x32_bf16 v[84:87], a[148:151], a[100:103], v[84:87]\n" \
"v_exp_f32_e64 v122, v122\n" \
"v_mfma_f32_16x16x32_bf16 v[84:87], a[144:147], a[96:99], v[84:87]\n" \
"v_exp_f32_e64 v123, v123\n" \
"v_mfma_f32_16x16x32_bf16 v[92:95], a[144:147], a[112:115], v[44:47]\n" \
"v_exp_f32_e64 v124, v124\n" \
"v_mfma_f32_16x16x32_bf16 v[92:95], a[148:151], a[116:119], v[92:95]\n" \
"buffer_load_dwordx4 a[232:235], v17, s[56:59], s97 offen offset:1024 sc1\n" \
"v_mfma_f32_16x16x32_bf16 v[92:95], a[152:155], a[120:123], v[92:95]\n" \
"buffer_load_dwordx4 v4, s[84:87], s92 offen offset:0 lds\n" \
"v_mfma_f32_16x16x32_bf16 v[92:95], a[156:159], a[124:127], v[92:95]\n" \
"v_exp_f32_e64 v125, v125\n" \
"v_mfma_f32_16x16x32_bf16 v[88:91], a[140:143], a[124:127], v[44:47]\n" \
"v_exp_f32_e64 v126, v126\n" \
"v_mfma_f32_16x16x32_bf16 v[88:91], a[136:139], a[120:123], v[88:91]\n" \
"v_exp_f32_e64 v127, v127\n" \
"v_mfma_f32_16x16x32_bf16 v[88:91], a[132:135], a[116:119], v[88:91]\n" \
"v_cvt_pk_bf16_f32 v121, v122, v123\n" \
"v_cvt_pk_bf16_f32 v122, v124, v125\n" \
"v_mfma_f32_16x16x32_bf16 v[88:91], a[128:131], a[112:115], v[88:91]\n" \
"v_cvt_pk_bf16_f32 v123, v126, v127\n" \
"s_waitcnt lgkmcnt(0)\n" \
"s_nop 0\n" \
"v_mfma_f32_16x16x32_bf16 v[128:131], a[160:163], v[100:103], v[128:131]\n" \
"ds_read_b128 a[128:131], v48 offset:8192\n" \
"ds_read_b128 a[144:147], v48 offset:12288\n" \
"v_mfma_f32_16x16x32_bf16 v[160:163], a[160:163], v[104:107], v[160:163]\n" \
"buffer_load_dwordx4 v5, s[84:87], s92 offen offset:1024 lds\n" \
"v_mfma_f32_16x16x32_bf16 v[132:135], a[164:167], v[100:103], v[132:135]\n" \
"buffer_load_dwordx4 a[240:243], v20, s[56:59], s98 offen offset:1024 sc1\n" \
"v_mfma_f32_16x16x32_bf16 v[164:167], a[164:167], v[104:107], v[164:167]\n" \
"ds_read_b128 a[132:135], v48 offset:9216\n" \
"ds_read_b128 a[148:151], v48 offset:13312\n" \
"v_mfma_f32_16x16x32_bf16 v[136:139], a[168:171], v[100:103], v[136:139]\n" \
"ds_read_b128 a[136:139], v48 offset:10240\n" \
"ds_read_b128 a[152:155], v48 offset:14336\n" \
"v_mfma_f32_16x16x32_bf16 v[168:171], a[168:171], v[104:107], v[168:171]\n" \
"ds_read_b128 a[140:143], v48 offset:11264\n" \
"ds_read_b128 a[156:159], v48 offset:15360\n" \
"v_mfma_f32_16x16x32_bf16 v[140:143], a[172:175], v[100:103], v[140:143]\n" \
"v_exp_f32_e64 v70, v70\n" \
"v_mfma_f32_16x16x32_bf16 v[172:175], a[172:175], v[104:107], v[172:175]\n" \
"v_exp_f32_e64 v71, v71\n" \
"v_mfma_f32_16x16x32_bf16 v[144:147], a[176:179], v[100:103], v[144:147]\n" \
"buffer_load_dwordx4 v6, s[84:87], s92 offen offset:2048 lds\n" \
"v_mfma_f32_16x16x32_bf16 v[176:179], a[176:179], v[104:107], v[176:179]\n" \
"v_exp_f32_e64 v72, v72\n" \
"v_mfma_f32_16x16x32_bf16 v[148:151], a[180:183], v[100:103], v[148:151]\n" \
"v_exp_f32_e64 v73, v73\n" \
"v_mfma_f32_16x16x32_bf16 v[180:183], a[180:183], v[104:107], v[180:183]\n" \
"buffer_load_dwordx4 a[248:251], v17, s[56:59], s98 offen offset:1024 sc1\n" \
"v_mfma_f32_16x16x32_bf16 v[152:155], a[184:187], v[100:103], v[152:155]\n" \
"v_exp_f32_e64 v68, v68\n" \
"v_mfma_f32_16x16x32_bf16 v[184:187], a[184:187], v[104:107], v[184:187]\n" \
"v_exp_f32_e64 v69, v69\n" \
"v_mfma_f32_16x16x32_bf16 v[156:159], a[188:191], v[100:103], v[156:159]\n" \
"buffer_load_dwordx4 v7, s[84:87], s92 offen offset:3072 lds\n" \
"v_mfma_f32_16x16x32_bf16 v[188:191], a[188:191], v[104:107], v[188:191]\n" \
"s_add_u32 m0, s91, 0x2000\n" \
"v_cvt_pk_bf16_f32 v71, v70, v71\n" \
"v_smfmac_f32_16x16x64_bf16 a[192:195], a[200:203], v[100:107], v21\n" \
"v_exp_f32_e64 v74, v74\n" \
"v_mfma_f32_16x16x32_bf16 v[220:223], a[188:191], v[116:119], v[220:223]\n" \
"v_exp_f32_e64 v75, v75\n" \
"v_mfma_f32_16x16x32_bf16 v[252:255], a[188:191], v[120:123], v[252:255]\n" \
"v_exp_f32_e64 v66, v66\n" \
"v_mfma_f32_16x16x32_bf16 v[216:219], a[184:187], v[116:119], v[216:219]\n" \
"v_exp_f32_e64 v67, v67\n" \
"v_mfma_f32_16x16x32_bf16 v[248:251], a[184:187], v[120:123], v[248:251]\n" \
"v_exp_f32_e64 v64, v64\n" \
"v_mfma_f32_16x16x32_bf16 v[212:215], a[180:183], v[116:119], v[212:215]\n" \
"buffer_load_dwordx4 v8, s[84:87], s92 offen offset:0 lds\n" \
"v_mfma_f32_16x16x32_bf16 v[244:247], a[180:183], v[120:123], v[244:247]\n" \
"v_exp_f32_e64 v65, v65\n" \
"v_mfma_f32_16x16x32_bf16 v[208:211], a[176:179], v[116:119], v[208:211]\n" \
"v_cvt_pk_bf16_f32 v72, v72, v73\n" \
"v_cvt_pk_bf16_f32 v70, v68, v69\n" \
"v_mfma_f32_16x16x32_bf16 v[240:243], a[176:179], v[120:123], v[240:243]\n" \
"v_exp_f32_e64 v76, v76\n" \
"v_mfma_f32_16x16x32_bf16 v[204:207], a[172:175], v[116:119], v[204:207]\n" \
"v_exp_f32_e64 v77, v77\n" \
"v_mfma_f32_16x16x32_bf16 v[236:239], a[172:175], v[120:123], v[236:239]\n" \
"v_exp_f32_e64 v78, v78\n" \
"v_mfma_f32_16x16x32_bf16 v[200:203], a[168:171], v[116:119], v[200:203]\n" \
"v_exp_f32_e64 v79, v79\n" \
"v_mfma_f32_16x16x32_bf16 v[232:235], a[168:171], v[120:123], v[232:235]\n" \
"v_cvt_pk_bf16_f32 v73, v74, v75\n" \
"v_cvt_pk_bf16_f32 v69, v66, v67\n" \
"v_mfma_f32_16x16x32_bf16 v[196:199], a[164:167], v[116:119], v[196:199]\n" \
"v_cvt_pk_bf16_f32 v68, v64, v65\n" \
"v_cvt_pk_bf16_f32 v74, v76, v77\n" \
"v_mfma_f32_16x16x32_bf16 v[228:231], a[164:167], v[120:123], v[228:231]\n" \
"v_cvt_pk_bf16_f32 v75, v78, v79\n" \
"v_xor_b32_e32 v48, 0x8000, v48\n" \
"v_mfma_f32_16x16x32_bf16 v[192:195], a[160:163], v[116:119], v[192:195]\n" \
"s_add_u32 s64, s64, 0x10000\n" \
"v_mfma_f32_16x16x32_bf16 v[224:227], a[160:163], v[120:123], v[224:227]\n" \
"v_smfmac_f32_16x16x64_bf16 a[196:199], a[200:203], v[116:123], v21\n" \
"s_waitcnt vmcnt(14) lgkmcnt(0)\n" \
"s_barrier\n" \
"s_cmp_lg_u32 s16, 0\n" \
"s_cbranch_scc1 L_f1_%=\n" \
"ds_read_b128 a[160:163], v49 offset:0\n" \
"ds_read_b128 a[164:167], v49 offset:1024\n" \
"ds_read_b128 a[168:171], v49 offset:2048\n" \
"ds_read_b128 a[172:175], v49 offset:3072\n" \
"ds_read_b128 a[176:179], v49 offset:4096\n" \
"ds_read_b128 a[180:183], v49 offset:5120\n" \
"ds_read_b128 a[184:187], v49 offset:6144\n" \
"ds_read_b128 a[188:191], v49 offset:7168\n" \
"v_add_u32_e32 v54, 0x49000, v20\n" \
"buffer_load_dwordx4 a[96:99], v20, s[56:59], s60 offen offset:0 sc1\n" \
"v_add_u32_e32 v50, s15, v20\n" \
"v_add_u32_e32 v51, 0x10000, v50\n" \
"ds_read_b32 v60, v18 offset:0\n" \
"ds_read_b32 v61, v18 offset:128\n" \
"ds_read_b128 a[0:3], v49 offset:8192\n" \
"ds_read_b128 a[4:7], v49 offset:9216\n" \
"ds_read_b128 a[8:11], v49 offset:10240\n" \
"ds_read_b128 a[12:15], v49 offset:11264\n" \
"ds_read_b128 a[16:19], v49 offset:12288\n" \
"ds_read_b128 a[20:23], v49 offset:13312\n" \
"ds_read_b128 a[24:27], v49 offset:14336\n" \
"ds_read_b128 a[28:31], v49 offset:15360\n" \
"v_exp_f32_e32 v86, v86\n" \
"v_exp_f32_e32 v87, v87\n" \
"v_exp_f32_e32 v84, v84\n" \
"v_exp_f32_e32 v85, v85\n" \
"v_cvt_pk_bf16_f32 v87, v86, v87\n" \
"buffer_load_dwordx4 a[100:103], v20, s[56:59], s60 offen offset:1024 sc1\n" \
"v_exp_f32_e32 v82, v82\n" \
"v_exp_f32_e32 v83, v83\n" \
"v_exp_f32_e32 v80, v80\n" \
"v_exp_f32_e32 v81, v81\n" \
"s_waitcnt lgkmcnt(15)\n" \
"v_mfma_f32_16x16x32_bf16 v[128:131], a[160:163], v[68:71], v[128:131]\n" \
"v_cvt_pk_bf16_f32 v86, v84, v85\n" \
"v_cvt_pk_bf16_f32 v85, v82, v83\n" \
"v_mfma_f32_16x16x32_bf16 v[160:163], a[160:163], v[72:75], v[160:163]\n" \
"v_cvt_pk_bf16_f32 v84, v80, v81\n" \
"v_and_b32_e32 v84, v12, v84\n" \
"v_mfma_f32_16x16x32_bf16 v[132:135], a[164:167], v[68:71], v[132:135]\n" \
"v_and_b32_e32 v85, v13, v85\n" \
"v_and_b32_e32 v86, v14, v86\n" \
"v_mfma_f32_16x16x32_bf16 v[164:167], a[164:167], v[72:75], v[164:167]\n" \
"buffer_load_dwordx4 a[104:107], v20, s[56:59], s60 offen offset:2048 sc1\n" \
"v_mfma_f32_16x16x32_bf16 v[136:139], a[168:171], v[68:71], v[136:139]\n" \
"v_cvt_pk_bf16_f32 v128, v128, v129\n" \
"v_cvt_pk_bf16_f32 v160, v160, v161\n" \
"v_mfma_f32_16x16x32_bf16 v[168:171], a[168:171], v[72:75], v[168:171]\n" \
"v_cvt_pk_bf16_f32 v129, v130, v131\n" \
"s_waitcnt lgkmcnt(10)\n" \
"v_mfma_f32_16x16x32_bf16 v[140:143], a[172:175], v[68:71], v[140:143]\n" \
"v_cvt_pk_bf16_f32 v161, v162, v163\n" \
"v_cvt_pk_bf16_f32 v130, v132, v133\n" \
"v_mfma_f32_16x16x32_bf16 v[172:175], a[172:175], v[72:75], v[172:175]\n" \
"v_cvt_pk_bf16_f32 v131, v134, v135\n" \
"v_cvt_pk_bf16_f32 v162, v164, v165\n" \
"v_mfma_f32_16x16x32_bf16 v[144:147], a[176:179], v[68:71], v[144:147]\n" \
"v_cvt_pk_bf16_f32 v163, v166, v167\n" \
"v_cvt_pk_bf16_f32 v132, v136, v137\n" \
"v_mfma_f32_16x16x32_bf16 v[176:179], a[176:179], v[72:75], v[176:179]\n" \
"buffer_load_dwordx4 a[108:111], v20, s[56:59], s60 offen offset:3072 sc1\n" \
"v_mfma_f32_16x16x32_bf16 v[148:151], a[180:183], v[68:71], v[148:151]\n" \
"v_cvt_pk_bf16_f32 v133, v138, v139\n" \
"v_cvt_pk_bf16_f32 v134, v140, v141\n" \
"v_mfma_f32_16x16x32_bf16 v[180:183], a[180:183], v[72:75], v[180:183]\n" \
"v_cvt_pk_bf16_f32 v135, v142, v143\n" \
"v_cvt_pk_bf16_f32 v164, v168, v169\n" \
"v_mfma_f32_16x16x32_bf16 v[152:155], a[184:187], v[68:71], v[152:155]\n" \
"v_cvt_pk_bf16_f32 v165, v170, v171\n" \
"v_cvt_pk_bf16_f32 v166, v172, v173\n" \
"v_mfma_f32_16x16x32_bf16 v[184:187], a[184:187], v[72:75], v[184:187]\n" \
"v_cvt_pk_bf16_f32 v167, v174, v175\n" \
"v_cvt_pk_bf16_f32 v136, v144, v145\n" \
"v_mfma_f32_16x16x32_bf16 v[156:159], a[188:191], v[68:71], v[156:159]\n" \
"v_cvt_pk_bf16_f32 v137, v146, v147\n" \
"v_cvt_pk_bf16_f32 v138, v148, v149\n" \
"v_mfma_f32_16x16x32_bf16 v[188:191], a[188:191], v[72:75], v[188:191]\n" \
"v_cvt_pk_bf16_f32 v139, v150, v151\n" \
"v_cvt_pk_bf16_f32 v140, v152, v153\n" \
"v_smfmac_f32_16x16x64_bf16 a[192:195], a[200:203], v[68:75], v21\n" \
"v_cvt_pk_bf16_f32 v141, v154, v155\n" \
"v_cvt_pk_bf16_f32 v168, v176, v177\n" \
"v_mfma_f32_16x16x32_bf16 v[120:123], a[128:131], a[112:115], v[44:47]\n" \
"v_cvt_pk_bf16_f32 v142, v156, v157\n" \
"v_cvt_pk_bf16_f32 v143, v158, v159\n" \
"v_mfma_f32_16x16x32_bf16 v[120:123], a[132:135], a[116:119], v[120:123]\n" \
"v_cvt_pk_bf16_f32 v169, v178, v179\n" \
"v_cvt_pk_bf16_f32 v170, v180, v181\n" \
"v_cvt_pk_bf16_f32 v171, v182, v183\n" \
"v_cvt_pk_bf16_f32 v172, v184, v185\n" \
"v_cvt_pk_bf16_f32 v173, v186, v187\n" \
"v_cvt_pk_bf16_f32 v174, v188, v189\n" \
"v_cvt_pk_bf16_f32 v175, v190, v191\n" \
"ds_write_b128 v50, v[128:131] offset:0\n" \
"v_mfma_f32_16x16x32_bf16 v[120:123], a[136:139], a[120:123], v[120:123]\n" \
"v_and_b32_e32 v87, v15, v87\n" \
"v_accvgpr_read_b32 v40, a192\n" \
"v_mfma_f32_16x16x32_bf16 v[120:123], a[140:143], a[124:127], v[120:123]\n" \
"ds_write_b128 v50, v[132:135] offset:1024\n" \
"v_mfma_f32_16x16x32_bf16 v[124:127], a[156:159], a[124:127], v[44:47]\n" \
"buffer_load_dwordx4 a[124:127], v54, s[56:59], s60 offen offset:3072 sc1\n" \
"v_mfma_f32_16x16x32_bf16 v[124:127], a[152:155], a[120:123], v[124:127]\n" \
"ds_write_b128 v50, v[136:139] offset:2048\n" \
"v_mfma_f32_16x16x32_bf16 v[124:127], a[148:151], a[116:119], v[124:127]\n" \
"v_accvgpr_read_b32 v22, a193\n" \
"v_accvgpr_read_b32 v41, a194\n" \
"v_mfma_f32_16x16x32_bf16 v[124:127], a[144:147], a[112:115], v[124:127]\n" \
"ds_write_b128 v50, v[140:143] offset:3072\n" \
"v_mfma_f32_16x16x32_bf16 a[196:199], a[204:207], v[84:87], a[196:199]\n" \
"v_accvgpr_read_b32 v23, a195\n" \
"v_add_f32_e32 v40, v40, v22\n" \
"v_mfma_f32_16x16x32_bf16 v[192:195], a[160:163], v[84:87], v[192:195]\n" \
"ds_write_b128 v51, v[160:163] offset:32768\n" \
"v_mfma_f32_16x16x32_bf16 v[196:199], a[164:167], v[84:87], v[196:199]\n" \
"buffer_load_dwordx4 a[112:115], v54, s[56:59], s60 offen offset:0 sc1\n" \
"v_mfma_f32_16x16x32_bf16 v[200:203], a[168:171], v[84:87], v[200:203]\n" \
"ds_write_b128 v51, v[164:167] offset:33792\n" \
"v_add_f32_e32 v41, v41, v23\n" \
"ds_write_b64 v19, v[40:41]\n" \
"v_mfma_f32_16x16x32_bf16 v[204:207], a[172:175], v[84:87], v[204:207]\n" \
"v_lshl_add_u32 v52, s15, 2, v20\n" \
"v_add_u32_e32 v52, 0x1a000, v52\n" \
"ds_write_b128 v51, v[168:171] offset:34816\n" \
"v_mfma_f32_16x16x32_bf16 v[208:211], a[176:179], v[84:87], v[208:211]\n" \
"v_exp_f32_e32 v88, v88\n" \
"v_mfma_f32_16x16x32_bf16 v[212:215], a[180:183], v[84:87], v[212:215]\n" \
"ds_write_b128 v51, v[172:175] offset:35840\n" \
"v_mfma_f32_16x16x32_bf16 v[216:219], a[184:187], v[84:87], v[216:219]\n" \
"buffer_load_dwordx4 a[116:119], v54, s[56:59], s60 offen offset:1024 sc1\n" \
"v_mfma_f32_16x16x32_bf16 v[220:223], a[188:191], v[84:87], v[220:223]\n" \
"v_exp_f32_e32 v89, v89\n" \
"v_exp_f32_e32 v90, v90\n" \
"v_exp_f32_e32 v91, v91\n" \
"v_cvt_pk_bf16_f32 v88, v88, v89\n" \
"v_exp_f32_e32 v92, v92\n" \
"v_exp_f32_e32 v93, v93\n" \
"v_exp_f32_e32 v94, v94\n" \
"v_exp_f32_e32 v95, v95\n" \
"v_cvt_pk_bf16_f32 v89, v90, v91\n" \
"v_cvt_pk_bf16_f32 v90, v92, v93\n" \
"v_cvt_pk_bf16_f32 v91, v94, v95\n" \
"v_xor_b32_e32 v63, 0x400, v19\n" \
"buffer_load_dwordx4 a[120:123], v54, s[56:59], s60 offen offset:2048 sc1\n" \
"v_exp_f32_e32 v120, v120\n" \
"v_exp_f32_e32 v121, v121\n" \
"v_exp_f32_e32 v122, v122\n" \
"v_exp_f32_e32 v123, v123\n" \
".fill (L_ab + 12288 - .) / 4, 4, 0xBF800000\n" \
"L_ap3:\n" \
"s_cbranch_execz L_ap0\n" \
"v_cvt_pk_bf16_f32 v120, v120, v121\n" \
"v_exp_f32_e32 v124, v124\n" \
"v_exp_f32_e32 v125, v125\n" \
"s_waitcnt lgkmcnt(0)\n" \
"s_barrier\n" \
"ds_read_b128 v[144:147], v52 offset:0\n" \
"ds_read_b128 v[148:151], v52 offset:1024\n" \
"ds_read_b128 v[152:155], v52 offset:2048\n" \
"ds_read_b128 v[156:159], v52 offset:3072\n" \
"ds_read_b128 v[176:179], v52 offset:4096\n" \
"ds_read_b128 v[180:183], v52 offset:5120\n" \
"ds_read_b128 v[184:187], v52 offset:6144\n" \
"ds_read_b128 v[188:191], v52 offset:7168\n" \
"v_mfma_f32_16x16x32_bf16 a[196:199], a[212:215], v[88:91], a[196:199]\n" \
"ds_read_b64 v[42:43], v63\n" \
"v_cvt_pk_bf16_f32 v121, v122, v123\n" \
"v_mfma_f32_16x16x32_bf16 v[224:227], a[160:163], v[88:91], v[224:227]\n" \
"v_exp_f32_e32 v126, v126\n" \
"v_mfma_f32_16x16x32_bf16 v[228:231], a[164:167], v[88:91], v[228:231]\n" \
"v_exp_f32_e32 v127, v127\n" \
"v_mfma_f32_16x16x32_bf16 v[232:235], a[168:171], v[88:91], v[232:235]\n" \
"v_cvt_pk_bf16_f32 v122, v124, v125\n" \
"v_cvt_pk_bf16_f32 v123, v126, v127\n" \
"v_mfma_f32_16x16x32_bf16 v[236:239], a[172:175], v[88:91], v[236:239]\n" \
"v_and_b32_e32 v120, v12, v120\n" \
"v_and_b32_e32 v121, v13, v121\n" \
"v_mfma_f32_16x16x32_bf16 v[240:243], a[176:179], v[88:91], v[240:243]\n" \
"v_and_b32_e32 v122, v14, v122\n" \
"v_and_b32_e32 v123, v15, v123\n" \
"v_mfma_f32_16x16x32_bf16 v[244:247], a[180:183], v[88:91], v[244:247]\n" \
"v_readlane_b32 s93, v11, 22\n" \
"v_readlane_b32 s92, v11, 27\n" \
"v_mfma_f32_16x16x32_bf16 v[248:251], a[184:187], v[88:91], v[248:251]\n" \
"s_add_u32 m0, s93, 0x0\n" \
"v_mov_b32_e32 v130, v28\n" \
"v_mfma_f32_16x16x32_bf16 v[252:255], a[188:191], v[88:91], v[252:255]\n" \
"v_mov_b32_e32 v131, v29\n" \
"v_mov_b32_e32 v132, 0\n" \
"v_mov_b32_e32 v133, 0\n" \
"v_mov_b32_e32 v128, 0\n" \
"v_mov_b32_e32 v129, 0\n" \
"s_waitcnt vmcnt(8) lgkmcnt(1)\n" \
"s_barrier\n" \
"v_mfma_f32_16x16x32_bf16 a[196:199], a[212:215], v[120:123], a[196:199]\n" \
"buffer_load_dwordx4 v0, s[80:83], s92 offen offset:0 lds\n" \
"v_mfma_f32_16x16x32_bf16 v[224:227], a[0:3], v[120:123], v[224:227]\n" \
"v_mfma_f32_16x16x32_bf16 v[228:231], a[4:7], v[120:123], v[228:231]\n" \
"v_mfma_f32_16x16x32_bf16 v[232:235], a[8:11], v[120:123], v[232:235]\n" \
"v_mfma_f32_16x16x32_bf16 v[236:239], a[12:15], v[120:123], v[236:239]\n" \
"v_mfma_f32_16x16x32_bf16 v[240:243], a[16:19], v[120:123], v[240:243]\n" \
"v_mfma_f32_16x16x32_bf16 v[244:247], a[20:23], v[120:123], v[244:247]\n" \
"buffer_load_dwordx4 v1, s[80:83], s92 offen offset:1024 lds\n" \
"v_mfma_f32_16x16x32_bf16 v[248:251], a[24:27], v[120:123], v[248:251]\n" \
"v_accvgpr_read_b32 v56, a196\n" \
"v_accvgpr_read_b32 v24, a197\n" \
"v_mfma_f32_16x16x32_bf16 v[252:255], a[28:31], v[120:123], v[252:255]\n" \
"v_add_f32_e32 v56, v56, v24\n" \
"s_waitcnt lgkmcnt(0)\n" \
"v_add_f32_e32 v56, v56, v42\n" \
"v_mfma_f32_16x16x32_bf16 v[192:195], v[130:133], v[144:147], v[192:195]\n" \
"v_max_f32_e32 v62, v62, v56\n" \
"v_accvgpr_read_b32 v57, a198\n" \
"v_mfma_f32_16x16x32_bf16 v[196:199], v[128:131], v[144:147], v[196:199]\n" \
"v_rcp_f32_e32 v56, v56\n" \
"v_mfma_f32_16x16x32_bf16 v[200:203], v[130:133], v[148:151], v[200:203]\n" \
"v_mul_f32_e32 v56, v56, v60\n" \
"v_accvgpr_read_b32 v25, a199\n" \
"buffer_load_dwordx4 v2, s[80:83], s92 offen offset:2048 lds\n" \
"v_mfma_f32_16x16x32_bf16 v[204:207], v[128:131], v[148:151], v[204:207]\n" \
"v_mul_f32_e32 v192, v56, v192\n" \
"v_mul_f32_e32 v193, v56, v193\n" \
"v_mfma_f32_16x16x32_bf16 v[208:211], v[130:133], v[152:155], v[208:211]\n" \
"v_mul_f32_e32 v194, v56, v194\n" \
"v_mul_f32_e32 v195, v56, v195\n" \
"v_mfma_f32_16x16x32_bf16 v[212:215], v[128:131], v[152:155], v[212:215]\n" \
"v_mul_f32_e32 v196, v56, v196\n" \
"v_mul_f32_e32 v197, v56, v197\n" \
"v_mfma_f32_16x16x32_bf16 v[216:219], v[130:133], v[156:159], v[216:219]\n" \
"v_mul_f32_e32 v198, v56, v198\n" \
"v_mul_f32_e32 v199, v56, v199\n" \
"v_mfma_f32_16x16x32_bf16 v[220:223], v[128:131], v[156:159], v[220:223]\n" \
"v_cvt_pk_bf16_f32 v64, v192, v193\n" \
"v_cvt_pk_bf16_f32 v65, v194, v195\n" \
"v_mfma_f32_16x16x32_bf16 v[224:227], v[130:133], v[176:179], v[224:227]\n" \
"buffer_load_dwordx4 v3, s[80:83], s92 offen offset:3072 lds\n" \
"s_add_u32 m0, s93, 0x1000\n" \
"v_mfma_f32_16x16x32_bf16 v[228:231], v[128:131], v[176:179], v[228:231]\n" \
"v_cvt_pk_bf16_f32 v66, v196, v197\n" \
"v_cvt_pk_bf16_f32 v67, v198, v199\n" \
"v_mfma_f32_16x16x32_bf16 v[232:235], v[130:133], v[180:183], v[232:235]\n" \
"v_mul_f32_e32 v200, v56, v200\n" \
"v_mul_f32_e32 v201, v56, v201\n" \
"v_mfma_f32_16x16x32_bf16 v[236:239], v[128:131], v[180:183], v[236:239]\n" \
"v_mfma_f32_16x16x32_bf16 a[208:211], a[216:219], v[64:67], a[208:211]\n" \
"v_mul_f32_e32 v202, v56, v202\n" \
"v_mul_f32_e32 v203, v56, v203\n" \
"v_mfma_f32_16x16x32_bf16 v[240:243], v[130:133], v[184:187], v[240:243]\n" \
"v_mul_f32_e32 v204, v56, v204\n" \
"v_mul_f32_e32 v205, v56, v205\n" \
"buffer_load_dwordx4 v4, s[80:83], s92 offen offset:0 lds\n" \
"v_mul_f32_e32 v206, v56, v206\n" \
"v_mul_f32_e32 v207, v56, v207\n" \
"v_add_f32_e32 v57, v57, v25\n" \
"v_cvt_pk_bf16_f32 v68, v200, v201\n" \
"v_cvt_pk_bf16_f32 v69, v202, v203\n" \
"v_cvt_pk_bf16_f32 v70, v204, v205\n" \
"v_cvt_pk_bf16_f32 v71, v206, v207\n" \
"v_add_f32_e32 v57, v57, v43\n" \
"v_mul_f32_e32 v208, v56, v208\n" \
"v_mfma_f32_16x16x32_bf16 a[208:211], a[216:219], v[68:71], a[208:211]\n" \
"v_mul_f32_e32 v209, v56, v209\n" \
"v_mul_f32_e32 v210, v56, v210\n" \
"v_mul_f32_e32 v211, v56, v211\n" \
"v_mul_f32_e32 v212, v56, v212\n" \
"v_mul_f32_e32 v213, v56, v213\n" \
"v_mul_f32_e32 v214, v56, v214\n" \
"v_mul_f32_e32 v215, v56, v215\n" \
"v_max_f32_e32 v62, v62, v57\n" \
"v_cvt_pk_bf16_f32 v72, v208, v209\n" \
"v_cvt_pk_bf16_f32 v73, v210, v211\n" \
"buffer_load_dwordx4 v5, s[80:83], s92 offen offset:1024 lds\n" \
"v_cvt_pk_bf16_f32 v74, v212, v213\n" \
"v_cvt_pk_bf16_f32 v75, v214, v215\n" \
"v_rcp_f32_e32 v57, v57\n" \
"v_mfma_f32_16x16x32_bf16 v[244:247], v[128:131], v[184:187], v[244:247]\n" \
"v_mfma_f32_16x16x32_bf16 a[208:211], a[216:219], v[72:75], a[208:211]\n" \
"v_mul_f32_e32 v216, v56, v216\n" \
"v_mul_f32_e32 v217, v56, v217\n" \
"v_mul_f32_e32 v218, v56, v218\n" \
"v_mul_f32_e32 v219, v56, v219\n" \
"v_mul_f32_e32 v220, v56, v220\n" \
"v_mul_f32_e32 v221, v56, v221\n" \
"v_mul_f32_e32 v222, v56, v222\n" \
"v_mul_f32_e32 v223, v56, v223\n" \
"v_cvt_pk_bf16_f32 v76, v216, v217\n" \
"v_cvt_pk_bf16_f32 v77, v218, v219\n" \
"v_cvt_pk_bf16_f32 v78, v220, v221\n" \
"buffer_load_dwordx4 v6, s[80:83], s92 offen offset:2048 lds\n" \
"v_cvt_pk_bf16_f32 v79, v222, v223\n" \
"v_mul_f32_e32 v57, v57, v61\n" \
"v_mul_f32_e32 v224, v57, v224\n" \
"v_mfma_f32_16x16x32_bf16 a[208:211], a[216:219], v[76:79], a[208:211]\n" \
"v_mul_f32_e32 v225, v57, v225\n" \
"v_mul_f32_e32 v226, v57, v226\n" \
"v_mul_f32_e32 v227, v57, v227\n" \
"v_mul_f32_e32 v228, v57, v228\n" \
"v_mul_f32_e32 v229, v57, v229\n" \
"v_mul_f32_e32 v230, v57, v230\n" \
"v_mul_f32_e32 v231, v57, v231\n" \
"v_cvt_pk_bf16_f32 v80, v224, v225\n" \
"v_cvt_pk_bf16_f32 v81, v226, v227\n" \
"v_cvt_pk_bf16_f32 v82, v228, v229\n" \
"v_cvt_pk_bf16_f32 v83, v230, v231\n" \
"v_mfma_f32_16x16x32_bf16 v[248:251], v[130:133], v[188:191], v[248:251]\n" \
"v_mul_f32_e32 v232, v57, v232\n" \
"v_mul_f32_e32 v233, v57, v233\n" \
"v_mfma_f32_16x16x32_bf16 a[208:211], a[216:219], v[80:83], a[208:211]\n" \
"buffer_load_dwordx4 v7, s[80:83], s92 offen offset:3072 lds\n" \
"s_add_u32 m0, s93, 0x2000\n" \
"v_mul_f32_e32 v234, v57, v234\n" \
"v_mul_f32_e32 v235, v57, v235\n" \
"v_mul_f32_e32 v236, v57, v236\n" \
"v_mul_f32_e32 v237, v57, v237\n" \
"v_mul_f32_e32 v238, v57, v238\n" \
"v_mul_f32_e32 v239, v57, v239\n" \
"v_cvt_pk_bf16_f32 v84, v232, v233\n" \
"v_cvt_pk_bf16_f32 v85, v234, v235\n" \
"v_cvt_pk_bf16_f32 v86, v236, v237\n" \
"v_cvt_pk_bf16_f32 v87, v238, v239\n" \
"v_mul_f32_e32 v240, v57, v240\n" \
"v_mul_f32_e32 v241, v57, v241\n" \
"v_mfma_f32_16x16x32_bf16 a[208:211], a[216:219], v[84:87], a[208:211]\n" \
"v_mul_f32_e32 v242, v57, v242\n" \
"v_mul_f32_e32 v243, v57, v243\n" \
"v_mul_f32_e32 v244, v57, v244\n" \
"v_mul_f32_e32 v245, v57, v245\n" \
"v_mul_f32_e32 v246, v57, v246\n" \
"v_mul_f32_e32 v247, v57, v247\n" \
"buffer_load_dwordx4 v8, s[80:83], s92 offen offset:0 lds\n" \
"v_cvt_pk_bf16_f32 v96, v240, v241\n" \
"v_cvt_pk_bf16_f32 v97, v242, v243\n" \
"v_cvt_pk_bf16_f32 v98, v244, v245\n" \
"v_cvt_pk_bf16_f32 v99, v246, v247\n" \
"v_mfma_f32_16x16x32_bf16 v[252:255], v[128:131], v[188:191], v[252:255]\n" \
"v_mul_f32_e32 v248, v57, v248\n" \
"v_mfma_f32_16x16x32_bf16 a[208:211], a[216:219], v[96:99], a[208:211]\n" \
"v_mul_f32_e32 v249, v57, v249\n" \
"v_mul_f32_e32 v250, v57, v250\n" \
"v_mul_f32_e32 v251, v57, v251\n" \
"v_cvt_pk_bf16_f32 v100, v248, v249\n" \
"v_cvt_pk_bf16_f32 v101, v250, v251\n" \
"v_cmp_lt_f32_e64 s[72:73], s21, v62\n" \
"v_mul_f32_e32 v252, v57, v252\n" \
"v_mul_f32_e32 v253, v57, v253\n" \
"v_mul_f32_e32 v254, v57, v254\n" \
"v_mul_f32_e32 v255, v57, v255\n" \
"v_cvt_pk_bf16_f32 v102, v252, v253\n" \
"v_cvt_pk_bf16_f32 v103, v254, v255\n" \
"s_nop 1\n" \
"v_mfma_f32_16x16x32_bf16 a[208:211], a[216:219], v[100:103], a[208:211]\n" \
"s_nop 7\n" \
"v_accvgpr_read_b32 v63, a208\n" \
"v_cmp_u_f32_e32 vcc, v63, v63\n" \
"s_nop 0\n" \
"s_or_b64 vcc, s[72:73], vcc\n" \
"s_cbranch_scc0 L_out_%=\n" \
"s_branch L_end_%=\n" \
"L_skb_%=:\n" \
"s_sleep 1\n" \
"buffer_load_dword v57, v61, s[56:59], s96 offen sc1\n" \
"s_add_u32 s101, s101, 1\n" \
"s_waitcnt vmcnt(0)\n" \
"v_cmp_ne_u32_e32 vcc, s62, v57\n" \
"s_cmp_gt_u32 s101, 0x100000\n" \
"s_cbranch_scc1 L_rkb_%=\n" \
"s_nop 2\n" \
"s_cbranch_vccnz L_skb_%=\n" \
"s_branch L_rkb_%=\n" \
"L_sol_%=:\n" \
"s_sleep 1\n" \
"buffer_load_dword v26, v27, s[56:59], s96 offen sc1\n" \
"s_add_u32 s101, s101, 1\n" \
"s_waitcnt vmcnt(0)\n" \
"v_cmp_ne_u32_e32 vcc, s62, v26\n" \
"s_cmp_gt_u32 s101, 0x100000\n" \
"s_cbranch_scc1 L_rol_%=\n" \
"s_nop 2\n" \
"s_cbranch_vccnz L_sol_%=\n" \
"s_branch L_rol_%=\n" \
"L_sos_%=:\n" \
"s_sleep 1\n" \
"buffer_load_dword v26, v27, s[56:59], s96 offen sc1\n" \
"s_add_u32 s101, s101, 1\n" \
"s_waitcnt vmcnt(0)\n" \
"v_cmp_ne_u32_e32 vcc, s62, v26\n" \
"s_cmp_gt_u32 s101, 0x100000\n" \
"s_cbranch_scc1 L_ros_%=\n" \
"s_nop 2\n" \
"s_cbranch_vccnz L_sos_%=\n" \
"s_branch L_ros_%=\n" \
"L_sa_%=:\n" \
"s_sleep 1\n" \
"buffer_load_dword v26, v38, s[56:59], s64 offen sc1\n" \
"s_add_u32 s101, s101, 1\n" \
"s_waitcnt vmcnt(0)\n" \
"v_cmp_ne_u32_e32 vcc, s62, v26\n" \
"s_cmp_gt_u32 s101, 0x100000\n" \
"s_cbranch_scc1 L_ra_%=\n" \
"s_nop 2\n" \
"s_cbranch_vccnz L_sa_%=\n" \
"s_branch L_ra_%=\n" \
"L_sb_%=:\n" \
"s_sleep 1\n" \
"buffer_load_dword v47, v38, s[56:59], s71 offen sc1\n" \
"s_add_u32 s101, s101, 1\n" \
"s_waitcnt vmcnt(0)\n" \
"v_cmp_ne_u32_e32 vcc, s62, v47\n" \
"s_cmp_gt_u32 s101, 0x100000\n" \
"s_cbranch_scc1 L_rb_%=\n" \
"s_nop 2\n" \
"s_cbranch_vccnz L_sb_%=\n" \
"s_branch L_rb_%=\n" \
"L_sna_%=:\n" \
"buffer_load_dword v37, v40, s[56:59], s74 offen sc1\n" \
"s_add_u32 s101, s101, 1\n" \
"s_waitcnt vmcnt(0)\n" \
"v_cmp_eq_u32_e64 s[98:99], s62, v37\n" \
"s_cmp_gt_u32 s101, 0x100000\n" \
"s_cbranch_scc1 L_rna_%=\n" \
"s_andn2_b32 s75, s20, s98\n" \
"s_cbranch_scc0 L_gna_%=\n" \
"s_sleep 1\n" \
"s_branch L_sna_%=\n" \
"L_gna_%=:\n" \
"s_bitcmp1_b32 s20, 0\n" \
"s_cbranch_scc0 L_kna0_%=\n" \
"buffer_load_dwordx4 v[116:119], v20, s[56:59], s12 offen offset:0 sc1\n" \
"L_kna0_%=:\n" \
"s_bitcmp1_b32 s20, 1\n" \
"s_cbranch_scc0 L_kna1_%=\n" \
"buffer_load_dwordx4 v[124:127], v20, s[56:59], s12 offen offset:2048 sc1\n" \
"L_kna1_%=:\n" \
"s_bitcmp1_b32 s20, 2\n" \
"s_cbranch_scc0 L_kna2_%=\n" \
"buffer_load_dwordx4 v[132:135], v20, s[56:59], s13 offen offset:0 sc1\n" \
"L_kna2_%=:\n" \
"s_bitcmp1_b32 s20, 3\n" \
"s_cbranch_scc0 L_kna3_%=\n" \
"buffer_load_dwordx4 v[140:143], v20, s[56:59], s13 offen offset:2048 sc1\n" \
"L_kna3_%=:\n" \
"s_bitcmp1_b32 s20, 4\n" \
"s_cbranch_scc0 L_kna4_%=\n" \
"buffer_load_dwordx4 v[148:151], v20, s[56:59], s14 offen offset:0 sc1\n" \
"L_kna4_%=:\n" \
"s_bitcmp1_b32 s20, 5\n" \
"s_cbranch_scc0 L_kna5_%=\n" \
"buffer_load_dwordx4 v[156:159], v20, s[56:59], s14 offen offset:2048 sc1\n" \
"L_kna5_%=:\n" \
"s_bitcmp1_b32 s20, 6\n" \
"s_cbranch_scc0 L_kna6_%=\n" \
"buffer_load_dwordx4 v[164:167], v20, s[56:59], s15 offen offset:0 sc1\n" \
"L_kna6_%=:\n" \
"s_waitcnt vmcnt(0)\n" \
"s_branch L_rna_%=\n" \
"L_snx_%=:\n" \
"buffer_load_dword v37, v40, s[56:59], s74 offen sc1\n" \
"s_add_u32 s101, s101, 1\n" \
"s_waitcnt vmcnt(0)\n" \
"v_cmp_eq_u32_e64 s[98:99], s62, v37\n" \
"s_cmp_gt_u32 s101, 0x100000\n" \
"s_cbranch_scc1 L_rnx_%=\n" \
"s_andn2_b32 s75, s21, s98\n" \
"s_cbranch_scc0 L_gnx_%=\n" \
"s_sleep 1\n" \
"s_branch L_snx_%=\n" \
"L_gnx_%=:\n" \
"s_bitcmp1_b32 s21, 16\n" \
"s_cbranch_scc0 L_knx16_%=\n" \
"buffer_load_dwordx4 v[112:115], v20, s[56:59], s16 offen offset:0 sc1\n" \
"L_knx16_%=:\n" \
"s_bitcmp1_b32 s21, 17\n" \
"s_cbranch_scc0 L_knx17_%=\n" \
"buffer_load_dwordx4 v[184:187], v20, s[56:59], s16 offen offset:2048 sc1\n" \
"L_knx17_%=:\n" \
"s_bitcmp1_b32 s21, 18\n" \
"s_cbranch_scc0 L_knx18_%=\n" \
"buffer_load_dwordx4 v[120:123], v20, s[56:59], s17 offen offset:0 sc1\n" \
"L_knx18_%=:\n" \
"s_bitcmp1_b32 s21, 19\n" \
"s_cbranch_scc0 L_knx19_%=\n" \
"buffer_load_dwordx4 v[192:195], v20, s[56:59], s17 offen offset:2048 sc1\n" \
"L_knx19_%=:\n" \
"s_bitcmp1_b32 s21, 20\n" \
"s_cbranch_scc0 L_knx20_%=\n" \
"buffer_load_dwordx4 v[128:131], v20, s[56:59], s18 offen offset:0 sc1\n" \
"L_knx20_%=:\n" \
"s_bitcmp1_b32 s21, 21\n" \
"s_cbranch_scc0 L_knx21_%=\n" \
"buffer_load_dwordx4 v[200:203], v20, s[56:59], s18 offen offset:2048 sc1\n" \
"L_knx21_%=:\n" \
"s_bitcmp1_b32 s21, 22\n" \
"s_cbranch_scc0 L_knx22_%=\n" \
"buffer_load_dwordx4 v[176:179], v20, s[56:59], s19 offen offset:0 sc1\n" \
"L_knx22_%=:\n" \
"s_bitcmp1_b32 s21, 23\n" \
"s_cbranch_scc0 L_knx23_%=\n" \
"buffer_load_dwordx4 v[248:251], v20, s[56:59], s19 offen offset:2048 sc1\n" \
"L_knx23_%=:\n" \
"s_bitcmp1_b32 s21, 8\n" \
"s_cbranch_scc0 L_knx8_%=\n" \
"buffer_load_dwordx4 v[188:191], v20, s[56:59], s12 offen offset:1024 sc1\n" \
"L_knx8_%=:\n" \
"s_bitcmp1_b32 s21, 9\n" \
"s_cbranch_scc0 L_knx9_%=\n" \
"buffer_load_dwordx4 v[196:199], v20, s[56:59], s12 offen offset:3072 sc1\n" \
"L_knx9_%=:\n" \
"s_waitcnt vmcnt(0)\n" \
"s_branch L_rnx_%=\n" \
"L_sx0_%=:\n" \
"s_sleep 1\n" \
"buffer_load_dword v37, v40, s[56:59], s74 offen sc1\n" \
"s_add_u32 s101, s101, 1\n" \
"s_waitcnt vmcnt(0)\n" \
"v_cmp_eq_u32_e64 s[98:99], s62, v37\n" \
"s_cmp_gt_u32 s101, 0x100000\n" \
"s_cbranch_scc1 L_rx0_%=\n" \
"s_andn2_b32 s75, 0x400, s98\n" \
"s_cbranch_scc1 L_sx0_%=\n" \
"s_branch L_rx0_%=\n" \
"L_sx1_%=:\n" \
"s_sleep 1\n" \
"buffer_load_dword v37, v40, s[56:59], s74 offen sc1\n" \
"s_add_u32 s101, s101, 1\n" \
"s_waitcnt vmcnt(0)\n" \
"v_cmp_eq_u32_e64 s[98:99], s62, v37\n" \
"s_cmp_gt_u32 s101, 0x100000\n" \
"s_cbranch_scc1 L_rx1_%=\n" \
"s_andn2_b32 s75, 0x800, s98\n" \
"s_cbranch_scc1 L_sx1_%=\n" \
"s_branch L_rx1_%=\n" \
"L_sx2_%=:\n" \
"s_sleep 1\n" \
"buffer_load_dword v37, v40, s[56:59], s74 offen sc1\n" \
"s_add_u32 s101, s101, 1\n" \
"s_waitcnt vmcnt(0)\n" \
"v_cmp_eq_u32_e64 s[98:99], s62, v37\n" \
"s_cmp_gt_u32 s101, 0x100000\n" \
"s_cbranch_scc1 L_rx2_%=\n" \
"s_andn2_b32 s75, 0x1000, s98\n" \
"s_cbranch_scc1 L_sx2_%=\n" \
"s_branch L_rx2_%=\n" \
"L_sx3_%=:\n" \
"s_sleep 1\n" \
"buffer_load_dword v37, v40, s[56:59], s74 offen sc1\n" \
"s_add_u32 s101, s101, 1\n" \
"s_waitcnt vmcnt(0)\n" \
"v_cmp_eq_u32_e64 s[98:99], s62, v37\n" \
"s_cmp_gt_u32 s101, 0x100000\n" \
"s_cbranch_scc1 L_rx3_%=\n" \
"s_andn2_b32 s75, 0x2000, s98\n" \
"s_cbranch_scc1 L_sx3_%=\n" \
"s_branch L_rx3_%=\n" \
"L_sx4_%=:\n" \
"s_sleep 1\n" \
"buffer_load_dword v37, v40, s[56:59], s74 offen sc1\n" \
"s_add_u32 s101, s101, 1\n" \
"s_waitcnt vmcnt(0)\n" \
"v_cmp_eq_u32_e64 s[98:99], s62, v37\n" \
"s_cmp_gt_u32 s101, 0x100000\n" \
"s_cbranch_scc1 L_rx4_%=\n" \
"s_andn2_b32 s75, 0x4000, s98\n" \
"s_cbranch_scc1 L_sx4_%=\n" \
"s_branch L_rx4_%=\n" \
"L_att_stub:\n" \
"s_mov_b32 s101, 0\n" \
"v_add_u32_e32 v32, 0x49000, v20\n" \
"v_readlane_b32 s96, v11, 36\n" \
"v_readlane_b32 s97, v11, 37\n" \
"buffer_load_dwordx4 a[96:99], v20, s[56:59], s60 offen offset:0 sc1\n" \
"buffer_load_dwordx4 a[112:115], v32, s[56:59], s60 offen offset:0 sc1\n" \
"buffer_load_dwordx4 a[100:103], v20, s[56:59], s60 offen offset:1024 sc1\n" \
"buffer_load_dwordx4 a[116:119], v32, s[56:59], s60 offen offset:1024 sc1\n" \
"buffer_load_dwordx4 a[104:107], v20, s[56:59], s60 offen offset:2048 sc1\n" \
"buffer_load_dwordx4 a[120:123], v32, s[56:59], s60 offen offset:2048 sc1\n" \
"buffer_load_dwordx4 a[108:111], v20, s[56:59], s60 offen offset:3072 sc1\n" \
"buffer_load_dwordx4 a[124:127], v32, s[56:59], s60 offen offset:3072 sc1\n" \
"buffer_load_dwordx4 a[220:223], v20, s[56:59], s96 offen offset:0 sc1\n" \
"buffer_load_dwordx4 a[228:231], v32, s[56:59], s96 offen offset:0 sc1\n" \
"buffer_load_dwordx4 a[236:239], v20, s[56:59], s97 offen offset:0 sc1\n" \
"buffer_load_dwordx4 a[244:247], v32, s[56:59], s97 offen offset:0 sc1\n" \
"buffer_load_dwordx4 a[224:227], v20, s[56:59], s96 offen offset:1024 sc1\n" \
"buffer_load_dwordx4 a[232:235], v32, s[56:59], s96 offen offset:1024 sc1\n" \
"buffer_load_dwordx4 a[240:243], v20, s[56:59], s97 offen offset:1024 sc1\n" \
"buffer_load_dwordx4 a[248:251], v32, s[56:59], s97 offen offset:1024 sc1\n" \
"buffer_load_dwordx4 v[64:67], v20, s[56:59], s61 offen offset:0 sc1\n" \
"buffer_load_dwordx4 v[68:71], v20, s[56:59], s61 offen offset:1024 sc1\n" \
"buffer_load_dwordx4 v[72:75], v20, s[56:59], s61 offen offset:2048 sc1\n" \
"buffer_load_dwordx4 v[76:79], v20, s[56:59], s61 offen offset:3072 sc1\n" \
"buffer_load_dwordx4 v[80:83], v32, s[56:59], s61 offen offset:0 sc1\n" \
"buffer_load_dwordx4 v[84:87], v32, s[56:59], s61 offen offset:1024 sc1\n" \
"buffer_load_dwordx4 v[96:99], v32, s[56:59], s61 offen offset:2048 sc1\n" \
"buffer_load_dwordx4 v[100:103], v32, s[56:59], s61 offen offset:3072 sc1\n" \
"v_not_b32_e32 v26, s62\n" \
"s_waitcnt vmcnt(0)\n" \
"s_branch L_out_%=\n" \
".fill (L_ab + 16384 - .) / 4, 4, 0xBF800000\n" \
".fill (L_ab + 16384 - .) / 4, 4, 0xBF800000\n" \
"L_ap4:\n" \
"s_setpc_b64 s[88:89]\n" \
"L_gsw:\n" \
"v_mfma_f32_16x16x32_bf16 v[104:107], a[128:131], a[16:19], v[36:39]\n" \
"s_add_u32 s68, s60, 0xfff6e000\n" \
"s_add_u32 s69, s60, 0xfffb7000\n" \
"s_add_u32 s70, s60, 0x0\n" \
"s_add_u32 s71, s60, 0x49000\n" \
"v_mfma_f32_16x16x32_bf16 v[104:107], a[132:135], a[20:23], v[104:107]\n" \
"s_xor_b32 m0, s65, 0x12000\n" \
"ds_read_b128 a[160:163], v49 offset:0\n" \
"v_mfma_f32_16x16x32_bf16 v[104:107], a[136:139], a[24:27], v[104:107]\n" \
"buffer_load_dwordx4 v50, s[8:11], s64 offen offset:0 sc1 lds\n" \
"v_mfma_f32_16x16x32_bf16 v[104:107], a[140:143], a[28:31], v[104:107]\n" \
"ds_read_b128 a[164:167], v49 offset:1024\n" \
"ds_read_b128 a[168:171], v49 offset:2048\n" \
"v_mfma_f32_16x16x32_bf16 v[108:111], a[156:159], a[28:31], v[36:39]\n" \
"ds_read_b128 a[172:175], v49 offset:3072\n" \
"ds_read_b128 a[176:179], v49 offset:4096\n" \
"v_mfma_f32_16x16x32_bf16 v[108:111], a[152:155], a[24:27], v[108:111]\n" \
"buffer_load_dwordx4 v50, s[8:11], s64 offen offset:1024 sc1 lds\n" \
"v_mfma_f32_16x16x32_bf16 v[108:111], a[148:151], a[20:23], v[108:111]\n" \
"s_mov_b32 s65, s13\n" \
"v_add_u32_e32 v63, s15, v20\n" \
"v_mfma_f32_16x16x32_bf16 v[108:111], a[144:147], a[16:19], v[108:111]\n" \
"s_add_u32 s67, s14, 40960\n" \
"v_add_u32_e32 v50, 0x6000, v63\n" \
"v_mfma_f32_16x16x32_bf16 v[116:119], a[144:147], a[32:35], v[40:43]\n" \
"s_xor_b32 m0, s65, 0x8000\n" \
"ds_read_b128 a[180:183], v49 offset:5120\n" \
"v_mfma_f32_16x16x32_bf16 v[116:119], a[148:151], a[36:39], v[116:119]\n" \
"ds_read_b128 a[184:187], v49 offset:6144\n" \
"v_and_b32_e32 v68, v12, v68\n" \
"v_mfma_f32_16x16x32_bf16 v[116:119], a[152:155], a[40:43], v[116:119]\n" \
"v_and_b32_e32 v69, v13, v69\n" \
"v_and_b32_e32 v70, v14, v70\n" \
"v_mfma_f32_16x16x32_bf16 v[116:119], a[156:159], a[44:47], v[116:119]\n" \
"buffer_load_dwordx4 v50, s[8:11], s67 offen offset:0 sc1 lds\n" \
"v_mfma_f32_16x16x32_bf16 v[112:115], a[140:143], a[44:47], v[40:43]\n" \
"v_and_b32_e32 v71, v15, v71\n" \
"ds_read_b128 a[188:191], v49 offset:7168\n" \
"v_mfma_f32_16x16x32_bf16 v[112:115], a[136:139], a[40:43], v[112:115]\n" \
"v_exp_f32_e32 v86, v86\n" \
"v_mfma_f32_16x16x32_bf16 v[112:115], a[132:135], a[36:39], v[112:115]\n" \
"v_exp_f32_e32 v87, v87\n" \
"v_mfma_f32_16x16x32_bf16 v[112:115], a[128:131], a[32:35], v[112:115]\n" \
"v_exp_f32_e32 v84, v84\n" \
"v_mfma_f32_16x16x32_bf16 v[120:123], a[128:131], a[48:51], v[44:47]\n" \
"buffer_load_dwordx4 v50, s[8:11], s67 offen offset:1024 sc1 lds\n" \
"v_mfma_f32_16x16x32_bf16 v[120:123], a[132:135], a[52:55], v[120:123]\n" \
"v_exp_f32_e32 v85, v85\n" \
"v_mfma_f32_16x16x32_bf16 v[120:123], a[136:139], a[56:59], v[120:123]\n" \
"v_cvt_pk_bf16_f32 v87, v86, v87\n" \
"v_cvt_pk_bf16_f32 v86, v84, v85\n" \
"v_mfma_f32_16x16x32_bf16 v[120:123], a[140:143], a[60:63], v[120:123]\n" \
"v_exp_f32_e32 v82, v82\n" \
"v_mfma_f32_16x16x32_bf16 v[124:127], a[156:159], a[60:63], v[44:47]\n" \
"v_exp_f32_e32 v83, v83\n" \
"v_mfma_f32_16x16x32_bf16 v[124:127], a[152:155], a[56:59], v[124:127]\n" \
"v_exp_f32_e32 v80, v80\n" \
"v_mfma_f32_16x16x32_bf16 v[124:127], a[148:151], a[52:55], v[124:127]\n" \
"buffer_load_dwordx4 v50, s[8:11], s67 offen offset:2048 sc1 lds\n" \
"v_mfma_f32_16x16x32_bf16 v[124:127], a[144:147], a[48:51], v[124:127]\n" \
"v_cvt_pk_bf16_f32 v85, v82, v83\n" \
"s_waitcnt lgkmcnt(1)\n" \
"v_mfma_f32_16x16x32_bf16 v[128:131], a[160:163], v[68:71], v[128:131]\n" \
"v_exp_f32_e32 v81, v81\n" \
"v_mfma_f32_16x16x32_bf16 v[160:163], a[160:163], v[72:75], v[160:163]\n" \
"v_cvt_pk_bf16_f32 v84, v80, v81\n" \
"s_mov_b32 s64, s14\n" \
"v_mfma_f32_16x16x32_bf16 v[132:135], a[164:167], v[68:71], v[132:135]\n" \
"v_exp_f32_e32 v88, v88\n" \
"v_mfma_f32_16x16x32_bf16 v[164:167], a[164:167], v[72:75], v[164:167]\n" \
"v_exp_f32_e32 v89, v89\n" \
"v_mfma_f32_16x16x32_bf16 v[136:139], a[168:171], v[68:71], v[136:139]\n" \
"buffer_load_dwordx4 v50, s[8:11], s67 offen offset:3072 sc1 lds\n" \
"v_mfma_f32_16x16x32_bf16 v[168:171], a[168:171], v[72:75], v[168:171]\n" \
"v_exp_f32_e32 v90, v90\n" \
"v_mfma_f32_16x16x32_bf16 v[140:143], a[172:175], v[68:71], v[140:143]\n" \
"v_exp_f32_e32 v91, v91\n" \
"v_mfma_f32_16x16x32_bf16 v[172:175], a[172:175], v[72:75], v[172:175]\n" \
"v_cvt_pk_bf16_f32 v88, v88, v89\n" \
"v_cvt_pk_bf16_f32 v89, v90, v91\n" \
"v_mfma_f32_16x16x32_bf16 v[144:147], a[176:179], v[68:71], v[144:147]\n" \
"v_exp_f32_e32 v92, v92\n" \
"v_mfma_f32_16x16x32_bf16 v[176:179], a[176:179], v[72:75], v[176:179]\n" \
"v_exp_f32_e32 v93, v93\n" \
"v_mfma_f32_16x16x32_bf16 v[148:151], a[180:183], v[68:71], v[148:151]\n" \
"v_exp_f32_e32 v94, v94\n" \
"v_mfma_f32_16x16x32_bf16 v[180:183], a[180:183], v[72:75], v[180:183]\n" \
"v_exp_f32_e32 v95, v95\n" \
"v_mfma_f32_16x16x32_bf16 v[152:155], a[184:187], v[68:71], v[152:155]\n" \
"v_cvt_pk_bf16_f32 v90, v92, v93\n" \
"v_cvt_pk_bf16_f32 v91, v94, v95\n" \
"v_mfma_f32_16x16x32_bf16 v[184:187], a[184:187], v[72:75], v[184:187]\n" \
"v_add_u32_e32 v51, 0x12000, v63\n" \
"s_waitcnt lgkmcnt(0)\n" \
"v_mfma_f32_16x16x32_bf16 v[156:159], a[188:191], v[68:71], v[156:159]\n" \
"s_xor_b32 m0, s65, 0xa000\n" \
"ds_read_b32 v58, v18 offset:512\n" \
"v_mfma_f32_16x16x32_bf16 v[188:191], a[188:191], v[72:75], v[188:191]\n" \
"buffer_load_dwordx4 v51, s[8:11], s64 offen offset:0 sc1 lds\n" \
"v_smfmac_f32_16x16x64_bf16 a[192:195], a[200:203], v[68:75], v21\n" \
"buffer_load_dwordx4 v51, s[8:11], s64 offen offset:1024 sc1 lds\n" \
"v_mfma_f32_16x16x32_bf16 v[220:223], a[188:191], v[84:87], v[220:223]\n" \
"ds_read_b32 v59, v18 offset:640\n" \
"ds_read_b32 v60, v18 offset:768\n" \
"v_mfma_f32_16x16x32_bf16 v[252:255], a[188:191], v[88:91], v[252:255]\n" \
"ds_read_b32 v61, v18 offset:896\n" \
"ds_read_b128 a[128:131], v48 offset:0\n" \
"v_mfma_f32_16x16x32_bf16 v[216:219], a[184:187], v[84:87], v[216:219]\n" \
"ds_read_b128 a[144:147], v48 offset:4096\n" \
"ds_read_b128 a[132:135], v48 offset:1024\n" \
"v_mfma_f32_16x16x32_bf16 v[248:251], a[184:187], v[88:91], v[248:251]\n" \
"ds_read_b128 a[148:151], v48 offset:5120\n" \
"ds_read_b128 a[136:139], v48 offset:2048\n" \
"v_mfma_f32_16x16x32_bf16 v[212:215], a[180:183], v[84:87], v[212:215]\n" \
"ds_read_b128 a[152:155], v48 offset:6144\n" \
"ds_read_b128 a[140:143], v48 offset:3072\n" \
"v_mfma_f32_16x16x32_bf16 v[244:247], a[180:183], v[88:91], v[244:247]\n" \
"ds_read_b128 a[156:159], v48 offset:7168\n" \
"v_add_u32_e32 v52, 0x14000, v63\n" \
"v_mfma_f32_16x16x32_bf16 v[208:211], a[176:179], v[84:87], v[208:211]\n" \
"buffer_load_dwordx4 v51, s[8:11], s64 offen offset:2048 sc1 lds\n" \
"v_mfma_f32_16x16x32_bf16 v[240:243], a[176:179], v[88:91], v[240:243]\n" \
"v_exp_f32_e32 v104, v104\n" \
"v_mfma_f32_16x16x32_bf16 v[204:207], a[172:175], v[84:87], v[204:207]\n" \
"v_exp_f32_e32 v105, v105\n" \
"v_mfma_f32_16x16x32_bf16 v[236:239], a[172:175], v[88:91], v[236:239]\n" \
"buffer_load_dwordx4 v51, s[8:11], s64 offen offset:3072 sc1 lds\n" \
"v_mfma_f32_16x16x32_bf16 v[200:203], a[168:171], v[84:87], v[200:203]\n" \
"v_exp_f32_e32 v106, v106\n" \
"v_mfma_f32_16x16x32_bf16 v[232:235], a[168:171], v[88:91], v[232:235]\n" \
"v_exp_f32_e32 v107, v107\n" \
"v_mfma_f32_16x16x32_bf16 v[196:199], a[164:167], v[84:87], v[196:199]\n" \
"v_cvt_pk_bf16_f32 v104, v104, v105\n" \
"v_cvt_pk_bf16_f32 v105, v106, v107\n" \
"v_mfma_f32_16x16x32_bf16 v[228:231], a[164:167], v[88:91], v[228:231]\n" \
"v_exp_f32_e32 v108, v108\n" \
"v_mfma_f32_16x16x32_bf16 v[192:195], a[160:163], v[84:87], v[192:195]\n" \
"v_exp_f32_e32 v109, v109\n" \
"v_mfma_f32_16x16x32_bf16 v[224:227], a[160:163], v[88:91], v[224:227]\n" \
"v_exp_f32_e32 v110, v110\n" \
"v_smfmac_f32_16x16x64_bf16 a[196:199], a[200:203], v[84:91], v21\n" \
"v_exp_f32_e32 v111, v111\n" \
"v_cvt_pk_bf16_f32 v106, v108, v109\n" \
"v_cvt_pk_bf16_f32 v107, v110, v111\n" \
"v_and_b32_e32 v104, v12, v104\n" \
"v_and_b32_e32 v105, v13, v105\n" \
"v_and_b32_e32 v106, v14, v106\n" \
"v_and_b32_e32 v107, v15, v107\n" \
"v_accvgpr_read_b32 v54, a192\n" \
"v_accvgpr_read_b32 v22, a193\n" \
"v_add_u32_e32 v53, 0x20000, v63\n" \
"v_exp_f32_e32 v118, v118\n" \
"v_exp_f32_e32 v119, v119\n" \
"v_exp_f32_e32 v116, v116\n" \
"v_exp_f32_e32 v117, v117\n" \
"v_cvt_pk_bf16_f32 v119, v118, v119\n" \
"v_exp_f32_e32 v114, v114\n" \
"v_exp_f32_e32 v115, v115\n" \
"v_exp_f32_e32 v112, v112\n" \
"v_exp_f32_e32 v113, v113\n" \
"v_cvt_pk_bf16_f32 v118, v116, v117\n" \
"v_cvt_pk_bf16_f32 v117, v114, v115\n" \
"v_cvt_pk_bf16_f32 v116, v112, v113\n" \
"v_readlane_b32 s93, v11, 19\n" \
"s_waitcnt vmcnt(10) lgkmcnt(0)\n" \
"s_barrier\n" \
"v_mfma_f32_16x16x32_bf16 v[80:83], a[128:131], a[32:35], v[40:43]\n" \
"ds_read_b128 a[160:163], v49 offset:8192\n" \
"ds_read_b128 a[164:167], v49 offset:9216\n" \
"v_mfma_f32_16x16x32_bf16 v[80:83], a[132:135], a[36:39], v[80:83]\n" \
"ds_read_b128 a[168:171], v49 offset:10240\n" \
"s_xor_b32 m0, s65, 0x18000\n" \
"v_mfma_f32_16x16x32_bf16 v[80:83], a[136:139], a[40:43], v[80:83]\n" \
"buffer_load_dwordx4 v52, s[8:11], s64 offen offset:0 sc1 lds\n" \
"v_mfma_f32_16x16x32_bf16 v[80:83], a[140:143], a[44:47], v[80:83]\n" \
"ds_read_b128 a[172:175], v49 offset:11264\n" \
"ds_read_b128 a[176:179], v49 offset:12288\n" \
"v_mfma_f32_16x16x32_bf16 v[84:87], a[156:159], a[44:47], v[40:43]\n" \
"ds_read_b128 a[180:183], v49 offset:13312\n" \
"ds_read_b128 a[184:187], v49 offset:14336\n" \
"v_mfma_f32_16x16x32_bf16 v[84:87], a[152:155], a[40:43], v[84:87]\n" \
"buffer_load_dwordx4 v52, s[8:11], s64 offen offset:1024 sc1 lds\n" \
"v_mfma_f32_16x16x32_bf16 v[84:87], a[148:151], a[36:39], v[84:87]\n" \
"ds_read_b128 a[188:191], v49 offset:15360\n" \
"v_readlane_b32 s80, v11, 11\n" \
"v_mfma_f32_16x16x32_bf16 v[84:87], a[144:147], a[32:35], v[84:87]\n" \
"buffer_load_dwordx4 v52, s[8:11], s64 offen offset:2048 sc1 lds\n" \
"v_mfma_f32_16x16x32_bf16 v[92:95], a[144:147], a[48:51], v[44:47]\n" \
"v_exp_f32_e32 v120, v120\n" \
"v_mfma_f32_16x16x32_bf16 v[92:95], a[148:151], a[52:55], v[92:95]\n" \
"v_exp_f32_e32 v121, v121\n" \
"v_mfma_f32_16x16x32_bf16 v[92:95], a[152:155], a[56:59], v[92:95]\n" \
"buffer_load_dwordx4 v52, s[8:11], s64 offen offset:3072 sc1 lds\n" \
"v_mfma_f32_16x16x32_bf16 v[92:95], a[156:159], a[60:63], v[92:95]\n" \
"s_mov_b32 m0, s65\n" \
"v_cvt_pk_bf16_f32 v120, v120, v121\n" \
"v_mfma_f32_16x16x32_bf16 v[88:91], a[140:143], a[60:63], v[44:47]\n" \
"v_exp_f32_e32 v122, v122\n" \
"v_mfma_f32_16x16x32_bf16 v[88:91], a[136:139], a[56:59], v[88:91]\n" \
"buffer_load_dwordx4 v53, s[8:11], s64 offen offset:0 sc1 lds\n" \
"v_mfma_f32_16x16x32_bf16 v[88:91], a[132:135], a[52:55], v[88:91]\n" \
"v_exp_f32_e32 v123, v123\n" \
"v_mfma_f32_16x16x32_bf16 v[88:91], a[128:131], a[48:51], v[88:91]\n" \
"v_exp_f32_e32 v124, v124\n" \
"v_mfma_f32_16x16x32_bf16 a[192:195], a[212:215], v[104:107], a[192:195]\n" \
"v_cvt_pk_bf16_f32 v121, v122, v123\n" \
"s_waitcnt lgkmcnt(5)\n" \
"v_mfma_f32_16x16x32_bf16 v[160:163], a[160:163], v[104:107], v[160:163]\n" \
"buffer_load_dwordx4 v53, s[8:11], s64 offen offset:1024 sc1 lds\n" \
"v_mfma_f32_16x16x32_bf16 v[164:167], a[164:167], v[104:107], v[164:167]\n" \
"v_exp_f32_e32 v125, v125\n" \
"v_mfma_f32_16x16x32_bf16 v[168:171], a[168:171], v[104:107], v[168:171]\n" \
"v_cvt_pk_bf16_f32 v122, v124, v125\n" \
"s_waitcnt lgkmcnt(1)\n" \
"v_mfma_f32_16x16x32_bf16 v[172:175], a[172:175], v[104:107], v[172:175]\n" \
"v_exp_f32_e32 v126, v126\n" \
"v_mfma_f32_16x16x32_bf16 v[176:179], a[176:179], v[104:107], v[176:179]\n" \
"v_exp_f32_e32 v127, v127\n" \
"v_mfma_f32_16x16x32_bf16 v[180:183], a[180:183], v[104:107], v[180:183]\n" \
"v_cvt_pk_bf16_f32 v123, v126, v127\n" \
"v_readlane_b32 s81, v11, 12\n" \
"v_mfma_f32_16x16x32_bf16 v[184:187], a[184:187], v[104:107], v[184:187]\n" \
"s_mov_b32 s82, 0x47fca0\n" \
"s_waitcnt lgkmcnt(0)\n" \
"v_mfma_f32_16x16x32_bf16 v[188:191], a[188:191], v[104:107], v[188:191]\n" \
"buffer_load_dwordx4 v53, s[8:11], s64 offen offset:2048 sc1 lds\n" \
"v_mfma_f32_16x16x32_bf16 v[220:223], a[188:191], v[116:119], v[220:223]\n" \
"buffer_load_dwordx4 v53, s[8:11], s64 offen offset:3072 sc1 lds\n" \
"v_mfma_f32_16x16x32_bf16 v[252:255], a[188:191], v[120:123], v[252:255]\n" \
"s_mov_b32 s83, 0x20000\n" \
"v_readlane_b32 s92, v11, 26\n" \
"v_mfma_f32_16x16x32_bf16 v[216:219], a[184:187], v[116:119], v[216:219]\n" \
"s_add_u32 m0, s93, 0x0\n" \
"ds_read_b128 a[128:131], v48 offset:8192\n" \
"v_mfma_f32_16x16x32_bf16 v[248:251], a[184:187], v[120:123], v[248:251]\n" \
"ds_read_b128 a[144:147], v48 offset:12288\n" \
"ds_read_b128 a[132:135], v48 offset:9216\n" \
"v_mfma_f32_16x16x32_bf16 v[212:215], a[180:183], v[116:119], v[212:215]\n" \
"buffer_load_dwordx4 v0, s[80:83], s92 offen offset:0 lds\n" \
"v_mfma_f32_16x16x32_bf16 v[244:247], a[180:183], v[120:123], v[244:247]\n" \
"buffer_load_dwordx4 v1, s[80:83], s92 offen offset:1024 lds\n" \
"v_mfma_f32_16x16x32_bf16 v[208:211], a[176:179], v[116:119], v[208:211]\n" \
"ds_read_b128 a[148:151], v48 offset:13312\n" \
"ds_read_b128 a[136:139], v48 offset:10240\n" \
"v_mfma_f32_16x16x32_bf16 v[240:243], a[176:179], v[120:123], v[240:243]\n" \
"ds_read_b128 a[152:155], v48 offset:14336\n" \
"ds_read_b128 a[140:143], v48 offset:11264\n" \
"v_mfma_f32_16x16x32_bf16 v[204:207], a[172:175], v[116:119], v[204:207]\n" \
"buffer_load_dwordx4 v2, s[80:83], s92 offen offset:2048 lds\n" \
"v_mfma_f32_16x16x32_bf16 v[236:239], a[172:175], v[120:123], v[236:239]\n" \
"ds_read_b128 a[156:159], v48 offset:15360\n" \
"s_xor_b32 s65, s65, 0x8000\n" \
"v_mfma_f32_16x16x32_bf16 v[200:203], a[168:171], v[116:119], v[200:203]\n" \
"s_add_u32 s64, s64, 0x10000\n" \
"v_add_u32_e32 v49, 0x10000, v48\n" \
"v_mfma_f32_16x16x32_bf16 v[232:235], a[168:171], v[120:123], v[232:235]\n" \
"buffer_load_dwordx4 v3, s[80:83], s92 offen offset:3072 lds\n" \
"v_mfma_f32_16x16x32_bf16 v[196:199], a[164:167], v[116:119], v[196:199]\n" \
"v_exp_f32_e32 v86, v86\n" \
"v_mfma_f32_16x16x32_bf16 v[228:231], a[164:167], v[120:123], v[228:231]\n" \
"v_exp_f32_e32 v87, v87\n" \
"v_mfma_f32_16x16x32_bf16 v[192:195], a[160:163], v[116:119], v[192:195]\n" \
"v_exp_f32_e32 v84, v84\n" \
"v_mfma_f32_16x16x32_bf16 v[224:227], a[160:163], v[120:123], v[224:227]\n" \
"v_exp_f32_e32 v85, v85\n" \
"v_smfmac_f32_16x16x64_bf16 a[196:199], a[200:203], v[116:123], v21\n" \
"v_exp_f32_e32 v88, v88\n" \
"v_exp_f32_e32 v89, v89\n" \
"v_cvt_pk_bf16_f32 v87, v86, v87\n" \
"v_exp_f32_e32 v82, v82\n" \
"v_exp_f32_e32 v83, v83\n" \
"v_exp_f32_e32 v80, v80\n" \
"v_exp_f32_e32 v81, v81\n" \
"v_exp_f32_e32 v90, v90\n" \
"v_exp_f32_e32 v91, v91\n" \
"v_cvt_pk_bf16_f32 v86, v84, v85\n" \
"v_cvt_pk_bf16_f32 v88, v88, v89\n" \
"v_exp_f32_e32 v92, v92\n" \
"v_exp_f32_e32 v93, v93\n" \
"v_exp_f32_e32 v94, v94\n" \
"v_exp_f32_e32 v95, v95\n" \
"v_cvt_pk_bf16_f32 v85, v82, v83\n" \
"v_cvt_pk_bf16_f32 v84, v80, v81\n" \
"v_cvt_pk_bf16_f32 v89, v90, v91\n" \
"v_and_b32_e32 v84, v12, v84\n" \
"v_and_b32_e32 v85, v13, v85\n" \
"v_and_b32_e32 v86, v14, v86\n" \
"v_and_b32_e32 v87, v15, v87\n" \
"v_cvt_pk_bf16_f32 v90, v92, v93\n" \
"v_cvt_pk_bf16_f32 v91, v94, v95\n" \
"v_add_u32_e32 v48, s16, v20\n" \
"v_add_f32_e32 v54, v54, v22\n" \
"v_accvgpr_read_b32 v55, a194\n" \
"v_accvgpr_read_b32 v23, a195\n" \
"v_mfma_f32_16x16x32_bf16 a[192:195], a[216:219], a[216:219], 0\n" \
"v_max_f32_e32 v62, v62, v54\n" \
"v_add_f32_e32 v55, v55, v23\n" \
"v_max_f32_e32 v62, v62, v55\n" \
"v_rcp_f32_e32 v54, v54\n" \
"v_rcp_f32_e32 v55, v55\n" \
"v_mul_f32_e32 v54, v54, v58\n" \
"v_mul_f32_e32 v55, v55, v59\n" \
"v_mul_f32_e32 v128, v54, v128\n" \
"v_mul_f32_e32 v129, v54, v129\n" \
"v_mul_f32_e32 v136, v54, v136\n" \
"v_mul_f32_e32 v137, v54, v137\n" \
"v_mul_f32_e32 v144, v54, v144\n" \
"v_mul_f32_e32 v145, v54, v145\n" \
"v_mul_f32_e32 v152, v54, v152\n" \
"v_mul_f32_e32 v153, v54, v153\n" \
"v_mul_f32_e32 v160, v55, v160\n" \
"v_mul_f32_e32 v161, v55, v161\n" \
"v_mul_f32_e32 v168, v55, v168\n" \
"v_mul_f32_e32 v169, v55, v169\n" \
"v_mul_f32_e32 v176, v55, v176\n" \
"v_mul_f32_e32 v177, v55, v177\n" \
"v_mul_f32_e32 v184, v55, v184\n" \
"v_mul_f32_e32 v185, v55, v185\n" \
"v_cvt_pk_bf16_f32 v128, v128, v129\n" \
"v_mul_f32_e32 v130, v54, v130\n" \
"v_mul_f32_e32 v131, v54, v131\n" \
"v_cvt_pk_bf16_f32 v136, v136, v137\n" \
"v_mul_f32_e32 v138, v54, v138\n" \
"v_mul_f32_e32 v139, v54, v139\n" \
"v_cvt_pk_bf16_f32 v144, v144, v145\n" \
"v_mul_f32_e32 v146, v54, v146\n" \
"v_mul_f32_e32 v147, v54, v147\n" \
"v_cvt_pk_bf16_f32 v152, v152, v153\n" \
"v_mul_f32_e32 v154, v54, v154\n" \
"v_mul_f32_e32 v155, v54, v155\n" \
"v_cvt_pk_bf16_f32 v160, v160, v161\n" \
"v_mul_f32_e32 v162, v55, v162\n" \
"v_mul_f32_e32 v163, v55, v163\n" \
"v_cvt_pk_bf16_f32 v168, v168, v169\n" \
"v_mul_f32_e32 v170, v55, v170\n" \
"v_mul_f32_e32 v171, v55, v171\n" \
"v_cvt_pk_bf16_f32 v176, v176, v177\n" \
"v_mul_f32_e32 v178, v55, v178\n" \
"v_mul_f32_e32 v179, v55, v179\n" \
"v_cvt_pk_bf16_f32 v184, v184, v185\n" \
"v_mul_f32_e32 v186, v55, v186\n" \
"v_mul_f32_e32 v187, v55, v187\n" \
"v_cvt_pk_bf16_f32 v129, v130, v131\n" \
"v_mul_f32_e32 v132, v54, v132\n" \
"v_mul_f32_e32 v133, v54, v133\n" \
"v_mul_f32_e32 v134, v54, v134\n" \
"v_mul_f32_e32 v135, v54, v135\n" \
"v_cvt_pk_bf16_f32 v137, v138, v139\n" \
"v_mul_f32_e32 v140, v54, v140\n" \
"s_waitcnt vmcnt(12) lgkmcnt(0)\n" \
"s_barrier\n" \
"v_mfma_f32_16x16x32_bf16 v[120:123], a[128:131], a[48:51], v[44:47]\n" \
"s_xor_b32 m0, s65, 0x12000\n" \
"ds_read_b128 a[188:191], v49 offset:7168\n" \
"v_mfma_f32_16x16x32_bf16 v[120:123], a[132:135], a[52:55], v[120:123]\n" \
"buffer_load_dwordx4 v50, s[8:11], s64 offen offset:0 sc1 lds\n" \
"v_mfma_f32_16x16x32_bf16 v[120:123], a[136:139], a[56:59], v[120:123]\n" \
"ds_read_b128 a[184:187], v49 offset:6144\n" \
"ds_read_b128 a[180:183], v49 offset:5120\n" \
"v_mfma_f32_16x16x32_bf16 v[120:123], a[140:143], a[60:63], v[120:123]\n" \
"buffer_load_dwordx4 v50, s[8:11], s64 offen offset:1024 sc1 lds\n" \
"v_mfma_f32_16x16x32_bf16 v[124:127], a[156:159], a[60:63], v[44:47]\n" \
"ds_read_b128 a[176:179], v49 offset:4096\n" \
"ds_read_b128 a[172:175], v49 offset:3072\n" \
"v_mfma_f32_16x16x32_bf16 v[124:127], a[152:155], a[56:59], v[124:127]\n" \
"ds_read_b128 a[168:171], v49 offset:2048\n" \
"ds_read_b128 a[164:167], v49 offset:1024\n" \
"v_mfma_f32_16x16x32_bf16 v[124:127], a[148:151], a[52:55], v[124:127]\n" \
"ds_read_b128 a[160:163], v49 offset:0\n" \
"ds_read_b128 a[0:3], v49 offset:8192\n" \
"v_mfma_f32_16x16x32_bf16 v[124:127], a[144:147], a[48:51], v[124:127]\n" \
"buffer_load_dwordx4 v50, s[8:11], s64 offen offset:2048 sc1 lds\n" \
"v_mfma_f32_16x16x32_bf16 v[32:35], a[220:223], a[64:67], 0\n" \
"ds_read_b128 a[4:7], v49 offset:9216\n" \
"ds_read_b128 a[8:11], v49 offset:10240\n" \
"v_mfma_f32_16x16x32_bf16 v[36:39], a[220:223], a[80:83], 0\n" \
"ds_read_b128 a[12:15], v49 offset:11264\n" \
"ds_read_b128 a[16:19], v49 offset:12288\n" \
"v_mfma_f32_16x16x32_bf16 v[40:43], a[220:223], a[96:99], 0\n" \
"ds_read_b128 a[20:23], v49 offset:13312\n" \
"ds_read_b128 a[24:27], v49 offset:14336\n" \
"v_mfma_f32_16x16x32_bf16 v[44:47], a[220:223], a[112:115], 0\n" \
"buffer_load_dwordx4 v50, s[8:11], s64 offen offset:3072 sc1 lds\n" \
"v_mfma_f32_16x16x32_bf16 v[32:35], a[224:227], a[68:71], v[32:35]\n" \
"s_xor_b32 m0, s65, 0xa000\n" \
"ds_read_b128 a[28:31], v49 offset:15360\n" \
"v_mfma_f32_16x16x32_bf16 v[36:39], a[224:227], a[84:87], v[36:39]\n" \
"v_exp_f32_e32 v120, v120\n" \
"v_mfma_f32_16x16x32_bf16 v[40:43], a[224:227], a[100:103], v[40:43]\n" \
"buffer_load_dwordx4 v51, s[8:11], s64 offen offset:0 sc1 lds\n" \
"v_mfma_f32_16x16x32_bf16 v[44:47], a[224:227], a[116:119], v[44:47]\n" \
"v_exp_f32_e32 v121, v121\n" \
"v_mfma_f32_16x16x32_bf16 v[32:35], a[228:231], a[72:75], v[32:35]\n" \
"v_exp_f32_e32 v122, v122\n" \
"v_mfma_f32_16x16x32_bf16 v[36:39], a[228:231], a[88:91], v[36:39]\n" \
"v_exp_f32_e32 v123, v123\n" \
"v_mfma_f32_16x16x32_bf16 v[40:43], a[228:231], a[104:107], v[40:43]\n" \
"buffer_load_dwordx4 v51, s[8:11], s64 offen offset:1024 sc1 lds\n" \
"v_mfma_f32_16x16x32_bf16 v[44:47], a[228:231], a[120:123], v[44:47]\n" \
"v_cvt_pk_bf16_f32 v120, v120, v121\n" \
"v_cvt_pk_bf16_f32 v121, v122, v123\n" \
"v_mfma_f32_16x16x32_bf16 v[32:35], a[232:235], a[76:79], v[32:35]\n" \
"v_exp_f32_e32 v124, v124\n" \
"v_mfma_f32_16x16x32_bf16 v[36:39], a[232:235], a[92:95], v[36:39]\n" \
"v_exp_f32_e32 v125, v125\n" \
"v_mfma_f32_16x16x32_bf16 v[40:43], a[232:235], a[108:111], v[40:43]\n" \
"buffer_load_dwordx4 v51, s[8:11], s64 offen offset:2048 sc1 lds\n" \
"v_mfma_f32_16x16x32_bf16 v[44:47], a[232:235], a[124:127], v[44:47]\n" \
"v_exp_f32_e32 v126, v126\n" \
"v_smfmac_f32_16x16x64_bf16 a[196:199], a[200:203], v[84:91], v21\n" \
"v_cvt_pk_bf16_f32 v122, v124, v125\n" \
"s_waitcnt lgkmcnt(1)\n" \
"v_mfma_f32_16x16x32_bf16 v[220:223], a[188:191], v[84:87], v[220:223]\n" \
"buffer_load_dwordx4 v51, s[8:11], s64 offen offset:3072 sc1 lds\n" \
"v_mfma_f32_16x16x32_bf16 v[252:255], a[188:191], v[88:91], v[252:255]\n" \
"s_add_u32 m0, s93, 0x1000\n" \
"v_and_b32_e32 v120, v12, v120\n" \
"v_mfma_f32_16x16x32_bf16 v[216:219], a[184:187], v[84:87], v[216:219]\n" \
"v_exp_f32_e32 v127, v127\n" \
"v_mfma_f32_16x16x32_bf16 v[248:251], a[184:187], v[88:91], v[248:251]\n" \
"v_cvt_pk_bf16_f32 v123, v126, v127\n" \
"v_and_b32_e32 v121, v13, v121\n" \
"v_mfma_f32_16x16x32_bf16 v[212:215], a[180:183], v[84:87], v[212:215]\n" \
"buffer_load_dwordx4 v4, s[80:83], s92 offen offset:0 lds\n" \
"v_mfma_f32_16x16x32_bf16 v[244:247], a[180:183], v[88:91], v[244:247]\n" \
"v_and_b32_e32 v122, v14, v122\n" \
"v_and_b32_e32 v123, v15, v123\n" \
"v_mfma_f32_16x16x32_bf16 v[208:211], a[176:179], v[84:87], v[208:211]\n" \
"v_accvgpr_read_b32 v56, a196\n" \
"v_accvgpr_read_b32 v24, a197\n" \
"v_mfma_f32_16x16x32_bf16 v[240:243], a[176:179], v[88:91], v[240:243]\n" \
"ds_read_b128 a[128:131], v48 offset:0\n" \
"ds_read_b128 a[144:147], v48 offset:4096\n" \
"v_mfma_f32_16x16x32_bf16 v[204:207], a[172:175], v[84:87], v[204:207]\n" \
"buffer_load_dwordx4 v5, s[80:83], s92 offen offset:1024 lds\n" \
"v_mfma_f32_16x16x32_bf16 v[236:239], a[172:175], v[88:91], v[236:239]\n" \
"ds_read_b128 a[132:135], v48 offset:1024\n" \
"ds_read_b128 a[148:151], v48 offset:5120\n" \
"v_mfma_f32_16x16x32_bf16 v[200:203], a[168:171], v[84:87], v[200:203]\n" \
"ds_read_b128 a[136:139], v48 offset:2048\n" \
"ds_read_b128 a[152:155], v48 offset:6144\n" \
"v_mfma_f32_16x16x32_bf16 v[232:235], a[168:171], v[88:91], v[232:235]\n" \
"buffer_load_dwordx4 v6, s[80:83], s92 offen offset:2048 lds\n" \
"v_mfma_f32_16x16x32_bf16 v[196:199], a[164:167], v[84:87], v[196:199]\n" \
"ds_read_b128 a[140:143], v48 offset:3072\n" \
"ds_read_b128 a[156:159], v48 offset:7168\n" \
"v_mfma_f32_16x16x32_bf16 v[228:231], a[164:167], v[88:91], v[228:231]\n" \
"v_mul_f32_e32 v141, v54, v141\n" \
"v_mul_f32_e32 v142, v54, v142\n" \
"v_mfma_f32_16x16x32_bf16 v[192:195], a[160:163], v[84:87], v[192:195]\n" \
"v_mul_f32_e32 v143, v54, v143\n" \
"v_cvt_pk_bf16_f32 v145, v146, v147\n" \
"v_mfma_f32_16x16x32_bf16 v[224:227], a[160:163], v[88:91], v[224:227]\n" \
"buffer_load_dwordx4 v7, s[80:83], s92 offen offset:3072 lds\n" \
"v_mfma_f32_16x16x32_bf16 a[196:199], a[212:215], v[120:123], a[196:199]\n" \
"v_mul_f32_e32 v148, v54, v148\n" \
"v_mul_f32_e32 v149, v54, v149\n" \
"v_mfma_f32_16x16x32_bf16 v[224:227], a[0:3], v[120:123], v[224:227]\n" \
"v_mul_f32_e32 v150, v54, v150\n" \
"v_mul_f32_e32 v151, v54, v151\n" \
"v_mfma_f32_16x16x32_bf16 v[228:231], a[4:7], v[120:123], v[228:231]\n" \
"v_cvt_pk_bf16_f32 v153, v154, v155\n" \
"v_mul_f32_e32 v156, v54, v156\n" \
"v_mfma_f32_16x16x32_bf16 v[232:235], a[8:11], v[120:123], v[232:235]\n" \
"v_mul_f32_e32 v157, v54, v157\n" \
"v_mul_f32_e32 v158, v54, v158\n" \
"v_mfma_f32_16x16x32_bf16 v[236:239], a[12:15], v[120:123], v[236:239]\n" \
"v_mul_f32_e32 v159, v54, v159\n" \
"v_cvt_pk_bf16_f32 v161, v162, v163\n" \
"v_mfma_f32_16x16x32_bf16 v[240:243], a[16:19], v[120:123], v[240:243]\n" \
"v_mul_f32_e32 v164, v55, v164\n" \
"v_mul_f32_e32 v165, v55, v165\n" \
"v_mfma_f32_16x16x32_bf16 v[244:247], a[20:23], v[120:123], v[244:247]\n" \
"v_mul_f32_e32 v166, v55, v166\n" \
"v_mul_f32_e32 v167, v55, v167\n" \
"v_mfma_f32_16x16x32_bf16 v[248:251], a[24:27], v[120:123], v[248:251]\n" \
"v_cvt_pk_bf16_f32 v169, v170, v171\n" \
"s_waitcnt lgkmcnt(4)\n" \
"v_mfma_f32_16x16x32_bf16 v[252:255], a[28:31], v[120:123], v[252:255]\n" \
"v_mul_f32_e32 v172, v55, v172\n" \
"v_mul_f32_e32 v173, v55, v173\n" \
"v_mul_f32_e32 v174, v55, v174\n" \
"v_mul_f32_e32 v175, v55, v175\n" \
"v_cvt_pk_bf16_f32 v177, v178, v179\n" \
"v_mul_f32_e32 v180, v55, v180\n" \
"v_mul_f32_e32 v181, v55, v181\n" \
"v_mul_f32_e32 v182, v55, v182\n" \
"v_mul_f32_e32 v183, v55, v183\n" \
"v_cvt_pk_bf16_f32 v185, v186, v187\n" \
"v_mul_f32_e32 v188, v55, v188\n" \
"v_mul_f32_e32 v189, v55, v189\n" \
"v_mul_f32_e32 v190, v55, v190\n" \
"v_mul_f32_e32 v191, v55, v191\n" \
"v_cvt_pk_bf16_f32 v130, v132, v133\n" \
"v_cvt_pk_bf16_f32 v131, v134, v135\n" \
"buffer_store_dwordx4 v[128:131], v20, s[56:59], s68 offen offset:0 sc1\n" \
"v_cvt_pk_bf16_f32 v138, v140, v141\n" \
"v_cvt_pk_bf16_f32 v139, v142, v143\n" \
"buffer_store_dwordx4 v[136:139], v20, s[56:59], s68 offen offset:1024 sc1\n" \
"v_cvt_pk_bf16_f32 v146, v148, v149\n" \
"v_cvt_pk_bf16_f32 v147, v150, v151\n" \
"buffer_store_dwordx4 v[144:147], v20, s[56:59], s68 offen offset:2048 sc1\n" \
"v_cvt_pk_bf16_f32 v154, v156, v157\n" \
"v_cvt_pk_bf16_f32 v155, v158, v159\n" \
"buffer_store_dwordx4 v[152:155], v20, s[56:59], s68 offen offset:3072 sc1\n" \
"v_cvt_pk_bf16_f32 v162, v164, v165\n" \
"v_cvt_pk_bf16_f32 v163, v166, v167\n" \
"buffer_store_dwordx4 v[160:163], v20, s[56:59], s69 offen offset:0 sc1\n" \
"v_cvt_pk_bf16_f32 v170, v172, v173\n" \
"v_cvt_pk_bf16_f32 v171, v174, v175\n" \
"buffer_store_dwordx4 v[168:171], v20, s[56:59], s69 offen offset:1024 sc1\n" \
"v_cvt_pk_bf16_f32 v178, v180, v181\n" \
"v_cvt_pk_bf16_f32 v179, v182, v183\n" \
"buffer_store_dwordx4 v[176:179], v20, s[56:59], s69 offen offset:2048 sc1\n" \
"v_cvt_pk_bf16_f32 v186, v188, v189\n" \
"v_cvt_pk_bf16_f32 v187, v190, v191\n" \
"buffer_store_dwordx4 v[184:187], v20, s[56:59], s69 offen offset:3072 sc1\n" \
"s_add_u32 m0, s93, 0x2000\n" \
"v_mfma_f32_16x16x32_bf16 a[208:211], a[216:219], v[128:131], a[208:211]\n" \
"buffer_load_dwordx4 v8, s[80:83], s92 offen offset:0 lds\n" \
"v_mfma_f32_16x16x32_bf16 a[208:211], a[216:219], v[136:139], a[208:211]\n" \
"v_mfma_f32_32x32x16_bf16 v[128:143], a[216:219], a[216:219], 0\n" \
"v_mfma_f32_16x16x32_bf16 a[208:211], a[216:219], v[144:147], a[208:211]\n" \
"v_mfma_f32_16x16x32_bf16 a[208:211], a[216:219], v[152:155], a[208:211]\n" \
"v_mfma_f32_32x32x16_bf16 v[144:159], a[216:219], a[216:219], 0\n" \
"v_add_f32_e32 v56, v56, v24\n" \
"v_mfma_f32_16x16x32_bf16 a[208:211], a[216:219], v[160:163], a[208:211]\n" \
"v_max_f32_e32 v62, v62, v56\n" \
"v_accvgpr_read_b32 v57, a198\n" \
"s_waitcnt vmcnt(21) lgkmcnt(0)\n" \
"s_barrier\n" \
"v_mfma_f32_16x16x32_bf16 v[64:67], a[128:131], a[64:67], v[32:35]\n" \
"s_xor_b32 m0, s65, 0x18000\n" \
"v_accvgpr_read_b32 v25, a199\n" \
"v_mfma_f32_16x16x32_bf16 a[196:199], a[216:219], a[216:219], 0\n" \
"buffer_load_dwordx4 v52, s[8:11], s64 offen offset:0 sc1 lds\n" \
"v_mfma_f32_16x16x32_bf16 v[64:67], a[132:135], a[68:71], v[64:67]\n" \
"v_rcp_f32_e32 v56, v56\n" \
"v_mfma_f32_16x16x32_bf16 v[64:67], a[136:139], a[72:75], v[64:67]\n" \
"v_mfma_f32_16x16x32_bf16 a[208:211], a[216:219], v[168:171], a[208:211]\n" \
"v_mfma_f32_32x32x16_bf16 v[160:175], a[216:219], a[216:219], 0\n" \
"v_mul_f32_e32 v56, v56, v60\n" \
"v_mfma_f32_16x16x32_bf16 a[208:211], a[216:219], v[176:179], a[208:211]\n" \
"v_mul_f32_e32 v192, v56, v192\n" \
"v_mul_f32_e32 v193, v56, v193\n" \
"v_mfma_f32_16x16x32_bf16 v[64:67], a[140:143], a[76:79], v[64:67]\n" \
"buffer_load_dwordx4 v52, s[8:11], s64 offen offset:1024 sc1 lds\n" \
"v_mfma_f32_16x16x32_bf16 v[68:71], a[156:159], a[76:79], v[32:35]\n" \
"v_cvt_pk_bf16_f32 v192, v192, v193\n" \
"v_mul_f32_e32 v194, v56, v194\n" \
"v_mfma_f32_16x16x32_bf16 v[68:71], a[152:155], a[72:75], v[68:71]\n" \
"v_mul_f32_e32 v195, v56, v195\n" \
"v_cvt_pk_bf16_f32 v193, v194, v195\n" \
"v_mfma_f32_16x16x32_bf16 v[68:71], a[148:151], a[68:71], v[68:71]\n" \
"v_mfma_f32_16x16x32_bf16 a[208:211], a[216:219], v[184:187], a[208:211]\n" \
"v_mfma_f32_32x32x16_bf16 v[176:191], a[216:219], a[216:219], 0\n" \
"v_mul_f32_e32 v196, v56, v196\n" \
"v_mul_f32_e32 v197, v56, v197\n" \
"v_mul_f32_e32 v198, v56, v198\n" \
"v_mul_f32_e32 v199, v56, v199\n" \
"v_mul_f32_e32 v200, v56, v200\n" \
"v_mul_f32_e32 v201, v56, v201\n" \
"v_mfma_f32_16x16x32_bf16 v[68:71], a[144:147], a[64:67], v[68:71]\n" \
"buffer_load_dwordx4 v52, s[8:11], s64 offen offset:2048 sc1 lds\n" \
"v_mfma_f32_16x16x32_bf16 v[76:79], a[144:147], a[80:83], v[36:39]\n" \
"v_cvt_pk_bf16_f32 v194, v196, v197\n" \
"v_cvt_pk_bf16_f32 v195, v198, v199\n" \
"buffer_store_dwordx4 v[192:195], v20, s[56:59], s70 offen offset:0 sc1\n" \
"v_mfma_f32_16x16x32_bf16 v[76:79], a[148:151], a[84:87], v[76:79]\n" \
"v_cvt_pk_bf16_f32 v200, v200, v201\n" \
"v_mul_f32_e32 v202, v56, v202\n" \
"v_mfma_f32_16x16x32_bf16 v[76:79], a[152:155], a[88:91], v[76:79]\n" \
"v_mul_f32_e32 v203, v56, v203\n" \
"v_add_f32_e32 v57, v57, v25\n" \
"v_mfma_f32_16x16x32_bf16 v[76:79], a[156:159], a[92:95], v[76:79]\n" \
"buffer_load_dwordx4 v52, s[8:11], s64 offen offset:3072 sc1 lds\n" \
"v_mfma_f32_16x16x32_bf16 v[72:75], a[140:143], a[92:95], v[36:39]\n" \
"s_mov_b32 m0, s65\n" \
"v_cvt_pk_bf16_f32 v201, v202, v203\n" \
"v_mfma_f32_16x16x32_bf16 v[72:75], a[136:139], a[88:91], v[72:75]\n" \
"v_mfma_f32_16x16x32_bf16 a[208:211], a[216:219], v[192:195], a[208:211]\n" \
"v_mul_f32_e32 v204, v56, v204\n" \
"v_mul_f32_e32 v205, v56, v205\n" \
"v_mfma_f32_16x16x32_bf16 v[72:75], a[132:135], a[84:87], v[72:75]\n" \
"v_mul_f32_e32 v206, v56, v206\n" \
"v_mul_f32_e32 v207, v56, v207\n" \
"v_mfma_f32_16x16x32_bf16 v[72:75], a[128:131], a[80:83], v[72:75]\n" \
"buffer_load_dwordx4 v53, s[8:11], s64 offen offset:0 sc1 lds\n" \
"v_mfma_f32_16x16x32_bf16 v[80:83], a[128:131], a[96:99], v[40:43]\n" \
"v_mul_f32_e32 v208, v56, v208\n" \
"v_mul_f32_e32 v209, v56, v209\n" \
"v_mfma_f32_16x16x32_bf16 v[80:83], a[132:135], a[100:103], v[80:83]\n" \
"v_max_f32_e32 v62, v62, v57\n" \
"v_cvt_pk_bf16_f32 v202, v204, v205\n" \
"v_mfma_f32_16x16x32_bf16 v[80:83], a[136:139], a[104:107], v[80:83]\n" \
"buffer_load_dwordx4 v53, s[8:11], s64 offen offset:1024 sc1 lds\n" \
"v_mfma_f32_16x16x32_bf16 v[80:83], a[140:143], a[108:111], v[80:83]\n" \
"v_cvt_pk_bf16_f32 v203, v206, v207\n" \
"v_cvt_pk_bf16_f32 v208, v208, v209\n" \
"buffer_store_dwordx4 v[200:203], v20, s[56:59], s70 offen offset:1024 sc1\n" \
"v_mfma_f32_16x16x32_bf16 v[84:87], a[156:159], a[108:111], v[40:43]\n" \
"v_mul_f32_e32 v210, v56, v210\n" \
"v_mul_f32_e32 v211, v56, v211\n" \
"v_mfma_f32_16x16x32_bf16 v[84:87], a[152:155], a[104:107], v[84:87]\n" \
"v_rcp_f32_e32 v57, v57\n" \
"v_mfma_f32_16x16x32_bf16 v[84:87], a[148:151], a[100:103], v[84:87]\n" \
"buffer_load_dwordx4 v53, s[8:11], s64 offen offset:2048 sc1 lds\n" \
"v_mfma_f32_16x16x32_bf16 v[84:87], a[144:147], a[96:99], v[84:87]\n" \
"v_mfma_f32_16x16x32_bf16 a[208:211], a[216:219], v[200:203], a[208:211]\n" \
"v_mfma_f32_32x32x16_bf16 v[192:207], a[216:219], a[216:219], 0\n" \
"v_cvt_pk_bf16_f32 v209, v210, v211\n" \
"v_mul_f32_e32 v212, v56, v212\n" \
".fill (L_ab + 20480 - .) / 4, 4, 0xBF800000\n" \
"L_ap5:\n" \
"s_cbranch_execz L_ap0\n" \
"v_mul_f32_e32 v213, v56, v213\n" \
"v_mul_f32_e32 v214, v56, v214\n" \
"v_mul_f32_e32 v215, v56, v215\n" \
"v_mul_f32_e32 v216, v56, v216\n" \
"v_mfma_f32_16x16x32_bf16 v[92:95], a[144:147], a[112:115], v[44:47]\n" \
"ds_read_b128 a[144:147], v48 offset:12288\n" \
"v_mul_f32_e32 v217, v56, v217\n" \
"v_mfma_f32_16x16x32_bf16 v[92:95], a[148:151], a[116:119], v[92:95]\n" \
"ds_read_b128 a[148:151], v48 offset:13312\n" \
"v_cvt_pk_bf16_f32 v210, v212, v213\n" \
"v_mfma_f32_16x16x32_bf16 v[92:95], a[152:155], a[120:123], v[92:95]\n" \
"ds_read_b128 a[152:155], v48 offset:14336\n" \
"v_cvt_pk_bf16_f32 v211, v214, v215\n" \
"buffer_store_dwordx4 v[208:211], v20, s[56:59], s70 offen offset:2048 sc1\n" \
"v_mfma_f32_16x16x32_bf16 v[92:95], a[156:159], a[124:127], v[92:95]\n" \
"ds_read_b128 a[156:159], v48 offset:15360\n" \
"v_cvt_pk_bf16_f32 v216, v216, v217\n" \
"v_mfma_f32_16x16x32_bf16 v[88:91], a[140:143], a[124:127], v[44:47]\n" \
"ds_read_b128 a[140:143], v48 offset:11264\n" \
"v_mul_f32_e32 v218, v56, v218\n" \
"v_mfma_f32_16x16x32_bf16 v[88:91], a[136:139], a[120:123], v[88:91]\n" \
"ds_read_b128 a[136:139], v48 offset:10240\n" \
"v_mul_f32_e32 v219, v56, v219\n" \
"v_mfma_f32_16x16x32_bf16 v[88:91], a[132:135], a[116:119], v[88:91]\n" \
"ds_read_b128 a[132:135], v48 offset:9216\n" \
"v_mul_f32_e32 v57, v57, v61\n" \
"v_mfma_f32_16x16x32_bf16 v[88:91], a[128:131], a[112:115], v[88:91]\n" \
"ds_read_b128 a[128:131], v48 offset:8192\n" \
"v_mfma_f32_16x16x32_bf16 a[208:211], a[216:219], v[208:211], a[208:211]\n" \
"v_cvt_pk_bf16_f32 v217, v218, v219\n" \
"v_mul_f32_e32 v220, v56, v220\n" \
"v_mul_f32_e32 v221, v56, v221\n" \
"v_mul_f32_e32 v222, v56, v222\n" \
"v_mul_f32_e32 v223, v56, v223\n" \
"v_mul_f32_e32 v224, v57, v224\n" \
"v_mul_f32_e32 v225, v57, v225\n" \
"v_cvt_pk_bf16_f32 v218, v220, v221\n" \
"v_cvt_pk_bf16_f32 v219, v222, v223\n" \
"buffer_store_dwordx4 v[216:219], v20, s[56:59], s70 offen offset:3072 sc1\n" \
"v_cvt_pk_bf16_f32 v224, v224, v225\n" \
"v_mul_f32_e32 v226, v57, v226\n" \
"v_mul_f32_e32 v227, v57, v227\n" \
"v_mfma_f32_16x16x32_bf16 a[208:211], a[216:219], v[216:219], a[208:211]\n" \
"v_mfma_f32_32x32x16_bf16 v[208:223], a[216:219], a[216:219], 0\n" \
"v_cvt_pk_bf16_f32 v225, v226, v227\n" \
"v_mul_f32_e32 v228, v57, v228\n" \
"v_mul_f32_e32 v229, v57, v229\n" \
"v_mul_f32_e32 v230, v57, v230\n" \
"v_mul_f32_e32 v231, v57, v231\n" \
"v_mul_f32_e32 v232, v57, v232\n" \
"v_mul_f32_e32 v233, v57, v233\n" \
"v_cvt_pk_bf16_f32 v226, v228, v229\n" \
"v_cvt_pk_bf16_f32 v227, v230, v231\n" \
"buffer_store_dwordx4 v[224:227], v20, s[56:59], s71 offen offset:0 sc1\n" \
"v_cvt_pk_bf16_f32 v232, v232, v233\n" \
"v_mul_f32_e32 v234, v57, v234\n" \
"v_mul_f32_e32 v235, v57, v235\n" \
"v_mfma_f32_16x16x32_bf16 a[208:211], a[216:219], v[224:227], a[208:211]\n" \
"v_cvt_pk_bf16_f32 v233, v234, v235\n" \
"v_mul_f32_e32 v236, v57, v236\n" \
"v_mul_f32_e32 v237, v57, v237\n" \
"v_mul_f32_e32 v238, v57, v238\n" \
"v_mul_f32_e32 v239, v57, v239\n" \
"v_mul_f32_e32 v240, v57, v240\n" \
"v_mul_f32_e32 v241, v57, v241\n" \
"v_cvt_pk_bf16_f32 v234, v236, v237\n" \
"v_cvt_pk_bf16_f32 v235, v238, v239\n" \
"buffer_store_dwordx4 v[232:235], v20, s[56:59], s71 offen offset:1024 sc1\n" \
"v_cvt_pk_bf16_f32 v240, v240, v241\n" \
"v_mul_f32_e32 v242, v57, v242\n" \
"v_mul_f32_e32 v243, v57, v243\n" \
"v_mfma_f32_16x16x32_bf16 a[208:211], a[216:219], v[232:235], a[208:211]\n" \
"v_mfma_f32_32x32x16_bf16 v[224:239], a[216:219], a[216:219], 0\n" \
"v_cvt_pk_bf16_f32 v241, v242, v243\n" \
"v_mul_f32_e32 v244, v57, v244\n" \
"v_mul_f32_e32 v245, v57, v245\n" \
"v_mul_f32_e32 v246, v57, v246\n" \
"v_mul_f32_e32 v247, v57, v247\n" \
"v_mul_f32_e32 v248, v57, v248\n" \
"v_mul_f32_e32 v249, v57, v249\n" \
"v_cvt_pk_bf16_f32 v242, v244, v245\n" \
"v_cvt_pk_bf16_f32 v243, v246, v247\n" \
"buffer_store_dwordx4 v[240:243], v20, s[56:59], s71 offen offset:2048 sc1\n" \
"v_cvt_pk_bf16_f32 v248, v248, v249\n" \
"v_mul_f32_e32 v250, v57, v250\n" \
"v_mul_f32_e32 v251, v57, v251\n" \
"v_mfma_f32_16x16x32_bf16 a[208:211], a[216:219], v[240:243], a[208:211]\n" \
"v_cvt_pk_bf16_f32 v249, v250, v251\n" \
"v_mul_f32_e32 v252, v57, v252\n" \
"v_mul_f32_e32 v253, v57, v253\n" \
"v_mul_f32_e32 v254, v57, v254\n" \
"v_mul_f32_e32 v255, v57, v255\n" \
"v_cvt_pk_bf16_f32 v250, v252, v253\n" \
"v_cvt_pk_bf16_f32 v251, v254, v255\n" \
"buffer_store_dwordx4 v[248:251], v20, s[56:59], s71 offen offset:3072 sc1\n" \
"buffer_load_dwordx4 v53, s[8:11], s64 offen offset:3072 sc1 lds\n" \
"s_waitcnt vmcnt(16) lgkmcnt(0)\n" \
"s_barrier\n" \
"v_mfma_f32_16x16x32_bf16 a[208:211], a[216:219], v[248:251], a[208:211]\n" \
"v_mfma_f32_32x32x16_bf16 v[240:255], a[216:219], a[216:219], 0\n" \
"v_exp_f32_e32 v70, v70\n" \
"v_exp_f32_e32 v71, v71\n" \
"v_exp_f32_e32 v72, v72\n" \
"v_exp_f32_e32 v73, v73\n" \
"s_add_u32 s64, s64, 0x10000\n" \
"v_cmp_lt_f32_e64 s[72:73], s21, v62\n" \
"v_readlane_b32 s95, v11, 3\n" \
"v_accvgpr_read_b32 v63, a208\n" \
"v_cmp_u_f32_e32 vcc, v63, v63\n" \
"v_exp_f32_e32 v68, v68\n" \
"v_exp_f32_e32 v69, v69\n" \
"v_exp_f32_e32 v74, v74\n" \
"v_exp_f32_e32 v75, v75\n" \
"s_or_b64 vcc, s[72:73], vcc\n" \
"s_cselect_b32 s95, 0x40000000, s95\n" \
"s_cselect_b32 s94, 0x7f800000, 0\n" \
"v_cvt_pk_bf16_f32 v71, v70, v71\n" \
"v_exp_f32_e32 v66, v66\n" \
"v_exp_f32_e32 v67, v67\n" \
"v_exp_f32_e32 v64, v64\n" \
"v_exp_f32_e32 v65, v65\n" \
"v_cvt_pk_bf16_f32 v72, v72, v73\n" \
"v_exp_f32_e32 v76, v76\n" \
"v_exp_f32_e32 v77, v77\n" \
"v_exp_f32_e32 v78, v78\n" \
"v_exp_f32_e32 v79, v79\n" \
"v_cvt_pk_bf16_f32 v70, v68, v69\n" \
"v_cvt_pk_bf16_f32 v73, v74, v75\n" \
"v_add_u32_e32 v49, 0x10000, v48\n" \
"s_xor_b32 s65, s65, 0x8000\n" \
"v_cvt_pk_bf16_f32 v69, v66, v67\n" \
"v_cvt_pk_bf16_f32 v68, v64, v65\n" \
"v_cvt_pk_bf16_f32 v74, v76, v77\n" \
"v_cvt_pk_bf16_f32 v75, v78, v79\n" \
"v_xor_b32_e32 v48, 0x8000, v48\n" \
"s_sub_u32 s66, s19, 2\n" \
"s_branch L_2_%=\n" \
"L_f1_%=:\n" \
"ds_read_b128 a[160:163], v49 offset:0\n" \
"ds_read_b128 a[164:167], v49 offset:1024\n" \
"ds_read_b128 a[168:171], v49 offset:2048\n" \
"v_mfma_f32_16x16x32_bf16 v[104:107], a[128:131], a[80:83], v[36:39]\n" \
"v_cvt_pk_bf16_f32 v192, v192, v193\n" \
"v_cvt_pk_bf16_f32 v224, v224, v225\n" \
"v_cvt_pk_bf16_f32 v193, v194, v195\n" \
"v_cvt_pk_bf16_f32 v225, v226, v227\n" \
"ds_read_b128 a[172:175], v49 offset:3072\n" \
"v_cvt_pk_bf16_f32 v194, v196, v197\n" \
"v_cvt_pk_bf16_f32 v195, v198, v199\n" \
"v_cvt_pk_bf16_f32 v226, v228, v229\n" \
"v_cvt_pk_bf16_f32 v227, v230, v231\n" \
"v_mfma_f32_16x16x32_bf16 v[104:107], a[132:135], a[84:87], v[104:107]\n" \
"v_lshl_add_u32 v50, s15, 2, v20\n" \
"v_cvt_pk_bf16_f32 v196, v200, v201\n" \
"v_cvt_pk_bf16_f32 v197, v202, v203\n" \
"v_cvt_pk_bf16_f32 v198, v204, v205\n" \
"v_cvt_pk_bf16_f32 v199, v206, v207\n" \
"v_cvt_pk_bf16_f32 v228, v232, v233\n" \
"v_cvt_pk_bf16_f32 v229, v234, v235\n" \
"v_cvt_pk_bf16_f32 v230, v236, v237\n" \
"v_cvt_pk_bf16_f32 v231, v238, v239\n" \
"v_add_u32_e32 v50, 0x1a000, v50\n" \
"v_mov_b32_e32 v61, s62\n" \
"v_cvt_pk_bf16_f32 v200, v208, v209\n" \
"v_cvt_pk_bf16_f32 v201, v210, v211\n" \
"v_cvt_pk_bf16_f32 v202, v212, v213\n" \
"v_cvt_pk_bf16_f32 v203, v214, v215\n" \
"v_cvt_pk_bf16_f32 v204, v216, v217\n" \
"v_cvt_pk_bf16_f32 v205, v218, v219\n" \
"v_cvt_pk_bf16_f32 v206, v220, v221\n" \
"v_cvt_pk_bf16_f32 v207, v222, v223\n" \
"v_cvt_pk_bf16_f32 v232, v240, v241\n" \
"v_cvt_pk_bf16_f32 v233, v242, v243\n" \
"v_cvt_pk_bf16_f32 v234, v244, v245\n" \
"v_cvt_pk_bf16_f32 v235, v246, v247\n" \
"v_cvt_pk_bf16_f32 v236, v248, v249\n" \
"v_cvt_pk_bf16_f32 v237, v250, v251\n" \
"v_cvt_pk_bf16_f32 v238, v252, v253\n" \
"v_cvt_pk_bf16_f32 v239, v254, v255\n" \
"ds_write_b128 v50, v[192:195] offset:0\n" \
"v_mfma_f32_16x16x32_bf16 v[104:107], a[136:139], a[88:91], v[104:107]\n" \
"ds_read_b128 a[176:179], v49 offset:4096\n" \
"v_add_u32_e32 v56, 0x49000, v20\n" \
"v_mfma_f32_16x16x32_bf16 v[104:107], a[140:143], a[92:95], v[104:107]\n" \
"ds_write_b128 v50, v[196:199] offset:1024\n" \
"v_mfma_f32_16x16x32_bf16 v[108:111], a[156:159], a[92:95], v[36:39]\n" \
"ds_read_b128 a[180:183], v49 offset:5120\n" \
"ds_read_b128 a[184:187], v49 offset:6144\n" \
"v_mfma_f32_16x16x32_bf16 v[108:111], a[152:155], a[88:91], v[108:111]\n" \
"ds_write_b128 v50, v[200:203] offset:2048\n" \
"v_mfma_f32_16x16x32_bf16 v[108:111], a[148:151], a[84:87], v[108:111]\n" \
"v_and_b32_e32 v68, v12, v68\n" \
"v_and_b32_e32 v69, v13, v69\n" \
"v_mfma_f32_16x16x32_bf16 v[108:111], a[144:147], a[80:83], v[108:111]\n" \
"ds_write_b128 v50, v[204:207] offset:3072\n" \
"v_and_b32_e32 v70, v14, v70\n" \
"v_and_b32_e32 v71, v15, v71\n" \
"ds_read_b128 a[188:191], v49 offset:7168\n" \
"ds_read_b128 a[0:3], v20 offset:8192\n" \
"s_waitcnt lgkmcnt(9)\n" \
"v_mfma_f32_16x16x32_bf16 v[128:131], a[160:163], v[68:71], v[128:131]\n" \
"ds_write_b128 v50, v[224:227] offset:4096\n" \
"v_mfma_f32_16x16x32_bf16 v[160:163], a[160:163], v[72:75], v[160:163]\n" \
"ds_read_b128 a[4:7], v20 offset:9216\n" \
"ds_read_b128 a[8:11], v20 offset:10240\n" \
"v_mfma_f32_16x16x32_bf16 v[132:135], a[164:167], v[68:71], v[132:135]\n" \
"ds_write_b128 v50, v[228:231] offset:5120\n" \
"v_mfma_f32_16x16x32_bf16 v[164:167], a[164:167], v[72:75], v[164:167]\n" \
"ds_read_b128 a[12:15], v20 offset:11264\n" \
"ds_read_b128 a[28:31], v20 offset:15360\n" \
"v_mfma_f32_16x16x32_bf16 v[136:139], a[168:171], v[68:71], v[136:139]\n" \
"ds_write_b128 v50, v[232:235] offset:6144\n" \
"v_mfma_f32_16x16x32_bf16 v[168:171], a[168:171], v[72:75], v[168:171]\n" \
"ds_read_b128 a[24:27], v20 offset:14336\n" \
"ds_read_b128 a[20:23], v20 offset:13312\n" \
"v_mfma_f32_16x16x32_bf16 v[140:143], a[172:175], v[68:71], v[140:143]\n" \
"ds_write_b128 v50, v[236:239] offset:7168\n" \
"v_mfma_f32_16x16x32_bf16 v[172:175], a[172:175], v[72:75], v[172:175]\n" \
"ds_read_b128 a[16:19], v20 offset:12288\n" \
"s_waitcnt lgkmcnt(15)\n" \
"v_mfma_f32_16x16x32_bf16 v[144:147], a[176:179], v[68:71], v[144:147]\n" \
"v_xor_b32_e32 v48, 0x4000, v49\n" \
"ds_read_b32 v58, v18 offset:256\n" \
"v_mfma_f32_16x16x32_bf16 v[176:179], a[176:179], v[72:75], v[176:179]\n" \
"ds_read_b32 v59, v18 offset:384\n" \
"ds_read_b128 a[32:35], v48 offset:8192\n" \
"v_mfma_f32_16x16x32_bf16 v[148:151], a[180:183], v[68:71], v[148:151]\n" \
"ds_read_b128 a[36:39], v48 offset:9216\n" \
"ds_read_b128 a[40:43], v48 offset:10240\n" \
"v_mfma_f32_16x16x32_bf16 v[180:183], a[180:183], v[72:75], v[180:183]\n" \
"ds_read_b128 a[44:47], v48 offset:11264\n" \
"ds_read_b128 a[48:51], v48 offset:12288\n" \
"v_mfma_f32_16x16x32_bf16 v[152:155], a[184:187], v[68:71], v[152:155]\n" \
"ds_read_b128 a[52:55], v48 offset:13312\n" \
"ds_read_b128 a[56:59], v48 offset:14336\n" \
"v_mfma_f32_16x16x32_bf16 v[184:187], a[184:187], v[72:75], v[184:187]\n" \
"ds_read_b128 a[60:63], v48 offset:15360\n" \
"s_waitcnt lgkmcnt(15)\n" \
"v_mfma_f32_16x16x32_bf16 v[156:159], a[188:191], v[68:71], v[156:159]\n" \
"ds_read_b128 a[128:131], v49 offset:8192\n" \
"ds_read_b128 a[132:135], v49 offset:9216\n" \
"v_mfma_f32_16x16x32_bf16 v[188:191], a[188:191], v[72:75], v[188:191]\n" \
"ds_read_b128 a[136:139], v49 offset:10240\n" \
"ds_read_b128 a[140:143], v49 offset:11264\n" \
"v_smfmac_f32_16x16x64_bf16 a[192:195], a[200:203], v[68:75], v21\n" \
"ds_read_b128 a[144:147], v49 offset:12288\n" \
"ds_read_b128 a[148:151], v49 offset:13312\n" \
"v_mfma_f32_16x16x32_bf16 v[112:115], a[0:3], a[64:67], v[32:35]\n" \
"ds_read_b128 a[152:155], v49 offset:14336\n" \
"ds_read_b128 a[156:159], v49 offset:15360\n" \
"v_mfma_f32_16x16x32_bf16 v[112:115], a[4:7], a[68:71], v[112:115]\n" \
"v_accvgpr_read_b32 v40, a196\n" \
"v_accvgpr_read_b32 v24, a197\n" \
"v_mfma_f32_16x16x32_bf16 v[112:115], a[8:11], a[72:75], v[112:115]\n" \
"v_accvgpr_read_b32 v41, a198\n" \
"v_accvgpr_read_b32 v25, a199\n" \
"v_mfma_f32_16x16x32_bf16 v[112:115], a[12:15], a[76:79], v[112:115]\n" \
"v_add_f32_e32 v40, v40, v24\n" \
"v_add_f32_e32 v41, v41, v25\n" \
"v_mfma_f32_16x16x32_bf16 v[116:119], a[28:31], a[76:79], v[32:35]\n" \
"v_add_f32_e32 v40, s94, v40\n" \
"s_waitcnt lgkmcnt(15)\n" \
"v_mfma_f32_16x16x32_bf16 v[116:119], a[24:27], a[72:75], v[116:119]\n" \
"v_add_f32_e32 v41, s94, v41\n" \
"ds_write_b64 v19, v[40:41]\n" \
"v_mfma_f32_16x16x32_bf16 v[116:119], a[20:23], a[68:71], v[116:119]\n" \
"v_add_u32_e32 v52, s15, v20\n" \
"v_add_u32_e32 v51, 0x10000, v52\n" \
"v_mfma_f32_16x16x32_bf16 v[116:119], a[16:19], a[64:67], v[116:119]\n" \
"v_exp_f32_e32 v104, v104\n" \
"v_mfma_f32_16x16x32_bf16 v[124:127], a[16:19], a[80:83], v[36:39]\n" \
"v_exp_f32_e32 v105, v105\n" \
"v_mfma_f32_16x16x32_bf16 v[124:127], a[20:23], a[84:87], v[124:127]\n" \
"v_exp_f32_e32 v106, v106\n" \
"v_mfma_f32_16x16x32_bf16 v[124:127], a[24:27], a[88:91], v[124:127]\n" \
"v_exp_f32_e32 v107, v107\n" \
"v_mfma_f32_16x16x32_bf16 v[124:127], a[28:31], a[92:95], v[124:127]\n" \
"v_cvt_pk_bf16_f32 v104, v104, v105\n" \
"v_cvt_pk_bf16_f32 v105, v106, v107\n" \
"v_mfma_f32_16x16x32_bf16 v[120:123], a[12:15], a[92:95], v[36:39]\n" \
"v_exp_f32_e32 v108, v108\n" \
"v_mfma_f32_16x16x32_bf16 v[120:123], a[8:11], a[88:91], v[120:123]\n" \
"v_exp_f32_e32 v109, v109\n" \
"v_mfma_f32_16x16x32_bf16 v[120:123], a[4:7], a[84:87], v[120:123]\n" \
"v_exp_f32_e32 v110, v110\n" \
"v_mfma_f32_16x16x32_bf16 v[120:123], a[0:3], a[80:83], v[120:123]\n" \
"v_exp_f32_e32 v111, v111\n" \
"s_waitcnt lgkmcnt(0)\n" \
"s_barrier\n" \
"ds_read_b128 v[208:211], v52 offset:0\n" \
"ds_read_b128 v[212:215], v52 offset:1024\n" \
"ds_read_b128 v[216:219], v52 offset:2048\n" \
"ds_read_b128 v[220:223], v52 offset:3072\n" \
"ds_read_b128 v[240:243], v51 offset:32768\n" \
"ds_read_b128 v[244:247], v51 offset:33792\n" \
"ds_read_b128 v[248:251], v51 offset:34816\n" \
"ds_read_b128 v[252:255], v51 offset:35840\n" \
"v_cvt_pk_bf16_f32 v106, v108, v109\n" \
"v_cvt_pk_bf16_f32 v107, v110, v111\n" \
"v_and_b32_e32 v104, v12, v104\n" \
"v_and_b32_e32 v105, v13, v105\n" \
"v_and_b32_e32 v106, v14, v106\n" \
"s_waitcnt vmcnt(0)\n" \
"s_mov_b64 exec, 1\n" \
"buffer_store_dword v61, off, s[56:59], s95 offset:0 sc1\n" \
"s_mov_b64 exec, -1\n" \
"v_and_b32_e32 v107, v15, v107\n" \
"v_xor_b32_e32 v63, 0x400, v19\n" \
"ds_read_b64 v[42:43], v63\n" \
"v_mfma_f32_16x16x32_bf16 a[192:195], a[212:215], v[104:107], a[192:195]\n" \
"v_mov_b32_e32 v194, v28\n" \
"v_mov_b32_e32 v195, v29\n" \
"v_mfma_f32_16x16x32_bf16 v[160:163], a[128:131], v[104:107], v[160:163]\n" \
"v_mov_b32_e32 v196, 0\n" \
"v_mov_b32_e32 v197, 0\n" \
"v_mfma_f32_16x16x32_bf16 v[164:167], a[132:135], v[104:107], v[164:167]\n" \
"v_mov_b32_e32 v192, 0\n" \
"v_mov_b32_e32 v193, 0\n" \
"v_mfma_f32_16x16x32_bf16 v[168:171], a[136:139], v[104:107], v[168:171]\n" \
"v_readlane_b32 s93, v11, 22\n" \
"v_readlane_b32 s92, v11, 27\n" \
"v_mfma_f32_16x16x32_bf16 v[172:175], a[140:143], v[104:107], v[172:175]\n" \
"buffer_load_dwordx4 a[96:99], v20, s[56:59], s60 offen offset:0 sc1\n" \
"v_mfma_f32_16x16x32_bf16 v[176:179], a[144:147], v[104:107], v[176:179]\n" \
"v_exp_f32_e32 v118, v118\n" \
"v_mfma_f32_16x16x32_bf16 v[180:183], a[148:151], v[104:107], v[180:183]\n" \
"v_exp_f32_e32 v119, v119\n" \
"v_mfma_f32_16x16x32_bf16 v[184:187], a[152:155], v[104:107], v[184:187]\n" \
"s_add_u32 m0, s93, 0x0\n" \
"v_cvt_pk_bf16_f32 v119, v118, v119\n" \
"v_mfma_f32_16x16x32_bf16 v[188:191], a[156:159], v[104:107], v[188:191]\n" \
"v_exp_f32_e32 v120, v120\n" \
"s_waitcnt lgkmcnt(1)\n" \
"s_barrier\n" \
"buffer_load_dwordx4 a[112:115], v56, s[56:59], s60 offen offset:0 sc1\n" \
"v_mfma_f32_16x16x32_bf16 v[128:131], v[194:197], v[208:211], v[128:131]\n" \
"v_exp_f32_e32 v121, v121\n" \
"v_mfma_f32_16x16x32_bf16 v[132:135], v[192:195], v[208:211], v[132:135]\n" \
"v_exp_f32_e32 v116, v116\n" \
"v_mfma_f32_16x16x32_bf16 v[136:139], v[194:197], v[212:215], v[136:139]\n" \
"v_exp_f32_e32 v117, v117\n" \
"v_mfma_f32_16x16x32_bf16 v[140:143], v[192:195], v[212:215], v[140:143]\n" \
"v_exp_f32_e32 v122, v122\n" \
"v_mfma_f32_16x16x32_bf16 v[144:147], v[194:197], v[216:219], v[144:147]\n" \
"v_exp_f32_e32 v123, v123\n" \
"v_mfma_f32_16x16x32_bf16 v[148:151], v[192:195], v[216:219], v[148:151]\n" \
"buffer_load_dwordx4 a[100:103], v20, s[56:59], s60 offen offset:1024 sc1\n" \
"v_mfma_f32_16x16x32_bf16 v[152:155], v[194:197], v[220:223], v[152:155]\n" \
"v_exp_f32_e32 v114, v114\n" \
"v_mfma_f32_16x16x32_bf16 v[156:159], v[192:195], v[220:223], v[156:159]\n" \
"v_exp_f32_e32 v115, v115\n" \
"v_mfma_f32_16x16x32_bf16 v[160:163], v[194:197], v[240:243], v[160:163]\n" \
"v_exp_f32_e32 v112, v112\n" \
"v_mfma_f32_16x16x32_bf16 v[164:167], v[192:195], v[240:243], v[164:167]\n" \
"v_exp_f32_e32 v113, v113\n" \
"v_mfma_f32_16x16x32_bf16 v[168:171], v[194:197], v[244:247], v[168:171]\n" \
"v_cvt_pk_bf16_f32 v120, v120, v121\n" \
"v_cvt_pk_bf16_f32 v118, v116, v117\n" \
"v_mfma_f32_16x16x32_bf16 v[172:175], v[192:195], v[244:247], v[172:175]\n" \
"buffer_load_dwordx4 a[116:119], v56, s[56:59], s60 offen offset:1024 sc1\n" \
"v_mfma_f32_16x16x32_bf16 v[176:179], v[194:197], v[248:251], v[176:179]\n" \
"v_exp_f32_e32 v124, v124\n" \
"v_mfma_f32_16x16x32_bf16 v[180:183], v[192:195], v[248:251], v[180:183]\n" \
"v_exp_f32_e32 v125, v125\n" \
"v_mfma_f32_16x16x32_bf16 v[184:187], v[194:197], v[252:255], v[184:187]\n" \
"v_exp_f32_e32 v126, v126\n" \
"v_mfma_f32_16x16x32_bf16 v[188:191], v[192:195], v[252:255], v[188:191]\n" \
"v_exp_f32_e32 v127, v127\n" \
"v_cvt_pk_bf16_f32 v121, v122, v123\n" \
"v_cvt_pk_bf16_f32 v117, v114, v115\n" \
"v_cvt_pk_bf16_f32 v116, v112, v113\n" \
"v_cvt_pk_bf16_f32 v122, v124, v125\n" \
"v_cvt_pk_bf16_f32 v123, v126, v127\n" \
"buffer_load_dwordx4 a[104:107], v20, s[56:59], s60 offen offset:2048 sc1\n" \
"s_nop 0\n" \
"v_smfmac_f32_16x16x64_bf16 a[192:195], a[200:203], v[116:123], v21\n" \
"v_mfma_f32_16x16x32_bf16 v[128:131], a[32:35], v[116:119], v[128:131]\n" \
"v_mfma_f32_16x16x32_bf16 v[160:163], a[32:35], v[120:123], v[160:163]\n" \
"v_mfma_f32_16x16x32_bf16 v[132:135], a[36:39], v[116:119], v[132:135]\n" \
"v_mfma_f32_16x16x32_bf16 v[164:167], a[36:39], v[120:123], v[164:167]\n" \
"v_mfma_f32_16x16x32_bf16 v[136:139], a[40:43], v[116:119], v[136:139]\n" \
"buffer_load_dwordx4 a[120:123], v56, s[56:59], s60 offen offset:2048 sc1\n" \
"v_mfma_f32_16x16x32_bf16 v[168:171], a[40:43], v[120:123], v[168:171]\n" \
"v_mfma_f32_16x16x32_bf16 v[140:143], a[44:47], v[116:119], v[140:143]\n" \
"v_accvgpr_read_b32 v54, a192\n" \
"v_accvgpr_read_b32 v22, a193\n" \
"v_mfma_f32_16x16x32_bf16 v[172:175], a[44:47], v[120:123], v[172:175]\n" \
"v_add_f32_e32 v54, v54, v22\n" \
"s_waitcnt lgkmcnt(0)\n" \
"v_add_f32_e32 v54, v54, v42\n" \
"v_mfma_f32_16x16x32_bf16 v[144:147], a[48:51], v[116:119], v[144:147]\n" \
"v_max_f32_e32 v62, v62, v54\n" \
"v_accvgpr_read_b32 v55, a194\n" \
"v_mfma_f32_16x16x32_bf16 v[176:179], a[48:51], v[120:123], v[176:179]\n" \
"v_rcp_f32_e32 v54, v54\n" \
"buffer_load_dwordx4 a[108:111], v20, s[56:59], s60 offen offset:3072 sc1\n" \
"v_mfma_f32_16x16x32_bf16 v[148:151], a[52:55], v[116:119], v[148:151]\n" \
"v_mul_f32_e32 v54, v54, v58\n" \
"v_mul_f32_e32 v128, v54, v128\n" \
"v_mfma_f32_16x16x32_bf16 v[180:183], a[52:55], v[120:123], v[180:183]\n" \
"v_mul_f32_e32 v129, v54, v129\n" \
"v_mul_f32_e32 v130, v54, v130\n" \
"v_mul_f32_e32 v131, v54, v131\n" \
"v_mul_f32_e32 v132, v54, v132\n" \
"v_mul_f32_e32 v133, v54, v133\n" \
"v_mul_f32_e32 v134, v54, v134\n" \
"v_mul_f32_e32 v135, v54, v135\n" \
"v_cvt_pk_bf16_f32 v64, v128, v129\n" \
"v_cvt_pk_bf16_f32 v65, v130, v131\n" \
"v_cvt_pk_bf16_f32 v66, v132, v133\n" \
"v_cvt_pk_bf16_f32 v67, v134, v135\n" \
"v_accvgpr_read_b32 v23, a195\n" \
"v_mfma_f32_16x16x32_bf16 v[152:155], a[56:59], v[116:119], v[152:155]\n" \
"v_mfma_f32_16x16x32_bf16 a[208:211], a[216:219], v[64:67], a[208:211]\n" \
"buffer_load_dwordx4 a[124:127], v56, s[56:59], s60 offen offset:3072 sc1\n" \
"v_mul_f32_e32 v136, v54, v136\n" \
"v_mul_f32_e32 v137, v54, v137\n" \
"v_mul_f32_e32 v138, v54, v138\n" \
"v_mul_f32_e32 v139, v54, v139\n" \
"v_mul_f32_e32 v140, v54, v140\n" \
"v_mul_f32_e32 v141, v54, v141\n" \
"v_mul_f32_e32 v142, v54, v142\n" \
"v_mul_f32_e32 v143, v54, v143\n" \
"v_add_f32_e32 v55, v55, v23\n" \
"v_cvt_pk_bf16_f32 v68, v136, v137\n" \
"v_cvt_pk_bf16_f32 v69, v138, v139\n" \
"v_cvt_pk_bf16_f32 v70, v140, v141\n" \
"v_cvt_pk_bf16_f32 v71, v142, v143\n" \
"v_add_f32_e32 v55, v55, v43\n" \
"v_mul_f32_e32 v144, v54, v144\n" \
"v_mfma_f32_16x16x32_bf16 a[208:211], a[216:219], v[68:71], a[208:211]\n" \
"v_mul_f32_e32 v145, v54, v145\n" \
"v_mul_f32_e32 v146, v54, v146\n" \
"v_mul_f32_e32 v147, v54, v147\n" \
"v_mul_f32_e32 v148, v54, v148\n" \
"buffer_load_dwordx4 v0, s[80:83], s92 offen offset:0 lds\n" \
"v_mul_f32_e32 v149, v54, v149\n" \
"v_mul_f32_e32 v150, v54, v150\n" \
"v_mul_f32_e32 v151, v54, v151\n" \
"v_max_f32_e32 v62, v62, v55\n" \
"v_cvt_pk_bf16_f32 v72, v144, v145\n" \
"v_cvt_pk_bf16_f32 v73, v146, v147\n" \
"v_cvt_pk_bf16_f32 v74, v148, v149\n" \
"v_cvt_pk_bf16_f32 v75, v150, v151\n" \
"v_rcp_f32_e32 v55, v55\n" \
"v_mfma_f32_16x16x32_bf16 v[184:187], a[56:59], v[120:123], v[184:187]\n" \
"v_mfma_f32_16x16x32_bf16 a[208:211], a[216:219], v[72:75], a[208:211]\n" \
"v_mul_f32_e32 v152, v54, v152\n" \
"v_mul_f32_e32 v153, v54, v153\n" \
"v_mul_f32_e32 v154, v54, v154\n" \
"v_mul_f32_e32 v155, v54, v155\n" \
"v_cvt_pk_bf16_f32 v76, v152, v153\n" \
"buffer_load_dwordx4 v1, s[80:83], s92 offen offset:1024 lds\n" \
"v_cvt_pk_bf16_f32 v77, v154, v155\n" \
"v_mul_f32_e32 v55, v55, v59\n" \
"v_mul_f32_e32 v160, v55, v160\n" \
"v_mul_f32_e32 v161, v55, v161\n" \
"v_mul_f32_e32 v162, v55, v162\n" \
"v_mul_f32_e32 v163, v55, v163\n" \
"v_mul_f32_e32 v164, v55, v164\n" \
"v_mul_f32_e32 v165, v55, v165\n" \
"v_mul_f32_e32 v166, v55, v166\n" \
"v_mul_f32_e32 v167, v55, v167\n" \
"v_cvt_pk_bf16_f32 v80, v160, v161\n" \
"v_cvt_pk_bf16_f32 v81, v162, v163\n" \
"v_cvt_pk_bf16_f32 v82, v164, v165\n" \
"v_cvt_pk_bf16_f32 v83, v166, v167\n" \
"v_mfma_f32_16x16x32_bf16 v[156:159], a[60:63], v[116:119], v[156:159]\n" \
"v_mul_f32_e32 v168, v55, v168\n" \
"v_mul_f32_e32 v169, v55, v169\n" \
"v_mul_f32_e32 v170, v55, v170\n" \
"v_mul_f32_e32 v171, v55, v171\n" \
"v_mul_f32_e32 v172, v55, v172\n" \
"buffer_load_dwordx4 v2, s[80:83], s92 offen offset:2048 lds\n" \
"v_mul_f32_e32 v173, v55, v173\n" \
"v_mul_f32_e32 v174, v55, v174\n" \
"v_mul_f32_e32 v156, v54, v156\n" \
"v_mul_f32_e32 v157, v54, v157\n" \
"v_mul_f32_e32 v158, v54, v158\n" \
"v_mul_f32_e32 v159, v54, v159\n" \
"v_cvt_pk_bf16_f32 v78, v156, v157\n" \
"v_cvt_pk_bf16_f32 v79, v158, v159\n" \
"v_mul_f32_e32 v175, v55, v175\n" \
"v_cvt_pk_bf16_f32 v84, v168, v169\n" \
"v_mfma_f32_16x16x32_bf16 a[208:211], a[216:219], v[76:79], a[208:211]\n" \
"v_mfma_f32_16x16x32_bf16 a[208:211], a[216:219], v[80:83], a[208:211]\n" \
"v_cvt_pk_bf16_f32 v85, v170, v171\n" \
"v_cvt_pk_bf16_f32 v86, v172, v173\n" \
"v_cvt_pk_bf16_f32 v87, v174, v175\n" \
"v_mul_f32_e32 v176, v55, v176\n" \
"v_mul_f32_e32 v177, v55, v177\n" \
"buffer_load_dwordx4 v3, s[80:83], s92 offen offset:3072 lds\n" \
"s_add_u32 m0, s93, 0x1000\n" \
"v_mfma_f32_16x16x32_bf16 a[208:211], a[216:219], v[84:87], a[208:211]\n" \
"v_mul_f32_e32 v178, v55, v178\n" \
"v_mul_f32_e32 v179, v55, v179\n" \
"v_mul_f32_e32 v180, v55, v180\n" \
"v_mul_f32_e32 v181, v55, v181\n" \
"v_mul_f32_e32 v182, v55, v182\n" \
"v_mul_f32_e32 v183, v55, v183\n" \
"v_cvt_pk_bf16_f32 v96, v176, v177\n" \
"v_cvt_pk_bf16_f32 v97, v178, v179\n" \
"v_cvt_pk_bf16_f32 v98, v180, v181\n" \
"v_cvt_pk_bf16_f32 v99, v182, v183\n" \
"v_mfma_f32_16x16x32_bf16 v[188:191], a[60:63], v[120:123], v[188:191]\n" \
"v_mul_f32_e32 v184, v55, v184\n" \
"v_mfma_f32_16x16x32_bf16 a[208:211], a[216:219], v[96:99], a[208:211]\n" \
"v_mul_f32_e32 v185, v55, v185\n" \
"v_mul_f32_e32 v186, v55, v186\n" \
"buffer_load_dwordx4 v4, s[80:83], s92 offen offset:0 lds\n" \
"v_mul_f32_e32 v187, v55, v187\n" \
"v_cvt_pk_bf16_f32 v100, v184, v185\n" \
"v_cvt_pk_bf16_f32 v101, v186, v187\n" \
"v_mul_f32_e32 v188, v55, v188\n" \
"v_mul_f32_e32 v189, v55, v189\n" \
"v_mul_f32_e32 v190, v55, v190\n" \
"v_mul_f32_e32 v191, v55, v191\n" \
"v_cvt_pk_bf16_f32 v102, v188, v189\n" \
"v_cvt_pk_bf16_f32 v103, v190, v191\n" \
"v_cmp_lt_f32_e64 s[72:73], s21, v62\n" \
"buffer_load_dwordx4 v5, s[80:83], s92 offen offset:1024 lds\n" \
"v_mfma_f32_16x16x32_bf16 a[208:211], a[216:219], v[100:103], a[208:211]\n" \
"buffer_load_dwordx4 v6, s[80:83], s92 offen offset:2048 lds\n" \
"buffer_load_dwordx4 v7, s[80:83], s92 offen offset:3072 lds\n" \
"s_add_u32 m0, s93, 0x2000\n" \
"s_nop 0\n" \
"buffer_load_dwordx4 v8, s[80:83], s92 offen offset:0 lds\n" \
"s_nop 2\n" \
"v_accvgpr_read_b32 v63, a208\n" \
"v_cmp_u_f32_e32 vcc, v63, v63\n" \
"s_nop 0\n" \
"s_or_b64 vcc, s[72:73], vcc\n" \
"s_cbranch_scc0 L_out_%=\n" \
"s_branch L_end_%=\n" \
".fill (L_ab + 24576 - .) / 4, 4, 0xBF800000\n" \
".fill (L_ab + 24576 - .) / 4, 4, 0xBF800000\n" \
"L_ap6:\n" \
"s_setpc_b64 s[88:89]\n" \
"L_gs7:\n" \
"v_mfma_f32_16x16x32_bf16 v[104:107], a[128:131], a[16:19], v[36:39]\n" \
"s_add_u32 s68, s60, 0xfff6e000\n" \
"s_add_u32 s69, s60, 0xfffb7000\n" \
"s_add_u32 s70, s60, 0x0\n" \
"s_add_u32 s71, s60, 0x49000\n" \
"v_mfma_f32_16x16x32_bf16 v[104:107], a[132:135], a[20:23], v[104:107]\n" \
"s_xor_b32 m0, s65, 0x12000\n" \
"ds_read_b128 a[160:163], v49 offset:0\n" \
"v_mfma_f32_16x16x32_bf16 v[104:107], a[136:139], a[24:27], v[104:107]\n" \
"buffer_load_dwordx4 v50, s[8:11], s64 offen offset:0 sc1 lds\n" \
"v_mfma_f32_16x16x32_bf16 v[104:107], a[140:143], a[28:31], v[104:107]\n" \
"ds_read_b128 a[164:167], v49 offset:1024\n" \
"ds_read_b128 a[168:171], v49 offset:2048\n" \
"v_mfma_f32_16x16x32_bf16 v[108:111], a[156:159], a[28:31], v[36:39]\n" \
"ds_read_b128 a[172:175], v49 offset:3072\n" \
"ds_read_b128 a[176:179], v49 offset:4096\n" \
"v_mfma_f32_16x16x32_bf16 v[108:111], a[152:155], a[24:27], v[108:111]\n" \
"s_mov_b32 s65, s13\n" \
"v_add_u32_e32 v63, s15, v20\n" \
"v_mfma_f32_16x16x32_bf16 v[108:111], a[148:151], a[20:23], v[108:111]\n" \
"buffer_load_dwordx4 v50, s[8:11], s64 offen offset:1024 sc1 lds\n" \
"v_mfma_f32_16x16x32_bf16 v[108:111], a[144:147], a[16:19], v[108:111]\n" \
"s_add_u32 s67, s14, 40960\n" \
"v_add_u32_e32 v50, 0x6000, v63\n" \
"v_mfma_f32_16x16x32_bf16 v[116:119], a[144:147], a[32:35], v[40:43]\n" \
"s_xor_b32 m0, s65, 0x8000\n" \
"ds_read_b128 a[180:183], v49 offset:5120\n" \
"v_mfma_f32_16x16x32_bf16 v[116:119], a[148:151], a[36:39], v[116:119]\n" \
"ds_read_b128 a[184:187], v49 offset:6144\n" \
"v_and_b32_e32 v68, v12, v68\n" \
"v_mfma_f32_16x16x32_bf16 v[116:119], a[152:155], a[40:43], v[116:119]\n" \
"v_and_b32_e32 v69, v13, v69\n" \
"v_and_b32_e32 v70, v14, v70\n" \
"v_mfma_f32_16x16x32_bf16 v[116:119], a[156:159], a[44:47], v[116:119]\n" \
"buffer_load_dwordx4 v50, s[8:11], s67 offen offset:0 sc1 lds\n" \
"v_mfma_f32_16x16x32_bf16 v[112:115], a[140:143], a[44:47], v[40:43]\n" \
"v_and_b32_e32 v71, v15, v71\n" \
"ds_read_b128 a[188:191], v49 offset:7168\n" \
"v_mfma_f32_16x16x32_bf16 v[112:115], a[136:139], a[40:43], v[112:115]\n" \
"v_exp_f32_e32 v86, v86\n" \
"v_mfma_f32_16x16x32_bf16 v[112:115], a[132:135], a[36:39], v[112:115]\n" \
"v_exp_f32_e32 v87, v87\n" \
"v_mfma_f32_16x16x32_bf16 v[112:115], a[128:131], a[32:35], v[112:115]\n" \
"v_exp_f32_e32 v84, v84\n" \
"v_mfma_f32_16x16x32_bf16 v[120:123], a[128:131], a[48:51], v[44:47]\n" \
"buffer_load_dwordx4 v50, s[8:11], s67 offen offset:1024 sc1 lds\n" \
"v_mfma_f32_16x16x32_bf16 v[120:123], a[132:135], a[52:55], v[120:123]\n" \
"v_exp_f32_e32 v85, v85\n" \
"v_mfma_f32_16x16x32_bf16 v[120:123], a[136:139], a[56:59], v[120:123]\n" \
"v_cvt_pk_bf16_f32 v87, v86, v87\n" \
"v_cvt_pk_bf16_f32 v86, v84, v85\n" \
"v_mfma_f32_16x16x32_bf16 v[120:123], a[140:143], a[60:63], v[120:123]\n" \
"v_exp_f32_e32 v82, v82\n" \
"v_mfma_f32_16x16x32_bf16 v[124:127], a[156:159], a[60:63], v[44:47]\n" \
"v_exp_f32_e32 v83, v83\n" \
"v_mfma_f32_16x16x32_bf16 v[124:127], a[152:155], a[56:59], v[124:127]\n" \
"v_exp_f32_e32 v80, v80\n" \
"v_mfma_f32_16x16x32_bf16 v[124:127], a[148:151], a[52:55], v[124:127]\n" \
"buffer_load_dwordx4 v50, s[8:11], s67 offen offset:2048 sc1 lds\n" \
"v_mfma_f32_16x16x32_bf16 v[124:127], a[144:147], a[48:51], v[124:127]\n" \
"v_cvt_pk_bf16_f32 v85, v82, v83\n" \
"s_waitcnt lgkmcnt(1)\n" \
"v_mfma_f32_16x16x32_bf16 v[128:131], a[160:163], v[68:71], v[128:131]\n" \
"v_exp_f32_e32 v81, v81\n" \
"v_mfma_f32_16x16x32_bf16 v[160:163], a[160:163], v[72:75], v[160:163]\n" \
"v_cvt_pk_bf16_f32 v84, v80, v81\n" \
"s_mov_b32 s64, s14\n" \
"v_mfma_f32_16x16x32_bf16 v[132:135], a[164:167], v[68:71], v[132:135]\n" \
"v_exp_f32_e32 v88, v88\n" \
"v_mfma_f32_16x16x32_bf16 v[164:167], a[164:167], v[72:75], v[164:167]\n" \
"v_exp_f32_e32 v89, v89\n" \
"v_mfma_f32_16x16x32_bf16 v[136:139], a[168:171], v[68:71], v[136:139]\n" \
"buffer_load_dwordx4 v50, s[8:11], s67 offen offset:3072 sc1 lds\n" \
"v_mfma_f32_16x16x32_bf16 v[168:171], a[168:171], v[72:75], v[168:171]\n" \
"v_exp_f32_e32 v90, v90\n" \
"v_mfma_f32_16x16x32_bf16 v[140:143], a[172:175], v[68:71], v[140:143]\n" \
"v_exp_f32_e32 v91, v91\n" \
"v_mfma_f32_16x16x32_bf16 v[172:175], a[172:175], v[72:75], v[172:175]\n" \
"v_cvt_pk_bf16_f32 v88, v88, v89\n" \
"v_cvt_pk_bf16_f32 v89, v90, v91\n" \
"v_mfma_f32_16x16x32_bf16 v[144:147], a[176:179], v[68:71], v[144:147]\n" \
"v_exp_f32_e32 v92, v92\n" \
"v_mfma_f32_16x16x32_bf16 v[176:179], a[176:179], v[72:75], v[176:179]\n" \
"v_exp_f32_e32 v93, v93\n" \
"v_mfma_f32_16x16x32_bf16 v[148:151], a[180:183], v[68:71], v[148:151]\n" \
"v_exp_f32_e32 v94, v94\n" \
"v_mfma_f32_16x16x32_bf16 v[180:183], a[180:183], v[72:75], v[180:183]\n" \
"v_exp_f32_e32 v95, v95\n" \
"v_mfma_f32_16x16x32_bf16 v[152:155], a[184:187], v[68:71], v[152:155]\n" \
"v_cvt_pk_bf16_f32 v90, v92, v93\n" \
"v_cvt_pk_bf16_f32 v91, v94, v95\n" \
"v_mfma_f32_16x16x32_bf16 v[184:187], a[184:187], v[72:75], v[184:187]\n" \
"v_add_u32_e32 v51, 0x12000, v63\n" \
"s_waitcnt lgkmcnt(0)\n" \
"v_mfma_f32_16x16x32_bf16 v[156:159], a[188:191], v[68:71], v[156:159]\n" \
"s_xor_b32 m0, s65, 0xa000\n" \
"ds_read_b32 v58, v18 offset:512\n" \
"v_mfma_f32_16x16x32_bf16 v[188:191], a[188:191], v[72:75], v[188:191]\n" \
"buffer_load_dwordx4 v51, s[8:11], s64 offen offset:0 sc1 lds\n" \
"v_smfmac_f32_16x16x64_bf16 a[192:195], a[200:203], v[68:75], v21\n" \
"ds_read_b32 v59, v18 offset:640\n" \
"ds_read_b32 v60, v18 offset:768\n" \
"v_mfma_f32_16x16x32_bf16 v[220:223], a[188:191], v[84:87], v[220:223]\n" \
"ds_read_b32 v61, v18 offset:896\n" \
"ds_read_b128 a[128:131], v48 offset:0\n" \
"v_mfma_f32_16x16x32_bf16 v[252:255], a[188:191], v[88:91], v[252:255]\n" \
"ds_read_b128 a[144:147], v48 offset:4096\n" \
"ds_read_b128 a[132:135], v48 offset:1024\n" \
"v_mfma_f32_16x16x32_bf16 v[216:219], a[184:187], v[84:87], v[216:219]\n" \
"buffer_load_dwordx4 v51, s[8:11], s64 offen offset:1024 sc1 lds\n" \
"v_mfma_f32_16x16x32_bf16 v[248:251], a[184:187], v[88:91], v[248:251]\n" \
"ds_read_b128 a[148:151], v48 offset:5120\n" \
"ds_read_b128 a[136:139], v48 offset:2048\n" \
"v_mfma_f32_16x16x32_bf16 v[212:215], a[180:183], v[84:87], v[212:215]\n" \
"ds_read_b128 a[152:155], v48 offset:6144\n" \
"ds_read_b128 a[140:143], v48 offset:3072\n" \
"v_mfma_f32_16x16x32_bf16 v[244:247], a[180:183], v[88:91], v[244:247]\n" \
"ds_read_b128 a[156:159], v48 offset:7168\n" \
"v_add_u32_e32 v52, 0x14000, v63\n" \
"v_mfma_f32_16x16x32_bf16 v[208:211], a[176:179], v[84:87], v[208:211]\n" \
"buffer_load_dwordx4 v51, s[8:11], s64 offen offset:2048 sc1 lds\n" \
"v_mfma_f32_16x16x32_bf16 v[240:243], a[176:179], v[88:91], v[240:243]\n" \
"v_exp_f32_e32 v104, v104\n" \
"v_mfma_f32_16x16x32_bf16 v[204:207], a[172:175], v[84:87], v[204:207]\n" \
"v_exp_f32_e32 v105, v105\n" \
"v_mfma_f32_16x16x32_bf16 v[236:239], a[172:175], v[88:91], v[236:239]\n" \
"v_exp_f32_e32 v106, v106\n" \
"v_mfma_f32_16x16x32_bf16 v[200:203], a[168:171], v[84:87], v[200:203]\n" \
"buffer_load_dwordx4 v51, s[8:11], s64 offen offset:3072 sc1 lds\n" \
"v_mfma_f32_16x16x32_bf16 v[232:235], a[168:171], v[88:91], v[232:235]\n" \
"v_exp_f32_e32 v107, v107\n" \
"v_mfma_f32_16x16x32_bf16 v[196:199], a[164:167], v[84:87], v[196:199]\n" \
"v_cvt_pk_bf16_f32 v104, v104, v105\n" \
"v_cvt_pk_bf16_f32 v105, v106, v107\n" \
"v_mfma_f32_16x16x32_bf16 v[228:231], a[164:167], v[88:91], v[228:231]\n" \
"v_exp_f32_e32 v108, v108\n" \
"v_mfma_f32_16x16x32_bf16 v[192:195], a[160:163], v[84:87], v[192:195]\n" \
"v_exp_f32_e32 v109, v109\n" \
"v_mfma_f32_16x16x32_bf16 v[224:227], a[160:163], v[88:91], v[224:227]\n" \
"v_exp_f32_e32 v110, v110\n" \
"v_smfmac_f32_16x16x64_bf16 a[196:199], a[200:203], v[84:91], v21\n" \
"v_exp_f32_e32 v111, v111\n" \
"v_cvt_pk_bf16_f32 v106, v108, v109\n" \
"v_cvt_pk_bf16_f32 v107, v110, v111\n" \
"v_and_b32_e32 v104, v12, v104\n" \
"v_and_b32_e32 v105, v13, v105\n" \
"v_and_b32_e32 v106, v14, v106\n" \
"v_and_b32_e32 v107, v15, v107\n" \
"v_accvgpr_read_b32 v54, a192\n" \
"v_accvgpr_read_b32 v22, a193\n" \
"v_readlane_b32 s93, v11, 19\n" \
"v_readlane_b32 s80, v11, 11\n" \
"v_readlane_b32 s81, v11, 12\n" \
"s_mov_b32 s82, 0x47fca0\n" \
"s_mov_b32 s83, 0x20000\n" \
"v_readlane_b32 s92, v11, 26\n" \
"v_exp_f32_e32 v118, v118\n" \
"v_exp_f32_e32 v119, v119\n" \
"v_exp_f32_e32 v116, v116\n" \
"v_exp_f32_e32 v117, v117\n" \
"v_cvt_pk_bf16_f32 v119, v118, v119\n" \
"v_exp_f32_e32 v114, v114\n" \
"v_exp_f32_e32 v115, v115\n" \
"v_exp_f32_e32 v112, v112\n" \
"v_cvt_pk_bf16_f32 v118, v116, v117\n" \
"s_waitcnt vmcnt(10) lgkmcnt(0)\n" \
"s_barrier\n" \
"v_mfma_f32_16x16x32_bf16 v[80:83], a[128:131], a[32:35], v[40:43]\n" \
"ds_read_b128 a[160:163], v49 offset:8192\n" \
"ds_read_b128 a[164:167], v49 offset:9216\n" \
"v_mfma_f32_16x16x32_bf16 v[80:83], a[132:135], a[36:39], v[80:83]\n" \
"ds_read_b128 a[168:171], v49 offset:10240\n" \
"s_xor_b32 m0, s65, 0x18000\n" \
"v_mfma_f32_16x16x32_bf16 v[80:83], a[136:139], a[40:43], v[80:83]\n" \
"buffer_load_dwordx4 v52, s[8:11], s64 offen offset:0 sc1 lds\n" \
"v_mfma_f32_16x16x32_bf16 v[80:83], a[140:143], a[44:47], v[80:83]\n" \
"ds_read_b128 a[172:175], v49 offset:11264\n" \
"ds_read_b128 a[176:179], v49 offset:12288\n" \
"v_mfma_f32_16x16x32_bf16 v[84:87], a[156:159], a[44:47], v[40:43]\n" \
"ds_read_b128 a[180:183], v49 offset:13312\n" \
"ds_read_b128 a[184:187], v49 offset:14336\n" \
"v_mfma_f32_16x16x32_bf16 v[84:87], a[152:155], a[40:43], v[84:87]\n" \
"ds_read_b128 a[188:191], v49 offset:15360\n" \
"v_cvt_pk_bf16_f32 v117, v114, v115\n" \
"v_mfma_f32_16x16x32_bf16 v[84:87], a[148:151], a[36:39], v[84:87]\n" \
"buffer_load_dwordx4 v52, s[8:11], s64 offen offset:1024 sc1 lds\n" \
"v_mfma_f32_16x16x32_bf16 v[84:87], a[144:147], a[32:35], v[84:87]\n" \
"v_exp_f32_e32 v113, v113\n" \
"v_mfma_f32_16x16x32_bf16 v[92:95], a[144:147], a[48:51], v[44:47]\n" \
"v_cvt_pk_bf16_f32 v116, v112, v113\n" \
"ds_read_b128 a[144:147], v48 offset:12288\n" \
"v_mfma_f32_16x16x32_bf16 v[92:95], a[148:151], a[52:55], v[92:95]\n" \
"v_exp_f32_e32 v120, v120\n" \
"v_mfma_f32_16x16x32_bf16 v[92:95], a[152:155], a[56:59], v[92:95]\n" \
"buffer_load_dwordx4 v52, s[8:11], s64 offen offset:2048 sc1 lds\n" \
"v_mfma_f32_16x16x32_bf16 v[92:95], a[156:159], a[60:63], v[92:95]\n" \
"v_exp_f32_e32 v121, v121\n" \
"v_mfma_f32_16x16x32_bf16 v[88:91], a[140:143], a[60:63], v[44:47]\n" \
"v_exp_f32_e32 v122, v122\n" \
"v_mfma_f32_16x16x32_bf16 v[88:91], a[136:139], a[56:59], v[88:91]\n" \
"v_exp_f32_e32 v123, v123\n" \
"v_mfma_f32_16x16x32_bf16 v[88:91], a[132:135], a[52:55], v[88:91]\n" \
"buffer_load_dwordx4 v52, s[8:11], s64 offen offset:3072 sc1 lds\n" \
"v_mfma_f32_16x16x32_bf16 v[88:91], a[128:131], a[48:51], v[88:91]\n" \
"s_add_u32 m0, s93, 0x0\n" \
"v_cvt_pk_bf16_f32 v120, v120, v121\n" \
"v_mfma_f32_16x16x32_bf16 a[192:195], a[212:215], v[104:107], a[192:195]\n" \
"v_cvt_pk_bf16_f32 v121, v122, v123\n" \
"s_waitcnt lgkmcnt(6)\n" \
"v_mfma_f32_16x16x32_bf16 v[160:163], a[160:163], v[104:107], v[160:163]\n" \
"v_exp_f32_e32 v124, v124\n" \
"v_mfma_f32_16x16x32_bf16 v[164:167], a[164:167], v[104:107], v[164:167]\n" \
"buffer_load_dwordx4 v0, s[80:83], s92 offen offset:0 lds\n" \
"v_mfma_f32_16x16x32_bf16 v[168:171], a[168:171], v[104:107], v[168:171]\n" \
"ds_read_b128 a[128:131], v48 offset:8192\n" \
"s_waitcnt lgkmcnt(2)\n" \
"v_mfma_f32_16x16x32_bf16 v[172:175], a[172:175], v[104:107], v[172:175]\n" \
"v_exp_f32_e32 v125, v125\n" \
"v_mfma_f32_16x16x32_bf16 v[176:179], a[176:179], v[104:107], v[176:179]\n" \
"v_exp_f32_e32 v126, v126\n" \
"buffer_load_dwordx4 v1, s[80:83], s92 offen offset:1024 lds\n" \
"v_mfma_f32_16x16x32_bf16 v[180:183], a[180:183], v[104:107], v[180:183]\n" \
"v_exp_f32_e32 v127, v127\n" \
"v_mfma_f32_16x16x32_bf16 v[184:187], a[184:187], v[104:107], v[184:187]\n" \
"v_cvt_pk_bf16_f32 v122, v124, v125\n" \
"v_cvt_pk_bf16_f32 v123, v126, v127\n" \
"v_mfma_f32_16x16x32_bf16 v[188:191], a[188:191], v[104:107], v[188:191]\n" \
"ds_read_b128 a[132:135], v48 offset:9216\n" \
"ds_read_b128 a[148:151], v48 offset:13312\n" \
"buffer_load_dwordx4 v2, s[80:83], s92 offen offset:2048 lds\n" \
"v_mfma_f32_16x16x32_bf16 v[220:223], a[188:191], v[116:119], v[220:223]\n" \
"ds_read_b128 a[136:139], v48 offset:10240\n" \
"ds_read_b128 a[152:155], v48 offset:14336\n" \
"v_mfma_f32_16x16x32_bf16 v[252:255], a[188:191], v[120:123], v[252:255]\n" \
"ds_read_b128 a[140:143], v48 offset:11264\n" \
"ds_read_b128 a[156:159], v48 offset:15360\n" \
"v_mfma_f32_16x16x32_bf16 v[216:219], a[184:187], v[116:119], v[216:219]\n" \
"v_readlane_b32 s96, v11, 1\n" \
"v_bfe_u32 v27, v20, 2, 7\n" \
"buffer_load_dwordx4 v3, s[80:83], s92 offen offset:3072 lds\n" \
"v_mfma_f32_16x16x32_bf16 v[248:251], a[184:187], v[120:123], v[248:251]\n" \
"s_add_u32 m0, s93, 0x1000\n" \
"v_add_f32_e32 v54, v54, v22\n" \
"v_mfma_f32_16x16x32_bf16 v[212:215], a[180:183], v[116:119], v[212:215]\n" \
"v_max_f32_e32 v62, v62, v54\n" \
"v_add_u32_e32 v49, 0x10000, v48\n" \
"v_mfma_f32_16x16x32_bf16 v[244:247], a[180:183], v[120:123], v[244:247]\n" \
"v_rcp_f32_e32 v54, v54\n" \
"buffer_load_dwordx4 v4, s[80:83], s92 offen offset:0 lds\n" \
"v_mfma_f32_16x16x32_bf16 v[208:211], a[176:179], v[116:119], v[208:211]\n" \
"v_mul_f32_e32 v54, v54, v58\n" \
"v_mul_f32_e32 v128, v54, v128\n" \
"v_mfma_f32_16x16x32_bf16 v[240:243], a[176:179], v[120:123], v[240:243]\n" \
"v_mul_f32_e32 v129, v54, v129\n" \
"v_mul_f32_e32 v136, v54, v136\n" \
"v_mfma_f32_16x16x32_bf16 v[204:207], a[172:175], v[116:119], v[204:207]\n" \
"v_mul_f32_e32 v137, v54, v137\n" \
"v_mul_f32_e32 v144, v54, v144\n" \
"buffer_load_dwordx4 v5, s[80:83], s92 offen offset:1024 lds\n" \
"v_mfma_f32_16x16x32_bf16 v[236:239], a[172:175], v[120:123], v[236:239]\n" \
"v_mul_f32_e32 v145, v54, v145\n" \
"v_mul_f32_e32 v152, v54, v152\n" \
"v_mfma_f32_16x16x32_bf16 v[200:203], a[168:171], v[116:119], v[200:203]\n" \
"v_mul_f32_e32 v153, v54, v153\n" \
"v_cvt_pk_bf16_f32 v128, v128, v129\n" \
"v_mfma_f32_16x16x32_bf16 v[232:235], a[168:171], v[120:123], v[232:235]\n" \
"v_mul_f32_e32 v130, v54, v130\n" \
"v_mul_f32_e32 v131, v54, v131\n" \
"buffer_load_dwordx4 v6, s[80:83], s92 offen offset:2048 lds\n" \
"v_mfma_f32_16x16x32_bf16 v[196:199], a[164:167], v[116:119], v[196:199]\n" \
"v_cvt_pk_bf16_f32 v136, v136, v137\n" \
"v_mul_f32_e32 v138, v54, v138\n" \
"v_mfma_f32_16x16x32_bf16 v[228:231], a[164:167], v[120:123], v[228:231]\n" \
"v_mul_f32_e32 v139, v54, v139\n" \
"v_cvt_pk_bf16_f32 v144, v144, v145\n" \
"v_mfma_f32_16x16x32_bf16 v[192:195], a[160:163], v[116:119], v[192:195]\n" \
"v_mul_f32_e32 v146, v54, v146\n" \
"v_mul_f32_e32 v147, v54, v147\n" \
"buffer_load_dwordx4 v7, s[80:83], s92 offen offset:3072 lds\n" \
"v_mfma_f32_16x16x32_bf16 v[224:227], a[160:163], v[120:123], v[224:227]\n" \
"v_cvt_pk_bf16_f32 v152, v152, v153\n" \
"v_mul_f32_e32 v154, v54, v154\n" \
"v_smfmac_f32_16x16x64_bf16 a[196:199], a[200:203], v[116:123], v21\n" \
"v_mul_f32_e32 v155, v54, v155\n" \
"v_cvt_pk_bf16_f32 v129, v130, v131\n" \
"v_mul_f32_e32 v132, v54, v132\n" \
"v_mul_f32_e32 v133, v54, v133\n" \
"v_mul_f32_e32 v134, v54, v134\n" \
"v_mul_f32_e32 v135, v54, v135\n" \
"v_cvt_pk_bf16_f32 v137, v138, v139\n" \
"v_mul_f32_e32 v140, v54, v140\n" \
"v_mul_f32_e32 v141, v54, v141\n" \
"v_mul_f32_e32 v142, v54, v142\n" \
"v_mul_f32_e32 v143, v54, v143\n" \
"v_cvt_pk_bf16_f32 v145, v146, v147\n" \
"v_mul_f32_e32 v148, v54, v148\n" \
"v_mul_f32_e32 v149, v54, v149\n" \
"v_mul_f32_e32 v150, v54, v150\n" \
"v_mul_f32_e32 v151, v54, v151\n" \
"v_cvt_pk_bf16_f32 v153, v154, v155\n" \
"v_mul_f32_e32 v156, v54, v156\n" \
"v_mul_f32_e32 v157, v54, v157\n" \
"v_mul_f32_e32 v158, v54, v158\n" \
"v_mul_f32_e32 v159, v54, v159\n" \
"v_cvt_pk_bf16_f32 v130, v132, v133\n" \
"v_cvt_pk_bf16_f32 v131, v134, v135\n" \
"buffer_store_dwordx4 v[128:131], v20, s[56:59], s68 offen offset:0 sc1\n" \
"v_cvt_pk_bf16_f32 v138, v140, v141\n" \
"v_cvt_pk_bf16_f32 v139, v142, v143\n" \
"v_cvt_pk_bf16_f32 v146, v148, v149\n" \
"v_cvt_pk_bf16_f32 v147, v150, v151\n" \
"v_cvt_pk_bf16_f32 v154, v156, v157\n" \
"v_cvt_pk_bf16_f32 v155, v158, v159\n" \
"s_add_u32 m0, s93, 0x2000\n" \
"s_xor_b32 s65, s65, 0x8000\n" \
"s_add_u32 s64, s64, 0x10000\n" \
"v_exp_f32_e32 v86, v86\n" \
"buffer_store_dwordx4 v[136:139], v20, s[56:59], s68 offen offset:1024 sc1\n" \
"v_exp_f32_e32 v87, v87\n" \
"v_exp_f32_e32 v84, v84\n" \
"v_exp_f32_e32 v85, v85\n" \
"v_exp_f32_e32 v88, v88\n" \
"v_exp_f32_e32 v89, v89\n" \
"v_cvt_pk_bf16_f32 v87, v86, v87\n" \
"buffer_store_dwordx4 v[144:147], v20, s[56:59], s68 offen offset:2048 sc1\n" \
"v_exp_f32_e32 v82, v82\n" \
"v_exp_f32_e32 v83, v83\n" \
"v_exp_f32_e32 v80, v80\n" \
"v_exp_f32_e32 v81, v81\n" \
"v_exp_f32_e32 v90, v90\n" \
"v_exp_f32_e32 v91, v91\n" \
"buffer_store_dwordx4 v[152:155], v20, s[56:59], s68 offen offset:3072 sc1\n" \
"v_cvt_pk_bf16_f32 v86, v84, v85\n" \
"v_cvt_pk_bf16_f32 v88, v88, v89\n" \
"v_exp_f32_e32 v92, v92\n" \
"v_exp_f32_e32 v93, v93\n" \
"v_exp_f32_e32 v94, v94\n" \
"v_exp_f32_e32 v95, v95\n" \
"v_cvt_pk_bf16_f32 v85, v82, v83\n" \
"buffer_load_dwordx4 v8, s[80:83], s92 offen offset:0 lds\n" \
"v_cvt_pk_bf16_f32 v84, v80, v81\n" \
"v_cvt_pk_bf16_f32 v89, v90, v91\n" \
"v_and_b32_e32 v84, v12, v84\n" \
"v_and_b32_e32 v85, v13, v85\n" \
"v_and_b32_e32 v86, v14, v86\n" \
"v_and_b32_e32 v87, v15, v87\n" \
"s_waitcnt vmcnt(17) lgkmcnt(0)\n" \
"s_barrier\n" \
"v_mfma_f32_16x16x32_bf16 v[120:123], a[128:131], a[48:51], v[44:47]\n" \
"buffer_load_dword v26, v27, s[56:59], s96 offen sc1\n" \
"v_mfma_f32_16x16x32_bf16 v[120:123], a[132:135], a[52:55], v[120:123]\n" \
"ds_read_b128 a[188:191], v49 offset:7168\n" \
"ds_read_b128 a[184:187], v49 offset:6144\n" \
"v_mfma_f32_16x16x32_bf16 v[120:123], a[136:139], a[56:59], v[120:123]\n" \
"s_xor_b32 m0, s65, 0x12000\n" \
"ds_read_b128 a[180:183], v49 offset:5120\n" \
"v_mfma_f32_16x16x32_bf16 v[120:123], a[140:143], a[60:63], v[120:123]\n" \
"ds_read_b128 a[176:179], v49 offset:4096\n" \
"ds_read_b128 a[172:175], v49 offset:3072\n" \
"v_mfma_f32_16x16x32_bf16 v[124:127], a[156:159], a[60:63], v[44:47]\n" \
"buffer_load_dwordx4 v50, s[8:11], s64 offen offset:0 sc1 lds\n" \
"v_mfma_f32_16x16x32_bf16 v[124:127], a[152:155], a[56:59], v[124:127]\n" \
"ds_read_b128 a[168:171], v49 offset:2048\n" \
"v_cvt_pk_bf16_f32 v90, v92, v93\n" \
"v_mfma_f32_16x16x32_bf16 v[124:127], a[148:151], a[52:55], v[124:127]\n" \
"v_cvt_pk_bf16_f32 v91, v94, v95\n" \
"ds_read_b128 a[164:167], v49 offset:1024\n" \
"v_mfma_f32_16x16x32_bf16 v[124:127], a[144:147], a[48:51], v[124:127]\n" \
"ds_read_b128 a[160:163], v49 offset:0\n" \
"ds_read_b128 a[0:3], v49 offset:8192\n" \
"v_mfma_f32_16x16x32_bf16 v[32:35], a[220:223], a[64:67], 0\n" \
"buffer_load_dwordx4 v50, s[8:11], s64 offen offset:1024 sc1 lds\n" \
"v_mfma_f32_16x16x32_bf16 v[36:39], a[220:223], a[80:83], 0\n" \
"ds_read_b128 a[4:7], v49 offset:9216\n" \
"ds_read_b128 a[8:11], v49 offset:10240\n" \
"v_mfma_f32_16x16x32_bf16 v[32:35], a[224:227], a[68:71], v[32:35]\n" \
"ds_read_b128 a[12:15], v49 offset:11264\n" \
"ds_read_b128 a[16:19], v49 offset:12288\n" \
"v_mfma_f32_16x16x32_bf16 v[36:39], a[224:227], a[84:87], v[36:39]\n" \
"ds_read_b128 a[20:23], v49 offset:13312\n" \
"ds_read_b128 a[24:27], v49 offset:14336\n" \
"v_mfma_f32_16x16x32_bf16 v[32:35], a[228:231], a[72:75], v[32:35]\n" \
"buffer_load_dwordx4 v50, s[8:11], s64 offen offset:2048 sc1 lds\n" \
"v_mfma_f32_16x16x32_bf16 v[36:39], a[228:231], a[88:91], v[36:39]\n" \
"ds_read_b128 a[28:31], v49 offset:15360\n" \
"v_readlane_b32 s97, v11, 36\n" \
"v_mfma_f32_16x16x32_bf16 v[32:35], a[232:235], a[76:79], v[32:35]\n" \
"v_exp_f32_e32 v120, v120\n" \
"v_mfma_f32_16x16x32_bf16 v[36:39], a[232:235], a[92:95], v[36:39]\n" \
"v_exp_f32_e32 v121, v121\n" \
"v_smfmac_f32_16x16x64_bf16 a[196:199], a[200:203], v[84:91], v21\n" \
"v_cvt_pk_bf16_f32 v120, v120, v121\n" \
"s_waitcnt lgkmcnt(13)\n" \
"v_mfma_f32_16x16x32_bf16 v[220:223], a[188:191], v[84:87], v[220:223]\n" \
"buffer_load_dwordx4 v50, s[8:11], s64 offen offset:3072 sc1 lds\n" \
"v_mfma_f32_16x16x32_bf16 v[252:255], a[188:191], v[88:91], v[252:255]\n" \
"v_exp_f32_e32 v122, v122\n" \
"v_mfma_f32_16x16x32_bf16 v[216:219], a[184:187], v[84:87], v[216:219]\n" \
"v_exp_f32_e32 v123, v123\n" \
"v_mfma_f32_16x16x32_bf16 v[248:251], a[184:187], v[88:91], v[248:251]\n" \
"v_exp_f32_e32 v124, v124\n" \
"v_mfma_f32_16x16x32_bf16 v[212:215], a[180:183], v[84:87], v[212:215]\n" \
"v_exp_f32_e32 v125, v125\n" \
"v_mfma_f32_16x16x32_bf16 v[244:247], a[180:183], v[88:91], v[244:247]\n" \
"v_cvt_pk_bf16_f32 v121, v122, v123\n" \
"s_waitcnt lgkmcnt(7)\n" \
"v_mfma_f32_16x16x32_bf16 v[208:211], a[176:179], v[84:87], v[208:211]\n" \
"v_exp_f32_e32 v126, v126\n" \
"v_mfma_f32_16x16x32_bf16 v[240:243], a[176:179], v[88:91], v[240:243]\n" \
"v_exp_f32_e32 v127, v127\n" \
"v_mfma_f32_16x16x32_bf16 v[204:207], a[172:175], v[84:87], v[204:207]\n" \
"v_cvt_pk_bf16_f32 v122, v124, v125\n" \
"v_cvt_pk_bf16_f32 v123, v126, v127\n" \
"v_mfma_f32_16x16x32_bf16 v[236:239], a[172:175], v[88:91], v[236:239]\n" \
"v_and_b32_e32 v120, v12, v120\n" \
"v_and_b32_e32 v121, v13, v121\n" \
"v_mfma_f32_16x16x32_bf16 v[200:203], a[168:171], v[84:87], v[200:203]\n" \
"v_and_b32_e32 v122, v14, v122\n" \
"v_and_b32_e32 v123, v15, v123\n" \
"v_mfma_f32_16x16x32_bf16 v[232:235], a[168:171], v[88:91], v[232:235]\n" \
"v_accvgpr_read_b32 v56, a196\n" \
"v_accvgpr_read_b32 v24, a197\n" \
"v_mfma_f32_16x16x32_bf16 v[196:199], a[164:167], v[84:87], v[196:199]\n" \
"v_add_u32_e32 v17, 0x49000, v20\n" \
"v_readlane_b32 s98, v11, 37\n" \
"v_mfma_f32_16x16x32_bf16 v[228:231], a[164:167], v[88:91], v[228:231]\n" \
"v_add_u32_e32 v48, s16, v20\n" \
"ds_read_b128 a[128:131], v48 offset:0\n" \
"v_mfma_f32_16x16x32_bf16 v[192:195], a[160:163], v[84:87], v[192:195]\n" \
"ds_read_b128 a[144:147], v48 offset:4096\n" \
"ds_read_b128 a[132:135], v48 offset:1024\n" \
"v_mfma_f32_16x16x32_bf16 v[224:227], a[160:163], v[88:91], v[224:227]\n" \
"ds_read_b128 a[148:151], v48 offset:5120\n" \
"ds_read_b128 a[136:139], v48 offset:2048\n" \
"v_mfma_f32_16x16x32_bf16 a[196:199], a[212:215], v[120:123], a[196:199]\n" \
"ds_read_b128 a[152:155], v48 offset:6144\n" \
"ds_read_b128 a[140:143], v48 offset:3072\n" \
"v_mfma_f32_16x16x32_bf16 v[224:227], a[0:3], v[120:123], v[224:227]\n" \
"ds_read_b128 a[156:159], v48 offset:7168\n" \
"s_waitcnt lgkmcnt(8)\n" \
"v_mfma_f32_16x16x32_bf16 v[228:231], a[4:7], v[120:123], v[228:231]\n" \
"v_accvgpr_read_b32 v55, a194\n" \
"v_accvgpr_read_b32 v23, a195\n" \
"v_mfma_f32_16x16x32_bf16 a[192:195], a[216:219], a[216:219], 0\n" \
"v_add_f32_e32 v55, v55, v23\n" \
"v_max_f32_e32 v62, v62, v55\n" \
"v_mfma_f32_16x16x32_bf16 v[232:235], a[8:11], v[120:123], v[232:235]\n" \
"v_rcp_f32_e32 v55, v55\n" \
"v_mfma_f32_16x16x32_bf16 v[236:239], a[12:15], v[120:123], v[236:239]\n" \
"v_mul_f32_e32 v55, v55, v59\n" \
"v_mul_f32_e32 v160, v55, v160\n" \
"v_mfma_f32_16x16x32_bf16 v[240:243], a[16:19], v[120:123], v[240:243]\n" \
"v_mul_f32_e32 v161, v55, v161\n" \
"v_mul_f32_e32 v168, v55, v168\n" \
"v_mfma_f32_16x16x32_bf16 v[244:247], a[20:23], v[120:123], v[244:247]\n" \
"v_mul_f32_e32 v169, v55, v169\n" \
"v_mul_f32_e32 v176, v55, v176\n" \
"v_mfma_f32_16x16x32_bf16 v[248:251], a[24:27], v[120:123], v[248:251]\n" \
"v_mul_f32_e32 v177, v55, v177\n" \
"v_mul_f32_e32 v184, v55, v184\n" \
"v_mfma_f32_16x16x32_bf16 v[252:255], a[28:31], v[120:123], v[252:255]\n" \
"v_mul_f32_e32 v185, v55, v185\n" \
"v_cvt_pk_bf16_f32 v160, v160, v161\n" \
"v_mul_f32_e32 v162, v55, v162\n" \
"v_mul_f32_e32 v163, v55, v163\n" \
"v_cvt_pk_bf16_f32 v168, v168, v169\n" \
"v_mul_f32_e32 v170, v55, v170\n" \
"v_mul_f32_e32 v171, v55, v171\n" \
"v_cvt_pk_bf16_f32 v176, v176, v177\n" \
"v_mul_f32_e32 v178, v55, v178\n" \
"s_waitcnt vmcnt(4)\n" \
"v_cmp_ne_u32_e32 vcc, s62, v26\n" \
"v_mul_f32_e32 v179, v55, v179\n" \
"v_cvt_pk_bf16_f32 v184, v184, v185\n" \
"v_mul_f32_e32 v186, v55, v186\n" \
"v_mul_f32_e32 v187, v55, v187\n" \
"v_cvt_pk_bf16_f32 v161, v162, v163\n" \
"s_cbranch_vccnz L_sos_%=\n" \
"L_ros_%=:\n" \
"buffer_load_dwordx4 a[220:223], v20, s[56:59], s97 offen offset:0 sc1\n" \
"v_mul_f32_e32 v164, v55, v164\n" \
"v_mul_f32_e32 v165, v55, v165\n" \
"v_mul_f32_e32 v166, v55, v166\n" \
"v_mul_f32_e32 v167, v55, v167\n" \
"v_cvt_pk_bf16_f32 v169, v170, v171\n" \
"v_mul_f32_e32 v172, v55, v172\n" \
"v_mul_f32_e32 v173, v55, v173\n" \
"v_mul_f32_e32 v174, v55, v174\n" \
"v_mul_f32_e32 v175, v55, v175\n" \
"v_cvt_pk_bf16_f32 v177, v178, v179\n" \
"v_mul_f32_e32 v180, v55, v180\n" \
"buffer_load_dwordx4 a[228:231], v17, s[56:59], s97 offen offset:0 sc1\n" \
"v_mul_f32_e32 v181, v55, v181\n" \
"v_mul_f32_e32 v182, v55, v182\n" \
"v_mul_f32_e32 v183, v55, v183\n" \
"v_cvt_pk_bf16_f32 v185, v186, v187\n" \
"v_mul_f32_e32 v188, v55, v188\n" \
"v_mul_f32_e32 v189, v55, v189\n" \
"v_mul_f32_e32 v190, v55, v190\n" \
"v_mul_f32_e32 v191, v55, v191\n" \
"v_cvt_pk_bf16_f32 v162, v164, v165\n" \
"v_cvt_pk_bf16_f32 v163, v166, v167\n" \
"v_cvt_pk_bf16_f32 v170, v172, v173\n" \
"buffer_store_dwordx4 v[160:163], v20, s[56:59], s69 offen offset:0 sc1\n" \
"v_cvt_pk_bf16_f32 v171, v174, v175\n" \
"v_cvt_pk_bf16_f32 v178, v180, v181\n" \
"v_cvt_pk_bf16_f32 v179, v182, v183\n" \
"v_cvt_pk_bf16_f32 v186, v188, v189\n" \
"v_cvt_pk_bf16_f32 v187, v190, v191\n" \
"v_readlane_b32 s93, v11, 20\n" \
"v_readlane_b32 s84, v11, 15\n" \
"v_readlane_b32 s85, v11, 16\n" \
"v_readlane_b32 s86, v11, 17\n" \
"s_mov_b32 s87, 0x20000\n" \
"v_readlane_b32 s92, v11, 18\n" \
"buffer_store_dwordx4 v[168:171], v20, s[56:59], s69 offen offset:1024 sc1\n" \
"v_readlane_b32 s91, v11, 21\n" \
"v_mfma_f32_16x16x32_bf16 a[208:211], a[216:219], v[128:131], a[208:211]\n" \
"v_mfma_f32_16x16x32_bf16 a[208:211], a[216:219], v[136:139], a[208:211]\n" \
"v_mfma_f32_32x32x16_bf16 v[128:143], a[216:219], a[216:219], 0\n" \
"buffer_store_dwordx4 v[176:179], v20, s[56:59], s69 offen offset:2048 sc1\n" \
"v_mfma_f32_16x16x32_bf16 a[208:211], a[216:219], v[144:147], a[208:211]\n" \
"v_mfma_f32_16x16x32_bf16 a[208:211], a[216:219], v[152:155], a[208:211]\n" \
"v_mfma_f32_32x32x16_bf16 v[144:159], a[216:219], a[216:219], 0\n" \
"buffer_store_dwordx4 v[184:187], v20, s[56:59], s69 offen offset:3072 sc1\n" \
"v_add_f32_e32 v56, v56, v24\n" \
"v_mfma_f32_16x16x32_bf16 a[208:211], a[216:219], v[160:163], a[208:211]\n" \
"v_max_f32_e32 v62, v62, v56\n" \
"v_rcp_f32_e32 v56, v56\n" \
"v_mfma_f32_16x16x32_bf16 a[208:211], a[216:219], v[168:171], a[208:211]\n" \
"v_mfma_f32_32x32x16_bf16 v[160:175], a[216:219], a[216:219], 0\n" \
"buffer_load_dwordx4 a[236:239], v20, s[56:59], s98 offen offset:0 sc1\n" \
"v_mul_f32_e32 v56, v56, v60\n" \
"v_mfma_f32_16x16x32_bf16 a[208:211], a[216:219], v[176:179], a[208:211]\n" \
"v_mul_f32_e32 v192, v56, v192\n" \
"v_mul_f32_e32 v193, v56, v193\n" \
"v_cvt_pk_bf16_f32 v192, v192, v193\n" \
"v_mul_f32_e32 v194, v56, v194\n" \
"v_mul_f32_e32 v195, v56, v195\n" \
"buffer_load_dwordx4 a[244:247], v17, s[56:59], s98 offen offset:0 sc1\n" \
"v_mfma_f32_16x16x32_bf16 a[208:211], a[216:219], v[184:187], a[208:211]\n" \
"v_mfma_f32_32x32x16_bf16 v[176:191], a[216:219], a[216:219], 0\n" \
"v_cvt_pk_bf16_f32 v193, v194, v195\n" \
"v_mul_f32_e32 v196, v56, v196\n" \
"v_mul_f32_e32 v197, v56, v197\n" \
"v_mul_f32_e32 v198, v56, v198\n" \
"v_mul_f32_e32 v199, v56, v199\n" \
"buffer_load_dwordx4 a[224:227], v20, s[56:59], s97 offen offset:1024 sc1\n" \
"v_mul_f32_e32 v200, v56, v200\n" \
"v_mul_f32_e32 v201, v56, v201\n" \
"v_accvgpr_read_b32 v57, a198\n" \
"v_accvgpr_read_b32 v25, a199\n" \
"v_mfma_f32_16x16x32_bf16 a[196:199], a[216:219], a[216:219], 0\n" \
"v_cvt_pk_bf16_f32 v194, v196, v197\n" \
"v_cvt_pk_bf16_f32 v195, v198, v199\n" \
"v_cvt_pk_bf16_f32 v200, v200, v201\n" \
"v_mul_f32_e32 v202, v56, v202\n" \
"v_mul_f32_e32 v203, v56, v203\n" \
"buffer_store_dwordx4 v[192:195], v20, s[56:59], s70 offen offset:0 sc1\n" \
"v_add_f32_e32 v57, v57, v25\n" \
"v_mfma_f32_16x16x32_bf16 a[208:211], a[216:219], v[192:195], a[208:211]\n" \
"v_cvt_pk_bf16_f32 v201, v202, v203\n" \
"v_mul_f32_e32 v204, v56, v204\n" \
"v_mul_f32_e32 v205, v56, v205\n" \
"v_mul_f32_e32 v206, v56, v206\n" \
"v_mul_f32_e32 v207, v56, v207\n" \
"v_mul_f32_e32 v208, v56, v208\n" \
"v_mul_f32_e32 v209, v56, v209\n" \
"v_max_f32_e32 v62, v62, v57\n" \
"buffer_load_dwordx4 a[232:235], v17, s[56:59], s97 offen offset:1024 sc1\n" \
"s_waitcnt vmcnt(16) lgkmcnt(0)\n" \
"s_barrier\n" \
"v_mfma_f32_16x16x32_bf16 v[64:67], a[128:131], a[64:67], v[32:35]\n" \
"s_add_u32 m0, s93, 0x0\n" \
"v_cvt_pk_bf16_f32 v202, v204, v205\n" \
"v_mfma_f32_16x16x32_bf16 v[64:67], a[132:135], a[68:71], v[64:67]\n" \
"buffer_load_dwordx4 a[240:243], v20, s[56:59], s98 offen offset:1024 sc1\n" \
"v_mfma_f32_16x16x32_bf16 v[64:67], a[136:139], a[72:75], v[64:67]\n" \
"v_cvt_pk_bf16_f32 v203, v206, v207\n" \
"v_cvt_pk_bf16_f32 v208, v208, v209\n" \
"v_mfma_f32_16x16x32_bf16 v[64:67], a[140:143], a[76:79], v[64:67]\n" \
"v_mul_f32_e32 v210, v56, v210\n" \
"v_mul_f32_e32 v211, v56, v211\n" \
"v_mfma_f32_16x16x32_bf16 v[68:71], a[156:159], a[76:79], v[32:35]\n" \
"v_rcp_f32_e32 v57, v57\n" \
"buffer_store_dwordx4 v[200:203], v20, s[56:59], s70 offen offset:1024 sc1\n" \
"v_mfma_f32_16x16x32_bf16 v[68:71], a[152:155], a[72:75], v[68:71]\n" \
"v_mfma_f32_16x16x32_bf16 a[208:211], a[216:219], v[200:203], a[208:211]\n" \
"v_mfma_f32_32x32x16_bf16 v[192:207], a[216:219], a[216:219], 0\n" \
"v_cvt_pk_bf16_f32 v209, v210, v211\n" \
"buffer_load_dwordx4 a[248:251], v17, s[56:59], s98 offen offset:1024 sc1\n" \
"v_mul_f32_e32 v212, v56, v212\n" \
"v_mul_f32_e32 v213, v56, v213\n" \
"v_mul_f32_e32 v214, v56, v214\n" \
"v_mfma_f32_16x16x32_bf16 v[68:71], a[148:151], a[68:71], v[68:71]\n" \
"v_mul_f32_e32 v215, v56, v215\n" \
"v_mul_f32_e32 v216, v56, v216\n" \
"v_mfma_f32_16x16x32_bf16 v[68:71], a[144:147], a[64:67], v[68:71]\n" \
"v_mul_f32_e32 v217, v56, v217\n" \
"v_cvt_pk_bf16_f32 v210, v212, v213\n" \
"buffer_load_dwordx4 v0, s[84:87], s92 offen offset:0 lds\n" \
"v_mfma_f32_16x16x32_bf16 v[76:79], a[144:147], a[80:83], v[36:39]\n" \
"ds_read_b128 a[144:147], v48 offset:12288\n" \
"v_cvt_pk_bf16_f32 v211, v214, v215\n" \
"v_mfma_f32_16x16x32_bf16 v[76:79], a[148:151], a[84:87], v[76:79]\n" \
"ds_read_b128 a[148:151], v48 offset:13312\n" \
"v_cvt_pk_bf16_f32 v216, v216, v217\n" \
"v_mfma_f32_16x16x32_bf16 v[76:79], a[152:155], a[88:91], v[76:79]\n" \
"ds_read_b128 a[152:155], v48 offset:14336\n" \
"v_mul_f32_e32 v218, v56, v218\n" \
"buffer_store_dwordx4 v[208:211], v20, s[56:59], s70 offen offset:2048 sc1\n" \
"v_mfma_f32_16x16x32_bf16 v[76:79], a[156:159], a[92:95], v[76:79]\n" \
"ds_read_b128 a[156:159], v48 offset:15360\n" \
"v_mul_f32_e32 v219, v56, v219\n" \
"v_mfma_f32_16x16x32_bf16 v[72:75], a[140:143], a[92:95], v[36:39]\n" \
"ds_read_b128 a[140:143], v48 offset:11264\n" \
"v_mul_f32_e32 v57, v57, v61\n" \
"v_mfma_f32_16x16x32_bf16 v[72:75], a[136:139], a[88:91], v[72:75]\n" \
".fill (L_ab + 28672 - .) / 4, 4, 0xBF800000\n" \
"L_ap7:\n" \
"s_cbranch_execz L_ap0\n" \
"ds_read_b128 a[136:139], v48 offset:10240\n" \
"v_cvt_pk_bf16_f32 v217, v218, v219\n" \
"buffer_load_dwordx4 v1, s[84:87], s92 offen offset:1024 lds\n" \
"v_mfma_f32_16x16x32_bf16 v[72:75], a[132:135], a[84:87], v[72:75]\n" \
"ds_read_b128 a[132:135], v48 offset:9216\n" \
"v_mul_f32_e32 v220, v56, v220\n" \
"v_mfma_f32_16x16x32_bf16 v[72:75], a[128:131], a[80:83], v[72:75]\n" \
"ds_read_b128 a[128:131], v48 offset:8192\n" \
"v_mfma_f32_16x16x32_bf16 a[208:211], a[216:219], v[208:211], a[208:211]\n" \
"v_mul_f32_e32 v221, v56, v221\n" \
"buffer_load_dwordx4 v2, s[84:87], s92 offen offset:2048 lds\n" \
"v_mul_f32_e32 v222, v56, v222\n" \
"v_mul_f32_e32 v223, v56, v223\n" \
"v_mul_f32_e32 v224, v57, v224\n" \
"v_mul_f32_e32 v225, v57, v225\n" \
"v_cvt_pk_bf16_f32 v218, v220, v221\n" \
"v_cvt_pk_bf16_f32 v219, v222, v223\n" \
"v_cvt_pk_bf16_f32 v224, v224, v225\n" \
"v_mul_f32_e32 v226, v57, v226\n" \
"v_mul_f32_e32 v227, v57, v227\n" \
"v_mfma_f32_16x16x32_bf16 a[208:211], a[216:219], v[216:219], a[208:211]\n" \
"buffer_store_dwordx4 v[216:219], v20, s[56:59], s70 offen offset:3072 sc1\n" \
"v_cvt_pk_bf16_f32 v225, v226, v227\n" \
"v_mul_f32_e32 v228, v57, v228\n" \
"v_mfma_f32_32x32x16_bf16 v[208:223], a[216:219], a[216:219], 0\n" \
"v_mul_f32_e32 v229, v57, v229\n" \
"v_mul_f32_e32 v230, v57, v230\n" \
"v_mul_f32_e32 v231, v57, v231\n" \
"v_mul_f32_e32 v232, v57, v232\n" \
"v_mul_f32_e32 v233, v57, v233\n" \
"v_cvt_pk_bf16_f32 v226, v228, v229\n" \
"v_cvt_pk_bf16_f32 v227, v230, v231\n" \
"buffer_store_dwordx4 v[224:227], v20, s[56:59], s71 offen offset:0 sc1\n" \
"v_cvt_pk_bf16_f32 v232, v232, v233\n" \
"v_mul_f32_e32 v234, v57, v234\n" \
"v_mul_f32_e32 v235, v57, v235\n" \
"v_mfma_f32_16x16x32_bf16 a[208:211], a[216:219], v[224:227], a[208:211]\n" \
"v_cvt_pk_bf16_f32 v233, v234, v235\n" \
"v_mul_f32_e32 v236, v57, v236\n" \
"v_mul_f32_e32 v237, v57, v237\n" \
"v_mul_f32_e32 v238, v57, v238\n" \
"v_mul_f32_e32 v239, v57, v239\n" \
"v_mul_f32_e32 v240, v57, v240\n" \
"buffer_load_dwordx4 v3, s[84:87], s92 offen offset:3072 lds\n" \
"s_add_u32 m0, s91, 0x1000\n" \
"v_mul_f32_e32 v241, v57, v241\n" \
"v_cvt_pk_bf16_f32 v234, v236, v237\n" \
"v_cvt_pk_bf16_f32 v235, v238, v239\n" \
"v_cvt_pk_bf16_f32 v240, v240, v241\n" \
"v_mul_f32_e32 v242, v57, v242\n" \
"v_mul_f32_e32 v243, v57, v243\n" \
"v_mfma_f32_16x16x32_bf16 a[208:211], a[216:219], v[232:235], a[208:211]\n" \
"v_cvt_pk_bf16_f32 v241, v242, v243\n" \
"v_mul_f32_e32 v244, v57, v244\n" \
"buffer_store_dwordx4 v[232:235], v20, s[56:59], s71 offen offset:1024 sc1\n" \
"v_mul_f32_e32 v245, v57, v245\n" \
"v_mul_f32_e32 v246, v57, v246\n" \
"v_mfma_f32_32x32x16_bf16 v[224:239], a[216:219], a[216:219], 0\n" \
"v_mul_f32_e32 v247, v57, v247\n" \
"v_mul_f32_e32 v248, v57, v248\n" \
"v_mul_f32_e32 v249, v57, v249\n" \
"v_cvt_pk_bf16_f32 v242, v244, v245\n" \
"v_cvt_pk_bf16_f32 v243, v246, v247\n" \
"v_cvt_pk_bf16_f32 v248, v248, v249\n" \
"v_mul_f32_e32 v250, v57, v250\n" \
"buffer_store_dwordx4 v[240:243], v20, s[56:59], s71 offen offset:2048 sc1\n" \
"v_mul_f32_e32 v251, v57, v251\n" \
"v_mfma_f32_16x16x32_bf16 a[208:211], a[216:219], v[240:243], a[208:211]\n" \
"v_cvt_pk_bf16_f32 v249, v250, v251\n" \
"v_mul_f32_e32 v252, v57, v252\n" \
"v_mul_f32_e32 v253, v57, v253\n" \
"v_mul_f32_e32 v254, v57, v254\n" \
"v_mul_f32_e32 v255, v57, v255\n" \
"v_cvt_pk_bf16_f32 v250, v252, v253\n" \
"v_cvt_pk_bf16_f32 v251, v254, v255\n" \
"v_add_u32_e32 v53, 0x20000, v63\n" \
"buffer_store_dwordx4 v[248:251], v20, s[56:59], s71 offen offset:3072 sc1\n" \
"v_mfma_f32_16x16x32_bf16 a[208:211], a[216:219], v[248:251], a[208:211]\n" \
"v_exp_f32_e32 v70, v70\n" \
"v_mfma_f32_32x32x16_bf16 v[240:255], a[216:219], a[216:219], 0\n" \
"v_exp_f32_e32 v71, v71\n" \
"v_exp_f32_e32 v72, v72\n" \
"v_exp_f32_e32 v73, v73\n" \
"buffer_load_dwordx4 v4, s[84:87], s92 offen offset:0 lds\n" \
"s_add_u32 s64, s64, 0x10000\n" \
"v_cmp_lt_f32_e64 s[72:73], s21, v62\n" \
"v_accvgpr_read_b32 v63, a208\n" \
"v_cmp_u_f32_e32 vcc, v63, v63\n" \
"v_readlane_b32 s95, v11, 3\n" \
"v_exp_f32_e32 v68, v68\n" \
"v_exp_f32_e32 v69, v69\n" \
"v_exp_f32_e32 v74, v74\n" \
"buffer_load_dwordx4 v5, s[84:87], s92 offen offset:1024 lds\n" \
"v_exp_f32_e32 v75, v75\n" \
"s_or_b64 vcc, s[72:73], vcc\n" \
"s_cselect_b32 s95, 0x40000000, s95\n" \
"s_cselect_b32 s94, 0x7f800000, 0\n" \
"v_cvt_pk_bf16_f32 v71, v70, v71\n" \
"v_exp_f32_e32 v66, v66\n" \
"v_exp_f32_e32 v67, v67\n" \
"v_exp_f32_e32 v64, v64\n" \
"buffer_load_dwordx4 v6, s[84:87], s92 offen offset:2048 lds\n" \
"v_exp_f32_e32 v65, v65\n" \
"v_cvt_pk_bf16_f32 v72, v72, v73\n" \
"v_exp_f32_e32 v76, v76\n" \
"v_exp_f32_e32 v77, v77\n" \
"v_exp_f32_e32 v78, v78\n" \
"v_exp_f32_e32 v79, v79\n" \
"buffer_load_dwordx4 v7, s[84:87], s92 offen offset:3072 lds\n" \
"s_add_u32 m0, s91, 0x2000\n" \
"v_cvt_pk_bf16_f32 v70, v68, v69\n" \
"v_cvt_pk_bf16_f32 v73, v74, v75\n" \
"v_add_u32_e32 v49, 0x10000, v48\n" \
"s_xor_b32 s65, s65, 0x8000\n" \
"v_cvt_pk_bf16_f32 v69, v66, v67\n" \
"v_cvt_pk_bf16_f32 v68, v64, v65\n" \
"v_cvt_pk_bf16_f32 v74, v76, v77\n" \
"v_cvt_pk_bf16_f32 v75, v78, v79\n" \
"v_xor_b32_e32 v48, 0x8000, v48\n" \
"s_sub_u32 s66, s19, 2\n" \
"buffer_load_dwordx4 v8, s[84:87], s92 offen offset:0 lds\n" \
"s_waitcnt vmcnt(18) lgkmcnt(0)\n" \
"s_barrier\n" \
"s_branch L_f1_%=\n" \
".fill (L_ab + 32768 - .) / 4, 4, 0xBF800000\n" \
".fill (L_ab + 32768 - .) / 4, 4, 0xBF800000\n" \
"L_ap8:\n" \
"s_setpc_b64 s[88:89]\n" \
"L_out_%=:\n" \
"v_bfe_u32 v38, v20, 2, 4\n" \
"v_readlane_b32 s64, v11, 2\n" \
"v_add_u32_e32 v32, 0x49000, v20\n" \
"v_add_u32_e32 v34, 0x20000, v10\n" \
"ds_read_b64_tr_b16 a[0:1], v34 offset:0\n" \
"ds_read_b64_tr_b16 a[2:3], v34 offset:4608\n" \
"ds_read_b64_tr_b16 a[4:5], v34 offset:32\n" \
"buffer_load_dword v26, v38, s[56:59], s64 offen sc1\n" \
"ds_read_b64_tr_b16 a[6:7], v34 offset:4640\n" \
"ds_read_b64_tr_b16 a[8:9], v34 offset:64\n" \
"ds_read_b64_tr_b16 a[10:11], v34 offset:4672\n" \
"ds_read_b64_tr_b16 a[12:13], v34 offset:96\n" \
"buffer_store_dwordx4 v[64:67], v20, s[56:59], s61 offen offset:0 sc1\n" \
"ds_read_b64_tr_b16 a[14:15], v34 offset:4704\n" \
"v_mov_b32_e32 v39, s62\n" \
"v_readlane_b32 s70, v11, 5\n" \
"v_readlane_b32 s71, v11, 4\n" \
"buffer_store_dwordx4 v[68:71], v20, s[56:59], s61 offen offset:1024 sc1\n" \
"ds_read_b64_tr_b16 a[16:17], v34 offset:128\n" \
"ds_read_b64_tr_b16 a[18:19], v34 offset:4736\n" \
"ds_read_b64_tr_b16 a[20:21], v34 offset:160\n" \
"ds_read_b64_tr_b16 a[22:23], v34 offset:4768\n" \
"buffer_store_dwordx4 v[72:75], v20, s[56:59], s61 offen offset:2048 sc1\n" \
"ds_read_b64_tr_b16 a[24:25], v34 offset:192\n" \
"ds_read_b64_tr_b16 a[26:27], v34 offset:4800\n" \
"ds_read_b64_tr_b16 a[28:29], v34 offset:224\n" \
"ds_read_b64_tr_b16 a[30:31], v34 offset:4832\n" \
"buffer_store_dwordx4 v[76:79], v20, s[56:59], s61 offen offset:3072 sc1\n" \
"ds_read_b64_tr_b16 a[192:193], v34 offset:256\n" \
"ds_read_b64_tr_b16 a[194:195], v34 offset:4864\n" \
"ds_read_b64_tr_b16 a[196:197], v34 offset:9216\n" \
"ds_read_b64_tr_b16 a[198:199], v34 offset:13824\n" \
"buffer_store_dwordx4 v[80:83], v32, s[56:59], s61 offen offset:0 sc1\n" \
"ds_read_b64_tr_b16 a[200:201], v34 offset:9248\n" \
"ds_read_b64_tr_b16 a[202:203], v34 offset:13856\n" \
"ds_read_b64_tr_b16 a[204:205], v34 offset:9280\n" \
"ds_read_b64_tr_b16 a[206:207], v34 offset:13888\n" \
"buffer_store_dwordx4 v[84:87], v32, s[56:59], s61 offen offset:1024 sc1\n" \
"ds_read_b64_tr_b16 a[208:209], v34 offset:9312\n" \
"s_waitcnt vmcnt(16) lgkmcnt(15)\n" \
"v_mfma_f32_16x16x32_bf16 v[112:115], a[0:3], a[96:99], 0\n" \
"buffer_store_dwordx4 v[96:99], v32, s[56:59], s61 offen offset:2048 sc1\n" \
"v_mfma_f32_16x16x32_bf16 v[116:119], a[0:3], a[112:115], 0\n" \
"ds_read_b64_tr_b16 a[210:211], v34 offset:13920\n" \
"ds_read_b64_tr_b16 a[212:213], v34 offset:9344\n" \
"buffer_store_dwordx4 v[100:103], v32, s[56:59], s61 offen offset:3072 sc1\n" \
"v_mfma_f32_16x16x32_bf16 v[184:187], a[0:3], v[64:67], 0\n" \
"ds_read_b64_tr_b16 a[214:215], v34 offset:13952\n" \
"ds_read_b64_tr_b16 a[216:217], v34 offset:9376\n" \
"v_mfma_f32_16x16x32_bf16 v[188:191], a[0:3], v[80:83], 0\n" \
"ds_read_b64_tr_b16 a[218:219], v34 offset:13984\n" \
"ds_read_b64_tr_b16 a[0:1], v34 offset:9408\n" \
"v_mfma_f32_16x16x32_bf16 v[120:123], a[4:7], a[96:99], 0\n" \
"ds_read_b64_tr_b16 a[2:3], v34 offset:14016\n" \
"v_readlane_b32 s65, v11, 30\n" \
"v_mfma_f32_16x16x32_bf16 v[124:127], a[4:7], a[112:115], 0\n" \
"v_lshrrev_b32_e32 v44, 9, v20\n" \
"v_mul_u32_u24_e32 v44, 0xb40, v44\n" \
"v_mfma_f32_16x16x32_bf16 v[192:195], a[4:7], v[64:67], 0\n" \
"v_add_u32_e32 v35, v44, v10\n" \
"v_readlane_b32 s67, v11, 23\n" \
"v_mfma_f32_16x16x32_bf16 v[196:199], a[4:7], v[80:83], 0\n" \
"ds_read_b64_tr_b16 a[4:5], v34 offset:9440\n" \
"ds_read_b64_tr_b16 a[6:7], v34 offset:14048\n" \
"v_mfma_f32_16x16x32_bf16 v[128:131], a[8:11], a[96:99], 0\n" \
"v_readlane_b32 s80, v11, 11\n" \
"v_readlane_b32 s81, v11, 12\n" \
"v_mfma_f32_16x16x32_bf16 v[132:135], a[8:11], a[112:115], 0\n" \
"s_mov_b32 s82, 0x47fca0\n" \
"s_mov_b32 s83, 0x20000\n" \
"v_mfma_f32_16x16x32_bf16 v[200:203], a[8:11], v[64:67], 0\n" \
"v_readlane_b32 s66, v11, 28\n" \
"s_add_u32 m0, s67, 0x0\n" \
"v_mfma_f32_16x16x32_bf16 v[204:207], a[8:11], v[80:83], 0\n" \
"ds_read_b64_tr_b16 a[8:9], v34 offset:9472\n" \
"ds_read_b64_tr_b16 a[10:11], v34 offset:14080\n" \
"v_mfma_f32_16x16x32_bf16 v[136:139], a[12:15], a[96:99], 0\n" \
"buffer_load_dwordx4 v0, s[80:83], s66 offen offset:0 lds\n" \
"v_mfma_f32_16x16x32_bf16 v[140:143], a[12:15], a[112:115], 0\n" \
"v_readlane_b32 s92, v11, 24\n" \
"v_readlane_b32 s73, v11, 29\n" \
"v_mfma_f32_16x16x32_bf16 v[208:211], a[12:15], v[64:67], 0\n" \
"v_readlane_b32 s94, v11, 25\n" \
"v_readlane_b32 s84, v11, 9\n" \
"v_mfma_f32_16x16x32_bf16 v[212:215], a[12:15], v[80:83], 0\n" \
"ds_read_b64_tr_b16 a[12:13], v34 offset:18432\n" \
"ds_read_b64_tr_b16 a[14:15], v34 offset:23040\n" \
"v_mfma_f32_16x16x32_bf16 v[144:147], a[16:19], a[96:99], 0\n" \
"v_readlane_b32 s85, v11, 10\n" \
"s_mov_b32 s86, 0xa1ca0\n" \
"v_mfma_f32_16x16x32_bf16 v[148:151], a[16:19], a[112:115], 0\n" \
"buffer_load_dwordx4 v1, s[80:83], s66 offen offset:1024 lds\n" \
"v_mfma_f32_16x16x32_bf16 v[216:219], a[16:19], v[64:67], 0\n" \
"s_mov_b32 s87, 0x20000\n" \
"v_readlane_b32 s68, v11, 31\n" \
"v_mfma_f32_16x16x32_bf16 v[220:223], a[16:19], v[80:83], 0\n" \
"ds_read_b64_tr_b16 a[16:17], v34 offset:18464\n" \
"s_waitcnt lgkmcnt(7)\n" \
"v_mfma_f32_16x16x32_bf16 v[152:155], a[20:23], a[96:99], 0\n" \
"ds_read_b64_tr_b16 a[18:19], v34 offset:23072\n" \
"v_add_u32_e32 v33, 0x10000, v10\n" \
"v_mfma_f32_16x16x32_bf16 v[156:159], a[20:23], a[112:115], 0\n" \
"v_readlane_b32 s69, v11, 32\n" \
"v_readlane_b32 s72, v11, 34\n" \
"v_mfma_f32_16x16x32_bf16 v[224:227], a[20:23], v[64:67], 0\n" \
"buffer_load_dwordx4 v2, s[80:83], s66 offen offset:2048 lds\n" \
"v_mfma_f32_16x16x32_bf16 v[228:231], a[20:23], v[80:83], 0\n" \
"ds_read_b64_tr_b16 a[20:21], v34 offset:18496\n" \
"ds_read_b64_tr_b16 a[22:23], v34 offset:23104\n" \
"v_mfma_f32_16x16x32_bf16 v[160:163], a[24:27], a[96:99], 0\n" \
"v_readlane_b32 s93, v11, 35\n" \
"v_readlane_b32 s63, v11, 41\n" \
"v_mfma_f32_16x16x32_bf16 v[164:167], a[24:27], a[112:115], 0\n" \
"v_bfe_u32 v40, v20, 2, 7\n" \
"s_add_u32 s74, s63, 0x3800\n" \
"v_mfma_f32_16x16x32_bf16 v[232:235], a[24:27], v[64:67], 0\n" \
"v_lshlrev_b32_e32 v41, 12, v20\n" \
"s_add_u32 s12, s63, 0x0\n" \
"v_mfma_f32_16x16x32_bf16 v[236:239], a[24:27], v[80:83], 0\n" \
"ds_read_b64_tr_b16 a[24:25], v34 offset:18528\n" \
"ds_read_b64_tr_b16 a[26:27], v34 offset:23136\n" \
"v_mfma_f32_16x16x32_bf16 v[168:171], a[28:31], a[96:99], 0\n" \
"buffer_load_dwordx4 v3, s[80:83], s66 offen offset:3072 lds\n" \
"v_mfma_f32_16x16x32_bf16 v[172:175], a[28:31], a[112:115], 0\n" \
"s_add_u32 m0, s67, 0x1000\n" \
"v_readlane_b32 s95, v11, 38\n" \
"v_mfma_f32_16x16x32_bf16 v[240:243], a[28:31], v[64:67], 0\n" \
"v_readlane_b32 s96, v11, 39\n" \
"s_add_u32 s13, s63, 0x1000\n" \
"v_mfma_f32_16x16x32_bf16 v[244:247], a[28:31], v[80:83], 0\n" \
"ds_read_b64_tr_b16 a[28:29], v34 offset:18560\n" \
"ds_read_b64_tr_b16 a[30:31], v34 offset:23168\n" \
"v_mfma_f32_16x16x32_bf16 v[176:179], a[192:195], a[96:99], 0\n" \
"v_readlane_b32 s97, v11, 40\n" \
"s_add_u32 s14, s63, 0x2000\n" \
"v_mfma_f32_16x16x32_bf16 v[180:183], a[192:195], a[112:115], 0\n" \
"buffer_load_dwordx4 v4, s[80:83], s66 offen offset:0 lds\n" \
"v_mfma_f32_16x16x32_bf16 v[248:251], a[192:195], v[64:67], 0\n" \
"s_add_u32 s15, s63, 0x3000\n" \
"v_readlane_b32 s100, v11, 7\n" \
"v_mfma_f32_16x16x32_bf16 v[252:255], a[192:195], v[80:83], 0\n" \
"ds_read_b64_tr_b16 a[192:193], v34 offset:18592\n" \
"ds_read_b64_tr_b16 a[194:195], v34 offset:23200\n" \
"v_mfma_f32_16x16x32_bf16 v[112:115], a[196:199], a[100:103], v[112:115]\n" \
"v_readlane_b32 s16, v11, 42\n" \
"v_bfe_u32 v44, v20, 4, 4\n" \
"v_mfma_f32_16x16x32_bf16 v[116:119], a[196:199], a[116:119], v[116:119]\n" \
"s_add_u32 s17, s16, 0x1000\n" \
"s_add_u32 s18, s16, 0x2000\n" \
"v_mfma_f32_16x16x32_bf16 v[184:187], a[196:199], v[68:71], v[184:187]\n" \
"buffer_load_dwordx4 v5, s[80:83], s66 offen offset:1024 lds\n" \
"v_mfma_f32_16x16x32_bf16 v[188:191], a[196:199], v[84:87], v[188:191]\n" \
"ds_read_b64_tr_b16 a[196:197], v34 offset:18624\n" \
"ds_read_b64_tr_b16 a[198:199], v34 offset:23232\n" \
"v_mfma_f32_16x16x32_bf16 v[120:123], a[200:203], a[100:103], v[120:123]\n" \
"s_add_u32 s19, s16, 0x3000\n" \
"v_bfe_u32 v45, v20, 8, 2\n" \
"v_mfma_f32_16x16x32_bf16 v[124:127], a[200:203], a[116:119], v[124:127]\n" \
"v_mul_u32_u24_e32 v42, 0x480, v44\n" \
"v_lshl_add_u32 v42, v45, 3, v42\n" \
"v_mfma_f32_16x16x32_bf16 v[192:195], a[200:203], v[68:71], v[192:195]\n" \
"s_waitcnt vmcnt(14)\n" \
"v_cmp_ne_u32_e32 vcc, s62, v26\n" \
"v_mfma_f32_16x16x32_bf16 v[196:199], a[200:203], v[84:87], v[196:199]\n" \
"ds_read_b64_tr_b16 a[200:201], v34 offset:18656\n" \
"ds_read_b64_tr_b16 a[202:203], v34 offset:23264\n" \
"v_mfma_f32_16x16x32_bf16 v[128:131], a[204:207], a[100:103], v[128:131]\n" \
"buffer_load_dwordx4 v6, s[80:83], s66 offen offset:2048 lds\n" \
"v_mfma_f32_16x16x32_bf16 v[132:135], a[204:207], a[116:119], v[132:135]\n" \
"s_cbranch_vccnz L_sa_%=\n" \
"L_ra_%=:\n" \
"v_readlane_b32 s64, v11, 33\n" \
"v_mfma_f32_16x16x32_bf16 v[200:203], a[204:207], v[68:71], v[200:203]\n" \
"v_lshrrev_b32_e32 v44, 1, v44\n" \
"v_lshlrev_b32_e32 v45, 1, v45\n" \
"v_mfma_f32_16x16x32_bf16 v[204:207], a[204:207], v[84:87], v[204:207]\n" \
"ds_read_b64_tr_b16 a[204:205], v34 offset:18688\n" \
"ds_read_b64_tr_b16 a[206:207], v34 offset:23296\n" \
"v_mfma_f32_16x16x32_bf16 v[136:139], a[208:211], a[100:103], v[136:139]\n" \
"v_sub_u32_e32 v44, v44, v45\n" \
"v_and_b32_e32 v45, 16, v20\n" \
"v_mfma_f32_16x16x32_bf16 v[140:143], a[208:211], a[116:119], v[140:143]\n" \
"buffer_load_dwordx4 a[64:67], v20, s[56:59], s65 offen offset:0 sc1\n" \
"v_mfma_f32_16x16x32_bf16 v[208:211], a[208:211], v[68:71], v[208:211]\n" \
"v_mov_b32_e32 v46, 0x3f80\n" \
"v_lshlrev_b32_e32 v45, v45, v46\n" \
"v_mfma_f32_16x16x32_bf16 v[212:215], a[208:211], v[84:87], v[212:215]\n" \
"v_mov_b32_e32 v52, 0\n" \
"v_mov_b32_e32 v53, 0\n" \
"v_mfma_f32_16x16x32_bf16 v[144:147], a[212:215], a[100:103], v[144:147]\n" \
"v_mov_b32_e32 v48, 0\n" \
"v_mov_b32_e32 v49, 0\n" \
"v_mfma_f32_16x16x32_bf16 v[148:151], a[212:215], a[116:119], v[148:151]\n" \
"v_add_u32_e32 v43, 0x9000, v42\n" \
"v_readlane_b32 s88, v11, 13\n" \
"v_mfma_f32_16x16x32_bf16 v[216:219], a[212:215], v[68:71], v[216:219]\n" \
"buffer_load_dwordx4 a[80:83], v32, s[56:59], s65 offen offset:0 sc1\n" \
"v_mfma_f32_16x16x32_bf16 v[220:223], a[212:215], v[84:87], v[220:223]\n" \
"v_readlane_b32 s89, v11, 14\n" \
"s_mov_b32 s90, 0x240000\n" \
"v_mfma_f32_16x16x32_bf16 v[152:155], a[216:219], a[100:103], v[152:155]\n" \
"s_mov_b32 s91, 0x20000\n" \
"v_mov_b32_e32 v46, s62\n" \
"v_mfma_f32_16x16x32_bf16 v[156:159], a[216:219], a[116:119], v[156:159]\n" \
"v_mfma_f32_16x16x32_bf16 v[224:227], a[216:219], v[68:71], v[224:227]\n" \
"v_mfma_f32_16x16x32_bf16 v[228:231], a[216:219], v[84:87], v[228:231]\n" \
"buffer_load_dwordx4 a[68:71], v20, s[56:59], s65 offen offset:1024 sc1\n" \
"v_mfma_f32_16x16x32_bf16 v[160:163], a[0:3], a[100:103], v[160:163]\n" \
"v_mfma_f32_16x16x32_bf16 v[164:167], a[0:3], a[116:119], v[164:167]\n" \
"v_mfma_f32_16x16x32_bf16 v[232:235], a[0:3], v[68:71], v[232:235]\n" \
"v_mfma_f32_16x16x32_bf16 v[236:239], a[0:3], v[84:87], v[236:239]\n" \
"s_waitcnt lgkmcnt(0)\n" \
"v_mfma_f32_16x16x32_bf16 v[168:171], a[4:7], a[100:103], v[168:171]\n" \
"buffer_load_dwordx4 a[84:87], v32, s[56:59], s65 offen offset:1024 sc1\n" \
"v_mfma_f32_16x16x32_bf16 v[172:175], a[4:7], a[116:119], v[172:175]\n" \
"v_mfma_f32_16x16x32_bf16 v[240:243], a[4:7], v[68:71], v[240:243]\n" \
"v_mfma_f32_16x16x32_bf16 v[244:247], a[4:7], v[84:87], v[244:247]\n" \
"v_mfma_f32_16x16x32_bf16 v[176:179], a[8:11], a[100:103], v[176:179]\n" \
"v_mfma_f32_16x16x32_bf16 v[180:183], a[8:11], a[116:119], v[180:183]\n" \
"buffer_load_dwordx4 a[72:75], v20, s[56:59], s65 offen offset:2048 sc1\n" \
"v_mfma_f32_16x16x32_bf16 v[248:251], a[8:11], v[68:71], v[248:251]\n" \
"v_mfma_f32_16x16x32_bf16 v[252:255], a[8:11], v[84:87], v[252:255]\n" \
"v_mfma_f32_16x16x32_bf16 v[112:115], a[12:15], a[104:107], v[112:115]\n" \
"v_mfma_f32_16x16x32_bf16 v[116:119], a[12:15], a[120:123], v[116:119]\n" \
"v_mfma_f32_16x16x32_bf16 v[184:187], a[12:15], v[72:75], v[184:187]\n" \
"buffer_load_dwordx4 a[88:91], v32, s[56:59], s65 offen offset:2048 sc1\n" \
"v_mfma_f32_16x16x32_bf16 v[188:191], a[12:15], v[96:99], v[188:191]\n" \
"v_mfma_f32_16x16x32_bf16 v[120:123], a[16:19], a[104:107], v[120:123]\n" \
"v_mfma_f32_16x16x32_bf16 v[124:127], a[16:19], a[120:123], v[124:127]\n" \
"v_mfma_f32_16x16x32_bf16 v[192:195], a[16:19], v[72:75], v[192:195]\n" \
"v_mfma_f32_16x16x32_bf16 v[196:199], a[16:19], v[96:99], v[196:199]\n" \
"buffer_load_dwordx4 v7, s[80:83], s66 offen offset:3072 lds\n" \
"v_mfma_f32_16x16x32_bf16 v[128:131], a[20:23], a[104:107], v[128:131]\n" \
"s_add_u32 m0, s67, 0x2000\n" \
"v_readlane_b32 s67, v11, 48\n" \
"v_mfma_f32_16x16x32_bf16 v[132:135], a[20:23], a[120:123], v[132:135]\n" \
"v_mfma_f32_16x16x32_bf16 v[200:203], a[20:23], v[72:75], v[200:203]\n" \
"v_mfma_f32_16x16x32_bf16 v[204:207], a[20:23], v[96:99], v[204:207]\n" \
"s_barrier\n" \
"v_mfma_f32_16x16x32_bf16 v[136:139], a[24:27], a[104:107], v[136:139]\n" \
"ds_read_b64_tr_b16 a[208:209], v10 offset:32768\n" \
"ds_read_b64_tr_b16 a[210:211], v10 offset:37376\n" \
"v_mfma_f32_16x16x32_bf16 v[140:143], a[24:27], a[120:123], v[140:143]\n" \
"ds_read_b64_tr_b16 a[212:213], v10 offset:32800\n" \
"ds_read_b64_tr_b16 a[214:215], v10 offset:37408\n" \
"v_mfma_f32_16x16x32_bf16 v[208:211], a[24:27], v[72:75], v[208:211]\n" \
"ds_read_b64_tr_b16 a[216:217], v10 offset:32832\n" \
"ds_read_b64_tr_b16 a[218:219], v10 offset:37440\n" \
"v_mfma_f32_16x16x32_bf16 v[212:215], a[24:27], v[96:99], v[212:215]\n" \
"ds_read_b64_tr_b16 a[0:1], v10 offset:32864\n" \
"ds_read_b64_tr_b16 a[2:3], v10 offset:37472\n" \
"v_mfma_f32_16x16x32_bf16 v[144:147], a[28:31], a[104:107], v[144:147]\n" \
"ds_read_b64_tr_b16 a[4:5], v10 offset:32896\n" \
"ds_read_b64_tr_b16 a[6:7], v10 offset:37504\n" \
"v_mfma_f32_16x16x32_bf16 v[148:151], a[28:31], a[120:123], v[148:151]\n" \
"ds_read_b64_tr_b16 a[8:9], v10 offset:32928\n" \
"ds_read_b64_tr_b16 a[10:11], v10 offset:37536\n" \
"v_mfma_f32_16x16x32_bf16 v[216:219], a[28:31], v[72:75], v[216:219]\n" \
"ds_read_b64_tr_b16 a[12:13], v10 offset:32960\n" \
"ds_read_b64_tr_b16 a[14:15], v10 offset:37568\n" \
"v_mfma_f32_16x16x32_bf16 v[220:223], a[28:31], v[96:99], v[220:223]\n" \
"ds_read_b64_tr_b16 a[16:17], v10 offset:32992\n" \
"ds_read_b64_tr_b16 a[18:19], v10 offset:37600\n" \
"v_mfma_f32_16x16x32_bf16 v[152:155], a[192:195], a[104:107], v[152:155]\n" \
"ds_read_b64_tr_b16 a[20:21], v10 offset:33024\n" \
"ds_read_b64_tr_b16 a[22:23], v10 offset:37632\n" \
"v_mfma_f32_16x16x32_bf16 v[156:159], a[192:195], a[120:123], v[156:159]\n" \
"ds_read_b64_tr_b16 a[24:25], v35 offset:41984\n" \
"ds_read_b64_tr_b16 a[26:27], v35 offset:44288\n" \
"v_mfma_f32_16x16x32_bf16 v[224:227], a[192:195], v[72:75], v[224:227]\n" \
"ds_read_b64_tr_b16 a[28:29], v35 offset:42016\n" \
"ds_read_b64_tr_b16 a[30:31], v35 offset:44320\n" \
"v_mfma_f32_16x16x32_bf16 v[228:231], a[192:195], v[96:99], v[228:231]\n" \
"ds_read_b64_tr_b16 a[192:193], v35 offset:42048\n" \
"ds_read_b64_tr_b16 a[194:195], v35 offset:44352\n" \
"v_mfma_f32_16x16x32_bf16 v[160:163], a[196:199], a[104:107], v[160:163]\n" \
"buffer_load_dwordx4 v8, s[80:83], s66 offen offset:0 lds\n" \
"v_mfma_f32_16x16x32_bf16 v[164:167], a[196:199], a[120:123], v[164:167]\n" \
"s_add_u32 m0, s92, 0x0\n" \
"v_readlane_b32 s66, v11, 47\n" \
"v_mfma_f32_16x16x32_bf16 v[232:235], a[196:199], v[72:75], v[232:235]\n" \
"v_mfma_f32_16x16x32_bf16 v[236:239], a[196:199], v[96:99], v[236:239]\n" \
"ds_read_b64_tr_b16 a[196:197], v35 offset:42080\n" \
"ds_read_b64_tr_b16 a[198:199], v35 offset:44384\n" \
"v_mfma_f32_16x16x32_bf16 v[168:171], a[200:203], a[104:107], v[168:171]\n" \
"v_mfma_f32_16x16x32_bf16 v[172:175], a[200:203], a[120:123], v[172:175]\n" \
"buffer_load_dwordx4 v0, s[80:83], s73 offen offset:0 lds\n" \
"v_mfma_f32_16x16x32_bf16 v[240:243], a[200:203], v[72:75], v[240:243]\n" \
"v_mfma_f32_16x16x32_bf16 v[244:247], a[200:203], v[96:99], v[244:247]\n" \
"ds_read_b64_tr_b16 a[200:201], v35 offset:42112\n" \
"ds_read_b64_tr_b16 a[202:203], v35 offset:44416\n" \
"v_mfma_f32_16x16x32_bf16 v[176:179], a[204:207], a[104:107], v[176:179]\n" \
"v_mfma_f32_16x16x32_bf16 v[180:183], a[204:207], a[120:123], v[180:183]\n" \
"v_mfma_f32_16x16x32_bf16 v[248:251], a[204:207], v[72:75], v[248:251]\n" \
"buffer_load_dwordx4 v1, s[80:83], s73 offen offset:1024 lds\n" \
"v_mfma_f32_16x16x32_bf16 v[252:255], a[204:207], v[96:99], v[252:255]\n" \
"ds_read_b64_tr_b16 a[204:205], v35 offset:42144\n" \
"s_waitcnt lgkmcnt(9)\n" \
"v_mfma_f32_16x16x32_bf16 v[112:115], a[208:211], a[108:111], v[112:115]\n" \
"ds_read_b64_tr_b16 a[206:207], v35 offset:44448\n" \
"v_mfma_f32_16x16x32_bf16 v[116:119], a[208:211], a[124:127], v[116:119]\n" \
"v_mfma_f32_16x16x32_bf16 v[184:187], a[208:211], v[76:79], v[184:187]\n" \
"v_mfma_f32_16x16x32_bf16 v[188:191], a[208:211], v[100:103], v[188:191]\n" \
"ds_read_b64_tr_b16 a[208:209], v35 offset:42176\n" \
"ds_read_b64_tr_b16 a[210:211], v35 offset:44480\n" \
"v_mfma_f32_16x16x32_bf16 v[120:123], a[212:215], a[108:111], v[120:123]\n" \
"buffer_load_dwordx4 v2, s[80:83], s73 offen offset:2048 lds\n" \
"v_mfma_f32_16x16x32_bf16 v[124:127], a[212:215], a[124:127], v[124:127]\n" \
"v_mfma_f32_16x16x32_bf16 v[192:195], a[212:215], v[76:79], v[192:195]\n" \
"s_waitcnt vmcnt(18)\n" \
"s_mov_b64 exec, 1\n" \
"buffer_store_dword v39, off, s[56:59], s70 offset:0 sc1\n" \
"s_mov_b64 exec, -1\n" \
"v_mfma_f32_16x16x32_bf16 v[196:199], a[212:215], v[100:103], v[196:199]\n" \
"ds_read_b64_tr_b16 a[212:213], v35 offset:42208\n" \
"ds_read_b64_tr_b16 a[214:215], v35 offset:44512\n" \
"v_mfma_f32_16x16x32_bf16 v[128:131], a[216:219], a[108:111], v[128:131]\n" \
"v_readlane_b32 s70, v11, 51\n" \
"v_mfma_f32_16x16x32_bf16 v[132:135], a[216:219], a[124:127], v[132:135]\n" \
"v_mfma_f32_16x16x32_bf16 v[200:203], a[216:219], v[76:79], v[200:203]\n" \
"buffer_load_dwordx4 v3, s[80:83], s73 offen offset:3072 lds\n" \
"v_mfma_f32_16x16x32_bf16 v[204:207], a[216:219], v[100:103], v[204:207]\n" \
"ds_read_b64_tr_b16 a[216:217], v35 offset:42240\n" \
"ds_read_b64_tr_b16 a[218:219], v35 offset:44544\n" \
"v_mfma_f32_16x16x32_bf16 v[136:139], a[0:3], a[108:111], v[136:139]\n" \
"s_add_u32 m0, s92, 0x1000\n" \
"v_mfma_f32_16x16x32_bf16 v[140:143], a[0:3], a[124:127], v[140:143]\n" \
"v_mfma_f32_16x16x32_bf16 v[208:211], a[0:3], v[76:79], v[208:211]\n" \
"v_mfma_f32_16x16x32_bf16 v[212:215], a[0:3], v[100:103], v[212:215]\n" \
"ds_read_b64_tr_b16 a[0:1], v35 offset:52352\n" \
"ds_read_b64_tr_b16 a[2:3], v35 offset:54656\n" \
"v_mfma_f32_16x16x32_bf16 v[144:147], a[4:7], a[108:111], v[144:147]\n" \
"buffer_load_dwordx4 v4, s[80:83], s73 offen offset:0 lds\n" \
"v_mfma_f32_16x16x32_bf16 v[148:151], a[4:7], a[124:127], v[148:151]\n" \
"v_mfma_f32_16x16x32_bf16 v[216:219], a[4:7], v[76:79], v[216:219]\n" \
"v_mfma_f32_16x16x32_bf16 v[220:223], a[4:7], v[100:103], v[220:223]\n" \
"ds_read_b64_tr_b16 a[4:5], v35 offset:52384\n" \
"ds_read_b64_tr_b16 a[6:7], v35 offset:54688\n" \
"v_mfma_f32_16x16x32_bf16 v[152:155], a[8:11], a[108:111], v[152:155]\n" \
"v_mfma_f32_16x16x32_bf16 v[156:159], a[8:11], a[124:127], v[156:159]\n" \
"buffer_load_dwordx4 v5, s[80:83], s73 offen offset:1024 lds\n" \
"v_mfma_f32_16x16x32_bf16 v[224:227], a[8:11], v[76:79], v[224:227]\n" \
"v_mfma_f32_16x16x32_bf16 v[228:231], a[8:11], v[100:103], v[228:231]\n" \
"ds_read_b64_tr_b16 a[8:9], v35 offset:52416\n" \
"ds_read_b64_tr_b16 a[10:11], v35 offset:54720\n" \
"v_mfma_f32_16x16x32_bf16 v[160:163], a[12:15], a[108:111], v[160:163]\n" \
"v_mfma_f32_16x16x32_bf16 v[164:167], a[12:15], a[124:127], v[164:167]\n" \
"v_mfma_f32_16x16x32_bf16 v[232:235], a[12:15], v[76:79], v[232:235]\n" \
"buffer_load_dwordx4 v6, s[80:83], s73 offen offset:2048 lds\n" \
"v_mfma_f32_16x16x32_bf16 v[236:239], a[12:15], v[100:103], v[236:239]\n" \
"ds_read_b64_tr_b16 a[12:13], v35 offset:52448\n" \
"ds_read_b64_tr_b16 a[14:15], v35 offset:54752\n" \
"v_mfma_f32_16x16x32_bf16 v[168:171], a[16:19], a[108:111], v[168:171]\n" \
"v_mfma_f32_16x16x32_bf16 v[172:175], a[16:19], a[124:127], v[172:175]\n" \
"v_mfma_f32_16x16x32_bf16 v[240:243], a[16:19], v[76:79], v[240:243]\n" \
"v_mfma_f32_16x16x32_bf16 v[244:247], a[16:19], v[100:103], v[244:247]\n" \
"buffer_load_dwordx4 v7, s[80:83], s73 offen offset:3072 lds\n" \
"v_mfma_f32_16x16x32_bf16 v[176:179], a[20:23], a[108:111], v[176:179]\n" \
"s_add_u32 m0, s92, 0x2000\n" \
"ds_read_b64_tr_b16 a[16:17], v35 offset:52480\n" \
"v_mfma_f32_16x16x32_bf16 v[180:183], a[20:23], a[124:127], v[180:183]\n" \
"ds_read_b64_tr_b16 a[18:19], v35 offset:54784\n" \
"v_readlane_b32 s92, v11, 46\n" \
"v_mfma_f32_16x16x32_bf16 v[248:251], a[20:23], v[76:79], v[248:251]\n" \
"v_mfma_f32_16x16x32_bf16 v[252:255], a[20:23], v[100:103], v[252:255]\n" \
"ds_read_b64_tr_b16 a[20:21], v35 offset:52512\n" \
"ds_read_b64_tr_b16 a[22:23], v35 offset:54816\n" \
"v_mfma_f32_16x16x32_bf16 v[112:115], a[24:27], a[220:223], v[112:115]\n" \
"buffer_load_dwordx4 v8, s[80:83], s73 offen offset:0 lds\n" \
"v_mfma_f32_16x16x32_bf16 v[116:119], a[24:27], a[228:231], v[116:119]\n" \
"s_mov_b32 m0, s94\n" \
"v_readlane_b32 s73, v11, 6\n" \
"v_mfma_f32_16x16x32_bf16 v[184:187], a[24:27], a[236:239], v[184:187]\n" \
"v_mfma_f32_16x16x32_bf16 v[188:191], a[24:27], a[244:247], v[188:191]\n" \
"ds_read_b64_tr_b16 a[24:25], v35 offset:52544\n" \
"s_waitcnt lgkmcnt(7)\n" \
"v_mfma_f32_16x16x32_bf16 v[120:123], a[28:31], a[220:223], v[120:123]\n" \
"ds_read_b64_tr_b16 a[26:27], v35 offset:54848\n" \
"buffer_load_dword v47, v38, s[56:59], s71 offen sc1\n" \
"v_mfma_f32_16x16x32_bf16 v[124:127], a[28:31], a[228:231], v[124:127]\n" \
"v_mfma_f32_16x16x32_bf16 v[192:195], a[28:31], a[236:239], v[192:195]\n" \
"v_mfma_f32_16x16x32_bf16 v[196:199], a[28:31], a[244:247], v[196:199]\n" \
"ds_read_b64_tr_b16 a[28:29], v35 offset:52576\n" \
"ds_read_b64_tr_b16 a[30:31], v35 offset:54880\n" \
"v_mfma_f32_16x16x32_bf16 v[128:131], a[192:195], a[220:223], v[128:131]\n" \
"v_mfma_f32_16x16x32_bf16 v[132:135], a[192:195], a[228:231], v[132:135]\n" \
"buffer_load_dwordx4 v9, s[84:87], 0 offen lds\n" \
"v_mfma_f32_16x16x32_bf16 v[200:203], a[192:195], a[236:239], v[200:203]\n" \
"v_mfma_f32_16x16x32_bf16 v[204:207], a[192:195], a[244:247], v[204:207]\n" \
"ds_read_b64_tr_b16 a[192:193], v35 offset:52608\n" \
"ds_read_b64_tr_b16 a[194:195], v35 offset:54912\n" \
"v_mfma_f32_16x16x32_bf16 v[136:139], a[196:199], a[220:223], v[136:139]\n" \
"v_mfma_f32_16x16x32_bf16 v[140:143], a[196:199], a[228:231], v[140:143]\n" \
"v_mfma_f32_16x16x32_bf16 v[208:211], a[196:199], a[236:239], v[208:211]\n" \
"buffer_load_dwordx4 a[76:79], v20, s[56:59], s65 offen offset:3072 sc1\n" \
"v_mfma_f32_16x16x32_bf16 v[212:215], a[196:199], a[244:247], v[212:215]\n" \
"v_mfma_f32_16x16x32_bf16 v[144:147], a[200:203], a[220:223], v[144:147]\n" \
"v_mfma_f32_16x16x32_bf16 v[148:151], a[200:203], a[228:231], v[148:151]\n" \
"v_mfma_f32_16x16x32_bf16 v[216:219], a[200:203], a[236:239], v[216:219]\n" \
"v_mfma_f32_16x16x32_bf16 v[220:223], a[200:203], a[244:247], v[220:223]\n" \
"buffer_load_dwordx4 a[92:95], v32, s[56:59], s65 offen offset:3072 sc1\n" \
"v_mfma_f32_16x16x32_bf16 v[152:155], a[204:207], a[220:223], v[152:155]\n" \
"v_readlane_b32 s65, v11, 46\n" \
"v_mfma_f32_16x16x32_bf16 v[156:159], a[204:207], a[228:231], v[156:159]\n" \
"v_mfma_f32_16x16x32_bf16 v[224:227], a[204:207], a[236:239], v[224:227]\n" \
"v_mfma_f32_16x16x32_bf16 v[228:231], a[204:207], a[244:247], v[228:231]\n" \
"v_mfma_f32_16x16x32_bf16 v[160:163], a[208:211], a[220:223], v[160:163]\n" \
"buffer_load_dwordx4 a[128:131], v20, s[56:59], s68 offen offset:0 sc1\n" \
"v_mfma_f32_16x16x32_bf16 v[164:167], a[208:211], a[228:231], v[164:167]\n" \
"v_mfma_f32_16x16x32_bf16 v[232:235], a[208:211], a[236:239], v[232:235]\n" \
"v_mfma_f32_16x16x32_bf16 v[236:239], a[208:211], a[244:247], v[236:239]\n" \
"v_mfma_f32_16x16x32_bf16 v[168:171], a[212:215], a[220:223], v[168:171]\n" \
"v_mfma_f32_16x16x32_bf16 v[172:175], a[212:215], a[228:231], v[172:175]\n" \
"buffer_load_dwordx4 a[144:147], v32, s[56:59], s68 offen offset:0 sc1\n" \
"v_mfma_f32_16x16x32_bf16 v[240:243], a[212:215], a[236:239], v[240:243]\n" \
"v_mfma_f32_16x16x32_bf16 v[244:247], a[212:215], a[244:247], v[244:247]\n" \
"v_mfma_f32_16x16x32_bf16 v[176:179], a[216:219], a[220:223], v[176:179]\n" \
"v_mfma_f32_16x16x32_bf16 v[180:183], a[216:219], a[228:231], v[180:183]\n" \
"v_mfma_f32_16x16x32_bf16 v[248:251], a[216:219], a[236:239], v[248:251]\n" \
"buffer_load_dwordx4 a[132:135], v20, s[56:59], s68 offen offset:1024 sc1\n" \
"v_mfma_f32_16x16x32_bf16 v[252:255], a[216:219], a[244:247], v[252:255]\n" \
"v_mfma_f32_16x16x32_bf16 v[112:115], a[0:3], a[224:227], v[112:115]\n" \
"v_mfma_f32_16x16x32_bf16 v[116:119], a[0:3], a[232:235], v[116:119]\n" \
"v_mfma_f32_16x16x32_bf16 v[184:187], a[0:3], a[240:243], v[184:187]\n" \
"v_mfma_f32_16x16x32_bf16 v[188:191], a[0:3], a[248:251], v[188:191]\n" \
"buffer_load_dwordx4 a[148:151], v32, s[56:59], s68 offen offset:1024 sc1\n" \
"v_mfma_f32_16x16x32_bf16 v[120:123], a[4:7], a[224:227], v[120:123]\n" \
"v_mfma_f32_16x16x32_bf16 v[124:127], a[4:7], a[232:235], v[124:127]\n" \
"v_mfma_f32_16x16x32_bf16 v[192:195], a[4:7], a[240:243], v[192:195]\n" \
"v_mfma_f32_16x16x32_bf16 v[196:199], a[4:7], a[248:251], v[196:199]\n" \
"v_mfma_f32_16x16x32_bf16 v[128:131], a[8:11], a[224:227], v[128:131]\n" \
"buffer_load_dwordx4 a[136:139], v20, s[56:59], s68 offen offset:2048 sc1\n" \
"v_mfma_f32_16x16x32_bf16 v[132:135], a[8:11], a[232:235], v[132:135]\n" \
"v_mfma_f32_16x16x32_bf16 v[200:203], a[8:11], a[240:243], v[200:203]\n" \
"v_mfma_f32_16x16x32_bf16 v[204:207], a[8:11], a[248:251], v[204:207]\n" \
"s_waitcnt lgkmcnt(0)\n" \
"v_mfma_f32_16x16x32_bf16 v[136:139], a[12:15], a[224:227], v[136:139]\n" \
"v_mfma_f32_16x16x32_bf16 v[140:143], a[12:15], a[232:235], v[140:143]\n" \
"buffer_load_dwordx4 a[152:155], v32, s[56:59], s68 offen offset:2048 sc1\n" \
"v_mfma_f32_16x16x32_bf16 v[208:211], a[12:15], a[240:243], v[208:211]\n" \
"v_mfma_f32_16x16x32_bf16 v[212:215], a[12:15], a[248:251], v[212:215]\n" \
"s_barrier\n" \
"v_mfma_f32_16x16x32_bf16 v[144:147], a[16:19], a[224:227], v[144:147]\n" \
"ds_read_b64_tr_b16 a[196:197], v33 offset:0\n" \
"ds_read_b64_tr_b16 a[198:199], v33 offset:4608\n" \
"v_mfma_f32_16x16x32_bf16 v[148:151], a[16:19], a[232:235], v[148:151]\n" \
"buffer_load_dwordx4 a[140:143], v20, s[56:59], s68 offen offset:3072 sc1\n" \
"v_mfma_f32_16x16x32_bf16 v[216:219], a[16:19], a[240:243], v[216:219]\n" \
"ds_read_b64_tr_b16 a[200:201], v33 offset:32\n" \
"ds_read_b64_tr_b16 a[202:203], v33 offset:4640\n" \
"v_mfma_f32_16x16x32_bf16 v[220:223], a[16:19], a[248:251], v[220:223]\n" \
"ds_read_b64_tr_b16 a[204:205], v33 offset:64\n" \
"ds_read_b64_tr_b16 a[206:207], v33 offset:4672\n" \
"v_mfma_f32_16x16x32_bf16 v[152:155], a[20:23], a[224:227], v[152:155]\n" \
"ds_read_b64_tr_b16 a[208:209], v33 offset:96\n" \
"ds_read_b64_tr_b16 a[210:211], v33 offset:4704\n" \
"v_mfma_f32_16x16x32_bf16 v[156:159], a[20:23], a[232:235], v[156:159]\n" \
"ds_read_b64_tr_b16 a[212:213], v33 offset:128\n" \
"ds_read_b64_tr_b16 a[214:215], v33 offset:4736\n" \
"v_mfma_f32_16x16x32_bf16 v[224:227], a[20:23], a[240:243], v[224:227]\n" \
"buffer_load_dwordx4 a[156:159], v32, s[56:59], s68 offen offset:3072 sc1\n" \
"v_mfma_f32_16x16x32_bf16 v[228:231], a[20:23], a[248:251], v[228:231]\n" \
"ds_read_b64_tr_b16 a[216:217], v33 offset:160\n" \
"ds_read_b64_tr_b16 a[218:219], v33 offset:4768\n" \
"v_mfma_f32_16x16x32_bf16 v[160:163], a[24:27], a[224:227], v[160:163]\n" \
"ds_read_b64_tr_b16 a[0:1], v33 offset:192\n" \
"ds_read_b64_tr_b16 a[2:3], v33 offset:4800\n" \
"v_mfma_f32_16x16x32_bf16 v[164:167], a[24:27], a[232:235], v[164:167]\n" \
"ds_read_b64_tr_b16 a[4:5], v33 offset:224\n" \
"ds_read_b64_tr_b16 a[6:7], v33 offset:4832\n" \
"v_mfma_f32_16x16x32_bf16 v[232:235], a[24:27], a[240:243], v[232:235]\n" \
"ds_read_b64_tr_b16 a[8:9], v33 offset:256\n" \
"ds_read_b64_tr_b16 a[10:11], v33 offset:4864\n" \
"buffer_load_dwordx4 a[32:35], v20, s[56:59], s69 offen offset:0 sc1\n" \
"v_mfma_f32_16x16x32_bf16 v[236:239], a[24:27], a[248:251], v[236:239]\n" \
"ds_read_b64_tr_b16 a[12:13], v33 offset:9216\n" \
"ds_read_b64_tr_b16 a[14:15], v33 offset:13824\n" \
"v_mfma_f32_16x16x32_bf16 v[168:171], a[28:31], a[224:227], v[168:171]\n" \
"ds_read_b64_tr_b16 a[16:17], v33 offset:9248\n" \
"ds_read_b64_tr_b16 a[18:19], v33 offset:13856\n" \
"v_mfma_f32_16x16x32_bf16 v[172:175], a[28:31], a[232:235], v[172:175]\n" \
"ds_read_b64_tr_b16 a[20:21], v33 offset:9280\n" \
"ds_read_b64_tr_b16 a[22:23], v33 offset:13888\n" \
"v_mfma_f32_16x16x32_bf16 v[240:243], a[28:31], a[240:243], v[240:243]\n" \
"ds_read_b64_tr_b16 a[24:25], v33 offset:9312\n" \
"ds_read_b64_tr_b16 a[26:27], v33 offset:13920\n" \
"buffer_load_dwordx4 a[48:51], v32, s[56:59], s69 offen offset:0 sc1\n" \
"v_mfma_f32_16x16x32_bf16 v[244:247], a[28:31], a[248:251], v[244:247]\n" \
"ds_read_b64_tr_b16 a[28:29], v33 offset:9344\n" \
"ds_read_b64_tr_b16 a[30:31], v33 offset:13952\n" \
"v_mfma_f32_16x16x32_bf16 v[176:179], a[192:195], a[224:227], v[176:179]\n" \
"v_readlane_b32 s68, v11, 49\n" \
"s_waitcnt vmcnt(13)\n" \
"v_cmp_ne_u32_e32 vcc, s62, v47\n" \
"v_mfma_f32_16x16x32_bf16 v[180:183], a[192:195], a[232:235], v[180:183]\n" \
"v_mfma_f32_16x16x32_bf16 v[248:251], a[192:195], a[240:243], v[248:251]\n" \
"buffer_load_dwordx4 a[36:39], v20, s[56:59], s69 offen offset:1024 sc1\n" \
"v_mfma_f32_16x16x32_bf16 v[252:255], a[192:195], a[248:251], v[252:255]\n" \
"ds_read_b64_tr_b16 a[192:193], v33 offset:9376\n" \
"s_waitcnt lgkmcnt(15)\n" \
"v_mfma_f32_16x16x32_bf16 v[112:115], a[196:199], a[64:67], v[112:115]\n" \
"ds_read_b64_tr_b16 a[194:195], v33 offset:13984\n" \
"s_cbranch_vccnz L_sb_%=\n" \
"L_rb_%=:\n" \
"v_mfma_f32_16x16x32_bf16 v[116:119], a[196:199], a[80:83], v[116:119]\n" \
"ds_read_b64_tr_b16 a[196:197], v33 offset:9408\n" \
"ds_read_b64_tr_b16 a[198:199], v33 offset:14016\n" \
"v_mfma_f32_16x16x32_bf16 v[120:123], a[200:203], a[64:67], v[120:123]\n" \
"v_readlane_b32 s71, v11, 52\n" \
"v_cmp_eq_u32_e32 vcc, 0, v44\n" \
"buffer_load_dwordx4 a[52:55], v32, s[56:59], s69 offen offset:1024 sc1\n" \
"v_mfma_f32_16x16x32_bf16 v[124:127], a[200:203], a[80:83], v[124:127]\n" \
"ds_read_b64_tr_b16 a[200:201], v33 offset:9440\n" \
"ds_read_b64_tr_b16 a[202:203], v33 offset:14048\n" \
"v_mfma_f32_16x16x32_bf16 v[128:131], a[204:207], a[64:67], v[128:131]\n" \
"v_cndmask_b32_e32 v50, 0, v45, vcc\n" \
"v_cmp_eq_u32_e32 vcc, 1, v44\n" \
"v_mfma_f32_16x16x32_bf16 v[132:135], a[204:207], a[80:83], v[132:135]\n" \
"ds_read_b64_tr_b16 a[204:205], v33 offset:9472\n" \
"ds_read_b64_tr_b16 a[206:207], v33 offset:14080\n" \
"v_mfma_f32_16x16x32_bf16 v[136:139], a[208:211], a[64:67], v[136:139]\n" \
"v_cndmask_b32_e32 v51, 0, v45, vcc\n" \
"v_bfe_u32 v45, v20, 1, 5\n" \
"buffer_load_dwordx4 a[40:43], v20, s[56:59], s69 offen offset:2048 sc1\n" \
"v_mfma_f32_16x16x32_bf16 v[140:143], a[208:211], a[80:83], v[140:143]\n" \
"ds_read_b64_tr_b16 a[208:209], v33 offset:18432\n" \
"ds_read_b64_tr_b16 a[210:211], v33 offset:23040\n" \
"v_mfma_f32_16x16x32_bf16 v[144:147], a[212:215], a[64:67], v[144:147]\n" \
"v_and_b32_e32 v45, 24, v45\n" \
"v_and_b32_e32 v44, 0x3c0, v20\n" \
"v_mfma_f32_16x16x32_bf16 v[148:151], a[212:215], a[80:83], v[148:151]\n" \
"ds_read_b64_tr_b16 a[212:213], v33 offset:18464\n" \
"ds_read_b64_tr_b16 a[214:215], v33 offset:23072\n" \
"v_mfma_f32_16x16x32_bf16 v[152:155], a[216:219], a[64:67], v[152:155]\n" \
".fill (L_ab + 36864 - .) / 4, 4, 0xBF800000\n" \
"L_ap9:\n" \
"s_cbranch_execz L_ap0\n" \
"v_add_u32_e32 v45, 0x4800, v45\n" \
"v_add_u32_e32 v36, v44, v45\n" \
"buffer_load_dwordx4 a[56:59], v32, s[56:59], s69 offen offset:2048 sc1\n" \
"v_mfma_f32_16x16x32_bf16 v[156:159], a[216:219], a[80:83], v[156:159]\n" \
"ds_read_b64_tr_b16 a[216:217], v33 offset:18496\n" \
"ds_read_b64_tr_b16 a[218:219], v33 offset:23104\n" \
"v_mfma_f32_16x16x32_bf16 v[160:163], a[0:3], a[64:67], v[160:163]\n" \
"v_mfma_f32_16x16x32_bf16 v[164:167], a[0:3], a[80:83], v[164:167]\n" \
"ds_read_b64_tr_b16 a[0:1], v33 offset:18528\n" \
"s_waitcnt lgkmcnt(13)\n" \
"v_mfma_f32_16x16x32_bf16 v[168:171], a[4:7], a[64:67], v[168:171]\n" \
"ds_read_b64_tr_b16 a[2:3], v33 offset:23136\n" \
"buffer_load_dwordx4 a[44:47], v20, s[56:59], s69 offen offset:3072 sc1\n" \
"v_mfma_f32_16x16x32_bf16 v[172:175], a[4:7], a[80:83], v[172:175]\n" \
"ds_read_b64_tr_b16 a[4:5], v33 offset:18560\n" \
"ds_read_b64_tr_b16 a[6:7], v33 offset:23168\n" \
"v_mfma_f32_16x16x32_bf16 v[176:179], a[8:11], a[64:67], v[176:179]\n" \
"v_mfma_f32_16x16x32_bf16 v[180:183], a[8:11], a[80:83], v[180:183]\n" \
"ds_read_b64_tr_b16 a[8:9], v33 offset:18592\n" \
"ds_read_b64_tr_b16 a[10:11], v33 offset:23200\n" \
"v_mfma_f32_16x16x32_bf16 v[112:115], a[12:15], a[68:71], v[112:115]\n" \
"v_mfma_f32_16x16x32_bf16 v[116:119], a[12:15], a[84:87], v[116:119]\n" \
"buffer_load_dwordx4 a[60:63], v32, s[56:59], s69 offen offset:3072 sc1\n" \
"v_mfma_f32_16x16x32_bf16 v[120:123], a[16:19], a[68:71], v[120:123]\n" \
"ds_read_b64_tr_b16 a[12:13], v33 offset:18624\n" \
"ds_read_b64_tr_b16 a[14:15], v33 offset:23232\n" \
"v_mfma_f32_16x16x32_bf16 v[124:127], a[16:19], a[84:87], v[124:127]\n" \
"ds_read_b64_tr_b16 a[16:17], v33 offset:18656\n" \
"ds_read_b64_tr_b16 a[18:19], v33 offset:23264\n" \
"v_mfma_f32_16x16x32_bf16 v[128:131], a[20:23], a[68:71], v[128:131]\n" \
"v_readlane_b32 s69, v11, 50\n" \
"v_mfma_f32_16x16x32_bf16 v[132:135], a[20:23], a[84:87], v[132:135]\n" \
"ds_read_b64_tr_b16 a[20:21], v33 offset:18688\n" \
"ds_read_b64_tr_b16 a[22:23], v33 offset:23296\n" \
"v_mfma_f32_16x16x32_bf16 v[136:139], a[24:27], a[68:71], v[136:139]\n" \
"buffer_load_dwordx4 a[220:223], v20, s[56:59], s64 offen offset:0 sc1\n" \
"v_mfma_f32_16x16x32_bf16 v[140:143], a[24:27], a[84:87], v[140:143]\n" \
"ds_read_b64_tr_b16 a[24:25], v33 offset:27648\n" \
"ds_read_b64_tr_b16 a[26:27], v33 offset:32256\n" \
"v_mfma_f32_16x16x32_bf16 v[144:147], a[28:31], a[68:71], v[144:147]\n" \
"v_mfma_f32_16x16x32_bf16 v[148:151], a[28:31], a[84:87], v[148:151]\n" \
"ds_read_b64_tr_b16 a[28:29], v33 offset:27680\n" \
"ds_read_b64_tr_b16 a[30:31], v33 offset:32288\n" \
"v_mfma_f32_16x16x32_bf16 v[152:155], a[192:195], a[68:71], v[152:155]\n" \
"v_mfma_f32_16x16x32_bf16 v[156:159], a[192:195], a[84:87], v[156:159]\n" \
"ds_read_b64_tr_b16 a[192:193], v33 offset:27712\n" \
"s_waitcnt lgkmcnt(13)\n" \
"v_mfma_f32_16x16x32_bf16 v[160:163], a[196:199], a[68:71], v[160:163]\n" \
"buffer_load_dwordx4 a[236:239], v32, s[56:59], s64 offen offset:0 sc1\n" \
"v_mfma_f32_16x16x32_bf16 v[164:167], a[196:199], a[84:87], v[164:167]\n" \
"ds_read_b64_tr_b16 a[194:195], v33 offset:32320\n" \
"ds_read_b64_tr_b16 a[196:197], v33 offset:27744\n" \
"v_mfma_f32_16x16x32_bf16 v[168:171], a[200:203], a[68:71], v[168:171]\n" \
"ds_read_b64_tr_b16 a[198:199], v33 offset:32352\n" \
"v_mfma_f32_16x16x32_bf16 v[172:175], a[200:203], a[84:87], v[172:175]\n" \
"ds_read_b64_tr_b16 a[200:201], v33 offset:27776\n" \
"ds_read_b64_tr_b16 a[202:203], v33 offset:32384\n" \
"v_mfma_f32_16x16x32_bf16 v[176:179], a[204:207], a[68:71], v[176:179]\n" \
"v_mfma_f32_16x16x32_bf16 v[180:183], a[204:207], a[84:87], v[180:183]\n" \
"ds_read_b64_tr_b16 a[204:205], v33 offset:27808\n" \
"ds_read_b64_tr_b16 a[206:207], v33 offset:32416\n" \
"v_mfma_f32_16x16x32_bf16 v[112:115], a[208:211], a[72:75], v[112:115]\n" \
"buffer_load_dwordx4 a[224:227], v20, s[56:59], s64 offen offset:1024 sc1\n" \
"v_mfma_f32_16x16x32_bf16 v[116:119], a[208:211], a[88:91], v[116:119]\n" \
"ds_read_b64_tr_b16 a[208:209], v33 offset:27840\n" \
"ds_read_b64_tr_b16 a[210:211], v33 offset:32448\n" \
"v_mfma_f32_16x16x32_bf16 v[120:123], a[212:215], a[72:75], v[120:123]\n" \
"v_mfma_f32_16x16x32_bf16 v[124:127], a[212:215], a[88:91], v[124:127]\n" \
"ds_read_b64_tr_b16 a[212:213], v33 offset:27872\n" \
"ds_read_b64_tr_b16 a[214:215], v33 offset:32480\n" \
"v_mfma_f32_16x16x32_bf16 v[128:131], a[216:219], a[72:75], v[128:131]\n" \
"v_mfma_f32_16x16x32_bf16 v[132:135], a[216:219], a[88:91], v[132:135]\n" \
"ds_read_b64_tr_b16 a[216:217], v33 offset:27904\n" \
"ds_read_b64_tr_b16 a[218:219], v33 offset:32512\n" \
"v_mfma_f32_16x16x32_bf16 v[136:139], a[0:3], a[72:75], v[136:139]\n" \
"buffer_load_dwordx4 a[240:243], v32, s[56:59], s64 offen offset:1024 sc1\n" \
"v_mfma_f32_16x16x32_bf16 v[140:143], a[0:3], a[88:91], v[140:143]\n" \
"v_mfma_f32_16x16x32_bf16 v[144:147], a[4:7], a[72:75], v[144:147]\n" \
"v_mfma_f32_16x16x32_bf16 v[148:151], a[4:7], a[88:91], v[148:151]\n" \
"s_waitcnt lgkmcnt(12)\n" \
"v_mfma_f32_16x16x32_bf16 v[152:155], a[8:11], a[72:75], v[152:155]\n" \
"v_mfma_f32_16x16x32_bf16 v[156:159], a[8:11], a[88:91], v[156:159]\n" \
"buffer_load_dwordx4 a[228:231], v20, s[56:59], s64 offen offset:2048 sc1\n" \
"v_mfma_f32_16x16x32_bf16 v[160:163], a[12:15], a[72:75], v[160:163]\n" \
"v_mfma_f32_16x16x32_bf16 v[164:167], a[12:15], a[88:91], v[164:167]\n" \
"v_mfma_f32_16x16x32_bf16 v[168:171], a[16:19], a[72:75], v[168:171]\n" \
"v_mfma_f32_16x16x32_bf16 v[172:175], a[16:19], a[88:91], v[172:175]\n" \
"v_mfma_f32_16x16x32_bf16 v[176:179], a[20:23], a[72:75], v[176:179]\n" \
"buffer_load_dwordx4 a[244:247], v32, s[56:59], s64 offen offset:2048 sc1\n" \
"v_mfma_f32_16x16x32_bf16 v[180:183], a[20:23], a[88:91], v[180:183]\n" \
"s_waitcnt vmcnt(24)\n" \
"s_barrier\n" \
"s_waitcnt vmcnt(23)\n" \
"v_mfma_f32_16x16x32_bf16 v[112:115], a[24:27], a[76:79], v[112:115]\n" \
"ds_read_b64_tr_b16 a[0:1], v33 offset:36864\n" \
"s_waitcnt vmcnt(22)\n" \
"v_mfma_f32_16x16x32_bf16 v[116:119], a[24:27], a[92:95], v[116:119]\n" \
"ds_read_b64_tr_b16 a[2:3], v33 offset:41472\n" \
"ds_read_b64_tr_b16 a[4:5], v33 offset:46080\n" \
"v_mfma_f32_16x16x32_bf16 v[120:123], a[28:31], a[76:79], v[120:123]\n" \
"ds_read_b64_tr_b16 a[6:7], v33 offset:50688\n" \
"ds_read_b64_tr_b16 a[8:9], v33 offset:55296\n" \
"v_mfma_f32_16x16x32_bf16 v[124:127], a[28:31], a[92:95], v[124:127]\n" \
"ds_read_b64_tr_b16 a[10:11], v33 offset:59904\n" \
"ds_read_b64_tr_b16 a[12:13], v10 offset:0\n" \
"v_mfma_f32_16x16x32_bf16 v[128:131], a[192:195], a[76:79], v[128:131]\n" \
"ds_read_b64_tr_b16 a[14:15], v10 offset:4608\n" \
"ds_read_b64_tr_b16 a[16:17], v34 offset:0\n" \
"v_mfma_f32_16x16x32_bf16 v[132:135], a[192:195], a[92:95], v[132:135]\n" \
"ds_read_b64_tr_b16 a[18:19], v34 offset:4608\n" \
"s_waitcnt lgkmcnt(10)\n" \
"v_mfma_f32_16x16x32_bf16 v[136:139], a[196:199], a[76:79], v[136:139]\n" \
"ds_read_b64_tr_b16 a[20:21], v34 offset:9216\n" \
"ds_read_b64_tr_b16 a[22:23], v34 offset:13824\n" \
"v_mfma_f32_16x16x32_bf16 v[140:143], a[196:199], a[92:95], v[140:143]\n" \
"ds_read_b64_tr_b16 a[24:25], v34 offset:18432\n" \
"ds_read_b64_tr_b16 a[26:27], v34 offset:23040\n" \
"v_mfma_f32_16x16x32_bf16 v[144:147], a[200:203], a[76:79], v[144:147]\n" \
"ds_read_b64_tr_b16 a[28:29], v10 offset:9216\n" \
"ds_read_b64_tr_b16 a[30:31], v10 offset:13824\n" \
"v_mfma_f32_16x16x32_bf16 v[148:151], a[200:203], a[92:95], v[148:151]\n" \
"ds_read_b64_tr_b16 a[192:193], v33 offset:36896\n" \
"ds_read_b64_tr_b16 a[194:195], v33 offset:41504\n" \
"v_mfma_f32_16x16x32_bf16 v[152:155], a[204:207], a[76:79], v[152:155]\n" \
"ds_read_b64_tr_b16 a[196:197], v33 offset:46112\n" \
"ds_read_b64_tr_b16 a[198:199], v33 offset:50720\n" \
"v_mfma_f32_16x16x32_bf16 v[156:159], a[204:207], a[92:95], v[156:159]\n" \
"ds_read_b64_tr_b16 a[200:201], v33 offset:55328\n" \
"ds_read_b64_tr_b16 a[202:203], v33 offset:59936\n" \
"v_mfma_f32_16x16x32_bf16 v[160:163], a[208:211], a[76:79], v[160:163]\n" \
"ds_read_b64_tr_b16 a[204:205], v10 offset:32\n" \
"ds_read_b64_tr_b16 a[206:207], v10 offset:4640\n" \
"v_mfma_f32_16x16x32_bf16 v[164:167], a[208:211], a[92:95], v[164:167]\n" \
"ds_read_b64_tr_b16 a[208:209], v34 offset:32\n" \
"ds_read_b64_tr_b16 a[210:211], v34 offset:4640\n" \
"v_mfma_f32_16x16x32_bf16 v[168:171], a[212:215], a[76:79], v[168:171]\n" \
"buffer_load_dwordx4 a[232:235], v20, s[56:59], s64 offen offset:3072 sc1\n" \
"v_mfma_f32_16x16x32_bf16 v[172:175], a[212:215], a[92:95], v[172:175]\n" \
"ds_read_b64_tr_b16 a[212:213], v34 offset:9248\n" \
"ds_read_b64_tr_b16 a[214:215], v34 offset:13856\n" \
"v_mfma_f32_16x16x32_bf16 v[176:179], a[216:219], a[76:79], v[176:179]\n" \
"v_mfma_f32_16x16x32_bf16 v[180:183], a[216:219], a[92:95], v[180:183]\n" \
"ds_read_b64_tr_b16 a[216:217], v34 offset:18464\n" \
"s_waitcnt vmcnt(22) lgkmcnt(15)\n" \
"v_mfma_f32_16x16x32_bf16 v[112:115], a[0:3], a[128:131], v[112:115]\n" \
"ds_read_b64_tr_b16 a[218:219], v34 offset:23072\n" \
"s_waitcnt vmcnt(21)\n" \
"v_mfma_f32_16x16x32_bf16 v[116:119], a[0:3], a[144:147], v[116:119]\n" \
"ds_read_b64_tr_b16 a[0:1], v10 offset:9248\n" \
"s_waitcnt vmcnt(20)\n" \
"v_mfma_f32_16x16x32_bf16 v[112:115], a[4:7], a[132:135], v[112:115]\n" \
"ds_read_b64_tr_b16 a[2:3], v10 offset:13856\n" \
"s_waitcnt vmcnt(19)\n" \
"v_mfma_f32_16x16x32_bf16 v[116:119], a[4:7], a[148:151], v[116:119]\n" \
"ds_read_b64_tr_b16 a[4:5], v33 offset:36928\n" \
"s_waitcnt vmcnt(18)\n" \
"v_mfma_f32_16x16x32_bf16 v[112:115], a[8:11], a[136:139], v[112:115]\n" \
"ds_read_b64_tr_b16 a[6:7], v33 offset:41536\n" \
"s_waitcnt vmcnt(17)\n" \
"v_mfma_f32_16x16x32_bf16 v[116:119], a[8:11], a[152:155], v[116:119]\n" \
"ds_read_b64_tr_b16 a[8:9], v33 offset:46144\n" \
"s_waitcnt vmcnt(16)\n" \
"v_mfma_f32_16x16x32_bf16 v[112:115], a[12:15], a[140:143], v[112:115]\n" \
"ds_read_b64_tr_b16 a[10:11], v33 offset:50752\n" \
"s_waitcnt vmcnt(15)\n" \
"v_mfma_f32_16x16x32_bf16 v[116:119], a[12:15], a[156:159], v[116:119]\n" \
"ds_read_b64_tr_b16 a[12:13], v33 offset:55360\n" \
"ds_read_b64_tr_b16 a[14:15], v33 offset:59968\n" \
"buffer_load_dwordx4 a[248:251], v32, s[56:59], s64 offen offset:3072 sc1\n" \
"s_waitcnt vmcnt(15)\n" \
"v_mfma_f32_16x16x32_bf16 v[112:115], a[16:19], a[32:35], v[112:115]\n" \
"buffer_load_dwordx4 a[160:163], v20, s[56:59], s72 offen offset:0 sc1\n" \
"s_waitcnt vmcnt(15)\n" \
"v_mfma_f32_16x16x32_bf16 v[116:119], a[16:19], a[48:51], v[116:119]\n" \
"ds_read_b64_tr_b16 a[16:17], v10 offset:64\n" \
"ds_read_b64_tr_b16 a[18:19], v10 offset:4672\n" \
"buffer_load_dwordx4 a[176:179], v32, s[56:59], s72 offen offset:0 sc1\n" \
"s_waitcnt vmcnt(15)\n" \
"v_mfma_f32_16x16x32_bf16 v[112:115], a[20:23], a[36:39], v[112:115]\n" \
"buffer_load_dwordx4 a[164:167], v20, s[56:59], s72 offen offset:1024 sc1\n" \
"s_waitcnt vmcnt(15)\n" \
"v_mfma_f32_16x16x32_bf16 v[116:119], a[20:23], a[52:55], v[116:119]\n" \
"ds_read_b64_tr_b16 a[20:21], v34 offset:64\n" \
"ds_read_b64_tr_b16 a[22:23], v34 offset:4672\n" \
"buffer_load_dwordx4 a[180:183], v32, s[56:59], s72 offen offset:1024 sc1\n" \
"s_waitcnt vmcnt(15)\n" \
"v_mfma_f32_16x16x32_bf16 v[112:115], a[24:27], a[40:43], v[112:115]\n" \
"buffer_load_dwordx4 a[168:171], v20, s[56:59], s72 offen offset:2048 sc1\n" \
"s_waitcnt vmcnt(15)\n" \
"v_mfma_f32_16x16x32_bf16 v[116:119], a[24:27], a[56:59], v[116:119]\n" \
"ds_read_b64_tr_b16 a[24:25], v34 offset:9280\n" \
"ds_read_b64_tr_b16 a[26:27], v34 offset:13888\n" \
"buffer_load_dwordx4 a[184:187], v32, s[56:59], s72 offen offset:2048 sc1\n" \
"s_waitcnt vmcnt(15) lgkmcnt(4)\n" \
"v_mfma_f32_16x16x32_bf16 v[112:115], a[28:31], a[44:47], v[112:115]\n" \
"buffer_load_dwordx4 a[172:175], v20, s[56:59], s72 offen offset:3072 sc1\n" \
"s_waitcnt vmcnt(15)\n" \
"v_mfma_f32_16x16x32_bf16 v[116:119], a[28:31], a[60:63], v[116:119]\n" \
"ds_read_b64_tr_b16 a[28:29], v34 offset:18496\n" \
"ds_read_b64_tr_b16 a[30:31], v34 offset:23104\n" \
"v_mfma_f32_16x16x32_bf16 v[120:123], a[192:195], a[128:131], v[120:123]\n" \
"buffer_load_dwordx4 a[188:191], v32, s[56:59], s72 offen offset:3072 sc1\n" \
"v_mfma_f32_16x16x32_bf16 v[124:127], a[192:195], a[144:147], v[124:127]\n" \
"v_cvt_pk_bf16_f32 v112, v112, v113\n" \
"v_cvt_pk_bf16_f32 v113, v114, v115\n" \
"v_mfma_f32_16x16x32_bf16 v[120:123], a[196:199], a[132:135], v[120:123]\n" \
"v_cvt_pk_bf16_f32 v114, v116, v117\n" \
"v_cvt_pk_bf16_f32 v115, v118, v119\n" \
"buffer_store_dwordx4 v[112:115], v20, s[56:59], s65 offen offset:0 sc1\n" \
"s_add_u32 s65, s65, 0x3800\n" \
"v_mfma_f32_16x16x32_bf16 v[124:127], a[196:199], a[148:151], v[124:127]\n" \
"ds_read_b64_tr_b16 a[192:193], v10 offset:9280\n" \
"ds_read_b64_tr_b16 a[194:195], v10 offset:13888\n" \
"v_mfma_f32_16x16x32_bf16 v[120:123], a[200:203], a[136:139], v[120:123]\n" \
"ds_read_b64_tr_b16 a[196:197], v33 offset:36960\n" \
"ds_read_b64_tr_b16 a[198:199], v33 offset:41568\n" \
"v_mfma_f32_16x16x32_bf16 v[124:127], a[200:203], a[152:155], v[124:127]\n" \
"ds_read_b64_tr_b16 a[200:201], v33 offset:46176\n" \
"ds_read_b64_tr_b16 a[202:203], v33 offset:50784\n" \
"v_mfma_f32_16x16x32_bf16 v[120:123], a[204:207], a[140:143], v[120:123]\n" \
"buffer_load_dwordx4 a[96:99], v20, s[56:59], s93 offen offset:0 sc1\n" \
"v_mfma_f32_16x16x32_bf16 v[124:127], a[204:207], a[156:159], v[124:127]\n" \
"ds_read_b64_tr_b16 a[204:205], v33 offset:55392\n" \
"ds_read_b64_tr_b16 a[206:207], v33 offset:60000\n" \
"v_mfma_f32_16x16x32_bf16 v[120:123], a[208:211], a[32:35], v[120:123]\n" \
"v_readlane_b32 s72, v11, 53\n" \
"v_mfma_f32_16x16x32_bf16 v[124:127], a[208:211], a[48:51], v[124:127]\n" \
"ds_read_b64_tr_b16 a[208:209], v10 offset:96\n" \
"ds_read_b64_tr_b16 a[210:211], v10 offset:4704\n" \
"v_mfma_f32_16x16x32_bf16 v[120:123], a[212:215], a[36:39], v[120:123]\n" \
"v_mfma_f32_16x16x32_bf16 v[124:127], a[212:215], a[52:55], v[124:127]\n" \
"ds_read_b64_tr_b16 a[212:213], v34 offset:96\n" \
"ds_read_b64_tr_b16 a[214:215], v34 offset:4704\n" \
"v_mfma_f32_16x16x32_bf16 v[120:123], a[216:219], a[40:43], v[120:123]\n" \
"buffer_load_dwordx4 a[112:115], v32, s[56:59], s93 offen offset:0 sc1\n" \
"v_mfma_f32_16x16x32_bf16 v[124:127], a[216:219], a[56:59], v[124:127]\n" \
"ds_read_b64_tr_b16 a[216:217], v34 offset:9312\n" \
"ds_read_b64_tr_b16 a[218:219], v34 offset:13920\n" \
"v_mfma_f32_16x16x32_bf16 v[120:123], a[0:3], a[44:47], v[120:123]\n" \
"v_mfma_f32_16x16x32_bf16 v[124:127], a[0:3], a[60:63], v[124:127]\n" \
"ds_read_b64_tr_b16 a[0:1], v34 offset:18528\n" \
"ds_read_b64_tr_b16 a[2:3], v34 offset:23136\n" \
"v_mfma_f32_16x16x32_bf16 v[128:131], a[4:7], a[128:131], v[128:131]\n" \
"v_mfma_f32_16x16x32_bf16 v[132:135], a[4:7], a[144:147], v[132:135]\n" \
"buffer_load_dwordx4 a[100:103], v20, s[56:59], s93 offen offset:1024 sc1\n" \
"v_mfma_f32_16x16x32_bf16 v[128:131], a[8:11], a[132:135], v[128:131]\n" \
"ds_read_b64_tr_b16 a[4:5], v10 offset:9312\n" \
"v_cvt_pk_bf16_f32 v120, v120, v121\n" \
"v_cvt_pk_bf16_f32 v121, v122, v123\n" \
"v_cvt_pk_bf16_f32 v122, v124, v125\n" \
"v_cvt_pk_bf16_f32 v123, v126, v127\n" \
"v_mfma_f32_16x16x32_bf16 v[132:135], a[8:11], a[148:151], v[132:135]\n" \
"buffer_store_dwordx4 v[120:123], v20, s[56:59], s66 offen offset:0 sc1\n" \
"s_add_u32 s66, s66, 0x3000\n" \
"v_mfma_f32_16x16x32_bf16 v[128:131], a[12:15], a[136:139], v[128:131]\n" \
"ds_read_b64_tr_b16 a[6:7], v10 offset:13920\n" \
"ds_read_b64_tr_b16 a[8:9], v33 offset:36992\n" \
"v_mfma_f32_16x16x32_bf16 v[132:135], a[12:15], a[152:155], v[132:135]\n" \
"ds_read_b64_tr_b16 a[10:11], v33 offset:41600\n" \
"ds_read_b64_tr_b16 a[12:13], v33 offset:46208\n" \
"v_mfma_f32_16x16x32_bf16 v[128:131], a[16:19], a[140:143], v[128:131]\n" \
"ds_read_b64_tr_b16 a[14:15], v33 offset:50816\n" \
"v_mfma_f32_16x16x32_bf16 v[132:135], a[16:19], a[156:159], v[132:135]\n" \
"ds_read_b64_tr_b16 a[16:17], v33 offset:55424\n" \
"s_waitcnt lgkmcnt(13)\n" \
"v_mfma_f32_16x16x32_bf16 v[128:131], a[20:23], a[32:35], v[128:131]\n" \
"buffer_load_dwordx4 a[116:119], v32, s[56:59], s93 offen offset:1024 sc1\n" \
"v_mfma_f32_16x16x32_bf16 v[132:135], a[20:23], a[48:51], v[132:135]\n" \
"ds_read_b64_tr_b16 a[18:19], v33 offset:60032\n" \
"ds_read_b64_tr_b16 a[20:21], v10 offset:128\n" \
"v_mfma_f32_16x16x32_bf16 v[128:131], a[24:27], a[36:39], v[128:131]\n" \
"ds_read_b64_tr_b16 a[22:23], v10 offset:4736\n" \
"v_mfma_f32_16x16x32_bf16 v[132:135], a[24:27], a[52:55], v[132:135]\n" \
"ds_read_b64_tr_b16 a[24:25], v34 offset:128\n" \
"ds_read_b64_tr_b16 a[26:27], v34 offset:4736\n" \
"v_mfma_f32_16x16x32_bf16 v[128:131], a[28:31], a[40:43], v[128:131]\n" \
"v_mfma_f32_16x16x32_bf16 v[132:135], a[28:31], a[56:59], v[132:135]\n" \
"buffer_load_dwordx4 a[104:107], v20, s[56:59], s93 offen offset:2048 sc1\n" \
"v_mfma_f32_16x16x32_bf16 v[128:131], a[192:195], a[44:47], v[128:131]\n" \
"ds_read_b64_tr_b16 a[28:29], v34 offset:9344\n" \
"ds_read_b64_tr_b16 a[30:31], v34 offset:13952\n" \
"v_mfma_f32_16x16x32_bf16 v[132:135], a[192:195], a[60:63], v[132:135]\n" \
"ds_read_b64_tr_b16 a[192:193], v34 offset:18560\n" \
"ds_read_b64_tr_b16 a[194:195], v34 offset:23168\n" \
"v_mfma_f32_16x16x32_bf16 v[136:139], a[196:199], a[128:131], v[136:139]\n" \
"v_mfma_f32_16x16x32_bf16 v[140:143], a[196:199], a[144:147], v[140:143]\n" \
"ds_read_b64_tr_b16 a[196:197], v10 offset:9344\n" \
"v_cvt_pk_bf16_f32 v128, v128, v129\n" \
"v_cvt_pk_bf16_f32 v129, v130, v131\n" \
"v_mfma_f32_16x16x32_bf16 v[136:139], a[200:203], a[132:135], v[136:139]\n" \
"v_cvt_pk_bf16_f32 v130, v132, v133\n" \
"v_cvt_pk_bf16_f32 v131, v134, v135\n" \
"buffer_store_dwordx4 v[128:131], v20, s[56:59], s67 offen offset:0 sc1\n" \
"s_add_u32 s67, s67, 0x2800\n" \
"v_mfma_f32_16x16x32_bf16 v[140:143], a[200:203], a[148:151], v[140:143]\n" \
"ds_read_b64_tr_b16 a[198:199], v10 offset:13952\n" \
"ds_read_b64_tr_b16 a[200:201], v33 offset:37024\n" \
"v_mfma_f32_16x16x32_bf16 v[136:139], a[204:207], a[136:139], v[136:139]\n" \
"ds_read_b64_tr_b16 a[202:203], v33 offset:41632\n" \
"v_mfma_f32_16x16x32_bf16 v[140:143], a[204:207], a[152:155], v[140:143]\n" \
"ds_read_b64_tr_b16 a[204:205], v33 offset:46240\n" \
"ds_read_b64_tr_b16 a[206:207], v33 offset:50848\n" \
"v_mfma_f32_16x16x32_bf16 v[136:139], a[208:211], a[140:143], v[136:139]\n" \
"buffer_load_dwordx4 a[120:123], v32, s[56:59], s93 offen offset:2048 sc1\n" \
"v_mfma_f32_16x16x32_bf16 v[140:143], a[208:211], a[156:159], v[140:143]\n" \
"ds_read_b64_tr_b16 a[208:209], v33 offset:55456\n" \
"s_waitcnt lgkmcnt(13)\n" \
"v_mfma_f32_16x16x32_bf16 v[136:139], a[212:215], a[32:35], v[136:139]\n" \
"ds_read_b64_tr_b16 a[210:211], v33 offset:60064\n" \
"v_mfma_f32_16x16x32_bf16 v[140:143], a[212:215], a[48:51], v[140:143]\n" \
"ds_read_b64_tr_b16 a[212:213], v10 offset:160\n" \
"ds_read_b64_tr_b16 a[214:215], v10 offset:4768\n" \
"v_mfma_f32_16x16x32_bf16 v[136:139], a[216:219], a[36:39], v[136:139]\n" \
"v_mfma_f32_16x16x32_bf16 v[140:143], a[216:219], a[52:55], v[140:143]\n" \
"buffer_load_dwordx4 a[108:111], v20, s[56:59], s93 offen offset:3072 sc1\n" \
"v_mfma_f32_16x16x32_bf16 v[136:139], a[0:3], a[40:43], v[136:139]\n" \
"ds_read_b64_tr_b16 a[216:217], v34 offset:160\n" \
"ds_read_b64_tr_b16 a[218:219], v34 offset:4768\n" \
"v_mfma_f32_16x16x32_bf16 v[140:143], a[0:3], a[56:59], v[140:143]\n" \
"ds_read_b64_tr_b16 a[0:1], v34 offset:9376\n" \
"ds_read_b64_tr_b16 a[2:3], v34 offset:13984\n" \
"v_mfma_f32_16x16x32_bf16 v[136:139], a[4:7], a[44:47], v[136:139]\n" \
"v_mfma_f32_16x16x32_bf16 v[140:143], a[4:7], a[60:63], v[140:143]\n" \
"ds_read_b64_tr_b16 a[4:5], v34 offset:18592\n" \
"ds_read_b64_tr_b16 a[6:7], v34 offset:23200\n" \
"buffer_load_dwordx4 a[124:127], v32, s[56:59], s93 offen offset:3072 sc1\n" \
"v_mfma_f32_16x16x32_bf16 v[144:147], a[8:11], a[128:131], v[144:147]\n" \
"v_mfma_f32_16x16x32_bf16 v[148:151], a[8:11], a[144:147], v[148:151]\n" \
"ds_read_b64_tr_b16 a[8:9], v10 offset:9376\n" \
"ds_read_b64_tr_b16 a[10:11], v10 offset:13984\n" \
"v_cvt_pk_bf16_f32 v136, v136, v137\n" \
"v_cvt_pk_bf16_f32 v137, v138, v139\n" \
"v_cvt_pk_bf16_f32 v138, v140, v141\n" \
"v_cvt_pk_bf16_f32 v139, v142, v143\n" \
"buffer_store_dwordx4 v[136:139], v20, s[56:59], s68 offen offset:0 sc1\n" \
"s_add_u32 s68, s68, 0x2000\n" \
"v_mfma_f32_16x16x32_bf16 v[144:147], a[12:15], a[132:135], v[144:147]\n" \
"v_mfma_f32_16x16x32_bf16 v[148:151], a[12:15], a[148:151], v[148:151]\n" \
"ds_read_b64_tr_b16 a[12:13], v33 offset:37056\n" \
"ds_read_b64_tr_b16 a[14:15], v33 offset:41664\n" \
"v_mfma_f32_16x16x32_bf16 v[144:147], a[16:19], a[136:139], v[144:147]\n" \
"v_mfma_f32_16x16x32_bf16 v[148:151], a[16:19], a[152:155], v[148:151]\n" \
"ds_read_b64_tr_b16 a[16:17], v33 offset:46272\n" \
"ds_read_b64_tr_b16 a[18:19], v33 offset:50880\n" \
"v_mfma_f32_16x16x32_bf16 v[144:147], a[20:23], a[140:143], v[144:147]\n" \
"buffer_load_dwordx4 v[64:67], v20, s[56:59], s95 offen offset:0 sc1\n" \
"v_mfma_f32_16x16x32_bf16 v[148:151], a[20:23], a[156:159], v[148:151]\n" \
"ds_read_b64_tr_b16 a[20:21], v33 offset:55488\n" \
"s_waitcnt lgkmcnt(13)\n" \
"v_mfma_f32_16x16x32_bf16 v[144:147], a[24:27], a[32:35], v[144:147]\n" \
"ds_read_b64_tr_b16 a[22:23], v33 offset:60096\n" \
"v_mfma_f32_16x16x32_bf16 v[148:151], a[24:27], a[48:51], v[148:151]\n" \
"ds_read_b64_tr_b16 a[24:25], v10 offset:192\n" \
"ds_read_b64_tr_b16 a[26:27], v10 offset:4800\n" \
"v_mfma_f32_16x16x32_bf16 v[144:147], a[28:31], a[36:39], v[144:147]\n" \
"v_mfma_f32_16x16x32_bf16 v[148:151], a[28:31], a[52:55], v[148:151]\n" \
"ds_read_b64_tr_b16 a[28:29], v34 offset:192\n" \
"ds_read_b64_tr_b16 a[30:31], v34 offset:4800\n" \
"v_mfma_f32_16x16x32_bf16 v[144:147], a[192:195], a[40:43], v[144:147]\n" \
"buffer_load_dwordx4 v[68:71], v20, s[56:59], s95 offen offset:1024 sc1\n" \
"v_mfma_f32_16x16x32_bf16 v[148:151], a[192:195], a[56:59], v[148:151]\n" \
"ds_read_b64_tr_b16 a[192:193], v34 offset:9408\n" \
"ds_read_b64_tr_b16 a[194:195], v34 offset:14016\n" \
"v_mfma_f32_16x16x32_bf16 v[144:147], a[196:199], a[44:47], v[144:147]\n" \
"v_mfma_f32_16x16x32_bf16 v[148:151], a[196:199], a[60:63], v[148:151]\n" \
"ds_read_b64_tr_b16 a[196:197], v34 offset:18624\n" \
"ds_read_b64_tr_b16 a[198:199], v34 offset:23232\n" \
"v_mfma_f32_16x16x32_bf16 v[152:155], a[200:203], a[128:131], v[152:155]\n" \
"v_mfma_f32_16x16x32_bf16 v[156:159], a[200:203], a[144:147], v[156:159]\n" \
"ds_read_b64_tr_b16 a[200:201], v10 offset:9408\n" \
"ds_read_b64_tr_b16 a[202:203], v10 offset:14016\n" \
"v_mfma_f32_16x16x32_bf16 v[152:155], a[204:207], a[132:135], v[152:155]\n" \
"v_cvt_pk_bf16_f32 v144, v144, v145\n" \
"v_cvt_pk_bf16_f32 v145, v146, v147\n" \
"v_cvt_pk_bf16_f32 v146, v148, v149\n" \
"v_cvt_pk_bf16_f32 v147, v150, v151\n" \
"buffer_store_dwordx4 v[144:147], v20, s[56:59], s69 offen offset:0 sc1\n" \
"s_add_u32 s69, s69, 0x1800\n" \
"v_mfma_f32_16x16x32_bf16 v[156:159], a[204:207], a[148:151], v[156:159]\n" \
"ds_read_b64_tr_b16 a[204:205], v33 offset:256\n" \
"ds_read_b64_tr_b16 a[206:207], v33 offset:4864\n" \
"v_mfma_f32_16x16x32_bf16 v[152:155], a[208:211], a[136:139], v[152:155]\n" \
"v_mfma_f32_16x16x32_bf16 v[156:159], a[208:211], a[152:155], v[156:159]\n" \
"ds_read_b64_tr_b16 a[208:209], v33 offset:9472\n" \
"ds_read_b64_tr_b16 a[210:211], v33 offset:14080\n" \
"v_mfma_f32_16x16x32_bf16 v[152:155], a[212:215], a[140:143], v[152:155]\n" \
"buffer_load_dwordx4 v[72:75], v32, s[56:59], s95 offen offset:0 sc1\n" \
"v_mfma_f32_16x16x32_bf16 v[156:159], a[212:215], a[156:159], v[156:159]\n" \
"ds_read_b64_tr_b16 a[212:213], v33 offset:18688\n" \
"s_waitcnt lgkmcnt(13)\n" \
"v_mfma_f32_16x16x32_bf16 v[152:155], a[216:219], a[32:35], v[152:155]\n" \
"ds_read_b64_tr_b16 a[214:215], v33 offset:23296\n" \
"v_mfma_f32_16x16x32_bf16 v[156:159], a[216:219], a[48:51], v[156:159]\n" \
"ds_read_b64_tr_b16 a[216:217], v33 offset:27904\n" \
"ds_read_b64_tr_b16 a[218:219], v33 offset:32512\n" \
"v_mfma_f32_16x16x32_bf16 v[152:155], a[0:3], a[36:39], v[152:155]\n" \
"v_mfma_f32_16x16x32_bf16 v[156:159], a[0:3], a[52:55], v[156:159]\n" \
"ds_read_b64_tr_b16 a[0:1], v33 offset:37120\n" \
"ds_read_b64_tr_b16 a[2:3], v33 offset:41728\n" \
"v_mfma_f32_16x16x32_bf16 v[152:155], a[4:7], a[40:43], v[152:155]\n" \
"buffer_load_dwordx4 v[76:79], v32, s[56:59], s95 offen offset:1024 sc1\n" \
"v_mfma_f32_16x16x32_bf16 v[156:159], a[4:7], a[56:59], v[156:159]\n" \
"ds_read_b64_tr_b16 a[4:5], v33 offset:46336\n" \
"ds_read_b64_tr_b16 a[6:7], v33 offset:50944\n" \
"v_mfma_f32_16x16x32_bf16 v[152:155], a[8:11], a[44:47], v[152:155]\n" \
"v_mfma_f32_16x16x32_bf16 v[156:159], a[8:11], a[60:63], v[156:159]\n" \
"ds_read_b64_tr_b16 a[8:9], v33 offset:55552\n" \
"ds_read_b64_tr_b16 a[10:11], v33 offset:60160\n" \
"v_mfma_f32_16x16x32_bf16 v[160:163], a[12:15], a[128:131], v[160:163]\n" \
"v_mfma_f32_16x16x32_bf16 v[164:167], a[12:15], a[144:147], v[164:167]\n" \
"ds_read_b64_tr_b16 a[12:13], v10 offset:256\n" \
"ds_read_b64_tr_b16 a[14:15], v10 offset:4864\n" \
"v_mfma_f32_16x16x32_bf16 v[160:163], a[16:19], a[132:135], v[160:163]\n" \
"v_cvt_pk_bf16_f32 v152, v152, v153\n" \
"v_cvt_pk_bf16_f32 v153, v154, v155\n" \
"v_cvt_pk_bf16_f32 v154, v156, v157\n" \
"v_cvt_pk_bf16_f32 v155, v158, v159\n" \
"buffer_store_dwordx4 v[152:155], v20, s[56:59], s70 offen offset:0 sc1\n" \
"s_add_u32 s70, s70, 0x1000\n" \
"v_mfma_f32_16x16x32_bf16 v[164:167], a[16:19], a[148:151], v[164:167]\n" \
"ds_read_b64_tr_b16 a[16:17], v34 offset:256\n" \
"ds_read_b64_tr_b16 a[18:19], v34 offset:4864\n" \
"v_mfma_f32_16x16x32_bf16 v[160:163], a[20:23], a[136:139], v[160:163]\n" \
"v_mfma_f32_16x16x32_bf16 v[164:167], a[20:23], a[152:155], v[164:167]\n" \
"ds_read_b64_tr_b16 a[20:21], v34 offset:9472\n" \
"ds_read_b64_tr_b16 a[22:23], v34 offset:14080\n" \
"v_mfma_f32_16x16x32_bf16 v[160:163], a[24:27], a[140:143], v[160:163]\n" \
"buffer_load_dwordx4 v[80:83], v20, s[56:59], s96 offen offset:0 sc1\n" \
"v_mfma_f32_16x16x32_bf16 v[164:167], a[24:27], a[156:159], v[164:167]\n" \
"ds_read_b64_tr_b16 a[24:25], v34 offset:18688\n" \
"s_waitcnt lgkmcnt(13)\n" \
"v_mfma_f32_16x16x32_bf16 v[160:163], a[28:31], a[32:35], v[160:163]\n" \
"ds_read_b64_tr_b16 a[26:27], v34 offset:23296\n" \
"v_mfma_f32_16x16x32_bf16 v[164:167], a[28:31], a[48:51], v[164:167]\n" \
"ds_read_b64_tr_b16 a[28:29], v10 offset:9472\n" \
"ds_read_b64_tr_b16 a[30:31], v10 offset:14080\n" \
"s_waitcnt vmcnt(18)\n" \
"s_mov_b64 exec, 1\n" \
"buffer_store_dword v39, off, s[56:59], s65 offset:0 sc1\n" \
"s_mov_b64 exec, -1\n" \
"v_mfma_f32_16x16x32_bf16 v[160:163], a[192:195], a[36:39], v[160:163]\n" \
"v_mfma_f32_16x16x32_bf16 v[164:167], a[192:195], a[52:55], v[164:167]\n" \
"ds_read_b64_tr_b16 a[192:193], v33 offset:0\n" \
"ds_read_b64_tr_b16 a[194:195], v33 offset:4608\n" \
"v_mfma_f32_16x16x32_bf16 v[160:163], a[196:199], a[40:43], v[160:163]\n" \
"v_mfma_f32_16x16x32_bf16 v[164:167], a[196:199], a[56:59], v[164:167]\n" \
"ds_read_b64_tr_b16 a[196:197], v33 offset:9216\n" \
"ds_read_b64_tr_b16 a[198:199], v33 offset:13824\n" \
"v_mfma_f32_16x16x32_bf16 v[160:163], a[200:203], a[44:47], v[160:163]\n" \
"buffer_load_dwordx4 v[84:87], v20, s[56:59], s96 offen offset:1024 sc1\n" \
"v_mfma_f32_16x16x32_bf16 v[164:167], a[200:203], a[60:63], v[164:167]\n" \
"ds_read_b64_tr_b16 a[200:201], v33 offset:18432\n" \
"ds_read_b64_tr_b16 a[202:203], v33 offset:23040\n" \
"v_mfma_f32_16x16x32_bf16 v[248:251], a[204:207], a[220:223], v[248:251]\n" \
"v_mfma_f32_16x16x32_bf16 v[252:255], a[204:207], a[236:239], v[252:255]\n" \
"ds_read_b64_tr_b16 a[204:205], v33 offset:27648\n" \
"ds_read_b64_tr_b16 a[206:207], v33 offset:32256\n" \
"v_cvt_pk_bf16_f32 v160, v160, v161\n" \
"v_cvt_pk_bf16_f32 v161, v162, v163\n" \
"v_cvt_pk_bf16_f32 v162, v164, v165\n" \
"v_cvt_pk_bf16_f32 v163, v166, v167\n" \
"buffer_store_dwordx4 v[160:163], v20, s[56:59], s71 offen offset:0 sc1\n" \
"s_add_u32 s71, s71, 0x800\n" \
"v_mfma_f32_16x16x32_bf16 v[248:251], a[208:211], a[224:227], v[248:251]\n" \
"v_mfma_f32_16x16x32_bf16 v[252:255], a[208:211], a[240:243], v[252:255]\n" \
"ds_read_b64_tr_b16 a[208:209], v33 offset:36864\n" \
"ds_read_b64_tr_b16 a[210:211], v33 offset:41472\n" \
"v_mfma_f32_16x16x32_bf16 v[248:251], a[212:215], a[228:231], v[248:251]\n" \
"v_mfma_f32_16x16x32_bf16 v[252:255], a[212:215], a[244:247], v[252:255]\n" \
"ds_read_b64_tr_b16 a[212:213], v33 offset:46080\n" \
"ds_read_b64_tr_b16 a[214:215], v33 offset:50688\n" \
"v_mfma_f32_16x16x32_bf16 v[248:251], a[216:219], a[232:235], v[248:251]\n" \
"buffer_load_dwordx4 v[88:91], v32, s[56:59], s96 offen offset:0 sc1\n" \
"v_mfma_f32_16x16x32_bf16 v[252:255], a[216:219], a[248:251], v[252:255]\n" \
"ds_read_b64_tr_b16 a[216:217], v33 offset:55296\n" \
"s_waitcnt lgkmcnt(12)\n" \
"v_mfma_f32_16x16x32_bf16 v[176:179], a[0:3], a[128:131], v[176:179]\n" \
"ds_read_b64_tr_b16 a[218:219], v33 offset:59904\n" \
"s_waitcnt vmcnt(18)\n" \
"s_mov_b64 exec, 1\n" \
"buffer_store_dword v39, off, s[56:59], s66 offset:4 sc1\n" \
"s_mov_b64 exec, -1\n" \
"v_readlane_b32 s66, v11, 47\n" \
"v_mfma_f32_16x16x32_bf16 v[180:183], a[0:3], a[144:147], v[180:183]\n" \
"v_mfma_f32_16x16x32_bf16 v[248:251], a[0:3], a[160:163], v[248:251]\n" \
"v_mfma_f32_16x16x32_bf16 v[252:255], a[0:3], a[176:179], v[252:255]\n" \
"ds_read_b64_tr_b16 a[0:1], v10 offset:0\n" \
"ds_read_b64_tr_b16 a[2:3], v10 offset:4608\n" \
"v_mfma_f32_16x16x32_bf16 v[176:179], a[4:7], a[132:135], v[176:179]\n" \
"buffer_load_dwordx4 v[92:95], v32, s[56:59], s96 offen offset:1024 sc1\n" \
"v_mfma_f32_16x16x32_bf16 v[180:183], a[4:7], a[148:151], v[180:183]\n" \
"v_mfma_f32_16x16x32_bf16 v[248:251], a[4:7], a[164:167], v[248:251]\n" \
"v_mfma_f32_16x16x32_bf16 v[252:255], a[4:7], a[180:183], v[252:255]\n" \
"ds_read_b64_tr_b16 a[4:5], v34 offset:0\n" \
"ds_read_b64_tr_b16 a[6:7], v34 offset:4608\n" \
"v_mfma_f32_16x16x32_bf16 v[176:179], a[8:11], a[136:139], v[176:179]\n" \
"v_mfma_f32_16x16x32_bf16 v[180:183], a[8:11], a[152:155], v[180:183]\n" \
"buffer_load_dwordx4 v[96:99], v20, s[56:59], s97 offen offset:0 sc1\n" \
"v_mfma_f32_16x16x32_bf16 v[248:251], a[8:11], a[168:171], v[248:251]\n" \
"v_mfma_f32_16x16x32_bf16 v[252:255], a[8:11], a[184:187], v[252:255]\n" \
"ds_read_b64_tr_b16 a[8:9], v34 offset:9216\n" \
"ds_read_b64_tr_b16 a[10:11], v34 offset:13824\n" \
"v_mfma_f32_16x16x32_bf16 v[176:179], a[12:15], a[140:143], v[176:179]\n" \
"v_mfma_f32_16x16x32_bf16 v[180:183], a[12:15], a[156:159], v[180:183]\n" \
"v_mfma_f32_16x16x32_bf16 v[248:251], a[12:15], a[172:175], v[248:251]\n" \
"buffer_load_dwordx4 v[100:103], v20, s[56:59], s97 offen offset:1024 sc1\n" \
"v_mfma_f32_16x16x32_bf16 v[252:255], a[12:15], a[188:191], v[252:255]\n" \
"ds_read_b64_tr_b16 a[12:13], v34 offset:18432\n" \
"ds_read_b64_tr_b16 a[14:15], v34 offset:23040\n" \
"s_waitcnt vmcnt(19)\n" \
"s_mov_b64 exec, 1\n" \
"buffer_store_dword v39, off, s[56:59], s67 offset:8 sc1\n" \
"s_mov_b64 exec, -1\n" \
"v_readlane_b32 s67, v11, 48\n" \
"v_mfma_f32_16x16x32_bf16 v[176:179], a[16:19], a[32:35], v[176:179]\n" \
"v_mfma_f32_16x16x32_bf16 v[180:183], a[16:19], a[48:51], v[180:183]\n" \
"v_mfma_f32_16x16x32_bf16 v[248:251], a[16:19], a[96:99], v[248:251]\n" \
"v_mfma_f32_16x16x32_bf16 v[252:255], a[16:19], a[112:115], v[252:255]\n" \
"ds_read_b64_tr_b16 a[16:17], v10 offset:9216\n" \
"ds_read_b64_tr_b16 a[18:19], v10 offset:13824\n" \
"v_mfma_f32_16x16x32_bf16 v[176:179], a[20:23], a[36:39], v[176:179]\n" \
"buffer_load_dwordx4 v[104:107], v32, s[56:59], s97 offen offset:0 sc1\n" \
"v_mfma_f32_16x16x32_bf16 v[180:183], a[20:23], a[52:55], v[180:183]\n" \
"v_mfma_f32_16x16x32_bf16 v[248:251], a[20:23], a[100:103], v[248:251]\n" \
"v_mfma_f32_16x16x32_bf16 v[252:255], a[20:23], a[116:119], v[252:255]\n" \
"ds_read_b64_tr_b16 a[20:21], v33 offset:32\n" \
"ds_read_b64_tr_b16 a[22:23], v33 offset:4640\n" \
"v_mfma_f32_16x16x32_bf16 v[176:179], a[24:27], a[40:43], v[176:179]\n" \
"v_mfma_f32_16x16x32_bf16 v[180:183], a[24:27], a[56:59], v[180:183]\n" \
"buffer_load_dwordx4 v[108:111], v32, s[56:59], s97 offen offset:1024 sc1\n" \
"v_mfma_f32_16x16x32_bf16 v[248:251], a[24:27], a[104:107], v[248:251]\n" \
"s_waitcnt vmcnt(21)\n" \
".fill (L_ab + 40960 - .) / 4, 4, 0xBF800000\n" \
"L_ap10:\n" \
"s_cbranch_execz L_ap0\n" \
"v_mfma_f32_16x16x32_bf16 v[252:255], a[24:27], a[120:123], v[252:255]\n" \
"ds_read_b64_tr_b16 a[24:25], v33 offset:9248\n" \
"ds_read_b64_tr_b16 a[26:27], v33 offset:13856\n" \
"v_mfma_f32_16x16x32_bf16 v[176:179], a[28:31], a[44:47], v[176:179]\n" \
"v_mfma_f32_16x16x32_bf16 v[180:183], a[28:31], a[60:63], v[180:183]\n" \
"s_waitcnt vmcnt(20)\n" \
"v_mfma_f32_16x16x32_bf16 v[248:251], a[28:31], a[108:111], v[248:251]\n" \
"s_waitcnt vmcnt(19)\n" \
"v_mfma_f32_16x16x32_bf16 v[252:255], a[28:31], a[124:127], v[252:255]\n" \
"ds_read_b64_tr_b16 a[28:29], v33 offset:18464\n" \
"s_waitcnt vmcnt(18)\n" \
"s_mov_b64 exec, 1\n" \
"buffer_store_dword v39, off, s[56:59], s68 offset:12 sc1\n" \
"s_mov_b64 exec, -1\n" \
"v_cvt_pk_bf16_f32 v176, v176, v177\n" \
"v_cvt_pk_bf16_f32 v248, v248, v249\n" \
"v_cvt_pk_bf16_f32 v177, v178, v179\n" \
"v_cvt_pk_bf16_f32 v249, v250, v251\n" \
"v_cvt_pk_bf16_f32 v178, v180, v181\n" \
"v_cvt_pk_bf16_f32 v179, v182, v183\n" \
"v_cvt_pk_bf16_f32 v250, v252, v253\n" \
"v_cvt_pk_bf16_f32 v251, v254, v255\n" \
"buffer_store_dwordx4 v[176:179], v20, s[56:59], s72 offen offset:0 sc1\n" \
"s_waitcnt lgkmcnt(5)\n" \
"v_mfma_f32_16x16x32_bf16 v[184:187], a[192:195], a[220:223], v[184:187]\n" \
"v_readlane_b32 s68, v11, 49\n" \
"ds_read_b64_tr_b16 a[30:31], v33 offset:23072\n" \
"v_mfma_f32_16x16x32_bf16 v[188:191], a[192:195], a[236:239], v[188:191]\n" \
"ds_read_b64_tr_b16 a[192:193], v33 offset:27680\n" \
"ds_read_b64_tr_b16 a[194:195], v33 offset:32288\n" \
"buffer_store_dwordx4 v[248:251], v20, s[56:59], s72 offen offset:1024 sc1\n" \
"v_mfma_f32_16x16x32_bf16 v[184:187], a[196:199], a[224:227], v[184:187]\n" \
"v_mfma_f32_16x16x32_bf16 v[188:191], a[196:199], a[240:243], v[188:191]\n" \
"ds_read_b64_tr_b16 a[196:197], v33 offset:36896\n" \
"ds_read_b64_tr_b16 a[198:199], v33 offset:41504\n" \
"v_mfma_f32_16x16x32_bf16 v[184:187], a[200:203], a[228:231], v[184:187]\n" \
"v_mfma_f32_16x16x32_bf16 v[188:191], a[200:203], a[244:247], v[188:191]\n" \
"ds_read_b64_tr_b16 a[200:201], v33 offset:46112\n" \
"ds_read_b64_tr_b16 a[202:203], v33 offset:50720\n" \
"v_mfma_f32_16x16x32_bf16 v[184:187], a[204:207], a[232:235], v[184:187]\n" \
"v_mfma_f32_16x16x32_bf16 v[188:191], a[204:207], a[248:251], v[188:191]\n" \
"ds_read_b64_tr_b16 a[204:205], v33 offset:55328\n" \
"ds_read_b64_tr_b16 a[206:207], v33 offset:59936\n" \
"v_mfma_f32_16x16x32_bf16 v[184:187], a[208:211], a[160:163], v[184:187]\n" \
"v_mfma_f32_16x16x32_bf16 v[188:191], a[208:211], a[176:179], v[188:191]\n" \
"ds_read_b64_tr_b16 a[208:209], v10 offset:32\n" \
"ds_read_b64_tr_b16 a[210:211], v10 offset:4640\n" \
"v_mfma_f32_16x16x32_bf16 v[184:187], a[212:215], a[164:167], v[184:187]\n" \
"v_mfma_f32_16x16x32_bf16 v[188:191], a[212:215], a[180:183], v[188:191]\n" \
"ds_read_b64_tr_b16 a[212:213], v34 offset:32\n" \
"ds_read_b64_tr_b16 a[214:215], v34 offset:4640\n" \
"v_mfma_f32_16x16x32_bf16 v[184:187], a[216:219], a[168:171], v[184:187]\n" \
"v_mfma_f32_16x16x32_bf16 v[188:191], a[216:219], a[184:187], v[188:191]\n" \
"ds_read_b64_tr_b16 a[216:217], v34 offset:9248\n" \
"ds_read_b64_tr_b16 a[218:219], v34 offset:13856\n" \
"s_waitcnt vmcnt(18)\n" \
"s_mov_b64 exec, 1\n" \
"buffer_store_dword v39, off, s[56:59], s69 offset:16 sc1\n" \
"s_mov_b64 exec, -1\n" \
"v_mfma_f32_16x16x32_bf16 v[184:187], a[0:3], a[172:175], v[184:187]\n" \
"v_readlane_b32 s69, v11, 50\n" \
"v_mfma_f32_16x16x32_bf16 v[188:191], a[0:3], a[188:191], v[188:191]\n" \
"ds_read_b64_tr_b16 a[0:1], v34 offset:18464\n" \
"ds_read_b64_tr_b16 a[2:3], v34 offset:23072\n" \
"v_mfma_f32_16x16x32_bf16 v[184:187], a[4:7], a[96:99], v[184:187]\n" \
"v_mfma_f32_16x16x32_bf16 v[188:191], a[4:7], a[112:115], v[188:191]\n" \
"ds_read_b64_tr_b16 a[4:5], v10 offset:9248\n" \
"ds_read_b64_tr_b16 a[6:7], v10 offset:13856\n" \
"v_mfma_f32_16x16x32_bf16 v[184:187], a[8:11], a[100:103], v[184:187]\n" \
"v_mfma_f32_16x16x32_bf16 v[188:191], a[8:11], a[116:119], v[188:191]\n" \
"ds_read_b64_tr_b16 a[8:9], v33 offset:64\n" \
"ds_read_b64_tr_b16 a[10:11], v33 offset:4672\n" \
"v_mfma_f32_16x16x32_bf16 v[184:187], a[12:15], a[104:107], v[184:187]\n" \
"v_mfma_f32_16x16x32_bf16 v[188:191], a[12:15], a[120:123], v[188:191]\n" \
"ds_read_b64_tr_b16 a[12:13], v33 offset:9280\n" \
"ds_read_b64_tr_b16 a[14:15], v33 offset:13888\n" \
"v_mfma_f32_16x16x32_bf16 v[184:187], a[16:19], a[108:111], v[184:187]\n" \
"v_mfma_f32_16x16x32_bf16 v[188:191], a[16:19], a[124:127], v[188:191]\n" \
"ds_read_b64_tr_b16 a[16:17], v33 offset:18496\n" \
"buffer_load_dword v37, v40, s[56:59], s74 offen sc1\n" \
"s_waitcnt lgkmcnt(12)\n" \
"v_mfma_f32_16x16x32_bf16 v[192:195], a[20:23], a[220:223], v[192:195]\n" \
"ds_read_b64_tr_b16 a[18:19], v33 offset:23104\n" \
"v_mfma_f32_16x16x32_bf16 v[196:199], a[20:23], a[236:239], v[196:199]\n" \
"ds_read_b64_tr_b16 a[20:21], v33 offset:27712\n" \
"v_cvt_pk_bf16_f32 v184, v184, v185\n" \
"v_cvt_pk_bf16_f32 v185, v186, v187\n" \
"v_cvt_pk_bf16_f32 v186, v188, v189\n" \
"v_cvt_pk_bf16_f32 v187, v190, v191\n" \
"buffer_store_dwordx4 v[184:187], v20, s[56:59], s92 offen offset:1024 sc1\n" \
"s_add_u32 s92, s92, 0x3800\n" \
"v_mfma_f32_16x16x32_bf16 v[192:195], a[24:27], a[224:227], v[192:195]\n" \
"ds_read_b64_tr_b16 a[22:23], v33 offset:32320\n" \
"v_mfma_f32_16x16x32_bf16 v[196:199], a[24:27], a[240:243], v[196:199]\n" \
"ds_read_b64_tr_b16 a[24:25], v33 offset:36928\n" \
"ds_read_b64_tr_b16 a[26:27], v33 offset:41536\n" \
"s_waitcnt vmcnt(18)\n" \
"s_mov_b64 exec, 1\n" \
"buffer_store_dword v39, off, s[56:59], s70 offset:20 sc1\n" \
"s_mov_b64 exec, -1\n" \
"v_mfma_f32_16x16x32_bf16 v[192:195], a[28:31], a[228:231], v[192:195]\n" \
"v_mfma_f32_16x16x32_bf16 v[196:199], a[28:31], a[244:247], v[196:199]\n" \
"ds_read_b64_tr_b16 a[28:29], v33 offset:46144\n" \
"ds_read_b64_tr_b16 a[30:31], v33 offset:50752\n" \
"v_mfma_f32_16x16x32_bf16 v[192:195], a[192:195], a[232:235], v[192:195]\n" \
"v_mfma_f32_16x16x32_bf16 v[196:199], a[192:195], a[248:251], v[196:199]\n" \
"ds_read_b64_tr_b16 a[192:193], v33 offset:55360\n" \
"ds_read_b64_tr_b16 a[194:195], v33 offset:59968\n" \
"v_mfma_f32_16x16x32_bf16 v[192:195], a[196:199], a[160:163], v[192:195]\n" \
"v_mfma_f32_16x16x32_bf16 v[196:199], a[196:199], a[176:179], v[196:199]\n" \
"ds_read_b64_tr_b16 a[196:197], v10 offset:64\n" \
"ds_read_b64_tr_b16 a[198:199], v10 offset:4672\n" \
"v_mfma_f32_16x16x32_bf16 v[192:195], a[200:203], a[164:167], v[192:195]\n" \
"v_mfma_f32_16x16x32_bf16 v[196:199], a[200:203], a[180:183], v[196:199]\n" \
"ds_read_b64_tr_b16 a[200:201], v34 offset:64\n" \
"ds_read_b64_tr_b16 a[202:203], v34 offset:4672\n" \
"v_mfma_f32_16x16x32_bf16 v[192:195], a[204:207], a[168:171], v[192:195]\n" \
"v_mfma_f32_16x16x32_bf16 v[196:199], a[204:207], a[184:187], v[196:199]\n" \
"ds_read_b64_tr_b16 a[204:205], v34 offset:9280\n" \
"ds_read_b64_tr_b16 a[206:207], v34 offset:13888\n" \
"v_mfma_f32_16x16x32_bf16 v[192:195], a[208:211], a[172:175], v[192:195]\n" \
"v_mfma_f32_16x16x32_bf16 v[196:199], a[208:211], a[188:191], v[196:199]\n" \
"ds_read_b64_tr_b16 a[208:209], v34 offset:18496\n" \
"s_waitcnt lgkmcnt(13)\n" \
"v_mfma_f32_16x16x32_bf16 v[192:195], a[212:215], a[96:99], v[192:195]\n" \
"ds_read_b64_tr_b16 a[210:211], v34 offset:23104\n" \
"v_mfma_f32_16x16x32_bf16 v[196:199], a[212:215], a[112:115], v[196:199]\n" \
"ds_read_b64_tr_b16 a[212:213], v10 offset:9280\n" \
"ds_read_b64_tr_b16 a[214:215], v10 offset:13888\n" \
"v_mfma_f32_16x16x32_bf16 v[192:195], a[216:219], a[100:103], v[192:195]\n" \
"v_mfma_f32_16x16x32_bf16 v[196:199], a[216:219], a[116:119], v[196:199]\n" \
"ds_read_b64_tr_b16 a[216:217], v33 offset:96\n" \
"ds_read_b64_tr_b16 a[218:219], v33 offset:4704\n" \
"s_waitcnt vmcnt(15)\n" \
"s_mov_b64 exec, 1\n" \
"buffer_store_dword v39, off, s[56:59], s71 offset:24 sc1\n" \
"s_mov_b64 exec, -1\n" \
"v_mfma_f32_16x16x32_bf16 v[192:195], a[0:3], a[104:107], v[192:195]\n" \
"v_mfma_f32_16x16x32_bf16 v[196:199], a[0:3], a[120:123], v[196:199]\n" \
"ds_read_b64_tr_b16 a[0:1], v33 offset:9312\n" \
"ds_read_b64_tr_b16 a[2:3], v33 offset:13920\n" \
"v_mfma_f32_16x16x32_bf16 v[192:195], a[4:7], a[108:111], v[192:195]\n" \
"v_mfma_f32_16x16x32_bf16 v[196:199], a[4:7], a[124:127], v[196:199]\n" \
"ds_read_b64_tr_b16 a[4:5], v33 offset:18528\n" \
"ds_read_b64_tr_b16 a[6:7], v33 offset:23136\n" \
"v_mfma_f32_16x16x32_bf16 v[200:203], a[8:11], a[220:223], v[200:203]\n" \
"v_mfma_f32_16x16x32_bf16 v[204:207], a[8:11], a[236:239], v[204:207]\n" \
"ds_read_b64_tr_b16 a[8:9], v33 offset:27744\n" \
"ds_read_b64_tr_b16 a[10:11], v33 offset:32352\n" \
"v_mfma_f32_16x16x32_bf16 v[200:203], a[12:15], a[224:227], v[200:203]\n" \
"v_cvt_pk_bf16_f32 v192, v192, v193\n" \
"v_cvt_pk_bf16_f32 v193, v194, v195\n" \
"v_cvt_pk_bf16_f32 v194, v196, v197\n" \
"v_cvt_pk_bf16_f32 v195, v198, v199\n" \
"buffer_store_dwordx4 v[192:195], v20, s[56:59], s66 offen offset:1024 sc1\n" \
"v_mfma_f32_16x16x32_bf16 v[204:207], a[12:15], a[240:243], v[204:207]\n" \
"ds_read_b64_tr_b16 a[12:13], v33 offset:36960\n" \
"ds_read_b64_tr_b16 a[14:15], v33 offset:41568\n" \
"v_mfma_f32_16x16x32_bf16 v[200:203], a[16:19], a[228:231], v[200:203]\n" \
"v_mfma_f32_16x16x32_bf16 v[204:207], a[16:19], a[244:247], v[204:207]\n" \
"ds_read_b64_tr_b16 a[16:17], v33 offset:46176\n" \
"ds_read_b64_tr_b16 a[18:19], v33 offset:50784\n" \
"v_mfma_f32_16x16x32_bf16 v[200:203], a[20:23], a[232:235], v[200:203]\n" \
"v_mfma_f32_16x16x32_bf16 v[204:207], a[20:23], a[248:251], v[204:207]\n" \
"ds_read_b64_tr_b16 a[20:21], v33 offset:55392\n" \
"s_waitcnt lgkmcnt(11)\n" \
"v_mfma_f32_16x16x32_bf16 v[200:203], a[24:27], a[160:163], v[200:203]\n" \
"ds_read_b64_tr_b16 a[22:23], v33 offset:60000\n" \
"v_mfma_f32_16x16x32_bf16 v[204:207], a[24:27], a[176:179], v[204:207]\n" \
"ds_read_b64_tr_b16 a[24:25], v10 offset:96\n" \
"ds_read_b64_tr_b16 a[26:27], v10 offset:4704\n" \
"v_mfma_f32_16x16x32_bf16 v[200:203], a[28:31], a[164:167], v[200:203]\n" \
"v_mfma_f32_16x16x32_bf16 v[204:207], a[28:31], a[180:183], v[204:207]\n" \
"ds_read_b64_tr_b16 a[28:29], v34 offset:96\n" \
"ds_read_b64_tr_b16 a[30:31], v34 offset:4704\n" \
"v_mfma_f32_16x16x32_bf16 v[200:203], a[192:195], a[168:171], v[200:203]\n" \
"v_mfma_f32_16x16x32_bf16 v[204:207], a[192:195], a[184:187], v[204:207]\n" \
"ds_read_b64_tr_b16 a[192:193], v34 offset:9312\n" \
"ds_read_b64_tr_b16 a[194:195], v34 offset:13920\n" \
"v_mfma_f32_16x16x32_bf16 v[200:203], a[196:199], a[172:175], v[200:203]\n" \
"v_mfma_f32_16x16x32_bf16 v[204:207], a[196:199], a[188:191], v[204:207]\n" \
"ds_read_b64_tr_b16 a[196:197], v34 offset:18528\n" \
"ds_read_b64_tr_b16 a[198:199], v34 offset:23136\n" \
"v_mfma_f32_16x16x32_bf16 v[200:203], a[200:203], a[96:99], v[200:203]\n" \
"v_mfma_f32_16x16x32_bf16 v[204:207], a[200:203], a[112:115], v[204:207]\n" \
"ds_read_b64_tr_b16 a[200:201], v10 offset:9312\n" \
"ds_read_b64_tr_b16 a[202:203], v10 offset:13920\n" \
"v_mfma_f32_16x16x32_bf16 v[200:203], a[204:207], a[100:103], v[200:203]\n" \
"v_mfma_f32_16x16x32_bf16 v[204:207], a[204:207], a[116:119], v[204:207]\n" \
"ds_read_b64_tr_b16 a[204:205], v33 offset:128\n" \
"ds_read_b64_tr_b16 a[206:207], v33 offset:4736\n" \
"v_mfma_f32_16x16x32_bf16 v[200:203], a[208:211], a[104:107], v[200:203]\n" \
"v_mfma_f32_16x16x32_bf16 v[204:207], a[208:211], a[120:123], v[204:207]\n" \
"ds_read_b64_tr_b16 a[208:209], v33 offset:9344\n" \
"ds_read_b64_tr_b16 a[210:211], v33 offset:13952\n" \
"v_mfma_f32_16x16x32_bf16 v[200:203], a[212:215], a[108:111], v[200:203]\n" \
"v_mfma_f32_16x16x32_bf16 v[204:207], a[212:215], a[124:127], v[204:207]\n" \
"ds_read_b64_tr_b16 a[212:213], v33 offset:18560\n" \
"ds_read_b64_tr_b16 a[214:215], v33 offset:23168\n" \
"v_mfma_f32_16x16x32_bf16 v[208:211], a[216:219], a[220:223], v[208:211]\n" \
"v_mfma_f32_16x16x32_bf16 v[212:215], a[216:219], a[236:239], v[212:215]\n" \
"ds_read_b64_tr_b16 a[216:217], v33 offset:27776\n" \
"s_waitcnt lgkmcnt(13)\n" \
"v_mfma_f32_16x16x32_bf16 v[208:211], a[0:3], a[224:227], v[208:211]\n" \
"v_cvt_pk_bf16_f32 v200, v200, v201\n" \
"v_cvt_pk_bf16_f32 v201, v202, v203\n" \
"v_cvt_pk_bf16_f32 v202, v204, v205\n" \
"v_cvt_pk_bf16_f32 v203, v206, v207\n" \
"buffer_store_dwordx4 v[200:203], v20, s[56:59], s67 offen offset:1024 sc1\n" \
"v_mfma_f32_16x16x32_bf16 v[212:215], a[0:3], a[240:243], v[212:215]\n" \
"ds_read_b64_tr_b16 a[218:219], v33 offset:32384\n" \
"ds_read_b64_tr_b16 a[0:1], v33 offset:36992\n" \
"v_mfma_f32_16x16x32_bf16 v[208:211], a[4:7], a[228:231], v[208:211]\n" \
"ds_read_b64_tr_b16 a[2:3], v33 offset:41600\n" \
"v_mfma_f32_16x16x32_bf16 v[212:215], a[4:7], a[244:247], v[212:215]\n" \
"ds_read_b64_tr_b16 a[4:5], v33 offset:46208\n" \
"ds_read_b64_tr_b16 a[6:7], v33 offset:50816\n" \
"v_mfma_f32_16x16x32_bf16 v[208:211], a[8:11], a[232:235], v[208:211]\n" \
"v_mfma_f32_16x16x32_bf16 v[212:215], a[8:11], a[248:251], v[212:215]\n" \
"ds_read_b64_tr_b16 a[8:9], v33 offset:55424\n" \
"ds_read_b64_tr_b16 a[10:11], v33 offset:60032\n" \
"v_mfma_f32_16x16x32_bf16 v[208:211], a[12:15], a[160:163], v[208:211]\n" \
"v_mfma_f32_16x16x32_bf16 v[212:215], a[12:15], a[176:179], v[212:215]\n" \
"ds_read_b64_tr_b16 a[12:13], v10 offset:128\n" \
"ds_read_b64_tr_b16 a[14:15], v10 offset:4736\n" \
"v_mfma_f32_16x16x32_bf16 v[208:211], a[16:19], a[164:167], v[208:211]\n" \
"s_waitcnt vmcnt(5)\n" \
"v_cmp_eq_u32_e64 s[98:99], s62, v37\n" \
"s_mov_b64 exec, 3\n" \
"buffer_store_dword v39, v41, s[56:59], s73 offen sc1\n" \
"s_mov_b64 exec, -1\n" \
"s_andn2_b32 s75, 0x7f, s98\n" \
"s_mov_b32 s20, s75\n" \
"s_bitcmp1_b32 s20, 0\n" \
"s_cselect_b32 s93, 0x40000000, s12\n" \
"s_bitcmp1_b32 s20, 1\n" \
"s_cselect_b32 s94, 0x40000000, s12\n" \
"s_bitcmp1_b32 s20, 2\n" \
"s_cselect_b32 s95, 0x40000000, s13\n" \
"s_bitcmp1_b32 s20, 3\n" \
"s_cselect_b32 s96, 0x40000000, s13\n" \
"s_bitcmp1_b32 s20, 4\n" \
"s_cselect_b32 s97, 0x40000000, s14\n" \
"s_bitcmp1_b32 s20, 5\n" \
"s_cselect_b32 s64, 0x40000000, s14\n" \
"s_bitcmp1_b32 s20, 6\n" \
"s_cselect_b32 s65, 0x40000000, s15\n" \
"s_add_u32 s66, s66, 0x3000\n" \
"s_add_u32 s67, s67, 0x2800\n" \
"v_mfma_f32_16x16x32_bf16 v[212:215], a[16:19], a[180:183], v[212:215]\n" \
"buffer_load_dwordx4 v[116:119], v20, s[56:59], s93 offen offset:0 sc1\n" \
"v_mfma_f32_16x16x32_bf16 v[208:211], a[20:23], a[168:171], v[208:211]\n" \
"ds_read_b64_tr_b16 a[16:17], v34 offset:128\n" \
"ds_read_b64_tr_b16 a[18:19], v34 offset:4736\n" \
"v_mfma_f32_16x16x32_bf16 v[212:215], a[20:23], a[184:187], v[212:215]\n" \
"ds_read_b64_tr_b16 a[20:21], v34 offset:9344\n" \
"ds_read_b64_tr_b16 a[22:23], v34 offset:13952\n" \
"v_mfma_f32_16x16x32_bf16 v[208:211], a[24:27], a[172:175], v[208:211]\n" \
"v_mfma_f32_16x16x32_bf16 v[212:215], a[24:27], a[188:191], v[212:215]\n" \
"ds_read_b64_tr_b16 a[24:25], v34 offset:18560\n" \
"ds_read_b64_tr_b16 a[26:27], v34 offset:23168\n" \
"v_mfma_f32_16x16x32_bf16 v[208:211], a[28:31], a[96:99], v[208:211]\n" \
"v_mfma_f32_16x16x32_bf16 v[212:215], a[28:31], a[112:115], v[212:215]\n" \
"ds_read_b64_tr_b16 a[28:29], v10 offset:9344\n" \
"s_waitcnt lgkmcnt(9)\n" \
"v_mfma_f32_16x16x32_bf16 v[208:211], a[192:195], a[100:103], v[208:211]\n" \
"ds_read_b64_tr_b16 a[30:31], v10 offset:13952\n" \
"v_mfma_f32_16x16x32_bf16 v[212:215], a[192:195], a[116:119], v[212:215]\n" \
"ds_read_b64_tr_b16 a[192:193], v33 offset:160\n" \
"ds_read_b64_tr_b16 a[194:195], v33 offset:4768\n" \
"v_mfma_f32_16x16x32_bf16 v[208:211], a[196:199], a[104:107], v[208:211]\n" \
"v_mfma_f32_16x16x32_bf16 v[212:215], a[196:199], a[120:123], v[212:215]\n" \
"ds_read_b64_tr_b16 a[196:197], v33 offset:9376\n" \
"ds_read_b64_tr_b16 a[198:199], v33 offset:13984\n" \
"v_mfma_f32_16x16x32_bf16 v[208:211], a[200:203], a[108:111], v[208:211]\n" \
"v_mfma_f32_16x16x32_bf16 v[212:215], a[200:203], a[124:127], v[212:215]\n" \
"ds_read_b64_tr_b16 a[200:201], v33 offset:18592\n" \
"ds_read_b64_tr_b16 a[202:203], v33 offset:23200\n" \
"v_mfma_f32_16x16x32_bf16 v[216:219], a[204:207], a[220:223], v[216:219]\n" \
"v_mfma_f32_16x16x32_bf16 v[220:223], a[204:207], a[236:239], v[220:223]\n" \
"buffer_load_dwordx4 v[124:127], v20, s[56:59], s94 offen offset:2048 sc1\n" \
"v_mfma_f32_16x16x32_bf16 v[216:219], a[208:211], a[224:227], v[216:219]\n" \
"ds_read_b64_tr_b16 a[204:205], v33 offset:27808\n" \
"v_cvt_pk_bf16_f32 v208, v208, v209\n" \
"v_cvt_pk_bf16_f32 v209, v210, v211\n" \
"v_cvt_pk_bf16_f32 v210, v212, v213\n" \
"v_cvt_pk_bf16_f32 v211, v214, v215\n" \
"v_mfma_f32_16x16x32_bf16 v[220:223], a[208:211], a[240:243], v[220:223]\n" \
"buffer_store_dwordx4 v[208:211], v20, s[56:59], s68 offen offset:1024 sc1\n" \
"s_add_u32 s68, s68, 0x2000\n" \
"v_mfma_f32_16x16x32_bf16 v[216:219], a[212:215], a[228:231], v[216:219]\n" \
"ds_read_b64_tr_b16 a[206:207], v33 offset:32416\n" \
"ds_read_b64_tr_b16 a[208:209], v33 offset:37024\n" \
"v_mfma_f32_16x16x32_bf16 v[220:223], a[212:215], a[244:247], v[220:223]\n" \
"ds_read_b64_tr_b16 a[210:211], v33 offset:41632\n" \
"ds_read_b64_tr_b16 a[212:213], v33 offset:46240\n" \
"v_mfma_f32_16x16x32_bf16 v[216:219], a[216:219], a[232:235], v[216:219]\n" \
"ds_read_b64_tr_b16 a[214:215], v33 offset:50848\n" \
"v_mfma_f32_16x16x32_bf16 v[220:223], a[216:219], a[248:251], v[220:223]\n" \
"ds_read_b64_tr_b16 a[216:217], v33 offset:55456\n" \
"ds_read_b64_tr_b16 a[218:219], v33 offset:60064\n" \
"v_mfma_f32_16x16x32_bf16 v[216:219], a[0:3], a[160:163], v[216:219]\n" \
"v_mfma_f32_16x16x32_bf16 v[220:223], a[0:3], a[176:179], v[220:223]\n" \
"ds_read_b64_tr_b16 a[0:1], v10 offset:160\n" \
"ds_read_b64_tr_b16 a[2:3], v10 offset:4768\n" \
"v_mfma_f32_16x16x32_bf16 v[216:219], a[4:7], a[164:167], v[216:219]\n" \
"v_mfma_f32_16x16x32_bf16 v[220:223], a[4:7], a[180:183], v[220:223]\n" \
"ds_read_b64_tr_b16 a[4:5], v34 offset:160\n" \
"ds_read_b64_tr_b16 a[6:7], v34 offset:4768\n" \
"v_mfma_f32_16x16x32_bf16 v[216:219], a[8:11], a[168:171], v[216:219]\n" \
"s_waitcnt vmcnt(8)\n" \
"s_mov_b64 exec, 1\n" \
"buffer_store_dword v39, off, s[56:59], s92 offset:32 sc1\n" \
"s_mov_b64 exec, -1\n" \
"v_mfma_f32_16x16x32_bf16 v[220:223], a[8:11], a[184:187], v[220:223]\n" \
"ds_read_b64_tr_b16 a[8:9], v34 offset:9376\n" \
"s_waitcnt lgkmcnt(13)\n" \
"v_mfma_f32_16x16x32_bf16 v[216:219], a[12:15], a[172:175], v[216:219]\n" \
"ds_read_b64_tr_b16 a[10:11], v34 offset:13984\n" \
"v_mfma_f32_16x16x32_bf16 v[220:223], a[12:15], a[188:191], v[220:223]\n" \
"ds_read_b64_tr_b16 a[12:13], v34 offset:18592\n" \
"ds_read_b64_tr_b16 a[14:15], v34 offset:23200\n" \
"v_mfma_f32_16x16x32_bf16 v[216:219], a[16:19], a[96:99], v[216:219]\n" \
"buffer_load_dwordx4 v[132:135], v20, s[56:59], s95 offen offset:0 sc1\n" \
"v_mfma_f32_16x16x32_bf16 v[220:223], a[16:19], a[112:115], v[220:223]\n" \
"ds_read_b64_tr_b16 a[16:17], v10 offset:9376\n" \
"ds_read_b64_tr_b16 a[18:19], v10 offset:13984\n" \
"v_mfma_f32_16x16x32_bf16 v[216:219], a[20:23], a[100:103], v[216:219]\n" \
"v_mfma_f32_16x16x32_bf16 v[220:223], a[20:23], a[116:119], v[220:223]\n" \
"ds_read_b64_tr_b16 a[20:21], v33 offset:192\n" \
"ds_read_b64_tr_b16 a[22:23], v33 offset:4800\n" \
"v_mfma_f32_16x16x32_bf16 v[216:219], a[24:27], a[104:107], v[216:219]\n" \
"v_mfma_f32_16x16x32_bf16 v[220:223], a[24:27], a[120:123], v[220:223]\n" \
"ds_read_b64_tr_b16 a[24:25], v33 offset:9408\n" \
"ds_read_b64_tr_b16 a[26:27], v33 offset:14016\n" \
"v_mfma_f32_16x16x32_bf16 v[216:219], a[28:31], a[108:111], v[216:219]\n" \
"v_mfma_f32_16x16x32_bf16 v[220:223], a[28:31], a[124:127], v[220:223]\n" \
"buffer_load_dword v37, v40, s[56:59], s74 offen sc1\n" \
"v_mfma_f32_16x16x32_bf16 v[224:227], a[192:195], a[220:223], v[224:227]\n" \
"ds_read_b64_tr_b16 a[28:29], v33 offset:18624\n" \
"ds_read_b64_tr_b16 a[30:31], v33 offset:23232\n" \
"v_mfma_f32_16x16x32_bf16 v[228:231], a[192:195], a[236:239], v[228:231]\n" \
"ds_read_b64_tr_b16 a[192:193], v33 offset:27840\n" \
"ds_read_b64_tr_b16 a[194:195], v33 offset:32448\n" \
"v_cvt_pk_bf16_f32 v216, v216, v217\n" \
"v_cvt_pk_bf16_f32 v217, v218, v219\n" \
"v_cvt_pk_bf16_f32 v218, v220, v221\n" \
"v_cvt_pk_bf16_f32 v219, v222, v223\n" \
"buffer_store_dwordx4 v[216:219], v20, s[56:59], s69 offen offset:1024 sc1\n" \
"s_add_u32 s69, s69, 0x1800\n" \
"v_mfma_f32_16x16x32_bf16 v[224:227], a[196:199], a[224:227], v[224:227]\n" \
"v_mfma_f32_16x16x32_bf16 v[228:231], a[196:199], a[240:243], v[228:231]\n" \
"ds_read_b64_tr_b16 a[196:197], v33 offset:37056\n" \
"ds_read_b64_tr_b16 a[198:199], v33 offset:41664\n" \
"v_mfma_f32_16x16x32_bf16 v[224:227], a[200:203], a[228:231], v[224:227]\n" \
"v_mfma_f32_16x16x32_bf16 v[228:231], a[200:203], a[244:247], v[228:231]\n" \
"ds_read_b64_tr_b16 a[200:201], v33 offset:46272\n" \
"s_waitcnt lgkmcnt(12)\n" \
"v_mfma_f32_16x16x32_bf16 v[224:227], a[204:207], a[232:235], v[224:227]\n" \
"buffer_load_dwordx4 v[140:143], v20, s[56:59], s96 offen offset:2048 sc1\n" \
"v_mfma_f32_16x16x32_bf16 v[228:231], a[204:207], a[248:251], v[228:231]\n" \
"ds_read_b64_tr_b16 a[202:203], v33 offset:50880\n" \
"ds_read_b64_tr_b16 a[204:205], v33 offset:55488\n" \
"v_mfma_f32_16x16x32_bf16 v[224:227], a[208:211], a[160:163], v[224:227]\n" \
"ds_read_b64_tr_b16 a[206:207], v33 offset:60096\n" \
"v_mfma_f32_16x16x32_bf16 v[228:231], a[208:211], a[176:179], v[228:231]\n" \
"ds_read_b64_tr_b16 a[208:209], v10 offset:192\n" \
"ds_read_b64_tr_b16 a[210:211], v10 offset:4800\n" \
"v_mfma_f32_16x16x32_bf16 v[224:227], a[212:215], a[164:167], v[224:227]\n" \
"v_mfma_f32_16x16x32_bf16 v[228:231], a[212:215], a[180:183], v[228:231]\n" \
"ds_read_b64_tr_b16 a[212:213], v34 offset:192\n" \
"ds_read_b64_tr_b16 a[214:215], v34 offset:4800\n" \
"v_mfma_f32_16x16x32_bf16 v[224:227], a[216:219], a[168:171], v[224:227]\n" \
"v_mfma_f32_16x16x32_bf16 v[228:231], a[216:219], a[184:187], v[228:231]\n" \
"ds_read_b64_tr_b16 a[216:217], v34 offset:9408\n" \
"ds_read_b64_tr_b16 a[218:219], v34 offset:14016\n" \
"v_mfma_f32_16x16x32_bf16 v[224:227], a[0:3], a[172:175], v[224:227]\n" \
"v_mfma_f32_16x16x32_bf16 v[228:231], a[0:3], a[188:191], v[228:231]\n" \
"ds_read_b64_tr_b16 a[0:1], v34 offset:18624\n" \
"ds_read_b64_tr_b16 a[2:3], v34 offset:23232\n" \
"s_waitcnt vmcnt(10)\n" \
"s_mov_b64 exec, 1\n" \
"buffer_store_dword v39, off, s[56:59], s66 offset:36 sc1\n" \
"s_mov_b64 exec, -1\n" \
"v_mfma_f32_16x16x32_bf16 v[224:227], a[4:7], a[96:99], v[224:227]\n" \
"v_mfma_f32_16x16x32_bf16 v[228:231], a[4:7], a[112:115], v[228:231]\n" \
"ds_read_b64_tr_b16 a[4:5], v10 offset:9408\n" \
"ds_read_b64_tr_b16 a[6:7], v10 offset:14016\n" \
"v_mfma_f32_16x16x32_bf16 v[224:227], a[8:11], a[100:103], v[224:227]\n" \
"v_mfma_f32_16x16x32_bf16 v[228:231], a[8:11], a[116:119], v[228:231]\n" \
"buffer_load_dwordx4 v[148:151], v20, s[56:59], s97 offen offset:0 sc1\n" \
"v_mfma_f32_16x16x32_bf16 v[224:227], a[12:15], a[104:107], v[224:227]\n" \
"ds_read_b64_tr_b16 a[8:9], v33 offset:224\n" \
"ds_read_b64_tr_b16 a[10:11], v33 offset:4832\n" \
"v_mfma_f32_16x16x32_bf16 v[228:231], a[12:15], a[120:123], v[228:231]\n" \
"ds_read_b64_tr_b16 a[12:13], v33 offset:9440\n" \
"s_waitcnt lgkmcnt(13)\n" \
"v_mfma_f32_16x16x32_bf16 v[224:227], a[16:19], a[108:111], v[224:227]\n" \
"ds_read_b64_tr_b16 a[14:15], v33 offset:14048\n" \
"v_mfma_f32_16x16x32_bf16 v[228:231], a[16:19], a[124:127], v[228:231]\n" \
"ds_read_b64_tr_b16 a[16:17], v33 offset:18656\n" \
"ds_read_b64_tr_b16 a[18:19], v33 offset:23264\n" \
"v_mfma_f32_16x16x32_bf16 v[232:235], a[20:23], a[220:223], v[232:235]\n" \
"v_mfma_f32_16x16x32_bf16 v[236:239], a[20:23], a[236:239], v[236:239]\n" \
"ds_read_b64_tr_b16 a[20:21], v33 offset:27872\n" \
"ds_read_b64_tr_b16 a[22:23], v33 offset:32480\n" \
"v_cvt_pk_bf16_f32 v224, v224, v225\n" \
"v_cvt_pk_bf16_f32 v225, v226, v227\n" \
"v_cvt_pk_bf16_f32 v226, v228, v229\n" \
"v_cvt_pk_bf16_f32 v227, v230, v231\n" \
"v_mfma_f32_16x16x32_bf16 v[232:235], a[24:27], a[224:227], v[232:235]\n" \
"v_mfma_f32_16x16x32_bf16 v[236:239], a[24:27], a[240:243], v[236:239]\n" \
"ds_read_b64_tr_b16 a[24:25], v33 offset:37088\n" \
"ds_read_b64_tr_b16 a[26:27], v33 offset:41696\n" \
"v_mfma_f32_16x16x32_bf16 v[232:235], a[28:31], a[228:231], v[232:235]\n" \
"v_mfma_f32_16x16x32_bf16 v[236:239], a[28:31], a[244:247], v[236:239]\n" \
"ds_read_b64_tr_b16 a[28:29], v33 offset:46304\n" \
"ds_read_b64_tr_b16 a[30:31], v33 offset:50912\n" \
"v_mfma_f32_16x16x32_bf16 v[232:235], a[192:195], a[232:235], v[232:235]\n" \
"v_mfma_f32_16x16x32_bf16 v[236:239], a[192:195], a[248:251], v[236:239]\n" \
"ds_read_b64_tr_b16 a[192:193], v33 offset:55520\n" \
"ds_read_b64_tr_b16 a[194:195], v33 offset:60128\n" \
"v_mfma_f32_16x16x32_bf16 v[232:235], a[196:199], a[160:163], v[232:235]\n" \
"v_mfma_f32_16x16x32_bf16 v[236:239], a[196:199], a[176:179], v[236:239]\n" \
"buffer_load_dwordx4 v[156:159], v20, s[56:59], s64 offen offset:2048 sc1\n" \
"v_readlane_b32 s64, v11, 51\n" \
"v_mfma_f32_16x16x32_bf16 v[232:235], a[200:203], a[164:167], v[232:235]\n" \
"ds_read_b64_tr_b16 a[196:197], v10 offset:224\n" \
"ds_read_b64_tr_b16 a[198:199], v10 offset:4832\n" \
"v_mfma_f32_16x16x32_bf16 v[236:239], a[200:203], a[180:183], v[236:239]\n" \
"ds_read_b64_tr_b16 a[200:201], v34 offset:224\n" \
"ds_read_b64_tr_b16 a[202:203], v34 offset:4832\n" \
"buffer_store_dwordx4 v[224:227], v20, s[56:59], s64 offen offset:1024 sc1\n" \
"v_mfma_f32_16x16x32_bf16 v[232:235], a[204:207], a[168:171], v[232:235]\n" \
"v_mfma_f32_16x16x32_bf16 v[236:239], a[204:207], a[184:187], v[236:239]\n" \
"ds_read_b64_tr_b16 a[204:205], v34 offset:9440\n" \
"s_waitcnt vmcnt(13)\n" \
"s_mov_b64 exec, 1\n" \
"buffer_store_dword v39, off, s[56:59], s67 offset:40 sc1\n" \
"s_mov_b64 exec, -1\n" \
"s_waitcnt lgkmcnt(11)\n" \
"v_mfma_f32_16x16x32_bf16 v[232:235], a[208:211], a[172:175], v[232:235]\n" \
"ds_read_b64_tr_b16 a[206:207], v34 offset:14048\n" \
"v_mfma_f32_16x16x32_bf16 v[236:239], a[208:211], a[188:191], v[236:239]\n" \
"ds_read_b64_tr_b16 a[208:209], v34 offset:18656\n" \
"ds_read_b64_tr_b16 a[210:211], v34 offset:23264\n" \
"v_mfma_f32_16x16x32_bf16 v[232:235], a[212:215], a[96:99], v[232:235]\n" \
"v_mfma_f32_16x16x32_bf16 v[236:239], a[212:215], a[112:115], v[236:239]\n" \
"ds_read_b64_tr_b16 a[212:213], v10 offset:9440\n" \
"ds_read_b64_tr_b16 a[214:215], v10 offset:14048\n" \
"v_mfma_f32_16x16x32_bf16 v[232:235], a[216:219], a[100:103], v[232:235]\n" \
"v_mfma_f32_16x16x32_bf16 v[236:239], a[216:219], a[116:119], v[236:239]\n" \
"ds_read_b64_tr_b16 a[216:217], v36 offset:0\n" \
"ds_read_b64_tr_b16 a[218:219], v36 offset:1024\n" \
"v_mfma_f32_16x16x32_bf16 v[232:235], a[0:3], a[104:107], v[232:235]\n" \
"v_mfma_f32_16x16x32_bf16 v[236:239], a[0:3], a[120:123], v[236:239]\n" \
"ds_read_b64_tr_b16 a[0:1], v36 offset:2048\n" \
"ds_read_b64_tr_b16 a[2:3], v36 offset:3072\n" \
"v_mfma_f32_16x16x32_bf16 v[232:235], a[4:7], a[108:111], v[232:235]\n" \
"v_mfma_f32_16x16x32_bf16 v[236:239], a[4:7], a[124:127], v[236:239]\n" \
"buffer_load_dwordx4 v[164:167], v20, s[56:59], s65 offen offset:0 sc1\n" \
"v_readlane_b32 s65, v11, 52\n" \
"v_mfma_f32_16x16x32_bf16 v[240:243], a[8:11], a[220:223], v[240:243]\n" \
"ds_read_b64_tr_b16 a[4:5], v36 offset:32\n" \
"ds_read_b64_tr_b16 a[6:7], v36 offset:1056\n" \
"v_mfma_f32_16x16x32_bf16 v[244:247], a[8:11], a[236:239], v[244:247]\n" \
"ds_read_b64_tr_b16 a[8:9], v36 offset:2080\n" \
"v_cvt_pk_bf16_f32 v232, v232, v233\n" \
"v_cvt_pk_bf16_f32 v233, v234, v235\n" \
"v_cvt_pk_bf16_f32 v234, v236, v237\n" \
"v_cvt_pk_bf16_f32 v235, v238, v239\n" \
"buffer_store_dwordx4 v[232:235], v20, s[56:59], s65 offen offset:1024 sc1\n" \
"v_mfma_f32_16x16x32_bf16 v[240:243], a[12:15], a[224:227], v[240:243]\n" \
"ds_read_b64_tr_b16 a[10:11], v36 offset:3104\n" \
"v_mfma_f32_16x16x32_bf16 v[244:247], a[12:15], a[240:243], v[244:247]\n" \
"v_mfma_f32_16x16x32_bf16 v[240:243], a[16:19], a[228:231], v[240:243]\n" \
"v_mfma_f32_16x16x32_bf16 v[244:247], a[16:19], a[244:247], v[244:247]\n" \
"v_mfma_f32_16x16x32_bf16 v[240:243], a[20:23], a[232:235], v[240:243]\n" \
"v_mfma_f32_16x16x32_bf16 v[244:247], a[20:23], a[248:251], v[244:247]\n" \
"s_waitcnt lgkmcnt(8)\n" \
"v_mfma_f32_16x16x32_bf16 v[168:171], a[24:27], a[128:131], v[168:171]\n" \
"v_mfma_f32_16x16x32_bf16 v[172:175], a[24:27], a[144:147], v[172:175]\n" \
"v_mfma_f32_16x16x32_bf16 v[240:243], a[24:27], a[160:163], v[240:243]\n" \
"s_waitcnt vmcnt(9)\n" \
"v_cmp_eq_u32_e64 s[98:99], s62, v37\n" \
"buffer_load_dword v37, v40, s[56:59], s74 offen sc1\n" \
"s_mov_b64 exec, 1\n" \
"buffer_store_dword v39, off, s[56:59], s68 offset:44 sc1\n" \
"s_mov_b64 exec, -1\n" \
"s_or_b32 s75, 0x300, s100\n" \
"s_andn2_b32 s75, s75, s98\n" \
"s_mov_b32 s21, s75\n" \
"s_bitcmp1_b32 s21, 16\n" \
"s_cselect_b32 s70, 0x40000000, s16\n" \
"s_bitcmp1_b32 s21, 17\n" \
"s_cselect_b32 s71, 0x40000000, s16\n" \
"s_bitcmp1_b32 s21, 18\n" \
"s_cselect_b32 s72, 0x40000000, s17\n" \
"s_bitcmp1_b32 s21, 19\n" \
"s_cselect_b32 s73, 0x40000000, s17\n" \
"s_bitcmp1_b32 s21, 20\n" \
"s_cselect_b32 s92, 0x40000000, s18\n" \
"s_bitcmp1_b32 s21, 21\n" \
"s_cselect_b32 s93, 0x40000000, s18\n" \
"s_bitcmp1_b32 s21, 22\n" \
"s_cselect_b32 s94, 0x40000000, s19\n" \
"s_bitcmp1_b32 s21, 23\n" \
"s_cselect_b32 s95, 0x40000000, s19\n" \
"s_bitcmp1_b32 s21, 8\n" \
"s_cselect_b32 s96, 0x40000000, s12\n" \
"s_bitcmp1_b32 s21, 9\n" \
"s_cselect_b32 s97, 0x40000000, s12\n" \
"s_add_u32 s64, s64, 0x1000\n" \
"s_add_u32 s65, s65, 0x800\n" \
"v_mfma_f32_16x16x32_bf16 v[244:247], a[24:27], a[176:179], v[244:247]\n" \
"buffer_load_dwordx4 v[112:115], v20, s[56:59], s70 offen offset:0 sc1\n" \
"v_mfma_f32_16x16x32_bf16 v[168:171], a[28:31], a[132:135], v[168:171]\n" \
"v_mfma_f32_16x16x32_bf16 v[172:175], a[28:31], a[148:151], v[172:175]\n" \
"v_mfma_f32_16x16x32_bf16 v[240:243], a[28:31], a[164:167], v[240:243]\n" \
"v_mfma_f32_16x16x32_bf16 v[244:247], a[28:31], a[180:183], v[244:247]\n" \
"buffer_load_dwordx4 v[184:187], v20, s[56:59], s71 offen offset:2048 sc1\n" \
"v_mfma_f32_16x16x32_bf16 v[168:171], a[192:195], a[136:139], v[168:171]\n" \
"v_readlane_b32 s71, v11, 54\n" \
"v_mfma_f32_16x16x32_bf16 v[172:175], a[192:195], a[152:155], v[172:175]\n" \
"v_mfma_f32_16x16x32_bf16 v[240:243], a[192:195], a[168:171], v[240:243]\n" \
"v_mfma_f32_16x16x32_bf16 v[244:247], a[192:195], a[184:187], v[244:247]\n" \
"buffer_load_dwordx4 v[120:123], v20, s[56:59], s72 offen offset:0 sc1\n" \
"v_mfma_f32_16x16x32_bf16 v[168:171], a[196:199], a[140:143], v[168:171]\n" \
"v_readlane_b32 s72, v11, 45\n" \
"v_mfma_f32_16x16x32_bf16 v[172:175], a[196:199], a[156:159], v[172:175]\n" \
"v_mfma_f32_16x16x32_bf16 v[240:243], a[196:199], a[172:175], v[240:243]\n" \
"v_mfma_f32_16x16x32_bf16 v[244:247], a[196:199], a[188:191], v[244:247]\n" \
"buffer_load_dwordx4 v[192:195], v20, s[56:59], s73 offen offset:2048 sc1\n" \
"v_mfma_f32_16x16x32_bf16 v[168:171], a[200:203], a[32:35], v[168:171]\n" \
"v_readlane_b32 s73, v11, 43\n" \
"v_mfma_f32_16x16x32_bf16 v[172:175], a[200:203], a[48:51], v[172:175]\n" \
"v_mfma_f32_16x16x32_bf16 v[240:243], a[200:203], a[96:99], v[240:243]\n" \
"v_mfma_f32_16x16x32_bf16 v[244:247], a[200:203], a[112:115], v[244:247]\n" \
"buffer_load_dwordx4 v[128:131], v20, s[56:59], s92 offen offset:0 sc1\n" \
"v_mfma_f32_16x16x32_bf16 v[168:171], a[204:207], a[36:39], v[168:171]\n" \
"v_readlane_b32 s92, v11, 44\n" \
"v_mfma_f32_16x16x32_bf16 v[172:175], a[204:207], a[52:55], v[172:175]\n" \
"v_mfma_f32_16x16x32_bf16 v[240:243], a[204:207], a[100:103], v[240:243]\n" \
"v_mfma_f32_16x16x32_bf16 v[244:247], a[204:207], a[116:119], v[244:247]\n" \
"buffer_load_dwordx4 v[200:203], v20, s[56:59], s93 offen offset:2048 sc1\n" \
"v_mfma_f32_16x16x32_bf16 v[168:171], a[208:211], a[40:43], v[168:171]\n" \
"v_mfma_f32_16x16x32_bf16 v[172:175], a[208:211], a[56:59], v[172:175]\n" \
"v_mfma_f32_16x16x32_bf16 v[240:243], a[208:211], a[104:107], v[240:243]\n" \
"s_waitcnt vmcnt(16)\n" \
"s_mov_b64 exec, 1\n" \
"buffer_store_dword v39, off, s[56:59], s69 offset:48 sc1\n" \
".fill (L_ab + 45056 - .) / 4, 4, 0xBF800000\n" \
"L_ap11:\n" \
"s_cbranch_execz L_ap0\n" \
"s_mov_b64 exec, -1\n" \
"v_mfma_f32_16x16x32_bf16 v[244:247], a[208:211], a[120:123], v[244:247]\n" \
"v_mfma_f32_16x16x32_bf16 v[168:171], a[212:215], a[44:47], v[168:171]\n" \
"v_mfma_f32_16x16x32_bf16 v[172:175], a[212:215], a[60:63], v[172:175]\n" \
"v_mfma_f32_16x16x32_bf16 v[240:243], a[212:215], a[108:111], v[240:243]\n" \
"buffer_load_dwordx4 v[176:179], v20, s[56:59], s94 offen offset:0 sc1\n" \
"v_mfma_f32_16x16x32_bf16 v[244:247], a[212:215], a[124:127], v[244:247]\n" \
"s_waitcnt lgkmcnt(0)\n" \
"v_mfma_f32_16x16x32_bf16 v[168:171], a[216:219], v[64:67], v[168:171]\n" \
"v_mfma_f32_16x16x32_bf16 v[172:175], a[216:219], v[72:75], v[172:175]\n" \
"v_mfma_f32_16x16x32_bf16 v[240:243], a[216:219], v[80:83], v[240:243]\n" \
"buffer_load_dwordx4 v[248:251], v20, s[56:59], s95 offen offset:2048 sc1\n" \
"v_mfma_f32_16x16x32_bf16 v[244:247], a[216:219], v[88:91], v[244:247]\n" \
"v_mfma_f32_16x16x32_bf16 v[168:171], a[0:3], v[68:71], v[168:171]\n" \
"v_mfma_f32_16x16x32_bf16 v[172:175], a[0:3], v[76:79], v[172:175]\n" \
"v_mfma_f32_16x16x32_bf16 v[240:243], a[0:3], v[84:87], v[240:243]\n" \
"buffer_load_dwordx4 v[188:191], v20, s[56:59], s96 offen offset:1024 sc1\n" \
"v_mfma_f32_16x16x32_bf16 v[244:247], a[0:3], v[92:95], v[244:247]\n" \
"v_mfma_f32_16x16x32_bf16 v[56:59], a[4:7], v[96:99], 0\n" \
"v_mfma_f32_16x16x32_bf16 v[60:63], a[4:7], v[104:107], 0\n" \
"v_mfma_f32_16x16x32_bf16 v[56:59], a[8:11], v[100:103], v[56:59]\n" \
"buffer_load_dwordx4 v[196:199], v20, s[56:59], s97 offen offset:3072 sc1\n" \
"v_mfma_f32_16x16x32_bf16 v[60:63], a[8:11], v[108:111], v[60:63]\n" \
"s_waitcnt vmcnt(16)\n" \
"s_mov_b64 exec, 1\n" \
"buffer_store_dword v39, off, s[56:59], s64 offset:52 sc1\n" \
"s_mov_b64 exec, -1\n" \
"s_waitcnt vmcnt(14)\n" \
"s_mov_b64 exec, 1\n" \
"buffer_store_dword v39, off, s[56:59], s65 offset:56 sc1\n" \
"s_mov_b64 exec, -1\n" \
"s_cmp_lg_u32 s20, 0\n" \
"s_cbranch_scc1 L_sna_%=\n" \
"L_rna_%=:\n" \
"s_cmp_lg_u32 s21, 0\n" \
"s_cbranch_scc1 L_snx_%=\n" \
"L_rnx_%=:\n" \
"s_waitcnt vmcnt(14)\n" \
"v_cmp_eq_u32_e64 s[98:99], s62, v37\n" \
"v_mfma_f32_16x16x32_bf16 v[168:171], v[50:53], v[116:119], v[168:171]\n" \
"s_andn2_b32 s75, 0x400, s98\n" \
"s_cbranch_scc1 L_sx0_%=\n" \
"L_rx0_%=:\n" \
"s_add_u32 s66, s63, 0x1000\n" \
"v_mfma_f32_16x16x32_bf16 v[172:175], v[48:51], v[116:119], v[172:175]\n" \
"buffer_load_dwordx4 v[204:207], v20, s[56:59], s66 offen offset:1024 sc1\n" \
"s_andn2_b32 s75, 0x800, s98\n" \
"s_cbranch_scc1 L_sx1_%=\n" \
"L_rx1_%=:\n" \
"s_add_u32 s67, s63, 0x1800\n" \
"v_mfma_f32_16x16x32_bf16 v[168:171], v[50:53], v[124:127], v[168:171]\n" \
"v_mfma_f32_16x16x32_bf16 v[172:175], v[48:51], v[124:127], v[172:175]\n" \
"v_mfma_f32_16x16x32_bf16 v[168:171], v[50:53], v[132:135], v[168:171]\n" \
"buffer_load_dwordx4 v[212:215], v20, s[56:59], s67 offen offset:1024 sc1\n" \
"s_andn2_b32 s75, 0x1000, s98\n" \
"s_cbranch_scc1 L_sx2_%=\n" \
"L_rx2_%=:\n" \
"s_add_u32 s68, s63, 0x2000\n" \
"v_mfma_f32_16x16x32_bf16 v[172:175], v[48:51], v[132:135], v[172:175]\n" \
"v_mfma_f32_16x16x32_bf16 v[168:171], v[50:53], v[140:143], v[168:171]\n" \
"v_mfma_f32_16x16x32_bf16 v[172:175], v[48:51], v[140:143], v[172:175]\n" \
"buffer_load_dwordx4 v[220:223], v20, s[56:59], s68 offen offset:1024 sc1\n" \
"s_andn2_b32 s75, 0x2000, s98\n" \
"s_cbranch_scc1 L_sx3_%=\n" \
"L_rx3_%=:\n" \
"s_add_u32 s69, s63, 0x2800\n" \
"v_mfma_f32_16x16x32_bf16 v[168:171], v[50:53], v[148:151], v[168:171]\n" \
"v_mfma_f32_16x16x32_bf16 v[172:175], v[48:51], v[148:151], v[172:175]\n" \
"v_mfma_f32_16x16x32_bf16 v[168:171], v[50:53], v[156:159], v[168:171]\n" \
"buffer_load_dwordx4 v[228:231], v20, s[56:59], s69 offen offset:1024 sc1\n" \
"s_andn2_b32 s75, 0x4000, s98\n" \
"s_cbranch_scc1 L_sx4_%=\n" \
"L_rx4_%=:\n" \
"s_add_u32 s70, s63, 0x3000\n" \
"v_mfma_f32_16x16x32_bf16 v[172:175], v[48:51], v[156:159], v[172:175]\n" \
"v_mfma_f32_16x16x32_bf16 v[168:171], v[50:53], v[164:167], v[168:171]\n" \
"v_mfma_f32_16x16x32_bf16 v[172:175], v[48:51], v[164:167], v[172:175]\n" \
"buffer_load_dwordx4 v[236:239], v20, s[56:59], s70 offen offset:1024 sc1\n" \
"s_waitcnt vmcnt(17)\n" \
"v_mfma_f32_16x16x32_bf16 v[56:59], v[50:53], v[112:115], v[56:59]\n" \
"v_mfma_f32_16x16x32_bf16 v[60:63], v[48:51], v[112:115], v[60:63]\n" \
"s_waitcnt vmcnt(16)\n" \
"v_mfma_f32_16x16x32_bf16 v[56:59], v[50:53], v[184:187], v[56:59]\n" \
"v_mfma_f32_16x16x32_bf16 v[60:63], v[48:51], v[184:187], v[60:63]\n" \
"v_cvt_pk_bf16_f32 v168, v168, v169\n" \
"v_cvt_pk_bf16_f32 v172, v172, v173\n" \
"v_cvt_pk_bf16_f32 v169, v170, v171\n" \
"v_cvt_pk_bf16_f32 v173, v174, v175\n" \
"buffer_store_dwordx2 v[168:169], v42, s[88:91], s73 offen sc1\n" \
"buffer_store_dwordx2 v[172:173], v43, s[88:91], s73 offen sc1\n" \
"s_waitcnt vmcnt(17)\n" \
"v_mfma_f32_16x16x32_bf16 v[56:59], v[50:53], v[120:123], v[56:59]\n" \
"v_mfma_f32_16x16x32_bf16 v[60:63], v[48:51], v[120:123], v[60:63]\n" \
"s_waitcnt vmcnt(16)\n" \
"v_mfma_f32_16x16x32_bf16 v[56:59], v[50:53], v[192:195], v[56:59]\n" \
"v_mfma_f32_16x16x32_bf16 v[60:63], v[48:51], v[192:195], v[60:63]\n" \
"s_waitcnt vmcnt(15)\n" \
"v_mfma_f32_16x16x32_bf16 v[56:59], v[50:53], v[128:131], v[56:59]\n" \
"v_mfma_f32_16x16x32_bf16 v[60:63], v[48:51], v[128:131], v[60:63]\n" \
"s_waitcnt vmcnt(14)\n" \
"v_mfma_f32_16x16x32_bf16 v[56:59], v[50:53], v[200:203], v[56:59]\n" \
"v_mfma_f32_16x16x32_bf16 v[60:63], v[48:51], v[200:203], v[60:63]\n" \
"s_waitcnt vmcnt(12)\n" \
"v_mfma_f32_16x16x32_bf16 v[56:59], v[50:53], v[176:179], v[56:59]\n" \
"v_mfma_f32_16x16x32_bf16 v[60:63], v[48:51], v[176:179], v[60:63]\n" \
"s_waitcnt vmcnt(11)\n" \
"v_mfma_f32_16x16x32_bf16 v[56:59], v[50:53], v[248:251], v[56:59]\n" \
"v_mfma_f32_16x16x32_bf16 v[60:63], v[48:51], v[248:251], v[60:63]\n" \
"s_waitcnt vmcnt(10)\n" \
"v_mfma_f32_16x16x32_bf16 v[240:243], v[50:53], v[188:191], v[240:243]\n" \
"v_mfma_f32_16x16x32_bf16 v[244:247], v[48:51], v[188:191], v[244:247]\n" \
"s_waitcnt vmcnt(9)\n" \
"v_mfma_f32_16x16x32_bf16 v[240:243], v[50:53], v[196:199], v[240:243]\n" \
"v_mfma_f32_16x16x32_bf16 v[244:247], v[48:51], v[196:199], v[244:247]\n" \
"s_mov_b64 exec, 1\n" \
"buffer_store_dword v46, off, s[56:59], s71 offset:0 sc1\n" \
"s_mov_b64 exec, -1\n" \
"v_cvt_pk_bf16_f32 v56, v56, v57\n" \
"v_cvt_pk_bf16_f32 v60, v60, v61\n" \
"v_cvt_pk_bf16_f32 v57, v58, v59\n" \
"v_cvt_pk_bf16_f32 v61, v62, v63\n" \
"buffer_store_dwordx2 v[56:57], v42, s[88:91], s72 offen sc1\n" \
"buffer_store_dwordx2 v[60:61], v43, s[88:91], s72 offen sc1\n" \
"s_waitcnt vmcnt(9)\n" \
"v_mfma_f32_16x16x32_bf16 v[240:243], v[50:53], v[204:207], v[240:243]\n" \
"v_mfma_f32_16x16x32_bf16 v[244:247], v[48:51], v[204:207], v[244:247]\n" \
"s_waitcnt vmcnt(8)\n" \
"v_mfma_f32_16x16x32_bf16 v[240:243], v[50:53], v[212:215], v[240:243]\n" \
"v_mfma_f32_16x16x32_bf16 v[244:247], v[48:51], v[212:215], v[244:247]\n" \
"s_waitcnt vmcnt(7)\n" \
"v_mfma_f32_16x16x32_bf16 v[240:243], v[50:53], v[220:223], v[240:243]\n" \
"v_mfma_f32_16x16x32_bf16 v[244:247], v[48:51], v[220:223], v[244:247]\n" \
"s_waitcnt vmcnt(6)\n" \
"v_mfma_f32_16x16x32_bf16 v[240:243], v[50:53], v[228:231], v[240:243]\n" \
"v_mfma_f32_16x16x32_bf16 v[244:247], v[48:51], v[228:231], v[244:247]\n" \
"s_waitcnt vmcnt(5)\n" \
"v_mfma_f32_16x16x32_bf16 v[240:243], v[50:53], v[236:239], v[240:243]\n" \
"v_mfma_f32_16x16x32_bf16 v[244:247], v[48:51], v[236:239], v[244:247]\n" \
"s_nop 6\n" \
"v_cvt_pk_bf16_f32 v240, v240, v241\n" \
"v_cvt_pk_bf16_f32 v244, v244, v245\n" \
"v_cvt_pk_bf16_f32 v241, v242, v243\n" \
"v_cvt_pk_bf16_f32 v245, v246, v247\n" \
"buffer_store_dwordx2 v[240:241], v42, s[88:91], s92 offen sc1\n" \
"buffer_store_dwordx2 v[244:245], v43, s[88:91], s92 offen sc1\n"
#define ATT_VCLOB "v21","v22","v23","v24","v25","v26","v27","v28","v29","v32","v33","v34","v35","v36","v37","v38","v39","v40","v41","v42","v43","v44","v45","v46","v47","v48","v49","v50","v51","v52","v53","v54","v55","v56","v57","v58","v59","v60","v61","v62","v63","v64","v65","v66","v67","v68","v69","v70","v71","v72","v73","v74","v75","v76","v77","v78","v79","v80","v81","v82","v83","v84","v85","v86","v87","v88","v89","v90","v91","v92","v93","v94","v95","v96","v97","v98","v99","v100","v101","v102","v103","v104","v105","v106","v107","v108","v109","v110","v111","v112","v113","v114","v115","v116","v117","v118","v119","v120","v121","v122","v123","v124","v125","v126","v127","v128","v129","v130","v131","v132","v133","v134","v135","v136","v137","v138","v139","v140","v141","v142","v143","v144","v145","v146","v147","v148","v149","v150","v151","v152","v153","v154","v155","v156","v157","v158","v159","v160","v161","v162","v163","v164","v165","v166","v167","v168","v169","v170","v171","v172","v173","v174","v175","v176","v177","v178","v179","v180","v181","v182","v183","v184","v185","v186","v187","v188","v189","v190","v191","v192","v193","v194","v195","v196","v197","v198","v199","v200","v201","v202","v203","v204","v205","v206","v207","v208","v209","v210","v211","v212","v213","v214","v215","v216","v217","v218","v219","v220","v221","v222","v223","v224","v225","v226","v227","v228","v229","v230","v231","v232","v233","v234","v235","v236","v237","v238","v239","v240","v241","v242","v243","v244","v245","v246","v247","v248","v249","v250","v251","v252","v253","v254","v255"
#define ATT_SCLOB "s63","s64","s65","s66","s67","s68","s69","s70","s71","s72","s73","s74","s75","s76","s77","s78","s79","s80","s81","s82","s83","s84","s85","s86","s87","s88","s89","s90","s91","s92","s93","s95","s96","s97","s98","s99","s100","s101"
#define ATT_WTCH 0
#define ATT_PEEL 1
#define ATT_PEEL_FOW 0
enum { T_fo, T_fj, T_fa, T_fas, T_fb, T_fbs, T_f8, T_mk8, T_wt, T_wol, T_woh, T_wbl, T_wbh, T_outl, T_outh, T_r1l, T_r1h, T_r1n, T_r1s, T_m0, T_m1, T_m1b, T_mw, T_m3, T_m4, T_ml, T_w0, T_w1, T_w2, T_w3, T_A1, T_A2, T_A3, T_B1, T_B2, T_B3, T_Ao, T_Bo, T_Al, T_Bl, T_l8, T_xr, T_x8r, T_oA, T_oB, T_o8, T_x0, T_x1, T_x2, T_x3, T_x4, T_x5, T_x6, T_x8, T_ep };

__device__ __forceinline__ void out_consts(u32x4 (&vp)[3], const u16* wo, const u16* wob, const u16* out, const unsigned b, const unsigned w, const unsigned lane) {
    const unsigned sp = b & 7u, nt = (b >> 3) & 3u, j = b >> 5, head = 4u * sp + nt, OOR = 0x40000000u;
    unsigned vw[9];
#pragma unroll
    for (unsigned i = 0; i < 9u; ++i) {
        const unsigned q = 64u * i + lane, row = __umul24(q, 3641u) >> 16, c = q - __umul24(row, 18u);
        vw[i] = __umul24(row, DM * 2) + (c < 16u ? (c * 16u + sp * 32u + 32u) & 255u : c * 16u) - (i & 3u) * 1024u;
    }
    const unsigned row = 16u * w + (lane >> 2), r9 = __umul24(16u * (row >> 5) + (row & 12u) + 2u * ((row >> 4) & 1u) + ((row >> 1) & 1u), 18u) + 16u + (row & 1u);
    unsigned vwl = __umul24(r9, DM * 2) + ((lane & 2u) ? 256u : sp * 32u) + (lane & 1u) * 16u, vrd = __umul24(lane >> 2, 288u) + (lane & 3u) * 8u;
    unsigned tab = 0;
#define TB(n, v) asm("v_writelane_b32 %0, %1, %2" : "+v"(tab) : "s"((unsigned)(v)), "n"((int)T_##n))
    const unsigned FJ = (unsigned)WS_F + (F_J + 640u * j) * 4u, mt = 2u * (w & 2u) + (w & 1u), w2 = w < 2u ? w : 2u, hw = head * 147456u;
    const unsigned FR = (unsigned)WS_F + F_R * 4u + (8u * j + sp) * 1024u;
    TB(fo, FJ + (ATT_PEEL_FOW ? 16u * head + 4u * w : 4u * head)); TB(fj, FJ); TB(fa, FR + (w | 2u) * 256u); TB(fas, FR + w * 256u + nt * 4u);
    TB(fb, FR + 128u + w * 256u); TB(fbs, FR + 128u + w * 256u + nt * 4u);
    TB(ep, ssel(b + w, OOR, (unsigned)WS_F + F_EPOCH * 4u));
    const u16* pwo = wo + nt * 144u; const u16* pwb = wob + nt * 144u;
    TB(wol, PLO(pwo)); TB(woh, PHI(pwo) & 0xffffu); TB(wbl, PLO(pwb)); TB(wbh, PHI(pwb) & 0xffffu); TB(outl, PLO(out)); TB(outh, PHI(out) & 0xffffu); TB(r1l, sslt<3>(w, PLO(pwo), PLO(pwb))); TB(r1h, sslt<3>(w, PHI(pwo), PHI(pwb)) & 0xffffu);
    TB(r1n, sslt<3>(w, 72u * sp + 71u, 4095u) * 1152u + 288u); TB(r1s, sslt<3>(w, (72u * sp + 32u * w) * 1152u, hw + 110592u));
    TB(m0, 131072u + 9216u * w2); TB(w0, hw + 36864u * w2); TB(m1, ATT_PEEL ? sslt<3>(w, 41984u + 9216u * w, 32768u) : 24576u + 9216u * w); TB(m1b, ssel(w ^ 2u, sslt<3>(w, 41984u + 9216u * w, 32768u), 114688u - 4096u)); TB(mw, 65536u + 9216u * w); TB(m3, sslt<3>(w, 102400u + 9216u * w, 0u)); TB(m4, sslt<3>(w, 131072u + 9216u * w, 9216u)); TB(ml, (ATT_PEEL ? 18432u : 61440u) + 1024u * w);
    const unsigned tA = (unsigned)WS_AF + (8u * j + mt) * (KS_TOT * 1024u), tB = (unsigned)WS_AF + (120u - 8u * j + mt) * (KS_TOT * 1024u);
#define HK(k) (4u * sp + ((nt + k) & 3u))
    TB(w1, HK(1u) * 147456u + w * 36864u); TB(w2, HK(2u) * 147456u + w * 36864u); TB(w3, HK(3u) * 147456u + w * 36864u);
    TB(A1, tA + 18432u + HK(1u) * 4096u); TB(A2, tA + 18432u + HK(2u) * 4096u); TB(A3, tA + 18432u + HK(3u) * 4096u);
    TB(B1, tB + 18432u + HK(1u) * 4096u); TB(B2, tB + 18432u + HK(2u) * 4096u); TB(B3, tB + 18432u + HK(3u) * 4096u);
    TB(Ao, tA + sp * 2048u); TB(Bo, tB + sp * 2048u); TB(Al, tA + 16384u); TB(Bl, tB + 16384u);
    const unsigned nred = w ^ (sp >> 1), blk = sp & 1u;
    const unsigned xg = (unsigned)WS_X + (4u * j + nt) * 589824u + w * 16384u, oA = (128u * j + 16u * mt) * 1152u + nt * 288u, oB = (1920u - 128u * j + 16u * mt) * 1152u + nt * 288u;
    TB(l8, ssel(nred, OOR, ssel(blk, tB, tA) + 16384u)); TB(xr, xg + sp * 65536u); TB(x8r, ssel(nred, OOR, xg + 524288u + (sp & 1u) * 1024u));
    TB(oA, oA + sp * 32u); TB(oB, oB + sp * 32u); TB(o8, ssel(nred, OOR, ssel(blk, oB, oA) + 256u)); TB(x8, xg + 524288u + sp * 2048u);
    TB(f8, xg + w * 131072u + 14400u + sp * 4u); TB(mk8, ssel(nred, 0u, 0xff0000u)); TB(wt, hw + j * 18432u + w * 4608u);
#define XI(i) TB(x##i, xg + ((sp + 1u + i) & 7u) * 65536u + i * 2048u)
    XI(0); XI(1); XI(2); XI(3); XI(4); XI(5); XI(6);
#undef XI
#undef HK
#undef TB
    vp[0] = (u32x4){vw[0], vw[1], vw[2], vw[3]}; vp[1] = (u32x4){vw[4], vw[5], vw[6], vw[7]}; vp[2] = (u32x4){vw[8], vwl, vrd, tab};
}
__global__ __launch_bounds__(256, 1) __attribute__((aligned(4096))) void fused_kernel(const u16* __restrict__ x, const u16* __restrict__ ckv, const u16* __restrict__ wq, const u16* __restrict__ wuk,
                                                       const u16* __restrict__ wuv, const u16* __restrict__ wqkv, const u16* __restrict__ gw,
                                                       const u16* __restrict__ wo, const u16* __restrict__ wob, char* __restrict__ ws_, u16* __restrict__ out) {
    using namespace att;
    extern __shared__ __attribute__((aligned(16))) char lds[];
    u32x4 sa[5]; u32x2 s24, s28; char* ws; unsigned tdum; kaptr ka = (kaptr)__builtin_amdgcn_kernarg_segment_ptr();
    asm volatile("s_load_dwordx8 s[8:15], %[ka], 0x0\n\ts_load_dwordx8 s[16:23], %[ka], 0x20\n\ts_getpc_b64 s[4:5]\n\tv_lshlrev_b32_e32 v191, 7, %[t]\n\ts_bfe_u32 s6, %[b], 0x20003\n\t"
                 "v_lshl_add_u32 v191, s6, 15, v191\n\tv_min_u32_e32 v191, %[e], v191\n\tglobal_load_dword v191, v191, s[4:5]\n\ts_load_dwordx4 s[24:27], %[ka], 0x40\n\ts_load_dwordx2 s[28:29], %[ka], 0x50"
                 : "={s[8:11]}"(sa[0]), "={s[12:15]}"(sa[1]), "={s[16:19]}"(sa[2]), "={s[20:23]}"(sa[3]), "={s[24:25]}"(s24), "={s[26:27]}"(ws), "={s[28:29]}"(s28), "={v191}"(tdum), [ka] "+{s[0:1]}"(ka)
                 : [t] "v"(threadIdx.x), [b] "s"(blockIdx.x), [e] "i"(CT_END) : "s4", "s5", "s6", "scc");
    SBAR();
    const unsigned wid = rfl(threadIdx.x >> 6);
    unsigned tag, vl;
    const int b = blockIdx.x;
    u32x4 vp[5], srdA; u32x2 sd;
    proj_phase(ka, ws, wid, vp, vl, sa, s24, s28, tdum, srdA, sd, tag
    );
    unsigned* const flags = (unsigned*)(ws + WS_F);
    RTS(2);
#define AST(L) do {} while (0)
#define ATT_TAIL "s_endpgm\n"
    unsigned abad;
    asm volatile(ATT_ASM ATT_TAIL "L_end_%=:\n s_nop 1"
                 : "={s94}"(abad)
                 : "{v[0:3]}"(vp[0]), "{v[4:7]}"(vp[1]), "{v[8:11]}"(vp[2]), "{v[12:15]}"(vp[3]), "{v[16:19]}"(vp[4]), "{v20}"(vl), "{s[56:59]}"(srdA), "{s[60:61]}"(sd), "{s62}"(tag),
                   "{s[8:11]}"(sa[0]), "{s[12:15]}"(sa[1]), "{s[16:19]}"(sa[2]), "{s[20:23]}"(sa[3]), "{s24}"(sa[4][0]), "{s3}"(wid), "{s[28:29]}"(s28)
                 : "memory", "m0", "vcc", "scc", ATT_VCLOB, ATT_SCLOB, ACLOB_LO, ACLOB_HI);
    {
        unsigned b2 = b, w2 = wid;
        asm volatile("s_waitcnt vmcnt(0)" : "+s"(ka), "+s"(b2), "+s"(w2), "+s"(ws) :: "memory");
        const unsigned j2 = b2 >> 5, p2 = w2 & 1u, h2 = (b2 & 7u) * 4u + ((b2 >> 3) & 3u);
#pragma unroll 1
        for (unsigned i = w2 & 2u; i < ((w2 & 2u) ? (abad ? 8u : 4u) : 2u); ++i)
            att_exact(ka[0], ka[2], ws, h2, (i < 4u ? 1920u - 128u * j2 : 128u * j2 - 128u) + 32u * i + 16u * p2, *reinterpret_cast<const float*>(lds + LDS_G + (32u * i + 16u * p2 + (lane_id() & 15u)) * 4u));
        asm volatile("s_waitcnt vmcnt(0)" ::: "memory");
        unsigned* fa = (unsigned*)(ws + WS_F) + F_R + (8u * j2 + (b2 & 7u)) * 256u + (2u + p2) * 64u + ((b2 >> 3) & 3u);
        if (w2 & 2u) st_flag(fa, tag);
        wait_flags(fa, ld_flag(fa), tag);
        const unsigned l2 = lane_id(); u32x4 c[3]; out_consts(c, ka[7], ka[8], ka[10], b2, w2, l2);
        asm volatile("s_waitcnt vmcnt(0) lgkmcnt(0)\n\ts_nop 7\n\ts_branch L_att_stub" :: "{v[0:3]}"(c[0]), "{v[4:7]}"(c[1]), "{v[8:11]}"(c[2]), "{v20}"(l2 * 16u), "{s[56:59]}"(SRDA), "{s60}"(SD(8u * j2, h2, w2)),
                     "{s61}"(SD(120u - 8u * j2, h2, w2)), "{s62}"(tag) : "memory");
    }
    RTS(5);
}

extern "C" void kernel_launch(void* const* d_in, const int* in_sizes, int n_in, void* d_out, int out_size, void* d_ws, size_t ws_size,
                              hipStream_t stream) {
    (void)hipFuncSetAttribute((const void*)fused_kernel, hipFuncAttributeMaxDynamicSharedMemorySize, att::LDS_BYTES);
    fused_kernel<<<dim3(256), 256, att::LDS_BYTES, stream>>>((const u16*)d_in[0], (const u16*)d_in[4], (const u16*)d_in[5], (const u16*)d_in[6], (const u16*)d_in[7], (const u16*)d_in[1],
                                                             (const u16*)d_in[9], (const u16*)d_in[2], (const u16*)d_in[8], (char*)d_ws, (u16*)d_out);
}
```

```cpp
#include <hip/hip_runtime.h>
#include <hip/hip_bf16.h>
#include <stdint.h>
#include <utility>

typedef short bf16x8 __attribute__((ext_vector_type(8)));
typedef short s16x4 __attribute__((ext_vector_type(4)));
typedef float f32x16 __attribute__((ext_vector_type(16)));
typedef float f32x4 __attribute__((ext_vector_type(4)));
typedef unsigned u32x4 __attribute__((ext_vector_type(4)));
typedef unsigned u32x2 __attribute__((ext_vector_type(2)));
typedef unsigned short u16;

constexpr int S = 2048, DM = 576, NH = 32, HD = 128, DC = 512, NQKV = 1728, DHA = 18;
constexpr int NBIG = NH * HD;
constexpr int KS_TOT = 146;

#define SBAR() __builtin_amdgcn_sched_barrier(0)
constexpr size_t WS_Q = 0, WS_K = WS_Q + (size_t)S * NBIG * 2, WS_V = WS_K + (size_t)S * NBIG * 2, WS_AF = WS_V + (size_t)S * NBIG * 2, WS_X = WS_AF + (size_t)128 * KS_TOT * 1024,
                 WS_P = WS_X + (size_t)64 * 16 * 9216, WS_F = WS_P + (size_t)128 * (4 * 32 * 256 + 1024) * 4;
constexpr size_t WS_L = (size_t)96 << 20;
static_assert(WS_F == 96206848, "workspace map is baked into the generated asm");
constexpr int F_EPOCH = 0, F_J = 1024, F_ST = 8192, F_KV = 40960, F_R = 65536;
constexpr unsigned TAG_STEP = 0x9E3779B1u;
__device__ __forceinline__ unsigned ld_flag(const unsigned* p) { return __hip_atomic_load(p, __ATOMIC_RELAXED, __HIP_MEMORY_SCOPE_AGENT); }
__device__ __forceinline__ void st_flag(unsigned* p, unsigned v) { __hip_atomic_store(p, v, __ATOMIC_RELAXED, __HIP_MEMORY_SCOPE_AGENT); }
__device__ __forceinline__ unsigned ld_flag_async(const unsigned* p) { unsigned v; asm volatile("global_load_dword %0, %1, off sc1" : "=v"(v) : "v"(p) : "memory"); return v; }
#define WAITV(v, N) asm volatile("s_waitcnt vmcnt(" #N ")" : "+v"(v) :: "memory")
__device__ __forceinline__ void wait_flags(const unsigned* p, unsigned v, unsigned tag) {
    if (__builtin_expect(__builtin_amdgcn_ballot_w64(v != tag) != 0ull, 0)) {
        unsigned spins = 0;
        do { __builtin_amdgcn_s_sleep(1); } while (__builtin_amdgcn_ballot_w64(ld_flag(p) != tag) != 0ull && ++spins < (1u << 22));
    }
    asm volatile("" ::: "memory");
}

__device__ __forceinline__ unsigned cvtpk(float lo, float hi) {
    unsigned r; asm volatile("v_cvt_pk_bf16_f32 %0, %1, %2" : "=v"(r) : "v"(lo), "v"(hi)); return r;
}
__device__ __forceinline__ float bf2f(u16 v) { return __uint_as_float(((unsigned)v) << 16); }
__device__ __forceinline__ int crow(int r, int hi) { return (r & 3) + 8 * (r >> 2) + 4 * hi; }

typedef __attribute__((address_space(3))) s16x4* lds_s16x4_p;
__device__ __forceinline__ s16x4 trrd(unsigned addr) { return __builtin_amdgcn_ds_read_tr16_b64_v4i16((lds_s16x4_p)addr); }

__device__ __forceinline__ unsigned lane_id() { unsigned l; asm volatile("v_mbcnt_lo_u32_b32 %0, -1, 0\n\tv_mbcnt_hi_u32_b32 %0, -1, %0" : "=v"(l)); return l; }
#define RTS(slot) do {} while (0)
#define CT_END 0x1f300
#define ACLOB_LO "a0","a1","a2","a3","a4","a5","a6","a7","a8","a9","a10","a11","a12","a13","a14","a15","a16","a17","a18","a19","a20","a21","a22","a23","a24","a25","a26","a27","a28","a29","a30","a31","a32","a33","a34","a35","a36","a37","a38","a39","a40","a41","a42","a43","a44","a45","a46","a47","a48","a49","a50","a51","a52","a53","a54","a55","a56","a57","a58","a59","a60","a61","a62","a63","a64","a65","a66","a67","a68","a69","a70","a71","a72","a73","a74","a75","a76","a77","a78","a79","a80","a81","a82","a83","a84","a85","a86","a87","a88","a89","a90","a91","a92","a93","a94","a95","a96","a97","a98","a99","a100","a101","a102","a103","a104","a105","a106","a107","a108","a109","a110","a111","a112","a113","a114","a115","a116","a117","a118","a119","a120","a121","a122","a123","a124","a125","a126","a127"
#define ACLOB_HI "a128","a129","a130","a131","a132","a133","a134","a135","a136","a137","a138","a139","a140","a141","a142","a143","a144","a145","a146","a147","a148","a149","a150","a151","a152","a153","a154","a155","a156","a157","a158","a159","a160","a161","a162","a163","a164","a165","a166","a167","a168","a169","a170","a171","a172","a173","a174","a175","a176","a177","a178","a179","a180","a181","a182","a183","a184","a185","a186","a187","a188","a189","a190","a191","a192","a193","a194","a195","a196","a197","a198","a199","a200","a201","a202","a203","a204","a205","a206","a207","a208","a209","a210","a211","a212","a213","a214","a215","a216","a217","a218","a219","a220","a221","a222","a223","a224","a225","a226","a227","a228","a229","a230","a231","a232","a233","a234","a235","a236","a237","a238","a239","a240","a241","a242","a243","a244","a245","a246","a247","a248","a249","a250","a251","a252","a253","a254","a255"
#define PLO(p) ((unsigned)(size_t)(p))
#define PHI(p) ((unsigned)((size_t)(p) >> 32))
__device__ __forceinline__ unsigned vsel(bool c, unsigned a, unsigned b) { asm("" : "+v"(a), "+v"(b)); return c ? a : b; }
__device__ __forceinline__ unsigned ssel(unsigned c, unsigned a, unsigned b) { unsigned r; asm("s_cmp_lg_u32 %1, 0\n\ts_cselect_b32 %0, %2, %3" : "=s"(r) : "s"(c), "s"(a), "s"(b) : "scc"); return r; }
template <int N> __device__ __forceinline__ unsigned sslt(unsigned c, unsigned a, unsigned b) { unsigned r; asm("s_cmpk_lt_u32 %1, %4\n\ts_cselect_b32 %0, %2, %3" : "=s"(r) : "s"(c), "s"(a), "s"(b), "n"(N) : "scc"); return r; }
namespace att {
constexpr int LDS_BYTES = 163840;
constexpr unsigned LDS_G = 159744u, LDS_L = 161792u;
}
#define SD(J8, H, W) ((unsigned)WS_AF + (((J8) + 2u * ((W) & 2u) + ((W) & 1u)) * KS_TOT + 18u + 4u * (H)) * 1024u)
#define SRDA (u32x4){PLO(ws), PHI(ws) & 0xffffu, (unsigned)WS_L, 0x00020000u}
#define PROJ_ASM \
".p2align 3\n" \
"L_pb:\n" \
"s_waitcnt lgkmcnt(0)\n" \
"s_mov_b32 s64, s10\n" \
"s_and_b32 s65, s11, 0xffff\n" \
"s_mov_b32 s66, 0x200000\n" \
"s_mov_b32 s67, 0x20000\n" \
"s_mov_b32 s68, s14\n" \
"s_and_b32 s69, s15, 0xffff\n" \
"s_mov_b32 s70, 0x400000\n" \
"s_mov_b32 s71, 0x20000\n" \
"s_mov_b32 s72, s16\n" \
"s_and_b32 s73, s17, 0xffff\n" \
"s_mov_b32 s74, 0x400000\n" \
"s_mov_b32 s75, 0x20000\n" \
"s_add_u32 m0, s30, 0x0\n" \
"s_movk_i32 s84, 0x0\n" \
"buffer_load_dwordx4 v22, s[64:67], s84 offen offset:0 lds\n" \
"s_movk_i32 s84, 0x1c00\n" \
"buffer_load_dwordx4 v23, s[64:67], s84 offen offset:1024 lds\n" \
"s_movk_i32 s84, 0x3800\n" \
"buffer_load_dwordx4 v22, s[64:67], s84 offen offset:2048 lds\n" \
"s_movk_i32 s84, 0x5400\n" \
"buffer_load_dwordx4 v23, s[64:67], s84 offen offset:3072 lds\n" \
"s_add_u32 m0, s31, 0x0\n" \
"s_movk_i32 s84, 0x0\n" \
"buffer_load_dwordx4 v26, s[68:71], s84 offen offset:0 lds\n" \
"s_movk_i32 s84, 0x7c00\n" \
"buffer_load_dwordx4 v26, s[68:71], s84 offen offset:1024 lds\n" \
"s_addk_i32 s84, 0x7c00\n" \
"buffer_load_dwordx4 v27, s[68:71], s84 offen offset:2048 lds\n" \
"s_addk_i32 s84, 0x7c00\n" \
"buffer_load_dwordx4 v27, s[68:71], s84 offen offset:3072 lds\n" \
"s_movk_i32 s7, 0x300\n" \
"s_lshr_b32 s4, s2, 1\n" \
"s_and_b32 s4, s4, 60\n" \
"s_or_b32 s4, s4, s3\n" \
"v_lshrrev_b32_e32 v212, 4, v20\n" \
"v_min_u32_e32 v212, 59, v212\n" \
"s_cmpk_lt_u32 s2, 0x80\n" \
"s_cselect_b32 s6, s14, s16\n" \
"v_subrev_u32_e32 v214, 48, v212\n" \
"v_mov_b32_e32 v212, s10\n" \
"v_mov_b32_e32 v215, s6\n" \
"s_cmpk_lt_u32 s2, 0x80\n" \
"s_cselect_b32 s5, s15, s17\n" \
"v_cmp_gt_u32_e32 vcc, s7, v20\n" \
"v_mov_b32_e64 v213, s11\n" \
"v_bfe_u32 v216, v20, 4, 3\n" \
"v_cndmask_b32_e32 v212, v212, v215, vcc\n" \
"v_mov_b32_e32 v215, s5\n" \
"s_lshl_b32 s5, s3, 4\n" \
"v_cndmask_b32_e32 v213, v213, v215, vcc\n" \
"v_lshlrev_b32_e64 v215, 5, v20\n" \
"v_and_b32_e32 v215, 0x7000, v215\n" \
"v_lshl_add_u32 v215, s4, 6, v215\n" \
"s_lshl_b32 s4, s2, 3\n" \
"s_and_b32 s4, s4, 56\n" \
"v_or3_b32 v215, v215, s4, v216\n" \
"s_lshl_b32 s4, s2, 6\n" \
"s_or_b32 s6, s5, s4\n" \
"v_cmp_gt_u32_e64 s[4:5], 6, v214\n" \
"v_add_u32_e32 v215, 0x2000, v215\n" \
"s_nop 0\n" \
"v_cndmask_b32_e64 v216, 4, 2, s[4:5]\n" \
"v_add3_u32 v214, s6, v214, v216\n" \
"s_lshl_b32 s4, s3, 9\n" \
"v_cndmask_b32_e32 v214, v214, v215, vcc\n" \
"v_lshlrev_b32_e32 v214, 7, v214\n" \
"v_mov_b32_e64 v215, 0\n" \
"v_lshl_add_u64 v[184:185], v[212:213], 0, v[214:215]\n" \
"s_or_b32 s32, s4, 0x25000\n" \
"s_add_u32 m0, s30, 0x1000\n" \
"s_mov_b32 s84, 0x8000\n" \
"buffer_load_dwordx4 v22, s[64:67], s84 offen offset:0 lds\n" \
"s_and_b32 s33, s3, 1\n" \
"v_lshrrev_b32_e32 v212, 4, v20\n" \
"v_lshrrev_b32_e64 v214, 8, v20\n" \
"v_bfe_u32 v212, v212, 1, 3\n" \
"v_lshlrev_b32_e32 v215, 2, v20\n" \
"s_addk_i32 s84, 0x1c00\n" \
"buffer_load_dwordx4 v23, s[64:67], s84 offen offset:1024 lds\n" \
"v_lshrrev_b32_e64 v216, 1, v20\n" \
"v_bfe_u32 v213, v20, 4, 4\n" \
"s_lshl_b32 s4, s3, 5\n" \
"v_xor_b32_e32 v212, v212, v214\n" \
"v_lshlrev_b32_e32 v214, 11, v214\n" \
"s_addk_i32 s84, 0x1c00\n" \
"buffer_load_dwordx4 v22, s[64:67], s84 offen offset:2048 lds\n" \
"v_and_b32_e32 v215, 0x300, v215\n" \
"v_and_b32_e64 v217, 24, v216\n" \
"v_and_or_b32 v213, s4, 64, v213\n" \
"v_or3_b32 v214, v215, v214, v217\n" \
"s_lshl_b32 s4, s33, 14\n" \
"s_addk_i32 s84, 0x1c00\n" \
"buffer_load_dwordx4 v23, s[64:67], s84 offen offset:3072 lds\n" \
"v_and_b32_e32 v215, 0xe0, v216\n" \
"v_lshlrev_b32_e32 v213, 7, v213\n" \
"v_lshlrev_b32_e32 v212, 4, v212\n" \
"v_or3_b32 v214, v214, s4, v215\n" \
"v_or_b32_e64 v28, v212, v213\n" \
"s_add_u32 m0, s31, 0x4000\n" \
"s_movk_i32 s84, 0x0\n" \
"buffer_load_dwordx4 v26, s[72:75], s84 offen offset:0 lds\n" \
"v_add_u32_e32 v32, 0x10000, v214\n" \
"v_bitop3_b32 v29, v212, 64, v213 bitop3:0x36\n" \
"v_xor_b32_e32 v33, 0x20, v32\n" \
"v_xor_b32_e64 v34, 0x40, v32\n" \
"v_xor_b32_e32 v35, 0x60, v32\n" \
"s_movk_i32 s84, 0x7c00\n" \
"buffer_load_dwordx4 v26, s[72:75], s84 offen offset:1024 lds\n" \
"v_xor_b32_e32 v36, 0x80, v32\n" \
"v_xor_b32_e32 v37, 0xa0, v32\n" \
"v_xor_b32_e32 v38, 0xc0, v32\n" \
"v_xor_b32_e32 v39, 0xe0, v32\n" \
"s_mov_b32 s80, s26\n" \
"s_addk_i32 s84, 0x7c00\n" \
"buffer_load_dwordx4 v27, s[72:75], s84 offen offset:2048 lds\n" \
"s_and_b32 s81, s27, 0xffff\n" \
"s_mov_b32 s82, 0x10000000\n" \
"s_mov_b32 s83, 0x20000\n" \
"s_mov_b32 s76, s18\n" \
"s_and_b32 s77, s19, 0xffff\n" \
"s_addk_i32 s84, 0x7c00\n" \
"buffer_load_dwordx4 v27, s[72:75], s84 offen offset:3072 lds\n" \
"s_mov_b32 s78, 0x1e6000\n" \
"s_mov_b32 s79, 0x20000\n" \
"s_branch L_pgo_%=\n" \
"L_pret:\n" \
"s_setpc_b64 s[88:89]\n" \
"L_pgo_%=:\n" \
"s_mul_i32 s88, s33, 0xa000\n" \
"s_lshl_b32 s89, s3, 11\n" \
"s_and_b32 s89, s89, 0x1000\n" \
"s_add_u32 s88, s88, s89\n" \
"s_getpc_b64 s[86:87]\n" \
"L_hpa_%=:\n" \
"s_add_u32 s86, s86, L_pp1-L_hpa_%=\n" \
"s_addc_u32 s87, s87, 0\n" \
"s_add_u32 s86, s86, s88\n" \
"s_addc_u32 s87, s87, 0\n" \
"s_add_u32 m0, s30, 0x8000\n" \
"s_movk_i32 s84, 0x80\n" \
"buffer_load_dwordx4 v22, s[64:67], s84 offen offset:0 lds\n" \
"s_lshr_b32 s4, s2, 1\n" \
"s_and_b32 s4, s4, 0x7c\n" \
"v_bfe_u32 v212, v20, 4, 5\n" \
"s_movk_i32 s84, 0x1c80\n" \
"buffer_load_dwordx4 v23, s[64:67], s84 offen offset:1024 lds\n" \
"s_or_b32 s4, s4, s3\n" \
"v_min_u32_e32 v212, 26, v212\n" \
"v_mad_u64_u32 v[212:213], s[4:5], s4, 27, v[212:213]\n" \
"s_movk_i32 s84, 0x3880\n" \
"buffer_load_dwordx4 v22, s[64:67], s84 offen offset:2048 lds\n" \
"v_mul_u32_u24_e32 v213, 0xaaab, v212\n" \
"v_lshrrev_b32_e32 v213, 18, v213\n" \
"s_mov_b32 s4, 0x1fffffa\n" \
"s_movk_i32 s84, 0x5480\n" \
"buffer_load_dwordx4 v23, s[64:67], s84 offen offset:3072 lds\n" \
"v_mad_u64_u32 v[214:215], s[4:5], v213, s4, v[212:213]\n" \
"s_and_b32 s6, s2, 7\n" \
"v_mul_u32_u24_e32 v213, 27, v213\n" \
"s_mov_b64 exec, 0\n" \
"s_swappc_b64 s[88:89], s[86:87]\n" \
"s_mov_b64 exec, -1\n" \
"s_add_u32 s86, s86, 0x2000\n" \
"s_addc_u32 s87, s87, 0\n" \
"s_add_u32 m0, s31, 0x8000\n" \
"s_mov_b32 s84, 0x80000\n" \
"buffer_load_dwordx4 v26, s[68:71], s84 offen offset:0 lds\n" \
"v_lshrrev_b32_e32 v214, 1, v214\n" \
"v_and_b32_e32 v212, 1, v212\n" \
"v_mul_u32_u24_e32 v214, 9, v214\n" \
"s_addk_i32 s84, 0x7c00\n" \
"buffer_load_dwordx4 v26, s[68:71], s84 offen offset:1024 lds\n" \
"v_add3_u32 v212, v212, s6, v213\n" \
"v_add_lshl_u32 v212, v212, v214, 7\n" \
"v_mov_b32_e32 v213, 0\n" \
"s_mov_b64 exec, 0\n" \
"s_swappc_b64 s[88:89], s[86:87]\n" \
"s_mov_b64 exec, -1\n" \
"s_add_u32 s86, s86, 0x2000\n" \
"s_addc_u32 s87, s87, 0\n" \
"s_addk_i32 s84, 0x7c00\n" \
"buffer_load_dwordx4 v27, s[68:71], s84 offen offset:2048 lds\n" \
"v_lshl_add_u64 v[186:187], s[18:19], 0, v[212:213]\n" \
"s_lshl_b32 s35, s2, 10\n" \
"s_lshr_b32 s37, s2, 1\n" \
"s_addk_i32 s84, 0x7c00\n" \
"buffer_load_dwordx4 v27, s[68:71], s84 offen offset:3072 lds\n" \
"s_mul_i32 s37, s37, 0x24800\n" \
"s_and_b32 s35, s35, 0x400\n" \
"s_lshl_b32 s36, s3, 8\n" \
"s_mov_b64 exec, 0\n" \
"s_swappc_b64 s[88:89], s[86:87]\n" \
"s_mov_b64 exec, -1\n" \
"s_add_u32 s86, s86, 0x2000\n" \
"s_addc_u32 s87, s87, 0\n" \
"s_add_u32 m0, s30, 0x9000\n" \
"s_mov_b32 s84, 0x8080\n" \
"buffer_load_dwordx4 v22, s[64:67], s84 offen offset:0 lds\n" \
"s_or_b32 s35, s37, s35\n" \
"s_and_b32 s4, s2, 7\n" \
"s_lshr_b32 s6, s2, 5\n" \
"s_addk_i32 s84, 0x1c00\n" \
"buffer_load_dwordx4 v23, s[64:67], s84 offen offset:1024 lds\n" \
"s_lshl_b32 s34, s2, 5\n" \
"s_add_i32 s35, s35, s36\n" \
"s_lshl_b32 s7, s4, 10\n" \
"s_addk_i32 s84, 0x1c00\n" \
"buffer_load_dwordx4 v22, s[64:67], s84 offen offset:2048 lds\n" \
"s_and_b32 s34, s34, 0x300\n" \
"s_add_i32 s35, s35, 0x3004000\n" \
"s_lshl_b32 s37, s6, 13\n" \
"s_mov_b64 exec, 0\n" \
"s_swappc_b64 s[88:89], s[86:87]\n" \
"s_mov_b64 exec, -1\n" \
"s_andn2_b32 s85, 2, s3\n" \
"s_lshl_b32 s85, s85, 12\n" \
"s_add_u32 s86, s86, s85\n" \
"s_addc_u32 s87, s87, 0\n" \
"s_addk_i32 s84, 0x1c00\n" \
"buffer_load_dwordx4 v23, s[64:67], s84 offen offset:3072 lds\n" \
"s_or_b32 s34, s7, s34\n" \
"s_or_b32 s7, s37, s7\n" \
"v_mov_b32_e64 v213, s35\n" \
"s_add_u32 m0, s31, 0xc000\n" \
"s_mov_b32 s84, 0x80000\n" \
"buffer_load_dwordx4 v26, s[72:75], s84 offen offset:0 lds\n" \
"s_or_b32 s7, s7, s36\n" \
"s_lshr_b32 s5, s2, 3\n" \
"s_or_b32 s7, s7, 0x5c00000\n" \
"s_mov_b64 exec, 0\n" \
"s_swappc_b64 s[88:89], s[86:87]\n" \
"s_mov_b64 exec, -1\n" \
"s_addk_i32 s84, 0x7c00\n" \
"buffer_load_dwordx4 v26, s[72:75], s84 offen offset:1024 lds\n" \
"v_writelane_b32 v213, s7, 2\n" \
"s_mul_i32 s5, s5, 0x90000\n" \
"v_writelane_b32 v213, s7, 3\n" \
"s_addk_i32 s84, 0x7c00\n" \
"buffer_load_dwordx4 v27, s[72:75], s84 offen offset:2048 lds\n" \
"s_lshl_b32 s7, s3, 14\n" \
"s_lshl_b32 s4, s4, 16\n" \
"s_add_i32 s5, s5, s7\n" \
"s_addk_i32 s84, 0x7c00\n" \
"buffer_load_dwordx4 v27, s[72:75], s84 offen offset:3072 lds\n" \
"s_or_b32 s34, s34, 0x5be8000\n" \
"v_writelane_b32 v213, s34, 4\n" \
"s_add_i32 s4, s5, s4\n" \
"v_writelane_b32 v213, s34, 5\n" \
"s_add_i32 s4, s4, 0x4243800\n" \
"v_lshlrev_b32_e64 v212, 3, v20\n" \
"v_and_b32_e32 v212, 0x80, v212\n" \
"v_writelane_b32 v213, s4, 6\n" \
"s_mul_i32 s4, s6, 0xa00\n" \
"s_add_i32 s4, s4, 0x5bc0f80\n" \
"v_writelane_b32 v213, s4, 7\n" \
"v_add_u32_e32 v49, v213, v212\n" \
"s_mov_b32 m0, s32\n" \
"s_mov_b32 exec_lo, 0xffffff\n" \
"s_mov_b32 exec_hi, 0xfff0000\n" \
"global_load_lds_dword v[184:185], off\n" \
"s_mov_b64 exec, -1\n" \
"s_mov_b32 m0, s32\n" \
"s_mov_b64 exec, 0xff\n" \
"buffer_load_dword v49, s[80:83], 0 offen lds\n" \
"s_mov_b64 exec, -1\n" \
"s_mov_b32 s84, 0x5bc0000\n" \
"buffer_load_dword v180, off, s[80:83], s84 sc1\n" \
"s_cmp_lg_u32 s33, 0\n" \
"s_cbranch_scc1 L_w1_%=\n" \
"L_w0_%=:\n" \
"s_waitcnt vmcnt(19)\n" \
"s_barrier\n" \
"s_nop 0\n" \
"ds_read_b64_tr_b16 a[160:161], v32 offset:0\n" \
"ds_read_b64_tr_b16 a[162:163], v32 offset:1024\n" \
"ds_read_b128 a[128:131], v28 offset:16384\n" \
"ds_read_b64_tr_b16 a[224:225], v32 offset:8192\n" \
"ds_read_b64_tr_b16 a[226:227], v32 offset:9216\n" \
"ds_read_b128 a[192:195], v29 offset:16384\n" \
"ds_read_b128 a[132:135], v28 offset:18432\n" \
"ds_read_b128 a[196:199], v29 offset:18432\n" \
"ds_read_b64_tr_b16 a[164:165], v33 offset:0\n" \
"ds_read_b64_tr_b16 a[166:167], v33 offset:1024\n" \
"ds_read_b64_tr_b16 a[228:229], v33 offset:8192\n" \
"ds_read_b64_tr_b16 a[230:231], v33 offset:9216\n" \
"ds_read_b128 a[136:139], v28 offset:20480\n" \
"ds_read_b128 a[200:203], v29 offset:20480\n" \
"s_waitcnt lgkmcnt(11)\n" \
"v_mfma_f32_16x16x32_bf16 a[0:3], a[160:163], a[128:131], 0\n" \
"ds_read_b64_tr_b16 a[168:169], v34 offset:0\n" \
"s_lshr_b32 s4, s2, 1\n" \
"s_waitcnt lgkmcnt(9)\n" \
"v_mfma_f32_16x16x32_bf16 a[0:3], a[224:227], a[192:195], a[0:3]\n" \
"ds_read_b64_tr_b16 a[170:171], v34 offset:1024\n" \
"s_and_b32 s4, s4, 0x7c\n" \
"s_waitcnt lgkmcnt(9)\n" \
"v_mfma_f32_16x16x32_bf16 a[16:19], a[160:163], a[132:135], 0\n" \
"ds_read_b64_tr_b16 a[232:233], v34 offset:8192\n" \
"v_mov_b32_e32 v213, s13\n" \
"s_waitcnt lgkmcnt(9)\n" \
"v_mfma_f32_16x16x32_bf16 a[16:19], a[224:227], a[196:199], a[16:19]\n" \
"ds_read_b64_tr_b16 a[234:235], v34 offset:9216\n" \
"s_or_b32 s4, s4, s3\n" \
"s_waitcnt lgkmcnt(8)\n" \
"v_mfma_f32_16x16x32_bf16 a[20:23], a[164:167], a[132:135], 0\n" \
"ds_read_b128 a[140:143], v28 offset:22528\n" \
"v_lshrrev_b32_e32 v212, 4, v20\n" \
"s_waitcnt lgkmcnt(7)\n" \
"v_mfma_f32_16x16x32_bf16 a[20:23], a[228:231], a[196:199], a[20:23]\n" \
"ds_read_b128 a[204:207], v29 offset:22528\n" \
"v_mad_u64_u32 v[214:215], s[4:5], s4, 36, v[212:213]\n" \
"v_mfma_f32_16x16x32_bf16 a[4:7], a[164:167], a[128:131], 0\n" \
"ds_read_b64_tr_b16 a[172:173], v35 offset:0\n" \
"v_mov_b32_e64 v217, s9\n" \
"v_mfma_f32_16x16x32_bf16 a[4:7], a[228:231], a[192:195], a[4:7]\n" \
"ds_read_b64_tr_b16 a[174:175], v35 offset:1024\n" \
"s_movk_i32 s4, 0x240\n" \
"s_waitcnt lgkmcnt(9)\n" \
"v_mfma_f32_16x16x32_bf16 a[32:35], a[160:163], a[136:139], 0\n" \
"ds_read_b64_tr_b16 a[236:237], v35 offset:8192\n" \
"v_cmp_gt_u32_e32 vcc, s4, v20\n" \
"s_waitcnt lgkmcnt(9)\n" \
"v_mfma_f32_16x16x32_bf16 a[32:35], a[224:227], a[200:203], a[32:35]\n" \
"ds_read_b64_tr_b16 a[238:239], v35 offset:9216\n" \
"s_lshl_b32 s4, s2, 3\n" \
"v_mfma_f32_16x16x32_bf16 a[36:39], a[164:167], a[136:139], 0\n" \
"ds_read_b128 a[144:147], v28 offset:0\n" \
"s_and_b32 s4, s4, 56\n" \
"v_mfma_f32_16x16x32_bf16 a[36:39], a[228:231], a[200:203], a[36:39]\n" \
"ds_read_b128 a[208:211], v29 offset:0\n" \
"v_cndmask_b32_e32 v217, v217, v213, vcc\n" \
"s_waitcnt lgkmcnt(10)\n" \
"v_mfma_f32_16x16x32_bf16 a[40:43], a[168:171], a[136:139], 0\n" \
"ds_read_b128 a[148:151], v28 offset:2048\n" \
"v_lshlrev_b32_e32 v213, 3, v214\n" \
"s_waitcnt lgkmcnt(9)\n" \
"v_mfma_f32_16x16x32_bf16 a[40:43], a[232:235], a[200:203], a[40:43]\n" \
"ds_read_b128 a[212:215], v29 offset:2048\n" \
"v_and_b32_e32 v213, 0x1ffc0, v213\n" \
"v_mfma_f32_16x16x32_bf16 a[24:27], a[168:171], a[132:135], 0\n" \
"ds_read_b128 a[152:155], v28 offset:4096\n" \
"v_and_b32_e64 v214, 7, v214\n" \
"v_mfma_f32_16x16x32_bf16 a[24:27], a[232:235], a[196:199], a[24:27]\n" \
"ds_read_b128 a[216:219], v29 offset:4096\n" \
"v_or3_b32 v213, v214, s4, v213\n" \
"v_mfma_f32_16x16x32_bf16 a[8:11], a[168:171], a[128:131], 0\n" \
"ds_read_b128 a[156:159], v28 offset:6144\n" \
"s_lshl_b32 s4, s2, 2\n" \
"v_mfma_f32_16x16x32_bf16 a[8:11], a[232:235], a[192:195], a[8:11]\n" \
"ds_read_b128 a[220:223], v29 offset:6144\n" \
"s_mov_b32 m0, s32\n" \
"s_waitcnt lgkmcnt(13)\n" \
"v_mfma_f32_16x16x32_bf16 a[48:51], a[160:163], a[140:143], 0\n" \
"s_mov_b32 exec_lo, 0xff000000\n" \
"s_mov_b32 exec_hi, 0x0\n" \
"global_load_lds_dword v[184:185], off\n" \
"s_mov_b64 exec, -1\n" \
"s_waitcnt lgkmcnt(12)\n" \
"v_mfma_f32_16x16x32_bf16 a[48:51], a[224:227], a[204:207], a[48:51]\n" \
"ds_read_b64_tr_b16 a[176:177], v36 offset:0\n" \
"s_or_b32 s4, s4, s3\n" \
"v_mfma_f32_16x16x32_bf16 a[52:55], a[164:167], a[140:143], 0\n" \
"ds_read_b64_tr_b16 a[178:179], v36 offset:1024\n" \
"s_mul_i32 s4, s4, 18\n" \
"v_mfma_f32_16x16x32_bf16 a[52:55], a[228:231], a[204:207], a[52:55]\n" \
"s_mov_b32 m0, s32\n" \
"ds_read_b64_tr_b16 a[240:241], v36 offset:8192\n" \
"v_mfma_f32_16x16x32_bf16 a[56:59], a[168:171], a[140:143], 0\n" \
"s_mov_b32 exec_lo, 0xffff\n" \
"s_mov_b32 exec_hi, 0x0\n" \
"global_load_lds_dword v[186:187], off\n" \
"s_mov_b64 exec, -1\n" \
"v_mfma_f32_16x16x32_bf16 a[56:59], a[232:235], a[204:207], a[56:59]\n" \
"ds_read_b64_tr_b16 a[242:243], v36 offset:9216\n" \
"v_min_u32_e32 v212, 53, v212\n" \
"s_waitcnt lgkmcnt(14)\n" \
"v_mfma_f32_16x16x32_bf16 a[60:63], a[172:175], a[140:143], 0\n" \
"ds_read_b64_tr_b16 a[180:181], v37 offset:0\n" \
"s_sub_i32 s4, s4, 36\n" \
"s_waitcnt lgkmcnt(13)\n" \
"v_mfma_f32_16x16x32_bf16 a[60:63], a[236:239], a[204:207], a[60:63]\n" \
"ds_read_b64_tr_b16 a[182:183], v37 offset:1024\n" \
"v_add_u32_e32 v212, s4, v212\n" \
"v_mfma_f32_16x16x32_bf16 a[44:47], a[172:175], a[136:139], 0\n" \
"ds_read_b64_tr_b16 a[244:245], v37 offset:8192\n" \
"v_mov_b32_e64 v215, s12\n" \
"v_mfma_f32_16x16x32_bf16 a[44:47], a[236:239], a[200:203], a[44:47]\n" \
"ds_read_b64_tr_b16 a[246:247], v37 offset:9216\n" \
"v_mov_b32_e64 v216, s8\n" \
"v_mfma_f32_16x16x32_bf16 a[28:31], a[172:175], a[132:135], 0\n" \
"ds_read_b64_tr_b16 a[184:185], v38 offset:0\n" \
"v_cndmask_b32_e64 v212, v212, v213, vcc\n" \
"v_mfma_f32_16x16x32_bf16 a[28:31], a[236:239], a[196:199], a[28:31]\n" \
"ds_read_b64_tr_b16 a[186:187], v38 offset:1024\n" \
"v_cndmask_b32_e64 v216, v216, v215, vcc\n" \
"v_mfma_f32_16x16x32_bf16 a[12:15], a[172:175], a[128:131], 0\n" \
"v_lshlrev_b32_e32 v212, 7, v212\n" \
"v_mov_b32_e32 v213, 0\n" \
"v_mfma_f32_16x16x32_bf16 a[12:15], a[236:239], a[192:195], a[12:15]\n" \
"v_lshl_add_u64 v[188:189], v[216:217], 0, v[212:213]\n" \
"s_movk_i32 s5, 0xd80\n" \
"s_waitcnt lgkmcnt(15)\n" \
"v_mfma_f32_16x16x32_bf16 a[76:79], a[172:175], a[144:147], 0\n" \
"ds_read_b64_tr_b16 a[248:249], v38 offset:8192\n" \
"s_lshr_b32 s6, s2, 5\n" \
"v_mfma_f32_16x16x32_bf16 a[76:79], a[236:239], a[208:211], a[76:79]\n" \
"v_lshrrev_b32_e32 v212, 4, v20\n" \
"v_lshl_or_b32 v215, s3, 6, v212\n" \
"v_mfma_f32_16x16x32_bf16 a[72:75], a[168:171], a[144:147], 0\n" \
"v_mul_u32_u24_e32 v216, 0x2aab, v215\n" \
"v_lshrrev_b32_e64 v216, 16, v216\n" \
"v_mfma_f32_16x16x32_bf16 a[72:75], a[232:235], a[208:211], a[72:75]\n" \
"ds_read_b64_tr_b16 a[250:251], v38 offset:9216\n" \
"v_mad_i32_i24 v217, v216, -6, v215\n" \
"v_mfma_f32_16x16x32_bf16 a[68:71], a[164:167], a[144:147], 0\n" \
"v_add_u32_e32 v218, 0xfffffe50, v215\n" \
"s_mul_i32 s35, s6, 0x48\n" \
"v_mfma_f32_16x16x32_bf16 a[68:71], a[228:231], a[208:211], a[68:71]\n" \
"v_lshrrev_b32_e32 v219, 1, v217\n" \
"v_add_u32_e32 v218, s35, v216\n" \
"v_mfma_f32_16x16x32_bf16 a[64:67], a[160:163], a[144:147], 0\n" \
"v_mul_u32_u24_e32 v219, 0x480, v219\n" \
"v_and_b32_e64 v214, 16, v20\n" \
"v_mfma_f32_16x16x32_bf16 a[64:67], a[224:227], a[208:211], a[64:67]\n" \
"ds_read_b64_tr_b16 a[188:189], v39 offset:0\n" \
"v_mad_u32_u24 v219, v218, s5, v219\n" \
"v_mfma_f32_16x16x32_bf16 a[80:83], a[160:163], a[148:151], 0\n" \
"v_or_b32_e32 v192, v219, v214\n" \
"v_mov_b32_e32 v220, 2.0\n" \
"s_waitcnt lgkmcnt(15)\n" \
"v_mfma_f32_16x16x32_bf16 a[80:83], a[224:227], a[212:215], a[80:83]\n" \
"v_mov_b32_e32 v222, 7\n" \
"v_lshlrev_b32_e64 v220, 7, v218\n" \
"v_mfma_f32_16x16x32_bf16 a[84:87], a[164:167], a[148:151], 0\n" \
"ds_read_b64_tr_b16 a[190:191], v39 offset:1024\n" \
"v_lshrrev_b32_e64 v218, 1, v218\n" \
"v_mfma_f32_16x16x32_bf16 a[84:87], a[228:231], a[212:215], a[84:87]\n" \
"v_and_b32_e32 v218, 4, v218\n" \
"v_and_b32_e32 v216, 2, v216\n" \
"v_mfma_f32_16x16x32_bf16 a[88:91], a[168:171], a[148:151], 0\n" \
"s_mul_i32 s7, s6, 0x2400\n" \
"s_sub_i32 s34, 0x22000, s7\n" \
"v_mfma_f32_16x16x32_bf16 a[88:91], a[232:235], a[212:215], a[88:91]\n" \
"ds_read_b64_tr_b16 a[252:253], v39 offset:8192\n" \
"v_bitop3_b32 v216, v217, v218, v216 bitop3:0x1e\n" \
"v_mfma_f32_16x16x32_bf16 a[92:95], a[172:175], a[148:151], 0\n" \
"v_or_b32_e32 v213, 0x24400, v20\n" \
"v_lshlrev_b32_e64 v216, 4, v216\n" \
"v_mfma_f32_16x16x32_bf16 a[92:95], a[236:239], a[212:215], a[92:95]\n" \
"v_add3_u32 v196, v220, s34, v216\n" \
"v_mov_b32_e64 v216, v213\n" \
"v_mfma_f32_16x16x32_bf16 a[108:111], a[172:175], a[152:155], 0\n" \
"ds_read_b64_tr_b16 a[254:255], v39 offset:9216\n" \
"s_movk_i32 s36, 0x1b0\n" \
"v_mfma_f32_16x16x32_bf16 a[108:111], a[236:239], a[216:219], a[108:111]\n" \
"v_or_b32_e32 v216, 0x100, v215\n" \
"v_mul_u32_u24_e32 v217, 0x2aab, v216\n" \
"v_mfma_f32_16x16x32_bf16 a[104:107], a[168:171], a[152:155], 0\n" \
"v_add_u32_e32 v215, 0xffffff50, v215\n" \
"v_lshrrev_b32_e32 v217, 16, v217\n" \
"v_mfma_f32_16x16x32_bf16 a[104:107], a[232:235], a[216:219], a[104:107]\n" \
"v_mov_b32_e64 v220, v215\n" \
"v_mad_i32_i24 v218, v217, -6, v216\n" \
"v_mfma_f32_16x16x32_bf16 a[100:103], a[164:167], a[152:155], 0\n" \
"v_cmp_gt_u32_e32 vcc, s36, v216\n" \
"v_mov_b32_e32 v221, 7\n" \
"v_mfma_f32_16x16x32_bf16 a[100:103], a[228:231], a[216:219], a[100:103]\n" \
"v_lshrrev_b32_e32 v222, 1, v218\n" \
"v_cndmask_b32_e32 v217, v220, v217, vcc\n" \
"v_mfma_f32_16x16x32_bf16 a[96:99], a[160:163], a[152:155], 0\n" \
"s_movk_i32 s4, 0x48\n" \
"v_add_u32_e32 v220, s35, v217\n" \
"v_mfma_f32_16x16x32_bf16 a[96:99], a[224:227], a[216:219], a[96:99]\n" \
"v_mul_u32_u24_e32 v222, 0x480, v222\n" \
"v_mad_u32_u24 v222, v220, s5, v222\n" \
"s_waitcnt lgkmcnt(15)\n" \
"v_mfma_f32_16x16x32_bf16 a[112:115], a[160:163], a[156:159], 0\n" \
"v_cmp_gt_u32_e64 s[4:5], s4, v215\n" \
"v_cndmask_b32_e32 v215, v221, v218, vcc\n" \
"v_mfma_f32_16x16x32_bf16 a[112:115], a[224:227], a[220:223], a[112:115]\n" \
"v_lshrrev_b32_e32 v218, 1, v220\n" \
"v_or_b32_e32 v214, v222, v214\n" \
"v_mfma_f32_16x16x32_bf16 a[116:119], a[164:167], a[156:159], 0\n" \
"v_mov_b32_e32 v222, 2.0\n" \
"v_and_b32_e32 v218, 4, v218\n" \
"v_mfma_f32_16x16x32_bf16 a[116:119], a[228:231], a[220:223], a[116:119]\n" \
"v_and_b32_e64 v217, 2, v217\n" \
"v_bitop3_b32 v215, v215, v218, v217 bitop3:0x1e\n" \
"v_mfma_f32_16x16x32_bf16 a[120:123], a[168:171], a[156:159], 0\n" \
"v_lshlrev_b32_e32 v223, 1, v220\n" \
"v_mov_b32_e32 v224, 2.0\n" \
"v_mfma_f32_16x16x32_bf16 a[120:123], a[232:235], a[220:223], a[120:123]\n" \
"v_cndmask_b32_e32 v193, v222, v214, vcc\n" \
"v_lshlrev_b32_e32 v214, 7, v220\n" \
"v_mfma_f32_16x16x32_bf16 a[124:127], a[172:175], a[156:159], 0\n" \
"v_lshlrev_b32_e32 v215, 4, v215\n" \
"s_mul_i32 s39, s3, 18\n" \
"v_mfma_f32_16x16x32_bf16 a[124:127], a[236:239], a[220:223], a[124:127]\n" \
"s_waitcnt vmcnt(5) lgkmcnt(0)\n" \
"s_barrier\n" \
"v_mfma_f32_16x16x32_bf16 v[52:55], a[176:179], a[128:131], 0\n" \
"s_add_u32 m0, s30, 0x0\n" \
"s_movk_i32 s84, 0x100\n" \
"v_mfma_f32_16x16x32_bf16 v[52:55], a[240:243], a[192:195], v[52:55]\n" \
"buffer_load_dwordx4 v22, s[64:67], s84 offen offset:0 lds\n" \
"v_mfma_f32_16x16x32_bf16 v[56:59], a[180:183], a[128:131], 0\n" \
"ds_read_b64_tr_b16 a[160:161], v32 offset:32768\n" \
"v_add3_u32 v214, v214, s34, v215\n" \
"v_mfma_f32_16x16x32_bf16 v[56:59], a[244:247], a[192:195], v[56:59]\n" \
"s_movk_i32 s84, 0x1d00\n" \
"v_mfma_f32_16x16x32_bf16 v[60:63], a[184:187], a[128:131], 0\n" \
"buffer_load_dwordx4 v23, s[64:67], s84 offen offset:1024 lds\n" \
"v_mfma_f32_16x16x32_bf16 v[60:63], a[248:251], a[192:195], v[60:63]\n" \
"ds_read_b64_tr_b16 a[162:163], v32 offset:33792\n" \
"v_cndmask_b32_e64 v195, v224, v223, s[4:5]\n" \
"v_mfma_f32_16x16x32_bf16 v[64:67], a[188:191], a[128:131], 0\n" \
"ds_read_b64_tr_b16 a[224:225], v32 offset:40960\n" \
"s_movk_i32 s4, 0x1f8\n" \
"v_mfma_f32_16x16x32_bf16 v[64:67], a[252:255], a[192:195], v[64:67]\n" \
"ds_read_b128 a[128:131], v28 offset:49152\n" \
"v_mov_b32_e64 v215, v213\n" \
"v_mfma_f32_16x16x32_bf16 v[80:83], a[188:191], a[132:135], 0\n" \
"ds_read_b128 a[192:195], v29 offset:49152\n" \
"s_add_i32 s35, s35, s39\n" \
"v_mfma_f32_16x16x32_bf16 v[80:83], a[252:255], a[196:199], v[80:83]\n" \
"s_movk_i32 s84, 0x3900\n" \
"v_mfma_f32_16x16x32_bf16 v[76:79], a[184:187], a[132:135], 0\n" \
"buffer_load_dwordx4 v22, s[64:67], s84 offen offset:2048 lds\n" \
"v_mfma_f32_16x16x32_bf16 v[76:79], a[248:251], a[196:199], v[76:79]\n" \
"s_movk_i32 s84, 0x5500\n" \
"v_mfma_f32_16x16x32_bf16 v[72:75], a[180:183], a[132:135], 0\n" \
"buffer_load_dwordx4 v23, s[64:67], s84 offen offset:3072 lds\n" \
"v_mfma_f32_16x16x32_bf16 v[72:75], a[244:247], a[196:199], v[72:75]\n" \
"s_add_u32 m0, s31, 0x0\n" \
"s_mov_b32 s84, 0x100000\n" \
"v_mfma_f32_16x16x32_bf16 v[68:71], a[176:179], a[132:135], 0\n" \
"buffer_load_dwordx4 v26, s[68:71], s84 offen offset:0 lds\n" \
"v_mfma_f32_16x16x32_bf16 v[68:71], a[240:243], a[196:199], v[68:71]\n" \
"ds_read_b128 a[132:135], v28 offset:51200\n" \
"v_cmp_gt_u32_e64 vcc, s4, v216\n" \
"v_mfma_f32_16x16x32_bf16 v[84:87], a[176:179], a[136:139], 0\n" \
"ds_read_b128 a[196:199], v29 offset:51200\n" \
"v_add_u32_e64 v212, s35, v212\n" \
"v_mfma_f32_16x16x32_bf16 v[84:87], a[240:243], a[200:203], v[84:87]\n" \
"s_addk_i32 s84, 0x7c00\n" \
"v_mfma_f32_16x16x32_bf16 v[88:91], a[180:183], a[136:139], 0\n" \
"buffer_load_dwordx4 v26, s[68:71], s84 offen offset:1024 lds\n" \
"v_mfma_f32_16x16x32_bf16 v[88:91], a[244:247], a[200:203], v[88:91]\n" \
"ds_read_b64_tr_b16 a[226:227], v32 offset:41984\n" \
"v_mov_b32_e32 v219, 2.0\n" \
"v_mfma_f32_16x16x32_bf16 v[92:95], a[184:187], a[136:139], 0\n" \
"s_addk_i32 s84, 0x7c00\n" \
"v_mfma_f32_16x16x32_bf16 v[92:95], a[248:251], a[200:203], v[92:95]\n" \
"buffer_load_dwordx4 v27, s[68:71], s84 offen offset:2048 lds\n" \
"v_mfma_f32_16x16x32_bf16 v[96:99], a[188:191], a[136:139], 0\n" \
"ds_read_b64_tr_b16 a[164:165], v33 offset:32768\n" \
"v_cndmask_b32_e32 v197, v215, v214, vcc\n" \
"v_mfma_f32_16x16x32_bf16 v[96:99], a[252:255], a[200:203], v[96:99]\n" \
"ds_read_b128 a[136:139], v28 offset:53248\n" \
"s_movk_i32 s35, 0x120\n" \
"v_mfma_f32_16x16x32_bf16 v[112:115], a[188:191], a[140:143], 0\n" \
"ds_read_b128 a[200:203], v29 offset:53248\n" \
"v_mul_u32_u24_e32 v214, 0xd80, v212\n" \
"v_mfma_f32_16x16x32_bf16 v[112:115], a[252:255], a[204:207], v[112:115]\n" \
"s_addk_i32 s84, 0x7c00\n" \
"v_mfma_f32_16x16x32_bf16 v[108:111], a[184:187], a[140:143], 0\n" \
"buffer_load_dwordx4 v27, s[68:71], s84 offen offset:3072 lds\n" \
"v_mfma_f32_16x16x32_bf16 v[108:111], a[248:251], a[204:207], v[108:111]\n" \
"s_add_u32 m0, s30, 0x1000\n" \
"s_mov_b32 s84, 0x8100\n" \
"v_mfma_f32_16x16x32_bf16 v[104:107], a[180:183], a[140:143], 0\n" \
"buffer_load_dwordx4 v22, s[64:67], s84 offen offset:0 lds\n" \
"v_mfma_f32_16x16x32_bf16 v[104:107], a[244:247], a[204:207], v[104:107]\n" \
"ds_read_b64_tr_b16 a[166:167], v33 offset:33792\n" \
"v_cmp_gt_u32_e64 vcc, s35, v20\n" \
"v_mfma_f32_16x16x32_bf16 v[100:103], a[176:179], a[140:143], 0\n" \
"ds_read_b64_tr_b16 a[228:229], v33 offset:40960\n" \
"v_lshrrev_b32_e64 v215, 1, v212\n" \
"v_mfma_f32_16x16x32_bf16 v[100:103], a[240:243], a[204:207], v[100:103]\n" \
"ds_read_b128 a[140:143], v28 offset:55296\n" \
"s_and_b32 s4, s2, 7\n" \
"v_mfma_f32_16x16x32_bf16 v[116:119], a[176:179], a[144:147], 0\n" \
"ds_read_b128 a[204:207], v29 offset:55296\n" \
"v_cndmask_b32_e32 v194, v219, v214, vcc\n" \
"v_mfma_f32_16x16x32_bf16 v[116:119], a[240:243], a[208:211], v[116:119]\n" \
"s_addk_i32 s84, 0x1c00\n" \
"v_mfma_f32_16x16x32_bf16 v[120:123], a[180:183], a[144:147], 0\n" \
"buffer_load_dwordx4 v23, s[64:67], s84 offen offset:1024 lds\n" \
"v_mfma_f32_16x16x32_bf16 v[120:123], a[244:247], a[208:211], v[120:123]\n" \
"s_addk_i32 s84, 0x1c00\n" \
"v_mfma_f32_16x16x32_bf16 v[124:127], a[184:187], a[144:147], 0\n" \
"buffer_load_dwordx4 v22, s[64:67], s84 offen offset:2048 lds\n" \
"v_mfma_f32_16x16x32_bf16 v[124:127], a[248:251], a[208:211], v[124:127]\n" \
"s_addk_i32 s84, 0x1c00\n" \
".fill (L_pb + 3808 - .) / 4, 4, 0xBF800000\n" \
"L_pp1:\n" \
"s_cbranch_execz L_pret\n" \
"v_mfma_f32_16x16x32_bf16 v[128:131], a[188:191], a[144:147], 0\n" \
"buffer_load_dwordx4 v23, s[64:67], s84 offen offset:3072 lds\n" \
"v_mfma_f32_16x16x32_bf16 v[128:131], a[252:255], a[208:211], v[128:131]\n" \
"ds_read_b128 a[144:147], v28 offset:32768\n" \
"v_lshlrev_b32_e32 v214, 7, v212\n" \
"v_mfma_f32_16x16x32_bf16 v[144:147], a[188:191], a[148:151], 0\n" \
"ds_read_b128 a[208:211], v29 offset:32768\n" \
"v_lshrrev_b32_e64 v212, 2, v212\n" \
"v_mfma_f32_16x16x32_bf16 v[144:147], a[252:255], a[212:215], v[144:147]\n" \
"s_add_u32 m0, s31, 0x4000\n" \
"s_mov_b32 s84, 0x100000\n" \
"v_mfma_f32_16x16x32_bf16 v[140:143], a[184:187], a[148:151], 0\n" \
"buffer_load_dwordx4 v26, s[72:75], s84 offen offset:0 lds\n" \
"v_mfma_f32_16x16x32_bf16 v[140:143], a[248:251], a[212:215], v[140:143]\n" \
"ds_read_b64_tr_b16 a[230:231], v33 offset:41984\n" \
"v_and_b32_e64 v212, 2, v212\n" \
"v_mfma_f32_16x16x32_bf16 v[136:139], a[180:183], a[148:151], 0\n" \
"s_addk_i32 s84, 0x7c00\n" \
"v_mfma_f32_16x16x32_bf16 v[136:139], a[244:247], a[212:215], v[136:139]\n" \
"buffer_load_dwordx4 v26, s[72:75], s84 offen offset:1024 lds\n" \
"v_mfma_f32_16x16x32_bf16 v[132:135], a[176:179], a[148:151], 0\n" \
"ds_read_b64_tr_b16 a[168:169], v34 offset:32768\n" \
"v_and_or_b32 v212, v215, 1, v212\n" \
"v_mfma_f32_16x16x32_bf16 v[132:135], a[240:243], a[212:215], v[132:135]\n" \
"ds_read_b128 a[148:151], v28 offset:34816\n" \
"s_lshl_b32 s5, s4, 2\n" \
"v_mfma_f32_16x16x32_bf16 v[148:151], a[176:179], a[152:155], 0\n" \
"ds_read_b128 a[212:215], v29 offset:34816\n" \
"s_bfe_u32 s38, s2, 0x20003\n" \
"v_mfma_f32_16x16x32_bf16 v[148:151], a[240:243], a[216:219], v[148:151]\n" \
"ds_read_b64_tr_b16 a[170:171], v34 offset:33792\n" \
"v_lshlrev_b32_e64 v212, 5, v212\n" \
"v_mfma_f32_16x16x32_bf16 v[152:155], a[180:183], a[152:155], 0\n" \
"ds_read_b64_tr_b16 a[232:233], v34 offset:40960\n" \
"s_movk_i32 s35, 0x60\n" \
"v_mfma_f32_16x16x32_bf16 v[152:155], a[244:247], a[216:219], v[152:155]\n" \
"ds_read_b64_tr_b16 a[234:235], v34 offset:41984\n" \
"s_or_b32 s5, s5, s38\n" \
"v_mfma_f32_16x16x32_bf16 v[156:159], a[184:187], a[152:155], 0\n" \
"ds_read_b64_tr_b16 a[172:173], v35 offset:32768\n" \
"v_bitop3_b32 v212, v212, s35, v214 bitop3:0x36\n" \
"v_mfma_f32_16x16x32_bf16 v[156:159], a[248:251], a[216:219], v[156:159]\n" \
"ds_read_b64_tr_b16 a[174:175], v35 offset:33792\n" \
"s_lshr_b32 s36, s2, 3\n" \
"v_mfma_f32_16x16x32_bf16 v[160:163], a[188:191], a[152:155], 0\n" \
"ds_read_b64_tr_b16 a[236:237], v35 offset:40960\n" \
"v_add_u32_e32 v212, s34, v212\n" \
"v_mfma_f32_16x16x32_bf16 v[160:163], a[252:255], a[216:219], v[160:163]\n" \
"ds_read_b128 a[152:155], v28 offset:36864\n" \
"s_mul_i32 s34, s5, 0x12000\n" \
"v_mfma_f32_16x16x32_bf16 v[176:179], a[188:191], a[156:159], 0\n" \
"ds_read_b128 a[216:219], v29 offset:36864\n" \
"s_or_b32 s40, s34, 0x6000000\n" \
"v_mfma_f32_16x16x32_bf16 v[176:179], a[252:255], a[220:223], v[176:179]\n" \
"ds_read_b64_tr_b16 a[238:239], v35 offset:41984\n" \
"s_mul_i32 s34, s36, 0x90000\n" \
"v_mfma_f32_16x16x32_bf16 v[172:175], a[184:187], a[156:159], 0\n" \
"s_lshl_b32 s36, s3, 14\n" \
"s_add_i32 s34, s34, s36\n" \
"v_mfma_f32_16x16x32_bf16 v[172:175], a[248:251], a[220:223], v[172:175]\n" \
"s_lshl_b32 s38, s5, 8\n" \
"s_add_i32 s41, s34, 0x4240000\n" \
"v_mfma_f32_16x16x32_bf16 v[168:171], a[180:183], a[156:159], 0\n" \
"s_mul_i32 s34, s5, 36\n" \
"s_add_i32 s5, s7, s40\n" \
"v_mfma_f32_16x16x32_bf16 v[168:171], a[244:247], a[220:223], v[168:171]\n" \
"s_addk_i32 s5, 0x2000\n" \
"s_mov_b32 s37, 2.0\n" \
"v_mfma_f32_16x16x32_bf16 v[164:167], a[176:179], a[156:159], 0\n" \
"s_or_b32 s38, s38, 0x5be8000\n" \
"v_mfma_f32_16x16x32_bf16 v[164:167], a[240:243], a[220:223], v[164:167]\n" \
"ds_read_b128 a[156:159], v28 offset:38912\n" \
"ds_read_b128 a[220:223], v29 offset:38912\n" \
"s_waitcnt lgkmcnt(15)\n" \
"v_mfma_f32_16x16x32_bf16 a[0:3], a[160:163], a[128:131], a[0:3]\n" \
"s_addk_i32 s84, 0x7c00\n" \
"v_mfma_f32_16x16x32_bf16 a[0:3], a[224:227], a[192:195], a[0:3]\n" \
"buffer_load_dwordx4 v27, s[72:75], s84 offen offset:2048 lds\n" \
"v_mfma_f32_16x16x32_bf16 a[4:7], a[164:167], a[128:131], a[4:7]\n" \
"ds_read_b64_tr_b16 a[176:177], v36 offset:32768\n" \
"s_waitcnt lgkmcnt(15)\n" \
"v_mfma_f32_16x16x32_bf16 a[4:7], a[228:231], a[192:195], a[4:7]\n" \
"s_addk_i32 s84, 0x7c00\n" \
"s_waitcnt lgkmcnt(11)\n" \
"v_mfma_f32_16x16x32_bf16 a[8:11], a[168:171], a[128:131], a[8:11]\n" \
"buffer_load_dwordx4 v27, s[72:75], s84 offen offset:3072 lds\n" \
"s_waitcnt lgkmcnt(9)\n" \
"v_mfma_f32_16x16x32_bf16 a[8:11], a[232:235], a[192:195], a[8:11]\n" \
"ds_read_b64_tr_b16 a[178:179], v36 offset:33792\n" \
"s_waitcnt lgkmcnt(8)\n" \
"v_mfma_f32_16x16x32_bf16 a[12:15], a[172:175], a[128:131], a[12:15]\n" \
"ds_read_b64_tr_b16 a[240:241], v36 offset:40960\n" \
"s_waitcnt lgkmcnt(5)\n" \
"v_mfma_f32_16x16x32_bf16 a[12:15], a[236:239], a[192:195], a[12:15]\n" \
"s_cmp_lg_u32 s3, 0\n" \
"s_cselect_b32 s36, s37, s5\n" \
"v_mfma_f32_16x16x32_bf16 a[28:31], a[172:175], a[132:135], a[28:31]\n" \
"s_lshl_b32 s5, s6, 2\n" \
"s_or_b32 s5, s38, s5\n" \
"v_mfma_f32_16x16x32_bf16 a[28:31], a[236:239], a[196:199], a[28:31]\n" \
"ds_read_b64_tr_b16 a[242:243], v36 offset:41984\n" \
"s_lshl_b32 s35, s3, 10\n" \
"v_mfma_f32_16x16x32_bf16 a[24:27], a[168:171], a[132:135], a[24:27]\n" \
"s_cmpk_lt_u32 s3, 3\n" \
"s_cselect_b32 s37, s37, s5\n" \
"v_mfma_f32_16x16x32_bf16 a[24:27], a[232:235], a[196:199], a[24:27]\n" \
"s_lshl_b32 s5, s4, 16\n" \
"s_lshl_b32 s4, s4, 11\n" \
"v_mfma_f32_16x16x32_bf16 a[20:23], a[164:167], a[132:135], a[20:23]\n" \
"v_cndmask_b32_e32 v198, v213, v212, vcc\n" \
"v_or_b32_e32 v212, s35, v20\n" \
"v_mfma_f32_16x16x32_bf16 a[20:23], a[228:231], a[196:199], a[20:23]\n" \
"ds_read_b64_tr_b16 a[180:181], v37 offset:32768\n" \
"s_or_b32 s39, s40, s35\n" \
"v_mfma_f32_16x16x32_bf16 a[16:19], a[160:163], a[132:135], a[16:19]\n" \
"s_or_b32 s55, s41, s4\n" \
"v_or_b32_e32 v199, 0x22000, v212\n" \
"v_mfma_f32_16x16x32_bf16 a[16:19], a[224:227], a[196:199], a[16:19]\n" \
"s_add_i32 s35, s39, s7\n" \
"s_add_i32 s54, s41, s5\n" \
"v_mfma_f32_16x16x32_bf16 a[32:35], a[160:163], a[136:139], a[32:35]\n" \
"s_mov_b32 m0, s32\n" \
"ds_read_b64_tr_b16 a[182:183], v37 offset:33792\n" \
"v_mfma_f32_16x16x32_bf16 a[32:35], a[224:227], a[200:203], a[32:35]\n" \
"s_mov_b32 exec_lo, 0x0\n" \
"s_mov_b32 exec_hi, 0xff\n" \
"global_load_lds_dword v[184:185], off\n" \
"s_mov_b64 exec, -1\n" \
"v_mfma_f32_16x16x32_bf16 a[36:39], a[164:167], a[136:139], a[36:39]\n" \
"s_add_i32 s55, s55, 0x80000\n" \
"v_mov_b32_e32 v215, 0xffff\n" \
"v_mfma_f32_16x16x32_bf16 a[36:39], a[228:231], a[200:203], a[36:39]\n" \
"ds_read_b64_tr_b16 a[244:245], v37 offset:40960\n" \
"s_and_b32 s4, s3, 1\n" \
"v_mfma_f32_16x16x32_bf16 a[40:43], a[168:171], a[136:139], a[40:43]\n" \
"s_lshl_b32 s5, s4, 4\n" \
"v_lshrrev_b32_e32 v212, 4, v20\n" \
"v_mfma_f32_16x16x32_bf16 a[40:43], a[232:235], a[200:203], a[40:43]\n" \
"s_mov_b32 m0, s32\n" \
"v_lshrrev_b32_e32 v218, 6, v20\n" \
"v_mfma_f32_16x16x32_bf16 a[44:47], a[172:175], a[136:139], a[44:47]\n" \
"s_mov_b32 exec_lo, 0xffff0000\n" \
"s_mov_b32 exec_hi, 0x0\n" \
"global_load_lds_dword v[186:187], off\n" \
"s_mov_b64 exec, -1\n" \
"v_mfma_f32_16x16x32_bf16 a[44:47], a[236:239], a[200:203], a[44:47]\n" \
"ds_read_b64_tr_b16 a[246:247], v37 offset:41984\n" \
"v_and_b32_e64 v219, 12, v218\n" \
"v_mfma_f32_16x16x32_bf16 a[60:63], a[172:175], a[140:143], a[60:63]\n" \
"v_and_or_b32 v214, v212, 15, s5\n" \
"v_cmp_gt_u32_e64 vcc, v219, v214\n" \
"v_mfma_f32_16x16x32_bf16 a[60:63], a[236:239], a[204:207], a[60:63]\n" \
"v_mov_b32_e32 v220, 0xffff0000\n" \
"s_lshl_b32 s5, s2, 2\n" \
"v_mfma_f32_16x16x32_bf16 a[56:59], a[168:171], a[140:143], a[56:59]\n" \
"ds_read_b64_tr_b16 a[184:185], v38 offset:32768\n" \
"v_cndmask_b32_e64 v217, v215, 0, vcc\n" \
"v_mfma_f32_16x16x32_bf16 a[56:59], a[232:235], a[204:207], a[56:59]\n" \
"v_cmp_lt_u32_e32 vcc, v219, v214\n" \
"v_mov_b32_e64 v213, s23\n" \
"v_mfma_f32_16x16x32_bf16 a[52:55], a[164:167], a[140:143], a[52:55]\n" \
"s_or_b32 s5, s5, s3\n" \
"v_cndmask_b32_e32 v221, 0, v220, vcc\n" \
"v_mfma_f32_16x16x32_bf16 a[52:55], a[228:231], a[204:207], a[52:55]\n" \
"ds_read_b64_tr_b16 a[186:187], v38 offset:33792\n" \
"v_or_b32_e64 v12, v217, v221\n" \
"v_mfma_f32_16x16x32_bf16 a[48:51], a[160:163], a[140:143], a[48:51]\n" \
"v_or_b32_e32 v217, 2, v219\n" \
"v_cmp_gt_u32_e32 vcc, v217, v214\n" \
"v_mfma_f32_16x16x32_bf16 a[48:51], a[224:227], a[204:207], a[48:51]\n" \
"v_mov_b32_e32 v216, s25\n" \
"s_movk_i32 s40, 0xf400\n" \
"v_mfma_f32_16x16x32_bf16 a[64:67], a[160:163], a[144:147], a[64:67]\n" \
"ds_read_b64_tr_b16 a[248:249], v38 offset:40960\n" \
"v_cndmask_b32_e64 v221, v215, 0, vcc\n" \
"v_mfma_f32_16x16x32_bf16 a[64:67], a[224:227], a[208:211], a[64:67]\n" \
"v_cmp_lt_u32_e32 vcc, v217, v214\n" \
"s_lshl_b32 s4, s4, 6\n" \
"v_mfma_f32_16x16x32_bf16 a[68:71], a[164:167], a[144:147], a[68:71]\n" \
"v_cndmask_b32_e32 v217, 0, v220, vcc\n" \
"v_or_b32_e32 v13, v221, v217\n" \
"v_mfma_f32_16x16x32_bf16 a[68:71], a[228:231], a[208:211], a[68:71]\n" \
"ds_read_b64_tr_b16 a[250:251], v38 offset:41984\n" \
"v_or_b32_e64 v217, 16, v219\n" \
"v_mfma_f32_16x16x32_bf16 a[72:75], a[168:171], a[144:147], a[72:75]\n" \
"v_cmp_gt_u32_e32 vcc, v217, v214\n" \
"s_nop 1\n" \
"v_cndmask_b32_e64 v221, v215, 0, vcc\n" \
"v_mfma_f32_16x16x32_bf16 a[72:75], a[232:235], a[208:211], a[72:75]\n" \
"v_cmp_lt_u32_e32 vcc, v217, v214\n" \
"s_nop 1\n" \
"v_cndmask_b32_e64 v217, 0, v220, vcc\n" \
"v_mfma_f32_16x16x32_bf16 a[76:79], a[172:175], a[144:147], a[76:79]\n" \
"v_or_b32_e32 v14, v221, v217\n" \
"v_or_b32_e32 v217, 18, v219\n" \
"v_mfma_f32_16x16x32_bf16 a[76:79], a[236:239], a[208:211], a[76:79]\n" \
"ds_read_b64_tr_b16 a[188:189], v39 offset:32768\n" \
"v_cmp_gt_u32_e64 vcc, v217, v214\n" \
"v_mfma_f32_16x16x32_bf16 a[92:95], a[172:175], a[148:151], a[92:95]\n" \
"s_nop 0\n" \
"v_cndmask_b32_e64 v215, v215, 0, vcc\n" \
"v_cmp_lt_u32_e32 vcc, v217, v214\n" \
"v_mfma_f32_16x16x32_bf16 a[92:95], a[236:239], a[212:215], a[92:95]\n" \
"s_nop 0\n" \
"v_cndmask_b32_e32 v214, 0, v220, vcc\n" \
"v_or_b32_e64 v15, v215, v214\n" \
"v_mfma_f32_16x16x32_bf16 a[88:91], a[168:171], a[148:151], a[88:91]\n" \
"ds_read_b64_tr_b16 a[190:191], v39 offset:33792\n" \
"v_mad_u64_u32 v[214:215], s[6:7], s5, 42, v[212:213]\n" \
"v_mfma_f32_16x16x32_bf16 a[88:91], a[232:235], a[212:215], a[88:91]\n" \
"s_movk_i32 s5, 0x1440\n" \
"v_cmp_gt_u32_e32 vcc, s5, v214\n" \
"v_mfma_f32_16x16x32_bf16 a[84:87], a[164:167], a[148:151], a[84:87]\n" \
"v_min_u32_e32 v217, 0xa43f, v214\n" \
"v_mov_b32_e64 v214, s24\n" \
"v_mfma_f32_16x16x32_bf16 a[84:87], a[228:231], a[212:215], a[84:87]\n" \
"ds_read_b64_tr_b16 a[252:253], v39 offset:40960\n" \
"v_cndmask_b32_e64 v215, v216, v213, vcc\n" \
"v_mfma_f32_16x16x32_bf16 a[80:83], a[160:163], a[148:151], a[80:83]\n" \
"v_mov_b32_e32 v213, s22\n" \
"s_movk_i32 s6, 0xffee\n" \
"v_mfma_f32_16x16x32_bf16 a[80:83], a[224:227], a[212:215], a[80:83]\n" \
"v_cndmask_b32_e64 v214, v214, v213, vcc\n" \
"v_add_u32_e32 v213, 0xffffebc0, v217\n" \
"v_mfma_f32_16x16x32_bf16 a[96:99], a[160:163], a[152:155], a[96:99]\n" \
"ds_read_b64_tr_b16 a[254:255], v39 offset:41984\n" \
"s_lshl_b32 s5, s2, 5\n" \
"v_mfma_f32_16x16x32_bf16 a[96:99], a[224:227], a[216:219], a[96:99]\n" \
"v_cndmask_b32_e32 v213, v213, v217, vcc\n" \
"v_lshlrev_b32_e64 v216, 7, v213\n" \
"v_mfma_f32_16x16x32_bf16 a[100:103], a[164:167], a[152:155], a[100:103]\n" \
"v_mul_u32_u24_e32 v213, 0xe39, v212\n" \
"v_mov_b32_e64 v217, 0\n" \
"v_mfma_f32_16x16x32_bf16 a[100:103], a[228:231], a[216:219], a[100:103]\n" \
"v_lshrrev_b32_e64 v213, 16, v213\n" \
"v_lshl_add_u64 v[16:17], v[214:215], 0, v[216:217]\n" \
"v_mfma_f32_16x16x32_bf16 a[104:107], a[168:171], a[152:155], a[104:107]\n" \
"v_mad_i32_i24 v214, v213, s6, v212\n" \
"s_add_i32 s5, s5, 32\n" \
"v_mfma_f32_16x16x32_bf16 a[104:107], a[232:235], a[216:219], a[104:107]\n" \
"v_lshlrev_b32_e32 v215, 4, v214\n" \
"v_add_u32_e64 v216, s5, v215\n" \
"v_mfma_f32_16x16x32_bf16 a[108:111], a[172:175], a[152:155], a[108:111]\n" \
"v_and_b32_e32 v216, 0xf0, v216\n" \
"v_cmp_gt_u32_e64 vcc, 16, v214\n" \
"v_mfma_f32_16x16x32_bf16 a[108:111], a[236:239], a[216:219], a[108:111]\n" \
"s_movk_i32 s7, 0xf800\n" \
"v_cndmask_b32_e32 v214, v215, v216, vcc\n" \
"s_waitcnt lgkmcnt(15)\n" \
"v_mfma_f32_16x16x32_bf16 a[124:127], a[172:175], a[156:159], a[124:127]\n" \
"v_or_b32_e32 v215, 64, v212\n" \
"v_mul_u32_u24_e32 v216, 0xe39, v215\n" \
"v_mfma_f32_16x16x32_bf16 a[124:127], a[236:239], a[220:223], a[124:127]\n" \
"v_lshrrev_b32_e64 v216, 16, v216\n" \
"v_mad_i32_i24 v215, v216, s6, v215\n" \
"v_mfma_f32_16x16x32_bf16 a[120:123], a[168:171], a[156:159], a[120:123]\n" \
"v_lshlrev_b32_e32 v217, 4, v215\n" \
"v_add_u32_e32 v220, s5, v217\n" \
"v_mfma_f32_16x16x32_bf16 a[120:123], a[232:235], a[220:223], a[120:123]\n" \
"v_and_b32_e32 v220, 0xf0, v220\n" \
"v_cmp_gt_u32_e64 vcc, 16, v215\n" \
"v_mfma_f32_16x16x32_bf16 a[116:119], a[164:167], a[156:159], a[116:119]\n" \
"s_nop 0\n" \
"v_cndmask_b32_e32 v215, v217, v220, vcc\n" \
"v_or_b32_e32 v217, 0x80, v212\n" \
"v_mfma_f32_16x16x32_bf16 a[116:119], a[228:231], a[220:223], a[116:119]\n" \
"v_mul_u32_u24_e32 v220, 0xe39, v217\n" \
"v_lshrrev_b32_e64 v220, 16, v220\n" \
"v_mfma_f32_16x16x32_bf16 a[112:115], a[160:163], a[156:159], a[112:115]\n" \
"v_mad_i32_i24 v217, v220, s6, v217\n" \
"v_lshlrev_b32_e64 v221, 4, v217\n" \
"v_mfma_f32_16x16x32_bf16 a[112:115], a[224:227], a[220:223], a[112:115]\n" \
"s_waitcnt vmcnt(20)\n" \
"v_readfirstlane_b32 s62, v180\n" \
"s_nop 3\n" \
"s_add_u32 s62, s62, 0x9E3779B1\n" \
"s_waitcnt vmcnt(2) lgkmcnt(0)\n" \
"s_barrier\n" \
"v_mfma_f32_16x16x32_bf16 v[52:55], a[176:179], a[128:131], v[52:55]\n" \
"s_add_u32 m0, s30, 0x8000\n" \
"s_movk_i32 s84, 0x180\n" \
"v_mfma_f32_16x16x32_bf16 v[52:55], a[240:243], a[192:195], v[52:55]\n" \
"buffer_load_dwordx4 v22, s[64:67], s84 offen offset:0 lds\n" \
"v_mfma_f32_16x16x32_bf16 v[56:59], a[180:183], a[128:131], v[56:59]\n" \
"ds_read_b64_tr_b16 a[160:161], v32 offset:0\n" \
"v_add_u32_e64 v222, s5, v221\n" \
"v_mfma_f32_16x16x32_bf16 v[56:59], a[244:247], a[192:195], v[56:59]\n" \
"s_movk_i32 s84, 0x1d80\n" \
"v_mfma_f32_16x16x32_bf16 v[60:63], a[184:187], a[128:131], v[60:63]\n" \
"buffer_load_dwordx4 v23, s[64:67], s84 offen offset:1024 lds\n" \
"v_mfma_f32_16x16x32_bf16 v[60:63], a[248:251], a[192:195], v[60:63]\n" \
"ds_read_b64_tr_b16 a[162:163], v32 offset:1024\n" \
"v_and_b32_e32 v222, 0xf0, v222\n" \
"v_mfma_f32_16x16x32_bf16 v[64:67], a[188:191], a[128:131], v[64:67]\n" \
"ds_read_b64_tr_b16 a[224:225], v32 offset:8192\n" \
"v_cmp_gt_u32_e32 vcc, 16, v217\n" \
"v_mfma_f32_16x16x32_bf16 v[64:67], a[252:255], a[192:195], v[64:67]\n" \
"ds_read_b128 a[128:131], v28 offset:16384\n" \
"v_cndmask_b32_e64 v217, v221, v222, vcc\n" \
"v_mfma_f32_16x16x32_bf16 v[80:83], a[188:191], a[132:135], v[80:83]\n" \
"ds_read_b128 a[192:195], v29 offset:16384\n" \
"v_or_b32_e32 v221, 0xc0, v212\n" \
"v_mfma_f32_16x16x32_bf16 v[80:83], a[252:255], a[196:199], v[80:83]\n" \
"s_movk_i32 s84, 0x3980\n" \
"v_mfma_f32_16x16x32_bf16 v[76:79], a[184:187], a[132:135], v[76:79]\n" \
"buffer_load_dwordx4 v22, s[64:67], s84 offen offset:2048 lds\n" \
"v_mfma_f32_16x16x32_bf16 v[76:79], a[248:251], a[196:199], v[76:79]\n" \
"s_movk_i32 s84, 0x5580\n" \
"v_mfma_f32_16x16x32_bf16 v[72:75], a[180:183], a[132:135], v[72:75]\n" \
"buffer_load_dwordx4 v23, s[64:67], s84 offen offset:3072 lds\n" \
"v_mfma_f32_16x16x32_bf16 v[72:75], a[244:247], a[196:199], v[72:75]\n" \
"s_add_u32 m0, s31, 0x8000\n" \
"s_mov_b32 s84, 0x180000\n" \
"v_mfma_f32_16x16x32_bf16 v[68:71], a[176:179], a[132:135], v[68:71]\n" \
"buffer_load_dwordx4 v26, s[68:71], s84 offen offset:0 lds\n" \
"v_mfma_f32_16x16x32_bf16 v[68:71], a[240:243], a[196:199], v[68:71]\n" \
"ds_read_b128 a[132:135], v28 offset:18432\n" \
"v_mul_u32_u24_e32 v222, 0xe39, v221\n" \
"v_mfma_f32_16x16x32_bf16 v[84:87], a[176:179], a[136:139], v[84:87]\n" \
"ds_read_b128 a[196:199], v29 offset:18432\n" \
"v_lshrrev_b32_e64 v222, 16, v222\n" \
"v_mfma_f32_16x16x32_bf16 v[84:87], a[240:243], a[200:203], v[84:87]\n" \
"s_addk_i32 s84, 0x7c00\n" \
"v_mfma_f32_16x16x32_bf16 v[88:91], a[180:183], a[136:139], v[88:91]\n" \
"buffer_load_dwordx4 v26, s[68:71], s84 offen offset:1024 lds\n" \
"v_mfma_f32_16x16x32_bf16 v[88:91], a[244:247], a[200:203], v[88:91]\n" \
"ds_read_b64_tr_b16 a[226:227], v32 offset:9216\n" \
"v_mad_i32_i24 v221, v222, s6, v221\n" \
"v_mfma_f32_16x16x32_bf16 v[92:95], a[184:187], a[136:139], v[92:95]\n" \
"s_addk_i32 s84, 0x7c00\n" \
"v_mfma_f32_16x16x32_bf16 v[92:95], a[248:251], a[200:203], v[92:95]\n" \
"buffer_load_dwordx4 v27, s[68:71], s84 offen offset:2048 lds\n" \
"v_mfma_f32_16x16x32_bf16 v[96:99], a[188:191], a[136:139], v[96:99]\n" \
"ds_read_b64_tr_b16 a[164:165], v33 offset:0\n" \
"v_lshlrev_b32_e64 v223, 4, v221\n" \
"v_mfma_f32_16x16x32_bf16 v[96:99], a[252:255], a[200:203], v[96:99]\n" \
"ds_read_b128 a[136:139], v28 offset:20480\n" \
"v_add_u32_e64 v224, s5, v223\n" \
"v_mfma_f32_16x16x32_bf16 v[112:115], a[188:191], a[140:143], v[112:115]\n" \
"ds_read_b128 a[200:203], v29 offset:20480\n" \
"v_and_b32_e32 v224, 0xf0, v224\n" \
"v_mfma_f32_16x16x32_bf16 v[112:115], a[252:255], a[204:207], v[112:115]\n" \
"s_addk_i32 s84, 0x7c00\n" \
"v_mfma_f32_16x16x32_bf16 v[108:111], a[184:187], a[140:143], v[108:111]\n" \
"buffer_load_dwordx4 v27, s[68:71], s84 offen offset:3072 lds\n" \
"v_mfma_f32_16x16x32_bf16 v[108:111], a[248:251], a[204:207], v[108:111]\n" \
"s_add_u32 m0, s30, 0x9000\n" \
"s_mov_b32 s84, 0x8180\n" \
"v_mfma_f32_16x16x32_bf16 v[104:107], a[180:183], a[140:143], v[104:107]\n" \
"buffer_load_dwordx4 v22, s[64:67], s84 offen offset:0 lds\n" \
"v_mfma_f32_16x16x32_bf16 v[104:107], a[244:247], a[204:207], v[104:107]\n" \
"ds_read_b64_tr_b16 a[166:167], v33 offset:1024\n" \
"v_cmp_gt_u32_e32 vcc, 16, v221\n" \
"v_mfma_f32_16x16x32_bf16 v[100:103], a[176:179], a[140:143], v[100:103]\n" \
"ds_read_b64_tr_b16 a[228:229], v33 offset:8192\n" \
"v_cndmask_b32_e64 v221, v223, v224, vcc\n" \
"v_mfma_f32_16x16x32_bf16 v[100:103], a[240:243], a[204:207], v[100:103]\n" \
"ds_read_b128 a[140:143], v28 offset:22528\n" \
"v_or_b32_e32 v223, 0x100, v212\n" \
"v_mfma_f32_16x16x32_bf16 v[116:119], a[176:179], a[144:147], v[116:119]\n" \
"ds_read_b128 a[204:207], v29 offset:22528\n" \
"v_mul_u32_u24_e32 v224, 0xe39, v223\n" \
"v_mfma_f32_16x16x32_bf16 v[116:119], a[240:243], a[208:211], v[116:119]\n" \
"s_addk_i32 s84, 0x1c00\n" \
"v_mfma_f32_16x16x32_bf16 v[120:123], a[180:183], a[144:147], v[120:123]\n" \
"buffer_load_dwordx4 v23, s[64:67], s84 offen offset:1024 lds\n" \
"v_mfma_f32_16x16x32_bf16 v[120:123], a[244:247], a[208:211], v[120:123]\n" \
"s_addk_i32 s84, 0x1c00\n" \
"v_mfma_f32_16x16x32_bf16 v[124:127], a[184:187], a[144:147], v[124:127]\n" \
"buffer_load_dwordx4 v22, s[64:67], s84 offen offset:2048 lds\n" \
"v_mfma_f32_16x16x32_bf16 v[124:127], a[248:251], a[208:211], v[124:127]\n" \
"s_addk_i32 s84, 0x1c00\n" \
"v_mfma_f32_16x16x32_bf16 v[128:131], a[188:191], a[144:147], v[128:131]\n" \
"buffer_load_dwordx4 v23, s[64:67], s84 offen offset:3072 lds\n" \
"v_mfma_f32_16x16x32_bf16 v[128:131], a[252:255], a[208:211], v[128:131]\n" \
"ds_read_b128 a[144:147], v28 offset:0\n" \
"v_lshrrev_b32_e32 v224, 16, v224\n" \
"v_mfma_f32_16x16x32_bf16 v[144:147], a[188:191], a[148:151], v[144:147]\n" \
"ds_read_b128 a[208:211], v29 offset:0\n" \
"v_mad_i32_i24 v223, v224, s6, v223\n" \
"v_mfma_f32_16x16x32_bf16 v[144:147], a[252:255], a[212:215], v[144:147]\n" \
"s_add_u32 m0, s31, 0xc000\n" \
"s_mov_b32 s84, 0x180000\n" \
"v_mfma_f32_16x16x32_bf16 v[140:143], a[184:187], a[148:151], v[140:143]\n" \
"buffer_load_dwordx4 v26, s[72:75], s84 offen offset:0 lds\n" \
"v_mfma_f32_16x16x32_bf16 v[140:143], a[248:251], a[212:215], v[140:143]\n" \
"ds_read_b64_tr_b16 a[230:231], v33 offset:9216\n" \
"v_lshlrev_b32_e64 v225, 4, v223\n" \
"v_mfma_f32_16x16x32_bf16 v[136:139], a[180:183], a[148:151], v[136:139]\n" \
"s_addk_i32 s84, 0x7c00\n" \
"v_mfma_f32_16x16x32_bf16 v[136:139], a[244:247], a[212:215], v[136:139]\n" \
"buffer_load_dwordx4 v26, s[72:75], s84 offen offset:1024 lds\n" \
"v_mfma_f32_16x16x32_bf16 v[132:135], a[176:179], a[148:151], v[132:135]\n" \
"ds_read_b64_tr_b16 a[168:169], v34 offset:0\n" \
"v_add_u32_e32 v226, s5, v225\n" \
"v_mfma_f32_16x16x32_bf16 v[132:135], a[240:243], a[212:215], v[132:135]\n" \
"ds_read_b128 a[148:151], v28 offset:2048\n" \
"v_and_b32_e32 v226, 0xf0, v226\n" \
"v_mfma_f32_16x16x32_bf16 v[148:151], a[176:179], a[152:155], v[148:151]\n" \
"ds_read_b128 a[212:215], v29 offset:2048\n" \
"v_cmp_gt_u32_e64 vcc, 16, v223\n" \
"v_mfma_f32_16x16x32_bf16 v[148:151], a[240:243], a[216:219], v[148:151]\n" \
"ds_read_b64_tr_b16 a[170:171], v34 offset:1024\n" \
"v_cndmask_b32_e64 v223, v225, v226, vcc\n" \
"v_mfma_f32_16x16x32_bf16 v[152:155], a[180:183], a[152:155], v[152:155]\n" \
"ds_read_b64_tr_b16 a[232:233], v34 offset:8192\n" \
"v_or_b32_e32 v225, 0x140, v212\n" \
"v_mfma_f32_16x16x32_bf16 v[152:155], a[244:247], a[216:219], v[152:155]\n" \
"ds_read_b64_tr_b16 a[234:235], v34 offset:9216\n" \
"v_mul_u32_u24_e32 v226, 0xe39, v225\n" \
"v_mfma_f32_16x16x32_bf16 v[156:159], a[184:187], a[152:155], v[156:159]\n" \
"ds_read_b64_tr_b16 a[172:173], v35 offset:0\n" \
"v_lshrrev_b32_e64 v226, 16, v226\n" \
"v_mfma_f32_16x16x32_bf16 v[156:159], a[248:251], a[216:219], v[156:159]\n" \
"ds_read_b64_tr_b16 a[174:175], v35 offset:1024\n" \
"v_mad_i32_i24 v225, v226, s6, v225\n" \
"v_mfma_f32_16x16x32_bf16 v[160:163], a[188:191], a[152:155], v[160:163]\n" \
"ds_read_b64_tr_b16 a[236:237], v35 offset:8192\n" \
"v_lshlrev_b32_e64 v227, 4, v225\n" \
"v_mfma_f32_16x16x32_bf16 v[160:163], a[252:255], a[216:219], v[160:163]\n" \
"ds_read_b128 a[152:155], v28 offset:4096\n" \
"v_add_u32_e64 v228, s5, v227\n" \
"v_mfma_f32_16x16x32_bf16 v[176:179], a[188:191], a[156:159], v[176:179]\n" \
"ds_read_b128 a[216:219], v29 offset:4096\n" \
"v_and_b32_e32 v228, 0xf0, v228\n" \
"v_mfma_f32_16x16x32_bf16 v[176:179], a[252:255], a[220:223], v[176:179]\n" \
"ds_read_b64_tr_b16 a[238:239], v35 offset:9216\n" \
"v_cmp_gt_u32_e64 vcc, 16, v225\n" \
"v_mfma_f32_16x16x32_bf16 v[172:175], a[184:187], a[156:159], v[172:175]\n" \
"s_nop 0\n" \
"v_cndmask_b32_e32 v225, v227, v228, vcc\n" \
"v_or_b32_e32 v227, 0x180, v212\n" \
"v_mfma_f32_16x16x32_bf16 v[172:175], a[248:251], a[220:223], v[172:175]\n" \
"v_mul_u32_u24_e32 v228, 0xe39, v227\n" \
"v_lshrrev_b32_e64 v228, 16, v228\n" \
"v_mfma_f32_16x16x32_bf16 v[168:171], a[180:183], a[156:159], v[168:171]\n" \
"v_mad_i32_i24 v227, v228, s6, v227\n" \
"v_lshlrev_b32_e64 v229, 4, v227\n" \
"v_mfma_f32_16x16x32_bf16 v[168:171], a[244:247], a[220:223], v[168:171]\n" \
"v_add_u32_e64 v230, s5, v229\n" \
"v_and_b32_e32 v230, 0xf0, v230\n" \
"v_mfma_f32_16x16x32_bf16 v[164:167], a[176:179], a[156:159], v[164:167]\n" \
"v_cmp_gt_u32_e32 vcc, 16, v227\n" \
"s_nop 1\n" \
"v_cndmask_b32_e64 v227, v229, v230, vcc\n" \
"v_mfma_f32_16x16x32_bf16 v[164:167], a[240:243], a[220:223], v[164:167]\n" \
"ds_read_b128 a[156:159], v28 offset:6144\n" \
"ds_read_b128 a[220:223], v29 offset:6144\n" \
"s_waitcnt lgkmcnt(15)\n" \
"v_mfma_f32_16x16x32_bf16 a[0:3], a[160:163], a[128:131], a[0:3]\n" \
"s_addk_i32 s84, 0x7c00\n" \
"v_mfma_f32_16x16x32_bf16 a[0:3], a[224:227], a[192:195], a[0:3]\n" \
"buffer_load_dwordx4 v27, s[72:75], s84 offen offset:2048 lds\n" \
"v_mfma_f32_16x16x32_bf16 a[4:7], a[164:167], a[128:131], a[4:7]\n" \
"ds_read_b64_tr_b16 a[176:177], v36 offset:0\n" \
"v_or_b32_e32 v229, 0x1c0, v212\n" \
"s_waitcnt lgkmcnt(15)\n" \
"v_mfma_f32_16x16x32_bf16 a[4:7], a[228:231], a[192:195], a[4:7]\n" \
"s_addk_i32 s84, 0x7c00\n" \
"s_waitcnt lgkmcnt(11)\n" \
"v_mfma_f32_16x16x32_bf16 a[8:11], a[168:171], a[128:131], a[8:11]\n" \
"buffer_load_dwordx4 v27, s[72:75], s84 offen offset:3072 lds\n" \
"s_waitcnt lgkmcnt(9)\n" \
"v_mfma_f32_16x16x32_bf16 a[8:11], a[232:235], a[192:195], a[8:11]\n" \
"ds_read_b64_tr_b16 a[178:179], v36 offset:1024\n" \
"v_mul_u32_u24_e32 v230, 0xe39, v229\n" \
"s_waitcnt lgkmcnt(8)\n" \
"v_mfma_f32_16x16x32_bf16 a[12:15], a[172:175], a[128:131], a[12:15]\n" \
"ds_read_b64_tr_b16 a[240:241], v36 offset:8192\n" \
"v_lshrrev_b32_e32 v230, 16, v230\n" \
"s_waitcnt lgkmcnt(5)\n" \
"v_mfma_f32_16x16x32_bf16 a[12:15], a[236:239], a[192:195], a[12:15]\n" \
"v_mad_i32_i24 v229, v230, s6, v229\n" \
"v_lshlrev_b32_e32 v231, 4, v229\n" \
"v_mfma_f32_16x16x32_bf16 a[28:31], a[172:175], a[132:135], a[28:31]\n" \
"v_add_u32_e64 v232, s5, v231\n" \
"v_and_b32_e32 v232, 0xf0, v232\n" \
"v_mfma_f32_16x16x32_bf16 a[28:31], a[236:239], a[196:199], a[28:31]\n" \
"ds_read_b64_tr_b16 a[242:243], v36 offset:9216\n" \
"v_cmp_gt_u32_e64 vcc, 16, v229\n" \
"v_mfma_f32_16x16x32_bf16 a[24:27], a[168:171], a[132:135], a[24:27]\n" \
"buffer_load_dwordx4 v[200:203], v192, s[76:79], s34 offen offset:0\n" \
"v_mfma_f32_16x16x32_bf16 a[24:27], a[232:235], a[196:199], a[24:27]\n" \
"v_cndmask_b32_e64 v229, v231, v232, vcc\n" \
"v_or_b32_e32 v231, 0x200, v212\n" \
"v_mfma_f32_16x16x32_bf16 a[20:23], a[164:167], a[132:135], a[20:23]\n" \
"v_mul_u32_u24_e32 v232, 0xe39, v231\n" \
"v_lshrrev_b32_e64 v232, 16, v232\n" \
"v_mfma_f32_16x16x32_bf16 a[20:23], a[228:231], a[196:199], a[20:23]\n" \
"ds_read_b64_tr_b16 a[180:181], v37 offset:0\n" \
"v_mad_i32_i24 v231, v232, s6, v231\n" \
"v_mfma_f32_16x16x32_bf16 a[16:19], a[160:163], a[132:135], a[16:19]\n" \
"v_lshlrev_b32_e32 v233, 4, v231\n" \
"v_add_u32_e32 v234, s5, v233\n" \
"v_mfma_f32_16x16x32_bf16 a[16:19], a[224:227], a[196:199], a[16:19]\n" \
"s_movk_i32 s5, 0x480\n" \
"v_mad_u32_u24 v0, v213, s5, v214\n" \
"v_mfma_f32_16x16x32_bf16 a[32:35], a[160:163], a[136:139], a[32:35]\n" \
"buffer_load_dwordx4 v[204:207], v193, s[76:79], s34 offen offset:0\n" \
"v_mfma_f32_16x16x32_bf16 a[32:35], a[224:227], a[200:203], a[32:35]\n" \
"s_mov_b32 m0, s32\n" \
"ds_read_b64_tr_b16 a[182:183], v37 offset:1024\n" \
"v_mfma_f32_16x16x32_bf16 a[36:39], a[164:167], a[136:139], a[36:39]\n" \
"s_mov_b32 exec_lo, 0x0\n" \
"s_mov_b32 exec_hi, 0xff00\n" \
"global_load_lds_dword v[184:185], off\n" \
"s_mov_b64 exec, -1\n" \
"v_mfma_f32_16x16x32_bf16 a[36:39], a[228:231], a[200:203], a[36:39]\n" \
"ds_read_b64_tr_b16 a[244:245], v37 offset:8192\n" \
"v_mul_u32_u24_e32 v213, 0x480, v216\n" \
"v_mfma_f32_16x16x32_bf16 a[40:43], a[168:171], a[136:139], a[40:43]\n" \
"s_movk_i32 s6, 0xfc00\n" \
"s_nop 0\n" \
"v_add3_u32 v1, v213, v215, s6\n" \
"v_mfma_f32_16x16x32_bf16 a[40:43], a[232:235], a[200:203], a[40:43]\n" \
"buffer_load_dword v208, v194, s[76:79], s34 offen offset:32\n" \
"v_mfma_f32_16x16x32_bf16 a[44:47], a[172:175], a[136:139], a[44:47]\n" \
"ds_read_b64_tr_b16 a[246:247], v37 offset:9216\n" \
"v_mul_u32_u24_e32 v213, 0x480, v220\n" \
"v_mfma_f32_16x16x32_bf16 a[44:47], a[236:239], a[200:203], a[44:47]\n" \
"v_add3_u32 v2, v213, v217, s7\n" \
"v_mul_u32_u24_e32 v213, 0x480, v222\n" \
"v_mfma_f32_16x16x32_bf16 a[60:63], a[172:175], a[140:143], a[60:63]\n" \
"v_add3_u32 v3, v213, v221, s40\n" \
"v_mul_u32_u24_e32 v213, 0x480, v226\n" \
"v_mfma_f32_16x16x32_bf16 a[60:63], a[236:239], a[204:207], a[60:63]\n" \
"v_add3_u32 v5, v213, v225, s6\n" \
"v_mul_u32_u24_e32 v213, 0x480, v228\n" \
"v_mfma_f32_16x16x32_bf16 a[56:59], a[168:171], a[140:143], a[56:59]\n" \
"buffer_load_dword v209, v194, s[76:79], s34 offen offset:1184\n" \
"v_mfma_f32_16x16x32_bf16 a[56:59], a[232:235], a[204:207], a[56:59]\n" \
"ds_read_b64_tr_b16 a[184:185], v38 offset:0\n" \
"v_add3_u32 v6, v213, v227, s7\n" \
"v_mfma_f32_16x16x32_bf16 a[52:55], a[164:167], a[140:143], a[52:55]\n" \
"v_mul_u32_u24_e32 v213, 0x480, v230\n" \
"v_add3_u32 v7, v213, v229, s40\n" \
"v_mfma_f32_16x16x32_bf16 a[52:55], a[228:231], a[204:207], a[52:55]\n" \
"ds_read_b64_tr_b16 a[186:187], v38 offset:1024\n" \
"s_lshl_b32 s7, s3, 3\n" \
"v_mfma_f32_16x16x32_bf16 a[48:51], a[160:163], a[140:143], a[48:51]\n" \
"s_lshl_b32 s40, s3, 1\n" \
"s_and_b32 s7, s7, 16\n" \
"v_mfma_f32_16x16x32_bf16 a[48:51], a[224:227], a[204:207], a[48:51]\n" \
"s_and_b32 s40, s40, 2\n" \
"s_and_b32 s6, s2, 7\n" \
"v_mfma_f32_16x16x32_bf16 a[64:67], a[160:163], a[144:147], a[64:67]\n" \
"buffer_load_dword v211, v194, s[76:79], s34 offen offset:2336\n" \
"v_mfma_f32_16x16x32_bf16 a[64:67], a[224:227], a[208:211], a[64:67]\n" \
"ds_read_b64_tr_b16 a[248:249], v38 offset:8192\n" \
"v_bfe_u32 v213, v20, 7, 1\n" \
"v_mfma_f32_16x16x32_bf16 a[68:71], a[164:167], a[144:147], a[68:71]\n" \
"s_or_b32 s7, s40, s7\n" \
"v_and_b32_e32 v234, 0xf0, v234\n" \
"v_mfma_f32_16x16x32_bf16 a[68:71], a[228:231], a[208:211], a[68:71]\n" \
"ds_read_b64_tr_b16 a[250:251], v38 offset:9216\n" \
"v_cmp_gt_u32_e64 vcc, 16, v231\n" \
"v_mfma_f32_16x16x32_bf16 a[72:75], a[168:171], a[144:147], a[72:75]\n" \
"v_or3_b32 v213, s7, v213, v219\n" \
"v_and_b32_e64 v214, 32, v20\n" \
"v_mfma_f32_16x16x32_bf16 a[72:75], a[232:235], a[208:211], a[72:75]\n" \
"s_mov_b32 s76, s20\n" \
"s_and_b32 s77, s21, 0xffff\n" \
"v_mfma_f32_16x16x32_bf16 a[76:79], a[172:175], a[144:147], a[76:79]\n" \
"s_mov_b32 s78, 0x480\n" \
"s_lshl_b32 s6, s6, 5\n" \
"v_mfma_f32_16x16x32_bf16 a[76:79], a[236:239], a[208:211], a[76:79]\n" \
"buffer_load_ushort v210, v195, s[76:79], 0 offen\n" \
"v_mfma_f32_16x16x32_bf16 a[92:95], a[172:175], a[148:151], a[92:95]\n" \
"ds_read_b64_tr_b16 a[188:189], v39 offset:0\n" \
"v_cndmask_b32_e64 v231, v233, v234, vcc\n" \
"v_mfma_f32_16x16x32_bf16 a[92:95], a[236:239], a[212:215], a[92:95]\n" \
"v_mul_u32_u24_e64 v213, 18, v213\n" \
"v_mov_b32_e32 v215, 0x100\n" \
"v_mfma_f32_16x16x32_bf16 a[88:91], a[168:171], a[148:151], a[88:91]\n" \
"s_mov_b32 s76, s8\n" \
"s_and_b32 s77, s9, 0xffff\n" \
"v_mfma_f32_16x16x32_bf16 a[88:91], a[232:235], a[212:215], a[88:91]\n" \
"s_mov_b32 s78, 0x240000\n" \
"ds_read_b64_tr_b16 a[190:191], v39 offset:1024\n" \
".fill (L_pb + 7904 - .) / 4, 4, 0xBF800000\n" \
"L_pp2:\n" \
"s_cbranch_execz L_pret\n" \
"v_mfma_f32_16x16x32_bf16 a[84:87], a[164:167], a[148:151], a[84:87]\n" \
"v_mov_b32_e32 v216, s6\n" \
"v_cmp_eq_u32_e64 vcc, 0, v214\n" \
"v_mfma_f32_16x16x32_bf16 a[84:87], a[228:231], a[212:215], a[84:87]\n" \
"ds_read_b64_tr_b16 a[252:253], v39 offset:8192\n" \
"v_and_or_b32 v213, v218, 1, v213\n" \
"v_mfma_f32_16x16x32_bf16 a[80:83], a[160:163], a[148:151], a[80:83]\n" \
"v_lshlrev_b32_e32 v212, 3, v212\n" \
"v_cndmask_b32_e32 v214, v215, v216, vcc\n" \
"v_mfma_f32_16x16x32_bf16 a[80:83], a[224:227], a[212:215], a[80:83]\n" \
"v_mad_u32_u24 v213, v213, s5, v214\n" \
"v_and_or_b32 v213, v20, 16, v213\n" \
"v_mfma_f32_16x16x32_bf16 a[96:99], a[160:163], a[152:155], a[96:99]\n" \
"ds_read_b64_tr_b16 a[254:255], v39 offset:9216\n" \
"v_add_u32_e32 v9, 0x4800, v213\n" \
"v_mfma_f32_16x16x32_bf16 a[96:99], a[224:227], a[216:219], a[96:99]\n" \
"v_mul_u32_u24_e32 v213, 0x120, v218\n" \
"v_and_or_b32 v10, v212, 24, v213\n" \
"v_mfma_f32_16x16x32_bf16 a[100:103], a[164:167], a[152:155], a[100:103]\n" \
"v_lshrrev_b32_e64 v213, 2, v20\n" \
"v_and_or_b32 v213, v213, 60, s4\n" \
"v_mfma_f32_16x16x32_bf16 a[100:103], a[228:231], a[216:219], a[100:103]\n" \
"v_lshl_or_b32 v212, s3, 9, v212\n" \
"v_mad_u32_u24 v4, v224, s5, v223\n" \
"v_mfma_f32_16x16x32_bf16 a[104:107], a[168:171], a[152:155], a[104:107]\n" \
"v_mad_u32_u24 v8, v232, s5, v231\n" \
"v_or_b32_e32 v18, 0x27000, v213\n" \
"v_mfma_f32_16x16x32_bf16 a[104:107], a[232:235], a[216:219], a[104:107]\n" \
"v_or_b32_e32 v19, 0x27800, v212\n" \
"v_mov_b32_e32 v215, 0x1000c0c\n" \
"v_mfma_f32_16x16x32_bf16 a[108:111], a[172:175], a[152:155], a[108:111]\n" \
"v_mov_b32_e32 v216, 0xc0c0100\n" \
"v_bfe_u32 v214, v20, 4, 1\n" \
"v_mfma_f32_16x16x32_bf16 a[108:111], a[236:239], a[216:219], a[108:111]\n" \
"v_lshrrev_b32_e32 v212, 8, v20\n" \
"v_cmp_eq_u32_e32 vcc, 0, v214\n" \
"s_waitcnt lgkmcnt(15)\n" \
"v_mfma_f32_16x16x32_bf16 a[124:127], a[172:175], a[156:159], a[124:127]\n" \
"v_bfe_u32 v213, v20, 4, 4\n" \
"v_mov_b32_e32 v217, 0xc0c0302\n" \
"v_mfma_f32_16x16x32_bf16 a[124:127], a[236:239], a[220:223], a[124:127]\n" \
"v_cndmask_b32_e32 v215, v215, v216, vcc\n" \
"v_mov_b32_e32 v216, 0x3020c0c\n" \
"v_mfma_f32_16x16x32_bf16 a[120:123], a[168:171], a[156:159], a[120:123]\n" \
"v_lshl_or_b32 v214, v212, 2, v214\n" \
"v_cndmask_b32_e64 v216, v216, v217, vcc\n" \
"v_mfma_f32_16x16x32_bf16 a[120:123], a[232:235], a[220:223], a[120:123]\n" \
"v_mov_b32_e32 v217, 0xc0c0c0c\n" \
"v_cmp_gt_u32_e64 vcc, v214, v213\n" \
"v_mfma_f32_16x16x32_bf16 a[116:119], a[164:167], a[156:159], a[116:119]\n" \
"v_or_b32_e32 v214, 2, v214\n" \
"s_lshr_b32 s4, s2, 5\n" \
"v_mfma_f32_16x16x32_bf16 a[116:119], a[228:231], a[220:223], a[116:119]\n" \
"v_cndmask_b32_e32 v40, v217, v215, vcc\n" \
"v_cndmask_b32_e32 v42, v215, v217, vcc\n" \
"v_mfma_f32_16x16x32_bf16 a[112:115], a[160:163], a[156:159], a[112:115]\n" \
"v_cmp_gt_u32_e32 vcc, v214, v213\n" \
"v_and_b32_e32 v214, 16, v20\n" \
"v_mfma_f32_16x16x32_bf16 a[112:115], a[224:227], a[220:223], a[112:115]\n" \
"s_waitcnt vmcnt(7) lgkmcnt(0)\n" \
"s_barrier\n" \
"v_mfma_f32_16x16x32_bf16 v[52:55], a[176:179], a[128:131], v[52:55]\n" \
"s_add_u32 m0, s30, 0x0\n" \
"s_movk_i32 s84, 0x200\n" \
"v_mfma_f32_16x16x32_bf16 v[52:55], a[240:243], a[192:195], v[52:55]\n" \
"buffer_load_dwordx4 v22, s[64:67], s84 offen offset:0 lds\n" \
"v_mfma_f32_16x16x32_bf16 v[56:59], a[180:183], a[128:131], v[56:59]\n" \
"ds_read_b64_tr_b16 a[160:161], v32 offset:32768\n" \
"s_movk_i32 s42, 0x3f80\n" \
"v_mfma_f32_16x16x32_bf16 v[56:59], a[244:247], a[192:195], v[56:59]\n" \
"s_movk_i32 s84, 0x1e00\n" \
"v_mfma_f32_16x16x32_bf16 v[60:63], a[184:187], a[128:131], v[60:63]\n" \
"buffer_load_dwordx4 v23, s[64:67], s84 offen offset:1024 lds\n" \
"v_mfma_f32_16x16x32_bf16 v[60:63], a[248:251], a[192:195], v[60:63]\n" \
"ds_read_b64_tr_b16 a[162:163], v32 offset:33792\n" \
"s_xor_b32 s5, s4, 15\n" \
"v_mfma_f32_16x16x32_bf16 v[64:67], a[188:191], a[128:131], v[64:67]\n" \
"ds_read_b64_tr_b16 a[224:225], v32 offset:40960\n" \
"v_mfma_f32_16x16x32_bf16 v[64:67], a[252:255], a[192:195], v[64:67]\n" \
"ds_read_b128 a[128:131], v28 offset:49152\n" \
"v_mfma_f32_16x16x32_bf16 v[80:83], a[188:191], a[132:135], v[80:83]\n" \
"ds_read_b128 a[192:195], v29 offset:49152\n" \
"v_mfma_f32_16x16x32_bf16 v[80:83], a[252:255], a[196:199], v[80:83]\n" \
"s_movk_i32 s84, 0x3a00\n" \
"v_mfma_f32_16x16x32_bf16 v[76:79], a[184:187], a[132:135], v[76:79]\n" \
"buffer_load_dwordx4 v22, s[64:67], s84 offen offset:2048 lds\n" \
"v_mfma_f32_16x16x32_bf16 v[76:79], a[248:251], a[196:199], v[76:79]\n" \
"s_movk_i32 s84, 0x5600\n" \
"v_mfma_f32_16x16x32_bf16 v[72:75], a[180:183], a[132:135], v[72:75]\n" \
"buffer_load_dwordx4 v23, s[64:67], s84 offen offset:3072 lds\n" \
"v_mfma_f32_16x16x32_bf16 v[72:75], a[244:247], a[196:199], v[72:75]\n" \
"s_add_u32 m0, s31, 0x0\n" \
"s_mov_b32 s84, 0x200000\n" \
"v_mfma_f32_16x16x32_bf16 v[68:71], a[176:179], a[132:135], v[68:71]\n" \
"buffer_load_dwordx4 v26, s[68:71], s84 offen offset:0 lds\n" \
"v_mfma_f32_16x16x32_bf16 v[68:71], a[240:243], a[196:199], v[68:71]\n" \
"ds_read_b128 a[132:135], v28 offset:51200\n" \
"v_mfma_f32_16x16x32_bf16 v[84:87], a[176:179], a[136:139], v[84:87]\n" \
"ds_read_b128 a[196:199], v29 offset:51200\n" \
"v_mfma_f32_16x16x32_bf16 v[84:87], a[240:243], a[200:203], v[84:87]\n" \
"s_addk_i32 s84, 0x7c00\n" \
"v_mfma_f32_16x16x32_bf16 v[88:91], a[180:183], a[136:139], v[88:91]\n" \
"buffer_load_dwordx4 v26, s[68:71], s84 offen offset:1024 lds\n" \
"v_mfma_f32_16x16x32_bf16 v[88:91], a[244:247], a[200:203], v[88:91]\n" \
"ds_read_b64_tr_b16 a[226:227], v32 offset:41984\n" \
"v_mfma_f32_16x16x32_bf16 v[92:95], a[184:187], a[136:139], v[92:95]\n" \
"s_addk_i32 s84, 0x7c00\n" \
"v_mfma_f32_16x16x32_bf16 v[92:95], a[248:251], a[200:203], v[92:95]\n" \
"buffer_load_dwordx4 v27, s[68:71], s84 offen offset:2048 lds\n" \
"v_mfma_f32_16x16x32_bf16 v[96:99], a[188:191], a[136:139], v[96:99]\n" \
"ds_read_b64_tr_b16 a[164:165], v33 offset:32768\n" \
"v_mfma_f32_16x16x32_bf16 v[96:99], a[252:255], a[200:203], v[96:99]\n" \
"ds_read_b128 a[136:139], v28 offset:53248\n" \
"v_mfma_f32_16x16x32_bf16 v[112:115], a[188:191], a[140:143], v[112:115]\n" \
"ds_read_b128 a[200:203], v29 offset:53248\n" \
"v_mfma_f32_16x16x32_bf16 v[112:115], a[252:255], a[204:207], v[112:115]\n" \
"s_addk_i32 s84, 0x7c00\n" \
"s_nop 0\n" \
"v_mfma_f32_16x16x32_bf16 v[108:111], a[184:187], a[140:143], v[108:111]\n" \
"buffer_load_dwordx4 v27, s[68:71], s84 offen offset:3072 lds\n" \
"v_mfma_f32_16x16x32_bf16 v[108:111], a[248:251], a[204:207], v[108:111]\n" \
"s_add_u32 m0, s30, 0x1000\n" \
"s_mov_b32 s84, 0x8200\n" \
"v_mfma_f32_16x16x32_bf16 v[104:107], a[180:183], a[140:143], v[104:107]\n" \
"buffer_load_dwordx4 v22, s[64:67], s84 offen offset:0 lds\n" \
"v_mfma_f32_16x16x32_bf16 v[104:107], a[244:247], a[204:207], v[104:107]\n" \
"ds_read_b64_tr_b16 a[166:167], v33 offset:33792\n" \
"v_mfma_f32_16x16x32_bf16 v[100:103], a[176:179], a[140:143], v[100:103]\n" \
"ds_read_b64_tr_b16 a[228:229], v33 offset:40960\n" \
"v_mfma_f32_16x16x32_bf16 v[100:103], a[240:243], a[204:207], v[100:103]\n" \
"ds_read_b128 a[140:143], v28 offset:55296\n" \
"v_mfma_f32_16x16x32_bf16 v[116:119], a[176:179], a[144:147], v[116:119]\n" \
"ds_read_b128 a[204:207], v29 offset:55296\n" \
"v_mfma_f32_16x16x32_bf16 v[116:119], a[240:243], a[208:211], v[116:119]\n" \
"s_addk_i32 s84, 0x1c00\n" \
"v_mfma_f32_16x16x32_bf16 v[120:123], a[180:183], a[144:147], v[120:123]\n" \
"buffer_load_dwordx4 v23, s[64:67], s84 offen offset:1024 lds\n" \
"v_mfma_f32_16x16x32_bf16 v[120:123], a[244:247], a[208:211], v[120:123]\n" \
"s_addk_i32 s84, 0x1c00\n" \
"v_mfma_f32_16x16x32_bf16 v[124:127], a[184:187], a[144:147], v[124:127]\n" \
"buffer_load_dwordx4 v22, s[64:67], s84 offen offset:2048 lds\n" \
"v_mfma_f32_16x16x32_bf16 v[124:127], a[248:251], a[208:211], v[124:127]\n" \
"s_addk_i32 s84, 0x1c00\n" \
"s_nop 0\n" \
"v_mfma_f32_16x16x32_bf16 v[128:131], a[188:191], a[144:147], v[128:131]\n" \
"buffer_load_dwordx4 v23, s[64:67], s84 offen offset:3072 lds\n" \
"v_mfma_f32_16x16x32_bf16 v[128:131], a[252:255], a[208:211], v[128:131]\n" \
"ds_read_b128 a[144:147], v28 offset:32768\n" \
"v_mfma_f32_16x16x32_bf16 v[144:147], a[188:191], a[148:151], v[144:147]\n" \
"ds_read_b128 a[208:211], v29 offset:32768\n" \
"v_mfma_f32_16x16x32_bf16 v[144:147], a[252:255], a[212:215], v[144:147]\n" \
"s_add_u32 m0, s31, 0x4000\n" \
"s_mov_b32 s84, 0x200000\n" \
"v_mfma_f32_16x16x32_bf16 v[140:143], a[184:187], a[148:151], v[140:143]\n" \
"buffer_load_dwordx4 v26, s[72:75], s84 offen offset:0 lds\n" \
"v_mfma_f32_16x16x32_bf16 v[140:143], a[248:251], a[212:215], v[140:143]\n" \
"ds_read_b64_tr_b16 a[230:231], v33 offset:41984\n" \
"v_mfma_f32_16x16x32_bf16 v[136:139], a[180:183], a[148:151], v[136:139]\n" \
"s_addk_i32 s84, 0x7c00\n" \
"s_nop 0\n" \
"v_mfma_f32_16x16x32_bf16 v[136:139], a[244:247], a[212:215], v[136:139]\n" \
"buffer_load_dwordx4 v26, s[72:75], s84 offen offset:1024 lds\n" \
"v_mfma_f32_16x16x32_bf16 v[132:135], a[176:179], a[148:151], v[132:135]\n" \
"ds_read_b64_tr_b16 a[168:169], v34 offset:32768\n" \
"v_mfma_f32_16x16x32_bf16 v[132:135], a[240:243], a[212:215], v[132:135]\n" \
"ds_read_b128 a[148:151], v28 offset:34816\n" \
"v_mfma_f32_16x16x32_bf16 v[148:151], a[176:179], a[152:155], v[148:151]\n" \
"ds_read_b128 a[212:215], v29 offset:34816\n" \
"v_mfma_f32_16x16x32_bf16 v[148:151], a[240:243], a[216:219], v[148:151]\n" \
"ds_read_b64_tr_b16 a[170:171], v34 offset:33792\n" \
"v_mfma_f32_16x16x32_bf16 v[152:155], a[180:183], a[152:155], v[152:155]\n" \
"ds_read_b64_tr_b16 a[232:233], v34 offset:40960\n" \
"v_mfma_f32_16x16x32_bf16 v[152:155], a[244:247], a[216:219], v[152:155]\n" \
"ds_read_b64_tr_b16 a[234:235], v34 offset:41984\n" \
"v_mfma_f32_16x16x32_bf16 v[156:159], a[184:187], a[152:155], v[156:159]\n" \
"ds_read_b64_tr_b16 a[172:173], v35 offset:32768\n" \
"v_mfma_f32_16x16x32_bf16 v[156:159], a[248:251], a[216:219], v[156:159]\n" \
"ds_read_b64_tr_b16 a[174:175], v35 offset:33792\n" \
"v_mfma_f32_16x16x32_bf16 v[160:163], a[188:191], a[152:155], v[160:163]\n" \
"ds_read_b64_tr_b16 a[236:237], v35 offset:40960\n" \
"v_mfma_f32_16x16x32_bf16 v[160:163], a[252:255], a[216:219], v[160:163]\n" \
"ds_read_b128 a[152:155], v28 offset:36864\n" \
"v_mfma_f32_16x16x32_bf16 v[176:179], a[188:191], a[156:159], v[176:179]\n" \
"ds_read_b128 a[216:219], v29 offset:36864\n" \
"v_mfma_f32_16x16x32_bf16 v[176:179], a[252:255], a[220:223], v[176:179]\n" \
"ds_read_b64_tr_b16 a[238:239], v35 offset:41984\n" \
"v_mfma_f32_16x16x32_bf16 v[172:175], a[184:187], a[156:159], v[172:175]\n" \
"s_cmpk_lt_u32 s3, 2\n" \
"s_cselect_b32 s6, s5, s4\n" \
"v_mfma_f32_16x16x32_bf16 v[172:175], a[248:251], a[220:223], v[172:175]\n" \
"s_lshl_b32 s7, s6, 7\n" \
"v_and_b32_e32 v213, 0x320, v20\n" \
"v_mfma_f32_16x16x32_bf16 v[168:171], a[180:183], a[156:159], v[168:171]\n" \
"s_movk_i32 s41, 0x100\n" \
"v_lshlrev_b32_e64 v214, v214, s42\n" \
"v_mfma_f32_16x16x32_bf16 v[168:171], a[244:247], a[220:223], v[168:171]\n" \
"v_mov_b32_e32 v215, 0\n" \
"s_add_i32 s40, s7, -16\n" \
"v_mfma_f32_16x16x32_bf16 v[164:167], a[176:179], a[156:159], v[164:167]\n" \
"v_cndmask_b32_e32 v41, v217, v216, vcc\n" \
"v_cndmask_b32_e32 v43, v216, v217, vcc\n" \
"v_mfma_f32_16x16x32_bf16 v[164:167], a[240:243], a[220:223], v[164:167]\n" \
"ds_read_b128 a[156:159], v28 offset:38912\n" \
"ds_read_b128 a[220:223], v29 offset:38912\n" \
"s_waitcnt lgkmcnt(15)\n" \
"v_mfma_f32_16x16x32_bf16 a[0:3], a[160:163], a[128:131], a[0:3]\n" \
"s_addk_i32 s84, 0x7c00\n" \
"v_mfma_f32_16x16x32_bf16 a[0:3], a[224:227], a[192:195], a[0:3]\n" \
"buffer_load_dwordx4 v27, s[72:75], s84 offen offset:2048 lds\n" \
"v_mfma_f32_16x16x32_bf16 a[4:7], a[164:167], a[128:131], a[4:7]\n" \
"ds_read_b64_tr_b16 a[176:177], v36 offset:32768\n" \
"v_cmp_eq_u32_e32 vcc, s41, v213\n" \
"s_waitcnt lgkmcnt(15)\n" \
"v_mfma_f32_16x16x32_bf16 a[4:7], a[228:231], a[192:195], a[4:7]\n" \
"s_addk_i32 s84, 0x7c00\n" \
"s_waitcnt lgkmcnt(11)\n" \
"v_mfma_f32_16x16x32_bf16 a[8:11], a[168:171], a[128:131], a[8:11]\n" \
"buffer_load_dwordx4 v27, s[72:75], s84 offen offset:3072 lds\n" \
"s_waitcnt lgkmcnt(9)\n" \
"v_mfma_f32_16x16x32_bf16 a[8:11], a[232:235], a[192:195], a[8:11]\n" \
"ds_read_b64_tr_b16 a[178:179], v36 offset:33792\n" \
"s_mov_b32 s41, 0\n" \
"s_waitcnt lgkmcnt(8)\n" \
"v_mfma_f32_16x16x32_bf16 a[12:15], a[172:175], a[128:131], a[12:15]\n" \
"ds_read_b64_tr_b16 a[240:241], v36 offset:40960\n" \
"s_and_b32 s43, s3, 1\n" \
"s_waitcnt lgkmcnt(5)\n" \
"v_mfma_f32_16x16x32_bf16 a[12:15], a[236:239], a[192:195], a[12:15]\n" \
"v_cndmask_b32_e32 v46, v215, v214, vcc\n" \
"v_and_b32_e32 v213, 0x330, v20\n" \
"v_mfma_f32_16x16x32_bf16 a[28:31], a[172:175], a[132:135], a[28:31]\n" \
"s_cmp_lg_u32 s7, 0\n" \
"s_cselect_b32 s40, s40, s41\n" \
"v_mfma_f32_16x16x32_bf16 a[28:31], a[236:239], a[196:199], a[28:31]\n" \
"ds_read_b64_tr_b16 a[242:243], v36 offset:41984\n" \
"s_mov_b32 s42, 0xff800000\n" \
"v_mfma_f32_16x16x32_bf16 a[24:27], a[168:171], a[132:135], a[24:27]\n" \
"s_cmp_lg_u32 s7, 0\n" \
"s_cselect_b32 s47, s41, s42\n" \
"v_mfma_f32_16x16x32_bf16 a[24:27], a[232:235], a[196:199], a[24:27]\n" \
"s_lshl_b32 s41, s43, 6\n" \
"v_lshrrev_b32_e32 v215, 6, v20\n" \
"v_mfma_f32_16x16x32_bf16 a[20:23], a[164:167], a[132:135], a[20:23]\n" \
"v_mov_b32_e32 v214, 0x3f80\n" \
"v_cmp_eq_u32_e64 vcc, 32, v213\n" \
"v_mfma_f32_16x16x32_bf16 a[20:23], a[228:231], a[196:199], a[20:23]\n" \
"ds_read_b64_tr_b16 a[180:181], v37 offset:32768\n" \
"s_or_b32 s7, s41, s7\n" \
"v_mfma_f32_16x16x32_bf16 a[16:19], a[160:163], a[132:135], a[16:19]\n" \
"v_lshrrev_b32_e32 v213, 7, v20\n" \
"v_bfe_u32 v216, v20, 6, 2\n" \
"v_mfma_f32_16x16x32_bf16 a[16:19], a[224:227], a[196:199], a[16:19]\n" \
"v_lshrrev_b32_e64 v219, 1, v20\n" \
"v_bfe_u32 v215, v215, 1, 1\n" \
"v_mfma_f32_16x16x32_bf16 a[32:35], a[160:163], a[136:139], a[32:35]\n" \
"s_waitcnt vmcnt(16)\n" \
"v_or_b32_e32 v204, v210, v204\n" \
"v_or_b32_e64 v206, v210, v206\n" \
"v_mfma_f32_16x16x32_bf16 a[32:35], a[224:227], a[200:203], a[32:35]\n" \
"v_mov_b32_e64 v210, 0\n" \
"s_add_u32 s88, s35, 0x1000\n" \
"v_mfma_f32_16x16x32_bf16 a[36:39], a[164:167], a[136:139], a[36:39]\n" \
"ds_read_b64_tr_b16 a[182:183], v37 offset:33792\n" \
"v_cndmask_b32_e64 v47, 0, v214, vcc\n" \
"v_mfma_f32_16x16x32_bf16 a[36:39], a[228:231], a[200:203], a[36:39]\n" \
"ds_read_b64_tr_b16 a[244:245], v37 offset:40960\n" \
"v_or_b32_e64 v214, s7, v213\n" \
"v_mfma_f32_16x16x32_bf16 a[40:43], a[168:171], a[136:139], a[40:43]\n" \
"s_mov_b32 m0, s32\n" \
"v_lshl_or_b32 v217, s43, 3, v213\n" \
"v_mfma_f32_16x16x32_bf16 a[40:43], a[232:235], a[200:203], a[40:43]\n" \
"s_mov_b32 exec_lo, 0xffffffff\n" \
"s_mov_b32 exec_hi, 0x0\n" \
"global_load_lds_dword v[188:189], off\n" \
"s_mov_b64 exec, -1\n" \
"v_mfma_f32_16x16x32_bf16 a[44:47], a[172:175], a[136:139], a[44:47]\n" \
"ds_read_b64_tr_b16 a[246:247], v37 offset:41984\n" \
"v_lshlrev_b32_e64 v218, 10, v212\n" \
"v_mfma_f32_16x16x32_bf16 a[44:47], a[236:239], a[200:203], a[44:47]\n" \
"v_lshlrev_b32_e32 v216, 7, v216\n" \
"v_and_b32_e32 v219, 24, v219\n" \
"v_mfma_f32_16x16x32_bf16 a[60:63], a[172:175], a[140:143], a[60:63]\n" \
"ds_write_b128 v196, v[200:203]\n" \
"v_mfma_f32_16x16x32_bf16 a[60:63], a[236:239], a[204:207], a[60:63]\n" \
"v_and_or_b32 v213, v213, 2, v215\n" \
"v_add_u32_e64 v217, s40, v217\n" \
"v_mfma_f32_16x16x32_bf16 a[56:59], a[168:171], a[140:143], a[56:59]\n" \
"ds_read_b64_tr_b16 a[184:185], v38 offset:32768\n" \
"v_or3_b32 v216, v216, v218, v219\n" \
"v_mfma_f32_16x16x32_bf16 a[56:59], a[232:235], a[204:207], a[56:59]\n" \
"v_lshlrev_b32_e64 v213, 5, v213\n" \
"s_mov_b32 s40, 0x14000\n" \
"v_mfma_f32_16x16x32_bf16 a[52:55], a[164:167], a[140:143], a[52:55]\n" \
"s_lshl_b32 s7, s2, 2\n" \
"v_or3_b32 v48, v216, v213, s40\n" \
"v_mfma_f32_16x16x32_bf16 a[52:55], a[228:231], a[204:207], a[52:55]\n" \
"ds_read_b64_tr_b16 a[186:187], v38 offset:33792\n" \
"s_lshl_b32 s40, s43, 2\n" \
"v_mfma_f32_16x16x32_bf16 a[48:51], a[160:163], a[140:143], a[48:51]\n" \
"s_lshl_b32 s6, s6, 3\n" \
"v_or_b32_e32 v213, s40, v212\n" \
"v_mfma_f32_16x16x32_bf16 a[48:51], a[224:227], a[204:207], a[48:51]\n" \
"s_or_b32 s6, s6, s40\n" \
"s_and_b32 s40, s7, 0x7c\n" \
"v_mfma_f32_16x16x32_bf16 a[64:67], a[160:163], a[144:147], a[64:67]\n" \
"ds_write_b128 v197, v[204:207]\n" \
"v_mfma_f32_16x16x32_bf16 a[64:67], a[224:227], a[208:211], a[64:67]\n" \
"ds_read_b64_tr_b16 a[248:249], v38 offset:40960\n" \
"s_or_b32 s40, s40, s3\n" \
"v_mfma_f32_16x16x32_bf16 a[68:71], a[164:167], a[144:147], a[68:71]\n" \
"s_cmpk_lt_u32 s40, 64\n" \
"s_cselect_b32 s41, s4, s5\n" \
"v_mfma_f32_16x16x32_bf16 a[68:71], a[228:231], a[208:211], a[68:71]\n" \
"ds_read_b64_tr_b16 a[250:251], v38 offset:41984\n" \
"s_lshl_b32 s40, s40, 1\n" \
"v_mfma_f32_16x16x32_bf16 a[72:75], a[168:171], a[144:147], a[72:75]\n" \
"s_lshl_b32 s41, s41, 7\n" \
"s_and_b32 s40, s40, 0x7e\n" \
"v_mfma_f32_16x16x32_bf16 a[72:75], a[232:235], a[208:211], a[72:75]\n" \
"s_or_b32 s40, s41, s40\n" \
"s_mulk_i32 s40, 0x480\n" \
"v_mfma_f32_16x16x32_bf16 a[76:79], a[172:175], a[144:147], a[76:79]\n" \
"v_add_u32_e64 v49, s40, v20\n" \
"s_mov_b32 s40, 0xc000\n" \
"v_mfma_f32_16x16x32_bf16 a[76:79], a[236:239], a[208:211], a[76:79]\n" \
"ds_read_b64_tr_b16 a[188:189], v39 offset:32768\n" \
"s_and_b32 s7, s7, 28\n" \
"v_mfma_f32_16x16x32_bf16 a[92:95], a[172:175], a[148:151], a[92:95]\n" \
"ds_write_b128 v198, v[208:211]\n" \
"v_mfma_f32_16x16x32_bf16 a[92:95], a[236:239], a[212:215], a[92:95]\n" \
"s_bfe_u32 s45, s2, 0x20003\n" \
"s_mov_b32 s41, 0x8000\n" \
"v_mfma_f32_16x16x32_bf16 a[88:91], a[168:171], a[148:151], a[88:91]\n" \
"ds_read_b64_tr_b16 a[190:191], v39 offset:33792\n" \
"v_mfma_f32_16x16x32_bf16 a[88:91], a[232:235], a[212:215], a[88:91]\n" \
"s_cmp_lg_u32 s4, 0\n" \
"s_cselect_b32 s51, s40, s41\n" \
"v_mfma_f32_16x16x32_bf16 a[84:87], a[164:167], a[148:151], a[84:87]\n" \
"s_movk_i32 s40, 0x4000\n" \
"s_lshr_b32 s44, s3, 1\n" \
"v_mfma_f32_16x16x32_bf16 a[84:87], a[228:231], a[212:215], a[84:87]\n" \
"ds_read_b64_tr_b16 a[252:253], v39 offset:40960\n" \
"s_or_b32 s46, s7, s45\n" \
"v_mfma_f32_16x16x32_bf16 a[80:83], a[160:163], a[148:151], a[80:83]\n" \
"s_cmp_lg_u32 s4, 0\n" \
"s_cselect_b32 s53, s41, s40\n" \
"v_mfma_f32_16x16x32_bf16 a[80:83], a[224:227], a[212:215], a[80:83]\n" \
"s_lshl_b32 s40, s44, 15\n" \
"s_lshl_b32 s41, s43, 14\n" \
"v_mfma_f32_16x16x32_bf16 a[96:99], a[160:163], a[152:155], a[96:99]\n" \
"ds_read_b64_tr_b16 a[254:255], v39 offset:41984\n" \
"s_lshl_b32 s48, s46, 20\n" \
"v_mfma_f32_16x16x32_bf16 a[96:99], a[224:227], a[216:219], a[96:99]\n" \
"s_or_b32 s41, s40, s41\n" \
"s_add_i32 s49, s48, 0x1000000\n" \
"v_mfma_f32_16x16x32_bf16 a[100:103], a[164:167], a[152:155], a[100:103]\n" \
"s_or_b32 s42, s49, s41\n" \
"s_lshl_b32 s4, s4, 16\n" \
"v_mfma_f32_16x16x32_bf16 a[100:103], a[228:231], a[216:219], a[100:103]\n" \
"s_or_b32 s41, s42, s4\n" \
"s_lshl_b32 s4, s5, 16\n" \
"v_mfma_f32_16x16x32_bf16 a[104:107], a[168:171], a[152:155], a[104:107]\n" \
"s_mul_i32 s5, s6, 0x24800\n" \
"s_lshl_b32 s6, s46, 9\n" \
"v_mfma_f32_16x16x32_bf16 a[104:107], a[232:235], a[216:219], a[104:107]\n" \
"s_lshl_b32 s7, s7, 6\n" \
"s_add_i32 s6, s5, s6\n" \
"v_mfma_f32_16x16x32_bf16 a[108:111], a[172:175], a[152:155], a[108:111]\n" \
"s_or_b32 s5, s5, s7\n" \
"s_lshl_b32 s7, s45, 2\n" \
"v_mfma_f32_16x16x32_bf16 a[108:111], a[236:239], a[216:219], a[108:111]\n" \
"s_or_b32 s5, s5, s7\n" \
"s_lshl_b32 s7, s3, 12\n" \
"s_waitcnt lgkmcnt(15)\n" \
"v_mfma_f32_16x16x32_bf16 a[124:127], a[172:175], a[156:159], a[124:127]\n" \
"s_or_b32 s50, s49, s7\n" \
"s_lshl_b32 s7, s3, 11\n" \
"v_mfma_f32_16x16x32_bf16 a[124:127], a[236:239], a[220:223], a[124:127]\n" \
"v_lshrrev_b32_e64 v215, 4, v20\n" \
"v_bfe_u32 v216, v20, 4, 3\n" \
"v_mfma_f32_16x16x32_bf16 a[120:123], a[168:171], a[156:159], a[120:123]\n" \
"s_or_b32 s42, s42, s4\n" \
"s_lshl_b32 s4, s43, 11\n" \
"v_mfma_f32_16x16x32_bf16 a[120:123], a[232:235], a[220:223], a[120:123]\n" \
"s_add_i32 s52, s48, s7\n" \
"s_add_i32 s53, s53, s7\n" \
"v_mfma_f32_16x16x32_bf16 a[116:119], a[164:167], a[156:159], a[116:119]\n" \
"v_mul_u32_u24_e32 v214, 0x480, v214\n" \
"v_bitop3_b32 v218, v212, v215, 7 bitop3:0x78\n" \
"v_mfma_f32_16x16x32_bf16 a[116:119], a[228:231], a[220:223], a[116:119]\n" \
"v_bitop3_b32 v212, v212, v216, 4 bitop3:0x36\n" \
"s_lshl_b32 s7, s3, 10\n" \
"v_mfma_f32_16x16x32_bf16 a[112:115], a[160:163], a[156:159], a[112:115]\n" \
"s_or_b32 s4, s40, s4\n" \
"v_lshl_or_b32 v25, v212, 4, v214\n" \
"v_mfma_f32_16x16x32_bf16 a[112:115], a[224:227], a[220:223], a[112:115]\n" \
"s_waitcnt vmcnt(1) lgkmcnt(0)\n" \
"s_barrier\n" \
"v_mfma_f32_16x16x32_bf16 v[52:55], a[176:179], a[128:131], v[52:55]\n" \
"s_add_u32 m0, s30, 0x8000\n" \
"s_movk_i32 s84, 0x280\n" \
"v_mfma_f32_16x16x32_bf16 v[52:55], a[240:243], a[192:195], v[52:55]\n" \
"buffer_load_dwordx4 v22, s[64:67], s84 offen offset:0 lds\n" \
"v_mfma_f32_16x16x32_bf16 v[56:59], a[180:183], a[128:131], v[56:59]\n" \
"s_movk_i32 s84, 0x1e80\n" \
"v_mfma_f32_16x16x32_bf16 v[56:59], a[244:247], a[192:195], v[56:59]\n" \
"buffer_load_dwordx4 v23, s[64:67], s84 offen offset:1024 lds\n" \
"v_mfma_f32_16x16x32_bf16 v[60:63], a[184:187], a[128:131], v[60:63]\n" \
"ds_read_b64_tr_b16 a[160:161], v32 offset:0\n" \
"v_mul_u32_u24_e32 v212, 0x480, v217\n" \
"v_mfma_f32_16x16x32_bf16 v[60:63], a[248:251], a[192:195], v[60:63]\n" \
"s_movk_i32 s84, 0x3a80\n" \
"v_mfma_f32_16x16x32_bf16 v[64:67], a[188:191], a[128:131], v[64:67]\n" \
"buffer_load_dwordx4 v22, s[64:67], s84 offen offset:2048 lds\n" \
"v_mfma_f32_16x16x32_bf16 v[64:67], a[252:255], a[192:195], v[64:67]\n" \
"ds_read_b128 a[128:131], v28 offset:16384\n" \
"v_bitop3_b32 v213, v215, v213, 7 bitop3:0x6c\n" \
"v_mfma_f32_16x16x32_bf16 v[80:83], a[188:191], a[132:135], v[80:83]\n" \
"ds_read_b128 a[192:195], v29 offset:16384\n" \
"s_or_b32 s40, s7, 0x14000\n" \
"v_mfma_f32_16x16x32_bf16 v[80:83], a[252:255], a[196:199], v[80:83]\n" \
"s_movk_i32 s84, 0x5680\n" \
"v_mfma_f32_16x16x32_bf16 v[76:79], a[184:187], a[132:135], v[76:79]\n" \
"buffer_load_dwordx4 v23, s[64:67], s84 offen offset:3072 lds\n" \
"v_mfma_f32_16x16x32_bf16 v[76:79], a[248:251], a[196:199], v[76:79]\n" \
"s_add_u32 m0, s31, 0x8000\n" \
"s_mov_b32 s84, 0x280000\n" \
"v_mfma_f32_16x16x32_bf16 v[72:75], a[180:183], a[132:135], v[72:75]\n" \
"buffer_load_dwordx4 v26, s[68:71], s84 offen offset:0 lds\n" \
"v_mfma_f32_16x16x32_bf16 v[72:75], a[244:247], a[196:199], v[72:75]\n" \
"ds_read_b64_tr_b16 a[162:163], v32 offset:1024\n" \
"s_lshl_b32 s43, s44, 13\n" \
"v_mfma_f32_16x16x32_bf16 v[68:71], a[176:179], a[132:135], v[68:71]\n" \
"ds_read_b64_tr_b16 a[224:225], v32 offset:8192\n" \
"s_mulk_i32 s44, 0xe800\n" \
"v_mfma_f32_16x16x32_bf16 v[68:71], a[240:243], a[196:199], v[68:71]\n" \
"ds_read_b128 a[132:135], v28 offset:18432\n" \
"s_lshl_b32 s7, s3, 8\n" \
"v_mfma_f32_16x16x32_bf16 v[84:87], a[176:179], a[136:139], v[84:87]\n" \
"ds_read_b128 a[196:199], v29 offset:18432\n" \
"v_lshl_or_b32 v24, v218, 4, v214\n" \
"v_mfma_f32_16x16x32_bf16 v[84:87], a[240:243], a[200:203], v[84:87]\n" \
"s_addk_i32 s84, 0x7c00\n" \
"v_mfma_f32_16x16x32_bf16 v[88:91], a[180:183], a[136:139], v[88:91]\n" \
"buffer_load_dwordx4 v26, s[68:71], s84 offen offset:1024 lds\n" \
"v_mfma_f32_16x16x32_bf16 v[88:91], a[244:247], a[200:203], v[88:91]\n" \
"s_addk_i32 s84, 0x7c00\n" \
"v_mfma_f32_16x16x32_bf16 v[92:95], a[184:187], a[136:139], v[92:95]\n" \
"buffer_load_dwordx4 v27, s[68:71], s84 offen offset:2048 lds\n" \
"v_mfma_f32_16x16x32_bf16 v[92:95], a[248:251], a[200:203], v[92:95]\n" \
"s_addk_i32 s84, 0x7c00\n" \
"v_mfma_f32_16x16x32_bf16 v[96:99], a[188:191], a[136:139], v[96:99]\n" \
"buffer_load_dwordx4 v27, s[68:71], s84 offen offset:3072 lds\n" \
"v_mfma_f32_16x16x32_bf16 v[96:99], a[252:255], a[200:203], v[96:99]\n" \
"ds_read_b128 a[136:139], v28 offset:20480\n" \
"v_lshl_or_b32 v21, v213, 4, v212\n" \
"v_mfma_f32_16x16x32_bf16 v[112:115], a[188:191], a[140:143], v[112:115]\n" \
"ds_read_b128 a[200:203], v29 offset:20480\n" \
"s_add_i32 s44, s44, 0x17000\n" \
"v_mfma_f32_16x16x32_bf16 v[112:115], a[252:255], a[204:207], v[112:115]\n" \
"s_add_u32 m0, s30, 0x9000\n" \
"s_mov_b32 s84, 0x8280\n" \
"v_mfma_f32_16x16x32_bf16 v[108:111], a[184:187], a[140:143], v[108:111]\n" \
"buffer_load_dwordx4 v22, s[64:67], s84 offen offset:0 lds\n" \
"v_mfma_f32_16x16x32_bf16 v[108:111], a[248:251], a[204:207], v[108:111]\n" \
"s_addk_i32 s84, 0x1c00\n" \
"v_mfma_f32_16x16x32_bf16 v[104:107], a[180:183], a[140:143], v[104:107]\n" \
"buffer_load_dwordx4 v23, s[64:67], s84 offen offset:1024 lds\n" \
"v_mfma_f32_16x16x32_bf16 v[104:107], a[244:247], a[204:207], v[104:107]\n" \
"s_addk_i32 s84, 0x1c00\n" \
"v_mfma_f32_16x16x32_bf16 v[100:103], a[176:179], a[140:143], v[100:103]\n" \
"buffer_load_dwordx4 v22, s[64:67], s84 offen offset:2048 lds\n" \
"v_mfma_f32_16x16x32_bf16 v[100:103], a[240:243], a[204:207], v[100:103]\n" \
"ds_read_b128 a[140:143], v28 offset:22528\n" \
"s_or_b32 s45, s7, 0x27000\n" \
"v_mfma_f32_16x16x32_bf16 v[116:119], a[176:179], a[144:147], v[116:119]\n" \
"ds_read_b128 a[204:207], v29 offset:22528\n" \
"s_or_b32 s46, s4, 0x10000\n" \
"v_mfma_f32_16x16x32_bf16 v[116:119], a[240:243], a[208:211], v[116:119]\n" \
"s_addk_i32 s84, 0x1c00\n" \
"v_mfma_f32_16x16x32_bf16 v[120:123], a[180:183], a[144:147], v[120:123]\n" \
"buffer_load_dwordx4 v23, s[64:67], s84 offen offset:3072 lds\n" \
"v_mfma_f32_16x16x32_bf16 v[120:123], a[244:247], a[208:211], v[120:123]\n" \
"s_add_u32 m0, s31, 0xc000\n" \
"s_mov_b32 s84, 0x280000\n" \
"v_mfma_f32_16x16x32_bf16 v[124:127], a[184:187], a[144:147], v[124:127]\n" \
"buffer_load_dwordx4 v26, s[72:75], s84 offen offset:0 lds\n" \
"v_mfma_f32_16x16x32_bf16 v[124:127], a[248:251], a[208:211], v[124:127]\n" \
"s_addk_i32 s84, 0x7c00\n" \
"v_mfma_f32_16x16x32_bf16 v[128:131], a[188:191], a[144:147], v[128:131]\n" \
"buffer_load_dwordx4 v26, s[72:75], s84 offen offset:1024 lds\n" \
"v_mfma_f32_16x16x32_bf16 v[128:131], a[252:255], a[208:211], v[128:131]\n" \
"ds_read_b128 a[144:147], v28 offset:0\n" \
"s_add_i32 s48, s6, 0x3000000\n" \
"v_mfma_f32_16x16x32_bf16 v[144:147], a[188:191], a[148:151], v[144:147]\n" \
"ds_read_b128 a[208:211], v29 offset:0\n" \
"s_add_i32 s49, s5, 0x3004000\n" \
"v_mfma_f32_16x16x32_bf16 v[144:147], a[252:255], a[212:215], v[144:147]\n" \
"ds_read_b128 v[200:203], v199 offset:0\n" \
"s_add_i32 s52, s52, 0x1010000\n" \
"v_mfma_f32_16x16x32_bf16 v[140:143], a[184:187], a[148:151], v[140:143]\n" \
"ds_read_b128 v[204:207], v199 offset:4096\n" \
"v_mfma_f32_16x16x32_bf16 v[140:143], a[248:251], a[212:215], v[140:143]\n" \
"ds_read_b64_tr_b16 a[226:227], v32 offset:9216\n" \
"v_mfma_f32_16x16x32_bf16 v[136:139], a[180:183], a[148:151], v[136:139]\n" \
"ds_read_b64_tr_b16 a[164:165], v33 offset:0\n" \
"v_mfma_f32_16x16x32_bf16 v[136:139], a[244:247], a[212:215], v[136:139]\n" \
"ds_read_b128 v[208:211], v199 offset:8192\n" \
"v_mfma_f32_16x16x32_bf16 v[132:135], a[176:179], a[148:151], v[132:135]\n" \
"ds_read_b64_tr_b16 a[166:167], v33 offset:1024\n" \
"v_mfma_f32_16x16x32_bf16 v[132:135], a[240:243], a[212:215], v[132:135]\n" \
"ds_read_b128 a[148:151], v28 offset:2048\n" \
"v_mfma_f32_16x16x32_bf16 v[148:151], a[176:179], a[152:155], v[148:151]\n" \
"ds_read_b128 a[212:215], v29 offset:2048\n" \
"v_mfma_f32_16x16x32_bf16 v[148:151], a[240:243], a[216:219], v[148:151]\n" \
"ds_read_b64_tr_b16 a[228:229], v33 offset:8192\n" \
"v_mfma_f32_16x16x32_bf16 v[152:155], a[180:183], a[152:155], v[152:155]\n" \
"ds_read_b64_tr_b16 a[230:231], v33 offset:9216\n" \
"v_mfma_f32_16x16x32_bf16 v[152:155], a[244:247], a[216:219], v[152:155]\n" \
"ds_read_b64_tr_b16 a[168:169], v34 offset:0\n" \
"v_mfma_f32_16x16x32_bf16 v[156:159], a[184:187], a[152:155], v[156:159]\n" \
"ds_read_b64_tr_b16 a[170:171], v34 offset:1024\n" \
"v_mfma_f32_16x16x32_bf16 v[156:159], a[248:251], a[216:219], v[156:159]\n" \
"ds_read_b64_tr_b16 a[232:233], v34 offset:8192\n" \
"v_mfma_f32_16x16x32_bf16 v[160:163], a[188:191], a[152:155], v[160:163]\n" \
"ds_read_b64_tr_b16 a[234:235], v34 offset:9216\n" \
"v_mfma_f32_16x16x32_bf16 v[160:163], a[252:255], a[216:219], v[160:163]\n" \
"ds_read_b128 a[152:155], v28 offset:4096\n" \
"v_mfma_f32_16x16x32_bf16 v[176:179], a[188:191], a[156:159], v[176:179]\n" \
"ds_read_b128 a[216:219], v29 offset:4096\n" \
"v_mfma_f32_16x16x32_bf16 v[176:179], a[252:255], a[220:223], v[176:179]\n" \
"ds_read_b64_tr_b16 a[172:173], v35 offset:0\n" \
"v_mfma_f32_16x16x32_bf16 v[172:175], a[184:187], a[156:159], v[172:175]\n" \
"ds_read_b64_tr_b16 a[174:175], v35 offset:1024\n" \
"v_mfma_f32_16x16x32_bf16 v[172:175], a[248:251], a[220:223], v[172:175]\n" \
"ds_read_b64_tr_b16 a[236:237], v35 offset:8192\n" \
"v_mfma_f32_16x16x32_bf16 v[168:171], a[180:183], a[156:159], v[168:171]\n" \
"ds_read_b64_tr_b16 a[238:239], v35 offset:9216\n" \
"v_mfma_f32_16x16x32_bf16 v[168:171], a[244:247], a[220:223], v[168:171]\n" \
"v_mfma_f32_16x16x32_bf16 v[164:167], a[176:179], a[156:159], v[164:167]\n" \
"v_mfma_f32_16x16x32_bf16 v[164:167], a[240:243], a[220:223], v[164:167]\n" \
"ds_read_b128 a[156:159], v28 offset:6144\n" \
"ds_read_b128 a[220:223], v29 offset:6144\n" \
"s_waitcnt lgkmcnt(15)\n" \
"v_mfma_f32_16x16x32_bf16 a[0:3], a[160:163], a[128:131], a[0:3]\n" \
"s_addk_i32 s84, 0x7c00\n" \
"v_mfma_f32_16x16x32_bf16 a[0:3], a[224:227], a[192:195], a[0:3]\n" \
"buffer_load_dwordx4 v27, s[72:75], s84 offen offset:2048 lds\n" \
"v_mfma_f32_16x16x32_bf16 a[4:7], a[164:167], a[128:131], a[4:7]\n" \
"ds_read_b64_tr_b16 a[176:177], v36 offset:0\n" \
"s_waitcnt lgkmcnt(13)\n" \
"v_mfma_f32_16x16x32_bf16 a[4:7], a[228:231], a[192:195], a[4:7]\n" \
"s_addk_i32 s84, 0x7c00\n" \
"s_waitcnt lgkmcnt(11)\n" \
"v_mfma_f32_16x16x32_bf16 a[8:11], a[168:171], a[128:131], a[8:11]\n" \
"buffer_load_dwordx4 v27, s[72:75], s84 offen offset:3072 lds\n" \
"s_waitcnt lgkmcnt(9)\n" \
"v_mfma_f32_16x16x32_bf16 a[8:11], a[232:235], a[192:195], a[8:11]\n" \
"ds_read_b64_tr_b16 a[178:179], v36 offset:1024\n" \
"s_waitcnt lgkmcnt(6)\n" \
"v_mfma_f32_16x16x32_bf16 a[12:15], a[172:175], a[128:131], a[12:15]\n" \
"ds_read_b64_tr_b16 a[240:241], v36 offset:8192\n" \
"s_waitcnt lgkmcnt(5)\n" \
"v_mfma_f32_16x16x32_bf16 a[12:15], a[236:239], a[192:195], a[12:15]\n" \
"v_mfma_f32_16x16x32_bf16 a[28:31], a[172:175], a[132:135], a[28:31]\n" \
"v_mfma_f32_16x16x32_bf16 a[28:31], a[236:239], a[196:199], a[28:31]\n" \
"ds_read_b64_tr_b16 a[242:243], v36 offset:9216\n" \
"v_mfma_f32_16x16x32_bf16 a[24:27], a[168:171], a[132:135], a[24:27]\n" \
"v_mfma_f32_16x16x32_bf16 a[24:27], a[232:235], a[196:199], a[24:27]\n" \
"v_mfma_f32_16x16x32_bf16 a[20:23], a[164:167], a[132:135], a[20:23]\n" \
"v_mfma_f32_16x16x32_bf16 a[20:23], a[228:231], a[196:199], a[20:23]\n" \
"ds_read_b64_tr_b16 a[180:181], v37 offset:0\n" \
"v_mfma_f32_16x16x32_bf16 a[16:19], a[160:163], a[132:135], a[16:19]\n" \
"v_mfma_f32_16x16x32_bf16 a[16:19], a[224:227], a[196:199], a[16:19]\n" \
"v_mfma_f32_16x16x32_bf16 a[32:35], a[160:163], a[136:139], a[32:35]\n" \
"ds_read_b64_tr_b16 a[182:183], v37 offset:1024\n" \
"v_mfma_f32_16x16x32_bf16 a[32:35], a[224:227], a[200:203], a[32:35]\n" \
"v_mfma_f32_16x16x32_bf16 a[36:39], a[164:167], a[136:139], a[36:39]\n" \
"v_mfma_f32_16x16x32_bf16 a[36:39], a[228:231], a[200:203], a[36:39]\n" \
"buffer_store_dwordx4 v[200:203], v20, s[80:83], s35 offen sc1\n" \
".fill (L_pb + 12000 - .) / 4, 4, 0xBF800000\n" \
"L_pp3:\n" \
"s_cbranch_execz L_pret\n" \
"v_mfma_f32_16x16x32_bf16 a[40:43], a[168:171], a[136:139], a[40:43]\n" \
"s_mov_b32 m0, s32\n" \
"ds_read_b64_tr_b16 a[244:245], v37 offset:8192\n" \
"v_mfma_f32_16x16x32_bf16 a[40:43], a[232:235], a[200:203], a[40:43]\n" \
"s_mov_b32 exec_lo, 0x0\n" \
"s_mov_b32 exec_hi, 0xffffffff\n" \
"global_load_lds_dword v[188:189], off\n" \
"s_mov_b64 exec, -1\n" \
"s_nop 0\n" \
"v_mfma_f32_16x16x32_bf16 a[44:47], a[172:175], a[136:139], a[44:47]\n" \
"ds_read_b64_tr_b16 a[246:247], v37 offset:9216\n" \
"v_mfma_f32_16x16x32_bf16 a[44:47], a[236:239], a[200:203], a[44:47]\n" \
"v_mfma_f32_16x16x32_bf16 a[60:63], a[172:175], a[140:143], a[60:63]\n" \
"v_mfma_f32_16x16x32_bf16 a[60:63], a[236:239], a[204:207], a[60:63]\n" \
"v_mfma_f32_16x16x32_bf16 a[56:59], a[168:171], a[140:143], a[56:59]\n" \
"ds_read_b64_tr_b16 a[184:185], v38 offset:0\n" \
"v_mfma_f32_16x16x32_bf16 a[56:59], a[232:235], a[204:207], a[56:59]\n" \
"v_mfma_f32_16x16x32_bf16 a[52:55], a[164:167], a[140:143], a[52:55]\n" \
"buffer_store_dwordx4 v[204:207], v20, s[80:83], s88 offen sc1\n" \
"v_mfma_f32_16x16x32_bf16 a[52:55], a[228:231], a[204:207], a[52:55]\n" \
"ds_read_b64_tr_b16 a[186:187], v38 offset:1024\n" \
"v_mfma_f32_16x16x32_bf16 a[48:51], a[160:163], a[140:143], a[48:51]\n" \
"v_mfma_f32_16x16x32_bf16 a[48:51], a[224:227], a[204:207], a[48:51]\n" \
"v_mfma_f32_16x16x32_bf16 a[64:67], a[160:163], a[144:147], a[64:67]\n" \
"ds_read_b64_tr_b16 a[248:249], v38 offset:8192\n" \
"v_mfma_f32_16x16x32_bf16 a[64:67], a[224:227], a[208:211], a[64:67]\n" \
"v_mfma_f32_16x16x32_bf16 a[68:71], a[164:167], a[144:147], a[68:71]\n" \
"v_mfma_f32_16x16x32_bf16 a[68:71], a[228:231], a[208:211], a[68:71]\n" \
"ds_read_b64_tr_b16 a[250:251], v38 offset:9216\n" \
"v_mfma_f32_16x16x32_bf16 a[72:75], a[168:171], a[144:147], a[72:75]\n" \
"v_mfma_f32_16x16x32_bf16 a[72:75], a[232:235], a[208:211], a[72:75]\n" \
"v_mfma_f32_16x16x32_bf16 a[76:79], a[172:175], a[144:147], a[76:79]\n" \
"buffer_store_dwordx4 v[208:211], v20, s[80:83], s36 offen sc1\n" \
"v_mfma_f32_16x16x32_bf16 a[76:79], a[236:239], a[208:211], a[76:79]\n" \
"ds_read_b64_tr_b16 a[188:189], v39 offset:0\n" \
"v_mfma_f32_16x16x32_bf16 a[92:95], a[172:175], a[148:151], a[92:95]\n" \
"v_mfma_f32_16x16x32_bf16 a[92:95], a[236:239], a[212:215], a[92:95]\n" \
"v_mfma_f32_16x16x32_bf16 a[88:91], a[168:171], a[148:151], a[88:91]\n" \
"ds_read_b64_tr_b16 a[190:191], v39 offset:1024\n" \
"v_mfma_f32_16x16x32_bf16 a[88:91], a[232:235], a[212:215], a[88:91]\n" \
"v_mfma_f32_16x16x32_bf16 a[84:87], a[164:167], a[148:151], a[84:87]\n" \
"v_mfma_f32_16x16x32_bf16 a[84:87], a[228:231], a[212:215], a[84:87]\n" \
"ds_read_b64_tr_b16 a[252:253], v39 offset:8192\n" \
"v_mfma_f32_16x16x32_bf16 a[80:83], a[160:163], a[148:151], a[80:83]\n" \
"v_mfma_f32_16x16x32_bf16 a[80:83], a[224:227], a[212:215], a[80:83]\n" \
"v_mfma_f32_16x16x32_bf16 a[96:99], a[160:163], a[152:155], a[96:99]\n" \
"ds_read_b64_tr_b16 a[254:255], v39 offset:9216\n" \
"v_mfma_f32_16x16x32_bf16 a[96:99], a[224:227], a[216:219], a[96:99]\n" \
"v_mfma_f32_16x16x32_bf16 a[100:103], a[164:167], a[152:155], a[100:103]\n" \
"v_mfma_f32_16x16x32_bf16 a[100:103], a[228:231], a[216:219], a[100:103]\n" \
"v_mfma_f32_16x16x32_bf16 a[104:107], a[168:171], a[152:155], a[104:107]\n" \
"v_mfma_f32_16x16x32_bf16 a[104:107], a[232:235], a[216:219], a[104:107]\n" \
"v_mfma_f32_16x16x32_bf16 a[108:111], a[172:175], a[152:155], a[108:111]\n" \
"v_mfma_f32_16x16x32_bf16 a[108:111], a[236:239], a[216:219], a[108:111]\n" \
"s_waitcnt lgkmcnt(15)\n" \
"v_mfma_f32_16x16x32_bf16 a[124:127], a[172:175], a[156:159], a[124:127]\n" \
"v_mfma_f32_16x16x32_bf16 a[124:127], a[236:239], a[220:223], a[124:127]\n" \
"v_mfma_f32_16x16x32_bf16 a[120:123], a[168:171], a[156:159], a[120:123]\n" \
"v_mfma_f32_16x16x32_bf16 a[120:123], a[232:235], a[220:223], a[120:123]\n" \
"v_mfma_f32_16x16x32_bf16 a[116:119], a[164:167], a[156:159], a[116:119]\n" \
"v_mfma_f32_16x16x32_bf16 a[116:119], a[228:231], a[220:223], a[116:119]\n" \
"v_mfma_f32_16x16x32_bf16 a[112:115], a[160:163], a[156:159], a[112:115]\n" \
"v_mfma_f32_16x16x32_bf16 a[112:115], a[224:227], a[220:223], a[112:115]\n" \
"s_waitcnt vmcnt(4) lgkmcnt(0)\n" \
"s_barrier\n" \
"v_mfma_f32_16x16x32_bf16 v[52:55], a[176:179], a[128:131], v[52:55]\n" \
"s_add_u32 m0, s30, 0x0\n" \
"s_movk_i32 s84, 0x300\n" \
"v_mfma_f32_16x16x32_bf16 v[52:55], a[240:243], a[192:195], v[52:55]\n" \
"buffer_load_dwordx4 v22, s[64:67], s84 offen offset:0 lds\n" \
"v_mfma_f32_16x16x32_bf16 v[56:59], a[180:183], a[128:131], v[56:59]\n" \
"ds_read_b64_tr_b16 a[160:161], v32 offset:32768\n" \
"v_mfma_f32_16x16x32_bf16 v[56:59], a[244:247], a[192:195], v[56:59]\n" \
"s_movk_i32 s84, 0x1f00\n" \
"v_mfma_f32_16x16x32_bf16 v[60:63], a[184:187], a[128:131], v[60:63]\n" \
"buffer_load_dwordx4 v23, s[64:67], s84 offen offset:1024 lds\n" \
"v_mfma_f32_16x16x32_bf16 v[60:63], a[248:251], a[192:195], v[60:63]\n" \
"ds_read_b64_tr_b16 a[162:163], v32 offset:33792\n" \
"v_mfma_f32_16x16x32_bf16 v[64:67], a[188:191], a[128:131], v[64:67]\n" \
"ds_read_b64_tr_b16 a[224:225], v32 offset:40960\n" \
"v_mfma_f32_16x16x32_bf16 v[64:67], a[252:255], a[192:195], v[64:67]\n" \
"ds_read_b128 a[128:131], v28 offset:49152\n" \
"v_mfma_f32_16x16x32_bf16 v[80:83], a[188:191], a[132:135], v[80:83]\n" \
"ds_read_b128 a[192:195], v29 offset:49152\n" \
"v_mfma_f32_16x16x32_bf16 v[80:83], a[252:255], a[196:199], v[80:83]\n" \
"s_movk_i32 s84, 0x3b00\n" \
"v_mfma_f32_16x16x32_bf16 v[76:79], a[184:187], a[132:135], v[76:79]\n" \
"buffer_load_dwordx4 v22, s[64:67], s84 offen offset:2048 lds\n" \
"v_mfma_f32_16x16x32_bf16 v[76:79], a[248:251], a[196:199], v[76:79]\n" \
"s_movk_i32 s84, 0x5700\n" \
"v_mfma_f32_16x16x32_bf16 v[72:75], a[180:183], a[132:135], v[72:75]\n" \
"buffer_load_dwordx4 v23, s[64:67], s84 offen offset:3072 lds\n" \
"v_mfma_f32_16x16x32_bf16 v[72:75], a[244:247], a[196:199], v[72:75]\n" \
"s_add_u32 m0, s31, 0x0\n" \
"s_mov_b32 s84, 0x300000\n" \
"v_mfma_f32_16x16x32_bf16 v[68:71], a[176:179], a[132:135], v[68:71]\n" \
"buffer_load_dwordx4 v26, s[68:71], s84 offen offset:0 lds\n" \
"v_mfma_f32_16x16x32_bf16 v[68:71], a[240:243], a[196:199], v[68:71]\n" \
"ds_read_b128 a[132:135], v28 offset:51200\n" \
"v_mfma_f32_16x16x32_bf16 v[84:87], a[176:179], a[136:139], v[84:87]\n" \
"ds_read_b128 a[196:199], v29 offset:51200\n" \
"v_mfma_f32_16x16x32_bf16 v[84:87], a[240:243], a[200:203], v[84:87]\n" \
"s_addk_i32 s84, 0x7c00\n" \
"v_mfma_f32_16x16x32_bf16 v[88:91], a[180:183], a[136:139], v[88:91]\n" \
"buffer_load_dwordx4 v26, s[68:71], s84 offen offset:1024 lds\n" \
"v_mfma_f32_16x16x32_bf16 v[88:91], a[244:247], a[200:203], v[88:91]\n" \
"ds_read_b64_tr_b16 a[226:227], v32 offset:41984\n" \
"v_mfma_f32_16x16x32_bf16 v[92:95], a[184:187], a[136:139], v[92:95]\n" \
"s_addk_i32 s84, 0x7c00\n" \
"v_mfma_f32_16x16x32_bf16 v[92:95], a[248:251], a[200:203], v[92:95]\n" \
"buffer_load_dwordx4 v27, s[68:71], s84 offen offset:2048 lds\n" \
"v_mfma_f32_16x16x32_bf16 v[96:99], a[188:191], a[136:139], v[96:99]\n" \
"ds_read_b64_tr_b16 a[164:165], v33 offset:32768\n" \
"v_mfma_f32_16x16x32_bf16 v[96:99], a[252:255], a[200:203], v[96:99]\n" \
"ds_read_b128 a[136:139], v28 offset:53248\n" \
"v_mfma_f32_16x16x32_bf16 v[112:115], a[188:191], a[140:143], v[112:115]\n" \
"ds_read_b128 a[200:203], v29 offset:53248\n" \
"v_mfma_f32_16x16x32_bf16 v[112:115], a[252:255], a[204:207], v[112:115]\n" \
"s_addk_i32 s84, 0x7c00\n" \
"v_mfma_f32_16x16x32_bf16 v[108:111], a[184:187], a[140:143], v[108:111]\n" \
"buffer_load_dwordx4 v27, s[68:71], s84 offen offset:3072 lds\n" \
"v_mfma_f32_16x16x32_bf16 v[108:111], a[248:251], a[204:207], v[108:111]\n" \
"s_add_u32 m0, s30, 0x1000\n" \
"s_mov_b32 s84, 0x8300\n" \
"v_mfma_f32_16x16x32_bf16 v[104:107], a[180:183], a[140:143], v[104:107]\n" \
"buffer_load_dwordx4 v22, s[64:67], s84 offen offset:0 lds\n" \
"v_mfma_f32_16x16x32_bf16 v[104:107], a[244:247], a[204:207], v[104:107]\n" \
"ds_read_b64_tr_b16 a[166:167], v33 offset:33792\n" \
"v_mfma_f32_16x16x32_bf16 v[100:103], a[176:179], a[140:143], v[100:103]\n" \
"ds_read_b64_tr_b16 a[228:229], v33 offset:40960\n" \
"v_mfma_f32_16x16x32_bf16 v[100:103], a[240:243], a[204:207], v[100:103]\n" \
"ds_read_b128 a[140:143], v28 offset:55296\n" \
"v_mfma_f32_16x16x32_bf16 v[116:119], a[176:179], a[144:147], v[116:119]\n" \
"ds_read_b128 a[204:207], v29 offset:55296\n" \
"v_mfma_f32_16x16x32_bf16 v[116:119], a[240:243], a[208:211], v[116:119]\n" \
"s_addk_i32 s84, 0x1c00\n" \
"v_mfma_f32_16x16x32_bf16 v[120:123], a[180:183], a[144:147], v[120:123]\n" \
"buffer_load_dwordx4 v23, s[64:67], s84 offen offset:1024 lds\n" \
"v_mfma_f32_16x16x32_bf16 v[120:123], a[244:247], a[208:211], v[120:123]\n" \
"s_addk_i32 s84, 0x1c00\n" \
"v_mfma_f32_16x16x32_bf16 v[124:127], a[184:187], a[144:147], v[124:127]\n" \
"buffer_load_dwordx4 v22, s[64:67], s84 offen offset:2048 lds\n" \
"v_mfma_f32_16x16x32_bf16 v[124:127], a[248:251], a[208:211], v[124:127]\n" \
"s_addk_i32 s84, 0x1c00\n" \
"s_nop 0\n" \
"v_mfma_f32_16x16x32_bf16 v[128:131], a[188:191], a[144:147], v[128:131]\n" \
"buffer_load_dwordx4 v23, s[64:67], s84 offen offset:3072 lds\n" \
"v_mfma_f32_16x16x32_bf16 v[128:131], a[252:255], a[208:211], v[128:131]\n" \
"ds_read_b128 a[144:147], v28 offset:32768\n" \
"v_mfma_f32_16x16x32_bf16 v[144:147], a[188:191], a[148:151], v[144:147]\n" \
"ds_read_b128 a[208:211], v29 offset:32768\n" \
"v_mfma_f32_16x16x32_bf16 v[144:147], a[252:255], a[212:215], v[144:147]\n" \
"s_add_u32 m0, s31, 0x4000\n" \
"s_mov_b32 s84, 0x300000\n" \
"v_mfma_f32_16x16x32_bf16 v[140:143], a[184:187], a[148:151], v[140:143]\n" \
"buffer_load_dwordx4 v26, s[72:75], s84 offen offset:0 lds\n" \
"v_mfma_f32_16x16x32_bf16 v[140:143], a[248:251], a[212:215], v[140:143]\n" \
"ds_read_b64_tr_b16 a[230:231], v33 offset:41984\n" \
"v_mfma_f32_16x16x32_bf16 v[136:139], a[180:183], a[148:151], v[136:139]\n" \
"s_addk_i32 s84, 0x7c00\n" \
"s_nop 0\n" \
"v_mfma_f32_16x16x32_bf16 v[136:139], a[244:247], a[212:215], v[136:139]\n" \
"buffer_load_dwordx4 v26, s[72:75], s84 offen offset:1024 lds\n" \
"v_mfma_f32_16x16x32_bf16 v[132:135], a[176:179], a[148:151], v[132:135]\n" \
"ds_read_b64_tr_b16 a[168:169], v34 offset:32768\n" \
"v_mfma_f32_16x16x32_bf16 v[132:135], a[240:243], a[212:215], v[132:135]\n" \
"ds_read_b128 a[148:151], v28 offset:34816\n" \
"v_mfma_f32_16x16x32_bf16 v[148:151], a[176:179], a[152:155], v[148:151]\n" \
"ds_read_b128 a[212:215], v29 offset:34816\n" \
"v_mfma_f32_16x16x32_bf16 v[148:151], a[240:243], a[216:219], v[148:151]\n" \
"ds_read_b64_tr_b16 a[170:171], v34 offset:33792\n" \
"v_mfma_f32_16x16x32_bf16 v[152:155], a[180:183], a[152:155], v[152:155]\n" \
"ds_read_b64_tr_b16 a[232:233], v34 offset:40960\n" \
"v_mfma_f32_16x16x32_bf16 v[152:155], a[244:247], a[216:219], v[152:155]\n" \
"ds_read_b64_tr_b16 a[234:235], v34 offset:41984\n" \
"v_mfma_f32_16x16x32_bf16 v[156:159], a[184:187], a[152:155], v[156:159]\n" \
"ds_read_b64_tr_b16 a[172:173], v35 offset:32768\n" \
"v_mfma_f32_16x16x32_bf16 v[156:159], a[248:251], a[216:219], v[156:159]\n" \
"ds_read_b64_tr_b16 a[174:175], v35 offset:33792\n" \
"v_mfma_f32_16x16x32_bf16 v[160:163], a[188:191], a[152:155], v[160:163]\n" \
"ds_read_b64_tr_b16 a[236:237], v35 offset:40960\n" \
"v_mfma_f32_16x16x32_bf16 v[160:163], a[252:255], a[216:219], v[160:163]\n" \
"ds_read_b128 a[152:155], v28 offset:36864\n" \
"v_mfma_f32_16x16x32_bf16 v[176:179], a[188:191], a[156:159], v[176:179]\n" \
"ds_read_b128 a[216:219], v29 offset:36864\n" \
"v_mfma_f32_16x16x32_bf16 v[176:179], a[252:255], a[220:223], v[176:179]\n" \
"ds_read_b64_tr_b16 a[238:239], v35 offset:41984\n" \
"v_mfma_f32_16x16x32_bf16 v[172:175], a[184:187], a[156:159], v[172:175]\n" \
"v_mfma_f32_16x16x32_bf16 v[172:175], a[248:251], a[220:223], v[172:175]\n" \
"v_mfma_f32_16x16x32_bf16 v[168:171], a[180:183], a[156:159], v[168:171]\n" \
"v_mfma_f32_16x16x32_bf16 v[168:171], a[244:247], a[220:223], v[168:171]\n" \
"v_mfma_f32_16x16x32_bf16 v[164:167], a[176:179], a[156:159], v[164:167]\n" \
"v_mfma_f32_16x16x32_bf16 v[164:167], a[240:243], a[220:223], v[164:167]\n" \
"ds_read_b128 a[156:159], v28 offset:38912\n" \
"ds_read_b128 a[220:223], v29 offset:38912\n" \
"s_waitcnt lgkmcnt(15)\n" \
"v_mfma_f32_16x16x32_bf16 a[0:3], a[160:163], a[128:131], a[0:3]\n" \
"s_addk_i32 s84, 0x7c00\n" \
"v_mfma_f32_16x16x32_bf16 a[0:3], a[224:227], a[192:195], a[0:3]\n" \
"buffer_load_dwordx4 v27, s[72:75], s84 offen offset:2048 lds\n" \
"v_mfma_f32_16x16x32_bf16 a[4:7], a[164:167], a[128:131], a[4:7]\n" \
"ds_read_b64_tr_b16 a[176:177], v36 offset:32768\n" \
"s_waitcnt lgkmcnt(15)\n" \
"v_mfma_f32_16x16x32_bf16 a[4:7], a[228:231], a[192:195], a[4:7]\n" \
"s_addk_i32 s84, 0x7c00\n" \
"s_waitcnt lgkmcnt(11)\n" \
"v_mfma_f32_16x16x32_bf16 a[8:11], a[168:171], a[128:131], a[8:11]\n" \
"buffer_load_dwordx4 v27, s[72:75], s84 offen offset:3072 lds\n" \
"s_waitcnt lgkmcnt(9)\n" \
"v_mfma_f32_16x16x32_bf16 a[8:11], a[232:235], a[192:195], a[8:11]\n" \
"ds_read_b64_tr_b16 a[178:179], v36 offset:33792\n" \
"s_waitcnt lgkmcnt(8)\n" \
"v_mfma_f32_16x16x32_bf16 a[12:15], a[172:175], a[128:131], a[12:15]\n" \
"ds_read_b64_tr_b16 a[240:241], v36 offset:40960\n" \
"s_waitcnt lgkmcnt(5)\n" \
"v_mfma_f32_16x16x32_bf16 a[12:15], a[236:239], a[192:195], a[12:15]\n" \
"v_mfma_f32_16x16x32_bf16 a[28:31], a[172:175], a[132:135], a[28:31]\n" \
"v_mfma_f32_16x16x32_bf16 a[28:31], a[236:239], a[196:199], a[28:31]\n" \
"ds_read_b64_tr_b16 a[242:243], v36 offset:41984\n" \
"v_mfma_f32_16x16x32_bf16 a[24:27], a[168:171], a[132:135], a[24:27]\n" \
"v_mfma_f32_16x16x32_bf16 a[24:27], a[232:235], a[196:199], a[24:27]\n" \
"v_mfma_f32_16x16x32_bf16 a[20:23], a[164:167], a[132:135], a[20:23]\n" \
"v_mfma_f32_16x16x32_bf16 a[20:23], a[228:231], a[196:199], a[20:23]\n" \
"ds_read_b64_tr_b16 a[180:181], v37 offset:32768\n" \
"v_mfma_f32_16x16x32_bf16 a[16:19], a[160:163], a[132:135], a[16:19]\n" \
"v_mfma_f32_16x16x32_bf16 a[16:19], a[224:227], a[196:199], a[16:19]\n" \
"v_mfma_f32_16x16x32_bf16 a[32:35], a[160:163], a[136:139], a[32:35]\n" \
"ds_read_b64_tr_b16 a[182:183], v37 offset:33792\n" \
"v_mfma_f32_16x16x32_bf16 a[32:35], a[224:227], a[200:203], a[32:35]\n" \
"v_mfma_f32_16x16x32_bf16 a[36:39], a[164:167], a[136:139], a[36:39]\n" \
"v_lshlrev_b32_e32 v45, 3, v20\n" \
"v_add_u32_e32 v45, s31, v45\n" \
"v_mfma_f32_16x16x32_bf16 a[36:39], a[228:231], a[200:203], a[36:39]\n" \
"s_getpc_b64 s[86:87]\n" \
"s_sub_u32 s86, s86, 0xf800\n" \
"s_subb_u32 s87, s87, 0\n" \
"s_mov_b32 m0, s32\n" \
"s_nop 0\n" \
"v_mfma_f32_16x16x32_bf16 a[40:43], a[168:171], a[136:139], a[40:43]\n" \
"global_load_lds_dword v45, s[86:87]\n" \
"v_mfma_f32_16x16x32_bf16 a[40:43], a[232:235], a[200:203], a[40:43]\n" \
"ds_read_b64_tr_b16 a[244:245], v37 offset:40960\n" \
"v_mfma_f32_16x16x32_bf16 a[44:47], a[172:175], a[136:139], a[44:47]\n" \
"ds_read_b64_tr_b16 a[246:247], v37 offset:41984\n" \
"v_mfma_f32_16x16x32_bf16 a[44:47], a[236:239], a[200:203], a[44:47]\n" \
"v_mfma_f32_16x16x32_bf16 a[60:63], a[172:175], a[140:143], a[60:63]\n" \
"v_mfma_f32_16x16x32_bf16 a[60:63], a[236:239], a[204:207], a[60:63]\n" \
"v_mfma_f32_16x16x32_bf16 a[56:59], a[168:171], a[140:143], a[56:59]\n" \
"ds_read_b64_tr_b16 a[184:185], v38 offset:32768\n" \
"v_mfma_f32_16x16x32_bf16 a[56:59], a[232:235], a[204:207], a[56:59]\n" \
"v_mfma_f32_16x16x32_bf16 a[52:55], a[164:167], a[140:143], a[52:55]\n" \
"v_mfma_f32_16x16x32_bf16 a[52:55], a[228:231], a[204:207], a[52:55]\n" \
"ds_read_b64_tr_b16 a[186:187], v38 offset:33792\n" \
"v_mfma_f32_16x16x32_bf16 a[48:51], a[160:163], a[140:143], a[48:51]\n" \
"v_mfma_f32_16x16x32_bf16 a[48:51], a[224:227], a[204:207], a[48:51]\n" \
"v_mfma_f32_16x16x32_bf16 a[64:67], a[160:163], a[144:147], a[64:67]\n" \
"ds_read_b64_tr_b16 a[248:249], v38 offset:40960\n" \
"v_mfma_f32_16x16x32_bf16 a[64:67], a[224:227], a[208:211], a[64:67]\n" \
"v_mfma_f32_16x16x32_bf16 a[68:71], a[164:167], a[144:147], a[68:71]\n" \
"v_mfma_f32_16x16x32_bf16 a[68:71], a[228:231], a[208:211], a[68:71]\n" \
"ds_read_b64_tr_b16 a[250:251], v38 offset:41984\n" \
"v_mfma_f32_16x16x32_bf16 a[72:75], a[168:171], a[144:147], a[72:75]\n" \
"v_mfma_f32_16x16x32_bf16 a[72:75], a[232:235], a[208:211], a[72:75]\n" \
"v_mfma_f32_16x16x32_bf16 a[76:79], a[172:175], a[144:147], a[76:79]\n" \
"v_mfma_f32_16x16x32_bf16 a[76:79], a[236:239], a[208:211], a[76:79]\n" \
"ds_read_b64_tr_b16 a[188:189], v39 offset:32768\n" \
"v_mfma_f32_16x16x32_bf16 a[92:95], a[172:175], a[148:151], a[92:95]\n" \
"v_mfma_f32_16x16x32_bf16 a[92:95], a[236:239], a[212:215], a[92:95]\n" \
"v_mfma_f32_16x16x32_bf16 a[88:91], a[168:171], a[148:151], a[88:91]\n" \
"ds_read_b64_tr_b16 a[190:191], v39 offset:33792\n" \
"v_mfma_f32_16x16x32_bf16 a[88:91], a[232:235], a[212:215], a[88:91]\n" \
"v_mfma_f32_16x16x32_bf16 a[84:87], a[164:167], a[148:151], a[84:87]\n" \
"v_mfma_f32_16x16x32_bf16 a[84:87], a[228:231], a[212:215], a[84:87]\n" \
"ds_read_b64_tr_b16 a[252:253], v39 offset:40960\n" \
"v_mfma_f32_16x16x32_bf16 a[80:83], a[160:163], a[148:151], a[80:83]\n" \
"v_mfma_f32_16x16x32_bf16 a[80:83], a[224:227], a[212:215], a[80:83]\n" \
"v_mfma_f32_16x16x32_bf16 a[96:99], a[160:163], a[152:155], a[96:99]\n" \
"ds_read_b64_tr_b16 a[254:255], v39 offset:41984\n" \
"v_mfma_f32_16x16x32_bf16 a[96:99], a[224:227], a[216:219], a[96:99]\n" \
"v_mfma_f32_16x16x32_bf16 a[100:103], a[164:167], a[152:155], a[100:103]\n" \
"v_mfma_f32_16x16x32_bf16 a[100:103], a[228:231], a[216:219], a[100:103]\n" \
"v_mfma_f32_16x16x32_bf16 a[104:107], a[168:171], a[152:155], a[104:107]\n" \
"v_mfma_f32_16x16x32_bf16 a[104:107], a[232:235], a[216:219], a[104:107]\n" \
"v_mfma_f32_16x16x32_bf16 a[108:111], a[172:175], a[152:155], a[108:111]\n" \
"v_mfma_f32_16x16x32_bf16 a[108:111], a[236:239], a[216:219], a[108:111]\n" \
"s_waitcnt lgkmcnt(15)\n" \
"v_mfma_f32_16x16x32_bf16 a[124:127], a[172:175], a[156:159], a[124:127]\n" \
"v_mfma_f32_16x16x32_bf16 a[124:127], a[236:239], a[220:223], a[124:127]\n" \
"v_mfma_f32_16x16x32_bf16 a[120:123], a[168:171], a[156:159], a[120:123]\n" \
"v_mfma_f32_16x16x32_bf16 a[120:123], a[232:235], a[220:223], a[120:123]\n" \
"v_mfma_f32_16x16x32_bf16 a[116:119], a[164:167], a[156:159], a[116:119]\n" \
"v_mfma_f32_16x16x32_bf16 a[116:119], a[228:231], a[220:223], a[116:119]\n" \
"v_mfma_f32_16x16x32_bf16 a[112:115], a[160:163], a[156:159], a[112:115]\n" \
"v_mfma_f32_16x16x32_bf16 a[112:115], a[224:227], a[220:223], a[112:115]\n" \
"s_waitcnt vmcnt(1) lgkmcnt(0)\n" \
"s_barrier\n" \
"v_mov_b32_e32 v45, s62\n" \
"s_mov_b64 exec, 1\n" \
"buffer_store_dword v45, off, s[80:83], s37 offset:128 sc1\n" \
"s_mov_b64 exec, -1\n" \
"v_mfma_f32_16x16x32_bf16 v[52:55], a[176:179], a[128:131], v[52:55]\n" \
"s_add_u32 m0, s30, 0x8000\n" \
"s_movk_i32 s84, 0x380\n" \
"v_mfma_f32_16x16x32_bf16 v[52:55], a[240:243], a[192:195], v[52:55]\n" \
"buffer_load_dwordx4 v22, s[64:67], s84 offen offset:0 lds\n" \
"v_mfma_f32_16x16x32_bf16 v[56:59], a[180:183], a[128:131], v[56:59]\n" \
"ds_read_b64_tr_b16 a[160:161], v32 offset:0\n" \
"v_mfma_f32_16x16x32_bf16 v[56:59], a[244:247], a[192:195], v[56:59]\n" \
"s_movk_i32 s84, 0x1f80\n" \
"v_mfma_f32_16x16x32_bf16 v[60:63], a[184:187], a[128:131], v[60:63]\n" \
"buffer_load_dwordx4 v23, s[64:67], s84 offen offset:1024 lds\n" \
"v_mfma_f32_16x16x32_bf16 v[60:63], a[248:251], a[192:195], v[60:63]\n" \
"ds_read_b64_tr_b16 a[162:163], v32 offset:1024\n" \
"v_mfma_f32_16x16x32_bf16 v[64:67], a[188:191], a[128:131], v[64:67]\n" \
"ds_read_b64_tr_b16 a[224:225], v32 offset:8192\n" \
"v_mfma_f32_16x16x32_bf16 v[64:67], a[252:255], a[192:195], v[64:67]\n" \
"ds_read_b128 a[128:131], v28 offset:16384\n" \
"v_mfma_f32_16x16x32_bf16 v[80:83], a[188:191], a[132:135], v[80:83]\n" \
"ds_read_b128 a[192:195], v29 offset:16384\n" \
"v_mfma_f32_16x16x32_bf16 v[80:83], a[252:255], a[196:199], v[80:83]\n" \
"s_movk_i32 s84, 0x3b80\n" \
"v_mfma_f32_16x16x32_bf16 v[76:79], a[184:187], a[132:135], v[76:79]\n" \
"buffer_load_dwordx4 v22, s[64:67], s84 offen offset:2048 lds\n" \
"v_mfma_f32_16x16x32_bf16 v[76:79], a[248:251], a[196:199], v[76:79]\n" \
"s_movk_i32 s84, 0x5780\n" \
"v_mfma_f32_16x16x32_bf16 v[72:75], a[180:183], a[132:135], v[72:75]\n" \
"buffer_load_dwordx4 v23, s[64:67], s84 offen offset:3072 lds\n" \
"v_mfma_f32_16x16x32_bf16 v[72:75], a[244:247], a[196:199], v[72:75]\n" \
"s_add_u32 m0, s31, 0x8000\n" \
"s_mov_b32 s84, 0x380000\n" \
"v_mfma_f32_16x16x32_bf16 v[68:71], a[176:179], a[132:135], v[68:71]\n" \
"buffer_load_dwordx4 v26, s[68:71], s84 offen offset:0 lds\n" \
"v_mfma_f32_16x16x32_bf16 v[68:71], a[240:243], a[196:199], v[68:71]\n" \
"ds_read_b128 a[132:135], v28 offset:18432\n" \
"v_mfma_f32_16x16x32_bf16 v[84:87], a[176:179], a[136:139], v[84:87]\n" \
"ds_read_b128 a[196:199], v29 offset:18432\n" \
"v_mfma_f32_16x16x32_bf16 v[84:87], a[240:243], a[200:203], v[84:87]\n" \
"s_addk_i32 s84, 0x7c00\n" \
"v_mfma_f32_16x16x32_bf16 v[88:91], a[180:183], a[136:139], v[88:91]\n" \
"buffer_load_dwordx4 v26, s[68:71], s84 offen offset:1024 lds\n" \
"v_mfma_f32_16x16x32_bf16 v[88:91], a[244:247], a[200:203], v[88:91]\n" \
"ds_read_b64_tr_b16 a[226:227], v32 offset:9216\n" \
"v_mfma_f32_16x16x32_bf16 v[92:95], a[184:187], a[136:139], v[92:95]\n" \
"s_addk_i32 s84, 0x7c00\n" \
"v_mfma_f32_16x16x32_bf16 v[92:95], a[248:251], a[200:203], v[92:95]\n" \
"buffer_load_dwordx4 v27, s[68:71], s84 offen offset:2048 lds\n" \
"v_mfma_f32_16x16x32_bf16 v[96:99], a[188:191], a[136:139], v[96:99]\n" \
"ds_read_b64_tr_b16 a[164:165], v33 offset:0\n" \
"v_mfma_f32_16x16x32_bf16 v[96:99], a[252:255], a[200:203], v[96:99]\n" \
"ds_read_b128 a[136:139], v28 offset:20480\n" \
"v_mfma_f32_16x16x32_bf16 v[112:115], a[188:191], a[140:143], v[112:115]\n" \
"ds_read_b128 a[200:203], v29 offset:20480\n" \
"v_mfma_f32_16x16x32_bf16 v[112:115], a[252:255], a[204:207], v[112:115]\n" \
"s_addk_i32 s84, 0x7c00\n" \
"s_nop 0\n" \
"v_mfma_f32_16x16x32_bf16 v[108:111], a[184:187], a[140:143], v[108:111]\n" \
"buffer_load_dwordx4 v27, s[68:71], s84 offen offset:3072 lds\n" \
"v_mfma_f32_16x16x32_bf16 v[108:111], a[248:251], a[204:207], v[108:111]\n" \
"s_add_u32 m0, s30, 0x9000\n" \
"s_mov_b32 s84, 0x8380\n" \
"v_mfma_f32_16x16x32_bf16 v[104:107], a[180:183], a[140:143], v[104:107]\n" \
"buffer_load_dwordx4 v22, s[64:67], s84 offen offset:0 lds\n" \
"v_mfma_f32_16x16x32_bf16 v[104:107], a[244:247], a[204:207], v[104:107]\n" \
"ds_read_b64_tr_b16 a[166:167], v33 offset:1024\n" \
"v_mfma_f32_16x16x32_bf16 v[100:103], a[176:179], a[140:143], v[100:103]\n" \
"ds_read_b64_tr_b16 a[228:229], v33 offset:8192\n" \
"v_mfma_f32_16x16x32_bf16 v[100:103], a[240:243], a[204:207], v[100:103]\n" \
"ds_read_b128 a[140:143], v28 offset:22528\n" \
"v_mfma_f32_16x16x32_bf16 v[116:119], a[176:179], a[144:147], v[116:119]\n" \
"ds_read_b128 a[204:207], v29 offset:22528\n" \
"v_mfma_f32_16x16x32_bf16 v[116:119], a[240:243], a[208:211], v[116:119]\n" \
"s_addk_i32 s84, 0x1c00\n" \
"v_mfma_f32_16x16x32_bf16 v[120:123], a[180:183], a[144:147], v[120:123]\n" \
"buffer_load_dwordx4 v23, s[64:67], s84 offen offset:1024 lds\n" \
"v_mfma_f32_16x16x32_bf16 v[120:123], a[244:247], a[208:211], v[120:123]\n" \
"s_addk_i32 s84, 0x1c00\n" \
"v_mfma_f32_16x16x32_bf16 v[124:127], a[184:187], a[144:147], v[124:127]\n" \
"buffer_load_dwordx4 v22, s[64:67], s84 offen offset:2048 lds\n" \
"v_mfma_f32_16x16x32_bf16 v[124:127], a[248:251], a[208:211], v[124:127]\n" \
"s_addk_i32 s84, 0x1c00\n" \
"s_nop 0\n" \
"v_mfma_f32_16x16x32_bf16 v[128:131], a[188:191], a[144:147], v[128:131]\n" \
"buffer_load_dwordx4 v23, s[64:67], s84 offen offset:3072 lds\n" \
"v_mfma_f32_16x16x32_bf16 v[128:131], a[252:255], a[208:211], v[128:131]\n" \
"ds_read_b128 a[144:147], v28 offset:0\n" \
"v_mfma_f32_16x16x32_bf16 v[144:147], a[188:191], a[148:151], v[144:147]\n" \
"ds_read_b128 a[208:211], v29 offset:0\n" \
"v_mfma_f32_16x16x32_bf16 v[144:147], a[252:255], a[212:215], v[144:147]\n" \
"s_add_u32 m0, s31, 0xc000\n" \
"s_mov_b32 s84, 0x380000\n" \
"v_mfma_f32_16x16x32_bf16 v[140:143], a[184:187], a[148:151], v[140:143]\n" \
"buffer_load_dwordx4 v26, s[72:75], s84 offen offset:0 lds\n" \
"v_mfma_f32_16x16x32_bf16 v[140:143], a[248:251], a[212:215], v[140:143]\n" \
"ds_read_b64_tr_b16 a[230:231], v33 offset:9216\n" \
"v_mfma_f32_16x16x32_bf16 v[136:139], a[180:183], a[148:151], v[136:139]\n" \
"s_addk_i32 s84, 0x7c00\n" \
"s_nop 0\n" \
"v_mfma_f32_16x16x32_bf16 v[136:139], a[244:247], a[212:215], v[136:139]\n" \
"buffer_load_dwordx4 v26, s[72:75], s84 offen offset:1024 lds\n" \
"v_mfma_f32_16x16x32_bf16 v[132:135], a[176:179], a[148:151], v[132:135]\n" \
"ds_read_b64_tr_b16 a[168:169], v34 offset:0\n" \
"v_mfma_f32_16x16x32_bf16 v[132:135], a[240:243], a[212:215], v[132:135]\n" \
"ds_read_b128 a[148:151], v28 offset:2048\n" \
"v_mfma_f32_16x16x32_bf16 v[148:151], a[176:179], a[152:155], v[148:151]\n" \
"ds_read_b128 a[212:215], v29 offset:2048\n" \
"v_mfma_f32_16x16x32_bf16 v[148:151], a[240:243], a[216:219], v[148:151]\n" \
"ds_read_b64_tr_b16 a[170:171], v34 offset:1024\n" \
"v_mfma_f32_16x16x32_bf16 v[152:155], a[180:183], a[152:155], v[152:155]\n" \
"ds_read_b64_tr_b16 a[232:233], v34 offset:8192\n" \
"v_mfma_f32_16x16x32_bf16 v[152:155], a[244:247], a[216:219], v[152:155]\n" \
"ds_read_b64_tr_b16 a[234:235], v34 offset:9216\n" \
"v_mfma_f32_16x16x32_bf16 v[156:159], a[184:187], a[152:155], v[156:159]\n" \
"ds_read_b64_tr_b16 a[172:173], v35 offset:0\n" \
"v_mfma_f32_16x16x32_bf16 v[156:159], a[248:251], a[216:219], v[156:159]\n" \
"ds_read_b64_tr_b16 a[174:175], v35 offset:1024\n" \
"v_mfma_f32_16x16x32_bf16 v[160:163], a[188:191], a[152:155], v[160:163]\n" \
"ds_read_b64_tr_b16 a[236:237], v35 offset:8192\n" \
"v_mfma_f32_16x16x32_bf16 v[160:163], a[252:255], a[216:219], v[160:163]\n" \
"ds_read_b128 a[152:155], v28 offset:4096\n" \
"v_mfma_f32_16x16x32_bf16 v[176:179], a[188:191], a[156:159], v[176:179]\n" \
"ds_read_b128 a[216:219], v29 offset:4096\n" \
"v_mfma_f32_16x16x32_bf16 v[176:179], a[252:255], a[220:223], v[176:179]\n" \
"ds_read_b64_tr_b16 a[238:239], v35 offset:9216\n" \
"v_mfma_f32_16x16x32_bf16 v[172:175], a[184:187], a[156:159], v[172:175]\n" \
"v_mfma_f32_16x16x32_bf16 v[172:175], a[248:251], a[220:223], v[172:175]\n" \
"v_mfma_f32_16x16x32_bf16 v[168:171], a[180:183], a[156:159], v[168:171]\n" \
"v_mfma_f32_16x16x32_bf16 v[168:171], a[244:247], a[220:223], v[168:171]\n" \
"v_mfma_f32_16x16x32_bf16 v[164:167], a[176:179], a[156:159], v[164:167]\n" \
"v_mfma_f32_16x16x32_bf16 v[164:167], a[240:243], a[220:223], v[164:167]\n" \
"ds_read_b128 a[156:159], v28 offset:6144\n" \
"ds_read_b128 a[220:223], v29 offset:6144\n" \
"s_waitcnt lgkmcnt(15)\n" \
"v_mfma_f32_16x16x32_bf16 a[0:3], a[160:163], a[128:131], a[0:3]\n" \
"s_addk_i32 s84, 0x7c00\n" \
"v_mfma_f32_16x16x32_bf16 a[0:3], a[224:227], a[192:195], a[0:3]\n" \
"buffer_load_dwordx4 v27, s[72:75], s84 offen offset:2048 lds\n" \
"v_mfma_f32_16x16x32_bf16 a[4:7], a[164:167], a[128:131], a[4:7]\n" \
"ds_read_b64_tr_b16 a[176:177], v36 offset:0\n" \
"s_waitcnt lgkmcnt(15)\n" \
"v_mfma_f32_16x16x32_bf16 a[4:7], a[228:231], a[192:195], a[4:7]\n" \
"s_addk_i32 s84, 0x7c00\n" \
"s_waitcnt lgkmcnt(11)\n" \
"v_mfma_f32_16x16x32_bf16 a[8:11], a[168:171], a[128:131], a[8:11]\n" \
"buffer_load_dwordx4 v27, s[72:75], s84 offen offset:3072 lds\n" \
"s_waitcnt lgkmcnt(9)\n" \
"v_mfma_f32_16x16x32_bf16 a[8:11], a[232:235], a[192:195], a[8:11]\n" \
"ds_read_b64_tr_b16 a[178:179], v36 offset:1024\n" \
"s_waitcnt lgkmcnt(8)\n" \
"v_mfma_f32_16x16x32_bf16 a[12:15], a[172:175], a[128:131], a[12:15]\n" \
"ds_read_b64_tr_b16 a[240:241], v36 offset:8192\n" \
"s_waitcnt lgkmcnt(5)\n" \
"v_mfma_f32_16x16x32_bf16 a[12:15], a[236:239], a[192:195], a[12:15]\n" \
"v_mfma_f32_16x16x32_bf16 a[28:31], a[172:175], a[132:135], a[28:31]\n" \
"v_mfma_f32_16x16x32_bf16 a[28:31], a[236:239], a[196:199], a[28:31]\n" \
"ds_read_b64_tr_b16 a[242:243], v36 offset:9216\n" \
"v_mfma_f32_16x16x32_bf16 a[24:27], a[168:171], a[132:135], a[24:27]\n" \
"v_mfma_f32_16x16x32_bf16 a[24:27], a[232:235], a[196:199], a[24:27]\n" \
"v_mfma_f32_16x16x32_bf16 a[20:23], a[164:167], a[132:135], a[20:23]\n" \
"v_mfma_f32_16x16x32_bf16 a[20:23], a[228:231], a[196:199], a[20:23]\n" \
"ds_read_b64_tr_b16 a[180:181], v37 offset:0\n" \
"v_mfma_f32_16x16x32_bf16 a[16:19], a[160:163], a[132:135], a[16:19]\n" \
"v_mfma_f32_16x16x32_bf16 a[16:19], a[224:227], a[196:199], a[16:19]\n" \
"v_mfma_f32_16x16x32_bf16 a[32:35], a[160:163], a[136:139], a[32:35]\n" \
"v_bfe_u32 v44, v20, 2, 5\n" \
"ds_read_b64_tr_b16 a[182:183], v37 offset:1024\n" \
"v_mfma_f32_16x16x32_bf16 a[32:35], a[224:227], a[200:203], a[32:35]\n" \
"buffer_load_dword v44, v44, s[80:83], s38 offen offset:128 sc1\n" \
"v_mfma_f32_16x16x32_bf16 a[36:39], a[164:167], a[136:139], a[36:39]\n" \
"v_mfma_f32_16x16x32_bf16 a[36:39], a[228:231], a[200:203], a[36:39]\n" \
"ds_read_b64_tr_b16 a[244:245], v37 offset:8192\n" \
"v_mfma_f32_16x16x32_bf16 a[40:43], a[168:171], a[136:139], a[40:43]\n" \
"v_mfma_f32_16x16x32_bf16 a[40:43], a[232:235], a[200:203], a[40:43]\n" \
"v_mfma_f32_16x16x32_bf16 a[44:47], a[172:175], a[136:139], a[44:47]\n" \
"ds_read_b64_tr_b16 a[246:247], v37 offset:9216\n" \
"v_mfma_f32_16x16x32_bf16 a[44:47], a[236:239], a[200:203], a[44:47]\n" \
"v_mfma_f32_16x16x32_bf16 a[60:63], a[172:175], a[140:143], a[60:63]\n" \
"global_store_dwordx4 v49, v[0:3], s[28:29] offset:0 sc1\n" \
"v_mfma_f32_16x16x32_bf16 a[60:63], a[236:239], a[204:207], a[60:63]\n" \
"v_mfma_f32_16x16x32_bf16 a[56:59], a[168:171], a[140:143], a[56:59]\n" \
"ds_read_b64_tr_b16 a[184:185], v38 offset:0\n" \
"v_mfma_f32_16x16x32_bf16 a[56:59], a[232:235], a[204:207], a[56:59]\n" \
"v_mfma_f32_16x16x32_bf16 a[52:55], a[164:167], a[140:143], a[52:55]\n" \
"v_mfma_f32_16x16x32_bf16 a[52:55], a[228:231], a[204:207], a[52:55]\n" \
"ds_read_b64_tr_b16 a[186:187], v38 offset:1024\n" \
"v_mfma_f32_16x16x32_bf16 a[48:51], a[160:163], a[140:143], a[48:51]\n" \
"v_mfma_f32_16x16x32_bf16 a[48:51], a[224:227], a[204:207], a[48:51]\n" \
"v_mfma_f32_16x16x32_bf16 a[64:67], a[160:163], a[144:147], a[64:67]\n" \
"global_store_dwordx4 v49, v[0:3], s[28:29] offset:1024 sc1\n" \
"v_mfma_f32_16x16x32_bf16 a[64:67], a[224:227], a[208:211], a[64:67]\n" \
"ds_read_b64_tr_b16 a[248:249], v38 offset:8192\n" \
"v_mfma_f32_16x16x32_bf16 a[68:71], a[164:167], a[144:147], a[68:71]\n" \
"v_mfma_f32_16x16x32_bf16 a[68:71], a[228:231], a[208:211], a[68:71]\n" \
"ds_read_b64_tr_b16 a[250:251], v38 offset:9216\n" \
"v_mfma_f32_16x16x32_bf16 a[72:75], a[168:171], a[144:147], a[72:75]\n" \
"v_mfma_f32_16x16x32_bf16 a[72:75], a[232:235], a[208:211], a[72:75]\n" \
"v_mfma_f32_16x16x32_bf16 a[76:79], a[172:175], a[144:147], a[76:79]\n" \
"v_mfma_f32_16x16x32_bf16 a[76:79], a[236:239], a[208:211], a[76:79]\n" \
"ds_read_b64_tr_b16 a[188:189], v39 offset:0\n" \
"v_mfma_f32_16x16x32_bf16 a[92:95], a[172:175], a[148:151], a[92:95]\n" \
"s_mov_b64 exec, 0xffff\n" \
"global_store_dwordx4 v49, v[0:3], s[28:29] offset:2048 sc1\n" \
"s_mov_b64 exec, -1\n" \
"s_nop 0\n" \
"v_mfma_f32_16x16x32_bf16 a[92:95], a[236:239], a[212:215], a[92:95]\n" \
"v_mfma_f32_16x16x32_bf16 a[88:91], a[168:171], a[148:151], a[88:91]\n" \
"ds_read_b64_tr_b16 a[190:191], v39 offset:1024\n" \
"v_mfma_f32_16x16x32_bf16 a[88:91], a[232:235], a[212:215], a[88:91]\n" \
"v_mfma_f32_16x16x32_bf16 a[84:87], a[164:167], a[148:151], a[84:87]\n" \
"v_mfma_f32_16x16x32_bf16 a[84:87], a[228:231], a[212:215], a[84:87]\n" \
"ds_read_b64_tr_b16 a[252:253], v39 offset:8192\n" \
"v_mfma_f32_16x16x32_bf16 a[80:83], a[160:163], a[148:151], a[80:83]\n" \
"v_mfma_f32_16x16x32_bf16 a[80:83], a[224:227], a[212:215], a[80:83]\n" \
"v_mfma_f32_16x16x32_bf16 a[96:99], a[160:163], a[152:155], a[96:99]\n" \
"ds_read_b64_tr_b16 a[254:255], v39 offset:9216\n" \
"v_mfma_f32_16x16x32_bf16 a[96:99], a[224:227], a[216:219], a[96:99]\n" \
"v_mfma_f32_16x16x32_bf16 a[100:103], a[164:167], a[152:155], a[100:103]\n" \
"v_mfma_f32_16x16x32_bf16 a[100:103], a[228:231], a[216:219], a[100:103]\n" \
"v_mfma_f32_16x16x32_bf16 a[104:107], a[168:171], a[152:155], a[104:107]\n" \
"v_mfma_f32_16x16x32_bf16 a[104:107], a[232:235], a[216:219], a[104:107]\n" \
"v_mfma_f32_16x16x32_bf16 a[108:111], a[172:175], a[152:155], a[108:111]\n" \
"v_mfma_f32_16x16x32_bf16 a[108:111], a[236:239], a[216:219], a[108:111]\n" \
"s_waitcnt lgkmcnt(15)\n" \
"v_mfma_f32_16x16x32_bf16 a[124:127], a[172:175], a[156:159], a[124:127]\n" \
"v_mfma_f32_16x16x32_bf16 a[124:127], a[236:239], a[220:223], a[124:127]\n" \
"v_mfma_f32_16x16x32_bf16 a[120:123], a[168:171], a[156:159], a[120:123]\n" \
"v_mfma_f32_16x16x32_bf16 a[120:123], a[232:235], a[220:223], a[120:123]\n" \
"v_mfma_f32_16x16x32_bf16 a[116:119], a[164:167], a[156:159], a[116:119]\n" \
"v_mfma_f32_16x16x32_bf16 a[116:119], a[228:231], a[220:223], a[116:119]\n" \
"v_mfma_f32_16x16x32_bf16 a[112:115], a[160:163], a[156:159], a[112:115]\n" \
"v_mfma_f32_16x16x32_bf16 a[112:115], a[224:227], a[220:223], a[112:115]\n" \
"s_waitcnt vmcnt(3) lgkmcnt(0)\n" \
"s_barrier\n" \
"v_cmp_ne_u32_e32 vcc, s62, v44\n" \
"s_nop 4\n" \
"s_cbranch_vccnz L_poll60_%=\n" \
"L_pollr60_%=:\n" \
"s_mov_b32 s68, s12\n" \
"s_and_b32 s69, s13, 0xffff\n" \
"s_mov_b32 s70, 0x480000\n" \
"s_mov_b32 s71, 0x20000\n" \
"v_mfma_f32_16x16x32_bf16 v[52:55], a[176:179], a[128:131], v[52:55]\n" \
"s_add_u32 m0, s40, 0x0\n" \
"s_add_u32 s85, s39, 0x0\n" \
"s_nop 0\n" \
"v_mfma_f32_16x16x32_bf16 v[52:55], a[240:243], a[192:195], v[52:55]\n" \
"buffer_load_dwordx4 v20, s[80:83], s85 offen sc1 lds\n" \
"v_mfma_f32_16x16x32_bf16 v[56:59], a[180:183], a[128:131], v[56:59]\n" \
"ds_read_b64_tr_b16 a[160:161], v32 offset:32768\n" \
"v_mfma_f32_16x16x32_bf16 v[56:59], a[244:247], a[192:195], v[56:59]\n" \
"s_add_u32 m0, s40, 0x1000\n" \
"s_add_u32 s85, s39, 0x1000\n" \
".fill (L_pb + 16096 - .) / 4, 4, 0xBF800000\n" \
"L_pp4:\n" \
"s_cbranch_execz L_pret\n" \
"s_nop 0\n" \
"v_mfma_f32_16x16x32_bf16 v[60:63], a[184:187], a[128:131], v[60:63]\n" \
"buffer_load_dwordx4 v20, s[80:83], s85 offen sc1 lds\n" \
"v_mfma_f32_16x16x32_bf16 v[60:63], a[248:251], a[192:195], v[60:63]\n" \
"ds_read_b64_tr_b16 a[162:163], v32 offset:33792\n" \
"v_mfma_f32_16x16x32_bf16 v[64:67], a[188:191], a[128:131], v[64:67]\n" \
"ds_read_b64_tr_b16 a[224:225], v32 offset:40960\n" \
"v_mfma_f32_16x16x32_bf16 v[64:67], a[252:255], a[192:195], v[64:67]\n" \
"ds_read_b128 a[128:131], v28 offset:49152\n" \
"v_mfma_f32_16x16x32_bf16 v[80:83], a[188:191], a[132:135], v[80:83]\n" \
"ds_read_b128 a[192:195], v29 offset:49152\n" \
"v_mfma_f32_16x16x32_bf16 v[80:83], a[252:255], a[196:199], v[80:83]\n" \
"s_add_u32 m0, s40, 0x3000\n" \
"s_movk_i32 s84, 0x0\n" \
"v_mfma_f32_16x16x32_bf16 v[76:79], a[184:187], a[132:135], v[76:79]\n" \
"buffer_load_dwordx4 v21, s[76:79], s84 offen offset:0 lds\n" \
"v_mfma_f32_16x16x32_bf16 v[76:79], a[248:251], a[196:199], v[76:79]\n" \
"s_add_u32 m0, s30, 0x0\n" \
"s_movk_i32 s84, 0x0\n" \
"s_nop 0\n" \
"v_mfma_f32_16x16x32_bf16 v[72:75], a[180:183], a[132:135], v[72:75]\n" \
"buffer_load_dwordx4 v24, s[76:79], s84 offen offset:0 lds\n" \
"v_mfma_f32_16x16x32_bf16 v[72:75], a[244:247], a[196:199], v[72:75]\n" \
"ds_read_b64_tr_b16 a[226:227], v32 offset:41984\n" \
"v_mfma_f32_16x16x32_bf16 v[68:71], a[176:179], a[132:135], v[68:71]\n" \
"ds_read_b64_tr_b16 a[164:165], v33 offset:32768\n" \
"v_mfma_f32_16x16x32_bf16 v[68:71], a[240:243], a[196:199], v[68:71]\n" \
"ds_read_b128 a[132:135], v28 offset:51200\n" \
"v_mfma_f32_16x16x32_bf16 v[84:87], a[176:179], a[136:139], v[84:87]\n" \
"ds_read_b128 a[196:199], v29 offset:51200\n" \
"v_mfma_f32_16x16x32_bf16 v[84:87], a[240:243], a[200:203], v[84:87]\n" \
"s_movk_i32 s84, 0x2000\n" \
"v_mfma_f32_16x16x32_bf16 v[88:91], a[180:183], a[136:139], v[88:91]\n" \
"buffer_load_dwordx4 v25, s[76:79], s84 offen offset:1024 lds\n" \
"v_mfma_f32_16x16x32_bf16 v[88:91], a[244:247], a[200:203], v[88:91]\n" \
"s_movk_i32 s84, 0x4000\n" \
"v_mfma_f32_16x16x32_bf16 v[92:95], a[184:187], a[136:139], v[92:95]\n" \
"buffer_load_dwordx4 v24, s[76:79], s84 offen offset:2048 lds\n" \
"v_mfma_f32_16x16x32_bf16 v[92:95], a[248:251], a[200:203], v[92:95]\n" \
"ds_read_b64_tr_b16 a[166:167], v33 offset:33792\n" \
"v_mfma_f32_16x16x32_bf16 v[96:99], a[188:191], a[136:139], v[96:99]\n" \
"ds_read_b64_tr_b16 a[228:229], v33 offset:40960\n" \
"v_mfma_f32_16x16x32_bf16 v[96:99], a[252:255], a[200:203], v[96:99]\n" \
"ds_read_b128 a[136:139], v28 offset:53248\n" \
"v_mfma_f32_16x16x32_bf16 v[112:115], a[188:191], a[140:143], v[112:115]\n" \
"ds_read_b128 a[200:203], v29 offset:53248\n" \
"v_mfma_f32_16x16x32_bf16 v[112:115], a[252:255], a[204:207], v[112:115]\n" \
"s_movk_i32 s84, 0x6000\n" \
"v_mfma_f32_16x16x32_bf16 v[108:111], a[184:187], a[140:143], v[108:111]\n" \
"buffer_load_dwordx4 v25, s[76:79], s84 offen offset:3072 lds\n" \
"v_mfma_f32_16x16x32_bf16 v[108:111], a[248:251], a[204:207], v[108:111]\n" \
"s_add_u32 m0, s31, 0x0\n" \
"s_movk_i32 s84, 0x0\n" \
"v_mfma_f32_16x16x32_bf16 v[104:107], a[180:183], a[140:143], v[104:107]\n" \
"buffer_load_dwordx4 v26, s[68:71], s84 offen offset:0 lds\n" \
"v_mfma_f32_16x16x32_bf16 v[104:107], a[244:247], a[204:207], v[104:107]\n" \
"s_movk_i32 s84, 0x7c00\n" \
"v_mfma_f32_16x16x32_bf16 v[100:103], a[176:179], a[140:143], v[100:103]\n" \
"buffer_load_dwordx4 v26, s[68:71], s84 offen offset:1024 lds\n" \
"v_mfma_f32_16x16x32_bf16 v[100:103], a[240:243], a[204:207], v[100:103]\n" \
"ds_read_b128 a[140:143], v28 offset:55296\n" \
"v_mfma_f32_16x16x32_bf16 v[116:119], a[176:179], a[144:147], v[116:119]\n" \
"ds_read_b128 a[204:207], v29 offset:55296\n" \
"v_mfma_f32_16x16x32_bf16 v[116:119], a[240:243], a[208:211], v[116:119]\n" \
"s_addk_i32 s84, 0x7c00\n" \
"v_mfma_f32_16x16x32_bf16 v[120:123], a[180:183], a[144:147], v[120:123]\n" \
"buffer_load_dwordx4 v27, s[68:71], s84 offen offset:2048 lds\n" \
"v_mfma_f32_16x16x32_bf16 v[120:123], a[244:247], a[208:211], v[120:123]\n" \
"ds_read_b64_tr_b16 a[230:231], v33 offset:41984\n" \
"v_mfma_f32_16x16x32_bf16 v[124:127], a[184:187], a[144:147], v[124:127]\n" \
"s_addk_i32 s84, 0x7c00\n" \
"v_mfma_f32_16x16x32_bf16 v[124:127], a[248:251], a[208:211], v[124:127]\n" \
"buffer_load_dwordx4 v27, s[68:71], s84 offen offset:3072 lds\n" \
"v_mfma_f32_16x16x32_bf16 v[128:131], a[188:191], a[144:147], v[128:131]\n" \
"ds_read_b64_tr_b16 a[168:169], v34 offset:32768\n" \
"v_mfma_f32_16x16x32_bf16 v[128:131], a[252:255], a[208:211], v[128:131]\n" \
"ds_read_b128 a[144:147], v28 offset:32768\n" \
"v_mfma_f32_16x16x32_bf16 v[144:147], a[188:191], a[148:151], v[144:147]\n" \
"ds_read_b128 a[208:211], v29 offset:32768\n" \
"v_mfma_f32_16x16x32_bf16 v[144:147], a[252:255], a[212:215], v[144:147]\n" \
"s_add_u32 m0, s30, 0x1000\n" \
"s_mov_b32 s84, 0x9000\n" \
"v_mfma_f32_16x16x32_bf16 v[140:143], a[184:187], a[148:151], v[140:143]\n" \
"buffer_load_dwordx4 v24, s[76:79], s84 offen offset:0 lds\n" \
"v_mfma_f32_16x16x32_bf16 v[140:143], a[248:251], a[212:215], v[140:143]\n" \
"ds_read_b64_tr_b16 a[170:171], v34 offset:33792\n" \
"v_mfma_f32_16x16x32_bf16 v[136:139], a[180:183], a[148:151], v[136:139]\n" \
"s_addk_i32 s84, 0x2000\n" \
"s_nop 0\n" \
"v_mfma_f32_16x16x32_bf16 v[136:139], a[244:247], a[212:215], v[136:139]\n" \
"buffer_load_dwordx4 v25, s[76:79], s84 offen offset:1024 lds\n" \
"v_mfma_f32_16x16x32_bf16 v[132:135], a[176:179], a[148:151], v[132:135]\n" \
"ds_read_b64_tr_b16 a[232:233], v34 offset:40960\n" \
"v_mfma_f32_16x16x32_bf16 v[132:135], a[240:243], a[212:215], v[132:135]\n" \
"ds_read_b128 a[148:151], v28 offset:34816\n" \
"v_mfma_f32_16x16x32_bf16 v[148:151], a[176:179], a[152:155], v[148:151]\n" \
"ds_read_b128 a[212:215], v29 offset:34816\n" \
"v_mfma_f32_16x16x32_bf16 v[148:151], a[240:243], a[216:219], v[148:151]\n" \
"ds_read_b64_tr_b16 a[234:235], v34 offset:41984\n" \
"v_mfma_f32_16x16x32_bf16 v[152:155], a[180:183], a[152:155], v[152:155]\n" \
"ds_read_b64_tr_b16 a[172:173], v35 offset:32768\n" \
"v_mfma_f32_16x16x32_bf16 v[152:155], a[244:247], a[216:219], v[152:155]\n" \
"ds_read_b64_tr_b16 a[174:175], v35 offset:33792\n" \
"v_mfma_f32_16x16x32_bf16 v[156:159], a[184:187], a[152:155], v[156:159]\n" \
"ds_read_b64_tr_b16 a[236:237], v35 offset:40960\n" \
"v_mfma_f32_16x16x32_bf16 v[156:159], a[248:251], a[216:219], v[156:159]\n" \
"ds_read_b64_tr_b16 a[238:239], v35 offset:41984\n" \
"v_mfma_f32_16x16x32_bf16 v[160:163], a[188:191], a[152:155], v[160:163]\n" \
"v_mfma_f32_16x16x32_bf16 v[160:163], a[252:255], a[216:219], v[160:163]\n" \
"ds_read_b128 a[152:155], v28 offset:36864\n" \
"v_mfma_f32_16x16x32_bf16 v[176:179], a[188:191], a[156:159], v[176:179]\n" \
"ds_read_b128 a[216:219], v29 offset:36864\n" \
"v_mfma_f32_16x16x32_bf16 v[176:179], a[252:255], a[220:223], v[176:179]\n" \
"v_mfma_f32_16x16x32_bf16 v[172:175], a[184:187], a[156:159], v[172:175]\n" \
"v_mfma_f32_16x16x32_bf16 v[172:175], a[248:251], a[220:223], v[172:175]\n" \
"v_mfma_f32_16x16x32_bf16 v[168:171], a[180:183], a[156:159], v[168:171]\n" \
"v_mfma_f32_16x16x32_bf16 v[168:171], a[244:247], a[220:223], v[168:171]\n" \
"v_mfma_f32_16x16x32_bf16 v[164:167], a[176:179], a[156:159], v[164:167]\n" \
"v_mfma_f32_16x16x32_bf16 v[164:167], a[240:243], a[220:223], v[164:167]\n" \
"ds_read_b128 a[156:159], v28 offset:38912\n" \
"ds_read_b128 a[220:223], v29 offset:38912\n" \
"s_waitcnt lgkmcnt(15)\n" \
"v_mfma_f32_16x16x32_bf16 a[0:3], a[160:163], a[128:131], a[0:3]\n" \
"s_addk_i32 s84, 0x2000\n" \
"v_mfma_f32_16x16x32_bf16 a[0:3], a[224:227], a[192:195], a[0:3]\n" \
"buffer_load_dwordx4 v24, s[76:79], s84 offen offset:2048 lds\n" \
"v_mfma_f32_16x16x32_bf16 a[4:7], a[164:167], a[128:131], a[4:7]\n" \
"ds_read_b64_tr_b16 a[176:177], v36 offset:32768\n" \
"v_mfma_f32_16x16x32_bf16 a[4:7], a[228:231], a[192:195], a[4:7]\n" \
"s_addk_i32 s84, 0x2000\n" \
"s_waitcnt lgkmcnt(13)\n" \
"v_mfma_f32_16x16x32_bf16 a[8:11], a[168:171], a[128:131], a[8:11]\n" \
"buffer_load_dwordx4 v25, s[76:79], s84 offen offset:3072 lds\n" \
"s_waitcnt lgkmcnt(9)\n" \
"v_mfma_f32_16x16x32_bf16 a[8:11], a[232:235], a[192:195], a[8:11]\n" \
"ds_read_b64_tr_b16 a[178:179], v36 offset:33792\n" \
"s_waitcnt lgkmcnt(8)\n" \
"v_mfma_f32_16x16x32_bf16 a[12:15], a[172:175], a[128:131], a[12:15]\n" \
"ds_read_b64_tr_b16 a[240:241], v36 offset:40960\n" \
"v_accvgpr_read_b32 v184, a0\n" \
"s_waitcnt lgkmcnt(7)\n" \
"v_mfma_f32_16x16x32_bf16 a[12:15], a[236:239], a[192:195], a[12:15]\n" \
"v_accvgpr_read_b32 v185, a1\n" \
"v_accvgpr_read_b32 v186, a2\n" \
"v_mfma_f32_16x16x32_bf16 a[28:31], a[172:175], a[132:135], a[28:31]\n" \
"v_accvgpr_read_b32 v187, a3\n" \
"v_accvgpr_read_b32 v180, a4\n" \
"v_mfma_f32_16x16x32_bf16 a[28:31], a[236:239], a[196:199], a[28:31]\n" \
"s_add_u32 m0, s30, 0x20000\n" \
"s_movk_i32 s84, 0x100\n" \
"v_mfma_f32_16x16x32_bf16 a[24:27], a[168:171], a[132:135], a[24:27]\n" \
"buffer_load_dwordx4 v24, s[76:79], s84 offen offset:0 lds\n" \
"v_mfma_f32_16x16x32_bf16 a[24:27], a[232:235], a[196:199], a[24:27]\n" \
"ds_read_b64_tr_b16 a[242:243], v36 offset:41984\n" \
"v_accvgpr_read_b32 v181, a5\n" \
"v_mfma_f32_16x16x32_bf16 a[20:23], a[164:167], a[132:135], a[20:23]\n" \
"v_accvgpr_read_b32 v182, a6\n" \
"v_accvgpr_read_b32 v183, a7\n" \
"v_mfma_f32_16x16x32_bf16 a[20:23], a[228:231], a[196:199], a[20:23]\n" \
"ds_read_b64_tr_b16 a[180:181], v37 offset:32768\n" \
"v_cvt_pk_bf16_f32 v184, v184, v185\n" \
"v_mfma_f32_16x16x32_bf16 a[16:19], a[160:163], a[132:135], a[16:19]\n" \
"s_movk_i32 s84, 0x2100\n" \
"s_nop 0\n" \
"v_mfma_f32_16x16x32_bf16 a[16:19], a[224:227], a[196:199], a[16:19]\n" \
"buffer_load_dwordx4 v25, s[76:79], s84 offen offset:1024 lds\n" \
"v_mfma_f32_16x16x32_bf16 a[32:35], a[160:163], a[136:139], a[32:35]\n" \
"ds_read_b64_tr_b16 a[182:183], v37 offset:33792\n" \
"v_cvt_pk_bf16_f32 v185, v186, v187\n" \
"v_mfma_f32_16x16x32_bf16 a[32:35], a[224:227], a[200:203], a[32:35]\n" \
"v_cvt_pk_bf16_f32 v186, v180, v181\n" \
"v_cvt_pk_bf16_f32 v187, v182, v183\n" \
"v_mfma_f32_16x16x32_bf16 a[36:39], a[164:167], a[136:139], a[36:39]\n" \
"v_accvgpr_read_b32 v188, a8\n" \
"v_accvgpr_read_b32 v189, a9\n" \
"v_mfma_f32_16x16x32_bf16 a[36:39], a[228:231], a[200:203], a[36:39]\n" \
"s_movk_i32 s84, 0x4100\n" \
"s_nop 0\n" \
"v_mfma_f32_16x16x32_bf16 a[40:43], a[168:171], a[136:139], a[40:43]\n" \
"buffer_load_dwordx4 v24, s[76:79], s84 offen offset:2048 lds\n" \
"v_mfma_f32_16x16x32_bf16 a[40:43], a[232:235], a[200:203], a[40:43]\n" \
"ds_read_b64_tr_b16 a[244:245], v37 offset:40960\n" \
"v_accvgpr_read_b32 v190, a10\n" \
"v_mfma_f32_16x16x32_bf16 a[44:47], a[172:175], a[136:139], a[44:47]\n" \
"ds_read_b64_tr_b16 a[246:247], v37 offset:41984\n" \
"v_accvgpr_read_b32 v191, a11\n" \
"v_mfma_f32_16x16x32_bf16 a[44:47], a[236:239], a[200:203], a[44:47]\n" \
"v_accvgpr_read_b32 v180, a12\n" \
"v_accvgpr_read_b32 v181, a13\n" \
"v_mfma_f32_16x16x32_bf16 a[60:63], a[172:175], a[140:143], a[60:63]\n" \
"s_movk_i32 s84, 0x6100\n" \
"v_mfma_f32_16x16x32_bf16 a[60:63], a[236:239], a[204:207], a[60:63]\n" \
"buffer_load_dwordx4 v25, s[76:79], s84 offen offset:3072 lds\n" \
"v_mfma_f32_16x16x32_bf16 a[56:59], a[168:171], a[140:143], a[56:59]\n" \
"ds_read_b64_tr_b16 a[184:185], v38 offset:32768\n" \
"v_accvgpr_read_b32 v182, a14\n" \
"v_mfma_f32_16x16x32_bf16 a[56:59], a[232:235], a[204:207], a[56:59]\n" \
"v_accvgpr_read_b32 v183, a15\n" \
"v_cvt_pk_bf16_f32 v188, v188, v189\n" \
"v_mfma_f32_16x16x32_bf16 a[52:55], a[164:167], a[140:143], a[52:55]\n" \
"s_add_u32 m0, s30, 0x21000\n" \
"s_addk_i32 s84, 0x3000\n" \
"v_mfma_f32_16x16x32_bf16 a[52:55], a[228:231], a[204:207], a[52:55]\n" \
"buffer_load_dwordx4 v24, s[76:79], s84 offen offset:0 lds\n" \
"v_mfma_f32_16x16x32_bf16 a[48:51], a[160:163], a[140:143], a[48:51]\n" \
"ds_read_b64_tr_b16 a[186:187], v38 offset:33792\n" \
"v_cvt_pk_bf16_f32 v189, v190, v191\n" \
"v_mfma_f32_16x16x32_bf16 a[48:51], a[224:227], a[204:207], a[48:51]\n" \
"v_cvt_pk_bf16_f32 v190, v180, v181\n" \
"v_cvt_pk_bf16_f32 v191, v182, v183\n" \
"v_mfma_f32_16x16x32_bf16 a[64:67], a[160:163], a[144:147], a[64:67]\n" \
"ds_read_b64_tr_b16 a[248:249], v38 offset:40960\n" \
"v_accvgpr_read_b32 v192, a24\n" \
"v_mfma_f32_16x16x32_bf16 a[64:67], a[224:227], a[208:211], a[64:67]\n" \
"s_addk_i32 s84, 0x2000\n" \
"s_nop 0\n" \
"v_mfma_f32_16x16x32_bf16 a[68:71], a[164:167], a[144:147], a[68:71]\n" \
"buffer_load_dwordx4 v25, s[76:79], s84 offen offset:1024 lds\n" \
"v_mfma_f32_16x16x32_bf16 a[68:71], a[228:231], a[208:211], a[68:71]\n" \
"ds_read_b64_tr_b16 a[250:251], v38 offset:41984\n" \
"v_accvgpr_read_b32 v193, a25\n" \
"v_mfma_f32_16x16x32_bf16 a[72:75], a[168:171], a[144:147], a[72:75]\n" \
"v_accvgpr_read_b32 v194, a26\n" \
"v_accvgpr_read_b32 v195, a27\n" \
"v_mfma_f32_16x16x32_bf16 a[72:75], a[232:235], a[208:211], a[72:75]\n" \
"v_accvgpr_read_b32 v180, a28\n" \
"v_accvgpr_read_b32 v181, a29\n" \
"v_mfma_f32_16x16x32_bf16 a[76:79], a[172:175], a[144:147], a[76:79]\n" \
"s_addk_i32 s84, 0x2000\n" \
"v_mfma_f32_16x16x32_bf16 a[76:79], a[236:239], a[208:211], a[76:79]\n" \
"buffer_load_dwordx4 v24, s[76:79], s84 offen offset:2048 lds\n" \
"v_mfma_f32_16x16x32_bf16 a[92:95], a[172:175], a[148:151], a[92:95]\n" \
"s_add_u32 s89, s41, 0x0\n" \
"buffer_store_dwordx4 v[184:187], v20, s[80:83], s89 offen offset:0 sc1\n" \
"v_mfma_f32_16x16x32_bf16 a[92:95], a[236:239], a[212:215], a[92:95]\n" \
"ds_read_b64_tr_b16 a[188:189], v39 offset:32768\n" \
"v_accvgpr_read_b32 v182, a30\n" \
"v_mfma_f32_16x16x32_bf16 a[88:91], a[168:171], a[148:151], a[88:91]\n" \
"ds_read_b64_tr_b16 a[190:191], v39 offset:33792\n" \
"v_accvgpr_read_b32 v183, a31\n" \
"v_mfma_f32_16x16x32_bf16 a[88:91], a[232:235], a[212:215], a[88:91]\n" \
"s_addk_i32 s84, 0x2000\n" \
"v_mfma_f32_16x16x32_bf16 a[84:87], a[164:167], a[148:151], a[84:87]\n" \
"buffer_load_dwordx4 v25, s[76:79], s84 offen offset:3072 lds\n" \
"v_mfma_f32_16x16x32_bf16 a[84:87], a[228:231], a[212:215], a[84:87]\n" \
"ds_read_b64_tr_b16 a[252:253], v39 offset:40960\n" \
"v_cvt_pk_bf16_f32 v192, v192, v193\n" \
"v_mfma_f32_16x16x32_bf16 a[80:83], a[160:163], a[148:151], a[80:83]\n" \
"s_add_u32 s88, s41, 0x0\n" \
"buffer_store_dwordx4 v[188:191], v20, s[80:83], s88 offen offset:1024 sc1\n" \
"v_mfma_f32_16x16x32_bf16 a[80:83], a[224:227], a[212:215], a[80:83]\n" \
"v_cvt_pk_bf16_f32 v193, v194, v195\n" \
"v_cvt_pk_bf16_f32 v194, v180, v181\n" \
"s_waitcnt lgkmcnt(15)\n" \
"s_nop 0\n" \
"v_mfma_f32_16x16x32_bf16 a[96:99], a[160:163], a[152:155], a[96:99]\n" \
"ds_read_b64_tr_b16 a[254:255], v39 offset:41984\n" \
"v_cvt_pk_bf16_f32 v195, v182, v183\n" \
"v_mfma_f32_16x16x32_bf16 a[96:99], a[224:227], a[216:219], a[96:99]\n" \
"v_accvgpr_read_b32 v196, a16\n" \
"v_accvgpr_read_b32 v197, a17\n" \
"v_mfma_f32_16x16x32_bf16 a[100:103], a[164:167], a[152:155], a[100:103]\n" \
"v_accvgpr_read_b32 v198, a18\n" \
"v_accvgpr_read_b32 v199, a19\n" \
"v_mfma_f32_16x16x32_bf16 a[100:103], a[228:231], a[216:219], a[100:103]\n" \
"v_accvgpr_read_b32 v180, a20\n" \
"v_accvgpr_read_b32 v181, a21\n" \
"v_mfma_f32_16x16x32_bf16 a[104:107], a[168:171], a[152:155], a[104:107]\n" \
"s_add_u32 s89, s41, 0x1000\n" \
"buffer_store_dwordx4 v[192:195], v20, s[80:83], s89 offen offset:1024 sc1\n" \
"v_mfma_f32_16x16x32_bf16 a[104:107], a[232:235], a[216:219], a[104:107]\n" \
"v_accvgpr_read_b32 v182, a22\n" \
"v_accvgpr_read_b32 v183, a23\n" \
"v_mfma_f32_16x16x32_bf16 a[108:111], a[172:175], a[152:155], a[108:111]\n" \
"v_cvt_pk_bf16_f32 v196, v196, v197\n" \
"v_cvt_pk_bf16_f32 v197, v198, v199\n" \
"v_mfma_f32_16x16x32_bf16 a[108:111], a[236:239], a[216:219], a[108:111]\n" \
"v_cvt_pk_bf16_f32 v198, v180, v181\n" \
"v_cvt_pk_bf16_f32 v199, v182, v183\n" \
"v_mfma_f32_16x16x32_bf16 a[124:127], a[172:175], a[156:159], a[124:127]\n" \
"v_accvgpr_read_b32 v200, a32\n" \
"v_accvgpr_read_b32 v201, a33\n" \
"v_mfma_f32_16x16x32_bf16 a[124:127], a[236:239], a[220:223], a[124:127]\n" \
"v_accvgpr_read_b32 v202, a34\n" \
"v_accvgpr_read_b32 v203, a35\n" \
"v_mfma_f32_16x16x32_bf16 a[120:123], a[168:171], a[156:159], a[120:123]\n" \
"s_add_u32 s88, s41, 0x1000\n" \
"buffer_store_dwordx4 v[196:199], v20, s[80:83], s88 offen offset:0 sc1\n" \
"v_mfma_f32_16x16x32_bf16 a[120:123], a[232:235], a[220:223], a[120:123]\n" \
"v_accvgpr_read_b32 v180, a36\n" \
"v_accvgpr_read_b32 v181, a37\n" \
"v_mfma_f32_16x16x32_bf16 a[116:119], a[164:167], a[156:159], a[116:119]\n" \
"v_accvgpr_read_b32 v182, a38\n" \
"v_accvgpr_read_b32 v183, a39\n" \
"v_mfma_f32_16x16x32_bf16 a[116:119], a[228:231], a[220:223], a[116:119]\n" \
"v_cvt_pk_bf16_f32 v200, v200, v201\n" \
"v_cvt_pk_bf16_f32 v201, v202, v203\n" \
"v_mfma_f32_16x16x32_bf16 a[112:115], a[160:163], a[156:159], a[112:115]\n" \
"v_cvt_pk_bf16_f32 v202, v180, v181\n" \
"v_cvt_pk_bf16_f32 v203, v182, v183\n" \
"v_mfma_f32_16x16x32_bf16 a[112:115], a[224:227], a[220:223], a[112:115]\n" \
"v_accvgpr_read_b32 v204, a40\n" \
"v_accvgpr_read_b32 v205, a41\n" \
"s_waitcnt vmcnt(12) lgkmcnt(0)\n" \
"s_barrier\n" \
"v_mov_b32_e32 v36, v32\n" \
"v_xor_b32_e32 v32, 0x0, v48\n" \
"v_mov_b32_e32 v37, v33\n" \
"v_xor_b32_e32 v33, 0x20, v48\n" \
"v_mov_b32_e32 v38, v34\n" \
"v_xor_b32_e32 v34, 0x40, v48\n" \
"v_mov_b32_e64 v39, v35\n" \
"v_xor_b32_e32 v35, 0x60, v48\n" \
"v_add_u32_e32 v22, s44, v28\n" \
"v_add_u32_e32 v28, s43, v28\n" \
"v_add_u32_e32 v23, s44, v29\n" \
"v_add_u32_e32 v29, s43, v29\n" \
"v_mfma_f32_16x16x32_bf16 v[52:55], a[176:179], a[128:131], v[52:55]\n" \
"s_add_u32 m0, s40, 0x8000\n" \
"s_add_u32 s85, s39, 0x2000\n" \
"v_mfma_f32_16x16x32_bf16 v[52:55], a[240:243], a[192:195], v[52:55]\n" \
"buffer_load_dwordx4 v20, s[80:83], s85 offen sc1 lds\n" \
"v_mfma_f32_16x16x32_bf16 v[56:59], a[180:183], a[128:131], v[56:59]\n" \
"ds_read_b64_tr_b16 a[160:161], v32 offset:0\n" \
"v_accvgpr_read_b32 v206, a42\n" \
"v_mfma_f32_16x16x32_bf16 v[56:59], a[244:247], a[192:195], v[56:59]\n" \
"s_add_u32 m0, s40, 0x9000\n" \
"s_add_u32 s85, s39, 0x3000\n" \
"v_mfma_f32_16x16x32_bf16 v[60:63], a[184:187], a[128:131], v[60:63]\n" \
"buffer_load_dwordx4 v20, s[80:83], s85 offen sc1 lds\n" \
"v_mfma_f32_16x16x32_bf16 v[60:63], a[248:251], a[192:195], v[60:63]\n" \
"ds_read_b64_tr_b16 a[162:163], v32 offset:512\n" \
"v_accvgpr_read_b32 v207, a43\n" \
"v_mfma_f32_16x16x32_bf16 v[64:67], a[188:191], a[128:131], v[64:67]\n" \
"ds_read_b64_tr_b16 a[224:225], v32 offset:4096\n" \
"v_accvgpr_read_b32 v180, a44\n" \
"v_mfma_f32_16x16x32_bf16 v[64:67], a[252:255], a[192:195], v[64:67]\n" \
"ds_read_b128 a[128:131], v28 offset:0\n" \
"v_accvgpr_read_b32 v181, a45\n" \
"v_mfma_f32_16x16x32_bf16 v[80:83], a[188:191], a[132:135], v[80:83]\n" \
"ds_read_b128 a[192:195], v29 offset:0\n" \
"v_accvgpr_read_b32 v182, a46\n" \
"v_mfma_f32_16x16x32_bf16 v[80:83], a[252:255], a[196:199], v[80:83]\n" \
"s_add_u32 m0, s40, 0xb000\n" \
"s_movk_i32 s84, 0x80\n" \
"v_mfma_f32_16x16x32_bf16 v[76:79], a[184:187], a[132:135], v[76:79]\n" \
"buffer_load_dwordx4 v21, s[76:79], s84 offen offset:0 lds\n" \
"v_mfma_f32_16x16x32_bf16 v[76:79], a[248:251], a[196:199], v[76:79]\n" \
"s_add_u32 m0, s30, 0x8000\n" \
"s_movk_i32 s84, 0x80\n" \
"v_mfma_f32_16x16x32_bf16 v[72:75], a[180:183], a[132:135], v[72:75]\n" \
"buffer_load_dwordx4 v24, s[76:79], s84 offen offset:0 lds\n" \
"v_mfma_f32_16x16x32_bf16 v[72:75], a[244:247], a[196:199], v[72:75]\n" \
"ds_read_b64_tr_b16 a[226:227], v32 offset:4608\n" \
"v_accvgpr_read_b32 v183, a47\n" \
"v_mfma_f32_16x16x32_bf16 v[68:71], a[176:179], a[132:135], v[68:71]\n" \
"ds_read_b64_tr_b16 a[164:165], v33 offset:0\n" \
"v_cvt_pk_bf16_f32 v204, v204, v205\n" \
"v_mfma_f32_16x16x32_bf16 v[68:71], a[240:243], a[196:199], v[68:71]\n" \
"ds_read_b128 a[132:135], v28 offset:2048\n" \
"v_cvt_pk_bf16_f32 v205, v206, v207\n" \
"v_mfma_f32_16x16x32_bf16 v[84:87], a[176:179], a[136:139], v[84:87]\n" \
"ds_read_b128 a[196:199], v29 offset:2048\n" \
"v_cvt_pk_bf16_f32 v206, v180, v181\n" \
"v_mfma_f32_16x16x32_bf16 v[84:87], a[240:243], a[200:203], v[84:87]\n" \
"s_movk_i32 s84, 0x2080\n" \
"v_mfma_f32_16x16x32_bf16 v[88:91], a[180:183], a[136:139], v[88:91]\n" \
"buffer_load_dwordx4 v25, s[76:79], s84 offen offset:1024 lds\n" \
"v_mfma_f32_16x16x32_bf16 v[88:91], a[244:247], a[200:203], v[88:91]\n" \
"s_movk_i32 s84, 0x4080\n" \
"v_mfma_f32_16x16x32_bf16 v[92:95], a[184:187], a[136:139], v[92:95]\n" \
"buffer_load_dwordx4 v24, s[76:79], s84 offen offset:2048 lds\n" \
"v_mfma_f32_16x16x32_bf16 v[92:95], a[248:251], a[200:203], v[92:95]\n" \
"ds_read_b64_tr_b16 a[166:167], v33 offset:512\n" \
"v_cvt_pk_bf16_f32 v207, v182, v183\n" \
"v_mfma_f32_16x16x32_bf16 v[96:99], a[188:191], a[136:139], v[96:99]\n" \
"ds_read_b64_tr_b16 a[228:229], v33 offset:4096\n" \
"v_accvgpr_read_b32 v208, a56\n" \
"v_mfma_f32_16x16x32_bf16 v[96:99], a[252:255], a[200:203], v[96:99]\n" \
"ds_read_b128 a[136:139], v28 offset:4096\n" \
"v_accvgpr_read_b32 v209, a57\n" \
"v_mfma_f32_16x16x32_bf16 v[112:115], a[188:191], a[140:143], v[112:115]\n" \
"ds_read_b128 a[200:203], v29 offset:4096\n" \
"v_accvgpr_read_b32 v210, a58\n" \
"v_mfma_f32_16x16x32_bf16 v[112:115], a[252:255], a[204:207], v[112:115]\n" \
"s_movk_i32 s84, 0x6080\n" \
"v_mfma_f32_16x16x32_bf16 v[108:111], a[184:187], a[140:143], v[108:111]\n" \
"buffer_load_dwordx4 v25, s[76:79], s84 offen offset:3072 lds\n" \
"v_mfma_f32_16x16x32_bf16 v[108:111], a[248:251], a[204:207], v[108:111]\n" \
"s_add_u32 m0, s31, 0x8000\n" \
"s_mov_b32 s84, 0x80000\n" \
"v_mfma_f32_16x16x32_bf16 v[104:107], a[180:183], a[140:143], v[104:107]\n" \
"buffer_load_dwordx4 v26, s[68:71], s84 offen offset:0 lds\n" \
"v_mfma_f32_16x16x32_bf16 v[104:107], a[244:247], a[204:207], v[104:107]\n" \
"s_addk_i32 s84, 0x7c00\n" \
"v_mfma_f32_16x16x32_bf16 v[100:103], a[176:179], a[140:143], v[100:103]\n" \
"buffer_load_dwordx4 v26, s[68:71], s84 offen offset:1024 lds\n" \
"v_mfma_f32_16x16x32_bf16 v[100:103], a[240:243], a[204:207], v[100:103]\n" \
"ds_read_b128 a[140:143], v28 offset:6144\n" \
"v_accvgpr_read_b32 v211, a59\n" \
"v_mfma_f32_16x16x32_bf16 v[116:119], a[176:179], a[144:147], v[116:119]\n" \
"ds_read_b128 a[204:207], v29 offset:6144\n" \
"v_accvgpr_read_b32 v180, a60\n" \
"v_mfma_f32_16x16x32_bf16 v[116:119], a[240:243], a[208:211], v[116:119]\n" \
"s_addk_i32 s84, 0x7c00\n" \
"v_mfma_f32_16x16x32_bf16 v[120:123], a[180:183], a[144:147], v[120:123]\n" \
"buffer_load_dwordx4 v27, s[68:71], s84 offen offset:2048 lds\n" \
"v_mfma_f32_16x16x32_bf16 v[120:123], a[244:247], a[208:211], v[120:123]\n" \
"ds_read_b64_tr_b16 a[230:231], v33 offset:4608\n" \
"v_accvgpr_read_b32 v181, a61\n" \
"v_mfma_f32_16x16x32_bf16 v[124:127], a[184:187], a[144:147], v[124:127]\n" \
"s_addk_i32 s84, 0x7c00\n" \
"v_mfma_f32_16x16x32_bf16 v[124:127], a[248:251], a[208:211], v[124:127]\n" \
"buffer_load_dwordx4 v27, s[68:71], s84 offen offset:3072 lds\n" \
"v_mfma_f32_16x16x32_bf16 v[128:131], a[188:191], a[144:147], v[128:131]\n" \
"ds_read_b64_tr_b16 a[172:173], v35 offset:0\n" \
"v_accvgpr_read_b32 v182, a62\n" \
"v_mfma_f32_16x16x32_bf16 v[128:131], a[252:255], a[208:211], v[128:131]\n" \
"ds_read_b128 a[144:147], v28 offset:8192\n" \
"v_accvgpr_read_b32 v183, a63\n" \
"v_mfma_f32_16x16x32_bf16 v[144:147], a[188:191], a[148:151], v[144:147]\n" \
"ds_read_b128 a[208:211], v29 offset:8192\n" \
"v_cvt_pk_bf16_f32 v208, v208, v209\n" \
"v_mfma_f32_16x16x32_bf16 v[144:147], a[252:255], a[212:215], v[144:147]\n" \
"s_add_u32 m0, s30, 0x9000\n" \
"s_mov_b32 s84, 0x9080\n" \
"v_mfma_f32_16x16x32_bf16 v[140:143], a[184:187], a[148:151], v[140:143]\n" \
"buffer_load_dwordx4 v24, s[76:79], s84 offen offset:0 lds\n" \
"v_mfma_f32_16x16x32_bf16 v[140:143], a[248:251], a[212:215], v[140:143]\n" \
"ds_read_b64_tr_b16 a[174:175], v35 offset:512\n" \
"v_cvt_pk_bf16_f32 v209, v210, v211\n" \
"v_mfma_f32_16x16x32_bf16 v[136:139], a[180:183], a[148:151], v[136:139]\n" \
"s_addk_i32 s84, 0x2000\n" \
"s_nop 0\n" \
"v_mfma_f32_16x16x32_bf16 v[136:139], a[244:247], a[212:215], v[136:139]\n" \
"buffer_load_dwordx4 v25, s[76:79], s84 offen offset:1024 lds\n" \
"v_mfma_f32_16x16x32_bf16 v[132:135], a[176:179], a[148:151], v[132:135]\n" \
"ds_read_b64_tr_b16 a[236:237], v35 offset:4096\n" \
"v_cvt_pk_bf16_f32 v210, v180, v181\n" \
"v_mfma_f32_16x16x32_bf16 v[132:135], a[240:243], a[212:215], v[132:135]\n" \
"ds_read_b128 a[148:151], v28 offset:10240\n" \
"v_cvt_pk_bf16_f32 v211, v182, v183\n" \
"v_mfma_f32_16x16x32_bf16 v[148:151], a[176:179], a[152:155], v[148:151]\n" \
"ds_read_b128 a[212:215], v29 offset:10240\n" \
"v_accvgpr_read_b32 v212, a48\n" \
"v_mfma_f32_16x16x32_bf16 v[148:151], a[240:243], a[216:219], v[148:151]\n" \
"ds_read_b64_tr_b16 a[238:239], v35 offset:4608\n" \
"v_accvgpr_read_b32 v213, a49\n" \
"v_mfma_f32_16x16x32_bf16 v[152:155], a[180:183], a[152:155], v[152:155]\n" \
"ds_read_b64_tr_b16 a[168:169], v34 offset:0\n" \
"v_accvgpr_read_b32 v214, a50\n" \
"v_mfma_f32_16x16x32_bf16 v[152:155], a[244:247], a[216:219], v[152:155]\n" \
"ds_read_b64_tr_b16 a[170:171], v34 offset:512\n" \
"v_accvgpr_read_b32 v215, a51\n" \
"v_mfma_f32_16x16x32_bf16 v[156:159], a[184:187], a[152:155], v[156:159]\n" \
"ds_read_b64_tr_b16 a[232:233], v34 offset:4096\n" \
"v_accvgpr_read_b32 v180, a52\n" \
"v_mfma_f32_16x16x32_bf16 v[156:159], a[248:251], a[216:219], v[156:159]\n" \
"ds_read_b64_tr_b16 a[234:235], v34 offset:4608\n" \
"v_accvgpr_read_b32 v181, a53\n" \
"v_mfma_f32_16x16x32_bf16 v[160:163], a[188:191], a[152:155], v[160:163]\n" \
"ds_read_b128 v[248:251], v22 offset:0\n" \
"v_accvgpr_read_b32 v182, a54\n" \
"v_mfma_f32_16x16x32_bf16 v[160:163], a[252:255], a[216:219], v[160:163]\n" \
"ds_read_b128 a[152:155], v28 offset:12288\n" \
"v_accvgpr_read_b32 v183, a55\n" \
"v_mfma_f32_16x16x32_bf16 v[176:179], a[188:191], a[156:159], v[176:179]\n" \
"ds_read_b128 a[216:219], v29 offset:12288\n" \
"v_cvt_pk_bf16_f32 v212, v212, v213\n" \
"v_mfma_f32_16x16x32_bf16 v[176:179], a[252:255], a[220:223], v[176:179]\n" \
"ds_read_b128 v[48:51], v23 offset:0\n" \
"v_cvt_pk_bf16_f32 v213, v214, v215\n" \
"v_mfma_f32_16x16x32_bf16 v[172:175], a[184:187], a[156:159], v[172:175]\n" \
"v_cvt_pk_bf16_f32 v214, v180, v181\n" \
"v_cvt_pk_bf16_f32 v215, v182, v183\n" \
"v_mfma_f32_16x16x32_bf16 v[172:175], a[248:251], a[220:223], v[172:175]\n" \
"v_accvgpr_read_b32 v216, a64\n" \
"v_accvgpr_read_b32 v217, a65\n" \
"v_mfma_f32_16x16x32_bf16 v[168:171], a[180:183], a[156:159], v[168:171]\n" \
"v_accvgpr_read_b32 v218, a66\n" \
"v_accvgpr_read_b32 v219, a67\n" \
"v_mfma_f32_16x16x32_bf16 v[168:171], a[244:247], a[220:223], v[168:171]\n" \
"v_accvgpr_read_b32 v180, a68\n" \
"v_accvgpr_read_b32 v181, a69\n" \
"v_mfma_f32_16x16x32_bf16 v[164:167], a[176:179], a[156:159], v[164:167]\n" \
"v_accvgpr_read_b32 v182, a70\n" \
"v_accvgpr_read_b32 v183, a71\n" \
"v_mfma_f32_16x16x32_bf16 v[164:167], a[240:243], a[220:223], v[164:167]\n" \
"ds_read_b128 a[156:159], v28 offset:14336\n" \
"ds_read_b128 a[220:223], v29 offset:14336\n" \
"s_waitcnt lgkmcnt(15)\n" \
"v_mfma_f32_16x16x32_bf16 a[0:3], a[160:163], a[128:131], 0\n" \
"s_addk_i32 s84, 0x2000\n" \
"v_cvt_pk_bf16_f32 v216, v216, v217\n" \
"v_mfma_f32_16x16x32_bf16 a[0:3], a[224:227], a[192:195], a[0:3]\n" \
"buffer_load_dwordx4 v24, s[76:79], s84 offen offset:2048 lds\n" \
"v_cvt_pk_bf16_f32 v217, v218, v219\n" \
"v_mfma_f32_16x16x32_bf16 a[4:7], a[164:167], a[128:131], 0\n" \
"ds_read_b64_tr_b16 a[176:177], v36 offset:0\n" \
"v_cvt_pk_bf16_f32 v218, v180, v181\n" \
"v_mfma_f32_16x16x32_bf16 a[4:7], a[228:231], a[192:195], a[4:7]\n" \
"s_addk_i32 s84, 0x2000\n" \
"v_cvt_pk_bf16_f32 v219, v182, v183\n" \
"s_waitcnt lgkmcnt(15)\n" \
"v_mfma_f32_16x16x32_bf16 a[12:15], a[172:175], a[128:131], 0\n" \
"buffer_load_dwordx4 v25, s[76:79], s84 offen offset:3072 lds\n" \
"v_accvgpr_read_b32 v220, a80\n" \
"s_waitcnt lgkmcnt(11)\n" \
"v_mfma_f32_16x16x32_bf16 a[12:15], a[236:239], a[192:195], a[12:15]\n" \
"ds_read_b64_tr_b16 a[178:179], v36 offset:1024\n" \
"v_accvgpr_read_b32 v221, a81\n" \
"s_waitcnt lgkmcnt(10)\n" \
"v_mfma_f32_16x16x32_bf16 a[8:11], a[128:131], a[168:171], 0\n" \
"ds_read_b64_tr_b16 a[240:241], v36 offset:8192\n" \
"v_accvgpr_read_b32 v222, a82\n" \
"s_waitcnt lgkmcnt(9)\n" \
"s_nop 0\n" \
"v_mfma_f32_16x16x32_bf16 a[8:11], a[192:195], a[232:235], a[8:11]\n" \
"ds_read_b64_tr_b16 a[242:243], v36 offset:9216\n" \
"v_accvgpr_read_b32 v223, a83\n" \
"v_mfma_f32_16x16x32_bf16 a[24:27], a[132:135], a[168:171], 0\n" \
"ds_read_b64_tr_b16 a[180:181], v37 offset:0\n" \
"v_accvgpr_read_b32 v180, a84\n" \
"v_mfma_f32_16x16x32_bf16 a[24:27], a[196:199], a[232:235], a[24:27]\n" \
"ds_read_b64_tr_b16 a[182:183], v37 offset:1024\n" \
"v_accvgpr_read_b32 v181, a85\n" \
"v_mfma_f32_16x16x32_bf16 a[28:31], a[172:175], a[132:135], 0\n" \
"ds_read_b64_tr_b16 a[244:245], v37 offset:8192\n" \
"v_accvgpr_read_b32 v182, a86\n" \
"v_mfma_f32_16x16x32_bf16 a[28:31], a[236:239], a[196:199], a[28:31]\n" \
"s_add_u32 s89, s41, 0x2000\n" \
"buffer_store_dwordx4 v[200:203], v20, s[80:83], s89 offen offset:0 sc1\n" \
"v_mfma_f32_16x16x32_bf16 a[20:23], a[164:167], a[132:135], 0\n" \
"ds_read_b64_tr_b16 a[246:247], v37 offset:9216\n" \
"v_accvgpr_read_b32 v183, a87\n" \
"v_mfma_f32_16x16x32_bf16 a[20:23], a[228:231], a[196:199], a[20:23]\n" \
"ds_read_b64_tr_b16 a[184:185], v38 offset:0\n" \
"v_cvt_pk_bf16_f32 v220, v220, v221\n" \
"v_mfma_f32_16x16x32_bf16 a[16:19], a[160:163], a[132:135], 0\n" \
"ds_read_b64_tr_b16 a[186:187], v38 offset:1024\n" \
"v_cvt_pk_bf16_f32 v221, v222, v223\n" \
"v_mfma_f32_16x16x32_bf16 a[16:19], a[224:227], a[196:199], a[16:19]\n" \
"v_cvt_pk_bf16_f32 v222, v180, v181\n" \
"v_cvt_pk_bf16_f32 v223, v182, v183\n" \
"v_cvt_pk_bf16_f32 v224, v52, v53\n" \
".fill (L_pb + 20192 - .) / 4, 4, 0xBF800000\n" \
"L_pp5:\n" \
"s_cbranch_execz L_pret\n" \
"s_nop 0\n" \
"v_mfma_f32_16x16x32_bf16 a[32:35], a[160:163], a[136:139], 0\n" \
"s_add_u32 s88, s41, 0x2000\n" \
"buffer_store_dwordx4 v[204:207], v20, s[80:83], s88 offen offset:1024 sc1\n" \
"v_mfma_f32_16x16x32_bf16 a[32:35], a[224:227], a[200:203], a[32:35]\n" \
"ds_read_b64_tr_b16 a[248:249], v38 offset:8192\n" \
"v_cvt_pk_bf16_f32 v225, v54, v55\n" \
"v_mfma_f32_16x16x32_bf16 a[36:39], a[164:167], a[136:139], 0\n" \
"ds_read_b64_tr_b16 a[250:251], v38 offset:9216\n" \
"v_cvt_pk_bf16_f32 v226, v56, v57\n" \
"v_mfma_f32_16x16x32_bf16 a[36:39], a[228:231], a[200:203], a[36:39]\n" \
"ds_read_b64_tr_b16 a[188:189], v39 offset:0\n" \
"v_cvt_pk_bf16_f32 v227, v58, v59\n" \
"v_mfma_f32_16x16x32_bf16 a[44:47], a[172:175], a[136:139], 0\n" \
"v_cvt_pk_bf16_f32 v228, v60, v61\n" \
"v_cvt_pk_bf16_f32 v229, v62, v63\n" \
"v_cvt_pk_bf16_f32 v230, v64, v65\n" \
"v_mfma_f32_16x16x32_bf16 a[44:47], a[236:239], a[200:203], a[44:47]\n" \
"ds_read_b64_tr_b16 a[190:191], v39 offset:1024\n" \
"v_cvt_pk_bf16_f32 v231, v66, v67\n" \
"v_mfma_f32_16x16x32_bf16 a[40:43], a[136:139], a[168:171], 0\n" \
"s_add_u32 s89, s41, 0x3000\n" \
"buffer_store_dwordx4 v[208:211], v20, s[80:83], s89 offen offset:1024 sc1\n" \
"v_mfma_f32_16x16x32_bf16 a[40:43], a[200:203], a[232:235], a[40:43]\n" \
"ds_read_b64_tr_b16 a[252:253], v39 offset:8192\n" \
"v_cvt_pk_bf16_f32 v232, v76, v77\n" \
"v_mfma_f32_16x16x32_bf16 a[56:59], a[140:143], a[168:171], 0\n" \
"ds_read_b64_tr_b16 a[254:255], v39 offset:9216\n" \
"v_cvt_pk_bf16_f32 v233, v78, v79\n" \
"v_mfma_f32_16x16x32_bf16 a[56:59], a[204:207], a[232:235], a[56:59]\n" \
"v_cvt_pk_bf16_f32 v234, v80, v81\n" \
"v_cvt_pk_bf16_f32 v235, v82, v83\n" \
"v_cvt_pk_bf16_f32 v236, v68, v69\n" \
"v_mfma_f32_16x16x32_bf16 a[60:63], a[172:175], a[140:143], 0\n" \
"v_cvt_pk_bf16_f32 v237, v70, v71\n" \
"v_cvt_pk_bf16_f32 v238, v72, v73\n" \
"v_cvt_pk_bf16_f32 v239, v74, v75\n" \
"v_mfma_f32_16x16x32_bf16 a[60:63], a[236:239], a[204:207], a[60:63]\n" \
"s_add_u32 s88, s41, 0x3000\n" \
"buffer_store_dwordx4 v[212:215], v20, s[80:83], s88 offen offset:0 sc1\n" \
"v_mfma_f32_16x16x32_bf16 a[52:55], a[164:167], a[140:143], 0\n" \
"v_cvt_pk_bf16_f32 v240, v84, v85\n" \
"v_cvt_pk_bf16_f32 v241, v86, v87\n" \
"v_cvt_pk_bf16_f32 v242, v88, v89\n" \
"v_mfma_f32_16x16x32_bf16 a[52:55], a[228:231], a[204:207], a[52:55]\n" \
"v_cvt_pk_bf16_f32 v243, v90, v91\n" \
"v_cvt_pk_bf16_f32 v244, v92, v93\n" \
"v_cvt_pk_bf16_f32 v245, v94, v95\n" \
"v_mfma_f32_16x16x32_bf16 a[48:51], a[160:163], a[140:143], 0\n" \
"v_cvt_pk_bf16_f32 v246, v96, v97\n" \
"v_cvt_pk_bf16_f32 v247, v98, v99\n" \
"v_cvt_pk_bf16_f32 v184, v108, v109\n" \
"v_mfma_f32_16x16x32_bf16 a[48:51], a[224:227], a[204:207], a[48:51]\n" \
"v_cvt_pk_bf16_f32 v185, v110, v111\n" \
"v_cvt_pk_bf16_f32 v186, v112, v113\n" \
"v_cvt_pk_bf16_f32 v187, v114, v115\n" \
"s_waitcnt lgkmcnt(15)\n" \
"s_nop 0\n" \
"v_mfma_f32_16x16x32_bf16 a[64:67], a[164:167], v[248:251], 0\n" \
"v_cvt_pk_bf16_f32 v188, v100, v101\n" \
"v_cvt_pk_bf16_f32 v189, v102, v103\n" \
"v_cvt_pk_bf16_f32 v190, v104, v105\n" \
"v_mfma_f32_16x16x32_bf16 a[64:67], a[228:231], v[48:51], a[64:67]\n" \
"v_cvt_pk_bf16_f32 v191, v106, v107\n" \
"v_cvt_pk_bf16_f32 v192, v116, v117\n" \
"v_cvt_pk_bf16_f32 v193, v118, v119\n" \
"v_mfma_f32_16x16x32_bf16 a[80:83], a[172:175], v[248:251], 0\n" \
"v_cvt_pk_bf16_f32 v194, v120, v121\n" \
"v_cvt_pk_bf16_f32 v195, v122, v123\n" \
"v_cvt_pk_bf16_f32 v196, v124, v125\n" \
"v_mfma_f32_16x16x32_bf16 a[80:83], a[236:239], v[48:51], a[80:83]\n" \
"v_cvt_pk_bf16_f32 v197, v126, v127\n" \
"v_cvt_pk_bf16_f32 v198, v128, v129\n" \
"v_cvt_pk_bf16_f32 v199, v130, v131\n" \
"v_mfma_f32_16x16x32_bf16 a[68:71], v[248:251], a[168:171], 0\n" \
"v_cvt_pk_bf16_f32 v252, v140, v141\n" \
"v_cvt_pk_bf16_f32 v253, v142, v143\n" \
"v_cvt_pk_bf16_f32 v254, v144, v145\n" \
"v_mfma_f32_16x16x32_bf16 a[68:71], v[48:51], a[232:235], a[68:71]\n" \
"v_cvt_pk_bf16_f32 v255, v146, v147\n" \
"v_accvgpr_write_b32 a84, v252\n" \
"v_accvgpr_write_b32 a85, v253\n" \
"s_waitcnt vmcnt(4) lgkmcnt(0)\n" \
"s_barrier\n" \
"v_mfma_f32_16x16x32_bf16 v[52:55], a[176:179], a[128:131], 0\n" \
"s_add_u32 m0, s40, 0x0\n" \
"s_nop 0\n" \
"s_add_u32 s85, s39, 0x4000\n" \
"v_accvgpr_write_b32 a86, v254\n" \
"v_mfma_f32_16x16x32_bf16 v[52:55], a[240:243], a[192:195], v[52:55]\n" \
"buffer_load_dwordx4 v20, s[80:83], s85 offen sc1 lds\n" \
"v_accvgpr_write_b32 a87, v255\n" \
"v_mfma_f32_16x16x32_bf16 v[56:59], a[180:183], a[128:131], 0\n" \
"ds_read_b64_tr_b16 a[160:161], v32 offset:32768\n" \
"v_cvt_pk_bf16_f32 v252, v132, v133\n" \
"v_mfma_f32_16x16x32_bf16 v[56:59], a[244:247], a[192:195], v[56:59]\n" \
"ds_read_b64_tr_b16 a[162:163], v32 offset:33280\n" \
"v_cvt_pk_bf16_f32 v253, v134, v135\n" \
"v_mfma_f32_16x16x32_bf16 v[60:63], a[184:187], a[128:131], 0\n" \
"s_add_u32 m0, s40, 0x1000\n" \
"s_add_u32 s85, s39, 0x5000\n" \
"v_cvt_pk_bf16_f32 v254, v136, v137\n" \
"v_mfma_f32_16x16x32_bf16 v[60:63], a[248:251], a[192:195], v[60:63]\n" \
"buffer_load_dwordx4 v20, s[80:83], s85 offen sc1 lds\n" \
"v_cvt_pk_bf16_f32 v255, v138, v139\n" \
"v_mfma_f32_16x16x32_bf16 v[64:67], a[188:191], a[128:131], 0\n" \
"ds_read_b64_tr_b16 a[224:225], v32 offset:36864\n" \
"v_cvt_pk_bf16_f32 v200, v148, v149\n" \
"v_mfma_f32_16x16x32_bf16 v[64:67], a[252:255], a[192:195], v[64:67]\n" \
"ds_read_b128 a[128:131], v28 offset:32768\n" \
"v_cvt_pk_bf16_f32 v201, v150, v151\n" \
"v_mfma_f32_16x16x32_bf16 v[80:83], a[188:191], a[132:135], 0\n" \
"ds_read_b128 a[192:195], v29 offset:32768\n" \
"v_cvt_pk_bf16_f32 v202, v152, v153\n" \
"v_mfma_f32_16x16x32_bf16 v[80:83], a[252:255], a[196:199], v[80:83]\n" \
"s_add_u32 m0, s40, 0x3000\n" \
"s_movk_i32 s84, 0x100\n" \
"s_nop 0\n" \
"v_cvt_pk_bf16_f32 v203, v154, v155\n" \
"v_mfma_f32_16x16x32_bf16 v[76:79], a[184:187], a[132:135], 0\n" \
"buffer_load_dwordx4 v21, s[76:79], s84 offen offset:0 lds\n" \
"v_cvt_pk_bf16_f32 v204, v156, v157\n" \
"v_mfma_f32_16x16x32_bf16 v[76:79], a[248:251], a[196:199], v[76:79]\n" \
"ds_read_b64_tr_b16 a[226:227], v32 offset:37376\n" \
"v_cvt_pk_bf16_f32 v205, v158, v159\n" \
"v_mfma_f32_16x16x32_bf16 v[72:75], a[180:183], a[132:135], 0\n" \
"ds_read_b64_tr_b16 a[164:165], v33 offset:32768\n" \
"v_cvt_pk_bf16_f32 v206, v160, v161\n" \
"v_mfma_f32_16x16x32_bf16 v[72:75], a[244:247], a[196:199], v[72:75]\n" \
"v_cvt_pk_bf16_f32 v207, v162, v163\n" \
"v_cvt_pk_bf16_f32 v208, v172, v173\n" \
"v_cvt_pk_bf16_f32 v209, v174, v175\n" \
"v_mfma_f32_16x16x32_bf16 v[68:71], a[176:179], a[132:135], 0\n" \
"ds_read_b64_tr_b16 a[166:167], v33 offset:33280\n" \
"v_cvt_pk_bf16_f32 v210, v176, v177\n" \
"v_mfma_f32_16x16x32_bf16 v[68:71], a[240:243], a[196:199], v[68:71]\n" \
"ds_read_b128 a[132:135], v28 offset:34816\n" \
"v_cvt_pk_bf16_f32 v211, v178, v179\n" \
"v_mfma_f32_16x16x32_bf16 v[84:87], a[176:179], a[136:139], 0\n" \
"ds_read_b128 a[196:199], v29 offset:34816\n" \
"v_cvt_pk_bf16_f32 v212, v164, v165\n" \
"v_mfma_f32_16x16x32_bf16 v[84:87], a[240:243], a[200:203], v[84:87]\n" \
"s_add_u32 m0, s31, 0x0\n" \
"s_mov_b32 s84, 0x100000\n" \
"v_cvt_pk_bf16_f32 v213, v166, v167\n" \
"v_mfma_f32_16x16x32_bf16 v[88:91], a[180:183], a[136:139], 0\n" \
"buffer_load_dwordx4 v26, s[68:71], s84 offen offset:0 lds\n" \
"v_cvt_pk_bf16_f32 v214, v168, v169\n" \
"v_mfma_f32_16x16x32_bf16 v[88:91], a[244:247], a[200:203], v[88:91]\n" \
"s_add_u32 s89, s41, 0x0\n" \
"buffer_store_dwordx4 v[224:227], v20, s[80:83], s89 offen offset:2048 sc1\n" \
"v_mfma_f32_16x16x32_bf16 v[92:95], a[184:187], a[136:139], 0\n" \
"ds_read_b64_tr_b16 a[228:229], v33 offset:36864\n" \
"v_cvt_pk_bf16_f32 v215, v170, v171\n" \
"v_mfma_f32_16x16x32_bf16 v[92:95], a[248:251], a[200:203], v[92:95]\n" \
"ds_read_b64_tr_b16 a[230:231], v33 offset:37376\n" \
"v_mfma_f32_16x16x32_bf16 v[96:99], a[188:191], a[136:139], 0\n" \
"v_mfma_f32_16x16x32_bf16 v[96:99], a[252:255], a[200:203], v[96:99]\n" \
"ds_read_b128 a[136:139], v28 offset:36864\n" \
"v_mfma_f32_16x16x32_bf16 v[112:115], a[188:191], a[140:143], 0\n" \
"ds_read_b128 a[200:203], v29 offset:36864\n" \
"v_mfma_f32_16x16x32_bf16 v[112:115], a[252:255], a[204:207], v[112:115]\n" \
"s_addk_i32 s84, 0x7c00\n" \
"v_mfma_f32_16x16x32_bf16 v[108:111], a[184:187], a[140:143], 0\n" \
"buffer_load_dwordx4 v26, s[68:71], s84 offen offset:1024 lds\n" \
"v_mfma_f32_16x16x32_bf16 v[108:111], a[248:251], a[204:207], v[108:111]\n" \
"ds_read_b64_tr_b16 a[172:173], v35 offset:32768\n" \
"v_mfma_f32_16x16x32_bf16 v[104:107], a[180:183], a[140:143], 0\n" \
"s_addk_i32 s84, 0x7c00\n" \
"v_mfma_f32_16x16x32_bf16 v[104:107], a[244:247], a[204:207], v[104:107]\n" \
"buffer_load_dwordx4 v27, s[68:71], s84 offen offset:2048 lds\n" \
"v_mfma_f32_16x16x32_bf16 v[100:103], a[176:179], a[140:143], 0\n" \
"ds_read_b64_tr_b16 a[174:175], v35 offset:33280\n" \
"v_mfma_f32_16x16x32_bf16 v[100:103], a[240:243], a[204:207], v[100:103]\n" \
"ds_read_b128 a[140:143], v28 offset:38912\n" \
"v_mfma_f32_16x16x32_bf16 v[116:119], a[176:179], a[144:147], 0\n" \
"ds_read_b128 a[204:207], v29 offset:38912\n" \
"v_mfma_f32_16x16x32_bf16 v[116:119], a[240:243], a[208:211], v[116:119]\n" \
"s_addk_i32 s84, 0x7c00\n" \
"v_mfma_f32_16x16x32_bf16 v[120:123], a[180:183], a[144:147], 0\n" \
"buffer_load_dwordx4 v27, s[68:71], s84 offen offset:3072 lds\n" \
"v_mfma_f32_16x16x32_bf16 v[120:123], a[244:247], a[208:211], v[120:123]\n" \
"ds_read_b64_tr_b16 a[236:237], v35 offset:36864\n" \
"v_mfma_f32_16x16x32_bf16 v[124:127], a[184:187], a[144:147], 0\n" \
"ds_read_b64_tr_b16 a[238:239], v35 offset:37376\n" \
"v_mfma_f32_16x16x32_bf16 v[124:127], a[248:251], a[208:211], v[124:127]\n" \
"s_add_u32 m0, s30, 0x0\n" \
"s_movk_i32 s84, 0x180\n" \
"v_mfma_f32_16x16x32_bf16 v[128:131], a[188:191], a[144:147], 0\n" \
"buffer_load_dwordx4 v24, s[76:79], s84 offen offset:0 lds\n" \
"v_mfma_f32_16x16x32_bf16 v[128:131], a[252:255], a[208:211], v[128:131]\n" \
"ds_read_b128 a[144:147], v28 offset:40960\n" \
"v_mfma_f32_16x16x32_bf16 v[144:147], a[188:191], a[148:151], 0\n" \
"ds_read_b128 a[208:211], v29 offset:40960\n" \
"v_mfma_f32_16x16x32_bf16 v[144:147], a[252:255], a[212:215], v[144:147]\n" \
"ds_read_b64_tr_b16 a[168:169], v34 offset:32768\n" \
"v_mfma_f32_16x16x32_bf16 v[140:143], a[184:187], a[148:151], 0\n" \
"s_movk_i32 s84, 0x2180\n" \
"v_mfma_f32_16x16x32_bf16 v[140:143], a[248:251], a[212:215], v[140:143]\n" \
"buffer_load_dwordx4 v25, s[76:79], s84 offen offset:1024 lds\n" \
"v_mfma_f32_16x16x32_bf16 v[136:139], a[180:183], a[148:151], 0\n" \
"ds_read_b64_tr_b16 a[170:171], v34 offset:33280\n" \
"v_mfma_f32_16x16x32_bf16 v[136:139], a[244:247], a[212:215], v[136:139]\n" \
"ds_read_b64_tr_b16 a[232:233], v34 offset:36864\n" \
"v_mfma_f32_16x16x32_bf16 v[132:135], a[176:179], a[148:151], 0\n" \
"ds_read_b64_tr_b16 a[234:235], v34 offset:37376\n" \
"v_mfma_f32_16x16x32_bf16 v[132:135], a[240:243], a[212:215], v[132:135]\n" \
"ds_read_b128 a[148:151], v28 offset:43008\n" \
"v_mfma_f32_16x16x32_bf16 v[148:151], a[176:179], a[152:155], 0\n" \
"ds_read_b128 a[212:215], v29 offset:43008\n" \
"v_mfma_f32_16x16x32_bf16 v[148:151], a[240:243], a[216:219], v[148:151]\n" \
"s_movk_i32 s84, 0x4180\n" \
"v_mfma_f32_16x16x32_bf16 v[152:155], a[180:183], a[152:155], 0\n" \
"buffer_load_dwordx4 v24, s[76:79], s84 offen offset:2048 lds\n" \
"v_mfma_f32_16x16x32_bf16 v[152:155], a[244:247], a[216:219], v[152:155]\n" \
"s_movk_i32 s84, 0x6180\n" \
"v_mfma_f32_16x16x32_bf16 v[156:159], a[184:187], a[152:155], 0\n" \
"buffer_load_dwordx4 v25, s[76:79], s84 offen offset:3072 lds\n" \
"v_mfma_f32_16x16x32_bf16 v[156:159], a[248:251], a[216:219], v[156:159]\n" \
"ds_read_b128 v[248:251], v22 offset:32768\n" \
"v_mfma_f32_16x16x32_bf16 v[160:163], a[188:191], a[152:155], 0\n" \
"ds_read_b128 v[48:51], v23 offset:32768\n" \
"v_mfma_f32_16x16x32_bf16 v[160:163], a[252:255], a[216:219], v[160:163]\n" \
"ds_read_b128 a[152:155], v28 offset:45056\n" \
"v_mfma_f32_16x16x32_bf16 v[176:179], a[188:191], a[156:159], 0\n" \
"ds_read_b128 a[216:219], v29 offset:45056\n" \
"v_mfma_f32_16x16x32_bf16 v[176:179], a[252:255], a[220:223], v[176:179]\n" \
"v_mfma_f32_16x16x32_bf16 v[172:175], a[184:187], a[156:159], 0\n" \
"v_mfma_f32_16x16x32_bf16 v[172:175], a[248:251], a[220:223], v[172:175]\n" \
"v_mfma_f32_16x16x32_bf16 v[168:171], a[180:183], a[156:159], 0\n" \
"v_mfma_f32_16x16x32_bf16 v[168:171], a[244:247], a[220:223], v[168:171]\n" \
"v_mfma_f32_16x16x32_bf16 v[164:167], a[176:179], a[156:159], 0\n" \
"v_mfma_f32_16x16x32_bf16 v[164:167], a[240:243], a[220:223], v[164:167]\n" \
"ds_read_b128 a[156:159], v28 offset:47104\n" \
"ds_read_b128 a[220:223], v29 offset:47104\n" \
"s_waitcnt lgkmcnt(15)\n" \
"v_mfma_f32_16x16x32_bf16 a[0:3], a[160:163], a[128:131], a[0:3]\n" \
"s_add_u32 m0, s30, 0x1000\n" \
"s_addk_i32 s84, 0x3000\n" \
"v_mfma_f32_16x16x32_bf16 a[0:3], a[224:227], a[192:195], a[0:3]\n" \
"buffer_load_dwordx4 v24, s[76:79], s84 offen offset:0 lds\n" \
"v_mfma_f32_16x16x32_bf16 a[4:7], a[164:167], a[128:131], a[4:7]\n" \
"ds_read_b64_tr_b16 a[176:177], v36 offset:32768\n" \
"v_mov_b32_e64 v11, 0\n" \
"v_mfma_f32_16x16x32_bf16 a[4:7], a[228:231], a[192:195], a[4:7]\n" \
"ds_read_b64_tr_b16 a[178:179], v36 offset:33792\n" \
"s_and_b32 s6, s2, 7\n" \
"v_mfma_f32_16x16x32_bf16 a[12:15], a[172:175], a[128:131], a[12:15]\n" \
"ds_read_b64_tr_b16 a[240:241], v36 offset:40960\n" \
"s_lshl_b32 s7, s3, 1\n" \
"s_waitcnt lgkmcnt(15)\n" \
"v_mfma_f32_16x16x32_bf16 a[12:15], a[236:239], a[192:195], a[12:15]\n" \
"s_add_u32 s88, s41, 0x0\n" \
"buffer_store_dwordx4 v[228:231], v20, s[80:83], s88 offen offset:3072 sc1\n" \
"s_waitcnt lgkmcnt(13)\n" \
"v_mfma_f32_16x16x32_bf16 a[8:11], a[128:131], a[168:171], a[8:11]\n" \
"ds_read_b64_tr_b16 a[242:243], v36 offset:41984\n" \
"s_lshr_b32 s4, s2, 5\n" \
"s_waitcnt lgkmcnt(12)\n" \
"v_mfma_f32_16x16x32_bf16 a[8:11], a[192:195], a[232:235], a[8:11]\n" \
"ds_read_b64_tr_b16 a[180:181], v37 offset:32768\n" \
"s_lshl_b32 s5, s6, 2\n" \
"v_mfma_f32_16x16x32_bf16 a[24:27], a[132:135], a[168:171], a[24:27]\n" \
"ds_read_b64_tr_b16 a[182:183], v37 offset:33792\n" \
"s_bfe_u32 s8, s2, 0x20003\n" \
"v_mfma_f32_16x16x32_bf16 a[24:27], a[196:199], a[232:235], a[24:27]\n" \
"ds_read_b64_tr_b16 a[244:245], v37 offset:40960\n" \
"s_and_b32 s7, s7, 4\n" \
"v_mfma_f32_16x16x32_bf16 a[28:31], a[172:175], a[132:135], a[28:31]\n" \
"s_and_b32 s9, s3, 1\n" \
"s_or_b32 s13, s5, s8\n" \
"v_mfma_f32_16x16x32_bf16 a[28:31], a[236:239], a[196:199], a[28:31]\n" \
"s_addk_i32 s84, 0x2000\n" \
"v_mfma_f32_16x16x32_bf16 a[20:23], a[164:167], a[132:135], a[20:23]\n" \
"buffer_load_dwordx4 v25, s[76:79], s84 offen offset:1024 lds\n" \
"v_mfma_f32_16x16x32_bf16 a[20:23], a[228:231], a[196:199], a[20:23]\n" \
"ds_read_b64_tr_b16 a[246:247], v37 offset:41984\n" \
"s_or_b32 s9, s7, s9\n" \
"v_mfma_f32_16x16x32_bf16 a[16:19], a[160:163], a[132:135], a[16:19]\n" \
"ds_read_b64_tr_b16 a[184:185], v38 offset:32768\n" \
"s_mul_i32 s7, s4, 0xa00\n" \
"v_mfma_f32_16x16x32_bf16 a[16:19], a[224:227], a[196:199], a[16:19]\n" \
"ds_read_b64_tr_b16 a[186:187], v38 offset:33792\n" \
"s_add_i32 s7, s7, 0x5bc1000\n" \
"v_mfma_f32_16x16x32_bf16 a[32:35], a[160:163], a[136:139], a[32:35]\n" \
"s_addk_i32 s84, 0x2000\n" \
"v_mfma_f32_16x16x32_bf16 a[32:35], a[224:227], a[200:203], a[32:35]\n" \
"buffer_load_dwordx4 v24, s[76:79], s84 offen offset:2048 lds\n" \
"v_mfma_f32_16x16x32_bf16 a[36:39], a[164:167], a[136:139], a[36:39]\n" \
"ds_read_b64_tr_b16 a[248:249], v38 offset:40960\n" \
"s_lshl_b32 s10, s13, 2\n" \
"v_mfma_f32_16x16x32_bf16 a[36:39], a[228:231], a[200:203], a[36:39]\n" \
"ds_read_b64_tr_b16 a[250:251], v38 offset:41984\n" \
"s_or_b32 s10, s10, s7\n" \
"v_mfma_f32_16x16x32_bf16 a[44:47], a[172:175], a[136:139], a[44:47]\n" \
"ds_read_b64_tr_b16 a[188:189], v39 offset:32768\n" \
"v_writelane_b32 v11, s10, 0\n" \
"v_mfma_f32_16x16x32_bf16 a[44:47], a[236:239], a[200:203], a[44:47]\n" \
"ds_read_b64_tr_b16 a[190:191], v39 offset:33792\n" \
"s_lshl_b32 s14, s4, 3\n" \
"v_mfma_f32_16x16x32_bf16 a[40:43], a[136:139], a[168:171], a[40:43]\n" \
"s_add_u32 s89, s41, 0x1000\n" \
"buffer_store_dwordx4 v[232:235], v20, s[80:83], s89 offen offset:3072 sc1\n" \
"v_mfma_f32_16x16x32_bf16 a[40:43], a[200:203], a[232:235], a[40:43]\n" \
"ds_read_b64_tr_b16 a[252:253], v39 offset:40960\n" \
"v_writelane_b32 v11, s7, 1\n" \
"v_mfma_f32_16x16x32_bf16 a[56:59], a[140:143], a[168:171], a[56:59]\n" \
"ds_read_b64_tr_b16 a[254:255], v39 offset:41984\n" \
"s_or_b32 s7, s14, s6\n" \
"v_mfma_f32_16x16x32_bf16 a[56:59], a[204:207], a[232:235], a[56:59]\n" \
"s_lshl_b32 s7, s7, 10\n" \
"s_lshl_b32 s10, s3, 8\n" \
"v_mfma_f32_16x16x32_bf16 a[60:63], a[172:175], a[140:143], a[60:63]\n" \
"s_or_b32 s7, s7, s10\n" \
"s_or_b32 s10, s7, 0x5c00200\n" \
"v_mfma_f32_16x16x32_bf16 a[60:63], a[236:239], a[204:207], a[60:63]\n" \
"s_addk_i32 s84, 0x2000\n" \
"v_mfma_f32_16x16x32_bf16 a[52:55], a[164:167], a[140:143], a[52:55]\n" \
"buffer_load_dwordx4 v25, s[76:79], s84 offen offset:3072 lds\n" \
"v_mfma_f32_16x16x32_bf16 a[52:55], a[228:231], a[204:207], a[52:55]\n" \
"v_writelane_b32 v11, s10, 2\n" \
"s_lshl_b32 s10, s8, 2\n" \
"v_mfma_f32_16x16x32_bf16 a[48:51], a[160:163], a[140:143], a[48:51]\n" \
"s_or_b32 s11, s10, s7\n" \
"s_or_b32 s11, s11, 0x5c00000\n" \
"v_mfma_f32_16x16x32_bf16 a[48:51], a[224:227], a[204:207], a[48:51]\n" \
"v_writelane_b32 v11, s11, 3\n" \
"s_or_b32 s7, s7, 0x5c00080\n" \
"s_waitcnt lgkmcnt(15)\n" \
"v_mfma_f32_16x16x32_bf16 a[64:67], a[164:167], v[248:251], a[64:67]\n" \
"v_writelane_b32 v11, s7, 4\n" \
"s_or_b32 s7, s7, s10\n" \
"v_mfma_f32_16x16x32_bf16 a[64:67], a[228:231], v[48:51], a[64:67]\n" \
"s_add_i32 s10, s3, s2\n" \
"v_writelane_b32 v11, s7, 5\n" \
"v_mfma_f32_16x16x32_bf16 a[80:83], a[172:175], v[248:251], a[80:83]\n" \
"s_mov_b32 s7, 2.0\n" \
"s_mov_b32 s11, 0x5bc0000\n" \
"v_mfma_f32_16x16x32_bf16 a[80:83], a[236:239], v[48:51], a[80:83]\n" \
"s_cmp_lg_u32 s10, 0\n" \
"s_cselect_b32 s10, s7, s11\n" \
"v_mfma_f32_16x16x32_bf16 a[68:71], v[248:251], a[168:171], a[68:71]\n" \
"s_lshr_b32 s15, s2, 3\n" \
"v_writelane_b32 v11, s10, 54\n" \
"v_mfma_f32_16x16x32_bf16 a[68:71], v[48:51], a[232:235], a[68:71]\n" \
"s_waitcnt vmcnt(10) lgkmcnt(0)\n" \
"s_barrier\n" \
"v_xor_b32_e32 v28, 0x20000, v28\n" \
"v_xor_b32_e32 v29, 0x20000, v29\n" \
"v_mfma_f32_16x16x32_bf16 v[52:55], a[176:179], a[128:131], v[52:55]\n" \
"s_add_u32 m0, s40, 0x8000\n" \
"s_add_u32 s85, s39, 0x6000\n" \
"v_mfma_f32_16x16x32_bf16 v[52:55], a[240:243], a[192:195], v[52:55]\n" \
"buffer_load_dwordx4 v20, s[80:83], s85 offen sc1 lds\n" \
"v_mfma_f32_16x16x32_bf16 v[56:59], a[180:183], a[128:131], v[56:59]\n" \
"ds_read_b64_tr_b16 a[160:161], v32 offset:0\n" \
"s_lshl_b32 s10, s6, 5\n" \
"v_mfma_f32_16x16x32_bf16 v[56:59], a[244:247], a[192:195], v[56:59]\n" \
"ds_read_b64_tr_b16 a[162:163], v32 offset:512\n" \
"s_min_u32 s16, s3, 2\n" \
"s_nop 0\n" \
"v_mfma_f32_16x16x32_bf16 v[60:63], a[184:187], a[128:131], v[60:63]\n" \
"s_add_u32 m0, s40, 0x9000\n" \
"s_add_u32 s85, s39, 0x7000\n" \
"v_mfma_f32_16x16x32_bf16 v[60:63], a[248:251], a[192:195], v[60:63]\n" \
"buffer_load_dwordx4 v20, s[80:83], s85 offen sc1 lds\n" \
"v_mfma_f32_16x16x32_bf16 v[64:67], a[188:191], a[128:131], v[64:67]\n" \
"ds_read_b64_tr_b16 a[224:225], v32 offset:4096\n" \
"s_mul_i32 s12, s8, 0x120\n" \
"v_mfma_f32_16x16x32_bf16 v[64:67], a[252:255], a[192:195], v[64:67]\n" \
"ds_read_b128 a[128:131], v28 offset:0\n" \
"v_mfma_f32_16x16x32_bf16 v[80:83], a[188:191], a[132:135], v[80:83]\n" \
"ds_read_b128 a[192:195], v29 offset:0\n" \
"v_mfma_f32_16x16x32_bf16 v[80:83], a[252:255], a[196:199], v[80:83]\n" \
"s_add_u32 s88, s41, 0x1000\n" \
"buffer_store_dwordx4 v[236:239], v20, s[80:83], s88 offen offset:2048 sc1\n" \
"v_mfma_f32_16x16x32_bf16 v[76:79], a[184:187], a[132:135], v[76:79]\n" \
"ds_read_b64_tr_b16 a[226:227], v32 offset:4608\n" \
"v_mfma_f32_16x16x32_bf16 v[76:79], a[248:251], a[196:199], v[76:79]\n" \
"ds_read_b64_tr_b16 a[164:165], v33 offset:0\n" \
"v_mfma_f32_16x16x32_bf16 v[72:75], a[180:183], a[132:135], v[72:75]\n" \
"s_add_u32 m0, s40, 0xb000\n" \
"s_movk_i32 s84, 0x180\n" \
"s_nop 0\n" \
"v_mfma_f32_16x16x32_bf16 v[72:75], a[244:247], a[196:199], v[72:75]\n" \
"buffer_load_dwordx4 v21, s[76:79], s84 offen offset:0 lds\n" \
"v_mfma_f32_16x16x32_bf16 v[68:71], a[176:179], a[132:135], v[68:71]\n" \
"ds_read_b64_tr_b16 a[166:167], v33 offset:512\n" \
"v_mfma_f32_16x16x32_bf16 v[68:71], a[240:243], a[196:199], v[68:71]\n" \
"ds_read_b128 a[132:135], v28 offset:2048\n" \
"v_mfma_f32_16x16x32_bf16 v[84:87], a[176:179], a[136:139], v[84:87]\n" \
"ds_read_b128 a[196:199], v29 offset:2048\n" \
"v_mfma_f32_16x16x32_bf16 v[84:87], a[240:243], a[200:203], v[84:87]\n" \
"s_add_u32 m0, s31, 0x8000\n" \
"s_mov_b32 s84, 0x180000\n" \
"v_mfma_f32_16x16x32_bf16 v[88:91], a[180:183], a[136:139], v[88:91]\n" \
"buffer_load_dwordx4 v26, s[68:71], s84 offen offset:0 lds\n" \
"v_mfma_f32_16x16x32_bf16 v[88:91], a[244:247], a[200:203], v[88:91]\n" \
"ds_read_b64_tr_b16 a[228:229], v33 offset:4096\n" \
"v_mfma_f32_16x16x32_bf16 v[92:95], a[184:187], a[136:139], v[92:95]\n" \
"ds_read_b64_tr_b16 a[230:231], v33 offset:4608\n" \
"v_mfma_f32_16x16x32_bf16 v[92:95], a[248:251], a[200:203], v[92:95]\n" \
"s_addk_i32 s84, 0x7c00\n" \
"v_mfma_f32_16x16x32_bf16 v[96:99], a[188:191], a[136:139], v[96:99]\n" \
"buffer_load_dwordx4 v26, s[68:71], s84 offen offset:1024 lds\n" \
"v_mfma_f32_16x16x32_bf16 v[96:99], a[252:255], a[200:203], v[96:99]\n" \
"ds_read_b128 a[136:139], v28 offset:4096\n" \
"v_mfma_f32_16x16x32_bf16 v[112:115], a[188:191], a[140:143], v[112:115]\n" \
"ds_read_b128 a[200:203], v29 offset:4096\n" \
"v_mfma_f32_16x16x32_bf16 v[112:115], a[252:255], a[204:207], v[112:115]\n" \
"s_addk_i32 s84, 0x7c00\n" \
"v_mfma_f32_16x16x32_bf16 v[108:111], a[184:187], a[140:143], v[108:111]\n" \
"buffer_load_dwordx4 v27, s[68:71], s84 offen offset:2048 lds\n" \
"v_mfma_f32_16x16x32_bf16 v[108:111], a[248:251], a[204:207], v[108:111]\n" \
"ds_read_b64_tr_b16 a[172:173], v35 offset:0\n" \
"v_mfma_f32_16x16x32_bf16 v[104:107], a[180:183], a[140:143], v[104:107]\n" \
"ds_read_b64_tr_b16 a[174:175], v35 offset:512\n" \
"v_mfma_f32_16x16x32_bf16 v[104:107], a[244:247], a[204:207], v[104:107]\n" \
"ds_read_b64_tr_b16 a[236:237], v35 offset:4096\n" \
"v_mfma_f32_16x16x32_bf16 v[100:103], a[176:179], a[140:143], v[100:103]\n" \
"s_add_u32 s11, s22, s12\n" \
"s_addc_u32 s17, s23, 0\n" \
"v_mfma_f32_16x16x32_bf16 v[100:103], a[240:243], a[204:207], v[100:103]\n" \
"ds_read_b128 a[140:143], v28 offset:6144\n" \
"v_mfma_f32_16x16x32_bf16 v[116:119], a[176:179], a[144:147], v[116:119]\n" \
"ds_read_b128 a[204:207], v29 offset:6144\n" \
"v_mfma_f32_16x16x32_bf16 v[116:119], a[240:243], a[208:211], v[116:119]\n" \
"s_addk_i32 s84, 0x7c00\n" \
"v_mfma_f32_16x16x32_bf16 v[120:123], a[180:183], a[144:147], v[120:123]\n" \
"buffer_load_dwordx4 v27, s[68:71], s84 offen offset:3072 lds\n" \
"v_mfma_f32_16x16x32_bf16 v[120:123], a[244:247], a[208:211], v[120:123]\n" \
"s_add_u32 m0, s30, 0x8000\n" \
"s_movk_i32 s84, 0x200\n" \
"v_mfma_f32_16x16x32_bf16 v[124:127], a[184:187], a[144:147], v[124:127]\n" \
"buffer_load_dwordx4 v24, s[76:79], s84 offen offset:0 lds\n" \
"v_mfma_f32_16x16x32_bf16 v[124:127], a[248:251], a[208:211], v[124:127]\n" \
"ds_read_b64_tr_b16 a[238:239], v35 offset:4608\n" \
"v_mfma_f32_16x16x32_bf16 v[128:131], a[188:191], a[144:147], v[128:131]\n" \
"s_add_u32 s89, s41, 0x2000\n" \
"buffer_store_dwordx4 v[240:243], v20, s[80:83], s89 offen offset:2048 sc1\n" \
"v_mfma_f32_16x16x32_bf16 v[128:131], a[252:255], a[208:211], v[128:131]\n" \
"ds_read_b128 a[144:147], v28 offset:8192\n" \
"v_mfma_f32_16x16x32_bf16 v[144:147], a[188:191], a[148:151], v[144:147]\n" \
"ds_read_b128 a[208:211], v29 offset:8192\n" \
"v_mfma_f32_16x16x32_bf16 v[144:147], a[252:255], a[212:215], v[144:147]\n" \
"ds_read_b64_tr_b16 a[168:169], v34 offset:0\n" \
"v_mfma_f32_16x16x32_bf16 v[140:143], a[184:187], a[148:151], v[140:143]\n" \
"ds_read_b64_tr_b16 a[170:171], v34 offset:512\n" \
"v_mfma_f32_16x16x32_bf16 v[140:143], a[248:251], a[212:215], v[140:143]\n" \
"s_movk_i32 s84, 0x2200\n" \
"s_nop 0\n" \
"v_mfma_f32_16x16x32_bf16 v[136:139], a[180:183], a[148:151], v[136:139]\n" \
"buffer_load_dwordx4 v25, s[76:79], s84 offen offset:1024 lds\n" \
"v_mfma_f32_16x16x32_bf16 v[136:139], a[244:247], a[212:215], v[136:139]\n" \
"ds_read_b64_tr_b16 a[232:233], v34 offset:4096\n" \
"v_mfma_f32_16x16x32_bf16 v[132:135], a[176:179], a[148:151], v[132:135]\n" \
"ds_read_b64_tr_b16 a[234:235], v34 offset:4608\n" \
"v_mfma_f32_16x16x32_bf16 v[132:135], a[240:243], a[212:215], v[132:135]\n" \
"ds_read_b128 a[148:151], v28 offset:10240\n" \
"v_mfma_f32_16x16x32_bf16 v[148:151], a[176:179], a[152:155], v[148:151]\n" \
"ds_read_b128 a[212:215], v29 offset:10240\n" \
"v_mfma_f32_16x16x32_bf16 v[148:151], a[240:243], a[216:219], v[148:151]\n" \
"s_movk_i32 s84, 0x4200\n" \
"v_mfma_f32_16x16x32_bf16 v[152:155], a[180:183], a[152:155], v[152:155]\n" \
"buffer_load_dwordx4 v24, s[76:79], s84 offen offset:2048 lds\n" \
"v_mfma_f32_16x16x32_bf16 v[152:155], a[244:247], a[216:219], v[152:155]\n" \
"s_movk_i32 s84, 0x6200\n" \
"v_mfma_f32_16x16x32_bf16 v[156:159], a[184:187], a[152:155], v[156:159]\n" \
"buffer_load_dwordx4 v25, s[76:79], s84 offen offset:3072 lds\n" \
"v_mfma_f32_16x16x32_bf16 v[156:159], a[248:251], a[216:219], v[156:159]\n" \
"ds_read_b128 v[248:251], v22 offset:0\n" \
"v_mfma_f32_16x16x32_bf16 v[160:163], a[188:191], a[152:155], v[160:163]\n" \
"ds_read_b128 v[48:51], v23 offset:0\n" \
"v_mfma_f32_16x16x32_bf16 v[160:163], a[252:255], a[216:219], v[160:163]\n" \
"ds_read_b128 a[152:155], v28 offset:12288\n" \
"v_mfma_f32_16x16x32_bf16 v[176:179], a[188:191], a[156:159], v[176:179]\n" \
"ds_read_b128 a[216:219], v29 offset:12288\n" \
"v_mfma_f32_16x16x32_bf16 v[176:179], a[252:255], a[220:223], v[176:179]\n" \
"s_add_u32 s18, s24, s12\n" \
"v_writelane_b32 v11, s11, 9\n" \
"s_addc_u32 s19, s25, 0\n" \
"v_mfma_f32_16x16x32_bf16 v[172:175], a[184:187], a[156:159], v[172:175]\n" \
"s_and_b32 s20, s17, 0xffff\n" \
"v_writelane_b32 v11, s20, 10\n" \
"v_mfma_f32_16x16x32_bf16 v[172:175], a[248:251], a[220:223], v[172:175]\n" \
"s_and_b32 s20, s19, 0xffff\n" \
"v_writelane_b32 v11, s18, 11\n" \
"v_mfma_f32_16x16x32_bf16 v[168:171], a[180:183], a[156:159], v[168:171]\n" \
"s_cmpk_lt_u32 s3, 3\n" \
"s_cselect_b32 s11, s11, s18\n" \
"v_mfma_f32_16x16x32_bf16 v[168:171], a[244:247], a[220:223], v[168:171]\n" \
"s_movk_i32 s18, 0xfff\n" \
"v_writelane_b32 v11, s20, 12\n" \
"v_mfma_f32_16x16x32_bf16 v[164:167], a[176:179], a[156:159], v[164:167]\n" \
"s_and_b32 s20, s29, 0xffff\n" \
"v_writelane_b32 v11, s28, 13\n" \
"v_mfma_f32_16x16x32_bf16 v[164:167], a[240:243], a[220:223], v[164:167]\n" \
"ds_read_b128 a[156:159], v28 offset:14336\n" \
"ds_read_b128 a[220:223], v29 offset:14336\n" \
"s_waitcnt lgkmcnt(15)\n" \
"v_mfma_f32_16x16x32_bf16 a[0:3], a[160:163], a[128:131], a[0:3]\n" \
"s_add_u32 m0, s30, 0x9000\n" \
"s_addk_i32 s84, 0x3000\n" \
"v_mfma_f32_16x16x32_bf16 a[0:3], a[224:227], a[192:195], a[0:3]\n" \
"buffer_load_dwordx4 v24, s[76:79], s84 offen offset:0 lds\n" \
"v_mfma_f32_16x16x32_bf16 a[4:7], a[164:167], a[128:131], a[4:7]\n" \
"ds_read_b64_tr_b16 a[176:177], v36 offset:0\n" \
"v_writelane_b32 v11, s20, 14\n" \
"v_mfma_f32_16x16x32_bf16 a[4:7], a[228:231], a[192:195], a[4:7]\n" \
"ds_read_b64_tr_b16 a[178:179], v36 offset:1024\n" \
"v_writelane_b32 v11, s11, 15\n" \
"v_mfma_f32_16x16x32_bf16 a[12:15], a[172:175], a[128:131], a[12:15]\n" \
"ds_read_b64_tr_b16 a[240:241], v36 offset:8192\n" \
"s_waitcnt lgkmcnt(15)\n" \
"v_mfma_f32_16x16x32_bf16 a[12:15], a[236:239], a[192:195], a[12:15]\n" \
"s_add_u32 s88, s41, 0x2000\n" \
"buffer_store_dwordx4 v[244:247], v20, s[80:83], s88 offen offset:3072 sc1\n" \
"s_waitcnt lgkmcnt(13)\n" \
"v_mfma_f32_16x16x32_bf16 a[8:11], a[128:131], a[168:171], a[8:11]\n" \
"ds_read_b64_tr_b16 a[242:243], v36 offset:9216\n" \
"s_waitcnt lgkmcnt(12)\n" \
"v_mfma_f32_16x16x32_bf16 a[8:11], a[192:195], a[232:235], a[8:11]\n" \
"ds_read_b64_tr_b16 a[180:181], v37 offset:0\n" \
"v_mfma_f32_16x16x32_bf16 a[24:27], a[132:135], a[168:171], a[24:27]\n" \
"ds_read_b64_tr_b16 a[182:183], v37 offset:1024\n" \
"v_mfma_f32_16x16x32_bf16 a[24:27], a[196:199], a[232:235], a[24:27]\n" \
"ds_read_b64_tr_b16 a[244:245], v37 offset:8192\n" \
"v_mfma_f32_16x16x32_bf16 a[28:31], a[172:175], a[132:135], a[28:31]\n" \
"s_cmpk_lt_u32 s3, 3\n" \
"s_cselect_b32 s11, s17, s19\n" \
"v_mfma_f32_16x16x32_bf16 a[28:31], a[236:239], a[196:199], a[28:31]\n" \
"s_addk_i32 s84, 0x2000\n" \
"s_nop 0\n" \
"v_mfma_f32_16x16x32_bf16 a[20:23], a[164:167], a[132:135], a[20:23]\n" \
"buffer_load_dwordx4 v25, s[76:79], s84 offen offset:1024 lds\n" \
"v_mfma_f32_16x16x32_bf16 a[20:23], a[228:231], a[196:199], a[20:23]\n" \
"ds_read_b64_tr_b16 a[246:247], v37 offset:9216\n" \
"s_and_b32 s11, s11, 0xffff\n" \
"v_mfma_f32_16x16x32_bf16 a[16:19], a[160:163], a[132:135], a[16:19]\n" \
"ds_read_b64_tr_b16 a[184:185], v38 offset:0\n" \
"v_writelane_b32 v11, s11, 16\n" \
"v_mfma_f32_16x16x32_bf16 a[16:19], a[224:227], a[196:199], a[16:19]\n" \
"ds_read_b64_tr_b16 a[186:187], v38 offset:1024\n" \
"s_mul_i32 s11, s6, 0x48\n" \
"v_mfma_f32_16x16x32_bf16 a[32:35], a[160:163], a[136:139], a[32:35]\n" \
"s_addk_i32 s84, 0x2000\n" \
"s_nop 0\n" \
"v_mfma_f32_16x16x32_bf16 a[32:35], a[224:227], a[200:203], a[32:35]\n" \
"buffer_load_dwordx4 v24, s[76:79], s84 offen offset:2048 lds\n" \
"v_mfma_f32_16x16x32_bf16 a[36:39], a[164:167], a[136:139], a[36:39]\n" \
"ds_read_b64_tr_b16 a[248:249], v38 offset:8192\n" \
"s_add_i32 s17, s11, 0x47\n" \
"v_mfma_f32_16x16x32_bf16 a[36:39], a[228:231], a[200:203], a[36:39]\n" \
"ds_read_b64_tr_b16 a[250:251], v38 offset:9216\n" \
".fill (L_pb + 24288 - .) / 4, 4, 0xBF800000\n" \
"L_pp6:\n" \
"s_cbranch_execz L_pret\n" \
"s_nop 0\n" \
"v_mfma_f32_16x16x32_bf16 a[44:47], a[172:175], a[136:139], a[44:47]\n" \
"ds_read_b64_tr_b16 a[188:189], v39 offset:0\n" \
"v_mfma_f32_16x16x32_bf16 a[44:47], a[236:239], a[200:203], a[44:47]\n" \
"ds_read_b64_tr_b16 a[190:191], v39 offset:1024\n" \
"v_mfma_f32_16x16x32_bf16 a[40:43], a[136:139], a[168:171], a[40:43]\n" \
"s_add_u32 s89, s41, 0x3000\n" \
"buffer_store_dwordx4 v[184:187], v20, s[80:83], s89 offen offset:3072 sc1\n" \
"v_mfma_f32_16x16x32_bf16 a[40:43], a[200:203], a[232:235], a[40:43]\n" \
"ds_read_b64_tr_b16 a[252:253], v39 offset:8192\n" \
"v_mfma_f32_16x16x32_bf16 a[56:59], a[140:143], a[168:171], a[56:59]\n" \
"ds_read_b64_tr_b16 a[254:255], v39 offset:9216\n" \
"v_mfma_f32_16x16x32_bf16 a[56:59], a[204:207], a[232:235], a[56:59]\n" \
"s_cmpk_lt_u32 s3, 3\n" \
"s_cselect_b32 s17, s17, s18\n" \
"v_mfma_f32_16x16x32_bf16 a[60:63], a[172:175], a[140:143], a[60:63]\n" \
"s_mulk_i32 s17, 0x480\n" \
"s_addk_i32 s17, 0x120\n" \
"v_mfma_f32_16x16x32_bf16 a[60:63], a[236:239], a[204:207], a[60:63]\n" \
"s_addk_i32 s84, 0x2000\n" \
"v_mfma_f32_16x16x32_bf16 a[52:55], a[164:167], a[140:143], a[52:55]\n" \
"buffer_load_dwordx4 v25, s[76:79], s84 offen offset:3072 lds\n" \
"v_mfma_f32_16x16x32_bf16 a[52:55], a[228:231], a[204:207], a[52:55]\n" \
"v_writelane_b32 v11, s17, 17\n" \
"s_lshl_b32 s17, s3, 5\n" \
"v_mfma_f32_16x16x32_bf16 a[48:51], a[160:163], a[140:143], a[48:51]\n" \
"s_add_i32 s11, s11, s17\n" \
"s_mul_i32 s17, s11, 0x480\n" \
"v_mfma_f32_16x16x32_bf16 a[48:51], a[224:227], a[204:207], a[48:51]\n" \
"s_mul_i32 s11, s13, 0x24000\n" \
"s_add_i32 s18, s11, 0x1b000\n" \
"s_waitcnt lgkmcnt(15)\n" \
"v_mfma_f32_16x16x32_bf16 a[64:67], a[164:167], v[248:251], a[64:67]\n" \
"s_cmpk_lt_u32 s3, 3\n" \
"s_cselect_b32 s17, s17, s18\n" \
"v_mfma_f32_16x16x32_bf16 a[64:67], a[228:231], v[48:51], a[64:67]\n" \
"s_mov_b32 s18, 0x8000\n" \
"v_writelane_b32 v11, s17, 18\n" \
"v_mfma_f32_16x16x32_bf16 a[80:83], a[172:175], v[248:251], a[80:83]\n" \
"s_mul_i32 s17, s16, 0x2400\n" \
"s_mul_i32 s16, s16, 0x9000\n" \
"v_mfma_f32_16x16x32_bf16 a[80:83], a[236:239], v[48:51], a[80:83]\n" \
"s_bitset1_b32 s17, 17\n" \
"v_writelane_b32 v11, s17, 19\n" \
"v_mfma_f32_16x16x32_bf16 a[68:71], v[248:251], a[168:171], a[68:71]\n" \
"s_add_i32 s16, s11, s16\n" \
"v_writelane_b32 v11, s16, 26\n" \
"v_mfma_f32_16x16x32_bf16 a[68:71], v[48:51], a[232:235], a[68:71]\n" \
"s_waitcnt vmcnt(11) lgkmcnt(0)\n" \
"s_barrier\n" \
"v_xor_b32_e32 v28, 0x20000, v28\n" \
"v_xor_b32_e32 v29, 0x20000, v29\n" \
"v_mfma_f32_16x16x32_bf16 v[52:55], a[176:179], a[128:131], v[52:55]\n" \
"s_add_u32 m0, s40, 0x0\n" \
"s_nop 0\n" \
"s_add_u32 s85, s39, 0x8000\n" \
"v_mfma_f32_16x16x32_bf16 v[52:55], a[240:243], a[192:195], v[52:55]\n" \
"buffer_load_dwordx4 v20, s[80:83], s85 offen sc1 lds\n" \
"v_mfma_f32_16x16x32_bf16 v[56:59], a[180:183], a[128:131], v[56:59]\n" \
"ds_read_b64_tr_b16 a[160:161], v32 offset:32768\n" \
"s_mul_i32 s16, s3, 0x2400\n" \
"v_mfma_f32_16x16x32_bf16 v[56:59], a[244:247], a[192:195], v[56:59]\n" \
"ds_read_b64_tr_b16 a[162:163], v32 offset:33280\n" \
"s_add_i32 s17, s16, 0xa400\n" \
"v_mfma_f32_16x16x32_bf16 v[60:63], a[184:187], a[128:131], v[60:63]\n" \
"s_add_u32 m0, s40, 0x1000\n" \
"s_add_u32 s85, s39, 0x9000\n" \
"v_mfma_f32_16x16x32_bf16 v[60:63], a[248:251], a[192:195], v[60:63]\n" \
"buffer_load_dwordx4 v20, s[80:83], s85 offen sc1 lds\n" \
"v_mfma_f32_16x16x32_bf16 v[64:67], a[188:191], a[128:131], v[64:67]\n" \
"ds_read_b64_tr_b16 a[224:225], v32 offset:36864\n" \
"v_mfma_f32_16x16x32_bf16 v[64:67], a[252:255], a[192:195], v[64:67]\n" \
"ds_read_b128 a[128:131], v28 offset:0\n" \
"v_mfma_f32_16x16x32_bf16 v[80:83], a[188:191], a[132:135], v[80:83]\n" \
"ds_read_b128 a[192:195], v29 offset:0\n" \
"v_mfma_f32_16x16x32_bf16 v[80:83], a[252:255], a[196:199], v[80:83]\n" \
"s_add_u32 s88, s41, 0x3000\n" \
"buffer_store_dwordx4 v[188:191], v20, s[80:83], s88 offen offset:2048 sc1\n" \
"v_mfma_f32_16x16x32_bf16 v[76:79], a[184:187], a[132:135], v[76:79]\n" \
"ds_read_b64_tr_b16 a[226:227], v32 offset:37376\n" \
"v_mfma_f32_16x16x32_bf16 v[76:79], a[248:251], a[196:199], v[76:79]\n" \
"ds_read_b64_tr_b16 a[164:165], v33 offset:32768\n" \
"v_mfma_f32_16x16x32_bf16 v[72:75], a[180:183], a[132:135], v[72:75]\n" \
"s_add_u32 m0, s40, 0x3000\n" \
"s_movk_i32 s84, 0x200\n" \
"v_mfma_f32_16x16x32_bf16 v[72:75], a[244:247], a[196:199], v[72:75]\n" \
"buffer_load_dwordx4 v21, s[76:79], s84 offen offset:0 lds\n" \
"v_mfma_f32_16x16x32_bf16 v[68:71], a[176:179], a[132:135], v[68:71]\n" \
"ds_read_b64_tr_b16 a[166:167], v33 offset:33280\n" \
"v_mfma_f32_16x16x32_bf16 v[68:71], a[240:243], a[196:199], v[68:71]\n" \
"ds_read_b128 a[132:135], v28 offset:2048\n" \
"v_mfma_f32_16x16x32_bf16 v[84:87], a[176:179], a[136:139], v[84:87]\n" \
"ds_read_b128 a[196:199], v29 offset:2048\n" \
"v_mfma_f32_16x16x32_bf16 v[84:87], a[240:243], a[200:203], v[84:87]\n" \
"s_add_u32 m0, s31, 0x0\n" \
"s_mov_b32 s84, 0x200000\n" \
"v_mfma_f32_16x16x32_bf16 v[88:91], a[180:183], a[136:139], v[88:91]\n" \
"buffer_load_dwordx4 v26, s[68:71], s84 offen offset:0 lds\n" \
"v_mfma_f32_16x16x32_bf16 v[88:91], a[244:247], a[200:203], v[88:91]\n" \
"ds_read_b64_tr_b16 a[228:229], v33 offset:36864\n" \
"v_mfma_f32_16x16x32_bf16 v[92:95], a[184:187], a[136:139], v[92:95]\n" \
"ds_read_b64_tr_b16 a[230:231], v33 offset:37376\n" \
"v_mfma_f32_16x16x32_bf16 v[92:95], a[248:251], a[200:203], v[92:95]\n" \
"s_addk_i32 s84, 0x7c00\n" \
"v_mfma_f32_16x16x32_bf16 v[96:99], a[188:191], a[136:139], v[96:99]\n" \
"buffer_load_dwordx4 v26, s[68:71], s84 offen offset:1024 lds\n" \
"v_mfma_f32_16x16x32_bf16 v[96:99], a[252:255], a[200:203], v[96:99]\n" \
"ds_read_b128 a[136:139], v28 offset:4096\n" \
"v_mfma_f32_16x16x32_bf16 v[112:115], a[188:191], a[140:143], v[112:115]\n" \
"ds_read_b128 a[200:203], v29 offset:4096\n" \
"v_mfma_f32_16x16x32_bf16 v[112:115], a[252:255], a[204:207], v[112:115]\n" \
"s_addk_i32 s84, 0x7c00\n" \
"v_mfma_f32_16x16x32_bf16 v[108:111], a[184:187], a[140:143], v[108:111]\n" \
"buffer_load_dwordx4 v27, s[68:71], s84 offen offset:2048 lds\n" \
"v_mfma_f32_16x16x32_bf16 v[108:111], a[248:251], a[204:207], v[108:111]\n" \
"ds_read_b64_tr_b16 a[172:173], v35 offset:32768\n" \
"v_mfma_f32_16x16x32_bf16 v[104:107], a[180:183], a[140:143], v[104:107]\n" \
"ds_read_b64_tr_b16 a[174:175], v35 offset:33280\n" \
"v_mfma_f32_16x16x32_bf16 v[104:107], a[244:247], a[204:207], v[104:107]\n" \
"ds_read_b64_tr_b16 a[236:237], v35 offset:36864\n" \
"v_mfma_f32_16x16x32_bf16 v[100:103], a[176:179], a[140:143], v[100:103]\n" \
"s_cmpk_lt_u32 s3, 3\n" \
"s_cselect_b32 s17, s17, s18\n" \
"v_mfma_f32_16x16x32_bf16 v[100:103], a[240:243], a[204:207], v[100:103]\n" \
"ds_read_b128 a[140:143], v28 offset:6144\n" \
"s_mov_b32 s18, 0x1b000\n" \
"v_mfma_f32_16x16x32_bf16 v[116:119], a[176:179], a[144:147], v[116:119]\n" \
"ds_read_b128 a[204:207], v29 offset:6144\n" \
"s_xor_b32 s19, s3, 2\n" \
"v_mfma_f32_16x16x32_bf16 v[116:119], a[240:243], a[208:211], v[116:119]\n" \
"s_addk_i32 s84, 0x7c00\n" \
"v_mfma_f32_16x16x32_bf16 v[120:123], a[180:183], a[144:147], v[120:123]\n" \
"buffer_load_dwordx4 v27, s[68:71], s84 offen offset:3072 lds\n" \
"v_mfma_f32_16x16x32_bf16 v[120:123], a[244:247], a[208:211], v[120:123]\n" \
"s_add_u32 m0, s30, 0x20000\n" \
"s_movk_i32 s84, 0x280\n" \
"v_mfma_f32_16x16x32_bf16 v[124:127], a[184:187], a[144:147], v[124:127]\n" \
"buffer_load_dwordx4 v24, s[76:79], s84 offen offset:0 lds\n" \
"v_mfma_f32_16x16x32_bf16 v[124:127], a[248:251], a[208:211], v[124:127]\n" \
"ds_read_b64_tr_b16 a[238:239], v35 offset:37376\n" \
"v_mfma_f32_16x16x32_bf16 v[128:131], a[188:191], a[144:147], v[128:131]\n" \
"s_add_u32 s89, s42, 0x0\n" \
"buffer_store_dwordx4 v[216:219], v20, s[80:83], s89 offen offset:0 sc1\n" \
"v_mfma_f32_16x16x32_bf16 v[128:131], a[252:255], a[208:211], v[128:131]\n" \
"ds_read_b128 a[144:147], v28 offset:8192\n" \
"v_mfma_f32_16x16x32_bf16 v[144:147], a[188:191], a[148:151], v[144:147]\n" \
"ds_read_b128 a[208:211], v29 offset:8192\n" \
"v_mfma_f32_16x16x32_bf16 v[144:147], a[252:255], a[212:215], v[144:147]\n" \
"ds_read_b64_tr_b16 a[168:169], v34 offset:32768\n" \
"v_mfma_f32_16x16x32_bf16 v[140:143], a[184:187], a[148:151], v[140:143]\n" \
"ds_read_b64_tr_b16 a[170:171], v34 offset:33280\n" \
"v_mfma_f32_16x16x32_bf16 v[140:143], a[248:251], a[212:215], v[140:143]\n" \
"s_movk_i32 s84, 0x2280\n" \
"s_nop 0\n" \
"v_mfma_f32_16x16x32_bf16 v[136:139], a[180:183], a[148:151], v[136:139]\n" \
"buffer_load_dwordx4 v25, s[76:79], s84 offen offset:1024 lds\n" \
"v_mfma_f32_16x16x32_bf16 v[136:139], a[244:247], a[212:215], v[136:139]\n" \
"ds_read_b64_tr_b16 a[232:233], v34 offset:36864\n" \
"v_mfma_f32_16x16x32_bf16 v[132:135], a[176:179], a[148:151], v[132:135]\n" \
"ds_read_b64_tr_b16 a[234:235], v34 offset:37376\n" \
"v_mfma_f32_16x16x32_bf16 v[132:135], a[240:243], a[212:215], v[132:135]\n" \
"ds_read_b128 a[148:151], v28 offset:10240\n" \
"v_mfma_f32_16x16x32_bf16 v[148:151], a[176:179], a[152:155], v[148:151]\n" \
"ds_read_b128 a[212:215], v29 offset:10240\n" \
"v_mfma_f32_16x16x32_bf16 v[148:151], a[240:243], a[216:219], v[148:151]\n" \
"s_movk_i32 s84, 0x4280\n" \
"v_mfma_f32_16x16x32_bf16 v[152:155], a[180:183], a[152:155], v[152:155]\n" \
"buffer_load_dwordx4 v24, s[76:79], s84 offen offset:2048 lds\n" \
"v_mfma_f32_16x16x32_bf16 v[152:155], a[244:247], a[216:219], v[152:155]\n" \
"s_movk_i32 s84, 0x6280\n" \
"v_mfma_f32_16x16x32_bf16 v[156:159], a[184:187], a[152:155], v[156:159]\n" \
"buffer_load_dwordx4 v25, s[76:79], s84 offen offset:3072 lds\n" \
"v_mfma_f32_16x16x32_bf16 v[156:159], a[248:251], a[216:219], v[156:159]\n" \
"ds_read_b128 v[248:251], v22 offset:32768\n" \
"v_mfma_f32_16x16x32_bf16 v[160:163], a[188:191], a[152:155], v[160:163]\n" \
"ds_read_b128 v[48:51], v23 offset:32768\n" \
"v_mfma_f32_16x16x32_bf16 v[160:163], a[252:255], a[216:219], v[160:163]\n" \
"ds_read_b128 a[152:155], v28 offset:12288\n" \
"v_mfma_f32_16x16x32_bf16 v[176:179], a[188:191], a[156:159], v[176:179]\n" \
"ds_read_b128 a[216:219], v29 offset:12288\n" \
"v_mfma_f32_16x16x32_bf16 v[176:179], a[252:255], a[220:223], v[176:179]\n" \
"s_cmp_lg_u32 s19, 0\n" \
"s_cselect_b32 s18, s17, s18\n" \
"v_mfma_f32_16x16x32_bf16 v[172:175], a[184:187], a[156:159], v[172:175]\n" \
"v_writelane_b32 v11, s17, 20\n" \
"s_or_b32 s17, s16, 0x10000\n" \
"v_mfma_f32_16x16x32_bf16 v[172:175], a[248:251], a[220:223], v[172:175]\n" \
"v_writelane_b32 v11, s18, 21\n" \
"s_add_i32 s18, s16, 0x19000\n" \
"v_mfma_f32_16x16x32_bf16 v[168:171], a[180:183], a[156:159], v[168:171]\n" \
"v_writelane_b32 v11, s17, 22\n" \
"s_mov_b32 s17, 0\n" \
"v_mfma_f32_16x16x32_bf16 v[168:171], a[244:247], a[220:223], v[168:171]\n" \
"s_cmpk_lt_u32 s3, 3\n" \
"s_cselect_b32 s18, s18, s17\n" \
"v_mfma_f32_16x16x32_bf16 v[164:167], a[176:179], a[156:159], v[164:167]\n" \
"s_bitset1_b32 s16, 17\n" \
"v_writelane_b32 v11, s18, 23\n" \
"v_mfma_f32_16x16x32_bf16 v[164:167], a[240:243], a[220:223], v[164:167]\n" \
"ds_read_b128 a[156:159], v28 offset:14336\n" \
"ds_read_b128 a[220:223], v29 offset:14336\n" \
"s_waitcnt lgkmcnt(15)\n" \
"v_mfma_f32_16x16x32_bf16 a[0:3], a[160:163], a[128:131], a[0:3]\n" \
"s_add_u32 m0, s30, 0x21000\n" \
"s_addk_i32 s84, 0x3000\n" \
"v_mfma_f32_16x16x32_bf16 a[0:3], a[224:227], a[192:195], a[0:3]\n" \
"buffer_load_dwordx4 v24, s[76:79], s84 offen offset:0 lds\n" \
"v_mfma_f32_16x16x32_bf16 a[4:7], a[164:167], a[128:131], a[4:7]\n" \
"ds_read_b64_tr_b16 a[176:177], v36 offset:32768\n" \
"s_movk_i32 s18, 0x2400\n" \
"v_mfma_f32_16x16x32_bf16 a[4:7], a[228:231], a[192:195], a[4:7]\n" \
"ds_read_b64_tr_b16 a[178:179], v36 offset:33792\n" \
"v_mfma_f32_16x16x32_bf16 a[12:15], a[172:175], a[128:131], a[12:15]\n" \
"ds_read_b64_tr_b16 a[240:241], v36 offset:40960\n" \
"s_waitcnt lgkmcnt(15)\n" \
"v_mfma_f32_16x16x32_bf16 a[12:15], a[236:239], a[192:195], a[12:15]\n" \
"s_add_u32 s88, s42, 0x1000\n" \
"buffer_store_dwordx4 v[220:223], v20, s[80:83], s88 offen offset:0 sc1\n" \
"s_waitcnt lgkmcnt(13)\n" \
"v_mfma_f32_16x16x32_bf16 a[8:11], a[128:131], a[168:171], a[8:11]\n" \
"ds_read_b64_tr_b16 a[242:243], v36 offset:41984\n" \
"s_waitcnt lgkmcnt(12)\n" \
"v_mfma_f32_16x16x32_bf16 a[8:11], a[192:195], a[232:235], a[8:11]\n" \
"ds_read_b64_tr_b16 a[180:181], v37 offset:32768\n" \
"v_mfma_f32_16x16x32_bf16 a[24:27], a[132:135], a[168:171], a[24:27]\n" \
"ds_read_b64_tr_b16 a[182:183], v37 offset:33792\n" \
"v_mfma_f32_16x16x32_bf16 a[24:27], a[196:199], a[232:235], a[24:27]\n" \
"ds_read_b64_tr_b16 a[244:245], v37 offset:40960\n" \
"v_mfma_f32_16x16x32_bf16 a[28:31], a[172:175], a[132:135], a[28:31]\n" \
"s_cmpk_lt_u32 s3, 3\n" \
"s_cselect_b32 s16, s16, s18\n" \
"v_mfma_f32_16x16x32_bf16 a[28:31], a[236:239], a[196:199], a[28:31]\n" \
"s_addk_i32 s84, 0x2000\n" \
"v_mfma_f32_16x16x32_bf16 a[20:23], a[164:167], a[132:135], a[20:23]\n" \
"buffer_load_dwordx4 v25, s[76:79], s84 offen offset:1024 lds\n" \
"v_mfma_f32_16x16x32_bf16 a[20:23], a[228:231], a[196:199], a[20:23]\n" \
"ds_read_b64_tr_b16 a[246:247], v37 offset:41984\n" \
"s_mul_i32 s19, s3, 0x9000\n" \
"v_mfma_f32_16x16x32_bf16 a[16:19], a[160:163], a[132:135], a[16:19]\n" \
"ds_read_b64_tr_b16 a[184:185], v38 offset:32768\n" \
"v_writelane_b32 v11, s16, 24\n" \
"v_mfma_f32_16x16x32_bf16 a[16:19], a[224:227], a[196:199], a[16:19]\n" \
"ds_read_b64_tr_b16 a[186:187], v38 offset:33792\n" \
"s_lshl_b32 s16, s3, 10\n" \
"v_mfma_f32_16x16x32_bf16 a[32:35], a[160:163], a[136:139], a[32:35]\n" \
"s_addk_i32 s84, 0x2000\n" \
"v_mfma_f32_16x16x32_bf16 a[32:35], a[224:227], a[200:203], a[32:35]\n" \
"buffer_load_dwordx4 v24, s[76:79], s84 offen offset:2048 lds\n" \
"v_mfma_f32_16x16x32_bf16 a[36:39], a[164:167], a[136:139], a[36:39]\n" \
"ds_read_b64_tr_b16 a[248:249], v38 offset:40960\n" \
"s_addk_i32 s16, 0x4800\n" \
"v_mfma_f32_16x16x32_bf16 a[36:39], a[228:231], a[200:203], a[36:39]\n" \
"ds_read_b64_tr_b16 a[250:251], v38 offset:41984\n" \
"v_writelane_b32 v11, s16, 25\n" \
"v_mfma_f32_16x16x32_bf16 a[44:47], a[172:175], a[136:139], a[44:47]\n" \
"ds_read_b64_tr_b16 a[188:189], v39 offset:32768\n" \
"s_add_i32 s16, s15, 1\n" \
"v_mfma_f32_16x16x32_bf16 a[44:47], a[236:239], a[200:203], a[44:47]\n" \
"ds_read_b64_tr_b16 a[190:191], v39 offset:33792\n" \
"s_and_b32 s16, s16, 3\n" \
"v_mfma_f32_16x16x32_bf16 a[40:43], a[136:139], a[168:171], a[40:43]\n" \
"s_add_u32 s89, s42, 0x0\n" \
"buffer_store_dwordx4 v[192:195], v20, s[80:83], s89 offen offset:2048 sc1\n" \
"v_mfma_f32_16x16x32_bf16 a[40:43], a[200:203], a[232:235], a[40:43]\n" \
"ds_read_b64_tr_b16 a[252:253], v39 offset:40960\n" \
"s_or_b32 s16, s16, s5\n" \
"v_mfma_f32_16x16x32_bf16 a[56:59], a[140:143], a[168:171], a[56:59]\n" \
"ds_read_b64_tr_b16 a[254:255], v39 offset:41984\n" \
"s_mul_i32 s18, s16, 0x24000\n" \
"v_mfma_f32_16x16x32_bf16 a[56:59], a[204:207], a[232:235], a[56:59]\n" \
"s_add_i32 s18, s18, s19\n" \
"s_xor_b32 s13, s13, 2\n" \
"v_mfma_f32_16x16x32_bf16 a[60:63], a[172:175], a[140:143], a[60:63]\n" \
"s_add_i32 s15, s15, -1\n" \
"v_writelane_b32 v11, s18, 27\n" \
"v_mfma_f32_16x16x32_bf16 a[60:63], a[236:239], a[204:207], a[60:63]\n" \
"s_addk_i32 s84, 0x2000\n" \
"v_mfma_f32_16x16x32_bf16 a[52:55], a[164:167], a[140:143], a[52:55]\n" \
"buffer_load_dwordx4 v25, s[76:79], s84 offen offset:3072 lds\n" \
"v_mfma_f32_16x16x32_bf16 a[52:55], a[228:231], a[204:207], a[52:55]\n" \
"s_mul_i32 s18, s13, 0x24000\n" \
"s_and_b32 s15, s15, 3\n" \
"v_mfma_f32_16x16x32_bf16 a[48:51], a[160:163], a[140:143], a[48:51]\n" \
"s_add_i32 s18, s18, s19\n" \
"s_or_b32 s15, s15, s5\n" \
"v_mfma_f32_16x16x32_bf16 a[48:51], a[224:227], a[204:207], a[48:51]\n" \
"v_writelane_b32 v11, s18, 28\n" \
"s_mul_i32 s18, s15, 0x24000\n" \
"s_waitcnt lgkmcnt(15)\n" \
"v_mfma_f32_16x16x32_bf16 a[64:67], a[164:167], v[248:251], a[64:67]\n" \
"s_add_i32 s18, s18, s19\n" \
"v_writelane_b32 v11, s18, 29\n" \
"v_mfma_f32_16x16x32_bf16 a[64:67], a[228:231], v[48:51], a[64:67]\n" \
"s_or_b32 s18, s9, s14\n" \
"s_mul_i32 s18, s18, 0x24800\n" \
"v_mfma_f32_16x16x32_bf16 a[80:83], a[172:175], v[248:251], a[80:83]\n" \
"s_lshl_b32 s16, s16, 12\n" \
"s_add_i32 s19, s18, 0x3004800\n" \
"v_mfma_f32_16x16x32_bf16 a[80:83], a[236:239], v[48:51], a[80:83]\n" \
"s_add_i32 s20, s19, s16\n" \
"v_writelane_b32 v11, s20, 30\n" \
"v_mfma_f32_16x16x32_bf16 a[68:71], v[248:251], a[168:171], a[68:71]\n" \
"s_lshl_b32 s13, s13, 12\n" \
"s_lshl_b32 s15, s15, 12\n" \
"v_mfma_f32_16x16x32_bf16 a[68:71], v[48:51], a[232:235], a[68:71]\n" \
"s_waitcnt vmcnt(11) lgkmcnt(0)\n" \
"s_barrier\n" \
"v_mfma_f32_16x16x32_bf16 v[52:55], a[176:179], a[128:131], v[52:55]\n" \
"s_add_u32 m0, s40, 0x8000\n" \
"s_add_u32 s85, s39, 0xa000\n" \
"v_mfma_f32_16x16x32_bf16 v[52:55], a[240:243], a[192:195], v[52:55]\n" \
"buffer_load_dwordx4 v20, s[80:83], s85 offen sc1 lds\n" \
"v_mfma_f32_16x16x32_bf16 v[56:59], a[180:183], a[128:131], v[56:59]\n" \
"ds_read_b64_tr_b16 a[160:161], v32 offset:0\n" \
"v_accvgpr_read_b32 v224, a120\n" \
"v_mfma_f32_16x16x32_bf16 v[56:59], a[244:247], a[192:195], v[56:59]\n" \
"ds_read_b64_tr_b16 a[162:163], v32 offset:512\n" \
"v_accvgpr_read_b32 v225, a121\n" \
"v_mfma_f32_16x16x32_bf16 v[60:63], a[184:187], a[128:131], v[60:63]\n" \
"s_add_u32 m0, s40, 0x9000\n" \
"s_add_u32 s85, s39, 0xb000\n" \
"v_mfma_f32_16x16x32_bf16 v[60:63], a[248:251], a[192:195], v[60:63]\n" \
"buffer_load_dwordx4 v20, s[80:83], s85 offen sc1 lds\n" \
"v_mfma_f32_16x16x32_bf16 v[64:67], a[188:191], a[128:131], v[64:67]\n" \
"ds_read_b64_tr_b16 a[224:225], v32 offset:4096\n" \
"v_accvgpr_read_b32 v226, a122\n" \
"v_mfma_f32_16x16x32_bf16 v[64:67], a[252:255], a[192:195], v[64:67]\n" \
"ds_read_b128 a[128:131], v28 offset:32768\n" \
"v_accvgpr_read_b32 v227, a123\n" \
"v_mfma_f32_16x16x32_bf16 v[80:83], a[188:191], a[132:135], v[80:83]\n" \
"ds_read_b128 a[192:195], v29 offset:32768\n" \
"v_accvgpr_read_b32 v180, a124\n" \
"v_mfma_f32_16x16x32_bf16 v[80:83], a[252:255], a[196:199], v[80:83]\n" \
"s_add_u32 s88, s42, 0x0\n" \
"buffer_store_dwordx4 v[196:199], v20, s[80:83], s88 offen offset:3072 sc1\n" \
"v_mfma_f32_16x16x32_bf16 v[76:79], a[184:187], a[132:135], v[76:79]\n" \
"ds_read_b64_tr_b16 a[226:227], v32 offset:4608\n" \
"v_accvgpr_read_b32 v181, a125\n" \
"v_mfma_f32_16x16x32_bf16 v[76:79], a[248:251], a[196:199], v[76:79]\n" \
"ds_read_b64_tr_b16 a[164:165], v33 offset:0\n" \
"v_accvgpr_read_b32 v182, a126\n" \
"v_mfma_f32_16x16x32_bf16 v[72:75], a[180:183], a[132:135], v[72:75]\n" \
"s_add_u32 m0, s40, 0xb000\n" \
"s_movk_i32 s84, 0x280\n" \
"v_mfma_f32_16x16x32_bf16 v[72:75], a[244:247], a[196:199], v[72:75]\n" \
"buffer_load_dwordx4 v21, s[76:79], s84 offen offset:0 lds\n" \
"v_mfma_f32_16x16x32_bf16 v[68:71], a[176:179], a[132:135], v[68:71]\n" \
"ds_read_b64_tr_b16 a[166:167], v33 offset:512\n" \
"v_accvgpr_read_b32 v183, a127\n" \
"v_mfma_f32_16x16x32_bf16 v[68:71], a[240:243], a[196:199], v[68:71]\n" \
"ds_read_b128 a[132:135], v28 offset:34816\n" \
"v_cvt_pk_bf16_f32 v224, v224, v225\n" \
"v_mfma_f32_16x16x32_bf16 v[84:87], a[176:179], a[136:139], v[84:87]\n" \
"ds_read_b128 a[196:199], v29 offset:34816\n" \
"v_cvt_pk_bf16_f32 v225, v226, v227\n" \
"v_mfma_f32_16x16x32_bf16 v[84:87], a[240:243], a[200:203], v[84:87]\n" \
"s_add_u32 m0, s31, 0x8000\n" \
"s_mov_b32 s84, 0x280000\n" \
"v_mfma_f32_16x16x32_bf16 v[88:91], a[180:183], a[136:139], v[88:91]\n" \
"buffer_load_dwordx4 v26, s[68:71], s84 offen offset:0 lds\n" \
"v_mfma_f32_16x16x32_bf16 v[88:91], a[244:247], a[200:203], v[88:91]\n" \
"ds_read_b64_tr_b16 a[228:229], v33 offset:4096\n" \
"v_cvt_pk_bf16_f32 v226, v180, v181\n" \
"v_mfma_f32_16x16x32_bf16 v[92:95], a[184:187], a[136:139], v[92:95]\n" \
"ds_read_b64_tr_b16 a[230:231], v33 offset:4608\n" \
"v_cvt_pk_bf16_f32 v227, v182, v183\n" \
"v_mfma_f32_16x16x32_bf16 v[92:95], a[248:251], a[200:203], v[92:95]\n" \
"s_addk_i32 s84, 0x7c00\n" \
"v_mfma_f32_16x16x32_bf16 v[96:99], a[188:191], a[136:139], v[96:99]\n" \
"buffer_load_dwordx4 v26, s[68:71], s84 offen offset:1024 lds\n" \
"v_mfma_f32_16x16x32_bf16 v[96:99], a[252:255], a[200:203], v[96:99]\n" \
"ds_read_b128 a[136:139], v28 offset:36864\n" \
"v_accvgpr_read_b32 v220, a112\n" \
"v_mfma_f32_16x16x32_bf16 v[112:115], a[188:191], a[140:143], v[112:115]\n" \
"ds_read_b128 a[200:203], v29 offset:36864\n" \
"v_accvgpr_read_b32 v221, a113\n" \
"v_mfma_f32_16x16x32_bf16 v[112:115], a[252:255], a[204:207], v[112:115]\n" \
"s_addk_i32 s84, 0x7c00\n" \
"v_mfma_f32_16x16x32_bf16 v[108:111], a[184:187], a[140:143], v[108:111]\n" \
"buffer_load_dwordx4 v27, s[68:71], s84 offen offset:2048 lds\n" \
"v_mfma_f32_16x16x32_bf16 v[108:111], a[248:251], a[204:207], v[108:111]\n" \
"ds_read_b64_tr_b16 a[172:173], v35 offset:0\n" \
"v_accvgpr_read_b32 v222, a114\n" \
"v_mfma_f32_16x16x32_bf16 v[104:107], a[180:183], a[140:143], v[104:107]\n" \
"ds_read_b64_tr_b16 a[174:175], v35 offset:512\n" \
"v_accvgpr_read_b32 v223, a115\n" \
"v_mfma_f32_16x16x32_bf16 v[104:107], a[244:247], a[204:207], v[104:107]\n" \
"ds_read_b64_tr_b16 a[236:237], v35 offset:4096\n" \
"v_accvgpr_read_b32 v180, a116\n" \
"v_mfma_f32_16x16x32_bf16 v[100:103], a[176:179], a[140:143], v[100:103]\n" \
"v_accvgpr_read_b32 v181, a117\n" \
"v_accvgpr_read_b32 v182, a118\n" \
"v_mfma_f32_16x16x32_bf16 v[100:103], a[240:243], a[204:207], v[100:103]\n" \
"ds_read_b128 a[140:143], v28 offset:38912\n" \
"v_accvgpr_read_b32 v183, a119\n" \
"v_mfma_f32_16x16x32_bf16 v[116:119], a[176:179], a[144:147], v[116:119]\n" \
"ds_read_b128 a[204:207], v29 offset:38912\n" \
"v_cvt_pk_bf16_f32 v220, v220, v221\n" \
"v_mfma_f32_16x16x32_bf16 v[116:119], a[240:243], a[208:211], v[116:119]\n" \
"s_addk_i32 s84, 0x7c00\n" \
"v_mfma_f32_16x16x32_bf16 v[120:123], a[180:183], a[144:147], v[120:123]\n" \
"buffer_load_dwordx4 v27, s[68:71], s84 offen offset:3072 lds\n" \
"v_mfma_f32_16x16x32_bf16 v[120:123], a[244:247], a[208:211], v[120:123]\n" \
"s_add_u32 m0, s30, 0x0\n" \
"s_movk_i32 s84, 0x300\n" \
"s_nop 0\n" \
"v_mfma_f32_16x16x32_bf16 v[124:127], a[184:187], a[144:147], v[124:127]\n" \
"buffer_load_dwordx4 v24, s[76:79], s84 offen offset:0 lds\n" \
"v_mfma_f32_16x16x32_bf16 v[124:127], a[248:251], a[208:211], v[124:127]\n" \
"ds_read_b64_tr_b16 a[238:239], v35 offset:4608\n" \
"v_cvt_pk_bf16_f32 v221, v222, v223\n" \
"v_mfma_f32_16x16x32_bf16 v[128:131], a[188:191], a[144:147], v[128:131]\n" \
"s_add_u32 s89, s42, 0x1000\n" \
"buffer_store_dwordx4 a[84:87], v20, s[80:83], s89 offen offset:3072 sc1\n" \
"v_mfma_f32_16x16x32_bf16 v[128:131], a[252:255], a[208:211], v[128:131]\n" \
"ds_read_b128 a[144:147], v28 offset:40960\n" \
"v_cvt_pk_bf16_f32 v222, v180, v181\n" \
"v_mfma_f32_16x16x32_bf16 v[144:147], a[188:191], a[148:151], v[144:147]\n" \
"ds_read_b128 a[208:211], v29 offset:40960\n" \
"v_cvt_pk_bf16_f32 v223, v182, v183\n" \
"v_mfma_f32_16x16x32_bf16 v[144:147], a[252:255], a[212:215], v[144:147]\n" \
"ds_read_b64_tr_b16 a[168:169], v34 offset:0\n" \
"v_accvgpr_read_b32 v216, a104\n" \
"v_mfma_f32_16x16x32_bf16 v[140:143], a[184:187], a[148:151], v[140:143]\n" \
"ds_read_b64_tr_b16 a[170:171], v34 offset:512\n" \
"v_accvgpr_read_b32 v217, a105\n" \
"v_mfma_f32_16x16x32_bf16 v[140:143], a[248:251], a[212:215], v[140:143]\n" \
"s_movk_i32 s84, 0x2300\n" \
"s_nop 0\n" \
"v_mfma_f32_16x16x32_bf16 v[136:139], a[180:183], a[148:151], v[136:139]\n" \
"buffer_load_dwordx4 v25, s[76:79], s84 offen offset:1024 lds\n" \
"v_mfma_f32_16x16x32_bf16 v[136:139], a[244:247], a[212:215], v[136:139]\n" \
"ds_read_b64_tr_b16 a[232:233], v34 offset:4096\n" \
"v_accvgpr_read_b32 v218, a106\n" \
"v_mfma_f32_16x16x32_bf16 v[132:135], a[176:179], a[148:151], v[132:135]\n" \
"ds_read_b64_tr_b16 a[234:235], v34 offset:4608\n" \
"v_accvgpr_read_b32 v219, a107\n" \
"v_mfma_f32_16x16x32_bf16 v[132:135], a[240:243], a[212:215], v[132:135]\n" \
"ds_read_b128 a[148:151], v28 offset:43008\n" \
"v_accvgpr_read_b32 v180, a108\n" \
"v_mfma_f32_16x16x32_bf16 v[148:151], a[176:179], a[152:155], v[148:151]\n" \
"ds_read_b128 a[212:215], v29 offset:43008\n" \
"v_accvgpr_read_b32 v181, a109\n" \
"v_mfma_f32_16x16x32_bf16 v[148:151], a[240:243], a[216:219], v[148:151]\n" \
"s_movk_i32 s84, 0x4300\n" \
"v_mfma_f32_16x16x32_bf16 v[152:155], a[180:183], a[152:155], v[152:155]\n" \
"buffer_load_dwordx4 v24, s[76:79], s84 offen offset:2048 lds\n" \
"v_mfma_f32_16x16x32_bf16 v[152:155], a[244:247], a[216:219], v[152:155]\n" \
"s_movk_i32 s84, 0x6300\n" \
"v_mfma_f32_16x16x32_bf16 v[156:159], a[184:187], a[152:155], v[156:159]\n" \
"buffer_load_dwordx4 v25, s[76:79], s84 offen offset:3072 lds\n" \
"v_mfma_f32_16x16x32_bf16 v[156:159], a[248:251], a[216:219], v[156:159]\n" \
"ds_read_b128 v[248:251], v22 offset:0\n" \
"v_accvgpr_read_b32 v182, a110\n" \
"v_mfma_f32_16x16x32_bf16 v[160:163], a[188:191], a[152:155], v[160:163]\n" \
"ds_read_b128 v[48:51], v23 offset:0\n" \
"v_accvgpr_read_b32 v183, a111\n" \
"v_mfma_f32_16x16x32_bf16 v[160:163], a[252:255], a[216:219], v[160:163]\n" \
"ds_read_b128 a[152:155], v28 offset:45056\n" \
"v_cvt_pk_bf16_f32 v216, v216, v217\n" \
"v_mfma_f32_16x16x32_bf16 v[176:179], a[188:191], a[156:159], v[176:179]\n" \
"ds_read_b128 a[216:219], v29 offset:45056\n" \
"v_cvt_pk_bf16_f32 v217, v218, v219\n" \
"v_mfma_f32_16x16x32_bf16 v[176:179], a[252:255], a[220:223], v[176:179]\n" \
"v_cvt_pk_bf16_f32 v218, v180, v181\n" \
"v_cvt_pk_bf16_f32 v219, v182, v183\n" \
"v_mfma_f32_16x16x32_bf16 v[172:175], a[184:187], a[156:159], v[172:175]\n" \
"v_accvgpr_read_b32 v192, a96\n" \
"v_accvgpr_read_b32 v193, a97\n" \
"v_mfma_f32_16x16x32_bf16 v[172:175], a[248:251], a[220:223], v[172:175]\n" \
"v_accvgpr_read_b32 v194, a98\n" \
"v_accvgpr_read_b32 v195, a99\n" \
"v_mfma_f32_16x16x32_bf16 v[168:171], a[180:183], a[156:159], v[168:171]\n" \
"v_accvgpr_read_b32 v180, a100\n" \
"v_accvgpr_read_b32 v181, a101\n" \
"v_mfma_f32_16x16x32_bf16 v[168:171], a[244:247], a[220:223], v[168:171]\n" \
"v_accvgpr_read_b32 v182, a102\n" \
"v_accvgpr_read_b32 v183, a103\n" \
"v_mfma_f32_16x16x32_bf16 v[164:167], a[176:179], a[156:159], v[164:167]\n" \
"v_cvt_pk_bf16_f32 v192, v192, v193\n" \
"v_cvt_pk_bf16_f32 v193, v194, v195\n" \
"v_mfma_f32_16x16x32_bf16 v[164:167], a[240:243], a[220:223], v[164:167]\n" \
"ds_read_b128 a[156:159], v28 offset:47104\n" \
"ds_read_b128 a[220:223], v29 offset:47104\n" \
"s_waitcnt lgkmcnt(15)\n" \
"v_mfma_f32_16x16x32_bf16 a[0:3], a[160:163], a[128:131], a[0:3]\n" \
"s_add_u32 m0, s30, 0x1000\n" \
"s_addk_i32 s84, 0x3000\n" \
"v_mfma_f32_16x16x32_bf16 a[0:3], a[224:227], a[192:195], a[0:3]\n" \
"buffer_load_dwordx4 v24, s[76:79], s84 offen offset:0 lds\n" \
"v_mfma_f32_16x16x32_bf16 a[4:7], a[164:167], a[128:131], a[4:7]\n" \
"ds_read_b64_tr_b16 a[176:177], v36 offset:0\n" \
"v_cvt_pk_bf16_f32 v194, v180, v181\n" \
"v_mfma_f32_16x16x32_bf16 a[4:7], a[228:231], a[192:195], a[4:7]\n" \
"ds_read_b64_tr_b16 a[178:179], v36 offset:1024\n" \
"v_cvt_pk_bf16_f32 v195, v182, v183\n" \
"v_mfma_f32_16x16x32_bf16 a[12:15], a[172:175], a[128:131], a[12:15]\n" \
"ds_read_b64_tr_b16 a[240:241], v36 offset:8192\n" \
"v_accvgpr_read_b32 v188, a88\n" \
"s_waitcnt lgkmcnt(15)\n" \
"v_mfma_f32_16x16x32_bf16 a[12:15], a[236:239], a[192:195], a[12:15]\n" \
"s_add_u32 s88, s42, 0x1000\n" \
"buffer_store_dwordx4 v[252:255], v20, s[80:83], s88 offen offset:2048 sc1\n" \
"s_waitcnt lgkmcnt(13)\n" \
"v_mfma_f32_16x16x32_bf16 a[8:11], a[128:131], a[168:171], a[8:11]\n" \
"ds_read_b64_tr_b16 a[242:243], v36 offset:9216\n" \
"v_accvgpr_read_b32 v189, a89\n" \
"s_waitcnt lgkmcnt(12)\n" \
"s_nop 0\n" \
"v_mfma_f32_16x16x32_bf16 a[8:11], a[192:195], a[232:235], a[8:11]\n" \
"ds_read_b64_tr_b16 a[180:181], v37 offset:0\n" \
"v_accvgpr_read_b32 v190, a90\n" \
"v_mfma_f32_16x16x32_bf16 a[24:27], a[132:135], a[168:171], a[24:27]\n" \
"ds_read_b64_tr_b16 a[182:183], v37 offset:1024\n" \
"v_accvgpr_read_b32 v191, a91\n" \
"v_mfma_f32_16x16x32_bf16 a[24:27], a[196:199], a[232:235], a[24:27]\n" \
"ds_read_b64_tr_b16 a[244:245], v37 offset:8192\n" \
"v_accvgpr_read_b32 v180, a92\n" \
"v_mfma_f32_16x16x32_bf16 a[28:31], a[172:175], a[132:135], a[28:31]\n" \
"v_accvgpr_read_b32 v181, a93\n" \
"v_accvgpr_read_b32 v182, a94\n" \
"v_mfma_f32_16x16x32_bf16 a[28:31], a[236:239], a[196:199], a[28:31]\n" \
"s_addk_i32 s84, 0x2000\n" \
"s_nop 0\n" \
"v_mfma_f32_16x16x32_bf16 a[20:23], a[164:167], a[132:135], a[20:23]\n" \
"buffer_load_dwordx4 v25, s[76:79], s84 offen offset:1024 lds\n" \
"v_mfma_f32_16x16x32_bf16 a[20:23], a[228:231], a[196:199], a[20:23]\n" \
"ds_read_b64_tr_b16 a[246:247], v37 offset:9216\n" \
"v_accvgpr_read_b32 v183, a95\n" \
"v_mfma_f32_16x16x32_bf16 a[16:19], a[160:163], a[132:135], a[16:19]\n" \
"ds_read_b64_tr_b16 a[184:185], v38 offset:0\n" \
"v_cvt_pk_bf16_f32 v188, v188, v189\n" \
"v_mfma_f32_16x16x32_bf16 a[16:19], a[224:227], a[196:199], a[16:19]\n" \
"ds_read_b64_tr_b16 a[186:187], v38 offset:1024\n" \
"v_cvt_pk_bf16_f32 v189, v190, v191\n" \
"v_mfma_f32_16x16x32_bf16 a[32:35], a[160:163], a[136:139], a[32:35]\n" \
"s_addk_i32 s84, 0x2000\n" \
"s_nop 0\n" \
"v_mfma_f32_16x16x32_bf16 a[32:35], a[224:227], a[200:203], a[32:35]\n" \
"buffer_load_dwordx4 v24, s[76:79], s84 offen offset:2048 lds\n" \
"v_mfma_f32_16x16x32_bf16 a[36:39], a[164:167], a[136:139], a[36:39]\n" \
"ds_read_b64_tr_b16 a[248:249], v38 offset:8192\n" \
"v_cvt_pk_bf16_f32 v190, v180, v181\n" \
"v_mfma_f32_16x16x32_bf16 a[36:39], a[228:231], a[200:203], a[36:39]\n" \
"ds_read_b64_tr_b16 a[250:251], v38 offset:9216\n" \
"v_cvt_pk_bf16_f32 v191, v182, v183\n" \
"v_mfma_f32_16x16x32_bf16 a[44:47], a[172:175], a[136:139], a[44:47]\n" \
"ds_read_b64_tr_b16 a[188:189], v39 offset:0\n" \
"v_accvgpr_read_b32 v184, a72\n" \
"v_mfma_f32_16x16x32_bf16 a[44:47], a[236:239], a[200:203], a[44:47]\n" \
"ds_read_b64_tr_b16 a[190:191], v39 offset:1024\n" \
"v_accvgpr_read_b32 v185, a73\n" \
"v_mfma_f32_16x16x32_bf16 a[40:43], a[136:139], a[168:171], a[40:43]\n" \
"s_add_u32 s89, s42, 0x2000\n" \
"buffer_store_dwordx4 v[200:203], v20, s[80:83], s89 offen offset:2048 sc1\n" \
"v_mfma_f32_16x16x32_bf16 a[40:43], a[200:203], a[232:235], a[40:43]\n" \
"ds_read_b64_tr_b16 a[252:253], v39 offset:8192\n" \
".fill (L_pb + 28384 - .) / 4, 4, 0xBF800000\n" \
"L_pp7:\n" \
"s_cbranch_execz L_pret\n" \
"s_nop 0\n" \
"v_accvgpr_read_b32 v186, a74\n" \
"v_mfma_f32_16x16x32_bf16 a[56:59], a[140:143], a[168:171], a[56:59]\n" \
"ds_read_b64_tr_b16 a[254:255], v39 offset:9216\n" \
"v_accvgpr_read_b32 v187, a75\n" \
"v_mfma_f32_16x16x32_bf16 a[56:59], a[204:207], a[232:235], a[56:59]\n" \
"v_accvgpr_read_b32 v180, a76\n" \
"v_accvgpr_read_b32 v181, a77\n" \
"v_mfma_f32_16x16x32_bf16 a[60:63], a[172:175], a[140:143], a[60:63]\n" \
"v_accvgpr_read_b32 v182, a78\n" \
"v_accvgpr_read_b32 v183, a79\n" \
"v_mfma_f32_16x16x32_bf16 a[60:63], a[236:239], a[204:207], a[60:63]\n" \
"s_addk_i32 s84, 0x2000\n" \
"v_mfma_f32_16x16x32_bf16 a[52:55], a[164:167], a[140:143], a[52:55]\n" \
"buffer_load_dwordx4 v25, s[76:79], s84 offen offset:3072 lds\n" \
"v_mfma_f32_16x16x32_bf16 a[52:55], a[228:231], a[204:207], a[52:55]\n" \
"v_cvt_pk_bf16_f32 v184, v184, v185\n" \
"v_cvt_pk_bf16_f32 v185, v186, v187\n" \
"v_mfma_f32_16x16x32_bf16 a[48:51], a[160:163], a[140:143], a[48:51]\n" \
"v_cvt_pk_bf16_f32 v186, v180, v181\n" \
"v_cvt_pk_bf16_f32 v187, v182, v183\n" \
"v_mfma_f32_16x16x32_bf16 a[48:51], a[224:227], a[204:207], a[48:51]\n" \
"s_sub_i32 s14, s9, s14\n" \
"s_add_i32 s20, s19, s13\n" \
"s_waitcnt lgkmcnt(15)\n" \
"v_mfma_f32_16x16x32_bf16 a[64:67], a[164:167], v[248:251], a[64:67]\n" \
"v_writelane_b32 v11, s20, 31\n" \
"s_add_i32 s19, s19, s15\n" \
"v_mfma_f32_16x16x32_bf16 a[64:67], a[228:231], v[48:51], a[64:67]\n" \
"s_mul_i32 s14, s14, 0x24800\n" \
"v_writelane_b32 v11, s19, 32\n" \
"v_mfma_f32_16x16x32_bf16 a[80:83], a[172:175], v[248:251], a[80:83]\n" \
"s_add_i32 s19, s14, 0x4120800\n" \
"s_add_i32 s16, s19, s16\n" \
"v_mfma_f32_16x16x32_bf16 a[80:83], a[236:239], v[48:51], a[80:83]\n" \
"v_writelane_b32 v11, s16, 33\n" \
"s_add_i32 s13, s19, s13\n" \
"v_mfma_f32_16x16x32_bf16 a[68:71], v[248:251], a[168:171], a[68:71]\n" \
"v_writelane_b32 v11, s13, 34\n" \
"s_add_i32 s19, s19, s15\n" \
"v_mfma_f32_16x16x32_bf16 a[68:71], v[48:51], a[232:235], a[68:71]\n" \
"s_waitcnt vmcnt(11) lgkmcnt(0)\n" \
"s_barrier\n" \
"v_mov_b32_e32 v45, s62\n" \
"s_mov_b64 exec, 1\n" \
"buffer_store_dword v45, off, s[80:83], s37 offset:0 sc1\n" \
"s_mov_b64 exec, -1\n" \
"v_xor_b32_e32 v28, 0x20000, v28\n" \
"v_xor_b32_e32 v29, 0x20000, v29\n" \
"v_mfma_f32_16x16x32_bf16 v[52:55], a[176:179], a[128:131], v[52:55]\n" \
"s_add_u32 m0, s40, 0x0\n" \
"s_add_u32 s85, s39, 0xc000\n" \
"v_mfma_f32_16x16x32_bf16 v[52:55], a[240:243], a[192:195], v[52:55]\n" \
"buffer_load_dwordx4 v20, s[80:83], s85 offen sc1 lds\n" \
"v_mfma_f32_16x16x32_bf16 v[56:59], a[180:183], a[128:131], v[56:59]\n" \
"ds_read_b64_tr_b16 a[160:161], v32 offset:32768\n" \
"s_or_b32 s13, s18, 0x3000000\n" \
"v_mfma_f32_16x16x32_bf16 v[56:59], a[244:247], a[192:195], v[56:59]\n" \
"ds_read_b64_tr_b16 a[162:163], v32 offset:33280\n" \
"s_lshl_b32 s15, s6, 11\n" \
"v_mfma_f32_16x16x32_bf16 v[60:63], a[184:187], a[128:131], v[60:63]\n" \
"s_add_u32 m0, s40, 0x1000\n" \
"s_add_u32 s85, s39, 0xd000\n" \
"v_mfma_f32_16x16x32_bf16 v[60:63], a[248:251], a[192:195], v[60:63]\n" \
"buffer_load_dwordx4 v20, s[80:83], s85 offen sc1 lds\n" \
"v_mfma_f32_16x16x32_bf16 v[64:67], a[188:191], a[128:131], v[64:67]\n" \
"ds_read_b64_tr_b16 a[224:225], v32 offset:36864\n" \
"v_writelane_b32 v11, s19, 35\n" \
"v_mfma_f32_16x16x32_bf16 v[64:67], a[252:255], a[192:195], v[64:67]\n" \
"ds_read_b128 a[128:131], v28 offset:0\n" \
"s_add_i32 s16, s13, s15\n" \
"v_mfma_f32_16x16x32_bf16 v[80:83], a[188:191], a[132:135], v[80:83]\n" \
"ds_read_b128 a[192:195], v29 offset:0\n" \
"v_writelane_b32 v11, s16, 36\n" \
"v_mfma_f32_16x16x32_bf16 v[80:83], a[252:255], a[196:199], v[80:83]\n" \
"s_add_u32 m0, s40, 0x3000\n" \
"s_movk_i32 s84, 0x300\n" \
"v_mfma_f32_16x16x32_bf16 v[76:79], a[184:187], a[132:135], v[76:79]\n" \
"buffer_load_dwordx4 v21, s[76:79], s84 offen offset:0 lds\n" \
"v_mfma_f32_16x16x32_bf16 v[76:79], a[248:251], a[196:199], v[76:79]\n" \
"ds_read_b64_tr_b16 a[226:227], v32 offset:37376\n" \
"s_add_i32 s16, s14, 0x411c000\n" \
"v_mfma_f32_16x16x32_bf16 v[72:75], a[180:183], a[132:135], v[72:75]\n" \
"ds_read_b64_tr_b16 a[164:165], v33 offset:32768\n" \
"s_add_i32 s19, s16, s15\n" \
"v_mfma_f32_16x16x32_bf16 v[72:75], a[244:247], a[196:199], v[72:75]\n" \
"v_writelane_b32 v11, s19, 37\n" \
"s_add_i32 s18, s18, 0x3004000\n" \
"v_mfma_f32_16x16x32_bf16 v[68:71], a[176:179], a[132:135], v[68:71]\n" \
"ds_read_b64_tr_b16 a[166:167], v33 offset:33280\n" \
"v_writelane_b32 v11, s18, 38\n" \
"v_mfma_f32_16x16x32_bf16 v[68:71], a[240:243], a[196:199], v[68:71]\n" \
"ds_read_b128 a[132:135], v28 offset:2048\n" \
"s_add_i32 s14, s14, 0x4120000\n" \
"v_mfma_f32_16x16x32_bf16 v[84:87], a[176:179], a[136:139], v[84:87]\n" \
"ds_read_b128 a[196:199], v29 offset:2048\n" \
"v_writelane_b32 v11, s14, 39\n" \
"v_mfma_f32_16x16x32_bf16 v[84:87], a[240:243], a[200:203], v[84:87]\n" \
"s_add_u32 m0, s31, 0x0\n" \
"s_mov_b32 s84, 0x300000\n" \
"v_mfma_f32_16x16x32_bf16 v[88:91], a[180:183], a[136:139], v[88:91]\n" \
"buffer_load_dwordx4 v26, s[68:71], s84 offen offset:0 lds\n" \
"v_mfma_f32_16x16x32_bf16 v[88:91], a[244:247], a[200:203], v[88:91]\n" \
"s_add_u32 s88, s42, 0x2000\n" \
"buffer_store_dwordx4 v[204:207], v20, s[80:83], s88 offen offset:3072 sc1\n" \
"v_mfma_f32_16x16x32_bf16 v[92:95], a[184:187], a[136:139], v[92:95]\n" \
"ds_read_b64_tr_b16 a[228:229], v33 offset:36864\n" \
"s_lshl_b32 s14, s4, 2\n" \
"v_mfma_f32_16x16x32_bf16 v[92:95], a[248:251], a[200:203], v[92:95]\n" \
"ds_read_b64_tr_b16 a[230:231], v33 offset:37376\n" \
"s_or_b32 s8, s14, s8\n" \
"v_mfma_f32_16x16x32_bf16 v[96:99], a[188:191], a[136:139], v[96:99]\n" \
"s_mul_i32 s8, s8, 0x90000\n" \
"s_lshl_b32 s14, s3, 14\n" \
"v_mfma_f32_16x16x32_bf16 v[96:99], a[252:255], a[200:203], v[96:99]\n" \
"ds_read_b128 a[136:139], v28 offset:4096\n" \
"s_add_i32 s8, s8, s14\n" \
"v_mfma_f32_16x16x32_bf16 v[112:115], a[188:191], a[140:143], v[112:115]\n" \
"ds_read_b128 a[200:203], v29 offset:4096\n" \
"s_lshl_b32 s9, s9, 4\n" \
"v_mfma_f32_16x16x32_bf16 v[112:115], a[252:255], a[204:207], v[112:115]\n" \
"s_addk_i32 s84, 0x7c00\n" \
"v_mfma_f32_16x16x32_bf16 v[108:111], a[184:187], a[140:143], v[108:111]\n" \
"buffer_load_dwordx4 v26, s[68:71], s84 offen offset:1024 lds\n" \
"v_mfma_f32_16x16x32_bf16 v[108:111], a[248:251], a[204:207], v[108:111]\n" \
"ds_read_b64_tr_b16 a[172:173], v35 offset:32768\n" \
"s_lshl_b32 s14, s4, 7\n" \
"v_mfma_f32_16x16x32_bf16 v[104:107], a[180:183], a[140:143], v[104:107]\n" \
"s_addk_i32 s84, 0x7c00\n" \
"v_mfma_f32_16x16x32_bf16 v[104:107], a[244:247], a[204:207], v[104:107]\n" \
"buffer_load_dwordx4 v27, s[68:71], s84 offen offset:2048 lds\n" \
"v_mfma_f32_16x16x32_bf16 v[100:103], a[176:179], a[140:143], v[100:103]\n" \
"ds_read_b64_tr_b16 a[174:175], v35 offset:33280\n" \
"s_or_b32 s18, s9, s14\n" \
"v_mfma_f32_16x16x32_bf16 v[100:103], a[240:243], a[204:207], v[100:103]\n" \
"ds_read_b128 a[140:143], v28 offset:6144\n" \
"s_sub_i32 s9, s9, s14\n" \
"v_mfma_f32_16x16x32_bf16 v[116:119], a[176:179], a[144:147], v[116:119]\n" \
"ds_read_b128 a[204:207], v29 offset:6144\n" \
"s_mulk_i32 s18, 0x480\n" \
"v_mfma_f32_16x16x32_bf16 v[116:119], a[240:243], a[208:211], v[116:119]\n" \
"s_addk_i32 s84, 0x7c00\n" \
"v_mfma_f32_16x16x32_bf16 v[120:123], a[180:183], a[144:147], v[120:123]\n" \
"buffer_load_dwordx4 v27, s[68:71], s84 offen offset:3072 lds\n" \
"v_mfma_f32_16x16x32_bf16 v[120:123], a[244:247], a[208:211], v[120:123]\n" \
"ds_read_b64_tr_b16 a[236:237], v35 offset:36864\n" \
"s_mulk_i32 s9, 0x480\n" \
"v_mfma_f32_16x16x32_bf16 v[124:127], a[184:187], a[144:147], v[124:127]\n" \
"ds_read_b64_tr_b16 a[238:239], v35 offset:37376\n" \
"s_or_b32 s14, s18, s12\n" \
"v_mfma_f32_16x16x32_bf16 v[124:127], a[248:251], a[208:211], v[124:127]\n" \
"s_add_u32 m0, s30, 0x8000\n" \
"s_movk_i32 s84, 0x380\n" \
"v_mfma_f32_16x16x32_bf16 v[128:131], a[188:191], a[144:147], v[128:131]\n" \
"buffer_load_dwordx4 v24, s[76:79], s84 offen offset:0 lds\n" \
"v_mfma_f32_16x16x32_bf16 v[128:131], a[252:255], a[208:211], v[128:131]\n" \
"ds_read_b128 a[144:147], v28 offset:8192\n" \
"s_or_b32 s9, s9, s12\n" \
"v_mfma_f32_16x16x32_bf16 v[144:147], a[188:191], a[148:151], v[144:147]\n" \
"ds_read_b128 a[208:211], v29 offset:8192\n" \
"s_and_b32 s12, s2, 1\n" \
"v_mfma_f32_16x16x32_bf16 v[144:147], a[252:255], a[212:215], v[144:147]\n" \
"ds_read_b64_tr_b16 a[168:169], v34 offset:32768\n" \
"v_mfma_f32_16x16x32_bf16 v[140:143], a[184:187], a[148:151], v[140:143]\n" \
"s_movk_i32 s84, 0x2380\n" \
"s_nop 0\n" \
"v_mfma_f32_16x16x32_bf16 v[140:143], a[248:251], a[212:215], v[140:143]\n" \
"buffer_load_dwordx4 v25, s[76:79], s84 offen offset:1024 lds\n" \
"v_mfma_f32_16x16x32_bf16 v[136:139], a[180:183], a[148:151], v[136:139]\n" \
"ds_read_b64_tr_b16 a[170:171], v34 offset:33280\n" \
"v_mfma_f32_16x16x32_bf16 v[136:139], a[244:247], a[212:215], v[136:139]\n" \
"ds_read_b64_tr_b16 a[232:233], v34 offset:36864\n" \
"v_mfma_f32_16x16x32_bf16 v[132:135], a[176:179], a[148:151], v[132:135]\n" \
"ds_read_b64_tr_b16 a[234:235], v34 offset:37376\n" \
"v_mfma_f32_16x16x32_bf16 v[132:135], a[240:243], a[212:215], v[132:135]\n" \
"ds_read_b128 a[148:151], v28 offset:10240\n" \
"v_mfma_f32_16x16x32_bf16 v[148:151], a[176:179], a[152:155], v[148:151]\n" \
"ds_read_b128 a[212:215], v29 offset:10240\n" \
"v_mfma_f32_16x16x32_bf16 v[148:151], a[240:243], a[216:219], v[148:151]\n" \
"s_movk_i32 s84, 0x4380\n" \
"v_mfma_f32_16x16x32_bf16 v[152:155], a[180:183], a[152:155], v[152:155]\n" \
"buffer_load_dwordx4 v24, s[76:79], s84 offen offset:2048 lds\n" \
"v_mfma_f32_16x16x32_bf16 v[152:155], a[244:247], a[216:219], v[152:155]\n" \
"s_movk_i32 s84, 0x6380\n" \
"v_mfma_f32_16x16x32_bf16 v[156:159], a[184:187], a[152:155], v[156:159]\n" \
"buffer_load_dwordx4 v25, s[76:79], s84 offen offset:3072 lds\n" \
"v_mfma_f32_16x16x32_bf16 v[156:159], a[248:251], a[216:219], v[156:159]\n" \
"ds_read_b128 v[248:251], v22 offset:32768\n" \
"v_mfma_f32_16x16x32_bf16 v[160:163], a[188:191], a[152:155], v[160:163]\n" \
"ds_read_b128 v[48:51], v23 offset:32768\n" \
"v_mfma_f32_16x16x32_bf16 v[160:163], a[252:255], a[216:219], v[160:163]\n" \
"ds_read_b128 a[152:155], v28 offset:12288\n" \
"v_mfma_f32_16x16x32_bf16 v[176:179], a[188:191], a[156:159], v[176:179]\n" \
"ds_read_b128 a[216:219], v29 offset:12288\n" \
"v_mfma_f32_16x16x32_bf16 v[176:179], a[252:255], a[220:223], v[176:179]\n" \
"s_cmp_lg_u32 s12, 0\n" \
"s_cselect_b32 s13, s16, s13\n" \
"v_mfma_f32_16x16x32_bf16 v[172:175], a[184:187], a[156:159], v[172:175]\n" \
"s_bfe_u32 s16, s2, 0x20001\n" \
"s_addk_i32 s13, 0x4000\n" \
"v_mfma_f32_16x16x32_bf16 v[172:175], a[248:251], a[220:223], v[172:175]\n" \
"s_xor_b32 s16, s16, s3\n" \
"v_mfma_f32_16x16x32_bf16 v[168:171], a[180:183], a[156:159], v[168:171]\n" \
"s_cmp_lg_u32 s16, 0\n" \
"s_cselect_b32 s13, s7, s13\n" \
"v_mfma_f32_16x16x32_bf16 v[168:171], a[244:247], a[220:223], v[168:171]\n" \
"s_lshl_b32 s6, s6, 16\n" \
"v_writelane_b32 v11, s13, 40\n" \
"v_mfma_f32_16x16x32_bf16 v[164:167], a[176:179], a[156:159], v[164:167]\n" \
"s_add_i32 s13, s8, 0x4240000\n" \
"s_add_i32 s6, s13, s6\n" \
"v_mfma_f32_16x16x32_bf16 v[164:167], a[240:243], a[220:223], v[164:167]\n" \
"ds_read_b128 a[156:159], v28 offset:14336\n" \
"ds_read_b128 a[220:223], v29 offset:14336\n" \
"s_waitcnt lgkmcnt(15)\n" \
"v_mfma_f32_16x16x32_bf16 a[0:3], a[160:163], a[128:131], a[0:3]\n" \
"s_add_u32 m0, s30, 0x9000\n" \
"s_addk_i32 s84, 0x3000\n" \
"v_mfma_f32_16x16x32_bf16 a[0:3], a[224:227], a[192:195], a[0:3]\n" \
"buffer_load_dwordx4 v24, s[76:79], s84 offen offset:0 lds\n" \
"v_mfma_f32_16x16x32_bf16 a[4:7], a[164:167], a[128:131], a[4:7]\n" \
"ds_read_b64_tr_b16 a[176:177], v36 offset:32768\n" \
"v_writelane_b32 v11, s6, 41\n" \
"v_mfma_f32_16x16x32_bf16 a[4:7], a[228:231], a[192:195], a[4:7]\n" \
"ds_read_b64_tr_b16 a[178:179], v36 offset:33792\n" \
"s_add_i32 s8, s8, 0x42c0000\n" \
"v_mfma_f32_16x16x32_bf16 a[12:15], a[172:175], a[128:131], a[12:15]\n" \
"ds_read_b64_tr_b16 a[240:241], v36 offset:40960\n" \
"s_lshl_b32 s6, s12, 10\n" \
"s_waitcnt lgkmcnt(15)\n" \
"v_mfma_f32_16x16x32_bf16 a[12:15], a[236:239], a[192:195], a[12:15]\n" \
"s_add_u32 s89, s42, 0x3000\n" \
"buffer_store_dwordx4 v[208:211], v20, s[80:83], s89 offen offset:3072 sc1\n" \
"s_waitcnt lgkmcnt(13)\n" \
"v_mfma_f32_16x16x32_bf16 a[8:11], a[128:131], a[168:171], a[8:11]\n" \
"ds_read_b64_tr_b16 a[242:243], v36 offset:41984\n" \
"s_or_b32 s6, s8, s6\n" \
"s_waitcnt lgkmcnt(12)\n" \
"v_mfma_f32_16x16x32_bf16 a[8:11], a[192:195], a[232:235], a[8:11]\n" \
"ds_read_b64_tr_b16 a[180:181], v37 offset:32768\n" \
"v_mfma_f32_16x16x32_bf16 a[24:27], a[132:135], a[168:171], a[24:27]\n" \
"ds_read_b64_tr_b16 a[182:183], v37 offset:33792\n" \
"v_mfma_f32_16x16x32_bf16 a[24:27], a[196:199], a[232:235], a[24:27]\n" \
"ds_read_b64_tr_b16 a[244:245], v37 offset:40960\n" \
"v_mfma_f32_16x16x32_bf16 a[28:31], a[172:175], a[132:135], a[28:31]\n" \
"s_cmp_lg_u32 s16, 0\n" \
"s_cselect_b32 s6, s7, s6\n" \
"v_mfma_f32_16x16x32_bf16 a[28:31], a[236:239], a[196:199], a[28:31]\n" \
"s_addk_i32 s84, 0x2000\n" \
"v_mfma_f32_16x16x32_bf16 a[20:23], a[164:167], a[132:135], a[20:23]\n" \
"buffer_load_dwordx4 v25, s[76:79], s84 offen offset:1024 lds\n" \
"v_mfma_f32_16x16x32_bf16 a[20:23], a[228:231], a[196:199], a[20:23]\n" \
"ds_read_b64_tr_b16 a[246:247], v37 offset:41984\n" \
"s_mulk_i32 s4, 0x4800\n" \
"v_mfma_f32_16x16x32_bf16 a[16:19], a[160:163], a[132:135], a[16:19]\n" \
"ds_read_b64_tr_b16 a[184:185], v38 offset:32768\n" \
"v_writelane_b32 v11, s6, 42\n" \
"v_mfma_f32_16x16x32_bf16 a[16:19], a[224:227], a[196:199], a[16:19]\n" \
"ds_read_b64_tr_b16 a[186:187], v38 offset:33792\n" \
"s_add_i32 s6, s14, s10\n" \
"v_mfma_f32_16x16x32_bf16 a[32:35], a[160:163], a[136:139], a[32:35]\n" \
"s_addk_i32 s84, 0x2000\n" \
"v_mfma_f32_16x16x32_bf16 a[32:35], a[224:227], a[200:203], a[32:35]\n" \
"buffer_load_dwordx4 v24, s[76:79], s84 offen offset:2048 lds\n" \
"v_mfma_f32_16x16x32_bf16 a[36:39], a[164:167], a[136:139], a[36:39]\n" \
"ds_read_b64_tr_b16 a[248:249], v38 offset:40960\n" \
"v_writelane_b32 v11, s6, 43\n" \
"v_mfma_f32_16x16x32_bf16 a[36:39], a[228:231], a[200:203], a[36:39]\n" \
"ds_read_b64_tr_b16 a[250:251], v38 offset:41984\n" \
"s_add_i32 s6, s9, 0x21c000\n" \
"v_mfma_f32_16x16x32_bf16 a[44:47], a[172:175], a[136:139], a[44:47]\n" \
"ds_read_b64_tr_b16 a[188:189], v39 offset:32768\n" \
"s_add_i32 s9, s6, s10\n" \
"v_mfma_f32_16x16x32_bf16 a[44:47], a[236:239], a[200:203], a[44:47]\n" \
"ds_read_b64_tr_b16 a[190:191], v39 offset:33792\n" \
"v_mfma_f32_16x16x32_bf16 a[40:43], a[136:139], a[168:171], a[40:43]\n" \
"s_add_u32 s88, s42, 0x3000\n" \
"buffer_store_dwordx4 v[212:215], v20, s[80:83], s88 offen offset:2048 sc1\n" \
"v_mfma_f32_16x16x32_bf16 a[40:43], a[200:203], a[232:235], a[40:43]\n" \
"ds_read_b64_tr_b16 a[252:253], v39 offset:40960\n" \
"v_mfma_f32_16x16x32_bf16 a[56:59], a[140:143], a[168:171], a[56:59]\n" \
"ds_read_b64_tr_b16 a[254:255], v39 offset:41984\n" \
"v_mfma_f32_16x16x32_bf16 a[56:59], a[204:207], a[232:235], a[56:59]\n" \
"s_cmp_lg_u32 s12, 0\n" \
"s_cselect_b32 s6, s6, s14\n" \
"v_mfma_f32_16x16x32_bf16 a[60:63], a[172:175], a[140:143], a[60:63]\n" \
"s_addk_i32 s6, 0x100\n" \
"v_writelane_b32 v11, s9, 44\n" \
"v_mfma_f32_16x16x32_bf16 a[60:63], a[236:239], a[204:207], a[60:63]\n" \
"s_addk_i32 s84, 0x2000\n" \
"v_mfma_f32_16x16x32_bf16 a[52:55], a[164:167], a[140:143], a[52:55]\n" \
"buffer_load_dwordx4 v25, s[76:79], s84 offen offset:3072 lds\n" \
"v_mfma_f32_16x16x32_bf16 a[52:55], a[228:231], a[204:207], a[52:55]\n" \
"s_cmp_lg_u32 s16, 0\n" \
"s_cselect_b32 s6, s7, s6\n" \
"v_mfma_f32_16x16x32_bf16 a[48:51], a[160:163], a[140:143], a[48:51]\n" \
"v_writelane_b32 v11, s6, 45\n" \
"s_or_b32 s6, s8, s15\n" \
"v_mfma_f32_16x16x32_bf16 a[48:51], a[224:227], a[204:207], a[48:51]\n" \
"v_writelane_b32 v11, s6, 53\n" \
"s_lshl_b32 s6, s3, 17\n" \
"s_waitcnt lgkmcnt(15)\n" \
"v_mfma_f32_16x16x32_bf16 a[64:67], a[164:167], v[248:251], a[64:67]\n" \
"s_add_i32 s6, s13, s6\n" \
"s_or_b32 s5, s6, s5\n" \
"v_mfma_f32_16x16x32_bf16 a[64:67], a[228:231], v[48:51], a[64:67]\n" \
"s_or_b32 s5, s5, 0x3840\n" \
"v_writelane_b32 v11, s5, 6\n" \
"v_mfma_f32_16x16x32_bf16 a[80:83], a[172:175], v[248:251], a[80:83]\n" \
"s_mov_b32 s5, 0xff0000\n" \
"v_mfma_f32_16x16x32_bf16 a[80:83], a[236:239], v[48:51], a[80:83]\n" \
"s_cmp_lg_u32 s16, 0\n" \
"s_cselect_b32 s5, s17, s5\n" \
"v_mfma_f32_16x16x32_bf16 a[68:71], v[248:251], a[168:171], a[68:71]\n" \
"v_writelane_b32 v11, s5, 7\n" \
"s_mul_i32 s5, s3, 0x1200\n" \
"v_mfma_f32_16x16x32_bf16 a[68:71], v[48:51], a[232:235], a[68:71]\n" \
"s_waitcnt vmcnt(10) lgkmcnt(0)\n" \
"s_barrier\n" \
"v_xor_b32_e32 v28, 0x20000, v28\n" \
"v_xor_b32_e32 v29, 0x20000, v29\n" \
"v_mfma_f32_16x16x32_bf16 v[52:55], a[176:179], a[128:131], v[52:55]\n" \
"s_add_u32 m0, s40, 0x8000\n" \
"s_add_u32 s85, s39, 0xe000\n" \
"v_mfma_f32_16x16x32_bf16 v[52:55], a[240:243], a[192:195], v[52:55]\n" \
"buffer_load_dwordx4 v20, s[80:83], s85 offen sc1 lds\n" \
"v_mfma_f32_16x16x32_bf16 v[56:59], a[180:183], a[128:131], v[56:59]\n" \
"ds_read_b64_tr_b16 a[160:161], v32 offset:0\n" \
"s_add_i32 s4, s5, s4\n" \
"v_mfma_f32_16x16x32_bf16 v[56:59], a[244:247], a[192:195], v[56:59]\n" \
"ds_read_b64_tr_b16 a[162:163], v32 offset:512\n" \
"s_add_i32 s4, s4, s11\n" \
"v_mfma_f32_16x16x32_bf16 v[60:63], a[184:187], a[128:131], v[60:63]\n" \
"s_add_u32 m0, s40, 0x9000\n" \
"s_add_u32 s85, s39, 0xf000\n" \
"v_mfma_f32_16x16x32_bf16 v[60:63], a[248:251], a[192:195], v[60:63]\n" \
"buffer_load_dwordx4 v20, s[80:83], s85 offen sc1 lds\n" \
"v_mfma_f32_16x16x32_bf16 v[64:67], a[188:191], a[128:131], v[64:67]\n" \
"ds_read_b64_tr_b16 a[224:225], v32 offset:4096\n" \
"v_writelane_b32 v11, s4, 8\n" \
"v_mfma_f32_16x16x32_bf16 v[64:67], a[252:255], a[192:195], v[64:67]\n" \
"ds_read_b128 a[128:131], v28 offset:0\n" \
"s_lshl_b32 s4, s2, 16\n" \
"v_mfma_f32_16x16x32_bf16 v[80:83], a[188:191], a[132:135], v[80:83]\n" \
"ds_read_b128 a[192:195], v29 offset:0\n" \
"s_add_i32 s5, s4, 0x10000\n" \
"v_mfma_f32_16x16x32_bf16 v[80:83], a[252:255], a[196:199], v[80:83]\n" \
"s_add_u32 m0, s40, 0xb000\n" \
"s_movk_i32 s84, 0x380\n" \
"v_mfma_f32_16x16x32_bf16 v[76:79], a[184:187], a[132:135], v[76:79]\n" \
"buffer_load_dwordx4 v21, s[76:79], s84 offen offset:0 lds\n" \
"v_mfma_f32_16x16x32_bf16 v[76:79], a[248:251], a[196:199], v[76:79]\n" \
"ds_read_b64_tr_b16 a[226:227], v32 offset:4608\n" \
"s_and_b32 s5, s5, 0x70000\n" \
"v_mfma_f32_16x16x32_bf16 v[72:75], a[180:183], a[132:135], v[72:75]\n" \
"ds_read_b64_tr_b16 a[164:165], v33 offset:0\n" \
"s_add_i32 s5, s13, s5\n" \
"v_mfma_f32_16x16x32_bf16 v[72:75], a[244:247], a[196:199], v[72:75]\n" \
"v_writelane_b32 v11, s5, 46\n" \
"s_add_i32 s5, s4, 0x20000\n" \
"v_mfma_f32_16x16x32_bf16 v[68:71], a[176:179], a[132:135], v[68:71]\n" \
"ds_read_b64_tr_b16 a[166:167], v33 offset:512\n" \
"s_and_b32 s5, s5, 0x70000\n" \
"v_mfma_f32_16x16x32_bf16 v[68:71], a[240:243], a[196:199], v[68:71]\n" \
"ds_read_b128 a[132:135], v28 offset:2048\n" \
"s_add_i32 s5, s13, s5\n" \
"v_mfma_f32_16x16x32_bf16 v[84:87], a[176:179], a[136:139], v[84:87]\n" \
"ds_read_b128 a[196:199], v29 offset:2048\n" \
"s_bitset1_b32 s5, 11\n" \
"v_mfma_f32_16x16x32_bf16 v[84:87], a[240:243], a[200:203], v[84:87]\n" \
"s_add_u32 m0, s31, 0x8000\n" \
"s_mov_b32 s84, 0x380000\n" \
"v_mfma_f32_16x16x32_bf16 v[88:91], a[180:183], a[136:139], v[88:91]\n" \
"buffer_load_dwordx4 v26, s[68:71], s84 offen offset:0 lds\n" \
"v_mfma_f32_16x16x32_bf16 v[88:91], a[244:247], a[200:203], v[88:91]\n" \
"s_add_u32 s89, s42, 0x0\n" \
"buffer_store_dwordx4 v[184:187], v20, s[80:83], s89 offen offset:1024 sc1\n" \
"v_mfma_f32_16x16x32_bf16 v[92:95], a[184:187], a[136:139], v[92:95]\n" \
"ds_read_b64_tr_b16 a[228:229], v33 offset:4096\n" \
"v_writelane_b32 v11, s5, 47\n" \
"v_mfma_f32_16x16x32_bf16 v[92:95], a[248:251], a[200:203], v[92:95]\n" \
"ds_read_b64_tr_b16 a[230:231], v33 offset:4608\n" \
"s_add_i32 s5, s4, 0x30000\n" \
"v_mfma_f32_16x16x32_bf16 v[96:99], a[188:191], a[136:139], v[96:99]\n" \
"s_and_b32 s5, s5, 0x70000\n" \
"s_add_i32 s5, s13, s5\n" \
"v_mfma_f32_16x16x32_bf16 v[96:99], a[252:255], a[200:203], v[96:99]\n" \
"ds_read_b128 a[136:139], v28 offset:4096\n" \
"s_bitset1_b32 s5, 12\n" \
"v_mfma_f32_16x16x32_bf16 v[112:115], a[188:191], a[140:143], v[112:115]\n" \
"ds_read_b128 a[200:203], v29 offset:4096\n" \
"v_writelane_b32 v11, s5, 48\n" \
"v_mfma_f32_16x16x32_bf16 v[112:115], a[252:255], a[204:207], v[112:115]\n" \
"s_addk_i32 s84, 0x7c00\n" \
"v_mfma_f32_16x16x32_bf16 v[108:111], a[184:187], a[140:143], v[108:111]\n" \
"buffer_load_dwordx4 v26, s[68:71], s84 offen offset:1024 lds\n" \
"v_mfma_f32_16x16x32_bf16 v[108:111], a[248:251], a[204:207], v[108:111]\n" \
"ds_read_b64_tr_b16 a[172:173], v35 offset:0\n" \
"s_and_b32 s5, s4, 0x70000\n" \
"v_mfma_f32_16x16x32_bf16 v[104:107], a[180:183], a[140:143], v[104:107]\n" \
"s_addk_i32 s84, 0x7c00\n" \
"v_mfma_f32_16x16x32_bf16 v[104:107], a[244:247], a[204:207], v[104:107]\n" \
"buffer_load_dwordx4 v27, s[68:71], s84 offen offset:2048 lds\n" \
"v_mfma_f32_16x16x32_bf16 v[100:103], a[176:179], a[140:143], v[100:103]\n" \
"ds_read_b64_tr_b16 a[174:175], v35 offset:512\n" \
"s_xor_b32 s5, s5, 0x40000\n" \
"v_mfma_f32_16x16x32_bf16 v[100:103], a[240:243], a[204:207], v[100:103]\n" \
"ds_read_b128 a[140:143], v28 offset:6144\n" \
"s_add_i32 s5, s13, s5\n" \
"v_mfma_f32_16x16x32_bf16 v[116:119], a[176:179], a[144:147], v[116:119]\n" \
"ds_read_b128 a[204:207], v29 offset:6144\n" \
"s_or_b32 s5, s5, 0x1800\n" \
"v_mfma_f32_16x16x32_bf16 v[116:119], a[240:243], a[208:211], v[116:119]\n" \
"s_addk_i32 s84, 0x7c00\n" \
"v_mfma_f32_16x16x32_bf16 v[120:123], a[180:183], a[144:147], v[120:123]\n" \
"buffer_load_dwordx4 v27, s[68:71], s84 offen offset:3072 lds\n" \
"v_mfma_f32_16x16x32_bf16 v[120:123], a[244:247], a[208:211], v[120:123]\n" \
"ds_read_b64_tr_b16 a[236:237], v35 offset:4096\n" \
"v_writelane_b32 v11, s5, 49\n" \
"v_mfma_f32_16x16x32_bf16 v[124:127], a[184:187], a[144:147], v[124:127]\n" \
"ds_read_b64_tr_b16 a[238:239], v35 offset:4608\n" \
"s_add_i32 s5, s4, 0x50000\n" \
"v_mfma_f32_16x16x32_bf16 v[124:127], a[248:251], a[208:211], v[124:127]\n" \
"s_add_u32 m0, s30, 0x20000\n" \
"s_movk_i32 s84, 0x400\n" \
"v_mfma_f32_16x16x32_bf16 v[128:131], a[188:191], a[144:147], v[128:131]\n" \
"buffer_load_dwordx4 v24, s[76:79], s84 offen offset:0 lds\n" \
"v_mfma_f32_16x16x32_bf16 v[128:131], a[252:255], a[208:211], v[128:131]\n" \
"ds_read_b128 a[144:147], v28 offset:8192\n" \
"s_and_b32 s5, s5, 0x70000\n" \
"v_mfma_f32_16x16x32_bf16 v[144:147], a[188:191], a[148:151], v[144:147]\n" \
"ds_read_b128 a[208:211], v29 offset:8192\n" \
"s_add_i32 s5, s13, s5\n" \
"v_mfma_f32_16x16x32_bf16 v[144:147], a[252:255], a[212:215], v[144:147]\n" \
"ds_read_b64_tr_b16 a[168:169], v34 offset:0\n" \
"s_bitset1_b32 s5, 13\n" \
"v_mfma_f32_16x16x32_bf16 v[140:143], a[184:187], a[148:151], v[140:143]\n" \
"s_movk_i32 s84, 0x2400\n" \
"s_nop 0\n" \
"v_mfma_f32_16x16x32_bf16 v[140:143], a[248:251], a[212:215], v[140:143]\n" \
"buffer_load_dwordx4 v25, s[76:79], s84 offen offset:1024 lds\n" \
"v_mfma_f32_16x16x32_bf16 v[136:139], a[180:183], a[148:151], v[136:139]\n" \
"ds_read_b64_tr_b16 a[170:171], v34 offset:512\n" \
"v_writelane_b32 v11, s5, 50\n" \
"v_mfma_f32_16x16x32_bf16 v[136:139], a[244:247], a[212:215], v[136:139]\n" \
"ds_read_b64_tr_b16 a[232:233], v34 offset:4096\n" \
"s_add_i32 s5, s4, 0x60000\n" \
"v_mfma_f32_16x16x32_bf16 v[132:135], a[176:179], a[148:151], v[132:135]\n" \
"ds_read_b64_tr_b16 a[234:235], v34 offset:4608\n" \
"s_and_b32 s5, s5, 0x70000\n" \
"v_mfma_f32_16x16x32_bf16 v[132:135], a[240:243], a[212:215], v[132:135]\n" \
"ds_read_b128 a[148:151], v28 offset:10240\n" \
"s_add_i32 s4, s4, 0x70000\n" \
"v_mfma_f32_16x16x32_bf16 v[148:151], a[176:179], a[152:155], v[148:151]\n" \
"ds_read_b128 a[212:215], v29 offset:10240\n" \
"s_add_i32 s5, s13, s5\n" \
"v_mfma_f32_16x16x32_bf16 v[148:151], a[240:243], a[216:219], v[148:151]\n" \
"s_movk_i32 s84, 0x4400\n" \
"v_mfma_f32_16x16x32_bf16 v[152:155], a[180:183], a[152:155], v[152:155]\n" \
"buffer_load_dwordx4 v24, s[76:79], s84 offen offset:2048 lds\n" \
"v_mfma_f32_16x16x32_bf16 v[152:155], a[244:247], a[216:219], v[152:155]\n" \
"s_movk_i32 s84, 0x6400\n" \
"s_nop 0\n" \
"v_mfma_f32_16x16x32_bf16 v[156:159], a[184:187], a[152:155], v[156:159]\n" \
"buffer_load_dwordx4 v25, s[76:79], s84 offen offset:3072 lds\n" \
"v_mfma_f32_16x16x32_bf16 v[156:159], a[248:251], a[216:219], v[156:159]\n" \
"ds_read_b128 v[248:251], v22 offset:0\n" \
"s_and_b32 s4, s4, 0x70000\n" \
"v_mfma_f32_16x16x32_bf16 v[160:163], a[188:191], a[152:155], v[160:163]\n" \
"ds_read_b128 v[48:51], v23 offset:0\n" \
"s_or_b32 s5, s5, 0x2800\n" \
"v_mfma_f32_16x16x32_bf16 v[160:163], a[252:255], a[216:219], v[160:163]\n" \
"ds_read_b128 a[152:155], v28 offset:12288\n" \
"v_writelane_b32 v11, s5, 51\n" \
"v_mfma_f32_16x16x32_bf16 v[176:179], a[188:191], a[156:159], v[176:179]\n" \
"ds_read_b128 a[216:219], v29 offset:12288\n" \
"s_add_i32 s13, s13, s4\n" \
"v_mfma_f32_16x16x32_bf16 v[176:179], a[252:255], a[220:223], v[176:179]\n" \
"s_or_b32 s4, s13, 0x3000\n" \
"v_writelane_b32 v11, s4, 52\n" \
"v_mfma_f32_16x16x32_bf16 v[172:175], a[184:187], a[156:159], v[172:175]\n" \
"s_movk_i32 s11, 0x4000\n" \
"s_lshl_b32 s4, s2, 2\n" \
"v_mfma_f32_16x16x32_bf16 v[172:175], a[248:251], a[220:223], v[172:175]\n" \
"s_and_b32 s4, s4, 28\n" \
"s_bfe_u32 s5, s2, 0x20003\n" \
"v_mfma_f32_16x16x32_bf16 v[168:171], a[180:183], a[156:159], v[168:171]\n" \
"s_or_b32 s4, s4, s5\n" \
"s_lshr_b32 s5, s2, 5\n" \
"v_mfma_f32_16x16x32_bf16 v[168:171], a[244:247], a[220:223], v[168:171]\n" \
"s_lshr_b32 s19, s3, 1\n" \
"s_lshl_b32 s18, s5, 1\n" \
"v_mfma_f32_16x16x32_bf16 v[164:167], a[176:179], a[156:159], v[164:167]\n" \
"s_lshl_b32 s6, s4, 20\n" \
"s_lshl_b32 s7, s4, 8\n" \
"v_mfma_f32_16x16x32_bf16 v[164:167], a[240:243], a[220:223], v[164:167]\n" \
"ds_read_b128 a[156:159], v28 offset:14336\n" \
"ds_read_b128 a[220:223], v29 offset:14336\n" \
"s_waitcnt lgkmcnt(15)\n" \
"v_mfma_f32_16x16x32_bf16 a[0:3], a[160:163], a[128:131], a[0:3]\n" \
"s_add_u32 m0, s30, 0x21000\n" \
"s_addk_i32 s84, 0x3000\n" \
"v_mfma_f32_16x16x32_bf16 a[0:3], a[224:227], a[192:195], a[0:3]\n" \
"buffer_load_dwordx4 v24, s[76:79], s84 offen offset:0 lds\n" \
"v_mfma_f32_16x16x32_bf16 a[4:7], a[164:167], a[128:131], a[4:7]\n" \
"ds_read_b64_tr_b16 a[176:177], v36 offset:0\n" \
"s_and_b32 s13, s3, 1\n" \
"v_mfma_f32_16x16x32_bf16 a[4:7], a[228:231], a[192:195], a[4:7]\n" \
"ds_read_b64_tr_b16 a[178:179], v36 offset:1024\n" \
"s_add_i32 s14, s18, s19\n" \
"v_mfma_f32_16x16x32_bf16 a[12:15], a[172:175], a[128:131], a[12:15]\n" \
"ds_read_b64_tr_b16 a[240:241], v36 offset:8192\n" \
"s_waitcnt lgkmcnt(15)\n" \
"v_mfma_f32_16x16x32_bf16 a[12:15], a[236:239], a[192:195], a[12:15]\n" \
"s_add_u32 s88, s42, 0x1000\n" \
"buffer_store_dwordx4 v[188:191], v20, s[80:83], s88 offen offset:1024 sc1\n" \
"s_waitcnt lgkmcnt(13)\n" \
"v_mfma_f32_16x16x32_bf16 a[8:11], a[128:131], a[168:171], a[8:11]\n" \
"ds_read_b64_tr_b16 a[242:243], v36 offset:9216\n" \
"s_waitcnt lgkmcnt(12)\n" \
"v_mfma_f32_16x16x32_bf16 a[8:11], a[192:195], a[232:235], a[8:11]\n" \
"ds_read_b64_tr_b16 a[180:181], v37 offset:0\n" \
"v_mfma_f32_16x16x32_bf16 a[24:27], a[132:135], a[168:171], a[24:27]\n" \
"ds_read_b64_tr_b16 a[182:183], v37 offset:1024\n" \
"v_mfma_f32_16x16x32_bf16 a[24:27], a[196:199], a[232:235], a[24:27]\n" \
"ds_read_b64_tr_b16 a[244:245], v37 offset:8192\n" \
"v_mfma_f32_16x16x32_bf16 a[28:31], a[172:175], a[132:135], a[28:31]\n" \
"s_add_u32 s6, s26, s6\n" \
"s_addc_u32 s9, s27, 0\n" \
"v_mfma_f32_16x16x32_bf16 a[28:31], a[236:239], a[196:199], a[28:31]\n" \
"s_addk_i32 s84, 0x2000\n" \
"v_mfma_f32_16x16x32_bf16 a[20:23], a[164:167], a[132:135], a[20:23]\n" \
"buffer_load_dwordx4 v25, s[76:79], s84 offen offset:1024 lds\n" \
"v_mfma_f32_16x16x32_bf16 a[20:23], a[228:231], a[196:199], a[20:23]\n" \
"ds_read_b64_tr_b16 a[246:247], v37 offset:9216\n" \
"v_mfma_f32_16x16x32_bf16 a[16:19], a[160:163], a[132:135], a[16:19]\n" \
"ds_read_b64_tr_b16 a[184:185], v38 offset:0\n" \
"v_mfma_f32_16x16x32_bf16 a[16:19], a[224:227], a[196:199], a[16:19]\n" \
"ds_read_b64_tr_b16 a[186:187], v38 offset:1024\n" \
"v_mfma_f32_16x16x32_bf16 a[32:35], a[160:163], a[136:139], a[32:35]\n" \
"s_addk_i32 s84, 0x2000\n" \
"s_nop 0\n" \
"v_mfma_f32_16x16x32_bf16 a[32:35], a[224:227], a[200:203], a[32:35]\n" \
"buffer_load_dwordx4 v24, s[76:79], s84 offen offset:2048 lds\n" \
"v_mfma_f32_16x16x32_bf16 a[36:39], a[164:167], a[136:139], a[36:39]\n" \
"ds_read_b64_tr_b16 a[248:249], v38 offset:8192\n" \
"v_mfma_f32_16x16x32_bf16 a[36:39], a[228:231], a[200:203], a[36:39]\n" \
"ds_read_b64_tr_b16 a[250:251], v38 offset:9216\n" \
"v_mfma_f32_16x16x32_bf16 a[44:47], a[172:175], a[136:139], a[44:47]\n" \
"ds_read_b64_tr_b16 a[188:189], v39 offset:0\n" \
"v_mfma_f32_16x16x32_bf16 a[44:47], a[236:239], a[200:203], a[44:47]\n" \
"ds_read_b64_tr_b16 a[190:191], v39 offset:1024\n" \
"v_mfma_f32_16x16x32_bf16 a[40:43], a[136:139], a[168:171], a[40:43]\n" \
"s_add_u32 s89, s42, 0x2000\n" \
"buffer_store_dwordx4 v[192:195], v20, s[80:83], s89 offen offset:0 sc1\n" \
"v_mfma_f32_16x16x32_bf16 a[40:43], a[200:203], a[232:235], a[40:43]\n" \
"ds_read_b64_tr_b16 a[252:253], v39 offset:8192\n" \
"v_mfma_f32_16x16x32_bf16 a[56:59], a[140:143], a[168:171], a[56:59]\n" \
"ds_read_b64_tr_b16 a[254:255], v39 offset:9216\n" \
"v_mfma_f32_16x16x32_bf16 a[56:59], a[204:207], a[232:235], a[56:59]\n" \
"s_add_u32 s8, s6, 0x1000000\n" \
"s_addc_u32 s6, s9, 0\n" \
"v_mfma_f32_16x16x32_bf16 a[60:63], a[172:175], a[140:143], a[60:63]\n" \
"s_and_b32 s9, s6, 0xffff\n" \
"s_lshl_b32 s6, s5, 16\n" \
"v_mfma_f32_16x16x32_bf16 a[60:63], a[236:239], a[204:207], a[60:63]\n" \
"s_addk_i32 s84, 0x2000\n" \
"v_mfma_f32_16x16x32_bf16 a[52:55], a[164:167], a[140:143], a[52:55]\n" \
"buffer_load_dwordx4 v25, s[76:79], s84 offen offset:3072 lds\n" \
"v_mfma_f32_16x16x32_bf16 a[52:55], a[228:231], a[204:207], a[52:55]\n" \
"s_sub_i32 s10, 0x100000, s6\n" \
"s_mov_b32 s6, 0x8000\n" \
"v_mfma_f32_16x16x32_bf16 a[48:51], a[160:163], a[140:143], a[48:51]\n" \
"s_cmp_lg_u32 s5, 0\n" \
"s_cselect_b32 s17, s11, s6\n" \
".fill (L_pb + 32480 - .) / 4, 4, 0xBF800000\n" \
"L_pp8:\n" \
"s_cbranch_execz L_pret\n" \
"v_mfma_f32_16x16x32_bf16 a[48:51], a[224:227], a[204:207], a[48:51]\n" \
"s_lshl_b32 s6, s3, 1\n" \
"s_lshl_b32 s5, s5, 3\n" \
"s_waitcnt lgkmcnt(15)\n" \
"v_mfma_f32_16x16x32_bf16 a[64:67], a[164:167], v[248:251], a[64:67]\n" \
"s_and_b32 s6, s6, 4\n" \
"s_or_b32 s12, s5, s6\n" \
"v_mfma_f32_16x16x32_bf16 a[64:67], a[228:231], v[48:51], a[64:67]\n" \
"s_sub_i32 s5, s13, s5\n" \
"s_or_b32 s5, s5, s6\n" \
"v_mfma_f32_16x16x32_bf16 a[80:83], a[172:175], v[248:251], a[80:83]\n" \
"s_or_b32 s6, s12, s13\n" \
"s_lshl_b32 s4, s4, 2\n" \
"v_mfma_f32_16x16x32_bf16 a[80:83], a[236:239], v[48:51], a[80:83]\n" \
"s_mulk_i32 s6, 0x92\n" \
"s_add_i32 s4, s4, 18\n" \
"v_mfma_f32_16x16x32_bf16 a[68:71], v[248:251], a[168:171], a[68:71]\n" \
"s_mulk_i32 s5, 0x92\n" \
"s_add_i32 s6, s4, s6\n" \
"v_mfma_f32_16x16x32_bf16 a[68:71], v[48:51], a[232:235], a[68:71]\n" \
"s_waitcnt vmcnt(10) lgkmcnt(0)\n" \
"s_barrier\n" \
"v_mfma_f32_16x16x32_bf16 v[52:55], a[176:179], a[128:131], v[52:55]\n" \
"s_add_u32 m0, s40, 0x0\n" \
"s_add_u32 s85, s39, 0x10000\n" \
"v_mfma_f32_16x16x32_bf16 v[52:55], a[240:243], a[192:195], v[52:55]\n" \
"buffer_load_dwordx4 v20, s[80:83], s85 offen sc1 lds\n" \
"v_mfma_f32_16x16x32_bf16 v[56:59], a[180:183], a[128:131], v[56:59]\n" \
"ds_read_b64_tr_b16 a[160:161], v32 offset:32768\n" \
"s_add_i32 s5, s5, s4\n" \
"v_mfma_f32_16x16x32_bf16 v[56:59], a[244:247], a[192:195], v[56:59]\n" \
"ds_read_b64_tr_b16 a[162:163], v32 offset:33280\n" \
"s_lshl_b32 s4, s3, 11\n" \
"v_mfma_f32_16x16x32_bf16 v[60:63], a[184:187], a[128:131], v[60:63]\n" \
"s_lshl_b32 s15, s13, 12\n" \
"s_lshl_b32 s16, s19, 14\n" \
"v_mfma_f32_16x16x32_bf16 v[60:63], a[248:251], a[192:195], v[60:63]\n" \
"ds_read_b64_tr_b16 a[224:225], v32 offset:36864\n" \
"s_mul_i32 s19, s19, 0xe000\n" \
"v_mfma_f32_16x16x32_bf16 v[64:67], a[188:191], a[128:131], v[64:67]\n" \
"s_mov_b32 s11, 0x20000\n" \
"s_or_b32 s12, s4, 0xc000\n" \
"v_mfma_f32_16x16x32_bf16 v[64:67], a[252:255], a[192:195], v[64:67]\n" \
"ds_read_b128 a[128:131], v28 offset:32768\n" \
"s_lshl_b32 s4, s3, 8\n" \
"v_mfma_f32_16x16x32_bf16 v[80:83], a[188:191], a[132:135], v[80:83]\n" \
"ds_read_b128 a[192:195], v29 offset:32768\n" \
"s_or_b32 s22, s15, s16\n" \
"v_mfma_f32_16x16x32_bf16 v[80:83], a[252:255], a[196:199], v[80:83]\n" \
"s_add_u32 m0, s40, 0x1000\n" \
"s_add_u32 s85, s39, 0x11000\n" \
"v_mfma_f32_16x16x32_bf16 v[76:79], a[184:187], a[132:135], v[76:79]\n" \
"buffer_load_dwordx4 v20, s[80:83], s85 offen sc1 lds\n" \
"v_mfma_f32_16x16x32_bf16 v[76:79], a[248:251], a[196:199], v[76:79]\n" \
"ds_read_b64_tr_b16 a[226:227], v32 offset:37376\n" \
"s_or_b32 s23, s19, s15\n" \
"v_mfma_f32_16x16x32_bf16 v[72:75], a[180:183], a[132:135], v[72:75]\n" \
"ds_read_b64_tr_b16 a[164:165], v33 offset:32768\n" \
"s_lshl_b32 s60, s6, 10\n" \
"v_mfma_f32_16x16x32_bf16 v[72:75], a[244:247], a[196:199], v[72:75]\n" \
"s_lshl_b32 s61, s5, 10\n" \
"s_lshl_b32 s14, s14, 15\n" \
"s_nop 0\n" \
"v_mfma_f32_16x16x32_bf16 v[68:71], a[176:179], a[132:135], v[68:71]\n" \
"ds_read_b64_tr_b16 a[166:167], v33 offset:33280\n" \
"s_or_b32 s20, s4, 0x26c00\n" \
"v_mfma_f32_16x16x32_bf16 v[68:71], a[240:243], a[196:199], v[68:71]\n" \
"ds_read_b128 a[132:135], v28 offset:34816\n" \
"s_or_b32 s13, s22, 0x8000\n" \
"v_mfma_f32_16x16x32_bf16 v[84:87], a[176:179], a[136:139], v[84:87]\n" \
"ds_read_b128 a[196:199], v29 offset:34816\n" \
"s_add_i32 s22, s22, 0x14000\n" \
"v_mfma_f32_16x16x32_bf16 v[84:87], a[240:243], a[200:203], v[84:87]\n" \
"s_add_u32 m0, s40, 0x3000\n" \
"s_movk_i32 s84, 0x400\n" \
"v_mfma_f32_16x16x32_bf16 v[88:91], a[180:183], a[136:139], v[88:91]\n" \
"buffer_load_dwordx4 v21, s[76:79], s84 offen offset:0 lds\n" \
"v_mfma_f32_16x16x32_bf16 v[88:91], a[244:247], a[200:203], v[88:91]\n" \
"ds_read_b64_tr_b16 a[228:229], v33 offset:36864\n" \
"s_bitset1_b32 s23, 16\n" \
"v_mfma_f32_16x16x32_bf16 v[92:95], a[184:187], a[136:139], v[92:95]\n" \
"ds_read_b64_tr_b16 a[230:231], v33 offset:37376\n" \
"s_or_b32 s24, s7, 0x5be8020\n" \
"v_mfma_f32_16x16x32_bf16 v[92:95], a[248:251], a[200:203], v[92:95]\n" \
"s_add_u32 s88, s42, 0x2000\n" \
"buffer_store_dwordx4 v[216:219], v20, s[80:83], s88 offen offset:1024 sc1\n" \
"v_mfma_f32_16x16x32_bf16 v[96:99], a[188:191], a[136:139], v[96:99]\n" \
"s_and_b32 s57, s27, 0xffff\n" \
"s_mov_b32 s58, 0x6000000\n" \
"v_mfma_f32_16x16x32_bf16 v[96:99], a[252:255], a[200:203], v[96:99]\n" \
"ds_read_b128 a[136:139], v28 offset:36864\n" \
"s_mov_b32 s56, s26\n" \
"v_mfma_f32_16x16x32_bf16 v[112:115], a[188:191], a[140:143], v[112:115]\n" \
"ds_read_b128 a[200:203], v29 offset:36864\n" \
"s_mov_b32 s59, s11\n" \
"v_mfma_f32_16x16x32_bf16 v[112:115], a[252:255], a[204:207], v[112:115]\n" \
"ds_read_b64_tr_b16 a[172:173], v35 offset:32768\n" \
"s_add_i32 s60, s60, 0x3000000\n" \
"v_mfma_f32_16x16x32_bf16 v[108:111], a[184:187], a[140:143], v[108:111]\n" \
"ds_read_b64_tr_b16 a[174:175], v35 offset:33280\n" \
"s_add_i32 s61, s61, 0x411c000\n" \
"v_mfma_f32_16x16x32_bf16 v[108:111], a[248:251], a[204:207], v[108:111]\n" \
"s_sub_i32 s19, 14, s18\n" \
"s_mov_b32 s21, 0x71800000\n" \
"v_mfma_f32_16x16x32_bf16 v[104:107], a[180:183], a[140:143], v[104:107]\n" \
"s_add_u32 m0, s31, 0x0\n" \
"s_mov_b32 s84, 0x400000\n" \
"v_mfma_f32_16x16x32_bf16 v[104:107], a[244:247], a[204:207], v[104:107]\n" \
"buffer_load_dwordx4 v26, s[68:71], s84 offen offset:0 lds\n" \
"v_mfma_f32_16x16x32_bf16 v[100:103], a[176:179], a[140:143], v[100:103]\n" \
"ds_read_b64_tr_b16 a[236:237], v35 offset:36864\n" \
"s_mov_b32 s4, L_gsw-L_gtr\n" \
"v_mfma_f32_16x16x32_bf16 v[100:103], a[240:243], a[204:207], v[100:103]\n" \
"ds_read_b128 a[140:143], v28 offset:38912\n" \
"v_mfma_f32_16x16x32_bf16 v[116:119], a[176:179], a[144:147], v[116:119]\n" \
"ds_read_b128 a[204:207], v29 offset:38912\n" \
"v_mfma_f32_16x16x32_bf16 v[116:119], a[240:243], a[208:211], v[116:119]\n" \
"ds_read_b64_tr_b16 a[238:239], v35 offset:37376\n" \
"v_mfma_f32_16x16x32_bf16 v[120:123], a[180:183], a[144:147], v[120:123]\n" \
"ds_read_b64_tr_b16 a[168:169], v34 offset:32768\n" \
"v_mfma_f32_16x16x32_bf16 v[120:123], a[244:247], a[208:211], v[120:123]\n" \
"s_addk_i32 s84, 0x7c00\n" \
"v_mfma_f32_16x16x32_bf16 v[124:127], a[184:187], a[144:147], v[124:127]\n" \
"buffer_load_dwordx4 v26, s[68:71], s84 offen offset:1024 lds\n" \
"v_mfma_f32_16x16x32_bf16 v[124:127], a[248:251], a[208:211], v[124:127]\n" \
"ds_read_b64_tr_b16 a[170:171], v34 offset:33280\n" \
"v_mfma_f32_16x16x32_bf16 v[128:131], a[188:191], a[144:147], v[128:131]\n" \
"s_cmp_eq_u32 s19, 0\n" \
"s_cselect_b32 s4, L_gs7-L_gtr, s4\n" \
"v_mfma_f32_16x16x32_bf16 v[128:131], a[252:255], a[208:211], v[128:131]\n" \
"ds_read_b128 a[144:147], v28 offset:40960\n" \
"v_mfma_f32_16x16x32_bf16 v[144:147], a[188:191], a[148:151], v[144:147]\n" \
"ds_read_b128 a[208:211], v29 offset:40960\n" \
"v_mfma_f32_16x16x32_bf16 v[144:147], a[252:255], a[212:215], v[144:147]\n" \
"ds_read_b64_tr_b16 a[232:233], v34 offset:36864\n" \
"v_mfma_f32_16x16x32_bf16 v[140:143], a[184:187], a[148:151], v[140:143]\n" \
"s_addk_i32 s84, 0x7c00\n" \
"v_mfma_f32_16x16x32_bf16 v[140:143], a[248:251], a[212:215], v[140:143]\n" \
"buffer_load_dwordx4 v27, s[68:71], s84 offen offset:2048 lds\n" \
"v_mfma_f32_16x16x32_bf16 v[136:139], a[180:183], a[148:151], v[136:139]\n" \
"ds_read_b64_tr_b16 a[234:235], v34 offset:37376\n" \
"v_mfma_f32_16x16x32_bf16 v[136:139], a[244:247], a[212:215], v[136:139]\n" \
"s_cmp_eq_u32 s16, 0\n" \
"s_cselect_b32 s4, 0, s4\n" \
"v_mfma_f32_16x16x32_bf16 v[132:135], a[176:179], a[148:151], v[132:135]\n" \
"ds_read_b128 v[248:251], v22 offset:32768\n" \
"v_mfma_f32_16x16x32_bf16 v[132:135], a[240:243], a[212:215], v[132:135]\n" \
"ds_read_b128 a[148:151], v28 offset:43008\n" \
"v_mfma_f32_16x16x32_bf16 v[148:151], a[176:179], a[152:155], v[148:151]\n" \
"ds_read_b128 a[212:215], v29 offset:43008\n" \
"v_mfma_f32_16x16x32_bf16 v[148:151], a[240:243], a[216:219], v[148:151]\n" \
"s_addk_i32 s84, 0x7c00\n" \
"v_mfma_f32_16x16x32_bf16 v[152:155], a[180:183], a[152:155], v[152:155]\n" \
"buffer_load_dwordx4 v27, s[68:71], s84 offen offset:3072 lds\n" \
"v_mfma_f32_16x16x32_bf16 v[152:155], a[244:247], a[216:219], v[152:155]\n" \
"ds_read_b128 v[48:51], v23 offset:32768\n" \
"v_mfma_f32_16x16x32_bf16 v[156:159], a[184:187], a[152:155], v[156:159]\n" \
"s_getpc_b64 s[28:29]\n" \
"L_gp0_%=:\n" \
"s_add_u32 s28, s28, L_gtr-L_gp0_%=\n" \
"s_addc_u32 s29, s29, 0\n" \
"v_mfma_f32_16x16x32_bf16 v[156:159], a[248:251], a[216:219], v[156:159]\n" \
"s_add_u32 s28, s28, s4\n" \
"s_addc_u32 s29, s29, 0\n" \
"v_mfma_f32_16x16x32_bf16 v[160:163], a[188:191], a[152:155], v[160:163]\n" \
"v_mfma_f32_16x16x32_bf16 v[160:163], a[252:255], a[216:219], v[160:163]\n" \
"ds_read_b128 a[152:155], v28 offset:45056\n" \
"v_mfma_f32_16x16x32_bf16 v[176:179], a[188:191], a[156:159], v[176:179]\n" \
"ds_read_b128 a[216:219], v29 offset:45056\n" \
"v_mfma_f32_16x16x32_bf16 v[176:179], a[252:255], a[220:223], v[176:179]\n" \
"v_mfma_f32_16x16x32_bf16 v[172:175], a[184:187], a[156:159], v[172:175]\n" \
"v_mfma_f32_16x16x32_bf16 v[172:175], a[248:251], a[220:223], v[172:175]\n" \
"v_mfma_f32_16x16x32_bf16 v[168:171], a[180:183], a[156:159], v[168:171]\n" \
"v_mfma_f32_16x16x32_bf16 v[168:171], a[244:247], a[220:223], v[168:171]\n" \
"v_mfma_f32_16x16x32_bf16 v[164:167], a[176:179], a[156:159], v[164:167]\n" \
"v_mfma_f32_16x16x32_bf16 v[164:167], a[240:243], a[220:223], v[164:167]\n" \
"ds_read_b128 a[156:159], v28 offset:47104\n" \
"ds_read_b128 a[220:223], v29 offset:47104\n" \
"s_waitcnt lgkmcnt(15)\n" \
"s_nop 0\n" \
"v_mfma_f32_16x16x32_bf16 a[0:3], a[160:163], a[128:131], a[0:3]\n" \
"s_add_u32 s89, s42, 0x3000\n" \
"buffer_store_dwordx4 v[220:223], v20, s[80:83], s89 offen offset:0 sc1\n" \
"v_mfma_f32_16x16x32_bf16 a[0:3], a[224:227], a[192:195], a[0:3]\n" \
"ds_read_b64_tr_b16 a[176:177], v36 offset:32768\n" \
"v_mfma_f32_16x16x32_bf16 a[4:7], a[164:167], a[128:131], a[4:7]\n" \
"ds_read_b64_tr_b16 a[178:179], v36 offset:33792\n" \
"v_mfma_f32_16x16x32_bf16 a[4:7], a[228:231], a[192:195], a[4:7]\n" \
"ds_read_b64_tr_b16 a[240:241], v36 offset:40960\n" \
"v_mfma_f32_16x16x32_bf16 a[12:15], a[172:175], a[128:131], a[12:15]\n" \
"ds_read_b64_tr_b16 a[242:243], v36 offset:41984\n" \
"s_waitcnt lgkmcnt(15)\n" \
"v_mfma_f32_16x16x32_bf16 a[12:15], a[236:239], a[192:195], a[12:15]\n" \
"v_mfma_f32_16x16x32_bf16 a[8:11], a[128:131], a[168:171], a[8:11]\n" \
"ds_read_b64_tr_b16 a[180:181], v37 offset:32768\n" \
"s_waitcnt lgkmcnt(13)\n" \
"v_mfma_f32_16x16x32_bf16 a[8:11], a[192:195], a[232:235], a[8:11]\n" \
"v_mfma_f32_16x16x32_bf16 a[24:27], a[132:135], a[168:171], a[24:27]\n" \
"ds_read_b64_tr_b16 a[182:183], v37 offset:33792\n" \
"v_mfma_f32_16x16x32_bf16 a[24:27], a[196:199], a[232:235], a[24:27]\n" \
"ds_read_b64_tr_b16 a[244:245], v37 offset:40960\n" \
"v_mfma_f32_16x16x32_bf16 a[28:31], a[172:175], a[132:135], a[28:31]\n" \
"v_mfma_f32_16x16x32_bf16 a[28:31], a[236:239], a[196:199], a[28:31]\n" \
"ds_read_b64_tr_b16 a[246:247], v37 offset:41984\n" \
"v_mfma_f32_16x16x32_bf16 a[20:23], a[164:167], a[132:135], a[20:23]\n" \
"v_mfma_f32_16x16x32_bf16 a[20:23], a[228:231], a[196:199], a[20:23]\n" \
"ds_read_b64_tr_b16 a[184:185], v38 offset:32768\n" \
"v_mfma_f32_16x16x32_bf16 a[16:19], a[160:163], a[132:135], a[16:19]\n" \
"ds_read_b64_tr_b16 a[186:187], v38 offset:33792\n" \
"v_mfma_f32_16x16x32_bf16 a[16:19], a[224:227], a[196:199], a[16:19]\n" \
"v_mfma_f32_16x16x32_bf16 a[32:35], a[160:163], a[136:139], a[32:35]\n" \
"s_add_u32 s88, s42, 0x3000\n" \
"buffer_store_dwordx4 v[224:227], v20, s[80:83], s88 offen offset:1024 sc1\n" \
"v_mfma_f32_16x16x32_bf16 a[32:35], a[224:227], a[200:203], a[32:35]\n" \
"ds_read_b64_tr_b16 a[248:249], v38 offset:40960\n" \
"v_mfma_f32_16x16x32_bf16 a[36:39], a[164:167], a[136:139], a[36:39]\n" \
"v_lshlrev_b32_e32 v45, 3, v20\n" \
"v_add_u32_e32 v45, s30, v45\n" \
"v_mfma_f32_16x16x32_bf16 a[36:39], a[228:231], a[200:203], a[36:39]\n" \
"s_getpc_b64 s[86:87]\n" \
"L_ct0_%=:\n" \
"s_add_u32 s86, s86, L_ap0-L_ct0_%=\n" \
"s_addc_u32 s87, s87, 0\n" \
"s_add_u32 m0, s40, 0x2000\n" \
"v_mfma_f32_16x16x32_bf16 a[44:47], a[172:175], a[136:139], a[44:47]\n" \
"global_load_lds_dword v45, s[86:87]\n" \
"v_mfma_f32_16x16x32_bf16 a[44:47], a[236:239], a[200:203], a[44:47]\n" \
"ds_read_b64_tr_b16 a[250:251], v38 offset:41984\n" \
"v_mfma_f32_16x16x32_bf16 a[40:43], a[136:139], a[168:171], a[40:43]\n" \
"ds_read_b64_tr_b16 a[188:189], v39 offset:32768\n" \
"v_mfma_f32_16x16x32_bf16 a[40:43], a[200:203], a[232:235], a[40:43]\n" \
"ds_read_b64_tr_b16 a[190:191], v39 offset:33792\n" \
"v_mfma_f32_16x16x32_bf16 a[56:59], a[140:143], a[168:171], a[56:59]\n" \
"ds_read_b64_tr_b16 a[252:253], v39 offset:40960\n" \
"v_mfma_f32_16x16x32_bf16 a[56:59], a[204:207], a[232:235], a[56:59]\n" \
"ds_read_b64_tr_b16 a[254:255], v39 offset:41984\n" \
"v_mfma_f32_16x16x32_bf16 a[60:63], a[172:175], a[140:143], a[60:63]\n" \
"v_mfma_f32_16x16x32_bf16 a[60:63], a[236:239], a[204:207], a[60:63]\n" \
"v_mfma_f32_16x16x32_bf16 a[52:55], a[164:167], a[140:143], a[52:55]\n" \
"v_mfma_f32_16x16x32_bf16 a[52:55], a[228:231], a[204:207], a[52:55]\n" \
"v_mfma_f32_16x16x32_bf16 a[48:51], a[160:163], a[140:143], a[48:51]\n" \
"v_mfma_f32_16x16x32_bf16 a[48:51], a[224:227], a[204:207], a[48:51]\n" \
"s_waitcnt lgkmcnt(15)\n" \
"v_mfma_f32_16x16x32_bf16 a[64:67], a[164:167], v[248:251], a[64:67]\n" \
"v_mfma_f32_16x16x32_bf16 a[64:67], a[228:231], v[48:51], a[64:67]\n" \
"v_mfma_f32_16x16x32_bf16 a[80:83], a[172:175], v[248:251], a[80:83]\n" \
"v_mfma_f32_16x16x32_bf16 a[80:83], a[236:239], v[48:51], a[80:83]\n" \
"v_mfma_f32_16x16x32_bf16 a[68:71], v[248:251], a[168:171], a[68:71]\n" \
"v_mfma_f32_16x16x32_bf16 a[68:71], v[48:51], a[232:235], a[68:71]\n" \
"s_waitcnt vmcnt(3) lgkmcnt(0)\n" \
"s_barrier\n" \
"s_nop 0\n" \
"v_xor_b32_e32 v28, 0x20000, v28\n" \
"v_xor_b32_e32 v29, 0x20000, v29\n" \
"v_mfma_f32_16x16x32_bf16 v[52:55], a[176:179], a[128:131], v[52:55]\n" \
"ds_read_b64_tr_b16 a[160:161], v32 offset:0\n" \
"v_accvgpr_read_b32 v184, a0\n" \
"v_mfma_f32_16x16x32_bf16 v[52:55], a[240:243], a[192:195], v[52:55]\n" \
"v_accvgpr_read_b32 v185, a1\n" \
"v_accvgpr_read_b32 v186, a2\n" \
"v_mfma_f32_16x16x32_bf16 v[56:59], a[180:183], a[128:131], v[56:59]\n" \
"ds_read_b64_tr_b16 a[162:163], v32 offset:512\n" \
"v_accvgpr_read_b32 v187, a3\n" \
"v_mfma_f32_16x16x32_bf16 v[56:59], a[244:247], a[192:195], v[56:59]\n" \
"v_accvgpr_read_b32 v188, a4\n" \
"v_accvgpr_read_b32 v189, a5\n" \
"v_mfma_f32_16x16x32_bf16 v[60:63], a[184:187], a[128:131], v[60:63]\n" \
"v_accvgpr_read_b32 v190, a6\n" \
"v_accvgpr_read_b32 v191, a7\n" \
"v_mfma_f32_16x16x32_bf16 v[60:63], a[248:251], a[192:195], v[60:63]\n" \
"ds_read_b64_tr_b16 a[224:225], v32 offset:4096\n" \
"v_accvgpr_read_b32 v196, a12\n" \
"v_mfma_f32_16x16x32_bf16 v[64:67], a[188:191], a[128:131], v[64:67]\n" \
"v_accvgpr_read_b32 v197, a13\n" \
"v_accvgpr_read_b32 v198, a14\n" \
"v_mfma_f32_16x16x32_bf16 v[64:67], a[252:255], a[192:195], v[64:67]\n" \
"ds_read_b128 a[128:131], v28 offset:0\n" \
"v_accvgpr_read_b32 v199, a15\n" \
"v_mfma_f32_16x16x32_bf16 v[80:83], a[188:191], a[132:135], v[80:83]\n" \
"ds_read_b128 a[192:195], v29 offset:0\n" \
"v_accvgpr_read_b32 v192, a8\n" \
"v_mfma_f32_16x16x32_bf16 v[80:83], a[252:255], a[196:199], v[80:83]\n" \
"ds_read_b64_tr_b16 a[226:227], v32 offset:4608\n" \
"v_accvgpr_read_b32 v193, a9\n" \
"v_mfma_f32_16x16x32_bf16 v[76:79], a[184:187], a[132:135], v[76:79]\n" \
"v_accvgpr_read_b32 v194, a10\n" \
"v_accvgpr_read_b32 v195, a11\n" \
"v_mfma_f32_16x16x32_bf16 v[76:79], a[248:251], a[196:199], v[76:79]\n" \
"ds_read_b64_tr_b16 a[164:165], v33 offset:0\n" \
"v_accvgpr_read_b32 v208, a24\n" \
"v_mfma_f32_16x16x32_bf16 v[72:75], a[180:183], a[132:135], v[72:75]\n" \
"v_accvgpr_read_b32 v209, a25\n" \
"v_accvgpr_read_b32 v210, a26\n" \
"v_mfma_f32_16x16x32_bf16 v[72:75], a[244:247], a[196:199], v[72:75]\n" \
"v_accvgpr_read_b32 v211, a27\n" \
"v_accvgpr_read_b32 v212, a28\n" \
"v_mfma_f32_16x16x32_bf16 v[68:71], a[176:179], a[132:135], v[68:71]\n" \
"ds_read_b64_tr_b16 a[166:167], v33 offset:512\n" \
"v_accvgpr_read_b32 v213, a29\n" \
"v_mfma_f32_16x16x32_bf16 v[68:71], a[240:243], a[196:199], v[68:71]\n" \
"ds_read_b128 a[132:135], v28 offset:2048\n" \
"v_accvgpr_read_b32 v214, a30\n" \
"v_mfma_f32_16x16x32_bf16 v[84:87], a[176:179], a[136:139], v[84:87]\n" \
"ds_read_b128 a[196:199], v29 offset:2048\n" \
"v_accvgpr_read_b32 v215, a31\n" \
"v_mfma_f32_16x16x32_bf16 v[84:87], a[240:243], a[200:203], v[84:87]\n" \
"ds_read_b64_tr_b16 a[228:229], v33 offset:4096\n" \
"v_accvgpr_read_b32 v204, a20\n" \
"v_mfma_f32_16x16x32_bf16 v[88:91], a[180:183], a[136:139], v[88:91]\n" \
"v_accvgpr_read_b32 v205, a21\n" \
"v_accvgpr_read_b32 v206, a22\n" \
"v_mfma_f32_16x16x32_bf16 v[88:91], a[244:247], a[200:203], v[88:91]\n" \
"v_accvgpr_read_b32 v207, a23\n" \
"v_accvgpr_read_b32 v200, a16\n" \
"v_mfma_f32_16x16x32_bf16 v[92:95], a[184:187], a[136:139], v[92:95]\n" \
"ds_read_b64_tr_b16 a[230:231], v33 offset:4608\n" \
"v_accvgpr_read_b32 v201, a17\n" \
"v_mfma_f32_16x16x32_bf16 v[92:95], a[248:251], a[200:203], v[92:95]\n" \
"v_accvgpr_read_b32 v202, a18\n" \
"v_accvgpr_read_b32 v203, a19\n" \
"v_mfma_f32_16x16x32_bf16 v[96:99], a[188:191], a[136:139], v[96:99]\n" \
"v_accvgpr_read_b32 v216, a32\n" \
"v_accvgpr_read_b32 v217, a33\n" \
"v_mfma_f32_16x16x32_bf16 v[96:99], a[252:255], a[200:203], v[96:99]\n" \
"ds_read_b128 a[136:139], v28 offset:4096\n" \
"v_accvgpr_read_b32 v218, a34\n" \
"v_mfma_f32_16x16x32_bf16 v[112:115], a[188:191], a[140:143], v[112:115]\n" \
"ds_read_b128 a[200:203], v29 offset:4096\n" \
"v_accvgpr_read_b32 v219, a35\n" \
"v_mfma_f32_16x16x32_bf16 v[112:115], a[252:255], a[204:207], v[112:115]\n" \
"ds_read_b64_tr_b16 a[172:173], v35 offset:0\n" \
"v_accvgpr_read_b32 v220, a36\n" \
"v_mfma_f32_16x16x32_bf16 v[108:111], a[184:187], a[140:143], v[108:111]\n" \
"ds_read_b64_tr_b16 a[174:175], v35 offset:512\n" \
"v_accvgpr_read_b32 v221, a37\n" \
"v_mfma_f32_16x16x32_bf16 v[108:111], a[248:251], a[204:207], v[108:111]\n" \
"v_accvgpr_read_b32 v222, a38\n" \
"v_accvgpr_read_b32 v223, a39\n" \
"v_mfma_f32_16x16x32_bf16 v[104:107], a[180:183], a[140:143], v[104:107]\n" \
"ds_read_b64_tr_b16 a[236:237], v35 offset:4096\n" \
"v_accvgpr_read_b32 v228, a44\n" \
"v_mfma_f32_16x16x32_bf16 v[104:107], a[244:247], a[204:207], v[104:107]\n" \
"v_accvgpr_read_b32 v229, a45\n" \
"v_accvgpr_read_b32 v230, a46\n" \
"v_mfma_f32_16x16x32_bf16 v[100:103], a[176:179], a[140:143], v[100:103]\n" \
"v_accvgpr_read_b32 v231, a47\n" \
"v_accvgpr_read_b32 v224, a40\n" \
"v_mfma_f32_16x16x32_bf16 v[100:103], a[240:243], a[204:207], v[100:103]\n" \
"ds_read_b128 a[140:143], v28 offset:6144\n" \
"v_accvgpr_read_b32 v225, a41\n" \
"v_mfma_f32_16x16x32_bf16 v[116:119], a[176:179], a[144:147], v[116:119]\n" \
"ds_read_b128 a[204:207], v29 offset:6144\n" \
"v_accvgpr_read_b32 v226, a42\n" \
"v_mfma_f32_16x16x32_bf16 v[116:119], a[240:243], a[208:211], v[116:119]\n" \
"ds_read_b64_tr_b16 a[238:239], v35 offset:4608\n" \
"v_accvgpr_read_b32 v227, a43\n" \
"v_mfma_f32_16x16x32_bf16 v[120:123], a[180:183], a[144:147], v[120:123]\n" \
"ds_read_b64_tr_b16 a[168:169], v34 offset:0\n" \
"v_accvgpr_read_b32 v240, a56\n" \
"v_mfma_f32_16x16x32_bf16 v[120:123], a[244:247], a[208:211], v[120:123]\n" \
"v_accvgpr_read_b32 v241, a57\n" \
"v_accvgpr_read_b32 v242, a58\n" \
"v_mfma_f32_16x16x32_bf16 v[124:127], a[184:187], a[144:147], v[124:127]\n" \
"v_accvgpr_read_b32 v243, a59\n" \
"v_accvgpr_read_b32 v244, a60\n" \
"v_mfma_f32_16x16x32_bf16 v[124:127], a[248:251], a[208:211], v[124:127]\n" \
"ds_read_b64_tr_b16 a[170:171], v34 offset:512\n" \
"v_accvgpr_read_b32 v245, a61\n" \
"v_mfma_f32_16x16x32_bf16 v[128:131], a[188:191], a[144:147], v[128:131]\n" \
"v_accvgpr_read_b32 v246, a62\n" \
"v_accvgpr_read_b32 v247, a63\n" \
"v_mfma_f32_16x16x32_bf16 v[128:131], a[252:255], a[208:211], v[128:131]\n" \
"ds_read_b128 a[144:147], v28 offset:8192\n" \
"v_accvgpr_read_b32 v236, a52\n" \
"v_mfma_f32_16x16x32_bf16 v[144:147], a[188:191], a[148:151], v[144:147]\n" \
"ds_read_b128 a[208:211], v29 offset:8192\n" \
"v_accvgpr_read_b32 v237, a53\n" \
"v_mfma_f32_16x16x32_bf16 v[144:147], a[252:255], a[212:215], v[144:147]\n" \
"ds_read_b64_tr_b16 a[232:233], v34 offset:4096\n" \
"v_accvgpr_read_b32 v238, a54\n" \
"v_mfma_f32_16x16x32_bf16 v[140:143], a[184:187], a[148:151], v[140:143]\n" \
"v_accvgpr_read_b32 v239, a55\n" \
"v_accvgpr_read_b32 v232, a48\n" \
"v_mfma_f32_16x16x32_bf16 v[140:143], a[248:251], a[212:215], v[140:143]\n" \
"ds_read_b64_tr_b16 a[234:235], v34 offset:4608\n" \
"v_accvgpr_read_b32 v233, a49\n" \
"v_mfma_f32_16x16x32_bf16 v[136:139], a[180:183], a[148:151], v[136:139]\n" \
"v_accvgpr_read_b32 v234, a50\n" \
"v_accvgpr_read_b32 v235, a51\n" \
"v_mfma_f32_16x16x32_bf16 v[136:139], a[244:247], a[212:215], v[136:139]\n" \
"v_accvgpr_read_b32 v180, a64\n" \
"v_accvgpr_read_b32 v181, a65\n" \
"v_mfma_f32_16x16x32_bf16 v[132:135], a[176:179], a[148:151], v[132:135]\n" \
"ds_read_b128 v[248:251], v22 offset:0\n" \
"v_accvgpr_read_b32 v182, a66\n" \
"v_mfma_f32_16x16x32_bf16 v[132:135], a[240:243], a[212:215], v[132:135]\n" \
"ds_read_b128 a[148:151], v28 offset:10240\n" \
"v_accvgpr_read_b32 v183, a67\n" \
"v_mfma_f32_16x16x32_bf16 v[148:151], a[176:179], a[152:155], v[148:151]\n" \
"ds_read_b128 a[212:215], v29 offset:10240\n" \
"v_accvgpr_read_b32 v252, a68\n" \
"v_mfma_f32_16x16x32_bf16 v[148:151], a[240:243], a[216:219], v[148:151]\n" \
"ds_read_b128 v[48:51], v23 offset:0\n" \
"v_accvgpr_read_b32 v253, a69\n" \
"v_mfma_f32_16x16x32_bf16 v[152:155], a[180:183], a[152:155], v[152:155]\n" \
"v_accvgpr_read_b32 v254, a70\n" \
"v_accvgpr_read_b32 v255, a71\n" \
"v_mfma_f32_16x16x32_bf16 v[152:155], a[244:247], a[216:219], v[152:155]\n" \
"v_lshrrev_b32_e64 v24, 9, v20\n" \
"v_and_b32_e32 v26, 0x1ff, v20\n" \
"v_mfma_f32_16x16x32_bf16 v[156:159], a[184:187], a[152:155], v[156:159]\n" \
"v_lshl_add_u32 v24, v24, 3, v26\n" \
"v_lshrrev_b32_e64 v25, 8, v20\n" \
"v_mfma_f32_16x16x32_bf16 v[156:159], a[248:251], a[216:219], v[156:159]\n" \
"v_mul_u32_u24_e32 v25, 0x24700, v25\n" \
"v_add_u32_e64 v25, v25, v20\n" \
"v_mfma_f32_16x16x32_bf16 v[160:163], a[188:191], a[152:155], v[160:163]\n" \
"v_lshrrev_b32_e32 v26, 2, v20\n" \
"v_add_u32_e32 v26, s45, v26\n" \
"v_mfma_f32_16x16x32_bf16 v[160:163], a[252:255], a[216:219], v[160:163]\n" \
"ds_read_b128 a[152:155], v28 offset:12288\n" \
"v_add_u32_e64 v27, s46, v20\n" \
"v_mfma_f32_16x16x32_bf16 v[176:179], a[188:191], a[156:159], v[176:179]\n" \
"ds_read_b128 a[216:219], v29 offset:12288\n" \
"v_add_u32_e64 v21, s43, v27\n" \
"v_mfma_f32_16x16x32_bf16 v[176:179], a[252:255], a[220:223], v[176:179]\n" \
"v_mfma_f32_16x16x32_bf16 v[172:175], a[184:187], a[156:159], v[172:175]\n" \
"v_mfma_f32_16x16x32_bf16 v[172:175], a[248:251], a[220:223], v[172:175]\n" \
"v_mfma_f32_16x16x32_bf16 v[168:171], a[180:183], a[156:159], v[168:171]\n" \
"v_mfma_f32_16x16x32_bf16 v[168:171], a[244:247], a[220:223], v[168:171]\n" \
"v_mfma_f32_16x16x32_bf16 v[164:167], a[176:179], a[156:159], v[164:167]\n" \
"v_mfma_f32_16x16x32_bf16 v[164:167], a[240:243], a[220:223], v[164:167]\n" \
"ds_read_b128 a[156:159], v28 offset:14336\n" \
"ds_read_b128 a[220:223], v29 offset:14336\n" \
"s_waitcnt lgkmcnt(15)\n" \
"s_nop 0\n" \
"v_mfma_f32_16x16x32_bf16 v[184:187], a[160:163], a[128:131], v[184:187]\n" \
"v_bfe_u32 v44, v20, 2, 5\n" \
"ds_read_b64_tr_b16 a[176:177], v36 offset:0\n" \
"v_mfma_f32_16x16x32_bf16 v[184:187], a[224:227], a[192:195], v[184:187]\n" \
"buffer_load_dword v44, v44, s[80:83], s38 offen offset:0 sc1\n" \
"v_mfma_f32_16x16x32_bf16 v[188:191], a[164:167], a[128:131], v[188:191]\n" \
"ds_read_b64_tr_b16 a[178:179], v36 offset:1024\n" \
"v_mfma_f32_16x16x32_bf16 v[188:191], a[228:231], a[192:195], v[188:191]\n" \
"ds_read_b64_tr_b16 a[240:241], v36 offset:8192\n" \
"v_mfma_f32_16x16x32_bf16 v[196:199], a[172:175], a[128:131], v[196:199]\n" \
"ds_read_b64_tr_b16 a[242:243], v36 offset:9216\n" \
"s_waitcnt lgkmcnt(15)\n" \
"v_mfma_f32_16x16x32_bf16 v[196:199], a[236:239], a[192:195], v[196:199]\n" \
"v_mfma_f32_16x16x32_bf16 v[192:195], a[128:131], a[168:171], v[192:195]\n" \
"ds_read_b64_tr_b16 a[180:181], v37 offset:0\n" \
"s_waitcnt lgkmcnt(13)\n" \
"v_mfma_f32_16x16x32_bf16 v[192:195], a[192:195], a[232:235], v[192:195]\n" \
"v_mfma_f32_16x16x32_bf16 v[208:211], a[132:135], a[168:171], v[208:211]\n" \
"ds_read_b64_tr_b16 a[182:183], v37 offset:1024\n" \
"v_mfma_f32_16x16x32_bf16 v[208:211], a[196:199], a[232:235], v[208:211]\n" \
"ds_read_b64_tr_b16 a[244:245], v37 offset:8192\n" \
"v_mfma_f32_16x16x32_bf16 v[212:215], a[172:175], a[132:135], v[212:215]\n" \
"v_mfma_f32_16x16x32_bf16 v[212:215], a[236:239], a[196:199], v[212:215]\n" \
"ds_read_b64_tr_b16 a[246:247], v37 offset:9216\n" \
"v_cvt_pk_bf16_f32 v188, v188, v189\n" \
"v_mfma_f32_16x16x32_bf16 v[204:207], a[164:167], a[132:135], v[204:207]\n" \
"v_cvt_pk_bf16_f32 v189, v190, v191\n" \
"v_cvt_pk_bf16_f32 v190, v198, v199\n" \
"v_mfma_f32_16x16x32_bf16 v[204:207], a[228:231], a[196:199], v[204:207]\n" \
"ds_read_b64_tr_b16 a[184:185], v38 offset:0\n" \
"v_mov_b32_e32 v191, 0x3f80\n" \
"v_mfma_f32_16x16x32_bf16 v[200:203], a[160:163], a[132:135], v[200:203]\n" \
"ds_read_b64_tr_b16 a[186:187], v38 offset:1024\n" \
"v_cvt_pk_bf16_f32 v184, v184, v185\n" \
"v_mfma_f32_16x16x32_bf16 v[200:203], a[224:227], a[196:199], v[200:203]\n" \
"v_cvt_pk_bf16_f32 v185, v186, v187\n" \
"v_cvt_pk_bf16_f32 v186, v196, v197\n" \
"v_mfma_f32_16x16x32_bf16 v[216:219], a[160:163], a[136:139], v[216:219]\n" \
"ds_read_b64_tr_b16 a[248:249], v38 offset:8192\n" \
"v_mov_b32_e64 v187, 0\n" \
"v_mfma_f32_16x16x32_bf16 v[216:219], a[224:227], a[200:203], v[216:219]\n" \
"ds_read_b64_tr_b16 a[250:251], v38 offset:9216\n" \
"v_mfma_f32_16x16x32_bf16 v[220:223], a[164:167], a[136:139], v[220:223]\n" \
"v_mfma_f32_16x16x32_bf16 v[220:223], a[228:231], a[200:203], v[220:223]\n" \
"ds_read_b64_tr_b16 a[188:189], v39 offset:0\n" \
"v_cvt_pk_bf16_f32 v204, v204, v205\n" \
"v_mfma_f32_16x16x32_bf16 v[228:231], a[172:175], a[136:139], v[228:231]\n" \
"v_cvt_pk_bf16_f32 v205, v206, v207\n" \
"v_cvt_pk_bf16_f32 v206, v214, v215\n" \
"v_mfma_f32_16x16x32_bf16 v[228:231], a[236:239], a[200:203], v[228:231]\n" \
"ds_read_b64_tr_b16 a[190:191], v39 offset:1024\n" \
"v_mov_b32_e32 v207, 0x3f80\n" \
"v_mfma_f32_16x16x32_bf16 v[224:227], a[136:139], a[168:171], v[224:227]\n" \
"ds_read_b64_tr_b16 a[252:253], v39 offset:8192\n" \
"v_cvt_pk_bf16_f32 v200, v200, v201\n" \
"v_mfma_f32_16x16x32_bf16 v[224:227], a[200:203], a[232:235], v[224:227]\n" \
"v_cvt_pk_bf16_f32 v201, v202, v203\n" \
"v_cvt_pk_bf16_f32 v202, v212, v213\n" \
"v_mfma_f32_16x16x32_bf16 v[240:243], a[140:143], a[168:171], v[240:243]\n" \
"ds_read_b64_tr_b16 a[254:255], v39 offset:9216\n" \
"v_mov_b32_e64 v203, 0\n" \
"v_mfma_f32_16x16x32_bf16 v[240:243], a[204:207], a[232:235], v[240:243]\n" \
"v_mfma_f32_16x16x32_bf16 v[244:247], a[172:175], a[140:143], v[244:247]\n" \
"v_mfma_f32_16x16x32_bf16 v[244:247], a[236:239], a[204:207], v[244:247]\n" \
"v_cvt_pk_bf16_f32 v220, v220, v221\n" \
"v_cvt_pk_bf16_f32 v221, v222, v223\n" \
"v_mfma_f32_16x16x32_bf16 v[236:239], a[164:167], a[140:143], v[236:239]\n" \
"v_cvt_pk_bf16_f32 v222, v230, v231\n" \
"v_mov_b32_e32 v223, 0x3f80\n" \
"v_mfma_f32_16x16x32_bf16 v[236:239], a[228:231], a[204:207], v[236:239]\n" \
"v_cvt_pk_bf16_f32 v216, v216, v217\n" \
"v_cvt_pk_bf16_f32 v217, v218, v219\n" \
"v_mfma_f32_16x16x32_bf16 v[232:235], a[160:163], a[140:143], v[232:235]\n" \
"v_cvt_pk_bf16_f32 v218, v228, v229\n" \
"v_mov_b32_e64 v219, 0\n" \
"v_mfma_f32_16x16x32_bf16 v[232:235], a[224:227], a[204:207], v[232:235]\n" \
"s_waitcnt lgkmcnt(15)\n" \
"s_nop 0\n" \
"v_mfma_f32_16x16x32_bf16 v[180:183], a[164:167], v[248:251], v[180:183]\n" \
"v_mfma_f32_16x16x32_bf16 v[180:183], a[228:231], v[48:51], v[180:183]\n" \
"v_mfma_f32_16x16x32_bf16 a[80:83], a[172:175], v[248:251], a[80:83]\n" \
"v_mfma_f32_16x16x32_bf16 a[80:83], a[236:239], v[48:51], a[80:83]\n" \
"v_cvt_pk_bf16_f32 v236, v236, v237\n" \
"v_cvt_pk_bf16_f32 v237, v238, v239\n" \
"v_mfma_f32_16x16x32_bf16 v[252:255], v[248:251], a[168:171], v[252:255]\n" \
"v_cvt_pk_bf16_f32 v238, v246, v247\n" \
"v_mov_b32_e32 v239, 0x3f80\n" \
"v_mfma_f32_16x16x32_bf16 v[252:255], v[48:51], a[232:235], v[252:255]\n" \
"v_cvt_pk_bf16_f32 v232, v232, v233\n" \
"v_cvt_pk_bf16_f32 v233, v234, v235\n" \
"s_waitcnt vmcnt(0) lgkmcnt(0)\n" \
"s_barrier\n" \
"v_mov_b32_e32 v45, s62\n" \
"s_mov_b64 exec, 1\n" \
"buffer_store_dword v45, off, s[80:83], s37 offset:32 sc1\n" \
"s_mov_b64 exec, -1\n" \
"v_cmp_ne_u32_e32 vcc, s62, v44\n" \
"s_nop 4\n" \
"s_cbranch_vccnz L_poll160_%=\n" \
"L_pollr160_%=:\n" \
"v_mfma_f32_16x16x32_bf16 v[52:55], a[176:179], a[128:131], v[52:55]\n" \
"s_mov_b32 s73, s50\n" \
"s_sub_u32 m0, s31, 0x10000\n" \
"v_mfma_f32_16x16x32_bf16 v[52:55], a[240:243], a[192:195], v[52:55]\n" \
"buffer_load_dwordx4 v20, s[80:83], s73 offen offset:0 sc1 lds\n" \
"v_mfma_f32_16x16x32_bf16 v[56:59], a[180:183], a[128:131], v[56:59]\n" \
"s_add_u32 s74, s50, 0x4000\n" \
"s_add_u32 s75, s50, 0x8000\n" \
"v_mfma_f32_16x16x32_bf16 v[56:59], a[244:247], a[192:195], v[56:59]\n" \
"s_mov_b32 s70, 0x3e0293ee\n" \
"s_mov_b32 s76, s52\n" \
"v_mfma_f32_16x16x32_bf16 v[60:63], a[184:187], a[128:131], v[60:63]\n" \
"v_mul_f32_e32 v52, s70, v52\n" \
"v_mul_f32_e32 v53, s70, v53\n" \
"v_mfma_f32_16x16x32_bf16 v[60:63], a[248:251], a[192:195], v[60:63]\n" \
"buffer_load_dwordx4 v20, s[80:83], s73 offen offset:1024 sc1 lds\n" \
".fill (L_pb + 36576 - .) / 4, 4, 0xBF800000\n" \
"L_pp9:\n" \
"s_cbranch_execz L_pret\n" \
"v_mfma_f32_16x16x32_bf16 v[64:67], a[188:191], a[128:131], v[64:67]\n" \
"v_mul_f32_e32 v54, s70, v54\n" \
"v_mul_f32_e64 v55, s70, v55\n" \
"v_mfma_f32_16x16x32_bf16 v[64:67], a[252:255], a[192:195], v[64:67]\n" \
"v_cvt_pk_bf16_f32 v52, v52, v53\n" \
"v_mul_f32_e64 v56, s70, v56\n" \
"v_mfma_f32_16x16x32_bf16 v[80:83], a[188:191], a[132:135], v[80:83]\n" \
"v_mul_f32_e32 v60, s70, v60\n" \
"v_mul_f32_e32 v61, s70, v61\n" \
"v_mfma_f32_16x16x32_bf16 v[80:83], a[252:255], a[196:199], v[80:83]\n" \
"buffer_load_dwordx4 v20, s[80:83], s73 offen offset:2048 sc1 lds\n" \
"v_mfma_f32_16x16x32_bf16 v[76:79], a[184:187], a[132:135], v[76:79]\n" \
"v_mul_f32_e32 v62, s70, v62\n" \
"v_mul_f32_e32 v63, s70, v63\n" \
"v_mfma_f32_16x16x32_bf16 v[76:79], a[248:251], a[196:199], v[76:79]\n" \
"v_cvt_pk_bf16_f32 v60, v60, v61\n" \
"v_mul_f32_e64 v57, s70, v57\n" \
"v_mfma_f32_16x16x32_bf16 v[72:75], a[180:183], a[132:135], v[72:75]\n" \
"v_mul_f32_e32 v58, s70, v58\n" \
"v_mul_f32_e32 v59, s70, v59\n" \
"v_mfma_f32_16x16x32_bf16 v[72:75], a[244:247], a[196:199], v[72:75]\n" \
"buffer_load_dwordx4 v20, s[80:83], s73 offen offset:3072 sc1 lds\n" \
"v_mfma_f32_16x16x32_bf16 v[68:71], a[176:179], a[132:135], v[68:71]\n" \
"s_sub_u32 m0, s31, s51\n" \
"v_cvt_pk_bf16_f32 v53, v54, v55\n" \
"v_mfma_f32_16x16x32_bf16 v[68:71], a[240:243], a[196:199], v[68:71]\n" \
"v_mul_f32_e32 v64, s70, v64\n" \
"v_mul_f32_e64 v65, s70, v65\n" \
"v_mfma_f32_16x16x32_bf16 v[84:87], a[176:179], a[136:139], v[84:87]\n" \
"v_mul_f32_e32 v66, s70, v66\n" \
"v_mul_f32_e32 v67, s70, v67\n" \
"v_mfma_f32_16x16x32_bf16 v[84:87], a[240:243], a[200:203], v[84:87]\n" \
"buffer_load_dwordx4 v20, s[80:83], s74 offen offset:0 sc1 lds\n" \
"v_mfma_f32_16x16x32_bf16 v[88:91], a[180:183], a[136:139], v[88:91]\n" \
"v_cvt_pk_bf16_f32 v61, v62, v63\n" \
"v_cvt_pk_bf16_f32 v54, v56, v57\n" \
"v_mfma_f32_16x16x32_bf16 v[88:91], a[244:247], a[200:203], v[88:91]\n" \
"v_cvt_pk_bf16_f32 v55, v58, v59\n" \
"v_cvt_pk_bf16_f32 v62, v64, v65\n" \
"v_mfma_f32_16x16x32_bf16 v[92:95], a[184:187], a[136:139], v[92:95]\n" \
"v_cvt_pk_bf16_f32 v63, v66, v67\n" \
"v_mul_f32_e64 v68, s70, v68\n" \
"v_mfma_f32_16x16x32_bf16 v[92:95], a[248:251], a[200:203], v[92:95]\n" \
"buffer_load_dwordx4 v20, s[80:83], s74 offen offset:1024 sc1 lds\n" \
"v_mfma_f32_16x16x32_bf16 v[96:99], a[188:191], a[136:139], v[96:99]\n" \
"ds_write_b128 v27, v[52:55] offset:0\n" \
"v_mfma_f32_16x16x32_bf16 v[96:99], a[252:255], a[200:203], v[96:99]\n" \
"v_mul_f32_e32 v69, s70, v69\n" \
"v_mul_f32_e32 v76, s70, v76\n" \
"v_mfma_f32_16x16x32_bf16 v[112:115], a[188:191], a[140:143], v[112:115]\n" \
"v_mul_f32_e32 v77, s70, v77\n" \
"v_mul_f32_e32 v70, s70, v70\n" \
"v_mfma_f32_16x16x32_bf16 v[112:115], a[252:255], a[204:207], v[112:115]\n" \
"buffer_load_dwordx4 v20, s[80:83], s74 offen offset:2048 sc1 lds\n" \
"v_mfma_f32_16x16x32_bf16 v[108:111], a[184:187], a[140:143], v[108:111]\n" \
"ds_write_b128 v27, v[60:63] offset:1024\n" \
"v_mfma_f32_16x16x32_bf16 v[108:111], a[248:251], a[204:207], v[108:111]\n" \
"v_mul_f32_e64 v71, s70, v71\n" \
"v_cvt_pk_bf16_f32 v68, v68, v69\n" \
"v_mfma_f32_16x16x32_bf16 v[104:107], a[180:183], a[140:143], v[104:107]\n" \
"v_mul_f32_e32 v78, s70, v78\n" \
"v_mul_f32_e32 v79, s70, v79\n" \
"v_mfma_f32_16x16x32_bf16 v[104:107], a[244:247], a[204:207], v[104:107]\n" \
"buffer_load_dwordx4 v20, s[80:83], s74 offen offset:3072 sc1 lds\n" \
"v_mfma_f32_16x16x32_bf16 v[100:103], a[176:179], a[140:143], v[100:103]\n" \
"s_sub_u32 m0, s31, 0x4000\n" \
"v_cvt_pk_bf16_f32 v76, v76, v77\n" \
"v_mfma_f32_16x16x32_bf16 v[100:103], a[240:243], a[204:207], v[100:103]\n" \
"v_mul_f32_e32 v72, s70, v72\n" \
"v_mul_f32_e32 v73, s70, v73\n" \
"v_mfma_f32_16x16x32_bf16 v[116:119], a[176:179], a[144:147], v[116:119]\n" \
"v_mul_f32_e32 v74, s70, v74\n" \
"v_mul_f32_e32 v75, s70, v75\n" \
"v_mfma_f32_16x16x32_bf16 v[116:119], a[240:243], a[208:211], v[116:119]\n" \
"buffer_load_dwordx4 v20, s[80:83], s75 offen offset:0 sc1 lds\n" \
"v_mfma_f32_16x16x32_bf16 v[120:123], a[180:183], a[144:147], v[120:123]\n" \
"v_cvt_pk_bf16_f32 v69, v70, v71\n" \
"v_mul_f32_e64 v80, s70, v80\n" \
"v_mfma_f32_16x16x32_bf16 v[120:123], a[244:247], a[208:211], v[120:123]\n" \
"v_mul_f32_e32 v81, s70, v81\n" \
"v_mul_f32_e32 v82, s70, v82\n" \
"v_mfma_f32_16x16x32_bf16 v[124:127], a[184:187], a[144:147], v[124:127]\n" \
"v_mul_f32_e64 v83, s70, v83\n" \
"v_cvt_pk_bf16_f32 v77, v78, v79\n" \
"v_mfma_f32_16x16x32_bf16 v[124:127], a[248:251], a[208:211], v[124:127]\n" \
"buffer_load_dwordx4 v20, s[80:83], s75 offen offset:1024 sc1 lds\n" \
"v_mfma_f32_16x16x32_bf16 v[128:131], a[188:191], a[144:147], v[128:131]\n" \
"v_cvt_pk_bf16_f32 v70, v72, v73\n" \
"v_cvt_pk_bf16_f32 v71, v74, v75\n" \
"v_mfma_f32_16x16x32_bf16 v[128:131], a[252:255], a[208:211], v[128:131]\n" \
"v_cvt_pk_bf16_f32 v78, v80, v81\n" \
"v_cvt_pk_bf16_f32 v79, v82, v83\n" \
"v_mfma_f32_16x16x32_bf16 v[144:147], a[188:191], a[148:151], v[144:147]\n" \
"ds_write_b128 v27, v[68:71] offset:4096\n" \
"v_mfma_f32_16x16x32_bf16 v[144:147], a[252:255], a[212:215], v[144:147]\n" \
"buffer_load_dwordx4 v20, s[80:83], s75 offen offset:2048 sc1 lds\n" \
"v_mfma_f32_16x16x32_bf16 v[140:143], a[184:187], a[148:151], v[140:143]\n" \
"v_cvt_pk_bf16_f32 v180, v180, v181\n" \
"v_accvgpr_read_b32 v50, a82\n" \
"v_mfma_f32_16x16x32_bf16 v[140:143], a[248:251], a[212:215], v[140:143]\n" \
"ds_write_b128 v27, v[76:79] offset:5120\n" \
"v_mfma_f32_16x16x32_bf16 v[136:139], a[180:183], a[148:151], v[136:139]\n" \
"v_accvgpr_read_b32 v51, a83\n" \
"v_cvt_pk_bf16_f32 v181, v182, v183\n" \
"v_mfma_f32_16x16x32_bf16 v[136:139], a[244:247], a[212:215], v[136:139]\n" \
"buffer_load_dwordx4 v20, s[80:83], s75 offen offset:3072 sc1 lds\n" \
"v_mfma_f32_16x16x32_bf16 v[132:135], a[176:179], a[148:151], v[132:135]\n" \
"s_mov_b32 m0, s53\n" \
"v_cvt_pk_bf16_f32 v182, v50, v51\n" \
"v_mfma_f32_16x16x32_bf16 v[132:135], a[240:243], a[212:215], v[132:135]\n" \
"v_mov_b32_e32 v183, 0x3f80\n" \
"v_cvt_pk_bf16_f32 v234, v244, v245\n" \
"v_mfma_f32_16x16x32_bf16 v[148:151], a[176:179], a[152:155], v[148:151]\n" \
"v_mov_b32_e32 v235, 0\n" \
"v_cvt_pk_bf16_f32 v54, v192, v193\n" \
"v_mfma_f32_16x16x32_bf16 v[148:151], a[240:243], a[216:219], v[148:151]\n" \
"buffer_load_dwordx4 v20, s[80:83], s76 offen offset:0 sc1 lds\n" \
"v_mfma_f32_16x16x32_bf16 v[152:155], a[180:183], a[152:155], v[152:155]\n" \
"v_cvt_pk_bf16_f32 v55, v194, v195\n" \
"v_cvt_pk_bf16_f32 v56, v208, v209\n" \
"v_mfma_f32_16x16x32_bf16 v[152:155], a[244:247], a[216:219], v[152:155]\n" \
"v_cvt_pk_bf16_f32 v57, v210, v211\n" \
"v_cvt_pk_bf16_f32 v58, v224, v225\n" \
"v_cvt_pk_bf16_f32 v59, v226, v227\n" \
"v_cvt_pk_bf16_f32 v60, v240, v241\n" \
"v_cvt_pk_bf16_f32 v61, v242, v243\n" \
"v_mfma_f32_16x16x32_bf16 v[32:35], v[180:183], v[184:187], 0\n" \
"buffer_load_dwordx4 v20, s[80:83], s76 offen offset:1024 sc1 lds\n" \
"v_mfma_f32_16x16x32_bf16 v[156:159], a[184:187], a[152:155], v[156:159]\n" \
"v_mfma_f32_16x16x32_bf16 v[192:195], v[188:191], v[184:187], 0\n" \
"v_mfma_f32_16x16x32_bf16 v[208:211], v[204:207], v[200:203], 0\n" \
"v_mfma_f32_16x16x32_bf16 v[36:39], v[188:191], v[200:203], 0\n" \
"v_mfma_f32_16x16x32_bf16 v[224:227], v[220:223], v[216:219], 0\n" \
"v_mfma_f32_16x16x32_bf16 v[248:251], v[204:207], v[216:219], 0\n" \
"v_mfma_f32_16x16x32_bf16 v[240:243], v[236:239], v[232:235], 0\n" \
"v_mfma_f32_16x16x32_bf16 v[64:67], v[220:223], v[232:235], 0\n" \
"s_mov_b32 s64, 0xaaaaaaaa\n" \
"s_mov_b32 s65, 0xaaaaaaaa\n" \
"v_mfma_f32_16x16x32_bf16 v[156:159], a[248:251], a[216:219], v[156:159]\n" \
"v_cndmask_b32_e64 v32, v32, v33, s[64:65]\n" \
"v_cndmask_b32_e64 v34, v34, v35, s[64:65]\n" \
"v_mfma_f32_16x16x32_bf16 v[160:163], a[188:191], a[152:155], v[160:163]\n" \
"s_mov_b32 s66, 0xfff0ffff\n" \
"s_mov_b32 s67, 0xf000ff00\n" \
"s_mov_b32 s68, 0xffc0fffc\n" \
"s_mov_b32 s69, 0xc000fc00\n" \
"v_cndmask_b32_e64 v192, v192, v193, s[64:65]\n" \
"v_add_f32_e64 v32, s47, v32\n" \
"v_cndmask_b32_e64 v194, v194, v195, s[64:65]\n" \
"v_add_f32_e64 v34, s47, v34\n" \
"v_cndmask_b32_e64 v208, v208, v209, s[64:65]\n" \
"v_cndmask_b32_e64 v36, v36, v37, s[64:65]\n" \
"v_cndmask_b32_e64 v210, v210, v211, s[64:65]\n" \
"v_cndmask_b32_e64 v38, v38, v39, s[64:65]\n" \
"v_cndmask_b32_e64 v224, v224, v225, s[64:65]\n" \
"v_cndmask_b32_e64 v248, v248, v249, s[64:65]\n" \
"v_cndmask_b32_e64 v226, v226, v227, s[64:65]\n" \
"v_cndmask_b32_e64 v250, v250, v251, s[64:65]\n" \
"v_cndmask_b32_e64 v240, v240, v241, s[64:65]\n" \
"v_cndmask_b32_e64 v64, v64, v65, s[64:65]\n" \
"v_cndmask_b32_e64 v242, v242, v243, s[64:65]\n" \
"v_cndmask_b32_e64 v66, v66, v67, s[64:65]\n" \
"v_cndmask_b32_e64 v192, v32, v192, s[66:67]\n" \
"v_cndmask_b32_e64 v194, v34, v194, s[68:69]\n" \
"v_cndmask_b32_e64 v208, v36, v208, s[66:67]\n" \
"v_cndmask_b32_e64 v210, v38, v210, s[68:69]\n" \
"v_cndmask_b32_e64 v224, v248, v224, s[66:67]\n" \
"v_cndmask_b32_e64 v226, v250, v226, s[68:69]\n" \
"v_cndmask_b32_e64 v240, v64, v240, s[66:67]\n" \
"v_cndmask_b32_e64 v242, v66, v242, s[68:69]\n" \
"v_mfma_f32_16x16x32_bf16 v[160:163], a[252:255], a[216:219], v[160:163]\n" \
"v_max_f32_e32 v193, v192, v194\n" \
"v_max_f32_e32 v209, v208, v210\n" \
"v_max_f32_e32 v225, v224, v226\n" \
"v_max_f32_e32 v241, v240, v242\n" \
"v_mul_f32_e32 v84, s70, v84\n" \
"v_permlane32_swap_b32_e32 v193, v225\n" \
"v_permlane32_swap_b32_e32 v209, v241\n" \
"v_mul_f32_e32 v85, s70, v85\n" \
"v_mul_f32_e32 v92, s70, v92\n" \
"v_mul_f32_e32 v93, s70, v93\n" \
"v_permlane32_swap_b32_e32 v196, v228\n" \
"v_permlane32_swap_b32_e32 v212, v244\n" \
"v_max_f32_e32 v193, v193, v225\n" \
"v_max_f32_e32 v209, v209, v241\n" \
"v_accvgpr_read_b32 v48, a80\n" \
"v_accvgpr_read_b32 v49, a81\n" \
"v_mul_f32_e32 v86, s70, v86\n" \
"v_mul_f32_e32 v87, s70, v87\n" \
"v_cvt_pk_bf16_f32 v84, v84, v85\n" \
"v_mul_f32_e32 v94, s70, v94\n" \
"v_mul_f32_e32 v95, s70, v95\n" \
"v_cvt_pk_bf16_f32 v92, v92, v93\n" \
"v_permlane16_swap_b32_e32 v228, v244\n" \
"v_permlane16_swap_b32_e32 v193, v209\n" \
"v_mfma_f32_16x16x32_bf16 v[176:179], a[188:191], a[156:159], v[176:179]\n" \
"s_mov_b32 s72, 0x3eae1a93\n" \
"v_mul_f32_e32 v88, s70, v88\n" \
"v_mul_f32_e32 v89, s70, v89\n" \
"v_mul_f32_e32 v90, s70, v90\n" \
"v_mul_f32_e32 v91, s70, v91\n" \
"v_cvt_pk_bf16_f32 v85, v86, v87\n" \
"v_mul_f32_e32 v96, s70, v96\n" \
"v_mul_f32_e32 v97, s70, v97\n" \
"v_mul_f32_e32 v98, s70, v98\n" \
"v_mul_f32_e32 v99, s70, v99\n" \
"v_cvt_pk_bf16_f32 v93, v94, v95\n" \
"v_mul_f32_e32 v45, 0xbfb8aa3b, v228\n" \
"v_mfma_f32_16x16x16_bf16 v[48:51], v[182:183], v[46:47], 0\n" \
"v_mfma_f32_16x16x16_bf16 v[196:199], v[190:191], v[46:47], 0\n" \
"v_mfma_f32_16x16x16_bf16 v[212:215], v[206:207], v[46:47], 0\n" \
"v_max_f32_e64 v193, v193, v209\n" \
"v_cvt_pk_bf16_f32 v86, v88, v89\n" \
"v_cvt_pk_bf16_f32 v87, v90, v91\n" \
"v_cvt_pk_bf16_f32 v94, v96, v97\n" \
"v_cvt_pk_bf16_f32 v95, v98, v99\n" \
"v_mfma_f32_16x16x16_bf16 v[228:231], v[222:223], v[46:47], 0\n" \
"v_mul_f32_e32 v193, s72, v193\n" \
"v_mov_b32_e32 v209, v193\n" \
"ds_write_b128 v27, v[84:87] offset:8192\n" \
"v_mfma_f32_16x16x16_bf16 v[244:247], v[238:239], v[46:47], 0\n" \
"v_permlane16_swap_b32_e32 v193, v209\n" \
"s_mov_b32 s71, 0x3e0293ee\n" \
"v_mfma_f32_16x16x32_bf16 v[176:179], a[252:255], a[220:223], v[176:179]\n" \
"v_mov_b32_e32 v225, v193\n" \
"v_mov_b32_e32 v241, v209\n" \
"v_mul_f32_e32 v100, s70, v100\n" \
"v_mul_f32_e64 v101, s70, v101\n" \
"ds_write_b128 v27, v[92:95] offset:9216\n" \
"v_mul_f32_e32 v108, s70, v108\n" \
"v_mul_f32_e32 v109, s70, v109\n" \
"v_pk_mul_f32 v[116:117], v[116:117], s[70:71]\n" \
"v_pk_mul_f32 v[124:125], v[124:125], s[70:71]\n" \
"v_pk_mul_f32 v[132:133], v[132:133], s[70:71]\n" \
"v_pk_mul_f32 v[140:141], v[140:141], s[70:71]\n" \
"v_pk_mul_f32 v[148:149], v[148:149], s[70:71]\n" \
"v_pk_mul_f32 v[156:157], v[156:157], s[70:71]\n" \
"v_permlane32_swap_b32_e32 v193, v225\n" \
"v_permlane32_swap_b32_e32 v209, v241\n" \
"v_mul_f32_e32 v102, s70, v102\n" \
"v_mul_f32_e32 v103, s70, v103\n" \
"v_cvt_pk_bf16_f32 v100, v100, v101\n" \
"v_mul_f32_e32 v110, s70, v110\n" \
"v_mul_f32_e32 v111, s70, v111\n" \
"v_cvt_pk_bf16_f32 v108, v108, v109\n" \
"v_pk_mul_f32 v[118:119], v[118:119], s[70:71]\n" \
"v_cvt_pk_bf16_f32 v116, v116, v117\n" \
"v_pk_mul_f32 v[126:127], v[126:127], s[70:71]\n" \
"v_cvt_pk_bf16_f32 v124, v124, v125\n" \
"v_pk_mul_f32 v[134:135], v[134:135], s[70:71]\n" \
"v_cvt_pk_bf16_f32 v132, v132, v133\n" \
"v_pk_mul_f32 v[142:143], v[142:143], s[70:71]\n" \
"v_cvt_pk_bf16_f32 v140, v140, v141\n" \
"v_pk_mul_f32 v[150:151], v[150:151], s[70:71]\n" \
"v_cvt_pk_bf16_f32 v148, v148, v149\n" \
"v_pk_mul_f32 v[158:159], v[158:159], s[70:71]\n" \
"v_cvt_pk_bf16_f32 v156, v156, v157\n" \
"v_fma_f32 v192, v192, s72, -v193\n" \
"v_fma_f32 v194, v194, s72, -v193\n" \
"v_fma_f32 v208, v208, s72, -v209\n" \
"v_fma_f32 v210, v210, s72, -v209\n" \
"v_mul_f32_e32 v104, s70, v104\n" \
"v_mul_f32_e32 v105, s70, v105\n" \
"v_mul_f32_e32 v106, s70, v106\n" \
"v_mul_f32_e32 v107, s70, v107\n" \
"v_cvt_pk_bf16_f32 v101, v102, v103\n" \
"v_mul_f32_e32 v112, s70, v112\n" \
"v_mul_f32_e32 v113, s70, v113\n" \
"v_mul_f32_e32 v114, s70, v114\n" \
"v_mul_f32_e32 v115, s70, v115\n" \
"v_cvt_pk_bf16_f32 v109, v110, v111\n" \
"v_pk_mul_f32 v[120:121], v[120:121], s[70:71]\n" \
"v_pk_mul_f32 v[122:123], v[122:123], s[70:71]\n" \
"v_cvt_pk_bf16_f32 v117, v118, v119\n" \
"v_pk_mul_f32 v[128:129], v[128:129], s[70:71]\n" \
"v_pk_mul_f32 v[130:131], v[130:131], s[70:71]\n" \
"v_cvt_pk_bf16_f32 v125, v126, v127\n" \
"v_pk_mul_f32 v[136:137], v[136:137], s[70:71]\n" \
"v_pk_mul_f32 v[138:139], v[138:139], s[70:71]\n" \
"v_cvt_pk_bf16_f32 v133, v134, v135\n" \
"v_pk_mul_f32 v[144:145], v[144:145], s[70:71]\n" \
"v_pk_mul_f32 v[146:147], v[146:147], s[70:71]\n" \
"v_cvt_pk_bf16_f32 v141, v142, v143\n" \
"v_pk_mul_f32 v[152:153], v[152:153], s[70:71]\n" \
"v_pk_mul_f32 v[154:155], v[154:155], s[70:71]\n" \
"v_cvt_pk_bf16_f32 v149, v150, v151\n" \
"v_pk_mul_f32 v[160:161], v[160:161], s[70:71]\n" \
"v_pk_mul_f32 v[162:163], v[162:163], s[70:71]\n" \
"v_cvt_pk_bf16_f32 v157, v158, v159\n" \
"v_pk_mul_f32 v[176:177], v[176:177], s[70:71]\n" \
"v_pk_mul_f32 v[178:179], v[178:179], s[70:71]\n" \
"v_exp_f32_e32 v192, v192\n" \
"v_exp_f32_e32 v194, v194\n" \
"v_exp_f32_e32 v208, v208\n" \
"v_exp_f32_e32 v210, v210\n" \
"v_fma_f32 v224, v224, s72, -v225\n" \
"v_fma_f32 v226, v226, s72, -v225\n" \
"v_mfma_f32_16x16x32_bf16 v[172:175], a[184:187], a[156:159], v[172:175]\n" \
"v_cvt_pk_bf16_f32 v102, v104, v105\n" \
"v_cvt_pk_bf16_f32 v103, v106, v107\n" \
"v_cvt_pk_bf16_f32 v110, v112, v113\n" \
"v_cvt_pk_bf16_f32 v111, v114, v115\n" \
"v_cvt_pk_bf16_f32 v118, v120, v121\n" \
"v_cvt_pk_bf16_f32 v119, v122, v123\n" \
"v_cvt_pk_bf16_f32 v126, v128, v129\n" \
"v_cvt_pk_bf16_f32 v127, v130, v131\n" \
"v_cvt_pk_bf16_f32 v134, v136, v137\n" \
"v_cvt_pk_bf16_f32 v135, v138, v139\n" \
"v_cvt_pk_bf16_f32 v142, v144, v145\n" \
"v_cvt_pk_bf16_f32 v143, v146, v147\n" \
"v_cvt_pk_bf16_f32 v150, v152, v153\n" \
"v_cvt_pk_bf16_f32 v151, v154, v155\n" \
"v_cvt_pk_bf16_f32 v158, v160, v161\n" \
"v_cvt_pk_bf16_f32 v159, v162, v163\n" \
"v_exp_f32_e32 v224, v224\n" \
"v_exp_f32_e32 v226, v226\n" \
"v_fma_f32 v240, v240, s72, -v241\n" \
"v_fma_f32 v242, v242, s72, -v241\n" \
"ds_write_b128 v27, v[100:103] offset:12288\n" \
"v_cvt_pk_bf16_f32 v192, v192, v194\n" \
"v_cvt_pk_bf16_f32 v208, v208, v210\n" \
"v_exp_f32_e32 v240, v240\n" \
"v_exp_f32_e32 v242, v242\n" \
"v_cvt_pk_bf16_f32 v68, v48, v49\n" \
"v_cvt_pk_bf16_f32 v69, v50, v51\n" \
"v_cvt_pk_bf16_f32 v70, v196, v197\n" \
"v_cvt_pk_bf16_f32 v71, v198, v199\n" \
"ds_write_b128 v27, v[108:111] offset:13312\n" \
"v_cvt_pk_bf16_f32 v72, v212, v213\n" \
"v_cvt_pk_bf16_f32 v73, v214, v215\n" \
"v_perm_b32 v184, v192, v192, v40\n" \
"v_perm_b32 v185, v192, v192, v41\n" \
"v_perm_b32 v186, v192, v192, v42\n" \
"v_perm_b32 v187, v192, v192, v43\n" \
"v_perm_b32 v200, v208, v208, v40\n" \
"v_perm_b32 v201, v208, v208, v41\n" \
"v_perm_b32 v202, v208, v208, v42\n" \
"v_perm_b32 v203, v208, v208, v43\n" \
"ds_write_b128 v21, v[116:119] offset:16384\n" \
"v_cvt_pk_bf16_f32 v224, v224, v226\n" \
"v_cvt_pk_bf16_f32 v74, v228, v229\n" \
"v_cvt_pk_bf16_f32 v75, v230, v231\n" \
"v_mfma_f32_16x16x32_bf16 v[32:35], v[68:71], v[184:187], 0\n" \
"v_mfma_f32_16x16x32_bf16 v[36:39], v[70:73], v[200:203], 0\n" \
"v_perm_b32 v216, v224, v224, v40\n" \
"v_perm_b32 v217, v224, v224, v41\n" \
"ds_write_b128 v21, v[124:127] offset:17408\n" \
"v_perm_b32 v218, v224, v224, v42\n" \
"v_perm_b32 v219, v224, v224, v43\n" \
"v_cvt_pk_bf16_f32 v240, v240, v242\n" \
"v_mfma_f32_16x16x32_bf16 v[172:175], a[248:251], a[220:223], v[172:175]\n" \
"v_cvt_pk_bf16_f32 v76, v244, v245\n" \
"v_cvt_pk_bf16_f32 v77, v246, v247\n" \
"v_mfma_f32_16x16x32_bf16 v[248:251], v[72:75], v[216:219], 0\n" \
"v_perm_b32 v232, v240, v240, v40\n" \
"v_perm_b32 v233, v240, v240, v41\n" \
"ds_write_b128 v21, v[132:135] offset:20480\n" \
"v_perm_b32 v234, v240, v240, v42\n" \
"v_perm_b32 v235, v240, v240, v43\n" \
"v_pk_mul_f32 v[172:173], v[172:173], s[70:71]\n" \
"v_pk_mul_f32 v[174:175], v[174:175], s[70:71]\n" \
"v_cvt_pk_bf16_f32 v172, v172, v173\n" \
"v_cvt_pk_bf16_f32 v173, v174, v175\n" \
"v_cvt_pk_bf16_f32 v174, v176, v177\n" \
"v_cvt_pk_bf16_f32 v175, v178, v179\n" \
"v_mfma_f32_16x16x32_bf16 v[64:67], v[74:77], v[232:235], 0\n" \
"ds_write_b128 v21, v[140:143] offset:21504\n" \
"v_mfma_f32_16x16x32_bf16 v[168:171], a[180:183], a[156:159], v[168:171]\n" \
"v_cvt_pk_bf16_f32 v52, v252, v253\n" \
"v_cvt_pk_bf16_f32 v53, v254, v255\n" \
"v_mfma_f32_16x16x32_bf16 v[168:171], a[244:247], a[220:223], v[168:171]\n" \
"v_exp_f32_e64 v45, v45\n" \
"v_mfma_f32_16x16x32_bf16 v[192:195], v[52:55], v[184:187], 0\n" \
"ds_write_b128 v21, v[148:151] offset:24576\n" \
"v_mfma_f32_16x16x32_bf16 v[208:211], v[54:57], v[200:203], 0\n" \
"v_mfma_f32_16x16x32_bf16 v[224:227], v[56:59], v[216:219], 0\n" \
"v_mfma_f32_16x16x32_bf16 v[240:243], v[58:61], v[232:235], 0\n" \
"ds_write_b128 v21, v[156:159] offset:25600\n" \
"v_mov_b32_dpp v34, v38 quad_perm:[0,1,2,3] row_mask:0x2 bank_mask:0xf\n" \
"v_pk_mul_f32 v[168:169], v[168:169], s[70:71]\n" \
"v_pk_mul_f32 v[170:171], v[170:171], s[70:71]\n" \
"v_mov_b32_dpp v34, v250 quad_perm:[0,1,2,3] row_mask:0x4 bank_mask:0xf\n" \
"v_add_f32_e64 v45, 1.0, v45\n" \
"v_mfma_f32_16x16x32_bf16 v[164:167], a[176:179], a[156:159], v[164:167]\n" \
"v_mov_b32_dpp v34, v66 quad_perm:[0,1,2,3] row_mask:0x8 bank_mask:0xf\n" \
"v_mov_b32_dpp v32, v36 quad_perm:[0,1,2,3] row_mask:0x2 bank_mask:0xf\n" \
"v_rcp_f32_e64 v45, v45\n" \
"ds_write_b128 v21, v[172:175] offset:29696\n" \
"v_mov_b32_dpp v33, v37 quad_perm:[0,1,2,3] row_mask:0x2 bank_mask:0xf\n" \
"v_rcp_f32_e64 v34, v34\n" \
"v_mov_b32_dpp v32, v248 quad_perm:[0,1,2,3] row_mask:0x4 bank_mask:0xf\n" \
"v_mov_b32_dpp v33, v249 quad_perm:[0,1,2,3] row_mask:0x4 bank_mask:0xf\n" \
"v_mul_f32_e64 v34, v45, v34\n" \
"v_mov_b32_dpp v32, v64 quad_perm:[0,1,2,3] row_mask:0x8 bank_mask:0xf\n" \
"v_mov_b32_dpp v33, v65 quad_perm:[0,1,2,3] row_mask:0x8 bank_mask:0xf\n" \
"v_mul_f32_e32 v32, v34, v32\n" \
"v_mul_f32_e32 v33, v34, v33\n" \
"v_mov_b32_e64 v38, v34\n" \
"v_mfma_f32_16x16x32_bf16 v[164:167], a[240:243], a[220:223], v[164:167]\n" \
"v_sub_f32_e32 v44, 1.0, v45\n" \
"v_permlane16_swap_b32_e32 v34, v38\n" \
"v_mov_b32_e32 v250, v34\n" \
"v_mov_b32_e32 v66, v38\n" \
"v_cvt_pk_bf16_f32 v32, v32, v33\n" \
"v_permlane32_swap_b32_e32 v34, v250\n" \
"v_permlane32_swap_b32_e32 v38, v66\n" \
"v_mul_f32_e64 v192, v34, v192\n" \
"v_pk_mul_f32 v[164:165], v[164:165], s[70:71]\n" \
"v_pk_mul_f32 v[166:167], v[166:167], s[70:71]\n" \
"v_cvt_pk_bf16_f32 v164, v164, v165\n" \
"v_cvt_pk_bf16_f32 v165, v166, v167\n" \
"v_cvt_pk_bf16_f32 v166, v168, v169\n" \
"v_cvt_pk_bf16_f32 v167, v170, v171\n" \
"ds_write_b128 v21, v[164:167] offset:28672\n" \
"v_mul_f32_e32 v193, v34, v193\n" \
"v_mul_f32_e32 v208, v38, v208\n" \
"v_mul_f32_e32 v209, v38, v209\n" \
"v_mul_f32_e32 v224, v250, v224\n" \
"v_mul_f32_e32 v225, v250, v225\n" \
"v_mul_f32_e32 v240, v66, v240\n" \
"v_mul_f32_e32 v241, v66, v241\n" \
"v_mul_f32_e32 v194, v34, v194\n" \
"v_mul_f32_e64 v195, v34, v195\n" \
"v_cvt_pk_bf16_f32 v192, v192, v193\n" \
"v_mul_f32_e32 v210, v38, v210\n" \
"v_mul_f32_e32 v211, v38, v211\n" \
"v_cvt_pk_bf16_f32 v208, v208, v209\n" \
"v_mul_f32_e32 v226, v250, v226\n" \
"v_mul_f32_e32 v227, v250, v227\n" \
"v_cvt_pk_bf16_f32 v224, v224, v225\n" \
"v_mul_f32_e32 v242, v66, v242\n" \
"v_mul_f32_e32 v243, v66, v243\n" \
"v_cvt_pk_bf16_f32 v240, v240, v241\n" \
"v_cvt_pk_bf16_f32 v193, v194, v195\n" \
"s_add_u32 s84, s48, 0x0\n" \
"s_nop 0\n" \
"v_cvt_pk_bf16_f32 v209, v210, v211\n" \
"s_add_u32 s85, s48, 0x24800\n" \
"v_cvt_pk_bf16_f32 v225, v226, v227\n" \
"s_add_u32 s86, s48, 0x49000\n" \
"v_cvt_pk_bf16_f32 v241, v242, v243\n" \
"s_add_u32 s87, s48, 0x6d800\n" \
"ds_write_b32 v26, v44\n" \
"buffer_store_dword v32, v25, s[80:83], s49 offen sc1\n" \
"buffer_store_dwordx2 v[192:193], v24, s[80:83], s84 offen sc1\n" \
"buffer_store_dwordx2 v[208:209], v24, s[80:83], s85 offen sc1\n" \
"buffer_store_dwordx2 v[224:225], v24, s[80:83], s86 offen sc1\n" \
"buffer_store_dwordx2 v[240:241], v24, s[80:83], s87 offen sc1\n" \
"s_branch L_end_%=\n" \
"L_poll60_%=:\n" \
"s_mov_b32 s84, 0\n" \
"L_pl60_%=:\n" \
"s_sleep 1\n" \
"v_bfe_u32 v44, v20, 2, 5\n" \
"buffer_load_dword v44, v44, s[80:83], s38 offen offset:128 sc1\n" \
"s_waitcnt vmcnt(0)\n" \
"v_cmp_ne_u32_e32 vcc, s62, v44\n" \
"s_add_u32 s84, s84, 1\n" \
"s_nop 3\n" \
"s_cbranch_vccz L_pollr60_%=\n" \
"s_cmp_gt_u32 s84, 0x400000\n" \
"s_cbranch_scc0 L_pl60_%=\n" \
"s_branch L_pollr60_%=\n" \
"L_poll160_%=:\n" \
"s_mov_b32 s84, 0\n" \
"L_pl160_%=:\n" \
"s_sleep 1\n" \
"v_bfe_u32 v44, v20, 2, 5\n" \
"buffer_load_dword v44, v44, s[80:83], s38 offen offset:0 sc1\n" \
"s_waitcnt vmcnt(0)\n" \
"v_cmp_ne_u32_e32 vcc, s62, v44\n" \
"s_add_u32 s84, s84, 1\n" \
"s_nop 3\n" \
"s_cbranch_vccz L_pollr160_%=\n" \
"s_cmp_gt_u32 s84, 0x400000\n" \
"s_cbranch_scc0 L_pl160_%=\n" \
"s_branch L_pollr160_%=\n" \
".fill (L_pb + 42548 - .) / 4, 4, 0xBF800000\n" \
"L_w1_%=:\n" \
"s_waitcnt vmcnt(19)\n" \
"s_barrier\n" \
"s_nop 0\n" \
"ds_read_b64_tr_b16 a[160:161], v32 offset:0\n" \
"ds_read_b64_tr_b16 a[162:163], v32 offset:1024\n" \
"ds_read_b128 a[128:131], v28 offset:16384\n" \
"ds_read_b64_tr_b16 a[224:225], v32 offset:8192\n" \
"ds_read_b64_tr_b16 a[226:227], v32 offset:9216\n" \
"ds_read_b128 a[192:195], v29 offset:16384\n" \
"ds_read_b128 a[132:135], v28 offset:18432\n" \
"ds_read_b128 a[196:199], v29 offset:18432\n" \
"ds_read_b64_tr_b16 a[164:165], v33 offset:0\n" \
"ds_read_b64_tr_b16 a[166:167], v33 offset:1024\n" \
"ds_read_b64_tr_b16 a[228:229], v33 offset:8192\n" \
"ds_read_b64_tr_b16 a[230:231], v33 offset:9216\n" \
"ds_read_b128 a[136:139], v28 offset:20480\n" \
"ds_read_b128 a[200:203], v29 offset:20480\n" \
"s_waitcnt lgkmcnt(11)\n" \
"v_mfma_f32_16x16x32_bf16 a[0:3], a[128:131], a[160:163], 0\n" \
"ds_read_b64_tr_b16 a[168:169], v34 offset:0\n" \
"s_lshr_b32 s4, s2, 1\n" \
"s_waitcnt lgkmcnt(9)\n" \
"v_mfma_f32_16x16x32_bf16 a[0:3], a[192:195], a[224:227], a[0:3]\n" \
"ds_read_b64_tr_b16 a[170:171], v34 offset:1024\n" \
"s_and_b32 s4, s4, 0x7c\n" \
"s_waitcnt lgkmcnt(9)\n" \
"v_mfma_f32_16x16x32_bf16 a[16:19], a[132:135], a[160:163], 0\n" \
"ds_read_b64_tr_b16 a[232:233], v34 offset:8192\n" \
"v_mov_b32_e32 v213, s13\n" \
"s_waitcnt lgkmcnt(9)\n" \
"v_mfma_f32_16x16x32_bf16 a[16:19], a[196:199], a[224:227], a[16:19]\n" \
"ds_read_b64_tr_b16 a[234:235], v34 offset:9216\n" \
"s_or_b32 s4, s4, s3\n" \
"s_waitcnt lgkmcnt(8)\n" \
"v_mfma_f32_16x16x32_bf16 a[20:23], a[132:135], a[164:167], 0\n" \
"ds_read_b128 a[140:143], v28 offset:22528\n" \
"v_lshrrev_b32_e32 v212, 4, v20\n" \
"s_waitcnt lgkmcnt(7)\n" \
"v_mfma_f32_16x16x32_bf16 a[20:23], a[196:199], a[228:231], a[20:23]\n" \
"ds_read_b128 a[204:207], v29 offset:22528\n" \
"v_mad_u64_u32 v[214:215], s[4:5], s4, 36, v[212:213]\n" \
"v_mfma_f32_16x16x32_bf16 a[4:7], a[128:131], a[164:167], 0\n" \
"ds_read_b64_tr_b16 a[172:173], v35 offset:0\n" \
"v_mov_b32_e64 v217, s9\n" \
"v_mfma_f32_16x16x32_bf16 a[4:7], a[192:195], a[228:231], a[4:7]\n" \
"ds_read_b64_tr_b16 a[174:175], v35 offset:1024\n" \
"s_movk_i32 s4, 0x240\n" \
"s_waitcnt lgkmcnt(9)\n" \
"v_mfma_f32_16x16x32_bf16 a[32:35], a[136:139], a[160:163], 0\n" \
"ds_read_b64_tr_b16 a[236:237], v35 offset:8192\n" \
"v_cmp_gt_u32_e32 vcc, s4, v20\n" \
"s_waitcnt lgkmcnt(9)\n" \
"v_mfma_f32_16x16x32_bf16 a[32:35], a[200:203], a[224:227], a[32:35]\n" \
"ds_read_b64_tr_b16 a[238:239], v35 offset:9216\n" \
"s_lshl_b32 s4, s2, 3\n" \
"v_mfma_f32_16x16x32_bf16 a[36:39], a[136:139], a[164:167], 0\n" \
"ds_read_b128 a[144:147], v28 offset:0\n" \
"s_and_b32 s4, s4, 56\n" \
"v_mfma_f32_16x16x32_bf16 a[36:39], a[200:203], a[228:231], a[36:39]\n" \
"ds_read_b128 a[208:211], v29 offset:0\n" \
"v_cndmask_b32_e32 v217, v217, v213, vcc\n" \
"s_waitcnt lgkmcnt(10)\n" \
"v_mfma_f32_16x16x32_bf16 a[40:43], a[136:139], a[168:171], 0\n" \
"ds_read_b128 a[148:151], v28 offset:2048\n" \
"v_lshlrev_b32_e32 v213, 3, v214\n" \
"s_waitcnt lgkmcnt(9)\n" \
"v_mfma_f32_16x16x32_bf16 a[40:43], a[200:203], a[232:235], a[40:43]\n" \
"ds_read_b128 a[212:215], v29 offset:2048\n" \
"v_and_b32_e32 v213, 0x1ffc0, v213\n" \
"v_mfma_f32_16x16x32_bf16 a[24:27], a[132:135], a[168:171], 0\n" \
"ds_read_b128 a[152:155], v28 offset:4096\n" \
"v_and_b32_e64 v214, 7, v214\n" \
"v_mfma_f32_16x16x32_bf16 a[24:27], a[196:199], a[232:235], a[24:27]\n" \
"ds_read_b128 a[216:219], v29 offset:4096\n" \
"v_or3_b32 v213, v214, s4, v213\n" \
"v_mfma_f32_16x16x32_bf16 a[8:11], a[128:131], a[168:171], 0\n" \
"ds_read_b128 a[156:159], v28 offset:6144\n" \
"s_lshl_b32 s4, s2, 2\n" \
"v_mfma_f32_16x16x32_bf16 a[8:11], a[192:195], a[232:235], a[8:11]\n" \
"ds_read_b128 a[220:223], v29 offset:6144\n" \
"s_mov_b32 m0, s32\n" \
"s_waitcnt lgkmcnt(13)\n" \
"v_mfma_f32_16x16x32_bf16 a[48:51], a[140:143], a[160:163], 0\n" \
"s_mov_b32 exec_lo, 0xff000000\n" \
"s_mov_b32 exec_hi, 0x0\n" \
"global_load_lds_dword v[184:185], off\n" \
"s_mov_b64 exec, -1\n" \
"s_waitcnt lgkmcnt(12)\n" \
"v_mfma_f32_16x16x32_bf16 a[48:51], a[204:207], a[224:227], a[48:51]\n" \
"ds_read_b64_tr_b16 a[176:177], v36 offset:0\n" \
"s_or_b32 s4, s4, s3\n" \
"v_mfma_f32_16x16x32_bf16 a[52:55], a[140:143], a[164:167], 0\n" \
"ds_read_b64_tr_b16 a[178:179], v36 offset:1024\n" \
"s_mul_i32 s4, s4, 18\n" \
"v_mfma_f32_16x16x32_bf16 a[52:55], a[204:207], a[228:231], a[52:55]\n" \
"s_mov_b32 m0, s32\n" \
"ds_read_b64_tr_b16 a[240:241], v36 offset:8192\n" \
"v_mfma_f32_16x16x32_bf16 a[56:59], a[140:143], a[168:171], 0\n" \
"s_mov_b32 exec_lo, 0xffff\n" \
"s_mov_b32 exec_hi, 0x0\n" \
"global_load_lds_dword v[186:187], off\n" \
"s_mov_b64 exec, -1\n" \
"v_mfma_f32_16x16x32_bf16 a[56:59], a[204:207], a[232:235], a[56:59]\n" \
"ds_read_b64_tr_b16 a[242:243], v36 offset:9216\n" \
"v_min_u32_e32 v212, 53, v212\n" \
"s_waitcnt lgkmcnt(14)\n" \
"v_mfma_f32_16x16x32_bf16 a[60:63], a[140:143], a[172:175], 0\n" \
"ds_read_b64_tr_b16 a[180:181], v37 offset:0\n" \
"s_sub_i32 s4, s4, 36\n" \
"s_waitcnt lgkmcnt(13)\n" \
"v_mfma_f32_16x16x32_bf16 a[60:63], a[204:207], a[236:239], a[60:63]\n" \
"ds_read_b64_tr_b16 a[182:183], v37 offset:1024\n" \
"v_add_u32_e32 v212, s4, v212\n" \
"v_mfma_f32_16x16x32_bf16 a[44:47], a[136:139], a[172:175], 0\n" \
"ds_read_b64_tr_b16 a[244:245], v37 offset:8192\n" \
"v_mov_b32_e64 v215, s12\n" \
"v_mfma_f32_16x16x32_bf16 a[44:47], a[200:203], a[236:239], a[44:47]\n" \
"ds_read_b64_tr_b16 a[246:247], v37 offset:9216\n" \
"v_mov_b32_e64 v216, s8\n" \
"v_mfma_f32_16x16x32_bf16 a[28:31], a[132:135], a[172:175], 0\n" \
"ds_read_b64_tr_b16 a[184:185], v38 offset:0\n" \
"v_cndmask_b32_e64 v212, v212, v213, vcc\n" \
"v_mfma_f32_16x16x32_bf16 a[28:31], a[196:199], a[236:239], a[28:31]\n" \
"ds_read_b64_tr_b16 a[186:187], v38 offset:1024\n" \
"v_cndmask_b32_e64 v216, v216, v215, vcc\n" \
"v_mfma_f32_16x16x32_bf16 a[12:15], a[128:131], a[172:175], 0\n" \
"v_lshlrev_b32_e32 v212, 7, v212\n" \
"v_mov_b32_e32 v213, 0\n" \
"v_mfma_f32_16x16x32_bf16 a[12:15], a[192:195], a[236:239], a[12:15]\n" \
"v_lshl_add_u64 v[188:189], v[216:217], 0, v[212:213]\n" \
"s_movk_i32 s5, 0xd80\n" \
"s_waitcnt lgkmcnt(15)\n" \
"v_mfma_f32_16x16x32_bf16 a[76:79], a[144:147], a[172:175], 0\n" \
"ds_read_b64_tr_b16 a[248:249], v38 offset:8192\n" \
"s_lshr_b32 s6, s2, 5\n" \
"v_mfma_f32_16x16x32_bf16 a[76:79], a[208:211], a[236:239], a[76:79]\n" \
"v_lshrrev_b32_e32 v212, 4, v20\n" \
"v_lshl_or_b32 v215, s3, 6, v212\n" \
"v_mfma_f32_16x16x32_bf16 a[72:75], a[144:147], a[168:171], 0\n" \
"v_mul_u32_u24_e32 v216, 0x2aab, v215\n" \
"v_lshrrev_b32_e64 v216, 16, v216\n" \
"v_mfma_f32_16x16x32_bf16 a[72:75], a[208:211], a[232:235], a[72:75]\n" \
"ds_read_b64_tr_b16 a[250:251], v38 offset:9216\n" \
"v_mad_i32_i24 v217, v216, -6, v215\n" \
"v_mfma_f32_16x16x32_bf16 a[68:71], a[144:147], a[164:167], 0\n" \
"v_add_u32_e32 v218, 0xfffffe50, v215\n" \
"s_mul_i32 s35, s6, 0x48\n" \
"v_mfma_f32_16x16x32_bf16 a[68:71], a[208:211], a[228:231], a[68:71]\n" \
"v_lshrrev_b32_e32 v219, 1, v217\n" \
"v_add_u32_e32 v218, s35, v216\n" \
"v_mfma_f32_16x16x32_bf16 a[64:67], a[144:147], a[160:163], 0\n" \
"v_mul_u32_u24_e32 v219, 0x480, v219\n" \
"v_and_b32_e64 v214, 16, v20\n" \
"v_mfma_f32_16x16x32_bf16 a[64:67], a[208:211], a[224:227], a[64:67]\n" \
"ds_read_b64_tr_b16 a[188:189], v39 offset:0\n" \
"v_mad_u32_u24 v219, v218, s5, v219\n" \
"v_mfma_f32_16x16x32_bf16 a[80:83], a[148:151], a[160:163], 0\n" \
"v_or_b32_e32 v192, v219, v214\n" \
"v_mov_b32_e32 v220, 2.0\n" \
"s_waitcnt lgkmcnt(15)\n" \
"v_mfma_f32_16x16x32_bf16 a[80:83], a[212:215], a[224:227], a[80:83]\n" \
"v_mov_b32_e32 v222, 7\n" \
"v_lshlrev_b32_e64 v220, 7, v218\n" \
"v_mfma_f32_16x16x32_bf16 a[84:87], a[148:151], a[164:167], 0\n" \
"ds_read_b64_tr_b16 a[190:191], v39 offset:1024\n" \
"v_lshrrev_b32_e64 v218, 1, v218\n" \
"v_mfma_f32_16x16x32_bf16 a[84:87], a[212:215], a[228:231], a[84:87]\n" \
"v_and_b32_e32 v218, 4, v218\n" \
"v_and_b32_e32 v216, 2, v216\n" \
"v_mfma_f32_16x16x32_bf16 a[88:91], a[148:151], a[168:171], 0\n" \
"s_mul_i32 s7, s6, 0x2400\n" \
"s_sub_i32 s34, 0x22000, s7\n" \
"v_mfma_f32_16x16x32_bf16 a[88:91], a[212:215], a[232:235], a[88:91]\n" \
"ds_read_b64_tr_b16 a[252:253], v39 offset:8192\n" \
"v_bitop3_b32 v216, v217, v218, v216 bitop3:0x1e\n" \
"v_mfma_f32_16x16x32_bf16 a[92:95], a[148:151], a[172:175], 0\n" \
"v_or_b32_e32 v213, 0x24400, v20\n" \
"v_lshlrev_b32_e64 v216, 4, v216\n" \
"v_mfma_f32_16x16x32_bf16 a[92:95], a[212:215], a[236:239], a[92:95]\n" \
"v_add3_u32 v196, v220, s34, v216\n" \
"v_mov_b32_e64 v216, v213\n" \
"v_mfma_f32_16x16x32_bf16 a[108:111], a[152:155], a[172:175], 0\n" \
"ds_read_b64_tr_b16 a[254:255], v39 offset:9216\n" \
"s_movk_i32 s36, 0x1b0\n" \
"v_mfma_f32_16x16x32_bf16 a[108:111], a[216:219], a[236:239], a[108:111]\n" \
"v_or_b32_e32 v216, 0x100, v215\n" \
"v_mul_u32_u24_e32 v217, 0x2aab, v216\n" \
"v_mfma_f32_16x16x32_bf16 a[104:107], a[152:155], a[168:171], 0\n" \
"v_add_u32_e32 v215, 0xffffff50, v215\n" \
"v_lshrrev_b32_e32 v217, 16, v217\n" \
"v_mfma_f32_16x16x32_bf16 a[104:107], a[216:219], a[232:235], a[104:107]\n" \
"v_mov_b32_e64 v220, v215\n" \
"v_mad_i32_i24 v218, v217, -6, v216\n" \
"v_mfma_f32_16x16x32_bf16 a[100:103], a[152:155], a[164:167], 0\n" \
"v_cmp_gt_u32_e32 vcc, s36, v216\n" \
"v_mov_b32_e32 v221, 7\n" \
"v_mfma_f32_16x16x32_bf16 a[100:103], a[216:219], a[228:231], a[100:103]\n" \
"v_lshrrev_b32_e32 v222, 1, v218\n" \
"v_cndmask_b32_e32 v217, v220, v217, vcc\n" \
"v_mfma_f32_16x16x32_bf16 a[96:99], a[152:155], a[160:163], 0\n" \
"s_movk_i32 s4, 0x48\n" \
"v_add_u32_e32 v220, s35, v217\n" \
"v_mfma_f32_16x16x32_bf16 a[96:99], a[216:219], a[224:227], a[96:99]\n" \
"v_mul_u32_u24_e32 v222, 0x480, v222\n" \
"v_mad_u32_u24 v222, v220, s5, v222\n" \
"s_waitcnt lgkmcnt(15)\n" \
"v_mfma_f32_16x16x32_bf16 a[112:115], a[156:159], a[160:163], 0\n" \
"v_cmp_gt_u32_e64 s[4:5], s4, v215\n" \
"v_cndmask_b32_e32 v215, v221, v218, vcc\n" \
"v_mfma_f32_16x16x32_bf16 a[112:115], a[220:223], a[224:227], a[112:115]\n" \
"v_lshrrev_b32_e32 v218, 1, v220\n" \
"v_or_b32_e32 v214, v222, v214\n" \
"v_mfma_f32_16x16x32_bf16 a[116:119], a[156:159], a[164:167], 0\n" \
"v_mov_b32_e32 v222, 2.0\n" \
"v_and_b32_e32 v218, 4, v218\n" \
"v_mfma_f32_16x16x32_bf16 a[116:119], a[220:223], a[228:231], a[116:119]\n" \
"v_and_b32_e64 v217, 2, v217\n" \
"v_bitop3_b32 v215, v215, v218, v217 bitop3:0x1e\n" \
"v_mfma_f32_16x16x32_bf16 a[120:123], a[156:159], a[168:171], 0\n" \
"v_lshlrev_b32_e32 v223, 1, v220\n" \
"v_mov_b32_e32 v224, 2.0\n" \
"v_mfma_f32_16x16x32_bf16 a[120:123], a[220:223], a[232:235], a[120:123]\n" \
"v_cndmask_b32_e32 v193, v222, v214, vcc\n" \
"v_lshlrev_b32_e32 v214, 7, v220\n" \
"v_mfma_f32_16x16x32_bf16 a[124:127], a[156:159], a[172:175], 0\n" \
"v_lshlrev_b32_e32 v215, 4, v215\n" \
"s_mul_i32 s39, s3, 18\n" \
"v_mfma_f32_16x16x32_bf16 a[124:127], a[220:223], a[236:239], a[124:127]\n" \
"s_waitcnt vmcnt(5) lgkmcnt(0)\n" \
"s_barrier\n" \
"v_mfma_f32_16x16x32_bf16 v[52:55], a[128:131], a[176:179], 0\n" \
"s_add_u32 m0, s30, 0x0\n" \
"s_movk_i32 s84, 0x100\n" \
"v_mfma_f32_16x16x32_bf16 v[52:55], a[192:195], a[240:243], v[52:55]\n" \
"buffer_load_dwordx4 v22, s[64:67], s84 offen offset:0 lds\n" \
"v_mfma_f32_16x16x32_bf16 v[56:59], a[128:131], a[180:183], 0\n" \
"ds_read_b64_tr_b16 a[160:161], v32 offset:32768\n" \
"v_add3_u32 v214, v214, s34, v215\n" \
"v_mfma_f32_16x16x32_bf16 v[56:59], a[192:195], a[244:247], v[56:59]\n" \
"s_movk_i32 s84, 0x1d00\n" \
"v_mfma_f32_16x16x32_bf16 v[60:63], a[128:131], a[184:187], 0\n" \
"buffer_load_dwordx4 v23, s[64:67], s84 offen offset:1024 lds\n" \
"v_mfma_f32_16x16x32_bf16 v[60:63], a[192:195], a[248:251], v[60:63]\n" \
"ds_read_b64_tr_b16 a[162:163], v32 offset:33792\n" \
"v_cndmask_b32_e64 v195, v224, v223, s[4:5]\n" \
"v_mfma_f32_16x16x32_bf16 v[64:67], a[128:131], a[188:191], 0\n" \
"ds_read_b64_tr_b16 a[224:225], v32 offset:40960\n" \
"s_movk_i32 s4, 0x1f8\n" \
"v_mfma_f32_16x16x32_bf16 v[64:67], a[192:195], a[252:255], v[64:67]\n" \
"ds_read_b128 a[128:131], v28 offset:49152\n" \
"v_mov_b32_e64 v215, v213\n" \
"v_mfma_f32_16x16x32_bf16 v[80:83], a[132:135], a[188:191], 0\n" \
"ds_read_b128 a[192:195], v29 offset:49152\n" \
"s_add_i32 s35, s35, s39\n" \
"v_mfma_f32_16x16x32_bf16 v[80:83], a[196:199], a[252:255], v[80:83]\n" \
"s_movk_i32 s84, 0x3900\n" \
"v_mfma_f32_16x16x32_bf16 v[76:79], a[132:135], a[184:187], 0\n" \
"buffer_load_dwordx4 v22, s[64:67], s84 offen offset:2048 lds\n" \
"v_mfma_f32_16x16x32_bf16 v[76:79], a[196:199], a[248:251], v[76:79]\n" \
"s_movk_i32 s84, 0x5500\n" \
"v_mfma_f32_16x16x32_bf16 v[72:75], a[132:135], a[180:183], 0\n" \
"buffer_load_dwordx4 v23, s[64:67], s84 offen offset:3072 lds\n" \
"v_mfma_f32_16x16x32_bf16 v[72:75], a[196:199], a[244:247], v[72:75]\n" \
"s_add_u32 m0, s31, 0x0\n" \
"s_mov_b32 s84, 0x100000\n" \
"v_mfma_f32_16x16x32_bf16 v[68:71], a[132:135], a[176:179], 0\n" \
"buffer_load_dwordx4 v26, s[68:71], s84 offen offset:0 lds\n" \
"v_mfma_f32_16x16x32_bf16 v[68:71], a[196:199], a[240:243], v[68:71]\n" \
"ds_read_b128 a[132:135], v28 offset:51200\n" \
"v_cmp_gt_u32_e64 vcc, s4, v216\n" \
"v_mfma_f32_16x16x32_bf16 v[84:87], a[136:139], a[176:179], 0\n" \
"ds_read_b128 a[196:199], v29 offset:51200\n" \
"v_add_u32_e64 v212, s35, v212\n" \
"v_mfma_f32_16x16x32_bf16 v[84:87], a[200:203], a[240:243], v[84:87]\n" \
"s_addk_i32 s84, 0x7c00\n" \
"v_mfma_f32_16x16x32_bf16 v[88:91], a[136:139], a[180:183], 0\n" \
"buffer_load_dwordx4 v26, s[68:71], s84 offen offset:1024 lds\n" \
"v_mfma_f32_16x16x32_bf16 v[88:91], a[200:203], a[244:247], v[88:91]\n" \
"ds_read_b64_tr_b16 a[226:227], v32 offset:41984\n" \
"v_mov_b32_e32 v219, 2.0\n" \
"v_mfma_f32_16x16x32_bf16 v[92:95], a[136:139], a[184:187], 0\n" \
"s_addk_i32 s84, 0x7c00\n" \
"v_mfma_f32_16x16x32_bf16 v[92:95], a[200:203], a[248:251], v[92:95]\n" \
"buffer_load_dwordx4 v27, s[68:71], s84 offen offset:2048 lds\n" \
"v_mfma_f32_16x16x32_bf16 v[96:99], a[136:139], a[188:191], 0\n" \
"ds_read_b64_tr_b16 a[164:165], v33 offset:32768\n" \
"v_cndmask_b32_e32 v197, v215, v214, vcc\n" \
"v_mfma_f32_16x16x32_bf16 v[96:99], a[200:203], a[252:255], v[96:99]\n" \
"ds_read_b128 a[136:139], v28 offset:53248\n" \
"s_movk_i32 s35, 0x120\n" \
"v_mfma_f32_16x16x32_bf16 v[112:115], a[140:143], a[188:191], 0\n" \
"ds_read_b128 a[200:203], v29 offset:53248\n" \
"v_mul_u32_u24_e32 v214, 0xd80, v212\n" \
"v_mfma_f32_16x16x32_bf16 v[112:115], a[204:207], a[252:255], v[112:115]\n" \
"s_addk_i32 s84, 0x7c00\n" \
"v_mfma_f32_16x16x32_bf16 v[108:111], a[140:143], a[184:187], 0\n" \
"buffer_load_dwordx4 v27, s[68:71], s84 offen offset:3072 lds\n" \
"v_mfma_f32_16x16x32_bf16 v[108:111], a[204:207], a[248:251], v[108:111]\n" \
"s_add_u32 m0, s30, 0x1000\n" \
"s_mov_b32 s84, 0x8100\n" \
"v_mfma_f32_16x16x32_bf16 v[104:107], a[140:143], a[180:183], 0\n" \
"buffer_load_dwordx4 v22, s[64:67], s84 offen offset:0 lds\n" \
"v_mfma_f32_16x16x32_bf16 v[104:107], a[204:207], a[244:247], v[104:107]\n" \
"ds_read_b64_tr_b16 a[166:167], v33 offset:33792\n" \
"v_cmp_gt_u32_e64 vcc, s35, v20\n" \
"v_mfma_f32_16x16x32_bf16 v[100:103], a[140:143], a[176:179], 0\n" \
"ds_read_b64_tr_b16 a[228:229], v33 offset:40960\n" \
"v_lshrrev_b32_e64 v215, 1, v212\n" \
"v_mfma_f32_16x16x32_bf16 v[100:103], a[204:207], a[240:243], v[100:103]\n" \
"ds_read_b128 a[140:143], v28 offset:55296\n" \
"s_and_b32 s4, s2, 7\n" \
"v_mfma_f32_16x16x32_bf16 v[116:119], a[144:147], a[176:179], 0\n" \
"ds_read_b128 a[204:207], v29 offset:55296\n" \
"v_cndmask_b32_e32 v194, v219, v214, vcc\n" \
"v_mfma_f32_16x16x32_bf16 v[116:119], a[208:211], a[240:243], v[116:119]\n" \
"s_addk_i32 s84, 0x1c00\n" \
"v_mfma_f32_16x16x32_bf16 v[120:123], a[144:147], a[180:183], 0\n" \
"buffer_load_dwordx4 v23, s[64:67], s84 offen offset:1024 lds\n" \
"v_mfma_f32_16x16x32_bf16 v[120:123], a[208:211], a[244:247], v[120:123]\n" \
"s_addk_i32 s84, 0x1c00\n" \
"v_mfma_f32_16x16x32_bf16 v[124:127], a[144:147], a[184:187], 0\n" \
"buffer_load_dwordx4 v22, s[64:67], s84 offen offset:2048 lds\n" \
"v_mfma_f32_16x16x32_bf16 v[124:127], a[208:211], a[248:251], v[124:127]\n" \
"s_addk_i32 s84, 0x1c00\n" \
".fill (L_pb + 44768 - .) / 4, 4, 0xBF800000\n" \
"L_pp11:\n" \
"s_cbranch_execz L_pret\n" \
"v_mfma_f32_16x16x32_bf16 v[128:131], a[144:147], a[188:191], 0\n" \
"buffer_load_dwordx4 v23, s[64:67], s84 offen offset:3072 lds\n" \
"v_mfma_f32_16x16x32_bf16 v[128:131], a[208:211], a[252:255], v[128:131]\n" \
"ds_read_b128 a[144:147], v28 offset:32768\n" \
"v_lshlrev_b32_e32 v214, 7, v212\n" \
"v_mfma_f32_16x16x32_bf16 v[144:147], a[148:151], a[188:191], 0\n" \
"ds_read_b128 a[208:211], v29 offset:32768\n" \
"v_lshrrev_b32_e64 v212, 2, v212\n" \
"v_mfma_f32_16x16x32_bf16 v[144:147], a[212:215], a[252:255], v[144:147]\n" \
"s_add_u32 m0, s31, 0x4000\n" \
"s_mov_b32 s84, 0x100000\n" \
"v_mfma_f32_16x16x32_bf16 v[140:143], a[148:151], a[184:187], 0\n" \
"buffer_load_dwordx4 v26, s[72:75], s84 offen offset:0 lds\n" \
"v_mfma_f32_16x16x32_bf16 v[140:143], a[212:215], a[248:251], v[140:143]\n" \
"ds_read_b64_tr_b16 a[230:231], v33 offset:41984\n" \
"v_and_b32_e64 v212, 2, v212\n" \
"v_mfma_f32_16x16x32_bf16 v[136:139], a[148:151], a[180:183], 0\n" \
"s_addk_i32 s84, 0x7c00\n" \
"v_mfma_f32_16x16x32_bf16 v[136:139], a[212:215], a[244:247], v[136:139]\n" \
"buffer_load_dwordx4 v26, s[72:75], s84 offen offset:1024 lds\n" \
"v_mfma_f32_16x16x32_bf16 v[132:135], a[148:151], a[176:179], 0\n" \
"ds_read_b64_tr_b16 a[168:169], v34 offset:32768\n" \
"v_and_or_b32 v212, v215, 1, v212\n" \
"v_mfma_f32_16x16x32_bf16 v[132:135], a[212:215], a[240:243], v[132:135]\n" \
"ds_read_b128 a[148:151], v28 offset:34816\n" \
"s_lshl_b32 s5, s4, 2\n" \
"v_mfma_f32_16x16x32_bf16 v[148:151], a[152:155], a[176:179], 0\n" \
"ds_read_b128 a[212:215], v29 offset:34816\n" \
"s_bfe_u32 s38, s2, 0x20003\n" \
"v_mfma_f32_16x16x32_bf16 v[148:151], a[216:219], a[240:243], v[148:151]\n" \
"ds_read_b64_tr_b16 a[170:171], v34 offset:33792\n" \
"v_lshlrev_b32_e64 v212, 5, v212\n" \
"v_mfma_f32_16x16x32_bf16 v[152:155], a[152:155], a[180:183], 0\n" \
"ds_read_b64_tr_b16 a[232:233], v34 offset:40960\n" \
"s_movk_i32 s35, 0x60\n" \
"v_mfma_f32_16x16x32_bf16 v[152:155], a[216:219], a[244:247], v[152:155]\n" \
"ds_read_b64_tr_b16 a[234:235], v34 offset:41984\n" \
"s_or_b32 s5, s5, s38\n" \
"v_mfma_f32_16x16x32_bf16 v[156:159], a[152:155], a[184:187], 0\n" \
"ds_read_b64_tr_b16 a[172:173], v35 offset:32768\n" \
"v_bitop3_b32 v212, v212, s35, v214 bitop3:0x36\n" \
"v_mfma_f32_16x16x32_bf16 v[156:159], a[216:219], a[248:251], v[156:159]\n" \
"ds_read_b64_tr_b16 a[174:175], v35 offset:33792\n" \
"s_lshr_b32 s36, s2, 3\n" \
"v_mfma_f32_16x16x32_bf16 v[160:163], a[152:155], a[188:191], 0\n" \
"ds_read_b64_tr_b16 a[236:237], v35 offset:40960\n" \
"v_add_u32_e32 v212, s34, v212\n" \
"v_mfma_f32_16x16x32_bf16 v[160:163], a[216:219], a[252:255], v[160:163]\n" \
"ds_read_b128 a[152:155], v28 offset:36864\n" \
"s_mul_i32 s34, s5, 0x12000\n" \
"v_mfma_f32_16x16x32_bf16 v[176:179], a[156:159], a[188:191], 0\n" \
"ds_read_b128 a[216:219], v29 offset:36864\n" \
"s_or_b32 s40, s34, 0x6000000\n" \
"v_mfma_f32_16x16x32_bf16 v[176:179], a[220:223], a[252:255], v[176:179]\n" \
"ds_read_b64_tr_b16 a[238:239], v35 offset:41984\n" \
"s_mul_i32 s34, s36, 0x90000\n" \
"v_mfma_f32_16x16x32_bf16 v[172:175], a[156:159], a[184:187], 0\n" \
"s_lshl_b32 s36, s3, 14\n" \
"s_add_i32 s34, s34, s36\n" \
"v_mfma_f32_16x16x32_bf16 v[172:175], a[220:223], a[248:251], v[172:175]\n" \
"s_lshl_b32 s38, s5, 8\n" \
"s_add_i32 s41, s34, 0x4240000\n" \
"v_mfma_f32_16x16x32_bf16 v[168:171], a[156:159], a[180:183], 0\n" \
"s_mul_i32 s34, s5, 36\n" \
"s_add_i32 s5, s7, s40\n" \
"v_mfma_f32_16x16x32_bf16 v[168:171], a[220:223], a[244:247], v[168:171]\n" \
"s_addk_i32 s5, 0x2000\n" \
"s_mov_b32 s37, 2.0\n" \
"v_mfma_f32_16x16x32_bf16 v[164:167], a[156:159], a[176:179], 0\n" \
"s_or_b32 s38, s38, 0x5be8000\n" \
"v_mfma_f32_16x16x32_bf16 v[164:167], a[220:223], a[240:243], v[164:167]\n" \
"ds_read_b128 a[156:159], v28 offset:38912\n" \
"ds_read_b128 a[220:223], v29 offset:38912\n" \
"s_waitcnt lgkmcnt(15)\n" \
"v_mfma_f32_16x16x32_bf16 a[0:3], a[128:131], a[160:163], a[0:3]\n" \
"s_addk_i32 s84, 0x7c00\n" \
"v_mfma_f32_16x16x32_bf16 a[0:3], a[192:195], a[224:227], a[0:3]\n" \
"buffer_load_dwordx4 v27, s[72:75], s84 offen offset:2048 lds\n" \
"v_mfma_f32_16x16x32_bf16 a[4:7], a[128:131], a[164:167], a[4:7]\n" \
"ds_read_b64_tr_b16 a[176:177], v36 offset:32768\n" \
"s_waitcnt lgkmcnt(15)\n" \
"v_mfma_f32_16x16x32_bf16 a[4:7], a[192:195], a[228:231], a[4:7]\n" \
"s_addk_i32 s84, 0x7c00\n" \
"s_waitcnt lgkmcnt(11)\n" \
"v_mfma_f32_16x16x32_bf16 a[8:11], a[128:131], a[168:171], a[8:11]\n" \
"buffer_load_dwordx4 v27, s[72:75], s84 offen offset:3072 lds\n" \
"s_waitcnt lgkmcnt(9)\n" \
"v_mfma_f32_16x16x32_bf16 a[8:11], a[192:195], a[232:235], a[8:11]\n" \
"ds_read_b64_tr_b16 a[178:179], v36 offset:33792\n" \
"s_waitcnt lgkmcnt(8)\n" \
"v_mfma_f32_16x16x32_bf16 a[12:15], a[128:131], a[172:175], a[12:15]\n" \
"ds_read_b64_tr_b16 a[240:241], v36 offset:40960\n" \
"s_waitcnt lgkmcnt(5)\n" \
"v_mfma_f32_16x16x32_bf16 a[12:15], a[192:195], a[236:239], a[12:15]\n" \
"s_cmp_lg_u32 s3, 0\n" \
"s_cselect_b32 s36, s37, s5\n" \
"v_mfma_f32_16x16x32_bf16 a[28:31], a[132:135], a[172:175], a[28:31]\n" \
"s_lshl_b32 s5, s6, 2\n" \
"s_or_b32 s5, s38, s5\n" \
"v_mfma_f32_16x16x32_bf16 a[28:31], a[196:199], a[236:239], a[28:31]\n" \
"ds_read_b64_tr_b16 a[242:243], v36 offset:41984\n" \
"s_lshl_b32 s35, s3, 10\n" \
"v_mfma_f32_16x16x32_bf16 a[24:27], a[132:135], a[168:171], a[24:27]\n" \
"s_cmpk_lt_u32 s3, 3\n" \
"s_cselect_b32 s37, s37, s5\n" \
"v_mfma_f32_16x16x32_bf16 a[24:27], a[196:199], a[232:235], a[24:27]\n" \
"s_lshl_b32 s5, s4, 16\n" \
"s_lshl_b32 s4, s4, 11\n" \
"v_mfma_f32_16x16x32_bf16 a[20:23], a[132:135], a[164:167], a[20:23]\n" \
"v_cndmask_b32_e32 v198, v213, v212, vcc\n" \
"v_or_b32_e32 v212, s35, v20\n" \
"v_mfma_f32_16x16x32_bf16 a[20:23], a[196:199], a[228:231], a[20:23]\n" \
"ds_read_b64_tr_b16 a[180:181], v37 offset:32768\n" \
"s_or_b32 s39, s40, s35\n" \
"v_mfma_f32_16x16x32_bf16 a[16:19], a[132:135], a[160:163], a[16:19]\n" \
"s_or_b32 s55, s41, s4\n" \
"v_or_b32_e32 v199, 0x22000, v212\n" \
"v_mfma_f32_16x16x32_bf16 a[16:19], a[196:199], a[224:227], a[16:19]\n" \
"s_add_i32 s35, s39, s7\n" \
"s_add_i32 s54, s41, s5\n" \
"v_mfma_f32_16x16x32_bf16 a[32:35], a[136:139], a[160:163], a[32:35]\n" \
"s_mov_b32 m0, s32\n" \
"ds_read_b64_tr_b16 a[182:183], v37 offset:33792\n" \
"v_mfma_f32_16x16x32_bf16 a[32:35], a[200:203], a[224:227], a[32:35]\n" \
"s_mov_b32 exec_lo, 0x0\n" \
"s_mov_b32 exec_hi, 0xff\n" \
"global_load_lds_dword v[184:185], off\n" \
"s_mov_b64 exec, -1\n" \
"v_mfma_f32_16x16x32_bf16 a[36:39], a[136:139], a[164:167], a[36:39]\n" \
"s_add_i32 s55, s55, 0x80000\n" \
"v_mov_b32_e32 v215, 0xffff\n" \
"v_mfma_f32_16x16x32_bf16 a[36:39], a[200:203], a[228:231], a[36:39]\n" \
"ds_read_b64_tr_b16 a[244:245], v37 offset:40960\n" \
"s_and_b32 s4, s3, 1\n" \
"v_mfma_f32_16x16x32_bf16 a[40:43], a[136:139], a[168:171], a[40:43]\n" \
"s_lshl_b32 s5, s4, 4\n" \
"v_lshrrev_b32_e32 v212, 4, v20\n" \
"v_mfma_f32_16x16x32_bf16 a[40:43], a[200:203], a[232:235], a[40:43]\n" \
"s_mov_b32 m0, s32\n" \
"v_lshrrev_b32_e32 v218, 6, v20\n" \
"v_mfma_f32_16x16x32_bf16 a[44:47], a[136:139], a[172:175], a[44:47]\n" \
"s_mov_b32 exec_lo, 0xffff0000\n" \
"s_mov_b32 exec_hi, 0x0\n" \
"global_load_lds_dword v[186:187], off\n" \
"s_mov_b64 exec, -1\n" \
"v_mfma_f32_16x16x32_bf16 a[44:47], a[200:203], a[236:239], a[44:47]\n" \
"ds_read_b64_tr_b16 a[246:247], v37 offset:41984\n" \
"v_and_b32_e64 v219, 12, v218\n" \
"v_mfma_f32_16x16x32_bf16 a[60:63], a[140:143], a[172:175], a[60:63]\n" \
"v_and_or_b32 v214, v212, 15, s5\n" \
"v_cmp_gt_u32_e64 vcc, v219, v214\n" \
"v_mfma_f32_16x16x32_bf16 a[60:63], a[204:207], a[236:239], a[60:63]\n" \
"v_mov_b32_e32 v220, 0xffff0000\n" \
"s_lshl_b32 s5, s2, 2\n" \
"v_mfma_f32_16x16x32_bf16 a[56:59], a[140:143], a[168:171], a[56:59]\n" \
"ds_read_b64_tr_b16 a[184:185], v38 offset:32768\n" \
"v_cndmask_b32_e64 v217, v215, 0, vcc\n" \
"v_mfma_f32_16x16x32_bf16 a[56:59], a[204:207], a[232:235], a[56:59]\n" \
"v_cmp_lt_u32_e32 vcc, v219, v214\n" \
"v_mov_b32_e64 v213, s23\n" \
"v_mfma_f32_16x16x32_bf16 a[52:55], a[140:143], a[164:167], a[52:55]\n" \
"s_or_b32 s5, s5, s3\n" \
"v_cndmask_b32_e32 v221, 0, v220, vcc\n" \
"v_mfma_f32_16x16x32_bf16 a[52:55], a[204:207], a[228:231], a[52:55]\n" \
"ds_read_b64_tr_b16 a[186:187], v38 offset:33792\n" \
"v_or_b32_e64 v12, v217, v221\n" \
"v_mfma_f32_16x16x32_bf16 a[48:51], a[140:143], a[160:163], a[48:51]\n" \
"v_or_b32_e32 v217, 2, v219\n" \
"v_cmp_gt_u32_e32 vcc, v217, v214\n" \
"v_mfma_f32_16x16x32_bf16 a[48:51], a[204:207], a[224:227], a[48:51]\n" \
"v_mov_b32_e32 v216, s25\n" \
"s_movk_i32 s40, 0xf400\n" \
"v_mfma_f32_16x16x32_bf16 a[64:67], a[144:147], a[160:163], a[64:67]\n" \
"ds_read_b64_tr_b16 a[248:249], v38 offset:40960\n" \
"v_cndmask_b32_e64 v221, v215, 0, vcc\n" \
"v_mfma_f32_16x16x32_bf16 a[64:67], a[208:211], a[224:227], a[64:67]\n" \
"v_cmp_lt_u32_e32 vcc, v217, v214\n" \
"s_lshl_b32 s4, s4, 6\n" \
"v_mfma_f32_16x16x32_bf16 a[68:71], a[144:147], a[164:167], a[68:71]\n" \
"v_cndmask_b32_e32 v217, 0, v220, vcc\n" \
"v_or_b32_e32 v13, v221, v217\n" \
"v_mfma_f32_16x16x32_bf16 a[68:71], a[208:211], a[228:231], a[68:71]\n" \
"ds_read_b64_tr_b16 a[250:251], v38 offset:41984\n" \
"v_or_b32_e64 v217, 16, v219\n" \
"v_mfma_f32_16x16x32_bf16 a[72:75], a[144:147], a[168:171], a[72:75]\n" \
"v_cmp_gt_u32_e32 vcc, v217, v214\n" \
"s_nop 1\n" \
"v_cndmask_b32_e64 v221, v215, 0, vcc\n" \
"v_mfma_f32_16x16x32_bf16 a[72:75], a[208:211], a[232:235], a[72:75]\n" \
"v_cmp_lt_u32_e32 vcc, v217, v214\n" \
"s_nop 1\n" \
"v_cndmask_b32_e64 v217, 0, v220, vcc\n" \
"v_mfma_f32_16x16x32_bf16 a[76:79], a[144:147], a[172:175], a[76:79]\n" \
"v_or_b32_e32 v14, v221, v217\n" \
"v_or_b32_e32 v217, 18, v219\n" \
"v_mfma_f32_16x16x32_bf16 a[76:79], a[208:211], a[236:239], a[76:79]\n" \
"ds_read_b64_tr_b16 a[188:189], v39 offset:32768\n" \
"v_cmp_gt_u32_e64 vcc, v217, v214\n" \
"v_mfma_f32_16x16x32_bf16 a[92:95], a[148:151], a[172:175], a[92:95]\n" \
"s_nop 0\n" \
"v_cndmask_b32_e64 v215, v215, 0, vcc\n" \
"v_cmp_lt_u32_e32 vcc, v217, v214\n" \
"v_mfma_f32_16x16x32_bf16 a[92:95], a[212:215], a[236:239], a[92:95]\n" \
"s_nop 0\n" \
"v_cndmask_b32_e32 v214, 0, v220, vcc\n" \
"v_or_b32_e64 v15, v215, v214\n" \
"v_mfma_f32_16x16x32_bf16 a[88:91], a[148:151], a[168:171], a[88:91]\n" \
"ds_read_b64_tr_b16 a[190:191], v39 offset:33792\n" \
"v_mad_u64_u32 v[214:215], s[6:7], s5, 42, v[212:213]\n" \
"v_mfma_f32_16x16x32_bf16 a[88:91], a[212:215], a[232:235], a[88:91]\n" \
"s_movk_i32 s5, 0x1440\n" \
"v_cmp_gt_u32_e32 vcc, s5, v214\n" \
"v_mfma_f32_16x16x32_bf16 a[84:87], a[148:151], a[164:167], a[84:87]\n" \
"v_min_u32_e32 v217, 0xa43f, v214\n" \
"v_mov_b32_e64 v214, s24\n" \
"v_mfma_f32_16x16x32_bf16 a[84:87], a[212:215], a[228:231], a[84:87]\n" \
"ds_read_b64_tr_b16 a[252:253], v39 offset:40960\n" \
"v_cndmask_b32_e64 v215, v216, v213, vcc\n" \
"v_mfma_f32_16x16x32_bf16 a[80:83], a[148:151], a[160:163], a[80:83]\n" \
"v_mov_b32_e32 v213, s22\n" \
"s_movk_i32 s6, 0xffee\n" \
"v_mfma_f32_16x16x32_bf16 a[80:83], a[212:215], a[224:227], a[80:83]\n" \
"v_cndmask_b32_e64 v214, v214, v213, vcc\n" \
"v_add_u32_e32 v213, 0xffffebc0, v217\n" \
"v_mfma_f32_16x16x32_bf16 a[96:99], a[152:155], a[160:163], a[96:99]\n" \
"ds_read_b64_tr_b16 a[254:255], v39 offset:41984\n" \
"s_lshl_b32 s5, s2, 5\n" \
"v_mfma_f32_16x16x32_bf16 a[96:99], a[216:219], a[224:227], a[96:99]\n" \
"v_cndmask_b32_e32 v213, v213, v217, vcc\n" \
"v_lshlrev_b32_e64 v216, 7, v213\n" \
"v_mfma_f32_16x16x32_bf16 a[100:103], a[152:155], a[164:167], a[100:103]\n" \
"v_mul_u32_u24_e32 v213, 0xe39, v212\n" \
"v_mov_b32_e64 v217, 0\n" \
"v_mfma_f32_16x16x32_bf16 a[100:103], a[216:219], a[228:231], a[100:103]\n" \
"v_lshrrev_b32_e64 v213, 16, v213\n" \
"v_lshl_add_u64 v[16:17], v[214:215], 0, v[216:217]\n" \
"v_mfma_f32_16x16x32_bf16 a[104:107], a[152:155], a[168:171], a[104:107]\n" \
"v_mad_i32_i24 v214, v213, s6, v212\n" \
"s_add_i32 s5, s5, 32\n" \
"v_mfma_f32_16x16x32_bf16 a[104:107], a[216:219], a[232:235], a[104:107]\n" \
"v_lshlrev_b32_e32 v215, 4, v214\n" \
"v_add_u32_e64 v216, s5, v215\n" \
"v_mfma_f32_16x16x32_bf16 a[108:111], a[152:155], a[172:175], a[108:111]\n" \
"v_and_b32_e32 v216, 0xf0, v216\n" \
"v_cmp_gt_u32_e64 vcc, 16, v214\n" \
"v_mfma_f32_16x16x32_bf16 a[108:111], a[216:219], a[236:239], a[108:111]\n" \
"s_movk_i32 s7, 0xf800\n" \
"v_cndmask_b32_e32 v214, v215, v216, vcc\n" \
"s_waitcnt lgkmcnt(15)\n" \
"v_mfma_f32_16x16x32_bf16 a[124:127], a[156:159], a[172:175], a[124:127]\n" \
"v_or_b32_e32 v215, 64, v212\n" \
"v_mul_u32_u24_e32 v216, 0xe39, v215\n" \
"v_mfma_f32_16x16x32_bf16 a[124:127], a[220:223], a[236:239], a[124:127]\n" \
"v_lshrrev_b32_e64 v216, 16, v216\n" \
"v_mad_i32_i24 v215, v216, s6, v215\n" \
"v_mfma_f32_16x16x32_bf16 a[120:123], a[156:159], a[168:171], a[120:123]\n" \
"v_lshlrev_b32_e32 v217, 4, v215\n" \
"v_add_u32_e32 v220, s5, v217\n" \
"v_mfma_f32_16x16x32_bf16 a[120:123], a[220:223], a[232:235], a[120:123]\n" \
"v_and_b32_e32 v220, 0xf0, v220\n" \
"v_cmp_gt_u32_e64 vcc, 16, v215\n" \
"v_mfma_f32_16x16x32_bf16 a[116:119], a[156:159], a[164:167], a[116:119]\n" \
"s_nop 0\n" \
"v_cndmask_b32_e32 v215, v217, v220, vcc\n" \
"v_or_b32_e32 v217, 0x80, v212\n" \
"v_mfma_f32_16x16x32_bf16 a[116:119], a[220:223], a[228:231], a[116:119]\n" \
"v_mul_u32_u24_e32 v220, 0xe39, v217\n" \
"v_lshrrev_b32_e64 v220, 16, v220\n" \
"v_mfma_f32_16x16x32_bf16 a[112:115], a[156:159], a[160:163], a[112:115]\n" \
"v_mad_i32_i24 v217, v220, s6, v217\n" \
"v_lshlrev_b32_e64 v221, 4, v217\n" \
"v_mfma_f32_16x16x32_bf16 a[112:115], a[220:223], a[224:227], a[112:115]\n" \
"s_waitcnt vmcnt(20)\n" \
"v_readfirstlane_b32 s62, v180\n" \
"s_nop 3\n" \
"s_add_u32 s62, s62, 0x9E3779B1\n" \
"s_waitcnt vmcnt(2) lgkmcnt(0)\n" \
"s_barrier\n" \
"v_mfma_f32_16x16x32_bf16 v[52:55], a[128:131], a[176:179], v[52:55]\n" \
"s_add_u32 m0, s30, 0x8000\n" \
"s_movk_i32 s84, 0x180\n" \
"v_mfma_f32_16x16x32_bf16 v[52:55], a[192:195], a[240:243], v[52:55]\n" \
"buffer_load_dwordx4 v22, s[64:67], s84 offen offset:0 lds\n" \
"v_mfma_f32_16x16x32_bf16 v[56:59], a[128:131], a[180:183], v[56:59]\n" \
"ds_read_b64_tr_b16 a[160:161], v32 offset:0\n" \
"v_add_u32_e64 v222, s5, v221\n" \
"v_mfma_f32_16x16x32_bf16 v[56:59], a[192:195], a[244:247], v[56:59]\n" \
"s_movk_i32 s84, 0x1d80\n" \
"v_mfma_f32_16x16x32_bf16 v[60:63], a[128:131], a[184:187], v[60:63]\n" \
"buffer_load_dwordx4 v23, s[64:67], s84 offen offset:1024 lds\n" \
"v_mfma_f32_16x16x32_bf16 v[60:63], a[192:195], a[248:251], v[60:63]\n" \
"ds_read_b64_tr_b16 a[162:163], v32 offset:1024\n" \
"v_and_b32_e32 v222, 0xf0, v222\n" \
"v_mfma_f32_16x16x32_bf16 v[64:67], a[128:131], a[188:191], v[64:67]\n" \
"ds_read_b64_tr_b16 a[224:225], v32 offset:8192\n" \
"v_cmp_gt_u32_e32 vcc, 16, v217\n" \
"v_mfma_f32_16x16x32_bf16 v[64:67], a[192:195], a[252:255], v[64:67]\n" \
"ds_read_b128 a[128:131], v28 offset:16384\n" \
"v_cndmask_b32_e64 v217, v221, v222, vcc\n" \
"v_mfma_f32_16x16x32_bf16 v[80:83], a[132:135], a[188:191], v[80:83]\n" \
"ds_read_b128 a[192:195], v29 offset:16384\n" \
"v_or_b32_e32 v221, 0xc0, v212\n" \
"v_mfma_f32_16x16x32_bf16 v[80:83], a[196:199], a[252:255], v[80:83]\n" \
"s_movk_i32 s84, 0x3980\n" \
"v_mfma_f32_16x16x32_bf16 v[76:79], a[132:135], a[184:187], v[76:79]\n" \
"buffer_load_dwordx4 v22, s[64:67], s84 offen offset:2048 lds\n" \
"v_mfma_f32_16x16x32_bf16 v[76:79], a[196:199], a[248:251], v[76:79]\n" \
"s_movk_i32 s84, 0x5580\n" \
"v_mfma_f32_16x16x32_bf16 v[72:75], a[132:135], a[180:183], v[72:75]\n" \
"buffer_load_dwordx4 v23, s[64:67], s84 offen offset:3072 lds\n" \
"v_mfma_f32_16x16x32_bf16 v[72:75], a[196:199], a[244:247], v[72:75]\n" \
"s_add_u32 m0, s31, 0x8000\n" \
"s_mov_b32 s84, 0x180000\n" \
"v_mfma_f32_16x16x32_bf16 v[68:71], a[132:135], a[176:179], v[68:71]\n" \
"buffer_load_dwordx4 v26, s[68:71], s84 offen offset:0 lds\n" \
"v_mfma_f32_16x16x32_bf16 v[68:71], a[196:199], a[240:243], v[68:71]\n" \
"ds_read_b128 a[132:135], v28 offset:18432\n" \
"v_mul_u32_u24_e32 v222, 0xe39, v221\n" \
"v_mfma_f32_16x16x32_bf16 v[84:87], a[136:139], a[176:179], v[84:87]\n" \
"ds_read_b128 a[196:199], v29 offset:18432\n" \
"v_lshrrev_b32_e64 v222, 16, v222\n" \
"v_mfma_f32_16x16x32_bf16 v[84:87], a[200:203], a[240:243], v[84:87]\n" \
"s_addk_i32 s84, 0x7c00\n" \
"v_mfma_f32_16x16x32_bf16 v[88:91], a[136:139], a[180:183], v[88:91]\n" \
"buffer_load_dwordx4 v26, s[68:71], s84 offen offset:1024 lds\n" \
"v_mfma_f32_16x16x32_bf16 v[88:91], a[200:203], a[244:247], v[88:91]\n" \
"ds_read_b64_tr_b16 a[226:227], v32 offset:9216\n" \
"v_mad_i32_i24 v221, v222, s6, v221\n" \
"v_mfma_f32_16x16x32_bf16 v[92:95], a[136:139], a[184:187], v[92:95]\n" \
"s_addk_i32 s84, 0x7c00\n" \
"v_mfma_f32_16x16x32_bf16 v[92:95], a[200:203], a[248:251], v[92:95]\n" \
"buffer_load_dwordx4 v27, s[68:71], s84 offen offset:2048 lds\n" \
"v_mfma_f32_16x16x32_bf16 v[96:99], a[136:139], a[188:191], v[96:99]\n" \
"ds_read_b64_tr_b16 a[164:165], v33 offset:0\n" \
"v_lshlrev_b32_e64 v223, 4, v221\n" \
"v_mfma_f32_16x16x32_bf16 v[96:99], a[200:203], a[252:255], v[96:99]\n" \
"ds_read_b128 a[136:139], v28 offset:20480\n" \
"v_add_u32_e64 v224, s5, v223\n" \
"v_mfma_f32_16x16x32_bf16 v[112:115], a[140:143], a[188:191], v[112:115]\n" \
"ds_read_b128 a[200:203], v29 offset:20480\n" \
"v_and_b32_e32 v224, 0xf0, v224\n" \
"v_mfma_f32_16x16x32_bf16 v[112:115], a[204:207], a[252:255], v[112:115]\n" \
"s_addk_i32 s84, 0x7c00\n" \
"v_mfma_f32_16x16x32_bf16 v[108:111], a[140:143], a[184:187], v[108:111]\n" \
"buffer_load_dwordx4 v27, s[68:71], s84 offen offset:3072 lds\n" \
"v_mfma_f32_16x16x32_bf16 v[108:111], a[204:207], a[248:251], v[108:111]\n" \
"s_add_u32 m0, s30, 0x9000\n" \
"s_mov_b32 s84, 0x8180\n" \
"v_mfma_f32_16x16x32_bf16 v[104:107], a[140:143], a[180:183], v[104:107]\n" \
"buffer_load_dwordx4 v22, s[64:67], s84 offen offset:0 lds\n" \
"v_mfma_f32_16x16x32_bf16 v[104:107], a[204:207], a[244:247], v[104:107]\n" \
"ds_read_b64_tr_b16 a[166:167], v33 offset:1024\n" \
"v_cmp_gt_u32_e32 vcc, 16, v221\n" \
"v_mfma_f32_16x16x32_bf16 v[100:103], a[140:143], a[176:179], v[100:103]\n" \
"ds_read_b64_tr_b16 a[228:229], v33 offset:8192\n" \
"v_cndmask_b32_e64 v221, v223, v224, vcc\n" \
"v_mfma_f32_16x16x32_bf16 v[100:103], a[204:207], a[240:243], v[100:103]\n" \
"ds_read_b128 a[140:143], v28 offset:22528\n" \
"v_or_b32_e32 v223, 0x100, v212\n" \
"v_mfma_f32_16x16x32_bf16 v[116:119], a[144:147], a[176:179], v[116:119]\n" \
"ds_read_b128 a[204:207], v29 offset:22528\n" \
"v_mul_u32_u24_e32 v224, 0xe39, v223\n" \
"v_mfma_f32_16x16x32_bf16 v[116:119], a[208:211], a[240:243], v[116:119]\n" \
"s_addk_i32 s84, 0x1c00\n" \
"v_mfma_f32_16x16x32_bf16 v[120:123], a[144:147], a[180:183], v[120:123]\n" \
"buffer_load_dwordx4 v23, s[64:67], s84 offen offset:1024 lds\n" \
"v_mfma_f32_16x16x32_bf16 v[120:123], a[208:211], a[244:247], v[120:123]\n" \
"s_addk_i32 s84, 0x1c00\n" \
"v_mfma_f32_16x16x32_bf16 v[124:127], a[144:147], a[184:187], v[124:127]\n" \
"buffer_load_dwordx4 v22, s[64:67], s84 offen offset:2048 lds\n" \
"v_mfma_f32_16x16x32_bf16 v[124:127], a[208:211], a[248:251], v[124:127]\n" \
"s_addk_i32 s84, 0x1c00\n" \
"v_mfma_f32_16x16x32_bf16 v[128:131], a[144:147], a[188:191], v[128:131]\n" \
"buffer_load_dwordx4 v23, s[64:67], s84 offen offset:3072 lds\n" \
"v_mfma_f32_16x16x32_bf16 v[128:131], a[208:211], a[252:255], v[128:131]\n" \
"ds_read_b128 a[144:147], v28 offset:0\n" \
"v_lshrrev_b32_e32 v224, 16, v224\n" \
"v_mfma_f32_16x16x32_bf16 v[144:147], a[148:151], a[188:191], v[144:147]\n" \
"ds_read_b128 a[208:211], v29 offset:0\n" \
"v_mad_i32_i24 v223, v224, s6, v223\n" \
"v_mfma_f32_16x16x32_bf16 v[144:147], a[212:215], a[252:255], v[144:147]\n" \
"s_add_u32 m0, s31, 0xc000\n" \
"s_mov_b32 s84, 0x180000\n" \
"v_mfma_f32_16x16x32_bf16 v[140:143], a[148:151], a[184:187], v[140:143]\n" \
"buffer_load_dwordx4 v26, s[72:75], s84 offen offset:0 lds\n" \
"v_mfma_f32_16x16x32_bf16 v[140:143], a[212:215], a[248:251], v[140:143]\n" \
"ds_read_b64_tr_b16 a[230:231], v33 offset:9216\n" \
"v_lshlrev_b32_e64 v225, 4, v223\n" \
"v_mfma_f32_16x16x32_bf16 v[136:139], a[148:151], a[180:183], v[136:139]\n" \
"s_addk_i32 s84, 0x7c00\n" \
"v_mfma_f32_16x16x32_bf16 v[136:139], a[212:215], a[244:247], v[136:139]\n" \
"buffer_load_dwordx4 v26, s[72:75], s84 offen offset:1024 lds\n" \
"v_mfma_f32_16x16x32_bf16 v[132:135], a[148:151], a[176:179], v[132:135]\n" \
"ds_read_b64_tr_b16 a[168:169], v34 offset:0\n" \
"v_add_u32_e32 v226, s5, v225\n" \
"v_mfma_f32_16x16x32_bf16 v[132:135], a[212:215], a[240:243], v[132:135]\n" \
"ds_read_b128 a[148:151], v28 offset:2048\n" \
"v_and_b32_e32 v226, 0xf0, v226\n" \
"v_mfma_f32_16x16x32_bf16 v[148:151], a[152:155], a[176:179], v[148:151]\n" \
"ds_read_b128 a[212:215], v29 offset:2048\n" \
"v_cmp_gt_u32_e64 vcc, 16, v223\n" \
"v_mfma_f32_16x16x32_bf16 v[148:151], a[216:219], a[240:243], v[148:151]\n" \
"ds_read_b64_tr_b16 a[170:171], v34 offset:1024\n" \
"v_cndmask_b32_e64 v223, v225, v226, vcc\n" \
"v_mfma_f32_16x16x32_bf16 v[152:155], a[152:155], a[180:183], v[152:155]\n" \
"ds_read_b64_tr_b16 a[232:233], v34 offset:8192\n" \
"v_or_b32_e32 v225, 0x140, v212\n" \
"v_mfma_f32_16x16x32_bf16 v[152:155], a[216:219], a[244:247], v[152:155]\n" \
"ds_read_b64_tr_b16 a[234:235], v34 offset:9216\n" \
"v_mul_u32_u24_e32 v226, 0xe39, v225\n" \
"v_mfma_f32_16x16x32_bf16 v[156:159], a[152:155], a[184:187], v[156:159]\n" \
"ds_read_b64_tr_b16 a[172:173], v35 offset:0\n" \
"v_lshrrev_b32_e64 v226, 16, v226\n" \
"v_mfma_f32_16x16x32_bf16 v[156:159], a[216:219], a[248:251], v[156:159]\n" \
"ds_read_b64_tr_b16 a[174:175], v35 offset:1024\n" \
"v_mad_i32_i24 v225, v226, s6, v225\n" \
"v_mfma_f32_16x16x32_bf16 v[160:163], a[152:155], a[188:191], v[160:163]\n" \
"ds_read_b64_tr_b16 a[236:237], v35 offset:8192\n" \
"v_lshlrev_b32_e64 v227, 4, v225\n" \
"v_mfma_f32_16x16x32_bf16 v[160:163], a[216:219], a[252:255], v[160:163]\n" \
"ds_read_b128 a[152:155], v28 offset:4096\n" \
"v_add_u32_e64 v228, s5, v227\n" \
"v_mfma_f32_16x16x32_bf16 v[176:179], a[156:159], a[188:191], v[176:179]\n" \
"ds_read_b128 a[216:219], v29 offset:4096\n" \
"v_and_b32_e32 v228, 0xf0, v228\n" \
"v_mfma_f32_16x16x32_bf16 v[176:179], a[220:223], a[252:255], v[176:179]\n" \
"ds_read_b64_tr_b16 a[238:239], v35 offset:9216\n" \
"v_cmp_gt_u32_e64 vcc, 16, v225\n" \
"v_mfma_f32_16x16x32_bf16 v[172:175], a[156:159], a[184:187], v[172:175]\n" \
"s_nop 0\n" \
"v_cndmask_b32_e32 v225, v227, v228, vcc\n" \
"v_or_b32_e32 v227, 0x180, v212\n" \
"v_mfma_f32_16x16x32_bf16 v[172:175], a[220:223], a[248:251], v[172:175]\n" \
"v_mul_u32_u24_e32 v228, 0xe39, v227\n" \
"v_lshrrev_b32_e64 v228, 16, v228\n" \
"v_mfma_f32_16x16x32_bf16 v[168:171], a[156:159], a[180:183], v[168:171]\n" \
"v_mad_i32_i24 v227, v228, s6, v227\n" \
"v_lshlrev_b32_e64 v229, 4, v227\n" \
"v_mfma_f32_16x16x32_bf16 v[168:171], a[220:223], a[244:247], v[168:171]\n" \
"v_add_u32_e64 v230, s5, v229\n" \
"v_and_b32_e32 v230, 0xf0, v230\n" \
"v_mfma_f32_16x16x32_bf16 v[164:167], a[156:159], a[176:179], v[164:167]\n" \
"v_cmp_gt_u32_e32 vcc, 16, v227\n" \
"s_nop 1\n" \
"v_cndmask_b32_e64 v227, v229, v230, vcc\n" \
"v_mfma_f32_16x16x32_bf16 v[164:167], a[220:223], a[240:243], v[164:167]\n" \
"ds_read_b128 a[156:159], v28 offset:6144\n" \
"ds_read_b128 a[220:223], v29 offset:6144\n" \
"s_waitcnt lgkmcnt(15)\n" \
"v_mfma_f32_16x16x32_bf16 a[0:3], a[128:131], a[160:163], a[0:3]\n" \
"s_addk_i32 s84, 0x7c00\n" \
"v_mfma_f32_16x16x32_bf16 a[0:3], a[192:195], a[224:227], a[0:3]\n" \
"buffer_load_dwordx4 v27, s[72:75], s84 offen offset:2048 lds\n" \
"v_mfma_f32_16x16x32_bf16 a[4:7], a[128:131], a[164:167], a[4:7]\n" \
"ds_read_b64_tr_b16 a[176:177], v36 offset:0\n" \
"v_or_b32_e32 v229, 0x1c0, v212\n" \
"s_waitcnt lgkmcnt(15)\n" \
"v_mfma_f32_16x16x32_bf16 a[4:7], a[192:195], a[228:231], a[4:7]\n" \
"s_addk_i32 s84, 0x7c00\n" \
"s_waitcnt lgkmcnt(11)\n" \
"v_mfma_f32_16x16x32_bf16 a[8:11], a[128:131], a[168:171], a[8:11]\n" \
"buffer_load_dwordx4 v27, s[72:75], s84 offen offset:3072 lds\n" \
"s_waitcnt lgkmcnt(9)\n" \
"v_mfma_f32_16x16x32_bf16 a[8:11], a[192:195], a[232:235], a[8:11]\n" \
"ds_read_b64_tr_b16 a[178:179], v36 offset:1024\n" \
"v_mul_u32_u24_e32 v230, 0xe39, v229\n" \
"s_waitcnt lgkmcnt(8)\n" \
"v_mfma_f32_16x16x32_bf16 a[12:15], a[128:131], a[172:175], a[12:15]\n" \
"ds_read_b64_tr_b16 a[240:241], v36 offset:8192\n" \
"v_lshrrev_b32_e32 v230, 16, v230\n" \
"s_waitcnt lgkmcnt(5)\n" \
"v_mfma_f32_16x16x32_bf16 a[12:15], a[192:195], a[236:239], a[12:15]\n" \
"v_mad_i32_i24 v229, v230, s6, v229\n" \
"v_lshlrev_b32_e32 v231, 4, v229\n" \
"v_mfma_f32_16x16x32_bf16 a[28:31], a[132:135], a[172:175], a[28:31]\n" \
"v_add_u32_e64 v232, s5, v231\n" \
"v_and_b32_e32 v232, 0xf0, v232\n" \
"v_mfma_f32_16x16x32_bf16 a[28:31], a[196:199], a[236:239], a[28:31]\n" \
"ds_read_b64_tr_b16 a[242:243], v36 offset:9216\n" \
"v_cmp_gt_u32_e64 vcc, 16, v229\n" \
"v_mfma_f32_16x16x32_bf16 a[24:27], a[132:135], a[168:171], a[24:27]\n" \
"buffer_load_dwordx4 v[200:203], v192, s[76:79], s34 offen offset:0\n" \
"v_mfma_f32_16x16x32_bf16 a[24:27], a[196:199], a[232:235], a[24:27]\n" \
"v_cndmask_b32_e64 v229, v231, v232, vcc\n" \
"v_or_b32_e32 v231, 0x200, v212\n" \
"v_mfma_f32_16x16x32_bf16 a[20:23], a[132:135], a[164:167], a[20:23]\n" \
"v_mul_u32_u24_e32 v232, 0xe39, v231\n" \
"v_lshrrev_b32_e64 v232, 16, v232\n" \
"v_mfma_f32_16x16x32_bf16 a[20:23], a[196:199], a[228:231], a[20:23]\n" \
"ds_read_b64_tr_b16 a[180:181], v37 offset:0\n" \
"v_mad_i32_i24 v231, v232, s6, v231\n" \
"v_mfma_f32_16x16x32_bf16 a[16:19], a[132:135], a[160:163], a[16:19]\n" \
"v_lshlrev_b32_e32 v233, 4, v231\n" \
"v_add_u32_e32 v234, s5, v233\n" \
"v_mfma_f32_16x16x32_bf16 a[16:19], a[196:199], a[224:227], a[16:19]\n" \
"s_movk_i32 s5, 0x480\n" \
"v_mad_u32_u24 v0, v213, s5, v214\n" \
"v_mfma_f32_16x16x32_bf16 a[32:35], a[136:139], a[160:163], a[32:35]\n" \
"buffer_load_dwordx4 v[204:207], v193, s[76:79], s34 offen offset:0\n" \
"v_mfma_f32_16x16x32_bf16 a[32:35], a[200:203], a[224:227], a[32:35]\n" \
"s_mov_b32 m0, s32\n" \
"ds_read_b64_tr_b16 a[182:183], v37 offset:1024\n" \
"v_mfma_f32_16x16x32_bf16 a[36:39], a[136:139], a[164:167], a[36:39]\n" \
"s_mov_b32 exec_lo, 0x0\n" \
"s_mov_b32 exec_hi, 0xff00\n" \
"global_load_lds_dword v[184:185], off\n" \
"s_mov_b64 exec, -1\n" \
"v_mfma_f32_16x16x32_bf16 a[36:39], a[200:203], a[228:231], a[36:39]\n" \
"ds_read_b64_tr_b16 a[244:245], v37 offset:8192\n" \
"v_mul_u32_u24_e32 v213, 0x480, v216\n" \
"v_mfma_f32_16x16x32_bf16 a[40:43], a[136:139], a[168:171], a[40:43]\n" \
"s_movk_i32 s6, 0xfc00\n" \
"s_nop 0\n" \
"v_add3_u32 v1, v213, v215, s6\n" \
"v_mfma_f32_16x16x32_bf16 a[40:43], a[200:203], a[232:235], a[40:43]\n" \
"buffer_load_dword v208, v194, s[76:79], s34 offen offset:32\n" \
"v_mfma_f32_16x16x32_bf16 a[44:47], a[136:139], a[172:175], a[44:47]\n" \
"ds_read_b64_tr_b16 a[246:247], v37 offset:9216\n" \
"v_mul_u32_u24_e32 v213, 0x480, v220\n" \
"v_mfma_f32_16x16x32_bf16 a[44:47], a[200:203], a[236:239], a[44:47]\n" \
"v_add3_u32 v2, v213, v217, s7\n" \
"v_mul_u32_u24_e32 v213, 0x480, v222\n" \
"v_mfma_f32_16x16x32_bf16 a[60:63], a[140:143], a[172:175], a[60:63]\n" \
"v_add3_u32 v3, v213, v221, s40\n" \
"v_mul_u32_u24_e32 v213, 0x480, v226\n" \
"v_mfma_f32_16x16x32_bf16 a[60:63], a[204:207], a[236:239], a[60:63]\n" \
"v_add3_u32 v5, v213, v225, s6\n" \
"v_mul_u32_u24_e32 v213, 0x480, v228\n" \
"v_mfma_f32_16x16x32_bf16 a[56:59], a[140:143], a[168:171], a[56:59]\n" \
"buffer_load_dword v209, v194, s[76:79], s34 offen offset:1184\n" \
"v_mfma_f32_16x16x32_bf16 a[56:59], a[204:207], a[232:235], a[56:59]\n" \
"ds_read_b64_tr_b16 a[184:185], v38 offset:0\n" \
"v_add3_u32 v6, v213, v227, s7\n" \
"v_mfma_f32_16x16x32_bf16 a[52:55], a[140:143], a[164:167], a[52:55]\n" \
"v_mul_u32_u24_e32 v213, 0x480, v230\n" \
"v_add3_u32 v7, v213, v229, s40\n" \
"v_mfma_f32_16x16x32_bf16 a[52:55], a[204:207], a[228:231], a[52:55]\n" \
"ds_read_b64_tr_b16 a[186:187], v38 offset:1024\n" \
"s_lshl_b32 s7, s3, 3\n" \
"v_mfma_f32_16x16x32_bf16 a[48:51], a[140:143], a[160:163], a[48:51]\n" \
"s_lshl_b32 s40, s3, 1\n" \
"s_and_b32 s7, s7, 16\n" \
"v_mfma_f32_16x16x32_bf16 a[48:51], a[204:207], a[224:227], a[48:51]\n" \
"s_and_b32 s40, s40, 2\n" \
"s_and_b32 s6, s2, 7\n" \
"v_mfma_f32_16x16x32_bf16 a[64:67], a[144:147], a[160:163], a[64:67]\n" \
"buffer_load_dword v211, v194, s[76:79], s34 offen offset:2336\n" \
"v_mfma_f32_16x16x32_bf16 a[64:67], a[208:211], a[224:227], a[64:67]\n" \
"ds_read_b64_tr_b16 a[248:249], v38 offset:8192\n" \
"v_bfe_u32 v213, v20, 7, 1\n" \
"v_mfma_f32_16x16x32_bf16 a[68:71], a[144:147], a[164:167], a[68:71]\n" \
"s_or_b32 s7, s40, s7\n" \
"v_and_b32_e32 v234, 0xf0, v234\n" \
"v_mfma_f32_16x16x32_bf16 a[68:71], a[208:211], a[228:231], a[68:71]\n" \
"ds_read_b64_tr_b16 a[250:251], v38 offset:9216\n" \
"v_cmp_gt_u32_e64 vcc, 16, v231\n" \
"v_mfma_f32_16x16x32_bf16 a[72:75], a[144:147], a[168:171], a[72:75]\n" \
"v_or3_b32 v213, s7, v213, v219\n" \
"v_and_b32_e64 v214, 32, v20\n" \
"v_mfma_f32_16x16x32_bf16 a[72:75], a[208:211], a[232:235], a[72:75]\n" \
"s_mov_b32 s76, s20\n" \
"s_and_b32 s77, s21, 0xffff\n" \
"v_mfma_f32_16x16x32_bf16 a[76:79], a[144:147], a[172:175], a[76:79]\n" \
"s_mov_b32 s78, 0x480\n" \
"s_lshl_b32 s6, s6, 5\n" \
"v_mfma_f32_16x16x32_bf16 a[76:79], a[208:211], a[236:239], a[76:79]\n" \
"buffer_load_ushort v210, v195, s[76:79], 0 offen\n" \
"v_mfma_f32_16x16x32_bf16 a[92:95], a[148:151], a[172:175], a[92:95]\n" \
"ds_read_b64_tr_b16 a[188:189], v39 offset:0\n" \
"v_cndmask_b32_e64 v231, v233, v234, vcc\n" \
"v_mfma_f32_16x16x32_bf16 a[92:95], a[212:215], a[236:239], a[92:95]\n" \
"v_mul_u32_u24_e64 v213, 18, v213\n" \
"v_mov_b32_e32 v215, 0x100\n" \
"v_mfma_f32_16x16x32_bf16 a[88:91], a[148:151], a[168:171], a[88:91]\n" \
"s_mov_b32 s76, s8\n" \
"s_and_b32 s77, s9, 0xffff\n" \
"v_mfma_f32_16x16x32_bf16 a[88:91], a[212:215], a[232:235], a[88:91]\n" \
"s_mov_b32 s78, 0x240000\n" \
"ds_read_b64_tr_b16 a[190:191], v39 offset:1024\n" \
".fill (L_pb + 48864 - .) / 4, 4, 0xBF800000\n" \
"L_pp12:\n" \
"s_cbranch_execz L_pret\n" \
"v_mfma_f32_16x16x32_bf16 a[84:87], a[148:151], a[164:167], a[84:87]\n" \
"v_mov_b32_e32 v216, s6\n" \
"v_cmp_eq_u32_e64 vcc, 0, v214\n" \
"v_mfma_f32_16x16x32_bf16 a[84:87], a[212:215], a[228:231], a[84:87]\n" \
"ds_read_b64_tr_b16 a[252:253], v39 offset:8192\n" \
"v_and_or_b32 v213, v218, 1, v213\n" \
"v_mfma_f32_16x16x32_bf16 a[80:83], a[148:151], a[160:163], a[80:83]\n" \
"v_lshlrev_b32_e32 v212, 3, v212\n" \
"v_cndmask_b32_e32 v214, v215, v216, vcc\n" \
"v_mfma_f32_16x16x32_bf16 a[80:83], a[212:215], a[224:227], a[80:83]\n" \
"v_mad_u32_u24 v213, v213, s5, v214\n" \
"v_and_or_b32 v213, v20, 16, v213\n" \
"v_mfma_f32_16x16x32_bf16 a[96:99], a[152:155], a[160:163], a[96:99]\n" \
"ds_read_b64_tr_b16 a[254:255], v39 offset:9216\n" \
"v_add_u32_e32 v9, 0x4800, v213\n" \
"v_mfma_f32_16x16x32_bf16 a[96:99], a[216:219], a[224:227], a[96:99]\n" \
"v_mul_u32_u24_e32 v213, 0x120, v218\n" \
"v_and_or_b32 v10, v212, 24, v213\n" \
"v_mfma_f32_16x16x32_bf16 a[100:103], a[152:155], a[164:167], a[100:103]\n" \
"v_lshrrev_b32_e64 v213, 2, v20\n" \
"v_and_or_b32 v213, v213, 60, s4\n" \
"v_mfma_f32_16x16x32_bf16 a[100:103], a[216:219], a[228:231], a[100:103]\n" \
"v_lshl_or_b32 v212, s3, 9, v212\n" \
"v_mad_u32_u24 v4, v224, s5, v223\n" \
"v_mfma_f32_16x16x32_bf16 a[104:107], a[152:155], a[168:171], a[104:107]\n" \
"v_mad_u32_u24 v8, v232, s5, v231\n" \
"v_or_b32_e32 v18, 0x27000, v213\n" \
"v_mfma_f32_16x16x32_bf16 a[104:107], a[216:219], a[232:235], a[104:107]\n" \
"v_or_b32_e32 v19, 0x27800, v212\n" \
"v_mov_b32_e32 v215, 0x1000c0c\n" \
"v_mfma_f32_16x16x32_bf16 a[108:111], a[152:155], a[172:175], a[108:111]\n" \
"v_mov_b32_e32 v216, 0xc0c0100\n" \
"v_bfe_u32 v214, v20, 4, 1\n" \
"v_mfma_f32_16x16x32_bf16 a[108:111], a[216:219], a[236:239], a[108:111]\n" \
"v_lshrrev_b32_e32 v212, 8, v20\n" \
"v_cmp_eq_u32_e32 vcc, 0, v214\n" \
"s_waitcnt lgkmcnt(15)\n" \
"v_mfma_f32_16x16x32_bf16 a[124:127], a[156:159], a[172:175], a[124:127]\n" \
"v_bfe_u32 v213, v20, 4, 4\n" \
"v_mov_b32_e32 v217, 0xc0c0302\n" \
"v_mfma_f32_16x16x32_bf16 a[124:127], a[220:223], a[236:239], a[124:127]\n" \
"v_cndmask_b32_e32 v215, v215, v216, vcc\n" \
"v_mov_b32_e32 v216, 0x3020c0c\n" \
"v_mfma_f32_16x16x32_bf16 a[120:123], a[156:159], a[168:171], a[120:123]\n" \
"v_lshl_or_b32 v214, v212, 2, v214\n" \
"v_cndmask_b32_e64 v216, v216, v217, vcc\n" \
"v_mfma_f32_16x16x32_bf16 a[120:123], a[220:223], a[232:235], a[120:123]\n" \
"v_mov_b32_e32 v217, 0xc0c0c0c\n" \
"v_cmp_gt_u32_e64 vcc, v214, v213\n" \
"v_mfma_f32_16x16x32_bf16 a[116:119], a[156:159], a[164:167], a[116:119]\n" \
"v_or_b32_e32 v214, 2, v214\n" \
"s_lshr_b32 s4, s2, 5\n" \
"v_mfma_f32_16x16x32_bf16 a[116:119], a[220:223], a[228:231], a[116:119]\n" \
"v_cndmask_b32_e32 v40, v217, v215, vcc\n" \
"v_cndmask_b32_e32 v42, v215, v217, vcc\n" \
"v_mfma_f32_16x16x32_bf16 a[112:115], a[156:159], a[160:163], a[112:115]\n" \
"v_cmp_gt_u32_e32 vcc, v214, v213\n" \
"v_and_b32_e32 v214, 16, v20\n" \
"v_mfma_f32_16x16x32_bf16 a[112:115], a[220:223], a[224:227], a[112:115]\n" \
"s_waitcnt vmcnt(7) lgkmcnt(0)\n" \
"s_barrier\n" \
"v_mfma_f32_16x16x32_bf16 v[52:55], a[128:131], a[176:179], v[52:55]\n" \
"s_add_u32 m0, s30, 0x0\n" \
"s_movk_i32 s84, 0x200\n" \
"v_mfma_f32_16x16x32_bf16 v[52:55], a[192:195], a[240:243], v[52:55]\n" \
"buffer_load_dwordx4 v22, s[64:67], s84 offen offset:0 lds\n" \
"v_mfma_f32_16x16x32_bf16 v[56:59], a[128:131], a[180:183], v[56:59]\n" \
"ds_read_b64_tr_b16 a[160:161], v32 offset:32768\n" \
"s_movk_i32 s42, 0x3f80\n" \
"v_mfma_f32_16x16x32_bf16 v[56:59], a[192:195], a[244:247], v[56:59]\n" \
"s_movk_i32 s84, 0x1e00\n" \
"v_mfma_f32_16x16x32_bf16 v[60:63], a[128:131], a[184:187], v[60:63]\n" \
"buffer_load_dwordx4 v23, s[64:67], s84 offen offset:1024 lds\n" \
"v_mfma_f32_16x16x32_bf16 v[60:63], a[192:195], a[248:251], v[60:63]\n" \
"ds_read_b64_tr_b16 a[162:163], v32 offset:33792\n" \
"s_xor_b32 s5, s4, 15\n" \
"v_mfma_f32_16x16x32_bf16 v[64:67], a[128:131], a[188:191], v[64:67]\n" \
"ds_read_b64_tr_b16 a[224:225], v32 offset:40960\n" \
"v_mfma_f32_16x16x32_bf16 v[64:67], a[192:195], a[252:255], v[64:67]\n" \
"ds_read_b128 a[128:131], v28 offset:49152\n" \
"v_mfma_f32_16x16x32_bf16 v[80:83], a[132:135], a[188:191], v[80:83]\n" \
"ds_read_b128 a[192:195], v29 offset:49152\n" \
"v_mfma_f32_16x16x32_bf16 v[80:83], a[196:199], a[252:255], v[80:83]\n" \
"s_movk_i32 s84, 0x3a00\n" \
"v_mfma_f32_16x16x32_bf16 v[76:79], a[132:135], a[184:187], v[76:79]\n" \
"buffer_load_dwordx4 v22, s[64:67], s84 offen offset:2048 lds\n" \
"v_mfma_f32_16x16x32_bf16 v[76:79], a[196:199], a[248:251], v[76:79]\n" \
"s_movk_i32 s84, 0x5600\n" \
"v_mfma_f32_16x16x32_bf16 v[72:75], a[132:135], a[180:183], v[72:75]\n" \
"buffer_load_dwordx4 v23, s[64:67], s84 offen offset:3072 lds\n" \
"v_mfma_f32_16x16x32_bf16 v[72:75], a[196:199], a[244:247], v[72:75]\n" \
"s_add_u32 m0, s31, 0x0\n" \
"s_mov_b32 s84, 0x200000\n" \
"v_mfma_f32_16x16x32_bf16 v[68:71], a[132:135], a[176:179], v[68:71]\n" \
"buffer_load_dwordx4 v26, s[68:71], s84 offen offset:0 lds\n" \
"v_mfma_f32_16x16x32_bf16 v[68:71], a[196:199], a[240:243], v[68:71]\n" \
"ds_read_b128 a[132:135], v28 offset:51200\n" \
"v_mfma_f32_16x16x32_bf16 v[84:87], a[136:139], a[176:179], v[84:87]\n" \
"ds_read_b128 a[196:199], v29 offset:51200\n" \
"v_mfma_f32_16x16x32_bf16 v[84:87], a[200:203], a[240:243], v[84:87]\n" \
"s_addk_i32 s84, 0x7c00\n" \
"v_mfma_f32_16x16x32_bf16 v[88:91], a[136:139], a[180:183], v[88:91]\n" \
"buffer_load_dwordx4 v26, s[68:71], s84 offen offset:1024 lds\n" \
"v_mfma_f32_16x16x32_bf16 v[88:91], a[200:203], a[244:247], v[88:91]\n" \
"ds_read_b64_tr_b16 a[226:227], v32 offset:41984\n" \
"v_mfma_f32_16x16x32_bf16 v[92:95], a[136:139], a[184:187], v[92:95]\n" \
"s_addk_i32 s84, 0x7c00\n" \
"v_mfma_f32_16x16x32_bf16 v[92:95], a[200:203], a[248:251], v[92:95]\n" \
"buffer_load_dwordx4 v27, s[68:71], s84 offen offset:2048 lds\n" \
"v_mfma_f32_16x16x32_bf16 v[96:99], a[136:139], a[188:191], v[96:99]\n" \
"ds_read_b64_tr_b16 a[164:165], v33 offset:32768\n" \
"v_mfma_f32_16x16x32_bf16 v[96:99], a[200:203], a[252:255], v[96:99]\n" \
"ds_read_b128 a[136:139], v28 offset:53248\n" \
"v_mfma_f32_16x16x32_bf16 v[112:115], a[140:143], a[188:191], v[112:115]\n" \
"ds_read_b128 a[200:203], v29 offset:53248\n" \
"v_mfma_f32_16x16x32_bf16 v[112:115], a[204:207], a[252:255], v[112:115]\n" \
"s_addk_i32 s84, 0x7c00\n" \
"s_nop 0\n" \
"v_mfma_f32_16x16x32_bf16 v[108:111], a[140:143], a[184:187], v[108:111]\n" \
"buffer_load_dwordx4 v27, s[68:71], s84 offen offset:3072 lds\n" \
"v_mfma_f32_16x16x32_bf16 v[108:111], a[204:207], a[248:251], v[108:111]\n" \
"s_add_u32 m0, s30, 0x1000\n" \
"s_mov_b32 s84, 0x8200\n" \
"v_mfma_f32_16x16x32_bf16 v[104:107], a[140:143], a[180:183], v[104:107]\n" \
"buffer_load_dwordx4 v22, s[64:67], s84 offen offset:0 lds\n" \
"v_mfma_f32_16x16x32_bf16 v[104:107], a[204:207], a[244:247], v[104:107]\n" \
"ds_read_b64_tr_b16 a[166:167], v33 offset:33792\n" \
"v_mfma_f32_16x16x32_bf16 v[100:103], a[140:143], a[176:179], v[100:103]\n" \
"ds_read_b64_tr_b16 a[228:229], v33 offset:40960\n" \
"v_mfma_f32_16x16x32_bf16 v[100:103], a[204:207], a[240:243], v[100:103]\n" \
"ds_read_b128 a[140:143], v28 offset:55296\n" \
"v_mfma_f32_16x16x32_bf16 v[116:119], a[144:147], a[176:179], v[116:119]\n" \
"ds_read_b128 a[204:207], v29 offset:55296\n" \
"v_mfma_f32_16x16x32_bf16 v[116:119], a[208:211], a[240:243], v[116:119]\n" \
"s_addk_i32 s84, 0x1c00\n" \
"v_mfma_f32_16x16x32_bf16 v[120:123], a[144:147], a[180:183], v[120:123]\n" \
"buffer_load_dwordx4 v23, s[64:67], s84 offen offset:1024 lds\n" \
"v_mfma_f32_16x16x32_bf16 v[120:123], a[208:211], a[244:247], v[120:123]\n" \
"s_addk_i32 s84, 0x1c00\n" \
"v_mfma_f32_16x16x32_bf16 v[124:127], a[144:147], a[184:187], v[124:127]\n" \
"buffer_load_dwordx4 v22, s[64:67], s84 offen offset:2048 lds\n" \
"v_mfma_f32_16x16x32_bf16 v[124:127], a[208:211], a[248:251], v[124:127]\n" \
"s_addk_i32 s84, 0x1c00\n" \
"s_nop 0\n" \
"v_mfma_f32_16x16x32_bf16 v[128:131], a[144:147], a[188:191], v[128:131]\n" \
"buffer_load_dwordx4 v23, s[64:67], s84 offen offset:3072 lds\n" \
"v_mfma_f32_16x16x32_bf16 v[128:131], a[208:211], a[252:255], v[128:131]\n" \
"ds_read_b128 a[144:147], v28 offset:32768\n" \
"v_mfma_f32_16x16x32_bf16 v[144:147], a[148:151], a[188:191], v[144:147]\n" \
"ds_read_b128 a[208:211], v29 offset:32768\n" \
"v_mfma_f32_16x16x32_bf16 v[144:147], a[212:215], a[252:255], v[144:147]\n" \
"s_add_u32 m0, s31, 0x4000\n" \
"s_mov_b32 s84, 0x200000\n" \
"v_mfma_f32_16x16x32_bf16 v[140:143], a[148:151], a[184:187], v[140:143]\n" \
"buffer_load_dwordx4 v26, s[72:75], s84 offen offset:0 lds\n" \
"v_mfma_f32_16x16x32_bf16 v[140:143], a[212:215], a[248:251], v[140:143]\n" \
"ds_read_b64_tr_b16 a[230:231], v33 offset:41984\n" \
"v_mfma_f32_16x16x32_bf16 v[136:139], a[148:151], a[180:183], v[136:139]\n" \
"s_addk_i32 s84, 0x7c00\n" \
"s_nop 0\n" \
"v_mfma_f32_16x16x32_bf16 v[136:139], a[212:215], a[244:247], v[136:139]\n" \
"buffer_load_dwordx4 v26, s[72:75], s84 offen offset:1024 lds\n" \
"v_mfma_f32_16x16x32_bf16 v[132:135], a[148:151], a[176:179], v[132:135]\n" \
"ds_read_b64_tr_b16 a[168:169], v34 offset:32768\n" \
"v_mfma_f32_16x16x32_bf16 v[132:135], a[212:215], a[240:243], v[132:135]\n" \
"ds_read_b128 a[148:151], v28 offset:34816\n" \
"v_mfma_f32_16x16x32_bf16 v[148:151], a[152:155], a[176:179], v[148:151]\n" \
"ds_read_b128 a[212:215], v29 offset:34816\n" \
"v_mfma_f32_16x16x32_bf16 v[148:151], a[216:219], a[240:243], v[148:151]\n" \
"ds_read_b64_tr_b16 a[170:171], v34 offset:33792\n" \
"v_mfma_f32_16x16x32_bf16 v[152:155], a[152:155], a[180:183], v[152:155]\n" \
"ds_read_b64_tr_b16 a[232:233], v34 offset:40960\n" \
"v_mfma_f32_16x16x32_bf16 v[152:155], a[216:219], a[244:247], v[152:155]\n" \
"ds_read_b64_tr_b16 a[234:235], v34 offset:41984\n" \
"v_mfma_f32_16x16x32_bf16 v[156:159], a[152:155], a[184:187], v[156:159]\n" \
"ds_read_b64_tr_b16 a[172:173], v35 offset:32768\n" \
"v_mfma_f32_16x16x32_bf16 v[156:159], a[216:219], a[248:251], v[156:159]\n" \
"ds_read_b64_tr_b16 a[174:175], v35 offset:33792\n" \
"v_mfma_f32_16x16x32_bf16 v[160:163], a[152:155], a[188:191], v[160:163]\n" \
"ds_read_b64_tr_b16 a[236:237], v35 offset:40960\n" \
"v_mfma_f32_16x16x32_bf16 v[160:163], a[216:219], a[252:255], v[160:163]\n" \
"ds_read_b128 a[152:155], v28 offset:36864\n" \
"v_mfma_f32_16x16x32_bf16 v[176:179], a[156:159], a[188:191], v[176:179]\n" \
"ds_read_b128 a[216:219], v29 offset:36864\n" \
"v_mfma_f32_16x16x32_bf16 v[176:179], a[220:223], a[252:255], v[176:179]\n" \
"ds_read_b64_tr_b16 a[238:239], v35 offset:41984\n" \
"v_mfma_f32_16x16x32_bf16 v[172:175], a[156:159], a[184:187], v[172:175]\n" \
"s_cmpk_lt_u32 s3, 2\n" \
"s_cselect_b32 s6, s5, s4\n" \
"v_mfma_f32_16x16x32_bf16 v[172:175], a[220:223], a[248:251], v[172:175]\n" \
"s_lshl_b32 s7, s6, 7\n" \
"v_and_b32_e32 v213, 0x320, v20\n" \
"v_mfma_f32_16x16x32_bf16 v[168:171], a[156:159], a[180:183], v[168:171]\n" \
"s_movk_i32 s41, 0x100\n" \
"v_lshlrev_b32_e64 v214, v214, s42\n" \
"v_mfma_f32_16x16x32_bf16 v[168:171], a[220:223], a[244:247], v[168:171]\n" \
"v_mov_b32_e32 v215, 0\n" \
"s_add_i32 s40, s7, -16\n" \
"v_mfma_f32_16x16x32_bf16 v[164:167], a[156:159], a[176:179], v[164:167]\n" \
"v_cndmask_b32_e32 v41, v217, v216, vcc\n" \
"v_cndmask_b32_e32 v43, v216, v217, vcc\n" \
"v_mfma_f32_16x16x32_bf16 v[164:167], a[220:223], a[240:243], v[164:167]\n" \
"ds_read_b128 a[156:159], v28 offset:38912\n" \
"ds_read_b128 a[220:223], v29 offset:38912\n" \
"s_waitcnt lgkmcnt(15)\n" \
"v_mfma_f32_16x16x32_bf16 a[0:3], a[128:131], a[160:163], a[0:3]\n" \
"s_addk_i32 s84, 0x7c00\n" \
"v_mfma_f32_16x16x32_bf16 a[0:3], a[192:195], a[224:227], a[0:3]\n" \
"buffer_load_dwordx4 v27, s[72:75], s84 offen offset:2048 lds\n" \
"v_mfma_f32_16x16x32_bf16 a[4:7], a[128:131], a[164:167], a[4:7]\n" \
"ds_read_b64_tr_b16 a[176:177], v36 offset:32768\n" \
"v_cmp_eq_u32_e32 vcc, s41, v213\n" \
"s_waitcnt lgkmcnt(15)\n" \
"v_mfma_f32_16x16x32_bf16 a[4:7], a[192:195], a[228:231], a[4:7]\n" \
"s_addk_i32 s84, 0x7c00\n" \
"s_waitcnt lgkmcnt(11)\n" \
"v_mfma_f32_16x16x32_bf16 a[8:11], a[128:131], a[168:171], a[8:11]\n" \
"buffer_load_dwordx4 v27, s[72:75], s84 offen offset:3072 lds\n" \
"s_waitcnt lgkmcnt(9)\n" \
"v_mfma_f32_16x16x32_bf16 a[8:11], a[192:195], a[232:235], a[8:11]\n" \
"ds_read_b64_tr_b16 a[178:179], v36 offset:33792\n" \
"s_mov_b32 s41, 0\n" \
"s_waitcnt lgkmcnt(8)\n" \
"v_mfma_f32_16x16x32_bf16 a[12:15], a[128:131], a[172:175], a[12:15]\n" \
"ds_read_b64_tr_b16 a[240:241], v36 offset:40960\n" \
"s_and_b32 s43, s3, 1\n" \
"s_waitcnt lgkmcnt(5)\n" \
"v_mfma_f32_16x16x32_bf16 a[12:15], a[192:195], a[236:239], a[12:15]\n" \
"v_cndmask_b32_e32 v46, v215, v214, vcc\n" \
"v_and_b32_e32 v213, 0x330, v20\n" \
"v_mfma_f32_16x16x32_bf16 a[28:31], a[132:135], a[172:175], a[28:31]\n" \
"s_cmp_lg_u32 s7, 0\n" \
"s_cselect_b32 s40, s40, s41\n" \
"v_mfma_f32_16x16x32_bf16 a[28:31], a[196:199], a[236:239], a[28:31]\n" \
"ds_read_b64_tr_b16 a[242:243], v36 offset:41984\n" \
"s_mov_b32 s42, 0xff800000\n" \
"v_mfma_f32_16x16x32_bf16 a[24:27], a[132:135], a[168:171], a[24:27]\n" \
"s_cmp_lg_u32 s7, 0\n" \
"s_cselect_b32 s47, s41, s42\n" \
"v_mfma_f32_16x16x32_bf16 a[24:27], a[196:199], a[232:235], a[24:27]\n" \
"s_lshl_b32 s41, s43, 6\n" \
"v_lshrrev_b32_e32 v215, 6, v20\n" \
"v_mfma_f32_16x16x32_bf16 a[20:23], a[132:135], a[164:167], a[20:23]\n" \
"v_mov_b32_e32 v214, 0x3f80\n" \
"v_cmp_eq_u32_e64 vcc, 32, v213\n" \
"v_mfma_f32_16x16x32_bf16 a[20:23], a[196:199], a[228:231], a[20:23]\n" \
"ds_read_b64_tr_b16 a[180:181], v37 offset:32768\n" \
"s_or_b32 s7, s41, s7\n" \
"v_mfma_f32_16x16x32_bf16 a[16:19], a[132:135], a[160:163], a[16:19]\n" \
"v_lshrrev_b32_e32 v213, 7, v20\n" \
"v_bfe_u32 v216, v20, 6, 2\n" \
"v_mfma_f32_16x16x32_bf16 a[16:19], a[196:199], a[224:227], a[16:19]\n" \
"v_lshrrev_b32_e64 v219, 1, v20\n" \
"v_bfe_u32 v215, v215, 1, 1\n" \
"v_mfma_f32_16x16x32_bf16 a[32:35], a[136:139], a[160:163], a[32:35]\n" \
"s_waitcnt vmcnt(16)\n" \
"v_or_b32_e32 v204, v210, v204\n" \
"v_or_b32_e64 v206, v210, v206\n" \
"v_mfma_f32_16x16x32_bf16 a[32:35], a[200:203], a[224:227], a[32:35]\n" \
"v_mov_b32_e64 v210, 0\n" \
"s_add_u32 s88, s35, 0x1000\n" \
"v_mfma_f32_16x16x32_bf16 a[36:39], a[136:139], a[164:167], a[36:39]\n" \
"ds_read_b64_tr_b16 a[182:183], v37 offset:33792\n" \
"v_cndmask_b32_e64 v47, 0, v214, vcc\n" \
"v_mfma_f32_16x16x32_bf16 a[36:39], a[200:203], a[228:231], a[36:39]\n" \
"ds_read_b64_tr_b16 a[244:245], v37 offset:40960\n" \
"v_or_b32_e64 v214, s7, v213\n" \
"v_mfma_f32_16x16x32_bf16 a[40:43], a[136:139], a[168:171], a[40:43]\n" \
"s_mov_b32 m0, s32\n" \
"v_lshl_or_b32 v217, s43, 3, v213\n" \
"v_mfma_f32_16x16x32_bf16 a[40:43], a[200:203], a[232:235], a[40:43]\n" \
"s_mov_b32 exec_lo, 0xffffffff\n" \
"s_mov_b32 exec_hi, 0x0\n" \
"global_load_lds_dword v[188:189], off\n" \
"s_mov_b64 exec, -1\n" \
"v_mfma_f32_16x16x32_bf16 a[44:47], a[136:139], a[172:175], a[44:47]\n" \
"ds_read_b64_tr_b16 a[246:247], v37 offset:41984\n" \
"v_lshlrev_b32_e64 v218, 10, v212\n" \
"v_mfma_f32_16x16x32_bf16 a[44:47], a[200:203], a[236:239], a[44:47]\n" \
"v_lshlrev_b32_e32 v216, 7, v216\n" \
"v_and_b32_e32 v219, 24, v219\n" \
"v_mfma_f32_16x16x32_bf16 a[60:63], a[140:143], a[172:175], a[60:63]\n" \
"ds_write_b128 v196, v[200:203]\n" \
"v_mfma_f32_16x16x32_bf16 a[60:63], a[204:207], a[236:239], a[60:63]\n" \
"v_and_or_b32 v213, v213, 2, v215\n" \
"v_add_u32_e64 v217, s40, v217\n" \
"v_mfma_f32_16x16x32_bf16 a[56:59], a[140:143], a[168:171], a[56:59]\n" \
"ds_read_b64_tr_b16 a[184:185], v38 offset:32768\n" \
"v_or3_b32 v216, v216, v218, v219\n" \
"v_mfma_f32_16x16x32_bf16 a[56:59], a[204:207], a[232:235], a[56:59]\n" \
"v_lshlrev_b32_e64 v213, 5, v213\n" \
"s_mov_b32 s40, 0x14000\n" \
"v_mfma_f32_16x16x32_bf16 a[52:55], a[140:143], a[164:167], a[52:55]\n" \
"s_lshl_b32 s7, s2, 2\n" \
"v_or3_b32 v48, v216, v213, s40\n" \
"v_mfma_f32_16x16x32_bf16 a[52:55], a[204:207], a[228:231], a[52:55]\n" \
"ds_read_b64_tr_b16 a[186:187], v38 offset:33792\n" \
"s_lshl_b32 s40, s43, 2\n" \
"v_mfma_f32_16x16x32_bf16 a[48:51], a[140:143], a[160:163], a[48:51]\n" \
"s_lshl_b32 s6, s6, 3\n" \
"v_or_b32_e32 v213, s40, v212\n" \
"v_mfma_f32_16x16x32_bf16 a[48:51], a[204:207], a[224:227], a[48:51]\n" \
"s_or_b32 s6, s6, s40\n" \
"s_and_b32 s40, s7, 0x7c\n" \
"v_mfma_f32_16x16x32_bf16 a[64:67], a[144:147], a[160:163], a[64:67]\n" \
"ds_write_b128 v197, v[204:207]\n" \
"v_mfma_f32_16x16x32_bf16 a[64:67], a[208:211], a[224:227], a[64:67]\n" \
"ds_read_b64_tr_b16 a[248:249], v38 offset:40960\n" \
"s_or_b32 s40, s40, s3\n" \
"v_mfma_f32_16x16x32_bf16 a[68:71], a[144:147], a[164:167], a[68:71]\n" \
"s_cmpk_lt_u32 s40, 64\n" \
"s_cselect_b32 s41, s4, s5\n" \
"v_mfma_f32_16x16x32_bf16 a[68:71], a[208:211], a[228:231], a[68:71]\n" \
"ds_read_b64_tr_b16 a[250:251], v38 offset:41984\n" \
"s_lshl_b32 s40, s40, 1\n" \
"v_mfma_f32_16x16x32_bf16 a[72:75], a[144:147], a[168:171], a[72:75]\n" \
"s_lshl_b32 s41, s41, 7\n" \
"s_and_b32 s40, s40, 0x7e\n" \
"v_mfma_f32_16x16x32_bf16 a[72:75], a[208:211], a[232:235], a[72:75]\n" \
"s_or_b32 s40, s41, s40\n" \
"s_mulk_i32 s40, 0x480\n" \
"v_mfma_f32_16x16x32_bf16 a[76:79], a[144:147], a[172:175], a[76:79]\n" \
"v_add_u32_e64 v49, s40, v20\n" \
"s_mov_b32 s40, 0xc000\n" \
"v_mfma_f32_16x16x32_bf16 a[76:79], a[208:211], a[236:239], a[76:79]\n" \
"ds_read_b64_tr_b16 a[188:189], v39 offset:32768\n" \
"s_and_b32 s7, s7, 28\n" \
"v_mfma_f32_16x16x32_bf16 a[92:95], a[148:151], a[172:175], a[92:95]\n" \
"ds_write_b128 v198, v[208:211]\n" \
"v_mfma_f32_16x16x32_bf16 a[92:95], a[212:215], a[236:239], a[92:95]\n" \
"s_bfe_u32 s45, s2, 0x20003\n" \
"s_mov_b32 s41, 0x8000\n" \
"v_mfma_f32_16x16x32_bf16 a[88:91], a[148:151], a[168:171], a[88:91]\n" \
"ds_read_b64_tr_b16 a[190:191], v39 offset:33792\n" \
"v_mfma_f32_16x16x32_bf16 a[88:91], a[212:215], a[232:235], a[88:91]\n" \
"s_cmp_lg_u32 s4, 0\n" \
"s_cselect_b32 s51, s40, s41\n" \
"v_mfma_f32_16x16x32_bf16 a[84:87], a[148:151], a[164:167], a[84:87]\n" \
"s_movk_i32 s40, 0x4000\n" \
"s_lshr_b32 s44, s3, 1\n" \
"v_mfma_f32_16x16x32_bf16 a[84:87], a[212:215], a[228:231], a[84:87]\n" \
"ds_read_b64_tr_b16 a[252:253], v39 offset:40960\n" \
"s_or_b32 s46, s7, s45\n" \
"v_mfma_f32_16x16x32_bf16 a[80:83], a[148:151], a[160:163], a[80:83]\n" \
"s_cmp_lg_u32 s4, 0\n" \
"s_cselect_b32 s53, s41, s40\n" \
"v_mfma_f32_16x16x32_bf16 a[80:83], a[212:215], a[224:227], a[80:83]\n" \
"s_lshl_b32 s40, s44, 15\n" \
"s_lshl_b32 s41, s43, 14\n" \
"v_mfma_f32_16x16x32_bf16 a[96:99], a[152:155], a[160:163], a[96:99]\n" \
"ds_read_b64_tr_b16 a[254:255], v39 offset:41984\n" \
"s_lshl_b32 s48, s46, 20\n" \
"v_mfma_f32_16x16x32_bf16 a[96:99], a[216:219], a[224:227], a[96:99]\n" \
"s_or_b32 s41, s40, s41\n" \
"s_add_i32 s49, s48, 0x1000000\n" \
"v_mfma_f32_16x16x32_bf16 a[100:103], a[152:155], a[164:167], a[100:103]\n" \
"s_or_b32 s42, s49, s41\n" \
"s_lshl_b32 s4, s4, 16\n" \
"v_mfma_f32_16x16x32_bf16 a[100:103], a[216:219], a[228:231], a[100:103]\n" \
"s_or_b32 s41, s42, s4\n" \
"s_lshl_b32 s4, s5, 16\n" \
"v_mfma_f32_16x16x32_bf16 a[104:107], a[152:155], a[168:171], a[104:107]\n" \
"s_mul_i32 s5, s6, 0x24800\n" \
"s_lshl_b32 s6, s46, 9\n" \
"v_mfma_f32_16x16x32_bf16 a[104:107], a[216:219], a[232:235], a[104:107]\n" \
"s_lshl_b32 s7, s7, 6\n" \
"s_add_i32 s6, s5, s6\n" \
"v_mfma_f32_16x16x32_bf16 a[108:111], a[152:155], a[172:175], a[108:111]\n" \
"s_or_b32 s5, s5, s7\n" \
"s_lshl_b32 s7, s45, 2\n" \
"v_mfma_f32_16x16x32_bf16 a[108:111], a[216:219], a[236:239], a[108:111]\n" \
"s_or_b32 s5, s5, s7\n" \
"s_lshl_b32 s7, s3, 12\n" \
"s_waitcnt lgkmcnt(15)\n" \
"v_mfma_f32_16x16x32_bf16 a[124:127], a[156:159], a[172:175], a[124:127]\n" \
"s_or_b32 s50, s49, s7\n" \
"s_lshl_b32 s7, s3, 11\n" \
"v_mfma_f32_16x16x32_bf16 a[124:127], a[220:223], a[236:239], a[124:127]\n" \
"v_lshrrev_b32_e64 v215, 4, v20\n" \
"v_bfe_u32 v216, v20, 4, 3\n" \
"v_mfma_f32_16x16x32_bf16 a[120:123], a[156:159], a[168:171], a[120:123]\n" \
"s_or_b32 s42, s42, s4\n" \
"s_lshl_b32 s4, s43, 11\n" \
"v_mfma_f32_16x16x32_bf16 a[120:123], a[220:223], a[232:235], a[120:123]\n" \
"s_add_i32 s52, s48, s7\n" \
"s_add_i32 s53, s53, s7\n" \
"v_mfma_f32_16x16x32_bf16 a[116:119], a[156:159], a[164:167], a[116:119]\n" \
"v_mul_u32_u24_e32 v214, 0x480, v214\n" \
"v_bitop3_b32 v218, v212, v215, 7 bitop3:0x78\n" \
"v_mfma_f32_16x16x32_bf16 a[116:119], a[220:223], a[228:231], a[116:119]\n" \
"v_bitop3_b32 v212, v212, v216, 4 bitop3:0x36\n" \
"s_lshl_b32 s7, s3, 10\n" \
"v_mfma_f32_16x16x32_bf16 a[112:115], a[156:159], a[160:163], a[112:115]\n" \
"s_or_b32 s4, s40, s4\n" \
"v_lshl_or_b32 v25, v212, 4, v214\n" \
"v_mfma_f32_16x16x32_bf16 a[112:115], a[220:223], a[224:227], a[112:115]\n" \
"s_waitcnt vmcnt(1) lgkmcnt(0)\n" \
"s_barrier\n" \
"v_mfma_f32_16x16x32_bf16 v[52:55], a[128:131], a[176:179], v[52:55]\n" \
"s_add_u32 m0, s30, 0x8000\n" \
"s_movk_i32 s84, 0x280\n" \
"v_mfma_f32_16x16x32_bf16 v[52:55], a[192:195], a[240:243], v[52:55]\n" \
"buffer_load_dwordx4 v22, s[64:67], s84 offen offset:0 lds\n" \
"v_mfma_f32_16x16x32_bf16 v[56:59], a[128:131], a[180:183], v[56:59]\n" \
"s_movk_i32 s84, 0x1e80\n" \
"v_mfma_f32_16x16x32_bf16 v[56:59], a[192:195], a[244:247], v[56:59]\n" \
"buffer_load_dwordx4 v23, s[64:67], s84 offen offset:1024 lds\n" \
"v_mfma_f32_16x16x32_bf16 v[60:63], a[128:131], a[184:187], v[60:63]\n" \
"ds_read_b64_tr_b16 a[160:161], v32 offset:0\n" \
"v_mul_u32_u24_e32 v212, 0x480, v217\n" \
"v_mfma_f32_16x16x32_bf16 v[60:63], a[192:195], a[248:251], v[60:63]\n" \
"s_movk_i32 s84, 0x3a80\n" \
"v_mfma_f32_16x16x32_bf16 v[64:67], a[128:131], a[188:191], v[64:67]\n" \
"buffer_load_dwordx4 v22, s[64:67], s84 offen offset:2048 lds\n" \
"v_mfma_f32_16x16x32_bf16 v[64:67], a[192:195], a[252:255], v[64:67]\n" \
"ds_read_b128 a[128:131], v28 offset:16384\n" \
"v_bitop3_b32 v213, v215, v213, 7 bitop3:0x6c\n" \
"v_mfma_f32_16x16x32_bf16 v[80:83], a[132:135], a[188:191], v[80:83]\n" \
"ds_read_b128 a[192:195], v29 offset:16384\n" \
"s_or_b32 s40, s7, 0x14000\n" \
"v_mfma_f32_16x16x32_bf16 v[80:83], a[196:199], a[252:255], v[80:83]\n" \
"s_movk_i32 s84, 0x5680\n" \
"v_mfma_f32_16x16x32_bf16 v[76:79], a[132:135], a[184:187], v[76:79]\n" \
"buffer_load_dwordx4 v23, s[64:67], s84 offen offset:3072 lds\n" \
"v_mfma_f32_16x16x32_bf16 v[76:79], a[196:199], a[248:251], v[76:79]\n" \
"s_add_u32 m0, s31, 0x8000\n" \
"s_mov_b32 s84, 0x280000\n" \
"v_mfma_f32_16x16x32_bf16 v[72:75], a[132:135], a[180:183], v[72:75]\n" \
"buffer_load_dwordx4 v26, s[68:71], s84 offen offset:0 lds\n" \
"v_mfma_f32_16x16x32_bf16 v[72:75], a[196:199], a[244:247], v[72:75]\n" \
"ds_read_b64_tr_b16 a[162:163], v32 offset:1024\n" \
"s_lshl_b32 s43, s44, 13\n" \
"v_mfma_f32_16x16x32_bf16 v[68:71], a[132:135], a[176:179], v[68:71]\n" \
"ds_read_b64_tr_b16 a[224:225], v32 offset:8192\n" \
"s_mulk_i32 s44, 0xe800\n" \
"v_mfma_f32_16x16x32_bf16 v[68:71], a[196:199], a[240:243], v[68:71]\n" \
"ds_read_b128 a[132:135], v28 offset:18432\n" \
"s_lshl_b32 s7, s3, 8\n" \
"v_mfma_f32_16x16x32_bf16 v[84:87], a[136:139], a[176:179], v[84:87]\n" \
"ds_read_b128 a[196:199], v29 offset:18432\n" \
"v_lshl_or_b32 v24, v218, 4, v214\n" \
"v_mfma_f32_16x16x32_bf16 v[84:87], a[200:203], a[240:243], v[84:87]\n" \
"s_addk_i32 s84, 0x7c00\n" \
"v_mfma_f32_16x16x32_bf16 v[88:91], a[136:139], a[180:183], v[88:91]\n" \
"buffer_load_dwordx4 v26, s[68:71], s84 offen offset:1024 lds\n" \
"v_mfma_f32_16x16x32_bf16 v[88:91], a[200:203], a[244:247], v[88:91]\n" \
"s_addk_i32 s84, 0x7c00\n" \
"v_mfma_f32_16x16x32_bf16 v[92:95], a[136:139], a[184:187], v[92:95]\n" \
"buffer_load_dwordx4 v27, s[68:71], s84 offen offset:2048 lds\n" \
"v_mfma_f32_16x16x32_bf16 v[92:95], a[200:203], a[248:251], v[92:95]\n" \
"s_addk_i32 s84, 0x7c00\n" \
"v_mfma_f32_16x16x32_bf16 v[96:99], a[136:139], a[188:191], v[96:99]\n" \
"buffer_load_dwordx4 v27, s[68:71], s84 offen offset:3072 lds\n" \
"v_mfma_f32_16x16x32_bf16 v[96:99], a[200:203], a[252:255], v[96:99]\n" \
"ds_read_b128 a[136:139], v28 offset:20480\n" \
"v_lshl_or_b32 v21, v213, 4, v212\n" \
"v_mfma_f32_16x16x32_bf16 v[112:115], a[140:143], a[188:191], v[112:115]\n" \
"ds_read_b128 a[200:203], v29 offset:20480\n" \
"s_add_i32 s44, s44, 0x17000\n" \
"v_mfma_f32_16x16x32_bf16 v[112:115], a[204:207], a[252:255], v[112:115]\n" \
"s_add_u32 m0, s30, 0x9000\n" \
"s_mov_b32 s84, 0x8280\n" \
"v_mfma_f32_16x16x32_bf16 v[108:111], a[140:143], a[184:187], v[108:111]\n" \
"buffer_load_dwordx4 v22, s[64:67], s84 offen offset:0 lds\n" \
"v_mfma_f32_16x16x32_bf16 v[108:111], a[204:207], a[248:251], v[108:111]\n" \
"s_addk_i32 s84, 0x1c00\n" \
"v_mfma_f32_16x16x32_bf16 v[104:107], a[140:143], a[180:183], v[104:107]\n" \
"buffer_load_dwordx4 v23, s[64:67], s84 offen offset:1024 lds\n" \
"v_mfma_f32_16x16x32_bf16 v[104:107], a[204:207], a[244:247], v[104:107]\n" \
"s_addk_i32 s84, 0x1c00\n" \
"v_mfma_f32_16x16x32_bf16 v[100:103], a[140:143], a[176:179], v[100:103]\n" \
"buffer_load_dwordx4 v22, s[64:67], s84 offen offset:2048 lds\n" \
"v_mfma_f32_16x16x32_bf16 v[100:103], a[204:207], a[240:243], v[100:103]\n" \
"ds_read_b128 a[140:143], v28 offset:22528\n" \
"s_or_b32 s45, s7, 0x27000\n" \
"v_mfma_f32_16x16x32_bf16 v[116:119], a[144:147], a[176:179], v[116:119]\n" \
"ds_read_b128 a[204:207], v29 offset:22528\n" \
"s_or_b32 s46, s4, 0x10000\n" \
"v_mfma_f32_16x16x32_bf16 v[116:119], a[208:211], a[240:243], v[116:119]\n" \
"s_addk_i32 s84, 0x1c00\n" \
"v_mfma_f32_16x16x32_bf16 v[120:123], a[144:147], a[180:183], v[120:123]\n" \
"buffer_load_dwordx4 v23, s[64:67], s84 offen offset:3072 lds\n" \
"v_mfma_f32_16x16x32_bf16 v[120:123], a[208:211], a[244:247], v[120:123]\n" \
"s_add_u32 m0, s31, 0xc000\n" \
"s_mov_b32 s84, 0x280000\n" \
"v_mfma_f32_16x16x32_bf16 v[124:127], a[144:147], a[184:187], v[124:127]\n" \
"buffer_load_dwordx4 v26, s[72:75], s84 offen offset:0 lds\n" \
"v_mfma_f32_16x16x32_bf16 v[124:127], a[208:211], a[248:251], v[124:127]\n" \
"s_addk_i32 s84, 0x7c00\n" \
"v_mfma_f32_16x16x32_bf16 v[128:131], a[144:147], a[188:191], v[128:131]\n" \
"buffer_load_dwordx4 v26, s[72:75], s84 offen offset:1024 lds\n" \
"v_mfma_f32_16x16x32_bf16 v[128:131], a[208:211], a[252:255], v[128:131]\n" \
"ds_read_b128 a[144:147], v28 offset:0\n" \
"s_add_i32 s48, s6, 0x3000000\n" \
"v_mfma_f32_16x16x32_bf16 v[144:147], a[148:151], a[188:191], v[144:147]\n" \
"ds_read_b128 a[208:211], v29 offset:0\n" \
"s_add_i32 s49, s5, 0x3004000\n" \
"v_mfma_f32_16x16x32_bf16 v[144:147], a[212:215], a[252:255], v[144:147]\n" \
"ds_read_b128 v[200:203], v199 offset:0\n" \
"s_add_i32 s52, s52, 0x1010000\n" \
"v_mfma_f32_16x16x32_bf16 v[140:143], a[148:151], a[184:187], v[140:143]\n" \
"ds_read_b128 v[204:207], v199 offset:4096\n" \
"v_mfma_f32_16x16x32_bf16 v[140:143], a[212:215], a[248:251], v[140:143]\n" \
"ds_read_b64_tr_b16 a[226:227], v32 offset:9216\n" \
"v_mfma_f32_16x16x32_bf16 v[136:139], a[148:151], a[180:183], v[136:139]\n" \
"ds_read_b64_tr_b16 a[164:165], v33 offset:0\n" \
"v_mfma_f32_16x16x32_bf16 v[136:139], a[212:215], a[244:247], v[136:139]\n" \
"ds_read_b128 v[208:211], v199 offset:8192\n" \
"v_mfma_f32_16x16x32_bf16 v[132:135], a[148:151], a[176:179], v[132:135]\n" \
"ds_read_b64_tr_b16 a[166:167], v33 offset:1024\n" \
"v_mfma_f32_16x16x32_bf16 v[132:135], a[212:215], a[240:243], v[132:135]\n" \
"ds_read_b128 a[148:151], v28 offset:2048\n" \
"v_mfma_f32_16x16x32_bf16 v[148:151], a[152:155], a[176:179], v[148:151]\n" \
"ds_read_b128 a[212:215], v29 offset:2048\n" \
"v_mfma_f32_16x16x32_bf16 v[148:151], a[216:219], a[240:243], v[148:151]\n" \
"ds_read_b64_tr_b16 a[228:229], v33 offset:8192\n" \
"v_mfma_f32_16x16x32_bf16 v[152:155], a[152:155], a[180:183], v[152:155]\n" \
"ds_read_b64_tr_b16 a[230:231], v33 offset:9216\n" \
"v_mfma_f32_16x16x32_bf16 v[152:155], a[216:219], a[244:247], v[152:155]\n" \
"ds_read_b64_tr_b16 a[168:169], v34 offset:0\n" \
"v_mfma_f32_16x16x32_bf16 v[156:159], a[152:155], a[184:187], v[156:159]\n" \
"ds_read_b64_tr_b16 a[170:171], v34 offset:1024\n" \
"v_mfma_f32_16x16x32_bf16 v[156:159], a[216:219], a[248:251], v[156:159]\n" \
"ds_read_b64_tr_b16 a[232:233], v34 offset:8192\n" \
"v_mfma_f32_16x16x32_bf16 v[160:163], a[152:155], a[188:191], v[160:163]\n" \
"ds_read_b64_tr_b16 a[234:235], v34 offset:9216\n" \
"v_mfma_f32_16x16x32_bf16 v[160:163], a[216:219], a[252:255], v[160:163]\n" \
"ds_read_b128 a[152:155], v28 offset:4096\n" \
"v_mfma_f32_16x16x32_bf16 v[176:179], a[156:159], a[188:191], v[176:179]\n" \
"ds_read_b128 a[216:219], v29 offset:4096\n" \
"v_mfma_f32_16x16x32_bf16 v[176:179], a[220:223], a[252:255], v[176:179]\n" \
"ds_read_b64_tr_b16 a[172:173], v35 offset:0\n" \
"v_mfma_f32_16x16x32_bf16 v[172:175], a[156:159], a[184:187], v[172:175]\n" \
"ds_read_b64_tr_b16 a[174:175], v35 offset:1024\n" \
"v_mfma_f32_16x16x32_bf16 v[172:175], a[220:223], a[248:251], v[172:175]\n" \
"ds_read_b64_tr_b16 a[236:237], v35 offset:8192\n" \
"v_mfma_f32_16x16x32_bf16 v[168:171], a[156:159], a[180:183], v[168:171]\n" \
"ds_read_b64_tr_b16 a[238:239], v35 offset:9216\n" \
"v_mfma_f32_16x16x32_bf16 v[168:171], a[220:223], a[244:247], v[168:171]\n" \
"v_mfma_f32_16x16x32_bf16 v[164:167], a[156:159], a[176:179], v[164:167]\n" \
"v_mfma_f32_16x16x32_bf16 v[164:167], a[220:223], a[240:243], v[164:167]\n" \
"ds_read_b128 a[156:159], v28 offset:6144\n" \
"ds_read_b128 a[220:223], v29 offset:6144\n" \
"s_waitcnt lgkmcnt(15)\n" \
"v_mfma_f32_16x16x32_bf16 a[0:3], a[128:131], a[160:163], a[0:3]\n" \
"s_addk_i32 s84, 0x7c00\n" \
"v_mfma_f32_16x16x32_bf16 a[0:3], a[192:195], a[224:227], a[0:3]\n" \
"buffer_load_dwordx4 v27, s[72:75], s84 offen offset:2048 lds\n" \
"v_mfma_f32_16x16x32_bf16 a[4:7], a[128:131], a[164:167], a[4:7]\n" \
"ds_read_b64_tr_b16 a[176:177], v36 offset:0\n" \
"s_waitcnt lgkmcnt(13)\n" \
"v_mfma_f32_16x16x32_bf16 a[4:7], a[192:195], a[228:231], a[4:7]\n" \
"s_addk_i32 s84, 0x7c00\n" \
"s_waitcnt lgkmcnt(11)\n" \
"v_mfma_f32_16x16x32_bf16 a[8:11], a[128:131], a[168:171], a[8:11]\n" \
"buffer_load_dwordx4 v27, s[72:75], s84 offen offset:3072 lds\n" \
"s_waitcnt lgkmcnt(9)\n" \
"v_mfma_f32_16x16x32_bf16 a[8:11], a[192:195], a[232:235], a[8:11]\n" \
"ds_read_b64_tr_b16 a[178:179], v36 offset:1024\n" \
"s_waitcnt lgkmcnt(6)\n" \
"v_mfma_f32_16x16x32_bf16 a[12:15], a[128:131], a[172:175], a[12:15]\n" \
"ds_read_b64_tr_b16 a[240:241], v36 offset:8192\n" \
"s_waitcnt lgkmcnt(5)\n" \
"v_mfma_f32_16x16x32_bf16 a[12:15], a[192:195], a[236:239], a[12:15]\n" \
"v_mfma_f32_16x16x32_bf16 a[28:31], a[132:135], a[172:175], a[28:31]\n" \
"v_mfma_f32_16x16x32_bf16 a[28:31], a[196:199], a[236:239], a[28:31]\n" \
"ds_read_b64_tr_b16 a[242:243], v36 offset:9216\n" \
"v_mfma_f32_16x16x32_bf16 a[24:27], a[132:135], a[168:171], a[24:27]\n" \
"v_mfma_f32_16x16x32_bf16 a[24:27], a[196:199], a[232:235], a[24:27]\n" \
"v_mfma_f32_16x16x32_bf16 a[20:23], a[132:135], a[164:167], a[20:23]\n" \
"v_mfma_f32_16x16x32_bf16 a[20:23], a[196:199], a[228:231], a[20:23]\n" \
"ds_read_b64_tr_b16 a[180:181], v37 offset:0\n" \
"v_mfma_f32_16x16x32_bf16 a[16:19], a[132:135], a[160:163], a[16:19]\n" \
"v_mfma_f32_16x16x32_bf16 a[16:19], a[196:199], a[224:227], a[16:19]\n" \
"v_mfma_f32_16x16x32_bf16 a[32:35], a[136:139], a[160:163], a[32:35]\n" \
"ds_read_b64_tr_b16 a[182:183], v37 offset:1024\n" \
"v_mfma_f32_16x16x32_bf16 a[32:35], a[200:203], a[224:227], a[32:35]\n" \
"v_mfma_f32_16x16x32_bf16 a[36:39], a[136:139], a[164:167], a[36:39]\n" \
"v_mfma_f32_16x16x32_bf16 a[36:39], a[200:203], a[228:231], a[36:39]\n" \
"buffer_store_dwordx4 v[200:203], v20, s[80:83], s35 offen sc1\n" \
".fill (L_pb + 52960 - .) / 4, 4, 0xBF800000\n" \
"L_pp13:\n" \
"s_cbranch_execz L_pret\n" \
"v_mfma_f32_16x16x32_bf16 a[40:43], a[136:139], a[168:171], a[40:43]\n" \
"s_mov_b32 m0, s32\n" \
"ds_read_b64_tr_b16 a[244:245], v37 offset:8192\n" \
"v_mfma_f32_16x16x32_bf16 a[40:43], a[200:203], a[232:235], a[40:43]\n" \
"s_mov_b32 exec_lo, 0x0\n" \
"s_mov_b32 exec_hi, 0xffffffff\n" \
"global_load_lds_dword v[188:189], off\n" \
"s_mov_b64 exec, -1\n" \
"s_nop 0\n" \
"v_mfma_f32_16x16x32_bf16 a[44:47], a[136:139], a[172:175], a[44:47]\n" \
"ds_read_b64_tr_b16 a[246:247], v37 offset:9216\n" \
"v_mfma_f32_16x16x32_bf16 a[44:47], a[200:203], a[236:239], a[44:47]\n" \
"v_mfma_f32_16x16x32_bf16 a[60:63], a[140:143], a[172:175], a[60:63]\n" \
"v_mfma_f32_16x16x32_bf16 a[60:63], a[204:207], a[236:239], a[60:63]\n" \
"v_mfma_f32_16x16x32_bf16 a[56:59], a[140:143], a[168:171], a[56:59]\n" \
"ds_read_b64_tr_b16 a[184:185], v38 offset:0\n" \
"v_mfma_f32_16x16x32_bf16 a[56:59], a[204:207], a[232:235], a[56:59]\n" \
"v_mfma_f32_16x16x32_bf16 a[52:55], a[140:143], a[164:167], a[52:55]\n" \
"buffer_store_dwordx4 v[204:207], v20, s[80:83], s88 offen sc1\n" \
"v_mfma_f32_16x16x32_bf16 a[52:55], a[204:207], a[228:231], a[52:55]\n" \
"ds_read_b64_tr_b16 a[186:187], v38 offset:1024\n" \
"v_mfma_f32_16x16x32_bf16 a[48:51], a[140:143], a[160:163], a[48:51]\n" \
"v_mfma_f32_16x16x32_bf16 a[48:51], a[204:207], a[224:227], a[48:51]\n" \
"v_mfma_f32_16x16x32_bf16 a[64:67], a[144:147], a[160:163], a[64:67]\n" \
"ds_read_b64_tr_b16 a[248:249], v38 offset:8192\n" \
"v_mfma_f32_16x16x32_bf16 a[64:67], a[208:211], a[224:227], a[64:67]\n" \
"v_mfma_f32_16x16x32_bf16 a[68:71], a[144:147], a[164:167], a[68:71]\n" \
"v_mfma_f32_16x16x32_bf16 a[68:71], a[208:211], a[228:231], a[68:71]\n" \
"ds_read_b64_tr_b16 a[250:251], v38 offset:9216\n" \
"v_mfma_f32_16x16x32_bf16 a[72:75], a[144:147], a[168:171], a[72:75]\n" \
"v_mfma_f32_16x16x32_bf16 a[72:75], a[208:211], a[232:235], a[72:75]\n" \
"v_mfma_f32_16x16x32_bf16 a[76:79], a[144:147], a[172:175], a[76:79]\n" \
"buffer_store_dwordx4 v[208:211], v20, s[80:83], s36 offen sc1\n" \
"v_mfma_f32_16x16x32_bf16 a[76:79], a[208:211], a[236:239], a[76:79]\n" \
"ds_read_b64_tr_b16 a[188:189], v39 offset:0\n" \
"v_mfma_f32_16x16x32_bf16 a[92:95], a[148:151], a[172:175], a[92:95]\n" \
"v_mfma_f32_16x16x32_bf16 a[92:95], a[212:215], a[236:239], a[92:95]\n" \
"v_mfma_f32_16x16x32_bf16 a[88:91], a[148:151], a[168:171], a[88:91]\n" \
"ds_read_b64_tr_b16 a[190:191], v39 offset:1024\n" \
"v_mfma_f32_16x16x32_bf16 a[88:91], a[212:215], a[232:235], a[88:91]\n" \
"v_mfma_f32_16x16x32_bf16 a[84:87], a[148:151], a[164:167], a[84:87]\n" \
"v_mfma_f32_16x16x32_bf16 a[84:87], a[212:215], a[228:231], a[84:87]\n" \
"ds_read_b64_tr_b16 a[252:253], v39 offset:8192\n" \
"v_mfma_f32_16x16x32_bf16 a[80:83], a[148:151], a[160:163], a[80:83]\n" \
"v_mfma_f32_16x16x32_bf16 a[80:83], a[212:215], a[224:227], a[80:83]\n" \
"v_mfma_f32_16x16x32_bf16 a[96:99], a[152:155], a[160:163], a[96:99]\n" \
"ds_read_b64_tr_b16 a[254:255], v39 offset:9216\n" \
"v_mfma_f32_16x16x32_bf16 a[96:99], a[216:219], a[224:227], a[96:99]\n" \
"v_mfma_f32_16x16x32_bf16 a[100:103], a[152:155], a[164:167], a[100:103]\n" \
"v_mfma_f32_16x16x32_bf16 a[100:103], a[216:219], a[228:231], a[100:103]\n" \
"v_mfma_f32_16x16x32_bf16 a[104:107], a[152:155], a[168:171], a[104:107]\n" \
"v_mfma_f32_16x16x32_bf16 a[104:107], a[216:219], a[232:235], a[104:107]\n" \
"v_mfma_f32_16x16x32_bf16 a[108:111], a[152:155], a[172:175], a[108:111]\n" \
"v_mfma_f32_16x16x32_bf16 a[108:111], a[216:219], a[236:239], a[108:111]\n" \
"s_waitcnt lgkmcnt(15)\n" \
"v_mfma_f32_16x16x32_bf16 a[124:127], a[156:159], a[172:175], a[124:127]\n" \
"v_mfma_f32_16x16x32_bf16 a[124:127], a[220:223], a[236:239], a[124:127]\n" \
"v_mfma_f32_16x16x32_bf16 a[120:123], a[156:159], a[168:171], a[120:123]\n" \
"v_mfma_f32_16x16x32_bf16 a[120:123], a[220:223], a[232:235], a[120:123]\n" \
"v_mfma_f32_16x16x32_bf16 a[116:119], a[156:159], a[164:167], a[116:119]\n" \
"v_mfma_f32_16x16x32_bf16 a[116:119], a[220:223], a[228:231], a[116:119]\n" \
"v_mfma_f32_16x16x32_bf16 a[112:115], a[156:159], a[160:163], a[112:115]\n" \
"v_mfma_f32_16x16x32_bf16 a[112:115], a[220:223], a[224:227], a[112:115]\n" \
"s_waitcnt vmcnt(4) lgkmcnt(0)\n" \
"s_barrier\n" \
"v_mfma_f32_16x16x32_bf16 v[52:55], a[128:131], a[176:179], v[52:55]\n" \
"s_add_u32 m0, s30, 0x0\n" \
"s_movk_i32 s84, 0x300\n" \
"v_mfma_f32_16x16x32_bf16 v[52:55], a[192:195], a[240:243], v[52:55]\n" \
"buffer_load_dwordx4 v22, s[64:67], s84 offen offset:0 lds\n" \
"v_mfma_f32_16x16x32_bf16 v[56:59], a[128:131], a[180:183], v[56:59]\n" \
"ds_read_b64_tr_b16 a[160:161], v32 offset:32768\n" \
"v_mfma_f32_16x16x32_bf16 v[56:59], a[192:195], a[244:247], v[56:59]\n" \
"s_movk_i32 s84, 0x1f00\n" \
"v_mfma_f32_16x16x32_bf16 v[60:63], a[128:131], a[184:187], v[60:63]\n" \
"buffer_load_dwordx4 v23, s[64:67], s84 offen offset:1024 lds\n" \
"v_mfma_f32_16x16x32_bf16 v[60:63], a[192:195], a[248:251], v[60:63]\n" \
"ds_read_b64_tr_b16 a[162:163], v32 offset:33792\n" \
"v_mfma_f32_16x16x32_bf16 v[64:67], a[128:131], a[188:191], v[64:67]\n" \
"ds_read_b64_tr_b16 a[224:225], v32 offset:40960\n" \
"v_mfma_f32_16x16x32_bf16 v[64:67], a[192:195], a[252:255], v[64:67]\n" \
"ds_read_b128 a[128:131], v28 offset:49152\n" \
"v_mfma_f32_16x16x32_bf16 v[80:83], a[132:135], a[188:191], v[80:83]\n" \
"ds_read_b128 a[192:195], v29 offset:49152\n" \
"v_mfma_f32_16x16x32_bf16 v[80:83], a[196:199], a[252:255], v[80:83]\n" \
"s_movk_i32 s84, 0x3b00\n" \
"v_mfma_f32_16x16x32_bf16 v[76:79], a[132:135], a[184:187], v[76:79]\n" \
"buffer_load_dwordx4 v22, s[64:67], s84 offen offset:2048 lds\n" \
"v_mfma_f32_16x16x32_bf16 v[76:79], a[196:199], a[248:251], v[76:79]\n" \
"s_movk_i32 s84, 0x5700\n" \
"v_mfma_f32_16x16x32_bf16 v[72:75], a[132:135], a[180:183], v[72:75]\n" \
"buffer_load_dwordx4 v23, s[64:67], s84 offen offset:3072 lds\n" \
"v_mfma_f32_16x16x32_bf16 v[72:75], a[196:199], a[244:247], v[72:75]\n" \
"s_add_u32 m0, s31, 0x0\n" \
"s_mov_b32 s84, 0x300000\n" \
"v_mfma_f32_16x16x32_bf16 v[68:71], a[132:135], a[176:179], v[68:71]\n" \
"buffer_load_dwordx4 v26, s[68:71], s84 offen offset:0 lds\n" \
"v_mfma_f32_16x16x32_bf16 v[68:71], a[196:199], a[240:243], v[68:71]\n" \
"ds_read_b128 a[132:135], v28 offset:51200\n" \
"v_mfma_f32_16x16x32_bf16 v[84:87], a[136:139], a[176:179], v[84:87]\n" \
"ds_read_b128 a[196:199], v29 offset:51200\n" \
"v_mfma_f32_16x16x32_bf16 v[84:87], a[200:203], a[240:243], v[84:87]\n" \
"s_addk_i32 s84, 0x7c00\n" \
"v_mfma_f32_16x16x32_bf16 v[88:91], a[136:139], a[180:183], v[88:91]\n" \
"buffer_load_dwordx4 v26, s[68:71], s84 offen offset:1024 lds\n" \
"v_mfma_f32_16x16x32_bf16 v[88:91], a[200:203], a[244:247], v[88:91]\n" \
"ds_read_b64_tr_b16 a[226:227], v32 offset:41984\n" \
"v_mfma_f32_16x16x32_bf16 v[92:95], a[136:139], a[184:187], v[92:95]\n" \
"s_addk_i32 s84, 0x7c00\n" \
"v_mfma_f32_16x16x32_bf16 v[92:95], a[200:203], a[248:251], v[92:95]\n" \
"buffer_load_dwordx4 v27, s[68:71], s84 offen offset:2048 lds\n" \
"v_mfma_f32_16x16x32_bf16 v[96:99], a[136:139], a[188:191], v[96:99]\n" \
"ds_read_b64_tr_b16 a[164:165], v33 offset:32768\n" \
"v_mfma_f32_16x16x32_bf16 v[96:99], a[200:203], a[252:255], v[96:99]\n" \
"ds_read_b128 a[136:139], v28 offset:53248\n" \
"v_mfma_f32_16x16x32_bf16 v[112:115], a[140:143], a[188:191], v[112:115]\n" \
"ds_read_b128 a[200:203], v29 offset:53248\n" \
"v_mfma_f32_16x16x32_bf16 v[112:115], a[204:207], a[252:255], v[112:115]\n" \
"s_addk_i32 s84, 0x7c00\n" \
"v_mfma_f32_16x16x32_bf16 v[108:111], a[140:143], a[184:187], v[108:111]\n" \
"buffer_load_dwordx4 v27, s[68:71], s84 offen offset:3072 lds\n" \
"v_mfma_f32_16x16x32_bf16 v[108:111], a[204:207], a[248:251], v[108:111]\n" \
"s_add_u32 m0, s30, 0x1000\n" \
"s_mov_b32 s84, 0x8300\n" \
"v_mfma_f32_16x16x32_bf16 v[104:107], a[140:143], a[180:183], v[104:107]\n" \
"buffer_load_dwordx4 v22, s[64:67], s84 offen offset:0 lds\n" \
"v_mfma_f32_16x16x32_bf16 v[104:107], a[204:207], a[244:247], v[104:107]\n" \
"ds_read_b64_tr_b16 a[166:167], v33 offset:33792\n" \
"v_mfma_f32_16x16x32_bf16 v[100:103], a[140:143], a[176:179], v[100:103]\n" \
"ds_read_b64_tr_b16 a[228:229], v33 offset:40960\n" \
"v_mfma_f32_16x16x32_bf16 v[100:103], a[204:207], a[240:243], v[100:103]\n" \
"ds_read_b128 a[140:143], v28 offset:55296\n" \
"v_mfma_f32_16x16x32_bf16 v[116:119], a[144:147], a[176:179], v[116:119]\n" \
"ds_read_b128 a[204:207], v29 offset:55296\n" \
"v_mfma_f32_16x16x32_bf16 v[116:119], a[208:211], a[240:243], v[116:119]\n" \
"s_addk_i32 s84, 0x1c00\n" \
"v_mfma_f32_16x16x32_bf16 v[120:123], a[144:147], a[180:183], v[120:123]\n" \
"buffer_load_dwordx4 v23, s[64:67], s84 offen offset:1024 lds\n" \
"v_mfma_f32_16x16x32_bf16 v[120:123], a[208:211], a[244:247], v[120:123]\n" \
"s_addk_i32 s84, 0x1c00\n" \
"v_mfma_f32_16x16x32_bf16 v[124:127], a[144:147], a[184:187], v[124:127]\n" \
"buffer_load_dwordx4 v22, s[64:67], s84 offen offset:2048 lds\n" \
"v_mfma_f32_16x16x32_bf16 v[124:127], a[208:211], a[248:251], v[124:127]\n" \
"s_addk_i32 s84, 0x1c00\n" \
"s_nop 0\n" \
"v_mfma_f32_16x16x32_bf16 v[128:131], a[144:147], a[188:191], v[128:131]\n" \
"buffer_load_dwordx4 v23, s[64:67], s84 offen offset:3072 lds\n" \
"v_mfma_f32_16x16x32_bf16 v[128:131], a[208:211], a[252:255], v[128:131]\n" \
"ds_read_b128 a[144:147], v28 offset:32768\n" \
"v_mfma_f32_16x16x32_bf16 v[144:147], a[148:151], a[188:191], v[144:147]\n" \
"ds_read_b128 a[208:211], v29 offset:32768\n" \
"v_mfma_f32_16x16x32_bf16 v[144:147], a[212:215], a[252:255], v[144:147]\n" \
"s_add_u32 m0, s31, 0x4000\n" \
"s_mov_b32 s84, 0x300000\n" \
"v_mfma_f32_16x16x32_bf16 v[140:143], a[148:151], a[184:187], v[140:143]\n" \
"buffer_load_dwordx4 v26, s[72:75], s84 offen offset:0 lds\n" \
"v_mfma_f32_16x16x32_bf16 v[140:143], a[212:215], a[248:251], v[140:143]\n" \
"ds_read_b64_tr_b16 a[230:231], v33 offset:41984\n" \
"v_mfma_f32_16x16x32_bf16 v[136:139], a[148:151], a[180:183], v[136:139]\n" \
"s_addk_i32 s84, 0x7c00\n" \
"s_nop 0\n" \
"v_mfma_f32_16x16x32_bf16 v[136:139], a[212:215], a[244:247], v[136:139]\n" \
"buffer_load_dwordx4 v26, s[72:75], s84 offen offset:1024 lds\n" \
"v_mfma_f32_16x16x32_bf16 v[132:135], a[148:151], a[176:179], v[132:135]\n" \
"ds_read_b64_tr_b16 a[168:169], v34 offset:32768\n" \
"v_mfma_f32_16x16x32_bf16 v[132:135], a[212:215], a[240:243], v[132:135]\n" \
"ds_read_b128 a[148:151], v28 offset:34816\n" \
"v_mfma_f32_16x16x32_bf16 v[148:151], a[152:155], a[176:179], v[148:151]\n" \
"ds_read_b128 a[212:215], v29 offset:34816\n" \
"v_mfma_f32_16x16x32_bf16 v[148:151], a[216:219], a[240:243], v[148:151]\n" \
"ds_read_b64_tr_b16 a[170:171], v34 offset:33792\n" \
"v_mfma_f32_16x16x32_bf16 v[152:155], a[152:155], a[180:183], v[152:155]\n" \
"ds_read_b64_tr_b16 a[232:233], v34 offset:40960\n" \
"v_mfma_f32_16x16x32_bf16 v[152:155], a[216:219], a[244:247], v[152:155]\n" \
"ds_read_b64_tr_b16 a[234:235], v34 offset:41984\n" \
"v_mfma_f32_16x16x32_bf16 v[156:159], a[152:155], a[184:187], v[156:159]\n" \
"ds_read_b64_tr_b16 a[172:173], v35 offset:32768\n" \
"v_mfma_f32_16x16x32_bf16 v[156:159], a[216:219], a[248:251], v[156:159]\n" \
"ds_read_b64_tr_b16 a[174:175], v35 offset:33792\n" \
"v_mfma_f32_16x16x32_bf16 v[160:163], a[152:155], a[188:191], v[160:163]\n" \
"ds_read_b64_tr_b16 a[236:237], v35 offset:40960\n" \
"v_mfma_f32_16x16x32_bf16 v[160:163], a[216:219], a[252:255], v[160:163]\n" \
"ds_read_b128 a[152:155], v28 offset:36864\n" \
"v_mfma_f32_16x16x32_bf16 v[176:179], a[156:159], a[188:191], v[176:179]\n" \
"ds_read_b128 a[216:219], v29 offset:36864\n" \
"v_mfma_f32_16x16x32_bf16 v[176:179], a[220:223], a[252:255], v[176:179]\n" \
"ds_read_b64_tr_b16 a[238:239], v35 offset:41984\n" \
"v_mfma_f32_16x16x32_bf16 v[172:175], a[156:159], a[184:187], v[172:175]\n" \
"v_mfma_f32_16x16x32_bf16 v[172:175], a[220:223], a[248:251], v[172:175]\n" \
"v_mfma_f32_16x16x32_bf16 v[168:171], a[156:159], a[180:183], v[168:171]\n" \
"v_mfma_f32_16x16x32_bf16 v[168:171], a[220:223], a[244:247], v[168:171]\n" \
"v_mfma_f32_16x16x32_bf16 v[164:167], a[156:159], a[176:179], v[164:167]\n" \
"v_mfma_f32_16x16x32_bf16 v[164:167], a[220:223], a[240:243], v[164:167]\n" \
"ds_read_b128 a[156:159], v28 offset:38912\n" \
"ds_read_b128 a[220:223], v29 offset:38912\n" \
"s_waitcnt lgkmcnt(15)\n" \
"v_mfma_f32_16x16x32_bf16 a[0:3], a[128:131], a[160:163], a[0:3]\n" \
"s_addk_i32 s84, 0x7c00\n" \
"v_mfma_f32_16x16x32_bf16 a[0:3], a[192:195], a[224:227], a[0:3]\n" \
"buffer_load_dwordx4 v27, s[72:75], s84 offen offset:2048 lds\n" \
"v_mfma_f32_16x16x32_bf16 a[4:7], a[128:131], a[164:167], a[4:7]\n" \
"ds_read_b64_tr_b16 a[176:177], v36 offset:32768\n" \
"s_waitcnt lgkmcnt(15)\n" \
"v_mfma_f32_16x16x32_bf16 a[4:7], a[192:195], a[228:231], a[4:7]\n" \
"s_addk_i32 s84, 0x7c00\n" \
"s_waitcnt lgkmcnt(11)\n" \
"v_mfma_f32_16x16x32_bf16 a[8:11], a[128:131], a[168:171], a[8:11]\n" \
"buffer_load_dwordx4 v27, s[72:75], s84 offen offset:3072 lds\n" \
"s_waitcnt lgkmcnt(9)\n" \
"v_mfma_f32_16x16x32_bf16 a[8:11], a[192:195], a[232:235], a[8:11]\n" \
"ds_read_b64_tr_b16 a[178:179], v36 offset:33792\n" \
"s_waitcnt lgkmcnt(8)\n" \
"v_mfma_f32_16x16x32_bf16 a[12:15], a[128:131], a[172:175], a[12:15]\n" \
"ds_read_b64_tr_b16 a[240:241], v36 offset:40960\n" \
"s_waitcnt lgkmcnt(5)\n" \
"v_mfma_f32_16x16x32_bf16 a[12:15], a[192:195], a[236:239], a[12:15]\n" \
"v_mfma_f32_16x16x32_bf16 a[28:31], a[132:135], a[172:175], a[28:31]\n" \
"v_mfma_f32_16x16x32_bf16 a[28:31], a[196:199], a[236:239], a[28:31]\n" \
"ds_read_b64_tr_b16 a[242:243], v36 offset:41984\n" \
"v_mfma_f32_16x16x32_bf16 a[24:27], a[132:135], a[168:171], a[24:27]\n" \
"v_mfma_f32_16x16x32_bf16 a[24:27], a[196:199], a[232:235], a[24:27]\n" \
"v_mfma_f32_16x16x32_bf16 a[20:23], a[132:135], a[164:167], a[20:23]\n" \
"v_mfma_f32_16x16x32_bf16 a[20:23], a[196:199], a[228:231], a[20:23]\n" \
"ds_read_b64_tr_b16 a[180:181], v37 offset:32768\n" \
"v_mfma_f32_16x16x32_bf16 a[16:19], a[132:135], a[160:163], a[16:19]\n" \
"v_mfma_f32_16x16x32_bf16 a[16:19], a[196:199], a[224:227], a[16:19]\n" \
"v_mfma_f32_16x16x32_bf16 a[32:35], a[136:139], a[160:163], a[32:35]\n" \
"ds_read_b64_tr_b16 a[182:183], v37 offset:33792\n" \
"v_mfma_f32_16x16x32_bf16 a[32:35], a[200:203], a[224:227], a[32:35]\n" \
"v_mfma_f32_16x16x32_bf16 a[36:39], a[136:139], a[164:167], a[36:39]\n" \
"v_lshlrev_b32_e32 v45, 3, v20\n" \
"v_add_u32_e32 v45, s31, v45\n" \
"v_mfma_f32_16x16x32_bf16 a[36:39], a[200:203], a[228:231], a[36:39]\n" \
"s_getpc_b64 s[86:87]\n" \
"s_sub_u32 s86, s86, 0x10800\n" \
"s_subb_u32 s87, s87, 0\n" \
"s_mov_b32 m0, s32\n" \
"s_nop 0\n" \
"v_mfma_f32_16x16x32_bf16 a[40:43], a[136:139], a[168:171], a[40:43]\n" \
"global_load_lds_dword v45, s[86:87]\n" \
"v_mfma_f32_16x16x32_bf16 a[40:43], a[200:203], a[232:235], a[40:43]\n" \
"ds_read_b64_tr_b16 a[244:245], v37 offset:40960\n" \
"v_mfma_f32_16x16x32_bf16 a[44:47], a[136:139], a[172:175], a[44:47]\n" \
"ds_read_b64_tr_b16 a[246:247], v37 offset:41984\n" \
"v_mfma_f32_16x16x32_bf16 a[44:47], a[200:203], a[236:239], a[44:47]\n" \
"v_mfma_f32_16x16x32_bf16 a[60:63], a[140:143], a[172:175], a[60:63]\n" \
"v_mfma_f32_16x16x32_bf16 a[60:63], a[204:207], a[236:239], a[60:63]\n" \
"v_mfma_f32_16x16x32_bf16 a[56:59], a[140:143], a[168:171], a[56:59]\n" \
"ds_read_b64_tr_b16 a[184:185], v38 offset:32768\n" \
"v_mfma_f32_16x16x32_bf16 a[56:59], a[204:207], a[232:235], a[56:59]\n" \
"v_mfma_f32_16x16x32_bf16 a[52:55], a[140:143], a[164:167], a[52:55]\n" \
"v_mfma_f32_16x16x32_bf16 a[52:55], a[204:207], a[228:231], a[52:55]\n" \
"ds_read_b64_tr_b16 a[186:187], v38 offset:33792\n" \
"v_mfma_f32_16x16x32_bf16 a[48:51], a[140:143], a[160:163], a[48:51]\n" \
"v_mfma_f32_16x16x32_bf16 a[48:51], a[204:207], a[224:227], a[48:51]\n" \
"v_mfma_f32_16x16x32_bf16 a[64:67], a[144:147], a[160:163], a[64:67]\n" \
"ds_read_b64_tr_b16 a[248:249], v38 offset:40960\n" \
"v_mfma_f32_16x16x32_bf16 a[64:67], a[208:211], a[224:227], a[64:67]\n" \
"v_mfma_f32_16x16x32_bf16 a[68:71], a[144:147], a[164:167], a[68:71]\n" \
"v_mfma_f32_16x16x32_bf16 a[68:71], a[208:211], a[228:231], a[68:71]\n" \
"ds_read_b64_tr_b16 a[250:251], v38 offset:41984\n" \
"v_mfma_f32_16x16x32_bf16 a[72:75], a[144:147], a[168:171], a[72:75]\n" \
"v_mfma_f32_16x16x32_bf16 a[72:75], a[208:211], a[232:235], a[72:75]\n" \
"v_mfma_f32_16x16x32_bf16 a[76:79], a[144:147], a[172:175], a[76:79]\n" \
"v_mfma_f32_16x16x32_bf16 a[76:79], a[208:211], a[236:239], a[76:79]\n" \
"ds_read_b64_tr_b16 a[188:189], v39 offset:32768\n" \
"v_mfma_f32_16x16x32_bf16 a[92:95], a[148:151], a[172:175], a[92:95]\n" \
"v_mfma_f32_16x16x32_bf16 a[92:95], a[212:215], a[236:239], a[92:95]\n" \
"v_mfma_f32_16x16x32_bf16 a[88:91], a[148:151], a[168:171], a[88:91]\n" \
"ds_read_b64_tr_b16 a[190:191], v39 offset:33792\n" \
"v_mfma_f32_16x16x32_bf16 a[88:91], a[212:215], a[232:235], a[88:91]\n" \
"v_mfma_f32_16x16x32_bf16 a[84:87], a[148:151], a[164:167], a[84:87]\n" \
"v_mfma_f32_16x16x32_bf16 a[84:87], a[212:215], a[228:231], a[84:87]\n" \
"ds_read_b64_tr_b16 a[252:253], v39 offset:40960\n" \
"v_mfma_f32_16x16x32_bf16 a[80:83], a[148:151], a[160:163], a[80:83]\n" \
"v_mfma_f32_16x16x32_bf16 a[80:83], a[212:215], a[224:227], a[80:83]\n" \
"v_mfma_f32_16x16x32_bf16 a[96:99], a[152:155], a[160:163], a[96:99]\n" \
"ds_read_b64_tr_b16 a[254:255], v39 offset:41984\n" \
"v_mfma_f32_16x16x32_bf16 a[96:99], a[216:219], a[224:227], a[96:99]\n" \
"v_mfma_f32_16x16x32_bf16 a[100:103], a[152:155], a[164:167], a[100:103]\n" \
"v_mfma_f32_16x16x32_bf16 a[100:103], a[216:219], a[228:231], a[100:103]\n" \
"v_mfma_f32_16x16x32_bf16 a[104:107], a[152:155], a[168:171], a[104:107]\n" \
"v_mfma_f32_16x16x32_bf16 a[104:107], a[216:219], a[232:235], a[104:107]\n" \
"v_mfma_f32_16x16x32_bf16 a[108:111], a[152:155], a[172:175], a[108:111]\n" \
"v_mfma_f32_16x16x32_bf16 a[108:111], a[216:219], a[236:239], a[108:111]\n" \
"s_waitcnt lgkmcnt(15)\n" \
"v_mfma_f32_16x16x32_bf16 a[124:127], a[156:159], a[172:175], a[124:127]\n" \
"v_mfma_f32_16x16x32_bf16 a[124:127], a[220:223], a[236:239], a[124:127]\n" \
"v_mfma_f32_16x16x32_bf16 a[120:123], a[156:159], a[168:171], a[120:123]\n" \
"v_mfma_f32_16x16x32_bf16 a[120:123], a[220:223], a[232:235], a[120:123]\n" \
"v_mfma_f32_16x16x32_bf16 a[116:119], a[156:159], a[164:167], a[116:119]\n" \
"v_mfma_f32_16x16x32_bf16 a[116:119], a[220:223], a[228:231], a[116:119]\n" \
"v_mfma_f32_16x16x32_bf16 a[112:115], a[156:159], a[160:163], a[112:115]\n" \
"v_mfma_f32_16x16x32_bf16 a[112:115], a[220:223], a[224:227], a[112:115]\n" \
"s_waitcnt vmcnt(1) lgkmcnt(0)\n" \
"s_barrier\n" \
"v_mov_b32_e32 v45, s62\n" \
"s_mov_b64 exec, 1\n" \
"buffer_store_dword v45, off, s[80:83], s37 offset:128 sc1\n" \
"s_mov_b64 exec, -1\n" \
"v_mfma_f32_16x16x32_bf16 v[52:55], a[128:131], a[176:179], v[52:55]\n" \
"s_add_u32 m0, s30, 0x8000\n" \
"s_movk_i32 s84, 0x380\n" \
"v_mfma_f32_16x16x32_bf16 v[52:55], a[192:195], a[240:243], v[52:55]\n" \
"buffer_load_dwordx4 v22, s[64:67], s84 offen offset:0 lds\n" \
"v_mfma_f32_16x16x32_bf16 v[56:59], a[128:131], a[180:183], v[56:59]\n" \
"ds_read_b64_tr_b16 a[160:161], v32 offset:0\n" \
"v_mfma_f32_16x16x32_bf16 v[56:59], a[192:195], a[244:247], v[56:59]\n" \
"s_movk_i32 s84, 0x1f80\n" \
"v_mfma_f32_16x16x32_bf16 v[60:63], a[128:131], a[184:187], v[60:63]\n" \
"buffer_load_dwordx4 v23, s[64:67], s84 offen offset:1024 lds\n" \
"v_mfma_f32_16x16x32_bf16 v[60:63], a[192:195], a[248:251], v[60:63]\n" \
"ds_read_b64_tr_b16 a[162:163], v32 offset:1024\n" \
"v_mfma_f32_16x16x32_bf16 v[64:67], a[128:131], a[188:191], v[64:67]\n" \
"ds_read_b64_tr_b16 a[224:225], v32 offset:8192\n" \
"v_mfma_f32_16x16x32_bf16 v[64:67], a[192:195], a[252:255], v[64:67]\n" \
"ds_read_b128 a[128:131], v28 offset:16384\n" \
"v_mfma_f32_16x16x32_bf16 v[80:83], a[132:135], a[188:191], v[80:83]\n" \
"ds_read_b128 a[192:195], v29 offset:16384\n" \
"v_mfma_f32_16x16x32_bf16 v[80:83], a[196:199], a[252:255], v[80:83]\n" \
"s_movk_i32 s84, 0x3b80\n" \
"v_mfma_f32_16x16x32_bf16 v[76:79], a[132:135], a[184:187], v[76:79]\n" \
"buffer_load_dwordx4 v22, s[64:67], s84 offen offset:2048 lds\n" \
"v_mfma_f32_16x16x32_bf16 v[76:79], a[196:199], a[248:251], v[76:79]\n" \
"s_movk_i32 s84, 0x5780\n" \
"v_mfma_f32_16x16x32_bf16 v[72:75], a[132:135], a[180:183], v[72:75]\n" \
"buffer_load_dwordx4 v23, s[64:67], s84 offen offset:3072 lds\n" \
"v_mfma_f32_16x16x32_bf16 v[72:75], a[196:199], a[244:247], v[72:75]\n" \
"s_add_u32 m0, s31, 0x8000\n" \
"s_mov_b32 s84, 0x380000\n" \
"v_mfma_f32_16x16x32_bf16 v[68:71], a[132:135], a[176:179], v[68:71]\n" \
"buffer_load_dwordx4 v26, s[68:71], s84 offen offset:0 lds\n" \
"v_mfma_f32_16x16x32_bf16 v[68:71], a[196:199], a[240:243], v[68:71]\n" \
"ds_read_b128 a[132:135], v28 offset:18432\n" \
"v_mfma_f32_16x16x32_bf16 v[84:87], a[136:139], a[176:179], v[84:87]\n" \
"ds_read_b128 a[196:199], v29 offset:18432\n" \
"v_mfma_f32_16x16x32_bf16 v[84:87], a[200:203], a[240:243], v[84:87]\n" \
"s_addk_i32 s84, 0x7c00\n" \
"v_mfma_f32_16x16x32_bf16 v[88:91], a[136:139], a[180:183], v[88:91]\n" \
"buffer_load_dwordx4 v26, s[68:71], s84 offen offset:1024 lds\n" \
"v_mfma_f32_16x16x32_bf16 v[88:91], a[200:203], a[244:247], v[88:91]\n" \
"ds_read_b64_tr_b16 a[226:227], v32 offset:9216\n" \
"v_mfma_f32_16x16x32_bf16 v[92:95], a[136:139], a[184:187], v[92:95]\n" \
"s_addk_i32 s84, 0x7c00\n" \
"v_mfma_f32_16x16x32_bf16 v[92:95], a[200:203], a[248:251], v[92:95]\n" \
"buffer_load_dwordx4 v27, s[68:71], s84 offen offset:2048 lds\n" \
"v_mfma_f32_16x16x32_bf16 v[96:99], a[136:139], a[188:191], v[96:99]\n" \
"ds_read_b64_tr_b16 a[164:165], v33 offset:0\n" \
"v_mfma_f32_16x16x32_bf16 v[96:99], a[200:203], a[252:255], v[96:99]\n" \
"ds_read_b128 a[136:139], v28 offset:20480\n" \
"v_mfma_f32_16x16x32_bf16 v[112:115], a[140:143], a[188:191], v[112:115]\n" \
"ds_read_b128 a[200:203], v29 offset:20480\n" \
"v_mfma_f32_16x16x32_bf16 v[112:115], a[204:207], a[252:255], v[112:115]\n" \
"s_addk_i32 s84, 0x7c00\n" \
"s_nop 0\n" \
"v_mfma_f32_16x16x32_bf16 v[108:111], a[140:143], a[184:187], v[108:111]\n" \
"buffer_load_dwordx4 v27, s[68:71], s84 offen offset:3072 lds\n" \
"v_mfma_f32_16x16x32_bf16 v[108:111], a[204:207], a[248:251], v[108:111]\n" \
"s_add_u32 m0, s30, 0x9000\n" \
"s_mov_b32 s84, 0x8380\n" \
"v_mfma_f32_16x16x32_bf16 v[104:107], a[140:143], a[180:183], v[104:107]\n" \
"buffer_load_dwordx4 v22, s[64:67], s84 offen offset:0 lds\n" \
"v_mfma_f32_16x16x32_bf16 v[104:107], a[204:207], a[244:247], v[104:107]\n" \
"ds_read_b64_tr_b16 a[166:167], v33 offset:1024\n" \
"v_mfma_f32_16x16x32_bf16 v[100:103], a[140:143], a[176:179], v[100:103]\n" \
"ds_read_b64_tr_b16 a[228:229], v33 offset:8192\n" \
"v_mfma_f32_16x16x32_bf16 v[100:103], a[204:207], a[240:243], v[100:103]\n" \
"ds_read_b128 a[140:143], v28 offset:22528\n" \
"v_mfma_f32_16x16x32_bf16 v[116:119], a[144:147], a[176:179], v[116:119]\n" \
"ds_read_b128 a[204:207], v29 offset:22528\n" \
"v_mfma_f32_16x16x32_bf16 v[116:119], a[208:211], a[240:243], v[116:119]\n" \
"s_addk_i32 s84, 0x1c00\n" \
"v_mfma_f32_16x16x32_bf16 v[120:123], a[144:147], a[180:183], v[120:123]\n" \
"buffer_load_dwordx4 v23, s[64:67], s84 offen offset:1024 lds\n" \
"v_mfma_f32_16x16x32_bf16 v[120:123], a[208:211], a[244:247], v[120:123]\n" \
"s_addk_i32 s84, 0x1c00\n" \
"v_mfma_f32_16x16x32_bf16 v[124:127], a[144:147], a[184:187], v[124:127]\n" \
"buffer_load_dwordx4 v22, s[64:67], s84 offen offset:2048 lds\n" \
"v_mfma_f32_16x16x32_bf16 v[124:127], a[208:211], a[248:251], v[124:127]\n" \
"s_addk_i32 s84, 0x1c00\n" \
"s_nop 0\n" \
"v_mfma_f32_16x16x32_bf16 v[128:131], a[144:147], a[188:191], v[128:131]\n" \
"buffer_load_dwordx4 v23, s[64:67], s84 offen offset:3072 lds\n" \
"v_mfma_f32_16x16x32_bf16 v[128:131], a[208:211], a[252:255], v[128:131]\n" \
"ds_read_b128 a[144:147], v28 offset:0\n" \
"v_mfma_f32_16x16x32_bf16 v[144:147], a[148:151], a[188:191], v[144:147]\n" \
"ds_read_b128 a[208:211], v29 offset:0\n" \
"v_mfma_f32_16x16x32_bf16 v[144:147], a[212:215], a[252:255], v[144:147]\n" \
"s_add_u32 m0, s31, 0xc000\n" \
"s_mov_b32 s84, 0x380000\n" \
"v_mfma_f32_16x16x32_bf16 v[140:143], a[148:151], a[184:187], v[140:143]\n" \
"buffer_load_dwordx4 v26, s[72:75], s84 offen offset:0 lds\n" \
"v_mfma_f32_16x16x32_bf16 v[140:143], a[212:215], a[248:251], v[140:143]\n" \
"ds_read_b64_tr_b16 a[230:231], v33 offset:9216\n" \
"v_mfma_f32_16x16x32_bf16 v[136:139], a[148:151], a[180:183], v[136:139]\n" \
"s_addk_i32 s84, 0x7c00\n" \
"s_nop 0\n" \
"v_mfma_f32_16x16x32_bf16 v[136:139], a[212:215], a[244:247], v[136:139]\n" \
"buffer_load_dwordx4 v26, s[72:75], s84 offen offset:1024 lds\n" \
"v_mfma_f32_16x16x32_bf16 v[132:135], a[148:151], a[176:179], v[132:135]\n" \
"ds_read_b64_tr_b16 a[168:169], v34 offset:0\n" \
"v_mfma_f32_16x16x32_bf16 v[132:135], a[212:215], a[240:243], v[132:135]\n" \
"ds_read_b128 a[148:151], v28 offset:2048\n" \
"v_mfma_f32_16x16x32_bf16 v[148:151], a[152:155], a[176:179], v[148:151]\n" \
"ds_read_b128 a[212:215], v29 offset:2048\n" \
"v_mfma_f32_16x16x32_bf16 v[148:151], a[216:219], a[240:243], v[148:151]\n" \
"ds_read_b64_tr_b16 a[170:171], v34 offset:1024\n" \
"v_mfma_f32_16x16x32_bf16 v[152:155], a[152:155], a[180:183], v[152:155]\n" \
"ds_read_b64_tr_b16 a[232:233], v34 offset:8192\n" \
"v_mfma_f32_16x16x32_bf16 v[152:155], a[216:219], a[244:247], v[152:155]\n" \
"ds_read_b64_tr_b16 a[234:235], v34 offset:9216\n" \
"v_mfma_f32_16x16x32_bf16 v[156:159], a[152:155], a[184:187], v[156:159]\n" \
"ds_read_b64_tr_b16 a[172:173], v35 offset:0\n" \
"v_mfma_f32_16x16x32_bf16 v[156:159], a[216:219], a[248:251], v[156:159]\n" \
"ds_read_b64_tr_b16 a[174:175], v35 offset:1024\n" \
"v_mfma_f32_16x16x32_bf16 v[160:163], a[152:155], a[188:191], v[160:163]\n" \
"ds_read_b64_tr_b16 a[236:237], v35 offset:8192\n" \
"v_mfma_f32_16x16x32_bf16 v[160:163], a[216:219], a[252:255], v[160:163]\n" \
"ds_read_b128 a[152:155], v28 offset:4096\n" \
"v_mfma_f32_16x16x32_bf16 v[176:179], a[156:159], a[188:191], v[176:179]\n" \
"ds_read_b128 a[216:219], v29 offset:4096\n" \
"v_mfma_f32_16x16x32_bf16 v[176:179], a[220:223], a[252:255], v[176:179]\n" \
"ds_read_b64_tr_b16 a[238:239], v35 offset:9216\n" \
"v_mfma_f32_16x16x32_bf16 v[172:175], a[156:159], a[184:187], v[172:175]\n" \
"v_mfma_f32_16x16x32_bf16 v[172:175], a[220:223], a[248:251], v[172:175]\n" \
"v_mfma_f32_16x16x32_bf16 v[168:171], a[156:159], a[180:183], v[168:171]\n" \
"v_mfma_f32_16x16x32_bf16 v[168:171], a[220:223], a[244:247], v[168:171]\n" \
"v_mfma_f32_16x16x32_bf16 v[164:167], a[156:159], a[176:179], v[164:167]\n" \
"v_mfma_f32_16x16x32_bf16 v[164:167], a[220:223], a[240:243], v[164:167]\n" \
"ds_read_b128 a[156:159], v28 offset:6144\n" \
"ds_read_b128 a[220:223], v29 offset:6144\n" \
"s_waitcnt lgkmcnt(15)\n" \
"v_mfma_f32_16x16x32_bf16 a[0:3], a[128:131], a[160:163], a[0:3]\n" \
"s_addk_i32 s84, 0x7c00\n" \
"v_mfma_f32_16x16x32_bf16 a[0:3], a[192:195], a[224:227], a[0:3]\n" \
"buffer_load_dwordx4 v27, s[72:75], s84 offen offset:2048 lds\n" \
"v_mfma_f32_16x16x32_bf16 a[4:7], a[128:131], a[164:167], a[4:7]\n" \
"ds_read_b64_tr_b16 a[176:177], v36 offset:0\n" \
"s_waitcnt lgkmcnt(15)\n" \
"v_mfma_f32_16x16x32_bf16 a[4:7], a[192:195], a[228:231], a[4:7]\n" \
"s_addk_i32 s84, 0x7c00\n" \
"s_waitcnt lgkmcnt(11)\n" \
"v_mfma_f32_16x16x32_bf16 a[8:11], a[128:131], a[168:171], a[8:11]\n" \
"buffer_load_dwordx4 v27, s[72:75], s84 offen offset:3072 lds\n" \
"s_waitcnt lgkmcnt(9)\n" \
"v_mfma_f32_16x16x32_bf16 a[8:11], a[192:195], a[232:235], a[8:11]\n" \
"ds_read_b64_tr_b16 a[178:179], v36 offset:1024\n" \
"s_waitcnt lgkmcnt(8)\n" \
"v_mfma_f32_16x16x32_bf16 a[12:15], a[128:131], a[172:175], a[12:15]\n" \
"ds_read_b64_tr_b16 a[240:241], v36 offset:8192\n" \
"s_waitcnt lgkmcnt(5)\n" \
"v_mfma_f32_16x16x32_bf16 a[12:15], a[192:195], a[236:239], a[12:15]\n" \
"v_mfma_f32_16x16x32_bf16 a[28:31], a[132:135], a[172:175], a[28:31]\n" \
"v_mfma_f32_16x16x32_bf16 a[28:31], a[196:199], a[236:239], a[28:31]\n" \
"ds_read_b64_tr_b16 a[242:243], v36 offset:9216\n" \
"v_mfma_f32_16x16x32_bf16 a[24:27], a[132:135], a[168:171], a[24:27]\n" \
"v_mfma_f32_16x16x32_bf16 a[24:27], a[196:199], a[232:235], a[24:27]\n" \
"v_mfma_f32_16x16x32_bf16 a[20:23], a[132:135], a[164:167], a[20:23]\n" \
"v_mfma_f32_16x16x32_bf16 a[20:23], a[196:199], a[228:231], a[20:23]\n" \
"ds_read_b64_tr_b16 a[180:181], v37 offset:0\n" \
"v_mfma_f32_16x16x32_bf16 a[16:19], a[132:135], a[160:163], a[16:19]\n" \
"v_mfma_f32_16x16x32_bf16 a[16:19], a[196:199], a[224:227], a[16:19]\n" \
"v_mfma_f32_16x16x32_bf16 a[32:35], a[136:139], a[160:163], a[32:35]\n" \
"v_bfe_u32 v44, v20, 2, 5\n" \
"ds_read_b64_tr_b16 a[182:183], v37 offset:1024\n" \
"v_mfma_f32_16x16x32_bf16 a[32:35], a[200:203], a[224:227], a[32:35]\n" \
"buffer_load_dword v44, v44, s[80:83], s38 offen offset:128 sc1\n" \
"v_mfma_f32_16x16x32_bf16 a[36:39], a[136:139], a[164:167], a[36:39]\n" \
"v_mfma_f32_16x16x32_bf16 a[36:39], a[200:203], a[228:231], a[36:39]\n" \
"ds_read_b64_tr_b16 a[244:245], v37 offset:8192\n" \
"v_mfma_f32_16x16x32_bf16 a[40:43], a[136:139], a[168:171], a[40:43]\n" \
"v_mfma_f32_16x16x32_bf16 a[40:43], a[200:203], a[232:235], a[40:43]\n" \
"v_mfma_f32_16x16x32_bf16 a[44:47], a[136:139], a[172:175], a[44:47]\n" \
"ds_read_b64_tr_b16 a[246:247], v37 offset:9216\n" \
"v_mfma_f32_16x16x32_bf16 a[44:47], a[200:203], a[236:239], a[44:47]\n" \
"v_mfma_f32_16x16x32_bf16 a[60:63], a[140:143], a[172:175], a[60:63]\n" \
"global_store_dwordx4 v49, v[0:3], s[28:29] offset:0 sc1\n" \
"v_mfma_f32_16x16x32_bf16 a[60:63], a[204:207], a[236:239], a[60:63]\n" \
"v_mfma_f32_16x16x32_bf16 a[56:59], a[140:143], a[168:171], a[56:59]\n" \
"ds_read_b64_tr_b16 a[184:185], v38 offset:0\n" \
"v_mfma_f32_16x16x32_bf16 a[56:59], a[204:207], a[232:235], a[56:59]\n" \
"v_mfma_f32_16x16x32_bf16 a[52:55], a[140:143], a[164:167], a[52:55]\n" \
"v_mfma_f32_16x16x32_bf16 a[52:55], a[204:207], a[228:231], a[52:55]\n" \
"ds_read_b64_tr_b16 a[186:187], v38 offset:1024\n" \
"v_mfma_f32_16x16x32_bf16 a[48:51], a[140:143], a[160:163], a[48:51]\n" \
"v_mfma_f32_16x16x32_bf16 a[48:51], a[204:207], a[224:227], a[48:51]\n" \
"v_mfma_f32_16x16x32_bf16 a[64:67], a[144:147], a[160:163], a[64:67]\n" \
"global_store_dwordx4 v49, v[0:3], s[28:29] offset:1024 sc1\n" \
"v_mfma_f32_16x16x32_bf16 a[64:67], a[208:211], a[224:227], a[64:67]\n" \
"ds_read_b64_tr_b16 a[248:249], v38 offset:8192\n" \
"v_mfma_f32_16x16x32_bf16 a[68:71], a[144:147], a[164:167], a[68:71]\n" \
"v_mfma_f32_16x16x32_bf16 a[68:71], a[208:211], a[228:231], a[68:71]\n" \
"ds_read_b64_tr_b16 a[250:251], v38 offset:9216\n" \
"v_mfma_f32_16x16x32_bf16 a[72:75], a[144:147], a[168:171], a[72:75]\n" \
"v_mfma_f32_16x16x32_bf16 a[72:75], a[208:211], a[232:235], a[72:75]\n" \
"v_mfma_f32_16x16x32_bf16 a[76:79], a[144:147], a[172:175], a[76:79]\n" \
"v_mfma_f32_16x16x32_bf16 a[76:79], a[208:211], a[236:239], a[76:79]\n" \
"ds_read_b64_tr_b16 a[188:189], v39 offset:0\n" \
"v_mfma_f32_16x16x32_bf16 a[92:95], a[148:151], a[172:175], a[92:95]\n" \
"s_mov_b64 exec, 0xffff\n" \
"global_store_dwordx4 v49, v[0:3], s[28:29] offset:2048 sc1\n" \
"s_mov_b64 exec, -1\n" \
"s_nop 0\n" \
"v_mfma_f32_16x16x32_bf16 a[92:95], a[212:215], a[236:239], a[92:95]\n" \
"v_mfma_f32_16x16x32_bf16 a[88:91], a[148:151], a[168:171], a[88:91]\n" \
"ds_read_b64_tr_b16 a[190:191], v39 offset:1024\n" \
"v_mfma_f32_16x16x32_bf16 a[88:91], a[212:215], a[232:235], a[88:91]\n" \
"v_mfma_f32_16x16x32_bf16 a[84:87], a[148:151], a[164:167], a[84:87]\n" \
"v_mfma_f32_16x16x32_bf16 a[84:87], a[212:215], a[228:231], a[84:87]\n" \
"ds_read_b64_tr_b16 a[252:253], v39 offset:8192\n" \
"v_mfma_f32_16x16x32_bf16 a[80:83], a[148:151], a[160:163], a[80:83]\n" \
"v_mfma_f32_16x16x32_bf16 a[80:83], a[212:215], a[224:227], a[80:83]\n" \
"v_mfma_f32_16x16x32_bf16 a[96:99], a[152:155], a[160:163], a[96:99]\n" \
"ds_read_b64_tr_b16 a[254:255], v39 offset:9216\n" \
"v_mfma_f32_16x16x32_bf16 a[96:99], a[216:219], a[224:227], a[96:99]\n" \
"v_mfma_f32_16x16x32_bf16 a[100:103], a[152:155], a[164:167], a[100:103]\n" \
"v_mfma_f32_16x16x32_bf16 a[100:103], a[216:219], a[228:231], a[100:103]\n" \
"v_mfma_f32_16x16x32_bf16 a[104:107], a[152:155], a[168:171], a[104:107]\n" \
"v_mfma_f32_16x16x32_bf16 a[104:107], a[216:219], a[232:235], a[104:107]\n" \
"v_mfma_f32_16x16x32_bf16 a[108:111], a[152:155], a[172:175], a[108:111]\n" \
"v_mfma_f32_16x16x32_bf16 a[108:111], a[216:219], a[236:239], a[108:111]\n" \
"s_waitcnt lgkmcnt(15)\n" \
"v_mfma_f32_16x16x32_bf16 a[124:127], a[156:159], a[172:175], a[124:127]\n" \
"v_mfma_f32_16x16x32_bf16 a[124:127], a[220:223], a[236:239], a[124:127]\n" \
"v_mfma_f32_16x16x32_bf16 a[120:123], a[156:159], a[168:171], a[120:123]\n" \
"v_mfma_f32_16x16x32_bf16 a[120:123], a[220:223], a[232:235], a[120:123]\n" \
"v_mfma_f32_16x16x32_bf16 a[116:119], a[156:159], a[164:167], a[116:119]\n" \
"v_mfma_f32_16x16x32_bf16 a[116:119], a[220:223], a[228:231], a[116:119]\n" \
"v_mfma_f32_16x16x32_bf16 a[112:115], a[156:159], a[160:163], a[112:115]\n" \
"v_mfma_f32_16x16x32_bf16 a[112:115], a[220:223], a[224:227], a[112:115]\n" \
"s_waitcnt vmcnt(3) lgkmcnt(0)\n" \
"s_barrier\n" \
"v_cmp_ne_u32_e32 vcc, s62, v44\n" \
"s_nop 4\n" \
"s_cbranch_vccnz L_poll61_%=\n" \
"L_pollr61_%=:\n" \
"s_mov_b32 s68, s12\n" \
"s_and_b32 s69, s13, 0xffff\n" \
"s_mov_b32 s70, 0x480000\n" \
"s_mov_b32 s71, 0x20000\n" \
"v_mfma_f32_16x16x32_bf16 v[52:55], a[128:131], a[176:179], v[52:55]\n" \
"s_add_u32 m0, s40, 0x0\n" \
"s_add_u32 s85, s39, 0x0\n" \
"s_nop 0\n" \
"v_mfma_f32_16x16x32_bf16 v[52:55], a[192:195], a[240:243], v[52:55]\n" \
"buffer_load_dwordx4 v20, s[80:83], s85 offen sc1 lds\n" \
"v_mfma_f32_16x16x32_bf16 v[56:59], a[128:131], a[180:183], v[56:59]\n" \
"ds_read_b64_tr_b16 a[160:161], v32 offset:32768\n" \
"v_mfma_f32_16x16x32_bf16 v[56:59], a[192:195], a[244:247], v[56:59]\n" \
"s_add_u32 m0, s40, 0x1000\n" \
"s_add_u32 s85, s39, 0x1000\n" \
".fill (L_pb + 57056 - .) / 4, 4, 0xBF800000\n" \
"L_pp14:\n" \
"s_cbranch_execz L_pret\n" \
"s_nop 0\n" \
"v_mfma_f32_16x16x32_bf16 v[60:63], a[128:131], a[184:187], v[60:63]\n" \
"buffer_load_dwordx4 v20, s[80:83], s85 offen sc1 lds\n" \
"v_mfma_f32_16x16x32_bf16 v[60:63], a[192:195], a[248:251], v[60:63]\n" \
"ds_read_b64_tr_b16 a[162:163], v32 offset:33792\n" \
"v_mfma_f32_16x16x32_bf16 v[64:67], a[128:131], a[188:191], v[64:67]\n" \
"ds_read_b64_tr_b16 a[224:225], v32 offset:40960\n" \
"v_mfma_f32_16x16x32_bf16 v[64:67], a[192:195], a[252:255], v[64:67]\n" \
"ds_read_b128 a[128:131], v28 offset:49152\n" \
"v_mfma_f32_16x16x32_bf16 v[80:83], a[132:135], a[188:191], v[80:83]\n" \
"ds_read_b128 a[192:195], v29 offset:49152\n" \
"v_mfma_f32_16x16x32_bf16 v[80:83], a[196:199], a[252:255], v[80:83]\n" \
"s_add_u32 m0, s40, 0x3000\n" \
"s_movk_i32 s84, 0x0\n" \
"v_mfma_f32_16x16x32_bf16 v[76:79], a[132:135], a[184:187], v[76:79]\n" \
"buffer_load_dwordx4 v21, s[76:79], s84 offen offset:0 lds\n" \
"v_mfma_f32_16x16x32_bf16 v[76:79], a[196:199], a[248:251], v[76:79]\n" \
"s_add_u32 m0, s30, 0x0\n" \
"s_movk_i32 s84, 0x0\n" \
"s_nop 0\n" \
"v_mfma_f32_16x16x32_bf16 v[72:75], a[132:135], a[180:183], v[72:75]\n" \
"buffer_load_dwordx4 v24, s[76:79], s84 offen offset:0 lds\n" \
"v_mfma_f32_16x16x32_bf16 v[72:75], a[196:199], a[244:247], v[72:75]\n" \
"ds_read_b64_tr_b16 a[226:227], v32 offset:41984\n" \
"v_mfma_f32_16x16x32_bf16 v[68:71], a[132:135], a[176:179], v[68:71]\n" \
"ds_read_b64_tr_b16 a[164:165], v33 offset:32768\n" \
"v_mfma_f32_16x16x32_bf16 v[68:71], a[196:199], a[240:243], v[68:71]\n" \
"ds_read_b128 a[132:135], v28 offset:51200\n" \
"v_mfma_f32_16x16x32_bf16 v[84:87], a[136:139], a[176:179], v[84:87]\n" \
"ds_read_b128 a[196:199], v29 offset:51200\n" \
"v_mfma_f32_16x16x32_bf16 v[84:87], a[200:203], a[240:243], v[84:87]\n" \
"s_movk_i32 s84, 0x2000\n" \
"v_mfma_f32_16x16x32_bf16 v[88:91], a[136:139], a[180:183], v[88:91]\n" \
"buffer_load_dwordx4 v25, s[76:79], s84 offen offset:1024 lds\n" \
"v_mfma_f32_16x16x32_bf16 v[88:91], a[200:203], a[244:247], v[88:91]\n" \
"s_movk_i32 s84, 0x4000\n" \
"v_mfma_f32_16x16x32_bf16 v[92:95], a[136:139], a[184:187], v[92:95]\n" \
"buffer_load_dwordx4 v24, s[76:79], s84 offen offset:2048 lds\n" \
"v_mfma_f32_16x16x32_bf16 v[92:95], a[200:203], a[248:251], v[92:95]\n" \
"ds_read_b64_tr_b16 a[166:167], v33 offset:33792\n" \
"v_mfma_f32_16x16x32_bf16 v[96:99], a[136:139], a[188:191], v[96:99]\n" \
"ds_read_b64_tr_b16 a[228:229], v33 offset:40960\n" \
"v_mfma_f32_16x16x32_bf16 v[96:99], a[200:203], a[252:255], v[96:99]\n" \
"ds_read_b128 a[136:139], v28 offset:53248\n" \
"v_mfma_f32_16x16x32_bf16 v[112:115], a[140:143], a[188:191], v[112:115]\n" \
"ds_read_b128 a[200:203], v29 offset:53248\n" \
"v_mfma_f32_16x16x32_bf16 v[112:115], a[204:207], a[252:255], v[112:115]\n" \
"s_movk_i32 s84, 0x6000\n" \
"v_mfma_f32_16x16x32_bf16 v[108:111], a[140:143], a[184:187], v[108:111]\n" \
"buffer_load_dwordx4 v25, s[76:79], s84 offen offset:3072 lds\n" \
"v_mfma_f32_16x16x32_bf16 v[108:111], a[204:207], a[248:251], v[108:111]\n" \
"s_add_u32 m0, s31, 0x0\n" \
"s_movk_i32 s84, 0x0\n" \
"v_mfma_f32_16x16x32_bf16 v[104:107], a[140:143], a[180:183], v[104:107]\n" \
"buffer_load_dwordx4 v26, s[68:71], s84 offen offset:0 lds\n" \
"v_mfma_f32_16x16x32_bf16 v[104:107], a[204:207], a[244:247], v[104:107]\n" \
"s_movk_i32 s84, 0x7c00\n" \
"v_mfma_f32_16x16x32_bf16 v[100:103], a[140:143], a[176:179], v[100:103]\n" \
"buffer_load_dwordx4 v26, s[68:71], s84 offen offset:1024 lds\n" \
"v_mfma_f32_16x16x32_bf16 v[100:103], a[204:207], a[240:243], v[100:103]\n" \
"ds_read_b128 a[140:143], v28 offset:55296\n" \
"v_mfma_f32_16x16x32_bf16 v[116:119], a[144:147], a[176:179], v[116:119]\n" \
"ds_read_b128 a[204:207], v29 offset:55296\n" \
"v_mfma_f32_16x16x32_bf16 v[116:119], a[208:211], a[240:243], v[116:119]\n" \
"s_addk_i32 s84, 0x7c00\n" \
"v_mfma_f32_16x16x32_bf16 v[120:123], a[144:147], a[180:183], v[120:123]\n" \
"buffer_load_dwordx4 v27, s[68:71], s84 offen offset:2048 lds\n" \
"v_mfma_f32_16x16x32_bf16 v[120:123], a[208:211], a[244:247], v[120:123]\n" \
"ds_read_b64_tr_b16 a[230:231], v33 offset:41984\n" \
"v_mfma_f32_16x16x32_bf16 v[124:127], a[144:147], a[184:187], v[124:127]\n" \
"s_addk_i32 s84, 0x7c00\n" \
"v_mfma_f32_16x16x32_bf16 v[124:127], a[208:211], a[248:251], v[124:127]\n" \
"buffer_load_dwordx4 v27, s[68:71], s84 offen offset:3072 lds\n" \
"v_mfma_f32_16x16x32_bf16 v[128:131], a[144:147], a[188:191], v[128:131]\n" \
"ds_read_b64_tr_b16 a[168:169], v34 offset:32768\n" \
"v_mfma_f32_16x16x32_bf16 v[128:131], a[208:211], a[252:255], v[128:131]\n" \
"ds_read_b128 a[144:147], v28 offset:32768\n" \
"v_mfma_f32_16x16x32_bf16 v[144:147], a[148:151], a[188:191], v[144:147]\n" \
"ds_read_b128 a[208:211], v29 offset:32768\n" \
"v_mfma_f32_16x16x32_bf16 v[144:147], a[212:215], a[252:255], v[144:147]\n" \
"s_add_u32 m0, s30, 0x1000\n" \
"s_mov_b32 s84, 0x9000\n" \
"v_mfma_f32_16x16x32_bf16 v[140:143], a[148:151], a[184:187], v[140:143]\n" \
"buffer_load_dwordx4 v24, s[76:79], s84 offen offset:0 lds\n" \
"v_mfma_f32_16x16x32_bf16 v[140:143], a[212:215], a[248:251], v[140:143]\n" \
"ds_read_b64_tr_b16 a[170:171], v34 offset:33792\n" \
"v_mfma_f32_16x16x32_bf16 v[136:139], a[148:151], a[180:183], v[136:139]\n" \
"s_addk_i32 s84, 0x2000\n" \
"s_nop 0\n" \
"v_mfma_f32_16x16x32_bf16 v[136:139], a[212:215], a[244:247], v[136:139]\n" \
"buffer_load_dwordx4 v25, s[76:79], s84 offen offset:1024 lds\n" \
"v_mfma_f32_16x16x32_bf16 v[132:135], a[148:151], a[176:179], v[132:135]\n" \
"ds_read_b64_tr_b16 a[232:233], v34 offset:40960\n" \
"v_mfma_f32_16x16x32_bf16 v[132:135], a[212:215], a[240:243], v[132:135]\n" \
"ds_read_b128 a[148:151], v28 offset:34816\n" \
"v_mfma_f32_16x16x32_bf16 v[148:151], a[152:155], a[176:179], v[148:151]\n" \
"ds_read_b128 a[212:215], v29 offset:34816\n" \
"v_mfma_f32_16x16x32_bf16 v[148:151], a[216:219], a[240:243], v[148:151]\n" \
"ds_read_b64_tr_b16 a[234:235], v34 offset:41984\n" \
"v_mfma_f32_16x16x32_bf16 v[152:155], a[152:155], a[180:183], v[152:155]\n" \
"ds_read_b64_tr_b16 a[172:173], v35 offset:32768\n" \
"v_mfma_f32_16x16x32_bf16 v[152:155], a[216:219], a[244:247], v[152:155]\n" \
"ds_read_b64_tr_b16 a[174:175], v35 offset:33792\n" \
"v_mfma_f32_16x16x32_bf16 v[156:159], a[152:155], a[184:187], v[156:159]\n" \
"ds_read_b64_tr_b16 a[236:237], v35 offset:40960\n" \
"v_mfma_f32_16x16x32_bf16 v[156:159], a[216:219], a[248:251], v[156:159]\n" \
"ds_read_b64_tr_b16 a[238:239], v35 offset:41984\n" \
"v_mfma_f32_16x16x32_bf16 v[160:163], a[152:155], a[188:191], v[160:163]\n" \
"v_mfma_f32_16x16x32_bf16 v[160:163], a[216:219], a[252:255], v[160:163]\n" \
"ds_read_b128 a[152:155], v28 offset:36864\n" \
"v_mfma_f32_16x16x32_bf16 v[176:179], a[156:159], a[188:191], v[176:179]\n" \
"ds_read_b128 a[216:219], v29 offset:36864\n" \
"v_mfma_f32_16x16x32_bf16 v[176:179], a[220:223], a[252:255], v[176:179]\n" \
"v_mfma_f32_16x16x32_bf16 v[172:175], a[156:159], a[184:187], v[172:175]\n" \
"v_mfma_f32_16x16x32_bf16 v[172:175], a[220:223], a[248:251], v[172:175]\n" \
"v_mfma_f32_16x16x32_bf16 v[168:171], a[156:159], a[180:183], v[168:171]\n" \
"v_mfma_f32_16x16x32_bf16 v[168:171], a[220:223], a[244:247], v[168:171]\n" \
"v_mfma_f32_16x16x32_bf16 v[164:167], a[156:159], a[176:179], v[164:167]\n" \
"v_mfma_f32_16x16x32_bf16 v[164:167], a[220:223], a[240:243], v[164:167]\n" \
"ds_read_b128 a[156:159], v28 offset:38912\n" \
"ds_read_b128 a[220:223], v29 offset:38912\n" \
"s_waitcnt lgkmcnt(15)\n" \
"v_mfma_f32_16x16x32_bf16 a[0:3], a[128:131], a[160:163], a[0:3]\n" \
"s_addk_i32 s84, 0x2000\n" \
"v_mfma_f32_16x16x32_bf16 a[0:3], a[192:195], a[224:227], a[0:3]\n" \
"buffer_load_dwordx4 v24, s[76:79], s84 offen offset:2048 lds\n" \
"v_mfma_f32_16x16x32_bf16 a[4:7], a[128:131], a[164:167], a[4:7]\n" \
"ds_read_b64_tr_b16 a[176:177], v36 offset:32768\n" \
"v_mfma_f32_16x16x32_bf16 a[4:7], a[192:195], a[228:231], a[4:7]\n" \
"s_addk_i32 s84, 0x2000\n" \
"s_waitcnt lgkmcnt(13)\n" \
"v_mfma_f32_16x16x32_bf16 a[8:11], a[128:131], a[168:171], a[8:11]\n" \
"buffer_load_dwordx4 v25, s[76:79], s84 offen offset:3072 lds\n" \
"s_waitcnt lgkmcnt(9)\n" \
"v_mfma_f32_16x16x32_bf16 a[8:11], a[192:195], a[232:235], a[8:11]\n" \
"ds_read_b64_tr_b16 a[178:179], v36 offset:33792\n" \
"s_waitcnt lgkmcnt(8)\n" \
"v_mfma_f32_16x16x32_bf16 a[12:15], a[128:131], a[172:175], a[12:15]\n" \
"ds_read_b64_tr_b16 a[240:241], v36 offset:40960\n" \
"s_waitcnt lgkmcnt(7)\n" \
"v_mfma_f32_16x16x32_bf16 a[12:15], a[192:195], a[236:239], a[12:15]\n" \
"v_mfma_f32_16x16x32_bf16 a[28:31], a[132:135], a[172:175], a[28:31]\n" \
"v_mfma_f32_16x16x32_bf16 a[28:31], a[196:199], a[236:239], a[28:31]\n" \
"s_add_u32 m0, s30, 0x20000\n" \
"s_movk_i32 s84, 0x100\n" \
"v_mfma_f32_16x16x32_bf16 a[24:27], a[132:135], a[168:171], a[24:27]\n" \
"buffer_load_dwordx4 v24, s[76:79], s84 offen offset:0 lds\n" \
"v_mfma_f32_16x16x32_bf16 a[24:27], a[196:199], a[232:235], a[24:27]\n" \
"ds_read_b64_tr_b16 a[242:243], v36 offset:41984\n" \
"v_mfma_f32_16x16x32_bf16 a[20:23], a[132:135], a[164:167], a[20:23]\n" \
"v_accvgpr_read_b32 v184, a12\n" \
"v_accvgpr_read_b32 v185, a13\n" \
"v_mfma_f32_16x16x32_bf16 a[20:23], a[196:199], a[228:231], a[20:23]\n" \
"ds_read_b64_tr_b16 a[180:181], v37 offset:32768\n" \
"v_accvgpr_read_b32 v186, a14\n" \
"v_mfma_f32_16x16x32_bf16 a[16:19], a[132:135], a[160:163], a[16:19]\n" \
"s_movk_i32 s84, 0x2100\n" \
"s_nop 0\n" \
"v_mfma_f32_16x16x32_bf16 a[16:19], a[196:199], a[224:227], a[16:19]\n" \
"buffer_load_dwordx4 v25, s[76:79], s84 offen offset:1024 lds\n" \
"v_mfma_f32_16x16x32_bf16 a[32:35], a[136:139], a[160:163], a[32:35]\n" \
"ds_read_b64_tr_b16 a[182:183], v37 offset:33792\n" \
"v_accvgpr_read_b32 v187, a15\n" \
"v_mfma_f32_16x16x32_bf16 a[32:35], a[200:203], a[224:227], a[32:35]\n" \
"v_accvgpr_read_b32 v180, a28\n" \
"v_accvgpr_read_b32 v181, a29\n" \
"v_mfma_f32_16x16x32_bf16 a[36:39], a[136:139], a[164:167], a[36:39]\n" \
"v_accvgpr_read_b32 v182, a30\n" \
"v_accvgpr_read_b32 v183, a31\n" \
"v_mfma_f32_16x16x32_bf16 a[36:39], a[200:203], a[228:231], a[36:39]\n" \
"s_movk_i32 s84, 0x4100\n" \
"s_nop 0\n" \
"v_mfma_f32_16x16x32_bf16 a[40:43], a[136:139], a[168:171], a[40:43]\n" \
"buffer_load_dwordx4 v24, s[76:79], s84 offen offset:2048 lds\n" \
"v_mfma_f32_16x16x32_bf16 a[40:43], a[200:203], a[232:235], a[40:43]\n" \
"ds_read_b64_tr_b16 a[244:245], v37 offset:40960\n" \
"v_cvt_pk_bf16_f32 v184, v184, v185\n" \
"v_mfma_f32_16x16x32_bf16 a[44:47], a[136:139], a[172:175], a[44:47]\n" \
"ds_read_b64_tr_b16 a[246:247], v37 offset:41984\n" \
"v_cvt_pk_bf16_f32 v185, v186, v187\n" \
"v_mfma_f32_16x16x32_bf16 a[44:47], a[200:203], a[236:239], a[44:47]\n" \
"v_cvt_pk_bf16_f32 v186, v180, v181\n" \
"v_cvt_pk_bf16_f32 v187, v182, v183\n" \
"v_mfma_f32_16x16x32_bf16 a[60:63], a[140:143], a[172:175], a[60:63]\n" \
"s_movk_i32 s84, 0x6100\n" \
"v_mfma_f32_16x16x32_bf16 a[60:63], a[204:207], a[236:239], a[60:63]\n" \
"buffer_load_dwordx4 v25, s[76:79], s84 offen offset:3072 lds\n" \
"v_mfma_f32_16x16x32_bf16 a[56:59], a[140:143], a[168:171], a[56:59]\n" \
"ds_read_b64_tr_b16 a[184:185], v38 offset:32768\n" \
"v_accvgpr_read_b32 v188, a8\n" \
"v_mfma_f32_16x16x32_bf16 a[56:59], a[204:207], a[232:235], a[56:59]\n" \
"v_accvgpr_read_b32 v189, a9\n" \
"v_accvgpr_read_b32 v190, a10\n" \
"v_mfma_f32_16x16x32_bf16 a[52:55], a[140:143], a[164:167], a[52:55]\n" \
"s_add_u32 m0, s30, 0x21000\n" \
"s_addk_i32 s84, 0x3000\n" \
"v_mfma_f32_16x16x32_bf16 a[52:55], a[204:207], a[228:231], a[52:55]\n" \
"buffer_load_dwordx4 v24, s[76:79], s84 offen offset:0 lds\n" \
"v_mfma_f32_16x16x32_bf16 a[48:51], a[140:143], a[160:163], a[48:51]\n" \
"ds_read_b64_tr_b16 a[186:187], v38 offset:33792\n" \
"v_accvgpr_read_b32 v191, a11\n" \
"v_mfma_f32_16x16x32_bf16 a[48:51], a[204:207], a[224:227], a[48:51]\n" \
"v_accvgpr_read_b32 v180, a24\n" \
"v_accvgpr_read_b32 v181, a25\n" \
"v_mfma_f32_16x16x32_bf16 a[64:67], a[144:147], a[160:163], a[64:67]\n" \
"ds_read_b64_tr_b16 a[248:249], v38 offset:40960\n" \
"v_accvgpr_read_b32 v182, a26\n" \
"v_mfma_f32_16x16x32_bf16 a[64:67], a[208:211], a[224:227], a[64:67]\n" \
"s_addk_i32 s84, 0x2000\n" \
"s_nop 0\n" \
"v_mfma_f32_16x16x32_bf16 a[68:71], a[144:147], a[164:167], a[68:71]\n" \
"buffer_load_dwordx4 v25, s[76:79], s84 offen offset:1024 lds\n" \
"v_mfma_f32_16x16x32_bf16 a[68:71], a[208:211], a[228:231], a[68:71]\n" \
"ds_read_b64_tr_b16 a[250:251], v38 offset:41984\n" \
"v_accvgpr_read_b32 v183, a27\n" \
"v_mfma_f32_16x16x32_bf16 a[72:75], a[144:147], a[168:171], a[72:75]\n" \
"v_cvt_pk_bf16_f32 v188, v188, v189\n" \
"v_cvt_pk_bf16_f32 v189, v190, v191\n" \
"v_mfma_f32_16x16x32_bf16 a[72:75], a[208:211], a[232:235], a[72:75]\n" \
"v_cvt_pk_bf16_f32 v190, v180, v181\n" \
"v_cvt_pk_bf16_f32 v191, v182, v183\n" \
"v_mfma_f32_16x16x32_bf16 a[76:79], a[144:147], a[172:175], a[76:79]\n" \
"s_addk_i32 s84, 0x2000\n" \
"v_mfma_f32_16x16x32_bf16 a[76:79], a[208:211], a[236:239], a[76:79]\n" \
"buffer_load_dwordx4 v24, s[76:79], s84 offen offset:2048 lds\n" \
"v_mfma_f32_16x16x32_bf16 a[92:95], a[148:151], a[172:175], a[92:95]\n" \
"s_add_u32 s89, s41, 0x0\n" \
"buffer_store_dwordx4 v[184:187], v20, s[80:83], s89 offen offset:3072 sc1\n" \
"v_mfma_f32_16x16x32_bf16 a[92:95], a[212:215], a[236:239], a[92:95]\n" \
"ds_read_b64_tr_b16 a[188:189], v39 offset:32768\n" \
"v_accvgpr_read_b32 v192, a4\n" \
"v_mfma_f32_16x16x32_bf16 a[88:91], a[148:151], a[168:171], a[88:91]\n" \
"ds_read_b64_tr_b16 a[190:191], v39 offset:33792\n" \
"v_accvgpr_read_b32 v193, a5\n" \
"v_mfma_f32_16x16x32_bf16 a[88:91], a[212:215], a[232:235], a[88:91]\n" \
"s_addk_i32 s84, 0x2000\n" \
"v_mfma_f32_16x16x32_bf16 a[84:87], a[148:151], a[164:167], a[84:87]\n" \
"buffer_load_dwordx4 v25, s[76:79], s84 offen offset:3072 lds\n" \
"v_mfma_f32_16x16x32_bf16 a[84:87], a[212:215], a[228:231], a[84:87]\n" \
"ds_read_b64_tr_b16 a[252:253], v39 offset:40960\n" \
"v_accvgpr_read_b32 v194, a6\n" \
"v_mfma_f32_16x16x32_bf16 a[80:83], a[148:151], a[160:163], a[80:83]\n" \
"s_add_u32 s88, s41, 0x0\n" \
"buffer_store_dwordx4 v[188:191], v20, s[80:83], s88 offen offset:2048 sc1\n" \
"v_mfma_f32_16x16x32_bf16 a[80:83], a[212:215], a[224:227], a[80:83]\n" \
"v_accvgpr_read_b32 v195, a7\n" \
"v_accvgpr_read_b32 v180, a20\n" \
"s_waitcnt lgkmcnt(15)\n" \
"s_nop 0\n" \
"v_mfma_f32_16x16x32_bf16 a[96:99], a[152:155], a[160:163], a[96:99]\n" \
"ds_read_b64_tr_b16 a[254:255], v39 offset:41984\n" \
"v_accvgpr_read_b32 v181, a21\n" \
"v_mfma_f32_16x16x32_bf16 a[96:99], a[216:219], a[224:227], a[96:99]\n" \
"v_accvgpr_read_b32 v182, a22\n" \
"v_accvgpr_read_b32 v183, a23\n" \
"v_mfma_f32_16x16x32_bf16 a[100:103], a[152:155], a[164:167], a[100:103]\n" \
"v_cvt_pk_bf16_f32 v192, v192, v193\n" \
"v_cvt_pk_bf16_f32 v193, v194, v195\n" \
"v_mfma_f32_16x16x32_bf16 a[100:103], a[216:219], a[228:231], a[100:103]\n" \
"v_cvt_pk_bf16_f32 v194, v180, v181\n" \
"v_cvt_pk_bf16_f32 v195, v182, v183\n" \
"v_mfma_f32_16x16x32_bf16 a[104:107], a[152:155], a[168:171], a[104:107]\n" \
"s_add_u32 s89, s41, 0x0\n" \
"s_nop 0\n" \
"buffer_store_dwordx4 v[192:195], v20, s[80:83], s89 offen offset:1024 sc1\n" \
"v_mfma_f32_16x16x32_bf16 a[104:107], a[216:219], a[232:235], a[104:107]\n" \
"v_accvgpr_read_b32 v196, a0\n" \
"v_accvgpr_read_b32 v197, a1\n" \
"v_mfma_f32_16x16x32_bf16 a[108:111], a[152:155], a[172:175], a[108:111]\n" \
"v_accvgpr_read_b32 v198, a2\n" \
"v_accvgpr_read_b32 v199, a3\n" \
"v_mfma_f32_16x16x32_bf16 a[108:111], a[216:219], a[236:239], a[108:111]\n" \
"v_accvgpr_read_b32 v180, a16\n" \
"v_accvgpr_read_b32 v181, a17\n" \
"v_mfma_f32_16x16x32_bf16 a[124:127], a[156:159], a[172:175], a[124:127]\n" \
"v_accvgpr_read_b32 v182, a18\n" \
"v_accvgpr_read_b32 v183, a19\n" \
"v_mfma_f32_16x16x32_bf16 a[124:127], a[220:223], a[236:239], a[124:127]\n" \
"v_cvt_pk_bf16_f32 v196, v196, v197\n" \
"v_cvt_pk_bf16_f32 v197, v198, v199\n" \
"v_mfma_f32_16x16x32_bf16 a[120:123], a[156:159], a[168:171], a[120:123]\n" \
"v_cvt_pk_bf16_f32 v198, v180, v181\n" \
"v_cvt_pk_bf16_f32 v199, v182, v183\n" \
"s_add_u32 s88, s41, 0x0\n" \
"s_nop 0\n" \
"buffer_store_dwordx4 v[196:199], v20, s[80:83], s88 offen offset:0 sc1\n" \
"v_mfma_f32_16x16x32_bf16 a[120:123], a[220:223], a[232:235], a[120:123]\n" \
"v_accvgpr_read_b32 v200, a44\n" \
"v_accvgpr_read_b32 v201, a45\n" \
"v_mfma_f32_16x16x32_bf16 a[116:119], a[156:159], a[164:167], a[116:119]\n" \
"v_accvgpr_read_b32 v202, a46\n" \
"v_accvgpr_read_b32 v203, a47\n" \
"v_mfma_f32_16x16x32_bf16 a[116:119], a[220:223], a[228:231], a[116:119]\n" \
"v_accvgpr_read_b32 v180, a60\n" \
"v_accvgpr_read_b32 v181, a61\n" \
"v_mfma_f32_16x16x32_bf16 a[112:115], a[156:159], a[160:163], a[112:115]\n" \
"v_accvgpr_read_b32 v182, a62\n" \
"v_accvgpr_read_b32 v183, a63\n" \
"v_mfma_f32_16x16x32_bf16 a[112:115], a[220:223], a[224:227], a[112:115]\n" \
"v_cvt_pk_bf16_f32 v200, v200, v201\n" \
"v_cvt_pk_bf16_f32 v201, v202, v203\n" \
"s_waitcnt vmcnt(12) lgkmcnt(0)\n" \
"s_barrier\n" \
"v_xor_b32_e32 v36, 0x4000, v36\n" \
"v_xor_b32_e64 v32, 0x0, v48\n" \
"v_xor_b32_e32 v37, 0x4000, v37\n" \
"v_xor_b32_e64 v33, 0x20, v48\n" \
"v_xor_b32_e32 v38, 0x4000, v38\n" \
"v_xor_b32_e64 v34, 0x40, v48\n" \
"v_xor_b32_e32 v39, 0x4000, v39\n" \
"v_xor_b32_e32 v35, 0x60, v48\n" \
"v_add_u32_e32 v28, s43, v28\n" \
"v_add_u32_e32 v29, s43, v29\n" \
"v_mfma_f32_16x16x32_bf16 v[116:119], a[144:147], a[176:179], v[116:119]\n" \
"s_add_u32 m0, s40, 0x8000\n" \
"s_add_u32 s85, s39, 0x2000\n" \
"v_mfma_f32_16x16x32_bf16 v[116:119], a[208:211], a[240:243], v[116:119]\n" \
"buffer_load_dwordx4 v20, s[80:83], s85 offen sc1 lds\n" \
"v_mfma_f32_16x16x32_bf16 v[120:123], a[144:147], a[180:183], v[120:123]\n" \
"ds_read_b64_tr_b16 a[160:161], v32 offset:0\n" \
"v_cvt_pk_bf16_f32 v202, v180, v181\n" \
"v_mfma_f32_16x16x32_bf16 v[120:123], a[208:211], a[244:247], v[120:123]\n" \
"s_add_u32 m0, s40, 0x9000\n" \
"s_add_u32 s85, s39, 0x3000\n" \
"v_mfma_f32_16x16x32_bf16 v[124:127], a[144:147], a[184:187], v[124:127]\n" \
"buffer_load_dwordx4 v20, s[80:83], s85 offen sc1 lds\n" \
"v_mfma_f32_16x16x32_bf16 v[124:127], a[208:211], a[248:251], v[124:127]\n" \
"ds_read_b64_tr_b16 a[162:163], v32 offset:512\n" \
"v_cvt_pk_bf16_f32 v203, v182, v183\n" \
"v_mfma_f32_16x16x32_bf16 v[128:131], a[144:147], a[188:191], v[128:131]\n" \
"ds_read_b64_tr_b16 a[224:225], v32 offset:4096\n" \
"v_accvgpr_read_b32 v204, a40\n" \
"v_mfma_f32_16x16x32_bf16 v[128:131], a[208:211], a[252:255], v[128:131]\n" \
"ds_read_b128 a[144:147], v28 offset:8192\n" \
"v_accvgpr_read_b32 v205, a41\n" \
"v_mfma_f32_16x16x32_bf16 v[144:147], a[148:151], a[188:191], v[144:147]\n" \
"ds_read_b128 a[208:211], v29 offset:8192\n" \
"v_accvgpr_read_b32 v206, a42\n" \
"v_mfma_f32_16x16x32_bf16 v[144:147], a[212:215], a[252:255], v[144:147]\n" \
"s_add_u32 m0, s40, 0xb000\n" \
"s_movk_i32 s84, 0x80\n" \
"v_mfma_f32_16x16x32_bf16 v[140:143], a[148:151], a[184:187], v[140:143]\n" \
"buffer_load_dwordx4 v21, s[76:79], s84 offen offset:0 lds\n" \
"v_mfma_f32_16x16x32_bf16 v[140:143], a[212:215], a[248:251], v[140:143]\n" \
"s_add_u32 m0, s30, 0x8000\n" \
"s_movk_i32 s84, 0x80\n" \
"v_mfma_f32_16x16x32_bf16 v[136:139], a[148:151], a[180:183], v[136:139]\n" \
"buffer_load_dwordx4 v24, s[76:79], s84 offen offset:0 lds\n" \
"v_mfma_f32_16x16x32_bf16 v[136:139], a[212:215], a[244:247], v[136:139]\n" \
"ds_read_b64_tr_b16 a[226:227], v32 offset:4608\n" \
"v_accvgpr_read_b32 v207, a43\n" \
"v_mfma_f32_16x16x32_bf16 v[132:135], a[148:151], a[176:179], v[132:135]\n" \
"ds_read_b64_tr_b16 a[164:165], v33 offset:0\n" \
"v_accvgpr_read_b32 v180, a56\n" \
"v_mfma_f32_16x16x32_bf16 v[132:135], a[212:215], a[240:243], v[132:135]\n" \
"ds_read_b128 a[148:151], v28 offset:10240\n" \
"v_accvgpr_read_b32 v181, a57\n" \
"v_mfma_f32_16x16x32_bf16 v[148:151], a[152:155], a[176:179], v[148:151]\n" \
"ds_read_b128 a[212:215], v29 offset:10240\n" \
"v_accvgpr_read_b32 v182, a58\n" \
"v_mfma_f32_16x16x32_bf16 v[148:151], a[216:219], a[240:243], v[148:151]\n" \
"s_movk_i32 s84, 0x2080\n" \
"v_mfma_f32_16x16x32_bf16 v[152:155], a[152:155], a[180:183], v[152:155]\n" \
"buffer_load_dwordx4 v25, s[76:79], s84 offen offset:1024 lds\n" \
"v_mfma_f32_16x16x32_bf16 v[152:155], a[216:219], a[244:247], v[152:155]\n" \
"s_movk_i32 s84, 0x4080\n" \
"v_mfma_f32_16x16x32_bf16 v[156:159], a[152:155], a[184:187], v[156:159]\n" \
"buffer_load_dwordx4 v24, s[76:79], s84 offen offset:2048 lds\n" \
"v_mfma_f32_16x16x32_bf16 v[156:159], a[216:219], a[248:251], v[156:159]\n" \
"ds_read_b64_tr_b16 a[166:167], v33 offset:512\n" \
"v_accvgpr_read_b32 v183, a59\n" \
"v_mfma_f32_16x16x32_bf16 v[160:163], a[152:155], a[188:191], v[160:163]\n" \
"ds_read_b64_tr_b16 a[228:229], v33 offset:4096\n" \
"v_cvt_pk_bf16_f32 v204, v204, v205\n" \
"v_mfma_f32_16x16x32_bf16 v[160:163], a[216:219], a[252:255], v[160:163]\n" \
"ds_read_b128 a[152:155], v28 offset:12288\n" \
"v_cvt_pk_bf16_f32 v205, v206, v207\n" \
"v_mfma_f32_16x16x32_bf16 v[176:179], a[156:159], a[188:191], v[176:179]\n" \
"ds_read_b128 a[216:219], v29 offset:12288\n" \
"v_cvt_pk_bf16_f32 v206, v180, v181\n" \
"v_mfma_f32_16x16x32_bf16 v[176:179], a[220:223], a[252:255], v[176:179]\n" \
"s_movk_i32 s84, 0x6080\n" \
"v_mfma_f32_16x16x32_bf16 v[172:175], a[156:159], a[184:187], v[172:175]\n" \
"buffer_load_dwordx4 v25, s[76:79], s84 offen offset:3072 lds\n" \
"v_mfma_f32_16x16x32_bf16 v[172:175], a[220:223], a[248:251], v[172:175]\n" \
"s_add_u32 m0, s31, 0x8000\n" \
"s_mov_b32 s84, 0x80000\n" \
"v_mfma_f32_16x16x32_bf16 v[168:171], a[156:159], a[180:183], v[168:171]\n" \
"buffer_load_dwordx4 v26, s[68:71], s84 offen offset:0 lds\n" \
"v_mfma_f32_16x16x32_bf16 v[168:171], a[220:223], a[244:247], v[168:171]\n" \
"s_addk_i32 s84, 0x7c00\n" \
"v_mfma_f32_16x16x32_bf16 v[164:167], a[156:159], a[176:179], v[164:167]\n" \
"buffer_load_dwordx4 v26, s[68:71], s84 offen offset:1024 lds\n" \
"v_mfma_f32_16x16x32_bf16 v[164:167], a[220:223], a[240:243], v[164:167]\n" \
"ds_read_b128 a[156:159], v28 offset:14336\n" \
"v_cvt_pk_bf16_f32 v207, v182, v183\n" \
"v_mfma_f32_16x16x32_bf16 v[84:87], a[136:139], a[176:179], v[84:87]\n" \
"ds_read_b128 a[220:223], v29 offset:14336\n" \
"v_accvgpr_read_b32 v208, a36\n" \
"v_mfma_f32_16x16x32_bf16 v[84:87], a[200:203], a[240:243], v[84:87]\n" \
"s_addk_i32 s84, 0x7c00\n" \
"v_mfma_f32_16x16x32_bf16 v[88:91], a[136:139], a[180:183], v[88:91]\n" \
"buffer_load_dwordx4 v27, s[68:71], s84 offen offset:2048 lds\n" \
"v_mfma_f32_16x16x32_bf16 v[88:91], a[200:203], a[244:247], v[88:91]\n" \
"ds_read_b64_tr_b16 a[230:231], v33 offset:4608\n" \
"v_accvgpr_read_b32 v209, a37\n" \
"v_mfma_f32_16x16x32_bf16 v[92:95], a[136:139], a[184:187], v[92:95]\n" \
"s_addk_i32 s84, 0x7c00\n" \
"v_mfma_f32_16x16x32_bf16 v[92:95], a[200:203], a[248:251], v[92:95]\n" \
"buffer_load_dwordx4 v27, s[68:71], s84 offen offset:3072 lds\n" \
"v_mfma_f32_16x16x32_bf16 v[96:99], a[136:139], a[188:191], v[96:99]\n" \
"ds_read_b64_tr_b16 a[172:173], v35 offset:0\n" \
"v_accvgpr_read_b32 v210, a38\n" \
"v_mfma_f32_16x16x32_bf16 v[96:99], a[200:203], a[252:255], v[96:99]\n" \
"ds_read_b128 a[136:139], v28 offset:4096\n" \
"v_accvgpr_read_b32 v211, a39\n" \
"v_mfma_f32_16x16x32_bf16 v[112:115], a[140:143], a[188:191], v[112:115]\n" \
"ds_read_b128 a[200:203], v29 offset:4096\n" \
"v_accvgpr_read_b32 v180, a52\n" \
"v_mfma_f32_16x16x32_bf16 v[112:115], a[204:207], a[252:255], v[112:115]\n" \
"s_add_u32 m0, s30, 0x9000\n" \
"s_mov_b32 s84, 0x9080\n" \
"v_mfma_f32_16x16x32_bf16 v[108:111], a[140:143], a[184:187], v[108:111]\n" \
"buffer_load_dwordx4 v24, s[76:79], s84 offen offset:0 lds\n" \
"v_mfma_f32_16x16x32_bf16 v[108:111], a[204:207], a[248:251], v[108:111]\n" \
"ds_read_b64_tr_b16 a[174:175], v35 offset:512\n" \
"v_accvgpr_read_b32 v181, a53\n" \
"v_mfma_f32_16x16x32_bf16 v[104:107], a[140:143], a[180:183], v[104:107]\n" \
"s_addk_i32 s84, 0x2000\n" \
"s_nop 0\n" \
"v_mfma_f32_16x16x32_bf16 v[104:107], a[204:207], a[244:247], v[104:107]\n" \
"buffer_load_dwordx4 v25, s[76:79], s84 offen offset:1024 lds\n" \
"v_mfma_f32_16x16x32_bf16 v[100:103], a[140:143], a[176:179], v[100:103]\n" \
"ds_read_b64_tr_b16 a[236:237], v35 offset:4096\n" \
"v_accvgpr_read_b32 v182, a54\n" \
"v_mfma_f32_16x16x32_bf16 v[100:103], a[204:207], a[240:243], v[100:103]\n" \
"ds_read_b128 a[140:143], v28 offset:6144\n" \
"v_accvgpr_read_b32 v183, a55\n" \
"v_mfma_f32_16x16x32_bf16 v[52:55], a[128:131], a[176:179], v[52:55]\n" \
"ds_read_b128 a[204:207], v29 offset:6144\n" \
"v_cvt_pk_bf16_f32 v208, v208, v209\n" \
"v_mfma_f32_16x16x32_bf16 v[52:55], a[192:195], a[240:243], v[52:55]\n" \
"ds_read_b64_tr_b16 a[238:239], v35 offset:4608\n" \
"v_cvt_pk_bf16_f32 v209, v210, v211\n" \
"v_mfma_f32_16x16x32_bf16 v[56:59], a[128:131], a[180:183], v[56:59]\n" \
"ds_read_b64_tr_b16 a[168:169], v34 offset:0\n" \
"v_cvt_pk_bf16_f32 v210, v180, v181\n" \
"v_mfma_f32_16x16x32_bf16 v[56:59], a[192:195], a[244:247], v[56:59]\n" \
"ds_read_b64_tr_b16 a[170:171], v34 offset:512\n" \
"v_cvt_pk_bf16_f32 v211, v182, v183\n" \
"v_mfma_f32_16x16x32_bf16 v[60:63], a[128:131], a[184:187], v[60:63]\n" \
"ds_read_b64_tr_b16 a[232:233], v34 offset:4096\n" \
"v_accvgpr_read_b32 v212, a32\n" \
"v_mfma_f32_16x16x32_bf16 v[60:63], a[192:195], a[248:251], v[60:63]\n" \
"ds_read_b64_tr_b16 a[234:235], v34 offset:4608\n" \
"v_accvgpr_read_b32 v213, a33\n" \
"v_mfma_f32_16x16x32_bf16 v[64:67], a[128:131], a[188:191], v[64:67]\n" \
"v_accvgpr_read_b32 v214, a34\n" \
"v_accvgpr_read_b32 v215, a35\n" \
"v_mfma_f32_16x16x32_bf16 v[64:67], a[192:195], a[252:255], v[64:67]\n" \
"ds_read_b128 a[128:131], v28 offset:0\n" \
"v_accvgpr_read_b32 v180, a48\n" \
"v_mfma_f32_16x16x32_bf16 v[80:83], a[132:135], a[188:191], v[80:83]\n" \
"ds_read_b128 a[192:195], v29 offset:0\n" \
"v_accvgpr_read_b32 v181, a49\n" \
"v_mfma_f32_16x16x32_bf16 v[80:83], a[196:199], a[252:255], v[80:83]\n" \
"v_accvgpr_read_b32 v182, a50\n" \
"v_accvgpr_read_b32 v183, a51\n" \
"v_mfma_f32_16x16x32_bf16 v[76:79], a[132:135], a[184:187], v[76:79]\n" \
"v_cvt_pk_bf16_f32 v212, v212, v213\n" \
"v_cvt_pk_bf16_f32 v213, v214, v215\n" \
"v_mfma_f32_16x16x32_bf16 v[76:79], a[196:199], a[248:251], v[76:79]\n" \
"v_cvt_pk_bf16_f32 v214, v180, v181\n" \
"v_cvt_pk_bf16_f32 v215, v182, v183\n" \
"v_mfma_f32_16x16x32_bf16 v[72:75], a[132:135], a[180:183], v[72:75]\n" \
"v_accvgpr_read_b32 v216, a68\n" \
"v_accvgpr_read_b32 v217, a69\n" \
"v_mfma_f32_16x16x32_bf16 v[72:75], a[196:199], a[244:247], v[72:75]\n" \
"v_accvgpr_read_b32 v218, a70\n" \
"v_accvgpr_read_b32 v219, a71\n" \
"v_mfma_f32_16x16x32_bf16 v[68:71], a[132:135], a[176:179], v[68:71]\n" \
"v_accvgpr_read_b32 v180, a84\n" \
"v_accvgpr_read_b32 v181, a85\n" \
"v_mfma_f32_16x16x32_bf16 v[68:71], a[196:199], a[240:243], v[68:71]\n" \
"ds_read_b128 a[132:135], v28 offset:2048\n" \
"ds_read_b128 a[196:199], v29 offset:2048\n" \
"s_waitcnt lgkmcnt(15)\n" \
"v_mfma_f32_16x16x32_bf16 a[0:3], a[160:163], a[144:147], 0\n" \
"s_addk_i32 s84, 0x2000\n" \
"v_accvgpr_read_b32 v182, a86\n" \
"v_mfma_f32_16x16x32_bf16 a[0:3], a[224:227], a[208:211], a[0:3]\n" \
"buffer_load_dwordx4 v24, s[76:79], s84 offen offset:2048 lds\n" \
"v_accvgpr_read_b32 v183, a87\n" \
"v_mfma_f32_16x16x32_bf16 a[4:7], a[164:167], a[144:147], 0\n" \
"ds_read_b64_tr_b16 a[176:177], v36 offset:0\n" \
"v_cvt_pk_bf16_f32 v216, v216, v217\n" \
"v_mfma_f32_16x16x32_bf16 a[4:7], a[228:231], a[208:211], a[4:7]\n" \
"s_addk_i32 s84, 0x2000\n" \
"v_cvt_pk_bf16_f32 v217, v218, v219\n" \
"s_waitcnt lgkmcnt(13)\n" \
"v_mfma_f32_16x16x32_bf16 a[12:15], a[172:175], a[144:147], 0\n" \
"buffer_load_dwordx4 v25, s[76:79], s84 offen offset:3072 lds\n" \
"v_cvt_pk_bf16_f32 v218, v180, v181\n" \
"s_waitcnt lgkmcnt(9)\n" \
"v_mfma_f32_16x16x32_bf16 a[12:15], a[236:239], a[208:211], a[12:15]\n" \
"ds_read_b64_tr_b16 a[178:179], v36 offset:1024\n" \
"v_cvt_pk_bf16_f32 v219, v182, v183\n" \
"s_waitcnt lgkmcnt(8)\n" \
"v_mfma_f32_16x16x32_bf16 a[8:11], a[144:147], a[168:171], 0\n" \
"ds_read_b64_tr_b16 a[240:241], v36 offset:8192\n" \
"v_accvgpr_read_b32 v220, a64\n" \
"s_waitcnt lgkmcnt(7)\n" \
"s_nop 0\n" \
"v_mfma_f32_16x16x32_bf16 a[8:11], a[208:211], a[232:235], a[8:11]\n" \
"ds_read_b64_tr_b16 a[242:243], v36 offset:9216\n" \
"v_accvgpr_read_b32 v221, a65\n" \
"v_mfma_f32_16x16x32_bf16 a[24:27], a[148:151], a[168:171], 0\n" \
"ds_read_b64_tr_b16 a[180:181], v37 offset:0\n" \
"v_accvgpr_read_b32 v222, a66\n" \
"v_mfma_f32_16x16x32_bf16 a[24:27], a[212:215], a[232:235], a[24:27]\n" \
"ds_read_b64_tr_b16 a[182:183], v37 offset:1024\n" \
"v_accvgpr_read_b32 v223, a67\n" \
"v_mfma_f32_16x16x32_bf16 a[28:31], a[172:175], a[148:151], 0\n" \
"ds_read_b64_tr_b16 a[244:245], v37 offset:8192\n" \
"v_accvgpr_read_b32 v180, a80\n" \
"v_mfma_f32_16x16x32_bf16 a[28:31], a[236:239], a[212:215], a[28:31]\n" \
"s_add_u32 s89, s41, 0x2000\n" \
"buffer_store_dwordx4 v[200:203], v20, s[80:83], s89 offen offset:3072 sc1\n" \
"v_mfma_f32_16x16x32_bf16 a[20:23], a[164:167], a[148:151], 0\n" \
"ds_read_b64_tr_b16 a[246:247], v37 offset:9216\n" \
"v_accvgpr_read_b32 v181, a81\n" \
"v_mfma_f32_16x16x32_bf16 a[20:23], a[228:231], a[212:215], a[20:23]\n" \
"ds_read_b64_tr_b16 a[184:185], v38 offset:0\n" \
"v_accvgpr_read_b32 v182, a82\n" \
"v_mfma_f32_16x16x32_bf16 a[16:19], a[160:163], a[148:151], 0\n" \
"ds_read_b64_tr_b16 a[186:187], v38 offset:1024\n" \
"v_accvgpr_read_b32 v183, a83\n" \
"v_mfma_f32_16x16x32_bf16 a[16:19], a[224:227], a[212:215], a[16:19]\n" \
"v_cvt_pk_bf16_f32 v220, v220, v221\n" \
"v_cvt_pk_bf16_f32 v221, v222, v223\n" \
"v_cvt_pk_bf16_f32 v222, v180, v181\n" \
"v_mfma_f32_16x16x32_bf16 a[32:35], a[160:163], a[152:155], 0\n" \
"s_add_u32 s88, s41, 0x2000\n" \
".fill (L_pb + 61152 - .) / 4, 4, 0xBF800000\n" \
"L_pp15:\n" \
"s_cbranch_execz L_pret\n" \
"s_nop 0\n" \
"buffer_store_dwordx4 v[204:207], v20, s[80:83], s88 offen offset:2048 sc1\n" \
"v_mfma_f32_16x16x32_bf16 a[32:35], a[224:227], a[216:219], a[32:35]\n" \
"ds_read_b64_tr_b16 a[248:249], v38 offset:8192\n" \
"v_cvt_pk_bf16_f32 v223, v182, v183\n" \
"v_mfma_f32_16x16x32_bf16 a[36:39], a[164:167], a[152:155], 0\n" \
"ds_read_b64_tr_b16 a[250:251], v38 offset:9216\n" \
"v_cvt_pk_bf16_f32 v224, v128, v129\n" \
"v_mfma_f32_16x16x32_bf16 a[36:39], a[228:231], a[216:219], a[36:39]\n" \
"ds_read_b64_tr_b16 a[188:189], v39 offset:0\n" \
"v_cvt_pk_bf16_f32 v225, v130, v131\n" \
"v_mfma_f32_16x16x32_bf16 a[44:47], a[172:175], a[152:155], 0\n" \
"v_cvt_pk_bf16_f32 v226, v144, v145\n" \
"v_cvt_pk_bf16_f32 v227, v146, v147\n" \
"v_cvt_pk_bf16_f32 v228, v124, v125\n" \
"v_mfma_f32_16x16x32_bf16 a[44:47], a[236:239], a[216:219], a[44:47]\n" \
"ds_read_b64_tr_b16 a[190:191], v39 offset:1024\n" \
"v_cvt_pk_bf16_f32 v229, v126, v127\n" \
"v_mfma_f32_16x16x32_bf16 a[40:43], a[152:155], a[168:171], 0\n" \
"s_add_u32 s89, s41, 0x2000\n" \
"buffer_store_dwordx4 v[208:211], v20, s[80:83], s89 offen offset:1024 sc1\n" \
"v_mfma_f32_16x16x32_bf16 a[40:43], a[216:219], a[232:235], a[40:43]\n" \
"ds_read_b64_tr_b16 a[252:253], v39 offset:8192\n" \
"v_cvt_pk_bf16_f32 v230, v140, v141\n" \
"v_mfma_f32_16x16x32_bf16 a[56:59], a[156:159], a[168:171], 0\n" \
"ds_read_b64_tr_b16 a[254:255], v39 offset:9216\n" \
"v_cvt_pk_bf16_f32 v231, v142, v143\n" \
"v_mfma_f32_16x16x32_bf16 a[56:59], a[220:223], a[232:235], a[56:59]\n" \
"v_cvt_pk_bf16_f32 v232, v120, v121\n" \
"v_cvt_pk_bf16_f32 v233, v122, v123\n" \
"v_cvt_pk_bf16_f32 v234, v136, v137\n" \
"v_mfma_f32_16x16x32_bf16 a[60:63], a[172:175], a[156:159], 0\n" \
"v_cvt_pk_bf16_f32 v235, v138, v139\n" \
"v_cvt_pk_bf16_f32 v236, v116, v117\n" \
"v_cvt_pk_bf16_f32 v237, v118, v119\n" \
"v_mfma_f32_16x16x32_bf16 a[60:63], a[236:239], a[220:223], a[60:63]\n" \
"s_add_u32 s88, s41, 0x2000\n" \
"buffer_store_dwordx4 v[212:215], v20, s[80:83], s88 offen offset:0 sc1\n" \
"v_mfma_f32_16x16x32_bf16 a[52:55], a[164:167], a[156:159], 0\n" \
"v_cvt_pk_bf16_f32 v238, v132, v133\n" \
"v_cvt_pk_bf16_f32 v239, v134, v135\n" \
"v_cvt_pk_bf16_f32 v240, v160, v161\n" \
"v_mfma_f32_16x16x32_bf16 a[52:55], a[228:231], a[220:223], a[52:55]\n" \
"v_cvt_pk_bf16_f32 v241, v162, v163\n" \
"v_cvt_pk_bf16_f32 v242, v176, v177\n" \
"v_cvt_pk_bf16_f32 v243, v178, v179\n" \
"v_mfma_f32_16x16x32_bf16 a[48:51], a[160:163], a[156:159], 0\n" \
"v_cvt_pk_bf16_f32 v244, v156, v157\n" \
"v_cvt_pk_bf16_f32 v245, v158, v159\n" \
"v_cvt_pk_bf16_f32 v246, v172, v173\n" \
"v_mfma_f32_16x16x32_bf16 a[48:51], a[224:227], a[220:223], a[48:51]\n" \
"v_cvt_pk_bf16_f32 v247, v174, v175\n" \
"v_cvt_pk_bf16_f32 v184, v152, v153\n" \
"v_cvt_pk_bf16_f32 v185, v154, v155\n" \
"v_mfma_f32_16x16x32_bf16 a[64:67], a[164:167], a[140:143], 0\n" \
"v_cvt_pk_bf16_f32 v186, v168, v169\n" \
"v_cvt_pk_bf16_f32 v187, v170, v171\n" \
"v_cvt_pk_bf16_f32 v188, v148, v149\n" \
"v_mfma_f32_16x16x32_bf16 a[64:67], a[228:231], a[204:207], a[64:67]\n" \
"v_cvt_pk_bf16_f32 v189, v150, v151\n" \
"v_cvt_pk_bf16_f32 v190, v164, v165\n" \
"v_cvt_pk_bf16_f32 v191, v166, v167\n" \
"v_mfma_f32_16x16x32_bf16 a[80:83], a[172:175], a[140:143], 0\n" \
"v_cvt_pk_bf16_f32 v192, v96, v97\n" \
"v_cvt_pk_bf16_f32 v193, v98, v99\n" \
"v_cvt_pk_bf16_f32 v194, v112, v113\n" \
"v_mfma_f32_16x16x32_bf16 a[80:83], a[236:239], a[204:207], a[80:83]\n" \
"v_cvt_pk_bf16_f32 v195, v114, v115\n" \
"v_cvt_pk_bf16_f32 v196, v92, v93\n" \
"v_cvt_pk_bf16_f32 v197, v94, v95\n" \
"v_mfma_f32_16x16x32_bf16 a[68:71], a[140:143], a[168:171], 0\n" \
"v_cvt_pk_bf16_f32 v198, v108, v109\n" \
"v_cvt_pk_bf16_f32 v199, v110, v111\n" \
"v_cvt_pk_bf16_f32 v252, v88, v89\n" \
"v_mfma_f32_16x16x32_bf16 a[68:71], a[204:207], a[232:235], a[68:71]\n" \
"v_cvt_pk_bf16_f32 v253, v90, v91\n" \
"v_cvt_pk_bf16_f32 v254, v104, v105\n" \
"v_cvt_pk_bf16_f32 v255, v106, v107\n" \
"s_waitcnt vmcnt(4) lgkmcnt(0)\n" \
"s_barrier\n" \
"v_mfma_f32_16x16x32_bf16 v[116:119], a[176:179], a[144:147], 0\n" \
"s_add_u32 m0, s40, 0x0\n" \
"s_nop 0\n" \
"s_add_u32 s85, s39, 0x4000\n" \
"v_accvgpr_write_b32 a84, v252\n" \
"v_mfma_f32_16x16x32_bf16 v[116:119], a[240:243], a[208:211], v[116:119]\n" \
"buffer_load_dwordx4 v20, s[80:83], s85 offen sc1 lds\n" \
"v_accvgpr_write_b32 a85, v253\n" \
"v_mfma_f32_16x16x32_bf16 v[120:123], a[180:183], a[144:147], 0\n" \
"ds_read_b64_tr_b16 a[160:161], v32 offset:32768\n" \
"v_accvgpr_write_b32 a86, v254\n" \
"v_mfma_f32_16x16x32_bf16 v[120:123], a[244:247], a[208:211], v[120:123]\n" \
"ds_read_b64_tr_b16 a[162:163], v32 offset:33280\n" \
"v_accvgpr_write_b32 a87, v255\n" \
"v_mfma_f32_16x16x32_bf16 v[124:127], a[184:187], a[144:147], 0\n" \
"s_add_u32 m0, s40, 0x1000\n" \
"s_add_u32 s85, s39, 0x5000\n" \
"v_cvt_pk_bf16_f32 v252, v84, v85\n" \
"v_mfma_f32_16x16x32_bf16 v[124:127], a[248:251], a[208:211], v[124:127]\n" \
"buffer_load_dwordx4 v20, s[80:83], s85 offen sc1 lds\n" \
"v_cvt_pk_bf16_f32 v253, v86, v87\n" \
"v_mfma_f32_16x16x32_bf16 v[128:131], a[188:191], a[144:147], 0\n" \
"ds_read_b64_tr_b16 a[224:225], v32 offset:36864\n" \
"v_cvt_pk_bf16_f32 v254, v100, v101\n" \
"v_mfma_f32_16x16x32_bf16 v[128:131], a[252:255], a[208:211], v[128:131]\n" \
"ds_read_b128 a[144:147], v28 offset:40960\n" \
"v_cvt_pk_bf16_f32 v255, v102, v103\n" \
"v_mfma_f32_16x16x32_bf16 v[144:147], a[188:191], a[148:151], 0\n" \
"ds_read_b128 a[208:211], v29 offset:40960\n" \
"v_cvt_pk_bf16_f32 v200, v64, v65\n" \
"v_mfma_f32_16x16x32_bf16 v[144:147], a[252:255], a[212:215], v[144:147]\n" \
"s_add_u32 m0, s40, 0x3000\n" \
"s_movk_i32 s84, 0x100\n" \
"s_nop 0\n" \
"v_cvt_pk_bf16_f32 v201, v66, v67\n" \
"v_mfma_f32_16x16x32_bf16 v[140:143], a[184:187], a[148:151], 0\n" \
"buffer_load_dwordx4 v21, s[76:79], s84 offen offset:0 lds\n" \
"v_cvt_pk_bf16_f32 v202, v80, v81\n" \
"v_mfma_f32_16x16x32_bf16 v[140:143], a[248:251], a[212:215], v[140:143]\n" \
"ds_read_b64_tr_b16 a[226:227], v32 offset:37376\n" \
"v_cvt_pk_bf16_f32 v203, v82, v83\n" \
"v_mfma_f32_16x16x32_bf16 v[136:139], a[180:183], a[148:151], 0\n" \
"v_cvt_pk_bf16_f32 v204, v60, v61\n" \
"v_cvt_pk_bf16_f32 v205, v62, v63\n" \
"v_cvt_pk_bf16_f32 v206, v76, v77\n" \
"v_mfma_f32_16x16x32_bf16 v[136:139], a[244:247], a[212:215], v[136:139]\n" \
"ds_read_b64_tr_b16 a[164:165], v33 offset:32768\n" \
"v_cvt_pk_bf16_f32 v207, v78, v79\n" \
"v_mfma_f32_16x16x32_bf16 v[132:135], a[176:179], a[148:151], 0\n" \
"v_cvt_pk_bf16_f32 v208, v56, v57\n" \
"v_cvt_pk_bf16_f32 v209, v58, v59\n" \
"v_cvt_pk_bf16_f32 v210, v72, v73\n" \
"v_mfma_f32_16x16x32_bf16 v[132:135], a[240:243], a[212:215], v[132:135]\n" \
"ds_read_b128 a[148:151], v28 offset:43008\n" \
"v_cvt_pk_bf16_f32 v211, v74, v75\n" \
"v_mfma_f32_16x16x32_bf16 v[148:151], a[176:179], a[152:155], 0\n" \
"ds_read_b128 a[212:215], v29 offset:43008\n" \
"v_cvt_pk_bf16_f32 v212, v52, v53\n" \
"v_mfma_f32_16x16x32_bf16 v[148:151], a[240:243], a[216:219], v[148:151]\n" \
"s_add_u32 m0, s31, 0x0\n" \
"s_mov_b32 s84, 0x100000\n" \
"v_cvt_pk_bf16_f32 v213, v54, v55\n" \
"v_mfma_f32_16x16x32_bf16 v[152:155], a[180:183], a[152:155], 0\n" \
"buffer_load_dwordx4 v26, s[68:71], s84 offen offset:0 lds\n" \
"v_cvt_pk_bf16_f32 v214, v68, v69\n" \
"v_mfma_f32_16x16x32_bf16 v[152:155], a[244:247], a[216:219], v[152:155]\n" \
"s_addk_i32 s84, 0x7c00\n" \
"v_cvt_pk_bf16_f32 v215, v70, v71\n" \
"v_mfma_f32_16x16x32_bf16 v[156:159], a[184:187], a[152:155], 0\n" \
"buffer_load_dwordx4 v26, s[68:71], s84 offen offset:1024 lds\n" \
"v_mfma_f32_16x16x32_bf16 v[156:159], a[248:251], a[216:219], v[156:159]\n" \
"ds_read_b64_tr_b16 a[166:167], v33 offset:33280\n" \
"v_mfma_f32_16x16x32_bf16 v[160:163], a[188:191], a[152:155], 0\n" \
"ds_read_b64_tr_b16 a[228:229], v33 offset:36864\n" \
"v_mfma_f32_16x16x32_bf16 v[160:163], a[252:255], a[216:219], v[160:163]\n" \
"ds_read_b128 a[152:155], v28 offset:45056\n" \
"v_mfma_f32_16x16x32_bf16 v[176:179], a[188:191], a[156:159], 0\n" \
"ds_read_b128 a[216:219], v29 offset:45056\n" \
"v_mfma_f32_16x16x32_bf16 v[176:179], a[252:255], a[220:223], v[176:179]\n" \
"s_addk_i32 s84, 0x7c00\n" \
"v_mfma_f32_16x16x32_bf16 v[172:175], a[184:187], a[156:159], 0\n" \
"buffer_load_dwordx4 v27, s[68:71], s84 offen offset:2048 lds\n" \
"v_mfma_f32_16x16x32_bf16 v[172:175], a[248:251], a[220:223], v[172:175]\n" \
"ds_read_b64_tr_b16 a[230:231], v33 offset:37376\n" \
"v_mfma_f32_16x16x32_bf16 v[168:171], a[180:183], a[156:159], 0\n" \
"s_addk_i32 s84, 0x7c00\n" \
"v_mfma_f32_16x16x32_bf16 v[168:171], a[244:247], a[220:223], v[168:171]\n" \
"buffer_load_dwordx4 v27, s[68:71], s84 offen offset:3072 lds\n" \
"v_mfma_f32_16x16x32_bf16 v[164:167], a[176:179], a[156:159], 0\n" \
"ds_read_b64_tr_b16 a[172:173], v35 offset:32768\n" \
"v_mfma_f32_16x16x32_bf16 v[164:167], a[240:243], a[220:223], v[164:167]\n" \
"ds_read_b128 a[156:159], v28 offset:47104\n" \
"v_mfma_f32_16x16x32_bf16 v[84:87], a[176:179], a[136:139], 0\n" \
"ds_read_b128 a[220:223], v29 offset:47104\n" \
"v_mfma_f32_16x16x32_bf16 v[84:87], a[240:243], a[200:203], v[84:87]\n" \
"s_add_u32 s89, s41, 0x3000\n" \
"buffer_store_dwordx4 v[192:195], v20, s[80:83], s89 offen offset:3072 sc1\n" \
"v_mfma_f32_16x16x32_bf16 v[88:91], a[180:183], a[136:139], 0\n" \
"ds_read_b64_tr_b16 a[174:175], v35 offset:33280\n" \
"v_mfma_f32_16x16x32_bf16 v[88:91], a[244:247], a[200:203], v[88:91]\n" \
"ds_read_b64_tr_b16 a[236:237], v35 offset:36864\n" \
"v_mfma_f32_16x16x32_bf16 v[92:95], a[184:187], a[136:139], 0\n" \
"ds_read_b64_tr_b16 a[238:239], v35 offset:37376\n" \
"v_mfma_f32_16x16x32_bf16 v[92:95], a[248:251], a[200:203], v[92:95]\n" \
"s_add_u32 m0, s30, 0x0\n" \
"s_movk_i32 s84, 0x180\n" \
"v_mfma_f32_16x16x32_bf16 v[96:99], a[188:191], a[136:139], 0\n" \
"buffer_load_dwordx4 v24, s[76:79], s84 offen offset:0 lds\n" \
"v_mfma_f32_16x16x32_bf16 v[96:99], a[252:255], a[200:203], v[96:99]\n" \
"ds_read_b128 a[136:139], v28 offset:36864\n" \
"v_mfma_f32_16x16x32_bf16 v[112:115], a[188:191], a[140:143], 0\n" \
"ds_read_b128 a[200:203], v29 offset:36864\n" \
"v_mfma_f32_16x16x32_bf16 v[112:115], a[252:255], a[204:207], v[112:115]\n" \
"ds_read_b64_tr_b16 a[168:169], v34 offset:32768\n" \
"v_mfma_f32_16x16x32_bf16 v[108:111], a[184:187], a[140:143], 0\n" \
"s_movk_i32 s84, 0x2180\n" \
"s_nop 0\n" \
"v_mfma_f32_16x16x32_bf16 v[108:111], a[248:251], a[204:207], v[108:111]\n" \
"buffer_load_dwordx4 v25, s[76:79], s84 offen offset:1024 lds\n" \
"v_mfma_f32_16x16x32_bf16 v[104:107], a[180:183], a[140:143], 0\n" \
"ds_read_b64_tr_b16 a[170:171], v34 offset:33280\n" \
"v_mfma_f32_16x16x32_bf16 v[104:107], a[244:247], a[204:207], v[104:107]\n" \
"ds_read_b64_tr_b16 a[232:233], v34 offset:36864\n" \
"v_mfma_f32_16x16x32_bf16 v[100:103], a[176:179], a[140:143], 0\n" \
"v_mfma_f32_16x16x32_bf16 v[100:103], a[240:243], a[204:207], v[100:103]\n" \
"ds_read_b128 a[140:143], v28 offset:38912\n" \
"v_mfma_f32_16x16x32_bf16 v[52:55], a[176:179], a[128:131], 0\n" \
"ds_read_b128 a[204:207], v29 offset:38912\n" \
"v_mfma_f32_16x16x32_bf16 v[52:55], a[240:243], a[192:195], v[52:55]\n" \
"s_movk_i32 s84, 0x4180\n" \
"v_mfma_f32_16x16x32_bf16 v[56:59], a[180:183], a[128:131], 0\n" \
"buffer_load_dwordx4 v24, s[76:79], s84 offen offset:2048 lds\n" \
"v_mfma_f32_16x16x32_bf16 v[56:59], a[244:247], a[192:195], v[56:59]\n" \
"s_movk_i32 s84, 0x6180\n" \
"v_mfma_f32_16x16x32_bf16 v[60:63], a[184:187], a[128:131], 0\n" \
"buffer_load_dwordx4 v25, s[76:79], s84 offen offset:3072 lds\n" \
"v_mfma_f32_16x16x32_bf16 v[60:63], a[248:251], a[192:195], v[60:63]\n" \
"ds_read_b64_tr_b16 a[234:235], v34 offset:37376\n" \
"v_mfma_f32_16x16x32_bf16 v[64:67], a[188:191], a[128:131], 0\n" \
"v_mfma_f32_16x16x32_bf16 v[64:67], a[252:255], a[192:195], v[64:67]\n" \
"ds_read_b128 a[128:131], v28 offset:32768\n" \
"v_mfma_f32_16x16x32_bf16 v[80:83], a[188:191], a[132:135], 0\n" \
"ds_read_b128 a[192:195], v29 offset:32768\n" \
"v_mfma_f32_16x16x32_bf16 v[80:83], a[252:255], a[196:199], v[80:83]\n" \
"v_mfma_f32_16x16x32_bf16 v[76:79], a[184:187], a[132:135], 0\n" \
"v_mfma_f32_16x16x32_bf16 v[76:79], a[248:251], a[196:199], v[76:79]\n" \
"v_mfma_f32_16x16x32_bf16 v[72:75], a[180:183], a[132:135], 0\n" \
"v_mfma_f32_16x16x32_bf16 v[72:75], a[244:247], a[196:199], v[72:75]\n" \
"v_mfma_f32_16x16x32_bf16 v[68:71], a[176:179], a[132:135], 0\n" \
"v_mfma_f32_16x16x32_bf16 v[68:71], a[240:243], a[196:199], v[68:71]\n" \
"ds_read_b128 a[132:135], v28 offset:34816\n" \
"ds_read_b128 a[196:199], v29 offset:34816\n" \
"s_waitcnt lgkmcnt(15)\n" \
"v_mfma_f32_16x16x32_bf16 a[0:3], a[160:163], a[144:147], a[0:3]\n" \
"s_add_u32 m0, s30, 0x1000\n" \
"s_addk_i32 s84, 0x3000\n" \
"v_mfma_f32_16x16x32_bf16 a[0:3], a[224:227], a[208:211], a[0:3]\n" \
"buffer_load_dwordx4 v24, s[76:79], s84 offen offset:0 lds\n" \
"v_mfma_f32_16x16x32_bf16 a[4:7], a[164:167], a[144:147], a[4:7]\n" \
"ds_read_b64_tr_b16 a[176:177], v36 offset:32768\n" \
"v_mov_b32_e64 v11, 0\n" \
"v_mfma_f32_16x16x32_bf16 a[4:7], a[228:231], a[208:211], a[4:7]\n" \
"ds_read_b64_tr_b16 a[178:179], v36 offset:33792\n" \
"s_and_b32 s6, s2, 7\n" \
"s_waitcnt lgkmcnt(15)\n" \
"v_mfma_f32_16x16x32_bf16 a[12:15], a[172:175], a[144:147], a[12:15]\n" \
"ds_read_b64_tr_b16 a[240:241], v36 offset:40960\n" \
"s_lshl_b32 s7, s3, 1\n" \
"s_waitcnt lgkmcnt(15)\n" \
"v_mfma_f32_16x16x32_bf16 a[12:15], a[236:239], a[208:211], a[12:15]\n" \
"s_add_u32 s88, s41, 0x3000\n" \
"buffer_store_dwordx4 v[196:199], v20, s[80:83], s88 offen offset:2048 sc1\n" \
"s_waitcnt lgkmcnt(11)\n" \
"v_mfma_f32_16x16x32_bf16 a[8:11], a[144:147], a[168:171], a[8:11]\n" \
"ds_read_b64_tr_b16 a[242:243], v36 offset:41984\n" \
"s_lshr_b32 s4, s2, 5\n" \
"s_waitcnt lgkmcnt(8)\n" \
"v_mfma_f32_16x16x32_bf16 a[8:11], a[208:211], a[232:235], a[8:11]\n" \
"ds_read_b64_tr_b16 a[180:181], v37 offset:32768\n" \
"s_lshl_b32 s5, s6, 2\n" \
"v_mfma_f32_16x16x32_bf16 a[24:27], a[148:151], a[168:171], a[24:27]\n" \
"ds_read_b64_tr_b16 a[182:183], v37 offset:33792\n" \
"s_bfe_u32 s8, s2, 0x20003\n" \
"v_mfma_f32_16x16x32_bf16 a[24:27], a[212:215], a[232:235], a[24:27]\n" \
"ds_read_b64_tr_b16 a[244:245], v37 offset:40960\n" \
"s_and_b32 s7, s7, 4\n" \
"v_mfma_f32_16x16x32_bf16 a[28:31], a[172:175], a[148:151], a[28:31]\n" \
"s_and_b32 s9, s3, 1\n" \
"s_or_b32 s13, s5, s8\n" \
"v_mfma_f32_16x16x32_bf16 a[28:31], a[236:239], a[212:215], a[28:31]\n" \
"s_addk_i32 s84, 0x2000\n" \
"v_mfma_f32_16x16x32_bf16 a[20:23], a[164:167], a[148:151], a[20:23]\n" \
"buffer_load_dwordx4 v25, s[76:79], s84 offen offset:1024 lds\n" \
"v_mfma_f32_16x16x32_bf16 a[20:23], a[228:231], a[212:215], a[20:23]\n" \
"ds_read_b64_tr_b16 a[246:247], v37 offset:41984\n" \
"s_or_b32 s9, s7, s9\n" \
"v_mfma_f32_16x16x32_bf16 a[16:19], a[160:163], a[148:151], a[16:19]\n" \
"ds_read_b64_tr_b16 a[184:185], v38 offset:32768\n" \
"s_mul_i32 s7, s4, 0xa00\n" \
"v_mfma_f32_16x16x32_bf16 a[16:19], a[224:227], a[212:215], a[16:19]\n" \
"ds_read_b64_tr_b16 a[186:187], v38 offset:33792\n" \
"s_add_i32 s7, s7, 0x5bc1000\n" \
"v_mfma_f32_16x16x32_bf16 a[32:35], a[160:163], a[152:155], a[32:35]\n" \
"s_addk_i32 s84, 0x2000\n" \
"v_mfma_f32_16x16x32_bf16 a[32:35], a[224:227], a[216:219], a[32:35]\n" \
"buffer_load_dwordx4 v24, s[76:79], s84 offen offset:2048 lds\n" \
"v_mfma_f32_16x16x32_bf16 a[36:39], a[164:167], a[152:155], a[36:39]\n" \
"ds_read_b64_tr_b16 a[248:249], v38 offset:40960\n" \
"s_lshl_b32 s10, s13, 2\n" \
"v_mfma_f32_16x16x32_bf16 a[36:39], a[228:231], a[216:219], a[36:39]\n" \
"ds_read_b64_tr_b16 a[250:251], v38 offset:41984\n" \
"s_or_b32 s10, s10, s7\n" \
"v_mfma_f32_16x16x32_bf16 a[44:47], a[172:175], a[152:155], a[44:47]\n" \
"ds_read_b64_tr_b16 a[188:189], v39 offset:32768\n" \
"v_writelane_b32 v11, s10, 0\n" \
"v_mfma_f32_16x16x32_bf16 a[44:47], a[236:239], a[216:219], a[44:47]\n" \
"ds_read_b64_tr_b16 a[190:191], v39 offset:33792\n" \
"s_lshl_b32 s14, s4, 3\n" \
"v_mfma_f32_16x16x32_bf16 a[40:43], a[152:155], a[168:171], a[40:43]\n" \
"s_add_u32 s89, s41, 0x3000\n" \
"buffer_store_dwordx4 a[84:87], v20, s[80:83], s89 offen offset:1024 sc1\n" \
"v_mfma_f32_16x16x32_bf16 a[40:43], a[216:219], a[232:235], a[40:43]\n" \
"ds_read_b64_tr_b16 a[252:253], v39 offset:40960\n" \
"v_writelane_b32 v11, s7, 1\n" \
"v_mfma_f32_16x16x32_bf16 a[56:59], a[156:159], a[168:171], a[56:59]\n" \
"ds_read_b64_tr_b16 a[254:255], v39 offset:41984\n" \
"s_or_b32 s7, s14, s6\n" \
"v_mfma_f32_16x16x32_bf16 a[56:59], a[220:223], a[232:235], a[56:59]\n" \
"s_lshl_b32 s7, s7, 10\n" \
"s_lshl_b32 s10, s3, 8\n" \
"v_mfma_f32_16x16x32_bf16 a[60:63], a[172:175], a[156:159], a[60:63]\n" \
"s_or_b32 s7, s7, s10\n" \
"s_or_b32 s10, s7, 0x5c00200\n" \
"v_mfma_f32_16x16x32_bf16 a[60:63], a[236:239], a[220:223], a[60:63]\n" \
"s_addk_i32 s84, 0x2000\n" \
"v_mfma_f32_16x16x32_bf16 a[52:55], a[164:167], a[156:159], a[52:55]\n" \
"buffer_load_dwordx4 v25, s[76:79], s84 offen offset:3072 lds\n" \
"v_mfma_f32_16x16x32_bf16 a[52:55], a[228:231], a[220:223], a[52:55]\n" \
"v_writelane_b32 v11, s10, 2\n" \
"s_lshl_b32 s10, s8, 2\n" \
"v_mfma_f32_16x16x32_bf16 a[48:51], a[160:163], a[156:159], a[48:51]\n" \
"s_or_b32 s11, s10, s7\n" \
"s_or_b32 s11, s11, 0x5c00000\n" \
"v_mfma_f32_16x16x32_bf16 a[48:51], a[224:227], a[220:223], a[48:51]\n" \
"v_writelane_b32 v11, s11, 3\n" \
"s_or_b32 s7, s7, 0x5c00080\n" \
"v_mfma_f32_16x16x32_bf16 a[64:67], a[164:167], a[140:143], a[64:67]\n" \
"v_writelane_b32 v11, s7, 4\n" \
"s_or_b32 s7, s7, s10\n" \
"v_mfma_f32_16x16x32_bf16 a[64:67], a[228:231], a[204:207], a[64:67]\n" \
"s_add_i32 s10, s3, s2\n" \
"v_writelane_b32 v11, s7, 5\n" \
"v_mfma_f32_16x16x32_bf16 a[80:83], a[172:175], a[140:143], a[80:83]\n" \
"s_mov_b32 s7, 2.0\n" \
"s_mov_b32 s11, 0x5bc0000\n" \
"v_mfma_f32_16x16x32_bf16 a[80:83], a[236:239], a[204:207], a[80:83]\n" \
"s_cmp_lg_u32 s10, 0\n" \
"s_cselect_b32 s10, s7, s11\n" \
"v_mfma_f32_16x16x32_bf16 a[68:71], a[140:143], a[168:171], a[68:71]\n" \
"s_lshr_b32 s15, s2, 3\n" \
"v_writelane_b32 v11, s10, 54\n" \
"v_mfma_f32_16x16x32_bf16 a[68:71], a[204:207], a[232:235], a[68:71]\n" \
"s_waitcnt vmcnt(11) lgkmcnt(0)\n" \
"s_barrier\n" \
"v_xor_b32_e32 v28, 0x20000, v28\n" \
"v_xor_b32_e32 v29, 0x20000, v29\n" \
"v_mfma_f32_16x16x32_bf16 v[116:119], a[176:179], a[144:147], v[116:119]\n" \
"s_add_u32 m0, s40, 0x8000\n" \
"s_add_u32 s85, s39, 0x6000\n" \
"v_mfma_f32_16x16x32_bf16 v[116:119], a[240:243], a[208:211], v[116:119]\n" \
"buffer_load_dwordx4 v20, s[80:83], s85 offen sc1 lds\n" \
"v_mfma_f32_16x16x32_bf16 v[120:123], a[180:183], a[144:147], v[120:123]\n" \
"ds_read_b64_tr_b16 a[160:161], v32 offset:0\n" \
"s_lshl_b32 s10, s6, 5\n" \
"v_mfma_f32_16x16x32_bf16 v[120:123], a[244:247], a[208:211], v[120:123]\n" \
"ds_read_b64_tr_b16 a[162:163], v32 offset:512\n" \
"s_min_u32 s16, s3, 2\n" \
"v_mfma_f32_16x16x32_bf16 v[124:127], a[184:187], a[144:147], v[124:127]\n" \
"s_add_u32 m0, s40, 0x9000\n" \
"s_add_u32 s85, s39, 0x7000\n" \
"v_mfma_f32_16x16x32_bf16 v[124:127], a[248:251], a[208:211], v[124:127]\n" \
"buffer_load_dwordx4 v20, s[80:83], s85 offen sc1 lds\n" \
"v_mfma_f32_16x16x32_bf16 v[128:131], a[188:191], a[144:147], v[128:131]\n" \
"ds_read_b64_tr_b16 a[224:225], v32 offset:4096\n" \
"s_mul_i32 s12, s8, 0x120\n" \
"v_mfma_f32_16x16x32_bf16 v[128:131], a[252:255], a[208:211], v[128:131]\n" \
"ds_read_b128 a[144:147], v28 offset:8192\n" \
"v_mfma_f32_16x16x32_bf16 v[144:147], a[188:191], a[148:151], v[144:147]\n" \
"ds_read_b128 a[208:211], v29 offset:8192\n" \
"v_mfma_f32_16x16x32_bf16 v[144:147], a[252:255], a[212:215], v[144:147]\n" \
"s_add_u32 m0, s40, 0xb000\n" \
"s_movk_i32 s84, 0x180\n" \
"v_mfma_f32_16x16x32_bf16 v[140:143], a[184:187], a[148:151], v[140:143]\n" \
"buffer_load_dwordx4 v21, s[76:79], s84 offen offset:0 lds\n" \
"v_mfma_f32_16x16x32_bf16 v[140:143], a[248:251], a[212:215], v[140:143]\n" \
"ds_read_b64_tr_b16 a[226:227], v32 offset:4608\n" \
"v_mfma_f32_16x16x32_bf16 v[136:139], a[180:183], a[148:151], v[136:139]\n" \
"s_add_u32 s88, s41, 0x3000\n" \
"buffer_store_dwordx4 v[252:255], v20, s[80:83], s88 offen offset:0 sc1\n" \
"v_mfma_f32_16x16x32_bf16 v[136:139], a[244:247], a[212:215], v[136:139]\n" \
"ds_read_b64_tr_b16 a[164:165], v33 offset:0\n" \
"v_mfma_f32_16x16x32_bf16 v[132:135], a[176:179], a[148:151], v[132:135]\n" \
"s_add_u32 s11, s22, s12\n" \
"s_addc_u32 s17, s23, 0\n" \
"s_nop 0\n" \
"v_mfma_f32_16x16x32_bf16 v[132:135], a[240:243], a[212:215], v[132:135]\n" \
"ds_read_b128 a[148:151], v28 offset:10240\n" \
"v_mfma_f32_16x16x32_bf16 v[148:151], a[176:179], a[152:155], v[148:151]\n" \
"ds_read_b128 a[212:215], v29 offset:10240\n" \
"v_mfma_f32_16x16x32_bf16 v[148:151], a[240:243], a[216:219], v[148:151]\n" \
"s_add_u32 m0, s31, 0x8000\n" \
"s_mov_b32 s84, 0x180000\n" \
"v_mfma_f32_16x16x32_bf16 v[152:155], a[180:183], a[152:155], v[152:155]\n" \
"buffer_load_dwordx4 v26, s[68:71], s84 offen offset:0 lds\n" \
"v_mfma_f32_16x16x32_bf16 v[152:155], a[244:247], a[216:219], v[152:155]\n" \
"ds_read_b64_tr_b16 a[166:167], v33 offset:512\n" \
"v_mfma_f32_16x16x32_bf16 v[156:159], a[184:187], a[152:155], v[156:159]\n" \
"ds_read_b64_tr_b16 a[228:229], v33 offset:4096\n" \
"v_mfma_f32_16x16x32_bf16 v[156:159], a[248:251], a[216:219], v[156:159]\n" \
"s_addk_i32 s84, 0x7c00\n" \
"v_mfma_f32_16x16x32_bf16 v[160:163], a[188:191], a[152:155], v[160:163]\n" \
"buffer_load_dwordx4 v26, s[68:71], s84 offen offset:1024 lds\n" \
"v_mfma_f32_16x16x32_bf16 v[160:163], a[252:255], a[216:219], v[160:163]\n" \
"ds_read_b128 a[152:155], v28 offset:12288\n" \
"v_mfma_f32_16x16x32_bf16 v[176:179], a[188:191], a[156:159], v[176:179]\n" \
"ds_read_b128 a[216:219], v29 offset:12288\n" \
"v_mfma_f32_16x16x32_bf16 v[176:179], a[252:255], a[220:223], v[176:179]\n" \
"s_addk_i32 s84, 0x7c00\n" \
"v_mfma_f32_16x16x32_bf16 v[172:175], a[184:187], a[156:159], v[172:175]\n" \
"buffer_load_dwordx4 v27, s[68:71], s84 offen offset:2048 lds\n" \
"v_mfma_f32_16x16x32_bf16 v[172:175], a[248:251], a[220:223], v[172:175]\n" \
"ds_read_b64_tr_b16 a[230:231], v33 offset:4608\n" \
"v_mfma_f32_16x16x32_bf16 v[168:171], a[180:183], a[156:159], v[168:171]\n" \
"ds_read_b64_tr_b16 a[172:173], v35 offset:0\n" \
"v_mfma_f32_16x16x32_bf16 v[168:171], a[244:247], a[220:223], v[168:171]\n" \
"ds_read_b64_tr_b16 a[174:175], v35 offset:512\n" \
"v_mfma_f32_16x16x32_bf16 v[164:167], a[176:179], a[156:159], v[164:167]\n" \
"s_add_u32 s18, s24, s12\n" \
"v_writelane_b32 v11, s11, 9\n" \
"s_addc_u32 s19, s25, 0\n" \
"v_mfma_f32_16x16x32_bf16 v[164:167], a[240:243], a[220:223], v[164:167]\n" \
"ds_read_b128 a[156:159], v28 offset:14336\n" \
"s_and_b32 s20, s17, 0xffff\n" \
"v_mfma_f32_16x16x32_bf16 v[84:87], a[176:179], a[136:139], v[84:87]\n" \
"ds_read_b128 a[220:223], v29 offset:14336\n" \
"v_writelane_b32 v11, s20, 10\n" \
"v_mfma_f32_16x16x32_bf16 v[84:87], a[240:243], a[200:203], v[84:87]\n" \
"s_addk_i32 s84, 0x7c00\n" \
"v_mfma_f32_16x16x32_bf16 v[88:91], a[180:183], a[136:139], v[88:91]\n" \
"buffer_load_dwordx4 v27, s[68:71], s84 offen offset:3072 lds\n" \
"v_mfma_f32_16x16x32_bf16 v[88:91], a[244:247], a[200:203], v[88:91]\n" \
"s_add_u32 m0, s30, 0x8000\n" \
"s_movk_i32 s84, 0x200\n" \
"v_mfma_f32_16x16x32_bf16 v[92:95], a[184:187], a[136:139], v[92:95]\n" \
"buffer_load_dwordx4 v24, s[76:79], s84 offen offset:0 lds\n" \
"v_mfma_f32_16x16x32_bf16 v[92:95], a[248:251], a[200:203], v[92:95]\n" \
"ds_read_b64_tr_b16 a[236:237], v35 offset:4096\n" \
"s_and_b32 s20, s19, 0xffff\n" \
"v_mfma_f32_16x16x32_bf16 v[96:99], a[188:191], a[136:139], v[96:99]\n" \
"ds_read_b64_tr_b16 a[238:239], v35 offset:4608\n" \
"v_writelane_b32 v11, s18, 11\n" \
"v_mfma_f32_16x16x32_bf16 v[96:99], a[252:255], a[200:203], v[96:99]\n" \
"ds_read_b128 a[136:139], v28 offset:4096\n" \
"v_mfma_f32_16x16x32_bf16 v[112:115], a[188:191], a[140:143], v[112:115]\n" \
"ds_read_b128 a[200:203], v29 offset:4096\n" \
"v_mfma_f32_16x16x32_bf16 v[112:115], a[252:255], a[204:207], v[112:115]\n" \
"s_movk_i32 s84, 0x2200\n" \
"s_nop 0\n" \
"v_mfma_f32_16x16x32_bf16 v[108:111], a[184:187], a[140:143], v[108:111]\n" \
"buffer_load_dwordx4 v25, s[76:79], s84 offen offset:1024 lds\n" \
"v_mfma_f32_16x16x32_bf16 v[108:111], a[248:251], a[204:207], v[108:111]\n" \
"s_add_u32 s89, s41, 0x1000\n" \
"buffer_store_dwordx4 v[200:203], v20, s[80:83], s89 offen offset:3072 sc1\n" \
"v_mfma_f32_16x16x32_bf16 v[104:107], a[180:183], a[140:143], v[104:107]\n" \
"ds_read_b64_tr_b16 a[168:169], v34 offset:0\n" \
"v_mfma_f32_16x16x32_bf16 v[104:107], a[244:247], a[204:207], v[104:107]\n" \
"ds_read_b64_tr_b16 a[170:171], v34 offset:512\n" \
"v_mfma_f32_16x16x32_bf16 v[100:103], a[176:179], a[140:143], v[100:103]\n" \
"ds_read_b64_tr_b16 a[232:233], v34 offset:4096\n" \
"v_mfma_f32_16x16x32_bf16 v[100:103], a[240:243], a[204:207], v[100:103]\n" \
"ds_read_b128 a[140:143], v28 offset:6144\n" \
"v_mfma_f32_16x16x32_bf16 v[52:55], a[176:179], a[128:131], v[52:55]\n" \
"ds_read_b128 a[204:207], v29 offset:6144\n" \
"v_mfma_f32_16x16x32_bf16 v[52:55], a[240:243], a[192:195], v[52:55]\n" \
"s_movk_i32 s84, 0x4200\n" \
"v_mfma_f32_16x16x32_bf16 v[56:59], a[180:183], a[128:131], v[56:59]\n" \
"buffer_load_dwordx4 v24, s[76:79], s84 offen offset:2048 lds\n" \
"v_mfma_f32_16x16x32_bf16 v[56:59], a[244:247], a[192:195], v[56:59]\n" \
"s_movk_i32 s84, 0x6200\n" \
"v_mfma_f32_16x16x32_bf16 v[60:63], a[184:187], a[128:131], v[60:63]\n" \
"buffer_load_dwordx4 v25, s[76:79], s84 offen offset:3072 lds\n" \
"v_mfma_f32_16x16x32_bf16 v[60:63], a[248:251], a[192:195], v[60:63]\n" \
"ds_read_b64_tr_b16 a[234:235], v34 offset:4608\n" \
"v_mfma_f32_16x16x32_bf16 v[64:67], a[188:191], a[128:131], v[64:67]\n" \
"s_cmpk_lt_u32 s3, 3\n" \
"s_cselect_b32 s11, s11, s18\n" \
"v_mfma_f32_16x16x32_bf16 v[64:67], a[252:255], a[192:195], v[64:67]\n" \
"ds_read_b128 a[128:131], v28 offset:0\n" \
"s_movk_i32 s18, 0xfff\n" \
"v_mfma_f32_16x16x32_bf16 v[80:83], a[188:191], a[132:135], v[80:83]\n" \
"ds_read_b128 a[192:195], v29 offset:0\n" \
"v_writelane_b32 v11, s20, 12\n" \
"v_mfma_f32_16x16x32_bf16 v[80:83], a[252:255], a[196:199], v[80:83]\n" \
"s_and_b32 s20, s29, 0xffff\n" \
"v_writelane_b32 v11, s28, 13\n" \
"v_mfma_f32_16x16x32_bf16 v[76:79], a[184:187], a[132:135], v[76:79]\n" \
"v_writelane_b32 v11, s20, 14\n" \
"v_writelane_b32 v11, s11, 15\n" \
"v_mfma_f32_16x16x32_bf16 v[76:79], a[248:251], a[196:199], v[76:79]\n" \
"s_cmpk_lt_u32 s3, 3\n" \
"s_cselect_b32 s11, s17, s19\n" \
"v_mfma_f32_16x16x32_bf16 v[72:75], a[180:183], a[132:135], v[72:75]\n" \
"s_and_b32 s11, s11, 0xffff\n" \
"v_writelane_b32 v11, s11, 16\n" \
"v_mfma_f32_16x16x32_bf16 v[72:75], a[244:247], a[196:199], v[72:75]\n" \
"s_mul_i32 s11, s6, 0x48\n" \
"s_add_i32 s17, s11, 0x47\n" \
"v_mfma_f32_16x16x32_bf16 v[68:71], a[176:179], a[132:135], v[68:71]\n" \
"s_cmpk_lt_u32 s3, 3\n" \
"s_cselect_b32 s17, s17, s18\n" \
"v_mfma_f32_16x16x32_bf16 v[68:71], a[240:243], a[196:199], v[68:71]\n" \
"ds_read_b128 a[132:135], v28 offset:2048\n" \
"ds_read_b128 a[196:199], v29 offset:2048\n" \
"s_waitcnt lgkmcnt(15)\n" \
"v_mfma_f32_16x16x32_bf16 a[0:3], a[160:163], a[144:147], a[0:3]\n" \
"s_add_u32 m0, s30, 0x9000\n" \
"s_addk_i32 s84, 0x3000\n" \
"v_mfma_f32_16x16x32_bf16 a[0:3], a[224:227], a[208:211], a[0:3]\n" \
"buffer_load_dwordx4 v24, s[76:79], s84 offen offset:0 lds\n" \
"v_mfma_f32_16x16x32_bf16 a[4:7], a[164:167], a[144:147], a[4:7]\n" \
"ds_read_b64_tr_b16 a[176:177], v36 offset:0\n" \
"s_mulk_i32 s17, 0x480\n" \
"v_mfma_f32_16x16x32_bf16 a[4:7], a[228:231], a[208:211], a[4:7]\n" \
"ds_read_b64_tr_b16 a[178:179], v36 offset:1024\n" \
"s_addk_i32 s17, 0x120\n" \
"v_mfma_f32_16x16x32_bf16 a[12:15], a[172:175], a[144:147], a[12:15]\n" \
"ds_read_b64_tr_b16 a[240:241], v36 offset:8192\n" \
"v_writelane_b32 v11, s17, 17\n" \
"s_waitcnt lgkmcnt(15)\n" \
"v_mfma_f32_16x16x32_bf16 a[12:15], a[236:239], a[208:211], a[12:15]\n" \
"s_add_u32 s88, s41, 0x1000\n" \
"buffer_store_dwordx4 v[204:207], v20, s[80:83], s88 offen offset:2048 sc1\n" \
"s_waitcnt lgkmcnt(11)\n" \
"v_mfma_f32_16x16x32_bf16 a[8:11], a[144:147], a[168:171], a[8:11]\n" \
"ds_read_b64_tr_b16 a[242:243], v36 offset:9216\n" \
"s_lshl_b32 s17, s3, 5\n" \
"s_waitcnt lgkmcnt(8)\n" \
"v_mfma_f32_16x16x32_bf16 a[8:11], a[208:211], a[232:235], a[8:11]\n" \
"ds_read_b64_tr_b16 a[180:181], v37 offset:0\n" \
"s_add_i32 s11, s11, s17\n" \
"v_mfma_f32_16x16x32_bf16 a[24:27], a[148:151], a[168:171], a[24:27]\n" \
"ds_read_b64_tr_b16 a[182:183], v37 offset:1024\n" \
"s_mul_i32 s17, s11, 0x480\n" \
"v_mfma_f32_16x16x32_bf16 a[24:27], a[212:215], a[232:235], a[24:27]\n" \
"ds_read_b64_tr_b16 a[244:245], v37 offset:8192\n" \
"s_mul_i32 s11, s13, 0x24000\n" \
"v_mfma_f32_16x16x32_bf16 a[28:31], a[172:175], a[148:151], a[28:31]\n" \
"s_add_i32 s18, s11, 0x1b000\n" \
"v_mfma_f32_16x16x32_bf16 a[28:31], a[236:239], a[212:215], a[28:31]\n" \
"s_addk_i32 s84, 0x2000\n" \
"v_mfma_f32_16x16x32_bf16 a[20:23], a[164:167], a[148:151], a[20:23]\n" \
"buffer_load_dwordx4 v25, s[76:79], s84 offen offset:1024 lds\n" \
"v_mfma_f32_16x16x32_bf16 a[20:23], a[228:231], a[212:215], a[20:23]\n" \
"ds_read_b64_tr_b16 a[246:247], v37 offset:9216\n" \
"v_mfma_f32_16x16x32_bf16 a[16:19], a[160:163], a[148:151], a[16:19]\n" \
"ds_read_b64_tr_b16 a[184:185], v38 offset:0\n" \
"v_mfma_f32_16x16x32_bf16 a[16:19], a[224:227], a[212:215], a[16:19]\n" \
"ds_read_b64_tr_b16 a[186:187], v38 offset:1024\n" \
"v_mfma_f32_16x16x32_bf16 a[32:35], a[160:163], a[152:155], a[32:35]\n" \
"s_addk_i32 s84, 0x2000\n" \
"v_mfma_f32_16x16x32_bf16 a[32:35], a[224:227], a[216:219], a[32:35]\n" \
"buffer_load_dwordx4 v24, s[76:79], s84 offen offset:2048 lds\n" \
"v_mfma_f32_16x16x32_bf16 a[36:39], a[164:167], a[152:155], a[36:39]\n" \
"ds_read_b64_tr_b16 a[248:249], v38 offset:8192\n" \
".fill (L_pb + 65248 - .) / 4, 4, 0xBF800000\n" \
"L_pp16:\n" \
"s_cbranch_execz L_pret\n" \
"s_nop 0\n" \
"v_mfma_f32_16x16x32_bf16 a[36:39], a[228:231], a[216:219], a[36:39]\n" \
"ds_read_b64_tr_b16 a[250:251], v38 offset:9216\n" \
"v_mfma_f32_16x16x32_bf16 a[44:47], a[172:175], a[152:155], a[44:47]\n" \
"ds_read_b64_tr_b16 a[188:189], v39 offset:0\n" \
"v_mfma_f32_16x16x32_bf16 a[44:47], a[236:239], a[216:219], a[44:47]\n" \
"ds_read_b64_tr_b16 a[190:191], v39 offset:1024\n" \
"v_mfma_f32_16x16x32_bf16 a[40:43], a[152:155], a[168:171], a[40:43]\n" \
"s_add_u32 s89, s41, 0x1000\n" \
"buffer_store_dwordx4 v[208:211], v20, s[80:83], s89 offen offset:1024 sc1\n" \
"v_mfma_f32_16x16x32_bf16 a[40:43], a[216:219], a[232:235], a[40:43]\n" \
"ds_read_b64_tr_b16 a[252:253], v39 offset:8192\n" \
"v_mfma_f32_16x16x32_bf16 a[56:59], a[156:159], a[168:171], a[56:59]\n" \
"ds_read_b64_tr_b16 a[254:255], v39 offset:9216\n" \
"v_mfma_f32_16x16x32_bf16 a[56:59], a[220:223], a[232:235], a[56:59]\n" \
"s_cmpk_lt_u32 s3, 3\n" \
"s_cselect_b32 s17, s17, s18\n" \
"v_mfma_f32_16x16x32_bf16 a[60:63], a[172:175], a[156:159], a[60:63]\n" \
"s_mov_b32 s18, 0x8000\n" \
"v_writelane_b32 v11, s17, 18\n" \
"v_mfma_f32_16x16x32_bf16 a[60:63], a[236:239], a[220:223], a[60:63]\n" \
"s_addk_i32 s84, 0x2000\n" \
"v_mfma_f32_16x16x32_bf16 a[52:55], a[164:167], a[156:159], a[52:55]\n" \
"buffer_load_dwordx4 v25, s[76:79], s84 offen offset:3072 lds\n" \
"v_mfma_f32_16x16x32_bf16 a[52:55], a[228:231], a[220:223], a[52:55]\n" \
"s_mul_i32 s17, s16, 0x2400\n" \
"s_mul_i32 s16, s16, 0x9000\n" \
"v_mfma_f32_16x16x32_bf16 a[48:51], a[160:163], a[156:159], a[48:51]\n" \
"s_bitset1_b32 s17, 17\n" \
"v_writelane_b32 v11, s17, 19\n" \
"v_mfma_f32_16x16x32_bf16 a[48:51], a[224:227], a[220:223], a[48:51]\n" \
"s_add_i32 s16, s11, s16\n" \
"v_writelane_b32 v11, s16, 26\n" \
"v_mfma_f32_16x16x32_bf16 a[64:67], a[164:167], a[140:143], a[64:67]\n" \
"s_mul_i32 s16, s3, 0x2400\n" \
"s_add_i32 s17, s16, 0xa400\n" \
"v_mfma_f32_16x16x32_bf16 a[64:67], a[228:231], a[204:207], a[64:67]\n" \
"s_cmpk_lt_u32 s3, 3\n" \
"s_cselect_b32 s17, s17, s18\n" \
"v_mfma_f32_16x16x32_bf16 a[80:83], a[172:175], a[140:143], a[80:83]\n" \
"s_mov_b32 s18, 0x1b000\n" \
"s_xor_b32 s19, s3, 2\n" \
"v_mfma_f32_16x16x32_bf16 a[80:83], a[236:239], a[204:207], a[80:83]\n" \
"s_cmp_lg_u32 s19, 0\n" \
"s_cselect_b32 s18, s17, s18\n" \
"v_mfma_f32_16x16x32_bf16 a[68:71], a[140:143], a[168:171], a[68:71]\n" \
"v_writelane_b32 v11, s17, 20\n" \
"s_or_b32 s17, s16, 0x10000\n" \
"v_mfma_f32_16x16x32_bf16 a[68:71], a[204:207], a[232:235], a[68:71]\n" \
"s_waitcnt vmcnt(11) lgkmcnt(0)\n" \
"s_barrier\n" \
"v_xor_b32_e32 v28, 0x20000, v28\n" \
"v_xor_b32_e32 v29, 0x20000, v29\n" \
"v_mfma_f32_16x16x32_bf16 v[116:119], a[176:179], a[144:147], v[116:119]\n" \
"s_add_u32 m0, s40, 0x0\n" \
"s_nop 0\n" \
"s_add_u32 s85, s39, 0x8000\n" \
"v_mfma_f32_16x16x32_bf16 v[116:119], a[240:243], a[208:211], v[116:119]\n" \
"buffer_load_dwordx4 v20, s[80:83], s85 offen sc1 lds\n" \
"v_mfma_f32_16x16x32_bf16 v[120:123], a[180:183], a[144:147], v[120:123]\n" \
"ds_read_b64_tr_b16 a[160:161], v32 offset:32768\n" \
"v_writelane_b32 v11, s18, 21\n" \
"v_mfma_f32_16x16x32_bf16 v[120:123], a[244:247], a[208:211], v[120:123]\n" \
"ds_read_b64_tr_b16 a[162:163], v32 offset:33280\n" \
"s_add_i32 s18, s16, 0x19000\n" \
"v_mfma_f32_16x16x32_bf16 v[124:127], a[184:187], a[144:147], v[124:127]\n" \
"s_add_u32 m0, s40, 0x1000\n" \
"s_add_u32 s85, s39, 0x9000\n" \
"v_mfma_f32_16x16x32_bf16 v[124:127], a[248:251], a[208:211], v[124:127]\n" \
"buffer_load_dwordx4 v20, s[80:83], s85 offen sc1 lds\n" \
"v_mfma_f32_16x16x32_bf16 v[128:131], a[188:191], a[144:147], v[128:131]\n" \
"ds_read_b64_tr_b16 a[224:225], v32 offset:36864\n" \
"v_writelane_b32 v11, s17, 22\n" \
"v_mfma_f32_16x16x32_bf16 v[128:131], a[252:255], a[208:211], v[128:131]\n" \
"ds_read_b128 a[144:147], v28 offset:8192\n" \
"s_mov_b32 s17, 0\n" \
"v_mfma_f32_16x16x32_bf16 v[144:147], a[188:191], a[148:151], v[144:147]\n" \
"ds_read_b128 a[208:211], v29 offset:8192\n" \
"v_mfma_f32_16x16x32_bf16 v[144:147], a[252:255], a[212:215], v[144:147]\n" \
"s_add_u32 m0, s40, 0x3000\n" \
"s_movk_i32 s84, 0x200\n" \
"v_mfma_f32_16x16x32_bf16 v[140:143], a[184:187], a[148:151], v[140:143]\n" \
"buffer_load_dwordx4 v21, s[76:79], s84 offen offset:0 lds\n" \
"v_mfma_f32_16x16x32_bf16 v[140:143], a[248:251], a[212:215], v[140:143]\n" \
"ds_read_b64_tr_b16 a[226:227], v32 offset:37376\n" \
"v_mfma_f32_16x16x32_bf16 v[136:139], a[180:183], a[148:151], v[136:139]\n" \
"s_add_u32 s88, s41, 0x1000\n" \
"buffer_store_dwordx4 v[212:215], v20, s[80:83], s88 offen offset:0 sc1\n" \
"v_mfma_f32_16x16x32_bf16 v[136:139], a[244:247], a[212:215], v[136:139]\n" \
"ds_read_b64_tr_b16 a[164:165], v33 offset:32768\n" \
"v_mfma_f32_16x16x32_bf16 v[132:135], a[176:179], a[148:151], v[132:135]\n" \
"s_cmpk_lt_u32 s3, 3\n" \
"s_cselect_b32 s18, s18, s17\n" \
"v_mfma_f32_16x16x32_bf16 v[132:135], a[240:243], a[212:215], v[132:135]\n" \
"ds_read_b128 a[148:151], v28 offset:10240\n" \
"s_bitset1_b32 s16, 17\n" \
"v_mfma_f32_16x16x32_bf16 v[148:151], a[176:179], a[152:155], v[148:151]\n" \
"ds_read_b128 a[212:215], v29 offset:10240\n" \
"v_writelane_b32 v11, s18, 23\n" \
"v_mfma_f32_16x16x32_bf16 v[148:151], a[240:243], a[216:219], v[148:151]\n" \
"s_add_u32 m0, s31, 0x0\n" \
"s_mov_b32 s84, 0x200000\n" \
"v_mfma_f32_16x16x32_bf16 v[152:155], a[180:183], a[152:155], v[152:155]\n" \
"buffer_load_dwordx4 v26, s[68:71], s84 offen offset:0 lds\n" \
"v_mfma_f32_16x16x32_bf16 v[152:155], a[244:247], a[216:219], v[152:155]\n" \
"ds_read_b64_tr_b16 a[166:167], v33 offset:33280\n" \
"s_movk_i32 s18, 0x2400\n" \
"v_mfma_f32_16x16x32_bf16 v[156:159], a[184:187], a[152:155], v[156:159]\n" \
"ds_read_b64_tr_b16 a[228:229], v33 offset:36864\n" \
"v_mfma_f32_16x16x32_bf16 v[156:159], a[248:251], a[216:219], v[156:159]\n" \
"s_addk_i32 s84, 0x7c00\n" \
"v_mfma_f32_16x16x32_bf16 v[160:163], a[188:191], a[152:155], v[160:163]\n" \
"buffer_load_dwordx4 v26, s[68:71], s84 offen offset:1024 lds\n" \
"v_mfma_f32_16x16x32_bf16 v[160:163], a[252:255], a[216:219], v[160:163]\n" \
"ds_read_b128 a[152:155], v28 offset:12288\n" \
"v_mfma_f32_16x16x32_bf16 v[176:179], a[188:191], a[156:159], v[176:179]\n" \
"ds_read_b128 a[216:219], v29 offset:12288\n" \
"v_mfma_f32_16x16x32_bf16 v[176:179], a[252:255], a[220:223], v[176:179]\n" \
"s_addk_i32 s84, 0x7c00\n" \
"v_mfma_f32_16x16x32_bf16 v[172:175], a[184:187], a[156:159], v[172:175]\n" \
"buffer_load_dwordx4 v27, s[68:71], s84 offen offset:2048 lds\n" \
"v_mfma_f32_16x16x32_bf16 v[172:175], a[248:251], a[220:223], v[172:175]\n" \
"ds_read_b64_tr_b16 a[230:231], v33 offset:37376\n" \
"v_mfma_f32_16x16x32_bf16 v[168:171], a[180:183], a[156:159], v[168:171]\n" \
"ds_read_b64_tr_b16 a[172:173], v35 offset:32768\n" \
"v_mfma_f32_16x16x32_bf16 v[168:171], a[244:247], a[220:223], v[168:171]\n" \
"ds_read_b64_tr_b16 a[174:175], v35 offset:33280\n" \
"v_mfma_f32_16x16x32_bf16 v[164:167], a[176:179], a[156:159], v[164:167]\n" \
"s_cmpk_lt_u32 s3, 3\n" \
"s_cselect_b32 s16, s16, s18\n" \
"v_mfma_f32_16x16x32_bf16 v[164:167], a[240:243], a[220:223], v[164:167]\n" \
"ds_read_b128 a[156:159], v28 offset:14336\n" \
"s_mul_i32 s19, s3, 0x9000\n" \
"v_mfma_f32_16x16x32_bf16 v[84:87], a[176:179], a[136:139], v[84:87]\n" \
"ds_read_b128 a[220:223], v29 offset:14336\n" \
"v_writelane_b32 v11, s16, 24\n" \
"v_mfma_f32_16x16x32_bf16 v[84:87], a[240:243], a[200:203], v[84:87]\n" \
"s_addk_i32 s84, 0x7c00\n" \
"v_mfma_f32_16x16x32_bf16 v[88:91], a[180:183], a[136:139], v[88:91]\n" \
"buffer_load_dwordx4 v27, s[68:71], s84 offen offset:3072 lds\n" \
"v_mfma_f32_16x16x32_bf16 v[88:91], a[244:247], a[200:203], v[88:91]\n" \
"s_add_u32 m0, s30, 0x20000\n" \
"s_movk_i32 s84, 0x280\n" \
"v_mfma_f32_16x16x32_bf16 v[92:95], a[184:187], a[136:139], v[92:95]\n" \
"buffer_load_dwordx4 v24, s[76:79], s84 offen offset:0 lds\n" \
"v_mfma_f32_16x16x32_bf16 v[92:95], a[248:251], a[200:203], v[92:95]\n" \
"ds_read_b64_tr_b16 a[236:237], v35 offset:36864\n" \
"s_lshl_b32 s16, s3, 10\n" \
"v_mfma_f32_16x16x32_bf16 v[96:99], a[188:191], a[136:139], v[96:99]\n" \
"ds_read_b64_tr_b16 a[238:239], v35 offset:37376\n" \
"s_addk_i32 s16, 0x4800\n" \
"v_mfma_f32_16x16x32_bf16 v[96:99], a[252:255], a[200:203], v[96:99]\n" \
"ds_read_b128 a[136:139], v28 offset:4096\n" \
"v_writelane_b32 v11, s16, 25\n" \
"v_mfma_f32_16x16x32_bf16 v[112:115], a[188:191], a[140:143], v[112:115]\n" \
"ds_read_b128 a[200:203], v29 offset:4096\n" \
"s_add_i32 s16, s15, 1\n" \
"v_mfma_f32_16x16x32_bf16 v[112:115], a[252:255], a[204:207], v[112:115]\n" \
"s_movk_i32 s84, 0x2280\n" \
"v_mfma_f32_16x16x32_bf16 v[108:111], a[184:187], a[140:143], v[108:111]\n" \
"buffer_load_dwordx4 v25, s[76:79], s84 offen offset:1024 lds\n" \
"v_mfma_f32_16x16x32_bf16 v[108:111], a[248:251], a[204:207], v[108:111]\n" \
"s_add_u32 s89, s42, 0x0\n" \
"buffer_store_dwordx4 v[216:219], v20, s[80:83], s89 offen offset:1024 sc1\n" \
"v_mfma_f32_16x16x32_bf16 v[104:107], a[180:183], a[140:143], v[104:107]\n" \
"ds_read_b64_tr_b16 a[168:169], v34 offset:32768\n" \
"s_and_b32 s16, s16, 3\n" \
"v_mfma_f32_16x16x32_bf16 v[104:107], a[244:247], a[204:207], v[104:107]\n" \
"ds_read_b64_tr_b16 a[170:171], v34 offset:33280\n" \
"s_or_b32 s16, s16, s5\n" \
"v_mfma_f32_16x16x32_bf16 v[100:103], a[176:179], a[140:143], v[100:103]\n" \
"ds_read_b64_tr_b16 a[232:233], v34 offset:36864\n" \
"s_mul_i32 s18, s16, 0x24000\n" \
"v_mfma_f32_16x16x32_bf16 v[100:103], a[240:243], a[204:207], v[100:103]\n" \
"ds_read_b128 a[140:143], v28 offset:6144\n" \
"s_add_i32 s18, s18, s19\n" \
"v_mfma_f32_16x16x32_bf16 v[52:55], a[176:179], a[128:131], v[52:55]\n" \
"ds_read_b128 a[204:207], v29 offset:6144\n" \
"s_xor_b32 s13, s13, 2\n" \
"v_mfma_f32_16x16x32_bf16 v[52:55], a[240:243], a[192:195], v[52:55]\n" \
"s_movk_i32 s84, 0x4280\n" \
"v_mfma_f32_16x16x32_bf16 v[56:59], a[180:183], a[128:131], v[56:59]\n" \
"buffer_load_dwordx4 v24, s[76:79], s84 offen offset:2048 lds\n" \
"v_mfma_f32_16x16x32_bf16 v[56:59], a[244:247], a[192:195], v[56:59]\n" \
"s_movk_i32 s84, 0x6280\n" \
"v_mfma_f32_16x16x32_bf16 v[60:63], a[184:187], a[128:131], v[60:63]\n" \
"buffer_load_dwordx4 v25, s[76:79], s84 offen offset:3072 lds\n" \
"v_mfma_f32_16x16x32_bf16 v[60:63], a[248:251], a[192:195], v[60:63]\n" \
"ds_read_b64_tr_b16 a[234:235], v34 offset:37376\n" \
"s_add_i32 s15, s15, -1\n" \
"v_mfma_f32_16x16x32_bf16 v[64:67], a[188:191], a[128:131], v[64:67]\n" \
"v_writelane_b32 v11, s18, 27\n" \
"s_mul_i32 s18, s13, 0x24000\n" \
"v_mfma_f32_16x16x32_bf16 v[64:67], a[252:255], a[192:195], v[64:67]\n" \
"ds_read_b128 a[128:131], v28 offset:0\n" \
"s_and_b32 s15, s15, 3\n" \
"v_mfma_f32_16x16x32_bf16 v[80:83], a[188:191], a[132:135], v[80:83]\n" \
"ds_read_b128 a[192:195], v29 offset:0\n" \
"s_add_i32 s18, s18, s19\n" \
"v_mfma_f32_16x16x32_bf16 v[80:83], a[252:255], a[196:199], v[80:83]\n" \
"s_or_b32 s15, s15, s5\n" \
"v_writelane_b32 v11, s18, 28\n" \
"v_mfma_f32_16x16x32_bf16 v[76:79], a[184:187], a[132:135], v[76:79]\n" \
"s_mul_i32 s18, s15, 0x24000\n" \
"s_add_i32 s18, s18, s19\n" \
"v_mfma_f32_16x16x32_bf16 v[76:79], a[248:251], a[196:199], v[76:79]\n" \
"v_writelane_b32 v11, s18, 29\n" \
"s_or_b32 s18, s9, s14\n" \
"v_mfma_f32_16x16x32_bf16 v[72:75], a[180:183], a[132:135], v[72:75]\n" \
"s_mul_i32 s18, s18, 0x24800\n" \
"s_lshl_b32 s16, s16, 12\n" \
"v_mfma_f32_16x16x32_bf16 v[72:75], a[244:247], a[196:199], v[72:75]\n" \
"s_add_i32 s19, s18, 0x3004800\n" \
"s_add_i32 s20, s19, s16\n" \
"v_mfma_f32_16x16x32_bf16 v[68:71], a[176:179], a[132:135], v[68:71]\n" \
"v_writelane_b32 v11, s20, 30\n" \
"s_lshl_b32 s13, s13, 12\n" \
"v_mfma_f32_16x16x32_bf16 v[68:71], a[240:243], a[196:199], v[68:71]\n" \
"ds_read_b128 a[132:135], v28 offset:2048\n" \
"ds_read_b128 a[196:199], v29 offset:2048\n" \
"s_waitcnt lgkmcnt(15)\n" \
"v_mfma_f32_16x16x32_bf16 a[0:3], a[160:163], a[144:147], a[0:3]\n" \
"s_add_u32 m0, s30, 0x21000\n" \
"s_addk_i32 s84, 0x3000\n" \
"v_mfma_f32_16x16x32_bf16 a[0:3], a[224:227], a[208:211], a[0:3]\n" \
"buffer_load_dwordx4 v24, s[76:79], s84 offen offset:0 lds\n" \
"v_mfma_f32_16x16x32_bf16 a[4:7], a[164:167], a[144:147], a[4:7]\n" \
"ds_read_b64_tr_b16 a[176:177], v36 offset:32768\n" \
"s_lshl_b32 s15, s15, 12\n" \
"v_mfma_f32_16x16x32_bf16 a[4:7], a[228:231], a[208:211], a[4:7]\n" \
"ds_read_b64_tr_b16 a[178:179], v36 offset:33792\n" \
"s_sub_i32 s14, s9, s14\n" \
"v_mfma_f32_16x16x32_bf16 a[12:15], a[172:175], a[144:147], a[12:15]\n" \
"ds_read_b64_tr_b16 a[240:241], v36 offset:40960\n" \
"s_add_i32 s20, s19, s13\n" \
"s_waitcnt lgkmcnt(15)\n" \
"v_mfma_f32_16x16x32_bf16 a[12:15], a[236:239], a[208:211], a[12:15]\n" \
"s_add_u32 s88, s42, 0x0\n" \
"buffer_store_dwordx4 v[220:223], v20, s[80:83], s88 offen offset:0 sc1\n" \
"s_waitcnt lgkmcnt(11)\n" \
"v_mfma_f32_16x16x32_bf16 a[8:11], a[144:147], a[168:171], a[8:11]\n" \
"ds_read_b64_tr_b16 a[242:243], v36 offset:41984\n" \
"v_writelane_b32 v11, s20, 31\n" \
"s_waitcnt lgkmcnt(8)\n" \
"v_mfma_f32_16x16x32_bf16 a[8:11], a[208:211], a[232:235], a[8:11]\n" \
"ds_read_b64_tr_b16 a[180:181], v37 offset:32768\n" \
"s_add_i32 s19, s19, s15\n" \
"v_mfma_f32_16x16x32_bf16 a[24:27], a[148:151], a[168:171], a[24:27]\n" \
"ds_read_b64_tr_b16 a[182:183], v37 offset:33792\n" \
"s_mul_i32 s14, s14, 0x24800\n" \
"v_mfma_f32_16x16x32_bf16 a[24:27], a[212:215], a[232:235], a[24:27]\n" \
"ds_read_b64_tr_b16 a[244:245], v37 offset:40960\n" \
"v_writelane_b32 v11, s19, 32\n" \
"v_mfma_f32_16x16x32_bf16 a[28:31], a[172:175], a[148:151], a[28:31]\n" \
"s_add_i32 s19, s14, 0x4120800\n" \
"s_add_i32 s16, s19, s16\n" \
"v_mfma_f32_16x16x32_bf16 a[28:31], a[236:239], a[212:215], a[28:31]\n" \
"s_addk_i32 s84, 0x2000\n" \
"v_mfma_f32_16x16x32_bf16 a[20:23], a[164:167], a[148:151], a[20:23]\n" \
"buffer_load_dwordx4 v25, s[76:79], s84 offen offset:1024 lds\n" \
"v_mfma_f32_16x16x32_bf16 a[20:23], a[228:231], a[212:215], a[20:23]\n" \
"ds_read_b64_tr_b16 a[246:247], v37 offset:41984\n" \
"v_writelane_b32 v11, s16, 33\n" \
"v_mfma_f32_16x16x32_bf16 a[16:19], a[160:163], a[148:151], a[16:19]\n" \
"ds_read_b64_tr_b16 a[184:185], v38 offset:32768\n" \
"s_add_i32 s13, s19, s13\n" \
"v_mfma_f32_16x16x32_bf16 a[16:19], a[224:227], a[212:215], a[16:19]\n" \
"ds_read_b64_tr_b16 a[186:187], v38 offset:33792\n" \
"v_writelane_b32 v11, s13, 34\n" \
"v_mfma_f32_16x16x32_bf16 a[32:35], a[160:163], a[152:155], a[32:35]\n" \
"s_addk_i32 s84, 0x2000\n" \
"v_mfma_f32_16x16x32_bf16 a[32:35], a[224:227], a[216:219], a[32:35]\n" \
"buffer_load_dwordx4 v24, s[76:79], s84 offen offset:2048 lds\n" \
"v_mfma_f32_16x16x32_bf16 a[36:39], a[164:167], a[152:155], a[36:39]\n" \
"ds_read_b64_tr_b16 a[248:249], v38 offset:40960\n" \
"s_add_i32 s19, s19, s15\n" \
"v_mfma_f32_16x16x32_bf16 a[36:39], a[228:231], a[216:219], a[36:39]\n" \
"ds_read_b64_tr_b16 a[250:251], v38 offset:41984\n" \
"s_or_b32 s13, s18, 0x3000000\n" \
"v_mfma_f32_16x16x32_bf16 a[44:47], a[172:175], a[152:155], a[44:47]\n" \
"ds_read_b64_tr_b16 a[188:189], v39 offset:32768\n" \
"s_lshl_b32 s15, s6, 11\n" \
"v_mfma_f32_16x16x32_bf16 a[44:47], a[236:239], a[216:219], a[44:47]\n" \
"ds_read_b64_tr_b16 a[190:191], v39 offset:33792\n" \
"v_writelane_b32 v11, s19, 35\n" \
"v_mfma_f32_16x16x32_bf16 a[40:43], a[152:155], a[168:171], a[40:43]\n" \
"s_add_u32 s89, s42, 0x1000\n" \
"buffer_store_dwordx4 v[224:227], v20, s[80:83], s89 offen offset:3072 sc1\n" \
"v_mfma_f32_16x16x32_bf16 a[40:43], a[216:219], a[232:235], a[40:43]\n" \
"ds_read_b64_tr_b16 a[252:253], v39 offset:40960\n" \
"s_add_i32 s16, s13, s15\n" \
"v_mfma_f32_16x16x32_bf16 a[56:59], a[156:159], a[168:171], a[56:59]\n" \
"ds_read_b64_tr_b16 a[254:255], v39 offset:41984\n" \
"v_writelane_b32 v11, s16, 36\n" \
"v_mfma_f32_16x16x32_bf16 a[56:59], a[220:223], a[232:235], a[56:59]\n" \
"s_add_i32 s16, s14, 0x411c000\n" \
"s_add_i32 s19, s16, s15\n" \
"v_mfma_f32_16x16x32_bf16 a[60:63], a[172:175], a[156:159], a[60:63]\n" \
"v_writelane_b32 v11, s19, 37\n" \
"s_add_i32 s18, s18, 0x3004000\n" \
"v_mfma_f32_16x16x32_bf16 a[60:63], a[236:239], a[220:223], a[60:63]\n" \
"s_addk_i32 s84, 0x2000\n" \
"v_mfma_f32_16x16x32_bf16 a[52:55], a[164:167], a[156:159], a[52:55]\n" \
"buffer_load_dwordx4 v25, s[76:79], s84 offen offset:3072 lds\n" \
"v_mfma_f32_16x16x32_bf16 a[52:55], a[228:231], a[220:223], a[52:55]\n" \
"v_writelane_b32 v11, s18, 38\n" \
"s_add_i32 s14, s14, 0x4120000\n" \
"v_mfma_f32_16x16x32_bf16 a[48:51], a[160:163], a[156:159], a[48:51]\n" \
"v_writelane_b32 v11, s14, 39\n" \
"s_lshl_b32 s14, s4, 2\n" \
"v_mfma_f32_16x16x32_bf16 a[48:51], a[224:227], a[220:223], a[48:51]\n" \
"s_or_b32 s8, s14, s8\n" \
"s_mul_i32 s8, s8, 0x90000\n" \
"v_mfma_f32_16x16x32_bf16 a[64:67], a[164:167], a[140:143], a[64:67]\n" \
"s_lshl_b32 s14, s3, 14\n" \
"s_add_i32 s8, s8, s14\n" \
"v_mfma_f32_16x16x32_bf16 a[64:67], a[228:231], a[204:207], a[64:67]\n" \
"s_lshl_b32 s9, s9, 4\n" \
"s_lshl_b32 s14, s4, 7\n" \
"v_mfma_f32_16x16x32_bf16 a[80:83], a[172:175], a[140:143], a[80:83]\n" \
"s_or_b32 s18, s9, s14\n" \
"s_sub_i32 s9, s9, s14\n" \
"v_mfma_f32_16x16x32_bf16 a[80:83], a[236:239], a[204:207], a[80:83]\n" \
"s_mulk_i32 s18, 0x480\n" \
"s_mulk_i32 s9, 0x480\n" \
"v_mfma_f32_16x16x32_bf16 a[68:71], a[140:143], a[168:171], a[68:71]\n" \
"s_or_b32 s14, s18, s12\n" \
"s_or_b32 s9, s9, s12\n" \
"v_mfma_f32_16x16x32_bf16 a[68:71], a[204:207], a[232:235], a[68:71]\n" \
"s_waitcnt vmcnt(11) lgkmcnt(0)\n" \
"s_barrier\n" \
"v_mfma_f32_16x16x32_bf16 v[116:119], a[176:179], a[144:147], v[116:119]\n" \
"s_add_u32 m0, s40, 0x8000\n" \
"s_add_u32 s85, s39, 0xa000\n" \
"v_mfma_f32_16x16x32_bf16 v[116:119], a[240:243], a[208:211], v[116:119]\n" \
"buffer_load_dwordx4 v20, s[80:83], s85 offen sc1 lds\n" \
"v_mfma_f32_16x16x32_bf16 v[120:123], a[180:183], a[144:147], v[120:123]\n" \
"ds_read_b64_tr_b16 a[160:161], v32 offset:0\n" \
"v_accvgpr_read_b32 v212, a108\n" \
"v_mfma_f32_16x16x32_bf16 v[120:123], a[244:247], a[208:211], v[120:123]\n" \
"ds_read_b64_tr_b16 a[162:163], v32 offset:512\n" \
"v_accvgpr_read_b32 v213, a109\n" \
"v_mfma_f32_16x16x32_bf16 v[124:127], a[184:187], a[144:147], v[124:127]\n" \
"s_add_u32 m0, s40, 0x9000\n" \
"s_add_u32 s85, s39, 0xb000\n" \
"v_mfma_f32_16x16x32_bf16 v[124:127], a[248:251], a[208:211], v[124:127]\n" \
"buffer_load_dwordx4 v20, s[80:83], s85 offen sc1 lds\n" \
"v_mfma_f32_16x16x32_bf16 v[128:131], a[188:191], a[144:147], v[128:131]\n" \
"ds_read_b64_tr_b16 a[224:225], v32 offset:4096\n" \
"v_accvgpr_read_b32 v214, a110\n" \
"v_mfma_f32_16x16x32_bf16 v[128:131], a[252:255], a[208:211], v[128:131]\n" \
"ds_read_b128 a[144:147], v28 offset:40960\n" \
"v_accvgpr_read_b32 v215, a111\n" \
"v_mfma_f32_16x16x32_bf16 v[144:147], a[188:191], a[148:151], v[144:147]\n" \
"ds_read_b128 a[208:211], v29 offset:40960\n" \
"v_accvgpr_read_b32 v180, a124\n" \
"v_mfma_f32_16x16x32_bf16 v[144:147], a[252:255], a[212:215], v[144:147]\n" \
"s_add_u32 m0, s40, 0xb000\n" \
"s_movk_i32 s84, 0x280\n" \
"s_nop 0\n" \
"v_mfma_f32_16x16x32_bf16 v[140:143], a[184:187], a[148:151], v[140:143]\n" \
"buffer_load_dwordx4 v21, s[76:79], s84 offen offset:0 lds\n" \
"v_mfma_f32_16x16x32_bf16 v[140:143], a[248:251], a[212:215], v[140:143]\n" \
"ds_read_b64_tr_b16 a[226:227], v32 offset:4608\n" \
"v_accvgpr_read_b32 v181, a125\n" \
"v_mfma_f32_16x16x32_bf16 v[136:139], a[180:183], a[148:151], v[136:139]\n" \
"s_add_u32 s88, s42, 0x1000\n" \
"buffer_store_dwordx4 v[228:231], v20, s[80:83], s88 offen offset:2048 sc1\n" \
"v_mfma_f32_16x16x32_bf16 v[136:139], a[244:247], a[212:215], v[136:139]\n" \
"ds_read_b64_tr_b16 a[164:165], v33 offset:0\n" \
"v_accvgpr_read_b32 v182, a126\n" \
"v_mfma_f32_16x16x32_bf16 v[132:135], a[176:179], a[148:151], v[132:135]\n" \
"v_accvgpr_read_b32 v183, a127\n" \
"v_cvt_pk_bf16_f32 v212, v212, v213\n" \
"v_mfma_f32_16x16x32_bf16 v[132:135], a[240:243], a[212:215], v[132:135]\n" \
"ds_read_b128 a[148:151], v28 offset:43008\n" \
"v_cvt_pk_bf16_f32 v213, v214, v215\n" \
"v_mfma_f32_16x16x32_bf16 v[148:151], a[176:179], a[152:155], v[148:151]\n" \
"ds_read_b128 a[212:215], v29 offset:43008\n" \
"v_cvt_pk_bf16_f32 v214, v180, v181\n" \
"v_mfma_f32_16x16x32_bf16 v[148:151], a[240:243], a[216:219], v[148:151]\n" \
"s_add_u32 m0, s31, 0x8000\n" \
"s_mov_b32 s84, 0x280000\n" \
"v_mfma_f32_16x16x32_bf16 v[152:155], a[180:183], a[152:155], v[152:155]\n" \
"buffer_load_dwordx4 v26, s[68:71], s84 offen offset:0 lds\n" \
"v_mfma_f32_16x16x32_bf16 v[152:155], a[244:247], a[216:219], v[152:155]\n" \
"ds_read_b64_tr_b16 a[166:167], v33 offset:512\n" \
"v_cvt_pk_bf16_f32 v215, v182, v183\n" \
"v_mfma_f32_16x16x32_bf16 v[156:159], a[184:187], a[152:155], v[156:159]\n" \
"ds_read_b64_tr_b16 a[228:229], v33 offset:4096\n" \
"v_accvgpr_read_b32 v208, a104\n" \
"v_mfma_f32_16x16x32_bf16 v[156:159], a[248:251], a[216:219], v[156:159]\n" \
"s_addk_i32 s84, 0x7c00\n" \
"v_mfma_f32_16x16x32_bf16 v[160:163], a[188:191], a[152:155], v[160:163]\n" \
"buffer_load_dwordx4 v26, s[68:71], s84 offen offset:1024 lds\n" \
"v_mfma_f32_16x16x32_bf16 v[160:163], a[252:255], a[216:219], v[160:163]\n" \
"ds_read_b128 a[152:155], v28 offset:45056\n" \
"v_accvgpr_read_b32 v209, a105\n" \
"v_mfma_f32_16x16x32_bf16 v[176:179], a[188:191], a[156:159], v[176:179]\n" \
"ds_read_b128 a[216:219], v29 offset:45056\n" \
"v_accvgpr_read_b32 v210, a106\n" \
"v_mfma_f32_16x16x32_bf16 v[176:179], a[252:255], a[220:223], v[176:179]\n" \
"s_addk_i32 s84, 0x7c00\n" \
"v_mfma_f32_16x16x32_bf16 v[172:175], a[184:187], a[156:159], v[172:175]\n" \
"buffer_load_dwordx4 v27, s[68:71], s84 offen offset:2048 lds\n" \
"v_mfma_f32_16x16x32_bf16 v[172:175], a[248:251], a[220:223], v[172:175]\n" \
"ds_read_b64_tr_b16 a[230:231], v33 offset:4608\n" \
"v_accvgpr_read_b32 v211, a107\n" \
"v_mfma_f32_16x16x32_bf16 v[168:171], a[180:183], a[156:159], v[168:171]\n" \
"ds_read_b64_tr_b16 a[172:173], v35 offset:0\n" \
"v_accvgpr_read_b32 v180, a120\n" \
"v_mfma_f32_16x16x32_bf16 v[168:171], a[244:247], a[220:223], v[168:171]\n" \
"ds_read_b64_tr_b16 a[174:175], v35 offset:512\n" \
"v_accvgpr_read_b32 v181, a121\n" \
"v_mfma_f32_16x16x32_bf16 v[164:167], a[176:179], a[156:159], v[164:167]\n" \
"v_accvgpr_read_b32 v182, a122\n" \
"v_accvgpr_read_b32 v183, a123\n" \
"v_mfma_f32_16x16x32_bf16 v[164:167], a[240:243], a[220:223], v[164:167]\n" \
"ds_read_b128 a[156:159], v28 offset:47104\n" \
"v_cvt_pk_bf16_f32 v208, v208, v209\n" \
"v_mfma_f32_16x16x32_bf16 v[84:87], a[176:179], a[136:139], v[84:87]\n" \
"ds_read_b128 a[220:223], v29 offset:47104\n" \
"v_cvt_pk_bf16_f32 v209, v210, v211\n" \
"v_mfma_f32_16x16x32_bf16 v[84:87], a[240:243], a[200:203], v[84:87]\n" \
"s_addk_i32 s84, 0x7c00\n" \
"v_mfma_f32_16x16x32_bf16 v[88:91], a[180:183], a[136:139], v[88:91]\n" \
"buffer_load_dwordx4 v27, s[68:71], s84 offen offset:3072 lds\n" \
"v_mfma_f32_16x16x32_bf16 v[88:91], a[244:247], a[200:203], v[88:91]\n" \
"s_add_u32 m0, s30, 0x0\n" \
"s_movk_i32 s84, 0x300\n" \
"s_nop 0\n" \
"v_mfma_f32_16x16x32_bf16 v[92:95], a[184:187], a[136:139], v[92:95]\n" \
"buffer_load_dwordx4 v24, s[76:79], s84 offen offset:0 lds\n" \
"v_mfma_f32_16x16x32_bf16 v[92:95], a[248:251], a[200:203], v[92:95]\n" \
"ds_read_b64_tr_b16 a[236:237], v35 offset:4096\n" \
"v_cvt_pk_bf16_f32 v210, v180, v181\n" \
"v_mfma_f32_16x16x32_bf16 v[96:99], a[188:191], a[136:139], v[96:99]\n" \
"ds_read_b64_tr_b16 a[238:239], v35 offset:4608\n" \
"v_cvt_pk_bf16_f32 v211, v182, v183\n" \
"v_mfma_f32_16x16x32_bf16 v[96:99], a[252:255], a[200:203], v[96:99]\n" \
"ds_read_b128 a[136:139], v28 offset:36864\n" \
"v_accvgpr_read_b32 v204, a100\n" \
"v_mfma_f32_16x16x32_bf16 v[112:115], a[188:191], a[140:143], v[112:115]\n" \
"ds_read_b128 a[200:203], v29 offset:36864\n" \
"v_accvgpr_read_b32 v205, a101\n" \
"v_mfma_f32_16x16x32_bf16 v[112:115], a[252:255], a[204:207], v[112:115]\n" \
"s_movk_i32 s84, 0x2300\n" \
"s_nop 0\n" \
"v_mfma_f32_16x16x32_bf16 v[108:111], a[184:187], a[140:143], v[108:111]\n" \
"buffer_load_dwordx4 v25, s[76:79], s84 offen offset:1024 lds\n" \
"v_mfma_f32_16x16x32_bf16 v[108:111], a[248:251], a[204:207], v[108:111]\n" \
"s_add_u32 s89, s42, 0x1000\n" \
"buffer_store_dwordx4 v[232:235], v20, s[80:83], s89 offen offset:1024 sc1\n" \
"v_mfma_f32_16x16x32_bf16 v[104:107], a[180:183], a[140:143], v[104:107]\n" \
"ds_read_b64_tr_b16 a[168:169], v34 offset:0\n" \
"v_accvgpr_read_b32 v206, a102\n" \
"v_mfma_f32_16x16x32_bf16 v[104:107], a[244:247], a[204:207], v[104:107]\n" \
"ds_read_b64_tr_b16 a[170:171], v34 offset:512\n" \
"v_accvgpr_read_b32 v207, a103\n" \
"v_mfma_f32_16x16x32_bf16 v[100:103], a[176:179], a[140:143], v[100:103]\n" \
"ds_read_b64_tr_b16 a[232:233], v34 offset:4096\n" \
"v_accvgpr_read_b32 v180, a116\n" \
"v_mfma_f32_16x16x32_bf16 v[100:103], a[240:243], a[204:207], v[100:103]\n" \
"ds_read_b128 a[140:143], v28 offset:38912\n" \
"v_accvgpr_read_b32 v181, a117\n" \
"v_mfma_f32_16x16x32_bf16 v[52:55], a[176:179], a[128:131], v[52:55]\n" \
"ds_read_b128 a[204:207], v29 offset:38912\n" \
"v_accvgpr_read_b32 v182, a118\n" \
"v_mfma_f32_16x16x32_bf16 v[52:55], a[240:243], a[192:195], v[52:55]\n" \
"s_movk_i32 s84, 0x4300\n" \
"v_mfma_f32_16x16x32_bf16 v[56:59], a[180:183], a[128:131], v[56:59]\n" \
"buffer_load_dwordx4 v24, s[76:79], s84 offen offset:2048 lds\n" \
"v_mfma_f32_16x16x32_bf16 v[56:59], a[244:247], a[192:195], v[56:59]\n" \
"s_movk_i32 s84, 0x6300\n" \
"v_mfma_f32_16x16x32_bf16 v[60:63], a[184:187], a[128:131], v[60:63]\n" \
"buffer_load_dwordx4 v25, s[76:79], s84 offen offset:3072 lds\n" \
"v_mfma_f32_16x16x32_bf16 v[60:63], a[248:251], a[192:195], v[60:63]\n" \
"ds_read_b64_tr_b16 a[234:235], v34 offset:4608\n" \
"v_accvgpr_read_b32 v183, a119\n" \
"v_mfma_f32_16x16x32_bf16 v[64:67], a[188:191], a[128:131], v[64:67]\n" \
"v_cvt_pk_bf16_f32 v204, v204, v205\n" \
"v_cvt_pk_bf16_f32 v205, v206, v207\n" \
"v_mfma_f32_16x16x32_bf16 v[64:67], a[252:255], a[192:195], v[64:67]\n" \
"ds_read_b128 a[128:131], v28 offset:32768\n" \
"v_cvt_pk_bf16_f32 v206, v180, v181\n" \
"v_mfma_f32_16x16x32_bf16 v[80:83], a[188:191], a[132:135], v[80:83]\n" \
"ds_read_b128 a[192:195], v29 offset:32768\n" \
"v_cvt_pk_bf16_f32 v207, v182, v183\n" \
"v_mfma_f32_16x16x32_bf16 v[80:83], a[252:255], a[196:199], v[80:83]\n" \
"v_accvgpr_read_b32 v200, a96\n" \
"v_accvgpr_read_b32 v201, a97\n" \
"v_mfma_f32_16x16x32_bf16 v[76:79], a[184:187], a[132:135], v[76:79]\n" \
"v_accvgpr_read_b32 v202, a98\n" \
"v_accvgpr_read_b32 v203, a99\n" \
"v_mfma_f32_16x16x32_bf16 v[76:79], a[248:251], a[196:199], v[76:79]\n" \
"v_accvgpr_read_b32 v180, a112\n" \
"v_accvgpr_read_b32 v181, a113\n" \
"v_mfma_f32_16x16x32_bf16 v[72:75], a[180:183], a[132:135], v[72:75]\n" \
"v_accvgpr_read_b32 v182, a114\n" \
"v_accvgpr_read_b32 v183, a115\n" \
"v_mfma_f32_16x16x32_bf16 v[72:75], a[244:247], a[196:199], v[72:75]\n" \
"v_cvt_pk_bf16_f32 v200, v200, v201\n" \
"v_cvt_pk_bf16_f32 v201, v202, v203\n" \
"v_mfma_f32_16x16x32_bf16 v[68:71], a[176:179], a[132:135], v[68:71]\n" \
"v_cvt_pk_bf16_f32 v202, v180, v181\n" \
"v_cvt_pk_bf16_f32 v203, v182, v183\n" \
"v_mfma_f32_16x16x32_bf16 v[68:71], a[240:243], a[196:199], v[68:71]\n" \
"ds_read_b128 a[132:135], v28 offset:34816\n" \
"ds_read_b128 a[196:199], v29 offset:34816\n" \
"s_waitcnt lgkmcnt(15)\n" \
"v_mfma_f32_16x16x32_bf16 a[0:3], a[160:163], a[144:147], a[0:3]\n" \
"s_add_u32 m0, s30, 0x1000\n" \
"s_addk_i32 s84, 0x3000\n" \
"v_mfma_f32_16x16x32_bf16 a[0:3], a[224:227], a[208:211], a[0:3]\n" \
"buffer_load_dwordx4 v24, s[76:79], s84 offen offset:0 lds\n" \
"v_mfma_f32_16x16x32_bf16 a[4:7], a[164:167], a[144:147], a[4:7]\n" \
"ds_read_b64_tr_b16 a[176:177], v36 offset:0\n" \
"v_accvgpr_read_b32 v196, a76\n" \
"v_mfma_f32_16x16x32_bf16 a[4:7], a[228:231], a[208:211], a[4:7]\n" \
"ds_read_b64_tr_b16 a[178:179], v36 offset:1024\n" \
"v_accvgpr_read_b32 v197, a77\n" \
"v_mfma_f32_16x16x32_bf16 a[12:15], a[172:175], a[144:147], a[12:15]\n" \
"ds_read_b64_tr_b16 a[240:241], v36 offset:8192\n" \
"v_accvgpr_read_b32 v198, a78\n" \
"s_waitcnt lgkmcnt(15)\n" \
"v_mfma_f32_16x16x32_bf16 a[12:15], a[236:239], a[208:211], a[12:15]\n" \
"s_add_u32 s88, s42, 0x1000\n" \
"buffer_store_dwordx4 v[236:239], v20, s[80:83], s88 offen offset:0 sc1\n" \
"s_waitcnt lgkmcnt(11)\n" \
"v_mfma_f32_16x16x32_bf16 a[8:11], a[144:147], a[168:171], a[8:11]\n" \
"ds_read_b64_tr_b16 a[242:243], v36 offset:9216\n" \
"v_accvgpr_read_b32 v199, a79\n" \
"s_waitcnt lgkmcnt(8)\n" \
"s_nop 0\n" \
"v_mfma_f32_16x16x32_bf16 a[8:11], a[208:211], a[232:235], a[8:11]\n" \
"ds_read_b64_tr_b16 a[180:181], v37 offset:0\n" \
"v_accvgpr_read_b32 v180, a92\n" \
"v_mfma_f32_16x16x32_bf16 a[24:27], a[148:151], a[168:171], a[24:27]\n" \
"ds_read_b64_tr_b16 a[182:183], v37 offset:1024\n" \
"v_accvgpr_read_b32 v181, a93\n" \
"v_mfma_f32_16x16x32_bf16 a[24:27], a[212:215], a[232:235], a[24:27]\n" \
"ds_read_b64_tr_b16 a[244:245], v37 offset:8192\n" \
"v_accvgpr_read_b32 v182, a94\n" \
"v_mfma_f32_16x16x32_bf16 a[28:31], a[172:175], a[148:151], a[28:31]\n" \
"v_accvgpr_read_b32 v183, a95\n" \
"v_cvt_pk_bf16_f32 v196, v196, v197\n" \
"v_mfma_f32_16x16x32_bf16 a[28:31], a[236:239], a[212:215], a[28:31]\n" \
"s_addk_i32 s84, 0x2000\n" \
"s_nop 0\n" \
"v_mfma_f32_16x16x32_bf16 a[20:23], a[164:167], a[148:151], a[20:23]\n" \
"buffer_load_dwordx4 v25, s[76:79], s84 offen offset:1024 lds\n" \
"v_mfma_f32_16x16x32_bf16 a[20:23], a[228:231], a[212:215], a[20:23]\n" \
"ds_read_b64_tr_b16 a[246:247], v37 offset:9216\n" \
"v_cvt_pk_bf16_f32 v197, v198, v199\n" \
"v_mfma_f32_16x16x32_bf16 a[16:19], a[160:163], a[148:151], a[16:19]\n" \
"ds_read_b64_tr_b16 a[184:185], v38 offset:0\n" \
"v_cvt_pk_bf16_f32 v198, v180, v181\n" \
"v_mfma_f32_16x16x32_bf16 a[16:19], a[224:227], a[212:215], a[16:19]\n" \
"ds_read_b64_tr_b16 a[186:187], v38 offset:1024\n" \
"v_cvt_pk_bf16_f32 v199, v182, v183\n" \
"v_mfma_f32_16x16x32_bf16 a[32:35], a[160:163], a[152:155], a[32:35]\n" \
"s_addk_i32 s84, 0x2000\n" \
"s_nop 0\n" \
".fill (L_pb + 69344 - .) / 4, 4, 0xBF800000\n" \
"L_pp17:\n" \
"s_cbranch_execz L_pret\n" \
"s_nop 0\n" \
"v_mfma_f32_16x16x32_bf16 a[32:35], a[224:227], a[216:219], a[32:35]\n" \
"buffer_load_dwordx4 v24, s[76:79], s84 offen offset:2048 lds\n" \
"v_mfma_f32_16x16x32_bf16 a[36:39], a[164:167], a[152:155], a[36:39]\n" \
"ds_read_b64_tr_b16 a[248:249], v38 offset:8192\n" \
"v_accvgpr_read_b32 v192, a72\n" \
"v_mfma_f32_16x16x32_bf16 a[36:39], a[228:231], a[216:219], a[36:39]\n" \
"ds_read_b64_tr_b16 a[250:251], v38 offset:9216\n" \
"v_accvgpr_read_b32 v193, a73\n" \
"v_mfma_f32_16x16x32_bf16 a[44:47], a[172:175], a[152:155], a[44:47]\n" \
"ds_read_b64_tr_b16 a[188:189], v39 offset:0\n" \
"v_accvgpr_read_b32 v194, a74\n" \
"v_mfma_f32_16x16x32_bf16 a[44:47], a[236:239], a[216:219], a[44:47]\n" \
"ds_read_b64_tr_b16 a[190:191], v39 offset:1024\n" \
"v_accvgpr_read_b32 v195, a75\n" \
"v_mfma_f32_16x16x32_bf16 a[40:43], a[152:155], a[168:171], a[40:43]\n" \
"s_add_u32 s89, s42, 0x3000\n" \
"buffer_store_dwordx4 v[240:243], v20, s[80:83], s89 offen offset:3072 sc1\n" \
"v_mfma_f32_16x16x32_bf16 a[40:43], a[216:219], a[232:235], a[40:43]\n" \
"ds_read_b64_tr_b16 a[252:253], v39 offset:8192\n" \
"v_accvgpr_read_b32 v180, a88\n" \
"v_mfma_f32_16x16x32_bf16 a[56:59], a[156:159], a[168:171], a[56:59]\n" \
"ds_read_b64_tr_b16 a[254:255], v39 offset:9216\n" \
"v_accvgpr_read_b32 v181, a89\n" \
"v_mfma_f32_16x16x32_bf16 a[56:59], a[220:223], a[232:235], a[56:59]\n" \
"v_accvgpr_read_b32 v182, a90\n" \
"v_accvgpr_read_b32 v183, a91\n" \
"v_mfma_f32_16x16x32_bf16 a[60:63], a[172:175], a[156:159], a[60:63]\n" \
"v_cvt_pk_bf16_f32 v192, v192, v193\n" \
"v_cvt_pk_bf16_f32 v193, v194, v195\n" \
"v_mfma_f32_16x16x32_bf16 a[60:63], a[236:239], a[220:223], a[60:63]\n" \
"s_addk_i32 s84, 0x2000\n" \
"v_mfma_f32_16x16x32_bf16 a[52:55], a[164:167], a[156:159], a[52:55]\n" \
"buffer_load_dwordx4 v25, s[76:79], s84 offen offset:3072 lds\n" \
"v_mfma_f32_16x16x32_bf16 a[52:55], a[228:231], a[220:223], a[52:55]\n" \
"v_cvt_pk_bf16_f32 v194, v180, v181\n" \
"v_cvt_pk_bf16_f32 v195, v182, v183\n" \
"v_mfma_f32_16x16x32_bf16 a[48:51], a[160:163], a[156:159], a[48:51]\n" \
"s_and_b32 s12, s2, 1\n" \
"v_mfma_f32_16x16x32_bf16 a[48:51], a[224:227], a[220:223], a[48:51]\n" \
"s_cmp_lg_u32 s12, 0\n" \
"s_cselect_b32 s13, s16, s13\n" \
"v_mfma_f32_16x16x32_bf16 a[64:67], a[164:167], a[140:143], a[64:67]\n" \
"s_bfe_u32 s16, s2, 0x20001\n" \
"s_addk_i32 s13, 0x4000\n" \
"v_mfma_f32_16x16x32_bf16 a[64:67], a[228:231], a[204:207], a[64:67]\n" \
"s_xor_b32 s16, s16, s3\n" \
"v_mfma_f32_16x16x32_bf16 a[80:83], a[172:175], a[140:143], a[80:83]\n" \
"s_cmp_lg_u32 s16, 0\n" \
"s_cselect_b32 s13, s7, s13\n" \
"v_mfma_f32_16x16x32_bf16 a[80:83], a[236:239], a[204:207], a[80:83]\n" \
"s_lshl_b32 s6, s6, 16\n" \
"v_writelane_b32 v11, s13, 40\n" \
"v_mfma_f32_16x16x32_bf16 a[68:71], a[140:143], a[168:171], a[68:71]\n" \
"s_add_i32 s13, s8, 0x4240000\n" \
"s_add_i32 s6, s13, s6\n" \
"v_mfma_f32_16x16x32_bf16 a[68:71], a[204:207], a[232:235], a[68:71]\n" \
"s_waitcnt vmcnt(11) lgkmcnt(0)\n" \
"s_barrier\n" \
"v_mov_b32_e32 v45, s62\n" \
"s_mov_b64 exec, 1\n" \
"buffer_store_dword v45, off, s[80:83], s37 offset:0 sc1\n" \
"s_mov_b64 exec, -1\n" \
"v_xor_b32_e32 v28, 0x20000, v28\n" \
"v_xor_b32_e32 v29, 0x20000, v29\n" \
"v_mfma_f32_16x16x32_bf16 v[116:119], a[176:179], a[144:147], v[116:119]\n" \
"s_add_u32 m0, s40, 0x0\n" \
"s_add_u32 s85, s39, 0xc000\n" \
"v_mfma_f32_16x16x32_bf16 v[116:119], a[240:243], a[208:211], v[116:119]\n" \
"buffer_load_dwordx4 v20, s[80:83], s85 offen sc1 lds\n" \
"v_mfma_f32_16x16x32_bf16 v[120:123], a[180:183], a[144:147], v[120:123]\n" \
"ds_read_b64_tr_b16 a[160:161], v32 offset:32768\n" \
"v_writelane_b32 v11, s6, 41\n" \
"v_mfma_f32_16x16x32_bf16 v[120:123], a[244:247], a[208:211], v[120:123]\n" \
"ds_read_b64_tr_b16 a[162:163], v32 offset:33280\n" \
"s_add_i32 s8, s8, 0x42c0000\n" \
"v_mfma_f32_16x16x32_bf16 v[124:127], a[184:187], a[144:147], v[124:127]\n" \
"s_add_u32 m0, s40, 0x1000\n" \
"s_add_u32 s85, s39, 0xd000\n" \
"v_mfma_f32_16x16x32_bf16 v[124:127], a[248:251], a[208:211], v[124:127]\n" \
"buffer_load_dwordx4 v20, s[80:83], s85 offen sc1 lds\n" \
"v_mfma_f32_16x16x32_bf16 v[128:131], a[188:191], a[144:147], v[128:131]\n" \
"ds_read_b64_tr_b16 a[224:225], v32 offset:36864\n" \
"s_lshl_b32 s6, s12, 10\n" \
"v_mfma_f32_16x16x32_bf16 v[128:131], a[252:255], a[208:211], v[128:131]\n" \
"ds_read_b128 a[144:147], v28 offset:8192\n" \
"s_or_b32 s6, s8, s6\n" \
"v_mfma_f32_16x16x32_bf16 v[144:147], a[188:191], a[148:151], v[144:147]\n" \
"ds_read_b128 a[208:211], v29 offset:8192\n" \
"v_mfma_f32_16x16x32_bf16 v[144:147], a[252:255], a[212:215], v[144:147]\n" \
"s_add_u32 m0, s40, 0x3000\n" \
"s_movk_i32 s84, 0x300\n" \
"v_mfma_f32_16x16x32_bf16 v[140:143], a[184:187], a[148:151], v[140:143]\n" \
"buffer_load_dwordx4 v21, s[76:79], s84 offen offset:0 lds\n" \
"v_mfma_f32_16x16x32_bf16 v[140:143], a[248:251], a[212:215], v[140:143]\n" \
"ds_read_b64_tr_b16 a[226:227], v32 offset:37376\n" \
"v_mfma_f32_16x16x32_bf16 v[136:139], a[180:183], a[148:151], v[136:139]\n" \
"s_cmp_lg_u32 s16, 0\n" \
"s_cselect_b32 s6, s7, s6\n" \
"v_mfma_f32_16x16x32_bf16 v[136:139], a[244:247], a[212:215], v[136:139]\n" \
"ds_read_b64_tr_b16 a[164:165], v33 offset:32768\n" \
"s_mulk_i32 s4, 0x4800\n" \
"v_mfma_f32_16x16x32_bf16 v[132:135], a[176:179], a[148:151], v[132:135]\n" \
"v_writelane_b32 v11, s6, 42\n" \
"s_add_i32 s6, s14, s10\n" \
"v_mfma_f32_16x16x32_bf16 v[132:135], a[240:243], a[212:215], v[132:135]\n" \
"ds_read_b128 a[148:151], v28 offset:10240\n" \
"v_writelane_b32 v11, s6, 43\n" \
"v_mfma_f32_16x16x32_bf16 v[148:151], a[176:179], a[152:155], v[148:151]\n" \
"ds_read_b128 a[212:215], v29 offset:10240\n" \
"s_add_i32 s6, s9, 0x21c000\n" \
"v_mfma_f32_16x16x32_bf16 v[148:151], a[240:243], a[216:219], v[148:151]\n" \
"s_add_u32 m0, s31, 0x0\n" \
"s_mov_b32 s84, 0x300000\n" \
"v_mfma_f32_16x16x32_bf16 v[152:155], a[180:183], a[152:155], v[152:155]\n" \
"buffer_load_dwordx4 v26, s[68:71], s84 offen offset:0 lds\n" \
"v_mfma_f32_16x16x32_bf16 v[152:155], a[244:247], a[216:219], v[152:155]\n" \
"s_addk_i32 s84, 0x7c00\n" \
"v_mfma_f32_16x16x32_bf16 v[156:159], a[184:187], a[152:155], v[156:159]\n" \
"buffer_load_dwordx4 v26, s[68:71], s84 offen offset:1024 lds\n" \
"v_mfma_f32_16x16x32_bf16 v[156:159], a[248:251], a[216:219], v[156:159]\n" \
"ds_read_b64_tr_b16 a[166:167], v33 offset:33280\n" \
"s_add_i32 s9, s6, s10\n" \
"v_mfma_f32_16x16x32_bf16 v[160:163], a[188:191], a[152:155], v[160:163]\n" \
"ds_read_b64_tr_b16 a[228:229], v33 offset:36864\n" \
"v_mfma_f32_16x16x32_bf16 v[160:163], a[252:255], a[216:219], v[160:163]\n" \
"ds_read_b128 a[152:155], v28 offset:12288\n" \
"v_mfma_f32_16x16x32_bf16 v[176:179], a[188:191], a[156:159], v[176:179]\n" \
"ds_read_b128 a[216:219], v29 offset:12288\n" \
"v_mfma_f32_16x16x32_bf16 v[176:179], a[252:255], a[220:223], v[176:179]\n" \
"s_addk_i32 s84, 0x7c00\n" \
"v_mfma_f32_16x16x32_bf16 v[172:175], a[184:187], a[156:159], v[172:175]\n" \
"buffer_load_dwordx4 v27, s[68:71], s84 offen offset:2048 lds\n" \
"v_mfma_f32_16x16x32_bf16 v[172:175], a[248:251], a[220:223], v[172:175]\n" \
"ds_read_b64_tr_b16 a[230:231], v33 offset:37376\n" \
"v_mfma_f32_16x16x32_bf16 v[168:171], a[180:183], a[156:159], v[168:171]\n" \
"s_addk_i32 s84, 0x7c00\n" \
"v_mfma_f32_16x16x32_bf16 v[168:171], a[244:247], a[220:223], v[168:171]\n" \
"buffer_load_dwordx4 v27, s[68:71], s84 offen offset:3072 lds\n" \
"v_mfma_f32_16x16x32_bf16 v[164:167], a[176:179], a[156:159], v[164:167]\n" \
"ds_read_b64_tr_b16 a[172:173], v35 offset:32768\n" \
"v_mfma_f32_16x16x32_bf16 v[164:167], a[240:243], a[220:223], v[164:167]\n" \
"ds_read_b128 a[156:159], v28 offset:14336\n" \
"v_mfma_f32_16x16x32_bf16 v[84:87], a[176:179], a[136:139], v[84:87]\n" \
"ds_read_b128 a[220:223], v29 offset:14336\n" \
"v_mfma_f32_16x16x32_bf16 v[84:87], a[240:243], a[200:203], v[84:87]\n" \
"s_add_u32 s88, s42, 0x3000\n" \
"buffer_store_dwordx4 v[244:247], v20, s[80:83], s88 offen offset:2048 sc1\n" \
"v_mfma_f32_16x16x32_bf16 v[88:91], a[180:183], a[136:139], v[88:91]\n" \
"ds_read_b64_tr_b16 a[174:175], v35 offset:33280\n" \
"v_mfma_f32_16x16x32_bf16 v[88:91], a[244:247], a[200:203], v[88:91]\n" \
"ds_read_b64_tr_b16 a[236:237], v35 offset:36864\n" \
"v_mfma_f32_16x16x32_bf16 v[92:95], a[184:187], a[136:139], v[92:95]\n" \
"ds_read_b64_tr_b16 a[238:239], v35 offset:37376\n" \
"v_mfma_f32_16x16x32_bf16 v[92:95], a[248:251], a[200:203], v[92:95]\n" \
"s_add_u32 m0, s30, 0x8000\n" \
"s_movk_i32 s84, 0x380\n" \
"v_mfma_f32_16x16x32_bf16 v[96:99], a[188:191], a[136:139], v[96:99]\n" \
"buffer_load_dwordx4 v24, s[76:79], s84 offen offset:0 lds\n" \
"v_mfma_f32_16x16x32_bf16 v[96:99], a[252:255], a[200:203], v[96:99]\n" \
"ds_read_b128 a[136:139], v28 offset:4096\n" \
"v_mfma_f32_16x16x32_bf16 v[112:115], a[188:191], a[140:143], v[112:115]\n" \
"ds_read_b128 a[200:203], v29 offset:4096\n" \
"v_mfma_f32_16x16x32_bf16 v[112:115], a[252:255], a[204:207], v[112:115]\n" \
"ds_read_b64_tr_b16 a[168:169], v34 offset:32768\n" \
"v_mfma_f32_16x16x32_bf16 v[108:111], a[184:187], a[140:143], v[108:111]\n" \
"s_movk_i32 s84, 0x2380\n" \
"v_mfma_f32_16x16x32_bf16 v[108:111], a[248:251], a[204:207], v[108:111]\n" \
"buffer_load_dwordx4 v25, s[76:79], s84 offen offset:1024 lds\n" \
"v_mfma_f32_16x16x32_bf16 v[104:107], a[180:183], a[140:143], v[104:107]\n" \
"ds_read_b64_tr_b16 a[170:171], v34 offset:33280\n" \
"v_mfma_f32_16x16x32_bf16 v[104:107], a[244:247], a[204:207], v[104:107]\n" \
"ds_read_b64_tr_b16 a[232:233], v34 offset:36864\n" \
"v_mfma_f32_16x16x32_bf16 v[100:103], a[176:179], a[140:143], v[100:103]\n" \
"s_cmp_lg_u32 s12, 0\n" \
"s_cselect_b32 s6, s6, s14\n" \
"v_mfma_f32_16x16x32_bf16 v[100:103], a[240:243], a[204:207], v[100:103]\n" \
"ds_read_b128 a[140:143], v28 offset:6144\n" \
"s_addk_i32 s6, 0x100\n" \
"v_mfma_f32_16x16x32_bf16 v[52:55], a[176:179], a[128:131], v[52:55]\n" \
"ds_read_b128 a[204:207], v29 offset:6144\n" \
"v_writelane_b32 v11, s9, 44\n" \
"v_mfma_f32_16x16x32_bf16 v[52:55], a[240:243], a[192:195], v[52:55]\n" \
"s_movk_i32 s84, 0x4380\n" \
"v_mfma_f32_16x16x32_bf16 v[56:59], a[180:183], a[128:131], v[56:59]\n" \
"buffer_load_dwordx4 v24, s[76:79], s84 offen offset:2048 lds\n" \
"v_mfma_f32_16x16x32_bf16 v[56:59], a[244:247], a[192:195], v[56:59]\n" \
"s_movk_i32 s84, 0x6380\n" \
"v_mfma_f32_16x16x32_bf16 v[60:63], a[184:187], a[128:131], v[60:63]\n" \
"buffer_load_dwordx4 v25, s[76:79], s84 offen offset:3072 lds\n" \
"v_mfma_f32_16x16x32_bf16 v[60:63], a[248:251], a[192:195], v[60:63]\n" \
"ds_read_b64_tr_b16 a[234:235], v34 offset:37376\n" \
"v_mfma_f32_16x16x32_bf16 v[64:67], a[188:191], a[128:131], v[64:67]\n" \
"s_cmp_lg_u32 s16, 0\n" \
"s_cselect_b32 s6, s7, s6\n" \
"v_mfma_f32_16x16x32_bf16 v[64:67], a[252:255], a[192:195], v[64:67]\n" \
"ds_read_b128 a[128:131], v28 offset:0\n" \
"v_writelane_b32 v11, s6, 45\n" \
"v_mfma_f32_16x16x32_bf16 v[80:83], a[188:191], a[132:135], v[80:83]\n" \
"ds_read_b128 a[192:195], v29 offset:0\n" \
"s_or_b32 s6, s8, s15\n" \
"v_mfma_f32_16x16x32_bf16 v[80:83], a[252:255], a[196:199], v[80:83]\n" \
"v_writelane_b32 v11, s6, 53\n" \
"s_lshl_b32 s6, s3, 17\n" \
"v_mfma_f32_16x16x32_bf16 v[76:79], a[184:187], a[132:135], v[76:79]\n" \
"s_add_i32 s6, s13, s6\n" \
"s_or_b32 s5, s6, s5\n" \
"v_mfma_f32_16x16x32_bf16 v[76:79], a[248:251], a[196:199], v[76:79]\n" \
"s_or_b32 s5, s5, 0x3840\n" \
"v_writelane_b32 v11, s5, 6\n" \
"v_mfma_f32_16x16x32_bf16 v[72:75], a[180:183], a[132:135], v[72:75]\n" \
"s_mov_b32 s5, 0xff0000\n" \
"v_mfma_f32_16x16x32_bf16 v[72:75], a[244:247], a[196:199], v[72:75]\n" \
"s_cmp_lg_u32 s16, 0\n" \
"s_cselect_b32 s5, s17, s5\n" \
"v_mfma_f32_16x16x32_bf16 v[68:71], a[176:179], a[132:135], v[68:71]\n" \
"v_writelane_b32 v11, s5, 7\n" \
"s_mul_i32 s5, s3, 0x1200\n" \
"v_mfma_f32_16x16x32_bf16 v[68:71], a[240:243], a[196:199], v[68:71]\n" \
"ds_read_b128 a[132:135], v28 offset:2048\n" \
"ds_read_b128 a[196:199], v29 offset:2048\n" \
"s_waitcnt lgkmcnt(15)\n" \
"v_mfma_f32_16x16x32_bf16 a[0:3], a[160:163], a[144:147], a[0:3]\n" \
"s_add_u32 m0, s30, 0x9000\n" \
"s_addk_i32 s84, 0x3000\n" \
"v_mfma_f32_16x16x32_bf16 a[0:3], a[224:227], a[208:211], a[0:3]\n" \
"buffer_load_dwordx4 v24, s[76:79], s84 offen offset:0 lds\n" \
"v_mfma_f32_16x16x32_bf16 a[4:7], a[164:167], a[144:147], a[4:7]\n" \
"ds_read_b64_tr_b16 a[176:177], v36 offset:32768\n" \
"s_add_i32 s4, s5, s4\n" \
"v_mfma_f32_16x16x32_bf16 a[4:7], a[228:231], a[208:211], a[4:7]\n" \
"ds_read_b64_tr_b16 a[178:179], v36 offset:33792\n" \
"s_add_i32 s4, s4, s11\n" \
"s_waitcnt lgkmcnt(15)\n" \
"v_mfma_f32_16x16x32_bf16 a[12:15], a[172:175], a[144:147], a[12:15]\n" \
"ds_read_b64_tr_b16 a[240:241], v36 offset:40960\n" \
"v_writelane_b32 v11, s4, 8\n" \
"s_waitcnt lgkmcnt(15)\n" \
"v_mfma_f32_16x16x32_bf16 a[12:15], a[236:239], a[208:211], a[12:15]\n" \
"s_add_u32 s89, s42, 0x3000\n" \
"buffer_store_dwordx4 v[184:187], v20, s[80:83], s89 offen offset:1024 sc1\n" \
"s_waitcnt lgkmcnt(11)\n" \
"v_mfma_f32_16x16x32_bf16 a[8:11], a[144:147], a[168:171], a[8:11]\n" \
"ds_read_b64_tr_b16 a[242:243], v36 offset:41984\n" \
"s_lshl_b32 s4, s2, 16\n" \
"s_waitcnt lgkmcnt(8)\n" \
"v_mfma_f32_16x16x32_bf16 a[8:11], a[208:211], a[232:235], a[8:11]\n" \
"ds_read_b64_tr_b16 a[180:181], v37 offset:32768\n" \
"s_add_i32 s5, s4, 0x10000\n" \
"v_mfma_f32_16x16x32_bf16 a[24:27], a[148:151], a[168:171], a[24:27]\n" \
"ds_read_b64_tr_b16 a[182:183], v37 offset:33792\n" \
"s_and_b32 s5, s5, 0x70000\n" \
"v_mfma_f32_16x16x32_bf16 a[24:27], a[212:215], a[232:235], a[24:27]\n" \
"ds_read_b64_tr_b16 a[244:245], v37 offset:40960\n" \
"s_add_i32 s5, s13, s5\n" \
"v_mfma_f32_16x16x32_bf16 a[28:31], a[172:175], a[148:151], a[28:31]\n" \
"v_writelane_b32 v11, s5, 46\n" \
"s_add_i32 s5, s4, 0x20000\n" \
"v_mfma_f32_16x16x32_bf16 a[28:31], a[236:239], a[212:215], a[28:31]\n" \
"s_addk_i32 s84, 0x2000\n" \
"v_mfma_f32_16x16x32_bf16 a[20:23], a[164:167], a[148:151], a[20:23]\n" \
"buffer_load_dwordx4 v25, s[76:79], s84 offen offset:1024 lds\n" \
"v_mfma_f32_16x16x32_bf16 a[20:23], a[228:231], a[212:215], a[20:23]\n" \
"ds_read_b64_tr_b16 a[246:247], v37 offset:41984\n" \
"s_and_b32 s5, s5, 0x70000\n" \
"v_mfma_f32_16x16x32_bf16 a[16:19], a[160:163], a[148:151], a[16:19]\n" \
"ds_read_b64_tr_b16 a[184:185], v38 offset:32768\n" \
"s_add_i32 s5, s13, s5\n" \
"v_mfma_f32_16x16x32_bf16 a[16:19], a[224:227], a[212:215], a[16:19]\n" \
"ds_read_b64_tr_b16 a[186:187], v38 offset:33792\n" \
"s_bitset1_b32 s5, 11\n" \
"v_mfma_f32_16x16x32_bf16 a[32:35], a[160:163], a[152:155], a[32:35]\n" \
"s_addk_i32 s84, 0x2000\n" \
"s_nop 0\n" \
"v_mfma_f32_16x16x32_bf16 a[32:35], a[224:227], a[216:219], a[32:35]\n" \
"buffer_load_dwordx4 v24, s[76:79], s84 offen offset:2048 lds\n" \
"v_mfma_f32_16x16x32_bf16 a[36:39], a[164:167], a[152:155], a[36:39]\n" \
"ds_read_b64_tr_b16 a[248:249], v38 offset:40960\n" \
"v_writelane_b32 v11, s5, 47\n" \
"v_mfma_f32_16x16x32_bf16 a[36:39], a[228:231], a[216:219], a[36:39]\n" \
"ds_read_b64_tr_b16 a[250:251], v38 offset:41984\n" \
"s_add_i32 s5, s4, 0x30000\n" \
"v_mfma_f32_16x16x32_bf16 a[44:47], a[172:175], a[152:155], a[44:47]\n" \
"ds_read_b64_tr_b16 a[188:189], v39 offset:32768\n" \
"s_and_b32 s5, s5, 0x70000\n" \
"v_mfma_f32_16x16x32_bf16 a[44:47], a[236:239], a[216:219], a[44:47]\n" \
"ds_read_b64_tr_b16 a[190:191], v39 offset:33792\n" \
"s_add_i32 s5, s13, s5\n" \
"v_mfma_f32_16x16x32_bf16 a[40:43], a[152:155], a[168:171], a[40:43]\n" \
"s_add_u32 s88, s42, 0x3000\n" \
"buffer_store_dwordx4 v[188:191], v20, s[80:83], s88 offen offset:0 sc1\n" \
"v_mfma_f32_16x16x32_bf16 a[40:43], a[216:219], a[232:235], a[40:43]\n" \
"ds_read_b64_tr_b16 a[252:253], v39 offset:40960\n" \
"s_bitset1_b32 s5, 12\n" \
"v_mfma_f32_16x16x32_bf16 a[56:59], a[156:159], a[168:171], a[56:59]\n" \
"ds_read_b64_tr_b16 a[254:255], v39 offset:41984\n" \
"v_writelane_b32 v11, s5, 48\n" \
"v_mfma_f32_16x16x32_bf16 a[56:59], a[220:223], a[232:235], a[56:59]\n" \
"s_and_b32 s5, s4, 0x70000\n" \
"s_xor_b32 s5, s5, 0x40000\n" \
"v_mfma_f32_16x16x32_bf16 a[60:63], a[172:175], a[156:159], a[60:63]\n" \
"s_add_i32 s5, s13, s5\n" \
"s_or_b32 s5, s5, 0x1800\n" \
"v_mfma_f32_16x16x32_bf16 a[60:63], a[236:239], a[220:223], a[60:63]\n" \
"s_addk_i32 s84, 0x2000\n" \
"v_mfma_f32_16x16x32_bf16 a[52:55], a[164:167], a[156:159], a[52:55]\n" \
"buffer_load_dwordx4 v25, s[76:79], s84 offen offset:3072 lds\n" \
"v_mfma_f32_16x16x32_bf16 a[52:55], a[228:231], a[220:223], a[52:55]\n" \
"v_writelane_b32 v11, s5, 49\n" \
"s_add_i32 s5, s4, 0x50000\n" \
"v_mfma_f32_16x16x32_bf16 a[48:51], a[160:163], a[156:159], a[48:51]\n" \
"s_and_b32 s5, s5, 0x70000\n" \
"s_add_i32 s5, s13, s5\n" \
"v_mfma_f32_16x16x32_bf16 a[48:51], a[224:227], a[220:223], a[48:51]\n" \
"s_bitset1_b32 s5, 13\n" \
"v_writelane_b32 v11, s5, 50\n" \
"v_mfma_f32_16x16x32_bf16 a[64:67], a[164:167], a[140:143], a[64:67]\n" \
"s_add_i32 s5, s4, 0x60000\n" \
"s_and_b32 s5, s5, 0x70000\n" \
"v_mfma_f32_16x16x32_bf16 a[64:67], a[228:231], a[204:207], a[64:67]\n" \
"s_add_i32 s4, s4, 0x70000\n" \
"s_add_i32 s5, s13, s5\n" \
"v_mfma_f32_16x16x32_bf16 a[80:83], a[172:175], a[140:143], a[80:83]\n" \
"s_and_b32 s4, s4, 0x70000\n" \
"s_or_b32 s5, s5, 0x2800\n" \
"v_mfma_f32_16x16x32_bf16 a[80:83], a[236:239], a[204:207], a[80:83]\n" \
"v_writelane_b32 v11, s5, 51\n" \
"s_add_i32 s13, s13, s4\n" \
"v_mfma_f32_16x16x32_bf16 a[68:71], a[140:143], a[168:171], a[68:71]\n" \
"s_or_b32 s4, s13, 0x3000\n" \
"v_writelane_b32 v11, s4, 52\n" \
"v_mfma_f32_16x16x32_bf16 a[68:71], a[204:207], a[232:235], a[68:71]\n" \
"s_waitcnt vmcnt(11) lgkmcnt(0)\n" \
"s_barrier\n" \
"v_xor_b32_e32 v28, 0x20000, v28\n" \
"v_xor_b32_e32 v29, 0x20000, v29\n" \
"v_mfma_f32_16x16x32_bf16 v[116:119], a[176:179], a[144:147], v[116:119]\n" \
"s_add_u32 m0, s40, 0x8000\n" \
"s_add_u32 s85, s39, 0xe000\n" \
"v_mfma_f32_16x16x32_bf16 v[116:119], a[240:243], a[208:211], v[116:119]\n" \
"buffer_load_dwordx4 v20, s[80:83], s85 offen sc1 lds\n" \
"v_mfma_f32_16x16x32_bf16 v[120:123], a[180:183], a[144:147], v[120:123]\n" \
"ds_read_b64_tr_b16 a[160:161], v32 offset:0\n" \
"s_movk_i32 s11, 0x4000\n" \
"v_mfma_f32_16x16x32_bf16 v[120:123], a[244:247], a[208:211], v[120:123]\n" \
"ds_read_b64_tr_b16 a[162:163], v32 offset:512\n" \
"s_lshl_b32 s4, s2, 2\n" \
"v_mfma_f32_16x16x32_bf16 v[124:127], a[184:187], a[144:147], v[124:127]\n" \
"s_add_u32 m0, s40, 0x9000\n" \
"s_add_u32 s85, s39, 0xf000\n" \
"v_mfma_f32_16x16x32_bf16 v[124:127], a[248:251], a[208:211], v[124:127]\n" \
"buffer_load_dwordx4 v20, s[80:83], s85 offen sc1 lds\n" \
"v_mfma_f32_16x16x32_bf16 v[128:131], a[188:191], a[144:147], v[128:131]\n" \
"ds_read_b64_tr_b16 a[224:225], v32 offset:4096\n" \
"s_and_b32 s4, s4, 28\n" \
"v_mfma_f32_16x16x32_bf16 v[128:131], a[252:255], a[208:211], v[128:131]\n" \
"ds_read_b128 a[144:147], v28 offset:8192\n" \
"s_bfe_u32 s5, s2, 0x20003\n" \
"v_mfma_f32_16x16x32_bf16 v[144:147], a[188:191], a[148:151], v[144:147]\n" \
"ds_read_b128 a[208:211], v29 offset:8192\n" \
"s_or_b32 s4, s4, s5\n" \
"v_mfma_f32_16x16x32_bf16 v[144:147], a[252:255], a[212:215], v[144:147]\n" \
"s_add_u32 m0, s40, 0xb000\n" \
"s_movk_i32 s84, 0x380\n" \
"v_mfma_f32_16x16x32_bf16 v[140:143], a[184:187], a[148:151], v[140:143]\n" \
"buffer_load_dwordx4 v21, s[76:79], s84 offen offset:0 lds\n" \
"v_mfma_f32_16x16x32_bf16 v[140:143], a[248:251], a[212:215], v[140:143]\n" \
"ds_read_b64_tr_b16 a[226:227], v32 offset:4608\n" \
"s_lshr_b32 s5, s2, 5\n" \
"v_mfma_f32_16x16x32_bf16 v[136:139], a[180:183], a[148:151], v[136:139]\n" \
"s_lshr_b32 s19, s3, 1\n" \
"s_lshl_b32 s18, s5, 1\n" \
"v_mfma_f32_16x16x32_bf16 v[136:139], a[244:247], a[212:215], v[136:139]\n" \
"ds_read_b64_tr_b16 a[164:165], v33 offset:0\n" \
"s_lshl_b32 s6, s4, 20\n" \
"v_mfma_f32_16x16x32_bf16 v[132:135], a[176:179], a[148:151], v[132:135]\n" \
"s_lshl_b32 s7, s4, 8\n" \
"s_and_b32 s13, s3, 1\n" \
"v_mfma_f32_16x16x32_bf16 v[132:135], a[240:243], a[212:215], v[132:135]\n" \
"ds_read_b128 a[148:151], v28 offset:10240\n" \
"s_add_i32 s14, s18, s19\n" \
"v_mfma_f32_16x16x32_bf16 v[148:151], a[176:179], a[152:155], v[148:151]\n" \
"ds_read_b128 a[212:215], v29 offset:10240\n" \
"v_mfma_f32_16x16x32_bf16 v[148:151], a[240:243], a[216:219], v[148:151]\n" \
"s_add_u32 m0, s31, 0x8000\n" \
"s_mov_b32 s84, 0x380000\n" \
"v_mfma_f32_16x16x32_bf16 v[152:155], a[180:183], a[152:155], v[152:155]\n" \
"buffer_load_dwordx4 v26, s[68:71], s84 offen offset:0 lds\n" \
"v_mfma_f32_16x16x32_bf16 v[152:155], a[244:247], a[216:219], v[152:155]\n" \
"s_addk_i32 s84, 0x7c00\n" \
"s_nop 0\n" \
"v_mfma_f32_16x16x32_bf16 v[156:159], a[184:187], a[152:155], v[156:159]\n" \
"buffer_load_dwordx4 v26, s[68:71], s84 offen offset:1024 lds\n" \
"v_mfma_f32_16x16x32_bf16 v[156:159], a[248:251], a[216:219], v[156:159]\n" \
"ds_read_b64_tr_b16 a[166:167], v33 offset:512\n" \
"v_mfma_f32_16x16x32_bf16 v[160:163], a[188:191], a[152:155], v[160:163]\n" \
"ds_read_b64_tr_b16 a[228:229], v33 offset:4096\n" \
"v_mfma_f32_16x16x32_bf16 v[160:163], a[252:255], a[216:219], v[160:163]\n" \
"ds_read_b128 a[152:155], v28 offset:12288\n" \
"v_mfma_f32_16x16x32_bf16 v[176:179], a[188:191], a[156:159], v[176:179]\n" \
"ds_read_b128 a[216:219], v29 offset:12288\n" \
"v_mfma_f32_16x16x32_bf16 v[176:179], a[252:255], a[220:223], v[176:179]\n" \
"s_addk_i32 s84, 0x7c00\n" \
"v_mfma_f32_16x16x32_bf16 v[172:175], a[184:187], a[156:159], v[172:175]\n" \
"buffer_load_dwordx4 v27, s[68:71], s84 offen offset:2048 lds\n" \
"v_mfma_f32_16x16x32_bf16 v[172:175], a[248:251], a[220:223], v[172:175]\n" \
"ds_read_b64_tr_b16 a[230:231], v33 offset:4608\n" \
"v_mfma_f32_16x16x32_bf16 v[168:171], a[180:183], a[156:159], v[168:171]\n" \
"s_addk_i32 s84, 0x7c00\n" \
"v_mfma_f32_16x16x32_bf16 v[168:171], a[244:247], a[220:223], v[168:171]\n" \
"buffer_load_dwordx4 v27, s[68:71], s84 offen offset:3072 lds\n" \
"v_mfma_f32_16x16x32_bf16 v[164:167], a[176:179], a[156:159], v[164:167]\n" \
"ds_read_b64_tr_b16 a[172:173], v35 offset:0\n" \
"v_mfma_f32_16x16x32_bf16 v[164:167], a[240:243], a[220:223], v[164:167]\n" \
"ds_read_b128 a[156:159], v28 offset:14336\n" \
"v_mfma_f32_16x16x32_bf16 v[84:87], a[176:179], a[136:139], v[84:87]\n" \
"ds_read_b128 a[220:223], v29 offset:14336\n" \
"v_mfma_f32_16x16x32_bf16 v[84:87], a[240:243], a[200:203], v[84:87]\n" \
"s_add_u32 s89, s42, 0x0\n" \
"buffer_store_dwordx4 v[192:195], v20, s[80:83], s89 offen offset:2048 sc1\n" \
"v_mfma_f32_16x16x32_bf16 v[88:91], a[180:183], a[136:139], v[88:91]\n" \
"ds_read_b64_tr_b16 a[174:175], v35 offset:512\n" \
"v_mfma_f32_16x16x32_bf16 v[88:91], a[244:247], a[200:203], v[88:91]\n" \
"ds_read_b64_tr_b16 a[236:237], v35 offset:4096\n" \
"v_mfma_f32_16x16x32_bf16 v[92:95], a[184:187], a[136:139], v[92:95]\n" \
"ds_read_b64_tr_b16 a[238:239], v35 offset:4608\n" \
"v_mfma_f32_16x16x32_bf16 v[92:95], a[248:251], a[200:203], v[92:95]\n" \
"s_add_u32 m0, s30, 0x20000\n" \
"s_movk_i32 s84, 0x400\n" \
"v_mfma_f32_16x16x32_bf16 v[96:99], a[188:191], a[136:139], v[96:99]\n" \
"buffer_load_dwordx4 v24, s[76:79], s84 offen offset:0 lds\n" \
"v_mfma_f32_16x16x32_bf16 v[96:99], a[252:255], a[200:203], v[96:99]\n" \
"ds_read_b128 a[136:139], v28 offset:4096\n" \
"v_mfma_f32_16x16x32_bf16 v[112:115], a[188:191], a[140:143], v[112:115]\n" \
"ds_read_b128 a[200:203], v29 offset:4096\n" \
"v_mfma_f32_16x16x32_bf16 v[112:115], a[252:255], a[204:207], v[112:115]\n" \
"ds_read_b64_tr_b16 a[168:169], v34 offset:0\n" \
"v_mfma_f32_16x16x32_bf16 v[108:111], a[184:187], a[140:143], v[108:111]\n" \
"s_movk_i32 s84, 0x2400\n" \
"v_mfma_f32_16x16x32_bf16 v[108:111], a[248:251], a[204:207], v[108:111]\n" \
"buffer_load_dwordx4 v25, s[76:79], s84 offen offset:1024 lds\n" \
"v_mfma_f32_16x16x32_bf16 v[104:107], a[180:183], a[140:143], v[104:107]\n" \
"ds_read_b64_tr_b16 a[170:171], v34 offset:512\n" \
"v_mfma_f32_16x16x32_bf16 v[104:107], a[244:247], a[204:207], v[104:107]\n" \
"ds_read_b64_tr_b16 a[232:233], v34 offset:4096\n" \
"v_mfma_f32_16x16x32_bf16 v[100:103], a[176:179], a[140:143], v[100:103]\n" \
"s_add_u32 s6, s26, s6\n" \
"s_addc_u32 s9, s27, 0\n" \
"v_mfma_f32_16x16x32_bf16 v[100:103], a[240:243], a[204:207], v[100:103]\n" \
"ds_read_b128 a[140:143], v28 offset:6144\n" \
"v_mfma_f32_16x16x32_bf16 v[52:55], a[176:179], a[128:131], v[52:55]\n" \
"ds_read_b128 a[204:207], v29 offset:6144\n" \
"v_mfma_f32_16x16x32_bf16 v[52:55], a[240:243], a[192:195], v[52:55]\n" \
"s_movk_i32 s84, 0x4400\n" \
"v_mfma_f32_16x16x32_bf16 v[56:59], a[180:183], a[128:131], v[56:59]\n" \
"buffer_load_dwordx4 v24, s[76:79], s84 offen offset:2048 lds\n" \
"v_mfma_f32_16x16x32_bf16 v[56:59], a[244:247], a[192:195], v[56:59]\n" \
"s_movk_i32 s84, 0x6400\n" \
"v_mfma_f32_16x16x32_bf16 v[60:63], a[184:187], a[128:131], v[60:63]\n" \
"buffer_load_dwordx4 v25, s[76:79], s84 offen offset:3072 lds\n" \
"v_mfma_f32_16x16x32_bf16 v[60:63], a[248:251], a[192:195], v[60:63]\n" \
"ds_read_b64_tr_b16 a[234:235], v34 offset:4608\n" \
"v_mfma_f32_16x16x32_bf16 v[64:67], a[188:191], a[128:131], v[64:67]\n" \
"s_add_u32 s8, s6, 0x1000000\n" \
"s_addc_u32 s6, s9, 0\n" \
"v_mfma_f32_16x16x32_bf16 v[64:67], a[252:255], a[192:195], v[64:67]\n" \
"ds_read_b128 a[128:131], v28 offset:0\n" \
"s_and_b32 s9, s6, 0xffff\n" \
"v_mfma_f32_16x16x32_bf16 v[80:83], a[188:191], a[132:135], v[80:83]\n" \
"ds_read_b128 a[192:195], v29 offset:0\n" \
"s_lshl_b32 s6, s5, 16\n" \
"v_mfma_f32_16x16x32_bf16 v[80:83], a[252:255], a[196:199], v[80:83]\n" \
"s_sub_i32 s10, 0x100000, s6\n" \
"s_mov_b32 s6, 0x8000\n" \
"v_mfma_f32_16x16x32_bf16 v[76:79], a[184:187], a[132:135], v[76:79]\n" \
"s_cmp_lg_u32 s5, 0\n" \
"s_cselect_b32 s17, s11, s6\n" \
"v_mfma_f32_16x16x32_bf16 v[76:79], a[248:251], a[196:199], v[76:79]\n" \
"s_lshl_b32 s6, s3, 1\n" \
"s_lshl_b32 s5, s5, 3\n" \
"v_mfma_f32_16x16x32_bf16 v[72:75], a[180:183], a[132:135], v[72:75]\n" \
"s_and_b32 s6, s6, 4\n" \
"s_or_b32 s12, s5, s6\n" \
"v_mfma_f32_16x16x32_bf16 v[72:75], a[244:247], a[196:199], v[72:75]\n" \
"s_sub_i32 s5, s13, s5\n" \
"s_or_b32 s5, s5, s6\n" \
"v_mfma_f32_16x16x32_bf16 v[68:71], a[176:179], a[132:135], v[68:71]\n" \
"s_or_b32 s6, s12, s13\n" \
"s_lshl_b32 s4, s4, 2\n" \
"v_mfma_f32_16x16x32_bf16 v[68:71], a[240:243], a[196:199], v[68:71]\n" \
"ds_read_b128 a[132:135], v28 offset:2048\n" \
"ds_read_b128 a[196:199], v29 offset:2048\n" \
"s_waitcnt lgkmcnt(15)\n" \
"v_mfma_f32_16x16x32_bf16 a[0:3], a[160:163], a[144:147], a[0:3]\n" \
"s_add_u32 m0, s30, 0x21000\n" \
"s_addk_i32 s84, 0x3000\n" \
"v_mfma_f32_16x16x32_bf16 a[0:3], a[224:227], a[208:211], a[0:3]\n" \
"buffer_load_dwordx4 v24, s[76:79], s84 offen offset:0 lds\n" \
"v_mfma_f32_16x16x32_bf16 a[4:7], a[164:167], a[144:147], a[4:7]\n" \
"ds_read_b64_tr_b16 a[176:177], v36 offset:0\n" \
"s_mulk_i32 s6, 0x92\n" \
"v_mfma_f32_16x16x32_bf16 a[4:7], a[228:231], a[208:211], a[4:7]\n" \
"ds_read_b64_tr_b16 a[178:179], v36 offset:1024\n" \
"s_add_i32 s4, s4, 18\n" \
"s_waitcnt lgkmcnt(15)\n" \
"v_mfma_f32_16x16x32_bf16 a[12:15], a[172:175], a[144:147], a[12:15]\n" \
"ds_read_b64_tr_b16 a[240:241], v36 offset:8192\n" \
"s_mulk_i32 s5, 0x92\n" \
"s_waitcnt lgkmcnt(15)\n" \
"v_mfma_f32_16x16x32_bf16 a[12:15], a[236:239], a[208:211], a[12:15]\n" \
"s_add_u32 s88, s42, 0x0\n" \
"buffer_store_dwordx4 v[196:199], v20, s[80:83], s88 offen offset:3072 sc1\n" \
"s_waitcnt lgkmcnt(11)\n" \
"v_mfma_f32_16x16x32_bf16 a[8:11], a[144:147], a[168:171], a[8:11]\n" \
"ds_read_b64_tr_b16 a[242:243], v36 offset:9216\n" \
"s_add_i32 s6, s4, s6\n" \
"s_waitcnt lgkmcnt(8)\n" \
"v_mfma_f32_16x16x32_bf16 a[8:11], a[208:211], a[232:235], a[8:11]\n" \
"ds_read_b64_tr_b16 a[180:181], v37 offset:0\n" \
"s_add_i32 s5, s5, s4\n" \
"v_mfma_f32_16x16x32_bf16 a[24:27], a[148:151], a[168:171], a[24:27]\n" \
"ds_read_b64_tr_b16 a[182:183], v37 offset:1024\n" \
"s_lshl_b32 s4, s3, 11\n" \
"v_mfma_f32_16x16x32_bf16 a[24:27], a[212:215], a[232:235], a[24:27]\n" \
"ds_read_b64_tr_b16 a[244:245], v37 offset:8192\n" \
"s_lshl_b32 s15, s13, 12\n" \
"v_mfma_f32_16x16x32_bf16 a[28:31], a[172:175], a[148:151], a[28:31]\n" \
"s_lshl_b32 s16, s19, 14\n" \
"s_mul_i32 s19, s19, 0xe000\n" \
"v_mfma_f32_16x16x32_bf16 a[28:31], a[236:239], a[212:215], a[28:31]\n" \
"s_addk_i32 s84, 0x2000\n" \
"v_mfma_f32_16x16x32_bf16 a[20:23], a[164:167], a[148:151], a[20:23]\n" \
"buffer_load_dwordx4 v25, s[76:79], s84 offen offset:1024 lds\n" \
"v_mfma_f32_16x16x32_bf16 a[20:23], a[228:231], a[212:215], a[20:23]\n" \
"ds_read_b64_tr_b16 a[246:247], v37 offset:9216\n" \
"s_mov_b32 s11, 0x20000\n" \
"v_mfma_f32_16x16x32_bf16 a[16:19], a[160:163], a[148:151], a[16:19]\n" \
"ds_read_b64_tr_b16 a[184:185], v38 offset:0\n" \
"s_or_b32 s12, s4, 0xc000\n" \
"v_mfma_f32_16x16x32_bf16 a[16:19], a[224:227], a[212:215], a[16:19]\n" \
"ds_read_b64_tr_b16 a[186:187], v38 offset:1024\n" \
"s_lshl_b32 s4, s3, 8\n" \
"v_mfma_f32_16x16x32_bf16 a[32:35], a[160:163], a[152:155], a[32:35]\n" \
"s_addk_i32 s84, 0x2000\n" \
"v_mfma_f32_16x16x32_bf16 a[32:35], a[224:227], a[216:219], a[32:35]\n" \
"buffer_load_dwordx4 v24, s[76:79], s84 offen offset:2048 lds\n" \
"v_mfma_f32_16x16x32_bf16 a[36:39], a[164:167], a[152:155], a[36:39]\n" \
"ds_read_b64_tr_b16 a[248:249], v38 offset:8192\n" \
"s_or_b32 s22, s15, s16\n" \
"v_mfma_f32_16x16x32_bf16 a[36:39], a[228:231], a[216:219], a[36:39]\n" \
"ds_read_b64_tr_b16 a[250:251], v38 offset:9216\n" \
"s_or_b32 s23, s19, s15\n" \
"v_mfma_f32_16x16x32_bf16 a[44:47], a[172:175], a[152:155], a[44:47]\n" \
"ds_read_b64_tr_b16 a[188:189], v39 offset:0\n" \
"s_lshl_b32 s60, s6, 10\n" \
"v_mfma_f32_16x16x32_bf16 a[44:47], a[236:239], a[216:219], a[44:47]\n" \
"ds_read_b64_tr_b16 a[190:191], v39 offset:1024\n" \
"s_lshl_b32 s61, s5, 10\n" \
"v_mfma_f32_16x16x32_bf16 a[40:43], a[152:155], a[168:171], a[40:43]\n" \
"s_add_u32 s89, s42, 0x2000\n" \
"buffer_store_dwordx4 v[200:203], v20, s[80:83], s89 offen offset:0 sc1\n" \
"v_mfma_f32_16x16x32_bf16 a[40:43], a[216:219], a[232:235], a[40:43]\n" \
"ds_read_b64_tr_b16 a[252:253], v39 offset:8192\n" \
"s_lshl_b32 s14, s14, 15\n" \
"v_mfma_f32_16x16x32_bf16 a[56:59], a[156:159], a[168:171], a[56:59]\n" \
"ds_read_b64_tr_b16 a[254:255], v39 offset:9216\n" \
"s_or_b32 s20, s4, 0x26c00\n" \
"v_mfma_f32_16x16x32_bf16 a[56:59], a[220:223], a[232:235], a[56:59]\n" \
"s_or_b32 s13, s22, 0x8000\n" \
"s_add_i32 s22, s22, 0x14000\n" \
"v_mfma_f32_16x16x32_bf16 a[60:63], a[172:175], a[156:159], a[60:63]\n" \
"s_bitset1_b32 s23, 16\n" \
"s_or_b32 s24, s7, 0x5be8020\n" \
"v_mfma_f32_16x16x32_bf16 a[60:63], a[236:239], a[220:223], a[60:63]\n" \
"s_addk_i32 s84, 0x2000\n" \
"v_mfma_f32_16x16x32_bf16 a[52:55], a[164:167], a[156:159], a[52:55]\n" \
".fill (L_pb + 73440 - .) / 4, 4, 0xBF800000\n" \
"L_pp18:\n" \
"s_cbranch_execz L_pret\n" \
"buffer_load_dwordx4 v25, s[76:79], s84 offen offset:3072 lds\n" \
"v_mfma_f32_16x16x32_bf16 a[52:55], a[228:231], a[220:223], a[52:55]\n" \
"s_and_b32 s57, s27, 0xffff\n" \
"s_mov_b32 s58, 0x6000000\n" \
"v_mfma_f32_16x16x32_bf16 a[48:51], a[160:163], a[156:159], a[48:51]\n" \
"s_mov_b32 s56, s26\n" \
"s_mov_b32 s59, s11\n" \
"v_mfma_f32_16x16x32_bf16 a[48:51], a[224:227], a[220:223], a[48:51]\n" \
"s_add_i32 s60, s60, 0x3000000\n" \
"s_add_i32 s61, s61, 0x411c000\n" \
"v_mfma_f32_16x16x32_bf16 a[64:67], a[164:167], a[140:143], a[64:67]\n" \
"s_sub_i32 s19, 14, s18\n" \
"s_mov_b32 s21, 0x71800000\n" \
"v_mfma_f32_16x16x32_bf16 a[64:67], a[228:231], a[204:207], a[64:67]\n" \
"s_mov_b32 s4, L_gsw-L_gtr\n" \
"v_mfma_f32_16x16x32_bf16 a[80:83], a[172:175], a[140:143], a[80:83]\n" \
"s_cmp_eq_u32 s19, 0\n" \
"s_cselect_b32 s4, L_gs7-L_gtr, s4\n" \
"v_mfma_f32_16x16x32_bf16 a[80:83], a[236:239], a[204:207], a[80:83]\n" \
"s_cmp_eq_u32 s16, 0\n" \
"s_cselect_b32 s4, 0, s4\n" \
"v_mfma_f32_16x16x32_bf16 a[68:71], a[140:143], a[168:171], a[68:71]\n" \
"s_getpc_b64 s[28:29]\n" \
"L_gp1_%=:\n" \
"s_add_u32 s28, s28, L_gtr-L_gp1_%=\n" \
"s_addc_u32 s29, s29, 0\n" \
"v_mfma_f32_16x16x32_bf16 a[68:71], a[204:207], a[232:235], a[68:71]\n" \
"s_waitcnt vmcnt(11) lgkmcnt(0)\n" \
"s_barrier\n" \
"v_mfma_f32_16x16x32_bf16 v[116:119], a[176:179], a[144:147], v[116:119]\n" \
"s_add_u32 m0, s40, 0x0\n" \
"s_add_u32 s85, s39, 0x10000\n" \
"v_mfma_f32_16x16x32_bf16 v[116:119], a[240:243], a[208:211], v[116:119]\n" \
"buffer_load_dwordx4 v20, s[80:83], s85 offen sc1 lds\n" \
"v_mfma_f32_16x16x32_bf16 v[120:123], a[180:183], a[144:147], v[120:123]\n" \
"ds_read_b64_tr_b16 a[160:161], v32 offset:32768\n" \
"v_mfma_f32_16x16x32_bf16 v[120:123], a[244:247], a[208:211], v[120:123]\n" \
"ds_read_b64_tr_b16 a[162:163], v32 offset:33280\n" \
"v_mfma_f32_16x16x32_bf16 v[124:127], a[184:187], a[144:147], v[124:127]\n" \
"s_add_u32 s28, s28, s4\n" \
"s_addc_u32 s29, s29, 0\n" \
"v_mfma_f32_16x16x32_bf16 v[124:127], a[248:251], a[208:211], v[124:127]\n" \
"v_mfma_f32_16x16x32_bf16 v[128:131], a[188:191], a[144:147], v[128:131]\n" \
"ds_read_b64_tr_b16 a[224:225], v32 offset:36864\n" \
"v_mfma_f32_16x16x32_bf16 v[128:131], a[252:255], a[208:211], v[128:131]\n" \
"ds_read_b128 a[144:147], v28 offset:40960\n" \
"v_mfma_f32_16x16x32_bf16 v[144:147], a[188:191], a[148:151], v[144:147]\n" \
"ds_read_b128 a[208:211], v29 offset:40960\n" \
"v_mfma_f32_16x16x32_bf16 v[144:147], a[252:255], a[212:215], v[144:147]\n" \
"s_add_u32 m0, s40, 0x1000\n" \
"s_add_u32 s85, s39, 0x11000\n" \
"v_mfma_f32_16x16x32_bf16 v[140:143], a[184:187], a[148:151], v[140:143]\n" \
"buffer_load_dwordx4 v20, s[80:83], s85 offen sc1 lds\n" \
"v_mfma_f32_16x16x32_bf16 v[140:143], a[248:251], a[212:215], v[140:143]\n" \
"ds_read_b64_tr_b16 a[226:227], v32 offset:37376\n" \
"v_mfma_f32_16x16x32_bf16 v[136:139], a[180:183], a[148:151], v[136:139]\n" \
"v_mfma_f32_16x16x32_bf16 v[136:139], a[244:247], a[212:215], v[136:139]\n" \
"ds_read_b64_tr_b16 a[164:165], v33 offset:32768\n" \
"v_mfma_f32_16x16x32_bf16 v[132:135], a[176:179], a[148:151], v[132:135]\n" \
"v_mfma_f32_16x16x32_bf16 v[132:135], a[240:243], a[212:215], v[132:135]\n" \
"ds_read_b128 a[148:151], v28 offset:43008\n" \
"v_mfma_f32_16x16x32_bf16 v[148:151], a[176:179], a[152:155], v[148:151]\n" \
"ds_read_b128 a[212:215], v29 offset:43008\n" \
"v_mfma_f32_16x16x32_bf16 v[148:151], a[240:243], a[216:219], v[148:151]\n" \
"s_add_u32 m0, s40, 0x3000\n" \
"s_movk_i32 s84, 0x400\n" \
"v_mfma_f32_16x16x32_bf16 v[152:155], a[180:183], a[152:155], v[152:155]\n" \
"buffer_load_dwordx4 v21, s[76:79], s84 offen offset:0 lds\n" \
"v_mfma_f32_16x16x32_bf16 v[152:155], a[244:247], a[216:219], v[152:155]\n" \
"ds_read_b64_tr_b16 a[166:167], v33 offset:33280\n" \
"v_mfma_f32_16x16x32_bf16 v[156:159], a[184:187], a[152:155], v[156:159]\n" \
"ds_read_b64_tr_b16 a[228:229], v33 offset:36864\n" \
"v_mfma_f32_16x16x32_bf16 v[156:159], a[248:251], a[216:219], v[156:159]\n" \
"s_add_u32 m0, s31, 0x0\n" \
"s_mov_b32 s84, 0x400000\n" \
"v_mfma_f32_16x16x32_bf16 v[160:163], a[188:191], a[152:155], v[160:163]\n" \
"buffer_load_dwordx4 v26, s[68:71], s84 offen offset:0 lds\n" \
"v_mfma_f32_16x16x32_bf16 v[160:163], a[252:255], a[216:219], v[160:163]\n" \
"ds_read_b128 a[152:155], v28 offset:45056\n" \
"v_mfma_f32_16x16x32_bf16 v[176:179], a[188:191], a[156:159], v[176:179]\n" \
"ds_read_b128 a[216:219], v29 offset:45056\n" \
"v_mfma_f32_16x16x32_bf16 v[176:179], a[252:255], a[220:223], v[176:179]\n" \
"ds_read_b64_tr_b16 a[230:231], v33 offset:37376\n" \
"v_mfma_f32_16x16x32_bf16 v[172:175], a[184:187], a[156:159], v[172:175]\n" \
"ds_read_b64_tr_b16 a[172:173], v35 offset:32768\n" \
"v_mfma_f32_16x16x32_bf16 v[172:175], a[248:251], a[220:223], v[172:175]\n" \
"v_mfma_f32_16x16x32_bf16 v[168:171], a[180:183], a[156:159], v[168:171]\n" \
"s_add_u32 s88, s42, 0x2000\n" \
"buffer_store_dwordx4 v[204:207], v20, s[80:83], s88 offen offset:1024 sc1\n" \
"v_mfma_f32_16x16x32_bf16 v[168:171], a[244:247], a[220:223], v[168:171]\n" \
"ds_read_b64_tr_b16 a[174:175], v35 offset:33280\n" \
"v_mfma_f32_16x16x32_bf16 v[164:167], a[176:179], a[156:159], v[164:167]\n" \
"v_mfma_f32_16x16x32_bf16 v[164:167], a[240:243], a[220:223], v[164:167]\n" \
"ds_read_b128 a[156:159], v28 offset:47104\n" \
"v_mfma_f32_16x16x32_bf16 v[84:87], a[176:179], a[136:139], v[84:87]\n" \
"ds_read_b128 a[220:223], v29 offset:47104\n" \
"v_mfma_f32_16x16x32_bf16 v[84:87], a[240:243], a[200:203], v[84:87]\n" \
"ds_read_b64_tr_b16 a[236:237], v35 offset:36864\n" \
"v_mfma_f32_16x16x32_bf16 v[88:91], a[180:183], a[136:139], v[88:91]\n" \
"v_mfma_f32_16x16x32_bf16 v[88:91], a[244:247], a[200:203], v[88:91]\n" \
"s_addk_i32 s84, 0x7c00\n" \
"s_nop 0\n" \
"v_mfma_f32_16x16x32_bf16 v[92:95], a[184:187], a[136:139], v[92:95]\n" \
"buffer_load_dwordx4 v26, s[68:71], s84 offen offset:1024 lds\n" \
"v_mfma_f32_16x16x32_bf16 v[92:95], a[248:251], a[200:203], v[92:95]\n" \
"ds_read_b64_tr_b16 a[238:239], v35 offset:37376\n" \
"v_mfma_f32_16x16x32_bf16 v[96:99], a[188:191], a[136:139], v[96:99]\n" \
"v_mfma_f32_16x16x32_bf16 v[96:99], a[252:255], a[200:203], v[96:99]\n" \
"ds_read_b128 a[136:139], v28 offset:36864\n" \
"v_mfma_f32_16x16x32_bf16 v[112:115], a[188:191], a[140:143], v[112:115]\n" \
"ds_read_b128 a[200:203], v29 offset:36864\n" \
"v_mfma_f32_16x16x32_bf16 v[112:115], a[252:255], a[204:207], v[112:115]\n" \
"ds_read_b64_tr_b16 a[168:169], v34 offset:32768\n" \
"v_mfma_f32_16x16x32_bf16 v[108:111], a[184:187], a[140:143], v[108:111]\n" \
"s_addk_i32 s84, 0x7c00\n" \
"s_nop 0\n" \
"v_mfma_f32_16x16x32_bf16 v[108:111], a[248:251], a[204:207], v[108:111]\n" \
"buffer_load_dwordx4 v27, s[68:71], s84 offen offset:2048 lds\n" \
"v_mfma_f32_16x16x32_bf16 v[104:107], a[180:183], a[140:143], v[104:107]\n" \
"ds_read_b64_tr_b16 a[170:171], v34 offset:33280\n" \
"v_mfma_f32_16x16x32_bf16 v[104:107], a[244:247], a[204:207], v[104:107]\n" \
"ds_read_b64_tr_b16 a[232:233], v34 offset:36864\n" \
"v_mfma_f32_16x16x32_bf16 v[100:103], a[176:179], a[140:143], v[100:103]\n" \
"v_mfma_f32_16x16x32_bf16 v[100:103], a[240:243], a[204:207], v[100:103]\n" \
"ds_read_b128 a[140:143], v28 offset:38912\n" \
"v_mfma_f32_16x16x32_bf16 v[52:55], a[176:179], a[128:131], v[52:55]\n" \
"ds_read_b128 a[204:207], v29 offset:38912\n" \
"v_mfma_f32_16x16x32_bf16 v[52:55], a[240:243], a[192:195], v[52:55]\n" \
"s_addk_i32 s84, 0x7c00\n" \
"s_nop 0\n" \
"v_mfma_f32_16x16x32_bf16 v[56:59], a[180:183], a[128:131], v[56:59]\n" \
"buffer_load_dwordx4 v27, s[68:71], s84 offen offset:3072 lds\n" \
"v_mfma_f32_16x16x32_bf16 v[56:59], a[244:247], a[192:195], v[56:59]\n" \
"ds_read_b64_tr_b16 a[234:235], v34 offset:37376\n" \
"v_mfma_f32_16x16x32_bf16 v[60:63], a[184:187], a[128:131], v[60:63]\n" \
"v_mfma_f32_16x16x32_bf16 v[60:63], a[248:251], a[192:195], v[60:63]\n" \
"v_mfma_f32_16x16x32_bf16 v[64:67], a[188:191], a[128:131], v[64:67]\n" \
"v_mfma_f32_16x16x32_bf16 v[64:67], a[252:255], a[192:195], v[64:67]\n" \
"ds_read_b128 a[128:131], v28 offset:32768\n" \
"v_mfma_f32_16x16x32_bf16 v[80:83], a[188:191], a[132:135], v[80:83]\n" \
"ds_read_b128 a[192:195], v29 offset:32768\n" \
"v_mfma_f32_16x16x32_bf16 v[80:83], a[252:255], a[196:199], v[80:83]\n" \
"v_mfma_f32_16x16x32_bf16 v[76:79], a[184:187], a[132:135], v[76:79]\n" \
"v_mfma_f32_16x16x32_bf16 v[76:79], a[248:251], a[196:199], v[76:79]\n" \
"v_mfma_f32_16x16x32_bf16 v[72:75], a[180:183], a[132:135], v[72:75]\n" \
"v_mfma_f32_16x16x32_bf16 v[72:75], a[244:247], a[196:199], v[72:75]\n" \
"v_mfma_f32_16x16x32_bf16 v[68:71], a[176:179], a[132:135], v[68:71]\n" \
"v_mfma_f32_16x16x32_bf16 v[68:71], a[240:243], a[196:199], v[68:71]\n" \
"ds_read_b128 a[132:135], v28 offset:34816\n" \
"ds_read_b128 a[196:199], v29 offset:34816\n" \
"s_waitcnt lgkmcnt(15)\n" \
"s_nop 0\n" \
"v_mfma_f32_16x16x32_bf16 a[0:3], a[160:163], a[144:147], a[0:3]\n" \
"s_add_u32 s89, s42, 0x2000\n" \
"buffer_store_dwordx4 v[208:211], v20, s[80:83], s89 offen offset:2048 sc1\n" \
"v_mfma_f32_16x16x32_bf16 a[0:3], a[224:227], a[208:211], a[0:3]\n" \
"ds_read_b64_tr_b16 a[176:177], v36 offset:32768\n" \
"v_mfma_f32_16x16x32_bf16 a[4:7], a[164:167], a[144:147], a[4:7]\n" \
"ds_read_b64_tr_b16 a[178:179], v36 offset:33792\n" \
"v_mfma_f32_16x16x32_bf16 a[4:7], a[228:231], a[208:211], a[4:7]\n" \
"ds_read_b64_tr_b16 a[240:241], v36 offset:40960\n" \
"v_mfma_f32_16x16x32_bf16 a[12:15], a[172:175], a[144:147], a[12:15]\n" \
"ds_read_b64_tr_b16 a[242:243], v36 offset:41984\n" \
"s_waitcnt lgkmcnt(15)\n" \
"v_mfma_f32_16x16x32_bf16 a[12:15], a[236:239], a[208:211], a[12:15]\n" \
"s_waitcnt lgkmcnt(12)\n" \
"v_mfma_f32_16x16x32_bf16 a[8:11], a[144:147], a[168:171], a[8:11]\n" \
"ds_read_b64_tr_b16 a[180:181], v37 offset:32768\n" \
"s_waitcnt lgkmcnt(9)\n" \
"s_nop 0\n" \
"v_mfma_f32_16x16x32_bf16 a[8:11], a[208:211], a[232:235], a[8:11]\n" \
"v_mfma_f32_16x16x32_bf16 a[24:27], a[148:151], a[168:171], a[24:27]\n" \
"ds_read_b64_tr_b16 a[182:183], v37 offset:33792\n" \
"v_mfma_f32_16x16x32_bf16 a[24:27], a[212:215], a[232:235], a[24:27]\n" \
"ds_read_b64_tr_b16 a[244:245], v37 offset:40960\n" \
"v_mfma_f32_16x16x32_bf16 a[28:31], a[172:175], a[148:151], a[28:31]\n" \
"v_mfma_f32_16x16x32_bf16 a[28:31], a[236:239], a[212:215], a[28:31]\n" \
"ds_read_b64_tr_b16 a[246:247], v37 offset:41984\n" \
"v_mfma_f32_16x16x32_bf16 a[20:23], a[164:167], a[148:151], a[20:23]\n" \
"v_mfma_f32_16x16x32_bf16 a[20:23], a[228:231], a[212:215], a[20:23]\n" \
"ds_read_b64_tr_b16 a[184:185], v38 offset:32768\n" \
"v_mfma_f32_16x16x32_bf16 a[16:19], a[160:163], a[148:151], a[16:19]\n" \
"ds_read_b64_tr_b16 a[186:187], v38 offset:33792\n" \
"v_mfma_f32_16x16x32_bf16 a[16:19], a[224:227], a[212:215], a[16:19]\n" \
"v_mfma_f32_16x16x32_bf16 a[32:35], a[160:163], a[152:155], a[32:35]\n" \
"s_add_u32 s88, s42, 0x2000\n" \
"buffer_store_dwordx4 v[212:215], v20, s[80:83], s88 offen offset:3072 sc1\n" \
"v_mfma_f32_16x16x32_bf16 a[32:35], a[224:227], a[216:219], a[32:35]\n" \
"ds_read_b64_tr_b16 a[248:249], v38 offset:40960\n" \
"v_mfma_f32_16x16x32_bf16 a[36:39], a[164:167], a[152:155], a[36:39]\n" \
"v_lshlrev_b32_e32 v45, 3, v20\n" \
"v_add_u32_e32 v45, s30, v45\n" \
"v_mfma_f32_16x16x32_bf16 a[36:39], a[228:231], a[216:219], a[36:39]\n" \
"s_getpc_b64 s[86:87]\n" \
"L_ct1_%=:\n" \
"s_add_u32 s86, s86, L_ap0-L_ct1_%=\n" \
"s_addc_u32 s87, s87, 0\n" \
"s_add_u32 m0, s40, 0x2000\n" \
"v_mfma_f32_16x16x32_bf16 a[44:47], a[172:175], a[152:155], a[44:47]\n" \
"global_load_lds_dword v45, s[86:87]\n" \
"v_mfma_f32_16x16x32_bf16 a[44:47], a[236:239], a[216:219], a[44:47]\n" \
"ds_read_b64_tr_b16 a[250:251], v38 offset:41984\n" \
"v_mfma_f32_16x16x32_bf16 a[40:43], a[152:155], a[168:171], a[40:43]\n" \
"ds_read_b64_tr_b16 a[188:189], v39 offset:32768\n" \
"v_mfma_f32_16x16x32_bf16 a[40:43], a[216:219], a[232:235], a[40:43]\n" \
"ds_read_b64_tr_b16 a[190:191], v39 offset:33792\n" \
"v_mfma_f32_16x16x32_bf16 a[56:59], a[156:159], a[168:171], a[56:59]\n" \
"ds_read_b64_tr_b16 a[252:253], v39 offset:40960\n" \
"v_mfma_f32_16x16x32_bf16 a[56:59], a[220:223], a[232:235], a[56:59]\n" \
"ds_read_b64_tr_b16 a[254:255], v39 offset:41984\n" \
"v_mfma_f32_16x16x32_bf16 a[60:63], a[172:175], a[156:159], a[60:63]\n" \
"v_mfma_f32_16x16x32_bf16 a[60:63], a[236:239], a[220:223], a[60:63]\n" \
"v_mfma_f32_16x16x32_bf16 a[52:55], a[164:167], a[156:159], a[52:55]\n" \
"v_mfma_f32_16x16x32_bf16 a[52:55], a[228:231], a[220:223], a[52:55]\n" \
"v_mfma_f32_16x16x32_bf16 a[48:51], a[160:163], a[156:159], a[48:51]\n" \
"v_mfma_f32_16x16x32_bf16 a[48:51], a[224:227], a[220:223], a[48:51]\n" \
"v_mfma_f32_16x16x32_bf16 a[64:67], a[164:167], a[140:143], a[64:67]\n" \
"v_mfma_f32_16x16x32_bf16 a[64:67], a[228:231], a[204:207], a[64:67]\n" \
"v_mfma_f32_16x16x32_bf16 a[80:83], a[172:175], a[140:143], a[80:83]\n" \
"v_mfma_f32_16x16x32_bf16 a[80:83], a[236:239], a[204:207], a[80:83]\n" \
"v_mfma_f32_16x16x32_bf16 a[68:71], a[140:143], a[168:171], a[68:71]\n" \
"v_mfma_f32_16x16x32_bf16 a[68:71], a[204:207], a[232:235], a[68:71]\n" \
"s_waitcnt vmcnt(3) lgkmcnt(0)\n" \
"s_barrier\n" \
"v_xor_b32_e32 v28, 0x20000, v28\n" \
"v_xor_b32_e32 v29, 0x20000, v29\n" \
"v_mfma_f32_16x16x32_bf16 v[116:119], a[176:179], a[144:147], v[116:119]\n" \
"ds_read_b64_tr_b16 a[160:161], v32 offset:0\n" \
"v_accvgpr_read_b32 v184, a0\n" \
"v_mfma_f32_16x16x32_bf16 v[116:119], a[240:243], a[208:211], v[116:119]\n" \
"v_accvgpr_read_b32 v185, a1\n" \
"v_accvgpr_read_b32 v186, a2\n" \
"v_mfma_f32_16x16x32_bf16 v[120:123], a[180:183], a[144:147], v[120:123]\n" \
"v_accvgpr_read_b32 v187, a3\n" \
"v_accvgpr_read_b32 v188, a4\n" \
"v_mfma_f32_16x16x32_bf16 v[120:123], a[244:247], a[208:211], v[120:123]\n" \
"ds_read_b64_tr_b16 a[162:163], v32 offset:512\n" \
"v_accvgpr_read_b32 v189, a5\n" \
"v_mfma_f32_16x16x32_bf16 v[124:127], a[184:187], a[144:147], v[124:127]\n" \
"v_accvgpr_read_b32 v190, a6\n" \
"v_accvgpr_read_b32 v191, a7\n" \
"v_mfma_f32_16x16x32_bf16 v[124:127], a[248:251], a[208:211], v[124:127]\n" \
"v_accvgpr_read_b32 v196, a12\n" \
"v_accvgpr_read_b32 v197, a13\n" \
"v_mfma_f32_16x16x32_bf16 v[128:131], a[188:191], a[144:147], v[128:131]\n" \
"ds_read_b64_tr_b16 a[224:225], v32 offset:4096\n" \
"v_accvgpr_read_b32 v198, a14\n" \
"v_mfma_f32_16x16x32_bf16 v[128:131], a[252:255], a[208:211], v[128:131]\n" \
"ds_read_b128 a[144:147], v28 offset:8192\n" \
"v_accvgpr_read_b32 v199, a15\n" \
"v_mfma_f32_16x16x32_bf16 v[144:147], a[188:191], a[148:151], v[144:147]\n" \
"ds_read_b128 a[208:211], v29 offset:8192\n" \
"v_accvgpr_read_b32 v192, a8\n" \
"v_mfma_f32_16x16x32_bf16 v[144:147], a[252:255], a[212:215], v[144:147]\n" \
"ds_read_b64_tr_b16 a[226:227], v32 offset:4608\n" \
"v_accvgpr_read_b32 v193, a9\n" \
"v_mfma_f32_16x16x32_bf16 v[140:143], a[184:187], a[148:151], v[140:143]\n" \
"v_accvgpr_read_b32 v194, a10\n" \
"v_accvgpr_read_b32 v195, a11\n" \
"v_mfma_f32_16x16x32_bf16 v[140:143], a[248:251], a[212:215], v[140:143]\n" \
"v_accvgpr_read_b32 v208, a24\n" \
"v_accvgpr_read_b32 v209, a25\n" \
"v_mfma_f32_16x16x32_bf16 v[136:139], a[180:183], a[148:151], v[136:139]\n" \
"v_accvgpr_read_b32 v210, a26\n" \
"v_accvgpr_read_b32 v211, a27\n" \
"v_mfma_f32_16x16x32_bf16 v[136:139], a[244:247], a[212:215], v[136:139]\n" \
"ds_read_b64_tr_b16 a[164:165], v33 offset:0\n" \
"v_accvgpr_read_b32 v212, a28\n" \
"v_mfma_f32_16x16x32_bf16 v[132:135], a[176:179], a[148:151], v[132:135]\n" \
"v_accvgpr_read_b32 v213, a29\n" \
"v_accvgpr_read_b32 v214, a30\n" \
"v_mfma_f32_16x16x32_bf16 v[132:135], a[240:243], a[212:215], v[132:135]\n" \
"ds_read_b128 a[148:151], v28 offset:10240\n" \
"v_accvgpr_read_b32 v215, a31\n" \
"v_mfma_f32_16x16x32_bf16 v[148:151], a[176:179], a[152:155], v[148:151]\n" \
"ds_read_b128 a[212:215], v29 offset:10240\n" \
"v_accvgpr_read_b32 v204, a20\n" \
"v_mfma_f32_16x16x32_bf16 v[148:151], a[240:243], a[216:219], v[148:151]\n" \
"ds_read_b64_tr_b16 a[166:167], v33 offset:512\n" \
"v_accvgpr_read_b32 v205, a21\n" \
"v_mfma_f32_16x16x32_bf16 v[152:155], a[180:183], a[152:155], v[152:155]\n" \
"v_accvgpr_read_b32 v206, a22\n" \
"v_accvgpr_read_b32 v207, a23\n" \
"v_mfma_f32_16x16x32_bf16 v[152:155], a[244:247], a[216:219], v[152:155]\n" \
"ds_read_b64_tr_b16 a[228:229], v33 offset:4096\n" \
"v_accvgpr_read_b32 v200, a16\n" \
"v_mfma_f32_16x16x32_bf16 v[156:159], a[184:187], a[152:155], v[156:159]\n" \
"v_accvgpr_read_b32 v201, a17\n" \
"v_accvgpr_read_b32 v202, a18\n" \
"v_mfma_f32_16x16x32_bf16 v[156:159], a[248:251], a[216:219], v[156:159]\n" \
"v_accvgpr_read_b32 v203, a19\n" \
"v_accvgpr_read_b32 v216, a32\n" \
"v_mfma_f32_16x16x32_bf16 v[160:163], a[188:191], a[152:155], v[160:163]\n" \
"ds_read_b64_tr_b16 a[230:231], v33 offset:4608\n" \
"v_accvgpr_read_b32 v217, a33\n" \
"v_mfma_f32_16x16x32_bf16 v[160:163], a[252:255], a[216:219], v[160:163]\n" \
"ds_read_b128 a[152:155], v28 offset:12288\n" \
"v_accvgpr_read_b32 v218, a34\n" \
"v_mfma_f32_16x16x32_bf16 v[176:179], a[188:191], a[156:159], v[176:179]\n" \
"ds_read_b128 a[216:219], v29 offset:12288\n" \
"v_accvgpr_read_b32 v219, a35\n" \
"v_mfma_f32_16x16x32_bf16 v[176:179], a[252:255], a[220:223], v[176:179]\n" \
"v_accvgpr_read_b32 v220, a36\n" \
"v_accvgpr_read_b32 v221, a37\n" \
"v_mfma_f32_16x16x32_bf16 v[172:175], a[184:187], a[156:159], v[172:175]\n" \
"ds_read_b64_tr_b16 a[172:173], v35 offset:0\n" \
"v_accvgpr_read_b32 v222, a38\n" \
"v_mfma_f32_16x16x32_bf16 v[172:175], a[248:251], a[220:223], v[172:175]\n" \
"v_accvgpr_read_b32 v223, a39\n" \
"v_accvgpr_read_b32 v228, a44\n" \
"v_mfma_f32_16x16x32_bf16 v[168:171], a[180:183], a[156:159], v[168:171]\n" \
"v_accvgpr_read_b32 v229, a45\n" \
"v_accvgpr_read_b32 v230, a46\n" \
"v_mfma_f32_16x16x32_bf16 v[168:171], a[244:247], a[220:223], v[168:171]\n" \
"ds_read_b64_tr_b16 a[174:175], v35 offset:512\n" \
"v_accvgpr_read_b32 v231, a47\n" \
"v_mfma_f32_16x16x32_bf16 v[164:167], a[176:179], a[156:159], v[164:167]\n" \
"v_accvgpr_read_b32 v224, a40\n" \
"v_accvgpr_read_b32 v225, a41\n" \
"v_mfma_f32_16x16x32_bf16 v[164:167], a[240:243], a[220:223], v[164:167]\n" \
"ds_read_b128 a[156:159], v28 offset:14336\n" \
"v_accvgpr_read_b32 v226, a42\n" \
"v_mfma_f32_16x16x32_bf16 v[84:87], a[176:179], a[136:139], v[84:87]\n" \
"ds_read_b128 a[220:223], v29 offset:14336\n" \
"v_accvgpr_read_b32 v227, a43\n" \
"v_mfma_f32_16x16x32_bf16 v[84:87], a[240:243], a[200:203], v[84:87]\n" \
"ds_read_b64_tr_b16 a[236:237], v35 offset:4096\n" \
"v_accvgpr_read_b32 v240, a56\n" \
"v_mfma_f32_16x16x32_bf16 v[88:91], a[180:183], a[136:139], v[88:91]\n" \
"v_accvgpr_read_b32 v241, a57\n" \
"v_accvgpr_read_b32 v242, a58\n" \
"v_mfma_f32_16x16x32_bf16 v[88:91], a[244:247], a[200:203], v[88:91]\n" \
"ds_read_b64_tr_b16 a[238:239], v35 offset:4608\n" \
"v_accvgpr_read_b32 v243, a59\n" \
"v_mfma_f32_16x16x32_bf16 v[92:95], a[184:187], a[136:139], v[92:95]\n" \
"v_accvgpr_read_b32 v244, a60\n" \
"v_accvgpr_read_b32 v245, a61\n" \
"v_mfma_f32_16x16x32_bf16 v[92:95], a[248:251], a[200:203], v[92:95]\n" \
"v_accvgpr_read_b32 v246, a62\n" \
"v_accvgpr_read_b32 v247, a63\n" \
"v_mfma_f32_16x16x32_bf16 v[96:99], a[188:191], a[136:139], v[96:99]\n" \
"v_accvgpr_read_b32 v236, a52\n" \
"v_accvgpr_read_b32 v237, a53\n" \
"v_mfma_f32_16x16x32_bf16 v[96:99], a[252:255], a[200:203], v[96:99]\n" \
"ds_read_b128 a[136:139], v28 offset:4096\n" \
"v_accvgpr_read_b32 v238, a54\n" \
"v_mfma_f32_16x16x32_bf16 v[112:115], a[188:191], a[140:143], v[112:115]\n" \
"ds_read_b128 a[200:203], v29 offset:4096\n" \
"v_accvgpr_read_b32 v239, a55\n" \
"v_mfma_f32_16x16x32_bf16 v[112:115], a[252:255], a[204:207], v[112:115]\n" \
"ds_read_b64_tr_b16 a[168:169], v34 offset:0\n" \
"v_accvgpr_read_b32 v232, a48\n" \
"v_mfma_f32_16x16x32_bf16 v[108:111], a[184:187], a[140:143], v[108:111]\n" \
"ds_read_b64_tr_b16 a[170:171], v34 offset:512\n" \
"v_accvgpr_read_b32 v233, a49\n" \
"v_mfma_f32_16x16x32_bf16 v[108:111], a[248:251], a[204:207], v[108:111]\n" \
"v_accvgpr_read_b32 v234, a50\n" \
"v_accvgpr_read_b32 v235, a51\n" \
"v_mfma_f32_16x16x32_bf16 v[104:107], a[180:183], a[140:143], v[104:107]\n" \
"v_accvgpr_read_b32 v180, a64\n" \
"v_accvgpr_read_b32 v181, a65\n" \
"v_mfma_f32_16x16x32_bf16 v[104:107], a[244:247], a[204:207], v[104:107]\n" \
"ds_read_b64_tr_b16 a[232:233], v34 offset:4096\n" \
"v_accvgpr_read_b32 v182, a66\n" \
"v_mfma_f32_16x16x32_bf16 v[100:103], a[176:179], a[140:143], v[100:103]\n" \
"v_accvgpr_read_b32 v183, a67\n" \
"v_accvgpr_read_b32 v48, a80\n" \
"v_mfma_f32_16x16x32_bf16 v[100:103], a[240:243], a[204:207], v[100:103]\n" \
"ds_read_b128 a[140:143], v28 offset:6144\n" \
"v_accvgpr_read_b32 v49, a81\n" \
"v_mfma_f32_16x16x32_bf16 v[52:55], a[176:179], a[128:131], v[52:55]\n" \
"ds_read_b128 a[204:207], v29 offset:6144\n" \
"v_accvgpr_read_b32 v50, a82\n" \
"v_mfma_f32_16x16x32_bf16 v[52:55], a[240:243], a[192:195], v[52:55]\n" \
"ds_read_b64_tr_b16 a[234:235], v34 offset:4608\n" \
"v_accvgpr_read_b32 v51, a83\n" \
"v_mfma_f32_16x16x32_bf16 v[56:59], a[180:183], a[128:131], v[56:59]\n" \
"v_accvgpr_read_b32 v252, a68\n" \
"v_accvgpr_read_b32 v253, a69\n" \
"v_mfma_f32_16x16x32_bf16 v[56:59], a[244:247], a[192:195], v[56:59]\n" \
"v_accvgpr_read_b32 v254, a70\n" \
"v_accvgpr_read_b32 v255, a71\n" \
"v_mfma_f32_16x16x32_bf16 v[60:63], a[184:187], a[128:131], v[60:63]\n" \
"v_lshrrev_b32_e64 v24, 9, v20\n" \
"v_and_b32_e32 v26, 0x1ff, v20\n" \
"v_mfma_f32_16x16x32_bf16 v[60:63], a[248:251], a[192:195], v[60:63]\n" \
"v_lshl_add_u32 v24, v24, 3, v26\n" \
"v_lshrrev_b32_e64 v25, 8, v20\n" \
"v_mfma_f32_16x16x32_bf16 v[64:67], a[188:191], a[128:131], v[64:67]\n" \
"v_mul_u32_u24_e32 v25, 0x24700, v25\n" \
"v_add_u32_e64 v25, v25, v20\n" \
"v_mfma_f32_16x16x32_bf16 v[64:67], a[252:255], a[192:195], v[64:67]\n" \
"ds_read_b128 a[128:131], v28 offset:0\n" \
"v_lshrrev_b32_e64 v26, 2, v20\n" \
"v_mfma_f32_16x16x32_bf16 v[80:83], a[188:191], a[132:135], v[80:83]\n" \
"ds_read_b128 a[192:195], v29 offset:0\n" \
"v_add_u32_e64 v26, s45, v26\n" \
"v_mfma_f32_16x16x32_bf16 v[80:83], a[252:255], a[196:199], v[80:83]\n" \
"v_add_u32_e32 v27, s46, v20\n" \
"v_add_u32_e32 v21, s43, v27\n" \
"v_mfma_f32_16x16x32_bf16 v[76:79], a[184:187], a[132:135], v[76:79]\n" \
"v_mfma_f32_16x16x32_bf16 v[76:79], a[248:251], a[196:199], v[76:79]\n" \
"v_mfma_f32_16x16x32_bf16 v[72:75], a[180:183], a[132:135], v[72:75]\n" \
"v_mfma_f32_16x16x32_bf16 v[72:75], a[244:247], a[196:199], v[72:75]\n" \
"v_mfma_f32_16x16x32_bf16 v[68:71], a[176:179], a[132:135], v[68:71]\n" \
"v_mfma_f32_16x16x32_bf16 v[68:71], a[240:243], a[196:199], v[68:71]\n" \
"ds_read_b128 a[132:135], v28 offset:2048\n" \
"ds_read_b128 a[196:199], v29 offset:2048\n" \
"s_waitcnt lgkmcnt(15)\n" \
"s_nop 0\n" \
"v_mfma_f32_16x16x32_bf16 v[184:187], a[160:163], a[144:147], v[184:187]\n" \
"v_bfe_u32 v44, v20, 2, 5\n" \
"ds_read_b64_tr_b16 a[176:177], v36 offset:0\n" \
"v_mfma_f32_16x16x32_bf16 v[184:187], a[224:227], a[208:211], v[184:187]\n" \
"buffer_load_dword v44, v44, s[80:83], s38 offen offset:0 sc1\n" \
"v_mfma_f32_16x16x32_bf16 v[188:191], a[164:167], a[144:147], v[188:191]\n" \
"ds_read_b64_tr_b16 a[178:179], v36 offset:1024\n" \
"v_mfma_f32_16x16x32_bf16 v[188:191], a[228:231], a[208:211], v[188:191]\n" \
"ds_read_b64_tr_b16 a[240:241], v36 offset:8192\n" \
"v_mfma_f32_16x16x32_bf16 v[196:199], a[172:175], a[144:147], v[196:199]\n" \
"ds_read_b64_tr_b16 a[242:243], v36 offset:9216\n" \
"s_waitcnt lgkmcnt(15)\n" \
"v_mfma_f32_16x16x32_bf16 v[196:199], a[236:239], a[208:211], v[196:199]\n" \
"s_waitcnt lgkmcnt(12)\n" \
"v_mfma_f32_16x16x32_bf16 v[192:195], a[144:147], a[168:171], v[192:195]\n" \
"ds_read_b64_tr_b16 a[180:181], v37 offset:0\n" \
"s_waitcnt lgkmcnt(9)\n" \
"s_nop 0\n" \
"v_mfma_f32_16x16x32_bf16 v[192:195], a[208:211], a[232:235], v[192:195]\n" \
"v_mfma_f32_16x16x32_bf16 v[208:211], a[148:151], a[168:171], v[208:211]\n" \
"ds_read_b64_tr_b16 a[182:183], v37 offset:1024\n" \
"v_mfma_f32_16x16x32_bf16 v[208:211], a[212:215], a[232:235], v[208:211]\n" \
"ds_read_b64_tr_b16 a[244:245], v37 offset:8192\n" \
"v_mfma_f32_16x16x32_bf16 v[212:215], a[172:175], a[148:151], v[212:215]\n" \
"v_mfma_f32_16x16x32_bf16 v[212:215], a[236:239], a[212:215], v[212:215]\n" \
"ds_read_b64_tr_b16 a[246:247], v37 offset:9216\n" \
"v_cvt_pk_bf16_f32 v188, v188, v189\n" \
"v_mfma_f32_16x16x32_bf16 v[204:207], a[164:167], a[148:151], v[204:207]\n" \
"v_cvt_pk_bf16_f32 v189, v190, v191\n" \
"v_cvt_pk_bf16_f32 v190, v198, v199\n" \
"v_mfma_f32_16x16x32_bf16 v[204:207], a[228:231], a[212:215], v[204:207]\n" \
"ds_read_b64_tr_b16 a[184:185], v38 offset:0\n" \
"v_mov_b32_e32 v191, 0x3f80\n" \
"v_mfma_f32_16x16x32_bf16 v[200:203], a[160:163], a[148:151], v[200:203]\n" \
"ds_read_b64_tr_b16 a[186:187], v38 offset:1024\n" \
"v_cvt_pk_bf16_f32 v184, v184, v185\n" \
"v_mfma_f32_16x16x32_bf16 v[200:203], a[224:227], a[212:215], v[200:203]\n" \
"v_cvt_pk_bf16_f32 v185, v186, v187\n" \
"v_cvt_pk_bf16_f32 v186, v196, v197\n" \
"v_mfma_f32_16x16x32_bf16 v[216:219], a[160:163], a[152:155], v[216:219]\n" \
"ds_read_b64_tr_b16 a[248:249], v38 offset:8192\n" \
"v_mov_b32_e64 v187, 0\n" \
"v_mfma_f32_16x16x32_bf16 v[216:219], a[224:227], a[216:219], v[216:219]\n" \
"ds_read_b64_tr_b16 a[250:251], v38 offset:9216\n" \
"v_mfma_f32_16x16x32_bf16 v[220:223], a[164:167], a[152:155], v[220:223]\n" \
"v_mfma_f32_16x16x32_bf16 v[220:223], a[228:231], a[216:219], v[220:223]\n" \
"ds_read_b64_tr_b16 a[188:189], v39 offset:0\n" \
"v_cvt_pk_bf16_f32 v204, v204, v205\n" \
"v_mfma_f32_16x16x32_bf16 v[228:231], a[172:175], a[152:155], v[228:231]\n" \
"v_cvt_pk_bf16_f32 v205, v206, v207\n" \
"v_cvt_pk_bf16_f32 v206, v214, v215\n" \
"v_mfma_f32_16x16x32_bf16 v[228:231], a[236:239], a[216:219], v[228:231]\n" \
"ds_read_b64_tr_b16 a[190:191], v39 offset:1024\n" \
"v_mov_b32_e32 v207, 0x3f80\n" \
"v_mfma_f32_16x16x32_bf16 v[224:227], a[152:155], a[168:171], v[224:227]\n" \
"ds_read_b64_tr_b16 a[252:253], v39 offset:8192\n" \
"v_cvt_pk_bf16_f32 v200, v200, v201\n" \
"v_mfma_f32_16x16x32_bf16 v[224:227], a[216:219], a[232:235], v[224:227]\n" \
"v_cvt_pk_bf16_f32 v201, v202, v203\n" \
"v_cvt_pk_bf16_f32 v202, v212, v213\n" \
"v_mfma_f32_16x16x32_bf16 v[240:243], a[156:159], a[168:171], v[240:243]\n" \
"ds_read_b64_tr_b16 a[254:255], v39 offset:9216\n" \
"v_mov_b32_e64 v203, 0\n" \
"v_mfma_f32_16x16x32_bf16 v[240:243], a[220:223], a[232:235], v[240:243]\n" \
"v_mfma_f32_16x16x32_bf16 v[244:247], a[172:175], a[156:159], v[244:247]\n" \
"v_mfma_f32_16x16x32_bf16 v[244:247], a[236:239], a[220:223], v[244:247]\n" \
"v_cvt_pk_bf16_f32 v220, v220, v221\n" \
"v_cvt_pk_bf16_f32 v221, v222, v223\n" \
"v_mfma_f32_16x16x32_bf16 v[236:239], a[164:167], a[156:159], v[236:239]\n" \
"v_cvt_pk_bf16_f32 v222, v230, v231\n" \
"v_mov_b32_e32 v223, 0x3f80\n" \
"v_mfma_f32_16x16x32_bf16 v[236:239], a[228:231], a[220:223], v[236:239]\n" \
"v_cvt_pk_bf16_f32 v216, v216, v217\n" \
"v_cvt_pk_bf16_f32 v217, v218, v219\n" \
"v_mfma_f32_16x16x32_bf16 v[232:235], a[160:163], a[156:159], v[232:235]\n" \
"v_cvt_pk_bf16_f32 v218, v228, v229\n" \
"v_mov_b32_e64 v219, 0\n" \
"v_mfma_f32_16x16x32_bf16 v[232:235], a[224:227], a[220:223], v[232:235]\n" \
"v_mfma_f32_16x16x32_bf16 v[180:183], a[164:167], a[140:143], v[180:183]\n" \
"v_mfma_f32_16x16x32_bf16 v[180:183], a[228:231], a[204:207], v[180:183]\n" \
"v_mfma_f32_16x16x32_bf16 v[48:51], a[172:175], a[140:143], v[48:51]\n" \
"v_mfma_f32_16x16x32_bf16 v[48:51], a[236:239], a[204:207], v[48:51]\n" \
"v_cvt_pk_bf16_f32 v236, v236, v237\n" \
"v_cvt_pk_bf16_f32 v237, v238, v239\n" \
"v_mfma_f32_16x16x32_bf16 v[252:255], a[140:143], a[168:171], v[252:255]\n" \
"v_cvt_pk_bf16_f32 v238, v246, v247\n" \
"v_mov_b32_e32 v239, 0x3f80\n" \
"v_mfma_f32_16x16x32_bf16 v[252:255], a[204:207], a[232:235], v[252:255]\n" \
"v_cvt_pk_bf16_f32 v232, v232, v233\n" \
"v_cvt_pk_bf16_f32 v233, v234, v235\n" \
"s_waitcnt vmcnt(0) lgkmcnt(0)\n" \
"s_barrier\n" \
"v_mov_b32_e32 v45, s62\n" \
"s_mov_b64 exec, 1\n" \
"buffer_store_dword v45, off, s[80:83], s37 offset:32 sc1\n" \
"s_mov_b64 exec, -1\n" \
"v_cmp_ne_u32_e32 vcc, s62, v44\n" \
"s_nop 4\n" \
"s_cbranch_vccnz L_poll161_%=\n" \
"L_pollr161_%=:\n" \
"v_mfma_f32_16x16x32_bf16 v[116:119], a[176:179], a[144:147], v[116:119]\n" \
"s_mov_b32 s73, s50\n" \
"s_sub_u32 m0, s31, 0x10000\n" \
"v_mfma_f32_16x16x32_bf16 v[116:119], a[240:243], a[208:211], v[116:119]\n" \
"buffer_load_dwordx4 v20, s[80:83], s73 offen offset:0 sc1 lds\n" \
"v_mfma_f32_16x16x32_bf16 v[120:123], a[180:183], a[144:147], v[120:123]\n" \
"s_add_u32 s74, s50, 0x4000\n" \
"s_add_u32 s75, s50, 0x8000\n" \
"v_mfma_f32_16x16x32_bf16 v[120:123], a[244:247], a[208:211], v[120:123]\n" \
"s_mov_b32 s70, 0x3e0293ee\n" \
"s_mov_b32 s76, s52\n" \
"v_mfma_f32_16x16x32_bf16 v[124:127], a[184:187], a[144:147], v[124:127]\n" \
"v_mul_f32_e32 v116, s70, v116\n" \
"v_mul_f32_e32 v117, s70, v117\n" \
"v_mfma_f32_16x16x32_bf16 v[124:127], a[248:251], a[208:211], v[124:127]\n" \
"buffer_load_dwordx4 v20, s[80:83], s73 offen offset:1024 sc1 lds\n" \
"v_mfma_f32_16x16x32_bf16 v[128:131], a[188:191], a[144:147], v[128:131]\n" \
"v_mul_f32_e32 v118, s70, v118\n" \
"v_mul_f32_e32 v119, s70, v119\n" \
"v_mfma_f32_16x16x32_bf16 v[128:131], a[252:255], a[208:211], v[128:131]\n" \
"v_cvt_pk_bf16_f32 v116, v116, v117\n" \
"v_mul_f32_e64 v120, s70, v120\n" \
"v_mfma_f32_16x16x32_bf16 v[144:147], a[188:191], a[148:151], v[144:147]\n" \
"v_mul_f32_e32 v124, s70, v124\n" \
"v_mul_f32_e32 v125, s70, v125\n" \
"v_mfma_f32_16x16x32_bf16 v[144:147], a[252:255], a[212:215], v[144:147]\n" \
"buffer_load_dwordx4 v20, s[80:83], s73 offen offset:2048 sc1 lds\n" \
"v_mfma_f32_16x16x32_bf16 v[140:143], a[184:187], a[148:151], v[140:143]\n" \
"v_mul_f32_e32 v126, s70, v126\n" \
"v_mul_f32_e32 v127, s70, v127\n" \
"v_mfma_f32_16x16x32_bf16 v[140:143], a[248:251], a[212:215], v[140:143]\n" \
"v_cvt_pk_bf16_f32 v124, v124, v125\n" \
"v_mul_f32_e64 v121, s70, v121\n" \
"v_mfma_f32_16x16x32_bf16 v[136:139], a[180:183], a[148:151], v[136:139]\n" \
".fill (L_pb + 77536 - .) / 4, 4, 0xBF800000\n" \
"L_pp19:\n" \
"s_cbranch_execz L_pret\n" \
"v_mul_f32_e32 v122, s70, v122\n" \
"v_mul_f32_e64 v123, s70, v123\n" \
"v_mfma_f32_16x16x32_bf16 v[136:139], a[244:247], a[212:215], v[136:139]\n" \
"buffer_load_dwordx4 v20, s[80:83], s73 offen offset:3072 sc1 lds\n" \
"v_mfma_f32_16x16x32_bf16 v[132:135], a[176:179], a[148:151], v[132:135]\n" \
"s_sub_u32 m0, s31, s51\n" \
"v_cvt_pk_bf16_f32 v117, v118, v119\n" \
"v_mfma_f32_16x16x32_bf16 v[132:135], a[240:243], a[212:215], v[132:135]\n" \
"v_mul_f32_e32 v128, s70, v128\n" \
"v_mul_f32_e64 v129, s70, v129\n" \
"v_mfma_f32_16x16x32_bf16 v[148:151], a[176:179], a[152:155], v[148:151]\n" \
"v_mul_f32_e32 v130, s70, v130\n" \
"v_mul_f32_e32 v131, s70, v131\n" \
"v_mfma_f32_16x16x32_bf16 v[148:151], a[240:243], a[216:219], v[148:151]\n" \
"buffer_load_dwordx4 v20, s[80:83], s74 offen offset:0 sc1 lds\n" \
"v_mfma_f32_16x16x32_bf16 v[152:155], a[180:183], a[152:155], v[152:155]\n" \
"v_cvt_pk_bf16_f32 v125, v126, v127\n" \
"v_cvt_pk_bf16_f32 v118, v120, v121\n" \
"v_mfma_f32_16x16x32_bf16 v[152:155], a[244:247], a[216:219], v[152:155]\n" \
"v_cvt_pk_bf16_f32 v119, v122, v123\n" \
"v_cvt_pk_bf16_f32 v126, v128, v129\n" \
"v_mfma_f32_16x16x32_bf16 v[156:159], a[184:187], a[152:155], v[156:159]\n" \
"v_cvt_pk_bf16_f32 v127, v130, v131\n" \
"v_mul_f32_e64 v132, s70, v132\n" \
"v_mfma_f32_16x16x32_bf16 v[156:159], a[248:251], a[216:219], v[156:159]\n" \
"buffer_load_dwordx4 v20, s[80:83], s74 offen offset:1024 sc1 lds\n" \
"v_mfma_f32_16x16x32_bf16 v[160:163], a[188:191], a[152:155], v[160:163]\n" \
"ds_write_b128 v21, v[116:119] offset:16384\n" \
"v_mfma_f32_16x16x32_bf16 v[160:163], a[252:255], a[216:219], v[160:163]\n" \
"v_mul_f32_e32 v133, s70, v133\n" \
"v_mul_f32_e32 v140, s70, v140\n" \
"v_mfma_f32_16x16x32_bf16 v[176:179], a[188:191], a[156:159], v[176:179]\n" \
"v_mul_f32_e32 v141, s70, v141\n" \
"v_mul_f32_e32 v134, s70, v134\n" \
"v_mfma_f32_16x16x32_bf16 v[176:179], a[252:255], a[220:223], v[176:179]\n" \
"buffer_load_dwordx4 v20, s[80:83], s74 offen offset:2048 sc1 lds\n" \
"v_mfma_f32_16x16x32_bf16 v[172:175], a[184:187], a[156:159], v[172:175]\n" \
"ds_write_b128 v21, v[124:127] offset:17408\n" \
"v_mfma_f32_16x16x32_bf16 v[172:175], a[248:251], a[220:223], v[172:175]\n" \
"v_mul_f32_e64 v135, s70, v135\n" \
"v_cvt_pk_bf16_f32 v132, v132, v133\n" \
"v_mfma_f32_16x16x32_bf16 v[168:171], a[180:183], a[156:159], v[168:171]\n" \
"v_mul_f32_e32 v142, s70, v142\n" \
"v_mul_f32_e32 v143, s70, v143\n" \
"v_mfma_f32_16x16x32_bf16 v[168:171], a[244:247], a[220:223], v[168:171]\n" \
"buffer_load_dwordx4 v20, s[80:83], s74 offen offset:3072 sc1 lds\n" \
"v_mfma_f32_16x16x32_bf16 v[164:167], a[176:179], a[156:159], v[164:167]\n" \
"s_sub_u32 m0, s31, 0x4000\n" \
"v_cvt_pk_bf16_f32 v140, v140, v141\n" \
"v_mfma_f32_16x16x32_bf16 v[164:167], a[240:243], a[220:223], v[164:167]\n" \
"v_mul_f32_e32 v136, s70, v136\n" \
"v_mul_f32_e32 v137, s70, v137\n" \
"v_mfma_f32_16x16x32_bf16 v[84:87], a[176:179], a[136:139], v[84:87]\n" \
"v_mul_f32_e32 v138, s70, v138\n" \
"v_mul_f32_e32 v139, s70, v139\n" \
"v_mfma_f32_16x16x32_bf16 v[84:87], a[240:243], a[200:203], v[84:87]\n" \
"buffer_load_dwordx4 v20, s[80:83], s75 offen offset:0 sc1 lds\n" \
"v_mfma_f32_16x16x32_bf16 v[88:91], a[180:183], a[136:139], v[88:91]\n" \
"v_cvt_pk_bf16_f32 v133, v134, v135\n" \
"v_mul_f32_e64 v144, s70, v144\n" \
"v_mfma_f32_16x16x32_bf16 v[88:91], a[244:247], a[200:203], v[88:91]\n" \
"v_mul_f32_e32 v145, s70, v145\n" \
"v_mul_f32_e32 v146, s70, v146\n" \
"v_mfma_f32_16x16x32_bf16 v[92:95], a[184:187], a[136:139], v[92:95]\n" \
"v_mul_f32_e64 v147, s70, v147\n" \
"v_cvt_pk_bf16_f32 v141, v142, v143\n" \
"v_mfma_f32_16x16x32_bf16 v[92:95], a[248:251], a[200:203], v[92:95]\n" \
"buffer_load_dwordx4 v20, s[80:83], s75 offen offset:1024 sc1 lds\n" \
"v_mfma_f32_16x16x32_bf16 v[96:99], a[188:191], a[136:139], v[96:99]\n" \
"v_cvt_pk_bf16_f32 v134, v136, v137\n" \
"v_cvt_pk_bf16_f32 v135, v138, v139\n" \
"v_mfma_f32_16x16x32_bf16 v[96:99], a[252:255], a[200:203], v[96:99]\n" \
"v_cvt_pk_bf16_f32 v142, v144, v145\n" \
"v_cvt_pk_bf16_f32 v143, v146, v147\n" \
"v_mfma_f32_16x16x32_bf16 v[112:115], a[188:191], a[140:143], v[112:115]\n" \
"ds_write_b128 v21, v[132:135] offset:20480\n" \
"v_mfma_f32_16x16x32_bf16 v[112:115], a[252:255], a[204:207], v[112:115]\n" \
"buffer_load_dwordx4 v20, s[80:83], s75 offen offset:2048 sc1 lds\n" \
"v_mfma_f32_16x16x32_bf16 v[108:111], a[184:187], a[140:143], v[108:111]\n" \
"v_cvt_pk_bf16_f32 v180, v180, v181\n" \
"v_cvt_pk_bf16_f32 v181, v182, v183\n" \
"v_mfma_f32_16x16x32_bf16 v[108:111], a[248:251], a[204:207], v[108:111]\n" \
"ds_write_b128 v21, v[140:143] offset:21504\n" \
"v_mfma_f32_16x16x32_bf16 v[104:107], a[180:183], a[140:143], v[104:107]\n" \
"v_cvt_pk_bf16_f32 v234, v244, v245\n" \
"v_mov_b32_e64 v235, 0\n" \
"v_mfma_f32_16x16x32_bf16 v[104:107], a[244:247], a[204:207], v[104:107]\n" \
"buffer_load_dwordx4 v20, s[80:83], s75 offen offset:3072 sc1 lds\n" \
"v_mfma_f32_16x16x32_bf16 v[100:103], a[176:179], a[140:143], v[100:103]\n" \
"s_mov_b32 m0, s53\n" \
"s_nop 0\n" \
"v_cvt_pk_bf16_f32 v182, v50, v51\n" \
"v_mfma_f32_16x16x32_bf16 v[100:103], a[240:243], a[204:207], v[100:103]\n" \
"v_mov_b32_e32 v183, 0x3f80\n" \
"v_cvt_pk_bf16_f32 v118, v192, v193\n" \
"v_mfma_f32_16x16x32_bf16 v[52:55], a[176:179], a[128:131], v[52:55]\n" \
"v_cvt_pk_bf16_f32 v119, v194, v195\n" \
"v_cvt_pk_bf16_f32 v120, v208, v209\n" \
"v_mfma_f32_16x16x32_bf16 v[52:55], a[240:243], a[192:195], v[52:55]\n" \
"buffer_load_dwordx4 v20, s[80:83], s76 offen offset:0 sc1 lds\n" \
"v_mfma_f32_16x16x32_bf16 v[56:59], a[180:183], a[128:131], v[56:59]\n" \
"v_cvt_pk_bf16_f32 v121, v210, v211\n" \
"v_cvt_pk_bf16_f32 v122, v224, v225\n" \
"v_mfma_f32_16x16x32_bf16 v[56:59], a[244:247], a[192:195], v[56:59]\n" \
"v_cvt_pk_bf16_f32 v123, v226, v227\n" \
"v_cvt_pk_bf16_f32 v124, v240, v241\n" \
"v_cvt_pk_bf16_f32 v125, v242, v243\n" \
"v_mfma_f32_16x16x32_bf16 v[60:63], a[184:187], a[128:131], v[60:63]\n" \
"v_mfma_f32_16x16x32_bf16 v[192:195], v[188:191], v[184:187], 0\n" \
"buffer_load_dwordx4 v20, s[80:83], s76 offen offset:1024 sc1 lds\n" \
"v_mfma_f32_16x16x32_bf16 v[32:35], v[180:183], v[184:187], 0\n" \
"v_mfma_f32_16x16x32_bf16 v[208:211], v[204:207], v[200:203], 0\n" \
"v_mfma_f32_16x16x32_bf16 v[36:39], v[188:191], v[200:203], 0\n" \
"v_mfma_f32_16x16x32_bf16 v[224:227], v[220:223], v[216:219], 0\n" \
"v_mfma_f32_16x16x32_bf16 v[248:251], v[204:207], v[216:219], 0\n" \
"v_mfma_f32_16x16x32_bf16 v[240:243], v[236:239], v[232:235], 0\n" \
"v_mfma_f32_16x16x32_bf16 v[128:131], v[220:223], v[232:235], 0\n" \
"s_mov_b32 s64, 0xaaaaaaaa\n" \
"s_mov_b32 s65, 0xaaaaaaaa\n" \
"v_mfma_f32_16x16x32_bf16 v[60:63], a[248:251], a[192:195], v[60:63]\n" \
"s_mov_b32 s66, 0xfff0ffff\n" \
"s_mov_b32 s67, 0xf000ff00\n" \
"v_mfma_f32_16x16x32_bf16 v[64:67], a[188:191], a[128:131], v[64:67]\n" \
"s_mov_b32 s68, 0xffc0fffc\n" \
"s_mov_b32 s69, 0xc000fc00\n" \
"v_cndmask_b32_e64 v192, v192, v193, s[64:65]\n" \
"v_cndmask_b32_e64 v32, v32, v33, s[64:65]\n" \
"v_cndmask_b32_e64 v194, v194, v195, s[64:65]\n" \
"v_cndmask_b32_e64 v34, v34, v35, s[64:65]\n" \
"v_cndmask_b32_e64 v208, v208, v209, s[64:65]\n" \
"v_cndmask_b32_e64 v36, v36, v37, s[64:65]\n" \
"v_cndmask_b32_e64 v210, v210, v211, s[64:65]\n" \
"v_cndmask_b32_e64 v38, v38, v39, s[64:65]\n" \
"v_cndmask_b32_e64 v224, v224, v225, s[64:65]\n" \
"v_cndmask_b32_e64 v248, v248, v249, s[64:65]\n" \
"v_cndmask_b32_e64 v226, v226, v227, s[64:65]\n" \
"v_cndmask_b32_e64 v250, v250, v251, s[64:65]\n" \
"v_cndmask_b32_e64 v240, v240, v241, s[64:65]\n" \
"v_cndmask_b32_e64 v128, v128, v129, s[64:65]\n" \
"v_cndmask_b32_e64 v242, v242, v243, s[64:65]\n" \
"v_cndmask_b32_e64 v130, v130, v131, s[64:65]\n" \
"v_cndmask_b32_e64 v192, v32, v192, s[66:67]\n" \
"v_cndmask_b32_e64 v194, v34, v194, s[68:69]\n" \
"v_cndmask_b32_e64 v208, v36, v208, s[66:67]\n" \
"v_cndmask_b32_e64 v210, v38, v210, s[68:69]\n" \
"v_cndmask_b32_e64 v224, v248, v224, s[66:67]\n" \
"v_cndmask_b32_e64 v226, v250, v226, s[68:69]\n" \
"v_cndmask_b32_e64 v240, v128, v240, s[66:67]\n" \
"v_cndmask_b32_e64 v242, v130, v242, s[68:69]\n" \
"v_mfma_f32_16x16x32_bf16 v[64:67], a[252:255], a[192:195], v[64:67]\n" \
"v_max_f32_e32 v193, v192, v194\n" \
"v_max_f32_e32 v209, v208, v210\n" \
"v_max_f32_e32 v225, v224, v226\n" \
"v_max_f32_e32 v241, v240, v242\n" \
"v_mul_f32_e32 v148, s70, v148\n" \
"v_permlane32_swap_b32_e32 v193, v225\n" \
"v_permlane32_swap_b32_e32 v209, v241\n" \
"v_mul_f32_e32 v149, s70, v149\n" \
"v_mul_f32_e32 v156, s70, v156\n" \
"v_mul_f32_e32 v157, s70, v157\n" \
"v_permlane32_swap_b32_e32 v196, v228\n" \
"v_permlane32_swap_b32_e32 v212, v244\n" \
"v_max_f32_e32 v193, v193, v225\n" \
"v_max_f32_e32 v209, v209, v241\n" \
"v_mul_f32_e32 v150, s70, v150\n" \
"v_mul_f32_e32 v151, s70, v151\n" \
"v_cvt_pk_bf16_f32 v148, v148, v149\n" \
"v_mul_f32_e32 v158, s70, v158\n" \
"v_mul_f32_e32 v159, s70, v159\n" \
"v_cvt_pk_bf16_f32 v156, v156, v157\n" \
"v_permlane16_swap_b32_e32 v228, v244\n" \
"v_permlane16_swap_b32_e32 v193, v209\n" \
"v_mfma_f32_16x16x32_bf16 v[80:83], a[188:191], a[132:135], v[80:83]\n" \
"s_mov_b32 s72, 0x3eae1a93\n" \
"v_mul_f32_e32 v152, s70, v152\n" \
"v_mul_f32_e32 v153, s70, v153\n" \
"v_mul_f32_e32 v154, s70, v154\n" \
"v_mul_f32_e32 v155, s70, v155\n" \
"v_cvt_pk_bf16_f32 v149, v150, v151\n" \
"v_mul_f32_e32 v160, s70, v160\n" \
"v_mul_f32_e32 v161, s70, v161\n" \
"v_mul_f32_e32 v162, s70, v162\n" \
"v_mul_f32_e32 v163, s70, v163\n" \
"v_cvt_pk_bf16_f32 v157, v158, v159\n" \
"v_mul_f32_e32 v45, 0xbfb8aa3b, v228\n" \
"v_mfma_f32_16x16x16_bf16 v[48:51], v[182:183], v[46:47], 0\n" \
"v_mfma_f32_16x16x16_bf16 v[196:199], v[190:191], v[46:47], 0\n" \
"v_mfma_f32_16x16x16_bf16 v[212:215], v[206:207], v[46:47], 0\n" \
"v_max_f32_e64 v193, v193, v209\n" \
"v_cvt_pk_bf16_f32 v150, v152, v153\n" \
"v_cvt_pk_bf16_f32 v151, v154, v155\n" \
"v_cvt_pk_bf16_f32 v158, v160, v161\n" \
"v_cvt_pk_bf16_f32 v159, v162, v163\n" \
"v_mfma_f32_16x16x16_bf16 v[228:231], v[222:223], v[46:47], 0\n" \
"v_mul_f32_e32 v193, s72, v193\n" \
"v_mov_b32_e32 v209, v193\n" \
"ds_write_b128 v21, v[148:151] offset:24576\n" \
"v_mfma_f32_16x16x16_bf16 v[244:247], v[238:239], v[46:47], 0\n" \
"v_permlane16_swap_b32_e32 v193, v209\n" \
"s_mov_b32 s71, 0x3e0293ee\n" \
"v_mfma_f32_16x16x32_bf16 v[80:83], a[252:255], a[196:199], v[80:83]\n" \
"v_mov_b32_e32 v225, v193\n" \
"v_mov_b32_e32 v241, v209\n" \
"v_mul_f32_e32 v164, s70, v164\n" \
"v_mul_f32_e64 v165, s70, v165\n" \
"ds_write_b128 v21, v[156:159] offset:25600\n" \
"v_mul_f32_e32 v172, s70, v172\n" \
"v_mul_f32_e32 v173, s70, v173\n" \
"v_pk_mul_f32 v[84:85], v[84:85], s[70:71]\n" \
"v_pk_mul_f32 v[92:93], v[92:93], s[70:71]\n" \
"v_pk_mul_f32 v[100:101], v[100:101], s[70:71]\n" \
"v_pk_mul_f32 v[108:109], v[108:109], s[70:71]\n" \
"v_pk_mul_f32 v[52:53], v[52:53], s[70:71]\n" \
"v_pk_mul_f32 v[60:61], v[60:61], s[70:71]\n" \
"v_permlane32_swap_b32_e32 v193, v225\n" \
"v_permlane32_swap_b32_e32 v209, v241\n" \
"v_mul_f32_e32 v166, s70, v166\n" \
"v_mul_f32_e32 v167, s70, v167\n" \
"v_cvt_pk_bf16_f32 v164, v164, v165\n" \
"v_mul_f32_e32 v174, s70, v174\n" \
"v_mul_f32_e32 v175, s70, v175\n" \
"v_cvt_pk_bf16_f32 v172, v172, v173\n" \
"v_pk_mul_f32 v[86:87], v[86:87], s[70:71]\n" \
"v_cvt_pk_bf16_f32 v84, v84, v85\n" \
"v_pk_mul_f32 v[94:95], v[94:95], s[70:71]\n" \
"v_cvt_pk_bf16_f32 v92, v92, v93\n" \
"v_pk_mul_f32 v[102:103], v[102:103], s[70:71]\n" \
"v_cvt_pk_bf16_f32 v100, v100, v101\n" \
"v_pk_mul_f32 v[110:111], v[110:111], s[70:71]\n" \
"v_cvt_pk_bf16_f32 v108, v108, v109\n" \
"v_pk_mul_f32 v[54:55], v[54:55], s[70:71]\n" \
"v_cvt_pk_bf16_f32 v52, v52, v53\n" \
"v_pk_mul_f32 v[62:63], v[62:63], s[70:71]\n" \
"v_cvt_pk_bf16_f32 v60, v60, v61\n" \
"v_fma_f32 v192, v192, s72, -v193\n" \
"v_fma_f32 v194, v194, s72, -v193\n" \
"v_fma_f32 v208, v208, s72, -v209\n" \
"v_fma_f32 v210, v210, s72, -v209\n" \
"v_mul_f32_e32 v168, s70, v168\n" \
"v_mul_f32_e32 v169, s70, v169\n" \
"v_mul_f32_e32 v170, s70, v170\n" \
"v_mul_f32_e32 v171, s70, v171\n" \
"v_cvt_pk_bf16_f32 v165, v166, v167\n" \
"v_mul_f32_e32 v176, s70, v176\n" \
"v_mul_f32_e32 v177, s70, v177\n" \
"v_mul_f32_e32 v178, s70, v178\n" \
"v_mul_f32_e32 v179, s70, v179\n" \
"v_cvt_pk_bf16_f32 v173, v174, v175\n" \
"v_pk_mul_f32 v[88:89], v[88:89], s[70:71]\n" \
"v_pk_mul_f32 v[90:91], v[90:91], s[70:71]\n" \
"v_cvt_pk_bf16_f32 v85, v86, v87\n" \
"v_pk_mul_f32 v[96:97], v[96:97], s[70:71]\n" \
"v_pk_mul_f32 v[98:99], v[98:99], s[70:71]\n" \
"v_cvt_pk_bf16_f32 v93, v94, v95\n" \
"v_pk_mul_f32 v[104:105], v[104:105], s[70:71]\n" \
"v_pk_mul_f32 v[106:107], v[106:107], s[70:71]\n" \
"v_cvt_pk_bf16_f32 v101, v102, v103\n" \
"v_pk_mul_f32 v[112:113], v[112:113], s[70:71]\n" \
"v_pk_mul_f32 v[114:115], v[114:115], s[70:71]\n" \
"v_cvt_pk_bf16_f32 v109, v110, v111\n" \
"v_pk_mul_f32 v[56:57], v[56:57], s[70:71]\n" \
"v_pk_mul_f32 v[58:59], v[58:59], s[70:71]\n" \
"v_cvt_pk_bf16_f32 v53, v54, v55\n" \
"v_pk_mul_f32 v[64:65], v[64:65], s[70:71]\n" \
"v_pk_mul_f32 v[66:67], v[66:67], s[70:71]\n" \
"v_cvt_pk_bf16_f32 v61, v62, v63\n" \
"v_pk_mul_f32 v[80:81], v[80:81], s[70:71]\n" \
"v_pk_mul_f32 v[82:83], v[82:83], s[70:71]\n" \
"v_exp_f32_e32 v192, v192\n" \
"v_exp_f32_e32 v194, v194\n" \
"v_exp_f32_e32 v208, v208\n" \
"v_exp_f32_e32 v210, v210\n" \
"v_fma_f32 v224, v224, s72, -v225\n" \
"v_fma_f32 v226, v226, s72, -v225\n" \
"v_mfma_f32_16x16x32_bf16 v[76:79], a[184:187], a[132:135], v[76:79]\n" \
"v_cvt_pk_bf16_f32 v166, v168, v169\n" \
"v_cvt_pk_bf16_f32 v167, v170, v171\n" \
"v_cvt_pk_bf16_f32 v174, v176, v177\n" \
"v_cvt_pk_bf16_f32 v175, v178, v179\n" \
"v_cvt_pk_bf16_f32 v86, v88, v89\n" \
"v_cvt_pk_bf16_f32 v87, v90, v91\n" \
"v_cvt_pk_bf16_f32 v94, v96, v97\n" \
"v_cvt_pk_bf16_f32 v95, v98, v99\n" \
"v_cvt_pk_bf16_f32 v102, v104, v105\n" \
"v_cvt_pk_bf16_f32 v103, v106, v107\n" \
"v_cvt_pk_bf16_f32 v110, v112, v113\n" \
"v_cvt_pk_bf16_f32 v111, v114, v115\n" \
"v_cvt_pk_bf16_f32 v54, v56, v57\n" \
"v_cvt_pk_bf16_f32 v55, v58, v59\n" \
"v_cvt_pk_bf16_f32 v62, v64, v65\n" \
"v_cvt_pk_bf16_f32 v63, v66, v67\n" \
"v_exp_f32_e32 v224, v224\n" \
"v_exp_f32_e32 v226, v226\n" \
"v_fma_f32 v240, v240, s72, -v241\n" \
"v_fma_f32 v242, v242, s72, -v241\n" \
"ds_write_b128 v21, v[164:167] offset:28672\n" \
"v_cvt_pk_bf16_f32 v192, v192, v194\n" \
"v_cvt_pk_bf16_f32 v208, v208, v210\n" \
"v_exp_f32_e32 v240, v240\n" \
"v_exp_f32_e32 v242, v242\n" \
"v_cvt_pk_bf16_f32 v132, v48, v49\n" \
"v_cvt_pk_bf16_f32 v133, v50, v51\n" \
"v_cvt_pk_bf16_f32 v134, v196, v197\n" \
"v_cvt_pk_bf16_f32 v135, v198, v199\n" \
"ds_write_b128 v21, v[172:175] offset:29696\n" \
"v_cvt_pk_bf16_f32 v136, v212, v213\n" \
"v_cvt_pk_bf16_f32 v137, v214, v215\n" \
"v_perm_b32 v184, v192, v192, v40\n" \
"v_perm_b32 v185, v192, v192, v41\n" \
"v_perm_b32 v186, v192, v192, v42\n" \
"v_perm_b32 v187, v192, v192, v43\n" \
"v_perm_b32 v200, v208, v208, v40\n" \
"v_perm_b32 v201, v208, v208, v41\n" \
"v_perm_b32 v202, v208, v208, v42\n" \
"v_perm_b32 v203, v208, v208, v43\n" \
"ds_write_b128 v27, v[84:87] offset:8192\n" \
"v_cvt_pk_bf16_f32 v224, v224, v226\n" \
"v_cvt_pk_bf16_f32 v138, v228, v229\n" \
"v_cvt_pk_bf16_f32 v139, v230, v231\n" \
"v_mfma_f32_16x16x32_bf16 v[32:35], v[132:135], v[184:187], 0\n" \
"v_mfma_f32_16x16x32_bf16 v[36:39], v[134:137], v[200:203], 0\n" \
"v_perm_b32 v216, v224, v224, v40\n" \
"v_perm_b32 v217, v224, v224, v41\n" \
"ds_write_b128 v27, v[92:95] offset:9216\n" \
"v_perm_b32 v218, v224, v224, v42\n" \
"v_perm_b32 v219, v224, v224, v43\n" \
"v_cvt_pk_bf16_f32 v240, v240, v242\n" \
"v_mfma_f32_16x16x32_bf16 v[76:79], a[248:251], a[196:199], v[76:79]\n" \
"v_cvt_pk_bf16_f32 v140, v244, v245\n" \
"v_cvt_pk_bf16_f32 v141, v246, v247\n" \
"v_mfma_f32_16x16x32_bf16 v[248:251], v[136:139], v[216:219], 0\n" \
"v_perm_b32 v232, v240, v240, v40\n" \
"v_perm_b32 v233, v240, v240, v41\n" \
"ds_write_b128 v27, v[100:103] offset:12288\n" \
"v_perm_b32 v234, v240, v240, v42\n" \
"v_perm_b32 v235, v240, v240, v43\n" \
"v_pk_mul_f32 v[76:77], v[76:77], s[70:71]\n" \
"v_pk_mul_f32 v[78:79], v[78:79], s[70:71]\n" \
"v_cvt_pk_bf16_f32 v76, v76, v77\n" \
"v_cvt_pk_bf16_f32 v77, v78, v79\n" \
"v_cvt_pk_bf16_f32 v78, v80, v81\n" \
"v_cvt_pk_bf16_f32 v79, v82, v83\n" \
"v_mfma_f32_16x16x32_bf16 v[128:131], v[138:141], v[232:235], 0\n" \
"ds_write_b128 v27, v[108:111] offset:13312\n" \
"v_mfma_f32_16x16x32_bf16 v[72:75], a[180:183], a[132:135], v[72:75]\n" \
"v_cvt_pk_bf16_f32 v116, v252, v253\n" \
"v_cvt_pk_bf16_f32 v117, v254, v255\n" \
"v_mfma_f32_16x16x32_bf16 v[72:75], a[244:247], a[196:199], v[72:75]\n" \
"v_exp_f32_e64 v45, v45\n" \
"v_mfma_f32_16x16x32_bf16 v[192:195], v[116:119], v[184:187], 0\n" \
"ds_write_b128 v27, v[52:55] offset:0\n" \
"v_mfma_f32_16x16x32_bf16 v[208:211], v[118:121], v[200:203], 0\n" \
"v_mfma_f32_16x16x32_bf16 v[224:227], v[120:123], v[216:219], 0\n" \
"v_mfma_f32_16x16x32_bf16 v[240:243], v[122:125], v[232:235], 0\n" \
"ds_write_b128 v27, v[60:63] offset:1024\n" \
"v_mov_b32_dpp v34, v38 quad_perm:[0,1,2,3] row_mask:0x2 bank_mask:0xf\n" \
"v_pk_mul_f32 v[72:73], v[72:73], s[70:71]\n" \
"v_pk_mul_f32 v[74:75], v[74:75], s[70:71]\n" \
"v_mov_b32_dpp v34, v250 quad_perm:[0,1,2,3] row_mask:0x4 bank_mask:0xf\n" \
"v_add_f32_e64 v45, 1.0, v45\n" \
"v_mfma_f32_16x16x32_bf16 v[68:71], a[176:179], a[132:135], v[68:71]\n" \
"v_mov_b32_dpp v34, v130 quad_perm:[0,1,2,3] row_mask:0x8 bank_mask:0xf\n" \
"v_mov_b32_dpp v32, v36 quad_perm:[0,1,2,3] row_mask:0x2 bank_mask:0xf\n" \
"v_rcp_f32_e64 v45, v45\n" \
"ds_write_b128 v27, v[76:79] offset:5120\n" \
"v_mov_b32_dpp v33, v37 quad_perm:[0,1,2,3] row_mask:0x2 bank_mask:0xf\n" \
"v_rcp_f32_e64 v34, v34\n" \
"v_mov_b32_dpp v32, v248 quad_perm:[0,1,2,3] row_mask:0x4 bank_mask:0xf\n" \
"v_mov_b32_dpp v33, v249 quad_perm:[0,1,2,3] row_mask:0x4 bank_mask:0xf\n" \
"v_mul_f32_e64 v34, v45, v34\n" \
"v_mov_b32_dpp v32, v128 quad_perm:[0,1,2,3] row_mask:0x8 bank_mask:0xf\n" \
"v_mov_b32_dpp v33, v129 quad_perm:[0,1,2,3] row_mask:0x8 bank_mask:0xf\n" \
"v_mul_f32_e32 v32, v34, v32\n" \
"v_mul_f32_e32 v33, v34, v33\n" \
"v_mov_b32_e64 v38, v34\n" \
"v_mfma_f32_16x16x32_bf16 v[68:71], a[240:243], a[196:199], v[68:71]\n" \
"v_sub_f32_e32 v44, 1.0, v45\n" \
"v_permlane16_swap_b32_e32 v34, v38\n" \
"v_mov_b32_e32 v250, v34\n" \
"v_mov_b32_e32 v130, v38\n" \
"v_cvt_pk_bf16_f32 v32, v32, v33\n" \
"v_permlane32_swap_b32_e32 v34, v250\n" \
"v_permlane32_swap_b32_e32 v38, v130\n" \
"v_mul_f32_e64 v192, v34, v192\n" \
"v_pk_mul_f32 v[68:69], v[68:69], s[70:71]\n" \
"v_pk_mul_f32 v[70:71], v[70:71], s[70:71]\n" \
"v_cvt_pk_bf16_f32 v68, v68, v69\n" \
"v_cvt_pk_bf16_f32 v69, v70, v71\n" \
"v_cvt_pk_bf16_f32 v70, v72, v73\n" \
"v_cvt_pk_bf16_f32 v71, v74, v75\n" \
"ds_write_b128 v27, v[68:71] offset:4096\n" \
"v_mul_f32_e32 v193, v34, v193\n" \
"v_mul_f32_e32 v208, v38, v208\n" \
"v_mul_f32_e32 v209, v38, v209\n" \
"v_mul_f32_e32 v224, v250, v224\n" \
"v_mul_f32_e32 v225, v250, v225\n" \
"v_mul_f32_e32 v240, v130, v240\n" \
"v_mul_f32_e32 v241, v130, v241\n" \
"v_mul_f32_e32 v194, v34, v194\n" \
"v_mul_f32_e64 v195, v34, v195\n" \
"v_cvt_pk_bf16_f32 v192, v192, v193\n" \
"v_mul_f32_e32 v210, v38, v210\n" \
"v_mul_f32_e32 v211, v38, v211\n" \
"v_cvt_pk_bf16_f32 v208, v208, v209\n" \
"v_mul_f32_e32 v226, v250, v226\n" \
"v_mul_f32_e32 v227, v250, v227\n" \
"v_cvt_pk_bf16_f32 v224, v224, v225\n" \
"v_mul_f32_e32 v242, v130, v242\n" \
"v_mul_f32_e32 v243, v130, v243\n" \
"v_cvt_pk_bf16_f32 v240, v240, v241\n" \
"v_cvt_pk_bf16_f32 v193, v194, v195\n" \
"s_add_u32 s84, s48, 0x0\n" \
"s_nop 0\n" \
"v_cvt_pk_bf16_f32 v209, v210, v211\n" \
"s_add_u32 s85, s48, 0x24800\n" \
"v_cvt_pk_bf16_f32 v225, v226, v227\n" \
"s_add_u32 s86, s48, 0x49000\n" \
"v_cvt_pk_bf16_f32 v241, v242, v243\n" \
"s_add_u32 s87, s48, 0x6d800\n" \
"ds_write_b32 v26, v44\n" \
"buffer_store_dword v32, v25, s[80:83], s49 offen sc1\n" \
"buffer_store_dwordx2 v[192:193], v24, s[80:83], s84 offen sc1\n" \
"buffer_store_dwordx2 v[208:209], v24, s[80:83], s85 offen sc1\n" \
"buffer_store_dwordx2 v[224:225], v24, s[80:83], s86 offen sc1\n" \
"buffer_store_dwordx2 v[240:241], v24, s[80:83], s87 offen sc1\n" \
"s_branch L_end_%=\n" \
"L_poll61_%=:\n" \
"s_mov_b32 s84, 0\n" \
"L_pl61_%=:\n" \
"s_sleep 1\n" \
"v_bfe_u32 v44, v20, 2, 5\n" \
"buffer_load_dword v44, v44, s[80:83], s38 offen offset:128 sc1\n" \
"s_waitcnt vmcnt(0)\n" \
"v_cmp_ne_u32_e32 vcc, s62, v44\n" \
"s_add_u32 s84, s84, 1\n" \
"s_nop 3\n" \
"s_cbranch_vccz L_pollr61_%=\n" \
"s_cmp_gt_u32 s84, 0x400000\n" \
"s_cbranch_scc0 L_pl61_%=\n" \
"s_branch L_pollr61_%=\n" \
"L_poll161_%=:\n" \
"s_mov_b32 s84, 0\n" \
"L_pl161_%=:\n" \
"s_sleep 1\n" \
"v_bfe_u32 v44, v20, 2, 5\n" \
"buffer_load_dword v44, v44, s[80:83], s38 offen offset:0 sc1\n" \
"s_waitcnt vmcnt(0)\n" \
"v_cmp_ne_u32_e32 vcc, s62, v44\n" \
"s_add_u32 s84, s84, 1\n" \
"s_nop 3\n" \
"s_cbranch_vccz L_pollr161_%=\n" \
"s_cmp_gt_u32 s84, 0x400000\n" \
"s_cbranch_scc0 L_pl161_%=\n" \
"s_branch L_pollr161_%=\n" \
"L_end_%=:\n"
#define PROJ_VCLOB "v21","v24","v25","v28","v29","v32","v33","v34","v35","v36","v37","v38","v39","v40","v41","v42","v43","v44","v45","v46","v47","v48","v49","v50","v51","v52","v53","v54","v55","v56","v57","v58","v59","v60","v61","v62","v63","v64","v65","v66","v67","v68","v69","v70","v71","v72","v73","v74","v75","v76","v77","v78","v79","v80","v81","v82","v83","v84","v85","v86","v87","v88","v89","v90","v91","v92","v93","v94","v95","v96","v97","v98","v99","v100","v101","v102","v103","v104","v105","v106","v107","v108","v109","v110","v111","v112","v113","v114","v115","v116","v117","v118","v119","v120","v121","v122","v123","v124","v125","v126","v127","v128","v129","v130","v131","v132","v133","v134","v135","v136","v137","v138","v139","v140","v141","v142","v143","v144","v145","v146","v147","v148","v149","v150","v151","v152","v153","v154","v155","v156","v157","v158","v159","v160","v161","v162","v163","v164","v165","v166","v167","v168","v169","v170","v171","v172","v173","v174","v175","v176","v177","v178","v179","v180","v181","v182","v183","v184","v185","v186","v187","v188","v189","v190","v192","v193","v194","v195","v196","v197","v198","v199","v200","v201","v202","v203","v204","v205","v206","v207","v208","v209","v210","v211","v212","v213","v214","v215","v216","v217","v218","v219","v220","v221","v222","v223","v224","v225","v226","v227","v228","v229","v230","v231","v232","v233","v234","v235","v236","v237","v238","v239","v240","v241","v242","v243","v244","v245","v246","v247","v248","v249","v250","v251","v252","v253","v254","v255"
#define PROJ_SCLOB "s4","s5","s6","s7","s32","s33","s34","s35","s36","s37","s38","s39","s40","s41","s42","s43","s44","s45","s46","s47","s48","s49","s50","s51","s52","s53","s54","s55","s63","s64","s65","s66","s67","s68","s69","s70","s71","s72","s73","s74","s75","s76","s77","s78","s79","s80","s81","s82","s83","s84","s85","s86","s87","s88","s89","s90","s91","s92","s93","s94","s95","s96","s97","s98","s99","s100","s101"

typedef const u16* const __attribute__((address_space(4)))* kaptr;
__device__ __forceinline__ void proj_phase(kaptr& ka, char*& ws, const unsigned wid, u32x4 (&vp)[5], unsigned& vl, u32x4 (&sa)[5], u32x2 s24, u32x2& s28, unsigned tdum, u32x4& srdA, u32x2& sd, unsigned& tag
    ) {
    const unsigned b = blockIdx.x, h = (b & 7) * 4 + ((b >> 3) & 3), mt = b >> 5;
    const unsigned rb = (wid < 2u ? 15u - mt : mt) * 128u;
    const unsigned lane = threadIdx.x & 63u, wc = wid & 1u;
    u32x2 vAc, vB;
#pragma unroll
    for (int par = 0; par < 2; ++par) {
        const unsigned lr = lane >> 3, cc = (lane & 7u) ^ (4u * par + (lr >> 1)), r0 = rb + wc * 64u + lr;
        vAc[par] = r0 * (DC * 2) + cc * 16u;
        const unsigned br = lane >> 4, p16 = lane & 15u, sc = (((p16 >> 1) ^ (br | (par << 2))) << 1) | (p16 & 1u);
        vB[par] = (16u * wid + br) * (NBIG * 2) + h * (HD * 2) + sc * 16u;
    }
    vl = lane * 16u;
    u32x2 s30 = {wid * 8192u, 65536u + wid * 4096u};
    asm volatile(PROJ_ASM
                 : "={s62}"(tag), "={s[56:59]}"(srdA), "={s[60:61]}"(sd), "+{s[8:11]}"(sa[0]), "+{s[12:15]}"(sa[1]), "+{s[16:19]}"(sa[2]), "+{s[20:23]}"(sa[3]), "+{s[24:25]}"(s24), "+{s[28:29]}"(s28), "+{s[30:31]}"(s30),
                   "={v[0:3]}"(vp[0]), "={v[4:7]}"(vp[1]), "={v[8:11]}"(vp[2]), "={v[12:15]}"(vp[3]), "={v[16:19]}"(vp[4]), "+{v[22:23]}"(vAc), "+{v[26:27]}"(vB), "+{v191}"(tdum), "+{s[26:27]}"(ws), "+{s[0:1]}"(ka)
                 : "{s2}"(b), "{s3}"(wid), "{v20}"(vl)
                 : "memory", "m0", "vcc", "scc", PROJ_VCLOB, PROJ_SCLOB, ACLOB_LO, ACLOB_HI);
    sa[4] = (u32x4){s24[0], 0u, 0u, 0u};
}

namespace att {
#define DI __device__ __forceinline__
typedef __bf16 bfx8 __attribute__((ext_vector_type(8)));
DI unsigned rfl(unsigned x) { return __builtin_amdgcn_readfirstlane(x); }
DI f32x4 mfma(u32x4 a, u32x4 b, f32x4 c) { return __builtin_amdgcn_mfma_f32_16x16x32_bf16(__builtin_bit_cast(bfx8, a), __builtin_bit_cast(bfx8, b), c, 0, 0, 0); }
typedef __bf16 bfx2 __attribute__((ext_vector_type(2)));
typedef float f32x2 __attribute__((ext_vector_type(2)));
DI unsigned cvtpk2(float lo, float hi) { f32x2 v = {lo, hi}; bfx2 b = __builtin_convertvector(v, bfx2); return __builtin_bit_cast(unsigned, b); }
DI void att_exact(const u16* __restrict__ x, const u16* __restrict__ wq, char* __restrict__ ws, unsigned h, unsigned r0, float g1) {
    const unsigned lane = lane_id(), g = lane >> 4, c = lane & 15u;
    f32x4 o[8];
#pragma unroll
    for (int n = 0; n < 8; ++n) o[n] = (f32x4){0.f, 0.f, 0.f, 0.f};
#pragma unroll 1
    for (unsigned kk = 0; kk < (unsigned)DM; kk += 32u) {
        const unsigned k = kk + 8u * g;
        const u32x4 xf = *reinterpret_cast<const u32x4*>(x + (r0 + c) * DM + k);
#pragma unroll
        for (int n = 0; n < 8; ++n) {
            const u16* w = wq + k * NBIG + h * HD + n * 16 + c; u32x4 wf;
#pragma unroll
            for (int e = 0; e < 4; ++e) wf[e] = (unsigned)w[2 * e * NBIG] | ((unsigned)w[(2 * e + 1) * NBIG] << 16);
            o[n] = mfma(wf, xf, o[n]);
        }
    }
    u32x4 q[4];
#pragma unroll
    for (int p = 0; p < 4; ++p) {
        const f32x4 a = o[2 * p] * 0.12751743082f, b = o[2 * p + 1] * 0.12751743082f;
        q[p] = (u32x4){cvtpk2(a[0], a[1]), cvtpk2(a[2], a[3]), cvtpk2(b[0], b[1]), cvtpk2(b[2], b[3])};
    }
#pragma unroll
    for (int n = 0; n < 8; ++n) o[n] = (f32x4){0.f, 0.f, 0.f, 0.f};
    const __amdgpu_buffer_rsrc_t srd = __builtin_amdgcn_make_buffer_rsrc((void*)(ws + WS_K + (size_t)h * 1048576), (short)0, 1048576, 0x00020000);
    float m = -3.0e38f, l = 0.f;
#pragma unroll 1
    for (unsigned k0 = 0; k0 <= r0 + 15u; k0 += 32u) {
        const unsigned tb = (k0 >> 6) * 32768u + (k0 & 32u) * 256u;
        f32x4 s[2]; float mx = m;
#pragma unroll
        for (int kt = 0; kt < 2; ++kt) {
            s[kt] = (f32x4){0.f, 0.f, 0.f, 0.f};
#pragma unroll
            for (int p = 0; p < 4; ++p) s[kt] = mfma(__builtin_amdgcn_raw_buffer_load_b128(srd, lane * 16u + (kt * 4 + p) * 1024u, tb, 16), q[p], s[kt]);
#pragma unroll
            for (int e = 0; e < 4; ++e) { if (k0 + 16u * kt + 4u * g + e > r0 + c) s[kt][e] = -3.0e38f; mx = fmaxf(mx, s[kt][e]); }
        }
        mx = fmaxf(mx, __shfl_xor(mx, 16)); mx = fmaxf(mx, __shfl_xor(mx, 32));
        const float al = __builtin_amdgcn_exp2f(m - mx); m = mx;
        u32x4 pf;
#pragma unroll
        for (int e = 0; e < 4; ++e) pf[e] = cvtpk2(__builtin_amdgcn_exp2f(s[e >> 1][2 * (e & 1)] - m), __builtin_amdgcn_exp2f(s[e >> 1][2 * (e & 1) + 1] - m));
        l *= al;
#pragma unroll
        for (int e = 0; e < 4; ++e) l += __uint_as_float(pf[e] << 16) + __uint_as_float(pf[e] & 0xffff0000u);
#pragma unroll
        for (int n = 0; n < 8; ++n) o[n] = mfma(__builtin_amdgcn_raw_buffer_load_b128(srd, lane * 16u + 16384u + n * 1024u, tb, 16), pf, o[n] * al);
    }
    l += __shfl_xor(l, 16); l += __shfl_xor(l, 32);
    const float f = g1 / l;
    const __amdgpu_buffer_rsrc_t srdA = __builtin_amdgcn_make_buffer_rsrc((void*)(ws + WS_AF), (short)0, 128 * KS_TOT * 1024, 0x00020000);
#pragma unroll
    for (int p = 0; p < 4; ++p) {
        const f32x4 a = o[2 * p] * f, b = o[2 * p + 1] * f;
        __builtin_amdgcn_raw_buffer_store_b128((u32x4){cvtpk2(a[0], a[1]), cvtpk2(a[2], a[3]), cvtpk2(b[0], b[1]), cvtpk2(b[2], b[3])}, srdA, lane * 16u + p * 1024u, ((r0 >> 4) * KS_TOT + 18u + 4u * h) * 1024u, 16);
    }
}
}

#define ATT_ASM \
"s_branch L_ago_%=\n" \
".p2align 12\n" \
"L_ab:\n" \
".fill (L_ab + 0 - .) / 4, 4, 0xBF800000\n" \
"L_ap0:\n" \
"s_setpc_b64 s[88:89]\n" \
"L_ago_%=:\n" \
"s_mov_b32 s101, 0\n" \
"s_waitcnt vmcnt(15) lgkmcnt(0)\n" \
"s_barrier\n" \
"v_accvgpr_write_b32 a216, 0\n" \
"v_accvgpr_write_b32 a217, 0\n" \
"v_accvgpr_write_b32 a218, 0\n" \
"v_accvgpr_write_b32 a219, 0\n" \
"v_bfe_u32 v61, v20, 2, 5\n" \
"s_mov_b32 s96, s24\n" \
"v_mfma_f32_32x32x16_bf16 v[128:143], a[216:219], a[216:219], 0\n" \
"buffer_load_dword v57, v61, s[56:59], s96 offen sc1\n" \
"v_and_b32_e32 v63, 0x300, v20\n" \
"ds_read_b128 v[96:99], v63 offset:0\n" \
"v_add_u32_e32 v58, s22, v20\n" \
"ds_read_b128 a[0:3], v58 offset:0\n" \
"v_mfma_f32_32x32x16_bf16 v[144:159], a[216:219], a[216:219], 0\n" \
"ds_read_b128 a[16:19], v58 offset:8192\n" \
"v_add_u32_e32 v59, s23, v20\n" \
"ds_read_b128 a[32:35], v59 offset:0\n" \
"ds_read_b128 a[48:51], v59 offset:8192\n" \
"ds_read_b128 v[100:103], v63 offset:1024\n" \
"ds_read_b128 a[4:7], v58 offset:1024\n" \
"v_mfma_f32_32x32x16_bf16 v[160:175], a[216:219], a[216:219], 0\n" \
"ds_read_b128 a[20:23], v58 offset:9216\n" \
"ds_read_b128 a[36:39], v59 offset:1024\n" \
"ds_read_b128 a[52:55], v59 offset:9216\n" \
"s_sub_u32 s67, s12, 0xc000\n" \
"ds_read_b128 v[104:107], v63 offset:2048\n" \
"s_mov_b32 s65, s12\n" \
"v_mfma_f32_32x32x16_bf16 v[176:191], a[216:219], a[216:219], 0\n" \
"v_add_u32_e32 v55, s67, v20\n" \
"v_add_u32_e32 v52, 0xc000, v55\n" \
"s_xor_b32 m0, s65, 0x10000\n" \
"ds_read_b128 a[8:11], v58 offset:2048\n" \
"buffer_load_dwordx4 v52, s[8:11], 0 offen offset:0 sc1 lds\n" \
"v_mfma_f32_32x32x16_bf16 v[192:207], a[216:219], a[216:219], 0\n" \
"ds_read_b128 a[24:27], v58 offset:10240\n" \
"ds_read_b128 a[40:43], v59 offset:2048\n" \
"ds_read_b128 a[56:59], v59 offset:10240\n" \
"ds_read_b128 v[108:111], v63 offset:3072\n" \
"buffer_load_dwordx4 v52, s[8:11], 0 offen offset:1024 sc1 lds\n" \
"v_mfma_f32_32x32x16_bf16 v[208:223], a[216:219], a[216:219], 0\n" \
"ds_read_b128 a[12:15], v58 offset:3072\n" \
"ds_read_b128 a[28:31], v58 offset:11264\n" \
"ds_read_b128 a[44:47], v59 offset:3072\n" \
"ds_read_b128 a[60:63], v59 offset:11264\n" \
"s_waitcnt lgkmcnt(15)\n" \
"v_xor_b32_e32 v96, 0x80008000, v96\n" \
"v_mfma_f32_32x32x16_bf16 v[224:239], a[216:219], a[216:219], 0\n" \
"v_xor_b32_e32 v97, 0x80008000, v97\n" \
"v_xor_b32_e32 v98, 0x80008000, v98\n" \
"v_xor_b32_e32 v99, 0x80008000, v99\n" \
"ds_read_b128 a[160:163], v20 offset:0\n" \
"ds_read_b128 a[164:167], v20 offset:1024\n" \
"s_waitcnt lgkmcnt(15)\n" \
"v_xor_b32_e32 v100, 0x80008000, v100\n" \
"v_mfma_f32_32x32x16_bf16 v[240:255], a[216:219], a[216:219], 0\n" \
"v_xor_b32_e32 v101, 0x80008000, v101\n" \
"v_xor_b32_e32 v102, 0x80008000, v102\n" \
"v_xor_b32_e32 v103, 0x80008000, v103\n" \
"ds_read_b128 a[168:171], v20 offset:2048\n" \
"ds_read_b128 a[172:175], v20 offset:3072\n" \
"ds_read_b128 a[188:191], v20 offset:7168\n" \
"v_mfma_f32_16x16x32_bf16 a[192:195], a[216:219], a[216:219], 0\n" \
"ds_read_b128 a[184:187], v20 offset:6144\n" \
"s_waitcnt lgkmcnt(15)\n" \
"v_xor_b32_e32 v104, 0x80008000, v104\n" \
"v_mfma_f32_16x16x32_bf16 a[196:199], a[216:219], a[216:219], 0\n" \
"v_xor_b32_e32 v105, 0x80008000, v105\n" \
"v_xor_b32_e32 v106, 0x80008000, v106\n" \
"v_mfma_f32_16x16x32_bf16 v[32:35], v[96:99], a[0:3], 0\n" \
"v_xor_b32_e32 v107, 0x80008000, v107\n" \
"ds_read_b128 a[180:183], v20 offset:5120\n" \
"v_mfma_f32_16x16x32_bf16 v[36:39], v[96:99], a[16:19], 0\n" \
"ds_read_b128 a[176:179], v20 offset:4096\n" \
"s_waitcnt lgkmcnt(12)\n" \
"v_xor_b32_e32 v108, 0x80008000, v108\n" \
"v_mfma_f32_16x16x32_bf16 v[40:43], v[96:99], a[32:35], 0\n" \
"v_xor_b32_e32 v109, 0x80008000, v109\n" \
"v_xor_b32_e32 v110, 0x80008000, v110\n" \
"v_mfma_f32_16x16x32_bf16 v[44:47], v[96:99], a[48:51], 0\n" \
"v_xor_b32_e32 v111, 0x80008000, v111\n" \
"v_add_u32_e32 v60, s15, v20\n" \
"v_mfma_f32_16x16x32_bf16 v[32:35], v[100:103], a[4:7], v[32:35]\n" \
"v_add_u32_e32 v60, 0x10000, v60\n" \
"ds_read_b128 a[128:131], v20 offset:8192\n" \
"v_mfma_f32_16x16x32_bf16 v[36:39], v[100:103], a[20:23], v[36:39]\n" \
"ds_read_b128 a[144:147], v20 offset:12288\n" \
"ds_read_b128 a[132:135], v20 offset:9216\n" \
"v_mfma_f32_16x16x32_bf16 v[40:43], v[100:103], a[36:39], v[40:43]\n" \
"ds_read_b128 a[148:151], v20 offset:13312\n" \
"ds_read_b128 a[136:139], v20 offset:10240\n" \
"v_mfma_f32_16x16x32_bf16 v[44:47], v[100:103], a[52:55], v[44:47]\n" \
"ds_read_b128 a[152:155], v20 offset:14336\n" \
"ds_read_b128 a[140:143], v20 offset:11264\n" \
"v_mfma_f32_16x16x32_bf16 v[32:35], v[104:107], a[8:11], v[32:35]\n" \
"ds_read_b128 a[156:159], v20 offset:15360\n" \
"ds_read_b128 a[64:67], v60 offset:16384\n" \
"v_mfma_f32_16x16x32_bf16 v[36:39], v[104:107], a[24:27], v[36:39]\n" \
"ds_read_b128 a[68:71], v60 offset:17408\n" \
"ds_read_b128 a[72:75], v60 offset:18432\n" \
"v_mfma_f32_16x16x32_bf16 v[40:43], v[104:107], a[40:43], v[40:43]\n" \
"ds_read_b128 a[76:79], v60 offset:19456\n" \
"ds_read_b128 a[80:83], v60 offset:24576\n" \
"v_mfma_f32_16x16x32_bf16 v[44:47], v[104:107], a[56:59], v[44:47]\n" \
"ds_read_b128 a[84:87], v60 offset:25600\n" \
"s_waitcnt lgkmcnt(15)\n" \
"v_mfma_f32_16x16x32_bf16 v[32:35], v[108:111], a[12:15], v[32:35]\n" \
"ds_read_b128 a[88:91], v60 offset:26624\n" \
"ds_read_b128 a[92:95], v60 offset:27648\n" \
"v_mfma_f32_16x16x32_bf16 v[36:39], v[108:111], a[28:31], v[36:39]\n" \
"ds_read_b128 a[96:99], v60 offset:0\n" \
"ds_read_b128 a[100:103], v60 offset:1024\n" \
"v_mfma_f32_16x16x32_bf16 v[40:43], v[108:111], a[44:47], v[40:43]\n" \
"ds_read_b128 a[104:107], v60 offset:2048\n" \
"ds_read_b128 a[108:111], v60 offset:3072\n" \
"v_mfma_f32_16x16x32_bf16 v[44:47], v[108:111], a[60:63], v[44:47]\n" \
"ds_read_b128 a[112:115], v60 offset:8192\n" \
"ds_read_b128 a[116:119], v60 offset:9216\n" \
"v_mfma_f32_16x16x32_bf16 v[64:67], a[160:163], a[0:3], v[32:35]\n" \
"ds_read_b128 a[120:123], v60 offset:10240\n" \
"ds_read_b128 a[124:127], v60 offset:11264\n" \
"v_mfma_f32_16x16x32_bf16 v[64:67], a[164:167], a[4:7], v[64:67]\n" \
"v_bfe_u32 v26, v20, 8, 2\n" \
"s_mov_b32 m0, s20\n" \
"v_mfma_f32_16x16x32_bf16 v[64:67], a[168:171], a[8:11], v[64:67]\n" \
"v_bfe_u32 v25, v20, 5, 3\n" \
"v_lshlrev_b32_e32 v26, 1, v26\n" \
"v_mfma_f32_16x16x32_bf16 v[64:67], a[172:175], a[12:15], v[64:67]\n" \
"global_load_lds_dword v[16:17], off\n" \
"v_mfma_f32_16x16x32_bf16 v[68:71], a[188:191], a[12:15], v[32:35]\n" \
"v_sub_u32_e32 v25, v25, v26\n" \
"v_and_b32_e32 v26, 16, v20\n" \
"v_mfma_f32_16x16x32_bf16 v[68:71], a[184:187], a[8:11], v[68:71]\n" \
"v_mov_b32_e32 v27, 0x3f80\n" \
"v_lshlrev_b32_e32 v26, v26, v27\n" \
"v_mfma_f32_16x16x32_bf16 v[68:71], a[180:183], a[4:7], v[68:71]\n" \
"v_mov_b32_e32 v54, 0x3f803f80\n" \
"s_waitcnt lgkmcnt(15)\n" \
"v_mfma_f32_16x16x32_bf16 v[68:71], a[176:179], a[0:3], v[68:71]\n" \
"s_mov_b32 s84, 0x33333333\n" \
"s_mov_b32 s85, 0xcccccccc\n" \
"v_mfma_f32_16x16x32_bf16 v[76:79], a[176:179], a[16:19], v[36:39]\n" \
"s_mov_b32 s86, 0x11111111\n" \
"s_mov_b32 s87, 0x11111111\n" \
"v_mfma_f32_16x16x32_bf16 v[76:79], a[180:183], a[20:23], v[76:79]\n" \
"s_mov_b32 s88, 0x44444444\n" \
"s_mov_b32 s89, 0x44444444\n" \
"v_mfma_f32_16x16x32_bf16 v[76:79], a[184:187], a[24:27], v[76:79]\n" \
"v_exp_f32_e32 v70, v70\n" \
"v_mfma_f32_16x16x32_bf16 v[76:79], a[188:191], a[28:31], v[76:79]\n" \
"v_exp_f32_e32 v71, v71\n" \
"v_mfma_f32_16x16x32_bf16 v[72:75], a[172:175], a[28:31], v[36:39]\n" \
"v_exp_f32_e32 v68, v68\n" \
"v_mfma_f32_16x16x32_bf16 v[72:75], a[168:171], a[24:27], v[72:75]\n" \
"v_exp_f32_e32 v69, v69\n" \
"v_mfma_f32_16x16x32_bf16 v[72:75], a[164:167], a[20:23], v[72:75]\n" \
"v_bfe_i32 v21, v20, 4, 1\n" \
"v_cvt_pk_bf16_f32 v71, v70, v71\n" \
"v_mfma_f32_16x16x32_bf16 v[72:75], a[160:163], a[16:19], v[72:75]\n" \
"v_exp_f32_e32 v66, v66\n" \
"v_mfma_f32_16x16x32_bf16 v[80:83], a[160:163], a[32:35], v[40:43]\n" \
"v_exp_f32_e32 v67, v67\n" \
"v_mfma_f32_16x16x32_bf16 v[80:83], a[164:167], a[36:39], v[80:83]\n" \
"v_exp_f32_e32 v64, v64\n" \
"v_mfma_f32_16x16x32_bf16 v[80:83], a[168:171], a[40:43], v[80:83]\n" \
"v_exp_f32_e32 v65, v65\n" \
"v_mfma_f32_16x16x32_bf16 v[80:83], a[172:175], a[44:47], v[80:83]\n" \
"v_exp_f32_e32 v72, v72\n" \
"v_mfma_f32_16x16x32_bf16 v[84:87], a[188:191], a[44:47], v[40:43]\n" \
"s_waitcnt vmcnt(3)\n" \
"v_cmp_ne_u32_e32 vcc, s62, v57\n" \
"v_mfma_f32_16x16x32_bf16 v[84:87], a[184:187], a[40:43], v[84:87]\n" \
"v_exp_f32_e32 v73, v73\n" \
"v_mfma_f32_16x16x32_bf16 v[84:87], a[180:183], a[36:39], v[84:87]\n" \
"v_exp_f32_e32 v74, v74\n" \
"v_mfma_f32_16x16x32_bf16 v[84:87], a[176:179], a[32:35], v[84:87]\n" \
"s_cbranch_vccnz L_skb_%=\n" \
"L_rkb_%=:\n" \
"v_cmp_eq_u32_e32 vcc, 0, v25\n" \
"v_mfma_f32_16x16x32_bf16 v[92:95], a[176:179], a[48:51], v[44:47]\n" \
"v_exp_f32_e32 v75, v75\n" \
"v_mfma_f32_16x16x32_bf16 v[92:95], a[180:183], a[52:55], v[92:95]\n" \
"v_cndmask_b32_e32 v28, 0, v26, vcc\n" \
"v_cvt_pk_bf16_f32 v72, v72, v73\n" \
"v_mfma_f32_16x16x32_bf16 v[92:95], a[184:187], a[56:59], v[92:95]\n" \
"v_exp_f32_e32 v76, v76\n" \
"v_mfma_f32_16x16x32_bf16 v[92:95], a[188:191], a[60:63], v[92:95]\n" \
"v_exp_f32_e32 v77, v77\n" \
"v_mfma_f32_16x16x32_bf16 v[88:91], a[172:175], a[60:63], v[44:47]\n" \
"v_exp_f32_e32 v78, v78\n" \
"v_mfma_f32_16x16x32_bf16 v[88:91], a[168:171], a[56:59], v[88:91]\n" \
"v_exp_f32_e32 v79, v79\n" \
"v_mfma_f32_16x16x32_bf16 v[88:91], a[164:167], a[52:55], v[88:91]\n" \
"v_cndmask_b32_e64 v22, 0, v54, s[84:85]\n" \
"v_cndmask_b32_e64 v23, 0, v54, s[86:87]\n" \
"v_mfma_f32_16x16x32_bf16 v[88:91], a[160:163], a[48:51], v[88:91]\n" \
"v_cndmask_b32_e64 v24, 0, v54, s[88:89]\n" \
"v_and_b32_e32 v21, 0xaaaaaaaa, v21\n" \
"s_waitcnt vmcnt(4) lgkmcnt(0)\n" \
"s_barrier\n" \
"v_cmp_eq_u32_e32 vcc, 1, v25\n" \
"v_cvt_pk_bf16_f32 v70, v68, v69\n" \
"v_cvt_pk_bf16_f32 v73, v74, v75\n" \
"v_mov_b32_e32 v62, 0\n" \
"v_accvgpr_write_b32 a208, 0\n" \
"v_accvgpr_write_b32 a209, 0\n" \
"v_accvgpr_write_b32 a210, 0\n" \
"v_accvgpr_write_b32 a211, 0\n" \
"v_accvgpr_write_b32 a200, v22\n" \
"v_accvgpr_write_b32 a201, v22\n" \
"v_accvgpr_write_b32 a202, v22\n" \
"v_accvgpr_write_b32 a203, v22\n" \
"v_accvgpr_write_b32 a204, v23\n" \
"v_accvgpr_write_b32 a205, v23\n" \
"v_accvgpr_write_b32 a206, v23\n" \
"v_accvgpr_write_b32 a207, v23\n" \
"v_accvgpr_write_b32 a212, v24\n" \
"v_accvgpr_write_b32 a213, v24\n" \
"v_accvgpr_write_b32 a214, v24\n" \
"v_accvgpr_write_b32 a215, v24\n" \
"v_or_b32_e32 v21, 0x44444444, v21\n" \
"s_mov_b32 s64, 0x8000\n" \
"v_add_u32_e32 v50, 0x6000, v55\n" \
"v_add_u32_e32 v51, 0xa000, v55\n" \
"v_add_u32_e32 v53, 0x10000, v55\n" \
"v_add_u32_e32 v48, 0xc000, v20\n" \
"v_add_u32_e32 v49, s17, v20\n" \
"v_cndmask_b32_e32 v29, 0, v26, vcc\n" \
"v_accvgpr_write_b32 a220, v96\n" \
"v_accvgpr_write_b32 a221, v97\n" \
"v_accvgpr_write_b32 a222, v98\n" \
"v_accvgpr_write_b32 a223, v99\n" \
"v_accvgpr_write_b32 a224, v100\n" \
"v_accvgpr_write_b32 a225, v101\n" \
"v_accvgpr_write_b32 a226, v102\n" \
"v_accvgpr_write_b32 a227, v103\n" \
"v_accvgpr_write_b32 a228, v104\n" \
"v_accvgpr_write_b32 a229, v105\n" \
"v_accvgpr_write_b32 a230, v106\n" \
"v_accvgpr_write_b32 a231, v107\n" \
"v_accvgpr_write_b32 a232, v108\n" \
"v_accvgpr_write_b32 a233, v109\n" \
"v_accvgpr_write_b32 a234, v110\n" \
"v_accvgpr_write_b32 a235, v111\n" \
"v_cvt_pk_bf16_f32 v69, v66, v67\n" \
"v_cvt_pk_bf16_f32 v68, v64, v65\n" \
"v_cvt_pk_bf16_f32 v74, v76, v77\n" \
"v_cvt_pk_bf16_f32 v75, v78, v79\n" \
"s_sub_u32 s66, s18, 1\n" \
"s_cbranch_scc1 L_tr_%=\n" \
".fill (L_ab + 1856 - .) / 4, 4, 0xBF800000\n" \
"L_1_%=:\n" \
"v_mfma_f32_16x16x32_bf16 v[96:99], a[128:131], a[0:3], v[32:35]\n" \
"s_xor_b32 m0, s65, 0x12000\n" \
"ds_read_b128 a[160:163], v49 offset:0\n" \
"v_mfma_f32_16x16x32_bf16 v[96:99], a[132:135], a[4:7], v[96:99]\n" \
"buffer_load_dwordx4 v50, s[8:11], s64 offen offset:0 sc1 lds\n" \
"v_mfma_f32_16x16x32_bf16 v[96:99], a[136:139], a[8:11], v[96:99]\n" \
"ds_read_b128 a[164:167], v49 offset:1024\n" \
"ds_read_b128 a[168:171], v49 offset:2048\n" \
"v_mfma_f32_16x16x32_bf16 v[96:99], a[140:143], a[12:15], v[96:99]\n" \
"ds_read_b128 a[172:175], v49 offset:3072\n" \
"ds_read_b128 a[176:179], v49 offset:4096\n" \
"v_mfma_f32_16x16x32_bf16 v[100:103], a[156:159], a[12:15], v[32:35]\n" \
"ds_read_b128 a[180:183], v49 offset:5120\n" \
"ds_read_b128 a[184:187], v49 offset:6144\n" \
"v_mfma_f32_16x16x32_bf16 v[100:103], a[152:155], a[8:11], v[100:103]\n" \
"v_exp_f32_e64 v86, v86\n" \
"v_mfma_f32_16x16x32_bf16 v[100:103], a[148:151], a[4:7], v[100:103]\n" \
"v_exp_f32_e64 v87, v87\n" \
"v_mfma_f32_16x16x32_bf16 v[100:103], a[144:147], a[0:3], v[100:103]\n" \
"ds_read_b128 a[188:191], v49 offset:7168\n" \
"v_cvt_pk_bf16_f32 v87, v86, v87\n" \
"v_mfma_f32_16x16x32_bf16 v[108:111], a[144:147], a[16:19], v[36:39]\n" \
"v_exp_f32_e64 v84, v84\n" \
"v_mfma_f32_16x16x32_bf16 v[108:111], a[148:151], a[20:23], v[108:111]\n" \
"v_exp_f32_e64 v85, v85\n" \
"v_mfma_f32_16x16x32_bf16 v[108:111], a[152:155], a[24:27], v[108:111]\n" \
"v_exp_f32_e64 v82, v82\n" \
"v_mfma_f32_16x16x32_bf16 v[108:111], a[156:159], a[28:31], v[108:111]\n" \
"v_exp_f32_e64 v83, v83\n" \
"v_mfma_f32_16x16x32_bf16 v[104:107], a[140:143], a[28:31], v[36:39]\n" \
"v_exp_f32_e64 v80, v80\n" \
"v_mfma_f32_16x16x32_bf16 v[104:107], a[136:139], a[24:27], v[104:107]\n" \
"v_exp_f32_e64 v81, v81\n" \
"v_mfma_f32_16x16x32_bf16 v[104:107], a[132:135], a[20:23], v[104:107]\n" \
"v_exp_f32_e64 v88, v88\n" \
"v_mfma_f32_16x16x32_bf16 v[104:107], a[128:131], a[16:19], v[104:107]\n" \
"buffer_load_dwordx4 v50, s[8:11], s64 offen offset:1024 sc1 lds\n" \
"v_mfma_f32_16x16x32_bf16 v[112:115], a[128:131], a[32:35], v[40:43]\n" \
"s_xor_b32 m0, s65, 0x6000\n" \
"v_cvt_pk_bf16_f32 v86, v84, v85\n" \
"v_mfma_f32_16x16x32_bf16 v[112:115], a[132:135], a[36:39], v[112:115]\n" \
"v_cvt_pk_bf16_f32 v85, v82, v83\n" \
"v_cvt_pk_bf16_f32 v84, v80, v81\n" \
"v_mfma_f32_16x16x32_bf16 v[112:115], a[136:139], a[40:43], v[112:115]\n" \
"v_exp_f32_e64 v89, v89\n" \
"v_mfma_f32_16x16x32_bf16 v[112:115], a[140:143], a[44:47], v[112:115]\n" \
"v_exp_f32_e64 v90, v90\n" \
"v_mfma_f32_16x16x32_bf16 v[116:119], a[156:159], a[44:47], v[40:43]\n" \
"v_exp_f32_e64 v91, v91\n" \
"v_mfma_f32_16x16x32_bf16 v[116:119], a[152:155], a[40:43], v[116:119]\n" \
"v_exp_f32_e64 v92, v92\n" \
"v_mfma_f32_16x16x32_bf16 v[116:119], a[148:151], a[36:39], v[116:119]\n" \
"v_exp_f32_e64 v93, v93\n" \
"v_mfma_f32_16x16x32_bf16 v[116:119], a[144:147], a[32:35], v[116:119]\n" \
"v_exp_f32_e64 v94, v94\n" \
"v_mfma_f32_16x16x32_bf16 v[124:127], a[144:147], a[48:51], v[44:47]\n" \
"v_cvt_pk_bf16_f32 v88, v88, v89\n" \
"v_cvt_pk_bf16_f32 v89, v90, v91\n" \
"v_mfma_f32_16x16x32_bf16 v[124:127], a[148:151], a[52:55], v[124:127]\n" \
"v_exp_f32_e64 v95, v95\n" \
"v_mfma_f32_16x16x32_bf16 v[124:127], a[152:155], a[56:59], v[124:127]\n" \
"v_cvt_pk_bf16_f32 v90, v92, v93\n" \
"v_cvt_pk_bf16_f32 v91, v94, v95\n" \
"v_mfma_f32_16x16x32_bf16 v[124:127], a[156:159], a[60:63], v[124:127]\n" \
"ds_read_b128 a[144:147], v48 offset:4096\n" \
"ds_read_b128 a[148:151], v48 offset:5120\n" \
"v_mfma_f32_16x16x32_bf16 v[120:123], a[140:143], a[60:63], v[44:47]\n" \
"ds_read_b128 a[152:155], v48 offset:6144\n" \
"ds_read_b128 a[140:143], v48 offset:3072\n" \
"v_mfma_f32_16x16x32_bf16 v[120:123], a[136:139], a[56:59], v[120:123]\n" \
"ds_read_b128 a[136:139], v48 offset:2048\n" \
"ds_read_b128 a[156:159], v48 offset:7168\n" \
"v_mfma_f32_16x16x32_bf16 v[120:123], a[132:135], a[52:55], v[120:123]\n" \
"buffer_load_dwordx4 v51, s[8:11], s64 offen offset:0 sc1 lds\n" \
"v_mfma_f32_16x16x32_bf16 v[120:123], a[128:131], a[48:51], v[120:123]\n" \
"ds_read_b128 a[128:131], v48 offset:0\n" \
"s_waitcnt lgkmcnt(7)\n" \
"v_mfma_f32_16x16x32_bf16 v[128:131], a[160:163], v[68:71], v[128:131]\n" \
"v_exp_f32_e32 v102, v102\n" \
"v_mfma_f32_16x16x32_bf16 v[160:163], a[160:163], v[72:75], v[160:163]\n" \
"v_exp_f32_e64 v103, v103\n" \
"v_mfma_f32_16x16x32_bf16 v[132:135], a[164:167], v[68:71], v[132:135]\n" \
"ds_read_b128 a[132:135], v48 offset:1024\n" \
"v_cvt_pk_bf16_f32 v103, v102, v103\n" \
"v_mfma_f32_16x16x32_bf16 v[164:167], a[164:167], v[72:75], v[164:167]\n" \
"v_exp_f32_e64 v100, v100\n" \
"v_mfma_f32_16x16x32_bf16 v[136:139], a[168:171], v[68:71], v[136:139]\n" \
"v_exp_f32_e64 v101, v101\n" \
"v_mfma_f32_16x16x32_bf16 v[168:171], a[168:171], v[72:75], v[168:171]\n" \
"v_exp_f32_e64 v98, v98\n" \
"v_mfma_f32_16x16x32_bf16 v[140:143], a[172:175], v[68:71], v[140:143]\n" \
"v_exp_f32_e64 v99, v99\n" \
"v_mfma_f32_16x16x32_bf16 v[172:175], a[172:175], v[72:75], v[172:175]\n" \
"v_exp_f32_e64 v96, v96\n" \
"v_mfma_f32_16x16x32_bf16 v[144:147], a[176:179], v[68:71], v[144:147]\n" \
"v_exp_f32_e64 v97, v97\n" \
"v_mfma_f32_16x16x32_bf16 v[176:179], a[176:179], v[72:75], v[176:179]\n" \
"v_exp_f32_e64 v104, v104\n" \
"v_mfma_f32_16x16x32_bf16 v[148:151], a[180:183], v[68:71], v[148:151]\n" \
"v_exp_f32_e64 v105, v105\n" \
"v_mfma_f32_16x16x32_bf16 v[180:183], a[180:183], v[72:75], v[180:183]\n" \
"v_cvt_pk_bf16_f32 v102, v100, v101\n" \
"v_cvt_pk_bf16_f32 v101, v98, v99\n" \
"v_mfma_f32_16x16x32_bf16 v[152:155], a[184:187], v[68:71], v[152:155]\n" \
"v_cvt_pk_bf16_f32 v100, v96, v97\n" \
"v_cvt_pk_bf16_f32 v104, v104, v105\n" \
"v_mfma_f32_16x16x32_bf16 v[184:187], a[184:187], v[72:75], v[184:187]\n" \
"buffer_load_dwordx4 v51, s[8:11], s64 offen offset:1024 sc1 lds\n" \
"v_mfma_f32_16x16x32_bf16 v[156:159], a[188:191], v[68:71], v[156:159]\n" \
"v_exp_f32_e64 v106, v106\n" \
"v_mfma_f32_16x16x32_bf16 v[188:191], a[188:191], v[72:75], v[188:191]\n" \
"v_exp_f32_e64 v107, v107\n" \
"v_smfmac_f32_16x16x64_bf16 a[192:195], a[200:203], v[68:75], v21\n" \
"v_exp_f32_e64 v108, v108\n" \
"v_mfma_f32_16x16x32_bf16 v[220:223], a[188:191], v[84:87], v[220:223]\n" \
"v_exp_f32_e64 v109, v109\n" \
"v_mfma_f32_16x16x32_bf16 v[252:255], a[188:191], v[88:91], v[252:255]\n" \
"v_exp_f32_e64 v110, v110\n" \
"v_mfma_f32_16x16x32_bf16 v[216:219], a[184:187], v[84:87], v[216:219]\n" \
"v_exp_f32_e64 v111, v111\n" \
"v_mfma_f32_16x16x32_bf16 v[248:251], a[184:187], v[88:91], v[248:251]\n" \
"v_exp_f32_e64 v118, v118\n" \
"v_mfma_f32_16x16x32_bf16 v[212:215], a[180:183], v[84:87], v[212:215]\n" \
"v_exp_f32_e64 v119, v119\n" \
"v_mfma_f32_16x16x32_bf16 v[244:247], a[180:183], v[88:91], v[244:247]\n" \
"v_cvt_pk_bf16_f32 v105, v106, v107\n" \
"v_cvt_pk_bf16_f32 v106, v108, v109\n" \
"v_mfma_f32_16x16x32_bf16 v[208:211], a[176:179], v[84:87], v[208:211]\n" \
"v_cvt_pk_bf16_f32 v107, v110, v111\n" \
"v_cvt_pk_bf16_f32 v119, v118, v119\n" \
"v_mfma_f32_16x16x32_bf16 v[240:243], a[176:179], v[88:91], v[240:243]\n" \
"v_exp_f32_e64 v116, v116\n" \
"v_mfma_f32_16x16x32_bf16 v[204:207], a[172:175], v[84:87], v[204:207]\n" \
"v_exp_f32_e64 v117, v117\n" \
"v_mfma_f32_16x16x32_bf16 v[236:239], a[172:175], v[88:91], v[236:239]\n" \
"v_exp_f32_e64 v114, v114\n" \
"v_mfma_f32_16x16x32_bf16 v[200:203], a[168:171], v[84:87], v[200:203]\n" \
"v_exp_f32_e64 v115, v115\n" \
"v_mfma_f32_16x16x32_bf16 v[232:235], a[168:171], v[88:91], v[232:235]\n" \
"v_exp_f32_e64 v112, v112\n" \
"v_mfma_f32_16x16x32_bf16 v[196:199], a[164:167], v[84:87], v[196:199]\n" \
"v_exp_f32_e64 v113, v113\n" \
"v_mfma_f32_16x16x32_bf16 v[228:231], a[164:167], v[88:91], v[228:231]\n" \
"v_exp_f32_e64 v120, v120\n" \
"v_mfma_f32_16x16x32_bf16 v[192:195], a[160:163], v[84:87], v[192:195]\n" \
"v_exp_f32_e64 v121, v121\n" \
"v_mfma_f32_16x16x32_bf16 v[224:227], a[160:163], v[88:91], v[224:227]\n" \
"v_cvt_pk_bf16_f32 v118, v116, v117\n" \
"v_cvt_pk_bf16_f32 v117, v114, v115\n" \
"v_smfmac_f32_16x16x64_bf16 a[196:199], a[200:203], v[84:91], v21\n" \
"v_cvt_pk_bf16_f32 v116, v112, v113\n" \
"v_cvt_pk_bf16_f32 v120, v120, v121\n" \
"s_waitcnt vmcnt(4) lgkmcnt(0)\n" \
"s_barrier\n" \
"v_mfma_f32_16x16x32_bf16 v[64:67], a[128:131], a[0:3], v[32:35]\n" \
"s_xor_b32 m0, s65, 0x14000\n" \
"ds_read_b128 a[160:163], v49 offset:8192\n" \
"v_mfma_f32_16x16x32_bf16 v[64:67], a[132:135], a[4:7], v[64:67]\n" \
"buffer_load_dwordx4 v52, s[8:11], s64 offen offset:0 sc1 lds\n" \
"v_mfma_f32_16x16x32_bf16 v[64:67], a[136:139], a[8:11], v[64:67]\n" \
"ds_read_b128 a[164:167], v49 offset:9216\n" \
"ds_read_b128 a[168:171], v49 offset:10240\n" \
"v_mfma_f32_16x16x32_bf16 v[64:67], a[140:143], a[12:15], v[64:67]\n" \
"ds_read_b128 a[172:175], v49 offset:11264\n" \
"ds_read_b128 a[176:179], v49 offset:12288\n" \
"v_mfma_f32_16x16x32_bf16 v[68:71], a[156:159], a[12:15], v[32:35]\n" \
"ds_read_b128 a[180:183], v49 offset:13312\n" \
"ds_read_b128 a[184:187], v49 offset:14336\n" \
"v_mfma_f32_16x16x32_bf16 v[68:71], a[152:155], a[8:11], v[68:71]\n" \
"v_exp_f32_e64 v122, v122\n" \
"v_mfma_f32_16x16x32_bf16 v[68:71], a[148:151], a[4:7], v[68:71]\n" \
"v_exp_f32_e64 v123, v123\n" \
"v_mfma_f32_16x16x32_bf16 v[68:71], a[144:147], a[0:3], v[68:71]\n" \
"ds_read_b128 a[188:191], v49 offset:15360\n" \
"v_cvt_pk_bf16_f32 v121, v122, v123\n" \
"v_mfma_f32_16x16x32_bf16 v[76:79], a[144:147], a[16:19], v[36:39]\n" \
"v_exp_f32_e64 v124, v124\n" \
"v_mfma_f32_16x16x32_bf16 v[76:79], a[148:151], a[20:23], v[76:79]\n" \
"v_exp_f32_e64 v125, v125\n" \
"v_mfma_f32_16x16x32_bf16 v[76:79], a[152:155], a[24:27], v[76:79]\n" \
"v_exp_f32_e64 v126, v126\n" \
"v_mfma_f32_16x16x32_bf16 v[76:79], a[156:159], a[28:31], v[76:79]\n" \
"v_exp_f32_e64 v127, v127\n" \
"v_mfma_f32_16x16x32_bf16 v[72:75], a[140:143], a[28:31], v[36:39]\n" \
"v_cvt_pk_bf16_f32 v122, v124, v125\n" \
"v_cvt_pk_bf16_f32 v123, v126, v127\n" \
"v_mfma_f32_16x16x32_bf16 v[72:75], a[136:139], a[24:27], v[72:75]\n" \
"v_exp_f32_e64 v70, v70\n" \
"v_mfma_f32_16x16x32_bf16 v[72:75], a[132:135], a[20:23], v[72:75]\n" \
"v_exp_f32_e64 v71, v71\n" \
"v_mfma_f32_16x16x32_bf16 v[72:75], a[128:131], a[16:19], v[72:75]\n" \
"buffer_load_dwordx4 v52, s[8:11], s64 offen offset:1024 sc1 lds\n" \
"v_mfma_f32_16x16x32_bf16 v[80:83], a[128:131], a[32:35], v[40:43]\n" \
"s_mov_b32 m0, s65\n" \
"v_cvt_pk_bf16_f32 v71, v70, v71\n" \
"v_mfma_f32_16x16x32_bf16 v[80:83], a[132:135], a[36:39], v[80:83]\n" \
"v_exp_f32_e32 v68, v68\n" \
"v_mfma_f32_16x16x32_bf16 v[80:83], a[136:139], a[40:43], v[80:83]\n" \
"v_exp_f32_e64 v69, v69\n" \
"v_mfma_f32_16x16x32_bf16 v[80:83], a[140:143], a[44:47], v[80:83]\n" \
"v_exp_f32_e64 v72, v72\n" \
"v_mfma_f32_16x16x32_bf16 v[84:87], a[156:159], a[44:47], v[40:43]\n" \
"v_exp_f32_e64 v73, v73\n" \
"v_mfma_f32_16x16x32_bf16 v[84:87], a[152:155], a[40:43], v[84:87]\n" \
"v_exp_f32_e64 v74, v74\n" \
"v_mfma_f32_16x16x32_bf16 v[84:87], a[148:151], a[36:39], v[84:87]\n" \
"v_exp_f32_e64 v75, v75\n" \
"v_mfma_f32_16x16x32_bf16 v[84:87], a[144:147], a[32:35], v[84:87]\n" \
"v_exp_f32_e64 v66, v66\n" \
"v_mfma_f32_16x16x32_bf16 v[92:95], a[144:147], a[48:51], v[44:47]\n" \
"ds_read_b128 a[144:147], v48 offset:12288\n" \
"v_cvt_pk_bf16_f32 v72, v72, v73\n" \
"v_mfma_f32_16x16x32_bf16 v[92:95], a[148:151], a[52:55], v[92:95]\n" \
"ds_read_b128 a[148:151], v48 offset:13312\n" \
"v_cvt_pk_bf16_f32 v70, v68, v69\n" \
"v_mfma_f32_16x16x32_bf16 v[92:95], a[152:155], a[56:59], v[92:95]\n" \
"ds_read_b128 a[152:155], v48 offset:14336\n" \
"v_cvt_pk_bf16_f32 v73, v74, v75\n" \
"v_mfma_f32_16x16x32_bf16 v[92:95], a[156:159], a[60:63], v[92:95]\n" \
"ds_read_b128 a[156:159], v48 offset:15360\n" \
"v_add_u32_e32 v49, 0x10000, v48\n" \
"v_mfma_f32_16x16x32_bf16 v[88:91], a[140:143], a[60:63], v[44:47]\n" \
"ds_read_b128 a[140:143], v48 offset:11264\n" \
"s_xor_b32 s65, s65, 0x4000\n" \
"v_mfma_f32_16x16x32_bf16 v[88:91], a[136:139], a[56:59], v[88:91]\n" \
"v_exp_f32_e64 v67, v67\n" \
"v_mfma_f32_16x16x32_bf16 v[88:91], a[132:135], a[52:55], v[88:91]\n" \
"buffer_load_dwordx4 v53, s[8:11], s64 offen offset:0 sc1 lds\n" \
"v_mfma_f32_16x16x32_bf16 v[88:91], a[128:131], a[48:51], v[88:91]\n" \
"ds_read_b128 a[128:131], v48 offset:8192\n" \
"s_waitcnt lgkmcnt(6)\n" \
"v_mfma_f32_16x16x32_bf16 v[128:131], a[160:163], v[100:103], v[128:131]\n" \
"ds_read_b128 a[132:135], v48 offset:9216\n" \
"ds_read_b128 a[136:139], v48 offset:10240\n" \
"v_mfma_f32_16x16x32_bf16 v[160:163], a[160:163], v[104:107], v[160:163]\n" \
"v_exp_f32_e32 v64, v64\n" \
"v_mfma_f32_16x16x32_bf16 v[132:135], a[164:167], v[100:103], v[132:135]\n" \
"v_exp_f32_e64 v65, v65\n" \
"v_mfma_f32_16x16x32_bf16 v[164:167], a[164:167], v[104:107], v[164:167]\n" \
"v_exp_f32_e64 v76, v76\n" \
"v_mfma_f32_16x16x32_bf16 v[136:139], a[168:171], v[100:103], v[136:139]\n" \
"v_exp_f32_e64 v77, v77\n" \
"v_mfma_f32_16x16x32_bf16 v[168:171], a[168:171], v[104:107], v[168:171]\n" \
"v_exp_f32_e64 v78, v78\n" \
"v_mfma_f32_16x16x32_bf16 v[140:143], a[172:175], v[100:103], v[140:143]\n" \
"v_exp_f32_e64 v79, v79\n" \
"v_mfma_f32_16x16x32_bf16 v[172:175], a[172:175], v[104:107], v[172:175]\n" \
"v_cvt_pk_bf16_f32 v69, v66, v67\n" \
"v_cvt_pk_bf16_f32 v68, v64, v65\n" \
"v_mfma_f32_16x16x32_bf16 v[144:147], a[176:179], v[100:103], v[144:147]\n" \
"v_cvt_pk_bf16_f32 v74, v76, v77\n" \
"v_cvt_pk_bf16_f32 v75, v78, v79\n" \
"v_mfma_f32_16x16x32_bf16 v[176:179], a[176:179], v[104:107], v[176:179]\n" \
"v_xor_b32_e32 v48, 0x4000, v48\n" \
"v_mfma_f32_16x16x32_bf16 v[148:151], a[180:183], v[100:103], v[148:151]\n" \
"v_mfma_f32_16x16x32_bf16 v[180:183], a[180:183], v[104:107], v[180:183]\n" \
"v_mfma_f32_16x16x32_bf16 v[152:155], a[184:187], v[100:103], v[152:155]\n" \
"v_mfma_f32_16x16x32_bf16 v[184:187], a[184:187], v[104:107], v[184:187]\n" \
"buffer_load_dwordx4 v53, s[8:11], s64 offen offset:1024 sc1 lds\n" \
"v_mfma_f32_16x16x32_bf16 v[156:159], a[188:191], v[100:103], v[156:159]\n" \
"s_add_u32 s64, s64, 0x8000\n" \
"s_sub_u32 s66, s66, 1\n" \
"s_nop 0\n" \
"v_mfma_f32_16x16x32_bf16 v[188:191], a[188:191], v[104:107], v[188:191]\n" \
"v_smfmac_f32_16x16x64_bf16 a[192:195], a[200:203], v[100:107], v21\n" \
"v_mfma_f32_16x16x32_bf16 v[220:223], a[188:191], v[116:119], v[220:223]\n" \
"v_mfma_f32_16x16x32_bf16 v[252:255], a[188:191], v[120:123], v[252:255]\n" \
"v_mfma_f32_16x16x32_bf16 v[216:219], a[184:187], v[116:119], v[216:219]\n" \
"v_mfma_f32_16x16x32_bf16 v[248:251], a[184:187], v[120:123], v[248:251]\n" \
"v_mfma_f32_16x16x32_bf16 v[212:215], a[180:183], v[116:119], v[212:215]\n" \
"v_mfma_f32_16x16x32_bf16 v[244:247], a[180:183], v[120:123], v[244:247]\n" \
"v_mfma_f32_16x16x32_bf16 v[208:211], a[176:179], v[116:119], v[208:211]\n" \
"v_mfma_f32_16x16x32_bf16 v[240:243], a[176:179], v[120:123], v[240:243]\n" \
"v_mfma_f32_16x16x32_bf16 v[204:207], a[172:175], v[116:119], v[204:207]\n" \
"v_mfma_f32_16x16x32_bf16 v[236:239], a[172:175], v[120:123], v[236:239]\n" \
"v_mfma_f32_16x16x32_bf16 v[200:203], a[168:171], v[116:119], v[200:203]\n" \
"v_mfma_f32_16x16x32_bf16 v[232:235], a[168:171], v[120:123], v[232:235]\n" \
"v_mfma_f32_16x16x32_bf16 v[196:199], a[164:167], v[116:119], v[196:199]\n" \
"v_mfma_f32_16x16x32_bf16 v[228:231], a[164:167], v[120:123], v[228:231]\n" \
"v_mfma_f32_16x16x32_bf16 v[192:195], a[160:163], v[116:119], v[192:195]\n" \
"v_mfma_f32_16x16x32_bf16 v[224:227], a[160:163], v[120:123], v[224:227]\n" \
"v_smfmac_f32_16x16x64_bf16 a[196:199], a[200:203], v[116:123], v21\n" \
"s_waitcnt vmcnt(4) lgkmcnt(0)\n" \
"s_barrier\n" \
"s_cbranch_scc0 L_1_%=\n" \
"L_tr_%=:\n" \
"s_setpc_b64 s[28:29]\n" \
".fill (L_ab + 4096 - .) / 4, 4, 0xBF800000\n" \
".fill (L_ab + 4096 - .) / 4, 4, 0xBF800000\n" \
"L_ap1:\n" \
"s_setpc_b64 s[88:89]\n" \
"L_gtr:\n" \
"v_mfma_f32_16x16x32_bf16 v[96:99], a[128:131], a[0:3], v[32:35]\n" \
"v_mov_b32_e32 v56, s62\n" \
"v_readlane_b32 s68, v11, 0\n" \
"v_mfma_f32_16x16x32_bf16 v[96:99], a[132:135], a[4:7], v[96:99]\n" \
"s_xor_b32 m0, s65, 0x12000\n" \
"s_mov_b32 s65, s13\n" \
"v_mfma_f32_16x16x32_bf16 v[96:99], a[136:139], a[8:11], v[96:99]\n" \
"buffer_load_dwordx4 v50, s[8:11], s64 offen offset:0 sc1 lds\n" \
"v_mfma_f32_16x16x32_bf16 v[96:99], a[140:143], a[12:15], v[96:99]\n" \
"v_add_u32_e32 v63, s15, v20\n" \
"s_add_u32 s67, s14, 40960\n" \
"v_mfma_f32_16x16x32_bf16 v[100:103], a[156:159], a[12:15], v[32:35]\n" \
"buffer_load_dwordx4 v50, s[8:11], s64 offen offset:1024 sc1 lds\n" \
"s_mov_b64 exec, 1\n" \
"buffer_store_dword v56, off, s[56:59], s68 offset:0 sc1\n" \
"s_mov_b64 exec, -1\n" \
"v_mfma_f32_16x16x32_bf16 v[100:103], a[152:155], a[8:11], v[100:103]\n" \
"v_add_u32_e32 v50, 0x6000, v63\n" \
"s_xor_b32 m0, s65, 0x8000\n" \
"v_mfma_f32_16x16x32_bf16 v[100:103], a[148:151], a[4:7], v[100:103]\n" \
"ds_read_b128 a[160:163], v49 offset:0\n" \
"ds_read_b128 a[164:167], v49 offset:1024\n" \
"v_mfma_f32_16x16x32_bf16 v[100:103], a[144:147], a[0:3], v[100:103]\n" \
"ds_read_b128 a[168:171], v49 offset:2048\n" \
"ds_read_b128 a[172:175], v49 offset:3072\n" \
"v_mfma_f32_16x16x32_bf16 v[108:111], a[144:147], a[16:19], v[36:39]\n" \
"buffer_load_dwordx4 v50, s[8:11], s67 offen offset:0 sc1 lds\n" \
"v_mfma_f32_16x16x32_bf16 v[108:111], a[148:151], a[20:23], v[108:111]\n" \
"ds_read_b128 a[176:179], v49 offset:4096\n" \
"ds_read_b128 a[180:183], v49 offset:5120\n" \
"v_mfma_f32_16x16x32_bf16 v[108:111], a[152:155], a[24:27], v[108:111]\n" \
"ds_read_b128 a[184:187], v49 offset:6144\n" \
"ds_read_b128 a[188:191], v49 offset:7168\n" \
"v_mfma_f32_16x16x32_bf16 v[108:111], a[156:159], a[28:31], v[108:111]\n" \
"s_mov_b32 s64, s14\n" \
"v_add_u32_e32 v51, 0x12000, v63\n" \
"v_mfma_f32_16x16x32_bf16 v[104:107], a[140:143], a[28:31], v[36:39]\n" \
"buffer_load_dwordx4 v50, s[8:11], s67 offen offset:1024 sc1 lds\n" \
"v_mfma_f32_16x16x32_bf16 v[104:107], a[136:139], a[24:27], v[104:107]\n" \
"v_exp_f32_e32 v86, v86\n" \
"v_mfma_f32_16x16x32_bf16 v[104:107], a[132:135], a[20:23], v[104:107]\n" \
"v_exp_f32_e32 v87, v87\n" \
"v_mfma_f32_16x16x32_bf16 v[104:107], a[128:131], a[16:19], v[104:107]\n" \
"v_exp_f32_e32 v84, v84\n" \
"v_mfma_f32_16x16x32_bf16 v[112:115], a[128:131], a[32:35], v[40:43]\n" \
"v_exp_f32_e32 v85, v85\n" \
"v_mfma_f32_16x16x32_bf16 v[112:115], a[132:135], a[36:39], v[112:115]\n" \
"buffer_load_dwordx4 v50, s[8:11], s67 offen offset:2048 sc1 lds\n" \
"v_mfma_f32_16x16x32_bf16 v[112:115], a[136:139], a[40:43], v[112:115]\n" \
"v_cvt_pk_bf16_f32 v87, v86, v87\n" \
"v_cvt_pk_bf16_f32 v86, v84, v85\n" \
"v_mfma_f32_16x16x32_bf16 v[112:115], a[140:143], a[44:47], v[112:115]\n" \
"v_exp_f32_e32 v82, v82\n" \
"v_mfma_f32_16x16x32_bf16 v[116:119], a[156:159], a[44:47], v[40:43]\n" \
"v_exp_f32_e32 v83, v83\n" \
"v_mfma_f32_16x16x32_bf16 v[116:119], a[152:155], a[40:43], v[116:119]\n" \
"buffer_load_dwordx4 v50, s[8:11], s67 offen offset:3072 sc1 lds\n" \
"v_mfma_f32_16x16x32_bf16 v[116:119], a[148:151], a[36:39], v[116:119]\n" \
"s_xor_b32 m0, s65, 0xa000\n" \
"v_cvt_pk_bf16_f32 v85, v82, v83\n" \
"v_mfma_f32_16x16x32_bf16 v[116:119], a[144:147], a[32:35], v[116:119]\n" \
"v_exp_f32_e32 v80, v80\n" \
"v_mfma_f32_16x16x32_bf16 v[124:127], a[144:147], a[48:51], v[44:47]\n" \
"v_exp_f32_e32 v81, v81\n" \
"v_mfma_f32_16x16x32_bf16 v[124:127], a[148:151], a[52:55], v[124:127]\n" \
"buffer_load_dwordx4 v51, s[8:11], s64 offen offset:0 sc1 lds\n" \
"v_mfma_f32_16x16x32_bf16 v[124:127], a[152:155], a[56:59], v[124:127]\n" \
"v_cvt_pk_bf16_f32 v84, v80, v81\n" \
"v_readlane_b32 s93, v11, 19\n" \
"v_mfma_f32_16x16x32_bf16 v[124:127], a[156:159], a[60:63], v[124:127]\n" \
"v_exp_f32_e32 v88, v88\n" \
"v_mfma_f32_16x16x32_bf16 v[120:123], a[140:143], a[60:63], v[44:47]\n" \
"v_exp_f32_e32 v89, v89\n" \
"v_mfma_f32_16x16x32_bf16 v[120:123], a[136:139], a[56:59], v[120:123]\n" \
"v_exp_f32_e32 v90, v90\n" \
"v_mfma_f32_16x16x32_bf16 v[120:123], a[132:135], a[52:55], v[120:123]\n" \
"buffer_load_dwordx4 v51, s[8:11], s64 offen offset:1024 sc1 lds\n" \
"v_mfma_f32_16x16x32_bf16 v[120:123], a[128:131], a[48:51], v[120:123]\n" \
"v_cvt_pk_bf16_f32 v88, v88, v89\n" \
"s_waitcnt lgkmcnt(0)\n" \
"v_mfma_f32_16x16x32_bf16 v[128:131], a[160:163], v[68:71], v[128:131]\n" \
"v_exp_f32_e32 v91, v91\n" \
"v_mfma_f32_16x16x32_bf16 v[160:163], a[160:163], v[72:75], v[160:163]\n" \
"v_exp_f32_e32 v92, v92\n" \
"v_mfma_f32_16x16x32_bf16 v[132:135], a[164:167], v[68:71], v[132:135]\n" \
"buffer_load_dwordx4 v51, s[8:11], s64 offen offset:2048 sc1 lds\n" \
"v_mfma_f32_16x16x32_bf16 v[164:167], a[164:167], v[72:75], v[164:167]\n" \
"v_exp_f32_e32 v93, v93\n" \
"v_mfma_f32_16x16x32_bf16 v[136:139], a[168:171], v[68:71], v[136:139]\n" \
"v_exp_f32_e32 v94, v94\n" \
"v_mfma_f32_16x16x32_bf16 v[168:171], a[168:171], v[72:75], v[168:171]\n" \
"v_exp_f32_e32 v95, v95\n" \
"v_mfma_f32_16x16x32_bf16 v[140:143], a[172:175], v[68:71], v[140:143]\n" \
"buffer_load_dwordx4 v51, s[8:11], s64 offen offset:3072 sc1 lds\n" \
"v_mfma_f32_16x16x32_bf16 v[172:175], a[172:175], v[72:75], v[172:175]\n" \
"v_cvt_pk_bf16_f32 v89, v90, v91\n" \
"v_cvt_pk_bf16_f32 v90, v92, v93\n" \
"v_mfma_f32_16x16x32_bf16 v[144:147], a[176:179], v[68:71], v[144:147]\n" \
"v_cvt_pk_bf16_f32 v91, v94, v95\n" \
"v_readlane_b32 s80, v11, 11\n" \
"v_mfma_f32_16x16x32_bf16 v[176:179], a[176:179], v[72:75], v[176:179]\n" \
"v_readlane_b32 s81, v11, 12\n" \
"s_mov_b32 s82, 0x47fca0\n" \
"v_mfma_f32_16x16x32_bf16 v[148:151], a[180:183], v[68:71], v[148:151]\n" \
"s_mov_b32 s83, 0x20000\n" \
"v_readlane_b32 s92, v11, 26\n" \
"v_mfma_f32_16x16x32_bf16 v[180:183], a[180:183], v[72:75], v[180:183]\n" \
"s_add_u32 m0, s93, 0x0\n" \
"ds_read_b128 a[128:131], v48 offset:0\n" \
"v_mfma_f32_16x16x32_bf16 v[152:155], a[184:187], v[68:71], v[152:155]\n" \
"ds_read_b128 a[144:147], v48 offset:4096\n" \
"ds_read_b128 a[132:135], v48 offset:1024\n" \
"v_mfma_f32_16x16x32_bf16 v[184:187], a[184:187], v[72:75], v[184:187]\n" \
"buffer_load_dwordx4 v0, s[80:83], s92 offen offset:0 lds\n" \
"v_mfma_f32_16x16x32_bf16 v[156:159], a[188:191], v[68:71], v[156:159]\n" \
"ds_read_b128 a[148:151], v48 offset:5120\n" \
"ds_read_b128 a[136:139], v48 offset:2048\n" \
"v_mfma_f32_16x16x32_bf16 v[188:191], a[188:191], v[72:75], v[188:191]\n" \
"ds_read_b128 a[152:155], v48 offset:6144\n" \
"ds_read_b128 a[140:143], v48 offset:3072\n" \
"v_smfmac_f32_16x16x64_bf16 a[192:195], a[200:203], v[68:75], v21\n" \
"ds_read_b128 a[156:159], v48 offset:7168\n" \
"v_add_u32_e32 v52, 0x14000, v63\n" \
"v_mfma_f32_16x16x32_bf16 v[220:223], a[188:191], v[84:87], v[220:223]\n" \
"buffer_load_dwordx4 v1, s[80:83], s92 offen offset:1024 lds\n" \
"v_mfma_f32_16x16x32_bf16 v[252:255], a[188:191], v[88:91], v[252:255]\n" \
"v_exp_f32_e32 v102, v102\n" \
"v_mfma_f32_16x16x32_bf16 v[216:219], a[184:187], v[84:87], v[216:219]\n" \
"buffer_load_dwordx4 v2, s[80:83], s92 offen offset:2048 lds\n" \
"v_mfma_f32_16x16x32_bf16 v[248:251], a[184:187], v[88:91], v[248:251]\n" \
"v_exp_f32_e32 v103, v103\n" \
"v_mfma_f32_16x16x32_bf16 v[212:215], a[180:183], v[84:87], v[212:215]\n" \
"v_add_u32_e32 v53, 0x20000, v63\n" \
"v_cvt_pk_bf16_f32 v103, v102, v103\n" \
"v_mfma_f32_16x16x32_bf16 v[244:247], a[180:183], v[88:91], v[244:247]\n" \
"v_exp_f32_e32 v100, v100\n" \
"v_mfma_f32_16x16x32_bf16 v[208:211], a[176:179], v[84:87], v[208:211]\n" \
"buffer_load_dwordx4 v3, s[80:83], s92 offen offset:3072 lds\n" \
"v_mfma_f32_16x16x32_bf16 v[240:243], a[176:179], v[88:91], v[240:243]\n" \
"v_exp_f32_e32 v101, v101\n" \
"v_mfma_f32_16x16x32_bf16 v[204:207], a[172:175], v[84:87], v[204:207]\n" \
"v_exp_f32_e32 v98, v98\n" \
"v_mfma_f32_16x16x32_bf16 v[236:239], a[172:175], v[88:91], v[236:239]\n" \
"v_exp_f32_e32 v99, v99\n" \
"v_mfma_f32_16x16x32_bf16 v[200:203], a[168:171], v[84:87], v[200:203]\n" \
"v_exp_f32_e32 v96, v96\n" \
"v_mfma_f32_16x16x32_bf16 v[232:235], a[168:171], v[88:91], v[232:235]\n" \
"v_exp_f32_e32 v97, v97\n" \
"v_mfma_f32_16x16x32_bf16 v[196:199], a[164:167], v[84:87], v[196:199]\n" \
"v_exp_f32_e32 v104, v104\n" \
"v_mfma_f32_16x16x32_bf16 v[228:231], a[164:167], v[88:91], v[228:231]\n" \
"v_exp_f32_e32 v105, v105\n" \
"v_mfma_f32_16x16x32_bf16 v[192:195], a[160:163], v[84:87], v[192:195]\n" \
"v_cvt_pk_bf16_f32 v102, v100, v101\n" \
"v_cvt_pk_bf16_f32 v101, v98, v99\n" \
"v_mfma_f32_16x16x32_bf16 v[224:227], a[160:163], v[88:91], v[224:227]\n" \
"v_cvt_pk_bf16_f32 v100, v96, v97\n" \
"v_cvt_pk_bf16_f32 v104, v104, v105\n" \
"v_smfmac_f32_16x16x64_bf16 a[196:199], a[200:203], v[84:91], v21\n" \
"v_exp_f32_e32 v106, v106\n" \
"s_waitcnt vmcnt(15) lgkmcnt(0)\n" \
"s_barrier\n" \
"v_mfma_f32_16x16x32_bf16 v[64:67], a[128:131], a[0:3], v[32:35]\n" \
"s_xor_b32 m0, s65, 0x18000\n" \
"ds_read_b128 a[160:163], v49 offset:8192\n" \
"v_mfma_f32_16x16x32_bf16 v[64:67], a[132:135], a[4:7], v[64:67]\n" \
"buffer_load_dwordx4 v52, s[8:11], s64 offen offset:0 sc1 lds\n" \
"v_mfma_f32_16x16x32_bf16 v[64:67], a[136:139], a[8:11], v[64:67]\n" \
"ds_read_b128 a[164:167], v49 offset:9216\n" \
"ds_read_b128 a[168:171], v49 offset:10240\n" \
"v_mfma_f32_16x16x32_bf16 v[64:67], a[140:143], a[12:15], v[64:67]\n" \
"ds_read_b128 a[172:175], v49 offset:11264\n" \
"ds_read_b128 a[176:179], v49 offset:12288\n" \
"v_mfma_f32_16x16x32_bf16 v[68:71], a[156:159], a[12:15], v[32:35]\n" \
"buffer_load_dwordx4 v52, s[8:11], s64 offen offset:1024 sc1 lds\n" \
"v_mfma_f32_16x16x32_bf16 v[68:71], a[152:155], a[8:11], v[68:71]\n" \
"ds_read_b128 a[180:183], v49 offset:13312\n" \
"ds_read_b128 a[184:187], v49 offset:14336\n" \
"v_mfma_f32_16x16x32_bf16 v[68:71], a[148:151], a[4:7], v[68:71]\n" \
"v_exp_f32_e32 v107, v107\n" \
"v_mfma_f32_16x16x32_bf16 v[68:71], a[144:147], a[0:3], v[68:71]\n" \
"v_exp_f32_e32 v108, v108\n" \
"v_mfma_f32_16x16x32_bf16 v[76:79], a[144:147], a[16:19], v[36:39]\n" \
"v_exp_f32_e32 v109, v109\n" \
"v_mfma_f32_16x16x32_bf16 v[76:79], a[148:151], a[20:23], v[76:79]\n" \
"buffer_load_dwordx4 v52, s[8:11], s64 offen offset:2048 sc1 lds\n" \
"v_mfma_f32_16x16x32_bf16 v[76:79], a[152:155], a[24:27], v[76:79]\n" \
"v_exp_f32_e32 v110, v110\n" \
"v_mfma_f32_16x16x32_bf16 v[76:79], a[156:159], a[28:31], v[76:79]\n" \
"v_exp_f32_e32 v111, v111\n" \
"v_mfma_f32_16x16x32_bf16 v[72:75], a[140:143], a[28:31], v[36:39]\n" \
"v_cvt_pk_bf16_f32 v105, v106, v107\n" \
"v_cvt_pk_bf16_f32 v106, v108, v109\n" \
"v_mfma_f32_16x16x32_bf16 v[72:75], a[136:139], a[24:27], v[72:75]\n" \
"buffer_load_dwordx4 v52, s[8:11], s64 offen offset:3072 sc1 lds\n" \
"v_mfma_f32_16x16x32_bf16 v[72:75], a[132:135], a[20:23], v[72:75]\n" \
"s_mov_b32 m0, s65\n" \
"v_cvt_pk_bf16_f32 v107, v110, v111\n" \
"v_mfma_f32_16x16x32_bf16 v[72:75], a[128:131], a[16:19], v[72:75]\n" \
"ds_read_b128 a[188:191], v49 offset:15360\n" \
"v_add_u32_e32 v49, 0x10000, v48\n" \
"v_mfma_f32_16x16x32_bf16 v[80:83], a[128:131], a[32:35], v[40:43]\n" \
"v_exp_f32_e32 v118, v118\n" \
"v_mfma_f32_16x16x32_bf16 v[80:83], a[132:135], a[36:39], v[80:83]\n" \
"v_exp_f32_e32 v119, v119\n" \
"v_mfma_f32_16x16x32_bf16 v[80:83], a[136:139], a[40:43], v[80:83]\n" \
"buffer_load_dwordx4 v53, s[8:11], s64 offen offset:0 sc1 lds\n" \
"v_mfma_f32_16x16x32_bf16 v[80:83], a[140:143], a[44:47], v[80:83]\n" \
"v_exp_f32_e32 v116, v116\n" \
"v_mfma_f32_16x16x32_bf16 v[84:87], a[156:159], a[44:47], v[40:43]\n" \
"v_exp_f32_e32 v117, v117\n" \
"v_mfma_f32_16x16x32_bf16 v[84:87], a[152:155], a[40:43], v[84:87]\n" \
"v_cvt_pk_bf16_f32 v119, v118, v119\n" \
"v_cvt_pk_bf16_f32 v118, v116, v117\n" \
"v_mfma_f32_16x16x32_bf16 v[84:87], a[148:151], a[36:39], v[84:87]\n" \
"v_exp_f32_e32 v114, v114\n" \
"v_mfma_f32_16x16x32_bf16 v[84:87], a[144:147], a[32:35], v[84:87]\n" \
"buffer_load_dwordx4 v53, s[8:11], s64 offen offset:1024 sc1 lds\n" \
"v_mfma_f32_16x16x32_bf16 v[92:95], a[144:147], a[48:51], v[44:47]\n" \
"v_exp_f32_e32 v115, v115\n" \
"v_mfma_f32_16x16x32_bf16 v[92:95], a[148:151], a[52:55], v[92:95]\n" \
"v_exp_f32_e32 v112, v112\n" \
"v_mfma_f32_16x16x32_bf16 v[92:95], a[152:155], a[56:59], v[92:95]\n" \
"v_exp_f32_e32 v113, v113\n" \
"v_mfma_f32_16x16x32_bf16 v[92:95], a[156:159], a[60:63], v[92:95]\n" \
"buffer_load_dwordx4 v53, s[8:11], s64 offen offset:2048 sc1 lds\n" \
"v_mfma_f32_16x16x32_bf16 v[88:91], a[140:143], a[60:63], v[44:47]\n" \
"v_cvt_pk_bf16_f32 v117, v114, v115\n" \
"v_cvt_pk_bf16_f32 v116, v112, v113\n" \
"v_mfma_f32_16x16x32_bf16 v[88:91], a[136:139], a[56:59], v[88:91]\n" \
"v_exp_f32_e32 v120, v120\n" \
"v_mfma_f32_16x16x32_bf16 v[88:91], a[132:135], a[52:55], v[88:91]\n" \
"v_exp_f32_e32 v121, v121\n" \
"v_mfma_f32_16x16x32_bf16 v[88:91], a[128:131], a[48:51], v[88:91]\n" \
"v_cvt_pk_bf16_f32 v120, v120, v121\n" \
"s_waitcnt lgkmcnt(0)\n" \
"v_mfma_f32_16x16x32_bf16 v[128:131], a[160:163], v[100:103], v[128:131]\n" \
"buffer_load_dwordx4 v53, s[8:11], s64 offen offset:3072 sc1 lds\n" \
"v_mfma_f32_16x16x32_bf16 v[160:163], a[160:163], v[104:107], v[160:163]\n" \
"s_add_u32 m0, s93, 0x1000\n" \
"ds_read_b128 a[128:131], v48 offset:8192\n" \
"v_mfma_f32_16x16x32_bf16 v[132:135], a[164:167], v[100:103], v[132:135]\n" \
"v_exp_f32_e32 v122, v122\n" \
"v_mfma_f32_16x16x32_bf16 v[164:167], a[164:167], v[104:107], v[164:167]\n" \
"v_exp_f32_e32 v123, v123\n" \
"v_mfma_f32_16x16x32_bf16 v[136:139], a[168:171], v[100:103], v[136:139]\n" \
"buffer_load_dwordx4 v4, s[80:83], s92 offen offset:0 lds\n" \
"v_mfma_f32_16x16x32_bf16 v[168:171], a[168:171], v[104:107], v[168:171]\n" \
"v_exp_f32_e32 v124, v124\n" \
"v_mfma_f32_16x16x32_bf16 v[140:143], a[172:175], v[100:103], v[140:143]\n" \
"v_exp_f32_e32 v125, v125\n" \
"v_mfma_f32_16x16x32_bf16 v[172:175], a[172:175], v[104:107], v[172:175]\n" \
"v_exp_f32_e32 v126, v126\n" \
"v_mfma_f32_16x16x32_bf16 v[144:147], a[176:179], v[100:103], v[144:147]\n" \
"v_exp_f32_e32 v127, v127\n" \
"v_mfma_f32_16x16x32_bf16 v[176:179], a[176:179], v[104:107], v[176:179]\n" \
"v_cvt_pk_bf16_f32 v121, v122, v123\n" \
"v_cvt_pk_bf16_f32 v122, v124, v125\n" \
"v_mfma_f32_16x16x32_bf16 v[148:151], a[180:183], v[100:103], v[148:151]\n" \
"buffer_load_dwordx4 v5, s[80:83], s92 offen offset:1024 lds\n" \
"v_mfma_f32_16x16x32_bf16 v[180:183], a[180:183], v[104:107], v[180:183]\n" \
"v_cvt_pk_bf16_f32 v123, v126, v127\n" \
"ds_read_b128 a[144:147], v48 offset:12288\n" \
"v_mfma_f32_16x16x32_bf16 v[152:155], a[184:187], v[100:103], v[152:155]\n" \
"ds_read_b128 a[132:135], v48 offset:9216\n" \
"ds_read_b128 a[148:151], v48 offset:13312\n" \
"v_mfma_f32_16x16x32_bf16 v[184:187], a[184:187], v[104:107], v[184:187]\n" \
"ds_read_b128 a[136:139], v48 offset:10240\n" \
"ds_read_b128 a[152:155], v48 offset:14336\n" \
"v_mfma_f32_16x16x32_bf16 v[156:159], a[188:191], v[100:103], v[156:159]\n" \
"ds_read_b128 a[140:143], v48 offset:11264\n" \
"ds_read_b128 a[156:159], v48 offset:15360\n" \
"v_mfma_f32_16x16x32_bf16 v[188:191], a[188:191], v[104:107], v[188:191]\n" \
"buffer_load_dwordx4 v6, s[80:83], s92 offen offset:2048 lds\n" \
"v_smfmac_f32_16x16x64_bf16 a[192:195], a[200:203], v[100:107], v21\n" \
"v_exp_f32_e32 v70, v70\n" \
"v_mfma_f32_16x16x32_bf16 v[220:223], a[188:191], v[116:119], v[220:223]\n" \
"v_exp_f32_e32 v71, v71\n" \
"v_mfma_f32_16x16x32_bf16 v[252:255], a[188:191], v[120:123], v[252:255]\n" \
"buffer_load_dwordx4 v7, s[80:83], s92 offen offset:3072 lds\n" \
"v_mfma_f32_16x16x32_bf16 v[216:219], a[184:187], v[116:119], v[216:219]\n" \
"s_add_u32 m0, s93, 0x2000\n" \
"v_cvt_pk_bf16_f32 v71, v70, v71\n" \
"v_mfma_f32_16x16x32_bf16 v[248:251], a[184:187], v[120:123], v[248:251]\n" \
"v_exp_f32_e32 v72, v72\n" \
"v_mfma_f32_16x16x32_bf16 v[212:215], a[180:183], v[116:119], v[212:215]\n" \
"v_exp_f32_e32 v73, v73\n" \
"v_mfma_f32_16x16x32_bf16 v[244:247], a[180:183], v[120:123], v[244:247]\n" \
"v_exp_f32_e32 v68, v68\n" \
"v_mfma_f32_16x16x32_bf16 v[208:211], a[176:179], v[116:119], v[208:211]\n" \
"buffer_load_dwordx4 v8, s[80:83], s92 offen offset:0 lds\n" \
"v_mfma_f32_16x16x32_bf16 v[240:243], a[176:179], v[120:123], v[240:243]\n" \
"v_exp_f32_e32 v69, v69\n" \
"v_mfma_f32_16x16x32_bf16 v[204:207], a[172:175], v[116:119], v[204:207]\n" \
"v_exp_f32_e32 v74, v74\n" \
"v_mfma_f32_16x16x32_bf16 v[236:239], a[172:175], v[120:123], v[236:239]\n" \
"v_exp_f32_e32 v75, v75\n" \
"v_mfma_f32_16x16x32_bf16 v[200:203], a[168:171], v[116:119], v[200:203]\n" \
"v_exp_f32_e32 v66, v66\n" \
"v_mfma_f32_16x16x32_bf16 v[232:235], a[168:171], v[120:123], v[232:235]\n" \
"v_exp_f32_e32 v67, v67\n" \
"v_mfma_f32_16x16x32_bf16 v[196:199], a[164:167], v[116:119], v[196:199]\n" \
"v_exp_f32_e32 v64, v64\n" \
"v_mfma_f32_16x16x32_bf16 v[228:231], a[164:167], v[120:123], v[228:231]\n" \
"v_exp_f32_e32 v65, v65\n" \
"v_mfma_f32_16x16x32_bf16 v[192:195], a[160:163], v[116:119], v[192:195]\n" \
"v_cvt_pk_bf16_f32 v72, v72, v73\n" \
"v_cvt_pk_bf16_f32 v70, v68, v69\n" \
"v_mfma_f32_16x16x32_bf16 v[224:227], a[160:163], v[120:123], v[224:227]\n" \
"v_exp_f32_e32 v76, v76\n" \
"v_smfmac_f32_16x16x64_bf16 a[196:199], a[200:203], v[116:123], v21\n" \
"v_exp_f32_e32 v77, v77\n" \
"s_waitcnt vmcnt(13) lgkmcnt(0)\n" \
"s_barrier\n" \
"v_exp_f32_e32 v78, v78\n" \
"v_exp_f32_e32 v79, v79\n" \
"v_cvt_pk_bf16_f32 v73, v74, v75\n" \
"v_cvt_pk_bf16_f32 v69, v66, v67\n" \
"v_cvt_pk_bf16_f32 v68, v64, v65\n" \
"v_cvt_pk_bf16_f32 v74, v76, v77\n" \
"v_cvt_pk_bf16_f32 v75, v78, v79\n" \
"v_add_u32_e32 v48, s16, v20\n" \
"s_xor_b32 s65, s65, 0x8000\n" \
"s_add_u32 s64, s64, 0x10000\n" \
"s_getpc_b64 s[86:87]\n" \
"L_hpt_%=:\n" \
"s_add_u32 s86, s86, L_ap8-L_hpt_%=\n" \
"s_addc_u32 s87, s87, 0\n" \
"s_add_u32 s86, s86, s15\n" \
"s_addc_u32 s87, s87, 0\n" \
"s_mov_b64 exec, 0\n" \
"s_swappc_b64 s[88:89], s[86:87]\n" \
"s_mov_b64 exec, -1\n" \
"s_sub_u32 s66, s19, 1\n" \
"s_cbranch_scc1 L_la_%=\n" \
".fill (L_ab + 6464 - .) / 4, 4, 0xBF800000\n" \
"L_2_%=:\n" \
"v_mfma_f32_16x16x32_bf16 v[96:99], a[128:131], a[64:67], v[32:35]\n" \
"s_xor_b32 m0, s65, 0x12000\n" \
"ds_read_b128 a[160:163], v49 offset:0\n" \
"v_mfma_f32_16x16x32_bf16 v[96:99], a[132:135], a[68:71], v[96:99]\n" \
"buffer_load_dwordx4 v50, s[8:11], s64 offen offset:0 sc1 lds\n" \
"v_mfma_f32_16x16x32_bf16 v[96:99], a[136:139], a[72:75], v[96:99]\n" \
"ds_read_b128 a[164:167], v49 offset:1024\n" \
"ds_read_b128 a[168:171], v49 offset:2048\n" \
"v_mfma_f32_16x16x32_bf16 v[96:99], a[140:143], a[76:79], v[96:99]\n" \
"ds_read_b128 a[172:175], v49 offset:3072\n" \
"ds_read_b128 a[176:179], v49 offset:4096\n" \
"v_mfma_f32_16x16x32_bf16 v[100:103], a[156:159], a[76:79], v[32:35]\n" \
"ds_read_b128 a[180:183], v49 offset:5120\n" \
"ds_read_b128 a[184:187], v49 offset:6144\n" \
"v_mfma_f32_16x16x32_bf16 v[100:103], a[152:155], a[72:75], v[100:103]\n" \
"v_exp_f32_e64 v86, v86\n" \
"v_mfma_f32_16x16x32_bf16 v[100:103], a[148:151], a[68:71], v[100:103]\n" \
"v_exp_f32_e64 v87, v87\n" \
"v_mfma_f32_16x16x32_bf16 v[100:103], a[144:147], a[64:67], v[100:103]\n" \
"buffer_load_dwordx4 v50, s[8:11], s64 offen offset:1024 sc1 lds\n" \
"v_mfma_f32_16x16x32_bf16 v[108:111], a[144:147], a[80:83], v[36:39]\n" \
"ds_read_b128 a[188:191], v49 offset:7168\n" \
"v_cvt_pk_bf16_f32 v87, v86, v87\n" \
"v_mfma_f32_16x16x32_bf16 v[108:111], a[148:151], a[84:87], v[108:111]\n" \
"v_exp_f32_e64 v84, v84\n" \
"v_mfma_f32_16x16x32_bf16 v[108:111], a[152:155], a[88:91], v[108:111]\n" \
"v_exp_f32_e64 v85, v85\n" \
"v_mfma_f32_16x16x32_bf16 v[108:111], a[156:159], a[92:95], v[108:111]\n" \
"v_exp_f32_e64 v82, v82\n" \
"v_mfma_f32_16x16x32_bf16 v[104:107], a[140:143], a[92:95], v[36:39]\n" \
"v_exp_f32_e64 v83, v83\n" \
"v_mfma_f32_16x16x32_bf16 v[104:107], a[136:139], a[88:91], v[104:107]\n" \
"v_exp_f32_e64 v80, v80\n" \
"v_mfma_f32_16x16x32_bf16 v[104:107], a[132:135], a[84:87], v[104:107]\n" \
"v_exp_f32_e64 v81, v81\n" \
"v_mfma_f32_16x16x32_bf16 v[104:107], a[128:131], a[80:83], v[104:107]\n" \
"buffer_load_dwordx4 v50, s[8:11], s64 offen offset:2048 sc1 lds\n" \
"v_mfma_f32_16x16x32_bf16 v[112:115], a[128:131], a[96:99], v[40:43]\n" \
"v_exp_f32_e64 v88, v88\n" \
"v_mfma_f32_16x16x32_bf16 v[112:115], a[132:135], a[100:103], v[112:115]\n" \
"v_exp_f32_e64 v89, v89\n" \
"v_mfma_f32_16x16x32_bf16 v[112:115], a[136:139], a[104:107], v[112:115]\n" \
"v_cvt_pk_bf16_f32 v86, v84, v85\n" \
"v_cvt_pk_bf16_f32 v85, v82, v83\n" \
"v_mfma_f32_16x16x32_bf16 v[112:115], a[140:143], a[108:111], v[112:115]\n" \
"v_cvt_pk_bf16_f32 v84, v80, v81\n" \
"v_cvt_pk_bf16_f32 v88, v88, v89\n" \
"v_mfma_f32_16x16x32_bf16 v[116:119], a[156:159], a[108:111], v[40:43]\n" \
"v_exp_f32_e64 v90, v90\n" \
"v_mfma_f32_16x16x32_bf16 v[116:119], a[152:155], a[104:107], v[116:119]\n" \
"v_exp_f32_e64 v91, v91\n" \
"v_mfma_f32_16x16x32_bf16 v[116:119], a[148:151], a[100:103], v[116:119]\n" \
"buffer_load_dwordx4 v50, s[8:11], s64 offen offset:3072 sc1 lds\n" \
"v_mfma_f32_16x16x32_bf16 v[116:119], a[144:147], a[96:99], v[116:119]\n" \
"s_xor_b32 m0, s65, 0xa000\n" \
"v_cvt_pk_bf16_f32 v89, v90, v91\n" \
"v_mfma_f32_16x16x32_bf16 v[124:127], a[144:147], a[112:115], v[44:47]\n" \
"v_exp_f32_e64 v92, v92\n" \
"v_mfma_f32_16x16x32_bf16 v[124:127], a[148:151], a[116:119], v[124:127]\n" \
"v_exp_f32_e64 v93, v93\n" \
"v_mfma_f32_16x16x32_bf16 v[124:127], a[152:155], a[120:123], v[124:127]\n" \
"v_exp_f32_e64 v94, v94\n" \
"v_mfma_f32_16x16x32_bf16 v[124:127], a[156:159], a[124:127], v[124:127]\n" \
"v_exp_f32_e64 v95, v95\n" \
"v_mfma_f32_16x16x32_bf16 v[120:123], a[140:143], a[124:127], v[44:47]\n" \
"v_cvt_pk_bf16_f32 v90, v92, v93\n" \
"v_cvt_pk_bf16_f32 v91, v94, v95\n" \
"v_mfma_f32_16x16x32_bf16 v[120:123], a[136:139], a[120:123], v[120:123]\n" \
"ds_read_b128 a[144:147], v48 offset:4096\n" \
"ds_read_b128 a[148:151], v48 offset:5120\n" \
"v_mfma_f32_16x16x32_bf16 v[120:123], a[132:135], a[116:119], v[120:123]\n" \
"buffer_load_dwordx4 v51, s[8:11], s64 offen offset:0 sc1 lds\n" \
"v_mfma_f32_16x16x32_bf16 v[120:123], a[128:131], a[112:115], v[120:123]\n" \
"ds_read_b128 a[128:131], v48 offset:0\n" \
"s_waitcnt lgkmcnt(3)\n" \
"v_mfma_f32_16x16x32_bf16 v[128:131], a[160:163], v[68:71], v[128:131]\n" \
"ds_read_b128 a[132:135], v48 offset:1024\n" \
"ds_read_b128 a[136:139], v48 offset:2048\n" \
"v_mfma_f32_16x16x32_bf16 v[160:163], a[160:163], v[72:75], v[160:163]\n" \
"ds_read_b128 a[152:155], v48 offset:6144\n" \
"ds_read_b128 a[140:143], v48 offset:3072\n" \
"v_mfma_f32_16x16x32_bf16 v[132:135], a[164:167], v[68:71], v[132:135]\n" \
"v_exp_f32_e32 v102, v102\n" \
"v_mfma_f32_16x16x32_bf16 v[164:167], a[164:167], v[72:75], v[164:167]\n" \
"v_exp_f32_e64 v103, v103\n" \
"v_mfma_f32_16x16x32_bf16 v[136:139], a[168:171], v[68:71], v[136:139]\n" \
"ds_read_b128 a[156:159], v48 offset:7168\n" \
"v_cvt_pk_bf16_f32 v103, v102, v103\n" \
"v_mfma_f32_16x16x32_bf16 v[168:171], a[168:171], v[72:75], v[168:171]\n" \
"buffer_load_dwordx4 v51, s[8:11], s64 offen offset:1024 sc1 lds\n" \
"v_mfma_f32_16x16x32_bf16 v[140:143], a[172:175], v[68:71], v[140:143]\n" \
"v_exp_f32_e64 v100, v100\n" \
"v_mfma_f32_16x16x32_bf16 v[172:175], a[172:175], v[72:75], v[172:175]\n" \
"v_exp_f32_e64 v101, v101\n" \
"v_mfma_f32_16x16x32_bf16 v[144:147], a[176:179], v[68:71], v[144:147]\n" \
"v_exp_f32_e64 v98, v98\n" \
"v_mfma_f32_16x16x32_bf16 v[176:179], a[176:179], v[72:75], v[176:179]\n" \
"v_exp_f32_e64 v99, v99\n" \
"v_mfma_f32_16x16x32_bf16 v[148:151], a[180:183], v[68:71], v[148:151]\n" \
"v_exp_f32_e64 v96, v96\n" \
"v_mfma_f32_16x16x32_bf16 v[180:183], a[180:183], v[72:75], v[180:183]\n" \
"v_exp_f32_e64 v97, v97\n" \
"v_mfma_f32_16x16x32_bf16 v[152:155], a[184:187], v[68:71], v[152:155]\n" \
"v_exp_f32_e64 v104, v104\n" \
"v_mfma_f32_16x16x32_bf16 v[184:187], a[184:187], v[72:75], v[184:187]\n" \
"buffer_load_dwordx4 v51, s[8:11], s64 offen offset:2048 sc1 lds\n" \
"v_mfma_f32_16x16x32_bf16 v[156:159], a[188:191], v[68:71], v[156:159]\n" \
"v_exp_f32_e64 v105, v105\n" \
"v_mfma_f32_16x16x32_bf16 v[188:191], a[188:191], v[72:75], v[188:191]\n" \
"v_cvt_pk_bf16_f32 v102, v100, v101\n" \
"v_cvt_pk_bf16_f32 v101, v98, v99\n" \
"v_smfmac_f32_16x16x64_bf16 a[192:195], a[200:203], v[68:75], v21\n" \
"v_cvt_pk_bf16_f32 v100, v96, v97\n" \
"v_cvt_pk_bf16_f32 v104, v104, v105\n" \
"v_mfma_f32_16x16x32_bf16 v[220:223], a[188:191], v[84:87], v[220:223]\n" \
"v_exp_f32_e64 v106, v106\n" \
"v_mfma_f32_16x16x32_bf16 v[252:255], a[188:191], v[88:91], v[252:255]\n" \
"v_exp_f32_e64 v107, v107\n" \
"v_mfma_f32_16x16x32_bf16 v[216:219], a[184:187], v[84:87], v[216:219]\n" \
"v_exp_f32_e64 v108, v108\n" \
"v_mfma_f32_16x16x32_bf16 v[248:251], a[184:187], v[88:91], v[248:251]\n" \
"buffer_load_dwordx4 v51, s[8:11], s64 offen offset:3072 sc1 lds\n" \
"v_mfma_f32_16x16x32_bf16 v[212:215], a[180:183], v[84:87], v[212:215]\n" \
"v_exp_f32_e64 v109, v109\n" \
"v_mfma_f32_16x16x32_bf16 v[244:247], a[180:183], v[88:91], v[244:247]\n" \
"v_exp_f32_e64 v110, v110\n" \
"v_mfma_f32_16x16x32_bf16 v[208:211], a[176:179], v[84:87], v[208:211]\n" \
"v_exp_f32_e64 v111, v111\n" \
"v_mfma_f32_16x16x32_bf16 v[240:243], a[176:179], v[88:91], v[240:243]\n" \
"v_exp_f32_e64 v118, v118\n" \
"v_mfma_f32_16x16x32_bf16 v[204:207], a[172:175], v[84:87], v[204:207]\n" \
"v_exp_f32_e64 v119, v119\n" \
"v_mfma_f32_16x16x32_bf16 v[236:239], a[172:175], v[88:91], v[236:239]\n" \
"v_cvt_pk_bf16_f32 v105, v106, v107\n" \
"v_cvt_pk_bf16_f32 v106, v108, v109\n" \
"v_mfma_f32_16x16x32_bf16 v[200:203], a[168:171], v[84:87], v[200:203]\n" \
"v_cvt_pk_bf16_f32 v107, v110, v111\n" \
"v_cvt_pk_bf16_f32 v119, v118, v119\n" \
"v_mfma_f32_16x16x32_bf16 v[232:235], a[168:171], v[88:91], v[232:235]\n" \
"v_exp_f32_e64 v116, v116\n" \
"v_mfma_f32_16x16x32_bf16 v[196:199], a[164:167], v[84:87], v[196:199]\n" \
"v_exp_f32_e64 v117, v117\n" \
"v_mfma_f32_16x16x32_bf16 v[228:231], a[164:167], v[88:91], v[228:231]\n" \
"v_exp_f32_e64 v114, v114\n" \
"v_mfma_f32_16x16x32_bf16 v[192:195], a[160:163], v[84:87], v[192:195]\n" \
"v_exp_f32_e64 v115, v115\n" \
"v_mfma_f32_16x16x32_bf16 v[224:227], a[160:163], v[88:91], v[224:227]\n" \
"v_exp_f32_e64 v112, v112\n" \
"v_smfmac_f32_16x16x64_bf16 a[196:199], a[200:203], v[84:91], v21\n" \
"v_exp_f32_e32 v113, v113\n" \
"s_waitcnt vmcnt(8) lgkmcnt(0)\n" \
"s_barrier\n" \
"s_nop 0\n" \
"v_mfma_f32_16x16x32_bf16 v[64:67], a[128:131], a[64:67], v[32:35]\n" \
"s_xor_b32 m0, s65, 0x18000\n" \
"ds_read_b128 a[160:163], v49 offset:8192\n" \
"v_mfma_f32_16x16x32_bf16 v[64:67], a[132:135], a[68:71], v[64:67]\n" \
"buffer_load_dwordx4 v52, s[8:11], s64 offen offset:0 sc1 lds\n" \
"v_mfma_f32_16x16x32_bf16 v[64:67], a[136:139], a[72:75], v[64:67]\n" \
"ds_read_b128 a[164:167], v49 offset:9216\n" \
"ds_read_b128 a[168:171], v49 offset:10240\n" \
"v_mfma_f32_16x16x32_bf16 v[64:67], a[140:143], a[76:79], v[64:67]\n" \
"ds_read_b128 a[172:175], v49 offset:11264\n" \
"ds_read_b128 a[176:179], v49 offset:12288\n" \
"v_mfma_f32_16x16x32_bf16 v[68:71], a[156:159], a[76:79], v[32:35]\n" \
"ds_read_b128 a[180:183], v49 offset:13312\n" \
"ds_read_b128 a[184:187], v49 offset:14336\n" \
"v_mfma_f32_16x16x32_bf16 v[68:71], a[152:155], a[72:75], v[68:71]\n" \
"ds_read_b128 a[188:191], v49 offset:15360\n" \
"v_cvt_pk_bf16_f32 v118, v116, v117\n" \
"v_mfma_f32_16x16x32_bf16 v[68:71], a[148:151], a[68:71], v[68:71]\n" \
"v_cvt_pk_bf16_f32 v117, v114, v115\n" \
"v_cvt_pk_bf16_f32 v116, v112, v113\n" \
"v_mfma_f32_16x16x32_bf16 v[68:71], a[144:147], a[64:67], v[68:71]\n" \
"buffer_load_dwordx4 v52, s[8:11], s64 offen offset:1024 sc1 lds\n" \
"v_mfma_f32_16x16x32_bf16 v[76:79], a[144:147], a[80:83], v[36:39]\n" \
"v_exp_f32_e64 v120, v120\n" \
"v_mfma_f32_16x16x32_bf16 v[76:79], a[148:151], a[84:87], v[76:79]\n" \
"v_exp_f32_e64 v121, v121\n" \
"v_mfma_f32_16x16x32_bf16 v[76:79], a[152:155], a[88:91], v[76:79]\n" \
"v_exp_f32_e64 v122, v122\n" \
"v_mfma_f32_16x16x32_bf16 v[76:79], a[156:159], a[92:95], v[76:79]\n" \
"v_exp_f32_e64 v123, v123\n" \
"v_mfma_f32_16x16x32_bf16 v[72:75], a[140:143], a[92:95], v[36:39]\n" \
"v_cvt_pk_bf16_f32 v120, v120, v121\n" \
"v_cvt_pk_bf16_f32 v121, v122, v123\n" \
"v_mfma_f32_16x16x32_bf16 v[72:75], a[136:139], a[88:91], v[72:75]\n" \
"v_exp_f32_e64 v124, v124\n" \
"v_mfma_f32_16x16x32_bf16 v[72:75], a[132:135], a[84:87], v[72:75]\n" \
"v_exp_f32_e64 v125, v125\n" \
"v_mfma_f32_16x16x32_bf16 v[72:75], a[128:131], a[80:83], v[72:75]\n" \
"buffer_load_dwordx4 v52, s[8:11], s64 offen offset:2048 sc1 lds\n" \
"v_mfma_f32_16x16x32_bf16 v[80:83], a[128:131], a[96:99], v[40:43]\n" \
"v_exp_f32_e64 v126, v126\n" \
"v_mfma_f32_16x16x32_bf16 v[80:83], a[132:135], a[100:103], v[80:83]\n" \
"v_exp_f32_e64 v127, v127\n" \
"v_mfma_f32_16x16x32_bf16 v[80:83], a[136:139], a[104:107], v[80:83]\n" \
"v_cvt_pk_bf16_f32 v122, v124, v125\n" \
"v_cvt_pk_bf16_f32 v123, v126, v127\n" \
"v_mfma_f32_16x16x32_bf16 v[80:83], a[140:143], a[108:111], v[80:83]\n" \
"v_exp_f32_e64 v70, v70\n" \
"v_mfma_f32_16x16x32_bf16 v[84:87], a[156:159], a[108:111], v[40:43]\n" \
"v_exp_f32_e64 v71, v71\n" \
"v_mfma_f32_16x16x32_bf16 v[84:87], a[152:155], a[104:107], v[84:87]\n" \
"v_exp_f32_e64 v72, v72\n" \
"v_mfma_f32_16x16x32_bf16 v[84:87], a[148:151], a[100:103], v[84:87]\n" \
"buffer_load_dwordx4 v52, s[8:11], s64 offen offset:3072 sc1 lds\n" \
"v_mfma_f32_16x16x32_bf16 v[84:87], a[144:147], a[96:99], v[84:87]\n" \
"s_mov_b32 m0, s65\n" \
"v_cvt_pk_bf16_f32 v71, v70, v71\n" \
"v_mfma_f32_16x16x32_bf16 v[92:95], a[144:147], a[112:115], v[44:47]\n" \
"ds_read_b128 a[144:147], v48 offset:12288\n" \
"v_add_u32_e32 v49, 0x10000, v48\n" \
"v_mfma_f32_16x16x32_bf16 v[92:95], a[148:151], a[116:119], v[92:95]\n" \
"ds_read_b128 a[148:151], v48 offset:13312\n" \
"s_xor_b32 s65, s65, 0x8000\n" \
"v_mfma_f32_16x16x32_bf16 v[92:95], a[152:155], a[120:123], v[92:95]\n" \
"v_exp_f32_e32 v73, v73\n" \
"v_mfma_f32_16x16x32_bf16 v[92:95], a[156:159], a[124:127], v[92:95]\n" \
"ds_read_b128 a[152:155], v48 offset:14336\n" \
".fill (L_ab + 8192 - .) / 4, 4, 0xBF800000\n" \
"L_ap2:\n" \
"s_cbranch_execz L_ap0\n" \
"ds_read_b128 a[156:159], v48 offset:15360\n" \
"v_mfma_f32_16x16x32_bf16 v[88:91], a[140:143], a[124:127], v[44:47]\n" \
"ds_read_b128 a[140:143], v48 offset:11264\n" \
"v_cvt_pk_bf16_f32 v72, v72, v73\n" \
"v_mfma_f32_16x16x32_bf16 v[88:91], a[136:139], a[120:123], v[88:91]\n" \
"v_exp_f32_e32 v68, v68\n" \
"v_mfma_f32_16x16x32_bf16 v[88:91], a[132:135], a[116:119], v[88:91]\n" \
"buffer_load_dwordx4 v53, s[8:11], s64 offen offset:0 sc1 lds\n" \
"v_mfma_f32_16x16x32_bf16 v[88:91], a[128:131], a[112:115], v[88:91]\n" \
"ds_read_b128 a[128:131], v48 offset:8192\n" \
"s_waitcnt lgkmcnt(6)\n" \
"v_mfma_f32_16x16x32_bf16 v[128:131], a[160:163], v[100:103], v[128:131]\n" \
"ds_read_b128 a[132:135], v48 offset:9216\n" \
"ds_read_b128 a[136:139], v48 offset:10240\n" \
"v_mfma_f32_16x16x32_bf16 v[160:163], a[160:163], v[104:107], v[160:163]\n" \
"v_exp_f32_e32 v69, v69\n" \
"v_mfma_f32_16x16x32_bf16 v[132:135], a[164:167], v[100:103], v[132:135]\n" \
"v_exp_f32_e64 v74, v74\n" \
"v_mfma_f32_16x16x32_bf16 v[164:167], a[164:167], v[104:107], v[164:167]\n" \
"v_exp_f32_e64 v75, v75\n" \
"v_mfma_f32_16x16x32_bf16 v[136:139], a[168:171], v[100:103], v[136:139]\n" \
"v_exp_f32_e64 v66, v66\n" \
"v_mfma_f32_16x16x32_bf16 v[168:171], a[168:171], v[104:107], v[168:171]\n" \
"buffer_load_dwordx4 v53, s[8:11], s64 offen offset:1024 sc1 lds\n" \
"v_mfma_f32_16x16x32_bf16 v[140:143], a[172:175], v[100:103], v[140:143]\n" \
"v_exp_f32_e64 v67, v67\n" \
"v_mfma_f32_16x16x32_bf16 v[172:175], a[172:175], v[104:107], v[172:175]\n" \
"v_exp_f32_e64 v64, v64\n" \
"v_mfma_f32_16x16x32_bf16 v[144:147], a[176:179], v[100:103], v[144:147]\n" \
"v_exp_f32_e64 v65, v65\n" \
"v_mfma_f32_16x16x32_bf16 v[176:179], a[176:179], v[104:107], v[176:179]\n" \
"v_exp_f32_e64 v76, v76\n" \
"v_mfma_f32_16x16x32_bf16 v[148:151], a[180:183], v[100:103], v[148:151]\n" \
"v_exp_f32_e64 v77, v77\n" \
"v_mfma_f32_16x16x32_bf16 v[180:183], a[180:183], v[104:107], v[180:183]\n" \
"v_exp_f32_e64 v78, v78\n" \
"v_mfma_f32_16x16x32_bf16 v[152:155], a[184:187], v[100:103], v[152:155]\n" \
"v_exp_f32_e64 v79, v79\n" \
"v_mfma_f32_16x16x32_bf16 v[184:187], a[184:187], v[104:107], v[184:187]\n" \
"buffer_load_dwordx4 v53, s[8:11], s64 offen offset:2048 sc1 lds\n" \
"v_mfma_f32_16x16x32_bf16 v[156:159], a[188:191], v[100:103], v[156:159]\n" \
"v_cvt_pk_bf16_f32 v70, v68, v69\n" \
"v_cvt_pk_bf16_f32 v73, v74, v75\n" \
"v_mfma_f32_16x16x32_bf16 v[188:191], a[188:191], v[104:107], v[188:191]\n" \
"v_cvt_pk_bf16_f32 v69, v66, v67\n" \
"v_cvt_pk_bf16_f32 v68, v64, v65\n" \
"v_smfmac_f32_16x16x64_bf16 a[192:195], a[200:203], v[100:107], v21\n" \
"v_cvt_pk_bf16_f32 v74, v76, v77\n" \
"v_cvt_pk_bf16_f32 v75, v78, v79\n" \
"v_mfma_f32_16x16x32_bf16 v[220:223], a[188:191], v[116:119], v[220:223]\n" \
"v_xor_b32_e32 v48, 0x8000, v48\n" \
"v_mfma_f32_16x16x32_bf16 v[252:255], a[188:191], v[120:123], v[252:255]\n" \
"v_mfma_f32_16x16x32_bf16 v[216:219], a[184:187], v[116:119], v[216:219]\n" \
"v_mfma_f32_16x16x32_bf16 v[248:251], a[184:187], v[120:123], v[248:251]\n" \
"buffer_load_dwordx4 v53, s[8:11], s64 offen offset:3072 sc1 lds\n" \
"v_mfma_f32_16x16x32_bf16 v[212:215], a[180:183], v[116:119], v[212:215]\n" \
"s_add_u32 s64, s64, 0x10000\n" \
"s_sub_u32 s66, s66, 1\n" \
"s_nop 0\n" \
"v_mfma_f32_16x16x32_bf16 v[244:247], a[180:183], v[120:123], v[244:247]\n" \
"v_mfma_f32_16x16x32_bf16 v[208:211], a[176:179], v[116:119], v[208:211]\n" \
"v_mfma_f32_16x16x32_bf16 v[240:243], a[176:179], v[120:123], v[240:243]\n" \
"v_mfma_f32_16x16x32_bf16 v[204:207], a[172:175], v[116:119], v[204:207]\n" \
"v_mfma_f32_16x16x32_bf16 v[236:239], a[172:175], v[120:123], v[236:239]\n" \
"v_mfma_f32_16x16x32_bf16 v[200:203], a[168:171], v[116:119], v[200:203]\n" \
"v_mfma_f32_16x16x32_bf16 v[232:235], a[168:171], v[120:123], v[232:235]\n" \
"v_mfma_f32_16x16x32_bf16 v[196:199], a[164:167], v[116:119], v[196:199]\n" \
"v_mfma_f32_16x16x32_bf16 v[228:231], a[164:167], v[120:123], v[228:231]\n" \
"v_mfma_f32_16x16x32_bf16 v[192:195], a[160:163], v[116:119], v[192:195]\n" \
"v_mfma_f32_16x16x32_bf16 v[224:227], a[160:163], v[120:123], v[224:227]\n" \
"v_smfmac_f32_16x16x64_bf16 a[196:199], a[200:203], v[116:123], v21\n" \
"s_waitcnt vmcnt(8) lgkmcnt(0)\n" \
"s_barrier\n" \
"s_cbranch_scc0 L_2_%=\n" \
"s_nop 0\n" \
"L_la_%=:\n" \
"v_mfma_f32_16x16x32_bf16 v[96:99], a[128:131], a[64:67], v[32:35]\n" \
"v_readlane_b32 s96, v11, 1\n" \
"v_bfe_u32 v27, v20, 2, 7\n" \
"v_mfma_f32_16x16x32_bf16 v[96:99], a[132:135], a[68:71], v[96:99]\n" \
"s_xor_b32 m0, s65, 0x12000\n" \
"ds_read_b128 a[160:163], v49 offset:0\n" \
"v_mfma_f32_16x16x32_bf16 v[96:99], a[136:139], a[72:75], v[96:99]\n" \
"buffer_load_dword v26, v27, s[56:59], s96 offen sc1\n" \
"v_mfma_f32_16x16x32_bf16 v[96:99], a[140:143], a[76:79], v[96:99]\n" \
"buffer_load_dwordx4 v50, s[8:11], s64 offen offset:0 sc1 lds\n" \
"v_mfma_f32_16x16x32_bf16 v[100:103], a[156:159], a[76:79], v[32:35]\n" \
"ds_read_b128 a[164:167], v49 offset:1024\n" \
"ds_read_b128 a[168:171], v49 offset:2048\n" \
"v_mfma_f32_16x16x32_bf16 v[100:103], a[152:155], a[72:75], v[100:103]\n" \
"ds_read_b128 a[172:175], v49 offset:3072\n" \
"ds_read_b128 a[176:179], v49 offset:4096\n" \
"v_mfma_f32_16x16x32_bf16 v[100:103], a[148:151], a[68:71], v[100:103]\n" \
"ds_read_b128 a[180:183], v49 offset:5120\n" \
"ds_read_b128 a[184:187], v49 offset:6144\n" \
"v_mfma_f32_16x16x32_bf16 v[100:103], a[144:147], a[64:67], v[100:103]\n" \
"ds_read_b128 a[188:191], v49 offset:7168\n" \
"v_readlane_b32 s97, v11, 36\n" \
"v_mfma_f32_16x16x32_bf16 v[108:111], a[144:147], a[80:83], v[36:39]\n" \
"v_exp_f32_e64 v86, v86\n" \
"v_mfma_f32_16x16x32_bf16 v[108:111], a[148:151], a[84:87], v[108:111]\n" \
"v_exp_f32_e64 v87, v87\n" \
"v_mfma_f32_16x16x32_bf16 v[108:111], a[152:155], a[88:91], v[108:111]\n" \
"buffer_load_dwordx4 v50, s[8:11], s64 offen offset:1024 sc1 lds\n" \
"v_mfma_f32_16x16x32_bf16 v[108:111], a[156:159], a[92:95], v[108:111]\n" \
"v_exp_f32_e64 v84, v84\n" \
"v_mfma_f32_16x16x32_bf16 v[104:107], a[140:143], a[92:95], v[36:39]\n" \
"v_exp_f32_e64 v85, v85\n" \
"v_mfma_f32_16x16x32_bf16 v[104:107], a[136:139], a[88:91], v[104:107]\n" \
"v_cvt_pk_bf16_f32 v87, v86, v87\n" \
"v_cvt_pk_bf16_f32 v86, v84, v85\n" \
"v_mfma_f32_16x16x32_bf16 v[104:107], a[132:135], a[84:87], v[104:107]\n" \
"v_exp_f32_e64 v82, v82\n" \
"v_mfma_f32_16x16x32_bf16 v[104:107], a[128:131], a[80:83], v[104:107]\n" \
"v_exp_f32_e64 v83, v83\n" \
"v_mfma_f32_16x16x32_bf16 v[112:115], a[128:131], a[96:99], v[40:43]\n" \
"v_exp_f32_e64 v80, v80\n" \
"v_mfma_f32_16x16x32_bf16 v[112:115], a[132:135], a[100:103], v[112:115]\n" \
"v_exp_f32_e64 v81, v81\n" \
"v_mfma_f32_16x16x32_bf16 v[112:115], a[136:139], a[104:107], v[112:115]\n" \
"buffer_load_dwordx4 v50, s[8:11], s64 offen offset:2048 sc1 lds\n" \
"v_mfma_f32_16x16x32_bf16 v[112:115], a[140:143], a[108:111], v[112:115]\n" \
"v_cvt_pk_bf16_f32 v85, v82, v83\n" \
"v_cvt_pk_bf16_f32 v84, v80, v81\n" \
"v_mfma_f32_16x16x32_bf16 v[116:119], a[156:159], a[108:111], v[40:43]\n" \
"v_exp_f32_e64 v88, v88\n" \
"v_mfma_f32_16x16x32_bf16 v[116:119], a[152:155], a[104:107], v[116:119]\n" \
"v_exp_f32_e64 v89, v89\n" \
"v_mfma_f32_16x16x32_bf16 v[116:119], a[148:151], a[100:103], v[116:119]\n" \
"v_exp_f32_e64 v90, v90\n" \
"v_mfma_f32_16x16x32_bf16 v[116:119], a[144:147], a[96:99], v[116:119]\n" \
"v_exp_f32_e64 v91, v91\n" \
"v_mfma_f32_16x16x32_bf16 v[124:127], a[144:147], a[112:115], v[44:47]\n" \
"v_cvt_pk_bf16_f32 v88, v88, v89\n" \
"v_cvt_pk_bf16_f32 v89, v90, v91\n" \
"v_mfma_f32_16x16x32_bf16 v[124:127], a[148:151], a[116:119], v[124:127]\n" \
"buffer_load_dwordx4 v50, s[8:11], s64 offen offset:3072 sc1 lds\n" \
"v_mfma_f32_16x16x32_bf16 v[124:127], a[152:155], a[120:123], v[124:127]\n" \
"v_exp_f32_e64 v92, v92\n" \
"v_mfma_f32_16x16x32_bf16 v[124:127], a[156:159], a[124:127], v[124:127]\n" \
"v_exp_f32_e64 v93, v93\n" \
"v_mfma_f32_16x16x32_bf16 v[120:123], a[140:143], a[124:127], v[44:47]\n" \
"v_exp_f32_e64 v94, v94\n" \
"v_mfma_f32_16x16x32_bf16 v[120:123], a[136:139], a[120:123], v[120:123]\n" \
"v_exp_f32_e64 v95, v95\n" \
"v_mfma_f32_16x16x32_bf16 v[120:123], a[132:135], a[116:119], v[120:123]\n" \
"v_cvt_pk_bf16_f32 v90, v92, v93\n" \
"v_cvt_pk_bf16_f32 v91, v94, v95\n" \
"v_mfma_f32_16x16x32_bf16 v[120:123], a[128:131], a[112:115], v[120:123]\n" \
"ds_read_b128 a[128:131], v48 offset:0\n" \
"s_waitcnt lgkmcnt(1)\n" \
"s_nop 0\n" \
"v_mfma_f32_16x16x32_bf16 v[128:131], a[160:163], v[68:71], v[128:131]\n" \
"ds_read_b128 a[144:147], v48 offset:4096\n" \
"ds_read_b128 a[132:135], v48 offset:1024\n" \
"v_mfma_f32_16x16x32_bf16 v[160:163], a[160:163], v[72:75], v[160:163]\n" \
"ds_read_b128 a[148:151], v48 offset:5120\n" \
"ds_read_b128 a[136:139], v48 offset:2048\n" \
"v_mfma_f32_16x16x32_bf16 v[132:135], a[164:167], v[68:71], v[132:135]\n" \
"ds_read_b128 a[152:155], v48 offset:6144\n" \
"ds_read_b128 a[140:143], v48 offset:3072\n" \
"v_mfma_f32_16x16x32_bf16 v[164:167], a[164:167], v[72:75], v[164:167]\n" \
"ds_read_b128 a[156:159], v48 offset:7168\n" \
"v_add_u32_e32 v17, 0x49000, v20\n" \
"v_mfma_f32_16x16x32_bf16 v[136:139], a[168:171], v[68:71], v[136:139]\n" \
"v_readlane_b32 s98, v11, 37\n" \
"v_readlane_b32 s93, v11, 20\n" \
"v_mfma_f32_16x16x32_bf16 v[168:171], a[168:171], v[72:75], v[168:171]\n" \
"v_readlane_b32 s84, v11, 15\n" \
"v_readlane_b32 s85, v11, 16\n" \
"v_mfma_f32_16x16x32_bf16 v[140:143], a[172:175], v[68:71], v[140:143]\n" \
"v_readlane_b32 s86, v11, 17\n" \
"s_mov_b32 s87, 0x20000\n" \
"v_mfma_f32_16x16x32_bf16 v[172:175], a[172:175], v[72:75], v[172:175]\n" \
"v_readlane_b32 s92, v11, 18\n" \
"v_readlane_b32 s91, v11, 21\n" \
"v_mfma_f32_16x16x32_bf16 v[144:147], a[176:179], v[68:71], v[144:147]\n" \
"v_exp_f32_e64 v102, v102\n" \
"v_mfma_f32_16x16x32_bf16 v[176:179], a[176:179], v[72:75], v[176:179]\n" \
"v_exp_f32_e64 v103, v103\n" \
"v_mfma_f32_16x16x32_bf16 v[148:151], a[180:183], v[68:71], v[148:151]\n" \
"v_exp_f32_e64 v100, v100\n" \
"v_mfma_f32_16x16x32_bf16 v[180:183], a[180:183], v[72:75], v[180:183]\n" \
"v_exp_f32_e64 v101, v101\n" \
"v_mfma_f32_16x16x32_bf16 v[152:155], a[184:187], v[68:71], v[152:155]\n" \
"v_cvt_pk_bf16_f32 v103, v102, v103\n" \
"v_cvt_pk_bf16_f32 v102, v100, v101\n" \
"v_mfma_f32_16x16x32_bf16 v[184:187], a[184:187], v[72:75], v[184:187]\n" \
"v_exp_f32_e64 v98, v98\n" \
"v_mfma_f32_16x16x32_bf16 v[156:159], a[188:191], v[68:71], v[156:159]\n" \
"v_exp_f32_e64 v99, v99\n" \
"v_mfma_f32_16x16x32_bf16 v[188:191], a[188:191], v[72:75], v[188:191]\n" \
"s_waitcnt vmcnt(4)\n" \
"v_cmp_ne_u32_e32 vcc, s62, v26\n" \
"v_smfmac_f32_16x16x64_bf16 a[192:195], a[200:203], v[68:75], v21\n" \
"v_exp_f32_e64 v96, v96\n" \
"v_mfma_f32_16x16x32_bf16 v[220:223], a[188:191], v[84:87], v[220:223]\n" \
"v_exp_f32_e64 v97, v97\n" \
"v_mfma_f32_16x16x32_bf16 v[252:255], a[188:191], v[88:91], v[252:255]\n" \
"s_cbranch_vccnz L_sol_%=\n" \
"L_rol_%=:\n" \
"v_cvt_pk_bf16_f32 v101, v98, v99\n" \
"buffer_load_dwordx4 a[220:223], v20, s[56:59], s97 offen offset:0 sc1\n" \
"v_mfma_f32_16x16x32_bf16 v[216:219], a[184:187], v[84:87], v[216:219]\n" \
"v_cvt_pk_bf16_f32 v100, v96, v97\n" \
"s_xor_b32 s65, s65, 0x8000\n" \
"v_mfma_f32_16x16x32_bf16 v[248:251], a[184:187], v[88:91], v[248:251]\n" \
"v_exp_f32_e32 v104, v104\n" \
"v_mfma_f32_16x16x32_bf16 v[212:215], a[180:183], v[84:87], v[212:215]\n" \
"v_exp_f32_e64 v105, v105\n" \
"v_mfma_f32_16x16x32_bf16 v[244:247], a[180:183], v[88:91], v[244:247]\n" \
"v_exp_f32_e64 v106, v106\n" \
"v_mfma_f32_16x16x32_bf16 v[208:211], a[176:179], v[84:87], v[208:211]\n" \
"v_exp_f32_e64 v107, v107\n" \
"v_mfma_f32_16x16x32_bf16 v[240:243], a[176:179], v[88:91], v[240:243]\n" \
"buffer_load_dwordx4 a[228:231], v17, s[56:59], s97 offen offset:0 sc1\n" \
"v_mfma_f32_16x16x32_bf16 v[204:207], a[172:175], v[84:87], v[204:207]\n" \
"v_cvt_pk_bf16_f32 v104, v104, v105\n" \
"v_cvt_pk_bf16_f32 v105, v106, v107\n" \
"v_mfma_f32_16x16x32_bf16 v[236:239], a[172:175], v[88:91], v[236:239]\n" \
"v_exp_f32_e64 v108, v108\n" \
"v_mfma_f32_16x16x32_bf16 v[200:203], a[168:171], v[84:87], v[200:203]\n" \
"v_exp_f32_e64 v109, v109\n" \
"v_mfma_f32_16x16x32_bf16 v[232:235], a[168:171], v[88:91], v[232:235]\n" \
"v_exp_f32_e64 v110, v110\n" \
"v_mfma_f32_16x16x32_bf16 v[196:199], a[164:167], v[84:87], v[196:199]\n" \
"v_exp_f32_e64 v111, v111\n" \
"v_mfma_f32_16x16x32_bf16 v[228:231], a[164:167], v[88:91], v[228:231]\n" \
"v_cvt_pk_bf16_f32 v106, v108, v109\n" \
"v_cvt_pk_bf16_f32 v107, v110, v111\n" \
"v_mfma_f32_16x16x32_bf16 v[192:195], a[160:163], v[84:87], v[192:195]\n" \
"v_exp_f32_e64 v118, v118\n" \
"v_mfma_f32_16x16x32_bf16 v[224:227], a[160:163], v[88:91], v[224:227]\n" \
"v_exp_f32_e64 v119, v119\n" \
"v_smfmac_f32_16x16x64_bf16 a[196:199], a[200:203], v[84:91], v21\n" \
"buffer_load_dwordx4 a[236:239], v20, s[56:59], s98 offen offset:0 sc1\n" \
"s_waitcnt vmcnt(8) lgkmcnt(0)\n" \
"s_barrier\n" \
"v_mfma_f32_16x16x32_bf16 v[64:67], a[128:131], a[64:67], v[32:35]\n" \
"s_add_u32 m0, s93, 0x0\n" \
"s_nop 0\n" \
"ds_read_b128 a[160:163], v49 offset:8192\n" \
"v_mfma_f32_16x16x32_bf16 v[64:67], a[132:135], a[68:71], v[64:67]\n" \
"buffer_load_dwordx4 v0, s[84:87], s92 offen offset:0 lds\n" \
"v_mfma_f32_16x16x32_bf16 v[64:67], a[136:139], a[72:75], v[64:67]\n" \
"ds_read_b128 a[164:167], v49 offset:9216\n" \
"ds_read_b128 a[168:171], v49 offset:10240\n" \
"v_mfma_f32_16x16x32_bf16 v[64:67], a[140:143], a[76:79], v[64:67]\n" \
"ds_read_b128 a[172:175], v49 offset:11264\n" \
"ds_read_b128 a[176:179], v49 offset:12288\n" \
"v_mfma_f32_16x16x32_bf16 v[68:71], a[156:159], a[76:79], v[32:35]\n" \
"ds_read_b128 a[180:183], v49 offset:13312\n" \
"ds_read_b128 a[184:187], v49 offset:14336\n" \
"v_mfma_f32_16x16x32_bf16 v[68:71], a[152:155], a[72:75], v[68:71]\n" \
"ds_read_b128 a[188:191], v49 offset:15360\n" \
"v_cvt_pk_bf16_f32 v119, v118, v119\n" \
"v_mfma_f32_16x16x32_bf16 v[68:71], a[148:151], a[68:71], v[68:71]\n" \
"buffer_load_dwordx4 v1, s[84:87], s92 offen offset:1024 lds\n" \
"v_mfma_f32_16x16x32_bf16 v[68:71], a[144:147], a[64:67], v[68:71]\n" \
"buffer_load_dwordx4 a[244:247], v17, s[56:59], s98 offen offset:0 sc1\n" \
"v_mfma_f32_16x16x32_bf16 v[76:79], a[144:147], a[80:83], v[36:39]\n" \
"v_exp_f32_e64 v116, v116\n" \
"v_mfma_f32_16x16x32_bf16 v[76:79], a[148:151], a[84:87], v[76:79]\n" \
"v_exp_f32_e64 v117, v117\n" \
"v_mfma_f32_16x16x32_bf16 v[76:79], a[152:155], a[88:91], v[76:79]\n" \
"v_exp_f32_e64 v114, v114\n" \
"v_mfma_f32_16x16x32_bf16 v[76:79], a[156:159], a[92:95], v[76:79]\n" \
"v_exp_f32_e64 v115, v115\n" \
"v_mfma_f32_16x16x32_bf16 v[72:75], a[140:143], a[92:95], v[36:39]\n" \
"v_exp_f32_e64 v112, v112\n" \
"v_mfma_f32_16x16x32_bf16 v[72:75], a[136:139], a[88:91], v[72:75]\n" \
"buffer_load_dwordx4 v2, s[84:87], s92 offen offset:2048 lds\n" \
"v_mfma_f32_16x16x32_bf16 v[72:75], a[132:135], a[84:87], v[72:75]\n" \
"v_exp_f32_e64 v113, v113\n" \
"v_mfma_f32_16x16x32_bf16 v[72:75], a[128:131], a[80:83], v[72:75]\n" \
"v_cvt_pk_bf16_f32 v118, v116, v117\n" \
"v_cvt_pk_bf16_f32 v117, v114, v115\n" \
"v_mfma_f32_16x16x32_bf16 v[80:83], a[128:131], a[96:99], v[40:43]\n" \
"buffer_load_dwordx4 a[224:227], v20, s[56:59], s97 offen offset:1024 sc1\n" \
"v_mfma_f32_16x16x32_bf16 v[80:83], a[132:135], a[100:103], v[80:83]\n" \
"v_cvt_pk_bf16_f32 v116, v112, v113\n" \
"v_add_u32_e32 v49, 0x10000, v48\n" \
"v_mfma_f32_16x16x32_bf16 v[80:83], a[136:139], a[104:107], v[80:83]\n" \
"v_exp_f32_e64 v120, v120\n" \
"v_mfma_f32_16x16x32_bf16 v[80:83], a[140:143], a[108:111], v[80:83]\n" \
"v_exp_f32_e64 v121, v121\n" \
"v_mfma_f32_16x16x32_bf16 v[84:87], a[156:159], a[108:111], v[40:43]\n" \
"buffer_load_dwordx4 v3, s[84:87], s92 offen offset:3072 lds\n" \
"v_mfma_f32_16x16x32_bf16 v[84:87], a[152:155], a[104:107], v[84:87]\n" \
"s_add_u32 m0, s91, 0x1000\n" \
"v_cvt_pk_bf16_f32 v120, v120, v121\n" \
"v_mfma_f32_16x16x32_bf16 v[84:87], a[148:151], a[100:103], v[84:87]\n" \
"v_exp_f32_e64 v122, v122\n" \
"v_mfma_f32_16x16x32_bf16 v[84:87], a[144:147], a[96:99], v[84:87]\n" \
"v_exp_f32_e64 v123, v123\n" \
"v_mfma_f32_16x16x32_bf16 v[92:95], a[144:147], a[112:115], v[44:47]\n" \
"v_exp_f32_e64 v124, v124\n" \
"v_mfma_f32_16x16x32_bf16 v[92:95], a[148:151], a[116:119], v[92:95]\n" \
"buffer_load_dwordx4 a[232:235], v17, s[56:59], s97 offen offset:1024 sc1\n" \
"v_mfma_f32_16x16x32_bf16 v[92:95], a[152:155], a[120:123], v[92:95]\n" \
"buffer_load_dwordx4 v4, s[84:87], s92 offen offset:0 lds\n" \
"v_mfma_f32_16x16x32_bf16 v[92:95], a[156:159], a[124:127], v[92:95]\n" \
"v_exp_f32_e64 v125, v125\n" \
"v_mfma_f32_16x16x32_bf16 v[88:91], a[140:143], a[124:127], v[44:47]\n" \
"v_exp_f32_e64 v126, v126\n" \
"v_mfma_f32_16x16x32_bf16 v[88:91], a[136:139], a[120:123], v[88:91]\n" \
"v_exp_f32_e64 v127, v127\n" \
"v_mfma_f32_16x16x32_bf16 v[88:91], a[132:135], a[116:119], v[88:91]\n" \
"v_cvt_pk_bf16_f32 v121, v122, v123\n" \
"v_cvt_pk_bf16_f32 v122, v124, v125\n" \
"v_mfma_f32_16x16x32_bf16 v[88:91], a[128:131], a[112:115], v[88:91]\n" \
"v_cvt_pk_bf16_f32 v123, v126, v127\n" \
"s_waitcnt lgkmcnt(0)\n" \
"s_nop 0\n" \
"v_mfma_f32_16x16x32_bf16 v[128:131], a[160:163], v[100:103], v[128:131]\n" \
"ds_read_b128 a[128:131], v48 offset:8192\n" \
"ds_read_b128 a[144:147], v48 offset:12288\n" \
"v_mfma_f32_16x16x32_bf16 v[160:163], a[160:163], v[104:107], v[160:163]\n" \
"buffer_load_dwordx4 v5, s[84:87], s92 offen offset:1024 lds\n" \
"v_mfma_f32_16x16x32_bf16 v[132:135], a[164:167], v[100:103], v[132:135]\n" \
"buffer_load_dwordx4 a[240:243], v20, s[56:59], s98 offen offset:1024 sc1\n" \
"v_mfma_f32_16x16x32_bf16 v[164:167], a[164:167], v[104:107], v[164:167]\n" \
"ds_read_b128 a[132:135], v48 offset:9216\n" \
"ds_read_b128 a[148:151], v48 offset:13312\n" \
"v_mfma_f32_16x16x32_bf16 v[136:139], a[168:171], v[100:103], v[136:139]\n" \
"ds_read_b128 a[136:139], v48 offset:10240\n" \
"ds_read_b128 a[152:155], v48 offset:14336\n" \
"v_mfma_f32_16x16x32_bf16 v[168:171], a[168:171], v[104:107], v[168:171]\n" \
"ds_read_b128 a[140:143], v48 offset:11264\n" \
"ds_read_b128 a[156:159], v48 offset:15360\n" \
"v_mfma_f32_16x16x32_bf16 v[140:143], a[172:175], v[100:103], v[140:143]\n" \
"v_exp_f32_e64 v70, v70\n" \
"v_mfma_f32_16x16x32_bf16 v[172:175], a[172:175], v[104:107], v[172:175]\n" \
"v_exp_f32_e64 v71, v71\n" \
"v_mfma_f32_16x16x32_bf16 v[144:147], a[176:179], v[100:103], v[144:147]\n" \
"buffer_load_dwordx4 v6, s[84:87], s92 offen offset:2048 lds\n" \
"v_mfma_f32_16x16x32_bf16 v[176:179], a[176:179], v[104:107], v[176:179]\n" \
"v_exp_f32_e64 v72, v72\n" \
"v_mfma_f32_16x16x32_bf16 v[148:151], a[180:183], v[100:103], v[148:151]\n" \
"v_exp_f32_e64 v73, v73\n" \
"v_mfma_f32_16x16x32_bf16 v[180:183], a[180:183], v[104:107], v[180:183]\n" \
"buffer_load_dwordx4 a[248:251], v17, s[56:59], s98 offen offset:1024 sc1\n" \
"v_mfma_f32_16x16x32_bf16 v[152:155], a[184:187], v[100:103], v[152:155]\n" \
"v_exp_f32_e64 v68, v68\n" \
"v_mfma_f32_16x16x32_bf16 v[184:187], a[184:187], v[104:107], v[184:187]\n" \
"v_exp_f32_e64 v69, v69\n" \
"v_mfma_f32_16x16x32_bf16 v[156:159], a[188:191], v[100:103], v[156:159]\n" \
"buffer_load_dwordx4 v7, s[84:87], s92 offen offset:3072 lds\n" \
"v_mfma_f32_16x16x32_bf16 v[188:191], a[188:191], v[104:107], v[188:191]\n" \
"s_add_u32 m0, s91, 0x2000\n" \
"v_cvt_pk_bf16_f32 v71, v70, v71\n" \
"v_smfmac_f32_16x16x64_bf16 a[192:195], a[200:203], v[100:107], v21\n" \
"v_exp_f32_e64 v74, v74\n" \
"v_mfma_f32_16x16x32_bf16 v[220:223], a[188:191], v[116:119], v[220:223]\n" \
"v_exp_f32_e64 v75, v75\n" \
"v_mfma_f32_16x16x32_bf16 v[252:255], a[188:191], v[120:123], v[252:255]\n" \
"v_exp_f32_e64 v66, v66\n" \
"v_mfma_f32_16x16x32_bf16 v[216:219], a[184:187], v[116:119], v[216:219]\n" \
"v_exp_f32_e64 v67, v67\n" \
"v_mfma_f32_16x16x32_bf16 v[248:251], a[184:187], v[120:123], v[248:251]\n" \
"v_exp_f32_e64 v64, v64\n" \
"v_mfma_f32_16x16x32_bf16 v[212:215], a[180:183], v[116:119], v[212:215]\n" \
"buffer_load_dwordx4 v8, s[84:87], s92 offen offset:0 lds\n" \
"v_mfma_f32_16x16x32_bf16 v[244:247], a[180:183], v[120:123], v[244:247]\n" \
"v_exp_f32_e64 v65, v65\n" \
"v_mfma_f32_16x16x32_bf16 v[208:211], a[176:179], v[116:119], v[208:211]\n" \
"v_cvt_pk_bf16_f32 v72, v72, v73\n" \
"v_cvt_pk_bf16_f32 v70, v68, v69\n" \
"v_mfma_f32_16x16x32_bf16 v[240:243], a[176:179], v[120:123], v[240:243]\n" \
"v_exp_f32_e64 v76, v76\n" \
"v_mfma_f32_16x16x32_bf16 v[204:207], a[172:175], v[116:119], v[204:207]\n" \
"v_exp_f32_e64 v77, v77\n" \
"v_mfma_f32_16x16x32_bf16 v[236:239], a[172:175], v[120:123], v[236:239]\n" \
"v_exp_f32_e64 v78, v78\n" \
"v_mfma_f32_16x16x32_bf16 v[200:203], a[168:171], v[116:119], v[200:203]\n" \
"v_exp_f32_e64 v79, v79\n" \
"v_mfma_f32_16x16x32_bf16 v[232:235], a[168:171], v[120:123], v[232:235]\n" \
"v_cvt_pk_bf16_f32 v73, v74, v75\n" \
"v_cvt_pk_bf16_f32 v69, v66, v67\n" \
"v_mfma_f32_16x16x32_bf16 v[196:199], a[164:167], v[116:119], v[196:199]\n" \
"v_cvt_pk_bf16_f32 v68, v64, v65\n" \
"v_cvt_pk_bf16_f32 v74, v76, v77\n" \
"v_mfma_f32_16x16x32_bf16 v[228:231], a[164:167], v[120:123], v[228:231]\n" \
"v_cvt_pk_bf16_f32 v75, v78, v79\n" \
"v_xor_b32_e32 v48, 0x8000, v48\n" \
"v_mfma_f32_16x16x32_bf16 v[192:195], a[160:163], v[116:119], v[192:195]\n" \
"s_add_u32 s64, s64, 0x10000\n" \
"v_mfma_f32_16x16x32_bf16 v[224:227], a[160:163], v[120:123], v[224:227]\n" \
"v_smfmac_f32_16x16x64_bf16 a[196:199], a[200:203], v[116:123], v21\n" \
"s_waitcnt vmcnt(14) lgkmcnt(0)\n" \
"s_barrier\n" \
"s_cmp_lg_u32 s16, 0\n" \
"s_cbranch_scc1 L_f1_%=\n" \
"ds_read_b128 a[160:163], v49 offset:0\n" \
"ds_read_b128 a[164:167], v49 offset:1024\n" \
"ds_read_b128 a[168:171], v49 offset:2048\n" \
"ds_read_b128 a[172:175], v49 offset:3072\n" \
"ds_read_b128 a[176:179], v49 offset:4096\n" \
"ds_read_b128 a[180:183], v49 offset:5120\n" \
"ds_read_b128 a[184:187], v49 offset:6144\n" \
"ds_read_b128 a[188:191], v49 offset:7168\n" \
"v_add_u32_e32 v54, 0x49000, v20\n" \
"buffer_load_dwordx4 a[96:99], v20, s[56:59], s60 offen offset:0 sc1\n" \
"v_add_u32_e32 v50, s15, v20\n" \
"v_add_u32_e32 v51, 0x10000, v50\n" \
"ds_read_b32 v60, v18 offset:0\n" \
"ds_read_b32 v61, v18 offset:128\n" \
"ds_read_b128 a[0:3], v49 offset:8192\n" \
"ds_read_b128 a[4:7], v49 offset:9216\n" \
"ds_read_b128 a[8:11], v49 offset:10240\n" \
"ds_read_b128 a[12:15], v49 offset:11264\n" \
"ds_read_b128 a[16:19], v49 offset:12288\n" \
"ds_read_b128 a[20:23], v49 offset:13312\n" \
"ds_read_b128 a[24:27], v49 offset:14336\n" \
"ds_read_b128 a[28:31], v49 offset:15360\n" \
"v_exp_f32_e32 v86, v86\n" \
"v_exp_f32_e32 v87, v87\n" \
"v_exp_f32_e32 v84, v84\n" \
"v_exp_f32_e32 v85, v85\n" \
"v_cvt_pk_bf16_f32 v87, v86, v87\n" \
"buffer_load_dwordx4 a[100:103], v20, s[56:59], s60 offen offset:1024 sc1\n" \
"v_exp_f32_e32 v82, v82\n" \
"v_exp_f32_e32 v83, v83\n" \
"v_exp_f32_e32 v80, v80\n" \
"v_exp_f32_e32 v81, v81\n" \
"s_waitcnt lgkmcnt(15)\n" \
"v_mfma_f32_16x16x32_bf16 v[128:131], a[160:163], v[68:71], v[128:131]\n" \
"v_cvt_pk_bf16_f32 v86, v84, v85\n" \
"v_cvt_pk_bf16_f32 v85, v82, v83\n" \
"v_mfma_f32_16x16x32_bf16 v[160:163], a[160:163], v[72:75], v[160:163]\n" \
"v_cvt_pk_bf16_f32 v84, v80, v81\n" \
"v_and_b32_e32 v84, v12, v84\n" \
"v_mfma_f32_16x16x32_bf16 v[132:135], a[164:167], v[68:71], v[132:135]\n" \
"v_and_b32_e32 v85, v13, v85\n" \
"v_and_b32_e32 v86, v14, v86\n" \
"v_mfma_f32_16x16x32_bf16 v[164:167], a[164:167], v[72:75], v[164:167]\n" \
"buffer_load_dwordx4 a[104:107], v20, s[56:59], s60 offen offset:2048 sc1\n" \
"v_mfma_f32_16x16x32_bf16 v[136:139], a[168:171], v[68:71], v[136:139]\n" \
"v_cvt_pk_bf16_f32 v128, v128, v129\n" \
"v_cvt_pk_bf16_f32 v160, v160, v161\n" \
"v_mfma_f32_16x16x32_bf16 v[168:171], a[168:171], v[72:75], v[168:171]\n" \
"v_cvt_pk_bf16_f32 v129, v130, v131\n" \
"s_waitcnt lgkmcnt(10)\n" \
"v_mfma_f32_16x16x32_bf16 v[140:143], a[172:175], v[68:71], v[140:143]\n" \
"v_cvt_pk_bf16_f32 v161, v162, v163\n" \
"v_cvt_pk_bf16_f32 v130, v132, v133\n" \
"v_mfma_f32_16x16x32_bf16 v[172:175], a[172:175], v[72:75], v[172:175]\n" \
"v_cvt_pk_bf16_f32 v131, v134, v135\n" \
"v_cvt_pk_bf16_f32 v162, v164, v165\n" \
"v_mfma_f32_16x16x32_bf16 v[144:147], a[176:179], v[68:71], v[144:147]\n" \
"v_cvt_pk_bf16_f32 v163, v166, v167\n" \
"v_cvt_pk_bf16_f32 v132, v136, v137\n" \
"v_mfma_f32_16x16x32_bf16 v[176:179], a[176:179], v[72:75], v[176:179]\n" \
"buffer_load_dwordx4 a[108:111], v20, s[56:59], s60 offen offset:3072 sc1\n" \
"v_mfma_f32_16x16x32_bf16 v[148:151], a[180:183], v[68:71], v[148:151]\n" \
"v_cvt_pk_bf16_f32 v133, v138, v139\n" \
"v_cvt_pk_bf16_f32 v134, v140, v141\n" \
"v_mfma_f32_16x16x32_bf16 v[180:183], a[180:183], v[72:75], v[180:183]\n" \
"v_cvt_pk_bf16_f32 v135, v142, v143\n" \
"v_cvt_pk_bf16_f32 v164, v168, v169\n" \
"v_mfma_f32_16x16x32_bf16 v[152:155], a[184:187], v[68:71], v[152:155]\n" \
"v_cvt_pk_bf16_f32 v165, v170, v171\n" \
"v_cvt_pk_bf16_f32 v166, v172, v173\n" \
"v_mfma_f32_16x16x32_bf16 v[184:187], a[184:187], v[72:75], v[184:187]\n" \
"v_cvt_pk_bf16_f32 v167, v174, v175\n" \
"v_cvt_pk_bf16_f32 v136, v144, v145\n" \
"v_mfma_f32_16x16x32_bf16 v[156:159], a[188:191], v[68:71], v[156:159]\n" \
"v_cvt_pk_bf16_f32 v137, v146, v147\n" \
"v_cvt_pk_bf16_f32 v138, v148, v149\n" \
"v_mfma_f32_16x16x32_bf16 v[188:191], a[188:191], v[72:75], v[188:191]\n" \
"v_cvt_pk_bf16_f32 v139, v150, v151\n" \
"v_cvt_pk_bf16_f32 v140, v152, v153\n" \
"v_smfmac_f32_16x16x64_bf16 a[192:195], a[200:203], v[68:75], v21\n" \
"v_cvt_pk_bf16_f32 v141, v154, v155\n" \
"v_cvt_pk_bf16_f32 v168, v176, v177\n" \
"v_mfma_f32_16x16x32_bf16 v[120:123], a[128:131], a[112:115], v[44:47]\n" \
"v_cvt_pk_bf16_f32 v142, v156, v157\n" \
"v_cvt_pk_bf16_f32 v143, v158, v159\n" \
"v_mfma_f32_16x16x32_bf16 v[120:123], a[132:135], a[116:119], v[120:123]\n" \
"v_cvt_pk_bf16_f32 v169, v178, v179\n" \
"v_cvt_pk_bf16_f32 v170, v180, v181\n" \
"v_cvt_pk_bf16_f32 v171, v182, v183\n" \
"v_cvt_pk_bf16_f32 v172, v184, v185\n" \
"v_cvt_pk_bf16_f32 v173, v186, v187\n" \
"v_cvt_pk_bf16_f32 v174, v188, v189\n" \
"v_cvt_pk_bf16_f32 v175, v190, v191\n" \
"ds_write_b128 v50, v[128:131] offset:0\n" \
"v_mfma_f32_16x16x32_bf16 v[120:123], a[136:139], a[120:123], v[120:123]\n" \
"v_and_b32_e32 v87, v15, v87\n" \
"v_accvgpr_read_b32 v40, a192\n" \
"v_mfma_f32_16x16x32_bf16 v[120:123], a[140:143], a[124:127], v[120:123]\n" \
"ds_write_b128 v50, v[132:135] offset:1024\n" \
"v_mfma_f32_16x16x32_bf16 v[124:127], a[156:159], a[124:127], v[44:47]\n" \
"buffer_load_dwordx4 a[124:127], v54, s[56:59], s60 offen offset:3072 sc1\n" \
"v_mfma_f32_16x16x32_bf16 v[124:127], a[152:155], a[120:123], v[124:127]\n" \
"ds_write_b128 v50, v[136:139] offset:2048\n" \
"v_mfma_f32_16x16x32_bf16 v[124:127], a[148:151], a[116:119], v[124:127]\n" \
"v_accvgpr_read_b32 v22, a193\n" \
"v_accvgpr_read_b32 v41, a194\n" \
"v_mfma_f32_16x16x32_bf16 v[124:127], a[144:147], a[112:115], v[124:127]\n" \
"ds_write_b128 v50, v[140:143] offset:3072\n" \
"v_mfma_f32_16x16x32_bf16 a[196:199], a[204:207], v[84:87], a[196:199]\n" \
"v_accvgpr_read_b32 v23, a195\n" \
"v_add_f32_e32 v40, v40, v22\n" \
"v_mfma_f32_16x16x32_bf16 v[192:195], a[160:163], v[84:87], v[192:195]\n" \
"ds_write_b128 v51, v[160:163] offset:32768\n" \
"v_mfma_f32_16x16x32_bf16 v[196:199], a[164:167], v[84:87], v[196:199]\n" \
"buffer_load_dwordx4 a[112:115], v54, s[56:59], s60 offen offset:0 sc1\n" \
"v_mfma_f32_16x16x32_bf16 v[200:203], a[168:171], v[84:87], v[200:203]\n" \
"ds_write_b128 v51, v[164:167] offset:33792\n" \
"v_add_f32_e32 v41, v41, v23\n" \
"ds_write_b64 v19, v[40:41]\n" \
"v_mfma_f32_16x16x32_bf16 v[204:207], a[172:175], v[84:87], v[204:207]\n" \
"v_lshl_add_u32 v52, s15, 2, v20\n" \
"v_add_u32_e32 v52, 0x1a000, v52\n" \
"ds_write_b128 v51, v[168:171] offset:34816\n" \
"v_mfma_f32_16x16x32_bf16 v[208:211], a[176:179], v[84:87], v[208:211]\n" \
"v_exp_f32_e32 v88, v88\n" \
"v_mfma_f32_16x16x32_bf16 v[212:215], a[180:183], v[84:87], v[212:215]\n" \
"ds_write_b128 v51, v[172:175] offset:35840\n" \
"v_mfma_f32_16x16x32_bf16 v[216:219], a[184:187], v[84:87], v[216:219]\n" \
"buffer_load_dwordx4 a[116:119], v54, s[56:59], s60 offen offset:1024 sc1\n" \
"v_mfma_f32_16x16x32_bf16 v[220:223], a[188:191], v[84:87], v[220:223]\n" \
"v_exp_f32_e32 v89, v89\n" \
"v_exp_f32_e32 v90, v90\n" \
"v_exp_f32_e32 v91, v91\n" \
"v_cvt_pk_bf16_f32 v88, v88, v89\n" \
"v_exp_f32_e32 v92, v92\n" \
"v_exp_f32_e32 v93, v93\n" \
"v_exp_f32_e32 v94, v94\n" \
"v_exp_f32_e32 v95, v95\n" \
"v_cvt_pk_bf16_f32 v89, v90, v91\n" \
"v_cvt_pk_bf16_f32 v90, v92, v93\n" \
"v_cvt_pk_bf16_f32 v91, v94, v95\n" \
"v_xor_b32_e32 v63, 0x400, v19\n" \
"buffer_load_dwordx4 a[120:123], v54, s[56:59], s60 offen offset:2048 sc1\n" \
"v_exp_f32_e32 v120, v120\n" \
"v_exp_f32_e32 v121, v121\n" \
"v_exp_f32_e32 v122, v122\n" \
"v_exp_f32_e32 v123, v123\n" \
".fill (L_ab + 12288 - .) / 4, 4, 0xBF800000\n" \
"L_ap3:\n" \
"s_cbranch_execz L_ap0\n" \
"v_cvt_pk_bf16_f32 v120, v120, v121\n" \
"v_exp_f32_e32 v124, v124\n" \
"v_exp_f32_e32 v125, v125\n" \
"s_waitcnt lgkmcnt(0)\n" \
"s_barrier\n" \
"ds_read_b128 v[144:147], v52 offset:0\n" \
"ds_read_b128 v[148:151], v52 offset:1024\n" \
"ds_read_b128 v[152:155], v52 offset:2048\n" \
"ds_read_b128 v[156:159], v52 offset:3072\n" \
"ds_read_b128 v[176:179], v52 offset:4096\n" \
"ds_read_b128 v[180:183], v52 offset:5120\n" \
"ds_read_b128 v[184:187], v52 offset:6144\n" \
"ds_read_b128 v[188:191], v52 offset:7168\n" \
"v_mfma_f32_16x16x32_bf16 a[196:199], a[212:215], v[88:91], a[196:199]\n" \
"ds_read_b64 v[42:43], v63\n" \
"v_cvt_pk_bf16_f32 v121, v122, v123\n" \
"v_mfma_f32_16x16x32_bf16 v[224:227], a[160:163], v[88:91], v[224:227]\n" \
"v_exp_f32_e32 v126, v126\n" \
"v_mfma_f32_16x16x32_bf16 v[228:231], a[164:167], v[88:91], v[228:231]\n" \
"v_exp_f32_e32 v127, v127\n" \
"v_mfma_f32_16x16x32_bf16 v[232:235], a[168:171], v[88:91], v[232:235]\n" \
"v_cvt_pk_bf16_f32 v122, v124, v125\n" \
"v_cvt_pk_bf16_f32 v123, v126, v127\n" \
"v_mfma_f32_16x16x32_bf16 v[236:239], a[172:175], v[88:91], v[236:239]\n" \
"v_and_b32_e32 v120, v12, v120\n" \
"v_and_b32_e32 v121, v13, v121\n" \
"v_mfma_f32_16x16x32_bf16 v[240:243], a[176:179], v[88:91], v[240:243]\n" \
"v_and_b32_e32 v122, v14, v122\n" \
"v_and_b32_e32 v123, v15, v123\n" \
"v_mfma_f32_16x16x32_bf16 v[244:247], a[180:183], v[88:91], v[244:247]\n" \
"v_readlane_b32 s93, v11, 22\n" \
"v_readlane_b32 s92, v11, 27\n" \
"v_mfma_f32_16x16x32_bf16 v[248:251], a[184:187], v[88:91], v[248:251]\n" \
"s_add_u32 m0, s93, 0x0\n" \
"v_mov_b32_e32 v130, v28\n" \
"v_mfma_f32_16x16x32_bf16 v[252:255], a[188:191], v[88:91], v[252:255]\n" \
"v_mov_b32_e32 v131, v29\n" \
"v_mov_b32_e32 v132, 0\n" \
"v_mov_b32_e32 v133, 0\n" \
"v_mov_b32_e32 v128, 0\n" \
"v_mov_b32_e32 v129, 0\n" \
"s_waitcnt vmcnt(8) lgkmcnt(1)\n" \
"s_barrier\n" \
"v_mfma_f32_16x16x32_bf16 a[196:199], a[212:215], v[120:123], a[196:199]\n" \
"buffer_load_dwordx4 v0, s[80:83], s92 offen offset:0 lds\n" \
"v_mfma_f32_16x16x32_bf16 v[224:227], a[0:3], v[120:123], v[224:227]\n" \
"v_mfma_f32_16x16x32_bf16 v[228:231], a[4:7], v[120:123], v[228:231]\n" \
"v_mfma_f32_16x16x32_bf16 v[232:235], a[8:11], v[120:123], v[232:235]\n" \
"v_mfma_f32_16x16x32_bf16 v[236:239], a[12:15], v[120:123], v[236:239]\n" \
"v_mfma_f32_16x16x32_bf16 v[240:243], a[16:19], v[120:123], v[240:243]\n" \
"v_mfma_f32_16x16x32_bf16 v[244:247], a[20:23], v[120:123], v[244:247]\n" \
"buffer_load_dwordx4 v1, s[80:83], s92 offen offset:1024 lds\n" \
"v_mfma_f32_16x16x32_bf16 v[248:251], a[24:27], v[120:123], v[248:251]\n" \
"v_accvgpr_read_b32 v56, a196\n" \
"v_accvgpr_read_b32 v24, a197\n" \
"v_mfma_f32_16x16x32_bf16 v[252:255], a[28:31], v[120:123], v[252:255]\n" \
"v_add_f32_e32 v56, v56, v24\n" \
"s_waitcnt lgkmcnt(0)\n" \
"v_add_f32_e32 v56, v56, v42\n" \
"v_mfma_f32_16x16x32_bf16 v[192:195], v[130:133], v[144:147], v[192:195]\n" \
"v_max_f32_e32 v62, v62, v56\n" \
"v_accvgpr_read_b32 v57, a198\n" \
"v_mfma_f32_16x16x32_bf16 v[196:199], v[128:131], v[144:147], v[196:199]\n" \
"v_rcp_f32_e32 v56, v56\n" \
"v_mfma_f32_16x16x32_bf16 v[200:203], v[130:133], v[148:151], v[200:203]\n" \
"v_mul_f32_e32 v56, v56, v60\n" \
"v_accvgpr_read_b32 v25, a199\n" \
"buffer_load_dwordx4 v2, s[80:83], s92 offen offset:2048 lds\n" \
"v_mfma_f32_16x16x32_bf16 v[204:207], v[128:131], v[148:151], v[204:207]\n" \
"v_mul_f32_e32 v192, v56, v192\n" \
"v_mul_f32_e32 v193, v56, v193\n" \
"v_mfma_f32_16x16x32_bf16 v[208:211], v[130:133], v[152:155], v[208:211]\n" \
"v_mul_f32_e32 v194, v56, v194\n" \
"v_mul_f32_e32 v195, v56, v195\n" \
"v_mfma_f32_16x16x32_bf16 v[212:215], v[128:131], v[152:155], v[212:215]\n" \
"v_mul_f32_e32 v196, v56, v196\n" \
"v_mul_f32_e32 v197, v56, v197\n" \
"v_mfma_f32_16x16x32_bf16 v[216:219], v[130:133], v[156:159], v[216:219]\n" \
"v_mul_f32_e32 v198, v56, v198\n" \
"v_mul_f32_e32 v199, v56, v199\n" \
"v_mfma_f32_16x16x32_bf16 v[220:223], v[128:131], v[156:159], v[220:223]\n" \
"v_cvt_pk_bf16_f32 v64, v192, v193\n" \
"v_cvt_pk_bf16_f32 v65, v194, v195\n" \
"v_mfma_f32_16x16x32_bf16 v[224:227], v[130:133], v[176:179], v[224:227]\n" \
"buffer_load_dwordx4 v3, s[80:83], s92 offen offset:3072 lds\n" \
"s_add_u32 m0, s93, 0x1000\n" \
"v_mfma_f32_16x16x32_bf16 v[228:231], v[128:131], v[176:179], v[228:231]\n" \
"v_cvt_pk_bf16_f32 v66, v196, v197\n" \
"v_cvt_pk_bf16_f32 v67, v198, v199\n" \
"v_mfma_f32_16x16x32_bf16 v[232:235], v[130:133], v[180:183], v[232:235]\n" \
"v_mul_f32_e32 v200, v56, v200\n" \
"v_mul_f32_e32 v201, v56, v201\n" \
"v_mfma_f32_16x16x32_bf16 v[236:239], v[128:131], v[180:183], v[236:239]\n" \
"v_mfma_f32_16x16x32_bf16 a[208:211], a[216:219], v[64:67], a[208:211]\n" \
"v_mul_f32_e32 v202, v56, v202\n" \
"v_mul_f32_e32 v203, v56, v203\n" \
"v_mfma_f32_16x16x32_bf16 v[240:243], v[130:133], v[184:187], v[240:243]\n" \
"v_mul_f32_e32 v204, v56, v204\n" \
"v_mul_f32_e32 v205, v56, v205\n" \
"buffer_load_dwordx4 v4, s[80:83], s92 offen offset:0 lds\n" \
"v_mul_f32_e32 v206, v56, v206\n" \
"v_mul_f32_e32 v207, v56, v207\n" \
"v_add_f32_e32 v57, v57, v25\n" \
"v_cvt_pk_bf16_f32 v68, v200, v201\n" \
"v_cvt_pk_bf16_f32 v69, v202, v203\n" \
"v_cvt_pk_bf16_f32 v70, v204, v205\n" \
"v_cvt_pk_bf16_f32 v71, v206, v207\n" \
"v_add_f32_e32 v57, v57, v43\n" \
"v_mul_f32_e32 v208, v56, v208\n" \
"v_mfma_f32_16x16x32_bf16 a[208:211], a[216:219], v[68:71], a[208:211]\n" \
"v_mul_f32_e32 v209, v56, v209\n" \
"v_mul_f32_e32 v210, v56, v210\n" \
"v_mul_f32_e32 v211, v56, v211\n" \
"v_mul_f32_e32 v212, v56, v212\n" \
"v_mul_f32_e32 v213, v56, v213\n" \
"v_mul_f32_e32 v214, v56, v214\n" \
"v_mul_f32_e32 v215, v56, v215\n" \
"v_max_f32_e32 v62, v62, v57\n" \
"v_cvt_pk_bf16_f32 v72, v208, v209\n" \
"v_cvt_pk_bf16_f32 v73, v210, v211\n" \
"buffer_load_dwordx4 v5, s[80:83], s92 offen offset:1024 lds\n" \
"v_cvt_pk_bf16_f32 v74, v212, v213\n" \
"v_cvt_pk_bf16_f32 v75, v214, v215\n" \
"v_rcp_f32_e32 v57, v57\n" \
"v_mfma_f32_16x16x32_bf16 v[244:247], v[128:131], v[184:187], v[244:247]\n" \
"v_mfma_f32_16x16x32_bf16 a[208:211], a[216:219], v[72:75], a[208:211]\n" \
"v_mul_f32_e32 v216, v56, v216\n" \
"v_mul_f32_e32 v217, v56, v217\n" \
"v_mul_f32_e32 v218, v56, v218\n" \
"v_mul_f32_e32 v219, v56, v219\n" \
"v_mul_f32_e32 v220, v56, v220\n" \
"v_mul_f32_e32 v221, v56, v221\n" \
"v_mul_f32_e32 v222, v56, v222\n" \
"v_mul_f32_e32 v223, v56, v223\n" \
"v_cvt_pk_bf16_f32 v76, v216, v217\n" \
"v_cvt_pk_bf16_f32 v77, v218, v219\n" \
"v_cvt_pk_bf16_f32 v78, v220, v221\n" \
"buffer_load_dwordx4 v6, s[80:83], s92 offen offset:2048 lds\n" \
"v_cvt_pk_bf16_f32 v79, v222, v223\n" \
"v_mul_f32_e32 v57, v57, v61\n" \
"v_mul_f32_e32 v224, v57, v224\n" \
"v_mfma_f32_16x16x32_bf16 a[208:211], a[216:219], v[76:79], a[208:211]\n" \
"v_mul_f32_e32 v225, v57, v225\n" \
"v_mul_f32_e32 v226, v57, v226\n" \
"v_mul_f32_e32 v227, v57, v227\n" \
"v_mul_f32_e32 v228, v57, v228\n" \
"v_mul_f32_e32 v229, v57, v229\n" \
"v_mul_f32_e32 v230, v57, v230\n" \
"v_mul_f32_e32 v231, v57, v231\n" \
"v_cvt_pk_bf16_f32 v80, v224, v225\n" \
"v_cvt_pk_bf16_f32 v81, v226, v227\n" \
"v_cvt_pk_bf16_f32 v82, v228, v229\n" \
"v_cvt_pk_bf16_f32 v83, v230, v231\n" \
"v_mfma_f32_16x16x32_bf16 v[248:251], v[130:133], v[188:191], v[248:251]\n" \
"v_mul_f32_e32 v232, v57, v232\n" \
"v_mul_f32_e32 v233, v57, v233\n" \
"v_mfma_f32_16x16x32_bf16 a[208:211], a[216:219], v[80:83], a[208:211]\n" \
"buffer_load_dwordx4 v7, s[80:83], s92 offen offset:3072 lds\n" \
"s_add_u32 m0, s93, 0x2000\n" \
"v_mul_f32_e32 v234, v57, v234\n" \
"v_mul_f32_e32 v235, v57, v235\n" \
"v_mul_f32_e32 v236, v57, v236\n" \
"v_mul_f32_e32 v237, v57, v237\n" \
"v_mul_f32_e32 v238, v57, v238\n" \
"v_mul_f32_e32 v239, v57, v239\n" \
"v_cvt_pk_bf16_f32 v84, v232, v233\n" \
"v_cvt_pk_bf16_f32 v85, v234, v235\n" \
"v_cvt_pk_bf16_f32 v86, v236, v237\n" \
"v_cvt_pk_bf16_f32 v87, v238, v239\n" \
"v_mul_f32_e32 v240, v57, v240\n" \
"v_mul_f32_e32 v241, v57, v241\n" \
"v_mfma_f32_16x16x32_bf16 a[208:211], a[216:219], v[84:87], a[208:211]\n" \
"v_mul_f32_e32 v242, v57, v242\n" \
"v_mul_f32_e32 v243, v57, v243\n" \
"v_mul_f32_e32 v244, v57, v244\n" \
"v_mul_f32_e32 v245, v57, v245\n" \
"v_mul_f32_e32 v246, v57, v246\n" \
"v_mul_f32_e32 v247, v57, v247\n" \
"buffer_load_dwordx4 v8, s[80:83], s92 offen offset:0 lds\n" \
"v_cvt_pk_bf16_f32 v96, v240, v241\n" \
"v_cvt_pk_bf16_f32 v97, v242, v243\n" \
"v_cvt_pk_bf16_f32 v98, v244, v245\n" \
"v_cvt_pk_bf16_f32 v99, v246, v247\n" \
"v_mfma_f32_16x16x32_bf16 v[252:255], v[128:131], v[188:191], v[252:255]\n" \
"v_mul_f32_e32 v248, v57, v248\n" \
"v_mfma_f32_16x16x32_bf16 a[208:211], a[216:219], v[96:99], a[208:211]\n" \
"v_mul_f32_e32 v249, v57, v249\n" \
"v_mul_f32_e32 v250, v57, v250\n" \
"v_mul_f32_e32 v251, v57, v251\n" \
"v_cvt_pk_bf16_f32 v100, v248, v249\n" \
"v_cvt_pk_bf16_f32 v101, v250, v251\n" \
"v_cmp_lt_f32_e64 s[72:73], s21, v62\n" \
"v_mul_f32_e32 v252, v57, v252\n" \
"v_mul_f32_e32 v253, v57, v253\n" \
"v_mul_f32_e32 v254, v57, v254\n" \
"v_mul_f32_e32 v255, v57, v255\n" \
"v_cvt_pk_bf16_f32 v102, v252, v253\n" \
"v_cvt_pk_bf16_f32 v103, v254, v255\n" \
"s_nop 1\n" \
"v_mfma_f32_16x16x32_bf16 a[208:211], a[216:219], v[100:103], a[208:211]\n" \
"s_nop 7\n" \
"v_accvgpr_read_b32 v63, a208\n" \
"v_cmp_u_f32_e32 vcc, v63, v63\n" \
"s_nop 0\n" \
"s_or_b64 vcc, s[72:73], vcc\n" \
"s_cbranch_scc0 L_out_%=\n" \
"s_branch L_end_%=\n" \
"L_skb_%=:\n" \
"s_sleep 1\n" \
"buffer_load_dword v57, v61, s[56:59], s96 offen sc1\n" \
"s_add_u32 s101, s101, 1\n" \
"s_waitcnt vmcnt(0)\n" \
"v_cmp_ne_u32_e32 vcc, s62, v57\n" \
"s_cmp_gt_u32 s101, 0x100000\n" \
"s_cbranch_scc1 L_rkb_%=\n" \
"s_nop 2\n" \
"s_cbranch_vccnz L_skb_%=\n" \
"s_branch L_rkb_%=\n" \
"L_sol_%=:\n" \
"s_sleep 1\n" \
"buffer_load_dword v26, v27, s[56:59], s96 offen sc1\n" \
"s_add_u32 s101, s101, 1\n" \
"s_waitcnt vmcnt(0)\n" \
"v_cmp_ne_u32_e32 vcc, s62, v26\n" \
"s_cmp_gt_u32 s101, 0x100000\n" \
"s_cbranch_scc1 L_rol_%=\n" \
"s_nop 2\n" \
"s_cbranch_vccnz L_sol_%=\n" \
"s_branch L_rol_%=\n" \
"L_sos_%=:\n" \
"s_sleep 1\n" \
"buffer_load_dword v26, v27, s[56:59], s96 offen sc1\n" \
"s_add_u32 s101, s101, 1\n" \
"s_waitcnt vmcnt(0)\n" \
"v_cmp_ne_u32_e32 vcc, s62, v26\n" \
"s_cmp_gt_u32 s101, 0x100000\n" \
"s_cbranch_scc1 L_ros_%=\n" \
"s_nop 2\n" \
"s_cbranch_vccnz L_sos_%=\n" \
"s_branch L_ros_%=\n" \
"L_sa_%=:\n" \
"s_sleep 1\n" \
"buffer_load_dword v26, v38, s[56:59], s64 offen sc1\n" \
"s_add_u32 s101, s101, 1\n" \
"s_waitcnt vmcnt(0)\n" \
"v_cmp_ne_u32_e32 vcc, s62, v26\n" \
"s_cmp_gt_u32 s101, 0x100000\n" \
"s_cbranch_scc1 L_ra_%=\n" \
"s_nop 2\n" \
"s_cbranch_vccnz L_sa_%=\n" \
"s_branch L_ra_%=\n" \
"L_sb_%=:\n" \
"s_sleep 1\n" \
"buffer_load_dword v47, v38, s[56:59], s71 offen sc1\n" \
"s_add_u32 s101, s101, 1\n" \
"s_waitcnt vmcnt(0)\n" \
"v_cmp_ne_u32_e32 vcc, s62, v47\n" \
"s_cmp_gt_u32 s101, 0x100000\n" \
"s_cbranch_scc1 L_rb_%=\n" \
"s_nop 2\n" \
"s_cbranch_vccnz L_sb_%=\n" \
"s_branch L_rb_%=\n" \
"L_sna_%=:\n" \
"buffer_load_dword v37, v40, s[56:59], s74 offen sc1\n" \
"s_add_u32 s101, s101, 1\n" \
"s_waitcnt vmcnt(0)\n" \
"v_cmp_eq_u32_e64 s[98:99], s62, v37\n" \
"s_cmp_gt_u32 s101, 0x100000\n" \
"s_cbranch_scc1 L_rna_%=\n" \
"s_andn2_b32 s75, s20, s98\n" \
"s_cbranch_scc0 L_gna_%=\n" \
"s_sleep 1\n" \
"s_branch L_sna_%=\n" \
"L_gna_%=:\n" \
"s_bitcmp1_b32 s20, 0\n" \
"s_cbranch_scc0 L_kna0_%=\n" \
"buffer_load_dwordx4 v[116:119], v20, s[56:59], s12 offen offset:0 sc1\n" \
"L_kna0_%=:\n" \
"s_bitcmp1_b32 s20, 1\n" \
"s_cbranch_scc0 L_kna1_%=\n" \
"buffer_load_dwordx4 v[124:127], v20, s[56:59], s12 offen offset:2048 sc1\n" \
"L_kna1_%=:\n" \
"s_bitcmp1_b32 s20, 2\n" \
"s_cbranch_scc0 L_kna2_%=\n" \
"buffer_load_dwordx4 v[132:135], v20, s[56:59], s13 offen offset:0 sc1\n" \
"L_kna2_%=:\n" \
"s_bitcmp1_b32 s20, 3\n" \
"s_cbranch_scc0 L_kna3_%=\n" \
"buffer_load_dwordx4 v[140:143], v20, s[56:59], s13 offen offset:2048 sc1\n" \
"L_kna3_%=:\n" \
"s_bitcmp1_b32 s20, 4\n" \
"s_cbranch_scc0 L_kna4_%=\n" \
"buffer_load_dwordx4 v[148:151], v20, s[56:59], s14 offen offset:0 sc1\n" \
"L_kna4_%=:\n" \
"s_bitcmp1_b32 s20, 5\n" \
"s_cbranch_scc0 L_kna5_%=\n" \
"buffer_load_dwordx4 v[156:159], v20, s[56:59], s14 offen offset:2048 sc1\n" \
"L_kna5_%=:\n" \
"s_bitcmp1_b32 s20, 6\n" \
"s_cbranch_scc0 L_kna6_%=\n" \
"buffer_load_dwordx4 v[164:167], v20, s[56:59], s15 offen offset:0 sc1\n" \
"L_kna6_%=:\n" \
"s_waitcnt vmcnt(0)\n" \
"s_branch L_rna_%=\n" \
"L_snx_%=:\n" \
"buffer_load_dword v37, v40, s[56:59], s74 offen sc1\n" \
"s_add_u32 s101, s101, 1\n" \
"s_waitcnt vmcnt(0)\n" \
"v_cmp_eq_u32_e64 s[98:99], s62, v37\n" \
"s_cmp_gt_u32 s101, 0x100000\n" \
"s_cbranch_scc1 L_rnx_%=\n" \
"s_andn2_b32 s75, s21, s98\n" \
"s_cbranch_scc0 L_gnx_%=\n" \
"s_sleep 1\n" \
"s_branch L_snx_%=\n" \
"L_gnx_%=:\n" \
"s_bitcmp1_b32 s21, 16\n" \
"s_cbranch_scc0 L_knx16_%=\n" \
"buffer_load_dwordx4 v[112:115], v20, s[56:59], s16 offen offset:0 sc1\n" \
"L_knx16_%=:\n" \
"s_bitcmp1_b32 s21, 17\n" \
"s_cbranch_scc0 L_knx17_%=\n" \
"buffer_load_dwordx4 v[184:187], v20, s[56:59], s16 offen offset:2048 sc1\n" \
"L_knx17_%=:\n" \
"s_bitcmp1_b32 s21, 18\n" \
"s_cbranch_scc0 L_knx18_%=\n" \
"buffer_load_dwordx4 v[120:123], v20, s[56:59], s17 offen offset:0 sc1\n" \
"L_knx18_%=:\n" \
"s_bitcmp1_b32 s21, 19\n" \
"s_cbranch_scc0 L_knx19_%=\n" \
"buffer_load_dwordx4 v[192:195], v20, s[56:59], s17 offen offset:2048 sc1\n" \
"L_knx19_%=:\n" \
"s_bitcmp1_b32 s21, 20\n" \
"s_cbranch_scc0 L_knx20_%=\n" \
"buffer_load_dwordx4 v[128:131], v20, s[56:59], s18 offen offset:0 sc1\n" \
"L_knx20_%=:\n" \
"s_bitcmp1_b32 s21, 21\n" \
"s_cbranch_scc0 L_knx21_%=\n" \
"buffer_load_dwordx4 v[200:203], v20, s[56:59], s18 offen offset:2048 sc1\n" \
"L_knx21_%=:\n" \
"s_bitcmp1_b32 s21, 22\n" \
"s_cbranch_scc0 L_knx22_%=\n" \
"buffer_load_dwordx4 v[176:179], v20, s[56:59], s19 offen offset:0 sc1\n" \
"L_knx22_%=:\n" \
"s_bitcmp1_b32 s21, 23\n" \
"s_cbranch_scc0 L_knx23_%=\n" \
"buffer_load_dwordx4 v[248:251], v20, s[56:59], s19 offen offset:2048 sc1\n" \
"L_knx23_%=:\n" \
"s_bitcmp1_b32 s21, 8\n" \
"s_cbranch_scc0 L_knx8_%=\n" \
"buffer_load_dwordx4 v[188:191], v20, s[56:59], s12 offen offset:1024 sc1\n" \
"L_knx8_%=:\n" \
"s_bitcmp1_b32 s21, 9\n" \
"s_cbranch_scc0 L_knx9_%=\n" \
"buffer_load_dwordx4 v[196:199], v20, s[56:59], s12 offen offset:3072 sc1\n" \
"L_knx9_%=:\n" \
"s_waitcnt vmcnt(0)\n" \
"s_branch L_rnx_%=\n" \
"L_sx0_%=:\n" \
"s_sleep 1\n" \
"buffer_load_dword v37, v40, s[56:59], s74 offen sc1\n" \
"s_add_u32 s101, s101, 1\n" \
"s_waitcnt vmcnt(0)\n" \
"v_cmp_eq_u32_e64 s[98:99], s62, v37\n" \
"s_cmp_gt_u32 s101, 0x100000\n" \
"s_cbranch_scc1 L_rx0_%=\n" \
"s_andn2_b32 s75, 0x400, s98\n" \
"s_cbranch_scc1 L_sx0_%=\n" \
"s_branch L_rx0_%=\n" \
"L_sx1_%=:\n" \
"s_sleep 1\n" \
"buffer_load_dword v37, v40, s[56:59], s74 offen sc1\n" \
"s_add_u32 s101, s101, 1\n" \
"s_waitcnt vmcnt(0)\n" \
"v_cmp_eq_u32_e64 s[98:99], s62, v37\n" \
"s_cmp_gt_u32 s101, 0x100000\n" \
"s_cbranch_scc1 L_rx1_%=\n" \
"s_andn2_b32 s75, 0x800, s98\n" \
"s_cbranch_scc1 L_sx1_%=\n" \
"s_branch L_rx1_%=\n" \
"L_sx2_%=:\n" \
"s_sleep 1\n" \
"buffer_load_dword v37, v40, s[56:59], s74 offen sc1\n" \
"s_add_u32 s101, s101, 1\n" \
"s_waitcnt vmcnt(0)\n" \
"v_cmp_eq_u32_e64 s[98:99], s62, v37\n" \
"s_cmp_gt_u32 s101, 0x100000\n" \
"s_cbranch_scc1 L_rx2_%=\n" \
"s_andn2_b32 s75, 0x1000, s98\n" \
"s_cbranch_scc1 L_sx2_%=\n" \
"s_branch L_rx2_%=\n" \
"L_sx3_%=:\n" \
"s_sleep 1\n" \
"buffer_load_dword v37, v40, s[56:59], s74 offen sc1\n" \
"s_add_u32 s101, s101, 1\n" \
"s_waitcnt vmcnt(0)\n" \
"v_cmp_eq_u32_e64 s[98:99], s62, v37\n" \
"s_cmp_gt_u32 s101, 0x100000\n" \
"s_cbranch_scc1 L_rx3_%=\n" \
"s_andn2_b32 s75, 0x2000, s98\n" \
"s_cbranch_scc1 L_sx3_%=\n" \
"s_branch L_rx3_%=\n" \
"L_sx4_%=:\n" \
"s_sleep 1\n" \
"buffer_load_dword v37, v40, s[56:59], s74 offen sc1\n" \
"s_add_u32 s101, s101, 1\n" \
"s_waitcnt vmcnt(0)\n" \
"v_cmp_eq_u32_e64 s[98:99], s62, v37\n" \
"s_cmp_gt_u32 s101, 0x100000\n" \
"s_cbranch_scc1 L_rx4_%=\n" \
"s_andn2_b32 s75, 0x4000, s98\n" \
"s_cbranch_scc1 L_sx4_%=\n" \
"s_branch L_rx4_%=\n" \
"L_att_stub:\n" \
"s_mov_b32 s101, 0\n" \
"v_add_u32_e32 v32, 0x49000, v20\n" \
"v_readlane_b32 s96, v11, 36\n" \
"v_readlane_b32 s97, v11, 37\n" \
"buffer_load_dwordx4 a[96:99], v20, s[56:59], s60 offen offset:0 sc1\n" \
"buffer_load_dwordx4 a[112:115], v32, s[56:59], s60 offen offset:0 sc1\n" \
"buffer_load_dwordx4 a[100:103], v20, s[56:59], s60 offen offset:1024 sc1\n" \
"buffer_load_dwordx4 a[116:119], v32, s[56:59], s60 offen offset:1024 sc1\n" \
"buffer_load_dwordx4 a[104:107], v20, s[56:59], s60 offen offset:2048 sc1\n" \
"buffer_load_dwordx4 a[120:123], v32, s[56:59], s60 offen offset:2048 sc1\n" \
"buffer_load_dwordx4 a[108:111], v20, s[56:59], s60 offen offset:3072 sc1\n" \
"buffer_load_dwordx4 a[124:127], v32, s[56:59], s60 offen offset:3072 sc1\n" \
"buffer_load_dwordx4 a[220:223], v20, s[56:59], s96 offen offset:0 sc1\n" \
"buffer_load_dwordx4 a[228:231], v32, s[56:59], s96 offen offset:0 sc1\n" \
"buffer_load_dwordx4 a[236:239], v20, s[56:59], s97 offen offset:0 sc1\n" \
"buffer_load_dwordx4 a[244:247], v32, s[56:59], s97 offen offset:0 sc1\n" \
"buffer_load_dwordx4 a[224:227], v20, s[56:59], s96 offen offset:1024 sc1\n" \
"buffer_load_dwordx4 a[232:235], v32, s[56:59], s96 offen offset:1024 sc1\n" \
"buffer_load_dwordx4 a[240:243], v20, s[56:59], s97 offen offset:1024 sc1\n" \
"buffer_load_dwordx4 a[248:251], v32, s[56:59], s97 offen offset:1024 sc1\n" \
"buffer_load_dwordx4 v[64:67], v20, s[56:59], s61 offen offset:0 sc1\n" \
"buffer_load_dwordx4 v[68:71], v20, s[56:59], s61 offen offset:1024 sc1\n" \
"buffer_load_dwordx4 v[72:75], v20, s[56:59], s61 offen offset:2048 sc1\n" \
"buffer_load_dwordx4 v[76:79], v20, s[56:59], s61 offen offset:3072 sc1\n" \
"buffer_load_dwordx4 v[80:83], v32, s[56:59], s61 offen offset:0 sc1\n" \
"buffer_load_dwordx4 v[84:87], v32, s[56:59], s61 offen offset:1024 sc1\n" \
"buffer_load_dwordx4 v[96:99], v32, s[56:59], s61 offen offset:2048 sc1\n" \
"buffer_load_dwordx4 v[100:103], v32, s[56:59], s61 offen offset:3072 sc1\n" \
"v_not_b32_e32 v26, s62\n" \
"s_waitcnt vmcnt(0)\n" \
"s_branch L_out_%=\n" \
".fill (L_ab + 16384 - .) / 4, 4, 0xBF800000\n" \
".fill (L_ab + 16384 - .) / 4, 4, 0xBF800000\n" \
"L_ap4:\n" \
"s_setpc_b64 s[88:89]\n" \
"L_gsw:\n" \
"v_mfma_f32_16x16x32_bf16 v[104:107], a[128:131], a[16:19], v[36:39]\n" \
"s_add_u32 s68, s60, 0xfff6e000\n" \
"s_add_u32 s69, s60, 0xfffb7000\n" \
"s_add_u32 s70, s60, 0x0\n" \
"s_add_u32 s71, s60, 0x49000\n" \
"v_mfma_f32_16x16x32_bf16 v[104:107], a[132:135], a[20:23], v[104:107]\n" \
"s_xor_b32 m0, s65, 0x12000\n" \
"ds_read_b128 a[160:163], v49 offset:0\n" \
"v_mfma_f32_16x16x32_bf16 v[104:107], a[136:139], a[24:27], v[104:107]\n" \
"buffer_load_dwordx4 v50, s[8:11], s64 offen offset:0 sc1 lds\n" \
"v_mfma_f32_16x16x32_bf16 v[104:107], a[140:143], a[28:31], v[104:107]\n" \
"ds_read_b128 a[164:167], v49 offset:1024\n" \
"ds_read_b128 a[168:171], v49 offset:2048\n" \
"v_mfma_f32_16x16x32_bf16 v[108:111], a[156:159], a[28:31], v[36:39]\n" \
"ds_read_b128 a[172:175], v49 offset:3072\n" \
"ds_read_b128 a[176:179], v49 offset:4096\n" \
"v_mfma_f32_16x16x32_bf16 v[108:111], a[152:155], a[24:27], v[108:111]\n" \
"buffer_load_dwordx4 v50, s[8:11], s64 offen offset:1024 sc1 lds\n" \
"v_mfma_f32_16x16x32_bf16 v[108:111], a[148:151], a[20:23], v[108:111]\n" \
"s_mov_b32 s65, s13\n" \
"v_add_u32_e32 v63, s15, v20\n" \
"v_mfma_f32_16x16x32_bf16 v[108:111], a[144:147], a[16:19], v[108:111]\n" \
"s_add_u32 s67, s14, 40960\n" \
"v_add_u32_e32 v50, 0x6000, v63\n" \
"v_mfma_f32_16x16x32_bf16 v[116:119], a[144:147], a[32:35], v[40:43]\n" \
"s_xor_b32 m0, s65, 0x8000\n" \
"ds_read_b128 a[180:183], v49 offset:5120\n" \
"v_mfma_f32_16x16x32_bf16 v[116:119], a[148:151], a[36:39], v[116:119]\n" \
"ds_read_b128 a[184:187], v49 offset:6144\n" \
"v_and_b32_e32 v68, v12, v68\n" \
"v_mfma_f32_16x16x32_bf16 v[116:119], a[152:155], a[40:43], v[116:119]\n" \
"v_and_b32_e32 v69, v13, v69\n" \
"v_and_b32_e32 v70, v14, v70\n" \
"v_mfma_f32_16x16x32_bf16 v[116:119], a[156:159], a[44:47], v[116:119]\n" \
"buffer_load_dwordx4 v50, s[8:11], s67 offen offset:0 sc1 lds\n" \
"v_mfma_f32_16x16x32_bf16 v[112:115], a[140:143], a[44:47], v[40:43]\n" \
"v_and_b32_e32 v71, v15, v71\n" \
"ds_read_b128 a[188:191], v49 offset:7168\n" \
"v_mfma_f32_16x16x32_bf16 v[112:115], a[136:139], a[40:43], v[112:115]\n" \
"v_exp_f32_e32 v86, v86\n" \
"v_mfma_f32_16x16x32_bf16 v[112:115], a[132:135], a[36:39], v[112:115]\n" \
"v_exp_f32_e32 v87, v87\n" \
"v_mfma_f32_16x16x32_bf16 v[112:115], a[128:131], a[32:35], v[112:115]\n" \
"v_exp_f32_e32 v84, v84\n" \
"v_mfma_f32_16x16x32_bf16 v[120:123], a[128:131], a[48:51], v[44:47]\n" \
"buffer_load_dwordx4 v50, s[8:11], s67 offen offset:1024 sc1 lds\n" \
"v_mfma_f32_16x16x32_bf16 v[120:123], a[132:135], a[52:55], v[120:123]\n" \
"v_exp_f32_e32 v85, v85\n" \
"v_mfma_f32_16x16x32_bf16 v[120:123], a[136:139], a[56:59], v[120:123]\n" \
"v_cvt_pk_bf16_f32 v87, v86, v87\n" \
"v_cvt_pk_bf16_f32 v86, v84, v85\n" \
"v_mfma_f32_16x16x32_bf16 v[120:123], a[140:143], a[60:63], v[120:123]\n" \
"v_exp_f32_e32 v82, v82\n" \
"v_mfma_f32_16x16x32_bf16 v[124:127], a[156:159], a[60:63], v[44:47]\n" \
"v_exp_f32_e32 v83, v83\n" \
"v_mfma_f32_16x16x32_bf16 v[124:127], a[152:155], a[56:59], v[124:127]\n" \
"v_exp_f32_e32 v80, v80\n" \
"v_mfma_f32_16x16x32_bf16 v[124:127], a[148:151], a[52:55], v[124:127]\n" \
"buffer_load_dwordx4 v50, s[8:11], s67 offen offset:2048 sc1 lds\n" \
"v_mfma_f32_16x16x32_bf16 v[124:127], a[144:147], a[48:51], v[124:127]\n" \
"v_cvt_pk_bf16_f32 v85, v82, v83\n" \
"s_waitcnt lgkmcnt(1)\n" \
"v_mfma_f32_16x16x32_bf16 v[128:131], a[160:163], v[68:71], v[128:131]\n" \
"v_exp_f32_e32 v81, v81\n" \
"v_mfma_f32_16x16x32_bf16 v[160:163], a[160:163], v[72:75], v[160:163]\n" \
"v_cvt_pk_bf16_f32 v84, v80, v81\n" \
"s_mov_b32 s64, s14\n" \
"v_mfma_f32_16x16x32_bf16 v[132:135], a[164:167], v[68:71], v[132:135]\n" \
"v_exp_f32_e32 v88, v88\n" \
"v_mfma_f32_16x16x32_bf16 v[164:167], a[164:167], v[72:75], v[164:167]\n" \
"v_exp_f32_e32 v89, v89\n" \
"v_mfma_f32_16x16x32_bf16 v[136:139], a[168:171], v[68:71], v[136:139]\n" \
"buffer_load_dwordx4 v50, s[8:11], s67 offen offset:3072 sc1 lds\n" \
"v_mfma_f32_16x16x32_bf16 v[168:171], a[168:171], v[72:75], v[168:171]\n" \
"v_exp_f32_e32 v90, v90\n" \
"v_mfma_f32_16x16x32_bf16 v[140:143], a[172:175], v[68:71], v[140:143]\n" \
"v_exp_f32_e32 v91, v91\n" \
"v_mfma_f32_16x16x32_bf16 v[172:175], a[172:175], v[72:75], v[172:175]\n" \
"v_cvt_pk_bf16_f32 v88, v88, v89\n" \
"v_cvt_pk_bf16_f32 v89, v90, v91\n" \
"v_mfma_f32_16x16x32_bf16 v[144:147], a[176:179], v[68:71], v[144:147]\n" \
"v_exp_f32_e32 v92, v92\n" \
"v_mfma_f32_16x16x32_bf16 v[176:179], a[176:179], v[72:75], v[176:179]\n" \
"v_exp_f32_e32 v93, v93\n" \
"v_mfma_f32_16x16x32_bf16 v[148:151], a[180:183], v[68:71], v[148:151]\n" \
"v_exp_f32_e32 v94, v94\n" \
"v_mfma_f32_16x16x32_bf16 v[180:183], a[180:183], v[72:75], v[180:183]\n" \
"v_exp_f32_e32 v95, v95\n" \
"v_mfma_f32_16x16x32_bf16 v[152:155], a[184:187], v[68:71], v[152:155]\n" \
"v_cvt_pk_bf16_f32 v90, v92, v93\n" \
"v_cvt_pk_bf16_f32 v91, v94, v95\n" \
"v_mfma_f32_16x16x32_bf16 v[184:187], a[184:187], v[72:75], v[184:187]\n" \
"v_add_u32_e32 v51, 0x12000, v63\n" \
"s_waitcnt lgkmcnt(0)\n" \
"v_mfma_f32_16x16x32_bf16 v[156:159], a[188:191], v[68:71], v[156:159]\n" \
"s_xor_b32 m0, s65, 0xa000\n" \
"ds_read_b32 v58, v18 offset:512\n" \
"v_mfma_f32_16x16x32_bf16 v[188:191], a[188:191], v[72:75], v[188:191]\n" \
"buffer_load_dwordx4 v51, s[8:11], s64 offen offset:0 sc1 lds\n" \
"v_smfmac_f32_16x16x64_bf16 a[192:195], a[200:203], v[68:75], v21\n" \
"buffer_load_dwordx4 v51, s[8:11], s64 offen offset:1024 sc1 lds\n" \
"v_mfma_f32_16x16x32_bf16 v[220:223], a[188:191], v[84:87], v[220:223]\n" \
"ds_read_b32 v59, v18 offset:640\n" \
"ds_read_b32 v60, v18 offset:768\n" \
"v_mfma_f32_16x16x32_bf16 v[252:255], a[188:191], v[88:91], v[252:255]\n" \
"ds_read_b32 v61, v18 offset:896\n" \
"ds_read_b128 a[128:131], v48 offset:0\n" \
"v_mfma_f32_16x16x32_bf16 v[216:219], a[184:187], v[84:87], v[216:219]\n" \
"ds_read_b128 a[144:147], v48 offset:4096\n" \
"ds_read_b128 a[132:135], v48 offset:1024\n" \
"v_mfma_f32_16x16x32_bf16 v[248:251], a[184:187], v[88:91], v[248:251]\n" \
"ds_read_b128 a[148:151], v48 offset:5120\n" \
"ds_read_b128 a[136:139], v48 offset:2048\n" \
"v_mfma_f32_16x16x32_bf16 v[212:215], a[180:183], v[84:87], v[212:215]\n" \
"ds_read_b128 a[152:155], v48 offset:6144\n" \
"ds_read_b128 a[140:143], v48 offset:3072\n" \
"v_mfma_f32_16x16x32_bf16 v[244:247], a[180:183], v[88:91], v[244:247]\n" \
"ds_read_b128 a[156:159], v48 offset:7168\n" \
"v_add_u32_e32 v52, 0x14000, v63\n" \
"v_mfma_f32_16x16x32_bf16 v[208:211], a[176:179], v[84:87], v[208:211]\n" \
"buffer_load_dwordx4 v51, s[8:11], s64 offen offset:2048 sc1 lds\n" \
"v_mfma_f32_16x16x32_bf16 v[240:243], a[176:179], v[88:91], v[240:243]\n" \
"v_exp_f32_e32 v104, v104\n" \
"v_mfma_f32_16x16x32_bf16 v[204:207], a[172:175], v[84:87], v[204:207]\n" \
"v_exp_f32_e32 v105, v105\n" \
"v_mfma_f32_16x16x32_bf16 v[236:239], a[172:175], v[88:91], v[236:239]\n" \
"buffer_load_dwordx4 v51, s[8:11], s64 offen offset:3072 sc1 lds\n" \
"v_mfma_f32_16x16x32_bf16 v[200:203], a[168:171], v[84:87], v[200:203]\n" \
"v_exp_f32_e32 v106, v106\n" \
"v_mfma_f32_16x16x32_bf16 v[232:235], a[168:171], v[88:91], v[232:235]\n" \
"v_exp_f32_e32 v107, v107\n" \
"v_mfma_f32_16x16x32_bf16 v[196:199], a[164:167], v[84:87], v[196:199]\n" \
"v_cvt_pk_bf16_f32 v104, v104, v105\n" \
"v_cvt_pk_bf16_f32 v105, v106, v107\n" \
"v_mfma_f32_16x16x32_bf16 v[228:231], a[164:167], v[88:91], v[228:231]\n" \
"v_exp_f32_e32 v108, v108\n" \
"v_mfma_f32_16x16x32_bf16 v[192:195], a[160:163], v[84:87], v[192:195]\n" \
"v_exp_f32_e32 v109, v109\n" \
"v_mfma_f32_16x16x32_bf16 v[224:227], a[160:163], v[88:91], v[224:227]\n" \
"v_exp_f32_e32 v110, v110\n" \
"v_smfmac_f32_16x16x64_bf16 a[196:199], a[200:203], v[84:91], v21\n" \
"v_exp_f32_e32 v111, v111\n" \
"v_cvt_pk_bf16_f32 v106, v108, v109\n" \
"v_cvt_pk_bf16_f32 v107, v110, v111\n" \
"v_and_b32_e32 v104, v12, v104\n" \
"v_and_b32_e32 v105, v13, v105\n" \
"v_and_b32_e32 v106, v14, v106\n" \
"v_and_b32_e32 v107, v15, v107\n" \
"v_accvgpr_read_b32 v54, a192\n" \
"v_accvgpr_read_b32 v22, a193\n" \
"v_add_u32_e32 v53, 0x20000, v63\n" \
"v_exp_f32_e32 v118, v118\n" \
"v_exp_f32_e32 v119, v119\n" \
"v_exp_f32_e32 v116, v116\n" \
"v_exp_f32_e32 v117, v117\n" \
"v_cvt_pk_bf16_f32 v119, v118, v119\n" \
"v_exp_f32_e32 v114, v114\n" \
"v_exp_f32_e32 v115, v115\n" \
"v_exp_f32_e32 v112, v112\n" \
"v_exp_f32_e32 v113, v113\n" \
"v_cvt_pk_bf16_f32 v118, v116, v117\n" \
"v_cvt_pk_bf16_f32 v117, v114, v115\n" \
"v_cvt_pk_bf16_f32 v116, v112, v113\n" \
"v_readlane_b32 s93, v11, 19\n" \
"s_waitcnt vmcnt(10) lgkmcnt(0)\n" \
"s_barrier\n" \
"v_mfma_f32_16x16x32_bf16 v[80:83], a[128:131], a[32:35], v[40:43]\n" \
"ds_read_b128 a[160:163], v49 offset:8192\n" \
"ds_read_b128 a[164:167], v49 offset:9216\n" \
"v_mfma_f32_16x16x32_bf16 v[80:83], a[132:135], a[36:39], v[80:83]\n" \
"ds_read_b128 a[168:171], v49 offset:10240\n" \
"s_xor_b32 m0, s65, 0x18000\n" \
"v_mfma_f32_16x16x32_bf16 v[80:83], a[136:139], a[40:43], v[80:83]\n" \
"buffer_load_dwordx4 v52, s[8:11], s64 offen offset:0 sc1 lds\n" \
"v_mfma_f32_16x16x32_bf16 v[80:83], a[140:143], a[44:47], v[80:83]\n" \
"ds_read_b128 a[172:175], v49 offset:11264\n" \
"ds_read_b128 a[176:179], v49 offset:12288\n" \
"v_mfma_f32_16x16x32_bf16 v[84:87], a[156:159], a[44:47], v[40:43]\n" \
"ds_read_b128 a[180:183], v49 offset:13312\n" \
"ds_read_b128 a[184:187], v49 offset:14336\n" \
"v_mfma_f32_16x16x32_bf16 v[84:87], a[152:155], a[40:43], v[84:87]\n" \
"buffer_load_dwordx4 v52, s[8:11], s64 offen offset:1024 sc1 lds\n" \
"v_mfma_f32_16x16x32_bf16 v[84:87], a[148:151], a[36:39], v[84:87]\n" \
"ds_read_b128 a[188:191], v49 offset:15360\n" \
"v_readlane_b32 s80, v11, 11\n" \
"v_mfma_f32_16x16x32_bf16 v[84:87], a[144:147], a[32:35], v[84:87]\n" \
"buffer_load_dwordx4 v52, s[8:11], s64 offen offset:2048 sc1 lds\n" \
"v_mfma_f32_16x16x32_bf16 v[92:95], a[144:147], a[48:51], v[44:47]\n" \
"v_exp_f32_e32 v120, v120\n" \
"v_mfma_f32_16x16x32_bf16 v[92:95], a[148:151], a[52:55], v[92:95]\n" \
"v_exp_f32_e32 v121, v121\n" \
"v_mfma_f32_16x16x32_bf16 v[92:95], a[152:155], a[56:59], v[92:95]\n" \
"buffer_load_dwordx4 v52, s[8:11], s64 offen offset:3072 sc1 lds\n" \
"v_mfma_f32_16x16x32_bf16 v[92:95], a[156:159], a[60:63], v[92:95]\n" \
"s_mov_b32 m0, s65\n" \
"v_cvt_pk_bf16_f32 v120, v120, v121\n" \
"v_mfma_f32_16x16x32_bf16 v[88:91], a[140:143], a[60:63], v[44:47]\n" \
"v_exp_f32_e32 v122, v122\n" \
"v_mfma_f32_16x16x32_bf16 v[88:91], a[136:139], a[56:59], v[88:91]\n" \
"buffer_load_dwordx4 v53, s[8:11], s64 offen offset:0 sc1 lds\n" \
"v_mfma_f32_16x16x32_bf16 v[88:91], a[132:135], a[52:55], v[88:91]\n" \
"v_exp_f32_e32 v123, v123\n" \
"v_mfma_f32_16x16x32_bf16 v[88:91], a[128:131], a[48:51], v[88:91]\n" \
"v_exp_f32_e32 v124, v124\n" \
"v_mfma_f32_16x16x32_bf16 a[192:195], a[212:215], v[104:107], a[192:195]\n" \
"v_cvt_pk_bf16_f32 v121, v122, v123\n" \
"s_waitcnt lgkmcnt(5)\n" \
"v_mfma_f32_16x16x32_bf16 v[160:163], a[160:163], v[104:107], v[160:163]\n" \
"buffer_load_dwordx4 v53, s[8:11], s64 offen offset:1024 sc1 lds\n" \
"v_mfma_f32_16x16x32_bf16 v[164:167], a[164:167], v[104:107], v[164:167]\n" \
"v_exp_f32_e32 v125, v125\n" \
"v_mfma_f32_16x16x32_bf16 v[168:171], a[168:171], v[104:107], v[168:171]\n" \
"v_cvt_pk_bf16_f32 v122, v124, v125\n" \
"s_waitcnt lgkmcnt(1)\n" \
"v_mfma_f32_16x16x32_bf16 v[172:175], a[172:175], v[104:107], v[172:175]\n" \
"v_exp_f32_e32 v126, v126\n" \
"v_mfma_f32_16x16x32_bf16 v[176:179], a[176:179], v[104:107], v[176:179]\n" \
"v_exp_f32_e32 v127, v127\n" \
"v_mfma_f32_16x16x32_bf16 v[180:183], a[180:183], v[104:107], v[180:183]\n" \
"v_cvt_pk_bf16_f32 v123, v126, v127\n" \
"v_readlane_b32 s81, v11, 12\n" \
"v_mfma_f32_16x16x32_bf16 v[184:187], a[184:187], v[104:107], v[184:187]\n" \
"s_mov_b32 s82, 0x47fca0\n" \
"s_waitcnt lgkmcnt(0)\n" \
"v_mfma_f32_16x16x32_bf16 v[188:191], a[188:191], v[104:107], v[188:191]\n" \
"buffer_load_dwordx4 v53, s[8:11], s64 offen offset:2048 sc1 lds\n" \
"v_mfma_f32_16x16x32_bf16 v[220:223], a[188:191], v[116:119], v[220:223]\n" \
"buffer_load_dwordx4 v53, s[8:11], s64 offen offset:3072 sc1 lds\n" \
"v_mfma_f32_16x16x32_bf16 v[252:255], a[188:191], v[120:123], v[252:255]\n" \
"s_mov_b32 s83, 0x20000\n" \
"v_readlane_b32 s92, v11, 26\n" \
"v_mfma_f32_16x16x32_bf16 v[216:219], a[184:187], v[116:119], v[216:219]\n" \
"s_add_u32 m0, s93, 0x0\n" \
"ds_read_b128 a[128:131], v48 offset:8192\n" \
"v_mfma_f32_16x16x32_bf16 v[248:251], a[184:187], v[120:123], v[248:251]\n" \
"ds_read_b128 a[144:147], v48 offset:12288\n" \
"ds_read_b128 a[132:135], v48 offset:9216\n" \
"v_mfma_f32_16x16x32_bf16 v[212:215], a[180:183], v[116:119], v[212:215]\n" \
"buffer_load_dwordx4 v0, s[80:83], s92 offen offset:0 lds\n" \
"v_mfma_f32_16x16x32_bf16 v[244:247], a[180:183], v[120:123], v[244:247]\n" \
"buffer_load_dwordx4 v1, s[80:83], s92 offen offset:1024 lds\n" \
"v_mfma_f32_16x16x32_bf16 v[208:211], a[176:179], v[116:119], v[208:211]\n" \
"ds_read_b128 a[148:151], v48 offset:13312\n" \
"ds_read_b128 a[136:139], v48 offset:10240\n" \
"v_mfma_f32_16x16x32_bf16 v[240:243], a[176:179], v[120:123], v[240:243]\n" \
"ds_read_b128 a[152:155], v48 offset:14336\n" \
"ds_read_b128 a[140:143], v48 offset:11264\n" \
"v_mfma_f32_16x16x32_bf16 v[204:207], a[172:175], v[116:119], v[204:207]\n" \
"buffer_load_dwordx4 v2, s[80:83], s92 offen offset:2048 lds\n" \
"v_mfma_f32_16x16x32_bf16 v[236:239], a[172:175], v[120:123], v[236:239]\n" \
"ds_read_b128 a[156:159], v48 offset:15360\n" \
"s_xor_b32 s65, s65, 0x8000\n" \
"v_mfma_f32_16x16x32_bf16 v[200:203], a[168:171], v[116:119], v[200:203]\n" \
"s_add_u32 s64, s64, 0x10000\n" \
"v_add_u32_e32 v49, 0x10000, v48\n" \
"v_mfma_f32_16x16x32_bf16 v[232:235], a[168:171], v[120:123], v[232:235]\n" \
"buffer_load_dwordx4 v3, s[80:83], s92 offen offset:3072 lds\n" \
"v_mfma_f32_16x16x32_bf16 v[196:199], a[164:167], v[116:119], v[196:199]\n" \
"v_exp_f32_e32 v86, v86\n" \
"v_mfma_f32_16x16x32_bf16 v[228:231], a[164:167], v[120:123], v[228:231]\n" \
"v_exp_f32_e32 v87, v87\n" \
"v_mfma_f32_16x16x32_bf16 v[192:195], a[160:163], v[116:119], v[192:195]\n" \
"v_exp_f32_e32 v84, v84\n" \
"v_mfma_f32_16x16x32_bf16 v[224:227], a[160:163], v[120:123], v[224:227]\n" \
"v_exp_f32_e32 v85, v85\n" \
"v_smfmac_f32_16x16x64_bf16 a[196:199], a[200:203], v[116:123], v21\n" \
"v_exp_f32_e32 v88, v88\n" \
"v_exp_f32_e32 v89, v89\n" \
"v_cvt_pk_bf16_f32 v87, v86, v87\n" \
"v_exp_f32_e32 v82, v82\n" \
"v_exp_f32_e32 v83, v83\n" \
"v_exp_f32_e32 v80, v80\n" \
"v_exp_f32_e32 v81, v81\n" \
"v_exp_f32_e32 v90, v90\n" \
"v_exp_f32_e32 v91, v91\n" \
"v_cvt_pk_bf16_f32 v86, v84, v85\n" \
"v_cvt_pk_bf16_f32 v88, v88, v89\n" \
"v_exp_f32_e32 v92, v92\n" \
"v_exp_f32_e32 v93, v93\n" \
"v_exp_f32_e32 v94, v94\n" \
"v_exp_f32_e32 v95, v95\n" \
"v_cvt_pk_bf16_f32 v85, v82, v83\n" \
"v_cvt_pk_bf16_f32 v84, v80, v81\n" \
"v_cvt_pk_bf16_f32 v89, v90, v91\n" \
"v_and_b32_e32 v84, v12, v84\n" \
"v_and_b32_e32 v85, v13, v85\n" \
"v_and_b32_e32 v86, v14, v86\n" \
"v_and_b32_e32 v87, v15, v87\n" \
"v_cvt_pk_bf16_f32 v90, v92, v93\n" \
"v_cvt_pk_bf16_f32 v91, v94, v95\n" \
"v_add_u32_e32 v48, s16, v20\n" \
"v_add_f32_e32 v54, v54, v22\n" \
"v_accvgpr_read_b32 v55, a194\n" \
"v_accvgpr_read_b32 v23, a195\n" \
"v_mfma_f32_16x16x32_bf16 a[192:195], a[216:219], a[216:219], 0\n" \
"v_max_f32_e32 v62, v62, v54\n" \
"v_add_f32_e32 v55, v55, v23\n" \
"v_max_f32_e32 v62, v62, v55\n" \
"v_rcp_f32_e32 v54, v54\n" \
"v_rcp_f32_e32 v55, v55\n" \
"v_mul_f32_e32 v54, v54, v58\n" \
"v_mul_f32_e32 v55, v55, v59\n" \
"v_mul_f32_e32 v128, v54, v128\n" \
"v_mul_f32_e32 v129, v54, v129\n" \
"v_mul_f32_e32 v136, v54, v136\n" \
"v_mul_f32_e32 v137, v54, v137\n" \
"v_mul_f32_e32 v144, v54, v144\n" \
"v_mul_f32_e32 v145, v54, v145\n" \
"v_mul_f32_e32 v152, v54, v152\n" \
"v_mul_f32_e32 v153, v54, v153\n" \
"v_mul_f32_e32 v160, v55, v160\n" \
"v_mul_f32_e32 v161, v55, v161\n" \
"v_mul_f32_e32 v168, v55, v168\n" \
"v_mul_f32_e32 v169, v55, v169\n" \
"v_mul_f32_e32 v176, v55, v176\n" \
"v_mul_f32_e32 v177, v55, v177\n" \
"v_mul_f32_e32 v184, v55, v184\n" \
"v_mul_f32_e32 v185, v55, v185\n" \
"v_cvt_pk_bf16_f32 v128, v128, v129\n" \
"v_mul_f32_e32 v130, v54, v130\n" \
"v_mul_f32_e32 v131, v54, v131\n" \
"v_cvt_pk_bf16_f32 v136, v136, v137\n" \
"v_mul_f32_e32 v138, v54, v138\n" \
"v_mul_f32_e32 v139, v54, v139\n" \
"v_cvt_pk_bf16_f32 v144, v144, v145\n" \
"v_mul_f32_e32 v146, v54, v146\n" \
"v_mul_f32_e32 v147, v54, v147\n" \
"v_cvt_pk_bf16_f32 v152, v152, v153\n" \
"v_mul_f32_e32 v154, v54, v154\n" \
"v_mul_f32_e32 v155, v54, v155\n" \
"v_cvt_pk_bf16_f32 v160, v160, v161\n" \
"v_mul_f32_e32 v162, v55, v162\n" \
"v_mul_f32_e32 v163, v55, v163\n" \
"v_cvt_pk_bf16_f32 v168, v168, v169\n" \
"v_mul_f32_e32 v170, v55, v170\n" \
"v_mul_f32_e32 v171, v55, v171\n" \
"v_cvt_pk_bf16_f32 v176, v176, v177\n" \
"v_mul_f32_e32 v178, v55, v178\n" \
"v_mul_f32_e32 v179, v55, v179\n" \
"v_cvt_pk_bf16_f32 v184, v184, v185\n" \
"v_mul_f32_e32 v186, v55, v186\n" \
"v_mul_f32_e32 v187, v55, v187\n" \
"v_cvt_pk_bf16_f32 v129, v130, v131\n" \
"v_mul_f32_e32 v132, v54, v132\n" \
"v_mul_f32_e32 v133, v54, v133\n" \
"v_mul_f32_e32 v134, v54, v134\n" \
"v_mul_f32_e32 v135, v54, v135\n" \
"v_cvt_pk_bf16_f32 v137, v138, v139\n" \
"v_mul_f32_e32 v140, v54, v140\n" \
"s_waitcnt vmcnt(12) lgkmcnt(0)\n" \
"s_barrier\n" \
"v_mfma_f32_16x16x32_bf16 v[120:123], a[128:131], a[48:51], v[44:47]\n" \
"s_xor_b32 m0, s65, 0x12000\n" \
"ds_read_b128 a[188:191], v49 offset:7168\n" \
"v_mfma_f32_16x16x32_bf16 v[120:123], a[132:135], a[52:55], v[120:123]\n" \
"buffer_load_dwordx4 v50, s[8:11], s64 offen offset:0 sc1 lds\n" \
"v_mfma_f32_16x16x32_bf16 v[120:123], a[136:139], a[56:59], v[120:123]\n" \
"ds_read_b128 a[184:187], v49 offset:6144\n" \
"ds_read_b128 a[180:183], v49 offset:5120\n" \
"v_mfma_f32_16x16x32_bf16 v[120:123], a[140:143], a[60:63], v[120:123]\n" \
"buffer_load_dwordx4 v50, s[8:11], s64 offen offset:1024 sc1 lds\n" \
"v_mfma_f32_16x16x32_bf16 v[124:127], a[156:159], a[60:63], v[44:47]\n" \
"ds_read_b128 a[176:179], v49 offset:4096\n" \
"ds_read_b128 a[172:175], v49 offset:3072\n" \
"v_mfma_f32_16x16x32_bf16 v[124:127], a[152:155], a[56:59], v[124:127]\n" \
"ds_read_b128 a[168:171], v49 offset:2048\n" \
"ds_read_b128 a[164:167], v49 offset:1024\n" \
"v_mfma_f32_16x16x32_bf16 v[124:127], a[148:151], a[52:55], v[124:127]\n" \
"ds_read_b128 a[160:163], v49 offset:0\n" \
"ds_read_b128 a[0:3], v49 offset:8192\n" \
"v_mfma_f32_16x16x32_bf16 v[124:127], a[144:147], a[48:51], v[124:127]\n" \
"buffer_load_dwordx4 v50, s[8:11], s64 offen offset:2048 sc1 lds\n" \
"v_mfma_f32_16x16x32_bf16 v[32:35], a[220:223], a[64:67], 0\n" \
"ds_read_b128 a[4:7], v49 offset:9216\n" \
"ds_read_b128 a[8:11], v49 offset:10240\n" \
"v_mfma_f32_16x16x32_bf16 v[36:39], a[220:223], a[80:83], 0\n" \
"ds_read_b128 a[12:15], v49 offset:11264\n" \
"ds_read_b128 a[16:19], v49 offset:12288\n" \
"v_mfma_f32_16x16x32_bf16 v[40:43], a[220:223], a[96:99], 0\n" \
"ds_read_b128 a[20:23], v49 offset:13312\n" \
"ds_read_b128 a[24:27], v49 offset:14336\n" \
"v_mfma_f32_16x16x32_bf16 v[44:47], a[220:223], a[112:115], 0\n" \
"buffer_load_dwordx4 v50, s[8:11], s64 offen offset:3072 sc1 lds\n" \
"v_mfma_f32_16x16x32_bf16 v[32:35], a[224:227], a[68:71], v[32:35]\n" \
"s_xor_b32 m0, s65, 0xa000\n" \
"ds_read_b128 a[28:31], v49 offset:15360\n" \
"v_mfma_f32_16x16x32_bf16 v[36:39], a[224:227], a[84:87], v[36:39]\n" \
"v_exp_f32_e32 v120, v120\n" \
"v_mfma_f32_16x16x32_bf16 v[40:43], a[224:227], a[100:103], v[40:43]\n" \
"buffer_load_dwordx4 v51, s[8:11], s64 offen offset:0 sc1 lds\n" \
"v_mfma_f32_16x16x32_bf16 v[44:47], a[224:227], a[116:119], v[44:47]\n" \
"v_exp_f32_e32 v121, v121\n" \
"v_mfma_f32_16x16x32_bf16 v[32:35], a[228:231], a[72:75], v[32:35]\n" \
"v_exp_f32_e32 v122, v122\n" \
"v_mfma_f32_16x16x32_bf16 v[36:39], a[228:231], a[88:91], v[36:39]\n" \
"v_exp_f32_e32 v123, v123\n" \
"v_mfma_f32_16x16x32_bf16 v[40:43], a[228:231], a[104:107], v[40:43]\n" \
"buffer_load_dwordx4 v51, s[8:11], s64 offen offset:1024 sc1 lds\n" \
"v_mfma_f32_16x16x32_bf16 v[44:47], a[228:231], a[120:123], v[44:47]\n" \
"v_cvt_pk_bf16_f32 v120, v120, v121\n" \
"v_cvt_pk_bf16_f32 v121, v122, v123\n" \
"v_mfma_f32_16x16x32_bf16 v[32:35], a[232:235], a[76:79], v[32:35]\n" \
"v_exp_f32_e32 v124, v124\n" \
"v_mfma_f32_16x16x32_bf16 v[36:39], a[232:235], a[92:95], v[36:39]\n" \
"v_exp_f32_e32 v125, v125\n" \
"v_mfma_f32_16x16x32_bf16 v[40:43], a[232:235], a[108:111], v[40:43]\n" \
"buffer_load_dwordx4 v51, s[8:11], s64 offen offset:2048 sc1 lds\n" \
"v_mfma_f32_16x16x32_bf16 v[44:47], a[232:235], a[124:127], v[44:47]\n" \
"v_exp_f32_e32 v126, v126\n" \
"v_smfmac_f32_16x16x64_bf16 a[196:199], a[200:203], v[84:91], v21\n" \
"v_cvt_pk_bf16_f32 v122, v124, v125\n" \
"s_waitcnt lgkmcnt(1)\n" \
"v_mfma_f32_16x16x32_bf16 v[220:223], a[188:191], v[84:87], v[220:223]\n" \
"buffer_load_dwordx4 v51, s[8:11], s64 offen offset:3072 sc1 lds\n" \
"v_mfma_f32_16x16x32_bf16 v[252:255], a[188:191], v[88:91], v[252:255]\n" \
"s_add_u32 m0, s93, 0x1000\n" \
"v_and_b32_e32 v120, v12, v120\n" \
"v_mfma_f32_16x16x32_bf16 v[216:219], a[184:187], v[84:87], v[216:219]\n" \
"v_exp_f32_e32 v127, v127\n" \
"v_mfma_f32_16x16x32_bf16 v[248:251], a[184:187], v[88:91], v[248:251]\n" \
"v_cvt_pk_bf16_f32 v123, v126, v127\n" \
"v_and_b32_e32 v121, v13, v121\n" \
"v_mfma_f32_16x16x32_bf16 v[212:215], a[180:183], v[84:87], v[212:215]\n" \
"buffer_load_dwordx4 v4, s[80:83], s92 offen offset:0 lds\n" \
"v_mfma_f32_16x16x32_bf16 v[244:247], a[180:183], v[88:91], v[244:247]\n" \
"v_and_b32_e32 v122, v14, v122\n" \
"v_and_b32_e32 v123, v15, v123\n" \
"v_mfma_f32_16x16x32_bf16 v[208:211], a[176:179], v[84:87], v[208:211]\n" \
"v_accvgpr_read_b32 v56, a196\n" \
"v_accvgpr_read_b32 v24, a197\n" \
"v_mfma_f32_16x16x32_bf16 v[240:243], a[176:179], v[88:91], v[240:243]\n" \
"ds_read_b128 a[128:131], v48 offset:0\n" \
"ds_read_b128 a[144:147], v48 offset:4096\n" \
"v_mfma_f32_16x16x32_bf16 v[204:207], a[172:175], v[84:87], v[204:207]\n" \
"buffer_load_dwordx4 v5, s[80:83], s92 offen offset:1024 lds\n" \
"v_mfma_f32_16x16x32_bf16 v[236:239], a[172:175], v[88:91], v[236:239]\n" \
"ds_read_b128 a[132:135], v48 offset:1024\n" \
"ds_read_b128 a[148:151], v48 offset:5120\n" \
"v_mfma_f32_16x16x32_bf16 v[200:203], a[168:171], v[84:87], v[200:203]\n" \
"ds_read_b128 a[136:139], v48 offset:2048\n" \
"ds_read_b128 a[152:155], v48 offset:6144\n" \
"v_mfma_f32_16x16x32_bf16 v[232:235], a[168:171], v[88:91], v[232:235]\n" \
"buffer_load_dwordx4 v6, s[80:83], s92 offen offset:2048 lds\n" \
"v_mfma_f32_16x16x32_bf16 v[196:199], a[164:167], v[84:87], v[196:199]\n" \
"ds_read_b128 a[140:143], v48 offset:3072\n" \
"ds_read_b128 a[156:159], v48 offset:7168\n" \
"v_mfma_f32_16x16x32_bf16 v[228:231], a[164:167], v[88:91], v[228:231]\n" \
"v_mul_f32_e32 v141, v54, v141\n" \
"v_mul_f32_e32 v142, v54, v142\n" \
"v_mfma_f32_16x16x32_bf16 v[192:195], a[160:163], v[84:87], v[192:195]\n" \
"v_mul_f32_e32 v143, v54, v143\n" \
"v_cvt_pk_bf16_f32 v145, v146, v147\n" \
"v_mfma_f32_16x16x32_bf16 v[224:227], a[160:163], v[88:91], v[224:227]\n" \
"buffer_load_dwordx4 v7, s[80:83], s92 offen offset:3072 lds\n" \
"v_mfma_f32_16x16x32_bf16 a[196:199], a[212:215], v[120:123], a[196:199]\n" \
"v_mul_f32_e32 v148, v54, v148\n" \
"v_mul_f32_e32 v149, v54, v149\n" \
"v_mfma_f32_16x16x32_bf16 v[224:227], a[0:3], v[120:123], v[224:227]\n" \
"v_mul_f32_e32 v150, v54, v150\n" \
"v_mul_f32_e32 v151, v54, v151\n" \
"v_mfma_f32_16x16x32_bf16 v[228:231], a[4:7], v[120:123], v[228:231]\n" \
"v_cvt_pk_bf16_f32 v153, v154, v155\n" \
"v_mul_f32_e32 v156, v54, v156\n" \
"v_mfma_f32_16x16x32_bf16 v[232:235], a[8:11], v[120:123], v[232:235]\n" \
"v_mul_f32_e32 v157, v54, v157\n" \
"v_mul_f32_e32 v158, v54, v158\n" \
"v_mfma_f32_16x16x32_bf16 v[236:239], a[12:15], v[120:123], v[236:239]\n" \
"v_mul_f32_e32 v159, v54, v159\n" \
"v_cvt_pk_bf16_f32 v161, v162, v163\n" \
"v_mfma_f32_16x16x32_bf16 v[240:243], a[16:19], v[120:123], v[240:243]\n" \
"v_mul_f32_e32 v164, v55, v164\n" \
"v_mul_f32_e32 v165, v55, v165\n" \
"v_mfma_f32_16x16x32_bf16 v[244:247], a[20:23], v[120:123], v[244:247]\n" \
"v_mul_f32_e32 v166, v55, v166\n" \
"v_mul_f32_e32 v167, v55, v167\n" \
"v_mfma_f32_16x16x32_bf16 v[248:251], a[24:27], v[120:123], v[248:251]\n" \
"v_cvt_pk_bf16_f32 v169, v170, v171\n" \
"s_waitcnt lgkmcnt(4)\n" \
"v_mfma_f32_16x16x32_bf16 v[252:255], a[28:31], v[120:123], v[252:255]\n" \
"v_mul_f32_e32 v172, v55, v172\n" \
"v_mul_f32_e32 v173, v55, v173\n" \
"v_mul_f32_e32 v174, v55, v174\n" \
"v_mul_f32_e32 v175, v55, v175\n" \
"v_cvt_pk_bf16_f32 v177, v178, v179\n" \
"v_mul_f32_e32 v180, v55, v180\n" \
"v_mul_f32_e32 v181, v55, v181\n" \
"v_mul_f32_e32 v182, v55, v182\n" \
"v_mul_f32_e32 v183, v55, v183\n" \
"v_cvt_pk_bf16_f32 v185, v186, v187\n" \
"v_mul_f32_e32 v188, v55, v188\n" \
"v_mul_f32_e32 v189, v55, v189\n" \
"v_mul_f32_e32 v190, v55, v190\n" \
"v_mul_f32_e32 v191, v55, v191\n" \
"v_cvt_pk_bf16_f32 v130, v132, v133\n" \
"v_cvt_pk_bf16_f32 v131, v134, v135\n" \
"buffer_store_dwordx4 v[128:131], v20, s[56:59], s68 offen offset:0 sc1\n" \
"v_cvt_pk_bf16_f32 v138, v140, v141\n" \
"v_cvt_pk_bf16_f32 v139, v142, v143\n" \
"buffer_store_dwordx4 v[136:139], v20, s[56:59], s68 offen offset:1024 sc1\n" \
"v_cvt_pk_bf16_f32 v146, v148, v149\n" \
"v_cvt_pk_bf16_f32 v147, v150, v151\n" \
"buffer_store_dwordx4 v[144:147], v20, s[56:59], s68 offen offset:2048 sc1\n" \
"v_cvt_pk_bf16_f32 v154, v156, v157\n" \
"v_cvt_pk_bf16_f32 v155, v158, v159\n" \
"buffer_store_dwordx4 v[152:155], v20, s[56:59], s68 offen offset:3072 sc1\n" \
"v_cvt_pk_bf16_f32 v162, v164, v165\n" \
"v_cvt_pk_bf16_f32 v163, v166, v167\n" \
"buffer_store_dwordx4 v[160:163], v20, s[56:59], s69 offen offset:0 sc1\n" \
"v_cvt_pk_bf16_f32 v170, v172, v173\n" \
"v_cvt_pk_bf16_f32 v171, v174, v175\n" \
"buffer_store_dwordx4 v[168:171], v20, s[56:59], s69 offen offset:1024 sc1\n" \
"v_cvt_pk_bf16_f32 v178, v180, v181\n" \
"v_cvt_pk_bf16_f32 v179, v182, v183\n" \
"buffer_store_dwordx4 v[176:179], v20, s[56:59], s69 offen offset:2048 sc1\n" \
"v_cvt_pk_bf16_f32 v186, v188, v189\n" \
"v_cvt_pk_bf16_f32 v187, v190, v191\n" \
"buffer_store_dwordx4 v[184:187], v20, s[56:59], s69 offen offset:3072 sc1\n" \
"s_add_u32 m0, s93, 0x2000\n" \
"v_mfma_f32_16x16x32_bf16 a[208:211], a[216:219], v[128:131], a[208:211]\n" \
"buffer_load_dwordx4 v8, s[80:83], s92 offen offset:0 lds\n" \
"v_mfma_f32_16x16x32_bf16 a[208:211], a[216:219], v[136:139], a[208:211]\n" \
"v_mfma_f32_32x32x16_bf16 v[128:143], a[216:219], a[216:219], 0\n" \
"v_mfma_f32_16x16x32_bf16 a[208:211], a[216:219], v[144:147], a[208:211]\n" \
"v_mfma_f32_16x16x32_bf16 a[208:211], a[216:219], v[152:155], a[208:211]\n" \
"v_mfma_f32_32x32x16_bf16 v[144:159], a[216:219], a[216:219], 0\n" \
"v_add_f32_e32 v56, v56, v24\n" \
"v_mfma_f32_16x16x32_bf16 a[208:211], a[216:219], v[160:163], a[208:211]\n" \
"v_max_f32_e32 v62, v62, v56\n" \
"v_accvgpr_read_b32 v57, a198\n" \
"s_waitcnt vmcnt(21) lgkmcnt(0)\n" \
"s_barrier\n" \
"v_mfma_f32_16x16x32_bf16 v[64:67], a[128:131], a[64:67], v[32:35]\n" \
"s_xor_b32 m0, s65, 0x18000\n" \
"v_accvgpr_read_b32 v25, a199\n" \
"v_mfma_f32_16x16x32_bf16 a[196:199], a[216:219], a[216:219], 0\n" \
"buffer_load_dwordx4 v52, s[8:11], s64 offen offset:0 sc1 lds\n" \
"v_mfma_f32_16x16x32_bf16 v[64:67], a[132:135], a[68:71], v[64:67]\n" \
"v_rcp_f32_e32 v56, v56\n" \
"v_mfma_f32_16x16x32_bf16 v[64:67], a[136:139], a[72:75], v[64:67]\n" \
"v_mfma_f32_16x16x32_bf16 a[208:211], a[216:219], v[168:171], a[208:211]\n" \
"v_mfma_f32_32x32x16_bf16 v[160:175], a[216:219], a[216:219], 0\n" \
"v_mul_f32_e32 v56, v56, v60\n" \
"v_mfma_f32_16x16x32_bf16 a[208:211], a[216:219], v[176:179], a[208:211]\n" \
"v_mul_f32_e32 v192, v56, v192\n" \
"v_mul_f32_e32 v193, v56, v193\n" \
"v_mfma_f32_16x16x32_bf16 v[64:67], a[140:143], a[76:79], v[64:67]\n" \
"buffer_load_dwordx4 v52, s[8:11], s64 offen offset:1024 sc1 lds\n" \
"v_mfma_f32_16x16x32_bf16 v[68:71], a[156:159], a[76:79], v[32:35]\n" \
"v_cvt_pk_bf16_f32 v192, v192, v193\n" \
"v_mul_f32_e32 v194, v56, v194\n" \
"v_mfma_f32_16x16x32_bf16 v[68:71], a[152:155], a[72:75], v[68:71]\n" \
"v_mul_f32_e32 v195, v56, v195\n" \
"v_cvt_pk_bf16_f32 v193, v194, v195\n" \
"v_mfma_f32_16x16x32_bf16 v[68:71], a[148:151], a[68:71], v[68:71]\n" \
"v_mfma_f32_16x16x32_bf16 a[208:211], a[216:219], v[184:187], a[208:211]\n" \
"v_mfma_f32_32x32x16_bf16 v[176:191], a[216:219], a[216:219], 0\n" \
"v_mul_f32_e32 v196, v56, v196\n" \
"v_mul_f32_e32 v197, v56, v197\n" \
"v_mul_f32_e32 v198, v56, v198\n" \
"v_mul_f32_e32 v199, v56, v199\n" \
"v_mul_f32_e32 v200, v56, v200\n" \
"v_mul_f32_e32 v201, v56, v201\n" \
"v_mfma_f32_16x16x32_bf16 v[68:71], a[144:147], a[64:67], v[68:71]\n" \
"buffer_load_dwordx4 v52, s[8:11], s64 offen offset:2048 sc1 lds\n" \
"v_mfma_f32_16x16x32_bf16 v[76:79], a[144:147], a[80:83], v[36:39]\n" \
"v_cvt_pk_bf16_f32 v194, v196, v197\n" \
"v_cvt_pk_bf16_f32 v195, v198, v199\n" \
"buffer_store_dwordx4 v[192:195], v20, s[56:59], s70 offen offset:0 sc1\n" \
"v_mfma_f32_16x16x32_bf16 v[76:79], a[148:151], a[84:87], v[76:79]\n" \
"v_cvt_pk_bf16_f32 v200, v200, v201\n" \
"v_mul_f32_e32 v202, v56, v202\n" \
"v_mfma_f32_16x16x32_bf16 v[76:79], a[152:155], a[88:91], v[76:79]\n" \
"v_mul_f32_e32 v203, v56, v203\n" \
"v_add_f32_e32 v57, v57, v25\n" \
"v_mfma_f32_16x16x32_bf16 v[76:79], a[156:159], a[92:95], v[76:79]\n" \
"buffer_load_dwordx4 v52, s[8:11], s64 offen offset:3072 sc1 lds\n" \
"v_mfma_f32_16x16x32_bf16 v[72:75], a[140:143], a[92:95], v[36:39]\n" \
"s_mov_b32 m0, s65\n" \
"v_cvt_pk_bf16_f32 v201, v202, v203\n" \
"v_mfma_f32_16x16x32_bf16 v[72:75], a[136:139], a[88:91], v[72:75]\n" \
"v_mfma_f32_16x16x32_bf16 a[208:211], a[216:219], v[192:195], a[208:211]\n" \
"v_mul_f32_e32 v204, v56, v204\n" \
"v_mul_f32_e32 v205, v56, v205\n" \
"v_mfma_f32_16x16x32_bf16 v[72:75], a[132:135], a[84:87], v[72:75]\n" \
"v_mul_f32_e32 v206, v56, v206\n" \
"v_mul_f32_e32 v207, v56, v207\n" \
"v_mfma_f32_16x16x32_bf16 v[72:75], a[128:131], a[80:83], v[72:75]\n" \
"buffer_load_dwordx4 v53, s[8:11], s64 offen offset:0 sc1 lds\n" \
"v_mfma_f32_16x16x32_bf16 v[80:83], a[128:131], a[96:99], v[40:43]\n" \
"v_mul_f32_e32 v208, v56, v208\n" \
"v_mul_f32_e32 v209, v56, v209\n" \
"v_mfma_f32_16x16x32_bf16 v[80:83], a[132:135], a[100:103], v[80:83]\n" \
"v_max_f32_e32 v62, v62, v57\n" \
"v_cvt_pk_bf16_f32 v202, v204, v205\n" \
"v_mfma_f32_16x16x32_bf16 v[80:83], a[136:139], a[104:107], v[80:83]\n" \
"buffer_load_dwordx4 v53, s[8:11], s64 offen offset:1024 sc1 lds\n" \
"v_mfma_f32_16x16x32_bf16 v[80:83], a[140:143], a[108:111], v[80:83]\n" \
"v_cvt_pk_bf16_f32 v203, v206, v207\n" \
"v_cvt_pk_bf16_f32 v208, v208, v209\n" \
"buffer_store_dwordx4 v[200:203], v20, s[56:59], s70 offen offset:1024 sc1\n" \
"v_mfma_f32_16x16x32_bf16 v[84:87], a[156:159], a[108:111], v[40:43]\n" \
"v_mul_f32_e32 v210, v56, v210\n" \
"v_mul_f32_e32 v211, v56, v211\n" \
"v_mfma_f32_16x16x32_bf16 v[84:87], a[152:155], a[104:107], v[84:87]\n" \
"v_rcp_f32_e32 v57, v57\n" \
"v_mfma_f32_16x16x32_bf16 v[84:87], a[148:151], a[100:103], v[84:87]\n" \
"buffer_load_dwordx4 v53, s[8:11], s64 offen offset:2048 sc1 lds\n" \
"v_mfma_f32_16x16x32_bf16 v[84:87], a[144:147], a[96:99], v[84:87]\n" \
"v_mfma_f32_16x16x32_bf16 a[208:211], a[216:219], v[200:203], a[208:211]\n" \
"v_mfma_f32_32x32x16_bf16 v[192:207], a[216:219], a[216:219], 0\n" \
"v_cvt_pk_bf16_f32 v209, v210, v211\n" \
"v_mul_f32_e32 v212, v56, v212\n" \
".fill (L_ab + 20480 - .) / 4, 4, 0xBF800000\n" \
"L_ap5:\n" \
"s_cbranch_execz L_ap0\n" \
"v_mul_f32_e32 v213, v56, v213\n" \
"v_mul_f32_e32 v214, v56, v214\n" \
"v_mul_f32_e32 v215, v56, v215\n" \
"v_mul_f32_e32 v216, v56, v216\n" \
"v_mfma_f32_16x16x32_bf16 v[92:95], a[144:147], a[112:115], v[44:47]\n" \
"ds_read_b128 a[144:147], v48 offset:12288\n" \
"v_mul_f32_e32 v217, v56, v217\n" \
"v_mfma_f32_16x16x32_bf16 v[92:95], a[148:151], a[116:119], v[92:95]\n" \
"ds_read_b128 a[148:151], v48 offset:13312\n" \
"v_cvt_pk_bf16_f32 v210, v212, v213\n" \
"v_mfma_f32_16x16x32_bf16 v[92:95], a[152:155], a[120:123], v[92:95]\n" \
"ds_read_b128 a[152:155], v48 offset:14336\n" \
"v_cvt_pk_bf16_f32 v211, v214, v215\n" \
"buffer_store_dwordx4 v[208:211], v20, s[56:59], s70 offen offset:2048 sc1\n" \
"v_mfma_f32_16x16x32_bf16 v[92:95], a[156:159], a[124:127], v[92:95]\n" \
"ds_read_b128 a[156:159], v48 offset:15360\n" \
"v_cvt_pk_bf16_f32 v216, v216, v217\n" \
"v_mfma_f32_16x16x32_bf16 v[88:91], a[140:143], a[124:127], v[44:47]\n" \
"ds_read_b128 a[140:143], v48 offset:11264\n" \
"v_mul_f32_e32 v218, v56, v218\n" \
"v_mfma_f32_16x16x32_bf16 v[88:91], a[136:139], a[120:123], v[88:91]\n" \
"ds_read_b128 a[136:139], v48 offset:10240\n" \
"v_mul_f32_e32 v219, v56, v219\n" \
"v_mfma_f32_16x16x32_bf16 v[88:91], a[132:135], a[116:119], v[88:91]\n" \
"ds_read_b128 a[132:135], v48 offset:9216\n" \
"v_mul_f32_e32 v57, v57, v61\n" \
"v_mfma_f32_16x16x32_bf16 v[88:91], a[128:131], a[112:115], v[88:91]\n" \
"ds_read_b128 a[128:131], v48 offset:8192\n" \
"v_mfma_f32_16x16x32_bf16 a[208:211], a[216:219], v[208:211], a[208:211]\n" \
"v_cvt_pk_bf16_f32 v217, v218, v219\n" \
"v_mul_f32_e32 v220, v56, v220\n" \
"v_mul_f32_e32 v221, v56, v221\n" \
"v_mul_f32_e32 v222, v56, v222\n" \
"v_mul_f32_e32 v223, v56, v223\n" \
"v_mul_f32_e32 v224, v57, v224\n" \
"v_mul_f32_e32 v225, v57, v225\n" \
"v_cvt_pk_bf16_f32 v218, v220, v221\n" \
"v_cvt_pk_bf16_f32 v219, v222, v223\n" \
"buffer_store_dwordx4 v[216:219], v20, s[56:59], s70 offen offset:3072 sc1\n" \
"v_cvt_pk_bf16_f32 v224, v224, v225\n" \
"v_mul_f32_e32 v226, v57, v226\n" \
"v_mul_f32_e32 v227, v57, v227\n" \
"v_mfma_f32_16x16x32_bf16 a[208:211], a[216:219], v[216:219], a[208:211]\n" \
"v_mfma_f32_32x32x16_bf16 v[208:223], a[216:219], a[216:219], 0\n" \
"v_cvt_pk_bf16_f32 v225, v226, v227\n" \
"v_mul_f32_e32 v228, v57, v228\n" \
"v_mul_f32_e32 v229, v57, v229\n" \
"v_mul_f32_e32 v230, v57, v230\n" \
"v_mul_f32_e32 v231, v57, v231\n" \
"v_mul_f32_e32 v232, v57, v232\n" \
"v_mul_f32_e32 v233, v57, v233\n" \
"v_cvt_pk_bf16_f32 v226, v228, v229\n" \
"v_cvt_pk_bf16_f32 v227, v230, v231\n" \
"buffer_store_dwordx4 v[224:227], v20, s[56:59], s71 offen offset:0 sc1\n" \
"v_cvt_pk_bf16_f32 v232, v232, v233\n" \
"v_mul_f32_e32 v234, v57, v234\n" \
"v_mul_f32_e32 v235, v57, v235\n" \
"v_mfma_f32_16x16x32_bf16 a[208:211], a[216:219], v[224:227], a[208:211]\n" \
"v_cvt_pk_bf16_f32 v233, v234, v235\n" \
"v_mul_f32_e32 v236, v57, v236\n" \
"v_mul_f32_e32 v237, v57, v237\n" \
"v_mul_f32_e32 v238, v57, v238\n" \
"v_mul_f32_e32 v239, v57, v239\n" \
"v_mul_f32_e32 v240, v57, v240\n" \
"v_mul_f32_e32 v241, v57, v241\n" \
"v_cvt_pk_bf16_f32 v234, v236, v237\n" \
"v_cvt_pk_bf16_f32 v235, v238, v239\n" \
"buffer_store_dwordx4 v[232:235], v20, s[56:59], s71 offen offset:1024 sc1\n" \
"v_cvt_pk_bf16_f32 v240, v240, v241\n" \
"v_mul_f32_e32 v242, v57, v242\n" \
"v_mul_f32_e32 v243, v57, v243\n" \
"v_mfma_f32_16x16x32_bf16 a[208:211], a[216:219], v[232:235], a[208:211]\n" \
"v_mfma_f32_32x32x16_bf16 v[224:239], a[216:219], a[216:219], 0\n" \
"v_cvt_pk_bf16_f32 v241, v242, v243\n" \
"v_mul_f32_e32 v244, v57, v244\n" \
"v_mul_f32_e32 v245, v57, v245\n" \
"v_mul_f32_e32 v246, v57, v246\n" \
"v_mul_f32_e32 v247, v57, v247\n" \
"v_mul_f32_e32 v248, v57, v248\n" \
"v_mul_f32_e32 v249, v57, v249\n" \
"v_cvt_pk_bf16_f32 v242, v244, v245\n" \
"v_cvt_pk_bf16_f32 v243, v246, v247\n" \
"buffer_store_dwordx4 v[240:243], v20, s[56:59], s71 offen offset:2048 sc1\n" \
"v_cvt_pk_bf16_f32 v248, v248, v249\n" \
"v_mul_f32_e32 v250, v57, v250\n" \
"v_mul_f32_e32 v251, v57, v251\n" \
"v_mfma_f32_16x16x32_bf16 a[208:211], a[216:219], v[240:243], a[208:211]\n" \
"v_cvt_pk_bf16_f32 v249, v250, v251\n" \
"v_mul_f32_e32 v252, v57, v252\n" \
"v_mul_f32_e32 v253, v57, v253\n" \
"v_mul_f32_e32 v254, v57, v254\n" \
"v_mul_f32_e32 v255, v57, v255\n" \
"v_cvt_pk_bf16_f32 v250, v252, v253\n" \
"v_cvt_pk_bf16_f32 v251, v254, v255\n" \
"buffer_store_dwordx4 v[248:251], v20, s[56:59], s71 offen offset:3072 sc1\n" \
"buffer_load_dwordx4 v53, s[8:11], s64 offen offset:3072 sc1 lds\n" \
"s_waitcnt vmcnt(16) lgkmcnt(0)\n" \
"s_barrier\n" \
"v_mfma_f32_16x16x32_bf16 a[208:211], a[216:219], v[248:251], a[208:211]\n" \
"v_mfma_f32_32x32x16_bf16 v[240:255], a[216:219], a[216:219], 0\n" \
"v_exp_f32_e32 v70, v70\n" \
"v_exp_f32_e32 v71, v71\n" \
"v_exp_f32_e32 v72, v72\n" \
"v_exp_f32_e32 v73, v73\n" \
"s_add_u32 s64, s64, 0x10000\n" \
"v_cmp_lt_f32_e64 s[72:73], s21, v62\n" \
"v_readlane_b32 s95, v11, 3\n" \
"v_accvgpr_read_b32 v63, a208\n" \
"v_cmp_u_f32_e32 vcc, v63, v63\n" \
"v_exp_f32_e32 v68, v68\n" \
"v_exp_f32_e32 v69, v69\n" \
"v_exp_f32_e32 v74, v74\n" \
"v_exp_f32_e32 v75, v75\n" \
"s_or_b64 vcc, s[72:73], vcc\n" \
"s_cselect_b32 s95, 0x40000000, s95\n" \
"s_cselect_b32 s94, 0x7f800000, 0\n" \
"v_cvt_pk_bf16_f32 v71, v70, v71\n" \
"v_exp_f32_e32 v66, v66\n" \
"v_exp_f32_e32 v67, v67\n" \
"v_exp_f32_e32 v64, v64\n" \
"v_exp_f32_e32 v65, v65\n" \
"v_cvt_pk_bf16_f32 v72, v72, v73\n" \
"v_exp_f32_e32 v76, v76\n" \
"v_exp_f32_e32 v77, v77\n" \
"v_exp_f32_e32 v78, v78\n" \
"v_exp_f32_e32 v79, v79\n" \
"v_cvt_pk_bf16_f32 v70, v68, v69\n" \
"v_cvt_pk_bf16_f32 v73, v74, v75\n" \
"v_add_u32_e32 v49, 0x10000, v48\n" \
"s_xor_b32 s65, s65, 0x8000\n" \
"v_cvt_pk_bf16_f32 v69, v66, v67\n" \
"v_cvt_pk_bf16_f32 v68, v64, v65\n" \
"v_cvt_pk_bf16_f32 v74, v76, v77\n" \
"v_cvt_pk_bf16_f32 v75, v78, v79\n" \
"v_xor_b32_e32 v48, 0x8000, v48\n" \
"s_sub_u32 s66, s19, 2\n" \
"s_branch L_2_%=\n" \
"L_f1_%=:\n" \
"ds_read_b128 a[160:163], v49 offset:0\n" \
"ds_read_b128 a[164:167], v49 offset:1024\n" \
"ds_read_b128 a[168:171], v49 offset:2048\n" \
"v_mfma_f32_16x16x32_bf16 v[104:107], a[128:131], a[80:83], v[36:39]\n" \
"v_cvt_pk_bf16_f32 v192, v192, v193\n" \
"v_cvt_pk_bf16_f32 v224, v224, v225\n" \
"v_cvt_pk_bf16_f32 v193, v194, v195\n" \
"v_cvt_pk_bf16_f32 v225, v226, v227\n" \
"ds_read_b128 a[172:175], v49 offset:3072\n" \
"v_cvt_pk_bf16_f32 v194, v196, v197\n" \
"v_cvt_pk_bf16_f32 v195, v198, v199\n" \
"v_cvt_pk_bf16_f32 v226, v228, v229\n" \
"v_cvt_pk_bf16_f32 v227, v230, v231\n" \
"v_mfma_f32_16x16x32_bf16 v[104:107], a[132:135], a[84:87], v[104:107]\n" \
"v_lshl_add_u32 v50, s15, 2, v20\n" \
"v_cvt_pk_bf16_f32 v196, v200, v201\n" \
"v_cvt_pk_bf16_f32 v197, v202, v203\n" \
"v_cvt_pk_bf16_f32 v198, v204, v205\n" \
"v_cvt_pk_bf16_f32 v199, v206, v207\n" \
"v_cvt_pk_bf16_f32 v228, v232, v233\n" \
"v_cvt_pk_bf16_f32 v229, v234, v235\n" \
"v_cvt_pk_bf16_f32 v230, v236, v237\n" \
"v_cvt_pk_bf16_f32 v231, v238, v239\n" \
"v_add_u32_e32 v50, 0x1a000, v50\n" \
"v_mov_b32_e32 v61, s62\n" \
"v_cvt_pk_bf16_f32 v200, v208, v209\n" \
"v_cvt_pk_bf16_f32 v201, v210, v211\n" \
"v_cvt_pk_bf16_f32 v202, v212, v213\n" \
"v_cvt_pk_bf16_f32 v203, v214, v215\n" \
"v_cvt_pk_bf16_f32 v204, v216, v217\n" \
"v_cvt_pk_bf16_f32 v205, v218, v219\n" \
"v_cvt_pk_bf16_f32 v206, v220, v221\n" \
"v_cvt_pk_bf16_f32 v207, v222, v223\n" \
"v_cvt_pk_bf16_f32 v232, v240, v241\n" \
"v_cvt_pk_bf16_f32 v233, v242, v243\n" \
"v_cvt_pk_bf16_f32 v234, v244, v245\n" \
"v_cvt_pk_bf16_f32 v235, v246, v247\n" \
"v_cvt_pk_bf16_f32 v236, v248, v249\n" \
"v_cvt_pk_bf16_f32 v237, v250, v251\n" \
"v_cvt_pk_bf16_f32 v238, v252, v253\n" \
"v_cvt_pk_bf16_f32 v239, v254, v255\n" \
"ds_write_b128 v50, v[192:195] offset:0\n" \
"v_mfma_f32_16x16x32_bf16 v[104:107], a[136:139], a[88:91], v[104:107]\n" \
"ds_read_b128 a[176:179], v49 offset:4096\n" \
"v_add_u32_e32 v56, 0x49000, v20\n" \
"v_mfma_f32_16x16x32_bf16 v[104:107], a[140:143], a[92:95], v[104:107]\n" \
"ds_write_b128 v50, v[196:199] offset:1024\n" \
"v_mfma_f32_16x16x32_bf16 v[108:111], a[156:159], a[92:95], v[36:39]\n" \
"ds_read_b128 a[180:183], v49 offset:5120\n" \
"ds_read_b128 a[184:187], v49 offset:6144\n" \
"v_mfma_f32_16x16x32_bf16 v[108:111], a[152:155], a[88:91], v[108:111]\n" \
"ds_write_b128 v50, v[200:203] offset:2048\n" \
"v_mfma_f32_16x16x32_bf16 v[108:111], a[148:151], a[84:87], v[108:111]\n" \
"v_and_b32_e32 v68, v12, v68\n" \
"v_and_b32_e32 v69, v13, v69\n" \
"v_mfma_f32_16x16x32_bf16 v[108:111], a[144:147], a[80:83], v[108:111]\n" \
"ds_write_b128 v50, v[204:207] offset:3072\n" \
"v_and_b32_e32 v70, v14, v70\n" \
"v_and_b32_e32 v71, v15, v71\n" \
"ds_read_b128 a[188:191], v49 offset:7168\n" \
"ds_read_b128 a[0:3], v20 offset:8192\n" \
"s_waitcnt lgkmcnt(9)\n" \
"v_mfma_f32_16x16x32_bf16 v[128:131], a[160:163], v[68:71], v[128:131]\n" \
"ds_write_b128 v50, v[224:227] offset:4096\n" \
"v_mfma_f32_16x16x32_bf16 v[160:163], a[160:163], v[72:75], v[160:163]\n" \
"ds_read_b128 a[4:7], v20 offset:9216\n" \
"ds_read_b128 a[8:11], v20 offset:10240\n" \
"v_mfma_f32_16x16x32_bf16 v[132:135], a[164:167], v[68:71], v[132:135]\n" \
"ds_write_b128 v50, v[228:231] offset:5120\n" \
"v_mfma_f32_16x16x32_bf16 v[164:167], a[164:167], v[72:75], v[164:167]\n" \
"ds_read_b128 a[12:15], v20 offset:11264\n" \
"ds_read_b128 a[28:31], v20 offset:15360\n" \
"v_mfma_f32_16x16x32_bf16 v[136:139], a[168:171], v[68:71], v[136:139]\n" \
"ds_write_b128 v50, v[232:235] offset:6144\n" \
"v_mfma_f32_16x16x32_bf16 v[168:171], a[168:171], v[72:75], v[168:171]\n" \
"ds_read_b128 a[24:27], v20 offset:14336\n" \
"ds_read_b128 a[20:23], v20 offset:13312\n" \
"v_mfma_f32_16x16x32_bf16 v[140:143], a[172:175], v[68:71], v[140:143]\n" \
"ds_write_b128 v50, v[236:239] offset:7168\n" \
"v_mfma_f32_16x16x32_bf16 v[172:175], a[172:175], v[72:75], v[172:175]\n" \
"ds_read_b128 a[16:19], v20 offset:12288\n" \
"s_waitcnt lgkmcnt(15)\n" \
"v_mfma_f32_16x16x32_bf16 v[144:147], a[176:179], v[68:71], v[144:147]\n" \
"v_xor_b32_e32 v48, 0x4000, v49\n" \
"ds_read_b32 v58, v18 offset:256\n" \
"v_mfma_f32_16x16x32_bf16 v[176:179], a[176:179], v[72:75], v[176:179]\n" \
"ds_read_b32 v59, v18 offset:384\n" \
"ds_read_b128 a[32:35], v48 offset:8192\n" \
"v_mfma_f32_16x16x32_bf16 v[148:151], a[180:183], v[68:71], v[148:151]\n" \
"ds_read_b128 a[36:39], v48 offset:9216\n" \
"ds_read_b128 a[40:43], v48 offset:10240\n" \
"v_mfma_f32_16x16x32_bf16 v[180:183], a[180:183], v[72:75], v[180:183]\n" \
"ds_read_b128 a[44:47], v48 offset:11264\n" \
"ds_read_b128 a[48:51], v48 offset:12288\n" \
"v_mfma_f32_16x16x32_bf16 v[152:155], a[184:187], v[68:71], v[152:155]\n" \
"ds_read_b128 a[52:55], v48 offset:13312\n" \
"ds_read_b128 a[56:59], v48 offset:14336\n" \
"v_mfma_f32_16x16x32_bf16 v[184:187], a[184:187], v[72:75], v[184:187]\n" \
"ds_read_b128 a[60:63], v48 offset:15360\n" \
"s_waitcnt lgkmcnt(15)\n" \
"v_mfma_f32_16x16x32_bf16 v[156:159], a[188:191], v[68:71], v[156:159]\n" \
"ds_read_b128 a[128:131], v49 offset:8192\n" \
"ds_read_b128 a[132:135], v49 offset:9216\n" \
"v_mfma_f32_16x16x32_bf16 v[188:191], a[188:191], v[72:75], v[188:191]\n" \
"ds_read_b128 a[136:139], v49 offset:10240\n" \
"ds_read_b128 a[140:143], v49 offset:11264\n" \
"v_smfmac_f32_16x16x64_bf16 a[192:195], a[200:203], v[68:75], v21\n" \
"ds_read_b128 a[144:147], v49 offset:12288\n" \
"ds_read_b128 a[148:151], v49 offset:13312\n" \
"v_mfma_f32_16x16x32_bf16 v[112:115], a[0:3], a[64:67], v[32:35]\n" \
"ds_read_b128 a[152:155], v49 offset:14336\n" \
"ds_read_b128 a[156:159], v49 offset:15360\n" \
"v_mfma_f32_16x16x32_bf16 v[112:115], a[4:7], a[68:71], v[112:115]\n" \
"v_accvgpr_read_b32 v40, a196\n" \
"v_accvgpr_read_b32 v24, a197\n" \
"v_mfma_f32_16x16x32_bf16 v[112:115], a[8:11], a[72:75], v[112:115]\n" \
"v_accvgpr_read_b32 v41, a198\n" \
"v_accvgpr_read_b32 v25, a199\n" \
"v_mfma_f32_16x16x32_bf16 v[112:115], a[12:15], a[76:79], v[112:115]\n" \
"v_add_f32_e32 v40, v40, v24\n" \
"v_add_f32_e32 v41, v41, v25\n" \
"v_mfma_f32_16x16x32_bf16 v[116:119], a[28:31], a[76:79], v[32:35]\n" \
"v_add_f32_e32 v40, s94, v40\n" \
"s_waitcnt lgkmcnt(15)\n" \
"v_mfma_f32_16x16x32_bf16 v[116:119], a[24:27], a[72:75], v[116:119]\n" \
"v_add_f32_e32 v41, s94, v41\n" \
"ds_write_b64 v19, v[40:41]\n" \
"v_mfma_f32_16x16x32_bf16 v[116:119], a[20:23], a[68:71], v[116:119]\n" \
"v_add_u32_e32 v52, s15, v20\n" \
"v_add_u32_e32 v51, 0x10000, v52\n" \
"v_mfma_f32_16x16x32_bf16 v[116:119], a[16:19], a[64:67], v[116:119]\n" \
"v_exp_f32_e32 v104, v104\n" \
"v_mfma_f32_16x16x32_bf16 v[124:127], a[16:19], a[80:83], v[36:39]\n" \
"v_exp_f32_e32 v105, v105\n" \
"v_mfma_f32_16x16x32_bf16 v[124:127], a[20:23], a[84:87], v[124:127]\n" \
"v_exp_f32_e32 v106, v106\n" \
"v_mfma_f32_16x16x32_bf16 v[124:127], a[24:27], a[88:91], v[124:127]\n" \
"v_exp_f32_e32 v107, v107\n" \
"v_mfma_f32_16x16x32_bf16 v[124:127], a[28:31], a[92:95], v[124:127]\n" \
"v_cvt_pk_bf16_f32 v104, v104, v105\n" \
"v_cvt_pk_bf16_f32 v105, v106, v107\n" \
"v_mfma_f32_16x16x32_bf16 v[120:123], a[12:15], a[92:95], v[36:39]\n" \
"v_exp_f32_e32 v108, v108\n" \
"v_mfma_f32_16x16x32_bf16 v[120:123], a[8:11], a[88:91], v[120:123]\n" \
"v_exp_f32_e32 v109, v109\n" \
"v_mfma_f32_16x16x32_bf16 v[120:123], a[4:7], a[84:87], v[120:123]\n" \
"v_exp_f32_e32 v110, v110\n" \
"v_mfma_f32_16x16x32_bf16 v[120:123], a[0:3], a[80:83], v[120:123]\n" \
"v_exp_f32_e32 v111, v111\n" \
"s_waitcnt lgkmcnt(0)\n" \
"s_barrier\n" \
"ds_read_b128 v[208:211], v52 offset:0\n" \
"ds_read_b128 v[212:215], v52 offset:1024\n" \
"ds_read_b128 v[216:219], v52 offset:2048\n" \
"ds_read_b128 v[220:223], v52 offset:3072\n" \
"ds_read_b128 v[240:243], v51 offset:32768\n" \
"ds_read_b128 v[244:247], v51 offset:33792\n" \
"ds_read_b128 v[248:251], v51 offset:34816\n" \
"ds_read_b128 v[252:255], v51 offset:35840\n" \
"v_cvt_pk_bf16_f32 v106, v108, v109\n" \
"v_cvt_pk_bf16_f32 v107, v110, v111\n" \
"v_and_b32_e32 v104, v12, v104\n" \
"v_and_b32_e32 v105, v13, v105\n" \
"v_and_b32_e32 v106, v14, v106\n" \
"s_waitcnt vmcnt(0)\n" \
"s_mov_b64 exec, 1\n" \
"buffer_store_dword v61, off, s[56:59], s95 offset:0 sc1\n" \
"s_mov_b64 exec, -1\n" \
"v_and_b32_e32 v107, v15, v107\n" \
"v_xor_b32_e32 v63, 0x400, v19\n" \
"ds_read_b64 v[42:43], v63\n" \
"v_mfma_f32_16x16x32_bf16 a[192:195], a[212:215], v[104:107], a[192:195]\n" \
"v_mov_b32_e32 v194, v28\n" \
"v_mov_b32_e32 v195, v29\n" \
"v_mfma_f32_16x16x32_bf16 v[160:163], a[128:131], v[104:107], v[160:163]\n" \
"v_mov_b32_e32 v196, 0\n" \
"v_mov_b32_e32 v197, 0\n" \
"v_mfma_f32_16x16x32_bf16 v[164:167], a[132:135], v[104:107], v[164:167]\n" \
"v_mov_b32_e32 v192, 0\n" \
"v_mov_b32_e32 v193, 0\n" \
"v_mfma_f32_16x16x32_bf16 v[168:171], a[136:139], v[104:107], v[168:171]\n" \
"v_readlane_b32 s93, v11, 22\n" \
"v_readlane_b32 s92, v11, 27\n" \
"v_mfma_f32_16x16x32_bf16 v[172:175], a[140:143], v[104:107], v[172:175]\n" \
"buffer_load_dwordx4 a[96:99], v20, s[56:59], s60 offen offset:0 sc1\n" \
"v_mfma_f32_16x16x32_bf16 v[176:179], a[144:147], v[104:107], v[176:179]\n" \
"v_exp_f32_e32 v118, v118\n" \
"v_mfma_f32_16x16x32_bf16 v[180:183], a[148:151], v[104:107], v[180:183]\n" \
"v_exp_f32_e32 v119, v119\n" \
"v_mfma_f32_16x16x32_bf16 v[184:187], a[152:155], v[104:107], v[184:187]\n" \
"s_add_u32 m0, s93, 0x0\n" \
"v_cvt_pk_bf16_f32 v119, v118, v119\n" \
"v_mfma_f32_16x16x32_bf16 v[188:191], a[156:159], v[104:107], v[188:191]\n" \
"v_exp_f32_e32 v120, v120\n" \
"s_waitcnt lgkmcnt(1)\n" \
"s_barrier\n" \
"buffer_load_dwordx4 a[112:115], v56, s[56:59], s60 offen offset:0 sc1\n" \
"v_mfma_f32_16x16x32_bf16 v[128:131], v[194:197], v[208:211], v[128:131]\n" \
"v_exp_f32_e32 v121, v121\n" \
"v_mfma_f32_16x16x32_bf16 v[132:135], v[192:195], v[208:211], v[132:135]\n" \
"v_exp_f32_e32 v116, v116\n" \
"v_mfma_f32_16x16x32_bf16 v[136:139], v[194:197], v[212:215], v[136:139]\n" \
"v_exp_f32_e32 v117, v117\n" \
"v_mfma_f32_16x16x32_bf16 v[140:143], v[192:195], v[212:215], v[140:143]\n" \
"v_exp_f32_e32 v122, v122\n" \
"v_mfma_f32_16x16x32_bf16 v[144:147], v[194:197], v[216:219], v[144:147]\n" \
"v_exp_f32_e32 v123, v123\n" \
"v_mfma_f32_16x16x32_bf16 v[148:151], v[192:195], v[216:219], v[148:151]\n" \
"buffer_load_dwordx4 a[100:103], v20, s[56:59], s60 offen offset:1024 sc1\n" \
"v_mfma_f32_16x16x32_bf16 v[152:155], v[194:197], v[220:223], v[152:155]\n" \
"v_exp_f32_e32 v114, v114\n" \
"v_mfma_f32_16x16x32_bf16 v[156:159], v[192:195], v[220:223], v[156:159]\n" \
"v_exp_f32_e32 v115, v115\n" \
"v_mfma_f32_16x16x32_bf16 v[160:163], v[194:197], v[240:243], v[160:163]\n" \
"v_exp_f32_e32 v112, v112\n" \
"v_mfma_f32_16x16x32_bf16 v[164:167], v[192:195], v[240:243], v[164:167]\n" \
"v_exp_f32_e32 v113, v113\n" \
"v_mfma_f32_16x16x32_bf16 v[168:171], v[194:197], v[244:247], v[168:171]\n" \
"v_cvt_pk_bf16_f32 v120, v120, v121\n" \
"v_cvt_pk_bf16_f32 v118, v116, v117\n" \
"v_mfma_f32_16x16x32_bf16 v[172:175], v[192:195], v[244:247], v[172:175]\n" \
"buffer_load_dwordx4 a[116:119], v56, s[56:59], s60 offen offset:1024 sc1\n" \
"v_mfma_f32_16x16x32_bf16 v[176:179], v[194:197], v[248:251], v[176:179]\n" \
"v_exp_f32_e32 v124, v124\n" \
"v_mfma_f32_16x16x32_bf16 v[180:183], v[192:195], v[248:251], v[180:183]\n" \
"v_exp_f32_e32 v125, v125\n" \
"v_mfma_f32_16x16x32_bf16 v[184:187], v[194:197], v[252:255], v[184:187]\n" \
"v_exp_f32_e32 v126, v126\n" \
"v_mfma_f32_16x16x32_bf16 v[188:191], v[192:195], v[252:255], v[188:191]\n" \
"v_exp_f32_e32 v127, v127\n" \
"v_cvt_pk_bf16_f32 v121, v122, v123\n" \
"v_cvt_pk_bf16_f32 v117, v114, v115\n" \
"v_cvt_pk_bf16_f32 v116, v112, v113\n" \
"v_cvt_pk_bf16_f32 v122, v124, v125\n" \
"v_cvt_pk_bf16_f32 v123, v126, v127\n" \
"buffer_load_dwordx4 a[104:107], v20, s[56:59], s60 offen offset:2048 sc1\n" \
"s_nop 0\n" \
"v_smfmac_f32_16x16x64_bf16 a[192:195], a[200:203], v[116:123], v21\n" \
"v_mfma_f32_16x16x32_bf16 v[128:131], a[32:35], v[116:119], v[128:131]\n" \
"v_mfma_f32_16x16x32_bf16 v[160:163], a[32:35], v[120:123], v[160:163]\n" \
"v_mfma_f32_16x16x32_bf16 v[132:135], a[36:39], v[116:119], v[132:135]\n" \
"v_mfma_f32_16x16x32_bf16 v[164:167], a[36:39], v[120:123], v[164:167]\n" \
"v_mfma_f32_16x16x32_bf16 v[136:139], a[40:43], v[116:119], v[136:139]\n" \
"buffer_load_dwordx4 a[120:123], v56, s[56:59], s60 offen offset:2048 sc1\n" \
"v_mfma_f32_16x16x32_bf16 v[168:171], a[40:43], v[120:123], v[168:171]\n" \
"v_mfma_f32_16x16x32_bf16 v[140:143], a[44:47], v[116:119], v[140:143]\n" \
"v_accvgpr_read_b32 v54, a192\n" \
"v_accvgpr_read_b32 v22, a193\n" \
"v_mfma_f32_16x16x32_bf16 v[172:175], a[44:47], v[120:123], v[172:175]\n" \
"v_add_f32_e32 v54, v54, v22\n" \
"s_waitcnt lgkmcnt(0)\n" \
"v_add_f32_e32 v54, v54, v42\n" \
"v_mfma_f32_16x16x32_bf16 v[144:147], a[48:51], v[116:119], v[144:147]\n" \
"v_max_f32_e32 v62, v62, v54\n" \
"v_accvgpr_read_b32 v55, a194\n" \
"v_mfma_f32_16x16x32_bf16 v[176:179], a[48:51], v[120:123], v[176:179]\n" \
"v_rcp_f32_e32 v54, v54\n" \
"buffer_load_dwordx4 a[108:111], v20, s[56:59], s60 offen offset:3072 sc1\n" \
"v_mfma_f32_16x16x32_bf16 v[148:151], a[52:55], v[116:119], v[148:151]\n" \
"v_mul_f32_e32 v54, v54, v58\n" \
"v_mul_f32_e32 v128, v54, v128\n" \
"v_mfma_f32_16x16x32_bf16 v[180:183], a[52:55], v[120:123], v[180:183]\n" \
"v_mul_f32_e32 v129, v54, v129\n" \
"v_mul_f32_e32 v130, v54, v130\n" \
"v_mul_f32_e32 v131, v54, v131\n" \
"v_mul_f32_e32 v132, v54, v132\n" \
"v_mul_f32_e32 v133, v54, v133\n" \
"v_mul_f32_e32 v134, v54, v134\n" \
"v_mul_f32_e32 v135, v54, v135\n" \
"v_cvt_pk_bf16_f32 v64, v128, v129\n" \
"v_cvt_pk_bf16_f32 v65, v130, v131\n" \
"v_cvt_pk_bf16_f32 v66, v132, v133\n" \
"v_cvt_pk_bf16_f32 v67, v134, v135\n" \
"v_accvgpr_read_b32 v23, a195\n" \
"v_mfma_f32_16x16x32_bf16 v[152:155], a[56:59], v[116:119], v[152:155]\n" \
"v_mfma_f32_16x16x32_bf16 a[208:211], a[216:219], v[64:67], a[208:211]\n" \
"buffer_load_dwordx4 a[124:127], v56, s[56:59], s60 offen offset:3072 sc1\n" \
"v_mul_f32_e32 v136, v54, v136\n" \
"v_mul_f32_e32 v137, v54, v137\n" \
"v_mul_f32_e32 v138, v54, v138\n" \
"v_mul_f32_e32 v139, v54, v139\n" \
"v_mul_f32_e32 v140, v54, v140\n" \
"v_mul_f32_e32 v141, v54, v141\n" \
"v_mul_f32_e32 v142, v54, v142\n" \
"v_mul_f32_e32 v143, v54, v143\n" \
"v_add_f32_e32 v55, v55, v23\n" \
"v_cvt_pk_bf16_f32 v68, v136, v137\n" \
"v_cvt_pk_bf16_f32 v69, v138, v139\n" \
"v_cvt_pk_bf16_f32 v70, v140, v141\n" \
"v_cvt_pk_bf16_f32 v71, v142, v143\n" \
"v_add_f32_e32 v55, v55, v43\n" \
"v_mul_f32_e32 v144, v54, v144\n" \
"v_mfma_f32_16x16x32_bf16 a[208:211], a[216:219], v[68:71], a[208:211]\n" \
"v_mul_f32_e32 v145, v54, v145\n" \
"v_mul_f32_e32 v146, v54, v146\n" \
"v_mul_f32_e32 v147, v54, v147\n" \
"v_mul_f32_e32 v148, v54, v148\n" \
"buffer_load_dwordx4 v0, s[80:83], s92 offen offset:0 lds\n" \
"v_mul_f32_e32 v149, v54, v149\n" \
"v_mul_f32_e32 v150, v54, v150\n" \
"v_mul_f32_e32 v151, v54, v151\n" \
"v_max_f32_e32 v62, v62, v55\n" \
"v_cvt_pk_bf16_f32 v72, v144, v145\n" \
"v_cvt_pk_bf16_f32 v73, v146, v147\n" \
"v_cvt_pk_bf16_f32 v74, v148, v149\n" \
"v_cvt_pk_bf16_f32 v75, v150, v151\n" \
"v_rcp_f32_e32 v55, v55\n" \
"v_mfma_f32_16x16x32_bf16 v[184:187], a[56:59], v[120:123], v[184:187]\n" \
"v_mfma_f32_16x16x32_bf16 a[208:211], a[216:219], v[72:75], a[208:211]\n" \
"v_mul_f32_e32 v152, v54, v152\n" \
"v_mul_f32_e32 v153, v54, v153\n" \
"v_mul_f32_e32 v154, v54, v154\n" \
"v_mul_f32_e32 v155, v54, v155\n" \
"v_cvt_pk_bf16_f32 v76, v152, v153\n" \
"buffer_load_dwordx4 v1, s[80:83], s92 offen offset:1024 lds\n" \
"v_cvt_pk_bf16_f32 v77, v154, v155\n" \
"v_mul_f32_e32 v55, v55, v59\n" \
"v_mul_f32_e32 v160, v55, v160\n" \
"v_mul_f32_e32 v161, v55, v161\n" \
"v_mul_f32_e32 v162, v55, v162\n" \
"v_mul_f32_e32 v163, v55, v163\n" \
"v_mul_f32_e32 v164, v55, v164\n" \
"v_mul_f32_e32 v165, v55, v165\n" \
"v_mul_f32_e32 v166, v55, v166\n" \
"v_mul_f32_e32 v167, v55, v167\n" \
"v_cvt_pk_bf16_f32 v80, v160, v161\n" \
"v_cvt_pk_bf16_f32 v81, v162, v163\n" \
"v_cvt_pk_bf16_f32 v82, v164, v165\n" \
"v_cvt_pk_bf16_f32 v83, v166, v167\n" \
"v_mfma_f32_16x16x32_bf16 v[156:159], a[60:63], v[116:119], v[156:159]\n" \
"v_mul_f32_e32 v168, v55, v168\n" \
"v_mul_f32_e32 v169, v55, v169\n" \
"v_mul_f32_e32 v170, v55, v170\n" \
"v_mul_f32_e32 v171, v55, v171\n" \
"v_mul_f32_e32 v172, v55, v172\n" \
"buffer_load_dwordx4 v2, s[80:83], s92 offen offset:2048 lds\n" \
"v_mul_f32_e32 v173, v55, v173\n" \
"v_mul_f32_e32 v174, v55, v174\n" \
"v_mul_f32_e32 v156, v54, v156\n" \
"v_mul_f32_e32 v157, v54, v157\n" \
"v_mul_f32_e32 v158, v54, v158\n" \
"v_mul_f32_e32 v159, v54, v159\n" \
"v_cvt_pk_bf16_f32 v78, v156, v157\n" \
"v_cvt_pk_bf16_f32 v79, v158, v159\n" \
"v_mul_f32_e32 v175, v55, v175\n" \
"v_cvt_pk_bf16_f32 v84, v168, v169\n" \
"v_mfma_f32_16x16x32_bf16 a[208:211], a[216:219], v[76:79], a[208:211]\n" \
"v_mfma_f32_16x16x32_bf16 a[208:211], a[216:219], v[80:83], a[208:211]\n" \
"v_cvt_pk_bf16_f32 v85, v170, v171\n" \
"v_cvt_pk_bf16_f32 v86, v172, v173\n" \
"v_cvt_pk_bf16_f32 v87, v174, v175\n" \
"v_mul_f32_e32 v176, v55, v176\n" \
"v_mul_f32_e32 v177, v55, v177\n" \
"buffer_load_dwordx4 v3, s[80:83], s92 offen offset:3072 lds\n" \
"s_add_u32 m0, s93, 0x1000\n" \
"v_mfma_f32_16x16x32_bf16 a[208:211], a[216:219], v[84:87], a[208:211]\n" \
"v_mul_f32_e32 v178, v55, v178\n" \
"v_mul_f32_e32 v179, v55, v179\n" \
"v_mul_f32_e32 v180, v55, v180\n" \
"v_mul_f32_e32 v181, v55, v181\n" \
"v_mul_f32_e32 v182, v55, v182\n" \
"v_mul_f32_e32 v183, v55, v183\n" \
"v_cvt_pk_bf16_f32 v96, v176, v177\n" \
"v_cvt_pk_bf16_f32 v97, v178, v179\n" \
"v_cvt_pk_bf16_f32 v98, v180, v181\n" \
"v_cvt_pk_bf16_f32 v99, v182, v183\n" \
"v_mfma_f32_16x16x32_bf16 v[188:191], a[60:63], v[120:123], v[188:191]\n" \
"v_mul_f32_e32 v184, v55, v184\n" \
"v_mfma_f32_16x16x32_bf16 a[208:211], a[216:219], v[96:99], a[208:211]\n" \
"v_mul_f32_e32 v185, v55, v185\n" \
"v_mul_f32_e32 v186, v55, v186\n" \
"buffer_load_dwordx4 v4, s[80:83], s92 offen offset:0 lds\n" \
"v_mul_f32_e32 v187, v55, v187\n" \
"v_cvt_pk_bf16_f32 v100, v184, v185\n" \
"v_cvt_pk_bf16_f32 v101, v186, v187\n" \
"v_mul_f32_e32 v188, v55, v188\n" \
"v_mul_f32_e32 v189, v55, v189\n" \
"v_mul_f32_e32 v190, v55, v190\n" \
"v_mul_f32_e32 v191, v55, v191\n" \
"v_cvt_pk_bf16_f32 v102, v188, v189\n" \
"v_cvt_pk_bf16_f32 v103, v190, v191\n" \
"v_cmp_lt_f32_e64 s[72:73], s21, v62\n" \
"buffer_load_dwordx4 v5, s[80:83], s92 offen offset:1024 lds\n" \
"v_mfma_f32_16x16x32_bf16 a[208:211], a[216:219], v[100:103], a[208:211]\n" \
"buffer_load_dwordx4 v6, s[80:83], s92 offen offset:2048 lds\n" \
"buffer_load_dwordx4 v7, s[80:83], s92 offen offset:3072 lds\n" \
"s_add_u32 m0, s93, 0x2000\n" \
"s_nop 0\n" \
"buffer_load_dwordx4 v8, s[80:83], s92 offen offset:0 lds\n" \
"s_nop 2\n" \
"v_accvgpr_read_b32 v63, a208\n" \
"v_cmp_u_f32_e32 vcc, v63, v63\n" \
"s_nop 0\n" \
"s_or_b64 vcc, s[72:73], vcc\n" \
"s_cbranch_scc0 L_out_%=\n" \
"s_branch L_end_%=\n" \
".fill (L_ab + 24576 - .) / 4, 4, 0xBF800000\n" \
".fill (L_ab + 24576 - .) / 4, 4, 0xBF800000\n" \
"L_ap6:\n" \
"s_setpc_b64 s[88:89]\n" \
"L_gs7:\n" \
"v_mfma_f32_16x16x32_bf16 v[104:107], a[128:131], a[16:19], v[36:39]\n" \
"s_add_u32 s68, s60, 0xfff6e000\n" \
"s_add_u32 s69, s60, 0xfffb7000\n" \
"s_add_u32 s70, s60, 0x0\n" \
"s_add_u32 s71, s60, 0x49000\n" \
"v_mfma_f32_16x16x32_bf16 v[104:107], a[132:135], a[20:23], v[104:107]\n" \
"s_xor_b32 m0, s65, 0x12000\n" \
"ds_read_b128 a[160:163], v49 offset:0\n" \
"v_mfma_f32_16x16x32_bf16 v[104:107], a[136:139], a[24:27], v[104:107]\n" \
"buffer_load_dwordx4 v50, s[8:11], s64 offen offset:0 sc1 lds\n" \
"v_mfma_f32_16x16x32_bf16 v[104:107], a[140:143], a[28:31], v[104:107]\n" \
"ds_read_b128 a[164:167], v49 offset:1024\n" \
"ds_read_b128 a[168:171], v49 offset:2048\n" \
"v_mfma_f32_16x16x32_bf16 v[108:111], a[156:159], a[28:31], v[36:39]\n" \
"ds_read_b128 a[172:175], v49 offset:3072\n" \
"ds_read_b128 a[176:179], v49 offset:4096\n" \
"v_mfma_f32_16x16x32_bf16 v[108:111], a[152:155], a[24:27], v[108:111]\n" \
"s_mov_b32 s65, s13\n" \
"v_add_u32_e32 v63, s15, v20\n" \
"v_mfma_f32_16x16x32_bf16 v[108:111], a[148:151], a[20:23], v[108:111]\n" \
"buffer_load_dwordx4 v50, s[8:11], s64 offen offset:1024 sc1 lds\n" \
"v_mfma_f32_16x16x32_bf16 v[108:111], a[144:147], a[16:19], v[108:111]\n" \
"s_add_u32 s67, s14, 40960\n" \
"v_add_u32_e32 v50, 0x6000, v63\n" \
"v_mfma_f32_16x16x32_bf16 v[116:119], a[144:147], a[32:35], v[40:43]\n" \
"s_xor_b32 m0, s65, 0x8000\n" \
"ds_read_b128 a[180:183], v49 offset:5120\n" \
"v_mfma_f32_16x16x32_bf16 v[116:119], a[148:151], a[36:39], v[116:119]\n" \
"ds_read_b128 a[184:187], v49 offset:6144\n" \
"v_and_b32_e32 v68, v12, v68\n" \
"v_mfma_f32_16x16x32_bf16 v[116:119], a[152:155], a[40:43], v[116:119]\n" \
"v_and_b32_e32 v69, v13, v69\n" \
"v_and_b32_e32 v70, v14, v70\n" \
"v_mfma_f32_16x16x32_bf16 v[116:119], a[156:159], a[44:47], v[116:119]\n" \
"buffer_load_dwordx4 v50, s[8:11], s67 offen offset:0 sc1 lds\n" \
"v_mfma_f32_16x16x32_bf16 v[112:115], a[140:143], a[44:47], v[40:43]\n" \
"v_and_b32_e32 v71, v15, v71\n" \
"ds_read_b128 a[188:191], v49 offset:7168\n" \
"v_mfma_f32_16x16x32_bf16 v[112:115], a[136:139], a[40:43], v[112:115]\n" \
"v_exp_f32_e32 v86, v86\n" \
"v_mfma_f32_16x16x32_bf16 v[112:115], a[132:135], a[36:39], v[112:115]\n" \
"v_exp_f32_e32 v87, v87\n" \
"v_mfma_f32_16x16x32_bf16 v[112:115], a[128:131], a[32:35], v[112:115]\n" \
"v_exp_f32_e32 v84, v84\n" \
"v_mfma_f32_16x16x32_bf16 v[120:123], a[128:131], a[48:51], v[44:47]\n" \
"buffer_load_dwordx4 v50, s[8:11], s67 offen offset:1024 sc1 lds\n" \
"v_mfma_f32_16x16x32_bf16 v[120:123], a[132:135], a[52:55], v[120:123]\n" \
"v_exp_f32_e32 v85, v85\n" \
"v_mfma_f32_16x16x32_bf16 v[120:123], a[136:139], a[56:59], v[120:123]\n" \
"v_cvt_pk_bf16_f32 v87, v86, v87\n" \
"v_cvt_pk_bf16_f32 v86, v84, v85\n" \
"v_mfma_f32_16x16x32_bf16 v[120:123], a[140:143], a[60:63], v[120:123]\n" \
"v_exp_f32_e32 v82, v82\n" \
"v_mfma_f32_16x16x32_bf16 v[124:127], a[156:159], a[60:63], v[44:47]\n" \
"v_exp_f32_e32 v83, v83\n" \
"v_mfma_f32_16x16x32_bf16 v[124:127], a[152:155], a[56:59], v[124:127]\n" \
"v_exp_f32_e32 v80, v80\n" \
"v_mfma_f32_16x16x32_bf16 v[124:127], a[148:151], a[52:55], v[124:127]\n" \
"buffer_load_dwordx4 v50, s[8:11], s67 offen offset:2048 sc1 lds\n" \
"v_mfma_f32_16x16x32_bf16 v[124:127], a[144:147], a[48:51], v[124:127]\n" \
"v_cvt_pk_bf16_f32 v85, v82, v83\n" \
"s_waitcnt lgkmcnt(1)\n" \
"v_mfma_f32_16x16x32_bf16 v[128:131], a[160:163], v[68:71], v[128:131]\n" \
"v_exp_f32_e32 v81, v81\n" \
"v_mfma_f32_16x16x32_bf16 v[160:163], a[160:163], v[72:75], v[160:163]\n" \
"v_cvt_pk_bf16_f32 v84, v80, v81\n" \
"s_mov_b32 s64, s14\n" \
"v_mfma_f32_16x16x32_bf16 v[132:135], a[164:167], v[68:71], v[132:135]\n" \
"v_exp_f32_e32 v88, v88\n" \
"v_mfma_f32_16x16x32_bf16 v[164:167], a[164:167], v[72:75], v[164:167]\n" \
"v_exp_f32_e32 v89, v89\n" \
"v_mfma_f32_16x16x32_bf16 v[136:139], a[168:171], v[68:71], v[136:139]\n" \
"buffer_load_dwordx4 v50, s[8:11], s67 offen offset:3072 sc1 lds\n" \
"v_mfma_f32_16x16x32_bf16 v[168:171], a[168:171], v[72:75], v[168:171]\n" \
"v_exp_f32_e32 v90, v90\n" \
"v_mfma_f32_16x16x32_bf16 v[140:143], a[172:175], v[68:71], v[140:143]\n" \
"v_exp_f32_e32 v91, v91\n" \
"v_mfma_f32_16x16x32_bf16 v[172:175], a[172:175], v[72:75], v[172:175]\n" \
"v_cvt_pk_bf16_f32 v88, v88, v89\n" \
"v_cvt_pk_bf16_f32 v89, v90, v91\n" \
"v_mfma_f32_16x16x32_bf16 v[144:147], a[176:179], v[68:71], v[144:147]\n" \
"v_exp_f32_e32 v92, v92\n" \
"v_mfma_f32_16x16x32_bf16 v[176:179], a[176:179], v[72:75], v[176:179]\n" \
"v_exp_f32_e32 v93, v93\n" \
"v_mfma_f32_16x16x32_bf16 v[148:151], a[180:183], v[68:71], v[148:151]\n" \
"v_exp_f32_e32 v94, v94\n" \
"v_mfma_f32_16x16x32_bf16 v[180:183], a[180:183], v[72:75], v[180:183]\n" \
"v_exp_f32_e32 v95, v95\n" \
"v_mfma_f32_16x16x32_bf16 v[152:155], a[184:187], v[68:71], v[152:155]\n" \
"v_cvt_pk_bf16_f32 v90, v92, v93\n" \
"v_cvt_pk_bf16_f32 v91, v94, v95\n" \
"v_mfma_f32_16x16x32_bf16 v[184:187], a[184:187], v[72:75], v[184:187]\n" \
"v_add_u32_e32 v51, 0x12000, v63\n" \
"s_waitcnt lgkmcnt(0)\n" \
"v_mfma_f32_16x16x32_bf16 v[156:159], a[188:191], v[68:71], v[156:159]\n" \
"s_xor_b32 m0, s65, 0xa000\n" \
"ds_read_b32 v58, v18 offset:512\n" \
"v_mfma_f32_16x16x32_bf16 v[188:191], a[188:191], v[72:75], v[188:191]\n" \
"buffer_load_dwordx4 v51, s[8:11], s64 offen offset:0 sc1 lds\n" \
"v_smfmac_f32_16x16x64_bf16 a[192:195], a[200:203], v[68:75], v21\n" \
"ds_read_b32 v59, v18 offset:640\n" \
"ds_read_b32 v60, v18 offset:768\n" \
"v_mfma_f32_16x16x32_bf16 v[220:223], a[188:191], v[84:87], v[220:223]\n" \
"ds_read_b32 v61, v18 offset:896\n" \
"ds_read_b128 a[128:131], v48 offset:0\n" \
"v_mfma_f32_16x16x32_bf16 v[252:255], a[188:191], v[88:91], v[252:255]\n" \
"ds_read_b128 a[144:147], v48 offset:4096\n" \
"ds_read_b128 a[132:135], v48 offset:1024\n" \
"v_mfma_f32_16x16x32_bf16 v[216:219], a[184:187], v[84:87], v[216:219]\n" \
"buffer_load_dwordx4 v51, s[8:11], s64 offen offset:1024 sc1 lds\n" \
"v_mfma_f32_16x16x32_bf16 v[248:251], a[184:187], v[88:91], v[248:251]\n" \
"ds_read_b128 a[148:151], v48 offset:5120\n" \
"ds_read_b128 a[136:139], v48 offset:2048\n" \
"v_mfma_f32_16x16x32_bf16 v[212:215], a[180:183], v[84:87], v[212:215]\n" \
"ds_read_b128 a[152:155], v48 offset:6144\n" \
"ds_read_b128 a[140:143], v48 offset:3072\n" \
"v_mfma_f32_16x16x32_bf16 v[244:247], a[180:183], v[88:91], v[244:247]\n" \
"ds_read_b128 a[156:159], v48 offset:7168\n" \
"v_add_u32_e32 v52, 0x14000, v63\n" \
"v_mfma_f32_16x16x32_bf16 v[208:211], a[176:179], v[84:87], v[208:211]\n" \
"buffer_load_dwordx4 v51, s[8:11], s64 offen offset:2048 sc1 lds\n" \
"v_mfma_f32_16x16x32_bf16 v[240:243], a[176:179], v[88:91], v[240:243]\n" \
"v_exp_f32_e32 v104, v104\n" \
"v_mfma_f32_16x16x32_bf16 v[204:207], a[172:175], v[84:87], v[204:207]\n" \
"v_exp_f32_e32 v105, v105\n" \
"v_mfma_f32_16x16x32_bf16 v[236:239], a[172:175], v[88:91], v[236:239]\n" \
"v_exp_f32_e32 v106, v106\n" \
"v_mfma_f32_16x16x32_bf16 v[200:203], a[168:171], v[84:87], v[200:203]\n" \
"buffer_load_dwordx4 v51, s[8:11], s64 offen offset:3072 sc1 lds\n" \
"v_mfma_f32_16x16x32_bf16 v[232:235], a[168:171], v[88:91], v[232:235]\n" \
"v_exp_f32_e32 v107, v107\n" \
"v_mfma_f32_16x16x32_bf16 v[196:199], a[164:167], v[84:87], v[196:199]\n" \
"v_cvt_pk_bf16_f32 v104, v104, v105\n" \
"v_cvt_pk_bf16_f32 v105, v106, v107\n" \
"v_mfma_f32_16x16x32_bf16 v[228:231], a[164:167], v[88:91], v[228:231]\n" \
"v_exp_f32_e32 v108, v108\n" \
"v_mfma_f32_16x16x32_bf16 v[192:195], a[160:163], v[84:87], v[192:195]\n" \
"v_exp_f32_e32 v109, v109\n" \
"v_mfma_f32_16x16x32_bf16 v[224:227], a[160:163], v[88:91], v[224:227]\n" \
"v_exp_f32_e32 v110, v110\n" \
"v_smfmac_f32_16x16x64_bf16 a[196:199], a[200:203], v[84:91], v21\n" \
"v_exp_f32_e32 v111, v111\n" \
"v_cvt_pk_bf16_f32 v106, v108, v109\n" \
"v_cvt_pk_bf16_f32 v107, v110, v111\n" \
"v_and_b32_e32 v104, v12, v104\n" \
"v_and_b32_e32 v105, v13, v105\n" \
"v_and_b32_e32 v106, v14, v106\n" \
"v_and_b32_e32 v107, v15, v107\n" \
"v_accvgpr_read_b32 v54, a192\n" \
"v_accvgpr_read_b32 v22, a193\n" \
"v_readlane_b32 s93, v11, 19\n" \
"v_readlane_b32 s80, v11, 11\n" \
"v_readlane_b32 s81, v11, 12\n" \
"s_mov_b32 s82, 0x47fca0\n" \
"s_mov_b32 s83, 0x20000\n" \
"v_readlane_b32 s92, v11, 26\n" \
"v_exp_f32_e32 v118, v118\n" \
"v_exp_f32_e32 v119, v119\n" \
"v_exp_f32_e32 v116, v116\n" \
"v_exp_f32_e32 v117, v117\n" \
"v_cvt_pk_bf16_f32 v119, v118, v119\n" \
"v_exp_f32_e32 v114, v114\n" \
"v_exp_f32_e32 v115, v115\n" \
"v_exp_f32_e32 v112, v112\n" \
"v_cvt_pk_bf16_f32 v118, v116, v117\n" \
"s_waitcnt vmcnt(10) lgkmcnt(0)\n" \
"s_barrier\n" \
"v_mfma_f32_16x16x32_bf16 v[80:83], a[128:131], a[32:35], v[40:43]\n" \
"ds_read_b128 a[160:163], v49 offset:8192\n" \
"ds_read_b128 a[164:167], v49 offset:9216\n" \
"v_mfma_f32_16x16x32_bf16 v[80:83], a[132:135], a[36:39], v[80:83]\n" \
"ds_read_b128 a[168:171], v49 offset:10240\n" \
"s_xor_b32 m0, s65, 0x18000\n" \
"v_mfma_f32_16x16x32_bf16 v[80:83], a[136:139], a[40:43], v[80:83]\n" \
"buffer_load_dwordx4 v52, s[8:11], s64 offen offset:0 sc1 lds\n" \
"v_mfma_f32_16x16x32_bf16 v[80:83], a[140:143], a[44:47], v[80:83]\n" \
"ds_read_b128 a[172:175], v49 offset:11264\n" \
"ds_read_b128 a[176:179], v49 offset:12288\n" \
"v_mfma_f32_16x16x32_bf16 v[84:87], a[156:159], a[44:47], v[40:43]\n" \
"ds_read_b128 a[180:183], v49 offset:13312\n" \
"ds_read_b128 a[184:187], v49 offset:14336\n" \
"v_mfma_f32_16x16x32_bf16 v[84:87], a[152:155], a[40:43], v[84:87]\n" \
"ds_read_b128 a[188:191], v49 offset:15360\n" \
"v_cvt_pk_bf16_f32 v117, v114, v115\n" \
"v_mfma_f32_16x16x32_bf16 v[84:87], a[148:151], a[36:39], v[84:87]\n" \
"buffer_load_dwordx4 v52, s[8:11], s64 offen offset:1024 sc1 lds\n" \
"v_mfma_f32_16x16x32_bf16 v[84:87], a[144:147], a[32:35], v[84:87]\n" \
"v_exp_f32_e32 v113, v113\n" \
"v_mfma_f32_16x16x32_bf16 v[92:95], a[144:147], a[48:51], v[44:47]\n" \
"v_cvt_pk_bf16_f32 v116, v112, v113\n" \
"ds_read_b128 a[144:147], v48 offset:12288\n" \
"v_mfma_f32_16x16x32_bf16 v[92:95], a[148:151], a[52:55], v[92:95]\n" \
"v_exp_f32_e32 v120, v120\n" \
"v_mfma_f32_16x16x32_bf16 v[92:95], a[152:155], a[56:59], v[92:95]\n" \
"buffer_load_dwordx4 v52, s[8:11], s64 offen offset:2048 sc1 lds\n" \
"v_mfma_f32_16x16x32_bf16 v[92:95], a[156:159], a[60:63], v[92:95]\n" \
"v_exp_f32_e32 v121, v121\n" \
"v_mfma_f32_16x16x32_bf16 v[88:91], a[140:143], a[60:63], v[44:47]\n" \
"v_exp_f32_e32 v122, v122\n" \
"v_mfma_f32_16x16x32_bf16 v[88:91], a[136:139], a[56:59], v[88:91]\n" \
"v_exp_f32_e32 v123, v123\n" \
"v_mfma_f32_16x16x32_bf16 v[88:91], a[132:135], a[52:55], v[88:91]\n" \
"buffer_load_dwordx4 v52, s[8:11], s64 offen offset:3072 sc1 lds\n" \
"v_mfma_f32_16x16x32_bf16 v[88:91], a[128:131], a[48:51], v[88:91]\n" \
"s_add_u32 m0, s93, 0x0\n" \
"v_cvt_pk_bf16_f32 v120, v120, v121\n" \
"v_mfma_f32_16x16x32_bf16 a[192:195], a[212:215], v[104:107], a[192:195]\n" \
"v_cvt_pk_bf16_f32 v121, v122, v123\n" \
"s_waitcnt lgkmcnt(6)\n" \
"v_mfma_f32_16x16x32_bf16 v[160:163], a[160:163], v[104:107], v[160:163]\n" \
"v_exp_f32_e32 v124, v124\n" \
"v_mfma_f32_16x16x32_bf16 v[164:167], a[164:167], v[104:107], v[164:167]\n" \
"buffer_load_dwordx4 v0, s[80:83], s92 offen offset:0 lds\n" \
"v_mfma_f32_16x16x32_bf16 v[168:171], a[168:171], v[104:107], v[168:171]\n" \
"ds_read_b128 a[128:131], v48 offset:8192\n" \
"s_waitcnt lgkmcnt(2)\n" \
"v_mfma_f32_16x16x32_bf16 v[172:175], a[172:175], v[104:107], v[172:175]\n" \
"v_exp_f32_e32 v125, v125\n" \
"v_mfma_f32_16x16x32_bf16 v[176:179], a[176:179], v[104:107], v[176:179]\n" \
"v_exp_f32_e32 v126, v126\n" \
"buffer_load_dwordx4 v1, s[80:83], s92 offen offset:1024 lds\n" \
"v_mfma_f32_16x16x32_bf16 v[180:183], a[180:183], v[104:107], v[180:183]\n" \
"v_exp_f32_e32 v127, v127\n" \
"v_mfma_f32_16x16x32_bf16 v[184:187], a[184:187], v[104:107], v[184:187]\n" \
"v_cvt_pk_bf16_f32 v122, v124, v125\n" \
"v_cvt_pk_bf16_f32 v123, v126, v127\n" \
"v_mfma_f32_16x16x32_bf16 v[188:191], a[188:191], v[104:107], v[188:191]\n" \
"ds_read_b128 a[132:135], v48 offset:9216\n" \
"ds_read_b128 a[148:151], v48 offset:13312\n" \
"buffer_load_dwordx4 v2, s[80:83], s92 offen offset:2048 lds\n" \
"v_mfma_f32_16x16x32_bf16 v[220:223], a[188:191], v[116:119], v[220:223]\n" \
"ds_read_b128 a[136:139], v48 offset:10240\n" \
"ds_read_b128 a[152:155], v48 offset:14336\n" \
"v_mfma_f32_16x16x32_bf16 v[252:255], a[188:191], v[120:123], v[252:255]\n" \
"ds_read_b128 a[140:143], v48 offset:11264\n" \
"ds_read_b128 a[156:159], v48 offset:15360\n" \
"v_mfma_f32_16x16x32_bf16 v[216:219], a[184:187], v[116:119], v[216:219]\n" \
"v_readlane_b32 s96, v11, 1\n" \
"v_bfe_u32 v27, v20, 2, 7\n" \
"buffer_load_dwordx4 v3, s[80:83], s92 offen offset:3072 lds\n" \
"v_mfma_f32_16x16x32_bf16 v[248:251], a[184:187], v[120:123], v[248:251]\n" \
"s_add_u32 m0, s93, 0x1000\n" \
"v_add_f32_e32 v54, v54, v22\n" \
"v_mfma_f32_16x16x32_bf16 v[212:215], a[180:183], v[116:119], v[212:215]\n" \
"v_max_f32_e32 v62, v62, v54\n" \
"v_add_u32_e32 v49, 0x10000, v48\n" \
"v_mfma_f32_16x16x32_bf16 v[244:247], a[180:183], v[120:123], v[244:247]\n" \
"v_rcp_f32_e32 v54, v54\n" \
"buffer_load_dwordx4 v4, s[80:83], s92 offen offset:0 lds\n" \
"v_mfma_f32_16x16x32_bf16 v[208:211], a[176:179], v[116:119], v[208:211]\n" \
"v_mul_f32_e32 v54, v54, v58\n" \
"v_mul_f32_e32 v128, v54, v128\n" \
"v_mfma_f32_16x16x32_bf16 v[240:243], a[176:179], v[120:123], v[240:243]\n" \
"v_mul_f32_e32 v129, v54, v129\n" \
"v_mul_f32_e32 v136, v54, v136\n" \
"v_mfma_f32_16x16x32_bf16 v[204:207], a[172:175], v[116:119], v[204:207]\n" \
"v_mul_f32_e32 v137, v54, v137\n" \
"v_mul_f32_e32 v144, v54, v144\n" \
"buffer_load_dwordx4 v5, s[80:83], s92 offen offset:1024 lds\n" \
"v_mfma_f32_16x16x32_bf16 v[236:239], a[172:175], v[120:123], v[236:239]\n" \
"v_mul_f32_e32 v145, v54, v145\n" \
"v_mul_f32_e32 v152, v54, v152\n" \
"v_mfma_f32_16x16x32_bf16 v[200:203], a[168:171], v[116:119], v[200:203]\n" \
"v_mul_f32_e32 v153, v54, v153\n" \
"v_cvt_pk_bf16_f32 v128, v128, v129\n" \
"v_mfma_f32_16x16x32_bf16 v[232:235], a[168:171], v[120:123], v[232:235]\n" \
"v_mul_f32_e32 v130, v54, v130\n" \
"v_mul_f32_e32 v131, v54, v131\n" \
"buffer_load_dwordx4 v6, s[80:83], s92 offen offset:2048 lds\n" \
"v_mfma_f32_16x16x32_bf16 v[196:199], a[164:167], v[116:119], v[196:199]\n" \
"v_cvt_pk_bf16_f32 v136, v136, v137\n" \
"v_mul_f32_e32 v138, v54, v138\n" \
"v_mfma_f32_16x16x32_bf16 v[228:231], a[164:167], v[120:123], v[228:231]\n" \
"v_mul_f32_e32 v139, v54, v139\n" \
"v_cvt_pk_bf16_f32 v144, v144, v145\n" \
"v_mfma_f32_16x16x32_bf16 v[192:195], a[160:163], v[116:119], v[192:195]\n" \
"v_mul_f32_e32 v146, v54, v146\n" \
"v_mul_f32_e32 v147, v54, v147\n" \
"buffer_load_dwordx4 v7, s[80:83], s92 offen offset:3072 lds\n" \
"v_mfma_f32_16x16x32_bf16 v[224:227], a[160:163], v[120:123], v[224:227]\n" \
"v_cvt_pk_bf16_f32 v152, v152, v153\n" \
"v_mul_f32_e32 v154, v54, v154\n" \
"v_smfmac_f32_16x16x64_bf16 a[196:199], a[200:203], v[116:123], v21\n" \
"v_mul_f32_e32 v155, v54, v155\n" \
"v_cvt_pk_bf16_f32 v129, v130, v131\n" \
"v_mul_f32_e32 v132, v54, v132\n" \
"v_mul_f32_e32 v133, v54, v133\n" \
"v_mul_f32_e32 v134, v54, v134\n" \
"v_mul_f32_e32 v135, v54, v135\n" \
"v_cvt_pk_bf16_f32 v137, v138, v139\n" \
"v_mul_f32_e32 v140, v54, v140\n" \
"v_mul_f32_e32 v141, v54, v141\n" \
"v_mul_f32_e32 v142, v54, v142\n" \
"v_mul_f32_e32 v143, v54, v143\n" \
"v_cvt_pk_bf16_f32 v145, v146, v147\n" \
"v_mul_f32_e32 v148, v54, v148\n" \
"v_mul_f32_e32 v149, v54, v149\n" \
"v_mul_f32_e32 v150, v54, v150\n" \
"v_mul_f32_e32 v151, v54, v151\n" \
"v_cvt_pk_bf16_f32 v153, v154, v155\n" \
"v_mul_f32_e32 v156, v54, v156\n" \
"v_mul_f32_e32 v157, v54, v157\n" \
"v_mul_f32_e32 v158, v54, v158\n" \
"v_mul_f32_e32 v159, v54, v159\n" \
"v_cvt_pk_bf16_f32 v130, v132, v133\n" \
"v_cvt_pk_bf16_f32 v131, v134, v135\n" \
"buffer_store_dwordx4 v[128:131], v20, s[56:59], s68 offen offset:0 sc1\n" \
"v_cvt_pk_bf16_f32 v138, v140, v141\n" \
"v_cvt_pk_bf16_f32 v139, v142, v143\n" \
"v_cvt_pk_bf16_f32 v146, v148, v149\n" \
"v_cvt_pk_bf16_f32 v147, v150, v151\n" \
"v_cvt_pk_bf16_f32 v154, v156, v157\n" \
"v_cvt_pk_bf16_f32 v155, v158, v159\n" \
"s_add_u32 m0, s93, 0x2000\n" \
"s_xor_b32 s65, s65, 0x8000\n" \
"s_add_u32 s64, s64, 0x10000\n" \
"v_exp_f32_e32 v86, v86\n" \
"buffer_store_dwordx4 v[136:139], v20, s[56:59], s68 offen offset:1024 sc1\n" \
"v_exp_f32_e32 v87, v87\n" \
"v_exp_f32_e32 v84, v84\n" \
"v_exp_f32_e32 v85, v85\n" \
"v_exp_f32_e32 v88, v88\n" \
"v_exp_f32_e32 v89, v89\n" \
"v_cvt_pk_bf16_f32 v87, v86, v87\n" \
"buffer_store_dwordx4 v[144:147], v20, s[56:59], s68 offen offset:2048 sc1\n" \
"v_exp_f32_e32 v82, v82\n" \
"v_exp_f32_e32 v83, v83\n" \
"v_exp_f32_e32 v80, v80\n" \
"v_exp_f32_e32 v81, v81\n" \
"v_exp_f32_e32 v90, v90\n" \
"v_exp_f32_e32 v91, v91\n" \
"buffer_store_dwordx4 v[152:155], v20, s[56:59], s68 offen offset:3072 sc1\n" \
"v_cvt_pk_bf16_f32 v86, v84, v85\n" \
"v_cvt_pk_bf16_f32 v88, v88, v89\n" \
"v_exp_f32_e32 v92, v92\n" \
"v_exp_f32_e32 v93, v93\n" \
"v_exp_f32_e32 v94, v94\n" \
"v_exp_f32_e32 v95, v95\n" \
"v_cvt_pk_bf16_f32 v85, v82, v83\n" \
"buffer_load_dwordx4 v8, s[80:83], s92 offen offset:0 lds\n" \
"v_cvt_pk_bf16_f32 v84, v80, v81\n" \
"v_cvt_pk_bf16_f32 v89, v90, v91\n" \
"v_and_b32_e32 v84, v12, v84\n" \
"v_and_b32_e32 v85, v13, v85\n" \
"v_and_b32_e32 v86, v14, v86\n" \
"v_and_b32_e32 v87, v15, v87\n" \
"s_waitcnt vmcnt(17) lgkmcnt(0)\n" \
"s_barrier\n" \
"v_mfma_f32_16x16x32_bf16 v[120:123], a[128:131], a[48:51], v[44:47]\n" \
"buffer_load_dword v26, v27, s[56:59], s96 offen sc1\n" \
"v_mfma_f32_16x16x32_bf16 v[120:123], a[132:135], a[52:55], v[120:123]\n" \
"ds_read_b128 a[188:191], v49 offset:7168\n" \
"ds_read_b128 a[184:187], v49 offset:6144\n" \
"v_mfma_f32_16x16x32_bf16 v[120:123], a[136:139], a[56:59], v[120:123]\n" \
"s_xor_b32 m0, s65, 0x12000\n" \
"ds_read_b128 a[180:183], v49 offset:5120\n" \
"v_mfma_f32_16x16x32_bf16 v[120:123], a[140:143], a[60:63], v[120:123]\n" \
"ds_read_b128 a[176:179], v49 offset:4096\n" \
"ds_read_b128 a[172:175], v49 offset:3072\n" \
"v_mfma_f32_16x16x32_bf16 v[124:127], a[156:159], a[60:63], v[44:47]\n" \
"buffer_load_dwordx4 v50, s[8:11], s64 offen offset:0 sc1 lds\n" \
"v_mfma_f32_16x16x32_bf16 v[124:127], a[152:155], a[56:59], v[124:127]\n" \
"ds_read_b128 a[168:171], v49 offset:2048\n" \
"v_cvt_pk_bf16_f32 v90, v92, v93\n" \
"v_mfma_f32_16x16x32_bf16 v[124:127], a[148:151], a[52:55], v[124:127]\n" \
"v_cvt_pk_bf16_f32 v91, v94, v95\n" \
"ds_read_b128 a[164:167], v49 offset:1024\n" \
"v_mfma_f32_16x16x32_bf16 v[124:127], a[144:147], a[48:51], v[124:127]\n" \
"ds_read_b128 a[160:163], v49 offset:0\n" \
"ds_read_b128 a[0:3], v49 offset:8192\n" \
"v_mfma_f32_16x16x32_bf16 v[32:35], a[220:223], a[64:67], 0\n" \
"buffer_load_dwordx4 v50, s[8:11], s64 offen offset:1024 sc1 lds\n" \
"v_mfma_f32_16x16x32_bf16 v[36:39], a[220:223], a[80:83], 0\n" \
"ds_read_b128 a[4:7], v49 offset:9216\n" \
"ds_read_b128 a[8:11], v49 offset:10240\n" \
"v_mfma_f32_16x16x32_bf16 v[32:35], a[224:227], a[68:71], v[32:35]\n" \
"ds_read_b128 a[12:15], v49 offset:11264\n" \
"ds_read_b128 a[16:19], v49 offset:12288\n" \
"v_mfma_f32_16x16x32_bf16 v[36:39], a[224:227], a[84:87], v[36:39]\n" \
"ds_read_b128 a[20:23], v49 offset:13312\n" \
"ds_read_b128 a[24:27], v49 offset:14336\n" \
"v_mfma_f32_16x16x32_bf16 v[32:35], a[228:231], a[72:75], v[32:35]\n" \
"buffer_load_dwordx4 v50, s[8:11], s64 offen offset:2048 sc1 lds\n" \
"v_mfma_f32_16x16x32_bf16 v[36:39], a[228:231], a[88:91], v[36:39]\n" \
"ds_read_b128 a[28:31], v49 offset:15360\n" \
"v_readlane_b32 s97, v11, 36\n" \
"v_mfma_f32_16x16x32_bf16 v[32:35], a[232:235], a[76:79], v[32:35]\n" \
"v_exp_f32_e32 v120, v120\n" \
"v_mfma_f32_16x16x32_bf16 v[36:39], a[232:235], a[92:95], v[36:39]\n" \
"v_exp_f32_e32 v121, v121\n" \
"v_smfmac_f32_16x16x64_bf16 a[196:199], a[200:203], v[84:91], v21\n" \
"v_cvt_pk_bf16_f32 v120, v120, v121\n" \
"s_waitcnt lgkmcnt(13)\n" \
"v_mfma_f32_16x16x32_bf16 v[220:223], a[188:191], v[84:87], v[220:223]\n" \
"buffer_load_dwordx4 v50, s[8:11], s64 offen offset:3072 sc1 lds\n" \
"v_mfma_f32_16x16x32_bf16 v[252:255], a[188:191], v[88:91], v[252:255]\n" \
"v_exp_f32_e32 v122, v122\n" \
"v_mfma_f32_16x16x32_bf16 v[216:219], a[184:187], v[84:87], v[216:219]\n" \
"v_exp_f32_e32 v123, v123\n" \
"v_mfma_f32_16x16x32_bf16 v[248:251], a[184:187], v[88:91], v[248:251]\n" \
"v_exp_f32_e32 v124, v124\n" \
"v_mfma_f32_16x16x32_bf16 v[212:215], a[180:183], v[84:87], v[212:215]\n" \
"v_exp_f32_e32 v125, v125\n" \
"v_mfma_f32_16x16x32_bf16 v[244:247], a[180:183], v[88:91], v[244:247]\n" \
"v_cvt_pk_bf16_f32 v121, v122, v123\n" \
"s_waitcnt lgkmcnt(7)\n" \
"v_mfma_f32_16x16x32_bf16 v[208:211], a[176:179], v[84:87], v[208:211]\n" \
"v_exp_f32_e32 v126, v126\n" \
"v_mfma_f32_16x16x32_bf16 v[240:243], a[176:179], v[88:91], v[240:243]\n" \
"v_exp_f32_e32 v127, v127\n" \
"v_mfma_f32_16x16x32_bf16 v[204:207], a[172:175], v[84:87], v[204:207]\n" \
"v_cvt_pk_bf16_f32 v122, v124, v125\n" \
"v_cvt_pk_bf16_f32 v123, v126, v127\n" \
"v_mfma_f32_16x16x32_bf16 v[236:239], a[172:175], v[88:91], v[236:239]\n" \
"v_and_b32_e32 v120, v12, v120\n" \
"v_and_b32_e32 v121, v13, v121\n" \
"v_mfma_f32_16x16x32_bf16 v[200:203], a[168:171], v[84:87], v[200:203]\n" \
"v_and_b32_e32 v122, v14, v122\n" \
"v_and_b32_e32 v123, v15, v123\n" \
"v_mfma_f32_16x16x32_bf16 v[232:235], a[168:171], v[88:91], v[232:235]\n" \
"v_accvgpr_read_b32 v56, a196\n" \
"v_accvgpr_read_b32 v24, a197\n" \
"v_mfma_f32_16x16x32_bf16 v[196:199], a[164:167], v[84:87], v[196:199]\n" \
"v_add_u32_e32 v17, 0x49000, v20\n" \
"v_readlane_b32 s98, v11, 37\n" \
"v_mfma_f32_16x16x32_bf16 v[228:231], a[164:167], v[88:91], v[228:231]\n" \
"v_add_u32_e32 v48, s16, v20\n" \
"ds_read_b128 a[128:131], v48 offset:0\n" \
"v_mfma_f32_16x16x32_bf16 v[192:195], a[160:163], v[84:87], v[192:195]\n" \
"ds_read_b128 a[144:147], v48 offset:4096\n" \
"ds_read_b128 a[132:135], v48 offset:1024\n" \
"v_mfma_f32_16x16x32_bf16 v[224:227], a[160:163], v[88:91], v[224:227]\n" \
"ds_read_b128 a[148:151], v48 offset:5120\n" \
"ds_read_b128 a[136:139], v48 offset:2048\n" \
"v_mfma_f32_16x16x32_bf16 a[196:199], a[212:215], v[120:123], a[196:199]\n" \
"ds_read_b128 a[152:155], v48 offset:6144\n" \
"ds_read_b128 a[140:143], v48 offset:3072\n" \
"v_mfma_f32_16x16x32_bf16 v[224:227], a[0:3], v[120:123], v[224:227]\n" \
"ds_read_b128 a[156:159], v48 offset:7168\n" \
"s_waitcnt lgkmcnt(8)\n" \
"v_mfma_f32_16x16x32_bf16 v[228:231], a[4:7], v[120:123], v[228:231]\n" \
"v_accvgpr_read_b32 v55, a194\n" \
"v_accvgpr_read_b32 v23, a195\n" \
"v_mfma_f32_16x16x32_bf16 a[192:195], a[216:219], a[216:219], 0\n" \
"v_add_f32_e32 v55, v55, v23\n" \
"v_max_f32_e32 v62, v62, v55\n" \
"v_mfma_f32_16x16x32_bf16 v[232:235], a[8:11], v[120:123], v[232:235]\n" \
"v_rcp_f32_e32 v55, v55\n" \
"v_mfma_f32_16x16x32_bf16 v[236:239], a[12:15], v[120:123], v[236:239]\n" \
"v_mul_f32_e32 v55, v55, v59\n" \
"v_mul_f32_e32 v160, v55, v160\n" \
"v_mfma_f32_16x16x32_bf16 v[240:243], a[16:19], v[120:123], v[240:243]\n" \
"v_mul_f32_e32 v161, v55, v161\n" \
"v_mul_f32_e32 v168, v55, v168\n" \
"v_mfma_f32_16x16x32_bf16 v[244:247], a[20:23], v[120:123], v[244:247]\n" \
"v_mul_f32_e32 v169, v55, v169\n" \
"v_mul_f32_e32 v176, v55, v176\n" \
"v_mfma_f32_16x16x32_bf16 v[248:251], a[24:27], v[120:123], v[248:251]\n" \
"v_mul_f32_e32 v177, v55, v177\n" \
"v_mul_f32_e32 v184, v55, v184\n" \
"v_mfma_f32_16x16x32_bf16 v[252:255], a[28:31], v[120:123], v[252:255]\n" \
"v_mul_f32_e32 v185, v55, v185\n" \
"v_cvt_pk_bf16_f32 v160, v160, v161\n" \
"v_mul_f32_e32 v162, v55, v162\n" \
"v_mul_f32_e32 v163, v55, v163\n" \
"v_cvt_pk_bf16_f32 v168, v168, v169\n" \
"v_mul_f32_e32 v170, v55, v170\n" \
"v_mul_f32_e32 v171, v55, v171\n" \
"v_cvt_pk_bf16_f32 v176, v176, v177\n" \
"v_mul_f32_e32 v178, v55, v178\n" \
"s_waitcnt vmcnt(4)\n" \
"v_cmp_ne_u32_e32 vcc, s62, v26\n" \
"v_mul_f32_e32 v179, v55, v179\n" \
"v_cvt_pk_bf16_f32 v184, v184, v185\n" \
"v_mul_f32_e32 v186, v55, v186\n" \
"v_mul_f32_e32 v187, v55, v187\n" \
"v_cvt_pk_bf16_f32 v161, v162, v163\n" \
"s_cbranch_vccnz L_sos_%=\n" \
"L_ros_%=:\n" \
"buffer_load_dwordx4 a[220:223], v20, s[56:59], s97 offen offset:0 sc1\n" \
"v_mul_f32_e32 v164, v55, v164\n" \
"v_mul_f32_e32 v165, v55, v165\n" \
"v_mul_f32_e32 v166, v55, v166\n" \
"v_mul_f32_e32 v167, v55, v167\n" \
"v_cvt_pk_bf16_f32 v169, v170, v171\n" \
"v_mul_f32_e32 v172, v55, v172\n" \
"v_mul_f32_e32 v173, v55, v173\n" \
"v_mul_f32_e32 v174, v55, v174\n" \
"v_mul_f32_e32 v175, v55, v175\n" \
"v_cvt_pk_bf16_f32 v177, v178, v179\n" \
"v_mul_f32_e32 v180, v55, v180\n" \
"buffer_load_dwordx4 a[228:231], v17, s[56:59], s97 offen offset:0 sc1\n" \
"v_mul_f32_e32 v181, v55, v181\n" \
"v_mul_f32_e32 v182, v55, v182\n" \
"v_mul_f32_e32 v183, v55, v183\n" \
"v_cvt_pk_bf16_f32 v185, v186, v187\n" \
"v_mul_f32_e32 v188, v55, v188\n" \
"v_mul_f32_e32 v189, v55, v189\n" \
"v_mul_f32_e32 v190, v55, v190\n" \
"v_mul_f32_e32 v191, v55, v191\n" \
"v_cvt_pk_bf16_f32 v162, v164, v165\n" \
"v_cvt_pk_bf16_f32 v163, v166, v167\n" \
"v_cvt_pk_bf16_f32 v170, v172, v173\n" \
"buffer_store_dwordx4 v[160:163], v20, s[56:59], s69 offen offset:0 sc1\n" \
"v_cvt_pk_bf16_f32 v171, v174, v175\n" \
"v_cvt_pk_bf16_f32 v178, v180, v181\n" \
"v_cvt_pk_bf16_f32 v179, v182, v183\n" \
"v_cvt_pk_bf16_f32 v186, v188, v189\n" \
"v_cvt_pk_bf16_f32 v187, v190, v191\n" \
"v_readlane_b32 s93, v11, 20\n" \
"v_readlane_b32 s84, v11, 15\n" \
"v_readlane_b32 s85, v11, 16\n" \
"v_readlane_b32 s86, v11, 17\n" \
"s_mov_b32 s87, 0x20000\n" \
"v_readlane_b32 s92, v11, 18\n" \
"buffer_store_dwordx4 v[168:171], v20, s[56:59], s69 offen offset:1024 sc1\n" \
"v_readlane_b32 s91, v11, 21\n" \
"v_mfma_f32_16x16x32_bf16 a[208:211], a[216:219], v[128:131], a[208:211]\n" \
"v_mfma_f32_16x16x32_bf16 a[208:211], a[216:219], v[136:139], a[208:211]\n" \
"v_mfma_f32_32x32x16_bf16 v[128:143], a[216:219], a[216:219], 0\n" \
"buffer_store_dwordx4 v[176:179], v20, s[56:59], s69 offen offset:2048 sc1\n" \
"v_mfma_f32_16x16x32_bf16 a[208:211], a[216:219], v[144:147], a[208:211]\n" \
"v_mfma_f32_16x16x32_bf16 a[208:211], a[216:219], v[152:155], a[208:211]\n" \
"v_mfma_f32_32x32x16_bf16 v[144:159], a[216:219], a[216:219], 0\n" \
"buffer_store_dwordx4 v[184:187], v20, s[56:59], s69 offen offset:3072 sc1\n" \
"v_add_f32_e32 v56, v56, v24\n" \
"v_mfma_f32_16x16x32_bf16 a[208:211], a[216:219], v[160:163], a[208:211]\n" \
"v_max_f32_e32 v62, v62, v56\n" \
"v_rcp_f32_e32 v56, v56\n" \
"v_mfma_f32_16x16x32_bf16 a[208:211], a[216:219], v[168:171], a[208:211]\n" \
"v_mfma_f32_32x32x16_bf16 v[160:175], a[216:219], a[216:219], 0\n" \
"buffer_load_dwordx4 a[236:239], v20, s[56:59], s98 offen offset:0 sc1\n" \
"v_mul_f32_e32 v56, v56, v60\n" \
"v_mfma_f32_16x16x32_bf16 a[208:211], a[216:219], v[176:179], a[208:211]\n" \
"v_mul_f32_e32 v192, v56, v192\n" \
"v_mul_f32_e32 v193, v56, v193\n" \
"v_cvt_pk_bf16_f32 v192, v192, v193\n" \
"v_mul_f32_e32 v194, v56, v194\n" \
"v_mul_f32_e32 v195, v56, v195\n" \
"buffer_load_dwordx4 a[244:247], v17, s[56:59], s98 offen offset:0 sc1\n" \
"v_mfma_f32_16x16x32_bf16 a[208:211], a[216:219], v[184:187], a[208:211]\n" \
"v_mfma_f32_32x32x16_bf16 v[176:191], a[216:219], a[216:219], 0\n" \
"v_cvt_pk_bf16_f32 v193, v194, v195\n" \
"v_mul_f32_e32 v196, v56, v196\n" \
"v_mul_f32_e32 v197, v56, v197\n" \
"v_mul_f32_e32 v198, v56, v198\n" \
"v_mul_f32_e32 v199, v56, v199\n" \
"buffer_load_dwordx4 a[224:227], v20, s[56:59], s97 offen offset:1024 sc1\n" \
"v_mul_f32_e32 v200, v56, v200\n" \
"v_mul_f32_e32 v201, v56, v201\n" \
"v_accvgpr_read_b32 v57, a198\n" \
"v_accvgpr_read_b32 v25, a199\n" \
"v_mfma_f32_16x16x32_bf16 a[196:199], a[216:219], a[216:219], 0\n" \
"v_cvt_pk_bf16_f32 v194, v196, v197\n" \
"v_cvt_pk_bf16_f32 v195, v198, v199\n" \
"v_cvt_pk_bf16_f32 v200, v200, v201\n" \
"v_mul_f32_e32 v202, v56, v202\n" \
"v_mul_f32_e32 v203, v56, v203\n" \
"buffer_store_dwordx4 v[192:195], v20, s[56:59], s70 offen offset:0 sc1\n" \
"v_add_f32_e32 v57, v57, v25\n" \
"v_mfma_f32_16x16x32_bf16 a[208:211], a[216:219], v[192:195], a[208:211]\n" \
"v_cvt_pk_bf16_f32 v201, v202, v203\n" \
"v_mul_f32_e32 v204, v56, v204\n" \
"v_mul_f32_e32 v205, v56, v205\n" \
"v_mul_f32_e32 v206, v56, v206\n" \
"v_mul_f32_e32 v207, v56, v207\n" \
"v_mul_f32_e32 v208, v56, v208\n" \
"v_mul_f32_e32 v209, v56, v209\n" \
"v_max_f32_e32 v62, v62, v57\n" \
"buffer_load_dwordx4 a[232:235], v17, s[56:59], s97 offen offset:1024 sc1\n" \
"s_waitcnt vmcnt(16) lgkmcnt(0)\n" \
"s_barrier\n" \
"v_mfma_f32_16x16x32_bf16 v[64:67], a[128:131], a[64:67], v[32:35]\n" \
"s_add_u32 m0, s93, 0x0\n" \
"v_cvt_pk_bf16_f32 v202, v204, v205\n" \
"v_mfma_f32_16x16x32_bf16 v[64:67], a[132:135], a[68:71], v[64:67]\n" \
"buffer_load_dwordx4 a[240:243], v20, s[56:59], s98 offen offset:1024 sc1\n" \
"v_mfma_f32_16x16x32_bf16 v[64:67], a[136:139], a[72:75], v[64:67]\n" \
"v_cvt_pk_bf16_f32 v203, v206, v207\n" \
"v_cvt_pk_bf16_f32 v208, v208, v209\n" \
"v_mfma_f32_16x16x32_bf16 v[64:67], a[140:143], a[76:79], v[64:67]\n" \
"v_mul_f32_e32 v210, v56, v210\n" \
"v_mul_f32_e32 v211, v56, v211\n" \
"v_mfma_f32_16x16x32_bf16 v[68:71], a[156:159], a[76:79], v[32:35]\n" \
"v_rcp_f32_e32 v57, v57\n" \
"buffer_store_dwordx4 v[200:203], v20, s[56:59], s70 offen offset:1024 sc1\n" \
"v_mfma_f32_16x16x32_bf16 v[68:71], a[152:155], a[72:75], v[68:71]\n" \
"v_mfma_f32_16x16x32_bf16 a[208:211], a[216:219], v[200:203], a[208:211]\n" \
"v_mfma_f32_32x32x16_bf16 v[192:207], a[216:219], a[216:219], 0\n" \
"v_cvt_pk_bf16_f32 v209, v210, v211\n" \
"buffer_load_dwordx4 a[248:251], v17, s[56:59], s98 offen offset:1024 sc1\n" \
"v_mul_f32_e32 v212, v56, v212\n" \
"v_mul_f32_e32 v213, v56, v213\n" \
"v_mul_f32_e32 v214, v56, v214\n" \
"v_mfma_f32_16x16x32_bf16 v[68:71], a[148:151], a[68:71], v[68:71]\n" \
"v_mul_f32_e32 v215, v56, v215\n" \
"v_mul_f32_e32 v216, v56, v216\n" \
"v_mfma_f32_16x16x32_bf16 v[68:71], a[144:147], a[64:67], v[68:71]\n" \
"v_mul_f32_e32 v217, v56, v217\n" \
"v_cvt_pk_bf16_f32 v210, v212, v213\n" \
"buffer_load_dwordx4 v0, s[84:87], s92 offen offset:0 lds\n" \
"v_mfma_f32_16x16x32_bf16 v[76:79], a[144:147], a[80:83], v[36:39]\n" \
"ds_read_b128 a[144:147], v48 offset:12288\n" \
"v_cvt_pk_bf16_f32 v211, v214, v215\n" \
"v_mfma_f32_16x16x32_bf16 v[76:79], a[148:151], a[84:87], v[76:79]\n" \
"ds_read_b128 a[148:151], v48 offset:13312\n" \
"v_cvt_pk_bf16_f32 v216, v216, v217\n" \
"v_mfma_f32_16x16x32_bf16 v[76:79], a[152:155], a[88:91], v[76:79]\n" \
"ds_read_b128 a[152:155], v48 offset:14336\n" \
"v_mul_f32_e32 v218, v56, v218\n" \
"buffer_store_dwordx4 v[208:211], v20, s[56:59], s70 offen offset:2048 sc1\n" \
"v_mfma_f32_16x16x32_bf16 v[76:79], a[156:159], a[92:95], v[76:79]\n" \
"ds_read_b128 a[156:159], v48 offset:15360\n" \
"v_mul_f32_e32 v219, v56, v219\n" \
"v_mfma_f32_16x16x32_bf16 v[72:75], a[140:143], a[92:95], v[36:39]\n" \
"ds_read_b128 a[140:143], v48 offset:11264\n" \
"v_mul_f32_e32 v57, v57, v61\n" \
"v_mfma_f32_16x16x32_bf16 v[72:75], a[136:139], a[88:91], v[72:75]\n" \
".fill (L_ab + 28672 - .) / 4, 4, 0xBF800000\n" \
"L_ap7:\n" \
"s_cbranch_execz L_ap0\n" \
"ds_read_b128 a[136:139], v48 offset:10240\n" \
"v_cvt_pk_bf16_f32 v217, v218, v219\n" \
"buffer_load_dwordx4 v1, s[84:87], s92 offen offset:1024 lds\n" \
"v_mfma_f32_16x16x32_bf16 v[72:75], a[132:135], a[84:87], v[72:75]\n" \
"ds_read_b128 a[132:135], v48 offset:9216\n" \
"v_mul_f32_e32 v220, v56, v220\n" \
"v_mfma_f32_16x16x32_bf16 v[72:75], a[128:131], a[80:83], v[72:75]\n" \
"ds_read_b128 a[128:131], v48 offset:8192\n" \
"v_mfma_f32_16x16x32_bf16 a[208:211], a[216:219], v[208:211], a[208:211]\n" \
"v_mul_f32_e32 v221, v56, v221\n" \
"buffer_load_dwordx4 v2, s[84:87], s92 offen offset:2048 lds\n" \
"v_mul_f32_e32 v222, v56, v222\n" \
"v_mul_f32_e32 v223, v56, v223\n" \
"v_mul_f32_e32 v224, v57, v224\n" \
"v_mul_f32_e32 v225, v57, v225\n" \
"v_cvt_pk_bf16_f32 v218, v220, v221\n" \
"v_cvt_pk_bf16_f32 v219, v222, v223\n" \
"v_cvt_pk_bf16_f32 v224, v224, v225\n" \
"v_mul_f32_e32 v226, v57, v226\n" \
"v_mul_f32_e32 v227, v57, v227\n" \
"v_mfma_f32_16x16x32_bf16 a[208:211], a[216:219], v[216:219], a[208:211]\n" \
"buffer_store_dwordx4 v[216:219], v20, s[56:59], s70 offen offset:3072 sc1\n" \
"v_cvt_pk_bf16_f32 v225, v226, v227\n" \
"v_mul_f32_e32 v228, v57, v228\n" \
"v_mfma_f32_32x32x16_bf16 v[208:223], a[216:219], a[216:219], 0\n" \
"v_mul_f32_e32 v229, v57, v229\n" \
"v_mul_f32_e32 v230, v57, v230\n" \
"v_mul_f32_e32 v231, v57, v231\n" \
"v_mul_f32_e32 v232, v57, v232\n" \
"v_mul_f32_e32 v233, v57, v233\n" \
"v_cvt_pk_bf16_f32 v226, v228, v229\n" \
"v_cvt_pk_bf16_f32 v227, v230, v231\n" \
"buffer_store_dwordx4 v[224:227], v20, s[56:59], s71 offen offset:0 sc1\n" \
"v_cvt_pk_bf16_f32 v232, v232, v233\n" \
"v_mul_f32_e32 v234, v57, v234\n" \
"v_mul_f32_e32 v235, v57, v235\n" \
"v_mfma_f32_16x16x32_bf16 a[208:211], a[216:219], v[224:227], a[208:211]\n" \
"v_cvt_pk_bf16_f32 v233, v234, v235\n" \
"v_mul_f32_e32 v236, v57, v236\n" \
"v_mul_f32_e32 v237, v57, v237\n" \
"v_mul_f32_e32 v238, v57, v238\n" \
"v_mul_f32_e32 v239, v57, v239\n" \
"v_mul_f32_e32 v240, v57, v240\n" \
"buffer_load_dwordx4 v3, s[84:87], s92 offen offset:3072 lds\n" \
"s_add_u32 m0, s91, 0x1000\n" \
"v_mul_f32_e32 v241, v57, v241\n" \
"v_cvt_pk_bf16_f32 v234, v236, v237\n" \
"v_cvt_pk_bf16_f32 v235, v238, v239\n" \
"v_cvt_pk_bf16_f32 v240, v240, v241\n" \
"v_mul_f32_e32 v242, v57, v242\n" \
"v_mul_f32_e32 v243, v57, v243\n" \
"v_mfma_f32_16x16x32_bf16 a[208:211], a[216:219], v[232:235], a[208:211]\n" \
"v_cvt_pk_bf16_f32 v241, v242, v243\n" \
"v_mul_f32_e32 v244, v57, v244\n" \
"buffer_store_dwordx4 v[232:235], v20, s[56:59], s71 offen offset:1024 sc1\n" \
"v_mul_f32_e32 v245, v57, v245\n" \
"v_mul_f32_e32 v246, v57, v246\n" \
"v_mfma_f32_32x32x16_bf16 v[224:239], a[216:219], a[216:219], 0\n" \
"v_mul_f32_e32 v247, v57, v247\n" \
"v_mul_f32_e32 v248, v57, v248\n" \
"v_mul_f32_e32 v249, v57, v249\n" \
"v_cvt_pk_bf16_f32 v242, v244, v245\n" \
"v_cvt_pk_bf16_f32 v243, v246, v247\n" \
"v_cvt_pk_bf16_f32 v248, v248, v249\n" \
"v_mul_f32_e32 v250, v57, v250\n" \
"buffer_store_dwordx4 v[240:243], v20, s[56:59], s71 offen offset:2048 sc1\n" \
"v_mul_f32_e32 v251, v57, v251\n" \
"v_mfma_f32_16x16x32_bf16 a[208:211], a[216:219], v[240:243], a[208:211]\n" \
"v_cvt_pk_bf16_f32 v249, v250, v251\n" \
"v_mul_f32_e32 v252, v57, v252\n" \
"v_mul_f32_e32 v253, v57, v253\n" \
"v_mul_f32_e32 v254, v57, v254\n" \
"v_mul_f32_e32 v255, v57, v255\n" \
"v_cvt_pk_bf16_f32 v250, v252, v253\n" \
"v_cvt_pk_bf16_f32 v251, v254, v255\n" \
"v_add_u32_e32 v53, 0x20000, v63\n" \
"buffer_store_dwordx4 v[248:251], v20, s[56:59], s71 offen offset:3072 sc1\n" \
"v_mfma_f32_16x16x32_bf16 a[208:211], a[216:219], v[248:251], a[208:211]\n" \
"v_exp_f32_e32 v70, v70\n" \
"v_mfma_f32_32x32x16_bf16 v[240:255], a[216:219], a[216:219], 0\n" \
"v_exp_f32_e32 v71, v71\n" \
"v_exp_f32_e32 v72, v72\n" \
"v_exp_f32_e32 v73, v73\n" \
"buffer_load_dwordx4 v4, s[84:87], s92 offen offset:0 lds\n" \
"s_add_u32 s64, s64, 0x10000\n" \
"v_cmp_lt_f32_e64 s[72:73], s21, v62\n" \
"v_accvgpr_read_b32 v63, a208\n" \
"v_cmp_u_f32_e32 vcc, v63, v63\n" \
"v_readlane_b32 s95, v11, 3\n" \
"v_exp_f32_e32 v68, v68\n" \
"v_exp_f32_e32 v69, v69\n" \
"v_exp_f32_e32 v74, v74\n" \
"buffer_load_dwordx4 v5, s[84:87], s92 offen offset:1024 lds\n" \
"v_exp_f32_e32 v75, v75\n" \
"s_or_b64 vcc, s[72:73], vcc\n" \
"s_cselect_b32 s95, 0x40000000, s95\n" \
"s_cselect_b32 s94, 0x7f800000, 0\n" \
"v_cvt_pk_bf16_f32 v71, v70, v71\n" \
"v_exp_f32_e32 v66, v66\n" \
"v_exp_f32_e32 v67, v67\n" \
"v_exp_f32_e32 v64, v64\n" \
"buffer_load_dwordx4 v6, s[84:87], s92 offen offset:2048 lds\n" \
"v_exp_f32_e32 v65, v65\n" \
"v_cvt_pk_bf16_f32 v72, v72, v73\n" \
"v_exp_f32_e32 v76, v76\n" \
"v_exp_f32_e32 v77, v77\n" \
"v_exp_f32_e32 v78, v78\n" \
"v_exp_f32_e32 v79, v79\n" \
"buffer_load_dwordx4 v7, s[84:87], s92 offen offset:3072 lds\n" \
"s_add_u32 m0, s91, 0x2000\n" \
"v_cvt_pk_bf16_f32 v70, v68, v69\n" \
"v_cvt_pk_bf16_f32 v73, v74, v75\n" \
"v_add_u32_e32 v49, 0x10000, v48\n" \
"s_xor_b32 s65, s65, 0x8000\n" \
"v_cvt_pk_bf16_f32 v69, v66, v67\n" \
"v_cvt_pk_bf16_f32 v68, v64, v65\n" \
"v_cvt_pk_bf16_f32 v74, v76, v77\n" \
"v_cvt_pk_bf16_f32 v75, v78, v79\n" \
"v_xor_b32_e32 v48, 0x8000, v48\n" \
"s_sub_u32 s66, s19, 2\n" \
"buffer_load_dwordx4 v8, s[84:87], s92 offen offset:0 lds\n" \
"s_waitcnt vmcnt(18) lgkmcnt(0)\n" \
"s_barrier\n" \
"s_branch L_f1_%=\n" \
".fill (L_ab + 32768 - .) / 4, 4, 0xBF800000\n" \
".fill (L_ab + 32768 - .) / 4, 4, 0xBF800000\n" \
"L_ap8:\n" \
"s_setpc_b64 s[88:89]\n" \
"L_out_%=:\n" \
"v_bfe_u32 v38, v20, 2, 4\n" \
"v_readlane_b32 s64, v11, 2\n" \
"v_add_u32_e32 v32, 0x49000, v20\n" \
"v_add_u32_e32 v34, 0x20000, v10\n" \
"ds_read_b64_tr_b16 a[0:1], v34 offset:0\n" \
"ds_read_b64_tr_b16 a[2:3], v34 offset:4608\n" \
"ds_read_b64_tr_b16 a[4:5], v34 offset:32\n" \
"buffer_load_dword v26, v38, s[56:59], s64 offen sc1\n" \
"ds_read_b64_tr_b16 a[6:7], v34 offset:4640\n" \
"ds_read_b64_tr_b16 a[8:9], v34 offset:64\n" \
"ds_read_b64_tr_b16 a[10:11], v34 offset:4672\n" \
"ds_read_b64_tr_b16 a[12:13], v34 offset:96\n" \
"buffer_store_dwordx4 v[64:67], v20, s[56:59], s61 offen offset:0 sc1\n" \
"ds_read_b64_tr_b16 a[14:15], v34 offset:4704\n" \
"v_mov_b32_e32 v39, s62\n" \
"v_readlane_b32 s70, v11, 5\n" \
"v_readlane_b32 s71, v11, 4\n" \
"buffer_store_dwordx4 v[68:71], v20, s[56:59], s61 offen offset:1024 sc1\n" \
"ds_read_b64_tr_b16 a[16:17], v34 offset:128\n" \
"ds_read_b64_tr_b16 a[18:19], v34 offset:4736\n" \
"ds_read_b64_tr_b16 a[20:21], v34 offset:160\n" \
"ds_read_b64_tr_b16 a[22:23], v34 offset:4768\n" \
"buffer_store_dwordx4 v[72:75], v20, s[56:59], s61 offen offset:2048 sc1\n" \
"ds_read_b64_tr_b16 a[24:25], v34 offset:192\n" \
"ds_read_b64_tr_b16 a[26:27], v34 offset:4800\n" \
"ds_read_b64_tr_b16 a[28:29], v34 offset:224\n" \
"ds_read_b64_tr_b16 a[30:31], v34 offset:4832\n" \
"buffer_store_dwordx4 v[76:79], v20, s[56:59], s61 offen offset:3072 sc1\n" \
"ds_read_b64_tr_b16 a[192:193], v34 offset:256\n" \
"ds_read_b64_tr_b16 a[194:195], v34 offset:4864\n" \
"ds_read_b64_tr_b16 a[196:197], v34 offset:9216\n" \
"ds_read_b64_tr_b16 a[198:199], v34 offset:13824\n" \
"buffer_store_dwordx4 v[80:83], v32, s[56:59], s61 offen offset:0 sc1\n" \
"ds_read_b64_tr_b16 a[200:201], v34 offset:9248\n" \
"ds_read_b64_tr_b16 a[202:203], v34 offset:13856\n" \
"ds_read_b64_tr_b16 a[204:205], v34 offset:9280\n" \
"ds_read_b64_tr_b16 a[206:207], v34 offset:13888\n" \
"buffer_store_dwordx4 v[84:87], v32, s[56:59], s61 offen offset:1024 sc1\n" \
"ds_read_b64_tr_b16 a[208:209], v34 offset:9312\n" \
"s_waitcnt vmcnt(16) lgkmcnt(15)\n" \
"v_mfma_f32_16x16x32_bf16 v[112:115], a[0:3], a[96:99], 0\n" \
"buffer_store_dwordx4 v[96:99], v32, s[56:59], s61 offen offset:2048 sc1\n" \
"v_mfma_f32_16x16x32_bf16 v[116:119], a[0:3], a[112:115], 0\n" \
"ds_read_b64_tr_b16 a[210:211], v34 offset:13920\n" \
"ds_read_b64_tr_b16 a[212:213], v34 offset:9344\n" \
"buffer_store_dwordx4 v[100:103], v32, s[56:59], s61 offen offset:3072 sc1\n" \
"v_mfma_f32_16x16x32_bf16 v[184:187], a[0:3], v[64:67], 0\n" \
"ds_read_b64_tr_b16 a[214:215], v34 offset:13952\n" \
"ds_read_b64_tr_b16 a[216:217], v34 offset:9376\n" \
"v_mfma_f32_16x16x32_bf16 v[188:191], a[0:3], v[80:83], 0\n" \
"ds_read_b64_tr_b16 a[218:219], v34 offset:13984\n" \
"ds_read_b64_tr_b16 a[0:1], v34 offset:9408\n" \
"v_mfma_f32_16x16x32_bf16 v[120:123], a[4:7], a[96:99], 0\n" \
"ds_read_b64_tr_b16 a[2:3], v34 offset:14016\n" \
"v_lshrrev_b32_e32 v44, 9, v20\n" \
"v_mfma_f32_16x16x32_bf16 v[124:127], a[4:7], a[112:115], 0\n" \
"v_mul_u32_u24_e32 v44, 0xb40, v44\n" \
"v_add_u32_e32 v35, v44, v10\n" \
"v_mfma_f32_16x16x32_bf16 v[192:195], a[4:7], v[64:67], 0\n" \
"v_readlane_b32 s65, v11, 30\n" \
"v_readlane_b32 s67, v11, 23\n" \
"v_mfma_f32_16x16x32_bf16 v[196:199], a[4:7], v[80:83], 0\n" \
"ds_read_b64_tr_b16 a[4:5], v34 offset:9440\n" \
"ds_read_b64_tr_b16 a[6:7], v34 offset:14048\n" \
"v_mfma_f32_16x16x32_bf16 v[128:131], a[8:11], a[96:99], 0\n" \
"v_readlane_b32 s80, v11, 11\n" \
"v_readlane_b32 s81, v11, 12\n" \
"v_mfma_f32_16x16x32_bf16 v[132:135], a[8:11], a[112:115], 0\n" \
"s_mov_b32 s82, 0x47fca0\n" \
"s_mov_b32 s83, 0x20000\n" \
"v_mfma_f32_16x16x32_bf16 v[200:203], a[8:11], v[64:67], 0\n" \
"v_readlane_b32 s66, v11, 28\n" \
"s_add_u32 m0, s67, 0x0\n" \
"v_mfma_f32_16x16x32_bf16 v[204:207], a[8:11], v[80:83], 0\n" \
"ds_read_b64_tr_b16 a[8:9], v34 offset:9472\n" \
"ds_read_b64_tr_b16 a[10:11], v34 offset:14080\n" \
"v_mfma_f32_16x16x32_bf16 v[136:139], a[12:15], a[96:99], 0\n" \
"buffer_load_dwordx4 v0, s[80:83], s66 offen offset:0 lds\n" \
"v_mfma_f32_16x16x32_bf16 v[140:143], a[12:15], a[112:115], 0\n" \
"v_readlane_b32 s92, v11, 24\n" \
"v_readlane_b32 s73, v11, 29\n" \
"v_mfma_f32_16x16x32_bf16 v[208:211], a[12:15], v[64:67], 0\n" \
"v_readlane_b32 s94, v11, 25\n" \
"v_readlane_b32 s84, v11, 9\n" \
"v_mfma_f32_16x16x32_bf16 v[212:215], a[12:15], v[80:83], 0\n" \
"ds_read_b64_tr_b16 a[12:13], v34 offset:18432\n" \
"ds_read_b64_tr_b16 a[14:15], v34 offset:23040\n" \
"v_mfma_f32_16x16x32_bf16 v[144:147], a[16:19], a[96:99], 0\n" \
"v_readlane_b32 s85, v11, 10\n" \
"s_mov_b32 s86, 0xa1ca0\n" \
"v_mfma_f32_16x16x32_bf16 v[148:151], a[16:19], a[112:115], 0\n" \
"buffer_load_dwordx4 v1, s[80:83], s66 offen offset:1024 lds\n" \
"v_mfma_f32_16x16x32_bf16 v[216:219], a[16:19], v[64:67], 0\n" \
"s_mov_b32 s87, 0x20000\n" \
"v_add_u32_e32 v33, 0x10000, v10\n" \
"v_mfma_f32_16x16x32_bf16 v[220:223], a[16:19], v[80:83], 0\n" \
"ds_read_b64_tr_b16 a[16:17], v34 offset:18464\n" \
"s_waitcnt lgkmcnt(7)\n" \
"v_mfma_f32_16x16x32_bf16 v[152:155], a[20:23], a[96:99], 0\n" \
"ds_read_b64_tr_b16 a[18:19], v34 offset:23072\n" \
"v_readlane_b32 s68, v11, 31\n" \
"v_mfma_f32_16x16x32_bf16 v[156:159], a[20:23], a[112:115], 0\n" \
"v_readlane_b32 s69, v11, 32\n" \
"v_readlane_b32 s72, v11, 34\n" \
"v_mfma_f32_16x16x32_bf16 v[224:227], a[20:23], v[64:67], 0\n" \
"buffer_load_dwordx4 v2, s[80:83], s66 offen offset:2048 lds\n" \
"v_mfma_f32_16x16x32_bf16 v[228:231], a[20:23], v[80:83], 0\n" \
"ds_read_b64_tr_b16 a[20:21], v34 offset:18496\n" \
"ds_read_b64_tr_b16 a[22:23], v34 offset:23104\n" \
"v_mfma_f32_16x16x32_bf16 v[160:163], a[24:27], a[96:99], 0\n" \
"v_readlane_b32 s93, v11, 35\n" \
"v_readlane_b32 s63, v11, 41\n" \
"v_mfma_f32_16x16x32_bf16 v[164:167], a[24:27], a[112:115], 0\n" \
"v_bfe_u32 v40, v20, 2, 7\n" \
"s_add_u32 s74, s63, 0x3800\n" \
"v_mfma_f32_16x16x32_bf16 v[232:235], a[24:27], v[64:67], 0\n" \
"v_lshlrev_b32_e32 v41, 12, v20\n" \
"s_add_u32 s12, s63, 0x0\n" \
"v_mfma_f32_16x16x32_bf16 v[236:239], a[24:27], v[80:83], 0\n" \
"ds_read_b64_tr_b16 a[24:25], v34 offset:18528\n" \
"ds_read_b64_tr_b16 a[26:27], v34 offset:23136\n" \
"v_mfma_f32_16x16x32_bf16 v[168:171], a[28:31], a[96:99], 0\n" \
"buffer_load_dwordx4 v3, s[80:83], s66 offen offset:3072 lds\n" \
"v_mfma_f32_16x16x32_bf16 v[172:175], a[28:31], a[112:115], 0\n" \
"s_add_u32 m0, s67, 0x1000\n" \
"v_readlane_b32 s95, v11, 38\n" \
"v_mfma_f32_16x16x32_bf16 v[240:243], a[28:31], v[64:67], 0\n" \
"v_readlane_b32 s96, v11, 39\n" \
"s_add_u32 s13, s63, 0x1000\n" \
"v_mfma_f32_16x16x32_bf16 v[244:247], a[28:31], v[80:83], 0\n" \
"ds_read_b64_tr_b16 a[28:29], v34 offset:18560\n" \
"ds_read_b64_tr_b16 a[30:31], v34 offset:23168\n" \
"v_mfma_f32_16x16x32_bf16 v[176:179], a[192:195], a[96:99], 0\n" \
"v_readlane_b32 s97, v11, 40\n" \
"s_add_u32 s14, s63, 0x2000\n" \
"v_mfma_f32_16x16x32_bf16 v[180:183], a[192:195], a[112:115], 0\n" \
"buffer_load_dwordx4 v4, s[80:83], s66 offen offset:0 lds\n" \
"v_mfma_f32_16x16x32_bf16 v[248:251], a[192:195], v[64:67], 0\n" \
"s_add_u32 s15, s63, 0x3000\n" \
"v_readlane_b32 s100, v11, 7\n" \
"v_mfma_f32_16x16x32_bf16 v[252:255], a[192:195], v[80:83], 0\n" \
"ds_read_b64_tr_b16 a[192:193], v34 offset:18592\n" \
"ds_read_b64_tr_b16 a[194:195], v34 offset:23200\n" \
"v_mfma_f32_16x16x32_bf16 v[112:115], a[196:199], a[100:103], v[112:115]\n" \
"v_readlane_b32 s16, v11, 42\n" \
"v_bfe_u32 v44, v20, 4, 4\n" \
"v_mfma_f32_16x16x32_bf16 v[116:119], a[196:199], a[116:119], v[116:119]\n" \
"s_add_u32 s17, s16, 0x1000\n" \
"s_add_u32 s18, s16, 0x2000\n" \
"v_mfma_f32_16x16x32_bf16 v[184:187], a[196:199], v[68:71], v[184:187]\n" \
"buffer_load_dwordx4 v5, s[80:83], s66 offen offset:1024 lds\n" \
"v_mfma_f32_16x16x32_bf16 v[188:191], a[196:199], v[84:87], v[188:191]\n" \
"ds_read_b64_tr_b16 a[196:197], v34 offset:18624\n" \
"ds_read_b64_tr_b16 a[198:199], v34 offset:23232\n" \
"v_mfma_f32_16x16x32_bf16 v[120:123], a[200:203], a[100:103], v[120:123]\n" \
"s_add_u32 s19, s16, 0x3000\n" \
"v_bfe_u32 v45, v20, 8, 2\n" \
"v_mfma_f32_16x16x32_bf16 v[124:127], a[200:203], a[116:119], v[124:127]\n" \
"v_mul_u32_u24_e32 v42, 0x480, v44\n" \
"v_lshl_add_u32 v42, v45, 3, v42\n" \
"v_mfma_f32_16x16x32_bf16 v[192:195], a[200:203], v[68:71], v[192:195]\n" \
"s_waitcnt vmcnt(14)\n" \
"v_cmp_ne_u32_e32 vcc, s62, v26\n" \
"v_mfma_f32_16x16x32_bf16 v[196:199], a[200:203], v[84:87], v[196:199]\n" \
"ds_read_b64_tr_b16 a[200:201], v34 offset:18656\n" \
"ds_read_b64_tr_b16 a[202:203], v34 offset:23264\n" \
"v_mfma_f32_16x16x32_bf16 v[128:131], a[204:207], a[100:103], v[128:131]\n" \
"buffer_load_dwordx4 v6, s[80:83], s66 offen offset:2048 lds\n" \
"v_mfma_f32_16x16x32_bf16 v[132:135], a[204:207], a[116:119], v[132:135]\n" \
"s_cbranch_vccnz L_sa_%=\n" \
"L_ra_%=:\n" \
"v_readlane_b32 s64, v11, 33\n" \
"v_mfma_f32_16x16x32_bf16 v[200:203], a[204:207], v[68:71], v[200:203]\n" \
"v_lshrrev_b32_e32 v44, 1, v44\n" \
"v_lshlrev_b32_e32 v45, 1, v45\n" \
"v_mfma_f32_16x16x32_bf16 v[204:207], a[204:207], v[84:87], v[204:207]\n" \
"ds_read_b64_tr_b16 a[204:205], v34 offset:18688\n" \
"ds_read_b64_tr_b16 a[206:207], v34 offset:23296\n" \
"v_mfma_f32_16x16x32_bf16 v[136:139], a[208:211], a[100:103], v[136:139]\n" \
"v_sub_u32_e32 v44, v44, v45\n" \
"v_and_b32_e32 v45, 16, v20\n" \
"v_mfma_f32_16x16x32_bf16 v[140:143], a[208:211], a[116:119], v[140:143]\n" \
"buffer_load_dwordx4 a[64:67], v20, s[56:59], s65 offen offset:0 sc1\n" \
"v_mfma_f32_16x16x32_bf16 v[208:211], a[208:211], v[68:71], v[208:211]\n" \
"v_mov_b32_e32 v46, 0x3f80\n" \
"v_lshlrev_b32_e32 v45, v45, v46\n" \
"v_mfma_f32_16x16x32_bf16 v[212:215], a[208:211], v[84:87], v[212:215]\n" \
"ds_read_b64_tr_b16 a[208:209], v10 offset:32768\n" \
"ds_read_b64_tr_b16 a[210:211], v10 offset:37376\n" \
"v_mfma_f32_16x16x32_bf16 v[144:147], a[212:215], a[100:103], v[144:147]\n" \
"v_mov_b32_e32 v52, 0\n" \
"v_mov_b32_e32 v53, 0\n" \
"v_mfma_f32_16x16x32_bf16 v[148:151], a[212:215], a[116:119], v[148:151]\n" \
"v_mov_b32_e32 v48, 0\n" \
"v_mov_b32_e32 v49, 0\n" \
"v_mfma_f32_16x16x32_bf16 v[216:219], a[212:215], v[68:71], v[216:219]\n" \
"buffer_load_dwordx4 a[80:83], v32, s[56:59], s65 offen offset:0 sc1\n" \
"v_mfma_f32_16x16x32_bf16 v[220:223], a[212:215], v[84:87], v[220:223]\n" \
"ds_read_b64_tr_b16 a[212:213], v10 offset:32800\n" \
"ds_read_b64_tr_b16 a[214:215], v10 offset:37408\n" \
"v_mfma_f32_16x16x32_bf16 v[152:155], a[216:219], a[100:103], v[152:155]\n" \
"v_add_u32_e32 v43, 0x9000, v42\n" \
"v_readlane_b32 s88, v11, 13\n" \
"v_mfma_f32_16x16x32_bf16 v[156:159], a[216:219], a[116:119], v[156:159]\n" \
"v_readlane_b32 s89, v11, 14\n" \
"s_mov_b32 s90, 0x240000\n" \
"v_mfma_f32_16x16x32_bf16 v[224:227], a[216:219], v[68:71], v[224:227]\n" \
"s_mov_b32 s91, 0x20000\n" \
"v_mov_b32_e32 v46, s62\n" \
"v_mfma_f32_16x16x32_bf16 v[228:231], a[216:219], v[84:87], v[228:231]\n" \
"ds_read_b64_tr_b16 a[216:217], v10 offset:32832\n" \
"ds_read_b64_tr_b16 a[218:219], v10 offset:37440\n" \
"v_mfma_f32_16x16x32_bf16 v[160:163], a[0:3], a[100:103], v[160:163]\n" \
"buffer_load_dwordx4 a[68:71], v20, s[56:59], s65 offen offset:1024 sc1\n" \
"v_mfma_f32_16x16x32_bf16 v[164:167], a[0:3], a[116:119], v[164:167]\n" \
"v_mfma_f32_16x16x32_bf16 v[232:235], a[0:3], v[68:71], v[232:235]\n" \
"v_mfma_f32_16x16x32_bf16 v[236:239], a[0:3], v[84:87], v[236:239]\n" \
"ds_read_b64_tr_b16 a[0:1], v10 offset:32864\n" \
"s_waitcnt lgkmcnt(7)\n" \
"v_mfma_f32_16x16x32_bf16 v[168:171], a[4:7], a[100:103], v[168:171]\n" \
"ds_read_b64_tr_b16 a[2:3], v10 offset:37472\n" \
"v_mfma_f32_16x16x32_bf16 v[172:175], a[4:7], a[116:119], v[172:175]\n" \
"buffer_load_dwordx4 a[84:87], v32, s[56:59], s65 offen offset:1024 sc1\n" \
"v_mfma_f32_16x16x32_bf16 v[240:243], a[4:7], v[68:71], v[240:243]\n" \
"v_mfma_f32_16x16x32_bf16 v[244:247], a[4:7], v[84:87], v[244:247]\n" \
"ds_read_b64_tr_b16 a[4:5], v10 offset:32896\n" \
"ds_read_b64_tr_b16 a[6:7], v10 offset:37504\n" \
"v_mfma_f32_16x16x32_bf16 v[176:179], a[8:11], a[100:103], v[176:179]\n" \
"v_mfma_f32_16x16x32_bf16 v[180:183], a[8:11], a[116:119], v[180:183]\n" \
"v_mfma_f32_16x16x32_bf16 v[248:251], a[8:11], v[68:71], v[248:251]\n" \
"buffer_load_dwordx4 a[72:75], v20, s[56:59], s65 offen offset:2048 sc1\n" \
"v_mfma_f32_16x16x32_bf16 v[252:255], a[8:11], v[84:87], v[252:255]\n" \
"ds_read_b64_tr_b16 a[8:9], v10 offset:32928\n" \
"ds_read_b64_tr_b16 a[10:11], v10 offset:37536\n" \
"v_mfma_f32_16x16x32_bf16 v[112:115], a[12:15], a[104:107], v[112:115]\n" \
"v_mfma_f32_16x16x32_bf16 v[116:119], a[12:15], a[120:123], v[116:119]\n" \
"v_mfma_f32_16x16x32_bf16 v[184:187], a[12:15], v[72:75], v[184:187]\n" \
"v_mfma_f32_16x16x32_bf16 v[188:191], a[12:15], v[96:99], v[188:191]\n" \
"ds_read_b64_tr_b16 a[12:13], v10 offset:32960\n" \
"ds_read_b64_tr_b16 a[14:15], v10 offset:37568\n" \
"v_mfma_f32_16x16x32_bf16 v[120:123], a[16:19], a[104:107], v[120:123]\n" \
"buffer_load_dwordx4 a[88:91], v32, s[56:59], s65 offen offset:2048 sc1\n" \
"v_mfma_f32_16x16x32_bf16 v[124:127], a[16:19], a[120:123], v[124:127]\n" \
"v_mfma_f32_16x16x32_bf16 v[192:195], a[16:19], v[72:75], v[192:195]\n" \
"v_mfma_f32_16x16x32_bf16 v[196:199], a[16:19], v[96:99], v[196:199]\n" \
"ds_read_b64_tr_b16 a[16:17], v10 offset:32992\n" \
"ds_read_b64_tr_b16 a[18:19], v10 offset:37600\n" \
"v_mfma_f32_16x16x32_bf16 v[128:131], a[20:23], a[104:107], v[128:131]\n" \
"v_mfma_f32_16x16x32_bf16 v[132:135], a[20:23], a[120:123], v[132:135]\n" \
"buffer_load_dwordx4 v7, s[80:83], s66 offen offset:3072 lds\n" \
"v_mfma_f32_16x16x32_bf16 v[200:203], a[20:23], v[72:75], v[200:203]\n" \
"s_add_u32 m0, s67, 0x2000\n" \
"v_readlane_b32 s67, v11, 48\n" \
"v_mfma_f32_16x16x32_bf16 v[204:207], a[20:23], v[96:99], v[204:207]\n" \
"ds_read_b64_tr_b16 a[20:21], v10 offset:33024\n" \
"ds_read_b64_tr_b16 a[22:23], v10 offset:37632\n" \
"s_barrier\n" \
"v_mfma_f32_16x16x32_bf16 v[136:139], a[24:27], a[104:107], v[136:139]\n" \
"v_mfma_f32_16x16x32_bf16 v[140:143], a[24:27], a[120:123], v[140:143]\n" \
"buffer_load_dwordx4 v8, s[80:83], s66 offen offset:0 lds\n" \
"v_mfma_f32_16x16x32_bf16 v[208:211], a[24:27], v[72:75], v[208:211]\n" \
"s_add_u32 m0, s92, 0x0\n" \
"v_readlane_b32 s66, v11, 47\n" \
"v_mfma_f32_16x16x32_bf16 v[212:215], a[24:27], v[96:99], v[212:215]\n" \
"ds_read_b64_tr_b16 a[24:25], v35 offset:41984\n" \
"ds_read_b64_tr_b16 a[26:27], v35 offset:44288\n" \
"v_mfma_f32_16x16x32_bf16 v[144:147], a[28:31], a[104:107], v[144:147]\n" \
"v_mfma_f32_16x16x32_bf16 v[148:151], a[28:31], a[120:123], v[148:151]\n" \
"v_mfma_f32_16x16x32_bf16 v[216:219], a[28:31], v[72:75], v[216:219]\n" \
"buffer_load_dwordx4 v0, s[80:83], s73 offen offset:0 lds\n" \
"v_mfma_f32_16x16x32_bf16 v[220:223], a[28:31], v[96:99], v[220:223]\n" \
"ds_read_b64_tr_b16 a[28:29], v35 offset:42016\n" \
"ds_read_b64_tr_b16 a[30:31], v35 offset:44320\n" \
"v_mfma_f32_16x16x32_bf16 v[152:155], a[192:195], a[104:107], v[152:155]\n" \
"v_mfma_f32_16x16x32_bf16 v[156:159], a[192:195], a[120:123], v[156:159]\n" \
"v_mfma_f32_16x16x32_bf16 v[224:227], a[192:195], v[72:75], v[224:227]\n" \
"v_mfma_f32_16x16x32_bf16 v[228:231], a[192:195], v[96:99], v[228:231]\n" \
"ds_read_b64_tr_b16 a[192:193], v35 offset:42048\n" \
"ds_read_b64_tr_b16 a[194:195], v35 offset:44352\n" \
"v_mfma_f32_16x16x32_bf16 v[160:163], a[196:199], a[104:107], v[160:163]\n" \
"buffer_load_dwordx4 v1, s[80:83], s73 offen offset:1024 lds\n" \
"v_mfma_f32_16x16x32_bf16 v[164:167], a[196:199], a[120:123], v[164:167]\n" \
"v_mfma_f32_16x16x32_bf16 v[232:235], a[196:199], v[72:75], v[232:235]\n" \
"v_mfma_f32_16x16x32_bf16 v[236:239], a[196:199], v[96:99], v[236:239]\n" \
"ds_read_b64_tr_b16 a[196:197], v35 offset:42080\n" \
"ds_read_b64_tr_b16 a[198:199], v35 offset:44384\n" \
"v_mfma_f32_16x16x32_bf16 v[168:171], a[200:203], a[104:107], v[168:171]\n" \
"v_mfma_f32_16x16x32_bf16 v[172:175], a[200:203], a[120:123], v[172:175]\n" \
"buffer_load_dwordx4 v2, s[80:83], s73 offen offset:2048 lds\n" \
"v_mfma_f32_16x16x32_bf16 v[240:243], a[200:203], v[72:75], v[240:243]\n" \
"v_mfma_f32_16x16x32_bf16 v[244:247], a[200:203], v[96:99], v[244:247]\n" \
"ds_read_b64_tr_b16 a[200:201], v35 offset:42112\n" \
"ds_read_b64_tr_b16 a[202:203], v35 offset:44416\n" \
"v_mfma_f32_16x16x32_bf16 v[176:179], a[204:207], a[104:107], v[176:179]\n" \
"v_mfma_f32_16x16x32_bf16 v[180:183], a[204:207], a[120:123], v[180:183]\n" \
"v_mfma_f32_16x16x32_bf16 v[248:251], a[204:207], v[72:75], v[248:251]\n" \
"buffer_load_dwordx4 v3, s[80:83], s73 offen offset:3072 lds\n" \
"v_mfma_f32_16x16x32_bf16 v[252:255], a[204:207], v[96:99], v[252:255]\n" \
"ds_read_b64_tr_b16 a[204:205], v35 offset:42144\n" \
"s_waitcnt lgkmcnt(7)\n" \
"v_mfma_f32_16x16x32_bf16 v[112:115], a[208:211], a[108:111], v[112:115]\n" \
"ds_read_b64_tr_b16 a[206:207], v35 offset:44448\n" \
"s_add_u32 m0, s92, 0x1000\n" \
"v_mfma_f32_16x16x32_bf16 v[116:119], a[208:211], a[124:127], v[116:119]\n" \
"v_mfma_f32_16x16x32_bf16 v[184:187], a[208:211], v[76:79], v[184:187]\n" \
"v_mfma_f32_16x16x32_bf16 v[188:191], a[208:211], v[100:103], v[188:191]\n" \
"ds_read_b64_tr_b16 a[208:209], v35 offset:42176\n" \
"ds_read_b64_tr_b16 a[210:211], v35 offset:44480\n" \
"v_mfma_f32_16x16x32_bf16 v[120:123], a[212:215], a[108:111], v[120:123]\n" \
"buffer_load_dwordx4 v4, s[80:83], s73 offen offset:0 lds\n" \
"v_mfma_f32_16x16x32_bf16 v[124:127], a[212:215], a[124:127], v[124:127]\n" \
"s_waitcnt vmcnt(19)\n" \
"s_mov_b64 exec, 1\n" \
"buffer_store_dword v39, off, s[56:59], s70 offset:0 sc1\n" \
"s_mov_b64 exec, -1\n" \
"v_mfma_f32_16x16x32_bf16 v[192:195], a[212:215], v[76:79], v[192:195]\n" \
"v_readlane_b32 s70, v11, 51\n" \
"v_mfma_f32_16x16x32_bf16 v[196:199], a[212:215], v[100:103], v[196:199]\n" \
"ds_read_b64_tr_b16 a[212:213], v35 offset:42208\n" \
"ds_read_b64_tr_b16 a[214:215], v35 offset:44512\n" \
"v_mfma_f32_16x16x32_bf16 v[128:131], a[216:219], a[108:111], v[128:131]\n" \
"v_mfma_f32_16x16x32_bf16 v[132:135], a[216:219], a[124:127], v[132:135]\n" \
"buffer_load_dwordx4 v5, s[80:83], s73 offen offset:1024 lds\n" \
"v_mfma_f32_16x16x32_bf16 v[200:203], a[216:219], v[76:79], v[200:203]\n" \
"v_mfma_f32_16x16x32_bf16 v[204:207], a[216:219], v[100:103], v[204:207]\n" \
"ds_read_b64_tr_b16 a[216:217], v35 offset:42240\n" \
"ds_read_b64_tr_b16 a[218:219], v35 offset:44544\n" \
"v_mfma_f32_16x16x32_bf16 v[136:139], a[0:3], a[108:111], v[136:139]\n" \
"v_mfma_f32_16x16x32_bf16 v[140:143], a[0:3], a[124:127], v[140:143]\n" \
"v_mfma_f32_16x16x32_bf16 v[208:211], a[0:3], v[76:79], v[208:211]\n" \
"buffer_load_dwordx4 v6, s[80:83], s73 offen offset:2048 lds\n" \
"v_mfma_f32_16x16x32_bf16 v[212:215], a[0:3], v[100:103], v[212:215]\n" \
"ds_read_b64_tr_b16 a[0:1], v35 offset:52352\n" \
"ds_read_b64_tr_b16 a[2:3], v35 offset:54656\n" \
"v_mfma_f32_16x16x32_bf16 v[144:147], a[4:7], a[108:111], v[144:147]\n" \
"v_mfma_f32_16x16x32_bf16 v[148:151], a[4:7], a[124:127], v[148:151]\n" \
"v_mfma_f32_16x16x32_bf16 v[216:219], a[4:7], v[76:79], v[216:219]\n" \
"v_mfma_f32_16x16x32_bf16 v[220:223], a[4:7], v[100:103], v[220:223]\n" \
"ds_read_b64_tr_b16 a[4:5], v35 offset:52384\n" \
"ds_read_b64_tr_b16 a[6:7], v35 offset:54688\n" \
"v_mfma_f32_16x16x32_bf16 v[152:155], a[8:11], a[108:111], v[152:155]\n" \
"buffer_load_dwordx4 v7, s[80:83], s73 offen offset:3072 lds\n" \
"v_mfma_f32_16x16x32_bf16 v[156:159], a[8:11], a[124:127], v[156:159]\n" \
"s_add_u32 m0, s92, 0x2000\n" \
"v_readlane_b32 s92, v11, 46\n" \
"v_mfma_f32_16x16x32_bf16 v[224:227], a[8:11], v[76:79], v[224:227]\n" \
"v_mfma_f32_16x16x32_bf16 v[228:231], a[8:11], v[100:103], v[228:231]\n" \
"ds_read_b64_tr_b16 a[8:9], v35 offset:52416\n" \
"ds_read_b64_tr_b16 a[10:11], v35 offset:54720\n" \
"v_mfma_f32_16x16x32_bf16 v[160:163], a[12:15], a[108:111], v[160:163]\n" \
"v_mfma_f32_16x16x32_bf16 v[164:167], a[12:15], a[124:127], v[164:167]\n" \
"buffer_load_dwordx4 v8, s[80:83], s73 offen offset:0 lds\n" \
"v_mfma_f32_16x16x32_bf16 v[232:235], a[12:15], v[76:79], v[232:235]\n" \
"s_mov_b32 m0, s94\n" \
"v_readlane_b32 s73, v11, 6\n" \
"v_mfma_f32_16x16x32_bf16 v[236:239], a[12:15], v[100:103], v[236:239]\n" \
"ds_read_b64_tr_b16 a[12:13], v35 offset:52448\n" \
"ds_read_b64_tr_b16 a[14:15], v35 offset:54752\n" \
"v_mfma_f32_16x16x32_bf16 v[168:171], a[16:19], a[108:111], v[168:171]\n" \
"v_mfma_f32_16x16x32_bf16 v[172:175], a[16:19], a[124:127], v[172:175]\n" \
"v_mfma_f32_16x16x32_bf16 v[240:243], a[16:19], v[76:79], v[240:243]\n" \
"buffer_load_dwordx4 v9, s[84:87], 0 offen lds\n" \
"v_mfma_f32_16x16x32_bf16 v[244:247], a[16:19], v[100:103], v[244:247]\n" \
"ds_read_b64_tr_b16 a[16:17], v35 offset:52480\n" \
"ds_read_b64_tr_b16 a[18:19], v35 offset:54784\n" \
"v_mfma_f32_16x16x32_bf16 v[176:179], a[20:23], a[108:111], v[176:179]\n" \
"v_mfma_f32_16x16x32_bf16 v[180:183], a[20:23], a[124:127], v[180:183]\n" \
"v_mfma_f32_16x16x32_bf16 v[248:251], a[20:23], v[76:79], v[248:251]\n" \
"v_mfma_f32_16x16x32_bf16 v[252:255], a[20:23], v[100:103], v[252:255]\n" \
"ds_read_b64_tr_b16 a[20:21], v35 offset:52512\n" \
"ds_read_b64_tr_b16 a[22:23], v35 offset:54816\n" \
"v_mfma_f32_16x16x32_bf16 v[112:115], a[24:27], a[220:223], v[112:115]\n" \
"buffer_load_dwordx4 a[76:79], v20, s[56:59], s65 offen offset:3072 sc1\n" \
"v_mfma_f32_16x16x32_bf16 v[116:119], a[24:27], a[228:231], v[116:119]\n" \
"v_mfma_f32_16x16x32_bf16 v[184:187], a[24:27], a[236:239], v[184:187]\n" \
"v_mfma_f32_16x16x32_bf16 v[188:191], a[24:27], a[244:247], v[188:191]\n" \
"ds_read_b64_tr_b16 a[24:25], v35 offset:52544\n" \
"ds_read_b64_tr_b16 a[26:27], v35 offset:54848\n" \
"v_mfma_f32_16x16x32_bf16 v[120:123], a[28:31], a[220:223], v[120:123]\n" \
"v_mfma_f32_16x16x32_bf16 v[124:127], a[28:31], a[228:231], v[124:127]\n" \
"buffer_load_dword v47, v38, s[56:59], s71 offen sc1\n" \
"v_mfma_f32_16x16x32_bf16 v[192:195], a[28:31], a[236:239], v[192:195]\n" \
"v_mfma_f32_16x16x32_bf16 v[196:199], a[28:31], a[244:247], v[196:199]\n" \
"ds_read_b64_tr_b16 a[28:29], v35 offset:52576\n" \
"s_waitcnt lgkmcnt(7)\n" \
"v_mfma_f32_16x16x32_bf16 v[128:131], a[192:195], a[220:223], v[128:131]\n" \
"ds_read_b64_tr_b16 a[30:31], v35 offset:54880\n" \
"v_mfma_f32_16x16x32_bf16 v[132:135], a[192:195], a[228:231], v[132:135]\n" \
"v_mfma_f32_16x16x32_bf16 v[200:203], a[192:195], a[236:239], v[200:203]\n" \
"buffer_load_dwordx4 a[92:95], v32, s[56:59], s65 offen offset:3072 sc1\n" \
"v_mfma_f32_16x16x32_bf16 v[204:207], a[192:195], a[244:247], v[204:207]\n" \
"ds_read_b64_tr_b16 a[192:193], v35 offset:52608\n" \
"ds_read_b64_tr_b16 a[194:195], v35 offset:54912\n" \
"v_mfma_f32_16x16x32_bf16 v[136:139], a[196:199], a[220:223], v[136:139]\n" \
"v_readlane_b32 s65, v11, 46\n" \
"v_mfma_f32_16x16x32_bf16 v[140:143], a[196:199], a[228:231], v[140:143]\n" \
"v_mfma_f32_16x16x32_bf16 v[208:211], a[196:199], a[236:239], v[208:211]\n" \
"v_mfma_f32_16x16x32_bf16 v[212:215], a[196:199], a[244:247], v[212:215]\n" \
"ds_read_b64_tr_b16 a[196:197], v33 offset:0\n" \
"ds_read_b64_tr_b16 a[198:199], v33 offset:4608\n" \
"v_mfma_f32_16x16x32_bf16 v[144:147], a[200:203], a[220:223], v[144:147]\n" \
"buffer_load_dwordx4 a[128:131], v20, s[56:59], s68 offen offset:0 sc1\n" \
"v_mfma_f32_16x16x32_bf16 v[148:151], a[200:203], a[228:231], v[148:151]\n" \
"v_mfma_f32_16x16x32_bf16 v[216:219], a[200:203], a[236:239], v[216:219]\n" \
"v_mfma_f32_16x16x32_bf16 v[220:223], a[200:203], a[244:247], v[220:223]\n" \
"ds_read_b64_tr_b16 a[200:201], v33 offset:32\n" \
"ds_read_b64_tr_b16 a[202:203], v33 offset:4640\n" \
"v_mfma_f32_16x16x32_bf16 v[152:155], a[204:207], a[220:223], v[152:155]\n" \
"v_mfma_f32_16x16x32_bf16 v[156:159], a[204:207], a[228:231], v[156:159]\n" \
"buffer_load_dwordx4 a[144:147], v32, s[56:59], s68 offen offset:0 sc1\n" \
"v_mfma_f32_16x16x32_bf16 v[224:227], a[204:207], a[236:239], v[224:227]\n" \
"v_mfma_f32_16x16x32_bf16 v[228:231], a[204:207], a[244:247], v[228:231]\n" \
"ds_read_b64_tr_b16 a[204:205], v33 offset:64\n" \
"ds_read_b64_tr_b16 a[206:207], v33 offset:4672\n" \
"v_mfma_f32_16x16x32_bf16 v[160:163], a[208:211], a[220:223], v[160:163]\n" \
"v_mfma_f32_16x16x32_bf16 v[164:167], a[208:211], a[228:231], v[164:167]\n" \
"v_mfma_f32_16x16x32_bf16 v[232:235], a[208:211], a[236:239], v[232:235]\n" \
"buffer_load_dwordx4 a[132:135], v20, s[56:59], s68 offen offset:1024 sc1\n" \
"v_mfma_f32_16x16x32_bf16 v[236:239], a[208:211], a[244:247], v[236:239]\n" \
"ds_read_b64_tr_b16 a[208:209], v33 offset:96\n" \
"ds_read_b64_tr_b16 a[210:211], v33 offset:4704\n" \
"v_mfma_f32_16x16x32_bf16 v[168:171], a[212:215], a[220:223], v[168:171]\n" \
"v_mfma_f32_16x16x32_bf16 v[172:175], a[212:215], a[228:231], v[172:175]\n" \
"v_mfma_f32_16x16x32_bf16 v[240:243], a[212:215], a[236:239], v[240:243]\n" \
"v_mfma_f32_16x16x32_bf16 v[244:247], a[212:215], a[244:247], v[244:247]\n" \
"ds_read_b64_tr_b16 a[212:213], v33 offset:128\n" \
"ds_read_b64_tr_b16 a[214:215], v33 offset:4736\n" \
"v_mfma_f32_16x16x32_bf16 v[176:179], a[216:219], a[220:223], v[176:179]\n" \
"buffer_load_dwordx4 a[148:151], v32, s[56:59], s68 offen offset:1024 sc1\n" \
"v_mfma_f32_16x16x32_bf16 v[180:183], a[216:219], a[228:231], v[180:183]\n" \
"v_mfma_f32_16x16x32_bf16 v[248:251], a[216:219], a[236:239], v[248:251]\n" \
"v_mfma_f32_16x16x32_bf16 v[252:255], a[216:219], a[244:247], v[252:255]\n" \
"ds_read_b64_tr_b16 a[216:217], v33 offset:160\n" \
"ds_read_b64_tr_b16 a[218:219], v33 offset:4768\n" \
"v_mfma_f32_16x16x32_bf16 v[112:115], a[0:3], a[224:227], v[112:115]\n" \
"v_mfma_f32_16x16x32_bf16 v[116:119], a[0:3], a[232:235], v[116:119]\n" \
"buffer_load_dwordx4 a[136:139], v20, s[56:59], s68 offen offset:2048 sc1\n" \
"v_mfma_f32_16x16x32_bf16 v[184:187], a[0:3], a[240:243], v[184:187]\n" \
"v_mfma_f32_16x16x32_bf16 v[188:191], a[0:3], a[248:251], v[188:191]\n" \
"ds_read_b64_tr_b16 a[0:1], v33 offset:192\n" \
"ds_read_b64_tr_b16 a[2:3], v33 offset:4800\n" \
"v_mfma_f32_16x16x32_bf16 v[120:123], a[4:7], a[224:227], v[120:123]\n" \
"v_mfma_f32_16x16x32_bf16 v[124:127], a[4:7], a[232:235], v[124:127]\n" \
"v_mfma_f32_16x16x32_bf16 v[192:195], a[4:7], a[240:243], v[192:195]\n" \
"buffer_load_dwordx4 a[152:155], v32, s[56:59], s68 offen offset:2048 sc1\n" \
"v_mfma_f32_16x16x32_bf16 v[196:199], a[4:7], a[248:251], v[196:199]\n" \
"ds_read_b64_tr_b16 a[4:5], v33 offset:224\n" \
"ds_read_b64_tr_b16 a[6:7], v33 offset:4832\n" \
"v_mfma_f32_16x16x32_bf16 v[128:131], a[8:11], a[224:227], v[128:131]\n" \
"v_mfma_f32_16x16x32_bf16 v[132:135], a[8:11], a[232:235], v[132:135]\n" \
"v_mfma_f32_16x16x32_bf16 v[200:203], a[8:11], a[240:243], v[200:203]\n" \
"v_mfma_f32_16x16x32_bf16 v[204:207], a[8:11], a[248:251], v[204:207]\n" \
"ds_read_b64_tr_b16 a[8:9], v33 offset:256\n" \
"ds_read_b64_tr_b16 a[10:11], v33 offset:4864\n" \
"v_mfma_f32_16x16x32_bf16 v[136:139], a[12:15], a[224:227], v[136:139]\n" \
"buffer_load_dwordx4 a[140:143], v20, s[56:59], s68 offen offset:3072 sc1\n" \
"v_mfma_f32_16x16x32_bf16 v[140:143], a[12:15], a[232:235], v[140:143]\n" \
"v_mfma_f32_16x16x32_bf16 v[208:211], a[12:15], a[240:243], v[208:211]\n" \
"v_mfma_f32_16x16x32_bf16 v[212:215], a[12:15], a[248:251], v[212:215]\n" \
"ds_read_b64_tr_b16 a[12:13], v33 offset:9216\n" \
"s_waitcnt lgkmcnt(7)\n" \
"v_mfma_f32_16x16x32_bf16 v[144:147], a[16:19], a[224:227], v[144:147]\n" \
"ds_read_b64_tr_b16 a[14:15], v33 offset:13824\n" \
"v_mfma_f32_16x16x32_bf16 v[148:151], a[16:19], a[232:235], v[148:151]\n" \
"buffer_load_dwordx4 a[156:159], v32, s[56:59], s68 offen offset:3072 sc1\n" \
"v_mfma_f32_16x16x32_bf16 v[216:219], a[16:19], a[240:243], v[216:219]\n" \
"v_readlane_b32 s68, v11, 49\n" \
"v_mfma_f32_16x16x32_bf16 v[220:223], a[16:19], a[248:251], v[220:223]\n" \
"ds_read_b64_tr_b16 a[16:17], v33 offset:9248\n" \
"ds_read_b64_tr_b16 a[18:19], v33 offset:13856\n" \
"v_mfma_f32_16x16x32_bf16 v[152:155], a[20:23], a[224:227], v[152:155]\n" \
"v_mfma_f32_16x16x32_bf16 v[156:159], a[20:23], a[232:235], v[156:159]\n" \
"v_mfma_f32_16x16x32_bf16 v[224:227], a[20:23], a[240:243], v[224:227]\n" \
"buffer_load_dwordx4 a[32:35], v20, s[56:59], s69 offen offset:0 sc1\n" \
"v_mfma_f32_16x16x32_bf16 v[228:231], a[20:23], a[248:251], v[228:231]\n" \
"ds_read_b64_tr_b16 a[20:21], v33 offset:9280\n" \
"ds_read_b64_tr_b16 a[22:23], v33 offset:13888\n" \
"v_mfma_f32_16x16x32_bf16 v[160:163], a[24:27], a[224:227], v[160:163]\n" \
"v_mfma_f32_16x16x32_bf16 v[164:167], a[24:27], a[232:235], v[164:167]\n" \
"v_mfma_f32_16x16x32_bf16 v[232:235], a[24:27], a[240:243], v[232:235]\n" \
"s_waitcnt vmcnt(10)\n" \
"v_cmp_ne_u32_e32 vcc, s62, v47\n" \
"v_mfma_f32_16x16x32_bf16 v[236:239], a[24:27], a[248:251], v[236:239]\n" \
"ds_read_b64_tr_b16 a[24:25], v33 offset:9312\n" \
"ds_read_b64_tr_b16 a[26:27], v33 offset:13920\n" \
"v_mfma_f32_16x16x32_bf16 v[168:171], a[28:31], a[224:227], v[168:171]\n" \
"buffer_load_dwordx4 a[48:51], v32, s[56:59], s69 offen offset:0 sc1\n" \
"v_mfma_f32_16x16x32_bf16 v[172:175], a[28:31], a[232:235], v[172:175]\n" \
"s_cbranch_vccnz L_sb_%=\n" \
"L_rb_%=:\n" \
"v_readlane_b32 s71, v11, 52\n" \
"v_mfma_f32_16x16x32_bf16 v[240:243], a[28:31], a[240:243], v[240:243]\n" \
"v_cmp_eq_u32_e32 vcc, 0, v44\n" \
"v_mfma_f32_16x16x32_bf16 v[244:247], a[28:31], a[248:251], v[244:247]\n" \
"ds_read_b64_tr_b16 a[28:29], v33 offset:9344\n" \
"ds_read_b64_tr_b16 a[30:31], v33 offset:13952\n" \
"v_mfma_f32_16x16x32_bf16 v[176:179], a[192:195], a[224:227], v[176:179]\n" \
"v_cndmask_b32_e32 v50, 0, v45, vcc\n" \
"v_cmp_eq_u32_e32 vcc, 1, v44\n" \
"v_mfma_f32_16x16x32_bf16 v[180:183], a[192:195], a[232:235], v[180:183]\n" \
"buffer_load_dwordx4 a[36:39], v20, s[56:59], s69 offen offset:1024 sc1\n" \
"v_mfma_f32_16x16x32_bf16 v[248:251], a[192:195], a[240:243], v[248:251]\n" \
"v_cndmask_b32_e32 v51, 0, v45, vcc\n" \
"v_bfe_u32 v45, v20, 1, 5\n" \
"v_mfma_f32_16x16x32_bf16 v[252:255], a[192:195], a[248:251], v[252:255]\n" \
"ds_read_b64_tr_b16 a[192:193], v33 offset:9376\n" \
"ds_read_b64_tr_b16 a[194:195], v33 offset:13984\n" \
"v_mfma_f32_16x16x32_bf16 v[112:115], a[196:199], a[64:67], v[112:115]\n" \
"v_and_b32_e32 v45, 24, v45\n" \
"v_and_b32_e32 v44, 0x3c0, v20\n" \
"v_mfma_f32_16x16x32_bf16 v[116:119], a[196:199], a[80:83], v[116:119]\n" \
"ds_read_b64_tr_b16 a[196:197], v33 offset:9408\n" \
"ds_read_b64_tr_b16 a[198:199], v33 offset:14016\n" \
"v_mfma_f32_16x16x32_bf16 v[120:123], a[200:203], a[64:67], v[120:123]\n" \
"buffer_load_dwordx4 a[52:55], v32, s[56:59], s69 offen offset:1024 sc1\n" \
"v_mfma_f32_16x16x32_bf16 v[124:127], a[200:203], a[80:83], v[124:127]\n" \
"ds_read_b64_tr_b16 a[200:201], v33 offset:9440\n" \
"ds_read_b64_tr_b16 a[202:203], v33 offset:14048\n" \
"v_mfma_f32_16x16x32_bf16 v[128:131], a[204:207], a[64:67], v[128:131]\n" \
"v_add_u32_e32 v45, 0x4800, v45\n" \
"v_add_u32_e32 v36, v44, v45\n" \
"v_mfma_f32_16x16x32_bf16 v[132:135], a[204:207], a[80:83], v[132:135]\n" \
"ds_read_b64_tr_b16 a[204:205], v33 offset:9472\n" \
"ds_read_b64_tr_b16 a[206:207], v33 offset:14080\n" \
"v_mfma_f32_16x16x32_bf16 v[136:139], a[208:211], a[64:67], v[136:139]\n" \
"v_mfma_f32_16x16x32_bf16 v[140:143], a[208:211], a[80:83], v[140:143]\n" \
"buffer_load_dwordx4 a[40:43], v20, s[56:59], s69 offen offset:2048 sc1\n" \
"v_mfma_f32_16x16x32_bf16 v[144:147], a[212:215], a[64:67], v[144:147]\n" \
"ds_read_b64_tr_b16 a[208:209], v33 offset:18432\n" \
"ds_read_b64_tr_b16 a[210:211], v33 offset:23040\n" \
"v_mfma_f32_16x16x32_bf16 v[148:151], a[212:215], a[80:83], v[148:151]\n" \
"ds_read_b64_tr_b16 a[212:213], v33 offset:18464\n" \
"ds_read_b64_tr_b16 a[214:215], v33 offset:23072\n" \
".fill (L_ab + 36864 - .) / 4, 4, 0xBF800000\n" \
"L_ap9:\n" \
"s_cbranch_execz L_ap0\n" \
"v_mfma_f32_16x16x32_bf16 v[152:155], a[216:219], a[64:67], v[152:155]\n" \
"v_mfma_f32_16x16x32_bf16 v[156:159], a[216:219], a[80:83], v[156:159]\n" \
"ds_read_b64_tr_b16 a[216:217], v33 offset:18496\n" \
"s_waitcnt lgkmcnt(13)\n" \
"v_mfma_f32_16x16x32_bf16 v[160:163], a[0:3], a[64:67], v[160:163]\n" \
"buffer_load_dwordx4 a[56:59], v32, s[56:59], s69 offen offset:2048 sc1\n" \
"v_mfma_f32_16x16x32_bf16 v[164:167], a[0:3], a[80:83], v[164:167]\n" \
"ds_read_b64_tr_b16 a[218:219], v33 offset:23104\n" \
"ds_read_b64_tr_b16 a[0:1], v33 offset:18528\n" \
"v_mfma_f32_16x16x32_bf16 v[168:171], a[4:7], a[64:67], v[168:171]\n" \
"ds_read_b64_tr_b16 a[2:3], v33 offset:23136\n" \
"v_mfma_f32_16x16x32_bf16 v[172:175], a[4:7], a[80:83], v[172:175]\n" \
"ds_read_b64_tr_b16 a[4:5], v33 offset:18560\n" \
"ds_read_b64_tr_b16 a[6:7], v33 offset:23168\n" \
"v_mfma_f32_16x16x32_bf16 v[176:179], a[8:11], a[64:67], v[176:179]\n" \
"v_mfma_f32_16x16x32_bf16 v[180:183], a[8:11], a[80:83], v[180:183]\n" \
"buffer_load_dwordx4 a[44:47], v20, s[56:59], s69 offen offset:3072 sc1\n" \
"v_mfma_f32_16x16x32_bf16 v[112:115], a[12:15], a[68:71], v[112:115]\n" \
"ds_read_b64_tr_b16 a[8:9], v33 offset:18592\n" \
"ds_read_b64_tr_b16 a[10:11], v33 offset:23200\n" \
"v_mfma_f32_16x16x32_bf16 v[116:119], a[12:15], a[84:87], v[116:119]\n" \
"ds_read_b64_tr_b16 a[12:13], v33 offset:18624\n" \
"ds_read_b64_tr_b16 a[14:15], v33 offset:23232\n" \
"v_mfma_f32_16x16x32_bf16 v[120:123], a[16:19], a[68:71], v[120:123]\n" \
"v_mfma_f32_16x16x32_bf16 v[124:127], a[16:19], a[84:87], v[124:127]\n" \
"ds_read_b64_tr_b16 a[16:17], v33 offset:18656\n" \
"ds_read_b64_tr_b16 a[18:19], v33 offset:23264\n" \
"buffer_load_dwordx4 a[60:63], v32, s[56:59], s69 offen offset:3072 sc1\n" \
"v_mfma_f32_16x16x32_bf16 v[128:131], a[20:23], a[68:71], v[128:131]\n" \
"v_readlane_b32 s69, v11, 50\n" \
"v_mfma_f32_16x16x32_bf16 v[132:135], a[20:23], a[84:87], v[132:135]\n" \
"ds_read_b64_tr_b16 a[20:21], v33 offset:18688\n" \
"ds_read_b64_tr_b16 a[22:23], v33 offset:23296\n" \
"v_mfma_f32_16x16x32_bf16 v[136:139], a[24:27], a[68:71], v[136:139]\n" \
"v_mfma_f32_16x16x32_bf16 v[140:143], a[24:27], a[84:87], v[140:143]\n" \
"ds_read_b64_tr_b16 a[24:25], v33 offset:27648\n" \
"ds_read_b64_tr_b16 a[26:27], v33 offset:32256\n" \
"v_mfma_f32_16x16x32_bf16 v[144:147], a[28:31], a[68:71], v[144:147]\n" \
"buffer_load_dwordx4 a[220:223], v20, s[56:59], s64 offen offset:0 sc1\n" \
"v_mfma_f32_16x16x32_bf16 v[148:151], a[28:31], a[84:87], v[148:151]\n" \
"ds_read_b64_tr_b16 a[28:29], v33 offset:27680\n" \
"s_waitcnt lgkmcnt(14)\n" \
"v_mfma_f32_16x16x32_bf16 v[152:155], a[192:195], a[68:71], v[152:155]\n" \
"ds_read_b64_tr_b16 a[30:31], v33 offset:32288\n" \
"v_mfma_f32_16x16x32_bf16 v[156:159], a[192:195], a[84:87], v[156:159]\n" \
"ds_read_b64_tr_b16 a[192:193], v33 offset:27712\n" \
"ds_read_b64_tr_b16 a[194:195], v33 offset:32320\n" \
"v_mfma_f32_16x16x32_bf16 v[160:163], a[196:199], a[68:71], v[160:163]\n" \
"v_mfma_f32_16x16x32_bf16 v[164:167], a[196:199], a[84:87], v[164:167]\n" \
"ds_read_b64_tr_b16 a[196:197], v33 offset:27744\n" \
"ds_read_b64_tr_b16 a[198:199], v33 offset:32352\n" \
"v_mfma_f32_16x16x32_bf16 v[168:171], a[200:203], a[68:71], v[168:171]\n" \
"buffer_load_dwordx4 a[236:239], v32, s[56:59], s64 offen offset:0 sc1\n" \
"v_mfma_f32_16x16x32_bf16 v[172:175], a[200:203], a[84:87], v[172:175]\n" \
"ds_read_b64_tr_b16 a[200:201], v33 offset:27776\n" \
"ds_read_b64_tr_b16 a[202:203], v33 offset:32384\n" \
"v_mfma_f32_16x16x32_bf16 v[176:179], a[204:207], a[68:71], v[176:179]\n" \
"v_mfma_f32_16x16x32_bf16 v[180:183], a[204:207], a[84:87], v[180:183]\n" \
"ds_read_b64_tr_b16 a[204:205], v33 offset:27808\n" \
"ds_read_b64_tr_b16 a[206:207], v33 offset:32416\n" \
"v_mfma_f32_16x16x32_bf16 v[112:115], a[208:211], a[72:75], v[112:115]\n" \
"v_mfma_f32_16x16x32_bf16 v[116:119], a[208:211], a[88:91], v[116:119]\n" \
"ds_read_b64_tr_b16 a[208:209], v33 offset:27840\n" \
"ds_read_b64_tr_b16 a[210:211], v33 offset:32448\n" \
"v_mfma_f32_16x16x32_bf16 v[120:123], a[212:215], a[72:75], v[120:123]\n" \
"buffer_load_dwordx4 a[224:227], v20, s[56:59], s64 offen offset:1024 sc1\n" \
"v_mfma_f32_16x16x32_bf16 v[124:127], a[212:215], a[88:91], v[124:127]\n" \
"ds_read_b64_tr_b16 a[212:213], v33 offset:27872\n" \
"ds_read_b64_tr_b16 a[214:215], v33 offset:32480\n" \
"v_mfma_f32_16x16x32_bf16 v[128:131], a[216:219], a[72:75], v[128:131]\n" \
"v_mfma_f32_16x16x32_bf16 v[132:135], a[216:219], a[88:91], v[132:135]\n" \
"ds_read_b64_tr_b16 a[216:217], v33 offset:27904\n" \
"s_waitcnt lgkmcnt(14)\n" \
"v_mfma_f32_16x16x32_bf16 v[136:139], a[0:3], a[72:75], v[136:139]\n" \
"ds_read_b64_tr_b16 a[218:219], v33 offset:32512\n" \
"v_mfma_f32_16x16x32_bf16 v[140:143], a[0:3], a[88:91], v[140:143]\n" \
"buffer_load_dwordx4 a[240:243], v32, s[56:59], s64 offen offset:1024 sc1\n" \
"v_mfma_f32_16x16x32_bf16 v[144:147], a[4:7], a[72:75], v[144:147]\n" \
"v_mfma_f32_16x16x32_bf16 v[148:151], a[4:7], a[88:91], v[148:151]\n" \
"v_mfma_f32_16x16x32_bf16 v[152:155], a[8:11], a[72:75], v[152:155]\n" \
"v_mfma_f32_16x16x32_bf16 v[156:159], a[8:11], a[88:91], v[156:159]\n" \
"v_mfma_f32_16x16x32_bf16 v[160:163], a[12:15], a[72:75], v[160:163]\n" \
"buffer_load_dwordx4 a[228:231], v20, s[56:59], s64 offen offset:2048 sc1\n" \
"v_mfma_f32_16x16x32_bf16 v[164:167], a[12:15], a[88:91], v[164:167]\n" \
"v_mfma_f32_16x16x32_bf16 v[168:171], a[16:19], a[72:75], v[168:171]\n" \
"v_mfma_f32_16x16x32_bf16 v[172:175], a[16:19], a[88:91], v[172:175]\n" \
"v_mfma_f32_16x16x32_bf16 v[176:179], a[20:23], a[72:75], v[176:179]\n" \
"v_mfma_f32_16x16x32_bf16 v[180:183], a[20:23], a[88:91], v[180:183]\n" \
"buffer_load_dwordx4 a[244:247], v32, s[56:59], s64 offen offset:2048 sc1\n" \
"s_barrier\n" \
"v_mfma_f32_16x16x32_bf16 v[112:115], a[24:27], a[76:79], v[112:115]\n" \
"ds_read_b64_tr_b16 a[0:1], v33 offset:36864\n" \
"s_waitcnt vmcnt(19)\n" \
"v_mfma_f32_16x16x32_bf16 v[116:119], a[24:27], a[92:95], v[116:119]\n" \
"ds_read_b64_tr_b16 a[2:3], v33 offset:41472\n" \
"s_waitcnt lgkmcnt(2)\n" \
"v_mfma_f32_16x16x32_bf16 v[120:123], a[28:31], a[76:79], v[120:123]\n" \
"ds_read_b64_tr_b16 a[4:5], v33 offset:46080\n" \
"ds_read_b64_tr_b16 a[6:7], v33 offset:50688\n" \
"v_mfma_f32_16x16x32_bf16 v[124:127], a[28:31], a[92:95], v[124:127]\n" \
"ds_read_b64_tr_b16 a[8:9], v33 offset:55296\n" \
"ds_read_b64_tr_b16 a[10:11], v33 offset:59904\n" \
"v_mfma_f32_16x16x32_bf16 v[128:131], a[192:195], a[76:79], v[128:131]\n" \
"ds_read_b64_tr_b16 a[12:13], v10 offset:0\n" \
"ds_read_b64_tr_b16 a[14:15], v10 offset:4608\n" \
"v_mfma_f32_16x16x32_bf16 v[132:135], a[192:195], a[92:95], v[132:135]\n" \
"ds_read_b64_tr_b16 a[16:17], v34 offset:0\n" \
"ds_read_b64_tr_b16 a[18:19], v34 offset:4608\n" \
"v_mfma_f32_16x16x32_bf16 v[136:139], a[196:199], a[76:79], v[136:139]\n" \
"ds_read_b64_tr_b16 a[20:21], v34 offset:9216\n" \
"ds_read_b64_tr_b16 a[22:23], v34 offset:13824\n" \
"v_mfma_f32_16x16x32_bf16 v[140:143], a[196:199], a[92:95], v[140:143]\n" \
"ds_read_b64_tr_b16 a[24:25], v34 offset:18432\n" \
"ds_read_b64_tr_b16 a[26:27], v34 offset:23040\n" \
"v_mfma_f32_16x16x32_bf16 v[144:147], a[200:203], a[76:79], v[144:147]\n" \
"ds_read_b64_tr_b16 a[28:29], v10 offset:9216\n" \
"ds_read_b64_tr_b16 a[30:31], v10 offset:13824\n" \
"v_mfma_f32_16x16x32_bf16 v[148:151], a[200:203], a[92:95], v[148:151]\n" \
"ds_read_b64_tr_b16 a[192:193], v33 offset:36896\n" \
"ds_read_b64_tr_b16 a[194:195], v33 offset:41504\n" \
"v_mfma_f32_16x16x32_bf16 v[152:155], a[204:207], a[76:79], v[152:155]\n" \
"ds_read_b64_tr_b16 a[196:197], v33 offset:46112\n" \
"ds_read_b64_tr_b16 a[198:199], v33 offset:50720\n" \
"v_mfma_f32_16x16x32_bf16 v[156:159], a[204:207], a[92:95], v[156:159]\n" \
"ds_read_b64_tr_b16 a[200:201], v33 offset:55328\n" \
"ds_read_b64_tr_b16 a[202:203], v33 offset:59936\n" \
"v_mfma_f32_16x16x32_bf16 v[160:163], a[208:211], a[76:79], v[160:163]\n" \
"ds_read_b64_tr_b16 a[204:205], v10 offset:32\n" \
"ds_read_b64_tr_b16 a[206:207], v10 offset:4640\n" \
"v_mfma_f32_16x16x32_bf16 v[164:167], a[208:211], a[92:95], v[164:167]\n" \
"ds_read_b64_tr_b16 a[208:209], v34 offset:32\n" \
"ds_read_b64_tr_b16 a[210:211], v34 offset:4640\n" \
"v_mfma_f32_16x16x32_bf16 v[168:171], a[212:215], a[76:79], v[168:171]\n" \
"buffer_load_dwordx4 a[232:235], v20, s[56:59], s64 offen offset:3072 sc1\n" \
"v_mfma_f32_16x16x32_bf16 v[172:175], a[212:215], a[92:95], v[172:175]\n" \
"ds_read_b64_tr_b16 a[212:213], v34 offset:9248\n" \
"ds_read_b64_tr_b16 a[214:215], v34 offset:13856\n" \
"v_mfma_f32_16x16x32_bf16 v[176:179], a[216:219], a[76:79], v[176:179]\n" \
"v_mfma_f32_16x16x32_bf16 v[180:183], a[216:219], a[92:95], v[180:183]\n" \
"ds_read_b64_tr_b16 a[216:217], v34 offset:18464\n" \
"s_waitcnt lgkmcnt(15)\n" \
"v_mfma_f32_16x16x32_bf16 v[112:115], a[0:3], a[128:131], v[112:115]\n" \
"buffer_load_dwordx4 a[248:251], v32, s[56:59], s64 offen offset:3072 sc1\n" \
"v_mfma_f32_16x16x32_bf16 v[116:119], a[0:3], a[144:147], v[116:119]\n" \
"ds_read_b64_tr_b16 a[218:219], v34 offset:23072\n" \
"ds_read_b64_tr_b16 a[0:1], v10 offset:9248\n" \
"v_mfma_f32_16x16x32_bf16 v[112:115], a[4:7], a[132:135], v[112:115]\n" \
"ds_read_b64_tr_b16 a[2:3], v10 offset:13856\n" \
"s_waitcnt vmcnt(18)\n" \
"v_mfma_f32_16x16x32_bf16 v[116:119], a[4:7], a[148:151], v[116:119]\n" \
"ds_read_b64_tr_b16 a[4:5], v33 offset:36928\n" \
"ds_read_b64_tr_b16 a[6:7], v33 offset:41536\n" \
"v_mfma_f32_16x16x32_bf16 v[112:115], a[8:11], a[136:139], v[112:115]\n" \
"v_mfma_f32_16x16x32_bf16 v[116:119], a[8:11], a[152:155], v[116:119]\n" \
"ds_read_b64_tr_b16 a[8:9], v33 offset:46144\n" \
"s_waitcnt vmcnt(17)\n" \
"v_mfma_f32_16x16x32_bf16 v[112:115], a[12:15], a[140:143], v[112:115]\n" \
"ds_read_b64_tr_b16 a[10:11], v33 offset:50752\n" \
"s_waitcnt vmcnt(16)\n" \
"v_mfma_f32_16x16x32_bf16 v[116:119], a[12:15], a[156:159], v[116:119]\n" \
"ds_read_b64_tr_b16 a[12:13], v33 offset:55360\n" \
"s_waitcnt vmcnt(15)\n" \
"v_mfma_f32_16x16x32_bf16 v[112:115], a[16:19], a[32:35], v[112:115]\n" \
"ds_read_b64_tr_b16 a[14:15], v33 offset:59968\n" \
"s_waitcnt vmcnt(14)\n" \
"v_mfma_f32_16x16x32_bf16 v[116:119], a[16:19], a[48:51], v[116:119]\n" \
"ds_read_b64_tr_b16 a[16:17], v10 offset:64\n" \
"s_waitcnt vmcnt(13)\n" \
"v_mfma_f32_16x16x32_bf16 v[112:115], a[20:23], a[36:39], v[112:115]\n" \
"ds_read_b64_tr_b16 a[18:19], v10 offset:4672\n" \
"s_waitcnt vmcnt(12)\n" \
"v_mfma_f32_16x16x32_bf16 v[116:119], a[20:23], a[52:55], v[116:119]\n" \
"ds_read_b64_tr_b16 a[20:21], v34 offset:64\n" \
"s_waitcnt vmcnt(11)\n" \
"v_mfma_f32_16x16x32_bf16 v[112:115], a[24:27], a[40:43], v[112:115]\n" \
"ds_read_b64_tr_b16 a[22:23], v34 offset:4672\n" \
"s_waitcnt vmcnt(10)\n" \
"v_mfma_f32_16x16x32_bf16 v[116:119], a[24:27], a[56:59], v[116:119]\n" \
"ds_read_b64_tr_b16 a[24:25], v34 offset:9280\n" \
"s_waitcnt vmcnt(9) lgkmcnt(14)\n" \
"v_mfma_f32_16x16x32_bf16 v[112:115], a[28:31], a[44:47], v[112:115]\n" \
"ds_read_b64_tr_b16 a[26:27], v34 offset:13888\n" \
"buffer_load_dwordx4 a[160:163], v20, s[56:59], s72 offen offset:0 sc1\n" \
"s_waitcnt vmcnt(9)\n" \
"v_mfma_f32_16x16x32_bf16 v[116:119], a[28:31], a[60:63], v[116:119]\n" \
"ds_read_b64_tr_b16 a[28:29], v34 offset:18496\n" \
"ds_read_b64_tr_b16 a[30:31], v34 offset:23104\n" \
"v_mfma_f32_16x16x32_bf16 v[120:123], a[192:195], a[128:131], v[120:123]\n" \
"buffer_load_dwordx4 a[176:179], v32, s[56:59], s72 offen offset:0 sc1\n" \
"v_cvt_pk_bf16_f32 v112, v112, v113\n" \
"v_cvt_pk_bf16_f32 v113, v114, v115\n" \
"v_mfma_f32_16x16x32_bf16 v[124:127], a[192:195], a[144:147], v[124:127]\n" \
"ds_read_b64_tr_b16 a[192:193], v10 offset:9280\n" \
"v_cvt_pk_bf16_f32 v114, v116, v117\n" \
"v_cvt_pk_bf16_f32 v115, v118, v119\n" \
"v_mfma_f32_16x16x32_bf16 v[120:123], a[196:199], a[132:135], v[120:123]\n" \
"buffer_store_dwordx4 v[112:115], v20, s[56:59], s65 offen offset:0 sc1\n" \
"s_add_u32 s65, s65, 0x3800\n" \
"v_mfma_f32_16x16x32_bf16 v[124:127], a[196:199], a[148:151], v[124:127]\n" \
"ds_read_b64_tr_b16 a[194:195], v10 offset:13888\n" \
"ds_read_b64_tr_b16 a[196:197], v33 offset:36960\n" \
"v_mfma_f32_16x16x32_bf16 v[120:123], a[200:203], a[136:139], v[120:123]\n" \
"ds_read_b64_tr_b16 a[198:199], v33 offset:41568\n" \
"v_mfma_f32_16x16x32_bf16 v[124:127], a[200:203], a[152:155], v[124:127]\n" \
"ds_read_b64_tr_b16 a[200:201], v33 offset:46176\n" \
"ds_read_b64_tr_b16 a[202:203], v33 offset:50784\n" \
"v_mfma_f32_16x16x32_bf16 v[120:123], a[204:207], a[140:143], v[120:123]\n" \
"buffer_load_dwordx4 a[164:167], v20, s[56:59], s72 offen offset:1024 sc1\n" \
"v_mfma_f32_16x16x32_bf16 v[124:127], a[204:207], a[156:159], v[124:127]\n" \
"ds_read_b64_tr_b16 a[204:205], v33 offset:55392\n" \
"ds_read_b64_tr_b16 a[206:207], v33 offset:60000\n" \
"v_mfma_f32_16x16x32_bf16 v[120:123], a[208:211], a[32:35], v[120:123]\n" \
"v_mfma_f32_16x16x32_bf16 v[124:127], a[208:211], a[48:51], v[124:127]\n" \
"ds_read_b64_tr_b16 a[208:209], v10 offset:96\n" \
"ds_read_b64_tr_b16 a[210:211], v10 offset:4704\n" \
"v_mfma_f32_16x16x32_bf16 v[120:123], a[212:215], a[36:39], v[120:123]\n" \
"v_mfma_f32_16x16x32_bf16 v[124:127], a[212:215], a[52:55], v[124:127]\n" \
"ds_read_b64_tr_b16 a[212:213], v34 offset:96\n" \
"s_waitcnt lgkmcnt(13)\n" \
"v_mfma_f32_16x16x32_bf16 v[120:123], a[216:219], a[40:43], v[120:123]\n" \
"buffer_load_dwordx4 a[180:183], v32, s[56:59], s72 offen offset:1024 sc1\n" \
"v_mfma_f32_16x16x32_bf16 v[124:127], a[216:219], a[56:59], v[124:127]\n" \
"ds_read_b64_tr_b16 a[214:215], v34 offset:4704\n" \
"ds_read_b64_tr_b16 a[216:217], v34 offset:9312\n" \
"v_mfma_f32_16x16x32_bf16 v[120:123], a[0:3], a[44:47], v[120:123]\n" \
"ds_read_b64_tr_b16 a[218:219], v34 offset:13920\n" \
"v_mfma_f32_16x16x32_bf16 v[124:127], a[0:3], a[60:63], v[124:127]\n" \
"ds_read_b64_tr_b16 a[0:1], v34 offset:18528\n" \
"ds_read_b64_tr_b16 a[2:3], v34 offset:23136\n" \
"v_mfma_f32_16x16x32_bf16 v[128:131], a[4:7], a[128:131], v[128:131]\n" \
"v_mfma_f32_16x16x32_bf16 v[132:135], a[4:7], a[144:147], v[132:135]\n" \
"ds_read_b64_tr_b16 a[4:5], v10 offset:9312\n" \
"ds_read_b64_tr_b16 a[6:7], v10 offset:13920\n" \
"v_cvt_pk_bf16_f32 v120, v120, v121\n" \
"v_cvt_pk_bf16_f32 v121, v122, v123\n" \
"v_cvt_pk_bf16_f32 v122, v124, v125\n" \
"v_cvt_pk_bf16_f32 v123, v126, v127\n" \
"buffer_store_dwordx4 v[120:123], v20, s[56:59], s66 offen offset:0 sc1\n" \
"s_add_u32 s66, s66, 0x3000\n" \
"v_mfma_f32_16x16x32_bf16 v[128:131], a[8:11], a[132:135], v[128:131]\n" \
"v_mfma_f32_16x16x32_bf16 v[132:135], a[8:11], a[148:151], v[132:135]\n" \
"ds_read_b64_tr_b16 a[8:9], v33 offset:36992\n" \
"ds_read_b64_tr_b16 a[10:11], v33 offset:41600\n" \
"v_mfma_f32_16x16x32_bf16 v[128:131], a[12:15], a[136:139], v[128:131]\n" \
"v_mfma_f32_16x16x32_bf16 v[132:135], a[12:15], a[152:155], v[132:135]\n" \
"ds_read_b64_tr_b16 a[12:13], v33 offset:46208\n" \
"ds_read_b64_tr_b16 a[14:15], v33 offset:50816\n" \
"v_mfma_f32_16x16x32_bf16 v[128:131], a[16:19], a[140:143], v[128:131]\n" \
"buffer_load_dwordx4 a[168:171], v20, s[56:59], s72 offen offset:2048 sc1\n" \
"v_mfma_f32_16x16x32_bf16 v[132:135], a[16:19], a[156:159], v[132:135]\n" \
"ds_read_b64_tr_b16 a[16:17], v33 offset:55424\n" \
"ds_read_b64_tr_b16 a[18:19], v33 offset:60032\n" \
"v_mfma_f32_16x16x32_bf16 v[128:131], a[20:23], a[32:35], v[128:131]\n" \
"v_mfma_f32_16x16x32_bf16 v[132:135], a[20:23], a[48:51], v[132:135]\n" \
"ds_read_b64_tr_b16 a[20:21], v10 offset:128\n" \
"ds_read_b64_tr_b16 a[22:23], v10 offset:4736\n" \
"v_mfma_f32_16x16x32_bf16 v[128:131], a[24:27], a[36:39], v[128:131]\n" \
"v_mfma_f32_16x16x32_bf16 v[132:135], a[24:27], a[52:55], v[132:135]\n" \
"ds_read_b64_tr_b16 a[24:25], v34 offset:128\n" \
"s_waitcnt lgkmcnt(12)\n" \
"v_mfma_f32_16x16x32_bf16 v[128:131], a[28:31], a[40:43], v[128:131]\n" \
"buffer_load_dwordx4 a[184:187], v32, s[56:59], s72 offen offset:2048 sc1\n" \
"v_mfma_f32_16x16x32_bf16 v[132:135], a[28:31], a[56:59], v[132:135]\n" \
"ds_read_b64_tr_b16 a[26:27], v34 offset:4736\n" \
"ds_read_b64_tr_b16 a[28:29], v34 offset:9344\n" \
"v_mfma_f32_16x16x32_bf16 v[128:131], a[192:195], a[44:47], v[128:131]\n" \
"ds_read_b64_tr_b16 a[30:31], v34 offset:13952\n" \
"v_mfma_f32_16x16x32_bf16 v[132:135], a[192:195], a[60:63], v[132:135]\n" \
"ds_read_b64_tr_b16 a[192:193], v34 offset:18560\n" \
"ds_read_b64_tr_b16 a[194:195], v34 offset:23168\n" \
"v_mfma_f32_16x16x32_bf16 v[136:139], a[196:199], a[128:131], v[136:139]\n" \
"v_mfma_f32_16x16x32_bf16 v[140:143], a[196:199], a[144:147], v[140:143]\n" \
"buffer_load_dwordx4 a[172:175], v20, s[56:59], s72 offen offset:3072 sc1\n" \
"v_mfma_f32_16x16x32_bf16 v[136:139], a[200:203], a[132:135], v[136:139]\n" \
"v_cvt_pk_bf16_f32 v128, v128, v129\n" \
"v_cvt_pk_bf16_f32 v129, v130, v131\n" \
"v_cvt_pk_bf16_f32 v130, v132, v133\n" \
"v_cvt_pk_bf16_f32 v131, v134, v135\n" \
"v_mfma_f32_16x16x32_bf16 v[140:143], a[200:203], a[148:151], v[140:143]\n" \
"buffer_store_dwordx4 v[128:131], v20, s[56:59], s67 offen offset:0 sc1\n" \
"s_add_u32 s67, s67, 0x2800\n" \
"v_mfma_f32_16x16x32_bf16 v[136:139], a[204:207], a[136:139], v[136:139]\n" \
"ds_read_b64_tr_b16 a[196:197], v10 offset:9344\n" \
"ds_read_b64_tr_b16 a[198:199], v10 offset:13952\n" \
"v_mfma_f32_16x16x32_bf16 v[140:143], a[204:207], a[152:155], v[140:143]\n" \
"ds_read_b64_tr_b16 a[200:201], v33 offset:37024\n" \
"ds_read_b64_tr_b16 a[202:203], v33 offset:41632\n" \
"v_mfma_f32_16x16x32_bf16 v[136:139], a[208:211], a[140:143], v[136:139]\n" \
"ds_read_b64_tr_b16 a[204:205], v33 offset:46240\n" \
"ds_read_b64_tr_b16 a[206:207], v33 offset:50848\n" \
"v_mfma_f32_16x16x32_bf16 v[140:143], a[208:211], a[156:159], v[140:143]\n" \
"buffer_load_dwordx4 a[188:191], v32, s[56:59], s72 offen offset:3072 sc1\n" \
"v_mfma_f32_16x16x32_bf16 v[136:139], a[212:215], a[32:35], v[136:139]\n" \
"v_readlane_b32 s72, v11, 53\n" \
"ds_read_b64_tr_b16 a[208:209], v33 offset:55456\n" \
"v_mfma_f32_16x16x32_bf16 v[140:143], a[212:215], a[48:51], v[140:143]\n" \
"ds_read_b64_tr_b16 a[210:211], v33 offset:60064\n" \
"ds_read_b64_tr_b16 a[212:213], v10 offset:160\n" \
"v_mfma_f32_16x16x32_bf16 v[136:139], a[216:219], a[36:39], v[136:139]\n" \
"ds_read_b64_tr_b16 a[214:215], v10 offset:4768\n" \
"v_mfma_f32_16x16x32_bf16 v[140:143], a[216:219], a[52:55], v[140:143]\n" \
"ds_read_b64_tr_b16 a[216:217], v34 offset:160\n" \
"s_waitcnt lgkmcnt(13)\n" \
"v_mfma_f32_16x16x32_bf16 v[136:139], a[0:3], a[40:43], v[136:139]\n" \
"buffer_load_dwordx4 a[96:99], v20, s[56:59], s93 offen offset:0 sc1\n" \
"v_mfma_f32_16x16x32_bf16 v[140:143], a[0:3], a[56:59], v[140:143]\n" \
"ds_read_b64_tr_b16 a[218:219], v34 offset:4768\n" \
"ds_read_b64_tr_b16 a[0:1], v34 offset:9376\n" \
"v_mfma_f32_16x16x32_bf16 v[136:139], a[4:7], a[44:47], v[136:139]\n" \
"ds_read_b64_tr_b16 a[2:3], v34 offset:13984\n" \
"v_mfma_f32_16x16x32_bf16 v[140:143], a[4:7], a[60:63], v[140:143]\n" \
"ds_read_b64_tr_b16 a[4:5], v34 offset:18592\n" \
"ds_read_b64_tr_b16 a[6:7], v34 offset:23200\n" \
"v_mfma_f32_16x16x32_bf16 v[144:147], a[8:11], a[128:131], v[144:147]\n" \
"v_mfma_f32_16x16x32_bf16 v[148:151], a[8:11], a[144:147], v[148:151]\n" \
"buffer_load_dwordx4 a[112:115], v32, s[56:59], s93 offen offset:0 sc1\n" \
"v_mfma_f32_16x16x32_bf16 v[144:147], a[12:15], a[132:135], v[144:147]\n" \
"v_cvt_pk_bf16_f32 v136, v136, v137\n" \
"v_cvt_pk_bf16_f32 v137, v138, v139\n" \
"v_cvt_pk_bf16_f32 v138, v140, v141\n" \
"v_cvt_pk_bf16_f32 v139, v142, v143\n" \
"v_mfma_f32_16x16x32_bf16 v[148:151], a[12:15], a[148:151], v[148:151]\n" \
"buffer_store_dwordx4 v[136:139], v20, s[56:59], s68 offen offset:0 sc1\n" \
"s_add_u32 s68, s68, 0x2000\n" \
"v_mfma_f32_16x16x32_bf16 v[144:147], a[16:19], a[136:139], v[144:147]\n" \
"ds_read_b64_tr_b16 a[8:9], v10 offset:9376\n" \
"ds_read_b64_tr_b16 a[10:11], v10 offset:13984\n" \
"v_mfma_f32_16x16x32_bf16 v[148:151], a[16:19], a[152:155], v[148:151]\n" \
"ds_read_b64_tr_b16 a[12:13], v33 offset:37056\n" \
"ds_read_b64_tr_b16 a[14:15], v33 offset:41664\n" \
"v_mfma_f32_16x16x32_bf16 v[144:147], a[20:23], a[140:143], v[144:147]\n" \
"ds_read_b64_tr_b16 a[16:17], v33 offset:46272\n" \
"ds_read_b64_tr_b16 a[18:19], v33 offset:50880\n" \
"v_mfma_f32_16x16x32_bf16 v[148:151], a[20:23], a[156:159], v[148:151]\n" \
"buffer_load_dwordx4 a[100:103], v20, s[56:59], s93 offen offset:1024 sc1\n" \
"v_mfma_f32_16x16x32_bf16 v[144:147], a[24:27], a[32:35], v[144:147]\n" \
"ds_read_b64_tr_b16 a[20:21], v33 offset:55488\n" \
"ds_read_b64_tr_b16 a[22:23], v33 offset:60096\n" \
"v_mfma_f32_16x16x32_bf16 v[148:151], a[24:27], a[48:51], v[148:151]\n" \
"ds_read_b64_tr_b16 a[24:25], v10 offset:192\n" \
"ds_read_b64_tr_b16 a[26:27], v10 offset:4800\n" \
"v_mfma_f32_16x16x32_bf16 v[144:147], a[28:31], a[36:39], v[144:147]\n" \
"v_mfma_f32_16x16x32_bf16 v[148:151], a[28:31], a[52:55], v[148:151]\n" \
"ds_read_b64_tr_b16 a[28:29], v34 offset:192\n" \
"buffer_load_dwordx4 a[116:119], v32, s[56:59], s93 offen offset:1024 sc1\n" \
"s_waitcnt lgkmcnt(13)\n" \
"v_mfma_f32_16x16x32_bf16 v[144:147], a[192:195], a[40:43], v[144:147]\n" \
"ds_read_b64_tr_b16 a[30:31], v34 offset:4800\n" \
"v_mfma_f32_16x16x32_bf16 v[148:151], a[192:195], a[56:59], v[148:151]\n" \
"ds_read_b64_tr_b16 a[192:193], v34 offset:9408\n" \
"ds_read_b64_tr_b16 a[194:195], v34 offset:14016\n" \
"v_mfma_f32_16x16x32_bf16 v[144:147], a[196:199], a[44:47], v[144:147]\n" \
"v_mfma_f32_16x16x32_bf16 v[148:151], a[196:199], a[60:63], v[148:151]\n" \
"buffer_load_dwordx4 a[104:107], v20, s[56:59], s93 offen offset:2048 sc1\n" \
"v_mfma_f32_16x16x32_bf16 v[152:155], a[200:203], a[128:131], v[152:155]\n" \
"ds_read_b64_tr_b16 a[196:197], v34 offset:18624\n" \
"ds_read_b64_tr_b16 a[198:199], v34 offset:23232\n" \
"v_mfma_f32_16x16x32_bf16 v[156:159], a[200:203], a[144:147], v[156:159]\n" \
"ds_read_b64_tr_b16 a[200:201], v10 offset:9408\n" \
"ds_read_b64_tr_b16 a[202:203], v10 offset:14016\n" \
"v_cvt_pk_bf16_f32 v144, v144, v145\n" \
"v_cvt_pk_bf16_f32 v145, v146, v147\n" \
"v_cvt_pk_bf16_f32 v146, v148, v149\n" \
"v_cvt_pk_bf16_f32 v147, v150, v151\n" \
"buffer_store_dwordx4 v[144:147], v20, s[56:59], s69 offen offset:0 sc1\n" \
"s_add_u32 s69, s69, 0x1800\n" \
"v_mfma_f32_16x16x32_bf16 v[152:155], a[204:207], a[132:135], v[152:155]\n" \
"v_mfma_f32_16x16x32_bf16 v[156:159], a[204:207], a[148:151], v[156:159]\n" \
"ds_read_b64_tr_b16 a[204:205], v33 offset:256\n" \
"ds_read_b64_tr_b16 a[206:207], v33 offset:4864\n" \
"v_mfma_f32_16x16x32_bf16 v[152:155], a[208:211], a[136:139], v[152:155]\n" \
"v_mfma_f32_16x16x32_bf16 v[156:159], a[208:211], a[152:155], v[156:159]\n" \
"buffer_load_dwordx4 a[120:123], v32, s[56:59], s93 offen offset:2048 sc1\n" \
"v_mfma_f32_16x16x32_bf16 v[152:155], a[212:215], a[140:143], v[152:155]\n" \
"ds_read_b64_tr_b16 a[208:209], v33 offset:9472\n" \
"ds_read_b64_tr_b16 a[210:211], v33 offset:14080\n" \
"v_mfma_f32_16x16x32_bf16 v[156:159], a[212:215], a[156:159], v[156:159]\n" \
"ds_read_b64_tr_b16 a[212:213], v33 offset:18688\n" \
"ds_read_b64_tr_b16 a[214:215], v33 offset:23296\n" \
"v_mfma_f32_16x16x32_bf16 v[152:155], a[216:219], a[32:35], v[152:155]\n" \
"v_mfma_f32_16x16x32_bf16 v[156:159], a[216:219], a[48:51], v[156:159]\n" \
"ds_read_b64_tr_b16 a[216:217], v33 offset:27904\n" \
"ds_read_b64_tr_b16 a[218:219], v33 offset:32512\n" \
"buffer_load_dwordx4 a[108:111], v20, s[56:59], s93 offen offset:3072 sc1\n" \
"v_mfma_f32_16x16x32_bf16 v[152:155], a[0:3], a[36:39], v[152:155]\n" \
"v_mfma_f32_16x16x32_bf16 v[156:159], a[0:3], a[52:55], v[156:159]\n" \
"ds_read_b64_tr_b16 a[0:1], v33 offset:37120\n" \
"s_waitcnt lgkmcnt(13)\n" \
"v_mfma_f32_16x16x32_bf16 v[152:155], a[4:7], a[40:43], v[152:155]\n" \
"ds_read_b64_tr_b16 a[2:3], v33 offset:41728\n" \
"v_mfma_f32_16x16x32_bf16 v[156:159], a[4:7], a[56:59], v[156:159]\n" \
"ds_read_b64_tr_b16 a[4:5], v33 offset:46336\n" \
"ds_read_b64_tr_b16 a[6:7], v33 offset:50944\n" \
"buffer_load_dwordx4 a[124:127], v32, s[56:59], s93 offen offset:3072 sc1\n" \
"v_mfma_f32_16x16x32_bf16 v[152:155], a[8:11], a[44:47], v[152:155]\n" \
"v_mfma_f32_16x16x32_bf16 v[156:159], a[8:11], a[60:63], v[156:159]\n" \
"ds_read_b64_tr_b16 a[8:9], v33 offset:55552\n" \
"ds_read_b64_tr_b16 a[10:11], v33 offset:60160\n" \
"v_mfma_f32_16x16x32_bf16 v[160:163], a[12:15], a[128:131], v[160:163]\n" \
"v_mfma_f32_16x16x32_bf16 v[164:167], a[12:15], a[144:147], v[164:167]\n" \
"ds_read_b64_tr_b16 a[12:13], v10 offset:256\n" \
"ds_read_b64_tr_b16 a[14:15], v10 offset:4864\n" \
"v_mfma_f32_16x16x32_bf16 v[160:163], a[16:19], a[132:135], v[160:163]\n" \
"v_cvt_pk_bf16_f32 v152, v152, v153\n" \
"v_cvt_pk_bf16_f32 v153, v154, v155\n" \
"v_cvt_pk_bf16_f32 v154, v156, v157\n" \
"v_cvt_pk_bf16_f32 v155, v158, v159\n" \
"buffer_store_dwordx4 v[152:155], v20, s[56:59], s70 offen offset:0 sc1\n" \
"s_add_u32 s70, s70, 0x1000\n" \
"v_mfma_f32_16x16x32_bf16 v[164:167], a[16:19], a[148:151], v[164:167]\n" \
"ds_read_b64_tr_b16 a[16:17], v34 offset:256\n" \
"ds_read_b64_tr_b16 a[18:19], v34 offset:4864\n" \
"v_mfma_f32_16x16x32_bf16 v[160:163], a[20:23], a[136:139], v[160:163]\n" \
"v_mfma_f32_16x16x32_bf16 v[164:167], a[20:23], a[152:155], v[164:167]\n" \
"ds_read_b64_tr_b16 a[20:21], v34 offset:9472\n" \
"ds_read_b64_tr_b16 a[22:23], v34 offset:14080\n" \
"v_mfma_f32_16x16x32_bf16 v[160:163], a[24:27], a[140:143], v[160:163]\n" \
"buffer_load_dwordx4 v[64:67], v20, s[56:59], s95 offen offset:0 sc1\n" \
"v_mfma_f32_16x16x32_bf16 v[164:167], a[24:27], a[156:159], v[164:167]\n" \
"ds_read_b64_tr_b16 a[24:25], v34 offset:18688\n" \
"ds_read_b64_tr_b16 a[26:27], v34 offset:23296\n" \
"v_mfma_f32_16x16x32_bf16 v[160:163], a[28:31], a[32:35], v[160:163]\n" \
"v_mfma_f32_16x16x32_bf16 v[164:167], a[28:31], a[48:51], v[164:167]\n" \
"ds_read_b64_tr_b16 a[28:29], v10 offset:9472\n" \
"ds_read_b64_tr_b16 a[30:31], v10 offset:14080\n" \
"s_waitcnt vmcnt(20)\n" \
"s_mov_b64 exec, 1\n" \
"buffer_store_dword v39, off, s[56:59], s65 offset:0 sc1\n" \
"s_mov_b64 exec, -1\n" \
"v_mfma_f32_16x16x32_bf16 v[160:163], a[192:195], a[36:39], v[160:163]\n" \
"v_mfma_f32_16x16x32_bf16 v[164:167], a[192:195], a[52:55], v[164:167]\n" \
"ds_read_b64_tr_b16 a[192:193], v33 offset:0\n" \
"s_waitcnt lgkmcnt(11)\n" \
"v_mfma_f32_16x16x32_bf16 v[160:163], a[196:199], a[40:43], v[160:163]\n" \
"ds_read_b64_tr_b16 a[194:195], v33 offset:4608\n" \
"v_mfma_f32_16x16x32_bf16 v[164:167], a[196:199], a[56:59], v[164:167]\n" \
"ds_read_b64_tr_b16 a[196:197], v33 offset:9216\n" \
"ds_read_b64_tr_b16 a[198:199], v33 offset:13824\n" \
"v_mfma_f32_16x16x32_bf16 v[160:163], a[200:203], a[44:47], v[160:163]\n" \
"buffer_load_dwordx4 v[68:71], v20, s[56:59], s95 offen offset:1024 sc1\n" \
"v_mfma_f32_16x16x32_bf16 v[164:167], a[200:203], a[60:63], v[164:167]\n" \
"ds_read_b64_tr_b16 a[200:201], v33 offset:18432\n" \
"ds_read_b64_tr_b16 a[202:203], v33 offset:23040\n" \
"v_mfma_f32_16x16x32_bf16 v[248:251], a[204:207], a[220:223], v[248:251]\n" \
"v_mfma_f32_16x16x32_bf16 v[252:255], a[204:207], a[236:239], v[252:255]\n" \
"ds_read_b64_tr_b16 a[204:205], v33 offset:27648\n" \
"ds_read_b64_tr_b16 a[206:207], v33 offset:32256\n" \
"v_cvt_pk_bf16_f32 v160, v160, v161\n" \
"v_cvt_pk_bf16_f32 v161, v162, v163\n" \
"v_cvt_pk_bf16_f32 v162, v164, v165\n" \
"v_cvt_pk_bf16_f32 v163, v166, v167\n" \
"buffer_store_dwordx4 v[160:163], v20, s[56:59], s71 offen offset:0 sc1\n" \
"s_add_u32 s71, s71, 0x800\n" \
"v_mfma_f32_16x16x32_bf16 v[248:251], a[208:211], a[224:227], v[248:251]\n" \
"v_mfma_f32_16x16x32_bf16 v[252:255], a[208:211], a[240:243], v[252:255]\n" \
"ds_read_b64_tr_b16 a[208:209], v33 offset:36864\n" \
"ds_read_b64_tr_b16 a[210:211], v33 offset:41472\n" \
"v_mfma_f32_16x16x32_bf16 v[248:251], a[212:215], a[228:231], v[248:251]\n" \
"v_mfma_f32_16x16x32_bf16 v[252:255], a[212:215], a[244:247], v[252:255]\n" \
"ds_read_b64_tr_b16 a[212:213], v33 offset:46080\n" \
"ds_read_b64_tr_b16 a[214:215], v33 offset:50688\n" \
"v_mfma_f32_16x16x32_bf16 v[248:251], a[216:219], a[232:235], v[248:251]\n" \
"buffer_load_dwordx4 v[72:75], v32, s[56:59], s95 offen offset:0 sc1\n" \
"s_waitcnt vmcnt(21)\n" \
"s_mov_b64 exec, 1\n" \
"buffer_store_dword v39, off, s[56:59], s66 offset:4 sc1\n" \
"s_mov_b64 exec, -1\n" \
"v_readlane_b32 s66, v11, 47\n" \
"v_mfma_f32_16x16x32_bf16 v[252:255], a[216:219], a[248:251], v[252:255]\n" \
"ds_read_b64_tr_b16 a[216:217], v33 offset:55296\n" \
"ds_read_b64_tr_b16 a[218:219], v33 offset:59904\n" \
"v_mfma_f32_16x16x32_bf16 v[176:179], a[0:3], a[128:131], v[176:179]\n" \
"v_mfma_f32_16x16x32_bf16 v[180:183], a[0:3], a[144:147], v[180:183]\n" \
"v_mfma_f32_16x16x32_bf16 v[248:251], a[0:3], a[160:163], v[248:251]\n" \
"buffer_load_dwordx4 v[76:79], v32, s[56:59], s95 offen offset:1024 sc1\n" \
"v_mfma_f32_16x16x32_bf16 v[252:255], a[0:3], a[176:179], v[252:255]\n" \
"ds_read_b64_tr_b16 a[0:1], v10 offset:0\n" \
"ds_read_b64_tr_b16 a[2:3], v10 offset:4608\n" \
"v_mfma_f32_16x16x32_bf16 v[176:179], a[4:7], a[132:135], v[176:179]\n" \
"v_mfma_f32_16x16x32_bf16 v[180:183], a[4:7], a[148:151], v[180:183]\n" \
"v_mfma_f32_16x16x32_bf16 v[248:251], a[4:7], a[164:167], v[248:251]\n" \
"v_mfma_f32_16x16x32_bf16 v[252:255], a[4:7], a[180:183], v[252:255]\n" \
"ds_read_b64_tr_b16 a[4:5], v34 offset:0\n" \
"ds_read_b64_tr_b16 a[6:7], v34 offset:4608\n" \
"v_mfma_f32_16x16x32_bf16 v[176:179], a[8:11], a[136:139], v[176:179]\n" \
"buffer_load_dwordx4 v[80:83], v20, s[56:59], s96 offen offset:0 sc1\n" \
"v_mfma_f32_16x16x32_bf16 v[180:183], a[8:11], a[152:155], v[180:183]\n" \
"s_waitcnt vmcnt(23)\n" \
"v_mfma_f32_16x16x32_bf16 v[248:251], a[8:11], a[168:171], v[248:251]\n" \
"s_waitcnt vmcnt(22)\n" \
"v_mfma_f32_16x16x32_bf16 v[252:255], a[8:11], a[184:187], v[252:255]\n" \
"ds_read_b64_tr_b16 a[8:9], v34 offset:9216\n" \
"s_waitcnt lgkmcnt(7)\n" \
"v_mfma_f32_16x16x32_bf16 v[176:179], a[12:15], a[140:143], v[176:179]\n" \
"ds_read_b64_tr_b16 a[10:11], v34 offset:13824\n" \
"v_mfma_f32_16x16x32_bf16 v[180:183], a[12:15], a[156:159], v[180:183]\n" \
"s_waitcnt vmcnt(21)\n" \
"v_mfma_f32_16x16x32_bf16 v[248:251], a[12:15], a[172:175], v[248:251]\n" \
"s_waitcnt vmcnt(19)\n" \
"v_mfma_f32_16x16x32_bf16 v[252:255], a[12:15], a[188:191], v[252:255]\n" \
"ds_read_b64_tr_b16 a[12:13], v34 offset:18432\n" \
"ds_read_b64_tr_b16 a[14:15], v34 offset:23040\n" \
"s_mov_b64 exec, 1\n" \
"buffer_store_dword v39, off, s[56:59], s67 offset:8 sc1\n" \
"s_mov_b64 exec, -1\n" \
"v_readlane_b32 s67, v11, 48\n" \
"v_mfma_f32_16x16x32_bf16 v[176:179], a[16:19], a[32:35], v[176:179]\n" \
"v_mfma_f32_16x16x32_bf16 v[180:183], a[16:19], a[48:51], v[180:183]\n" \
"s_waitcnt vmcnt(19)\n" \
"v_mfma_f32_16x16x32_bf16 v[248:251], a[16:19], a[96:99], v[248:251]\n" \
"s_waitcnt vmcnt(18)\n" \
"v_mfma_f32_16x16x32_bf16 v[252:255], a[16:19], a[112:115], v[252:255]\n" \
"ds_read_b64_tr_b16 a[16:17], v10 offset:9216\n" \
"ds_read_b64_tr_b16 a[18:19], v10 offset:13824\n" \
"v_mfma_f32_16x16x32_bf16 v[176:179], a[20:23], a[36:39], v[176:179]\n" \
"buffer_load_dwordx4 v[84:87], v20, s[56:59], s96 offen offset:1024 sc1\n" \
"v_mfma_f32_16x16x32_bf16 v[180:183], a[20:23], a[52:55], v[180:183]\n" \
"s_waitcnt vmcnt(17)\n" \
"v_mfma_f32_16x16x32_bf16 v[248:251], a[20:23], a[100:103], v[248:251]\n" \
"s_mov_b64 exec, 1\n" \
"buffer_store_dword v39, off, s[56:59], s68 offset:12 sc1\n" \
"s_mov_b64 exec, -1\n" \
"s_waitcnt vmcnt(17)\n" \
"v_mfma_f32_16x16x32_bf16 v[252:255], a[20:23], a[116:119], v[252:255]\n" \
"v_readlane_b32 s68, v11, 49\n" \
"ds_read_b64_tr_b16 a[20:21], v33 offset:32\n" \
"v_mfma_f32_16x16x32_bf16 v[176:179], a[24:27], a[40:43], v[176:179]\n" \
"ds_read_b64_tr_b16 a[22:23], v33 offset:4640\n" \
"v_mfma_f32_16x16x32_bf16 v[180:183], a[24:27], a[56:59], v[180:183]\n" \
"s_waitcnt vmcnt(16)\n" \
"v_mfma_f32_16x16x32_bf16 v[248:251], a[24:27], a[104:107], v[248:251]\n" \
"s_waitcnt vmcnt(14)\n" \
"v_mfma_f32_16x16x32_bf16 v[252:255], a[24:27], a[120:123], v[252:255]\n" \
"ds_read_b64_tr_b16 a[24:25], v33 offset:9248\n" \
".fill (L_ab + 40960 - .) / 4, 4, 0xBF800000\n" \
"L_ap10:\n" \
"s_cbranch_execz L_ap0\n" \
"ds_read_b64_tr_b16 a[26:27], v33 offset:13856\n" \
"s_mov_b64 exec, 1\n" \
"buffer_store_dword v39, off, s[56:59], s69 offset:16 sc1\n" \
"s_mov_b64 exec, -1\n" \
"v_mfma_f32_16x16x32_bf16 v[176:179], a[28:31], a[44:47], v[176:179]\n" \
"v_readlane_b32 s69, v11, 50\n" \
"v_mfma_f32_16x16x32_bf16 v[180:183], a[28:31], a[60:63], v[180:183]\n" \
"s_waitcnt vmcnt(14)\n" \
"v_mfma_f32_16x16x32_bf16 v[248:251], a[28:31], a[108:111], v[248:251]\n" \
"buffer_load_dwordx4 v[88:91], v32, s[56:59], s96 offen offset:0 sc1\n" \
"s_waitcnt vmcnt(14)\n" \
"v_mfma_f32_16x16x32_bf16 v[252:255], a[28:31], a[124:127], v[252:255]\n" \
"ds_read_b64_tr_b16 a[28:29], v33 offset:18464\n" \
"v_cvt_pk_bf16_f32 v176, v176, v177\n" \
"v_cvt_pk_bf16_f32 v177, v178, v179\n" \
"v_cvt_pk_bf16_f32 v178, v180, v181\n" \
"v_cvt_pk_bf16_f32 v179, v182, v183\n" \
"v_cvt_pk_bf16_f32 v248, v248, v249\n" \
"v_cvt_pk_bf16_f32 v249, v250, v251\n" \
"buffer_store_dwordx4 v[176:179], v20, s[56:59], s72 offen offset:0 sc1\n" \
"v_cvt_pk_bf16_f32 v250, v252, v253\n" \
"v_cvt_pk_bf16_f32 v251, v254, v255\n" \
"v_mfma_f32_16x16x32_bf16 v[184:187], a[192:195], a[220:223], v[184:187]\n" \
"ds_read_b64_tr_b16 a[30:31], v33 offset:23072\n" \
"v_mfma_f32_16x16x32_bf16 v[188:191], a[192:195], a[236:239], v[188:191]\n" \
"buffer_store_dwordx4 v[248:251], v20, s[56:59], s72 offen offset:1024 sc1\n" \
"v_mfma_f32_16x16x32_bf16 v[184:187], a[196:199], a[224:227], v[184:187]\n" \
"ds_read_b64_tr_b16 a[192:193], v33 offset:27680\n" \
"ds_read_b64_tr_b16 a[194:195], v33 offset:32288\n" \
"v_mfma_f32_16x16x32_bf16 v[188:191], a[196:199], a[240:243], v[188:191]\n" \
"ds_read_b64_tr_b16 a[196:197], v33 offset:36896\n" \
"ds_read_b64_tr_b16 a[198:199], v33 offset:41504\n" \
"v_mfma_f32_16x16x32_bf16 v[184:187], a[200:203], a[228:231], v[184:187]\n" \
"v_mfma_f32_16x16x32_bf16 v[188:191], a[200:203], a[244:247], v[188:191]\n" \
"ds_read_b64_tr_b16 a[200:201], v33 offset:46112\n" \
"ds_read_b64_tr_b16 a[202:203], v33 offset:50720\n" \
"v_mfma_f32_16x16x32_bf16 v[184:187], a[204:207], a[232:235], v[184:187]\n" \
"buffer_load_dwordx4 v[92:95], v32, s[56:59], s96 offen offset:1024 sc1\n" \
"v_mfma_f32_16x16x32_bf16 v[188:191], a[204:207], a[248:251], v[188:191]\n" \
"ds_read_b64_tr_b16 a[204:205], v33 offset:55328\n" \
"ds_read_b64_tr_b16 a[206:207], v33 offset:59936\n" \
"v_mfma_f32_16x16x32_bf16 v[184:187], a[208:211], a[160:163], v[184:187]\n" \
"v_mfma_f32_16x16x32_bf16 v[188:191], a[208:211], a[176:179], v[188:191]\n" \
"ds_read_b64_tr_b16 a[208:209], v10 offset:32\n" \
"ds_read_b64_tr_b16 a[210:211], v10 offset:4640\n" \
"v_mfma_f32_16x16x32_bf16 v[184:187], a[212:215], a[164:167], v[184:187]\n" \
"v_mfma_f32_16x16x32_bf16 v[188:191], a[212:215], a[180:183], v[188:191]\n" \
"ds_read_b64_tr_b16 a[212:213], v34 offset:32\n" \
"s_waitcnt lgkmcnt(12)\n" \
"v_mfma_f32_16x16x32_bf16 v[184:187], a[216:219], a[168:171], v[184:187]\n" \
"buffer_load_dwordx4 v[96:99], v20, s[56:59], s97 offen offset:0 sc1\n" \
"v_mfma_f32_16x16x32_bf16 v[188:191], a[216:219], a[184:187], v[188:191]\n" \
"ds_read_b64_tr_b16 a[214:215], v34 offset:4640\n" \
"ds_read_b64_tr_b16 a[216:217], v34 offset:9248\n" \
"v_mfma_f32_16x16x32_bf16 v[184:187], a[0:3], a[172:175], v[184:187]\n" \
"ds_read_b64_tr_b16 a[218:219], v34 offset:13856\n" \
"v_mfma_f32_16x16x32_bf16 v[188:191], a[0:3], a[188:191], v[188:191]\n" \
"ds_read_b64_tr_b16 a[0:1], v34 offset:18464\n" \
"ds_read_b64_tr_b16 a[2:3], v34 offset:23072\n" \
"v_mfma_f32_16x16x32_bf16 v[184:187], a[4:7], a[96:99], v[184:187]\n" \
"v_mfma_f32_16x16x32_bf16 v[188:191], a[4:7], a[112:115], v[188:191]\n" \
"ds_read_b64_tr_b16 a[4:5], v10 offset:9248\n" \
"ds_read_b64_tr_b16 a[6:7], v10 offset:13856\n" \
"v_mfma_f32_16x16x32_bf16 v[184:187], a[8:11], a[100:103], v[184:187]\n" \
"buffer_load_dwordx4 v[100:103], v20, s[56:59], s97 offen offset:1024 sc1\n" \
"v_mfma_f32_16x16x32_bf16 v[188:191], a[8:11], a[116:119], v[188:191]\n" \
"ds_read_b64_tr_b16 a[8:9], v33 offset:64\n" \
"ds_read_b64_tr_b16 a[10:11], v33 offset:4672\n" \
"v_mfma_f32_16x16x32_bf16 v[184:187], a[12:15], a[104:107], v[184:187]\n" \
"v_mfma_f32_16x16x32_bf16 v[188:191], a[12:15], a[120:123], v[188:191]\n" \
"ds_read_b64_tr_b16 a[12:13], v33 offset:9280\n" \
"ds_read_b64_tr_b16 a[14:15], v33 offset:13888\n" \
"v_mfma_f32_16x16x32_bf16 v[184:187], a[16:19], a[108:111], v[184:187]\n" \
"v_mfma_f32_16x16x32_bf16 v[188:191], a[16:19], a[124:127], v[188:191]\n" \
"buffer_load_dword v37, v40, s[56:59], s74 offen sc1\n" \
"v_mfma_f32_16x16x32_bf16 v[192:195], a[20:23], a[220:223], v[192:195]\n" \
"ds_read_b64_tr_b16 a[16:17], v33 offset:18496\n" \
"ds_read_b64_tr_b16 a[18:19], v33 offset:23104\n" \
"v_mfma_f32_16x16x32_bf16 v[196:199], a[20:23], a[236:239], v[196:199]\n" \
"ds_read_b64_tr_b16 a[20:21], v33 offset:27712\n" \
"ds_read_b64_tr_b16 a[22:23], v33 offset:32320\n" \
"v_cvt_pk_bf16_f32 v184, v184, v185\n" \
"v_cvt_pk_bf16_f32 v185, v186, v187\n" \
"v_cvt_pk_bf16_f32 v186, v188, v189\n" \
"v_cvt_pk_bf16_f32 v187, v190, v191\n" \
"buffer_store_dwordx4 v[184:187], v20, s[56:59], s92 offen offset:1024 sc1\n" \
"s_waitcnt vmcnt(20)\n" \
"s_mov_b64 exec, 1\n" \
"buffer_store_dword v39, off, s[56:59], s70 offset:20 sc1\n" \
"s_mov_b64 exec, -1\n" \
"s_add_u32 s92, s92, 0x3800\n" \
"v_mfma_f32_16x16x32_bf16 v[192:195], a[24:27], a[224:227], v[192:195]\n" \
"v_mfma_f32_16x16x32_bf16 v[196:199], a[24:27], a[240:243], v[196:199]\n" \
"ds_read_b64_tr_b16 a[24:25], v33 offset:36928\n" \
"s_waitcnt lgkmcnt(11)\n" \
"v_mfma_f32_16x16x32_bf16 v[192:195], a[28:31], a[228:231], v[192:195]\n" \
"ds_read_b64_tr_b16 a[26:27], v33 offset:41536\n" \
"v_mfma_f32_16x16x32_bf16 v[196:199], a[28:31], a[244:247], v[196:199]\n" \
"ds_read_b64_tr_b16 a[28:29], v33 offset:46144\n" \
"ds_read_b64_tr_b16 a[30:31], v33 offset:50752\n" \
"v_mfma_f32_16x16x32_bf16 v[192:195], a[192:195], a[232:235], v[192:195]\n" \
"buffer_load_dwordx4 v[104:107], v32, s[56:59], s97 offen offset:0 sc1\n" \
"v_mfma_f32_16x16x32_bf16 v[196:199], a[192:195], a[248:251], v[196:199]\n" \
"ds_read_b64_tr_b16 a[192:193], v33 offset:55360\n" \
"ds_read_b64_tr_b16 a[194:195], v33 offset:59968\n" \
"v_mfma_f32_16x16x32_bf16 v[192:195], a[196:199], a[160:163], v[192:195]\n" \
"v_mfma_f32_16x16x32_bf16 v[196:199], a[196:199], a[176:179], v[196:199]\n" \
"ds_read_b64_tr_b16 a[196:197], v10 offset:64\n" \
"ds_read_b64_tr_b16 a[198:199], v10 offset:4672\n" \
"v_mfma_f32_16x16x32_bf16 v[192:195], a[200:203], a[164:167], v[192:195]\n" \
"v_mfma_f32_16x16x32_bf16 v[196:199], a[200:203], a[180:183], v[196:199]\n" \
"buffer_load_dwordx4 v[108:111], v32, s[56:59], s97 offen offset:1024 sc1\n" \
"v_mfma_f32_16x16x32_bf16 v[192:195], a[204:207], a[168:171], v[192:195]\n" \
"ds_read_b64_tr_b16 a[200:201], v34 offset:64\n" \
"ds_read_b64_tr_b16 a[202:203], v34 offset:4672\n" \
"v_mfma_f32_16x16x32_bf16 v[196:199], a[204:207], a[184:187], v[196:199]\n" \
"ds_read_b64_tr_b16 a[204:205], v34 offset:9280\n" \
"ds_read_b64_tr_b16 a[206:207], v34 offset:13888\n" \
"v_mfma_f32_16x16x32_bf16 v[192:195], a[208:211], a[172:175], v[192:195]\n" \
"v_mfma_f32_16x16x32_bf16 v[196:199], a[208:211], a[188:191], v[196:199]\n" \
"ds_read_b64_tr_b16 a[208:209], v34 offset:18496\n" \
"ds_read_b64_tr_b16 a[210:211], v34 offset:23104\n" \
"v_mfma_f32_16x16x32_bf16 v[192:195], a[212:215], a[96:99], v[192:195]\n" \
"v_mfma_f32_16x16x32_bf16 v[196:199], a[212:215], a[112:115], v[196:199]\n" \
"ds_read_b64_tr_b16 a[212:213], v10 offset:9280\n" \
"ds_read_b64_tr_b16 a[214:215], v10 offset:13888\n" \
"v_mfma_f32_16x16x32_bf16 v[192:195], a[216:219], a[100:103], v[192:195]\n" \
"s_waitcnt vmcnt(19)\n" \
"s_mov_b64 exec, 1\n" \
"buffer_store_dword v39, off, s[56:59], s71 offset:24 sc1\n" \
"s_mov_b64 exec, -1\n" \
"v_mfma_f32_16x16x32_bf16 v[196:199], a[216:219], a[116:119], v[196:199]\n" \
"ds_read_b64_tr_b16 a[216:217], v33 offset:96\n" \
"ds_read_b64_tr_b16 a[218:219], v33 offset:4704\n" \
"v_mfma_f32_16x16x32_bf16 v[192:195], a[0:3], a[104:107], v[192:195]\n" \
"v_mfma_f32_16x16x32_bf16 v[196:199], a[0:3], a[120:123], v[196:199]\n" \
"ds_read_b64_tr_b16 a[0:1], v33 offset:9312\n" \
"s_waitcnt lgkmcnt(13)\n" \
"v_mfma_f32_16x16x32_bf16 v[192:195], a[4:7], a[108:111], v[192:195]\n" \
"ds_read_b64_tr_b16 a[2:3], v33 offset:13920\n" \
"v_mfma_f32_16x16x32_bf16 v[196:199], a[4:7], a[124:127], v[196:199]\n" \
"ds_read_b64_tr_b16 a[4:5], v33 offset:18528\n" \
"ds_read_b64_tr_b16 a[6:7], v33 offset:23136\n" \
"v_mfma_f32_16x16x32_bf16 v[200:203], a[8:11], a[220:223], v[200:203]\n" \
"v_mfma_f32_16x16x32_bf16 v[204:207], a[8:11], a[236:239], v[204:207]\n" \
"ds_read_b64_tr_b16 a[8:9], v33 offset:27744\n" \
"ds_read_b64_tr_b16 a[10:11], v33 offset:32352\n" \
"v_cvt_pk_bf16_f32 v192, v192, v193\n" \
"v_cvt_pk_bf16_f32 v193, v194, v195\n" \
"v_cvt_pk_bf16_f32 v194, v196, v197\n" \
"v_cvt_pk_bf16_f32 v195, v198, v199\n" \
"buffer_store_dwordx4 v[192:195], v20, s[56:59], s66 offen offset:1024 sc1\n" \
"v_mfma_f32_16x16x32_bf16 v[200:203], a[12:15], a[224:227], v[200:203]\n" \
"v_mfma_f32_16x16x32_bf16 v[204:207], a[12:15], a[240:243], v[204:207]\n" \
"ds_read_b64_tr_b16 a[12:13], v33 offset:36960\n" \
"ds_read_b64_tr_b16 a[14:15], v33 offset:41568\n" \
"v_mfma_f32_16x16x32_bf16 v[200:203], a[16:19], a[228:231], v[200:203]\n" \
"v_mfma_f32_16x16x32_bf16 v[204:207], a[16:19], a[244:247], v[204:207]\n" \
"ds_read_b64_tr_b16 a[16:17], v33 offset:46176\n" \
"ds_read_b64_tr_b16 a[18:19], v33 offset:50784\n" \
"v_mfma_f32_16x16x32_bf16 v[200:203], a[20:23], a[232:235], v[200:203]\n" \
"v_mfma_f32_16x16x32_bf16 v[204:207], a[20:23], a[248:251], v[204:207]\n" \
"ds_read_b64_tr_b16 a[20:21], v33 offset:55392\n" \
"ds_read_b64_tr_b16 a[22:23], v33 offset:60000\n" \
"v_mfma_f32_16x16x32_bf16 v[200:203], a[24:27], a[160:163], v[200:203]\n" \
"v_mfma_f32_16x16x32_bf16 v[204:207], a[24:27], a[176:179], v[204:207]\n" \
"ds_read_b64_tr_b16 a[24:25], v10 offset:96\n" \
"ds_read_b64_tr_b16 a[26:27], v10 offset:4704\n" \
"v_mfma_f32_16x16x32_bf16 v[200:203], a[28:31], a[164:167], v[200:203]\n" \
"v_mfma_f32_16x16x32_bf16 v[204:207], a[28:31], a[180:183], v[204:207]\n" \
"ds_read_b64_tr_b16 a[28:29], v34 offset:96\n" \
"ds_read_b64_tr_b16 a[30:31], v34 offset:4704\n" \
"v_mfma_f32_16x16x32_bf16 v[200:203], a[192:195], a[168:171], v[200:203]\n" \
"v_mfma_f32_16x16x32_bf16 v[204:207], a[192:195], a[184:187], v[204:207]\n" \
"ds_read_b64_tr_b16 a[192:193], v34 offset:9312\n" \
"s_waitcnt lgkmcnt(13)\n" \
"v_mfma_f32_16x16x32_bf16 v[200:203], a[196:199], a[172:175], v[200:203]\n" \
"ds_read_b64_tr_b16 a[194:195], v34 offset:13920\n" \
"v_mfma_f32_16x16x32_bf16 v[204:207], a[196:199], a[188:191], v[204:207]\n" \
"ds_read_b64_tr_b16 a[196:197], v34 offset:18528\n" \
"ds_read_b64_tr_b16 a[198:199], v34 offset:23136\n" \
"v_mfma_f32_16x16x32_bf16 v[200:203], a[200:203], a[96:99], v[200:203]\n" \
"v_mfma_f32_16x16x32_bf16 v[204:207], a[200:203], a[112:115], v[204:207]\n" \
"ds_read_b64_tr_b16 a[200:201], v10 offset:9312\n" \
"ds_read_b64_tr_b16 a[202:203], v10 offset:13920\n" \
"v_mfma_f32_16x16x32_bf16 v[200:203], a[204:207], a[100:103], v[200:203]\n" \
"v_mfma_f32_16x16x32_bf16 v[204:207], a[204:207], a[116:119], v[204:207]\n" \
"ds_read_b64_tr_b16 a[204:205], v33 offset:128\n" \
"ds_read_b64_tr_b16 a[206:207], v33 offset:4736\n" \
"v_mfma_f32_16x16x32_bf16 v[200:203], a[208:211], a[104:107], v[200:203]\n" \
"v_mfma_f32_16x16x32_bf16 v[204:207], a[208:211], a[120:123], v[204:207]\n" \
"ds_read_b64_tr_b16 a[208:209], v33 offset:9344\n" \
"ds_read_b64_tr_b16 a[210:211], v33 offset:13952\n" \
"v_mfma_f32_16x16x32_bf16 v[200:203], a[212:215], a[108:111], v[200:203]\n" \
"v_mfma_f32_16x16x32_bf16 v[204:207], a[212:215], a[124:127], v[204:207]\n" \
"ds_read_b64_tr_b16 a[212:213], v33 offset:18560\n" \
"ds_read_b64_tr_b16 a[214:215], v33 offset:23168\n" \
"v_mfma_f32_16x16x32_bf16 v[208:211], a[216:219], a[220:223], v[208:211]\n" \
"v_mfma_f32_16x16x32_bf16 v[212:215], a[216:219], a[236:239], v[212:215]\n" \
"ds_read_b64_tr_b16 a[216:217], v33 offset:27776\n" \
"ds_read_b64_tr_b16 a[218:219], v33 offset:32384\n" \
"v_mfma_f32_16x16x32_bf16 v[208:211], a[0:3], a[224:227], v[208:211]\n" \
"v_cvt_pk_bf16_f32 v200, v200, v201\n" \
"v_cvt_pk_bf16_f32 v201, v202, v203\n" \
"v_cvt_pk_bf16_f32 v202, v204, v205\n" \
"v_cvt_pk_bf16_f32 v203, v206, v207\n" \
"buffer_store_dwordx4 v[200:203], v20, s[56:59], s67 offen offset:1024 sc1\n" \
"v_mfma_f32_16x16x32_bf16 v[212:215], a[0:3], a[240:243], v[212:215]\n" \
"ds_read_b64_tr_b16 a[0:1], v33 offset:36992\n" \
"ds_read_b64_tr_b16 a[2:3], v33 offset:41600\n" \
"v_mfma_f32_16x16x32_bf16 v[208:211], a[4:7], a[228:231], v[208:211]\n" \
"v_mfma_f32_16x16x32_bf16 v[212:215], a[4:7], a[244:247], v[212:215]\n" \
"ds_read_b64_tr_b16 a[4:5], v33 offset:46208\n" \
"s_waitcnt lgkmcnt(13)\n" \
"v_mfma_f32_16x16x32_bf16 v[208:211], a[8:11], a[232:235], v[208:211]\n" \
"ds_read_b64_tr_b16 a[6:7], v33 offset:50816\n" \
"v_mfma_f32_16x16x32_bf16 v[212:215], a[8:11], a[248:251], v[212:215]\n" \
"ds_read_b64_tr_b16 a[8:9], v33 offset:55424\n" \
"ds_read_b64_tr_b16 a[10:11], v33 offset:60032\n" \
"v_mfma_f32_16x16x32_bf16 v[208:211], a[12:15], a[160:163], v[208:211]\n" \
"v_mfma_f32_16x16x32_bf16 v[212:215], a[12:15], a[176:179], v[212:215]\n" \
"ds_read_b64_tr_b16 a[12:13], v10 offset:128\n" \
"ds_read_b64_tr_b16 a[14:15], v10 offset:4736\n" \
"v_mfma_f32_16x16x32_bf16 v[208:211], a[16:19], a[164:167], v[208:211]\n" \
"s_waitcnt vmcnt(7)\n" \
"v_cmp_eq_u32_e64 s[98:99], s62, v37\n" \
"s_mov_b64 exec, 3\n" \
"buffer_store_dword v39, v41, s[56:59], s73 offen sc1\n" \
"s_mov_b64 exec, -1\n" \
"s_andn2_b32 s75, 0x7f, s98\n" \
"s_mov_b32 s20, s75\n" \
"s_bitcmp1_b32 s20, 0\n" \
"s_cselect_b32 s93, 0x40000000, s12\n" \
"s_bitcmp1_b32 s20, 1\n" \
"s_cselect_b32 s94, 0x40000000, s12\n" \
"s_bitcmp1_b32 s20, 2\n" \
"s_cselect_b32 s95, 0x40000000, s13\n" \
"s_bitcmp1_b32 s20, 3\n" \
"s_cselect_b32 s96, 0x40000000, s13\n" \
"s_bitcmp1_b32 s20, 4\n" \
"s_cselect_b32 s97, 0x40000000, s14\n" \
"s_bitcmp1_b32 s20, 5\n" \
"s_cselect_b32 s64, 0x40000000, s14\n" \
"s_bitcmp1_b32 s20, 6\n" \
"s_cselect_b32 s65, 0x40000000, s15\n" \
"s_add_u32 s66, s66, 0x3000\n" \
"s_add_u32 s67, s67, 0x2800\n" \
"v_mfma_f32_16x16x32_bf16 v[212:215], a[16:19], a[180:183], v[212:215]\n" \
"buffer_load_dwordx4 v[116:119], v20, s[56:59], s93 offen offset:0 sc1\n" \
"v_mfma_f32_16x16x32_bf16 v[208:211], a[20:23], a[168:171], v[208:211]\n" \
"ds_read_b64_tr_b16 a[16:17], v34 offset:128\n" \
"ds_read_b64_tr_b16 a[18:19], v34 offset:4736\n" \
"v_mfma_f32_16x16x32_bf16 v[212:215], a[20:23], a[184:187], v[212:215]\n" \
"ds_read_b64_tr_b16 a[20:21], v34 offset:9344\n" \
"ds_read_b64_tr_b16 a[22:23], v34 offset:13952\n" \
"v_mfma_f32_16x16x32_bf16 v[208:211], a[24:27], a[172:175], v[208:211]\n" \
"v_mfma_f32_16x16x32_bf16 v[212:215], a[24:27], a[188:191], v[212:215]\n" \
"ds_read_b64_tr_b16 a[24:25], v34 offset:18560\n" \
"ds_read_b64_tr_b16 a[26:27], v34 offset:23168\n" \
"v_mfma_f32_16x16x32_bf16 v[208:211], a[28:31], a[96:99], v[208:211]\n" \
"v_mfma_f32_16x16x32_bf16 v[212:215], a[28:31], a[112:115], v[212:215]\n" \
"ds_read_b64_tr_b16 a[28:29], v10 offset:9344\n" \
"ds_read_b64_tr_b16 a[30:31], v10 offset:13952\n" \
"v_mfma_f32_16x16x32_bf16 v[208:211], a[192:195], a[100:103], v[208:211]\n" \
"v_mfma_f32_16x16x32_bf16 v[212:215], a[192:195], a[116:119], v[212:215]\n" \
"ds_read_b64_tr_b16 a[192:193], v33 offset:160\n" \
"ds_read_b64_tr_b16 a[194:195], v33 offset:4768\n" \
"v_mfma_f32_16x16x32_bf16 v[208:211], a[196:199], a[104:107], v[208:211]\n" \
"v_mfma_f32_16x16x32_bf16 v[212:215], a[196:199], a[120:123], v[212:215]\n" \
"ds_read_b64_tr_b16 a[196:197], v33 offset:9376\n" \
"s_waitcnt lgkmcnt(11)\n" \
"v_mfma_f32_16x16x32_bf16 v[208:211], a[200:203], a[108:111], v[208:211]\n" \
"ds_read_b64_tr_b16 a[198:199], v33 offset:13984\n" \
"v_mfma_f32_16x16x32_bf16 v[212:215], a[200:203], a[124:127], v[212:215]\n" \
"ds_read_b64_tr_b16 a[200:201], v33 offset:18592\n" \
"ds_read_b64_tr_b16 a[202:203], v33 offset:23200\n" \
"v_mfma_f32_16x16x32_bf16 v[216:219], a[204:207], a[220:223], v[216:219]\n" \
"v_mfma_f32_16x16x32_bf16 v[220:223], a[204:207], a[236:239], v[220:223]\n" \
"buffer_load_dwordx4 v[124:127], v20, s[56:59], s94 offen offset:2048 sc1\n" \
"v_mfma_f32_16x16x32_bf16 v[216:219], a[208:211], a[224:227], v[216:219]\n" \
"v_cvt_pk_bf16_f32 v208, v208, v209\n" \
"v_cvt_pk_bf16_f32 v209, v210, v211\n" \
"v_cvt_pk_bf16_f32 v210, v212, v213\n" \
"v_cvt_pk_bf16_f32 v211, v214, v215\n" \
"v_mfma_f32_16x16x32_bf16 v[220:223], a[208:211], a[240:243], v[220:223]\n" \
"buffer_store_dwordx4 v[208:211], v20, s[56:59], s68 offen offset:1024 sc1\n" \
"s_add_u32 s68, s68, 0x2000\n" \
"v_mfma_f32_16x16x32_bf16 v[216:219], a[212:215], a[228:231], v[216:219]\n" \
"ds_read_b64_tr_b16 a[204:205], v33 offset:27808\n" \
"ds_read_b64_tr_b16 a[206:207], v33 offset:32416\n" \
"v_mfma_f32_16x16x32_bf16 v[220:223], a[212:215], a[244:247], v[220:223]\n" \
"ds_read_b64_tr_b16 a[208:209], v33 offset:37024\n" \
"ds_read_b64_tr_b16 a[210:211], v33 offset:41632\n" \
"v_mfma_f32_16x16x32_bf16 v[216:219], a[216:219], a[232:235], v[216:219]\n" \
"ds_read_b64_tr_b16 a[212:213], v33 offset:46240\n" \
"ds_read_b64_tr_b16 a[214:215], v33 offset:50848\n" \
"v_mfma_f32_16x16x32_bf16 v[220:223], a[216:219], a[248:251], v[220:223]\n" \
"ds_read_b64_tr_b16 a[216:217], v33 offset:55456\n" \
"ds_read_b64_tr_b16 a[218:219], v33 offset:60064\n" \
"v_mfma_f32_16x16x32_bf16 v[216:219], a[0:3], a[160:163], v[216:219]\n" \
"v_mfma_f32_16x16x32_bf16 v[220:223], a[0:3], a[176:179], v[220:223]\n" \
"ds_read_b64_tr_b16 a[0:1], v10 offset:160\n" \
"ds_read_b64_tr_b16 a[2:3], v10 offset:4768\n" \
"v_mfma_f32_16x16x32_bf16 v[216:219], a[4:7], a[164:167], v[216:219]\n" \
"v_mfma_f32_16x16x32_bf16 v[220:223], a[4:7], a[180:183], v[220:223]\n" \
"ds_read_b64_tr_b16 a[4:5], v34 offset:160\n" \
"ds_read_b64_tr_b16 a[6:7], v34 offset:4768\n" \
"v_mfma_f32_16x16x32_bf16 v[216:219], a[8:11], a[168:171], v[216:219]\n" \
"v_mfma_f32_16x16x32_bf16 v[220:223], a[8:11], a[184:187], v[220:223]\n" \
"ds_read_b64_tr_b16 a[8:9], v34 offset:9376\n" \
"ds_read_b64_tr_b16 a[10:11], v34 offset:13984\n" \
"s_waitcnt vmcnt(10)\n" \
"s_mov_b64 exec, 1\n" \
"buffer_store_dword v39, off, s[56:59], s92 offset:32 sc1\n" \
"s_mov_b64 exec, -1\n" \
"v_mfma_f32_16x16x32_bf16 v[216:219], a[12:15], a[172:175], v[216:219]\n" \
"v_mfma_f32_16x16x32_bf16 v[220:223], a[12:15], a[188:191], v[220:223]\n" \
"ds_read_b64_tr_b16 a[12:13], v34 offset:18592\n" \
"s_waitcnt lgkmcnt(15)\n" \
"v_mfma_f32_16x16x32_bf16 v[216:219], a[16:19], a[96:99], v[216:219]\n" \
"ds_read_b64_tr_b16 a[14:15], v34 offset:23200\n" \
"v_mfma_f32_16x16x32_bf16 v[220:223], a[16:19], a[112:115], v[220:223]\n" \
"buffer_load_dwordx4 v[132:135], v20, s[56:59], s95 offen offset:0 sc1\n" \
"v_mfma_f32_16x16x32_bf16 v[216:219], a[20:23], a[100:103], v[216:219]\n" \
"ds_read_b64_tr_b16 a[16:17], v10 offset:9376\n" \
"ds_read_b64_tr_b16 a[18:19], v10 offset:13984\n" \
"v_mfma_f32_16x16x32_bf16 v[220:223], a[20:23], a[116:119], v[220:223]\n" \
"ds_read_b64_tr_b16 a[20:21], v33 offset:192\n" \
"ds_read_b64_tr_b16 a[22:23], v33 offset:4800\n" \
"v_mfma_f32_16x16x32_bf16 v[216:219], a[24:27], a[104:107], v[216:219]\n" \
"v_mfma_f32_16x16x32_bf16 v[220:223], a[24:27], a[120:123], v[220:223]\n" \
"ds_read_b64_tr_b16 a[24:25], v33 offset:9408\n" \
"ds_read_b64_tr_b16 a[26:27], v33 offset:14016\n" \
"v_mfma_f32_16x16x32_bf16 v[216:219], a[28:31], a[108:111], v[216:219]\n" \
"v_mfma_f32_16x16x32_bf16 v[220:223], a[28:31], a[124:127], v[220:223]\n" \
"buffer_load_dword v37, v40, s[56:59], s74 offen sc1\n" \
"v_mfma_f32_16x16x32_bf16 v[224:227], a[192:195], a[220:223], v[224:227]\n" \
"ds_read_b64_tr_b16 a[28:29], v33 offset:18624\n" \
"ds_read_b64_tr_b16 a[30:31], v33 offset:23232\n" \
"v_mfma_f32_16x16x32_bf16 v[228:231], a[192:195], a[236:239], v[228:231]\n" \
"ds_read_b64_tr_b16 a[192:193], v33 offset:27840\n" \
"ds_read_b64_tr_b16 a[194:195], v33 offset:32448\n" \
"v_cvt_pk_bf16_f32 v216, v216, v217\n" \
"v_cvt_pk_bf16_f32 v217, v218, v219\n" \
"v_cvt_pk_bf16_f32 v218, v220, v221\n" \
"v_cvt_pk_bf16_f32 v219, v222, v223\n" \
"buffer_store_dwordx4 v[216:219], v20, s[56:59], s69 offen offset:1024 sc1\n" \
"s_add_u32 s69, s69, 0x1800\n" \
"v_mfma_f32_16x16x32_bf16 v[224:227], a[196:199], a[224:227], v[224:227]\n" \
"v_mfma_f32_16x16x32_bf16 v[228:231], a[196:199], a[240:243], v[228:231]\n" \
"ds_read_b64_tr_b16 a[196:197], v33 offset:37056\n" \
"ds_read_b64_tr_b16 a[198:199], v33 offset:41664\n" \
"v_mfma_f32_16x16x32_bf16 v[224:227], a[200:203], a[228:231], v[224:227]\n" \
"v_mfma_f32_16x16x32_bf16 v[228:231], a[200:203], a[244:247], v[228:231]\n" \
"ds_read_b64_tr_b16 a[200:201], v33 offset:46272\n" \
"s_waitcnt lgkmcnt(13)\n" \
"v_mfma_f32_16x16x32_bf16 v[224:227], a[204:207], a[232:235], v[224:227]\n" \
"buffer_load_dwordx4 v[140:143], v20, s[56:59], s96 offen offset:2048 sc1\n" \
"v_mfma_f32_16x16x32_bf16 v[228:231], a[204:207], a[248:251], v[228:231]\n" \
"ds_read_b64_tr_b16 a[202:203], v33 offset:50880\n" \
"ds_read_b64_tr_b16 a[204:205], v33 offset:55488\n" \
"v_mfma_f32_16x16x32_bf16 v[224:227], a[208:211], a[160:163], v[224:227]\n" \
"ds_read_b64_tr_b16 a[206:207], v33 offset:60096\n" \
"v_mfma_f32_16x16x32_bf16 v[228:231], a[208:211], a[176:179], v[228:231]\n" \
"ds_read_b64_tr_b16 a[208:209], v10 offset:192\n" \
"ds_read_b64_tr_b16 a[210:211], v10 offset:4800\n" \
"v_mfma_f32_16x16x32_bf16 v[224:227], a[212:215], a[164:167], v[224:227]\n" \
"v_mfma_f32_16x16x32_bf16 v[228:231], a[212:215], a[180:183], v[228:231]\n" \
"ds_read_b64_tr_b16 a[212:213], v34 offset:192\n" \
"ds_read_b64_tr_b16 a[214:215], v34 offset:4800\n" \
"v_mfma_f32_16x16x32_bf16 v[224:227], a[216:219], a[168:171], v[224:227]\n" \
"v_mfma_f32_16x16x32_bf16 v[228:231], a[216:219], a[184:187], v[228:231]\n" \
"ds_read_b64_tr_b16 a[216:217], v34 offset:9408\n" \
"ds_read_b64_tr_b16 a[218:219], v34 offset:14016\n" \
"s_waitcnt vmcnt(10)\n" \
"s_mov_b64 exec, 1\n" \
"buffer_store_dword v39, off, s[56:59], s66 offset:36 sc1\n" \
"s_mov_b64 exec, -1\n" \
"v_mfma_f32_16x16x32_bf16 v[224:227], a[0:3], a[172:175], v[224:227]\n" \
"v_mfma_f32_16x16x32_bf16 v[228:231], a[0:3], a[188:191], v[228:231]\n" \
"ds_read_b64_tr_b16 a[0:1], v34 offset:18624\n" \
"ds_read_b64_tr_b16 a[2:3], v34 offset:23232\n" \
"v_mfma_f32_16x16x32_bf16 v[224:227], a[4:7], a[96:99], v[224:227]\n" \
"v_mfma_f32_16x16x32_bf16 v[228:231], a[4:7], a[112:115], v[228:231]\n" \
"ds_read_b64_tr_b16 a[4:5], v10 offset:9408\n" \
"ds_read_b64_tr_b16 a[6:7], v10 offset:14016\n" \
"v_mfma_f32_16x16x32_bf16 v[224:227], a[8:11], a[100:103], v[224:227]\n" \
"v_mfma_f32_16x16x32_bf16 v[228:231], a[8:11], a[116:119], v[228:231]\n" \
"buffer_load_dwordx4 v[148:151], v20, s[56:59], s97 offen offset:0 sc1\n" \
"v_mfma_f32_16x16x32_bf16 v[224:227], a[12:15], a[104:107], v[224:227]\n" \
"ds_read_b64_tr_b16 a[8:9], v33 offset:224\n" \
"ds_read_b64_tr_b16 a[10:11], v33 offset:4832\n" \
"v_mfma_f32_16x16x32_bf16 v[228:231], a[12:15], a[120:123], v[228:231]\n" \
"ds_read_b64_tr_b16 a[12:13], v33 offset:9440\n" \
"s_waitcnt lgkmcnt(13)\n" \
"v_mfma_f32_16x16x32_bf16 v[224:227], a[16:19], a[108:111], v[224:227]\n" \
"ds_read_b64_tr_b16 a[14:15], v33 offset:14048\n" \
"v_mfma_f32_16x16x32_bf16 v[228:231], a[16:19], a[124:127], v[228:231]\n" \
"ds_read_b64_tr_b16 a[16:17], v33 offset:18656\n" \
"ds_read_b64_tr_b16 a[18:19], v33 offset:23264\n" \
"v_mfma_f32_16x16x32_bf16 v[232:235], a[20:23], a[220:223], v[232:235]\n" \
"v_mfma_f32_16x16x32_bf16 v[236:239], a[20:23], a[236:239], v[236:239]\n" \
"ds_read_b64_tr_b16 a[20:21], v33 offset:27872\n" \
"ds_read_b64_tr_b16 a[22:23], v33 offset:32480\n" \
"v_cvt_pk_bf16_f32 v224, v224, v225\n" \
"v_cvt_pk_bf16_f32 v225, v226, v227\n" \
"v_cvt_pk_bf16_f32 v226, v228, v229\n" \
"v_cvt_pk_bf16_f32 v227, v230, v231\n" \
"v_mfma_f32_16x16x32_bf16 v[232:235], a[24:27], a[224:227], v[232:235]\n" \
"v_mfma_f32_16x16x32_bf16 v[236:239], a[24:27], a[240:243], v[236:239]\n" \
"ds_read_b64_tr_b16 a[24:25], v33 offset:37088\n" \
"ds_read_b64_tr_b16 a[26:27], v33 offset:41696\n" \
"v_mfma_f32_16x16x32_bf16 v[232:235], a[28:31], a[228:231], v[232:235]\n" \
"v_mfma_f32_16x16x32_bf16 v[236:239], a[28:31], a[244:247], v[236:239]\n" \
"ds_read_b64_tr_b16 a[28:29], v33 offset:46304\n" \
"ds_read_b64_tr_b16 a[30:31], v33 offset:50912\n" \
"v_mfma_f32_16x16x32_bf16 v[232:235], a[192:195], a[232:235], v[232:235]\n" \
"v_mfma_f32_16x16x32_bf16 v[236:239], a[192:195], a[248:251], v[236:239]\n" \
"ds_read_b64_tr_b16 a[192:193], v33 offset:55520\n" \
"ds_read_b64_tr_b16 a[194:195], v33 offset:60128\n" \
"v_mfma_f32_16x16x32_bf16 v[232:235], a[196:199], a[160:163], v[232:235]\n" \
"v_mfma_f32_16x16x32_bf16 v[236:239], a[196:199], a[176:179], v[236:239]\n" \
"buffer_load_dwordx4 v[156:159], v20, s[56:59], s64 offen offset:2048 sc1\n" \
"v_readlane_b32 s64, v11, 51\n" \
"v_mfma_f32_16x16x32_bf16 v[232:235], a[200:203], a[164:167], v[232:235]\n" \
"ds_read_b64_tr_b16 a[196:197], v10 offset:224\n" \
"ds_read_b64_tr_b16 a[198:199], v10 offset:4832\n" \
"v_mfma_f32_16x16x32_bf16 v[236:239], a[200:203], a[180:183], v[236:239]\n" \
"ds_read_b64_tr_b16 a[200:201], v34 offset:224\n" \
"ds_read_b64_tr_b16 a[202:203], v34 offset:4832\n" \
"buffer_store_dwordx4 v[224:227], v20, s[56:59], s64 offen offset:1024 sc1\n" \
"v_mfma_f32_16x16x32_bf16 v[232:235], a[204:207], a[168:171], v[232:235]\n" \
"s_waitcnt vmcnt(13)\n" \
"s_mov_b64 exec, 1\n" \
"buffer_store_dword v39, off, s[56:59], s67 offset:40 sc1\n" \
"s_mov_b64 exec, -1\n" \
"v_mfma_f32_16x16x32_bf16 v[236:239], a[204:207], a[184:187], v[236:239]\n" \
"ds_read_b64_tr_b16 a[204:205], v34 offset:9440\n" \
"s_waitcnt lgkmcnt(11)\n" \
"v_mfma_f32_16x16x32_bf16 v[232:235], a[208:211], a[172:175], v[232:235]\n" \
"ds_read_b64_tr_b16 a[206:207], v34 offset:14048\n" \
"v_mfma_f32_16x16x32_bf16 v[236:239], a[208:211], a[188:191], v[236:239]\n" \
"ds_read_b64_tr_b16 a[208:209], v34 offset:18656\n" \
"ds_read_b64_tr_b16 a[210:211], v34 offset:23264\n" \
"v_mfma_f32_16x16x32_bf16 v[232:235], a[212:215], a[96:99], v[232:235]\n" \
"v_mfma_f32_16x16x32_bf16 v[236:239], a[212:215], a[112:115], v[236:239]\n" \
"ds_read_b64_tr_b16 a[212:213], v10 offset:9440\n" \
"ds_read_b64_tr_b16 a[214:215], v10 offset:14048\n" \
"v_mfma_f32_16x16x32_bf16 v[232:235], a[216:219], a[100:103], v[232:235]\n" \
"v_mfma_f32_16x16x32_bf16 v[236:239], a[216:219], a[116:119], v[236:239]\n" \
"ds_read_b64_tr_b16 a[216:217], v36 offset:0\n" \
"ds_read_b64_tr_b16 a[218:219], v36 offset:1024\n" \
"v_mfma_f32_16x16x32_bf16 v[232:235], a[0:3], a[104:107], v[232:235]\n" \
"v_mfma_f32_16x16x32_bf16 v[236:239], a[0:3], a[120:123], v[236:239]\n" \
"ds_read_b64_tr_b16 a[0:1], v36 offset:2048\n" \
"ds_read_b64_tr_b16 a[2:3], v36 offset:3072\n" \
"v_mfma_f32_16x16x32_bf16 v[232:235], a[4:7], a[108:111], v[232:235]\n" \
"v_mfma_f32_16x16x32_bf16 v[236:239], a[4:7], a[124:127], v[236:239]\n" \
"buffer_load_dwordx4 v[164:167], v20, s[56:59], s65 offen offset:0 sc1\n" \
"v_readlane_b32 s65, v11, 52\n" \
"v_mfma_f32_16x16x32_bf16 v[240:243], a[8:11], a[220:223], v[240:243]\n" \
"ds_read_b64_tr_b16 a[4:5], v36 offset:32\n" \
"ds_read_b64_tr_b16 a[6:7], v36 offset:1056\n" \
"v_mfma_f32_16x16x32_bf16 v[244:247], a[8:11], a[236:239], v[244:247]\n" \
"ds_read_b64_tr_b16 a[8:9], v36 offset:2080\n" \
"v_cvt_pk_bf16_f32 v232, v232, v233\n" \
"v_cvt_pk_bf16_f32 v233, v234, v235\n" \
"v_cvt_pk_bf16_f32 v234, v236, v237\n" \
"v_cvt_pk_bf16_f32 v235, v238, v239\n" \
"buffer_store_dwordx4 v[232:235], v20, s[56:59], s65 offen offset:1024 sc1\n" \
"v_mfma_f32_16x16x32_bf16 v[240:243], a[12:15], a[224:227], v[240:243]\n" \
"ds_read_b64_tr_b16 a[10:11], v36 offset:3104\n" \
"v_mfma_f32_16x16x32_bf16 v[244:247], a[12:15], a[240:243], v[244:247]\n" \
"v_mfma_f32_16x16x32_bf16 v[240:243], a[16:19], a[228:231], v[240:243]\n" \
"v_mfma_f32_16x16x32_bf16 v[244:247], a[16:19], a[244:247], v[244:247]\n" \
"v_mfma_f32_16x16x32_bf16 v[240:243], a[20:23], a[232:235], v[240:243]\n" \
"v_mfma_f32_16x16x32_bf16 v[244:247], a[20:23], a[248:251], v[244:247]\n" \
"s_waitcnt lgkmcnt(8)\n" \
"v_mfma_f32_16x16x32_bf16 v[168:171], a[24:27], a[128:131], v[168:171]\n" \
"v_mfma_f32_16x16x32_bf16 v[172:175], a[24:27], a[144:147], v[172:175]\n" \
"v_mfma_f32_16x16x32_bf16 v[240:243], a[24:27], a[160:163], v[240:243]\n" \
"s_waitcnt vmcnt(9)\n" \
"v_cmp_eq_u32_e64 s[98:99], s62, v37\n" \
"buffer_load_dword v37, v40, s[56:59], s74 offen sc1\n" \
"s_mov_b64 exec, 1\n" \
"buffer_store_dword v39, off, s[56:59], s68 offset:44 sc1\n" \
"s_mov_b64 exec, -1\n" \
"s_or_b32 s75, 0x300, s100\n" \
"s_andn2_b32 s75, s75, s98\n" \
"s_mov_b32 s21, s75\n" \
"s_bitcmp1_b32 s21, 16\n" \
"s_cselect_b32 s70, 0x40000000, s16\n" \
"s_bitcmp1_b32 s21, 17\n" \
"s_cselect_b32 s71, 0x40000000, s16\n" \
"s_bitcmp1_b32 s21, 18\n" \
"s_cselect_b32 s72, 0x40000000, s17\n" \
"s_bitcmp1_b32 s21, 19\n" \
"s_cselect_b32 s73, 0x40000000, s17\n" \
"s_bitcmp1_b32 s21, 20\n" \
"s_cselect_b32 s92, 0x40000000, s18\n" \
"s_bitcmp1_b32 s21, 21\n" \
"s_cselect_b32 s93, 0x40000000, s18\n" \
"s_bitcmp1_b32 s21, 22\n" \
"s_cselect_b32 s94, 0x40000000, s19\n" \
"s_bitcmp1_b32 s21, 23\n" \
"s_cselect_b32 s95, 0x40000000, s19\n" \
"s_bitcmp1_b32 s21, 8\n" \
"s_cselect_b32 s96, 0x40000000, s12\n" \
"s_bitcmp1_b32 s21, 9\n" \
"s_cselect_b32 s97, 0x40000000, s12\n" \
"s_add_u32 s64, s64, 0x1000\n" \
"s_add_u32 s65, s65, 0x800\n" \
"v_mfma_f32_16x16x32_bf16 v[244:247], a[24:27], a[176:179], v[244:247]\n" \
"buffer_load_dwordx4 v[112:115], v20, s[56:59], s70 offen offset:0 sc1\n" \
"v_mfma_f32_16x16x32_bf16 v[168:171], a[28:31], a[132:135], v[168:171]\n" \
"v_mfma_f32_16x16x32_bf16 v[172:175], a[28:31], a[148:151], v[172:175]\n" \
"v_mfma_f32_16x16x32_bf16 v[240:243], a[28:31], a[164:167], v[240:243]\n" \
"v_mfma_f32_16x16x32_bf16 v[244:247], a[28:31], a[180:183], v[244:247]\n" \
"buffer_load_dwordx4 v[184:187], v20, s[56:59], s71 offen offset:2048 sc1\n" \
"v_mfma_f32_16x16x32_bf16 v[168:171], a[192:195], a[136:139], v[168:171]\n" \
"v_readlane_b32 s71, v11, 54\n" \
"v_mfma_f32_16x16x32_bf16 v[172:175], a[192:195], a[152:155], v[172:175]\n" \
"v_mfma_f32_16x16x32_bf16 v[240:243], a[192:195], a[168:171], v[240:243]\n" \
"v_mfma_f32_16x16x32_bf16 v[244:247], a[192:195], a[184:187], v[244:247]\n" \
"buffer_load_dwordx4 v[120:123], v20, s[56:59], s72 offen offset:0 sc1\n" \
"v_mfma_f32_16x16x32_bf16 v[168:171], a[196:199], a[140:143], v[168:171]\n" \
"v_readlane_b32 s72, v11, 45\n" \
"v_mfma_f32_16x16x32_bf16 v[172:175], a[196:199], a[156:159], v[172:175]\n" \
"v_mfma_f32_16x16x32_bf16 v[240:243], a[196:199], a[172:175], v[240:243]\n" \
"v_mfma_f32_16x16x32_bf16 v[244:247], a[196:199], a[188:191], v[244:247]\n" \
"buffer_load_dwordx4 v[192:195], v20, s[56:59], s73 offen offset:2048 sc1\n" \
"v_mfma_f32_16x16x32_bf16 v[168:171], a[200:203], a[32:35], v[168:171]\n" \
"v_readlane_b32 s73, v11, 43\n" \
"v_mfma_f32_16x16x32_bf16 v[172:175], a[200:203], a[48:51], v[172:175]\n" \
"v_mfma_f32_16x16x32_bf16 v[240:243], a[200:203], a[96:99], v[240:243]\n" \
"v_mfma_f32_16x16x32_bf16 v[244:247], a[200:203], a[112:115], v[244:247]\n" \
"buffer_load_dwordx4 v[128:131], v20, s[56:59], s92 offen offset:0 sc1\n" \
"v_mfma_f32_16x16x32_bf16 v[168:171], a[204:207], a[36:39], v[168:171]\n" \
"v_readlane_b32 s92, v11, 44\n" \
"v_mfma_f32_16x16x32_bf16 v[172:175], a[204:207], a[52:55], v[172:175]\n" \
"v_mfma_f32_16x16x32_bf16 v[240:243], a[204:207], a[100:103], v[240:243]\n" \
"v_mfma_f32_16x16x32_bf16 v[244:247], a[204:207], a[116:119], v[244:247]\n" \
"buffer_load_dwordx4 v[200:203], v20, s[56:59], s93 offen offset:2048 sc1\n" \
"v_mfma_f32_16x16x32_bf16 v[168:171], a[208:211], a[40:43], v[168:171]\n" \
"v_mfma_f32_16x16x32_bf16 v[172:175], a[208:211], a[56:59], v[172:175]\n" \
"v_mfma_f32_16x16x32_bf16 v[240:243], a[208:211], a[104:107], v[240:243]\n" \
"s_waitcnt vmcnt(16)\n" \
"s_mov_b64 exec, 1\n" \
"buffer_store_dword v39, off, s[56:59], s69 offset:48 sc1\n" \
".fill (L_ab + 45056 - .) / 4, 4, 0xBF800000\n" \
"L_ap11:\n" \
"s_cbranch_execz L_ap0\n" \
"s_mov_b64 exec, -1\n" \
"v_mfma_f32_16x16x32_bf16 v[244:247], a[208:211], a[120:123], v[244:247]\n" \
"v_mfma_f32_16x16x32_bf16 v[168:171], a[212:215], a[44:47], v[168:171]\n" \
"v_mfma_f32_16x16x32_bf16 v[172:175], a[212:215], a[60:63], v[172:175]\n" \
"v_mfma_f32_16x16x32_bf16 v[240:243], a[212:215], a[108:111], v[240:243]\n" \
"buffer_load_dwordx4 v[176:179], v20, s[56:59], s94 offen offset:0 sc1\n" \
"v_mfma_f32_16x16x32_bf16 v[244:247], a[212:215], a[124:127], v[244:247]\n" \
"s_waitcnt lgkmcnt(0)\n" \
"v_mfma_f32_16x16x32_bf16 v[168:171], a[216:219], v[64:67], v[168:171]\n" \
"v_mfma_f32_16x16x32_bf16 v[172:175], a[216:219], v[72:75], v[172:175]\n" \
"v_mfma_f32_16x16x32_bf16 v[240:243], a[216:219], v[80:83], v[240:243]\n" \
"buffer_load_dwordx4 v[248:251], v20, s[56:59], s95 offen offset:2048 sc1\n" \
"v_mfma_f32_16x16x32_bf16 v[244:247], a[216:219], v[88:91], v[244:247]\n" \
"v_mfma_f32_16x16x32_bf16 v[168:171], a[0:3], v[68:71], v[168:171]\n" \
"v_mfma_f32_16x16x32_bf16 v[172:175], a[0:3], v[76:79], v[172:175]\n" \
"v_mfma_f32_16x16x32_bf16 v[240:243], a[0:3], v[84:87], v[240:243]\n" \
"buffer_load_dwordx4 v[188:191], v20, s[56:59], s96 offen offset:1024 sc1\n" \
"v_mfma_f32_16x16x32_bf16 v[244:247], a[0:3], v[92:95], v[244:247]\n" \
"v_mfma_f32_16x16x32_bf16 v[56:59], a[4:7], v[96:99], 0\n" \
"v_mfma_f32_16x16x32_bf16 v[60:63], a[4:7], v[104:107], 0\n" \
"v_mfma_f32_16x16x32_bf16 v[56:59], a[8:11], v[100:103], v[56:59]\n" \
"buffer_load_dwordx4 v[196:199], v20, s[56:59], s97 offen offset:3072 sc1\n" \
"v_mfma_f32_16x16x32_bf16 v[60:63], a[8:11], v[108:111], v[60:63]\n" \
"s_waitcnt vmcnt(16)\n" \
"s_mov_b64 exec, 1\n" \
"buffer_store_dword v39, off, s[56:59], s64 offset:52 sc1\n" \
"s_mov_b64 exec, -1\n" \
"s_waitcnt vmcnt(14)\n" \
"s_mov_b64 exec, 1\n" \
"buffer_store_dword v39, off, s[56:59], s65 offset:56 sc1\n" \
"s_mov_b64 exec, -1\n" \
"s_cmp_lg_u32 s20, 0\n" \
"s_cbranch_scc1 L_sna_%=\n" \
"L_rna_%=:\n" \
"s_cmp_lg_u32 s21, 0\n" \
"s_cbranch_scc1 L_snx_%=\n" \
"L_rnx_%=:\n" \
"s_waitcnt vmcnt(14)\n" \
"v_cmp_eq_u32_e64 s[98:99], s62, v37\n" \
"v_mfma_f32_16x16x32_bf16 v[168:171], v[50:53], v[116:119], v[168:171]\n" \
"s_andn2_b32 s75, 0x400, s98\n" \
"s_cbranch_scc1 L_sx0_%=\n" \
"L_rx0_%=:\n" \
"s_add_u32 s66, s63, 0x1000\n" \
"v_mfma_f32_16x16x32_bf16 v[172:175], v[48:51], v[116:119], v[172:175]\n" \
"buffer_load_dwordx4 v[204:207], v20, s[56:59], s66 offen offset:1024 sc1\n" \
"s_andn2_b32 s75, 0x800, s98\n" \
"s_cbranch_scc1 L_sx1_%=\n" \
"L_rx1_%=:\n" \
"s_add_u32 s67, s63, 0x1800\n" \
"v_mfma_f32_16x16x32_bf16 v[168:171], v[50:53], v[124:127], v[168:171]\n" \
"v_mfma_f32_16x16x32_bf16 v[172:175], v[48:51], v[124:127], v[172:175]\n" \
"v_mfma_f32_16x16x32_bf16 v[168:171], v[50:53], v[132:135], v[168:171]\n" \
"buffer_load_dwordx4 v[212:215], v20, s[56:59], s67 offen offset:1024 sc1\n" \
"s_andn2_b32 s75, 0x1000, s98\n" \
"s_cbranch_scc1 L_sx2_%=\n" \
"L_rx2_%=:\n" \
"s_add_u32 s68, s63, 0x2000\n" \
"v_mfma_f32_16x16x32_bf16 v[172:175], v[48:51], v[132:135], v[172:175]\n" \
"v_mfma_f32_16x16x32_bf16 v[168:171], v[50:53], v[140:143], v[168:171]\n" \
"v_mfma_f32_16x16x32_bf16 v[172:175], v[48:51], v[140:143], v[172:175]\n" \
"buffer_load_dwordx4 v[220:223], v20, s[56:59], s68 offen offset:1024 sc1\n" \
"s_andn2_b32 s75, 0x2000, s98\n" \
"s_cbranch_scc1 L_sx3_%=\n" \
"L_rx3_%=:\n" \
"s_add_u32 s69, s63, 0x2800\n" \
"v_mfma_f32_16x16x32_bf16 v[168:171], v[50:53], v[148:151], v[168:171]\n" \
"v_mfma_f32_16x16x32_bf16 v[172:175], v[48:51], v[148:151], v[172:175]\n" \
"v_mfma_f32_16x16x32_bf16 v[168:171], v[50:53], v[156:159], v[168:171]\n" \
"buffer_load_dwordx4 v[228:231], v20, s[56:59], s69 offen offset:1024 sc1\n" \
"s_andn2_b32 s75, 0x4000, s98\n" \
"s_cbranch_scc1 L_sx4_%=\n" \
"L_rx4_%=:\n" \
"s_add_u32 s70, s63, 0x3000\n" \
"v_mfma_f32_16x16x32_bf16 v[172:175], v[48:51], v[156:159], v[172:175]\n" \
"v_mfma_f32_16x16x32_bf16 v[168:171], v[50:53], v[164:167], v[168:171]\n" \
"v_mfma_f32_16x16x32_bf16 v[172:175], v[48:51], v[164:167], v[172:175]\n" \
"buffer_load_dwordx4 v[236:239], v20, s[56:59], s70 offen offset:1024 sc1\n" \
"s_waitcnt vmcnt(17)\n" \
"v_mfma_f32_16x16x32_bf16 v[56:59], v[50:53], v[112:115], v[56:59]\n" \
"v_mfma_f32_16x16x32_bf16 v[60:63], v[48:51], v[112:115], v[60:63]\n" \
"s_waitcnt vmcnt(16)\n" \
"v_mfma_f32_16x16x32_bf16 v[56:59], v[50:53], v[184:187], v[56:59]\n" \
"v_mfma_f32_16x16x32_bf16 v[60:63], v[48:51], v[184:187], v[60:63]\n" \
"v_cvt_pk_bf16_f32 v168, v168, v169\n" \
"s_waitcnt vmcnt(15)\n" \
"v_mfma_f32_16x16x32_bf16 v[56:59], v[50:53], v[120:123], v[56:59]\n" \
"v_cvt_pk_bf16_f32 v172, v172, v173\n" \
"v_cvt_pk_bf16_f32 v169, v170, v171\n" \
"v_mfma_f32_16x16x32_bf16 v[60:63], v[48:51], v[120:123], v[60:63]\n" \
"v_cvt_pk_bf16_f32 v173, v174, v175\n" \
"s_waitcnt vmcnt(14)\n" \
"v_mfma_f32_16x16x32_bf16 v[56:59], v[50:53], v[192:195], v[56:59]\n" \
"buffer_store_dwordx2 v[168:169], v42, s[88:91], s73 offen sc1\n" \
"v_mfma_f32_16x16x32_bf16 v[60:63], v[48:51], v[192:195], v[60:63]\n" \
"s_waitcnt vmcnt(14)\n" \
"v_mfma_f32_16x16x32_bf16 v[56:59], v[50:53], v[128:131], v[56:59]\n" \
"buffer_store_dwordx2 v[172:173], v43, s[88:91], s73 offen sc1\n" \
"v_mfma_f32_16x16x32_bf16 v[60:63], v[48:51], v[128:131], v[60:63]\n" \
"s_waitcnt vmcnt(14)\n" \
"v_mfma_f32_16x16x32_bf16 v[56:59], v[50:53], v[200:203], v[56:59]\n" \
"v_mfma_f32_16x16x32_bf16 v[60:63], v[48:51], v[200:203], v[60:63]\n" \
"s_waitcnt vmcnt(12)\n" \
"v_mfma_f32_16x16x32_bf16 v[56:59], v[50:53], v[176:179], v[56:59]\n" \
"v_mfma_f32_16x16x32_bf16 v[60:63], v[48:51], v[176:179], v[60:63]\n" \
"s_waitcnt vmcnt(11)\n" \
"v_mfma_f32_16x16x32_bf16 v[56:59], v[50:53], v[248:251], v[56:59]\n" \
"v_mfma_f32_16x16x32_bf16 v[60:63], v[48:51], v[248:251], v[60:63]\n" \
"s_waitcnt vmcnt(10)\n" \
"v_mfma_f32_16x16x32_bf16 v[240:243], v[50:53], v[188:191], v[240:243]\n" \
"v_mfma_f32_16x16x32_bf16 v[244:247], v[48:51], v[188:191], v[244:247]\n" \
"s_waitcnt vmcnt(9)\n" \
"v_mfma_f32_16x16x32_bf16 v[240:243], v[50:53], v[196:199], v[240:243]\n" \
"v_mfma_f32_16x16x32_bf16 v[244:247], v[48:51], v[196:199], v[244:247]\n" \
"s_mov_b64 exec, 1\n" \
"buffer_store_dword v46, off, s[56:59], s71 offset:0 sc1\n" \
"s_mov_b64 exec, -1\n" \
"v_cvt_pk_bf16_f32 v56, v56, v57\n" \
"v_cvt_pk_bf16_f32 v60, v60, v61\n" \
"v_cvt_pk_bf16_f32 v57, v58, v59\n" \
"v_cvt_pk_bf16_f32 v61, v62, v63\n" \
"buffer_store_dwordx2 v[56:57], v42, s[88:91], s72 offen sc1\n" \
"buffer_store_dwordx2 v[60:61], v43, s[88:91], s72 offen sc1\n" \
"s_waitcnt vmcnt(9)\n" \
"v_mfma_f32_16x16x32_bf16 v[240:243], v[50:53], v[204:207], v[240:243]\n" \
"v_mfma_f32_16x16x32_bf16 v[244:247], v[48:51], v[204:207], v[244:247]\n" \
"s_waitcnt vmcnt(8)\n" \
"v_mfma_f32_16x16x32_bf16 v[240:243], v[50:53], v[212:215], v[240:243]\n" \
"v_mfma_f32_16x16x32_bf16 v[244:247], v[48:51], v[212:215], v[244:247]\n" \
"s_waitcnt vmcnt(7)\n" \
"v_mfma_f32_16x16x32_bf16 v[240:243], v[50:53], v[220:223], v[240:243]\n" \
"v_mfma_f32_16x16x32_bf16 v[244:247], v[48:51], v[220:223], v[244:247]\n" \
"s_waitcnt vmcnt(6)\n" \
"v_mfma_f32_16x16x32_bf16 v[240:243], v[50:53], v[228:231], v[240:243]\n" \
"v_mfma_f32_16x16x32_bf16 v[244:247], v[48:51], v[228:231], v[244:247]\n" \
"s_waitcnt vmcnt(5)\n" \
"v_mfma_f32_16x16x32_bf16 v[240:243], v[50:53], v[236:239], v[240:243]\n" \
"v_mfma_f32_16x16x32_bf16 v[244:247], v[48:51], v[236:239], v[244:247]\n" \
"s_nop 6\n" \
"v_cvt_pk_bf16_f32 v240, v240, v241\n" \
"v_cvt_pk_bf16_f32 v244, v244, v245\n" \
"v_cvt_pk_bf16_f32 v241, v242, v243\n" \
"v_cvt_pk_bf16_f32 v245, v246, v247\n" \
"buffer_store_dwordx2 v[240:241], v42, s[88:91], s92 offen sc1\n" \
"buffer_store_dwordx2 v[244:245], v43, s[88:91], s92 offen sc1\n"
#define ATT_VCLOB "v21","v22","v23","v24","v25","v26","v27","v28","v29","v32","v33","v34","v35","v36","v37","v38","v39","v40","v41","v42","v43","v44","v45","v46","v47","v48","v49","v50","v51","v52","v53","v54","v55","v56","v57","v58","v59","v60","v61","v62","v63","v64","v65","v66","v67","v68","v69","v70","v71","v72","v73","v74","v75","v76","v77","v78","v79","v80","v81","v82","v83","v84","v85","v86","v87","v88","v89","v90","v91","v92","v93","v94","v95","v96","v97","v98","v99","v100","v101","v102","v103","v104","v105","v106","v107","v108","v109","v110","v111","v112","v113","v114","v115","v116","v117","v118","v119","v120","v121","v122","v123","v124","v125","v126","v127","v128","v129","v130","v131","v132","v133","v134","v135","v136","v137","v138","v139","v140","v141","v142","v143","v144","v145","v146","v147","v148","v149","v150","v151","v152","v153","v154","v155","v156","v157","v158","v159","v160","v161","v162","v163","v164","v165","v166","v167","v168","v169","v170","v171","v172","v173","v174","v175","v176","v177","v178","v179","v180","v181","v182","v183","v184","v185","v186","v187","v188","v189","v190","v191","v192","v193","v194","v195","v196","v197","v198","v199","v200","v201","v202","v203","v204","v205","v206","v207","v208","v209","v210","v211","v212","v213","v214","v215","v216","v217","v218","v219","v220","v221","v222","v223","v224","v225","v226","v227","v228","v229","v230","v231","v232","v233","v234","v235","v236","v237","v238","v239","v240","v241","v242","v243","v244","v245","v246","v247","v248","v249","v250","v251","v252","v253","v254","v255"
#define ATT_SCLOB "s63","s64","s65","s66","s67","s68","s69","s70","s71","s72","s73","s74","s75","s76","s77","s78","s79","s80","s81","s82","s83","s84","s85","s86","s87","s88","s89","s90","s91","s92","s93","s95","s96","s97","s98","s99","s100","s101"
#define ATT_WTCH 0
#define ATT_PEEL 1
#define ATT_PEEL_FOW 0
enum { T_fo, T_fj, T_fa, T_fas, T_fb, T_fbs, T_f8, T_mk8, T_wt, T_wol, T_woh, T_wbl, T_wbh, T_outl, T_outh, T_r1l, T_r1h, T_r1n, T_r1s, T_m0, T_m1, T_m1b, T_mw, T_m3, T_m4, T_ml, T_w0, T_w1, T_w2, T_w3, T_A1, T_A2, T_A3, T_B1, T_B2, T_B3, T_Ao, T_Bo, T_Al, T_Bl, T_l8, T_xr, T_x8r, T_oA, T_oB, T_o8, T_x0, T_x1, T_x2, T_x3, T_x4, T_x5, T_x6, T_x8, T_ep };

__device__ __forceinline__ void out_consts(u32x4 (&vp)[3], const u16* wo, const u16* wob, const u16* out, const unsigned b, const unsigned w, const unsigned lane) {
    const unsigned sp = b & 7u, nt = (b >> 3) & 3u, j = b >> 5, head = 4u * sp + nt, OOR = 0x40000000u;
    unsigned vw[9];
#pragma unroll
    for (unsigned i = 0; i < 9u; ++i) {
        const unsigned q = 64u * i + lane, row = __umul24(q, 3641u) >> 16, c = q - __umul24(row, 18u);
        vw[i] = __umul24(row, DM * 2) + (c < 16u ? (c * 16u + sp * 32u + 32u) & 255u : c * 16u) - (i & 3u) * 1024u;
    }
    const unsigned row = 16u * w + (lane >> 2), r9 = __umul24(16u * (row >> 5) + (row & 12u) + 2u * ((row >> 4) & 1u) + ((row >> 1) & 1u), 18u) + 16u + (row & 1u);
    unsigned vwl = __umul24(r9, DM * 2) + ((lane & 2u) ? 256u : sp * 32u) + (lane & 1u) * 16u, vrd = __umul24(lane >> 2, 288u) + (lane & 3u) * 8u;
    unsigned tab = 0;
#define TB(n, v) asm("v_writelane_b32 %0, %1, %2" : "+v"(tab) : "s"((unsigned)(v)), "n"((int)T_##n))
    const unsigned FJ = (unsigned)WS_F + (F_J + 640u * j) * 4u, mt = 2u * (w & 2u) + (w & 1u), w2 = w < 2u ? w : 2u, hw = head * 147456u;
    const unsigned FR = (unsigned)WS_F + F_R * 4u + (8u * j + sp) * 1024u;
    TB(fo, FJ + (ATT_PEEL_FOW ? 16u * head + 4u * w : 4u * head)); TB(fj, FJ); TB(fa, FR + (w | 2u) * 256u); TB(fas, FR + w * 256u + nt * 4u);
    TB(fb, FR + 128u + w * 256u); TB(fbs, FR + 128u + w * 256u + nt * 4u);
    TB(ep, ssel(b + w, OOR, (unsigned)WS_F + F_EPOCH * 4u));
    const u16* pwo = wo + nt * 144u; const u16* pwb = wob + nt * 144u;
    TB(wol, PLO(pwo)); TB(woh, PHI(pwo) & 0xffffu); TB(wbl, PLO(pwb)); TB(wbh, PHI(pwb) & 0xffffu); TB(outl, PLO(out)); TB(outh, PHI(out) & 0xffffu); TB(r1l, sslt<3>(w, PLO(pwo), PLO(pwb))); TB(r1h, sslt<3>(w, PHI(pwo), PHI(pwb)) & 0xffffu);
    TB(r1n, sslt<3>(w, 72u * sp + 71u, 4095u) * 1152u + 288u); TB(r1s, sslt<3>(w, (72u * sp + 32u * w) * 1152u, hw + 110592u));
    TB(m0, 131072u + 9216u * w2); TB(w0, hw + 36864u * w2); TB(m1, ATT_PEEL ? sslt<3>(w, 41984u + 9216u * w, 32768u) : 24576u + 9216u * w); TB(m1b, ssel(w ^ 2u, sslt<3>(w, 41984u + 9216u * w, 32768u), 114688u - 4096u)); TB(mw, 65536u + 9216u * w); TB(m3, sslt<3>(w, 102400u + 9216u * w, 0u)); TB(m4, sslt<3>(w, 131072u + 9216u * w, 9216u)); TB(ml, (ATT_PEEL ? 18432u : 61440u) + 1024u * w);
    const unsigned tA = (unsigned)WS_AF + (8u * j + mt) * (KS_TOT * 1024u), tB = (unsigned)WS_AF + (120u - 8u * j + mt) * (KS_TOT * 1024u);
#define HK(k) (4u * sp + ((nt + k) & 3u))
    TB(w1, HK(1u) * 147456u + w * 36864u); TB(w2, HK(2u) * 147456u + w * 36864u); TB(w3, HK(3u) * 147456u + w * 36864u);
    TB(A1, tA + 18432u + HK(1u) * 4096u); TB(A2, tA + 18432u + HK(2u) * 4096u); TB(A3, tA + 18432u + HK(3u) * 4096u);
    TB(B1, tB + 18432u + HK(1u) * 4096u); TB(B2, tB + 18432u + HK(2u) * 4096u); TB(B3, tB + 18432u + HK(3u) * 4096u);
    TB(Ao, tA + sp * 2048u); TB(Bo, tB + sp * 2048u); TB(Al, tA + 16384u); TB(Bl, tB + 16384u);
    const unsigned nred = w ^ (sp >> 1), blk = sp & 1u;
    const unsigned xg = (unsigned)WS_X + (4u * j + nt) * 589824u + w * 16384u, oA = (128u * j + 16u * mt) * 1152u + nt * 288u, oB = (1920u - 128u * j + 16u * mt) * 1152u + nt * 288u;
    TB(l8, ssel(nred, OOR, ssel(blk, tB, tA) + 16384u)); TB(xr, xg + sp * 65536u); TB(x8r, ssel(nred, OOR, xg + 524288u + (sp & 1u) * 1024u));
    TB(oA, oA + sp * 32u); TB(oB, oB + sp * 32u); TB(o8, ssel(nred, OOR, ssel(blk, oB, oA) + 256u)); TB(x8, xg + 524288u + sp * 2048u);
    TB(f8, xg + w * 131072u + 14400u + sp * 4u); TB(mk8, ssel(nred, 0u, 0xff0000u)); TB(wt, hw + j * 18432u + w * 4608u);
#define XI(i) TB(x##i, xg + ((sp + 1u + i) & 7u) * 65536u + i * 2048u)
    XI(0); XI(1); XI(2); XI(3); XI(4); XI(5); XI(6);
#undef XI
#undef HK
#undef TB
    vp[0] = (u32x4){vw[0], vw[1], vw[2], vw[3]}; vp[1] = (u32x4){vw[4], vw[5], vw[6], vw[7]}; vp[2] = (u32x4){vw[8], vwl, vrd, tab};
}
__global__ __launch_bounds__(256, 1) __attribute__((aligned(4096))) void fused_kernel(const u16* __restrict__ x, const u16* __restrict__ ckv, const u16* __restrict__ wq, const u16* __restrict__ wuk,
                                                       const u16* __restrict__ wuv, const u16* __restrict__ wqkv, const u16* __restrict__ gw,
                                                       const u16* __restrict__ wo, const u16* __restrict__ wob, char* __restrict__ ws_, u16* __restrict__ out) {
    using namespace att;
    extern __shared__ __attribute__((aligned(16))) char lds[];
    u32x4 sa[5]; u32x2 s24, s28; char* ws; unsigned tdum; kaptr ka = (kaptr)__builtin_amdgcn_kernarg_segment_ptr();
    asm volatile("s_load_dwordx8 s[8:15], %[ka], 0x0\n\ts_load_dwordx8 s[16:23], %[ka], 0x20\n\ts_getpc_b64 s[4:5]\n\tv_lshlrev_b32_e32 v191, 7, %[t]\n\ts_bfe_u32 s6, %[b], 0x20003\n\t"
                 "v_lshl_add_u32 v191, s6, 15, v191\n\tv_min_u32_e32 v191, %[e], v191\n\tglobal_load_dword v191, v191, s[4:5]\n\ts_load_dwordx4 s[24:27], %[ka], 0x40\n\ts_load_dwordx2 s[28:29], %[ka], 0x50"
                 : "={s[8:11]}"(sa[0]), "={s[12:15]}"(sa[1]), "={s[16:19]}"(sa[2]), "={s[20:23]}"(sa[3]), "={s[24:25]}"(s24), "={s[26:27]}"(ws), "={s[28:29]}"(s28), "={v191}"(tdum), [ka] "+{s[0:1]}"(ka)
                 : [t] "v"(threadIdx.x), [b] "s"(blockIdx.x), [e] "i"(CT_END) : "s4", "s5", "s6", "scc");
    SBAR();
    const unsigned wid = rfl(threadIdx.x >> 6);
    unsigned tag, vl;
    const int b = blockIdx.x;
    u32x4 vp[5], srdA; u32x2 sd;
    proj_phase(ka, ws, wid, vp, vl, sa, s24, s28, tdum, srdA, sd, tag
    );
    unsigned* const flags = (unsigned*)(ws + WS_F);
    RTS(2);
#define AST(L) do {} while (0)
#define ATT_TAIL "s_endpgm\n"
    unsigned abad;
    asm volatile(ATT_ASM ATT_TAIL "L_end_%=:\n s_nop 1"
                 : "={s94}"(abad)
                 : "{v[0:3]}"(vp[0]), "{v[4:7]}"(vp[1]), "{v[8:11]}"(vp[2]), "{v[12:15]}"(vp[3]), "{v[16:19]}"(vp[4]), "{v20}"(vl), "{s[56:59]}"(srdA), "{s[60:61]}"(sd), "{s62}"(tag),
                   "{s[8:11]}"(sa[0]), "{s[12:15]}"(sa[1]), "{s[16:19]}"(sa[2]), "{s[20:23]}"(sa[3]), "{s24}"(sa[4][0]), "{s3}"(wid), "{s[28:29]}"(s28)
                 : "memory", "m0", "vcc", "scc", ATT_VCLOB, ATT_SCLOB, ACLOB_LO, ACLOB_HI);
    {
        unsigned b2 = b, w2 = wid;
        asm volatile("s_waitcnt vmcnt(0)" : "+s"(ka), "+s"(b2), "+s"(w2), "+s"(ws) :: "memory");
        const unsigned j2 = b2 >> 5, p2 = w2 & 1u, h2 = (b2 & 7u) * 4u + ((b2 >> 3) & 3u);
#pragma unroll 1
        for (unsigned i = w2 & 2u; i < ((w2 & 2u) ? (abad ? 8u : 4u) : 2u); ++i)
            att_exact(ka[0], ka[2], ws, h2, (i < 4u ? 1920u - 128u * j2 : 128u * j2 - 128u) + 32u * i + 16u * p2, *reinterpret_cast<const float*>(lds + LDS_G + (32u * i + 16u * p2 + (lane_id() & 15u)) * 4u));
        asm volatile("s_waitcnt vmcnt(0)" ::: "memory");
        unsigned* fa = (unsigned*)(ws + WS_F) + F_R + (8u * j2 + (b2 & 7u)) * 256u + (2u + p2) * 64u + ((b2 >> 3) & 3u);
        if (w2 & 2u) st_flag(fa, tag);
        wait_flags(fa, ld_flag(fa), tag);
        const unsigned l2 = lane_id(); u32x4 c[3]; out_consts(c, ka[7], ka[8], ka[10], b2, w2, l2);
        asm volatile("s_waitcnt vmcnt(0) lgkmcnt(0)\n\ts_nop 7\n\ts_branch L_att_stub" :: "{v[0:3]}"(c[0]), "{v[4:7]}"(c[1]), "{v[8:11]}"(c[2]), "{v20}"(l2 * 16u), "{s[56:59]}"(SRDA), "{s60}"(SD(8u * j2, h2, w2)),
                     "{s61}"(SD(120u - 8u * j2, h2, w2)), "{s62}"(tag), "{s3}"(w2) : "memory");
    }
    RTS(5);
}

extern "C" void kernel_launch(void* const* d_in, const int* in_sizes, int n_in, void* d_out, int out_size, void* d_ws, size_t ws_size,
                              hipStream_t stream) {
    (void)hipFuncSetAttribute((const void*)fused_kernel, hipFuncAttributeMaxDynamicSharedMemorySize, att::LDS_BYTES);
    fused_kernel<<<dim3(256), 256, att::LDS_BYTES, stream>>>((const u16*)d_in[0], (const u16*)d_in[4], (const u16*)d_in[5], (const u16*)d_in[6], (const u16*)d_in[7], (const u16*)d_in[1],
                                                             (const u16*)d_in[9], (const u16*)d_in[2], (const u16*)d_in[8], (char*)d_ws, (u16*)d_out);
}
```

```cpp
#include <hip/hip_runtime.h>
#include <hip/hip_bf16.h>
#include <stdint.h>
#include <utility>

typedef short bf16x8 __attribute__((ext_vector_type(8)));
typedef short s16x4 __attribute__((ext_vector_type(4)));
typedef float f32x16 __attribute__((ext_vector_type(16)));
typedef float f32x4 __attribute__((ext_vector_type(4)));
typedef unsigned u32x4 __attribute__((ext_vector_type(4)));
typedef unsigned u32x2 __attribute__((ext_vector_type(2)));
typedef unsigned short u16;

constexpr int S = 2048, DM = 576, NH = 32, HD = 128, DC = 512, NQKV = 1728, DHA = 18;
constexpr int NBIG = NH * HD;
constexpr int KS_TOT = 146;

#define SBAR() __builtin_amdgcn_sched_barrier(0)
constexpr size_t WS_Q = 0, WS_K = WS_Q + (size_t)S * NBIG * 2, WS_V = WS_K + (size_t)S * NBIG * 2, WS_AF = WS_V + (size_t)S * NBIG * 2, WS_X = WS_AF + (size_t)128 * KS_TOT * 1024,
                 WS_P = WS_X + (size_t)64 * 16 * 9216, WS_F = WS_P + (size_t)128 * (4 * 32 * 256 + 1024) * 4;
constexpr size_t WS_L = (size_t)96 << 20;
static_assert(WS_F == 96206848, "workspace map is baked into the generated asm");
constexpr int F_EPOCH = 0, F_J = 1024, F_ST = 8192, F_KV = 40960, F_R = 65536;
constexpr unsigned TAG_STEP = 0x9E3779B1u;
__device__ __forceinline__ unsigned ld_flag(const unsigned* p) { return __hip_atomic_load(p, __ATOMIC_RELAXED, __HIP_MEMORY_SCOPE_AGENT); }
__device__ __forceinline__ void st_flag(unsigned* p, unsigned v) { __hip_atomic_store(p, v, __ATOMIC_RELAXED, __HIP_MEMORY_SCOPE_AGENT); }
__device__ __forceinline__ unsigned ld_flag_async(const unsigned* p) { unsigned v; asm volatile("global_load_dword %0, %1, off sc1" : "=v"(v) : "v"(p) : "memory"); return v; }
#define WAITV(v, N) asm volatile("s_waitcnt vmcnt(" #N ")" : "+v"(v) :: "memory")
__device__ __forceinline__ void wait_flags(const unsigned* p, unsigned v, unsigned tag) {
    if (__builtin_expect(__builtin_amdgcn_ballot_w64(v != tag) != 0ull, 0)) {
        unsigned spins = 0;
        do { __builtin_amdgcn_s_sleep(1); } while (__builtin_amdgcn_ballot_w64(ld_flag(p) != tag) != 0ull && ++spins < (1u << 22));
    }
    asm volatile("" ::: "memory");
}

__device__ __forceinline__ unsigned cvtpk(float lo, float hi) {
    unsigned r; asm volatile("v_cvt_pk_bf16_f32 %0, %1, %2" : "=v"(r) : "v"(lo), "v"(hi)); return r;
}
__device__ __forceinline__ float bf2f(u16 v) { return __uint_as_float(((unsigned)v) << 16); }
__device__ __forceinline__ int crow(int r, int hi) { return (r & 3) + 8 * (r >> 2) + 4 * hi; }

typedef __attribute__((address_space(3))) s16x4* lds_s16x4_p;
__device__ __forceinline__ s16x4 trrd(unsigned addr) { return __builtin_amdgcn_ds_read_tr16_b64_v4i16((lds_s16x4_p)addr); }

__device__ __forceinline__ unsigned lane_id() { unsigned l; asm volatile("v_mbcnt_lo_u32_b32 %0, -1, 0\n\tv_mbcnt_hi_u32_b32 %0, -1, %0" : "=v"(l)); return l; }
#define RTS(slot) do {} while (0)
#define CT_END 0x1f300
#define ACLOB_LO "a0","a1","a2","a3","a4","a5","a6","a7","a8","a9","a10","a11","a12","a13","a14","a15","a16","a17","a18","a19","a20","a21","a22","a23","a24","a25","a26","a27","a28","a29","a30","a31","a32","a33","a34","a35","a36","a37","a38","a39","a40","a41","a42","a43","a44","a45","a46","a47","a48","a49","a50","a51","a52","a53","a54","a55","a56","a57","a58","a59","a60","a61","a62","a63","a64","a65","a66","a67","a68","a69","a70","a71","a72","a73","a74","a75","a76","a77","a78","a79","a80","a81","a82","a83","a84","a85","a86","a87","a88","a89","a90","a91","a92","a93","a94","a95","a96","a97","a98","a99","a100","a101","a102","a103","a104","a105","a106","a107","a108","a109","a110","a111","a112","a113","a114","a115","a116","a117","a118","a119","a120","a121","a122","a123","a124","a125","a126","a127"
#define ACLOB_HI "a128","a129","a130","a131","a132","a133","a134","a135","a136","a137","a138","a139","a140","a141","a142","a143","a144","a145","a146","a147","a148","a149","a150","a151","a152","a153","a154","a155","a156","a157","a158","a159","a160","a161","a162","a163","a164","a165","a166","a167","a168","a169","a170","a171","a172","a173","a174","a175","a176","a177","a178","a179","a180","a181","a182","a183","a184","a185","a186","a187","a188","a189","a190","a191","a192","a193","a194","a195","a196","a197","a198","a199","a200","a201","a202","a203","a204","a205","a206","a207","a208","a209","a210","a211","a212","a213","a214","a215","a216","a217","a218","a219","a220","a221","a222","a223","a224","a225","a226","a227","a228","a229","a230","a231","a232","a233","a234","a235","a236","a237","a238","a239","a240","a241","a242","a243","a244","a245","a246","a247","a248","a249","a250","a251","a252","a253","a254","a255"
#define PLO(p) ((unsigned)(size_t)(p))
#define PHI(p) ((unsigned)((size_t)(p) >> 32))
__device__ __forceinline__ unsigned vsel(bool c, unsigned a, unsigned b) { asm("" : "+v"(a), "+v"(b)); return c ? a : b; }
__device__ __forceinline__ unsigned ssel(unsigned c, unsigned a, unsigned b) { unsigned r; asm("s_cmp_lg_u32 %1, 0\n\ts_cselect_b32 %0, %2, %3" : "=s"(r) : "s"(c), "s"(a), "s"(b) : "scc"); return r; }
template <int N> __device__ __forceinline__ unsigned sslt(unsigned c, unsigned a, unsigned b) { unsigned r; asm("s_cmpk_lt_u32 %1, %4\n\ts_cselect_b32 %0, %2, %3" : "=s"(r) : "s"(c), "s"(a), "s"(b), "n"(N) : "scc"); return r; }
namespace att {
constexpr int LDS_BYTES = 163840;
constexpr unsigned LDS_G = 159744u, LDS_L = 161792u;
}
#define SD(J8, H, W) ((unsigned)WS_AF + (((J8) + 2u * ((W) & 2u) + ((W) & 1u)) * KS_TOT + 18u + 4u * (H)) * 1024u)
#define SRDA (u32x4){PLO(ws), PHI(ws) & 0xffffu, (unsigned)WS_L, 0x00020000u}
#define PROJ_ASM \
".p2align 3\n" \
"L_pb:\n" \
"s_waitcnt lgkmcnt(0)\n" \
"s_mov_b32 s64, s10\n" \
"s_and_b32 s65, s11, 0xffff\n" \
"s_mov_b32 s66, 0x200000\n" \
"s_mov_b32 s67, 0x20000\n" \
"s_mov_b32 s68, s14\n" \
"s_and_b32 s69, s15, 0xffff\n" \
"s_mov_b32 s70, 0x400000\n" \
"s_mov_b32 s71, 0x20000\n" \
"s_mov_b32 s72, s16\n" \
"s_and_b32 s73, s17, 0xffff\n" \
"s_mov_b32 s74, 0x400000\n" \
"s_mov_b32 s75, 0x20000\n" \
"s_add_u32 m0, s30, 0x0\n" \
"s_movk_i32 s84, 0x0\n" \
"buffer_load_dwordx4 v22, s[64:67], s84 offen offset:0 lds\n" \
"s_movk_i32 s84, 0x1c00\n" \
"buffer_load_dwordx4 v23, s[64:67], s84 offen offset:1024 lds\n" \
"s_movk_i32 s84, 0x3800\n" \
"buffer_load_dwordx4 v22, s[64:67], s84 offen offset:2048 lds\n" \
"s_movk_i32 s84, 0x5400\n" \
"buffer_load_dwordx4 v23, s[64:67], s84 offen offset:3072 lds\n" \
"s_add_u32 m0, s31, 0x0\n" \
"s_movk_i32 s84, 0x0\n" \
"buffer_load_dwordx4 v26, s[68:71], s84 offen offset:0 lds\n" \
"s_movk_i32 s84, 0x7c00\n" \
"buffer_load_dwordx4 v26, s[68:71], s84 offen offset:1024 lds\n" \
"s_addk_i32 s84, 0x7c00\n" \
"buffer_load_dwordx4 v27, s[68:71], s84 offen offset:2048 lds\n" \
"s_addk_i32 s84, 0x7c00\n" \
"buffer_load_dwordx4 v27, s[68:71], s84 offen offset:3072 lds\n" \
"s_movk_i32 s7, 0x300\n" \
"s_lshr_b32 s4, s2, 1\n" \
"s_and_b32 s4, s4, 60\n" \
"s_or_b32 s4, s4, s3\n" \
"v_lshrrev_b32_e32 v212, 4, v20\n" \
"v_min_u32_e32 v212, 59, v212\n" \
"s_cmpk_lt_u32 s2, 0x80\n" \
"s_cselect_b32 s6, s14, s16\n" \
"v_subrev_u32_e32 v214, 48, v212\n" \
"v_mov_b32_e32 v212, s10\n" \
"v_mov_b32_e32 v215, s6\n" \
"s_cmpk_lt_u32 s2, 0x80\n" \
"s_cselect_b32 s5, s15, s17\n" \
"v_cmp_gt_u32_e32 vcc, s7, v20\n" \
"v_mov_b32_e64 v213, s11\n" \
"v_bfe_u32 v216, v20, 4, 3\n" \
"v_cndmask_b32_e32 v212, v212, v215, vcc\n" \
"v_mov_b32_e32 v215, s5\n" \
"s_lshl_b32 s5, s3, 4\n" \
"v_cndmask_b32_e32 v213, v213, v215, vcc\n" \
"v_lshlrev_b32_e64 v215, 5, v20\n" \
"v_and_b32_e32 v215, 0x7000, v215\n" \
"v_lshl_add_u32 v215, s4, 6, v215\n" \
"s_lshl_b32 s4, s2, 3\n" \
"s_and_b32 s4, s4, 56\n" \
"v_or3_b32 v215, v215, s4, v216\n" \
"s_lshl_b32 s4, s2, 6\n" \
"s_or_b32 s6, s5, s4\n" \
"v_cmp_gt_u32_e64 s[4:5], 6, v214\n" \
"v_add_u32_e32 v215, 0x2000, v215\n" \
"s_nop 0\n" \
"v_cndmask_b32_e64 v216, 4, 2, s[4:5]\n" \
"v_add3_u32 v214, s6, v214, v216\n" \
"s_lshl_b32 s4, s3, 9\n" \
"v_cndmask_b32_e32 v214, v214, v215, vcc\n" \
"v_lshlrev_b32_e32 v214, 7, v214\n" \
"v_mov_b32_e64 v215, 0\n" \
"v_lshl_add_u64 v[184:185], v[212:213], 0, v[214:215]\n" \
"s_or_b32 s32, s4, 0x25000\n" \
"s_add_u32 m0, s30, 0x1000\n" \
"s_mov_b32 s84, 0x8000\n" \
"buffer_load_dwordx4 v22, s[64:67], s84 offen offset:0 lds\n" \
"s_and_b32 s33, s3, 1\n" \
"v_lshrrev_b32_e32 v212, 4, v20\n" \
"v_lshrrev_b32_e64 v214, 8, v20\n" \
"v_bfe_u32 v212, v212, 1, 3\n" \
"v_lshlrev_b32_e32 v215, 2, v20\n" \
"s_addk_i32 s84, 0x1c00\n" \
"buffer_load_dwordx4 v23, s[64:67], s84 offen offset:1024 lds\n" \
"v_lshrrev_b32_e64 v216, 1, v20\n" \
"v_bfe_u32 v213, v20, 4, 4\n" \
"s_lshl_b32 s4, s3, 5\n" \
"v_xor_b32_e32 v212, v212, v214\n" \
"v_lshlrev_b32_e32 v214, 11, v214\n" \
"s_addk_i32 s84, 0x1c00\n" \
"buffer_load_dwordx4 v22, s[64:67], s84 offen offset:2048 lds\n" \
"v_and_b32_e32 v215, 0x300, v215\n" \
"v_and_b32_e64 v217, 24, v216\n" \
"v_and_or_b32 v213, s4, 64, v213\n" \
"v_or3_b32 v214, v215, v214, v217\n" \
"s_lshl_b32 s4, s33, 14\n" \
"s_addk_i32 s84, 0x1c00\n" \
"buffer_load_dwordx4 v23, s[64:67], s84 offen offset:3072 lds\n" \
"v_and_b32_e32 v215, 0xe0, v216\n" \
"v_lshlrev_b32_e32 v213, 7, v213\n" \
"v_lshlrev_b32_e32 v212, 4, v212\n" \
"v_or3_b32 v214, v214, s4, v215\n" \
"v_or_b32_e64 v28, v212, v213\n" \
"s_add_u32 m0, s31, 0x4000\n" \
"s_movk_i32 s84, 0x0\n" \
"buffer_load_dwordx4 v26, s[72:75], s84 offen offset:0 lds\n" \
"v_add_u32_e32 v32, 0x10000, v214\n" \
"v_bitop3_b32 v29, v212, 64, v213 bitop3:0x36\n" \
"v_xor_b32_e32 v33, 0x20, v32\n" \
"v_xor_b32_e64 v34, 0x40, v32\n" \
"v_xor_b32_e32 v35, 0x60, v32\n" \
"s_movk_i32 s84, 0x7c00\n" \
"buffer_load_dwordx4 v26, s[72:75], s84 offen offset:1024 lds\n" \
"v_xor_b32_e32 v36, 0x80, v32\n" \
"v_xor_b32_e32 v37, 0xa0, v32\n" \
"v_xor_b32_e32 v38, 0xc0, v32\n" \
"v_xor_b32_e32 v39, 0xe0, v32\n" \
"s_mov_b32 s80, s26\n" \
"s_addk_i32 s84, 0x7c00\n" \
"buffer_load_dwordx4 v27, s[72:75], s84 offen offset:2048 lds\n" \
"s_and_b32 s81, s27, 0xffff\n" \
"s_mov_b32 s82, 0x10000000\n" \
"s_mov_b32 s83, 0x20000\n" \
"s_mov_b32 s76, s18\n" \
"s_and_b32 s77, s19, 0xffff\n" \
"s_addk_i32 s84, 0x7c00\n" \
"buffer_load_dwordx4 v27, s[72:75], s84 offen offset:3072 lds\n" \
"s_mov_b32 s78, 0x1e6000\n" \
"s_mov_b32 s79, 0x20000\n" \
"s_branch L_pgo_%=\n" \
"L_pret:\n" \
"s_setpc_b64 s[88:89]\n" \
"L_pgo_%=:\n" \
"s_mul_i32 s88, s33, 0xa000\n" \
"s_lshl_b32 s89, s3, 11\n" \
"s_and_b32 s89, s89, 0x1000\n" \
"s_add_u32 s88, s88, s89\n" \
"s_getpc_b64 s[86:87]\n" \
"L_hpa_%=:\n" \
"s_add_u32 s86, s86, L_pp1-L_hpa_%=\n" \
"s_addc_u32 s87, s87, 0\n" \
"s_add_u32 s86, s86, s88\n" \
"s_addc_u32 s87, s87, 0\n" \
"s_add_u32 m0, s30, 0x8000\n" \
"s_movk_i32 s84, 0x80\n" \
"buffer_load_dwordx4 v22, s[64:67], s84 offen offset:0 lds\n" \
"s_lshr_b32 s4, s2, 1\n" \
"s_and_b32 s4, s4, 0x7c\n" \
"v_bfe_u32 v212, v20, 4, 5\n" \
"s_movk_i32 s84, 0x1c80\n" \
"buffer_load_dwordx4 v23, s[64:67], s84 offen offset:1024 lds\n" \
"s_or_b32 s4, s4, s3\n" \
"v_min_u32_e32 v212, 26, v212\n" \
"v_mad_u64_u32 v[212:213], s[4:5], s4, 27, v[212:213]\n" \
"s_movk_i32 s84, 0x3880\n" \
"buffer_load_dwordx4 v22, s[64:67], s84 offen offset:2048 lds\n" \
"v_mul_u32_u24_e32 v213, 0xaaab, v212\n" \
"v_lshrrev_b32_e32 v213, 18, v213\n" \
"s_mov_b32 s4, 0x1fffffa\n" \
"s_movk_i32 s84, 0x5480\n" \
"buffer_load_dwordx4 v23, s[64:67], s84 offen offset:3072 lds\n" \
"v_mad_u64_u32 v[214:215], s[4:5], v213, s4, v[212:213]\n" \
"s_and_b32 s6, s2, 7\n" \
"v_mul_u32_u24_e32 v213, 27, v213\n" \
"s_mov_b64 exec, 0\n" \
"s_swappc_b64 s[88:89], s[86:87]\n" \
"s_mov_b64 exec, -1\n" \
"s_add_u32 s86, s86, 0x2000\n" \
"s_addc_u32 s87, s87, 0\n" \
"s_add_u32 m0, s31, 0x8000\n" \
"s_mov_b32 s84, 0x80000\n" \
"buffer_load_dwordx4 v26, s[68:71], s84 offen offset:0 lds\n" \
"v_lshrrev_b32_e32 v214, 1, v214\n" \
"v_and_b32_e32 v212, 1, v212\n" \
"v_mul_u32_u24_e32 v214, 9, v214\n" \
"s_addk_i32 s84, 0x7c00\n" \
"buffer_load_dwordx4 v26, s[68:71], s84 offen offset:1024 lds\n" \
"v_add3_u32 v212, v212, s6, v213\n" \
"v_add_lshl_u32 v212, v212, v214, 7\n" \
"v_mov_b32_e32 v213, 0\n" \
"s_mov_b64 exec, 0\n" \
"s_swappc_b64 s[88:89], s[86:87]\n" \
"s_mov_b64 exec, -1\n" \
"s_add_u32 s86, s86, 0x2000\n" \
"s_addc_u32 s87, s87, 0\n" \
"s_addk_i32 s84, 0x7c00\n" \
"buffer_load_dwordx4 v27, s[68:71], s84 offen offset:2048 lds\n" \
"v_lshl_add_u64 v[186:187], s[18:19], 0, v[212:213]\n" \
"s_lshl_b32 s35, s2, 10\n" \
"s_lshr_b32 s37, s2, 1\n" \
"s_addk_i32 s84, 0x7c00\n" \
"buffer_load_dwordx4 v27, s[68:71], s84 offen offset:3072 lds\n" \
"s_mul_i32 s37, s37, 0x24800\n" \
"s_and_b32 s35, s35, 0x400\n" \
"s_lshl_b32 s36, s3, 8\n" \
"s_mov_b64 exec, 0\n" \
"s_swappc_b64 s[88:89], s[86:87]\n" \
"s_mov_b64 exec, -1\n" \
"s_add_u32 s86, s86, 0x2000\n" \
"s_addc_u32 s87, s87, 0\n" \
"s_add_u32 m0, s30, 0x9000\n" \
"s_mov_b32 s84, 0x8080\n" \
"buffer_load_dwordx4 v22, s[64:67], s84 offen offset:0 lds\n" \
"s_or_b32 s35, s37, s35\n" \
"s_and_b32 s4, s2, 7\n" \
"s_lshr_b32 s6, s2, 5\n" \
"s_addk_i32 s84, 0x1c00\n" \
"buffer_load_dwordx4 v23, s[64:67], s84 offen offset:1024 lds\n" \
"s_lshl_b32 s34, s2, 5\n" \
"s_add_i32 s35, s35, s36\n" \
"s_lshl_b32 s7, s4, 10\n" \
"s_addk_i32 s84, 0x1c00\n" \
"buffer_load_dwordx4 v22, s[64:67], s84 offen offset:2048 lds\n" \
"s_and_b32 s34, s34, 0x300\n" \
"s_add_i32 s35, s35, 0x3004000\n" \
"s_lshl_b32 s37, s6, 13\n" \
"s_mov_b64 exec, 0\n" \
"s_swappc_b64 s[88:89], s[86:87]\n" \
"s_mov_b64 exec, -1\n" \
"s_andn2_b32 s85, 2, s3\n" \
"s_lshl_b32 s85, s85, 12\n" \
"s_add_u32 s86, s86, s85\n" \
"s_addc_u32 s87, s87, 0\n" \
"s_addk_i32 s84, 0x1c00\n" \
"buffer_load_dwordx4 v23, s[64:67], s84 offen offset:3072 lds\n" \
"s_or_b32 s34, s7, s34\n" \
"s_or_b32 s7, s37, s7\n" \
"v_mov_b32_e64 v213, s35\n" \
"s_add_u32 m0, s31, 0xc000\n" \
"s_mov_b32 s84, 0x80000\n" \
"buffer_load_dwordx4 v26, s[72:75], s84 offen offset:0 lds\n" \
"s_or_b32 s7, s7, s36\n" \
"s_lshr_b32 s5, s2, 3\n" \
"s_or_b32 s7, s7, 0x5c00000\n" \
"s_mov_b64 exec, 0\n" \
"s_swappc_b64 s[88:89], s[86:87]\n" \
"s_mov_b64 exec, -1\n" \
"s_addk_i32 s84, 0x7c00\n" \
"buffer_load_dwordx4 v26, s[72:75], s84 offen offset:1024 lds\n" \
"v_writelane_b32 v213, s7, 2\n" \
"s_mul_i32 s5, s5, 0x90000\n" \
"v_writelane_b32 v213, s7, 3\n" \
"s_addk_i32 s84, 0x7c00\n" \
"buffer_load_dwordx4 v27, s[72:75], s84 offen offset:2048 lds\n" \
"s_lshl_b32 s7, s3, 14\n" \
"s_lshl_b32 s4, s4, 16\n" \
"s_add_i32 s5, s5, s7\n" \
"s_addk_i32 s84, 0x7c00\n" \
"buffer_load_dwordx4 v27, s[72:75], s84 offen offset:3072 lds\n" \
"s_or_b32 s34, s34, 0x5be8000\n" \
"v_writelane_b32 v213, s34, 4\n" \
"s_add_i32 s4, s5, s4\n" \
"v_writelane_b32 v213, s34, 5\n" \
"s_add_i32 s4, s4, 0x4243800\n" \
"v_lshlrev_b32_e64 v212, 3, v20\n" \
"v_and_b32_e32 v212, 0x80, v212\n" \
"v_writelane_b32 v213, s4, 6\n" \
"s_mul_i32 s4, s6, 0xa00\n" \
"s_add_i32 s4, s4, 0x5bc0f80\n" \
"v_writelane_b32 v213, s4, 7\n" \
"v_add_u32_e32 v49, v213, v212\n" \
"s_mov_b32 m0, s32\n" \
"s_mov_b32 exec_lo, 0xffffff\n" \
"s_mov_b32 exec_hi, 0xfff0000\n" \
"global_load_lds_dword v[184:185], off\n" \
"s_mov_b64 exec, -1\n" \
"s_mov_b32 m0, s32\n" \
"s_mov_b64 exec, 0xff\n" \
"buffer_load_dword v49, s[80:83], 0 offen lds\n" \
"s_mov_b64 exec, -1\n" \
"s_mov_b32 s84, 0x5bc0000\n" \
"buffer_load_dword v180, off, s[80:83], s84 sc1\n" \
"s_cmp_lg_u32 s33, 0\n" \
"s_cbranch_scc1 L_w1_%=\n" \
"L_w0_%=:\n" \
"s_waitcnt vmcnt(19)\n" \
"s_barrier\n" \
"s_nop 0\n" \
"ds_read_b64_tr_b16 a[160:161], v32 offset:0\n" \
"ds_read_b64_tr_b16 a[162:163], v32 offset:1024\n" \
"ds_read_b128 a[128:131], v28 offset:16384\n" \
"ds_read_b64_tr_b16 a[224:225], v32 offset:8192\n" \
"ds_read_b64_tr_b16 a[226:227], v32 offset:9216\n" \
"ds_read_b128 a[192:195], v29 offset:16384\n" \
"ds_read_b128 a[132:135], v28 offset:18432\n" \
"ds_read_b128 a[196:199], v29 offset:18432\n" \
"ds_read_b64_tr_b16 a[164:165], v33 offset:0\n" \
"ds_read_b64_tr_b16 a[166:167], v33 offset:1024\n" \
"ds_read_b64_tr_b16 a[228:229], v33 offset:8192\n" \
"ds_read_b64_tr_b16 a[230:231], v33 offset:9216\n" \
"ds_read_b128 a[136:139], v28 offset:20480\n" \
"ds_read_b128 a[200:203], v29 offset:20480\n" \
"s_waitcnt lgkmcnt(11)\n" \
"v_mfma_f32_16x16x32_bf16 a[0:3], a[160:163], a[128:131], 0\n" \
"ds_read_b64_tr_b16 a[168:169], v34 offset:0\n" \
"s_lshr_b32 s4, s2, 1\n" \
"s_waitcnt lgkmcnt(9)\n" \
"v_mfma_f32_16x16x32_bf16 a[0:3], a[224:227], a[192:195], a[0:3]\n" \
"ds_read_b64_tr_b16 a[170:171], v34 offset:1024\n" \
"s_and_b32 s4, s4, 0x7c\n" \
"s_waitcnt lgkmcnt(9)\n" \
"v_mfma_f32_16x16x32_bf16 a[16:19], a[160:163], a[132:135], 0\n" \
"ds_read_b64_tr_b16 a[232:233], v34 offset:8192\n" \
"v_mov_b32_e32 v213, s13\n" \
"s_waitcnt lgkmcnt(9)\n" \
"v_mfma_f32_16x16x32_bf16 a[16:19], a[224:227], a[196:199], a[16:19]\n" \
"ds_read_b64_tr_b16 a[234:235], v34 offset:9216\n" \
"s_or_b32 s4, s4, s3\n" \
"s_waitcnt lgkmcnt(8)\n" \
"v_mfma_f32_16x16x32_bf16 a[20:23], a[164:167], a[132:135], 0\n" \
"ds_read_b128 a[140:143], v28 offset:22528\n" \
"v_lshrrev_b32_e32 v212, 4, v20\n" \
"s_waitcnt lgkmcnt(7)\n" \
"v_mfma_f32_16x16x32_bf16 a[20:23], a[228:231], a[196:199], a[20:23]\n" \
"ds_read_b128 a[204:207], v29 offset:22528\n" \
"v_mad_u64_u32 v[214:215], s[4:5], s4, 36, v[212:213]\n" \
"v_mfma_f32_16x16x32_bf16 a[4:7], a[164:167], a[128:131], 0\n" \
"ds_read_b64_tr_b16 a[172:173], v35 offset:0\n" \
"v_mov_b32_e64 v217, s9\n" \
"v_mfma_f32_16x16x32_bf16 a[4:7], a[228:231], a[192:195], a[4:7]\n" \
"ds_read_b64_tr_b16 a[174:175], v35 offset:1024\n" \
"s_movk_i32 s4, 0x240\n" \
"s_waitcnt lgkmcnt(9)\n" \
"v_mfma_f32_16x16x32_bf16 a[32:35], a[160:163], a[136:139], 0\n" \
"ds_read_b64_tr_b16 a[236:237], v35 offset:8192\n" \
"v_cmp_gt_u32_e32 vcc, s4, v20\n" \
"s_waitcnt lgkmcnt(9)\n" \
"v_mfma_f32_16x16x32_bf16 a[32:35], a[224:227], a[200:203], a[32:35]\n" \
"ds_read_b64_tr_b16 a[238:239], v35 offset:9216\n" \
"s_lshl_b32 s4, s2, 3\n" \
"v_mfma_f32_16x16x32_bf16 a[36:39], a[164:167], a[136:139], 0\n" \
"ds_read_b128 a[144:147], v28 offset:0\n" \
"s_and_b32 s4, s4, 56\n" \
"v_mfma_f32_16x16x32_bf16 a[36:39], a[228:231], a[200:203], a[36:39]\n" \
"ds_read_b128 a[208:211], v29 offset:0\n" \
"v_cndmask_b32_e32 v217, v217, v213, vcc\n" \
"s_waitcnt lgkmcnt(10)\n" \
"v_mfma_f32_16x16x32_bf16 a[40:43], a[168:171], a[136:139], 0\n" \
"ds_read_b128 a[148:151], v28 offset:2048\n" \
"v_lshlrev_b32_e32 v213, 3, v214\n" \
"s_waitcnt lgkmcnt(9)\n" \
"v_mfma_f32_16x16x32_bf16 a[40:43], a[232:235], a[200:203], a[40:43]\n" \
"ds_read_b128 a[212:215], v29 offset:2048\n" \
"v_and_b32_e32 v213, 0x1ffc0, v213\n" \
"v_mfma_f32_16x16x32_bf16 a[24:27], a[168:171], a[132:135], 0\n" \
"ds_read_b128 a[152:155], v28 offset:4096\n" \
"v_and_b32_e64 v214, 7, v214\n" \
"v_mfma_f32_16x16x32_bf16 a[24:27], a[232:235], a[196:199], a[24:27]\n" \
"ds_read_b128 a[216:219], v29 offset:4096\n" \
"v_or3_b32 v213, v214, s4, v213\n" \
"v_mfma_f32_16x16x32_bf16 a[8:11], a[168:171], a[128:131], 0\n" \
"ds_read_b128 a[156:159], v28 offset:6144\n" \
"s_lshl_b32 s4, s2, 2\n" \
"v_mfma_f32_16x16x32_bf16 a[8:11], a[232:235], a[192:195], a[8:11]\n" \
"ds_read_b128 a[220:223], v29 offset:6144\n" \
"s_mov_b32 m0, s32\n" \
"s_waitcnt lgkmcnt(13)\n" \
"v_mfma_f32_16x16x32_bf16 a[48:51], a[160:163], a[140:143], 0\n" \
"s_mov_b32 exec_lo, 0xff000000\n" \
"s_mov_b32 exec_hi, 0x0\n" \
"global_load_lds_dword v[184:185], off\n" \
"s_mov_b64 exec, -1\n" \
"s_waitcnt lgkmcnt(12)\n" \
"v_mfma_f32_16x16x32_bf16 a[48:51], a[224:227], a[204:207], a[48:51]\n" \
"ds_read_b64_tr_b16 a[176:177], v36 offset:0\n" \
"s_or_b32 s4, s4, s3\n" \
"v_mfma_f32_16x16x32_bf16 a[52:55], a[164:167], a[140:143], 0\n" \
"ds_read_b64_tr_b16 a[178:179], v36 offset:1024\n" \
"s_mul_i32 s4, s4, 18\n" \
"v_mfma_f32_16x16x32_bf16 a[52:55], a[228:231], a[204:207], a[52:55]\n" \
"s_mov_b32 m0, s32\n" \
"ds_read_b64_tr_b16 a[240:241], v36 offset:8192\n" \
"v_mfma_f32_16x16x32_bf16 a[56:59], a[168:171], a[140:143], 0\n" \
"s_mov_b32 exec_lo, 0xffff\n" \
"s_mov_b32 exec_hi, 0x0\n" \
"global_load_lds_dword v[186:187], off\n" \
"s_mov_b64 exec, -1\n" \
"v_mfma_f32_16x16x32_bf16 a[56:59], a[232:235], a[204:207], a[56:59]\n" \
"ds_read_b64_tr_b16 a[242:243], v36 offset:9216\n" \
"v_min_u32_e32 v212, 53, v212\n" \
"s_waitcnt lgkmcnt(14)\n" \
"v_mfma_f32_16x16x32_bf16 a[60:63], a[172:175], a[140:143], 0\n" \
"ds_read_b64_tr_b16 a[180:181], v37 offset:0\n" \
"s_sub_i32 s4, s4, 36\n" \
"s_waitcnt lgkmcnt(13)\n" \
"v_mfma_f32_16x16x32_bf16 a[60:63], a[236:239], a[204:207], a[60:63]\n" \
"ds_read_b64_tr_b16 a[182:183], v37 offset:1024\n" \
"v_add_u32_e32 v212, s4, v212\n" \
"v_mfma_f32_16x16x32_bf16 a[44:47], a[172:175], a[136:139], 0\n" \
"ds_read_b64_tr_b16 a[244:245], v37 offset:8192\n" \
"v_mov_b32_e64 v215, s12\n" \
"v_mfma_f32_16x16x32_bf16 a[44:47], a[236:239], a[200:203], a[44:47]\n" \
"ds_read_b64_tr_b16 a[246:247], v37 offset:9216\n" \
"v_mov_b32_e64 v216, s8\n" \
"v_mfma_f32_16x16x32_bf16 a[28:31], a[172:175], a[132:135], 0\n" \
"ds_read_b64_tr_b16 a[184:185], v38 offset:0\n" \
"v_cndmask_b32_e64 v212, v212, v213, vcc\n" \
"v_mfma_f32_16x16x32_bf16 a[28:31], a[236:239], a[196:199], a[28:31]\n" \
"ds_read_b64_tr_b16 a[186:187], v38 offset:1024\n" \
"v_cndmask_b32_e64 v216, v216, v215, vcc\n" \
"v_mfma_f32_16x16x32_bf16 a[12:15], a[172:175], a[128:131], 0\n" \
"v_lshlrev_b32_e32 v212, 7, v212\n" \
"v_mov_b32_e32 v213, 0\n" \
"v_mfma_f32_16x16x32_bf16 a[12:15], a[236:239], a[192:195], a[12:15]\n" \
"v_lshl_add_u64 v[188:189], v[216:217], 0, v[212:213]\n" \
"s_movk_i32 s5, 0xd80\n" \
"s_waitcnt lgkmcnt(15)\n" \
"v_mfma_f32_16x16x32_bf16 a[76:79], a[172:175], a[144:147], 0\n" \
"ds_read_b64_tr_b16 a[248:249], v38 offset:8192\n" \
"s_lshr_b32 s6, s2, 5\n" \
"v_mfma_f32_16x16x32_bf16 a[76:79], a[236:239], a[208:211], a[76:79]\n" \
"v_lshrrev_b32_e32 v212, 4, v20\n" \
"v_lshl_or_b32 v215, s3, 6, v212\n" \
"v_mfma_f32_16x16x32_bf16 a[72:75], a[168:171], a[144:147], 0\n" \
"v_mul_u32_u24_e32 v216, 0x2aab, v215\n" \
"v_lshrrev_b32_e64 v216, 16, v216\n" \
"v_mfma_f32_16x16x32_bf16 a[72:75], a[232:235], a[208:211], a[72:75]\n" \
"ds_read_b64_tr_b16 a[250:251], v38 offset:9216\n" \
"v_mad_i32_i24 v217, v216, -6, v215\n" \
"v_mfma_f32_16x16x32_bf16 a[68:71], a[164:167], a[144:147], 0\n" \
"v_add_u32_e32 v218, 0xfffffe50, v215\n" \
"s_mul_i32 s35, s6, 0x48\n" \
"v_mfma_f32_16x16x32_bf16 a[68:71], a[228:231], a[208:211], a[68:71]\n" \
"v_lshrrev_b32_e32 v219, 1, v217\n" \
"v_add_u32_e32 v218, s35, v216\n" \
"v_mfma_f32_16x16x32_bf16 a[64:67], a[160:163], a[144:147], 0\n" \
"v_mul_u32_u24_e32 v219, 0x480, v219\n" \
"v_and_b32_e64 v214, 16, v20\n" \
"v_mfma_f32_16x16x32_bf16 a[64:67], a[224:227], a[208:211], a[64:67]\n" \
"ds_read_b64_tr_b16 a[188:189], v39 offset:0\n" \
"v_mad_u32_u24 v219, v218, s5, v219\n" \
"v_mfma_f32_16x16x32_bf16 a[80:83], a[160:163], a[148:151], 0\n" \
"v_or_b32_e32 v192, v219, v214\n" \
"v_mov_b32_e32 v220, 2.0\n" \
"s_waitcnt lgkmcnt(15)\n" \
"v_mfma_f32_16x16x32_bf16 a[80:83], a[224:227], a[212:215], a[80:83]\n" \
"v_mov_b32_e32 v222, 7\n" \
"v_lshlrev_b32_e64 v220, 7, v218\n" \
"v_mfma_f32_16x16x32_bf16 a[84:87], a[164:167], a[148:151], 0\n" \
"ds_read_b64_tr_b16 a[190:191], v39 offset:1024\n" \
"v_lshrrev_b32_e64 v218, 1, v218\n" \
"v_mfma_f32_16x16x32_bf16 a[84:87], a[228:231], a[212:215], a[84:87]\n" \
"v_and_b32_e32 v218, 4, v218\n" \
"v_and_b32_e32 v216, 2, v216\n" \
"v_mfma_f32_16x16x32_bf16 a[88:91], a[168:171], a[148:151], 0\n" \
"s_mul_i32 s7, s6, 0x2400\n" \
"s_sub_i32 s34, 0x22000, s7\n" \
"v_mfma_f32_16x16x32_bf16 a[88:91], a[232:235], a[212:215], a[88:91]\n" \
"ds_read_b64_tr_b16 a[252:253], v39 offset:8192\n" \
"v_bitop3_b32 v216, v217, v218, v216 bitop3:0x1e\n" \
"v_mfma_f32_16x16x32_bf16 a[92:95], a[172:175], a[148:151], 0\n" \
"v_or_b32_e32 v213, 0x24400, v20\n" \
"v_lshlrev_b32_e64 v216, 4, v216\n" \
"v_mfma_f32_16x16x32_bf16 a[92:95], a[236:239], a[212:215], a[92:95]\n" \
"v_add3_u32 v196, v220, s34, v216\n" \
"v_mov_b32_e64 v216, v213\n" \
"v_mfma_f32_16x16x32_bf16 a[108:111], a[172:175], a[152:155], 0\n" \
"ds_read_b64_tr_b16 a[254:255], v39 offset:9216\n" \
"s_movk_i32 s36, 0x1b0\n" \
"v_mfma_f32_16x16x32_bf16 a[108:111], a[236:239], a[216:219], a[108:111]\n" \
"v_or_b32_e32 v216, 0x100, v215\n" \
"v_mul_u32_u24_e32 v217, 0x2aab, v216\n" \
"v_mfma_f32_16x16x32_bf16 a[104:107], a[168:171], a[152:155], 0\n" \
"v_add_u32_e32 v215, 0xffffff50, v215\n" \
"v_lshrrev_b32_e32 v217, 16, v217\n" \
"v_mfma_f32_16x16x32_bf16 a[104:107], a[232:235], a[216:219], a[104:107]\n" \
"v_mov_b32_e64 v220, v215\n" \
"v_mad_i32_i24 v218, v217, -6, v216\n" \
"v_mfma_f32_16x16x32_bf16 a[100:103], a[164:167], a[152:155], 0\n" \
"v_cmp_gt_u32_e32 vcc, s36, v216\n" \
"v_mov_b32_e32 v221, 7\n" \
"v_mfma_f32_16x16x32_bf16 a[100:103], a[228:231], a[216:219], a[100:103]\n" \
"v_lshrrev_b32_e32 v222, 1, v218\n" \
"v_cndmask_b32_e32 v217, v220, v217, vcc\n" \
"v_mfma_f32_16x16x32_bf16 a[96:99], a[160:163], a[152:155], 0\n" \
"s_movk_i32 s4, 0x48\n" \
"v_add_u32_e32 v220, s35, v217\n" \
"v_mfma_f32_16x16x32_bf16 a[96:99], a[224:227], a[216:219], a[96:99]\n" \
"v_mul_u32_u24_e32 v222, 0x480, v222\n" \
"v_mad_u32_u24 v222, v220, s5, v222\n" \
"s_waitcnt lgkmcnt(15)\n" \
"v_mfma_f32_16x16x32_bf16 a[112:115], a[160:163], a[156:159], 0\n" \
"v_cmp_gt_u32_e64 s[4:5], s4, v215\n" \
"v_cndmask_b32_e32 v215, v221, v218, vcc\n" \
"v_mfma_f32_16x16x32_bf16 a[112:115], a[224:227], a[220:223], a[112:115]\n" \
"v_lshrrev_b32_e32 v218, 1, v220\n" \
"v_or_b32_e32 v214, v222, v214\n" \
"v_mfma_f32_16x16x32_bf16 a[116:119], a[164:167], a[156:159], 0\n" \
"v_mov_b32_e32 v222, 2.0\n" \
"v_and_b32_e32 v218, 4, v218\n" \
"v_mfma_f32_16x16x32_bf16 a[116:119], a[228:231], a[220:223], a[116:119]\n" \
"v_and_b32_e64 v217, 2, v217\n" \
"v_bitop3_b32 v215, v215, v218, v217 bitop3:0x1e\n" \
"v_mfma_f32_16x16x32_bf16 a[120:123], a[168:171], a[156:159], 0\n" \
"v_lshlrev_b32_e32 v223, 1, v220\n" \
"v_mov_b32_e32 v224, 2.0\n" \
"v_mfma_f32_16x16x32_bf16 a[120:123], a[232:235], a[220:223], a[120:123]\n" \
"v_cndmask_b32_e32 v193, v222, v214, vcc\n" \
"v_lshlrev_b32_e32 v214, 7, v220\n" \
"v_mfma_f32_16x16x32_bf16 a[124:127], a[172:175], a[156:159], 0\n" \
"v_lshlrev_b32_e32 v215, 4, v215\n" \
"s_mul_i32 s39, s3, 18\n" \
"v_mfma_f32_16x16x32_bf16 a[124:127], a[236:239], a[220:223], a[124:127]\n" \
"s_waitcnt vmcnt(5) lgkmcnt(0)\n" \
"s_barrier\n" \
"v_mfma_f32_16x16x32_bf16 v[52:55], a[176:179], a[128:131], 0\n" \
"s_add_u32 m0, s30, 0x0\n" \
"s_movk_i32 s84, 0x100\n" \
"v_mfma_f32_16x16x32_bf16 v[52:55], a[240:243], a[192:195], v[52:55]\n" \
"buffer_load_dwordx4 v22, s[64:67], s84 offen offset:0 lds\n" \
"v_mfma_f32_16x16x32_bf16 v[56:59], a[180:183], a[128:131], 0\n" \
"ds_read_b64_tr_b16 a[160:161], v32 offset:32768\n" \
"v_add3_u32 v214, v214, s34, v215\n" \
"v_mfma_f32_16x16x32_bf16 v[56:59], a[244:247], a[192:195], v[56:59]\n" \
"s_movk_i32 s84, 0x1d00\n" \
"v_mfma_f32_16x16x32_bf16 v[60:63], a[184:187], a[128:131], 0\n" \
"buffer_load_dwordx4 v23, s[64:67], s84 offen offset:1024 lds\n" \
"v_mfma_f32_16x16x32_bf16 v[60:63], a[248:251], a[192:195], v[60:63]\n" \
"ds_read_b64_tr_b16 a[162:163], v32 offset:33792\n" \
"v_cndmask_b32_e64 v195, v224, v223, s[4:5]\n" \
"v_mfma_f32_16x16x32_bf16 v[64:67], a[188:191], a[128:131], 0\n" \
"ds_read_b64_tr_b16 a[224:225], v32 offset:40960\n" \
"s_movk_i32 s4, 0x1f8\n" \
"v_mfma_f32_16x16x32_bf16 v[64:67], a[252:255], a[192:195], v[64:67]\n" \
"ds_read_b128 a[128:131], v28 offset:49152\n" \
"v_mov_b32_e64 v215, v213\n" \
"v_mfma_f32_16x16x32_bf16 v[80:83], a[188:191], a[132:135], 0\n" \
"ds_read_b128 a[192:195], v29 offset:49152\n" \
"s_add_i32 s35, s35, s39\n" \
"v_mfma_f32_16x16x32_bf16 v[80:83], a[252:255], a[196:199], v[80:83]\n" \
"s_movk_i32 s84, 0x3900\n" \
"v_mfma_f32_16x16x32_bf16 v[76:79], a[184:187], a[132:135], 0\n" \
"buffer_load_dwordx4 v22, s[64:67], s84 offen offset:2048 lds\n" \
"v_mfma_f32_16x16x32_bf16 v[76:79], a[248:251], a[196:199], v[76:79]\n" \
"s_movk_i32 s84, 0x5500\n" \
"v_mfma_f32_16x16x32_bf16 v[72:75], a[180:183], a[132:135], 0\n" \
"buffer_load_dwordx4 v23, s[64:67], s84 offen offset:3072 lds\n" \
"v_mfma_f32_16x16x32_bf16 v[72:75], a[244:247], a[196:199], v[72:75]\n" \
"s_add_u32 m0, s31, 0x0\n" \
"s_mov_b32 s84, 0x100000\n" \
"v_mfma_f32_16x16x32_bf16 v[68:71], a[176:179], a[132:135], 0\n" \
"buffer_load_dwordx4 v26, s[68:71], s84 offen offset:0 lds\n" \
"v_mfma_f32_16x16x32_bf16 v[68:71], a[240:243], a[196:199], v[68:71]\n" \
"ds_read_b128 a[132:135], v28 offset:51200\n" \
"v_cmp_gt_u32_e64 vcc, s4, v216\n" \
"v_mfma_f32_16x16x32_bf16 v[84:87], a[176:179], a[136:139], 0\n" \
"ds_read_b128 a[196:199], v29 offset:51200\n" \
"v_add_u32_e64 v212, s35, v212\n" \
"v_mfma_f32_16x16x32_bf16 v[84:87], a[240:243], a[200:203], v[84:87]\n" \
"s_addk_i32 s84, 0x7c00\n" \
"v_mfma_f32_16x16x32_bf16 v[88:91], a[180:183], a[136:139], 0\n" \
"buffer_load_dwordx4 v26, s[68:71], s84 offen offset:1024 lds\n" \
"v_mfma_f32_16x16x32_bf16 v[88:91], a[244:247], a[200:203], v[88:91]\n" \
"ds_read_b64_tr_b16 a[226:227], v32 offset:41984\n" \
"v_mov_b32_e32 v219, 2.0\n" \
"v_mfma_f32_16x16x32_bf16 v[92:95], a[184:187], a[136:139], 0\n" \
"s_addk_i32 s84, 0x7c00\n" \
"v_mfma_f32_16x16x32_bf16 v[92:95], a[248:251], a[200:203], v[92:95]\n" \
"buffer_load_dwordx4 v27, s[68:71], s84 offen offset:2048 lds\n" \
"v_mfma_f32_16x16x32_bf16 v[96:99], a[188:191], a[136:139], 0\n" \
"ds_read_b64_tr_b16 a[164:165], v33 offset:32768\n" \
"v_cndmask_b32_e32 v197, v215, v214, vcc\n" \
"v_mfma_f32_16x16x32_bf16 v[96:99], a[252:255], a[200:203], v[96:99]\n" \
"ds_read_b128 a[136:139], v28 offset:53248\n" \
"s_movk_i32 s35, 0x120\n" \
"v_mfma_f32_16x16x32_bf16 v[112:115], a[188:191], a[140:143], 0\n" \
"ds_read_b128 a[200:203], v29 offset:53248\n" \
"v_mul_u32_u24_e32 v214, 0xd80, v212\n" \
"v_mfma_f32_16x16x32_bf16 v[112:115], a[252:255], a[204:207], v[112:115]\n" \
"s_addk_i32 s84, 0x7c00\n" \
"v_mfma_f32_16x16x32_bf16 v[108:111], a[184:187], a[140:143], 0\n" \
"buffer_load_dwordx4 v27, s[68:71], s84 offen offset:3072 lds\n" \
"v_mfma_f32_16x16x32_bf16 v[108:111], a[248:251], a[204:207], v[108:111]\n" \
"s_add_u32 m0, s30, 0x1000\n" \
"s_mov_b32 s84, 0x8100\n" \
"v_mfma_f32_16x16x32_bf16 v[104:107], a[180:183], a[140:143], 0\n" \
"buffer_load_dwordx4 v22, s[64:67], s84 offen offset:0 lds\n" \
"v_mfma_f32_16x16x32_bf16 v[104:107], a[244:247], a[204:207], v[104:107]\n" \
"ds_read_b64_tr_b16 a[166:167], v33 offset:33792\n" \
"v_cmp_gt_u32_e64 vcc, s35, v20\n" \
"v_mfma_f32_16x16x32_bf16 v[100:103], a[176:179], a[140:143], 0\n" \
"ds_read_b64_tr_b16 a[228:229], v33 offset:40960\n" \
"v_lshrrev_b32_e64 v215, 1, v212\n" \
"v_mfma_f32_16x16x32_bf16 v[100:103], a[240:243], a[204:207], v[100:103]\n" \
"ds_read_b128 a[140:143], v28 offset:55296\n" \
"s_and_b32 s4, s2, 7\n" \
"v_mfma_f32_16x16x32_bf16 v[116:119], a[176:179], a[144:147], 0\n" \
"ds_read_b128 a[204:207], v29 offset:55296\n" \
"v_cndmask_b32_e32 v194, v219, v214, vcc\n" \
"v_mfma_f32_16x16x32_bf16 v[116:119], a[240:243], a[208:211], v[116:119]\n" \
"s_addk_i32 s84, 0x1c00\n" \
"v_mfma_f32_16x16x32_bf16 v[120:123], a[180:183], a[144:147], 0\n" \
"buffer_load_dwordx4 v23, s[64:67], s84 offen offset:1024 lds\n" \
"v_mfma_f32_16x16x32_bf16 v[120:123], a[244:247], a[208:211], v[120:123]\n" \
"s_addk_i32 s84, 0x1c00\n" \
"v_mfma_f32_16x16x32_bf16 v[124:127], a[184:187], a[144:147], 0\n" \
"buffer_load_dwordx4 v22, s[64:67], s84 offen offset:2048 lds\n" \
"v_mfma_f32_16x16x32_bf16 v[124:127], a[248:251], a[208:211], v[124:127]\n" \
"s_addk_i32 s84, 0x1c00\n" \
".fill (L_pb + 3808 - .) / 4, 4, 0xBF800000\n" \
"L_pp1:\n" \
"s_cbranch_execz L_pret\n" \
"v_mfma_f32_16x16x32_bf16 v[128:131], a[188:191], a[144:147], 0\n" \
"buffer_load_dwordx4 v23, s[64:67], s84 offen offset:3072 lds\n" \
"v_mfma_f32_16x16x32_bf16 v[128:131], a[252:255], a[208:211], v[128:131]\n" \
"ds_read_b128 a[144:147], v28 offset:32768\n" \
"v_lshlrev_b32_e32 v214, 7, v212\n" \
"v_mfma_f32_16x16x32_bf16 v[144:147], a[188:191], a[148:151], 0\n" \
"ds_read_b128 a[208:211], v29 offset:32768\n" \
"v_lshrrev_b32_e64 v212, 2, v212\n" \
"v_mfma_f32_16x16x32_bf16 v[144:147], a[252:255], a[212:215], v[144:147]\n" \
"s_add_u32 m0, s31, 0x4000\n" \
"s_mov_b32 s84, 0x100000\n" \
"v_mfma_f32_16x16x32_bf16 v[140:143], a[184:187], a[148:151], 0\n" \
"buffer_load_dwordx4 v26, s[72:75], s84 offen offset:0 lds\n" \
"v_mfma_f32_16x16x32_bf16 v[140:143], a[248:251], a[212:215], v[140:143]\n" \
"ds_read_b64_tr_b16 a[230:231], v33 offset:41984\n" \
"v_and_b32_e64 v212, 2, v212\n" \
"v_mfma_f32_16x16x32_bf16 v[136:139], a[180:183], a[148:151], 0\n" \
"s_addk_i32 s84, 0x7c00\n" \
"v_mfma_f32_16x16x32_bf16 v[136:139], a[244:247], a[212:215], v[136:139]\n" \
"buffer_load_dwordx4 v26, s[72:75], s84 offen offset:1024 lds\n" \
"v_mfma_f32_16x16x32_bf16 v[132:135], a[176:179], a[148:151], 0\n" \
"ds_read_b64_tr_b16 a[168:169], v34 offset:32768\n" \
"v_and_or_b32 v212, v215, 1, v212\n" \
"v_mfma_f32_16x16x32_bf16 v[132:135], a[240:243], a[212:215], v[132:135]\n" \
"ds_read_b128 a[148:151], v28 offset:34816\n" \
"s_lshl_b32 s5, s4, 2\n" \
"v_mfma_f32_16x16x32_bf16 v[148:151], a[176:179], a[152:155], 0\n" \
"ds_read_b128 a[212:215], v29 offset:34816\n" \
"s_bfe_u32 s38, s2, 0x20003\n" \
"v_mfma_f32_16x16x32_bf16 v[148:151], a[240:243], a[216:219], v[148:151]\n" \
"ds_read_b64_tr_b16 a[170:171], v34 offset:33792\n" \
"v_lshlrev_b32_e64 v212, 5, v212\n" \
"v_mfma_f32_16x16x32_bf16 v[152:155], a[180:183], a[152:155], 0\n" \
"ds_read_b64_tr_b16 a[232:233], v34 offset:40960\n" \
"s_movk_i32 s35, 0x60\n" \
"v_mfma_f32_16x16x32_bf16 v[152:155], a[244:247], a[216:219], v[152:155]\n" \
"ds_read_b64_tr_b16 a[234:235], v34 offset:41984\n" \
"s_or_b32 s5, s5, s38\n" \
"v_mfma_f32_16x16x32_bf16 v[156:159], a[184:187], a[152:155], 0\n" \
"ds_read_b64_tr_b16 a[172:173], v35 offset:32768\n" \
"v_bitop3_b32 v212, v212, s35, v214 bitop3:0x36\n" \
"v_mfma_f32_16x16x32_bf16 v[156:159], a[248:251], a[216:219], v[156:159]\n" \
"ds_read_b64_tr_b16 a[174:175], v35 offset:33792\n" \
"s_lshr_b32 s36, s2, 3\n" \
"v_mfma_f32_16x16x32_bf16 v[160:163], a[188:191], a[152:155], 0\n" \
"ds_read_b64_tr_b16 a[236:237], v35 offset:40960\n" \
"v_add_u32_e32 v212, s34, v212\n" \
"v_mfma_f32_16x16x32_bf16 v[160:163], a[252:255], a[216:219], v[160:163]\n" \
"ds_read_b128 a[152:155], v28 offset:36864\n" \
"s_mul_i32 s34, s5, 0x12000\n" \
"v_mfma_f32_16x16x32_bf16 v[176:179], a[188:191], a[156:159], 0\n" \
"ds_read_b128 a[216:219], v29 offset:36864\n" \
"s_or_b32 s40, s34, 0x6000000\n" \
"v_mfma_f32_16x16x32_bf16 v[176:179], a[252:255], a[220:223], v[176:179]\n" \
"ds_read_b64_tr_b16 a[238:239], v35 offset:41984\n" \
"s_mul_i32 s34, s36, 0x90000\n" \
"v_mfma_f32_16x16x32_bf16 v[172:175], a[184:187], a[156:159], 0\n" \
"s_lshl_b32 s36, s3, 14\n" \
"s_add_i32 s34, s34, s36\n" \
"v_mfma_f32_16x16x32_bf16 v[172:175], a[248:251], a[220:223], v[172:175]\n" \
"s_lshl_b32 s38, s5, 8\n" \
"s_add_i32 s41, s34, 0x4240000\n" \
"v_mfma_f32_16x16x32_bf16 v[168:171], a[180:183], a[156:159], 0\n" \
"s_mul_i32 s34, s5, 36\n" \
"s_add_i32 s5, s7, s40\n" \
"v_mfma_f32_16x16x32_bf16 v[168:171], a[244:247], a[220:223], v[168:171]\n" \
"s_addk_i32 s5, 0x2000\n" \
"s_mov_b32 s37, 2.0\n" \
"v_mfma_f32_16x16x32_bf16 v[164:167], a[176:179], a[156:159], 0\n" \
"s_or_b32 s38, s38, 0x5be8000\n" \
"v_mfma_f32_16x16x32_bf16 v[164:167], a[240:243], a[220:223], v[164:167]\n" \
"ds_read_b128 a[156:159], v28 offset:38912\n" \
"ds_read_b128 a[220:223], v29 offset:38912\n" \
"s_waitcnt lgkmcnt(15)\n" \
"v_mfma_f32_16x16x32_bf16 a[0:3], a[160:163], a[128:131], a[0:3]\n" \
"s_addk_i32 s84, 0x7c00\n" \
"v_mfma_f32_16x16x32_bf16 a[0:3], a[224:227], a[192:195], a[0:3]\n" \
"buffer_load_dwordx4 v27, s[72:75], s84 offen offset:2048 lds\n" \
"v_mfma_f32_16x16x32_bf16 a[4:7], a[164:167], a[128:131], a[4:7]\n" \
"ds_read_b64_tr_b16 a[176:177], v36 offset:32768\n" \
"s_waitcnt lgkmcnt(15)\n" \
"v_mfma_f32_16x16x32_bf16 a[4:7], a[228:231], a[192:195], a[4:7]\n" \
"s_addk_i32 s84, 0x7c00\n" \
"s_waitcnt lgkmcnt(11)\n" \
"v_mfma_f32_16x16x32_bf16 a[8:11], a[168:171], a[128:131], a[8:11]\n" \
"buffer_load_dwordx4 v27, s[72:75], s84 offen offset:3072 lds\n" \
"s_waitcnt lgkmcnt(9)\n" \
"v_mfma_f32_16x16x32_bf16 a[8:11], a[232:235], a[192:195], a[8:11]\n" \
"ds_read_b64_tr_b16 a[178:179], v36 offset:33792\n" \
"s_waitcnt lgkmcnt(8)\n" \
"v_mfma_f32_16x16x32_bf16 a[12:15], a[172:175], a[128:131], a[12:15]\n" \
"ds_read_b64_tr_b16 a[240:241], v36 offset:40960\n" \
"s_waitcnt lgkmcnt(5)\n" \
"v_mfma_f32_16x16x32_bf16 a[12:15], a[236:239], a[192:195], a[12:15]\n" \
"s_cmp_lg_u32 s3, 0\n" \
"s_cselect_b32 s36, s37, s5\n" \
"v_mfma_f32_16x16x32_bf16 a[28:31], a[172:175], a[132:135], a[28:31]\n" \
"s_lshl_b32 s5, s6, 2\n" \
"s_or_b32 s5, s38, s5\n" \
"v_mfma_f32_16x16x32_bf16 a[28:31], a[236:239], a[196:199], a[28:31]\n" \
"ds_read_b64_tr_b16 a[242:243], v36 offset:41984\n" \
"s_lshl_b32 s35, s3, 10\n" \
"v_mfma_f32_16x16x32_bf16 a[24:27], a[168:171], a[132:135], a[24:27]\n" \
"s_cmpk_lt_u32 s3, 3\n" \
"s_cselect_b32 s37, s37, s5\n" \
"v_mfma_f32_16x16x32_bf16 a[24:27], a[232:235], a[196:199], a[24:27]\n" \
"s_lshl_b32 s5, s4, 16\n" \
"s_lshl_b32 s4, s4, 11\n" \
"v_mfma_f32_16x16x32_bf16 a[20:23], a[164:167], a[132:135], a[20:23]\n" \
"v_cndmask_b32_e32 v198, v213, v212, vcc\n" \
"v_or_b32_e32 v212, s35, v20\n" \
"v_mfma_f32_16x16x32_bf16 a[20:23], a[228:231], a[196:199], a[20:23]\n" \
"ds_read_b64_tr_b16 a[180:181], v37 offset:32768\n" \
"s_or_b32 s39, s40, s35\n" \
"v_mfma_f32_16x16x32_bf16 a[16:19], a[160:163], a[132:135], a[16:19]\n" \
"s_or_b32 s55, s41, s4\n" \
"v_or_b32_e32 v199, 0x22000, v212\n" \
"v_mfma_f32_16x16x32_bf16 a[16:19], a[224:227], a[196:199], a[16:19]\n" \
"s_add_i32 s35, s39, s7\n" \
"s_add_i32 s54, s41, s5\n" \
"v_mfma_f32_16x16x32_bf16 a[32:35], a[160:163], a[136:139], a[32:35]\n" \
"s_mov_b32 m0, s32\n" \
"ds_read_b64_tr_b16 a[182:183], v37 offset:33792\n" \
"v_mfma_f32_16x16x32_bf16 a[32:35], a[224:227], a[200:203], a[32:35]\n" \
"s_mov_b32 exec_lo, 0x0\n" \
"s_mov_b32 exec_hi, 0xff\n" \
"global_load_lds_dword v[184:185], off\n" \
"s_mov_b64 exec, -1\n" \
"v_mfma_f32_16x16x32_bf16 a[36:39], a[164:167], a[136:139], a[36:39]\n" \
"s_add_i32 s55, s55, 0x80000\n" \
"v_mov_b32_e32 v215, 0xffff\n" \
"v_mfma_f32_16x16x32_bf16 a[36:39], a[228:231], a[200:203], a[36:39]\n" \
"ds_read_b64_tr_b16 a[244:245], v37 offset:40960\n" \
"s_and_b32 s4, s3, 1\n" \
"v_mfma_f32_16x16x32_bf16 a[40:43], a[168:171], a[136:139], a[40:43]\n" \
"s_lshl_b32 s5, s4, 4\n" \
"v_lshrrev_b32_e32 v212, 4, v20\n" \
"v_mfma_f32_16x16x32_bf16 a[40:43], a[232:235], a[200:203], a[40:43]\n" \
"s_mov_b32 m0, s32\n" \
"v_lshrrev_b32_e32 v218, 6, v20\n" \
"v_mfma_f32_16x16x32_bf16 a[44:47], a[172:175], a[136:139], a[44:47]\n" \
"s_mov_b32 exec_lo, 0xffff0000\n" \
"s_mov_b32 exec_hi, 0x0\n" \
"global_load_lds_dword v[186:187], off\n" \
"s_mov_b64 exec, -1\n" \
"v_mfma_f32_16x16x32_bf16 a[44:47], a[236:239], a[200:203], a[44:47]\n" \
"ds_read_b64_tr_b16 a[246:247], v37 offset:41984\n" \
"v_and_b32_e64 v219, 12, v218\n" \
"v_mfma_f32_16x16x32_bf16 a[60:63], a[172:175], a[140:143], a[60:63]\n" \
"v_and_or_b32 v214, v212, 15, s5\n" \
"v_cmp_gt_u32_e64 vcc, v219, v214\n" \
"v_mfma_f32_16x16x32_bf16 a[60:63], a[236:239], a[204:207], a[60:63]\n" \
"v_mov_b32_e32 v220, 0xffff0000\n" \
"s_lshl_b32 s5, s2, 2\n" \
"v_mfma_f32_16x16x32_bf16 a[56:59], a[168:171], a[140:143], a[56:59]\n" \
"ds_read_b64_tr_b16 a[184:185], v38 offset:32768\n" \
"v_cndmask_b32_e64 v217, v215, 0, vcc\n" \
"v_mfma_f32_16x16x32_bf16 a[56:59], a[232:235], a[204:207], a[56:59]\n" \
"v_cmp_lt_u32_e32 vcc, v219, v214\n" \
"v_mov_b32_e64 v213, s23\n" \
"v_mfma_f32_16x16x32_bf16 a[52:55], a[164:167], a[140:143], a[52:55]\n" \
"s_or_b32 s5, s5, s3\n" \
"v_cndmask_b32_e32 v221, 0, v220, vcc\n" \
"v_mfma_f32_16x16x32_bf16 a[52:55], a[228:231], a[204:207], a[52:55]\n" \
"ds_read_b64_tr_b16 a[186:187], v38 offset:33792\n" \
"v_or_b32_e64 v12, v217, v221\n" \
"v_mfma_f32_16x16x32_bf16 a[48:51], a[160:163], a[140:143], a[48:51]\n" \
"v_or_b32_e32 v217, 2, v219\n" \
"v_cmp_gt_u32_e32 vcc, v217, v214\n" \
"v_mfma_f32_16x16x32_bf16 a[48:51], a[224:227], a[204:207], a[48:51]\n" \
"v_mov_b32_e32 v216, s25\n" \
"s_movk_i32 s40, 0xf400\n" \
"v_mfma_f32_16x16x32_bf16 a[64:67], a[160:163], a[144:147], a[64:67]\n" \
"ds_read_b64_tr_b16 a[248:249], v38 offset:40960\n" \
"v_cndmask_b32_e64 v221, v215, 0, vcc\n" \
"v_mfma_f32_16x16x32_bf16 a[64:67], a[224:227], a[208:211], a[64:67]\n" \
"v_cmp_lt_u32_e32 vcc, v217, v214\n" \
"s_lshl_b32 s4, s4, 6\n" \
"v_mfma_f32_16x16x32_bf16 a[68:71], a[164:167], a[144:147], a[68:71]\n" \
"v_cndmask_b32_e32 v217, 0, v220, vcc\n" \
"v_or_b32_e32 v13, v221, v217\n" \
"v_mfma_f32_16x16x32_bf16 a[68:71], a[228:231], a[208:211], a[68:71]\n" \
"ds_read_b64_tr_b16 a[250:251], v38 offset:41984\n" \
"v_or_b32_e64 v217, 16, v219\n" \
"v_mfma_f32_16x16x32_bf16 a[72:75], a[168:171], a[144:147], a[72:75]\n" \
"v_cmp_gt_u32_e32 vcc, v217, v214\n" \
"s_nop 1\n" \
"v_cndmask_b32_e64 v221, v215, 0, vcc\n" \
"v_mfma_f32_16x16x32_bf16 a[72:75], a[232:235], a[208:211], a[72:75]\n" \
"v_cmp_lt_u32_e32 vcc, v217, v214\n" \
"s_nop 1\n" \
"v_cndmask_b32_e64 v217, 0, v220, vcc\n" \
"v_mfma_f32_16x16x32_bf16 a[76:79], a[172:175], a[144:147], a[76:79]\n" \
"v_or_b32_e32 v14, v221, v217\n" \
"v_or_b32_e32 v217, 18, v219\n" \
"v_mfma_f32_16x16x32_bf16 a[76:79], a[236:239], a[208:211], a[76:79]\n" \
"ds_read_b64_tr_b16 a[188:189], v39 offset:32768\n" \
"v_cmp_gt_u32_e64 vcc, v217, v214\n" \
"v_mfma_f32_16x16x32_bf16 a[92:95], a[172:175], a[148:151], a[92:95]\n" \
"s_nop 0\n" \
"v_cndmask_b32_e64 v215, v215, 0, vcc\n" \
"v_cmp_lt_u32_e32 vcc, v217, v214\n" \
"v_mfma_f32_16x16x32_bf16 a[92:95], a[236:239], a[212:215], a[92:95]\n" \
"s_nop 0\n" \
"v_cndmask_b32_e32 v214, 0, v220, vcc\n" \
"v_or_b32_e64 v15, v215, v214\n" \
"v_mfma_f32_16x16x32_bf16 a[88:91], a[168:171], a[148:151], a[88:91]\n" \
"ds_read_b64_tr_b16 a[190:191], v39 offset:33792\n" \
"v_mad_u64_u32 v[214:215], s[6:7], s5, 42, v[212:213]\n" \
"v_mfma_f32_16x16x32_bf16 a[88:91], a[232:235], a[212:215], a[88:91]\n" \
"s_movk_i32 s5, 0x1440\n" \
"v_cmp_gt_u32_e32 vcc, s5, v214\n" \
"v_mfma_f32_16x16x32_bf16 a[84:87], a[164:167], a[148:151], a[84:87]\n" \
"v_min_u32_e32 v217, 0xa43f, v214\n" \
"v_mov_b32_e64 v214, s24\n" \
"v_mfma_f32_16x16x32_bf16 a[84:87], a[228:231], a[212:215], a[84:87]\n" \
"ds_read_b64_tr_b16 a[252:253], v39 offset:40960\n" \
"v_cndmask_b32_e64 v215, v216, v213, vcc\n" \
"v_mfma_f32_16x16x32_bf16 a[80:83], a[160:163], a[148:151], a[80:83]\n" \
"v_mov_b32_e32 v213, s22\n" \
"s_movk_i32 s6, 0xffee\n" \
"v_mfma_f32_16x16x32_bf16 a[80:83], a[224:227], a[212:215], a[80:83]\n" \
"v_cndmask_b32_e64 v214, v214, v213, vcc\n" \
"v_add_u32_e32 v213, 0xffffebc0, v217\n" \
"v_mfma_f32_16x16x32_bf16 a[96:99], a[160:163], a[152:155], a[96:99]\n" \
"ds_read_b64_tr_b16 a[254:255], v39 offset:41984\n" \
"s_lshl_b32 s5, s2, 5\n" \
"v_mfma_f32_16x16x32_bf16 a[96:99], a[224:227], a[216:219], a[96:99]\n" \
"v_cndmask_b32_e32 v213, v213, v217, vcc\n" \
"v_lshlrev_b32_e64 v216, 7, v213\n" \
"v_mfma_f32_16x16x32_bf16 a[100:103], a[164:167], a[152:155], a[100:103]\n" \
"v_mul_u32_u24_e32 v213, 0xe39, v212\n" \
"v_mov_b32_e64 v217, 0\n" \
"v_mfma_f32_16x16x32_bf16 a[100:103], a[228:231], a[216:219], a[100:103]\n" \
"v_lshrrev_b32_e64 v213, 16, v213\n" \
"v_lshl_add_u64 v[16:17], v[214:215], 0, v[216:217]\n" \
"v_mfma_f32_16x16x32_bf16 a[104:107], a[168:171], a[152:155], a[104:107]\n" \
"v_mad_i32_i24 v214, v213, s6, v212\n" \
"s_add_i32 s5, s5, 32\n" \
"v_mfma_f32_16x16x32_bf16 a[104:107], a[232:235], a[216:219], a[104:107]\n" \
"v_lshlrev_b32_e32 v215, 4, v214\n" \
"v_add_u32_e64 v216, s5, v215\n" \
"v_mfma_f32_16x16x32_bf16 a[108:111], a[172:175], a[152:155], a[108:111]\n" \
"v_and_b32_e32 v216, 0xf0, v216\n" \
"v_cmp_gt_u32_e64 vcc, 16, v214\n" \
"v_mfma_f32_16x16x32_bf16 a[108:111], a[236:239], a[216:219], a[108:111]\n" \
"s_movk_i32 s7, 0xf800\n" \
"v_cndmask_b32_e32 v214, v215, v216, vcc\n" \
"s_waitcnt lgkmcnt(15)\n" \
"v_mfma_f32_16x16x32_bf16 a[124:127], a[172:175], a[156:159], a[124:127]\n" \
"v_or_b32_e32 v215, 64, v212\n" \
"v_mul_u32_u24_e32 v216, 0xe39, v215\n" \
"v_mfma_f32_16x16x32_bf16 a[124:127], a[236:239], a[220:223], a[124:127]\n" \
"v_lshrrev_b32_e64 v216, 16, v216\n" \
"v_mad_i32_i24 v215, v216, s6, v215\n" \
"v_mfma_f32_16x16x32_bf16 a[120:123], a[168:171], a[156:159], a[120:123]\n" \
"v_lshlrev_b32_e32 v217, 4, v215\n" \
"v_add_u32_e32 v220, s5, v217\n" \
"v_mfma_f32_16x16x32_bf16 a[120:123], a[232:235], a[220:223], a[120:123]\n" \
"v_and_b32_e32 v220, 0xf0, v220\n" \
"v_cmp_gt_u32_e64 vcc, 16, v215\n" \
"v_mfma_f32_16x16x32_bf16 a[116:119], a[164:167], a[156:159], a[116:119]\n" \
"s_nop 0\n" \
"v_cndmask_b32_e32 v215, v217, v220, vcc\n" \
"v_or_b32_e32 v217, 0x80, v212\n" \
"v_mfma_f32_16x16x32_bf16 a[116:119], a[228:231], a[220:223], a[116:119]\n" \
"v_mul_u32_u24_e32 v220, 0xe39, v217\n" \
"v_lshrrev_b32_e64 v220, 16, v220\n" \
"v_mfma_f32_16x16x32_bf16 a[112:115], a[160:163], a[156:159], a[112:115]\n" \
"v_mad_i32_i24 v217, v220, s6, v217\n" \
"v_lshlrev_b32_e64 v221, 4, v217\n" \
"v_mfma_f32_16x16x32_bf16 a[112:115], a[224:227], a[220:223], a[112:115]\n" \
"s_waitcnt vmcnt(20)\n" \
"v_readfirstlane_b32 s62, v180\n" \
"s_nop 3\n" \
"s_add_u32 s62, s62, 0x9E3779B1\n" \
"s_waitcnt vmcnt(2) lgkmcnt(0)\n" \
"s_barrier\n" \
"v_mfma_f32_16x16x32_bf16 v[52:55], a[176:179], a[128:131], v[52:55]\n" \
"s_add_u32 m0, s30, 0x8000\n" \
"s_movk_i32 s84, 0x180\n" \
"v_mfma_f32_16x16x32_bf16 v[52:55], a[240:243], a[192:195], v[52:55]\n" \
"buffer_load_dwordx4 v22, s[64:67], s84 offen offset:0 lds\n" \
"v_mfma_f32_16x16x32_bf16 v[56:59], a[180:183], a[128:131], v[56:59]\n" \
"ds_read_b64_tr_b16 a[160:161], v32 offset:0\n" \
"v_add_u32_e64 v222, s5, v221\n" \
"v_mfma_f32_16x16x32_bf16 v[56:59], a[244:247], a[192:195], v[56:59]\n" \
"s_movk_i32 s84, 0x1d80\n" \
"v_mfma_f32_16x16x32_bf16 v[60:63], a[184:187], a[128:131], v[60:63]\n" \
"buffer_load_dwordx4 v23, s[64:67], s84 offen offset:1024 lds\n" \
"v_mfma_f32_16x16x32_bf16 v[60:63], a[248:251], a[192:195], v[60:63]\n" \
"ds_read_b64_tr_b16 a[162:163], v32 offset:1024\n" \
"v_and_b32_e32 v222, 0xf0, v222\n" \
"v_mfma_f32_16x16x32_bf16 v[64:67], a[188:191], a[128:131], v[64:67]\n" \
"ds_read_b64_tr_b16 a[224:225], v32 offset:8192\n" \
"v_cmp_gt_u32_e32 vcc, 16, v217\n" \
"v_mfma_f32_16x16x32_bf16 v[64:67], a[252:255], a[192:195], v[64:67]\n" \
"ds_read_b128 a[128:131], v28 offset:16384\n" \
"v_cndmask_b32_e64 v217, v221, v222, vcc\n" \
"v_mfma_f32_16x16x32_bf16 v[80:83], a[188:191], a[132:135], v[80:83]\n" \
"ds_read_b128 a[192:195], v29 offset:16384\n" \
"v_or_b32_e32 v221, 0xc0, v212\n" \
"v_mfma_f32_16x16x32_bf16 v[80:83], a[252:255], a[196:199], v[80:83]\n" \
"s_movk_i32 s84, 0x3980\n" \
"v_mfma_f32_16x16x32_bf16 v[76:79], a[184:187], a[132:135], v[76:79]\n" \
"buffer_load_dwordx4 v22, s[64:67], s84 offen offset:2048 lds\n" \
"v_mfma_f32_16x16x32_bf16 v[76:79], a[248:251], a[196:199], v[76:79]\n" \
"s_movk_i32 s84, 0x5580\n" \
"v_mfma_f32_16x16x32_bf16 v[72:75], a[180:183], a[132:135], v[72:75]\n" \
"buffer_load_dwordx4 v23, s[64:67], s84 offen offset:3072 lds\n" \
"v_mfma_f32_16x16x32_bf16 v[72:75], a[244:247], a[196:199], v[72:75]\n" \
"s_add_u32 m0, s31, 0x8000\n" \
"s_mov_b32 s84, 0x180000\n" \
"v_mfma_f32_16x16x32_bf16 v[68:71], a[176:179], a[132:135], v[68:71]\n" \
"buffer_load_dwordx4 v26, s[68:71], s84 offen offset:0 lds\n" \
"v_mfma_f32_16x16x32_bf16 v[68:71], a[240:243], a[196:199], v[68:71]\n" \
"ds_read_b128 a[132:135], v28 offset:18432\n" \
"v_mul_u32_u24_e32 v222, 0xe39, v221\n" \
"v_mfma_f32_16x16x32_bf16 v[84:87], a[176:179], a[136:139], v[84:87]\n" \
"ds_read_b128 a[196:199], v29 offset:18432\n" \
"v_lshrrev_b32_e64 v222, 16, v222\n" \
"v_mfma_f32_16x16x32_bf16 v[84:87], a[240:243], a[200:203], v[84:87]\n" \
"s_addk_i32 s84, 0x7c00\n" \
"v_mfma_f32_16x16x32_bf16 v[88:91], a[180:183], a[136:139], v[88:91]\n" \
"buffer_load_dwordx4 v26, s[68:71], s84 offen offset:1024 lds\n" \
"v_mfma_f32_16x16x32_bf16 v[88:91], a[244:247], a[200:203], v[88:91]\n" \
"ds_read_b64_tr_b16 a[226:227], v32 offset:9216\n" \
"v_mad_i32_i24 v221, v222, s6, v221\n" \
"v_mfma_f32_16x16x32_bf16 v[92:95], a[184:187], a[136:139], v[92:95]\n" \
"s_addk_i32 s84, 0x7c00\n" \
"v_mfma_f32_16x16x32_bf16 v[92:95], a[248:251], a[200:203], v[92:95]\n" \
"buffer_load_dwordx4 v27, s[68:71], s84 offen offset:2048 lds\n" \
"v_mfma_f32_16x16x32_bf16 v[96:99], a[188:191], a[136:139], v[96:99]\n" \
"ds_read_b64_tr_b16 a[164:165], v33 offset:0\n" \
"v_lshlrev_b32_e64 v223, 4, v221\n" \
"v_mfma_f32_16x16x32_bf16 v[96:99], a[252:255], a[200:203], v[96:99]\n" \
"ds_read_b128 a[136:139], v28 offset:20480\n" \
"v_add_u32_e64 v224, s5, v223\n" \
"v_mfma_f32_16x16x32_bf16 v[112:115], a[188:191], a[140:143], v[112:115]\n" \
"ds_read_b128 a[200:203], v29 offset:20480\n" \
"v_and_b32_e32 v224, 0xf0, v224\n" \
"v_mfma_f32_16x16x32_bf16 v[112:115], a[252:255], a[204:207], v[112:115]\n" \
"s_addk_i32 s84, 0x7c00\n" \
"v_mfma_f32_16x16x32_bf16 v[108:111], a[184:187], a[140:143], v[108:111]\n" \
"buffer_load_dwordx4 v27, s[68:71], s84 offen offset:3072 lds\n" \
"v_mfma_f32_16x16x32_bf16 v[108:111], a[248:251], a[204:207], v[108:111]\n" \
"s_add_u32 m0, s30, 0x9000\n" \
"s_mov_b32 s84, 0x8180\n" \
"v_mfma_f32_16x16x32_bf16 v[104:107], a[180:183], a[140:143], v[104:107]\n" \
"buffer_load_dwordx4 v22, s[64:67], s84 offen offset:0 lds\n" \
"v_mfma_f32_16x16x32_bf16 v[104:107], a[244:247], a[204:207], v[104:107]\n" \
"ds_read_b64_tr_b16 a[166:167], v33 offset:1024\n" \
"v_cmp_gt_u32_e32 vcc, 16, v221\n" \
"v_mfma_f32_16x16x32_bf16 v[100:103], a[176:179], a[140:143], v[100:103]\n" \
"ds_read_b64_tr_b16 a[228:229], v33 offset:8192\n" \
"v_cndmask_b32_e64 v221, v223, v224, vcc\n" \
"v_mfma_f32_16x16x32_bf16 v[100:103], a[240:243], a[204:207], v[100:103]\n" \
"ds_read_b128 a[140:143], v28 offset:22528\n" \
"v_or_b32_e32 v223, 0x100, v212\n" \
"v_mfma_f32_16x16x32_bf16 v[116:119], a[176:179], a[144:147], v[116:119]\n" \
"ds_read_b128 a[204:207], v29 offset:22528\n" \
"v_mul_u32_u24_e32 v224, 0xe39, v223\n" \
"v_mfma_f32_16x16x32_bf16 v[116:119], a[240:243], a[208:211], v[116:119]\n" \
"s_addk_i32 s84, 0x1c00\n" \
"v_mfma_f32_16x16x32_bf16 v[120:123], a[180:183], a[144:147], v[120:123]\n" \
"buffer_load_dwordx4 v23, s[64:67], s84 offen offset:1024 lds\n" \
"v_mfma_f32_16x16x32_bf16 v[120:123], a[244:247], a[208:211], v[120:123]\n" \
"s_addk_i32 s84, 0x1c00\n" \
"v_mfma_f32_16x16x32_bf16 v[124:127], a[184:187], a[144:147], v[124:127]\n" \
"buffer_load_dwordx4 v22, s[64:67], s84 offen offset:2048 lds\n" \
"v_mfma_f32_16x16x32_bf16 v[124:127], a[248:251], a[208:211], v[124:127]\n" \
"s_addk_i32 s84, 0x1c00\n" \
"v_mfma_f32_16x16x32_bf16 v[128:131], a[188:191], a[144:147], v[128:131]\n" \
"buffer_load_dwordx4 v23, s[64:67], s84 offen offset:3072 lds\n" \
"v_mfma_f32_16x16x32_bf16 v[128:131], a[252:255], a[208:211], v[128:131]\n" \
"ds_read_b128 a[144:147], v28 offset:0\n" \
"v_lshrrev_b32_e32 v224, 16, v224\n" \
"v_mfma_f32_16x16x32_bf16 v[144:147], a[188:191], a[148:151], v[144:147]\n" \
"ds_read_b128 a[208:211], v29 offset:0\n" \
"v_mad_i32_i24 v223, v224, s6, v223\n" \
"v_mfma_f32_16x16x32_bf16 v[144:147], a[252:255], a[212:215], v[144:147]\n" \
"s_add_u32 m0, s31, 0xc000\n" \
"s_mov_b32 s84, 0x180000\n" \
"v_mfma_f32_16x16x32_bf16 v[140:143], a[184:187], a[148:151], v[140:143]\n" \
"buffer_load_dwordx4 v26, s[72:75], s84 offen offset:0 lds\n" \
"v_mfma_f32_16x16x32_bf16 v[140:143], a[248:251], a[212:215], v[140:143]\n" \
"ds_read_b64_tr_b16 a[230:231], v33 offset:9216\n" \
"v_lshlrev_b32_e64 v225, 4, v223\n" \
"v_mfma_f32_16x16x32_bf16 v[136:139], a[180:183], a[148:151], v[136:139]\n" \
"s_addk_i32 s84, 0x7c00\n" \
"v_mfma_f32_16x16x32_bf16 v[136:139], a[244:247], a[212:215], v[136:139]\n" \
"buffer_load_dwordx4 v26, s[72:75], s84 offen offset:1024 lds\n" \
"v_mfma_f32_16x16x32_bf16 v[132:135], a[176:179], a[148:151], v[132:135]\n" \
"ds_read_b64_tr_b16 a[168:169], v34 offset:0\n" \
"v_add_u32_e32 v226, s5, v225\n" \
"v_mfma_f32_16x16x32_bf16 v[132:135], a[240:243], a[212:215], v[132:135]\n" \
"ds_read_b128 a[148:151], v28 offset:2048\n" \
"v_and_b32_e32 v226, 0xf0, v226\n" \
"v_mfma_f32_16x16x32_bf16 v[148:151], a[176:179], a[152:155], v[148:151]\n" \
"ds_read_b128 a[212:215], v29 offset:2048\n" \
"v_cmp_gt_u32_e64 vcc, 16, v223\n" \
"v_mfma_f32_16x16x32_bf16 v[148:151], a[240:243], a[216:219], v[148:151]\n" \
"ds_read_b64_tr_b16 a[170:171], v34 offset:1024\n" \
"v_cndmask_b32_e64 v223, v225, v226, vcc\n" \
"v_mfma_f32_16x16x32_bf16 v[152:155], a[180:183], a[152:155], v[152:155]\n" \
"ds_read_b64_tr_b16 a[232:233], v34 offset:8192\n" \
"v_or_b32_e32 v225, 0x140, v212\n" \
"v_mfma_f32_16x16x32_bf16 v[152:155], a[244:247], a[216:219], v[152:155]\n" \
"ds_read_b64_tr_b16 a[234:235], v34 offset:9216\n" \
"v_mul_u32_u24_e32 v226, 0xe39, v225\n" \
"v_mfma_f32_16x16x32_bf16 v[156:159], a[184:187], a[152:155], v[156:159]\n" \
"ds_read_b64_tr_b16 a[172:173], v35 offset:0\n" \
"v_lshrrev_b32_e64 v226, 16, v226\n" \
"v_mfma_f32_16x16x32_bf16 v[156:159], a[248:251], a[216:219], v[156:159]\n" \
"ds_read_b64_tr_b16 a[174:175], v35 offset:1024\n" \
"v_mad_i32_i24 v225, v226, s6, v225\n" \
"v_mfma_f32_16x16x32_bf16 v[160:163], a[188:191], a[152:155], v[160:163]\n" \
"ds_read_b64_tr_b16 a[236:237], v35 offset:8192\n" \
"v_lshlrev_b32_e64 v227, 4, v225\n" \
"v_mfma_f32_16x16x32_bf16 v[160:163], a[252:255], a[216:219], v[160:163]\n" \
"ds_read_b128 a[152:155], v28 offset:4096\n" \
"v_add_u32_e64 v228, s5, v227\n" \
"v_mfma_f32_16x16x32_bf16 v[176:179], a[188:191], a[156:159], v[176:179]\n" \
"ds_read_b128 a[216:219], v29 offset:4096\n" \
"v_and_b32_e32 v228, 0xf0, v228\n" \
"v_mfma_f32_16x16x32_bf16 v[176:179], a[252:255], a[220:223], v[176:179]\n" \
"ds_read_b64_tr_b16 a[238:239], v35 offset:9216\n" \
"v_cmp_gt_u32_e64 vcc, 16, v225\n" \
"v_mfma_f32_16x16x32_bf16 v[172:175], a[184:187], a[156:159], v[172:175]\n" \
"s_nop 0\n" \
"v_cndmask_b32_e32 v225, v227, v228, vcc\n" \
"v_or_b32_e32 v227, 0x180, v212\n" \
"v_mfma_f32_16x16x32_bf16 v[172:175], a[248:251], a[220:223], v[172:175]\n" \
"v_mul_u32_u24_e32 v228, 0xe39, v227\n" \
"v_lshrrev_b32_e64 v228, 16, v228\n" \
"v_mfma_f32_16x16x32_bf16 v[168:171], a[180:183], a[156:159], v[168:171]\n" \
"v_mad_i32_i24 v227, v228, s6, v227\n" \
"v_lshlrev_b32_e64 v229, 4, v227\n" \
"v_mfma_f32_16x16x32_bf16 v[168:171], a[244:247], a[220:223], v[168:171]\n" \
"v_add_u32_e64 v230, s5, v229\n" \
"v_and_b32_e32 v230, 0xf0, v230\n" \
"v_mfma_f32_16x16x32_bf16 v[164:167], a[176:179], a[156:159], v[164:167]\n" \
"v_cmp_gt_u32_e32 vcc, 16, v227\n" \
"s_nop 1\n" \
"v_cndmask_b32_e64 v227, v229, v230, vcc\n" \
"v_mfma_f32_16x16x32_bf16 v[164:167], a[240:243], a[220:223], v[164:167]\n" \
"ds_read_b128 a[156:159], v28 offset:6144\n" \
"ds_read_b128 a[220:223], v29 offset:6144\n" \
"s_waitcnt lgkmcnt(15)\n" \
"v_mfma_f32_16x16x32_bf16 a[0:3], a[160:163], a[128:131], a[0:3]\n" \
"s_addk_i32 s84, 0x7c00\n" \
"v_mfma_f32_16x16x32_bf16 a[0:3], a[224:227], a[192:195], a[0:3]\n" \
"buffer_load_dwordx4 v27, s[72:75], s84 offen offset:2048 lds\n" \
"v_mfma_f32_16x16x32_bf16 a[4:7], a[164:167], a[128:131], a[4:7]\n" \
"ds_read_b64_tr_b16 a[176:177], v36 offset:0\n" \
"v_or_b32_e32 v229, 0x1c0, v212\n" \
"s_waitcnt lgkmcnt(15)\n" \
"v_mfma_f32_16x16x32_bf16 a[4:7], a[228:231], a[192:195], a[4:7]\n" \
"s_addk_i32 s84, 0x7c00\n" \
"s_waitcnt lgkmcnt(11)\n" \
"v_mfma_f32_16x16x32_bf16 a[8:11], a[168:171], a[128:131], a[8:11]\n" \
"buffer_load_dwordx4 v27, s[72:75], s84 offen offset:3072 lds\n" \
"s_waitcnt lgkmcnt(9)\n" \
"v_mfma_f32_16x16x32_bf16 a[8:11], a[232:235], a[192:195], a[8:11]\n" \
"ds_read_b64_tr_b16 a[178:179], v36 offset:1024\n" \
"v_mul_u32_u24_e32 v230, 0xe39, v229\n" \
"s_waitcnt lgkmcnt(8)\n" \
"v_mfma_f32_16x16x32_bf16 a[12:15], a[172:175], a[128:131], a[12:15]\n" \
"ds_read_b64_tr_b16 a[240:241], v36 offset:8192\n" \
"v_lshrrev_b32_e32 v230, 16, v230\n" \
"s_waitcnt lgkmcnt(5)\n" \
"v_mfma_f32_16x16x32_bf16 a[12:15], a[236:239], a[192:195], a[12:15]\n" \
"v_mad_i32_i24 v229, v230, s6, v229\n" \
"v_lshlrev_b32_e32 v231, 4, v229\n" \
"v_mfma_f32_16x16x32_bf16 a[28:31], a[172:175], a[132:135], a[28:31]\n" \
"v_add_u32_e64 v232, s5, v231\n" \
"v_and_b32_e32 v232, 0xf0, v232\n" \
"v_mfma_f32_16x16x32_bf16 a[28:31], a[236:239], a[196:199], a[28:31]\n" \
"ds_read_b64_tr_b16 a[242:243], v36 offset:9216\n" \
"v_cmp_gt_u32_e64 vcc, 16, v229\n" \
"v_mfma_f32_16x16x32_bf16 a[24:27], a[168:171], a[132:135], a[24:27]\n" \
"buffer_load_dwordx4 v[200:203], v192, s[76:79], s34 offen offset:0\n" \
"v_mfma_f32_16x16x32_bf16 a[24:27], a[232:235], a[196:199], a[24:27]\n" \
"v_cndmask_b32_e64 v229, v231, v232, vcc\n" \
"v_or_b32_e32 v231, 0x200, v212\n" \
"v_mfma_f32_16x16x32_bf16 a[20:23], a[164:167], a[132:135], a[20:23]\n" \
"v_mul_u32_u24_e32 v232, 0xe39, v231\n" \
"v_lshrrev_b32_e64 v232, 16, v232\n" \
"v_mfma_f32_16x16x32_bf16 a[20:23], a[228:231], a[196:199], a[20:23]\n" \
"ds_read_b64_tr_b16 a[180:181], v37 offset:0\n" \
"v_mad_i32_i24 v231, v232, s6, v231\n" \
"v_mfma_f32_16x16x32_bf16 a[16:19], a[160:163], a[132:135], a[16:19]\n" \
"v_lshlrev_b32_e32 v233, 4, v231\n" \
"v_add_u32_e32 v234, s5, v233\n" \
"v_mfma_f32_16x16x32_bf16 a[16:19], a[224:227], a[196:199], a[16:19]\n" \
"s_movk_i32 s5, 0x480\n" \
"v_mad_u32_u24 v0, v213, s5, v214\n" \
"v_mfma_f32_16x16x32_bf16 a[32:35], a[160:163], a[136:139], a[32:35]\n" \
"buffer_load_dwordx4 v[204:207], v193, s[76:79], s34 offen offset:0\n" \
"v_mfma_f32_16x16x32_bf16 a[32:35], a[224:227], a[200:203], a[32:35]\n" \
"s_mov_b32 m0, s32\n" \
"ds_read_b64_tr_b16 a[182:183], v37 offset:1024\n" \
"v_mfma_f32_16x16x32_bf16 a[36:39], a[164:167], a[136:139], a[36:39]\n" \
"s_mov_b32 exec_lo, 0x0\n" \
"s_mov_b32 exec_hi, 0xff00\n" \
"global_load_lds_dword v[184:185], off\n" \
"s_mov_b64 exec, -1\n" \
"v_mfma_f32_16x16x32_bf16 a[36:39], a[228:231], a[200:203], a[36:39]\n" \
"ds_read_b64_tr_b16 a[244:245], v37 offset:8192\n" \
"v_mul_u32_u24_e32 v213, 0x480, v216\n" \
"v_mfma_f32_16x16x32_bf16 a[40:43], a[168:171], a[136:139], a[40:43]\n" \
"s_movk_i32 s6, 0xfc00\n" \
"s_nop 0\n" \
"v_add3_u32 v1, v213, v215, s6\n" \
"v_mfma_f32_16x16x32_bf16 a[40:43], a[232:235], a[200:203], a[40:43]\n" \
"buffer_load_dword v208, v194, s[76:79], s34 offen offset:32\n" \
"v_mfma_f32_16x16x32_bf16 a[44:47], a[172:175], a[136:139], a[44:47]\n" \
"ds_read_b64_tr_b16 a[246:247], v37 offset:9216\n" \
"v_mul_u32_u24_e32 v213, 0x480, v220\n" \
"v_mfma_f32_16x16x32_bf16 a[44:47], a[236:239], a[200:203], a[44:47]\n" \
"v_add3_u32 v2, v213, v217, s7\n" \
"v_mul_u32_u24_e32 v213, 0x480, v222\n" \
"v_mfma_f32_16x16x32_bf16 a[60:63], a[172:175], a[140:143], a[60:63]\n" \
"v_add3_u32 v3, v213, v221, s40\n" \
"v_mul_u32_u24_e32 v213, 0x480, v226\n" \
"v_mfma_f32_16x16x32_bf16 a[60:63], a[236:239], a[204:207], a[60:63]\n" \
"v_add3_u32 v5, v213, v225, s6\n" \
"v_mul_u32_u24_e32 v213, 0x480, v228\n" \
"v_mfma_f32_16x16x32_bf16 a[56:59], a[168:171], a[140:143], a[56:59]\n" \
"buffer_load_dword v209, v194, s[76:79], s34 offen offset:1184\n" \
"v_mfma_f32_16x16x32_bf16 a[56:59], a[232:235], a[204:207], a[56:59]\n" \
"ds_read_b64_tr_b16 a[184:185], v38 offset:0\n" \
"v_add3_u32 v6, v213, v227, s7\n" \
"v_mfma_f32_16x16x32_bf16 a[52:55], a[164:167], a[140:143], a[52:55]\n" \
"v_mul_u32_u24_e32 v213, 0x480, v230\n" \
"v_add3_u32 v7, v213, v229, s40\n" \
"v_mfma_f32_16x16x32_bf16 a[52:55], a[228:231], a[204:207], a[52:55]\n" \
"ds_read_b64_tr_b16 a[186:187], v38 offset:1024\n" \
"s_lshl_b32 s7, s3, 3\n" \
"v_mfma_f32_16x16x32_bf16 a[48:51], a[160:163], a[140:143], a[48:51]\n" \
"s_lshl_b32 s40, s3, 1\n" \
"s_and_b32 s7, s7, 16\n" \
"v_mfma_f32_16x16x32_bf16 a[48:51], a[224:227], a[204:207], a[48:51]\n" \
"s_and_b32 s40, s40, 2\n" \
"s_and_b32 s6, s2, 7\n" \
"v_mfma_f32_16x16x32_bf16 a[64:67], a[160:163], a[144:147], a[64:67]\n" \
"buffer_load_dword v211, v194, s[76:79], s34 offen offset:2336\n" \
"v_mfma_f32_16x16x32_bf16 a[64:67], a[224:227], a[208:211], a[64:67]\n" \
"ds_read_b64_tr_b16 a[248:249], v38 offset:8192\n" \
"v_bfe_u32 v213, v20, 7, 1\n" \
"v_mfma_f32_16x16x32_bf16 a[68:71], a[164:167], a[144:147], a[68:71]\n" \
"s_or_b32 s7, s40, s7\n" \
"v_and_b32_e32 v234, 0xf0, v234\n" \
"v_mfma_f32_16x16x32_bf16 a[68:71], a[228:231], a[208:211], a[68:71]\n" \
"ds_read_b64_tr_b16 a[250:251], v38 offset:9216\n" \
"v_cmp_gt_u32_e64 vcc, 16, v231\n" \
"v_mfma_f32_16x16x32_bf16 a[72:75], a[168:171], a[144:147], a[72:75]\n" \
"v_or3_b32 v213, s7, v213, v219\n" \
"v_and_b32_e64 v214, 32, v20\n" \
"v_mfma_f32_16x16x32_bf16 a[72:75], a[232:235], a[208:211], a[72:75]\n" \
"s_mov_b32 s76, s20\n" \
"s_and_b32 s77, s21, 0xffff\n" \
"v_mfma_f32_16x16x32_bf16 a[76:79], a[172:175], a[144:147], a[76:79]\n" \
"s_mov_b32 s78, 0x480\n" \
"s_lshl_b32 s6, s6, 5\n" \
"v_mfma_f32_16x16x32_bf16 a[76:79], a[236:239], a[208:211], a[76:79]\n" \
"buffer_load_ushort v210, v195, s[76:79], 0 offen\n" \
"v_mfma_f32_16x16x32_bf16 a[92:95], a[172:175], a[148:151], a[92:95]\n" \
"ds_read_b64_tr_b16 a[188:189], v39 offset:0\n" \
"v_cndmask_b32_e64 v231, v233, v234, vcc\n" \
"v_mfma_f32_16x16x32_bf16 a[92:95], a[236:239], a[212:215], a[92:95]\n" \
"v_mul_u32_u24_e64 v213, 18, v213\n" \
"v_mov_b32_e32 v215, 0x100\n" \
"v_mfma_f32_16x16x32_bf16 a[88:91], a[168:171], a[148:151], a[88:91]\n" \
"s_mov_b32 s76, s8\n" \
"s_and_b32 s77, s9, 0xffff\n" \
"v_mfma_f32_16x16x32_bf16 a[88:91], a[232:235], a[212:215], a[88:91]\n" \
"s_mov_b32 s78, 0x240000\n" \
"ds_read_b64_tr_b16 a[190:191], v39 offset:1024\n" \
".fill (L_pb + 7904 - .) / 4, 4, 0xBF800000\n" \
"L_pp2:\n" \
"s_cbranch_execz L_pret\n" \
"v_mfma_f32_16x16x32_bf16 a[84:87], a[164:167], a[148:151], a[84:87]\n" \
"v_mov_b32_e32 v216, s6\n" \
"v_cmp_eq_u32_e64 vcc, 0, v214\n" \
"v_mfma_f32_16x16x32_bf16 a[84:87], a[228:231], a[212:215], a[84:87]\n" \
"ds_read_b64_tr_b16 a[252:253], v39 offset:8192\n" \
"v_and_or_b32 v213, v218, 1, v213\n" \
"v_mfma_f32_16x16x32_bf16 a[80:83], a[160:163], a[148:151], a[80:83]\n" \
"v_lshlrev_b32_e32 v212, 3, v212\n" \
"v_cndmask_b32_e32 v214, v215, v216, vcc\n" \
"v_mfma_f32_16x16x32_bf16 a[80:83], a[224:227], a[212:215], a[80:83]\n" \
"v_mad_u32_u24 v213, v213, s5, v214\n" \
"v_and_or_b32 v213, v20, 16, v213\n" \
"v_mfma_f32_16x16x32_bf16 a[96:99], a[160:163], a[152:155], a[96:99]\n" \
"ds_read_b64_tr_b16 a[254:255], v39 offset:9216\n" \
"v_add_u32_e32 v9, 0x4800, v213\n" \
"v_mfma_f32_16x16x32_bf16 a[96:99], a[224:227], a[216:219], a[96:99]\n" \
"v_mul_u32_u24_e32 v213, 0x120, v218\n" \
"v_and_or_b32 v10, v212, 24, v213\n" \
"v_mfma_f32_16x16x32_bf16 a[100:103], a[164:167], a[152:155], a[100:103]\n" \
"v_lshrrev_b32_e64 v213, 2, v20\n" \
"v_and_or_b32 v213, v213, 60, s4\n" \
"v_mfma_f32_16x16x32_bf16 a[100:103], a[228:231], a[216:219], a[100:103]\n" \
"v_lshl_or_b32 v212, s3, 9, v212\n" \
"v_mad_u32_u24 v4, v224, s5, v223\n" \
"v_mfma_f32_16x16x32_bf16 a[104:107], a[168:171], a[152:155], a[104:107]\n" \
"v_mad_u32_u24 v8, v232, s5, v231\n" \
"v_or_b32_e32 v18, 0x27000, v213\n" \
"v_mfma_f32_16x16x32_bf16 a[104:107], a[232:235], a[216:219], a[104:107]\n" \
"v_or_b32_e32 v19, 0x27800, v212\n" \
"v_mov_b32_e32 v215, 0x1000c0c\n" \
"v_mfma_f32_16x16x32_bf16 a[108:111], a[172:175], a[152:155], a[108:111]\n" \
"v_mov_b32_e32 v216, 0xc0c0100\n" \
"v_bfe_u32 v214, v20, 4, 1\n" \
"v_mfma_f32_16x16x32_bf16 a[108:111], a[236:239], a[216:219], a[108:111]\n" \
"v_lshrrev_b32_e32 v212, 8, v20\n" \
"v_cmp_eq_u32_e32 vcc, 0, v214\n" \
"s_waitcnt lgkmcnt(15)\n" \
"v_mfma_f32_16x16x32_bf16 a[124:127], a[172:175], a[156:159], a[124:127]\n" \
"v_bfe_u32 v213, v20, 4, 4\n" \
"v_mov_b32_e32 v217, 0xc0c0302\n" \
"v_mfma_f32_16x16x32_bf16 a[124:127], a[236:239], a[220:223], a[124:127]\n" \
"v_cndmask_b32_e32 v215, v215, v216, vcc\n" \
"v_mov_b32_e32 v216, 0x3020c0c\n" \
"v_mfma_f32_16x16x32_bf16 a[120:123], a[168:171], a[156:159], a[120:123]\n" \
"v_lshl_or_b32 v214, v212, 2, v214\n" \
"v_cndmask_b32_e64 v216, v216, v217, vcc\n" \
"v_mfma_f32_16x16x32_bf16 a[120:123], a[232:235], a[220:223], a[120:123]\n" \
"v_mov_b32_e32 v217, 0xc0c0c0c\n" \
"v_cmp_gt_u32_e64 vcc, v214, v213\n" \
"v_mfma_f32_16x16x32_bf16 a[116:119], a[164:167], a[156:159], a[116:119]\n" \
"v_or_b32_e32 v214, 2, v214\n" \
"s_lshr_b32 s4, s2, 5\n" \
"v_mfma_f32_16x16x32_bf16 a[116:119], a[228:231], a[220:223], a[116:119]\n" \
"v_cndmask_b32_e32 v40, v217, v215, vcc\n" \
"v_cndmask_b32_e32 v42, v215, v217, vcc\n" \
"v_mfma_f32_16x16x32_bf16 a[112:115], a[160:163], a[156:159], a[112:115]\n" \
"v_cmp_gt_u32_e32 vcc, v214, v213\n" \
"v_and_b32_e32 v214, 16, v20\n" \
"v_mfma_f32_16x16x32_bf16 a[112:115], a[224:227], a[220:223], a[112:115]\n" \
"s_waitcnt vmcnt(7) lgkmcnt(0)\n" \
"s_barrier\n" \
"v_mfma_f32_16x16x32_bf16 v[52:55], a[176:179], a[128:131], v[52:55]\n" \
"s_add_u32 m0, s30, 0x0\n" \
"s_movk_i32 s84, 0x200\n" \
"v_mfma_f32_16x16x32_bf16 v[52:55], a[240:243], a[192:195], v[52:55]\n" \
"buffer_load_dwordx4 v22, s[64:67], s84 offen offset:0 lds\n" \
"v_mfma_f32_16x16x32_bf16 v[56:59], a[180:183], a[128:131], v[56:59]\n" \
"ds_read_b64_tr_b16 a[160:161], v32 offset:32768\n" \
"s_movk_i32 s42, 0x3f80\n" \
"v_mfma_f32_16x16x32_bf16 v[56:59], a[244:247], a[192:195], v[56:59]\n" \
"s_movk_i32 s84, 0x1e00\n" \
"v_mfma_f32_16x16x32_bf16 v[60:63], a[184:187], a[128:131], v[60:63]\n" \
"buffer_load_dwordx4 v23, s[64:67], s84 offen offset:1024 lds\n" \
"v_mfma_f32_16x16x32_bf16 v[60:63], a[248:251], a[192:195], v[60:63]\n" \
"ds_read_b64_tr_b16 a[162:163], v32 offset:33792\n" \
"s_xor_b32 s5, s4, 15\n" \
"v_mfma_f32_16x16x32_bf16 v[64:67], a[188:191], a[128:131], v[64:67]\n" \
"ds_read_b64_tr_b16 a[224:225], v32 offset:40960\n" \
"v_mfma_f32_16x16x32_bf16 v[64:67], a[252:255], a[192:195], v[64:67]\n" \
"ds_read_b128 a[128:131], v28 offset:49152\n" \
"v_mfma_f32_16x16x32_bf16 v[80:83], a[188:191], a[132:135], v[80:83]\n" \
"ds_read_b128 a[192:195], v29 offset:49152\n" \
"v_mfma_f32_16x16x32_bf16 v[80:83], a[252:255], a[196:199], v[80:83]\n" \
"s_movk_i32 s84, 0x3a00\n" \
"v_mfma_f32_16x16x32_bf16 v[76:79], a[184:187], a[132:135], v[76:79]\n" \
"buffer_load_dwordx4 v22, s[64:67], s84 offen offset:2048 lds\n" \
"v_mfma_f32_16x16x32_bf16 v[76:79], a[248:251], a[196:199], v[76:79]\n" \
"s_movk_i32 s84, 0x5600\n" \
"v_mfma_f32_16x16x32_bf16 v[72:75], a[180:183], a[132:135], v[72:75]\n" \
"buffer_load_dwordx4 v23, s[64:67], s84 offen offset:3072 lds\n" \
"v_mfma_f32_16x16x32_bf16 v[72:75], a[244:247], a[196:199], v[72:75]\n" \
"s_add_u32 m0, s31, 0x0\n" \
"s_mov_b32 s84, 0x200000\n" \
"v_mfma_f32_16x16x32_bf16 v[68:71], a[176:179], a[132:135], v[68:71]\n" \
"buffer_load_dwordx4 v26, s[68:71], s84 offen offset:0 lds\n" \
"v_mfma_f32_16x16x32_bf16 v[68:71], a[240:243], a[196:199], v[68:71]\n" \
"ds_read_b128 a[132:135], v28 offset:51200\n" \
"v_mfma_f32_16x16x32_bf16 v[84:87], a[176:179], a[136:139], v[84:87]\n" \
"ds_read_b128 a[196:199], v29 offset:51200\n" \
"v_mfma_f32_16x16x32_bf16 v[84:87], a[240:243], a[200:203], v[84:87]\n" \
"s_addk_i32 s84, 0x7c00\n" \
"v_mfma_f32_16x16x32_bf16 v[88:91], a[180:183], a[136:139], v[88:91]\n" \
"buffer_load_dwordx4 v26, s[68:71], s84 offen offset:1024 lds\n" \
"v_mfma_f32_16x16x32_bf16 v[88:91], a[244:247], a[200:203], v[88:91]\n" \
"ds_read_b64_tr_b16 a[226:227], v32 offset:41984\n" \
"v_mfma_f32_16x16x32_bf16 v[92:95], a[184:187], a[136:139], v[92:95]\n" \
"s_addk_i32 s84, 0x7c00\n" \
"v_mfma_f32_16x16x32_bf16 v[92:95], a[248:251], a[200:203], v[92:95]\n" \
"buffer_load_dwordx4 v27, s[68:71], s84 offen offset:2048 lds\n" \
"v_mfma_f32_16x16x32_bf16 v[96:99], a[188:191], a[136:139], v[96:99]\n" \
"ds_read_b64_tr_b16 a[164:165], v33 offset:32768\n" \
"v_mfma_f32_16x16x32_bf16 v[96:99], a[252:255], a[200:203], v[96:99]\n" \
"ds_read_b128 a[136:139], v28 offset:53248\n" \
"v_mfma_f32_16x16x32_bf16 v[112:115], a[188:191], a[140:143], v[112:115]\n" \
"ds_read_b128 a[200:203], v29 offset:53248\n" \
"v_mfma_f32_16x16x32_bf16 v[112:115], a[252:255], a[204:207], v[112:115]\n" \
"s_addk_i32 s84, 0x7c00\n" \
"s_nop 0\n" \
"v_mfma_f32_16x16x32_bf16 v[108:111], a[184:187], a[140:143], v[108:111]\n" \
"buffer_load_dwordx4 v27, s[68:71], s84 offen offset:3072 lds\n" \
"v_mfma_f32_16x16x32_bf16 v[108:111], a[248:251], a[204:207], v[108:111]\n" \
"s_add_u32 m0, s30, 0x1000\n" \
"s_mov_b32 s84, 0x8200\n" \
"v_mfma_f32_16x16x32_bf16 v[104:107], a[180:183], a[140:143], v[104:107]\n" \
"buffer_load_dwordx4 v22, s[64:67], s84 offen offset:0 lds\n" \
"v_mfma_f32_16x16x32_bf16 v[104:107], a[244:247], a[204:207], v[104:107]\n" \
"ds_read_b64_tr_b16 a[166:167], v33 offset:33792\n" \
"v_mfma_f32_16x16x32_bf16 v[100:103], a[176:179], a[140:143], v[100:103]\n" \
"ds_read_b64_tr_b16 a[228:229], v33 offset:40960\n" \
"v_mfma_f32_16x16x32_bf16 v[100:103], a[240:243], a[204:207], v[100:103]\n" \
"ds_read_b128 a[140:143], v28 offset:55296\n" \
"v_mfma_f32_16x16x32_bf16 v[116:119], a[176:179], a[144:147], v[116:119]\n" \
"ds_read_b128 a[204:207], v29 offset:55296\n" \
"v_mfma_f32_16x16x32_bf16 v[116:119], a[240:243], a[208:211], v[116:119]\n" \
"s_addk_i32 s84, 0x1c00\n" \
"v_mfma_f32_16x16x32_bf16 v[120:123], a[180:183], a[144:147], v[120:123]\n" \
"buffer_load_dwordx4 v23, s[64:67], s84 offen offset:1024 lds\n" \
"v_mfma_f32_16x16x32_bf16 v[120:123], a[244:247], a[208:211], v[120:123]\n" \
"s_addk_i32 s84, 0x1c00\n" \
"v_mfma_f32_16x16x32_bf16 v[124:127], a[184:187], a[144:147], v[124:127]\n" \
"buffer_load_dwordx4 v22, s[64:67], s84 offen offset:2048 lds\n" \
"v_mfma_f32_16x16x32_bf16 v[124:127], a[248:251], a[208:211], v[124:127]\n" \
"s_addk_i32 s84, 0x1c00\n" \
"s_nop 0\n" \
"v_mfma_f32_16x16x32_bf16 v[128:131], a[188:191], a[144:147], v[128:131]\n" \
"buffer_load_dwordx4 v23, s[64:67], s84 offen offset:3072 lds\n" \
"v_mfma_f32_16x16x32_bf16 v[128:131], a[252:255], a[208:211], v[128:131]\n" \
"ds_read_b128 a[144:147], v28 offset:32768\n" \
"v_mfma_f32_16x16x32_bf16 v[144:147], a[188:191], a[148:151], v[144:147]\n" \
"ds_read_b128 a[208:211], v29 offset:32768\n" \
"v_mfma_f32_16x16x32_bf16 v[144:147], a[252:255], a[212:215], v[144:147]\n" \
"s_add_u32 m0, s31, 0x4000\n" \
"s_mov_b32 s84, 0x200000\n" \
"v_mfma_f32_16x16x32_bf16 v[140:143], a[184:187], a[148:151], v[140:143]\n" \
"buffer_load_dwordx4 v26, s[72:75], s84 offen offset:0 lds\n" \
"v_mfma_f32_16x16x32_bf16 v[140:143], a[248:251], a[212:215], v[140:143]\n" \
"ds_read_b64_tr_b16 a[230:231], v33 offset:41984\n" \
"v_mfma_f32_16x16x32_bf16 v[136:139], a[180:183], a[148:151], v[136:139]\n" \
"s_addk_i32 s84, 0x7c00\n" \
"s_nop 0\n" \
"v_mfma_f32_16x16x32_bf16 v[136:139], a[244:247], a[212:215], v[136:139]\n" \
"buffer_load_dwordx4 v26, s[72:75], s84 offen offset:1024 lds\n" \
"v_mfma_f32_16x16x32_bf16 v[132:135], a[176:179], a[148:151], v[132:135]\n" \
"ds_read_b64_tr_b16 a[168:169], v34 offset:32768\n" \
"v_mfma_f32_16x16x32_bf16 v[132:135], a[240:243], a[212:215], v[132:135]\n" \
"ds_read_b128 a[148:151], v28 offset:34816\n" \
"v_mfma_f32_16x16x32_bf16 v[148:151], a[176:179], a[152:155], v[148:151]\n" \
"ds_read_b128 a[212:215], v29 offset:34816\n" \
"v_mfma_f32_16x16x32_bf16 v[148:151], a[240:243], a[216:219], v[148:151]\n" \
"ds_read_b64_tr_b16 a[170:171], v34 offset:33792\n" \
"v_mfma_f32_16x16x32_bf16 v[152:155], a[180:183], a[152:155], v[152:155]\n" \
"ds_read_b64_tr_b16 a[232:233], v34 offset:40960\n" \
"v_mfma_f32_16x16x32_bf16 v[152:155], a[244:247], a[216:219], v[152:155]\n" \
"ds_read_b64_tr_b16 a[234:235], v34 offset:41984\n" \
"v_mfma_f32_16x16x32_bf16 v[156:159], a[184:187], a[152:155], v[156:159]\n" \
"ds_read_b64_tr_b16 a[172:173], v35 offset:32768\n" \
"v_mfma_f32_16x16x32_bf16 v[156:159], a[248:251], a[216:219], v[156:159]\n" \
"ds_read_b64_tr_b16 a[174:175], v35 offset:33792\n" \
"v_mfma_f32_16x16x32_bf16 v[160:163], a[188:191], a[152:155], v[160:163]\n" \
"ds_read_b64_tr_b16 a[236:237], v35 offset:40960\n" \
"v_mfma_f32_16x16x32_bf16 v[160:163], a[252:255], a[216:219], v[160:163]\n" \
"ds_read_b128 a[152:155], v28 offset:36864\n" \
"v_mfma_f32_16x16x32_bf16 v[176:179], a[188:191], a[156:159], v[176:179]\n" \
"ds_read_b128 a[216:219], v29 offset:36864\n" \
"v_mfma_f32_16x16x32_bf16 v[176:179], a[252:255], a[220:223], v[176:179]\n" \
"ds_read_b64_tr_b16 a[238:239], v35 offset:41984\n" \
"v_mfma_f32_16x16x32_bf16 v[172:175], a[184:187], a[156:159], v[172:175]\n" \
"s_cmpk_lt_u32 s3, 2\n" \
"s_cselect_b32 s6, s5, s4\n" \
"v_mfma_f32_16x16x32_bf16 v[172:175], a[248:251], a[220:223], v[172:175]\n" \
"s_lshl_b32 s7, s6, 7\n" \
"v_and_b32_e32 v213, 0x320, v20\n" \
"v_mfma_f32_16x16x32_bf16 v[168:171], a[180:183], a[156:159], v[168:171]\n" \
"s_movk_i32 s41, 0x100\n" \
"v_lshlrev_b32_e64 v214, v214, s42\n" \
"v_mfma_f32_16x16x32_bf16 v[168:171], a[244:247], a[220:223], v[168:171]\n" \
"v_mov_b32_e32 v215, 0\n" \
"s_add_i32 s40, s7, -16\n" \
"v_mfma_f32_16x16x32_bf16 v[164:167], a[176:179], a[156:159], v[164:167]\n" \
"v_cndmask_b32_e32 v41, v217, v216, vcc\n" \
"v_cndmask_b32_e32 v43, v216, v217, vcc\n" \
"v_mfma_f32_16x16x32_bf16 v[164:167], a[240:243], a[220:223], v[164:167]\n" \
"ds_read_b128 a[156:159], v28 offset:38912\n" \
"ds_read_b128 a[220:223], v29 offset:38912\n" \
"s_waitcnt lgkmcnt(15)\n" \
"v_mfma_f32_16x16x32_bf16 a[0:3], a[160:163], a[128:131], a[0:3]\n" \
"s_addk_i32 s84, 0x7c00\n" \
"v_mfma_f32_16x16x32_bf16 a[0:3], a[224:227], a[192:195], a[0:3]\n" \
"buffer_load_dwordx4 v27, s[72:75], s84 offen offset:2048 lds\n" \
"v_mfma_f32_16x16x32_bf16 a[4:7], a[164:167], a[128:131], a[4:7]\n" \
"ds_read_b64_tr_b16 a[176:177], v36 offset:32768\n" \
"v_cmp_eq_u32_e32 vcc, s41, v213\n" \
"s_waitcnt lgkmcnt(15)\n" \
"v_mfma_f32_16x16x32_bf16 a[4:7], a[228:231], a[192:195], a[4:7]\n" \
"s_addk_i32 s84, 0x7c00\n" \
"s_waitcnt lgkmcnt(11)\n" \
"v_mfma_f32_16x16x32_bf16 a[8:11], a[168:171], a[128:131], a[8:11]\n" \
"buffer_load_dwordx4 v27, s[72:75], s84 offen offset:3072 lds\n" \
"s_waitcnt lgkmcnt(9)\n" \
"v_mfma_f32_16x16x32_bf16 a[8:11], a[232:235], a[192:195], a[8:11]\n" \
"ds_read_b64_tr_b16 a[178:179], v36 offset:33792\n" \
"s_mov_b32 s41, 0\n" \
"s_waitcnt lgkmcnt(8)\n" \
"v_mfma_f32_16x16x32_bf16 a[12:15], a[172:175], a[128:131], a[12:15]\n" \
"ds_read_b64_tr_b16 a[240:241], v36 offset:40960\n" \
"s_and_b32 s43, s3, 1\n" \
"s_waitcnt lgkmcnt(5)\n" \
"v_mfma_f32_16x16x32_bf16 a[12:15], a[236:239], a[192:195], a[12:15]\n" \
"v_cndmask_b32_e32 v46, v215, v214, vcc\n" \
"v_and_b32_e32 v213, 0x330, v20\n" \
"v_mfma_f32_16x16x32_bf16 a[28:31], a[172:175], a[132:135], a[28:31]\n" \
"s_cmp_lg_u32 s7, 0\n" \
"s_cselect_b32 s40, s40, s41\n" \
"v_mfma_f32_16x16x32_bf16 a[28:31], a[236:239], a[196:199], a[28:31]\n" \
"ds_read_b64_tr_b16 a[242:243], v36 offset:41984\n" \
"s_mov_b32 s42, 0xff800000\n" \
"v_mfma_f32_16x16x32_bf16 a[24:27], a[168:171], a[132:135], a[24:27]\n" \
"s_cmp_lg_u32 s7, 0\n" \
"s_cselect_b32 s47, s41, s42\n" \
"v_mfma_f32_16x16x32_bf16 a[24:27], a[232:235], a[196:199], a[24:27]\n" \
"s_lshl_b32 s41, s43, 6\n" \
"v_lshrrev_b32_e32 v215, 6, v20\n" \
"v_mfma_f32_16x16x32_bf16 a[20:23], a[164:167], a[132:135], a[20:23]\n" \
"v_mov_b32_e32 v214, 0x3f80\n" \
"v_cmp_eq_u32_e64 vcc, 32, v213\n" \
"v_mfma_f32_16x16x32_bf16 a[20:23], a[228:231], a[196:199], a[20:23]\n" \
"ds_read_b64_tr_b16 a[180:181], v37 offset:32768\n" \
"s_or_b32 s7, s41, s7\n" \
"v_mfma_f32_16x16x32_bf16 a[16:19], a[160:163], a[132:135], a[16:19]\n" \
"v_lshrrev_b32_e32 v213, 7, v20\n" \
"v_bfe_u32 v216, v20, 6, 2\n" \
"v_mfma_f32_16x16x32_bf16 a[16:19], a[224:227], a[196:199], a[16:19]\n" \
"v_lshrrev_b32_e64 v219, 1, v20\n" \
"v_bfe_u32 v215, v215, 1, 1\n" \
"v_mfma_f32_16x16x32_bf16 a[32:35], a[160:163], a[136:139], a[32:35]\n" \
"s_waitcnt vmcnt(16)\n" \
"v_or_b32_e32 v204, v210, v204\n" \
"v_or_b32_e64 v206, v210, v206\n" \
"v_mfma_f32_16x16x32_bf16 a[32:35], a[224:227], a[200:203], a[32:35]\n" \
"v_mov_b32_e64 v210, 0\n" \
"s_add_u32 s88, s35, 0x1000\n" \
"v_mfma_f32_16x16x32_bf16 a[36:39], a[164:167], a[136:139], a[36:39]\n" \
"ds_read_b64_tr_b16 a[182:183], v37 offset:33792\n" \
"v_cndmask_b32_e64 v47, 0, v214, vcc\n" \
"v_mfma_f32_16x16x32_bf16 a[36:39], a[228:231], a[200:203], a[36:39]\n" \
"ds_read_b64_tr_b16 a[244:245], v37 offset:40960\n" \
"v_or_b32_e64 v214, s7, v213\n" \
"v_mfma_f32_16x16x32_bf16 a[40:43], a[168:171], a[136:139], a[40:43]\n" \
"s_mov_b32 m0, s32\n" \
"v_lshl_or_b32 v217, s43, 3, v213\n" \
"v_mfma_f32_16x16x32_bf16 a[40:43], a[232:235], a[200:203], a[40:43]\n" \
"s_mov_b32 exec_lo, 0xffffffff\n" \
"s_mov_b32 exec_hi, 0x0\n" \
"global_load_lds_dword v[188:189], off\n" \
"s_mov_b64 exec, -1\n" \
"v_mfma_f32_16x16x32_bf16 a[44:47], a[172:175], a[136:139], a[44:47]\n" \
"ds_read_b64_tr_b16 a[246:247], v37 offset:41984\n" \
"v_lshlrev_b32_e64 v218, 10, v212\n" \
"v_mfma_f32_16x16x32_bf16 a[44:47], a[236:239], a[200:203], a[44:47]\n" \
"v_lshlrev_b32_e32 v216, 7, v216\n" \
"v_and_b32_e32 v219, 24, v219\n" \
"v_mfma_f32_16x16x32_bf16 a[60:63], a[172:175], a[140:143], a[60:63]\n" \
"ds_write_b128 v196, v[200:203]\n" \
"v_mfma_f32_16x16x32_bf16 a[60:63], a[236:239], a[204:207], a[60:63]\n" \
"v_and_or_b32 v213, v213, 2, v215\n" \
"v_add_u32_e64 v217, s40, v217\n" \
"v_mfma_f32_16x16x32_bf16 a[56:59], a[168:171], a[140:143], a[56:59]\n" \
"ds_read_b64_tr_b16 a[184:185], v38 offset:32768\n" \
"v_or3_b32 v216, v216, v218, v219\n" \
"v_mfma_f32_16x16x32_bf16 a[56:59], a[232:235], a[204:207], a[56:59]\n" \
"v_lshlrev_b32_e64 v213, 5, v213\n" \
"s_mov_b32 s40, 0x14000\n" \
"v_mfma_f32_16x16x32_bf16 a[52:55], a[164:167], a[140:143], a[52:55]\n" \
"s_lshl_b32 s7, s2, 2\n" \
"v_or3_b32 v48, v216, v213, s40\n" \
"v_mfma_f32_16x16x32_bf16 a[52:55], a[228:231], a[204:207], a[52:55]\n" \
"ds_read_b64_tr_b16 a[186:187], v38 offset:33792\n" \
"s_lshl_b32 s40, s43, 2\n" \
"v_mfma_f32_16x16x32_bf16 a[48:51], a[160:163], a[140:143], a[48:51]\n" \
"s_lshl_b32 s6, s6, 3\n" \
"v_or_b32_e32 v213, s40, v212\n" \
"v_mfma_f32_16x16x32_bf16 a[48:51], a[224:227], a[204:207], a[48:51]\n" \
"s_or_b32 s6, s6, s40\n" \
"s_and_b32 s40, s7, 0x7c\n" \
"v_mfma_f32_16x16x32_bf16 a[64:67], a[160:163], a[144:147], a[64:67]\n" \
"ds_write_b128 v197, v[204:207]\n" \
"v_mfma_f32_16x16x32_bf16 a[64:67], a[224:227], a[208:211], a[64:67]\n" \
"ds_read_b64_tr_b16 a[248:249], v38 offset:40960\n" \
"s_or_b32 s40, s40, s3\n" \
"v_mfma_f32_16x16x32_bf16 a[68:71], a[164:167], a[144:147], a[68:71]\n" \
"s_cmpk_lt_u32 s40, 64\n" \
"s_cselect_b32 s41, s4, s5\n" \
"v_mfma_f32_16x16x32_bf16 a[68:71], a[228:231], a[208:211], a[68:71]\n" \
"ds_read_b64_tr_b16 a[250:251], v38 offset:41984\n" \
"s_lshl_b32 s40, s40, 1\n" \
"v_mfma_f32_16x16x32_bf16 a[72:75], a[168:171], a[144:147], a[72:75]\n" \
"s_lshl_b32 s41, s41, 7\n" \
"s_and_b32 s40, s40, 0x7e\n" \
"v_mfma_f32_16x16x32_bf16 a[72:75], a[232:235], a[208:211], a[72:75]\n" \
"s_or_b32 s40, s41, s40\n" \
"s_mulk_i32 s40, 0x480\n" \
"v_mfma_f32_16x16x32_bf16 a[76:79], a[172:175], a[144:147], a[76:79]\n" \
"v_add_u32_e64 v49, s40, v20\n" \
"s_mov_b32 s40, 0xc000\n" \
"v_mfma_f32_16x16x32_bf16 a[76:79], a[236:239], a[208:211], a[76:79]\n" \
"ds_read_b64_tr_b16 a[188:189], v39 offset:32768\n" \
"s_and_b32 s7, s7, 28\n" \
"v_mfma_f32_16x16x32_bf16 a[92:95], a[172:175], a[148:151], a[92:95]\n" \
"ds_write_b128 v198, v[208:211]\n" \
"v_mfma_f32_16x16x32_bf16 a[92:95], a[236:239], a[212:215], a[92:95]\n" \
"s_bfe_u32 s45, s2, 0x20003\n" \
"s_mov_b32 s41, 0x8000\n" \
"v_mfma_f32_16x16x32_bf16 a[88:91], a[168:171], a[148:151], a[88:91]\n" \
"ds_read_b64_tr_b16 a[190:191], v39 offset:33792\n" \
"v_mfma_f32_16x16x32_bf16 a[88:91], a[232:235], a[212:215], a[88:91]\n" \
"s_cmp_lg_u32 s4, 0\n" \
"s_cselect_b32 s51, s40, s41\n" \
"v_mfma_f32_16x16x32_bf16 a[84:87], a[164:167], a[148:151], a[84:87]\n" \
"s_movk_i32 s40, 0x4000\n" \
"s_lshr_b32 s44, s3, 1\n" \
"v_mfma_f32_16x16x32_bf16 a[84:87], a[228:231], a[212:215], a[84:87]\n" \
"ds_read_b64_tr_b16 a[252:253], v39 offset:40960\n" \
"s_or_b32 s46, s7, s45\n" \
"v_mfma_f32_16x16x32_bf16 a[80:83], a[160:163], a[148:151], a[80:83]\n" \
"s_cmp_lg_u32 s4, 0\n" \
"s_cselect_b32 s53, s41, s40\n" \
"v_mfma_f32_16x16x32_bf16 a[80:83], a[224:227], a[212:215], a[80:83]\n" \
"s_lshl_b32 s40, s44, 15\n" \
"s_lshl_b32 s41, s43, 14\n" \
"v_mfma_f32_16x16x32_bf16 a[96:99], a[160:163], a[152:155], a[96:99]\n" \
"ds_read_b64_tr_b16 a[254:255], v39 offset:41984\n" \
"s_lshl_b32 s48, s46, 20\n" \
"v_mfma_f32_16x16x32_bf16 a[96:99], a[224:227], a[216:219], a[96:99]\n" \
"s_or_b32 s41, s40, s41\n" \
"s_add_i32 s49, s48, 0x1000000\n" \
"v_mfma_f32_16x16x32_bf16 a[100:103], a[164:167], a[152:155], a[100:103]\n" \
"s_or_b32 s42, s49, s41\n" \
"s_lshl_b32 s4, s4, 16\n" \
"v_mfma_f32_16x16x32_bf16 a[100:103], a[228:231], a[216:219], a[100:103]\n" \
"s_or_b32 s41, s42, s4\n" \
"s_lshl_b32 s4, s5, 16\n" \
"v_mfma_f32_16x16x32_bf16 a[104:107], a[168:171], a[152:155], a[104:107]\n" \
"s_mul_i32 s5, s6, 0x24800\n" \
"s_lshl_b32 s6, s46, 9\n" \
"v_mfma_f32_16x16x32_bf16 a[104:107], a[232:235], a[216:219], a[104:107]\n" \
"s_lshl_b32 s7, s7, 6\n" \
"s_add_i32 s6, s5, s6\n" \
"v_mfma_f32_16x16x32_bf16 a[108:111], a[172:175], a[152:155], a[108:111]\n" \
"s_or_b32 s5, s5, s7\n" \
"s_lshl_b32 s7, s45, 2\n" \
"v_mfma_f32_16x16x32_bf16 a[108:111], a[236:239], a[216:219], a[108:111]\n" \
"s_or_b32 s5, s5, s7\n" \
"s_lshl_b32 s7, s3, 12\n" \
"s_waitcnt lgkmcnt(15)\n" \
"v_mfma_f32_16x16x32_bf16 a[124:127], a[172:175], a[156:159], a[124:127]\n" \
"s_or_b32 s50, s49, s7\n" \
"s_lshl_b32 s7, s3, 11\n" \
"v_mfma_f32_16x16x32_bf16 a[124:127], a[236:239], a[220:223], a[124:127]\n" \
"v_lshrrev_b32_e64 v215, 4, v20\n" \
"v_bfe_u32 v216, v20, 4, 3\n" \
"v_mfma_f32_16x16x32_bf16 a[120:123], a[168:171], a[156:159], a[120:123]\n" \
"s_or_b32 s42, s42, s4\n" \
"s_lshl_b32 s4, s43, 11\n" \
"v_mfma_f32_16x16x32_bf16 a[120:123], a[232:235], a[220:223], a[120:123]\n" \
"s_add_i32 s52, s48, s7\n" \
"s_add_i32 s53, s53, s7\n" \
"v_mfma_f32_16x16x32_bf16 a[116:119], a[164:167], a[156:159], a[116:119]\n" \
"v_mul_u32_u24_e32 v214, 0x480, v214\n" \
"v_bitop3_b32 v218, v212, v215, 7 bitop3:0x78\n" \
"v_mfma_f32_16x16x32_bf16 a[116:119], a[228:231], a[220:223], a[116:119]\n" \
"v_bitop3_b32 v212, v212, v216, 4 bitop3:0x36\n" \
"s_lshl_b32 s7, s3, 10\n" \
"v_mfma_f32_16x16x32_bf16 a[112:115], a[160:163], a[156:159], a[112:115]\n" \
"s_or_b32 s4, s40, s4\n" \
"v_lshl_or_b32 v25, v212, 4, v214\n" \
"v_mfma_f32_16x16x32_bf16 a[112:115], a[224:227], a[220:223], a[112:115]\n" \
"s_waitcnt vmcnt(1) lgkmcnt(0)\n" \
"s_barrier\n" \
"v_mfma_f32_16x16x32_bf16 v[52:55], a[176:179], a[128:131], v[52:55]\n" \
"s_add_u32 m0, s30, 0x8000\n" \
"s_movk_i32 s84, 0x280\n" \
"v_mfma_f32_16x16x32_bf16 v[52:55], a[240:243], a[192:195], v[52:55]\n" \
"buffer_load_dwordx4 v22, s[64:67], s84 offen offset:0 lds\n" \
"v_mfma_f32_16x16x32_bf16 v[56:59], a[180:183], a[128:131], v[56:59]\n" \
"s_movk_i32 s84, 0x1e80\n" \
"v_mfma_f32_16x16x32_bf16 v[56:59], a[244:247], a[192:195], v[56:59]\n" \
"buffer_load_dwordx4 v23, s[64:67], s84 offen offset:1024 lds\n" \
"v_mfma_f32_16x16x32_bf16 v[60:63], a[184:187], a[128:131], v[60:63]\n" \
"ds_read_b64_tr_b16 a[160:161], v32 offset:0\n" \
"v_mul_u32_u24_e32 v212, 0x480, v217\n" \
"v_mfma_f32_16x16x32_bf16 v[60:63], a[248:251], a[192:195], v[60:63]\n" \
"s_movk_i32 s84, 0x3a80\n" \
"v_mfma_f32_16x16x32_bf16 v[64:67], a[188:191], a[128:131], v[64:67]\n" \
"buffer_load_dwordx4 v22, s[64:67], s84 offen offset:2048 lds\n" \
"v_mfma_f32_16x16x32_bf16 v[64:67], a[252:255], a[192:195], v[64:67]\n" \
"ds_read_b128 a[128:131], v28 offset:16384\n" \
"v_bitop3_b32 v213, v215, v213, 7 bitop3:0x6c\n" \
"v_mfma_f32_16x16x32_bf16 v[80:83], a[188:191], a[132:135], v[80:83]\n" \
"ds_read_b128 a[192:195], v29 offset:16384\n" \
"s_or_b32 s40, s7, 0x14000\n" \
"v_mfma_f32_16x16x32_bf16 v[80:83], a[252:255], a[196:199], v[80:83]\n" \
"s_movk_i32 s84, 0x5680\n" \
"v_mfma_f32_16x16x32_bf16 v[76:79], a[184:187], a[132:135], v[76:79]\n" \
"buffer_load_dwordx4 v23, s[64:67], s84 offen offset:3072 lds\n" \
"v_mfma_f32_16x16x32_bf16 v[76:79], a[248:251], a[196:199], v[76:79]\n" \
"s_add_u32 m0, s31, 0x8000\n" \
"s_mov_b32 s84, 0x280000\n" \
"v_mfma_f32_16x16x32_bf16 v[72:75], a[180:183], a[132:135], v[72:75]\n" \
"buffer_load_dwordx4 v26, s[68:71], s84 offen offset:0 lds\n" \
"v_mfma_f32_16x16x32_bf16 v[72:75], a[244:247], a[196:199], v[72:75]\n" \
"ds_read_b64_tr_b16 a[162:163], v32 offset:1024\n" \
"s_lshl_b32 s43, s44, 13\n" \
"v_mfma_f32_16x16x32_bf16 v[68:71], a[176:179], a[132:135], v[68:71]\n" \
"ds_read_b64_tr_b16 a[224:225], v32 offset:8192\n" \
"s_mulk_i32 s44, 0xe800\n" \
"v_mfma_f32_16x16x32_bf16 v[68:71], a[240:243], a[196:199], v[68:71]\n" \
"ds_read_b128 a[132:135], v28 offset:18432\n" \
"s_lshl_b32 s7, s3, 8\n" \
"v_mfma_f32_16x16x32_bf16 v[84:87], a[176:179], a[136:139], v[84:87]\n" \
"ds_read_b128 a[196:199], v29 offset:18432\n" \
"v_lshl_or_b32 v24, v218, 4, v214\n" \
"v_mfma_f32_16x16x32_bf16 v[84:87], a[240:243], a[200:203], v[84:87]\n" \
"s_addk_i32 s84, 0x7c00\n" \
"v_mfma_f32_16x16x32_bf16 v[88:91], a[180:183], a[136:139], v[88:91]\n" \
"buffer_load_dwordx4 v26, s[68:71], s84 offen offset:1024 lds\n" \
"v_mfma_f32_16x16x32_bf16 v[88:91], a[244:247], a[200:203], v[88:91]\n" \
"s_addk_i32 s84, 0x7c00\n" \
"v_mfma_f32_16x16x32_bf16 v[92:95], a[184:187], a[136:139], v[92:95]\n" \
"buffer_load_dwordx4 v27, s[68:71], s84 offen offset:2048 lds\n" \
"v_mfma_f32_16x16x32_bf16 v[92:95], a[248:251], a[200:203], v[92:95]\n" \
"s_addk_i32 s84, 0x7c00\n" \
"v_mfma_f32_16x16x32_bf16 v[96:99], a[188:191], a[136:139], v[96:99]\n" \
"buffer_load_dwordx4 v27, s[68:71], s84 offen offset:3072 lds\n" \
"v_mfma_f32_16x16x32_bf16 v[96:99], a[252:255], a[200:203], v[96:99]\n" \
"ds_read_b128 a[136:139], v28 offset:20480\n" \
"v_lshl_or_b32 v21, v213, 4, v212\n" \
"v_mfma_f32_16x16x32_bf16 v[112:115], a[188:191], a[140:143], v[112:115]\n" \
"ds_read_b128 a[200:203], v29 offset:20480\n" \
"s_add_i32 s44, s44, 0x17000\n" \
"v_mfma_f32_16x16x32_bf16 v[112:115], a[252:255], a[204:207], v[112:115]\n" \
"s_add_u32 m0, s30, 0x9000\n" \
"s_mov_b32 s84, 0x8280\n" \
"v_mfma_f32_16x16x32_bf16 v[108:111], a[184:187], a[140:143], v[108:111]\n" \
"buffer_load_dwordx4 v22, s[64:67], s84 offen offset:0 lds\n" \
"v_mfma_f32_16x16x32_bf16 v[108:111], a[248:251], a[204:207], v[108:111]\n" \
"s_addk_i32 s84, 0x1c00\n" \
"v_mfma_f32_16x16x32_bf16 v[104:107], a[180:183], a[140:143], v[104:107]\n" \
"buffer_load_dwordx4 v23, s[64:67], s84 offen offset:1024 lds\n" \
"v_mfma_f32_16x16x32_bf16 v[104:107], a[244:247], a[204:207], v[104:107]\n" \
"s_addk_i32 s84, 0x1c00\n" \
"v_mfma_f32_16x16x32_bf16 v[100:103], a[176:179], a[140:143], v[100:103]\n" \
"buffer_load_dwordx4 v22, s[64:67], s84 offen offset:2048 lds\n" \
"v_mfma_f32_16x16x32_bf16 v[100:103], a[240:243], a[204:207], v[100:103]\n" \
"ds_read_b128 a[140:143], v28 offset:22528\n" \
"s_or_b32 s45, s7, 0x27000\n" \
"v_mfma_f32_16x16x32_bf16 v[116:119], a[176:179], a[144:147], v[116:119]\n" \
"ds_read_b128 a[204:207], v29 offset:22528\n" \
"s_or_b32 s46, s4, 0x10000\n" \
"v_mfma_f32_16x16x32_bf16 v[116:119], a[240:243], a[208:211], v[116:119]\n" \
"s_addk_i32 s84, 0x1c00\n" \
"v_mfma_f32_16x16x32_bf16 v[120:123], a[180:183], a[144:147], v[120:123]\n" \
"buffer_load_dwordx4 v23, s[64:67], s84 offen offset:3072 lds\n" \
"v_mfma_f32_16x16x32_bf16 v[120:123], a[244:247], a[208:211], v[120:123]\n" \
"s_add_u32 m0, s31, 0xc000\n" \
"s_mov_b32 s84, 0x280000\n" \
"v_mfma_f32_16x16x32_bf16 v[124:127], a[184:187], a[144:147], v[124:127]\n" \
"buffer_load_dwordx4 v26, s[72:75], s84 offen offset:0 lds\n" \
"v_mfma_f32_16x16x32_bf16 v[124:127], a[248:251], a[208:211], v[124:127]\n" \
"s_addk_i32 s84, 0x7c00\n" \
"v_mfma_f32_16x16x32_bf16 v[128:131], a[188:191], a[144:147], v[128:131]\n" \
"buffer_load_dwordx4 v26, s[72:75], s84 offen offset:1024 lds\n" \
"v_mfma_f32_16x16x32_bf16 v[128:131], a[252:255], a[208:211], v[128:131]\n" \
"ds_read_b128 a[144:147], v28 offset:0\n" \
"s_add_i32 s48, s6, 0x3000000\n" \
"v_mfma_f32_16x16x32_bf16 v[144:147], a[188:191], a[148:151], v[144:147]\n" \
"ds_read_b128 a[208:211], v29 offset:0\n" \
"s_add_i32 s49, s5, 0x3004000\n" \
"v_mfma_f32_16x16x32_bf16 v[144:147], a[252:255], a[212:215], v[144:147]\n" \
"ds_read_b128 v[200:203], v199 offset:0\n" \
"s_add_i32 s52, s52, 0x1010000\n" \
"v_mfma_f32_16x16x32_bf16 v[140:143], a[184:187], a[148:151], v[140:143]\n" \
"ds_read_b128 v[204:207], v199 offset:4096\n" \
"v_mfma_f32_16x16x32_bf16 v[140:143], a[248:251], a[212:215], v[140:143]\n" \
"ds_read_b64_tr_b16 a[226:227], v32 offset:9216\n" \
"v_mfma_f32_16x16x32_bf16 v[136:139], a[180:183], a[148:151], v[136:139]\n" \
"ds_read_b64_tr_b16 a[164:165], v33 offset:0\n" \
"v_mfma_f32_16x16x32_bf16 v[136:139], a[244:247], a[212:215], v[136:139]\n" \
"ds_read_b128 v[208:211], v199 offset:8192\n" \
"v_mfma_f32_16x16x32_bf16 v[132:135], a[176:179], a[148:151], v[132:135]\n" \
"ds_read_b64_tr_b16 a[166:167], v33 offset:1024\n" \
"v_mfma_f32_16x16x32_bf16 v[132:135], a[240:243], a[212:215], v[132:135]\n" \
"ds_read_b128 a[148:151], v28 offset:2048\n" \
"v_mfma_f32_16x16x32_bf16 v[148:151], a[176:179], a[152:155], v[148:151]\n" \
"ds_read_b128 a[212:215], v29 offset:2048\n" \
"v_mfma_f32_16x16x32_bf16 v[148:151], a[240:243], a[216:219], v[148:151]\n" \
"ds_read_b64_tr_b16 a[228:229], v33 offset:8192\n" \
"v_mfma_f32_16x16x32_bf16 v[152:155], a[180:183], a[152:155], v[152:155]\n" \
"ds_read_b64_tr_b16 a[230:231], v33 offset:9216\n" \
"v_mfma_f32_16x16x32_bf16 v[152:155], a[244:247], a[216:219], v[152:155]\n" \
"ds_read_b64_tr_b16 a[168:169], v34 offset:0\n" \
"v_mfma_f32_16x16x32_bf16 v[156:159], a[184:187], a[152:155], v[156:159]\n" \
"ds_read_b64_tr_b16 a[170:171], v34 offset:1024\n" \
"v_mfma_f32_16x16x32_bf16 v[156:159], a[248:251], a[216:219], v[156:159]\n" \
"ds_read_b64_tr_b16 a[232:233], v34 offset:8192\n" \
"v_mfma_f32_16x16x32_bf16 v[160:163], a[188:191], a[152:155], v[160:163]\n" \
"ds_read_b64_tr_b16 a[234:235], v34 offset:9216\n" \
"v_mfma_f32_16x16x32_bf16 v[160:163], a[252:255], a[216:219], v[160:163]\n" \
"ds_read_b128 a[152:155], v28 offset:4096\n" \
"v_mfma_f32_16x16x32_bf16 v[176:179], a[188:191], a[156:159], v[176:179]\n" \
"ds_read_b128 a[216:219], v29 offset:4096\n" \
"v_mfma_f32_16x16x32_bf16 v[176:179], a[252:255], a[220:223], v[176:179]\n" \
"ds_read_b64_tr_b16 a[172:173], v35 offset:0\n" \
"v_mfma_f32_16x16x32_bf16 v[172:175], a[184:187], a[156:159], v[172:175]\n" \
"ds_read_b64_tr_b16 a[174:175], v35 offset:1024\n" \
"v_mfma_f32_16x16x32_bf16 v[172:175], a[248:251], a[220:223], v[172:175]\n" \
"ds_read_b64_tr_b16 a[236:237], v35 offset:8192\n" \
"v_mfma_f32_16x16x32_bf16 v[168:171], a[180:183], a[156:159], v[168:171]\n" \
"ds_read_b64_tr_b16 a[238:239], v35 offset:9216\n" \
"v_mfma_f32_16x16x32_bf16 v[168:171], a[244:247], a[220:223], v[168:171]\n" \
"v_mfma_f32_16x16x32_bf16 v[164:167], a[176:179], a[156:159], v[164:167]\n" \
"v_mfma_f32_16x16x32_bf16 v[164:167], a[240:243], a[220:223], v[164:167]\n" \
"ds_read_b128 a[156:159], v28 offset:6144\n" \
"ds_read_b128 a[220:223], v29 offset:6144\n" \
"s_waitcnt lgkmcnt(15)\n" \
"v_mfma_f32_16x16x32_bf16 a[0:3], a[160:163], a[128:131], a[0:3]\n" \
"s_addk_i32 s84, 0x7c00\n" \
"v_mfma_f32_16x16x32_bf16 a[0:3], a[224:227], a[192:195], a[0:3]\n" \
"buffer_load_dwordx4 v27, s[72:75], s84 offen offset:2048 lds\n" \
"v_mfma_f32_16x16x32_bf16 a[4:7], a[164:167], a[128:131], a[4:7]\n" \
"ds_read_b64_tr_b16 a[176:177], v36 offset:0\n" \
"s_waitcnt lgkmcnt(13)\n" \
"v_mfma_f32_16x16x32_bf16 a[4:7], a[228:231], a[192:195], a[4:7]\n" \
"s_addk_i32 s84, 0x7c00\n" \
"s_waitcnt lgkmcnt(11)\n" \
"v_mfma_f32_16x16x32_bf16 a[8:11], a[168:171], a[128:131], a[8:11]\n" \
"buffer_load_dwordx4 v27, s[72:75], s84 offen offset:3072 lds\n" \
"s_waitcnt lgkmcnt(9)\n" \
"v_mfma_f32_16x16x32_bf16 a[8:11], a[232:235], a[192:195], a[8:11]\n" \
"ds_read_b64_tr_b16 a[178:179], v36 offset:1024\n" \
"s_waitcnt lgkmcnt(6)\n" \
"v_mfma_f32_16x16x32_bf16 a[12:15], a[172:175], a[128:131], a[12:15]\n" \
"ds_read_b64_tr_b16 a[240:241], v36 offset:8192\n" \
"s_waitcnt lgkmcnt(5)\n" \
"v_mfma_f32_16x16x32_bf16 a[12:15], a[236:239], a[192:195], a[12:15]\n" \
"v_mfma_f32_16x16x32_bf16 a[28:31], a[172:175], a[132:135], a[28:31]\n" \
"v_mfma_f32_16x16x32_bf16 a[28:31], a[236:239], a[196:199], a[28:31]\n" \
"ds_read_b64_tr_b16 a[242:243], v36 offset:9216\n" \
"v_mfma_f32_16x16x32_bf16 a[24:27], a[168:171], a[132:135], a[24:27]\n" \
"v_mfma_f32_16x16x32_bf16 a[24:27], a[232:235], a[196:199], a[24:27]\n" \
"v_mfma_f32_16x16x32_bf16 a[20:23], a[164:167], a[132:135], a[20:23]\n" \
"v_mfma_f32_16x16x32_bf16 a[20:23], a[228:231], a[196:199], a[20:23]\n" \
"ds_read_b64_tr_b16 a[180:181], v37 offset:0\n" \
"v_mfma_f32_16x16x32_bf16 a[16:19], a[160:163], a[132:135], a[16:19]\n" \
"v_mfma_f32_16x16x32_bf16 a[16:19], a[224:227], a[196:199], a[16:19]\n" \
"v_mfma_f32_16x16x32_bf16 a[32:35], a[160:163], a[136:139], a[32:35]\n" \
"ds_read_b64_tr_b16 a[182:183], v37 offset:1024\n" \
"v_mfma_f32_16x16x32_bf16 a[32:35], a[224:227], a[200:203], a[32:35]\n" \
"v_mfma_f32_16x16x32_bf16 a[36:39], a[164:167], a[136:139], a[36:39]\n" \
"v_mfma_f32_16x16x32_bf16 a[36:39], a[228:231], a[200:203], a[36:39]\n" \
"buffer_store_dwordx4 v[200:203], v20, s[80:83], s35 offen sc1\n" \
".fill (L_pb + 12000 - .) / 4, 4, 0xBF800000\n" \
"L_pp3:\n" \
"s_cbranch_execz L_pret\n" \
"v_mfma_f32_16x16x32_bf16 a[40:43], a[168:171], a[136:139], a[40:43]\n" \
"s_mov_b32 m0, s32\n" \
"ds_read_b64_tr_b16 a[244:245], v37 offset:8192\n" \
"v_mfma_f32_16x16x32_bf16 a[40:43], a[232:235], a[200:203], a[40:43]\n" \
"s_mov_b32 exec_lo, 0x0\n" \
"s_mov_b32 exec_hi, 0xffffffff\n" \
"global_load_lds_dword v[188:189], off\n" \
"s_mov_b64 exec, -1\n" \
"s_nop 0\n" \
"v_mfma_f32_16x16x32_bf16 a[44:47], a[172:175], a[136:139], a[44:47]\n" \
"ds_read_b64_tr_b16 a[246:247], v37 offset:9216\n" \
"v_mfma_f32_16x16x32_bf16 a[44:47], a[236:239], a[200:203], a[44:47]\n" \
"v_mfma_f32_16x16x32_bf16 a[60:63], a[172:175], a[140:143], a[60:63]\n" \
"v_mfma_f32_16x16x32_bf16 a[60:63], a[236:239], a[204:207], a[60:63]\n" \
"v_mfma_f32_16x16x32_bf16 a[56:59], a[168:171], a[140:143], a[56:59]\n" \
"ds_read_b64_tr_b16 a[184:185], v38 offset:0\n" \
"v_mfma_f32_16x16x32_bf16 a[56:59], a[232:235], a[204:207], a[56:59]\n" \
"v_mfma_f32_16x16x32_bf16 a[52:55], a[164:167], a[140:143], a[52:55]\n" \
"buffer_store_dwordx4 v[204:207], v20, s[80:83], s88 offen sc1\n" \
"v_mfma_f32_16x16x32_bf16 a[52:55], a[228:231], a[204:207], a[52:55]\n" \
"ds_read_b64_tr_b16 a[186:187], v38 offset:1024\n" \
"v_mfma_f32_16x16x32_bf16 a[48:51], a[160:163], a[140:143], a[48:51]\n" \
"v_mfma_f32_16x16x32_bf16 a[48:51], a[224:227], a[204:207], a[48:51]\n" \
"v_mfma_f32_16x16x32_bf16 a[64:67], a[160:163], a[144:147], a[64:67]\n" \
"ds_read_b64_tr_b16 a[248:249], v38 offset:8192\n" \
"v_mfma_f32_16x16x32_bf16 a[64:67], a[224:227], a[208:211], a[64:67]\n" \
"v_mfma_f32_16x16x32_bf16 a[68:71], a[164:167], a[144:147], a[68:71]\n" \
"v_mfma_f32_16x16x32_bf16 a[68:71], a[228:231], a[208:211], a[68:71]\n" \
"ds_read_b64_tr_b16 a[250:251], v38 offset:9216\n" \
"v_mfma_f32_16x16x32_bf16 a[72:75], a[168:171], a[144:147], a[72:75]\n" \
"v_mfma_f32_16x16x32_bf16 a[72:75], a[232:235], a[208:211], a[72:75]\n" \
"v_mfma_f32_16x16x32_bf16 a[76:79], a[172:175], a[144:147], a[76:79]\n" \
"buffer_store_dwordx4 v[208:211], v20, s[80:83], s36 offen sc1\n" \
"v_mfma_f32_16x16x32_bf16 a[76:79], a[236:239], a[208:211], a[76:79]\n" \
"ds_read_b64_tr_b16 a[188:189], v39 offset:0\n" \
"v_mfma_f32_16x16x32_bf16 a[92:95], a[172:175], a[148:151], a[92:95]\n" \
"v_mfma_f32_16x16x32_bf16 a[92:95], a[236:239], a[212:215], a[92:95]\n" \
"v_mfma_f32_16x16x32_bf16 a[88:91], a[168:171], a[148:151], a[88:91]\n" \
"ds_read_b64_tr_b16 a[190:191], v39 offset:1024\n" \
"v_mfma_f32_16x16x32_bf16 a[88:91], a[232:235], a[212:215], a[88:91]\n" \
"v_mfma_f32_16x16x32_bf16 a[84:87], a[164:167], a[148:151], a[84:87]\n" \
"v_mfma_f32_16x16x32_bf16 a[84:87], a[228:231], a[212:215], a[84:87]\n" \
"ds_read_b64_tr_b16 a[252:253], v39 offset:8192\n" \
"v_mfma_f32_16x16x32_bf16 a[80:83], a[160:163], a[148:151], a[80:83]\n" \
"v_mfma_f32_16x16x32_bf16 a[80:83], a[224:227], a[212:215], a[80:83]\n" \
"v_mfma_f32_16x16x32_bf16 a[96:99], a[160:163], a[152:155], a[96:99]\n" \
"ds_read_b64_tr_b16 a[254:255], v39 offset:9216\n" \
"v_mfma_f32_16x16x32_bf16 a[96:99], a[224:227], a[216:219], a[96:99]\n" \
"v_mfma_f32_16x16x32_bf16 a[100:103], a[164:167], a[152:155], a[100:103]\n" \
"v_mfma_f32_16x16x32_bf16 a[100:103], a[228:231], a[216:219], a[100:103]\n" \
"v_mfma_f32_16x16x32_bf16 a[104:107], a[168:171], a[152:155], a[104:107]\n" \
"v_mfma_f32_16x16x32_bf16 a[104:107], a[232:235], a[216:219], a[104:107]\n" \
"v_mfma_f32_16x16x32_bf16 a[108:111], a[172:175], a[152:155], a[108:111]\n" \
"v_mfma_f32_16x16x32_bf16 a[108:111], a[236:239], a[216:219], a[108:111]\n" \
"s_waitcnt lgkmcnt(15)\n" \
"v_mfma_f32_16x16x32_bf16 a[124:127], a[172:175], a[156:159], a[124:127]\n" \
"v_mfma_f32_16x16x32_bf16 a[124:127], a[236:239], a[220:223], a[124:127]\n" \
"v_mfma_f32_16x16x32_bf16 a[120:123], a[168:171], a[156:159], a[120:123]\n" \
"v_mfma_f32_16x16x32_bf16 a[120:123], a[232:235], a[220:223], a[120:123]\n" \
"v_mfma_f32_16x16x32_bf16 a[116:119], a[164:167], a[156:159], a[116:119]\n" \
"v_mfma_f32_16x16x32_bf16 a[116:119], a[228:231], a[220:223], a[116:119]\n" \
"v_mfma_f32_16x16x32_bf16 a[112:115], a[160:163], a[156:159], a[112:115]\n" \
"v_mfma_f32_16x16x32_bf16 a[112:115], a[224:227], a[220:223], a[112:115]\n" \
"s_waitcnt vmcnt(4) lgkmcnt(0)\n" \
"s_barrier\n" \
"v_mfma_f32_16x16x32_bf16 v[52:55], a[176:179], a[128:131], v[52:55]\n" \
"s_add_u32 m0, s30, 0x0\n" \
"s_movk_i32 s84, 0x300\n" \
"v_mfma_f32_16x16x32_bf16 v[52:55], a[240:243], a[192:195], v[52:55]\n" \
"buffer_load_dwordx4 v22, s[64:67], s84 offen offset:0 lds\n" \
"v_mfma_f32_16x16x32_bf16 v[56:59], a[180:183], a[128:131], v[56:59]\n" \
"ds_read_b64_tr_b16 a[160:161], v32 offset:32768\n" \
"v_mfma_f32_16x16x32_bf16 v[56:59], a[244:247], a[192:195], v[56:59]\n" \
"s_movk_i32 s84, 0x1f00\n" \
"v_mfma_f32_16x16x32_bf16 v[60:63], a[184:187], a[128:131], v[60:63]\n" \
"buffer_load_dwordx4 v23, s[64:67], s84 offen offset:1024 lds\n" \
"v_mfma_f32_16x16x32_bf16 v[60:63], a[248:251], a[192:195], v[60:63]\n" \
"ds_read_b64_tr_b16 a[162:163], v32 offset:33792\n" \
"v_mfma_f32_16x16x32_bf16 v[64:67], a[188:191], a[128:131], v[64:67]\n" \
"ds_read_b64_tr_b16 a[224:225], v32 offset:40960\n" \
"v_mfma_f32_16x16x32_bf16 v[64:67], a[252:255], a[192:195], v[64:67]\n" \
"ds_read_b128 a[128:131], v28 offset:49152\n" \
"v_mfma_f32_16x16x32_bf16 v[80:83], a[188:191], a[132:135], v[80:83]\n" \
"ds_read_b128 a[192:195], v29 offset:49152\n" \
"v_mfma_f32_16x16x32_bf16 v[80:83], a[252:255], a[196:199], v[80:83]\n" \
"s_movk_i32 s84, 0x3b00\n" \
"v_mfma_f32_16x16x32_bf16 v[76:79], a[184:187], a[132:135], v[76:79]\n" \
"buffer_load_dwordx4 v22, s[64:67], s84 offen offset:2048 lds\n" \
"v_mfma_f32_16x16x32_bf16 v[76:79], a[248:251], a[196:199], v[76:79]\n" \
"s_movk_i32 s84, 0x5700\n" \
"v_mfma_f32_16x16x32_bf16 v[72:75], a[180:183], a[132:135], v[72:75]\n" \
"buffer_load_dwordx4 v23, s[64:67], s84 offen offset:3072 lds\n" \
"v_mfma_f32_16x16x32_bf16 v[72:75], a[244:247], a[196:199], v[72:75]\n" \
"s_add_u32 m0, s31, 0x0\n" \
"s_mov_b32 s84, 0x300000\n" \
"v_mfma_f32_16x16x32_bf16 v[68:71], a[176:179], a[132:135], v[68:71]\n" \
"buffer_load_dwordx4 v26, s[68:71], s84 offen offset:0 lds\n" \
"v_mfma_f32_16x16x32_bf16 v[68:71], a[240:243], a[196:199], v[68:71]\n" \
"ds_read_b128 a[132:135], v28 offset:51200\n" \
"v_mfma_f32_16x16x32_bf16 v[84:87], a[176:179], a[136:139], v[84:87]\n" \
"ds_read_b128 a[196:199], v29 offset:51200\n" \
"v_mfma_f32_16x16x32_bf16 v[84:87], a[240:243], a[200:203], v[84:87]\n" \
"s_addk_i32 s84, 0x7c00\n" \
"v_mfma_f32_16x16x32_bf16 v[88:91], a[180:183], a[136:139], v[88:91]\n" \
"buffer_load_dwordx4 v26, s[68:71], s84 offen offset:1024 lds\n" \
"v_mfma_f32_16x16x32_bf16 v[88:91], a[244:247], a[200:203], v[88:91]\n" \
"ds_read_b64_tr_b16 a[226:227], v32 offset:41984\n" \
"v_mfma_f32_16x16x32_bf16 v[92:95], a[184:187], a[136:139], v[92:95]\n" \
"s_addk_i32 s84, 0x7c00\n" \
"v_mfma_f32_16x16x32_bf16 v[92:95], a[248:251], a[200:203], v[92:95]\n" \
"buffer_load_dwordx4 v27, s[68:71], s84 offen offset:2048 lds\n" \
"v_mfma_f32_16x16x32_bf16 v[96:99], a[188:191], a[136:139], v[96:99]\n" \
"ds_read_b64_tr_b16 a[164:165], v33 offset:32768\n" \
"v_mfma_f32_16x16x32_bf16 v[96:99], a[252:255], a[200:203], v[96:99]\n" \
"ds_read_b128 a[136:139], v28 offset:53248\n" \
"v_mfma_f32_16x16x32_bf16 v[112:115], a[188:191], a[140:143], v[112:115]\n" \
"ds_read_b128 a[200:203], v29 offset:53248\n" \
"v_mfma_f32_16x16x32_bf16 v[112:115], a[252:255], a[204:207], v[112:115]\n" \
"s_addk_i32 s84, 0x7c00\n" \
"v_mfma_f32_16x16x32_bf16 v[108:111], a[184:187], a[140:143], v[108:111]\n" \
"buffer_load_dwordx4 v27, s[68:71], s84 offen offset:3072 lds\n" \
"v_mfma_f32_16x16x32_bf16 v[108:111], a[248:251], a[204:207], v[108:111]\n" \
"s_add_u32 m0, s30, 0x1000\n" \
"s_mov_b32 s84, 0x8300\n" \
"v_mfma_f32_16x16x32_bf16 v[104:107], a[180:183], a[140:143], v[104:107]\n" \
"buffer_load_dwordx4 v22, s[64:67], s84 offen offset:0 lds\n" \
"v_mfma_f32_16x16x32_bf16 v[104:107], a[244:247], a[204:207], v[104:107]\n" \
"ds_read_b64_tr_b16 a[166:167], v33 offset:33792\n" \
"v_mfma_f32_16x16x32_bf16 v[100:103], a[176:179], a[140:143], v[100:103]\n" \
"ds_read_b64_tr_b16 a[228:229], v33 offset:40960\n" \
"v_mfma_f32_16x16x32_bf16 v[100:103], a[240:243], a[204:207], v[100:103]\n" \
"ds_read_b128 a[140:143], v28 offset:55296\n" \
"v_mfma_f32_16x16x32_bf16 v[116:119], a[176:179], a[144:147], v[116:119]\n" \
"ds_read_b128 a[204:207], v29 offset:55296\n" \
"v_mfma_f32_16x16x32_bf16 v[116:119], a[240:243], a[208:211], v[116:119]\n" \
"s_addk_i32 s84, 0x1c00\n" \
"v_mfma_f32_16x16x32_bf16 v[120:123], a[180:183], a[144:147], v[120:123]\n" \
"buffer_load_dwordx4 v23, s[64:67], s84 offen offset:1024 lds\n" \
"v_mfma_f32_16x16x32_bf16 v[120:123], a[244:247], a[208:211], v[120:123]\n" \
"s_addk_i32 s84, 0x1c00\n" \
"v_mfma_f32_16x16x32_bf16 v[124:127], a[184:187], a[144:147], v[124:127]\n" \
"buffer_load_dwordx4 v22, s[64:67], s84 offen offset:2048 lds\n" \
"v_mfma_f32_16x16x32_bf16 v[124:127], a[248:251], a[208:211], v[124:127]\n" \
"s_addk_i32 s84, 0x1c00\n" \
"s_nop 0\n" \
"v_mfma_f32_16x16x32_bf16 v[128:131], a[188:191], a[144:147], v[128:131]\n" \
"buffer_load_dwordx4 v23, s[64:67], s84 offen offset:3072 lds\n" \
"v_mfma_f32_16x16x32_bf16 v[128:131], a[252:255], a[208:211], v[128:131]\n" \
"ds_read_b128 a[144:147], v28 offset:32768\n" \
"v_mfma_f32_16x16x32_bf16 v[144:147], a[188:191], a[148:151], v[144:147]\n" \
"ds_read_b128 a[208:211], v29 offset:32768\n" \
"v_mfma_f32_16x16x32_bf16 v[144:147], a[252:255], a[212:215], v[144:147]\n" \
"s_add_u32 m0, s31, 0x4000\n" \
"s_mov_b32 s84, 0x300000\n" \
"v_mfma_f32_16x16x32_bf16 v[140:143], a[184:187], a[148:151], v[140:143]\n" \
"buffer_load_dwordx4 v26, s[72:75], s84 offen offset:0 lds\n" \
"v_mfma_f32_16x16x32_bf16 v[140:143], a[248:251], a[212:215], v[140:143]\n" \
"ds_read_b64_tr_b16 a[230:231], v33 offset:41984\n" \
"v_mfma_f32_16x16x32_bf16 v[136:139], a[180:183], a[148:151], v[136:139]\n" \
"s_addk_i32 s84, 0x7c00\n" \
"s_nop 0\n" \
"v_mfma_f32_16x16x32_bf16 v[136:139], a[244:247], a[212:215], v[136:139]\n" \
"buffer_load_dwordx4 v26, s[72:75], s84 offen offset:1024 lds\n" \
"v_mfma_f32_16x16x32_bf16 v[132:135], a[176:179], a[148:151], v[132:135]\n" \
"ds_read_b64_tr_b16 a[168:169], v34 offset:32768\n" \
"v_mfma_f32_16x16x32_bf16 v[132:135], a[240:243], a[212:215], v[132:135]\n" \
"ds_read_b128 a[148:151], v28 offset:34816\n" \
"v_mfma_f32_16x16x32_bf16 v[148:151], a[176:179], a[152:155], v[148:151]\n" \
"ds_read_b128 a[212:215], v29 offset:34816\n" \
"v_mfma_f32_16x16x32_bf16 v[148:151], a[240:243], a[216:219], v[148:151]\n" \
"ds_read_b64_tr_b16 a[170:171], v34 offset:33792\n" \
"v_mfma_f32_16x16x32_bf16 v[152:155], a[180:183], a[152:155], v[152:155]\n" \
"ds_read_b64_tr_b16 a[232:233], v34 offset:40960\n" \
"v_mfma_f32_16x16x32_bf16 v[152:155], a[244:247], a[216:219], v[152:155]\n" \
"ds_read_b64_tr_b16 a[234:235], v34 offset:41984\n" \
"v_mfma_f32_16x16x32_bf16 v[156:159], a[184:187], a[152:155], v[156:159]\n" \
"ds_read_b64_tr_b16 a[172:173], v35 offset:32768\n" \
"v_mfma_f32_16x16x32_bf16 v[156:159], a[248:251], a[216:219], v[156:159]\n" \
"ds_read_b64_tr_b16 a[174:175], v35 offset:33792\n" \
"v_mfma_f32_16x16x32_bf16 v[160:163], a[188:191], a[152:155], v[160:163]\n" \
"ds_read_b64_tr_b16 a[236:237], v35 offset:40960\n" \
"v_mfma_f32_16x16x32_bf16 v[160:163], a[252:255], a[216:219], v[160:163]\n" \
"ds_read_b128 a[152:155], v28 offset:36864\n" \
"v_mfma_f32_16x16x32_bf16 v[176:179], a[188:191], a[156:159], v[176:179]\n" \
"ds_read_b128 a[216:219], v29 offset:36864\n" \
"v_mfma_f32_16x16x32_bf16 v[176:179], a[252:255], a[220:223], v[176:179]\n" \
"ds_read_b64_tr_b16 a[238:239], v35 offset:41984\n" \
"v_mfma_f32_16x16x32_bf16 v[172:175], a[184:187], a[156:159], v[172:175]\n" \
"v_mfma_f32_16x16x32_bf16 v[172:175], a[248:251], a[220:223], v[172:175]\n" \
"v_mfma_f32_16x16x32_bf16 v[168:171], a[180:183], a[156:159], v[168:171]\n" \
"v_mfma_f32_16x16x32_bf16 v[168:171], a[244:247], a[220:223], v[168:171]\n" \
"v_mfma_f32_16x16x32_bf16 v[164:167], a[176:179], a[156:159], v[164:167]\n" \
"v_mfma_f32_16x16x32_bf16 v[164:167], a[240:243], a[220:223], v[164:167]\n" \
"ds_read_b128 a[156:159], v28 offset:38912\n" \
"ds_read_b128 a[220:223], v29 offset:38912\n" \
"s_waitcnt lgkmcnt(15)\n" \
"v_mfma_f32_16x16x32_bf16 a[0:3], a[160:163], a[128:131], a[0:3]\n" \
"s_addk_i32 s84, 0x7c00\n" \
"v_mfma_f32_16x16x32_bf16 a[0:3], a[224:227], a[192:195], a[0:3]\n" \
"buffer_load_dwordx4 v27, s[72:75], s84 offen offset:2048 lds\n" \
"v_mfma_f32_16x16x32_bf16 a[4:7], a[164:167], a[128:131], a[4:7]\n" \
"ds_read_b64_tr_b16 a[176:177], v36 offset:32768\n" \
"s_waitcnt lgkmcnt(15)\n" \
"v_mfma_f32_16x16x32_bf16 a[4:7], a[228:231], a[192:195], a[4:7]\n" \
"s_addk_i32 s84, 0x7c00\n" \
"s_waitcnt lgkmcnt(11)\n" \
"v_mfma_f32_16x16x32_bf16 a[8:11], a[168:171], a[128:131], a[8:11]\n" \
"buffer_load_dwordx4 v27, s[72:75], s84 offen offset:3072 lds\n" \
"s_waitcnt lgkmcnt(9)\n" \
"v_mfma_f32_16x16x32_bf16 a[8:11], a[232:235], a[192:195], a[8:11]\n" \
"ds_read_b64_tr_b16 a[178:179], v36 offset:33792\n" \
"s_waitcnt lgkmcnt(8)\n" \
"v_mfma_f32_16x16x32_bf16 a[12:15], a[172:175], a[128:131], a[12:15]\n" \
"ds_read_b64_tr_b16 a[240:241], v36 offset:40960\n" \
"s_waitcnt lgkmcnt(5)\n" \
"v_mfma_f32_16x16x32_bf16 a[12:15], a[236:239], a[192:195], a[12:15]\n" \
"v_mfma_f32_16x16x32_bf16 a[28:31], a[172:175], a[132:135], a[28:31]\n" \
"v_mfma_f32_16x16x32_bf16 a[28:31], a[236:239], a[196:199], a[28:31]\n" \
"ds_read_b64_tr_b16 a[242:243], v36 offset:41984\n" \
"v_mfma_f32_16x16x32_bf16 a[24:27], a[168:171], a[132:135], a[24:27]\n" \
"v_mfma_f32_16x16x32_bf16 a[24:27], a[232:235], a[196:199], a[24:27]\n" \
"v_mfma_f32_16x16x32_bf16 a[20:23], a[164:167], a[132:135], a[20:23]\n" \
"v_mfma_f32_16x16x32_bf16 a[20:23], a[228:231], a[196:199], a[20:23]\n" \
"ds_read_b64_tr_b16 a[180:181], v37 offset:32768\n" \
"v_mfma_f32_16x16x32_bf16 a[16:19], a[160:163], a[132:135], a[16:19]\n" \
"v_mfma_f32_16x16x32_bf16 a[16:19], a[224:227], a[196:199], a[16:19]\n" \
"v_mfma_f32_16x16x32_bf16 a[32:35], a[160:163], a[136:139], a[32:35]\n" \
"ds_read_b64_tr_b16 a[182:183], v37 offset:33792\n" \
"v_mfma_f32_16x16x32_bf16 a[32:35], a[224:227], a[200:203], a[32:35]\n" \
"v_mfma_f32_16x16x32_bf16 a[36:39], a[164:167], a[136:139], a[36:39]\n" \
"v_lshlrev_b32_e32 v45, 3, v20\n" \
"v_add_u32_e32 v45, s31, v45\n" \
"v_mfma_f32_16x16x32_bf16 a[36:39], a[228:231], a[200:203], a[36:39]\n" \
"s_getpc_b64 s[86:87]\n" \
"s_sub_u32 s86, s86, 0xf800\n" \
"s_subb_u32 s87, s87, 0\n" \
"s_mov_b32 m0, s32\n" \
"s_nop 0\n" \
"v_mfma_f32_16x16x32_bf16 a[40:43], a[168:171], a[136:139], a[40:43]\n" \
"global_load_lds_dword v45, s[86:87]\n" \
"v_mfma_f32_16x16x32_bf16 a[40:43], a[232:235], a[200:203], a[40:43]\n" \
"ds_read_b64_tr_b16 a[244:245], v37 offset:40960\n" \
"v_mfma_f32_16x16x32_bf16 a[44:47], a[172:175], a[136:139], a[44:47]\n" \
"ds_read_b64_tr_b16 a[246:247], v37 offset:41984\n" \
"v_mfma_f32_16x16x32_bf16 a[44:47], a[236:239], a[200:203], a[44:47]\n" \
"v_mfma_f32_16x16x32_bf16 a[60:63], a[172:175], a[140:143], a[60:63]\n" \
"v_mfma_f32_16x16x32_bf16 a[60:63], a[236:239], a[204:207], a[60:63]\n" \
"v_mfma_f32_16x16x32_bf16 a[56:59], a[168:171], a[140:143], a[56:59]\n" \
"ds_read_b64_tr_b16 a[184:185], v38 offset:32768\n" \
"v_mfma_f32_16x16x32_bf16 a[56:59], a[232:235], a[204:207], a[56:59]\n" \
"v_mfma_f32_16x16x32_bf16 a[52:55], a[164:167], a[140:143], a[52:55]\n" \
"v_mfma_f32_16x16x32_bf16 a[52:55], a[228:231], a[204:207], a[52:55]\n" \
"ds_read_b64_tr_b16 a[186:187], v38 offset:33792\n" \
"v_mfma_f32_16x16x32_bf16 a[48:51], a[160:163], a[140:143], a[48:51]\n" \
"v_mfma_f32_16x16x32_bf16 a[48:51], a[224:227], a[204:207], a[48:51]\n" \
"v_mfma_f32_16x16x32_bf16 a[64:67], a[160:163], a[144:147], a[64:67]\n" \
"ds_read_b64_tr_b16 a[248:249], v38 offset:40960\n" \
"v_mfma_f32_16x16x32_bf16 a[64:67], a[224:227], a[208:211], a[64:67]\n" \
"v_mfma_f32_16x16x32_bf16 a[68:71], a[164:167], a[144:147], a[68:71]\n" \
"v_mfma_f32_16x16x32_bf16 a[68:71], a[228:231], a[208:211], a[68:71]\n" \
"ds_read_b64_tr_b16 a[250:251], v38 offset:41984\n" \
"v_mfma_f32_16x16x32_bf16 a[72:75], a[168:171], a[144:147], a[72:75]\n" \
"v_mfma_f32_16x16x32_bf16 a[72:75], a[232:235], a[208:211], a[72:75]\n" \
"v_mfma_f32_16x16x32_bf16 a[76:79], a[172:175], a[144:147], a[76:79]\n" \
"v_mfma_f32_16x16x32_bf16 a[76:79], a[236:239], a[208:211], a[76:79]\n" \
"ds_read_b64_tr_b16 a[188:189], v39 offset:32768\n" \
"v_mfma_f32_16x16x32_bf16 a[92:95], a[172:175], a[148:151], a[92:95]\n" \
"v_mfma_f32_16x16x32_bf16 a[92:95], a[236:239], a[212:215], a[92:95]\n" \
"v_mfma_f32_16x16x32_bf16 a[88:91], a[168:171], a[148:151], a[88:91]\n" \
"ds_read_b64_tr_b16 a[190:191], v39 offset:33792\n" \
"v_mfma_f32_16x16x32_bf16 a[88:91], a[232:235], a[212:215], a[88:91]\n" \
"v_mfma_f32_16x16x32_bf16 a[84:87], a[164:167], a[148:151], a[84:87]\n" \
"v_mfma_f32_16x16x32_bf16 a[84:87], a[228:231], a[212:215], a[84:87]\n" \
"ds_read_b64_tr_b16 a[252:253], v39 offset:40960\n" \
"v_mfma_f32_16x16x32_bf16 a[80:83], a[160:163], a[148:151], a[80:83]\n" \
"v_mfma_f32_16x16x32_bf16 a[80:83], a[224:227], a[212:215], a[80:83]\n" \
"v_mfma_f32_16x16x32_bf16 a[96:99], a[160:163], a[152:155], a[96:99]\n" \
"ds_read_b64_tr_b16 a[254:255], v39 offset:41984\n" \
"v_mfma_f32_16x16x32_bf16 a[96:99], a[224:227], a[216:219], a[96:99]\n" \
"v_mfma_f32_16x16x32_bf16 a[100:103], a[164:167], a[152:155], a[100:103]\n" \
"v_mfma_f32_16x16x32_bf16 a[100:103], a[228:231], a[216:219], a[100:103]\n" \
"v_mfma_f32_16x16x32_bf16 a[104:107], a[168:171], a[152:155], a[104:107]\n" \
"v_mfma_f32_16x16x32_bf16 a[104:107], a[232:235], a[216:219], a[104:107]\n" \
"v_mfma_f32_16x16x32_bf16 a[108:111], a[172:175], a[152:155], a[108:111]\n" \
"v_mfma_f32_16x16x32_bf16 a[108:111], a[236:239], a[216:219], a[108:111]\n" \
"s_waitcnt lgkmcnt(15)\n" \
"v_mfma_f32_16x16x32_bf16 a[124:127], a[172:175], a[156:159], a[124:127]\n" \
"v_mfma_f32_16x16x32_bf16 a[124:127], a[236:239], a[220:223], a[124:127]\n" \
"v_mfma_f32_16x16x32_bf16 a[120:123], a[168:171], a[156:159], a[120:123]\n" \
"v_mfma_f32_16x16x32_bf16 a[120:123], a[232:235], a[220:223], a[120:123]\n" \
"v_mfma_f32_16x16x32_bf16 a[116:119], a[164:167], a[156:159], a[116:119]\n" \
"v_mfma_f32_16x16x32_bf16 a[116:119], a[228:231], a[220:223], a[116:119]\n" \
"v_mfma_f32_16x16x32_bf16 a[112:115], a[160:163], a[156:159], a[112:115]\n" \
"v_mfma_f32_16x16x32_bf16 a[112:115], a[224:227], a[220:223], a[112:115]\n" \
"s_waitcnt vmcnt(1) lgkmcnt(0)\n" \
"s_barrier\n" \
"v_mov_b32_e32 v45, s62\n" \
"s_mov_b64 exec, 1\n" \
"buffer_store_dword v45, off, s[80:83], s37 offset:128 sc1\n" \
"s_mov_b64 exec, -1\n" \
"v_mfma_f32_16x16x32_bf16 v[52:55], a[176:179], a[128:131], v[52:55]\n" \
"s_add_u32 m0, s30, 0x8000\n" \
"s_movk_i32 s84, 0x380\n" \
"v_mfma_f32_16x16x32_bf16 v[52:55], a[240:243], a[192:195], v[52:55]\n" \
"buffer_load_dwordx4 v22, s[64:67], s84 offen offset:0 lds\n" \
"v_mfma_f32_16x16x32_bf16 v[56:59], a[180:183], a[128:131], v[56:59]\n" \
"ds_read_b64_tr_b16 a[160:161], v32 offset:0\n" \
"v_mfma_f32_16x16x32_bf16 v[56:59], a[244:247], a[192:195], v[56:59]\n" \
"s_movk_i32 s84, 0x1f80\n" \
"v_mfma_f32_16x16x32_bf16 v[60:63], a[184:187], a[128:131], v[60:63]\n" \
"buffer_load_dwordx4 v23, s[64:67], s84 offen offset:1024 lds\n" \
"v_mfma_f32_16x16x32_bf16 v[60:63], a[248:251], a[192:195], v[60:63]\n" \
"ds_read_b64_tr_b16 a[162:163], v32 offset:1024\n" \
"v_mfma_f32_16x16x32_bf16 v[64:67], a[188:191], a[128:131], v[64:67]\n" \
"ds_read_b64_tr_b16 a[224:225], v32 offset:8192\n" \
"v_mfma_f32_16x16x32_bf16 v[64:67], a[252:255], a[192:195], v[64:67]\n" \
"ds_read_b128 a[128:131], v28 offset:16384\n" \
"v_mfma_f32_16x16x32_bf16 v[80:83], a[188:191], a[132:135], v[80:83]\n" \
"ds_read_b128 a[192:195], v29 offset:16384\n" \
"v_mfma_f32_16x16x32_bf16 v[80:83], a[252:255], a[196:199], v[80:83]\n" \
"s_movk_i32 s84, 0x3b80\n" \
"v_mfma_f32_16x16x32_bf16 v[76:79], a[184:187], a[132:135], v[76:79]\n" \
"buffer_load_dwordx4 v22, s[64:67], s84 offen offset:2048 lds\n" \
"v_mfma_f32_16x16x32_bf16 v[76:79], a[248:251], a[196:199], v[76:79]\n" \
"s_movk_i32 s84, 0x5780\n" \
"v_mfma_f32_16x16x32_bf16 v[72:75], a[180:183], a[132:135], v[72:75]\n" \
"buffer_load_dwordx4 v23, s[64:67], s84 offen offset:3072 lds\n" \
"v_mfma_f32_16x16x32_bf16 v[72:75], a[244:247], a[196:199], v[72:75]\n" \
"s_add_u32 m0, s31, 0x8000\n" \
"s_mov_b32 s84, 0x380000\n" \
"v_mfma_f32_16x16x32_bf16 v[68:71], a[176:179], a[132:135], v[68:71]\n" \
"buffer_load_dwordx4 v26, s[68:71], s84 offen offset:0 lds\n" \
"v_mfma_f32_16x16x32_bf16 v[68:71], a[240:243], a[196:199], v[68:71]\n" \
"ds_read_b128 a[132:135], v28 offset:18432\n" \
"v_mfma_f32_16x16x32_bf16 v[84:87], a[176:179], a[136:139], v[84:87]\n" \
"ds_read_b128 a[196:199], v29 offset:18432\n" \
"v_mfma_f32_16x16x32_bf16 v[84:87], a[240:243], a[200:203], v[84:87]\n" \
"s_addk_i32 s84, 0x7c00\n" \
"v_mfma_f32_16x16x32_bf16 v[88:91], a[180:183], a[136:139], v[88:91]\n" \
"buffer_load_dwordx4 v26, s[68:71], s84 offen offset:1024 lds\n" \
"v_mfma_f32_16x16x32_bf16 v[88:91], a[244:247], a[200:203], v[88:91]\n" \
"ds_read_b64_tr_b16 a[226:227], v32 offset:9216\n" \
"v_mfma_f32_16x16x32_bf16 v[92:95], a[184:187], a[136:139], v[92:95]\n" \
"s_addk_i32 s84, 0x7c00\n" \
"v_mfma_f32_16x16x32_bf16 v[92:95], a[248:251], a[200:203], v[92:95]\n" \
"buffer_load_dwordx4 v27, s[68:71], s84 offen offset:2048 lds\n" \
"v_mfma_f32_16x16x32_bf16 v[96:99], a[188:191], a[136:139], v[96:99]\n" \
"ds_read_b64_tr_b16 a[164:165], v33 offset:0\n" \
"v_mfma_f32_16x16x32_bf16 v[96:99], a[252:255], a[200:203], v[96:99]\n" \
"ds_read_b128 a[136:139], v28 offset:20480\n" \
"v_mfma_f32_16x16x32_bf16 v[112:115], a[188:191], a[140:143], v[112:115]\n" \
"ds_read_b128 a[200:203], v29 offset:20480\n" \
"v_mfma_f32_16x16x32_bf16 v[112:115], a[252:255], a[204:207], v[112:115]\n" \
"s_addk_i32 s84, 0x7c00\n" \
"s_nop 0\n" \
"v_mfma_f32_16x16x32_bf16 v[108:111], a[184:187], a[140:143], v[108:111]\n" \
"buffer_load_dwordx4 v27, s[68:71], s84 offen offset:3072 lds\n" \
"v_mfma_f32_16x16x32_bf16 v[108:111], a[248:251], a[204:207], v[108:111]\n" \
"s_add_u32 m0, s30, 0x9000\n" \
"s_mov_b32 s84, 0x8380\n" \
"v_mfma_f32_16x16x32_bf16 v[104:107], a[180:183], a[140:143], v[104:107]\n" \
"buffer_load_dwordx4 v22, s[64:67], s84 offen offset:0 lds\n" \
"v_mfma_f32_16x16x32_bf16 v[104:107], a[244:247], a[204:207], v[104:107]\n" \
"ds_read_b64_tr_b16 a[166:167], v33 offset:1024\n" \
"v_mfma_f32_16x16x32_bf16 v[100:103], a[176:179], a[140:143], v[100:103]\n" \
"ds_read_b64_tr_b16 a[228:229], v33 offset:8192\n" \
"v_mfma_f32_16x16x32_bf16 v[100:103], a[240:243], a[204:207], v[100:103]\n" \
"ds_read_b128 a[140:143], v28 offset:22528\n" \
"v_mfma_f32_16x16x32_bf16 v[116:119], a[176:179], a[144:147], v[116:119]\n" \
"ds_read_b128 a[204:207], v29 offset:22528\n" \
"v_mfma_f32_16x16x32_bf16 v[116:119], a[240:243], a[208:211], v[116:119]\n" \
"s_addk_i32 s84, 0x1c00\n" \
"v_mfma_f32_16x16x32_bf16 v[120:123], a[180:183], a[144:147], v[120:123]\n" \
"buffer_load_dwordx4 v23, s[64:67], s84 offen offset:1024 lds\n" \
"v_mfma_f32_16x16x32_bf16 v[120:123], a[244:247], a[208:211], v[120:123]\n" \
"s_addk_i32 s84, 0x1c00\n" \
"v_mfma_f32_16x16x32_bf16 v[124:127], a[184:187], a[144:147], v[124:127]\n" \
"buffer_load_dwordx4 v22, s[64:67], s84 offen offset:2048 lds\n" \
"v_mfma_f32_16x16x32_bf16 v[124:127], a[248:251], a[208:211], v[124:127]\n" \
"s_addk_i32 s84, 0x1c00\n" \
"s_nop 0\n" \
"v_mfma_f32_16x16x32_bf16 v[128:131], a[188:191], a[144:147], v[128:131]\n" \
"buffer_load_dwordx4 v23, s[64:67], s84 offen offset:3072 lds\n" \
"v_mfma_f32_16x16x32_bf16 v[128:131], a[252:255], a[208:211], v[128:131]\n" \
"ds_read_b128 a[144:147], v28 offset:0\n" \
"v_mfma_f32_16x16x32_bf16 v[144:147], a[188:191], a[148:151], v[144:147]\n" \
"ds_read_b128 a[208:211], v29 offset:0\n" \
"v_mfma_f32_16x16x32_bf16 v[144:147], a[252:255], a[212:215], v[144:147]\n" \
"s_add_u32 m0, s31, 0xc000\n" \
"s_mov_b32 s84, 0x380000\n" \
"v_mfma_f32_16x16x32_bf16 v[140:143], a[184:187], a[148:151], v[140:143]\n" \
"buffer_load_dwordx4 v26, s[72:75], s84 offen offset:0 lds\n" \
"v_mfma_f32_16x16x32_bf16 v[140:143], a[248:251], a[212:215], v[140:143]\n" \
"ds_read_b64_tr_b16 a[230:231], v33 offset:9216\n" \
"v_mfma_f32_16x16x32_bf16 v[136:139], a[180:183], a[148:151], v[136:139]\n" \
"s_addk_i32 s84, 0x7c00\n" \
"s_nop 0\n" \
"v_mfma_f32_16x16x32_bf16 v[136:139], a[244:247], a[212:215], v[136:139]\n" \
"buffer_load_dwordx4 v26, s[72:75], s84 offen offset:1024 lds\n" \
"v_mfma_f32_16x16x32_bf16 v[132:135], a[176:179], a[148:151], v[132:135]\n" \
"ds_read_b64_tr_b16 a[168:169], v34 offset:0\n" \
"v_mfma_f32_16x16x32_bf16 v[132:135], a[240:243], a[212:215], v[132:135]\n" \
"ds_read_b128 a[148:151], v28 offset:2048\n" \
"v_mfma_f32_16x16x32_bf16 v[148:151], a[176:179], a[152:155], v[148:151]\n" \
"ds_read_b128 a[212:215], v29 offset:2048\n" \
"v_mfma_f32_16x16x32_bf16 v[148:151], a[240:243], a[216:219], v[148:151]\n" \
"ds_read_b64_tr_b16 a[170:171], v34 offset:1024\n" \
"v_mfma_f32_16x16x32_bf16 v[152:155], a[180:183], a[152:155], v[152:155]\n" \
"ds_read_b64_tr_b16 a[232:233], v34 offset:8192\n" \
"v_mfma_f32_16x16x32_bf16 v[152:155], a[244:247], a[216:219], v[152:155]\n" \
"ds_read_b64_tr_b16 a[234:235], v34 offset:9216\n" \
"v_mfma_f32_16x16x32_bf16 v[156:159], a[184:187], a[152:155], v[156:159]\n" \
"ds_read_b64_tr_b16 a[172:173], v35 offset:0\n" \
"v_mfma_f32_16x16x32_bf16 v[156:159], a[248:251], a[216:219], v[156:159]\n" \
"ds_read_b64_tr_b16 a[174:175], v35 offset:1024\n" \
"v_mfma_f32_16x16x32_bf16 v[160:163], a[188:191], a[152:155], v[160:163]\n" \
"ds_read_b64_tr_b16 a[236:237], v35 offset:8192\n" \
"v_mfma_f32_16x16x32_bf16 v[160:163], a[252:255], a[216:219], v[160:163]\n" \
"ds_read_b128 a[152:155], v28 offset:4096\n" \
"v_mfma_f32_16x16x32_bf16 v[176:179], a[188:191], a[156:159], v[176:179]\n" \
"ds_read_b128 a[216:219], v29 offset:4096\n" \
"v_mfma_f32_16x16x32_bf16 v[176:179], a[252:255], a[220:223], v[176:179]\n" \
"ds_read_b64_tr_b16 a[238:239], v35 offset:9216\n" \
"v_mfma_f32_16x16x32_bf16 v[172:175], a[184:187], a[156:159], v[172:175]\n" \
"v_mfma_f32_16x16x32_bf16 v[172:175], a[248:251], a[220:223], v[172:175]\n" \
"v_mfma_f32_16x16x32_bf16 v[168:171], a[180:183], a[156:159], v[168:171]\n" \
"v_mfma_f32_16x16x32_bf16 v[168:171], a[244:247], a[220:223], v[168:171]\n" \
"v_mfma_f32_16x16x32_bf16 v[164:167], a[176:179], a[156:159], v[164:167]\n" \
"v_mfma_f32_16x16x32_bf16 v[164:167], a[240:243], a[220:223], v[164:167]\n" \
"ds_read_b128 a[156:159], v28 offset:6144\n" \
"ds_read_b128 a[220:223], v29 offset:6144\n" \
"s_waitcnt lgkmcnt(15)\n" \
"v_mfma_f32_16x16x32_bf16 a[0:3], a[160:163], a[128:131], a[0:3]\n" \
"s_addk_i32 s84, 0x7c00\n" \
"v_mfma_f32_16x16x32_bf16 a[0:3], a[224:227], a[192:195], a[0:3]\n" \
"buffer_load_dwordx4 v27, s[72:75], s84 offen offset:2048 lds\n" \
"v_mfma_f32_16x16x32_bf16 a[4:7], a[164:167], a[128:131], a[4:7]\n" \
"ds_read_b64_tr_b16 a[176:177], v36 offset:0\n" \
"s_waitcnt lgkmcnt(15)\n" \
"v_mfma_f32_16x16x32_bf16 a[4:7], a[228:231], a[192:195], a[4:7]\n" \
"s_addk_i32 s84, 0x7c00\n" \
"s_waitcnt lgkmcnt(11)\n" \
"v_mfma_f32_16x16x32_bf16 a[8:11], a[168:171], a[128:131], a[8:11]\n" \
"buffer_load_dwordx4 v27, s[72:75], s84 offen offset:3072 lds\n" \
"s_waitcnt lgkmcnt(9)\n" \
"v_mfma_f32_16x16x32_bf16 a[8:11], a[232:235], a[192:195], a[8:11]\n" \
"ds_read_b64_tr_b16 a[178:179], v36 offset:1024\n" \
"s_waitcnt lgkmcnt(8)\n" \
"v_mfma_f32_16x16x32_bf16 a[12:15], a[172:175], a[128:131], a[12:15]\n" \
"ds_read_b64_tr_b16 a[240:241], v36 offset:8192\n" \
"s_waitcnt lgkmcnt(5)\n" \
"v_mfma_f32_16x16x32_bf16 a[12:15], a[236:239], a[192:195], a[12:15]\n" \
"v_mfma_f32_16x16x32_bf16 a[28:31], a[172:175], a[132:135], a[28:31]\n" \
"v_mfma_f32_16x16x32_bf16 a[28:31], a[236:239], a[196:199], a[28:31]\n" \
"ds_read_b64_tr_b16 a[242:243], v36 offset:9216\n" \
"v_mfma_f32_16x16x32_bf16 a[24:27], a[168:171], a[132:135], a[24:27]\n" \
"v_mfma_f32_16x16x32_bf16 a[24:27], a[232:235], a[196:199], a[24:27]\n" \
"v_mfma_f32_16x16x32_bf16 a[20:23], a[164:167], a[132:135], a[20:23]\n" \
"v_mfma_f32_16x16x32_bf16 a[20:23], a[228:231], a[196:199], a[20:23]\n" \
"ds_read_b64_tr_b16 a[180:181], v37 offset:0\n" \
"v_mfma_f32_16x16x32_bf16 a[16:19], a[160:163], a[132:135], a[16:19]\n" \
"v_mfma_f32_16x16x32_bf16 a[16:19], a[224:227], a[196:199], a[16:19]\n" \
"v_mfma_f32_16x16x32_bf16 a[32:35], a[160:163], a[136:139], a[32:35]\n" \
"v_bfe_u32 v44, v20, 2, 5\n" \
"ds_read_b64_tr_b16 a[182:183], v37 offset:1024\n" \
"v_mfma_f32_16x16x32_bf16 a[32:35], a[224:227], a[200:203], a[32:35]\n" \
"buffer_load_dword v44, v44, s[80:83], s38 offen offset:128 sc1\n" \
"v_mfma_f32_16x16x32_bf16 a[36:39], a[164:167], a[136:139], a[36:39]\n" \
"v_mfma_f32_16x16x32_bf16 a[36:39], a[228:231], a[200:203], a[36:39]\n" \
"ds_read_b64_tr_b16 a[244:245], v37 offset:8192\n" \
"v_mfma_f32_16x16x32_bf16 a[40:43], a[168:171], a[136:139], a[40:43]\n" \
"v_mfma_f32_16x16x32_bf16 a[40:43], a[232:235], a[200:203], a[40:43]\n" \
"v_mfma_f32_16x16x32_bf16 a[44:47], a[172:175], a[136:139], a[44:47]\n" \
"ds_read_b64_tr_b16 a[246:247], v37 offset:9216\n" \
"v_mfma_f32_16x16x32_bf16 a[44:47], a[236:239], a[200:203], a[44:47]\n" \
"v_mfma_f32_16x16x32_bf16 a[60:63], a[172:175], a[140:143], a[60:63]\n" \
"global_store_dwordx4 v49, v[0:3], s[28:29] offset:0 sc1\n" \
"v_mfma_f32_16x16x32_bf16 a[60:63], a[236:239], a[204:207], a[60:63]\n" \
"v_mfma_f32_16x16x32_bf16 a[56:59], a[168:171], a[140:143], a[56:59]\n" \
"ds_read_b64_tr_b16 a[184:185], v38 offset:0\n" \
"v_mfma_f32_16x16x32_bf16 a[56:59], a[232:235], a[204:207], a[56:59]\n" \
"v_mfma_f32_16x16x32_bf16 a[52:55], a[164:167], a[140:143], a[52:55]\n" \
"v_mfma_f32_16x16x32_bf16 a[52:55], a[228:231], a[204:207], a[52:55]\n" \
"ds_read_b64_tr_b16 a[186:187], v38 offset:1024\n" \
"v_mfma_f32_16x16x32_bf16 a[48:51], a[160:163], a[140:143], a[48:51]\n" \
"v_mfma_f32_16x16x32_bf16 a[48:51], a[224:227], a[204:207], a[48:51]\n" \
"v_mfma_f32_16x16x32_bf16 a[64:67], a[160:163], a[144:147], a[64:67]\n" \
"global_store_dwordx4 v49, v[0:3], s[28:29] offset:1024 sc1\n" \
"v_mfma_f32_16x16x32_bf16 a[64:67], a[224:227], a[208:211], a[64:67]\n" \
"ds_read_b64_tr_b16 a[248:249], v38 offset:8192\n" \
"v_mfma_f32_16x16x32_bf16 a[68:71], a[164:167], a[144:147], a[68:71]\n" \
"v_mfma_f32_16x16x32_bf16 a[68:71], a[228:231], a[208:211], a[68:71]\n" \
"ds_read_b64_tr_b16 a[250:251], v38 offset:9216\n" \
"v_mfma_f32_16x16x32_bf16 a[72:75], a[168:171], a[144:147], a[72:75]\n" \
"v_mfma_f32_16x16x32_bf16 a[72:75], a[232:235], a[208:211], a[72:75]\n" \
"v_mfma_f32_16x16x32_bf16 a[76:79], a[172:175], a[144:147], a[76:79]\n" \
"v_mfma_f32_16x16x32_bf16 a[76:79], a[236:239], a[208:211], a[76:79]\n" \
"ds_read_b64_tr_b16 a[188:189], v39 offset:0\n" \
"v_mfma_f32_16x16x32_bf16 a[92:95], a[172:175], a[148:151], a[92:95]\n" \
"s_mov_b64 exec, 0xffff\n" \
"global_store_dwordx4 v49, v[0:3], s[28:29] offset:2048 sc1\n" \
"s_mov_b64 exec, -1\n" \
"s_nop 0\n" \
"v_mfma_f32_16x16x32_bf16 a[92:95], a[236:239], a[212:215], a[92:95]\n" \
"v_mfma_f32_16x16x32_bf16 a[88:91], a[168:171], a[148:151], a[88:91]\n" \
"ds_read_b64_tr_b16 a[190:191], v39 offset:1024\n" \
"v_mfma_f32_16x16x32_bf16 a[88:91], a[232:235], a[212:215], a[88:91]\n" \
"v_mfma_f32_16x16x32_bf16 a[84:87], a[164:167], a[148:151], a[84:87]\n" \
"v_mfma_f32_16x16x32_bf16 a[84:87], a[228:231], a[212:215], a[84:87]\n" \
"ds_read_b64_tr_b16 a[252:253], v39 offset:8192\n" \
"v_mfma_f32_16x16x32_bf16 a[80:83], a[160:163], a[148:151], a[80:83]\n" \
"v_mfma_f32_16x16x32_bf16 a[80:83], a[224:227], a[212:215], a[80:83]\n" \
"v_mfma_f32_16x16x32_bf16 a[96:99], a[160:163], a[152:155], a[96:99]\n" \
"ds_read_b64_tr_b16 a[254:255], v39 offset:9216\n" \
"v_mfma_f32_16x16x32_bf16 a[96:99], a[224:227], a[216:219], a[96:99]\n" \
"v_mfma_f32_16x16x32_bf16 a[100:103], a[164:167], a[152:155], a[100:103]\n" \
"v_mfma_f32_16x16x32_bf16 a[100:103], a[228:231], a[216:219], a[100:103]\n" \
"v_mfma_f32_16x16x32_bf16 a[104:107], a[168:171], a[152:155], a[104:107]\n" \
"v_mfma_f32_16x16x32_bf16 a[104:107], a[232:235], a[216:219], a[104:107]\n" \
"v_mfma_f32_16x16x32_bf16 a[108:111], a[172:175], a[152:155], a[108:111]\n" \
"v_mfma_f32_16x16x32_bf16 a[108:111], a[236:239], a[216:219], a[108:111]\n" \
"s_waitcnt lgkmcnt(15)\n" \
"v_mfma_f32_16x16x32_bf16 a[124:127], a[172:175], a[156:159], a[124:127]\n" \
"v_mfma_f32_16x16x32_bf16 a[124:127], a[236:239], a[220:223], a[124:127]\n" \
"v_mfma_f32_16x16x32_bf16 a[120:123], a[168:171], a[156:159], a[120:123]\n" \
"v_mfma_f32_16x16x32_bf16 a[120:123], a[232:235], a[220:223], a[120:123]\n" \
"v_mfma_f32_16x16x32_bf16 a[116:119], a[164:167], a[156:159], a[116:119]\n" \
"v_mfma_f32_16x16x32_bf16 a[116:119], a[228:231], a[220:223], a[116:119]\n" \
"v_mfma_f32_16x16x32_bf16 a[112:115], a[160:163], a[156:159], a[112:115]\n" \
"v_mfma_f32_16x16x32_bf16 a[112:115], a[224:227], a[220:223], a[112:115]\n" \
"s_waitcnt vmcnt(3) lgkmcnt(0)\n" \
"s_barrier\n" \
"v_cmp_ne_u32_e32 vcc, s62, v44\n" \
"s_nop 4\n" \
"s_cbranch_vccnz L_poll60_%=\n" \
"L_pollr60_%=:\n" \
"s_mov_b32 s68, s12\n" \
"s_and_b32 s69, s13, 0xffff\n" \
"s_mov_b32 s70, 0x480000\n" \
"s_mov_b32 s71, 0x20000\n" \
"v_mfma_f32_16x16x32_bf16 v[52:55], a[176:179], a[128:131], v[52:55]\n" \
"s_add_u32 m0, s40, 0x0\n" \
"s_add_u32 s85, s39, 0x0\n" \
"s_nop 0\n" \
"v_mfma_f32_16x16x32_bf16 v[52:55], a[240:243], a[192:195], v[52:55]\n" \
"buffer_load_dwordx4 v20, s[80:83], s85 offen sc1 lds\n" \
"v_mfma_f32_16x16x32_bf16 v[56:59], a[180:183], a[128:131], v[56:59]\n" \
"ds_read_b64_tr_b16 a[160:161], v32 offset:32768\n" \
"v_mfma_f32_16x16x32_bf16 v[56:59], a[244:247], a[192:195], v[56:59]\n" \
"s_add_u32 m0, s40, 0x1000\n" \
"s_add_u32 s85, s39, 0x1000\n" \
".fill (L_pb + 16096 - .) / 4, 4, 0xBF800000\n" \
"L_pp4:\n" \
"s_cbranch_execz L_pret\n" \
"s_nop 0\n" \
"v_mfma_f32_16x16x32_bf16 v[60:63], a[184:187], a[128:131], v[60:63]\n" \
"buffer_load_dwordx4 v20, s[80:83], s85 offen sc1 lds\n" \
"v_mfma_f32_16x16x32_bf16 v[60:63], a[248:251], a[192:195], v[60:63]\n" \
"ds_read_b64_tr_b16 a[162:163], v32 offset:33792\n" \
"v_mfma_f32_16x16x32_bf16 v[64:67], a[188:191], a[128:131], v[64:67]\n" \
"ds_read_b64_tr_b16 a[224:225], v32 offset:40960\n" \
"v_mfma_f32_16x16x32_bf16 v[64:67], a[252:255], a[192:195], v[64:67]\n" \
"ds_read_b128 a[128:131], v28 offset:49152\n" \
"v_mfma_f32_16x16x32_bf16 v[80:83], a[188:191], a[132:135], v[80:83]\n" \
"ds_read_b128 a[192:195], v29 offset:49152\n" \
"v_mfma_f32_16x16x32_bf16 v[80:83], a[252:255], a[196:199], v[80:83]\n" \
"s_add_u32 m0, s40, 0x3000\n" \
"s_movk_i32 s84, 0x0\n" \
"v_mfma_f32_16x16x32_bf16 v[76:79], a[184:187], a[132:135], v[76:79]\n" \
"buffer_load_dwordx4 v21, s[76:79], s84 offen offset:0 lds\n" \
"v_mfma_f32_16x16x32_bf16 v[76:79], a[248:251], a[196:199], v[76:79]\n" \
"s_add_u32 m0, s30, 0x0\n" \
"s_movk_i32 s84, 0x0\n" \
"s_nop 0\n" \
"v_mfma_f32_16x16x32_bf16 v[72:75], a[180:183], a[132:135], v[72:75]\n" \
"buffer_load_dwordx4 v24, s[76:79], s84 offen offset:0 lds\n" \
"v_mfma_f32_16x16x32_bf16 v[72:75], a[244:247], a[196:199], v[72:75]\n" \
"ds_read_b64_tr_b16 a[226:227], v32 offset:41984\n" \
"v_mfma_f32_16x16x32_bf16 v[68:71], a[176:179], a[132:135], v[68:71]\n" \
"ds_read_b64_tr_b16 a[164:165], v33 offset:32768\n" \
"v_mfma_f32_16x16x32_bf16 v[68:71], a[240:243], a[196:199], v[68:71]\n" \
"ds_read_b128 a[132:135], v28 offset:51200\n" \
"v_mfma_f32_16x16x32_bf16 v[84:87], a[176:179], a[136:139], v[84:87]\n" \
"ds_read_b128 a[196:199], v29 offset:51200\n" \
"v_mfma_f32_16x16x32_bf16 v[84:87], a[240:243], a[200:203], v[84:87]\n" \
"s_movk_i32 s84, 0x2000\n" \
"v_mfma_f32_16x16x32_bf16 v[88:91], a[180:183], a[136:139], v[88:91]\n" \
"buffer_load_dwordx4 v25, s[76:79], s84 offen offset:1024 lds\n" \
"v_mfma_f32_16x16x32_bf16 v[88:91], a[244:247], a[200:203], v[88:91]\n" \
"s_movk_i32 s84, 0x4000\n" \
"v_mfma_f32_16x16x32_bf16 v[92:95], a[184:187], a[136:139], v[92:95]\n" \
"buffer_load_dwordx4 v24, s[76:79], s84 offen offset:2048 lds\n" \
"v_mfma_f32_16x16x32_bf16 v[92:95], a[248:251], a[200:203], v[92:95]\n" \
"ds_read_b64_tr_b16 a[166:167], v33 offset:33792\n" \
"v_mfma_f32_16x16x32_bf16 v[96:99], a[188:191], a[136:139], v[96:99]\n" \
"ds_read_b64_tr_b16 a[228:229], v33 offset:40960\n" \
"v_mfma_f32_16x16x32_bf16 v[96:99], a[252:255], a[200:203], v[96:99]\n" \
"ds_read_b128 a[136:139], v28 offset:53248\n" \
"v_mfma_f32_16x16x32_bf16 v[112:115], a[188:191], a[140:143], v[112:115]\n" \
"ds_read_b128 a[200:203], v29 offset:53248\n" \
"v_mfma_f32_16x16x32_bf16 v[112:115], a[252:255], a[204:207], v[112:115]\n" \
"s_movk_i32 s84, 0x6000\n" \
"v_mfma_f32_16x16x32_bf16 v[108:111], a[184:187], a[140:143], v[108:111]\n" \
"buffer_load_dwordx4 v25, s[76:79], s84 offen offset:3072 lds\n" \
"v_mfma_f32_16x16x32_bf16 v[108:111], a[248:251], a[204:207], v[108:111]\n" \
"s_add_u32 m0, s31, 0x0\n" \
"s_movk_i32 s84, 0x0\n" \
"v_mfma_f32_16x16x32_bf16 v[104:107], a[180:183], a[140:143], v[104:107]\n" \
"buffer_load_dwordx4 v26, s[68:71], s84 offen offset:0 lds\n" \
"v_mfma_f32_16x16x32_bf16 v[104:107], a[244:247], a[204:207], v[104:107]\n" \
"s_movk_i32 s84, 0x7c00\n" \
"v_mfma_f32_16x16x32_bf16 v[100:103], a[176:179], a[140:143], v[100:103]\n" \
"buffer_load_dwordx4 v26, s[68:71], s84 offen offset:1024 lds\n" \
"v_mfma_f32_16x16x32_bf16 v[100:103], a[240:243], a[204:207], v[100:103]\n" \
"ds_read_b128 a[140:143], v28 offset:55296\n" \
"v_mfma_f32_16x16x32_bf16 v[116:119], a[176:179], a[144:147], v[116:119]\n" \
"ds_read_b128 a[204:207], v29 offset:55296\n" \
"v_mfma_f32_16x16x32_bf16 v[116:119], a[240:243], a[208:211], v[116:119]\n" \
"s_addk_i32 s84, 0x7c00\n" \
"v_mfma_f32_16x16x32_bf16 v[120:123], a[180:183], a[144:147], v[120:123]\n" \
"buffer_load_dwordx4 v27, s[68:71], s84 offen offset:2048 lds\n" \
"v_mfma_f32_16x16x32_bf16 v[120:123], a[244:247], a[208:211], v[120:123]\n" \
"ds_read_b64_tr_b16 a[230:231], v33 offset:41984\n" \
"v_mfma_f32_16x16x32_bf16 v[124:127], a[184:187], a[144:147], v[124:127]\n" \
"s_addk_i32 s84, 0x7c00\n" \
"v_mfma_f32_16x16x32_bf16 v[124:127], a[248:251], a[208:211], v[124:127]\n" \
"buffer_load_dwordx4 v27, s[68:71], s84 offen offset:3072 lds\n" \
"v_mfma_f32_16x16x32_bf16 v[128:131], a[188:191], a[144:147], v[128:131]\n" \
"ds_read_b64_tr_b16 a[168:169], v34 offset:32768\n" \
"v_mfma_f32_16x16x32_bf16 v[128:131], a[252:255], a[208:211], v[128:131]\n" \
"ds_read_b128 a[144:147], v28 offset:32768\n" \
"v_mfma_f32_16x16x32_bf16 v[144:147], a[188:191], a[148:151], v[144:147]\n" \
"ds_read_b128 a[208:211], v29 offset:32768\n" \
"v_mfma_f32_16x16x32_bf16 v[144:147], a[252:255], a[212:215], v[144:147]\n" \
"s_add_u32 m0, s30, 0x1000\n" \
"s_mov_b32 s84, 0x9000\n" \
"v_mfma_f32_16x16x32_bf16 v[140:143], a[184:187], a[148:151], v[140:143]\n" \
"buffer_load_dwordx4 v24, s[76:79], s84 offen offset:0 lds\n" \
"v_mfma_f32_16x16x32_bf16 v[140:143], a[248:251], a[212:215], v[140:143]\n" \
"ds_read_b64_tr_b16 a[170:171], v34 offset:33792\n" \
"v_mfma_f32_16x16x32_bf16 v[136:139], a[180:183], a[148:151], v[136:139]\n" \
"s_addk_i32 s84, 0x2000\n" \
"s_nop 0\n" \
"v_mfma_f32_16x16x32_bf16 v[136:139], a[244:247], a[212:215], v[136:139]\n" \
"buffer_load_dwordx4 v25, s[76:79], s84 offen offset:1024 lds\n" \
"v_mfma_f32_16x16x32_bf16 v[132:135], a[176:179], a[148:151], v[132:135]\n" \
"ds_read_b64_tr_b16 a[232:233], v34 offset:40960\n" \
"v_mfma_f32_16x16x32_bf16 v[132:135], a[240:243], a[212:215], v[132:135]\n" \
"ds_read_b128 a[148:151], v28 offset:34816\n" \
"v_mfma_f32_16x16x32_bf16 v[148:151], a[176:179], a[152:155], v[148:151]\n" \
"ds_read_b128 a[212:215], v29 offset:34816\n" \
"v_mfma_f32_16x16x32_bf16 v[148:151], a[240:243], a[216:219], v[148:151]\n" \
"ds_read_b64_tr_b16 a[234:235], v34 offset:41984\n" \
"v_mfma_f32_16x16x32_bf16 v[152:155], a[180:183], a[152:155], v[152:155]\n" \
"ds_read_b64_tr_b16 a[172:173], v35 offset:32768\n" \
"v_mfma_f32_16x16x32_bf16 v[152:155], a[244:247], a[216:219], v[152:155]\n" \
"ds_read_b64_tr_b16 a[174:175], v35 offset:33792\n" \
"v_mfma_f32_16x16x32_bf16 v[156:159], a[184:187], a[152:155], v[156:159]\n" \
"ds_read_b64_tr_b16 a[236:237], v35 offset:40960\n" \
"v_mfma_f32_16x16x32_bf16 v[156:159], a[248:251], a[216:219], v[156:159]\n" \
"ds_read_b64_tr_b16 a[238:239], v35 offset:41984\n" \
"v_mfma_f32_16x16x32_bf16 v[160:163], a[188:191], a[152:155], v[160:163]\n" \
"v_mfma_f32_16x16x32_bf16 v[160:163], a[252:255], a[216:219], v[160:163]\n" \
"ds_read_b128 a[152:155], v28 offset:36864\n" \
"v_mfma_f32_16x16x32_bf16 v[176:179], a[188:191], a[156:159], v[176:179]\n" \
"ds_read_b128 a[216:219], v29 offset:36864\n" \
"v_mfma_f32_16x16x32_bf16 v[176:179], a[252:255], a[220:223], v[176:179]\n" \
"v_mfma_f32_16x16x32_bf16 v[172:175], a[184:187], a[156:159], v[172:175]\n" \
"v_mfma_f32_16x16x32_bf16 v[172:175], a[248:251], a[220:223], v[172:175]\n" \
"v_mfma_f32_16x16x32_bf16 v[168:171], a[180:183], a[156:159], v[168:171]\n" \
"v_mfma_f32_16x16x32_bf16 v[168:171], a[244:247], a[220:223], v[168:171]\n" \
"v_mfma_f32_16x16x32_bf16 v[164:167], a[176:179], a[156:159], v[164:167]\n" \
"v_mfma_f32_16x16x32_bf16 v[164:167], a[240:243], a[220:223], v[164:167]\n" \
"ds_read_b128 a[156:159], v28 offset:38912\n" \
"ds_read_b128 a[220:223], v29 offset:38912\n" \
"s_waitcnt lgkmcnt(15)\n" \
"v_mfma_f32_16x16x32_bf16 a[0:3], a[160:163], a[128:131], a[0:3]\n" \
"s_addk_i32 s84, 0x2000\n" \
"v_mfma_f32_16x16x32_bf16 a[0:3], a[224:227], a[192:195], a[0:3]\n" \
"buffer_load_dwordx4 v24, s[76:79], s84 offen offset:2048 lds\n" \
"v_mfma_f32_16x16x32_bf16 a[4:7], a[164:167], a[128:131], a[4:7]\n" \
"ds_read_b64_tr_b16 a[176:177], v36 offset:32768\n" \
"v_mfma_f32_16x16x32_bf16 a[4:7], a[228:231], a[192:195], a[4:7]\n" \
"s_addk_i32 s84, 0x2000\n" \
"s_waitcnt lgkmcnt(13)\n" \
"v_mfma_f32_16x16x32_bf16 a[8:11], a[168:171], a[128:131], a[8:11]\n" \
"buffer_load_dwordx4 v25, s[76:79], s84 offen offset:3072 lds\n" \
"s_waitcnt lgkmcnt(9)\n" \
"v_mfma_f32_16x16x32_bf16 a[8:11], a[232:235], a[192:195], a[8:11]\n" \
"ds_read_b64_tr_b16 a[178:179], v36 offset:33792\n" \
"s_waitcnt lgkmcnt(8)\n" \
"v_mfma_f32_16x16x32_bf16 a[12:15], a[172:175], a[128:131], a[12:15]\n" \
"ds_read_b64_tr_b16 a[240:241], v36 offset:40960\n" \
"v_accvgpr_read_b32 v184, a0\n" \
"s_waitcnt lgkmcnt(7)\n" \
"v_mfma_f32_16x16x32_bf16 a[12:15], a[236:239], a[192:195], a[12:15]\n" \
"v_accvgpr_read_b32 v185, a1\n" \
"v_accvgpr_read_b32 v186, a2\n" \
"v_mfma_f32_16x16x32_bf16 a[28:31], a[172:175], a[132:135], a[28:31]\n" \
"v_accvgpr_read_b32 v187, a3\n" \
"v_accvgpr_read_b32 v180, a4\n" \
"v_mfma_f32_16x16x32_bf16 a[28:31], a[236:239], a[196:199], a[28:31]\n" \
"s_add_u32 m0, s30, 0x20000\n" \
"s_movk_i32 s84, 0x100\n" \
"v_mfma_f32_16x16x32_bf16 a[24:27], a[168:171], a[132:135], a[24:27]\n" \
"buffer_load_dwordx4 v24, s[76:79], s84 offen offset:0 lds\n" \
"v_mfma_f32_16x16x32_bf16 a[24:27], a[232:235], a[196:199], a[24:27]\n" \
"ds_read_b64_tr_b16 a[242:243], v36 offset:41984\n" \
"v_accvgpr_read_b32 v181, a5\n" \
"v_mfma_f32_16x16x32_bf16 a[20:23], a[164:167], a[132:135], a[20:23]\n" \
"v_accvgpr_read_b32 v182, a6\n" \
"v_accvgpr_read_b32 v183, a7\n" \
"v_mfma_f32_16x16x32_bf16 a[20:23], a[228:231], a[196:199], a[20:23]\n" \
"ds_read_b64_tr_b16 a[180:181], v37 offset:32768\n" \
"v_cvt_pk_bf16_f32 v184, v184, v185\n" \
"v_mfma_f32_16x16x32_bf16 a[16:19], a[160:163], a[132:135], a[16:19]\n" \
"s_movk_i32 s84, 0x2100\n" \
"s_nop 0\n" \
"v_mfma_f32_16x16x32_bf16 a[16:19], a[224:227], a[196:199], a[16:19]\n" \
"buffer_load_dwordx4 v25, s[76:79], s84 offen offset:1024 lds\n" \
"v_mfma_f32_16x16x32_bf16 a[32:35], a[160:163], a[136:139], a[32:35]\n" \
"ds_read_b64_tr_b16 a[182:183], v37 offset:33792\n" \
"v_cvt_pk_bf16_f32 v185, v186, v187\n" \
"v_mfma_f32_16x16x32_bf16 a[32:35], a[224:227], a[200:203], a[32:35]\n" \
"v_cvt_pk_bf16_f32 v186, v180, v181\n" \
"v_cvt_pk_bf16_f32 v187, v182, v183\n" \
"v_mfma_f32_16x16x32_bf16 a[36:39], a[164:167], a[136:139], a[36:39]\n" \
"v_accvgpr_read_b32 v188, a8\n" \
"v_accvgpr_read_b32 v189, a9\n" \
"v_mfma_f32_16x16x32_bf16 a[36:39], a[228:231], a[200:203], a[36:39]\n" \
"s_movk_i32 s84, 0x4100\n" \
"s_nop 0\n" \
"v_mfma_f32_16x16x32_bf16 a[40:43], a[168:171], a[136:139], a[40:43]\n" \
"buffer_load_dwordx4 v24, s[76:79], s84 offen offset:2048 lds\n" \
"v_mfma_f32_16x16x32_bf16 a[40:43], a[232:235], a[200:203], a[40:43]\n" \
"ds_read_b64_tr_b16 a[244:245], v37 offset:40960\n" \
"v_accvgpr_read_b32 v190, a10\n" \
"v_mfma_f32_16x16x32_bf16 a[44:47], a[172:175], a[136:139], a[44:47]\n" \
"ds_read_b64_tr_b16 a[246:247], v37 offset:41984\n" \
"v_accvgpr_read_b32 v191, a11\n" \
"v_mfma_f32_16x16x32_bf16 a[44:47], a[236:239], a[200:203], a[44:47]\n" \
"v_accvgpr_read_b32 v180, a12\n" \
"v_accvgpr_read_b32 v181, a13\n" \
"v_mfma_f32_16x16x32_bf16 a[60:63], a[172:175], a[140:143], a[60:63]\n" \
"s_movk_i32 s84, 0x6100\n" \
"v_mfma_f32_16x16x32_bf16 a[60:63], a[236:239], a[204:207], a[60:63]\n" \
"buffer_load_dwordx4 v25, s[76:79], s84 offen offset:3072 lds\n" \
"v_mfma_f32_16x16x32_bf16 a[56:59], a[168:171], a[140:143], a[56:59]\n" \
"ds_read_b64_tr_b16 a[184:185], v38 offset:32768\n" \
"v_accvgpr_read_b32 v182, a14\n" \
"v_mfma_f32_16x16x32_bf16 a[56:59], a[232:235], a[204:207], a[56:59]\n" \
"v_accvgpr_read_b32 v183, a15\n" \
"v_cvt_pk_bf16_f32 v188, v188, v189\n" \
"v_mfma_f32_16x16x32_bf16 a[52:55], a[164:167], a[140:143], a[52:55]\n" \
"s_add_u32 m0, s30, 0x21000\n" \
"s_addk_i32 s84, 0x3000\n" \
"v_mfma_f32_16x16x32_bf16 a[52:55], a[228:231], a[204:207], a[52:55]\n" \
"buffer_load_dwordx4 v24, s[76:79], s84 offen offset:0 lds\n" \
"v_mfma_f32_16x16x32_bf16 a[48:51], a[160:163], a[140:143], a[48:51]\n" \
"ds_read_b64_tr_b16 a[186:187], v38 offset:33792\n" \
"v_cvt_pk_bf16_f32 v189, v190, v191\n" \
"v_mfma_f32_16x16x32_bf16 a[48:51], a[224:227], a[204:207], a[48:51]\n" \
"v_cvt_pk_bf16_f32 v190, v180, v181\n" \
"v_cvt_pk_bf16_f32 v191, v182, v183\n" \
"v_mfma_f32_16x16x32_bf16 a[64:67], a[160:163], a[144:147], a[64:67]\n" \
"ds_read_b64_tr_b16 a[248:249], v38 offset:40960\n" \
"v_accvgpr_read_b32 v192, a24\n" \
"v_mfma_f32_16x16x32_bf16 a[64:67], a[224:227], a[208:211], a[64:67]\n" \
"s_addk_i32 s84, 0x2000\n" \
"s_nop 0\n" \
"v_mfma_f32_16x16x32_bf16 a[68:71], a[164:167], a[144:147], a[68:71]\n" \
"buffer_load_dwordx4 v25, s[76:79], s84 offen offset:1024 lds\n" \
"v_mfma_f32_16x16x32_bf16 a[68:71], a[228:231], a[208:211], a[68:71]\n" \
"ds_read_b64_tr_b16 a[250:251], v38 offset:41984\n" \
"v_accvgpr_read_b32 v193, a25\n" \
"v_mfma_f32_16x16x32_bf16 a[72:75], a[168:171], a[144:147], a[72:75]\n" \
"v_accvgpr_read_b32 v194, a26\n" \
"v_accvgpr_read_b32 v195, a27\n" \
"v_mfma_f32_16x16x32_bf16 a[72:75], a[232:235], a[208:211], a[72:75]\n" \
"v_accvgpr_read_b32 v180, a28\n" \
"v_accvgpr_read_b32 v181, a29\n" \
"v_mfma_f32_16x16x32_bf16 a[76:79], a[172:175], a[144:147], a[76:79]\n" \
"s_addk_i32 s84, 0x2000\n" \
"v_mfma_f32_16x16x32_bf16 a[76:79], a[236:239], a[208:211], a[76:79]\n" \
"buffer_load_dwordx4 v24, s[76:79], s84 offen offset:2048 lds\n" \
"v_mfma_f32_16x16x32_bf16 a[92:95], a[172:175], a[148:151], a[92:95]\n" \
"s_add_u32 s89, s41, 0x0\n" \
"buffer_store_dwordx4 v[184:187], v20, s[80:83], s89 offen offset:0 sc1\n" \
"v_mfma_f32_16x16x32_bf16 a[92:95], a[236:239], a[212:215], a[92:95]\n" \
"ds_read_b64_tr_b16 a[188:189], v39 offset:32768\n" \
"v_accvgpr_read_b32 v182, a30\n" \
"v_mfma_f32_16x16x32_bf16 a[88:91], a[168:171], a[148:151], a[88:91]\n" \
"ds_read_b64_tr_b16 a[190:191], v39 offset:33792\n" \
"v_accvgpr_read_b32 v183, a31\n" \
"v_mfma_f32_16x16x32_bf16 a[88:91], a[232:235], a[212:215], a[88:91]\n" \
"s_addk_i32 s84, 0x2000\n" \
"v_mfma_f32_16x16x32_bf16 a[84:87], a[164:167], a[148:151], a[84:87]\n" \
"buffer_load_dwordx4 v25, s[76:79], s84 offen offset:3072 lds\n" \
"v_mfma_f32_16x16x32_bf16 a[84:87], a[228:231], a[212:215], a[84:87]\n" \
"ds_read_b64_tr_b16 a[252:253], v39 offset:40960\n" \
"v_cvt_pk_bf16_f32 v192, v192, v193\n" \
"v_mfma_f32_16x16x32_bf16 a[80:83], a[160:163], a[148:151], a[80:83]\n" \
"s_add_u32 s88, s41, 0x0\n" \
"buffer_store_dwordx4 v[188:191], v20, s[80:83], s88 offen offset:1024 sc1\n" \
"v_mfma_f32_16x16x32_bf16 a[80:83], a[224:227], a[212:215], a[80:83]\n" \
"v_cvt_pk_bf16_f32 v193, v194, v195\n" \
"v_cvt_pk_bf16_f32 v194, v180, v181\n" \
"s_waitcnt lgkmcnt(15)\n" \
"s_nop 0\n" \
"v_mfma_f32_16x16x32_bf16 a[96:99], a[160:163], a[152:155], a[96:99]\n" \
"ds_read_b64_tr_b16 a[254:255], v39 offset:41984\n" \
"v_cvt_pk_bf16_f32 v195, v182, v183\n" \
"v_mfma_f32_16x16x32_bf16 a[96:99], a[224:227], a[216:219], a[96:99]\n" \
"v_accvgpr_read_b32 v196, a16\n" \
"v_accvgpr_read_b32 v197, a17\n" \
"v_mfma_f32_16x16x32_bf16 a[100:103], a[164:167], a[152:155], a[100:103]\n" \
"v_accvgpr_read_b32 v198, a18\n" \
"v_accvgpr_read_b32 v199, a19\n" \
"v_mfma_f32_16x16x32_bf16 a[100:103], a[228:231], a[216:219], a[100:103]\n" \
"v_accvgpr_read_b32 v180, a20\n" \
"v_accvgpr_read_b32 v181, a21\n" \
"v_mfma_f32_16x16x32_bf16 a[104:107], a[168:171], a[152:155], a[104:107]\n" \
"s_add_u32 s89, s41, 0x1000\n" \
"buffer_store_dwordx4 v[192:195], v20, s[80:83], s89 offen offset:1024 sc1\n" \
"v_mfma_f32_16x16x32_bf16 a[104:107], a[232:235], a[216:219], a[104:107]\n" \
"v_accvgpr_read_b32 v182, a22\n" \
"v_accvgpr_read_b32 v183, a23\n" \
"v_mfma_f32_16x16x32_bf16 a[108:111], a[172:175], a[152:155], a[108:111]\n" \
"v_cvt_pk_bf16_f32 v196, v196, v197\n" \
"v_cvt_pk_bf16_f32 v197, v198, v199\n" \
"v_mfma_f32_16x16x32_bf16 a[108:111], a[236:239], a[216:219], a[108:111]\n" \
"v_cvt_pk_bf16_f32 v198, v180, v181\n" \
"v_cvt_pk_bf16_f32 v199, v182, v183\n" \
"v_mfma_f32_16x16x32_bf16 a[124:127], a[172:175], a[156:159], a[124:127]\n" \
"v_accvgpr_read_b32 v200, a32\n" \
"v_accvgpr_read_b32 v201, a33\n" \
"v_mfma_f32_16x16x32_bf16 a[124:127], a[236:239], a[220:223], a[124:127]\n" \
"v_accvgpr_read_b32 v202, a34\n" \
"v_accvgpr_read_b32 v203, a35\n" \
"v_mfma_f32_16x16x32_bf16 a[120:123], a[168:171], a[156:159], a[120:123]\n" \
"s_add_u32 s88, s41, 0x1000\n" \
"buffer_store_dwordx4 v[196:199], v20, s[80:83], s88 offen offset:0 sc1\n" \
"v_mfma_f32_16x16x32_bf16 a[120:123], a[232:235], a[220:223], a[120:123]\n" \
"v_accvgpr_read_b32 v180, a36\n" \
"v_accvgpr_read_b32 v181, a37\n" \
"v_mfma_f32_16x16x32_bf16 a[116:119], a[164:167], a[156:159], a[116:119]\n" \
"v_accvgpr_read_b32 v182, a38\n" \
"v_accvgpr_read_b32 v183, a39\n" \
"v_mfma_f32_16x16x32_bf16 a[116:119], a[228:231], a[220:223], a[116:119]\n" \
"v_cvt_pk_bf16_f32 v200, v200, v201\n" \
"v_cvt_pk_bf16_f32 v201, v202, v203\n" \
"v_mfma_f32_16x16x32_bf16 a[112:115], a[160:163], a[156:159], a[112:115]\n" \
"v_cvt_pk_bf16_f32 v202, v180, v181\n" \
"v_cvt_pk_bf16_f32 v203, v182, v183\n" \
"v_mfma_f32_16x16x32_bf16 a[112:115], a[224:227], a[220:223], a[112:115]\n" \
"v_accvgpr_read_b32 v204, a40\n" \
"v_accvgpr_read_b32 v205, a41\n" \
"s_waitcnt vmcnt(12) lgkmcnt(0)\n" \
"s_barrier\n" \
"v_mov_b32_e32 v36, v32\n" \
"v_xor_b32_e32 v32, 0x0, v48\n" \
"v_mov_b32_e32 v37, v33\n" \
"v_xor_b32_e32 v33, 0x20, v48\n" \
"v_mov_b32_e32 v38, v34\n" \
"v_xor_b32_e32 v34, 0x40, v48\n" \
"v_mov_b32_e64 v39, v35\n" \
"v_xor_b32_e32 v35, 0x60, v48\n" \
"v_add_u32_e32 v22, s44, v28\n" \
"v_add_u32_e32 v28, s43, v28\n" \
"v_add_u32_e32 v23, s44, v29\n" \
"v_add_u32_e32 v29, s43, v29\n" \
"v_mfma_f32_16x16x32_bf16 v[52:55], a[176:179], a[128:131], v[52:55]\n" \
"s_add_u32 m0, s40, 0x8000\n" \
"s_add_u32 s85, s39, 0x2000\n" \
"v_mfma_f32_16x16x32_bf16 v[52:55], a[240:243], a[192:195], v[52:55]\n" \
"buffer_load_dwordx4 v20, s[80:83], s85 offen sc1 lds\n" \
"v_mfma_f32_16x16x32_bf16 v[56:59], a[180:183], a[128:131], v[56:59]\n" \
"ds_read_b64_tr_b16 a[160:161], v32 offset:0\n" \
"v_accvgpr_read_b32 v206, a42\n" \
"v_mfma_f32_16x16x32_bf16 v[56:59], a[244:247], a[192:195], v[56:59]\n" \
"s_add_u32 m0, s40, 0x9000\n" \
"s_add_u32 s85, s39, 0x3000\n" \
"v_mfma_f32_16x16x32_bf16 v[60:63], a[184:187], a[128:131], v[60:63]\n" \
"buffer_load_dwordx4 v20, s[80:83], s85 offen sc1 lds\n" \
"v_mfma_f32_16x16x32_bf16 v[60:63], a[248:251], a[192:195], v[60:63]\n" \
"ds_read_b64_tr_b16 a[162:163], v32 offset:512\n" \
"v_accvgpr_read_b32 v207, a43\n" \
"v_mfma_f32_16x16x32_bf16 v[64:67], a[188:191], a[128:131], v[64:67]\n" \
"ds_read_b64_tr_b16 a[224:225], v32 offset:4096\n" \
"v_accvgpr_read_b32 v180, a44\n" \
"v_mfma_f32_16x16x32_bf16 v[64:67], a[252:255], a[192:195], v[64:67]\n" \
"ds_read_b128 a[128:131], v28 offset:0\n" \
"v_accvgpr_read_b32 v181, a45\n" \
"v_mfma_f32_16x16x32_bf16 v[80:83], a[188:191], a[132:135], v[80:83]\n" \
"ds_read_b128 a[192:195], v29 offset:0\n" \
"v_accvgpr_read_b32 v182, a46\n" \
"v_mfma_f32_16x16x32_bf16 v[80:83], a[252:255], a[196:199], v[80:83]\n" \
"s_add_u32 m0, s40, 0xb000\n" \
"s_movk_i32 s84, 0x80\n" \
"v_mfma_f32_16x16x32_bf16 v[76:79], a[184:187], a[132:135], v[76:79]\n" \
"buffer_load_dwordx4 v21, s[76:79], s84 offen offset:0 lds\n" \
"v_mfma_f32_16x16x32_bf16 v[76:79], a[248:251], a[196:199], v[76:79]\n" \
"s_add_u32 m0, s30, 0x8000\n" \
"s_movk_i32 s84, 0x80\n" \
"v_mfma_f32_16x16x32_bf16 v[72:75], a[180:183], a[132:135], v[72:75]\n" \
"buffer_load_dwordx4 v24, s[76:79], s84 offen offset:0 lds\n" \
"v_mfma_f32_16x16x32_bf16 v[72:75], a[244:247], a[196:199], v[72:75]\n" \
"ds_read_b64_tr_b16 a[226:227], v32 offset:4608\n" \
"v_accvgpr_read_b32 v183, a47\n" \
"v_mfma_f32_16x16x32_bf16 v[68:71], a[176:179], a[132:135], v[68:71]\n" \
"ds_read_b64_tr_b16 a[164:165], v33 offset:0\n" \
"v_cvt_pk_bf16_f32 v204, v204, v205\n" \
"v_mfma_f32_16x16x32_bf16 v[68:71], a[240:243], a[196:199], v[68:71]\n" \
"ds_read_b128 a[132:135], v28 offset:2048\n" \
"v_cvt_pk_bf16_f32 v205, v206, v207\n" \
"v_mfma_f32_16x16x32_bf16 v[84:87], a[176:179], a[136:139], v[84:87]\n" \
"ds_read_b128 a[196:199], v29 offset:2048\n" \
"v_cvt_pk_bf16_f32 v206, v180, v181\n" \
"v_mfma_f32_16x16x32_bf16 v[84:87], a[240:243], a[200:203], v[84:87]\n" \
"s_movk_i32 s84, 0x2080\n" \
"v_mfma_f32_16x16x32_bf16 v[88:91], a[180:183], a[136:139], v[88:91]\n" \
"buffer_load_dwordx4 v25, s[76:79], s84 offen offset:1024 lds\n" \
"v_mfma_f32_16x16x32_bf16 v[88:91], a[244:247], a[200:203], v[88:91]\n" \
"s_movk_i32 s84, 0x4080\n" \
"v_mfma_f32_16x16x32_bf16 v[92:95], a[184:187], a[136:139], v[92:95]\n" \
"buffer_load_dwordx4 v24, s[76:79], s84 offen offset:2048 lds\n" \
"v_mfma_f32_16x16x32_bf16 v[92:95], a[248:251], a[200:203], v[92:95]\n" \
"ds_read_b64_tr_b16 a[166:167], v33 offset:512\n" \
"v_cvt_pk_bf16_f32 v207, v182, v183\n" \
"v_mfma_f32_16x16x32_bf16 v[96:99], a[188:191], a[136:139], v[96:99]\n" \
"ds_read_b64_tr_b16 a[228:229], v33 offset:4096\n" \
"v_accvgpr_read_b32 v208, a56\n" \
"v_mfma_f32_16x16x32_bf16 v[96:99], a[252:255], a[200:203], v[96:99]\n" \
"ds_read_b128 a[136:139], v28 offset:4096\n" \
"v_accvgpr_read_b32 v209, a57\n" \
"v_mfma_f32_16x16x32_bf16 v[112:115], a[188:191], a[140:143], v[112:115]\n" \
"ds_read_b128 a[200:203], v29 offset:4096\n" \
"v_accvgpr_read_b32 v210, a58\n" \
"v_mfma_f32_16x16x32_bf16 v[112:115], a[252:255], a[204:207], v[112:115]\n" \
"s_movk_i32 s84, 0x6080\n" \
"v_mfma_f32_16x16x32_bf16 v[108:111], a[184:187], a[140:143], v[108:111]\n" \
"buffer_load_dwordx4 v25, s[76:79], s84 offen offset:3072 lds\n" \
"v_mfma_f32_16x16x32_bf16 v[108:111], a[248:251], a[204:207], v[108:111]\n" \
"s_add_u32 m0, s31, 0x8000\n" \
"s_mov_b32 s84, 0x80000\n" \
"v_mfma_f32_16x16x32_bf16 v[104:107], a[180:183], a[140:143], v[104:107]\n" \
"buffer_load_dwordx4 v26, s[68:71], s84 offen offset:0 lds\n" \
"v_mfma_f32_16x16x32_bf16 v[104:107], a[244:247], a[204:207], v[104:107]\n" \
"s_addk_i32 s84, 0x7c00\n" \
"v_mfma_f32_16x16x32_bf16 v[100:103], a[176:179], a[140:143], v[100:103]\n" \
"buffer_load_dwordx4 v26, s[68:71], s84 offen offset:1024 lds\n" \
"v_mfma_f32_16x16x32_bf16 v[100:103], a[240:243], a[204:207], v[100:103]\n" \
"ds_read_b128 a[140:143], v28 offset:6144\n" \
"v_accvgpr_read_b32 v211, a59\n" \
"v_mfma_f32_16x16x32_bf16 v[116:119], a[176:179], a[144:147], v[116:119]\n" \
"ds_read_b128 a[204:207], v29 offset:6144\n" \
"v_accvgpr_read_b32 v180, a60\n" \
"v_mfma_f32_16x16x32_bf16 v[116:119], a[240:243], a[208:211], v[116:119]\n" \
"s_addk_i32 s84, 0x7c00\n" \
"v_mfma_f32_16x16x32_bf16 v[120:123], a[180:183], a[144:147], v[120:123]\n" \
"buffer_load_dwordx4 v27, s[68:71], s84 offen offset:2048 lds\n" \
"v_mfma_f32_16x16x32_bf16 v[120:123], a[244:247], a[208:211], v[120:123]\n" \
"ds_read_b64_tr_b16 a[230:231], v33 offset:4608\n" \
"v_accvgpr_read_b32 v181, a61\n" \
"v_mfma_f32_16x16x32_bf16 v[124:127], a[184:187], a[144:147], v[124:127]\n" \
"s_addk_i32 s84, 0x7c00\n" \
"v_mfma_f32_16x16x32_bf16 v[124:127], a[248:251], a[208:211], v[124:127]\n" \
"buffer_load_dwordx4 v27, s[68:71], s84 offen offset:3072 lds\n" \
"v_mfma_f32_16x16x32_bf16 v[128:131], a[188:191], a[144:147], v[128:131]\n" \
"ds_read_b64_tr_b16 a[172:173], v35 offset:0\n" \
"v_accvgpr_read_b32 v182, a62\n" \
"v_mfma_f32_16x16x32_bf16 v[128:131], a[252:255], a[208:211], v[128:131]\n" \
"ds_read_b128 a[144:147], v28 offset:8192\n" \
"v_accvgpr_read_b32 v183, a63\n" \
"v_mfma_f32_16x16x32_bf16 v[144:147], a[188:191], a[148:151], v[144:147]\n" \
"ds_read_b128 a[208:211], v29 offset:8192\n" \
"v_cvt_pk_bf16_f32 v208, v208, v209\n" \
"v_mfma_f32_16x16x32_bf16 v[144:147], a[252:255], a[212:215], v[144:147]\n" \
"s_add_u32 m0, s30, 0x9000\n" \
"s_mov_b32 s84, 0x9080\n" \
"v_mfma_f32_16x16x32_bf16 v[140:143], a[184:187], a[148:151], v[140:143]\n" \
"buffer_load_dwordx4 v24, s[76:79], s84 offen offset:0 lds\n" \
"v_mfma_f32_16x16x32_bf16 v[140:143], a[248:251], a[212:215], v[140:143]\n" \
"ds_read_b64_tr_b16 a[174:175], v35 offset:512\n" \
"v_cvt_pk_bf16_f32 v209, v210, v211\n" \
"v_mfma_f32_16x16x32_bf16 v[136:139], a[180:183], a[148:151], v[136:139]\n" \
"s_addk_i32 s84, 0x2000\n" \
"s_nop 0\n" \
"v_mfma_f32_16x16x32_bf16 v[136:139], a[244:247], a[212:215], v[136:139]\n" \
"buffer_load_dwordx4 v25, s[76:79], s84 offen offset:1024 lds\n" \
"v_mfma_f32_16x16x32_bf16 v[132:135], a[176:179], a[148:151], v[132:135]\n" \
"ds_read_b64_tr_b16 a[236:237], v35 offset:4096\n" \
"v_cvt_pk_bf16_f32 v210, v180, v181\n" \
"v_mfma_f32_16x16x32_bf16 v[132:135], a[240:243], a[212:215], v[132:135]\n" \
"ds_read_b128 a[148:151], v28 offset:10240\n" \
"v_cvt_pk_bf16_f32 v211, v182, v183\n" \
"v_mfma_f32_16x16x32_bf16 v[148:151], a[176:179], a[152:155], v[148:151]\n" \
"ds_read_b128 a[212:215], v29 offset:10240\n" \
"v_accvgpr_read_b32 v212, a48\n" \
"v_mfma_f32_16x16x32_bf16 v[148:151], a[240:243], a[216:219], v[148:151]\n" \
"ds_read_b64_tr_b16 a[238:239], v35 offset:4608\n" \
"v_accvgpr_read_b32 v213, a49\n" \
"v_mfma_f32_16x16x32_bf16 v[152:155], a[180:183], a[152:155], v[152:155]\n" \
"ds_read_b64_tr_b16 a[168:169], v34 offset:0\n" \
"v_accvgpr_read_b32 v214, a50\n" \
"v_mfma_f32_16x16x32_bf16 v[152:155], a[244:247], a[216:219], v[152:155]\n" \
"ds_read_b64_tr_b16 a[170:171], v34 offset:512\n" \
"v_accvgpr_read_b32 v215, a51\n" \
"v_mfma_f32_16x16x32_bf16 v[156:159], a[184:187], a[152:155], v[156:159]\n" \
"ds_read_b64_tr_b16 a[232:233], v34 offset:4096\n" \
"v_accvgpr_read_b32 v180, a52\n" \
"v_mfma_f32_16x16x32_bf16 v[156:159], a[248:251], a[216:219], v[156:159]\n" \
"ds_read_b64_tr_b16 a[234:235], v34 offset:4608\n" \
"v_accvgpr_read_b32 v181, a53\n" \
"v_mfma_f32_16x16x32_bf16 v[160:163], a[188:191], a[152:155], v[160:163]\n" \
"ds_read_b128 v[248:251], v22 offset:0\n" \
"v_accvgpr_read_b32 v182, a54\n" \
"v_mfma_f32_16x16x32_bf16 v[160:163], a[252:255], a[216:219], v[160:163]\n" \
"ds_read_b128 a[152:155], v28 offset:12288\n" \
"v_accvgpr_read_b32 v183, a55\n" \
"v_mfma_f32_16x16x32_bf16 v[176:179], a[188:191], a[156:159], v[176:179]\n" \
"ds_read_b128 a[216:219], v29 offset:12288\n" \
"v_cvt_pk_bf16_f32 v212, v212, v213\n" \
"v_mfma_f32_16x16x32_bf16 v[176:179], a[252:255], a[220:223], v[176:179]\n" \
"ds_read_b128 v[48:51], v23 offset:0\n" \
"v_cvt_pk_bf16_f32 v213, v214, v215\n" \
"v_mfma_f32_16x16x32_bf16 v[172:175], a[184:187], a[156:159], v[172:175]\n" \
"v_cvt_pk_bf16_f32 v214, v180, v181\n" \
"v_cvt_pk_bf16_f32 v215, v182, v183\n" \
"v_mfma_f32_16x16x32_bf16 v[172:175], a[248:251], a[220:223], v[172:175]\n" \
"v_accvgpr_read_b32 v216, a64\n" \
"v_accvgpr_read_b32 v217, a65\n" \
"v_mfma_f32_16x16x32_bf16 v[168:171], a[180:183], a[156:159], v[168:171]\n" \
"v_accvgpr_read_b32 v218, a66\n" \
"v_accvgpr_read_b32 v219, a67\n" \
"v_mfma_f32_16x16x32_bf16 v[168:171], a[244:247], a[220:223], v[168:171]\n" \
"v_accvgpr_read_b32 v180, a68\n" \
"v_accvgpr_read_b32 v181, a69\n" \
"v_mfma_f32_16x16x32_bf16 v[164:167], a[176:179], a[156:159], v[164:167]\n" \
"v_accvgpr_read_b32 v182, a70\n" \
"v_accvgpr_read_b32 v183, a71\n" \
"v_mfma_f32_16x16x32_bf16 v[164:167], a[240:243], a[220:223], v[164:167]\n" \
"ds_read_b128 a[156:159], v28 offset:14336\n" \
"ds_read_b128 a[220:223], v29 offset:14336\n" \
"s_waitcnt lgkmcnt(15)\n" \
"v_mfma_f32_16x16x32_bf16 a[0:3], a[160:163], a[128:131], 0\n" \
"s_addk_i32 s84, 0x2000\n" \
"v_cvt_pk_bf16_f32 v216, v216, v217\n" \
"v_mfma_f32_16x16x32_bf16 a[0:3], a[224:227], a[192:195], a[0:3]\n" \
"buffer_load_dwordx4 v24, s[76:79], s84 offen offset:2048 lds\n" \
"v_cvt_pk_bf16_f32 v217, v218, v219\n" \
"v_mfma_f32_16x16x32_bf16 a[4:7], a[164:167], a[128:131], 0\n" \
"ds_read_b64_tr_b16 a[176:177], v36 offset:0\n" \
"v_cvt_pk_bf16_f32 v218, v180, v181\n" \
"v_mfma_f32_16x16x32_bf16 a[4:7], a[228:231], a[192:195], a[4:7]\n" \
"s_addk_i32 s84, 0x2000\n" \
"v_cvt_pk_bf16_f32 v219, v182, v183\n" \
"s_waitcnt lgkmcnt(15)\n" \
"v_mfma_f32_16x16x32_bf16 a[12:15], a[172:175], a[128:131], 0\n" \
"buffer_load_dwordx4 v25, s[76:79], s84 offen offset:3072 lds\n" \
"v_accvgpr_read_b32 v220, a80\n" \
"s_waitcnt lgkmcnt(11)\n" \
"v_mfma_f32_16x16x32_bf16 a[12:15], a[236:239], a[192:195], a[12:15]\n" \
"ds_read_b64_tr_b16 a[178:179], v36 offset:1024\n" \
"v_accvgpr_read_b32 v221, a81\n" \
"s_waitcnt lgkmcnt(10)\n" \
"v_mfma_f32_16x16x32_bf16 a[8:11], a[128:131], a[168:171], 0\n" \
"ds_read_b64_tr_b16 a[240:241], v36 offset:8192\n" \
"v_accvgpr_read_b32 v222, a82\n" \
"s_waitcnt lgkmcnt(9)\n" \
"s_nop 0\n" \
"v_mfma_f32_16x16x32_bf16 a[8:11], a[192:195], a[232:235], a[8:11]\n" \
"ds_read_b64_tr_b16 a[242:243], v36 offset:9216\n" \
"v_accvgpr_read_b32 v223, a83\n" \
"v_mfma_f32_16x16x32_bf16 a[24:27], a[132:135], a[168:171], 0\n" \
"ds_read_b64_tr_b16 a[180:181], v37 offset:0\n" \
"v_accvgpr_read_b32 v180, a84\n" \
"v_mfma_f32_16x16x32_bf16 a[24:27], a[196:199], a[232:235], a[24:27]\n" \
"ds_read_b64_tr_b16 a[182:183], v37 offset:1024\n" \
"v_accvgpr_read_b32 v181, a85\n" \
"v_mfma_f32_16x16x32_bf16 a[28:31], a[172:175], a[132:135], 0\n" \
"ds_read_b64_tr_b16 a[244:245], v37 offset:8192\n" \
"v_accvgpr_read_b32 v182, a86\n" \
"v_mfma_f32_16x16x32_bf16 a[28:31], a[236:239], a[196:199], a[28:31]\n" \
"s_add_u32 s89, s41, 0x2000\n" \
"buffer_store_dwordx4 v[200:203], v20, s[80:83], s89 offen offset:0 sc1\n" \
"v_mfma_f32_16x16x32_bf16 a[20:23], a[164:167], a[132:135], 0\n" \
"ds_read_b64_tr_b16 a[246:247], v37 offset:9216\n" \
"v_accvgpr_read_b32 v183, a87\n" \
"v_mfma_f32_16x16x32_bf16 a[20:23], a[228:231], a[196:199], a[20:23]\n" \
"ds_read_b64_tr_b16 a[184:185], v38 offset:0\n" \
"v_cvt_pk_bf16_f32 v220, v220, v221\n" \
"v_mfma_f32_16x16x32_bf16 a[16:19], a[160:163], a[132:135], 0\n" \
"ds_read_b64_tr_b16 a[186:187], v38 offset:1024\n" \
"v_cvt_pk_bf16_f32 v221, v222, v223\n" \
"v_mfma_f32_16x16x32_bf16 a[16:19], a[224:227], a[196:199], a[16:19]\n" \
"v_cvt_pk_bf16_f32 v222, v180, v181\n" \
"v_cvt_pk_bf16_f32 v223, v182, v183\n" \
"v_cvt_pk_bf16_f32 v224, v52, v53\n" \
".fill (L_pb + 20192 - .) / 4, 4, 0xBF800000\n" \
"L_pp5:\n" \
"s_cbranch_execz L_pret\n" \
"s_nop 0\n" \
"v_mfma_f32_16x16x32_bf16 a[32:35], a[160:163], a[136:139], 0\n" \
"s_add_u32 s88, s41, 0x2000\n" \
"buffer_store_dwordx4 v[204:207], v20, s[80:83], s88 offen offset:1024 sc1\n" \
"v_mfma_f32_16x16x32_bf16 a[32:35], a[224:227], a[200:203], a[32:35]\n" \
"ds_read_b64_tr_b16 a[248:249], v38 offset:8192\n" \
"v_cvt_pk_bf16_f32 v225, v54, v55\n" \
"v_mfma_f32_16x16x32_bf16 a[36:39], a[164:167], a[136:139], 0\n" \
"ds_read_b64_tr_b16 a[250:251], v38 offset:9216\n" \
"v_cvt_pk_bf16_f32 v226, v56, v57\n" \
"v_mfma_f32_16x16x32_bf16 a[36:39], a[228:231], a[200:203], a[36:39]\n" \
"ds_read_b64_tr_b16 a[188:189], v39 offset:0\n" \
"v_cvt_pk_bf16_f32 v227, v58, v59\n" \
"v_mfma_f32_16x16x32_bf16 a[44:47], a[172:175], a[136:139], 0\n" \
"v_cvt_pk_bf16_f32 v228, v60, v61\n" \
"v_cvt_pk_bf16_f32 v229, v62, v63\n" \
"v_cvt_pk_bf16_f32 v230, v64, v65\n" \
"v_mfma_f32_16x16x32_bf16 a[44:47], a[236:239], a[200:203], a[44:47]\n" \
"ds_read_b64_tr_b16 a[190:191], v39 offset:1024\n" \
"v_cvt_pk_bf16_f32 v231, v66, v67\n" \
"v_mfma_f32_16x16x32_bf16 a[40:43], a[136:139], a[168:171], 0\n" \
"s_add_u32 s89, s41, 0x3000\n" \
"buffer_store_dwordx4 v[208:211], v20, s[80:83], s89 offen offset:1024 sc1\n" \
"v_mfma_f32_16x16x32_bf16 a[40:43], a[200:203], a[232:235], a[40:43]\n" \
"ds_read_b64_tr_b16 a[252:253], v39 offset:8192\n" \
"v_cvt_pk_bf16_f32 v232, v76, v77\n" \
"v_mfma_f32_16x16x32_bf16 a[56:59], a[140:143], a[168:171], 0\n" \
"ds_read_b64_tr_b16 a[254:255], v39 offset:9216\n" \
"v_cvt_pk_bf16_f32 v233, v78, v79\n" \
"v_mfma_f32_16x16x32_bf16 a[56:59], a[204:207], a[232:235], a[56:59]\n" \
"v_cvt_pk_bf16_f32 v234, v80, v81\n" \
"v_cvt_pk_bf16_f32 v235, v82, v83\n" \
"v_cvt_pk_bf16_f32 v236, v68, v69\n" \
"v_mfma_f32_16x16x32_bf16 a[60:63], a[172:175], a[140:143], 0\n" \
"v_cvt_pk_bf16_f32 v237, v70, v71\n" \
"v_cvt_pk_bf16_f32 v238, v72, v73\n" \
"v_cvt_pk_bf16_f32 v239, v74, v75\n" \
"v_mfma_f32_16x16x32_bf16 a[60:63], a[236:239], a[204:207], a[60:63]\n" \
"s_add_u32 s88, s41, 0x3000\n" \
"buffer_store_dwordx4 v[212:215], v20, s[80:83], s88 offen offset:0 sc1\n" \
"v_mfma_f32_16x16x32_bf16 a[52:55], a[164:167], a[140:143], 0\n" \
"v_cvt_pk_bf16_f32 v240, v84, v85\n" \
"v_cvt_pk_bf16_f32 v241, v86, v87\n" \
"v_cvt_pk_bf16_f32 v242, v88, v89\n" \
"v_mfma_f32_16x16x32_bf16 a[52:55], a[228:231], a[204:207], a[52:55]\n" \
"v_cvt_pk_bf16_f32 v243, v90, v91\n" \
"v_cvt_pk_bf16_f32 v244, v92, v93\n" \
"v_cvt_pk_bf16_f32 v245, v94, v95\n" \
"v_mfma_f32_16x16x32_bf16 a[48:51], a[160:163], a[140:143], 0\n" \
"v_cvt_pk_bf16_f32 v246, v96, v97\n" \
"v_cvt_pk_bf16_f32 v247, v98, v99\n" \
"v_cvt_pk_bf16_f32 v184, v108, v109\n" \
"v_mfma_f32_16x16x32_bf16 a[48:51], a[224:227], a[204:207], a[48:51]\n" \
"v_cvt_pk_bf16_f32 v185, v110, v111\n" \
"v_cvt_pk_bf16_f32 v186, v112, v113\n" \
"v_cvt_pk_bf16_f32 v187, v114, v115\n" \
"s_waitcnt lgkmcnt(15)\n" \
"s_nop 0\n" \
"v_mfma_f32_16x16x32_bf16 a[64:67], a[164:167], v[248:251], 0\n" \
"v_cvt_pk_bf16_f32 v188, v100, v101\n" \
"v_cvt_pk_bf16_f32 v189, v102, v103\n" \
"v_cvt_pk_bf16_f32 v190, v104, v105\n" \
"v_mfma_f32_16x16x32_bf16 a[64:67], a[228:231], v[48:51], a[64:67]\n" \
"v_cvt_pk_bf16_f32 v191, v106, v107\n" \
"v_cvt_pk_bf16_f32 v192, v116, v117\n" \
"v_cvt_pk_bf16_f32 v193, v118, v119\n" \
"v_mfma_f32_16x16x32_bf16 a[80:83], a[172:175], v[248:251], 0\n" \
"v_cvt_pk_bf16_f32 v194, v120, v121\n" \
"v_cvt_pk_bf16_f32 v195, v122, v123\n" \
"v_cvt_pk_bf16_f32 v196, v124, v125\n" \
"v_mfma_f32_16x16x32_bf16 a[80:83], a[236:239], v[48:51], a[80:83]\n" \
"v_cvt_pk_bf16_f32 v197, v126, v127\n" \
"v_cvt_pk_bf16_f32 v198, v128, v129\n" \
"v_cvt_pk_bf16_f32 v199, v130, v131\n" \
"v_mfma_f32_16x16x32_bf16 a[68:71], v[248:251], a[168:171], 0\n" \
"v_cvt_pk_bf16_f32 v252, v140, v141\n" \
"v_cvt_pk_bf16_f32 v253, v142, v143\n" \
"v_cvt_pk_bf16_f32 v254, v144, v145\n" \
"v_mfma_f32_16x16x32_bf16 a[68:71], v[48:51], a[232:235], a[68:71]\n" \
"v_cvt_pk_bf16_f32 v255, v146, v147\n" \
"v_accvgpr_write_b32 a84, v252\n" \
"v_accvgpr_write_b32 a85, v253\n" \
"s_waitcnt vmcnt(4) lgkmcnt(0)\n" \
"s_barrier\n" \
"v_mfma_f32_16x16x32_bf16 v[52:55], a[176:179], a[128:131], 0\n" \
"s_add_u32 m0, s40, 0x0\n" \
"s_nop 0\n" \
"s_add_u32 s85, s39, 0x4000\n" \
"v_accvgpr_write_b32 a86, v254\n" \
"v_mfma_f32_16x16x32_bf16 v[52:55], a[240:243], a[192:195], v[52:55]\n" \
"buffer_load_dwordx4 v20, s[80:83], s85 offen sc1 lds\n" \
"v_accvgpr_write_b32 a87, v255\n" \
"v_mfma_f32_16x16x32_bf16 v[56:59], a[180:183], a[128:131], 0\n" \
"ds_read_b64_tr_b16 a[160:161], v32 offset:32768\n" \
"v_cvt_pk_bf16_f32 v252, v132, v133\n" \
"v_mfma_f32_16x16x32_bf16 v[56:59], a[244:247], a[192:195], v[56:59]\n" \
"ds_read_b64_tr_b16 a[162:163], v32 offset:33280\n" \
"v_cvt_pk_bf16_f32 v253, v134, v135\n" \
"v_mfma_f32_16x16x32_bf16 v[60:63], a[184:187], a[128:131], 0\n" \
"s_add_u32 m0, s40, 0x1000\n" \
"s_add_u32 s85, s39, 0x5000\n" \
"v_cvt_pk_bf16_f32 v254, v136, v137\n" \
"v_mfma_f32_16x16x32_bf16 v[60:63], a[248:251], a[192:195], v[60:63]\n" \
"buffer_load_dwordx4 v20, s[80:83], s85 offen sc1 lds\n" \
"v_cvt_pk_bf16_f32 v255, v138, v139\n" \
"v_mfma_f32_16x16x32_bf16 v[64:67], a[188:191], a[128:131], 0\n" \
"ds_read_b64_tr_b16 a[224:225], v32 offset:36864\n" \
"v_cvt_pk_bf16_f32 v200, v148, v149\n" \
"v_mfma_f32_16x16x32_bf16 v[64:67], a[252:255], a[192:195], v[64:67]\n" \
"ds_read_b128 a[128:131], v28 offset:32768\n" \
"v_cvt_pk_bf16_f32 v201, v150, v151\n" \
"v_mfma_f32_16x16x32_bf16 v[80:83], a[188:191], a[132:135], 0\n" \
"ds_read_b128 a[192:195], v29 offset:32768\n" \
"v_cvt_pk_bf16_f32 v202, v152, v153\n" \
"v_mfma_f32_16x16x32_bf16 v[80:83], a[252:255], a[196:199], v[80:83]\n" \
"s_add_u32 m0, s40, 0x3000\n" \
"s_movk_i32 s84, 0x100\n" \
"s_nop 0\n" \
"v_cvt_pk_bf16_f32 v203, v154, v155\n" \
"v_mfma_f32_16x16x32_bf16 v[76:79], a[184:187], a[132:135], 0\n" \
"buffer_load_dwordx4 v21, s[76:79], s84 offen offset:0 lds\n" \
"v_cvt_pk_bf16_f32 v204, v156, v157\n" \
"v_mfma_f32_16x16x32_bf16 v[76:79], a[248:251], a[196:199], v[76:79]\n" \
"ds_read_b64_tr_b16 a[226:227], v32 offset:37376\n" \
"v_cvt_pk_bf16_f32 v205, v158, v159\n" \
"v_mfma_f32_16x16x32_bf16 v[72:75], a[180:183], a[132:135], 0\n" \
"ds_read_b64_tr_b16 a[164:165], v33 offset:32768\n" \
"v_cvt_pk_bf16_f32 v206, v160, v161\n" \
"v_mfma_f32_16x16x32_bf16 v[72:75], a[244:247], a[196:199], v[72:75]\n" \
"v_cvt_pk_bf16_f32 v207, v162, v163\n" \
"v_cvt_pk_bf16_f32 v208, v172, v173\n" \
"v_cvt_pk_bf16_f32 v209, v174, v175\n" \
"v_mfma_f32_16x16x32_bf16 v[68:71], a[176:179], a[132:135], 0\n" \
"ds_read_b64_tr_b16 a[166:167], v33 offset:33280\n" \
"v_cvt_pk_bf16_f32 v210, v176, v177\n" \
"v_mfma_f32_16x16x32_bf16 v[68:71], a[240:243], a[196:199], v[68:71]\n" \
"ds_read_b128 a[132:135], v28 offset:34816\n" \
"v_cvt_pk_bf16_f32 v211, v178, v179\n" \
"v_mfma_f32_16x16x32_bf16 v[84:87], a[176:179], a[136:139], 0\n" \
"ds_read_b128 a[196:199], v29 offset:34816\n" \
"v_cvt_pk_bf16_f32 v212, v164, v165\n" \
"v_mfma_f32_16x16x32_bf16 v[84:87], a[240:243], a[200:203], v[84:87]\n" \
"s_add_u32 m0, s31, 0x0\n" \
"s_mov_b32 s84, 0x100000\n" \
"v_cvt_pk_bf16_f32 v213, v166, v167\n" \
"v_mfma_f32_16x16x32_bf16 v[88:91], a[180:183], a[136:139], 0\n" \
"buffer_load_dwordx4 v26, s[68:71], s84 offen offset:0 lds\n" \
"v_cvt_pk_bf16_f32 v214, v168, v169\n" \
"v_mfma_f32_16x16x32_bf16 v[88:91], a[244:247], a[200:203], v[88:91]\n" \
"s_add_u32 s89, s41, 0x0\n" \
"buffer_store_dwordx4 v[224:227], v20, s[80:83], s89 offen offset:2048 sc1\n" \
"v_mfma_f32_16x16x32_bf16 v[92:95], a[184:187], a[136:139], 0\n" \
"ds_read_b64_tr_b16 a[228:229], v33 offset:36864\n" \
"v_cvt_pk_bf16_f32 v215, v170, v171\n" \
"v_mfma_f32_16x16x32_bf16 v[92:95], a[248:251], a[200:203], v[92:95]\n" \
"ds_read_b64_tr_b16 a[230:231], v33 offset:37376\n" \
"v_mfma_f32_16x16x32_bf16 v[96:99], a[188:191], a[136:139], 0\n" \
"v_mfma_f32_16x16x32_bf16 v[96:99], a[252:255], a[200:203], v[96:99]\n" \
"ds_read_b128 a[136:139], v28 offset:36864\n" \
"v_mfma_f32_16x16x32_bf16 v[112:115], a[188:191], a[140:143], 0\n" \
"ds_read_b128 a[200:203], v29 offset:36864\n" \
"v_mfma_f32_16x16x32_bf16 v[112:115], a[252:255], a[204:207], v[112:115]\n" \
"s_addk_i32 s84, 0x7c00\n" \
"v_mfma_f32_16x16x32_bf16 v[108:111], a[184:187], a[140:143], 0\n" \
"buffer_load_dwordx4 v26, s[68:71], s84 offen offset:1024 lds\n" \
"v_mfma_f32_16x16x32_bf16 v[108:111], a[248:251], a[204:207], v[108:111]\n" \
"ds_read_b64_tr_b16 a[172:173], v35 offset:32768\n" \
"v_mfma_f32_16x16x32_bf16 v[104:107], a[180:183], a[140:143], 0\n" \
"s_addk_i32 s84, 0x7c00\n" \
"v_mfma_f32_16x16x32_bf16 v[104:107], a[244:247], a[204:207], v[104:107]\n" \
"buffer_load_dwordx4 v27, s[68:71], s84 offen offset:2048 lds\n" \
"v_mfma_f32_16x16x32_bf16 v[100:103], a[176:179], a[140:143], 0\n" \
"ds_read_b64_tr_b16 a[174:175], v35 offset:33280\n" \
"v_mfma_f32_16x16x32_bf16 v[100:103], a[240:243], a[204:207], v[100:103]\n" \
"ds_read_b128 a[140:143], v28 offset:38912\n" \
"v_mfma_f32_16x16x32_bf16 v[116:119], a[176:179], a[144:147], 0\n" \
"ds_read_b128 a[204:207], v29 offset:38912\n" \
"v_mfma_f32_16x16x32_bf16 v[116:119], a[240:243], a[208:211], v[116:119]\n" \
"s_addk_i32 s84, 0x7c00\n" \
"v_mfma_f32_16x16x32_bf16 v[120:123], a[180:183], a[144:147], 0\n" \
"buffer_load_dwordx4 v27, s[68:71], s84 offen offset:3072 lds\n" \
"v_mfma_f32_16x16x32_bf16 v[120:123], a[244:247], a[208:211], v[120:123]\n" \
"ds_read_b64_tr_b16 a[236:237], v35 offset:36864\n" \
"v_mfma_f32_16x16x32_bf16 v[124:127], a[184:187], a[144:147], 0\n" \
"ds_read_b64_tr_b16 a[238:239], v35 offset:37376\n" \
"v_mfma_f32_16x16x32_bf16 v[124:127], a[248:251], a[208:211], v[124:127]\n" \
"s_add_u32 m0, s30, 0x0\n" \
"s_movk_i32 s84, 0x180\n" \
"v_mfma_f32_16x16x32_bf16 v[128:131], a[188:191], a[144:147], 0\n" \
"buffer_load_dwordx4 v24, s[76:79], s84 offen offset:0 lds\n" \
"v_mfma_f32_16x16x32_bf16 v[128:131], a[252:255], a[208:211], v[128:131]\n" \
"ds_read_b128 a[144:147], v28 offset:40960\n" \
"v_mfma_f32_16x16x32_bf16 v[144:147], a[188:191], a[148:151], 0\n" \
"ds_read_b128 a[208:211], v29 offset:40960\n" \
"v_mfma_f32_16x16x32_bf16 v[144:147], a[252:255], a[212:215], v[144:147]\n" \
"ds_read_b64_tr_b16 a[168:169], v34 offset:32768\n" \
"v_mfma_f32_16x16x32_bf16 v[140:143], a[184:187], a[148:151], 0\n" \
"s_movk_i32 s84, 0x2180\n" \
"v_mfma_f32_16x16x32_bf16 v[140:143], a[248:251], a[212:215], v[140:143]\n" \
"buffer_load_dwordx4 v25, s[76:79], s84 offen offset:1024 lds\n" \
"v_mfma_f32_16x16x32_bf16 v[136:139], a[180:183], a[148:151], 0\n" \
"ds_read_b64_tr_b16 a[170:171], v34 offset:33280\n" \
"v_mfma_f32_16x16x32_bf16 v[136:139], a[244:247], a[212:215], v[136:139]\n" \
"ds_read_b64_tr_b16 a[232:233], v34 offset:36864\n" \
"v_mfma_f32_16x16x32_bf16 v[132:135], a[176:179], a[148:151], 0\n" \
"ds_read_b64_tr_b16 a[234:235], v34 offset:37376\n" \
"v_mfma_f32_16x16x32_bf16 v[132:135], a[240:243], a[212:215], v[132:135]\n" \
"ds_read_b128 a[148:151], v28 offset:43008\n" \
"v_mfma_f32_16x16x32_bf16 v[148:151], a[176:179], a[152:155], 0\n" \
"ds_read_b128 a[212:215], v29 offset:43008\n" \
"v_mfma_f32_16x16x32_bf16 v[148:151], a[240:243], a[216:219], v[148:151]\n" \
"s_movk_i32 s84, 0x4180\n" \
"v_mfma_f32_16x16x32_bf16 v[152:155], a[180:183], a[152:155], 0\n" \
"buffer_load_dwordx4 v24, s[76:79], s84 offen offset:2048 lds\n" \
"v_mfma_f32_16x16x32_bf16 v[152:155], a[244:247], a[216:219], v[152:155]\n" \
"s_movk_i32 s84, 0x6180\n" \
"v_mfma_f32_16x16x32_bf16 v[156:159], a[184:187], a[152:155], 0\n" \
"buffer_load_dwordx4 v25, s[76:79], s84 offen offset:3072 lds\n" \
"v_mfma_f32_16x16x32_bf16 v[156:159], a[248:251], a[216:219], v[156:159]\n" \
"ds_read_b128 v[248:251], v22 offset:32768\n" \
"v_mfma_f32_16x16x32_bf16 v[160:163], a[188:191], a[152:155], 0\n" \
"ds_read_b128 v[48:51], v23 offset:32768\n" \
"v_mfma_f32_16x16x32_bf16 v[160:163], a[252:255], a[216:219], v[160:163]\n" \
"ds_read_b128 a[152:155], v28 offset:45056\n" \
"v_mfma_f32_16x16x32_bf16 v[176:179], a[188:191], a[156:159], 0\n" \
"ds_read_b128 a[216:219], v29 offset:45056\n" \
"v_mfma_f32_16x16x32_bf16 v[176:179], a[252:255], a[220:223], v[176:179]\n" \
"v_mfma_f32_16x16x32_bf16 v[172:175], a[184:187], a[156:159], 0\n" \
"v_mfma_f32_16x16x32_bf16 v[172:175], a[248:251], a[220:223], v[172:175]\n" \
"v_mfma_f32_16x16x32_bf16 v[168:171], a[180:183], a[156:159], 0\n" \
"v_mfma_f32_16x16x32_bf16 v[168:171], a[244:247], a[220:223], v[168:171]\n" \
"v_mfma_f32_16x16x32_bf16 v[164:167], a[176:179], a[156:159], 0\n" \
"v_mfma_f32_16x16x32_bf16 v[164:167], a[240:243], a[220:223], v[164:167]\n" \
"ds_read_b128 a[156:159], v28 offset:47104\n" \
"ds_read_b128 a[220:223], v29 offset:47104\n" \
"s_waitcnt lgkmcnt(15)\n" \
"v_mfma_f32_16x16x32_bf16 a[0:3], a[160:163], a[128:131], a[0:3]\n" \
"s_add_u32 m0, s30, 0x1000\n" \
"s_addk_i32 s84, 0x3000\n" \
"v_mfma_f32_16x16x32_bf16 a[0:3], a[224:227], a[192:195], a[0:3]\n" \
"buffer_load_dwordx4 v24, s[76:79], s84 offen offset:0 lds\n" \
"v_mfma_f32_16x16x32_bf16 a[4:7], a[164:167], a[128:131], a[4:7]\n" \
"ds_read_b64_tr_b16 a[176:177], v36 offset:32768\n" \
"v_mov_b32_e64 v11, 0\n" \
"v_mfma_f32_16x16x32_bf16 a[4:7], a[228:231], a[192:195], a[4:7]\n" \
"ds_read_b64_tr_b16 a[178:179], v36 offset:33792\n" \
"s_and_b32 s6, s2, 7\n" \
"v_mfma_f32_16x16x32_bf16 a[12:15], a[172:175], a[128:131], a[12:15]\n" \
"ds_read_b64_tr_b16 a[240:241], v36 offset:40960\n" \
"s_lshl_b32 s7, s3, 1\n" \
"s_waitcnt lgkmcnt(15)\n" \
"v_mfma_f32_16x16x32_bf16 a[12:15], a[236:239], a[192:195], a[12:15]\n" \
"s_add_u32 s88, s41, 0x0\n" \
"buffer_store_dwordx4 v[228:231], v20, s[80:83], s88 offen offset:3072 sc1\n" \
"s_waitcnt lgkmcnt(13)\n" \
"v_mfma_f32_16x16x32_bf16 a[8:11], a[128:131], a[168:171], a[8:11]\n" \
"ds_read_b64_tr_b16 a[242:243], v36 offset:41984\n" \
"s_lshr_b32 s4, s2, 5\n" \
"s_waitcnt lgkmcnt(12)\n" \
"v_mfma_f32_16x16x32_bf16 a[8:11], a[192:195], a[232:235], a[8:11]\n" \
"ds_read_b64_tr_b16 a[180:181], v37 offset:32768\n" \
"s_lshl_b32 s5, s6, 2\n" \
"v_mfma_f32_16x16x32_bf16 a[24:27], a[132:135], a[168:171], a[24:27]\n" \
"ds_read_b64_tr_b16 a[182:183], v37 offset:33792\n" \
"s_bfe_u32 s8, s2, 0x20003\n" \
"v_mfma_f32_16x16x32_bf16 a[24:27], a[196:199], a[232:235], a[24:27]\n" \
"ds_read_b64_tr_b16 a[244:245], v37 offset:40960\n" \
"s_and_b32 s7, s7, 4\n" \
"v_mfma_f32_16x16x32_bf16 a[28:31], a[172:175], a[132:135], a[28:31]\n" \
"s_and_b32 s9, s3, 1\n" \
"s_or_b32 s13, s5, s8\n" \
"v_mfma_f32_16x16x32_bf16 a[28:31], a[236:239], a[196:199], a[28:31]\n" \
"s_addk_i32 s84, 0x2000\n" \
"v_mfma_f32_16x16x32_bf16 a[20:23], a[164:167], a[132:135], a[20:23]\n" \
"buffer_load_dwordx4 v25, s[76:79], s84 offen offset:1024 lds\n" \
"v_mfma_f32_16x16x32_bf16 a[20:23], a[228:231], a[196:199], a[20:23]\n" \
"ds_read_b64_tr_b16 a[246:247], v37 offset:41984\n" \
"s_or_b32 s9, s7, s9\n" \
"v_mfma_f32_16x16x32_bf16 a[16:19], a[160:163], a[132:135], a[16:19]\n" \
"ds_read_b64_tr_b16 a[184:185], v38 offset:32768\n" \
"s_mul_i32 s7, s4, 0xa00\n" \
"v_mfma_f32_16x16x32_bf16 a[16:19], a[224:227], a[196:199], a[16:19]\n" \
"ds_read_b64_tr_b16 a[186:187], v38 offset:33792\n" \
"s_add_i32 s7, s7, 0x5bc1000\n" \
"v_mfma_f32_16x16x32_bf16 a[32:35], a[160:163], a[136:139], a[32:35]\n" \
"s_addk_i32 s84, 0x2000\n" \
"v_mfma_f32_16x16x32_bf16 a[32:35], a[224:227], a[200:203], a[32:35]\n" \
"buffer_load_dwordx4 v24, s[76:79], s84 offen offset:2048 lds\n" \
"v_mfma_f32_16x16x32_bf16 a[36:39], a[164:167], a[136:139], a[36:39]\n" \
"ds_read_b64_tr_b16 a[248:249], v38 offset:40960\n" \
"s_lshl_b32 s10, s13, 2\n" \
"v_mfma_f32_16x16x32_bf16 a[36:39], a[228:231], a[200:203], a[36:39]\n" \
"ds_read_b64_tr_b16 a[250:251], v38 offset:41984\n" \
"s_or_b32 s10, s10, s7\n" \
"v_mfma_f32_16x16x32_bf16 a[44:47], a[172:175], a[136:139], a[44:47]\n" \
"ds_read_b64_tr_b16 a[188:189], v39 offset:32768\n" \
"v_writelane_b32 v11, s10, 0\n" \
"v_mfma_f32_16x16x32_bf16 a[44:47], a[236:239], a[200:203], a[44:47]\n" \
"ds_read_b64_tr_b16 a[190:191], v39 offset:33792\n" \
"s_lshl_b32 s14, s4, 3\n" \
"v_mfma_f32_16x16x32_bf16 a[40:43], a[136:139], a[168:171], a[40:43]\n" \
"s_add_u32 s89, s41, 0x1000\n" \
"buffer_store_dwordx4 v[232:235], v20, s[80:83], s89 offen offset:3072 sc1\n" \
"v_mfma_f32_16x16x32_bf16 a[40:43], a[200:203], a[232:235], a[40:43]\n" \
"ds_read_b64_tr_b16 a[252:253], v39 offset:40960\n" \
"v_writelane_b32 v11, s7, 1\n" \
"v_mfma_f32_16x16x32_bf16 a[56:59], a[140:143], a[168:171], a[56:59]\n" \
"ds_read_b64_tr_b16 a[254:255], v39 offset:41984\n" \
"s_or_b32 s7, s14, s6\n" \
"v_mfma_f32_16x16x32_bf16 a[56:59], a[204:207], a[232:235], a[56:59]\n" \
"s_lshl_b32 s7, s7, 10\n" \
"s_lshl_b32 s10, s3, 8\n" \
"v_mfma_f32_16x16x32_bf16 a[60:63], a[172:175], a[140:143], a[60:63]\n" \
"s_or_b32 s7, s7, s10\n" \
"s_or_b32 s10, s7, 0x5c00200\n" \
"v_mfma_f32_16x16x32_bf16 a[60:63], a[236:239], a[204:207], a[60:63]\n" \
"s_addk_i32 s84, 0x2000\n" \
"v_mfma_f32_16x16x32_bf16 a[52:55], a[164:167], a[140:143], a[52:55]\n" \
"buffer_load_dwordx4 v25, s[76:79], s84 offen offset:3072 lds\n" \
"v_mfma_f32_16x16x32_bf16 a[52:55], a[228:231], a[204:207], a[52:55]\n" \
"v_writelane_b32 v11, s10, 2\n" \
"s_lshl_b32 s10, s8, 2\n" \
"v_mfma_f32_16x16x32_bf16 a[48:51], a[160:163], a[140:143], a[48:51]\n" \
"s_or_b32 s11, s10, s7\n" \
"s_or_b32 s11, s11, 0x5c00000\n" \
"v_mfma_f32_16x16x32_bf16 a[48:51], a[224:227], a[204:207], a[48:51]\n" \
"v_writelane_b32 v11, s11, 3\n" \
"s_or_b32 s7, s7, 0x5c00080\n" \
"s_waitcnt lgkmcnt(15)\n" \
"v_mfma_f32_16x16x32_bf16 a[64:67], a[164:167], v[248:251], a[64:67]\n" \
"v_writelane_b32 v11, s7, 4\n" \
"s_or_b32 s7, s7, s10\n" \
"v_mfma_f32_16x16x32_bf16 a[64:67], a[228:231], v[48:51], a[64:67]\n" \
"s_add_i32 s10, s3, s2\n" \
"v_writelane_b32 v11, s7, 5\n" \
"v_mfma_f32_16x16x32_bf16 a[80:83], a[172:175], v[248:251], a[80:83]\n" \
"s_mov_b32 s7, 2.0\n" \
"s_mov_b32 s11, 0x5bc0000\n" \
"v_mfma_f32_16x16x32_bf16 a[80:83], a[236:239], v[48:51], a[80:83]\n" \
"s_cmp_lg_u32 s10, 0\n" \
"s_cselect_b32 s10, s7, s11\n" \
"v_mfma_f32_16x16x32_bf16 a[68:71], v[248:251], a[168:171], a[68:71]\n" \
"s_lshr_b32 s15, s2, 3\n" \
"v_writelane_b32 v11, s10, 54\n" \
"v_mfma_f32_16x16x32_bf16 a[68:71], v[48:51], a[232:235], a[68:71]\n" \
"s_waitcnt vmcnt(10) lgkmcnt(0)\n" \
"s_barrier\n" \
"v_xor_b32_e32 v28, 0x20000, v28\n" \
"v_xor_b32_e32 v29, 0x20000, v29\n" \
"v_mfma_f32_16x16x32_bf16 v[52:55], a[176:179], a[128:131], v[52:55]\n" \
"s_add_u32 m0, s40, 0x8000\n" \
"s_add_u32 s85, s39, 0x6000\n" \
"v_mfma_f32_16x16x32_bf16 v[52:55], a[240:243], a[192:195], v[52:55]\n" \
"buffer_load_dwordx4 v20, s[80:83], s85 offen sc1 lds\n" \
"v_mfma_f32_16x16x32_bf16 v[56:59], a[180:183], a[128:131], v[56:59]\n" \
"ds_read_b64_tr_b16 a[160:161], v32 offset:0\n" \
"s_lshl_b32 s10, s6, 5\n" \
"v_mfma_f32_16x16x32_bf16 v[56:59], a[244:247], a[192:195], v[56:59]\n" \
"ds_read_b64_tr_b16 a[162:163], v32 offset:512\n" \
"s_min_u32 s16, s3, 2\n" \
"s_nop 0\n" \
"v_mfma_f32_16x16x32_bf16 v[60:63], a[184:187], a[128:131], v[60:63]\n" \
"s_add_u32 m0, s40, 0x9000\n" \
"s_add_u32 s85, s39, 0x7000\n" \
"v_mfma_f32_16x16x32_bf16 v[60:63], a[248:251], a[192:195], v[60:63]\n" \
"buffer_load_dwordx4 v20, s[80:83], s85 offen sc1 lds\n" \
"v_mfma_f32_16x16x32_bf16 v[64:67], a[188:191], a[128:131], v[64:67]\n" \
"ds_read_b64_tr_b16 a[224:225], v32 offset:4096\n" \
"s_mul_i32 s12, s8, 0x120\n" \
"v_mfma_f32_16x16x32_bf16 v[64:67], a[252:255], a[192:195], v[64:67]\n" \
"ds_read_b128 a[128:131], v28 offset:0\n" \
"v_mfma_f32_16x16x32_bf16 v[80:83], a[188:191], a[132:135], v[80:83]\n" \
"ds_read_b128 a[192:195], v29 offset:0\n" \
"v_mfma_f32_16x16x32_bf16 v[80:83], a[252:255], a[196:199], v[80:83]\n" \
"s_add_u32 s88, s41, 0x1000\n" \
"buffer_store_dwordx4 v[236:239], v20, s[80:83], s88 offen offset:2048 sc1\n" \
"v_mfma_f32_16x16x32_bf16 v[76:79], a[184:187], a[132:135], v[76:79]\n" \
"ds_read_b64_tr_b16 a[226:227], v32 offset:4608\n" \
"v_mfma_f32_16x16x32_bf16 v[76:79], a[248:251], a[196:199], v[76:79]\n" \
"ds_read_b64_tr_b16 a[164:165], v33 offset:0\n" \
"v_mfma_f32_16x16x32_bf16 v[72:75], a[180:183], a[132:135], v[72:75]\n" \
"s_add_u32 m0, s40, 0xb000\n" \
"s_movk_i32 s84, 0x180\n" \
"s_nop 0\n" \
"v_mfma_f32_16x16x32_bf16 v[72:75], a[244:247], a[196:199], v[72:75]\n" \
"buffer_load_dwordx4 v21, s[76:79], s84 offen offset:0 lds\n" \
"v_mfma_f32_16x16x32_bf16 v[68:71], a[176:179], a[132:135], v[68:71]\n" \
"ds_read_b64_tr_b16 a[166:167], v33 offset:512\n" \
"v_mfma_f32_16x16x32_bf16 v[68:71], a[240:243], a[196:199], v[68:71]\n" \
"ds_read_b128 a[132:135], v28 offset:2048\n" \
"v_mfma_f32_16x16x32_bf16 v[84:87], a[176:179], a[136:139], v[84:87]\n" \
"ds_read_b128 a[196:199], v29 offset:2048\n" \
"v_mfma_f32_16x16x32_bf16 v[84:87], a[240:243], a[200:203], v[84:87]\n" \
"s_add_u32 m0, s31, 0x8000\n" \
"s_mov_b32 s84, 0x180000\n" \
"v_mfma_f32_16x16x32_bf16 v[88:91], a[180:183], a[136:139], v[88:91]\n" \
"buffer_load_dwordx4 v26, s[68:71], s84 offen offset:0 lds\n" \
"v_mfma_f32_16x16x32_bf16 v[88:91], a[244:247], a[200:203], v[88:91]\n" \
"ds_read_b64_tr_b16 a[228:229], v33 offset:4096\n" \
"v_mfma_f32_16x16x32_bf16 v[92:95], a[184:187], a[136:139], v[92:95]\n" \
"ds_read_b64_tr_b16 a[230:231], v33 offset:4608\n" \
"v_mfma_f32_16x16x32_bf16 v[92:95], a[248:251], a[200:203], v[92:95]\n" \
"s_addk_i32 s84, 0x7c00\n" \
"v_mfma_f32_16x16x32_bf16 v[96:99], a[188:191], a[136:139], v[96:99]\n" \
"buffer_load_dwordx4 v26, s[68:71], s84 offen offset:1024 lds\n" \
"v_mfma_f32_16x16x32_bf16 v[96:99], a[252:255], a[200:203], v[96:99]\n" \
"ds_read_b128 a[136:139], v28 offset:4096\n" \
"v_mfma_f32_16x16x32_bf16 v[112:115], a[188:191], a[140:143], v[112:115]\n" \
"ds_read_b128 a[200:203], v29 offset:4096\n" \
"v_mfma_f32_16x16x32_bf16 v[112:115], a[252:255], a[204:207], v[112:115]\n" \
"s_addk_i32 s84, 0x7c00\n" \
"v_mfma_f32_16x16x32_bf16 v[108:111], a[184:187], a[140:143], v[108:111]\n" \
"buffer_load_dwordx4 v27, s[68:71], s84 offen offset:2048 lds\n" \
"v_mfma_f32_16x16x32_bf16 v[108:111], a[248:251], a[204:207], v[108:111]\n" \
"ds_read_b64_tr_b16 a[172:173], v35 offset:0\n" \
"v_mfma_f32_16x16x32_bf16 v[104:107], a[180:183], a[140:143], v[104:107]\n" \
"ds_read_b64_tr_b16 a[174:175], v35 offset:512\n" \
"v_mfma_f32_16x16x32_bf16 v[104:107], a[244:247], a[204:207], v[104:107]\n" \
"ds_read_b64_tr_b16 a[236:237], v35 offset:4096\n" \
"v_mfma_f32_16x16x32_bf16 v[100:103], a[176:179], a[140:143], v[100:103]\n" \
"s_add_u32 s11, s22, s12\n" \
"s_addc_u32 s17, s23, 0\n" \
"v_mfma_f32_16x16x32_bf16 v[100:103], a[240:243], a[204:207], v[100:103]\n" \
"ds_read_b128 a[140:143], v28 offset:6144\n" \
"v_mfma_f32_16x16x32_bf16 v[116:119], a[176:179], a[144:147], v[116:119]\n" \
"ds_read_b128 a[204:207], v29 offset:6144\n" \
"v_mfma_f32_16x16x32_bf16 v[116:119], a[240:243], a[208:211], v[116:119]\n" \
"s_addk_i32 s84, 0x7c00\n" \
"v_mfma_f32_16x16x32_bf16 v[120:123], a[180:183], a[144:147], v[120:123]\n" \
"buffer_load_dwordx4 v27, s[68:71], s84 offen offset:3072 lds\n" \
"v_mfma_f32_16x16x32_bf16 v[120:123], a[244:247], a[208:211], v[120:123]\n" \
"s_add_u32 m0, s30, 0x8000\n" \
"s_movk_i32 s84, 0x200\n" \
"v_mfma_f32_16x16x32_bf16 v[124:127], a[184:187], a[144:147], v[124:127]\n" \
"buffer_load_dwordx4 v24, s[76:79], s84 offen offset:0 lds\n" \
"v_mfma_f32_16x16x32_bf16 v[124:127], a[248:251], a[208:211], v[124:127]\n" \
"ds_read_b64_tr_b16 a[238:239], v35 offset:4608\n" \
"v_mfma_f32_16x16x32_bf16 v[128:131], a[188:191], a[144:147], v[128:131]\n" \
"s_add_u32 s89, s41, 0x2000\n" \
"buffer_store_dwordx4 v[240:243], v20, s[80:83], s89 offen offset:2048 sc1\n" \
"v_mfma_f32_16x16x32_bf16 v[128:131], a[252:255], a[208:211], v[128:131]\n" \
"ds_read_b128 a[144:147], v28 offset:8192\n" \
"v_mfma_f32_16x16x32_bf16 v[144:147], a[188:191], a[148:151], v[144:147]\n" \
"ds_read_b128 a[208:211], v29 offset:8192\n" \
"v_mfma_f32_16x16x32_bf16 v[144:147], a[252:255], a[212:215], v[144:147]\n" \
"ds_read_b64_tr_b16 a[168:169], v34 offset:0\n" \
"v_mfma_f32_16x16x32_bf16 v[140:143], a[184:187], a[148:151], v[140:143]\n" \
"ds_read_b64_tr_b16 a[170:171], v34 offset:512\n" \
"v_mfma_f32_16x16x32_bf16 v[140:143], a[248:251], a[212:215], v[140:143]\n" \
"s_movk_i32 s84, 0x2200\n" \
"s_nop 0\n" \
"v_mfma_f32_16x16x32_bf16 v[136:139], a[180:183], a[148:151], v[136:139]\n" \
"buffer_load_dwordx4 v25, s[76:79], s84 offen offset:1024 lds\n" \
"v_mfma_f32_16x16x32_bf16 v[136:139], a[244:247], a[212:215], v[136:139]\n" \
"ds_read_b64_tr_b16 a[232:233], v34 offset:4096\n" \
"v_mfma_f32_16x16x32_bf16 v[132:135], a[176:179], a[148:151], v[132:135]\n" \
"ds_read_b64_tr_b16 a[234:235], v34 offset:4608\n" \
"v_mfma_f32_16x16x32_bf16 v[132:135], a[240:243], a[212:215], v[132:135]\n" \
"ds_read_b128 a[148:151], v28 offset:10240\n" \
"v_mfma_f32_16x16x32_bf16 v[148:151], a[176:179], a[152:155], v[148:151]\n" \
"ds_read_b128 a[212:215], v29 offset:10240\n" \
"v_mfma_f32_16x16x32_bf16 v[148:151], a[240:243], a[216:219], v[148:151]\n" \
"s_movk_i32 s84, 0x4200\n" \
"v_mfma_f32_16x16x32_bf16 v[152:155], a[180:183], a[152:155], v[152:155]\n" \
"buffer_load_dwordx4 v24, s[76:79], s84 offen offset:2048 lds\n" \
"v_mfma_f32_16x16x32_bf16 v[152:155], a[244:247], a[216:219], v[152:155]\n" \
"s_movk_i32 s84, 0x6200\n" \
"v_mfma_f32_16x16x32_bf16 v[156:159], a[184:187], a[152:155], v[156:159]\n" \
"buffer_load_dwordx4 v25, s[76:79], s84 offen offset:3072 lds\n" \
"v_mfma_f32_16x16x32_bf16 v[156:159], a[248:251], a[216:219], v[156:159]\n" \
"ds_read_b128 v[248:251], v22 offset:0\n" \
"v_mfma_f32_16x16x32_bf16 v[160:163], a[188:191], a[152:155], v[160:163]\n" \
"ds_read_b128 v[48:51], v23 offset:0\n" \
"v_mfma_f32_16x16x32_bf16 v[160:163], a[252:255], a[216:219], v[160:163]\n" \
"ds_read_b128 a[152:155], v28 offset:12288\n" \
"v_mfma_f32_16x16x32_bf16 v[176:179], a[188:191], a[156:159], v[176:179]\n" \
"ds_read_b128 a[216:219], v29 offset:12288\n" \
"v_mfma_f32_16x16x32_bf16 v[176:179], a[252:255], a[220:223], v[176:179]\n" \
"s_add_u32 s18, s24, s12\n" \
"v_writelane_b32 v11, s11, 9\n" \
"s_addc_u32 s19, s25, 0\n" \
"v_mfma_f32_16x16x32_bf16 v[172:175], a[184:187], a[156:159], v[172:175]\n" \
"s_and_b32 s20, s17, 0xffff\n" \
"v_writelane_b32 v11, s20, 10\n" \
"v_mfma_f32_16x16x32_bf16 v[172:175], a[248:251], a[220:223], v[172:175]\n" \
"s_and_b32 s20, s19, 0xffff\n" \
"v_writelane_b32 v11, s18, 11\n" \
"v_mfma_f32_16x16x32_bf16 v[168:171], a[180:183], a[156:159], v[168:171]\n" \
"s_cmpk_lt_u32 s3, 3\n" \
"s_cselect_b32 s11, s11, s18\n" \
"v_mfma_f32_16x16x32_bf16 v[168:171], a[244:247], a[220:223], v[168:171]\n" \
"s_movk_i32 s18, 0xfff\n" \
"v_writelane_b32 v11, s20, 12\n" \
"v_mfma_f32_16x16x32_bf16 v[164:167], a[176:179], a[156:159], v[164:167]\n" \
"s_and_b32 s20, s29, 0xffff\n" \
"v_writelane_b32 v11, s28, 13\n" \
"v_mfma_f32_16x16x32_bf16 v[164:167], a[240:243], a[220:223], v[164:167]\n" \
"ds_read_b128 a[156:159], v28 offset:14336\n" \
"ds_read_b128 a[220:223], v29 offset:14336\n" \
"s_waitcnt lgkmcnt(15)\n" \
"v_mfma_f32_16x16x32_bf16 a[0:3], a[160:163], a[128:131], a[0:3]\n" \
"s_add_u32 m0, s30, 0x9000\n" \
"s_addk_i32 s84, 0x3000\n" \
"v_mfma_f32_16x16x32_bf16 a[0:3], a[224:227], a[192:195], a[0:3]\n" \
"buffer_load_dwordx4 v24, s[76:79], s84 offen offset:0 lds\n" \
"v_mfma_f32_16x16x32_bf16 a[4:7], a[164:167], a[128:131], a[4:7]\n" \
"ds_read_b64_tr_b16 a[176:177], v36 offset:0\n" \
"v_writelane_b32 v11, s20, 14\n" \
"v_mfma_f32_16x16x32_bf16 a[4:7], a[228:231], a[192:195], a[4:7]\n" \
"ds_read_b64_tr_b16 a[178:179], v36 offset:1024\n" \
"v_writelane_b32 v11, s11, 15\n" \
"v_mfma_f32_16x16x32_bf16 a[12:15], a[172:175], a[128:131], a[12:15]\n" \
"ds_read_b64_tr_b16 a[240:241], v36 offset:8192\n" \
"s_waitcnt lgkmcnt(15)\n" \
"v_mfma_f32_16x16x32_bf16 a[12:15], a[236:239], a[192:195], a[12:15]\n" \
"s_add_u32 s88, s41, 0x2000\n" \
"buffer_store_dwordx4 v[244:247], v20, s[80:83], s88 offen offset:3072 sc1\n" \
"s_waitcnt lgkmcnt(13)\n" \
"v_mfma_f32_16x16x32_bf16 a[8:11], a[128:131], a[168:171], a[8:11]\n" \
"ds_read_b64_tr_b16 a[242:243], v36 offset:9216\n" \
"s_waitcnt lgkmcnt(12)\n" \
"v_mfma_f32_16x16x32_bf16 a[8:11], a[192:195], a[232:235], a[8:11]\n" \
"ds_read_b64_tr_b16 a[180:181], v37 offset:0\n" \
"v_mfma_f32_16x16x32_bf16 a[24:27], a[132:135], a[168:171], a[24:27]\n" \
"ds_read_b64_tr_b16 a[182:183], v37 offset:1024\n" \
"v_mfma_f32_16x16x32_bf16 a[24:27], a[196:199], a[232:235], a[24:27]\n" \
"ds_read_b64_tr_b16 a[244:245], v37 offset:8192\n" \
"v_mfma_f32_16x16x32_bf16 a[28:31], a[172:175], a[132:135], a[28:31]\n" \
"s_cmpk_lt_u32 s3, 3\n" \
"s_cselect_b32 s11, s17, s19\n" \
"v_mfma_f32_16x16x32_bf16 a[28:31], a[236:239], a[196:199], a[28:31]\n" \
"s_addk_i32 s84, 0x2000\n" \
"s_nop 0\n" \
"v_mfma_f32_16x16x32_bf16 a[20:23], a[164:167], a[132:135], a[20:23]\n" \
"buffer_load_dwordx4 v25, s[76:79], s84 offen offset:1024 lds\n" \
"v_mfma_f32_16x16x32_bf16 a[20:23], a[228:231], a[196:199], a[20:23]\n" \
"ds_read_b64_tr_b16 a[246:247], v37 offset:9216\n" \
"s_and_b32 s11, s11, 0xffff\n" \
"v_mfma_f32_16x16x32_bf16 a[16:19], a[160:163], a[132:135], a[16:19]\n" \
"ds_read_b64_tr_b16 a[184:185], v38 offset:0\n" \
"v_writelane_b32 v11, s11, 16\n" \
"v_mfma_f32_16x16x32_bf16 a[16:19], a[224:227], a[196:199], a[16:19]\n" \
"ds_read_b64_tr_b16 a[186:187], v38 offset:1024\n" \
"s_mul_i32 s11, s6, 0x48\n" \
"v_mfma_f32_16x16x32_bf16 a[32:35], a[160:163], a[136:139], a[32:35]\n" \
"s_addk_i32 s84, 0x2000\n" \
"s_nop 0\n" \
"v_mfma_f32_16x16x32_bf16 a[32:35], a[224:227], a[200:203], a[32:35]\n" \
"buffer_load_dwordx4 v24, s[76:79], s84 offen offset:2048 lds\n" \
"v_mfma_f32_16x16x32_bf16 a[36:39], a[164:167], a[136:139], a[36:39]\n" \
"ds_read_b64_tr_b16 a[248:249], v38 offset:8192\n" \
"s_add_i32 s17, s11, 0x47\n" \
"v_mfma_f32_16x16x32_bf16 a[36:39], a[228:231], a[200:203], a[36:39]\n" \
"ds_read_b64_tr_b16 a[250:251], v38 offset:9216\n" \
".fill (L_pb + 24288 - .) / 4, 4, 0xBF800000\n" \
"L_pp6:\n" \
"s_cbranch_execz L_pret\n" \
"s_nop 0\n" \
"v_mfma_f32_16x16x32_bf16 a[44:47], a[172:175], a[136:139], a[44:47]\n" \
"ds_read_b64_tr_b16 a[188:189], v39 offset:0\n" \
"v_mfma_f32_16x16x32_bf16 a[44:47], a[236:239], a[200:203], a[44:47]\n" \
"ds_read_b64_tr_b16 a[190:191], v39 offset:1024\n" \
"v_mfma_f32_16x16x32_bf16 a[40:43], a[136:139], a[168:171], a[40:43]\n" \
"s_add_u32 s89, s41, 0x3000\n" \
"buffer_store_dwordx4 v[184:187], v20, s[80:83], s89 offen offset:3072 sc1\n" \
"v_mfma_f32_16x16x32_bf16 a[40:43], a[200:203], a[232:235], a[40:43]\n" \
"ds_read_b64_tr_b16 a[252:253], v39 offset:8192\n" \
"v_mfma_f32_16x16x32_bf16 a[56:59], a[140:143], a[168:171], a[56:59]\n" \
"ds_read_b64_tr_b16 a[254:255], v39 offset:9216\n" \
"v_mfma_f32_16x16x32_bf16 a[56:59], a[204:207], a[232:235], a[56:59]\n" \
"s_cmpk_lt_u32 s3, 3\n" \
"s_cselect_b32 s17, s17, s18\n" \
"v_mfma_f32_16x16x32_bf16 a[60:63], a[172:175], a[140:143], a[60:63]\n" \
"s_mulk_i32 s17, 0x480\n" \
"s_addk_i32 s17, 0x120\n" \
"v_mfma_f32_16x16x32_bf16 a[60:63], a[236:239], a[204:207], a[60:63]\n" \
"s_addk_i32 s84, 0x2000\n" \
"v_mfma_f32_16x16x32_bf16 a[52:55], a[164:167], a[140:143], a[52:55]\n" \
"buffer_load_dwordx4 v25, s[76:79], s84 offen offset:3072 lds\n" \
"v_mfma_f32_16x16x32_bf16 a[52:55], a[228:231], a[204:207], a[52:55]\n" \
"v_writelane_b32 v11, s17, 17\n" \
"s_lshl_b32 s17, s3, 5\n" \
"v_mfma_f32_16x16x32_bf16 a[48:51], a[160:163], a[140:143], a[48:51]\n" \
"s_add_i32 s11, s11, s17\n" \
"s_mul_i32 s17, s11, 0x480\n" \
"v_mfma_f32_16x16x32_bf16 a[48:51], a[224:227], a[204:207], a[48:51]\n" \
"s_mul_i32 s11, s13, 0x24000\n" \
"s_add_i32 s18, s11, 0x1b000\n" \
"s_waitcnt lgkmcnt(15)\n" \
"v_mfma_f32_16x16x32_bf16 a[64:67], a[164:167], v[248:251], a[64:67]\n" \
"s_cmpk_lt_u32 s3, 3\n" \
"s_cselect_b32 s17, s17, s18\n" \
"v_mfma_f32_16x16x32_bf16 a[64:67], a[228:231], v[48:51], a[64:67]\n" \
"s_mov_b32 s18, 0x8000\n" \
"v_writelane_b32 v11, s17, 18\n" \
"v_mfma_f32_16x16x32_bf16 a[80:83], a[172:175], v[248:251], a[80:83]\n" \
"s_mul_i32 s17, s16, 0x2400\n" \
"s_mul_i32 s16, s16, 0x9000\n" \
"v_mfma_f32_16x16x32_bf16 a[80:83], a[236:239], v[48:51], a[80:83]\n" \
"s_bitset1_b32 s17, 17\n" \
"v_writelane_b32 v11, s17, 19\n" \
"v_mfma_f32_16x16x32_bf16 a[68:71], v[248:251], a[168:171], a[68:71]\n" \
"s_add_i32 s16, s11, s16\n" \
"v_writelane_b32 v11, s16, 26\n" \
"v_mfma_f32_16x16x32_bf16 a[68:71], v[48:51], a[232:235], a[68:71]\n" \
"s_waitcnt vmcnt(11) lgkmcnt(0)\n" \
"s_barrier\n" \
"v_xor_b32_e32 v28, 0x20000, v28\n" \
"v_xor_b32_e32 v29, 0x20000, v29\n" \
"v_mfma_f32_16x16x32_bf16 v[52:55], a[176:179], a[128:131], v[52:55]\n" \
"s_add_u32 m0, s40, 0x0\n" \
"s_nop 0\n" \
"s_add_u32 s85, s39, 0x8000\n" \
"v_mfma_f32_16x16x32_bf16 v[52:55], a[240:243], a[192:195], v[52:55]\n" \
"buffer_load_dwordx4 v20, s[80:83], s85 offen sc1 lds\n" \
"v_mfma_f32_16x16x32_bf16 v[56:59], a[180:183], a[128:131], v[56:59]\n" \
"ds_read_b64_tr_b16 a[160:161], v32 offset:32768\n" \
"s_mul_i32 s16, s3, 0x2400\n" \
"v_mfma_f32_16x16x32_bf16 v[56:59], a[244:247], a[192:195], v[56:59]\n" \
"ds_read_b64_tr_b16 a[162:163], v32 offset:33280\n" \
"s_add_i32 s17, s16, 0xa400\n" \
"v_mfma_f32_16x16x32_bf16 v[60:63], a[184:187], a[128:131], v[60:63]\n" \
"s_add_u32 m0, s40, 0x1000\n" \
"s_add_u32 s85, s39, 0x9000\n" \
"v_mfma_f32_16x16x32_bf16 v[60:63], a[248:251], a[192:195], v[60:63]\n" \
"buffer_load_dwordx4 v20, s[80:83], s85 offen sc1 lds\n" \
"v_mfma_f32_16x16x32_bf16 v[64:67], a[188:191], a[128:131], v[64:67]\n" \
"ds_read_b64_tr_b16 a[224:225], v32 offset:36864\n" \
"v_mfma_f32_16x16x32_bf16 v[64:67], a[252:255], a[192:195], v[64:67]\n" \
"ds_read_b128 a[128:131], v28 offset:0\n" \
"v_mfma_f32_16x16x32_bf16 v[80:83], a[188:191], a[132:135], v[80:83]\n" \
"ds_read_b128 a[192:195], v29 offset:0\n" \
"v_mfma_f32_16x16x32_bf16 v[80:83], a[252:255], a[196:199], v[80:83]\n" \
"s_add_u32 s88, s41, 0x3000\n" \
"buffer_store_dwordx4 v[188:191], v20, s[80:83], s88 offen offset:2048 sc1\n" \
"v_mfma_f32_16x16x32_bf16 v[76:79], a[184:187], a[132:135], v[76:79]\n" \
"ds_read_b64_tr_b16 a[226:227], v32 offset:37376\n" \
"v_mfma_f32_16x16x32_bf16 v[76:79], a[248:251], a[196:199], v[76:79]\n" \
"ds_read_b64_tr_b16 a[164:165], v33 offset:32768\n" \
"v_mfma_f32_16x16x32_bf16 v[72:75], a[180:183], a[132:135], v[72:75]\n" \
"s_add_u32 m0, s40, 0x3000\n" \
"s_movk_i32 s84, 0x200\n" \
"v_mfma_f32_16x16x32_bf16 v[72:75], a[244:247], a[196:199], v[72:75]\n" \
"buffer_load_dwordx4 v21, s[76:79], s84 offen offset:0 lds\n" \
"v_mfma_f32_16x16x32_bf16 v[68:71], a[176:179], a[132:135], v[68:71]\n" \
"ds_read_b64_tr_b16 a[166:167], v33 offset:33280\n" \
"v_mfma_f32_16x16x32_bf16 v[68:71], a[240:243], a[196:199], v[68:71]\n" \
"ds_read_b128 a[132:135], v28 offset:2048\n" \
"v_mfma_f32_16x16x32_bf16 v[84:87], a[176:179], a[136:139], v[84:87]\n" \
"ds_read_b128 a[196:199], v29 offset:2048\n" \
"v_mfma_f32_16x16x32_bf16 v[84:87], a[240:243], a[200:203], v[84:87]\n" \
"s_add_u32 m0, s31, 0x0\n" \
"s_mov_b32 s84, 0x200000\n" \
"v_mfma_f32_16x16x32_bf16 v[88:91], a[180:183], a[136:139], v[88:91]\n" \
"buffer_load_dwordx4 v26, s[68:71], s84 offen offset:0 lds\n" \
"v_mfma_f32_16x16x32_bf16 v[88:91], a[244:247], a[200:203], v[88:91]\n" \
"ds_read_b64_tr_b16 a[228:229], v33 offset:36864\n" \
"v_mfma_f32_16x16x32_bf16 v[92:95], a[184:187], a[136:139], v[92:95]\n" \
"ds_read_b64_tr_b16 a[230:231], v33 offset:37376\n" \
"v_mfma_f32_16x16x32_bf16 v[92:95], a[248:251], a[200:203], v[92:95]\n" \
"s_addk_i32 s84, 0x7c00\n" \
"v_mfma_f32_16x16x32_bf16 v[96:99], a[188:191], a[136:139], v[96:99]\n" \
"buffer_load_dwordx4 v26, s[68:71], s84 offen offset:1024 lds\n" \
"v_mfma_f32_16x16x32_bf16 v[96:99], a[252:255], a[200:203], v[96:99]\n" \
"ds_read_b128 a[136:139], v28 offset:4096\n" \
"v_mfma_f32_16x16x32_bf16 v[112:115], a[188:191], a[140:143], v[112:115]\n" \
"ds_read_b128 a[200:203], v29 offset:4096\n" \
"v_mfma_f32_16x16x32_bf16 v[112:115], a[252:255], a[204:207], v[112:115]\n" \
"s_addk_i32 s84, 0x7c00\n" \
"v_mfma_f32_16x16x32_bf16 v[108:111], a[184:187], a[140:143], v[108:111]\n" \
"buffer_load_dwordx4 v27, s[68:71], s84 offen offset:2048 lds\n" \
"v_mfma_f32_16x16x32_bf16 v[108:111], a[248:251], a[204:207], v[108:111]\n" \
"ds_read_b64_tr_b16 a[172:173], v35 offset:32768\n" \
"v_mfma_f32_16x16x32_bf16 v[104:107], a[180:183], a[140:143], v[104:107]\n" \
"ds_read_b64_tr_b16 a[174:175], v35 offset:33280\n" \
"v_mfma_f32_16x16x32_bf16 v[104:107], a[244:247], a[204:207], v[104:107]\n" \
"ds_read_b64_tr_b16 a[236:237], v35 offset:36864\n" \
"v_mfma_f32_16x16x32_bf16 v[100:103], a[176:179], a[140:143], v[100:103]\n" \
"s_cmpk_lt_u32 s3, 3\n" \
"s_cselect_b32 s17, s17, s18\n" \
"v_mfma_f32_16x16x32_bf16 v[100:103], a[240:243], a[204:207], v[100:103]\n" \
"ds_read_b128 a[140:143], v28 offset:6144\n" \
"s_mov_b32 s18, 0x1b000\n" \
"v_mfma_f32_16x16x32_bf16 v[116:119], a[176:179], a[144:147], v[116:119]\n" \
"ds_read_b128 a[204:207], v29 offset:6144\n" \
"s_xor_b32 s19, s3, 2\n" \
"v_mfma_f32_16x16x32_bf16 v[116:119], a[240:243], a[208:211], v[116:119]\n" \
"s_addk_i32 s84, 0x7c00\n" \
"v_mfma_f32_16x16x32_bf16 v[120:123], a[180:183], a[144:147], v[120:123]\n" \
"buffer_load_dwordx4 v27, s[68:71], s84 offen offset:3072 lds\n" \
"v_mfma_f32_16x16x32_bf16 v[120:123], a[244:247], a[208:211], v[120:123]\n" \
"s_add_u32 m0, s30, 0x20000\n" \
"s_movk_i32 s84, 0x280\n" \
"v_mfma_f32_16x16x32_bf16 v[124:127], a[184:187], a[144:147], v[124:127]\n" \
"buffer_load_dwordx4 v24, s[76:79], s84 offen offset:0 lds\n" \
"v_mfma_f32_16x16x32_bf16 v[124:127], a[248:251], a[208:211], v[124:127]\n" \
"ds_read_b64_tr_b16 a[238:239], v35 offset:37376\n" \
"v_mfma_f32_16x16x32_bf16 v[128:131], a[188:191], a[144:147], v[128:131]\n" \
"s_add_u32 s89, s42, 0x0\n" \
"buffer_store_dwordx4 v[216:219], v20, s[80:83], s89 offen offset:0 sc1\n" \
"v_mfma_f32_16x16x32_bf16 v[128:131], a[252:255], a[208:211], v[128:131]\n" \
"ds_read_b128 a[144:147], v28 offset:8192\n" \
"v_mfma_f32_16x16x32_bf16 v[144:147], a[188:191], a[148:151], v[144:147]\n" \
"ds_read_b128 a[208:211], v29 offset:8192\n" \
"v_mfma_f32_16x16x32_bf16 v[144:147], a[252:255], a[212:215], v[144:147]\n" \
"ds_read_b64_tr_b16 a[168:169], v34 offset:32768\n" \
"v_mfma_f32_16x16x32_bf16 v[140:143], a[184:187], a[148:151], v[140:143]\n" \
"ds_read_b64_tr_b16 a[170:171], v34 offset:33280\n" \
"v_mfma_f32_16x16x32_bf16 v[140:143], a[248:251], a[212:215], v[140:143]\n" \
"s_movk_i32 s84, 0x2280\n" \
"s_nop 0\n" \
"v_mfma_f32_16x16x32_bf16 v[136:139], a[180:183], a[148:151], v[136:139]\n" \
"buffer_load_dwordx4 v25, s[76:79], s84 offen offset:1024 lds\n" \
"v_mfma_f32_16x16x32_bf16 v[136:139], a[244:247], a[212:215], v[136:139]\n" \
"ds_read_b64_tr_b16 a[232:233], v34 offset:36864\n" \
"v_mfma_f32_16x16x32_bf16 v[132:135], a[176:179], a[148:151], v[132:135]\n" \
"ds_read_b64_tr_b16 a[234:235], v34 offset:37376\n" \
"v_mfma_f32_16x16x32_bf16 v[132:135], a[240:243], a[212:215], v[132:135]\n" \
"ds_read_b128 a[148:151], v28 offset:10240\n" \
"v_mfma_f32_16x16x32_bf16 v[148:151], a[176:179], a[152:155], v[148:151]\n" \
"ds_read_b128 a[212:215], v29 offset:10240\n" \
"v_mfma_f32_16x16x32_bf16 v[148:151], a[240:243], a[216:219], v[148:151]\n" \
"s_movk_i32 s84, 0x4280\n" \
"v_mfma_f32_16x16x32_bf16 v[152:155], a[180:183], a[152:155], v[152:155]\n" \
"buffer_load_dwordx4 v24, s[76:79], s84 offen offset:2048 lds\n" \
"v_mfma_f32_16x16x32_bf16 v[152:155], a[244:247], a[216:219], v[152:155]\n" \
"s_movk_i32 s84, 0x6280\n" \
"v_mfma_f32_16x16x32_bf16 v[156:159], a[184:187], a[152:155], v[156:159]\n" \
"buffer_load_dwordx4 v25, s[76:79], s84 offen offset:3072 lds\n" \
"v_mfma_f32_16x16x32_bf16 v[156:159], a[248:251], a[216:219], v[156:159]\n" \
"ds_read_b128 v[248:251], v22 offset:32768\n" \
"v_mfma_f32_16x16x32_bf16 v[160:163], a[188:191], a[152:155], v[160:163]\n" \
"ds_read_b128 v[48:51], v23 offset:32768\n" \
"v_mfma_f32_16x16x32_bf16 v[160:163], a[252:255], a[216:219], v[160:163]\n" \
"ds_read_b128 a[152:155], v28 offset:12288\n" \
"v_mfma_f32_16x16x32_bf16 v[176:179], a[188:191], a[156:159], v[176:179]\n" \
"ds_read_b128 a[216:219], v29 offset:12288\n" \
"v_mfma_f32_16x16x32_bf16 v[176:179], a[252:255], a[220:223], v[176:179]\n" \
"s_cmp_lg_u32 s19, 0\n" \
"s_cselect_b32 s18, s17, s18\n" \
"v_mfma_f32_16x16x32_bf16 v[172:175], a[184:187], a[156:159], v[172:175]\n" \
"v_writelane_b32 v11, s17, 20\n" \
"s_or_b32 s17, s16, 0x10000\n" \
"v_mfma_f32_16x16x32_bf16 v[172:175], a[248:251], a[220:223], v[172:175]\n" \
"v_writelane_b32 v11, s18, 21\n" \
"s_add_i32 s18, s16, 0x19000\n" \
"v_mfma_f32_16x16x32_bf16 v[168:171], a[180:183], a[156:159], v[168:171]\n" \
"v_writelane_b32 v11, s17, 22\n" \
"s_mov_b32 s17, 0\n" \
"v_mfma_f32_16x16x32_bf16 v[168:171], a[244:247], a[220:223], v[168:171]\n" \
"s_cmpk_lt_u32 s3, 3\n" \
"s_cselect_b32 s18, s18, s17\n" \
"v_mfma_f32_16x16x32_bf16 v[164:167], a[176:179], a[156:159], v[164:167]\n" \
"s_bitset1_b32 s16, 17\n" \
"v_writelane_b32 v11, s18, 23\n" \
"v_mfma_f32_16x16x32_bf16 v[164:167], a[240:243], a[220:223], v[164:167]\n" \
"ds_read_b128 a[156:159], v28 offset:14336\n" \
"ds_read_b128 a[220:223], v29 offset:14336\n" \
"s_waitcnt lgkmcnt(15)\n" \
"v_mfma_f32_16x16x32_bf16 a[0:3], a[160:163], a[128:131], a[0:3]\n" \
"s_add_u32 m0, s30, 0x21000\n" \
"s_addk_i32 s84, 0x3000\n" \
"v_mfma_f32_16x16x32_bf16 a[0:3], a[224:227], a[192:195], a[0:3]\n" \
"buffer_load_dwordx4 v24, s[76:79], s84 offen offset:0 lds\n" \
"v_mfma_f32_16x16x32_bf16 a[4:7], a[164:167], a[128:131], a[4:7]\n" \
"ds_read_b64_tr_b16 a[176:177], v36 offset:32768\n" \
"s_movk_i32 s18, 0x2400\n" \
"v_mfma_f32_16x16x32_bf16 a[4:7], a[228:231], a[192:195], a[4:7]\n" \
"ds_read_b64_tr_b16 a[178:179], v36 offset:33792\n" \
"v_mfma_f32_16x16x32_bf16 a[12:15], a[172:175], a[128:131], a[12:15]\n" \
"ds_read_b64_tr_b16 a[240:241], v36 offset:40960\n" \
"s_waitcnt lgkmcnt(15)\n" \
"v_mfma_f32_16x16x32_bf16 a[12:15], a[236:239], a[192:195], a[12:15]\n" \
"s_add_u32 s88, s42, 0x1000\n" \
"buffer_store_dwordx4 v[220:223], v20, s[80:83], s88 offen offset:0 sc1\n" \
"s_waitcnt lgkmcnt(13)\n" \
"v_mfma_f32_16x16x32_bf16 a[8:11], a[128:131], a[168:171], a[8:11]\n" \
"ds_read_b64_tr_b16 a[242:243], v36 offset:41984\n" \
"s_waitcnt lgkmcnt(12)\n" \
"v_mfma_f32_16x16x32_bf16 a[8:11], a[192:195], a[232:235], a[8:11]\n" \
"ds_read_b64_tr_b16 a[180:181], v37 offset:32768\n" \
"v_mfma_f32_16x16x32_bf16 a[24:27], a[132:135], a[168:171], a[24:27]\n" \
"ds_read_b64_tr_b16 a[182:183], v37 offset:33792\n" \
"v_mfma_f32_16x16x32_bf16 a[24:27], a[196:199], a[232:235], a[24:27]\n" \
"ds_read_b64_tr_b16 a[244:245], v37 offset:40960\n" \
"v_mfma_f32_16x16x32_bf16 a[28:31], a[172:175], a[132:135], a[28:31]\n" \
"s_cmpk_lt_u32 s3, 3\n" \
"s_cselect_b32 s16, s16, s18\n" \
"v_mfma_f32_16x16x32_bf16 a[28:31], a[236:239], a[196:199], a[28:31]\n" \
"s_addk_i32 s84, 0x2000\n" \
"v_mfma_f32_16x16x32_bf16 a[20:23], a[164:167], a[132:135], a[20:23]\n" \
"buffer_load_dwordx4 v25, s[76:79], s84 offen offset:1024 lds\n" \
"v_mfma_f32_16x16x32_bf16 a[20:23], a[228:231], a[196:199], a[20:23]\n" \
"ds_read_b64_tr_b16 a[246:247], v37 offset:41984\n" \
"s_mul_i32 s19, s3, 0x9000\n" \
"v_mfma_f32_16x16x32_bf16 a[16:19], a[160:163], a[132:135], a[16:19]\n" \
"ds_read_b64_tr_b16 a[184:185], v38 offset:32768\n" \
"v_writelane_b32 v11, s16, 24\n" \
"v_mfma_f32_16x16x32_bf16 a[16:19], a[224:227], a[196:199], a[16:19]\n" \
"ds_read_b64_tr_b16 a[186:187], v38 offset:33792\n" \
"s_lshl_b32 s16, s3, 10\n" \
"v_mfma_f32_16x16x32_bf16 a[32:35], a[160:163], a[136:139], a[32:35]\n" \
"s_addk_i32 s84, 0x2000\n" \
"v_mfma_f32_16x16x32_bf16 a[32:35], a[224:227], a[200:203], a[32:35]\n" \
"buffer_load_dwordx4 v24, s[76:79], s84 offen offset:2048 lds\n" \
"v_mfma_f32_16x16x32_bf16 a[36:39], a[164:167], a[136:139], a[36:39]\n" \
"ds_read_b64_tr_b16 a[248:249], v38 offset:40960\n" \
"s_addk_i32 s16, 0x4800\n" \
"v_mfma_f32_16x16x32_bf16 a[36:39], a[228:231], a[200:203], a[36:39]\n" \
"ds_read_b64_tr_b16 a[250:251], v38 offset:41984\n" \
"v_writelane_b32 v11, s16, 25\n" \
"v_mfma_f32_16x16x32_bf16 a[44:47], a[172:175], a[136:139], a[44:47]\n" \
"ds_read_b64_tr_b16 a[188:189], v39 offset:32768\n" \
"s_add_i32 s16, s15, 1\n" \
"v_mfma_f32_16x16x32_bf16 a[44:47], a[236:239], a[200:203], a[44:47]\n" \
"ds_read_b64_tr_b16 a[190:191], v39 offset:33792\n" \
"s_and_b32 s16, s16, 3\n" \
"v_mfma_f32_16x16x32_bf16 a[40:43], a[136:139], a[168:171], a[40:43]\n" \
"s_add_u32 s89, s42, 0x0\n" \
"buffer_store_dwordx4 v[192:195], v20, s[80:83], s89 offen offset:2048 sc1\n" \
"v_mfma_f32_16x16x32_bf16 a[40:43], a[200:203], a[232:235], a[40:43]\n" \
"ds_read_b64_tr_b16 a[252:253], v39 offset:40960\n" \
"s_or_b32 s16, s16, s5\n" \
"v_mfma_f32_16x16x32_bf16 a[56:59], a[140:143], a[168:171], a[56:59]\n" \
"ds_read_b64_tr_b16 a[254:255], v39 offset:41984\n" \
"s_mul_i32 s18, s16, 0x24000\n" \
"v_mfma_f32_16x16x32_bf16 a[56:59], a[204:207], a[232:235], a[56:59]\n" \
"s_add_i32 s18, s18, s19\n" \
"s_xor_b32 s13, s13, 2\n" \
"v_mfma_f32_16x16x32_bf16 a[60:63], a[172:175], a[140:143], a[60:63]\n" \
"s_add_i32 s15, s15, -1\n" \
"v_writelane_b32 v11, s18, 27\n" \
"v_mfma_f32_16x16x32_bf16 a[60:63], a[236:239], a[204:207], a[60:63]\n" \
"s_addk_i32 s84, 0x2000\n" \
"v_mfma_f32_16x16x32_bf16 a[52:55], a[164:167], a[140:143], a[52:55]\n" \
"buffer_load_dwordx4 v25, s[76:79], s84 offen offset:3072 lds\n" \
"v_mfma_f32_16x16x32_bf16 a[52:55], a[228:231], a[204:207], a[52:55]\n" \
"s_mul_i32 s18, s13, 0x24000\n" \
"s_and_b32 s15, s15, 3\n" \
"v_mfma_f32_16x16x32_bf16 a[48:51], a[160:163], a[140:143], a[48:51]\n" \
"s_add_i32 s18, s18, s19\n" \
"s_or_b32 s15, s15, s5\n" \
"v_mfma_f32_16x16x32_bf16 a[48:51], a[224:227], a[204:207], a[48:51]\n" \
"v_writelane_b32 v11, s18, 28\n" \
"s_mul_i32 s18, s15, 0x24000\n" \
"s_waitcnt lgkmcnt(15)\n" \
"v_mfma_f32_16x16x32_bf16 a[64:67], a[164:167], v[248:251], a[64:67]\n" \
"s_add_i32 s18, s18, s19\n" \
"v_writelane_b32 v11, s18, 29\n" \
"v_mfma_f32_16x16x32_bf16 a[64:67], a[228:231], v[48:51], a[64:67]\n" \
"s_or_b32 s18, s9, s14\n" \
"s_mul_i32 s18, s18, 0x24800\n" \
"v_mfma_f32_16x16x32_bf16 a[80:83], a[172:175], v[248:251], a[80:83]\n" \
"s_lshl_b32 s16, s16, 12\n" \
"s_add_i32 s19, s18, 0x3004800\n" \
"v_mfma_f32_16x16x32_bf16 a[80:83], a[236:239], v[48:51], a[80:83]\n" \
"s_add_i32 s20, s19, s16\n" \
"v_writelane_b32 v11, s20, 30\n" \
"v_mfma_f32_16x16x32_bf16 a[68:71], v[248:251], a[168:171], a[68:71]\n" \
"s_lshl_b32 s13, s13, 12\n" \
"s_lshl_b32 s15, s15, 12\n" \
"v_mfma_f32_16x16x32_bf16 a[68:71], v[48:51], a[232:235], a[68:71]\n" \
"s_waitcnt vmcnt(11) lgkmcnt(0)\n" \
"s_barrier\n" \
"v_mfma_f32_16x16x32_bf16 v[52:55], a[176:179], a[128:131], v[52:55]\n" \
"s_add_u32 m0, s40, 0x8000\n" \
"s_add_u32 s85, s39, 0xa000\n" \
"v_mfma_f32_16x16x32_bf16 v[52:55], a[240:243], a[192:195], v[52:55]\n" \
"buffer_load_dwordx4 v20, s[80:83], s85 offen sc1 lds\n" \
"v_mfma_f32_16x16x32_bf16 v[56:59], a[180:183], a[128:131], v[56:59]\n" \
"ds_read_b64_tr_b16 a[160:161], v32 offset:0\n" \
"v_accvgpr_read_b32 v224, a120\n" \
"v_mfma_f32_16x16x32_bf16 v[56:59], a[244:247], a[192:195], v[56:59]\n" \
"ds_read_b64_tr_b16 a[162:163], v32 offset:512\n" \
"v_accvgpr_read_b32 v225, a121\n" \
"v_mfma_f32_16x16x32_bf16 v[60:63], a[184:187], a[128:131], v[60:63]\n" \
"s_add_u32 m0, s40, 0x9000\n" \
"s_add_u32 s85, s39, 0xb000\n" \
"v_mfma_f32_16x16x32_bf16 v[60:63], a[248:251], a[192:195], v[60:63]\n" \
"buffer_load_dwordx4 v20, s[80:83], s85 offen sc1 lds\n" \
"v_mfma_f32_16x16x32_bf16 v[64:67], a[188:191], a[128:131], v[64:67]\n" \
"ds_read_b64_tr_b16 a[224:225], v32 offset:4096\n" \
"v_accvgpr_read_b32 v226, a122\n" \
"v_mfma_f32_16x16x32_bf16 v[64:67], a[252:255], a[192:195], v[64:67]\n" \
"ds_read_b128 a[128:131], v28 offset:32768\n" \
"v_accvgpr_read_b32 v227, a123\n" \
"v_mfma_f32_16x16x32_bf16 v[80:83], a[188:191], a[132:135], v[80:83]\n" \
"ds_read_b128 a[192:195], v29 offset:32768\n" \
"v_accvgpr_read_b32 v180, a124\n" \
"v_mfma_f32_16x16x32_bf16 v[80:83], a[252:255], a[196:199], v[80:83]\n" \
"s_add_u32 s88, s42, 0x0\n" \
"buffer_store_dwordx4 v[196:199], v20, s[80:83], s88 offen offset:3072 sc1\n" \
"v_mfma_f32_16x16x32_bf16 v[76:79], a[184:187], a[132:135], v[76:79]\n" \
"ds_read_b64_tr_b16 a[226:227], v32 offset:4608\n" \
"v_accvgpr_read_b32 v181, a125\n" \
"v_mfma_f32_16x16x32_bf16 v[76:79], a[248:251], a[196:199], v[76:79]\n" \
"ds_read_b64_tr_b16 a[164:165], v33 offset:0\n" \
"v_accvgpr_read_b32 v182, a126\n" \
"v_mfma_f32_16x16x32_bf16 v[72:75], a[180:183], a[132:135], v[72:75]\n" \
"s_add_u32 m0, s40, 0xb000\n" \
"s_movk_i32 s84, 0x280\n" \
"v_mfma_f32_16x16x32_bf16 v[72:75], a[244:247], a[196:199], v[72:75]\n" \
"buffer_load_dwordx4 v21, s[76:79], s84 offen offset:0 lds\n" \
"v_mfma_f32_16x16x32_bf16 v[68:71], a[176:179], a[132:135], v[68:71]\n" \
"ds_read_b64_tr_b16 a[166:167], v33 offset:512\n" \
"v_accvgpr_read_b32 v183, a127\n" \
"v_mfma_f32_16x16x32_bf16 v[68:71], a[240:243], a[196:199], v[68:71]\n" \
"ds_read_b128 a[132:135], v28 offset:34816\n" \
"v_cvt_pk_bf16_f32 v224, v224, v225\n" \
"v_mfma_f32_16x16x32_bf16 v[84:87], a[176:179], a[136:139], v[84:87]\n" \
"ds_read_b128 a[196:199], v29 offset:34816\n" \
"v_cvt_pk_bf16_f32 v225, v226, v227\n" \
"v_mfma_f32_16x16x32_bf16 v[84:87], a[240:243], a[200:203], v[84:87]\n" \
"s_add_u32 m0, s31, 0x8000\n" \
"s_mov_b32 s84, 0x280000\n" \
"v_mfma_f32_16x16x32_bf16 v[88:91], a[180:183], a[136:139], v[88:91]\n" \
"buffer_load_dwordx4 v26, s[68:71], s84 offen offset:0 lds\n" \
"v_mfma_f32_16x16x32_bf16 v[88:91], a[244:247], a[200:203], v[88:91]\n" \
"ds_read_b64_tr_b16 a[228:229], v33 offset:4096\n" \
"v_cvt_pk_bf16_f32 v226, v180, v181\n" \
"v_mfma_f32_16x16x32_bf16 v[92:95], a[184:187], a[136:139], v[92:95]\n" \
"ds_read_b64_tr_b16 a[230:231], v33 offset:4608\n" \
"v_cvt_pk_bf16_f32 v227, v182, v183\n" \
"v_mfma_f32_16x16x32_bf16 v[92:95], a[248:251], a[200:203], v[92:95]\n" \
"s_addk_i32 s84, 0x7c00\n" \
"v_mfma_f32_16x16x32_bf16 v[96:99], a[188:191], a[136:139], v[96:99]\n" \
"buffer_load_dwordx4 v26, s[68:71], s84 offen offset:1024 lds\n" \
"v_mfma_f32_16x16x32_bf16 v[96:99], a[252:255], a[200:203], v[96:99]\n" \
"ds_read_b128 a[136:139], v28 offset:36864\n" \
"v_accvgpr_read_b32 v220, a112\n" \
"v_mfma_f32_16x16x32_bf16 v[112:115], a[188:191], a[140:143], v[112:115]\n" \
"ds_read_b128 a[200:203], v29 offset:36864\n" \
"v_accvgpr_read_b32 v221, a113\n" \
"v_mfma_f32_16x16x32_bf16 v[112:115], a[252:255], a[204:207], v[112:115]\n" \
"s_addk_i32 s84, 0x7c00\n" \
"v_mfma_f32_16x16x32_bf16 v[108:111], a[184:187], a[140:143], v[108:111]\n" \
"buffer_load_dwordx4 v27, s[68:71], s84 offen offset:2048 lds\n" \
"v_mfma_f32_16x16x32_bf16 v[108:111], a[248:251], a[204:207], v[108:111]\n" \
"ds_read_b64_tr_b16 a[172:173], v35 offset:0\n" \
"v_accvgpr_read_b32 v222, a114\n" \
"v_mfma_f32_16x16x32_bf16 v[104:107], a[180:183], a[140:143], v[104:107]\n" \
"ds_read_b64_tr_b16 a[174:175], v35 offset:512\n" \
"v_accvgpr_read_b32 v223, a115\n" \
"v_mfma_f32_16x16x32_bf16 v[104:107], a[244:247], a[204:207], v[104:107]\n" \
"ds_read_b64_tr_b16 a[236:237], v35 offset:4096\n" \
"v_accvgpr_read_b32 v180, a116\n" \
"v_mfma_f32_16x16x32_bf16 v[100:103], a[176:179], a[140:143], v[100:103]\n" \
"v_accvgpr_read_b32 v181, a117\n" \
"v_accvgpr_read_b32 v182, a118\n" \
"v_mfma_f32_16x16x32_bf16 v[100:103], a[240:243], a[204:207], v[100:103]\n" \
"ds_read_b128 a[140:143], v28 offset:38912\n" \
"v_accvgpr_read_b32 v183, a119\n" \
"v_mfma_f32_16x16x32_bf16 v[116:119], a[176:179], a[144:147], v[116:119]\n" \
"ds_read_b128 a[204:207], v29 offset:38912\n" \
"v_cvt_pk_bf16_f32 v220, v220, v221\n" \
"v_mfma_f32_16x16x32_bf16 v[116:119], a[240:243], a[208:211], v[116:119]\n" \
"s_addk_i32 s84, 0x7c00\n" \
"v_mfma_f32_16x16x32_bf16 v[120:123], a[180:183], a[144:147], v[120:123]\n" \
"buffer_load_dwordx4 v27, s[68:71], s84 offen offset:3072 lds\n" \
"v_mfma_f32_16x16x32_bf16 v[120:123], a[244:247], a[208:211], v[120:123]\n" \
"s_add_u32 m0, s30, 0x0\n" \
"s_movk_i32 s84, 0x300\n" \
"s_nop 0\n" \
"v_mfma_f32_16x16x32_bf16 v[124:127], a[184:187], a[144:147], v[124:127]\n" \
"buffer_load_dwordx4 v24, s[76:79], s84 offen offset:0 lds\n" \
"v_mfma_f32_16x16x32_bf16 v[124:127], a[248:251], a[208:211], v[124:127]\n" \
"ds_read_b64_tr_b16 a[238:239], v35 offset:4608\n" \
"v_cvt_pk_bf16_f32 v221, v222, v223\n" \
"v_mfma_f32_16x16x32_bf16 v[128:131], a[188:191], a[144:147], v[128:131]\n" \
"s_add_u32 s89, s42, 0x1000\n" \
"buffer_store_dwordx4 a[84:87], v20, s[80:83], s89 offen offset:3072 sc1\n" \
"v_mfma_f32_16x16x32_bf16 v[128:131], a[252:255], a[208:211], v[128:131]\n" \
"ds_read_b128 a[144:147], v28 offset:40960\n" \
"v_cvt_pk_bf16_f32 v222, v180, v181\n" \
"v_mfma_f32_16x16x32_bf16 v[144:147], a[188:191], a[148:151], v[144:147]\n" \
"ds_read_b128 a[208:211], v29 offset:40960\n" \
"v_cvt_pk_bf16_f32 v223, v182, v183\n" \
"v_mfma_f32_16x16x32_bf16 v[144:147], a[252:255], a[212:215], v[144:147]\n" \
"ds_read_b64_tr_b16 a[168:169], v34 offset:0\n" \
"v_accvgpr_read_b32 v216, a104\n" \
"v_mfma_f32_16x16x32_bf16 v[140:143], a[184:187], a[148:151], v[140:143]\n" \
"ds_read_b64_tr_b16 a[170:171], v34 offset:512\n" \
"v_accvgpr_read_b32 v217, a105\n" \
"v_mfma_f32_16x16x32_bf16 v[140:143], a[248:251], a[212:215], v[140:143]\n" \
"s_movk_i32 s84, 0x2300\n" \
"s_nop 0\n" \
"v_mfma_f32_16x16x32_bf16 v[136:139], a[180:183], a[148:151], v[136:139]\n" \
"buffer_load_dwordx4 v25, s[76:79], s84 offen offset:1024 lds\n" \
"v_mfma_f32_16x16x32_bf16 v[136:139], a[244:247], a[212:215], v[136:139]\n" \
"ds_read_b64_tr_b16 a[232:233], v34 offset:4096\n" \
"v_accvgpr_read_b32 v218, a106\n" \
"v_mfma_f32_16x16x32_bf16 v[132:135], a[176:179], a[148:151], v[132:135]\n" \
"ds_read_b64_tr_b16 a[234:235], v34 offset:4608\n" \
"v_accvgpr_read_b32 v219, a107\n" \
"v_mfma_f32_16x16x32_bf16 v[132:135], a[240:243], a[212:215], v[132:135]\n" \
"ds_read_b128 a[148:151], v28 offset:43008\n" \
"v_accvgpr_read_b32 v180, a108\n" \
"v_mfma_f32_16x16x32_bf16 v[148:151], a[176:179], a[152:155], v[148:151]\n" \
"ds_read_b128 a[212:215], v29 offset:43008\n" \
"v_accvgpr_read_b32 v181, a109\n" \
"v_mfma_f32_16x16x32_bf16 v[148:151], a[240:243], a[216:219], v[148:151]\n" \
"s_movk_i32 s84, 0x4300\n" \
"v_mfma_f32_16x16x32_bf16 v[152:155], a[180:183], a[152:155], v[152:155]\n" \
"buffer_load_dwordx4 v24, s[76:79], s84 offen offset:2048 lds\n" \
"v_mfma_f32_16x16x32_bf16 v[152:155], a[244:247], a[216:219], v[152:155]\n" \
"s_movk_i32 s84, 0x6300\n" \
"v_mfma_f32_16x16x32_bf16 v[156:159], a[184:187], a[152:155], v[156:159]\n" \
"buffer_load_dwordx4 v25, s[76:79], s84 offen offset:3072 lds\n" \
"v_mfma_f32_16x16x32_bf16 v[156:159], a[248:251], a[216:219], v[156:159]\n" \
"ds_read_b128 v[248:251], v22 offset:0\n" \
"v_accvgpr_read_b32 v182, a110\n" \
"v_mfma_f32_16x16x32_bf16 v[160:163], a[188:191], a[152:155], v[160:163]\n" \
"ds_read_b128 v[48:51], v23 offset:0\n" \
"v_accvgpr_read_b32 v183, a111\n" \
"v_mfma_f32_16x16x32_bf16 v[160:163], a[252:255], a[216:219], v[160:163]\n" \
"ds_read_b128 a[152:155], v28 offset:45056\n" \
"v_cvt_pk_bf16_f32 v216, v216, v217\n" \
"v_mfma_f32_16x16x32_bf16 v[176:179], a[188:191], a[156:159], v[176:179]\n" \
"ds_read_b128 a[216:219], v29 offset:45056\n" \
"v_cvt_pk_bf16_f32 v217, v218, v219\n" \
"v_mfma_f32_16x16x32_bf16 v[176:179], a[252:255], a[220:223], v[176:179]\n" \
"v_cvt_pk_bf16_f32 v218, v180, v181\n" \
"v_cvt_pk_bf16_f32 v219, v182, v183\n" \
"v_mfma_f32_16x16x32_bf16 v[172:175], a[184:187], a[156:159], v[172:175]\n" \
"v_accvgpr_read_b32 v192, a96\n" \
"v_accvgpr_read_b32 v193, a97\n" \
"v_mfma_f32_16x16x32_bf16 v[172:175], a[248:251], a[220:223], v[172:175]\n" \
"v_accvgpr_read_b32 v194, a98\n" \
"v_accvgpr_read_b32 v195, a99\n" \
"v_mfma_f32_16x16x32_bf16 v[168:171], a[180:183], a[156:159], v[168:171]\n" \
"v_accvgpr_read_b32 v180, a100\n" \
"v_accvgpr_read_b32 v181, a101\n" \
"v_mfma_f32_16x16x32_bf16 v[168:171], a[244:247], a[220:223], v[168:171]\n" \
"v_accvgpr_read_b32 v182, a102\n" \
"v_accvgpr_read_b32 v183, a103\n" \
"v_mfma_f32_16x16x32_bf16 v[164:167], a[176:179], a[156:159], v[164:167]\n" \
"v_cvt_pk_bf16_f32 v192, v192, v193\n" \
"v_cvt_pk_bf16_f32 v193, v194, v195\n" \
"v_mfma_f32_16x16x32_bf16 v[164:167], a[240:243], a[220:223], v[164:167]\n" \
"ds_read_b128 a[156:159], v28 offset:47104\n" \
"ds_read_b128 a[220:223], v29 offset:47104\n" \
"s_waitcnt lgkmcnt(15)\n" \
"v_mfma_f32_16x16x32_bf16 a[0:3], a[160:163], a[128:131], a[0:3]\n" \
"s_add_u32 m0, s30, 0x1000\n" \
"s_addk_i32 s84, 0x3000\n" \
"v_mfma_f32_16x16x32_bf16 a[0:3], a[224:227], a[192:195], a[0:3]\n" \
"buffer_load_dwordx4 v24, s[76:79], s84 offen offset:0 lds\n" \
"v_mfma_f32_16x16x32_bf16 a[4:7], a[164:167], a[128:131], a[4:7]\n" \
"ds_read_b64_tr_b16 a[176:177], v36 offset:0\n" \
"v_cvt_pk_bf16_f32 v194, v180, v181\n" \
"v_mfma_f32_16x16x32_bf16 a[4:7], a[228:231], a[192:195], a[4:7]\n" \
"ds_read_b64_tr_b16 a[178:179], v36 offset:1024\n" \
"v_cvt_pk_bf16_f32 v195, v182, v183\n" \
"v_mfma_f32_16x16x32_bf16 a[12:15], a[172:175], a[128:131], a[12:15]\n" \
"ds_read_b64_tr_b16 a[240:241], v36 offset:8192\n" \
"v_accvgpr_read_b32 v188, a88\n" \
"s_waitcnt lgkmcnt(15)\n" \
"v_mfma_f32_16x16x32_bf16 a[12:15], a[236:239], a[192:195], a[12:15]\n" \
"s_add_u32 s88, s42, 0x1000\n" \
"buffer_store_dwordx4 v[252:255], v20, s[80:83], s88 offen offset:2048 sc1\n" \
"s_waitcnt lgkmcnt(13)\n" \
"v_mfma_f32_16x16x32_bf16 a[8:11], a[128:131], a[168:171], a[8:11]\n" \
"ds_read_b64_tr_b16 a[242:243], v36 offset:9216\n" \
"v_accvgpr_read_b32 v189, a89\n" \
"s_waitcnt lgkmcnt(12)\n" \
"s_nop 0\n" \
"v_mfma_f32_16x16x32_bf16 a[8:11], a[192:195], a[232:235], a[8:11]\n" \
"ds_read_b64_tr_b16 a[180:181], v37 offset:0\n" \
"v_accvgpr_read_b32 v190, a90\n" \
"v_mfma_f32_16x16x32_bf16 a[24:27], a[132:135], a[168:171], a[24:27]\n" \
"ds_read_b64_tr_b16 a[182:183], v37 offset:1024\n" \
"v_accvgpr_read_b32 v191, a91\n" \
"v_mfma_f32_16x16x32_bf16 a[24:27], a[196:199], a[232:235], a[24:27]\n" \
"ds_read_b64_tr_b16 a[244:245], v37 offset:8192\n" \
"v_accvgpr_read_b32 v180, a92\n" \
"v_mfma_f32_16x16x32_bf16 a[28:31], a[172:175], a[132:135], a[28:31]\n" \
"v_accvgpr_read_b32 v181, a93\n" \
"v_accvgpr_read_b32 v182, a94\n" \
"v_mfma_f32_16x16x32_bf16 a[28:31], a[236:239], a[196:199], a[28:31]\n" \
"s_addk_i32 s84, 0x2000\n" \
"s_nop 0\n" \
"v_mfma_f32_16x16x32_bf16 a[20:23], a[164:167], a[132:135], a[20:23]\n" \
"buffer_load_dwordx4 v25, s[76:79], s84 offen offset:1024 lds\n" \
"v_mfma_f32_16x16x32_bf16 a[20:23], a[228:231], a[196:199], a[20:23]\n" \
"ds_read_b64_tr_b16 a[246:247], v37 offset:9216\n" \
"v_accvgpr_read_b32 v183, a95\n" \
"v_mfma_f32_16x16x32_bf16 a[16:19], a[160:163], a[132:135], a[16:19]\n" \
"ds_read_b64_tr_b16 a[184:185], v38 offset:0\n" \
"v_cvt_pk_bf16_f32 v188, v188, v189\n" \
"v_mfma_f32_16x16x32_bf16 a[16:19], a[224:227], a[196:199], a[16:19]\n" \
"ds_read_b64_tr_b16 a[186:187], v38 offset:1024\n" \
"v_cvt_pk_bf16_f32 v189, v190, v191\n" \
"v_mfma_f32_16x16x32_bf16 a[32:35], a[160:163], a[136:139], a[32:35]\n" \
"s_addk_i32 s84, 0x2000\n" \
"s_nop 0\n" \
"v_mfma_f32_16x16x32_bf16 a[32:35], a[224:227], a[200:203], a[32:35]\n" \
"buffer_load_dwordx4 v24, s[76:79], s84 offen offset:2048 lds\n" \
"v_mfma_f32_16x16x32_bf16 a[36:39], a[164:167], a[136:139], a[36:39]\n" \
"ds_read_b64_tr_b16 a[248:249], v38 offset:8192\n" \
"v_cvt_pk_bf16_f32 v190, v180, v181\n" \
"v_mfma_f32_16x16x32_bf16 a[36:39], a[228:231], a[200:203], a[36:39]\n" \
"ds_read_b64_tr_b16 a[250:251], v38 offset:9216\n" \
"v_cvt_pk_bf16_f32 v191, v182, v183\n" \
"v_mfma_f32_16x16x32_bf16 a[44:47], a[172:175], a[136:139], a[44:47]\n" \
"ds_read_b64_tr_b16 a[188:189], v39 offset:0\n" \
"v_accvgpr_read_b32 v184, a72\n" \
"v_mfma_f32_16x16x32_bf16 a[44:47], a[236:239], a[200:203], a[44:47]\n" \
"ds_read_b64_tr_b16 a[190:191], v39 offset:1024\n" \
"v_accvgpr_read_b32 v185, a73\n" \
"v_mfma_f32_16x16x32_bf16 a[40:43], a[136:139], a[168:171], a[40:43]\n" \
"s_add_u32 s89, s42, 0x2000\n" \
"buffer_store_dwordx4 v[200:203], v20, s[80:83], s89 offen offset:2048 sc1\n" \
"v_mfma_f32_16x16x32_bf16 a[40:43], a[200:203], a[232:235], a[40:43]\n" \
"ds_read_b64_tr_b16 a[252:253], v39 offset:8192\n" \
".fill (L_pb + 28384 - .) / 4, 4, 0xBF800000\n" \
"L_pp7:\n" \
"s_cbranch_execz L_pret\n" \
"s_nop 0\n" \
"v_accvgpr_read_b32 v186, a74\n" \
"v_mfma_f32_16x16x32_bf16 a[56:59], a[140:143], a[168:171], a[56:59]\n" \
"ds_read_b64_tr_b16 a[254:255], v39 offset:9216\n" \
"v_accvgpr_read_b32 v187, a75\n" \
"v_mfma_f32_16x16x32_bf16 a[56:59], a[204:207], a[232:235], a[56:59]\n" \
"v_accvgpr_read_b32 v180, a76\n" \
"v_accvgpr_read_b32 v181, a77\n" \
"v_mfma_f32_16x16x32_bf16 a[60:63], a[172:175], a[140:143], a[60:63]\n" \
"v_accvgpr_read_b32 v182, a78\n" \
"v_accvgpr_read_b32 v183, a79\n" \
"v_mfma_f32_16x16x32_bf16 a[60:63], a[236:239], a[204:207], a[60:63]\n" \
"s_addk_i32 s84, 0x2000\n" \
"v_mfma_f32_16x16x32_bf16 a[52:55], a[164:167], a[140:143], a[52:55]\n" \
"buffer_load_dwordx4 v25, s[76:79], s84 offen offset:3072 lds\n" \
"v_mfma_f32_16x16x32_bf16 a[52:55], a[228:231], a[204:207], a[52:55]\n" \
"v_cvt_pk_bf16_f32 v184, v184, v185\n" \
"v_cvt_pk_bf16_f32 v185, v186, v187\n" \
"v_mfma_f32_16x16x32_bf16 a[48:51], a[160:163], a[140:143], a[48:51]\n" \
"v_cvt_pk_bf16_f32 v186, v180, v181\n" \
"v_cvt_pk_bf16_f32 v187, v182, v183\n" \
"v_mfma_f32_16x16x32_bf16 a[48:51], a[224:227], a[204:207], a[48:51]\n" \
"s_sub_i32 s14, s9, s14\n" \
"s_add_i32 s20, s19, s13\n" \
"s_waitcnt lgkmcnt(15)\n" \
"v_mfma_f32_16x16x32_bf16 a[64:67], a[164:167], v[248:251], a[64:67]\n" \
"v_writelane_b32 v11, s20, 31\n" \
"s_add_i32 s19, s19, s15\n" \
"v_mfma_f32_16x16x32_bf16 a[64:67], a[228:231], v[48:51], a[64:67]\n" \
"s_mul_i32 s14, s14, 0x24800\n" \
"v_writelane_b32 v11, s19, 32\n" \
"v_mfma_f32_16x16x32_bf16 a[80:83], a[172:175], v[248:251], a[80:83]\n" \
"s_add_i32 s19, s14, 0x4120800\n" \
"s_add_i32 s16, s19, s16\n" \
"v_mfma_f32_16x16x32_bf16 a[80:83], a[236:239], v[48:51], a[80:83]\n" \
"v_writelane_b32 v11, s16, 33\n" \
"s_add_i32 s13, s19, s13\n" \
"v_mfma_f32_16x16x32_bf16 a[68:71], v[248:251], a[168:171], a[68:71]\n" \
"v_writelane_b32 v11, s13, 34\n" \
"s_add_i32 s19, s19, s15\n" \
"v_mfma_f32_16x16x32_bf16 a[68:71], v[48:51], a[232:235], a[68:71]\n" \
"s_waitcnt vmcnt(11) lgkmcnt(0)\n" \
"s_barrier\n" \
"v_mov_b32_e32 v45, s62\n" \
"s_mov_b64 exec, 1\n" \
"buffer_store_dword v45, off, s[80:83], s37 offset:0 sc1\n" \
"s_mov_b64 exec, -1\n" \
"v_xor_b32_e32 v28, 0x20000, v28\n" \
"v_xor_b32_e32 v29, 0x20000, v29\n" \
"v_mfma_f32_16x16x32_bf16 v[52:55], a[176:179], a[128:131], v[52:55]\n" \
"s_add_u32 m0, s40, 0x0\n" \
"s_add_u32 s85, s39, 0xc000\n" \
"v_mfma_f32_16x16x32_bf16 v[52:55], a[240:243], a[192:195], v[52:55]\n" \
"buffer_load_dwordx4 v20, s[80:83], s85 offen sc1 lds\n" \
"v_mfma_f32_16x16x32_bf16 v[56:59], a[180:183], a[128:131], v[56:59]\n" \
"ds_read_b64_tr_b16 a[160:161], v32 offset:32768\n" \
"s_or_b32 s13, s18, 0x3000000\n" \
"v_mfma_f32_16x16x32_bf16 v[56:59], a[244:247], a[192:195], v[56:59]\n" \
"ds_read_b64_tr_b16 a[162:163], v32 offset:33280\n" \
"s_lshl_b32 s15, s6, 11\n" \
"v_mfma_f32_16x16x32_bf16 v[60:63], a[184:187], a[128:131], v[60:63]\n" \
"s_add_u32 m0, s40, 0x1000\n" \
"s_add_u32 s85, s39, 0xd000\n" \
"v_mfma_f32_16x16x32_bf16 v[60:63], a[248:251], a[192:195], v[60:63]\n" \
"buffer_load_dwordx4 v20, s[80:83], s85 offen sc1 lds\n" \
"v_mfma_f32_16x16x32_bf16 v[64:67], a[188:191], a[128:131], v[64:67]\n" \
"ds_read_b64_tr_b16 a[224:225], v32 offset:36864\n" \
"v_writelane_b32 v11, s19, 35\n" \
"v_mfma_f32_16x16x32_bf16 v[64:67], a[252:255], a[192:195], v[64:67]\n" \
"ds_read_b128 a[128:131], v28 offset:0\n" \
"s_add_i32 s16, s13, s15\n" \
"v_mfma_f32_16x16x32_bf16 v[80:83], a[188:191], a[132:135], v[80:83]\n" \
"ds_read_b128 a[192:195], v29 offset:0\n" \
"v_writelane_b32 v11, s16, 36\n" \
"v_mfma_f32_16x16x32_bf16 v[80:83], a[252:255], a[196:199], v[80:83]\n" \
"s_add_u32 m0, s40, 0x3000\n" \
"s_movk_i32 s84, 0x300\n" \
"v_mfma_f32_16x16x32_bf16 v[76:79], a[184:187], a[132:135], v[76:79]\n" \
"buffer_load_dwordx4 v21, s[76:79], s84 offen offset:0 lds\n" \
"v_mfma_f32_16x16x32_bf16 v[76:79], a[248:251], a[196:199], v[76:79]\n" \
"ds_read_b64_tr_b16 a[226:227], v32 offset:37376\n" \
"s_add_i32 s16, s14, 0x411c000\n" \
"v_mfma_f32_16x16x32_bf16 v[72:75], a[180:183], a[132:135], v[72:75]\n" \
"ds_read_b64_tr_b16 a[164:165], v33 offset:32768\n" \
"s_add_i32 s19, s16, s15\n" \
"v_mfma_f32_16x16x32_bf16 v[72:75], a[244:247], a[196:199], v[72:75]\n" \
"v_writelane_b32 v11, s19, 37\n" \
"s_add_i32 s18, s18, 0x3004000\n" \
"v_mfma_f32_16x16x32_bf16 v[68:71], a[176:179], a[132:135], v[68:71]\n" \
"ds_read_b64_tr_b16 a[166:167], v33 offset:33280\n" \
"v_writelane_b32 v11, s18, 38\n" \
"v_mfma_f32_16x16x32_bf16 v[68:71], a[240:243], a[196:199], v[68:71]\n" \
"ds_read_b128 a[132:135], v28 offset:2048\n" \
"s_add_i32 s14, s14, 0x4120000\n" \
"v_mfma_f32_16x16x32_bf16 v[84:87], a[176:179], a[136:139], v[84:87]\n" \
"ds_read_b128 a[196:199], v29 offset:2048\n" \
"v_writelane_b32 v11, s14, 39\n" \
"v_mfma_f32_16x16x32_bf16 v[84:87], a[240:243], a[200:203], v[84:87]\n" \
"s_add_u32 m0, s31, 0x0\n" \
"s_mov_b32 s84, 0x300000\n" \
"v_mfma_f32_16x16x32_bf16 v[88:91], a[180:183], a[136:139], v[88:91]\n" \
"buffer_load_dwordx4 v26, s[68:71], s84 offen offset:0 lds\n" \
"v_mfma_f32_16x16x32_bf16 v[88:91], a[244:247], a[200:203], v[88:91]\n" \
"s_add_u32 s88, s42, 0x2000\n" \
"buffer_store_dwordx4 v[204:207], v20, s[80:83], s88 offen offset:3072 sc1\n" \
"v_mfma_f32_16x16x32_bf16 v[92:95], a[184:187], a[136:139], v[92:95]\n" \
"ds_read_b64_tr_b16 a[228:229], v33 offset:36864\n" \
"s_lshl_b32 s14, s4, 2\n" \
"v_mfma_f32_16x16x32_bf16 v[92:95], a[248:251], a[200:203], v[92:95]\n" \
"ds_read_b64_tr_b16 a[230:231], v33 offset:37376\n" \
"s_or_b32 s8, s14, s8\n" \
"v_mfma_f32_16x16x32_bf16 v[96:99], a[188:191], a[136:139], v[96:99]\n" \
"s_mul_i32 s8, s8, 0x90000\n" \
"s_lshl_b32 s14, s3, 14\n" \
"v_mfma_f32_16x16x32_bf16 v[96:99], a[252:255], a[200:203], v[96:99]\n" \
"ds_read_b128 a[136:139], v28 offset:4096\n" \
"s_add_i32 s8, s8, s14\n" \
"v_mfma_f32_16x16x32_bf16 v[112:115], a[188:191], a[140:143], v[112:115]\n" \
"ds_read_b128 a[200:203], v29 offset:4096\n" \
"s_lshl_b32 s9, s9, 4\n" \
"v_mfma_f32_16x16x32_bf16 v[112:115], a[252:255], a[204:207], v[112:115]\n" \
"s_addk_i32 s84, 0x7c00\n" \
"v_mfma_f32_16x16x32_bf16 v[108:111], a[184:187], a[140:143], v[108:111]\n" \
"buffer_load_dwordx4 v26, s[68:71], s84 offen offset:1024 lds\n" \
"v_mfma_f32_16x16x32_bf16 v[108:111], a[248:251], a[204:207], v[108:111]\n" \
"ds_read_b64_tr_b16 a[172:173], v35 offset:32768\n" \
"s_lshl_b32 s14, s4, 7\n" \
"v_mfma_f32_16x16x32_bf16 v[104:107], a[180:183], a[140:143], v[104:107]\n" \
"s_addk_i32 s84, 0x7c00\n" \
"v_mfma_f32_16x16x32_bf16 v[104:107], a[244:247], a[204:207], v[104:107]\n" \
"buffer_load_dwordx4 v27, s[68:71], s84 offen offset:2048 lds\n" \
"v_mfma_f32_16x16x32_bf16 v[100:103], a[176:179], a[140:143], v[100:103]\n" \
"ds_read_b64_tr_b16 a[174:175], v35 offset:33280\n" \
"s_or_b32 s18, s9, s14\n" \
"v_mfma_f32_16x16x32_bf16 v[100:103], a[240:243], a[204:207], v[100:103]\n" \
"ds_read_b128 a[140:143], v28 offset:6144\n" \
"s_sub_i32 s9, s9, s14\n" \
"v_mfma_f32_16x16x32_bf16 v[116:119], a[176:179], a[144:147], v[116:119]\n" \
"ds_read_b128 a[204:207], v29 offset:6144\n" \
"s_mulk_i32 s18, 0x480\n" \
"v_mfma_f32_16x16x32_bf16 v[116:119], a[240:243], a[208:211], v[116:119]\n" \
"s_addk_i32 s84, 0x7c00\n" \
"v_mfma_f32_16x16x32_bf16 v[120:123], a[180:183], a[144:147], v[120:123]\n" \
"buffer_load_dwordx4 v27, s[68:71], s84 offen offset:3072 lds\n" \
"v_mfma_f32_16x16x32_bf16 v[120:123], a[244:247], a[208:211], v[120:123]\n" \
"ds_read_b64_tr_b16 a[236:237], v35 offset:36864\n" \
"s_mulk_i32 s9, 0x480\n" \
"v_mfma_f32_16x16x32_bf16 v[124:127], a[184:187], a[144:147], v[124:127]\n" \
"ds_read_b64_tr_b16 a[238:239], v35 offset:37376\n" \
"s_or_b32 s14, s18, s12\n" \
"v_mfma_f32_16x16x32_bf16 v[124:127], a[248:251], a[208:211], v[124:127]\n" \
"s_add_u32 m0, s30, 0x8000\n" \
"s_movk_i32 s84, 0x380\n" \
"v_mfma_f32_16x16x32_bf16 v[128:131], a[188:191], a[144:147], v[128:131]\n" \
"buffer_load_dwordx4 v24, s[76:79], s84 offen offset:0 lds\n" \
"v_mfma_f32_16x16x32_bf16 v[128:131], a[252:255], a[208:211], v[128:131]\n" \
"ds_read_b128 a[144:147], v28 offset:8192\n" \
"s_or_b32 s9, s9, s12\n" \
"v_mfma_f32_16x16x32_bf16 v[144:147], a[188:191], a[148:151], v[144:147]\n" \
"ds_read_b128 a[208:211], v29 offset:8192\n" \
"s_and_b32 s12, s2, 1\n" \
"v_mfma_f32_16x16x32_bf16 v[144:147], a[252:255], a[212:215], v[144:147]\n" \
"ds_read_b64_tr_b16 a[168:169], v34 offset:32768\n" \
"v_mfma_f32_16x16x32_bf16 v[140:143], a[184:187], a[148:151], v[140:143]\n" \
"s_movk_i32 s84, 0x2380\n" \
"s_nop 0\n" \
"v_mfma_f32_16x16x32_bf16 v[140:143], a[248:251], a[212:215], v[140:143]\n" \
"buffer_load_dwordx4 v25, s[76:79], s84 offen offset:1024 lds\n" \
"v_mfma_f32_16x16x32_bf16 v[136:139], a[180:183], a[148:151], v[136:139]\n" \
"ds_read_b64_tr_b16 a[170:171], v34 offset:33280\n" \
"v_mfma_f32_16x16x32_bf16 v[136:139], a[244:247], a[212:215], v[136:139]\n" \
"ds_read_b64_tr_b16 a[232:233], v34 offset:36864\n" \
"v_mfma_f32_16x16x32_bf16 v[132:135], a[176:179], a[148:151], v[132:135]\n" \
"ds_read_b64_tr_b16 a[234:235], v34 offset:37376\n" \
"v_mfma_f32_16x16x32_bf16 v[132:135], a[240:243], a[212:215], v[132:135]\n" \
"ds_read_b128 a[148:151], v28 offset:10240\n" \
"v_mfma_f32_16x16x32_bf16 v[148:151], a[176:179], a[152:155], v[148:151]\n" \
"ds_read_b128 a[212:215], v29 offset:10240\n" \
"v_mfma_f32_16x16x32_bf16 v[148:151], a[240:243], a[216:219], v[148:151]\n" \
"s_movk_i32 s84, 0x4380\n" \
"v_mfma_f32_16x16x32_bf16 v[152:155], a[180:183], a[152:155], v[152:155]\n" \
"buffer_load_dwordx4 v24, s[76:79], s84 offen offset:2048 lds\n" \
"v_mfma_f32_16x16x32_bf16 v[152:155], a[244:247], a[216:219], v[152:155]\n" \
"s_movk_i32 s84, 0x6380\n" \
"v_mfma_f32_16x16x32_bf16 v[156:159], a[184:187], a[152:155], v[156:159]\n" \
"buffer_load_dwordx4 v25, s[76:79], s84 offen offset:3072 lds\n" \
"v_mfma_f32_16x16x32_bf16 v[156:159], a[248:251], a[216:219], v[156:159]\n" \
"ds_read_b128 v[248:251], v22 offset:32768\n" \
"v_mfma_f32_16x16x32_bf16 v[160:163], a[188:191], a[152:155], v[160:163]\n" \
"ds_read_b128 v[48:51], v23 offset:32768\n" \
"v_mfma_f32_16x16x32_bf16 v[160:163], a[252:255], a[216:219], v[160:163]\n" \
"ds_read_b128 a[152:155], v28 offset:12288\n" \
"v_mfma_f32_16x16x32_bf16 v[176:179], a[188:191], a[156:159], v[176:179]\n" \
"ds_read_b128 a[216:219], v29 offset:12288\n" \
"v_mfma_f32_16x16x32_bf16 v[176:179], a[252:255], a[220:223], v[176:179]\n" \
"s_cmp_lg_u32 s12, 0\n" \
"s_cselect_b32 s13, s16, s13\n" \
"v_mfma_f32_16x16x32_bf16 v[172:175], a[184:187], a[156:159], v[172:175]\n" \
"s_bfe_u32 s16, s2, 0x20001\n" \
"s_addk_i32 s13, 0x4000\n" \
"v_mfma_f32_16x16x32_bf16 v[172:175], a[248:251], a[220:223], v[172:175]\n" \
"s_xor_b32 s16, s16, s3\n" \
"v_mfma_f32_16x16x32_bf16 v[168:171], a[180:183], a[156:159], v[168:171]\n" \
"s_cmp_lg_u32 s16, 0\n" \
"s_cselect_b32 s13, s7, s13\n" \
"v_mfma_f32_16x16x32_bf16 v[168:171], a[244:247], a[220:223], v[168:171]\n" \
"s_lshl_b32 s6, s6, 16\n" \
"v_writelane_b32 v11, s13, 40\n" \
"v_mfma_f32_16x16x32_bf16 v[164:167], a[176:179], a[156:159], v[164:167]\n" \
"s_add_i32 s13, s8, 0x4240000\n" \
"s_add_i32 s6, s13, s6\n" \
"v_mfma_f32_16x16x32_bf16 v[164:167], a[240:243], a[220:223], v[164:167]\n" \
"ds_read_b128 a[156:159], v28 offset:14336\n" \
"ds_read_b128 a[220:223], v29 offset:14336\n" \
"s_waitcnt lgkmcnt(15)\n" \
"v_mfma_f32_16x16x32_bf16 a[0:3], a[160:163], a[128:131], a[0:3]\n" \
"s_add_u32 m0, s30, 0x9000\n" \
"s_addk_i32 s84, 0x3000\n" \
"v_mfma_f32_16x16x32_bf16 a[0:3], a[224:227], a[192:195], a[0:3]\n" \
"buffer_load_dwordx4 v24, s[76:79], s84 offen offset:0 lds\n" \
"v_mfma_f32_16x16x32_bf16 a[4:7], a[164:167], a[128:131], a[4:7]\n" \
"ds_read_b64_tr_b16 a[176:177], v36 offset:32768\n" \
"v_writelane_b32 v11, s6, 41\n" \
"v_mfma_f32_16x16x32_bf16 a[4:7], a[228:231], a[192:195], a[4:7]\n" \
"ds_read_b64_tr_b16 a[178:179], v36 offset:33792\n" \
"s_add_i32 s8, s8, 0x42c0000\n" \
"v_mfma_f32_16x16x32_bf16 a[12:15], a[172:175], a[128:131], a[12:15]\n" \
"ds_read_b64_tr_b16 a[240:241], v36 offset:40960\n" \
"s_lshl_b32 s6, s12, 10\n" \
"s_waitcnt lgkmcnt(15)\n" \
"v_mfma_f32_16x16x32_bf16 a[12:15], a[236:239], a[192:195], a[12:15]\n" \
"s_add_u32 s89, s42, 0x3000\n" \
"buffer_store_dwordx4 v[208:211], v20, s[80:83], s89 offen offset:3072 sc1\n" \
"s_waitcnt lgkmcnt(13)\n" \
"v_mfma_f32_16x16x32_bf16 a[8:11], a[128:131], a[168:171], a[8:11]\n" \
"ds_read_b64_tr_b16 a[242:243], v36 offset:41984\n" \
"s_or_b32 s6, s8, s6\n" \
"s_waitcnt lgkmcnt(12)\n" \
"v_mfma_f32_16x16x32_bf16 a[8:11], a[192:195], a[232:235], a[8:11]\n" \
"ds_read_b64_tr_b16 a[180:181], v37 offset:32768\n" \
"v_mfma_f32_16x16x32_bf16 a[24:27], a[132:135], a[168:171], a[24:27]\n" \
"ds_read_b64_tr_b16 a[182:183], v37 offset:33792\n" \
"v_mfma_f32_16x16x32_bf16 a[24:27], a[196:199], a[232:235], a[24:27]\n" \
"ds_read_b64_tr_b16 a[244:245], v37 offset:40960\n" \
"v_mfma_f32_16x16x32_bf16 a[28:31], a[172:175], a[132:135], a[28:31]\n" \
"s_cmp_lg_u32 s16, 0\n" \
"s_cselect_b32 s6, s7, s6\n" \
"v_mfma_f32_16x16x32_bf16 a[28:31], a[236:239], a[196:199], a[28:31]\n" \
"s_addk_i32 s84, 0x2000\n" \
"v_mfma_f32_16x16x32_bf16 a[20:23], a[164:167], a[132:135], a[20:23]\n" \
"buffer_load_dwordx4 v25, s[76:79], s84 offen offset:1024 lds\n" \
"v_mfma_f32_16x16x32_bf16 a[20:23], a[228:231], a[196:199], a[20:23]\n" \
"ds_read_b64_tr_b16 a[246:247], v37 offset:41984\n" \
"s_mulk_i32 s4, 0x4800\n" \
"v_mfma_f32_16x16x32_bf16 a[16:19], a[160:163], a[132:135], a[16:19]\n" \
"ds_read_b64_tr_b16 a[184:185], v38 offset:32768\n" \
"v_writelane_b32 v11, s6, 42\n" \
"v_mfma_f32_16x16x32_bf16 a[16:19], a[224:227], a[196:199], a[16:19]\n" \
"ds_read_b64_tr_b16 a[186:187], v38 offset:33792\n" \
"s_add_i32 s6, s14, s10\n" \
"v_mfma_f32_16x16x32_bf16 a[32:35], a[160:163], a[136:139], a[32:35]\n" \
"s_addk_i32 s84, 0x2000\n" \
"v_mfma_f32_16x16x32_bf16 a[32:35], a[224:227], a[200:203], a[32:35]\n" \
"buffer_load_dwordx4 v24, s[76:79], s84 offen offset:2048 lds\n" \
"v_mfma_f32_16x16x32_bf16 a[36:39], a[164:167], a[136:139], a[36:39]\n" \
"ds_read_b64_tr_b16 a[248:249], v38 offset:40960\n" \
"v_writelane_b32 v11, s6, 43\n" \
"v_mfma_f32_16x16x32_bf16 a[36:39], a[228:231], a[200:203], a[36:39]\n" \
"ds_read_b64_tr_b16 a[250:251], v38 offset:41984\n" \
"s_add_i32 s6, s9, 0x21c000\n" \
"v_mfma_f32_16x16x32_bf16 a[44:47], a[172:175], a[136:139], a[44:47]\n" \
"ds_read_b64_tr_b16 a[188:189], v39 offset:32768\n" \
"s_add_i32 s9, s6, s10\n" \
"v_mfma_f32_16x16x32_bf16 a[44:47], a[236:239], a[200:203], a[44:47]\n" \
"ds_read_b64_tr_b16 a[190:191], v39 offset:33792\n" \
"v_mfma_f32_16x16x32_bf16 a[40:43], a[136:139], a[168:171], a[40:43]\n" \
"s_add_u32 s88, s42, 0x3000\n" \
"buffer_store_dwordx4 v[212:215], v20, s[80:83], s88 offen offset:2048 sc1\n" \
"v_mfma_f32_16x16x32_bf16 a[40:43], a[200:203], a[232:235], a[40:43]\n" \
"ds_read_b64_tr_b16 a[252:253], v39 offset:40960\n" \
"v_mfma_f32_16x16x32_bf16 a[56:59], a[140:143], a[168:171], a[56:59]\n" \
"ds_read_b64_tr_b16 a[254:255], v39 offset:41984\n" \
"v_mfma_f32_16x16x32_bf16 a[56:59], a[204:207], a[232:235], a[56:59]\n" \
"s_cmp_lg_u32 s12, 0\n" \
"s_cselect_b32 s6, s6, s14\n" \
"v_mfma_f32_16x16x32_bf16 a[60:63], a[172:175], a[140:143], a[60:63]\n" \
"s_addk_i32 s6, 0x100\n" \
"v_writelane_b32 v11, s9, 44\n" \
"v_mfma_f32_16x16x32_bf16 a[60:63], a[236:239], a[204:207], a[60:63]\n" \
"s_addk_i32 s84, 0x2000\n" \
"v_mfma_f32_16x16x32_bf16 a[52:55], a[164:167], a[140:143], a[52:55]\n" \
"buffer_load_dwordx4 v25, s[76:79], s84 offen offset:3072 lds\n" \
"v_mfma_f32_16x16x32_bf16 a[52:55], a[228:231], a[204:207], a[52:55]\n" \
"s_cmp_lg_u32 s16, 0\n" \
"s_cselect_b32 s6, s7, s6\n" \
"v_mfma_f32_16x16x32_bf16 a[48:51], a[160:163], a[140:143], a[48:51]\n" \
"v_writelane_b32 v11, s6, 45\n" \
"s_or_b32 s6, s8, s15\n" \
"v_mfma_f32_16x16x32_bf16 a[48:51], a[224:227], a[204:207], a[48:51]\n" \
"v_writelane_b32 v11, s6, 53\n" \
"s_lshl_b32 s6, s3, 17\n" \
"s_waitcnt lgkmcnt(15)\n" \
"v_mfma_f32_16x16x32_bf16 a[64:67], a[164:167], v[248:251], a[64:67]\n" \
"s_add_i32 s6, s13, s6\n" \
"s_or_b32 s5, s6, s5\n" \
"v_mfma_f32_16x16x32_bf16 a[64:67], a[228:231], v[48:51], a[64:67]\n" \
"s_or_b32 s5, s5, 0x3840\n" \
"v_writelane_b32 v11, s5, 6\n" \
"v_mfma_f32_16x16x32_bf16 a[80:83], a[172:175], v[248:251], a[80:83]\n" \
"s_mov_b32 s5, 0xff0000\n" \
"v_mfma_f32_16x16x32_bf16 a[80:83], a[236:239], v[48:51], a[80:83]\n" \
"s_cmp_lg_u32 s16, 0\n" \
"s_cselect_b32 s5, s17, s5\n" \
"v_mfma_f32_16x16x32_bf16 a[68:71], v[248:251], a[168:171], a[68:71]\n" \
"v_writelane_b32 v11, s5, 7\n" \
"s_mul_i32 s5, s3, 0x1200\n" \
"v_mfma_f32_16x16x32_bf16 a[68:71], v[48:51], a[232:235], a[68:71]\n" \
"s_waitcnt vmcnt(10) lgkmcnt(0)\n" \
"s_barrier\n" \
"v_xor_b32_e32 v28, 0x20000, v28\n" \
"v_xor_b32_e32 v29, 0x20000, v29\n" \
"v_mfma_f32_16x16x32_bf16 v[52:55], a[176:179], a[128:131], v[52:55]\n" \
"s_add_u32 m0, s40, 0x8000\n" \
"s_add_u32 s85, s39, 0xe000\n" \
"v_mfma_f32_16x16x32_bf16 v[52:55], a[240:243], a[192:195], v[52:55]\n" \
"buffer_load_dwordx4 v20, s[80:83], s85 offen sc1 lds\n" \
"v_mfma_f32_16x16x32_bf16 v[56:59], a[180:183], a[128:131], v[56:59]\n" \
"ds_read_b64_tr_b16 a[160:161], v32 offset:0\n" \
"s_add_i32 s4, s5, s4\n" \
"v_mfma_f32_16x16x32_bf16 v[56:59], a[244:247], a[192:195], v[56:59]\n" \
"ds_read_b64_tr_b16 a[162:163], v32 offset:512\n" \
"s_add_i32 s4, s4, s11\n" \
"v_mfma_f32_16x16x32_bf16 v[60:63], a[184:187], a[128:131], v[60:63]\n" \
"s_add_u32 m0, s40, 0x9000\n" \
"s_add_u32 s85, s39, 0xf000\n" \
"v_mfma_f32_16x16x32_bf16 v[60:63], a[248:251], a[192:195], v[60:63]\n" \
"buffer_load_dwordx4 v20, s[80:83], s85 offen sc1 lds\n" \
"v_mfma_f32_16x16x32_bf16 v[64:67], a[188:191], a[128:131], v[64:67]\n" \
"ds_read_b64_tr_b16 a[224:225], v32 offset:4096\n" \
"v_writelane_b32 v11, s4, 8\n" \
"v_mfma_f32_16x16x32_bf16 v[64:67], a[252:255], a[192:195], v[64:67]\n" \
"ds_read_b128 a[128:131], v28 offset:0\n" \
"s_lshl_b32 s4, s2, 16\n" \
"v_mfma_f32_16x16x32_bf16 v[80:83], a[188:191], a[132:135], v[80:83]\n" \
"ds_read_b128 a[192:195], v29 offset:0\n" \
"s_add_i32 s5, s4, 0x10000\n" \
"v_mfma_f32_16x16x32_bf16 v[80:83], a[252:255], a[196:199], v[80:83]\n" \
"s_add_u32 m0, s40, 0xb000\n" \
"s_movk_i32 s84, 0x380\n" \
"v_mfma_f32_16x16x32_bf16 v[76:79], a[184:187], a[132:135], v[76:79]\n" \
"buffer_load_dwordx4 v21, s[76:79], s84 offen offset:0 lds\n" \
"v_mfma_f32_16x16x32_bf16 v[76:79], a[248:251], a[196:199], v[76:79]\n" \
"ds_read_b64_tr_b16 a[226:227], v32 offset:4608\n" \
"s_and_b32 s5, s5, 0x70000\n" \
"v_mfma_f32_16x16x32_bf16 v[72:75], a[180:183], a[132:135], v[72:75]\n" \
"ds_read_b64_tr_b16 a[164:165], v33 offset:0\n" \
"s_add_i32 s5, s13, s5\n" \
"v_mfma_f32_16x16x32_bf16 v[72:75], a[244:247], a[196:199], v[72:75]\n" \
"v_writelane_b32 v11, s5, 46\n" \
"s_add_i32 s5, s4, 0x20000\n" \
"v_mfma_f32_16x16x32_bf16 v[68:71], a[176:179], a[132:135], v[68:71]\n" \
"ds_read_b64_tr_b16 a[166:167], v33 offset:512\n" \
"s_and_b32 s5, s5, 0x70000\n" \
"v_mfma_f32_16x16x32_bf16 v[68:71], a[240:243], a[196:199], v[68:71]\n" \
"ds_read_b128 a[132:135], v28 offset:2048\n" \
"s_add_i32 s5, s13, s5\n" \
"v_mfma_f32_16x16x32_bf16 v[84:87], a[176:179], a[136:139], v[84:87]\n" \
"ds_read_b128 a[196:199], v29 offset:2048\n" \
"s_bitset1_b32 s5, 11\n" \
"v_mfma_f32_16x16x32_bf16 v[84:87], a[240:243], a[200:203], v[84:87]\n" \
"s_add_u32 m0, s31, 0x8000\n" \
"s_mov_b32 s84, 0x380000\n" \
"v_mfma_f32_16x16x32_bf16 v[88:91], a[180:183], a[136:139], v[88:91]\n" \
"buffer_load_dwordx4 v26, s[68:71], s84 offen offset:0 lds\n" \
"v_mfma_f32_16x16x32_bf16 v[88:91], a[244:247], a[200:203], v[88:91]\n" \
"s_add_u32 s89, s42, 0x0\n" \
"buffer_store_dwordx4 v[184:187], v20, s[80:83], s89 offen offset:1024 sc1\n" \
"v_mfma_f32_16x16x32_bf16 v[92:95], a[184:187], a[136:139], v[92:95]\n" \
"ds_read_b64_tr_b16 a[228:229], v33 offset:4096\n" \
"v_writelane_b32 v11, s5, 47\n" \
"v_mfma_f32_16x16x32_bf16 v[92:95], a[248:251], a[200:203], v[92:95]\n" \
"ds_read_b64_tr_b16 a[230:231], v33 offset:4608\n" \
"s_add_i32 s5, s4, 0x30000\n" \
"v_mfma_f32_16x16x32_bf16 v[96:99], a[188:191], a[136:139], v[96:99]\n" \
"s_and_b32 s5, s5, 0x70000\n" \
"s_add_i32 s5, s13, s5\n" \
"v_mfma_f32_16x16x32_bf16 v[96:99], a[252:255], a[200:203], v[96:99]\n" \
"ds_read_b128 a[136:139], v28 offset:4096\n" \
"s_bitset1_b32 s5, 12\n" \
"v_mfma_f32_16x16x32_bf16 v[112:115], a[188:191], a[140:143], v[112:115]\n" \
"ds_read_b128 a[200:203], v29 offset:4096\n" \
"v_writelane_b32 v11, s5, 48\n" \
"v_mfma_f32_16x16x32_bf16 v[112:115], a[252:255], a[204:207], v[112:115]\n" \
"s_addk_i32 s84, 0x7c00\n" \
"v_mfma_f32_16x16x32_bf16 v[108:111], a[184:187], a[140:143], v[108:111]\n" \
"buffer_load_dwordx4 v26, s[68:71], s84 offen offset:1024 lds\n" \
"v_mfma_f32_16x16x32_bf16 v[108:111], a[248:251], a[204:207], v[108:111]\n" \
"ds_read_b64_tr_b16 a[172:173], v35 offset:0\n" \
"s_and_b32 s5, s4, 0x70000\n" \
"v_mfma_f32_16x16x32_bf16 v[104:107], a[180:183], a[140:143], v[104:107]\n" \
"s_addk_i32 s84, 0x7c00\n" \
"v_mfma_f32_16x16x32_bf16 v[104:107], a[244:247], a[204:207], v[104:107]\n" \
"buffer_load_dwordx4 v27, s[68:71], s84 offen offset:2048 lds\n" \
"v_mfma_f32_16x16x32_bf16 v[100:103], a[176:179], a[140:143], v[100:103]\n" \
"ds_read_b64_tr_b16 a[174:175], v35 offset:512\n" \
"s_xor_b32 s5, s5, 0x40000\n" \
"v_mfma_f32_16x16x32_bf16 v[100:103], a[240:243], a[204:207], v[100:103]\n" \
"ds_read_b128 a[140:143], v28 offset:6144\n" \
"s_add_i32 s5, s13, s5\n" \
"v_mfma_f32_16x16x32_bf16 v[116:119], a[176:179], a[144:147], v[116:119]\n" \
"ds_read_b128 a[204:207], v29 offset:6144\n" \
"s_or_b32 s5, s5, 0x1800\n" \
"v_mfma_f32_16x16x32_bf16 v[116:119], a[240:243], a[208:211], v[116:119]\n" \
"s_addk_i32 s84, 0x7c00\n" \
"v_mfma_f32_16x16x32_bf16 v[120:123], a[180:183], a[144:147], v[120:123]\n" \
"buffer_load_dwordx4 v27, s[68:71], s84 offen offset:3072 lds\n" \
"v_mfma_f32_16x16x32_bf16 v[120:123], a[244:247], a[208:211], v[120:123]\n" \
"ds_read_b64_tr_b16 a[236:237], v35 offset:4096\n" \
"v_writelane_b32 v11, s5, 49\n" \
"v_mfma_f32_16x16x32_bf16 v[124:127], a[184:187], a[144:147], v[124:127]\n" \
"ds_read_b64_tr_b16 a[238:239], v35 offset:4608\n" \
"s_add_i32 s5, s4, 0x50000\n" \
"v_mfma_f32_16x16x32_bf16 v[124:127], a[248:251], a[208:211], v[124:127]\n" \
"s_add_u32 m0, s30, 0x20000\n" \
"s_movk_i32 s84, 0x400\n" \
"v_mfma_f32_16x16x32_bf16 v[128:131], a[188:191], a[144:147], v[128:131]\n" \
"buffer_load_dwordx4 v24, s[76:79], s84 offen offset:0 lds\n" \
"v_mfma_f32_16x16x32_bf16 v[128:131], a[252:255], a[208:211], v[128:131]\n" \
"ds_read_b128 a[144:147], v28 offset:8192\n" \
"s_and_b32 s5, s5, 0x70000\n" \
"v_mfma_f32_16x16x32_bf16 v[144:147], a[188:191], a[148:151], v[144:147]\n" \
"ds_read_b128 a[208:211], v29 offset:8192\n" \
"s_add_i32 s5, s13, s5\n" \
"v_mfma_f32_16x16x32_bf16 v[144:147], a[252:255], a[212:215], v[144:147]\n" \
"ds_read_b64_tr_b16 a[168:169], v34 offset:0\n" \
"s_bitset1_b32 s5, 13\n" \
"v_mfma_f32_16x16x32_bf16 v[140:143], a[184:187], a[148:151], v[140:143]\n" \
"s_movk_i32 s84, 0x2400\n" \
"s_nop 0\n" \
"v_mfma_f32_16x16x32_bf16 v[140:143], a[248:251], a[212:215], v[140:143]\n" \
"buffer_load_dwordx4 v25, s[76:79], s84 offen offset:1024 lds\n" \
"v_mfma_f32_16x16x32_bf16 v[136:139], a[180:183], a[148:151], v[136:139]\n" \
"ds_read_b64_tr_b16 a[170:171], v34 offset:512\n" \
"v_writelane_b32 v11, s5, 50\n" \
"v_mfma_f32_16x16x32_bf16 v[136:139], a[244:247], a[212:215], v[136:139]\n" \
"ds_read_b64_tr_b16 a[232:233], v34 offset:4096\n" \
"s_add_i32 s5, s4, 0x60000\n" \
"v_mfma_f32_16x16x32_bf16 v[132:135], a[176:179], a[148:151], v[132:135]\n" \
"ds_read_b64_tr_b16 a[234:235], v34 offset:4608\n" \
"s_and_b32 s5, s5, 0x70000\n" \
"v_mfma_f32_16x16x32_bf16 v[132:135], a[240:243], a[212:215], v[132:135]\n" \
"ds_read_b128 a[148:151], v28 offset:10240\n" \
"s_add_i32 s4, s4, 0x70000\n" \
"v_mfma_f32_16x16x32_bf16 v[148:151], a[176:179], a[152:155], v[148:151]\n" \
"ds_read_b128 a[212:215], v29 offset:10240\n" \
"s_add_i32 s5, s13, s5\n" \
"v_mfma_f32_16x16x32_bf16 v[148:151], a[240:243], a[216:219], v[148:151]\n" \
"s_movk_i32 s84, 0x4400\n" \
"v_mfma_f32_16x16x32_bf16 v[152:155], a[180:183], a[152:155], v[152:155]\n" \
"buffer_load_dwordx4 v24, s[76:79], s84 offen offset:2048 lds\n" \
"v_mfma_f32_16x16x32_bf16 v[152:155], a[244:247], a[216:219], v[152:155]\n" \
"s_movk_i32 s84, 0x6400\n" \
"s_nop 0\n" \
"v_mfma_f32_16x16x32_bf16 v[156:159], a[184:187], a[152:155], v[156:159]\n" \
"buffer_load_dwordx4 v25, s[76:79], s84 offen offset:3072 lds\n" \
"v_mfma_f32_16x16x32_bf16 v[156:159], a[248:251], a[216:219], v[156:159]\n" \
"ds_read_b128 v[248:251], v22 offset:0\n" \
"s_and_b32 s4, s4, 0x70000\n" \
"v_mfma_f32_16x16x32_bf16 v[160:163], a[188:191], a[152:155], v[160:163]\n" \
"ds_read_b128 v[48:51], v23 offset:0\n" \
"s_or_b32 s5, s5, 0x2800\n" \
"v_mfma_f32_16x16x32_bf16 v[160:163], a[252:255], a[216:219], v[160:163]\n" \
"ds_read_b128 a[152:155], v28 offset:12288\n" \
"v_writelane_b32 v11, s5, 51\n" \
"v_mfma_f32_16x16x32_bf16 v[176:179], a[188:191], a[156:159], v[176:179]\n" \
"ds_read_b128 a[216:219], v29 offset:12288\n" \
"s_add_i32 s13, s13, s4\n" \
"v_mfma_f32_16x16x32_bf16 v[176:179], a[252:255], a[220:223], v[176:179]\n" \
"s_or_b32 s4, s13, 0x3000\n" \
"v_writelane_b32 v11, s4, 52\n" \
"v_mfma_f32_16x16x32_bf16 v[172:175], a[184:187], a[156:159], v[172:175]\n" \
"s_movk_i32 s11, 0x4000\n" \
"s_lshl_b32 s4, s2, 2\n" \
"v_mfma_f32_16x16x32_bf16 v[172:175], a[248:251], a[220:223], v[172:175]\n" \
"s_and_b32 s4, s4, 28\n" \
"s_bfe_u32 s5, s2, 0x20003\n" \
"v_mfma_f32_16x16x32_bf16 v[168:171], a[180:183], a[156:159], v[168:171]\n" \
"s_or_b32 s4, s4, s5\n" \
"s_lshr_b32 s5, s2, 5\n" \
"v_mfma_f32_16x16x32_bf16 v[168:171], a[244:247], a[220:223], v[168:171]\n" \
"s_lshr_b32 s19, s3, 1\n" \
"s_lshl_b32 s18, s5, 1\n" \
"v_mfma_f32_16x16x32_bf16 v[164:167], a[176:179], a[156:159], v[164:167]\n" \
"s_lshl_b32 s6, s4, 20\n" \
"s_lshl_b32 s7, s4, 8\n" \
"v_mfma_f32_16x16x32_bf16 v[164:167], a[240:243], a[220:223], v[164:167]\n" \
"ds_read_b128 a[156:159], v28 offset:14336\n" \
"ds_read_b128 a[220:223], v29 offset:14336\n" \
"s_waitcnt lgkmcnt(15)\n" \
"v_mfma_f32_16x16x32_bf16 a[0:3], a[160:163], a[128:131], a[0:3]\n" \
"s_add_u32 m0, s30, 0x21000\n" \
"s_addk_i32 s84, 0x3000\n" \
"v_mfma_f32_16x16x32_bf16 a[0:3], a[224:227], a[192:195], a[0:3]\n" \
"buffer_load_dwordx4 v24, s[76:79], s84 offen offset:0 lds\n" \
"v_mfma_f32_16x16x32_bf16 a[4:7], a[164:167], a[128:131], a[4:7]\n" \
"ds_read_b64_tr_b16 a[176:177], v36 offset:0\n" \
"s_and_b32 s13, s3, 1\n" \
"v_mfma_f32_16x16x32_bf16 a[4:7], a[228:231], a[192:195], a[4:7]\n" \
"ds_read_b64_tr_b16 a[178:179], v36 offset:1024\n" \
"s_add_i32 s14, s18, s19\n" \
"v_mfma_f32_16x16x32_bf16 a[12:15], a[172:175], a[128:131], a[12:15]\n" \
"ds_read_b64_tr_b16 a[240:241], v36 offset:8192\n" \
"s_waitcnt lgkmcnt(15)\n" \
"v_mfma_f32_16x16x32_bf16 a[12:15], a[236:239], a[192:195], a[12:15]\n" \
"s_add_u32 s88, s42, 0x1000\n" \
"buffer_store_dwordx4 v[188:191], v20, s[80:83], s88 offen offset:1024 sc1\n" \
"s_waitcnt lgkmcnt(13)\n" \
"v_mfma_f32_16x16x32_bf16 a[8:11], a[128:131], a[168:171], a[8:11]\n" \
"ds_read_b64_tr_b16 a[242:243], v36 offset:9216\n" \
"s_waitcnt lgkmcnt(12)\n" \
"v_mfma_f32_16x16x32_bf16 a[8:11], a[192:195], a[232:235], a[8:11]\n" \
"ds_read_b64_tr_b16 a[180:181], v37 offset:0\n" \
"v_mfma_f32_16x16x32_bf16 a[24:27], a[132:135], a[168:171], a[24:27]\n" \
"ds_read_b64_tr_b16 a[182:183], v37 offset:1024\n" \
"v_mfma_f32_16x16x32_bf16 a[24:27], a[196:199], a[232:235], a[24:27]\n" \
"ds_read_b64_tr_b16 a[244:245], v37 offset:8192\n" \
"v_mfma_f32_16x16x32_bf16 a[28:31], a[172:175], a[132:135], a[28:31]\n" \
"s_add_u32 s6, s26, s6\n" \
"s_addc_u32 s9, s27, 0\n" \
"v_mfma_f32_16x16x32_bf16 a[28:31], a[236:239], a[196:199], a[28:31]\n" \
"s_addk_i32 s84, 0x2000\n" \
"v_mfma_f32_16x16x32_bf16 a[20:23], a[164:167], a[132:135], a[20:23]\n" \
"buffer_load_dwordx4 v25, s[76:79], s84 offen offset:1024 lds\n" \
"v_mfma_f32_16x16x32_bf16 a[20:23], a[228:231], a[196:199], a[20:23]\n" \
"ds_read_b64_tr_b16 a[246:247], v37 offset:9216\n" \
"v_mfma_f32_16x16x32_bf16 a[16:19], a[160:163], a[132:135], a[16:19]\n" \
"ds_read_b64_tr_b16 a[184:185], v38 offset:0\n" \
"v_mfma_f32_16x16x32_bf16 a[16:19], a[224:227], a[196:199], a[16:19]\n" \
"ds_read_b64_tr_b16 a[186:187], v38 offset:1024\n" \
"v_mfma_f32_16x16x32_bf16 a[32:35], a[160:163], a[136:139], a[32:35]\n" \
"s_addk_i32 s84, 0x2000\n" \
"s_nop 0\n" \
"v_mfma_f32_16x16x32_bf16 a[32:35], a[224:227], a[200:203], a[32:35]\n" \
"buffer_load_dwordx4 v24, s[76:79], s84 offen offset:2048 lds\n" \
"v_mfma_f32_16x16x32_bf16 a[36:39], a[164:167], a[136:139], a[36:39]\n" \
"ds_read_b64_tr_b16 a[248:249], v38 offset:8192\n" \
"v_mfma_f32_16x16x32_bf16 a[36:39], a[228:231], a[200:203], a[36:39]\n" \
"ds_read_b64_tr_b16 a[250:251], v38 offset:9216\n" \
"v_mfma_f32_16x16x32_bf16 a[44:47], a[172:175], a[136:139], a[44:47]\n" \
"ds_read_b64_tr_b16 a[188:189], v39 offset:0\n" \
"v_mfma_f32_16x16x32_bf16 a[44:47], a[236:239], a[200:203], a[44:47]\n" \
"ds_read_b64_tr_b16 a[190:191], v39 offset:1024\n" \
"v_mfma_f32_16x16x32_bf16 a[40:43], a[136:139], a[168:171], a[40:43]\n" \
"s_add_u32 s89, s42, 0x2000\n" \
"buffer_store_dwordx4 v[192:195], v20, s[80:83], s89 offen offset:0 sc1\n" \
"v_mfma_f32_16x16x32_bf16 a[40:43], a[200:203], a[232:235], a[40:43]\n" \
"ds_read_b64_tr_b16 a[252:253], v39 offset:8192\n" \
"v_mfma_f32_16x16x32_bf16 a[56:59], a[140:143], a[168:171], a[56:59]\n" \
"ds_read_b64_tr_b16 a[254:255], v39 offset:9216\n" \
"v_mfma_f32_16x16x32_bf16 a[56:59], a[204:207], a[232:235], a[56:59]\n" \
"s_add_u32 s8, s6, 0x1000000\n" \
"s_addc_u32 s6, s9, 0\n" \
"v_mfma_f32_16x16x32_bf16 a[60:63], a[172:175], a[140:143], a[60:63]\n" \
"s_and_b32 s9, s6, 0xffff\n" \
"s_lshl_b32 s6, s5, 16\n" \
"v_mfma_f32_16x16x32_bf16 a[60:63], a[236:239], a[204:207], a[60:63]\n" \
"s_addk_i32 s84, 0x2000\n" \
"v_mfma_f32_16x16x32_bf16 a[52:55], a[164:167], a[140:143], a[52:55]\n" \
"buffer_load_dwordx4 v25, s[76:79], s84 offen offset:3072 lds\n" \
"v_mfma_f32_16x16x32_bf16 a[52:55], a[228:231], a[204:207], a[52:55]\n" \
"s_sub_i32 s10, 0x100000, s6\n" \
"s_mov_b32 s6, 0x8000\n" \
"v_mfma_f32_16x16x32_bf16 a[48:51], a[160:163], a[140:143], a[48:51]\n" \
"s_cmp_lg_u32 s5, 0\n" \
"s_cselect_b32 s17, s11, s6\n" \
".fill (L_pb + 32480 - .) / 4, 4, 0xBF800000\n" \
"L_pp8:\n" \
"s_cbranch_execz L_pret\n" \
"v_mfma_f32_16x16x32_bf16 a[48:51], a[224:227], a[204:207], a[48:51]\n" \
"s_lshl_b32 s6, s3, 1\n" \
"s_lshl_b32 s5, s5, 3\n" \
"s_waitcnt lgkmcnt(15)\n" \
"v_mfma_f32_16x16x32_bf16 a[64:67], a[164:167], v[248:251], a[64:67]\n" \
"s_and_b32 s6, s6, 4\n" \
"s_or_b32 s12, s5, s6\n" \
"v_mfma_f32_16x16x32_bf16 a[64:67], a[228:231], v[48:51], a[64:67]\n" \
"s_sub_i32 s5, s13, s5\n" \
"s_or_b32 s5, s5, s6\n" \
"v_mfma_f32_16x16x32_bf16 a[80:83], a[172:175], v[248:251], a[80:83]\n" \
"s_or_b32 s6, s12, s13\n" \
"s_lshl_b32 s4, s4, 2\n" \
"v_mfma_f32_16x16x32_bf16 a[80:83], a[236:239], v[48:51], a[80:83]\n" \
"s_mulk_i32 s6, 0x92\n" \
"s_add_i32 s4, s4, 18\n" \
"v_mfma_f32_16x16x32_bf16 a[68:71], v[248:251], a[168:171], a[68:71]\n" \
"s_mulk_i32 s5, 0x92\n" \
"s_add_i32 s6, s4, s6\n" \
"v_mfma_f32_16x16x32_bf16 a[68:71], v[48:51], a[232:235], a[68:71]\n" \
"s_waitcnt vmcnt(10) lgkmcnt(0)\n" \
"s_barrier\n" \
"v_mfma_f32_16x16x32_bf16 v[52:55], a[176:179], a[128:131], v[52:55]\n" \
"s_add_u32 m0, s40, 0x0\n" \
"s_add_u32 s85, s39, 0x10000\n" \
"v_mfma_f32_16x16x32_bf16 v[52:55], a[240:243], a[192:195], v[52:55]\n" \
"buffer_load_dwordx4 v20, s[80:83], s85 offen sc1 lds\n" \
"v_mfma_f32_16x16x32_bf16 v[56:59], a[180:183], a[128:131], v[56:59]\n" \
"ds_read_b64_tr_b16 a[160:161], v32 offset:32768\n" \
"s_add_i32 s5, s5, s4\n" \
"v_mfma_f32_16x16x32_bf16 v[56:59], a[244:247], a[192:195], v[56:59]\n" \
"ds_read_b64_tr_b16 a[162:163], v32 offset:33280\n" \
"s_lshl_b32 s4, s3, 11\n" \
"v_mfma_f32_16x16x32_bf16 v[60:63], a[184:187], a[128:131], v[60:63]\n" \
"s_lshl_b32 s15, s13, 12\n" \
"s_lshl_b32 s16, s19, 14\n" \
"v_mfma_f32_16x16x32_bf16 v[60:63], a[248:251], a[192:195], v[60:63]\n" \
"ds_read_b64_tr_b16 a[224:225], v32 offset:36864\n" \
"s_mul_i32 s19, s19, 0xe000\n" \
"v_mfma_f32_16x16x32_bf16 v[64:67], a[188:191], a[128:131], v[64:67]\n" \
"s_mov_b32 s11, 0x20000\n" \
"s_or_b32 s12, s4, 0xc000\n" \
"v_mfma_f32_16x16x32_bf16 v[64:67], a[252:255], a[192:195], v[64:67]\n" \
"ds_read_b128 a[128:131], v28 offset:32768\n" \
"s_lshl_b32 s4, s3, 8\n" \
"v_mfma_f32_16x16x32_bf16 v[80:83], a[188:191], a[132:135], v[80:83]\n" \
"ds_read_b128 a[192:195], v29 offset:32768\n" \
"s_or_b32 s22, s15, s16\n" \
"v_mfma_f32_16x16x32_bf16 v[80:83], a[252:255], a[196:199], v[80:83]\n" \
"s_add_u32 m0, s40, 0x1000\n" \
"s_add_u32 s85, s39, 0x11000\n" \
"v_mfma_f32_16x16x32_bf16 v[76:79], a[184:187], a[132:135], v[76:79]\n" \
"buffer_load_dwordx4 v20, s[80:83], s85 offen sc1 lds\n" \
"v_mfma_f32_16x16x32_bf16 v[76:79], a[248:251], a[196:199], v[76:79]\n" \
"ds_read_b64_tr_b16 a[226:227], v32 offset:37376\n" \
"s_or_b32 s23, s19, s15\n" \
"v_mfma_f32_16x16x32_bf16 v[72:75], a[180:183], a[132:135], v[72:75]\n" \
"ds_read_b64_tr_b16 a[164:165], v33 offset:32768\n" \
"s_lshl_b32 s60, s6, 10\n" \
"v_mfma_f32_16x16x32_bf16 v[72:75], a[244:247], a[196:199], v[72:75]\n" \
"s_lshl_b32 s61, s5, 10\n" \
"s_lshl_b32 s14, s14, 15\n" \
"s_nop 0\n" \
"v_mfma_f32_16x16x32_bf16 v[68:71], a[176:179], a[132:135], v[68:71]\n" \
"ds_read_b64_tr_b16 a[166:167], v33 offset:33280\n" \
"s_or_b32 s20, s4, 0x26c00\n" \
"v_mfma_f32_16x16x32_bf16 v[68:71], a[240:243], a[196:199], v[68:71]\n" \
"ds_read_b128 a[132:135], v28 offset:34816\n" \
"s_or_b32 s13, s22, 0x8000\n" \
"v_mfma_f32_16x16x32_bf16 v[84:87], a[176:179], a[136:139], v[84:87]\n" \
"ds_read_b128 a[196:199], v29 offset:34816\n" \
"s_add_i32 s22, s22, 0x14000\n" \
"v_mfma_f32_16x16x32_bf16 v[84:87], a[240:243], a[200:203], v[84:87]\n" \
"s_add_u32 m0, s40, 0x3000\n" \
"s_movk_i32 s84, 0x400\n" \
"v_mfma_f32_16x16x32_bf16 v[88:91], a[180:183], a[136:139], v[88:91]\n" \
"buffer_load_dwordx4 v21, s[76:79], s84 offen offset:0 lds\n" \
"v_mfma_f32_16x16x32_bf16 v[88:91], a[244:247], a[200:203], v[88:91]\n" \
"ds_read_b64_tr_b16 a[228:229], v33 offset:36864\n" \
"s_bitset1_b32 s23, 16\n" \
"v_mfma_f32_16x16x32_bf16 v[92:95], a[184:187], a[136:139], v[92:95]\n" \
"ds_read_b64_tr_b16 a[230:231], v33 offset:37376\n" \
"s_or_b32 s24, s7, 0x5be8020\n" \
"v_mfma_f32_16x16x32_bf16 v[92:95], a[248:251], a[200:203], v[92:95]\n" \
"s_add_u32 s88, s42, 0x2000\n" \
"buffer_store_dwordx4 v[216:219], v20, s[80:83], s88 offen offset:1024 sc1\n" \
"v_mfma_f32_16x16x32_bf16 v[96:99], a[188:191], a[136:139], v[96:99]\n" \
"s_and_b32 s57, s27, 0xffff\n" \
"s_mov_b32 s58, 0x6000000\n" \
"v_mfma_f32_16x16x32_bf16 v[96:99], a[252:255], a[200:203], v[96:99]\n" \
"ds_read_b128 a[136:139], v28 offset:36864\n" \
"s_mov_b32 s56, s26\n" \
"v_mfma_f32_16x16x32_bf16 v[112:115], a[188:191], a[140:143], v[112:115]\n" \
"ds_read_b128 a[200:203], v29 offset:36864\n" \
"s_mov_b32 s59, s11\n" \
"v_mfma_f32_16x16x32_bf16 v[112:115], a[252:255], a[204:207], v[112:115]\n" \
"ds_read_b64_tr_b16 a[172:173], v35 offset:32768\n" \
"s_add_i32 s60, s60, 0x3000000\n" \
"v_mfma_f32_16x16x32_bf16 v[108:111], a[184:187], a[140:143], v[108:111]\n" \
"ds_read_b64_tr_b16 a[174:175], v35 offset:33280\n" \
"s_add_i32 s61, s61, 0x411c000\n" \
"v_mfma_f32_16x16x32_bf16 v[108:111], a[248:251], a[204:207], v[108:111]\n" \
"s_sub_i32 s19, 14, s18\n" \
"s_mov_b32 s21, 0x71800000\n" \
"v_mfma_f32_16x16x32_bf16 v[104:107], a[180:183], a[140:143], v[104:107]\n" \
"s_add_u32 m0, s31, 0x0\n" \
"s_mov_b32 s84, 0x400000\n" \
"v_mfma_f32_16x16x32_bf16 v[104:107], a[244:247], a[204:207], v[104:107]\n" \
"buffer_load_dwordx4 v26, s[68:71], s84 offen offset:0 lds\n" \
"v_mfma_f32_16x16x32_bf16 v[100:103], a[176:179], a[140:143], v[100:103]\n" \
"ds_read_b64_tr_b16 a[236:237], v35 offset:36864\n" \
"s_mov_b32 s4, L_gsw-L_gtr\n" \
"v_mfma_f32_16x16x32_bf16 v[100:103], a[240:243], a[204:207], v[100:103]\n" \
"ds_read_b128 a[140:143], v28 offset:38912\n" \
"v_mfma_f32_16x16x32_bf16 v[116:119], a[176:179], a[144:147], v[116:119]\n" \
"ds_read_b128 a[204:207], v29 offset:38912\n" \
"v_mfma_f32_16x16x32_bf16 v[116:119], a[240:243], a[208:211], v[116:119]\n" \
"ds_read_b64_tr_b16 a[238:239], v35 offset:37376\n" \
"v_mfma_f32_16x16x32_bf16 v[120:123], a[180:183], a[144:147], v[120:123]\n" \
"ds_read_b64_tr_b16 a[168:169], v34 offset:32768\n" \
"v_mfma_f32_16x16x32_bf16 v[120:123], a[244:247], a[208:211], v[120:123]\n" \
"s_addk_i32 s84, 0x7c00\n" \
"v_mfma_f32_16x16x32_bf16 v[124:127], a[184:187], a[144:147], v[124:127]\n" \
"buffer_load_dwordx4 v26, s[68:71], s84 offen offset:1024 lds\n" \
"v_mfma_f32_16x16x32_bf16 v[124:127], a[248:251], a[208:211], v[124:127]\n" \
"ds_read_b64_tr_b16 a[170:171], v34 offset:33280\n" \
"v_mfma_f32_16x16x32_bf16 v[128:131], a[188:191], a[144:147], v[128:131]\n" \
"s_cmp_eq_u32 s19, 0\n" \
"s_cselect_b32 s4, L_gs7-L_gtr, s4\n" \
"v_mfma_f32_16x16x32_bf16 v[128:131], a[252:255], a[208:211], v[128:131]\n" \
"ds_read_b128 a[144:147], v28 offset:40960\n" \
"v_mfma_f32_16x16x32_bf16 v[144:147], a[188:191], a[148:151], v[144:147]\n" \
"ds_read_b128 a[208:211], v29 offset:40960\n" \
"v_mfma_f32_16x16x32_bf16 v[144:147], a[252:255], a[212:215], v[144:147]\n" \
"ds_read_b64_tr_b16 a[232:233], v34 offset:36864\n" \
"v_mfma_f32_16x16x32_bf16 v[140:143], a[184:187], a[148:151], v[140:143]\n" \
"s_addk_i32 s84, 0x7c00\n" \
"v_mfma_f32_16x16x32_bf16 v[140:143], a[248:251], a[212:215], v[140:143]\n" \
"buffer_load_dwordx4 v27, s[68:71], s84 offen offset:2048 lds\n" \
"v_mfma_f32_16x16x32_bf16 v[136:139], a[180:183], a[148:151], v[136:139]\n" \
"ds_read_b64_tr_b16 a[234:235], v34 offset:37376\n" \
"v_mfma_f32_16x16x32_bf16 v[136:139], a[244:247], a[212:215], v[136:139]\n" \
"s_cmp_eq_u32 s16, 0\n" \
"s_cselect_b32 s4, 0, s4\n" \
"v_mfma_f32_16x16x32_bf16 v[132:135], a[176:179], a[148:151], v[132:135]\n" \
"ds_read_b128 v[248:251], v22 offset:32768\n" \
"v_mfma_f32_16x16x32_bf16 v[132:135], a[240:243], a[212:215], v[132:135]\n" \
"ds_read_b128 a[148:151], v28 offset:43008\n" \
"v_mfma_f32_16x16x32_bf16 v[148:151], a[176:179], a[152:155], v[148:151]\n" \
"ds_read_b128 a[212:215], v29 offset:43008\n" \
"v_mfma_f32_16x16x32_bf16 v[148:151], a[240:243], a[216:219], v[148:151]\n" \
"s_addk_i32 s84, 0x7c00\n" \
"v_mfma_f32_16x16x32_bf16 v[152:155], a[180:183], a[152:155], v[152:155]\n" \
"buffer_load_dwordx4 v27, s[68:71], s84 offen offset:3072 lds\n" \
"v_mfma_f32_16x16x32_bf16 v[152:155], a[244:247], a[216:219], v[152:155]\n" \
"ds_read_b128 v[48:51], v23 offset:32768\n" \
"v_mfma_f32_16x16x32_bf16 v[156:159], a[184:187], a[152:155], v[156:159]\n" \
"s_getpc_b64 s[28:29]\n" \
"L_gp0_%=:\n" \
"s_add_u32 s28, s28, L_gtr-L_gp0_%=\n" \
"s_addc_u32 s29, s29, 0\n" \
"v_mfma_f32_16x16x32_bf16 v[156:159], a[248:251], a[216:219], v[156:159]\n" \
"s_add_u32 s28, s28, s4\n" \
"s_addc_u32 s29, s29, 0\n" \
"v_mfma_f32_16x16x32_bf16 v[160:163], a[188:191], a[152:155], v[160:163]\n" \
"v_mfma_f32_16x16x32_bf16 v[160:163], a[252:255], a[216:219], v[160:163]\n" \
"ds_read_b128 a[152:155], v28 offset:45056\n" \
"v_mfma_f32_16x16x32_bf16 v[176:179], a[188:191], a[156:159], v[176:179]\n" \
"ds_read_b128 a[216:219], v29 offset:45056\n" \
"v_mfma_f32_16x16x32_bf16 v[176:179], a[252:255], a[220:223], v[176:179]\n" \
"v_mfma_f32_16x16x32_bf16 v[172:175], a[184:187], a[156:159], v[172:175]\n" \
"v_mfma_f32_16x16x32_bf16 v[172:175], a[248:251], a[220:223], v[172:175]\n" \
"v_mfma_f32_16x16x32_bf16 v[168:171], a[180:183], a[156:159], v[168:171]\n" \
"v_mfma_f32_16x16x32_bf16 v[168:171], a[244:247], a[220:223], v[168:171]\n" \
"v_mfma_f32_16x16x32_bf16 v[164:167], a[176:179], a[156:159], v[164:167]\n" \
"v_mfma_f32_16x16x32_bf16 v[164:167], a[240:243], a[220:223], v[164:167]\n" \
"ds_read_b128 a[156:159], v28 offset:47104\n" \
"ds_read_b128 a[220:223], v29 offset:47104\n" \
"s_waitcnt lgkmcnt(15)\n" \
"s_nop 0\n" \
"v_mfma_f32_16x16x32_bf16 a[0:3], a[160:163], a[128:131], a[0:3]\n" \
"s_add_u32 s89, s42, 0x3000\n" \
"buffer_store_dwordx4 v[220:223], v20, s[80:83], s89 offen offset:0 sc1\n" \
"v_mfma_f32_16x16x32_bf16 a[0:3], a[224:227], a[192:195], a[0:3]\n" \
"ds_read_b64_tr_b16 a[176:177], v36 offset:32768\n" \
"v_mfma_f32_16x16x32_bf16 a[4:7], a[164:167], a[128:131], a[4:7]\n" \
"ds_read_b64_tr_b16 a[178:179], v36 offset:33792\n" \
"v_mfma_f32_16x16x32_bf16 a[4:7], a[228:231], a[192:195], a[4:7]\n" \
"ds_read_b64_tr_b16 a[240:241], v36 offset:40960\n" \
"v_mfma_f32_16x16x32_bf16 a[12:15], a[172:175], a[128:131], a[12:15]\n" \
"ds_read_b64_tr_b16 a[242:243], v36 offset:41984\n" \
"s_waitcnt lgkmcnt(15)\n" \
"v_mfma_f32_16x16x32_bf16 a[12:15], a[236:239], a[192:195], a[12:15]\n" \
"v_mfma_f32_16x16x32_bf16 a[8:11], a[128:131], a[168:171], a[8:11]\n" \
"ds_read_b64_tr_b16 a[180:181], v37 offset:32768\n" \
"s_waitcnt lgkmcnt(13)\n" \
"v_mfma_f32_16x16x32_bf16 a[8:11], a[192:195], a[232:235], a[8:11]\n" \
"v_mfma_f32_16x16x32_bf16 a[24:27], a[132:135], a[168:171], a[24:27]\n" \
"ds_read_b64_tr_b16 a[182:183], v37 offset:33792\n" \
"v_mfma_f32_16x16x32_bf16 a[24:27], a[196:199], a[232:235], a[24:27]\n" \
"ds_read_b64_tr_b16 a[244:245], v37 offset:40960\n" \
"v_mfma_f32_16x16x32_bf16 a[28:31], a[172:175], a[132:135], a[28:31]\n" \
"v_mfma_f32_16x16x32_bf16 a[28:31], a[236:239], a[196:199], a[28:31]\n" \
"ds_read_b64_tr_b16 a[246:247], v37 offset:41984\n" \
"v_mfma_f32_16x16x32_bf16 a[20:23], a[164:167], a[132:135], a[20:23]\n" \
"v_mfma_f32_16x16x32_bf16 a[20:23], a[228:231], a[196:199], a[20:23]\n" \
"ds_read_b64_tr_b16 a[184:185], v38 offset:32768\n" \
"v_mfma_f32_16x16x32_bf16 a[16:19], a[160:163], a[132:135], a[16:19]\n" \
"ds_read_b64_tr_b16 a[186:187], v38 offset:33792\n" \
"v_mfma_f32_16x16x32_bf16 a[16:19], a[224:227], a[196:199], a[16:19]\n" \
"v_mfma_f32_16x16x32_bf16 a[32:35], a[160:163], a[136:139], a[32:35]\n" \
"s_add_u32 s88, s42, 0x3000\n" \
"buffer_store_dwordx4 v[224:227], v20, s[80:83], s88 offen offset:1024 sc1\n" \
"v_mfma_f32_16x16x32_bf16 a[32:35], a[224:227], a[200:203], a[32:35]\n" \
"ds_read_b64_tr_b16 a[248:249], v38 offset:40960\n" \
"v_mfma_f32_16x16x32_bf16 a[36:39], a[164:167], a[136:139], a[36:39]\n" \
"v_lshlrev_b32_e32 v45, 3, v20\n" \
"v_add_u32_e32 v45, s30, v45\n" \
"v_mfma_f32_16x16x32_bf16 a[36:39], a[228:231], a[200:203], a[36:39]\n" \
"s_getpc_b64 s[86:87]\n" \
"L_ct0_%=:\n" \
"s_add_u32 s86, s86, L_ap0-L_ct0_%=\n" \
"s_addc_u32 s87, s87, 0\n" \
"s_add_u32 m0, s40, 0x2000\n" \
"v_mfma_f32_16x16x32_bf16 a[44:47], a[172:175], a[136:139], a[44:47]\n" \
"global_load_lds_dword v45, s[86:87]\n" \
"v_mfma_f32_16x16x32_bf16 a[44:47], a[236:239], a[200:203], a[44:47]\n" \
"ds_read_b64_tr_b16 a[250:251], v38 offset:41984\n" \
"v_mfma_f32_16x16x32_bf16 a[40:43], a[136:139], a[168:171], a[40:43]\n" \
"ds_read_b64_tr_b16 a[188:189], v39 offset:32768\n" \
"v_mfma_f32_16x16x32_bf16 a[40:43], a[200:203], a[232:235], a[40:43]\n" \
"ds_read_b64_tr_b16 a[190:191], v39 offset:33792\n" \
"v_mfma_f32_16x16x32_bf16 a[56:59], a[140:143], a[168:171], a[56:59]\n" \
"ds_read_b64_tr_b16 a[252:253], v39 offset:40960\n" \
"v_mfma_f32_16x16x32_bf16 a[56:59], a[204:207], a[232:235], a[56:59]\n" \
"ds_read_b64_tr_b16 a[254:255], v39 offset:41984\n" \
"v_mfma_f32_16x16x32_bf16 a[60:63], a[172:175], a[140:143], a[60:63]\n" \
"v_mfma_f32_16x16x32_bf16 a[60:63], a[236:239], a[204:207], a[60:63]\n" \
"v_mfma_f32_16x16x32_bf16 a[52:55], a[164:167], a[140:143], a[52:55]\n" \
"v_mfma_f32_16x16x32_bf16 a[52:55], a[228:231], a[204:207], a[52:55]\n" \
"v_mfma_f32_16x16x32_bf16 a[48:51], a[160:163], a[140:143], a[48:51]\n" \
"v_mfma_f32_16x16x32_bf16 a[48:51], a[224:227], a[204:207], a[48:51]\n" \
"s_waitcnt lgkmcnt(15)\n" \
"v_mfma_f32_16x16x32_bf16 a[64:67], a[164:167], v[248:251], a[64:67]\n" \
"v_mfma_f32_16x16x32_bf16 a[64:67], a[228:231], v[48:51], a[64:67]\n" \
"v_mfma_f32_16x16x32_bf16 a[80:83], a[172:175], v[248:251], a[80:83]\n" \
"v_mfma_f32_16x16x32_bf16 a[80:83], a[236:239], v[48:51], a[80:83]\n" \
"v_mfma_f32_16x16x32_bf16 a[68:71], v[248:251], a[168:171], a[68:71]\n" \
"v_mfma_f32_16x16x32_bf16 a[68:71], v[48:51], a[232:235], a[68:71]\n" \
"s_waitcnt vmcnt(3) lgkmcnt(0)\n" \
"s_barrier\n" \
"s_nop 0\n" \
"v_xor_b32_e32 v28, 0x20000, v28\n" \
"v_xor_b32_e32 v29, 0x20000, v29\n" \
"v_mfma_f32_16x16x32_bf16 v[52:55], a[176:179], a[128:131], v[52:55]\n" \
"ds_read_b64_tr_b16 a[160:161], v32 offset:0\n" \
"v_accvgpr_read_b32 v184, a0\n" \
"v_mfma_f32_16x16x32_bf16 v[52:55], a[240:243], a[192:195], v[52:55]\n" \
"v_accvgpr_read_b32 v185, a1\n" \
"v_accvgpr_read_b32 v186, a2\n" \
"v_mfma_f32_16x16x32_bf16 v[56:59], a[180:183], a[128:131], v[56:59]\n" \
"ds_read_b64_tr_b16 a[162:163], v32 offset:512\n" \
"v_accvgpr_read_b32 v187, a3\n" \
"v_mfma_f32_16x16x32_bf16 v[56:59], a[244:247], a[192:195], v[56:59]\n" \
"v_accvgpr_read_b32 v188, a4\n" \
"v_accvgpr_read_b32 v189, a5\n" \
"v_mfma_f32_16x16x32_bf16 v[60:63], a[184:187], a[128:131], v[60:63]\n" \
"v_accvgpr_read_b32 v190, a6\n" \
"v_accvgpr_read_b32 v191, a7\n" \
"v_mfma_f32_16x16x32_bf16 v[60:63], a[248:251], a[192:195], v[60:63]\n" \
"ds_read_b64_tr_b16 a[224:225], v32 offset:4096\n" \
"v_accvgpr_read_b32 v196, a12\n" \
"v_mfma_f32_16x16x32_bf16 v[64:67], a[188:191], a[128:131], v[64:67]\n" \
"v_accvgpr_read_b32 v197, a13\n" \
"v_accvgpr_read_b32 v198, a14\n" \
"v_mfma_f32_16x16x32_bf16 v[64:67], a[252:255], a[192:195], v[64:67]\n" \
"ds_read_b128 a[128:131], v28 offset:0\n" \
"v_accvgpr_read_b32 v199, a15\n" \
"v_mfma_f32_16x16x32_bf16 v[80:83], a[188:191], a[132:135], v[80:83]\n" \
"ds_read_b128 a[192:195], v29 offset:0\n" \
"v_accvgpr_read_b32 v192, a8\n" \
"v_mfma_f32_16x16x32_bf16 v[80:83], a[252:255], a[196:199], v[80:83]\n" \
"ds_read_b64_tr_b16 a[226:227], v32 offset:4608\n" \
"v_accvgpr_read_b32 v193, a9\n" \
"v_mfma_f32_16x16x32_bf16 v[76:79], a[184:187], a[132:135], v[76:79]\n" \
"v_accvgpr_read_b32 v194, a10\n" \
"v_accvgpr_read_b32 v195, a11\n" \
"v_mfma_f32_16x16x32_bf16 v[76:79], a[248:251], a[196:199], v[76:79]\n" \
"ds_read_b64_tr_b16 a[164:165], v33 offset:0\n" \
"v_accvgpr_read_b32 v208, a24\n" \
"v_mfma_f32_16x16x32_bf16 v[72:75], a[180:183], a[132:135], v[72:75]\n" \
"v_accvgpr_read_b32 v209, a25\n" \
"v_accvgpr_read_b32 v210, a26\n" \
"v_mfma_f32_16x16x32_bf16 v[72:75], a[244:247], a[196:199], v[72:75]\n" \
"v_accvgpr_read_b32 v211, a27\n" \
"v_accvgpr_read_b32 v212, a28\n" \
"v_mfma_f32_16x16x32_bf16 v[68:71], a[176:179], a[132:135], v[68:71]\n" \
"ds_read_b64_tr_b16 a[166:167], v33 offset:512\n" \
"v_accvgpr_read_b32 v213, a29\n" \
"v_mfma_f32_16x16x32_bf16 v[68:71], a[240:243], a[196:199], v[68:71]\n" \
"ds_read_b128 a[132:135], v28 offset:2048\n" \
"v_accvgpr_read_b32 v214, a30\n" \
"v_mfma_f32_16x16x32_bf16 v[84:87], a[176:179], a[136:139], v[84:87]\n" \
"ds_read_b128 a[196:199], v29 offset:2048\n" \
"v_accvgpr_read_b32 v215, a31\n" \
"v_mfma_f32_16x16x32_bf16 v[84:87], a[240:243], a[200:203], v[84:87]\n" \
"ds_read_b64_tr_b16 a[228:229], v33 offset:4096\n" \
"v_accvgpr_read_b32 v204, a20\n" \
"v_mfma_f32_16x16x32_bf16 v[88:91], a[180:183], a[136:139], v[88:91]\n" \
"v_accvgpr_read_b32 v205, a21\n" \
"v_accvgpr_read_b32 v206, a22\n" \
"v_mfma_f32_16x16x32_bf16 v[88:91], a[244:247], a[200:203], v[88:91]\n" \
"v_accvgpr_read_b32 v207, a23\n" \
"v_accvgpr_read_b32 v200, a16\n" \
"v_mfma_f32_16x16x32_bf16 v[92:95], a[184:187], a[136:139], v[92:95]\n" \
"ds_read_b64_tr_b16 a[230:231], v33 offset:4608\n" \
"v_accvgpr_read_b32 v201, a17\n" \
"v_mfma_f32_16x16x32_bf16 v[92:95], a[248:251], a[200:203], v[92:95]\n" \
"v_accvgpr_read_b32 v202, a18\n" \
"v_accvgpr_read_b32 v203, a19\n" \
"v_mfma_f32_16x16x32_bf16 v[96:99], a[188:191], a[136:139], v[96:99]\n" \
"v_accvgpr_read_b32 v216, a32\n" \
"v_accvgpr_read_b32 v217, a33\n" \
"v_mfma_f32_16x16x32_bf16 v[96:99], a[252:255], a[200:203], v[96:99]\n" \
"ds_read_b128 a[136:139], v28 offset:4096\n" \
"v_accvgpr_read_b32 v218, a34\n" \
"v_mfma_f32_16x16x32_bf16 v[112:115], a[188:191], a[140:143], v[112:115]\n" \
"ds_read_b128 a[200:203], v29 offset:4096\n" \
"v_accvgpr_read_b32 v219, a35\n" \
"v_mfma_f32_16x16x32_bf16 v[112:115], a[252:255], a[204:207], v[112:115]\n" \
"ds_read_b64_tr_b16 a[172:173], v35 offset:0\n" \
"v_accvgpr_read_b32 v220, a36\n" \
"v_mfma_f32_16x16x32_bf16 v[108:111], a[184:187], a[140:143], v[108:111]\n" \
"ds_read_b64_tr_b16 a[174:175], v35 offset:512\n" \
"v_accvgpr_read_b32 v221, a37\n" \
"v_mfma_f32_16x16x32_bf16 v[108:111], a[248:251], a[204:207], v[108:111]\n" \
"v_accvgpr_read_b32 v222, a38\n" \
"v_accvgpr_read_b32 v223, a39\n" \
"v_mfma_f32_16x16x32_bf16 v[104:107], a[180:183], a[140:143], v[104:107]\n" \
"ds_read_b64_tr_b16 a[236:237], v35 offset:4096\n" \
"v_accvgpr_read_b32 v228, a44\n" \
"v_mfma_f32_16x16x32_bf16 v[104:107], a[244:247], a[204:207], v[104:107]\n" \
"v_accvgpr_read_b32 v229, a45\n" \
"v_accvgpr_read_b32 v230, a46\n" \
"v_mfma_f32_16x16x32_bf16 v[100:103], a[176:179], a[140:143], v[100:103]\n" \
"v_accvgpr_read_b32 v231, a47\n" \
"v_accvgpr_read_b32 v224, a40\n" \
"v_mfma_f32_16x16x32_bf16 v[100:103], a[240:243], a[204:207], v[100:103]\n" \
"ds_read_b128 a[140:143], v28 offset:6144\n" \
"v_accvgpr_read_b32 v225, a41\n" \
"v_mfma_f32_16x16x32_bf16 v[116:119], a[176:179], a[144:147], v[116:119]\n" \
"ds_read_b128 a[204:207], v29 offset:6144\n" \
"v_accvgpr_read_b32 v226, a42\n" \
"v_mfma_f32_16x16x32_bf16 v[116:119], a[240:243], a[208:211], v[116:119]\n" \
"ds_read_b64_tr_b16 a[238:239], v35 offset:4608\n" \
"v_accvgpr_read_b32 v227, a43\n" \
"v_mfma_f32_16x16x32_bf16 v[120:123], a[180:183], a[144:147], v[120:123]\n" \
"ds_read_b64_tr_b16 a[168:169], v34 offset:0\n" \
"v_accvgpr_read_b32 v240, a56\n" \
"v_mfma_f32_16x16x32_bf16 v[120:123], a[244:247], a[208:211], v[120:123]\n" \
"v_accvgpr_read_b32 v241, a57\n" \
"v_accvgpr_read_b32 v242, a58\n" \
"v_mfma_f32_16x16x32_bf16 v[124:127], a[184:187], a[144:147], v[124:127]\n" \
"v_accvgpr_read_b32 v243, a59\n" \
"v_accvgpr_read_b32 v244, a60\n" \
"v_mfma_f32_16x16x32_bf16 v[124:127], a[248:251], a[208:211], v[124:127]\n" \
"ds_read_b64_tr_b16 a[170:171], v34 offset:512\n" \
"v_accvgpr_read_b32 v245, a61\n" \
"v_mfma_f32_16x16x32_bf16 v[128:131], a[188:191], a[144:147], v[128:131]\n" \
"v_accvgpr_read_b32 v246, a62\n" \
"v_accvgpr_read_b32 v247, a63\n" \
"v_mfma_f32_16x16x32_bf16 v[128:131], a[252:255], a[208:211], v[128:131]\n" \
"ds_read_b128 a[144:147], v28 offset:8192\n" \
"v_accvgpr_read_b32 v236, a52\n" \
"v_mfma_f32_16x16x32_bf16 v[144:147], a[188:191], a[148:151], v[144:147]\n" \
"ds_read_b128 a[208:211], v29 offset:8192\n" \
"v_accvgpr_read_b32 v237, a53\n" \
"v_mfma_f32_16x16x32_bf16 v[144:147], a[252:255], a[212:215], v[144:147]\n" \
"ds_read_b64_tr_b16 a[232:233], v34 offset:4096\n" \
"v_accvgpr_read_b32 v238, a54\n" \
"v_mfma_f32_16x16x32_bf16 v[140:143], a[184:187], a[148:151], v[140:143]\n" \
"v_accvgpr_read_b32 v239, a55\n" \
"v_accvgpr_read_b32 v232, a48\n" \
"v_mfma_f32_16x16x32_bf16 v[140:143], a[248:251], a[212:215], v[140:143]\n" \
"ds_read_b64_tr_b16 a[234:235], v34 offset:4608\n" \
"v_accvgpr_read_b32 v233, a49\n" \
"v_mfma_f32_16x16x32_bf16 v[136:139], a[180:183], a[148:151], v[136:139]\n" \
"v_accvgpr_read_b32 v234, a50\n" \
"v_accvgpr_read_b32 v235, a51\n" \
"v_mfma_f32_16x16x32_bf16 v[136:139], a[244:247], a[212:215], v[136:139]\n" \
"v_accvgpr_read_b32 v180, a64\n" \
"v_accvgpr_read_b32 v181, a65\n" \
"v_mfma_f32_16x16x32_bf16 v[132:135], a[176:179], a[148:151], v[132:135]\n" \
"ds_read_b128 v[248:251], v22 offset:0\n" \
"v_accvgpr_read_b32 v182, a66\n" \
"v_mfma_f32_16x16x32_bf16 v[132:135], a[240:243], a[212:215], v[132:135]\n" \
"ds_read_b128 a[148:151], v28 offset:10240\n" \
"v_accvgpr_read_b32 v183, a67\n" \
"v_mfma_f32_16x16x32_bf16 v[148:151], a[176:179], a[152:155], v[148:151]\n" \
"ds_read_b128 a[212:215], v29 offset:10240\n" \
"v_accvgpr_read_b32 v252, a68\n" \
"v_mfma_f32_16x16x32_bf16 v[148:151], a[240:243], a[216:219], v[148:151]\n" \
"ds_read_b128 v[48:51], v23 offset:0\n" \
"v_accvgpr_read_b32 v253, a69\n" \
"v_mfma_f32_16x16x32_bf16 v[152:155], a[180:183], a[152:155], v[152:155]\n" \
"v_accvgpr_read_b32 v254, a70\n" \
"v_accvgpr_read_b32 v255, a71\n" \
"v_mfma_f32_16x16x32_bf16 v[152:155], a[244:247], a[216:219], v[152:155]\n" \
"v_lshrrev_b32_e64 v24, 9, v20\n" \
"v_and_b32_e32 v26, 0x1ff, v20\n" \
"v_mfma_f32_16x16x32_bf16 v[156:159], a[184:187], a[152:155], v[156:159]\n" \
"v_lshl_add_u32 v24, v24, 3, v26\n" \
"v_lshrrev_b32_e64 v25, 8, v20\n" \
"v_mfma_f32_16x16x32_bf16 v[156:159], a[248:251], a[216:219], v[156:159]\n" \
"v_mul_u32_u24_e32 v25, 0x24700, v25\n" \
"v_add_u32_e64 v25, v25, v20\n" \
"v_mfma_f32_16x16x32_bf16 v[160:163], a[188:191], a[152:155], v[160:163]\n" \
"v_lshrrev_b32_e32 v26, 2, v20\n" \
"v_add_u32_e32 v26, s45, v26\n" \
"v_mfma_f32_16x16x32_bf16 v[160:163], a[252:255], a[216:219], v[160:163]\n" \
"ds_read_b128 a[152:155], v28 offset:12288\n" \
"v_add_u32_e64 v27, s46, v20\n" \
"v_mfma_f32_16x16x32_bf16 v[176:179], a[188:191], a[156:159], v[176:179]\n" \
"ds_read_b128 a[216:219], v29 offset:12288\n" \
"v_add_u32_e64 v21, s43, v27\n" \
"v_mfma_f32_16x16x32_bf16 v[176:179], a[252:255], a[220:223], v[176:179]\n" \
"v_mfma_f32_16x16x32_bf16 v[172:175], a[184:187], a[156:159], v[172:175]\n" \
"v_mfma_f32_16x16x32_bf16 v[172:175], a[248:251], a[220:223], v[172:175]\n" \
"v_mfma_f32_16x16x32_bf16 v[168:171], a[180:183], a[156:159], v[168:171]\n" \
"v_mfma_f32_16x16x32_bf16 v[168:171], a[244:247], a[220:223], v[168:171]\n" \
"v_mfma_f32_16x16x32_bf16 v[164:167], a[176:179], a[156:159], v[164:167]\n" \
"v_mfma_f32_16x16x32_bf16 v[164:167], a[240:243], a[220:223], v[164:167]\n" \
"ds_read_b128 a[156:159], v28 offset:14336\n" \
"ds_read_b128 a[220:223], v29 offset:14336\n" \
"s_waitcnt lgkmcnt(15)\n" \
"s_nop 0\n" \
"v_mfma_f32_16x16x32_bf16 v[184:187], a[160:163], a[128:131], v[184:187]\n" \
"v_bfe_u32 v44, v20, 2, 5\n" \
"ds_read_b64_tr_b16 a[176:177], v36 offset:0\n" \
"v_mfma_f32_16x16x32_bf16 v[184:187], a[224:227], a[192:195], v[184:187]\n" \
"buffer_load_dword v44, v44, s[80:83], s38 offen offset:0 sc1\n" \
"v_mfma_f32_16x16x32_bf16 v[188:191], a[164:167], a[128:131], v[188:191]\n" \
"ds_read_b64_tr_b16 a[178:179], v36 offset:1024\n" \
"v_mfma_f32_16x16x32_bf16 v[188:191], a[228:231], a[192:195], v[188:191]\n" \
"ds_read_b64_tr_b16 a[240:241], v36 offset:8192\n" \
"v_mfma_f32_16x16x32_bf16 v[196:199], a[172:175], a[128:131], v[196:199]\n" \
"ds_read_b64_tr_b16 a[242:243], v36 offset:9216\n" \
"s_waitcnt lgkmcnt(15)\n" \
"v_mfma_f32_16x16x32_bf16 v[196:199], a[236:239], a[192:195], v[196:199]\n" \
"v_mfma_f32_16x16x32_bf16 v[192:195], a[128:131], a[168:171], v[192:195]\n" \
"ds_read_b64_tr_b16 a[180:181], v37 offset:0\n" \
"s_waitcnt lgkmcnt(13)\n" \
"v_mfma_f32_16x16x32_bf16 v[192:195], a[192:195], a[232:235], v[192:195]\n" \
"v_mfma_f32_16x16x32_bf16 v[208:211], a[132:135], a[168:171], v[208:211]\n" \
"ds_read_b64_tr_b16 a[182:183], v37 offset:1024\n" \
"v_mfma_f32_16x16x32_bf16 v[208:211], a[196:199], a[232:235], v[208:211]\n" \
"ds_read_b64_tr_b16 a[244:245], v37 offset:8192\n" \
"v_mfma_f32_16x16x32_bf16 v[212:215], a[172:175], a[132:135], v[212:215]\n" \
"v_mfma_f32_16x16x32_bf16 v[212:215], a[236:239], a[196:199], v[212:215]\n" \
"ds_read_b64_tr_b16 a[246:247], v37 offset:9216\n" \
"v_cvt_pk_bf16_f32 v188, v188, v189\n" \
"v_mfma_f32_16x16x32_bf16 v[204:207], a[164:167], a[132:135], v[204:207]\n" \
"v_cvt_pk_bf16_f32 v189, v190, v191\n" \
"v_cvt_pk_bf16_f32 v190, v198, v199\n" \
"v_mfma_f32_16x16x32_bf16 v[204:207], a[228:231], a[196:199], v[204:207]\n" \
"ds_read_b64_tr_b16 a[184:185], v38 offset:0\n" \
"v_mov_b32_e32 v191, 0x3f80\n" \
"v_mfma_f32_16x16x32_bf16 v[200:203], a[160:163], a[132:135], v[200:203]\n" \
"ds_read_b64_tr_b16 a[186:187], v38 offset:1024\n" \
"v_cvt_pk_bf16_f32 v184, v184, v185\n" \
"v_mfma_f32_16x16x32_bf16 v[200:203], a[224:227], a[196:199], v[200:203]\n" \
"v_cvt_pk_bf16_f32 v185, v186, v187\n" \
"v_cvt_pk_bf16_f32 v186, v196, v197\n" \
"v_mfma_f32_16x16x32_bf16 v[216:219], a[160:163], a[136:139], v[216:219]\n" \
"ds_read_b64_tr_b16 a[248:249], v38 offset:8192\n" \
"v_mov_b32_e64 v187, 0\n" \
"v_mfma_f32_16x16x32_bf16 v[216:219], a[224:227], a[200:203], v[216:219]\n" \
"ds_read_b64_tr_b16 a[250:251], v38 offset:9216\n" \
"v_mfma_f32_16x16x32_bf16 v[220:223], a[164:167], a[136:139], v[220:223]\n" \
"v_mfma_f32_16x16x32_bf16 v[220:223], a[228:231], a[200:203], v[220:223]\n" \
"ds_read_b64_tr_b16 a[188:189], v39 offset:0\n" \
"v_cvt_pk_bf16_f32 v204, v204, v205\n" \
"v_mfma_f32_16x16x32_bf16 v[228:231], a[172:175], a[136:139], v[228:231]\n" \
"v_cvt_pk_bf16_f32 v205, v206, v207\n" \
"v_cvt_pk_bf16_f32 v206, v214, v215\n" \
"v_mfma_f32_16x16x32_bf16 v[228:231], a[236:239], a[200:203], v[228:231]\n" \
"ds_read_b64_tr_b16 a[190:191], v39 offset:1024\n" \
"v_mov_b32_e32 v207, 0x3f80\n" \
"v_mfma_f32_16x16x32_bf16 v[224:227], a[136:139], a[168:171], v[224:227]\n" \
"ds_read_b64_tr_b16 a[252:253], v39 offset:8192\n" \
"v_cvt_pk_bf16_f32 v200, v200, v201\n" \
"v_mfma_f32_16x16x32_bf16 v[224:227], a[200:203], a[232:235], v[224:227]\n" \
"v_cvt_pk_bf16_f32 v201, v202, v203\n" \
"v_cvt_pk_bf16_f32 v202, v212, v213\n" \
"v_mfma_f32_16x16x32_bf16 v[240:243], a[140:143], a[168:171], v[240:243]\n" \
"ds_read_b64_tr_b16 a[254:255], v39 offset:9216\n" \
"v_mov_b32_e64 v203, 0\n" \
"v_mfma_f32_16x16x32_bf16 v[240:243], a[204:207], a[232:235], v[240:243]\n" \
"v_mfma_f32_16x16x32_bf16 v[244:247], a[172:175], a[140:143], v[244:247]\n" \
"v_mfma_f32_16x16x32_bf16 v[244:247], a[236:239], a[204:207], v[244:247]\n" \
"v_cvt_pk_bf16_f32 v220, v220, v221\n" \
"v_cvt_pk_bf16_f32 v221, v222, v223\n" \
"v_mfma_f32_16x16x32_bf16 v[236:239], a[164:167], a[140:143], v[236:239]\n" \
"v_cvt_pk_bf16_f32 v222, v230, v231\n" \
"v_mov_b32_e32 v223, 0x3f80\n" \
"v_mfma_f32_16x16x32_bf16 v[236:239], a[228:231], a[204:207], v[236:239]\n" \
"v_cvt_pk_bf16_f32 v216, v216, v217\n" \
"v_cvt_pk_bf16_f32 v217, v218, v219\n" \
"v_mfma_f32_16x16x32_bf16 v[232:235], a[160:163], a[140:143], v[232:235]\n" \
"v_cvt_pk_bf16_f32 v218, v228, v229\n" \
"v_mov_b32_e64 v219, 0\n" \
"v_mfma_f32_16x16x32_bf16 v[232:235], a[224:227], a[204:207], v[232:235]\n" \
"s_waitcnt lgkmcnt(15)\n" \
"s_nop 0\n" \
"v_mfma_f32_16x16x32_bf16 v[180:183], a[164:167], v[248:251], v[180:183]\n" \
"v_mfma_f32_16x16x32_bf16 v[180:183], a[228:231], v[48:51], v[180:183]\n" \
"v_mfma_f32_16x16x32_bf16 a[80:83], a[172:175], v[248:251], a[80:83]\n" \
"v_mfma_f32_16x16x32_bf16 a[80:83], a[236:239], v[48:51], a[80:83]\n" \
"v_cvt_pk_bf16_f32 v236, v236, v237\n" \
"v_cvt_pk_bf16_f32 v237, v238, v239\n" \
"v_mfma_f32_16x16x32_bf16 v[252:255], v[248:251], a[168:171], v[252:255]\n" \
"v_cvt_pk_bf16_f32 v238, v246, v247\n" \
"v_mov_b32_e32 v239, 0x3f80\n" \
"v_mfma_f32_16x16x32_bf16 v[252:255], v[48:51], a[232:235], v[252:255]\n" \
"v_cvt_pk_bf16_f32 v232, v232, v233\n" \
"v_cvt_pk_bf16_f32 v233, v234, v235\n" \
"s_waitcnt vmcnt(0) lgkmcnt(0)\n" \
"s_barrier\n" \
"v_mov_b32_e32 v45, s62\n" \
"s_mov_b64 exec, 1\n" \
"buffer_store_dword v45, off, s[80:83], s37 offset:32 sc1\n" \
"s_mov_b64 exec, -1\n" \
"v_cmp_ne_u32_e32 vcc, s62, v44\n" \
"s_nop 4\n" \
"s_cbranch_vccnz L_poll160_%=\n" \
"L_pollr160_%=:\n" \
"v_mfma_f32_16x16x32_bf16 v[52:55], a[176:179], a[128:131], v[52:55]\n" \
"s_mov_b32 s73, s50\n" \
"s_sub_u32 m0, s31, 0x10000\n" \
"v_mfma_f32_16x16x32_bf16 v[52:55], a[240:243], a[192:195], v[52:55]\n" \
"buffer_load_dwordx4 v20, s[80:83], s73 offen offset:0 sc1 lds\n" \
"v_mfma_f32_16x16x32_bf16 v[56:59], a[180:183], a[128:131], v[56:59]\n" \
"s_add_u32 s74, s50, 0x4000\n" \
"s_add_u32 s75, s50, 0x8000\n" \
"v_mfma_f32_16x16x32_bf16 v[56:59], a[244:247], a[192:195], v[56:59]\n" \
"s_mov_b32 s70, 0x3e0293ee\n" \
"s_mov_b32 s76, s52\n" \
"v_mfma_f32_16x16x32_bf16 v[60:63], a[184:187], a[128:131], v[60:63]\n" \
"v_mul_f32_e32 v52, s70, v52\n" \
"v_mul_f32_e32 v53, s70, v53\n" \
"v_mfma_f32_16x16x32_bf16 v[60:63], a[248:251], a[192:195], v[60:63]\n" \
"buffer_load_dwordx4 v20, s[80:83], s73 offen offset:1024 sc1 lds\n" \
".fill (L_pb + 36576 - .) / 4, 4, 0xBF800000\n" \
"L_pp9:\n" \
"s_cbranch_execz L_pret\n" \
"v_mfma_f32_16x16x32_bf16 v[64:67], a[188:191], a[128:131], v[64:67]\n" \
"v_mul_f32_e32 v54, s70, v54\n" \
"v_mul_f32_e64 v55, s70, v55\n" \
"v_mfma_f32_16x16x32_bf16 v[64:67], a[252:255], a[192:195], v[64:67]\n" \
"v_cvt_pk_bf16_f32 v52, v52, v53\n" \
"v_mul_f32_e64 v56, s70, v56\n" \
"v_mfma_f32_16x16x32_bf16 v[80:83], a[188:191], a[132:135], v[80:83]\n" \
"v_mul_f32_e32 v60, s70, v60\n" \
"v_mul_f32_e32 v61, s70, v61\n" \
"v_mfma_f32_16x16x32_bf16 v[80:83], a[252:255], a[196:199], v[80:83]\n" \
"buffer_load_dwordx4 v20, s[80:83], s73 offen offset:2048 sc1 lds\n" \
"v_mfma_f32_16x16x32_bf16 v[76:79], a[184:187], a[132:135], v[76:79]\n" \
"v_mul_f32_e32 v62, s70, v62\n" \
"v_mul_f32_e32 v63, s70, v63\n" \
"v_mfma_f32_16x16x32_bf16 v[76:79], a[248:251], a[196:199], v[76:79]\n" \
"v_cvt_pk_bf16_f32 v60, v60, v61\n" \
"v_mul_f32_e64 v57, s70, v57\n" \
"v_mfma_f32_16x16x32_bf16 v[72:75], a[180:183], a[132:135], v[72:75]\n" \
"v_mul_f32_e32 v58, s70, v58\n" \
"v_mul_f32_e32 v59, s70, v59\n" \
"v_mfma_f32_16x16x32_bf16 v[72:75], a[244:247], a[196:199], v[72:75]\n" \
"buffer_load_dwordx4 v20, s[80:83], s73 offen offset:3072 sc1 lds\n" \
"v_mfma_f32_16x16x32_bf16 v[68:71], a[176:179], a[132:135], v[68:71]\n" \
"s_sub_u32 m0, s31, s51\n" \
"v_cvt_pk_bf16_f32 v53, v54, v55\n" \
"v_mfma_f32_16x16x32_bf16 v[68:71], a[240:243], a[196:199], v[68:71]\n" \
"v_mul_f32_e32 v64, s70, v64\n" \
"v_mul_f32_e64 v65, s70, v65\n" \
"v_mfma_f32_16x16x32_bf16 v[84:87], a[176:179], a[136:139], v[84:87]\n" \
"v_mul_f32_e32 v66, s70, v66\n" \
"v_mul_f32_e32 v67, s70, v67\n" \
"v_mfma_f32_16x16x32_bf16 v[84:87], a[240:243], a[200:203], v[84:87]\n" \
"buffer_load_dwordx4 v20, s[80:83], s74 offen offset:0 sc1 lds\n" \
"v_mfma_f32_16x16x32_bf16 v[88:91], a[180:183], a[136:139], v[88:91]\n" \
"v_cvt_pk_bf16_f32 v61, v62, v63\n" \
"v_cvt_pk_bf16_f32 v54, v56, v57\n" \
"v_mfma_f32_16x16x32_bf16 v[88:91], a[244:247], a[200:203], v[88:91]\n" \
"v_cvt_pk_bf16_f32 v55, v58, v59\n" \
"v_cvt_pk_bf16_f32 v62, v64, v65\n" \
"v_mfma_f32_16x16x32_bf16 v[92:95], a[184:187], a[136:139], v[92:95]\n" \
"v_cvt_pk_bf16_f32 v63, v66, v67\n" \
"v_mul_f32_e64 v68, s70, v68\n" \
"v_mfma_f32_16x16x32_bf16 v[92:95], a[248:251], a[200:203], v[92:95]\n" \
"buffer_load_dwordx4 v20, s[80:83], s74 offen offset:1024 sc1 lds\n" \
"v_mfma_f32_16x16x32_bf16 v[96:99], a[188:191], a[136:139], v[96:99]\n" \
"ds_write_b128 v27, v[52:55] offset:0\n" \
"v_mfma_f32_16x16x32_bf16 v[96:99], a[252:255], a[200:203], v[96:99]\n" \
"v_mul_f32_e32 v69, s70, v69\n" \
"v_mul_f32_e32 v76, s70, v76\n" \
"v_mfma_f32_16x16x32_bf16 v[112:115], a[188:191], a[140:143], v[112:115]\n" \
"v_mul_f32_e32 v77, s70, v77\n" \
"v_mul_f32_e32 v70, s70, v70\n" \
"v_mfma_f32_16x16x32_bf16 v[112:115], a[252:255], a[204:207], v[112:115]\n" \
"buffer_load_dwordx4 v20, s[80:83], s74 offen offset:2048 sc1 lds\n" \
"v_mfma_f32_16x16x32_bf16 v[108:111], a[184:187], a[140:143], v[108:111]\n" \
"ds_write_b128 v27, v[60:63] offset:1024\n" \
"v_mfma_f32_16x16x32_bf16 v[108:111], a[248:251], a[204:207], v[108:111]\n" \
"v_mul_f32_e64 v71, s70, v71\n" \
"v_cvt_pk_bf16_f32 v68, v68, v69\n" \
"v_mfma_f32_16x16x32_bf16 v[104:107], a[180:183], a[140:143], v[104:107]\n" \
"v_mul_f32_e32 v78, s70, v78\n" \
"v_mul_f32_e32 v79, s70, v79\n" \
"v_mfma_f32_16x16x32_bf16 v[104:107], a[244:247], a[204:207], v[104:107]\n" \
"buffer_load_dwordx4 v20, s[80:83], s74 offen offset:3072 sc1 lds\n" \
"v_mfma_f32_16x16x32_bf16 v[100:103], a[176:179], a[140:143], v[100:103]\n" \
"s_sub_u32 m0, s31, 0x4000\n" \
"v_cvt_pk_bf16_f32 v76, v76, v77\n" \
"v_mfma_f32_16x16x32_bf16 v[100:103], a[240:243], a[204:207], v[100:103]\n" \
"v_mul_f32_e32 v72, s70, v72\n" \
"v_mul_f32_e32 v73, s70, v73\n" \
"v_mfma_f32_16x16x32_bf16 v[116:119], a[176:179], a[144:147], v[116:119]\n" \
"v_mul_f32_e32 v74, s70, v74\n" \
"v_mul_f32_e32 v75, s70, v75\n" \
"v_mfma_f32_16x16x32_bf16 v[116:119], a[240:243], a[208:211], v[116:119]\n" \
"buffer_load_dwordx4 v20, s[80:83], s75 offen offset:0 sc1 lds\n" \
"v_mfma_f32_16x16x32_bf16 v[120:123], a[180:183], a[144:147], v[120:123]\n" \
"v_cvt_pk_bf16_f32 v69, v70, v71\n" \
"v_mul_f32_e64 v80, s70, v80\n" \
"v_mfma_f32_16x16x32_bf16 v[120:123], a[244:247], a[208:211], v[120:123]\n" \
"v_mul_f32_e32 v81, s70, v81\n" \
"v_mul_f32_e32 v82, s70, v82\n" \
"v_mfma_f32_16x16x32_bf16 v[124:127], a[184:187], a[144:147], v[124:127]\n" \
"v_mul_f32_e64 v83, s70, v83\n" \
"v_cvt_pk_bf16_f32 v77, v78, v79\n" \
"v_mfma_f32_16x16x32_bf16 v[124:127], a[248:251], a[208:211], v[124:127]\n" \
"buffer_load_dwordx4 v20, s[80:83], s75 offen offset:1024 sc1 lds\n" \
"v_mfma_f32_16x16x32_bf16 v[128:131], a[188:191], a[144:147], v[128:131]\n" \
"v_cvt_pk_bf16_f32 v70, v72, v73\n" \
"v_cvt_pk_bf16_f32 v71, v74, v75\n" \
"v_mfma_f32_16x16x32_bf16 v[128:131], a[252:255], a[208:211], v[128:131]\n" \
"v_cvt_pk_bf16_f32 v78, v80, v81\n" \
"v_cvt_pk_bf16_f32 v79, v82, v83\n" \
"v_mfma_f32_16x16x32_bf16 v[144:147], a[188:191], a[148:151], v[144:147]\n" \
"ds_write_b128 v27, v[68:71] offset:4096\n" \
"v_mfma_f32_16x16x32_bf16 v[144:147], a[252:255], a[212:215], v[144:147]\n" \
"buffer_load_dwordx4 v20, s[80:83], s75 offen offset:2048 sc1 lds\n" \
"v_mfma_f32_16x16x32_bf16 v[140:143], a[184:187], a[148:151], v[140:143]\n" \
"v_cvt_pk_bf16_f32 v180, v180, v181\n" \
"v_accvgpr_read_b32 v50, a82\n" \
"v_mfma_f32_16x16x32_bf16 v[140:143], a[248:251], a[212:215], v[140:143]\n" \
"ds_write_b128 v27, v[76:79] offset:5120\n" \
"v_mfma_f32_16x16x32_bf16 v[136:139], a[180:183], a[148:151], v[136:139]\n" \
"v_accvgpr_read_b32 v51, a83\n" \
"v_cvt_pk_bf16_f32 v181, v182, v183\n" \
"v_mfma_f32_16x16x32_bf16 v[136:139], a[244:247], a[212:215], v[136:139]\n" \
"buffer_load_dwordx4 v20, s[80:83], s75 offen offset:3072 sc1 lds\n" \
"v_mfma_f32_16x16x32_bf16 v[132:135], a[176:179], a[148:151], v[132:135]\n" \
"s_mov_b32 m0, s53\n" \
"v_cvt_pk_bf16_f32 v182, v50, v51\n" \
"v_mfma_f32_16x16x32_bf16 v[132:135], a[240:243], a[212:215], v[132:135]\n" \
"v_mov_b32_e32 v183, 0x3f80\n" \
"v_cvt_pk_bf16_f32 v234, v244, v245\n" \
"v_mfma_f32_16x16x32_bf16 v[148:151], a[176:179], a[152:155], v[148:151]\n" \
"v_mov_b32_e32 v235, 0\n" \
"v_cvt_pk_bf16_f32 v54, v192, v193\n" \
"v_mfma_f32_16x16x32_bf16 v[148:151], a[240:243], a[216:219], v[148:151]\n" \
"buffer_load_dwordx4 v20, s[80:83], s76 offen offset:0 sc1 lds\n" \
"v_mfma_f32_16x16x32_bf16 v[152:155], a[180:183], a[152:155], v[152:155]\n" \
"v_cvt_pk_bf16_f32 v55, v194, v195\n" \
"v_cvt_pk_bf16_f32 v56, v208, v209\n" \
"v_mfma_f32_16x16x32_bf16 v[152:155], a[244:247], a[216:219], v[152:155]\n" \
"v_cvt_pk_bf16_f32 v57, v210, v211\n" \
"v_cvt_pk_bf16_f32 v58, v224, v225\n" \
"v_cvt_pk_bf16_f32 v59, v226, v227\n" \
"v_cvt_pk_bf16_f32 v60, v240, v241\n" \
"v_cvt_pk_bf16_f32 v61, v242, v243\n" \
"v_mfma_f32_16x16x32_bf16 v[32:35], v[180:183], v[184:187], 0\n" \
"buffer_load_dwordx4 v20, s[80:83], s76 offen offset:1024 sc1 lds\n" \
"v_mfma_f32_16x16x32_bf16 v[156:159], a[184:187], a[152:155], v[156:159]\n" \
"v_mfma_f32_16x16x32_bf16 v[192:195], v[188:191], v[184:187], 0\n" \
"v_mfma_f32_16x16x32_bf16 v[208:211], v[204:207], v[200:203], 0\n" \
"v_mfma_f32_16x16x32_bf16 v[36:39], v[188:191], v[200:203], 0\n" \
"v_mfma_f32_16x16x32_bf16 v[224:227], v[220:223], v[216:219], 0\n" \
"v_mfma_f32_16x16x32_bf16 v[248:251], v[204:207], v[216:219], 0\n" \
"v_mfma_f32_16x16x32_bf16 v[240:243], v[236:239], v[232:235], 0\n" \
"v_mfma_f32_16x16x32_bf16 v[64:67], v[220:223], v[232:235], 0\n" \
"s_mov_b32 s64, 0xaaaaaaaa\n" \
"s_mov_b32 s65, 0xaaaaaaaa\n" \
"v_mfma_f32_16x16x32_bf16 v[156:159], a[248:251], a[216:219], v[156:159]\n" \
"v_cndmask_b32_e64 v32, v32, v33, s[64:65]\n" \
"v_cndmask_b32_e64 v34, v34, v35, s[64:65]\n" \
"v_mfma_f32_16x16x32_bf16 v[160:163], a[188:191], a[152:155], v[160:163]\n" \
"s_mov_b32 s66, 0xfff0ffff\n" \
"s_mov_b32 s67, 0xf000ff00\n" \
"s_mov_b32 s68, 0xffc0fffc\n" \
"s_mov_b32 s69, 0xc000fc00\n" \
"v_cndmask_b32_e64 v192, v192, v193, s[64:65]\n" \
"v_add_f32_e64 v32, s47, v32\n" \
"v_cndmask_b32_e64 v194, v194, v195, s[64:65]\n" \
"v_add_f32_e64 v34, s47, v34\n" \
"v_cndmask_b32_e64 v208, v208, v209, s[64:65]\n" \
"v_cndmask_b32_e64 v36, v36, v37, s[64:65]\n" \
"v_cndmask_b32_e64 v210, v210, v211, s[64:65]\n" \
"v_cndmask_b32_e64 v38, v38, v39, s[64:65]\n" \
"v_cndmask_b32_e64 v224, v224, v225, s[64:65]\n" \
"v_cndmask_b32_e64 v248, v248, v249, s[64:65]\n" \
"v_cndmask_b32_e64 v226, v226, v227, s[64:65]\n" \
"v_cndmask_b32_e64 v250, v250, v251, s[64:65]\n" \
"v_cndmask_b32_e64 v240, v240, v241, s[64:65]\n" \
"v_cndmask_b32_e64 v64, v64, v65, s[64:65]\n" \
"v_cndmask_b32_e64 v242, v242, v243, s[64:65]\n" \
"v_cndmask_b32_e64 v66, v66, v67, s[64:65]\n" \
"v_cndmask_b32_e64 v192, v32, v192, s[66:67]\n" \
"v_cndmask_b32_e64 v194, v34, v194, s[68:69]\n" \
"v_cndmask_b32_e64 v208, v36, v208, s[66:67]\n" \
"v_cndmask_b32_e64 v210, v38, v210, s[68:69]\n" \
"v_cndmask_b32_e64 v224, v248, v224, s[66:67]\n" \
"v_cndmask_b32_e64 v226, v250, v226, s[68:69]\n" \
"v_cndmask_b32_e64 v240, v64, v240, s[66:67]\n" \
"v_cndmask_b32_e64 v242, v66, v242, s[68:69]\n" \
"v_mfma_f32_16x16x32_bf16 v[160:163], a[252:255], a[216:219], v[160:163]\n" \
"v_max_f32_e32 v193, v192, v194\n" \
"v_max_f32_e32 v209, v208, v210\n" \
"v_max_f32_e32 v225, v224, v226\n" \
"v_max_f32_e32 v241, v240, v242\n" \
"v_mul_f32_e32 v84, s70, v84\n" \
"v_permlane32_swap_b32_e32 v193, v225\n" \
"v_permlane32_swap_b32_e32 v209, v241\n" \
"v_mul_f32_e32 v85, s70, v85\n" \
"v_mul_f32_e32 v92, s70, v92\n" \
"v_mul_f32_e32 v93, s70, v93\n" \
"v_permlane32_swap_b32_e32 v196, v228\n" \
"v_permlane32_swap_b32_e32 v212, v244\n" \
"v_max_f32_e32 v193, v193, v225\n" \
"v_max_f32_e32 v209, v209, v241\n" \
"v_accvgpr_read_b32 v48, a80\n" \
"v_accvgpr_read_b32 v49, a81\n" \
"v_mul_f32_e32 v86, s70, v86\n" \
"v_mul_f32_e32 v87, s70, v87\n" \
"v_cvt_pk_bf16_f32 v84, v84, v85\n" \
"v_mul_f32_e32 v94, s70, v94\n" \
"v_mul_f32_e32 v95, s70, v95\n" \
"v_cvt_pk_bf16_f32 v92, v92, v93\n" \
"v_permlane16_swap_b32_e32 v228, v244\n" \
"v_permlane16_swap_b32_e32 v193, v209\n" \
"v_mfma_f32_16x16x32_bf16 v[176:179], a[188:191], a[156:159], v[176:179]\n" \
"s_mov_b32 s72, 0x3eae1a93\n" \
"v_mul_f32_e32 v88, s70, v88\n" \
"v_mul_f32_e32 v89, s70, v89\n" \
"v_mul_f32_e32 v90, s70, v90\n" \
"v_mul_f32_e32 v91, s70, v91\n" \
"v_cvt_pk_bf16_f32 v85, v86, v87\n" \
"v_mul_f32_e32 v96, s70, v96\n" \
"v_mul_f32_e32 v97, s70, v97\n" \
"v_mul_f32_e32 v98, s70, v98\n" \
"v_mul_f32_e32 v99, s70, v99\n" \
"v_cvt_pk_bf16_f32 v93, v94, v95\n" \
"v_mul_f32_e32 v45, 0xbfb8aa3b, v228\n" \
"v_mfma_f32_16x16x16_bf16 v[48:51], v[182:183], v[46:47], 0\n" \
"v_mfma_f32_16x16x16_bf16 v[196:199], v[190:191], v[46:47], 0\n" \
"v_mfma_f32_16x16x16_bf16 v[212:215], v[206:207], v[46:47], 0\n" \
"v_max_f32_e64 v193, v193, v209\n" \
"v_cvt_pk_bf16_f32 v86, v88, v89\n" \
"v_cvt_pk_bf16_f32 v87, v90, v91\n" \
"v_cvt_pk_bf16_f32 v94, v96, v97\n" \
"v_cvt_pk_bf16_f32 v95, v98, v99\n" \
"v_mfma_f32_16x16x16_bf16 v[228:231], v[222:223], v[46:47], 0\n" \
"v_mul_f32_e32 v193, s72, v193\n" \
"v_mov_b32_e32 v209, v193\n" \
"ds_write_b128 v27, v[84:87] offset:8192\n" \
"v_mfma_f32_16x16x16_bf16 v[244:247], v[238:239], v[46:47], 0\n" \
"v_permlane16_swap_b32_e32 v193, v209\n" \
"s_mov_b32 s71, 0x3e0293ee\n" \
"v_mfma_f32_16x16x32_bf16 v[176:179], a[252:255], a[220:223], v[176:179]\n" \
"v_mov_b32_e32 v225, v193\n" \
"v_mov_b32_e32 v241, v209\n" \
"v_mul_f32_e32 v100, s70, v100\n" \
"v_mul_f32_e64 v101, s70, v101\n" \
"ds_write_b128 v27, v[92:95] offset:9216\n" \
"v_mul_f32_e32 v108, s70, v108\n" \
"v_mul_f32_e32 v109, s70, v109\n" \
"v_pk_mul_f32 v[116:117], v[116:117], s[70:71]\n" \
"v_pk_mul_f32 v[124:125], v[124:125], s[70:71]\n" \
"v_pk_mul_f32 v[132:133], v[132:133], s[70:71]\n" \
"v_pk_mul_f32 v[140:141], v[140:141], s[70:71]\n" \
"v_pk_mul_f32 v[148:149], v[148:149], s[70:71]\n" \
"v_pk_mul_f32 v[156:157], v[156:157], s[70:71]\n" \
"v_permlane32_swap_b32_e32 v193, v225\n" \
"v_permlane32_swap_b32_e32 v209, v241\n" \
"v_mul_f32_e32 v102, s70, v102\n" \
"v_mul_f32_e32 v103, s70, v103\n" \
"v_cvt_pk_bf16_f32 v100, v100, v101\n" \
"v_mul_f32_e32 v110, s70, v110\n" \
"v_mul_f32_e32 v111, s70, v111\n" \
"v_cvt_pk_bf16_f32 v108, v108, v109\n" \
"v_pk_mul_f32 v[118:119], v[118:119], s[70:71]\n" \
"v_cvt_pk_bf16_f32 v116, v116, v117\n" \
"v_pk_mul_f32 v[126:127], v[126:127], s[70:71]\n" \
"v_cvt_pk_bf16_f32 v124, v124, v125\n" \
"v_pk_mul_f32 v[134:135], v[134:135], s[70:71]\n" \
"v_cvt_pk_bf16_f32 v132, v132, v133\n" \
"v_pk_mul_f32 v[142:143], v[142:143], s[70:71]\n" \
"v_cvt_pk_bf16_f32 v140, v140, v141\n" \
"v_pk_mul_f32 v[150:151], v[150:151], s[70:71]\n" \
"v_cvt_pk_bf16_f32 v148, v148, v149\n" \
"v_pk_mul_f32 v[158:159], v[158:159], s[70:71]\n" \
"v_cvt_pk_bf16_f32 v156, v156, v157\n" \
"v_fma_f32 v192, v192, s72, -v193\n" \
"v_fma_f32 v194, v194, s72, -v193\n" \
"v_fma_f32 v208, v208, s72, -v209\n" \
"v_fma_f32 v210, v210, s72, -v209\n" \
"v_mul_f32_e32 v104, s70, v104\n" \
"v_mul_f32_e32 v105, s70, v105\n" \
"v_mul_f32_e32 v106, s70, v106\n" \
"v_mul_f32_e32 v107, s70, v107\n" \
"v_cvt_pk_bf16_f32 v101, v102, v103\n" \
"v_mul_f32_e32 v112, s70, v112\n" \
"v_mul_f32_e32 v113, s70, v113\n" \
"v_mul_f32_e32 v114, s70, v114\n" \
"v_mul_f32_e32 v115, s70, v115\n" \
"v_cvt_pk_bf16_f32 v109, v110, v111\n" \
"v_pk_mul_f32 v[120:121], v[120:121], s[70:71]\n" \
"v_pk_mul_f32 v[122:123], v[122:123], s[70:71]\n" \
"v_cvt_pk_bf16_f32 v117, v118, v119\n" \
"v_pk_mul_f32 v[128:129], v[128:129], s[70:71]\n" \
"v_pk_mul_f32 v[130:131], v[130:131], s[70:71]\n" \
"v_cvt_pk_bf16_f32 v125, v126, v127\n" \
"v_pk_mul_f32 v[136:137], v[136:137], s[70:71]\n" \
"v_pk_mul_f32 v[138:139], v[138:139], s[70:71]\n" \
"v_cvt_pk_bf16_f32 v133, v134, v135\n" \
"v_pk_mul_f32 v[144:145], v[144:145], s[70:71]\n" \
"v_pk_mul_f32 v[146:147], v[146:147], s[70:71]\n" \
"v_cvt_pk_bf16_f32 v141, v142, v143\n" \
"v_pk_mul_f32 v[152:153], v[152:153], s[70:71]\n" \
"v_pk_mul_f32 v[154:155], v[154:155], s[70:71]\n" \
"v_cvt_pk_bf16_f32 v149, v150, v151\n" \
"v_pk_mul_f32 v[160:161], v[160:161], s[70:71]\n" \
"v_pk_mul_f32 v[162:163], v[162:163], s[70:71]\n" \
"v_cvt_pk_bf16_f32 v157, v158, v159\n" \
"v_pk_mul_f32 v[176:177], v[176:177], s[70:71]\n" \
"v_pk_mul_f32 v[178:179], v[178:179], s[70:71]\n" \
"v_exp_f32_e32 v192, v192\n" \
"v_exp_f32_e32 v194, v194\n" \
"v_exp_f32_e32 v208, v208\n" \
"v_exp_f32_e32 v210, v210\n" \
"v_fma_f32 v224, v224, s72, -v225\n" \
"v_fma_f32 v226, v226, s72, -v225\n" \
"v_mfma_f32_16x16x32_bf16 v[172:175], a[184:187], a[156:159], v[172:175]\n" \
"v_cvt_pk_bf16_f32 v102, v104, v105\n" \
"v_cvt_pk_bf16_f32 v103, v106, v107\n" \
"v_cvt_pk_bf16_f32 v110, v112, v113\n" \
"v_cvt_pk_bf16_f32 v111, v114, v115\n" \
"v_cvt_pk_bf16_f32 v118, v120, v121\n" \
"v_cvt_pk_bf16_f32 v119, v122, v123\n" \
"v_cvt_pk_bf16_f32 v126, v128, v129\n" \
"v_cvt_pk_bf16_f32 v127, v130, v131\n" \
"v_cvt_pk_bf16_f32 v134, v136, v137\n" \
"v_cvt_pk_bf16_f32 v135, v138, v139\n" \
"v_cvt_pk_bf16_f32 v142, v144, v145\n" \
"v_cvt_pk_bf16_f32 v143, v146, v147\n" \
"v_cvt_pk_bf16_f32 v150, v152, v153\n" \
"v_cvt_pk_bf16_f32 v151, v154, v155\n" \
"v_cvt_pk_bf16_f32 v158, v160, v161\n" \
"v_cvt_pk_bf16_f32 v159, v162, v163\n" \
"v_exp_f32_e32 v224, v224\n" \
"v_exp_f32_e32 v226, v226\n" \
"v_fma_f32 v240, v240, s72, -v241\n" \
"v_fma_f32 v242, v242, s72, -v241\n" \
"ds_write_b128 v27, v[100:103] offset:12288\n" \
"v_cvt_pk_bf16_f32 v192, v192, v194\n" \
"v_cvt_pk_bf16_f32 v208, v208, v210\n" \
"v_exp_f32_e32 v240, v240\n" \
"v_exp_f32_e32 v242, v242\n" \
"v_cvt_pk_bf16_f32 v68, v48, v49\n" \
"v_cvt_pk_bf16_f32 v69, v50, v51\n" \
"v_cvt_pk_bf16_f32 v70, v196, v197\n" \
"v_cvt_pk_bf16_f32 v71, v198, v199\n" \
"ds_write_b128 v27, v[108:111] offset:13312\n" \
"v_cvt_pk_bf16_f32 v72, v212, v213\n" \
"v_cvt_pk_bf16_f32 v73, v214, v215\n" \
"v_perm_b32 v184, v192, v192, v40\n" \
"v_perm_b32 v185, v192, v192, v41\n" \
"v_perm_b32 v186, v192, v192, v42\n" \
"v_perm_b32 v187, v192, v192, v43\n" \
"v_perm_b32 v200, v208, v208, v40\n" \
"v_perm_b32 v201, v208, v208, v41\n" \
"v_perm_b32 v202, v208, v208, v42\n" \
"v_perm_b32 v203, v208, v208, v43\n" \
"ds_write_b128 v21, v[116:119] offset:16384\n" \
"v_cvt_pk_bf16_f32 v224, v224, v226\n" \
"v_cvt_pk_bf16_f32 v74, v228, v229\n" \
"v_cvt_pk_bf16_f32 v75, v230, v231\n" \
"v_mfma_f32_16x16x32_bf16 v[32:35], v[68:71], v[184:187], 0\n" \
"v_mfma_f32_16x16x32_bf16 v[36:39], v[70:73], v[200:203], 0\n" \
"v_perm_b32 v216, v224, v224, v40\n" \
"v_perm_b32 v217, v224, v224, v41\n" \
"ds_write_b128 v21, v[124:127] offset:17408\n" \
"v_perm_b32 v218, v224, v224, v42\n" \
"v_perm_b32 v219, v224, v224, v43\n" \
"v_cvt_pk_bf16_f32 v240, v240, v242\n" \
"v_mfma_f32_16x16x32_bf16 v[172:175], a[248:251], a[220:223], v[172:175]\n" \
"v_cvt_pk_bf16_f32 v76, v244, v245\n" \
"v_cvt_pk_bf16_f32 v77, v246, v247\n" \
"v_mfma_f32_16x16x32_bf16 v[248:251], v[72:75], v[216:219], 0\n" \
"v_perm_b32 v232, v240, v240, v40\n" \
"v_perm_b32 v233, v240, v240, v41\n" \
"ds_write_b128 v21, v[132:135] offset:20480\n" \
"v_perm_b32 v234, v240, v240, v42\n" \
"v_perm_b32 v235, v240, v240, v43\n" \
"v_pk_mul_f32 v[172:173], v[172:173], s[70:71]\n" \
"v_pk_mul_f32 v[174:175], v[174:175], s[70:71]\n" \
"v_cvt_pk_bf16_f32 v172, v172, v173\n" \
"v_cvt_pk_bf16_f32 v173, v174, v175\n" \
"v_cvt_pk_bf16_f32 v174, v176, v177\n" \
"v_cvt_pk_bf16_f32 v175, v178, v179\n" \
"v_mfma_f32_16x16x32_bf16 v[64:67], v[74:77], v[232:235], 0\n" \
"ds_write_b128 v21, v[140:143] offset:21504\n" \
"v_mfma_f32_16x16x32_bf16 v[168:171], a[180:183], a[156:159], v[168:171]\n" \
"v_cvt_pk_bf16_f32 v52, v252, v253\n" \
"v_cvt_pk_bf16_f32 v53, v254, v255\n" \
"v_mfma_f32_16x16x32_bf16 v[168:171], a[244:247], a[220:223], v[168:171]\n" \
"v_exp_f32_e64 v45, v45\n" \
"v_mfma_f32_16x16x32_bf16 v[192:195], v[52:55], v[184:187], 0\n" \
"ds_write_b128 v21, v[148:151] offset:24576\n" \
"v_mfma_f32_16x16x32_bf16 v[208:211], v[54:57], v[200:203], 0\n" \
"v_mfma_f32_16x16x32_bf16 v[224:227], v[56:59], v[216:219], 0\n" \
"v_mfma_f32_16x16x32_bf16 v[240:243], v[58:61], v[232:235], 0\n" \
"ds_write_b128 v21, v[156:159] offset:25600\n" \
"v_mov_b32_dpp v34, v38 quad_perm:[0,1,2,3] row_mask:0x2 bank_mask:0xf\n" \
"v_pk_mul_f32 v[168:169], v[168:169], s[70:71]\n" \
"v_pk_mul_f32 v[170:171], v[170:171], s[70:71]\n" \
"v_mov_b32_dpp v34, v250 quad_perm:[0,1,2,3] row_mask:0x4 bank_mask:0xf\n" \
"v_add_f32_e64 v45, 1.0, v45\n" \
"v_mfma_f32_16x16x32_bf16 v[164:167], a[176:179], a[156:159], v[164:167]\n" \
"v_mov_b32_dpp v34, v66 quad_perm:[0,1,2,3] row_mask:0x8 bank_mask:0xf\n" \
"v_mov_b32_dpp v32, v36 quad_perm:[0,1,2,3] row_mask:0x2 bank_mask:0xf\n" \
"v_rcp_f32_e64 v45, v45\n" \
"ds_write_b128 v21, v[172:175] offset:29696\n" \
"v_mov_b32_dpp v33, v37 quad_perm:[0,1,2,3] row_mask:0x2 bank_mask:0xf\n" \
"v_rcp_f32_e64 v34, v34\n" \
"v_mov_b32_dpp v32, v248 quad_perm:[0,1,2,3] row_mask:0x4 bank_mask:0xf\n" \
"v_mov_b32_dpp v33, v249 quad_perm:[0,1,2,3] row_mask:0x4 bank_mask:0xf\n" \
"v_mul_f32_e64 v34, v45, v34\n" \
"v_mov_b32_dpp v32, v64 quad_perm:[0,1,2,3] row_mask:0x8 bank_mask:0xf\n" \
"v_mov_b32_dpp v33, v65 quad_perm:[0,1,2,3] row_mask:0x8 bank_mask:0xf\n" \
"v_mul_f32_e32 v32, v34, v32\n" \
"v_mul_f32_e32 v33, v34, v33\n" \
"v_mov_b32_e64 v38, v34\n" \
"v_mfma_f32_16x16x32_bf16 v[164:167], a[240:243], a[220:223], v[164:167]\n" \
"v_sub_f32_e32 v44, 1.0, v45\n" \
"v_permlane16_swap_b32_e32 v34, v38\n" \
"v_mov_b32_e32 v250, v34\n" \
"v_mov_b32_e32 v66, v38\n" \
"v_cvt_pk_bf16_f32 v32, v32, v33\n" \
"v_permlane32_swap_b32_e32 v34, v250\n" \
"v_permlane32_swap_b32_e32 v38, v66\n" \
"v_mul_f32_e64 v192, v34, v192\n" \
"v_pk_mul_f32 v[164:165], v[164:165], s[70:71]\n" \
"v_pk_mul_f32 v[166:167], v[166:167], s[70:71]\n" \
"v_cvt_pk_bf16_f32 v164, v164, v165\n" \
"v_cvt_pk_bf16_f32 v165, v166, v167\n" \
"v_cvt_pk_bf16_f32 v166, v168, v169\n" \
"v_cvt_pk_bf16_f32 v167, v170, v171\n" \
"ds_write_b128 v21, v[164:167] offset:28672\n" \
"v_mul_f32_e32 v193, v34, v193\n" \
"v_mul_f32_e32 v208, v38, v208\n" \
"v_mul_f32_e32 v209, v38, v209\n" \
"v_mul_f32_e32 v224, v250, v224\n" \
"v_mul_f32_e32 v225, v250, v225\n" \
"v_mul_f32_e32 v240, v66, v240\n" \
"v_mul_f32_e32 v241, v66, v241\n" \
"v_mul_f32_e32 v194, v34, v194\n" \
"v_mul_f32_e64 v195, v34, v195\n" \
"v_cvt_pk_bf16_f32 v192, v192, v193\n" \
"v_mul_f32_e32 v210, v38, v210\n" \
"v_mul_f32_e32 v211, v38, v211\n" \
"v_cvt_pk_bf16_f32 v208, v208, v209\n" \
"v_mul_f32_e32 v226, v250, v226\n" \
"v_mul_f32_e32 v227, v250, v227\n" \
"v_cvt_pk_bf16_f32 v224, v224, v225\n" \
"v_mul_f32_e32 v242, v66, v242\n" \
"v_mul_f32_e32 v243, v66, v243\n" \
"v_cvt_pk_bf16_f32 v240, v240, v241\n" \
"v_cvt_pk_bf16_f32 v193, v194, v195\n" \
"s_add_u32 s84, s48, 0x0\n" \
"s_nop 0\n" \
"v_cvt_pk_bf16_f32 v209, v210, v211\n" \
"s_add_u32 s85, s48, 0x24800\n" \
"v_cvt_pk_bf16_f32 v225, v226, v227\n" \
"s_add_u32 s86, s48, 0x49000\n" \
"v_cvt_pk_bf16_f32 v241, v242, v243\n" \
"s_add_u32 s87, s48, 0x6d800\n" \
"ds_write_b32 v26, v44\n" \
"buffer_store_dword v32, v25, s[80:83], s49 offen sc1\n" \
"buffer_store_dwordx2 v[192:193], v24, s[80:83], s84 offen sc1\n" \
"buffer_store_dwordx2 v[208:209], v24, s[80:83], s85 offen sc1\n" \
"buffer_store_dwordx2 v[224:225], v24, s[80:83], s86 offen sc1\n" \
"buffer_store_dwordx2 v[240:241], v24, s[80:83], s87 offen sc1\n" \
"s_branch L_end_%=\n" \
"L_poll60_%=:\n" \
"s_mov_b32 s84, 0\n" \
"L_pl60_%=:\n" \
"s_sleep 1\n" \
"v_bfe_u32 v44, v20, 2, 5\n" \
"buffer_load_dword v44, v44, s[80:83], s38 offen offset:128 sc1\n" \
"s_waitcnt vmcnt(0)\n" \
"v_cmp_ne_u32_e32 vcc, s62, v44\n" \
"s_add_u32 s84, s84, 1\n" \
"s_nop 3\n" \
"s_cbranch_vccz L_pollr60_%=\n" \
"s_cmp_gt_u32 s84, 0x400000\n" \
"s_cbranch_scc0 L_pl60_%=\n" \
"s_branch L_pollr60_%=\n" \
"L_poll160_%=:\n" \
"s_mov_b32 s84, 0\n" \
"L_pl160_%=:\n" \
"s_sleep 1\n" \
"v_bfe_u32 v44, v20, 2, 5\n" \
"buffer_load_dword v44, v44, s[80:83], s38 offen offset:0 sc1\n" \
"s_waitcnt vmcnt(0)\n" \
"v_cmp_ne_u32_e32 vcc, s62, v44\n" \
"s_add_u32 s84, s84, 1\n" \
"s_nop 3\n" \
"s_cbranch_vccz L_pollr160_%=\n" \
"s_cmp_gt_u32 s84, 0x400000\n" \
"s_cbranch_scc0 L_pl160_%=\n" \
"s_branch L_pollr160_%=\n" \
".fill (L_pb + 42548 - .) / 4, 4, 0xBF800000\n" \
"L_w1_%=:\n" \
"s_waitcnt vmcnt(19)\n" \
"s_barrier\n" \
"s_nop 0\n" \
"ds_read_b64_tr_b16 a[160:161], v32 offset:0\n" \
"ds_read_b64_tr_b16 a[162:163], v32 offset:1024\n" \
"ds_read_b128 a[128:131], v28 offset:16384\n" \
"ds_read_b64_tr_b16 a[224:225], v32 offset:8192\n" \
"ds_read_b64_tr_b16 a[226:227], v32 offset:9216\n" \
"ds_read_b128 a[192:195], v29 offset:16384\n" \
"ds_read_b128 a[132:135], v28 offset:18432\n" \
"ds_read_b128 a[196:199], v29 offset:18432\n" \
"ds_read_b64_tr_b16 a[164:165], v33 offset:0\n" \
"ds_read_b64_tr_b16 a[166:167], v33 offset:1024\n" \
"ds_read_b64_tr_b16 a[228:229], v33 offset:8192\n" \
"ds_read_b64_tr_b16 a[230:231], v33 offset:9216\n" \
"ds_read_b128 a[136:139], v28 offset:20480\n" \
"ds_read_b128 a[200:203], v29 offset:20480\n" \
"s_waitcnt lgkmcnt(11)\n" \
"v_mfma_f32_16x16x32_bf16 a[0:3], a[128:131], a[160:163], 0\n" \
"ds_read_b64_tr_b16 a[168:169], v34 offset:0\n" \
"s_lshr_b32 s4, s2, 1\n" \
"s_waitcnt lgkmcnt(9)\n" \
"v_mfma_f32_16x16x32_bf16 a[0:3], a[192:195], a[224:227], a[0:3]\n" \
"ds_read_b64_tr_b16 a[170:171], v34 offset:1024\n" \
"s_and_b32 s4, s4, 0x7c\n" \
"s_waitcnt lgkmcnt(9)\n" \
"v_mfma_f32_16x16x32_bf16 a[16:19], a[132:135], a[160:163], 0\n" \
"ds_read_b64_tr_b16 a[232:233], v34 offset:8192\n" \
"v_mov_b32_e32 v213, s13\n" \
"s_waitcnt lgkmcnt(9)\n" \
"v_mfma_f32_16x16x32_bf16 a[16:19], a[196:199], a[224:227], a[16:19]\n" \
"ds_read_b64_tr_b16 a[234:235], v34 offset:9216\n" \
"s_or_b32 s4, s4, s3\n" \
"s_waitcnt lgkmcnt(8)\n" \
"v_mfma_f32_16x16x32_bf16 a[20:23], a[132:135], a[164:167], 0\n" \
"ds_read_b128 a[140:143], v28 offset:22528\n" \
"v_lshrrev_b32_e32 v212, 4, v20\n" \
"s_waitcnt lgkmcnt(7)\n" \
"v_mfma_f32_16x16x32_bf16 a[20:23], a[196:199], a[228:231], a[20:23]\n" \
"ds_read_b128 a[204:207], v29 offset:22528\n" \
"v_mad_u64_u32 v[214:215], s[4:5], s4, 36, v[212:213]\n" \
"v_mfma_f32_16x16x32_bf16 a[4:7], a[128:131], a[164:167], 0\n" \
"ds_read_b64_tr_b16 a[172:173], v35 offset:0\n" \
"v_mov_b32_e64 v217, s9\n" \
"v_mfma_f32_16x16x32_bf16 a[4:7], a[192:195], a[228:231], a[4:7]\n" \
"ds_read_b64_tr_b16 a[174:175], v35 offset:1024\n" \
"s_movk_i32 s4, 0x240\n" \
"s_waitcnt lgkmcnt(9)\n" \
"v_mfma_f32_16x16x32_bf16 a[32:35], a[136:139], a[160:163], 0\n" \
"ds_read_b64_tr_b16 a[236:237], v35 offset:8192\n" \
"v_cmp_gt_u32_e32 vcc, s4, v20\n" \
"s_waitcnt lgkmcnt(9)\n" \
"v_mfma_f32_16x16x32_bf16 a[32:35], a[200:203], a[224:227], a[32:35]\n" \
"ds_read_b64_tr_b16 a[238:239], v35 offset:9216\n" \
"s_lshl_b32 s4, s2, 3\n" \
"v_mfma_f32_16x16x32_bf16 a[36:39], a[136:139], a[164:167], 0\n" \
"ds_read_b128 a[144:147], v28 offset:0\n" \
"s_and_b32 s4, s4, 56\n" \
"v_mfma_f32_16x16x32_bf16 a[36:39], a[200:203], a[228:231], a[36:39]\n" \
"ds_read_b128 a[208:211], v29 offset:0\n" \
"v_cndmask_b32_e32 v217, v217, v213, vcc\n" \
"s_waitcnt lgkmcnt(10)\n" \
"v_mfma_f32_16x16x32_bf16 a[40:43], a[136:139], a[168:171], 0\n" \
"ds_read_b128 a[148:151], v28 offset:2048\n" \
"v_lshlrev_b32_e32 v213, 3, v214\n" \
"s_waitcnt lgkmcnt(9)\n" \
"v_mfma_f32_16x16x32_bf16 a[40:43], a[200:203], a[232:235], a[40:43]\n" \
"ds_read_b128 a[212:215], v29 offset:2048\n" \
"v_and_b32_e32 v213, 0x1ffc0, v213\n" \
"v_mfma_f32_16x16x32_bf16 a[24:27], a[132:135], a[168:171], 0\n" \
"ds_read_b128 a[152:155], v28 offset:4096\n" \
"v_and_b32_e64 v214, 7, v214\n" \
"v_mfma_f32_16x16x32_bf16 a[24:27], a[196:199], a[232:235], a[24:27]\n" \
"ds_read_b128 a[216:219], v29 offset:4096\n" \
"v_or3_b32 v213, v214, s4, v213\n" \
"v_mfma_f32_16x16x32_bf16 a[8:11], a[128:131], a[168:171], 0\n" \
"ds_read_b128 a[156:159], v28 offset:6144\n" \
"s_lshl_b32 s4, s2, 2\n" \
"v_mfma_f32_16x16x32_bf16 a[8:11], a[192:195], a[232:235], a[8:11]\n" \
"ds_read_b128 a[220:223], v29 offset:6144\n" \
"s_mov_b32 m0, s32\n" \
"s_waitcnt lgkmcnt(13)\n" \
"v_mfma_f32_16x16x32_bf16 a[48:51], a[140:143], a[160:163], 0\n" \
"s_mov_b32 exec_lo, 0xff000000\n" \
"s_mov_b32 exec_hi, 0x0\n" \
"global_load_lds_dword v[184:185], off\n" \
"s_mov_b64 exec, -1\n" \
"s_waitcnt lgkmcnt(12)\n" \
"v_mfma_f32_16x16x32_bf16 a[48:51], a[204:207], a[224:227], a[48:51]\n" \
"ds_read_b64_tr_b16 a[176:177], v36 offset:0\n" \
"s_or_b32 s4, s4, s3\n" \
"v_mfma_f32_16x16x32_bf16 a[52:55], a[140:143], a[164:167], 0\n" \
"ds_read_b64_tr_b16 a[178:179], v36 offset:1024\n" \
"s_mul_i32 s4, s4, 18\n" \
"v_mfma_f32_16x16x32_bf16 a[52:55], a[204:207], a[228:231], a[52:55]\n" \
"s_mov_b32 m0, s32\n" \
"ds_read_b64_tr_b16 a[240:241], v36 offset:8192\n" \
"v_mfma_f32_16x16x32_bf16 a[56:59], a[140:143], a[168:171], 0\n" \
"s_mov_b32 exec_lo, 0xffff\n" \
"s_mov_b32 exec_hi, 0x0\n" \
"global_load_lds_dword v[186:187], off\n" \
"s_mov_b64 exec, -1\n" \
"v_mfma_f32_16x16x32_bf16 a[56:59], a[204:207], a[232:235], a[56:59]\n" \
"ds_read_b64_tr_b16 a[242:243], v36 offset:9216\n" \
"v_min_u32_e32 v212, 53, v212\n" \
"s_waitcnt lgkmcnt(14)\n" \
"v_mfma_f32_16x16x32_bf16 a[60:63], a[140:143], a[172:175], 0\n" \
"ds_read_b64_tr_b16 a[180:181], v37 offset:0\n" \
"s_sub_i32 s4, s4, 36\n" \
"s_waitcnt lgkmcnt(13)\n" \
"v_mfma_f32_16x16x32_bf16 a[60:63], a[204:207], a[236:239], a[60:63]\n" \
"ds_read_b64_tr_b16 a[182:183], v37 offset:1024\n" \
"v_add_u32_e32 v212, s4, v212\n" \
"v_mfma_f32_16x16x32_bf16 a[44:47], a[136:139], a[172:175], 0\n" \
"ds_read_b64_tr_b16 a[244:245], v37 offset:8192\n" \
"v_mov_b32_e64 v215, s12\n" \
"v_mfma_f32_16x16x32_bf16 a[44:47], a[200:203], a[236:239], a[44:47]\n" \
"ds_read_b64_tr_b16 a[246:247], v37 offset:9216\n" \
"v_mov_b32_e64 v216, s8\n" \
"v_mfma_f32_16x16x32_bf16 a[28:31], a[132:135], a[172:175], 0\n" \
"ds_read_b64_tr_b16 a[184:185], v38 offset:0\n" \
"v_cndmask_b32_e64 v212, v212, v213, vcc\n" \
"v_mfma_f32_16x16x32_bf16 a[28:31], a[196:199], a[236:239], a[28:31]\n" \
"ds_read_b64_tr_b16 a[186:187], v38 offset:1024\n" \
"v_cndmask_b32_e64 v216, v216, v215, vcc\n" \
"v_mfma_f32_16x16x32_bf16 a[12:15], a[128:131], a[172:175], 0\n" \
"v_lshlrev_b32_e32 v212, 7, v212\n" \
"v_mov_b32_e32 v213, 0\n" \
"v_mfma_f32_16x16x32_bf16 a[12:15], a[192:195], a[236:239], a[12:15]\n" \
"v_lshl_add_u64 v[188:189], v[216:217], 0, v[212:213]\n" \
"s_movk_i32 s5, 0xd80\n" \
"s_waitcnt lgkmcnt(15)\n" \
"v_mfma_f32_16x16x32_bf16 a[76:79], a[144:147], a[172:175], 0\n" \
"ds_read_b64_tr_b16 a[248:249], v38 offset:8192\n" \
"s_lshr_b32 s6, s2, 5\n" \
"v_mfma_f32_16x16x32_bf16 a[76:79], a[208:211], a[236:239], a[76:79]\n" \
"v_lshrrev_b32_e32 v212, 4, v20\n" \
"v_lshl_or_b32 v215, s3, 6, v212\n" \
"v_mfma_f32_16x16x32_bf16 a[72:75], a[144:147], a[168:171], 0\n" \
"v_mul_u32_u24_e32 v216, 0x2aab, v215\n" \
"v_lshrrev_b32_e64 v216, 16, v216\n" \
"v_mfma_f32_16x16x32_bf16 a[72:75], a[208:211], a[232:235], a[72:75]\n" \
"ds_read_b64_tr_b16 a[250:251], v38 offset:9216\n" \
"v_mad_i32_i24 v217, v216, -6, v215\n" \
"v_mfma_f32_16x16x32_bf16 a[68:71], a[144:147], a[164:167], 0\n" \
"v_add_u32_e32 v218, 0xfffffe50, v215\n" \
"s_mul_i32 s35, s6, 0x48\n" \
"v_mfma_f32_16x16x32_bf16 a[68:71], a[208:211], a[228:231], a[68:71]\n" \
"v_lshrrev_b32_e32 v219, 1, v217\n" \
"v_add_u32_e32 v218, s35, v216\n" \
"v_mfma_f32_16x16x32_bf16 a[64:67], a[144:147], a[160:163], 0\n" \
"v_mul_u32_u24_e32 v219, 0x480, v219\n" \
"v_and_b32_e64 v214, 16, v20\n" \
"v_mfma_f32_16x16x32_bf16 a[64:67], a[208:211], a[224:227], a[64:67]\n" \
"ds_read_b64_tr_b16 a[188:189], v39 offset:0\n" \
"v_mad_u32_u24 v219, v218, s5, v219\n" \
"v_mfma_f32_16x16x32_bf16 a[80:83], a[148:151], a[160:163], 0\n" \
"v_or_b32_e32 v192, v219, v214\n" \
"v_mov_b32_e32 v220, 2.0\n" \
"s_waitcnt lgkmcnt(15)\n" \
"v_mfma_f32_16x16x32_bf16 a[80:83], a[212:215], a[224:227], a[80:83]\n" \
"v_mov_b32_e32 v222, 7\n" \
"v_lshlrev_b32_e64 v220, 7, v218\n" \
"v_mfma_f32_16x16x32_bf16 a[84:87], a[148:151], a[164:167], 0\n" \
"ds_read_b64_tr_b16 a[190:191], v39 offset:1024\n" \
"v_lshrrev_b32_e64 v218, 1, v218\n" \
"v_mfma_f32_16x16x32_bf16 a[84:87], a[212:215], a[228:231], a[84:87]\n" \
"v_and_b32_e32 v218, 4, v218\n" \
"v_and_b32_e32 v216, 2, v216\n" \
"v_mfma_f32_16x16x32_bf16 a[88:91], a[148:151], a[168:171], 0\n" \
"s_mul_i32 s7, s6, 0x2400\n" \
"s_sub_i32 s34, 0x22000, s7\n" \
"v_mfma_f32_16x16x32_bf16 a[88:91], a[212:215], a[232:235], a[88:91]\n" \
"ds_read_b64_tr_b16 a[252:253], v39 offset:8192\n" \
"v_bitop3_b32 v216, v217, v218, v216 bitop3:0x1e\n" \
"v_mfma_f32_16x16x32_bf16 a[92:95], a[148:151], a[172:175], 0\n" \
"v_or_b32_e32 v213, 0x24400, v20\n" \
"v_lshlrev_b32_e64 v216, 4, v216\n" \
"v_mfma_f32_16x16x32_bf16 a[92:95], a[212:215], a[236:239], a[92:95]\n" \
"v_add3_u32 v196, v220, s34, v216\n" \
"v_mov_b32_e64 v216, v213\n" \
"v_mfma_f32_16x16x32_bf16 a[108:111], a[152:155], a[172:175], 0\n" \
"ds_read_b64_tr_b16 a[254:255], v39 offset:9216\n" \
"s_movk_i32 s36, 0x1b0\n" \
"v_mfma_f32_16x16x32_bf16 a[108:111], a[216:219], a[236:239], a[108:111]\n" \
"v_or_b32_e32 v216, 0x100, v215\n" \
"v_mul_u32_u24_e32 v217, 0x2aab, v216\n" \
"v_mfma_f32_16x16x32_bf16 a[104:107], a[152:155], a[168:171], 0\n" \
"v_add_u32_e32 v215, 0xffffff50, v215\n" \
"v_lshrrev_b32_e32 v217, 16, v217\n" \
"v_mfma_f32_16x16x32_bf16 a[104:107], a[216:219], a[232:235], a[104:107]\n" \
"v_mov_b32_e64 v220, v215\n" \
"v_mad_i32_i24 v218, v217, -6, v216\n" \
"v_mfma_f32_16x16x32_bf16 a[100:103], a[152:155], a[164:167], 0\n" \
"v_cmp_gt_u32_e32 vcc, s36, v216\n" \
"v_mov_b32_e32 v221, 7\n" \
"v_mfma_f32_16x16x32_bf16 a[100:103], a[216:219], a[228:231], a[100:103]\n" \
"v_lshrrev_b32_e32 v222, 1, v218\n" \
"v_cndmask_b32_e32 v217, v220, v217, vcc\n" \
"v_mfma_f32_16x16x32_bf16 a[96:99], a[152:155], a[160:163], 0\n" \
"s_movk_i32 s4, 0x48\n" \
"v_add_u32_e32 v220, s35, v217\n" \
"v_mfma_f32_16x16x32_bf16 a[96:99], a[216:219], a[224:227], a[96:99]\n" \
"v_mul_u32_u24_e32 v222, 0x480, v222\n" \
"v_mad_u32_u24 v222, v220, s5, v222\n" \
"s_waitcnt lgkmcnt(15)\n" \
"v_mfma_f32_16x16x32_bf16 a[112:115], a[156:159], a[160:163], 0\n" \
"v_cmp_gt_u32_e64 s[4:5], s4, v215\n" \
"v_cndmask_b32_e32 v215, v221, v218, vcc\n" \
"v_mfma_f32_16x16x32_bf16 a[112:115], a[220:223], a[224:227], a[112:115]\n" \
"v_lshrrev_b32_e32 v218, 1, v220\n" \
"v_or_b32_e32 v214, v222, v214\n" \
"v_mfma_f32_16x16x32_bf16 a[116:119], a[156:159], a[164:167], 0\n" \
"v_mov_b32_e32 v222, 2.0\n" \
"v_and_b32_e32 v218, 4, v218\n" \
"v_mfma_f32_16x16x32_bf16 a[116:119], a[220:223], a[228:231], a[116:119]\n" \
"v_and_b32_e64 v217, 2, v217\n" \
"v_bitop3_b32 v215, v215, v218, v217 bitop3:0x1e\n" \
"v_mfma_f32_16x16x32_bf16 a[120:123], a[156:159], a[168:171], 0\n" \
"v_lshlrev_b32_e32 v223, 1, v220\n" \
"v_mov_b32_e32 v224, 2.0\n" \
"v_mfma_f32_16x16x32_bf16 a[120:123], a[220:223], a[232:235], a[120:123]\n" \
"v_cndmask_b32_e32 v193, v222, v214, vcc\n" \
"v_lshlrev_b32_e32 v214, 7, v220\n" \
"v_mfma_f32_16x16x32_bf16 a[124:127], a[156:159], a[172:175], 0\n" \
"v_lshlrev_b32_e32 v215, 4, v215\n" \
"s_mul_i32 s39, s3, 18\n" \
"v_mfma_f32_16x16x32_bf16 a[124:127], a[220:223], a[236:239], a[124:127]\n" \
"s_waitcnt vmcnt(5) lgkmcnt(0)\n" \
"s_barrier\n" \
"v_mfma_f32_16x16x32_bf16 v[52:55], a[128:131], a[176:179], 0\n" \
"s_add_u32 m0, s30, 0x0\n" \
"s_movk_i32 s84, 0x100\n" \
"v_mfma_f32_16x16x32_bf16 v[52:55], a[192:195], a[240:243], v[52:55]\n" \
"buffer_load_dwordx4 v22, s[64:67], s84 offen offset:0 lds\n" \
"v_mfma_f32_16x16x32_bf16 v[56:59], a[128:131], a[180:183], 0\n" \
"ds_read_b64_tr_b16 a[160:161], v32 offset:32768\n" \
"v_add3_u32 v214, v214, s34, v215\n" \
"v_mfma_f32_16x16x32_bf16 v[56:59], a[192:195], a[244:247], v[56:59]\n" \
"s_movk_i32 s84, 0x1d00\n" \
"v_mfma_f32_16x16x32_bf16 v[60:63], a[128:131], a[184:187], 0\n" \
"buffer_load_dwordx4 v23, s[64:67], s84 offen offset:1024 lds\n" \
"v_mfma_f32_16x16x32_bf16 v[60:63], a[192:195], a[248:251], v[60:63]\n" \
"ds_read_b64_tr_b16 a[162:163], v32 offset:33792\n" \
"v_cndmask_b32_e64 v195, v224, v223, s[4:5]\n" \
"v_mfma_f32_16x16x32_bf16 v[64:67], a[128:131], a[188:191], 0\n" \
"ds_read_b64_tr_b16 a[224:225], v32 offset:40960\n" \
"s_movk_i32 s4, 0x1f8\n" \
"v_mfma_f32_16x16x32_bf16 v[64:67], a[192:195], a[252:255], v[64:67]\n" \
"ds_read_b128 a[128:131], v28 offset:49152\n" \
"v_mov_b32_e64 v215, v213\n" \
"v_mfma_f32_16x16x32_bf16 v[80:83], a[132:135], a[188:191], 0\n" \
"ds_read_b128 a[192:195], v29 offset:49152\n" \
"s_add_i32 s35, s35, s39\n" \
"v_mfma_f32_16x16x32_bf16 v[80:83], a[196:199], a[252:255], v[80:83]\n" \
"s_movk_i32 s84, 0x3900\n" \
"v_mfma_f32_16x16x32_bf16 v[76:79], a[132:135], a[184:187], 0\n" \
"buffer_load_dwordx4 v22, s[64:67], s84 offen offset:2048 lds\n" \
"v_mfma_f32_16x16x32_bf16 v[76:79], a[196:199], a[248:251], v[76:79]\n" \
"s_movk_i32 s84, 0x5500\n" \
"v_mfma_f32_16x16x32_bf16 v[72:75], a[132:135], a[180:183], 0\n" \
"buffer_load_dwordx4 v23, s[64:67], s84 offen offset:3072 lds\n" \
"v_mfma_f32_16x16x32_bf16 v[72:75], a[196:199], a[244:247], v[72:75]\n" \
"s_add_u32 m0, s31, 0x0\n" \
"s_mov_b32 s84, 0x100000\n" \
"v_mfma_f32_16x16x32_bf16 v[68:71], a[132:135], a[176:179], 0\n" \
"buffer_load_dwordx4 v26, s[68:71], s84 offen offset:0 lds\n" \
"v_mfma_f32_16x16x32_bf16 v[68:71], a[196:199], a[240:243], v[68:71]\n" \
"ds_read_b128 a[132:135], v28 offset:51200\n" \
"v_cmp_gt_u32_e64 vcc, s4, v216\n" \
"v_mfma_f32_16x16x32_bf16 v[84:87], a[136:139], a[176:179], 0\n" \
"ds_read_b128 a[196:199], v29 offset:51200\n" \
"v_add_u32_e64 v212, s35, v212\n" \
"v_mfma_f32_16x16x32_bf16 v[84:87], a[200:203], a[240:243], v[84:87]\n" \
"s_addk_i32 s84, 0x7c00\n" \
"v_mfma_f32_16x16x32_bf16 v[88:91], a[136:139], a[180:183], 0\n" \
"buffer_load_dwordx4 v26, s[68:71], s84 offen offset:1024 lds\n" \
"v_mfma_f32_16x16x32_bf16 v[88:91], a[200:203], a[244:247], v[88:91]\n" \
"ds_read_b64_tr_b16 a[226:227], v32 offset:41984\n" \
"v_mov_b32_e32 v219, 2.0\n" \
"v_mfma_f32_16x16x32_bf16 v[92:95], a[136:139], a[184:187], 0\n" \
"s_addk_i32 s84, 0x7c00\n" \
"v_mfma_f32_16x16x32_bf16 v[92:95], a[200:203], a[248:251], v[92:95]\n" \
"buffer_load_dwordx4 v27, s[68:71], s84 offen offset:2048 lds\n" \
"v_mfma_f32_16x16x32_bf16 v[96:99], a[136:139], a[188:191], 0\n" \
"ds_read_b64_tr_b16 a[164:165], v33 offset:32768\n" \
"v_cndmask_b32_e32 v197, v215, v214, vcc\n" \
"v_mfma_f32_16x16x32_bf16 v[96:99], a[200:203], a[252:255], v[96:99]\n" \
"ds_read_b128 a[136:139], v28 offset:53248\n" \
"s_movk_i32 s35, 0x120\n" \
"v_mfma_f32_16x16x32_bf16 v[112:115], a[140:143], a[188:191], 0\n" \
"ds_read_b128 a[200:203], v29 offset:53248\n" \
"v_mul_u32_u24_e32 v214, 0xd80, v212\n" \
"v_mfma_f32_16x16x32_bf16 v[112:115], a[204:207], a[252:255], v[112:115]\n" \
"s_addk_i32 s84, 0x7c00\n" \
"v_mfma_f32_16x16x32_bf16 v[108:111], a[140:143], a[184:187], 0\n" \
"buffer_load_dwordx4 v27, s[68:71], s84 offen offset:3072 lds\n" \
"v_mfma_f32_16x16x32_bf16 v[108:111], a[204:207], a[248:251], v[108:111]\n" \
"s_add_u32 m0, s30, 0x1000\n" \
"s_mov_b32 s84, 0x8100\n" \
"v_mfma_f32_16x16x32_bf16 v[104:107], a[140:143], a[180:183], 0\n" \
"buffer_load_dwordx4 v22, s[64:67], s84 offen offset:0 lds\n" \
"v_mfma_f32_16x16x32_bf16 v[104:107], a[204:207], a[244:247], v[104:107]\n" \
"ds_read_b64_tr_b16 a[166:167], v33 offset:33792\n" \
"v_cmp_gt_u32_e64 vcc, s35, v20\n" \
"v_mfma_f32_16x16x32_bf16 v[100:103], a[140:143], a[176:179], 0\n" \
"ds_read_b64_tr_b16 a[228:229], v33 offset:40960\n" \
"v_lshrrev_b32_e64 v215, 1, v212\n" \
"v_mfma_f32_16x16x32_bf16 v[100:103], a[204:207], a[240:243], v[100:103]\n" \
"ds_read_b128 a[140:143], v28 offset:55296\n" \
"s_and_b32 s4, s2, 7\n" \
"v_mfma_f32_16x16x32_bf16 v[116:119], a[144:147], a[176:179], 0\n" \
"ds_read_b128 a[204:207], v29 offset:55296\n" \
"v_cndmask_b32_e32 v194, v219, v214, vcc\n" \
"v_mfma_f32_16x16x32_bf16 v[116:119], a[208:211], a[240:243], v[116:119]\n" \
"s_addk_i32 s84, 0x1c00\n" \
"v_mfma_f32_16x16x32_bf16 v[120:123], a[144:147], a[180:183], 0\n" \
"buffer_load_dwordx4 v23, s[64:67], s84 offen offset:1024 lds\n" \
"v_mfma_f32_16x16x32_bf16 v[120:123], a[208:211], a[244:247], v[120:123]\n" \
"s_addk_i32 s84, 0x1c00\n" \
"v_mfma_f32_16x16x32_bf16 v[124:127], a[144:147], a[184:187], 0\n" \
"buffer_load_dwordx4 v22, s[64:67], s84 offen offset:2048 lds\n" \
"v_mfma_f32_16x16x32_bf16 v[124:127], a[208:211], a[248:251], v[124:127]\n" \
"s_addk_i32 s84, 0x1c00\n" \
".fill (L_pb + 44768 - .) / 4, 4, 0xBF800000\n" \
"L_pp11:\n" \
"s_cbranch_execz L_pret\n" \
"v_mfma_f32_16x16x32_bf16 v[128:131], a[144:147], a[188:191], 0\n" \
"buffer_load_dwordx4 v23, s[64:67], s84 offen offset:3072 lds\n" \
"v_mfma_f32_16x16x32_bf16 v[128:131], a[208:211], a[252:255], v[128:131]\n" \
"ds_read_b128 a[144:147], v28 offset:32768\n" \
"v_lshlrev_b32_e32 v214, 7, v212\n" \
"v_mfma_f32_16x16x32_bf16 v[144:147], a[148:151], a[188:191], 0\n" \
"ds_read_b128 a[208:211], v29 offset:32768\n" \
"v_lshrrev_b32_e64 v212, 2, v212\n" \
"v_mfma_f32_16x16x32_bf16 v[144:147], a[212:215], a[252:255], v[144:147]\n" \
"s_add_u32 m0, s31, 0x4000\n" \
"s_mov_b32 s84, 0x100000\n" \
"v_mfma_f32_16x16x32_bf16 v[140:143], a[148:151], a[184:187], 0\n" \
"buffer_load_dwordx4 v26, s[72:75], s84 offen offset:0 lds\n" \
"v_mfma_f32_16x16x32_bf16 v[140:143], a[212:215], a[248:251], v[140:143]\n" \
"ds_read_b64_tr_b16 a[230:231], v33 offset:41984\n" \
"v_and_b32_e64 v212, 2, v212\n" \
"v_mfma_f32_16x16x32_bf16 v[136:139], a[148:151], a[180:183], 0\n" \
"s_addk_i32 s84, 0x7c00\n" \
"v_mfma_f32_16x16x32_bf16 v[136:139], a[212:215], a[244:247], v[136:139]\n" \
"buffer_load_dwordx4 v26, s[72:75], s84 offen offset:1024 lds\n" \
"v_mfma_f32_16x16x32_bf16 v[132:135], a[148:151], a[176:179], 0\n" \
"ds_read_b64_tr_b16 a[168:169], v34 offset:32768\n" \
"v_and_or_b32 v212, v215, 1, v212\n" \
"v_mfma_f32_16x16x32_bf16 v[132:135], a[212:215], a[240:243], v[132:135]\n" \
"ds_read_b128 a[148:151], v28 offset:34816\n" \
"s_lshl_b32 s5, s4, 2\n" \
"v_mfma_f32_16x16x32_bf16 v[148:151], a[152:155], a[176:179], 0\n" \
"ds_read_b128 a[212:215], v29 offset:34816\n" \
"s_bfe_u32 s38, s2, 0x20003\n" \
"v_mfma_f32_16x16x32_bf16 v[148:151], a[216:219], a[240:243], v[148:151]\n" \
"ds_read_b64_tr_b16 a[170:171], v34 offset:33792\n" \
"v_lshlrev_b32_e64 v212, 5, v212\n" \
"v_mfma_f32_16x16x32_bf16 v[152:155], a[152:155], a[180:183], 0\n" \
"ds_read_b64_tr_b16 a[232:233], v34 offset:40960\n" \
"s_movk_i32 s35, 0x60\n" \
"v_mfma_f32_16x16x32_bf16 v[152:155], a[216:219], a[244:247], v[152:155]\n" \
"ds_read_b64_tr_b16 a[234:235], v34 offset:41984\n" \
"s_or_b32 s5, s5, s38\n" \
"v_mfma_f32_16x16x32_bf16 v[156:159], a[152:155], a[184:187], 0\n" \
"ds_read_b64_tr_b16 a[172:173], v35 offset:32768\n" \
"v_bitop3_b32 v212, v212, s35, v214 bitop3:0x36\n" \
"v_mfma_f32_16x16x32_bf16 v[156:159], a[216:219], a[248:251], v[156:159]\n" \
"ds_read_b64_tr_b16 a[174:175], v35 offset:33792\n" \
"s_lshr_b32 s36, s2, 3\n" \
"v_mfma_f32_16x16x32_bf16 v[160:163], a[152:155], a[188:191], 0\n" \
"ds_read_b64_tr_b16 a[236:237], v35 offset:40960\n" \
"v_add_u32_e32 v212, s34, v212\n" \
"v_mfma_f32_16x16x32_bf16 v[160:163], a[216:219], a[252:255], v[160:163]\n" \
"ds_read_b128 a[152:155], v28 offset:36864\n" \
"s_mul_i32 s34, s5, 0x12000\n" \
"v_mfma_f32_16x16x32_bf16 v[176:179], a[156:159], a[188:191], 0\n" \
"ds_read_b128 a[216:219], v29 offset:36864\n" \
"s_or_b32 s40, s34, 0x6000000\n" \
"v_mfma_f32_16x16x32_bf16 v[176:179], a[220:223], a[252:255], v[176:179]\n" \
"ds_read_b64_tr_b16 a[238:239], v35 offset:41984\n" \
"s_mul_i32 s34, s36, 0x90000\n" \
"v_mfma_f32_16x16x32_bf16 v[172:175], a[156:159], a[184:187], 0\n" \
"s_lshl_b32 s36, s3, 14\n" \
"s_add_i32 s34, s34, s36\n" \
"v_mfma_f32_16x16x32_bf16 v[172:175], a[220:223], a[248:251], v[172:175]\n" \
"s_lshl_b32 s38, s5, 8\n" \
"s_add_i32 s41, s34, 0x4240000\n" \
"v_mfma_f32_16x16x32_bf16 v[168:171], a[156:159], a[180:183], 0\n" \
"s_mul_i32 s34, s5, 36\n" \
"s_add_i32 s5, s7, s40\n" \
"v_mfma_f32_16x16x32_bf16 v[168:171], a[220:223], a[244:247], v[168:171]\n" \
"s_addk_i32 s5, 0x2000\n" \
"s_mov_b32 s37, 2.0\n" \
"v_mfma_f32_16x16x32_bf16 v[164:167], a[156:159], a[176:179], 0\n" \
"s_or_b32 s38, s38, 0x5be8000\n" \
"v_mfma_f32_16x16x32_bf16 v[164:167], a[220:223], a[240:243], v[164:167]\n" \
"ds_read_b128 a[156:159], v28 offset:38912\n" \
"ds_read_b128 a[220:223], v29 offset:38912\n" \
"s_waitcnt lgkmcnt(15)\n" \
"v_mfma_f32_16x16x32_bf16 a[0:3], a[128:131], a[160:163], a[0:3]\n" \
"s_addk_i32 s84, 0x7c00\n" \
"v_mfma_f32_16x16x32_bf16 a[0:3], a[192:195], a[224:227], a[0:3]\n" \
"buffer_load_dwordx4 v27, s[72:75], s84 offen offset:2048 lds\n" \
"v_mfma_f32_16x16x32_bf16 a[4:7], a[128:131], a[164:167], a[4:7]\n" \
"ds_read_b64_tr_b16 a[176:177], v36 offset:32768\n" \
"s_waitcnt lgkmcnt(15)\n" \
"v_mfma_f32_16x16x32_bf16 a[4:7], a[192:195], a[228:231], a[4:7]\n" \
"s_addk_i32 s84, 0x7c00\n" \
"s_waitcnt lgkmcnt(11)\n" \
"v_mfma_f32_16x16x32_bf16 a[8:11], a[128:131], a[168:171], a[8:11]\n" \
"buffer_load_dwordx4 v27, s[72:75], s84 offen offset:3072 lds\n" \
"s_waitcnt lgkmcnt(9)\n" \
"v_mfma_f32_16x16x32_bf16 a[8:11], a[192:195], a[232:235], a[8:11]\n" \
"ds_read_b64_tr_b16 a[178:179], v36 offset:33792\n" \
"s_waitcnt lgkmcnt(8)\n" \
"v_mfma_f32_16x16x32_bf16 a[12:15], a[128:131], a[172:175], a[12:15]\n" \
"ds_read_b64_tr_b16 a[240:241], v36 offset:40960\n" \
"s_waitcnt lgkmcnt(5)\n" \
"v_mfma_f32_16x16x32_bf16 a[12:15], a[192:195], a[236:239], a[12:15]\n" \
"s_cmp_lg_u32 s3, 0\n" \
"s_cselect_b32 s36, s37, s5\n" \
"v_mfma_f32_16x16x32_bf16 a[28:31], a[132:135], a[172:175], a[28:31]\n" \
"s_lshl_b32 s5, s6, 2\n" \
"s_or_b32 s5, s38, s5\n" \
"v_mfma_f32_16x16x32_bf16 a[28:31], a[196:199], a[236:239], a[28:31]\n" \
"ds_read_b64_tr_b16 a[242:243], v36 offset:41984\n" \
"s_lshl_b32 s35, s3, 10\n" \
"v_mfma_f32_16x16x32_bf16 a[24:27], a[132:135], a[168:171], a[24:27]\n" \
"s_cmpk_lt_u32 s3, 3\n" \
"s_cselect_b32 s37, s37, s5\n" \
"v_mfma_f32_16x16x32_bf16 a[24:27], a[196:199], a[232:235], a[24:27]\n" \
"s_lshl_b32 s5, s4, 16\n" \
"s_lshl_b32 s4, s4, 11\n" \
"v_mfma_f32_16x16x32_bf16 a[20:23], a[132:135], a[164:167], a[20:23]\n" \
"v_cndmask_b32_e32 v198, v213, v212, vcc\n" \
"v_or_b32_e32 v212, s35, v20\n" \
"v_mfma_f32_16x16x32_bf16 a[20:23], a[196:199], a[228:231], a[20:23]\n" \
"ds_read_b64_tr_b16 a[180:181], v37 offset:32768\n" \
"s_or_b32 s39, s40, s35\n" \
"v_mfma_f32_16x16x32_bf16 a[16:19], a[132:135], a[160:163], a[16:19]\n" \
"s_or_b32 s55, s41, s4\n" \
"v_or_b32_e32 v199, 0x22000, v212\n" \
"v_mfma_f32_16x16x32_bf16 a[16:19], a[196:199], a[224:227], a[16:19]\n" \
"s_add_i32 s35, s39, s7\n" \
"s_add_i32 s54, s41, s5\n" \
"v_mfma_f32_16x16x32_bf16 a[32:35], a[136:139], a[160:163], a[32:35]\n" \
"s_mov_b32 m0, s32\n" \
"ds_read_b64_tr_b16 a[182:183], v37 offset:33792\n" \
"v_mfma_f32_16x16x32_bf16 a[32:35], a[200:203], a[224:227], a[32:35]\n" \
"s_mov_b32 exec_lo, 0x0\n" \
"s_mov_b32 exec_hi, 0xff\n" \
"global_load_lds_dword v[184:185], off\n" \
"s_mov_b64 exec, -1\n" \
"v_mfma_f32_16x16x32_bf16 a[36:39], a[136:139], a[164:167], a[36:39]\n" \
"s_add_i32 s55, s55, 0x80000\n" \
"v_mov_b32_e32 v215, 0xffff\n" \
"v_mfma_f32_16x16x32_bf16 a[36:39], a[200:203], a[228:231], a[36:39]\n" \
"ds_read_b64_tr_b16 a[244:245], v37 offset:40960\n" \
"s_and_b32 s4, s3, 1\n" \
"v_mfma_f32_16x16x32_bf16 a[40:43], a[136:139], a[168:171], a[40:43]\n" \
"s_lshl_b32 s5, s4, 4\n" \
"v_lshrrev_b32_e32 v212, 4, v20\n" \
"v_mfma_f32_16x16x32_bf16 a[40:43], a[200:203], a[232:235], a[40:43]\n" \
"s_mov_b32 m0, s32\n" \
"v_lshrrev_b32_e32 v218, 6, v20\n" \
"v_mfma_f32_16x16x32_bf16 a[44:47], a[136:139], a[172:175], a[44:47]\n" \
"s_mov_b32 exec_lo, 0xffff0000\n" \
"s_mov_b32 exec_hi, 0x0\n" \
"global_load_lds_dword v[186:187], off\n" \
"s_mov_b64 exec, -1\n" \
"v_mfma_f32_16x16x32_bf16 a[44:47], a[200:203], a[236:239], a[44:47]\n" \
"ds_read_b64_tr_b16 a[246:247], v37 offset:41984\n" \
"v_and_b32_e64 v219, 12, v218\n" \
"v_mfma_f32_16x16x32_bf16 a[60:63], a[140:143], a[172:175], a[60:63]\n" \
"v_and_or_b32 v214, v212, 15, s5\n" \
"v_cmp_gt_u32_e64 vcc, v219, v214\n" \
"v_mfma_f32_16x16x32_bf16 a[60:63], a[204:207], a[236:239], a[60:63]\n" \
"v_mov_b32_e32 v220, 0xffff0000\n" \
"s_lshl_b32 s5, s2, 2\n" \
"v_mfma_f32_16x16x32_bf16 a[56:59], a[140:143], a[168:171], a[56:59]\n" \
"ds_read_b64_tr_b16 a[184:185], v38 offset:32768\n" \
"v_cndmask_b32_e64 v217, v215, 0, vcc\n" \
"v_mfma_f32_16x16x32_bf16 a[56:59], a[204:207], a[232:235], a[56:59]\n" \
"v_cmp_lt_u32_e32 vcc, v219, v214\n" \
"v_mov_b32_e64 v213, s23\n" \
"v_mfma_f32_16x16x32_bf16 a[52:55], a[140:143], a[164:167], a[52:55]\n" \
"s_or_b32 s5, s5, s3\n" \
"v_cndmask_b32_e32 v221, 0, v220, vcc\n" \
"v_mfma_f32_16x16x32_bf16 a[52:55], a[204:207], a[228:231], a[52:55]\n" \
"ds_read_b64_tr_b16 a[186:187], v38 offset:33792\n" \
"v_or_b32_e64 v12, v217, v221\n" \
"v_mfma_f32_16x16x32_bf16 a[48:51], a[140:143], a[160:163], a[48:51]\n" \
"v_or_b32_e32 v217, 2, v219\n" \
"v_cmp_gt_u32_e32 vcc, v217, v214\n" \
"v_mfma_f32_16x16x32_bf16 a[48:51], a[204:207], a[224:227], a[48:51]\n" \
"v_mov_b32_e32 v216, s25\n" \
"s_movk_i32 s40, 0xf400\n" \
"v_mfma_f32_16x16x32_bf16 a[64:67], a[144:147], a[160:163], a[64:67]\n" \
"ds_read_b64_tr_b16 a[248:249], v38 offset:40960\n" \
"v_cndmask_b32_e64 v221, v215, 0, vcc\n" \
"v_mfma_f32_16x16x32_bf16 a[64:67], a[208:211], a[224:227], a[64:67]\n" \
"v_cmp_lt_u32_e32 vcc, v217, v214\n" \
"s_lshl_b32 s4, s4, 6\n" \
"v_mfma_f32_16x16x32_bf16 a[68:71], a[144:147], a[164:167], a[68:71]\n" \
"v_cndmask_b32_e32 v217, 0, v220, vcc\n" \
"v_or_b32_e32 v13, v221, v217\n" \
"v_mfma_f32_16x16x32_bf16 a[68:71], a[208:211], a[228:231], a[68:71]\n" \
"ds_read_b64_tr_b16 a[250:251], v38 offset:41984\n" \
"v_or_b32_e64 v217, 16, v219\n" \
"v_mfma_f32_16x16x32_bf16 a[72:75], a[144:147], a[168:171], a[72:75]\n" \
"v_cmp_gt_u32_e32 vcc, v217, v214\n" \
"s_nop 1\n" \
"v_cndmask_b32_e64 v221, v215, 0, vcc\n" \
"v_mfma_f32_16x16x32_bf16 a[72:75], a[208:211], a[232:235], a[72:75]\n" \
"v_cmp_lt_u32_e32 vcc, v217, v214\n" \
"s_nop 1\n" \
"v_cndmask_b32_e64 v217, 0, v220, vcc\n" \
"v_mfma_f32_16x16x32_bf16 a[76:79], a[144:147], a[172:175], a[76:79]\n" \
"v_or_b32_e32 v14, v221, v217\n" \
"v_or_b32_e32 v217, 18, v219\n" \
"v_mfma_f32_16x16x32_bf16 a[76:79], a[208:211], a[236:239], a[76:79]\n" \
"ds_read_b64_tr_b16 a[188:189], v39 offset:32768\n" \
"v_cmp_gt_u32_e64 vcc, v217, v214\n" \
"v_mfma_f32_16x16x32_bf16 a[92:95], a[148:151], a[172:175], a[92:95]\n" \
"s_nop 0\n" \
"v_cndmask_b32_e64 v215, v215, 0, vcc\n" \
"v_cmp_lt_u32_e32 vcc, v217, v214\n" \
"v_mfma_f32_16x16x32_bf16 a[92:95], a[212:215], a[236:239], a[92:95]\n" \
"s_nop 0\n" \
"v_cndmask_b32_e32 v214, 0, v220, vcc\n" \
"v_or_b32_e64 v15, v215, v214\n" \
"v_mfma_f32_16x16x32_bf16 a[88:91], a[148:151], a[168:171], a[88:91]\n" \
"ds_read_b64_tr_b16 a[190:191], v39 offset:33792\n" \
"v_mad_u64_u32 v[214:215], s[6:7], s5, 42, v[212:213]\n" \
"v_mfma_f32_16x16x32_bf16 a[88:91], a[212:215], a[232:235], a[88:91]\n" \
"s_movk_i32 s5, 0x1440\n" \
"v_cmp_gt_u32_e32 vcc, s5, v214\n" \
"v_mfma_f32_16x16x32_bf16 a[84:87], a[148:151], a[164:167], a[84:87]\n" \
"v_min_u32_e32 v217, 0xa43f, v214\n" \
"v_mov_b32_e64 v214, s24\n" \
"v_mfma_f32_16x16x32_bf16 a[84:87], a[212:215], a[228:231], a[84:87]\n" \
"ds_read_b64_tr_b16 a[252:253], v39 offset:40960\n" \
"v_cndmask_b32_e64 v215, v216, v213, vcc\n" \
"v_mfma_f32_16x16x32_bf16 a[80:83], a[148:151], a[160:163], a[80:83]\n" \
"v_mov_b32_e32 v213, s22\n" \
"s_movk_i32 s6, 0xffee\n" \
"v_mfma_f32_16x16x32_bf16 a[80:83], a[212:215], a[224:227], a[80:83]\n" \
"v_cndmask_b32_e64 v214, v214, v213, vcc\n" \
"v_add_u32_e32 v213, 0xffffebc0, v217\n" \
"v_mfma_f32_16x16x32_bf16 a[96:99], a[152:155], a[160:163], a[96:99]\n" \
"ds_read_b64_tr_b16 a[254:255], v39 offset:41984\n" \
"s_lshl_b32 s5, s2, 5\n" \
"v_mfma_f32_16x16x32_bf16 a[96:99], a[216:219], a[224:227], a[96:99]\n" \
"v_cndmask_b32_e32 v213, v213, v217, vcc\n" \
"v_lshlrev_b32_e64 v216, 7, v213\n" \
"v_mfma_f32_16x16x32_bf16 a[100:103], a[152:155], a[164:167], a[100:103]\n" \
"v_mul_u32_u24_e32 v213, 0xe39, v212\n" \
"v_mov_b32_e64 v217, 0\n" \
"v_mfma_f32_16x16x32_bf16 a[100:103], a[216:219], a[228:231], a[100:103]\n" \
"v_lshrrev_b32_e64 v213, 16, v213\n" \
"v_lshl_add_u64 v[16:17], v[214:215], 0, v[216:217]\n" \
"v_mfma_f32_16x16x32_bf16 a[104:107], a[152:155], a[168:171], a[104:107]\n" \
"v_mad_i32_i24 v214, v213, s6, v212\n" \
"s_add_i32 s5, s5, 32\n" \
"v_mfma_f32_16x16x32_bf16 a[104:107], a[216:219], a[232:235], a[104:107]\n" \
"v_lshlrev_b32_e32 v215, 4, v214\n" \
"v_add_u32_e64 v216, s5, v215\n" \
"v_mfma_f32_16x16x32_bf16 a[108:111], a[152:155], a[172:175], a[108:111]\n" \
"v_and_b32_e32 v216, 0xf0, v216\n" \
"v_cmp_gt_u32_e64 vcc, 16, v214\n" \
"v_mfma_f32_16x16x32_bf16 a[108:111], a[216:219], a[236:239], a[108:111]\n" \
"s_movk_i32 s7, 0xf800\n" \
"v_cndmask_b32_e32 v214, v215, v216, vcc\n" \
"s_waitcnt lgkmcnt(15)\n" \
"v_mfma_f32_16x16x32_bf16 a[124:127], a[156:159], a[172:175], a[124:127]\n" \
"v_or_b32_e32 v215, 64, v212\n" \
"v_mul_u32_u24_e32 v216, 0xe39, v215\n" \
"v_mfma_f32_16x16x32_bf16 a[124:127], a[220:223], a[236:239], a[124:127]\n" \
"v_lshrrev_b32_e64 v216, 16, v216\n" \
"v_mad_i32_i24 v215, v216, s6, v215\n" \
"v_mfma_f32_16x16x32_bf16 a[120:123], a[156:159], a[168:171], a[120:123]\n" \
"v_lshlrev_b32_e32 v217, 4, v215\n" \
"v_add_u32_e32 v220, s5, v217\n" \
"v_mfma_f32_16x16x32_bf16 a[120:123], a[220:223], a[232:235], a[120:123]\n" \
"v_and_b32_e32 v220, 0xf0, v220\n" \
"v_cmp_gt_u32_e64 vcc, 16, v215\n" \
"v_mfma_f32_16x16x32_bf16 a[116:119], a[156:159], a[164:167], a[116:119]\n" \
"s_nop 0\n" \
"v_cndmask_b32_e32 v215, v217, v220, vcc\n" \
"v_or_b32_e32 v217, 0x80, v212\n" \
"v_mfma_f32_16x16x32_bf16 a[116:119], a[220:223], a[228:231], a[116:119]\n" \
"v_mul_u32_u24_e32 v220, 0xe39, v217\n" \
"v_lshrrev_b32_e64 v220, 16, v220\n" \
"v_mfma_f32_16x16x32_bf16 a[112:115], a[156:159], a[160:163], a[112:115]\n" \
"v_mad_i32_i24 v217, v220, s6, v217\n" \
"v_lshlrev_b32_e64 v221, 4, v217\n" \
"v_mfma_f32_16x16x32_bf16 a[112:115], a[220:223], a[224:227], a[112:115]\n" \
"s_waitcnt vmcnt(20)\n" \
"v_readfirstlane_b32 s62, v180\n" \
"s_nop 3\n" \
"s_add_u32 s62, s62, 0x9E3779B1\n" \
"s_waitcnt vmcnt(2) lgkmcnt(0)\n" \
"s_barrier\n" \
"v_mfma_f32_16x16x32_bf16 v[52:55], a[128:131], a[176:179], v[52:55]\n" \
"s_add_u32 m0, s30, 0x8000\n" \
"s_movk_i32 s84, 0x180\n" \
"v_mfma_f32_16x16x32_bf16 v[52:55], a[192:195], a[240:243], v[52:55]\n" \
"buffer_load_dwordx4 v22, s[64:67], s84 offen offset:0 lds\n" \
"v_mfma_f32_16x16x32_bf16 v[56:59], a[128:131], a[180:183], v[56:59]\n" \
"ds_read_b64_tr_b16 a[160:161], v32 offset:0\n" \
"v_add_u32_e64 v222, s5, v221\n" \
"v_mfma_f32_16x16x32_bf16 v[56:59], a[192:195], a[244:247], v[56:59]\n" \
"s_movk_i32 s84, 0x1d80\n" \
"v_mfma_f32_16x16x32_bf16 v[60:63], a[128:131], a[184:187], v[60:63]\n" \
"buffer_load_dwordx4 v23, s[64:67], s84 offen offset:1024 lds\n" \
"v_mfma_f32_16x16x32_bf16 v[60:63], a[192:195], a[248:251], v[60:63]\n" \
"ds_read_b64_tr_b16 a[162:163], v32 offset:1024\n" \
"v_and_b32_e32 v222, 0xf0, v222\n" \
"v_mfma_f32_16x16x32_bf16 v[64:67], a[128:131], a[188:191], v[64:67]\n" \
"ds_read_b64_tr_b16 a[224:225], v32 offset:8192\n" \
"v_cmp_gt_u32_e32 vcc, 16, v217\n" \
"v_mfma_f32_16x16x32_bf16 v[64:67], a[192:195], a[252:255], v[64:67]\n" \
"ds_read_b128 a[128:131], v28 offset:16384\n" \
"v_cndmask_b32_e64 v217, v221, v222, vcc\n" \
"v_mfma_f32_16x16x32_bf16 v[80:83], a[132:135], a[188:191], v[80:83]\n" \
"ds_read_b128 a[192:195], v29 offset:16384\n" \
"v_or_b32_e32 v221, 0xc0, v212\n" \
"v_mfma_f32_16x16x32_bf16 v[80:83], a[196:199], a[252:255], v[80:83]\n" \
"s_movk_i32 s84, 0x3980\n" \
"v_mfma_f32_16x16x32_bf16 v[76:79], a[132:135], a[184:187], v[76:79]\n" \
"buffer_load_dwordx4 v22, s[64:67], s84 offen offset:2048 lds\n" \
"v_mfma_f32_16x16x32_bf16 v[76:79], a[196:199], a[248:251], v[76:79]\n" \
"s_movk_i32 s84, 0x5580\n" \
"v_mfma_f32_16x16x32_bf16 v[72:75], a[132:135], a[180:183], v[72:75]\n" \
"buffer_load_dwordx4 v23, s[64:67], s84 offen offset:3072 lds\n" \
"v_mfma_f32_16x16x32_bf16 v[72:75], a[196:199], a[244:247], v[72:75]\n" \
"s_add_u32 m0, s31, 0x8000\n" \
"s_mov_b32 s84, 0x180000\n" \
"v_mfma_f32_16x16x32_bf16 v[68:71], a[132:135], a[176:179], v[68:71]\n" \
"buffer_load_dwordx4 v26, s[68:71], s84 offen offset:0 lds\n" \
"v_mfma_f32_16x16x32_bf16 v[68:71], a[196:199], a[240:243], v[68:71]\n" \
"ds_read_b128 a[132:135], v28 offset:18432\n" \
"v_mul_u32_u24_e32 v222, 0xe39, v221\n" \
"v_mfma_f32_16x16x32_bf16 v[84:87], a[136:139], a[176:179], v[84:87]\n" \
"ds_read_b128 a[196:199], v29 offset:18432\n" \
"v_lshrrev_b32_e64 v222, 16, v222\n" \
"v_mfma_f32_16x16x32_bf16 v[84:87], a[200:203], a[240:243], v[84:87]\n" \
"s_addk_i32 s84, 0x7c00\n" \
"v_mfma_f32_16x16x32_bf16 v[88:91], a[136:139], a[180:183], v[88:91]\n" \
"buffer_load_dwordx4 v26, s[68:71], s84 offen offset:1024 lds\n" \
"v_mfma_f32_16x16x32_bf16 v[88:91], a[200:203], a[244:247], v[88:91]\n" \
"ds_read_b64_tr_b16 a[226:227], v32 offset:9216\n" \
"v_mad_i32_i24 v221, v222, s6, v221\n" \
"v_mfma_f32_16x16x32_bf16 v[92:95], a[136:139], a[184:187], v[92:95]\n" \
"s_addk_i32 s84, 0x7c00\n" \
"v_mfma_f32_16x16x32_bf16 v[92:95], a[200:203], a[248:251], v[92:95]\n" \
"buffer_load_dwordx4 v27, s[68:71], s84 offen offset:2048 lds\n" \
"v_mfma_f32_16x16x32_bf16 v[96:99], a[136:139], a[188:191], v[96:99]\n" \
"ds_read_b64_tr_b16 a[164:165], v33 offset:0\n" \
"v_lshlrev_b32_e64 v223, 4, v221\n" \
"v_mfma_f32_16x16x32_bf16 v[96:99], a[200:203], a[252:255], v[96:99]\n" \
"ds_read_b128 a[136:139], v28 offset:20480\n" \
"v_add_u32_e64 v224, s5, v223\n" \
"v_mfma_f32_16x16x32_bf16 v[112:115], a[140:143], a[188:191], v[112:115]\n" \
"ds_read_b128 a[200:203], v29 offset:20480\n" \
"v_and_b32_e32 v224, 0xf0, v224\n" \
"v_mfma_f32_16x16x32_bf16 v[112:115], a[204:207], a[252:255], v[112:115]\n" \
"s_addk_i32 s84, 0x7c00\n" \
"v_mfma_f32_16x16x32_bf16 v[108:111], a[140:143], a[184:187], v[108:111]\n" \
"buffer_load_dwordx4 v27, s[68:71], s84 offen offset:3072 lds\n" \
"v_mfma_f32_16x16x32_bf16 v[108:111], a[204:207], a[248:251], v[108:111]\n" \
"s_add_u32 m0, s30, 0x9000\n" \
"s_mov_b32 s84, 0x8180\n" \
"v_mfma_f32_16x16x32_bf16 v[104:107], a[140:143], a[180:183], v[104:107]\n" \
"buffer_load_dwordx4 v22, s[64:67], s84 offen offset:0 lds\n" \
"v_mfma_f32_16x16x32_bf16 v[104:107], a[204:207], a[244:247], v[104:107]\n" \
"ds_read_b64_tr_b16 a[166:167], v33 offset:1024\n" \
"v_cmp_gt_u32_e32 vcc, 16, v221\n" \
"v_mfma_f32_16x16x32_bf16 v[100:103], a[140:143], a[176:179], v[100:103]\n" \
"ds_read_b64_tr_b16 a[228:229], v33 offset:8192\n" \
"v_cndmask_b32_e64 v221, v223, v224, vcc\n" \
"v_mfma_f32_16x16x32_bf16 v[100:103], a[204:207], a[240:243], v[100:103]\n" \
"ds_read_b128 a[140:143], v28 offset:22528\n" \
"v_or_b32_e32 v223, 0x100, v212\n" \
"v_mfma_f32_16x16x32_bf16 v[116:119], a[144:147], a[176:179], v[116:119]\n" \
"ds_read_b128 a[204:207], v29 offset:22528\n" \
"v_mul_u32_u24_e32 v224, 0xe39, v223\n" \
"v_mfma_f32_16x16x32_bf16 v[116:119], a[208:211], a[240:243], v[116:119]\n" \
"s_addk_i32 s84, 0x1c00\n" \
"v_mfma_f32_16x16x32_bf16 v[120:123], a[144:147], a[180:183], v[120:123]\n" \
"buffer_load_dwordx4 v23, s[64:67], s84 offen offset:1024 lds\n" \
"v_mfma_f32_16x16x32_bf16 v[120:123], a[208:211], a[244:247], v[120:123]\n" \
"s_addk_i32 s84, 0x1c00\n" \
"v_mfma_f32_16x16x32_bf16 v[124:127], a[144:147], a[184:187], v[124:127]\n" \
"buffer_load_dwordx4 v22, s[64:67], s84 offen offset:2048 lds\n" \
"v_mfma_f32_16x16x32_bf16 v[124:127], a[208:211], a[248:251], v[124:127]\n" \
"s_addk_i32 s84, 0x1c00\n" \
"v_mfma_f32_16x16x32_bf16 v[128:131], a[144:147], a[188:191], v[128:131]\n" \
"buffer_load_dwordx4 v23, s[64:67], s84 offen offset:3072 lds\n" \
"v_mfma_f32_16x16x32_bf16 v[128:131], a[208:211], a[252:255], v[128:131]\n" \
"ds_read_b128 a[144:147], v28 offset:0\n" \
"v_lshrrev_b32_e32 v224, 16, v224\n" \
"v_mfma_f32_16x16x32_bf16 v[144:147], a[148:151], a[188:191], v[144:147]\n" \
"ds_read_b128 a[208:211], v29 offset:0\n" \
"v_mad_i32_i24 v223, v224, s6, v223\n" \
"v_mfma_f32_16x16x32_bf16 v[144:147], a[212:215], a[252:255], v[144:147]\n" \
"s_add_u32 m0, s31, 0xc000\n" \
"s_mov_b32 s84, 0x180000\n" \
"v_mfma_f32_16x16x32_bf16 v[140:143], a[148:151], a[184:187], v[140:143]\n" \
"buffer_load_dwordx4 v26, s[72:75], s84 offen offset:0 lds\n" \
"v_mfma_f32_16x16x32_bf16 v[140:143], a[212:215], a[248:251], v[140:143]\n" \
"ds_read_b64_tr_b16 a[230:231], v33 offset:9216\n" \
"v_lshlrev_b32_e64 v225, 4, v223\n" \
"v_mfma_f32_16x16x32_bf16 v[136:139], a[148:151], a[180:183], v[136:139]\n" \
"s_addk_i32 s84, 0x7c00\n" \
"v_mfma_f32_16x16x32_bf16 v[136:139], a[212:215], a[244:247], v[136:139]\n" \
"buffer_load_dwordx4 v26, s[72:75], s84 offen offset:1024 lds\n" \
"v_mfma_f32_16x16x32_bf16 v[132:135], a[148:151], a[176:179], v[132:135]\n" \
"ds_read_b64_tr_b16 a[168:169], v34 offset:0\n" \
"v_add_u32_e32 v226, s5, v225\n" \
"v_mfma_f32_16x16x32_bf16 v[132:135], a[212:215], a[240:243], v[132:135]\n" \
"ds_read_b128 a[148:151], v28 offset:2048\n" \
"v_and_b32_e32 v226, 0xf0, v226\n" \
"v_mfma_f32_16x16x32_bf16 v[148:151], a[152:155], a[176:179], v[148:151]\n" \
"ds_read_b128 a[212:215], v29 offset:2048\n" \
"v_cmp_gt_u32_e64 vcc, 16, v223\n" \
"v_mfma_f32_16x16x32_bf16 v[148:151], a[216:219], a[240:243], v[148:151]\n" \
"ds_read_b64_tr_b16 a[170:171], v34 offset:1024\n" \
"v_cndmask_b32_e64 v223, v225, v226, vcc\n" \
"v_mfma_f32_16x16x32_bf16 v[152:155], a[152:155], a[180:183], v[152:155]\n" \
"ds_read_b64_tr_b16 a[232:233], v34 offset:8192\n" \
"v_or_b32_e32 v225, 0x140, v212\n" \
"v_mfma_f32_16x16x32_bf16 v[152:155], a[216:219], a[244:247], v[152:155]\n" \
"ds_read_b64_tr_b16 a[234:235], v34 offset:9216\n" \
"v_mul_u32_u24_e32 v226, 0xe39, v225\n" \
"v_mfma_f32_16x16x32_bf16 v[156:159], a[152:155], a[184:187], v[156:159]\n" \
"ds_read_b64_tr_b16 a[172:173], v35 offset:0\n" \
"v_lshrrev_b32_e64 v226, 16, v226\n" \
"v_mfma_f32_16x16x32_bf16 v[156:159], a[216:219], a[248:251], v[156:159]\n" \
"ds_read_b64_tr_b16 a[174:175], v35 offset:1024\n" \
"v_mad_i32_i24 v225, v226, s6, v225\n" \
"v_mfma_f32_16x16x32_bf16 v[160:163], a[152:155], a[188:191], v[160:163]\n" \
"ds_read_b64_tr_b16 a[236:237], v35 offset:8192\n" \
"v_lshlrev_b32_e64 v227, 4, v225\n" \
"v_mfma_f32_16x16x32_bf16 v[160:163], a[216:219], a[252:255], v[160:163]\n" \
"ds_read_b128 a[152:155], v28 offset:4096\n" \
"v_add_u32_e64 v228, s5, v227\n" \
"v_mfma_f32_16x16x32_bf16 v[176:179], a[156:159], a[188:191], v[176:179]\n" \
"ds_read_b128 a[216:219], v29 offset:4096\n" \
"v_and_b32_e32 v228, 0xf0, v228\n" \
"v_mfma_f32_16x16x32_bf16 v[176:179], a[220:223], a[252:255], v[176:179]\n" \
"ds_read_b64_tr_b16 a[238:239], v35 offset:9216\n" \
"v_cmp_gt_u32_e64 vcc, 16, v225\n" \
"v_mfma_f32_16x16x32_bf16 v[172:175], a[156:159], a[184:187], v[172:175]\n" \
"s_nop 0\n" \
"v_cndmask_b32_e32 v225, v227, v228, vcc\n" \
"v_or_b32_e32 v227, 0x180, v212\n" \
"v_mfma_f32_16x16x32_bf16 v[172:175], a[220:223], a[248:251], v[172:175]\n" \
"v_mul_u32_u24_e32 v228, 0xe39, v227\n" \
"v_lshrrev_b32_e64 v228, 16, v228\n" \
"v_mfma_f32_16x16x32_bf16 v[168:171], a[156:159], a[180:183], v[168:171]\n" \
"v_mad_i32_i24 v227, v228, s6, v227\n" \
"v_lshlrev_b32_e64 v229, 4, v227\n" \
"v_mfma_f32_16x16x32_bf16 v[168:171], a[220:223], a[244:247], v[168:171]\n" \
"v_add_u32_e64 v230, s5, v229\n" \
"v_and_b32_e32 v230, 0xf0, v230\n" \
"v_mfma_f32_16x16x32_bf16 v[164:167], a[156:159], a[176:179], v[164:167]\n" \
"v_cmp_gt_u32_e32 vcc, 16, v227\n" \
"s_nop 1\n" \
"v_cndmask_b32_e64 v227, v229, v230, vcc\n" \
"v_mfma_f32_16x16x32_bf16 v[164:167], a[220:223], a[240:243], v[164:167]\n" \
"ds_read_b128 a[156:159], v28 offset:6144\n" \
"ds_read_b128 a[220:223], v29 offset:6144\n" \
"s_waitcnt lgkmcnt(15)\n" \
"v_mfma_f32_16x16x32_bf16 a[0:3], a[128:131], a[160:163], a[0:3]\n" \
"s_addk_i32 s84, 0x7c00\n" \
"v_mfma_f32_16x16x32_bf16 a[0:3], a[192:195], a[224:227], a[0:3]\n" \
"buffer_load_dwordx4 v27, s[72:75], s84 offen offset:2048 lds\n" \
"v_mfma_f32_16x16x32_bf16 a[4:7], a[128:131], a[164:167], a[4:7]\n" \
"ds_read_b64_tr_b16 a[176:177], v36 offset:0\n" \
"v_or_b32_e32 v229, 0x1c0, v212\n" \
"s_waitcnt lgkmcnt(15)\n" \
"v_mfma_f32_16x16x32_bf16 a[4:7], a[192:195], a[228:231], a[4:7]\n" \
"s_addk_i32 s84, 0x7c00\n" \
"s_waitcnt lgkmcnt(11)\n" \
"v_mfma_f32_16x16x32_bf16 a[8:11], a[128:131], a[168:171], a[8:11]\n" \
"buffer_load_dwordx4 v27, s[72:75], s84 offen offset:3072 lds\n" \
"s_waitcnt lgkmcnt(9)\n" \
"v_mfma_f32_16x16x32_bf16 a[8:11], a[192:195], a[232:235], a[8:11]\n" \
"ds_read_b64_tr_b16 a[178:179], v36 offset:1024\n" \
"v_mul_u32_u24_e32 v230, 0xe39, v229\n" \
"s_waitcnt lgkmcnt(8)\n" \
"v_mfma_f32_16x16x32_bf16 a[12:15], a[128:131], a[172:175], a[12:15]\n" \
"ds_read_b64_tr_b16 a[240:241], v36 offset:8192\n" \
"v_lshrrev_b32_e32 v230, 16, v230\n" \
"s_waitcnt lgkmcnt(5)\n" \
"v_mfma_f32_16x16x32_bf16 a[12:15], a[192:195], a[236:239], a[12:15]\n" \
"v_mad_i32_i24 v229, v230, s6, v229\n" \
"v_lshlrev_b32_e32 v231, 4, v229\n" \
"v_mfma_f32_16x16x32_bf16 a[28:31], a[132:135], a[172:175], a[28:31]\n" \
"v_add_u32_e64 v232, s5, v231\n" \
"v_and_b32_e32 v232, 0xf0, v232\n" \
"v_mfma_f32_16x16x32_bf16 a[28:31], a[196:199], a[236:239], a[28:31]\n" \
"ds_read_b64_tr_b16 a[242:243], v36 offset:9216\n" \
"v_cmp_gt_u32_e64 vcc, 16, v229\n" \
"v_mfma_f32_16x16x32_bf16 a[24:27], a[132:135], a[168:171], a[24:27]\n" \
"buffer_load_dwordx4 v[200:203], v192, s[76:79], s34 offen offset:0\n" \
"v_mfma_f32_16x16x32_bf16 a[24:27], a[196:199], a[232:235], a[24:27]\n" \
"v_cndmask_b32_e64 v229, v231, v232, vcc\n" \
"v_or_b32_e32 v231, 0x200, v212\n" \
"v_mfma_f32_16x16x32_bf16 a[20:23], a[132:135], a[164:167], a[20:23]\n" \
"v_mul_u32_u24_e32 v232, 0xe39, v231\n" \
"v_lshrrev_b32_e64 v232, 16, v232\n" \
"v_mfma_f32_16x16x32_bf16 a[20:23], a[196:199], a[228:231], a[20:23]\n" \
"ds_read_b64_tr_b16 a[180:181], v37 offset:0\n" \
"v_mad_i32_i24 v231, v232, s6, v231\n" \
"v_mfma_f32_16x16x32_bf16 a[16:19], a[132:135], a[160:163], a[16:19]\n" \
"v_lshlrev_b32_e32 v233, 4, v231\n" \
"v_add_u32_e32 v234, s5, v233\n" \
"v_mfma_f32_16x16x32_bf16 a[16:19], a[196:199], a[224:227], a[16:19]\n" \
"s_movk_i32 s5, 0x480\n" \
"v_mad_u32_u24 v0, v213, s5, v214\n" \
"v_mfma_f32_16x16x32_bf16 a[32:35], a[136:139], a[160:163], a[32:35]\n" \
"buffer_load_dwordx4 v[204:207], v193, s[76:79], s34 offen offset:0\n" \
"v_mfma_f32_16x16x32_bf16 a[32:35], a[200:203], a[224:227], a[32:35]\n" \
"s_mov_b32 m0, s32\n" \
"ds_read_b64_tr_b16 a[182:183], v37 offset:1024\n" \
"v_mfma_f32_16x16x32_bf16 a[36:39], a[136:139], a[164:167], a[36:39]\n" \
"s_mov_b32 exec_lo, 0x0\n" \
"s_mov_b32 exec_hi, 0xff00\n" \
"global_load_lds_dword v[184:185], off\n" \
"s_mov_b64 exec, -1\n" \
"v_mfma_f32_16x16x32_bf16 a[36:39], a[200:203], a[228:231], a[36:39]\n" \
"ds_read_b64_tr_b16 a[244:245], v37 offset:8192\n" \
"v_mul_u32_u24_e32 v213, 0x480, v216\n" \
"v_mfma_f32_16x16x32_bf16 a[40:43], a[136:139], a[168:171], a[40:43]\n" \
"s_movk_i32 s6, 0xfc00\n" \
"s_nop 0\n" \
"v_add3_u32 v1, v213, v215, s6\n" \
"v_mfma_f32_16x16x32_bf16 a[40:43], a[200:203], a[232:235], a[40:43]\n" \
"buffer_load_dword v208, v194, s[76:79], s34 offen offset:32\n" \
"v_mfma_f32_16x16x32_bf16 a[44:47], a[136:139], a[172:175], a[44:47]\n" \
"ds_read_b64_tr_b16 a[246:247], v37 offset:9216\n" \
"v_mul_u32_u24_e32 v213, 0x480, v220\n" \
"v_mfma_f32_16x16x32_bf16 a[44:47], a[200:203], a[236:239], a[44:47]\n" \
"v_add3_u32 v2, v213, v217, s7\n" \
"v_mul_u32_u24_e32 v213, 0x480, v222\n" \
"v_mfma_f32_16x16x32_bf16 a[60:63], a[140:143], a[172:175], a[60:63]\n" \
"v_add3_u32 v3, v213, v221, s40\n" \
"v_mul_u32_u24_e32 v213, 0x480, v226\n" \
"v_mfma_f32_16x16x32_bf16 a[60:63], a[204:207], a[236:239], a[60:63]\n" \
"v_add3_u32 v5, v213, v225, s6\n" \
"v_mul_u32_u24_e32 v213, 0x480, v228\n" \
"v_mfma_f32_16x16x32_bf16 a[56:59], a[140:143], a[168:171], a[56:59]\n" \
"buffer_load_dword v209, v194, s[76:79], s34 offen offset:1184\n" \
"v_mfma_f32_16x16x32_bf16 a[56:59], a[204:207], a[232:235], a[56:59]\n" \
"ds_read_b64_tr_b16 a[184:185], v38 offset:0\n" \
"v_add3_u32 v6, v213, v227, s7\n" \
"v_mfma_f32_16x16x32_bf16 a[52:55], a[140:143], a[164:167], a[52:55]\n" \
"v_mul_u32_u24_e32 v213, 0x480, v230\n" \
"v_add3_u32 v7, v213, v229, s40\n" \
"v_mfma_f32_16x16x32_bf16 a[52:55], a[204:207], a[228:231], a[52:55]\n" \
"ds_read_b64_tr_b16 a[186:187], v38 offset:1024\n" \
"s_lshl_b32 s7, s3, 3\n" \
"v_mfma_f32_16x16x32_bf16 a[48:51], a[140:143], a[160:163], a[48:51]\n" \
"s_lshl_b32 s40, s3, 1\n" \
"s_and_b32 s7, s7, 16\n" \
"v_mfma_f32_16x16x32_bf16 a[48:51], a[204:207], a[224:227], a[48:51]\n" \
"s_and_b32 s40, s40, 2\n" \
"s_and_b32 s6, s2, 7\n" \
"v_mfma_f32_16x16x32_bf16 a[64:67], a[144:147], a[160:163], a[64:67]\n" \
"buffer_load_dword v211, v194, s[76:79], s34 offen offset:2336\n" \
"v_mfma_f32_16x16x32_bf16 a[64:67], a[208:211], a[224:227], a[64:67]\n" \
"ds_read_b64_tr_b16 a[248:249], v38 offset:8192\n" \
"v_bfe_u32 v213, v20, 7, 1\n" \
"v_mfma_f32_16x16x32_bf16 a[68:71], a[144:147], a[164:167], a[68:71]\n" \
"s_or_b32 s7, s40, s7\n" \
"v_and_b32_e32 v234, 0xf0, v234\n" \
"v_mfma_f32_16x16x32_bf16 a[68:71], a[208:211], a[228:231], a[68:71]\n" \
"ds_read_b64_tr_b16 a[250:251], v38 offset:9216\n" \
"v_cmp_gt_u32_e64 vcc, 16, v231\n" \
"v_mfma_f32_16x16x32_bf16 a[72:75], a[144:147], a[168:171], a[72:75]\n" \
"v_or3_b32 v213, s7, v213, v219\n" \
"v_and_b32_e64 v214, 32, v20\n" \
"v_mfma_f32_16x16x32_bf16 a[72:75], a[208:211], a[232:235], a[72:75]\n" \
"s_mov_b32 s76, s20\n" \
"s_and_b32 s77, s21, 0xffff\n" \
"v_mfma_f32_16x16x32_bf16 a[76:79], a[144:147], a[172:175], a[76:79]\n" \
"s_mov_b32 s78, 0x480\n" \
"s_lshl_b32 s6, s6, 5\n" \
"v_mfma_f32_16x16x32_bf16 a[76:79], a[208:211], a[236:239], a[76:79]\n" \
"buffer_load_ushort v210, v195, s[76:79], 0 offen\n" \
"v_mfma_f32_16x16x32_bf16 a[92:95], a[148:151], a[172:175], a[92:95]\n" \
"ds_read_b64_tr_b16 a[188:189], v39 offset:0\n" \
"v_cndmask_b32_e64 v231, v233, v234, vcc\n" \
"v_mfma_f32_16x16x32_bf16 a[92:95], a[212:215], a[236:239], a[92:95]\n" \
"v_mul_u32_u24_e64 v213, 18, v213\n" \
"v_mov_b32_e32 v215, 0x100\n" \
"v_mfma_f32_16x16x32_bf16 a[88:91], a[148:151], a[168:171], a[88:91]\n" \
"s_mov_b32 s76, s8\n" \
"s_and_b32 s77, s9, 0xffff\n" \
"v_mfma_f32_16x16x32_bf16 a[88:91], a[212:215], a[232:235], a[88:91]\n" \
"s_mov_b32 s78, 0x240000\n" \
"ds_read_b64_tr_b16 a[190:191], v39 offset:1024\n" \
".fill (L_pb + 48864 - .) / 4, 4, 0xBF800000\n" \
"L_pp12:\n" \
"s_cbranch_execz L_pret\n" \
"v_mfma_f32_16x16x32_bf16 a[84:87], a[148:151], a[164:167], a[84:87]\n" \
"v_mov_b32_e32 v216, s6\n" \
"v_cmp_eq_u32_e64 vcc, 0, v214\n" \
"v_mfma_f32_16x16x32_bf16 a[84:87], a[212:215], a[228:231], a[84:87]\n" \
"ds_read_b64_tr_b16 a[252:253], v39 offset:8192\n" \
"v_and_or_b32 v213, v218, 1, v213\n" \
"v_mfma_f32_16x16x32_bf16 a[80:83], a[148:151], a[160:163], a[80:83]\n" \
"v_lshlrev_b32_e32 v212, 3, v212\n" \
"v_cndmask_b32_e32 v214, v215, v216, vcc\n" \
"v_mfma_f32_16x16x32_bf16 a[80:83], a[212:215], a[224:227], a[80:83]\n" \
"v_mad_u32_u24 v213, v213, s5, v214\n" \
"v_and_or_b32 v213, v20, 16, v213\n" \
"v_mfma_f32_16x16x32_bf16 a[96:99], a[152:155], a[160:163], a[96:99]\n" \
"ds_read_b64_tr_b16 a[254:255], v39 offset:9216\n" \
"v_add_u32_e32 v9, 0x4800, v213\n" \
"v_mfma_f32_16x16x32_bf16 a[96:99], a[216:219], a[224:227], a[96:99]\n" \
"v_mul_u32_u24_e32 v213, 0x120, v218\n" \
"v_and_or_b32 v10, v212, 24, v213\n" \
"v_mfma_f32_16x16x32_bf16 a[100:103], a[152:155], a[164:167], a[100:103]\n" \
"v_lshrrev_b32_e64 v213, 2, v20\n" \
"v_and_or_b32 v213, v213, 60, s4\n" \
"v_mfma_f32_16x16x32_bf16 a[100:103], a[216:219], a[228:231], a[100:103]\n" \
"v_lshl_or_b32 v212, s3, 9, v212\n" \
"v_mad_u32_u24 v4, v224, s5, v223\n" \
"v_mfma_f32_16x16x32_bf16 a[104:107], a[152:155], a[168:171], a[104:107]\n" \
"v_mad_u32_u24 v8, v232, s5, v231\n" \
"v_or_b32_e32 v18, 0x27000, v213\n" \
"v_mfma_f32_16x16x32_bf16 a[104:107], a[216:219], a[232:235], a[104:107]\n" \
"v_or_b32_e32 v19, 0x27800, v212\n" \
"v_mov_b32_e32 v215, 0x1000c0c\n" \
"v_mfma_f32_16x16x32_bf16 a[108:111], a[152:155], a[172:175], a[108:111]\n" \
"v_mov_b32_e32 v216, 0xc0c0100\n" \
"v_bfe_u32 v214, v20, 4, 1\n" \
"v_mfma_f32_16x16x32_bf16 a[108:111], a[216:219], a[236:239], a[108:111]\n" \
"v_lshrrev_b32_e32 v212, 8, v20\n" \
"v_cmp_eq_u32_e32 vcc, 0, v214\n" \
"s_waitcnt lgkmcnt(15)\n" \
"v_mfma_f32_16x16x32_bf16 a[124:127], a[156:159], a[172:175], a[124:127]\n" \
"v_bfe_u32 v213, v20, 4, 4\n" \
"v_mov_b32_e32 v217, 0xc0c0302\n" \
"v_mfma_f32_16x16x32_bf16 a[124:127], a[220:223], a[236:239], a[124:127]\n" \
"v_cndmask_b32_e32 v215, v215, v216, vcc\n" \
"v_mov_b32_e32 v216, 0x3020c0c\n" \
"v_mfma_f32_16x16x32_bf16 a[120:123], a[156:159], a[168:171], a[120:123]\n" \
"v_lshl_or_b32 v214, v212, 2, v214\n" \
"v_cndmask_b32_e64 v216, v216, v217, vcc\n" \
"v_mfma_f32_16x16x32_bf16 a[120:123], a[220:223], a[232:235], a[120:123]\n" \
"v_mov_b32_e32 v217, 0xc0c0c0c\n" \
"v_cmp_gt_u32_e64 vcc, v214, v213\n" \
"v_mfma_f32_16x16x32_bf16 a[116:119], a[156:159], a[164:167], a[116:119]\n" \
"v_or_b32_e32 v214, 2, v214\n" \
"s_lshr_b32 s4, s2, 5\n" \
"v_mfma_f32_16x16x32_bf16 a[116:119], a[220:223], a[228:231], a[116:119]\n" \
"v_cndmask_b32_e32 v40, v217, v215, vcc\n" \
"v_cndmask_b32_e32 v42, v215, v217, vcc\n" \
"v_mfma_f32_16x16x32_bf16 a[112:115], a[156:159], a[160:163], a[112:115]\n" \
"v_cmp_gt_u32_e32 vcc, v214, v213\n" \
"v_and_b32_e32 v214, 16, v20\n" \
"v_mfma_f32_16x16x32_bf16 a[112:115], a[220:223], a[224:227], a[112:115]\n" \
"s_waitcnt vmcnt(7) lgkmcnt(0)\n" \
"s_barrier\n" \
"v_mfma_f32_16x16x32_bf16 v[52:55], a[128:131], a[176:179], v[52:55]\n" \
"s_add_u32 m0, s30, 0x0\n" \
"s_movk_i32 s84, 0x200\n" \
"v_mfma_f32_16x16x32_bf16 v[52:55], a[192:195], a[240:243], v[52:55]\n" \
"buffer_load_dwordx4 v22, s[64:67], s84 offen offset:0 lds\n" \
"v_mfma_f32_16x16x32_bf16 v[56:59], a[128:131], a[180:183], v[56:59]\n" \
"ds_read_b64_tr_b16 a[160:161], v32 offset:32768\n" \
"s_movk_i32 s42, 0x3f80\n" \
"v_mfma_f32_16x16x32_bf16 v[56:59], a[192:195], a[244:247], v[56:59]\n" \
"s_movk_i32 s84, 0x1e00\n" \
"v_mfma_f32_16x16x32_bf16 v[60:63], a[128:131], a[184:187], v[60:63]\n" \
"buffer_load_dwordx4 v23, s[64:67], s84 offen offset:1024 lds\n" \
"v_mfma_f32_16x16x32_bf16 v[60:63], a[192:195], a[248:251], v[60:63]\n" \
"ds_read_b64_tr_b16 a[162:163], v32 offset:33792\n" \
"s_xor_b32 s5, s4, 15\n" \
"v_mfma_f32_16x16x32_bf16 v[64:67], a[128:131], a[188:191], v[64:67]\n" \
"ds_read_b64_tr_b16 a[224:225], v32 offset:40960\n" \
"v_mfma_f32_16x16x32_bf16 v[64:67], a[192:195], a[252:255], v[64:67]\n" \
"ds_read_b128 a[128:131], v28 offset:49152\n" \
"v_mfma_f32_16x16x32_bf16 v[80:83], a[132:135], a[188:191], v[80:83]\n" \
"ds_read_b128 a[192:195], v29 offset:49152\n" \
"v_mfma_f32_16x16x32_bf16 v[80:83], a[196:199], a[252:255], v[80:83]\n" \
"s_movk_i32 s84, 0x3a00\n" \
"v_mfma_f32_16x16x32_bf16 v[76:79], a[132:135], a[184:187], v[76:79]\n" \
"buffer_load_dwordx4 v22, s[64:67], s84 offen offset:2048 lds\n" \
"v_mfma_f32_16x16x32_bf16 v[76:79], a[196:199], a[248:251], v[76:79]\n" \
"s_movk_i32 s84, 0x5600\n" \
"v_mfma_f32_16x16x32_bf16 v[72:75], a[132:135], a[180:183], v[72:75]\n" \
"buffer_load_dwordx4 v23, s[64:67], s84 offen offset:3072 lds\n" \
"v_mfma_f32_16x16x32_bf16 v[72:75], a[196:199], a[244:247], v[72:75]\n" \
"s_add_u32 m0, s31, 0x0\n" \
"s_mov_b32 s84, 0x200000\n" \
"v_mfma_f32_16x16x32_bf16 v[68:71], a[132:135], a[176:179], v[68:71]\n" \
"buffer_load_dwordx4 v26, s[68:71], s84 offen offset:0 lds\n" \
"v_mfma_f32_16x16x32_bf16 v[68:71], a[196:199], a[240:243], v[68:71]\n" \
"ds_read_b128 a[132:135], v28 offset:51200\n" \
"v_mfma_f32_16x16x32_bf16 v[84:87], a[136:139], a[176:179], v[84:87]\n" \
"ds_read_b128 a[196:199], v29 offset:51200\n" \
"v_mfma_f32_16x16x32_bf16 v[84:87], a[200:203], a[240:243], v[84:87]\n" \
"s_addk_i32 s84, 0x7c00\n" \
"v_mfma_f32_16x16x32_bf16 v[88:91], a[136:139], a[180:183], v[88:91]\n" \
"buffer_load_dwordx4 v26, s[68:71], s84 offen offset:1024 lds\n" \
"v_mfma_f32_16x16x32_bf16 v[88:91], a[200:203], a[244:247], v[88:91]\n" \
"ds_read_b64_tr_b16 a[226:227], v32 offset:41984\n" \
"v_mfma_f32_16x16x32_bf16 v[92:95], a[136:139], a[184:187], v[92:95]\n" \
"s_addk_i32 s84, 0x7c00\n" \
"v_mfma_f32_16x16x32_bf16 v[92:95], a[200:203], a[248:251], v[92:95]\n" \
"buffer_load_dwordx4 v27, s[68:71], s84 offen offset:2048 lds\n" \
"v_mfma_f32_16x16x32_bf16 v[96:99], a[136:139], a[188:191], v[96:99]\n" \
"ds_read_b64_tr_b16 a[164:165], v33 offset:32768\n" \
"v_mfma_f32_16x16x32_bf16 v[96:99], a[200:203], a[252:255], v[96:99]\n" \
"ds_read_b128 a[136:139], v28 offset:53248\n" \
"v_mfma_f32_16x16x32_bf16 v[112:115], a[140:143], a[188:191], v[112:115]\n" \
"ds_read_b128 a[200:203], v29 offset:53248\n" \
"v_mfma_f32_16x16x32_bf16 v[112:115], a[204:207], a[252:255], v[112:115]\n" \
"s_addk_i32 s84, 0x7c00\n" \
"s_nop 0\n" \
"v_mfma_f32_16x16x32_bf16 v[108:111], a[140:143], a[184:187], v[108:111]\n" \
"buffer_load_dwordx4 v27, s[68:71], s84 offen offset:3072 lds\n" \
"v_mfma_f32_16x16x32_bf16 v[108:111], a[204:207], a[248:251], v[108:111]\n" \
"s_add_u32 m0, s30, 0x1000\n" \
"s_mov_b32 s84, 0x8200\n" \
"v_mfma_f32_16x16x32_bf16 v[104:107], a[140:143], a[180:183], v[104:107]\n" \
"buffer_load_dwordx4 v22, s[64:67], s84 offen offset:0 lds\n" \
"v_mfma_f32_16x16x32_bf16 v[104:107], a[204:207], a[244:247], v[104:107]\n" \
"ds_read_b64_tr_b16 a[166:167], v33 offset:33792\n" \
"v_mfma_f32_16x16x32_bf16 v[100:103], a[140:143], a[176:179], v[100:103]\n" \
"ds_read_b64_tr_b16 a[228:229], v33 offset:40960\n" \
"v_mfma_f32_16x16x32_bf16 v[100:103], a[204:207], a[240:243], v[100:103]\n" \
"ds_read_b128 a[140:143], v28 offset:55296\n" \
"v_mfma_f32_16x16x32_bf16 v[116:119], a[144:147], a[176:179], v[116:119]\n" \
"ds_read_b128 a[204:207], v29 offset:55296\n" \
"v_mfma_f32_16x16x32_bf16 v[116:119], a[208:211], a[240:243], v[116:119]\n" \
"s_addk_i32 s84, 0x1c00\n" \
"v_mfma_f32_16x16x32_bf16 v[120:123], a[144:147], a[180:183], v[120:123]\n" \
"buffer_load_dwordx4 v23, s[64:67], s84 offen offset:1024 lds\n" \
"v_mfma_f32_16x16x32_bf16 v[120:123], a[208:211], a[244:247], v[120:123]\n" \
"s_addk_i32 s84, 0x1c00\n" \
"v_mfma_f32_16x16x32_bf16 v[124:127], a[144:147], a[184:187], v[124:127]\n" \
"buffer_load_dwordx4 v22, s[64:67], s84 offen offset:2048 lds\n" \
"v_mfma_f32_16x16x32_bf16 v[124:127], a[208:211], a[248:251], v[124:127]\n" \
"s_addk_i32 s84, 0x1c00\n" \
"s_nop 0\n" \
"v_mfma_f32_16x16x32_bf16 v[128:131], a[144:147], a[188:191], v[128:131]\n" \
"buffer_load_dwordx4 v23, s[64:67], s84 offen offset:3072 lds\n" \
"v_mfma_f32_16x16x32_bf16 v[128:131], a[208:211], a[252:255], v[128:131]\n" \
"ds_read_b128 a[144:147], v28 offset:32768\n" \
"v_mfma_f32_16x16x32_bf16 v[144:147], a[148:151], a[188:191], v[144:147]\n" \
"ds_read_b128 a[208:211], v29 offset:32768\n" \
"v_mfma_f32_16x16x32_bf16 v[144:147], a[212:215], a[252:255], v[144:147]\n" \
"s_add_u32 m0, s31, 0x4000\n" \
"s_mov_b32 s84, 0x200000\n" \
"v_mfma_f32_16x16x32_bf16 v[140:143], a[148:151], a[184:187], v[140:143]\n" \
"buffer_load_dwordx4 v26, s[72:75], s84 offen offset:0 lds\n" \
"v_mfma_f32_16x16x32_bf16 v[140:143], a[212:215], a[248:251], v[140:143]\n" \
"ds_read_b64_tr_b16 a[230:231], v33 offset:41984\n" \
"v_mfma_f32_16x16x32_bf16 v[136:139], a[148:151], a[180:183], v[136:139]\n" \
"s_addk_i32 s84, 0x7c00\n" \
"s_nop 0\n" \
"v_mfma_f32_16x16x32_bf16 v[136:139], a[212:215], a[244:247], v[136:139]\n" \
"buffer_load_dwordx4 v26, s[72:75], s84 offen offset:1024 lds\n" \
"v_mfma_f32_16x16x32_bf16 v[132:135], a[148:151], a[176:179], v[132:135]\n" \
"ds_read_b64_tr_b16 a[168:169], v34 offset:32768\n" \
"v_mfma_f32_16x16x32_bf16 v[132:135], a[212:215], a[240:243], v[132:135]\n" \
"ds_read_b128 a[148:151], v28 offset:34816\n" \
"v_mfma_f32_16x16x32_bf16 v[148:151], a[152:155], a[176:179], v[148:151]\n" \
"ds_read_b128 a[212:215], v29 offset:34816\n" \
"v_mfma_f32_16x16x32_bf16 v[148:151], a[216:219], a[240:243], v[148:151]\n" \
"ds_read_b64_tr_b16 a[170:171], v34 offset:33792\n" \
"v_mfma_f32_16x16x32_bf16 v[152:155], a[152:155], a[180:183], v[152:155]\n" \
"ds_read_b64_tr_b16 a[232:233], v34 offset:40960\n" \
"v_mfma_f32_16x16x32_bf16 v[152:155], a[216:219], a[244:247], v[152:155]\n" \
"ds_read_b64_tr_b16 a[234:235], v34 offset:41984\n" \
"v_mfma_f32_16x16x32_bf16 v[156:159], a[152:155], a[184:187], v[156:159]\n" \
"ds_read_b64_tr_b16 a[172:173], v35 offset:32768\n" \
"v_mfma_f32_16x16x32_bf16 v[156:159], a[216:219], a[248:251], v[156:159]\n" \
"ds_read_b64_tr_b16 a[174:175], v35 offset:33792\n" \
"v_mfma_f32_16x16x32_bf16 v[160:163], a[152:155], a[188:191], v[160:163]\n" \
"ds_read_b64_tr_b16 a[236:237], v35 offset:40960\n" \
"v_mfma_f32_16x16x32_bf16 v[160:163], a[216:219], a[252:255], v[160:163]\n" \
"ds_read_b128 a[152:155], v28 offset:36864\n" \
"v_mfma_f32_16x16x32_bf16 v[176:179], a[156:159], a[188:191], v[176:179]\n" \
"ds_read_b128 a[216:219], v29 offset:36864\n" \
"v_mfma_f32_16x16x32_bf16 v[176:179], a[220:223], a[252:255], v[176:179]\n" \
"ds_read_b64_tr_b16 a[238:239], v35 offset:41984\n" \
"v_mfma_f32_16x16x32_bf16 v[172:175], a[156:159], a[184:187], v[172:175]\n" \
"s_cmpk_lt_u32 s3, 2\n" \
"s_cselect_b32 s6, s5, s4\n" \
"v_mfma_f32_16x16x32_bf16 v[172:175], a[220:223], a[248:251], v[172:175]\n" \
"s_lshl_b32 s7, s6, 7\n" \
"v_and_b32_e32 v213, 0x320, v20\n" \
"v_mfma_f32_16x16x32_bf16 v[168:171], a[156:159], a[180:183], v[168:171]\n" \
"s_movk_i32 s41, 0x100\n" \
"v_lshlrev_b32_e64 v214, v214, s42\n" \
"v_mfma_f32_16x16x32_bf16 v[168:171], a[220:223], a[244:247], v[168:171]\n" \
"v_mov_b32_e32 v215, 0\n" \
"s_add_i32 s40, s7, -16\n" \
"v_mfma_f32_16x16x32_bf16 v[164:167], a[156:159], a[176:179], v[164:167]\n" \
"v_cndmask_b32_e32 v41, v217, v216, vcc\n" \
"v_cndmask_b32_e32 v43, v216, v217, vcc\n" \
"v_mfma_f32_16x16x32_bf16 v[164:167], a[220:223], a[240:243], v[164:167]\n" \
"ds_read_b128 a[156:159], v28 offset:38912\n" \
"ds_read_b128 a[220:223], v29 offset:38912\n" \
"s_waitcnt lgkmcnt(15)\n" \
"v_mfma_f32_16x16x32_bf16 a[0:3], a[128:131], a[160:163], a[0:3]\n" \
"s_addk_i32 s84, 0x7c00\n" \
"v_mfma_f32_16x16x32_bf16 a[0:3], a[192:195], a[224:227], a[0:3]\n" \
"buffer_load_dwordx4 v27, s[72:75], s84 offen offset:2048 lds\n" \
"v_mfma_f32_16x16x32_bf16 a[4:7], a[128:131], a[164:167], a[4:7]\n" \
"ds_read_b64_tr_b16 a[176:177], v36 offset:32768\n" \
"v_cmp_eq_u32_e32 vcc, s41, v213\n" \
"s_waitcnt lgkmcnt(15)\n" \
"v_mfma_f32_16x16x32_bf16 a[4:7], a[192:195], a[228:231], a[4:7]\n" \
"s_addk_i32 s84, 0x7c00\n" \
"s_waitcnt lgkmcnt(11)\n" \
"v_mfma_f32_16x16x32_bf16 a[8:11], a[128:131], a[168:171], a[8:11]\n" \
"buffer_load_dwordx4 v27, s[72:75], s84 offen offset:3072 lds\n" \
"s_waitcnt lgkmcnt(9)\n" \
"v_mfma_f32_16x16x32_bf16 a[8:11], a[192:195], a[232:235], a[8:11]\n" \
"ds_read_b64_tr_b16 a[178:179], v36 offset:33792\n" \
"s_mov_b32 s41, 0\n" \
"s_waitcnt lgkmcnt(8)\n" \
"v_mfma_f32_16x16x32_bf16 a[12:15], a[128:131], a[172:175], a[12:15]\n" \
"ds_read_b64_tr_b16 a[240:241], v36 offset:40960\n" \
"s_and_b32 s43, s3, 1\n" \
"s_waitcnt lgkmcnt(5)\n" \
"v_mfma_f32_16x16x32_bf16 a[12:15], a[192:195], a[236:239], a[12:15]\n" \
"v_cndmask_b32_e32 v46, v215, v214, vcc\n" \
"v_and_b32_e32 v213, 0x330, v20\n" \
"v_mfma_f32_16x16x32_bf16 a[28:31], a[132:135], a[172:175], a[28:31]\n" \
"s_cmp_lg_u32 s7, 0\n" \
"s_cselect_b32 s40, s40, s41\n" \
"v_mfma_f32_16x16x32_bf16 a[28:31], a[196:199], a[236:239], a[28:31]\n" \
"ds_read_b64_tr_b16 a[242:243], v36 offset:41984\n" \
"s_mov_b32 s42, 0xff800000\n" \
"v_mfma_f32_16x16x32_bf16 a[24:27], a[132:135], a[168:171], a[24:27]\n" \
"s_cmp_lg_u32 s7, 0\n" \
"s_cselect_b32 s47, s41, s42\n" \
"v_mfma_f32_16x16x32_bf16 a[24:27], a[196:199], a[232:235], a[24:27]\n" \
"s_lshl_b32 s41, s43, 6\n" \
"v_lshrrev_b32_e32 v215, 6, v20\n" \
"v_mfma_f32_16x16x32_bf16 a[20:23], a[132:135], a[164:167], a[20:23]\n" \
"v_mov_b32_e32 v214, 0x3f80\n" \
"v_cmp_eq_u32_e64 vcc, 32, v213\n" \
"v_mfma_f32_16x16x32_bf16 a[20:23], a[196:199], a[228:231], a[20:23]\n" \
"ds_read_b64_tr_b16 a[180:181], v37 offset:32768\n" \
"s_or_b32 s7, s41, s7\n" \
"v_mfma_f32_16x16x32_bf16 a[16:19], a[132:135], a[160:163], a[16:19]\n" \
"v_lshrrev_b32_e32 v213, 7, v20\n" \
"v_bfe_u32 v216, v20, 6, 2\n" \
"v_mfma_f32_16x16x32_bf16 a[16:19], a[196:199], a[224:227], a[16:19]\n" \
"v_lshrrev_b32_e64 v219, 1, v20\n" \
"v_bfe_u32 v215, v215, 1, 1\n" \
"v_mfma_f32_16x16x32_bf16 a[32:35], a[136:139], a[160:163], a[32:35]\n" \
"s_waitcnt vmcnt(16)\n" \
"v_or_b32_e32 v204, v210, v204\n" \
"v_or_b32_e64 v206, v210, v206\n" \
"v_mfma_f32_16x16x32_bf16 a[32:35], a[200:203], a[224:227], a[32:35]\n" \
"v_mov_b32_e64 v210, 0\n" \
"s_add_u32 s88, s35, 0x1000\n" \
"v_mfma_f32_16x16x32_bf16 a[36:39], a[136:139], a[164:167], a[36:39]\n" \
"ds_read_b64_tr_b16 a[182:183], v37 offset:33792\n" \
"v_cndmask_b32_e64 v47, 0, v214, vcc\n" \
"v_mfma_f32_16x16x32_bf16 a[36:39], a[200:203], a[228:231], a[36:39]\n" \
"ds_read_b64_tr_b16 a[244:245], v37 offset:40960\n" \
"v_or_b32_e64 v214, s7, v213\n" \
"v_mfma_f32_16x16x32_bf16 a[40:43], a[136:139], a[168:171], a[40:43]\n" \
"s_mov_b32 m0, s32\n" \
"v_lshl_or_b32 v217, s43, 3, v213\n" \
"v_mfma_f32_16x16x32_bf16 a[40:43], a[200:203], a[232:235], a[40:43]\n" \
"s_mov_b32 exec_lo, 0xffffffff\n" \
"s_mov_b32 exec_hi, 0x0\n" \
"global_load_lds_dword v[188:189], off\n" \
"s_mov_b64 exec, -1\n" \
"v_mfma_f32_16x16x32_bf16 a[44:47], a[136:139], a[172:175], a[44:47]\n" \
"ds_read_b64_tr_b16 a[246:247], v37 offset:41984\n" \
"v_lshlrev_b32_e64 v218, 10, v212\n" \
"v_mfma_f32_16x16x32_bf16 a[44:47], a[200:203], a[236:239], a[44:47]\n" \
"v_lshlrev_b32_e32 v216, 7, v216\n" \
"v_and_b32_e32 v219, 24, v219\n" \
"v_mfma_f32_16x16x32_bf16 a[60:63], a[140:143], a[172:175], a[60:63]\n" \
"ds_write_b128 v196, v[200:203]\n" \
"v_mfma_f32_16x16x32_bf16 a[60:63], a[204:207], a[236:239], a[60:63]\n" \
"v_and_or_b32 v213, v213, 2, v215\n" \
"v_add_u32_e64 v217, s40, v217\n" \
"v_mfma_f32_16x16x32_bf16 a[56:59], a[140:143], a[168:171], a[56:59]\n" \
"ds_read_b64_tr_b16 a[184:185], v38 offset:32768\n" \
"v_or3_b32 v216, v216, v218, v219\n" \
"v_mfma_f32_16x16x32_bf16 a[56:59], a[204:207], a[232:235], a[56:59]\n" \
"v_lshlrev_b32_e64 v213, 5, v213\n" \
"s_mov_b32 s40, 0x14000\n" \
"v_mfma_f32_16x16x32_bf16 a[52:55], a[140:143], a[164:167], a[52:55]\n" \
"s_lshl_b32 s7, s2, 2\n" \
"v_or3_b32 v48, v216, v213, s40\n" \
"v_mfma_f32_16x16x32_bf16 a[52:55], a[204:207], a[228:231], a[52:55]\n" \
"ds_read_b64_tr_b16 a[186:187], v38 offset:33792\n" \
"s_lshl_b32 s40, s43, 2\n" \
"v_mfma_f32_16x16x32_bf16 a[48:51], a[140:143], a[160:163], a[48:51]\n" \
"s_lshl_b32 s6, s6, 3\n" \
"v_or_b32_e32 v213, s40, v212\n" \
"v_mfma_f32_16x16x32_bf16 a[48:51], a[204:207], a[224:227], a[48:51]\n" \
"s_or_b32 s6, s6, s40\n" \
"s_and_b32 s40, s7, 0x7c\n" \
"v_mfma_f32_16x16x32_bf16 a[64:67], a[144:147], a[160:163], a[64:67]\n" \
"ds_write_b128 v197, v[204:207]\n" \
"v_mfma_f32_16x16x32_bf16 a[64:67], a[208:211], a[224:227], a[64:67]\n" \
"ds_read_b64_tr_b16 a[248:249], v38 offset:40960\n" \
"s_or_b32 s40, s40, s3\n" \
"v_mfma_f32_16x16x32_bf16 a[68:71], a[144:147], a[164:167], a[68:71]\n" \
"s_cmpk_lt_u32 s40, 64\n" \
"s_cselect_b32 s41, s4, s5\n" \
"v_mfma_f32_16x16x32_bf16 a[68:71], a[208:211], a[228:231], a[68:71]\n" \
"ds_read_b64_tr_b16 a[250:251], v38 offset:41984\n" \
"s_lshl_b32 s40, s40, 1\n" \
"v_mfma_f32_16x16x32_bf16 a[72:75], a[144:147], a[168:171], a[72:75]\n" \
"s_lshl_b32 s41, s41, 7\n" \
"s_and_b32 s40, s40, 0x7e\n" \
"v_mfma_f32_16x16x32_bf16 a[72:75], a[208:211], a[232:235], a[72:75]\n" \
"s_or_b32 s40, s41, s40\n" \
"s_mulk_i32 s40, 0x480\n" \
"v_mfma_f32_16x16x32_bf16 a[76:79], a[144:147], a[172:175], a[76:79]\n" \
"v_add_u32_e64 v49, s40, v20\n" \
"s_mov_b32 s40, 0xc000\n" \
"v_mfma_f32_16x16x32_bf16 a[76:79], a[208:211], a[236:239], a[76:79]\n" \
"ds_read_b64_tr_b16 a[188:189], v39 offset:32768\n" \
"s_and_b32 s7, s7, 28\n" \
"v_mfma_f32_16x16x32_bf16 a[92:95], a[148:151], a[172:175], a[92:95]\n" \
"ds_write_b128 v198, v[208:211]\n" \
"v_mfma_f32_16x16x32_bf16 a[92:95], a[212:215], a[236:239], a[92:95]\n" \
"s_bfe_u32 s45, s2, 0x20003\n" \
"s_mov_b32 s41, 0x8000\n" \
"v_mfma_f32_16x16x32_bf16 a[88:91], a[148:151], a[168:171], a[88:91]\n" \
"ds_read_b64_tr_b16 a[190:191], v39 offset:33792\n" \
"v_mfma_f32_16x16x32_bf16 a[88:91], a[212:215], a[232:235], a[88:91]\n" \
"s_cmp_lg_u32 s4, 0\n" \
"s_cselect_b32 s51, s40, s41\n" \
"v_mfma_f32_16x16x32_bf16 a[84:87], a[148:151], a[164:167], a[84:87]\n" \
"s_movk_i32 s40, 0x4000\n" \
"s_lshr_b32 s44, s3, 1\n" \
"v_mfma_f32_16x16x32_bf16 a[84:87], a[212:215], a[228:231], a[84:87]\n" \
"ds_read_b64_tr_b16 a[252:253], v39 offset:40960\n" \
"s_or_b32 s46, s7, s45\n" \
"v_mfma_f32_16x16x32_bf16 a[80:83], a[148:151], a[160:163], a[80:83]\n" \
"s_cmp_lg_u32 s4, 0\n" \
"s_cselect_b32 s53, s41, s40\n" \
"v_mfma_f32_16x16x32_bf16 a[80:83], a[212:215], a[224:227], a[80:83]\n" \
"s_lshl_b32 s40, s44, 15\n" \
"s_lshl_b32 s41, s43, 14\n" \
"v_mfma_f32_16x16x32_bf16 a[96:99], a[152:155], a[160:163], a[96:99]\n" \
"ds_read_b64_tr_b16 a[254:255], v39 offset:41984\n" \
"s_lshl_b32 s48, s46, 20\n" \
"v_mfma_f32_16x16x32_bf16 a[96:99], a[216:219], a[224:227], a[96:99]\n" \
"s_or_b32 s41, s40, s41\n" \
"s_add_i32 s49, s48, 0x1000000\n" \
"v_mfma_f32_16x16x32_bf16 a[100:103], a[152:155], a[164:167], a[100:103]\n" \
"s_or_b32 s42, s49, s41\n" \
"s_lshl_b32 s4, s4, 16\n" \
"v_mfma_f32_16x16x32_bf16 a[100:103], a[216:219], a[228:231], a[100:103]\n" \
"s_or_b32 s41, s42, s4\n" \
"s_lshl_b32 s4, s5, 16\n" \
"v_mfma_f32_16x16x32_bf16 a[104:107], a[152:155], a[168:171], a[104:107]\n" \
"s_mul_i32 s5, s6, 0x24800\n" \
"s_lshl_b32 s6, s46, 9\n" \
"v_mfma_f32_16x16x32_bf16 a[104:107], a[216:219], a[232:235], a[104:107]\n" \
"s_lshl_b32 s7, s7, 6\n" \
"s_add_i32 s6, s5, s6\n" \
"v_mfma_f32_16x16x32_bf16 a[108:111], a[152:155], a[172:175], a[108:111]\n" \
"s_or_b32 s5, s5, s7\n" \
"s_lshl_b32 s7, s45, 2\n" \
"v_mfma_f32_16x16x32_bf16 a[108:111], a[216:219], a[236:239], a[108:111]\n" \
"s_or_b32 s5, s5, s7\n" \
"s_lshl_b32 s7, s3, 12\n" \
"s_waitcnt lgkmcnt(15)\n" \
"v_mfma_f32_16x16x32_bf16 a[124:127], a[156:159], a[172:175], a[124:127]\n" \
"s_or_b32 s50, s49, s7\n" \
"s_lshl_b32 s7, s3, 11\n" \
"v_mfma_f32_16x16x32_bf16 a[124:127], a[220:223], a[236:239], a[124:127]\n" \
"v_lshrrev_b32_e64 v215, 4, v20\n" \
"v_bfe_u32 v216, v20, 4, 3\n" \
"v_mfma_f32_16x16x32_bf16 a[120:123], a[156:159], a[168:171], a[120:123]\n" \
"s_or_b32 s42, s42, s4\n" \
"s_lshl_b32 s4, s43, 11\n" \
"v_mfma_f32_16x16x32_bf16 a[120:123], a[220:223], a[232:235], a[120:123]\n" \
"s_add_i32 s52, s48, s7\n" \
"s_add_i32 s53, s53, s7\n" \
"v_mfma_f32_16x16x32_bf16 a[116:119], a[156:159], a[164:167], a[116:119]\n" \
"v_mul_u32_u24_e32 v214, 0x480, v214\n" \
"v_bitop3_b32 v218, v212, v215, 7 bitop3:0x78\n" \
"v_mfma_f32_16x16x32_bf16 a[116:119], a[220:223], a[228:231], a[116:119]\n" \
"v_bitop3_b32 v212, v212, v216, 4 bitop3:0x36\n" \
"s_lshl_b32 s7, s3, 10\n" \
"v_mfma_f32_16x16x32_bf16 a[112:115], a[156:159], a[160:163], a[112:115]\n" \
"s_or_b32 s4, s40, s4\n" \
"v_lshl_or_b32 v25, v212, 4, v214\n" \
"v_mfma_f32_16x16x32_bf16 a[112:115], a[220:223], a[224:227], a[112:115]\n" \
"s_waitcnt vmcnt(1) lgkmcnt(0)\n" \
"s_barrier\n" \
"v_mfma_f32_16x16x32_bf16 v[52:55], a[128:131], a[176:179], v[52:55]\n" \
"s_add_u32 m0, s30, 0x8000\n" \
"s_movk_i32 s84, 0x280\n" \
"v_mfma_f32_16x16x32_bf16 v[52:55], a[192:195], a[240:243], v[52:55]\n" \
"buffer_load_dwordx4 v22, s[64:67], s84 offen offset:0 lds\n" \
"v_mfma_f32_16x16x32_bf16 v[56:59], a[128:131], a[180:183], v[56:59]\n" \
"s_movk_i32 s84, 0x1e80\n" \
"v_mfma_f32_16x16x32_bf16 v[56:59], a[192:195], a[244:247], v[56:59]\n" \
"buffer_load_dwordx4 v23, s[64:67], s84 offen offset:1024 lds\n" \
"v_mfma_f32_16x16x32_bf16 v[60:63], a[128:131], a[184:187], v[60:63]\n" \
"ds_read_b64_tr_b16 a[160:161], v32 offset:0\n" \
"v_mul_u32_u24_e32 v212, 0x480, v217\n" \
"v_mfma_f32_16x16x32_bf16 v[60:63], a[192:195], a[248:251], v[60:63]\n" \
"s_movk_i32 s84, 0x3a80\n" \
"v_mfma_f32_16x16x32_bf16 v[64:67], a[128:131], a[188:191], v[64:67]\n" \
"buffer_load_dwordx4 v22, s[64:67], s84 offen offset:2048 lds\n" \
"v_mfma_f32_16x16x32_bf16 v[64:67], a[192:195], a[252:255], v[64:67]\n" \
"ds_read_b128 a[128:131], v28 offset:16384\n" \
"v_bitop3_b32 v213, v215, v213, 7 bitop3:0x6c\n" \
"v_mfma_f32_16x16x32_bf16 v[80:83], a[132:135], a[188:191], v[80:83]\n" \
"ds_read_b128 a[192:195], v29 offset:16384\n" \
"s_or_b32 s40, s7, 0x14000\n" \
"v_mfma_f32_16x16x32_bf16 v[80:83], a[196:199], a[252:255], v[80:83]\n" \
"s_movk_i32 s84, 0x5680\n" \
"v_mfma_f32_16x16x32_bf16 v[76:79], a[132:135], a[184:187], v[76:79]\n" \
"buffer_load_dwordx4 v23, s[64:67], s84 offen offset:3072 lds\n" \
"v_mfma_f32_16x16x32_bf16 v[76:79], a[196:199], a[248:251], v[76:79]\n" \
"s_add_u32 m0, s31, 0x8000\n" \
"s_mov_b32 s84, 0x280000\n" \
"v_mfma_f32_16x16x32_bf16 v[72:75], a[132:135], a[180:183], v[72:75]\n" \
"buffer_load_dwordx4 v26, s[68:71], s84 offen offset:0 lds\n" \
"v_mfma_f32_16x16x32_bf16 v[72:75], a[196:199], a[244:247], v[72:75]\n" \
"ds_read_b64_tr_b16 a[162:163], v32 offset:1024\n" \
"s_lshl_b32 s43, s44, 13\n" \
"v_mfma_f32_16x16x32_bf16 v[68:71], a[132:135], a[176:179], v[68:71]\n" \
"ds_read_b64_tr_b16 a[224:225], v32 offset:8192\n" \
"s_mulk_i32 s44, 0xe800\n" \
"v_mfma_f32_16x16x32_bf16 v[68:71], a[196:199], a[240:243], v[68:71]\n" \
"ds_read_b128 a[132:135], v28 offset:18432\n" \
"s_lshl_b32 s7, s3, 8\n" \
"v_mfma_f32_16x16x32_bf16 v[84:87], a[136:139], a[176:179], v[84:87]\n" \
"ds_read_b128 a[196:199], v29 offset:18432\n" \
"v_lshl_or_b32 v24, v218, 4, v214\n" \
"v_mfma_f32_16x16x32_bf16 v[84:87], a[200:203], a[240:243], v[84:87]\n" \
"s_addk_i32 s84, 0x7c00\n" \
"v_mfma_f32_16x16x32_bf16 v[88:91], a[136:139], a[180:183], v[88:91]\n" \
"buffer_load_dwordx4 v26, s[68:71], s84 offen offset:1024 lds\n" \
"v_mfma_f32_16x16x32_bf16 v[88:91], a[200:203], a[244:247], v[88:91]\n" \
"s_addk_i32 s84, 0x7c00\n" \
"v_mfma_f32_16x16x32_bf16 v[92:95], a[136:139], a[184:187], v[92:95]\n" \
"buffer_load_dwordx4 v27, s[68:71], s84 offen offset:2048 lds\n" \
"v_mfma_f32_16x16x32_bf16 v[92:95], a[200:203], a[248:251], v[92:95]\n" \
"s_addk_i32 s84, 0x7c00\n" \
"v_mfma_f32_16x16x32_bf16 v[96:99], a[136:139], a[188:191], v[96:99]\n" \
"buffer_load_dwordx4 v27, s[68:71], s84 offen offset:3072 lds\n" \
"v_mfma_f32_16x16x32_bf16 v[96:99], a[200:203], a[252:255], v[96:99]\n" \
"ds_read_b128 a[136:139], v28 offset:20480\n" \
"v_lshl_or_b32 v21, v213, 4, v212\n" \
"v_mfma_f32_16x16x32_bf16 v[112:115], a[140:143], a[188:191], v[112:115]\n" \
"ds_read_b128 a[200:203], v29 offset:20480\n" \
"s_add_i32 s44, s44, 0x17000\n" \
"v_mfma_f32_16x16x32_bf16 v[112:115], a[204:207], a[252:255], v[112:115]\n" \
"s_add_u32 m0, s30, 0x9000\n" \
"s_mov_b32 s84, 0x8280\n" \
"v_mfma_f32_16x16x32_bf16 v[108:111], a[140:143], a[184:187], v[108:111]\n" \
"buffer_load_dwordx4 v22, s[64:67], s84 offen offset:0 lds\n" \
"v_mfma_f32_16x16x32_bf16 v[108:111], a[204:207], a[248:251], v[108:111]\n" \
"s_addk_i32 s84, 0x1c00\n" \
"v_mfma_f32_16x16x32_bf16 v[104:107], a[140:143], a[180:183], v[104:107]\n" \
"buffer_load_dwordx4 v23, s[64:67], s84 offen offset:1024 lds\n" \
"v_mfma_f32_16x16x32_bf16 v[104:107], a[204:207], a[244:247], v[104:107]\n" \
"s_addk_i32 s84, 0x1c00\n" \
"v_mfma_f32_16x16x32_bf16 v[100:103], a[140:143], a[176:179], v[100:103]\n" \
"buffer_load_dwordx4 v22, s[64:67], s84 offen offset:2048 lds\n" \
"v_mfma_f32_16x16x32_bf16 v[100:103], a[204:207], a[240:243], v[100:103]\n" \
"ds_read_b128 a[140:143], v28 offset:22528\n" \
"s_or_b32 s45, s7, 0x27000\n" \
"v_mfma_f32_16x16x32_bf16 v[116:119], a[144:147], a[176:179], v[116:119]\n" \
"ds_read_b128 a[204:207], v29 offset:22528\n" \
"s_or_b32 s46, s4, 0x10000\n" \
"v_mfma_f32_16x16x32_bf16 v[116:119], a[208:211], a[240:243], v[116:119]\n" \
"s_addk_i32 s84, 0x1c00\n" \
"v_mfma_f32_16x16x32_bf16 v[120:123], a[144:147], a[180:183], v[120:123]\n" \
"buffer_load_dwordx4 v23, s[64:67], s84 offen offset:3072 lds\n" \
"v_mfma_f32_16x16x32_bf16 v[120:123], a[208:211], a[244:247], v[120:123]\n" \
"s_add_u32 m0, s31, 0xc000\n" \
"s_mov_b32 s84, 0x280000\n" \
"v_mfma_f32_16x16x32_bf16 v[124:127], a[144:147], a[184:187], v[124:127]\n" \
"buffer_load_dwordx4 v26, s[72:75], s84 offen offset:0 lds\n" \
"v_mfma_f32_16x16x32_bf16 v[124:127], a[208:211], a[248:251], v[124:127]\n" \
"s_addk_i32 s84, 0x7c00\n" \
"v_mfma_f32_16x16x32_bf16 v[128:131], a[144:147], a[188:191], v[128:131]\n" \
"buffer_load_dwordx4 v26, s[72:75], s84 offen offset:1024 lds\n" \
"v_mfma_f32_16x16x32_bf16 v[128:131], a[208:211], a[252:255], v[128:131]\n" \
"ds_read_b128 a[144:147], v28 offset:0\n" \
"s_add_i32 s48, s6, 0x3000000\n" \
"v_mfma_f32_16x16x32_bf16 v[144:147], a[148:151], a[188:191], v[144:147]\n" \
"ds_read_b128 a[208:211], v29 offset:0\n" \
"s_add_i32 s49, s5, 0x3004000\n" \
"v_mfma_f32_16x16x32_bf16 v[144:147], a[212:215], a[252:255], v[144:147]\n" \
"ds_read_b128 v[200:203], v199 offset:0\n" \
"s_add_i32 s52, s52, 0x1010000\n" \
"v_mfma_f32_16x16x32_bf16 v[140:143], a[148:151], a[184:187], v[140:143]\n" \
"ds_read_b128 v[204:207], v199 offset:4096\n" \
"v_mfma_f32_16x16x32_bf16 v[140:143], a[212:215], a[248:251], v[140:143]\n" \
"ds_read_b64_tr_b16 a[226:227], v32 offset:9216\n" \
"v_mfma_f32_16x16x32_bf16 v[136:139], a[148:151], a[180:183], v[136:139]\n" \
"ds_read_b64_tr_b16 a[164:165], v33 offset:0\n" \
"v_mfma_f32_16x16x32_bf16 v[136:139], a[212:215], a[244:247], v[136:139]\n" \
"ds_read_b128 v[208:211], v199 offset:8192\n" \
"v_mfma_f32_16x16x32_bf16 v[132:135], a[148:151], a[176:179], v[132:135]\n" \
"ds_read_b64_tr_b16 a[166:167], v33 offset:1024\n" \
"v_mfma_f32_16x16x32_bf16 v[132:135], a[212:215], a[240:243], v[132:135]\n" \
"ds_read_b128 a[148:151], v28 offset:2048\n" \
"v_mfma_f32_16x16x32_bf16 v[148:151], a[152:155], a[176:179], v[148:151]\n" \
"ds_read_b128 a[212:215], v29 offset:2048\n" \
"v_mfma_f32_16x16x32_bf16 v[148:151], a[216:219], a[240:243], v[148:151]\n" \
"ds_read_b64_tr_b16 a[228:229], v33 offset:8192\n" \
"v_mfma_f32_16x16x32_bf16 v[152:155], a[152:155], a[180:183], v[152:155]\n" \
"ds_read_b64_tr_b16 a[230:231], v33 offset:9216\n" \
"v_mfma_f32_16x16x32_bf16 v[152:155], a[216:219], a[244:247], v[152:155]\n" \
"ds_read_b64_tr_b16 a[168:169], v34 offset:0\n" \
"v_mfma_f32_16x16x32_bf16 v[156:159], a[152:155], a[184:187], v[156:159]\n" \
"ds_read_b64_tr_b16 a[170:171], v34 offset:1024\n" \
"v_mfma_f32_16x16x32_bf16 v[156:159], a[216:219], a[248:251], v[156:159]\n" \
"ds_read_b64_tr_b16 a[232:233], v34 offset:8192\n" \
"v_mfma_f32_16x16x32_bf16 v[160:163], a[152:155], a[188:191], v[160:163]\n" \
"ds_read_b64_tr_b16 a[234:235], v34 offset:9216\n" \
"v_mfma_f32_16x16x32_bf16 v[160:163], a[216:219], a[252:255], v[160:163]\n" \
"ds_read_b128 a[152:155], v28 offset:4096\n" \
"v_mfma_f32_16x16x32_bf16 v[176:179], a[156:159], a[188:191], v[176:179]\n" \
"ds_read_b128 a[216:219], v29 offset:4096\n" \
"v_mfma_f32_16x16x32_bf16 v[176:179], a[220:223], a[252:255], v[176:179]\n" \
"ds_read_b64_tr_b16 a[172:173], v35 offset:0\n" \
"v_mfma_f32_16x16x32_bf16 v[172:175], a[156:159], a[184:187], v[172:175]\n" \
"ds_read_b64_tr_b16 a[174:175], v35 offset:1024\n" \
"v_mfma_f32_16x16x32_bf16 v[172:175], a[220:223], a[248:251], v[172:175]\n" \
"ds_read_b64_tr_b16 a[236:237], v35 offset:8192\n" \
"v_mfma_f32_16x16x32_bf16 v[168:171], a[156:159], a[180:183], v[168:171]\n" \
"ds_read_b64_tr_b16 a[238:239], v35 offset:9216\n" \
"v_mfma_f32_16x16x32_bf16 v[168:171], a[220:223], a[244:247], v[168:171]\n" \
"v_mfma_f32_16x16x32_bf16 v[164:167], a[156:159], a[176:179], v[164:167]\n" \
"v_mfma_f32_16x16x32_bf16 v[164:167], a[220:223], a[240:243], v[164:167]\n" \
"ds_read_b128 a[156:159], v28 offset:6144\n" \
"ds_read_b128 a[220:223], v29 offset:6144\n" \
"s_waitcnt lgkmcnt(15)\n" \
"v_mfma_f32_16x16x32_bf16 a[0:3], a[128:131], a[160:163], a[0:3]\n" \
"s_addk_i32 s84, 0x7c00\n" \
"v_mfma_f32_16x16x32_bf16 a[0:3], a[192:195], a[224:227], a[0:3]\n" \
"buffer_load_dwordx4 v27, s[72:75], s84 offen offset:2048 lds\n" \
"v_mfma_f32_16x16x32_bf16 a[4:7], a[128:131], a[164:167], a[4:7]\n" \
"ds_read_b64_tr_b16 a[176:177], v36 offset:0\n" \
"s_waitcnt lgkmcnt(13)\n" \
"v_mfma_f32_16x16x32_bf16 a[4:7], a[192:195], a[228:231], a[4:7]\n" \
"s_addk_i32 s84, 0x7c00\n" \
"s_waitcnt lgkmcnt(11)\n" \
"v_mfma_f32_16x16x32_bf16 a[8:11], a[128:131], a[168:171], a[8:11]\n" \
"buffer_load_dwordx4 v27, s[72:75], s84 offen offset:3072 lds\n" \
"s_waitcnt lgkmcnt(9)\n" \
"v_mfma_f32_16x16x32_bf16 a[8:11], a[192:195], a[232:235], a[8:11]\n" \
"ds_read_b64_tr_b16 a[178:179], v36 offset:1024\n" \
"s_waitcnt lgkmcnt(6)\n" \
"v_mfma_f32_16x16x32_bf16 a[12:15], a[128:131], a[172:175], a[12:15]\n" \
"ds_read_b64_tr_b16 a[240:241], v36 offset:8192\n" \
"s_waitcnt lgkmcnt(5)\n" \
"v_mfma_f32_16x16x32_bf16 a[12:15], a[192:195], a[236:239], a[12:15]\n" \
"v_mfma_f32_16x16x32_bf16 a[28:31], a[132:135], a[172:175], a[28:31]\n" \
"v_mfma_f32_16x16x32_bf16 a[28:31], a[196:199], a[236:239], a[28:31]\n" \
"ds_read_b64_tr_b16 a[242:243], v36 offset:9216\n" \
"v_mfma_f32_16x16x32_bf16 a[24:27], a[132:135], a[168:171], a[24:27]\n" \
"v_mfma_f32_16x16x32_bf16 a[24:27], a[196:199], a[232:235], a[24:27]\n" \
"v_mfma_f32_16x16x32_bf16 a[20:23], a[132:135], a[164:167], a[20:23]\n" \
"v_mfma_f32_16x16x32_bf16 a[20:23], a[196:199], a[228:231], a[20:23]\n" \
"ds_read_b64_tr_b16 a[180:181], v37 offset:0\n" \
"v_mfma_f32_16x16x32_bf16 a[16:19], a[132:135], a[160:163], a[16:19]\n" \
"v_mfma_f32_16x16x32_bf16 a[16:19], a[196:199], a[224:227], a[16:19]\n" \
"v_mfma_f32_16x16x32_bf16 a[32:35], a[136:139], a[160:163], a[32:35]\n" \
"ds_read_b64_tr_b16 a[182:183], v37 offset:1024\n" \
"v_mfma_f32_16x16x32_bf16 a[32:35], a[200:203], a[224:227], a[32:35]\n" \
"v_mfma_f32_16x16x32_bf16 a[36:39], a[136:139], a[164:167], a[36:39]\n" \
"v_mfma_f32_16x16x32_bf16 a[36:39], a[200:203], a[228:231], a[36:39]\n" \
"buffer_store_dwordx4 v[200:203], v20, s[80:83], s35 offen sc1\n" \
".fill (L_pb + 52960 - .) / 4, 4, 0xBF800000\n" \
"L_pp13:\n" \
"s_cbranch_execz L_pret\n" \
"v_mfma_f32_16x16x32_bf16 a[40:43], a[136:139], a[168:171], a[40:43]\n" \
"s_mov_b32 m0, s32\n" \
"ds_read_b64_tr_b16 a[244:245], v37 offset:8192\n" \
"v_mfma_f32_16x16x32_bf16 a[40:43], a[200:203], a[232:235], a[40:43]\n" \
"s_mov_b32 exec_lo, 0x0\n" \
"s_mov_b32 exec_hi, 0xffffffff\n" \
"global_load_lds_dword v[188:189], off\n" \
"s_mov_b64 exec, -1\n" \
"s_nop 0\n" \
"v_mfma_f32_16x16x32_bf16 a[44:47], a[136:139], a[172:175], a[44:47]\n" \
"ds_read_b64_tr_b16 a[246:247], v37 offset:9216\n" \
"v_mfma_f32_16x16x32_bf16 a[44:47], a[200:203], a[236:239], a[44:47]\n" \
"v_mfma_f32_16x16x32_bf16 a[60:63], a[140:143], a[172:175], a[60:63]\n" \
"v_mfma_f32_16x16x32_bf16 a[60:63], a[204:207], a[236:239], a[60:63]\n" \
"v_mfma_f32_16x16x32_bf16 a[56:59], a[140:143], a[168:171], a[56:59]\n" \
"ds_read_b64_tr_b16 a[184:185], v38 offset:0\n" \
"v_mfma_f32_16x16x32_bf16 a[56:59], a[204:207], a[232:235], a[56:59]\n" \
"v_mfma_f32_16x16x32_bf16 a[52:55], a[140:143], a[164:167], a[52:55]\n" \
"buffer_store_dwordx4 v[204:207], v20, s[80:83], s88 offen sc1\n" \
"v_mfma_f32_16x16x32_bf16 a[52:55], a[204:207], a[228:231], a[52:55]\n" \
"ds_read_b64_tr_b16 a[186:187], v38 offset:1024\n" \
"v_mfma_f32_16x16x32_bf16 a[48:51], a[140:143], a[160:163], a[48:51]\n" \
"v_mfma_f32_16x16x32_bf16 a[48:51], a[204:207], a[224:227], a[48:51]\n" \
"v_mfma_f32_16x16x32_bf16 a[64:67], a[144:147], a[160:163], a[64:67]\n" \
"ds_read_b64_tr_b16 a[248:249], v38 offset:8192\n" \
"v_mfma_f32_16x16x32_bf16 a[64:67], a[208:211], a[224:227], a[64:67]\n" \
"v_mfma_f32_16x16x32_bf16 a[68:71], a[144:147], a[164:167], a[68:71]\n" \
"v_mfma_f32_16x16x32_bf16 a[68:71], a[208:211], a[228:231], a[68:71]\n" \
"ds_read_b64_tr_b16 a[250:251], v38 offset:9216\n" \
"v_mfma_f32_16x16x32_bf16 a[72:75], a[144:147], a[168:171], a[72:75]\n" \
"v_mfma_f32_16x16x32_bf16 a[72:75], a[208:211], a[232:235], a[72:75]\n" \
"v_mfma_f32_16x16x32_bf16 a[76:79], a[144:147], a[172:175], a[76:79]\n" \
"buffer_store_dwordx4 v[208:211], v20, s[80:83], s36 offen sc1\n" \
"v_mfma_f32_16x16x32_bf16 a[76:79], a[208:211], a[236:239], a[76:79]\n" \
"ds_read_b64_tr_b16 a[188:189], v39 offset:0\n" \
"v_mfma_f32_16x16x32_bf16 a[92:95], a[148:151], a[172:175], a[92:95]\n" \
"v_mfma_f32_16x16x32_bf16 a[92:95], a[212:215], a[236:239], a[92:95]\n" \
"v_mfma_f32_16x16x32_bf16 a[88:91], a[148:151], a[168:171], a[88:91]\n" \
"ds_read_b64_tr_b16 a[190:191], v39 offset:1024\n" \
"v_mfma_f32_16x16x32_bf16 a[88:91], a[212:215], a[232:235], a[88:91]\n" \
"v_mfma_f32_16x16x32_bf16 a[84:87], a[148:151], a[164:167], a[84:87]\n" \
"v_mfma_f32_16x16x32_bf16 a[84:87], a[212:215], a[228:231], a[84:87]\n" \
"ds_read_b64_tr_b16 a[252:253], v39 offset:8192\n" \
"v_mfma_f32_16x16x32_bf16 a[80:83], a[148:151], a[160:163], a[80:83]\n" \
"v_mfma_f32_16x16x32_bf16 a[80:83], a[212:215], a[224:227], a[80:83]\n" \
"v_mfma_f32_16x16x32_bf16 a[96:99], a[152:155], a[160:163], a[96:99]\n" \
"ds_read_b64_tr_b16 a[254:255], v39 offset:9216\n" \
"v_mfma_f32_16x16x32_bf16 a[96:99], a[216:219], a[224:227], a[96:99]\n" \
"v_mfma_f32_16x16x32_bf16 a[100:103], a[152:155], a[164:167], a[100:103]\n" \
"v_mfma_f32_16x16x32_bf16 a[100:103], a[216:219], a[228:231], a[100:103]\n" \
"v_mfma_f32_16x16x32_bf16 a[104:107], a[152:155], a[168:171], a[104:107]\n" \
"v_mfma_f32_16x16x32_bf16 a[104:107], a[216:219], a[232:235], a[104:107]\n" \
"v_mfma_f32_16x16x32_bf16 a[108:111], a[152:155], a[172:175], a[108:111]\n" \
"v_mfma_f32_16x16x32_bf16 a[108:111], a[216:219], a[236:239], a[108:111]\n" \
"s_waitcnt lgkmcnt(15)\n" \
"v_mfma_f32_16x16x32_bf16 a[124:127], a[156:159], a[172:175], a[124:127]\n" \
"v_mfma_f32_16x16x32_bf16 a[124:127], a[220:223], a[236:239], a[124:127]\n" \
"v_mfma_f32_16x16x32_bf16 a[120:123], a[156:159], a[168:171], a[120:123]\n" \
"v_mfma_f32_16x16x32_bf16 a[120:123], a[220:223], a[232:235], a[120:123]\n" \
"v_mfma_f32_16x16x32_bf16 a[116:119], a[156:159], a[164:167], a[116:119]\n" \
"v_mfma_f32_16x16x32_bf16 a[116:119], a[220:223], a[228:231], a[116:119]\n" \
"v_mfma_f32_16x16x32_bf16 a[112:115], a[156:159], a[160:163], a[112:115]\n" \
"v_mfma_f32_16x16x32_bf16 a[112:115], a[220:223], a[224:227], a[112:115]\n" \
"s_waitcnt vmcnt(4) lgkmcnt(0)\n" \
"s_barrier\n" \
"v_mfma_f32_16x16x32_bf16 v[52:55], a[128:131], a[176:179], v[52:55]\n" \
"s_add_u32 m0, s30, 0x0\n" \
"s_movk_i32 s84, 0x300\n" \
"v_mfma_f32_16x16x32_bf16 v[52:55], a[192:195], a[240:243], v[52:55]\n" \
"buffer_load_dwordx4 v22, s[64:67], s84 offen offset:0 lds\n" \
"v_mfma_f32_16x16x32_bf16 v[56:59], a[128:131], a[180:183], v[56:59]\n" \
"ds_read_b64_tr_b16 a[160:161], v32 offset:32768\n" \
"v_mfma_f32_16x16x32_bf16 v[56:59], a[192:195], a[244:247], v[56:59]\n" \
"s_movk_i32 s84, 0x1f00\n" \
"v_mfma_f32_16x16x32_bf16 v[60:63], a[128:131], a[184:187], v[60:63]\n" \
"buffer_load_dwordx4 v23, s[64:67], s84 offen offset:1024 lds\n" \
"v_mfma_f32_16x16x32_bf16 v[60:63], a[192:195], a[248:251], v[60:63]\n" \
"ds_read_b64_tr_b16 a[162:163], v32 offset:33792\n" \
"v_mfma_f32_16x16x32_bf16 v[64:67], a[128:131], a[188:191], v[64:67]\n" \
"ds_read_b64_tr_b16 a[224:225], v32 offset:40960\n" \
"v_mfma_f32_16x16x32_bf16 v[64:67], a[192:195], a[252:255], v[64:67]\n" \
"ds_read_b128 a[128:131], v28 offset:49152\n" \
"v_mfma_f32_16x16x32_bf16 v[80:83], a[132:135], a[188:191], v[80:83]\n" \
"ds_read_b128 a[192:195], v29 offset:49152\n" \
"v_mfma_f32_16x16x32_bf16 v[80:83], a[196:199], a[252:255], v[80:83]\n" \
"s_movk_i32 s84, 0x3b00\n" \
"v_mfma_f32_16x16x32_bf16 v[76:79], a[132:135], a[184:187], v[76:79]\n" \
"buffer_load_dwordx4 v22, s[64:67], s84 offen offset:2048 lds\n" \
"v_mfma_f32_16x16x32_bf16 v[76:79], a[196:199], a[248:251], v[76:79]\n" \
"s_movk_i32 s84, 0x5700\n" \
"v_mfma_f32_16x16x32_bf16 v[72:75], a[132:135], a[180:183], v[72:75]\n" \
"buffer_load_dwordx4 v23, s[64:67], s84 offen offset:3072 lds\n" \
"v_mfma_f32_16x16x32_bf16 v[72:75], a[196:199], a[244:247], v[72:75]\n" \
"s_add_u32 m0, s31, 0x0\n" \
"s_mov_b32 s84, 0x300000\n" \
"v_mfma_f32_16x16x32_bf16 v[68:71], a[132:135], a[176:179], v[68:71]\n" \
"buffer_load_dwordx4 v26, s[68:71], s84 offen offset:0 lds\n" \
"v_mfma_f32_16x16x32_bf16 v[68:71], a[196:199], a[240:243], v[68:71]\n" \
"ds_read_b128 a[132:135], v28 offset:51200\n" \
"v_mfma_f32_16x16x32_bf16 v[84:87], a[136:139], a[176:179], v[84:87]\n" \
"ds_read_b128 a[196:199], v29 offset:51200\n" \
"v_mfma_f32_16x16x32_bf16 v[84:87], a[200:203], a[240:243], v[84:87]\n" \
"s_addk_i32 s84, 0x7c00\n" \
"v_mfma_f32_16x16x32_bf16 v[88:91], a[136:139], a[180:183], v[88:91]\n" \
"buffer_load_dwordx4 v26, s[68:71], s84 offen offset:1024 lds\n" \
"v_mfma_f32_16x16x32_bf16 v[88:91], a[200:203], a[244:247], v[88:91]\n" \
"ds_read_b64_tr_b16 a[226:227], v32 offset:41984\n" \
"v_mfma_f32_16x16x32_bf16 v[92:95], a[136:139], a[184:187], v[92:95]\n" \
"s_addk_i32 s84, 0x7c00\n" \
"v_mfma_f32_16x16x32_bf16 v[92:95], a[200:203], a[248:251], v[92:95]\n" \
"buffer_load_dwordx4 v27, s[68:71], s84 offen offset:2048 lds\n" \
"v_mfma_f32_16x16x32_bf16 v[96:99], a[136:139], a[188:191], v[96:99]\n" \
"ds_read_b64_tr_b16 a[164:165], v33 offset:32768\n" \
"v_mfma_f32_16x16x32_bf16 v[96:99], a[200:203], a[252:255], v[96:99]\n" \
"ds_read_b128 a[136:139], v28 offset:53248\n" \
"v_mfma_f32_16x16x32_bf16 v[112:115], a[140:143], a[188:191], v[112:115]\n" \
"ds_read_b128 a[200:203], v29 offset:53248\n" \
"v_mfma_f32_16x16x32_bf16 v[112:115], a[204:207], a[252:255], v[112:115]\n" \
"s_addk_i32 s84, 0x7c00\n" \
"v_mfma_f32_16x16x32_bf16 v[108:111], a[140:143], a[184:187], v[108:111]\n" \
"buffer_load_dwordx4 v27, s[68:71], s84 offen offset:3072 lds\n" \
"v_mfma_f32_16x16x32_bf16 v[108:111], a[204:207], a[248:251], v[108:111]\n" \
"s_add_u32 m0, s30, 0x1000\n" \
"s_mov_b32 s84, 0x8300\n" \
"v_mfma_f32_16x16x32_bf16 v[104:107], a[140:143], a[180:183], v[104:107]\n" \
"buffer_load_dwordx4 v22, s[64:67], s84 offen offset:0 lds\n" \
"v_mfma_f32_16x16x32_bf16 v[104:107], a[204:207], a[244:247], v[104:107]\n" \
"ds_read_b64_tr_b16 a[166:167], v33 offset:33792\n" \
"v_mfma_f32_16x16x32_bf16 v[100:103], a[140:143], a[176:179], v[100:103]\n" \
"ds_read_b64_tr_b16 a[228:229], v33 offset:40960\n" \
"v_mfma_f32_16x16x32_bf16 v[100:103], a[204:207], a[240:243], v[100:103]\n" \
"ds_read_b128 a[140:143], v28 offset:55296\n" \
"v_mfma_f32_16x16x32_bf16 v[116:119], a[144:147], a[176:179], v[116:119]\n" \
"ds_read_b128 a[204:207], v29 offset:55296\n" \
"v_mfma_f32_16x16x32_bf16 v[116:119], a[208:211], a[240:243], v[116:119]\n" \
"s_addk_i32 s84, 0x1c00\n" \
"v_mfma_f32_16x16x32_bf16 v[120:123], a[144:147], a[180:183], v[120:123]\n" \
"buffer_load_dwordx4 v23, s[64:67], s84 offen offset:1024 lds\n" \
"v_mfma_f32_16x16x32_bf16 v[120:123], a[208:211], a[244:247], v[120:123]\n" \
"s_addk_i32 s84, 0x1c00\n" \
"v_mfma_f32_16x16x32_bf16 v[124:127], a[144:147], a[184:187], v[124:127]\n" \
"buffer_load_dwordx4 v22, s[64:67], s84 offen offset:2048 lds\n" \
"v_mfma_f32_16x16x32_bf16 v[124:127], a[208:211], a[248:251], v[124:127]\n" \
"s_addk_i32 s84, 0x1c00\n" \
"s_nop 0\n" \
"v_mfma_f32_16x16x32_bf16 v[128:131], a[144:147], a[188:191], v[128:131]\n" \
"buffer_load_dwordx4 v23, s[64:67], s84 offen offset:3072 lds\n" \
"v_mfma_f32_16x16x32_bf16 v[128:131], a[208:211], a[252:255], v[128:131]\n" \
"ds_read_b128 a[144:147], v28 offset:32768\n" \
"v_mfma_f32_16x16x32_bf16 v[144:147], a[148:151], a[188:191], v[144:147]\n" \
"ds_read_b128 a[208:211], v29 offset:32768\n" \
"v_mfma_f32_16x16x32_bf16 v[144:147], a[212:215], a[252:255], v[144:147]\n" \
"s_add_u32 m0, s31, 0x4000\n" \
"s_mov_b32 s84, 0x300000\n" \
"v_mfma_f32_16x16x32_bf16 v[140:143], a[148:151], a[184:187], v[140:143]\n" \
"buffer_load_dwordx4 v26, s[72:75], s84 offen offset:0 lds\n" \
"v_mfma_f32_16x16x32_bf16 v[140:143], a[212:215], a[248:251], v[140:143]\n" \
"ds_read_b64_tr_b16 a[230:231], v33 offset:41984\n" \
"v_mfma_f32_16x16x32_bf16 v[136:139], a[148:151], a[180:183], v[136:139]\n" \
"s_addk_i32 s84, 0x7c00\n" \
"s_nop 0\n" \
"v_mfma_f32_16x16x32_bf16 v[136:139], a[212:215], a[244:247], v[136:139]\n" \
"buffer_load_dwordx4 v26, s[72:75], s84 offen offset:1024 lds\n" \
"v_mfma_f32_16x16x32_bf16 v[132:135], a[148:151], a[176:179], v[132:135]\n" \
"ds_read_b64_tr_b16 a[168:169], v34 offset:32768\n" \
"v_mfma_f32_16x16x32_bf16 v[132:135], a[212:215], a[240:243], v[132:135]\n" \
"ds_read_b128 a[148:151], v28 offset:34816\n" \
"v_mfma_f32_16x16x32_bf16 v[148:151], a[152:155], a[176:179], v[148:151]\n" \
"ds_read_b128 a[212:215], v29 offset:34816\n" \
"v_mfma_f32_16x16x32_bf16 v[148:151], a[216:219], a[240:243], v[148:151]\n" \
"ds_read_b64_tr_b16 a[170:171], v34 offset:33792\n" \
"v_mfma_f32_16x16x32_bf16 v[152:155], a[152:155], a[180:183], v[152:155]\n" \
"ds_read_b64_tr_b16 a[232:233], v34 offset:40960\n" \
"v_mfma_f32_16x16x32_bf16 v[152:155], a[216:219], a[244:247], v[152:155]\n" \
"ds_read_b64_tr_b16 a[234:235], v34 offset:41984\n" \
"v_mfma_f32_16x16x32_bf16 v[156:159], a[152:155], a[184:187], v[156:159]\n" \
"ds_read_b64_tr_b16 a[172:173], v35 offset:32768\n" \
"v_mfma_f32_16x16x32_bf16 v[156:159], a[216:219], a[248:251], v[156:159]\n" \
"ds_read_b64_tr_b16 a[174:175], v35 offset:33792\n" \
"v_mfma_f32_16x16x32_bf16 v[160:163], a[152:155], a[188:191], v[160:163]\n" \
"ds_read_b64_tr_b16 a[236:237], v35 offset:40960\n" \
"v_mfma_f32_16x16x32_bf16 v[160:163], a[216:219], a[252:255], v[160:163]\n" \
"ds_read_b128 a[152:155], v28 offset:36864\n" \
"v_mfma_f32_16x16x32_bf16 v[176:179], a[156:159], a[188:191], v[176:179]\n" \
"ds_read_b128 a[216:219], v29 offset:36864\n" \
"v_mfma_f32_16x16x32_bf16 v[176:179], a[220:223], a[252:255], v[176:179]\n" \
"ds_read_b64_tr_b16 a[238:239], v35 offset:41984\n" \
"v_mfma_f32_16x16x32_bf16 v[172:175], a[156:159], a[184:187], v[172:175]\n" \
"v_mfma_f32_16x16x32_bf16 v[172:175], a[220:223], a[248:251], v[172:175]\n" \
"v_mfma_f32_16x16x32_bf16 v[168:171], a[156:159], a[180:183], v[168:171]\n" \
"v_mfma_f32_16x16x32_bf16 v[168:171], a[220:223], a[244:247], v[168:171]\n" \
"v_mfma_f32_16x16x32_bf16 v[164:167], a[156:159], a[176:179], v[164:167]\n" \
"v_mfma_f32_16x16x32_bf16 v[164:167], a[220:223], a[240:243], v[164:167]\n" \
"ds_read_b128 a[156:159], v28 offset:38912\n" \
"ds_read_b128 a[220:223], v29 offset:38912\n" \
"s_waitcnt lgkmcnt(15)\n" \
"v_mfma_f32_16x16x32_bf16 a[0:3], a[128:131], a[160:163], a[0:3]\n" \
"s_addk_i32 s84, 0x7c00\n" \
"v_mfma_f32_16x16x32_bf16 a[0:3], a[192:195], a[224:227], a[0:3]\n" \
"buffer_load_dwordx4 v27, s[72:75], s84 offen offset:2048 lds\n" \
"v_mfma_f32_16x16x32_bf16 a[4:7], a[128:131], a[164:167], a[4:7]\n" \
"ds_read_b64_tr_b16 a[176:177], v36 offset:32768\n" \
"s_waitcnt lgkmcnt(15)\n" \
"v_mfma_f32_16x16x32_bf16 a[4:7], a[192:195], a[228:231], a[4:7]\n" \
"s_addk_i32 s84, 0x7c00\n" \
"s_waitcnt lgkmcnt(11)\n" \
"v_mfma_f32_16x16x32_bf16 a[8:11], a[128:131], a[168:171], a[8:11]\n" \
"buffer_load_dwordx4 v27, s[72:75], s84 offen offset:3072 lds\n" \
"s_waitcnt lgkmcnt(9)\n" \
"v_mfma_f32_16x16x32_bf16 a[8:11], a[192:195], a[232:235], a[8:11]\n" \
"ds_read_b64_tr_b16 a[178:179], v36 offset:33792\n" \
"s_waitcnt lgkmcnt(8)\n" \
"v_mfma_f32_16x16x32_bf16 a[12:15], a[128:131], a[172:175], a[12:15]\n" \
"ds_read_b64_tr_b16 a[240:241], v36 offset:40960\n" \
"s_waitcnt lgkmcnt(5)\n" \
"v_mfma_f32_16x16x32_bf16 a[12:15], a[192:195], a[236:239], a[12:15]\n" \
"v_mfma_f32_16x16x32_bf16 a[28:31], a[132:135], a[172:175], a[28:31]\n" \
"v_mfma_f32_16x16x32_bf16 a[28:31], a[196:199], a[236:239], a[28:31]\n" \
"ds_read_b64_tr_b16 a[242:243], v36 offset:41984\n" \
"v_mfma_f32_16x16x32_bf16 a[24:27], a[132:135], a[168:171], a[24:27]\n" \
"v_mfma_f32_16x16x32_bf16 a[24:27], a[196:199], a[232:235], a[24:27]\n" \
"v_mfma_f32_16x16x32_bf16 a[20:23], a[132:135], a[164:167], a[20:23]\n" \
"v_mfma_f32_16x16x32_bf16 a[20:23], a[196:199], a[228:231], a[20:23]\n" \
"ds_read_b64_tr_b16 a[180:181], v37 offset:32768\n" \
"v_mfma_f32_16x16x32_bf16 a[16:19], a[132:135], a[160:163], a[16:19]\n" \
"v_mfma_f32_16x16x32_bf16 a[16:19], a[196:199], a[224:227], a[16:19]\n" \
"v_mfma_f32_16x16x32_bf16 a[32:35], a[136:139], a[160:163], a[32:35]\n" \
"ds_read_b64_tr_b16 a[182:183], v37 offset:33792\n" \
"v_mfma_f32_16x16x32_bf16 a[32:35], a[200:203], a[224:227], a[32:35]\n" \
"v_mfma_f32_16x16x32_bf16 a[36:39], a[136:139], a[164:167], a[36:39]\n" \
"v_lshlrev_b32_e32 v45, 3, v20\n" \
"v_add_u32_e32 v45, s31, v45\n" \
"v_mfma_f32_16x16x32_bf16 a[36:39], a[200:203], a[228:231], a[36:39]\n" \
"s_getpc_b64 s[86:87]\n" \
"s_sub_u32 s86, s86, 0x10800\n" \
"s_subb_u32 s87, s87, 0\n" \
"s_mov_b32 m0, s32\n" \
"s_nop 0\n" \
"v_mfma_f32_16x16x32_bf16 a[40:43], a[136:139], a[168:171], a[40:43]\n" \
"global_load_lds_dword v45, s[86:87]\n" \
"v_mfma_f32_16x16x32_bf16 a[40:43], a[200:203], a[232:235], a[40:43]\n" \
"ds_read_b64_tr_b16 a[244:245], v37 offset:40960\n" \
"v_mfma_f32_16x16x32_bf16 a[44:47], a[136:139], a[172:175], a[44:47]\n" \
"ds_read_b64_tr_b16 a[246:247], v37 offset:41984\n" \
"v_mfma_f32_16x16x32_bf16 a[44:47], a[200:203], a[236:239], a[44:47]\n" \
"v_mfma_f32_16x16x32_bf16 a[60:63], a[140:143], a[172:175], a[60:63]\n" \
"v_mfma_f32_16x16x32_bf16 a[60:63], a[204:207], a[236:239], a[60:63]\n" \
"v_mfma_f32_16x16x32_bf16 a[56:59], a[140:143], a[168:171], a[56:59]\n" \
"ds_read_b64_tr_b16 a[184:185], v38 offset:32768\n" \
"v_mfma_f32_16x16x32_bf16 a[56:59], a[204:207], a[232:235], a[56:59]\n" \
"v_mfma_f32_16x16x32_bf16 a[52:55], a[140:143], a[164:167], a[52:55]\n" \
"v_mfma_f32_16x16x32_bf16 a[52:55], a[204:207], a[228:231], a[52:55]\n" \
"ds_read_b64_tr_b16 a[186:187], v38 offset:33792\n" \
"v_mfma_f32_16x16x32_bf16 a[48:51], a[140:143], a[160:163], a[48:51]\n" \
"v_mfma_f32_16x16x32_bf16 a[48:51], a[204:207], a[224:227], a[48:51]\n" \
"v_mfma_f32_16x16x32_bf16 a[64:67], a[144:147], a[160:163], a[64:67]\n" \
"ds_read_b64_tr_b16 a[248:249], v38 offset:40960\n" \
"v_mfma_f32_16x16x32_bf16 a[64:67], a[208:211], a[224:227], a[64:67]\n" \
"v_mfma_f32_16x16x32_bf16 a[68:71], a[144:147], a[164:167], a[68:71]\n" \
"v_mfma_f32_16x16x32_bf16 a[68:71], a[208:211], a[228:231], a[68:71]\n" \
"ds_read_b64_tr_b16 a[250:251], v38 offset:41984\n" \
"v_mfma_f32_16x16x32_bf16 a[72:75], a[144:147], a[168:171], a[72:75]\n" \
"v_mfma_f32_16x16x32_bf16 a[72:75], a[208:211], a[232:235], a[72:75]\n" \
"v_mfma_f32_16x16x32_bf16 a[76:79], a[144:147], a[172:175], a[76:79]\n" \
"v_mfma_f32_16x16x32_bf16 a[76:79], a[208:211], a[236:239], a[76:79]\n" \
"ds_read_b64_tr_b16 a[188:189], v39 offset:32768\n" \
"v_mfma_f32_16x16x32_bf16 a[92:95], a[148:151], a[172:175], a[92:95]\n" \
"v_mfma_f32_16x16x32_bf16 a[92:95], a[212:215], a[236:239], a[92:95]\n" \
"v_mfma_f32_16x16x32_bf16 a[88:91], a[148:151], a[168:171], a[88:91]\n" \
"ds_read_b64_tr_b16 a[190:191], v39 offset:33792\n" \
"v_mfma_f32_16x16x32_bf16 a[88:91], a[212:215], a[232:235], a[88:91]\n" \
"v_mfma_f32_16x16x32_bf16 a[84:87], a[148:151], a[164:167], a[84:87]\n" \
"v_mfma_f32_16x16x32_bf16 a[84:87], a[212:215], a[228:231], a[84:87]\n" \
"ds_read_b64_tr_b16 a[252:253], v39 offset:40960\n" \
"v_mfma_f32_16x16x32_bf16 a[80:83], a[148:151], a[160:163], a[80:83]\n" \
"v_mfma_f32_16x16x32_bf16 a[80:83], a[212:215], a[224:227], a[80:83]\n" \
"v_mfma_f32_16x16x32_bf16 a[96:99], a[152:155], a[160:163], a[96:99]\n" \
"ds_read_b64_tr_b16 a[254:255], v39 offset:41984\n" \
"v_mfma_f32_16x16x32_bf16 a[96:99], a[216:219], a[224:227], a[96:99]\n" \
"v_mfma_f32_16x16x32_bf16 a[100:103], a[152:155], a[164:167], a[100:103]\n" \
"v_mfma_f32_16x16x32_bf16 a[100:103], a[216:219], a[228:231], a[100:103]\n" \
"v_mfma_f32_16x16x32_bf16 a[104:107], a[152:155], a[168:171], a[104:107]\n" \
"v_mfma_f32_16x16x32_bf16 a[104:107], a[216:219], a[232:235], a[104:107]\n" \
"v_mfma_f32_16x16x32_bf16 a[108:111], a[152:155], a[172:175], a[108:111]\n" \
"v_mfma_f32_16x16x32_bf16 a[108:111], a[216:219], a[236:239], a[108:111]\n" \
"s_waitcnt lgkmcnt(15)\n" \
"v_mfma_f32_16x16x32_bf16 a[124:127], a[156:159], a[172:175], a[124:127]\n" \
"v_mfma_f32_16x16x32_bf16 a[124:127], a[220:223], a[236:239], a[124:127]\n" \
"v_mfma_f32_16x16x32_bf16 a[120:123], a[156:159], a[168:171], a[120:123]\n" \
"v_mfma_f32_16x16x32_bf16 a[120:123], a[220:223], a[232:235], a[120:123]\n" \
"v_mfma_f32_16x16x32_bf16 a[116:119], a[156:159], a[164:167], a[116:119]\n" \
"v_mfma_f32_16x16x32_bf16 a[116:119], a[220:223], a[228:231], a[116:119]\n" \
"v_mfma_f32_16x16x32_bf16 a[112:115], a[156:159], a[160:163], a[112:115]\n" \
"v_mfma_f32_16x16x32_bf16 a[112:115], a[220:223], a[224:227], a[112:115]\n" \
"s_waitcnt vmcnt(1) lgkmcnt(0)\n" \
"s_barrier\n" \
"v_mov_b32_e32 v45, s62\n" \
"s_mov_b64 exec, 1\n" \
"buffer_store_dword v45, off, s[80:83], s37 offset:128 sc1\n" \
"s_mov_b64 exec, -1\n" \
"v_mfma_f32_16x16x32_bf16 v[52:55], a[128:131], a[176:179], v[52:55]\n" \
"s_add_u32 m0, s30, 0x8000\n" \
"s_movk_i32 s84, 0x380\n" \
"v_mfma_f32_16x16x32_bf16 v[52:55], a[192:195], a[240:243], v[52:55]\n" \
"buffer_load_dwordx4 v22, s[64:67], s84 offen offset:0 lds\n" \
"v_mfma_f32_16x16x32_bf16 v[56:59], a[128:131], a[180:183], v[56:59]\n" \
"ds_read_b64_tr_b16 a[160:161], v32 offset:0\n" \
"v_mfma_f32_16x16x32_bf16 v[56:59], a[192:195], a[244:247], v[56:59]\n" \
"s_movk_i32 s84, 0x1f80\n" \
"v_mfma_f32_16x16x32_bf16 v[60:63], a[128:131], a[184:187], v[60:63]\n" \
"buffer_load_dwordx4 v23, s[64:67], s84 offen offset:1024 lds\n" \
"v_mfma_f32_16x16x32_bf16 v[60:63], a[192:195], a[248:251], v[60:63]\n" \
"ds_read_b64_tr_b16 a[162:163], v32 offset:1024\n" \
"v_mfma_f32_16x16x32_bf16 v[64:67], a[128:131], a[188:191], v[64:67]\n" \
"ds_read_b64_tr_b16 a[224:225], v32 offset:8192\n" \
"v_mfma_f32_16x16x32_bf16 v[64:67], a[192:195], a[252:255], v[64:67]\n" \
"ds_read_b128 a[128:131], v28 offset:16384\n" \
"v_mfma_f32_16x16x32_bf16 v[80:83], a[132:135], a[188:191], v[80:83]\n" \
"ds_read_b128 a[192:195], v29 offset:16384\n" \
"v_mfma_f32_16x16x32_bf16 v[80:83], a[196:199], a[252:255], v[80:83]\n" \
"s_movk_i32 s84, 0x3b80\n" \
"v_mfma_f32_16x16x32_bf16 v[76:79], a[132:135], a[184:187], v[76:79]\n" \
"buffer_load_dwordx4 v22, s[64:67], s84 offen offset:2048 lds\n" \
"v_mfma_f32_16x16x32_bf16 v[76:79], a[196:199], a[248:251], v[76:79]\n" \
"s_movk_i32 s84, 0x5780\n" \
"v_mfma_f32_16x16x32_bf16 v[72:75], a[132:135], a[180:183], v[72:75]\n" \
"buffer_load_dwordx4 v23, s[64:67], s84 offen offset:3072 lds\n" \
"v_mfma_f32_16x16x32_bf16 v[72:75], a[196:199], a[244:247], v[72:75]\n" \
"s_add_u32 m0, s31, 0x8000\n" \
"s_mov_b32 s84, 0x380000\n" \
"v_mfma_f32_16x16x32_bf16 v[68:71], a[132:135], a[176:179], v[68:71]\n" \
"buffer_load_dwordx4 v26, s[68:71], s84 offen offset:0 lds\n" \
"v_mfma_f32_16x16x32_bf16 v[68:71], a[196:199], a[240:243], v[68:71]\n" \
"ds_read_b128 a[132:135], v28 offset:18432\n" \
"v_mfma_f32_16x16x32_bf16 v[84:87], a[136:139], a[176:179], v[84:87]\n" \
"ds_read_b128 a[196:199], v29 offset:18432\n" \
"v_mfma_f32_16x16x32_bf16 v[84:87], a[200:203], a[240:243], v[84:87]\n" \
"s_addk_i32 s84, 0x7c00\n" \
"v_mfma_f32_16x16x32_bf16 v[88:91], a[136:139], a[180:183], v[88:91]\n" \
"buffer_load_dwordx4 v26, s[68:71], s84 offen offset:1024 lds\n" \
"v_mfma_f32_16x16x32_bf16 v[88:91], a[200:203], a[244:247], v[88:91]\n" \
"ds_read_b64_tr_b16 a[226:227], v32 offset:9216\n" \
"v_mfma_f32_16x16x32_bf16 v[92:95], a[136:139], a[184:187], v[92:95]\n" \
"s_addk_i32 s84, 0x7c00\n" \
"v_mfma_f32_16x16x32_bf16 v[92:95], a[200:203], a[248:251], v[92:95]\n" \
"buffer_load_dwordx4 v27, s[68:71], s84 offen offset:2048 lds\n" \
"v_mfma_f32_16x16x32_bf16 v[96:99], a[136:139], a[188:191], v[96:99]\n" \
"ds_read_b64_tr_b16 a[164:165], v33 offset:0\n" \
"v_mfma_f32_16x16x32_bf16 v[96:99], a[200:203], a[252:255], v[96:99]\n" \
"ds_read_b128 a[136:139], v28 offset:20480\n" \
"v_mfma_f32_16x16x32_bf16 v[112:115], a[140:143], a[188:191], v[112:115]\n" \
"ds_read_b128 a[200:203], v29 offset:20480\n" \
"v_mfma_f32_16x16x32_bf16 v[112:115], a[204:207], a[252:255], v[112:115]\n" \
"s_addk_i32 s84, 0x7c00\n" \
"s_nop 0\n" \
"v_mfma_f32_16x16x32_bf16 v[108:111], a[140:143], a[184:187], v[108:111]\n" \
"buffer_load_dwordx4 v27, s[68:71], s84 offen offset:3072 lds\n" \
"v_mfma_f32_16x16x32_bf16 v[108:111], a[204:207], a[248:251], v[108:111]\n" \
"s_add_u32 m0, s30, 0x9000\n" \
"s_mov_b32 s84, 0x8380\n" \
"v_mfma_f32_16x16x32_bf16 v[104:107], a[140:143], a[180:183], v[104:107]\n" \
"buffer_load_dwordx4 v22, s[64:67], s84 offen offset:0 lds\n" \
"v_mfma_f32_16x16x32_bf16 v[104:107], a[204:207], a[244:247], v[104:107]\n" \
"ds_read_b64_tr_b16 a[166:167], v33 offset:1024\n" \
"v_mfma_f32_16x16x32_bf16 v[100:103], a[140:143], a[176:179], v[100:103]\n" \
"ds_read_b64_tr_b16 a[228:229], v33 offset:8192\n" \
"v_mfma_f32_16x16x32_bf16 v[100:103], a[204:207], a[240:243], v[100:103]\n" \
"ds_read_b128 a[140:143], v28 offset:22528\n" \
"v_mfma_f32_16x16x32_bf16 v[116:119], a[144:147], a[176:179], v[116:119]\n" \
"ds_read_b128 a[204:207], v29 offset:22528\n" \
"v_mfma_f32_16x16x32_bf16 v[116:119], a[208:211], a[240:243], v[116:119]\n" \
"s_addk_i32 s84, 0x1c00\n" \
"v_mfma_f32_16x16x32_bf16 v[120:123], a[144:147], a[180:183], v[120:123]\n" \
"buffer_load_dwordx4 v23, s[64:67], s84 offen offset:1024 lds\n" \
"v_mfma_f32_16x16x32_bf16 v[120:123], a[208:211], a[244:247], v[120:123]\n" \
"s_addk_i32 s84, 0x1c00\n" \
"v_mfma_f32_16x16x32_bf16 v[124:127], a[144:147], a[184:187], v[124:127]\n" \
"buffer_load_dwordx4 v22, s[64:67], s84 offen offset:2048 lds\n" \
"v_mfma_f32_16x16x32_bf16 v[124:127], a[208:211], a[248:251], v[124:127]\n" \
"s_addk_i32 s84, 0x1c00\n" \
"s_nop 0\n" \
"v_mfma_f32_16x16x32_bf16 v[128:131], a[144:147], a[188:191], v[128:131]\n" \
"buffer_load_dwordx4 v23, s[64:67], s84 offen offset:3072 lds\n" \
"v_mfma_f32_16x16x32_bf16 v[128:131], a[208:211], a[252:255], v[128:131]\n" \
"ds_read_b128 a[144:147], v28 offset:0\n" \
"v_mfma_f32_16x16x32_bf16 v[144:147], a[148:151], a[188:191], v[144:147]\n" \
"ds_read_b128 a[208:211], v29 offset:0\n" \
"v_mfma_f32_16x16x32_bf16 v[144:147], a[212:215], a[252:255], v[144:147]\n" \
"s_add_u32 m0, s31, 0xc000\n" \
"s_mov_b32 s84, 0x380000\n" \
"v_mfma_f32_16x16x32_bf16 v[140:143], a[148:151], a[184:187], v[140:143]\n" \
"buffer_load_dwordx4 v26, s[72:75], s84 offen offset:0 lds\n" \
"v_mfma_f32_16x16x32_bf16 v[140:143], a[212:215], a[248:251], v[140:143]\n" \
"ds_read_b64_tr_b16 a[230:231], v33 offset:9216\n" \
"v_mfma_f32_16x16x32_bf16 v[136:139], a[148:151], a[180:183], v[136:139]\n" \
"s_addk_i32 s84, 0x7c00\n" \
"s_nop 0\n" \
"v_mfma_f32_16x16x32_bf16 v[136:139], a[212:215], a[244:247], v[136:139]\n" \
"buffer_load_dwordx4 v26, s[72:75], s84 offen offset:1024 lds\n" \
"v_mfma_f32_16x16x32_bf16 v[132:135], a[148:151], a[176:179], v[132:135]\n" \
"ds_read_b64_tr_b16 a[168:169], v34 offset:0\n" \
"v_mfma_f32_16x16x32_bf16 v[132:135], a[212:215], a[240:243], v[132:135]\n" \
"ds_read_b128 a[148:151], v28 offset:2048\n" \
"v_mfma_f32_16x16x32_bf16 v[148:151], a[152:155], a[176:179], v[148:151]\n" \
"ds_read_b128 a[212:215], v29 offset:2048\n" \
"v_mfma_f32_16x16x32_bf16 v[148:151], a[216:219], a[240:243], v[148:151]\n" \
"ds_read_b64_tr_b16 a[170:171], v34 offset:1024\n" \
"v_mfma_f32_16x16x32_bf16 v[152:155], a[152:155], a[180:183], v[152:155]\n" \
"ds_read_b64_tr_b16 a[232:233], v34 offset:8192\n" \
"v_mfma_f32_16x16x32_bf16 v[152:155], a[216:219], a[244:247], v[152:155]\n" \
"ds_read_b64_tr_b16 a[234:235], v34 offset:9216\n" \
"v_mfma_f32_16x16x32_bf16 v[156:159], a[152:155], a[184:187], v[156:159]\n" \
"ds_read_b64_tr_b16 a[172:173], v35 offset:0\n" \
"v_mfma_f32_16x16x32_bf16 v[156:159], a[216:219], a[248:251], v[156:159]\n" \
"ds_read_b64_tr_b16 a[174:175], v35 offset:1024\n" \
"v_mfma_f32_16x16x32_bf16 v[160:163], a[152:155], a[188:191], v[160:163]\n" \
"ds_read_b64_tr_b16 a[236:237], v35 offset:8192\n" \
"v_mfma_f32_16x16x32_bf16 v[160:163], a[216:219], a[252:255], v[160:163]\n" \
"ds_read_b128 a[152:155], v28 offset:4096\n" \
"v_mfma_f32_16x16x32_bf16 v[176:179], a[156:159], a[188:191], v[176:179]\n" \
"ds_read_b128 a[216:219], v29 offset:4096\n" \
"v_mfma_f32_16x16x32_bf16 v[176:179], a[220:223], a[252:255], v[176:179]\n" \
"ds_read_b64_tr_b16 a[238:239], v35 offset:9216\n" \
"v_mfma_f32_16x16x32_bf16 v[172:175], a[156:159], a[184:187], v[172:175]\n" \
"v_mfma_f32_16x16x32_bf16 v[172:175], a[220:223], a[248:251], v[172:175]\n" \
"v_mfma_f32_16x16x32_bf16 v[168:171], a[156:159], a[180:183], v[168:171]\n" \
"v_mfma_f32_16x16x32_bf16 v[168:171], a[220:223], a[244:247], v[168:171]\n" \
"v_mfma_f32_16x16x32_bf16 v[164:167], a[156:159], a[176:179], v[164:167]\n" \
"v_mfma_f32_16x16x32_bf16 v[164:167], a[220:223], a[240:243], v[164:167]\n" \
"ds_read_b128 a[156:159], v28 offset:6144\n" \
"ds_read_b128 a[220:223], v29 offset:6144\n" \
"s_waitcnt lgkmcnt(15)\n" \
"v_mfma_f32_16x16x32_bf16 a[0:3], a[128:131], a[160:163], a[0:3]\n" \
"s_addk_i32 s84, 0x7c00\n" \
"v_mfma_f32_16x16x32_bf16 a[0:3], a[192:195], a[224:227], a[0:3]\n" \
"buffer_load_dwordx4 v27, s[72:75], s84 offen offset:2048 lds\n" \
"v_mfma_f32_16x16x32_bf16 a[4:7], a[128:131], a[164:167], a[4:7]\n" \
"ds_read_b64_tr_b16 a[176:177], v36 offset:0\n" \
"s_waitcnt lgkmcnt(15)\n" \
"v_mfma_f32_16x16x32_bf16 a[4:7], a[192:195], a[228:231], a[4:7]\n" \
"s_addk_i32 s84, 0x7c00\n" \
"s_waitcnt lgkmcnt(11)\n" \
"v_mfma_f32_16x16x32_bf16 a[8:11], a[128:131], a[168:171], a[8:11]\n" \
"buffer_load_dwordx4 v27, s[72:75], s84 offen offset:3072 lds\n" \
"s_waitcnt lgkmcnt(9)\n" \
"v_mfma_f32_16x16x32_bf16 a[8:11], a[192:195], a[232:235], a[8:11]\n" \
"ds_read_b64_tr_b16 a[178:179], v36 offset:1024\n" \
"s_waitcnt lgkmcnt(8)\n" \
"v_mfma_f32_16x16x32_bf16 a[12:15], a[128:131], a[172:175], a[12:15]\n" \
"ds_read_b64_tr_b16 a[240:241], v36 offset:8192\n" \
"s_waitcnt lgkmcnt(5)\n" \
"v_mfma_f32_16x16x32_bf16 a[12:15], a[192:195], a[236:239], a[12:15]\n" \
"v_mfma_f32_16x16x32_bf16 a[28:31], a[132:135], a[172:175], a[28:31]\n" \
"v_mfma_f32_16x16x32_bf16 a[28:31], a[196:199], a[236:239], a[28:31]\n" \
"ds_read_b64_tr_b16 a[242:243], v36 offset:9216\n" \
"v_mfma_f32_16x16x32_bf16 a[24:27], a[132:135], a[168:171], a[24:27]\n" \
"v_mfma_f32_16x16x32_bf16 a[24:27], a[196:199], a[232:235], a[24:27]\n" \
"v_mfma_f32_16x16x32_bf16 a[20:23], a[132:135], a[164:167], a[20:23]\n" \
"v_mfma_f32_16x16x32_bf16 a[20:23], a[196:199], a[228:231], a[20:23]\n" \
"ds_read_b64_tr_b16 a[180:181], v37 offset:0\n" \
"v_mfma_f32_16x16x32_bf16 a[16:19], a[132:135], a[160:163], a[16:19]\n" \
"v_mfma_f32_16x16x32_bf16 a[16:19], a[196:199], a[224:227], a[16:19]\n" \
"v_mfma_f32_16x16x32_bf16 a[32:35], a[136:139], a[160:163], a[32:35]\n" \
"v_bfe_u32 v44, v20, 2, 5\n" \
"ds_read_b64_tr_b16 a[182:183], v37 offset:1024\n" \
"v_mfma_f32_16x16x32_bf16 a[32:35], a[200:203], a[224:227], a[32:35]\n" \
"buffer_load_dword v44, v44, s[80:83], s38 offen offset:128 sc1\n" \
"v_mfma_f32_16x16x32_bf16 a[36:39], a[136:139], a[164:167], a[36:39]\n" \
"v_mfma_f32_16x16x32_bf16 a[36:39], a[200:203], a[228:231], a[36:39]\n" \
"ds_read_b64_tr_b16 a[244:245], v37 offset:8192\n" \
"v_mfma_f32_16x16x32_bf16 a[40:43], a[136:139], a[168:171], a[40:43]\n" \
"v_mfma_f32_16x16x32_bf16 a[40:43], a[200:203], a[232:235], a[40:43]\n" \
"v_mfma_f32_16x16x32_bf16 a[44:47], a[136:139], a[172:175], a[44:47]\n" \
"ds_read_b64_tr_b16 a[246:247], v37 offset:9216\n" \
"v_mfma_f32_16x16x32_bf16 a[44:47], a[200:203], a[236:239], a[44:47]\n" \
"v_mfma_f32_16x16x32_bf16 a[60:63], a[140:143], a[172:175], a[60:63]\n" \
"global_store_dwordx4 v49, v[0:3], s[28:29] offset:0 sc1\n" \
"v_mfma_f32_16x16x32_bf16 a[60:63], a[204:207], a[236:239], a[60:63]\n" \
"v_mfma_f32_16x16x32_bf16 a[56:59], a[140:143], a[168:171], a[56:59]\n" \
"ds_read_b64_tr_b16 a[184:185], v38 offset:0\n" \
"v_mfma_f32_16x16x32_bf16 a[56:59], a[204:207], a[232:235], a[56:59]\n" \
"v_mfma_f32_16x16x32_bf16 a[52:55], a[140:143], a[164:167], a[52:55]\n" \
"v_mfma_f32_16x16x32_bf16 a[52:55], a[204:207], a[228:231], a[52:55]\n" \
"ds_read_b64_tr_b16 a[186:187], v38 offset:1024\n" \
"v_mfma_f32_16x16x32_bf16 a[48:51], a[140:143], a[160:163], a[48:51]\n" \
"v_mfma_f32_16x16x32_bf16 a[48:51], a[204:207], a[224:227], a[48:51]\n" \
"v_mfma_f32_16x16x32_bf16 a[64:67], a[144:147], a[160:163], a[64:67]\n" \
"global_store_dwordx4 v49, v[0:3], s[28:29] offset:1024 sc1\n" \
"v_mfma_f32_16x16x32_bf16 a[64:67], a[208:211], a[224:227], a[64:67]\n" \
"ds_read_b64_tr_b16 a[248:249], v38 offset:8192\n" \
"v_mfma_f32_16x16x32_bf16 a[68:71], a[144:147], a[164:167], a[68:71]\n" \
"v_mfma_f32_16x16x32_bf16 a[68:71], a[208:211], a[228:231], a[68:71]\n" \
"ds_read_b64_tr_b16 a[250:251], v38 offset:9216\n" \
"v_mfma_f32_16x16x32_bf16 a[72:75], a[144:147], a[168:171], a[72:75]\n" \
"v_mfma_f32_16x16x32_bf16 a[72:75], a[208:211], a[232:235], a[72:75]\n" \
"v_mfma_f32_16x16x32_bf16 a[76:79], a[144:147], a[172:175], a[76:79]\n" \
"v_mfma_f32_16x16x32_bf16 a[76:79], a[208:211], a[236:239], a[76:79]\n" \
"ds_read_b64_tr_b16 a[188:189], v39 offset:0\n" \
"v_mfma_f32_16x16x32_bf16 a[92:95], a[148:151], a[172:175], a[92:95]\n" \
"s_mov_b64 exec, 0xffff\n" \
"global_store_dwordx4 v49, v[0:3], s[28:29] offset:2048 sc1\n" \
"s_mov_b64 exec, -1\n" \
"s_nop 0\n" \
"v_mfma_f32_16x16x32_bf16 a[92:95], a[212:215], a[236:239], a[92:95]\n" \
"v_mfma_f32_16x16x32_bf16 a[88:91], a[148:151], a[168:171], a[88:91]\n" \
"ds_read_b64_tr_b16 a[190:191], v39 offset:1024\n" \
"v_mfma_f32_16x16x32_bf16 a[88:91], a[212:215], a[232:235], a[88:91]\n" \
"v_mfma_f32_16x16x32_bf16 a[84:87], a[148:151], a[164:167], a[84:87]\n" \
"v_mfma_f32_16x16x32_bf16 a[84:87], a[212:215], a[228:231], a[84:87]\n" \
"ds_read_b64_tr_b16 a[252:253], v39 offset:8192\n" \
"v_mfma_f32_16x16x32_bf16 a[80:83], a[148:151], a[160:163], a[80:83]\n" \
"v_mfma_f32_16x16x32_bf16 a[80:83], a[212:215], a[224:227], a[80:83]\n" \
"v_mfma_f32_16x16x32_bf16 a[96:99], a[152:155], a[160:163], a[96:99]\n" \
"ds_read_b64_tr_b16 a[254:255], v39 offset:9216\n" \
"v_mfma_f32_16x16x32_bf16 a[96:99], a[216:219], a[224:227], a[96:99]\n" \
"v_mfma_f32_16x16x32_bf16 a[100:103], a[152:155], a[164:167], a[100:103]\n" \
"v_mfma_f32_16x16x32_bf16 a[100:103], a[216:219], a[228:231], a[100:103]\n" \
"v_mfma_f32_16x16x32_bf16 a[104:107], a[152:155], a[168:171], a[104:107]\n" \
"v_mfma_f32_16x16x32_bf16 a[104:107], a[216:219], a[232:235], a[104:107]\n" \
"v_mfma_f32_16x16x32_bf16 a[108:111], a[152:155], a[172:175], a[108:111]\n" \
"v_mfma_f32_16x16x32_bf16 a[108:111], a[216:219], a[236:239], a[108:111]\n" \
"s_waitcnt lgkmcnt(15)\n" \
"v_mfma_f32_16x16x32_bf16 a[124:127], a[156:159], a[172:175], a[124:127]\n" \
"v_mfma_f32_16x16x32_bf16 a[124:127], a[220:223], a[236:239], a[124:127]\n" \
"v_mfma_f32_16x16x32_bf16 a[120:123], a[156:159], a[168:171], a[120:123]\n" \
"v_mfma_f32_16x16x32_bf16 a[120:123], a[220:223], a[232:235], a[120:123]\n" \
"v_mfma_f32_16x16x32_bf16 a[116:119], a[156:159], a[164:167], a[116:119]\n" \
"v_mfma_f32_16x16x32_bf16 a[116:119], a[220:223], a[228:231], a[116:119]\n" \
"v_mfma_f32_16x16x32_bf16 a[112:115], a[156:159], a[160:163], a[112:115]\n" \
"v_mfma_f32_16x16x32_bf16 a[112:115], a[220:223], a[224:227], a[112:115]\n" \
"s_waitcnt vmcnt(3) lgkmcnt(0)\n" \
"s_barrier\n" \
"v_cmp_ne_u32_e32 vcc, s62, v44\n" \
"s_nop 4\n" \
"s_cbranch_vccnz L_poll61_%=\n" \
"L_pollr61_%=:\n" \
"s_mov_b32 s68, s12\n" \
"s_and_b32 s69, s13, 0xffff\n" \
"s_mov_b32 s70, 0x480000\n" \
"s_mov_b32 s71, 0x20000\n" \
"v_mfma_f32_16x16x32_bf16 v[52:55], a[128:131], a[176:179], v[52:55]\n" \
"s_add_u32 m0, s40, 0x0\n" \
"s_add_u32 s85, s39, 0x0\n" \
"s_nop 0\n" \
"v_mfma_f32_16x16x32_bf16 v[52:55], a[192:195], a[240:243], v[52:55]\n" \
"buffer_load_dwordx4 v20, s[80:83], s85 offen sc1 lds\n" \
"v_mfma_f32_16x16x32_bf16 v[56:59], a[128:131], a[180:183], v[56:59]\n" \
"ds_read_b64_tr_b16 a[160:161], v32 offset:32768\n" \
"v_mfma_f32_16x16x32_bf16 v[56:59], a[192:195], a[244:247], v[56:59]\n" \
"s_add_u32 m0, s40, 0x1000\n" \
"s_add_u32 s85, s39, 0x1000\n" \
".fill (L_pb + 57056 - .) / 4, 4, 0xBF800000\n" \
"L_pp14:\n" \
"s_cbranch_execz L_pret\n" \
"s_nop 0\n" \
"v_mfma_f32_16x16x32_bf16 v[60:63], a[128:131], a[184:187], v[60:63]\n" \
"buffer_load_dwordx4 v20, s[80:83], s85 offen sc1 lds\n" \
"v_mfma_f32_16x16x32_bf16 v[60:63], a[192:195], a[248:251], v[60:63]\n" \
"ds_read_b64_tr_b16 a[162:163], v32 offset:33792\n" \
"v_mfma_f32_16x16x32_bf16 v[64:67], a[128:131], a[188:191], v[64:67]\n" \
"ds_read_b64_tr_b16 a[224:225], v32 offset:40960\n" \
"v_mfma_f32_16x16x32_bf16 v[64:67], a[192:195], a[252:255], v[64:67]\n" \
"ds_read_b128 a[128:131], v28 offset:49152\n" \
"v_mfma_f32_16x16x32_bf16 v[80:83], a[132:135], a[188:191], v[80:83]\n" \
"ds_read_b128 a[192:195], v29 offset:49152\n" \
"v_mfma_f32_16x16x32_bf16 v[80:83], a[196:199], a[252:255], v[80:83]\n" \
"s_add_u32 m0, s40, 0x3000\n" \
"s_movk_i32 s84, 0x0\n" \
"v_mfma_f32_16x16x32_bf16 v[76:79], a[132:135], a[184:187], v[76:79]\n" \
"buffer_load_dwordx4 v21, s[76:79], s84 offen offset:0 lds\n" \
"v_mfma_f32_16x16x32_bf16 v[76:79], a[196:199], a[248:251], v[76:79]\n" \
"s_add_u32 m0, s30, 0x0\n" \
"s_movk_i32 s84, 0x0\n" \
"s_nop 0\n" \
"v_mfma_f32_16x16x32_bf16 v[72:75], a[132:135], a[180:183], v[72:75]\n" \
"buffer_load_dwordx4 v24, s[76:79], s84 offen offset:0 lds\n" \
"v_mfma_f32_16x16x32_bf16 v[72:75], a[196:199], a[244:247], v[72:75]\n" \
"ds_read_b64_tr_b16 a[226:227], v32 offset:41984\n" \
"v_mfma_f32_16x16x32_bf16 v[68:71], a[132:135], a[176:179], v[68:71]\n" \
"ds_read_b64_tr_b16 a[164:165], v33 offset:32768\n" \
"v_mfma_f32_16x16x32_bf16 v[68:71], a[196:199], a[240:243], v[68:71]\n" \
"ds_read_b128 a[132:135], v28 offset:51200\n" \
"v_mfma_f32_16x16x32_bf16 v[84:87], a[136:139], a[176:179], v[84:87]\n" \
"ds_read_b128 a[196:199], v29 offset:51200\n" \
"v_mfma_f32_16x16x32_bf16 v[84:87], a[200:203], a[240:243], v[84:87]\n" \
"s_movk_i32 s84, 0x2000\n" \
"v_mfma_f32_16x16x32_bf16 v[88:91], a[136:139], a[180:183], v[88:91]\n" \
"buffer_load_dwordx4 v25, s[76:79], s84 offen offset:1024 lds\n" \
"v_mfma_f32_16x16x32_bf16 v[88:91], a[200:203], a[244:247], v[88:91]\n" \
"s_movk_i32 s84, 0x4000\n" \
"v_mfma_f32_16x16x32_bf16 v[92:95], a[136:139], a[184:187], v[92:95]\n" \
"buffer_load_dwordx4 v24, s[76:79], s84 offen offset:2048 lds\n" \
"v_mfma_f32_16x16x32_bf16 v[92:95], a[200:203], a[248:251], v[92:95]\n" \
"ds_read_b64_tr_b16 a[166:167], v33 offset:33792\n" \
"v_mfma_f32_16x16x32_bf16 v[96:99], a[136:139], a[188:191], v[96:99]\n" \
"ds_read_b64_tr_b16 a[228:229], v33 offset:40960\n" \
"v_mfma_f32_16x16x32_bf16 v[96:99], a[200:203], a[252:255], v[96:99]\n" \
"ds_read_b128 a[136:139], v28 offset:53248\n" \
"v_mfma_f32_16x16x32_bf16 v[112:115], a[140:143], a[188:191], v[112:115]\n" \
"ds_read_b128 a[200:203], v29 offset:53248\n" \
"v_mfma_f32_16x16x32_bf16 v[112:115], a[204:207], a[252:255], v[112:115]\n" \
"s_movk_i32 s84, 0x6000\n" \
"v_mfma_f32_16x16x32_bf16 v[108:111], a[140:143], a[184:187], v[108:111]\n" \
"buffer_load_dwordx4 v25, s[76:79], s84 offen offset:3072 lds\n" \
"v_mfma_f32_16x16x32_bf16 v[108:111], a[204:207], a[248:251], v[108:111]\n" \
"s_add_u32 m0, s31, 0x0\n" \
"s_movk_i32 s84, 0x0\n" \
"v_mfma_f32_16x16x32_bf16 v[104:107], a[140:143], a[180:183], v[104:107]\n" \
"buffer_load_dwordx4 v26, s[68:71], s84 offen offset:0 lds\n" \
"v_mfma_f32_16x16x32_bf16 v[104:107], a[204:207], a[244:247], v[104:107]\n" \
"s_movk_i32 s84, 0x7c00\n" \
"v_mfma_f32_16x16x32_bf16 v[100:103], a[140:143], a[176:179], v[100:103]\n" \
"buffer_load_dwordx4 v26, s[68:71], s84 offen offset:1024 lds\n" \
"v_mfma_f32_16x16x32_bf16 v[100:103], a[204:207], a[240:243], v[100:103]\n" \
"ds_read_b128 a[140:143], v28 offset:55296\n" \
"v_mfma_f32_16x16x32_bf16 v[116:119], a[144:147], a[176:179], v[116:119]\n" \
"ds_read_b128 a[204:207], v29 offset:55296\n" \
"v_mfma_f32_16x16x32_bf16 v[116:119], a[208:211], a[240:243], v[116:119]\n" \
"s_addk_i32 s84, 0x7c00\n" \
"v_mfma_f32_16x16x32_bf16 v[120:123], a[144:147], a[180:183], v[120:123]\n" \
"buffer_load_dwordx4 v27, s[68:71], s84 offen offset:2048 lds\n" \
"v_mfma_f32_16x16x32_bf16 v[120:123], a[208:211], a[244:247], v[120:123]\n" \
"ds_read_b64_tr_b16 a[230:231], v33 offset:41984\n" \
"v_mfma_f32_16x16x32_bf16 v[124:127], a[144:147], a[184:187], v[124:127]\n" \
"s_addk_i32 s84, 0x7c00\n" \
"v_mfma_f32_16x16x32_bf16 v[124:127], a[208:211], a[248:251], v[124:127]\n" \
"buffer_load_dwordx4 v27, s[68:71], s84 offen offset:3072 lds\n" \
"v_mfma_f32_16x16x32_bf16 v[128:131], a[144:147], a[188:191], v[128:131]\n" \
"ds_read_b64_tr_b16 a[168:169], v34 offset:32768\n" \
"v_mfma_f32_16x16x32_bf16 v[128:131], a[208:211], a[252:255], v[128:131]\n" \
"ds_read_b128 a[144:147], v28 offset:32768\n" \
"v_mfma_f32_16x16x32_bf16 v[144:147], a[148:151], a[188:191], v[144:147]\n" \
"ds_read_b128 a[208:211], v29 offset:32768\n" \
"v_mfma_f32_16x16x32_bf16 v[144:147], a[212:215], a[252:255], v[144:147]\n" \
"s_add_u32 m0, s30, 0x1000\n" \
"s_mov_b32 s84, 0x9000\n" \
"v_mfma_f32_16x16x32_bf16 v[140:143], a[148:151], a[184:187], v[140:143]\n" \
"buffer_load_dwordx4 v24, s[76:79], s84 offen offset:0 lds\n" \
"v_mfma_f32_16x16x32_bf16 v[140:143], a[212:215], a[248:251], v[140:143]\n" \
"ds_read_b64_tr_b16 a[170:171], v34 offset:33792\n" \
"v_mfma_f32_16x16x32_bf16 v[136:139], a[148:151], a[180:183], v[136:139]\n" \
"s_addk_i32 s84, 0x2000\n" \
"s_nop 0\n" \
"v_mfma_f32_16x16x32_bf16 v[136:139], a[212:215], a[244:247], v[136:139]\n" \
"buffer_load_dwordx4 v25, s[76:79], s84 offen offset:1024 lds\n" \
"v_mfma_f32_16x16x32_bf16 v[132:135], a[148:151], a[176:179], v[132:135]\n" \
"ds_read_b64_tr_b16 a[232:233], v34 offset:40960\n" \
"v_mfma_f32_16x16x32_bf16 v[132:135], a[212:215], a[240:243], v[132:135]\n" \
"ds_read_b128 a[148:151], v28 offset:34816\n" \
"v_mfma_f32_16x16x32_bf16 v[148:151], a[152:155], a[176:179], v[148:151]\n" \
"ds_read_b128 a[212:215], v29 offset:34816\n" \
"v_mfma_f32_16x16x32_bf16 v[148:151], a[216:219], a[240:243], v[148:151]\n" \
"ds_read_b64_tr_b16 a[234:235], v34 offset:41984\n" \
"v_mfma_f32_16x16x32_bf16 v[152:155], a[152:155], a[180:183], v[152:155]\n" \
"ds_read_b64_tr_b16 a[172:173], v35 offset:32768\n" \
"v_mfma_f32_16x16x32_bf16 v[152:155], a[216:219], a[244:247], v[152:155]\n" \
"ds_read_b64_tr_b16 a[174:175], v35 offset:33792\n" \
"v_mfma_f32_16x16x32_bf16 v[156:159], a[152:155], a[184:187], v[156:159]\n" \
"ds_read_b64_tr_b16 a[236:237], v35 offset:40960\n" \
"v_mfma_f32_16x16x32_bf16 v[156:159], a[216:219], a[248:251], v[156:159]\n" \
"ds_read_b64_tr_b16 a[238:239], v35 offset:41984\n" \
"v_mfma_f32_16x16x32_bf16 v[160:163], a[152:155], a[188:191], v[160:163]\n" \
"v_mfma_f32_16x16x32_bf16 v[160:163], a[216:219], a[252:255], v[160:163]\n" \
"ds_read_b128 a[152:155], v28 offset:36864\n" \
"v_mfma_f32_16x16x32_bf16 v[176:179], a[156:159], a[188:191], v[176:179]\n" \
"ds_read_b128 a[216:219], v29 offset:36864\n" \
"v_mfma_f32_16x16x32_bf16 v[176:179], a[220:223], a[252:255], v[176:179]\n" \
"v_mfma_f32_16x16x32_bf16 v[172:175], a[156:159], a[184:187], v[172:175]\n" \
"v_mfma_f32_16x16x32_bf16 v[172:175], a[220:223], a[248:251], v[172:175]\n" \
"v_mfma_f32_16x16x32_bf16 v[168:171], a[156:159], a[180:183], v[168:171]\n" \
"v_mfma_f32_16x16x32_bf16 v[168:171], a[220:223], a[244:247], v[168:171]\n" \
"v_mfma_f32_16x16x32_bf16 v[164:167], a[156:159], a[176:179], v[164:167]\n" \
"v_mfma_f32_16x16x32_bf16 v[164:167], a[220:223], a[240:243], v[164:167]\n" \
"ds_read_b128 a[156:159], v28 offset:38912\n" \
"ds_read_b128 a[220:223], v29 offset:38912\n" \
"s_waitcnt lgkmcnt(15)\n" \
"v_mfma_f32_16x16x32_bf16 a[0:3], a[128:131], a[160:163], a[0:3]\n" \
"s_addk_i32 s84, 0x2000\n" \
"v_mfma_f32_16x16x32_bf16 a[0:3], a[192:195], a[224:227], a[0:3]\n" \
"buffer_load_dwordx4 v24, s[76:79], s84 offen offset:2048 lds\n" \
"v_mfma_f32_16x16x32_bf16 a[4:7], a[128:131], a[164:167], a[4:7]\n" \
"ds_read_b64_tr_b16 a[176:177], v36 offset:32768\n" \
"v_mfma_f32_16x16x32_bf16 a[4:7], a[192:195], a[228:231], a[4:7]\n" \
"s_addk_i32 s84, 0x2000\n" \
"s_waitcnt lgkmcnt(13)\n" \
"v_mfma_f32_16x16x32_bf16 a[8:11], a[128:131], a[168:171], a[8:11]\n" \
"buffer_load_dwordx4 v25, s[76:79], s84 offen offset:3072 lds\n" \
"s_waitcnt lgkmcnt(9)\n" \
"v_mfma_f32_16x16x32_bf16 a[8:11], a[192:195], a[232:235], a[8:11]\n" \
"ds_read_b64_tr_b16 a[178:179], v36 offset:33792\n" \
"s_waitcnt lgkmcnt(8)\n" \
"v_mfma_f32_16x16x32_bf16 a[12:15], a[128:131], a[172:175], a[12:15]\n" \
"ds_read_b64_tr_b16 a[240:241], v36 offset:40960\n" \
"s_waitcnt lgkmcnt(7)\n" \
"v_mfma_f32_16x16x32_bf16 a[12:15], a[192:195], a[236:239], a[12:15]\n" \
"v_mfma_f32_16x16x32_bf16 a[28:31], a[132:135], a[172:175], a[28:31]\n" \
"v_mfma_f32_16x16x32_bf16 a[28:31], a[196:199], a[236:239], a[28:31]\n" \
"s_add_u32 m0, s30, 0x20000\n" \
"s_movk_i32 s84, 0x100\n" \
"v_mfma_f32_16x16x32_bf16 a[24:27], a[132:135], a[168:171], a[24:27]\n" \
"buffer_load_dwordx4 v24, s[76:79], s84 offen offset:0 lds\n" \
"v_mfma_f32_16x16x32_bf16 a[24:27], a[196:199], a[232:235], a[24:27]\n" \
"ds_read_b64_tr_b16 a[242:243], v36 offset:41984\n" \
"v_mfma_f32_16x16x32_bf16 a[20:23], a[132:135], a[164:167], a[20:23]\n" \
"v_accvgpr_read_b32 v184, a12\n" \
"v_accvgpr_read_b32 v185, a13\n" \
"v_mfma_f32_16x16x32_bf16 a[20:23], a[196:199], a[228:231], a[20:23]\n" \
"ds_read_b64_tr_b16 a[180:181], v37 offset:32768\n" \
"v_accvgpr_read_b32 v186, a14\n" \
"v_mfma_f32_16x16x32_bf16 a[16:19], a[132:135], a[160:163], a[16:19]\n" \
"s_movk_i32 s84, 0x2100\n" \
"s_nop 0\n" \
"v_mfma_f32_16x16x32_bf16 a[16:19], a[196:199], a[224:227], a[16:19]\n" \
"buffer_load_dwordx4 v25, s[76:79], s84 offen offset:1024 lds\n" \
"v_mfma_f32_16x16x32_bf16 a[32:35], a[136:139], a[160:163], a[32:35]\n" \
"ds_read_b64_tr_b16 a[182:183], v37 offset:33792\n" \
"v_accvgpr_read_b32 v187, a15\n" \
"v_mfma_f32_16x16x32_bf16 a[32:35], a[200:203], a[224:227], a[32:35]\n" \
"v_accvgpr_read_b32 v180, a28\n" \
"v_accvgpr_read_b32 v181, a29\n" \
"v_mfma_f32_16x16x32_bf16 a[36:39], a[136:139], a[164:167], a[36:39]\n" \
"v_accvgpr_read_b32 v182, a30\n" \
"v_accvgpr_read_b32 v183, a31\n" \
"v_mfma_f32_16x16x32_bf16 a[36:39], a[200:203], a[228:231], a[36:39]\n" \
"s_movk_i32 s84, 0x4100\n" \
"s_nop 0\n" \
"v_mfma_f32_16x16x32_bf16 a[40:43], a[136:139], a[168:171], a[40:43]\n" \
"buffer_load_dwordx4 v24, s[76:79], s84 offen offset:2048 lds\n" \
"v_mfma_f32_16x16x32_bf16 a[40:43], a[200:203], a[232:235], a[40:43]\n" \
"ds_read_b64_tr_b16 a[244:245], v37 offset:40960\n" \
"v_cvt_pk_bf16_f32 v184, v184, v185\n" \
"v_mfma_f32_16x16x32_bf16 a[44:47], a[136:139], a[172:175], a[44:47]\n" \
"ds_read_b64_tr_b16 a[246:247], v37 offset:41984\n" \
"v_cvt_pk_bf16_f32 v185, v186, v187\n" \
"v_mfma_f32_16x16x32_bf16 a[44:47], a[200:203], a[236:239], a[44:47]\n" \
"v_cvt_pk_bf16_f32 v186, v180, v181\n" \
"v_cvt_pk_bf16_f32 v187, v182, v183\n" \
"v_mfma_f32_16x16x32_bf16 a[60:63], a[140:143], a[172:175], a[60:63]\n" \
"s_movk_i32 s84, 0x6100\n" \
"v_mfma_f32_16x16x32_bf16 a[60:63], a[204:207], a[236:239], a[60:63]\n" \
"buffer_load_dwordx4 v25, s[76:79], s84 offen offset:3072 lds\n" \
"v_mfma_f32_16x16x32_bf16 a[56:59], a[140:143], a[168:171], a[56:59]\n" \
"ds_read_b64_tr_b16 a[184:185], v38 offset:32768\n" \
"v_accvgpr_read_b32 v188, a8\n" \
"v_mfma_f32_16x16x32_bf16 a[56:59], a[204:207], a[232:235], a[56:59]\n" \
"v_accvgpr_read_b32 v189, a9\n" \
"v_accvgpr_read_b32 v190, a10\n" \
"v_mfma_f32_16x16x32_bf16 a[52:55], a[140:143], a[164:167], a[52:55]\n" \
"s_add_u32 m0, s30, 0x21000\n" \
"s_addk_i32 s84, 0x3000\n" \
"v_mfma_f32_16x16x32_bf16 a[52:55], a[204:207], a[228:231], a[52:55]\n" \
"buffer_load_dwordx4 v24, s[76:79], s84 offen offset:0 lds\n" \
"v_mfma_f32_16x16x32_bf16 a[48:51], a[140:143], a[160:163], a[48:51]\n" \
"ds_read_b64_tr_b16 a[186:187], v38 offset:33792\n" \
"v_accvgpr_read_b32 v191, a11\n" \
"v_mfma_f32_16x16x32_bf16 a[48:51], a[204:207], a[224:227], a[48:51]\n" \
"v_accvgpr_read_b32 v180, a24\n" \
"v_accvgpr_read_b32 v181, a25\n" \
"v_mfma_f32_16x16x32_bf16 a[64:67], a[144:147], a[160:163], a[64:67]\n" \
"ds_read_b64_tr_b16 a[248:249], v38 offset:40960\n" \
"v_accvgpr_read_b32 v182, a26\n" \
"v_mfma_f32_16x16x32_bf16 a[64:67], a[208:211], a[224:227], a[64:67]\n" \
"s_addk_i32 s84, 0x2000\n" \
"s_nop 0\n" \
"v_mfma_f32_16x16x32_bf16 a[68:71], a[144:147], a[164:167], a[68:71]\n" \
"buffer_load_dwordx4 v25, s[76:79], s84 offen offset:1024 lds\n" \
"v_mfma_f32_16x16x32_bf16 a[68:71], a[208:211], a[228:231], a[68:71]\n" \
"ds_read_b64_tr_b16 a[250:251], v38 offset:41984\n" \
"v_accvgpr_read_b32 v183, a27\n" \
"v_mfma_f32_16x16x32_bf16 a[72:75], a[144:147], a[168:171], a[72:75]\n" \
"v_cvt_pk_bf16_f32 v188, v188, v189\n" \
"v_cvt_pk_bf16_f32 v189, v190, v191\n" \
"v_mfma_f32_16x16x32_bf16 a[72:75], a[208:211], a[232:235], a[72:75]\n" \
"v_cvt_pk_bf16_f32 v190, v180, v181\n" \
"v_cvt_pk_bf16_f32 v191, v182, v183\n" \
"v_mfma_f32_16x16x32_bf16 a[76:79], a[144:147], a[172:175], a[76:79]\n" \
"s_addk_i32 s84, 0x2000\n" \
"v_mfma_f32_16x16x32_bf16 a[76:79], a[208:211], a[236:239], a[76:79]\n" \
"buffer_load_dwordx4 v24, s[76:79], s84 offen offset:2048 lds\n" \
"v_mfma_f32_16x16x32_bf16 a[92:95], a[148:151], a[172:175], a[92:95]\n" \
"s_add_u32 s89, s41, 0x0\n" \
"buffer_store_dwordx4 v[184:187], v20, s[80:83], s89 offen offset:3072 sc1\n" \
"v_mfma_f32_16x16x32_bf16 a[92:95], a[212:215], a[236:239], a[92:95]\n" \
"ds_read_b64_tr_b16 a[188:189], v39 offset:32768\n" \
"v_accvgpr_read_b32 v192, a4\n" \
"v_mfma_f32_16x16x32_bf16 a[88:91], a[148:151], a[168:171], a[88:91]\n" \
"ds_read_b64_tr_b16 a[190:191], v39 offset:33792\n" \
"v_accvgpr_read_b32 v193, a5\n" \
"v_mfma_f32_16x16x32_bf16 a[88:91], a[212:215], a[232:235], a[88:91]\n" \
"s_addk_i32 s84, 0x2000\n" \
"v_mfma_f32_16x16x32_bf16 a[84:87], a[148:151], a[164:167], a[84:87]\n" \
"buffer_load_dwordx4 v25, s[76:79], s84 offen offset:3072 lds\n" \
"v_mfma_f32_16x16x32_bf16 a[84:87], a[212:215], a[228:231], a[84:87]\n" \
"ds_read_b64_tr_b16 a[252:253], v39 offset:40960\n" \
"v_accvgpr_read_b32 v194, a6\n" \
"v_mfma_f32_16x16x32_bf16 a[80:83], a[148:151], a[160:163], a[80:83]\n" \
"s_add_u32 s88, s41, 0x0\n" \
"buffer_store_dwordx4 v[188:191], v20, s[80:83], s88 offen offset:2048 sc1\n" \
"v_mfma_f32_16x16x32_bf16 a[80:83], a[212:215], a[224:227], a[80:83]\n" \
"v_accvgpr_read_b32 v195, a7\n" \
"v_accvgpr_read_b32 v180, a20\n" \
"s_waitcnt lgkmcnt(15)\n" \
"s_nop 0\n" \
"v_mfma_f32_16x16x32_bf16 a[96:99], a[152:155], a[160:163], a[96:99]\n" \
"ds_read_b64_tr_b16 a[254:255], v39 offset:41984\n" \
"v_accvgpr_read_b32 v181, a21\n" \
"v_mfma_f32_16x16x32_bf16 a[96:99], a[216:219], a[224:227], a[96:99]\n" \
"v_accvgpr_read_b32 v182, a22\n" \
"v_accvgpr_read_b32 v183, a23\n" \
"v_mfma_f32_16x16x32_bf16 a[100:103], a[152:155], a[164:167], a[100:103]\n" \
"v_cvt_pk_bf16_f32 v192, v192, v193\n" \
"v_cvt_pk_bf16_f32 v193, v194, v195\n" \
"v_mfma_f32_16x16x32_bf16 a[100:103], a[216:219], a[228:231], a[100:103]\n" \
"v_cvt_pk_bf16_f32 v194, v180, v181\n" \
"v_cvt_pk_bf16_f32 v195, v182, v183\n" \
"v_mfma_f32_16x16x32_bf16 a[104:107], a[152:155], a[168:171], a[104:107]\n" \
"s_add_u32 s89, s41, 0x0\n" \
"s_nop 0\n" \
"buffer_store_dwordx4 v[192:195], v20, s[80:83], s89 offen offset:1024 sc1\n" \
"v_mfma_f32_16x16x32_bf16 a[104:107], a[216:219], a[232:235], a[104:107]\n" \
"v_accvgpr_read_b32 v196, a0\n" \
"v_accvgpr_read_b32 v197, a1\n" \
"v_mfma_f32_16x16x32_bf16 a[108:111], a[152:155], a[172:175], a[108:111]\n" \
"v_accvgpr_read_b32 v198, a2\n" \
"v_accvgpr_read_b32 v199, a3\n" \
"v_mfma_f32_16x16x32_bf16 a[108:111], a[216:219], a[236:239], a[108:111]\n" \
"v_accvgpr_read_b32 v180, a16\n" \
"v_accvgpr_read_b32 v181, a17\n" \
"v_mfma_f32_16x16x32_bf16 a[124:127], a[156:159], a[172:175], a[124:127]\n" \
"v_accvgpr_read_b32 v182, a18\n" \
"v_accvgpr_read_b32 v183, a19\n" \
"v_mfma_f32_16x16x32_bf16 a[124:127], a[220:223], a[236:239], a[124:127]\n" \
"v_cvt_pk_bf16_f32 v196, v196, v197\n" \
"v_cvt_pk_bf16_f32 v197, v198, v199\n" \
"v_mfma_f32_16x16x32_bf16 a[120:123], a[156:159], a[168:171], a[120:123]\n" \
"v_cvt_pk_bf16_f32 v198, v180, v181\n" \
"v_cvt_pk_bf16_f32 v199, v182, v183\n" \
"s_add_u32 s88, s41, 0x0\n" \
"s_nop 0\n" \
"buffer_store_dwordx4 v[196:199], v20, s[80:83], s88 offen offset:0 sc1\n" \
"v_mfma_f32_16x16x32_bf16 a[120:123], a[220:223], a[232:235], a[120:123]\n" \
"v_accvgpr_read_b32 v200, a44\n" \
"v_accvgpr_read_b32 v201, a45\n" \
"v_mfma_f32_16x16x32_bf16 a[116:119], a[156:159], a[164:167], a[116:119]\n" \
"v_accvgpr_read_b32 v202, a46\n" \
"v_accvgpr_read_b32 v203, a47\n" \
"v_mfma_f32_16x16x32_bf16 a[116:119], a[220:223], a[228:231], a[116:119]\n" \
"v_accvgpr_read_b32 v180, a60\n" \
"v_accvgpr_read_b32 v181, a61\n" \
"v_mfma_f32_16x16x32_bf16 a[112:115], a[156:159], a[160:163], a[112:115]\n" \
"v_accvgpr_read_b32 v182, a62\n" \
"v_accvgpr_read_b32 v183, a63\n" \
"v_mfma_f32_16x16x32_bf16 a[112:115], a[220:223], a[224:227], a[112:115]\n" \
"v_cvt_pk_bf16_f32 v200, v200, v201\n" \
"v_cvt_pk_bf16_f32 v201, v202, v203\n" \
"s_waitcnt vmcnt(12) lgkmcnt(0)\n" \
"s_barrier\n" \
"v_xor_b32_e32 v36, 0x4000, v36\n" \
"v_xor_b32_e64 v32, 0x0, v48\n" \
"v_xor_b32_e32 v37, 0x4000, v37\n" \
"v_xor_b32_e64 v33, 0x20, v48\n" \
"v_xor_b32_e32 v38, 0x4000, v38\n" \
"v_xor_b32_e64 v34, 0x40, v48\n" \
"v_xor_b32_e32 v39, 0x4000, v39\n" \
"v_xor_b32_e32 v35, 0x60, v48\n" \
"v_add_u32_e32 v28, s43, v28\n" \
"v_add_u32_e32 v29, s43, v29\n" \
"v_mfma_f32_16x16x32_bf16 v[116:119], a[144:147], a[176:179], v[116:119]\n" \
"s_add_u32 m0, s40, 0x8000\n" \
"s_add_u32 s85, s39, 0x2000\n" \
"v_mfma_f32_16x16x32_bf16 v[116:119], a[208:211], a[240:243], v[116:119]\n" \
"buffer_load_dwordx4 v20, s[80:83], s85 offen sc1 lds\n" \
"v_mfma_f32_16x16x32_bf16 v[120:123], a[144:147], a[180:183], v[120:123]\n" \
"ds_read_b64_tr_b16 a[160:161], v32 offset:0\n" \
"v_cvt_pk_bf16_f32 v202, v180, v181\n" \
"v_mfma_f32_16x16x32_bf16 v[120:123], a[208:211], a[244:247], v[120:123]\n" \
"s_add_u32 m0, s40, 0x9000\n" \
"s_add_u32 s85, s39, 0x3000\n" \
"v_mfma_f32_16x16x32_bf16 v[124:127], a[144:147], a[184:187], v[124:127]\n" \
"buffer_load_dwordx4 v20, s[80:83], s85 offen sc1 lds\n" \
"v_mfma_f32_16x16x32_bf16 v[124:127], a[208:211], a[248:251], v[124:127]\n" \
"ds_read_b64_tr_b16 a[162:163], v32 offset:512\n" \
"v_cvt_pk_bf16_f32 v203, v182, v183\n" \
"v_mfma_f32_16x16x32_bf16 v[128:131], a[144:147], a[188:191], v[128:131]\n" \
"ds_read_b64_tr_b16 a[224:225], v32 offset:4096\n" \
"v_accvgpr_read_b32 v204, a40\n" \
"v_mfma_f32_16x16x32_bf16 v[128:131], a[208:211], a[252:255], v[128:131]\n" \
"ds_read_b128 a[144:147], v28 offset:8192\n" \
"v_accvgpr_read_b32 v205, a41\n" \
"v_mfma_f32_16x16x32_bf16 v[144:147], a[148:151], a[188:191], v[144:147]\n" \
"ds_read_b128 a[208:211], v29 offset:8192\n" \
"v_accvgpr_read_b32 v206, a42\n" \
"v_mfma_f32_16x16x32_bf16 v[144:147], a[212:215], a[252:255], v[144:147]\n" \
"s_add_u32 m0, s40, 0xb000\n" \
"s_movk_i32 s84, 0x80\n" \
"v_mfma_f32_16x16x32_bf16 v[140:143], a[148:151], a[184:187], v[140:143]\n" \
"buffer_load_dwordx4 v21, s[76:79], s84 offen offset:0 lds\n" \
"v_mfma_f32_16x16x32_bf16 v[140:143], a[212:215], a[248:251], v[140:143]\n" \
"s_add_u32 m0, s30, 0x8000\n" \
"s_movk_i32 s84, 0x80\n" \
"v_mfma_f32_16x16x32_bf16 v[136:139], a[148:151], a[180:183], v[136:139]\n" \
"buffer_load_dwordx4 v24, s[76:79], s84 offen offset:0 lds\n" \
"v_mfma_f32_16x16x32_bf16 v[136:139], a[212:215], a[244:247], v[136:139]\n" \
"ds_read_b64_tr_b16 a[226:227], v32 offset:4608\n" \
"v_accvgpr_read_b32 v207, a43\n" \
"v_mfma_f32_16x16x32_bf16 v[132:135], a[148:151], a[176:179], v[132:135]\n" \
"ds_read_b64_tr_b16 a[164:165], v33 offset:0\n" \
"v_accvgpr_read_b32 v180, a56\n" \
"v_mfma_f32_16x16x32_bf16 v[132:135], a[212:215], a[240:243], v[132:135]\n" \
"ds_read_b128 a[148:151], v28 offset:10240\n" \
"v_accvgpr_read_b32 v181, a57\n" \
"v_mfma_f32_16x16x32_bf16 v[148:151], a[152:155], a[176:179], v[148:151]\n" \
"ds_read_b128 a[212:215], v29 offset:10240\n" \
"v_accvgpr_read_b32 v182, a58\n" \
"v_mfma_f32_16x16x32_bf16 v[148:151], a[216:219], a[240:243], v[148:151]\n" \
"s_movk_i32 s84, 0x2080\n" \
"v_mfma_f32_16x16x32_bf16 v[152:155], a[152:155], a[180:183], v[152:155]\n" \
"buffer_load_dwordx4 v25, s[76:79], s84 offen offset:1024 lds\n" \
"v_mfma_f32_16x16x32_bf16 v[152:155], a[216:219], a[244:247], v[152:155]\n" \
"s_movk_i32 s84, 0x4080\n" \
"v_mfma_f32_16x16x32_bf16 v[156:159], a[152:155], a[184:187], v[156:159]\n" \
"buffer_load_dwordx4 v24, s[76:79], s84 offen offset:2048 lds\n" \
"v_mfma_f32_16x16x32_bf16 v[156:159], a[216:219], a[248:251], v[156:159]\n" \
"ds_read_b64_tr_b16 a[166:167], v33 offset:512\n" \
"v_accvgpr_read_b32 v183, a59\n" \
"v_mfma_f32_16x16x32_bf16 v[160:163], a[152:155], a[188:191], v[160:163]\n" \
"ds_read_b64_tr_b16 a[228:229], v33 offset:4096\n" \
"v_cvt_pk_bf16_f32 v204, v204, v205\n" \
"v_mfma_f32_16x16x32_bf16 v[160:163], a[216:219], a[252:255], v[160:163]\n" \
"ds_read_b128 a[152:155], v28 offset:12288\n" \
"v_cvt_pk_bf16_f32 v205, v206, v207\n" \
"v_mfma_f32_16x16x32_bf16 v[176:179], a[156:159], a[188:191], v[176:179]\n" \
"ds_read_b128 a[216:219], v29 offset:12288\n" \
"v_cvt_pk_bf16_f32 v206, v180, v181\n" \
"v_mfma_f32_16x16x32_bf16 v[176:179], a[220:223], a[252:255], v[176:179]\n" \
"s_movk_i32 s84, 0x6080\n" \
"v_mfma_f32_16x16x32_bf16 v[172:175], a[156:159], a[184:187], v[172:175]\n" \
"buffer_load_dwordx4 v25, s[76:79], s84 offen offset:3072 lds\n" \
"v_mfma_f32_16x16x32_bf16 v[172:175], a[220:223], a[248:251], v[172:175]\n" \
"s_add_u32 m0, s31, 0x8000\n" \
"s_mov_b32 s84, 0x80000\n" \
"v_mfma_f32_16x16x32_bf16 v[168:171], a[156:159], a[180:183], v[168:171]\n" \
"buffer_load_dwordx4 v26, s[68:71], s84 offen offset:0 lds\n" \
"v_mfma_f32_16x16x32_bf16 v[168:171], a[220:223], a[244:247], v[168:171]\n" \
"s_addk_i32 s84, 0x7c00\n" \
"v_mfma_f32_16x16x32_bf16 v[164:167], a[156:159], a[176:179], v[164:167]\n" \
"buffer_load_dwordx4 v26, s[68:71], s84 offen offset:1024 lds\n" \
"v_mfma_f32_16x16x32_bf16 v[164:167], a[220:223], a[240:243], v[164:167]\n" \
"ds_read_b128 a[156:159], v28 offset:14336\n" \
"v_cvt_pk_bf16_f32 v207, v182, v183\n" \
"v_mfma_f32_16x16x32_bf16 v[84:87], a[136:139], a[176:179], v[84:87]\n" \
"ds_read_b128 a[220:223], v29 offset:14336\n" \
"v_accvgpr_read_b32 v208, a36\n" \
"v_mfma_f32_16x16x32_bf16 v[84:87], a[200:203], a[240:243], v[84:87]\n" \
"s_addk_i32 s84, 0x7c00\n" \
"v_mfma_f32_16x16x32_bf16 v[88:91], a[136:139], a[180:183], v[88:91]\n" \
"buffer_load_dwordx4 v27, s[68:71], s84 offen offset:2048 lds\n" \
"v_mfma_f32_16x16x32_bf16 v[88:91], a[200:203], a[244:247], v[88:91]\n" \
"ds_read_b64_tr_b16 a[230:231], v33 offset:4608\n" \
"v_accvgpr_read_b32 v209, a37\n" \
"v_mfma_f32_16x16x32_bf16 v[92:95], a[136:139], a[184:187], v[92:95]\n" \
"s_addk_i32 s84, 0x7c00\n" \
"v_mfma_f32_16x16x32_bf16 v[92:95], a[200:203], a[248:251], v[92:95]\n" \
"buffer_load_dwordx4 v27, s[68:71], s84 offen offset:3072 lds\n" \
"v_mfma_f32_16x16x32_bf16 v[96:99], a[136:139], a[188:191], v[96:99]\n" \
"ds_read_b64_tr_b16 a[172:173], v35 offset:0\n" \
"v_accvgpr_read_b32 v210, a38\n" \
"v_mfma_f32_16x16x32_bf16 v[96:99], a[200:203], a[252:255], v[96:99]\n" \
"ds_read_b128 a[136:139], v28 offset:4096\n" \
"v_accvgpr_read_b32 v211, a39\n" \
"v_mfma_f32_16x16x32_bf16 v[112:115], a[140:143], a[188:191], v[112:115]\n" \
"ds_read_b128 a[200:203], v29 offset:4096\n" \
"v_accvgpr_read_b32 v180, a52\n" \
"v_mfma_f32_16x16x32_bf16 v[112:115], a[204:207], a[252:255], v[112:115]\n" \
"s_add_u32 m0, s30, 0x9000\n" \
"s_mov_b32 s84, 0x9080\n" \
"v_mfma_f32_16x16x32_bf16 v[108:111], a[140:143], a[184:187], v[108:111]\n" \
"buffer_load_dwordx4 v24, s[76:79], s84 offen offset:0 lds\n" \
"v_mfma_f32_16x16x32_bf16 v[108:111], a[204:207], a[248:251], v[108:111]\n" \
"ds_read_b64_tr_b16 a[174:175], v35 offset:512\n" \
"v_accvgpr_read_b32 v181, a53\n" \
"v_mfma_f32_16x16x32_bf16 v[104:107], a[140:143], a[180:183], v[104:107]\n" \
"s_addk_i32 s84, 0x2000\n" \
"s_nop 0\n" \
"v_mfma_f32_16x16x32_bf16 v[104:107], a[204:207], a[244:247], v[104:107]\n" \
"buffer_load_dwordx4 v25, s[76:79], s84 offen offset:1024 lds\n" \
"v_mfma_f32_16x16x32_bf16 v[100:103], a[140:143], a[176:179], v[100:103]\n" \
"ds_read_b64_tr_b16 a[236:237], v35 offset:4096\n" \
"v_accvgpr_read_b32 v182, a54\n" \
"v_mfma_f32_16x16x32_bf16 v[100:103], a[204:207], a[240:243], v[100:103]\n" \
"ds_read_b128 a[140:143], v28 offset:6144\n" \
"v_accvgpr_read_b32 v183, a55\n" \
"v_mfma_f32_16x16x32_bf16 v[52:55], a[128:131], a[176:179], v[52:55]\n" \
"ds_read_b128 a[204:207], v29 offset:6144\n" \
"v_cvt_pk_bf16_f32 v208, v208, v209\n" \
"v_mfma_f32_16x16x32_bf16 v[52:55], a[192:195], a[240:243], v[52:55]\n" \
"ds_read_b64_tr_b16 a[238:239], v35 offset:4608\n" \
"v_cvt_pk_bf16_f32 v209, v210, v211\n" \
"v_mfma_f32_16x16x32_bf16 v[56:59], a[128:131], a[180:183], v[56:59]\n" \
"ds_read_b64_tr_b16 a[168:169], v34 offset:0\n" \
"v_cvt_pk_bf16_f32 v210, v180, v181\n" \
"v_mfma_f32_16x16x32_bf16 v[56:59], a[192:195], a[244:247], v[56:59]\n" \
"ds_read_b64_tr_b16 a[170:171], v34 offset:512\n" \
"v_cvt_pk_bf16_f32 v211, v182, v183\n" \
"v_mfma_f32_16x16x32_bf16 v[60:63], a[128:131], a[184:187], v[60:63]\n" \
"ds_read_b64_tr_b16 a[232:233], v34 offset:4096\n" \
"v_accvgpr_read_b32 v212, a32\n" \
"v_mfma_f32_16x16x32_bf16 v[60:63], a[192:195], a[248:251], v[60:63]\n" \
"ds_read_b64_tr_b16 a[234:235], v34 offset:4608\n" \
"v_accvgpr_read_b32 v213, a33\n" \
"v_mfma_f32_16x16x32_bf16 v[64:67], a[128:131], a[188:191], v[64:67]\n" \
"v_accvgpr_read_b32 v214, a34\n" \
"v_accvgpr_read_b32 v215, a35\n" \
"v_mfma_f32_16x16x32_bf16 v[64:67], a[192:195], a[252:255], v[64:67]\n" \
"ds_read_b128 a[128:131], v28 offset:0\n" \
"v_accvgpr_read_b32 v180, a48\n" \
"v_mfma_f32_16x16x32_bf16 v[80:83], a[132:135], a[188:191], v[80:83]\n" \
"ds_read_b128 a[192:195], v29 offset:0\n" \
"v_accvgpr_read_b32 v181, a49\n" \
"v_mfma_f32_16x16x32_bf16 v[80:83], a[196:199], a[252:255], v[80:83]\n" \
"v_accvgpr_read_b32 v182, a50\n" \
"v_accvgpr_read_b32 v183, a51\n" \
"v_mfma_f32_16x16x32_bf16 v[76:79], a[132:135], a[184:187], v[76:79]\n" \
"v_cvt_pk_bf16_f32 v212, v212, v213\n" \
"v_cvt_pk_bf16_f32 v213, v214, v215\n" \
"v_mfma_f32_16x16x32_bf16 v[76:79], a[196:199], a[248:251], v[76:79]\n" \
"v_cvt_pk_bf16_f32 v214, v180, v181\n" \
"v_cvt_pk_bf16_f32 v215, v182, v183\n" \
"v_mfma_f32_16x16x32_bf16 v[72:75], a[132:135], a[180:183], v[72:75]\n" \
"v_accvgpr_read_b32 v216, a68\n" \
"v_accvgpr_read_b32 v217, a69\n" \
"v_mfma_f32_16x16x32_bf16 v[72:75], a[196:199], a[244:247], v[72:75]\n" \
"v_accvgpr_read_b32 v218, a70\n" \
"v_accvgpr_read_b32 v219, a71\n" \
"v_mfma_f32_16x16x32_bf16 v[68:71], a[132:135], a[176:179], v[68:71]\n" \
"v_accvgpr_read_b32 v180, a84\n" \
"v_accvgpr_read_b32 v181, a85\n" \
"v_mfma_f32_16x16x32_bf16 v[68:71], a[196:199], a[240:243], v[68:71]\n" \
"ds_read_b128 a[132:135], v28 offset:2048\n" \
"ds_read_b128 a[196:199], v29 offset:2048\n" \
"s_waitcnt lgkmcnt(15)\n" \
"v_mfma_f32_16x16x32_bf16 a[0:3], a[160:163], a[144:147], 0\n" \
"s_addk_i32 s84, 0x2000\n" \
"v_accvgpr_read_b32 v182, a86\n" \
"v_mfma_f32_16x16x32_bf16 a[0:3], a[224:227], a[208:211], a[0:3]\n" \
"buffer_load_dwordx4 v24, s[76:79], s84 offen offset:2048 lds\n" \
"v_accvgpr_read_b32 v183, a87\n" \
"v_mfma_f32_16x16x32_bf16 a[4:7], a[164:167], a[144:147], 0\n" \
"ds_read_b64_tr_b16 a[176:177], v36 offset:0\n" \
"v_cvt_pk_bf16_f32 v216, v216, v217\n" \
"v_mfma_f32_16x16x32_bf16 a[4:7], a[228:231], a[208:211], a[4:7]\n" \
"s_addk_i32 s84, 0x2000\n" \
"v_cvt_pk_bf16_f32 v217, v218, v219\n" \
"s_waitcnt lgkmcnt(13)\n" \
"v_mfma_f32_16x16x32_bf16 a[12:15], a[172:175], a[144:147], 0\n" \
"buffer_load_dwordx4 v25, s[76:79], s84 offen offset:3072 lds\n" \
"v_cvt_pk_bf16_f32 v218, v180, v181\n" \
"s_waitcnt lgkmcnt(9)\n" \
"v_mfma_f32_16x16x32_bf16 a[12:15], a[236:239], a[208:211], a[12:15]\n" \
"ds_read_b64_tr_b16 a[178:179], v36 offset:1024\n" \
"v_cvt_pk_bf16_f32 v219, v182, v183\n" \
"s_waitcnt lgkmcnt(8)\n" \
"v_mfma_f32_16x16x32_bf16 a[8:11], a[144:147], a[168:171], 0\n" \
"ds_read_b64_tr_b16 a[240:241], v36 offset:8192\n" \
"v_accvgpr_read_b32 v220, a64\n" \
"s_waitcnt lgkmcnt(7)\n" \
"s_nop 0\n" \
"v_mfma_f32_16x16x32_bf16 a[8:11], a[208:211], a[232:235], a[8:11]\n" \
"ds_read_b64_tr_b16 a[242:243], v36 offset:9216\n" \
"v_accvgpr_read_b32 v221, a65\n" \
"v_mfma_f32_16x16x32_bf16 a[24:27], a[148:151], a[168:171], 0\n" \
"ds_read_b64_tr_b16 a[180:181], v37 offset:0\n" \
"v_accvgpr_read_b32 v222, a66\n" \
"v_mfma_f32_16x16x32_bf16 a[24:27], a[212:215], a[232:235], a[24:27]\n" \
"ds_read_b64_tr_b16 a[182:183], v37 offset:1024\n" \
"v_accvgpr_read_b32 v223, a67\n" \
"v_mfma_f32_16x16x32_bf16 a[28:31], a[172:175], a[148:151], 0\n" \
"ds_read_b64_tr_b16 a[244:245], v37 offset:8192\n" \
"v_accvgpr_read_b32 v180, a80\n" \
"v_mfma_f32_16x16x32_bf16 a[28:31], a[236:239], a[212:215], a[28:31]\n" \
"s_add_u32 s89, s41, 0x2000\n" \
"buffer_store_dwordx4 v[200:203], v20, s[80:83], s89 offen offset:3072 sc1\n" \
"v_mfma_f32_16x16x32_bf16 a[20:23], a[164:167], a[148:151], 0\n" \
"ds_read_b64_tr_b16 a[246:247], v37 offset:9216\n" \
"v_accvgpr_read_b32 v181, a81\n" \
"v_mfma_f32_16x16x32_bf16 a[20:23], a[228:231], a[212:215], a[20:23]\n" \
"ds_read_b64_tr_b16 a[184:185], v38 offset:0\n" \
"v_accvgpr_read_b32 v182, a82\n" \
"v_mfma_f32_16x16x32_bf16 a[16:19], a[160:163], a[148:151], 0\n" \
"ds_read_b64_tr_b16 a[186:187], v38 offset:1024\n" \
"v_accvgpr_read_b32 v183, a83\n" \
"v_mfma_f32_16x16x32_bf16 a[16:19], a[224:227], a[212:215], a[16:19]\n" \
"v_cvt_pk_bf16_f32 v220, v220, v221\n" \
"v_cvt_pk_bf16_f32 v221, v222, v223\n" \
"v_cvt_pk_bf16_f32 v222, v180, v181\n" \
"v_mfma_f32_16x16x32_bf16 a[32:35], a[160:163], a[152:155], 0\n" \
"s_add_u32 s88, s41, 0x2000\n" \
".fill (L_pb + 61152 - .) / 4, 4, 0xBF800000\n" \
"L_pp15:\n" \
"s_cbranch_execz L_pret\n" \
"s_nop 0\n" \
"buffer_store_dwordx4 v[204:207], v20, s[80:83], s88 offen offset:2048 sc1\n" \
"v_mfma_f32_16x16x32_bf16 a[32:35], a[224:227], a[216:219], a[32:35]\n" \
"ds_read_b64_tr_b16 a[248:249], v38 offset:8192\n" \
"v_cvt_pk_bf16_f32 v223, v182, v183\n" \
"v_mfma_f32_16x16x32_bf16 a[36:39], a[164:167], a[152:155], 0\n" \
"ds_read_b64_tr_b16 a[250:251], v38 offset:9216\n" \
"v_cvt_pk_bf16_f32 v224, v128, v129\n" \
"v_mfma_f32_16x16x32_bf16 a[36:39], a[228:231], a[216:219], a[36:39]\n" \
"ds_read_b64_tr_b16 a[188:189], v39 offset:0\n" \
"v_cvt_pk_bf16_f32 v225, v130, v131\n" \
"v_mfma_f32_16x16x32_bf16 a[44:47], a[172:175], a[152:155], 0\n" \
"v_cvt_pk_bf16_f32 v226, v144, v145\n" \
"v_cvt_pk_bf16_f32 v227, v146, v147\n" \
"v_cvt_pk_bf16_f32 v228, v124, v125\n" \
"v_mfma_f32_16x16x32_bf16 a[44:47], a[236:239], a[216:219], a[44:47]\n" \
"ds_read_b64_tr_b16 a[190:191], v39 offset:1024\n" \
"v_cvt_pk_bf16_f32 v229, v126, v127\n" \
"v_mfma_f32_16x16x32_bf16 a[40:43], a[152:155], a[168:171], 0\n" \
"s_add_u32 s89, s41, 0x2000\n" \
"buffer_store_dwordx4 v[208:211], v20, s[80:83], s89 offen offset:1024 sc1\n" \
"v_mfma_f32_16x16x32_bf16 a[40:43], a[216:219], a[232:235], a[40:43]\n" \
"ds_read_b64_tr_b16 a[252:253], v39 offset:8192\n" \
"v_cvt_pk_bf16_f32 v230, v140, v141\n" \
"v_mfma_f32_16x16x32_bf16 a[56:59], a[156:159], a[168:171], 0\n" \
"ds_read_b64_tr_b16 a[254:255], v39 offset:9216\n" \
"v_cvt_pk_bf16_f32 v231, v142, v143\n" \
"v_mfma_f32_16x16x32_bf16 a[56:59], a[220:223], a[232:235], a[56:59]\n" \
"v_cvt_pk_bf16_f32 v232, v120, v121\n" \
"v_cvt_pk_bf16_f32 v233, v122, v123\n" \
"v_cvt_pk_bf16_f32 v234, v136, v137\n" \
"v_mfma_f32_16x16x32_bf16 a[60:63], a[172:175], a[156:159], 0\n" \
"v_cvt_pk_bf16_f32 v235, v138, v139\n" \
"v_cvt_pk_bf16_f32 v236, v116, v117\n" \
"v_cvt_pk_bf16_f32 v237, v118, v119\n" \
"v_mfma_f32_16x16x32_bf16 a[60:63], a[236:239], a[220:223], a[60:63]\n" \
"s_add_u32 s88, s41, 0x2000\n" \
"buffer_store_dwordx4 v[212:215], v20, s[80:83], s88 offen offset:0 sc1\n" \
"v_mfma_f32_16x16x32_bf16 a[52:55], a[164:167], a[156:159], 0\n" \
"v_cvt_pk_bf16_f32 v238, v132, v133\n" \
"v_cvt_pk_bf16_f32 v239, v134, v135\n" \
"v_cvt_pk_bf16_f32 v240, v160, v161\n" \
"v_mfma_f32_16x16x32_bf16 a[52:55], a[228:231], a[220:223], a[52:55]\n" \
"v_cvt_pk_bf16_f32 v241, v162, v163\n" \
"v_cvt_pk_bf16_f32 v242, v176, v177\n" \
"v_cvt_pk_bf16_f32 v243, v178, v179\n" \
"v_mfma_f32_16x16x32_bf16 a[48:51], a[160:163], a[156:159], 0\n" \
"v_cvt_pk_bf16_f32 v244, v156, v157\n" \
"v_cvt_pk_bf16_f32 v245, v158, v159\n" \
"v_cvt_pk_bf16_f32 v246, v172, v173\n" \
"v_mfma_f32_16x16x32_bf16 a[48:51], a[224:227], a[220:223], a[48:51]\n" \
"v_cvt_pk_bf16_f32 v247, v174, v175\n" \
"v_cvt_pk_bf16_f32 v184, v152, v153\n" \
"v_cvt_pk_bf16_f32 v185, v154, v155\n" \
"v_mfma_f32_16x16x32_bf16 a[64:67], a[164:167], a[140:143], 0\n" \
"v_cvt_pk_bf16_f32 v186, v168, v169\n" \
"v_cvt_pk_bf16_f32 v187, v170, v171\n" \
"v_cvt_pk_bf16_f32 v188, v148, v149\n" \
"v_mfma_f32_16x16x32_bf16 a[64:67], a[228:231], a[204:207], a[64:67]\n" \
"v_cvt_pk_bf16_f32 v189, v150, v151\n" \
"v_cvt_pk_bf16_f32 v190, v164, v165\n" \
"v_cvt_pk_bf16_f32 v191, v166, v167\n" \
"v_mfma_f32_16x16x32_bf16 a[80:83], a[172:175], a[140:143], 0\n" \
"v_cvt_pk_bf16_f32 v192, v96, v97\n" \
"v_cvt_pk_bf16_f32 v193, v98, v99\n" \
"v_cvt_pk_bf16_f32 v194, v112, v113\n" \
"v_mfma_f32_16x16x32_bf16 a[80:83], a[236:239], a[204:207], a[80:83]\n" \
"v_cvt_pk_bf16_f32 v195, v114, v115\n" \
"v_cvt_pk_bf16_f32 v196, v92, v93\n" \
"v_cvt_pk_bf16_f32 v197, v94, v95\n" \
"v_mfma_f32_16x16x32_bf16 a[68:71], a[140:143], a[168:171], 0\n" \
"v_cvt_pk_bf16_f32 v198, v108, v109\n" \
"v_cvt_pk_bf16_f32 v199, v110, v111\n" \
"v_cvt_pk_bf16_f32 v252, v88, v89\n" \
"v_mfma_f32_16x16x32_bf16 a[68:71], a[204:207], a[232:235], a[68:71]\n" \
"v_cvt_pk_bf16_f32 v253, v90, v91\n" \
"v_cvt_pk_bf16_f32 v254, v104, v105\n" \
"v_cvt_pk_bf16_f32 v255, v106, v107\n" \
"s_waitcnt vmcnt(4) lgkmcnt(0)\n" \
"s_barrier\n" \
"v_mfma_f32_16x16x32_bf16 v[116:119], a[176:179], a[144:147], 0\n" \
"s_add_u32 m0, s40, 0x0\n" \
"s_nop 0\n" \
"s_add_u32 s85, s39, 0x4000\n" \
"v_accvgpr_write_b32 a84, v252\n" \
"v_mfma_f32_16x16x32_bf16 v[116:119], a[240:243], a[208:211], v[116:119]\n" \
"buffer_load_dwordx4 v20, s[80:83], s85 offen sc1 lds\n" \
"v_accvgpr_write_b32 a85, v253\n" \
"v_mfma_f32_16x16x32_bf16 v[120:123], a[180:183], a[144:147], 0\n" \
"ds_read_b64_tr_b16 a[160:161], v32 offset:32768\n" \
"v_accvgpr_write_b32 a86, v254\n" \
"v_mfma_f32_16x16x32_bf16 v[120:123], a[244:247], a[208:211], v[120:123]\n" \
"ds_read_b64_tr_b16 a[162:163], v32 offset:33280\n" \
"v_accvgpr_write_b32 a87, v255\n" \
"v_mfma_f32_16x16x32_bf16 v[124:127], a[184:187], a[144:147], 0\n" \
"s_add_u32 m0, s40, 0x1000\n" \
"s_add_u32 s85, s39, 0x5000\n" \
"v_cvt_pk_bf16_f32 v252, v84, v85\n" \
"v_mfma_f32_16x16x32_bf16 v[124:127], a[248:251], a[208:211], v[124:127]\n" \
"buffer_load_dwordx4 v20, s[80:83], s85 offen sc1 lds\n" \
"v_cvt_pk_bf16_f32 v253, v86, v87\n" \
"v_mfma_f32_16x16x32_bf16 v[128:131], a[188:191], a[144:147], 0\n" \
"ds_read_b64_tr_b16 a[224:225], v32 offset:36864\n" \
"v_cvt_pk_bf16_f32 v254, v100, v101\n" \
"v_mfma_f32_16x16x32_bf16 v[128:131], a[252:255], a[208:211], v[128:131]\n" \
"ds_read_b128 a[144:147], v28 offset:40960\n" \
"v_cvt_pk_bf16_f32 v255, v102, v103\n" \
"v_mfma_f32_16x16x32_bf16 v[144:147], a[188:191], a[148:151], 0\n" \
"ds_read_b128 a[208:211], v29 offset:40960\n" \
"v_cvt_pk_bf16_f32 v200, v64, v65\n" \
"v_mfma_f32_16x16x32_bf16 v[144:147], a[252:255], a[212:215], v[144:147]\n" \
"s_add_u32 m0, s40, 0x3000\n" \
"s_movk_i32 s84, 0x100\n" \
"s_nop 0\n" \
"v_cvt_pk_bf16_f32 v201, v66, v67\n" \
"v_mfma_f32_16x16x32_bf16 v[140:143], a[184:187], a[148:151], 0\n" \
"buffer_load_dwordx4 v21, s[76:79], s84 offen offset:0 lds\n" \
"v_cvt_pk_bf16_f32 v202, v80, v81\n" \
"v_mfma_f32_16x16x32_bf16 v[140:143], a[248:251], a[212:215], v[140:143]\n" \
"ds_read_b64_tr_b16 a[226:227], v32 offset:37376\n" \
"v_cvt_pk_bf16_f32 v203, v82, v83\n" \
"v_mfma_f32_16x16x32_bf16 v[136:139], a[180:183], a[148:151], 0\n" \
"v_cvt_pk_bf16_f32 v204, v60, v61\n" \
"v_cvt_pk_bf16_f32 v205, v62, v63\n" \
"v_cvt_pk_bf16_f32 v206, v76, v77\n" \
"v_mfma_f32_16x16x32_bf16 v[136:139], a[244:247], a[212:215], v[136:139]\n" \
"ds_read_b64_tr_b16 a[164:165], v33 offset:32768\n" \
"v_cvt_pk_bf16_f32 v207, v78, v79\n" \
"v_mfma_f32_16x16x32_bf16 v[132:135], a[176:179], a[148:151], 0\n" \
"v_cvt_pk_bf16_f32 v208, v56, v57\n" \
"v_cvt_pk_bf16_f32 v209, v58, v59\n" \
"v_cvt_pk_bf16_f32 v210, v72, v73\n" \
"v_mfma_f32_16x16x32_bf16 v[132:135], a[240:243], a[212:215], v[132:135]\n" \
"ds_read_b128 a[148:151], v28 offset:43008\n" \
"v_cvt_pk_bf16_f32 v211, v74, v75\n" \
"v_mfma_f32_16x16x32_bf16 v[148:151], a[176:179], a[152:155], 0\n" \
"ds_read_b128 a[212:215], v29 offset:43008\n" \
"v_cvt_pk_bf16_f32 v212, v52, v53\n" \
"v_mfma_f32_16x16x32_bf16 v[148:151], a[240:243], a[216:219], v[148:151]\n" \
"s_add_u32 m0, s31, 0x0\n" \
"s_mov_b32 s84, 0x100000\n" \
"v_cvt_pk_bf16_f32 v213, v54, v55\n" \
"v_mfma_f32_16x16x32_bf16 v[152:155], a[180:183], a[152:155], 0\n" \
"buffer_load_dwordx4 v26, s[68:71], s84 offen offset:0 lds\n" \
"v_cvt_pk_bf16_f32 v214, v68, v69\n" \
"v_mfma_f32_16x16x32_bf16 v[152:155], a[244:247], a[216:219], v[152:155]\n" \
"s_addk_i32 s84, 0x7c00\n" \
"v_cvt_pk_bf16_f32 v215, v70, v71\n" \
"v_mfma_f32_16x16x32_bf16 v[156:159], a[184:187], a[152:155], 0\n" \
"buffer_load_dwordx4 v26, s[68:71], s84 offen offset:1024 lds\n" \
"v_mfma_f32_16x16x32_bf16 v[156:159], a[248:251], a[216:219], v[156:159]\n" \
"ds_read_b64_tr_b16 a[166:167], v33 offset:33280\n" \
"v_mfma_f32_16x16x32_bf16 v[160:163], a[188:191], a[152:155], 0\n" \
"ds_read_b64_tr_b16 a[228:229], v33 offset:36864\n" \
"v_mfma_f32_16x16x32_bf16 v[160:163], a[252:255], a[216:219], v[160:163]\n" \
"ds_read_b128 a[152:155], v28 offset:45056\n" \
"v_mfma_f32_16x16x32_bf16 v[176:179], a[188:191], a[156:159], 0\n" \
"ds_read_b128 a[216:219], v29 offset:45056\n" \
"v_mfma_f32_16x16x32_bf16 v[176:179], a[252:255], a[220:223], v[176:179]\n" \
"s_addk_i32 s84, 0x7c00\n" \
"v_mfma_f32_16x16x32_bf16 v[172:175], a[184:187], a[156:159], 0\n" \
"buffer_load_dwordx4 v27, s[68:71], s84 offen offset:2048 lds\n" \
"v_mfma_f32_16x16x32_bf16 v[172:175], a[248:251], a[220:223], v[172:175]\n" \
"ds_read_b64_tr_b16 a[230:231], v33 offset:37376\n" \
"v_mfma_f32_16x16x32_bf16 v[168:171], a[180:183], a[156:159], 0\n" \
"s_addk_i32 s84, 0x7c00\n" \
"v_mfma_f32_16x16x32_bf16 v[168:171], a[244:247], a[220:223], v[168:171]\n" \
"buffer_load_dwordx4 v27, s[68:71], s84 offen offset:3072 lds\n" \
"v_mfma_f32_16x16x32_bf16 v[164:167], a[176:179], a[156:159], 0\n" \
"ds_read_b64_tr_b16 a[172:173], v35 offset:32768\n" \
"v_mfma_f32_16x16x32_bf16 v[164:167], a[240:243], a[220:223], v[164:167]\n" \
"ds_read_b128 a[156:159], v28 offset:47104\n" \
"v_mfma_f32_16x16x32_bf16 v[84:87], a[176:179], a[136:139], 0\n" \
"ds_read_b128 a[220:223], v29 offset:47104\n" \
"v_mfma_f32_16x16x32_bf16 v[84:87], a[240:243], a[200:203], v[84:87]\n" \
"s_add_u32 s89, s41, 0x3000\n" \
"buffer_store_dwordx4 v[192:195], v20, s[80:83], s89 offen offset:3072 sc1\n" \
"v_mfma_f32_16x16x32_bf16 v[88:91], a[180:183], a[136:139], 0\n" \
"ds_read_b64_tr_b16 a[174:175], v35 offset:33280\n" \
"v_mfma_f32_16x16x32_bf16 v[88:91], a[244:247], a[200:203], v[88:91]\n" \
"ds_read_b64_tr_b16 a[236:237], v35 offset:36864\n" \
"v_mfma_f32_16x16x32_bf16 v[92:95], a[184:187], a[136:139], 0\n" \
"ds_read_b64_tr_b16 a[238:239], v35 offset:37376\n" \
"v_mfma_f32_16x16x32_bf16 v[92:95], a[248:251], a[200:203], v[92:95]\n" \
"s_add_u32 m0, s30, 0x0\n" \
"s_movk_i32 s84, 0x180\n" \
"v_mfma_f32_16x16x32_bf16 v[96:99], a[188:191], a[136:139], 0\n" \
"buffer_load_dwordx4 v24, s[76:79], s84 offen offset:0 lds\n" \
"v_mfma_f32_16x16x32_bf16 v[96:99], a[252:255], a[200:203], v[96:99]\n" \
"ds_read_b128 a[136:139], v28 offset:36864\n" \
"v_mfma_f32_16x16x32_bf16 v[112:115], a[188:191], a[140:143], 0\n" \
"ds_read_b128 a[200:203], v29 offset:36864\n" \
"v_mfma_f32_16x16x32_bf16 v[112:115], a[252:255], a[204:207], v[112:115]\n" \
"ds_read_b64_tr_b16 a[168:169], v34 offset:32768\n" \
"v_mfma_f32_16x16x32_bf16 v[108:111], a[184:187], a[140:143], 0\n" \
"s_movk_i32 s84, 0x2180\n" \
"s_nop 0\n" \
"v_mfma_f32_16x16x32_bf16 v[108:111], a[248:251], a[204:207], v[108:111]\n" \
"buffer_load_dwordx4 v25, s[76:79], s84 offen offset:1024 lds\n" \
"v_mfma_f32_16x16x32_bf16 v[104:107], a[180:183], a[140:143], 0\n" \
"ds_read_b64_tr_b16 a[170:171], v34 offset:33280\n" \
"v_mfma_f32_16x16x32_bf16 v[104:107], a[244:247], a[204:207], v[104:107]\n" \
"ds_read_b64_tr_b16 a[232:233], v34 offset:36864\n" \
"v_mfma_f32_16x16x32_bf16 v[100:103], a[176:179], a[140:143], 0\n" \
"v_mfma_f32_16x16x32_bf16 v[100:103], a[240:243], a[204:207], v[100:103]\n" \
"ds_read_b128 a[140:143], v28 offset:38912\n" \
"v_mfma_f32_16x16x32_bf16 v[52:55], a[176:179], a[128:131], 0\n" \
"ds_read_b128 a[204:207], v29 offset:38912\n" \
"v_mfma_f32_16x16x32_bf16 v[52:55], a[240:243], a[192:195], v[52:55]\n" \
"s_movk_i32 s84, 0x4180\n" \
"v_mfma_f32_16x16x32_bf16 v[56:59], a[180:183], a[128:131], 0\n" \
"buffer_load_dwordx4 v24, s[76:79], s84 offen offset:2048 lds\n" \
"v_mfma_f32_16x16x32_bf16 v[56:59], a[244:247], a[192:195], v[56:59]\n" \
"s_movk_i32 s84, 0x6180\n" \
"v_mfma_f32_16x16x32_bf16 v[60:63], a[184:187], a[128:131], 0\n" \
"buffer_load_dwordx4 v25, s[76:79], s84 offen offset:3072 lds\n" \
"v_mfma_f32_16x16x32_bf16 v[60:63], a[248:251], a[192:195], v[60:63]\n" \
"ds_read_b64_tr_b16 a[234:235], v34 offset:37376\n" \
"v_mfma_f32_16x16x32_bf16 v[64:67], a[188:191], a[128:131], 0\n" \
"v_mfma_f32_16x16x32_bf16 v[64:67], a[252:255], a[192:195], v[64:67]\n" \
"ds_read_b128 a[128:131], v28 offset:32768\n" \
"v_mfma_f32_16x16x32_bf16 v[80:83], a[188:191], a[132:135], 0\n" \
"ds_read_b128 a[192:195], v29 offset:32768\n" \
"v_mfma_f32_16x16x32_bf16 v[80:83], a[252:255], a[196:199], v[80:83]\n" \
"v_mfma_f32_16x16x32_bf16 v[76:79], a[184:187], a[132:135], 0\n" \
"v_mfma_f32_16x16x32_bf16 v[76:79], a[248:251], a[196:199], v[76:79]\n" \
"v_mfma_f32_16x16x32_bf16 v[72:75], a[180:183], a[132:135], 0\n" \
"v_mfma_f32_16x16x32_bf16 v[72:75], a[244:247], a[196:199], v[72:75]\n" \
"v_mfma_f32_16x16x32_bf16 v[68:71], a[176:179], a[132:135], 0\n" \
"v_mfma_f32_16x16x32_bf16 v[68:71], a[240:243], a[196:199], v[68:71]\n" \
"ds_read_b128 a[132:135], v28 offset:34816\n" \
"ds_read_b128 a[196:199], v29 offset:34816\n" \
"s_waitcnt lgkmcnt(15)\n" \
"v_mfma_f32_16x16x32_bf16 a[0:3], a[160:163], a[144:147], a[0:3]\n" \
"s_add_u32 m0, s30, 0x1000\n" \
"s_addk_i32 s84, 0x3000\n" \
"v_mfma_f32_16x16x32_bf16 a[0:3], a[224:227], a[208:211], a[0:3]\n" \
"buffer_load_dwordx4 v24, s[76:79], s84 offen offset:0 lds\n" \
"v_mfma_f32_16x16x32_bf16 a[4:7], a[164:167], a[144:147], a[4:7]\n" \
"ds_read_b64_tr_b16 a[176:177], v36 offset:32768\n" \
"v_mov_b32_e64 v11, 0\n" \
"v_mfma_f32_16x16x32_bf16 a[4:7], a[228:231], a[208:211], a[4:7]\n" \
"ds_read_b64_tr_b16 a[178:179], v36 offset:33792\n" \
"s_and_b32 s6, s2, 7\n" \
"s_waitcnt lgkmcnt(15)\n" \
"v_mfma_f32_16x16x32_bf16 a[12:15], a[172:175], a[144:147], a[12:15]\n" \
"ds_read_b64_tr_b16 a[240:241], v36 offset:40960\n" \
"s_lshl_b32 s7, s3, 1\n" \
"s_waitcnt lgkmcnt(15)\n" \
"v_mfma_f32_16x16x32_bf16 a[12:15], a[236:239], a[208:211], a[12:15]\n" \
"s_add_u32 s88, s41, 0x3000\n" \
"buffer_store_dwordx4 v[196:199], v20, s[80:83], s88 offen offset:2048 sc1\n" \
"s_waitcnt lgkmcnt(11)\n" \
"v_mfma_f32_16x16x32_bf16 a[8:11], a[144:147], a[168:171], a[8:11]\n" \
"ds_read_b64_tr_b16 a[242:243], v36 offset:41984\n" \
"s_lshr_b32 s4, s2, 5\n" \
"s_waitcnt lgkmcnt(8)\n" \
"v_mfma_f32_16x16x32_bf16 a[8:11], a[208:211], a[232:235], a[8:11]\n" \
"ds_read_b64_tr_b16 a[180:181], v37 offset:32768\n" \
"s_lshl_b32 s5, s6, 2\n" \
"v_mfma_f32_16x16x32_bf16 a[24:27], a[148:151], a[168:171], a[24:27]\n" \
"ds_read_b64_tr_b16 a[182:183], v37 offset:33792\n" \
"s_bfe_u32 s8, s2, 0x20003\n" \
"v_mfma_f32_16x16x32_bf16 a[24:27], a[212:215], a[232:235], a[24:27]\n" \
"ds_read_b64_tr_b16 a[244:245], v37 offset:40960\n" \
"s_and_b32 s7, s7, 4\n" \
"v_mfma_f32_16x16x32_bf16 a[28:31], a[172:175], a[148:151], a[28:31]\n" \
"s_and_b32 s9, s3, 1\n" \
"s_or_b32 s13, s5, s8\n" \
"v_mfma_f32_16x16x32_bf16 a[28:31], a[236:239], a[212:215], a[28:31]\n" \
"s_addk_i32 s84, 0x2000\n" \
"v_mfma_f32_16x16x32_bf16 a[20:23], a[164:167], a[148:151], a[20:23]\n" \
"buffer_load_dwordx4 v25, s[76:79], s84 offen offset:1024 lds\n" \
"v_mfma_f32_16x16x32_bf16 a[20:23], a[228:231], a[212:215], a[20:23]\n" \
"ds_read_b64_tr_b16 a[246:247], v37 offset:41984\n" \
"s_or_b32 s9, s7, s9\n" \
"v_mfma_f32_16x16x32_bf16 a[16:19], a[160:163], a[148:151], a[16:19]\n" \
"ds_read_b64_tr_b16 a[184:185], v38 offset:32768\n" \
"s_mul_i32 s7, s4, 0xa00\n" \
"v_mfma_f32_16x16x32_bf16 a[16:19], a[224:227], a[212:215], a[16:19]\n" \
"ds_read_b64_tr_b16 a[186:187], v38 offset:33792\n" \
"s_add_i32 s7, s7, 0x5bc1000\n" \
"v_mfma_f32_16x16x32_bf16 a[32:35], a[160:163], a[152:155], a[32:35]\n" \
"s_addk_i32 s84, 0x2000\n" \
"v_mfma_f32_16x16x32_bf16 a[32:35], a[224:227], a[216:219], a[32:35]\n" \
"buffer_load_dwordx4 v24, s[76:79], s84 offen offset:2048 lds\n" \
"v_mfma_f32_16x16x32_bf16 a[36:39], a[164:167], a[152:155], a[36:39]\n" \
"ds_read_b64_tr_b16 a[248:249], v38 offset:40960\n" \
"s_lshl_b32 s10, s13, 2\n" \
"v_mfma_f32_16x16x32_bf16 a[36:39], a[228:231], a[216:219], a[36:39]\n" \
"ds_read_b64_tr_b16 a[250:251], v38 offset:41984\n" \
"s_or_b32 s10, s10, s7\n" \
"v_mfma_f32_16x16x32_bf16 a[44:47], a[172:175], a[152:155], a[44:47]\n" \
"ds_read_b64_tr_b16 a[188:189], v39 offset:32768\n" \
"v_writelane_b32 v11, s10, 0\n" \
"v_mfma_f32_16x16x32_bf16 a[44:47], a[236:239], a[216:219], a[44:47]\n" \
"ds_read_b64_tr_b16 a[190:191], v39 offset:33792\n" \
"s_lshl_b32 s14, s4, 3\n" \
"v_mfma_f32_16x16x32_bf16 a[40:43], a[152:155], a[168:171], a[40:43]\n" \
"s_add_u32 s89, s41, 0x3000\n" \
"buffer_store_dwordx4 a[84:87], v20, s[80:83], s89 offen offset:1024 sc1\n" \
"v_mfma_f32_16x16x32_bf16 a[40:43], a[216:219], a[232:235], a[40:43]\n" \
"ds_read_b64_tr_b16 a[252:253], v39 offset:40960\n" \
"v_writelane_b32 v11, s7, 1\n" \
"v_mfma_f32_16x16x32_bf16 a[56:59], a[156:159], a[168:171], a[56:59]\n" \
"ds_read_b64_tr_b16 a[254:255], v39 offset:41984\n" \
"s_or_b32 s7, s14, s6\n" \
"v_mfma_f32_16x16x32_bf16 a[56:59], a[220:223], a[232:235], a[56:59]\n" \
"s_lshl_b32 s7, s7, 10\n" \
"s_lshl_b32 s10, s3, 8\n" \
"v_mfma_f32_16x16x32_bf16 a[60:63], a[172:175], a[156:159], a[60:63]\n" \
"s_or_b32 s7, s7, s10\n" \
"s_or_b32 s10, s7, 0x5c00200\n" \
"v_mfma_f32_16x16x32_bf16 a[60:63], a[236:239], a[220:223], a[60:63]\n" \
"s_addk_i32 s84, 0x2000\n" \
"v_mfma_f32_16x16x32_bf16 a[52:55], a[164:167], a[156:159], a[52:55]\n" \
"buffer_load_dwordx4 v25, s[76:79], s84 offen offset:3072 lds\n" \
"v_mfma_f32_16x16x32_bf16 a[52:55], a[228:231], a[220:223], a[52:55]\n" \
"v_writelane_b32 v11, s10, 2\n" \
"s_lshl_b32 s10, s8, 2\n" \
"v_mfma_f32_16x16x32_bf16 a[48:51], a[160:163], a[156:159], a[48:51]\n" \
"s_or_b32 s11, s10, s7\n" \
"s_or_b32 s11, s11, 0x5c00000\n" \
"v_mfma_f32_16x16x32_bf16 a[48:51], a[224:227], a[220:223], a[48:51]\n" \
"v_writelane_b32 v11, s11, 3\n" \
"s_or_b32 s7, s7, 0x5c00080\n" \
"v_mfma_f32_16x16x32_bf16 a[64:67], a[164:167], a[140:143], a[64:67]\n" \
"v_writelane_b32 v11, s7, 4\n" \
"s_or_b32 s7, s7, s10\n" \
"v_mfma_f32_16x16x32_bf16 a[64:67], a[228:231], a[204:207], a[64:67]\n" \
"s_add_i32 s10, s3, s2\n" \
"v_writelane_b32 v11, s7, 5\n" \
"v_mfma_f32_16x16x32_bf16 a[80:83], a[172:175], a[140:143], a[80:83]\n" \
"s_mov_b32 s7, 2.0\n" \
"s_mov_b32 s11, 0x5bc0000\n" \
"v_mfma_f32_16x16x32_bf16 a[80:83], a[236:239], a[204:207], a[80:83]\n" \
"s_cmp_lg_u32 s10, 0\n" \
"s_cselect_b32 s10, s7, s11\n" \
"v_mfma_f32_16x16x32_bf16 a[68:71], a[140:143], a[168:171], a[68:71]\n" \
"s_lshr_b32 s15, s2, 3\n" \
"v_writelane_b32 v11, s10, 54\n" \
"v_mfma_f32_16x16x32_bf16 a[68:71], a[204:207], a[232:235], a[68:71]\n" \
"s_waitcnt vmcnt(11) lgkmcnt(0)\n" \
"s_barrier\n" \
"v_xor_b32_e32 v28, 0x20000, v28\n" \
"v_xor_b32_e32 v29, 0x20000, v29\n" \
"v_mfma_f32_16x16x32_bf16 v[116:119], a[176:179], a[144:147], v[116:119]\n" \
"s_add_u32 m0, s40, 0x8000\n" \
"s_add_u32 s85, s39, 0x6000\n" \
"v_mfma_f32_16x16x32_bf16 v[116:119], a[240:243], a[208:211], v[116:119]\n" \
"buffer_load_dwordx4 v20, s[80:83], s85 offen sc1 lds\n" \
"v_mfma_f32_16x16x32_bf16 v[120:123], a[180:183], a[144:147], v[120:123]\n" \
"ds_read_b64_tr_b16 a[160:161], v32 offset:0\n" \
"s_lshl_b32 s10, s6, 5\n" \
"v_mfma_f32_16x16x32_bf16 v[120:123], a[244:247], a[208:211], v[120:123]\n" \
"ds_read_b64_tr_b16 a[162:163], v32 offset:512\n" \
"s_min_u32 s16, s3, 2\n" \
"v_mfma_f32_16x16x32_bf16 v[124:127], a[184:187], a[144:147], v[124:127]\n" \
"s_add_u32 m0, s40, 0x9000\n" \
"s_add_u32 s85, s39, 0x7000\n" \
"v_mfma_f32_16x16x32_bf16 v[124:127], a[248:251], a[208:211], v[124:127]\n" \
"buffer_load_dwordx4 v20, s[80:83], s85 offen sc1 lds\n" \
"v_mfma_f32_16x16x32_bf16 v[128:131], a[188:191], a[144:147], v[128:131]\n" \
"ds_read_b64_tr_b16 a[224:225], v32 offset:4096\n" \
"s_mul_i32 s12, s8, 0x120\n" \
"v_mfma_f32_16x16x32_bf16 v[128:131], a[252:255], a[208:211], v[128:131]\n" \
"ds_read_b128 a[144:147], v28 offset:8192\n" \
"v_mfma_f32_16x16x32_bf16 v[144:147], a[188:191], a[148:151], v[144:147]\n" \
"ds_read_b128 a[208:211], v29 offset:8192\n" \
"v_mfma_f32_16x16x32_bf16 v[144:147], a[252:255], a[212:215], v[144:147]\n" \
"s_add_u32 m0, s40, 0xb000\n" \
"s_movk_i32 s84, 0x180\n" \
"v_mfma_f32_16x16x32_bf16 v[140:143], a[184:187], a[148:151], v[140:143]\n" \
"buffer_load_dwordx4 v21, s[76:79], s84 offen offset:0 lds\n" \
"v_mfma_f32_16x16x32_bf16 v[140:143], a[248:251], a[212:215], v[140:143]\n" \
"ds_read_b64_tr_b16 a[226:227], v32 offset:4608\n" \
"v_mfma_f32_16x16x32_bf16 v[136:139], a[180:183], a[148:151], v[136:139]\n" \
"s_add_u32 s88, s41, 0x3000\n" \
"buffer_store_dwordx4 v[252:255], v20, s[80:83], s88 offen offset:0 sc1\n" \
"v_mfma_f32_16x16x32_bf16 v[136:139], a[244:247], a[212:215], v[136:139]\n" \
"ds_read_b64_tr_b16 a[164:165], v33 offset:0\n" \
"v_mfma_f32_16x16x32_bf16 v[132:135], a[176:179], a[148:151], v[132:135]\n" \
"s_add_u32 s11, s22, s12\n" \
"s_addc_u32 s17, s23, 0\n" \
"s_nop 0\n" \
"v_mfma_f32_16x16x32_bf16 v[132:135], a[240:243], a[212:215], v[132:135]\n" \
"ds_read_b128 a[148:151], v28 offset:10240\n" \
"v_mfma_f32_16x16x32_bf16 v[148:151], a[176:179], a[152:155], v[148:151]\n" \
"ds_read_b128 a[212:215], v29 offset:10240\n" \
"v_mfma_f32_16x16x32_bf16 v[148:151], a[240:243], a[216:219], v[148:151]\n" \
"s_add_u32 m0, s31, 0x8000\n" \
"s_mov_b32 s84, 0x180000\n" \
"v_mfma_f32_16x16x32_bf16 v[152:155], a[180:183], a[152:155], v[152:155]\n" \
"buffer_load_dwordx4 v26, s[68:71], s84 offen offset:0 lds\n" \
"v_mfma_f32_16x16x32_bf16 v[152:155], a[244:247], a[216:219], v[152:155]\n" \
"ds_read_b64_tr_b16 a[166:167], v33 offset:512\n" \
"v_mfma_f32_16x16x32_bf16 v[156:159], a[184:187], a[152:155], v[156:159]\n" \
"ds_read_b64_tr_b16 a[228:229], v33 offset:4096\n" \
"v_mfma_f32_16x16x32_bf16 v[156:159], a[248:251], a[216:219], v[156:159]\n" \
"s_addk_i32 s84, 0x7c00\n" \
"v_mfma_f32_16x16x32_bf16 v[160:163], a[188:191], a[152:155], v[160:163]\n" \
"buffer_load_dwordx4 v26, s[68:71], s84 offen offset:1024 lds\n" \
"v_mfma_f32_16x16x32_bf16 v[160:163], a[252:255], a[216:219], v[160:163]\n" \
"ds_read_b128 a[152:155], v28 offset:12288\n" \
"v_mfma_f32_16x16x32_bf16 v[176:179], a[188:191], a[156:159], v[176:179]\n" \
"ds_read_b128 a[216:219], v29 offset:12288\n" \
"v_mfma_f32_16x16x32_bf16 v[176:179], a[252:255], a[220:223], v[176:179]\n" \
"s_addk_i32 s84, 0x7c00\n" \
"v_mfma_f32_16x16x32_bf16 v[172:175], a[184:187], a[156:159], v[172:175]\n" \
"buffer_load_dwordx4 v27, s[68:71], s84 offen offset:2048 lds\n" \
"v_mfma_f32_16x16x32_bf16 v[172:175], a[248:251], a[220:223], v[172:175]\n" \
"ds_read_b64_tr_b16 a[230:231], v33 offset:4608\n" \
"v_mfma_f32_16x16x32_bf16 v[168:171], a[180:183], a[156:159], v[168:171]\n" \
"ds_read_b64_tr_b16 a[172:173], v35 offset:0\n" \
"v_mfma_f32_16x16x32_bf16 v[168:171], a[244:247], a[220:223], v[168:171]\n" \
"ds_read_b64_tr_b16 a[174:175], v35 offset:512\n" \
"v_mfma_f32_16x16x32_bf16 v[164:167], a[176:179], a[156:159], v[164:167]\n" \
"s_add_u32 s18, s24, s12\n" \
"v_writelane_b32 v11, s11, 9\n" \
"s_addc_u32 s19, s25, 0\n" \
"v_mfma_f32_16x16x32_bf16 v[164:167], a[240:243], a[220:223], v[164:167]\n" \
"ds_read_b128 a[156:159], v28 offset:14336\n" \
"s_and_b32 s20, s17, 0xffff\n" \
"v_mfma_f32_16x16x32_bf16 v[84:87], a[176:179], a[136:139], v[84:87]\n" \
"ds_read_b128 a[220:223], v29 offset:14336\n" \
"v_writelane_b32 v11, s20, 10\n" \
"v_mfma_f32_16x16x32_bf16 v[84:87], a[240:243], a[200:203], v[84:87]\n" \
"s_addk_i32 s84, 0x7c00\n" \
"v_mfma_f32_16x16x32_bf16 v[88:91], a[180:183], a[136:139], v[88:91]\n" \
"buffer_load_dwordx4 v27, s[68:71], s84 offen offset:3072 lds\n" \
"v_mfma_f32_16x16x32_bf16 v[88:91], a[244:247], a[200:203], v[88:91]\n" \
"s_add_u32 m0, s30, 0x8000\n" \
"s_movk_i32 s84, 0x200\n" \
"v_mfma_f32_16x16x32_bf16 v[92:95], a[184:187], a[136:139], v[92:95]\n" \
"buffer_load_dwordx4 v24, s[76:79], s84 offen offset:0 lds\n" \
"v_mfma_f32_16x16x32_bf16 v[92:95], a[248:251], a[200:203], v[92:95]\n" \
"ds_read_b64_tr_b16 a[236:237], v35 offset:4096\n" \
"s_and_b32 s20, s19, 0xffff\n" \
"v_mfma_f32_16x16x32_bf16 v[96:99], a[188:191], a[136:139], v[96:99]\n" \
"ds_read_b64_tr_b16 a[238:239], v35 offset:4608\n" \
"v_writelane_b32 v11, s18, 11\n" \
"v_mfma_f32_16x16x32_bf16 v[96:99], a[252:255], a[200:203], v[96:99]\n" \
"ds_read_b128 a[136:139], v28 offset:4096\n" \
"v_mfma_f32_16x16x32_bf16 v[112:115], a[188:191], a[140:143], v[112:115]\n" \
"ds_read_b128 a[200:203], v29 offset:4096\n" \
"v_mfma_f32_16x16x32_bf16 v[112:115], a[252:255], a[204:207], v[112:115]\n" \
"s_movk_i32 s84, 0x2200\n" \
"s_nop 0\n" \
"v_mfma_f32_16x16x32_bf16 v[108:111], a[184:187], a[140:143], v[108:111]\n" \
"buffer_load_dwordx4 v25, s[76:79], s84 offen offset:1024 lds\n" \
"v_mfma_f32_16x16x32_bf16 v[108:111], a[248:251], a[204:207], v[108:111]\n" \
"s_add_u32 s89, s41, 0x1000\n" \
"buffer_store_dwordx4 v[200:203], v20, s[80:83], s89 offen offset:3072 sc1\n" \
"v_mfma_f32_16x16x32_bf16 v[104:107], a[180:183], a[140:143], v[104:107]\n" \
"ds_read_b64_tr_b16 a[168:169], v34 offset:0\n" \
"v_mfma_f32_16x16x32_bf16 v[104:107], a[244:247], a[204:207], v[104:107]\n" \
"ds_read_b64_tr_b16 a[170:171], v34 offset:512\n" \
"v_mfma_f32_16x16x32_bf16 v[100:103], a[176:179], a[140:143], v[100:103]\n" \
"ds_read_b64_tr_b16 a[232:233], v34 offset:4096\n" \
"v_mfma_f32_16x16x32_bf16 v[100:103], a[240:243], a[204:207], v[100:103]\n" \
"ds_read_b128 a[140:143], v28 offset:6144\n" \
"v_mfma_f32_16x16x32_bf16 v[52:55], a[176:179], a[128:131], v[52:55]\n" \
"ds_read_b128 a[204:207], v29 offset:6144\n" \
"v_mfma_f32_16x16x32_bf16 v[52:55], a[240:243], a[192:195], v[52:55]\n" \
"s_movk_i32 s84, 0x4200\n" \
"v_mfma_f32_16x16x32_bf16 v[56:59], a[180:183], a[128:131], v[56:59]\n" \
"buffer_load_dwordx4 v24, s[76:79], s84 offen offset:2048 lds\n" \
"v_mfma_f32_16x16x32_bf16 v[56:59], a[244:247], a[192:195], v[56:59]\n" \
"s_movk_i32 s84, 0x6200\n" \
"v_mfma_f32_16x16x32_bf16 v[60:63], a[184:187], a[128:131], v[60:63]\n" \
"buffer_load_dwordx4 v25, s[76:79], s84 offen offset:3072 lds\n" \
"v_mfma_f32_16x16x32_bf16 v[60:63], a[248:251], a[192:195], v[60:63]\n" \
"ds_read_b64_tr_b16 a[234:235], v34 offset:4608\n" \
"v_mfma_f32_16x16x32_bf16 v[64:67], a[188:191], a[128:131], v[64:67]\n" \
"s_cmpk_lt_u32 s3, 3\n" \
"s_cselect_b32 s11, s11, s18\n" \
"v_mfma_f32_16x16x32_bf16 v[64:67], a[252:255], a[192:195], v[64:67]\n" \
"ds_read_b128 a[128:131], v28 offset:0\n" \
"s_movk_i32 s18, 0xfff\n" \
"v_mfma_f32_16x16x32_bf16 v[80:83], a[188:191], a[132:135], v[80:83]\n" \
"ds_read_b128 a[192:195], v29 offset:0\n" \
"v_writelane_b32 v11, s20, 12\n" \
"v_mfma_f32_16x16x32_bf16 v[80:83], a[252:255], a[196:199], v[80:83]\n" \
"s_and_b32 s20, s29, 0xffff\n" \
"v_writelane_b32 v11, s28, 13\n" \
"v_mfma_f32_16x16x32_bf16 v[76:79], a[184:187], a[132:135], v[76:79]\n" \
"v_writelane_b32 v11, s20, 14\n" \
"v_writelane_b32 v11, s11, 15\n" \
"v_mfma_f32_16x16x32_bf16 v[76:79], a[248:251], a[196:199], v[76:79]\n" \
"s_cmpk_lt_u32 s3, 3\n" \
"s_cselect_b32 s11, s17, s19\n" \
"v_mfma_f32_16x16x32_bf16 v[72:75], a[180:183], a[132:135], v[72:75]\n" \
"s_and_b32 s11, s11, 0xffff\n" \
"v_writelane_b32 v11, s11, 16\n" \
"v_mfma_f32_16x16x32_bf16 v[72:75], a[244:247], a[196:199], v[72:75]\n" \
"s_mul_i32 s11, s6, 0x48\n" \
"s_add_i32 s17, s11, 0x47\n" \
"v_mfma_f32_16x16x32_bf16 v[68:71], a[176:179], a[132:135], v[68:71]\n" \
"s_cmpk_lt_u32 s3, 3\n" \
"s_cselect_b32 s17, s17, s18\n" \
"v_mfma_f32_16x16x32_bf16 v[68:71], a[240:243], a[196:199], v[68:71]\n" \
"ds_read_b128 a[132:135], v28 offset:2048\n" \
"ds_read_b128 a[196:199], v29 offset:2048\n" \
"s_waitcnt lgkmcnt(15)\n" \
"v_mfma_f32_16x16x32_bf16 a[0:3], a[160:163], a[144:147], a[0:3]\n" \
"s_add_u32 m0, s30, 0x9000\n" \
"s_addk_i32 s84, 0x3000\n" \
"v_mfma_f32_16x16x32_bf16 a[0:3], a[224:227], a[208:211], a[0:3]\n" \
"buffer_load_dwordx4 v24, s[76:79], s84 offen offset:0 lds\n" \
"v_mfma_f32_16x16x32_bf16 a[4:7], a[164:167], a[144:147], a[4:7]\n" \
"ds_read_b64_tr_b16 a[176:177], v36 offset:0\n" \
"s_mulk_i32 s17, 0x480\n" \
"v_mfma_f32_16x16x32_bf16 a[4:7], a[228:231], a[208:211], a[4:7]\n" \
"ds_read_b64_tr_b16 a[178:179], v36 offset:1024\n" \
"s_addk_i32 s17, 0x120\n" \
"v_mfma_f32_16x16x32_bf16 a[12:15], a[172:175], a[144:147], a[12:15]\n" \
"ds_read_b64_tr_b16 a[240:241], v36 offset:8192\n" \
"v_writelane_b32 v11, s17, 17\n" \
"s_waitcnt lgkmcnt(15)\n" \
"v_mfma_f32_16x16x32_bf16 a[12:15], a[236:239], a[208:211], a[12:15]\n" \
"s_add_u32 s88, s41, 0x1000\n" \
"buffer_store_dwordx4 v[204:207], v20, s[80:83], s88 offen offset:2048 sc1\n" \
"s_waitcnt lgkmcnt(11)\n" \
"v_mfma_f32_16x16x32_bf16 a[8:11], a[144:147], a[168:171], a[8:11]\n" \
"ds_read_b64_tr_b16 a[242:243], v36 offset:9216\n" \
"s_lshl_b32 s17, s3, 5\n" \
"s_waitcnt lgkmcnt(8)\n" \
"v_mfma_f32_16x16x32_bf16 a[8:11], a[208:211], a[232:235], a[8:11]\n" \
"ds_read_b64_tr_b16 a[180:181], v37 offset:0\n" \
"s_add_i32 s11, s11, s17\n" \
"v_mfma_f32_16x16x32_bf16 a[24:27], a[148:151], a[168:171], a[24:27]\n" \
"ds_read_b64_tr_b16 a[182:183], v37 offset:1024\n" \
"s_mul_i32 s17, s11, 0x480\n" \
"v_mfma_f32_16x16x32_bf16 a[24:27], a[212:215], a[232:235], a[24:27]\n" \
"ds_read_b64_tr_b16 a[244:245], v37 offset:8192\n" \
"s_mul_i32 s11, s13, 0x24000\n" \
"v_mfma_f32_16x16x32_bf16 a[28:31], a[172:175], a[148:151], a[28:31]\n" \
"s_add_i32 s18, s11, 0x1b000\n" \
"v_mfma_f32_16x16x32_bf16 a[28:31], a[236:239], a[212:215], a[28:31]\n" \
"s_addk_i32 s84, 0x2000\n" \
"v_mfma_f32_16x16x32_bf16 a[20:23], a[164:167], a[148:151], a[20:23]\n" \
"buffer_load_dwordx4 v25, s[76:79], s84 offen offset:1024 lds\n" \
"v_mfma_f32_16x16x32_bf16 a[20:23], a[228:231], a[212:215], a[20:23]\n" \
"ds_read_b64_tr_b16 a[246:247], v37 offset:9216\n" \
"v_mfma_f32_16x16x32_bf16 a[16:19], a[160:163], a[148:151], a[16:19]\n" \
"ds_read_b64_tr_b16 a[184:185], v38 offset:0\n" \
"v_mfma_f32_16x16x32_bf16 a[16:19], a[224:227], a[212:215], a[16:19]\n" \
"ds_read_b64_tr_b16 a[186:187], v38 offset:1024\n" \
"v_mfma_f32_16x16x32_bf16 a[32:35], a[160:163], a[152:155], a[32:35]\n" \
"s_addk_i32 s84, 0x2000\n" \
"v_mfma_f32_16x16x32_bf16 a[32:35], a[224:227], a[216:219], a[32:35]\n" \
"buffer_load_dwordx4 v24, s[76:79], s84 offen offset:2048 lds\n" \
"v_mfma_f32_16x16x32_bf16 a[36:39], a[164:167], a[152:155], a[36:39]\n" \
"ds_read_b64_tr_b16 a[248:249], v38 offset:8192\n" \
".fill (L_pb + 65248 - .) / 4, 4, 0xBF800000\n" \
"L_pp16:\n" \
"s_cbranch_execz L_pret\n" \
"s_nop 0\n" \
"v_mfma_f32_16x16x32_bf16 a[36:39], a[228:231], a[216:219], a[36:39]\n" \
"ds_read_b64_tr_b16 a[250:251], v38 offset:9216\n" \
"v_mfma_f32_16x16x32_bf16 a[44:47], a[172:175], a[152:155], a[44:47]\n" \
"ds_read_b64_tr_b16 a[188:189], v39 offset:0\n" \
"v_mfma_f32_16x16x32_bf16 a[44:47], a[236:239], a[216:219], a[44:47]\n" \
"ds_read_b64_tr_b16 a[190:191], v39 offset:1024\n" \
"v_mfma_f32_16x16x32_bf16 a[40:43], a[152:155], a[168:171], a[40:43]\n" \
"s_add_u32 s89, s41, 0x1000\n" \
"buffer_store_dwordx4 v[208:211], v20, s[80:83], s89 offen offset:1024 sc1\n" \
"v_mfma_f32_16x16x32_bf16 a[40:43], a[216:219], a[232:235], a[40:43]\n" \
"ds_read_b64_tr_b16 a[252:253], v39 offset:8192\n" \
"v_mfma_f32_16x16x32_bf16 a[56:59], a[156:159], a[168:171], a[56:59]\n" \
"ds_read_b64_tr_b16 a[254:255], v39 offset:9216\n" \
"v_mfma_f32_16x16x32_bf16 a[56:59], a[220:223], a[232:235], a[56:59]\n" \
"s_cmpk_lt_u32 s3, 3\n" \
"s_cselect_b32 s17, s17, s18\n" \
"v_mfma_f32_16x16x32_bf16 a[60:63], a[172:175], a[156:159], a[60:63]\n" \
"s_mov_b32 s18, 0x8000\n" \
"v_writelane_b32 v11, s17, 18\n" \
"v_mfma_f32_16x16x32_bf16 a[60:63], a[236:239], a[220:223], a[60:63]\n" \
"s_addk_i32 s84, 0x2000\n" \
"v_mfma_f32_16x16x32_bf16 a[52:55], a[164:167], a[156:159], a[52:55]\n" \
"buffer_load_dwordx4 v25, s[76:79], s84 offen offset:3072 lds\n" \
"v_mfma_f32_16x16x32_bf16 a[52:55], a[228:231], a[220:223], a[52:55]\n" \
"s_mul_i32 s17, s16, 0x2400\n" \
"s_mul_i32 s16, s16, 0x9000\n" \
"v_mfma_f32_16x16x32_bf16 a[48:51], a[160:163], a[156:159], a[48:51]\n" \
"s_bitset1_b32 s17, 17\n" \
"v_writelane_b32 v11, s17, 19\n" \
"v_mfma_f32_16x16x32_bf16 a[48:51], a[224:227], a[220:223], a[48:51]\n" \
"s_add_i32 s16, s11, s16\n" \
"v_writelane_b32 v11, s16, 26\n" \
"v_mfma_f32_16x16x32_bf16 a[64:67], a[164:167], a[140:143], a[64:67]\n" \
"s_mul_i32 s16, s3, 0x2400\n" \
"s_add_i32 s17, s16, 0xa400\n" \
"v_mfma_f32_16x16x32_bf16 a[64:67], a[228:231], a[204:207], a[64:67]\n" \
"s_cmpk_lt_u32 s3, 3\n" \
"s_cselect_b32 s17, s17, s18\n" \
"v_mfma_f32_16x16x32_bf16 a[80:83], a[172:175], a[140:143], a[80:83]\n" \
"s_mov_b32 s18, 0x1b000\n" \
"s_xor_b32 s19, s3, 2\n" \
"v_mfma_f32_16x16x32_bf16 a[80:83], a[236:239], a[204:207], a[80:83]\n" \
"s_cmp_lg_u32 s19, 0\n" \
"s_cselect_b32 s18, s17, s18\n" \
"v_mfma_f32_16x16x32_bf16 a[68:71], a[140:143], a[168:171], a[68:71]\n" \
"v_writelane_b32 v11, s17, 20\n" \
"s_or_b32 s17, s16, 0x10000\n" \
"v_mfma_f32_16x16x32_bf16 a[68:71], a[204:207], a[232:235], a[68:71]\n" \
"s_waitcnt vmcnt(11) lgkmcnt(0)\n" \
"s_barrier\n" \
"v_xor_b32_e32 v28, 0x20000, v28\n" \
"v_xor_b32_e32 v29, 0x20000, v29\n" \
"v_mfma_f32_16x16x32_bf16 v[116:119], a[176:179], a[144:147], v[116:119]\n" \
"s_add_u32 m0, s40, 0x0\n" \
"s_nop 0\n" \
"s_add_u32 s85, s39, 0x8000\n" \
"v_mfma_f32_16x16x32_bf16 v[116:119], a[240:243], a[208:211], v[116:119]\n" \
"buffer_load_dwordx4 v20, s[80:83], s85 offen sc1 lds\n" \
"v_mfma_f32_16x16x32_bf16 v[120:123], a[180:183], a[144:147], v[120:123]\n" \
"ds_read_b64_tr_b16 a[160:161], v32 offset:32768\n" \
"v_writelane_b32 v11, s18, 21\n" \
"v_mfma_f32_16x16x32_bf16 v[120:123], a[244:247], a[208:211], v[120:123]\n" \
"ds_read_b64_tr_b16 a[162:163], v32 offset:33280\n" \
"s_add_i32 s18, s16, 0x19000\n" \
"v_mfma_f32_16x16x32_bf16 v[124:127], a[184:187], a[144:147], v[124:127]\n" \
"s_add_u32 m0, s40, 0x1000\n" \
"s_add_u32 s85, s39, 0x9000\n" \
"v_mfma_f32_16x16x32_bf16 v[124:127], a[248:251], a[208:211], v[124:127]\n" \
"buffer_load_dwordx4 v20, s[80:83], s85 offen sc1 lds\n" \
"v_mfma_f32_16x16x32_bf16 v[128:131], a[188:191], a[144:147], v[128:131]\n" \
"ds_read_b64_tr_b16 a[224:225], v32 offset:36864\n" \
"v_writelane_b32 v11, s17, 22\n" \
"v_mfma_f32_16x16x32_bf16 v[128:131], a[252:255], a[208:211], v[128:131]\n" \
"ds_read_b128 a[144:147], v28 offset:8192\n" \
"s_mov_b32 s17, 0\n" \
"v_mfma_f32_16x16x32_bf16 v[144:147], a[188:191], a[148:151], v[144:147]\n" \
"ds_read_b128 a[208:211], v29 offset:8192\n" \
"v_mfma_f32_16x16x32_bf16 v[144:147], a[252:255], a[212:215], v[144:147]\n" \
"s_add_u32 m0, s40, 0x3000\n" \
"s_movk_i32 s84, 0x200\n" \
"v_mfma_f32_16x16x32_bf16 v[140:143], a[184:187], a[148:151], v[140:143]\n" \
"buffer_load_dwordx4 v21, s[76:79], s84 offen offset:0 lds\n" \
"v_mfma_f32_16x16x32_bf16 v[140:143], a[248:251], a[212:215], v[140:143]\n" \
"ds_read_b64_tr_b16 a[226:227], v32 offset:37376\n" \
"v_mfma_f32_16x16x32_bf16 v[136:139], a[180:183], a[148:151], v[136:139]\n" \
"s_add_u32 s88, s41, 0x1000\n" \
"buffer_store_dwordx4 v[212:215], v20, s[80:83], s88 offen offset:0 sc1\n" \
"v_mfma_f32_16x16x32_bf16 v[136:139], a[244:247], a[212:215], v[136:139]\n" \
"ds_read_b64_tr_b16 a[164:165], v33 offset:32768\n" \
"v_mfma_f32_16x16x32_bf16 v[132:135], a[176:179], a[148:151], v[132:135]\n" \
"s_cmpk_lt_u32 s3, 3\n" \
"s_cselect_b32 s18, s18, s17\n" \
"v_mfma_f32_16x16x32_bf16 v[132:135], a[240:243], a[212:215], v[132:135]\n" \
"ds_read_b128 a[148:151], v28 offset:10240\n" \
"s_bitset1_b32 s16, 17\n" \
"v_mfma_f32_16x16x32_bf16 v[148:151], a[176:179], a[152:155], v[148:151]\n" \
"ds_read_b128 a[212:215], v29 offset:10240\n" \
"v_writelane_b32 v11, s18, 23\n" \
"v_mfma_f32_16x16x32_bf16 v[148:151], a[240:243], a[216:219], v[148:151]\n" \
"s_add_u32 m0, s31, 0x0\n" \
"s_mov_b32 s84, 0x200000\n" \
"v_mfma_f32_16x16x32_bf16 v[152:155], a[180:183], a[152:155], v[152:155]\n" \
"buffer_load_dwordx4 v26, s[68:71], s84 offen offset:0 lds\n" \
"v_mfma_f32_16x16x32_bf16 v[152:155], a[244:247], a[216:219], v[152:155]\n" \
"ds_read_b64_tr_b16 a[166:167], v33 offset:33280\n" \
"s_movk_i32 s18, 0x2400\n" \
"v_mfma_f32_16x16x32_bf16 v[156:159], a[184:187], a[152:155], v[156:159]\n" \
"ds_read_b64_tr_b16 a[228:229], v33 offset:36864\n" \
"v_mfma_f32_16x16x32_bf16 v[156:159], a[248:251], a[216:219], v[156:159]\n" \
"s_addk_i32 s84, 0x7c00\n" \
"v_mfma_f32_16x16x32_bf16 v[160:163], a[188:191], a[152:155], v[160:163]\n" \
"buffer_load_dwordx4 v26, s[68:71], s84 offen offset:1024 lds\n" \
"v_mfma_f32_16x16x32_bf16 v[160:163], a[252:255], a[216:219], v[160:163]\n" \
"ds_read_b128 a[152:155], v28 offset:12288\n" \
"v_mfma_f32_16x16x32_bf16 v[176:179], a[188:191], a[156:159], v[176:179]\n" \
"ds_read_b128 a[216:219], v29 offset:12288\n" \
"v_mfma_f32_16x16x32_bf16 v[176:179], a[252:255], a[220:223], v[176:179]\n" \
"s_addk_i32 s84, 0x7c00\n" \
"v_mfma_f32_16x16x32_bf16 v[172:175], a[184:187], a[156:159], v[172:175]\n" \
"buffer_load_dwordx4 v27, s[68:71], s84 offen offset:2048 lds\n" \
"v_mfma_f32_16x16x32_bf16 v[172:175], a[248:251], a[220:223], v[172:175]\n" \
"ds_read_b64_tr_b16 a[230:231], v33 offset:37376\n" \
"v_mfma_f32_16x16x32_bf16 v[168:171], a[180:183], a[156:159], v[168:171]\n" \
"ds_read_b64_tr_b16 a[172:173], v35 offset:32768\n" \
"v_mfma_f32_16x16x32_bf16 v[168:171], a[244:247], a[220:223], v[168:171]\n" \
"ds_read_b64_tr_b16 a[174:175], v35 offset:33280\n" \
"v_mfma_f32_16x16x32_bf16 v[164:167], a[176:179], a[156:159], v[164:167]\n" \
"s_cmpk_lt_u32 s3, 3\n" \
"s_cselect_b32 s16, s16, s18\n" \
"v_mfma_f32_16x16x32_bf16 v[164:167], a[240:243], a[220:223], v[164:167]\n" \
"ds_read_b128 a[156:159], v28 offset:14336\n" \
"s_mul_i32 s19, s3, 0x9000\n" \
"v_mfma_f32_16x16x32_bf16 v[84:87], a[176:179], a[136:139], v[84:87]\n" \
"ds_read_b128 a[220:223], v29 offset:14336\n" \
"v_writelane_b32 v11, s16, 24\n" \
"v_mfma_f32_16x16x32_bf16 v[84:87], a[240:243], a[200:203], v[84:87]\n" \
"s_addk_i32 s84, 0x7c00\n" \
"v_mfma_f32_16x16x32_bf16 v[88:91], a[180:183], a[136:139], v[88:91]\n" \
"buffer_load_dwordx4 v27, s[68:71], s84 offen offset:3072 lds\n" \
"v_mfma_f32_16x16x32_bf16 v[88:91], a[244:247], a[200:203], v[88:91]\n" \
"s_add_u32 m0, s30, 0x20000\n" \
"s_movk_i32 s84, 0x280\n" \
"v_mfma_f32_16x16x32_bf16 v[92:95], a[184:187], a[136:139], v[92:95]\n" \
"buffer_load_dwordx4 v24, s[76:79], s84 offen offset:0 lds\n" \
"v_mfma_f32_16x16x32_bf16 v[92:95], a[248:251], a[200:203], v[92:95]\n" \
"ds_read_b64_tr_b16 a[236:237], v35 offset:36864\n" \
"s_lshl_b32 s16, s3, 10\n" \
"v_mfma_f32_16x16x32_bf16 v[96:99], a[188:191], a[136:139], v[96:99]\n" \
"ds_read_b64_tr_b16 a[238:239], v35 offset:37376\n" \
"s_addk_i32 s16, 0x4800\n" \
"v_mfma_f32_16x16x32_bf16 v[96:99], a[252:255], a[200:203], v[96:99]\n" \
"ds_read_b128 a[136:139], v28 offset:4096\n" \
"v_writelane_b32 v11, s16, 25\n" \
"v_mfma_f32_16x16x32_bf16 v[112:115], a[188:191], a[140:143], v[112:115]\n" \
"ds_read_b128 a[200:203], v29 offset:4096\n" \
"s_add_i32 s16, s15, 1\n" \
"v_mfma_f32_16x16x32_bf16 v[112:115], a[252:255], a[204:207], v[112:115]\n" \
"s_movk_i32 s84, 0x2280\n" \
"v_mfma_f32_16x16x32_bf16 v[108:111], a[184:187], a[140:143], v[108:111]\n" \
"buffer_load_dwordx4 v25, s[76:79], s84 offen offset:1024 lds\n" \
"v_mfma_f32_16x16x32_bf16 v[108:111], a[248:251], a[204:207], v[108:111]\n" \
"s_add_u32 s89, s42, 0x0\n" \
"buffer_store_dwordx4 v[216:219], v20, s[80:83], s89 offen offset:1024 sc1\n" \
"v_mfma_f32_16x16x32_bf16 v[104:107], a[180:183], a[140:143], v[104:107]\n" \
"ds_read_b64_tr_b16 a[168:169], v34 offset:32768\n" \
"s_and_b32 s16, s16, 3\n" \
"v_mfma_f32_16x16x32_bf16 v[104:107], a[244:247], a[204:207], v[104:107]\n" \
"ds_read_b64_tr_b16 a[170:171], v34 offset:33280\n" \
"s_or_b32 s16, s16, s5\n" \
"v_mfma_f32_16x16x32_bf16 v[100:103], a[176:179], a[140:143], v[100:103]\n" \
"ds_read_b64_tr_b16 a[232:233], v34 offset:36864\n" \
"s_mul_i32 s18, s16, 0x24000\n" \
"v_mfma_f32_16x16x32_bf16 v[100:103], a[240:243], a[204:207], v[100:103]\n" \
"ds_read_b128 a[140:143], v28 offset:6144\n" \
"s_add_i32 s18, s18, s19\n" \
"v_mfma_f32_16x16x32_bf16 v[52:55], a[176:179], a[128:131], v[52:55]\n" \
"ds_read_b128 a[204:207], v29 offset:6144\n" \
"s_xor_b32 s13, s13, 2\n" \
"v_mfma_f32_16x16x32_bf16 v[52:55], a[240:243], a[192:195], v[52:55]\n" \
"s_movk_i32 s84, 0x4280\n" \
"v_mfma_f32_16x16x32_bf16 v[56:59], a[180:183], a[128:131], v[56:59]\n" \
"buffer_load_dwordx4 v24, s[76:79], s84 offen offset:2048 lds\n" \
"v_mfma_f32_16x16x32_bf16 v[56:59], a[244:247], a[192:195], v[56:59]\n" \
"s_movk_i32 s84, 0x6280\n" \
"v_mfma_f32_16x16x32_bf16 v[60:63], a[184:187], a[128:131], v[60:63]\n" \
"buffer_load_dwordx4 v25, s[76:79], s84 offen offset:3072 lds\n" \
"v_mfma_f32_16x16x32_bf16 v[60:63], a[248:251], a[192:195], v[60:63]\n" \
"ds_read_b64_tr_b16 a[234:235], v34 offset:37376\n" \
"s_add_i32 s15, s15, -1\n" \
"v_mfma_f32_16x16x32_bf16 v[64:67], a[188:191], a[128:131], v[64:67]\n" \
"v_writelane_b32 v11, s18, 27\n" \
"s_mul_i32 s18, s13, 0x24000\n" \
"v_mfma_f32_16x16x32_bf16 v[64:67], a[252:255], a[192:195], v[64:67]\n" \
"ds_read_b128 a[128:131], v28 offset:0\n" \
"s_and_b32 s15, s15, 3\n" \
"v_mfma_f32_16x16x32_bf16 v[80:83], a[188:191], a[132:135], v[80:83]\n" \
"ds_read_b128 a[192:195], v29 offset:0\n" \
"s_add_i32 s18, s18, s19\n" \
"v_mfma_f32_16x16x32_bf16 v[80:83], a[252:255], a[196:199], v[80:83]\n" \
"s_or_b32 s15, s15, s5\n" \
"v_writelane_b32 v11, s18, 28\n" \
"v_mfma_f32_16x16x32_bf16 v[76:79], a[184:187], a[132:135], v[76:79]\n" \
"s_mul_i32 s18, s15, 0x24000\n" \
"s_add_i32 s18, s18, s19\n" \
"v_mfma_f32_16x16x32_bf16 v[76:79], a[248:251], a[196:199], v[76:79]\n" \
"v_writelane_b32 v11, s18, 29\n" \
"s_or_b32 s18, s9, s14\n" \
"v_mfma_f32_16x16x32_bf16 v[72:75], a[180:183], a[132:135], v[72:75]\n" \
"s_mul_i32 s18, s18, 0x24800\n" \
"s_lshl_b32 s16, s16, 12\n" \
"v_mfma_f32_16x16x32_bf16 v[72:75], a[244:247], a[196:199], v[72:75]\n" \
"s_add_i32 s19, s18, 0x3004800\n" \
"s_add_i32 s20, s19, s16\n" \
"v_mfma_f32_16x16x32_bf16 v[68:71], a[176:179], a[132:135], v[68:71]\n" \
"v_writelane_b32 v11, s20, 30\n" \
"s_lshl_b32 s13, s13, 12\n" \
"v_mfma_f32_16x16x32_bf16 v[68:71], a[240:243], a[196:199], v[68:71]\n" \
"ds_read_b128 a[132:135], v28 offset:2048\n" \
"ds_read_b128 a[196:199], v29 offset:2048\n" \
"s_waitcnt lgkmcnt(15)\n" \
"v_mfma_f32_16x16x32_bf16 a[0:3], a[160:163], a[144:147], a[0:3]\n" \
"s_add_u32 m0, s30, 0x21000\n" \
"s_addk_i32 s84, 0x3000\n" \
"v_mfma_f32_16x16x32_bf16 a[0:3], a[224:227], a[208:211], a[0:3]\n" \
"buffer_load_dwordx4 v24, s[76:79], s84 offen offset:0 lds\n" \
"v_mfma_f32_16x16x32_bf16 a[4:7], a[164:167], a[144:147], a[4:7]\n" \
"ds_read_b64_tr_b16 a[176:177], v36 offset:32768\n" \
"s_lshl_b32 s15, s15, 12\n" \
"v_mfma_f32_16x16x32_bf16 a[4:7], a[228:231], a[208:211], a[4:7]\n" \
"ds_read_b64_tr_b16 a[178:179], v36 offset:33792\n" \
"s_sub_i32 s14, s9, s14\n" \
"v_mfma_f32_16x16x32_bf16 a[12:15], a[172:175], a[144:147], a[12:15]\n" \
"ds_read_b64_tr_b16 a[240:241], v36 offset:40960\n" \
"s_add_i32 s20, s19, s13\n" \
"s_waitcnt lgkmcnt(15)\n" \
"v_mfma_f32_16x16x32_bf16 a[12:15], a[236:239], a[208:211], a[12:15]\n" \
"s_add_u32 s88, s42, 0x0\n" \
"buffer_store_dwordx4 v[220:223], v20, s[80:83], s88 offen offset:0 sc1\n" \
"s_waitcnt lgkmcnt(11)\n" \
"v_mfma_f32_16x16x32_bf16 a[8:11], a[144:147], a[168:171], a[8:11]\n" \
"ds_read_b64_tr_b16 a[242:243], v36 offset:41984\n" \
"v_writelane_b32 v11, s20, 31\n" \
"s_waitcnt lgkmcnt(8)\n" \
"v_mfma_f32_16x16x32_bf16 a[8:11], a[208:211], a[232:235], a[8:11]\n" \
"ds_read_b64_tr_b16 a[180:181], v37 offset:32768\n" \
"s_add_i32 s19, s19, s15\n" \
"v_mfma_f32_16x16x32_bf16 a[24:27], a[148:151], a[168:171], a[24:27]\n" \
"ds_read_b64_tr_b16 a[182:183], v37 offset:33792\n" \
"s_mul_i32 s14, s14, 0x24800\n" \
"v_mfma_f32_16x16x32_bf16 a[24:27], a[212:215], a[232:235], a[24:27]\n" \
"ds_read_b64_tr_b16 a[244:245], v37 offset:40960\n" \
"v_writelane_b32 v11, s19, 32\n" \
"v_mfma_f32_16x16x32_bf16 a[28:31], a[172:175], a[148:151], a[28:31]\n" \
"s_add_i32 s19, s14, 0x4120800\n" \
"s_add_i32 s16, s19, s16\n" \
"v_mfma_f32_16x16x32_bf16 a[28:31], a[236:239], a[212:215], a[28:31]\n" \
"s_addk_i32 s84, 0x2000\n" \
"v_mfma_f32_16x16x32_bf16 a[20:23], a[164:167], a[148:151], a[20:23]\n" \
"buffer_load_dwordx4 v25, s[76:79], s84 offen offset:1024 lds\n" \
"v_mfma_f32_16x16x32_bf16 a[20:23], a[228:231], a[212:215], a[20:23]\n" \
"ds_read_b64_tr_b16 a[246:247], v37 offset:41984\n" \
"v_writelane_b32 v11, s16, 33\n" \
"v_mfma_f32_16x16x32_bf16 a[16:19], a[160:163], a[148:151], a[16:19]\n" \
"ds_read_b64_tr_b16 a[184:185], v38 offset:32768\n" \
"s_add_i32 s13, s19, s13\n" \
"v_mfma_f32_16x16x32_bf16 a[16:19], a[224:227], a[212:215], a[16:19]\n" \
"ds_read_b64_tr_b16 a[186:187], v38 offset:33792\n" \
"v_writelane_b32 v11, s13, 34\n" \
"v_mfma_f32_16x16x32_bf16 a[32:35], a[160:163], a[152:155], a[32:35]\n" \
"s_addk_i32 s84, 0x2000\n" \
"v_mfma_f32_16x16x32_bf16 a[32:35], a[224:227], a[216:219], a[32:35]\n" \
"buffer_load_dwordx4 v24, s[76:79], s84 offen offset:2048 lds\n" \
"v_mfma_f32_16x16x32_bf16 a[36:39], a[164:167], a[152:155], a[36:39]\n" \
"ds_read_b64_tr_b16 a[248:249], v38 offset:40960\n" \
"s_add_i32 s19, s19, s15\n" \
"v_mfma_f32_16x16x32_bf16 a[36:39], a[228:231], a[216:219], a[36:39]\n" \
"ds_read_b64_tr_b16 a[250:251], v38 offset:41984\n" \
"s_or_b32 s13, s18, 0x3000000\n" \
"v_mfma_f32_16x16x32_bf16 a[44:47], a[172:175], a[152:155], a[44:47]\n" \
"ds_read_b64_tr_b16 a[188:189], v39 offset:32768\n" \
"s_lshl_b32 s15, s6, 11\n" \
"v_mfma_f32_16x16x32_bf16 a[44:47], a[236:239], a[216:219], a[44:47]\n" \
"ds_read_b64_tr_b16 a[190:191], v39 offset:33792\n" \
"v_writelane_b32 v11, s19, 35\n" \
"v_mfma_f32_16x16x32_bf16 a[40:43], a[152:155], a[168:171], a[40:43]\n" \
"s_add_u32 s89, s42, 0x1000\n" \
"buffer_store_dwordx4 v[224:227], v20, s[80:83], s89 offen offset:3072 sc1\n" \
"v_mfma_f32_16x16x32_bf16 a[40:43], a[216:219], a[232:235], a[40:43]\n" \
"ds_read_b64_tr_b16 a[252:253], v39 offset:40960\n" \
"s_add_i32 s16, s13, s15\n" \
"v_mfma_f32_16x16x32_bf16 a[56:59], a[156:159], a[168:171], a[56:59]\n" \
"ds_read_b64_tr_b16 a[254:255], v39 offset:41984\n" \
"v_writelane_b32 v11, s16, 36\n" \
"v_mfma_f32_16x16x32_bf16 a[56:59], a[220:223], a[232:235], a[56:59]\n" \
"s_add_i32 s16, s14, 0x411c000\n" \
"s_add_i32 s19, s16, s15\n" \
"v_mfma_f32_16x16x32_bf16 a[60:63], a[172:175], a[156:159], a[60:63]\n" \
"v_writelane_b32 v11, s19, 37\n" \
"s_add_i32 s18, s18, 0x3004000\n" \
"v_mfma_f32_16x16x32_bf16 a[60:63], a[236:239], a[220:223], a[60:63]\n" \
"s_addk_i32 s84, 0x2000\n" \
"v_mfma_f32_16x16x32_bf16 a[52:55], a[164:167], a[156:159], a[52:55]\n" \
"buffer_load_dwordx4 v25, s[76:79], s84 offen offset:3072 lds\n" \
"v_mfma_f32_16x16x32_bf16 a[52:55], a[228:231], a[220:223], a[52:55]\n" \
"v_writelane_b32 v11, s18, 38\n" \
"s_add_i32 s14, s14, 0x4120000\n" \
"v_mfma_f32_16x16x32_bf16 a[48:51], a[160:163], a[156:159], a[48:51]\n" \
"v_writelane_b32 v11, s14, 39\n" \
"s_lshl_b32 s14, s4, 2\n" \
"v_mfma_f32_16x16x32_bf16 a[48:51], a[224:227], a[220:223], a[48:51]\n" \
"s_or_b32 s8, s14, s8\n" \
"s_mul_i32 s8, s8, 0x90000\n" \
"v_mfma_f32_16x16x32_bf16 a[64:67], a[164:167], a[140:143], a[64:67]\n" \
"s_lshl_b32 s14, s3, 14\n" \
"s_add_i32 s8, s8, s14\n" \
"v_mfma_f32_16x16x32_bf16 a[64:67], a[228:231], a[204:207], a[64:67]\n" \
"s_lshl_b32 s9, s9, 4\n" \
"s_lshl_b32 s14, s4, 7\n" \
"v_mfma_f32_16x16x32_bf16 a[80:83], a[172:175], a[140:143], a[80:83]\n" \
"s_or_b32 s18, s9, s14\n" \
"s_sub_i32 s9, s9, s14\n" \
"v_mfma_f32_16x16x32_bf16 a[80:83], a[236:239], a[204:207], a[80:83]\n" \
"s_mulk_i32 s18, 0x480\n" \
"s_mulk_i32 s9, 0x480\n" \
"v_mfma_f32_16x16x32_bf16 a[68:71], a[140:143], a[168:171], a[68:71]\n" \
"s_or_b32 s14, s18, s12\n" \
"s_or_b32 s9, s9, s12\n" \
"v_mfma_f32_16x16x32_bf16 a[68:71], a[204:207], a[232:235], a[68:71]\n" \
"s_waitcnt vmcnt(11) lgkmcnt(0)\n" \
"s_barrier\n" \
"v_mfma_f32_16x16x32_bf16 v[116:119], a[176:179], a[144:147], v[116:119]\n" \
"s_add_u32 m0, s40, 0x8000\n" \
"s_add_u32 s85, s39, 0xa000\n" \
"v_mfma_f32_16x16x32_bf16 v[116:119], a[240:243], a[208:211], v[116:119]\n" \
"buffer_load_dwordx4 v20, s[80:83], s85 offen sc1 lds\n" \
"v_mfma_f32_16x16x32_bf16 v[120:123], a[180:183], a[144:147], v[120:123]\n" \
"ds_read_b64_tr_b16 a[160:161], v32 offset:0\n" \
"v_accvgpr_read_b32 v212, a108\n" \
"v_mfma_f32_16x16x32_bf16 v[120:123], a[244:247], a[208:211], v[120:123]\n" \
"ds_read_b64_tr_b16 a[162:163], v32 offset:512\n" \
"v_accvgpr_read_b32 v213, a109\n" \
"v_mfma_f32_16x16x32_bf16 v[124:127], a[184:187], a[144:147], v[124:127]\n" \
"s_add_u32 m0, s40, 0x9000\n" \
"s_add_u32 s85, s39, 0xb000\n" \
"v_mfma_f32_16x16x32_bf16 v[124:127], a[248:251], a[208:211], v[124:127]\n" \
"buffer_load_dwordx4 v20, s[80:83], s85 offen sc1 lds\n" \
"v_mfma_f32_16x16x32_bf16 v[128:131], a[188:191], a[144:147], v[128:131]\n" \
"ds_read_b64_tr_b16 a[224:225], v32 offset:4096\n" \
"v_accvgpr_read_b32 v214, a110\n" \
"v_mfma_f32_16x16x32_bf16 v[128:131], a[252:255], a[208:211], v[128:131]\n" \
"ds_read_b128 a[144:147], v28 offset:40960\n" \
"v_accvgpr_read_b32 v215, a111\n" \
"v_mfma_f32_16x16x32_bf16 v[144:147], a[188:191], a[148:151], v[144:147]\n" \
"ds_read_b128 a[208:211], v29 offset:40960\n" \
"v_accvgpr_read_b32 v180, a124\n" \
"v_mfma_f32_16x16x32_bf16 v[144:147], a[252:255], a[212:215], v[144:147]\n" \
"s_add_u32 m0, s40, 0xb000\n" \
"s_movk_i32 s84, 0x280\n" \
"s_nop 0\n" \
"v_mfma_f32_16x16x32_bf16 v[140:143], a[184:187], a[148:151], v[140:143]\n" \
"buffer_load_dwordx4 v21, s[76:79], s84 offen offset:0 lds\n" \
"v_mfma_f32_16x16x32_bf16 v[140:143], a[248:251], a[212:215], v[140:143]\n" \
"ds_read_b64_tr_b16 a[226:227], v32 offset:4608\n" \
"v_accvgpr_read_b32 v181, a125\n" \
"v_mfma_f32_16x16x32_bf16 v[136:139], a[180:183], a[148:151], v[136:139]\n" \
"s_add_u32 s88, s42, 0x1000\n" \
"buffer_store_dwordx4 v[228:231], v20, s[80:83], s88 offen offset:2048 sc1\n" \
"v_mfma_f32_16x16x32_bf16 v[136:139], a[244:247], a[212:215], v[136:139]\n" \
"ds_read_b64_tr_b16 a[164:165], v33 offset:0\n" \
"v_accvgpr_read_b32 v182, a126\n" \
"v_mfma_f32_16x16x32_bf16 v[132:135], a[176:179], a[148:151], v[132:135]\n" \
"v_accvgpr_read_b32 v183, a127\n" \
"v_cvt_pk_bf16_f32 v212, v212, v213\n" \
"v_mfma_f32_16x16x32_bf16 v[132:135], a[240:243], a[212:215], v[132:135]\n" \
"ds_read_b128 a[148:151], v28 offset:43008\n" \
"v_cvt_pk_bf16_f32 v213, v214, v215\n" \
"v_mfma_f32_16x16x32_bf16 v[148:151], a[176:179], a[152:155], v[148:151]\n" \
"ds_read_b128 a[212:215], v29 offset:43008\n" \
"v_cvt_pk_bf16_f32 v214, v180, v181\n" \
"v_mfma_f32_16x16x32_bf16 v[148:151], a[240:243], a[216:219], v[148:151]\n" \
"s_add_u32 m0, s31, 0x8000\n" \
"s_mov_b32 s84, 0x280000\n" \
"v_mfma_f32_16x16x32_bf16 v[152:155], a[180:183], a[152:155], v[152:155]\n" \
"buffer_load_dwordx4 v26, s[68:71], s84 offen offset:0 lds\n" \
"v_mfma_f32_16x16x32_bf16 v[152:155], a[244:247], a[216:219], v[152:155]\n" \
"ds_read_b64_tr_b16 a[166:167], v33 offset:512\n" \
"v_cvt_pk_bf16_f32 v215, v182, v183\n" \
"v_mfma_f32_16x16x32_bf16 v[156:159], a[184:187], a[152:155], v[156:159]\n" \
"ds_read_b64_tr_b16 a[228:229], v33 offset:4096\n" \
"v_accvgpr_read_b32 v208, a104\n" \
"v_mfma_f32_16x16x32_bf16 v[156:159], a[248:251], a[216:219], v[156:159]\n" \
"s_addk_i32 s84, 0x7c00\n" \
"v_mfma_f32_16x16x32_bf16 v[160:163], a[188:191], a[152:155], v[160:163]\n" \
"buffer_load_dwordx4 v26, s[68:71], s84 offen offset:1024 lds\n" \
"v_mfma_f32_16x16x32_bf16 v[160:163], a[252:255], a[216:219], v[160:163]\n" \
"ds_read_b128 a[152:155], v28 offset:45056\n" \
"v_accvgpr_read_b32 v209, a105\n" \
"v_mfma_f32_16x16x32_bf16 v[176:179], a[188:191], a[156:159], v[176:179]\n" \
"ds_read_b128 a[216:219], v29 offset:45056\n" \
"v_accvgpr_read_b32 v210, a106\n" \
"v_mfma_f32_16x16x32_bf16 v[176:179], a[252:255], a[220:223], v[176:179]\n" \
"s_addk_i32 s84, 0x7c00\n" \
"v_mfma_f32_16x16x32_bf16 v[172:175], a[184:187], a[156:159], v[172:175]\n" \
"buffer_load_dwordx4 v27, s[68:71], s84 offen offset:2048 lds\n" \
"v_mfma_f32_16x16x32_bf16 v[172:175], a[248:251], a[220:223], v[172:175]\n" \
"ds_read_b64_tr_b16 a[230:231], v33 offset:4608\n" \
"v_accvgpr_read_b32 v211, a107\n" \
"v_mfma_f32_16x16x32_bf16 v[168:171], a[180:183], a[156:159], v[168:171]\n" \
"ds_read_b64_tr_b16 a[172:173], v35 offset:0\n" \
"v_accvgpr_read_b32 v180, a120\n" \
"v_mfma_f32_16x16x32_bf16 v[168:171], a[244:247], a[220:223], v[168:171]\n" \
"ds_read_b64_tr_b16 a[174:175], v35 offset:512\n" \
"v_accvgpr_read_b32 v181, a121\n" \
"v_mfma_f32_16x16x32_bf16 v[164:167], a[176:179], a[156:159], v[164:167]\n" \
"v_accvgpr_read_b32 v182, a122\n" \
"v_accvgpr_read_b32 v183, a123\n" \
"v_mfma_f32_16x16x32_bf16 v[164:167], a[240:243], a[220:223], v[164:167]\n" \
"ds_read_b128 a[156:159], v28 offset:47104\n" \
"v_cvt_pk_bf16_f32 v208, v208, v209\n" \
"v_mfma_f32_16x16x32_bf16 v[84:87], a[176:179], a[136:139], v[84:87]\n" \
"ds_read_b128 a[220:223], v29 offset:47104\n" \
"v_cvt_pk_bf16_f32 v209, v210, v211\n" \
"v_mfma_f32_16x16x32_bf16 v[84:87], a[240:243], a[200:203], v[84:87]\n" \
"s_addk_i32 s84, 0x7c00\n" \
"v_mfma_f32_16x16x32_bf16 v[88:91], a[180:183], a[136:139], v[88:91]\n" \
"buffer_load_dwordx4 v27, s[68:71], s84 offen offset:3072 lds\n" \
"v_mfma_f32_16x16x32_bf16 v[88:91], a[244:247], a[200:203], v[88:91]\n" \
"s_add_u32 m0, s30, 0x0\n" \
"s_movk_i32 s84, 0x300\n" \
"s_nop 0\n" \
"v_mfma_f32_16x16x32_bf16 v[92:95], a[184:187], a[136:139], v[92:95]\n" \
"buffer_load_dwordx4 v24, s[76:79], s84 offen offset:0 lds\n" \
"v_mfma_f32_16x16x32_bf16 v[92:95], a[248:251], a[200:203], v[92:95]\n" \
"ds_read_b64_tr_b16 a[236:237], v35 offset:4096\n" \
"v_cvt_pk_bf16_f32 v210, v180, v181\n" \
"v_mfma_f32_16x16x32_bf16 v[96:99], a[188:191], a[136:139], v[96:99]\n" \
"ds_read_b64_tr_b16 a[238:239], v35 offset:4608\n" \
"v_cvt_pk_bf16_f32 v211, v182, v183\n" \
"v_mfma_f32_16x16x32_bf16 v[96:99], a[252:255], a[200:203], v[96:99]\n" \
"ds_read_b128 a[136:139], v28 offset:36864\n" \
"v_accvgpr_read_b32 v204, a100\n" \
"v_mfma_f32_16x16x32_bf16 v[112:115], a[188:191], a[140:143], v[112:115]\n" \
"ds_read_b128 a[200:203], v29 offset:36864\n" \
"v_accvgpr_read_b32 v205, a101\n" \
"v_mfma_f32_16x16x32_bf16 v[112:115], a[252:255], a[204:207], v[112:115]\n" \
"s_movk_i32 s84, 0x2300\n" \
"s_nop 0\n" \
"v_mfma_f32_16x16x32_bf16 v[108:111], a[184:187], a[140:143], v[108:111]\n" \
"buffer_load_dwordx4 v25, s[76:79], s84 offen offset:1024 lds\n" \
"v_mfma_f32_16x16x32_bf16 v[108:111], a[248:251], a[204:207], v[108:111]\n" \
"s_add_u32 s89, s42, 0x1000\n" \
"buffer_store_dwordx4 v[232:235], v20, s[80:83], s89 offen offset:1024 sc1\n" \
"v_mfma_f32_16x16x32_bf16 v[104:107], a[180:183], a[140:143], v[104:107]\n" \
"ds_read_b64_tr_b16 a[168:169], v34 offset:0\n" \
"v_accvgpr_read_b32 v206, a102\n" \
"v_mfma_f32_16x16x32_bf16 v[104:107], a[244:247], a[204:207], v[104:107]\n" \
"ds_read_b64_tr_b16 a[170:171], v34 offset:512\n" \
"v_accvgpr_read_b32 v207, a103\n" \
"v_mfma_f32_16x16x32_bf16 v[100:103], a[176:179], a[140:143], v[100:103]\n" \
"ds_read_b64_tr_b16 a[232:233], v34 offset:4096\n" \
"v_accvgpr_read_b32 v180, a116\n" \
"v_mfma_f32_16x16x32_bf16 v[100:103], a[240:243], a[204:207], v[100:103]\n" \
"ds_read_b128 a[140:143], v28 offset:38912\n" \
"v_accvgpr_read_b32 v181, a117\n" \
"v_mfma_f32_16x16x32_bf16 v[52:55], a[176:179], a[128:131], v[52:55]\n" \
"ds_read_b128 a[204:207], v29 offset:38912\n" \
"v_accvgpr_read_b32 v182, a118\n" \
"v_mfma_f32_16x16x32_bf16 v[52:55], a[240:243], a[192:195], v[52:55]\n" \
"s_movk_i32 s84, 0x4300\n" \
"v_mfma_f32_16x16x32_bf16 v[56:59], a[180:183], a[128:131], v[56:59]\n" \
"buffer_load_dwordx4 v24, s[76:79], s84 offen offset:2048 lds\n" \
"v_mfma_f32_16x16x32_bf16 v[56:59], a[244:247], a[192:195], v[56:59]\n" \
"s_movk_i32 s84, 0x6300\n" \
"v_mfma_f32_16x16x32_bf16 v[60:63], a[184:187], a[128:131], v[60:63]\n" \
"buffer_load_dwordx4 v25, s[76:79], s84 offen offset:3072 lds\n" \
"v_mfma_f32_16x16x32_bf16 v[60:63], a[248:251], a[192:195], v[60:63]\n" \
"ds_read_b64_tr_b16 a[234:235], v34 offset:4608\n" \
"v_accvgpr_read_b32 v183, a119\n" \
"v_mfma_f32_16x16x32_bf16 v[64:67], a[188:191], a[128:131], v[64:67]\n" \
"v_cvt_pk_bf16_f32 v204, v204, v205\n" \
"v_cvt_pk_bf16_f32 v205, v206, v207\n" \
"v_mfma_f32_16x16x32_bf16 v[64:67], a[252:255], a[192:195], v[64:67]\n" \
"ds_read_b128 a[128:131], v28 offset:32768\n" \
"v_cvt_pk_bf16_f32 v206, v180, v181\n" \
"v_mfma_f32_16x16x32_bf16 v[80:83], a[188:191], a[132:135], v[80:83]\n" \
"ds_read_b128 a[192:195], v29 offset:32768\n" \
"v_cvt_pk_bf16_f32 v207, v182, v183\n" \
"v_mfma_f32_16x16x32_bf16 v[80:83], a[252:255], a[196:199], v[80:83]\n" \
"v_accvgpr_read_b32 v200, a96\n" \
"v_accvgpr_read_b32 v201, a97\n" \
"v_mfma_f32_16x16x32_bf16 v[76:79], a[184:187], a[132:135], v[76:79]\n" \
"v_accvgpr_read_b32 v202, a98\n" \
"v_accvgpr_read_b32 v203, a99\n" \
"v_mfma_f32_16x16x32_bf16 v[76:79], a[248:251], a[196:199], v[76:79]\n" \
"v_accvgpr_read_b32 v180, a112\n" \
"v_accvgpr_read_b32 v181, a113\n" \
"v_mfma_f32_16x16x32_bf16 v[72:75], a[180:183], a[132:135], v[72:75]\n" \
"v_accvgpr_read_b32 v182, a114\n" \
"v_accvgpr_read_b32 v183, a115\n" \
"v_mfma_f32_16x16x32_bf16 v[72:75], a[244:247], a[196:199], v[72:75]\n" \
"v_cvt_pk_bf16_f32 v200, v200, v201\n" \
"v_cvt_pk_bf16_f32 v201, v202, v203\n" \
"v_mfma_f32_16x16x32_bf16 v[68:71], a[176:179], a[132:135], v[68:71]\n" \
"v_cvt_pk_bf16_f32 v202, v180, v181\n" \
"v_cvt_pk_bf16_f32 v203, v182, v183\n" \
"v_mfma_f32_16x16x32_bf16 v[68:71], a[240:243], a[196:199], v[68:71]\n" \
"ds_read_b128 a[132:135], v28 offset:34816\n" \
"ds_read_b128 a[196:199], v29 offset:34816\n" \
"s_waitcnt lgkmcnt(15)\n" \
"v_mfma_f32_16x16x32_bf16 a[0:3], a[160:163], a[144:147], a[0:3]\n" \
"s_add_u32 m0, s30, 0x1000\n" \
"s_addk_i32 s84, 0x3000\n" \
"v_mfma_f32_16x16x32_bf16 a[0:3], a[224:227], a[208:211], a[0:3]\n" \
"buffer_load_dwordx4 v24, s[76:79], s84 offen offset:0 lds\n" \
"v_mfma_f32_16x16x32_bf16 a[4:7], a[164:167], a[144:147], a[4:7]\n" \
"ds_read_b64_tr_b16 a[176:177], v36 offset:0\n" \
"v_accvgpr_read_b32 v196, a76\n" \
"v_mfma_f32_16x16x32_bf16 a[4:7], a[228:231], a[208:211], a[4:7]\n" \
"ds_read_b64_tr_b16 a[178:179], v36 offset:1024\n" \
"v_accvgpr_read_b32 v197, a77\n" \
"v_mfma_f32_16x16x32_bf16 a[12:15], a[172:175], a[144:147], a[12:15]\n" \
"ds_read_b64_tr_b16 a[240:241], v36 offset:8192\n" \
"v_accvgpr_read_b32 v198, a78\n" \
"s_waitcnt lgkmcnt(15)\n" \
"v_mfma_f32_16x16x32_bf16 a[12:15], a[236:239], a[208:211], a[12:15]\n" \
"s_add_u32 s88, s42, 0x1000\n" \
"buffer_store_dwordx4 v[236:239], v20, s[80:83], s88 offen offset:0 sc1\n" \
"s_waitcnt lgkmcnt(11)\n" \
"v_mfma_f32_16x16x32_bf16 a[8:11], a[144:147], a[168:171], a[8:11]\n" \
"ds_read_b64_tr_b16 a[242:243], v36 offset:9216\n" \
"v_accvgpr_read_b32 v199, a79\n" \
"s_waitcnt lgkmcnt(8)\n" \
"s_nop 0\n" \
"v_mfma_f32_16x16x32_bf16 a[8:11], a[208:211], a[232:235], a[8:11]\n" \
"ds_read_b64_tr_b16 a[180:181], v37 offset:0\n" \
"v_accvgpr_read_b32 v180, a92\n" \
"v_mfma_f32_16x16x32_bf16 a[24:27], a[148:151], a[168:171], a[24:27]\n" \
"ds_read_b64_tr_b16 a[182:183], v37 offset:1024\n" \
"v_accvgpr_read_b32 v181, a93\n" \
"v_mfma_f32_16x16x32_bf16 a[24:27], a[212:215], a[232:235], a[24:27]\n" \
"ds_read_b64_tr_b16 a[244:245], v37 offset:8192\n" \
"v_accvgpr_read_b32 v182, a94\n" \
"v_mfma_f32_16x16x32_bf16 a[28:31], a[172:175], a[148:151], a[28:31]\n" \
"v_accvgpr_read_b32 v183, a95\n" \
"v_cvt_pk_bf16_f32 v196, v196, v197\n" \
"v_mfma_f32_16x16x32_bf16 a[28:31], a[236:239], a[212:215], a[28:31]\n" \
"s_addk_i32 s84, 0x2000\n" \
"s_nop 0\n" \
"v_mfma_f32_16x16x32_bf16 a[20:23], a[164:167], a[148:151], a[20:23]\n" \
"buffer_load_dwordx4 v25, s[76:79], s84 offen offset:1024 lds\n" \
"v_mfma_f32_16x16x32_bf16 a[20:23], a[228:231], a[212:215], a[20:23]\n" \
"ds_read_b64_tr_b16 a[246:247], v37 offset:9216\n" \
"v_cvt_pk_bf16_f32 v197, v198, v199\n" \
"v_mfma_f32_16x16x32_bf16 a[16:19], a[160:163], a[148:151], a[16:19]\n" \
"ds_read_b64_tr_b16 a[184:185], v38 offset:0\n" \
"v_cvt_pk_bf16_f32 v198, v180, v181\n" \
"v_mfma_f32_16x16x32_bf16 a[16:19], a[224:227], a[212:215], a[16:19]\n" \
"ds_read_b64_tr_b16 a[186:187], v38 offset:1024\n" \
"v_cvt_pk_bf16_f32 v199, v182, v183\n" \
"v_mfma_f32_16x16x32_bf16 a[32:35], a[160:163], a[152:155], a[32:35]\n" \
"s_addk_i32 s84, 0x2000\n" \
"s_nop 0\n" \
".fill (L_pb + 69344 - .) / 4, 4, 0xBF800000\n" \
"L_pp17:\n" \
"s_cbranch_execz L_pret\n" \
"s_nop 0\n" \
"v_mfma_f32_16x16x32_bf16 a[32:35], a[224:227], a[216:219], a[32:35]\n" \
"buffer_load_dwordx4 v24, s[76:79], s84 offen offset:2048 lds\n" \
"v_mfma_f32_16x16x32_bf16 a[36:39], a[164:167], a[152:155], a[36:39]\n" \
"ds_read_b64_tr_b16 a[248:249], v38 offset:8192\n" \
"v_accvgpr_read_b32 v192, a72\n" \
"v_mfma_f32_16x16x32_bf16 a[36:39], a[228:231], a[216:219], a[36:39]\n" \
"ds_read_b64_tr_b16 a[250:251], v38 offset:9216\n" \
"v_accvgpr_read_b32 v193, a73\n" \
"v_mfma_f32_16x16x32_bf16 a[44:47], a[172:175], a[152:155], a[44:47]\n" \
"ds_read_b64_tr_b16 a[188:189], v39 offset:0\n" \
"v_accvgpr_read_b32 v194, a74\n" \
"v_mfma_f32_16x16x32_bf16 a[44:47], a[236:239], a[216:219], a[44:47]\n" \
"ds_read_b64_tr_b16 a[190:191], v39 offset:1024\n" \
"v_accvgpr_read_b32 v195, a75\n" \
"v_mfma_f32_16x16x32_bf16 a[40:43], a[152:155], a[168:171], a[40:43]\n" \
"s_add_u32 s89, s42, 0x3000\n" \
"buffer_store_dwordx4 v[240:243], v20, s[80:83], s89 offen offset:3072 sc1\n" \
"v_mfma_f32_16x16x32_bf16 a[40:43], a[216:219], a[232:235], a[40:43]\n" \
"ds_read_b64_tr_b16 a[252:253], v39 offset:8192\n" \
"v_accvgpr_read_b32 v180, a88\n" \
"v_mfma_f32_16x16x32_bf16 a[56:59], a[156:159], a[168:171], a[56:59]\n" \
"ds_read_b64_tr_b16 a[254:255], v39 offset:9216\n" \
"v_accvgpr_read_b32 v181, a89\n" \
"v_mfma_f32_16x16x32_bf16 a[56:59], a[220:223], a[232:235], a[56:59]\n" \
"v_accvgpr_read_b32 v182, a90\n" \
"v_accvgpr_read_b32 v183, a91\n" \
"v_mfma_f32_16x16x32_bf16 a[60:63], a[172:175], a[156:159], a[60:63]\n" \
"v_cvt_pk_bf16_f32 v192, v192, v193\n" \
"v_cvt_pk_bf16_f32 v193, v194, v195\n" \
"v_mfma_f32_16x16x32_bf16 a[60:63], a[236:239], a[220:223], a[60:63]\n" \
"s_addk_i32 s84, 0x2000\n" \
"v_mfma_f32_16x16x32_bf16 a[52:55], a[164:167], a[156:159], a[52:55]\n" \
"buffer_load_dwordx4 v25, s[76:79], s84 offen offset:3072 lds\n" \
"v_mfma_f32_16x16x32_bf16 a[52:55], a[228:231], a[220:223], a[52:55]\n" \
"v_cvt_pk_bf16_f32 v194, v180, v181\n" \
"v_cvt_pk_bf16_f32 v195, v182, v183\n" \
"v_mfma_f32_16x16x32_bf16 a[48:51], a[160:163], a[156:159], a[48:51]\n" \
"s_and_b32 s12, s2, 1\n" \
"v_mfma_f32_16x16x32_bf16 a[48:51], a[224:227], a[220:223], a[48:51]\n" \
"s_cmp_lg_u32 s12, 0\n" \
"s_cselect_b32 s13, s16, s13\n" \
"v_mfma_f32_16x16x32_bf16 a[64:67], a[164:167], a[140:143], a[64:67]\n" \
"s_bfe_u32 s16, s2, 0x20001\n" \
"s_addk_i32 s13, 0x4000\n" \
"v_mfma_f32_16x16x32_bf16 a[64:67], a[228:231], a[204:207], a[64:67]\n" \
"s_xor_b32 s16, s16, s3\n" \
"v_mfma_f32_16x16x32_bf16 a[80:83], a[172:175], a[140:143], a[80:83]\n" \
"s_cmp_lg_u32 s16, 0\n" \
"s_cselect_b32 s13, s7, s13\n" \
"v_mfma_f32_16x16x32_bf16 a[80:83], a[236:239], a[204:207], a[80:83]\n" \
"s_lshl_b32 s6, s6, 16\n" \
"v_writelane_b32 v11, s13, 40\n" \
"v_mfma_f32_16x16x32_bf16 a[68:71], a[140:143], a[168:171], a[68:71]\n" \
"s_add_i32 s13, s8, 0x4240000\n" \
"s_add_i32 s6, s13, s6\n" \
"v_mfma_f32_16x16x32_bf16 a[68:71], a[204:207], a[232:235], a[68:71]\n" \
"s_waitcnt vmcnt(11) lgkmcnt(0)\n" \
"s_barrier\n" \
"v_mov_b32_e32 v45, s62\n" \
"s_mov_b64 exec, 1\n" \
"buffer_store_dword v45, off, s[80:83], s37 offset:0 sc1\n" \
"s_mov_b64 exec, -1\n" \
"v_xor_b32_e32 v28, 0x20000, v28\n" \
"v_xor_b32_e32 v29, 0x20000, v29\n" \
"v_mfma_f32_16x16x32_bf16 v[116:119], a[176:179], a[144:147], v[116:119]\n" \
"s_add_u32 m0, s40, 0x0\n" \
"s_add_u32 s85, s39, 0xc000\n" \
"v_mfma_f32_16x16x32_bf16 v[116:119], a[240:243], a[208:211], v[116:119]\n" \
"buffer_load_dwordx4 v20, s[80:83], s85 offen sc1 lds\n" \
"v_mfma_f32_16x16x32_bf16 v[120:123], a[180:183], a[144:147], v[120:123]\n" \
"ds_read_b64_tr_b16 a[160:161], v32 offset:32768\n" \
"v_writelane_b32 v11, s6, 41\n" \
"v_mfma_f32_16x16x32_bf16 v[120:123], a[244:247], a[208:211], v[120:123]\n" \
"ds_read_b64_tr_b16 a[162:163], v32 offset:33280\n" \
"s_add_i32 s8, s8, 0x42c0000\n" \
"v_mfma_f32_16x16x32_bf16 v[124:127], a[184:187], a[144:147], v[124:127]\n" \
"s_add_u32 m0, s40, 0x1000\n" \
"s_add_u32 s85, s39, 0xd000\n" \
"v_mfma_f32_16x16x32_bf16 v[124:127], a[248:251], a[208:211], v[124:127]\n" \
"buffer_load_dwordx4 v20, s[80:83], s85 offen sc1 lds\n" \
"v_mfma_f32_16x16x32_bf16 v[128:131], a[188:191], a[144:147], v[128:131]\n" \
"ds_read_b64_tr_b16 a[224:225], v32 offset:36864\n" \
"s_lshl_b32 s6, s12, 10\n" \
"v_mfma_f32_16x16x32_bf16 v[128:131], a[252:255], a[208:211], v[128:131]\n" \
"ds_read_b128 a[144:147], v28 offset:8192\n" \
"s_or_b32 s6, s8, s6\n" \
"v_mfma_f32_16x16x32_bf16 v[144:147], a[188:191], a[148:151], v[144:147]\n" \
"ds_read_b128 a[208:211], v29 offset:8192\n" \
"v_mfma_f32_16x16x32_bf16 v[144:147], a[252:255], a[212:215], v[144:147]\n" \
"s_add_u32 m0, s40, 0x3000\n" \
"s_movk_i32 s84, 0x300\n" \
"v_mfma_f32_16x16x32_bf16 v[140:143], a[184:187], a[148:151], v[140:143]\n" \
"buffer_load_dwordx4 v21, s[76:79], s84 offen offset:0 lds\n" \
"v_mfma_f32_16x16x32_bf16 v[140:143], a[248:251], a[212:215], v[140:143]\n" \
"ds_read_b64_tr_b16 a[226:227], v32 offset:37376\n" \
"v_mfma_f32_16x16x32_bf16 v[136:139], a[180:183], a[148:151], v[136:139]\n" \
"s_cmp_lg_u32 s16, 0\n" \
"s_cselect_b32 s6, s7, s6\n" \
"v_mfma_f32_16x16x32_bf16 v[136:139], a[244:247], a[212:215], v[136:139]\n" \
"ds_read_b64_tr_b16 a[164:165], v33 offset:32768\n" \
"s_mulk_i32 s4, 0x4800\n" \
"v_mfma_f32_16x16x32_bf16 v[132:135], a[176:179], a[148:151], v[132:135]\n" \
"v_writelane_b32 v11, s6, 42\n" \
"s_add_i32 s6, s14, s10\n" \
"v_mfma_f32_16x16x32_bf16 v[132:135], a[240:243], a[212:215], v[132:135]\n" \
"ds_read_b128 a[148:151], v28 offset:10240\n" \
"v_writelane_b32 v11, s6, 43\n" \
"v_mfma_f32_16x16x32_bf16 v[148:151], a[176:179], a[152:155], v[148:151]\n" \
"ds_read_b128 a[212:215], v29 offset:10240\n" \
"s_add_i32 s6, s9, 0x21c000\n" \
"v_mfma_f32_16x16x32_bf16 v[148:151], a[240:243], a[216:219], v[148:151]\n" \
"s_add_u32 m0, s31, 0x0\n" \
"s_mov_b32 s84, 0x300000\n" \
"v_mfma_f32_16x16x32_bf16 v[152:155], a[180:183], a[152:155], v[152:155]\n" \
"buffer_load_dwordx4 v26, s[68:71], s84 offen offset:0 lds\n" \
"v_mfma_f32_16x16x32_bf16 v[152:155], a[244:247], a[216:219], v[152:155]\n" \
"s_addk_i32 s84, 0x7c00\n" \
"v_mfma_f32_16x16x32_bf16 v[156:159], a[184:187], a[152:155], v[156:159]\n" \
"buffer_load_dwordx4 v26, s[68:71], s84 offen offset:1024 lds\n" \
"v_mfma_f32_16x16x32_bf16 v[156:159], a[248:251], a[216:219], v[156:159]\n" \
"ds_read_b64_tr_b16 a[166:167], v33 offset:33280\n" \
"s_add_i32 s9, s6, s10\n" \
"v_mfma_f32_16x16x32_bf16 v[160:163], a[188:191], a[152:155], v[160:163]\n" \
"ds_read_b64_tr_b16 a[228:229], v33 offset:36864\n" \
"v_mfma_f32_16x16x32_bf16 v[160:163], a[252:255], a[216:219], v[160:163]\n" \
"ds_read_b128 a[152:155], v28 offset:12288\n" \
"v_mfma_f32_16x16x32_bf16 v[176:179], a[188:191], a[156:159], v[176:179]\n" \
"ds_read_b128 a[216:219], v29 offset:12288\n" \
"v_mfma_f32_16x16x32_bf16 v[176:179], a[252:255], a[220:223], v[176:179]\n" \
"s_addk_i32 s84, 0x7c00\n" \
"v_mfma_f32_16x16x32_bf16 v[172:175], a[184:187], a[156:159], v[172:175]\n" \
"buffer_load_dwordx4 v27, s[68:71], s84 offen offset:2048 lds\n" \
"v_mfma_f32_16x16x32_bf16 v[172:175], a[248:251], a[220:223], v[172:175]\n" \
"ds_read_b64_tr_b16 a[230:231], v33 offset:37376\n" \
"v_mfma_f32_16x16x32_bf16 v[168:171], a[180:183], a[156:159], v[168:171]\n" \
"s_addk_i32 s84, 0x7c00\n" \
"v_mfma_f32_16x16x32_bf16 v[168:171], a[244:247], a[220:223], v[168:171]\n" \
"buffer_load_dwordx4 v27, s[68:71], s84 offen offset:3072 lds\n" \
"v_mfma_f32_16x16x32_bf16 v[164:167], a[176:179], a[156:159], v[164:167]\n" \
"ds_read_b64_tr_b16 a[172:173], v35 offset:32768\n" \
"v_mfma_f32_16x16x32_bf16 v[164:167], a[240:243], a[220:223], v[164:167]\n" \
"ds_read_b128 a[156:159], v28 offset:14336\n" \
"v_mfma_f32_16x16x32_bf16 v[84:87], a[176:179], a[136:139], v[84:87]\n" \
"ds_read_b128 a[220:223], v29 offset:14336\n" \
"v_mfma_f32_16x16x32_bf16 v[84:87], a[240:243], a[200:203], v[84:87]\n" \
"s_add_u32 s88, s42, 0x3000\n" \
"buffer_store_dwordx4 v[244:247], v20, s[80:83], s88 offen offset:2048 sc1\n" \
"v_mfma_f32_16x16x32_bf16 v[88:91], a[180:183], a[136:139], v[88:91]\n" \
"ds_read_b64_tr_b16 a[174:175], v35 offset:33280\n" \
"v_mfma_f32_16x16x32_bf16 v[88:91], a[244:247], a[200:203], v[88:91]\n" \
"ds_read_b64_tr_b16 a[236:237], v35 offset:36864\n" \
"v_mfma_f32_16x16x32_bf16 v[92:95], a[184:187], a[136:139], v[92:95]\n" \
"ds_read_b64_tr_b16 a[238:239], v35 offset:37376\n" \
"v_mfma_f32_16x16x32_bf16 v[92:95], a[248:251], a[200:203], v[92:95]\n" \
"s_add_u32 m0, s30, 0x8000\n" \
"s_movk_i32 s84, 0x380\n" \
"v_mfma_f32_16x16x32_bf16 v[96:99], a[188:191], a[136:139], v[96:99]\n" \
"buffer_load_dwordx4 v24, s[76:79], s84 offen offset:0 lds\n" \
"v_mfma_f32_16x16x32_bf16 v[96:99], a[252:255], a[200:203], v[96:99]\n" \
"ds_read_b128 a[136:139], v28 offset:4096\n" \
"v_mfma_f32_16x16x32_bf16 v[112:115], a[188:191], a[140:143], v[112:115]\n" \
"ds_read_b128 a[200:203], v29 offset:4096\n" \
"v_mfma_f32_16x16x32_bf16 v[112:115], a[252:255], a[204:207], v[112:115]\n" \
"ds_read_b64_tr_b16 a[168:169], v34 offset:32768\n" \
"v_mfma_f32_16x16x32_bf16 v[108:111], a[184:187], a[140:143], v[108:111]\n" \
"s_movk_i32 s84, 0x2380\n" \
"v_mfma_f32_16x16x32_bf16 v[108:111], a[248:251], a[204:207], v[108:111]\n" \
"buffer_load_dwordx4 v25, s[76:79], s84 offen offset:1024 lds\n" \
"v_mfma_f32_16x16x32_bf16 v[104:107], a[180:183], a[140:143], v[104:107]\n" \
"ds_read_b64_tr_b16 a[170:171], v34 offset:33280\n" \
"v_mfma_f32_16x16x32_bf16 v[104:107], a[244:247], a[204:207], v[104:107]\n" \
"ds_read_b64_tr_b16 a[232:233], v34 offset:36864\n" \
"v_mfma_f32_16x16x32_bf16 v[100:103], a[176:179], a[140:143], v[100:103]\n" \
"s_cmp_lg_u32 s12, 0\n" \
"s_cselect_b32 s6, s6, s14\n" \
"v_mfma_f32_16x16x32_bf16 v[100:103], a[240:243], a[204:207], v[100:103]\n" \
"ds_read_b128 a[140:143], v28 offset:6144\n" \
"s_addk_i32 s6, 0x100\n" \
"v_mfma_f32_16x16x32_bf16 v[52:55], a[176:179], a[128:131], v[52:55]\n" \
"ds_read_b128 a[204:207], v29 offset:6144\n" \
"v_writelane_b32 v11, s9, 44\n" \
"v_mfma_f32_16x16x32_bf16 v[52:55], a[240:243], a[192:195], v[52:55]\n" \
"s_movk_i32 s84, 0x4380\n" \
"v_mfma_f32_16x16x32_bf16 v[56:59], a[180:183], a[128:131], v[56:59]\n" \
"buffer_load_dwordx4 v24, s[76:79], s84 offen offset:2048 lds\n" \
"v_mfma_f32_16x16x32_bf16 v[56:59], a[244:247], a[192:195], v[56:59]\n" \
"s_movk_i32 s84, 0x6380\n" \
"v_mfma_f32_16x16x32_bf16 v[60:63], a[184:187], a[128:131], v[60:63]\n" \
"buffer_load_dwordx4 v25, s[76:79], s84 offen offset:3072 lds\n" \
"v_mfma_f32_16x16x32_bf16 v[60:63], a[248:251], a[192:195], v[60:63]\n" \
"ds_read_b64_tr_b16 a[234:235], v34 offset:37376\n" \
"v_mfma_f32_16x16x32_bf16 v[64:67], a[188:191], a[128:131], v[64:67]\n" \
"s_cmp_lg_u32 s16, 0\n" \
"s_cselect_b32 s6, s7, s6\n" \
"v_mfma_f32_16x16x32_bf16 v[64:67], a[252:255], a[192:195], v[64:67]\n" \
"ds_read_b128 a[128:131], v28 offset:0\n" \
"v_writelane_b32 v11, s6, 45\n" \
"v_mfma_f32_16x16x32_bf16 v[80:83], a[188:191], a[132:135], v[80:83]\n" \
"ds_read_b128 a[192:195], v29 offset:0\n" \
"s_or_b32 s6, s8, s15\n" \
"v_mfma_f32_16x16x32_bf16 v[80:83], a[252:255], a[196:199], v[80:83]\n" \
"v_writelane_b32 v11, s6, 53\n" \
"s_lshl_b32 s6, s3, 17\n" \
"v_mfma_f32_16x16x32_bf16 v[76:79], a[184:187], a[132:135], v[76:79]\n" \
"s_add_i32 s6, s13, s6\n" \
"s_or_b32 s5, s6, s5\n" \
"v_mfma_f32_16x16x32_bf16 v[76:79], a[248:251], a[196:199], v[76:79]\n" \
"s_or_b32 s5, s5, 0x3840\n" \
"v_writelane_b32 v11, s5, 6\n" \
"v_mfma_f32_16x16x32_bf16 v[72:75], a[180:183], a[132:135], v[72:75]\n" \
"s_mov_b32 s5, 0xff0000\n" \
"v_mfma_f32_16x16x32_bf16 v[72:75], a[244:247], a[196:199], v[72:75]\n" \
"s_cmp_lg_u32 s16, 0\n" \
"s_cselect_b32 s5, s17, s5\n" \
"v_mfma_f32_16x16x32_bf16 v[68:71], a[176:179], a[132:135], v[68:71]\n" \
"v_writelane_b32 v11, s5, 7\n" \
"s_mul_i32 s5, s3, 0x1200\n" \
"v_mfma_f32_16x16x32_bf16 v[68:71], a[240:243], a[196:199], v[68:71]\n" \
"ds_read_b128 a[132:135], v28 offset:2048\n" \
"ds_read_b128 a[196:199], v29 offset:2048\n" \
"s_waitcnt lgkmcnt(15)\n" \
"v_mfma_f32_16x16x32_bf16 a[0:3], a[160:163], a[144:147], a[0:3]\n" \
"s_add_u32 m0, s30, 0x9000\n" \
"s_addk_i32 s84, 0x3000\n" \
"v_mfma_f32_16x16x32_bf16 a[0:3], a[224:227], a[208:211], a[0:3]\n" \
"buffer_load_dwordx4 v24, s[76:79], s84 offen offset:0 lds\n" \
"v_mfma_f32_16x16x32_bf16 a[4:7], a[164:167], a[144:147], a[4:7]\n" \
"ds_read_b64_tr_b16 a[176:177], v36 offset:32768\n" \
"s_add_i32 s4, s5, s4\n" \
"v_mfma_f32_16x16x32_bf16 a[4:7], a[228:231], a[208:211], a[4:7]\n" \
"ds_read_b64_tr_b16 a[178:179], v36 offset:33792\n" \
"s_add_i32 s4, s4, s11\n" \
"s_waitcnt lgkmcnt(15)\n" \
"v_mfma_f32_16x16x32_bf16 a[12:15], a[172:175], a[144:147], a[12:15]\n" \
"ds_read_b64_tr_b16 a[240:241], v36 offset:40960\n" \
"v_writelane_b32 v11, s4, 8\n" \
"s_waitcnt lgkmcnt(15)\n" \
"v_mfma_f32_16x16x32_bf16 a[12:15], a[236:239], a[208:211], a[12:15]\n" \
"s_add_u32 s89, s42, 0x3000\n" \
"buffer_store_dwordx4 v[184:187], v20, s[80:83], s89 offen offset:1024 sc1\n" \
"s_waitcnt lgkmcnt(11)\n" \
"v_mfma_f32_16x16x32_bf16 a[8:11], a[144:147], a[168:171], a[8:11]\n" \
"ds_read_b64_tr_b16 a[242:243], v36 offset:41984\n" \
"s_lshl_b32 s4, s2, 16\n" \
"s_waitcnt lgkmcnt(8)\n" \
"v_mfma_f32_16x16x32_bf16 a[8:11], a[208:211], a[232:235], a[8:11]\n" \
"ds_read_b64_tr_b16 a[180:181], v37 offset:32768\n" \
"s_add_i32 s5, s4, 0x10000\n" \
"v_mfma_f32_16x16x32_bf16 a[24:27], a[148:151], a[168:171], a[24:27]\n" \
"ds_read_b64_tr_b16 a[182:183], v37 offset:33792\n" \
"s_and_b32 s5, s5, 0x70000\n" \
"v_mfma_f32_16x16x32_bf16 a[24:27], a[212:215], a[232:235], a[24:27]\n" \
"ds_read_b64_tr_b16 a[244:245], v37 offset:40960\n" \
"s_add_i32 s5, s13, s5\n" \
"v_mfma_f32_16x16x32_bf16 a[28:31], a[172:175], a[148:151], a[28:31]\n" \
"v_writelane_b32 v11, s5, 46\n" \
"s_add_i32 s5, s4, 0x20000\n" \
"v_mfma_f32_16x16x32_bf16 a[28:31], a[236:239], a[212:215], a[28:31]\n" \
"s_addk_i32 s84, 0x2000\n" \
"v_mfma_f32_16x16x32_bf16 a[20:23], a[164:167], a[148:151], a[20:23]\n" \
"buffer_load_dwordx4 v25, s[76:79], s84 offen offset:1024 lds\n" \
"v_mfma_f32_16x16x32_bf16 a[20:23], a[228:231], a[212:215], a[20:23]\n" \
"ds_read_b64_tr_b16 a[246:247], v37 offset:41984\n" \
"s_and_b32 s5, s5, 0x70000\n" \
"v_mfma_f32_16x16x32_bf16 a[16:19], a[160:163], a[148:151], a[16:19]\n" \
"ds_read_b64_tr_b16 a[184:185], v38 offset:32768\n" \
"s_add_i32 s5, s13, s5\n" \
"v_mfma_f32_16x16x32_bf16 a[16:19], a[224:227], a[212:215], a[16:19]\n" \
"ds_read_b64_tr_b16 a[186:187], v38 offset:33792\n" \
"s_bitset1_b32 s5, 11\n" \
"v_mfma_f32_16x16x32_bf16 a[32:35], a[160:163], a[152:155], a[32:35]\n" \
"s_addk_i32 s84, 0x2000\n" \
"s_nop 0\n" \
"v_mfma_f32_16x16x32_bf16 a[32:35], a[224:227], a[216:219], a[32:35]\n" \
"buffer_load_dwordx4 v24, s[76:79], s84 offen offset:2048 lds\n" \
"v_mfma_f32_16x16x32_bf16 a[36:39], a[164:167], a[152:155], a[36:39]\n" \
"ds_read_b64_tr_b16 a[248:249], v38 offset:40960\n" \
"v_writelane_b32 v11, s5, 47\n" \
"v_mfma_f32_16x16x32_bf16 a[36:39], a[228:231], a[216:219], a[36:39]\n" \
"ds_read_b64_tr_b16 a[250:251], v38 offset:41984\n" \
"s_add_i32 s5, s4, 0x30000\n" \
"v_mfma_f32_16x16x32_bf16 a[44:47], a[172:175], a[152:155], a[44:47]\n" \
"ds_read_b64_tr_b16 a[188:189], v39 offset:32768\n" \
"s_and_b32 s5, s5, 0x70000\n" \
"v_mfma_f32_16x16x32_bf16 a[44:47], a[236:239], a[216:219], a[44:47]\n" \
"ds_read_b64_tr_b16 a[190:191], v39 offset:33792\n" \
"s_add_i32 s5, s13, s5\n" \
"v_mfma_f32_16x16x32_bf16 a[40:43], a[152:155], a[168:171], a[40:43]\n" \
"s_add_u32 s88, s42, 0x3000\n" \
"buffer_store_dwordx4 v[188:191], v20, s[80:83], s88 offen offset:0 sc1\n" \
"v_mfma_f32_16x16x32_bf16 a[40:43], a[216:219], a[232:235], a[40:43]\n" \
"ds_read_b64_tr_b16 a[252:253], v39 offset:40960\n" \
"s_bitset1_b32 s5, 12\n" \
"v_mfma_f32_16x16x32_bf16 a[56:59], a[156:159], a[168:171], a[56:59]\n" \
"ds_read_b64_tr_b16 a[254:255], v39 offset:41984\n" \
"v_writelane_b32 v11, s5, 48\n" \
"v_mfma_f32_16x16x32_bf16 a[56:59], a[220:223], a[232:235], a[56:59]\n" \
"s_and_b32 s5, s4, 0x70000\n" \
"s_xor_b32 s5, s5, 0x40000\n" \
"v_mfma_f32_16x16x32_bf16 a[60:63], a[172:175], a[156:159], a[60:63]\n" \
"s_add_i32 s5, s13, s5\n" \
"s_or_b32 s5, s5, 0x1800\n" \
"v_mfma_f32_16x16x32_bf16 a[60:63], a[236:239], a[220:223], a[60:63]\n" \
"s_addk_i32 s84, 0x2000\n" \
"v_mfma_f32_16x16x32_bf16 a[52:55], a[164:167], a[156:159], a[52:55]\n" \
"buffer_load_dwordx4 v25, s[76:79], s84 offen offset:3072 lds\n" \
"v_mfma_f32_16x16x32_bf16 a[52:55], a[228:231], a[220:223], a[52:55]\n" \
"v_writelane_b32 v11, s5, 49\n" \
"s_add_i32 s5, s4, 0x50000\n" \
"v_mfma_f32_16x16x32_bf16 a[48:51], a[160:163], a[156:159], a[48:51]\n" \
"s_and_b32 s5, s5, 0x70000\n" \
"s_add_i32 s5, s13, s5\n" \
"v_mfma_f32_16x16x32_bf16 a[48:51], a[224:227], a[220:223], a[48:51]\n" \
"s_bitset1_b32 s5, 13\n" \
"v_writelane_b32 v11, s5, 50\n" \
"v_mfma_f32_16x16x32_bf16 a[64:67], a[164:167], a[140:143], a[64:67]\n" \
"s_add_i32 s5, s4, 0x60000\n" \
"s_and_b32 s5, s5, 0x70000\n" \
"v_mfma_f32_16x16x32_bf16 a[64:67], a[228:231], a[204:207], a[64:67]\n" \
"s_add_i32 s4, s4, 0x70000\n" \
"s_add_i32 s5, s13, s5\n" \
"v_mfma_f32_16x16x32_bf16 a[80:83], a[172:175], a[140:143], a[80:83]\n" \
"s_and_b32 s4, s4, 0x70000\n" \
"s_or_b32 s5, s5, 0x2800\n" \
"v_mfma_f32_16x16x32_bf16 a[80:83], a[236:239], a[204:207], a[80:83]\n" \
"v_writelane_b32 v11, s5, 51\n" \
"s_add_i32 s13, s13, s4\n" \
"v_mfma_f32_16x16x32_bf16 a[68:71], a[140:143], a[168:171], a[68:71]\n" \
"s_or_b32 s4, s13, 0x3000\n" \
"v_writelane_b32 v11, s4, 52\n" \
"v_mfma_f32_16x16x32_bf16 a[68:71], a[204:207], a[232:235], a[68:71]\n" \
"s_waitcnt vmcnt(11) lgkmcnt(0)\n" \
"s_barrier\n" \
"v_xor_b32_e32 v28, 0x20000, v28\n" \
"v_xor_b32_e32 v29, 0x20000, v29\n" \
"v_mfma_f32_16x16x32_bf16 v[116:119], a[176:179], a[144:147], v[116:119]\n" \
"s_add_u32 m0, s40, 0x8000\n" \
"s_add_u32 s85, s39, 0xe000\n" \
"v_mfma_f32_16x16x32_bf16 v[116:119], a[240:243], a[208:211], v[116:119]\n" \
"buffer_load_dwordx4 v20, s[80:83], s85 offen sc1 lds\n" \
"v_mfma_f32_16x16x32_bf16 v[120:123], a[180:183], a[144:147], v[120:123]\n" \
"ds_read_b64_tr_b16 a[160:161], v32 offset:0\n" \
"s_movk_i32 s11, 0x4000\n" \
"v_mfma_f32_16x16x32_bf16 v[120:123], a[244:247], a[208:211], v[120:123]\n" \
"ds_read_b64_tr_b16 a[162:163], v32 offset:512\n" \
"s_lshl_b32 s4, s2, 2\n" \
"v_mfma_f32_16x16x32_bf16 v[124:127], a[184:187], a[144:147], v[124:127]\n" \
"s_add_u32 m0, s40, 0x9000\n" \
"s_add_u32 s85, s39, 0xf000\n" \
"v_mfma_f32_16x16x32_bf16 v[124:127], a[248:251], a[208:211], v[124:127]\n" \
"buffer_load_dwordx4 v20, s[80:83], s85 offen sc1 lds\n" \
"v_mfma_f32_16x16x32_bf16 v[128:131], a[188:191], a[144:147], v[128:131]\n" \
"ds_read_b64_tr_b16 a[224:225], v32 offset:4096\n" \
"s_and_b32 s4, s4, 28\n" \
"v_mfma_f32_16x16x32_bf16 v[128:131], a[252:255], a[208:211], v[128:131]\n" \
"ds_read_b128 a[144:147], v28 offset:8192\n" \
"s_bfe_u32 s5, s2, 0x20003\n" \
"v_mfma_f32_16x16x32_bf16 v[144:147], a[188:191], a[148:151], v[144:147]\n" \
"ds_read_b128 a[208:211], v29 offset:8192\n" \
"s_or_b32 s4, s4, s5\n" \
"v_mfma_f32_16x16x32_bf16 v[144:147], a[252:255], a[212:215], v[144:147]\n" \
"s_add_u32 m0, s40, 0xb000\n" \
"s_movk_i32 s84, 0x380\n" \
"v_mfma_f32_16x16x32_bf16 v[140:143], a[184:187], a[148:151], v[140:143]\n" \
"buffer_load_dwordx4 v21, s[76:79], s84 offen offset:0 lds\n" \
"v_mfma_f32_16x16x32_bf16 v[140:143], a[248:251], a[212:215], v[140:143]\n" \
"ds_read_b64_tr_b16 a[226:227], v32 offset:4608\n" \
"s_lshr_b32 s5, s2, 5\n" \
"v_mfma_f32_16x16x32_bf16 v[136:139], a[180:183], a[148:151], v[136:139]\n" \
"s_lshr_b32 s19, s3, 1\n" \
"s_lshl_b32 s18, s5, 1\n" \
"v_mfma_f32_16x16x32_bf16 v[136:139], a[244:247], a[212:215], v[136:139]\n" \
"ds_read_b64_tr_b16 a[164:165], v33 offset:0\n" \
"s_lshl_b32 s6, s4, 20\n" \
"v_mfma_f32_16x16x32_bf16 v[132:135], a[176:179], a[148:151], v[132:135]\n" \
"s_lshl_b32 s7, s4, 8\n" \
"s_and_b32 s13, s3, 1\n" \
"v_mfma_f32_16x16x32_bf16 v[132:135], a[240:243], a[212:215], v[132:135]\n" \
"ds_read_b128 a[148:151], v28 offset:10240\n" \
"s_add_i32 s14, s18, s19\n" \
"v_mfma_f32_16x16x32_bf16 v[148:151], a[176:179], a[152:155], v[148:151]\n" \
"ds_read_b128 a[212:215], v29 offset:10240\n" \
"v_mfma_f32_16x16x32_bf16 v[148:151], a[240:243], a[216:219], v[148:151]\n" \
"s_add_u32 m0, s31, 0x8000\n" \
"s_mov_b32 s84, 0x380000\n" \
"v_mfma_f32_16x16x32_bf16 v[152:155], a[180:183], a[152:155], v[152:155]\n" \
"buffer_load_dwordx4 v26, s[68:71], s84 offen offset:0 lds\n" \
"v_mfma_f32_16x16x32_bf16 v[152:155], a[244:247], a[216:219], v[152:155]\n" \
"s_addk_i32 s84, 0x7c00\n" \
"s_nop 0\n" \
"v_mfma_f32_16x16x32_bf16 v[156:159], a[184:187], a[152:155], v[156:159]\n" \
"buffer_load_dwordx4 v26, s[68:71], s84 offen offset:1024 lds\n" \
"v_mfma_f32_16x16x32_bf16 v[156:159], a[248:251], a[216:219], v[156:159]\n" \
"ds_read_b64_tr_b16 a[166:167], v33 offset:512\n" \
"v_mfma_f32_16x16x32_bf16 v[160:163], a[188:191], a[152:155], v[160:163]\n" \
"ds_read_b64_tr_b16 a[228:229], v33 offset:4096\n" \
"v_mfma_f32_16x16x32_bf16 v[160:163], a[252:255], a[216:219], v[160:163]\n" \
"ds_read_b128 a[152:155], v28 offset:12288\n" \
"v_mfma_f32_16x16x32_bf16 v[176:179], a[188:191], a[156:159], v[176:179]\n" \
"ds_read_b128 a[216:219], v29 offset:12288\n" \
"v_mfma_f32_16x16x32_bf16 v[176:179], a[252:255], a[220:223], v[176:179]\n" \
"s_addk_i32 s84, 0x7c00\n" \
"v_mfma_f32_16x16x32_bf16 v[172:175], a[184:187], a[156:159], v[172:175]\n" \
"buffer_load_dwordx4 v27, s[68:71], s84 offen offset:2048 lds\n" \
"v_mfma_f32_16x16x32_bf16 v[172:175], a[248:251], a[220:223], v[172:175]\n" \
"ds_read_b64_tr_b16 a[230:231], v33 offset:4608\n" \
"v_mfma_f32_16x16x32_bf16 v[168:171], a[180:183], a[156:159], v[168:171]\n" \
"s_addk_i32 s84, 0x7c00\n" \
"v_mfma_f32_16x16x32_bf16 v[168:171], a[244:247], a[220:223], v[168:171]\n" \
"buffer_load_dwordx4 v27, s[68:71], s84 offen offset:3072 lds\n" \
"v_mfma_f32_16x16x32_bf16 v[164:167], a[176:179], a[156:159], v[164:167]\n" \
"ds_read_b64_tr_b16 a[172:173], v35 offset:0\n" \
"v_mfma_f32_16x16x32_bf16 v[164:167], a[240:243], a[220:223], v[164:167]\n" \
"ds_read_b128 a[156:159], v28 offset:14336\n" \
"v_mfma_f32_16x16x32_bf16 v[84:87], a[176:179], a[136:139], v[84:87]\n" \
"ds_read_b128 a[220:223], v29 offset:14336\n" \
"v_mfma_f32_16x16x32_bf16 v[84:87], a[240:243], a[200:203], v[84:87]\n" \
"s_add_u32 s89, s42, 0x0\n" \
"buffer_store_dwordx4 v[192:195], v20, s[80:83], s89 offen offset:2048 sc1\n" \
"v_mfma_f32_16x16x32_bf16 v[88:91], a[180:183], a[136:139], v[88:91]\n" \
"ds_read_b64_tr_b16 a[174:175], v35 offset:512\n" \
"v_mfma_f32_16x16x32_bf16 v[88:91], a[244:247], a[200:203], v[88:91]\n" \
"ds_read_b64_tr_b16 a[236:237], v35 offset:4096\n" \
"v_mfma_f32_16x16x32_bf16 v[92:95], a[184:187], a[136:139], v[92:95]\n" \
"ds_read_b64_tr_b16 a[238:239], v35 offset:4608\n" \
"v_mfma_f32_16x16x32_bf16 v[92:95], a[248:251], a[200:203], v[92:95]\n" \
"s_add_u32 m0, s30, 0x20000\n" \
"s_movk_i32 s84, 0x400\n" \
"v_mfma_f32_16x16x32_bf16 v[96:99], a[188:191], a[136:139], v[96:99]\n" \
"buffer_load_dwordx4 v24, s[76:79], s84 offen offset:0 lds\n" \
"v_mfma_f32_16x16x32_bf16 v[96:99], a[252:255], a[200:203], v[96:99]\n" \
"ds_read_b128 a[136:139], v28 offset:4096\n" \
"v_mfma_f32_16x16x32_bf16 v[112:115], a[188:191], a[140:143], v[112:115]\n" \
"ds_read_b128 a[200:203], v29 offset:4096\n" \
"v_mfma_f32_16x16x32_bf16 v[112:115], a[252:255], a[204:207], v[112:115]\n" \
"ds_read_b64_tr_b16 a[168:169], v34 offset:0\n" \
"v_mfma_f32_16x16x32_bf16 v[108:111], a[184:187], a[140:143], v[108:111]\n" \
"s_movk_i32 s84, 0x2400\n" \
"v_mfma_f32_16x16x32_bf16 v[108:111], a[248:251], a[204:207], v[108:111]\n" \
"buffer_load_dwordx4 v25, s[76:79], s84 offen offset:1024 lds\n" \
"v_mfma_f32_16x16x32_bf16 v[104:107], a[180:183], a[140:143], v[104:107]\n" \
"ds_read_b64_tr_b16 a[170:171], v34 offset:512\n" \
"v_mfma_f32_16x16x32_bf16 v[104:107], a[244:247], a[204:207], v[104:107]\n" \
"ds_read_b64_tr_b16 a[232:233], v34 offset:4096\n" \
"v_mfma_f32_16x16x32_bf16 v[100:103], a[176:179], a[140:143], v[100:103]\n" \
"s_add_u32 s6, s26, s6\n" \
"s_addc_u32 s9, s27, 0\n" \
"v_mfma_f32_16x16x32_bf16 v[100:103], a[240:243], a[204:207], v[100:103]\n" \
"ds_read_b128 a[140:143], v28 offset:6144\n" \
"v_mfma_f32_16x16x32_bf16 v[52:55], a[176:179], a[128:131], v[52:55]\n" \
"ds_read_b128 a[204:207], v29 offset:6144\n" \
"v_mfma_f32_16x16x32_bf16 v[52:55], a[240:243], a[192:195], v[52:55]\n" \
"s_movk_i32 s84, 0x4400\n" \
"v_mfma_f32_16x16x32_bf16 v[56:59], a[180:183], a[128:131], v[56:59]\n" \
"buffer_load_dwordx4 v24, s[76:79], s84 offen offset:2048 lds\n" \
"v_mfma_f32_16x16x32_bf16 v[56:59], a[244:247], a[192:195], v[56:59]\n" \
"s_movk_i32 s84, 0x6400\n" \
"v_mfma_f32_16x16x32_bf16 v[60:63], a[184:187], a[128:131], v[60:63]\n" \
"buffer_load_dwordx4 v25, s[76:79], s84 offen offset:3072 lds\n" \
"v_mfma_f32_16x16x32_bf16 v[60:63], a[248:251], a[192:195], v[60:63]\n" \
"ds_read_b64_tr_b16 a[234:235], v34 offset:4608\n" \
"v_mfma_f32_16x16x32_bf16 v[64:67], a[188:191], a[128:131], v[64:67]\n" \
"s_add_u32 s8, s6, 0x1000000\n" \
"s_addc_u32 s6, s9, 0\n" \
"v_mfma_f32_16x16x32_bf16 v[64:67], a[252:255], a[192:195], v[64:67]\n" \
"ds_read_b128 a[128:131], v28 offset:0\n" \
"s_and_b32 s9, s6, 0xffff\n" \
"v_mfma_f32_16x16x32_bf16 v[80:83], a[188:191], a[132:135], v[80:83]\n" \
"ds_read_b128 a[192:195], v29 offset:0\n" \
"s_lshl_b32 s6, s5, 16\n" \
"v_mfma_f32_16x16x32_bf16 v[80:83], a[252:255], a[196:199], v[80:83]\n" \
"s_sub_i32 s10, 0x100000, s6\n" \
"s_mov_b32 s6, 0x8000\n" \
"v_mfma_f32_16x16x32_bf16 v[76:79], a[184:187], a[132:135], v[76:79]\n" \
"s_cmp_lg_u32 s5, 0\n" \
"s_cselect_b32 s17, s11, s6\n" \
"v_mfma_f32_16x16x32_bf16 v[76:79], a[248:251], a[196:199], v[76:79]\n" \
"s_lshl_b32 s6, s3, 1\n" \
"s_lshl_b32 s5, s5, 3\n" \
"v_mfma_f32_16x16x32_bf16 v[72:75], a[180:183], a[132:135], v[72:75]\n" \
"s_and_b32 s6, s6, 4\n" \
"s_or_b32 s12, s5, s6\n" \
"v_mfma_f32_16x16x32_bf16 v[72:75], a[244:247], a[196:199], v[72:75]\n" \
"s_sub_i32 s5, s13, s5\n" \
"s_or_b32 s5, s5, s6\n" \
"v_mfma_f32_16x16x32_bf16 v[68:71], a[176:179], a[132:135], v[68:71]\n" \
"s_or_b32 s6, s12, s13\n" \
"s_lshl_b32 s4, s4, 2\n" \
"v_mfma_f32_16x16x32_bf16 v[68:71], a[240:243], a[196:199], v[68:71]\n" \
"ds_read_b128 a[132:135], v28 offset:2048\n" \
"ds_read_b128 a[196:199], v29 offset:2048\n" \
"s_waitcnt lgkmcnt(15)\n" \
"v_mfma_f32_16x16x32_bf16 a[0:3], a[160:163], a[144:147], a[0:3]\n" \
"s_add_u32 m0, s30, 0x21000\n" \
"s_addk_i32 s84, 0x3000\n" \
"v_mfma_f32_16x16x32_bf16 a[0:3], a[224:227], a[208:211], a[0:3]\n" \
"buffer_load_dwordx4 v24, s[76:79], s84 offen offset:0 lds\n" \
"v_mfma_f32_16x16x32_bf16 a[4:7], a[164:167], a[144:147], a[4:7]\n" \
"ds_read_b64_tr_b16 a[176:177], v36 offset:0\n" \
"s_mulk_i32 s6, 0x92\n" \
"v_mfma_f32_16x16x32_bf16 a[4:7], a[228:231], a[208:211], a[4:7]\n" \
"ds_read_b64_tr_b16 a[178:179], v36 offset:1024\n" \
"s_add_i32 s4, s4, 18\n" \
"s_waitcnt lgkmcnt(15)\n" \
"v_mfma_f32_16x16x32_bf16 a[12:15], a[172:175], a[144:147], a[12:15]\n" \
"ds_read_b64_tr_b16 a[240:241], v36 offset:8192\n" \
"s_mulk_i32 s5, 0x92\n" \
"s_waitcnt lgkmcnt(15)\n" \
"v_mfma_f32_16x16x32_bf16 a[12:15], a[236:239], a[208:211], a[12:15]\n" \
"s_add_u32 s88, s42, 0x0\n" \
"buffer_store_dwordx4 v[196:199], v20, s[80:83], s88 offen offset:3072 sc1\n" \
"s_waitcnt lgkmcnt(11)\n" \
"v_mfma_f32_16x16x32_bf16 a[8:11], a[144:147], a[168:171], a[8:11]\n" \
"ds_read_b64_tr_b16 a[242:243], v36 offset:9216\n" \
"s_add_i32 s6, s4, s6\n" \
"s_waitcnt lgkmcnt(8)\n" \
"v_mfma_f32_16x16x32_bf16 a[8:11], a[208:211], a[232:235], a[8:11]\n" \
"ds_read_b64_tr_b16 a[180:181], v37 offset:0\n" \
"s_add_i32 s5, s5, s4\n" \
"v_mfma_f32_16x16x32_bf16 a[24:27], a[148:151], a[168:171], a[24:27]\n" \
"ds_read_b64_tr_b16 a[182:183], v37 offset:1024\n" \
"s_lshl_b32 s4, s3, 11\n" \
"v_mfma_f32_16x16x32_bf16 a[24:27], a[212:215], a[232:235], a[24:27]\n" \
"ds_read_b64_tr_b16 a[244:245], v37 offset:8192\n" \
"s_lshl_b32 s15, s13, 12\n" \
"v_mfma_f32_16x16x32_bf16 a[28:31], a[172:175], a[148:151], a[28:31]\n" \
"s_lshl_b32 s16, s19, 14\n" \
"s_mul_i32 s19, s19, 0xe000\n" \
"v_mfma_f32_16x16x32_bf16 a[28:31], a[236:239], a[212:215], a[28:31]\n" \
"s_addk_i32 s84, 0x2000\n" \
"v_mfma_f32_16x16x32_bf16 a[20:23], a[164:167], a[148:151], a[20:23]\n" \
"buffer_load_dwordx4 v25, s[76:79], s84 offen offset:1024 lds\n" \
"v_mfma_f32_16x16x32_bf16 a[20:23], a[228:231], a[212:215], a[20:23]\n" \
"ds_read_b64_tr_b16 a[246:247], v37 offset:9216\n" \
"s_mov_b32 s11, 0x20000\n" \
"v_mfma_f32_16x16x32_bf16 a[16:19], a[160:163], a[148:151], a[16:19]\n" \
"ds_read_b64_tr_b16 a[184:185], v38 offset:0\n" \
"s_or_b32 s12, s4, 0xc000\n" \
"v_mfma_f32_16x16x32_bf16 a[16:19], a[224:227], a[212:215], a[16:19]\n" \
"ds_read_b64_tr_b16 a[186:187], v38 offset:1024\n" \
"s_lshl_b32 s4, s3, 8\n" \
"v_mfma_f32_16x16x32_bf16 a[32:35], a[160:163], a[152:155], a[32:35]\n" \
"s_addk_i32 s84, 0x2000\n" \
"v_mfma_f32_16x16x32_bf16 a[32:35], a[224:227], a[216:219], a[32:35]\n" \
"buffer_load_dwordx4 v24, s[76:79], s84 offen offset:2048 lds\n" \
"v_mfma_f32_16x16x32_bf16 a[36:39], a[164:167], a[152:155], a[36:39]\n" \
"ds_read_b64_tr_b16 a[248:249], v38 offset:8192\n" \
"s_or_b32 s22, s15, s16\n" \
"v_mfma_f32_16x16x32_bf16 a[36:39], a[228:231], a[216:219], a[36:39]\n" \
"ds_read_b64_tr_b16 a[250:251], v38 offset:9216\n" \
"s_or_b32 s23, s19, s15\n" \
"v_mfma_f32_16x16x32_bf16 a[44:47], a[172:175], a[152:155], a[44:47]\n" \
"ds_read_b64_tr_b16 a[188:189], v39 offset:0\n" \
"s_lshl_b32 s60, s6, 10\n" \
"v_mfma_f32_16x16x32_bf16 a[44:47], a[236:239], a[216:219], a[44:47]\n" \
"ds_read_b64_tr_b16 a[190:191], v39 offset:1024\n" \
"s_lshl_b32 s61, s5, 10\n" \
"v_mfma_f32_16x16x32_bf16 a[40:43], a[152:155], a[168:171], a[40:43]\n" \
"s_add_u32 s89, s42, 0x2000\n" \
"buffer_store_dwordx4 v[200:203], v20, s[80:83], s89 offen offset:0 sc1\n" \
"v_mfma_f32_16x16x32_bf16 a[40:43], a[216:219], a[232:235], a[40:43]\n" \
"ds_read_b64_tr_b16 a[252:253], v39 offset:8192\n" \
"s_lshl_b32 s14, s14, 15\n" \
"v_mfma_f32_16x16x32_bf16 a[56:59], a[156:159], a[168:171], a[56:59]\n" \
"ds_read_b64_tr_b16 a[254:255], v39 offset:9216\n" \
"s_or_b32 s20, s4, 0x26c00\n" \
"v_mfma_f32_16x16x32_bf16 a[56:59], a[220:223], a[232:235], a[56:59]\n" \
"s_or_b32 s13, s22, 0x8000\n" \
"s_add_i32 s22, s22, 0x14000\n" \
"v_mfma_f32_16x16x32_bf16 a[60:63], a[172:175], a[156:159], a[60:63]\n" \
"s_bitset1_b32 s23, 16\n" \
"s_or_b32 s24, s7, 0x5be8020\n" \
"v_mfma_f32_16x16x32_bf16 a[60:63], a[236:239], a[220:223], a[60:63]\n" \
"s_addk_i32 s84, 0x2000\n" \
"v_mfma_f32_16x16x32_bf16 a[52:55], a[164:167], a[156:159], a[52:55]\n" \
".fill (L_pb + 73440 - .) / 4, 4, 0xBF800000\n" \
"L_pp18:\n" \
"s_cbranch_execz L_pret\n" \
"buffer_load_dwordx4 v25, s[76:79], s84 offen offset:3072 lds\n" \
"v_mfma_f32_16x16x32_bf16 a[52:55], a[228:231], a[220:223], a[52:55]\n" \
"s_and_b32 s57, s27, 0xffff\n" \
"s_mov_b32 s58, 0x6000000\n" \
"v_mfma_f32_16x16x32_bf16 a[48:51], a[160:163], a[156:159], a[48:51]\n" \
"s_mov_b32 s56, s26\n" \
"s_mov_b32 s59, s11\n" \
"v_mfma_f32_16x16x32_bf16 a[48:51], a[224:227], a[220:223], a[48:51]\n" \
"s_add_i32 s60, s60, 0x3000000\n" \
"s_add_i32 s61, s61, 0x411c000\n" \
"v_mfma_f32_16x16x32_bf16 a[64:67], a[164:167], a[140:143], a[64:67]\n" \
"s_sub_i32 s19, 14, s18\n" \
"s_mov_b32 s21, 0x71800000\n" \
"v_mfma_f32_16x16x32_bf16 a[64:67], a[228:231], a[204:207], a[64:67]\n" \
"s_mov_b32 s4, L_gsw-L_gtr\n" \
"v_mfma_f32_16x16x32_bf16 a[80:83], a[172:175], a[140:143], a[80:83]\n" \
"s_cmp_eq_u32 s19, 0\n" \
"s_cselect_b32 s4, L_gs7-L_gtr, s4\n" \
"v_mfma_f32_16x16x32_bf16 a[80:83], a[236:239], a[204:207], a[80:83]\n" \
"s_cmp_eq_u32 s16, 0\n" \
"s_cselect_b32 s4, 0, s4\n" \
"v_mfma_f32_16x16x32_bf16 a[68:71], a[140:143], a[168:171], a[68:71]\n" \
"s_getpc_b64 s[28:29]\n" \
"L_gp1_%=:\n" \
"s_add_u32 s28, s28, L_gtr-L_gp1_%=\n" \
"s_addc_u32 s29, s29, 0\n" \
"v_mfma_f32_16x16x32_bf16 a[68:71], a[204:207], a[232:235], a[68:71]\n" \
"s_waitcnt vmcnt(11) lgkmcnt(0)\n" \
"s_barrier\n" \
"v_mfma_f32_16x16x32_bf16 v[116:119], a[176:179], a[144:147], v[116:119]\n" \
"s_add_u32 m0, s40, 0x0\n" \
"s_add_u32 s85, s39, 0x10000\n" \
"v_mfma_f32_16x16x32_bf16 v[116:119], a[240:243], a[208:211], v[116:119]\n" \
"buffer_load_dwordx4 v20, s[80:83], s85 offen sc1 lds\n" \
"v_mfma_f32_16x16x32_bf16 v[120:123], a[180:183], a[144:147], v[120:123]\n" \
"ds_read_b64_tr_b16 a[160:161], v32 offset:32768\n" \
"v_mfma_f32_16x16x32_bf16 v[120:123], a[244:247], a[208:211], v[120:123]\n" \
"ds_read_b64_tr_b16 a[162:163], v32 offset:33280\n" \
"v_mfma_f32_16x16x32_bf16 v[124:127], a[184:187], a[144:147], v[124:127]\n" \
"s_add_u32 s28, s28, s4\n" \
"s_addc_u32 s29, s29, 0\n" \
"v_mfma_f32_16x16x32_bf16 v[124:127], a[248:251], a[208:211], v[124:127]\n" \
"v_mfma_f32_16x16x32_bf16 v[128:131], a[188:191], a[144:147], v[128:131]\n" \
"ds_read_b64_tr_b16 a[224:225], v32 offset:36864\n" \
"v_mfma_f32_16x16x32_bf16 v[128:131], a[252:255], a[208:211], v[128:131]\n" \
"ds_read_b128 a[144:147], v28 offset:40960\n" \
"v_mfma_f32_16x16x32_bf16 v[144:147], a[188:191], a[148:151], v[144:147]\n" \
"ds_read_b128 a[208:211], v29 offset:40960\n" \
"v_mfma_f32_16x16x32_bf16 v[144:147], a[252:255], a[212:215], v[144:147]\n" \
"s_add_u32 m0, s40, 0x1000\n" \
"s_add_u32 s85, s39, 0x11000\n" \
"v_mfma_f32_16x16x32_bf16 v[140:143], a[184:187], a[148:151], v[140:143]\n" \
"buffer_load_dwordx4 v20, s[80:83], s85 offen sc1 lds\n" \
"v_mfma_f32_16x16x32_bf16 v[140:143], a[248:251], a[212:215], v[140:143]\n" \
"ds_read_b64_tr_b16 a[226:227], v32 offset:37376\n" \
"v_mfma_f32_16x16x32_bf16 v[136:139], a[180:183], a[148:151], v[136:139]\n" \
"v_mfma_f32_16x16x32_bf16 v[136:139], a[244:247], a[212:215], v[136:139]\n" \
"ds_read_b64_tr_b16 a[164:165], v33 offset:32768\n" \
"v_mfma_f32_16x16x32_bf16 v[132:135], a[176:179], a[148:151], v[132:135]\n" \
"v_mfma_f32_16x16x32_bf16 v[132:135], a[240:243], a[212:215], v[132:135]\n" \
"ds_read_b128 a[148:151], v28 offset:43008\n" \
"v_mfma_f32_16x16x32_bf16 v[148:151], a[176:179], a[152:155], v[148:151]\n" \
"ds_read_b128 a[212:215], v29 offset:43008\n" \
"v_mfma_f32_16x16x32_bf16 v[148:151], a[240:243], a[216:219], v[148:151]\n" \
"s_add_u32 m0, s40, 0x3000\n" \
"s_movk_i32 s84, 0x400\n" \
"v_mfma_f32_16x16x32_bf16 v[152:155], a[180:183], a[152:155], v[152:155]\n" \
"buffer_load_dwordx4 v21, s[76:79], s84 offen offset:0 lds\n" \
"v_mfma_f32_16x16x32_bf16 v[152:155], a[244:247], a[216:219], v[152:155]\n" \
"ds_read_b64_tr_b16 a[166:167], v33 offset:33280\n" \
"v_mfma_f32_16x16x32_bf16 v[156:159], a[184:187], a[152:155], v[156:159]\n" \
"ds_read_b64_tr_b16 a[228:229], v33 offset:36864\n" \
"v_mfma_f32_16x16x32_bf16 v[156:159], a[248:251], a[216:219], v[156:159]\n" \
"s_add_u32 m0, s31, 0x0\n" \
"s_mov_b32 s84, 0x400000\n" \
"v_mfma_f32_16x16x32_bf16 v[160:163], a[188:191], a[152:155], v[160:163]\n" \
"buffer_load_dwordx4 v26, s[68:71], s84 offen offset:0 lds\n" \
"v_mfma_f32_16x16x32_bf16 v[160:163], a[252:255], a[216:219], v[160:163]\n" \
"ds_read_b128 a[152:155], v28 offset:45056\n" \
"v_mfma_f32_16x16x32_bf16 v[176:179], a[188:191], a[156:159], v[176:179]\n" \
"ds_read_b128 a[216:219], v29 offset:45056\n" \
"v_mfma_f32_16x16x32_bf16 v[176:179], a[252:255], a[220:223], v[176:179]\n" \
"ds_read_b64_tr_b16 a[230:231], v33 offset:37376\n" \
"v_mfma_f32_16x16x32_bf16 v[172:175], a[184:187], a[156:159], v[172:175]\n" \
"ds_read_b64_tr_b16 a[172:173], v35 offset:32768\n" \
"v_mfma_f32_16x16x32_bf16 v[172:175], a[248:251], a[220:223], v[172:175]\n" \
"v_mfma_f32_16x16x32_bf16 v[168:171], a[180:183], a[156:159], v[168:171]\n" \
"s_add_u32 s88, s42, 0x2000\n" \
"buffer_store_dwordx4 v[204:207], v20, s[80:83], s88 offen offset:1024 sc1\n" \
"v_mfma_f32_16x16x32_bf16 v[168:171], a[244:247], a[220:223], v[168:171]\n" \
"ds_read_b64_tr_b16 a[174:175], v35 offset:33280\n" \
"v_mfma_f32_16x16x32_bf16 v[164:167], a[176:179], a[156:159], v[164:167]\n" \
"v_mfma_f32_16x16x32_bf16 v[164:167], a[240:243], a[220:223], v[164:167]\n" \
"ds_read_b128 a[156:159], v28 offset:47104\n" \
"v_mfma_f32_16x16x32_bf16 v[84:87], a[176:179], a[136:139], v[84:87]\n" \
"ds_read_b128 a[220:223], v29 offset:47104\n" \
"v_mfma_f32_16x16x32_bf16 v[84:87], a[240:243], a[200:203], v[84:87]\n" \
"ds_read_b64_tr_b16 a[236:237], v35 offset:36864\n" \
"v_mfma_f32_16x16x32_bf16 v[88:91], a[180:183], a[136:139], v[88:91]\n" \
"v_mfma_f32_16x16x32_bf16 v[88:91], a[244:247], a[200:203], v[88:91]\n" \
"s_addk_i32 s84, 0x7c00\n" \
"s_nop 0\n" \
"v_mfma_f32_16x16x32_bf16 v[92:95], a[184:187], a[136:139], v[92:95]\n" \
"buffer_load_dwordx4 v26, s[68:71], s84 offen offset:1024 lds\n" \
"v_mfma_f32_16x16x32_bf16 v[92:95], a[248:251], a[200:203], v[92:95]\n" \
"ds_read_b64_tr_b16 a[238:239], v35 offset:37376\n" \
"v_mfma_f32_16x16x32_bf16 v[96:99], a[188:191], a[136:139], v[96:99]\n" \
"v_mfma_f32_16x16x32_bf16 v[96:99], a[252:255], a[200:203], v[96:99]\n" \
"ds_read_b128 a[136:139], v28 offset:36864\n" \
"v_mfma_f32_16x16x32_bf16 v[112:115], a[188:191], a[140:143], v[112:115]\n" \
"ds_read_b128 a[200:203], v29 offset:36864\n" \
"v_mfma_f32_16x16x32_bf16 v[112:115], a[252:255], a[204:207], v[112:115]\n" \
"ds_read_b64_tr_b16 a[168:169], v34 offset:32768\n" \
"v_mfma_f32_16x16x32_bf16 v[108:111], a[184:187], a[140:143], v[108:111]\n" \
"s_addk_i32 s84, 0x7c00\n" \
"s_nop 0\n" \
"v_mfma_f32_16x16x32_bf16 v[108:111], a[248:251], a[204:207], v[108:111]\n" \
"buffer_load_dwordx4 v27, s[68:71], s84 offen offset:2048 lds\n" \
"v_mfma_f32_16x16x32_bf16 v[104:107], a[180:183], a[140:143], v[104:107]\n" \
"ds_read_b64_tr_b16 a[170:171], v34 offset:33280\n" \
"v_mfma_f32_16x16x32_bf16 v[104:107], a[244:247], a[204:207], v[104:107]\n" \
"ds_read_b64_tr_b16 a[232:233], v34 offset:36864\n" \
"v_mfma_f32_16x16x32_bf16 v[100:103], a[176:179], a[140:143], v[100:103]\n" \
"v_mfma_f32_16x16x32_bf16 v[100:103], a[240:243], a[204:207], v[100:103]\n" \
"ds_read_b128 a[140:143], v28 offset:38912\n" \
"v_mfma_f32_16x16x32_bf16 v[52:55], a[176:179], a[128:131], v[52:55]\n" \
"ds_read_b128 a[204:207], v29 offset:38912\n" \
"v_mfma_f32_16x16x32_bf16 v[52:55], a[240:243], a[192:195], v[52:55]\n" \
"s_addk_i32 s84, 0x7c00\n" \
"s_nop 0\n" \
"v_mfma_f32_16x16x32_bf16 v[56:59], a[180:183], a[128:131], v[56:59]\n" \
"buffer_load_dwordx4 v27, s[68:71], s84 offen offset:3072 lds\n" \
"v_mfma_f32_16x16x32_bf16 v[56:59], a[244:247], a[192:195], v[56:59]\n" \
"ds_read_b64_tr_b16 a[234:235], v34 offset:37376\n" \
"v_mfma_f32_16x16x32_bf16 v[60:63], a[184:187], a[128:131], v[60:63]\n" \
"v_mfma_f32_16x16x32_bf16 v[60:63], a[248:251], a[192:195], v[60:63]\n" \
"v_mfma_f32_16x16x32_bf16 v[64:67], a[188:191], a[128:131], v[64:67]\n" \
"v_mfma_f32_16x16x32_bf16 v[64:67], a[252:255], a[192:195], v[64:67]\n" \
"ds_read_b128 a[128:131], v28 offset:32768\n" \
"v_mfma_f32_16x16x32_bf16 v[80:83], a[188:191], a[132:135], v[80:83]\n" \
"ds_read_b128 a[192:195], v29 offset:32768\n" \
"v_mfma_f32_16x16x32_bf16 v[80:83], a[252:255], a[196:199], v[80:83]\n" \
"v_mfma_f32_16x16x32_bf16 v[76:79], a[184:187], a[132:135], v[76:79]\n" \
"v_mfma_f32_16x16x32_bf16 v[76:79], a[248:251], a[196:199], v[76:79]\n" \
"v_mfma_f32_16x16x32_bf16 v[72:75], a[180:183], a[132:135], v[72:75]\n" \
"v_mfma_f32_16x16x32_bf16 v[72:75], a[244:247], a[196:199], v[72:75]\n" \
"v_mfma_f32_16x16x32_bf16 v[68:71], a[176:179], a[132:135], v[68:71]\n" \
"v_mfma_f32_16x16x32_bf16 v[68:71], a[240:243], a[196:199], v[68:71]\n" \
"ds_read_b128 a[132:135], v28 offset:34816\n" \
"ds_read_b128 a[196:199], v29 offset:34816\n" \
"s_waitcnt lgkmcnt(15)\n" \
"s_nop 0\n" \
"v_mfma_f32_16x16x32_bf16 a[0:3], a[160:163], a[144:147], a[0:3]\n" \
"s_add_u32 s89, s42, 0x2000\n" \
"buffer_store_dwordx4 v[208:211], v20, s[80:83], s89 offen offset:2048 sc1\n" \
"v_mfma_f32_16x16x32_bf16 a[0:3], a[224:227], a[208:211], a[0:3]\n" \
"ds_read_b64_tr_b16 a[176:177], v36 offset:32768\n" \
"v_mfma_f32_16x16x32_bf16 a[4:7], a[164:167], a[144:147], a[4:7]\n" \
"ds_read_b64_tr_b16 a[178:179], v36 offset:33792\n" \
"v_mfma_f32_16x16x32_bf16 a[4:7], a[228:231], a[208:211], a[4:7]\n" \
"ds_read_b64_tr_b16 a[240:241], v36 offset:40960\n" \
"v_mfma_f32_16x16x32_bf16 a[12:15], a[172:175], a[144:147], a[12:15]\n" \
"ds_read_b64_tr_b16 a[242:243], v36 offset:41984\n" \
"s_waitcnt lgkmcnt(15)\n" \
"v_mfma_f32_16x16x32_bf16 a[12:15], a[236:239], a[208:211], a[12:15]\n" \
"s_waitcnt lgkmcnt(12)\n" \
"v_mfma_f32_16x16x32_bf16 a[8:11], a[144:147], a[168:171], a[8:11]\n" \
"ds_read_b64_tr_b16 a[180:181], v37 offset:32768\n" \
"s_waitcnt lgkmcnt(9)\n" \
"s_nop 0\n" \
"v_mfma_f32_16x16x32_bf16 a[8:11], a[208:211], a[232:235], a[8:11]\n" \
"v_mfma_f32_16x16x32_bf16 a[24:27], a[148:151], a[168:171], a[24:27]\n" \
"ds_read_b64_tr_b16 a[182:183], v37 offset:33792\n" \
"v_mfma_f32_16x16x32_bf16 a[24:27], a[212:215], a[232:235], a[24:27]\n" \
"ds_read_b64_tr_b16 a[244:245], v37 offset:40960\n" \
"v_mfma_f32_16x16x32_bf16 a[28:31], a[172:175], a[148:151], a[28:31]\n" \
"v_mfma_f32_16x16x32_bf16 a[28:31], a[236:239], a[212:215], a[28:31]\n" \
"ds_read_b64_tr_b16 a[246:247], v37 offset:41984\n" \
"v_mfma_f32_16x16x32_bf16 a[20:23], a[164:167], a[148:151], a[20:23]\n" \
"v_mfma_f32_16x16x32_bf16 a[20:23], a[228:231], a[212:215], a[20:23]\n" \
"ds_read_b64_tr_b16 a[184:185], v38 offset:32768\n" \
"v_mfma_f32_16x16x32_bf16 a[16:19], a[160:163], a[148:151], a[16:19]\n" \
"ds_read_b64_tr_b16 a[186:187], v38 offset:33792\n" \
"v_mfma_f32_16x16x32_bf16 a[16:19], a[224:227], a[212:215], a[16:19]\n" \
"v_mfma_f32_16x16x32_bf16 a[32:35], a[160:163], a[152:155], a[32:35]\n" \
"s_add_u32 s88, s42, 0x2000\n" \
"buffer_store_dwordx4 v[212:215], v20, s[80:83], s88 offen offset:3072 sc1\n" \
"v_mfma_f32_16x16x32_bf16 a[32:35], a[224:227], a[216:219], a[32:35]\n" \
"ds_read_b64_tr_b16 a[248:249], v38 offset:40960\n" \
"v_mfma_f32_16x16x32_bf16 a[36:39], a[164:167], a[152:155], a[36:39]\n" \
"v_lshlrev_b32_e32 v45, 3, v20\n" \
"v_add_u32_e32 v45, s30, v45\n" \
"v_mfma_f32_16x16x32_bf16 a[36:39], a[228:231], a[216:219], a[36:39]\n" \
"s_getpc_b64 s[86:87]\n" \
"L_ct1_%=:\n" \
"s_add_u32 s86, s86, L_ap0-L_ct1_%=\n" \
"s_addc_u32 s87, s87, 0\n" \
"s_add_u32 m0, s40, 0x2000\n" \
"v_mfma_f32_16x16x32_bf16 a[44:47], a[172:175], a[152:155], a[44:47]\n" \
"global_load_lds_dword v45, s[86:87]\n" \
"v_mfma_f32_16x16x32_bf16 a[44:47], a[236:239], a[216:219], a[44:47]\n" \
"ds_read_b64_tr_b16 a[250:251], v38 offset:41984\n" \
"v_mfma_f32_16x16x32_bf16 a[40:43], a[152:155], a[168:171], a[40:43]\n" \
"ds_read_b64_tr_b16 a[188:189], v39 offset:32768\n" \
"v_mfma_f32_16x16x32_bf16 a[40:43], a[216:219], a[232:235], a[40:43]\n" \
"ds_read_b64_tr_b16 a[190:191], v39 offset:33792\n" \
"v_mfma_f32_16x16x32_bf16 a[56:59], a[156:159], a[168:171], a[56:59]\n" \
"ds_read_b64_tr_b16 a[252:253], v39 offset:40960\n" \
"v_mfma_f32_16x16x32_bf16 a[56:59], a[220:223], a[232:235], a[56:59]\n" \
"ds_read_b64_tr_b16 a[254:255], v39 offset:41984\n" \
"v_mfma_f32_16x16x32_bf16 a[60:63], a[172:175], a[156:159], a[60:63]\n" \
"v_mfma_f32_16x16x32_bf16 a[60:63], a[236:239], a[220:223], a[60:63]\n" \
"v_mfma_f32_16x16x32_bf16 a[52:55], a[164:167], a[156:159], a[52:55]\n" \
"v_mfma_f32_16x16x32_bf16 a[52:55], a[228:231], a[220:223], a[52:55]\n" \
"v_mfma_f32_16x16x32_bf16 a[48:51], a[160:163], a[156:159], a[48:51]\n" \
"v_mfma_f32_16x16x32_bf16 a[48:51], a[224:227], a[220:223], a[48:51]\n" \
"v_mfma_f32_16x16x32_bf16 a[64:67], a[164:167], a[140:143], a[64:67]\n" \
"v_mfma_f32_16x16x32_bf16 a[64:67], a[228:231], a[204:207], a[64:67]\n" \
"v_mfma_f32_16x16x32_bf16 a[80:83], a[172:175], a[140:143], a[80:83]\n" \
"v_mfma_f32_16x16x32_bf16 a[80:83], a[236:239], a[204:207], a[80:83]\n" \
"v_mfma_f32_16x16x32_bf16 a[68:71], a[140:143], a[168:171], a[68:71]\n" \
"v_mfma_f32_16x16x32_bf16 a[68:71], a[204:207], a[232:235], a[68:71]\n" \
"s_waitcnt vmcnt(3) lgkmcnt(0)\n" \
"s_barrier\n" \
"v_xor_b32_e32 v28, 0x20000, v28\n" \
"v_xor_b32_e32 v29, 0x20000, v29\n" \
"v_mfma_f32_16x16x32_bf16 v[116:119], a[176:179], a[144:147], v[116:119]\n" \
"ds_read_b64_tr_b16 a[160:161], v32 offset:0\n" \
"v_accvgpr_read_b32 v184, a0\n" \
"v_mfma_f32_16x16x32_bf16 v[116:119], a[240:243], a[208:211], v[116:119]\n" \
"v_accvgpr_read_b32 v185, a1\n" \
"v_accvgpr_read_b32 v186, a2\n" \
"v_mfma_f32_16x16x32_bf16 v[120:123], a[180:183], a[144:147], v[120:123]\n" \
"v_accvgpr_read_b32 v187, a3\n" \
"v_accvgpr_read_b32 v188, a4\n" \
"v_mfma_f32_16x16x32_bf16 v[120:123], a[244:247], a[208:211], v[120:123]\n" \
"ds_read_b64_tr_b16 a[162:163], v32 offset:512\n" \
"v_accvgpr_read_b32 v189, a5\n" \
"v_mfma_f32_16x16x32_bf16 v[124:127], a[184:187], a[144:147], v[124:127]\n" \
"v_accvgpr_read_b32 v190, a6\n" \
"v_accvgpr_read_b32 v191, a7\n" \
"v_mfma_f32_16x16x32_bf16 v[124:127], a[248:251], a[208:211], v[124:127]\n" \
"v_accvgpr_read_b32 v196, a12\n" \
"v_accvgpr_read_b32 v197, a13\n" \
"v_mfma_f32_16x16x32_bf16 v[128:131], a[188:191], a[144:147], v[128:131]\n" \
"ds_read_b64_tr_b16 a[224:225], v32 offset:4096\n" \
"v_accvgpr_read_b32 v198, a14\n" \
"v_mfma_f32_16x16x32_bf16 v[128:131], a[252:255], a[208:211], v[128:131]\n" \
"ds_read_b128 a[144:147], v28 offset:8192\n" \
"v_accvgpr_read_b32 v199, a15\n" \
"v_mfma_f32_16x16x32_bf16 v[144:147], a[188:191], a[148:151], v[144:147]\n" \
"ds_read_b128 a[208:211], v29 offset:8192\n" \
"v_accvgpr_read_b32 v192, a8\n" \
"v_mfma_f32_16x16x32_bf16 v[144:147], a[252:255], a[212:215], v[144:147]\n" \
"ds_read_b64_tr_b16 a[226:227], v32 offset:4608\n" \
"v_accvgpr_read_b32 v193, a9\n" \
"v_mfma_f32_16x16x32_bf16 v[140:143], a[184:187], a[148:151], v[140:143]\n" \
"v_accvgpr_read_b32 v194, a10\n" \
"v_accvgpr_read_b32 v195, a11\n" \
"v_mfma_f32_16x16x32_bf16 v[140:143], a[248:251], a[212:215], v[140:143]\n" \
"v_accvgpr_read_b32 v208, a24\n" \
"v_accvgpr_read_b32 v209, a25\n" \
"v_mfma_f32_16x16x32_bf16 v[136:139], a[180:183], a[148:151], v[136:139]\n" \
"v_accvgpr_read_b32 v210, a26\n" \
"v_accvgpr_read_b32 v211, a27\n" \
"v_mfma_f32_16x16x32_bf16 v[136:139], a[244:247], a[212:215], v[136:139]\n" \
"ds_read_b64_tr_b16 a[164:165], v33 offset:0\n" \
"v_accvgpr_read_b32 v212, a28\n" \
"v_mfma_f32_16x16x32_bf16 v[132:135], a[176:179], a[148:151], v[132:135]\n" \
"v_accvgpr_read_b32 v213, a29\n" \
"v_accvgpr_read_b32 v214, a30\n" \
"v_mfma_f32_16x16x32_bf16 v[132:135], a[240:243], a[212:215], v[132:135]\n" \
"ds_read_b128 a[148:151], v28 offset:10240\n" \
"v_accvgpr_read_b32 v215, a31\n" \
"v_mfma_f32_16x16x32_bf16 v[148:151], a[176:179], a[152:155], v[148:151]\n" \
"ds_read_b128 a[212:215], v29 offset:10240\n" \
"v_accvgpr_read_b32 v204, a20\n" \
"v_mfma_f32_16x16x32_bf16 v[148:151], a[240:243], a[216:219], v[148:151]\n" \
"ds_read_b64_tr_b16 a[166:167], v33 offset:512\n" \
"v_accvgpr_read_b32 v205, a21\n" \
"v_mfma_f32_16x16x32_bf16 v[152:155], a[180:183], a[152:155], v[152:155]\n" \
"v_accvgpr_read_b32 v206, a22\n" \
"v_accvgpr_read_b32 v207, a23\n" \
"v_mfma_f32_16x16x32_bf16 v[152:155], a[244:247], a[216:219], v[152:155]\n" \
"ds_read_b64_tr_b16 a[228:229], v33 offset:4096\n" \
"v_accvgpr_read_b32 v200, a16\n" \
"v_mfma_f32_16x16x32_bf16 v[156:159], a[184:187], a[152:155], v[156:159]\n" \
"v_accvgpr_read_b32 v201, a17\n" \
"v_accvgpr_read_b32 v202, a18\n" \
"v_mfma_f32_16x16x32_bf16 v[156:159], a[248:251], a[216:219], v[156:159]\n" \
"v_accvgpr_read_b32 v203, a19\n" \
"v_accvgpr_read_b32 v216, a32\n" \
"v_mfma_f32_16x16x32_bf16 v[160:163], a[188:191], a[152:155], v[160:163]\n" \
"ds_read_b64_tr_b16 a[230:231], v33 offset:4608\n" \
"v_accvgpr_read_b32 v217, a33\n" \
"v_mfma_f32_16x16x32_bf16 v[160:163], a[252:255], a[216:219], v[160:163]\n" \
"ds_read_b128 a[152:155], v28 offset:12288\n" \
"v_accvgpr_read_b32 v218, a34\n" \
"v_mfma_f32_16x16x32_bf16 v[176:179], a[188:191], a[156:159], v[176:179]\n" \
"ds_read_b128 a[216:219], v29 offset:12288\n" \
"v_accvgpr_read_b32 v219, a35\n" \
"v_mfma_f32_16x16x32_bf16 v[176:179], a[252:255], a[220:223], v[176:179]\n" \
"v_accvgpr_read_b32 v220, a36\n" \
"v_accvgpr_read_b32 v221, a37\n" \
"v_mfma_f32_16x16x32_bf16 v[172:175], a[184:187], a[156:159], v[172:175]\n" \
"ds_read_b64_tr_b16 a[172:173], v35 offset:0\n" \
"v_accvgpr_read_b32 v222, a38\n" \
"v_mfma_f32_16x16x32_bf16 v[172:175], a[248:251], a[220:223], v[172:175]\n" \
"v_accvgpr_read_b32 v223, a39\n" \
"v_accvgpr_read_b32 v228, a44\n" \
"v_mfma_f32_16x16x32_bf16 v[168:171], a[180:183], a[156:159], v[168:171]\n" \
"v_accvgpr_read_b32 v229, a45\n" \
"v_accvgpr_read_b32 v230, a46\n" \
"v_mfma_f32_16x16x32_bf16 v[168:171], a[244:247], a[220:223], v[168:171]\n" \
"ds_read_b64_tr_b16 a[174:175], v35 offset:512\n" \
"v_accvgpr_read_b32 v231, a47\n" \
"v_mfma_f32_16x16x32_bf16 v[164:167], a[176:179], a[156:159], v[164:167]\n" \
"v_accvgpr_read_b32 v224, a40\n" \
"v_accvgpr_read_b32 v225, a41\n" \
"v_mfma_f32_16x16x32_bf16 v[164:167], a[240:243], a[220:223], v[164:167]\n" \
"ds_read_b128 a[156:159], v28 offset:14336\n" \
"v_accvgpr_read_b32 v226, a42\n" \
"v_mfma_f32_16x16x32_bf16 v[84:87], a[176:179], a[136:139], v[84:87]\n" \
"ds_read_b128 a[220:223], v29 offset:14336\n" \
"v_accvgpr_read_b32 v227, a43\n" \
"v_mfma_f32_16x16x32_bf16 v[84:87], a[240:243], a[200:203], v[84:87]\n" \
"ds_read_b64_tr_b16 a[236:237], v35 offset:4096\n" \
"v_accvgpr_read_b32 v240, a56\n" \
"v_mfma_f32_16x16x32_bf16 v[88:91], a[180:183], a[136:139], v[88:91]\n" \
"v_accvgpr_read_b32 v241, a57\n" \
"v_accvgpr_read_b32 v242, a58\n" \
"v_mfma_f32_16x16x32_bf16 v[88:91], a[244:247], a[200:203], v[88:91]\n" \
"ds_read_b64_tr_b16 a[238:239], v35 offset:4608\n" \
"v_accvgpr_read_b32 v243, a59\n" \
"v_mfma_f32_16x16x32_bf16 v[92:95], a[184:187], a[136:139], v[92:95]\n" \
"v_accvgpr_read_b32 v244, a60\n" \
"v_accvgpr_read_b32 v245, a61\n" \
"v_mfma_f32_16x16x32_bf16 v[92:95], a[248:251], a[200:203], v[92:95]\n" \
"v_accvgpr_read_b32 v246, a62\n" \
"v_accvgpr_read_b32 v247, a63\n" \
"v_mfma_f32_16x16x32_bf16 v[96:99], a[188:191], a[136:139], v[96:99]\n" \
"v_accvgpr_read_b32 v236, a52\n" \
"v_accvgpr_read_b32 v237, a53\n" \
"v_mfma_f32_16x16x32_bf16 v[96:99], a[252:255], a[200:203], v[96:99]\n" \
"ds_read_b128 a[136:139], v28 offset:4096\n" \
"v_accvgpr_read_b32 v238, a54\n" \
"v_mfma_f32_16x16x32_bf16 v[112:115], a[188:191], a[140:143], v[112:115]\n" \
"ds_read_b128 a[200:203], v29 offset:4096\n" \
"v_accvgpr_read_b32 v239, a55\n" \
"v_mfma_f32_16x16x32_bf16 v[112:115], a[252:255], a[204:207], v[112:115]\n" \
"ds_read_b64_tr_b16 a[168:169], v34 offset:0\n" \
"v_accvgpr_read_b32 v232, a48\n" \
"v_mfma_f32_16x16x32_bf16 v[108:111], a[184:187], a[140:143], v[108:111]\n" \
"ds_read_b64_tr_b16 a[170:171], v34 offset:512\n" \
"v_accvgpr_read_b32 v233, a49\n" \
"v_mfma_f32_16x16x32_bf16 v[108:111], a[248:251], a[204:207], v[108:111]\n" \
"v_accvgpr_read_b32 v234, a50\n" \
"v_accvgpr_read_b32 v235, a51\n" \
"v_mfma_f32_16x16x32_bf16 v[104:107], a[180:183], a[140:143], v[104:107]\n" \
"v_accvgpr_read_b32 v180, a64\n" \
"v_accvgpr_read_b32 v181, a65\n" \
"v_mfma_f32_16x16x32_bf16 v[104:107], a[244:247], a[204:207], v[104:107]\n" \
"ds_read_b64_tr_b16 a[232:233], v34 offset:4096\n" \
"v_accvgpr_read_b32 v182, a66\n" \
"v_mfma_f32_16x16x32_bf16 v[100:103], a[176:179], a[140:143], v[100:103]\n" \
"v_accvgpr_read_b32 v183, a67\n" \
"v_accvgpr_read_b32 v48, a80\n" \
"v_mfma_f32_16x16x32_bf16 v[100:103], a[240:243], a[204:207], v[100:103]\n" \
"ds_read_b128 a[140:143], v28 offset:6144\n" \
"v_accvgpr_read_b32 v49, a81\n" \
"v_mfma_f32_16x16x32_bf16 v[52:55], a[176:179], a[128:131], v[52:55]\n" \
"ds_read_b128 a[204:207], v29 offset:6144\n" \
"v_accvgpr_read_b32 v50, a82\n" \
"v_mfma_f32_16x16x32_bf16 v[52:55], a[240:243], a[192:195], v[52:55]\n" \
"ds_read_b64_tr_b16 a[234:235], v34 offset:4608\n" \
"v_accvgpr_read_b32 v51, a83\n" \
"v_mfma_f32_16x16x32_bf16 v[56:59], a[180:183], a[128:131], v[56:59]\n" \
"v_accvgpr_read_b32 v252, a68\n" \
"v_accvgpr_read_b32 v253, a69\n" \
"v_mfma_f32_16x16x32_bf16 v[56:59], a[244:247], a[192:195], v[56:59]\n" \
"v_accvgpr_read_b32 v254, a70\n" \
"v_accvgpr_read_b32 v255, a71\n" \
"v_mfma_f32_16x16x32_bf16 v[60:63], a[184:187], a[128:131], v[60:63]\n" \
"v_lshrrev_b32_e64 v24, 9, v20\n" \
"v_and_b32_e32 v26, 0x1ff, v20\n" \
"v_mfma_f32_16x16x32_bf16 v[60:63], a[248:251], a[192:195], v[60:63]\n" \
"v_lshl_add_u32 v24, v24, 3, v26\n" \
"v_lshrrev_b32_e64 v25, 8, v20\n" \
"v_mfma_f32_16x16x32_bf16 v[64:67], a[188:191], a[128:131], v[64:67]\n" \
"v_mul_u32_u24_e32 v25, 0x24700, v25\n" \
"v_add_u32_e64 v25, v25, v20\n" \
"v_mfma_f32_16x16x32_bf16 v[64:67], a[252:255], a[192:195], v[64:67]\n" \
"ds_read_b128 a[128:131], v28 offset:0\n" \
"v_lshrrev_b32_e64 v26, 2, v20\n" \
"v_mfma_f32_16x16x32_bf16 v[80:83], a[188:191], a[132:135], v[80:83]\n" \
"ds_read_b128 a[192:195], v29 offset:0\n" \
"v_add_u32_e64 v26, s45, v26\n" \
"v_mfma_f32_16x16x32_bf16 v[80:83], a[252:255], a[196:199], v[80:83]\n" \
"v_add_u32_e32 v27, s46, v20\n" \
"v_add_u32_e32 v21, s43, v27\n" \
"v_mfma_f32_16x16x32_bf16 v[76:79], a[184:187], a[132:135], v[76:79]\n" \
"v_mfma_f32_16x16x32_bf16 v[76:79], a[248:251], a[196:199], v[76:79]\n" \
"v_mfma_f32_16x16x32_bf16 v[72:75], a[180:183], a[132:135], v[72:75]\n" \
"v_mfma_f32_16x16x32_bf16 v[72:75], a[244:247], a[196:199], v[72:75]\n" \
"v_mfma_f32_16x16x32_bf16 v[68:71], a[176:179], a[132:135], v[68:71]\n" \
"v_mfma_f32_16x16x32_bf16 v[68:71], a[240:243], a[196:199], v[68:71]\n" \
"ds_read_b128 a[132:135], v28 offset:2048\n" \
"ds_read_b128 a[196:199], v29 offset:2048\n" \
"s_waitcnt lgkmcnt(15)\n" \
"s_nop 0\n" \
"v_mfma_f32_16x16x32_bf16 v[184:187], a[160:163], a[144:147], v[184:187]\n" \
"v_bfe_u32 v44, v20, 2, 5\n" \
"ds_read_b64_tr_b16 a[176:177], v36 offset:0\n" \
"v_mfma_f32_16x16x32_bf16 v[184:187], a[224:227], a[208:211], v[184:187]\n" \
"buffer_load_dword v44, v44, s[80:83], s38 offen offset:0 sc1\n" \
"v_mfma_f32_16x16x32_bf16 v[188:191], a[164:167], a[144:147], v[188:191]\n" \
"ds_read_b64_tr_b16 a[178:179], v36 offset:1024\n" \
"v_mfma_f32_16x16x32_bf16 v[188:191], a[228:231], a[208:211], v[188:191]\n" \
"ds_read_b64_tr_b16 a[240:241], v36 offset:8192\n" \
"v_mfma_f32_16x16x32_bf16 v[196:199], a[172:175], a[144:147], v[196:199]\n" \
"ds_read_b64_tr_b16 a[242:243], v36 offset:9216\n" \
"s_waitcnt lgkmcnt(15)\n" \
"v_mfma_f32_16x16x32_bf16 v[196:199], a[236:239], a[208:211], v[196:199]\n" \
"s_waitcnt lgkmcnt(12)\n" \
"v_mfma_f32_16x16x32_bf16 v[192:195], a[144:147], a[168:171], v[192:195]\n" \
"ds_read_b64_tr_b16 a[180:181], v37 offset:0\n" \
"s_waitcnt lgkmcnt(9)\n" \
"s_nop 0\n" \
"v_mfma_f32_16x16x32_bf16 v[192:195], a[208:211], a[232:235], v[192:195]\n" \
"v_mfma_f32_16x16x32_bf16 v[208:211], a[148:151], a[168:171], v[208:211]\n" \
"ds_read_b64_tr_b16 a[182:183], v37 offset:1024\n" \
"v_mfma_f32_16x16x32_bf16 v[208:211], a[212:215], a[232:235], v[208:211]\n" \
"ds_read_b64_tr_b16 a[244:245], v37 offset:8192\n" \
"v_mfma_f32_16x16x32_bf16 v[212:215], a[172:175], a[148:151], v[212:215]\n" \
"v_mfma_f32_16x16x32_bf16 v[212:215], a[236:239], a[212:215], v[212:215]\n" \
"ds_read_b64_tr_b16 a[246:247], v37 offset:9216\n" \
"v_cvt_pk_bf16_f32 v188, v188, v189\n" \
"v_mfma_f32_16x16x32_bf16 v[204:207], a[164:167], a[148:151], v[204:207]\n" \
"v_cvt_pk_bf16_f32 v189, v190, v191\n" \
"v_cvt_pk_bf16_f32 v190, v198, v199\n" \
"v_mfma_f32_16x16x32_bf16 v[204:207], a[228:231], a[212:215], v[204:207]\n" \
"ds_read_b64_tr_b16 a[184:185], v38 offset:0\n" \
"v_mov_b32_e32 v191, 0x3f80\n" \
"v_mfma_f32_16x16x32_bf16 v[200:203], a[160:163], a[148:151], v[200:203]\n" \
"ds_read_b64_tr_b16 a[186:187], v38 offset:1024\n" \
"v_cvt_pk_bf16_f32 v184, v184, v185\n" \
"v_mfma_f32_16x16x32_bf16 v[200:203], a[224:227], a[212:215], v[200:203]\n" \
"v_cvt_pk_bf16_f32 v185, v186, v187\n" \
"v_cvt_pk_bf16_f32 v186, v196, v197\n" \
"v_mfma_f32_16x16x32_bf16 v[216:219], a[160:163], a[152:155], v[216:219]\n" \
"ds_read_b64_tr_b16 a[248:249], v38 offset:8192\n" \
"v_mov_b32_e64 v187, 0\n" \
"v_mfma_f32_16x16x32_bf16 v[216:219], a[224:227], a[216:219], v[216:219]\n" \
"ds_read_b64_tr_b16 a[250:251], v38 offset:9216\n" \
"v_mfma_f32_16x16x32_bf16 v[220:223], a[164:167], a[152:155], v[220:223]\n" \
"v_mfma_f32_16x16x32_bf16 v[220:223], a[228:231], a[216:219], v[220:223]\n" \
"ds_read_b64_tr_b16 a[188:189], v39 offset:0\n" \
"v_cvt_pk_bf16_f32 v204, v204, v205\n" \
"v_mfma_f32_16x16x32_bf16 v[228:231], a[172:175], a[152:155], v[228:231]\n" \
"v_cvt_pk_bf16_f32 v205, v206, v207\n" \
"v_cvt_pk_bf16_f32 v206, v214, v215\n" \
"v_mfma_f32_16x16x32_bf16 v[228:231], a[236:239], a[216:219], v[228:231]\n" \
"ds_read_b64_tr_b16 a[190:191], v39 offset:1024\n" \
"v_mov_b32_e32 v207, 0x3f80\n" \
"v_mfma_f32_16x16x32_bf16 v[224:227], a[152:155], a[168:171], v[224:227]\n" \
"ds_read_b64_tr_b16 a[252:253], v39 offset:8192\n" \
"v_cvt_pk_bf16_f32 v200, v200, v201\n" \
"v_mfma_f32_16x16x32_bf16 v[224:227], a[216:219], a[232:235], v[224:227]\n" \
"v_cvt_pk_bf16_f32 v201, v202, v203\n" \
"v_cvt_pk_bf16_f32 v202, v212, v213\n" \
"v_mfma_f32_16x16x32_bf16 v[240:243], a[156:159], a[168:171], v[240:243]\n" \
"ds_read_b64_tr_b16 a[254:255], v39 offset:9216\n" \
"v_mov_b32_e64 v203, 0\n" \
"v_mfma_f32_16x16x32_bf16 v[240:243], a[220:223], a[232:235], v[240:243]\n" \
"v_mfma_f32_16x16x32_bf16 v[244:247], a[172:175], a[156:159], v[244:247]\n" \
"v_mfma_f32_16x16x32_bf16 v[244:247], a[236:239], a[220:223], v[244:247]\n" \
"v_cvt_pk_bf16_f32 v220, v220, v221\n" \
"v_cvt_pk_bf16_f32 v221, v222, v223\n" \
"v_mfma_f32_16x16x32_bf16 v[236:239], a[164:167], a[156:159], v[236:239]\n" \
"v_cvt_pk_bf16_f32 v222, v230, v231\n" \
"v_mov_b32_e32 v223, 0x3f80\n" \
"v_mfma_f32_16x16x32_bf16 v[236:239], a[228:231], a[220:223], v[236:239]\n" \
"v_cvt_pk_bf16_f32 v216, v216, v217\n" \
"v_cvt_pk_bf16_f32 v217, v218, v219\n" \
"v_mfma_f32_16x16x32_bf16 v[232:235], a[160:163], a[156:159], v[232:235]\n" \
"v_cvt_pk_bf16_f32 v218, v228, v229\n" \
"v_mov_b32_e64 v219, 0\n" \
"v_mfma_f32_16x16x32_bf16 v[232:235], a[224:227], a[220:223], v[232:235]\n" \
"v_mfma_f32_16x16x32_bf16 v[180:183], a[164:167], a[140:143], v[180:183]\n" \
"v_mfma_f32_16x16x32_bf16 v[180:183], a[228:231], a[204:207], v[180:183]\n" \
"v_mfma_f32_16x16x32_bf16 v[48:51], a[172:175], a[140:143], v[48:51]\n" \
"v_mfma_f32_16x16x32_bf16 v[48:51], a[236:239], a[204:207], v[48:51]\n" \
"v_cvt_pk_bf16_f32 v236, v236, v237\n" \
"v_cvt_pk_bf16_f32 v237, v238, v239\n" \
"v_mfma_f32_16x16x32_bf16 v[252:255], a[140:143], a[168:171], v[252:255]\n" \
"v_cvt_pk_bf16_f32 v238, v246, v247\n" \
"v_mov_b32_e32 v239, 0x3f80\n" \
"v_mfma_f32_16x16x32_bf16 v[252:255], a[204:207], a[232:235], v[252:255]\n" \
"v_cvt_pk_bf16_f32 v232, v232, v233\n" \
"v_cvt_pk_bf16_f32 v233, v234, v235\n" \
"s_waitcnt vmcnt(0) lgkmcnt(0)\n" \
"s_barrier\n" \
"v_mov_b32_e32 v45, s62\n" \
"s_mov_b64 exec, 1\n" \
"buffer_store_dword v45, off, s[80:83], s37 offset:32 sc1\n" \
"s_mov_b64 exec, -1\n" \
"v_cmp_ne_u32_e32 vcc, s62, v44\n" \
"s_nop 4\n" \
"s_cbranch_vccnz L_poll161_%=\n" \
"L_pollr161_%=:\n" \
"v_mfma_f32_16x16x32_bf16 v[116:119], a[176:179], a[144:147], v[116:119]\n" \
"s_mov_b32 s73, s50\n" \
"s_sub_u32 m0, s31, 0x10000\n" \
"v_mfma_f32_16x16x32_bf16 v[116:119], a[240:243], a[208:211], v[116:119]\n" \
"buffer_load_dwordx4 v20, s[80:83], s73 offen offset:0 sc1 lds\n" \
"v_mfma_f32_16x16x32_bf16 v[120:123], a[180:183], a[144:147], v[120:123]\n" \
"s_add_u32 s74, s50, 0x4000\n" \
"s_add_u32 s75, s50, 0x8000\n" \
"v_mfma_f32_16x16x32_bf16 v[120:123], a[244:247], a[208:211], v[120:123]\n" \
"s_mov_b32 s70, 0x3e0293ee\n" \
"s_mov_b32 s76, s52\n" \
"v_mfma_f32_16x16x32_bf16 v[124:127], a[184:187], a[144:147], v[124:127]\n" \
"v_mul_f32_e32 v116, s70, v116\n" \
"v_mul_f32_e32 v117, s70, v117\n" \
"v_mfma_f32_16x16x32_bf16 v[124:127], a[248:251], a[208:211], v[124:127]\n" \
"buffer_load_dwordx4 v20, s[80:83], s73 offen offset:1024 sc1 lds\n" \
"v_mfma_f32_16x16x32_bf16 v[128:131], a[188:191], a[144:147], v[128:131]\n" \
"v_mul_f32_e32 v118, s70, v118\n" \
"v_mul_f32_e32 v119, s70, v119\n" \
"v_mfma_f32_16x16x32_bf16 v[128:131], a[252:255], a[208:211], v[128:131]\n" \
"v_cvt_pk_bf16_f32 v116, v116, v117\n" \
"v_mul_f32_e64 v120, s70, v120\n" \
"v_mfma_f32_16x16x32_bf16 v[144:147], a[188:191], a[148:151], v[144:147]\n" \
"v_mul_f32_e32 v124, s70, v124\n" \
"v_mul_f32_e32 v125, s70, v125\n" \
"v_mfma_f32_16x16x32_bf16 v[144:147], a[252:255], a[212:215], v[144:147]\n" \
"buffer_load_dwordx4 v20, s[80:83], s73 offen offset:2048 sc1 lds\n" \
"v_mfma_f32_16x16x32_bf16 v[140:143], a[184:187], a[148:151], v[140:143]\n" \
"v_mul_f32_e32 v126, s70, v126\n" \
"v_mul_f32_e32 v127, s70, v127\n" \
"v_mfma_f32_16x16x32_bf16 v[140:143], a[248:251], a[212:215], v[140:143]\n" \
"v_cvt_pk_bf16_f32 v124, v124, v125\n" \
"v_mul_f32_e64 v121, s70, v121\n" \
"v_mfma_f32_16x16x32_bf16 v[136:139], a[180:183], a[148:151], v[136:139]\n" \
".fill (L_pb + 77536 - .) / 4, 4, 0xBF800000\n" \
"L_pp19:\n" \
"s_cbranch_execz L_pret\n" \
"v_mul_f32_e32 v122, s70, v122\n" \
"v_mul_f32_e64 v123, s70, v123\n" \
"v_mfma_f32_16x16x32_bf16 v[136:139], a[244:247], a[212:215], v[136:139]\n" \
"buffer_load_dwordx4 v20, s[80:83], s73 offen offset:3072 sc1 lds\n" \
"v_mfma_f32_16x16x32_bf16 v[132:135], a[176:179], a[148:151], v[132:135]\n" \
"s_sub_u32 m0, s31, s51\n" \
"v_cvt_pk_bf16_f32 v117, v118, v119\n" \
"v_mfma_f32_16x16x32_bf16 v[132:135], a[240:243], a[212:215], v[132:135]\n" \
"v_mul_f32_e32 v128, s70, v128\n" \
"v_mul_f32_e64 v129, s70, v129\n" \
"v_mfma_f32_16x16x32_bf16 v[148:151], a[176:179], a[152:155], v[148:151]\n" \
"v_mul_f32_e32 v130, s70, v130\n" \
"v_mul_f32_e32 v131, s70, v131\n" \
"v_mfma_f32_16x16x32_bf16 v[148:151], a[240:243], a[216:219], v[148:151]\n" \
"buffer_load_dwordx4 v20, s[80:83], s74 offen offset:0 sc1 lds\n" \
"v_mfma_f32_16x16x32_bf16 v[152:155], a[180:183], a[152:155], v[152:155]\n" \
"v_cvt_pk_bf16_f32 v125, v126, v127\n" \
"v_cvt_pk_bf16_f32 v118, v120, v121\n" \
"v_mfma_f32_16x16x32_bf16 v[152:155], a[244:247], a[216:219], v[152:155]\n" \
"v_cvt_pk_bf16_f32 v119, v122, v123\n" \
"v_cvt_pk_bf16_f32 v126, v128, v129\n" \
"v_mfma_f32_16x16x32_bf16 v[156:159], a[184:187], a[152:155], v[156:159]\n" \
"v_cvt_pk_bf16_f32 v127, v130, v131\n" \
"v_mul_f32_e64 v132, s70, v132\n" \
"v_mfma_f32_16x16x32_bf16 v[156:159], a[248:251], a[216:219], v[156:159]\n" \
"buffer_load_dwordx4 v20, s[80:83], s74 offen offset:1024 sc1 lds\n" \
"v_mfma_f32_16x16x32_bf16 v[160:163], a[188:191], a[152:155], v[160:163]\n" \
"ds_write_b128 v21, v[116:119] offset:16384\n" \
"v_mfma_f32_16x16x32_bf16 v[160:163], a[252:255], a[216:219], v[160:163]\n" \
"v_mul_f32_e32 v133, s70, v133\n" \
"v_mul_f32_e32 v140, s70, v140\n" \
"v_mfma_f32_16x16x32_bf16 v[176:179], a[188:191], a[156:159], v[176:179]\n" \
"v_mul_f32_e32 v141, s70, v141\n" \
"v_mul_f32_e32 v134, s70, v134\n" \
"v_mfma_f32_16x16x32_bf16 v[176:179], a[252:255], a[220:223], v[176:179]\n" \
"buffer_load_dwordx4 v20, s[80:83], s74 offen offset:2048 sc1 lds\n" \
"v_mfma_f32_16x16x32_bf16 v[172:175], a[184:187], a[156:159], v[172:175]\n" \
"ds_write_b128 v21, v[124:127] offset:17408\n" \
"v_mfma_f32_16x16x32_bf16 v[172:175], a[248:251], a[220:223], v[172:175]\n" \
"v_mul_f32_e64 v135, s70, v135\n" \
"v_cvt_pk_bf16_f32 v132, v132, v133\n" \
"v_mfma_f32_16x16x32_bf16 v[168:171], a[180:183], a[156:159], v[168:171]\n" \
"v_mul_f32_e32 v142, s70, v142\n" \
"v_mul_f32_e32 v143, s70, v143\n" \
"v_mfma_f32_16x16x32_bf16 v[168:171], a[244:247], a[220:223], v[168:171]\n" \
"buffer_load_dwordx4 v20, s[80:83], s74 offen offset:3072 sc1 lds\n" \
"v_mfma_f32_16x16x32_bf16 v[164:167], a[176:179], a[156:159], v[164:167]\n" \
"s_sub_u32 m0, s31, 0x4000\n" \
"v_cvt_pk_bf16_f32 v140, v140, v141\n" \
"v_mfma_f32_16x16x32_bf16 v[164:167], a[240:243], a[220:223], v[164:167]\n" \
"v_mul_f32_e32 v136, s70, v136\n" \
"v_mul_f32_e32 v137, s70, v137\n" \
"v_mfma_f32_16x16x32_bf16 v[84:87], a[176:179], a[136:139], v[84:87]\n" \
"v_mul_f32_e32 v138, s70, v138\n" \
"v_mul_f32_e32 v139, s70, v139\n" \
"v_mfma_f32_16x16x32_bf16 v[84:87], a[240:243], a[200:203], v[84:87]\n" \
"buffer_load_dwordx4 v20, s[80:83], s75 offen offset:0 sc1 lds\n" \
"v_mfma_f32_16x16x32_bf16 v[88:91], a[180:183], a[136:139], v[88:91]\n" \
"v_cvt_pk_bf16_f32 v133, v134, v135\n" \
"v_mul_f32_e64 v144, s70, v144\n" \
"v_mfma_f32_16x16x32_bf16 v[88:91], a[244:247], a[200:203], v[88:91]\n" \
"v_mul_f32_e32 v145, s70, v145\n" \
"v_mul_f32_e32 v146, s70, v146\n" \
"v_mfma_f32_16x16x32_bf16 v[92:95], a[184:187], a[136:139], v[92:95]\n" \
"v_mul_f32_e64 v147, s70, v147\n" \
"v_cvt_pk_bf16_f32 v141, v142, v143\n" \
"v_mfma_f32_16x16x32_bf16 v[92:95], a[248:251], a[200:203], v[92:95]\n" \
"buffer_load_dwordx4 v20, s[80:83], s75 offen offset:1024 sc1 lds\n" \
"v_mfma_f32_16x16x32_bf16 v[96:99], a[188:191], a[136:139], v[96:99]\n" \
"v_cvt_pk_bf16_f32 v134, v136, v137\n" \
"v_cvt_pk_bf16_f32 v135, v138, v139\n" \
"v_mfma_f32_16x16x32_bf16 v[96:99], a[252:255], a[200:203], v[96:99]\n" \
"v_cvt_pk_bf16_f32 v142, v144, v145\n" \
"v_cvt_pk_bf16_f32 v143, v146, v147\n" \
"v_mfma_f32_16x16x32_bf16 v[112:115], a[188:191], a[140:143], v[112:115]\n" \
"ds_write_b128 v21, v[132:135] offset:20480\n" \
"v_mfma_f32_16x16x32_bf16 v[112:115], a[252:255], a[204:207], v[112:115]\n" \
"buffer_load_dwordx4 v20, s[80:83], s75 offen offset:2048 sc1 lds\n" \
"v_mfma_f32_16x16x32_bf16 v[108:111], a[184:187], a[140:143], v[108:111]\n" \
"v_cvt_pk_bf16_f32 v180, v180, v181\n" \
"v_cvt_pk_bf16_f32 v181, v182, v183\n" \
"v_mfma_f32_16x16x32_bf16 v[108:111], a[248:251], a[204:207], v[108:111]\n" \
"ds_write_b128 v21, v[140:143] offset:21504\n" \
"v_mfma_f32_16x16x32_bf16 v[104:107], a[180:183], a[140:143], v[104:107]\n" \
"v_cvt_pk_bf16_f32 v234, v244, v245\n" \
"v_mov_b32_e64 v235, 0\n" \
"v_mfma_f32_16x16x32_bf16 v[104:107], a[244:247], a[204:207], v[104:107]\n" \
"buffer_load_dwordx4 v20, s[80:83], s75 offen offset:3072 sc1 lds\n" \
"v_mfma_f32_16x16x32_bf16 v[100:103], a[176:179], a[140:143], v[100:103]\n" \
"s_mov_b32 m0, s53\n" \
"s_nop 0\n" \
"v_cvt_pk_bf16_f32 v182, v50, v51\n" \
"v_mfma_f32_16x16x32_bf16 v[100:103], a[240:243], a[204:207], v[100:103]\n" \
"v_mov_b32_e32 v183, 0x3f80\n" \
"v_cvt_pk_bf16_f32 v118, v192, v193\n" \
"v_mfma_f32_16x16x32_bf16 v[52:55], a[176:179], a[128:131], v[52:55]\n" \
"v_cvt_pk_bf16_f32 v119, v194, v195\n" \
"v_cvt_pk_bf16_f32 v120, v208, v209\n" \
"v_mfma_f32_16x16x32_bf16 v[52:55], a[240:243], a[192:195], v[52:55]\n" \
"buffer_load_dwordx4 v20, s[80:83], s76 offen offset:0 sc1 lds\n" \
"v_mfma_f32_16x16x32_bf16 v[56:59], a[180:183], a[128:131], v[56:59]\n" \
"v_cvt_pk_bf16_f32 v121, v210, v211\n" \
"v_cvt_pk_bf16_f32 v122, v224, v225\n" \
"v_mfma_f32_16x16x32_bf16 v[56:59], a[244:247], a[192:195], v[56:59]\n" \
"v_cvt_pk_bf16_f32 v123, v226, v227\n" \
"v_cvt_pk_bf16_f32 v124, v240, v241\n" \
"v_cvt_pk_bf16_f32 v125, v242, v243\n" \
"v_mfma_f32_16x16x32_bf16 v[60:63], a[184:187], a[128:131], v[60:63]\n" \
"v_mfma_f32_16x16x32_bf16 v[192:195], v[188:191], v[184:187], 0\n" \
"buffer_load_dwordx4 v20, s[80:83], s76 offen offset:1024 sc1 lds\n" \
"v_mfma_f32_16x16x32_bf16 v[32:35], v[180:183], v[184:187], 0\n" \
"v_mfma_f32_16x16x32_bf16 v[208:211], v[204:207], v[200:203], 0\n" \
"v_mfma_f32_16x16x32_bf16 v[36:39], v[188:191], v[200:203], 0\n" \
"v_mfma_f32_16x16x32_bf16 v[224:227], v[220:223], v[216:219], 0\n" \
"v_mfma_f32_16x16x32_bf16 v[248:251], v[204:207], v[216:219], 0\n" \
"v_mfma_f32_16x16x32_bf16 v[240:243], v[236:239], v[232:235], 0\n" \
"v_mfma_f32_16x16x32_bf16 v[128:131], v[220:223], v[232:235], 0\n" \
"s_mov_b32 s64, 0xaaaaaaaa\n" \
"s_mov_b32 s65, 0xaaaaaaaa\n" \
"v_mfma_f32_16x16x32_bf16 v[60:63], a[248:251], a[192:195], v[60:63]\n" \
"s_mov_b32 s66, 0xfff0ffff\n" \
"s_mov_b32 s67, 0xf000ff00\n" \
"v_mfma_f32_16x16x32_bf16 v[64:67], a[188:191], a[128:131], v[64:67]\n" \
"s_mov_b32 s68, 0xffc0fffc\n" \
"s_mov_b32 s69, 0xc000fc00\n" \
"v_cndmask_b32_e64 v192, v192, v193, s[64:65]\n" \
"v_cndmask_b32_e64 v32, v32, v33, s[64:65]\n" \
"v_cndmask_b32_e64 v194, v194, v195, s[64:65]\n" \
"v_cndmask_b32_e64 v34, v34, v35, s[64:65]\n" \
"v_cndmask_b32_e64 v208, v208, v209, s[64:65]\n" \
"v_cndmask_b32_e64 v36, v36, v37, s[64:65]\n" \
"v_cndmask_b32_e64 v210, v210, v211, s[64:65]\n" \
"v_cndmask_b32_e64 v38, v38, v39, s[64:65]\n" \
"v_cndmask_b32_e64 v224, v224, v225, s[64:65]\n" \
"v_cndmask_b32_e64 v248, v248, v249, s[64:65]\n" \
"v_cndmask_b32_e64 v226, v226, v227, s[64:65]\n" \
"v_cndmask_b32_e64 v250, v250, v251, s[64:65]\n" \
"v_cndmask_b32_e64 v240, v240, v241, s[64:65]\n" \
"v_cndmask_b32_e64 v128, v128, v129, s[64:65]\n" \
"v_cndmask_b32_e64 v242, v242, v243, s[64:65]\n" \
"v_cndmask_b32_e64 v130, v130, v131, s[64:65]\n" \
"v_cndmask_b32_e64 v192, v32, v192, s[66:67]\n" \
"v_cndmask_b32_e64 v194, v34, v194, s[68:69]\n" \
"v_cndmask_b32_e64 v208, v36, v208, s[66:67]\n" \
"v_cndmask_b32_e64 v210, v38, v210, s[68:69]\n" \
"v_cndmask_b32_e64 v224, v248, v224, s[66:67]\n" \
"v_cndmask_b32_e64 v226, v250, v226, s[68:69]\n" \
"v_cndmask_b32_e64 v240, v128, v240, s[66:67]\n" \
"v_cndmask_b32_e64 v242, v130, v242, s[68:69]\n" \
"v_mfma_f32_16x16x32_bf16 v[64:67], a[252:255], a[192:195], v[64:67]\n" \
"v_max_f32_e32 v193, v192, v194\n" \
"v_max_f32_e32 v209, v208, v210\n" \
"v_max_f32_e32 v225, v224, v226\n" \
"v_max_f32_e32 v241, v240, v242\n" \
"v_mul_f32_e32 v148, s70, v148\n" \
"v_permlane32_swap_b32_e32 v193, v225\n" \
"v_permlane32_swap_b32_e32 v209, v241\n" \
"v_mul_f32_e32 v149, s70, v149\n" \
"v_mul_f32_e32 v156, s70, v156\n" \
"v_mul_f32_e32 v157, s70, v157\n" \
"v_permlane32_swap_b32_e32 v196, v228\n" \
"v_permlane32_swap_b32_e32 v212, v244\n" \
"v_max_f32_e32 v193, v193, v225\n" \
"v_max_f32_e32 v209, v209, v241\n" \
"v_mul_f32_e32 v150, s70, v150\n" \
"v_mul_f32_e32 v151, s70, v151\n" \
"v_cvt_pk_bf16_f32 v148, v148, v149\n" \
"v_mul_f32_e32 v158, s70, v158\n" \
"v_mul_f32_e32 v159, s70, v159\n" \
"v_cvt_pk_bf16_f32 v156, v156, v157\n" \
"v_permlane16_swap_b32_e32 v228, v244\n" \
"v_permlane16_swap_b32_e32 v193, v209\n" \
"v_mfma_f32_16x16x32_bf16 v[80:83], a[188:191], a[132:135], v[80:83]\n" \
"s_mov_b32 s72, 0x3eae1a93\n" \
"v_mul_f32_e32 v152, s70, v152\n" \
"v_mul_f32_e32 v153, s70, v153\n" \
"v_mul_f32_e32 v154, s70, v154\n" \
"v_mul_f32_e32 v155, s70, v155\n" \
"v_cvt_pk_bf16_f32 v149, v150, v151\n" \
"v_mul_f32_e32 v160, s70, v160\n" \
"v_mul_f32_e32 v161, s70, v161\n" \
"v_mul_f32_e32 v162, s70, v162\n" \
"v_mul_f32_e32 v163, s70, v163\n" \
"v_cvt_pk_bf16_f32 v157, v158, v159\n" \
"v_mul_f32_e32 v45, 0xbfb8aa3b, v228\n" \
"v_mfma_f32_16x16x16_bf16 v[48:51], v[182:183], v[46:47], 0\n" \
"v_mfma_f32_16x16x16_bf16 v[196:199], v[190:191], v[46:47], 0\n" \
"v_mfma_f32_16x16x16_bf16 v[212:215], v[206:207], v[46:47], 0\n" \
"v_max_f32_e64 v193, v193, v209\n" \
"v_cvt_pk_bf16_f32 v150, v152, v153\n" \
"v_cvt_pk_bf16_f32 v151, v154, v155\n" \
"v_cvt_pk_bf16_f32 v158, v160, v161\n" \
"v_cvt_pk_bf16_f32 v159, v162, v163\n" \
"v_mfma_f32_16x16x16_bf16 v[228:231], v[222:223], v[46:47], 0\n" \
"v_mul_f32_e32 v193, s72, v193\n" \
"v_mov_b32_e32 v209, v193\n" \
"ds_write_b128 v21, v[148:151] offset:24576\n" \
"v_mfma_f32_16x16x16_bf16 v[244:247], v[238:239], v[46:47], 0\n" \
"v_permlane16_swap_b32_e32 v193, v209\n" \
"s_mov_b32 s71, 0x3e0293ee\n" \
"v_mfma_f32_16x16x32_bf16 v[80:83], a[252:255], a[196:199], v[80:83]\n" \
"v_mov_b32_e32 v225, v193\n" \
"v_mov_b32_e32 v241, v209\n" \
"v_mul_f32_e32 v164, s70, v164\n" \
"v_mul_f32_e64 v165, s70, v165\n" \
"ds_write_b128 v21, v[156:159] offset:25600\n" \
"v_mul_f32_e32 v172, s70, v172\n" \
"v_mul_f32_e32 v173, s70, v173\n" \
"v_pk_mul_f32 v[84:85], v[84:85], s[70:71]\n" \
"v_pk_mul_f32 v[92:93], v[92:93], s[70:71]\n" \
"v_pk_mul_f32 v[100:101], v[100:101], s[70:71]\n" \
"v_pk_mul_f32 v[108:109], v[108:109], s[70:71]\n" \
"v_pk_mul_f32 v[52:53], v[52:53], s[70:71]\n" \
"v_pk_mul_f32 v[60:61], v[60:61], s[70:71]\n" \
"v_permlane32_swap_b32_e32 v193, v225\n" \
"v_permlane32_swap_b32_e32 v209, v241\n" \
"v_mul_f32_e32 v166, s70, v166\n" \
"v_mul_f32_e32 v167, s70, v167\n" \
"v_cvt_pk_bf16_f32 v164, v164, v165\n" \
"v_mul_f32_e32 v174, s70, v174\n" \
"v_mul_f32_e32 v175, s70, v175\n" \
"v_cvt_pk_bf16_f32 v172, v172, v173\n" \
"v_pk_mul_f32 v[86:87], v[86:87], s[70:71]\n" \
"v_cvt_pk_bf16_f32 v84, v84, v85\n" \
"v_pk_mul_f32 v[94:95], v[94:95], s[70:71]\n" \
"v_cvt_pk_bf16_f32 v92, v92, v93\n" \
"v_pk_mul_f32 v[102:103], v[102:103], s[70:71]\n" \
"v_cvt_pk_bf16_f32 v100, v100, v101\n" \
"v_pk_mul_f32 v[110:111], v[110:111], s[70:71]\n" \
"v_cvt_pk_bf16_f32 v108, v108, v109\n" \
"v_pk_mul_f32 v[54:55], v[54:55], s[70:71]\n" \
"v_cvt_pk_bf16_f32 v52, v52, v53\n" \
"v_pk_mul_f32 v[62:63], v[62:63], s[70:71]\n" \
"v_cvt_pk_bf16_f32 v60, v60, v61\n" \
"v_fma_f32 v192, v192, s72, -v193\n" \
"v_fma_f32 v194, v194, s72, -v193\n" \
"v_fma_f32 v208, v208, s72, -v209\n" \
"v_fma_f32 v210, v210, s72, -v209\n" \
"v_mul_f32_e32 v168, s70, v168\n" \
"v_mul_f32_e32 v169, s70, v169\n" \
"v_mul_f32_e32 v170, s70, v170\n" \
"v_mul_f32_e32 v171, s70, v171\n" \
"v_cvt_pk_bf16_f32 v165, v166, v167\n" \
"v_mul_f32_e32 v176, s70, v176\n" \
"v_mul_f32_e32 v177, s70, v177\n" \
"v_mul_f32_e32 v178, s70, v178\n" \
"v_mul_f32_e32 v179, s70, v179\n" \
"v_cvt_pk_bf16_f32 v173, v174, v175\n" \
"v_pk_mul_f32 v[88:89], v[88:89], s[70:71]\n" \
"v_pk_mul_f32 v[90:91], v[90:91], s[70:71]\n" \
"v_cvt_pk_bf16_f32 v85, v86, v87\n" \
"v_pk_mul_f32 v[96:97], v[96:97], s[70:71]\n" \
"v_pk_mul_f32 v[98:99], v[98:99], s[70:71]\n" \
"v_cvt_pk_bf16_f32 v93, v94, v95\n" \
"v_pk_mul_f32 v[104:105], v[104:105], s[70:71]\n" \
"v_pk_mul_f32 v[106:107], v[106:107], s[70:71]\n" \
"v_cvt_pk_bf16_f32 v101, v102, v103\n" \
"v_pk_mul_f32 v[112:113], v[112:113], s[70:71]\n" \
"v_pk_mul_f32 v[114:115], v[114:115], s[70:71]\n" \
"v_cvt_pk_bf16_f32 v109, v110, v111\n" \
"v_pk_mul_f32 v[56:57], v[56:57], s[70:71]\n" \
"v_pk_mul_f32 v[58:59], v[58:59], s[70:71]\n" \
"v_cvt_pk_bf16_f32 v53, v54, v55\n" \
"v_pk_mul_f32 v[64:65], v[64:65], s[70:71]\n" \
"v_pk_mul_f32 v[66:67], v[66:67], s[70:71]\n" \
"v_cvt_pk_bf16_f32 v61, v62, v63\n" \
"v_pk_mul_f32 v[80:81], v[80:81], s[70:71]\n" \
"v_pk_mul_f32 v[82:83], v[82:83], s[70:71]\n" \
"v_exp_f32_e32 v192, v192\n" \
"v_exp_f32_e32 v194, v194\n" \
"v_exp_f32_e32 v208, v208\n" \
"v_exp_f32_e32 v210, v210\n" \
"v_fma_f32 v224, v224, s72, -v225\n" \
"v_fma_f32 v226, v226, s72, -v225\n" \
"v_mfma_f32_16x16x32_bf16 v[76:79], a[184:187], a[132:135], v[76:79]\n" \
"v_cvt_pk_bf16_f32 v166, v168, v169\n" \
"v_cvt_pk_bf16_f32 v167, v170, v171\n" \
"v_cvt_pk_bf16_f32 v174, v176, v177\n" \
"v_cvt_pk_bf16_f32 v175, v178, v179\n" \
"v_cvt_pk_bf16_f32 v86, v88, v89\n" \
"v_cvt_pk_bf16_f32 v87, v90, v91\n" \
"v_cvt_pk_bf16_f32 v94, v96, v97\n" \
"v_cvt_pk_bf16_f32 v95, v98, v99\n" \
"v_cvt_pk_bf16_f32 v102, v104, v105\n" \
"v_cvt_pk_bf16_f32 v103, v106, v107\n" \
"v_cvt_pk_bf16_f32 v110, v112, v113\n" \
"v_cvt_pk_bf16_f32 v111, v114, v115\n" \
"v_cvt_pk_bf16_f32 v54, v56, v57\n" \
"v_cvt_pk_bf16_f32 v55, v58, v59\n" \
"v_cvt_pk_bf16_f32 v62, v64, v65\n" \
"v_cvt_pk_bf16_f32 v63, v66, v67\n" \
"v_exp_f32_e32 v224, v224\n" \
"v_exp_f32_e32 v226, v226\n" \
"v_fma_f32 v240, v240, s72, -v241\n" \
"v_fma_f32 v242, v242, s72, -v241\n" \
"ds_write_b128 v21, v[164:167] offset:28672\n" \
"v_cvt_pk_bf16_f32 v192, v192, v194\n" \
"v_cvt_pk_bf16_f32 v208, v208, v210\n" \
"v_exp_f32_e32 v240, v240\n" \
"v_exp_f32_e32 v242, v242\n" \
"v_cvt_pk_bf16_f32 v132, v48, v49\n" \
"v_cvt_pk_bf16_f32 v133, v50, v51\n" \
"v_cvt_pk_bf16_f32 v134, v196, v197\n" \
"v_cvt_pk_bf16_f32 v135, v198, v199\n" \
"ds_write_b128 v21, v[172:175] offset:29696\n" \
"v_cvt_pk_bf16_f32 v136, v212, v213\n" \
"v_cvt_pk_bf16_f32 v137, v214, v215\n" \
"v_perm_b32 v184, v192, v192, v40\n" \
"v_perm_b32 v185, v192, v192, v41\n" \
"v_perm_b32 v186, v192, v192, v42\n" \
"v_perm_b32 v187, v192, v192, v43\n" \
"v_perm_b32 v200, v208, v208, v40\n" \
"v_perm_b32 v201, v208, v208, v41\n" \
"v_perm_b32 v202, v208, v208, v42\n" \
"v_perm_b32 v203, v208, v208, v43\n" \
"ds_write_b128 v27, v[84:87] offset:8192\n" \
"v_cvt_pk_bf16_f32 v224, v224, v226\n" \
"v_cvt_pk_bf16_f32 v138, v228, v229\n" \
"v_cvt_pk_bf16_f32 v139, v230, v231\n" \
"v_mfma_f32_16x16x32_bf16 v[32:35], v[132:135], v[184:187], 0\n" \
"v_mfma_f32_16x16x32_bf16 v[36:39], v[134:137], v[200:203], 0\n" \
"v_perm_b32 v216, v224, v224, v40\n" \
"v_perm_b32 v217, v224, v224, v41\n" \
"ds_write_b128 v27, v[92:95] offset:9216\n" \
"v_perm_b32 v218, v224, v224, v42\n" \
"v_perm_b32 v219, v224, v224, v43\n" \
"v_cvt_pk_bf16_f32 v240, v240, v242\n" \
"v_mfma_f32_16x16x32_bf16 v[76:79], a[248:251], a[196:199], v[76:79]\n" \
"v_cvt_pk_bf16_f32 v140, v244, v245\n" \
"v_cvt_pk_bf16_f32 v141, v246, v247\n" \
"v_mfma_f32_16x16x32_bf16 v[248:251], v[136:139], v[216:219], 0\n" \
"v_perm_b32 v232, v240, v240, v40\n" \
"v_perm_b32 v233, v240, v240, v41\n" \
"ds_write_b128 v27, v[100:103] offset:12288\n" \
"v_perm_b32 v234, v240, v240, v42\n" \
"v_perm_b32 v235, v240, v240, v43\n" \
"v_pk_mul_f32 v[76:77], v[76:77], s[70:71]\n" \
"v_pk_mul_f32 v[78:79], v[78:79], s[70:71]\n" \
"v_cvt_pk_bf16_f32 v76, v76, v77\n" \
"v_cvt_pk_bf16_f32 v77, v78, v79\n" \
"v_cvt_pk_bf16_f32 v78, v80, v81\n" \
"v_cvt_pk_bf16_f32 v79, v82, v83\n" \
"v_mfma_f32_16x16x32_bf16 v[128:131], v[138:141], v[232:235], 0\n" \
"ds_write_b128 v27, v[108:111] offset:13312\n" \
"v_mfma_f32_16x16x32_bf16 v[72:75], a[180:183], a[132:135], v[72:75]\n" \
"v_cvt_pk_bf16_f32 v116, v252, v253\n" \
"v_cvt_pk_bf16_f32 v117, v254, v255\n" \
"v_mfma_f32_16x16x32_bf16 v[72:75], a[244:247], a[196:199], v[72:75]\n" \
"v_exp_f32_e64 v45, v45\n" \
"v_mfma_f32_16x16x32_bf16 v[192:195], v[116:119], v[184:187], 0\n" \
"ds_write_b128 v27, v[52:55] offset:0\n" \
"v_mfma_f32_16x16x32_bf16 v[208:211], v[118:121], v[200:203], 0\n" \
"v_mfma_f32_16x16x32_bf16 v[224:227], v[120:123], v[216:219], 0\n" \
"v_mfma_f32_16x16x32_bf16 v[240:243], v[122:125], v[232:235], 0\n" \
"ds_write_b128 v27, v[60:63] offset:1024\n" \
"v_mov_b32_dpp v34, v38 quad_perm:[0,1,2,3] row_mask:0x2 bank_mask:0xf\n" \
"v_pk_mul_f32 v[72:73], v[72:73], s[70:71]\n" \
"v_pk_mul_f32 v[74:75], v[74:75], s[70:71]\n" \
"v_mov_b32_dpp v34, v250 quad_perm:[0,1,2,3] row_mask:0x4 bank_mask:0xf\n" \
"v_add_f32_e64 v45, 1.0, v45\n" \
"v_mfma_f32_16x16x32_bf16 v[68:71], a[176:179], a[132:135], v[68:71]\n" \
"v_mov_b32_dpp v34, v130 quad_perm:[0,1,2,3] row_mask:0x8 bank_mask:0xf\n" \
"v_mov_b32_dpp v32, v36 quad_perm:[0,1,2,3] row_mask:0x2 bank_mask:0xf\n" \
"v_rcp_f32_e64 v45, v45\n" \
"ds_write_b128 v27, v[76:79] offset:5120\n" \
"v_mov_b32_dpp v33, v37 quad_perm:[0,1,2,3] row_mask:0x2 bank_mask:0xf\n" \
"v_rcp_f32_e64 v34, v34\n" \
"v_mov_b32_dpp v32, v248 quad_perm:[0,1,2,3] row_mask:0x4 bank_mask:0xf\n" \
"v_mov_b32_dpp v33, v249 quad_perm:[0,1,2,3] row_mask:0x4 bank_mask:0xf\n" \
"v_mul_f32_e64 v34, v45, v34\n" \
"v_mov_b32_dpp v32, v128 quad_perm:[0,1,2,3] row_mask:0x8 bank_mask:0xf\n" \
"v_mov_b32_dpp v33, v129 quad_perm:[0,1,2,3] row_mask:0x8 bank_mask:0xf\n" \
"v_mul_f32_e32 v32, v34, v32\n" \
"v_mul_f32_e32 v33, v34, v33\n" \
"v_mov_b32_e64 v38, v34\n" \
"v_mfma_f32_16x16x32_bf16 v[68:71], a[240:243], a[196:199], v[68:71]\n" \
"v_sub_f32_e32 v44, 1.0, v45\n" \
"v_permlane16_swap_b32_e32 v34, v38\n" \
"v_mov_b32_e32 v250, v34\n" \
"v_mov_b32_e32 v130, v38\n" \
"v_cvt_pk_bf16_f32 v32, v32, v33\n" \
"v_permlane32_swap_b32_e32 v34, v250\n" \
"v_permlane32_swap_b32_e32 v38, v130\n" \
"v_mul_f32_e64 v192, v34, v192\n" \
"v_pk_mul_f32 v[68:69], v[68:69], s[70:71]\n" \
"v_pk_mul_f32 v[70:71], v[70:71], s[70:71]\n" \
"v_cvt_pk_bf16_f32 v68, v68, v69\n" \
"v_cvt_pk_bf16_f32 v69, v70, v71\n" \
"v_cvt_pk_bf16_f32 v70, v72, v73\n" \
"v_cvt_pk_bf16_f32 v71, v74, v75\n" \
"ds_write_b128 v27, v[68:71] offset:4096\n" \
"v_mul_f32_e32 v193, v34, v193\n" \
"v_mul_f32_e32 v208, v38, v208\n" \
"v_mul_f32_e32 v209, v38, v209\n" \
"v_mul_f32_e32 v224, v250, v224\n" \
"v_mul_f32_e32 v225, v250, v225\n" \
"v_mul_f32_e32 v240, v130, v240\n" \
"v_mul_f32_e32 v241, v130, v241\n" \
"v_mul_f32_e32 v194, v34, v194\n" \
"v_mul_f32_e64 v195, v34, v195\n" \
"v_cvt_pk_bf16_f32 v192, v192, v193\n" \
"v_mul_f32_e32 v210, v38, v210\n" \
"v_mul_f32_e32 v211, v38, v211\n" \
"v_cvt_pk_bf16_f32 v208, v208, v209\n" \
"v_mul_f32_e32 v226, v250, v226\n" \
"v_mul_f32_e32 v227, v250, v227\n" \
"v_cvt_pk_bf16_f32 v224, v224, v225\n" \
"v_mul_f32_e32 v242, v130, v242\n" \
"v_mul_f32_e32 v243, v130, v243\n" \
"v_cvt_pk_bf16_f32 v240, v240, v241\n" \
"v_cvt_pk_bf16_f32 v193, v194, v195\n" \
"s_add_u32 s84, s48, 0x0\n" \
"s_nop 0\n" \
"v_cvt_pk_bf16_f32 v209, v210, v211\n" \
"s_add_u32 s85, s48, 0x24800\n" \
"v_cvt_pk_bf16_f32 v225, v226, v227\n" \
"s_add_u32 s86, s48, 0x49000\n" \
"v_cvt_pk_bf16_f32 v241, v242, v243\n" \
"s_add_u32 s87, s48, 0x6d800\n" \
"ds_write_b32 v26, v44\n" \
"buffer_store_dword v32, v25, s[80:83], s49 offen sc1\n" \
"buffer_store_dwordx2 v[192:193], v24, s[80:83], s84 offen sc1\n" \
"buffer_store_dwordx2 v[208:209], v24, s[80:83], s85 offen sc1\n" \
"buffer_store_dwordx2 v[224:225], v24, s[80:83], s86 offen sc1\n" \
"buffer_store_dwordx2 v[240:241], v24, s[80:83], s87 offen sc1\n" \
"s_branch L_end_%=\n" \
"L_poll61_%=:\n" \
"s_mov_b32 s84, 0\n" \
"L_pl61_%=:\n" \
"s_sleep 1\n" \
"v_bfe_u32 v44, v20, 2, 5\n" \
"buffer_load_dword v44, v44, s[80:83], s38 offen offset:128 sc1\n" \
"s_waitcnt vmcnt(0)\n" \
"v_cmp_ne_u32_e32 vcc, s62, v44\n" \
"s_add_u32 s84, s84, 1\n" \
"s_nop 3\n" \
"s_cbranch_vccz L_pollr61_%=\n" \
"s_cmp_gt_u32 s84, 0x400000\n" \
"s_cbranch_scc0 L_pl61_%=\n" \
"s_branch L_pollr61_%=\n" \
"L_poll161_%=:\n" \
"s_mov_b32 s84, 0\n" \
"L_pl161_%=:\n" \
"s_sleep 1\n" \
"v_bfe_u32 v44, v20, 2, 5\n" \
"buffer_load_dword v44, v44, s[80:83], s38 offen offset:0 sc1\n" \
"s_waitcnt vmcnt(0)\n" \
"v_cmp_ne_u32_e32 vcc, s62, v44\n" \
"s_add_u32 s84, s84, 1\n" \
"s_nop 3\n" \
"s_cbranch_vccz L_pollr161_%=\n" \
"s_cmp_gt_u32 s84, 0x400000\n" \
"s_cbranch_scc0 L_pl161_%=\n" \
"s_branch L_pollr161_%=\n" \
"L_end_%=:\n"
#define PROJ_VCLOB "v21","v24","v25","v28","v29","v32","v33","v34","v35","v36","v37","v38","v39","v40","v41","v42","v43","v44","v45","v46","v47","v48","v49","v50","v51","v52","v53","v54","v55","v56","v57","v58","v59","v60","v61","v62","v63","v64","v65","v66","v67","v68","v69","v70","v71","v72","v73","v74","v75","v76","v77","v78","v79","v80","v81","v82","v83","v84","v85","v86","v87","v88","v89","v90","v91","v92","v93","v94","v95","v96","v97","v98","v99","v100","v101","v102","v103","v104","v105","v106","v107","v108","v109","v110","v111","v112","v113","v114","v115","v116","v117","v118","v119","v120","v121","v122","v123","v124","v125","v126","v127","v128","v129","v130","v131","v132","v133","v134","v135","v136","v137","v138","v139","v140","v141","v142","v143","v144","v145","v146","v147","v148","v149","v150","v151","v152","v153","v154","v155","v156","v157","v158","v159","v160","v161","v162","v163","v164","v165","v166","v167","v168","v169","v170","v171","v172","v173","v174","v175","v176","v177","v178","v179","v180","v181","v182","v183","v184","v185","v186","v187","v188","v189","v190","v192","v193","v194","v195","v196","v197","v198","v199","v200","v201","v202","v203","v204","v205","v206","v207","v208","v209","v210","v211","v212","v213","v214","v215","v216","v217","v218","v219","v220","v221","v222","v223","v224","v225","v226","v227","v228","v229","v230","v231","v232","v233","v234","v235","v236","v237","v238","v239","v240","v241","v242","v243","v244","v245","v246","v247","v248","v249","v250","v251","v252","v253","v254","v255"
#define PROJ_SCLOB "s4","s5","s6","s7","s32","s33","s34","s35","s36","s37","s38","s39","s40","s41","s42","s43","s44","s45","s46","s47","s48","s49","s50","s51","s52","s53","s54","s55","s63","s64","s65","s66","s67","s68","s69","s70","s71","s72","s73","s74","s75","s76","s77","s78","s79","s80","s81","s82","s83","s84","s85","s86","s87","s88","s89","s90","s91","s92","s93","s94","s95","s96","s97","s98","s99","s100","s101"

typedef const u16* const __attribute__((address_space(4)))* kaptr;
__device__ __forceinline__ void proj_phase(kaptr& ka, char*& ws, const unsigned wid, u32x4 (&vp)[5], unsigned& vl, u32x4 (&sa)[5], u32x2 s24, u32x2& s28, unsigned tdum, u32x4& srdA, u32x2& sd, unsigned& tag
    ) {
    const unsigned b = blockIdx.x, h = (b & 7) * 4 + ((b >> 3) & 3), mt = b >> 5;
    const unsigned rb = (wid < 2u ? 15u - mt : mt) * 128u;
    const unsigned lane = threadIdx.x & 63u, wc = wid & 1u;
    u32x2 vAc, vB;
#pragma unroll
    for (int par = 0; par < 2; ++par) {
        const unsigned lr = lane >> 3, cc = (lane & 7u) ^ (4u * par + (lr >> 1)), r0 = rb + wc * 64u + lr;
        vAc[par] = r0 * (DC * 2) + cc * 16u;
        const unsigned br = lane >> 4, p16 = lane & 15u, sc = (((p16 >> 1) ^ (br | (par << 2))) << 1) | (p16 & 1u);
        vB[par] = (16u * wid + br) * (NBIG * 2) + h * (HD * 2) + sc * 16u;
    }
    vl = lane * 16u;
    u32x2 s30 = {wid * 8192u, 65536u + wid * 4096u};
    asm volatile(PROJ_ASM
                 : "={s62}"(tag), "={s[56:59]}"(srdA), "={s[60:61]}"(sd), "+{s[8:11]}"(sa[0]), "+{s[12:15]}"(sa[1]), "+{s[16:19]}"(sa[2]), "+{s[20:23]}"(sa[3]), "+{s[24:25]}"(s24), "+{s[28:29]}"(s28), "+{s[30:31]}"(s30),
                   "={v[0:3]}"(vp[0]), "={v[4:7]}"(vp[1]), "={v[8:11]}"(vp[2]), "={v[12:15]}"(vp[3]), "={v[16:19]}"(vp[4]), "+{v[22:23]}"(vAc), "+{v[26:27]}"(vB), "+{v191}"(tdum), "+{s[26:27]}"(ws), "+{s[0:1]}"(ka)
                 : "{s2}"(b), "{s3}"(wid), "{v20}"(vl)
                 : "memory", "m0", "vcc", "scc", PROJ_VCLOB, PROJ_SCLOB, ACLOB_LO, ACLOB_HI);
    sa[4] = (u32x4){s24[0], 0u, 0u, 0u};
}

namespace att {
#define DI __device__ __forceinline__
typedef __bf16 bfx8 __attribute__((ext_vector_type(8)));
DI unsigned rfl(unsigned x) { return __builtin_amdgcn_readfirstlane(x); }
DI f32x4 mfma(u32x4 a, u32x4 b, f32x4 c) { return __builtin_amdgcn_mfma_f32_16x16x32_bf16(__builtin_bit_cast(bfx8, a), __builtin_bit_cast(bfx8, b), c, 0, 0, 0); }
typedef __bf16 bfx2 __attribute__((ext_vector_type(2)));
typedef float f32x2 __attribute__((ext_vector_type(2)));
DI unsigned cvtpk2(float lo, float hi) { f32x2 v = {lo, hi}; bfx2 b = __builtin_convertvector(v, bfx2); return __builtin_bit_cast(unsigned, b); }
DI void att_exact(const u16* __restrict__ x, const u16* __restrict__ wq, char* __restrict__ ws, unsigned h, unsigned r0, float g1) {
    const unsigned lane = lane_id(), g = lane >> 4, c = lane & 15u;
    f32x4 o[8];
#pragma unroll
    for (int n = 0; n < 8; ++n) o[n] = (f32x4){0.f, 0.f, 0.f, 0.f};
#pragma unroll 1
    for (unsigned kk = 0; kk < (unsigned)DM; kk += 32u) {
        const unsigned k = kk + 8u * g;
        const u32x4 xf = *reinterpret_cast<const u32x4*>(x + (r0 + c) * DM + k);
#pragma unroll
        for (int n = 0; n < 8; ++n) {
            const u16* w = wq + k * NBIG + h * HD + n * 16 + c; u32x4 wf;
#pragma unroll
            for (int e = 0; e < 4; ++e) wf[e] = (unsigned)w[2 * e * NBIG] | ((unsigned)w[(2 * e + 1) * NBIG] << 16);
            o[n] = mfma(wf, xf, o[n]);
        }
    }
    u32x4 q[4];
#pragma unroll
    for (int p = 0; p < 4; ++p) {
        const f32x4 a = o[2 * p] * 0.12751743082f, b = o[2 * p + 1] * 0.12751743082f;
        q[p] = (u32x4){cvtpk2(a[0], a[1]), cvtpk2(a[2], a[3]), cvtpk2(b[0], b[1]), cvtpk2(b[2], b[3])};
    }
#pragma unroll
    for (int n = 0; n < 8; ++n) o[n] = (f32x4){0.f, 0.f, 0.f, 0.f};
    const __amdgpu_buffer_rsrc_t srd = __builtin_amdgcn_make_buffer_rsrc((void*)(ws + WS_K + (size_t)h * 1048576), (short)0, 1048576, 0x00020000);
    float m = -3.0e38f, l = 0.f;
#pragma unroll 1
    for (unsigned k0 = 0; k0 <= r0 + 15u; k0 += 32u) {
        const unsigned tb = (k0 >> 6) * 32768u + (k0 & 32u) * 256u;
        f32x4 s[2]; float mx = m;
#pragma unroll
        for (int kt = 0; kt < 2; ++kt) {
            s[kt] = (f32x4){0.f, 0.f, 0.f, 0.f};
#pragma unroll
            for (int p = 0; p < 4; ++p) s[kt] = mfma(__builtin_amdgcn_raw_buffer_load_b128(srd, lane * 16u + (kt * 4 + p) * 1024u, tb, 16), q[p], s[kt]);
#pragma unroll
            for (int e = 0; e < 4; ++e) { if (k0 + 16u * kt + 4u * g + e > r0 + c) s[kt][e] = -3.0e38f; mx = fmaxf(mx, s[kt][e]); }
        }
        mx = fmaxf(mx, __shfl_xor(mx, 16)); mx = fmaxf(mx, __shfl_xor(mx, 32));
        const float al = __builtin_amdgcn_exp2f(m - mx); m = mx;
        u32x4 pf;
#pragma unroll
        for (int e = 0; e < 4; ++e) pf[e] = cvtpk2(__builtin_amdgcn_exp2f(s[e >> 1][2 * (e & 1)] - m), __builtin_amdgcn_exp2f(s[e >> 1][2 * (e & 1) + 1] - m));
        l *= al;
#pragma unroll
        for (int e = 0; e < 4; ++e) l += __uint_as_float(pf[e] << 16) + __uint_as_float(pf[e] & 0xffff0000u);
#pragma unroll
        for (int n = 0; n < 8; ++n) o[n] = mfma(__builtin_amdgcn_raw_buffer_load_b128(srd, lane * 16u + 16384u + n * 1024u, tb, 16), pf, o[n] * al);
    }
    l += __shfl_xor(l, 16); l += __shfl_xor(l, 32);
    const float f = g1 / l;
    const __amdgpu_buffer_rsrc_t srdA = __builtin_amdgcn_make_buffer_rsrc((void*)(ws + WS_AF), (short)0, 128 * KS_TOT * 1024, 0x00020000);
#pragma unroll
    for (int p = 0; p < 4; ++p) {
        const f32x4 a = o[2 * p] * f, b = o[2 * p + 1] * f;
        __builtin_amdgcn_raw_buffer_store_b128((u32x4){cvtpk2(a[0], a[1]), cvtpk2(a[2], a[3]), cvtpk2(b[0], b[1]), cvtpk2(b[2], b[3])}, srdA, lane * 16u + p * 1024u, ((r0 >> 4) * KS_TOT + 18u + 4u * h) * 1024u, 16);
    }
}
}

#define ATT_ASM \
"s_branch L_ago_%=\n" \
".p2align 12\n" \
"L_ab:\n" \
".fill (L_ab + 0 - .) / 4, 4, 0xBF800000\n" \
"L_ap0:\n" \
"s_setpc_b64 s[88:89]\n" \
"L_ago_%=:\n" \
"s_mov_b32 s101, 0\n" \
"s_waitcnt vmcnt(15) lgkmcnt(0)\n" \
"s_barrier\n" \
"v_accvgpr_write_b32 a216, 0\n" \
"v_accvgpr_write_b32 a217, 0\n" \
"v_accvgpr_write_b32 a218, 0\n" \
"v_accvgpr_write_b32 a219, 0\n" \
"v_bfe_u32 v61, v20, 2, 5\n" \
"s_mov_b32 s96, s24\n" \
"v_mfma_f32_32x32x16_bf16 v[128:143], a[216:219], a[216:219], 0\n" \
"buffer_load_dword v57, v61, s[56:59], s96 offen sc1\n" \
"v_and_b32_e32 v63, 0x300, v20\n" \
"ds_read_b128 v[96:99], v63 offset:0\n" \
"v_add_u32_e32 v58, s22, v20\n" \
"ds_read_b128 a[0:3], v58 offset:0\n" \
"v_mfma_f32_32x32x16_bf16 v[144:159], a[216:219], a[216:219], 0\n" \
"ds_read_b128 a[16:19], v58 offset:8192\n" \
"v_add_u32_e32 v59, s23, v20\n" \
"ds_read_b128 a[32:35], v59 offset:0\n" \
"ds_read_b128 a[48:51], v59 offset:8192\n" \
"ds_read_b128 v[100:103], v63 offset:1024\n" \
"ds_read_b128 a[4:7], v58 offset:1024\n" \
"v_mfma_f32_32x32x16_bf16 v[160:175], a[216:219], a[216:219], 0\n" \
"ds_read_b128 a[20:23], v58 offset:9216\n" \
"ds_read_b128 a[36:39], v59 offset:1024\n" \
"ds_read_b128 a[52:55], v59 offset:9216\n" \
"s_sub_u32 s67, s12, 0xc000\n" \
"ds_read_b128 v[104:107], v63 offset:2048\n" \
"s_mov_b32 s65, s12\n" \
"v_mfma_f32_32x32x16_bf16 v[176:191], a[216:219], a[216:219], 0\n" \
"v_add_u32_e32 v55, s67, v20\n" \
"v_add_u32_e32 v52, 0xc000, v55\n" \
"s_xor_b32 m0, s65, 0x10000\n" \
"ds_read_b128 a[8:11], v58 offset:2048\n" \
"buffer_load_dwordx4 v52, s[8:11], 0 offen offset:0 sc1 lds\n" \
"v_mfma_f32_32x32x16_bf16 v[192:207], a[216:219], a[216:219], 0\n" \
"ds_read_b128 a[24:27], v58 offset:10240\n" \
"ds_read_b128 a[40:43], v59 offset:2048\n" \
"ds_read_b128 a[56:59], v59 offset:10240\n" \
"ds_read_b128 v[108:111], v63 offset:3072\n" \
"buffer_load_dwordx4 v52, s[8:11], 0 offen offset:1024 sc1 lds\n" \
"v_mfma_f32_32x32x16_bf16 v[208:223], a[216:219], a[216:219], 0\n" \
"ds_read_b128 a[12:15], v58 offset:3072\n" \
"ds_read_b128 a[28:31], v58 offset:11264\n" \
"ds_read_b128 a[44:47], v59 offset:3072\n" \
"ds_read_b128 a[60:63], v59 offset:11264\n" \
"s_waitcnt lgkmcnt(15)\n" \
"v_xor_b32_e32 v96, 0x80008000, v96\n" \
"v_mfma_f32_32x32x16_bf16 v[224:239], a[216:219], a[216:219], 0\n" \
"v_xor_b32_e32 v97, 0x80008000, v97\n" \
"v_xor_b32_e32 v98, 0x80008000, v98\n" \
"v_xor_b32_e32 v99, 0x80008000, v99\n" \
"ds_read_b128 a[160:163], v20 offset:0\n" \
"ds_read_b128 a[164:167], v20 offset:1024\n" \
"s_waitcnt lgkmcnt(15)\n" \
"v_xor_b32_e32 v100, 0x80008000, v100\n" \
"v_mfma_f32_32x32x16_bf16 v[240:255], a[216:219], a[216:219], 0\n" \
"v_xor_b32_e32 v101, 0x80008000, v101\n" \
"v_xor_b32_e32 v102, 0x80008000, v102\n" \
"v_xor_b32_e32 v103, 0x80008000, v103\n" \
"ds_read_b128 a[168:171], v20 offset:2048\n" \
"ds_read_b128 a[172:175], v20 offset:3072\n" \
"ds_read_b128 a[188:191], v20 offset:7168\n" \
"v_mfma_f32_16x16x32_bf16 a[192:195], a[216:219], a[216:219], 0\n" \
"ds_read_b128 a[184:187], v20 offset:6144\n" \
"s_waitcnt lgkmcnt(15)\n" \
"v_xor_b32_e32 v104, 0x80008000, v104\n" \
"v_mfma_f32_16x16x32_bf16 a[196:199], a[216:219], a[216:219], 0\n" \
"v_xor_b32_e32 v105, 0x80008000, v105\n" \
"v_xor_b32_e32 v106, 0x80008000, v106\n" \
"v_mfma_f32_16x16x32_bf16 v[32:35], v[96:99], a[0:3], 0\n" \
"v_xor_b32_e32 v107, 0x80008000, v107\n" \
"ds_read_b128 a[180:183], v20 offset:5120\n" \
"v_mfma_f32_16x16x32_bf16 v[36:39], v[96:99], a[16:19], 0\n" \
"ds_read_b128 a[176:179], v20 offset:4096\n" \
"s_waitcnt lgkmcnt(12)\n" \
"v_xor_b32_e32 v108, 0x80008000, v108\n" \
"v_mfma_f32_16x16x32_bf16 v[40:43], v[96:99], a[32:35], 0\n" \
"v_xor_b32_e32 v109, 0x80008000, v109\n" \
"v_xor_b32_e32 v110, 0x80008000, v110\n" \
"v_mfma_f32_16x16x32_bf16 v[44:47], v[96:99], a[48:51], 0\n" \
"v_xor_b32_e32 v111, 0x80008000, v111\n" \
"v_add_u32_e32 v60, s15, v20\n" \
"v_mfma_f32_16x16x32_bf16 v[32:35], v[100:103], a[4:7], v[32:35]\n" \
"v_add_u32_e32 v60, 0x10000, v60\n" \
"ds_read_b128 a[128:131], v20 offset:8192\n" \
"v_mfma_f32_16x16x32_bf16 v[36:39], v[100:103], a[20:23], v[36:39]\n" \
"ds_read_b128 a[144:147], v20 offset:12288\n" \
"ds_read_b128 a[132:135], v20 offset:9216\n" \
"v_mfma_f32_16x16x32_bf16 v[40:43], v[100:103], a[36:39], v[40:43]\n" \
"ds_read_b128 a[148:151], v20 offset:13312\n" \
"ds_read_b128 a[136:139], v20 offset:10240\n" \
"v_mfma_f32_16x16x32_bf16 v[44:47], v[100:103], a[52:55], v[44:47]\n" \
"ds_read_b128 a[152:155], v20 offset:14336\n" \
"ds_read_b128 a[140:143], v20 offset:11264\n" \
"v_mfma_f32_16x16x32_bf16 v[32:35], v[104:107], a[8:11], v[32:35]\n" \
"ds_read_b128 a[156:159], v20 offset:15360\n" \
"ds_read_b128 a[64:67], v60 offset:16384\n" \
"v_mfma_f32_16x16x32_bf16 v[36:39], v[104:107], a[24:27], v[36:39]\n" \
"ds_read_b128 a[68:71], v60 offset:17408\n" \
"ds_read_b128 a[72:75], v60 offset:18432\n" \
"v_mfma_f32_16x16x32_bf16 v[40:43], v[104:107], a[40:43], v[40:43]\n" \
"ds_read_b128 a[76:79], v60 offset:19456\n" \
"ds_read_b128 a[80:83], v60 offset:24576\n" \
"v_mfma_f32_16x16x32_bf16 v[44:47], v[104:107], a[56:59], v[44:47]\n" \
"ds_read_b128 a[84:87], v60 offset:25600\n" \
"s_waitcnt lgkmcnt(15)\n" \
"v_mfma_f32_16x16x32_bf16 v[32:35], v[108:111], a[12:15], v[32:35]\n" \
"ds_read_b128 a[88:91], v60 offset:26624\n" \
"ds_read_b128 a[92:95], v60 offset:27648\n" \
"v_mfma_f32_16x16x32_bf16 v[36:39], v[108:111], a[28:31], v[36:39]\n" \
"ds_read_b128 a[96:99], v60 offset:0\n" \
"ds_read_b128 a[100:103], v60 offset:1024\n" \
"v_mfma_f32_16x16x32_bf16 v[40:43], v[108:111], a[44:47], v[40:43]\n" \
"ds_read_b128 a[104:107], v60 offset:2048\n" \
"ds_read_b128 a[108:111], v60 offset:3072\n" \
"v_mfma_f32_16x16x32_bf16 v[44:47], v[108:111], a[60:63], v[44:47]\n" \
"ds_read_b128 a[112:115], v60 offset:8192\n" \
"ds_read_b128 a[116:119], v60 offset:9216\n" \
"v_mfma_f32_16x16x32_bf16 v[64:67], a[160:163], a[0:3], v[32:35]\n" \
"ds_read_b128 a[120:123], v60 offset:10240\n" \
"ds_read_b128 a[124:127], v60 offset:11264\n" \
"v_mfma_f32_16x16x32_bf16 v[64:67], a[164:167], a[4:7], v[64:67]\n" \
"v_bfe_u32 v26, v20, 8, 2\n" \
"s_mov_b32 m0, s20\n" \
"v_mfma_f32_16x16x32_bf16 v[64:67], a[168:171], a[8:11], v[64:67]\n" \
"v_bfe_u32 v25, v20, 5, 3\n" \
"v_lshlrev_b32_e32 v26, 1, v26\n" \
"v_mfma_f32_16x16x32_bf16 v[64:67], a[172:175], a[12:15], v[64:67]\n" \
"global_load_lds_dword v[16:17], off\n" \
"v_mfma_f32_16x16x32_bf16 v[68:71], a[188:191], a[12:15], v[32:35]\n" \
"v_sub_u32_e32 v25, v25, v26\n" \
"v_and_b32_e32 v26, 16, v20\n" \
"v_mfma_f32_16x16x32_bf16 v[68:71], a[184:187], a[8:11], v[68:71]\n" \
"v_mov_b32_e32 v27, 0x3f80\n" \
"v_lshlrev_b32_e32 v26, v26, v27\n" \
"v_mfma_f32_16x16x32_bf16 v[68:71], a[180:183], a[4:7], v[68:71]\n" \
"v_mov_b32_e32 v54, 0x3f803f80\n" \
"s_waitcnt lgkmcnt(15)\n" \
"v_mfma_f32_16x16x32_bf16 v[68:71], a[176:179], a[0:3], v[68:71]\n" \
"s_mov_b32 s84, 0x33333333\n" \
"s_mov_b32 s85, 0xcccccccc\n" \
"v_mfma_f32_16x16x32_bf16 v[76:79], a[176:179], a[16:19], v[36:39]\n" \
"s_mov_b32 s86, 0x11111111\n" \
"s_mov_b32 s87, 0x11111111\n" \
"v_mfma_f32_16x16x32_bf16 v[76:79], a[180:183], a[20:23], v[76:79]\n" \
"s_mov_b32 s88, 0x44444444\n" \
"s_mov_b32 s89, 0x44444444\n" \
"v_mfma_f32_16x16x32_bf16 v[76:79], a[184:187], a[24:27], v[76:79]\n" \
"v_exp_f32_e32 v70, v70\n" \
"v_mfma_f32_16x16x32_bf16 v[76:79], a[188:191], a[28:31], v[76:79]\n" \
"v_exp_f32_e32 v71, v71\n" \
"v_mfma_f32_16x16x32_bf16 v[72:75], a[172:175], a[28:31], v[36:39]\n" \
"v_exp_f32_e32 v68, v68\n" \
"v_mfma_f32_16x16x32_bf16 v[72:75], a[168:171], a[24:27], v[72:75]\n" \
"v_exp_f32_e32 v69, v69\n" \
"v_mfma_f32_16x16x32_bf16 v[72:75], a[164:167], a[20:23], v[72:75]\n" \
"v_bfe_i32 v21, v20, 4, 1\n" \
"v_cvt_pk_bf16_f32 v71, v70, v71\n" \
"v_mfma_f32_16x16x32_bf16 v[72:75], a[160:163], a[16:19], v[72:75]\n" \
"v_exp_f32_e32 v66, v66\n" \
"v_mfma_f32_16x16x32_bf16 v[80:83], a[160:163], a[32:35], v[40:43]\n" \
"v_exp_f32_e32 v67, v67\n" \
"v_mfma_f32_16x16x32_bf16 v[80:83], a[164:167], a[36:39], v[80:83]\n" \
"v_exp_f32_e32 v64, v64\n" \
"v_mfma_f32_16x16x32_bf16 v[80:83], a[168:171], a[40:43], v[80:83]\n" \
"v_exp_f32_e32 v65, v65\n" \
"v_mfma_f32_16x16x32_bf16 v[80:83], a[172:175], a[44:47], v[80:83]\n" \
"v_exp_f32_e32 v72, v72\n" \
"v_mfma_f32_16x16x32_bf16 v[84:87], a[188:191], a[44:47], v[40:43]\n" \
"s_waitcnt vmcnt(3)\n" \
"v_cmp_ne_u32_e32 vcc, s62, v57\n" \
"v_mfma_f32_16x16x32_bf16 v[84:87], a[184:187], a[40:43], v[84:87]\n" \
"v_exp_f32_e32 v73, v73\n" \
"v_mfma_f32_16x16x32_bf16 v[84:87], a[180:183], a[36:39], v[84:87]\n" \
"v_exp_f32_e32 v74, v74\n" \
"v_mfma_f32_16x16x32_bf16 v[84:87], a[176:179], a[32:35], v[84:87]\n" \
"s_cbranch_vccnz L_skb_%=\n" \
"L_rkb_%=:\n" \
"v_cmp_eq_u32_e32 vcc, 0, v25\n" \
"v_mfma_f32_16x16x32_bf16 v[92:95], a[176:179], a[48:51], v[44:47]\n" \
"v_exp_f32_e32 v75, v75\n" \
"v_mfma_f32_16x16x32_bf16 v[92:95], a[180:183], a[52:55], v[92:95]\n" \
"v_cndmask_b32_e32 v28, 0, v26, vcc\n" \
"v_cvt_pk_bf16_f32 v72, v72, v73\n" \
"v_mfma_f32_16x16x32_bf16 v[92:95], a[184:187], a[56:59], v[92:95]\n" \
"v_exp_f32_e32 v76, v76\n" \
"v_mfma_f32_16x16x32_bf16 v[92:95], a[188:191], a[60:63], v[92:95]\n" \
"v_exp_f32_e32 v77, v77\n" \
"v_mfma_f32_16x16x32_bf16 v[88:91], a[172:175], a[60:63], v[44:47]\n" \
"v_exp_f32_e32 v78, v78\n" \
"v_mfma_f32_16x16x32_bf16 v[88:91], a[168:171], a[56:59], v[88:91]\n" \
"v_exp_f32_e32 v79, v79\n" \
"v_mfma_f32_16x16x32_bf16 v[88:91], a[164:167], a[52:55], v[88:91]\n" \
"v_cndmask_b32_e64 v22, 0, v54, s[84:85]\n" \
"v_cndmask_b32_e64 v23, 0, v54, s[86:87]\n" \
"v_mfma_f32_16x16x32_bf16 v[88:91], a[160:163], a[48:51], v[88:91]\n" \
"v_cndmask_b32_e64 v24, 0, v54, s[88:89]\n" \
"v_and_b32_e32 v21, 0xaaaaaaaa, v21\n" \
"s_waitcnt vmcnt(4) lgkmcnt(0)\n" \
"s_barrier\n" \
"v_cmp_eq_u32_e32 vcc, 1, v25\n" \
"v_cvt_pk_bf16_f32 v70, v68, v69\n" \
"v_cvt_pk_bf16_f32 v73, v74, v75\n" \
"v_mov_b32_e32 v62, 0\n" \
"v_accvgpr_write_b32 a208, 0\n" \
"v_accvgpr_write_b32 a209, 0\n" \
"v_accvgpr_write_b32 a210, 0\n" \
"v_accvgpr_write_b32 a211, 0\n" \
"v_accvgpr_write_b32 a200, v22\n" \
"v_accvgpr_write_b32 a201, v22\n" \
"v_accvgpr_write_b32 a202, v22\n" \
"v_accvgpr_write_b32 a203, v22\n" \
"v_accvgpr_write_b32 a204, v23\n" \
"v_accvgpr_write_b32 a205, v23\n" \
"v_accvgpr_write_b32 a206, v23\n" \
"v_accvgpr_write_b32 a207, v23\n" \
"v_accvgpr_write_b32 a212, v24\n" \
"v_accvgpr_write_b32 a213, v24\n" \
"v_accvgpr_write_b32 a214, v24\n" \
"v_accvgpr_write_b32 a215, v24\n" \
"v_or_b32_e32 v21, 0x44444444, v21\n" \
"s_mov_b32 s64, 0x8000\n" \
"v_add_u32_e32 v50, 0x6000, v55\n" \
"v_add_u32_e32 v51, 0xa000, v55\n" \
"v_add_u32_e32 v53, 0x10000, v55\n" \
"v_add_u32_e32 v48, 0xc000, v20\n" \
"v_add_u32_e32 v49, s17, v20\n" \
"v_cndmask_b32_e32 v29, 0, v26, vcc\n" \
"v_accvgpr_write_b32 a220, v96\n" \
"v_accvgpr_write_b32 a221, v97\n" \
"v_accvgpr_write_b32 a222, v98\n" \
"v_accvgpr_write_b32 a223, v99\n" \
"v_accvgpr_write_b32 a224, v100\n" \
"v_accvgpr_write_b32 a225, v101\n" \
"v_accvgpr_write_b32 a226, v102\n" \
"v_accvgpr_write_b32 a227, v103\n" \
"v_accvgpr_write_b32 a228, v104\n" \
"v_accvgpr_write_b32 a229, v105\n" \
"v_accvgpr_write_b32 a230, v106\n" \
"v_accvgpr_write_b32 a231, v107\n" \
"v_accvgpr_write_b32 a232, v108\n" \
"v_accvgpr_write_b32 a233, v109\n" \
"v_accvgpr_write_b32 a234, v110\n" \
"v_accvgpr_write_b32 a235, v111\n" \
"v_cvt_pk_bf16_f32 v69, v66, v67\n" \
"v_cvt_pk_bf16_f32 v68, v64, v65\n" \
"v_cvt_pk_bf16_f32 v74, v76, v77\n" \
"v_cvt_pk_bf16_f32 v75, v78, v79\n" \
"s_sub_u32 s66, s18, 1\n" \
"s_cbranch_scc1 L_tr_%=\n" \
".fill (L_ab + 1856 - .) / 4, 4, 0xBF800000\n" \
"L_1_%=:\n" \
"v_mfma_f32_16x16x32_bf16 v[96:99], a[128:131], a[0:3], v[32:35]\n" \
"s_xor_b32 m0, s65, 0x12000\n" \
"ds_read_b128 a[160:163], v49 offset:0\n" \
"v_mfma_f32_16x16x32_bf16 v[96:99], a[132:135], a[4:7], v[96:99]\n" \
"buffer_load_dwordx4 v50, s[8:11], s64 offen offset:0 sc1 lds\n" \
"v_mfma_f32_16x16x32_bf16 v[96:99], a[136:139], a[8:11], v[96:99]\n" \
"ds_read_b128 a[164:167], v49 offset:1024\n" \
"ds_read_b128 a[168:171], v49 offset:2048\n" \
"v_mfma_f32_16x16x32_bf16 v[96:99], a[140:143], a[12:15], v[96:99]\n" \
"ds_read_b128 a[172:175], v49 offset:3072\n" \
"ds_read_b128 a[176:179], v49 offset:4096\n" \
"v_mfma_f32_16x16x32_bf16 v[100:103], a[156:159], a[12:15], v[32:35]\n" \
"ds_read_b128 a[180:183], v49 offset:5120\n" \
"ds_read_b128 a[184:187], v49 offset:6144\n" \
"v_mfma_f32_16x16x32_bf16 v[100:103], a[152:155], a[8:11], v[100:103]\n" \
"v_exp_f32_e64 v86, v86\n" \
"v_mfma_f32_16x16x32_bf16 v[100:103], a[148:151], a[4:7], v[100:103]\n" \
"v_exp_f32_e64 v87, v87\n" \
"v_mfma_f32_16x16x32_bf16 v[100:103], a[144:147], a[0:3], v[100:103]\n" \
"ds_read_b128 a[188:191], v49 offset:7168\n" \
"v_cvt_pk_bf16_f32 v87, v86, v87\n" \
"v_mfma_f32_16x16x32_bf16 v[108:111], a[144:147], a[16:19], v[36:39]\n" \
"v_exp_f32_e64 v84, v84\n" \
"v_mfma_f32_16x16x32_bf16 v[108:111], a[148:151], a[20:23], v[108:111]\n" \
"v_exp_f32_e64 v85, v85\n" \
"v_mfma_f32_16x16x32_bf16 v[108:111], a[152:155], a[24:27], v[108:111]\n" \
"v_exp_f32_e64 v82, v82\n" \
"v_mfma_f32_16x16x32_bf16 v[108:111], a[156:159], a[28:31], v[108:111]\n" \
"v_exp_f32_e64 v83, v83\n" \
"v_mfma_f32_16x16x32_bf16 v[104:107], a[140:143], a[28:31], v[36:39]\n" \
"v_exp_f32_e64 v80, v80\n" \
"v_mfma_f32_16x16x32_bf16 v[104:107], a[136:139], a[24:27], v[104:107]\n" \
"v_exp_f32_e64 v81, v81\n" \
"v_mfma_f32_16x16x32_bf16 v[104:107], a[132:135], a[20:23], v[104:107]\n" \
"v_exp_f32_e64 v88, v88\n" \
"v_mfma_f32_16x16x32_bf16 v[104:107], a[128:131], a[16:19], v[104:107]\n" \
"buffer_load_dwordx4 v50, s[8:11], s64 offen offset:1024 sc1 lds\n" \
"v_mfma_f32_16x16x32_bf16 v[112:115], a[128:131], a[32:35], v[40:43]\n" \
"s_xor_b32 m0, s65, 0x6000\n" \
"v_cvt_pk_bf16_f32 v86, v84, v85\n" \
"v_mfma_f32_16x16x32_bf16 v[112:115], a[132:135], a[36:39], v[112:115]\n" \
"v_cvt_pk_bf16_f32 v85, v82, v83\n" \
"v_cvt_pk_bf16_f32 v84, v80, v81\n" \
"v_mfma_f32_16x16x32_bf16 v[112:115], a[136:139], a[40:43], v[112:115]\n" \
"v_exp_f32_e64 v89, v89\n" \
"v_mfma_f32_16x16x32_bf16 v[112:115], a[140:143], a[44:47], v[112:115]\n" \
"v_exp_f32_e64 v90, v90\n" \
"v_mfma_f32_16x16x32_bf16 v[116:119], a[156:159], a[44:47], v[40:43]\n" \
"v_exp_f32_e64 v91, v91\n" \
"v_mfma_f32_16x16x32_bf16 v[116:119], a[152:155], a[40:43], v[116:119]\n" \
"v_exp_f32_e64 v92, v92\n" \
"v_mfma_f32_16x16x32_bf16 v[116:119], a[148:151], a[36:39], v[116:119]\n" \
"v_exp_f32_e64 v93, v93\n" \
"v_mfma_f32_16x16x32_bf16 v[116:119], a[144:147], a[32:35], v[116:119]\n" \
"v_exp_f32_e64 v94, v94\n" \
"v_mfma_f32_16x16x32_bf16 v[124:127], a[144:147], a[48:51], v[44:47]\n" \
"v_cvt_pk_bf16_f32 v88, v88, v89\n" \
"v_cvt_pk_bf16_f32 v89, v90, v91\n" \
"v_mfma_f32_16x16x32_bf16 v[124:127], a[148:151], a[52:55], v[124:127]\n" \
"v_exp_f32_e64 v95, v95\n" \
"v_mfma_f32_16x16x32_bf16 v[124:127], a[152:155], a[56:59], v[124:127]\n" \
"v_cvt_pk_bf16_f32 v90, v92, v93\n" \
"v_cvt_pk_bf16_f32 v91, v94, v95\n" \
"v_mfma_f32_16x16x32_bf16 v[124:127], a[156:159], a[60:63], v[124:127]\n" \
"ds_read_b128 a[144:147], v48 offset:4096\n" \
"ds_read_b128 a[148:151], v48 offset:5120\n" \
"v_mfma_f32_16x16x32_bf16 v[120:123], a[140:143], a[60:63], v[44:47]\n" \
"ds_read_b128 a[152:155], v48 offset:6144\n" \
"ds_read_b128 a[140:143], v48 offset:3072\n" \
"v_mfma_f32_16x16x32_bf16 v[120:123], a[136:139], a[56:59], v[120:123]\n" \
"ds_read_b128 a[136:139], v48 offset:2048\n" \
"ds_read_b128 a[156:159], v48 offset:7168\n" \
"v_mfma_f32_16x16x32_bf16 v[120:123], a[132:135], a[52:55], v[120:123]\n" \
"buffer_load_dwordx4 v51, s[8:11], s64 offen offset:0 sc1 lds\n" \
"v_mfma_f32_16x16x32_bf16 v[120:123], a[128:131], a[48:51], v[120:123]\n" \
"ds_read_b128 a[128:131], v48 offset:0\n" \
"s_waitcnt lgkmcnt(7)\n" \
"v_mfma_f32_16x16x32_bf16 v[128:131], a[160:163], v[68:71], v[128:131]\n" \
"v_exp_f32_e32 v102, v102\n" \
"v_mfma_f32_16x16x32_bf16 v[160:163], a[160:163], v[72:75], v[160:163]\n" \
"v_exp_f32_e64 v103, v103\n" \
"v_mfma_f32_16x16x32_bf16 v[132:135], a[164:167], v[68:71], v[132:135]\n" \
"ds_read_b128 a[132:135], v48 offset:1024\n" \
"v_cvt_pk_bf16_f32 v103, v102, v103\n" \
"v_mfma_f32_16x16x32_bf16 v[164:167], a[164:167], v[72:75], v[164:167]\n" \
"v_exp_f32_e64 v100, v100\n" \
"v_mfma_f32_16x16x32_bf16 v[136:139], a[168:171], v[68:71], v[136:139]\n" \
"v_exp_f32_e64 v101, v101\n" \
"v_mfma_f32_16x16x32_bf16 v[168:171], a[168:171], v[72:75], v[168:171]\n" \
"v_exp_f32_e64 v98, v98\n" \
"v_mfma_f32_16x16x32_bf16 v[140:143], a[172:175], v[68:71], v[140:143]\n" \
"v_exp_f32_e64 v99, v99\n" \
"v_mfma_f32_16x16x32_bf16 v[172:175], a[172:175], v[72:75], v[172:175]\n" \
"v_exp_f32_e64 v96, v96\n" \
"v_mfma_f32_16x16x32_bf16 v[144:147], a[176:179], v[68:71], v[144:147]\n" \
"v_exp_f32_e64 v97, v97\n" \
"v_mfma_f32_16x16x32_bf16 v[176:179], a[176:179], v[72:75], v[176:179]\n" \
"v_exp_f32_e64 v104, v104\n" \
"v_mfma_f32_16x16x32_bf16 v[148:151], a[180:183], v[68:71], v[148:151]\n" \
"v_exp_f32_e64 v105, v105\n" \
"v_mfma_f32_16x16x32_bf16 v[180:183], a[180:183], v[72:75], v[180:183]\n" \
"v_cvt_pk_bf16_f32 v102, v100, v101\n" \
"v_cvt_pk_bf16_f32 v101, v98, v99\n" \
"v_mfma_f32_16x16x32_bf16 v[152:155], a[184:187], v[68:71], v[152:155]\n" \
"v_cvt_pk_bf16_f32 v100, v96, v97\n" \
"v_cvt_pk_bf16_f32 v104, v104, v105\n" \
"v_mfma_f32_16x16x32_bf16 v[184:187], a[184:187], v[72:75], v[184:187]\n" \
"buffer_load_dwordx4 v51, s[8:11], s64 offen offset:1024 sc1 lds\n" \
"v_mfma_f32_16x16x32_bf16 v[156:159], a[188:191], v[68:71], v[156:159]\n" \
"v_exp_f32_e64 v106, v106\n" \
"v_mfma_f32_16x16x32_bf16 v[188:191], a[188:191], v[72:75], v[188:191]\n" \
"v_exp_f32_e64 v107, v107\n" \
"v_smfmac_f32_16x16x64_bf16 a[192:195], a[200:203], v[68:75], v21\n" \
"v_exp_f32_e64 v108, v108\n" \
"v_mfma_f32_16x16x32_bf16 v[220:223], a[188:191], v[84:87], v[220:223]\n" \
"v_exp_f32_e64 v109, v109\n" \
"v_mfma_f32_16x16x32_bf16 v[252:255], a[188:191], v[88:91], v[252:255]\n" \
"v_exp_f32_e64 v110, v110\n" \
"v_mfma_f32_16x16x32_bf16 v[216:219], a[184:187], v[84:87], v[216:219]\n" \
"v_exp_f32_e64 v111, v111\n" \
"v_mfma_f32_16x16x32_bf16 v[248:251], a[184:187], v[88:91], v[248:251]\n" \
"v_exp_f32_e64 v118, v118\n" \
"v_mfma_f32_16x16x32_bf16 v[212:215], a[180:183], v[84:87], v[212:215]\n" \
"v_exp_f32_e64 v119, v119\n" \
"v_mfma_f32_16x16x32_bf16 v[244:247], a[180:183], v[88:91], v[244:247]\n" \
"v_cvt_pk_bf16_f32 v105, v106, v107\n" \
"v_cvt_pk_bf16_f32 v106, v108, v109\n" \
"v_mfma_f32_16x16x32_bf16 v[208:211], a[176:179], v[84:87], v[208:211]\n" \
"v_cvt_pk_bf16_f32 v107, v110, v111\n" \
"v_cvt_pk_bf16_f32 v119, v118, v119\n" \
"v_mfma_f32_16x16x32_bf16 v[240:243], a[176:179], v[88:91], v[240:243]\n" \
"v_exp_f32_e64 v116, v116\n" \
"v_mfma_f32_16x16x32_bf16 v[204:207], a[172:175], v[84:87], v[204:207]\n" \
"v_exp_f32_e64 v117, v117\n" \
"v_mfma_f32_16x16x32_bf16 v[236:239], a[172:175], v[88:91], v[236:239]\n" \
"v_exp_f32_e64 v114, v114\n" \
"v_mfma_f32_16x16x32_bf16 v[200:203], a[168:171], v[84:87], v[200:203]\n" \
"v_exp_f32_e64 v115, v115\n" \
"v_mfma_f32_16x16x32_bf16 v[232:235], a[168:171], v[88:91], v[232:235]\n" \
"v_exp_f32_e64 v112, v112\n" \
"v_mfma_f32_16x16x32_bf16 v[196:199], a[164:167], v[84:87], v[196:199]\n" \
"v_exp_f32_e64 v113, v113\n" \
"v_mfma_f32_16x16x32_bf16 v[228:231], a[164:167], v[88:91], v[228:231]\n" \
"v_exp_f32_e64 v120, v120\n" \
"v_mfma_f32_16x16x32_bf16 v[192:195], a[160:163], v[84:87], v[192:195]\n" \
"v_exp_f32_e64 v121, v121\n" \
"v_mfma_f32_16x16x32_bf16 v[224:227], a[160:163], v[88:91], v[224:227]\n" \
"v_cvt_pk_bf16_f32 v118, v116, v117\n" \
"v_cvt_pk_bf16_f32 v117, v114, v115\n" \
"v_smfmac_f32_16x16x64_bf16 a[196:199], a[200:203], v[84:91], v21\n" \
"v_cvt_pk_bf16_f32 v116, v112, v113\n" \
"v_cvt_pk_bf16_f32 v120, v120, v121\n" \
"s_waitcnt vmcnt(4) lgkmcnt(0)\n" \
"s_barrier\n" \
"v_mfma_f32_16x16x32_bf16 v[64:67], a[128:131], a[0:3], v[32:35]\n" \
"s_xor_b32 m0, s65, 0x14000\n" \
"ds_read_b128 a[160:163], v49 offset:8192\n" \
"v_mfma_f32_16x16x32_bf16 v[64:67], a[132:135], a[4:7], v[64:67]\n" \
"buffer_load_dwordx4 v52, s[8:11], s64 offen offset:0 sc1 lds\n" \
"v_mfma_f32_16x16x32_bf16 v[64:67], a[136:139], a[8:11], v[64:67]\n" \
"ds_read_b128 a[164:167], v49 offset:9216\n" \
"ds_read_b128 a[168:171], v49 offset:10240\n" \
"v_mfma_f32_16x16x32_bf16 v[64:67], a[140:143], a[12:15], v[64:67]\n" \
"ds_read_b128 a[172:175], v49 offset:11264\n" \
"ds_read_b128 a[176:179], v49 offset:12288\n" \
"v_mfma_f32_16x16x32_bf16 v[68:71], a[156:159], a[12:15], v[32:35]\n" \
"ds_read_b128 a[180:183], v49 offset:13312\n" \
"ds_read_b128 a[184:187], v49 offset:14336\n" \
"v_mfma_f32_16x16x32_bf16 v[68:71], a[152:155], a[8:11], v[68:71]\n" \
"v_exp_f32_e64 v122, v122\n" \
"v_mfma_f32_16x16x32_bf16 v[68:71], a[148:151], a[4:7], v[68:71]\n" \
"v_exp_f32_e64 v123, v123\n" \
"v_mfma_f32_16x16x32_bf16 v[68:71], a[144:147], a[0:3], v[68:71]\n" \
"ds_read_b128 a[188:191], v49 offset:15360\n" \
"v_cvt_pk_bf16_f32 v121, v122, v123\n" \
"v_mfma_f32_16x16x32_bf16 v[76:79], a[144:147], a[16:19], v[36:39]\n" \
"v_exp_f32_e64 v124, v124\n" \
"v_mfma_f32_16x16x32_bf16 v[76:79], a[148:151], a[20:23], v[76:79]\n" \
"v_exp_f32_e64 v125, v125\n" \
"v_mfma_f32_16x16x32_bf16 v[76:79], a[152:155], a[24:27], v[76:79]\n" \
"v_exp_f32_e64 v126, v126\n" \
"v_mfma_f32_16x16x32_bf16 v[76:79], a[156:159], a[28:31], v[76:79]\n" \
"v_exp_f32_e64 v127, v127\n" \
"v_mfma_f32_16x16x32_bf16 v[72:75], a[140:143], a[28:31], v[36:39]\n" \
"v_cvt_pk_bf16_f32 v122, v124, v125\n" \
"v_cvt_pk_bf16_f32 v123, v126, v127\n" \
"v_mfma_f32_16x16x32_bf16 v[72:75], a[136:139], a[24:27], v[72:75]\n" \
"v_exp_f32_e64 v70, v70\n" \
"v_mfma_f32_16x16x32_bf16 v[72:75], a[132:135], a[20:23], v[72:75]\n" \
"v_exp_f32_e64 v71, v71\n" \
"v_mfma_f32_16x16x32_bf16 v[72:75], a[128:131], a[16:19], v[72:75]\n" \
"buffer_load_dwordx4 v52, s[8:11], s64 offen offset:1024 sc1 lds\n" \
"v_mfma_f32_16x16x32_bf16 v[80:83], a[128:131], a[32:35], v[40:43]\n" \
"s_mov_b32 m0, s65\n" \
"v_cvt_pk_bf16_f32 v71, v70, v71\n" \
"v_mfma_f32_16x16x32_bf16 v[80:83], a[132:135], a[36:39], v[80:83]\n" \
"v_exp_f32_e32 v68, v68\n" \
"v_mfma_f32_16x16x32_bf16 v[80:83], a[136:139], a[40:43], v[80:83]\n" \
"v_exp_f32_e64 v69, v69\n" \
"v_mfma_f32_16x16x32_bf16 v[80:83], a[140:143], a[44:47], v[80:83]\n" \
"v_exp_f32_e64 v72, v72\n" \
"v_mfma_f32_16x16x32_bf16 v[84:87], a[156:159], a[44:47], v[40:43]\n" \
"v_exp_f32_e64 v73, v73\n" \
"v_mfma_f32_16x16x32_bf16 v[84:87], a[152:155], a[40:43], v[84:87]\n" \
"v_exp_f32_e64 v74, v74\n" \
"v_mfma_f32_16x16x32_bf16 v[84:87], a[148:151], a[36:39], v[84:87]\n" \
"v_exp_f32_e64 v75, v75\n" \
"v_mfma_f32_16x16x32_bf16 v[84:87], a[144:147], a[32:35], v[84:87]\n" \
"v_exp_f32_e64 v66, v66\n" \
"v_mfma_f32_16x16x32_bf16 v[92:95], a[144:147], a[48:51], v[44:47]\n" \
"ds_read_b128 a[144:147], v48 offset:12288\n" \
"v_cvt_pk_bf16_f32 v72, v72, v73\n" \
"v_mfma_f32_16x16x32_bf16 v[92:95], a[148:151], a[52:55], v[92:95]\n" \
"ds_read_b128 a[148:151], v48 offset:13312\n" \
"v_cvt_pk_bf16_f32 v70, v68, v69\n" \
"v_mfma_f32_16x16x32_bf16 v[92:95], a[152:155], a[56:59], v[92:95]\n" \
"ds_read_b128 a[152:155], v48 offset:14336\n" \
"v_cvt_pk_bf16_f32 v73, v74, v75\n" \
"v_mfma_f32_16x16x32_bf16 v[92:95], a[156:159], a[60:63], v[92:95]\n" \
"ds_read_b128 a[156:159], v48 offset:15360\n" \
"v_add_u32_e32 v49, 0x10000, v48\n" \
"v_mfma_f32_16x16x32_bf16 v[88:91], a[140:143], a[60:63], v[44:47]\n" \
"ds_read_b128 a[140:143], v48 offset:11264\n" \
"s_xor_b32 s65, s65, 0x4000\n" \
"v_mfma_f32_16x16x32_bf16 v[88:91], a[136:139], a[56:59], v[88:91]\n" \
"v_exp_f32_e64 v67, v67\n" \
"v_mfma_f32_16x16x32_bf16 v[88:91], a[132:135], a[52:55], v[88:91]\n" \
"buffer_load_dwordx4 v53, s[8:11], s64 offen offset:0 sc1 lds\n" \
"v_mfma_f32_16x16x32_bf16 v[88:91], a[128:131], a[48:51], v[88:91]\n" \
"ds_read_b128 a[128:131], v48 offset:8192\n" \
"s_waitcnt lgkmcnt(6)\n" \
"v_mfma_f32_16x16x32_bf16 v[128:131], a[160:163], v[100:103], v[128:131]\n" \
"ds_read_b128 a[132:135], v48 offset:9216\n" \
"ds_read_b128 a[136:139], v48 offset:10240\n" \
"v_mfma_f32_16x16x32_bf16 v[160:163], a[160:163], v[104:107], v[160:163]\n" \
"v_exp_f32_e32 v64, v64\n" \
"v_mfma_f32_16x16x32_bf16 v[132:135], a[164:167], v[100:103], v[132:135]\n" \
"v_exp_f32_e64 v65, v65\n" \
"v_mfma_f32_16x16x32_bf16 v[164:167], a[164:167], v[104:107], v[164:167]\n" \
"v_exp_f32_e64 v76, v76\n" \
"v_mfma_f32_16x16x32_bf16 v[136:139], a[168:171], v[100:103], v[136:139]\n" \
"v_exp_f32_e64 v77, v77\n" \
"v_mfma_f32_16x16x32_bf16 v[168:171], a[168:171], v[104:107], v[168:171]\n" \
"v_exp_f32_e64 v78, v78\n" \
"v_mfma_f32_16x16x32_bf16 v[140:143], a[172:175], v[100:103], v[140:143]\n" \
"v_exp_f32_e64 v79, v79\n" \
"v_mfma_f32_16x16x32_bf16 v[172:175], a[172:175], v[104:107], v[172:175]\n" \
"v_cvt_pk_bf16_f32 v69, v66, v67\n" \
"v_cvt_pk_bf16_f32 v68, v64, v65\n" \
"v_mfma_f32_16x16x32_bf16 v[144:147], a[176:179], v[100:103], v[144:147]\n" \
"v_cvt_pk_bf16_f32 v74, v76, v77\n" \
"v_cvt_pk_bf16_f32 v75, v78, v79\n" \
"v_mfma_f32_16x16x32_bf16 v[176:179], a[176:179], v[104:107], v[176:179]\n" \
"v_xor_b32_e32 v48, 0x4000, v48\n" \
"v_mfma_f32_16x16x32_bf16 v[148:151], a[180:183], v[100:103], v[148:151]\n" \
"v_mfma_f32_16x16x32_bf16 v[180:183], a[180:183], v[104:107], v[180:183]\n" \
"v_mfma_f32_16x16x32_bf16 v[152:155], a[184:187], v[100:103], v[152:155]\n" \
"v_mfma_f32_16x16x32_bf16 v[184:187], a[184:187], v[104:107], v[184:187]\n" \
"buffer_load_dwordx4 v53, s[8:11], s64 offen offset:1024 sc1 lds\n" \
"v_mfma_f32_16x16x32_bf16 v[156:159], a[188:191], v[100:103], v[156:159]\n" \
"s_add_u32 s64, s64, 0x8000\n" \
"s_sub_u32 s66, s66, 1\n" \
"s_nop 0\n" \
"v_mfma_f32_16x16x32_bf16 v[188:191], a[188:191], v[104:107], v[188:191]\n" \
"v_smfmac_f32_16x16x64_bf16 a[192:195], a[200:203], v[100:107], v21\n" \
"v_mfma_f32_16x16x32_bf16 v[220:223], a[188:191], v[116:119], v[220:223]\n" \
"v_mfma_f32_16x16x32_bf16 v[252:255], a[188:191], v[120:123], v[252:255]\n" \
"v_mfma_f32_16x16x32_bf16 v[216:219], a[184:187], v[116:119], v[216:219]\n" \
"v_mfma_f32_16x16x32_bf16 v[248:251], a[184:187], v[120:123], v[248:251]\n" \
"v_mfma_f32_16x16x32_bf16 v[212:215], a[180:183], v[116:119], v[212:215]\n" \
"v_mfma_f32_16x16x32_bf16 v[244:247], a[180:183], v[120:123], v[244:247]\n" \
"v_mfma_f32_16x16x32_bf16 v[208:211], a[176:179], v[116:119], v[208:211]\n" \
"v_mfma_f32_16x16x32_bf16 v[240:243], a[176:179], v[120:123], v[240:243]\n" \
"v_mfma_f32_16x16x32_bf16 v[204:207], a[172:175], v[116:119], v[204:207]\n" \
"v_mfma_f32_16x16x32_bf16 v[236:239], a[172:175], v[120:123], v[236:239]\n" \
"v_mfma_f32_16x16x32_bf16 v[200:203], a[168:171], v[116:119], v[200:203]\n" \
"v_mfma_f32_16x16x32_bf16 v[232:235], a[168:171], v[120:123], v[232:235]\n" \
"v_mfma_f32_16x16x32_bf16 v[196:199], a[164:167], v[116:119], v[196:199]\n" \
"v_mfma_f32_16x16x32_bf16 v[228:231], a[164:167], v[120:123], v[228:231]\n" \
"v_mfma_f32_16x16x32_bf16 v[192:195], a[160:163], v[116:119], v[192:195]\n" \
"v_mfma_f32_16x16x32_bf16 v[224:227], a[160:163], v[120:123], v[224:227]\n" \
"v_smfmac_f32_16x16x64_bf16 a[196:199], a[200:203], v[116:123], v21\n" \
"s_waitcnt vmcnt(4) lgkmcnt(0)\n" \
"s_barrier\n" \
"s_cbranch_scc0 L_1_%=\n" \
"L_tr_%=:\n" \
"s_setpc_b64 s[28:29]\n" \
".fill (L_ab + 4096 - .) / 4, 4, 0xBF800000\n" \
".fill (L_ab + 4096 - .) / 4, 4, 0xBF800000\n" \
"L_ap1:\n" \
"s_setpc_b64 s[88:89]\n" \
"L_gtr:\n" \
"v_mfma_f32_16x16x32_bf16 v[96:99], a[128:131], a[0:3], v[32:35]\n" \
"v_mov_b32_e32 v56, s62\n" \
"v_readlane_b32 s68, v11, 0\n" \
"v_mfma_f32_16x16x32_bf16 v[96:99], a[132:135], a[4:7], v[96:99]\n" \
"s_xor_b32 m0, s65, 0x12000\n" \
"s_mov_b32 s65, s13\n" \
"v_mfma_f32_16x16x32_bf16 v[96:99], a[136:139], a[8:11], v[96:99]\n" \
"buffer_load_dwordx4 v50, s[8:11], s64 offen offset:0 sc1 lds\n" \
"v_mfma_f32_16x16x32_bf16 v[96:99], a[140:143], a[12:15], v[96:99]\n" \
"v_add_u32_e32 v63, s15, v20\n" \
"s_add_u32 s67, s14, 40960\n" \
"v_mfma_f32_16x16x32_bf16 v[100:103], a[156:159], a[12:15], v[32:35]\n" \
"buffer_load_dwordx4 v50, s[8:11], s64 offen offset:1024 sc1 lds\n" \
"s_mov_b64 exec, 1\n" \
"buffer_store_dword v56, off, s[56:59], s68 offset:0 sc1\n" \
"s_mov_b64 exec, -1\n" \
"v_mfma_f32_16x16x32_bf16 v[100:103], a[152:155], a[8:11], v[100:103]\n" \
"v_add_u32_e32 v50, 0x6000, v63\n" \
"s_xor_b32 m0, s65, 0x8000\n" \
"v_mfma_f32_16x16x32_bf16 v[100:103], a[148:151], a[4:7], v[100:103]\n" \
"ds_read_b128 a[160:163], v49 offset:0\n" \
"ds_read_b128 a[164:167], v49 offset:1024\n" \
"v_mfma_f32_16x16x32_bf16 v[100:103], a[144:147], a[0:3], v[100:103]\n" \
"ds_read_b128 a[168:171], v49 offset:2048\n" \
"ds_read_b128 a[172:175], v49 offset:3072\n" \
"v_mfma_f32_16x16x32_bf16 v[108:111], a[144:147], a[16:19], v[36:39]\n" \
"buffer_load_dwordx4 v50, s[8:11], s67 offen offset:0 sc1 lds\n" \
"v_mfma_f32_16x16x32_bf16 v[108:111], a[148:151], a[20:23], v[108:111]\n" \
"ds_read_b128 a[176:179], v49 offset:4096\n" \
"ds_read_b128 a[180:183], v49 offset:5120\n" \
"v_mfma_f32_16x16x32_bf16 v[108:111], a[152:155], a[24:27], v[108:111]\n" \
"ds_read_b128 a[184:187], v49 offset:6144\n" \
"ds_read_b128 a[188:191], v49 offset:7168\n" \
"v_mfma_f32_16x16x32_bf16 v[108:111], a[156:159], a[28:31], v[108:111]\n" \
"s_mov_b32 s64, s14\n" \
"v_add_u32_e32 v51, 0x12000, v63\n" \
"v_mfma_f32_16x16x32_bf16 v[104:107], a[140:143], a[28:31], v[36:39]\n" \
"buffer_load_dwordx4 v50, s[8:11], s67 offen offset:1024 sc1 lds\n" \
"v_mfma_f32_16x16x32_bf16 v[104:107], a[136:139], a[24:27], v[104:107]\n" \
"v_exp_f32_e32 v86, v86\n" \
"v_mfma_f32_16x16x32_bf16 v[104:107], a[132:135], a[20:23], v[104:107]\n" \
"v_exp_f32_e32 v87, v87\n" \
"v_mfma_f32_16x16x32_bf16 v[104:107], a[128:131], a[16:19], v[104:107]\n" \
"v_exp_f32_e32 v84, v84\n" \
"v_mfma_f32_16x16x32_bf16 v[112:115], a[128:131], a[32:35], v[40:43]\n" \
"v_exp_f32_e32 v85, v85\n" \
"v_mfma_f32_16x16x32_bf16 v[112:115], a[132:135], a[36:39], v[112:115]\n" \
"buffer_load_dwordx4 v50, s[8:11], s67 offen offset:2048 sc1 lds\n" \
"v_mfma_f32_16x16x32_bf16 v[112:115], a[136:139], a[40:43], v[112:115]\n" \
"v_cvt_pk_bf16_f32 v87, v86, v87\n" \
"v_cvt_pk_bf16_f32 v86, v84, v85\n" \
"v_mfma_f32_16x16x32_bf16 v[112:115], a[140:143], a[44:47], v[112:115]\n" \
"v_exp_f32_e32 v82, v82\n" \
"v_mfma_f32_16x16x32_bf16 v[116:119], a[156:159], a[44:47], v[40:43]\n" \
"v_exp_f32_e32 v83, v83\n" \
"v_mfma_f32_16x16x32_bf16 v[116:119], a[152:155], a[40:43], v[116:119]\n" \
"buffer_load_dwordx4 v50, s[8:11], s67 offen offset:3072 sc1 lds\n" \
"v_mfma_f32_16x16x32_bf16 v[116:119], a[148:151], a[36:39], v[116:119]\n" \
"s_xor_b32 m0, s65, 0xa000\n" \
"v_cvt_pk_bf16_f32 v85, v82, v83\n" \
"v_mfma_f32_16x16x32_bf16 v[116:119], a[144:147], a[32:35], v[116:119]\n" \
"v_exp_f32_e32 v80, v80\n" \
"v_mfma_f32_16x16x32_bf16 v[124:127], a[144:147], a[48:51], v[44:47]\n" \
"v_exp_f32_e32 v81, v81\n" \
"v_mfma_f32_16x16x32_bf16 v[124:127], a[148:151], a[52:55], v[124:127]\n" \
"buffer_load_dwordx4 v51, s[8:11], s64 offen offset:0 sc1 lds\n" \
"v_mfma_f32_16x16x32_bf16 v[124:127], a[152:155], a[56:59], v[124:127]\n" \
"v_cvt_pk_bf16_f32 v84, v80, v81\n" \
"v_readlane_b32 s93, v11, 19\n" \
"v_mfma_f32_16x16x32_bf16 v[124:127], a[156:159], a[60:63], v[124:127]\n" \
"v_exp_f32_e32 v88, v88\n" \
"v_mfma_f32_16x16x32_bf16 v[120:123], a[140:143], a[60:63], v[44:47]\n" \
"v_exp_f32_e32 v89, v89\n" \
"v_mfma_f32_16x16x32_bf16 v[120:123], a[136:139], a[56:59], v[120:123]\n" \
"v_exp_f32_e32 v90, v90\n" \
"v_mfma_f32_16x16x32_bf16 v[120:123], a[132:135], a[52:55], v[120:123]\n" \
"buffer_load_dwordx4 v51, s[8:11], s64 offen offset:1024 sc1 lds\n" \
"v_mfma_f32_16x16x32_bf16 v[120:123], a[128:131], a[48:51], v[120:123]\n" \
"v_cvt_pk_bf16_f32 v88, v88, v89\n" \
"s_waitcnt lgkmcnt(0)\n" \
"v_mfma_f32_16x16x32_bf16 v[128:131], a[160:163], v[68:71], v[128:131]\n" \
"v_exp_f32_e32 v91, v91\n" \
"v_mfma_f32_16x16x32_bf16 v[160:163], a[160:163], v[72:75], v[160:163]\n" \
"v_exp_f32_e32 v92, v92\n" \
"v_mfma_f32_16x16x32_bf16 v[132:135], a[164:167], v[68:71], v[132:135]\n" \
"buffer_load_dwordx4 v51, s[8:11], s64 offen offset:2048 sc1 lds\n" \
"v_mfma_f32_16x16x32_bf16 v[164:167], a[164:167], v[72:75], v[164:167]\n" \
"v_exp_f32_e32 v93, v93\n" \
"v_mfma_f32_16x16x32_bf16 v[136:139], a[168:171], v[68:71], v[136:139]\n" \
"v_exp_f32_e32 v94, v94\n" \
"v_mfma_f32_16x16x32_bf16 v[168:171], a[168:171], v[72:75], v[168:171]\n" \
"v_exp_f32_e32 v95, v95\n" \
"v_mfma_f32_16x16x32_bf16 v[140:143], a[172:175], v[68:71], v[140:143]\n" \
"buffer_load_dwordx4 v51, s[8:11], s64 offen offset:3072 sc1 lds\n" \
"v_mfma_f32_16x16x32_bf16 v[172:175], a[172:175], v[72:75], v[172:175]\n" \
"v_cvt_pk_bf16_f32 v89, v90, v91\n" \
"v_cvt_pk_bf16_f32 v90, v92, v93\n" \
"v_mfma_f32_16x16x32_bf16 v[144:147], a[176:179], v[68:71], v[144:147]\n" \
"v_cvt_pk_bf16_f32 v91, v94, v95\n" \
"v_readlane_b32 s80, v11, 11\n" \
"v_mfma_f32_16x16x32_bf16 v[176:179], a[176:179], v[72:75], v[176:179]\n" \
"v_readlane_b32 s81, v11, 12\n" \
"s_mov_b32 s82, 0x47fca0\n" \
"v_mfma_f32_16x16x32_bf16 v[148:151], a[180:183], v[68:71], v[148:151]\n" \
"s_mov_b32 s83, 0x20000\n" \
"v_readlane_b32 s92, v11, 26\n" \
"v_mfma_f32_16x16x32_bf16 v[180:183], a[180:183], v[72:75], v[180:183]\n" \
"s_add_u32 m0, s93, 0x0\n" \
"ds_read_b128 a[128:131], v48 offset:0\n" \
"v_mfma_f32_16x16x32_bf16 v[152:155], a[184:187], v[68:71], v[152:155]\n" \
"ds_read_b128 a[144:147], v48 offset:4096\n" \
"ds_read_b128 a[132:135], v48 offset:1024\n" \
"v_mfma_f32_16x16x32_bf16 v[184:187], a[184:187], v[72:75], v[184:187]\n" \
"buffer_load_dwordx4 v0, s[80:83], s92 offen offset:0 lds\n" \
"v_mfma_f32_16x16x32_bf16 v[156:159], a[188:191], v[68:71], v[156:159]\n" \
"ds_read_b128 a[148:151], v48 offset:5120\n" \
"ds_read_b128 a[136:139], v48 offset:2048\n" \
"v_mfma_f32_16x16x32_bf16 v[188:191], a[188:191], v[72:75], v[188:191]\n" \
"ds_read_b128 a[152:155], v48 offset:6144\n" \
"ds_read_b128 a[140:143], v48 offset:3072\n" \
"v_smfmac_f32_16x16x64_bf16 a[192:195], a[200:203], v[68:75], v21\n" \
"ds_read_b128 a[156:159], v48 offset:7168\n" \
"v_add_u32_e32 v52, 0x14000, v63\n" \
"v_mfma_f32_16x16x32_bf16 v[220:223], a[188:191], v[84:87], v[220:223]\n" \
"buffer_load_dwordx4 v1, s[80:83], s92 offen offset:1024 lds\n" \
"v_mfma_f32_16x16x32_bf16 v[252:255], a[188:191], v[88:91], v[252:255]\n" \
"v_exp_f32_e32 v102, v102\n" \
"v_mfma_f32_16x16x32_bf16 v[216:219], a[184:187], v[84:87], v[216:219]\n" \
"buffer_load_dwordx4 v2, s[80:83], s92 offen offset:2048 lds\n" \
"v_mfma_f32_16x16x32_bf16 v[248:251], a[184:187], v[88:91], v[248:251]\n" \
"v_exp_f32_e32 v103, v103\n" \
"v_mfma_f32_16x16x32_bf16 v[212:215], a[180:183], v[84:87], v[212:215]\n" \
"v_add_u32_e32 v53, 0x20000, v63\n" \
"v_cvt_pk_bf16_f32 v103, v102, v103\n" \
"v_mfma_f32_16x16x32_bf16 v[244:247], a[180:183], v[88:91], v[244:247]\n" \
"v_exp_f32_e32 v100, v100\n" \
"v_mfma_f32_16x16x32_bf16 v[208:211], a[176:179], v[84:87], v[208:211]\n" \
"buffer_load_dwordx4 v3, s[80:83], s92 offen offset:3072 lds\n" \
"v_mfma_f32_16x16x32_bf16 v[240:243], a[176:179], v[88:91], v[240:243]\n" \
"v_exp_f32_e32 v101, v101\n" \
"v_mfma_f32_16x16x32_bf16 v[204:207], a[172:175], v[84:87], v[204:207]\n" \
"v_exp_f32_e32 v98, v98\n" \
"v_mfma_f32_16x16x32_bf16 v[236:239], a[172:175], v[88:91], v[236:239]\n" \
"v_exp_f32_e32 v99, v99\n" \
"v_mfma_f32_16x16x32_bf16 v[200:203], a[168:171], v[84:87], v[200:203]\n" \
"v_exp_f32_e32 v96, v96\n" \
"v_mfma_f32_16x16x32_bf16 v[232:235], a[168:171], v[88:91], v[232:235]\n" \
"v_exp_f32_e32 v97, v97\n" \
"v_mfma_f32_16x16x32_bf16 v[196:199], a[164:167], v[84:87], v[196:199]\n" \
"v_exp_f32_e32 v104, v104\n" \
"v_mfma_f32_16x16x32_bf16 v[228:231], a[164:167], v[88:91], v[228:231]\n" \
"v_exp_f32_e32 v105, v105\n" \
"v_mfma_f32_16x16x32_bf16 v[192:195], a[160:163], v[84:87], v[192:195]\n" \
"v_cvt_pk_bf16_f32 v102, v100, v101\n" \
"v_cvt_pk_bf16_f32 v101, v98, v99\n" \
"v_mfma_f32_16x16x32_bf16 v[224:227], a[160:163], v[88:91], v[224:227]\n" \
"v_cvt_pk_bf16_f32 v100, v96, v97\n" \
"v_cvt_pk_bf16_f32 v104, v104, v105\n" \
"v_smfmac_f32_16x16x64_bf16 a[196:199], a[200:203], v[84:91], v21\n" \
"v_exp_f32_e32 v106, v106\n" \
"s_waitcnt vmcnt(15) lgkmcnt(0)\n" \
"s_barrier\n" \
"v_mfma_f32_16x16x32_bf16 v[64:67], a[128:131], a[0:3], v[32:35]\n" \
"s_xor_b32 m0, s65, 0x18000\n" \
"ds_read_b128 a[160:163], v49 offset:8192\n" \
"v_mfma_f32_16x16x32_bf16 v[64:67], a[132:135], a[4:7], v[64:67]\n" \
"buffer_load_dwordx4 v52, s[8:11], s64 offen offset:0 sc1 lds\n" \
"v_mfma_f32_16x16x32_bf16 v[64:67], a[136:139], a[8:11], v[64:67]\n" \
"ds_read_b128 a[164:167], v49 offset:9216\n" \
"ds_read_b128 a[168:171], v49 offset:10240\n" \
"v_mfma_f32_16x16x32_bf16 v[64:67], a[140:143], a[12:15], v[64:67]\n" \
"ds_read_b128 a[172:175], v49 offset:11264\n" \
"ds_read_b128 a[176:179], v49 offset:12288\n" \
"v_mfma_f32_16x16x32_bf16 v[68:71], a[156:159], a[12:15], v[32:35]\n" \
"buffer_load_dwordx4 v52, s[8:11], s64 offen offset:1024 sc1 lds\n" \
"v_mfma_f32_16x16x32_bf16 v[68:71], a[152:155], a[8:11], v[68:71]\n" \
"ds_read_b128 a[180:183], v49 offset:13312\n" \
"ds_read_b128 a[184:187], v49 offset:14336\n" \
"v_mfma_f32_16x16x32_bf16 v[68:71], a[148:151], a[4:7], v[68:71]\n" \
"v_exp_f32_e32 v107, v107\n" \
"v_mfma_f32_16x16x32_bf16 v[68:71], a[144:147], a[0:3], v[68:71]\n" \
"v_exp_f32_e32 v108, v108\n" \
"v_mfma_f32_16x16x32_bf16 v[76:79], a[144:147], a[16:19], v[36:39]\n" \
"v_exp_f32_e32 v109, v109\n" \
"v_mfma_f32_16x16x32_bf16 v[76:79], a[148:151], a[20:23], v[76:79]\n" \
"buffer_load_dwordx4 v52, s[8:11], s64 offen offset:2048 sc1 lds\n" \
"v_mfma_f32_16x16x32_bf16 v[76:79], a[152:155], a[24:27], v[76:79]\n" \
"v_exp_f32_e32 v110, v110\n" \
"v_mfma_f32_16x16x32_bf16 v[76:79], a[156:159], a[28:31], v[76:79]\n" \
"v_exp_f32_e32 v111, v111\n" \
"v_mfma_f32_16x16x32_bf16 v[72:75], a[140:143], a[28:31], v[36:39]\n" \
"v_cvt_pk_bf16_f32 v105, v106, v107\n" \
"v_cvt_pk_bf16_f32 v106, v108, v109\n" \
"v_mfma_f32_16x16x32_bf16 v[72:75], a[136:139], a[24:27], v[72:75]\n" \
"buffer_load_dwordx4 v52, s[8:11], s64 offen offset:3072 sc1 lds\n" \
"v_mfma_f32_16x16x32_bf16 v[72:75], a[132:135], a[20:23], v[72:75]\n" \
"s_mov_b32 m0, s65\n" \
"v_cvt_pk_bf16_f32 v107, v110, v111\n" \
"v_mfma_f32_16x16x32_bf16 v[72:75], a[128:131], a[16:19], v[72:75]\n" \
"ds_read_b128 a[188:191], v49 offset:15360\n" \
"v_add_u32_e32 v49, 0x10000, v48\n" \
"v_mfma_f32_16x16x32_bf16 v[80:83], a[128:131], a[32:35], v[40:43]\n" \
"v_exp_f32_e32 v118, v118\n" \
"v_mfma_f32_16x16x32_bf16 v[80:83], a[132:135], a[36:39], v[80:83]\n" \
"v_exp_f32_e32 v119, v119\n" \
"v_mfma_f32_16x16x32_bf16 v[80:83], a[136:139], a[40:43], v[80:83]\n" \
"buffer_load_dwordx4 v53, s[8:11], s64 offen offset:0 sc1 lds\n" \
"v_mfma_f32_16x16x32_bf16 v[80:83], a[140:143], a[44:47], v[80:83]\n" \
"v_exp_f32_e32 v116, v116\n" \
"v_mfma_f32_16x16x32_bf16 v[84:87], a[156:159], a[44:47], v[40:43]\n" \
"v_exp_f32_e32 v117, v117\n" \
"v_mfma_f32_16x16x32_bf16 v[84:87], a[152:155], a[40:43], v[84:87]\n" \
"v_cvt_pk_bf16_f32 v119, v118, v119\n" \
"v_cvt_pk_bf16_f32 v118, v116, v117\n" \
"v_mfma_f32_16x16x32_bf16 v[84:87], a[148:151], a[36:39], v[84:87]\n" \
"v_exp_f32_e32 v114, v114\n" \
"v_mfma_f32_16x16x32_bf16 v[84:87], a[144:147], a[32:35], v[84:87]\n" \
"buffer_load_dwordx4 v53, s[8:11], s64 offen offset:1024 sc1 lds\n" \
"v_mfma_f32_16x16x32_bf16 v[92:95], a[144:147], a[48:51], v[44:47]\n" \
"v_exp_f32_e32 v115, v115\n" \
"v_mfma_f32_16x16x32_bf16 v[92:95], a[148:151], a[52:55], v[92:95]\n" \
"v_exp_f32_e32 v112, v112\n" \
"v_mfma_f32_16x16x32_bf16 v[92:95], a[152:155], a[56:59], v[92:95]\n" \
"v_exp_f32_e32 v113, v113\n" \
"v_mfma_f32_16x16x32_bf16 v[92:95], a[156:159], a[60:63], v[92:95]\n" \
"buffer_load_dwordx4 v53, s[8:11], s64 offen offset:2048 sc1 lds\n" \
"v_mfma_f32_16x16x32_bf16 v[88:91], a[140:143], a[60:63], v[44:47]\n" \
"v_cvt_pk_bf16_f32 v117, v114, v115\n" \
"v_cvt_pk_bf16_f32 v116, v112, v113\n" \
"v_mfma_f32_16x16x32_bf16 v[88:91], a[136:139], a[56:59], v[88:91]\n" \
"v_exp_f32_e32 v120, v120\n" \
"v_mfma_f32_16x16x32_bf16 v[88:91], a[132:135], a[52:55], v[88:91]\n" \
"v_exp_f32_e32 v121, v121\n" \
"v_mfma_f32_16x16x32_bf16 v[88:91], a[128:131], a[48:51], v[88:91]\n" \
"v_cvt_pk_bf16_f32 v120, v120, v121\n" \
"s_waitcnt lgkmcnt(0)\n" \
"v_mfma_f32_16x16x32_bf16 v[128:131], a[160:163], v[100:103], v[128:131]\n" \
"buffer_load_dwordx4 v53, s[8:11], s64 offen offset:3072 sc1 lds\n" \
"v_mfma_f32_16x16x32_bf16 v[160:163], a[160:163], v[104:107], v[160:163]\n" \
"s_add_u32 m0, s93, 0x1000\n" \
"ds_read_b128 a[128:131], v48 offset:8192\n" \
"v_mfma_f32_16x16x32_bf16 v[132:135], a[164:167], v[100:103], v[132:135]\n" \
"v_exp_f32_e32 v122, v122\n" \
"v_mfma_f32_16x16x32_bf16 v[164:167], a[164:167], v[104:107], v[164:167]\n" \
"v_exp_f32_e32 v123, v123\n" \
"v_mfma_f32_16x16x32_bf16 v[136:139], a[168:171], v[100:103], v[136:139]\n" \
"buffer_load_dwordx4 v4, s[80:83], s92 offen offset:0 lds\n" \
"v_mfma_f32_16x16x32_bf16 v[168:171], a[168:171], v[104:107], v[168:171]\n" \
"v_exp_f32_e32 v124, v124\n" \
"v_mfma_f32_16x16x32_bf16 v[140:143], a[172:175], v[100:103], v[140:143]\n" \
"v_exp_f32_e32 v125, v125\n" \
"v_mfma_f32_16x16x32_bf16 v[172:175], a[172:175], v[104:107], v[172:175]\n" \
"v_exp_f32_e32 v126, v126\n" \
"v_mfma_f32_16x16x32_bf16 v[144:147], a[176:179], v[100:103], v[144:147]\n" \
"v_exp_f32_e32 v127, v127\n" \
"v_mfma_f32_16x16x32_bf16 v[176:179], a[176:179], v[104:107], v[176:179]\n" \
"v_cvt_pk_bf16_f32 v121, v122, v123\n" \
"v_cvt_pk_bf16_f32 v122, v124, v125\n" \
"v_mfma_f32_16x16x32_bf16 v[148:151], a[180:183], v[100:103], v[148:151]\n" \
"buffer_load_dwordx4 v5, s[80:83], s92 offen offset:1024 lds\n" \
"v_mfma_f32_16x16x32_bf16 v[180:183], a[180:183], v[104:107], v[180:183]\n" \
"v_cvt_pk_bf16_f32 v123, v126, v127\n" \
"ds_read_b128 a[144:147], v48 offset:12288\n" \
"v_mfma_f32_16x16x32_bf16 v[152:155], a[184:187], v[100:103], v[152:155]\n" \
"ds_read_b128 a[132:135], v48 offset:9216\n" \
"ds_read_b128 a[148:151], v48 offset:13312\n" \
"v_mfma_f32_16x16x32_bf16 v[184:187], a[184:187], v[104:107], v[184:187]\n" \
"ds_read_b128 a[136:139], v48 offset:10240\n" \
"ds_read_b128 a[152:155], v48 offset:14336\n" \
"v_mfma_f32_16x16x32_bf16 v[156:159], a[188:191], v[100:103], v[156:159]\n" \
"ds_read_b128 a[140:143], v48 offset:11264\n" \
"ds_read_b128 a[156:159], v48 offset:15360\n" \
"v_mfma_f32_16x16x32_bf16 v[188:191], a[188:191], v[104:107], v[188:191]\n" \
"buffer_load_dwordx4 v6, s[80:83], s92 offen offset:2048 lds\n" \
"v_smfmac_f32_16x16x64_bf16 a[192:195], a[200:203], v[100:107], v21\n" \
"v_exp_f32_e32 v70, v70\n" \
"v_mfma_f32_16x16x32_bf16 v[220:223], a[188:191], v[116:119], v[220:223]\n" \
"v_exp_f32_e32 v71, v71\n" \
"v_mfma_f32_16x16x32_bf16 v[252:255], a[188:191], v[120:123], v[252:255]\n" \
"buffer_load_dwordx4 v7, s[80:83], s92 offen offset:3072 lds\n" \
"v_mfma_f32_16x16x32_bf16 v[216:219], a[184:187], v[116:119], v[216:219]\n" \
"s_add_u32 m0, s93, 0x2000\n" \
"v_cvt_pk_bf16_f32 v71, v70, v71\n" \
"v_mfma_f32_16x16x32_bf16 v[248:251], a[184:187], v[120:123], v[248:251]\n" \
"v_exp_f32_e32 v72, v72\n" \
"v_mfma_f32_16x16x32_bf16 v[212:215], a[180:183], v[116:119], v[212:215]\n" \
"v_exp_f32_e32 v73, v73\n" \
"v_mfma_f32_16x16x32_bf16 v[244:247], a[180:183], v[120:123], v[244:247]\n" \
"v_exp_f32_e32 v68, v68\n" \
"v_mfma_f32_16x16x32_bf16 v[208:211], a[176:179], v[116:119], v[208:211]\n" \
"buffer_load_dwordx4 v8, s[80:83], s92 offen offset:0 lds\n" \
"v_mfma_f32_16x16x32_bf16 v[240:243], a[176:179], v[120:123], v[240:243]\n" \
"v_exp_f32_e32 v69, v69\n" \
"v_mfma_f32_16x16x32_bf16 v[204:207], a[172:175], v[116:119], v[204:207]\n" \
"v_exp_f32_e32 v74, v74\n" \
"v_mfma_f32_16x16x32_bf16 v[236:239], a[172:175], v[120:123], v[236:239]\n" \
"v_exp_f32_e32 v75, v75\n" \
"v_mfma_f32_16x16x32_bf16 v[200:203], a[168:171], v[116:119], v[200:203]\n" \
"v_exp_f32_e32 v66, v66\n" \
"v_mfma_f32_16x16x32_bf16 v[232:235], a[168:171], v[120:123], v[232:235]\n" \
"v_exp_f32_e32 v67, v67\n" \
"v_mfma_f32_16x16x32_bf16 v[196:199], a[164:167], v[116:119], v[196:199]\n" \
"v_exp_f32_e32 v64, v64\n" \
"v_mfma_f32_16x16x32_bf16 v[228:231], a[164:167], v[120:123], v[228:231]\n" \
"v_exp_f32_e32 v65, v65\n" \
"v_mfma_f32_16x16x32_bf16 v[192:195], a[160:163], v[116:119], v[192:195]\n" \
"v_cvt_pk_bf16_f32 v72, v72, v73\n" \
"v_cvt_pk_bf16_f32 v70, v68, v69\n" \
"v_mfma_f32_16x16x32_bf16 v[224:227], a[160:163], v[120:123], v[224:227]\n" \
"v_exp_f32_e32 v76, v76\n" \
"v_smfmac_f32_16x16x64_bf16 a[196:199], a[200:203], v[116:123], v21\n" \
"v_exp_f32_e32 v77, v77\n" \
"s_waitcnt vmcnt(13) lgkmcnt(0)\n" \
"s_barrier\n" \
"v_exp_f32_e32 v78, v78\n" \
"v_exp_f32_e32 v79, v79\n" \
"v_cvt_pk_bf16_f32 v73, v74, v75\n" \
"v_cvt_pk_bf16_f32 v69, v66, v67\n" \
"v_cvt_pk_bf16_f32 v68, v64, v65\n" \
"v_cvt_pk_bf16_f32 v74, v76, v77\n" \
"v_cvt_pk_bf16_f32 v75, v78, v79\n" \
"v_add_u32_e32 v48, s16, v20\n" \
"s_xor_b32 s65, s65, 0x8000\n" \
"s_add_u32 s64, s64, 0x10000\n" \
"s_getpc_b64 s[86:87]\n" \
"L_hpt_%=:\n" \
"s_add_u32 s86, s86, L_ap8-L_hpt_%=\n" \
"s_addc_u32 s87, s87, 0\n" \
"s_add_u32 s86, s86, s15\n" \
"s_addc_u32 s87, s87, 0\n" \
"s_mov_b64 exec, 0\n" \
"s_swappc_b64 s[88:89], s[86:87]\n" \
"s_mov_b64 exec, -1\n" \
"s_sub_u32 s66, s19, 1\n" \
"s_cbranch_scc1 L_la_%=\n" \
".fill (L_ab + 6464 - .) / 4, 4, 0xBF800000\n" \
"L_2_%=:\n" \
"v_mfma_f32_16x16x32_bf16 v[96:99], a[128:131], a[64:67], v[32:35]\n" \
"s_xor_b32 m0, s65, 0x12000\n" \
"ds_read_b128 a[160:163], v49 offset:0\n" \
"v_mfma_f32_16x16x32_bf16 v[96:99], a[132:135], a[68:71], v[96:99]\n" \
"buffer_load_dwordx4 v50, s[8:11], s64 offen offset:0 sc1 lds\n" \
"v_mfma_f32_16x16x32_bf16 v[96:99], a[136:139], a[72:75], v[96:99]\n" \
"ds_read_b128 a[164:167], v49 offset:1024\n" \
"ds_read_b128 a[168:171], v49 offset:2048\n" \
"v_mfma_f32_16x16x32_bf16 v[96:99], a[140:143], a[76:79], v[96:99]\n" \
"ds_read_b128 a[172:175], v49 offset:3072\n" \
"ds_read_b128 a[176:179], v49 offset:4096\n" \
"v_mfma_f32_16x16x32_bf16 v[100:103], a[156:159], a[76:79], v[32:35]\n" \
"ds_read_b128 a[180:183], v49 offset:5120\n" \
"ds_read_b128 a[184:187], v49 offset:6144\n" \
"v_mfma_f32_16x16x32_bf16 v[100:103], a[152:155], a[72:75], v[100:103]\n" \
"v_exp_f32_e64 v86, v86\n" \
"v_mfma_f32_16x16x32_bf16 v[100:103], a[148:151], a[68:71], v[100:103]\n" \
"v_exp_f32_e64 v87, v87\n" \
"v_mfma_f32_16x16x32_bf16 v[100:103], a[144:147], a[64:67], v[100:103]\n" \
"buffer_load_dwordx4 v50, s[8:11], s64 offen offset:1024 sc1 lds\n" \
"v_mfma_f32_16x16x32_bf16 v[108:111], a[144:147], a[80:83], v[36:39]\n" \
"ds_read_b128 a[188:191], v49 offset:7168\n" \
"v_cvt_pk_bf16_f32 v87, v86, v87\n" \
"v_mfma_f32_16x16x32_bf16 v[108:111], a[148:151], a[84:87], v[108:111]\n" \
"v_exp_f32_e64 v84, v84\n" \
"v_mfma_f32_16x16x32_bf16 v[108:111], a[152:155], a[88:91], v[108:111]\n" \
"v_exp_f32_e64 v85, v85\n" \
"v_mfma_f32_16x16x32_bf16 v[108:111], a[156:159], a[92:95], v[108:111]\n" \
"v_exp_f32_e64 v82, v82\n" \
"v_mfma_f32_16x16x32_bf16 v[104:107], a[140:143], a[92:95], v[36:39]\n" \
"v_exp_f32_e64 v83, v83\n" \
"v_mfma_f32_16x16x32_bf16 v[104:107], a[136:139], a[88:91], v[104:107]\n" \
"v_exp_f32_e64 v80, v80\n" \
"v_mfma_f32_16x16x32_bf16 v[104:107], a[132:135], a[84:87], v[104:107]\n" \
"v_exp_f32_e64 v81, v81\n" \
"v_mfma_f32_16x16x32_bf16 v[104:107], a[128:131], a[80:83], v[104:107]\n" \
"buffer_load_dwordx4 v50, s[8:11], s64 offen offset:2048 sc1 lds\n" \
"v_mfma_f32_16x16x32_bf16 v[112:115], a[128:131], a[96:99], v[40:43]\n" \
"v_exp_f32_e64 v88, v88\n" \
"v_mfma_f32_16x16x32_bf16 v[112:115], a[132:135], a[100:103], v[112:115]\n" \
"v_exp_f32_e64 v89, v89\n" \
"v_mfma_f32_16x16x32_bf16 v[112:115], a[136:139], a[104:107], v[112:115]\n" \
"v_cvt_pk_bf16_f32 v86, v84, v85\n" \
"v_cvt_pk_bf16_f32 v85, v82, v83\n" \
"v_mfma_f32_16x16x32_bf16 v[112:115], a[140:143], a[108:111], v[112:115]\n" \
"v_cvt_pk_bf16_f32 v84, v80, v81\n" \
"v_cvt_pk_bf16_f32 v88, v88, v89\n" \
"v_mfma_f32_16x16x32_bf16 v[116:119], a[156:159], a[108:111], v[40:43]\n" \
"v_exp_f32_e64 v90, v90\n" \
"v_mfma_f32_16x16x32_bf16 v[116:119], a[152:155], a[104:107], v[116:119]\n" \
"v_exp_f32_e64 v91, v91\n" \
"v_mfma_f32_16x16x32_bf16 v[116:119], a[148:151], a[100:103], v[116:119]\n" \
"buffer_load_dwordx4 v50, s[8:11], s64 offen offset:3072 sc1 lds\n" \
"v_mfma_f32_16x16x32_bf16 v[116:119], a[144:147], a[96:99], v[116:119]\n" \
"s_xor_b32 m0, s65, 0xa000\n" \
"v_cvt_pk_bf16_f32 v89, v90, v91\n" \
"v_mfma_f32_16x16x32_bf16 v[124:127], a[144:147], a[112:115], v[44:47]\n" \
"v_exp_f32_e64 v92, v92\n" \
"v_mfma_f32_16x16x32_bf16 v[124:127], a[148:151], a[116:119], v[124:127]\n" \
"v_exp_f32_e64 v93, v93\n" \
"v_mfma_f32_16x16x32_bf16 v[124:127], a[152:155], a[120:123], v[124:127]\n" \
"v_exp_f32_e64 v94, v94\n" \
"v_mfma_f32_16x16x32_bf16 v[124:127], a[156:159], a[124:127], v[124:127]\n" \
"v_exp_f32_e64 v95, v95\n" \
"v_mfma_f32_16x16x32_bf16 v[120:123], a[140:143], a[124:127], v[44:47]\n" \
"v_cvt_pk_bf16_f32 v90, v92, v93\n" \
"v_cvt_pk_bf16_f32 v91, v94, v95\n" \
"v_mfma_f32_16x16x32_bf16 v[120:123], a[136:139], a[120:123], v[120:123]\n" \
"ds_read_b128 a[144:147], v48 offset:4096\n" \
"ds_read_b128 a[148:151], v48 offset:5120\n" \
"v_mfma_f32_16x16x32_bf16 v[120:123], a[132:135], a[116:119], v[120:123]\n" \
"buffer_load_dwordx4 v51, s[8:11], s64 offen offset:0 sc1 lds\n" \
"v_mfma_f32_16x16x32_bf16 v[120:123], a[128:131], a[112:115], v[120:123]\n" \
"ds_read_b128 a[128:131], v48 offset:0\n" \
"s_waitcnt lgkmcnt(3)\n" \
"v_mfma_f32_16x16x32_bf16 v[128:131], a[160:163], v[68:71], v[128:131]\n" \
"ds_read_b128 a[132:135], v48 offset:1024\n" \
"ds_read_b128 a[136:139], v48 offset:2048\n" \
"v_mfma_f32_16x16x32_bf16 v[160:163], a[160:163], v[72:75], v[160:163]\n" \
"ds_read_b128 a[152:155], v48 offset:6144\n" \
"ds_read_b128 a[140:143], v48 offset:3072\n" \
"v_mfma_f32_16x16x32_bf16 v[132:135], a[164:167], v[68:71], v[132:135]\n" \
"v_exp_f32_e32 v102, v102\n" \
"v_mfma_f32_16x16x32_bf16 v[164:167], a[164:167], v[72:75], v[164:167]\n" \
"v_exp_f32_e64 v103, v103\n" \
"v_mfma_f32_16x16x32_bf16 v[136:139], a[168:171], v[68:71], v[136:139]\n" \
"ds_read_b128 a[156:159], v48 offset:7168\n" \
"v_cvt_pk_bf16_f32 v103, v102, v103\n" \
"v_mfma_f32_16x16x32_bf16 v[168:171], a[168:171], v[72:75], v[168:171]\n" \
"buffer_load_dwordx4 v51, s[8:11], s64 offen offset:1024 sc1 lds\n" \
"v_mfma_f32_16x16x32_bf16 v[140:143], a[172:175], v[68:71], v[140:143]\n" \
"v_exp_f32_e64 v100, v100\n" \
"v_mfma_f32_16x16x32_bf16 v[172:175], a[172:175], v[72:75], v[172:175]\n" \
"v_exp_f32_e64 v101, v101\n" \
"v_mfma_f32_16x16x32_bf16 v[144:147], a[176:179], v[68:71], v[144:147]\n" \
"v_exp_f32_e64 v98, v98\n" \
"v_mfma_f32_16x16x32_bf16 v[176:179], a[176:179], v[72:75], v[176:179]\n" \
"v_exp_f32_e64 v99, v99\n" \
"v_mfma_f32_16x16x32_bf16 v[148:151], a[180:183], v[68:71], v[148:151]\n" \
"v_exp_f32_e64 v96, v96\n" \
"v_mfma_f32_16x16x32_bf16 v[180:183], a[180:183], v[72:75], v[180:183]\n" \
"v_exp_f32_e64 v97, v97\n" \
"v_mfma_f32_16x16x32_bf16 v[152:155], a[184:187], v[68:71], v[152:155]\n" \
"v_exp_f32_e64 v104, v104\n" \
"v_mfma_f32_16x16x32_bf16 v[184:187], a[184:187], v[72:75], v[184:187]\n" \
"buffer_load_dwordx4 v51, s[8:11], s64 offen offset:2048 sc1 lds\n" \
"v_mfma_f32_16x16x32_bf16 v[156:159], a[188:191], v[68:71], v[156:159]\n" \
"v_exp_f32_e64 v105, v105\n" \
"v_mfma_f32_16x16x32_bf16 v[188:191], a[188:191], v[72:75], v[188:191]\n" \
"v_cvt_pk_bf16_f32 v102, v100, v101\n" \
"v_cvt_pk_bf16_f32 v101, v98, v99\n" \
"v_smfmac_f32_16x16x64_bf16 a[192:195], a[200:203], v[68:75], v21\n" \
"v_cvt_pk_bf16_f32 v100, v96, v97\n" \
"v_cvt_pk_bf16_f32 v104, v104, v105\n" \
"v_mfma_f32_16x16x32_bf16 v[220:223], a[188:191], v[84:87], v[220:223]\n" \
"v_exp_f32_e64 v106, v106\n" \
"v_mfma_f32_16x16x32_bf16 v[252:255], a[188:191], v[88:91], v[252:255]\n" \
"v_exp_f32_e64 v107, v107\n" \
"v_mfma_f32_16x16x32_bf16 v[216:219], a[184:187], v[84:87], v[216:219]\n" \
"v_exp_f32_e64 v108, v108\n" \
"v_mfma_f32_16x16x32_bf16 v[248:251], a[184:187], v[88:91], v[248:251]\n" \
"buffer_load_dwordx4 v51, s[8:11], s64 offen offset:3072 sc1 lds\n" \
"v_mfma_f32_16x16x32_bf16 v[212:215], a[180:183], v[84:87], v[212:215]\n" \
"v_exp_f32_e64 v109, v109\n" \
"v_mfma_f32_16x16x32_bf16 v[244:247], a[180:183], v[88:91], v[244:247]\n" \
"v_exp_f32_e64 v110, v110\n" \
"v_mfma_f32_16x16x32_bf16 v[208:211], a[176:179], v[84:87], v[208:211]\n" \
"v_exp_f32_e64 v111, v111\n" \
"v_mfma_f32_16x16x32_bf16 v[240:243], a[176:179], v[88:91], v[240:243]\n" \
"v_exp_f32_e64 v118, v118\n" \
"v_mfma_f32_16x16x32_bf16 v[204:207], a[172:175], v[84:87], v[204:207]\n" \
"v_exp_f32_e64 v119, v119\n" \
"v_mfma_f32_16x16x32_bf16 v[236:239], a[172:175], v[88:91], v[236:239]\n" \
"v_cvt_pk_bf16_f32 v105, v106, v107\n" \
"v_cvt_pk_bf16_f32 v106, v108, v109\n" \
"v_mfma_f32_16x16x32_bf16 v[200:203], a[168:171], v[84:87], v[200:203]\n" \
"v_cvt_pk_bf16_f32 v107, v110, v111\n" \
"v_cvt_pk_bf16_f32 v119, v118, v119\n" \
"v_mfma_f32_16x16x32_bf16 v[232:235], a[168:171], v[88:91], v[232:235]\n" \
"v_exp_f32_e64 v116, v116\n" \
"v_mfma_f32_16x16x32_bf16 v[196:199], a[164:167], v[84:87], v[196:199]\n" \
"v_exp_f32_e64 v117, v117\n" \
"v_mfma_f32_16x16x32_bf16 v[228:231], a[164:167], v[88:91], v[228:231]\n" \
"v_exp_f32_e64 v114, v114\n" \
"v_mfma_f32_16x16x32_bf16 v[192:195], a[160:163], v[84:87], v[192:195]\n" \
"v_exp_f32_e64 v115, v115\n" \
"v_mfma_f32_16x16x32_bf16 v[224:227], a[160:163], v[88:91], v[224:227]\n" \
"v_exp_f32_e64 v112, v112\n" \
"v_smfmac_f32_16x16x64_bf16 a[196:199], a[200:203], v[84:91], v21\n" \
"v_exp_f32_e32 v113, v113\n" \
"s_waitcnt vmcnt(8) lgkmcnt(0)\n" \
"s_barrier\n" \
"s_nop 0\n" \
"v_mfma_f32_16x16x32_bf16 v[64:67], a[128:131], a[64:67], v[32:35]\n" \
"s_xor_b32 m0, s65, 0x18000\n" \
"ds_read_b128 a[160:163], v49 offset:8192\n" \
"v_mfma_f32_16x16x32_bf16 v[64:67], a[132:135], a[68:71], v[64:67]\n" \
"buffer_load_dwordx4 v52, s[8:11], s64 offen offset:0 sc1 lds\n" \
"v_mfma_f32_16x16x32_bf16 v[64:67], a[136:139], a[72:75], v[64:67]\n" \
"ds_read_b128 a[164:167], v49 offset:9216\n" \
"ds_read_b128 a[168:171], v49 offset:10240\n" \
"v_mfma_f32_16x16x32_bf16 v[64:67], a[140:143], a[76:79], v[64:67]\n" \
"ds_read_b128 a[172:175], v49 offset:11264\n" \
"ds_read_b128 a[176:179], v49 offset:12288\n" \
"v_mfma_f32_16x16x32_bf16 v[68:71], a[156:159], a[76:79], v[32:35]\n" \
"ds_read_b128 a[180:183], v49 offset:13312\n" \
"ds_read_b128 a[184:187], v49 offset:14336\n" \
"v_mfma_f32_16x16x32_bf16 v[68:71], a[152:155], a[72:75], v[68:71]\n" \
"ds_read_b128 a[188:191], v49 offset:15360\n" \
"v_cvt_pk_bf16_f32 v118, v116, v117\n" \
"v_mfma_f32_16x16x32_bf16 v[68:71], a[148:151], a[68:71], v[68:71]\n" \
"v_cvt_pk_bf16_f32 v117, v114, v115\n" \
"v_cvt_pk_bf16_f32 v116, v112, v113\n" \
"v_mfma_f32_16x16x32_bf16 v[68:71], a[144:147], a[64:67], v[68:71]\n" \
"buffer_load_dwordx4 v52, s[8:11], s64 offen offset:1024 sc1 lds\n" \
"v_mfma_f32_16x16x32_bf16 v[76:79], a[144:147], a[80:83], v[36:39]\n" \
"v_exp_f32_e64 v120, v120\n" \
"v_mfma_f32_16x16x32_bf16 v[76:79], a[148:151], a[84:87], v[76:79]\n" \
"v_exp_f32_e64 v121, v121\n" \
"v_mfma_f32_16x16x32_bf16 v[76:79], a[152:155], a[88:91], v[76:79]\n" \
"v_exp_f32_e64 v122, v122\n" \
"v_mfma_f32_16x16x32_bf16 v[76:79], a[156:159], a[92:95], v[76:79]\n" \
"v_exp_f32_e64 v123, v123\n" \
"v_mfma_f32_16x16x32_bf16 v[72:75], a[140:143], a[92:95], v[36:39]\n" \
"v_cvt_pk_bf16_f32 v120, v120, v121\n" \
"v_cvt_pk_bf16_f32 v121, v122, v123\n" \
"v_mfma_f32_16x16x32_bf16 v[72:75], a[136:139], a[88:91], v[72:75]\n" \
"v_exp_f32_e64 v124, v124\n" \
"v_mfma_f32_16x16x32_bf16 v[72:75], a[132:135], a[84:87], v[72:75]\n" \
"v_exp_f32_e64 v125, v125\n" \
"v_mfma_f32_16x16x32_bf16 v[72:75], a[128:131], a[80:83], v[72:75]\n" \
"buffer_load_dwordx4 v52, s[8:11], s64 offen offset:2048 sc1 lds\n" \
"v_mfma_f32_16x16x32_bf16 v[80:83], a[128:131], a[96:99], v[40:43]\n" \
"v_exp_f32_e64 v126, v126\n" \
"v_mfma_f32_16x16x32_bf16 v[80:83], a[132:135], a[100:103], v[80:83]\n" \
"v_exp_f32_e64 v127, v127\n" \
"v_mfma_f32_16x16x32_bf16 v[80:83], a[136:139], a[104:107], v[80:83]\n" \
"v_cvt_pk_bf16_f32 v122, v124, v125\n" \
"v_cvt_pk_bf16_f32 v123, v126, v127\n" \
"v_mfma_f32_16x16x32_bf16 v[80:83], a[140:143], a[108:111], v[80:83]\n" \
"v_exp_f32_e64 v70, v70\n" \
"v_mfma_f32_16x16x32_bf16 v[84:87], a[156:159], a[108:111], v[40:43]\n" \
"v_exp_f32_e64 v71, v71\n" \
"v_mfma_f32_16x16x32_bf16 v[84:87], a[152:155], a[104:107], v[84:87]\n" \
"v_exp_f32_e64 v72, v72\n" \
"v_mfma_f32_16x16x32_bf16 v[84:87], a[148:151], a[100:103], v[84:87]\n" \
"buffer_load_dwordx4 v52, s[8:11], s64 offen offset:3072 sc1 lds\n" \
"v_mfma_f32_16x16x32_bf16 v[84:87], a[144:147], a[96:99], v[84:87]\n" \
"s_mov_b32 m0, s65\n" \
"v_cvt_pk_bf16_f32 v71, v70, v71\n" \
"v_mfma_f32_16x16x32_bf16 v[92:95], a[144:147], a[112:115], v[44:47]\n" \
"ds_read_b128 a[144:147], v48 offset:12288\n" \
"v_add_u32_e32 v49, 0x10000, v48\n" \
"v_mfma_f32_16x16x32_bf16 v[92:95], a[148:151], a[116:119], v[92:95]\n" \
"ds_read_b128 a[148:151], v48 offset:13312\n" \
"s_xor_b32 s65, s65, 0x8000\n" \
"v_mfma_f32_16x16x32_bf16 v[92:95], a[152:155], a[120:123], v[92:95]\n" \
"v_exp_f32_e32 v73, v73\n" \
"v_mfma_f32_16x16x32_bf16 v[92:95], a[156:159], a[124:127], v[92:95]\n" \
"ds_read_b128 a[152:155], v48 offset:14336\n" \
".fill (L_ab + 8192 - .) / 4, 4, 0xBF800000\n" \
"L_ap2:\n" \
"s_cbranch_execz L_ap0\n" \
"ds_read_b128 a[156:159], v48 offset:15360\n" \
"v_mfma_f32_16x16x32_bf16 v[88:91], a[140:143], a[124:127], v[44:47]\n" \
"ds_read_b128 a[140:143], v48 offset:11264\n" \
"v_cvt_pk_bf16_f32 v72, v72, v73\n" \
"v_mfma_f32_16x16x32_bf16 v[88:91], a[136:139], a[120:123], v[88:91]\n" \
"v_exp_f32_e32 v68, v68\n" \
"v_mfma_f32_16x16x32_bf16 v[88:91], a[132:135], a[116:119], v[88:91]\n" \
"buffer_load_dwordx4 v53, s[8:11], s64 offen offset:0 sc1 lds\n" \
"v_mfma_f32_16x16x32_bf16 v[88:91], a[128:131], a[112:115], v[88:91]\n" \
"ds_read_b128 a[128:131], v48 offset:8192\n" \
"s_waitcnt lgkmcnt(6)\n" \
"v_mfma_f32_16x16x32_bf16 v[128:131], a[160:163], v[100:103], v[128:131]\n" \
"ds_read_b128 a[132:135], v48 offset:9216\n" \
"ds_read_b128 a[136:139], v48 offset:10240\n" \
"v_mfma_f32_16x16x32_bf16 v[160:163], a[160:163], v[104:107], v[160:163]\n" \
"v_exp_f32_e32 v69, v69\n" \
"v_mfma_f32_16x16x32_bf16 v[132:135], a[164:167], v[100:103], v[132:135]\n" \
"v_exp_f32_e64 v74, v74\n" \
"v_mfma_f32_16x16x32_bf16 v[164:167], a[164:167], v[104:107], v[164:167]\n" \
"v_exp_f32_e64 v75, v75\n" \
"v_mfma_f32_16x16x32_bf16 v[136:139], a[168:171], v[100:103], v[136:139]\n" \
"v_exp_f32_e64 v66, v66\n" \
"v_mfma_f32_16x16x32_bf16 v[168:171], a[168:171], v[104:107], v[168:171]\n" \
"buffer_load_dwordx4 v53, s[8:11], s64 offen offset:1024 sc1 lds\n" \
"v_mfma_f32_16x16x32_bf16 v[140:143], a[172:175], v[100:103], v[140:143]\n" \
"v_exp_f32_e64 v67, v67\n" \
"v_mfma_f32_16x16x32_bf16 v[172:175], a[172:175], v[104:107], v[172:175]\n" \
"v_exp_f32_e64 v64, v64\n" \
"v_mfma_f32_16x16x32_bf16 v[144:147], a[176:179], v[100:103], v[144:147]\n" \
"v_exp_f32_e64 v65, v65\n" \
"v_mfma_f32_16x16x32_bf16 v[176:179], a[176:179], v[104:107], v[176:179]\n" \
"v_exp_f32_e64 v76, v76\n" \
"v_mfma_f32_16x16x32_bf16 v[148:151], a[180:183], v[100:103], v[148:151]\n" \
"v_exp_f32_e64 v77, v77\n" \
"v_mfma_f32_16x16x32_bf16 v[180:183], a[180:183], v[104:107], v[180:183]\n" \
"v_exp_f32_e64 v78, v78\n" \
"v_mfma_f32_16x16x32_bf16 v[152:155], a[184:187], v[100:103], v[152:155]\n" \
"v_exp_f32_e64 v79, v79\n" \
"v_mfma_f32_16x16x32_bf16 v[184:187], a[184:187], v[104:107], v[184:187]\n" \
"buffer_load_dwordx4 v53, s[8:11], s64 offen offset:2048 sc1 lds\n" \
"v_mfma_f32_16x16x32_bf16 v[156:159], a[188:191], v[100:103], v[156:159]\n" \
"v_cvt_pk_bf16_f32 v70, v68, v69\n" \
"v_cvt_pk_bf16_f32 v73, v74, v75\n" \
"v_mfma_f32_16x16x32_bf16 v[188:191], a[188:191], v[104:107], v[188:191]\n" \
"v_cvt_pk_bf16_f32 v69, v66, v67\n" \
"v_cvt_pk_bf16_f32 v68, v64, v65\n" \
"v_smfmac_f32_16x16x64_bf16 a[192:195], a[200:203], v[100:107], v21\n" \
"v_cvt_pk_bf16_f32 v74, v76, v77\n" \
"v_cvt_pk_bf16_f32 v75, v78, v79\n" \
"v_mfma_f32_16x16x32_bf16 v[220:223], a[188:191], v[116:119], v[220:223]\n" \
"v_xor_b32_e32 v48, 0x8000, v48\n" \
"v_mfma_f32_16x16x32_bf16 v[252:255], a[188:191], v[120:123], v[252:255]\n" \
"v_mfma_f32_16x16x32_bf16 v[216:219], a[184:187], v[116:119], v[216:219]\n" \
"v_mfma_f32_16x16x32_bf16 v[248:251], a[184:187], v[120:123], v[248:251]\n" \
"buffer_load_dwordx4 v53, s[8:11], s64 offen offset:3072 sc1 lds\n" \
"v_mfma_f32_16x16x32_bf16 v[212:215], a[180:183], v[116:119], v[212:215]\n" \
"s_add_u32 s64, s64, 0x10000\n" \
"s_sub_u32 s66, s66, 1\n" \
"s_nop 0\n" \
"v_mfma_f32_16x16x32_bf16 v[244:247], a[180:183], v[120:123], v[244:247]\n" \
"v_mfma_f32_16x16x32_bf16 v[208:211], a[176:179], v[116:119], v[208:211]\n" \
"v_mfma_f32_16x16x32_bf16 v[240:243], a[176:179], v[120:123], v[240:243]\n" \
"v_mfma_f32_16x16x32_bf16 v[204:207], a[172:175], v[116:119], v[204:207]\n" \
"v_mfma_f32_16x16x32_bf16 v[236:239], a[172:175], v[120:123], v[236:239]\n" \
"v_mfma_f32_16x16x32_bf16 v[200:203], a[168:171], v[116:119], v[200:203]\n" \
"v_mfma_f32_16x16x32_bf16 v[232:235], a[168:171], v[120:123], v[232:235]\n" \
"v_mfma_f32_16x16x32_bf16 v[196:199], a[164:167], v[116:119], v[196:199]\n" \
"v_mfma_f32_16x16x32_bf16 v[228:231], a[164:167], v[120:123], v[228:231]\n" \
"v_mfma_f32_16x16x32_bf16 v[192:195], a[160:163], v[116:119], v[192:195]\n" \
"v_mfma_f32_16x16x32_bf16 v[224:227], a[160:163], v[120:123], v[224:227]\n" \
"v_smfmac_f32_16x16x64_bf16 a[196:199], a[200:203], v[116:123], v21\n" \
"s_waitcnt vmcnt(8) lgkmcnt(0)\n" \
"s_barrier\n" \
"s_cbranch_scc0 L_2_%=\n" \
"s_nop 0\n" \
"L_la_%=:\n" \
"v_mfma_f32_16x16x32_bf16 v[96:99], a[128:131], a[64:67], v[32:35]\n" \
"v_readlane_b32 s96, v11, 1\n" \
"v_bfe_u32 v27, v20, 2, 7\n" \
"v_mfma_f32_16x16x32_bf16 v[96:99], a[132:135], a[68:71], v[96:99]\n" \
"s_xor_b32 m0, s65, 0x12000\n" \
"ds_read_b128 a[160:163], v49 offset:0\n" \
"v_mfma_f32_16x16x32_bf16 v[96:99], a[136:139], a[72:75], v[96:99]\n" \
"buffer_load_dword v26, v27, s[56:59], s96 offen sc1\n" \
"v_mfma_f32_16x16x32_bf16 v[96:99], a[140:143], a[76:79], v[96:99]\n" \
"buffer_load_dwordx4 v50, s[8:11], s64 offen offset:0 sc1 lds\n" \
"v_mfma_f32_16x16x32_bf16 v[100:103], a[156:159], a[76:79], v[32:35]\n" \
"ds_read_b128 a[164:167], v49 offset:1024\n" \
"ds_read_b128 a[168:171], v49 offset:2048\n" \
"v_mfma_f32_16x16x32_bf16 v[100:103], a[152:155], a[72:75], v[100:103]\n" \
"ds_read_b128 a[172:175], v49 offset:3072\n" \
"ds_read_b128 a[176:179], v49 offset:4096\n" \
"v_mfma_f32_16x16x32_bf16 v[100:103], a[148:151], a[68:71], v[100:103]\n" \
"ds_read_b128 a[180:183], v49 offset:5120\n" \
"ds_read_b128 a[184:187], v49 offset:6144\n" \
"v_mfma_f32_16x16x32_bf16 v[100:103], a[144:147], a[64:67], v[100:103]\n" \
"ds_read_b128 a[188:191], v49 offset:7168\n" \
"v_readlane_b32 s97, v11, 36\n" \
"v_mfma_f32_16x16x32_bf16 v[108:111], a[144:147], a[80:83], v[36:39]\n" \
"v_exp_f32_e64 v86, v86\n" \
"v_mfma_f32_16x16x32_bf16 v[108:111], a[148:151], a[84:87], v[108:111]\n" \
"v_exp_f32_e64 v87, v87\n" \
"v_mfma_f32_16x16x32_bf16 v[108:111], a[152:155], a[88:91], v[108:111]\n" \
"buffer_load_dwordx4 v50, s[8:11], s64 offen offset:1024 sc1 lds\n" \
"v_mfma_f32_16x16x32_bf16 v[108:111], a[156:159], a[92:95], v[108:111]\n" \
"v_exp_f32_e64 v84, v84\n" \
"v_mfma_f32_16x16x32_bf16 v[104:107], a[140:143], a[92:95], v[36:39]\n" \
"v_exp_f32_e64 v85, v85\n" \
"v_mfma_f32_16x16x32_bf16 v[104:107], a[136:139], a[88:91], v[104:107]\n" \
"v_cvt_pk_bf16_f32 v87, v86, v87\n" \
"v_cvt_pk_bf16_f32 v86, v84, v85\n" \
"v_mfma_f32_16x16x32_bf16 v[104:107], a[132:135], a[84:87], v[104:107]\n" \
"v_exp_f32_e64 v82, v82\n" \
"v_mfma_f32_16x16x32_bf16 v[104:107], a[128:131], a[80:83], v[104:107]\n" \
"v_exp_f32_e64 v83, v83\n" \
"v_mfma_f32_16x16x32_bf16 v[112:115], a[128:131], a[96:99], v[40:43]\n" \
"v_exp_f32_e64 v80, v80\n" \
"v_mfma_f32_16x16x32_bf16 v[112:115], a[132:135], a[100:103], v[112:115]\n" \
"v_exp_f32_e64 v81, v81\n" \
"v_mfma_f32_16x16x32_bf16 v[112:115], a[136:139], a[104:107], v[112:115]\n" \
"buffer_load_dwordx4 v50, s[8:11], s64 offen offset:2048 sc1 lds\n" \
"v_mfma_f32_16x16x32_bf16 v[112:115], a[140:143], a[108:111], v[112:115]\n" \
"v_cvt_pk_bf16_f32 v85, v82, v83\n" \
"v_cvt_pk_bf16_f32 v84, v80, v81\n" \
"v_mfma_f32_16x16x32_bf16 v[116:119], a[156:159], a[108:111], v[40:43]\n" \
"v_exp_f32_e64 v88, v88\n" \
"v_mfma_f32_16x16x32_bf16 v[116:119], a[152:155], a[104:107], v[116:119]\n" \
"v_exp_f32_e64 v89, v89\n" \
"v_mfma_f32_16x16x32_bf16 v[116:119], a[148:151], a[100:103], v[116:119]\n" \
"v_exp_f32_e64 v90, v90\n" \
"v_mfma_f32_16x16x32_bf16 v[116:119], a[144:147], a[96:99], v[116:119]\n" \
"v_exp_f32_e64 v91, v91\n" \
"v_mfma_f32_16x16x32_bf16 v[124:127], a[144:147], a[112:115], v[44:47]\n" \
"v_cvt_pk_bf16_f32 v88, v88, v89\n" \
"v_cvt_pk_bf16_f32 v89, v90, v91\n" \
"v_mfma_f32_16x16x32_bf16 v[124:127], a[148:151], a[116:119], v[124:127]\n" \
"buffer_load_dwordx4 v50, s[8:11], s64 offen offset:3072 sc1 lds\n" \
"v_mfma_f32_16x16x32_bf16 v[124:127], a[152:155], a[120:123], v[124:127]\n" \
"v_exp_f32_e64 v92, v92\n" \
"v_mfma_f32_16x16x32_bf16 v[124:127], a[156:159], a[124:127], v[124:127]\n" \
"v_exp_f32_e64 v93, v93\n" \
"v_mfma_f32_16x16x32_bf16 v[120:123], a[140:143], a[124:127], v[44:47]\n" \
"v_exp_f32_e64 v94, v94\n" \
"v_mfma_f32_16x16x32_bf16 v[120:123], a[136:139], a[120:123], v[120:123]\n" \
"v_exp_f32_e64 v95, v95\n" \
"v_mfma_f32_16x16x32_bf16 v[120:123], a[132:135], a[116:119], v[120:123]\n" \
"v_cvt_pk_bf16_f32 v90, v92, v93\n" \
"v_cvt_pk_bf16_f32 v91, v94, v95\n" \
"v_mfma_f32_16x16x32_bf16 v[120:123], a[128:131], a[112:115], v[120:123]\n" \
"ds_read_b128 a[128:131], v48 offset:0\n" \
"s_waitcnt lgkmcnt(1)\n" \
"s_nop 0\n" \
"v_mfma_f32_16x16x32_bf16 v[128:131], a[160:163], v[68:71], v[128:131]\n" \
"ds_read_b128 a[144:147], v48 offset:4096\n" \
"ds_read_b128 a[132:135], v48 offset:1024\n" \
"v_mfma_f32_16x16x32_bf16 v[160:163], a[160:163], v[72:75], v[160:163]\n" \
"ds_read_b128 a[148:151], v48 offset:5120\n" \
"ds_read_b128 a[136:139], v48 offset:2048\n" \
"v_mfma_f32_16x16x32_bf16 v[132:135], a[164:167], v[68:71], v[132:135]\n" \
"ds_read_b128 a[152:155], v48 offset:6144\n" \
"ds_read_b128 a[140:143], v48 offset:3072\n" \
"v_mfma_f32_16x16x32_bf16 v[164:167], a[164:167], v[72:75], v[164:167]\n" \
"ds_read_b128 a[156:159], v48 offset:7168\n" \
"v_add_u32_e32 v17, 0x49000, v20\n" \
"v_mfma_f32_16x16x32_bf16 v[136:139], a[168:171], v[68:71], v[136:139]\n" \
"v_readlane_b32 s98, v11, 37\n" \
"v_readlane_b32 s93, v11, 20\n" \
"v_mfma_f32_16x16x32_bf16 v[168:171], a[168:171], v[72:75], v[168:171]\n" \
"v_readlane_b32 s84, v11, 15\n" \
"v_readlane_b32 s85, v11, 16\n" \
"v_mfma_f32_16x16x32_bf16 v[140:143], a[172:175], v[68:71], v[140:143]\n" \
"v_readlane_b32 s86, v11, 17\n" \
"s_mov_b32 s87, 0x20000\n" \
"v_mfma_f32_16x16x32_bf16 v[172:175], a[172:175], v[72:75], v[172:175]\n" \
"v_readlane_b32 s92, v11, 18\n" \
"v_readlane_b32 s91, v11, 21\n" \
"v_mfma_f32_16x16x32_bf16 v[144:147], a[176:179], v[68:71], v[144:147]\n" \
"v_exp_f32_e64 v102, v102\n" \
"v_mfma_f32_16x16x32_bf16 v[176:179], a[176:179], v[72:75], v[176:179]\n" \
"v_exp_f32_e64 v103, v103\n" \
"v_mfma_f32_16x16x32_bf16 v[148:151], a[180:183], v[68:71], v[148:151]\n" \
"v_exp_f32_e64 v100, v100\n" \
"v_mfma_f32_16x16x32_bf16 v[180:183], a[180:183], v[72:75], v[180:183]\n" \
"v_exp_f32_e64 v101, v101\n" \
"v_mfma_f32_16x16x32_bf16 v[152:155], a[184:187], v[68:71], v[152:155]\n" \
"v_cvt_pk_bf16_f32 v103, v102, v103\n" \
"v_cvt_pk_bf16_f32 v102, v100, v101\n" \
"v_mfma_f32_16x16x32_bf16 v[184:187], a[184:187], v[72:75], v[184:187]\n" \
"v_exp_f32_e64 v98, v98\n" \
"v_mfma_f32_16x16x32_bf16 v[156:159], a[188:191], v[68:71], v[156:159]\n" \
"v_exp_f32_e64 v99, v99\n" \
"v_mfma_f32_16x16x32_bf16 v[188:191], a[188:191], v[72:75], v[188:191]\n" \
"s_waitcnt vmcnt(4)\n" \
"v_cmp_ne_u32_e32 vcc, s62, v26\n" \
"v_smfmac_f32_16x16x64_bf16 a[192:195], a[200:203], v[68:75], v21\n" \
"v_exp_f32_e64 v96, v96\n" \
"v_mfma_f32_16x16x32_bf16 v[220:223], a[188:191], v[84:87], v[220:223]\n" \
"v_exp_f32_e64 v97, v97\n" \
"v_mfma_f32_16x16x32_bf16 v[252:255], a[188:191], v[88:91], v[252:255]\n" \
"s_cbranch_vccnz L_sol_%=\n" \
"L_rol_%=:\n" \
"v_cvt_pk_bf16_f32 v101, v98, v99\n" \
"buffer_load_dwordx4 a[220:223], v20, s[56:59], s97 offen offset:0 sc1\n" \
"v_mfma_f32_16x16x32_bf16 v[216:219], a[184:187], v[84:87], v[216:219]\n" \
"v_cvt_pk_bf16_f32 v100, v96, v97\n" \
"s_xor_b32 s65, s65, 0x8000\n" \
"v_mfma_f32_16x16x32_bf16 v[248:251], a[184:187], v[88:91], v[248:251]\n" \
"v_exp_f32_e32 v104, v104\n" \
"v_mfma_f32_16x16x32_bf16 v[212:215], a[180:183], v[84:87], v[212:215]\n" \
"v_exp_f32_e64 v105, v105\n" \
"v_mfma_f32_16x16x32_bf16 v[244:247], a[180:183], v[88:91], v[244:247]\n" \
"v_exp_f32_e64 v106, v106\n" \
"v_mfma_f32_16x16x32_bf16 v[208:211], a[176:179], v[84:87], v[208:211]\n" \
"v_exp_f32_e64 v107, v107\n" \
"v_mfma_f32_16x16x32_bf16 v[240:243], a[176:179], v[88:91], v[240:243]\n" \
"buffer_load_dwordx4 a[228:231], v17, s[56:59], s97 offen offset:0 sc1\n" \
"v_mfma_f32_16x16x32_bf16 v[204:207], a[172:175], v[84:87], v[204:207]\n" \
"v_cvt_pk_bf16_f32 v104, v104, v105\n" \
"v_cvt_pk_bf16_f32 v105, v106, v107\n" \
"v_mfma_f32_16x16x32_bf16 v[236:239], a[172:175], v[88:91], v[236:239]\n" \
"v_exp_f32_e64 v108, v108\n" \
"v_mfma_f32_16x16x32_bf16 v[200:203], a[168:171], v[84:87], v[200:203]\n" \
"v_exp_f32_e64 v109, v109\n" \
"v_mfma_f32_16x16x32_bf16 v[232:235], a[168:171], v[88:91], v[232:235]\n" \
"v_exp_f32_e64 v110, v110\n" \
"v_mfma_f32_16x16x32_bf16 v[196:199], a[164:167], v[84:87], v[196:199]\n" \
"v_exp_f32_e64 v111, v111\n" \
"v_mfma_f32_16x16x32_bf16 v[228:231], a[164:167], v[88:91], v[228:231]\n" \
"v_cvt_pk_bf16_f32 v106, v108, v109\n" \
"v_cvt_pk_bf16_f32 v107, v110, v111\n" \
"v_mfma_f32_16x16x32_bf16 v[192:195], a[160:163], v[84:87], v[192:195]\n" \
"v_exp_f32_e64 v118, v118\n" \
"v_mfma_f32_16x16x32_bf16 v[224:227], a[160:163], v[88:91], v[224:227]\n" \
"v_exp_f32_e64 v119, v119\n" \
"v_smfmac_f32_16x16x64_bf16 a[196:199], a[200:203], v[84:91], v21\n" \
"buffer_load_dwordx4 a[236:239], v20, s[56:59], s98 offen offset:0 sc1\n" \
"s_waitcnt vmcnt(8) lgkmcnt(0)\n" \
"s_barrier\n" \
"v_mfma_f32_16x16x32_bf16 v[64:67], a[128:131], a[64:67], v[32:35]\n" \
"s_add_u32 m0, s93, 0x0\n" \
"s_nop 0\n" \
"ds_read_b128 a[160:163], v49 offset:8192\n" \
"v_mfma_f32_16x16x32_bf16 v[64:67], a[132:135], a[68:71], v[64:67]\n" \
"buffer_load_dwordx4 v0, s[84:87], s92 offen offset:0 lds\n" \
"v_mfma_f32_16x16x32_bf16 v[64:67], a[136:139], a[72:75], v[64:67]\n" \
"ds_read_b128 a[164:167], v49 offset:9216\n" \
"ds_read_b128 a[168:171], v49 offset:10240\n" \
"v_mfma_f32_16x16x32_bf16 v[64:67], a[140:143], a[76:79], v[64:67]\n" \
"ds_read_b128 a[172:175], v49 offset:11264\n" \
"ds_read_b128 a[176:179], v49 offset:12288\n" \
"v_mfma_f32_16x16x32_bf16 v[68:71], a[156:159], a[76:79], v[32:35]\n" \
"ds_read_b128 a[180:183], v49 offset:13312\n" \
"ds_read_b128 a[184:187], v49 offset:14336\n" \
"v_mfma_f32_16x16x32_bf16 v[68:71], a[152:155], a[72:75], v[68:71]\n" \
"ds_read_b128 a[188:191], v49 offset:15360\n" \
"v_cvt_pk_bf16_f32 v119, v118, v119\n" \
"v_mfma_f32_16x16x32_bf16 v[68:71], a[148:151], a[68:71], v[68:71]\n" \
"buffer_load_dwordx4 v1, s[84:87], s92 offen offset:1024 lds\n" \
"v_mfma_f32_16x16x32_bf16 v[68:71], a[144:147], a[64:67], v[68:71]\n" \
"buffer_load_dwordx4 a[244:247], v17, s[56:59], s98 offen offset:0 sc1\n" \
"v_mfma_f32_16x16x32_bf16 v[76:79], a[144:147], a[80:83], v[36:39]\n" \
"v_exp_f32_e64 v116, v116\n" \
"v_mfma_f32_16x16x32_bf16 v[76:79], a[148:151], a[84:87], v[76:79]\n" \
"v_exp_f32_e64 v117, v117\n" \
"v_mfma_f32_16x16x32_bf16 v[76:79], a[152:155], a[88:91], v[76:79]\n" \
"v_exp_f32_e64 v114, v114\n" \
"v_mfma_f32_16x16x32_bf16 v[76:79], a[156:159], a[92:95], v[76:79]\n" \
"v_exp_f32_e64 v115, v115\n" \
"v_mfma_f32_16x16x32_bf16 v[72:75], a[140:143], a[92:95], v[36:39]\n" \
"v_exp_f32_e64 v112, v112\n" \
"v_mfma_f32_16x16x32_bf16 v[72:75], a[136:139], a[88:91], v[72:75]\n" \
"buffer_load_dwordx4 v2, s[84:87], s92 offen offset:2048 lds\n" \
"v_mfma_f32_16x16x32_bf16 v[72:75], a[132:135], a[84:87], v[72:75]\n" \
"v_exp_f32_e64 v113, v113\n" \
"v_mfma_f32_16x16x32_bf16 v[72:75], a[128:131], a[80:83], v[72:75]\n" \
"v_cvt_pk_bf16_f32 v118, v116, v117\n" \
"v_cvt_pk_bf16_f32 v117, v114, v115\n" \
"v_mfma_f32_16x16x32_bf16 v[80:83], a[128:131], a[96:99], v[40:43]\n" \
"buffer_load_dwordx4 a[224:227], v20, s[56:59], s97 offen offset:1024 sc1\n" \
"v_mfma_f32_16x16x32_bf16 v[80:83], a[132:135], a[100:103], v[80:83]\n" \
"v_cvt_pk_bf16_f32 v116, v112, v113\n" \
"v_add_u32_e32 v49, 0x10000, v48\n" \
"v_mfma_f32_16x16x32_bf16 v[80:83], a[136:139], a[104:107], v[80:83]\n" \
"v_exp_f32_e64 v120, v120\n" \
"v_mfma_f32_16x16x32_bf16 v[80:83], a[140:143], a[108:111], v[80:83]\n" \
"v_exp_f32_e64 v121, v121\n" \
"v_mfma_f32_16x16x32_bf16 v[84:87], a[156:159], a[108:111], v[40:43]\n" \
"buffer_load_dwordx4 v3, s[84:87], s92 offen offset:3072 lds\n" \
"v_mfma_f32_16x16x32_bf16 v[84:87], a[152:155], a[104:107], v[84:87]\n" \
"s_add_u32 m0, s91, 0x1000\n" \
"v_cvt_pk_bf16_f32 v120, v120, v121\n" \
"v_mfma_f32_16x16x32_bf16 v[84:87], a[148:151], a[100:103], v[84:87]\n" \
"v_exp_f32_e64 v122, v122\n" \
"v_mfma_f32_16x16x32_bf16 v[84:87], a[144:147], a[96:99], v[84:87]\n" \
"v_exp_f32_e64 v123, v123\n" \
"v_mfma_f32_16x16x32_bf16 v[92:95], a[144:147], a[112:115], v[44:47]\n" \
"v_exp_f32_e64 v124, v124\n" \
"v_mfma_f32_16x16x32_bf16 v[92:95], a[148:151], a[116:119], v[92:95]\n" \
"buffer_load_dwordx4 a[232:235], v17, s[56:59], s97 offen offset:1024 sc1\n" \
"v_mfma_f32_16x16x32_bf16 v[92:95], a[152:155], a[120:123], v[92:95]\n" \
"buffer_load_dwordx4 v4, s[84:87], s92 offen offset:0 lds\n" \
"v_mfma_f32_16x16x32_bf16 v[92:95], a[156:159], a[124:127], v[92:95]\n" \
"v_exp_f32_e64 v125, v125\n" \
"v_mfma_f32_16x16x32_bf16 v[88:91], a[140:143], a[124:127], v[44:47]\n" \
"v_exp_f32_e64 v126, v126\n" \
"v_mfma_f32_16x16x32_bf16 v[88:91], a[136:139], a[120:123], v[88:91]\n" \
"v_exp_f32_e64 v127, v127\n" \
"v_mfma_f32_16x16x32_bf16 v[88:91], a[132:135], a[116:119], v[88:91]\n" \
"v_cvt_pk_bf16_f32 v121, v122, v123\n" \
"v_cvt_pk_bf16_f32 v122, v124, v125\n" \
"v_mfma_f32_16x16x32_bf16 v[88:91], a[128:131], a[112:115], v[88:91]\n" \
"v_cvt_pk_bf16_f32 v123, v126, v127\n" \
"s_waitcnt lgkmcnt(0)\n" \
"s_nop 0\n" \
"v_mfma_f32_16x16x32_bf16 v[128:131], a[160:163], v[100:103], v[128:131]\n" \
"ds_read_b128 a[128:131], v48 offset:8192\n" \
"ds_read_b128 a[144:147], v48 offset:12288\n" \
"v_mfma_f32_16x16x32_bf16 v[160:163], a[160:163], v[104:107], v[160:163]\n" \
"buffer_load_dwordx4 v5, s[84:87], s92 offen offset:1024 lds\n" \
"v_mfma_f32_16x16x32_bf16 v[132:135], a[164:167], v[100:103], v[132:135]\n" \
"buffer_load_dwordx4 a[240:243], v20, s[56:59], s98 offen offset:1024 sc1\n" \
"v_mfma_f32_16x16x32_bf16 v[164:167], a[164:167], v[104:107], v[164:167]\n" \
"ds_read_b128 a[132:135], v48 offset:9216\n" \
"ds_read_b128 a[148:151], v48 offset:13312\n" \
"v_mfma_f32_16x16x32_bf16 v[136:139], a[168:171], v[100:103], v[136:139]\n" \
"ds_read_b128 a[136:139], v48 offset:10240\n" \
"ds_read_b128 a[152:155], v48 offset:14336\n" \
"v_mfma_f32_16x16x32_bf16 v[168:171], a[168:171], v[104:107], v[168:171]\n" \
"ds_read_b128 a[140:143], v48 offset:11264\n" \
"ds_read_b128 a[156:159], v48 offset:15360\n" \
"v_mfma_f32_16x16x32_bf16 v[140:143], a[172:175], v[100:103], v[140:143]\n" \
"v_exp_f32_e64 v70, v70\n" \
"v_mfma_f32_16x16x32_bf16 v[172:175], a[172:175], v[104:107], v[172:175]\n" \
"v_exp_f32_e64 v71, v71\n" \
"v_mfma_f32_16x16x32_bf16 v[144:147], a[176:179], v[100:103], v[144:147]\n" \
"buffer_load_dwordx4 v6, s[84:87], s92 offen offset:2048 lds\n" \
"v_mfma_f32_16x16x32_bf16 v[176:179], a[176:179], v[104:107], v[176:179]\n" \
"v_exp_f32_e64 v72, v72\n" \
"v_mfma_f32_16x16x32_bf16 v[148:151], a[180:183], v[100:103], v[148:151]\n" \
"v_exp_f32_e64 v73, v73\n" \
"v_mfma_f32_16x16x32_bf16 v[180:183], a[180:183], v[104:107], v[180:183]\n" \
"buffer_load_dwordx4 a[248:251], v17, s[56:59], s98 offen offset:1024 sc1\n" \
"v_mfma_f32_16x16x32_bf16 v[152:155], a[184:187], v[100:103], v[152:155]\n" \
"v_exp_f32_e64 v68, v68\n" \
"v_mfma_f32_16x16x32_bf16 v[184:187], a[184:187], v[104:107], v[184:187]\n" \
"v_exp_f32_e64 v69, v69\n" \
"v_mfma_f32_16x16x32_bf16 v[156:159], a[188:191], v[100:103], v[156:159]\n" \
"buffer_load_dwordx4 v7, s[84:87], s92 offen offset:3072 lds\n" \
"v_mfma_f32_16x16x32_bf16 v[188:191], a[188:191], v[104:107], v[188:191]\n" \
"s_add_u32 m0, s91, 0x2000\n" \
"v_cvt_pk_bf16_f32 v71, v70, v71\n" \
"v_smfmac_f32_16x16x64_bf16 a[192:195], a[200:203], v[100:107], v21\n" \
"v_exp_f32_e64 v74, v74\n" \
"v_mfma_f32_16x16x32_bf16 v[220:223], a[188:191], v[116:119], v[220:223]\n" \
"v_exp_f32_e64 v75, v75\n" \
"v_mfma_f32_16x16x32_bf16 v[252:255], a[188:191], v[120:123], v[252:255]\n" \
"v_exp_f32_e64 v66, v66\n" \
"v_mfma_f32_16x16x32_bf16 v[216:219], a[184:187], v[116:119], v[216:219]\n" \
"v_exp_f32_e64 v67, v67\n" \
"v_mfma_f32_16x16x32_bf16 v[248:251], a[184:187], v[120:123], v[248:251]\n" \
"v_exp_f32_e64 v64, v64\n" \
"v_mfma_f32_16x16x32_bf16 v[212:215], a[180:183], v[116:119], v[212:215]\n" \
"buffer_load_dwordx4 v8, s[84:87], s92 offen offset:0 lds\n" \
"v_mfma_f32_16x16x32_bf16 v[244:247], a[180:183], v[120:123], v[244:247]\n" \
"v_exp_f32_e64 v65, v65\n" \
"v_mfma_f32_16x16x32_bf16 v[208:211], a[176:179], v[116:119], v[208:211]\n" \
"v_cvt_pk_bf16_f32 v72, v72, v73\n" \
"v_cvt_pk_bf16_f32 v70, v68, v69\n" \
"v_mfma_f32_16x16x32_bf16 v[240:243], a[176:179], v[120:123], v[240:243]\n" \
"v_exp_f32_e64 v76, v76\n" \
"v_mfma_f32_16x16x32_bf16 v[204:207], a[172:175], v[116:119], v[204:207]\n" \
"v_exp_f32_e64 v77, v77\n" \
"v_mfma_f32_16x16x32_bf16 v[236:239], a[172:175], v[120:123], v[236:239]\n" \
"v_exp_f32_e64 v78, v78\n" \
"v_mfma_f32_16x16x32_bf16 v[200:203], a[168:171], v[116:119], v[200:203]\n" \
"v_exp_f32_e64 v79, v79\n" \
"v_mfma_f32_16x16x32_bf16 v[232:235], a[168:171], v[120:123], v[232:235]\n" \
"v_cvt_pk_bf16_f32 v73, v74, v75\n" \
"v_cvt_pk_bf16_f32 v69, v66, v67\n" \
"v_mfma_f32_16x16x32_bf16 v[196:199], a[164:167], v[116:119], v[196:199]\n" \
"v_cvt_pk_bf16_f32 v68, v64, v65\n" \
"v_cvt_pk_bf16_f32 v74, v76, v77\n" \
"v_mfma_f32_16x16x32_bf16 v[228:231], a[164:167], v[120:123], v[228:231]\n" \
"v_cvt_pk_bf16_f32 v75, v78, v79\n" \
"v_xor_b32_e32 v48, 0x8000, v48\n" \
"v_mfma_f32_16x16x32_bf16 v[192:195], a[160:163], v[116:119], v[192:195]\n" \
"s_add_u32 s64, s64, 0x10000\n" \
"v_mfma_f32_16x16x32_bf16 v[224:227], a[160:163], v[120:123], v[224:227]\n" \
"v_smfmac_f32_16x16x64_bf16 a[196:199], a[200:203], v[116:123], v21\n" \
"s_waitcnt vmcnt(14) lgkmcnt(0)\n" \
"s_barrier\n" \
"s_cmp_lg_u32 s16, 0\n" \
"s_cbranch_scc1 L_f1_%=\n" \
"ds_read_b128 a[160:163], v49 offset:0\n" \
"ds_read_b128 a[164:167], v49 offset:1024\n" \
"ds_read_b128 a[168:171], v49 offset:2048\n" \
"ds_read_b128 a[172:175], v49 offset:3072\n" \
"ds_read_b128 a[176:179], v49 offset:4096\n" \
"ds_read_b128 a[180:183], v49 offset:5120\n" \
"ds_read_b128 a[184:187], v49 offset:6144\n" \
"ds_read_b128 a[188:191], v49 offset:7168\n" \
"v_add_u32_e32 v54, 0x49000, v20\n" \
"buffer_load_dwordx4 a[96:99], v20, s[56:59], s60 offen offset:0 sc1\n" \
"v_add_u32_e32 v50, s15, v20\n" \
"v_add_u32_e32 v51, 0x10000, v50\n" \
"ds_read_b32 v60, v18 offset:0\n" \
"ds_read_b32 v61, v18 offset:128\n" \
"ds_read_b128 a[0:3], v49 offset:8192\n" \
"ds_read_b128 a[4:7], v49 offset:9216\n" \
"ds_read_b128 a[8:11], v49 offset:10240\n" \
"ds_read_b128 a[12:15], v49 offset:11264\n" \
"ds_read_b128 a[16:19], v49 offset:12288\n" \
"ds_read_b128 a[20:23], v49 offset:13312\n" \
"ds_read_b128 a[24:27], v49 offset:14336\n" \
"ds_read_b128 a[28:31], v49 offset:15360\n" \
"v_exp_f32_e32 v86, v86\n" \
"v_exp_f32_e32 v87, v87\n" \
"v_exp_f32_e32 v84, v84\n" \
"v_exp_f32_e32 v85, v85\n" \
"v_cvt_pk_bf16_f32 v87, v86, v87\n" \
"buffer_load_dwordx4 a[100:103], v20, s[56:59], s60 offen offset:1024 sc1\n" \
"v_exp_f32_e32 v82, v82\n" \
"v_exp_f32_e32 v83, v83\n" \
"v_exp_f32_e32 v80, v80\n" \
"v_exp_f32_e32 v81, v81\n" \
"s_waitcnt lgkmcnt(15)\n" \
"v_mfma_f32_16x16x32_bf16 v[128:131], a[160:163], v[68:71], v[128:131]\n" \
"v_cvt_pk_bf16_f32 v86, v84, v85\n" \
"v_cvt_pk_bf16_f32 v85, v82, v83\n" \
"v_mfma_f32_16x16x32_bf16 v[160:163], a[160:163], v[72:75], v[160:163]\n" \
"v_cvt_pk_bf16_f32 v84, v80, v81\n" \
"v_and_b32_e32 v84, v12, v84\n" \
"v_mfma_f32_16x16x32_bf16 v[132:135], a[164:167], v[68:71], v[132:135]\n" \
"v_and_b32_e32 v85, v13, v85\n" \
"v_and_b32_e32 v86, v14, v86\n" \
"v_mfma_f32_16x16x32_bf16 v[164:167], a[164:167], v[72:75], v[164:167]\n" \
"buffer_load_dwordx4 a[104:107], v20, s[56:59], s60 offen offset:2048 sc1\n" \
"v_mfma_f32_16x16x32_bf16 v[136:139], a[168:171], v[68:71], v[136:139]\n" \
"v_cvt_pk_bf16_f32 v128, v128, v129\n" \
"v_cvt_pk_bf16_f32 v160, v160, v161\n" \
"v_mfma_f32_16x16x32_bf16 v[168:171], a[168:171], v[72:75], v[168:171]\n" \
"v_cvt_pk_bf16_f32 v129, v130, v131\n" \
"s_waitcnt lgkmcnt(10)\n" \
"v_mfma_f32_16x16x32_bf16 v[140:143], a[172:175], v[68:71], v[140:143]\n" \
"v_cvt_pk_bf16_f32 v161, v162, v163\n" \
"v_cvt_pk_bf16_f32 v130, v132, v133\n" \
"v_mfma_f32_16x16x32_bf16 v[172:175], a[172:175], v[72:75], v[172:175]\n" \
"v_cvt_pk_bf16_f32 v131, v134, v135\n" \
"v_cvt_pk_bf16_f32 v162, v164, v165\n" \
"v_mfma_f32_16x16x32_bf16 v[144:147], a[176:179], v[68:71], v[144:147]\n" \
"v_cvt_pk_bf16_f32 v163, v166, v167\n" \
"v_cvt_pk_bf16_f32 v132, v136, v137\n" \
"v_mfma_f32_16x16x32_bf16 v[176:179], a[176:179], v[72:75], v[176:179]\n" \
"buffer_load_dwordx4 a[108:111], v20, s[56:59], s60 offen offset:3072 sc1\n" \
"v_mfma_f32_16x16x32_bf16 v[148:151], a[180:183], v[68:71], v[148:151]\n" \
"v_cvt_pk_bf16_f32 v133, v138, v139\n" \
"v_cvt_pk_bf16_f32 v134, v140, v141\n" \
"v_mfma_f32_16x16x32_bf16 v[180:183], a[180:183], v[72:75], v[180:183]\n" \
"v_cvt_pk_bf16_f32 v135, v142, v143\n" \
"v_cvt_pk_bf16_f32 v164, v168, v169\n" \
"v_mfma_f32_16x16x32_bf16 v[152:155], a[184:187], v[68:71], v[152:155]\n" \
"v_cvt_pk_bf16_f32 v165, v170, v171\n" \
"v_cvt_pk_bf16_f32 v166, v172, v173\n" \
"v_mfma_f32_16x16x32_bf16 v[184:187], a[184:187], v[72:75], v[184:187]\n" \
"v_cvt_pk_bf16_f32 v167, v174, v175\n" \
"v_cvt_pk_bf16_f32 v136, v144, v145\n" \
"v_mfma_f32_16x16x32_bf16 v[156:159], a[188:191], v[68:71], v[156:159]\n" \
"v_cvt_pk_bf16_f32 v137, v146, v147\n" \
"v_cvt_pk_bf16_f32 v138, v148, v149\n" \
"v_mfma_f32_16x16x32_bf16 v[188:191], a[188:191], v[72:75], v[188:191]\n" \
"v_cvt_pk_bf16_f32 v139, v150, v151\n" \
"v_cvt_pk_bf16_f32 v140, v152, v153\n" \
"v_smfmac_f32_16x16x64_bf16 a[192:195], a[200:203], v[68:75], v21\n" \
"v_cvt_pk_bf16_f32 v141, v154, v155\n" \
"v_cvt_pk_bf16_f32 v168, v176, v177\n" \
"v_mfma_f32_16x16x32_bf16 v[120:123], a[128:131], a[112:115], v[44:47]\n" \
"v_cvt_pk_bf16_f32 v142, v156, v157\n" \
"v_cvt_pk_bf16_f32 v143, v158, v159\n" \
"v_mfma_f32_16x16x32_bf16 v[120:123], a[132:135], a[116:119], v[120:123]\n" \
"v_cvt_pk_bf16_f32 v169, v178, v179\n" \
"v_cvt_pk_bf16_f32 v170, v180, v181\n" \
"v_cvt_pk_bf16_f32 v171, v182, v183\n" \
"v_cvt_pk_bf16_f32 v172, v184, v185\n" \
"v_cvt_pk_bf16_f32 v173, v186, v187\n" \
"v_cvt_pk_bf16_f32 v174, v188, v189\n" \
"v_cvt_pk_bf16_f32 v175, v190, v191\n" \
"ds_write_b128 v50, v[128:131] offset:0\n" \
"v_mfma_f32_16x16x32_bf16 v[120:123], a[136:139], a[120:123], v[120:123]\n" \
"v_and_b32_e32 v87, v15, v87\n" \
"v_accvgpr_read_b32 v40, a192\n" \
"v_mfma_f32_16x16x32_bf16 v[120:123], a[140:143], a[124:127], v[120:123]\n" \
"ds_write_b128 v50, v[132:135] offset:1024\n" \
"v_mfma_f32_16x16x32_bf16 v[124:127], a[156:159], a[124:127], v[44:47]\n" \
"buffer_load_dwordx4 a[124:127], v54, s[56:59], s60 offen offset:3072 sc1\n" \
"v_mfma_f32_16x16x32_bf16 v[124:127], a[152:155], a[120:123], v[124:127]\n" \
"ds_write_b128 v50, v[136:139] offset:2048\n" \
"v_mfma_f32_16x16x32_bf16 v[124:127], a[148:151], a[116:119], v[124:127]\n" \
"v_accvgpr_read_b32 v22, a193\n" \
"v_accvgpr_read_b32 v41, a194\n" \
"v_mfma_f32_16x16x32_bf16 v[124:127], a[144:147], a[112:115], v[124:127]\n" \
"ds_write_b128 v50, v[140:143] offset:3072\n" \
"v_mfma_f32_16x16x32_bf16 a[196:199], a[204:207], v[84:87], a[196:199]\n" \
"v_accvgpr_read_b32 v23, a195\n" \
"v_add_f32_e32 v40, v40, v22\n" \
"v_mfma_f32_16x16x32_bf16 v[192:195], a[160:163], v[84:87], v[192:195]\n" \
"ds_write_b128 v51, v[160:163] offset:32768\n" \
"v_mfma_f32_16x16x32_bf16 v[196:199], a[164:167], v[84:87], v[196:199]\n" \
"buffer_load_dwordx4 a[112:115], v54, s[56:59], s60 offen offset:0 sc1\n" \
"v_mfma_f32_16x16x32_bf16 v[200:203], a[168:171], v[84:87], v[200:203]\n" \
"ds_write_b128 v51, v[164:167] offset:33792\n" \
"v_add_f32_e32 v41, v41, v23\n" \
"ds_write_b64 v19, v[40:41]\n" \
"v_mfma_f32_16x16x32_bf16 v[204:207], a[172:175], v[84:87], v[204:207]\n" \
"v_lshl_add_u32 v52, s15, 2, v20\n" \
"v_add_u32_e32 v52, 0x1a000, v52\n" \
"ds_write_b128 v51, v[168:171] offset:34816\n" \
"v_mfma_f32_16x16x32_bf16 v[208:211], a[176:179], v[84:87], v[208:211]\n" \
"v_exp_f32_e32 v88, v88\n" \
"v_mfma_f32_16x16x32_bf16 v[212:215], a[180:183], v[84:87], v[212:215]\n" \
"ds_write_b128 v51, v[172:175] offset:35840\n" \
"v_mfma_f32_16x16x32_bf16 v[216:219], a[184:187], v[84:87], v[216:219]\n" \
"buffer_load_dwordx4 a[116:119], v54, s[56:59], s60 offen offset:1024 sc1\n" \
"v_mfma_f32_16x16x32_bf16 v[220:223], a[188:191], v[84:87], v[220:223]\n" \
"v_exp_f32_e32 v89, v89\n" \
"v_exp_f32_e32 v90, v90\n" \
"v_exp_f32_e32 v91, v91\n" \
"v_cvt_pk_bf16_f32 v88, v88, v89\n" \
"v_exp_f32_e32 v92, v92\n" \
"v_exp_f32_e32 v93, v93\n" \
"v_exp_f32_e32 v94, v94\n" \
"v_exp_f32_e32 v95, v95\n" \
"v_cvt_pk_bf16_f32 v89, v90, v91\n" \
"v_cvt_pk_bf16_f32 v90, v92, v93\n" \
"v_cvt_pk_bf16_f32 v91, v94, v95\n" \
"v_xor_b32_e32 v63, 0x400, v19\n" \
"buffer_load_dwordx4 a[120:123], v54, s[56:59], s60 offen offset:2048 sc1\n" \
"v_exp_f32_e32 v120, v120\n" \
"v_exp_f32_e32 v121, v121\n" \
"v_exp_f32_e32 v122, v122\n" \
"v_exp_f32_e32 v123, v123\n" \
".fill (L_ab + 12288 - .) / 4, 4, 0xBF800000\n" \
"L_ap3:\n" \
"s_cbranch_execz L_ap0\n" \
"v_cvt_pk_bf16_f32 v120, v120, v121\n" \
"v_exp_f32_e32 v124, v124\n" \
"v_exp_f32_e32 v125, v125\n" \
"s_waitcnt lgkmcnt(0)\n" \
"s_barrier\n" \
"ds_read_b128 v[144:147], v52 offset:0\n" \
"ds_read_b128 v[148:151], v52 offset:1024\n" \
"ds_read_b128 v[152:155], v52 offset:2048\n" \
"ds_read_b128 v[156:159], v52 offset:3072\n" \
"ds_read_b128 v[176:179], v52 offset:4096\n" \
"ds_read_b128 v[180:183], v52 offset:5120\n" \
"ds_read_b128 v[184:187], v52 offset:6144\n" \
"ds_read_b128 v[188:191], v52 offset:7168\n" \
"v_mfma_f32_16x16x32_bf16 a[196:199], a[212:215], v[88:91], a[196:199]\n" \
"ds_read_b64 v[42:43], v63\n" \
"v_cvt_pk_bf16_f32 v121, v122, v123\n" \
"v_mfma_f32_16x16x32_bf16 v[224:227], a[160:163], v[88:91], v[224:227]\n" \
"v_exp_f32_e32 v126, v126\n" \
"v_mfma_f32_16x16x32_bf16 v[228:231], a[164:167], v[88:91], v[228:231]\n" \
"v_exp_f32_e32 v127, v127\n" \
"v_mfma_f32_16x16x32_bf16 v[232:235], a[168:171], v[88:91], v[232:235]\n" \
"v_cvt_pk_bf16_f32 v122, v124, v125\n" \
"v_cvt_pk_bf16_f32 v123, v126, v127\n" \
"v_mfma_f32_16x16x32_bf16 v[236:239], a[172:175], v[88:91], v[236:239]\n" \
"v_and_b32_e32 v120, v12, v120\n" \
"v_and_b32_e32 v121, v13, v121\n" \
"v_mfma_f32_16x16x32_bf16 v[240:243], a[176:179], v[88:91], v[240:243]\n" \
"v_and_b32_e32 v122, v14, v122\n" \
"v_and_b32_e32 v123, v15, v123\n" \
"v_mfma_f32_16x16x32_bf16 v[244:247], a[180:183], v[88:91], v[244:247]\n" \
"v_readlane_b32 s93, v11, 22\n" \
"v_readlane_b32 s92, v11, 27\n" \
"v_mfma_f32_16x16x32_bf16 v[248:251], a[184:187], v[88:91], v[248:251]\n" \
"s_add_u32 m0, s93, 0x0\n" \
"v_mov_b32_e32 v130, v28\n" \
"v_mfma_f32_16x16x32_bf16 v[252:255], a[188:191], v[88:91], v[252:255]\n" \
"v_mov_b32_e32 v131, v29\n" \
"v_mov_b32_e32 v132, 0\n" \
"v_mov_b32_e32 v133, 0\n" \
"v_mov_b32_e32 v128, 0\n" \
"v_mov_b32_e32 v129, 0\n" \
"s_waitcnt vmcnt(8) lgkmcnt(1)\n" \
"s_barrier\n" \
"v_mfma_f32_16x16x32_bf16 a[196:199], a[212:215], v[120:123], a[196:199]\n" \
"buffer_load_dwordx4 v0, s[80:83], s92 offen offset:0 lds\n" \
"v_mfma_f32_16x16x32_bf16 v[224:227], a[0:3], v[120:123], v[224:227]\n" \
"v_mfma_f32_16x16x32_bf16 v[228:231], a[4:7], v[120:123], v[228:231]\n" \
"v_mfma_f32_16x16x32_bf16 v[232:235], a[8:11], v[120:123], v[232:235]\n" \
"v_mfma_f32_16x16x32_bf16 v[236:239], a[12:15], v[120:123], v[236:239]\n" \
"v_mfma_f32_16x16x32_bf16 v[240:243], a[16:19], v[120:123], v[240:243]\n" \
"v_mfma_f32_16x16x32_bf16 v[244:247], a[20:23], v[120:123], v[244:247]\n" \
"buffer_load_dwordx4 v1, s[80:83], s92 offen offset:1024 lds\n" \
"v_mfma_f32_16x16x32_bf16 v[248:251], a[24:27], v[120:123], v[248:251]\n" \
"v_accvgpr_read_b32 v56, a196\n" \
"v_accvgpr_read_b32 v24, a197\n" \
"v_mfma_f32_16x16x32_bf16 v[252:255], a[28:31], v[120:123], v[252:255]\n" \
"v_add_f32_e32 v56, v56, v24\n" \
"s_waitcnt lgkmcnt(0)\n" \
"v_add_f32_e32 v56, v56, v42\n" \
"v_mfma_f32_16x16x32_bf16 v[192:195], v[130:133], v[144:147], v[192:195]\n" \
"v_max_f32_e32 v62, v62, v56\n" \
"v_accvgpr_read_b32 v57, a198\n" \
"v_mfma_f32_16x16x32_bf16 v[196:199], v[128:131], v[144:147], v[196:199]\n" \
"v_rcp_f32_e32 v56, v56\n" \
"v_mfma_f32_16x16x32_bf16 v[200:203], v[130:133], v[148:151], v[200:203]\n" \
"v_mul_f32_e32 v56, v56, v60\n" \
"v_accvgpr_read_b32 v25, a199\n" \
"buffer_load_dwordx4 v2, s[80:83], s92 offen offset:2048 lds\n" \
"v_mfma_f32_16x16x32_bf16 v[204:207], v[128:131], v[148:151], v[204:207]\n" \
"v_mul_f32_e32 v192, v56, v192\n" \
"v_mul_f32_e32 v193, v56, v193\n" \
"v_mfma_f32_16x16x32_bf16 v[208:211], v[130:133], v[152:155], v[208:211]\n" \
"v_mul_f32_e32 v194, v56, v194\n" \
"v_mul_f32_e32 v195, v56, v195\n" \
"v_mfma_f32_16x16x32_bf16 v[212:215], v[128:131], v[152:155], v[212:215]\n" \
"v_mul_f32_e32 v196, v56, v196\n" \
"v_mul_f32_e32 v197, v56, v197\n" \
"v_mfma_f32_16x16x32_bf16 v[216:219], v[130:133], v[156:159], v[216:219]\n" \
"v_mul_f32_e32 v198, v56, v198\n" \
"v_mul_f32_e32 v199, v56, v199\n" \
"v_mfma_f32_16x16x32_bf16 v[220:223], v[128:131], v[156:159], v[220:223]\n" \
"v_cvt_pk_bf16_f32 v64, v192, v193\n" \
"v_cvt_pk_bf16_f32 v65, v194, v195\n" \
"v_mfma_f32_16x16x32_bf16 v[224:227], v[130:133], v[176:179], v[224:227]\n" \
"buffer_load_dwordx4 v3, s[80:83], s92 offen offset:3072 lds\n" \
"s_add_u32 m0, s93, 0x1000\n" \
"v_mfma_f32_16x16x32_bf16 v[228:231], v[128:131], v[176:179], v[228:231]\n" \
"v_cvt_pk_bf16_f32 v66, v196, v197\n" \
"v_cvt_pk_bf16_f32 v67, v198, v199\n" \
"v_mfma_f32_16x16x32_bf16 v[232:235], v[130:133], v[180:183], v[232:235]\n" \
"v_mul_f32_e32 v200, v56, v200\n" \
"v_mul_f32_e32 v201, v56, v201\n" \
"v_mfma_f32_16x16x32_bf16 v[236:239], v[128:131], v[180:183], v[236:239]\n" \
"v_mfma_f32_16x16x32_bf16 a[208:211], a[216:219], v[64:67], a[208:211]\n" \
"v_mul_f32_e32 v202, v56, v202\n" \
"v_mul_f32_e32 v203, v56, v203\n" \
"v_mfma_f32_16x16x32_bf16 v[240:243], v[130:133], v[184:187], v[240:243]\n" \
"v_mul_f32_e32 v204, v56, v204\n" \
"v_mul_f32_e32 v205, v56, v205\n" \
"buffer_load_dwordx4 v4, s[80:83], s92 offen offset:0 lds\n" \
"v_mul_f32_e32 v206, v56, v206\n" \
"v_mul_f32_e32 v207, v56, v207\n" \
"v_add_f32_e32 v57, v57, v25\n" \
"v_cvt_pk_bf16_f32 v68, v200, v201\n" \
"v_cvt_pk_bf16_f32 v69, v202, v203\n" \
"v_cvt_pk_bf16_f32 v70, v204, v205\n" \
"v_cvt_pk_bf16_f32 v71, v206, v207\n" \
"v_add_f32_e32 v57, v57, v43\n" \
"v_mul_f32_e32 v208, v56, v208\n" \
"v_mfma_f32_16x16x32_bf16 a[208:211], a[216:219], v[68:71], a[208:211]\n" \
"v_mul_f32_e32 v209, v56, v209\n" \
"v_mul_f32_e32 v210, v56, v210\n" \
"v_mul_f32_e32 v211, v56, v211\n" \
"v_mul_f32_e32 v212, v56, v212\n" \
"v_mul_f32_e32 v213, v56, v213\n" \
"v_mul_f32_e32 v214, v56, v214\n" \
"v_mul_f32_e32 v215, v56, v215\n" \
"v_max_f32_e32 v62, v62, v57\n" \
"v_cvt_pk_bf16_f32 v72, v208, v209\n" \
"v_cvt_pk_bf16_f32 v73, v210, v211\n" \
"buffer_load_dwordx4 v5, s[80:83], s92 offen offset:1024 lds\n" \
"v_cvt_pk_bf16_f32 v74, v212, v213\n" \
"v_cvt_pk_bf16_f32 v75, v214, v215\n" \
"v_rcp_f32_e32 v57, v57\n" \
"v_mfma_f32_16x16x32_bf16 v[244:247], v[128:131], v[184:187], v[244:247]\n" \
"v_mfma_f32_16x16x32_bf16 a[208:211], a[216:219], v[72:75], a[208:211]\n" \
"v_mul_f32_e32 v216, v56, v216\n" \
"v_mul_f32_e32 v217, v56, v217\n" \
"v_mul_f32_e32 v218, v56, v218\n" \
"v_mul_f32_e32 v219, v56, v219\n" \
"v_mul_f32_e32 v220, v56, v220\n" \
"v_mul_f32_e32 v221, v56, v221\n" \
"v_mul_f32_e32 v222, v56, v222\n" \
"v_mul_f32_e32 v223, v56, v223\n" \
"v_cvt_pk_bf16_f32 v76, v216, v217\n" \
"v_cvt_pk_bf16_f32 v77, v218, v219\n" \
"v_cvt_pk_bf16_f32 v78, v220, v221\n" \
"buffer_load_dwordx4 v6, s[80:83], s92 offen offset:2048 lds\n" \
"v_cvt_pk_bf16_f32 v79, v222, v223\n" \
"v_mul_f32_e32 v57, v57, v61\n" \
"v_mul_f32_e32 v224, v57, v224\n" \
"v_mfma_f32_16x16x32_bf16 a[208:211], a[216:219], v[76:79], a[208:211]\n" \
"v_mul_f32_e32 v225, v57, v225\n" \
"v_mul_f32_e32 v226, v57, v226\n" \
"v_mul_f32_e32 v227, v57, v227\n" \
"v_mul_f32_e32 v228, v57, v228\n" \
"v_mul_f32_e32 v229, v57, v229\n" \
"v_mul_f32_e32 v230, v57, v230\n" \
"v_mul_f32_e32 v231, v57, v231\n" \
"v_cvt_pk_bf16_f32 v80, v224, v225\n" \
"v_cvt_pk_bf16_f32 v81, v226, v227\n" \
"v_cvt_pk_bf16_f32 v82, v228, v229\n" \
"v_cvt_pk_bf16_f32 v83, v230, v231\n" \
"v_mfma_f32_16x16x32_bf16 v[248:251], v[130:133], v[188:191], v[248:251]\n" \
"v_mul_f32_e32 v232, v57, v232\n" \
"v_mul_f32_e32 v233, v57, v233\n" \
"v_mfma_f32_16x16x32_bf16 a[208:211], a[216:219], v[80:83], a[208:211]\n" \
"buffer_load_dwordx4 v7, s[80:83], s92 offen offset:3072 lds\n" \
"s_add_u32 m0, s93, 0x2000\n" \
"v_mul_f32_e32 v234, v57, v234\n" \
"v_mul_f32_e32 v235, v57, v235\n" \
"v_mul_f32_e32 v236, v57, v236\n" \
"v_mul_f32_e32 v237, v57, v237\n" \
"v_mul_f32_e32 v238, v57, v238\n" \
"v_mul_f32_e32 v239, v57, v239\n" \
"v_cvt_pk_bf16_f32 v84, v232, v233\n" \
"v_cvt_pk_bf16_f32 v85, v234, v235\n" \
"v_cvt_pk_bf16_f32 v86, v236, v237\n" \
"v_cvt_pk_bf16_f32 v87, v238, v239\n" \
"v_mul_f32_e32 v240, v57, v240\n" \
"v_mul_f32_e32 v241, v57, v241\n" \
"v_mfma_f32_16x16x32_bf16 a[208:211], a[216:219], v[84:87], a[208:211]\n" \
"v_mul_f32_e32 v242, v57, v242\n" \
"v_mul_f32_e32 v243, v57, v243\n" \
"v_mul_f32_e32 v244, v57, v244\n" \
"v_mul_f32_e32 v245, v57, v245\n" \
"v_mul_f32_e32 v246, v57, v246\n" \
"v_mul_f32_e32 v247, v57, v247\n" \
"buffer_load_dwordx4 v8, s[80:83], s92 offen offset:0 lds\n" \
"v_cvt_pk_bf16_f32 v96, v240, v241\n" \
"v_cvt_pk_bf16_f32 v97, v242, v243\n" \
"v_cvt_pk_bf16_f32 v98, v244, v245\n" \
"v_cvt_pk_bf16_f32 v99, v246, v247\n" \
"v_mfma_f32_16x16x32_bf16 v[252:255], v[128:131], v[188:191], v[252:255]\n" \
"v_mul_f32_e32 v248, v57, v248\n" \
"v_mfma_f32_16x16x32_bf16 a[208:211], a[216:219], v[96:99], a[208:211]\n" \
"v_mul_f32_e32 v249, v57, v249\n" \
"v_mul_f32_e32 v250, v57, v250\n" \
"v_mul_f32_e32 v251, v57, v251\n" \
"v_cvt_pk_bf16_f32 v100, v248, v249\n" \
"v_cvt_pk_bf16_f32 v101, v250, v251\n" \
"v_cmp_lt_f32_e64 s[72:73], s21, v62\n" \
"v_mul_f32_e32 v252, v57, v252\n" \
"v_mul_f32_e32 v253, v57, v253\n" \
"v_mul_f32_e32 v254, v57, v254\n" \
"v_mul_f32_e32 v255, v57, v255\n" \
"v_cvt_pk_bf16_f32 v102, v252, v253\n" \
"v_cvt_pk_bf16_f32 v103, v254, v255\n" \
"s_nop 1\n" \
"v_mfma_f32_16x16x32_bf16 a[208:211], a[216:219], v[100:103], a[208:211]\n" \
"s_nop 7\n" \
"v_accvgpr_read_b32 v63, a208\n" \
"v_cmp_u_f32_e32 vcc, v63, v63\n" \
"s_nop 0\n" \
"s_or_b64 vcc, s[72:73], vcc\n" \
"s_cbranch_scc0 L_out_%=\n" \
"s_branch L_end_%=\n" \
"L_skb_%=:\n" \
"s_sleep 1\n" \
"buffer_load_dword v57, v61, s[56:59], s96 offen sc1\n" \
"s_add_u32 s101, s101, 1\n" \
"s_waitcnt vmcnt(0)\n" \
"v_cmp_ne_u32_e32 vcc, s62, v57\n" \
"s_cmp_gt_u32 s101, 0x100000\n" \
"s_cbranch_scc1 L_rkb_%=\n" \
"s_nop 2\n" \
"s_cbranch_vccnz L_skb_%=\n" \
"s_branch L_rkb_%=\n" \
"L_sol_%=:\n" \
"s_sleep 1\n" \
"buffer_load_dword v26, v27, s[56:59], s96 offen sc1\n" \
"s_add_u32 s101, s101, 1\n" \
"s_waitcnt vmcnt(0)\n" \
"v_cmp_ne_u32_e32 vcc, s62, v26\n" \
"s_cmp_gt_u32 s101, 0x100000\n" \
"s_cbranch_scc1 L_rol_%=\n" \
"s_nop 2\n" \
"s_cbranch_vccnz L_sol_%=\n" \
"s_branch L_rol_%=\n" \
"L_sos_%=:\n" \
"s_sleep 1\n" \
"buffer_load_dword v26, v27, s[56:59], s96 offen sc1\n" \
"s_add_u32 s101, s101, 1\n" \
"s_waitcnt vmcnt(0)\n" \
"v_cmp_ne_u32_e32 vcc, s62, v26\n" \
"s_cmp_gt_u32 s101, 0x100000\n" \
"s_cbranch_scc1 L_ros_%=\n" \
"s_nop 2\n" \
"s_cbranch_vccnz L_sos_%=\n" \
"s_branch L_ros_%=\n" \
"L_sa_%=:\n" \
"s_sleep 1\n" \
"buffer_load_dword v26, v38, s[56:59], s64 offen sc1\n" \
"s_add_u32 s101, s101, 1\n" \
"s_waitcnt vmcnt(0)\n" \
"v_cmp_ne_u32_e32 vcc, s62, v26\n" \
"s_cmp_gt_u32 s101, 0x100000\n" \
"s_cbranch_scc1 L_ra_%=\n" \
"s_nop 2\n" \
"s_cbranch_vccnz L_sa_%=\n" \
"s_branch L_ra_%=\n" \
"L_sb_%=:\n" \
"s_sleep 1\n" \
"buffer_load_dword v47, v38, s[56:59], s71 offen sc1\n" \
"s_add_u32 s101, s101, 1\n" \
"s_waitcnt vmcnt(0)\n" \
"v_cmp_ne_u32_e32 vcc, s62, v47\n" \
"s_cmp_gt_u32 s101, 0x100000\n" \
"s_cbranch_scc1 L_rb_%=\n" \
"s_nop 2\n" \
"s_cbranch_vccnz L_sb_%=\n" \
"s_branch L_rb_%=\n" \
"L_sna_%=:\n" \
"buffer_load_dword v37, v40, s[56:59], s74 offen sc1\n" \
"s_add_u32 s101, s101, 1\n" \
"s_waitcnt vmcnt(0)\n" \
"v_cmp_eq_u32_e64 s[98:99], s62, v37\n" \
"s_cmp_gt_u32 s101, 0x100000\n" \
"s_cbranch_scc1 L_rna_%=\n" \
"s_andn2_b32 s75, s20, s98\n" \
"s_cbranch_scc0 L_gna_%=\n" \
"s_sleep 1\n" \
"s_branch L_sna_%=\n" \
"L_gna_%=:\n" \
"s_bitcmp1_b32 s20, 0\n" \
"s_cbranch_scc0 L_kna0_%=\n" \
"buffer_load_dwordx4 v[116:119], v20, s[56:59], s12 offen offset:0 sc1\n" \
"L_kna0_%=:\n" \
"s_bitcmp1_b32 s20, 1\n" \
"s_cbranch_scc0 L_kna1_%=\n" \
"buffer_load_dwordx4 v[124:127], v20, s[56:59], s12 offen offset:2048 sc1\n" \
"L_kna1_%=:\n" \
"s_bitcmp1_b32 s20, 2\n" \
"s_cbranch_scc0 L_kna2_%=\n" \
"buffer_load_dwordx4 v[132:135], v20, s[56:59], s13 offen offset:0 sc1\n" \
"L_kna2_%=:\n" \
"s_bitcmp1_b32 s20, 3\n" \
"s_cbranch_scc0 L_kna3_%=\n" \
"buffer_load_dwordx4 v[140:143], v20, s[56:59], s13 offen offset:2048 sc1\n" \
"L_kna3_%=:\n" \
"s_bitcmp1_b32 s20, 4\n" \
"s_cbranch_scc0 L_kna4_%=\n" \
"buffer_load_dwordx4 v[148:151], v20, s[56:59], s14 offen offset:0 sc1\n" \
"L_kna4_%=:\n" \
"s_bitcmp1_b32 s20, 5\n" \
"s_cbranch_scc0 L_kna5_%=\n" \
"buffer_load_dwordx4 v[156:159], v20, s[56:59], s14 offen offset:2048 sc1\n" \
"L_kna5_%=:\n" \
"s_bitcmp1_b32 s20, 6\n" \
"s_cbranch_scc0 L_kna6_%=\n" \
"buffer_load_dwordx4 v[164:167], v20, s[56:59], s15 offen offset:0 sc1\n" \
"L_kna6_%=:\n" \
"s_waitcnt vmcnt(0)\n" \
"s_branch L_rna_%=\n" \
"L_snx_%=:\n" \
"buffer_load_dword v37, v40, s[56:59], s74 offen sc1\n" \
"s_add_u32 s101, s101, 1\n" \
"s_waitcnt vmcnt(0)\n" \
"v_cmp_eq_u32_e64 s[98:99], s62, v37\n" \
"s_cmp_gt_u32 s101, 0x100000\n" \
"s_cbranch_scc1 L_rnx_%=\n" \
"s_andn2_b32 s75, s21, s98\n" \
"s_cbranch_scc0 L_gnx_%=\n" \
"s_sleep 1\n" \
"s_branch L_snx_%=\n" \
"L_gnx_%=:\n" \
"s_bitcmp1_b32 s21, 16\n" \
"s_cbranch_scc0 L_knx16_%=\n" \
"buffer_load_dwordx4 v[112:115], v20, s[56:59], s16 offen offset:0 sc1\n" \
"L_knx16_%=:\n" \
"s_bitcmp1_b32 s21, 17\n" \
"s_cbranch_scc0 L_knx17_%=\n" \
"buffer_load_dwordx4 v[184:187], v20, s[56:59], s16 offen offset:2048 sc1\n" \
"L_knx17_%=:\n" \
"s_bitcmp1_b32 s21, 18\n" \
"s_cbranch_scc0 L_knx18_%=\n" \
"buffer_load_dwordx4 v[120:123], v20, s[56:59], s17 offen offset:0 sc1\n" \
"L_knx18_%=:\n" \
"s_bitcmp1_b32 s21, 19\n" \
"s_cbranch_scc0 L_knx19_%=\n" \
"buffer_load_dwordx4 v[192:195], v20, s[56:59], s17 offen offset:2048 sc1\n" \
"L_knx19_%=:\n" \
"s_bitcmp1_b32 s21, 20\n" \
"s_cbranch_scc0 L_knx20_%=\n" \
"buffer_load_dwordx4 v[128:131], v20, s[56:59], s18 offen offset:0 sc1\n" \
"L_knx20_%=:\n" \
"s_bitcmp1_b32 s21, 21\n" \
"s_cbranch_scc0 L_knx21_%=\n" \
"buffer_load_dwordx4 v[200:203], v20, s[56:59], s18 offen offset:2048 sc1\n" \
"L_knx21_%=:\n" \
"s_bitcmp1_b32 s21, 22\n" \
"s_cbranch_scc0 L_knx22_%=\n" \
"buffer_load_dwordx4 v[176:179], v20, s[56:59], s19 offen offset:0 sc1\n" \
"L_knx22_%=:\n" \
"s_bitcmp1_b32 s21, 23\n" \
"s_cbranch_scc0 L_knx23_%=\n" \
"buffer_load_dwordx4 v[248:251], v20, s[56:59], s19 offen offset:2048 sc1\n" \
"L_knx23_%=:\n" \
"s_bitcmp1_b32 s21, 8\n" \
"s_cbranch_scc0 L_knx8_%=\n" \
"buffer_load_dwordx4 v[188:191], v20, s[56:59], s12 offen offset:1024 sc1\n" \
"L_knx8_%=:\n" \
"s_bitcmp1_b32 s21, 9\n" \
"s_cbranch_scc0 L_knx9_%=\n" \
"buffer_load_dwordx4 v[196:199], v20, s[56:59], s12 offen offset:3072 sc1\n" \
"L_knx9_%=:\n" \
"s_waitcnt vmcnt(0)\n" \
"s_branch L_rnx_%=\n" \
"L_sx0_%=:\n" \
"s_sleep 1\n" \
"buffer_load_dword v37, v40, s[56:59], s74 offen sc1\n" \
"s_add_u32 s101, s101, 1\n" \
"s_waitcnt vmcnt(0)\n" \
"v_cmp_eq_u32_e64 s[98:99], s62, v37\n" \
"s_cmp_gt_u32 s101, 0x100000\n" \
"s_cbranch_scc1 L_rx0_%=\n" \
"s_andn2_b32 s75, 0x400, s98\n" \
"s_cbranch_scc1 L_sx0_%=\n" \
"s_branch L_rx0_%=\n" \
"L_sx1_%=:\n" \
"s_sleep 1\n" \
"buffer_load_dword v37, v40, s[56:59], s74 offen sc1\n" \
"s_add_u32 s101, s101, 1\n" \
"s_waitcnt vmcnt(0)\n" \
"v_cmp_eq_u32_e64 s[98:99], s62, v37\n" \
"s_cmp_gt_u32 s101, 0x100000\n" \
"s_cbranch_scc1 L_rx1_%=\n" \
"s_andn2_b32 s75, 0x800, s98\n" \
"s_cbranch_scc1 L_sx1_%=\n" \
"s_branch L_rx1_%=\n" \
"L_sx2_%=:\n" \
"s_sleep 1\n" \
"buffer_load_dword v37, v40, s[56:59], s74 offen sc1\n" \
"s_add_u32 s101, s101, 1\n" \
"s_waitcnt vmcnt(0)\n" \
"v_cmp_eq_u32_e64 s[98:99], s62, v37\n" \
"s_cmp_gt_u32 s101, 0x100000\n" \
"s_cbranch_scc1 L_rx2_%=\n" \
"s_andn2_b32 s75, 0x1000, s98\n" \
"s_cbranch_scc1 L_sx2_%=\n" \
"s_branch L_rx2_%=\n" \
"L_sx3_%=:\n" \
"s_sleep 1\n" \
"buffer_load_dword v37, v40, s[56:59], s74 offen sc1\n" \
"s_add_u32 s101, s101, 1\n" \
"s_waitcnt vmcnt(0)\n" \
"v_cmp_eq_u32_e64 s[98:99], s62, v37\n" \
"s_cmp_gt_u32 s101, 0x100000\n" \
"s_cbranch_scc1 L_rx3_%=\n" \
"s_andn2_b32 s75, 0x2000, s98\n" \
"s_cbranch_scc1 L_sx3_%=\n" \
"s_branch L_rx3_%=\n" \
"L_sx4_%=:\n" \
"s_sleep 1\n" \
"buffer_load_dword v37, v40, s[56:59], s74 offen sc1\n" \
"s_add_u32 s101, s101, 1\n" \
"s_waitcnt vmcnt(0)\n" \
"v_cmp_eq_u32_e64 s[98:99], s62, v37\n" \
"s_cmp_gt_u32 s101, 0x100000\n" \
"s_cbranch_scc1 L_rx4_%=\n" \
"s_andn2_b32 s75, 0x4000, s98\n" \
"s_cbranch_scc1 L_sx4_%=\n" \
"s_branch L_rx4_%=\n" \
"L_att_stub:\n" \
"s_mov_b32 s101, 0\n" \
"v_add_u32_e32 v32, 0x49000, v20\n" \
"v_readlane_b32 s96, v11, 36\n" \
"v_readlane_b32 s97, v11, 37\n" \
"buffer_load_dwordx4 a[96:99], v20, s[56:59], s60 offen offset:0 sc1\n" \
"buffer_load_dwordx4 a[112:115], v32, s[56:59], s60 offen offset:0 sc1\n" \
"buffer_load_dwordx4 a[100:103], v20, s[56:59], s60 offen offset:1024 sc1\n" \
"buffer_load_dwordx4 a[116:119], v32, s[56:59], s60 offen offset:1024 sc1\n" \
"buffer_load_dwordx4 a[104:107], v20, s[56:59], s60 offen offset:2048 sc1\n" \
"buffer_load_dwordx4 a[120:123], v32, s[56:59], s60 offen offset:2048 sc1\n" \
"buffer_load_dwordx4 a[108:111], v20, s[56:59], s60 offen offset:3072 sc1\n" \
"buffer_load_dwordx4 a[124:127], v32, s[56:59], s60 offen offset:3072 sc1\n" \
"buffer_load_dwordx4 a[220:223], v20, s[56:59], s96 offen offset:0 sc1\n" \
"buffer_load_dwordx4 a[228:231], v32, s[56:59], s96 offen offset:0 sc1\n" \
"buffer_load_dwordx4 a[236:239], v20, s[56:59], s97 offen offset:0 sc1\n" \
"buffer_load_dwordx4 a[244:247], v32, s[56:59], s97 offen offset:0 sc1\n" \
"buffer_load_dwordx4 a[224:227], v20, s[56:59], s96 offen offset:1024 sc1\n" \
"buffer_load_dwordx4 a[232:235], v32, s[56:59], s96 offen offset:1024 sc1\n" \
"buffer_load_dwordx4 a[240:243], v20, s[56:59], s97 offen offset:1024 sc1\n" \
"buffer_load_dwordx4 a[248:251], v32, s[56:59], s97 offen offset:1024 sc1\n" \
"buffer_load_dwordx4 v[64:67], v20, s[56:59], s61 offen offset:0 sc1\n" \
"buffer_load_dwordx4 v[68:71], v20, s[56:59], s61 offen offset:1024 sc1\n" \
"buffer_load_dwordx4 v[72:75], v20, s[56:59], s61 offen offset:2048 sc1\n" \
"buffer_load_dwordx4 v[76:79], v20, s[56:59], s61 offen offset:3072 sc1\n" \
"buffer_load_dwordx4 v[80:83], v32, s[56:59], s61 offen offset:0 sc1\n" \
"buffer_load_dwordx4 v[84:87], v32, s[56:59], s61 offen offset:1024 sc1\n" \
"buffer_load_dwordx4 v[96:99], v32, s[56:59], s61 offen offset:2048 sc1\n" \
"buffer_load_dwordx4 v[100:103], v32, s[56:59], s61 offen offset:3072 sc1\n" \
"v_not_b32_e32 v26, s62\n" \
"s_waitcnt vmcnt(0)\n" \
"s_branch L_out_%=\n" \
".fill (L_ab + 16384 - .) / 4, 4, 0xBF800000\n" \
".fill (L_ab + 16384 - .) / 4, 4, 0xBF800000\n" \
"L_ap4:\n" \
"s_setpc_b64 s[88:89]\n" \
"L_gsw:\n" \
"v_mfma_f32_16x16x32_bf16 v[104:107], a[128:131], a[16:19], v[36:39]\n" \
"s_add_u32 s68, s60, 0xfff6e000\n" \
"s_add_u32 s69, s60, 0xfffb7000\n" \
"s_add_u32 s70, s60, 0x0\n" \
"s_add_u32 s71, s60, 0x49000\n" \
"v_mfma_f32_16x16x32_bf16 v[104:107], a[132:135], a[20:23], v[104:107]\n" \
"s_xor_b32 m0, s65, 0x12000\n" \
"ds_read_b128 a[160:163], v49 offset:0\n" \
"v_mfma_f32_16x16x32_bf16 v[104:107], a[136:139], a[24:27], v[104:107]\n" \
"buffer_load_dwordx4 v50, s[8:11], s64 offen offset:0 sc1 lds\n" \
"v_mfma_f32_16x16x32_bf16 v[104:107], a[140:143], a[28:31], v[104:107]\n" \
"ds_read_b128 a[164:167], v49 offset:1024\n" \
"ds_read_b128 a[168:171], v49 offset:2048\n" \
"v_mfma_f32_16x16x32_bf16 v[108:111], a[156:159], a[28:31], v[36:39]\n" \
"ds_read_b128 a[172:175], v49 offset:3072\n" \
"ds_read_b128 a[176:179], v49 offset:4096\n" \
"v_mfma_f32_16x16x32_bf16 v[108:111], a[152:155], a[24:27], v[108:111]\n" \
"buffer_load_dwordx4 v50, s[8:11], s64 offen offset:1024 sc1 lds\n" \
"v_mfma_f32_16x16x32_bf16 v[108:111], a[148:151], a[20:23], v[108:111]\n" \
"s_mov_b32 s65, s13\n" \
"v_add_u32_e32 v63, s15, v20\n" \
"v_mfma_f32_16x16x32_bf16 v[108:111], a[144:147], a[16:19], v[108:111]\n" \
"s_add_u32 s67, s14, 40960\n" \
"v_add_u32_e32 v50, 0x6000, v63\n" \
"v_mfma_f32_16x16x32_bf16 v[116:119], a[144:147], a[32:35], v[40:43]\n" \
"s_xor_b32 m0, s65, 0x8000\n" \
"ds_read_b128 a[180:183], v49 offset:5120\n" \
"v_mfma_f32_16x16x32_bf16 v[116:119], a[148:151], a[36:39], v[116:119]\n" \
"ds_read_b128 a[184:187], v49 offset:6144\n" \
"v_and_b32_e32 v68, v12, v68\n" \
"v_mfma_f32_16x16x32_bf16 v[116:119], a[152:155], a[40:43], v[116:119]\n" \
"v_and_b32_e32 v69, v13, v69\n" \
"v_and_b32_e32 v70, v14, v70\n" \
"v_mfma_f32_16x16x32_bf16 v[116:119], a[156:159], a[44:47], v[116:119]\n" \
"buffer_load_dwordx4 v50, s[8:11], s67 offen offset:0 sc1 lds\n" \
"v_mfma_f32_16x16x32_bf16 v[112:115], a[140:143], a[44:47], v[40:43]\n" \
"v_and_b32_e32 v71, v15, v71\n" \
"ds_read_b128 a[188:191], v49 offset:7168\n" \
"v_mfma_f32_16x16x32_bf16 v[112:115], a[136:139], a[40:43], v[112:115]\n" \
"v_exp_f32_e32 v86, v86\n" \
"v_mfma_f32_16x16x32_bf16 v[112:115], a[132:135], a[36:39], v[112:115]\n" \
"v_exp_f32_e32 v87, v87\n" \
"v_mfma_f32_16x16x32_bf16 v[112:115], a[128:131], a[32:35], v[112:115]\n" \
"v_exp_f32_e32 v84, v84\n" \
"v_mfma_f32_16x16x32_bf16 v[120:123], a[128:131], a[48:51], v[44:47]\n" \
"buffer_load_dwordx4 v50, s[8:11], s67 offen offset:1024 sc1 lds\n" \
"v_mfma_f32_16x16x32_bf16 v[120:123], a[132:135], a[52:55], v[120:123]\n" \
"v_exp_f32_e32 v85, v85\n" \
"v_mfma_f32_16x16x32_bf16 v[120:123], a[136:139], a[56:59], v[120:123]\n" \
"v_cvt_pk_bf16_f32 v87, v86, v87\n" \
"v_cvt_pk_bf16_f32 v86, v84, v85\n" \
"v_mfma_f32_16x16x32_bf16 v[120:123], a[140:143], a[60:63], v[120:123]\n" \
"v_exp_f32_e32 v82, v82\n" \
"v_mfma_f32_16x16x32_bf16 v[124:127], a[156:159], a[60:63], v[44:47]\n" \
"v_exp_f32_e32 v83, v83\n" \
"v_mfma_f32_16x16x32_bf16 v[124:127], a[152:155], a[56:59], v[124:127]\n" \
"v_exp_f32_e32 v80, v80\n" \
"v_mfma_f32_16x16x32_bf16 v[124:127], a[148:151], a[52:55], v[124:127]\n" \
"buffer_load_dwordx4 v50, s[8:11], s67 offen offset:2048 sc1 lds\n" \
"v_mfma_f32_16x16x32_bf16 v[124:127], a[144:147], a[48:51], v[124:127]\n" \
"v_cvt_pk_bf16_f32 v85, v82, v83\n" \
"s_waitcnt lgkmcnt(1)\n" \
"v_mfma_f32_16x16x32_bf16 v[128:131], a[160:163], v[68:71], v[128:131]\n" \
"v_exp_f32_e32 v81, v81\n" \
"v_mfma_f32_16x16x32_bf16 v[160:163], a[160:163], v[72:75], v[160:163]\n" \
"v_cvt_pk_bf16_f32 v84, v80, v81\n" \
"s_mov_b32 s64, s14\n" \
"v_mfma_f32_16x16x32_bf16 v[132:135], a[164:167], v[68:71], v[132:135]\n" \
"v_exp_f32_e32 v88, v88\n" \
"v_mfma_f32_16x16x32_bf16 v[164:167], a[164:167], v[72:75], v[164:167]\n" \
"v_exp_f32_e32 v89, v89\n" \
"v_mfma_f32_16x16x32_bf16 v[136:139], a[168:171], v[68:71], v[136:139]\n" \
"buffer_load_dwordx4 v50, s[8:11], s67 offen offset:3072 sc1 lds\n" \
"v_mfma_f32_16x16x32_bf16 v[168:171], a[168:171], v[72:75], v[168:171]\n" \
"v_exp_f32_e32 v90, v90\n" \
"v_mfma_f32_16x16x32_bf16 v[140:143], a[172:175], v[68:71], v[140:143]\n" \
"v_exp_f32_e32 v91, v91\n" \
"v_mfma_f32_16x16x32_bf16 v[172:175], a[172:175], v[72:75], v[172:175]\n" \
"v_cvt_pk_bf16_f32 v88, v88, v89\n" \
"v_cvt_pk_bf16_f32 v89, v90, v91\n" \
"v_mfma_f32_16x16x32_bf16 v[144:147], a[176:179], v[68:71], v[144:147]\n" \
"v_exp_f32_e32 v92, v92\n" \
"v_mfma_f32_16x16x32_bf16 v[176:179], a[176:179], v[72:75], v[176:179]\n" \
"v_exp_f32_e32 v93, v93\n" \
"v_mfma_f32_16x16x32_bf16 v[148:151], a[180:183], v[68:71], v[148:151]\n" \
"v_exp_f32_e32 v94, v94\n" \
"v_mfma_f32_16x16x32_bf16 v[180:183], a[180:183], v[72:75], v[180:183]\n" \
"v_exp_f32_e32 v95, v95\n" \
"v_mfma_f32_16x16x32_bf16 v[152:155], a[184:187], v[68:71], v[152:155]\n" \
"v_cvt_pk_bf16_f32 v90, v92, v93\n" \
"v_cvt_pk_bf16_f32 v91, v94, v95\n" \
"v_mfma_f32_16x16x32_bf16 v[184:187], a[184:187], v[72:75], v[184:187]\n" \
"v_add_u32_e32 v51, 0x12000, v63\n" \
"s_waitcnt lgkmcnt(0)\n" \
"v_mfma_f32_16x16x32_bf16 v[156:159], a[188:191], v[68:71], v[156:159]\n" \
"s_xor_b32 m0, s65, 0xa000\n" \
"ds_read_b32 v58, v18 offset:512\n" \
"v_mfma_f32_16x16x32_bf16 v[188:191], a[188:191], v[72:75], v[188:191]\n" \
"buffer_load_dwordx4 v51, s[8:11], s64 offen offset:0 sc1 lds\n" \
"v_smfmac_f32_16x16x64_bf16 a[192:195], a[200:203], v[68:75], v21\n" \
"buffer_load_dwordx4 v51, s[8:11], s64 offen offset:1024 sc1 lds\n" \
"v_mfma_f32_16x16x32_bf16 v[220:223], a[188:191], v[84:87], v[220:223]\n" \
"ds_read_b32 v59, v18 offset:640\n" \
"ds_read_b32 v60, v18 offset:768\n" \
"v_mfma_f32_16x16x32_bf16 v[252:255], a[188:191], v[88:91], v[252:255]\n" \
"ds_read_b32 v61, v18 offset:896\n" \
"ds_read_b128 a[128:131], v48 offset:0\n" \
"v_mfma_f32_16x16x32_bf16 v[216:219], a[184:187], v[84:87], v[216:219]\n" \
"ds_read_b128 a[144:147], v48 offset:4096\n" \
"ds_read_b128 a[132:135], v48 offset:1024\n" \
"v_mfma_f32_16x16x32_bf16 v[248:251], a[184:187], v[88:91], v[248:251]\n" \
"ds_read_b128 a[148:151], v48 offset:5120\n" \
"ds_read_b128 a[136:139], v48 offset:2048\n" \
"v_mfma_f32_16x16x32_bf16 v[212:215], a[180:183], v[84:87], v[212:215]\n" \
"ds_read_b128 a[152:155], v48 offset:6144\n" \
"ds_read_b128 a[140:143], v48 offset:3072\n" \
"v_mfma_f32_16x16x32_bf16 v[244:247], a[180:183], v[88:91], v[244:247]\n" \
"ds_read_b128 a[156:159], v48 offset:7168\n" \
"v_add_u32_e32 v52, 0x14000, v63\n" \
"v_mfma_f32_16x16x32_bf16 v[208:211], a[176:179], v[84:87], v[208:211]\n" \
"buffer_load_dwordx4 v51, s[8:11], s64 offen offset:2048 sc1 lds\n" \
"v_mfma_f32_16x16x32_bf16 v[240:243], a[176:179], v[88:91], v[240:243]\n" \
"v_exp_f32_e32 v104, v104\n" \
"v_mfma_f32_16x16x32_bf16 v[204:207], a[172:175], v[84:87], v[204:207]\n" \
"v_exp_f32_e32 v105, v105\n" \
"v_mfma_f32_16x16x32_bf16 v[236:239], a[172:175], v[88:91], v[236:239]\n" \
"buffer_load_dwordx4 v51, s[8:11], s64 offen offset:3072 sc1 lds\n" \
"v_mfma_f32_16x16x32_bf16 v[200:203], a[168:171], v[84:87], v[200:203]\n" \
"v_exp_f32_e32 v106, v106\n" \
"v_mfma_f32_16x16x32_bf16 v[232:235], a[168:171], v[88:91], v[232:235]\n" \
"v_exp_f32_e32 v107, v107\n" \
"v_mfma_f32_16x16x32_bf16 v[196:199], a[164:167], v[84:87], v[196:199]\n" \
"v_cvt_pk_bf16_f32 v104, v104, v105\n" \
"v_cvt_pk_bf16_f32 v105, v106, v107\n" \
"v_mfma_f32_16x16x32_bf16 v[228:231], a[164:167], v[88:91], v[228:231]\n" \
"v_exp_f32_e32 v108, v108\n" \
"v_mfma_f32_16x16x32_bf16 v[192:195], a[160:163], v[84:87], v[192:195]\n" \
"v_exp_f32_e32 v109, v109\n" \
"v_mfma_f32_16x16x32_bf16 v[224:227], a[160:163], v[88:91], v[224:227]\n" \
"v_exp_f32_e32 v110, v110\n" \
"v_smfmac_f32_16x16x64_bf16 a[196:199], a[200:203], v[84:91], v21\n" \
"v_exp_f32_e32 v111, v111\n" \
"v_cvt_pk_bf16_f32 v106, v108, v109\n" \
"v_cvt_pk_bf16_f32 v107, v110, v111\n" \
"v_and_b32_e32 v104, v12, v104\n" \
"v_and_b32_e32 v105, v13, v105\n" \
"v_and_b32_e32 v106, v14, v106\n" \
"v_and_b32_e32 v107, v15, v107\n" \
"v_accvgpr_read_b32 v54, a192\n" \
"v_accvgpr_read_b32 v22, a193\n" \
"v_add_u32_e32 v53, 0x20000, v63\n" \
"v_exp_f32_e32 v118, v118\n" \
"v_exp_f32_e32 v119, v119\n" \
"v_exp_f32_e32 v116, v116\n" \
"v_exp_f32_e32 v117, v117\n" \
"v_cvt_pk_bf16_f32 v119, v118, v119\n" \
"v_exp_f32_e32 v114, v114\n" \
"v_exp_f32_e32 v115, v115\n" \
"v_exp_f32_e32 v112, v112\n" \
"v_exp_f32_e32 v113, v113\n" \
"v_cvt_pk_bf16_f32 v118, v116, v117\n" \
"v_cvt_pk_bf16_f32 v117, v114, v115\n" \
"v_cvt_pk_bf16_f32 v116, v112, v113\n" \
"v_readlane_b32 s93, v11, 19\n" \
"s_waitcnt vmcnt(10) lgkmcnt(0)\n" \
"s_barrier\n" \
"v_mfma_f32_16x16x32_bf16 v[80:83], a[128:131], a[32:35], v[40:43]\n" \
"ds_read_b128 a[160:163], v49 offset:8192\n" \
"ds_read_b128 a[164:167], v49 offset:9216\n" \
"v_mfma_f32_16x16x32_bf16 v[80:83], a[132:135], a[36:39], v[80:83]\n" \
"ds_read_b128 a[168:171], v49 offset:10240\n" \
"s_xor_b32 m0, s65, 0x18000\n" \
"v_mfma_f32_16x16x32_bf16 v[80:83], a[136:139], a[40:43], v[80:83]\n" \
"buffer_load_dwordx4 v52, s[8:11], s64 offen offset:0 sc1 lds\n" \
"v_mfma_f32_16x16x32_bf16 v[80:83], a[140:143], a[44:47], v[80:83]\n" \
"ds_read_b128 a[172:175], v49 offset:11264\n" \
"ds_read_b128 a[176:179], v49 offset:12288\n" \
"v_mfma_f32_16x16x32_bf16 v[84:87], a[156:159], a[44:47], v[40:43]\n" \
"ds_read_b128 a[180:183], v49 offset:13312\n" \
"ds_read_b128 a[184:187], v49 offset:14336\n" \
"v_mfma_f32_16x16x32_bf16 v[84:87], a[152:155], a[40:43], v[84:87]\n" \
"buffer_load_dwordx4 v52, s[8:11], s64 offen offset:1024 sc1 lds\n" \
"v_mfma_f32_16x16x32_bf16 v[84:87], a[148:151], a[36:39], v[84:87]\n" \
"ds_read_b128 a[188:191], v49 offset:15360\n" \
"v_readlane_b32 s80, v11, 11\n" \
"v_mfma_f32_16x16x32_bf16 v[84:87], a[144:147], a[32:35], v[84:87]\n" \
"buffer_load_dwordx4 v52, s[8:11], s64 offen offset:2048 sc1 lds\n" \
"v_mfma_f32_16x16x32_bf16 v[92:95], a[144:147], a[48:51], v[44:47]\n" \
"v_exp_f32_e32 v120, v120\n" \
"v_mfma_f32_16x16x32_bf16 v[92:95], a[148:151], a[52:55], v[92:95]\n" \
"v_exp_f32_e32 v121, v121\n" \
"v_mfma_f32_16x16x32_bf16 v[92:95], a[152:155], a[56:59], v[92:95]\n" \
"buffer_load_dwordx4 v52, s[8:11], s64 offen offset:3072 sc1 lds\n" \
"v_mfma_f32_16x16x32_bf16 v[92:95], a[156:159], a[60:63], v[92:95]\n" \
"s_mov_b32 m0, s65\n" \
"v_cvt_pk_bf16_f32 v120, v120, v121\n" \
"v_mfma_f32_16x16x32_bf16 v[88:91], a[140:143], a[60:63], v[44:47]\n" \
"v_exp_f32_e32 v122, v122\n" \
"v_mfma_f32_16x16x32_bf16 v[88:91], a[136:139], a[56:59], v[88:91]\n" \
"buffer_load_dwordx4 v53, s[8:11], s64 offen offset:0 sc1 lds\n" \
"v_mfma_f32_16x16x32_bf16 v[88:91], a[132:135], a[52:55], v[88:91]\n" \
"v_exp_f32_e32 v123, v123\n" \
"v_mfma_f32_16x16x32_bf16 v[88:91], a[128:131], a[48:51], v[88:91]\n" \
"v_exp_f32_e32 v124, v124\n" \
"v_mfma_f32_16x16x32_bf16 a[192:195], a[212:215], v[104:107], a[192:195]\n" \
"v_cvt_pk_bf16_f32 v121, v122, v123\n" \
"s_waitcnt lgkmcnt(5)\n" \
"v_mfma_f32_16x16x32_bf16 v[160:163], a[160:163], v[104:107], v[160:163]\n" \
"buffer_load_dwordx4 v53, s[8:11], s64 offen offset:1024 sc1 lds\n" \
"v_mfma_f32_16x16x32_bf16 v[164:167], a[164:167], v[104:107], v[164:167]\n" \
"v_exp_f32_e32 v125, v125\n" \
"v_mfma_f32_16x16x32_bf16 v[168:171], a[168:171], v[104:107], v[168:171]\n" \
"v_cvt_pk_bf16_f32 v122, v124, v125\n" \
"s_waitcnt lgkmcnt(1)\n" \
"v_mfma_f32_16x16x32_bf16 v[172:175], a[172:175], v[104:107], v[172:175]\n" \
"v_exp_f32_e32 v126, v126\n" \
"v_mfma_f32_16x16x32_bf16 v[176:179], a[176:179], v[104:107], v[176:179]\n" \
"v_exp_f32_e32 v127, v127\n" \
"v_mfma_f32_16x16x32_bf16 v[180:183], a[180:183], v[104:107], v[180:183]\n" \
"v_cvt_pk_bf16_f32 v123, v126, v127\n" \
"v_readlane_b32 s81, v11, 12\n" \
"v_mfma_f32_16x16x32_bf16 v[184:187], a[184:187], v[104:107], v[184:187]\n" \
"s_mov_b32 s82, 0x47fca0\n" \
"s_waitcnt lgkmcnt(0)\n" \
"v_mfma_f32_16x16x32_bf16 v[188:191], a[188:191], v[104:107], v[188:191]\n" \
"buffer_load_dwordx4 v53, s[8:11], s64 offen offset:2048 sc1 lds\n" \
"v_mfma_f32_16x16x32_bf16 v[220:223], a[188:191], v[116:119], v[220:223]\n" \
"buffer_load_dwordx4 v53, s[8:11], s64 offen offset:3072 sc1 lds\n" \
"v_mfma_f32_16x16x32_bf16 v[252:255], a[188:191], v[120:123], v[252:255]\n" \
"s_mov_b32 s83, 0x20000\n" \
"v_readlane_b32 s92, v11, 26\n" \
"v_mfma_f32_16x16x32_bf16 v[216:219], a[184:187], v[116:119], v[216:219]\n" \
"s_add_u32 m0, s93, 0x0\n" \
"ds_read_b128 a[128:131], v48 offset:8192\n" \
"v_mfma_f32_16x16x32_bf16 v[248:251], a[184:187], v[120:123], v[248:251]\n" \
"ds_read_b128 a[144:147], v48 offset:12288\n" \
"ds_read_b128 a[132:135], v48 offset:9216\n" \
"v_mfma_f32_16x16x32_bf16 v[212:215], a[180:183], v[116:119], v[212:215]\n" \
"buffer_load_dwordx4 v0, s[80:83], s92 offen offset:0 lds\n" \
"v_mfma_f32_16x16x32_bf16 v[244:247], a[180:183], v[120:123], v[244:247]\n" \
"buffer_load_dwordx4 v1, s[80:83], s92 offen offset:1024 lds\n" \
"v_mfma_f32_16x16x32_bf16 v[208:211], a[176:179], v[116:119], v[208:211]\n" \
"ds_read_b128 a[148:151], v48 offset:13312\n" \
"ds_read_b128 a[136:139], v48 offset:10240\n" \
"v_mfma_f32_16x16x32_bf16 v[240:243], a[176:179], v[120:123], v[240:243]\n" \
"ds_read_b128 a[152:155], v48 offset:14336\n" \
"ds_read_b128 a[140:143], v48 offset:11264\n" \
"v_mfma_f32_16x16x32_bf16 v[204:207], a[172:175], v[116:119], v[204:207]\n" \
"buffer_load_dwordx4 v2, s[80:83], s92 offen offset:2048 lds\n" \
"v_mfma_f32_16x16x32_bf16 v[236:239], a[172:175], v[120:123], v[236:239]\n" \
"ds_read_b128 a[156:159], v48 offset:15360\n" \
"s_xor_b32 s65, s65, 0x8000\n" \
"v_mfma_f32_16x16x32_bf16 v[200:203], a[168:171], v[116:119], v[200:203]\n" \
"s_add_u32 s64, s64, 0x10000\n" \
"v_add_u32_e32 v49, 0x10000, v48\n" \
"v_mfma_f32_16x16x32_bf16 v[232:235], a[168:171], v[120:123], v[232:235]\n" \
"buffer_load_dwordx4 v3, s[80:83], s92 offen offset:3072 lds\n" \
"v_mfma_f32_16x16x32_bf16 v[196:199], a[164:167], v[116:119], v[196:199]\n" \
"v_exp_f32_e32 v86, v86\n" \
"v_mfma_f32_16x16x32_bf16 v[228:231], a[164:167], v[120:123], v[228:231]\n" \
"v_exp_f32_e32 v87, v87\n" \
"v_mfma_f32_16x16x32_bf16 v[192:195], a[160:163], v[116:119], v[192:195]\n" \
"v_exp_f32_e32 v84, v84\n" \
"v_mfma_f32_16x16x32_bf16 v[224:227], a[160:163], v[120:123], v[224:227]\n" \
"v_exp_f32_e32 v85, v85\n" \
"v_smfmac_f32_16x16x64_bf16 a[196:199], a[200:203], v[116:123], v21\n" \
"v_exp_f32_e32 v88, v88\n" \
"v_exp_f32_e32 v89, v89\n" \
"v_cvt_pk_bf16_f32 v87, v86, v87\n" \
"v_exp_f32_e32 v82, v82\n" \
"v_exp_f32_e32 v83, v83\n" \
"v_exp_f32_e32 v80, v80\n" \
"v_exp_f32_e32 v81, v81\n" \
"v_exp_f32_e32 v90, v90\n" \
"v_exp_f32_e32 v91, v91\n" \
"v_cvt_pk_bf16_f32 v86, v84, v85\n" \
"v_cvt_pk_bf16_f32 v88, v88, v89\n" \
"v_exp_f32_e32 v92, v92\n" \
"v_exp_f32_e32 v93, v93\n" \
"v_exp_f32_e32 v94, v94\n" \
"v_exp_f32_e32 v95, v95\n" \
"v_cvt_pk_bf16_f32 v85, v82, v83\n" \
"v_cvt_pk_bf16_f32 v84, v80, v81\n" \
"v_cvt_pk_bf16_f32 v89, v90, v91\n" \
"v_and_b32_e32 v84, v12, v84\n" \
"v_and_b32_e32 v85, v13, v85\n" \
"v_and_b32_e32 v86, v14, v86\n" \
"v_and_b32_e32 v87, v15, v87\n" \
"v_cvt_pk_bf16_f32 v90, v92, v93\n" \
"v_cvt_pk_bf16_f32 v91, v94, v95\n" \
"v_add_u32_e32 v48, s16, v20\n" \
"v_add_f32_e32 v54, v54, v22\n" \
"v_accvgpr_read_b32 v55, a194\n" \
"v_accvgpr_read_b32 v23, a195\n" \
"v_mfma_f32_16x16x32_bf16 a[192:195], a[216:219], a[216:219], 0\n" \
"v_max_f32_e32 v62, v62, v54\n" \
"v_add_f32_e32 v55, v55, v23\n" \
"v_max_f32_e32 v62, v62, v55\n" \
"v_rcp_f32_e32 v54, v54\n" \
"v_rcp_f32_e32 v55, v55\n" \
"v_mul_f32_e32 v54, v54, v58\n" \
"v_mul_f32_e32 v55, v55, v59\n" \
"v_mul_f32_e32 v128, v54, v128\n" \
"v_mul_f32_e32 v129, v54, v129\n" \
"v_mul_f32_e32 v136, v54, v136\n" \
"v_mul_f32_e32 v137, v54, v137\n" \
"v_mul_f32_e32 v144, v54, v144\n" \
"v_mul_f32_e32 v145, v54, v145\n" \
"v_mul_f32_e32 v152, v54, v152\n" \
"v_mul_f32_e32 v153, v54, v153\n" \
"v_mul_f32_e32 v160, v55, v160\n" \
"v_mul_f32_e32 v161, v55, v161\n" \
"v_mul_f32_e32 v168, v55, v168\n" \
"v_mul_f32_e32 v169, v55, v169\n" \
"v_mul_f32_e32 v176, v55, v176\n" \
"v_mul_f32_e32 v177, v55, v177\n" \
"v_mul_f32_e32 v184, v55, v184\n" \
"v_mul_f32_e32 v185, v55, v185\n" \
"v_cvt_pk_bf16_f32 v128, v128, v129\n" \
"v_mul_f32_e32 v130, v54, v130\n" \
"v_mul_f32_e32 v131, v54, v131\n" \
"v_cvt_pk_bf16_f32 v136, v136, v137\n" \
"v_mul_f32_e32 v138, v54, v138\n" \
"v_mul_f32_e32 v139, v54, v139\n" \
"v_cvt_pk_bf16_f32 v144, v144, v145\n" \
"v_mul_f32_e32 v146, v54, v146\n" \
"v_mul_f32_e32 v147, v54, v147\n" \
"v_cvt_pk_bf16_f32 v152, v152, v153\n" \
"v_mul_f32_e32 v154, v54, v154\n" \
"v_mul_f32_e32 v155, v54, v155\n" \
"v_cvt_pk_bf16_f32 v160, v160, v161\n" \
"v_mul_f32_e32 v162, v55, v162\n" \
"v_mul_f32_e32 v163, v55, v163\n" \
"v_cvt_pk_bf16_f32 v168, v168, v169\n" \
"v_mul_f32_e32 v170, v55, v170\n" \
"v_mul_f32_e32 v171, v55, v171\n" \
"v_cvt_pk_bf16_f32 v176, v176, v177\n" \
"v_mul_f32_e32 v178, v55, v178\n" \
"v_mul_f32_e32 v179, v55, v179\n" \
"v_cvt_pk_bf16_f32 v184, v184, v185\n" \
"v_mul_f32_e32 v186, v55, v186\n" \
"v_mul_f32_e32 v187, v55, v187\n" \
"v_cvt_pk_bf16_f32 v129, v130, v131\n" \
"v_mul_f32_e32 v132, v54, v132\n" \
"v_mul_f32_e32 v133, v54, v133\n" \
"v_mul_f32_e32 v134, v54, v134\n" \
"v_mul_f32_e32 v135, v54, v135\n" \
"v_cvt_pk_bf16_f32 v137, v138, v139\n" \
"v_mul_f32_e32 v140, v54, v140\n" \
"s_waitcnt vmcnt(12) lgkmcnt(0)\n" \
"s_barrier\n" \
"v_mfma_f32_16x16x32_bf16 v[120:123], a[128:131], a[48:51], v[44:47]\n" \
"s_xor_b32 m0, s65, 0x12000\n" \
"ds_read_b128 a[188:191], v49 offset:7168\n" \
"v_mfma_f32_16x16x32_bf16 v[120:123], a[132:135], a[52:55], v[120:123]\n" \
"buffer_load_dwordx4 v50, s[8:11], s64 offen offset:0 sc1 lds\n" \
"v_mfma_f32_16x16x32_bf16 v[120:123], a[136:139], a[56:59], v[120:123]\n" \
"ds_read_b128 a[184:187], v49 offset:6144\n" \
"ds_read_b128 a[180:183], v49 offset:5120\n" \
"v_mfma_f32_16x16x32_bf16 v[120:123], a[140:143], a[60:63], v[120:123]\n" \
"buffer_load_dwordx4 v50, s[8:11], s64 offen offset:1024 sc1 lds\n" \
"v_mfma_f32_16x16x32_bf16 v[124:127], a[156:159], a[60:63], v[44:47]\n" \
"ds_read_b128 a[176:179], v49 offset:4096\n" \
"ds_read_b128 a[172:175], v49 offset:3072\n" \
"v_mfma_f32_16x16x32_bf16 v[124:127], a[152:155], a[56:59], v[124:127]\n" \
"ds_read_b128 a[168:171], v49 offset:2048\n" \
"ds_read_b128 a[164:167], v49 offset:1024\n" \
"v_mfma_f32_16x16x32_bf16 v[124:127], a[148:151], a[52:55], v[124:127]\n" \
"ds_read_b128 a[160:163], v49 offset:0\n" \
"ds_read_b128 a[0:3], v49 offset:8192\n" \
"v_mfma_f32_16x16x32_bf16 v[124:127], a[144:147], a[48:51], v[124:127]\n" \
"buffer_load_dwordx4 v50, s[8:11], s64 offen offset:2048 sc1 lds\n" \
"v_mfma_f32_16x16x32_bf16 v[32:35], a[220:223], a[64:67], 0\n" \
"ds_read_b128 a[4:7], v49 offset:9216\n" \
"ds_read_b128 a[8:11], v49 offset:10240\n" \
"v_mfma_f32_16x16x32_bf16 v[36:39], a[220:223], a[80:83], 0\n" \
"ds_read_b128 a[12:15], v49 offset:11264\n" \
"ds_read_b128 a[16:19], v49 offset:12288\n" \
"v_mfma_f32_16x16x32_bf16 v[40:43], a[220:223], a[96:99], 0\n" \
"ds_read_b128 a[20:23], v49 offset:13312\n" \
"ds_read_b128 a[24:27], v49 offset:14336\n" \
"v_mfma_f32_16x16x32_bf16 v[44:47], a[220:223], a[112:115], 0\n" \
"buffer_load_dwordx4 v50, s[8:11], s64 offen offset:3072 sc1 lds\n" \
"v_mfma_f32_16x16x32_bf16 v[32:35], a[224:227], a[68:71], v[32:35]\n" \
"s_xor_b32 m0, s65, 0xa000\n" \
"ds_read_b128 a[28:31], v49 offset:15360\n" \
"v_mfma_f32_16x16x32_bf16 v[36:39], a[224:227], a[84:87], v[36:39]\n" \
"v_exp_f32_e32 v120, v120\n" \
"v_mfma_f32_16x16x32_bf16 v[40:43], a[224:227], a[100:103], v[40:43]\n" \
"buffer_load_dwordx4 v51, s[8:11], s64 offen offset:0 sc1 lds\n" \
"v_mfma_f32_16x16x32_bf16 v[44:47], a[224:227], a[116:119], v[44:47]\n" \
"v_exp_f32_e32 v121, v121\n" \
"v_mfma_f32_16x16x32_bf16 v[32:35], a[228:231], a[72:75], v[32:35]\n" \
"v_exp_f32_e32 v122, v122\n" \
"v_mfma_f32_16x16x32_bf16 v[36:39], a[228:231], a[88:91], v[36:39]\n" \
"v_exp_f32_e32 v123, v123\n" \
"v_mfma_f32_16x16x32_bf16 v[40:43], a[228:231], a[104:107], v[40:43]\n" \
"buffer_load_dwordx4 v51, s[8:11], s64 offen offset:1024 sc1 lds\n" \
"v_mfma_f32_16x16x32_bf16 v[44:47], a[228:231], a[120:123], v[44:47]\n" \
"v_cvt_pk_bf16_f32 v120, v120, v121\n" \
"v_cvt_pk_bf16_f32 v121, v122, v123\n" \
"v_mfma_f32_16x16x32_bf16 v[32:35], a[232:235], a[76:79], v[32:35]\n" \
"v_exp_f32_e32 v124, v124\n" \
"v_mfma_f32_16x16x32_bf16 v[36:39], a[232:235], a[92:95], v[36:39]\n" \
"v_exp_f32_e32 v125, v125\n" \
"v_mfma_f32_16x16x32_bf16 v[40:43], a[232:235], a[108:111], v[40:43]\n" \
"buffer_load_dwordx4 v51, s[8:11], s64 offen offset:2048 sc1 lds\n" \
"v_mfma_f32_16x16x32_bf16 v[44:47], a[232:235], a[124:127], v[44:47]\n" \
"v_exp_f32_e32 v126, v126\n" \
"v_smfmac_f32_16x16x64_bf16 a[196:199], a[200:203], v[84:91], v21\n" \
"v_cvt_pk_bf16_f32 v122, v124, v125\n" \
"s_waitcnt lgkmcnt(1)\n" \
"v_mfma_f32_16x16x32_bf16 v[220:223], a[188:191], v[84:87], v[220:223]\n" \
"buffer_load_dwordx4 v51, s[8:11], s64 offen offset:3072 sc1 lds\n" \
"v_mfma_f32_16x16x32_bf16 v[252:255], a[188:191], v[88:91], v[252:255]\n" \
"s_add_u32 m0, s93, 0x1000\n" \
"v_and_b32_e32 v120, v12, v120\n" \
"v_mfma_f32_16x16x32_bf16 v[216:219], a[184:187], v[84:87], v[216:219]\n" \
"v_exp_f32_e32 v127, v127\n" \
"v_mfma_f32_16x16x32_bf16 v[248:251], a[184:187], v[88:91], v[248:251]\n" \
"v_cvt_pk_bf16_f32 v123, v126, v127\n" \
"v_and_b32_e32 v121, v13, v121\n" \
"v_mfma_f32_16x16x32_bf16 v[212:215], a[180:183], v[84:87], v[212:215]\n" \
"buffer_load_dwordx4 v4, s[80:83], s92 offen offset:0 lds\n" \
"v_mfma_f32_16x16x32_bf16 v[244:247], a[180:183], v[88:91], v[244:247]\n" \
"v_and_b32_e32 v122, v14, v122\n" \
"v_and_b32_e32 v123, v15, v123\n" \
"v_mfma_f32_16x16x32_bf16 v[208:211], a[176:179], v[84:87], v[208:211]\n" \
"v_accvgpr_read_b32 v56, a196\n" \
"v_accvgpr_read_b32 v24, a197\n" \
"v_mfma_f32_16x16x32_bf16 v[240:243], a[176:179], v[88:91], v[240:243]\n" \
"ds_read_b128 a[128:131], v48 offset:0\n" \
"ds_read_b128 a[144:147], v48 offset:4096\n" \
"v_mfma_f32_16x16x32_bf16 v[204:207], a[172:175], v[84:87], v[204:207]\n" \
"buffer_load_dwordx4 v5, s[80:83], s92 offen offset:1024 lds\n" \
"v_mfma_f32_16x16x32_bf16 v[236:239], a[172:175], v[88:91], v[236:239]\n" \
"ds_read_b128 a[132:135], v48 offset:1024\n" \
"ds_read_b128 a[148:151], v48 offset:5120\n" \
"v_mfma_f32_16x16x32_bf16 v[200:203], a[168:171], v[84:87], v[200:203]\n" \
"ds_read_b128 a[136:139], v48 offset:2048\n" \
"ds_read_b128 a[152:155], v48 offset:6144\n" \
"v_mfma_f32_16x16x32_bf16 v[232:235], a[168:171], v[88:91], v[232:235]\n" \
"buffer_load_dwordx4 v6, s[80:83], s92 offen offset:2048 lds\n" \
"v_mfma_f32_16x16x32_bf16 v[196:199], a[164:167], v[84:87], v[196:199]\n" \
"ds_read_b128 a[140:143], v48 offset:3072\n" \
"ds_read_b128 a[156:159], v48 offset:7168\n" \
"v_mfma_f32_16x16x32_bf16 v[228:231], a[164:167], v[88:91], v[228:231]\n" \
"v_mul_f32_e32 v141, v54, v141\n" \
"v_mul_f32_e32 v142, v54, v142\n" \
"v_mfma_f32_16x16x32_bf16 v[192:195], a[160:163], v[84:87], v[192:195]\n" \
"v_mul_f32_e32 v143, v54, v143\n" \
"v_cvt_pk_bf16_f32 v145, v146, v147\n" \
"v_mfma_f32_16x16x32_bf16 v[224:227], a[160:163], v[88:91], v[224:227]\n" \
"buffer_load_dwordx4 v7, s[80:83], s92 offen offset:3072 lds\n" \
"v_mfma_f32_16x16x32_bf16 a[196:199], a[212:215], v[120:123], a[196:199]\n" \
"v_mul_f32_e32 v148, v54, v148\n" \
"v_mul_f32_e32 v149, v54, v149\n" \
"v_mfma_f32_16x16x32_bf16 v[224:227], a[0:3], v[120:123], v[224:227]\n" \
"v_mul_f32_e32 v150, v54, v150\n" \
"v_mul_f32_e32 v151, v54, v151\n" \
"v_mfma_f32_16x16x32_bf16 v[228:231], a[4:7], v[120:123], v[228:231]\n" \
"v_cvt_pk_bf16_f32 v153, v154, v155\n" \
"v_mul_f32_e32 v156, v54, v156\n" \
"v_mfma_f32_16x16x32_bf16 v[232:235], a[8:11], v[120:123], v[232:235]\n" \
"v_mul_f32_e32 v157, v54, v157\n" \
"v_mul_f32_e32 v158, v54, v158\n" \
"v_mfma_f32_16x16x32_bf16 v[236:239], a[12:15], v[120:123], v[236:239]\n" \
"v_mul_f32_e32 v159, v54, v159\n" \
"v_cvt_pk_bf16_f32 v161, v162, v163\n" \
"v_mfma_f32_16x16x32_bf16 v[240:243], a[16:19], v[120:123], v[240:243]\n" \
"v_mul_f32_e32 v164, v55, v164\n" \
"v_mul_f32_e32 v165, v55, v165\n" \
"v_mfma_f32_16x16x32_bf16 v[244:247], a[20:23], v[120:123], v[244:247]\n" \
"v_mul_f32_e32 v166, v55, v166\n" \
"v_mul_f32_e32 v167, v55, v167\n" \
"v_mfma_f32_16x16x32_bf16 v[248:251], a[24:27], v[120:123], v[248:251]\n" \
"v_cvt_pk_bf16_f32 v169, v170, v171\n" \
"s_waitcnt lgkmcnt(4)\n" \
"v_mfma_f32_16x16x32_bf16 v[252:255], a[28:31], v[120:123], v[252:255]\n" \
"v_mul_f32_e32 v172, v55, v172\n" \
"v_mul_f32_e32 v173, v55, v173\n" \
"v_mul_f32_e32 v174, v55, v174\n" \
"v_mul_f32_e32 v175, v55, v175\n" \
"v_cvt_pk_bf16_f32 v177, v178, v179\n" \
"v_mul_f32_e32 v180, v55, v180\n" \
"v_mul_f32_e32 v181, v55, v181\n" \
"v_mul_f32_e32 v182, v55, v182\n" \
"v_mul_f32_e32 v183, v55, v183\n" \
"v_cvt_pk_bf16_f32 v185, v186, v187\n" \
"v_mul_f32_e32 v188, v55, v188\n" \
"v_mul_f32_e32 v189, v55, v189\n" \
"v_mul_f32_e32 v190, v55, v190\n" \
"v_mul_f32_e32 v191, v55, v191\n" \
"v_cvt_pk_bf16_f32 v130, v132, v133\n" \
"v_cvt_pk_bf16_f32 v131, v134, v135\n" \
"buffer_store_dwordx4 v[128:131], v20, s[56:59], s68 offen offset:0 sc1\n" \
"v_cvt_pk_bf16_f32 v138, v140, v141\n" \
"v_cvt_pk_bf16_f32 v139, v142, v143\n" \
"buffer_store_dwordx4 v[136:139], v20, s[56:59], s68 offen offset:1024 sc1\n" \
"v_cvt_pk_bf16_f32 v146, v148, v149\n" \
"v_cvt_pk_bf16_f32 v147, v150, v151\n" \
"buffer_store_dwordx4 v[144:147], v20, s[56:59], s68 offen offset:2048 sc1\n" \
"v_cvt_pk_bf16_f32 v154, v156, v157\n" \
"v_cvt_pk_bf16_f32 v155, v158, v159\n" \
"buffer_store_dwordx4 v[152:155], v20, s[56:59], s68 offen offset:3072 sc1\n" \
"v_cvt_pk_bf16_f32 v162, v164, v165\n" \
"v_cvt_pk_bf16_f32 v163, v166, v167\n" \
"buffer_store_dwordx4 v[160:163], v20, s[56:59], s69 offen offset:0 sc1\n" \
"v_cvt_pk_bf16_f32 v170, v172, v173\n" \
"v_cvt_pk_bf16_f32 v171, v174, v175\n" \
"buffer_store_dwordx4 v[168:171], v20, s[56:59], s69 offen offset:1024 sc1\n" \
"v_cvt_pk_bf16_f32 v178, v180, v181\n" \
"v_cvt_pk_bf16_f32 v179, v182, v183\n" \
"buffer_store_dwordx4 v[176:179], v20, s[56:59], s69 offen offset:2048 sc1\n" \
"v_cvt_pk_bf16_f32 v186, v188, v189\n" \
"v_cvt_pk_bf16_f32 v187, v190, v191\n" \
"buffer_store_dwordx4 v[184:187], v20, s[56:59], s69 offen offset:3072 sc1\n" \
"s_add_u32 m0, s93, 0x2000\n" \
"v_mfma_f32_16x16x32_bf16 a[208:211], a[216:219], v[128:131], a[208:211]\n" \
"buffer_load_dwordx4 v8, s[80:83], s92 offen offset:0 lds\n" \
"v_mfma_f32_16x16x32_bf16 a[208:211], a[216:219], v[136:139], a[208:211]\n" \
"v_mfma_f32_32x32x16_bf16 v[128:143], a[216:219], a[216:219], 0\n" \
"v_mfma_f32_16x16x32_bf16 a[208:211], a[216:219], v[144:147], a[208:211]\n" \
"v_mfma_f32_16x16x32_bf16 a[208:211], a[216:219], v[152:155], a[208:211]\n" \
"v_mfma_f32_32x32x16_bf16 v[144:159], a[216:219], a[216:219], 0\n" \
"v_add_f32_e32 v56, v56, v24\n" \
"v_mfma_f32_16x16x32_bf16 a[208:211], a[216:219], v[160:163], a[208:211]\n" \
"v_max_f32_e32 v62, v62, v56\n" \
"v_accvgpr_read_b32 v57, a198\n" \
"s_waitcnt vmcnt(21) lgkmcnt(0)\n" \
"s_barrier\n" \
"v_mfma_f32_16x16x32_bf16 v[64:67], a[128:131], a[64:67], v[32:35]\n" \
"s_xor_b32 m0, s65, 0x18000\n" \
"v_accvgpr_read_b32 v25, a199\n" \
"v_mfma_f32_16x16x32_bf16 a[196:199], a[216:219], a[216:219], 0\n" \
"buffer_load_dwordx4 v52, s[8:11], s64 offen offset:0 sc1 lds\n" \
"v_mfma_f32_16x16x32_bf16 v[64:67], a[132:135], a[68:71], v[64:67]\n" \
"v_rcp_f32_e32 v56, v56\n" \
"v_mfma_f32_16x16x32_bf16 v[64:67], a[136:139], a[72:75], v[64:67]\n" \
"v_mfma_f32_16x16x32_bf16 a[208:211], a[216:219], v[168:171], a[208:211]\n" \
"v_mfma_f32_32x32x16_bf16 v[160:175], a[216:219], a[216:219], 0\n" \
"v_mul_f32_e32 v56, v56, v60\n" \
"v_mfma_f32_16x16x32_bf16 a[208:211], a[216:219], v[176:179], a[208:211]\n" \
"v_mul_f32_e32 v192, v56, v192\n" \
"v_mul_f32_e32 v193, v56, v193\n" \
"v_mfma_f32_16x16x32_bf16 v[64:67], a[140:143], a[76:79], v[64:67]\n" \
"buffer_load_dwordx4 v52, s[8:11], s64 offen offset:1024 sc1 lds\n" \
"v_mfma_f32_16x16x32_bf16 v[68:71], a[156:159], a[76:79], v[32:35]\n" \
"v_cvt_pk_bf16_f32 v192, v192, v193\n" \
"v_mul_f32_e32 v194, v56, v194\n" \
"v_mfma_f32_16x16x32_bf16 v[68:71], a[152:155], a[72:75], v[68:71]\n" \
"v_mul_f32_e32 v195, v56, v195\n" \
"v_cvt_pk_bf16_f32 v193, v194, v195\n" \
"v_mfma_f32_16x16x32_bf16 v[68:71], a[148:151], a[68:71], v[68:71]\n" \
"v_mfma_f32_16x16x32_bf16 a[208:211], a[216:219], v[184:187], a[208:211]\n" \
"v_mfma_f32_32x32x16_bf16 v[176:191], a[216:219], a[216:219], 0\n" \
"v_mul_f32_e32 v196, v56, v196\n" \
"v_mul_f32_e32 v197, v56, v197\n" \
"v_mul_f32_e32 v198, v56, v198\n" \
"v_mul_f32_e32 v199, v56, v199\n" \
"v_mul_f32_e32 v200, v56, v200\n" \
"v_mul_f32_e32 v201, v56, v201\n" \
"v_mfma_f32_16x16x32_bf16 v[68:71], a[144:147], a[64:67], v[68:71]\n" \
"buffer_load_dwordx4 v52, s[8:11], s64 offen offset:2048 sc1 lds\n" \
"v_mfma_f32_16x16x32_bf16 v[76:79], a[144:147], a[80:83], v[36:39]\n" \
"v_cvt_pk_bf16_f32 v194, v196, v197\n" \
"v_cvt_pk_bf16_f32 v195, v198, v199\n" \
"buffer_store_dwordx4 v[192:195], v20, s[56:59], s70 offen offset:0 sc1\n" \
"v_mfma_f32_16x16x32_bf16 v[76:79], a[148:151], a[84:87], v[76:79]\n" \
"v_cvt_pk_bf16_f32 v200, v200, v201\n" \
"v_mul_f32_e32 v202, v56, v202\n" \
"v_mfma_f32_16x16x32_bf16 v[76:79], a[152:155], a[88:91], v[76:79]\n" \
"v_mul_f32_e32 v203, v56, v203\n" \
"v_add_f32_e32 v57, v57, v25\n" \
"v_mfma_f32_16x16x32_bf16 v[76:79], a[156:159], a[92:95], v[76:79]\n" \
"buffer_load_dwordx4 v52, s[8:11], s64 offen offset:3072 sc1 lds\n" \
"v_mfma_f32_16x16x32_bf16 v[72:75], a[140:143], a[92:95], v[36:39]\n" \
"s_mov_b32 m0, s65\n" \
"v_cvt_pk_bf16_f32 v201, v202, v203\n" \
"v_mfma_f32_16x16x32_bf16 v[72:75], a[136:139], a[88:91], v[72:75]\n" \
"v_mfma_f32_16x16x32_bf16 a[208:211], a[216:219], v[192:195], a[208:211]\n" \
"v_mul_f32_e32 v204, v56, v204\n" \
"v_mul_f32_e32 v205, v56, v205\n" \
"v_mfma_f32_16x16x32_bf16 v[72:75], a[132:135], a[84:87], v[72:75]\n" \
"v_mul_f32_e32 v206, v56, v206\n" \
"v_mul_f32_e32 v207, v56, v207\n" \
"v_mfma_f32_16x16x32_bf16 v[72:75], a[128:131], a[80:83], v[72:75]\n" \
"buffer_load_dwordx4 v53, s[8:11], s64 offen offset:0 sc1 lds\n" \
"v_mfma_f32_16x16x32_bf16 v[80:83], a[128:131], a[96:99], v[40:43]\n" \
"v_mul_f32_e32 v208, v56, v208\n" \
"v_mul_f32_e32 v209, v56, v209\n" \
"v_mfma_f32_16x16x32_bf16 v[80:83], a[132:135], a[100:103], v[80:83]\n" \
"v_max_f32_e32 v62, v62, v57\n" \
"v_cvt_pk_bf16_f32 v202, v204, v205\n" \
"v_mfma_f32_16x16x32_bf16 v[80:83], a[136:139], a[104:107], v[80:83]\n" \
"buffer_load_dwordx4 v53, s[8:11], s64 offen offset:1024 sc1 lds\n" \
"v_mfma_f32_16x16x32_bf16 v[80:83], a[140:143], a[108:111], v[80:83]\n" \
"v_cvt_pk_bf16_f32 v203, v206, v207\n" \
"v_cvt_pk_bf16_f32 v208, v208, v209\n" \
"buffer_store_dwordx4 v[200:203], v20, s[56:59], s70 offen offset:1024 sc1\n" \
"v_mfma_f32_16x16x32_bf16 v[84:87], a[156:159], a[108:111], v[40:43]\n" \
"v_mul_f32_e32 v210, v56, v210\n" \
"v_mul_f32_e32 v211, v56, v211\n" \
"v_mfma_f32_16x16x32_bf16 v[84:87], a[152:155], a[104:107], v[84:87]\n" \
"v_rcp_f32_e32 v57, v57\n" \
"v_mfma_f32_16x16x32_bf16 v[84:87], a[148:151], a[100:103], v[84:87]\n" \
"buffer_load_dwordx4 v53, s[8:11], s64 offen offset:2048 sc1 lds\n" \
"v_mfma_f32_16x16x32_bf16 v[84:87], a[144:147], a[96:99], v[84:87]\n" \
"v_mfma_f32_16x16x32_bf16 a[208:211], a[216:219], v[200:203], a[208:211]\n" \
"v_mfma_f32_32x32x16_bf16 v[192:207], a[216:219], a[216:219], 0\n" \
"v_cvt_pk_bf16_f32 v209, v210, v211\n" \
"v_mul_f32_e32 v212, v56, v212\n" \
".fill (L_ab + 20480 - .) / 4, 4, 0xBF800000\n" \
"L_ap5:\n" \
"s_cbranch_execz L_ap0\n" \
"v_mul_f32_e32 v213, v56, v213\n" \
"v_mul_f32_e32 v214, v56, v214\n" \
"v_mul_f32_e32 v215, v56, v215\n" \
"v_mul_f32_e32 v216, v56, v216\n" \
"v_mfma_f32_16x16x32_bf16 v[92:95], a[144:147], a[112:115], v[44:47]\n" \
"ds_read_b128 a[144:147], v48 offset:12288\n" \
"v_mul_f32_e32 v217, v56, v217\n" \
"v_mfma_f32_16x16x32_bf16 v[92:95], a[148:151], a[116:119], v[92:95]\n" \
"ds_read_b128 a[148:151], v48 offset:13312\n" \
"v_cvt_pk_bf16_f32 v210, v212, v213\n" \
"v_mfma_f32_16x16x32_bf16 v[92:95], a[152:155], a[120:123], v[92:95]\n" \
"ds_read_b128 a[152:155], v48 offset:14336\n" \
"v_cvt_pk_bf16_f32 v211, v214, v215\n" \
"buffer_store_dwordx4 v[208:211], v20, s[56:59], s70 offen offset:2048 sc1\n" \
"v_mfma_f32_16x16x32_bf16 v[92:95], a[156:159], a[124:127], v[92:95]\n" \
"ds_read_b128 a[156:159], v48 offset:15360\n" \
"v_cvt_pk_bf16_f32 v216, v216, v217\n" \
"v_mfma_f32_16x16x32_bf16 v[88:91], a[140:143], a[124:127], v[44:47]\n" \
"ds_read_b128 a[140:143], v48 offset:11264\n" \
"v_mul_f32_e32 v218, v56, v218\n" \
"v_mfma_f32_16x16x32_bf16 v[88:91], a[136:139], a[120:123], v[88:91]\n" \
"ds_read_b128 a[136:139], v48 offset:10240\n" \
"v_mul_f32_e32 v219, v56, v219\n" \
"v_mfma_f32_16x16x32_bf16 v[88:91], a[132:135], a[116:119], v[88:91]\n" \
"ds_read_b128 a[132:135], v48 offset:9216\n" \
"v_mul_f32_e32 v57, v57, v61\n" \
"v_mfma_f32_16x16x32_bf16 v[88:91], a[128:131], a[112:115], v[88:91]\n" \
"ds_read_b128 a[128:131], v48 offset:8192\n" \
"v_mfma_f32_16x16x32_bf16 a[208:211], a[216:219], v[208:211], a[208:211]\n" \
"v_cvt_pk_bf16_f32 v217, v218, v219\n" \
"v_mul_f32_e32 v220, v56, v220\n" \
"v_mul_f32_e32 v221, v56, v221\n" \
"v_mul_f32_e32 v222, v56, v222\n" \
"v_mul_f32_e32 v223, v56, v223\n" \
"v_mul_f32_e32 v224, v57, v224\n" \
"v_mul_f32_e32 v225, v57, v225\n" \
"v_cvt_pk_bf16_f32 v218, v220, v221\n" \
"v_cvt_pk_bf16_f32 v219, v222, v223\n" \
"buffer_store_dwordx4 v[216:219], v20, s[56:59], s70 offen offset:3072 sc1\n" \
"v_cvt_pk_bf16_f32 v224, v224, v225\n" \
"v_mul_f32_e32 v226, v57, v226\n" \
"v_mul_f32_e32 v227, v57, v227\n" \
"v_mfma_f32_16x16x32_bf16 a[208:211], a[216:219], v[216:219], a[208:211]\n" \
"v_mfma_f32_32x32x16_bf16 v[208:223], a[216:219], a[216:219], 0\n" \
"v_cvt_pk_bf16_f32 v225, v226, v227\n" \
"v_mul_f32_e32 v228, v57, v228\n" \
"v_mul_f32_e32 v229, v57, v229\n" \
"v_mul_f32_e32 v230, v57, v230\n" \
"v_mul_f32_e32 v231, v57, v231\n" \
"v_mul_f32_e32 v232, v57, v232\n" \
"v_mul_f32_e32 v233, v57, v233\n" \
"v_cvt_pk_bf16_f32 v226, v228, v229\n" \
"v_cvt_pk_bf16_f32 v227, v230, v231\n" \
"buffer_store_dwordx4 v[224:227], v20, s[56:59], s71 offen offset:0 sc1\n" \
"v_cvt_pk_bf16_f32 v232, v232, v233\n" \
"v_mul_f32_e32 v234, v57, v234\n" \
"v_mul_f32_e32 v235, v57, v235\n" \
"v_mfma_f32_16x16x32_bf16 a[208:211], a[216:219], v[224:227], a[208:211]\n" \
"v_cvt_pk_bf16_f32 v233, v234, v235\n" \
"v_mul_f32_e32 v236, v57, v236\n" \
"v_mul_f32_e32 v237, v57, v237\n" \
"v_mul_f32_e32 v238, v57, v238\n" \
"v_mul_f32_e32 v239, v57, v239\n" \
"v_mul_f32_e32 v240, v57, v240\n" \
"v_mul_f32_e32 v241, v57, v241\n" \
"v_cvt_pk_bf16_f32 v234, v236, v237\n" \
"v_cvt_pk_bf16_f32 v235, v238, v239\n" \
"buffer_store_dwordx4 v[232:235], v20, s[56:59], s71 offen offset:1024 sc1\n" \
"v_cvt_pk_bf16_f32 v240, v240, v241\n" \
"v_mul_f32_e32 v242, v57, v242\n" \
"v_mul_f32_e32 v243, v57, v243\n" \
"v_mfma_f32_16x16x32_bf16 a[208:211], a[216:219], v[232:235], a[208:211]\n" \
"v_mfma_f32_32x32x16_bf16 v[224:239], a[216:219], a[216:219], 0\n" \
"v_cvt_pk_bf16_f32 v241, v242, v243\n" \
"v_mul_f32_e32 v244, v57, v244\n" \
"v_mul_f32_e32 v245, v57, v245\n" \
"v_mul_f32_e32 v246, v57, v246\n" \
"v_mul_f32_e32 v247, v57, v247\n" \
"v_mul_f32_e32 v248, v57, v248\n" \
"v_mul_f32_e32 v249, v57, v249\n" \
"v_cvt_pk_bf16_f32 v242, v244, v245\n" \
"v_cvt_pk_bf16_f32 v243, v246, v247\n" \
"buffer_store_dwordx4 v[240:243], v20, s[56:59], s71 offen offset:2048 sc1\n" \
"v_cvt_pk_bf16_f32 v248, v248, v249\n" \
"v_mul_f32_e32 v250, v57, v250\n" \
"v_mul_f32_e32 v251, v57, v251\n" \
"v_mfma_f32_16x16x32_bf16 a[208:211], a[216:219], v[240:243], a[208:211]\n" \
"v_cvt_pk_bf16_f32 v249, v250, v251\n" \
"v_mul_f32_e32 v252, v57, v252\n" \
"v_mul_f32_e32 v253, v57, v253\n" \
"v_mul_f32_e32 v254, v57, v254\n" \
"v_mul_f32_e32 v255, v57, v255\n" \
"v_cvt_pk_bf16_f32 v250, v252, v253\n" \
"v_cvt_pk_bf16_f32 v251, v254, v255\n" \
"buffer_store_dwordx4 v[248:251], v20, s[56:59], s71 offen offset:3072 sc1\n" \
"buffer_load_dwordx4 v53, s[8:11], s64 offen offset:3072 sc1 lds\n" \
"s_waitcnt vmcnt(16) lgkmcnt(0)\n" \
"s_barrier\n" \
"v_mfma_f32_16x16x32_bf16 a[208:211], a[216:219], v[248:251], a[208:211]\n" \
"v_mfma_f32_32x32x16_bf16 v[240:255], a[216:219], a[216:219], 0\n" \
"v_exp_f32_e32 v70, v70\n" \
"v_exp_f32_e32 v71, v71\n" \
"v_exp_f32_e32 v72, v72\n" \
"v_exp_f32_e32 v73, v73\n" \
"s_add_u32 s64, s64, 0x10000\n" \
"v_cmp_lt_f32_e64 s[72:73], s21, v62\n" \
"v_readlane_b32 s95, v11, 3\n" \
"v_accvgpr_read_b32 v63, a208\n" \
"v_cmp_u_f32_e32 vcc, v63, v63\n" \
"v_exp_f32_e32 v68, v68\n" \
"v_exp_f32_e32 v69, v69\n" \
"v_exp_f32_e32 v74, v74\n" \
"v_exp_f32_e32 v75, v75\n" \
"s_or_b64 vcc, s[72:73], vcc\n" \
"s_cselect_b32 s95, 0x40000000, s95\n" \
"s_cselect_b32 s94, 0x7f800000, 0\n" \
"v_cvt_pk_bf16_f32 v71, v70, v71\n" \
"v_exp_f32_e32 v66, v66\n" \
"v_exp_f32_e32 v67, v67\n" \
"v_exp_f32_e32 v64, v64\n" \
"v_exp_f32_e32 v65, v65\n" \
"v_cvt_pk_bf16_f32 v72, v72, v73\n" \
"v_exp_f32_e32 v76, v76\n" \
"v_exp_f32_e32 v77, v77\n" \
"v_exp_f32_e32 v78, v78\n" \
"v_exp_f32_e32 v79, v79\n" \
"v_cvt_pk_bf16_f32 v70, v68, v69\n" \
"v_cvt_pk_bf16_f32 v73, v74, v75\n" \
"v_add_u32_e32 v49, 0x10000, v48\n" \
"s_xor_b32 s65, s65, 0x8000\n" \
"v_cvt_pk_bf16_f32 v69, v66, v67\n" \
"v_cvt_pk_bf16_f32 v68, v64, v65\n" \
"v_cvt_pk_bf16_f32 v74, v76, v77\n" \
"v_cvt_pk_bf16_f32 v75, v78, v79\n" \
"v_xor_b32_e32 v48, 0x8000, v48\n" \
"s_sub_u32 s66, s19, 2\n" \
"s_branch L_2_%=\n" \
"L_f1_%=:\n" \
"ds_read_b128 a[160:163], v49 offset:0\n" \
"ds_read_b128 a[164:167], v49 offset:1024\n" \
"ds_read_b128 a[168:171], v49 offset:2048\n" \
"v_mfma_f32_16x16x32_bf16 v[104:107], a[128:131], a[80:83], v[36:39]\n" \
"v_cvt_pk_bf16_f32 v192, v192, v193\n" \
"v_cvt_pk_bf16_f32 v224, v224, v225\n" \
"v_cvt_pk_bf16_f32 v193, v194, v195\n" \
"v_cvt_pk_bf16_f32 v225, v226, v227\n" \
"ds_read_b128 a[172:175], v49 offset:3072\n" \
"v_cvt_pk_bf16_f32 v194, v196, v197\n" \
"v_cvt_pk_bf16_f32 v195, v198, v199\n" \
"v_cvt_pk_bf16_f32 v226, v228, v229\n" \
"v_cvt_pk_bf16_f32 v227, v230, v231\n" \
"v_mfma_f32_16x16x32_bf16 v[104:107], a[132:135], a[84:87], v[104:107]\n" \
"v_lshl_add_u32 v50, s15, 2, v20\n" \
"v_cvt_pk_bf16_f32 v196, v200, v201\n" \
"v_cvt_pk_bf16_f32 v197, v202, v203\n" \
"v_cvt_pk_bf16_f32 v198, v204, v205\n" \
"v_cvt_pk_bf16_f32 v199, v206, v207\n" \
"v_cvt_pk_bf16_f32 v228, v232, v233\n" \
"v_cvt_pk_bf16_f32 v229, v234, v235\n" \
"v_cvt_pk_bf16_f32 v230, v236, v237\n" \
"v_cvt_pk_bf16_f32 v231, v238, v239\n" \
"v_add_u32_e32 v50, 0x1a000, v50\n" \
"v_mov_b32_e32 v61, s62\n" \
"v_cvt_pk_bf16_f32 v200, v208, v209\n" \
"v_cvt_pk_bf16_f32 v201, v210, v211\n" \
"v_cvt_pk_bf16_f32 v202, v212, v213\n" \
"v_cvt_pk_bf16_f32 v203, v214, v215\n" \
"v_cvt_pk_bf16_f32 v204, v216, v217\n" \
"v_cvt_pk_bf16_f32 v205, v218, v219\n" \
"v_cvt_pk_bf16_f32 v206, v220, v221\n" \
"v_cvt_pk_bf16_f32 v207, v222, v223\n" \
"v_cvt_pk_bf16_f32 v232, v240, v241\n" \
"v_cvt_pk_bf16_f32 v233, v242, v243\n" \
"v_cvt_pk_bf16_f32 v234, v244, v245\n" \
"v_cvt_pk_bf16_f32 v235, v246, v247\n" \
"v_cvt_pk_bf16_f32 v236, v248, v249\n" \
"v_cvt_pk_bf16_f32 v237, v250, v251\n" \
"v_cvt_pk_bf16_f32 v238, v252, v253\n" \
"v_cvt_pk_bf16_f32 v239, v254, v255\n" \
"ds_write_b128 v50, v[192:195] offset:0\n" \
"v_mfma_f32_16x16x32_bf16 v[104:107], a[136:139], a[88:91], v[104:107]\n" \
"ds_read_b128 a[176:179], v49 offset:4096\n" \
"v_add_u32_e32 v56, 0x49000, v20\n" \
"v_mfma_f32_16x16x32_bf16 v[104:107], a[140:143], a[92:95], v[104:107]\n" \
"ds_write_b128 v50, v[196:199] offset:1024\n" \
"v_mfma_f32_16x16x32_bf16 v[108:111], a[156:159], a[92:95], v[36:39]\n" \
"ds_read_b128 a[180:183], v49 offset:5120\n" \
"ds_read_b128 a[184:187], v49 offset:6144\n" \
"v_mfma_f32_16x16x32_bf16 v[108:111], a[152:155], a[88:91], v[108:111]\n" \
"ds_write_b128 v50, v[200:203] offset:2048\n" \
"v_mfma_f32_16x16x32_bf16 v[108:111], a[148:151], a[84:87], v[108:111]\n" \
"v_and_b32_e32 v68, v12, v68\n" \
"v_and_b32_e32 v69, v13, v69\n" \
"v_mfma_f32_16x16x32_bf16 v[108:111], a[144:147], a[80:83], v[108:111]\n" \
"ds_write_b128 v50, v[204:207] offset:3072\n" \
"v_and_b32_e32 v70, v14, v70\n" \
"v_and_b32_e32 v71, v15, v71\n" \
"ds_read_b128 a[188:191], v49 offset:7168\n" \
"ds_read_b128 a[0:3], v20 offset:8192\n" \
"s_waitcnt lgkmcnt(9)\n" \
"v_mfma_f32_16x16x32_bf16 v[128:131], a[160:163], v[68:71], v[128:131]\n" \
"ds_write_b128 v50, v[224:227] offset:4096\n" \
"v_mfma_f32_16x16x32_bf16 v[160:163], a[160:163], v[72:75], v[160:163]\n" \
"ds_read_b128 a[4:7], v20 offset:9216\n" \
"ds_read_b128 a[8:11], v20 offset:10240\n" \
"v_mfma_f32_16x16x32_bf16 v[132:135], a[164:167], v[68:71], v[132:135]\n" \
"ds_write_b128 v50, v[228:231] offset:5120\n" \
"v_mfma_f32_16x16x32_bf16 v[164:167], a[164:167], v[72:75], v[164:167]\n" \
"ds_read_b128 a[12:15], v20 offset:11264\n" \
"ds_read_b128 a[28:31], v20 offset:15360\n" \
"v_mfma_f32_16x16x32_bf16 v[136:139], a[168:171], v[68:71], v[136:139]\n" \
"ds_write_b128 v50, v[232:235] offset:6144\n" \
"v_mfma_f32_16x16x32_bf16 v[168:171], a[168:171], v[72:75], v[168:171]\n" \
"ds_read_b128 a[24:27], v20 offset:14336\n" \
"ds_read_b128 a[20:23], v20 offset:13312\n" \
"v_mfma_f32_16x16x32_bf16 v[140:143], a[172:175], v[68:71], v[140:143]\n" \
"ds_write_b128 v50, v[236:239] offset:7168\n" \
"v_mfma_f32_16x16x32_bf16 v[172:175], a[172:175], v[72:75], v[172:175]\n" \
"ds_read_b128 a[16:19], v20 offset:12288\n" \
"s_waitcnt lgkmcnt(15)\n" \
"v_mfma_f32_16x16x32_bf16 v[144:147], a[176:179], v[68:71], v[144:147]\n" \
"v_xor_b32_e32 v48, 0x4000, v49\n" \
"ds_read_b32 v58, v18 offset:256\n" \
"v_mfma_f32_16x16x32_bf16 v[176:179], a[176:179], v[72:75], v[176:179]\n" \
"ds_read_b32 v59, v18 offset:384\n" \
"ds_read_b128 a[32:35], v48 offset:8192\n" \
"v_mfma_f32_16x16x32_bf16 v[148:151], a[180:183], v[68:71], v[148:151]\n" \
"ds_read_b128 a[36:39], v48 offset:9216\n" \
"ds_read_b128 a[40:43], v48 offset:10240\n" \
"v_mfma_f32_16x16x32_bf16 v[180:183], a[180:183], v[72:75], v[180:183]\n" \
"ds_read_b128 a[44:47], v48 offset:11264\n" \
"ds_read_b128 a[48:51], v48 offset:12288\n" \
"v_mfma_f32_16x16x32_bf16 v[152:155], a[184:187], v[68:71], v[152:155]\n" \
"ds_read_b128 a[52:55], v48 offset:13312\n" \
"ds_read_b128 a[56:59], v48 offset:14336\n" \
"v_mfma_f32_16x16x32_bf16 v[184:187], a[184:187], v[72:75], v[184:187]\n" \
"ds_read_b128 a[60:63], v48 offset:15360\n" \
"s_waitcnt lgkmcnt(15)\n" \
"v_mfma_f32_16x16x32_bf16 v[156:159], a[188:191], v[68:71], v[156:159]\n" \
"ds_read_b128 a[128:131], v49 offset:8192\n" \
"ds_read_b128 a[132:135], v49 offset:9216\n" \
"v_mfma_f32_16x16x32_bf16 v[188:191], a[188:191], v[72:75], v[188:191]\n" \
"ds_read_b128 a[136:139], v49 offset:10240\n" \
"ds_read_b128 a[140:143], v49 offset:11264\n" \
"v_smfmac_f32_16x16x64_bf16 a[192:195], a[200:203], v[68:75], v21\n" \
"ds_read_b128 a[144:147], v49 offset:12288\n" \
"ds_read_b128 a[148:151], v49 offset:13312\n" \
"v_mfma_f32_16x16x32_bf16 v[112:115], a[0:3], a[64:67], v[32:35]\n" \
"ds_read_b128 a[152:155], v49 offset:14336\n" \
"ds_read_b128 a[156:159], v49 offset:15360\n" \
"v_mfma_f32_16x16x32_bf16 v[112:115], a[4:7], a[68:71], v[112:115]\n" \
"v_accvgpr_read_b32 v40, a196\n" \
"v_accvgpr_read_b32 v24, a197\n" \
"v_mfma_f32_16x16x32_bf16 v[112:115], a[8:11], a[72:75], v[112:115]\n" \
"v_accvgpr_read_b32 v41, a198\n" \
"v_accvgpr_read_b32 v25, a199\n" \
"v_mfma_f32_16x16x32_bf16 v[112:115], a[12:15], a[76:79], v[112:115]\n" \
"v_add_f32_e32 v40, v40, v24\n" \
"v_add_f32_e32 v41, v41, v25\n" \
"v_mfma_f32_16x16x32_bf16 v[116:119], a[28:31], a[76:79], v[32:35]\n" \
"v_add_f32_e32 v40, s94, v40\n" \
"s_waitcnt lgkmcnt(15)\n" \
"v_mfma_f32_16x16x32_bf16 v[116:119], a[24:27], a[72:75], v[116:119]\n" \
"v_add_f32_e32 v41, s94, v41\n" \
"ds_write_b64 v19, v[40:41]\n" \
"v_mfma_f32_16x16x32_bf16 v[116:119], a[20:23], a[68:71], v[116:119]\n" \
"v_add_u32_e32 v52, s15, v20\n" \
"v_add_u32_e32 v51, 0x10000, v52\n" \
"v_mfma_f32_16x16x32_bf16 v[116:119], a[16:19], a[64:67], v[116:119]\n" \
"v_exp_f32_e32 v104, v104\n" \
"v_mfma_f32_16x16x32_bf16 v[124:127], a[16:19], a[80:83], v[36:39]\n" \
"v_exp_f32_e32 v105, v105\n" \
"v_mfma_f32_16x16x32_bf16 v[124:127], a[20:23], a[84:87], v[124:127]\n" \
"v_exp_f32_e32 v106, v106\n" \
"v_mfma_f32_16x16x32_bf16 v[124:127], a[24:27], a[88:91], v[124:127]\n" \
"v_exp_f32_e32 v107, v107\n" \
"v_mfma_f32_16x16x32_bf16 v[124:127], a[28:31], a[92:95], v[124:127]\n" \
"v_cvt_pk_bf16_f32 v104, v104, v105\n" \
"v_cvt_pk_bf16_f32 v105, v106, v107\n" \
"v_mfma_f32_16x16x32_bf16 v[120:123], a[12:15], a[92:95], v[36:39]\n" \
"v_exp_f32_e32 v108, v108\n" \
"v_mfma_f32_16x16x32_bf16 v[120:123], a[8:11], a[88:91], v[120:123]\n" \
"v_exp_f32_e32 v109, v109\n" \
"v_mfma_f32_16x16x32_bf16 v[120:123], a[4:7], a[84:87], v[120:123]\n" \
"v_exp_f32_e32 v110, v110\n" \
"v_mfma_f32_16x16x32_bf16 v[120:123], a[0:3], a[80:83], v[120:123]\n" \
"v_exp_f32_e32 v111, v111\n" \
"s_waitcnt lgkmcnt(0)\n" \
"s_barrier\n" \
"ds_read_b128 v[208:211], v52 offset:0\n" \
"ds_read_b128 v[212:215], v52 offset:1024\n" \
"ds_read_b128 v[216:219], v52 offset:2048\n" \
"ds_read_b128 v[220:223], v52 offset:3072\n" \
"ds_read_b128 v[240:243], v51 offset:32768\n" \
"ds_read_b128 v[244:247], v51 offset:33792\n" \
"ds_read_b128 v[248:251], v51 offset:34816\n" \
"ds_read_b128 v[252:255], v51 offset:35840\n" \
"v_cvt_pk_bf16_f32 v106, v108, v109\n" \
"v_cvt_pk_bf16_f32 v107, v110, v111\n" \
"v_and_b32_e32 v104, v12, v104\n" \
"v_and_b32_e32 v105, v13, v105\n" \
"v_and_b32_e32 v106, v14, v106\n" \
"s_waitcnt vmcnt(0)\n" \
"s_mov_b64 exec, 1\n" \
"buffer_store_dword v61, off, s[56:59], s95 offset:0 sc1\n" \
"s_mov_b64 exec, -1\n" \
"v_and_b32_e32 v107, v15, v107\n" \
"v_xor_b32_e32 v63, 0x400, v19\n" \
"ds_read_b64 v[42:43], v63\n" \
"v_mfma_f32_16x16x32_bf16 a[192:195], a[212:215], v[104:107], a[192:195]\n" \
"v_mov_b32_e32 v194, v28\n" \
"v_mov_b32_e32 v195, v29\n" \
"v_mfma_f32_16x16x32_bf16 v[160:163], a[128:131], v[104:107], v[160:163]\n" \
"v_mov_b32_e32 v196, 0\n" \
"v_mov_b32_e32 v197, 0\n" \
"v_mfma_f32_16x16x32_bf16 v[164:167], a[132:135], v[104:107], v[164:167]\n" \
"v_mov_b32_e32 v192, 0\n" \
"v_mov_b32_e32 v193, 0\n" \
"v_mfma_f32_16x16x32_bf16 v[168:171], a[136:139], v[104:107], v[168:171]\n" \
"v_readlane_b32 s93, v11, 22\n" \
"v_readlane_b32 s92, v11, 27\n" \
"v_mfma_f32_16x16x32_bf16 v[172:175], a[140:143], v[104:107], v[172:175]\n" \
"buffer_load_dwordx4 a[96:99], v20, s[56:59], s60 offen offset:0 sc1\n" \
"v_mfma_f32_16x16x32_bf16 v[176:179], a[144:147], v[104:107], v[176:179]\n" \
"v_exp_f32_e32 v118, v118\n" \
"v_mfma_f32_16x16x32_bf16 v[180:183], a[148:151], v[104:107], v[180:183]\n" \
"v_exp_f32_e32 v119, v119\n" \
"v_mfma_f32_16x16x32_bf16 v[184:187], a[152:155], v[104:107], v[184:187]\n" \
"s_add_u32 m0, s93, 0x0\n" \
"v_cvt_pk_bf16_f32 v119, v118, v119\n" \
"v_mfma_f32_16x16x32_bf16 v[188:191], a[156:159], v[104:107], v[188:191]\n" \
"v_exp_f32_e32 v120, v120\n" \
"s_waitcnt lgkmcnt(1)\n" \
"s_barrier\n" \
"buffer_load_dwordx4 a[112:115], v56, s[56:59], s60 offen offset:0 sc1\n" \
"v_mfma_f32_16x16x32_bf16 v[128:131], v[194:197], v[208:211], v[128:131]\n" \
"v_exp_f32_e32 v121, v121\n" \
"v_mfma_f32_16x16x32_bf16 v[132:135], v[192:195], v[208:211], v[132:135]\n" \
"v_exp_f32_e32 v116, v116\n" \
"v_mfma_f32_16x16x32_bf16 v[136:139], v[194:197], v[212:215], v[136:139]\n" \
"v_exp_f32_e32 v117, v117\n" \
"v_mfma_f32_16x16x32_bf16 v[140:143], v[192:195], v[212:215], v[140:143]\n" \
"v_exp_f32_e32 v122, v122\n" \
"v_mfma_f32_16x16x32_bf16 v[144:147], v[194:197], v[216:219], v[144:147]\n" \
"v_exp_f32_e32 v123, v123\n" \
"v_mfma_f32_16x16x32_bf16 v[148:151], v[192:195], v[216:219], v[148:151]\n" \
"buffer_load_dwordx4 a[100:103], v20, s[56:59], s60 offen offset:1024 sc1\n" \
"v_mfma_f32_16x16x32_bf16 v[152:155], v[194:197], v[220:223], v[152:155]\n" \
"v_exp_f32_e32 v114, v114\n" \
"v_mfma_f32_16x16x32_bf16 v[156:159], v[192:195], v[220:223], v[156:159]\n" \
"v_exp_f32_e32 v115, v115\n" \
"v_mfma_f32_16x16x32_bf16 v[160:163], v[194:197], v[240:243], v[160:163]\n" \
"v_exp_f32_e32 v112, v112\n" \
"v_mfma_f32_16x16x32_bf16 v[164:167], v[192:195], v[240:243], v[164:167]\n" \
"v_exp_f32_e32 v113, v113\n" \
"v_mfma_f32_16x16x32_bf16 v[168:171], v[194:197], v[244:247], v[168:171]\n" \
"v_cvt_pk_bf16_f32 v120, v120, v121\n" \
"v_cvt_pk_bf16_f32 v118, v116, v117\n" \
"v_mfma_f32_16x16x32_bf16 v[172:175], v[192:195], v[244:247], v[172:175]\n" \
"buffer_load_dwordx4 a[116:119], v56, s[56:59], s60 offen offset:1024 sc1\n" \
"v_mfma_f32_16x16x32_bf16 v[176:179], v[194:197], v[248:251], v[176:179]\n" \
"v_exp_f32_e32 v124, v124\n" \
"v_mfma_f32_16x16x32_bf16 v[180:183], v[192:195], v[248:251], v[180:183]\n" \
"v_exp_f32_e32 v125, v125\n" \
"v_mfma_f32_16x16x32_bf16 v[184:187], v[194:197], v[252:255], v[184:187]\n" \
"v_exp_f32_e32 v126, v126\n" \
"v_mfma_f32_16x16x32_bf16 v[188:191], v[192:195], v[252:255], v[188:191]\n" \
"v_exp_f32_e32 v127, v127\n" \
"v_cvt_pk_bf16_f32 v121, v122, v123\n" \
"v_cvt_pk_bf16_f32 v117, v114, v115\n" \
"v_cvt_pk_bf16_f32 v116, v112, v113\n" \
"v_cvt_pk_bf16_f32 v122, v124, v125\n" \
"v_cvt_pk_bf16_f32 v123, v126, v127\n" \
"buffer_load_dwordx4 a[104:107], v20, s[56:59], s60 offen offset:2048 sc1\n" \
"s_nop 0\n" \
"v_smfmac_f32_16x16x64_bf16 a[192:195], a[200:203], v[116:123], v21\n" \
"v_mfma_f32_16x16x32_bf16 v[128:131], a[32:35], v[116:119], v[128:131]\n" \
"v_mfma_f32_16x16x32_bf16 v[160:163], a[32:35], v[120:123], v[160:163]\n" \
"v_mfma_f32_16x16x32_bf16 v[132:135], a[36:39], v[116:119], v[132:135]\n" \
"v_mfma_f32_16x16x32_bf16 v[164:167], a[36:39], v[120:123], v[164:167]\n" \
"v_mfma_f32_16x16x32_bf16 v[136:139], a[40:43], v[116:119], v[136:139]\n" \
"buffer_load_dwordx4 a[120:123], v56, s[56:59], s60 offen offset:2048 sc1\n" \
"v_mfma_f32_16x16x32_bf16 v[168:171], a[40:43], v[120:123], v[168:171]\n" \
"v_mfma_f32_16x16x32_bf16 v[140:143], a[44:47], v[116:119], v[140:143]\n" \
"v_accvgpr_read_b32 v54, a192\n" \
"v_accvgpr_read_b32 v22, a193\n" \
"v_mfma_f32_16x16x32_bf16 v[172:175], a[44:47], v[120:123], v[172:175]\n" \
"v_add_f32_e32 v54, v54, v22\n" \
"s_waitcnt lgkmcnt(0)\n" \
"v_add_f32_e32 v54, v54, v42\n" \
"v_mfma_f32_16x16x32_bf16 v[144:147], a[48:51], v[116:119], v[144:147]\n" \
"v_max_f32_e32 v62, v62, v54\n" \
"v_accvgpr_read_b32 v55, a194\n" \
"v_mfma_f32_16x16x32_bf16 v[176:179], a[48:51], v[120:123], v[176:179]\n" \
"v_rcp_f32_e32 v54, v54\n" \
"buffer_load_dwordx4 a[108:111], v20, s[56:59], s60 offen offset:3072 sc1\n" \
"v_mfma_f32_16x16x32_bf16 v[148:151], a[52:55], v[116:119], v[148:151]\n" \
"v_mul_f32_e32 v54, v54, v58\n" \
"v_mul_f32_e32 v128, v54, v128\n" \
"v_mfma_f32_16x16x32_bf16 v[180:183], a[52:55], v[120:123], v[180:183]\n" \
"v_mul_f32_e32 v129, v54, v129\n" \
"v_mul_f32_e32 v130, v54, v130\n" \
"v_mul_f32_e32 v131, v54, v131\n" \
"v_mul_f32_e32 v132, v54, v132\n" \
"v_mul_f32_e32 v133, v54, v133\n" \
"v_mul_f32_e32 v134, v54, v134\n" \
"v_mul_f32_e32 v135, v54, v135\n" \
"v_cvt_pk_bf16_f32 v64, v128, v129\n" \
"v_cvt_pk_bf16_f32 v65, v130, v131\n" \
"v_cvt_pk_bf16_f32 v66, v132, v133\n" \
"v_cvt_pk_bf16_f32 v67, v134, v135\n" \
"v_accvgpr_read_b32 v23, a195\n" \
"v_mfma_f32_16x16x32_bf16 v[152:155], a[56:59], v[116:119], v[152:155]\n" \
"v_mfma_f32_16x16x32_bf16 a[208:211], a[216:219], v[64:67], a[208:211]\n" \
"buffer_load_dwordx4 a[124:127], v56, s[56:59], s60 offen offset:3072 sc1\n" \
"v_mul_f32_e32 v136, v54, v136\n" \
"v_mul_f32_e32 v137, v54, v137\n" \
"v_mul_f32_e32 v138, v54, v138\n" \
"v_mul_f32_e32 v139, v54, v139\n" \
"v_mul_f32_e32 v140, v54, v140\n" \
"v_mul_f32_e32 v141, v54, v141\n" \
"v_mul_f32_e32 v142, v54, v142\n" \
"v_mul_f32_e32 v143, v54, v143\n" \
"v_add_f32_e32 v55, v55, v23\n" \
"v_cvt_pk_bf16_f32 v68, v136, v137\n" \
"v_cvt_pk_bf16_f32 v69, v138, v139\n" \
"v_cvt_pk_bf16_f32 v70, v140, v141\n" \
"v_cvt_pk_bf16_f32 v71, v142, v143\n" \
"v_add_f32_e32 v55, v55, v43\n" \
"v_mul_f32_e32 v144, v54, v144\n" \
"v_mfma_f32_16x16x32_bf16 a[208:211], a[216:219], v[68:71], a[208:211]\n" \
"v_mul_f32_e32 v145, v54, v145\n" \
"v_mul_f32_e32 v146, v54, v146\n" \
"v_mul_f32_e32 v147, v54, v147\n" \
"v_mul_f32_e32 v148, v54, v148\n" \
"buffer_load_dwordx4 v0, s[80:83], s92 offen offset:0 lds\n" \
"v_mul_f32_e32 v149, v54, v149\n" \
"v_mul_f32_e32 v150, v54, v150\n" \
"v_mul_f32_e32 v151, v54, v151\n" \
"v_max_f32_e32 v62, v62, v55\n" \
"v_cvt_pk_bf16_f32 v72, v144, v145\n" \
"v_cvt_pk_bf16_f32 v73, v146, v147\n" \
"v_cvt_pk_bf16_f32 v74, v148, v149\n" \
"v_cvt_pk_bf16_f32 v75, v150, v151\n" \
"v_rcp_f32_e32 v55, v55\n" \
"v_mfma_f32_16x16x32_bf16 v[184:187], a[56:59], v[120:123], v[184:187]\n" \
"v_mfma_f32_16x16x32_bf16 a[208:211], a[216:219], v[72:75], a[208:211]\n" \
"v_mul_f32_e32 v152, v54, v152\n" \
"v_mul_f32_e32 v153, v54, v153\n" \
"v_mul_f32_e32 v154, v54, v154\n" \
"v_mul_f32_e32 v155, v54, v155\n" \
"v_cvt_pk_bf16_f32 v76, v152, v153\n" \
"buffer_load_dwordx4 v1, s[80:83], s92 offen offset:1024 lds\n" \
"v_cvt_pk_bf16_f32 v77, v154, v155\n" \
"v_mul_f32_e32 v55, v55, v59\n" \
"v_mul_f32_e32 v160, v55, v160\n" \
"v_mul_f32_e32 v161, v55, v161\n" \
"v_mul_f32_e32 v162, v55, v162\n" \
"v_mul_f32_e32 v163, v55, v163\n" \
"v_mul_f32_e32 v164, v55, v164\n" \
"v_mul_f32_e32 v165, v55, v165\n" \
"v_mul_f32_e32 v166, v55, v166\n" \
"v_mul_f32_e32 v167, v55, v167\n" \
"v_cvt_pk_bf16_f32 v80, v160, v161\n" \
"v_cvt_pk_bf16_f32 v81, v162, v163\n" \
"v_cvt_pk_bf16_f32 v82, v164, v165\n" \
"v_cvt_pk_bf16_f32 v83, v166, v167\n" \
"v_mfma_f32_16x16x32_bf16 v[156:159], a[60:63], v[116:119], v[156:159]\n" \
"v_mul_f32_e32 v168, v55, v168\n" \
"v_mul_f32_e32 v169, v55, v169\n" \
"v_mul_f32_e32 v170, v55, v170\n" \
"v_mul_f32_e32 v171, v55, v171\n" \
"v_mul_f32_e32 v172, v55, v172\n" \
"buffer_load_dwordx4 v2, s[80:83], s92 offen offset:2048 lds\n" \
"v_mul_f32_e32 v173, v55, v173\n" \
"v_mul_f32_e32 v174, v55, v174\n" \
"v_mul_f32_e32 v156, v54, v156\n" \
"v_mul_f32_e32 v157, v54, v157\n" \
"v_mul_f32_e32 v158, v54, v158\n" \
"v_mul_f32_e32 v159, v54, v159\n" \
"v_cvt_pk_bf16_f32 v78, v156, v157\n" \
"v_cvt_pk_bf16_f32 v79, v158, v159\n" \
"v_mul_f32_e32 v175, v55, v175\n" \
"v_cvt_pk_bf16_f32 v84, v168, v169\n" \
"v_mfma_f32_16x16x32_bf16 a[208:211], a[216:219], v[76:79], a[208:211]\n" \
"v_mfma_f32_16x16x32_bf16 a[208:211], a[216:219], v[80:83], a[208:211]\n" \
"v_cvt_pk_bf16_f32 v85, v170, v171\n" \
"v_cvt_pk_bf16_f32 v86, v172, v173\n" \
"v_cvt_pk_bf16_f32 v87, v174, v175\n" \
"v_mul_f32_e32 v176, v55, v176\n" \
"v_mul_f32_e32 v177, v55, v177\n" \
"buffer_load_dwordx4 v3, s[80:83], s92 offen offset:3072 lds\n" \
"s_add_u32 m0, s93, 0x1000\n" \
"v_mfma_f32_16x16x32_bf16 a[208:211], a[216:219], v[84:87], a[208:211]\n" \
"v_mul_f32_e32 v178, v55, v178\n" \
"v_mul_f32_e32 v179, v55, v179\n" \
"v_mul_f32_e32 v180, v55, v180\n" \
"v_mul_f32_e32 v181, v55, v181\n" \
"v_mul_f32_e32 v182, v55, v182\n" \
"v_mul_f32_e32 v183, v55, v183\n" \
"v_cvt_pk_bf16_f32 v96, v176, v177\n" \
"v_cvt_pk_bf16_f32 v97, v178, v179\n" \
"v_cvt_pk_bf16_f32 v98, v180, v181\n" \
"v_cvt_pk_bf16_f32 v99, v182, v183\n" \
"v_mfma_f32_16x16x32_bf16 v[188:191], a[60:63], v[120:123], v[188:191]\n" \
"v_mul_f32_e32 v184, v55, v184\n" \
"v_mfma_f32_16x16x32_bf16 a[208:211], a[216:219], v[96:99], a[208:211]\n" \
"v_mul_f32_e32 v185, v55, v185\n" \
"v_mul_f32_e32 v186, v55, v186\n" \
"buffer_load_dwordx4 v4, s[80:83], s92 offen offset:0 lds\n" \
"v_mul_f32_e32 v187, v55, v187\n" \
"v_cvt_pk_bf16_f32 v100, v184, v185\n" \
"v_cvt_pk_bf16_f32 v101, v186, v187\n" \
"v_mul_f32_e32 v188, v55, v188\n" \
"v_mul_f32_e32 v189, v55, v189\n" \
"v_mul_f32_e32 v190, v55, v190\n" \
"v_mul_f32_e32 v191, v55, v191\n" \
"v_cvt_pk_bf16_f32 v102, v188, v189\n" \
"v_cvt_pk_bf16_f32 v103, v190, v191\n" \
"v_cmp_lt_f32_e64 s[72:73], s21, v62\n" \
"buffer_load_dwordx4 v5, s[80:83], s92 offen offset:1024 lds\n" \
"v_mfma_f32_16x16x32_bf16 a[208:211], a[216:219], v[100:103], a[208:211]\n" \
"buffer_load_dwordx4 v6, s[80:83], s92 offen offset:2048 lds\n" \
"buffer_load_dwordx4 v7, s[80:83], s92 offen offset:3072 lds\n" \
"s_add_u32 m0, s93, 0x2000\n" \
"s_nop 0\n" \
"buffer_load_dwordx4 v8, s[80:83], s92 offen offset:0 lds\n" \
"s_nop 2\n" \
"v_accvgpr_read_b32 v63, a208\n" \
"v_cmp_u_f32_e32 vcc, v63, v63\n" \
"s_nop 0\n" \
"s_or_b64 vcc, s[72:73], vcc\n" \
"s_cbranch_scc0 L_out_%=\n" \
"s_branch L_end_%=\n" \
".fill (L_ab + 24576 - .) / 4, 4, 0xBF800000\n" \
".fill (L_ab + 24576 - .) / 4, 4, 0xBF800000\n" \
"L_ap6:\n" \
"s_setpc_b64 s[88:89]\n" \
"L_gs7:\n" \
"v_mfma_f32_16x16x32_bf16 v[104:107], a[128:131], a[16:19], v[36:39]\n" \
"s_add_u32 s68, s60, 0xfff6e000\n" \
"s_add_u32 s69, s60, 0xfffb7000\n" \
"s_add_u32 s70, s60, 0x0\n" \
"s_add_u32 s71, s60, 0x49000\n" \
"v_mfma_f32_16x16x32_bf16 v[104:107], a[132:135], a[20:23], v[104:107]\n" \
"s_xor_b32 m0, s65, 0x12000\n" \
"ds_read_b128 a[160:163], v49 offset:0\n" \
"v_mfma_f32_16x16x32_bf16 v[104:107], a[136:139], a[24:27], v[104:107]\n" \
"buffer_load_dwordx4 v50, s[8:11], s64 offen offset:0 sc1 lds\n" \
"v_mfma_f32_16x16x32_bf16 v[104:107], a[140:143], a[28:31], v[104:107]\n" \
"ds_read_b128 a[164:167], v49 offset:1024\n" \
"ds_read_b128 a[168:171], v49 offset:2048\n" \
"v_mfma_f32_16x16x32_bf16 v[108:111], a[156:159], a[28:31], v[36:39]\n" \
"ds_read_b128 a[172:175], v49 offset:3072\n" \
"ds_read_b128 a[176:179], v49 offset:4096\n" \
"v_mfma_f32_16x16x32_bf16 v[108:111], a[152:155], a[24:27], v[108:111]\n" \
"s_mov_b32 s65, s13\n" \
"v_add_u32_e32 v63, s15, v20\n" \
"v_mfma_f32_16x16x32_bf16 v[108:111], a[148:151], a[20:23], v[108:111]\n" \
"buffer_load_dwordx4 v50, s[8:11], s64 offen offset:1024 sc1 lds\n" \
"v_mfma_f32_16x16x32_bf16 v[108:111], a[144:147], a[16:19], v[108:111]\n" \
"s_add_u32 s67, s14, 40960\n" \
"v_add_u32_e32 v50, 0x6000, v63\n" \
"v_mfma_f32_16x16x32_bf16 v[116:119], a[144:147], a[32:35], v[40:43]\n" \
"s_xor_b32 m0, s65, 0x8000\n" \
"ds_read_b128 a[180:183], v49 offset:5120\n" \
"v_mfma_f32_16x16x32_bf16 v[116:119], a[148:151], a[36:39], v[116:119]\n" \
"ds_read_b128 a[184:187], v49 offset:6144\n" \
"v_and_b32_e32 v68, v12, v68\n" \
"v_mfma_f32_16x16x32_bf16 v[116:119], a[152:155], a[40:43], v[116:119]\n" \
"v_and_b32_e32 v69, v13, v69\n" \
"v_and_b32_e32 v70, v14, v70\n" \
"v_mfma_f32_16x16x32_bf16 v[116:119], a[156:159], a[44:47], v[116:119]\n" \
"buffer_load_dwordx4 v50, s[8:11], s67 offen offset:0 sc1 lds\n" \
"v_mfma_f32_16x16x32_bf16 v[112:115], a[140:143], a[44:47], v[40:43]\n" \
"v_and_b32_e32 v71, v15, v71\n" \
"ds_read_b128 a[188:191], v49 offset:7168\n" \
"v_mfma_f32_16x16x32_bf16 v[112:115], a[136:139], a[40:43], v[112:115]\n" \
"v_exp_f32_e32 v86, v86\n" \
"v_mfma_f32_16x16x32_bf16 v[112:115], a[132:135], a[36:39], v[112:115]\n" \
"v_exp_f32_e32 v87, v87\n" \
"v_mfma_f32_16x16x32_bf16 v[112:115], a[128:131], a[32:35], v[112:115]\n" \
"v_exp_f32_e32 v84, v84\n" \
"v_mfma_f32_16x16x32_bf16 v[120:123], a[128:131], a[48:51], v[44:47]\n" \
"buffer_load_dwordx4 v50, s[8:11], s67 offen offset:1024 sc1 lds\n" \
"v_mfma_f32_16x16x32_bf16 v[120:123], a[132:135], a[52:55], v[120:123]\n" \
"v_exp_f32_e32 v85, v85\n" \
"v_mfma_f32_16x16x32_bf16 v[120:123], a[136:139], a[56:59], v[120:123]\n" \
"v_cvt_pk_bf16_f32 v87, v86, v87\n" \
"v_cvt_pk_bf16_f32 v86, v84, v85\n" \
"v_mfma_f32_16x16x32_bf16 v[120:123], a[140:143], a[60:63], v[120:123]\n" \
"v_exp_f32_e32 v82, v82\n" \
"v_mfma_f32_16x16x32_bf16 v[124:127], a[156:159], a[60:63], v[44:47]\n" \
"v_exp_f32_e32 v83, v83\n" \
"v_mfma_f32_16x16x32_bf16 v[124:127], a[152:155], a[56:59], v[124:127]\n" \
"v_exp_f32_e32 v80, v80\n" \
"v_mfma_f32_16x16x32_bf16 v[124:127], a[148:151], a[52:55], v[124:127]\n" \
"buffer_load_dwordx4 v50, s[8:11], s67 offen offset:2048 sc1 lds\n" \
"v_mfma_f32_16x16x32_bf16 v[124:127], a[144:147], a[48:51], v[124:127]\n" \
"v_cvt_pk_bf16_f32 v85, v82, v83\n" \
"s_waitcnt lgkmcnt(1)\n" \
"v_mfma_f32_16x16x32_bf16 v[128:131], a[160:163], v[68:71], v[128:131]\n" \
"v_exp_f32_e32 v81, v81\n" \
"v_mfma_f32_16x16x32_bf16 v[160:163], a[160:163], v[72:75], v[160:163]\n" \
"v_cvt_pk_bf16_f32 v84, v80, v81\n" \
"s_mov_b32 s64, s14\n" \
"v_mfma_f32_16x16x32_bf16 v[132:135], a[164:167], v[68:71], v[132:135]\n" \
"v_exp_f32_e32 v88, v88\n" \
"v_mfma_f32_16x16x32_bf16 v[164:167], a[164:167], v[72:75], v[164:167]\n" \
"v_exp_f32_e32 v89, v89\n" \
"v_mfma_f32_16x16x32_bf16 v[136:139], a[168:171], v[68:71], v[136:139]\n" \
"buffer_load_dwordx4 v50, s[8:11], s67 offen offset:3072 sc1 lds\n" \
"v_mfma_f32_16x16x32_bf16 v[168:171], a[168:171], v[72:75], v[168:171]\n" \
"v_exp_f32_e32 v90, v90\n" \
"v_mfma_f32_16x16x32_bf16 v[140:143], a[172:175], v[68:71], v[140:143]\n" \
"v_exp_f32_e32 v91, v91\n" \
"v_mfma_f32_16x16x32_bf16 v[172:175], a[172:175], v[72:75], v[172:175]\n" \
"v_cvt_pk_bf16_f32 v88, v88, v89\n" \
"v_cvt_pk_bf16_f32 v89, v90, v91\n" \
"v_mfma_f32_16x16x32_bf16 v[144:147], a[176:179], v[68:71], v[144:147]\n" \
"v_exp_f32_e32 v92, v92\n" \
"v_mfma_f32_16x16x32_bf16 v[176:179], a[176:179], v[72:75], v[176:179]\n" \
"v_exp_f32_e32 v93, v93\n" \
"v_mfma_f32_16x16x32_bf16 v[148:151], a[180:183], v[68:71], v[148:151]\n" \
"v_exp_f32_e32 v94, v94\n" \
"v_mfma_f32_16x16x32_bf16 v[180:183], a[180:183], v[72:75], v[180:183]\n" \
"v_exp_f32_e32 v95, v95\n" \
"v_mfma_f32_16x16x32_bf16 v[152:155], a[184:187], v[68:71], v[152:155]\n" \
"v_cvt_pk_bf16_f32 v90, v92, v93\n" \
"v_cvt_pk_bf16_f32 v91, v94, v95\n" \
"v_mfma_f32_16x16x32_bf16 v[184:187], a[184:187], v[72:75], v[184:187]\n" \
"v_add_u32_e32 v51, 0x12000, v63\n" \
"s_waitcnt lgkmcnt(0)\n" \
"v_mfma_f32_16x16x32_bf16 v[156:159], a[188:191], v[68:71], v[156:159]\n" \
"s_xor_b32 m0, s65, 0xa000\n" \
"ds_read_b32 v58, v18 offset:512\n" \
"v_mfma_f32_16x16x32_bf16 v[188:191], a[188:191], v[72:75], v[188:191]\n" \
"buffer_load_dwordx4 v51, s[8:11], s64 offen offset:0 sc1 lds\n" \
"v_smfmac_f32_16x16x64_bf16 a[192:195], a[200:203], v[68:75], v21\n" \
"ds_read_b32 v59, v18 offset:640\n" \
"ds_read_b32 v60, v18 offset:768\n" \
"v_mfma_f32_16x16x32_bf16 v[220:223], a[188:191], v[84:87], v[220:223]\n" \
"ds_read_b32 v61, v18 offset:896\n" \
"ds_read_b128 a[128:131], v48 offset:0\n" \
"v_mfma_f32_16x16x32_bf16 v[252:255], a[188:191], v[88:91], v[252:255]\n" \
"ds_read_b128 a[144:147], v48 offset:4096\n" \
"ds_read_b128 a[132:135], v48 offset:1024\n" \
"v_mfma_f32_16x16x32_bf16 v[216:219], a[184:187], v[84:87], v[216:219]\n" \
"buffer_load_dwordx4 v51, s[8:11], s64 offen offset:1024 sc1 lds\n" \
"v_mfma_f32_16x16x32_bf16 v[248:251], a[184:187], v[88:91], v[248:251]\n" \
"ds_read_b128 a[148:151], v48 offset:5120\n" \
"ds_read_b128 a[136:139], v48 offset:2048\n" \
"v_mfma_f32_16x16x32_bf16 v[212:215], a[180:183], v[84:87], v[212:215]\n" \
"ds_read_b128 a[152:155], v48 offset:6144\n" \
"ds_read_b128 a[140:143], v48 offset:3072\n" \
"v_mfma_f32_16x16x32_bf16 v[244:247], a[180:183], v[88:91], v[244:247]\n" \
"ds_read_b128 a[156:159], v48 offset:7168\n" \
"v_add_u32_e32 v52, 0x14000, v63\n" \
"v_mfma_f32_16x16x32_bf16 v[208:211], a[176:179], v[84:87], v[208:211]\n" \
"buffer_load_dwordx4 v51, s[8:11], s64 offen offset:2048 sc1 lds\n" \
"v_mfma_f32_16x16x32_bf16 v[240:243], a[176:179], v[88:91], v[240:243]\n" \
"v_exp_f32_e32 v104, v104\n" \
"v_mfma_f32_16x16x32_bf16 v[204:207], a[172:175], v[84:87], v[204:207]\n" \
"v_exp_f32_e32 v105, v105\n" \
"v_mfma_f32_16x16x32_bf16 v[236:239], a[172:175], v[88:91], v[236:239]\n" \
"v_exp_f32_e32 v106, v106\n" \
"v_mfma_f32_16x16x32_bf16 v[200:203], a[168:171], v[84:87], v[200:203]\n" \
"buffer_load_dwordx4 v51, s[8:11], s64 offen offset:3072 sc1 lds\n" \
"v_mfma_f32_16x16x32_bf16 v[232:235], a[168:171], v[88:91], v[232:235]\n" \
"v_exp_f32_e32 v107, v107\n" \
"v_mfma_f32_16x16x32_bf16 v[196:199], a[164:167], v[84:87], v[196:199]\n" \
"v_cvt_pk_bf16_f32 v104, v104, v105\n" \
"v_cvt_pk_bf16_f32 v105, v106, v107\n" \
"v_mfma_f32_16x16x32_bf16 v[228:231], a[164:167], v[88:91], v[228:231]\n" \
"v_exp_f32_e32 v108, v108\n" \
"v_mfma_f32_16x16x32_bf16 v[192:195], a[160:163], v[84:87], v[192:195]\n" \
"v_exp_f32_e32 v109, v109\n" \
"v_mfma_f32_16x16x32_bf16 v[224:227], a[160:163], v[88:91], v[224:227]\n" \
"v_exp_f32_e32 v110, v110\n" \
"v_smfmac_f32_16x16x64_bf16 a[196:199], a[200:203], v[84:91], v21\n" \
"v_exp_f32_e32 v111, v111\n" \
"v_cvt_pk_bf16_f32 v106, v108, v109\n" \
"v_cvt_pk_bf16_f32 v107, v110, v111\n" \
"v_and_b32_e32 v104, v12, v104\n" \
"v_and_b32_e32 v105, v13, v105\n" \
"v_and_b32_e32 v106, v14, v106\n" \
"v_and_b32_e32 v107, v15, v107\n" \
"v_accvgpr_read_b32 v54, a192\n" \
"v_accvgpr_read_b32 v22, a193\n" \
"v_readlane_b32 s93, v11, 19\n" \
"v_readlane_b32 s80, v11, 11\n" \
"v_readlane_b32 s81, v11, 12\n" \
"s_mov_b32 s82, 0x47fca0\n" \
"s_mov_b32 s83, 0x20000\n" \
"v_readlane_b32 s92, v11, 26\n" \
"v_exp_f32_e32 v118, v118\n" \
"v_exp_f32_e32 v119, v119\n" \
"v_exp_f32_e32 v116, v116\n" \
"v_exp_f32_e32 v117, v117\n" \
"v_cvt_pk_bf16_f32 v119, v118, v119\n" \
"v_exp_f32_e32 v114, v114\n" \
"v_exp_f32_e32 v115, v115\n" \
"v_exp_f32_e32 v112, v112\n" \
"v_cvt_pk_bf16_f32 v118, v116, v117\n" \
"s_waitcnt vmcnt(10) lgkmcnt(0)\n" \
"s_barrier\n" \
"v_mfma_f32_16x16x32_bf16 v[80:83], a[128:131], a[32:35], v[40:43]\n" \
"ds_read_b128 a[160:163], v49 offset:8192\n" \
"ds_read_b128 a[164:167], v49 offset:9216\n" \
"v_mfma_f32_16x16x32_bf16 v[80:83], a[132:135], a[36:39], v[80:83]\n" \
"ds_read_b128 a[168:171], v49 offset:10240\n" \
"s_xor_b32 m0, s65, 0x18000\n" \
"v_mfma_f32_16x16x32_bf16 v[80:83], a[136:139], a[40:43], v[80:83]\n" \
"buffer_load_dwordx4 v52, s[8:11], s64 offen offset:0 sc1 lds\n" \
"v_mfma_f32_16x16x32_bf16 v[80:83], a[140:143], a[44:47], v[80:83]\n" \
"ds_read_b128 a[172:175], v49 offset:11264\n" \
"ds_read_b128 a[176:179], v49 offset:12288\n" \
"v_mfma_f32_16x16x32_bf16 v[84:87], a[156:159], a[44:47], v[40:43]\n" \
"ds_read_b128 a[180:183], v49 offset:13312\n" \
"ds_read_b128 a[184:187], v49 offset:14336\n" \
"v_mfma_f32_16x16x32_bf16 v[84:87], a[152:155], a[40:43], v[84:87]\n" \
"ds_read_b128 a[188:191], v49 offset:15360\n" \
"v_cvt_pk_bf16_f32 v117, v114, v115\n" \
"v_mfma_f32_16x16x32_bf16 v[84:87], a[148:151], a[36:39], v[84:87]\n" \
"buffer_load_dwordx4 v52, s[8:11], s64 offen offset:1024 sc1 lds\n" \
"v_mfma_f32_16x16x32_bf16 v[84:87], a[144:147], a[32:35], v[84:87]\n" \
"v_exp_f32_e32 v113, v113\n" \
"v_mfma_f32_16x16x32_bf16 v[92:95], a[144:147], a[48:51], v[44:47]\n" \
"v_cvt_pk_bf16_f32 v116, v112, v113\n" \
"ds_read_b128 a[144:147], v48 offset:12288\n" \
"v_mfma_f32_16x16x32_bf16 v[92:95], a[148:151], a[52:55], v[92:95]\n" \
"v_exp_f32_e32 v120, v120\n" \
"v_mfma_f32_16x16x32_bf16 v[92:95], a[152:155], a[56:59], v[92:95]\n" \
"buffer_load_dwordx4 v52, s[8:11], s64 offen offset:2048 sc1 lds\n" \
"v_mfma_f32_16x16x32_bf16 v[92:95], a[156:159], a[60:63], v[92:95]\n" \
"v_exp_f32_e32 v121, v121\n" \
"v_mfma_f32_16x16x32_bf16 v[88:91], a[140:143], a[60:63], v[44:47]\n" \
"v_exp_f32_e32 v122, v122\n" \
"v_mfma_f32_16x16x32_bf16 v[88:91], a[136:139], a[56:59], v[88:91]\n" \
"v_exp_f32_e32 v123, v123\n" \
"v_mfma_f32_16x16x32_bf16 v[88:91], a[132:135], a[52:55], v[88:91]\n" \
"buffer_load_dwordx4 v52, s[8:11], s64 offen offset:3072 sc1 lds\n" \
"v_mfma_f32_16x16x32_bf16 v[88:91], a[128:131], a[48:51], v[88:91]\n" \
"s_add_u32 m0, s93, 0x0\n" \
"v_cvt_pk_bf16_f32 v120, v120, v121\n" \
"v_mfma_f32_16x16x32_bf16 a[192:195], a[212:215], v[104:107], a[192:195]\n" \
"v_cvt_pk_bf16_f32 v121, v122, v123\n" \
"s_waitcnt lgkmcnt(6)\n" \
"v_mfma_f32_16x16x32_bf16 v[160:163], a[160:163], v[104:107], v[160:163]\n" \
"v_exp_f32_e32 v124, v124\n" \
"v_mfma_f32_16x16x32_bf16 v[164:167], a[164:167], v[104:107], v[164:167]\n" \
"buffer_load_dwordx4 v0, s[80:83], s92 offen offset:0 lds\n" \
"v_mfma_f32_16x16x32_bf16 v[168:171], a[168:171], v[104:107], v[168:171]\n" \
"ds_read_b128 a[128:131], v48 offset:8192\n" \
"s_waitcnt lgkmcnt(2)\n" \
"v_mfma_f32_16x16x32_bf16 v[172:175], a[172:175], v[104:107], v[172:175]\n" \
"v_exp_f32_e32 v125, v125\n" \
"v_mfma_f32_16x16x32_bf16 v[176:179], a[176:179], v[104:107], v[176:179]\n" \
"v_exp_f32_e32 v126, v126\n" \
"buffer_load_dwordx4 v1, s[80:83], s92 offen offset:1024 lds\n" \
"v_mfma_f32_16x16x32_bf16 v[180:183], a[180:183], v[104:107], v[180:183]\n" \
"v_exp_f32_e32 v127, v127\n" \
"v_mfma_f32_16x16x32_bf16 v[184:187], a[184:187], v[104:107], v[184:187]\n" \
"v_cvt_pk_bf16_f32 v122, v124, v125\n" \
"v_cvt_pk_bf16_f32 v123, v126, v127\n" \
"v_mfma_f32_16x16x32_bf16 v[188:191], a[188:191], v[104:107], v[188:191]\n" \
"ds_read_b128 a[132:135], v48 offset:9216\n" \
"ds_read_b128 a[148:151], v48 offset:13312\n" \
"buffer_load_dwordx4 v2, s[80:83], s92 offen offset:2048 lds\n" \
"v_mfma_f32_16x16x32_bf16 v[220:223], a[188:191], v[116:119], v[220:223]\n" \
"ds_read_b128 a[136:139], v48 offset:10240\n" \
"ds_read_b128 a[152:155], v48 offset:14336\n" \
"v_mfma_f32_16x16x32_bf16 v[252:255], a[188:191], v[120:123], v[252:255]\n" \
"ds_read_b128 a[140:143], v48 offset:11264\n" \
"ds_read_b128 a[156:159], v48 offset:15360\n" \
"v_mfma_f32_16x16x32_bf16 v[216:219], a[184:187], v[116:119], v[216:219]\n" \
"v_readlane_b32 s96, v11, 1\n" \
"v_bfe_u32 v27, v20, 2, 7\n" \
"buffer_load_dwordx4 v3, s[80:83], s92 offen offset:3072 lds\n" \
"v_mfma_f32_16x16x32_bf16 v[248:251], a[184:187], v[120:123], v[248:251]\n" \
"s_add_u32 m0, s93, 0x1000\n" \
"v_add_f32_e32 v54, v54, v22\n" \
"v_mfma_f32_16x16x32_bf16 v[212:215], a[180:183], v[116:119], v[212:215]\n" \
"v_max_f32_e32 v62, v62, v54\n" \
"v_add_u32_e32 v49, 0x10000, v48\n" \
"v_mfma_f32_16x16x32_bf16 v[244:247], a[180:183], v[120:123], v[244:247]\n" \
"v_rcp_f32_e32 v54, v54\n" \
"buffer_load_dwordx4 v4, s[80:83], s92 offen offset:0 lds\n" \
"v_mfma_f32_16x16x32_bf16 v[208:211], a[176:179], v[116:119], v[208:211]\n" \
"v_mul_f32_e32 v54, v54, v58\n" \
"v_mul_f32_e32 v128, v54, v128\n" \
"v_mfma_f32_16x16x32_bf16 v[240:243], a[176:179], v[120:123], v[240:243]\n" \
"v_mul_f32_e32 v129, v54, v129\n" \
"v_mul_f32_e32 v136, v54, v136\n" \
"v_mfma_f32_16x16x32_bf16 v[204:207], a[172:175], v[116:119], v[204:207]\n" \
"v_mul_f32_e32 v137, v54, v137\n" \
"v_mul_f32_e32 v144, v54, v144\n" \
"buffer_load_dwordx4 v5, s[80:83], s92 offen offset:1024 lds\n" \
"v_mfma_f32_16x16x32_bf16 v[236:239], a[172:175], v[120:123], v[236:239]\n" \
"v_mul_f32_e32 v145, v54, v145\n" \
"v_mul_f32_e32 v152, v54, v152\n" \
"v_mfma_f32_16x16x32_bf16 v[200:203], a[168:171], v[116:119], v[200:203]\n" \
"v_mul_f32_e32 v153, v54, v153\n" \
"v_cvt_pk_bf16_f32 v128, v128, v129\n" \
"v_mfma_f32_16x16x32_bf16 v[232:235], a[168:171], v[120:123], v[232:235]\n" \
"v_mul_f32_e32 v130, v54, v130\n" \
"v_mul_f32_e32 v131, v54, v131\n" \
"buffer_load_dwordx4 v6, s[80:83], s92 offen offset:2048 lds\n" \
"v_mfma_f32_16x16x32_bf16 v[196:199], a[164:167], v[116:119], v[196:199]\n" \
"v_cvt_pk_bf16_f32 v136, v136, v137\n" \
"v_mul_f32_e32 v138, v54, v138\n" \
"v_mfma_f32_16x16x32_bf16 v[228:231], a[164:167], v[120:123], v[228:231]\n" \
"v_mul_f32_e32 v139, v54, v139\n" \
"v_cvt_pk_bf16_f32 v144, v144, v145\n" \
"v_mfma_f32_16x16x32_bf16 v[192:195], a[160:163], v[116:119], v[192:195]\n" \
"v_mul_f32_e32 v146, v54, v146\n" \
"v_mul_f32_e32 v147, v54, v147\n" \
"buffer_load_dwordx4 v7, s[80:83], s92 offen offset:3072 lds\n" \
"v_mfma_f32_16x16x32_bf16 v[224:227], a[160:163], v[120:123], v[224:227]\n" \
"v_cvt_pk_bf16_f32 v152, v152, v153\n" \
"v_mul_f32_e32 v154, v54, v154\n" \
"v_smfmac_f32_16x16x64_bf16 a[196:199], a[200:203], v[116:123], v21\n" \
"v_mul_f32_e32 v155, v54, v155\n" \
"v_cvt_pk_bf16_f32 v129, v130, v131\n" \
"v_mul_f32_e32 v132, v54, v132\n" \
"v_mul_f32_e32 v133, v54, v133\n" \
"v_mul_f32_e32 v134, v54, v134\n" \
"v_mul_f32_e32 v135, v54, v135\n" \
"v_cvt_pk_bf16_f32 v137, v138, v139\n" \
"v_mul_f32_e32 v140, v54, v140\n" \
"v_mul_f32_e32 v141, v54, v141\n" \
"v_mul_f32_e32 v142, v54, v142\n" \
"v_mul_f32_e32 v143, v54, v143\n" \
"v_cvt_pk_bf16_f32 v145, v146, v147\n" \
"v_mul_f32_e32 v148, v54, v148\n" \
"v_mul_f32_e32 v149, v54, v149\n" \
"v_mul_f32_e32 v150, v54, v150\n" \
"v_mul_f32_e32 v151, v54, v151\n" \
"v_cvt_pk_bf16_f32 v153, v154, v155\n" \
"v_mul_f32_e32 v156, v54, v156\n" \
"v_mul_f32_e32 v157, v54, v157\n" \
"v_mul_f32_e32 v158, v54, v158\n" \
"v_mul_f32_e32 v159, v54, v159\n" \
"v_cvt_pk_bf16_f32 v130, v132, v133\n" \
"v_cvt_pk_bf16_f32 v131, v134, v135\n" \
"buffer_store_dwordx4 v[128:131], v20, s[56:59], s68 offen offset:0 sc1\n" \
"v_cvt_pk_bf16_f32 v138, v140, v141\n" \
"v_cvt_pk_bf16_f32 v139, v142, v143\n" \
"v_cvt_pk_bf16_f32 v146, v148, v149\n" \
"v_cvt_pk_bf16_f32 v147, v150, v151\n" \
"v_cvt_pk_bf16_f32 v154, v156, v157\n" \
"v_cvt_pk_bf16_f32 v155, v158, v159\n" \
"s_add_u32 m0, s93, 0x2000\n" \
"s_xor_b32 s65, s65, 0x8000\n" \
"s_add_u32 s64, s64, 0x10000\n" \
"v_exp_f32_e32 v86, v86\n" \
"buffer_store_dwordx4 v[136:139], v20, s[56:59], s68 offen offset:1024 sc1\n" \
"v_exp_f32_e32 v87, v87\n" \
"v_exp_f32_e32 v84, v84\n" \
"v_exp_f32_e32 v85, v85\n" \
"v_exp_f32_e32 v88, v88\n" \
"v_exp_f32_e32 v89, v89\n" \
"v_cvt_pk_bf16_f32 v87, v86, v87\n" \
"buffer_store_dwordx4 v[144:147], v20, s[56:59], s68 offen offset:2048 sc1\n" \
"v_exp_f32_e32 v82, v82\n" \
"v_exp_f32_e32 v83, v83\n" \
"v_exp_f32_e32 v80, v80\n" \
"v_exp_f32_e32 v81, v81\n" \
"v_exp_f32_e32 v90, v90\n" \
"v_exp_f32_e32 v91, v91\n" \
"buffer_store_dwordx4 v[152:155], v20, s[56:59], s68 offen offset:3072 sc1\n" \
"v_cvt_pk_bf16_f32 v86, v84, v85\n" \
"v_cvt_pk_bf16_f32 v88, v88, v89\n" \
"v_exp_f32_e32 v92, v92\n" \
"v_exp_f32_e32 v93, v93\n" \
"v_exp_f32_e32 v94, v94\n" \
"v_exp_f32_e32 v95, v95\n" \
"v_cvt_pk_bf16_f32 v85, v82, v83\n" \
"buffer_load_dwordx4 v8, s[80:83], s92 offen offset:0 lds\n" \
"v_cvt_pk_bf16_f32 v84, v80, v81\n" \
"v_cvt_pk_bf16_f32 v89, v90, v91\n" \
"v_and_b32_e32 v84, v12, v84\n" \
"v_and_b32_e32 v85, v13, v85\n" \
"v_and_b32_e32 v86, v14, v86\n" \
"v_and_b32_e32 v87, v15, v87\n" \
"s_waitcnt vmcnt(17) lgkmcnt(0)\n" \
"s_barrier\n" \
"v_mfma_f32_16x16x32_bf16 v[120:123], a[128:131], a[48:51], v[44:47]\n" \
"buffer_load_dword v26, v27, s[56:59], s96 offen sc1\n" \
"v_mfma_f32_16x16x32_bf16 v[120:123], a[132:135], a[52:55], v[120:123]\n" \
"ds_read_b128 a[188:191], v49 offset:7168\n" \
"ds_read_b128 a[184:187], v49 offset:6144\n" \
"v_mfma_f32_16x16x32_bf16 v[120:123], a[136:139], a[56:59], v[120:123]\n" \
"s_xor_b32 m0, s65, 0x12000\n" \
"ds_read_b128 a[180:183], v49 offset:5120\n" \
"v_mfma_f32_16x16x32_bf16 v[120:123], a[140:143], a[60:63], v[120:123]\n" \
"ds_read_b128 a[176:179], v49 offset:4096\n" \
"ds_read_b128 a[172:175], v49 offset:3072\n" \
"v_mfma_f32_16x16x32_bf16 v[124:127], a[156:159], a[60:63], v[44:47]\n" \
"buffer_load_dwordx4 v50, s[8:11], s64 offen offset:0 sc1 lds\n" \
"v_mfma_f32_16x16x32_bf16 v[124:127], a[152:155], a[56:59], v[124:127]\n" \
"ds_read_b128 a[168:171], v49 offset:2048\n" \
"v_cvt_pk_bf16_f32 v90, v92, v93\n" \
"v_mfma_f32_16x16x32_bf16 v[124:127], a[148:151], a[52:55], v[124:127]\n" \
"v_cvt_pk_bf16_f32 v91, v94, v95\n" \
"ds_read_b128 a[164:167], v49 offset:1024\n" \
"v_mfma_f32_16x16x32_bf16 v[124:127], a[144:147], a[48:51], v[124:127]\n" \
"ds_read_b128 a[160:163], v49 offset:0\n" \
"ds_read_b128 a[0:3], v49 offset:8192\n" \
"v_mfma_f32_16x16x32_bf16 v[32:35], a[220:223], a[64:67], 0\n" \
"buffer_load_dwordx4 v50, s[8:11], s64 offen offset:1024 sc1 lds\n" \
"v_mfma_f32_16x16x32_bf16 v[36:39], a[220:223], a[80:83], 0\n" \
"ds_read_b128 a[4:7], v49 offset:9216\n" \
"ds_read_b128 a[8:11], v49 offset:10240\n" \
"v_mfma_f32_16x16x32_bf16 v[32:35], a[224:227], a[68:71], v[32:35]\n" \
"ds_read_b128 a[12:15], v49 offset:11264\n" \
"ds_read_b128 a[16:19], v49 offset:12288\n" \
"v_mfma_f32_16x16x32_bf16 v[36:39], a[224:227], a[84:87], v[36:39]\n" \
"ds_read_b128 a[20:23], v49 offset:13312\n" \
"ds_read_b128 a[24:27], v49 offset:14336\n" \
"v_mfma_f32_16x16x32_bf16 v[32:35], a[228:231], a[72:75], v[32:35]\n" \
"buffer_load_dwordx4 v50, s[8:11], s64 offen offset:2048 sc1 lds\n" \
"v_mfma_f32_16x16x32_bf16 v[36:39], a[228:231], a[88:91], v[36:39]\n" \
"ds_read_b128 a[28:31], v49 offset:15360\n" \
"v_readlane_b32 s97, v11, 36\n" \
"v_mfma_f32_16x16x32_bf16 v[32:35], a[232:235], a[76:79], v[32:35]\n" \
"v_exp_f32_e32 v120, v120\n" \
"v_mfma_f32_16x16x32_bf16 v[36:39], a[232:235], a[92:95], v[36:39]\n" \
"v_exp_f32_e32 v121, v121\n" \
"v_smfmac_f32_16x16x64_bf16 a[196:199], a[200:203], v[84:91], v21\n" \
"v_cvt_pk_bf16_f32 v120, v120, v121\n" \
"s_waitcnt lgkmcnt(13)\n" \
"v_mfma_f32_16x16x32_bf16 v[220:223], a[188:191], v[84:87], v[220:223]\n" \
"buffer_load_dwordx4 v50, s[8:11], s64 offen offset:3072 sc1 lds\n" \
"v_mfma_f32_16x16x32_bf16 v[252:255], a[188:191], v[88:91], v[252:255]\n" \
"v_exp_f32_e32 v122, v122\n" \
"v_mfma_f32_16x16x32_bf16 v[216:219], a[184:187], v[84:87], v[216:219]\n" \
"v_exp_f32_e32 v123, v123\n" \
"v_mfma_f32_16x16x32_bf16 v[248:251], a[184:187], v[88:91], v[248:251]\n" \
"v_exp_f32_e32 v124, v124\n" \
"v_mfma_f32_16x16x32_bf16 v[212:215], a[180:183], v[84:87], v[212:215]\n" \
"v_exp_f32_e32 v125, v125\n" \
"v_mfma_f32_16x16x32_bf16 v[244:247], a[180:183], v[88:91], v[244:247]\n" \
"v_cvt_pk_bf16_f32 v121, v122, v123\n" \
"s_waitcnt lgkmcnt(7)\n" \
"v_mfma_f32_16x16x32_bf16 v[208:211], a[176:179], v[84:87], v[208:211]\n" \
"v_exp_f32_e32 v126, v126\n" \
"v_mfma_f32_16x16x32_bf16 v[240:243], a[176:179], v[88:91], v[240:243]\n" \
"v_exp_f32_e32 v127, v127\n" \
"v_mfma_f32_16x16x32_bf16 v[204:207], a[172:175], v[84:87], v[204:207]\n" \
"v_cvt_pk_bf16_f32 v122, v124, v125\n" \
"v_cvt_pk_bf16_f32 v123, v126, v127\n" \
"v_mfma_f32_16x16x32_bf16 v[236:239], a[172:175], v[88:91], v[236:239]\n" \
"v_and_b32_e32 v120, v12, v120\n" \
"v_and_b32_e32 v121, v13, v121\n" \
"v_mfma_f32_16x16x32_bf16 v[200:203], a[168:171], v[84:87], v[200:203]\n" \
"v_and_b32_e32 v122, v14, v122\n" \
"v_and_b32_e32 v123, v15, v123\n" \
"v_mfma_f32_16x16x32_bf16 v[232:235], a[168:171], v[88:91], v[232:235]\n" \
"v_accvgpr_read_b32 v56, a196\n" \
"v_accvgpr_read_b32 v24, a197\n" \
"v_mfma_f32_16x16x32_bf16 v[196:199], a[164:167], v[84:87], v[196:199]\n" \
"v_add_u32_e32 v17, 0x49000, v20\n" \
"v_readlane_b32 s98, v11, 37\n" \
"v_mfma_f32_16x16x32_bf16 v[228:231], a[164:167], v[88:91], v[228:231]\n" \
"v_add_u32_e32 v48, s16, v20\n" \
"ds_read_b128 a[128:131], v48 offset:0\n" \
"v_mfma_f32_16x16x32_bf16 v[192:195], a[160:163], v[84:87], v[192:195]\n" \
"ds_read_b128 a[144:147], v48 offset:4096\n" \
"ds_read_b128 a[132:135], v48 offset:1024\n" \
"v_mfma_f32_16x16x32_bf16 v[224:227], a[160:163], v[88:91], v[224:227]\n" \
"ds_read_b128 a[148:151], v48 offset:5120\n" \
"ds_read_b128 a[136:139], v48 offset:2048\n" \
"v_mfma_f32_16x16x32_bf16 a[196:199], a[212:215], v[120:123], a[196:199]\n" \
"ds_read_b128 a[152:155], v48 offset:6144\n" \
"ds_read_b128 a[140:143], v48 offset:3072\n" \
"v_mfma_f32_16x16x32_bf16 v[224:227], a[0:3], v[120:123], v[224:227]\n" \
"ds_read_b128 a[156:159], v48 offset:7168\n" \
"s_waitcnt lgkmcnt(8)\n" \
"v_mfma_f32_16x16x32_bf16 v[228:231], a[4:7], v[120:123], v[228:231]\n" \
"v_accvgpr_read_b32 v55, a194\n" \
"v_accvgpr_read_b32 v23, a195\n" \
"v_mfma_f32_16x16x32_bf16 a[192:195], a[216:219], a[216:219], 0\n" \
"v_add_f32_e32 v55, v55, v23\n" \
"v_max_f32_e32 v62, v62, v55\n" \
"v_mfma_f32_16x16x32_bf16 v[232:235], a[8:11], v[120:123], v[232:235]\n" \
"v_rcp_f32_e32 v55, v55\n" \
"v_mfma_f32_16x16x32_bf16 v[236:239], a[12:15], v[120:123], v[236:239]\n" \
"v_mul_f32_e32 v55, v55, v59\n" \
"v_mul_f32_e32 v160, v55, v160\n" \
"v_mfma_f32_16x16x32_bf16 v[240:243], a[16:19], v[120:123], v[240:243]\n" \
"v_mul_f32_e32 v161, v55, v161\n" \
"v_mul_f32_e32 v168, v55, v168\n" \
"v_mfma_f32_16x16x32_bf16 v[244:247], a[20:23], v[120:123], v[244:247]\n" \
"v_mul_f32_e32 v169, v55, v169\n" \
"v_mul_f32_e32 v176, v55, v176\n" \
"v_mfma_f32_16x16x32_bf16 v[248:251], a[24:27], v[120:123], v[248:251]\n" \
"v_mul_f32_e32 v177, v55, v177\n" \
"v_mul_f32_e32 v184, v55, v184\n" \
"v_mfma_f32_16x16x32_bf16 v[252:255], a[28:31], v[120:123], v[252:255]\n" \
"v_mul_f32_e32 v185, v55, v185\n" \
"v_cvt_pk_bf16_f32 v160, v160, v161\n" \
"v_mul_f32_e32 v162, v55, v162\n" \
"v_mul_f32_e32 v163, v55, v163\n" \
"v_cvt_pk_bf16_f32 v168, v168, v169\n" \
"v_mul_f32_e32 v170, v55, v170\n" \
"v_mul_f32_e32 v171, v55, v171\n" \
"v_cvt_pk_bf16_f32 v176, v176, v177\n" \
"v_mul_f32_e32 v178, v55, v178\n" \
"s_waitcnt vmcnt(4)\n" \
"v_cmp_ne_u32_e32 vcc, s62, v26\n" \
"v_mul_f32_e32 v179, v55, v179\n" \
"v_cvt_pk_bf16_f32 v184, v184, v185\n" \
"v_mul_f32_e32 v186, v55, v186\n" \
"v_mul_f32_e32 v187, v55, v187\n" \
"v_cvt_pk_bf16_f32 v161, v162, v163\n" \
"s_cbranch_vccnz L_sos_%=\n" \
"L_ros_%=:\n" \
"buffer_load_dwordx4 a[220:223], v20, s[56:59], s97 offen offset:0 sc1\n" \
"v_mul_f32_e32 v164, v55, v164\n" \
"v_mul_f32_e32 v165, v55, v165\n" \
"v_mul_f32_e32 v166, v55, v166\n" \
"v_mul_f32_e32 v167, v55, v167\n" \
"v_cvt_pk_bf16_f32 v169, v170, v171\n" \
"v_mul_f32_e32 v172, v55, v172\n" \
"v_mul_f32_e32 v173, v55, v173\n" \
"v_mul_f32_e32 v174, v55, v174\n" \
"v_mul_f32_e32 v175, v55, v175\n" \
"v_cvt_pk_bf16_f32 v177, v178, v179\n" \
"v_mul_f32_e32 v180, v55, v180\n" \
"buffer_load_dwordx4 a[228:231], v17, s[56:59], s97 offen offset:0 sc1\n" \
"v_mul_f32_e32 v181, v55, v181\n" \
"v_mul_f32_e32 v182, v55, v182\n" \
"v_mul_f32_e32 v183, v55, v183\n" \
"v_cvt_pk_bf16_f32 v185, v186, v187\n" \
"v_mul_f32_e32 v188, v55, v188\n" \
"v_mul_f32_e32 v189, v55, v189\n" \
"v_mul_f32_e32 v190, v55, v190\n" \
"v_mul_f32_e32 v191, v55, v191\n" \
"v_cvt_pk_bf16_f32 v162, v164, v165\n" \
"v_cvt_pk_bf16_f32 v163, v166, v167\n" \
"v_cvt_pk_bf16_f32 v170, v172, v173\n" \
"buffer_store_dwordx4 v[160:163], v20, s[56:59], s69 offen offset:0 sc1\n" \
"v_cvt_pk_bf16_f32 v171, v174, v175\n" \
"v_cvt_pk_bf16_f32 v178, v180, v181\n" \
"v_cvt_pk_bf16_f32 v179, v182, v183\n" \
"v_cvt_pk_bf16_f32 v186, v188, v189\n" \
"v_cvt_pk_bf16_f32 v187, v190, v191\n" \
"v_readlane_b32 s93, v11, 20\n" \
"v_readlane_b32 s84, v11, 15\n" \
"v_readlane_b32 s85, v11, 16\n" \
"v_readlane_b32 s86, v11, 17\n" \
"s_mov_b32 s87, 0x20000\n" \
"v_readlane_b32 s92, v11, 18\n" \
"buffer_store_dwordx4 v[168:171], v20, s[56:59], s69 offen offset:1024 sc1\n" \
"v_readlane_b32 s91, v11, 21\n" \
"v_mfma_f32_16x16x32_bf16 a[208:211], a[216:219], v[128:131], a[208:211]\n" \
"v_mfma_f32_16x16x32_bf16 a[208:211], a[216:219], v[136:139], a[208:211]\n" \
"v_mfma_f32_32x32x16_bf16 v[128:143], a[216:219], a[216:219], 0\n" \
"buffer_store_dwordx4 v[176:179], v20, s[56:59], s69 offen offset:2048 sc1\n" \
"v_mfma_f32_16x16x32_bf16 a[208:211], a[216:219], v[144:147], a[208:211]\n" \
"v_mfma_f32_16x16x32_bf16 a[208:211], a[216:219], v[152:155], a[208:211]\n" \
"v_mfma_f32_32x32x16_bf16 v[144:159], a[216:219], a[216:219], 0\n" \
"buffer_store_dwordx4 v[184:187], v20, s[56:59], s69 offen offset:3072 sc1\n" \
"v_add_f32_e32 v56, v56, v24\n" \
"v_mfma_f32_16x16x32_bf16 a[208:211], a[216:219], v[160:163], a[208:211]\n" \
"v_max_f32_e32 v62, v62, v56\n" \
"v_rcp_f32_e32 v56, v56\n" \
"v_mfma_f32_16x16x32_bf16 a[208:211], a[216:219], v[168:171], a[208:211]\n" \
"v_mfma_f32_32x32x16_bf16 v[160:175], a[216:219], a[216:219], 0\n" \
"buffer_load_dwordx4 a[236:239], v20, s[56:59], s98 offen offset:0 sc1\n" \
"v_mul_f32_e32 v56, v56, v60\n" \
"v_mfma_f32_16x16x32_bf16 a[208:211], a[216:219], v[176:179], a[208:211]\n" \
"v_mul_f32_e32 v192, v56, v192\n" \
"v_mul_f32_e32 v193, v56, v193\n" \
"v_cvt_pk_bf16_f32 v192, v192, v193\n" \
"v_mul_f32_e32 v194, v56, v194\n" \
"v_mul_f32_e32 v195, v56, v195\n" \
"buffer_load_dwordx4 a[244:247], v17, s[56:59], s98 offen offset:0 sc1\n" \
"v_mfma_f32_16x16x32_bf16 a[208:211], a[216:219], v[184:187], a[208:211]\n" \
"v_mfma_f32_32x32x16_bf16 v[176:191], a[216:219], a[216:219], 0\n" \
"v_cvt_pk_bf16_f32 v193, v194, v195\n" \
"v_mul_f32_e32 v196, v56, v196\n" \
"v_mul_f32_e32 v197, v56, v197\n" \
"v_mul_f32_e32 v198, v56, v198\n" \
"v_mul_f32_e32 v199, v56, v199\n" \
"buffer_load_dwordx4 a[224:227], v20, s[56:59], s97 offen offset:1024 sc1\n" \
"v_mul_f32_e32 v200, v56, v200\n" \
"v_mul_f32_e32 v201, v56, v201\n" \
"v_accvgpr_read_b32 v57, a198\n" \
"v_accvgpr_read_b32 v25, a199\n" \
"v_mfma_f32_16x16x32_bf16 a[196:199], a[216:219], a[216:219], 0\n" \
"v_cvt_pk_bf16_f32 v194, v196, v197\n" \
"v_cvt_pk_bf16_f32 v195, v198, v199\n" \
"v_cvt_pk_bf16_f32 v200, v200, v201\n" \
"v_mul_f32_e32 v202, v56, v202\n" \
"v_mul_f32_e32 v203, v56, v203\n" \
"buffer_store_dwordx4 v[192:195], v20, s[56:59], s70 offen offset:0 sc1\n" \
"v_add_f32_e32 v57, v57, v25\n" \
"v_mfma_f32_16x16x32_bf16 a[208:211], a[216:219], v[192:195], a[208:211]\n" \
"v_cvt_pk_bf16_f32 v201, v202, v203\n" \
"v_mul_f32_e32 v204, v56, v204\n" \
"v_mul_f32_e32 v205, v56, v205\n" \
"v_mul_f32_e32 v206, v56, v206\n" \
"v_mul_f32_e32 v207, v56, v207\n" \
"v_mul_f32_e32 v208, v56, v208\n" \
"v_mul_f32_e32 v209, v56, v209\n" \
"v_max_f32_e32 v62, v62, v57\n" \
"buffer_load_dwordx4 a[232:235], v17, s[56:59], s97 offen offset:1024 sc1\n" \
"s_waitcnt vmcnt(16) lgkmcnt(0)\n" \
"s_barrier\n" \
"v_mfma_f32_16x16x32_bf16 v[64:67], a[128:131], a[64:67], v[32:35]\n" \
"s_add_u32 m0, s93, 0x0\n" \
"v_cvt_pk_bf16_f32 v202, v204, v205\n" \
"v_mfma_f32_16x16x32_bf16 v[64:67], a[132:135], a[68:71], v[64:67]\n" \
"buffer_load_dwordx4 a[240:243], v20, s[56:59], s98 offen offset:1024 sc1\n" \
"v_mfma_f32_16x16x32_bf16 v[64:67], a[136:139], a[72:75], v[64:67]\n" \
"v_cvt_pk_bf16_f32 v203, v206, v207\n" \
"v_cvt_pk_bf16_f32 v208, v208, v209\n" \
"v_mfma_f32_16x16x32_bf16 v[64:67], a[140:143], a[76:79], v[64:67]\n" \
"v_mul_f32_e32 v210, v56, v210\n" \
"v_mul_f32_e32 v211, v56, v211\n" \
"v_mfma_f32_16x16x32_bf16 v[68:71], a[156:159], a[76:79], v[32:35]\n" \
"v_rcp_f32_e32 v57, v57\n" \
"buffer_store_dwordx4 v[200:203], v20, s[56:59], s70 offen offset:1024 sc1\n" \
"v_mfma_f32_16x16x32_bf16 v[68:71], a[152:155], a[72:75], v[68:71]\n" \
"v_mfma_f32_16x16x32_bf16 a[208:211], a[216:219], v[200:203], a[208:211]\n" \
"v_mfma_f32_32x32x16_bf16 v[192:207], a[216:219], a[216:219], 0\n" \
"v_cvt_pk_bf16_f32 v209, v210, v211\n" \
"buffer_load_dwordx4 a[248:251], v17, s[56:59], s98 offen offset:1024 sc1\n" \
"v_mul_f32_e32 v212, v56, v212\n" \
"v_mul_f32_e32 v213, v56, v213\n" \
"v_mul_f32_e32 v214, v56, v214\n" \
"v_mfma_f32_16x16x32_bf16 v[68:71], a[148:151], a[68:71], v[68:71]\n" \
"v_mul_f32_e32 v215, v56, v215\n" \
"v_mul_f32_e32 v216, v56, v216\n" \
"v_mfma_f32_16x16x32_bf16 v[68:71], a[144:147], a[64:67], v[68:71]\n" \
"v_mul_f32_e32 v217, v56, v217\n" \
"v_cvt_pk_bf16_f32 v210, v212, v213\n" \
"buffer_load_dwordx4 v0, s[84:87], s92 offen offset:0 lds\n" \
"v_mfma_f32_16x16x32_bf16 v[76:79], a[144:147], a[80:83], v[36:39]\n" \
"ds_read_b128 a[144:147], v48 offset:12288\n" \
"v_cvt_pk_bf16_f32 v211, v214, v215\n" \
"v_mfma_f32_16x16x32_bf16 v[76:79], a[148:151], a[84:87], v[76:79]\n" \
"ds_read_b128 a[148:151], v48 offset:13312\n" \
"v_cvt_pk_bf16_f32 v216, v216, v217\n" \
"v_mfma_f32_16x16x32_bf16 v[76:79], a[152:155], a[88:91], v[76:79]\n" \
"ds_read_b128 a[152:155], v48 offset:14336\n" \
"v_mul_f32_e32 v218, v56, v218\n" \
"buffer_store_dwordx4 v[208:211], v20, s[56:59], s70 offen offset:2048 sc1\n" \
"v_mfma_f32_16x16x32_bf16 v[76:79], a[156:159], a[92:95], v[76:79]\n" \
"ds_read_b128 a[156:159], v48 offset:15360\n" \
"v_mul_f32_e32 v219, v56, v219\n" \
"v_mfma_f32_16x16x32_bf16 v[72:75], a[140:143], a[92:95], v[36:39]\n" \
"ds_read_b128 a[140:143], v48 offset:11264\n" \
"v_mul_f32_e32 v57, v57, v61\n" \
"v_mfma_f32_16x16x32_bf16 v[72:75], a[136:139], a[88:91], v[72:75]\n" \
".fill (L_ab + 28672 - .) / 4, 4, 0xBF800000\n" \
"L_ap7:\n" \
"s_cbranch_execz L_ap0\n" \
"ds_read_b128 a[136:139], v48 offset:10240\n" \
"v_cvt_pk_bf16_f32 v217, v218, v219\n" \
"buffer_load_dwordx4 v1, s[84:87], s92 offen offset:1024 lds\n" \
"v_mfma_f32_16x16x32_bf16 v[72:75], a[132:135], a[84:87], v[72:75]\n" \
"ds_read_b128 a[132:135], v48 offset:9216\n" \
"v_mul_f32_e32 v220, v56, v220\n" \
"v_mfma_f32_16x16x32_bf16 v[72:75], a[128:131], a[80:83], v[72:75]\n" \
"ds_read_b128 a[128:131], v48 offset:8192\n" \
"v_mfma_f32_16x16x32_bf16 a[208:211], a[216:219], v[208:211], a[208:211]\n" \
"v_mul_f32_e32 v221, v56, v221\n" \
"buffer_load_dwordx4 v2, s[84:87], s92 offen offset:2048 lds\n" \
"v_mul_f32_e32 v222, v56, v222\n" \
"v_mul_f32_e32 v223, v56, v223\n" \
"v_mul_f32_e32 v224, v57, v224\n" \
"v_mul_f32_e32 v225, v57, v225\n" \
"v_cvt_pk_bf16_f32 v218, v220, v221\n" \
"v_cvt_pk_bf16_f32 v219, v222, v223\n" \
"v_cvt_pk_bf16_f32 v224, v224, v225\n" \
"v_mul_f32_e32 v226, v57, v226\n" \
"v_mul_f32_e32 v227, v57, v227\n" \
"v_mfma_f32_16x16x32_bf16 a[208:211], a[216:219], v[216:219], a[208:211]\n" \
"buffer_store_dwordx4 v[216:219], v20, s[56:59], s70 offen offset:3072 sc1\n" \
"v_cvt_pk_bf16_f32 v225, v226, v227\n" \
"v_mul_f32_e32 v228, v57, v228\n" \
"v_mfma_f32_32x32x16_bf16 v[208:223], a[216:219], a[216:219], 0\n" \
"v_mul_f32_e32 v229, v57, v229\n" \
"v_mul_f32_e32 v230, v57, v230\n" \
"v_mul_f32_e32 v231, v57, v231\n" \
"v_mul_f32_e32 v232, v57, v232\n" \
"v_mul_f32_e32 v233, v57, v233\n" \
"v_cvt_pk_bf16_f32 v226, v228, v229\n" \
"v_cvt_pk_bf16_f32 v227, v230, v231\n" \
"buffer_store_dwordx4 v[224:227], v20, s[56:59], s71 offen offset:0 sc1\n" \
"v_cvt_pk_bf16_f32 v232, v232, v233\n" \
"v_mul_f32_e32 v234, v57, v234\n" \
"v_mul_f32_e32 v235, v57, v235\n" \
"v_mfma_f32_16x16x32_bf16 a[208:211], a[216:219], v[224:227], a[208:211]\n" \
"v_cvt_pk_bf16_f32 v233, v234, v235\n" \
"v_mul_f32_e32 v236, v57, v236\n" \
"v_mul_f32_e32 v237, v57, v237\n" \
"v_mul_f32_e32 v238, v57, v238\n" \
"v_mul_f32_e32 v239, v57, v239\n" \
"v_mul_f32_e32 v240, v57, v240\n" \
"buffer_load_dwordx4 v3, s[84:87], s92 offen offset:3072 lds\n" \
"s_add_u32 m0, s91, 0x1000\n" \
"v_mul_f32_e32 v241, v57, v241\n" \
"v_cvt_pk_bf16_f32 v234, v236, v237\n" \
"v_cvt_pk_bf16_f32 v235, v238, v239\n" \
"v_cvt_pk_bf16_f32 v240, v240, v241\n" \
"v_mul_f32_e32 v242, v57, v242\n" \
"v_mul_f32_e32 v243, v57, v243\n" \
"v_mfma_f32_16x16x32_bf16 a[208:211], a[216:219], v[232:235], a[208:211]\n" \
"v_cvt_pk_bf16_f32 v241, v242, v243\n" \
"v_mul_f32_e32 v244, v57, v244\n" \
"buffer_store_dwordx4 v[232:235], v20, s[56:59], s71 offen offset:1024 sc1\n" \
"v_mul_f32_e32 v245, v57, v245\n" \
"v_mul_f32_e32 v246, v57, v246\n" \
"v_mfma_f32_32x32x16_bf16 v[224:239], a[216:219], a[216:219], 0\n" \
"v_mul_f32_e32 v247, v57, v247\n" \
"v_mul_f32_e32 v248, v57, v248\n" \
"v_mul_f32_e32 v249, v57, v249\n" \
"v_cvt_pk_bf16_f32 v242, v244, v245\n" \
"v_cvt_pk_bf16_f32 v243, v246, v247\n" \
"v_cvt_pk_bf16_f32 v248, v248, v249\n" \
"v_mul_f32_e32 v250, v57, v250\n" \
"buffer_store_dwordx4 v[240:243], v20, s[56:59], s71 offen offset:2048 sc1\n" \
"v_mul_f32_e32 v251, v57, v251\n" \
"v_mfma_f32_16x16x32_bf16 a[208:211], a[216:219], v[240:243], a[208:211]\n" \
"v_cvt_pk_bf16_f32 v249, v250, v251\n" \
"v_mul_f32_e32 v252, v57, v252\n" \
"v_mul_f32_e32 v253, v57, v253\n" \
"v_mul_f32_e32 v254, v57, v254\n" \
"v_mul_f32_e32 v255, v57, v255\n" \
"v_cvt_pk_bf16_f32 v250, v252, v253\n" \
"v_cvt_pk_bf16_f32 v251, v254, v255\n" \
"v_add_u32_e32 v53, 0x20000, v63\n" \
"buffer_store_dwordx4 v[248:251], v20, s[56:59], s71 offen offset:3072 sc1\n" \
"v_mfma_f32_16x16x32_bf16 a[208:211], a[216:219], v[248:251], a[208:211]\n" \
"v_exp_f32_e32 v70, v70\n" \
"v_mfma_f32_32x32x16_bf16 v[240:255], a[216:219], a[216:219], 0\n" \
"v_exp_f32_e32 v71, v71\n" \
"v_exp_f32_e32 v72, v72\n" \
"v_exp_f32_e32 v73, v73\n" \
"buffer_load_dwordx4 v4, s[84:87], s92 offen offset:0 lds\n" \
"s_add_u32 s64, s64, 0x10000\n" \
"v_cmp_lt_f32_e64 s[72:73], s21, v62\n" \
"v_accvgpr_read_b32 v63, a208\n" \
"v_cmp_u_f32_e32 vcc, v63, v63\n" \
"v_readlane_b32 s95, v11, 3\n" \
"v_exp_f32_e32 v68, v68\n" \
"v_exp_f32_e32 v69, v69\n" \
"v_exp_f32_e32 v74, v74\n" \
"buffer_load_dwordx4 v5, s[84:87], s92 offen offset:1024 lds\n" \
"v_exp_f32_e32 v75, v75\n" \
"s_or_b64 vcc, s[72:73], vcc\n" \
"s_cselect_b32 s95, 0x40000000, s95\n" \
"s_cselect_b32 s94, 0x7f800000, 0\n" \
"v_cvt_pk_bf16_f32 v71, v70, v71\n" \
"v_exp_f32_e32 v66, v66\n" \
"v_exp_f32_e32 v67, v67\n" \
"v_exp_f32_e32 v64, v64\n" \
"buffer_load_dwordx4 v6, s[84:87], s92 offen offset:2048 lds\n" \
"v_exp_f32_e32 v65, v65\n" \
"v_cvt_pk_bf16_f32 v72, v72, v73\n" \
"v_exp_f32_e32 v76, v76\n" \
"v_exp_f32_e32 v77, v77\n" \
"v_exp_f32_e32 v78, v78\n" \
"v_exp_f32_e32 v79, v79\n" \
"buffer_load_dwordx4 v7, s[84:87], s92 offen offset:3072 lds\n" \
"s_add_u32 m0, s91, 0x2000\n" \
"v_cvt_pk_bf16_f32 v70, v68, v69\n" \
"v_cvt_pk_bf16_f32 v73, v74, v75\n" \
"v_add_u32_e32 v49, 0x10000, v48\n" \
"s_xor_b32 s65, s65, 0x8000\n" \
"v_cvt_pk_bf16_f32 v69, v66, v67\n" \
"v_cvt_pk_bf16_f32 v68, v64, v65\n" \
"v_cvt_pk_bf16_f32 v74, v76, v77\n" \
"v_cvt_pk_bf16_f32 v75, v78, v79\n" \
"v_xor_b32_e32 v48, 0x8000, v48\n" \
"s_sub_u32 s66, s19, 2\n" \
"buffer_load_dwordx4 v8, s[84:87], s92 offen offset:0 lds\n" \
"s_waitcnt vmcnt(18) lgkmcnt(0)\n" \
"s_barrier\n" \
"s_branch L_f1_%=\n" \
".fill (L_ab + 32768 - .) / 4, 4, 0xBF800000\n" \
".fill (L_ab + 32768 - .) / 4, 4, 0xBF800000\n" \
"L_ap8:\n" \
"s_setpc_b64 s[88:89]\n" \
"L_out_%=:\n" \
"v_bfe_u32 v38, v20, 2, 4\n" \
"v_readlane_b32 s64, v11, 2\n" \
"v_add_u32_e32 v32, 0x49000, v20\n" \
"v_add_u32_e32 v34, 0x20000, v10\n" \
"ds_read_b64_tr_b16 a[0:1], v34 offset:0\n" \
"ds_read_b64_tr_b16 a[2:3], v34 offset:4608\n" \
"ds_read_b64_tr_b16 a[4:5], v34 offset:9216\n" \
"buffer_load_dword v26, v38, s[56:59], s64 offen sc1\n" \
"ds_read_b64_tr_b16 a[6:7], v34 offset:13824\n" \
"ds_read_b64_tr_b16 a[8:9], v34 offset:32\n" \
"ds_read_b64_tr_b16 a[10:11], v34 offset:4640\n" \
"ds_read_b64_tr_b16 a[12:13], v34 offset:9248\n" \
"buffer_store_dwordx4 v[64:67], v20, s[56:59], s61 offen offset:0 sc1\n" \
"ds_read_b64_tr_b16 a[14:15], v34 offset:13856\n" \
"v_mov_b32_e32 v39, s62\n" \
"v_readlane_b32 s70, v11, 5\n" \
"v_readlane_b32 s71, v11, 4\n" \
"buffer_store_dwordx4 v[68:71], v20, s[56:59], s61 offen offset:1024 sc1\n" \
"ds_read_b64_tr_b16 a[16:17], v34 offset:64\n" \
"ds_read_b64_tr_b16 a[18:19], v34 offset:4672\n" \
"ds_read_b64_tr_b16 a[20:21], v34 offset:9280\n" \
"ds_read_b64_tr_b16 a[22:23], v34 offset:13888\n" \
"buffer_store_dwordx4 v[72:75], v20, s[56:59], s61 offen offset:2048 sc1\n" \
"ds_read_b64_tr_b16 a[24:25], v34 offset:96\n" \
"ds_read_b64_tr_b16 a[26:27], v34 offset:4704\n" \
"ds_read_b64_tr_b16 a[28:29], v34 offset:9312\n" \
"ds_read_b64_tr_b16 a[30:31], v34 offset:13920\n" \
"buffer_store_dwordx4 v[76:79], v20, s[56:59], s61 offen offset:3072 sc1\n" \
"ds_read_b64_tr_b16 a[192:193], v34 offset:128\n" \
"ds_read_b64_tr_b16 a[194:195], v34 offset:4736\n" \
"ds_read_b64_tr_b16 a[196:197], v34 offset:9344\n" \
"ds_read_b64_tr_b16 a[198:199], v34 offset:13952\n" \
"buffer_store_dwordx4 v[80:83], v32, s[56:59], s61 offen offset:0 sc1\n" \
"ds_read_b64_tr_b16 a[200:201], v34 offset:160\n" \
"ds_read_b64_tr_b16 a[202:203], v34 offset:4768\n" \
"ds_read_b64_tr_b16 a[204:205], v34 offset:9376\n" \
"ds_read_b64_tr_b16 a[206:207], v34 offset:13984\n" \
"buffer_store_dwordx4 v[84:87], v32, s[56:59], s61 offen offset:1024 sc1\n" \
"ds_read_b64_tr_b16 a[208:209], v34 offset:192\n" \
"s_waitcnt vmcnt(16) lgkmcnt(15)\n" \
"v_mfma_f32_16x16x32_bf16 v[112:115], a[0:3], a[96:99], 0\n" \
"buffer_store_dwordx4 v[96:99], v32, s[56:59], s61 offen offset:2048 sc1\n" \
"v_mfma_f32_16x16x32_bf16 v[112:115], a[4:7], a[100:103], v[112:115]\n" \
"ds_read_b64_tr_b16 a[210:211], v34 offset:4800\n" \
"ds_read_b64_tr_b16 a[212:213], v34 offset:9408\n" \
"buffer_store_dwordx4 v[100:103], v32, s[56:59], s61 offen offset:3072 sc1\n" \
"v_mfma_f32_16x16x32_bf16 v[116:119], a[0:3], a[112:115], 0\n" \
"ds_read_b64_tr_b16 a[214:215], v34 offset:14016\n" \
"ds_read_b64_tr_b16 a[216:217], v34 offset:224\n" \
"v_mfma_f32_16x16x32_bf16 v[116:119], a[4:7], a[116:119], v[116:119]\n" \
"ds_read_b64_tr_b16 a[218:219], v34 offset:4832\n" \
"v_lshrrev_b32_e32 v44, 9, v20\n" \
"v_mfma_f32_16x16x32_bf16 v[184:187], a[0:3], v[64:67], 0\n" \
"v_mul_u32_u24_e32 v44, 0xb40, v44\n" \
"v_add_u32_e32 v35, v44, v10\n" \
"v_mfma_f32_16x16x32_bf16 v[184:187], a[4:7], v[68:71], v[184:187]\n" \
"v_readlane_b32 s65, v11, 30\n" \
"v_readlane_b32 s67, v11, 23\n" \
"v_mfma_f32_16x16x32_bf16 v[188:191], a[0:3], v[80:83], 0\n" \
"ds_read_b64_tr_b16 a[0:1], v34 offset:9440\n" \
"ds_read_b64_tr_b16 a[2:3], v34 offset:14048\n" \
"v_mfma_f32_16x16x32_bf16 v[188:191], a[4:7], v[84:87], v[188:191]\n" \
"ds_read_b64_tr_b16 a[4:5], v34 offset:256\n" \
"ds_read_b64_tr_b16 a[6:7], v34 offset:4864\n" \
"v_mfma_f32_16x16x32_bf16 v[196:199], a[8:11], v[80:83], 0\n" \
"v_readlane_b32 s80, v11, 11\n" \
"v_readlane_b32 s81, v11, 12\n" \
"v_mfma_f32_16x16x32_bf16 v[196:199], a[12:15], v[84:87], v[196:199]\n" \
"s_mov_b32 s82, 0x47fca0\n" \
"s_mov_b32 s83, 0x20000\n" \
"v_mfma_f32_16x16x32_bf16 v[192:195], a[8:11], v[64:67], 0\n" \
"v_readlane_b32 s66, v11, 28\n" \
"s_add_u32 m0, s67, 0x0\n" \
"v_mfma_f32_16x16x32_bf16 v[192:195], a[12:15], v[68:71], v[192:195]\n" \
"v_readlane_b32 s92, v11, 24\n" \
"v_readlane_b32 s73, v11, 29\n" \
"v_mfma_f32_16x16x32_bf16 v[124:127], a[8:11], a[112:115], 0\n" \
"buffer_load_dwordx4 v0, s[80:83], s66 offen offset:0 lds\n" \
"v_mfma_f32_16x16x32_bf16 v[124:127], a[12:15], a[116:119], v[124:127]\n" \
"v_readlane_b32 s94, v11, 25\n" \
"v_readlane_b32 s84, v11, 9\n" \
"v_mfma_f32_16x16x32_bf16 v[120:123], a[8:11], a[96:99], 0\n" \
"ds_read_b64_tr_b16 a[8:9], v34 offset:9472\n" \
"ds_read_b64_tr_b16 a[10:11], v34 offset:14080\n" \
"v_mfma_f32_16x16x32_bf16 v[120:123], a[12:15], a[100:103], v[120:123]\n" \
"ds_read_b64_tr_b16 a[12:13], v34 offset:18432\n" \
"ds_read_b64_tr_b16 a[14:15], v34 offset:23040\n" \
"v_mfma_f32_16x16x32_bf16 v[128:131], a[16:19], a[96:99], 0\n" \
"v_readlane_b32 s85, v11, 10\n" \
"s_waitcnt lgkmcnt(9)\n" \
"v_mfma_f32_16x16x32_bf16 v[128:131], a[20:23], a[100:103], v[128:131]\n" \
"buffer_load_dwordx4 v1, s[80:83], s66 offen offset:1024 lds\n" \
"v_mfma_f32_16x16x32_bf16 v[132:135], a[16:19], a[112:115], 0\n" \
"s_mov_b32 s86, 0xa1ca0\n" \
"s_mov_b32 s87, 0x20000\n" \
"v_mfma_f32_16x16x32_bf16 v[132:135], a[20:23], a[116:119], v[132:135]\n" \
"v_add_u32_e32 v33, 0x10000, v10\n" \
"v_readlane_b32 s68, v11, 31\n" \
"v_mfma_f32_16x16x32_bf16 v[200:203], a[16:19], v[64:67], 0\n" \
"v_readlane_b32 s69, v11, 32\n" \
"v_readlane_b32 s72, v11, 34\n" \
"v_mfma_f32_16x16x32_bf16 v[200:203], a[20:23], v[68:71], v[200:203]\n" \
"v_readlane_b32 s93, v11, 35\n" \
"v_readlane_b32 s63, v11, 41\n" \
"v_mfma_f32_16x16x32_bf16 v[204:207], a[16:19], v[80:83], 0\n" \
"ds_read_b64_tr_b16 a[16:17], v34 offset:18464\n" \
"ds_read_b64_tr_b16 a[18:19], v34 offset:23072\n" \
"v_mfma_f32_16x16x32_bf16 v[204:207], a[20:23], v[84:87], v[204:207]\n" \
"ds_read_b64_tr_b16 a[20:21], v34 offset:18496\n" \
"ds_read_b64_tr_b16 a[22:23], v34 offset:23104\n" \
"v_mfma_f32_16x16x32_bf16 v[212:215], a[24:27], v[80:83], 0\n" \
"buffer_load_dwordx4 v2, s[80:83], s66 offen offset:2048 lds\n" \
"v_mfma_f32_16x16x32_bf16 v[212:215], a[28:31], v[84:87], v[212:215]\n" \
"v_bfe_u32 v40, v20, 2, 7\n" \
"s_add_u32 s74, s63, 0x3800\n" \
"v_mfma_f32_16x16x32_bf16 v[208:211], a[24:27], v[64:67], 0\n" \
"v_lshlrev_b32_e32 v41, 12, v20\n" \
"s_add_u32 s12, s63, 0x0\n" \
"v_mfma_f32_16x16x32_bf16 v[208:211], a[28:31], v[68:71], v[208:211]\n" \
"v_readlane_b32 s95, v11, 38\n" \
"v_readlane_b32 s96, v11, 39\n" \
"v_mfma_f32_16x16x32_bf16 v[140:143], a[24:27], a[112:115], 0\n" \
"s_add_u32 s13, s63, 0x1000\n" \
"v_readlane_b32 s97, v11, 40\n" \
"v_mfma_f32_16x16x32_bf16 v[140:143], a[28:31], a[116:119], v[140:143]\n" \
"buffer_load_dwordx4 v3, s[80:83], s66 offen offset:3072 lds\n" \
"v_mfma_f32_16x16x32_bf16 v[136:139], a[24:27], a[96:99], 0\n" \
"ds_read_b64_tr_b16 a[24:25], v34 offset:18528\n" \
"ds_read_b64_tr_b16 a[26:27], v34 offset:23136\n" \
"v_mfma_f32_16x16x32_bf16 v[136:139], a[28:31], a[100:103], v[136:139]\n" \
"ds_read_b64_tr_b16 a[28:29], v34 offset:18560\n" \
"ds_read_b64_tr_b16 a[30:31], v34 offset:23168\n" \
"v_mfma_f32_16x16x32_bf16 v[144:147], a[192:195], a[96:99], 0\n" \
"s_add_u32 m0, s67, 0x1000\n" \
"s_add_u32 s14, s63, 0x2000\n" \
"v_mfma_f32_16x16x32_bf16 v[144:147], a[196:199], a[100:103], v[144:147]\n" \
"s_add_u32 s15, s63, 0x3000\n" \
"v_readlane_b32 s100, v11, 7\n" \
"v_mfma_f32_16x16x32_bf16 v[148:151], a[192:195], a[112:115], 0\n" \
"buffer_load_dwordx4 v4, s[80:83], s66 offen offset:0 lds\n" \
"v_mfma_f32_16x16x32_bf16 v[148:151], a[196:199], a[116:119], v[148:151]\n" \
"v_readlane_b32 s16, v11, 42\n" \
"v_bfe_u32 v44, v20, 4, 4\n" \
"v_mfma_f32_16x16x32_bf16 v[216:219], a[192:195], v[64:67], 0\n" \
"s_add_u32 s17, s16, 0x1000\n" \
"s_add_u32 s18, s16, 0x2000\n" \
"v_mfma_f32_16x16x32_bf16 v[216:219], a[196:199], v[68:71], v[216:219]\n" \
"s_add_u32 s19, s16, 0x3000\n" \
"v_bfe_u32 v45, v20, 8, 2\n" \
"v_mfma_f32_16x16x32_bf16 v[220:223], a[192:195], v[80:83], 0\n" \
"ds_read_b64_tr_b16 a[192:193], v34 offset:18592\n" \
"ds_read_b64_tr_b16 a[194:195], v34 offset:23200\n" \
"v_mfma_f32_16x16x32_bf16 v[220:223], a[196:199], v[84:87], v[220:223]\n" \
"ds_read_b64_tr_b16 a[196:197], v34 offset:18624\n" \
"ds_read_b64_tr_b16 a[198:199], v34 offset:23232\n" \
"v_mfma_f32_16x16x32_bf16 v[228:231], a[200:203], v[80:83], 0\n" \
"buffer_load_dwordx4 v5, s[80:83], s66 offen offset:1024 lds\n" \
"v_mfma_f32_16x16x32_bf16 v[228:231], a[204:207], v[84:87], v[228:231]\n" \
"v_mul_u32_u24_e32 v42, 0x480, v44\n" \
"v_lshl_add_u32 v42, v45, 3, v42\n" \
"v_mfma_f32_16x16x32_bf16 v[224:227], a[200:203], v[64:67], 0\n" \
"s_waitcnt vmcnt(14)\n" \
"v_cmp_ne_u32_e32 vcc, s62, v26\n" \
"v_mfma_f32_16x16x32_bf16 v[224:227], a[204:207], v[68:71], v[224:227]\n" \
"v_lshrrev_b32_e32 v44, 1, v44\n" \
"v_lshlrev_b32_e32 v45, 1, v45\n" \
"v_mfma_f32_16x16x32_bf16 v[156:159], a[200:203], a[112:115], 0\n" \
"v_sub_u32_e32 v44, v44, v45\n" \
"s_cbranch_vccnz L_sa_%=\n" \
"L_ra_%=:\n" \
"v_mfma_f32_16x16x32_bf16 v[156:159], a[204:207], a[116:119], v[156:159]\n" \
"buffer_load_dwordx4 a[64:67], v20, s[56:59], s65 offen offset:0 sc1\n" \
"v_mfma_f32_16x16x32_bf16 v[152:155], a[200:203], a[96:99], 0\n" \
"ds_read_b64_tr_b16 a[200:201], v34 offset:18656\n" \
"ds_read_b64_tr_b16 a[202:203], v34 offset:23264\n" \
"v_mfma_f32_16x16x32_bf16 v[152:155], a[204:207], a[100:103], v[152:155]\n" \
"ds_read_b64_tr_b16 a[204:205], v34 offset:18688\n" \
"ds_read_b64_tr_b16 a[206:207], v34 offset:23296\n" \
"v_mfma_f32_16x16x32_bf16 v[160:163], a[208:211], a[96:99], 0\n" \
"v_readlane_b32 s64, v11, 33\n" \
"v_and_b32_e32 v45, 16, v20\n" \
"v_mfma_f32_16x16x32_bf16 v[160:163], a[212:215], a[100:103], v[160:163]\n" \
"v_mov_b32_e32 v46, 0x3f80\n" \
"v_lshlrev_b32_e32 v45, v45, v46\n" \
"v_mfma_f32_16x16x32_bf16 v[164:167], a[208:211], a[112:115], 0\n" \
"buffer_load_dwordx4 a[68:71], v20, s[56:59], s65 offen offset:1024 sc1\n" \
"v_mfma_f32_16x16x32_bf16 v[164:167], a[212:215], a[116:119], v[164:167]\n" \
"v_mov_b32_e32 v52, 0\n" \
"v_mov_b32_e32 v53, 0\n" \
"v_mfma_f32_16x16x32_bf16 v[232:235], a[208:211], v[64:67], 0\n" \
"v_mov_b32_e32 v48, 0\n" \
"v_mov_b32_e32 v49, 0\n" \
"v_mfma_f32_16x16x32_bf16 v[232:235], a[212:215], v[68:71], v[232:235]\n" \
"v_add_u32_e32 v43, 0x9000, v42\n" \
"v_readlane_b32 s88, v11, 13\n" \
"v_mfma_f32_16x16x32_bf16 v[236:239], a[208:211], v[80:83], 0\n" \
"ds_read_b64_tr_b16 a[208:209], v10 offset:32768\n" \
"ds_read_b64_tr_b16 a[210:211], v10 offset:37376\n" \
"v_mfma_f32_16x16x32_bf16 v[236:239], a[212:215], v[84:87], v[236:239]\n" \
"ds_read_b64_tr_b16 a[212:213], v10 offset:32800\n" \
"s_waitcnt lgkmcnt(7)\n" \
"v_mfma_f32_16x16x32_bf16 v[244:247], a[216:219], v[80:83], 0\n" \
"ds_read_b64_tr_b16 a[214:215], v10 offset:37408\n" \
"v_readlane_b32 s89, v11, 14\n" \
"v_mfma_f32_16x16x32_bf16 v[244:247], a[0:3], v[84:87], v[244:247]\n" \
"buffer_load_dwordx4 a[80:83], v32, s[56:59], s65 offen offset:0 sc1\n" \
"v_mfma_f32_16x16x32_bf16 v[240:243], a[216:219], v[64:67], 0\n" \
"s_mov_b32 s90, 0x240000\n" \
"s_mov_b32 s91, 0x20000\n" \
"v_mfma_f32_16x16x32_bf16 v[240:243], a[0:3], v[68:71], v[240:243]\n" \
"v_mov_b32_e32 v46, s62\n" \
"v_mfma_f32_16x16x32_bf16 v[172:175], a[216:219], a[112:115], 0\n" \
"v_mfma_f32_16x16x32_bf16 v[172:175], a[0:3], a[116:119], v[172:175]\n" \
"v_mfma_f32_16x16x32_bf16 v[168:171], a[216:219], a[96:99], 0\n" \
"ds_read_b64_tr_b16 a[216:217], v10 offset:32832\n" \
"ds_read_b64_tr_b16 a[218:219], v10 offset:37440\n" \
"v_mfma_f32_16x16x32_bf16 v[168:171], a[0:3], a[100:103], v[168:171]\n" \
"ds_read_b64_tr_b16 a[0:1], v10 offset:32864\n" \
"ds_read_b64_tr_b16 a[2:3], v10 offset:37472\n" \
"v_mfma_f32_16x16x32_bf16 v[176:179], a[4:7], a[96:99], 0\n" \
"buffer_load_dwordx4 a[84:87], v32, s[56:59], s65 offen offset:1024 sc1\n" \
"v_mfma_f32_16x16x32_bf16 v[176:179], a[8:11], a[100:103], v[176:179]\n" \
"v_mfma_f32_16x16x32_bf16 v[180:183], a[4:7], a[112:115], 0\n" \
"v_mfma_f32_16x16x32_bf16 v[180:183], a[8:11], a[116:119], v[180:183]\n" \
"v_mfma_f32_16x16x32_bf16 v[248:251], a[4:7], v[64:67], 0\n" \
"v_mfma_f32_16x16x32_bf16 v[248:251], a[8:11], v[68:71], v[248:251]\n" \
"buffer_load_dwordx4 a[72:75], v20, s[56:59], s65 offen offset:2048 sc1\n" \
"v_mfma_f32_16x16x32_bf16 v[252:255], a[4:7], v[80:83], 0\n" \
"ds_read_b64_tr_b16 a[4:5], v10 offset:32896\n" \
"ds_read_b64_tr_b16 a[6:7], v10 offset:37504\n" \
"v_mfma_f32_16x16x32_bf16 v[252:255], a[8:11], v[84:87], v[252:255]\n" \
"ds_read_b64_tr_b16 a[8:9], v10 offset:32928\n" \
"ds_read_b64_tr_b16 a[10:11], v10 offset:37536\n" \
"v_mfma_f32_16x16x32_bf16 v[112:115], a[12:15], a[104:107], v[112:115]\n" \
"v_mfma_f32_16x16x32_bf16 v[116:119], a[12:15], a[120:123], v[116:119]\n" \
"v_mfma_f32_16x16x32_bf16 v[184:187], a[12:15], v[72:75], v[184:187]\n" \
"buffer_load_dwordx4 a[76:79], v20, s[56:59], s65 offen offset:3072 sc1\n" \
"v_mfma_f32_16x16x32_bf16 v[188:191], a[12:15], v[96:99], v[188:191]\n" \
"ds_read_b64_tr_b16 a[12:13], v10 offset:32960\n" \
"ds_read_b64_tr_b16 a[14:15], v10 offset:37568\n" \
"v_mfma_f32_16x16x32_bf16 v[196:199], a[16:19], v[96:99], v[196:199]\n" \
"v_mfma_f32_16x16x32_bf16 v[192:195], a[16:19], v[72:75], v[192:195]\n" \
"v_mfma_f32_16x16x32_bf16 v[124:127], a[16:19], a[120:123], v[124:127]\n" \
"v_mfma_f32_16x16x32_bf16 v[120:123], a[16:19], a[104:107], v[120:123]\n" \
"ds_read_b64_tr_b16 a[16:17], v10 offset:32992\n" \
"ds_read_b64_tr_b16 a[18:19], v10 offset:37600\n" \
"v_mfma_f32_16x16x32_bf16 v[128:131], a[20:23], a[104:107], v[128:131]\n" \
"buffer_load_dwordx4 a[88:91], v32, s[56:59], s65 offen offset:2048 sc1\n" \
"v_mfma_f32_16x16x32_bf16 v[132:135], a[20:23], a[120:123], v[132:135]\n" \
"v_mfma_f32_16x16x32_bf16 v[200:203], a[20:23], v[72:75], v[200:203]\n" \
"v_mfma_f32_16x16x32_bf16 v[204:207], a[20:23], v[96:99], v[204:207]\n" \
"ds_read_b64_tr_b16 a[20:21], v10 offset:33024\n" \
"ds_read_b64_tr_b16 a[22:23], v10 offset:37632\n" \
"s_waitcnt lgkmcnt(10)\n" \
"s_barrier\n" \
"v_mfma_f32_16x16x32_bf16 v[212:215], a[24:27], v[96:99], v[212:215]\n" \
"buffer_load_dwordx4 a[92:95], v32, s[56:59], s65 offen offset:3072 sc1\n" \
"v_mfma_f32_16x16x32_bf16 v[208:211], a[24:27], v[72:75], v[208:211]\n" \
"v_readlane_b32 s65, v11, 46\n" \
"v_mfma_f32_16x16x32_bf16 v[140:143], a[24:27], a[120:123], v[140:143]\n" \
"v_mfma_f32_16x16x32_bf16 v[136:139], a[24:27], a[104:107], v[136:139]\n" \
"ds_read_b64_tr_b16 a[24:25], v35 offset:41984\n" \
"ds_read_b64_tr_b16 a[26:27], v35 offset:44288\n" \
"v_mfma_f32_16x16x32_bf16 v[144:147], a[28:31], a[104:107], v[144:147]\n" \
"v_mfma_f32_16x16x32_bf16 v[148:151], a[28:31], a[120:123], v[148:151]\n" \
"buffer_load_dwordx4 v6, s[80:83], s66 offen offset:2048 lds\n" \
"v_mfma_f32_16x16x32_bf16 v[216:219], a[28:31], v[72:75], v[216:219]\n" \
"v_mfma_f32_16x16x32_bf16 v[220:223], a[28:31], v[96:99], v[220:223]\n" \
"ds_read_b64_tr_b16 a[28:29], v35 offset:52352\n" \
"ds_read_b64_tr_b16 a[30:31], v35 offset:54656\n" \
"v_mfma_f32_16x16x32_bf16 v[228:231], a[192:195], v[96:99], v[228:231]\n" \
"v_mfma_f32_16x16x32_bf16 v[224:227], a[192:195], v[72:75], v[224:227]\n" \
"v_mfma_f32_16x16x32_bf16 v[156:159], a[192:195], a[120:123], v[156:159]\n" \
"buffer_load_dwordx4 v7, s[80:83], s66 offen offset:3072 lds\n" \
"v_mfma_f32_16x16x32_bf16 v[152:155], a[192:195], a[104:107], v[152:155]\n" \
"ds_read_b64_tr_b16 a[192:193], v35 offset:42016\n" \
"ds_read_b64_tr_b16 a[194:195], v35 offset:44320\n" \
"v_mfma_f32_16x16x32_bf16 v[160:163], a[196:199], a[104:107], v[160:163]\n" \
"s_add_u32 m0, s67, 0x2000\n" \
"v_readlane_b32 s67, v11, 48\n" \
"v_mfma_f32_16x16x32_bf16 v[164:167], a[196:199], a[120:123], v[164:167]\n" \
"v_mfma_f32_16x16x32_bf16 v[232:235], a[196:199], v[72:75], v[232:235]\n" \
"v_mfma_f32_16x16x32_bf16 v[236:239], a[196:199], v[96:99], v[236:239]\n" \
"ds_read_b64_tr_b16 a[196:197], v35 offset:52384\n" \
"ds_read_b64_tr_b16 a[198:199], v35 offset:54688\n" \
"v_mfma_f32_16x16x32_bf16 v[244:247], a[200:203], v[96:99], v[244:247]\n" \
"buffer_load_dwordx4 v8, s[80:83], s66 offen offset:0 lds\n" \
"v_mfma_f32_16x16x32_bf16 v[240:243], a[200:203], v[72:75], v[240:243]\n" \
"s_add_u32 m0, s92, 0x0\n" \
"v_readlane_b32 s66, v11, 47\n" \
"v_mfma_f32_16x16x32_bf16 v[172:175], a[200:203], a[120:123], v[172:175]\n" \
"v_mfma_f32_16x16x32_bf16 v[168:171], a[200:203], a[104:107], v[168:171]\n" \
"ds_read_b64_tr_b16 a[200:201], v35 offset:42048\n" \
"ds_read_b64_tr_b16 a[202:203], v35 offset:44352\n" \
"v_mfma_f32_16x16x32_bf16 v[176:179], a[204:207], a[104:107], v[176:179]\n" \
"v_mfma_f32_16x16x32_bf16 v[180:183], a[204:207], a[120:123], v[180:183]\n" \
"buffer_load_dwordx4 v0, s[80:83], s73 offen offset:0 lds\n" \
"v_mfma_f32_16x16x32_bf16 v[248:251], a[204:207], v[72:75], v[248:251]\n" \
"v_mfma_f32_16x16x32_bf16 v[252:255], a[204:207], v[96:99], v[252:255]\n" \
"ds_read_b64_tr_b16 a[204:205], v35 offset:52416\n" \
"ds_read_b64_tr_b16 a[206:207], v35 offset:54720\n" \
"v_mfma_f32_16x16x32_bf16 v[112:115], a[208:211], a[108:111], v[112:115]\n" \
"v_mfma_f32_16x16x32_bf16 v[116:119], a[208:211], a[124:127], v[116:119]\n" \
"v_mfma_f32_16x16x32_bf16 v[184:187], a[208:211], v[76:79], v[184:187]\n" \
"buffer_load_dwordx4 v1, s[80:83], s73 offen offset:1024 lds\n" \
"v_mfma_f32_16x16x32_bf16 v[188:191], a[208:211], v[100:103], v[188:191]\n" \
"ds_read_b64_tr_b16 a[208:209], v35 offset:42080\n" \
"ds_read_b64_tr_b16 a[210:211], v35 offset:44384\n" \
"v_mfma_f32_16x16x32_bf16 v[196:199], a[212:215], v[100:103], v[196:199]\n" \
"v_mfma_f32_16x16x32_bf16 v[192:195], a[212:215], v[76:79], v[192:195]\n" \
"s_waitcnt vmcnt(18)\n" \
"s_mov_b64 exec, 1\n" \
"buffer_store_dword v39, off, s[56:59], s70 offset:0 sc1\n" \
"s_mov_b64 exec, -1\n" \
"v_mfma_f32_16x16x32_bf16 v[124:127], a[212:215], a[124:127], v[124:127]\n" \
"v_readlane_b32 s70, v11, 51\n" \
"v_mfma_f32_16x16x32_bf16 v[120:123], a[212:215], a[108:111], v[120:123]\n" \
"ds_read_b64_tr_b16 a[212:213], v35 offset:52448\n" \
"ds_read_b64_tr_b16 a[214:215], v35 offset:54752\n" \
"v_mfma_f32_16x16x32_bf16 v[128:131], a[216:219], a[108:111], v[128:131]\n" \
"v_mfma_f32_16x16x32_bf16 v[132:135], a[216:219], a[124:127], v[132:135]\n" \
"buffer_load_dwordx4 v2, s[80:83], s73 offen offset:2048 lds\n" \
"v_mfma_f32_16x16x32_bf16 v[200:203], a[216:219], v[76:79], v[200:203]\n" \
"v_mfma_f32_16x16x32_bf16 v[204:207], a[216:219], v[100:103], v[204:207]\n" \
"ds_read_b64_tr_b16 a[216:217], v35 offset:42112\n" \
"ds_read_b64_tr_b16 a[218:219], v35 offset:44416\n" \
"v_mfma_f32_16x16x32_bf16 v[212:215], a[0:3], v[100:103], v[212:215]\n" \
"v_mfma_f32_16x16x32_bf16 v[208:211], a[0:3], v[76:79], v[208:211]\n" \
"v_mfma_f32_16x16x32_bf16 v[140:143], a[0:3], a[124:127], v[140:143]\n" \
"buffer_load_dwordx4 v3, s[80:83], s73 offen offset:3072 lds\n" \
"v_mfma_f32_16x16x32_bf16 v[136:139], a[0:3], a[108:111], v[136:139]\n" \
"ds_read_b64_tr_b16 a[0:1], v35 offset:52480\n" \
"s_waitcnt lgkmcnt(7)\n" \
"v_mfma_f32_16x16x32_bf16 v[144:147], a[4:7], a[108:111], v[144:147]\n" \
"ds_read_b64_tr_b16 a[2:3], v35 offset:54784\n" \
"s_add_u32 m0, s92, 0x1000\n" \
"v_mfma_f32_16x16x32_bf16 v[148:151], a[4:7], a[124:127], v[148:151]\n" \
"v_mfma_f32_16x16x32_bf16 v[216:219], a[4:7], v[76:79], v[216:219]\n" \
"v_mfma_f32_16x16x32_bf16 v[220:223], a[4:7], v[100:103], v[220:223]\n" \
"ds_read_b64_tr_b16 a[4:5], v35 offset:42144\n" \
"ds_read_b64_tr_b16 a[6:7], v35 offset:44448\n" \
"v_mfma_f32_16x16x32_bf16 v[228:231], a[8:11], v[100:103], v[228:231]\n" \
"buffer_load_dwordx4 v4, s[80:83], s73 offen offset:0 lds\n" \
"v_mfma_f32_16x16x32_bf16 v[224:227], a[8:11], v[76:79], v[224:227]\n" \
"v_mfma_f32_16x16x32_bf16 v[156:159], a[8:11], a[124:127], v[156:159]\n" \
"v_mfma_f32_16x16x32_bf16 v[152:155], a[8:11], a[108:111], v[152:155]\n" \
"ds_read_b64_tr_b16 a[8:9], v35 offset:52512\n" \
"ds_read_b64_tr_b16 a[10:11], v35 offset:54816\n" \
"v_mfma_f32_16x16x32_bf16 v[160:163], a[12:15], a[108:111], v[160:163]\n" \
"v_mfma_f32_16x16x32_bf16 v[164:167], a[12:15], a[124:127], v[164:167]\n" \
"buffer_load_dwordx4 v5, s[80:83], s73 offen offset:1024 lds\n" \
"v_mfma_f32_16x16x32_bf16 v[232:235], a[12:15], v[76:79], v[232:235]\n" \
"v_mfma_f32_16x16x32_bf16 v[236:239], a[12:15], v[100:103], v[236:239]\n" \
"ds_read_b64_tr_b16 a[12:13], v35 offset:42176\n" \
"ds_read_b64_tr_b16 a[14:15], v35 offset:44480\n" \
"v_mfma_f32_16x16x32_bf16 v[244:247], a[16:19], v[100:103], v[244:247]\n" \
"v_mfma_f32_16x16x32_bf16 v[240:243], a[16:19], v[76:79], v[240:243]\n" \
"v_mfma_f32_16x16x32_bf16 v[172:175], a[16:19], a[124:127], v[172:175]\n" \
"buffer_load_dwordx4 v6, s[80:83], s73 offen offset:2048 lds\n" \
"v_mfma_f32_16x16x32_bf16 v[168:171], a[16:19], a[108:111], v[168:171]\n" \
"ds_read_b64_tr_b16 a[16:17], v35 offset:52544\n" \
"ds_read_b64_tr_b16 a[18:19], v35 offset:54848\n" \
"v_mfma_f32_16x16x32_bf16 v[176:179], a[20:23], a[108:111], v[176:179]\n" \
"v_mfma_f32_16x16x32_bf16 v[180:183], a[20:23], a[124:127], v[180:183]\n" \
"v_mfma_f32_16x16x32_bf16 v[248:251], a[20:23], v[76:79], v[248:251]\n" \
"v_mfma_f32_16x16x32_bf16 v[252:255], a[20:23], v[100:103], v[252:255]\n" \
"ds_read_b64_tr_b16 a[20:21], v35 offset:42208\n" \
"ds_read_b64_tr_b16 a[22:23], v35 offset:44512\n" \
"v_mfma_f32_16x16x32_bf16 v[112:115], a[24:27], a[220:223], v[112:115]\n" \
"buffer_load_dwordx4 v7, s[80:83], s73 offen offset:3072 lds\n" \
"v_mfma_f32_16x16x32_bf16 v[112:115], a[28:31], a[224:227], v[112:115]\n" \
"s_add_u32 m0, s92, 0x2000\n" \
"v_readlane_b32 s92, v11, 46\n" \
"v_mfma_f32_16x16x32_bf16 v[116:119], a[24:27], a[228:231], v[116:119]\n" \
"v_mfma_f32_16x16x32_bf16 v[116:119], a[28:31], a[232:235], v[116:119]\n" \
"v_mfma_f32_16x16x32_bf16 v[184:187], a[24:27], a[236:239], v[184:187]\n" \
"v_mfma_f32_16x16x32_bf16 v[184:187], a[28:31], a[240:243], v[184:187]\n" \
"buffer_load_dwordx4 v8, s[80:83], s73 offen offset:0 lds\n" \
"v_mfma_f32_16x16x32_bf16 v[188:191], a[24:27], a[244:247], v[188:191]\n" \
"ds_read_b64_tr_b16 a[24:25], v35 offset:52576\n" \
"ds_read_b64_tr_b16 a[26:27], v35 offset:54880\n" \
"v_mfma_f32_16x16x32_bf16 v[188:191], a[28:31], a[248:251], v[188:191]\n" \
"s_mov_b32 m0, s94\n" \
"ds_read_b64_tr_b16 a[28:29], v35 offset:42240\n" \
"v_mfma_f32_16x16x32_bf16 v[196:199], a[192:195], a[244:247], v[196:199]\n" \
"ds_read_b64_tr_b16 a[30:31], v35 offset:44544\n" \
"v_readlane_b32 s73, v11, 6\n" \
"v_mfma_f32_16x16x32_bf16 v[196:199], a[196:199], a[248:251], v[196:199]\n" \
"v_mfma_f32_16x16x32_bf16 v[192:195], a[192:195], a[236:239], v[192:195]\n" \
"buffer_load_dwordx4 v9, s[84:87], 0 offen lds\n" \
"v_mfma_f32_16x16x32_bf16 v[192:195], a[196:199], a[240:243], v[192:195]\n" \
"v_mfma_f32_16x16x32_bf16 v[124:127], a[192:195], a[228:231], v[124:127]\n" \
"v_mfma_f32_16x16x32_bf16 v[124:127], a[196:199], a[232:235], v[124:127]\n" \
"v_mfma_f32_16x16x32_bf16 v[120:123], a[192:195], a[220:223], v[120:123]\n" \
"ds_read_b64_tr_b16 a[192:193], v35 offset:52608\n" \
"ds_read_b64_tr_b16 a[194:195], v35 offset:54912\n" \
"v_mfma_f32_16x16x32_bf16 v[120:123], a[196:199], a[224:227], v[120:123]\n" \
"ds_read_b64_tr_b16 a[196:197], v33 offset:0\n" \
"ds_read_b64_tr_b16 a[198:199], v33 offset:4608\n" \
"v_mfma_f32_16x16x32_bf16 v[128:131], a[200:203], a[220:223], v[128:131]\n" \
"buffer_load_dwordx4 a[128:131], v20, s[56:59], s68 offen offset:0 sc1\n" \
"v_mfma_f32_16x16x32_bf16 v[128:131], a[204:207], a[224:227], v[128:131]\n" \
"v_mfma_f32_16x16x32_bf16 v[132:135], a[200:203], a[228:231], v[132:135]\n" \
"v_mfma_f32_16x16x32_bf16 v[132:135], a[204:207], a[232:235], v[132:135]\n" \
"v_mfma_f32_16x16x32_bf16 v[200:203], a[200:203], a[236:239], v[200:203]\n" \
"v_mfma_f32_16x16x32_bf16 v[200:203], a[204:207], a[240:243], v[200:203]\n" \
"buffer_load_dwordx4 a[132:135], v20, s[56:59], s68 offen offset:1024 sc1\n" \
"v_mfma_f32_16x16x32_bf16 v[204:207], a[200:203], a[244:247], v[204:207]\n" \
"ds_read_b64_tr_b16 a[200:201], v33 offset:9216\n" \
"ds_read_b64_tr_b16 a[202:203], v33 offset:13824\n" \
"v_mfma_f32_16x16x32_bf16 v[204:207], a[204:207], a[248:251], v[204:207]\n" \
"ds_read_b64_tr_b16 a[204:205], v33 offset:32\n" \
"s_waitcnt lgkmcnt(7)\n" \
"v_mfma_f32_16x16x32_bf16 v[212:215], a[208:211], a[244:247], v[212:215]\n" \
"ds_read_b64_tr_b16 a[206:207], v33 offset:4640\n" \
"v_mfma_f32_16x16x32_bf16 v[212:215], a[212:215], a[248:251], v[212:215]\n" \
"v_mfma_f32_16x16x32_bf16 v[208:211], a[208:211], a[236:239], v[208:211]\n" \
"buffer_load_dword v47, v38, s[56:59], s71 offen sc1\n" \
"v_mfma_f32_16x16x32_bf16 v[208:211], a[212:215], a[240:243], v[208:211]\n" \
"v_mfma_f32_16x16x32_bf16 v[140:143], a[208:211], a[228:231], v[140:143]\n" \
"v_mfma_f32_16x16x32_bf16 v[140:143], a[212:215], a[232:235], v[140:143]\n" \
"v_mfma_f32_16x16x32_bf16 v[136:139], a[208:211], a[220:223], v[136:139]\n" \
"ds_read_b64_tr_b16 a[208:209], v33 offset:9248\n" \
"ds_read_b64_tr_b16 a[210:211], v33 offset:13856\n" \
"v_mfma_f32_16x16x32_bf16 v[136:139], a[212:215], a[224:227], v[136:139]\n" \
"ds_read_b64_tr_b16 a[212:213], v33 offset:64\n" \
"ds_read_b64_tr_b16 a[214:215], v33 offset:4672\n" \
"v_mfma_f32_16x16x32_bf16 v[144:147], a[216:219], a[220:223], v[144:147]\n" \
"buffer_load_dwordx4 a[144:147], v32, s[56:59], s68 offen offset:0 sc1\n" \
"v_mfma_f32_16x16x32_bf16 v[144:147], a[0:3], a[224:227], v[144:147]\n" \
"v_mfma_f32_16x16x32_bf16 v[148:151], a[216:219], a[228:231], v[148:151]\n" \
"v_mfma_f32_16x16x32_bf16 v[148:151], a[0:3], a[232:235], v[148:151]\n" \
"v_mfma_f32_16x16x32_bf16 v[216:219], a[216:219], a[236:239], v[216:219]\n" \
"v_mfma_f32_16x16x32_bf16 v[216:219], a[0:3], a[240:243], v[216:219]\n" \
"buffer_load_dwordx4 a[148:151], v32, s[56:59], s68 offen offset:1024 sc1\n" \
"v_mfma_f32_16x16x32_bf16 v[220:223], a[216:219], a[244:247], v[220:223]\n" \
"ds_read_b64_tr_b16 a[216:217], v33 offset:9280\n" \
"ds_read_b64_tr_b16 a[218:219], v33 offset:13888\n" \
"v_mfma_f32_16x16x32_bf16 v[220:223], a[0:3], a[248:251], v[220:223]\n" \
"ds_read_b64_tr_b16 a[0:1], v33 offset:96\n" \
"ds_read_b64_tr_b16 a[2:3], v33 offset:4704\n" \
"v_mfma_f32_16x16x32_bf16 v[228:231], a[4:7], a[244:247], v[228:231]\n" \
"v_mfma_f32_16x16x32_bf16 v[228:231], a[8:11], a[248:251], v[228:231]\n" \
"v_mfma_f32_16x16x32_bf16 v[224:227], a[4:7], a[236:239], v[224:227]\n" \
"buffer_load_dwordx4 a[152:155], v32, s[56:59], s68 offen offset:2048 sc1\n" \
"v_mfma_f32_16x16x32_bf16 v[224:227], a[8:11], a[240:243], v[224:227]\n" \
"v_mfma_f32_16x16x32_bf16 v[156:159], a[4:7], a[228:231], v[156:159]\n" \
"v_mfma_f32_16x16x32_bf16 v[156:159], a[8:11], a[232:235], v[156:159]\n" \
"v_mfma_f32_16x16x32_bf16 v[152:155], a[4:7], a[220:223], v[152:155]\n" \
"ds_read_b64_tr_b16 a[4:5], v33 offset:9312\n" \
"ds_read_b64_tr_b16 a[6:7], v33 offset:13920\n" \
"v_mfma_f32_16x16x32_bf16 v[152:155], a[8:11], a[224:227], v[152:155]\n" \
"ds_read_b64_tr_b16 a[8:9], v33 offset:128\n" \
"ds_read_b64_tr_b16 a[10:11], v33 offset:4736\n" \
"v_mfma_f32_16x16x32_bf16 v[160:163], a[12:15], a[220:223], v[160:163]\n" \
"buffer_load_dwordx4 a[156:159], v32, s[56:59], s68 offen offset:3072 sc1\n" \
"v_mfma_f32_16x16x32_bf16 v[160:163], a[16:19], a[224:227], v[160:163]\n" \
"v_mfma_f32_16x16x32_bf16 v[164:167], a[12:15], a[228:231], v[164:167]\n" \
"v_mfma_f32_16x16x32_bf16 v[164:167], a[16:19], a[232:235], v[164:167]\n" \
"v_mfma_f32_16x16x32_bf16 v[232:235], a[12:15], a[236:239], v[232:235]\n" \
"v_mfma_f32_16x16x32_bf16 v[232:235], a[16:19], a[240:243], v[232:235]\n" \
"buffer_load_dwordx4 a[136:139], v20, s[56:59], s68 offen offset:2048 sc1\n" \
"v_mfma_f32_16x16x32_bf16 v[236:239], a[12:15], a[244:247], v[236:239]\n" \
"ds_read_b64_tr_b16 a[12:13], v33 offset:9344\n" \
"ds_read_b64_tr_b16 a[14:15], v33 offset:13952\n" \
"v_mfma_f32_16x16x32_bf16 v[236:239], a[16:19], a[248:251], v[236:239]\n" \
"ds_read_b64_tr_b16 a[16:17], v33 offset:160\n" \
"ds_read_b64_tr_b16 a[18:19], v33 offset:4768\n" \
"v_mfma_f32_16x16x32_bf16 v[244:247], a[20:23], a[244:247], v[244:247]\n" \
"v_mfma_f32_16x16x32_bf16 v[244:247], a[24:27], a[248:251], v[244:247]\n" \
"v_mfma_f32_16x16x32_bf16 v[240:243], a[20:23], a[236:239], v[240:243]\n" \
"buffer_load_dwordx4 a[140:143], v20, s[56:59], s68 offen offset:3072 sc1\n" \
"v_mfma_f32_16x16x32_bf16 v[240:243], a[24:27], a[240:243], v[240:243]\n" \
"v_readlane_b32 s68, v11, 49\n" \
"v_mfma_f32_16x16x32_bf16 v[172:175], a[20:23], a[228:231], v[172:175]\n" \
"v_mfma_f32_16x16x32_bf16 v[172:175], a[24:27], a[232:235], v[172:175]\n" \
"v_mfma_f32_16x16x32_bf16 v[168:171], a[20:23], a[220:223], v[168:171]\n" \
"ds_read_b64_tr_b16 a[20:21], v33 offset:9376\n" \
"ds_read_b64_tr_b16 a[22:23], v33 offset:13984\n" \
"v_mfma_f32_16x16x32_bf16 v[168:171], a[24:27], a[224:227], v[168:171]\n" \
"ds_read_b64_tr_b16 a[24:25], v33 offset:192\n" \
"ds_read_b64_tr_b16 a[26:27], v33 offset:4800\n" \
"v_mfma_f32_16x16x32_bf16 v[176:179], a[28:31], a[220:223], v[176:179]\n" \
"s_waitcnt lgkmcnt(8)\n" \
"v_mfma_f32_16x16x32_bf16 v[176:179], a[192:195], a[224:227], v[176:179]\n" \
"buffer_load_dwordx4 a[32:35], v20, s[56:59], s69 offen offset:0 sc1\n" \
"v_mfma_f32_16x16x32_bf16 v[180:183], a[28:31], a[228:231], v[180:183]\n" \
"v_mfma_f32_16x16x32_bf16 v[180:183], a[192:195], a[232:235], v[180:183]\n" \
"v_mfma_f32_16x16x32_bf16 v[248:251], a[28:31], a[236:239], v[248:251]\n" \
"v_mfma_f32_16x16x32_bf16 v[248:251], a[192:195], a[240:243], v[248:251]\n" \
"v_mfma_f32_16x16x32_bf16 v[252:255], a[28:31], a[244:247], v[252:255]\n" \
"ds_read_b64_tr_b16 a[28:29], v33 offset:9408\n" \
"ds_read_b64_tr_b16 a[30:31], v33 offset:14016\n" \
"v_mfma_f32_16x16x32_bf16 v[252:255], a[192:195], a[248:251], v[252:255]\n" \
"ds_read_b64_tr_b16 a[192:193], v33 offset:224\n" \
"s_waitcnt vmcnt(18)\n" \
"v_mfma_f32_16x16x32_bf16 v[112:115], a[196:199], a[64:67], v[112:115]\n" \
"buffer_load_dwordx4 a[36:39], v20, s[56:59], s69 offen offset:1024 sc1\n" \
"v_mfma_f32_16x16x32_bf16 v[112:115], a[200:203], a[68:71], v[112:115]\n" \
"ds_read_b64_tr_b16 a[194:195], v33 offset:4832\n" \
"v_mfma_f32_16x16x32_bf16 v[116:119], a[196:199], a[80:83], v[116:119]\n" \
"ds_read_b64_tr_b16 a[196:197], v33 offset:9440\n" \
"ds_read_b64_tr_b16 a[198:199], v33 offset:14048\n" \
"v_mfma_f32_16x16x32_bf16 v[116:119], a[200:203], a[84:87], v[116:119]\n" \
"ds_read_b64_tr_b16 a[200:201], v33 offset:256\n" \
"ds_read_b64_tr_b16 a[202:203], v33 offset:4864\n" \
"v_mfma_f32_16x16x32_bf16 v[124:127], a[204:207], a[80:83], v[124:127]\n" \
"v_mfma_f32_16x16x32_bf16 v[124:127], a[208:211], a[84:87], v[124:127]\n" \
"buffer_load_dwordx4 a[48:51], v32, s[56:59], s69 offen offset:0 sc1\n" \
"v_mfma_f32_16x16x32_bf16 v[120:123], a[204:207], a[64:67], v[120:123]\n" \
"ds_read_b64_tr_b16 a[204:205], v33 offset:9472\n" \
"ds_read_b64_tr_b16 a[206:207], v33 offset:14080\n" \
"v_mfma_f32_16x16x32_bf16 v[120:123], a[208:211], a[68:71], v[120:123]\n" \
"ds_read_b64_tr_b16 a[208:209], v33 offset:18432\n" \
"ds_read_b64_tr_b16 a[210:211], v33 offset:23040\n" \
"v_mfma_f32_16x16x32_bf16 v[128:131], a[212:215], a[64:67], v[128:131]\n" \
"v_mfma_f32_16x16x32_bf16 v[128:131], a[216:219], a[68:71], v[128:131]\n" \
"v_mfma_f32_16x16x32_bf16 v[132:135], a[212:215], a[80:83], v[132:135]\n" \
"buffer_load_dwordx4 a[52:55], v32, s[56:59], s69 offen offset:1024 sc1\n" \
"v_mfma_f32_16x16x32_bf16 v[132:135], a[216:219], a[84:87], v[132:135]\n" \
"ds_read_b64_tr_b16 a[212:213], v33 offset:27648\n" \
"ds_read_b64_tr_b16 a[214:215], v33 offset:32256\n" \
"v_mfma_f32_16x16x32_bf16 v[140:143], a[0:3], a[80:83], v[140:143]\n" \
"ds_read_b64_tr_b16 a[216:217], v33 offset:18464\n" \
"ds_read_b64_tr_b16 a[218:219], v33 offset:23072\n" \
"v_mfma_f32_16x16x32_bf16 v[140:143], a[4:7], a[84:87], v[140:143]\n" \
"s_waitcnt vmcnt(10)\n" \
"v_cmp_ne_u32_e32 vcc, s62, v47\n" \
"v_mfma_f32_16x16x32_bf16 v[136:139], a[0:3], a[64:67], v[136:139]\n" \
"ds_read_b64_tr_b16 a[0:1], v33 offset:27680\n" \
".fill (L_ab + 36864 - .) / 4, 4, 0xBF800000\n" \
"L_ap9:\n" \
"s_cbranch_execz L_ap0\n" \
"ds_read_b64_tr_b16 a[2:3], v33 offset:32288\n" \
"v_mfma_f32_16x16x32_bf16 v[136:139], a[4:7], a[68:71], v[136:139]\n" \
"buffer_load_dwordx4 a[56:59], v32, s[56:59], s69 offen offset:2048 sc1\n" \
"v_mfma_f32_16x16x32_bf16 v[144:147], a[8:11], a[64:67], v[144:147]\n" \
"ds_read_b64_tr_b16 a[4:5], v33 offset:18496\n" \
"s_waitcnt lgkmcnt(13)\n" \
"v_mfma_f32_16x16x32_bf16 v[144:147], a[12:15], a[68:71], v[144:147]\n" \
"ds_read_b64_tr_b16 a[6:7], v33 offset:23104\n" \
"s_cbranch_vccnz L_sb_%=\n" \
"L_rb_%=:\n" \
"v_mfma_f32_16x16x32_bf16 v[148:151], a[8:11], a[80:83], v[148:151]\n" \
"ds_read_b64_tr_b16 a[8:9], v33 offset:27712\n" \
"ds_read_b64_tr_b16 a[10:11], v33 offset:32320\n" \
"v_mfma_f32_16x16x32_bf16 v[148:151], a[12:15], a[84:87], v[148:151]\n" \
"ds_read_b64_tr_b16 a[12:13], v33 offset:18528\n" \
"ds_read_b64_tr_b16 a[14:15], v33 offset:23136\n" \
"buffer_load_dwordx4 a[60:63], v32, s[56:59], s69 offen offset:3072 sc1\n" \
"v_mfma_f32_16x16x32_bf16 v[156:159], a[16:19], a[80:83], v[156:159]\n" \
"v_readlane_b32 s71, v11, 52\n" \
"v_cmp_eq_u32_e32 vcc, 0, v44\n" \
"v_mfma_f32_16x16x32_bf16 v[156:159], a[20:23], a[84:87], v[156:159]\n" \
"v_mfma_f32_16x16x32_bf16 v[152:155], a[16:19], a[64:67], v[152:155]\n" \
"ds_read_b64_tr_b16 a[16:17], v33 offset:27744\n" \
"ds_read_b64_tr_b16 a[18:19], v33 offset:32352\n" \
"v_mfma_f32_16x16x32_bf16 v[152:155], a[20:23], a[68:71], v[152:155]\n" \
"ds_read_b64_tr_b16 a[20:21], v33 offset:18560\n" \
"ds_read_b64_tr_b16 a[22:23], v33 offset:23168\n" \
"buffer_load_dwordx4 a[40:43], v20, s[56:59], s69 offen offset:2048 sc1\n" \
"v_mfma_f32_16x16x32_bf16 v[160:163], a[24:27], a[64:67], v[160:163]\n" \
"v_cndmask_b32_e32 v50, 0, v45, vcc\n" \
"v_cmp_eq_u32_e32 vcc, 1, v44\n" \
"v_mfma_f32_16x16x32_bf16 v[160:163], a[28:31], a[68:71], v[160:163]\n" \
"v_and_b32_e32 v44, 0x3c0, v20\n" \
"v_cndmask_b32_e32 v51, 0, v45, vcc\n" \
"v_mfma_f32_16x16x32_bf16 v[164:167], a[24:27], a[80:83], v[164:167]\n" \
"ds_read_b64_tr_b16 a[24:25], v33 offset:27776\n" \
"ds_read_b64_tr_b16 a[26:27], v33 offset:32384\n" \
"v_mfma_f32_16x16x32_bf16 v[164:167], a[28:31], a[84:87], v[164:167]\n" \
"ds_read_b64_tr_b16 a[28:29], v33 offset:18592\n" \
"ds_read_b64_tr_b16 a[30:31], v33 offset:23200\n" \
"buffer_load_dwordx4 a[44:47], v20, s[56:59], s69 offen offset:3072 sc1\n" \
"v_mfma_f32_16x16x32_bf16 v[172:175], a[192:195], a[80:83], v[172:175]\n" \
"v_readlane_b32 s69, v11, 50\n" \
"v_bfe_u32 v45, v20, 1, 5\n" \
"v_mfma_f32_16x16x32_bf16 v[172:175], a[196:199], a[84:87], v[172:175]\n" \
"v_and_b32_e32 v45, 24, v45\n" \
"v_add_u32_e32 v45, 0x4800, v45\n" \
"v_mfma_f32_16x16x32_bf16 v[168:171], a[192:195], a[64:67], v[168:171]\n" \
"ds_read_b64_tr_b16 a[192:193], v33 offset:27808\n" \
"ds_read_b64_tr_b16 a[194:195], v33 offset:32416\n" \
"v_mfma_f32_16x16x32_bf16 v[168:171], a[196:199], a[68:71], v[168:171]\n" \
"ds_read_b64_tr_b16 a[196:197], v33 offset:18624\n" \
"s_waitcnt lgkmcnt(13)\n" \
"v_mfma_f32_16x16x32_bf16 v[176:179], a[200:203], a[64:67], v[176:179]\n" \
"ds_read_b64_tr_b16 a[198:199], v33 offset:23232\n" \
"v_add_u32_e32 v36, v44, v45\n" \
"v_mfma_f32_16x16x32_bf16 v[176:179], a[204:207], a[68:71], v[176:179]\n" \
"buffer_load_dwordx4 a[220:223], v20, s[56:59], s64 offen offset:0 sc1\n" \
"v_mfma_f32_16x16x32_bf16 v[180:183], a[200:203], a[80:83], v[180:183]\n" \
"ds_read_b64_tr_b16 a[200:201], v33 offset:27840\n" \
"ds_read_b64_tr_b16 a[202:203], v33 offset:32448\n" \
"v_mfma_f32_16x16x32_bf16 v[180:183], a[204:207], a[84:87], v[180:183]\n" \
"ds_read_b64_tr_b16 a[204:205], v33 offset:18656\n" \
"ds_read_b64_tr_b16 a[206:207], v33 offset:23264\n" \
"v_mfma_f32_16x16x32_bf16 v[112:115], a[208:211], a[72:75], v[112:115]\n" \
"v_mfma_f32_16x16x32_bf16 v[112:115], a[212:215], a[76:79], v[112:115]\n" \
"v_mfma_f32_16x16x32_bf16 v[116:119], a[208:211], a[88:91], v[116:119]\n" \
"ds_read_b64_tr_b16 a[208:209], v33 offset:27872\n" \
"ds_read_b64_tr_b16 a[210:211], v33 offset:32480\n" \
"v_mfma_f32_16x16x32_bf16 v[116:119], a[212:215], a[92:95], v[116:119]\n" \
"ds_read_b64_tr_b16 a[212:213], v33 offset:18688\n" \
"ds_read_b64_tr_b16 a[214:215], v33 offset:23296\n" \
"v_mfma_f32_16x16x32_bf16 v[124:127], a[216:219], a[88:91], v[124:127]\n" \
"buffer_load_dwordx4 a[224:227], v20, s[56:59], s64 offen offset:1024 sc1\n" \
"v_mfma_f32_16x16x32_bf16 v[124:127], a[0:3], a[92:95], v[124:127]\n" \
"v_mfma_f32_16x16x32_bf16 v[120:123], a[216:219], a[72:75], v[120:123]\n" \
"ds_read_b64_tr_b16 a[216:217], v33 offset:27904\n" \
"ds_read_b64_tr_b16 a[218:219], v33 offset:32512\n" \
"v_mfma_f32_16x16x32_bf16 v[120:123], a[0:3], a[76:79], v[120:123]\n" \
"s_barrier\n" \
"v_mfma_f32_16x16x32_bf16 v[128:131], a[4:7], a[72:75], v[128:131]\n" \
"ds_read_b64_tr_b16 a[0:1], v33 offset:36864\n" \
"ds_read_b64_tr_b16 a[2:3], v33 offset:41472\n" \
"v_mfma_f32_16x16x32_bf16 v[128:131], a[8:11], a[76:79], v[128:131]\n" \
"buffer_load_dwordx4 a[236:239], v32, s[56:59], s64 offen offset:0 sc1\n" \
"v_mfma_f32_16x16x32_bf16 v[132:135], a[4:7], a[88:91], v[132:135]\n" \
"ds_read_b64_tr_b16 a[4:5], v33 offset:46080\n" \
"ds_read_b64_tr_b16 a[6:7], v33 offset:50688\n" \
"v_mfma_f32_16x16x32_bf16 v[132:135], a[8:11], a[92:95], v[132:135]\n" \
"ds_read_b64_tr_b16 a[8:9], v33 offset:55296\n" \
"s_waitcnt lgkmcnt(15)\n" \
"v_mfma_f32_16x16x32_bf16 v[140:143], a[12:15], a[88:91], v[140:143]\n" \
"ds_read_b64_tr_b16 a[10:11], v33 offset:59904\n" \
"v_mfma_f32_16x16x32_bf16 v[140:143], a[16:19], a[92:95], v[140:143]\n" \
"v_mfma_f32_16x16x32_bf16 v[136:139], a[12:15], a[72:75], v[136:139]\n" \
"ds_read_b64_tr_b16 a[12:13], v10 offset:0\n" \
"ds_read_b64_tr_b16 a[14:15], v10 offset:4608\n" \
"v_mfma_f32_16x16x32_bf16 v[136:139], a[16:19], a[76:79], v[136:139]\n" \
"ds_read_b64_tr_b16 a[16:17], v34 offset:0\n" \
"ds_read_b64_tr_b16 a[18:19], v34 offset:4608\n" \
"v_mfma_f32_16x16x32_bf16 v[144:147], a[20:23], a[72:75], v[144:147]\n" \
"buffer_load_dwordx4 a[240:243], v32, s[56:59], s64 offen offset:1024 sc1\n" \
"v_mfma_f32_16x16x32_bf16 v[144:147], a[24:27], a[76:79], v[144:147]\n" \
"v_mfma_f32_16x16x32_bf16 v[148:151], a[20:23], a[88:91], v[148:151]\n" \
"ds_read_b64_tr_b16 a[20:21], v34 offset:9216\n" \
"ds_read_b64_tr_b16 a[22:23], v34 offset:13824\n" \
"v_mfma_f32_16x16x32_bf16 v[148:151], a[24:27], a[92:95], v[148:151]\n" \
"ds_read_b64_tr_b16 a[24:25], v34 offset:18432\n" \
"ds_read_b64_tr_b16 a[26:27], v34 offset:23040\n" \
"v_mfma_f32_16x16x32_bf16 v[156:159], a[28:31], a[88:91], v[156:159]\n" \
"v_mfma_f32_16x16x32_bf16 v[156:159], a[192:195], a[92:95], v[156:159]\n" \
"buffer_load_dwordx4 a[244:247], v32, s[56:59], s64 offen offset:2048 sc1\n" \
"v_mfma_f32_16x16x32_bf16 v[152:155], a[28:31], a[72:75], v[152:155]\n" \
"ds_read_b64_tr_b16 a[28:29], v10 offset:9216\n" \
"ds_read_b64_tr_b16 a[30:31], v10 offset:13824\n" \
"v_mfma_f32_16x16x32_bf16 v[152:155], a[192:195], a[76:79], v[152:155]\n" \
"ds_read_b64_tr_b16 a[192:193], v33 offset:36896\n" \
"ds_read_b64_tr_b16 a[194:195], v33 offset:41504\n" \
"v_mfma_f32_16x16x32_bf16 v[160:163], a[196:199], a[72:75], v[160:163]\n" \
"s_waitcnt lgkmcnt(15)\n" \
"v_mfma_f32_16x16x32_bf16 v[160:163], a[200:203], a[76:79], v[160:163]\n" \
"v_mfma_f32_16x16x32_bf16 v[164:167], a[196:199], a[88:91], v[164:167]\n" \
"ds_read_b64_tr_b16 a[196:197], v33 offset:46112\n" \
"ds_read_b64_tr_b16 a[198:199], v33 offset:50720\n" \
"v_mfma_f32_16x16x32_bf16 v[164:167], a[200:203], a[92:95], v[164:167]\n" \
"ds_read_b64_tr_b16 a[200:201], v33 offset:55328\n" \
"ds_read_b64_tr_b16 a[202:203], v33 offset:59936\n" \
"v_mfma_f32_16x16x32_bf16 v[172:175], a[204:207], a[88:91], v[172:175]\n" \
"buffer_load_dwordx4 a[248:251], v32, s[56:59], s64 offen offset:3072 sc1\n" \
"v_mfma_f32_16x16x32_bf16 v[172:175], a[208:211], a[92:95], v[172:175]\n" \
"v_mfma_f32_16x16x32_bf16 v[168:171], a[204:207], a[72:75], v[168:171]\n" \
"ds_read_b64_tr_b16 a[204:205], v10 offset:32\n" \
"ds_read_b64_tr_b16 a[206:207], v10 offset:4640\n" \
"v_mfma_f32_16x16x32_bf16 v[168:171], a[208:211], a[76:79], v[168:171]\n" \
"ds_read_b64_tr_b16 a[208:209], v34 offset:32\n" \
"ds_read_b64_tr_b16 a[210:211], v34 offset:4640\n" \
"v_mfma_f32_16x16x32_bf16 v[176:179], a[212:215], a[72:75], v[176:179]\n" \
"v_mfma_f32_16x16x32_bf16 v[176:179], a[216:219], a[76:79], v[176:179]\n" \
"buffer_load_dwordx4 a[228:231], v20, s[56:59], s64 offen offset:2048 sc1\n" \
"v_mfma_f32_16x16x32_bf16 v[180:183], a[212:215], a[88:91], v[180:183]\n" \
"ds_read_b64_tr_b16 a[212:213], v34 offset:9248\n" \
"ds_read_b64_tr_b16 a[214:215], v34 offset:13856\n" \
"v_mfma_f32_16x16x32_bf16 v[180:183], a[216:219], a[92:95], v[180:183]\n" \
"ds_read_b64_tr_b16 a[216:217], v34 offset:18464\n" \
"ds_read_b64_tr_b16 a[218:219], v34 offset:23072\n" \
"v_mfma_f32_16x16x32_bf16 v[112:115], a[0:3], a[128:131], v[112:115]\n" \
"s_waitcnt lgkmcnt(15)\n" \
"v_mfma_f32_16x16x32_bf16 v[112:115], a[4:7], a[132:135], v[112:115]\n" \
"s_waitcnt vmcnt(18)\n" \
"v_mfma_f32_16x16x32_bf16 v[116:119], a[0:3], a[144:147], v[116:119]\n" \
"ds_read_b64_tr_b16 a[0:1], v10 offset:9248\n" \
"ds_read_b64_tr_b16 a[2:3], v10 offset:13856\n" \
"v_mfma_f32_16x16x32_bf16 v[116:119], a[4:7], a[148:151], v[116:119]\n" \
"ds_read_b64_tr_b16 a[4:5], v33 offset:36928\n" \
"ds_read_b64_tr_b16 a[6:7], v33 offset:41536\n" \
"v_mfma_f32_16x16x32_bf16 v[116:119], a[8:11], a[152:155], v[116:119]\n" \
"s_waitcnt vmcnt(17)\n" \
"v_mfma_f32_16x16x32_bf16 v[116:119], a[12:15], a[156:159], v[116:119]\n" \
"s_waitcnt vmcnt(16)\n" \
"v_mfma_f32_16x16x32_bf16 v[112:115], a[8:11], a[136:139], v[112:115]\n" \
"ds_read_b64_tr_b16 a[8:9], v33 offset:46144\n" \
"s_waitcnt vmcnt(15)\n" \
"v_mfma_f32_16x16x32_bf16 v[112:115], a[12:15], a[140:143], v[112:115]\n" \
"ds_read_b64_tr_b16 a[10:11], v33 offset:50752\n" \
"s_waitcnt vmcnt(14)\n" \
"v_mfma_f32_16x16x32_bf16 v[112:115], a[16:19], a[32:35], v[112:115]\n" \
"ds_read_b64_tr_b16 a[12:13], v33 offset:55360\n" \
"s_waitcnt vmcnt(13)\n" \
"v_mfma_f32_16x16x32_bf16 v[112:115], a[20:23], a[36:39], v[112:115]\n" \
"ds_read_b64_tr_b16 a[14:15], v33 offset:59968\n" \
"s_waitcnt vmcnt(12)\n" \
"v_mfma_f32_16x16x32_bf16 v[116:119], a[16:19], a[48:51], v[116:119]\n" \
"ds_read_b64_tr_b16 a[16:17], v10 offset:64\n" \
"s_waitcnt vmcnt(11)\n" \
"v_mfma_f32_16x16x32_bf16 v[116:119], a[20:23], a[52:55], v[116:119]\n" \
"ds_read_b64_tr_b16 a[18:19], v10 offset:4672\n" \
"s_waitcnt vmcnt(10)\n" \
"v_mfma_f32_16x16x32_bf16 v[116:119], a[24:27], a[56:59], v[116:119]\n" \
"ds_read_b64_tr_b16 a[20:21], v34 offset:64\n" \
"ds_read_b64_tr_b16 a[22:23], v34 offset:4672\n" \
"buffer_load_dwordx4 a[232:235], v20, s[56:59], s64 offen offset:3072 sc1\n" \
"s_waitcnt vmcnt(10) lgkmcnt(12)\n" \
"v_mfma_f32_16x16x32_bf16 v[116:119], a[28:31], a[60:63], v[116:119]\n" \
"buffer_load_dwordx4 a[160:163], v20, s[56:59], s72 offen offset:0 sc1\n" \
"s_waitcnt vmcnt(10)\n" \
"v_mfma_f32_16x16x32_bf16 v[112:115], a[24:27], a[40:43], v[112:115]\n" \
"ds_read_b64_tr_b16 a[24:25], v34 offset:9280\n" \
"ds_read_b64_tr_b16 a[26:27], v34 offset:13888\n" \
"buffer_load_dwordx4 a[164:167], v20, s[56:59], s72 offen offset:1024 sc1\n" \
"s_waitcnt vmcnt(10)\n" \
"v_mfma_f32_16x16x32_bf16 v[112:115], a[28:31], a[44:47], v[112:115]\n" \
"ds_read_b64_tr_b16 a[28:29], v34 offset:18496\n" \
"ds_read_b64_tr_b16 a[30:31], v34 offset:23104\n" \
"v_mfma_f32_16x16x32_bf16 v[120:123], a[192:195], a[128:131], v[120:123]\n" \
"buffer_load_dwordx4 a[176:179], v32, s[56:59], s72 offen offset:0 sc1\n" \
"v_mfma_f32_16x16x32_bf16 v[120:123], a[196:199], a[132:135], v[120:123]\n" \
"v_mfma_f32_16x16x32_bf16 v[124:127], a[192:195], a[144:147], v[124:127]\n" \
"ds_read_b64_tr_b16 a[192:193], v10 offset:9280\n" \
"ds_read_b64_tr_b16 a[194:195], v10 offset:13888\n" \
"v_cvt_pk_bf16_f32 v112, v112, v113\n" \
"v_cvt_pk_bf16_f32 v113, v114, v115\n" \
"v_cvt_pk_bf16_f32 v114, v116, v117\n" \
"v_cvt_pk_bf16_f32 v115, v118, v119\n" \
"buffer_store_dwordx4 v[112:115], v20, s[56:59], s65 offen offset:0 sc1\n" \
"s_add_u32 s65, s65, 0x3800\n" \
"v_mfma_f32_16x16x32_bf16 v[124:127], a[196:199], a[148:151], v[124:127]\n" \
"ds_read_b64_tr_b16 a[196:197], v33 offset:36960\n" \
"ds_read_b64_tr_b16 a[198:199], v33 offset:41568\n" \
"v_mfma_f32_16x16x32_bf16 v[124:127], a[200:203], a[152:155], v[124:127]\n" \
"v_mfma_f32_16x16x32_bf16 v[124:127], a[204:207], a[156:159], v[124:127]\n" \
"v_mfma_f32_16x16x32_bf16 v[120:123], a[200:203], a[136:139], v[120:123]\n" \
"ds_read_b64_tr_b16 a[200:201], v33 offset:46176\n" \
"ds_read_b64_tr_b16 a[202:203], v33 offset:50784\n" \
"v_mfma_f32_16x16x32_bf16 v[120:123], a[204:207], a[140:143], v[120:123]\n" \
"ds_read_b64_tr_b16 a[204:205], v33 offset:55392\n" \
"ds_read_b64_tr_b16 a[206:207], v33 offset:60000\n" \
"v_mfma_f32_16x16x32_bf16 v[120:123], a[208:211], a[32:35], v[120:123]\n" \
"buffer_load_dwordx4 a[180:183], v32, s[56:59], s72 offen offset:1024 sc1\n" \
"v_mfma_f32_16x16x32_bf16 v[120:123], a[212:215], a[36:39], v[120:123]\n" \
"v_mfma_f32_16x16x32_bf16 v[124:127], a[208:211], a[48:51], v[124:127]\n" \
"ds_read_b64_tr_b16 a[208:209], v10 offset:96\n" \
"ds_read_b64_tr_b16 a[210:211], v10 offset:4704\n" \
"v_mfma_f32_16x16x32_bf16 v[124:127], a[212:215], a[52:55], v[124:127]\n" \
"ds_read_b64_tr_b16 a[212:213], v34 offset:96\n" \
"ds_read_b64_tr_b16 a[214:215], v34 offset:4704\n" \
"v_mfma_f32_16x16x32_bf16 v[124:127], a[216:219], a[56:59], v[124:127]\n" \
"s_waitcnt lgkmcnt(12)\n" \
"v_mfma_f32_16x16x32_bf16 v[124:127], a[0:3], a[60:63], v[124:127]\n" \
"buffer_load_dwordx4 a[168:171], v20, s[56:59], s72 offen offset:2048 sc1\n" \
"v_mfma_f32_16x16x32_bf16 v[120:123], a[216:219], a[40:43], v[120:123]\n" \
"ds_read_b64_tr_b16 a[216:217], v34 offset:9312\n" \
"ds_read_b64_tr_b16 a[218:219], v34 offset:13920\n" \
"v_mfma_f32_16x16x32_bf16 v[120:123], a[0:3], a[44:47], v[120:123]\n" \
"ds_read_b64_tr_b16 a[0:1], v34 offset:18528\n" \
"ds_read_b64_tr_b16 a[2:3], v34 offset:23136\n" \
"v_mfma_f32_16x16x32_bf16 v[128:131], a[4:7], a[128:131], v[128:131]\n" \
"v_mfma_f32_16x16x32_bf16 v[128:131], a[8:11], a[132:135], v[128:131]\n" \
"v_mfma_f32_16x16x32_bf16 v[132:135], a[4:7], a[144:147], v[132:135]\n" \
"ds_read_b64_tr_b16 a[4:5], v10 offset:9312\n" \
"ds_read_b64_tr_b16 a[6:7], v10 offset:13920\n" \
"v_mfma_f32_16x16x32_bf16 v[132:135], a[8:11], a[148:151], v[132:135]\n" \
"v_cvt_pk_bf16_f32 v120, v120, v121\n" \
"v_cvt_pk_bf16_f32 v121, v122, v123\n" \
"v_cvt_pk_bf16_f32 v122, v124, v125\n" \
"v_cvt_pk_bf16_f32 v123, v126, v127\n" \
"buffer_store_dwordx4 v[120:123], v20, s[56:59], s66 offen offset:0 sc1\n" \
"s_add_u32 s66, s66, 0x3000\n" \
"v_mfma_f32_16x16x32_bf16 v[132:135], a[12:15], a[152:155], v[132:135]\n" \
"ds_read_b64_tr_b16 a[8:9], v33 offset:36992\n" \
"ds_read_b64_tr_b16 a[10:11], v33 offset:41600\n" \
"v_mfma_f32_16x16x32_bf16 v[132:135], a[16:19], a[156:159], v[132:135]\n" \
"v_mfma_f32_16x16x32_bf16 v[128:131], a[12:15], a[136:139], v[128:131]\n" \
"ds_read_b64_tr_b16 a[12:13], v33 offset:46208\n" \
"ds_read_b64_tr_b16 a[14:15], v33 offset:50816\n" \
"v_mfma_f32_16x16x32_bf16 v[128:131], a[16:19], a[140:143], v[128:131]\n" \
"ds_read_b64_tr_b16 a[16:17], v33 offset:55424\n" \
"ds_read_b64_tr_b16 a[18:19], v33 offset:60032\n" \
"v_mfma_f32_16x16x32_bf16 v[128:131], a[20:23], a[32:35], v[128:131]\n" \
"buffer_load_dwordx4 a[184:187], v32, s[56:59], s72 offen offset:2048 sc1\n" \
"v_mfma_f32_16x16x32_bf16 v[128:131], a[24:27], a[36:39], v[128:131]\n" \
"v_mfma_f32_16x16x32_bf16 v[132:135], a[20:23], a[48:51], v[132:135]\n" \
"ds_read_b64_tr_b16 a[20:21], v10 offset:128\n" \
"ds_read_b64_tr_b16 a[22:23], v10 offset:4736\n" \
"v_mfma_f32_16x16x32_bf16 v[132:135], a[24:27], a[52:55], v[132:135]\n" \
"ds_read_b64_tr_b16 a[24:25], v34 offset:128\n" \
"ds_read_b64_tr_b16 a[26:27], v34 offset:4736\n" \
"v_mfma_f32_16x16x32_bf16 v[132:135], a[28:31], a[56:59], v[132:135]\n" \
"s_waitcnt lgkmcnt(12)\n" \
"v_mfma_f32_16x16x32_bf16 v[132:135], a[192:195], a[60:63], v[132:135]\n" \
"buffer_load_dwordx4 a[172:175], v20, s[56:59], s72 offen offset:3072 sc1\n" \
"v_mfma_f32_16x16x32_bf16 v[128:131], a[28:31], a[40:43], v[128:131]\n" \
"ds_read_b64_tr_b16 a[28:29], v34 offset:9344\n" \
"ds_read_b64_tr_b16 a[30:31], v34 offset:13952\n" \
"v_mfma_f32_16x16x32_bf16 v[128:131], a[192:195], a[44:47], v[128:131]\n" \
"ds_read_b64_tr_b16 a[192:193], v34 offset:18560\n" \
"ds_read_b64_tr_b16 a[194:195], v34 offset:23168\n" \
"v_mfma_f32_16x16x32_bf16 v[136:139], a[196:199], a[128:131], v[136:139]\n" \
"v_mfma_f32_16x16x32_bf16 v[136:139], a[200:203], a[132:135], v[136:139]\n" \
"v_mfma_f32_16x16x32_bf16 v[140:143], a[196:199], a[144:147], v[140:143]\n" \
"buffer_load_dwordx4 a[188:191], v32, s[56:59], s72 offen offset:3072 sc1\n" \
"v_mfma_f32_16x16x32_bf16 v[140:143], a[200:203], a[148:151], v[140:143]\n" \
"v_readlane_b32 s72, v11, 53\n" \
"v_cvt_pk_bf16_f32 v128, v128, v129\n" \
"v_cvt_pk_bf16_f32 v129, v130, v131\n" \
"v_cvt_pk_bf16_f32 v130, v132, v133\n" \
"v_cvt_pk_bf16_f32 v131, v134, v135\n" \
"v_mfma_f32_16x16x32_bf16 v[140:143], a[204:207], a[152:155], v[140:143]\n" \
"buffer_store_dwordx4 v[128:131], v20, s[56:59], s67 offen offset:0 sc1\n" \
"s_add_u32 s67, s67, 0x2800\n" \
"v_mfma_f32_16x16x32_bf16 v[140:143], a[208:211], a[156:159], v[140:143]\n" \
"ds_read_b64_tr_b16 a[196:197], v10 offset:9344\n" \
"ds_read_b64_tr_b16 a[198:199], v10 offset:13952\n" \
"v_mfma_f32_16x16x32_bf16 v[136:139], a[204:207], a[136:139], v[136:139]\n" \
"ds_read_b64_tr_b16 a[200:201], v33 offset:37024\n" \
"ds_read_b64_tr_b16 a[202:203], v33 offset:41632\n" \
"v_mfma_f32_16x16x32_bf16 v[136:139], a[208:211], a[140:143], v[136:139]\n" \
"ds_read_b64_tr_b16 a[204:205], v33 offset:46240\n" \
"ds_read_b64_tr_b16 a[206:207], v33 offset:50848\n" \
"v_mfma_f32_16x16x32_bf16 v[136:139], a[212:215], a[32:35], v[136:139]\n" \
"ds_read_b64_tr_b16 a[208:209], v33 offset:55456\n" \
"ds_read_b64_tr_b16 a[210:211], v33 offset:60064\n" \
"v_mfma_f32_16x16x32_bf16 v[136:139], a[216:219], a[36:39], v[136:139]\n" \
"buffer_load_dwordx4 a[96:99], v20, s[56:59], s93 offen offset:0 sc1\n" \
"v_mfma_f32_16x16x32_bf16 v[140:143], a[212:215], a[48:51], v[140:143]\n" \
"ds_read_b64_tr_b16 a[212:213], v10 offset:160\n" \
"ds_read_b64_tr_b16 a[214:215], v10 offset:4768\n" \
"v_mfma_f32_16x16x32_bf16 v[140:143], a[216:219], a[52:55], v[140:143]\n" \
"ds_read_b64_tr_b16 a[216:217], v34 offset:160\n" \
"ds_read_b64_tr_b16 a[218:219], v34 offset:4768\n" \
"v_mfma_f32_16x16x32_bf16 v[140:143], a[0:3], a[56:59], v[140:143]\n" \
"s_waitcnt lgkmcnt(13)\n" \
"v_mfma_f32_16x16x32_bf16 v[140:143], a[4:7], a[60:63], v[140:143]\n" \
"v_mfma_f32_16x16x32_bf16 v[136:139], a[0:3], a[40:43], v[136:139]\n" \
"buffer_load_dwordx4 a[100:103], v20, s[56:59], s93 offen offset:1024 sc1\n" \
"v_mfma_f32_16x16x32_bf16 v[136:139], a[4:7], a[44:47], v[136:139]\n" \
"ds_read_b64_tr_b16 a[0:1], v34 offset:9376\n" \
"ds_read_b64_tr_b16 a[2:3], v34 offset:13984\n" \
"v_mfma_f32_16x16x32_bf16 v[144:147], a[8:11], a[128:131], v[144:147]\n" \
"ds_read_b64_tr_b16 a[4:5], v34 offset:18592\n" \
"ds_read_b64_tr_b16 a[6:7], v34 offset:23200\n" \
"v_mfma_f32_16x16x32_bf16 v[144:147], a[12:15], a[132:135], v[144:147]\n" \
"v_mfma_f32_16x16x32_bf16 v[148:151], a[8:11], a[144:147], v[148:151]\n" \
"ds_read_b64_tr_b16 a[8:9], v10 offset:9376\n" \
"v_cvt_pk_bf16_f32 v136, v136, v137\n" \
"v_cvt_pk_bf16_f32 v137, v138, v139\n" \
"v_cvt_pk_bf16_f32 v138, v140, v141\n" \
"v_cvt_pk_bf16_f32 v139, v142, v143\n" \
"buffer_store_dwordx4 v[136:139], v20, s[56:59], s68 offen offset:0 sc1\n" \
"s_add_u32 s68, s68, 0x2000\n" \
"v_mfma_f32_16x16x32_bf16 v[148:151], a[12:15], a[148:151], v[148:151]\n" \
"ds_read_b64_tr_b16 a[10:11], v10 offset:13984\n" \
"ds_read_b64_tr_b16 a[12:13], v33 offset:37056\n" \
"v_mfma_f32_16x16x32_bf16 v[148:151], a[16:19], a[152:155], v[148:151]\n" \
"ds_read_b64_tr_b16 a[14:15], v33 offset:41664\n" \
"v_mfma_f32_16x16x32_bf16 v[148:151], a[20:23], a[156:159], v[148:151]\n" \
"v_mfma_f32_16x16x32_bf16 v[144:147], a[16:19], a[136:139], v[144:147]\n" \
"buffer_load_dwordx4 a[112:115], v32, s[56:59], s93 offen offset:0 sc1\n" \
"v_mfma_f32_16x16x32_bf16 v[144:147], a[20:23], a[140:143], v[144:147]\n" \
"ds_read_b64_tr_b16 a[16:17], v33 offset:46272\n" \
"ds_read_b64_tr_b16 a[18:19], v33 offset:50880\n" \
"v_mfma_f32_16x16x32_bf16 v[144:147], a[24:27], a[32:35], v[144:147]\n" \
"ds_read_b64_tr_b16 a[20:21], v33 offset:55488\n" \
"ds_read_b64_tr_b16 a[22:23], v33 offset:60096\n" \
"v_mfma_f32_16x16x32_bf16 v[144:147], a[28:31], a[36:39], v[144:147]\n" \
"v_mfma_f32_16x16x32_bf16 v[148:151], a[24:27], a[48:51], v[148:151]\n" \
"ds_read_b64_tr_b16 a[24:25], v10 offset:192\n" \
"ds_read_b64_tr_b16 a[26:27], v10 offset:4800\n" \
"buffer_load_dwordx4 a[116:119], v32, s[56:59], s93 offen offset:1024 sc1\n" \
"v_mfma_f32_16x16x32_bf16 v[148:151], a[28:31], a[52:55], v[148:151]\n" \
"ds_read_b64_tr_b16 a[28:29], v34 offset:192\n" \
"s_waitcnt lgkmcnt(13)\n" \
"v_mfma_f32_16x16x32_bf16 v[148:151], a[192:195], a[56:59], v[148:151]\n" \
"ds_read_b64_tr_b16 a[30:31], v34 offset:4800\n" \
"v_mfma_f32_16x16x32_bf16 v[148:151], a[196:199], a[60:63], v[148:151]\n" \
"v_mfma_f32_16x16x32_bf16 v[144:147], a[192:195], a[40:43], v[144:147]\n" \
"ds_read_b64_tr_b16 a[192:193], v34 offset:9408\n" \
"ds_read_b64_tr_b16 a[194:195], v34 offset:14016\n" \
"buffer_load_dwordx4 a[120:123], v32, s[56:59], s93 offen offset:2048 sc1\n" \
"v_mfma_f32_16x16x32_bf16 v[144:147], a[196:199], a[44:47], v[144:147]\n" \
"ds_read_b64_tr_b16 a[196:197], v34 offset:18624\n" \
"ds_read_b64_tr_b16 a[198:199], v34 offset:23232\n" \
"v_mfma_f32_16x16x32_bf16 v[152:155], a[200:203], a[128:131], v[152:155]\n" \
"v_mfma_f32_16x16x32_bf16 v[152:155], a[204:207], a[132:135], v[152:155]\n" \
"v_mfma_f32_16x16x32_bf16 v[156:159], a[200:203], a[144:147], v[156:159]\n" \
"ds_read_b64_tr_b16 a[200:201], v10 offset:9408\n" \
"ds_read_b64_tr_b16 a[202:203], v10 offset:14016\n" \
"buffer_load_dwordx4 a[124:127], v32, s[56:59], s93 offen offset:3072 sc1\n" \
"v_cvt_pk_bf16_f32 v144, v144, v145\n" \
"v_cvt_pk_bf16_f32 v145, v146, v147\n" \
"v_cvt_pk_bf16_f32 v146, v148, v149\n" \
"v_cvt_pk_bf16_f32 v147, v150, v151\n" \
"v_mfma_f32_16x16x32_bf16 v[156:159], a[204:207], a[148:151], v[156:159]\n" \
"ds_read_b64_tr_b16 a[204:205], v33 offset:256\n" \
"ds_read_b64_tr_b16 a[206:207], v33 offset:4864\n" \
"buffer_store_dwordx4 v[144:147], v20, s[56:59], s69 offen offset:0 sc1\n" \
"s_add_u32 s69, s69, 0x1800\n" \
"v_mfma_f32_16x16x32_bf16 v[156:159], a[208:211], a[152:155], v[156:159]\n" \
"v_mfma_f32_16x16x32_bf16 v[156:159], a[212:215], a[156:159], v[156:159]\n" \
"v_mfma_f32_16x16x32_bf16 v[152:155], a[208:211], a[136:139], v[152:155]\n" \
"ds_read_b64_tr_b16 a[208:209], v33 offset:9472\n" \
"ds_read_b64_tr_b16 a[210:211], v33 offset:14080\n" \
"v_mfma_f32_16x16x32_bf16 v[152:155], a[212:215], a[140:143], v[152:155]\n" \
"buffer_load_dwordx4 a[104:107], v20, s[56:59], s93 offen offset:2048 sc1\n" \
"v_mfma_f32_16x16x32_bf16 v[152:155], a[216:219], a[32:35], v[152:155]\n" \
"ds_read_b64_tr_b16 a[212:213], v33 offset:18688\n" \
"ds_read_b64_tr_b16 a[214:215], v33 offset:23296\n" \
"v_mfma_f32_16x16x32_bf16 v[152:155], a[0:3], a[36:39], v[152:155]\n" \
"v_mfma_f32_16x16x32_bf16 v[156:159], a[216:219], a[48:51], v[156:159]\n" \
"ds_read_b64_tr_b16 a[216:217], v33 offset:27904\n" \
"ds_read_b64_tr_b16 a[218:219], v33 offset:32512\n" \
"v_mfma_f32_16x16x32_bf16 v[156:159], a[0:3], a[52:55], v[156:159]\n" \
"ds_read_b64_tr_b16 a[0:1], v33 offset:37120\n" \
"buffer_load_dwordx4 a[108:111], v20, s[56:59], s93 offen offset:3072 sc1\n" \
"s_waitcnt lgkmcnt(11)\n" \
"v_mfma_f32_16x16x32_bf16 v[156:159], a[4:7], a[56:59], v[156:159]\n" \
"ds_read_b64_tr_b16 a[2:3], v33 offset:41728\n" \
"v_mfma_f32_16x16x32_bf16 v[156:159], a[8:11], a[60:63], v[156:159]\n" \
"v_mfma_f32_16x16x32_bf16 v[152:155], a[4:7], a[40:43], v[152:155]\n" \
"ds_read_b64_tr_b16 a[4:5], v33 offset:46336\n" \
"ds_read_b64_tr_b16 a[6:7], v33 offset:50944\n" \
"v_mfma_f32_16x16x32_bf16 v[152:155], a[8:11], a[44:47], v[152:155]\n" \
"ds_read_b64_tr_b16 a[8:9], v33 offset:55552\n" \
"ds_read_b64_tr_b16 a[10:11], v33 offset:60160\n" \
"v_mfma_f32_16x16x32_bf16 v[160:163], a[12:15], a[128:131], v[160:163]\n" \
"buffer_load_dwordx4 v[64:67], v20, s[56:59], s95 offen offset:0 sc1\n" \
"v_mfma_f32_16x16x32_bf16 v[160:163], a[16:19], a[132:135], v[160:163]\n" \
"v_mfma_f32_16x16x32_bf16 v[164:167], a[12:15], a[144:147], v[164:167]\n" \
"ds_read_b64_tr_b16 a[12:13], v10 offset:256\n" \
"ds_read_b64_tr_b16 a[14:15], v10 offset:4864\n" \
"v_cvt_pk_bf16_f32 v152, v152, v153\n" \
"v_cvt_pk_bf16_f32 v153, v154, v155\n" \
"v_cvt_pk_bf16_f32 v154, v156, v157\n" \
"v_cvt_pk_bf16_f32 v155, v158, v159\n" \
"buffer_store_dwordx4 v[152:155], v20, s[56:59], s70 offen offset:0 sc1\n" \
"s_add_u32 s70, s70, 0x1000\n" \
"v_mfma_f32_16x16x32_bf16 v[164:167], a[16:19], a[148:151], v[164:167]\n" \
"ds_read_b64_tr_b16 a[16:17], v34 offset:256\n" \
"ds_read_b64_tr_b16 a[18:19], v34 offset:4864\n" \
"v_mfma_f32_16x16x32_bf16 v[164:167], a[20:23], a[152:155], v[164:167]\n" \
"v_mfma_f32_16x16x32_bf16 v[164:167], a[24:27], a[156:159], v[164:167]\n" \
"v_mfma_f32_16x16x32_bf16 v[160:163], a[20:23], a[136:139], v[160:163]\n" \
"ds_read_b64_tr_b16 a[20:21], v34 offset:9472\n" \
"ds_read_b64_tr_b16 a[22:23], v34 offset:14080\n" \
"v_mfma_f32_16x16x32_bf16 v[160:163], a[24:27], a[140:143], v[160:163]\n" \
"ds_read_b64_tr_b16 a[24:25], v34 offset:18688\n" \
"ds_read_b64_tr_b16 a[26:27], v34 offset:23296\n" \
"s_waitcnt vmcnt(19)\n" \
"s_mov_b64 exec, 1\n" \
"buffer_store_dword v39, off, s[56:59], s65 offset:0 sc1\n" \
"s_mov_b64 exec, -1\n" \
"v_mfma_f32_16x16x32_bf16 v[160:163], a[28:31], a[32:35], v[160:163]\n" \
"v_mfma_f32_16x16x32_bf16 v[160:163], a[192:195], a[36:39], v[160:163]\n" \
"v_mfma_f32_16x16x32_bf16 v[164:167], a[28:31], a[48:51], v[164:167]\n" \
"ds_read_b64_tr_b16 a[28:29], v10 offset:9472\n" \
"ds_read_b64_tr_b16 a[30:31], v10 offset:14080\n" \
"v_mfma_f32_16x16x32_bf16 v[164:167], a[192:195], a[52:55], v[164:167]\n" \
"ds_read_b64_tr_b16 a[192:193], v33 offset:0\n" \
"ds_read_b64_tr_b16 a[194:195], v33 offset:4608\n" \
"v_mfma_f32_16x16x32_bf16 v[164:167], a[196:199], a[56:59], v[164:167]\n" \
"s_waitcnt lgkmcnt(12)\n" \
"v_mfma_f32_16x16x32_bf16 v[164:167], a[200:203], a[60:63], v[164:167]\n" \
"buffer_load_dwordx4 v[68:71], v20, s[56:59], s95 offen offset:1024 sc1\n" \
"v_mfma_f32_16x16x32_bf16 v[160:163], a[196:199], a[40:43], v[160:163]\n" \
"ds_read_b64_tr_b16 a[196:197], v33 offset:9216\n" \
"ds_read_b64_tr_b16 a[198:199], v33 offset:13824\n" \
"v_mfma_f32_16x16x32_bf16 v[160:163], a[200:203], a[44:47], v[160:163]\n" \
"ds_read_b64_tr_b16 a[200:201], v33 offset:18432\n" \
"ds_read_b64_tr_b16 a[202:203], v33 offset:23040\n" \
"v_mfma_f32_16x16x32_bf16 v[248:251], a[204:207], a[220:223], v[248:251]\n" \
"v_mfma_f32_16x16x32_bf16 v[248:251], a[208:211], a[224:227], v[248:251]\n" \
"v_mfma_f32_16x16x32_bf16 v[252:255], a[204:207], a[236:239], v[252:255]\n" \
"ds_read_b64_tr_b16 a[204:205], v33 offset:27648\n" \
"ds_read_b64_tr_b16 a[206:207], v33 offset:32256\n" \
"v_mfma_f32_16x16x32_bf16 v[252:255], a[208:211], a[240:243], v[252:255]\n" \
"v_cvt_pk_bf16_f32 v160, v160, v161\n" \
"v_cvt_pk_bf16_f32 v161, v162, v163\n" \
"v_cvt_pk_bf16_f32 v162, v164, v165\n" \
"v_cvt_pk_bf16_f32 v163, v166, v167\n" \
"buffer_store_dwordx4 v[160:163], v20, s[56:59], s71 offen offset:0 sc1\n" \
"s_add_u32 s71, s71, 0x800\n" \
"v_mfma_f32_16x16x32_bf16 v[252:255], a[212:215], a[244:247], v[252:255]\n" \
"ds_read_b64_tr_b16 a[208:209], v33 offset:36864\n" \
"ds_read_b64_tr_b16 a[210:211], v33 offset:41472\n" \
"v_mfma_f32_16x16x32_bf16 v[252:255], a[216:219], a[248:251], v[252:255]\n" \
"v_mfma_f32_16x16x32_bf16 v[248:251], a[212:215], a[228:231], v[248:251]\n" \
"ds_read_b64_tr_b16 a[212:213], v33 offset:46080\n" \
"ds_read_b64_tr_b16 a[214:215], v33 offset:50688\n" \
"s_waitcnt vmcnt(19)\n" \
"s_mov_b64 exec, 1\n" \
"buffer_store_dword v39, off, s[56:59], s66 offset:4 sc1\n" \
"s_mov_b64 exec, -1\n" \
"v_readlane_b32 s66, v11, 47\n" \
"v_mfma_f32_16x16x32_bf16 v[248:251], a[216:219], a[232:235], v[248:251]\n" \
"ds_read_b64_tr_b16 a[216:217], v33 offset:55296\n" \
"ds_read_b64_tr_b16 a[218:219], v33 offset:59904\n" \
"v_mfma_f32_16x16x32_bf16 v[176:179], a[0:3], a[128:131], v[176:179]\n" \
"v_mfma_f32_16x16x32_bf16 v[176:179], a[4:7], a[132:135], v[176:179]\n" \
"v_mfma_f32_16x16x32_bf16 v[180:183], a[0:3], a[144:147], v[180:183]\n" \
"buffer_load_dwordx4 v[72:75], v32, s[56:59], s95 offen offset:0 sc1\n" \
"v_mfma_f32_16x16x32_bf16 v[180:183], a[4:7], a[148:151], v[180:183]\n" \
"v_mfma_f32_16x16x32_bf16 v[248:251], a[0:3], a[160:163], v[248:251]\n" \
"v_mfma_f32_16x16x32_bf16 v[248:251], a[4:7], a[164:167], v[248:251]\n" \
"v_mfma_f32_16x16x32_bf16 v[252:255], a[0:3], a[176:179], v[252:255]\n" \
"ds_read_b64_tr_b16 a[0:1], v10 offset:0\n" \
"ds_read_b64_tr_b16 a[2:3], v10 offset:4608\n" \
"v_mfma_f32_16x16x32_bf16 v[252:255], a[4:7], a[180:183], v[252:255]\n" \
"ds_read_b64_tr_b16 a[4:5], v34 offset:0\n" \
"s_waitcnt vmcnt(20)\n" \
"v_mfma_f32_16x16x32_bf16 v[252:255], a[8:11], a[184:187], v[252:255]\n" \
"ds_read_b64_tr_b16 a[6:7], v34 offset:4608\n" \
"s_waitcnt vmcnt(18) lgkmcnt(10)\n" \
"v_mfma_f32_16x16x32_bf16 v[252:255], a[12:15], a[188:191], v[252:255]\n" \
"buffer_load_dwordx4 v[76:79], v32, s[56:59], s95 offen offset:1024 sc1\n" \
"v_mfma_f32_16x16x32_bf16 v[248:251], a[8:11], a[168:171], v[248:251]\n" \
"v_mfma_f32_16x16x32_bf16 v[248:251], a[12:15], a[172:175], v[248:251]\n" \
"v_mfma_f32_16x16x32_bf16 v[180:183], a[8:11], a[152:155], v[180:183]\n" \
"v_mfma_f32_16x16x32_bf16 v[180:183], a[12:15], a[156:159], v[180:183]\n" \
"v_mfma_f32_16x16x32_bf16 v[176:179], a[8:11], a[136:139], v[176:179]\n" \
"ds_read_b64_tr_b16 a[8:9], v34 offset:9216\n" \
"ds_read_b64_tr_b16 a[10:11], v34 offset:13824\n" \
"v_mfma_f32_16x16x32_bf16 v[176:179], a[12:15], a[140:143], v[176:179]\n" \
"ds_read_b64_tr_b16 a[12:13], v34 offset:18432\n" \
"ds_read_b64_tr_b16 a[14:15], v34 offset:23040\n" \
"s_waitcnt vmcnt(18)\n" \
"s_mov_b64 exec, 1\n" \
"buffer_store_dword v39, off, s[56:59], s67 offset:8 sc1\n" \
"s_mov_b64 exec, -1\n" \
"v_readlane_b32 s67, v11, 48\n" \
"v_mfma_f32_16x16x32_bf16 v[176:179], a[16:19], a[32:35], v[176:179]\n" \
"v_mfma_f32_16x16x32_bf16 v[176:179], a[20:23], a[36:39], v[176:179]\n" \
"v_mfma_f32_16x16x32_bf16 v[180:183], a[16:19], a[48:51], v[180:183]\n" \
"v_mfma_f32_16x16x32_bf16 v[180:183], a[20:23], a[52:55], v[180:183]\n" \
"s_waitcnt vmcnt(18)\n" \
"v_mfma_f32_16x16x32_bf16 v[248:251], a[16:19], a[96:99], v[248:251]\n" \
"s_waitcnt vmcnt(17)\n" \
"v_mfma_f32_16x16x32_bf16 v[248:251], a[20:23], a[100:103], v[248:251]\n" \
"s_waitcnt vmcnt(15)\n" \
"v_mfma_f32_16x16x32_bf16 v[252:255], a[16:19], a[112:115], v[252:255]\n" \
"ds_read_b64_tr_b16 a[16:17], v10 offset:9216\n" \
"s_mov_b64 exec, 1\n" \
"buffer_store_dword v39, off, s[56:59], s68 offset:12 sc1\n" \
"s_mov_b64 exec, -1\n" \
"s_waitcnt vmcnt(15)\n" \
"v_mfma_f32_16x16x32_bf16 v[252:255], a[20:23], a[116:119], v[252:255]\n" \
"v_readlane_b32 s68, v11, 49\n" \
"s_waitcnt vmcnt(14)\n" \
"v_mfma_f32_16x16x32_bf16 v[252:255], a[24:27], a[120:123], v[252:255]\n" \
"ds_read_b64_tr_b16 a[18:19], v10 offset:13824\n" \
"s_waitcnt vmcnt(13)\n" \
"v_mfma_f32_16x16x32_bf16 v[252:255], a[28:31], a[124:127], v[252:255]\n" \
"ds_read_b64_tr_b16 a[20:21], v33 offset:32\n" \
"ds_read_b64_tr_b16 a[22:23], v33 offset:4640\n" \
"buffer_load_dwordx4 v[80:83], v20, s[56:59], s96 offen offset:0 sc1\n" \
"s_waitcnt vmcnt(12)\n" \
"v_mfma_f32_16x16x32_bf16 v[248:251], a[24:27], a[104:107], v[248:251]\n" \
"s_mov_b64 exec, 1\n" \
"buffer_store_dword v39, off, s[56:59], s69 offset:16 sc1\n" \
"s_mov_b64 exec, -1\n" \
"v_readlane_b32 s69, v11, 50\n" \
".fill (L_ab + 40960 - .) / 4, 4, 0xBF800000\n" \
"L_ap10:\n" \
"s_cbranch_execz L_ap0\n" \
"s_waitcnt vmcnt(12)\n" \
"v_mfma_f32_16x16x32_bf16 v[248:251], a[28:31], a[108:111], v[248:251]\n" \
"v_mfma_f32_16x16x32_bf16 v[180:183], a[24:27], a[56:59], v[180:183]\n" \
"buffer_load_dwordx4 v[84:87], v20, s[56:59], s96 offen offset:1024 sc1\n" \
"v_mfma_f32_16x16x32_bf16 v[180:183], a[28:31], a[60:63], v[180:183]\n" \
"v_mfma_f32_16x16x32_bf16 v[176:179], a[24:27], a[40:43], v[176:179]\n" \
"ds_read_b64_tr_b16 a[24:25], v33 offset:9248\n" \
"ds_read_b64_tr_b16 a[26:27], v33 offset:13856\n" \
"v_mfma_f32_16x16x32_bf16 v[176:179], a[28:31], a[44:47], v[176:179]\n" \
"ds_read_b64_tr_b16 a[28:29], v33 offset:18464\n" \
"v_cvt_pk_bf16_f32 v248, v248, v249\n" \
"v_cvt_pk_bf16_f32 v249, v250, v251\n" \
"v_cvt_pk_bf16_f32 v250, v252, v253\n" \
"v_cvt_pk_bf16_f32 v251, v254, v255\n" \
"buffer_store_dwordx4 v[248:251], v20, s[56:59], s72 offen offset:1024 sc1\n" \
"v_mfma_f32_16x16x32_bf16 v[184:187], a[192:195], a[220:223], v[184:187]\n" \
"ds_read_b64_tr_b16 a[30:31], v33 offset:23072\n" \
"v_cvt_pk_bf16_f32 v176, v176, v177\n" \
"v_cvt_pk_bf16_f32 v177, v178, v179\n" \
"v_cvt_pk_bf16_f32 v178, v180, v181\n" \
"v_cvt_pk_bf16_f32 v179, v182, v183\n" \
"v_mfma_f32_16x16x32_bf16 v[184:187], a[196:199], a[224:227], v[184:187]\n" \
"buffer_store_dwordx4 v[176:179], v20, s[56:59], s72 offen offset:0 sc1\n" \
"v_mfma_f32_16x16x32_bf16 v[188:191], a[192:195], a[236:239], v[188:191]\n" \
"ds_read_b64_tr_b16 a[192:193], v33 offset:27680\n" \
"ds_read_b64_tr_b16 a[194:195], v33 offset:32288\n" \
"v_mfma_f32_16x16x32_bf16 v[188:191], a[196:199], a[240:243], v[188:191]\n" \
"ds_read_b64_tr_b16 a[196:197], v33 offset:36896\n" \
"ds_read_b64_tr_b16 a[198:199], v33 offset:41504\n" \
"v_mfma_f32_16x16x32_bf16 v[188:191], a[200:203], a[244:247], v[188:191]\n" \
"v_mfma_f32_16x16x32_bf16 v[188:191], a[204:207], a[248:251], v[188:191]\n" \
"v_mfma_f32_16x16x32_bf16 v[184:187], a[200:203], a[228:231], v[184:187]\n" \
"ds_read_b64_tr_b16 a[200:201], v33 offset:46112\n" \
"ds_read_b64_tr_b16 a[202:203], v33 offset:50720\n" \
"v_mfma_f32_16x16x32_bf16 v[184:187], a[204:207], a[232:235], v[184:187]\n" \
"ds_read_b64_tr_b16 a[204:205], v33 offset:55328\n" \
"s_waitcnt lgkmcnt(11)\n" \
"v_mfma_f32_16x16x32_bf16 v[184:187], a[208:211], a[160:163], v[184:187]\n" \
"buffer_load_dwordx4 v[88:91], v32, s[56:59], s96 offen offset:0 sc1\n" \
"v_mfma_f32_16x16x32_bf16 v[184:187], a[212:215], a[164:167], v[184:187]\n" \
"ds_read_b64_tr_b16 a[206:207], v33 offset:59936\n" \
"v_mfma_f32_16x16x32_bf16 v[188:191], a[208:211], a[176:179], v[188:191]\n" \
"ds_read_b64_tr_b16 a[208:209], v10 offset:32\n" \
"ds_read_b64_tr_b16 a[210:211], v10 offset:4640\n" \
"v_mfma_f32_16x16x32_bf16 v[188:191], a[212:215], a[180:183], v[188:191]\n" \
"ds_read_b64_tr_b16 a[212:213], v34 offset:32\n" \
"ds_read_b64_tr_b16 a[214:215], v34 offset:4640\n" \
"v_mfma_f32_16x16x32_bf16 v[188:191], a[216:219], a[184:187], v[188:191]\n" \
"v_mfma_f32_16x16x32_bf16 v[188:191], a[0:3], a[188:191], v[188:191]\n" \
"buffer_load_dwordx4 v[92:95], v32, s[56:59], s96 offen offset:1024 sc1\n" \
"v_mfma_f32_16x16x32_bf16 v[184:187], a[216:219], a[168:171], v[184:187]\n" \
"ds_read_b64_tr_b16 a[216:217], v34 offset:9248\n" \
"ds_read_b64_tr_b16 a[218:219], v34 offset:13856\n" \
"v_mfma_f32_16x16x32_bf16 v[184:187], a[0:3], a[172:175], v[184:187]\n" \
"ds_read_b64_tr_b16 a[0:1], v34 offset:18464\n" \
"ds_read_b64_tr_b16 a[2:3], v34 offset:23072\n" \
"v_mfma_f32_16x16x32_bf16 v[184:187], a[4:7], a[96:99], v[184:187]\n" \
"v_mfma_f32_16x16x32_bf16 v[184:187], a[8:11], a[100:103], v[184:187]\n" \
"v_mfma_f32_16x16x32_bf16 v[188:191], a[4:7], a[112:115], v[188:191]\n" \
"ds_read_b64_tr_b16 a[4:5], v10 offset:9248\n" \
"ds_read_b64_tr_b16 a[6:7], v10 offset:13856\n" \
"v_mfma_f32_16x16x32_bf16 v[188:191], a[8:11], a[116:119], v[188:191]\n" \
"ds_read_b64_tr_b16 a[8:9], v33 offset:64\n" \
"ds_read_b64_tr_b16 a[10:11], v33 offset:4672\n" \
"v_mfma_f32_16x16x32_bf16 v[188:191], a[12:15], a[120:123], v[188:191]\n" \
"buffer_load_dwordx4 v[96:99], v20, s[56:59], s97 offen offset:0 sc1\n" \
"v_mfma_f32_16x16x32_bf16 v[188:191], a[16:19], a[124:127], v[188:191]\n" \
"v_mfma_f32_16x16x32_bf16 v[184:187], a[12:15], a[104:107], v[184:187]\n" \
"ds_read_b64_tr_b16 a[12:13], v33 offset:9280\n" \
"ds_read_b64_tr_b16 a[14:15], v33 offset:13888\n" \
"v_mfma_f32_16x16x32_bf16 v[184:187], a[16:19], a[108:111], v[184:187]\n" \
"buffer_load_dword v37, v40, s[56:59], s74 offen sc1\n" \
"v_mfma_f32_16x16x32_bf16 v[192:195], a[20:23], a[220:223], v[192:195]\n" \
"ds_read_b64_tr_b16 a[16:17], v33 offset:18496\n" \
"s_waitcnt lgkmcnt(13)\n" \
"v_mfma_f32_16x16x32_bf16 v[192:195], a[24:27], a[224:227], v[192:195]\n" \
"ds_read_b64_tr_b16 a[18:19], v33 offset:23104\n" \
"v_mfma_f32_16x16x32_bf16 v[196:199], a[20:23], a[236:239], v[196:199]\n" \
"ds_read_b64_tr_b16 a[20:21], v33 offset:27712\n" \
"v_cvt_pk_bf16_f32 v184, v184, v185\n" \
"v_cvt_pk_bf16_f32 v185, v186, v187\n" \
"v_cvt_pk_bf16_f32 v186, v188, v189\n" \
"v_cvt_pk_bf16_f32 v187, v190, v191\n" \
"buffer_store_dwordx4 v[184:187], v20, s[56:59], s92 offen offset:1024 sc1\n" \
"s_waitcnt vmcnt(18)\n" \
"s_mov_b64 exec, 1\n" \
"buffer_store_dword v39, off, s[56:59], s70 offset:20 sc1\n" \
"s_mov_b64 exec, -1\n" \
"s_add_u32 s92, s92, 0x3800\n" \
"v_mfma_f32_16x16x32_bf16 v[196:199], a[24:27], a[240:243], v[196:199]\n" \
"ds_read_b64_tr_b16 a[22:23], v33 offset:32320\n" \
"ds_read_b64_tr_b16 a[24:25], v33 offset:36928\n" \
"v_mfma_f32_16x16x32_bf16 v[196:199], a[28:31], a[244:247], v[196:199]\n" \
"ds_read_b64_tr_b16 a[26:27], v33 offset:41536\n" \
"v_mfma_f32_16x16x32_bf16 v[196:199], a[192:195], a[248:251], v[196:199]\n" \
"v_mfma_f32_16x16x32_bf16 v[192:195], a[28:31], a[228:231], v[192:195]\n" \
"ds_read_b64_tr_b16 a[28:29], v33 offset:46144\n" \
"ds_read_b64_tr_b16 a[30:31], v33 offset:50752\n" \
"v_mfma_f32_16x16x32_bf16 v[192:195], a[192:195], a[232:235], v[192:195]\n" \
"buffer_load_dwordx4 v[100:103], v20, s[56:59], s97 offen offset:1024 sc1\n" \
"v_mfma_f32_16x16x32_bf16 v[192:195], a[196:199], a[160:163], v[192:195]\n" \
"ds_read_b64_tr_b16 a[192:193], v33 offset:55360\n" \
"ds_read_b64_tr_b16 a[194:195], v33 offset:59968\n" \
"v_mfma_f32_16x16x32_bf16 v[192:195], a[200:203], a[164:167], v[192:195]\n" \
"v_mfma_f32_16x16x32_bf16 v[196:199], a[196:199], a[176:179], v[196:199]\n" \
"ds_read_b64_tr_b16 a[196:197], v10 offset:64\n" \
"ds_read_b64_tr_b16 a[198:199], v10 offset:4672\n" \
"v_mfma_f32_16x16x32_bf16 v[196:199], a[200:203], a[180:183], v[196:199]\n" \
"ds_read_b64_tr_b16 a[200:201], v34 offset:64\n" \
"ds_read_b64_tr_b16 a[202:203], v34 offset:4672\n" \
"v_mfma_f32_16x16x32_bf16 v[196:199], a[204:207], a[184:187], v[196:199]\n" \
"buffer_load_dwordx4 v[104:107], v32, s[56:59], s97 offen offset:0 sc1\n" \
"v_mfma_f32_16x16x32_bf16 v[196:199], a[208:211], a[188:191], v[196:199]\n" \
"v_mfma_f32_16x16x32_bf16 v[192:195], a[204:207], a[168:171], v[192:195]\n" \
"ds_read_b64_tr_b16 a[204:205], v34 offset:9280\n" \
"ds_read_b64_tr_b16 a[206:207], v34 offset:13888\n" \
"v_mfma_f32_16x16x32_bf16 v[192:195], a[208:211], a[172:175], v[192:195]\n" \
"ds_read_b64_tr_b16 a[208:209], v34 offset:18496\n" \
"s_waitcnt lgkmcnt(14)\n" \
"v_mfma_f32_16x16x32_bf16 v[192:195], a[212:215], a[96:99], v[192:195]\n" \
"ds_read_b64_tr_b16 a[210:211], v34 offset:23104\n" \
"v_mfma_f32_16x16x32_bf16 v[192:195], a[216:219], a[100:103], v[192:195]\n" \
"buffer_load_dwordx4 v[108:111], v32, s[56:59], s97 offen offset:1024 sc1\n" \
"v_mfma_f32_16x16x32_bf16 v[196:199], a[212:215], a[112:115], v[196:199]\n" \
"ds_read_b64_tr_b16 a[212:213], v10 offset:9280\n" \
"ds_read_b64_tr_b16 a[214:215], v10 offset:13888\n" \
"v_mfma_f32_16x16x32_bf16 v[196:199], a[216:219], a[116:119], v[196:199]\n" \
"ds_read_b64_tr_b16 a[216:217], v33 offset:96\n" \
"ds_read_b64_tr_b16 a[218:219], v33 offset:4704\n" \
"s_waitcnt vmcnt(19)\n" \
"s_mov_b64 exec, 1\n" \
"buffer_store_dword v39, off, s[56:59], s71 offset:24 sc1\n" \
"s_mov_b64 exec, -1\n" \
"v_mfma_f32_16x16x32_bf16 v[196:199], a[0:3], a[120:123], v[196:199]\n" \
"v_mfma_f32_16x16x32_bf16 v[196:199], a[4:7], a[124:127], v[196:199]\n" \
"v_mfma_f32_16x16x32_bf16 v[192:195], a[0:3], a[104:107], v[192:195]\n" \
"ds_read_b64_tr_b16 a[0:1], v33 offset:9312\n" \
"ds_read_b64_tr_b16 a[2:3], v33 offset:13920\n" \
"v_mfma_f32_16x16x32_bf16 v[192:195], a[4:7], a[108:111], v[192:195]\n" \
"ds_read_b64_tr_b16 a[4:5], v33 offset:18528\n" \
"ds_read_b64_tr_b16 a[6:7], v33 offset:23136\n" \
"v_mfma_f32_16x16x32_bf16 v[200:203], a[8:11], a[220:223], v[200:203]\n" \
"v_mfma_f32_16x16x32_bf16 v[200:203], a[12:15], a[224:227], v[200:203]\n" \
"v_mfma_f32_16x16x32_bf16 v[204:207], a[8:11], a[236:239], v[204:207]\n" \
"ds_read_b64_tr_b16 a[8:9], v33 offset:27744\n" \
"ds_read_b64_tr_b16 a[10:11], v33 offset:32352\n" \
"v_mfma_f32_16x16x32_bf16 v[204:207], a[12:15], a[240:243], v[204:207]\n" \
"v_cvt_pk_bf16_f32 v192, v192, v193\n" \
"v_cvt_pk_bf16_f32 v193, v194, v195\n" \
"v_cvt_pk_bf16_f32 v194, v196, v197\n" \
"v_cvt_pk_bf16_f32 v195, v198, v199\n" \
"buffer_store_dwordx4 v[192:195], v20, s[56:59], s66 offen offset:1024 sc1\n" \
"v_mfma_f32_16x16x32_bf16 v[204:207], a[16:19], a[244:247], v[204:207]\n" \
"ds_read_b64_tr_b16 a[12:13], v33 offset:36960\n" \
"s_waitcnt lgkmcnt(11)\n" \
"v_mfma_f32_16x16x32_bf16 v[204:207], a[20:23], a[248:251], v[204:207]\n" \
"ds_read_b64_tr_b16 a[14:15], v33 offset:41568\n" \
"v_mfma_f32_16x16x32_bf16 v[200:203], a[16:19], a[228:231], v[200:203]\n" \
"ds_read_b64_tr_b16 a[16:17], v33 offset:46176\n" \
"ds_read_b64_tr_b16 a[18:19], v33 offset:50784\n" \
"v_mfma_f32_16x16x32_bf16 v[200:203], a[20:23], a[232:235], v[200:203]\n" \
"ds_read_b64_tr_b16 a[20:21], v33 offset:55392\n" \
"ds_read_b64_tr_b16 a[22:23], v33 offset:60000\n" \
"v_mfma_f32_16x16x32_bf16 v[200:203], a[24:27], a[160:163], v[200:203]\n" \
"v_mfma_f32_16x16x32_bf16 v[200:203], a[28:31], a[164:167], v[200:203]\n" \
"v_mfma_f32_16x16x32_bf16 v[204:207], a[24:27], a[176:179], v[204:207]\n" \
"ds_read_b64_tr_b16 a[24:25], v10 offset:96\n" \
"ds_read_b64_tr_b16 a[26:27], v10 offset:4704\n" \
"v_mfma_f32_16x16x32_bf16 v[204:207], a[28:31], a[180:183], v[204:207]\n" \
"ds_read_b64_tr_b16 a[28:29], v34 offset:96\n" \
"ds_read_b64_tr_b16 a[30:31], v34 offset:4704\n" \
"v_mfma_f32_16x16x32_bf16 v[204:207], a[192:195], a[184:187], v[204:207]\n" \
"v_mfma_f32_16x16x32_bf16 v[204:207], a[196:199], a[188:191], v[204:207]\n" \
"v_mfma_f32_16x16x32_bf16 v[200:203], a[192:195], a[168:171], v[200:203]\n" \
"ds_read_b64_tr_b16 a[192:193], v34 offset:9312\n" \
"ds_read_b64_tr_b16 a[194:195], v34 offset:13920\n" \
"v_mfma_f32_16x16x32_bf16 v[200:203], a[196:199], a[172:175], v[200:203]\n" \
"ds_read_b64_tr_b16 a[196:197], v34 offset:18528\n" \
"ds_read_b64_tr_b16 a[198:199], v34 offset:23136\n" \
"v_mfma_f32_16x16x32_bf16 v[200:203], a[200:203], a[96:99], v[200:203]\n" \
"v_mfma_f32_16x16x32_bf16 v[200:203], a[204:207], a[100:103], v[200:203]\n" \
"v_mfma_f32_16x16x32_bf16 v[204:207], a[200:203], a[112:115], v[204:207]\n" \
"ds_read_b64_tr_b16 a[200:201], v10 offset:9312\n" \
"ds_read_b64_tr_b16 a[202:203], v10 offset:13920\n" \
"v_mfma_f32_16x16x32_bf16 v[204:207], a[204:207], a[116:119], v[204:207]\n" \
"ds_read_b64_tr_b16 a[204:205], v33 offset:128\n" \
"ds_read_b64_tr_b16 a[206:207], v33 offset:4736\n" \
"v_mfma_f32_16x16x32_bf16 v[204:207], a[208:211], a[120:123], v[204:207]\n" \
"s_waitcnt lgkmcnt(15)\n" \
"v_mfma_f32_16x16x32_bf16 v[204:207], a[212:215], a[124:127], v[204:207]\n" \
"v_mfma_f32_16x16x32_bf16 v[200:203], a[208:211], a[104:107], v[200:203]\n" \
"ds_read_b64_tr_b16 a[208:209], v33 offset:9344\n" \
"ds_read_b64_tr_b16 a[210:211], v33 offset:13952\n" \
"v_mfma_f32_16x16x32_bf16 v[200:203], a[212:215], a[108:111], v[200:203]\n" \
"ds_read_b64_tr_b16 a[212:213], v33 offset:18560\n" \
"ds_read_b64_tr_b16 a[214:215], v33 offset:23168\n" \
"v_mfma_f32_16x16x32_bf16 v[208:211], a[216:219], a[220:223], v[208:211]\n" \
"v_mfma_f32_16x16x32_bf16 v[208:211], a[0:3], a[224:227], v[208:211]\n" \
"v_mfma_f32_16x16x32_bf16 v[212:215], a[216:219], a[236:239], v[212:215]\n" \
"ds_read_b64_tr_b16 a[216:217], v33 offset:27776\n" \
"ds_read_b64_tr_b16 a[218:219], v33 offset:32384\n" \
"v_mfma_f32_16x16x32_bf16 v[212:215], a[0:3], a[240:243], v[212:215]\n" \
"v_cvt_pk_bf16_f32 v200, v200, v201\n" \
"v_cvt_pk_bf16_f32 v201, v202, v203\n" \
"v_cvt_pk_bf16_f32 v202, v204, v205\n" \
"v_cvt_pk_bf16_f32 v203, v206, v207\n" \
"buffer_store_dwordx4 v[200:203], v20, s[56:59], s67 offen offset:1024 sc1\n" \
"v_mfma_f32_16x16x32_bf16 v[212:215], a[4:7], a[244:247], v[212:215]\n" \
"ds_read_b64_tr_b16 a[0:1], v33 offset:36992\n" \
"ds_read_b64_tr_b16 a[2:3], v33 offset:41600\n" \
"v_mfma_f32_16x16x32_bf16 v[212:215], a[8:11], a[248:251], v[212:215]\n" \
"v_mfma_f32_16x16x32_bf16 v[208:211], a[4:7], a[228:231], v[208:211]\n" \
"ds_read_b64_tr_b16 a[4:5], v33 offset:46208\n" \
"ds_read_b64_tr_b16 a[6:7], v33 offset:50816\n" \
"v_mfma_f32_16x16x32_bf16 v[208:211], a[8:11], a[232:235], v[208:211]\n" \
"ds_read_b64_tr_b16 a[8:9], v33 offset:55424\n" \
"ds_read_b64_tr_b16 a[10:11], v33 offset:60032\n" \
"v_mfma_f32_16x16x32_bf16 v[208:211], a[12:15], a[160:163], v[208:211]\n" \
"s_waitcnt lgkmcnt(13)\n" \
"v_mfma_f32_16x16x32_bf16 v[208:211], a[16:19], a[164:167], v[208:211]\n" \
"v_mfma_f32_16x16x32_bf16 v[212:215], a[12:15], a[176:179], v[212:215]\n" \
"s_waitcnt vmcnt(8)\n" \
"v_cmp_eq_u32_e64 s[98:99], s62, v37\n" \
"s_mov_b64 exec, 3\n" \
"buffer_store_dword v39, v41, s[56:59], s73 offen sc1\n" \
"s_mov_b64 exec, -1\n" \
"s_andn2_b32 s75, 0x7f, s98\n" \
"s_mov_b32 s20, s75\n" \
"s_bitcmp1_b32 s20, 0\n" \
"s_cselect_b32 s93, 0x40000000, s12\n" \
"s_bitcmp1_b32 s20, 1\n" \
"s_cselect_b32 s94, 0x40000000, s12\n" \
"s_bitcmp1_b32 s20, 2\n" \
"s_cselect_b32 s95, 0x40000000, s13\n" \
"s_bitcmp1_b32 s20, 3\n" \
"s_cselect_b32 s96, 0x40000000, s13\n" \
"s_bitcmp1_b32 s20, 4\n" \
"s_cselect_b32 s97, 0x40000000, s14\n" \
"s_bitcmp1_b32 s20, 5\n" \
"s_cselect_b32 s64, 0x40000000, s14\n" \
"s_bitcmp1_b32 s20, 6\n" \
"s_cselect_b32 s65, 0x40000000, s15\n" \
"s_add_u32 s66, s66, 0x3000\n" \
"s_add_u32 s67, s67, 0x2800\n" \
"v_mfma_f32_16x16x32_bf16 v[212:215], a[16:19], a[180:183], v[212:215]\n" \
"buffer_load_dwordx4 v[116:119], v20, s[56:59], s93 offen offset:0 sc1\n" \
"v_mfma_f32_16x16x32_bf16 v[212:215], a[20:23], a[184:187], v[212:215]\n" \
"ds_read_b64_tr_b16 a[12:13], v10 offset:128\n" \
"ds_read_b64_tr_b16 a[14:15], v10 offset:4736\n" \
"v_mfma_f32_16x16x32_bf16 v[212:215], a[24:27], a[188:191], v[212:215]\n" \
"ds_read_b64_tr_b16 a[16:17], v34 offset:128\n" \
"ds_read_b64_tr_b16 a[18:19], v34 offset:4736\n" \
"v_mfma_f32_16x16x32_bf16 v[208:211], a[20:23], a[168:171], v[208:211]\n" \
"ds_read_b64_tr_b16 a[20:21], v34 offset:9344\n" \
"ds_read_b64_tr_b16 a[22:23], v34 offset:13952\n" \
"v_mfma_f32_16x16x32_bf16 v[208:211], a[24:27], a[172:175], v[208:211]\n" \
"ds_read_b64_tr_b16 a[24:25], v34 offset:18560\n" \
"ds_read_b64_tr_b16 a[26:27], v34 offset:23168\n" \
"v_mfma_f32_16x16x32_bf16 v[208:211], a[28:31], a[96:99], v[208:211]\n" \
"v_mfma_f32_16x16x32_bf16 v[208:211], a[192:195], a[100:103], v[208:211]\n" \
"v_mfma_f32_16x16x32_bf16 v[212:215], a[28:31], a[112:115], v[212:215]\n" \
"ds_read_b64_tr_b16 a[28:29], v10 offset:9344\n" \
"ds_read_b64_tr_b16 a[30:31], v10 offset:13952\n" \
"v_mfma_f32_16x16x32_bf16 v[212:215], a[192:195], a[116:119], v[212:215]\n" \
"ds_read_b64_tr_b16 a[192:193], v33 offset:160\n" \
"ds_read_b64_tr_b16 a[194:195], v33 offset:4768\n" \
"v_mfma_f32_16x16x32_bf16 v[212:215], a[196:199], a[120:123], v[212:215]\n" \
"v_mfma_f32_16x16x32_bf16 v[212:215], a[200:203], a[124:127], v[212:215]\n" \
"v_mfma_f32_16x16x32_bf16 v[208:211], a[196:199], a[104:107], v[208:211]\n" \
"ds_read_b64_tr_b16 a[196:197], v33 offset:9376\n" \
"ds_read_b64_tr_b16 a[198:199], v33 offset:13984\n" \
"v_mfma_f32_16x16x32_bf16 v[208:211], a[200:203], a[108:111], v[208:211]\n" \
"ds_read_b64_tr_b16 a[200:201], v33 offset:18592\n" \
"s_waitcnt lgkmcnt(15)\n" \
"v_mfma_f32_16x16x32_bf16 v[216:219], a[204:207], a[220:223], v[216:219]\n" \
"ds_read_b64_tr_b16 a[202:203], v33 offset:23200\n" \
"v_mfma_f32_16x16x32_bf16 v[216:219], a[208:211], a[224:227], v[216:219]\n" \
"buffer_load_dwordx4 v[124:127], v20, s[56:59], s94 offen offset:2048 sc1\n" \
"v_mfma_f32_16x16x32_bf16 v[220:223], a[204:207], a[236:239], v[220:223]\n" \
"ds_read_b64_tr_b16 a[204:205], v33 offset:27808\n" \
"v_cvt_pk_bf16_f32 v208, v208, v209\n" \
"v_cvt_pk_bf16_f32 v209, v210, v211\n" \
"v_cvt_pk_bf16_f32 v210, v212, v213\n" \
"v_cvt_pk_bf16_f32 v211, v214, v215\n" \
"v_mfma_f32_16x16x32_bf16 v[220:223], a[208:211], a[240:243], v[220:223]\n" \
"buffer_store_dwordx4 v[208:211], v20, s[56:59], s68 offen offset:1024 sc1\n" \
"s_add_u32 s68, s68, 0x2000\n" \
"v_mfma_f32_16x16x32_bf16 v[220:223], a[212:215], a[244:247], v[220:223]\n" \
"ds_read_b64_tr_b16 a[206:207], v33 offset:32416\n" \
"ds_read_b64_tr_b16 a[208:209], v33 offset:37024\n" \
"v_mfma_f32_16x16x32_bf16 v[220:223], a[216:219], a[248:251], v[220:223]\n" \
"ds_read_b64_tr_b16 a[210:211], v33 offset:41632\n" \
"v_mfma_f32_16x16x32_bf16 v[216:219], a[212:215], a[228:231], v[216:219]\n" \
"ds_read_b64_tr_b16 a[212:213], v33 offset:46240\n" \
"ds_read_b64_tr_b16 a[214:215], v33 offset:50848\n" \
"v_mfma_f32_16x16x32_bf16 v[216:219], a[216:219], a[232:235], v[216:219]\n" \
"ds_read_b64_tr_b16 a[216:217], v33 offset:55456\n" \
"ds_read_b64_tr_b16 a[218:219], v33 offset:60064\n" \
"v_mfma_f32_16x16x32_bf16 v[216:219], a[0:3], a[160:163], v[216:219]\n" \
"v_mfma_f32_16x16x32_bf16 v[216:219], a[4:7], a[164:167], v[216:219]\n" \
"v_mfma_f32_16x16x32_bf16 v[220:223], a[0:3], a[176:179], v[220:223]\n" \
"ds_read_b64_tr_b16 a[0:1], v10 offset:160\n" \
"ds_read_b64_tr_b16 a[2:3], v10 offset:4768\n" \
"v_mfma_f32_16x16x32_bf16 v[220:223], a[4:7], a[180:183], v[220:223]\n" \
"ds_read_b64_tr_b16 a[4:5], v34 offset:160\n" \
"ds_read_b64_tr_b16 a[6:7], v34 offset:4768\n" \
"v_mfma_f32_16x16x32_bf16 v[220:223], a[8:11], a[184:187], v[220:223]\n" \
"s_waitcnt lgkmcnt(13)\n" \
"v_mfma_f32_16x16x32_bf16 v[220:223], a[12:15], a[188:191], v[220:223]\n" \
"s_waitcnt vmcnt(11)\n" \
"s_mov_b64 exec, 1\n" \
"buffer_store_dword v39, off, s[56:59], s92 offset:32 sc1\n" \
"s_mov_b64 exec, -1\n" \
"v_mfma_f32_16x16x32_bf16 v[216:219], a[8:11], a[168:171], v[216:219]\n" \
"ds_read_b64_tr_b16 a[8:9], v34 offset:9376\n" \
"ds_read_b64_tr_b16 a[10:11], v34 offset:13984\n" \
"v_mfma_f32_16x16x32_bf16 v[216:219], a[12:15], a[172:175], v[216:219]\n" \
"ds_read_b64_tr_b16 a[12:13], v34 offset:18592\n" \
"ds_read_b64_tr_b16 a[14:15], v34 offset:23200\n" \
"v_mfma_f32_16x16x32_bf16 v[216:219], a[16:19], a[96:99], v[216:219]\n" \
"v_mfma_f32_16x16x32_bf16 v[216:219], a[20:23], a[100:103], v[216:219]\n" \
"buffer_load_dwordx4 v[132:135], v20, s[56:59], s95 offen offset:0 sc1\n" \
"v_mfma_f32_16x16x32_bf16 v[220:223], a[16:19], a[112:115], v[220:223]\n" \
"ds_read_b64_tr_b16 a[16:17], v10 offset:9376\n" \
"ds_read_b64_tr_b16 a[18:19], v10 offset:13984\n" \
"v_mfma_f32_16x16x32_bf16 v[220:223], a[20:23], a[116:119], v[220:223]\n" \
"ds_read_b64_tr_b16 a[20:21], v33 offset:192\n" \
"ds_read_b64_tr_b16 a[22:23], v33 offset:4800\n" \
"v_mfma_f32_16x16x32_bf16 v[220:223], a[24:27], a[120:123], v[220:223]\n" \
"v_mfma_f32_16x16x32_bf16 v[220:223], a[28:31], a[124:127], v[220:223]\n" \
"v_mfma_f32_16x16x32_bf16 v[216:219], a[24:27], a[104:107], v[216:219]\n" \
"ds_read_b64_tr_b16 a[24:25], v33 offset:9408\n" \
"ds_read_b64_tr_b16 a[26:27], v33 offset:14016\n" \
"v_mfma_f32_16x16x32_bf16 v[216:219], a[28:31], a[108:111], v[216:219]\n" \
"buffer_load_dword v37, v40, s[56:59], s74 offen sc1\n" \
"v_mfma_f32_16x16x32_bf16 v[224:227], a[192:195], a[220:223], v[224:227]\n" \
"ds_read_b64_tr_b16 a[28:29], v33 offset:18624\n" \
"ds_read_b64_tr_b16 a[30:31], v33 offset:23232\n" \
"v_mfma_f32_16x16x32_bf16 v[224:227], a[196:199], a[224:227], v[224:227]\n" \
"v_mfma_f32_16x16x32_bf16 v[228:231], a[192:195], a[236:239], v[228:231]\n" \
"ds_read_b64_tr_b16 a[192:193], v33 offset:27840\n" \
"ds_read_b64_tr_b16 a[194:195], v33 offset:32448\n" \
"v_cvt_pk_bf16_f32 v216, v216, v217\n" \
"v_cvt_pk_bf16_f32 v217, v218, v219\n" \
"v_cvt_pk_bf16_f32 v218, v220, v221\n" \
"v_cvt_pk_bf16_f32 v219, v222, v223\n" \
"buffer_store_dwordx4 v[216:219], v20, s[56:59], s69 offen offset:1024 sc1\n" \
"s_add_u32 s69, s69, 0x1800\n" \
"v_mfma_f32_16x16x32_bf16 v[228:231], a[196:199], a[240:243], v[228:231]\n" \
"ds_read_b64_tr_b16 a[196:197], v33 offset:37056\n" \
"s_waitcnt lgkmcnt(12)\n" \
"v_mfma_f32_16x16x32_bf16 v[228:231], a[200:203], a[244:247], v[228:231]\n" \
"ds_read_b64_tr_b16 a[198:199], v33 offset:41664\n" \
"v_mfma_f32_16x16x32_bf16 v[228:231], a[204:207], a[248:251], v[228:231]\n" \
"v_mfma_f32_16x16x32_bf16 v[224:227], a[200:203], a[228:231], v[224:227]\n" \
"buffer_load_dwordx4 v[140:143], v20, s[56:59], s96 offen offset:2048 sc1\n" \
"v_mfma_f32_16x16x32_bf16 v[224:227], a[204:207], a[232:235], v[224:227]\n" \
"ds_read_b64_tr_b16 a[200:201], v33 offset:46272\n" \
"ds_read_b64_tr_b16 a[202:203], v33 offset:50880\n" \
"v_mfma_f32_16x16x32_bf16 v[224:227], a[208:211], a[160:163], v[224:227]\n" \
"ds_read_b64_tr_b16 a[204:205], v33 offset:55488\n" \
"ds_read_b64_tr_b16 a[206:207], v33 offset:60096\n" \
"v_mfma_f32_16x16x32_bf16 v[224:227], a[212:215], a[164:167], v[224:227]\n" \
"v_mfma_f32_16x16x32_bf16 v[228:231], a[208:211], a[176:179], v[228:231]\n" \
"ds_read_b64_tr_b16 a[208:209], v10 offset:192\n" \
"ds_read_b64_tr_b16 a[210:211], v10 offset:4800\n" \
"v_mfma_f32_16x16x32_bf16 v[228:231], a[212:215], a[180:183], v[228:231]\n" \
"ds_read_b64_tr_b16 a[212:213], v34 offset:192\n" \
"ds_read_b64_tr_b16 a[214:215], v34 offset:4800\n" \
"v_mfma_f32_16x16x32_bf16 v[228:231], a[216:219], a[184:187], v[228:231]\n" \
"v_mfma_f32_16x16x32_bf16 v[228:231], a[0:3], a[188:191], v[228:231]\n" \
"v_mfma_f32_16x16x32_bf16 v[224:227], a[216:219], a[168:171], v[224:227]\n" \
"ds_read_b64_tr_b16 a[216:217], v34 offset:9408\n" \
"ds_read_b64_tr_b16 a[218:219], v34 offset:14016\n" \
"v_mfma_f32_16x16x32_bf16 v[224:227], a[0:3], a[172:175], v[224:227]\n" \
"ds_read_b64_tr_b16 a[0:1], v34 offset:18624\n" \
"ds_read_b64_tr_b16 a[2:3], v34 offset:23232\n" \
"s_waitcnt vmcnt(10)\n" \
"s_mov_b64 exec, 1\n" \
"buffer_store_dword v39, off, s[56:59], s66 offset:36 sc1\n" \
"s_mov_b64 exec, -1\n" \
"v_mfma_f32_16x16x32_bf16 v[224:227], a[4:7], a[96:99], v[224:227]\n" \
"v_mfma_f32_16x16x32_bf16 v[224:227], a[8:11], a[100:103], v[224:227]\n" \
"v_mfma_f32_16x16x32_bf16 v[228:231], a[4:7], a[112:115], v[228:231]\n" \
"ds_read_b64_tr_b16 a[4:5], v10 offset:9408\n" \
"ds_read_b64_tr_b16 a[6:7], v10 offset:14016\n" \
"v_mfma_f32_16x16x32_bf16 v[228:231], a[8:11], a[116:119], v[228:231]\n" \
"ds_read_b64_tr_b16 a[8:9], v33 offset:224\n" \
"s_waitcnt lgkmcnt(15)\n" \
"v_mfma_f32_16x16x32_bf16 v[228:231], a[12:15], a[120:123], v[228:231]\n" \
"buffer_load_dwordx4 v[148:151], v20, s[56:59], s97 offen offset:0 sc1\n" \
"v_mfma_f32_16x16x32_bf16 v[228:231], a[16:19], a[124:127], v[228:231]\n" \
"ds_read_b64_tr_b16 a[10:11], v33 offset:4832\n" \
"v_mfma_f32_16x16x32_bf16 v[224:227], a[12:15], a[104:107], v[224:227]\n" \
"ds_read_b64_tr_b16 a[12:13], v33 offset:9440\n" \
"ds_read_b64_tr_b16 a[14:15], v33 offset:14048\n" \
"v_mfma_f32_16x16x32_bf16 v[224:227], a[16:19], a[108:111], v[224:227]\n" \
"ds_read_b64_tr_b16 a[16:17], v33 offset:18656\n" \
"ds_read_b64_tr_b16 a[18:19], v33 offset:23264\n" \
"v_mfma_f32_16x16x32_bf16 v[232:235], a[20:23], a[220:223], v[232:235]\n" \
"v_mfma_f32_16x16x32_bf16 v[232:235], a[24:27], a[224:227], v[232:235]\n" \
"v_mfma_f32_16x16x32_bf16 v[236:239], a[20:23], a[236:239], v[236:239]\n" \
"ds_read_b64_tr_b16 a[20:21], v33 offset:27872\n" \
"ds_read_b64_tr_b16 a[22:23], v33 offset:32480\n" \
"v_mfma_f32_16x16x32_bf16 v[236:239], a[24:27], a[240:243], v[236:239]\n" \
"v_cvt_pk_bf16_f32 v224, v224, v225\n" \
"v_cvt_pk_bf16_f32 v225, v226, v227\n" \
"v_cvt_pk_bf16_f32 v226, v228, v229\n" \
"v_cvt_pk_bf16_f32 v227, v230, v231\n" \
"v_mfma_f32_16x16x32_bf16 v[236:239], a[28:31], a[244:247], v[236:239]\n" \
"ds_read_b64_tr_b16 a[24:25], v33 offset:37088\n" \
"ds_read_b64_tr_b16 a[26:27], v33 offset:41696\n" \
"v_mfma_f32_16x16x32_bf16 v[236:239], a[192:195], a[248:251], v[236:239]\n" \
"v_mfma_f32_16x16x32_bf16 v[232:235], a[28:31], a[228:231], v[232:235]\n" \
"ds_read_b64_tr_b16 a[28:29], v33 offset:46304\n" \
"ds_read_b64_tr_b16 a[30:31], v33 offset:50912\n" \
"v_mfma_f32_16x16x32_bf16 v[232:235], a[192:195], a[232:235], v[232:235]\n" \
"ds_read_b64_tr_b16 a[192:193], v33 offset:55520\n" \
"ds_read_b64_tr_b16 a[194:195], v33 offset:60128\n" \
"v_mfma_f32_16x16x32_bf16 v[232:235], a[196:199], a[160:163], v[232:235]\n" \
"s_waitcnt lgkmcnt(14)\n" \
"v_mfma_f32_16x16x32_bf16 v[232:235], a[200:203], a[164:167], v[232:235]\n" \
"buffer_load_dwordx4 v[156:159], v20, s[56:59], s64 offen offset:2048 sc1\n" \
"v_readlane_b32 s64, v11, 51\n" \
"v_mfma_f32_16x16x32_bf16 v[236:239], a[196:199], a[176:179], v[236:239]\n" \
"ds_read_b64_tr_b16 a[196:197], v10 offset:224\n" \
"ds_read_b64_tr_b16 a[198:199], v10 offset:4832\n" \
"v_mfma_f32_16x16x32_bf16 v[236:239], a[200:203], a[180:183], v[236:239]\n" \
"ds_read_b64_tr_b16 a[200:201], v34 offset:224\n" \
"ds_read_b64_tr_b16 a[202:203], v34 offset:4832\n" \
"buffer_store_dwordx4 v[224:227], v20, s[56:59], s64 offen offset:1024 sc1\n" \
"v_mfma_f32_16x16x32_bf16 v[236:239], a[204:207], a[184:187], v[236:239]\n" \
"v_mfma_f32_16x16x32_bf16 v[236:239], a[208:211], a[188:191], v[236:239]\n" \
"v_mfma_f32_16x16x32_bf16 v[232:235], a[204:207], a[168:171], v[232:235]\n" \
"ds_read_b64_tr_b16 a[204:205], v34 offset:9440\n" \
"ds_read_b64_tr_b16 a[206:207], v34 offset:14048\n" \
"s_waitcnt vmcnt(13)\n" \
"s_mov_b64 exec, 1\n" \
"buffer_store_dword v39, off, s[56:59], s67 offset:40 sc1\n" \
"s_mov_b64 exec, -1\n" \
"v_mfma_f32_16x16x32_bf16 v[232:235], a[208:211], a[172:175], v[232:235]\n" \
"ds_read_b64_tr_b16 a[208:209], v34 offset:18656\n" \
"ds_read_b64_tr_b16 a[210:211], v34 offset:23264\n" \
"v_mfma_f32_16x16x32_bf16 v[232:235], a[212:215], a[96:99], v[232:235]\n" \
"v_mfma_f32_16x16x32_bf16 v[232:235], a[216:219], a[100:103], v[232:235]\n" \
"v_mfma_f32_16x16x32_bf16 v[236:239], a[212:215], a[112:115], v[236:239]\n" \
"ds_read_b64_tr_b16 a[212:213], v10 offset:9440\n" \
"ds_read_b64_tr_b16 a[214:215], v10 offset:14048\n" \
"v_mfma_f32_16x16x32_bf16 v[236:239], a[216:219], a[116:119], v[236:239]\n" \
"ds_read_b64_tr_b16 a[216:217], v36 offset:0\n" \
"ds_read_b64_tr_b16 a[218:219], v36 offset:1024\n" \
"v_mfma_f32_16x16x32_bf16 v[236:239], a[0:3], a[120:123], v[236:239]\n" \
"v_mfma_f32_16x16x32_bf16 v[236:239], a[4:7], a[124:127], v[236:239]\n" \
"v_mfma_f32_16x16x32_bf16 v[232:235], a[0:3], a[104:107], v[232:235]\n" \
"ds_read_b64_tr_b16 a[0:1], v36 offset:2048\n" \
"ds_read_b64_tr_b16 a[2:3], v36 offset:3072\n" \
"v_mfma_f32_16x16x32_bf16 v[232:235], a[4:7], a[108:111], v[232:235]\n" \
"ds_read_b64_tr_b16 a[4:5], v36 offset:32\n" \
"buffer_load_dwordx4 v[164:167], v20, s[56:59], s65 offen offset:0 sc1\n" \
"v_readlane_b32 s65, v11, 52\n" \
"s_waitcnt lgkmcnt(11)\n" \
"v_mfma_f32_16x16x32_bf16 v[240:243], a[8:11], a[220:223], v[240:243]\n" \
"ds_read_b64_tr_b16 a[6:7], v36 offset:1056\n" \
"v_mfma_f32_16x16x32_bf16 v[240:243], a[12:15], a[224:227], v[240:243]\n" \
"v_mfma_f32_16x16x32_bf16 v[244:247], a[8:11], a[236:239], v[244:247]\n" \
"v_cvt_pk_bf16_f32 v232, v232, v233\n" \
"v_cvt_pk_bf16_f32 v233, v234, v235\n" \
"v_cvt_pk_bf16_f32 v234, v236, v237\n" \
"v_cvt_pk_bf16_f32 v235, v238, v239\n" \
"buffer_store_dwordx4 v[232:235], v20, s[56:59], s65 offen offset:1024 sc1\n" \
"v_mfma_f32_16x16x32_bf16 v[244:247], a[12:15], a[240:243], v[244:247]\n" \
"ds_read_b64_tr_b16 a[8:9], v36 offset:2080\n" \
"ds_read_b64_tr_b16 a[10:11], v36 offset:3104\n" \
"v_mfma_f32_16x16x32_bf16 v[244:247], a[16:19], a[244:247], v[244:247]\n" \
"v_mfma_f32_16x16x32_bf16 v[244:247], a[20:23], a[248:251], v[244:247]\n" \
"v_mfma_f32_16x16x32_bf16 v[240:243], a[16:19], a[228:231], v[240:243]\n" \
"v_mfma_f32_16x16x32_bf16 v[240:243], a[20:23], a[232:235], v[240:243]\n" \
"v_mfma_f32_16x16x32_bf16 v[168:171], a[24:27], a[128:131], v[168:171]\n" \
"v_mfma_f32_16x16x32_bf16 v[168:171], a[28:31], a[132:135], v[168:171]\n" \
"v_mfma_f32_16x16x32_bf16 v[172:175], a[24:27], a[144:147], v[172:175]\n" \
"s_waitcnt vmcnt(9)\n" \
"v_cmp_eq_u32_e64 s[98:99], s62, v37\n" \
"buffer_load_dword v37, v40, s[56:59], s74 offen sc1\n" \
"s_mov_b64 exec, 1\n" \
"buffer_store_dword v39, off, s[56:59], s68 offset:44 sc1\n" \
"s_mov_b64 exec, -1\n" \
"s_or_b32 s75, 0x300, s100\n" \
"s_andn2_b32 s75, s75, s98\n" \
"s_mov_b32 s21, s75\n" \
"s_bitcmp1_b32 s21, 16\n" \
"s_cselect_b32 s70, 0x40000000, s16\n" \
"s_bitcmp1_b32 s21, 17\n" \
"s_cselect_b32 s71, 0x40000000, s16\n" \
"s_bitcmp1_b32 s21, 18\n" \
"s_cselect_b32 s72, 0x40000000, s17\n" \
"s_bitcmp1_b32 s21, 19\n" \
"s_cselect_b32 s73, 0x40000000, s17\n" \
"s_bitcmp1_b32 s21, 20\n" \
"s_cselect_b32 s92, 0x40000000, s18\n" \
"s_bitcmp1_b32 s21, 21\n" \
"s_cselect_b32 s93, 0x40000000, s18\n" \
"s_bitcmp1_b32 s21, 22\n" \
"s_cselect_b32 s94, 0x40000000, s19\n" \
"s_bitcmp1_b32 s21, 23\n" \
"s_cselect_b32 s95, 0x40000000, s19\n" \
"s_bitcmp1_b32 s21, 8\n" \
"s_cselect_b32 s96, 0x40000000, s12\n" \
"s_bitcmp1_b32 s21, 9\n" \
"s_cselect_b32 s97, 0x40000000, s12\n" \
"s_add_u32 s64, s64, 0x1000\n" \
"s_add_u32 s65, s65, 0x800\n" \
"v_mfma_f32_16x16x32_bf16 v[172:175], a[28:31], a[148:151], v[172:175]\n" \
"buffer_load_dwordx4 v[112:115], v20, s[56:59], s70 offen offset:0 sc1\n" \
"v_mfma_f32_16x16x32_bf16 v[240:243], a[24:27], a[160:163], v[240:243]\n" \
"v_mfma_f32_16x16x32_bf16 v[240:243], a[28:31], a[164:167], v[240:243]\n" \
"v_mfma_f32_16x16x32_bf16 v[244:247], a[24:27], a[176:179], v[244:247]\n" \
"v_mfma_f32_16x16x32_bf16 v[244:247], a[28:31], a[180:183], v[244:247]\n" \
"buffer_load_dwordx4 v[184:187], v20, s[56:59], s71 offen offset:2048 sc1\n" \
"v_mfma_f32_16x16x32_bf16 v[244:247], a[192:195], a[184:187], v[244:247]\n" \
"v_readlane_b32 s71, v11, 54\n" \
"v_mfma_f32_16x16x32_bf16 v[244:247], a[196:199], a[188:191], v[244:247]\n" \
"v_mfma_f32_16x16x32_bf16 v[240:243], a[192:195], a[168:171], v[240:243]\n" \
"v_mfma_f32_16x16x32_bf16 v[240:243], a[196:199], a[172:175], v[240:243]\n" \
"buffer_load_dwordx4 v[120:123], v20, s[56:59], s72 offen offset:0 sc1\n" \
"v_mfma_f32_16x16x32_bf16 v[172:175], a[192:195], a[152:155], v[172:175]\n" \
"v_readlane_b32 s72, v11, 45\n" \
"v_mfma_f32_16x16x32_bf16 v[172:175], a[196:199], a[156:159], v[172:175]\n" \
"v_mfma_f32_16x16x32_bf16 v[168:171], a[192:195], a[136:139], v[168:171]\n" \
"v_mfma_f32_16x16x32_bf16 v[168:171], a[196:199], a[140:143], v[168:171]\n" \
"buffer_load_dwordx4 v[192:195], v20, s[56:59], s73 offen offset:2048 sc1\n" \
"v_mfma_f32_16x16x32_bf16 v[168:171], a[200:203], a[32:35], v[168:171]\n" \
"v_readlane_b32 s73, v11, 43\n" \
"s_waitcnt lgkmcnt(0)\n" \
"v_mfma_f32_16x16x32_bf16 v[168:171], a[204:207], a[36:39], v[168:171]\n" \
"v_mfma_f32_16x16x32_bf16 v[172:175], a[200:203], a[48:51], v[172:175]\n" \
"v_mfma_f32_16x16x32_bf16 v[172:175], a[204:207], a[52:55], v[172:175]\n" \
"buffer_load_dwordx4 v[128:131], v20, s[56:59], s92 offen offset:0 sc1\n" \
"v_mfma_f32_16x16x32_bf16 v[240:243], a[200:203], a[96:99], v[240:243]\n" \
"v_readlane_b32 s92, v11, 44\n" \
"v_mfma_f32_16x16x32_bf16 v[240:243], a[204:207], a[100:103], v[240:243]\n" \
"v_mfma_f32_16x16x32_bf16 v[244:247], a[200:203], a[112:115], v[244:247]\n" \
"v_mfma_f32_16x16x32_bf16 v[244:247], a[204:207], a[116:119], v[244:247]\n" \
"buffer_load_dwordx4 v[200:203], v20, s[56:59], s93 offen offset:2048 sc1\n" \
"v_mfma_f32_16x16x32_bf16 v[244:247], a[208:211], a[120:123], v[244:247]\n" \
"v_mfma_f32_16x16x32_bf16 v[244:247], a[212:215], a[124:127], v[244:247]\n" \
"v_mfma_f32_16x16x32_bf16 v[240:243], a[208:211], a[104:107], v[240:243]\n" \
"v_mfma_f32_16x16x32_bf16 v[240:243], a[212:215], a[108:111], v[240:243]\n" \
".fill (L_ab + 45056 - .) / 4, 4, 0xBF800000\n" \
"L_ap11:\n" \
"s_cbranch_execz L_ap0\n" \
"buffer_load_dwordx4 v[176:179], v20, s[56:59], s94 offen offset:0 sc1\n" \
"s_waitcnt vmcnt(17)\n" \
"s_mov_b64 exec, 1\n" \
"buffer_store_dword v39, off, s[56:59], s69 offset:48 sc1\n" \
"s_mov_b64 exec, -1\n" \
"v_mfma_f32_16x16x32_bf16 v[172:175], a[208:211], a[56:59], v[172:175]\n" \
"v_mfma_f32_16x16x32_bf16 v[172:175], a[212:215], a[60:63], v[172:175]\n" \
"v_mfma_f32_16x16x32_bf16 v[168:171], a[208:211], a[40:43], v[168:171]\n" \
"v_mfma_f32_16x16x32_bf16 v[168:171], a[212:215], a[44:47], v[168:171]\n" \
"buffer_load_dwordx4 v[248:251], v20, s[56:59], s95 offen offset:2048 sc1\n" \
"v_mfma_f32_16x16x32_bf16 v[168:171], a[216:219], v[64:67], v[168:171]\n" \
"v_mfma_f32_16x16x32_bf16 v[172:175], a[216:219], v[72:75], v[172:175]\n" \
"v_mfma_f32_16x16x32_bf16 v[240:243], a[216:219], v[80:83], v[240:243]\n" \
"v_mfma_f32_16x16x32_bf16 v[244:247], a[216:219], v[88:91], v[244:247]\n" \
"buffer_load_dwordx4 v[188:191], v20, s[56:59], s96 offen offset:1024 sc1\n" \
"v_mfma_f32_16x16x32_bf16 v[168:171], a[0:3], v[68:71], v[168:171]\n" \
"v_mfma_f32_16x16x32_bf16 v[172:175], a[0:3], v[76:79], v[172:175]\n" \
"v_mfma_f32_16x16x32_bf16 v[240:243], a[0:3], v[84:87], v[240:243]\n" \
"v_mfma_f32_16x16x32_bf16 v[244:247], a[0:3], v[92:95], v[244:247]\n" \
"buffer_load_dwordx4 v[196:199], v20, s[56:59], s97 offen offset:3072 sc1\n" \
"v_mfma_f32_16x16x32_bf16 v[56:59], a[4:7], v[96:99], 0\n" \
"v_mfma_f32_16x16x32_bf16 v[60:63], a[4:7], v[104:107], 0\n" \
"v_mfma_f32_16x16x32_bf16 v[56:59], a[8:11], v[100:103], v[56:59]\n" \
"v_mfma_f32_16x16x32_bf16 v[60:63], a[8:11], v[108:111], v[60:63]\n" \
"s_waitcnt vmcnt(16)\n" \
"s_mov_b64 exec, 1\n" \
"buffer_store_dword v39, off, s[56:59], s64 offset:52 sc1\n" \
"s_mov_b64 exec, -1\n" \
"s_waitcnt vmcnt(14)\n" \
"s_mov_b64 exec, 1\n" \
"buffer_store_dword v39, off, s[56:59], s65 offset:56 sc1\n" \
"s_mov_b64 exec, -1\n" \
"s_cmp_lg_u32 s20, 0\n" \
"s_cbranch_scc1 L_sna_%=\n" \
"L_rna_%=:\n" \
"s_cmp_lg_u32 s21, 0\n" \
"s_cbranch_scc1 L_snx_%=\n" \
"L_rnx_%=:\n" \
"s_waitcnt vmcnt(14)\n" \
"v_cmp_eq_u32_e64 s[98:99], s62, v37\n" \
"v_mfma_f32_16x16x32_bf16 v[168:171], v[50:53], v[116:119], v[168:171]\n" \
"s_andn2_b32 s75, 0x400, s98\n" \
"s_cbranch_scc1 L_sx0_%=\n" \
"L_rx0_%=:\n" \
"s_add_u32 s66, s63, 0x1000\n" \
"v_mfma_f32_16x16x32_bf16 v[172:175], v[48:51], v[116:119], v[172:175]\n" \
"buffer_load_dwordx4 v[204:207], v20, s[56:59], s66 offen offset:1024 sc1\n" \
"s_andn2_b32 s75, 0x800, s98\n" \
"s_cbranch_scc1 L_sx1_%=\n" \
"L_rx1_%=:\n" \
"s_add_u32 s67, s63, 0x1800\n" \
"v_mfma_f32_16x16x32_bf16 v[168:171], v[50:53], v[124:127], v[168:171]\n" \
"v_mfma_f32_16x16x32_bf16 v[172:175], v[48:51], v[124:127], v[172:175]\n" \
"v_mfma_f32_16x16x32_bf16 v[168:171], v[50:53], v[132:135], v[168:171]\n" \
"buffer_load_dwordx4 v[212:215], v20, s[56:59], s67 offen offset:1024 sc1\n" \
"s_andn2_b32 s75, 0x1000, s98\n" \
"s_cbranch_scc1 L_sx2_%=\n" \
"L_rx2_%=:\n" \
"s_add_u32 s68, s63, 0x2000\n" \
"v_mfma_f32_16x16x32_bf16 v[172:175], v[48:51], v[132:135], v[172:175]\n" \
"v_mfma_f32_16x16x32_bf16 v[168:171], v[50:53], v[140:143], v[168:171]\n" \
"v_mfma_f32_16x16x32_bf16 v[172:175], v[48:51], v[140:143], v[172:175]\n" \
"buffer_load_dwordx4 v[220:223], v20, s[56:59], s68 offen offset:1024 sc1\n" \
"s_andn2_b32 s75, 0x2000, s98\n" \
"s_cbranch_scc1 L_sx3_%=\n" \
"L_rx3_%=:\n" \
"s_add_u32 s69, s63, 0x2800\n" \
"v_mfma_f32_16x16x32_bf16 v[168:171], v[50:53], v[148:151], v[168:171]\n" \
"v_mfma_f32_16x16x32_bf16 v[172:175], v[48:51], v[148:151], v[172:175]\n" \
"v_mfma_f32_16x16x32_bf16 v[168:171], v[50:53], v[156:159], v[168:171]\n" \
"buffer_load_dwordx4 v[228:231], v20, s[56:59], s69 offen offset:1024 sc1\n" \
"s_andn2_b32 s75, 0x4000, s98\n" \
"s_cbranch_scc1 L_sx4_%=\n" \
"L_rx4_%=:\n" \
"s_add_u32 s70, s63, 0x3000\n" \
"v_mfma_f32_16x16x32_bf16 v[172:175], v[48:51], v[156:159], v[172:175]\n" \
"v_mfma_f32_16x16x32_bf16 v[168:171], v[50:53], v[164:167], v[168:171]\n" \
"v_mfma_f32_16x16x32_bf16 v[172:175], v[48:51], v[164:167], v[172:175]\n" \
"buffer_load_dwordx4 v[236:239], v20, s[56:59], s70 offen offset:1024 sc1\n" \
"s_waitcnt vmcnt(17)\n" \
"v_mfma_f32_16x16x32_bf16 v[56:59], v[50:53], v[112:115], v[56:59]\n" \
"v_mfma_f32_16x16x32_bf16 v[60:63], v[48:51], v[112:115], v[60:63]\n" \
"s_waitcnt vmcnt(16)\n" \
"v_mfma_f32_16x16x32_bf16 v[56:59], v[50:53], v[184:187], v[56:59]\n" \
"v_mfma_f32_16x16x32_bf16 v[60:63], v[48:51], v[184:187], v[60:63]\n" \
"v_cvt_pk_bf16_f32 v168, v168, v169\n" \
"s_waitcnt vmcnt(15)\n" \
"v_mfma_f32_16x16x32_bf16 v[56:59], v[50:53], v[120:123], v[56:59]\n" \
"v_cvt_pk_bf16_f32 v172, v172, v173\n" \
"v_cvt_pk_bf16_f32 v169, v170, v171\n" \
"v_mfma_f32_16x16x32_bf16 v[60:63], v[48:51], v[120:123], v[60:63]\n" \
"v_cvt_pk_bf16_f32 v173, v174, v175\n" \
"s_waitcnt vmcnt(14)\n" \
"v_mfma_f32_16x16x32_bf16 v[56:59], v[50:53], v[192:195], v[56:59]\n" \
"buffer_store_dwordx2 v[168:169], v42, s[88:91], s73 offen sc1\n" \
"v_mfma_f32_16x16x32_bf16 v[60:63], v[48:51], v[192:195], v[60:63]\n" \
"s_waitcnt vmcnt(14)\n" \
"v_mfma_f32_16x16x32_bf16 v[56:59], v[50:53], v[128:131], v[56:59]\n" \
"buffer_store_dwordx2 v[172:173], v43, s[88:91], s73 offen sc1\n" \
"v_mfma_f32_16x16x32_bf16 v[60:63], v[48:51], v[128:131], v[60:63]\n" \
"s_waitcnt vmcnt(14)\n" \
"v_mfma_f32_16x16x32_bf16 v[56:59], v[50:53], v[200:203], v[56:59]\n" \
"v_mfma_f32_16x16x32_bf16 v[60:63], v[48:51], v[200:203], v[60:63]\n" \
"s_waitcnt vmcnt(13)\n" \
"v_mfma_f32_16x16x32_bf16 v[56:59], v[50:53], v[176:179], v[56:59]\n" \
"v_mfma_f32_16x16x32_bf16 v[60:63], v[48:51], v[176:179], v[60:63]\n" \
"s_waitcnt vmcnt(11)\n" \
"v_mfma_f32_16x16x32_bf16 v[56:59], v[50:53], v[248:251], v[56:59]\n" \
"v_mfma_f32_16x16x32_bf16 v[60:63], v[48:51], v[248:251], v[60:63]\n" \
"s_waitcnt vmcnt(10)\n" \
"v_mfma_f32_16x16x32_bf16 v[240:243], v[50:53], v[188:191], v[240:243]\n" \
"v_mfma_f32_16x16x32_bf16 v[244:247], v[48:51], v[188:191], v[244:247]\n" \
"s_waitcnt vmcnt(9)\n" \
"v_mfma_f32_16x16x32_bf16 v[240:243], v[50:53], v[196:199], v[240:243]\n" \
"v_mfma_f32_16x16x32_bf16 v[244:247], v[48:51], v[196:199], v[244:247]\n" \
"s_mov_b64 exec, 1\n" \
"buffer_store_dword v46, off, s[56:59], s71 offset:0 sc1\n" \
"s_mov_b64 exec, -1\n" \
"v_cvt_pk_bf16_f32 v56, v56, v57\n" \
"v_cvt_pk_bf16_f32 v60, v60, v61\n" \
"v_cvt_pk_bf16_f32 v57, v58, v59\n" \
"v_cvt_pk_bf16_f32 v61, v62, v63\n" \
"buffer_store_dwordx2 v[56:57], v42, s[88:91], s72 offen sc1\n" \
"buffer_store_dwordx2 v[60:61], v43, s[88:91], s72 offen sc1\n" \
"s_waitcnt vmcnt(9)\n" \
"v_mfma_f32_16x16x32_bf16 v[240:243], v[50:53], v[204:207], v[240:243]\n" \
"v_mfma_f32_16x16x32_bf16 v[244:247], v[48:51], v[204:207], v[244:247]\n" \
"s_waitcnt vmcnt(8)\n" \
"v_mfma_f32_16x16x32_bf16 v[240:243], v[50:53], v[212:215], v[240:243]\n" \
"v_mfma_f32_16x16x32_bf16 v[244:247], v[48:51], v[212:215], v[244:247]\n" \
"s_waitcnt vmcnt(7)\n" \
"v_mfma_f32_16x16x32_bf16 v[240:243], v[50:53], v[220:223], v[240:243]\n" \
"v_mfma_f32_16x16x32_bf16 v[244:247], v[48:51], v[220:223], v[244:247]\n" \
"s_waitcnt vmcnt(6)\n" \
"v_mfma_f32_16x16x32_bf16 v[240:243], v[50:53], v[228:231], v[240:243]\n" \
"v_mfma_f32_16x16x32_bf16 v[244:247], v[48:51], v[228:231], v[244:247]\n" \
"s_waitcnt vmcnt(5)\n" \
"v_mfma_f32_16x16x32_bf16 v[240:243], v[50:53], v[236:239], v[240:243]\n" \
"v_mfma_f32_16x16x32_bf16 v[244:247], v[48:51], v[236:239], v[244:247]\n" \
"s_nop 6\n" \
"v_cvt_pk_bf16_f32 v240, v240, v241\n" \
"v_cvt_pk_bf16_f32 v244, v244, v245\n" \
"v_cvt_pk_bf16_f32 v241, v242, v243\n" \
"v_cvt_pk_bf16_f32 v245, v246, v247\n" \
"buffer_store_dwordx2 v[240:241], v42, s[88:91], s92 offen sc1\n" \
"buffer_store_dwordx2 v[244:245], v43, s[88:91], s92 offen sc1\n"
#define ATT_VCLOB "v21","v22","v23","v24","v25","v26","v27","v28","v29","v32","v33","v34","v35","v36","v37","v38","v39","v40","v41","v42","v43","v44","v45","v46","v47","v48","v49","v50","v51","v52","v53","v54","v55","v56","v57","v58","v59","v60","v61","v62","v63","v64","v65","v66","v67","v68","v69","v70","v71","v72","v73","v74","v75","v76","v77","v78","v79","v80","v81","v82","v83","v84","v85","v86","v87","v88","v89","v90","v91","v92","v93","v94","v95","v96","v97","v98","v99","v100","v101","v102","v103","v104","v105","v106","v107","v108","v109","v110","v111","v112","v113","v114","v115","v116","v117","v118","v119","v120","v121","v122","v123","v124","v125","v126","v127","v128","v129","v130","v131","v132","v133","v134","v135","v136","v137","v138","v139","v140","v141","v142","v143","v144","v145","v146","v147","v148","v149","v150","v151","v152","v153","v154","v155","v156","v157","v158","v159","v160","v161","v162","v163","v164","v165","v166","v167","v168","v169","v170","v171","v172","v173","v174","v175","v176","v177","v178","v179","v180","v181","v182","v183","v184","v185","v186","v187","v188","v189","v190","v191","v192","v193","v194","v195","v196","v197","v198","v199","v200","v201","v202","v203","v204","v205","v206","v207","v208","v209","v210","v211","v212","v213","v214","v215","v216","v217","v218","v219","v220","v221","v222","v223","v224","v225","v226","v227","v228","v229","v230","v231","v232","v233","v234","v235","v236","v237","v238","v239","v240","v241","v242","v243","v244","v245","v246","v247","v248","v249","v250","v251","v252","v253","v254","v255"
#define ATT_SCLOB "s63","s64","s65","s66","s67","s68","s69","s70","s71","s72","s73","s74","s75","s76","s77","s78","s79","s80","s81","s82","s83","s84","s85","s86","s87","s88","s89","s90","s91","s92","s93","s95","s96","s97","s98","s99","s100","s101"
#define ATT_WTCH 0
#define ATT_PEEL 1
#define ATT_PEEL_FOW 0
enum { T_fo, T_fj, T_fa, T_fas, T_fb, T_fbs, T_f8, T_mk8, T_wt, T_wol, T_woh, T_wbl, T_wbh, T_outl, T_outh, T_r1l, T_r1h, T_r1n, T_r1s, T_m0, T_m1, T_m1b, T_mw, T_m3, T_m4, T_ml, T_w0, T_w1, T_w2, T_w3, T_A1, T_A2, T_A3, T_B1, T_B2, T_B3, T_Ao, T_Bo, T_Al, T_Bl, T_l8, T_xr, T_x8r, T_oA, T_oB, T_o8, T_x0, T_x1, T_x2, T_x3, T_x4, T_x5, T_x6, T_x8, T_ep };

__device__ __forceinline__ void out_consts(u32x4 (&vp)[3], const u16* wo, const u16* wob, const u16* out, const unsigned b, const unsigned w, const unsigned lane) {
    const unsigned sp = b & 7u, nt = (b >> 3) & 3u, j = b >> 5, head = 4u * sp + nt, OOR = 0x40000000u;
    unsigned vw[9];
#pragma unroll
    for (unsigned i = 0; i < 9u; ++i) {
        const unsigned q = 64u * i + lane, row = __umul24(q, 3641u) >> 16, c = q - __umul24(row, 18u);
        vw[i] = __umul24(row, DM * 2) + (c < 16u ? (c * 16u + sp * 32u + 32u) & 255u : c * 16u) - (i & 3u) * 1024u;
    }
    const unsigned row = 16u * w + (lane >> 2), r9 = __umul24(16u * (row >> 5) + (row & 12u) + 2u * ((row >> 4) & 1u) + ((row >> 1) & 1u), 18u) + 16u + (row & 1u);
    unsigned vwl = __umul24(r9, DM * 2) + ((lane & 2u) ? 256u : sp * 32u) + (lane & 1u) * 16u, vrd = __umul24(lane >> 2, 288u) + (lane & 3u) * 8u;
    unsigned tab = 0;
#define TB(n, v) asm("v_writelane_b32 %0, %1, %2" : "+v"(tab) : "s"((unsigned)(v)), "n"((int)T_##n))
    const unsigned FJ = (unsigned)WS_F + (F_J + 640u * j) * 4u, mt = 2u * (w & 2u) + (w & 1u), w2 = w < 2u ? w : 2u, hw = head * 147456u;
    const unsigned FR = (unsigned)WS_F + F_R * 4u + (8u * j + sp) * 1024u;
    TB(fo, FJ + (ATT_PEEL_FOW ? 16u * head + 4u * w : 4u * head)); TB(fj, FJ); TB(fa, FR + (w | 2u) * 256u); TB(fas, FR + w * 256u + nt * 4u);
    TB(fb, FR + 128u + w * 256u); TB(fbs, FR + 128u + w * 256u + nt * 4u);
    TB(ep, ssel(b + w, OOR, (unsigned)WS_F + F_EPOCH * 4u));
    const u16* pwo = wo + nt * 144u; const u16* pwb = wob + nt * 144u;
    TB(wol, PLO(pwo)); TB(woh, PHI(pwo) & 0xffffu); TB(wbl, PLO(pwb)); TB(wbh, PHI(pwb) & 0xffffu); TB(outl, PLO(out)); TB(outh, PHI(out) & 0xffffu); TB(r1l, sslt<3>(w, PLO(pwo), PLO(pwb))); TB(r1h, sslt<3>(w, PHI(pwo), PHI(pwb)) & 0xffffu);
    TB(r1n, sslt<3>(w, 72u * sp + 71u, 4095u) * 1152u + 288u); TB(r1s, sslt<3>(w, (72u * sp + 32u * w) * 1152u, hw + 110592u));
    TB(m0, 131072u + 9216u * w2); TB(w0, hw + 36864u * w2); TB(m1, ATT_PEEL ? sslt<3>(w, 41984u + 9216u * w, 32768u) : 24576u + 9216u * w); TB(m1b, ssel(w ^ 2u, sslt<3>(w, 41984u + 9216u * w, 32768u), 114688u - 4096u)); TB(mw, 65536u + 9216u * w); TB(m3, sslt<3>(w, 102400u + 9216u * w, 0u)); TB(m4, sslt<3>(w, 131072u + 9216u * w, 9216u)); TB(ml, (ATT_PEEL ? 18432u : 61440u) + 1024u * w);
    const unsigned tA = (unsigned)WS_AF + (8u * j + mt) * (KS_TOT * 1024u), tB = (unsigned)WS_AF + (120u - 8u * j + mt) * (KS_TOT * 1024u);
#define HK(k) (4u * sp + ((nt + k) & 3u))
    TB(w1, HK(1u) * 147456u + w * 36864u); TB(w2, HK(2u) * 147456u + w * 36864u); TB(w3, HK(3u) * 147456u + w * 36864u);
    TB(A1, tA + 18432u + HK(1u) * 4096u); TB(A2, tA + 18432u + HK(2u) * 4096u); TB(A3, tA + 18432u + HK(3u) * 4096u);
    TB(B1, tB + 18432u + HK(1u) * 4096u); TB(B2, tB + 18432u + HK(2u) * 4096u); TB(B3, tB + 18432u + HK(3u) * 4096u);
    TB(Ao, tA + sp * 2048u); TB(Bo, tB + sp * 2048u); TB(Al, tA + 16384u); TB(Bl, tB + 16384u);
    const unsigned nred = w ^ (sp >> 1), blk = sp & 1u;
    const unsigned xg = (unsigned)WS_X + (4u * j + nt) * 589824u + w * 16384u, oA = (128u * j + 16u * mt) * 1152u + nt * 288u, oB = (1920u - 128u * j + 16u * mt) * 1152u + nt * 288u;
    TB(l8, ssel(nred, OOR, ssel(blk, tB, tA) + 16384u)); TB(xr, xg + sp * 65536u); TB(x8r, ssel(nred, OOR, xg + 524288u + (sp & 1u) * 1024u));
    TB(oA, oA + sp * 32u); TB(oB, oB + sp * 32u); TB(o8, ssel(nred, OOR, ssel(blk, oB, oA) + 256u)); TB(x8, xg + 524288u + sp * 2048u);
    TB(f8, xg + w * 131072u + 14400u + sp * 4u); TB(mk8, ssel(nred, 0u, 0xff0000u)); TB(wt, hw + j * 18432u + w * 4608u);
#define XI(i) TB(x##i, xg + ((sp + 1u + i) & 7u) * 65536u + i * 2048u)
    XI(0); XI(1); XI(2); XI(3); XI(4); XI(5); XI(6);
#undef XI
#undef HK
#undef TB
    vp[0] = (u32x4){vw[0], vw[1], vw[2], vw[3]}; vp[1] = (u32x4){vw[4], vw[5], vw[6], vw[7]}; vp[2] = (u32x4){vw[8], vwl, vrd, tab};
}
__global__ __launch_bounds__(256, 1) __attribute__((aligned(4096))) void fused_kernel(const u16* __restrict__ x, const u16* __restrict__ ckv, const u16* __restrict__ wq, const u16* __restrict__ wuk,
                                                       const u16* __restrict__ wuv, const u16* __restrict__ wqkv, const u16* __restrict__ gw,
                                                       const u16* __restrict__ wo, const u16* __restrict__ wob, char* __restrict__ ws_, u16* __restrict__ out) {
    using namespace att;
    extern __shared__ __attribute__((aligned(16))) char lds[];
    u32x4 sa[5]; u32x2 s24, s28; char* ws; unsigned tdum; kaptr ka = (kaptr)__builtin_amdgcn_kernarg_segment_ptr();
    asm volatile("s_load_dwordx8 s[8:15], %[ka], 0x0\n\ts_load_dwordx8 s[16:23], %[ka], 0x20\n\ts_getpc_b64 s[4:5]\n\tv_lshlrev_b32_e32 v191, 7, %[t]\n\ts_bfe_u32 s6, %[b], 0x20003\n\t"
                 "v_lshl_add_u32 v191, s6, 15, v191\n\tv_min_u32_e32 v191, %[e], v191\n\tglobal_load_dword v191, v191, s[4:5]\n\ts_load_dwordx4 s[24:27], %[ka], 0x40\n\ts_load_dwordx2 s[28:29], %[ka], 0x50"
                 : "={s[8:11]}"(sa[0]), "={s[12:15]}"(sa[1]), "={s[16:19]}"(sa[2]), "={s[20:23]}"(sa[3]), "={s[24:25]}"(s24), "={s[26:27]}"(ws), "={s[28:29]}"(s28), "={v191}"(tdum), [ka] "+{s[0:1]}"(ka)
                 : [t] "v"(threadIdx.x), [b] "s"(blockIdx.x), [e] "i"(CT_END) : "s4", "s5", "s6", "scc");
    SBAR();
    const unsigned wid = rfl(threadIdx.x >> 6);
    unsigned tag, vl;
    const int b = blockIdx.x;
    u32x4 vp[5], srdA; u32x2 sd;
    proj_phase(ka, ws, wid, vp, vl, sa, s24, s28, tdum, srdA, sd, tag
    );
    unsigned* const flags = (unsigned*)(ws + WS_F);
    RTS(2);
#define AST(L) do {} while (0)
#define ATT_TAIL "s_endpgm\n"
    unsigned abad;
    asm volatile(ATT_ASM ATT_TAIL "L_end_%=:\n s_nop 1"
                 : "={s94}"(abad)
                 : "{v[0:3]}"(vp[0]), "{v[4:7]}"(vp[1]), "{v[8:11]}"(vp[2]), "{v[12:15]}"(vp[3]), "{v[16:19]}"(vp[4]), "{v20}"(vl), "{s[56:59]}"(srdA), "{s[60:61]}"(sd), "{s62}"(tag),
                   "{s[8:11]}"(sa[0]), "{s[12:15]}"(sa[1]), "{s[16:19]}"(sa[2]), "{s[20:23]}"(sa[3]), "{s24}"(sa[4][0]), "{s3}"(wid), "{s[28:29]}"(s28)
                 : "memory", "m0", "vcc", "scc", ATT_VCLOB, ATT_SCLOB, ACLOB_LO, ACLOB_HI);
    {
        unsigned b2 = b, w2 = wid;
        asm volatile("s_waitcnt vmcnt(0)" : "+s"(ka), "+s"(b2), "+s"(w2), "+s"(ws) :: "memory");
        const unsigned j2 = b2 >> 5, p2 = w2 & 1u, h2 = (b2 & 7u) * 4u + ((b2 >> 3) & 3u);
#pragma unroll 1
        for (unsigned i = w2 & 2u; i < ((w2 & 2u) ? (abad ? 8u : 4u) : 2u); ++i)
            att_exact(ka[0], ka[2], ws, h2, (i < 4u ? 1920u - 128u * j2 : 128u * j2 - 128u) + 32u * i + 16u * p2, *reinterpret_cast<const float*>(lds + LDS_G + (32u * i + 16u * p2 + (lane_id() & 15u)) * 4u));
        asm volatile("s_waitcnt vmcnt(0)" ::: "memory");
        unsigned* fa = (unsigned*)(ws + WS_F) + F_R + (8u * j2 + (b2 & 7u)) * 256u + (2u + p2) * 64u + ((b2 >> 3) & 3u);
        if (w2 & 2u) st_flag(fa, tag);
        wait_flags(fa, ld_flag(fa), tag);
        const unsigned l2 = lane_id(); u32x4 c[3]; out_consts(c, ka[7], ka[8], ka[10], b2, w2, l2);
        asm volatile("s_waitcnt vmcnt(0) lgkmcnt(0)\n\ts_nop 7\n\ts_branch L_att_stub" :: "{v[0:3]}"(c[0]), "{v[4:7]}"(c[1]), "{v[8:11]}"(c[2]), "{v20}"(l2 * 16u), "{s[56:59]}"(SRDA), "{s60}"(SD(8u * j2, h2, w2)),
                     "{s61}"(SD(120u - 8u * j2, h2, w2)), "{s62}"(tag), "{s3}"(w2) : "memory");
    }
    RTS(5);
}

extern "C" void kernel_launch(void* const* d_in, const int* in_sizes, int n_in, void* d_out, int out_size, void* d_ws, size_t ws_size,
                              hipStream_t stream) {
    (void)hipFuncSetAttribute((const void*)fused_kernel, hipFuncAttributeMaxDynamicSharedMemorySize, att::LDS_BYTES);
    fused_kernel<<<dim3(256), 256, att::LDS_BYTES, stream>>>((const u16*)d_in[0], (const u16*)d_in[4], (const u16*)d_in[5], (const u16*)d_in[6], (const u16*)d_in[7], (const u16*)d_in[1],
                                                             (const u16*)d_in[9], (const u16*)d_in[2], (const u16*)d_in[8], (char*)d_ws, (u16*)d_out);
}
```

```cpp
#include <hip/hip_runtime.h>
#include <hip/hip_bf16.h>
#include <stdint.h>
#include <utility>

typedef short bf16x8 __attribute__((ext_vector_type(8)));
typedef short s16x4 __attribute__((ext_vector_type(4)));
typedef float f32x16 __attribute__((ext_vector_type(16)));
typedef float f32x4 __attribute__((ext_vector_type(4)));
typedef unsigned u32x4 __attribute__((ext_vector_type(4)));
typedef unsigned u32x2 __attribute__((ext_vector_type(2)));
typedef unsigned short u16;

constexpr int S = 2048, DM = 576, NH = 32, HD = 128, DC = 512, NQKV = 1728, DHA = 18;
constexpr int NBIG = NH * HD;
constexpr int KS_TOT = 146;

#define SBAR() __builtin_amdgcn_sched_barrier(0)
constexpr size_t WS_Q = 0, WS_K = WS_Q + (size_t)S * NBIG * 2, WS_V = WS_K + (size_t)S * NBIG * 2, WS_AF = WS_V + (size_t)S * NBIG * 2, WS_X = WS_AF + (size_t)128 * KS_TOT * 1024,
                 WS_P = WS_X + (size_t)64 * 16 * 9216, WS_F = WS_P + (size_t)128 * (4 * 32 * 256 + 1024) * 4;
constexpr size_t WS_L = (size_t)96 << 20;
static_assert(WS_F == 96206848, "workspace map is baked into the generated asm");
constexpr int F_EPOCH = 0, F_J = 1024, F_ST = 8192, F_KV = 40960, F_R = 65536;
constexpr unsigned TAG_STEP = 0x9E3779B1u;
__device__ __forceinline__ unsigned ld_flag(const unsigned* p) { return __hip_atomic_load(p, __ATOMIC_RELAXED, __HIP_MEMORY_SCOPE_AGENT); }
__device__ __forceinline__ void st_flag(unsigned* p, unsigned v) { __hip_atomic_store(p, v, __ATOMIC_RELAXED, __HIP_MEMORY_SCOPE_AGENT); }
__device__ __forceinline__ unsigned ld_flag_async(const unsigned* p) { unsigned v; asm volatile("global_load_dword %0, %1, off sc1" : "=v"(v) : "v"(p) : "memory"); return v; }
#define WAITV(v, N) asm volatile("s_waitcnt vmcnt(" #N ")" : "+v"(v) :: "memory")
__device__ __forceinline__ void wait_flags(const unsigned* p, unsigned v, unsigned tag) {
    if (__builtin_expect(__builtin_amdgcn_ballot_w64(v != tag) != 0ull, 0)) {
        unsigned spins = 0;
        do { __builtin_amdgcn_s_sleep(1); } while (__builtin_amdgcn_ballot_w64(ld_flag(p) != tag) != 0ull && ++spins < (1u << 22));
    }
    asm volatile("" ::: "memory");
}

__device__ __forceinline__ unsigned cvtpk(float lo, float hi) {
    unsigned r; asm volatile("v_cvt_pk_bf16_f32 %0, %1, %2" : "=v"(r) : "v"(lo), "v"(hi)); return r;
}
__device__ __forceinline__ float bf2f(u16 v) { return __uint_as_float(((unsigned)v) << 16); }
__device__ __forceinline__ int crow(int r, int hi) { return (r & 3) + 8 * (r >> 2) + 4 * hi; }

typedef __attribute__((address_space(3))) s16x4* lds_s16x4_p;
__device__ __forceinline__ s16x4 trrd(unsigned addr) { return __builtin_amdgcn_ds_read_tr16_b64_v4i16((lds_s16x4_p)addr); }

__device__ __forceinline__ unsigned lane_id() { unsigned l; asm volatile("v_mbcnt_lo_u32_b32 %0, -1, 0\n\tv_mbcnt_hi_u32_b32 %0, -1, %0" : "=v"(l)); return l; }
#define RTS(slot) do {} while (0)
#define CT_END 0x1f300
#define ACLOB_LO "a0","a1","a2","a3","a4","a5","a6","a7","a8","a9","a10","a11","a12","a13","a14","a15","a16","a17","a18","a19","a20","a21","a22","a23","a24","a25","a26","a27","a28","a29","a30","a31","a32","a33","a34","a35","a36","a37","a38","a39","a40","a41","a42","a43","a44","a45","a46","a47","a48","a49","a50","a51","a52","a53","a54","a55","a56","a57","a58","a59","a60","a61","a62","a63","a64","a65","a66","a67","a68","a69","a70","a71","a72","a73","a74","a75","a76","a77","a78","a79","a80","a81","a82","a83","a84","a85","a86","a87","a88","a89","a90","a91","a92","a93","a94","a95","a96","a97","a98","a99","a100","a101","a102","a103","a104","a105","a106","a107","a108","a109","a110","a111","a112","a113","a114","a115","a116","a117","a118","a119","a120","a121","a122","a123","a124","a125","a126","a127"
#define ACLOB_HI "a128","a129","a130","a131","a132","a133","a134","a135","a136","a137","a138","a139","a140","a141","a142","a143","a144","a145","a146","a147","a148","a149","a150","a151","a152","a153","a154","a155","a156","a157","a158","a159","a160","a161","a162","a163","a164","a165","a166","a167","a168","a169","a170","a171","a172","a173","a174","a175","a176","a177","a178","a179","a180","a181","a182","a183","a184","a185","a186","a187","a188","a189","a190","a191","a192","a193","a194","a195","a196","a197","a198","a199","a200","a201","a202","a203","a204","a205","a206","a207","a208","a209","a210","a211","a212","a213","a214","a215","a216","a217","a218","a219","a220","a221","a222","a223","a224","a225","a226","a227","a228","a229","a230","a231","a232","a233","a234","a235","a236","a237","a238","a239","a240","a241","a242","a243","a244","a245","a246","a247","a248","a249","a250","a251","a252","a253","a254","a255"
#define PLO(p) ((unsigned)(size_t)(p))
#define PHI(p) ((unsigned)((size_t)(p) >> 32))
__device__ __forceinline__ unsigned vsel(bool c, unsigned a, unsigned b) { asm("" : "+v"(a), "+v"(b)); return c ? a : b; }
__device__ __forceinline__ unsigned ssel(unsigned c, unsigned a, unsigned b) { unsigned r; asm("s_cmp_lg_u32 %1, 0\n\ts_cselect_b32 %0, %2, %3" : "=s"(r) : "s"(c), "s"(a), "s"(b) : "scc"); return r; }
template <int N> __device__ __forceinline__ unsigned sslt(unsigned c, unsigned a, unsigned b) { unsigned r; asm("s_cmpk_lt_u32 %1, %4\n\ts_cselect_b32 %0, %2, %3" : "=s"(r) : "s"(c), "s"(a), "s"(b), "n"(N) : "scc"); return r; }
namespace att {
constexpr int LDS_BYTES = 163840;
constexpr unsigned LDS_G = 159744u, LDS_L = 161792u;
}
#define SD(J8, H, W) ((unsigned)WS_AF + (((J8) + 2u * ((W) & 2u) + ((W) & 1u)) * KS_TOT + 18u + 4u * (H)) * 1024u)
#define SRDA (u32x4){PLO(ws), PHI(ws) & 0xffffu, (unsigned)WS_L, 0x00020000u}
#define PROJ_ASM \
".p2align 3\n" \
"L_pb:\n" \
"s_waitcnt lgkmcnt(0)\n" \
"s_mov_b32 s64, s10\n" \
"s_and_b32 s65, s11, 0xffff\n" \
"s_mov_b32 s66, 0x200000\n" \
"s_mov_b32 s67, 0x20000\n" \
"s_mov_b32 s68, s14\n" \
"s_and_b32 s69, s15, 0xffff\n" \
"s_mov_b32 s70, 0x400000\n" \
"s_mov_b32 s71, 0x20000\n" \
"s_mov_b32 s72, s16\n" \
"s_and_b32 s73, s17, 0xffff\n" \
"s_mov_b32 s74, 0x400000\n" \
"s_mov_b32 s75, 0x20000\n" \
"s_add_u32 m0, s30, 0x0\n" \
"s_movk_i32 s84, 0x0\n" \
"buffer_load_dwordx4 v22, s[64:67], s84 offen offset:0 lds\n" \
"s_movk_i32 s84, 0x1c00\n" \
"buffer_load_dwordx4 v23, s[64:67], s84 offen offset:1024 lds\n" \
"s_movk_i32 s84, 0x3800\n" \
"buffer_load_dwordx4 v22, s[64:67], s84 offen offset:2048 lds\n" \
"s_movk_i32 s84, 0x5400\n" \
"buffer_load_dwordx4 v23, s[64:67], s84 offen offset:3072 lds\n" \
"s_add_u32 m0, s31, 0x0\n" \
"s_movk_i32 s84, 0x0\n" \
"buffer_load_dwordx4 v26, s[68:71], s84 offen offset:0 lds\n" \
"s_movk_i32 s84, 0x7c00\n" \
"buffer_load_dwordx4 v26, s[68:71], s84 offen offset:1024 lds\n" \
"s_addk_i32 s84, 0x7c00\n" \
"buffer_load_dwordx4 v27, s[68:71], s84 offen offset:2048 lds\n" \
"s_addk_i32 s84, 0x7c00\n" \
"buffer_load_dwordx4 v27, s[68:71], s84 offen offset:3072 lds\n" \
"s_movk_i32 s7, 0x300\n" \
"s_lshr_b32 s4, s2, 1\n" \
"s_and_b32 s4, s4, 60\n" \
"s_or_b32 s4, s4, s3\n" \
"v_lshrrev_b32_e32 v212, 4, v20\n" \
"v_min_u32_e32 v212, 59, v212\n" \
"s_cmpk_lt_u32 s2, 0x80\n" \
"s_cselect_b32 s6, s14, s16\n" \
"v_subrev_u32_e32 v214, 48, v212\n" \
"v_mov_b32_e32 v212, s10\n" \
"v_mov_b32_e32 v215, s6\n" \
"s_cmpk_lt_u32 s2, 0x80\n" \
"s_cselect_b32 s5, s15, s17\n" \
"v_cmp_gt_u32_e32 vcc, s7, v20\n" \
"v_mov_b32_e64 v213, s11\n" \
"v_bfe_u32 v216, v20, 4, 3\n" \
"v_cndmask_b32_e32 v212, v212, v215, vcc\n" \
"v_mov_b32_e32 v215, s5\n" \
"s_lshl_b32 s5, s3, 4\n" \
"v_cndmask_b32_e32 v213, v213, v215, vcc\n" \
"v_lshlrev_b32_e64 v215, 5, v20\n" \
"v_and_b32_e32 v215, 0x7000, v215\n" \
"v_lshl_add_u32 v215, s4, 6, v215\n" \
"s_lshl_b32 s4, s2, 3\n" \
"s_and_b32 s4, s4, 56\n" \
"v_or3_b32 v215, v215, s4, v216\n" \
"s_lshl_b32 s4, s2, 6\n" \
"s_or_b32 s6, s5, s4\n" \
"v_cmp_gt_u32_e64 s[4:5], 6, v214\n" \
"v_add_u32_e32 v215, 0x2000, v215\n" \
"s_nop 0\n" \
"v_cndmask_b32_e64 v216, 4, 2, s[4:5]\n" \
"v_add3_u32 v214, s6, v214, v216\n" \
"s_lshl_b32 s4, s3, 9\n" \
"v_cndmask_b32_e32 v214, v214, v215, vcc\n" \
"v_lshlrev_b32_e32 v214, 7, v214\n" \
"v_mov_b32_e64 v215, 0\n" \
"v_lshl_add_u64 v[184:185], v[212:213], 0, v[214:215]\n" \
"s_or_b32 s32, s4, 0x25000\n" \
"s_add_u32 m0, s30, 0x1000\n" \
"s_mov_b32 s84, 0x8000\n" \
"buffer_load_dwordx4 v22, s[64:67], s84 offen offset:0 lds\n" \
"s_and_b32 s33, s3, 1\n" \
"v_lshrrev_b32_e32 v212, 4, v20\n" \
"v_lshrrev_b32_e64 v214, 8, v20\n" \
"v_bfe_u32 v212, v212, 1, 3\n" \
"v_lshlrev_b32_e32 v215, 2, v20\n" \
"s_addk_i32 s84, 0x1c00\n" \
"buffer_load_dwordx4 v23, s[64:67], s84 offen offset:1024 lds\n" \
"v_lshrrev_b32_e64 v216, 1, v20\n" \
"v_bfe_u32 v213, v20, 4, 4\n" \
"s_lshl_b32 s4, s3, 5\n" \
"v_xor_b32_e32 v212, v212, v214\n" \
"v_lshlrev_b32_e32 v214, 11, v214\n" \
"s_addk_i32 s84, 0x1c00\n" \
"buffer_load_dwordx4 v22, s[64:67], s84 offen offset:2048 lds\n" \
"v_and_b32_e32 v215, 0x300, v215\n" \
"v_and_b32_e64 v217, 24, v216\n" \
"v_and_or_b32 v213, s4, 64, v213\n" \
"v_or3_b32 v214, v215, v214, v217\n" \
"s_lshl_b32 s4, s33, 14\n" \
"s_addk_i32 s84, 0x1c00\n" \
"buffer_load_dwordx4 v23, s[64:67], s84 offen offset:3072 lds\n" \
"v_and_b32_e32 v215, 0xe0, v216\n" \
"v_lshlrev_b32_e32 v213, 7, v213\n" \
"v_lshlrev_b32_e32 v212, 4, v212\n" \
"v_or3_b32 v214, v214, s4, v215\n" \
"v_or_b32_e64 v28, v212, v213\n" \
"s_add_u32 m0, s31, 0x4000\n" \
"s_movk_i32 s84, 0x0\n" \
"buffer_load_dwordx4 v26, s[72:75], s84 offen offset:0 lds\n" \
"v_add_u32_e32 v32, 0x10000, v214\n" \
"v_bitop3_b32 v29, v212, 64, v213 bitop3:0x36\n" \
"v_xor_b32_e32 v33, 0x20, v32\n" \
"v_xor_b32_e64 v34, 0x40, v32\n" \
"v_xor_b32_e32 v35, 0x60, v32\n" \
"s_movk_i32 s84, 0x7c00\n" \
"buffer_load_dwordx4 v26, s[72:75], s84 offen offset:1024 lds\n" \
"v_xor_b32_e32 v36, 0x80, v32\n" \
"v_xor_b32_e32 v37, 0xa0, v32\n" \
"v_xor_b32_e32 v38, 0xc0, v32\n" \
"v_xor_b32_e32 v39, 0xe0, v32\n" \
"s_mov_b32 s80, s26\n" \
"s_addk_i32 s84, 0x7c00\n" \
"buffer_load_dwordx4 v27, s[72:75], s84 offen offset:2048 lds\n" \
"s_and_b32 s81, s27, 0xffff\n" \
"s_mov_b32 s82, 0x10000000\n" \
"s_mov_b32 s83, 0x20000\n" \
"s_mov_b32 s76, s18\n" \
"s_and_b32 s77, s19, 0xffff\n" \
"s_addk_i32 s84, 0x7c00\n" \
"buffer_load_dwordx4 v27, s[72:75], s84 offen offset:3072 lds\n" \
"s_mov_b32 s78, 0x1e6000\n" \
"s_mov_b32 s79, 0x20000\n" \
"s_branch L_pgo_%=\n" \
"L_pret:\n" \
"s_setpc_b64 s[88:89]\n" \
"L_pgo_%=:\n" \
"s_mul_i32 s88, s33, 0xa000\n" \
"s_lshl_b32 s89, s3, 11\n" \
"s_and_b32 s89, s89, 0x1000\n" \
"s_add_u32 s88, s88, s89\n" \
"s_getpc_b64 s[86:87]\n" \
"L_hpa_%=:\n" \
"s_add_u32 s86, s86, L_pp1-L_hpa_%=\n" \
"s_addc_u32 s87, s87, 0\n" \
"s_add_u32 s86, s86, s88\n" \
"s_addc_u32 s87, s87, 0\n" \
"s_add_u32 m0, s30, 0x8000\n" \
"s_movk_i32 s84, 0x80\n" \
"buffer_load_dwordx4 v22, s[64:67], s84 offen offset:0 lds\n" \
"s_lshr_b32 s4, s2, 1\n" \
"s_and_b32 s4, s4, 0x7c\n" \
"v_bfe_u32 v212, v20, 4, 5\n" \
"s_movk_i32 s84, 0x1c80\n" \
"buffer_load_dwordx4 v23, s[64:67], s84 offen offset:1024 lds\n" \
"s_or_b32 s4, s4, s3\n" \
"v_min_u32_e32 v212, 26, v212\n" \
"v_mad_u64_u32 v[212:213], s[4:5], s4, 27, v[212:213]\n" \
"s_movk_i32 s84, 0x3880\n" \
"buffer_load_dwordx4 v22, s[64:67], s84 offen offset:2048 lds\n" \
"v_mul_u32_u24_e32 v213, 0xaaab, v212\n" \
"v_lshrrev_b32_e32 v213, 18, v213\n" \
"s_mov_b32 s4, 0x1fffffa\n" \
"s_movk_i32 s84, 0x5480\n" \
"buffer_load_dwordx4 v23, s[64:67], s84 offen offset:3072 lds\n" \
"v_mad_u64_u32 v[214:215], s[4:5], v213, s4, v[212:213]\n" \
"s_and_b32 s6, s2, 7\n" \
"v_mul_u32_u24_e32 v213, 27, v213\n" \
"s_mov_b64 exec, 0\n" \
"s_swappc_b64 s[88:89], s[86:87]\n" \
"s_mov_b64 exec, -1\n" \
"s_add_u32 s86, s86, 0x2000\n" \
"s_addc_u32 s87, s87, 0\n" \
"s_add_u32 m0, s31, 0x8000\n" \
"s_mov_b32 s84, 0x80000\n" \
"buffer_load_dwordx4 v26, s[68:71], s84 offen offset:0 lds\n" \
"v_lshrrev_b32_e32 v214, 1, v214\n" \
"v_and_b32_e32 v212, 1, v212\n" \
"v_mul_u32_u24_e32 v214, 9, v214\n" \
"s_addk_i32 s84, 0x7c00\n" \
"buffer_load_dwordx4 v26, s[68:71], s84 offen offset:1024 lds\n" \
"v_add3_u32 v212, v212, s6, v213\n" \
"v_add_lshl_u32 v212, v212, v214, 7\n" \
"v_mov_b32_e32 v213, 0\n" \
"s_mov_b64 exec, 0\n" \
"s_swappc_b64 s[88:89], s[86:87]\n" \
"s_mov_b64 exec, -1\n" \
"s_add_u32 s86, s86, 0x2000\n" \
"s_addc_u32 s87, s87, 0\n" \
"s_addk_i32 s84, 0x7c00\n" \
"buffer_load_dwordx4 v27, s[68:71], s84 offen offset:2048 lds\n" \
"v_lshl_add_u64 v[186:187], s[18:19], 0, v[212:213]\n" \
"s_lshl_b32 s35, s2, 10\n" \
"s_lshr_b32 s37, s2, 1\n" \
"s_addk_i32 s84, 0x7c00\n" \
"buffer_load_dwordx4 v27, s[68:71], s84 offen offset:3072 lds\n" \
"s_mul_i32 s37, s37, 0x24800\n" \
"s_and_b32 s35, s35, 0x400\n" \
"s_lshl_b32 s36, s3, 8\n" \
"s_mov_b64 exec, 0\n" \
"s_swappc_b64 s[88:89], s[86:87]\n" \
"s_mov_b64 exec, -1\n" \
"s_add_u32 s86, s86, 0x2000\n" \
"s_addc_u32 s87, s87, 0\n" \
"s_add_u32 m0, s30, 0x9000\n" \
"s_mov_b32 s84, 0x8080\n" \
"buffer_load_dwordx4 v22, s[64:67], s84 offen offset:0 lds\n" \
"s_or_b32 s35, s37, s35\n" \
"s_and_b32 s4, s2, 7\n" \
"s_lshr_b32 s6, s2, 5\n" \
"s_addk_i32 s84, 0x1c00\n" \
"buffer_load_dwordx4 v23, s[64:67], s84 offen offset:1024 lds\n" \
"s_lshl_b32 s34, s2, 5\n" \
"s_add_i32 s35, s35, s36\n" \
"s_lshl_b32 s7, s4, 10\n" \
"s_addk_i32 s84, 0x1c00\n" \
"buffer_load_dwordx4 v22, s[64:67], s84 offen offset:2048 lds\n" \
"s_and_b32 s34, s34, 0x300\n" \
"s_add_i32 s35, s35, 0x3004000\n" \
"s_lshl_b32 s37, s6, 13\n" \
"s_mov_b64 exec, 0\n" \
"s_swappc_b64 s[88:89], s[86:87]\n" \
"s_mov_b64 exec, -1\n" \
"s_andn2_b32 s85, 2, s3\n" \
"s_lshl_b32 s85, s85, 12\n" \
"s_add_u32 s86, s86, s85\n" \
"s_addc_u32 s87, s87, 0\n" \
"s_addk_i32 s84, 0x1c00\n" \
"buffer_load_dwordx4 v23, s[64:67], s84 offen offset:3072 lds\n" \
"s_or_b32 s34, s7, s34\n" \
"s_or_b32 s7, s37, s7\n" \
"v_mov_b32_e64 v213, s35\n" \
"s_add_u32 m0, s31, 0xc000\n" \
"s_mov_b32 s84, 0x80000\n" \
"buffer_load_dwordx4 v26, s[72:75], s84 offen offset:0 lds\n" \
"s_or_b32 s7, s7, s36\n" \
"s_lshr_b32 s5, s2, 3\n" \
"s_or_b32 s7, s7, 0x5c00000\n" \
"s_mov_b64 exec, 0\n" \
"s_swappc_b64 s[88:89], s[86:87]\n" \
"s_mov_b64 exec, -1\n" \
"s_addk_i32 s84, 0x7c00\n" \
"buffer_load_dwordx4 v26, s[72:75], s84 offen offset:1024 lds\n" \
"v_writelane_b32 v213, s7, 2\n" \
"s_mul_i32 s5, s5, 0x90000\n" \
"v_writelane_b32 v213, s7, 3\n" \
"s_addk_i32 s84, 0x7c00\n" \
"buffer_load_dwordx4 v27, s[72:75], s84 offen offset:2048 lds\n" \
"s_lshl_b32 s7, s3, 14\n" \
"s_lshl_b32 s4, s4, 16\n" \
"s_add_i32 s5, s5, s7\n" \
"s_addk_i32 s84, 0x7c00\n" \
"buffer_load_dwordx4 v27, s[72:75], s84 offen offset:3072 lds\n" \
"s_or_b32 s34, s34, 0x5be8000\n" \
"v_writelane_b32 v213, s34, 4\n" \
"s_add_i32 s4, s5, s4\n" \
"v_writelane_b32 v213, s34, 5\n" \
"s_add_i32 s4, s4, 0x4243800\n" \
"v_lshlrev_b32_e64 v212, 3, v20\n" \
"v_and_b32_e32 v212, 0x80, v212\n" \
"v_writelane_b32 v213, s4, 6\n" \
"s_mul_i32 s4, s6, 0xa00\n" \
"s_add_i32 s4, s4, 0x5bc0f80\n" \
"v_writelane_b32 v213, s4, 7\n" \
"v_add_u32_e32 v49, v213, v212\n" \
"s_mov_b32 m0, s32\n" \
"s_mov_b32 exec_lo, 0xffffff\n" \
"s_mov_b32 exec_hi, 0xfff0000\n" \
"global_load_lds_dword v[184:185], off\n" \
"s_mov_b64 exec, -1\n" \
"s_mov_b32 m0, s32\n" \
"s_mov_b64 exec, 0xff\n" \
"buffer_load_dword v49, s[80:83], 0 offen lds\n" \
"s_mov_b64 exec, -1\n" \
"s_mov_b32 s84, 0x5bc0000\n" \
"buffer_load_dword v180, off, s[80:83], s84 sc1\n" \
"s_cmp_lg_u32 s33, 0\n" \
"s_cbranch_scc1 L_w1_%=\n" \
"L_w0_%=:\n" \
"s_waitcnt vmcnt(19)\n" \
"s_barrier\n" \
"s_nop 0\n" \
"ds_read_b64_tr_b16 a[160:161], v32 offset:0\n" \
"ds_read_b64_tr_b16 a[162:163], v32 offset:1024\n" \
"ds_read_b128 a[128:131], v28 offset:16384\n" \
"ds_read_b64_tr_b16 a[224:225], v32 offset:8192\n" \
"ds_read_b64_tr_b16 a[226:227], v32 offset:9216\n" \
"ds_read_b128 a[192:195], v29 offset:16384\n" \
"ds_read_b128 a[132:135], v28 offset:18432\n" \
"ds_read_b128 a[196:199], v29 offset:18432\n" \
"ds_read_b64_tr_b16 a[164:165], v33 offset:0\n" \
"ds_read_b64_tr_b16 a[166:167], v33 offset:1024\n" \
"ds_read_b64_tr_b16 a[228:229], v33 offset:8192\n" \
"ds_read_b64_tr_b16 a[230:231], v33 offset:9216\n" \
"ds_read_b128 a[136:139], v28 offset:20480\n" \
"ds_read_b128 a[200:203], v29 offset:20480\n" \
"s_waitcnt lgkmcnt(11)\n" \
"v_mfma_f32_16x16x32_bf16 a[0:3], a[160:163], a[128:131], 0\n" \
"ds_read_b64_tr_b16 a[168:169], v34 offset:0\n" \
"s_lshr_b32 s4, s2, 1\n" \
"s_waitcnt lgkmcnt(9)\n" \
"v_mfma_f32_16x16x32_bf16 a[0:3], a[224:227], a[192:195], a[0:3]\n" \
"ds_read_b64_tr_b16 a[170:171], v34 offset:1024\n" \
"s_and_b32 s4, s4, 0x7c\n" \
"s_waitcnt lgkmcnt(9)\n" \
"v_mfma_f32_16x16x32_bf16 a[16:19], a[160:163], a[132:135], 0\n" \
"ds_read_b64_tr_b16 a[232:233], v34 offset:8192\n" \
"v_mov_b32_e32 v213, s13\n" \
"s_waitcnt lgkmcnt(9)\n" \
"v_mfma_f32_16x16x32_bf16 a[16:19], a[224:227], a[196:199], a[16:19]\n" \
"ds_read_b64_tr_b16 a[234:235], v34 offset:9216\n" \
"s_or_b32 s4, s4, s3\n" \
"s_waitcnt lgkmcnt(8)\n" \
"v_mfma_f32_16x16x32_bf16 a[20:23], a[164:167], a[132:135], 0\n" \
"ds_read_b128 a[140:143], v28 offset:22528\n" \
"v_lshrrev_b32_e32 v212, 4, v20\n" \
"s_waitcnt lgkmcnt(7)\n" \
"v_mfma_f32_16x16x32_bf16 a[20:23], a[228:231], a[196:199], a[20:23]\n" \
"ds_read_b128 a[204:207], v29 offset:22528\n" \
"v_mad_u64_u32 v[214:215], s[4:5], s4, 36, v[212:213]\n" \
"v_mfma_f32_16x16x32_bf16 a[4:7], a[164:167], a[128:131], 0\n" \
"ds_read_b64_tr_b16 a[172:173], v35 offset:0\n" \
"v_mov_b32_e64 v217, s9\n" \
"v_mfma_f32_16x16x32_bf16 a[4:7], a[228:231], a[192:195], a[4:7]\n" \
"ds_read_b64_tr_b16 a[174:175], v35 offset:1024\n" \
"s_movk_i32 s4, 0x240\n" \
"s_waitcnt lgkmcnt(9)\n" \
"v_mfma_f32_16x16x32_bf16 a[32:35], a[160:163], a[136:139], 0\n" \
"ds_read_b64_tr_b16 a[236:237], v35 offset:8192\n" \
"v_cmp_gt_u32_e32 vcc, s4, v20\n" \
"s_waitcnt lgkmcnt(9)\n" \
"v_mfma_f32_16x16x32_bf16 a[32:35], a[224:227], a[200:203], a[32:35]\n" \
"ds_read_b64_tr_b16 a[238:239], v35 offset:9216\n" \
"s_lshl_b32 s4, s2, 3\n" \
"v_mfma_f32_16x16x32_bf16 a[36:39], a[164:167], a[136:139], 0\n" \
"ds_read_b128 a[144:147], v28 offset:0\n" \
"s_and_b32 s4, s4, 56\n" \
"v_mfma_f32_16x16x32_bf16 a[36:39], a[228:231], a[200:203], a[36:39]\n" \
"ds_read_b128 a[208:211], v29 offset:0\n" \
"v_cndmask_b32_e32 v217, v217, v213, vcc\n" \
"s_waitcnt lgkmcnt(10)\n" \
"v_mfma_f32_16x16x32_bf16 a[40:43], a[168:171], a[136:139], 0\n" \
"ds_read_b128 a[148:151], v28 offset:2048\n" \
"v_lshlrev_b32_e32 v213, 3, v214\n" \
"s_waitcnt lgkmcnt(9)\n" \
"v_mfma_f32_16x16x32_bf16 a[40:43], a[232:235], a[200:203], a[40:43]\n" \
"ds_read_b128 a[212:215], v29 offset:2048\n" \
"v_and_b32_e32 v213, 0x1ffc0, v213\n" \
"v_mfma_f32_16x16x32_bf16 a[24:27], a[168:171], a[132:135], 0\n" \
"ds_read_b128 a[152:155], v28 offset:4096\n" \
"v_and_b32_e64 v214, 7, v214\n" \
"v_mfma_f32_16x16x32_bf16 a[24:27], a[232:235], a[196:199], a[24:27]\n" \
"ds_read_b128 a[216:219], v29 offset:4096\n" \
"v_or3_b32 v213, v214, s4, v213\n" \
"v_mfma_f32_16x16x32_bf16 a[8:11], a[168:171], a[128:131], 0\n" \
"ds_read_b128 a[156:159], v28 offset:6144\n" \
"s_lshl_b32 s4, s2, 2\n" \
"v_mfma_f32_16x16x32_bf16 a[8:11], a[232:235], a[192:195], a[8:11]\n" \
"ds_read_b128 a[220:223], v29 offset:6144\n" \
"s_mov_b32 m0, s32\n" \
"s_waitcnt lgkmcnt(13)\n" \
"v_mfma_f32_16x16x32_bf16 a[48:51], a[160:163], a[140:143], 0\n" \
"s_mov_b32 exec_lo, 0xff000000\n" \
"s_mov_b32 exec_hi, 0x0\n" \
"global_load_lds_dword v[184:185], off\n" \
"s_mov_b64 exec, -1\n" \
"s_waitcnt lgkmcnt(12)\n" \
"v_mfma_f32_16x16x32_bf16 a[48:51], a[224:227], a[204:207], a[48:51]\n" \
"ds_read_b64_tr_b16 a[176:177], v36 offset:0\n" \
"s_or_b32 s4, s4, s3\n" \
"v_mfma_f32_16x16x32_bf16 a[52:55], a[164:167], a[140:143], 0\n" \
"ds_read_b64_tr_b16 a[178:179], v36 offset:1024\n" \
"s_mul_i32 s4, s4, 18\n" \
"v_mfma_f32_16x16x32_bf16 a[52:55], a[228:231], a[204:207], a[52:55]\n" \
"s_mov_b32 m0, s32\n" \
"ds_read_b64_tr_b16 a[240:241], v36 offset:8192\n" \
"v_mfma_f32_16x16x32_bf16 a[56:59], a[168:171], a[140:143], 0\n" \
"s_mov_b32 exec_lo, 0xffff\n" \
"s_mov_b32 exec_hi, 0x0\n" \
"global_load_lds_dword v[186:187], off\n" \
"s_mov_b64 exec, -1\n" \
"v_mfma_f32_16x16x32_bf16 a[56:59], a[232:235], a[204:207], a[56:59]\n" \
"ds_read_b64_tr_b16 a[242:243], v36 offset:9216\n" \
"v_min_u32_e32 v212, 53, v212\n" \
"s_waitcnt lgkmcnt(14)\n" \
"v_mfma_f32_16x16x32_bf16 a[60:63], a[172:175], a[140:143], 0\n" \
"ds_read_b64_tr_b16 a[180:181], v37 offset:0\n" \
"s_sub_i32 s4, s4, 36\n" \
"s_waitcnt lgkmcnt(13)\n" \
"v_mfma_f32_16x16x32_bf16 a[60:63], a[236:239], a[204:207], a[60:63]\n" \
"ds_read_b64_tr_b16 a[182:183], v37 offset:1024\n" \
"v_add_u32_e32 v212, s4, v212\n" \
"v_mfma_f32_16x16x32_bf16 a[44:47], a[172:175], a[136:139], 0\n" \
"ds_read_b64_tr_b16 a[244:245], v37 offset:8192\n" \
"v_mov_b32_e64 v215, s12\n" \
"v_mfma_f32_16x16x32_bf16 a[44:47], a[236:239], a[200:203], a[44:47]\n" \
"ds_read_b64_tr_b16 a[246:247], v37 offset:9216\n" \
"v_mov_b32_e64 v216, s8\n" \
"v_mfma_f32_16x16x32_bf16 a[28:31], a[172:175], a[132:135], 0\n" \
"ds_read_b64_tr_b16 a[184:185], v38 offset:0\n" \
"v_cndmask_b32_e64 v212, v212, v213, vcc\n" \
"v_mfma_f32_16x16x32_bf16 a[28:31], a[236:239], a[196:199], a[28:31]\n" \
"ds_read_b64_tr_b16 a[186:187], v38 offset:1024\n" \
"v_cndmask_b32_e64 v216, v216, v215, vcc\n" \
"v_mfma_f32_16x16x32_bf16 a[12:15], a[172:175], a[128:131], 0\n" \
"v_lshlrev_b32_e32 v212, 7, v212\n" \
"v_mov_b32_e32 v213, 0\n" \
"v_mfma_f32_16x16x32_bf16 a[12:15], a[236:239], a[192:195], a[12:15]\n" \
"v_lshl_add_u64 v[188:189], v[216:217], 0, v[212:213]\n" \
"s_movk_i32 s5, 0xd80\n" \
"s_waitcnt lgkmcnt(15)\n" \
"v_mfma_f32_16x16x32_bf16 a[76:79], a[172:175], a[144:147], 0\n" \
"ds_read_b64_tr_b16 a[248:249], v38 offset:8192\n" \
"s_lshr_b32 s6, s2, 5\n" \
"v_mfma_f32_16x16x32_bf16 a[76:79], a[236:239], a[208:211], a[76:79]\n" \
"v_lshrrev_b32_e32 v212, 4, v20\n" \
"v_lshl_or_b32 v215, s3, 6, v212\n" \
"v_mfma_f32_16x16x32_bf16 a[72:75], a[168:171], a[144:147], 0\n" \
"v_mul_u32_u24_e32 v216, 0x2aab, v215\n" \
"v_lshrrev_b32_e64 v216, 16, v216\n" \
"v_mfma_f32_16x16x32_bf16 a[72:75], a[232:235], a[208:211], a[72:75]\n" \
"ds_read_b64_tr_b16 a[250:251], v38 offset:9216\n" \
"v_mad_i32_i24 v217, v216, -6, v215\n" \
"v_mfma_f32_16x16x32_bf16 a[68:71], a[164:167], a[144:147], 0\n" \
"v_add_u32_e32 v218, 0xfffffe50, v215\n" \
"s_mul_i32 s35, s6, 0x48\n" \
"v_mfma_f32_16x16x32_bf16 a[68:71], a[228:231], a[208:211], a[68:71]\n" \
"v_lshrrev_b32_e32 v219, 1, v217\n" \
"v_add_u32_e32 v218, s35, v216\n" \
"v_mfma_f32_16x16x32_bf16 a[64:67], a[160:163], a[144:147], 0\n" \
"v_mul_u32_u24_e32 v219, 0x480, v219\n" \
"v_and_b32_e64 v214, 16, v20\n" \
"v_mfma_f32_16x16x32_bf16 a[64:67], a[224:227], a[208:211], a[64:67]\n" \
"ds_read_b64_tr_b16 a[188:189], v39 offset:0\n" \
"v_mad_u32_u24 v219, v218, s5, v219\n" \
"v_mfma_f32_16x16x32_bf16 a[80:83], a[160:163], a[148:151], 0\n" \
"v_or_b32_e32 v192, v219, v214\n" \
"v_mov_b32_e32 v220, 2.0\n" \
"s_waitcnt lgkmcnt(15)\n" \
"v_mfma_f32_16x16x32_bf16 a[80:83], a[224:227], a[212:215], a[80:83]\n" \
"v_mov_b32_e32 v222, 7\n" \
"v_lshlrev_b32_e64 v220, 7, v218\n" \
"v_mfma_f32_16x16x32_bf16 a[84:87], a[164:167], a[148:151], 0\n" \
"ds_read_b64_tr_b16 a[190:191], v39 offset:1024\n" \
"v_lshrrev_b32_e64 v218, 1, v218\n" \
"v_mfma_f32_16x16x32_bf16 a[84:87], a[228:231], a[212:215], a[84:87]\n" \
"v_and_b32_e32 v218, 4, v218\n" \
"v_and_b32_e32 v216, 2, v216\n" \
"v_mfma_f32_16x16x32_bf16 a[88:91], a[168:171], a[148:151], 0\n" \
"s_mul_i32 s7, s6, 0x2400\n" \
"s_sub_i32 s34, 0x22000, s7\n" \
"v_mfma_f32_16x16x32_bf16 a[88:91], a[232:235], a[212:215], a[88:91]\n" \
"ds_read_b64_tr_b16 a[252:253], v39 offset:8192\n" \
"v_bitop3_b32 v216, v217, v218, v216 bitop3:0x1e\n" \
"v_mfma_f32_16x16x32_bf16 a[92:95], a[172:175], a[148:151], 0\n" \
"v_or_b32_e32 v213, 0x24400, v20\n" \
"v_lshlrev_b32_e64 v216, 4, v216\n" \
"v_mfma_f32_16x16x32_bf16 a[92:95], a[236:239], a[212:215], a[92:95]\n" \
"v_add3_u32 v196, v220, s34, v216\n" \
"v_mov_b32_e64 v216, v213\n" \
"v_mfma_f32_16x16x32_bf16 a[108:111], a[172:175], a[152:155], 0\n" \
"ds_read_b64_tr_b16 a[254:255], v39 offset:9216\n" \
"s_movk_i32 s36, 0x1b0\n" \
"v_mfma_f32_16x16x32_bf16 a[108:111], a[236:239], a[216:219], a[108:111]\n" \
"v_or_b32_e32 v216, 0x100, v215\n" \
"v_mul_u32_u24_e32 v217, 0x2aab, v216\n" \
"v_mfma_f32_16x16x32_bf16 a[104:107], a[168:171], a[152:155], 0\n" \
"v_add_u32_e32 v215, 0xffffff50, v215\n" \
"v_lshrrev_b32_e32 v217, 16, v217\n" \
"v_mfma_f32_16x16x32_bf16 a[104:107], a[232:235], a[216:219], a[104:107]\n" \
"v_mov_b32_e64 v220, v215\n" \
"v_mad_i32_i24 v218, v217, -6, v216\n" \
"v_mfma_f32_16x16x32_bf16 a[100:103], a[164:167], a[152:155], 0\n" \
"v_cmp_gt_u32_e32 vcc, s36, v216\n" \
"v_mov_b32_e32 v221, 7\n" \
"v_mfma_f32_16x16x32_bf16 a[100:103], a[228:231], a[216:219], a[100:103]\n" \
"v_lshrrev_b32_e32 v222, 1, v218\n" \
"v_cndmask_b32_e32 v217, v220, v217, vcc\n" \
"v_mfma_f32_16x16x32_bf16 a[96:99], a[160:163], a[152:155], 0\n" \
"s_movk_i32 s4, 0x48\n" \
"v_add_u32_e32 v220, s35, v217\n" \
"v_mfma_f32_16x16x32_bf16 a[96:99], a[224:227], a[216:219], a[96:99]\n" \
"v_mul_u32_u24_e32 v222, 0x480, v222\n" \
"v_mad_u32_u24 v222, v220, s5, v222\n" \
"s_waitcnt lgkmcnt(15)\n" \
"v_mfma_f32_16x16x32_bf16 a[112:115], a[160:163], a[156:159], 0\n" \
"v_cmp_gt_u32_e64 s[4:5], s4, v215\n" \
"v_cndmask_b32_e32 v215, v221, v218, vcc\n" \
"v_mfma_f32_16x16x32_bf16 a[112:115], a[224:227], a[220:223], a[112:115]\n" \
"v_lshrrev_b32_e32 v218, 1, v220\n" \
"v_or_b32_e32 v214, v222, v214\n" \
"v_mfma_f32_16x16x32_bf16 a[116:119], a[164:167], a[156:159], 0\n" \
"v_mov_b32_e32 v222, 2.0\n" \
"v_and_b32_e32 v218, 4, v218\n" \
"v_mfma_f32_16x16x32_bf16 a[116:119], a[228:231], a[220:223], a[116:119]\n" \
"v_and_b32_e64 v217, 2, v217\n" \
"v_bitop3_b32 v215, v215, v218, v217 bitop3:0x1e\n" \
"v_mfma_f32_16x16x32_bf16 a[120:123], a[168:171], a[156:159], 0\n" \
"v_lshlrev_b32_e32 v223, 1, v220\n" \
"v_mov_b32_e32 v224, 2.0\n" \
"v_mfma_f32_16x16x32_bf16 a[120:123], a[232:235], a[220:223], a[120:123]\n" \
"v_cndmask_b32_e32 v193, v222, v214, vcc\n" \
"v_lshlrev_b32_e32 v214, 7, v220\n" \
"v_mfma_f32_16x16x32_bf16 a[124:127], a[172:175], a[156:159], 0\n" \
"v_lshlrev_b32_e32 v215, 4, v215\n" \
"s_mul_i32 s39, s3, 18\n" \
"s_waitcnt vmcnt(5) lgkmcnt(0)\n" \
"v_mfma_f32_16x16x32_bf16 a[124:127], a[236:239], a[220:223], a[124:127]\n" \
"s_barrier\n" \
"v_mfma_f32_16x16x32_bf16 v[52:55], a[176:179], a[128:131], 0\n" \
"s_add_u32 m0, s30, 0x0\n" \
"s_movk_i32 s84, 0x100\n" \
"v_mfma_f32_16x16x32_bf16 v[52:55], a[240:243], a[192:195], v[52:55]\n" \
"buffer_load_dwordx4 v22, s[64:67], s84 offen offset:0 lds\n" \
"v_mfma_f32_16x16x32_bf16 v[56:59], a[180:183], a[128:131], 0\n" \
"ds_read_b64_tr_b16 a[160:161], v32 offset:32768\n" \
"v_add3_u32 v214, v214, s34, v215\n" \
"v_mfma_f32_16x16x32_bf16 v[56:59], a[244:247], a[192:195], v[56:59]\n" \
"s_movk_i32 s84, 0x1d00\n" \
"v_mfma_f32_16x16x32_bf16 v[60:63], a[184:187], a[128:131], 0\n" \
"buffer_load_dwordx4 v23, s[64:67], s84 offen offset:1024 lds\n" \
"v_mfma_f32_16x16x32_bf16 v[60:63], a[248:251], a[192:195], v[60:63]\n" \
"ds_read_b64_tr_b16 a[162:163], v32 offset:33792\n" \
"v_cndmask_b32_e64 v195, v224, v223, s[4:5]\n" \
"v_mfma_f32_16x16x32_bf16 v[64:67], a[188:191], a[128:131], 0\n" \
"ds_read_b64_tr_b16 a[224:225], v32 offset:40960\n" \
"s_movk_i32 s4, 0x1f8\n" \
"v_mfma_f32_16x16x32_bf16 v[64:67], a[252:255], a[192:195], v[64:67]\n" \
"ds_read_b128 a[128:131], v28 offset:49152\n" \
"v_mov_b32_e64 v215, v213\n" \
"v_mfma_f32_16x16x32_bf16 v[80:83], a[188:191], a[132:135], 0\n" \
"ds_read_b128 a[192:195], v29 offset:49152\n" \
"s_add_i32 s35, s35, s39\n" \
"v_mfma_f32_16x16x32_bf16 v[80:83], a[252:255], a[196:199], v[80:83]\n" \
"s_movk_i32 s84, 0x3900\n" \
"v_mfma_f32_16x16x32_bf16 v[76:79], a[184:187], a[132:135], 0\n" \
"buffer_load_dwordx4 v22, s[64:67], s84 offen offset:2048 lds\n" \
"v_mfma_f32_16x16x32_bf16 v[76:79], a[248:251], a[196:199], v[76:79]\n" \
"s_movk_i32 s84, 0x5500\n" \
"v_mfma_f32_16x16x32_bf16 v[72:75], a[180:183], a[132:135], 0\n" \
"buffer_load_dwordx4 v23, s[64:67], s84 offen offset:3072 lds\n" \
"v_mfma_f32_16x16x32_bf16 v[72:75], a[244:247], a[196:199], v[72:75]\n" \
"s_add_u32 m0, s31, 0x0\n" \
"s_mov_b32 s84, 0x100000\n" \
"v_mfma_f32_16x16x32_bf16 v[68:71], a[176:179], a[132:135], 0\n" \
"buffer_load_dwordx4 v26, s[68:71], s84 offen offset:0 lds\n" \
"v_mfma_f32_16x16x32_bf16 v[68:71], a[240:243], a[196:199], v[68:71]\n" \
"ds_read_b128 a[132:135], v28 offset:51200\n" \
"v_cmp_gt_u32_e64 vcc, s4, v216\n" \
"v_mfma_f32_16x16x32_bf16 v[84:87], a[176:179], a[136:139], 0\n" \
"ds_read_b128 a[196:199], v29 offset:51200\n" \
"v_add_u32_e64 v212, s35, v212\n" \
"v_mfma_f32_16x16x32_bf16 v[84:87], a[240:243], a[200:203], v[84:87]\n" \
"s_addk_i32 s84, 0x7c00\n" \
"v_mfma_f32_16x16x32_bf16 v[88:91], a[180:183], a[136:139], 0\n" \
"buffer_load_dwordx4 v26, s[68:71], s84 offen offset:1024 lds\n" \
"v_mfma_f32_16x16x32_bf16 v[88:91], a[244:247], a[200:203], v[88:91]\n" \
"ds_read_b64_tr_b16 a[226:227], v32 offset:41984\n" \
"v_mov_b32_e32 v219, 2.0\n" \
"v_mfma_f32_16x16x32_bf16 v[92:95], a[184:187], a[136:139], 0\n" \
"s_addk_i32 s84, 0x7c00\n" \
"v_mfma_f32_16x16x32_bf16 v[92:95], a[248:251], a[200:203], v[92:95]\n" \
"buffer_load_dwordx4 v27, s[68:71], s84 offen offset:2048 lds\n" \
"v_mfma_f32_16x16x32_bf16 v[96:99], a[188:191], a[136:139], 0\n" \
"ds_read_b64_tr_b16 a[164:165], v33 offset:32768\n" \
"v_cndmask_b32_e32 v197, v215, v214, vcc\n" \
"v_mfma_f32_16x16x32_bf16 v[96:99], a[252:255], a[200:203], v[96:99]\n" \
"ds_read_b128 a[136:139], v28 offset:53248\n" \
"s_movk_i32 s35, 0x120\n" \
"v_mfma_f32_16x16x32_bf16 v[112:115], a[188:191], a[140:143], 0\n" \
"ds_read_b128 a[200:203], v29 offset:53248\n" \
"v_mul_u32_u24_e32 v214, 0xd80, v212\n" \
"v_mfma_f32_16x16x32_bf16 v[112:115], a[252:255], a[204:207], v[112:115]\n" \
"s_addk_i32 s84, 0x7c00\n" \
"v_mfma_f32_16x16x32_bf16 v[108:111], a[184:187], a[140:143], 0\n" \
"buffer_load_dwordx4 v27, s[68:71], s84 offen offset:3072 lds\n" \
"v_mfma_f32_16x16x32_bf16 v[108:111], a[248:251], a[204:207], v[108:111]\n" \
"s_add_u32 m0, s30, 0x1000\n" \
"s_mov_b32 s84, 0x8100\n" \
"v_mfma_f32_16x16x32_bf16 v[104:107], a[180:183], a[140:143], 0\n" \
"buffer_load_dwordx4 v22, s[64:67], s84 offen offset:0 lds\n" \
"v_mfma_f32_16x16x32_bf16 v[104:107], a[244:247], a[204:207], v[104:107]\n" \
"ds_read_b64_tr_b16 a[166:167], v33 offset:33792\n" \
"v_cmp_gt_u32_e64 vcc, s35, v20\n" \
"v_mfma_f32_16x16x32_bf16 v[100:103], a[176:179], a[140:143], 0\n" \
"ds_read_b64_tr_b16 a[228:229], v33 offset:40960\n" \
"v_lshrrev_b32_e64 v215, 1, v212\n" \
"v_mfma_f32_16x16x32_bf16 v[100:103], a[240:243], a[204:207], v[100:103]\n" \
"ds_read_b128 a[140:143], v28 offset:55296\n" \
"s_and_b32 s4, s2, 7\n" \
"v_mfma_f32_16x16x32_bf16 v[116:119], a[176:179], a[144:147], 0\n" \
"ds_read_b128 a[204:207], v29 offset:55296\n" \
"v_cndmask_b32_e32 v194, v219, v214, vcc\n" \
"v_mfma_f32_16x16x32_bf16 v[116:119], a[240:243], a[208:211], v[116:119]\n" \
"s_addk_i32 s84, 0x1c00\n" \
"v_mfma_f32_16x16x32_bf16 v[120:123], a[180:183], a[144:147], 0\n" \
"buffer_load_dwordx4 v23, s[64:67], s84 offen offset:1024 lds\n" \
"v_mfma_f32_16x16x32_bf16 v[120:123], a[244:247], a[208:211], v[120:123]\n" \
"s_addk_i32 s84, 0x1c00\n" \
"v_mfma_f32_16x16x32_bf16 v[124:127], a[184:187], a[144:147], 0\n" \
"buffer_load_dwordx4 v22, s[64:67], s84 offen offset:2048 lds\n" \
"v_mfma_f32_16x16x32_bf16 v[124:127], a[248:251], a[208:211], v[124:127]\n" \
"s_addk_i32 s84, 0x1c00\n" \
".fill (L_pb + 3808 - .) / 4, 4, 0xBF800000\n" \
"L_pp1:\n" \
"s_cbranch_execz L_pret\n" \
"v_mfma_f32_16x16x32_bf16 v[128:131], a[188:191], a[144:147], 0\n" \
"buffer_load_dwordx4 v23, s[64:67], s84 offen offset:3072 lds\n" \
"v_mfma_f32_16x16x32_bf16 v[128:131], a[252:255], a[208:211], v[128:131]\n" \
"ds_read_b128 a[144:147], v28 offset:32768\n" \
"v_lshlrev_b32_e32 v214, 7, v212\n" \
"v_mfma_f32_16x16x32_bf16 v[144:147], a[188:191], a[148:151], 0\n" \
"ds_read_b128 a[208:211], v29 offset:32768\n" \
"v_lshrrev_b32_e64 v212, 2, v212\n" \
"v_mfma_f32_16x16x32_bf16 v[144:147], a[252:255], a[212:215], v[144:147]\n" \
"s_add_u32 m0, s31, 0x4000\n" \
"s_mov_b32 s84, 0x100000\n" \
"v_mfma_f32_16x16x32_bf16 v[140:143], a[184:187], a[148:151], 0\n" \
"buffer_load_dwordx4 v26, s[72:75], s84 offen offset:0 lds\n" \
"v_mfma_f32_16x16x32_bf16 v[140:143], a[248:251], a[212:215], v[140:143]\n" \
"ds_read_b64_tr_b16 a[230:231], v33 offset:41984\n" \
"v_and_b32_e64 v212, 2, v212\n" \
"v_mfma_f32_16x16x32_bf16 v[136:139], a[180:183], a[148:151], 0\n" \
"s_addk_i32 s84, 0x7c00\n" \
"v_mfma_f32_16x16x32_bf16 v[136:139], a[244:247], a[212:215], v[136:139]\n" \
"buffer_load_dwordx4 v26, s[72:75], s84 offen offset:1024 lds\n" \
"v_mfma_f32_16x16x32_bf16 v[132:135], a[176:179], a[148:151], 0\n" \
"ds_read_b64_tr_b16 a[168:169], v34 offset:32768\n" \
"v_and_or_b32 v212, v215, 1, v212\n" \
"v_mfma_f32_16x16x32_bf16 v[132:135], a[240:243], a[212:215], v[132:135]\n" \
"ds_read_b128 a[148:151], v28 offset:34816\n" \
"s_lshl_b32 s5, s4, 2\n" \
"v_mfma_f32_16x16x32_bf16 v[148:151], a[176:179], a[152:155], 0\n" \
"ds_read_b128 a[212:215], v29 offset:34816\n" \
"s_bfe_u32 s38, s2, 0x20003\n" \
"v_mfma_f32_16x16x32_bf16 v[148:151], a[240:243], a[216:219], v[148:151]\n" \
"ds_read_b64_tr_b16 a[170:171], v34 offset:33792\n" \
"v_lshlrev_b32_e64 v212, 5, v212\n" \
"v_mfma_f32_16x16x32_bf16 v[152:155], a[180:183], a[152:155], 0\n" \
"ds_read_b64_tr_b16 a[232:233], v34 offset:40960\n" \
"s_movk_i32 s35, 0x60\n" \
"v_mfma_f32_16x16x32_bf16 v[152:155], a[244:247], a[216:219], v[152:155]\n" \
"ds_read_b64_tr_b16 a[234:235], v34 offset:41984\n" \
"s_or_b32 s5, s5, s38\n" \
"v_mfma_f32_16x16x32_bf16 v[156:159], a[184:187], a[152:155], 0\n" \
"ds_read_b64_tr_b16 a[172:173], v35 offset:32768\n" \
"v_bitop3_b32 v212, v212, s35, v214 bitop3:0x36\n" \
"v_mfma_f32_16x16x32_bf16 v[156:159], a[248:251], a[216:219], v[156:159]\n" \
"ds_read_b64_tr_b16 a[174:175], v35 offset:33792\n" \
"s_lshr_b32 s36, s2, 3\n" \
"v_mfma_f32_16x16x32_bf16 v[160:163], a[188:191], a[152:155], 0\n" \
"ds_read_b64_tr_b16 a[236:237], v35 offset:40960\n" \
"v_add_u32_e32 v212, s34, v212\n" \
"v_mfma_f32_16x16x32_bf16 v[160:163], a[252:255], a[216:219], v[160:163]\n" \
"ds_read_b128 a[152:155], v28 offset:36864\n" \
"s_mul_i32 s34, s5, 0x12000\n" \
"v_mfma_f32_16x16x32_bf16 v[176:179], a[188:191], a[156:159], 0\n" \
"ds_read_b128 a[216:219], v29 offset:36864\n" \
"s_or_b32 s40, s34, 0x6000000\n" \
"v_mfma_f32_16x16x32_bf16 v[176:179], a[252:255], a[220:223], v[176:179]\n" \
"ds_read_b64_tr_b16 a[238:239], v35 offset:41984\n" \
"s_mul_i32 s34, s36, 0x90000\n" \
"v_mfma_f32_16x16x32_bf16 v[172:175], a[184:187], a[156:159], 0\n" \
"s_lshl_b32 s36, s3, 14\n" \
"s_add_i32 s34, s34, s36\n" \
"v_mfma_f32_16x16x32_bf16 v[172:175], a[248:251], a[220:223], v[172:175]\n" \
"s_lshl_b32 s38, s5, 8\n" \
"s_add_i32 s41, s34, 0x4240000\n" \
"v_mfma_f32_16x16x32_bf16 v[168:171], a[180:183], a[156:159], 0\n" \
"s_mul_i32 s34, s5, 36\n" \
"s_add_i32 s5, s7, s40\n" \
"v_mfma_f32_16x16x32_bf16 v[168:171], a[244:247], a[220:223], v[168:171]\n" \
"s_addk_i32 s5, 0x2000\n" \
"s_mov_b32 s37, 2.0\n" \
"v_mfma_f32_16x16x32_bf16 v[164:167], a[176:179], a[156:159], 0\n" \
"s_or_b32 s38, s38, 0x5be8000\n" \
"v_mfma_f32_16x16x32_bf16 v[164:167], a[240:243], a[220:223], v[164:167]\n" \
"ds_read_b128 a[156:159], v28 offset:38912\n" \
"ds_read_b128 a[220:223], v29 offset:38912\n" \
"s_waitcnt lgkmcnt(15)\n" \
"v_mfma_f32_16x16x32_bf16 a[0:3], a[160:163], a[128:131], a[0:3]\n" \
"s_addk_i32 s84, 0x7c00\n" \
"v_mfma_f32_16x16x32_bf16 a[0:3], a[224:227], a[192:195], a[0:3]\n" \
"buffer_load_dwordx4 v27, s[72:75], s84 offen offset:2048 lds\n" \
"v_mfma_f32_16x16x32_bf16 a[4:7], a[164:167], a[128:131], a[4:7]\n" \
"ds_read_b64_tr_b16 a[176:177], v36 offset:32768\n" \
"s_waitcnt lgkmcnt(15)\n" \
"v_mfma_f32_16x16x32_bf16 a[4:7], a[228:231], a[192:195], a[4:7]\n" \
"s_addk_i32 s84, 0x7c00\n" \
"s_waitcnt lgkmcnt(11)\n" \
"v_mfma_f32_16x16x32_bf16 a[8:11], a[168:171], a[128:131], a[8:11]\n" \
"buffer_load_dwordx4 v27, s[72:75], s84 offen offset:3072 lds\n" \
"s_waitcnt lgkmcnt(9)\n" \
"v_mfma_f32_16x16x32_bf16 a[8:11], a[232:235], a[192:195], a[8:11]\n" \
"ds_read_b64_tr_b16 a[178:179], v36 offset:33792\n" \
"s_waitcnt lgkmcnt(8)\n" \
"v_mfma_f32_16x16x32_bf16 a[12:15], a[172:175], a[128:131], a[12:15]\n" \
"ds_read_b64_tr_b16 a[240:241], v36 offset:40960\n" \
"s_waitcnt lgkmcnt(5)\n" \
"v_mfma_f32_16x16x32_bf16 a[12:15], a[236:239], a[192:195], a[12:15]\n" \
"s_cmp_lg_u32 s3, 0\n" \
"s_cselect_b32 s36, s37, s5\n" \
"v_mfma_f32_16x16x32_bf16 a[28:31], a[172:175], a[132:135], a[28:31]\n" \
"s_lshl_b32 s5, s6, 2\n" \
"s_or_b32 s5, s38, s5\n" \
"v_mfma_f32_16x16x32_bf16 a[28:31], a[236:239], a[196:199], a[28:31]\n" \
"ds_read_b64_tr_b16 a[242:243], v36 offset:41984\n" \
"s_lshl_b32 s35, s3, 10\n" \
"v_mfma_f32_16x16x32_bf16 a[24:27], a[168:171], a[132:135], a[24:27]\n" \
"s_cmpk_lt_u32 s3, 3\n" \
"s_cselect_b32 s37, s37, s5\n" \
"v_mfma_f32_16x16x32_bf16 a[24:27], a[232:235], a[196:199], a[24:27]\n" \
"s_lshl_b32 s5, s4, 16\n" \
"s_lshl_b32 s4, s4, 11\n" \
"v_mfma_f32_16x16x32_bf16 a[20:23], a[164:167], a[132:135], a[20:23]\n" \
"v_cndmask_b32_e32 v198, v213, v212, vcc\n" \
"v_or_b32_e32 v212, s35, v20\n" \
"v_mfma_f32_16x16x32_bf16 a[20:23], a[228:231], a[196:199], a[20:23]\n" \
"ds_read_b64_tr_b16 a[180:181], v37 offset:32768\n" \
"s_or_b32 s39, s40, s35\n" \
"v_mfma_f32_16x16x32_bf16 a[16:19], a[160:163], a[132:135], a[16:19]\n" \
"s_or_b32 s55, s41, s4\n" \
"v_or_b32_e32 v199, 0x22000, v212\n" \
"v_mfma_f32_16x16x32_bf16 a[16:19], a[224:227], a[196:199], a[16:19]\n" \
"s_add_i32 s35, s39, s7\n" \
"s_add_i32 s54, s41, s5\n" \
"v_mfma_f32_16x16x32_bf16 a[32:35], a[160:163], a[136:139], a[32:35]\n" \
"s_mov_b32 m0, s32\n" \
"ds_read_b64_tr_b16 a[182:183], v37 offset:33792\n" \
"v_mfma_f32_16x16x32_bf16 a[32:35], a[224:227], a[200:203], a[32:35]\n" \
"s_mov_b32 exec_lo, 0x0\n" \
"s_mov_b32 exec_hi, 0xff\n" \
"global_load_lds_dword v[184:185], off\n" \
"s_mov_b64 exec, -1\n" \
"v_mfma_f32_16x16x32_bf16 a[36:39], a[164:167], a[136:139], a[36:39]\n" \
"s_add_i32 s55, s55, 0x80000\n" \
"v_mov_b32_e32 v215, 0xffff\n" \
"v_mfma_f32_16x16x32_bf16 a[36:39], a[228:231], a[200:203], a[36:39]\n" \
"ds_read_b64_tr_b16 a[244:245], v37 offset:40960\n" \
"s_and_b32 s4, s3, 1\n" \
"v_mfma_f32_16x16x32_bf16 a[40:43], a[168:171], a[136:139], a[40:43]\n" \
"s_lshl_b32 s5, s4, 4\n" \
"v_lshrrev_b32_e32 v212, 4, v20\n" \
"v_mfma_f32_16x16x32_bf16 a[40:43], a[232:235], a[200:203], a[40:43]\n" \
"s_mov_b32 m0, s32\n" \
"v_lshrrev_b32_e32 v218, 6, v20\n" \
"v_mfma_f32_16x16x32_bf16 a[44:47], a[172:175], a[136:139], a[44:47]\n" \
"s_mov_b32 exec_lo, 0xffff0000\n" \
"s_mov_b32 exec_hi, 0x0\n" \
"global_load_lds_dword v[186:187], off\n" \
"s_mov_b64 exec, -1\n" \
"v_mfma_f32_16x16x32_bf16 a[44:47], a[236:239], a[200:203], a[44:47]\n" \
"ds_read_b64_tr_b16 a[246:247], v37 offset:41984\n" \
"v_and_b32_e64 v219, 12, v218\n" \
"v_mfma_f32_16x16x32_bf16 a[60:63], a[172:175], a[140:143], a[60:63]\n" \
"v_and_or_b32 v214, v212, 15, s5\n" \
"v_cmp_gt_u32_e64 vcc, v219, v214\n" \
"v_mfma_f32_16x16x32_bf16 a[60:63], a[236:239], a[204:207], a[60:63]\n" \
"v_mov_b32_e32 v220, 0xffff0000\n" \
"s_lshl_b32 s5, s2, 2\n" \
"v_mfma_f32_16x16x32_bf16 a[56:59], a[168:171], a[140:143], a[56:59]\n" \
"ds_read_b64_tr_b16 a[184:185], v38 offset:32768\n" \
"v_cndmask_b32_e64 v217, v215, 0, vcc\n" \
"v_mfma_f32_16x16x32_bf16 a[56:59], a[232:235], a[204:207], a[56:59]\n" \
"v_cmp_lt_u32_e32 vcc, v219, v214\n" \
"v_mov_b32_e64 v213, s23\n" \
"v_mfma_f32_16x16x32_bf16 a[52:55], a[164:167], a[140:143], a[52:55]\n" \
"s_or_b32 s5, s5, s3\n" \
"v_cndmask_b32_e32 v221, 0, v220, vcc\n" \
"v_mfma_f32_16x16x32_bf16 a[52:55], a[228:231], a[204:207], a[52:55]\n" \
"ds_read_b64_tr_b16 a[186:187], v38 offset:33792\n" \
"v_or_b32_e64 v12, v217, v221\n" \
"v_mfma_f32_16x16x32_bf16 a[48:51], a[160:163], a[140:143], a[48:51]\n" \
"v_or_b32_e32 v217, 2, v219\n" \
"v_cmp_gt_u32_e32 vcc, v217, v214\n" \
"v_mfma_f32_16x16x32_bf16 a[48:51], a[224:227], a[204:207], a[48:51]\n" \
"v_mov_b32_e32 v216, s25\n" \
"s_movk_i32 s40, 0xf400\n" \
"v_mfma_f32_16x16x32_bf16 a[64:67], a[160:163], a[144:147], a[64:67]\n" \
"ds_read_b64_tr_b16 a[248:249], v38 offset:40960\n" \
"v_cndmask_b32_e64 v221, v215, 0, vcc\n" \
"v_mfma_f32_16x16x32_bf16 a[64:67], a[224:227], a[208:211], a[64:67]\n" \
"v_cmp_lt_u32_e32 vcc, v217, v214\n" \
"s_lshl_b32 s4, s4, 6\n" \
"v_mfma_f32_16x16x32_bf16 a[68:71], a[164:167], a[144:147], a[68:71]\n" \
"v_cndmask_b32_e32 v217, 0, v220, vcc\n" \
"v_or_b32_e32 v13, v221, v217\n" \
"v_mfma_f32_16x16x32_bf16 a[68:71], a[228:231], a[208:211], a[68:71]\n" \
"ds_read_b64_tr_b16 a[250:251], v38 offset:41984\n" \
"v_or_b32_e64 v217, 16, v219\n" \
"v_mfma_f32_16x16x32_bf16 a[72:75], a[168:171], a[144:147], a[72:75]\n" \
"v_cmp_gt_u32_e32 vcc, v217, v214\n" \
"s_nop 1\n" \
"v_cndmask_b32_e64 v221, v215, 0, vcc\n" \
"v_mfma_f32_16x16x32_bf16 a[72:75], a[232:235], a[208:211], a[72:75]\n" \
"v_cmp_lt_u32_e32 vcc, v217, v214\n" \
"s_nop 1\n" \
"v_cndmask_b32_e64 v217, 0, v220, vcc\n" \
"v_mfma_f32_16x16x32_bf16 a[76:79], a[172:175], a[144:147], a[76:79]\n" \
"v_or_b32_e32 v14, v221, v217\n" \
"v_or_b32_e32 v217, 18, v219\n" \
"v_mfma_f32_16x16x32_bf16 a[76:79], a[236:239], a[208:211], a[76:79]\n" \
"ds_read_b64_tr_b16 a[188:189], v39 offset:32768\n" \
"v_cmp_gt_u32_e64 vcc, v217, v214\n" \
"v_mfma_f32_16x16x32_bf16 a[92:95], a[172:175], a[148:151], a[92:95]\n" \
"s_nop 0\n" \
"v_cndmask_b32_e64 v215, v215, 0, vcc\n" \
"v_cmp_lt_u32_e32 vcc, v217, v214\n" \
"v_mfma_f32_16x16x32_bf16 a[92:95], a[236:239], a[212:215], a[92:95]\n" \
"s_nop 0\n" \
"v_cndmask_b32_e32 v214, 0, v220, vcc\n" \
"v_or_b32_e64 v15, v215, v214\n" \
"v_mfma_f32_16x16x32_bf16 a[88:91], a[168:171], a[148:151], a[88:91]\n" \
"ds_read_b64_tr_b16 a[190:191], v39 offset:33792\n" \
"v_mad_u64_u32 v[214:215], s[6:7], s5, 42, v[212:213]\n" \
"v_mfma_f32_16x16x32_bf16 a[88:91], a[232:235], a[212:215], a[88:91]\n" \
"s_movk_i32 s5, 0x1440\n" \
"v_cmp_gt_u32_e32 vcc, s5, v214\n" \
"v_mfma_f32_16x16x32_bf16 a[84:87], a[164:167], a[148:151], a[84:87]\n" \
"v_min_u32_e32 v217, 0xa43f, v214\n" \
"v_mov_b32_e64 v214, s24\n" \
"v_mfma_f32_16x16x32_bf16 a[84:87], a[228:231], a[212:215], a[84:87]\n" \
"ds_read_b64_tr_b16 a[252:253], v39 offset:40960\n" \
"v_cndmask_b32_e64 v215, v216, v213, vcc\n" \
"v_mfma_f32_16x16x32_bf16 a[80:83], a[160:163], a[148:151], a[80:83]\n" \
"v_mov_b32_e32 v213, s22\n" \
"s_movk_i32 s6, 0xffee\n" \
"v_mfma_f32_16x16x32_bf16 a[80:83], a[224:227], a[212:215], a[80:83]\n" \
"v_cndmask_b32_e64 v214, v214, v213, vcc\n" \
"v_add_u32_e32 v213, 0xffffebc0, v217\n" \
"v_mfma_f32_16x16x32_bf16 a[96:99], a[160:163], a[152:155], a[96:99]\n" \
"ds_read_b64_tr_b16 a[254:255], v39 offset:41984\n" \
"s_lshl_b32 s5, s2, 5\n" \
"v_mfma_f32_16x16x32_bf16 a[96:99], a[224:227], a[216:219], a[96:99]\n" \
"v_cndmask_b32_e32 v213, v213, v217, vcc\n" \
"v_lshlrev_b32_e64 v216, 7, v213\n" \
"v_mfma_f32_16x16x32_bf16 a[100:103], a[164:167], a[152:155], a[100:103]\n" \
"v_mul_u32_u24_e32 v213, 0xe39, v212\n" \
"v_mov_b32_e64 v217, 0\n" \
"v_mfma_f32_16x16x32_bf16 a[100:103], a[228:231], a[216:219], a[100:103]\n" \
"v_lshrrev_b32_e64 v213, 16, v213\n" \
"v_lshl_add_u64 v[16:17], v[214:215], 0, v[216:217]\n" \
"v_mfma_f32_16x16x32_bf16 a[104:107], a[168:171], a[152:155], a[104:107]\n" \
"v_mad_i32_i24 v214, v213, s6, v212\n" \
"s_add_i32 s5, s5, 32\n" \
"v_mfma_f32_16x16x32_bf16 a[104:107], a[232:235], a[216:219], a[104:107]\n" \
"v_lshlrev_b32_e32 v215, 4, v214\n" \
"v_add_u32_e64 v216, s5, v215\n" \
"v_mfma_f32_16x16x32_bf16 a[108:111], a[172:175], a[152:155], a[108:111]\n" \
"v_and_b32_e32 v216, 0xf0, v216\n" \
"v_cmp_gt_u32_e64 vcc, 16, v214\n" \
"v_mfma_f32_16x16x32_bf16 a[108:111], a[236:239], a[216:219], a[108:111]\n" \
"s_movk_i32 s7, 0xf800\n" \
"v_cndmask_b32_e32 v214, v215, v216, vcc\n" \
"s_waitcnt lgkmcnt(15)\n" \
"v_mfma_f32_16x16x32_bf16 a[124:127], a[172:175], a[156:159], a[124:127]\n" \
"v_or_b32_e32 v215, 64, v212\n" \
"v_mul_u32_u24_e32 v216, 0xe39, v215\n" \
"v_mfma_f32_16x16x32_bf16 a[124:127], a[236:239], a[220:223], a[124:127]\n" \
"v_lshrrev_b32_e64 v216, 16, v216\n" \
"v_mad_i32_i24 v215, v216, s6, v215\n" \
"v_mfma_f32_16x16x32_bf16 a[120:123], a[168:171], a[156:159], a[120:123]\n" \
"v_lshlrev_b32_e32 v217, 4, v215\n" \
"v_add_u32_e32 v220, s5, v217\n" \
"v_mfma_f32_16x16x32_bf16 a[120:123], a[232:235], a[220:223], a[120:123]\n" \
"v_and_b32_e32 v220, 0xf0, v220\n" \
"v_cmp_gt_u32_e64 vcc, 16, v215\n" \
"v_mfma_f32_16x16x32_bf16 a[116:119], a[164:167], a[156:159], a[116:119]\n" \
"s_nop 0\n" \
"v_cndmask_b32_e32 v215, v217, v220, vcc\n" \
"v_or_b32_e32 v217, 0x80, v212\n" \
"v_mfma_f32_16x16x32_bf16 a[116:119], a[228:231], a[220:223], a[116:119]\n" \
"v_mul_u32_u24_e32 v220, 0xe39, v217\n" \
"v_lshrrev_b32_e64 v220, 16, v220\n" \
"v_mfma_f32_16x16x32_bf16 a[112:115], a[160:163], a[156:159], a[112:115]\n" \
"v_mad_i32_i24 v217, v220, s6, v217\n" \
"v_lshlrev_b32_e64 v221, 4, v217\n" \
"v_mfma_f32_16x16x32_bf16 a[112:115], a[224:227], a[220:223], a[112:115]\n" \
"s_waitcnt vmcnt(20)\n" \
"v_readfirstlane_b32 s62, v180\n" \
"s_nop 3\n" \
"s_add_u32 s62, s62, 0x9E3779B1\n" \
"s_waitcnt vmcnt(2) lgkmcnt(0)\n" \
"s_barrier\n" \
"v_mfma_f32_16x16x32_bf16 v[52:55], a[176:179], a[128:131], v[52:55]\n" \
"s_add_u32 m0, s30, 0x8000\n" \
"s_movk_i32 s84, 0x180\n" \
"v_mfma_f32_16x16x32_bf16 v[52:55], a[240:243], a[192:195], v[52:55]\n" \
"buffer_load_dwordx4 v22, s[64:67], s84 offen offset:0 lds\n" \
"v_mfma_f32_16x16x32_bf16 v[56:59], a[180:183], a[128:131], v[56:59]\n" \
"ds_read_b64_tr_b16 a[160:161], v32 offset:0\n" \
"v_add_u32_e64 v222, s5, v221\n" \
"v_mfma_f32_16x16x32_bf16 v[56:59], a[244:247], a[192:195], v[56:59]\n" \
"s_movk_i32 s84, 0x1d80\n" \
"v_mfma_f32_16x16x32_bf16 v[60:63], a[184:187], a[128:131], v[60:63]\n" \
"buffer_load_dwordx4 v23, s[64:67], s84 offen offset:1024 lds\n" \
"v_mfma_f32_16x16x32_bf16 v[60:63], a[248:251], a[192:195], v[60:63]\n" \
"ds_read_b64_tr_b16 a[162:163], v32 offset:1024\n" \
"v_and_b32_e32 v222, 0xf0, v222\n" \
"v_mfma_f32_16x16x32_bf16 v[64:67], a[188:191], a[128:131], v[64:67]\n" \
"ds_read_b64_tr_b16 a[224:225], v32 offset:8192\n" \
"v_cmp_gt_u32_e32 vcc, 16, v217\n" \
"v_mfma_f32_16x16x32_bf16 v[64:67], a[252:255], a[192:195], v[64:67]\n" \
"ds_read_b128 a[128:131], v28 offset:16384\n" \
"v_cndmask_b32_e64 v217, v221, v222, vcc\n" \
"v_mfma_f32_16x16x32_bf16 v[80:83], a[188:191], a[132:135], v[80:83]\n" \
"ds_read_b128 a[192:195], v29 offset:16384\n" \
"v_or_b32_e32 v221, 0xc0, v212\n" \
"v_mfma_f32_16x16x32_bf16 v[80:83], a[252:255], a[196:199], v[80:83]\n" \
"s_movk_i32 s84, 0x3980\n" \
"v_mfma_f32_16x16x32_bf16 v[76:79], a[184:187], a[132:135], v[76:79]\n" \
"buffer_load_dwordx4 v22, s[64:67], s84 offen offset:2048 lds\n" \
"v_mfma_f32_16x16x32_bf16 v[76:79], a[248:251], a[196:199], v[76:79]\n" \
"s_movk_i32 s84, 0x5580\n" \
"v_mfma_f32_16x16x32_bf16 v[72:75], a[180:183], a[132:135], v[72:75]\n" \
"buffer_load_dwordx4 v23, s[64:67], s84 offen offset:3072 lds\n" \
"v_mfma_f32_16x16x32_bf16 v[72:75], a[244:247], a[196:199], v[72:75]\n" \
"s_add_u32 m0, s31, 0x8000\n" \
"s_mov_b32 s84, 0x180000\n" \
"v_mfma_f32_16x16x32_bf16 v[68:71], a[176:179], a[132:135], v[68:71]\n" \
"buffer_load_dwordx4 v26, s[68:71], s84 offen offset:0 lds\n" \
"v_mfma_f32_16x16x32_bf16 v[68:71], a[240:243], a[196:199], v[68:71]\n" \
"ds_read_b128 a[132:135], v28 offset:18432\n" \
"v_mul_u32_u24_e32 v222, 0xe39, v221\n" \
"v_mfma_f32_16x16x32_bf16 v[84:87], a[176:179], a[136:139], v[84:87]\n" \
"ds_read_b128 a[196:199], v29 offset:18432\n" \
"v_lshrrev_b32_e64 v222, 16, v222\n" \
"v_mfma_f32_16x16x32_bf16 v[84:87], a[240:243], a[200:203], v[84:87]\n" \
"s_addk_i32 s84, 0x7c00\n" \
"v_mfma_f32_16x16x32_bf16 v[88:91], a[180:183], a[136:139], v[88:91]\n" \
"buffer_load_dwordx4 v26, s[68:71], s84 offen offset:1024 lds\n" \
"v_mfma_f32_16x16x32_bf16 v[88:91], a[244:247], a[200:203], v[88:91]\n" \
"ds_read_b64_tr_b16 a[226:227], v32 offset:9216\n" \
"v_mad_i32_i24 v221, v222, s6, v221\n" \
"v_mfma_f32_16x16x32_bf16 v[92:95], a[184:187], a[136:139], v[92:95]\n" \
"s_addk_i32 s84, 0x7c00\n" \
"v_mfma_f32_16x16x32_bf16 v[92:95], a[248:251], a[200:203], v[92:95]\n" \
"buffer_load_dwordx4 v27, s[68:71], s84 offen offset:2048 lds\n" \
"v_mfma_f32_16x16x32_bf16 v[96:99], a[188:191], a[136:139], v[96:99]\n" \
"ds_read_b64_tr_b16 a[164:165], v33 offset:0\n" \
"v_lshlrev_b32_e64 v223, 4, v221\n" \
"v_mfma_f32_16x16x32_bf16 v[96:99], a[252:255], a[200:203], v[96:99]\n" \
"ds_read_b128 a[136:139], v28 offset:20480\n" \
"v_add_u32_e64 v224, s5, v223\n" \
"v_mfma_f32_16x16x32_bf16 v[112:115], a[188:191], a[140:143], v[112:115]\n" \
"ds_read_b128 a[200:203], v29 offset:20480\n" \
"v_and_b32_e32 v224, 0xf0, v224\n" \
"v_mfma_f32_16x16x32_bf16 v[112:115], a[252:255], a[204:207], v[112:115]\n" \
"s_addk_i32 s84, 0x7c00\n" \
"v_mfma_f32_16x16x32_bf16 v[108:111], a[184:187], a[140:143], v[108:111]\n" \
"buffer_load_dwordx4 v27, s[68:71], s84 offen offset:3072 lds\n" \
"v_mfma_f32_16x16x32_bf16 v[108:111], a[248:251], a[204:207], v[108:111]\n" \
"s_add_u32 m0, s30, 0x9000\n" \
"s_mov_b32 s84, 0x8180\n" \
"v_mfma_f32_16x16x32_bf16 v[104:107], a[180:183], a[140:143], v[104:107]\n" \
"buffer_load_dwordx4 v22, s[64:67], s84 offen offset:0 lds\n" \
"v_mfma_f32_16x16x32_bf16 v[104:107], a[244:247], a[204:207], v[104:107]\n" \
"ds_read_b64_tr_b16 a[166:167], v33 offset:1024\n" \
"v_cmp_gt_u32_e32 vcc, 16, v221\n" \
"v_mfma_f32_16x16x32_bf16 v[100:103], a[176:179], a[140:143], v[100:103]\n" \
"ds_read_b64_tr_b16 a[228:229], v33 offset:8192\n" \
"v_cndmask_b32_e64 v221, v223, v224, vcc\n" \
"v_mfma_f32_16x16x32_bf16 v[100:103], a[240:243], a[204:207], v[100:103]\n" \
"ds_read_b128 a[140:143], v28 offset:22528\n" \
"v_or_b32_e32 v223, 0x100, v212\n" \
"v_mfma_f32_16x16x32_bf16 v[116:119], a[176:179], a[144:147], v[116:119]\n" \
"ds_read_b128 a[204:207], v29 offset:22528\n" \
"v_mul_u32_u24_e32 v224, 0xe39, v223\n" \
"v_mfma_f32_16x16x32_bf16 v[116:119], a[240:243], a[208:211], v[116:119]\n" \
"s_addk_i32 s84, 0x1c00\n" \
"v_mfma_f32_16x16x32_bf16 v[120:123], a[180:183], a[144:147], v[120:123]\n" \
"buffer_load_dwordx4 v23, s[64:67], s84 offen offset:1024 lds\n" \
"v_mfma_f32_16x16x32_bf16 v[120:123], a[244:247], a[208:211], v[120:123]\n" \
"s_addk_i32 s84, 0x1c00\n" \
"v_mfma_f32_16x16x32_bf16 v[124:127], a[184:187], a[144:147], v[124:127]\n" \
"buffer_load_dwordx4 v22, s[64:67], s84 offen offset:2048 lds\n" \
"v_mfma_f32_16x16x32_bf16 v[124:127], a[248:251], a[208:211], v[124:127]\n" \
"s_addk_i32 s84, 0x1c00\n" \
"v_mfma_f32_16x16x32_bf16 v[128:131], a[188:191], a[144:147], v[128:131]\n" \
"buffer_load_dwordx4 v23, s[64:67], s84 offen offset:3072 lds\n" \
"v_mfma_f32_16x16x32_bf16 v[128:131], a[252:255], a[208:211], v[128:131]\n" \
"ds_read_b128 a[144:147], v28 offset:0\n" \
"v_lshrrev_b32_e32 v224, 16, v224\n" \
"v_mfma_f32_16x16x32_bf16 v[144:147], a[188:191], a[148:151], v[144:147]\n" \
"ds_read_b128 a[208:211], v29 offset:0\n" \
"v_mad_i32_i24 v223, v224, s6, v223\n" \
"v_mfma_f32_16x16x32_bf16 v[144:147], a[252:255], a[212:215], v[144:147]\n" \
"s_add_u32 m0, s31, 0xc000\n" \
"s_mov_b32 s84, 0x180000\n" \
"v_mfma_f32_16x16x32_bf16 v[140:143], a[184:187], a[148:151], v[140:143]\n" \
"buffer_load_dwordx4 v26, s[72:75], s84 offen offset:0 lds\n" \
"v_mfma_f32_16x16x32_bf16 v[140:143], a[248:251], a[212:215], v[140:143]\n" \
"ds_read_b64_tr_b16 a[230:231], v33 offset:9216\n" \
"v_lshlrev_b32_e64 v225, 4, v223\n" \
"v_mfma_f32_16x16x32_bf16 v[136:139], a[180:183], a[148:151], v[136:139]\n" \
"s_addk_i32 s84, 0x7c00\n" \
"v_mfma_f32_16x16x32_bf16 v[136:139], a[244:247], a[212:215], v[136:139]\n" \
"buffer_load_dwordx4 v26, s[72:75], s84 offen offset:1024 lds\n" \
"v_mfma_f32_16x16x32_bf16 v[132:135], a[176:179], a[148:151], v[132:135]\n" \
"ds_read_b64_tr_b16 a[168:169], v34 offset:0\n" \
"v_add_u32_e32 v226, s5, v225\n" \
"v_mfma_f32_16x16x32_bf16 v[132:135], a[240:243], a[212:215], v[132:135]\n" \
"ds_read_b128 a[148:151], v28 offset:2048\n" \
"v_and_b32_e32 v226, 0xf0, v226\n" \
"v_mfma_f32_16x16x32_bf16 v[148:151], a[176:179], a[152:155], v[148:151]\n" \
"ds_read_b128 a[212:215], v29 offset:2048\n" \
"v_cmp_gt_u32_e64 vcc, 16, v223\n" \
"v_mfma_f32_16x16x32_bf16 v[148:151], a[240:243], a[216:219], v[148:151]\n" \
"ds_read_b64_tr_b16 a[170:171], v34 offset:1024\n" \
"v_cndmask_b32_e64 v223, v225, v226, vcc\n" \
"v_mfma_f32_16x16x32_bf16 v[152:155], a[180:183], a[152:155], v[152:155]\n" \
"ds_read_b64_tr_b16 a[232:233], v34 offset:8192\n" \
"v_or_b32_e32 v225, 0x140, v212\n" \
"v_mfma_f32_16x16x32_bf16 v[152:155], a[244:247], a[216:219], v[152:155]\n" \
"ds_read_b64_tr_b16 a[234:235], v34 offset:9216\n" \
"v_mul_u32_u24_e32 v226, 0xe39, v225\n" \
"v_mfma_f32_16x16x32_bf16 v[156:159], a[184:187], a[152:155], v[156:159]\n" \
"ds_read_b64_tr_b16 a[172:173], v35 offset:0\n" \
"v_lshrrev_b32_e64 v226, 16, v226\n" \
"v_mfma_f32_16x16x32_bf16 v[156:159], a[248:251], a[216:219], v[156:159]\n" \
"ds_read_b64_tr_b16 a[174:175], v35 offset:1024\n" \
"v_mad_i32_i24 v225, v226, s6, v225\n" \
"v_mfma_f32_16x16x32_bf16 v[160:163], a[188:191], a[152:155], v[160:163]\n" \
"ds_read_b64_tr_b16 a[236:237], v35 offset:8192\n" \
"v_lshlrev_b32_e64 v227, 4, v225\n" \
"v_mfma_f32_16x16x32_bf16 v[160:163], a[252:255], a[216:219], v[160:163]\n" \
"ds_read_b128 a[152:155], v28 offset:4096\n" \
"v_add_u32_e64 v228, s5, v227\n" \
"v_mfma_f32_16x16x32_bf16 v[176:179], a[188:191], a[156:159], v[176:179]\n" \
"ds_read_b128 a[216:219], v29 offset:4096\n" \
"v_and_b32_e32 v228, 0xf0, v228\n" \
"v_mfma_f32_16x16x32_bf16 v[176:179], a[252:255], a[220:223], v[176:179]\n" \
"ds_read_b64_tr_b16 a[238:239], v35 offset:9216\n" \
"v_cmp_gt_u32_e64 vcc, 16, v225\n" \
"v_mfma_f32_16x16x32_bf16 v[172:175], a[184:187], a[156:159], v[172:175]\n" \
"s_nop 0\n" \
"v_cndmask_b32_e32 v225, v227, v228, vcc\n" \
"v_or_b32_e32 v227, 0x180, v212\n" \
"v_mfma_f32_16x16x32_bf16 v[172:175], a[248:251], a[220:223], v[172:175]\n" \
"v_mul_u32_u24_e32 v228, 0xe39, v227\n" \
"v_lshrrev_b32_e64 v228, 16, v228\n" \
"v_mfma_f32_16x16x32_bf16 v[168:171], a[180:183], a[156:159], v[168:171]\n" \
"v_mad_i32_i24 v227, v228, s6, v227\n" \
"v_lshlrev_b32_e64 v229, 4, v227\n" \
"v_mfma_f32_16x16x32_bf16 v[168:171], a[244:247], a[220:223], v[168:171]\n" \
"v_add_u32_e64 v230, s5, v229\n" \
"v_and_b32_e32 v230, 0xf0, v230\n" \
"v_mfma_f32_16x16x32_bf16 v[164:167], a[176:179], a[156:159], v[164:167]\n" \
"v_cmp_gt_u32_e32 vcc, 16, v227\n" \
"s_nop 1\n" \
"v_cndmask_b32_e64 v227, v229, v230, vcc\n" \
"v_mfma_f32_16x16x32_bf16 v[164:167], a[240:243], a[220:223], v[164:167]\n" \
"ds_read_b128 a[156:159], v28 offset:6144\n" \
"ds_read_b128 a[220:223], v29 offset:6144\n" \
"s_waitcnt lgkmcnt(15)\n" \
"v_mfma_f32_16x16x32_bf16 a[0:3], a[160:163], a[128:131], a[0:3]\n" \
"s_addk_i32 s84, 0x7c00\n" \
"v_mfma_f32_16x16x32_bf16 a[0:3], a[224:227], a[192:195], a[0:3]\n" \
"buffer_load_dwordx4 v27, s[72:75], s84 offen offset:2048 lds\n" \
"v_mfma_f32_16x16x32_bf16 a[4:7], a[164:167], a[128:131], a[4:7]\n" \
"ds_read_b64_tr_b16 a[176:177], v36 offset:0\n" \
"v_or_b32_e32 v229, 0x1c0, v212\n" \
"s_waitcnt lgkmcnt(15)\n" \
"v_mfma_f32_16x16x32_bf16 a[4:7], a[228:231], a[192:195], a[4:7]\n" \
"s_addk_i32 s84, 0x7c00\n" \
"s_waitcnt lgkmcnt(11)\n" \
"v_mfma_f32_16x16x32_bf16 a[8:11], a[168:171], a[128:131], a[8:11]\n" \
"buffer_load_dwordx4 v27, s[72:75], s84 offen offset:3072 lds\n" \
"s_waitcnt lgkmcnt(9)\n" \
"v_mfma_f32_16x16x32_bf16 a[8:11], a[232:235], a[192:195], a[8:11]\n" \
"ds_read_b64_tr_b16 a[178:179], v36 offset:1024\n" \
"v_mul_u32_u24_e32 v230, 0xe39, v229\n" \
"s_waitcnt lgkmcnt(8)\n" \
"v_mfma_f32_16x16x32_bf16 a[12:15], a[172:175], a[128:131], a[12:15]\n" \
"ds_read_b64_tr_b16 a[240:241], v36 offset:8192\n" \
"v_lshrrev_b32_e32 v230, 16, v230\n" \
"s_waitcnt lgkmcnt(5)\n" \
"v_mfma_f32_16x16x32_bf16 a[12:15], a[236:239], a[192:195], a[12:15]\n" \
"v_mad_i32_i24 v229, v230, s6, v229\n" \
"v_lshlrev_b32_e32 v231, 4, v229\n" \
"v_mfma_f32_16x16x32_bf16 a[28:31], a[172:175], a[132:135], a[28:31]\n" \
"v_add_u32_e64 v232, s5, v231\n" \
"v_and_b32_e32 v232, 0xf0, v232\n" \
"v_mfma_f32_16x16x32_bf16 a[28:31], a[236:239], a[196:199], a[28:31]\n" \
"ds_read_b64_tr_b16 a[242:243], v36 offset:9216\n" \
"v_cmp_gt_u32_e64 vcc, 16, v229\n" \
"v_mfma_f32_16x16x32_bf16 a[24:27], a[168:171], a[132:135], a[24:27]\n" \
"buffer_load_dwordx4 v[200:203], v192, s[76:79], s34 offen offset:0\n" \
"v_mfma_f32_16x16x32_bf16 a[24:27], a[232:235], a[196:199], a[24:27]\n" \
"v_cndmask_b32_e64 v229, v231, v232, vcc\n" \
"v_or_b32_e32 v231, 0x200, v212\n" \
"v_mfma_f32_16x16x32_bf16 a[20:23], a[164:167], a[132:135], a[20:23]\n" \
"v_mul_u32_u24_e32 v232, 0xe39, v231\n" \
"v_lshrrev_b32_e64 v232, 16, v232\n" \
"v_mfma_f32_16x16x32_bf16 a[20:23], a[228:231], a[196:199], a[20:23]\n" \
"ds_read_b64_tr_b16 a[180:181], v37 offset:0\n" \
"v_mad_i32_i24 v231, v232, s6, v231\n" \
"v_mfma_f32_16x16x32_bf16 a[16:19], a[160:163], a[132:135], a[16:19]\n" \
"v_lshlrev_b32_e32 v233, 4, v231\n" \
"v_add_u32_e32 v234, s5, v233\n" \
"v_mfma_f32_16x16x32_bf16 a[16:19], a[224:227], a[196:199], a[16:19]\n" \
"s_movk_i32 s5, 0x480\n" \
"v_mad_u32_u24 v0, v213, s5, v214\n" \
"v_mfma_f32_16x16x32_bf16 a[32:35], a[160:163], a[136:139], a[32:35]\n" \
"buffer_load_dwordx4 v[204:207], v193, s[76:79], s34 offen offset:0\n" \
"v_mfma_f32_16x16x32_bf16 a[32:35], a[224:227], a[200:203], a[32:35]\n" \
"s_mov_b32 m0, s32\n" \
"ds_read_b64_tr_b16 a[182:183], v37 offset:1024\n" \
"v_mfma_f32_16x16x32_bf16 a[36:39], a[164:167], a[136:139], a[36:39]\n" \
"s_mov_b32 exec_lo, 0x0\n" \
"s_mov_b32 exec_hi, 0xff00\n" \
"global_load_lds_dword v[184:185], off\n" \
"s_mov_b64 exec, -1\n" \
"v_mfma_f32_16x16x32_bf16 a[36:39], a[228:231], a[200:203], a[36:39]\n" \
"ds_read_b64_tr_b16 a[244:245], v37 offset:8192\n" \
"v_mul_u32_u24_e32 v213, 0x480, v216\n" \
"v_mfma_f32_16x16x32_bf16 a[40:43], a[168:171], a[136:139], a[40:43]\n" \
"s_movk_i32 s6, 0xfc00\n" \
"s_nop 0\n" \
"v_add3_u32 v1, v213, v215, s6\n" \
"v_mfma_f32_16x16x32_bf16 a[40:43], a[232:235], a[200:203], a[40:43]\n" \
"buffer_load_dword v208, v194, s[76:79], s34 offen offset:32\n" \
"v_mfma_f32_16x16x32_bf16 a[44:47], a[172:175], a[136:139], a[44:47]\n" \
"ds_read_b64_tr_b16 a[246:247], v37 offset:9216\n" \
"v_mul_u32_u24_e32 v213, 0x480, v220\n" \
"v_mfma_f32_16x16x32_bf16 a[44:47], a[236:239], a[200:203], a[44:47]\n" \
"v_add3_u32 v2, v213, v217, s7\n" \
"v_mul_u32_u24_e32 v213, 0x480, v222\n" \
"v_mfma_f32_16x16x32_bf16 a[60:63], a[172:175], a[140:143], a[60:63]\n" \
"v_add3_u32 v3, v213, v221, s40\n" \
"v_mul_u32_u24_e32 v213, 0x480, v226\n" \
"v_mfma_f32_16x16x32_bf16 a[60:63], a[236:239], a[204:207], a[60:63]\n" \
"v_add3_u32 v5, v213, v225, s6\n" \
"v_mul_u32_u24_e32 v213, 0x480, v228\n" \
"v_mfma_f32_16x16x32_bf16 a[56:59], a[168:171], a[140:143], a[56:59]\n" \
"buffer_load_dword v209, v194, s[76:79], s34 offen offset:1184\n" \
"v_mfma_f32_16x16x32_bf16 a[56:59], a[232:235], a[204:207], a[56:59]\n" \
"ds_read_b64_tr_b16 a[184:185], v38 offset:0\n" \
"v_add3_u32 v6, v213, v227, s7\n" \
"v_mfma_f32_16x16x32_bf16 a[52:55], a[164:167], a[140:143], a[52:55]\n" \
"v_mul_u32_u24_e32 v213, 0x480, v230\n" \
"v_add3_u32 v7, v213, v229, s40\n" \
"v_mfma_f32_16x16x32_bf16 a[52:55], a[228:231], a[204:207], a[52:55]\n" \
"ds_read_b64_tr_b16 a[186:187], v38 offset:1024\n" \
"s_lshl_b32 s7, s3, 3\n" \
"v_mfma_f32_16x16x32_bf16 a[48:51], a[160:163], a[140:143], a[48:51]\n" \
"s_lshl_b32 s40, s3, 1\n" \
"s_and_b32 s7, s7, 16\n" \
"v_mfma_f32_16x16x32_bf16 a[48:51], a[224:227], a[204:207], a[48:51]\n" \
"s_and_b32 s40, s40, 2\n" \
"s_and_b32 s6, s2, 7\n" \
"v_mfma_f32_16x16x32_bf16 a[64:67], a[160:163], a[144:147], a[64:67]\n" \
"buffer_load_dword v211, v194, s[76:79], s34 offen offset:2336\n" \
"v_mfma_f32_16x16x32_bf16 a[64:67], a[224:227], a[208:211], a[64:67]\n" \
"ds_read_b64_tr_b16 a[248:249], v38 offset:8192\n" \
"v_bfe_u32 v213, v20, 7, 1\n" \
"v_mfma_f32_16x16x32_bf16 a[68:71], a[164:167], a[144:147], a[68:71]\n" \
"s_or_b32 s7, s40, s7\n" \
"v_and_b32_e32 v234, 0xf0, v234\n" \
"v_mfma_f32_16x16x32_bf16 a[68:71], a[228:231], a[208:211], a[68:71]\n" \
"ds_read_b64_tr_b16 a[250:251], v38 offset:9216\n" \
"v_cmp_gt_u32_e64 vcc, 16, v231\n" \
"v_mfma_f32_16x16x32_bf16 a[72:75], a[168:171], a[144:147], a[72:75]\n" \
"v_or3_b32 v213, s7, v213, v219\n" \
"v_and_b32_e64 v214, 32, v20\n" \
"v_mfma_f32_16x16x32_bf16 a[72:75], a[232:235], a[208:211], a[72:75]\n" \
"s_mov_b32 s76, s20\n" \
"s_and_b32 s77, s21, 0xffff\n" \
"v_mfma_f32_16x16x32_bf16 a[76:79], a[172:175], a[144:147], a[76:79]\n" \
"s_mov_b32 s78, 0x480\n" \
"s_lshl_b32 s6, s6, 5\n" \
"v_mfma_f32_16x16x32_bf16 a[76:79], a[236:239], a[208:211], a[76:79]\n" \
"buffer_load_ushort v210, v195, s[76:79], 0 offen\n" \
"v_mfma_f32_16x16x32_bf16 a[92:95], a[172:175], a[148:151], a[92:95]\n" \
"ds_read_b64_tr_b16 a[188:189], v39 offset:0\n" \
"v_cndmask_b32_e64 v231, v233, v234, vcc\n" \
"v_mfma_f32_16x16x32_bf16 a[92:95], a[236:239], a[212:215], a[92:95]\n" \
"v_mul_u32_u24_e64 v213, 18, v213\n" \
"v_mov_b32_e32 v215, 0x100\n" \
"v_mfma_f32_16x16x32_bf16 a[88:91], a[168:171], a[148:151], a[88:91]\n" \
"s_mov_b32 s76, s8\n" \
"s_and_b32 s77, s9, 0xffff\n" \
"v_mfma_f32_16x16x32_bf16 a[88:91], a[232:235], a[212:215], a[88:91]\n" \
"s_mov_b32 s78, 0x240000\n" \
"ds_read_b64_tr_b16 a[190:191], v39 offset:1024\n" \
".fill (L_pb + 7904 - .) / 4, 4, 0xBF800000\n" \
"L_pp2:\n" \
"s_cbranch_execz L_pret\n" \
"v_mfma_f32_16x16x32_bf16 a[84:87], a[164:167], a[148:151], a[84:87]\n" \
"v_mov_b32_e32 v216, s6\n" \
"v_cmp_eq_u32_e64 vcc, 0, v214\n" \
"v_mfma_f32_16x16x32_bf16 a[84:87], a[228:231], a[212:215], a[84:87]\n" \
"ds_read_b64_tr_b16 a[252:253], v39 offset:8192\n" \
"v_and_or_b32 v213, v218, 1, v213\n" \
"v_mfma_f32_16x16x32_bf16 a[80:83], a[160:163], a[148:151], a[80:83]\n" \
"v_lshlrev_b32_e32 v212, 3, v212\n" \
"v_cndmask_b32_e32 v214, v215, v216, vcc\n" \
"v_mfma_f32_16x16x32_bf16 a[80:83], a[224:227], a[212:215], a[80:83]\n" \
"v_mad_u32_u24 v213, v213, s5, v214\n" \
"v_and_or_b32 v213, v20, 16, v213\n" \
"v_mfma_f32_16x16x32_bf16 a[96:99], a[160:163], a[152:155], a[96:99]\n" \
"ds_read_b64_tr_b16 a[254:255], v39 offset:9216\n" \
"v_add_u32_e32 v9, 0x4800, v213\n" \
"v_mfma_f32_16x16x32_bf16 a[96:99], a[224:227], a[216:219], a[96:99]\n" \
"v_mul_u32_u24_e32 v213, 0x120, v218\n" \
"v_and_or_b32 v10, v212, 24, v213\n" \
"v_mfma_f32_16x16x32_bf16 a[100:103], a[164:167], a[152:155], a[100:103]\n" \
"v_lshrrev_b32_e64 v213, 2, v20\n" \
"v_and_or_b32 v213, v213, 60, s4\n" \
"v_mfma_f32_16x16x32_bf16 a[100:103], a[228:231], a[216:219], a[100:103]\n" \
"v_lshl_or_b32 v212, s3, 9, v212\n" \
"v_mad_u32_u24 v4, v224, s5, v223\n" \
"v_mfma_f32_16x16x32_bf16 a[104:107], a[168:171], a[152:155], a[104:107]\n" \
"v_mad_u32_u24 v8, v232, s5, v231\n" \
"v_or_b32_e32 v18, 0x27000, v213\n" \
"v_mfma_f32_16x16x32_bf16 a[104:107], a[232:235], a[216:219], a[104:107]\n" \
"v_or_b32_e32 v19, 0x27800, v212\n" \
"v_mov_b32_e32 v215, 0x1000c0c\n" \
"v_mfma_f32_16x16x32_bf16 a[108:111], a[172:175], a[152:155], a[108:111]\n" \
"v_mov_b32_e32 v216, 0xc0c0100\n" \
"v_bfe_u32 v214, v20, 4, 1\n" \
"v_mfma_f32_16x16x32_bf16 a[108:111], a[236:239], a[216:219], a[108:111]\n" \
"v_lshrrev_b32_e32 v212, 8, v20\n" \
"v_cmp_eq_u32_e32 vcc, 0, v214\n" \
"s_waitcnt lgkmcnt(15)\n" \
"v_mfma_f32_16x16x32_bf16 a[124:127], a[172:175], a[156:159], a[124:127]\n" \
"v_bfe_u32 v213, v20, 4, 4\n" \
"v_mov_b32_e32 v217, 0xc0c0302\n" \
"v_mfma_f32_16x16x32_bf16 a[124:127], a[236:239], a[220:223], a[124:127]\n" \
"v_cndmask_b32_e32 v215, v215, v216, vcc\n" \
"v_mov_b32_e32 v216, 0x3020c0c\n" \
"v_mfma_f32_16x16x32_bf16 a[120:123], a[168:171], a[156:159], a[120:123]\n" \
"v_lshl_or_b32 v214, v212, 2, v214\n" \
"v_cndmask_b32_e64 v216, v216, v217, vcc\n" \
"v_mfma_f32_16x16x32_bf16 a[120:123], a[232:235], a[220:223], a[120:123]\n" \
"v_mov_b32_e32 v217, 0xc0c0c0c\n" \
"v_cmp_gt_u32_e64 vcc, v214, v213\n" \
"v_mfma_f32_16x16x32_bf16 a[116:119], a[164:167], a[156:159], a[116:119]\n" \
"v_or_b32_e32 v214, 2, v214\n" \
"s_lshr_b32 s4, s2, 5\n" \
"v_mfma_f32_16x16x32_bf16 a[116:119], a[228:231], a[220:223], a[116:119]\n" \
"v_cndmask_b32_e32 v40, v217, v215, vcc\n" \
"v_cndmask_b32_e32 v42, v215, v217, vcc\n" \
"v_mfma_f32_16x16x32_bf16 a[112:115], a[160:163], a[156:159], a[112:115]\n" \
"v_cmp_gt_u32_e32 vcc, v214, v213\n" \
"v_and_b32_e32 v214, 16, v20\n" \
"s_waitcnt vmcnt(7) lgkmcnt(0)\n" \
"v_mfma_f32_16x16x32_bf16 a[112:115], a[224:227], a[220:223], a[112:115]\n" \
"s_barrier\n" \
"v_mfma_f32_16x16x32_bf16 v[52:55], a[176:179], a[128:131], v[52:55]\n" \
"s_add_u32 m0, s30, 0x0\n" \
"s_movk_i32 s84, 0x200\n" \
"v_mfma_f32_16x16x32_bf16 v[52:55], a[240:243], a[192:195], v[52:55]\n" \
"buffer_load_dwordx4 v22, s[64:67], s84 offen offset:0 lds\n" \
"v_mfma_f32_16x16x32_bf16 v[56:59], a[180:183], a[128:131], v[56:59]\n" \
"ds_read_b64_tr_b16 a[160:161], v32 offset:32768\n" \
"s_movk_i32 s42, 0x3f80\n" \
"v_mfma_f32_16x16x32_bf16 v[56:59], a[244:247], a[192:195], v[56:59]\n" \
"s_movk_i32 s84, 0x1e00\n" \
"v_mfma_f32_16x16x32_bf16 v[60:63], a[184:187], a[128:131], v[60:63]\n" \
"buffer_load_dwordx4 v23, s[64:67], s84 offen offset:1024 lds\n" \
"v_mfma_f32_16x16x32_bf16 v[60:63], a[248:251], a[192:195], v[60:63]\n" \
"ds_read_b64_tr_b16 a[162:163], v32 offset:33792\n" \
"s_xor_b32 s5, s4, 15\n" \
"v_mfma_f32_16x16x32_bf16 v[64:67], a[188:191], a[128:131], v[64:67]\n" \
"ds_read_b64_tr_b16 a[224:225], v32 offset:40960\n" \
"v_mfma_f32_16x16x32_bf16 v[64:67], a[252:255], a[192:195], v[64:67]\n" \
"ds_read_b128 a[128:131], v28 offset:49152\n" \
"v_mfma_f32_16x16x32_bf16 v[80:83], a[188:191], a[132:135], v[80:83]\n" \
"ds_read_b128 a[192:195], v29 offset:49152\n" \
"v_mfma_f32_16x16x32_bf16 v[80:83], a[252:255], a[196:199], v[80:83]\n" \
"s_movk_i32 s84, 0x3a00\n" \
"v_mfma_f32_16x16x32_bf16 v[76:79], a[184:187], a[132:135], v[76:79]\n" \
"buffer_load_dwordx4 v22, s[64:67], s84 offen offset:2048 lds\n" \
"v_mfma_f32_16x16x32_bf16 v[76:79], a[248:251], a[196:199], v[76:79]\n" \
"s_movk_i32 s84, 0x5600\n" \
"v_mfma_f32_16x16x32_bf16 v[72:75], a[180:183], a[132:135], v[72:75]\n" \
"buffer_load_dwordx4 v23, s[64:67], s84 offen offset:3072 lds\n" \
"v_mfma_f32_16x16x32_bf16 v[72:75], a[244:247], a[196:199], v[72:75]\n" \
"s_add_u32 m0, s31, 0x0\n" \
"s_mov_b32 s84, 0x200000\n" \
"v_mfma_f32_16x16x32_bf16 v[68:71], a[176:179], a[132:135], v[68:71]\n" \
"buffer_load_dwordx4 v26, s[68:71], s84 offen offset:0 lds\n" \
"v_mfma_f32_16x16x32_bf16 v[68:71], a[240:243], a[196:199], v[68:71]\n" \
"ds_read_b128 a[132:135], v28 offset:51200\n" \
"v_mfma_f32_16x16x32_bf16 v[84:87], a[176:179], a[136:139], v[84:87]\n" \
"ds_read_b128 a[196:199], v29 offset:51200\n" \
"v_mfma_f32_16x16x32_bf16 v[84:87], a[240:243], a[200:203], v[84:87]\n" \
"s_addk_i32 s84, 0x7c00\n" \
"v_mfma_f32_16x16x32_bf16 v[88:91], a[180:183], a[136:139], v[88:91]\n" \
"buffer_load_dwordx4 v26, s[68:71], s84 offen offset:1024 lds\n" \
"v_mfma_f32_16x16x32_bf16 v[88:91], a[244:247], a[200:203], v[88:91]\n" \
"ds_read_b64_tr_b16 a[226:227], v32 offset:41984\n" \
"v_mfma_f32_16x16x32_bf16 v[92:95], a[184:187], a[136:139], v[92:95]\n" \
"s_addk_i32 s84, 0x7c00\n" \
"v_mfma_f32_16x16x32_bf16 v[92:95], a[248:251], a[200:203], v[92:95]\n" \
"buffer_load_dwordx4 v27, s[68:71], s84 offen offset:2048 lds\n" \
"v_mfma_f32_16x16x32_bf16 v[96:99], a[188:191], a[136:139], v[96:99]\n" \
"ds_read_b64_tr_b16 a[164:165], v33 offset:32768\n" \
"v_mfma_f32_16x16x32_bf16 v[96:99], a[252:255], a[200:203], v[96:99]\n" \
"ds_read_b128 a[136:139], v28 offset:53248\n" \
"v_mfma_f32_16x16x32_bf16 v[112:115], a[188:191], a[140:143], v[112:115]\n" \
"ds_read_b128 a[200:203], v29 offset:53248\n" \
"v_mfma_f32_16x16x32_bf16 v[112:115], a[252:255], a[204:207], v[112:115]\n" \
"s_addk_i32 s84, 0x7c00\n" \
"s_nop 0\n" \
"v_mfma_f32_16x16x32_bf16 v[108:111], a[184:187], a[140:143], v[108:111]\n" \
"buffer_load_dwordx4 v27, s[68:71], s84 offen offset:3072 lds\n" \
"v_mfma_f32_16x16x32_bf16 v[108:111], a[248:251], a[204:207], v[108:111]\n" \
"s_add_u32 m0, s30, 0x1000\n" \
"s_mov_b32 s84, 0x8200\n" \
"v_mfma_f32_16x16x32_bf16 v[104:107], a[180:183], a[140:143], v[104:107]\n" \
"buffer_load_dwordx4 v22, s[64:67], s84 offen offset:0 lds\n" \
"v_mfma_f32_16x16x32_bf16 v[104:107], a[244:247], a[204:207], v[104:107]\n" \
"ds_read_b64_tr_b16 a[166:167], v33 offset:33792\n" \
"v_mfma_f32_16x16x32_bf16 v[100:103], a[176:179], a[140:143], v[100:103]\n" \
"ds_read_b64_tr_b16 a[228:229], v33 offset:40960\n" \
"v_mfma_f32_16x16x32_bf16 v[100:103], a[240:243], a[204:207], v[100:103]\n" \
"ds_read_b128 a[140:143], v28 offset:55296\n" \
"v_mfma_f32_16x16x32_bf16 v[116:119], a[176:179], a[144:147], v[116:119]\n" \
"ds_read_b128 a[204:207], v29 offset:55296\n" \
"v_mfma_f32_16x16x32_bf16 v[116:119], a[240:243], a[208:211], v[116:119]\n" \
"s_addk_i32 s84, 0x1c00\n" \
"v_mfma_f32_16x16x32_bf16 v[120:123], a[180:183], a[144:147], v[120:123]\n" \
"buffer_load_dwordx4 v23, s[64:67], s84 offen offset:1024 lds\n" \
"v_mfma_f32_16x16x32_bf16 v[120:123], a[244:247], a[208:211], v[120:123]\n" \
"s_addk_i32 s84, 0x1c00\n" \
"v_mfma_f32_16x16x32_bf16 v[124:127], a[184:187], a[144:147], v[124:127]\n" \
"buffer_load_dwordx4 v22, s[64:67], s84 offen offset:2048 lds\n" \
"v_mfma_f32_16x16x32_bf16 v[124:127], a[248:251], a[208:211], v[124:127]\n" \
"s_addk_i32 s84, 0x1c00\n" \
"s_nop 0\n" \
"v_mfma_f32_16x16x32_bf16 v[128:131], a[188:191], a[144:147], v[128:131]\n" \
"buffer_load_dwordx4 v23, s[64:67], s84 offen offset:3072 lds\n" \
"v_mfma_f32_16x16x32_bf16 v[128:131], a[252:255], a[208:211], v[128:131]\n" \
"ds_read_b128 a[144:147], v28 offset:32768\n" \
"v_mfma_f32_16x16x32_bf16 v[144:147], a[188:191], a[148:151], v[144:147]\n" \
"ds_read_b128 a[208:211], v29 offset:32768\n" \
"v_mfma_f32_16x16x32_bf16 v[144:147], a[252:255], a[212:215], v[144:147]\n" \
"s_add_u32 m0, s31, 0x4000\n" \
"s_mov_b32 s84, 0x200000\n" \
"v_mfma_f32_16x16x32_bf16 v[140:143], a[184:187], a[148:151], v[140:143]\n" \
"buffer_load_dwordx4 v26, s[72:75], s84 offen offset:0 lds\n" \
"v_mfma_f32_16x16x32_bf16 v[140:143], a[248:251], a[212:215], v[140:143]\n" \
"ds_read_b64_tr_b16 a[230:231], v33 offset:41984\n" \
"v_mfma_f32_16x16x32_bf16 v[136:139], a[180:183], a[148:151], v[136:139]\n" \
"s_addk_i32 s84, 0x7c00\n" \
"s_nop 0\n" \
"v_mfma_f32_16x16x32_bf16 v[136:139], a[244:247], a[212:215], v[136:139]\n" \
"buffer_load_dwordx4 v26, s[72:75], s84 offen offset:1024 lds\n" \
"v_mfma_f32_16x16x32_bf16 v[132:135], a[176:179], a[148:151], v[132:135]\n" \
"ds_read_b64_tr_b16 a[168:169], v34 offset:32768\n" \
"v_mfma_f32_16x16x32_bf16 v[132:135], a[240:243], a[212:215], v[132:135]\n" \
"ds_read_b128 a[148:151], v28 offset:34816\n" \
"v_mfma_f32_16x16x32_bf16 v[148:151], a[176:179], a[152:155], v[148:151]\n" \
"ds_read_b128 a[212:215], v29 offset:34816\n" \
"v_mfma_f32_16x16x32_bf16 v[148:151], a[240:243], a[216:219], v[148:151]\n" \
"ds_read_b64_tr_b16 a[170:171], v34 offset:33792\n" \
"v_mfma_f32_16x16x32_bf16 v[152:155], a[180:183], a[152:155], v[152:155]\n" \
"ds_read_b64_tr_b16 a[232:233], v34 offset:40960\n" \
"v_mfma_f32_16x16x32_bf16 v[152:155], a[244:247], a[216:219], v[152:155]\n" \
"ds_read_b64_tr_b16 a[234:235], v34 offset:41984\n" \
"v_mfma_f32_16x16x32_bf16 v[156:159], a[184:187], a[152:155], v[156:159]\n" \
"ds_read_b64_tr_b16 a[172:173], v35 offset:32768\n" \
"v_mfma_f32_16x16x32_bf16 v[156:159], a[248:251], a[216:219], v[156:159]\n" \
"ds_read_b64_tr_b16 a[174:175], v35 offset:33792\n" \
"v_mfma_f32_16x16x32_bf16 v[160:163], a[188:191], a[152:155], v[160:163]\n" \
"ds_read_b64_tr_b16 a[236:237], v35 offset:40960\n" \
"v_mfma_f32_16x16x32_bf16 v[160:163], a[252:255], a[216:219], v[160:163]\n" \
"ds_read_b128 a[152:155], v28 offset:36864\n" \
"v_mfma_f32_16x16x32_bf16 v[176:179], a[188:191], a[156:159], v[176:179]\n" \
"ds_read_b128 a[216:219], v29 offset:36864\n" \
"v_mfma_f32_16x16x32_bf16 v[176:179], a[252:255], a[220:223], v[176:179]\n" \
"ds_read_b64_tr_b16 a[238:239], v35 offset:41984\n" \
"v_mfma_f32_16x16x32_bf16 v[172:175], a[184:187], a[156:159], v[172:175]\n" \
"s_cmpk_lt_u32 s3, 2\n" \
"s_cselect_b32 s6, s5, s4\n" \
"v_mfma_f32_16x16x32_bf16 v[172:175], a[248:251], a[220:223], v[172:175]\n" \
"s_lshl_b32 s7, s6, 7\n" \
"v_and_b32_e32 v213, 0x320, v20\n" \
"v_mfma_f32_16x16x32_bf16 v[168:171], a[180:183], a[156:159], v[168:171]\n" \
"s_movk_i32 s41, 0x100\n" \
"v_lshlrev_b32_e64 v214, v214, s42\n" \
"v_mfma_f32_16x16x32_bf16 v[168:171], a[244:247], a[220:223], v[168:171]\n" \
"v_mov_b32_e32 v215, 0\n" \
"s_add_i32 s40, s7, -16\n" \
"v_mfma_f32_16x16x32_bf16 v[164:167], a[176:179], a[156:159], v[164:167]\n" \
"v_cndmask_b32_e32 v41, v217, v216, vcc\n" \
"v_cndmask_b32_e32 v43, v216, v217, vcc\n" \
"v_mfma_f32_16x16x32_bf16 v[164:167], a[240:243], a[220:223], v[164:167]\n" \
"ds_read_b128 a[156:159], v28 offset:38912\n" \
"ds_read_b128 a[220:223], v29 offset:38912\n" \
"s_waitcnt lgkmcnt(15)\n" \
"v_mfma_f32_16x16x32_bf16 a[0:3], a[160:163], a[128:131], a[0:3]\n" \
"s_addk_i32 s84, 0x7c00\n" \
"v_mfma_f32_16x16x32_bf16 a[0:3], a[224:227], a[192:195], a[0:3]\n" \
"buffer_load_dwordx4 v27, s[72:75], s84 offen offset:2048 lds\n" \
"v_mfma_f32_16x16x32_bf16 a[4:7], a[164:167], a[128:131], a[4:7]\n" \
"ds_read_b64_tr_b16 a[176:177], v36 offset:32768\n" \
"v_cmp_eq_u32_e32 vcc, s41, v213\n" \
"s_waitcnt lgkmcnt(15)\n" \
"v_mfma_f32_16x16x32_bf16 a[4:7], a[228:231], a[192:195], a[4:7]\n" \
"s_addk_i32 s84, 0x7c00\n" \
"s_waitcnt lgkmcnt(11)\n" \
"v_mfma_f32_16x16x32_bf16 a[8:11], a[168:171], a[128:131], a[8:11]\n" \
"buffer_load_dwordx4 v27, s[72:75], s84 offen offset:3072 lds\n" \
"s_waitcnt lgkmcnt(9)\n" \
"v_mfma_f32_16x16x32_bf16 a[8:11], a[232:235], a[192:195], a[8:11]\n" \
"ds_read_b64_tr_b16 a[178:179], v36 offset:33792\n" \
"s_mov_b32 s41, 0\n" \
"s_waitcnt lgkmcnt(8)\n" \
"v_mfma_f32_16x16x32_bf16 a[12:15], a[172:175], a[128:131], a[12:15]\n" \
"ds_read_b64_tr_b16 a[240:241], v36 offset:40960\n" \
"s_and_b32 s43, s3, 1\n" \
"s_waitcnt lgkmcnt(5)\n" \
"v_mfma_f32_16x16x32_bf16 a[12:15], a[236:239], a[192:195], a[12:15]\n" \
"v_cndmask_b32_e32 v46, v215, v214, vcc\n" \
"v_and_b32_e32 v213, 0x330, v20\n" \
"v_mfma_f32_16x16x32_bf16 a[28:31], a[172:175], a[132:135], a[28:31]\n" \
"s_cmp_lg_u32 s7, 0\n" \
"s_cselect_b32 s40, s40, s41\n" \
"v_mfma_f32_16x16x32_bf16 a[28:31], a[236:239], a[196:199], a[28:31]\n" \
"ds_read_b64_tr_b16 a[242:243], v36 offset:41984\n" \
"s_mov_b32 s42, 0xff800000\n" \
"v_mfma_f32_16x16x32_bf16 a[24:27], a[168:171], a[132:135], a[24:27]\n" \
"s_cmp_lg_u32 s7, 0\n" \
"s_cselect_b32 s47, s41, s42\n" \
"v_mfma_f32_16x16x32_bf16 a[24:27], a[232:235], a[196:199], a[24:27]\n" \
"s_lshl_b32 s41, s43, 6\n" \
"v_lshrrev_b32_e32 v215, 6, v20\n" \
"v_mfma_f32_16x16x32_bf16 a[20:23], a[164:167], a[132:135], a[20:23]\n" \
"v_mov_b32_e32 v214, 0x3f80\n" \
"v_cmp_eq_u32_e64 vcc, 32, v213\n" \
"v_mfma_f32_16x16x32_bf16 a[20:23], a[228:231], a[196:199], a[20:23]\n" \
"ds_read_b64_tr_b16 a[180:181], v37 offset:32768\n" \
"s_or_b32 s7, s41, s7\n" \
"v_mfma_f32_16x16x32_bf16 a[16:19], a[160:163], a[132:135], a[16:19]\n" \
"v_lshrrev_b32_e32 v213, 7, v20\n" \
"v_bfe_u32 v216, v20, 6, 2\n" \
"v_mfma_f32_16x16x32_bf16 a[16:19], a[224:227], a[196:199], a[16:19]\n" \
"v_lshrrev_b32_e64 v219, 1, v20\n" \
"v_bfe_u32 v215, v215, 1, 1\n" \
"v_mfma_f32_16x16x32_bf16 a[32:35], a[160:163], a[136:139], a[32:35]\n" \
"s_waitcnt vmcnt(16)\n" \
"v_or_b32_e32 v204, v210, v204\n" \
"v_or_b32_e64 v206, v210, v206\n" \
"v_mfma_f32_16x16x32_bf16 a[32:35], a[224:227], a[200:203], a[32:35]\n" \
"v_mov_b32_e64 v210, 0\n" \
"s_add_u32 s88, s35, 0x1000\n" \
"v_mfma_f32_16x16x32_bf16 a[36:39], a[164:167], a[136:139], a[36:39]\n" \
"ds_read_b64_tr_b16 a[182:183], v37 offset:33792\n" \
"v_cndmask_b32_e64 v47, 0, v214, vcc\n" \
"v_mfma_f32_16x16x32_bf16 a[36:39], a[228:231], a[200:203], a[36:39]\n" \
"ds_read_b64_tr_b16 a[244:245], v37 offset:40960\n" \
"v_or_b32_e64 v214, s7, v213\n" \
"v_mfma_f32_16x16x32_bf16 a[40:43], a[168:171], a[136:139], a[40:43]\n" \
"s_mov_b32 m0, s32\n" \
"v_lshl_or_b32 v217, s43, 3, v213\n" \
"v_mfma_f32_16x16x32_bf16 a[40:43], a[232:235], a[200:203], a[40:43]\n" \
"s_mov_b32 exec_lo, 0xffffffff\n" \
"s_mov_b32 exec_hi, 0x0\n" \
"global_load_lds_dword v[188:189], off\n" \
"s_mov_b64 exec, -1\n" \
"v_mfma_f32_16x16x32_bf16 a[44:47], a[172:175], a[136:139], a[44:47]\n" \
"ds_read_b64_tr_b16 a[246:247], v37 offset:41984\n" \
"v_lshlrev_b32_e64 v218, 10, v212\n" \
"v_mfma_f32_16x16x32_bf16 a[44:47], a[236:239], a[200:203], a[44:47]\n" \
"v_lshlrev_b32_e32 v216, 7, v216\n" \
"v_and_b32_e32 v219, 24, v219\n" \
"v_mfma_f32_16x16x32_bf16 a[60:63], a[172:175], a[140:143], a[60:63]\n" \
"ds_write_b128 v196, v[200:203]\n" \
"v_mfma_f32_16x16x32_bf16 a[60:63], a[236:239], a[204:207], a[60:63]\n" \
"v_and_or_b32 v213, v213, 2, v215\n" \
"v_add_u32_e64 v217, s40, v217\n" \
"v_mfma_f32_16x16x32_bf16 a[56:59], a[168:171], a[140:143], a[56:59]\n" \
"ds_read_b64_tr_b16 a[184:185], v38 offset:32768\n" \
"v_or3_b32 v216, v216, v218, v219\n" \
"v_mfma_f32_16x16x32_bf16 a[56:59], a[232:235], a[204:207], a[56:59]\n" \
"v_lshlrev_b32_e64 v213, 5, v213\n" \
"s_mov_b32 s40, 0x14000\n" \
"v_mfma_f32_16x16x32_bf16 a[52:55], a[164:167], a[140:143], a[52:55]\n" \
"s_lshl_b32 s7, s2, 2\n" \
"v_or3_b32 v48, v216, v213, s40\n" \
"v_mfma_f32_16x16x32_bf16 a[52:55], a[228:231], a[204:207], a[52:55]\n" \
"ds_read_b64_tr_b16 a[186:187], v38 offset:33792\n" \
"s_lshl_b32 s40, s43, 2\n" \
"v_mfma_f32_16x16x32_bf16 a[48:51], a[160:163], a[140:143], a[48:51]\n" \
"s_lshl_b32 s6, s6, 3\n" \
"v_or_b32_e32 v213, s40, v212\n" \
"v_mfma_f32_16x16x32_bf16 a[48:51], a[224:227], a[204:207], a[48:51]\n" \
"s_or_b32 s6, s6, s40\n" \
"s_and_b32 s40, s7, 0x7c\n" \
"v_mfma_f32_16x16x32_bf16 a[64:67], a[160:163], a[144:147], a[64:67]\n" \
"ds_write_b128 v197, v[204:207]\n" \
"v_mfma_f32_16x16x32_bf16 a[64:67], a[224:227], a[208:211], a[64:67]\n" \
"ds_read_b64_tr_b16 a[248:249], v38 offset:40960\n" \
"s_or_b32 s40, s40, s3\n" \
"v_mfma_f32_16x16x32_bf16 a[68:71], a[164:167], a[144:147], a[68:71]\n" \
"s_cmpk_lt_u32 s40, 64\n" \
"s_cselect_b32 s41, s4, s5\n" \
"v_mfma_f32_16x16x32_bf16 a[68:71], a[228:231], a[208:211], a[68:71]\n" \
"ds_read_b64_tr_b16 a[250:251], v38 offset:41984\n" \
"s_lshl_b32 s40, s40, 1\n" \
"v_mfma_f32_16x16x32_bf16 a[72:75], a[168:171], a[144:147], a[72:75]\n" \
"s_lshl_b32 s41, s41, 7\n" \
"s_and_b32 s40, s40, 0x7e\n" \
"v_mfma_f32_16x16x32_bf16 a[72:75], a[232:235], a[208:211], a[72:75]\n" \
"s_or_b32 s40, s41, s40\n" \
"s_mulk_i32 s40, 0x480\n" \
"v_mfma_f32_16x16x32_bf16 a[76:79], a[172:175], a[144:147], a[76:79]\n" \
"v_add_u32_e64 v49, s40, v20\n" \
"s_mov_b32 s40, 0xc000\n" \
"v_mfma_f32_16x16x32_bf16 a[76:79], a[236:239], a[208:211], a[76:79]\n" \
"ds_read_b64_tr_b16 a[188:189], v39 offset:32768\n" \
"s_and_b32 s7, s7, 28\n" \
"v_mfma_f32_16x16x32_bf16 a[92:95], a[172:175], a[148:151], a[92:95]\n" \
"ds_write_b128 v198, v[208:211]\n" \
"v_mfma_f32_16x16x32_bf16 a[92:95], a[236:239], a[212:215], a[92:95]\n" \
"s_bfe_u32 s45, s2, 0x20003\n" \
"s_mov_b32 s41, 0x8000\n" \
"v_mfma_f32_16x16x32_bf16 a[88:91], a[168:171], a[148:151], a[88:91]\n" \
"ds_read_b64_tr_b16 a[190:191], v39 offset:33792\n" \
"v_mfma_f32_16x16x32_bf16 a[88:91], a[232:235], a[212:215], a[88:91]\n" \
"s_cmp_lg_u32 s4, 0\n" \
"s_cselect_b32 s51, s40, s41\n" \
"v_mfma_f32_16x16x32_bf16 a[84:87], a[164:167], a[148:151], a[84:87]\n" \
"s_movk_i32 s40, 0x4000\n" \
"s_lshr_b32 s44, s3, 1\n" \
"v_mfma_f32_16x16x32_bf16 a[84:87], a[228:231], a[212:215], a[84:87]\n" \
"ds_read_b64_tr_b16 a[252:253], v39 offset:40960\n" \
"s_or_b32 s46, s7, s45\n" \
"v_mfma_f32_16x16x32_bf16 a[80:83], a[160:163], a[148:151], a[80:83]\n" \
"s_cmp_lg_u32 s4, 0\n" \
"s_cselect_b32 s53, s41, s40\n" \
"v_mfma_f32_16x16x32_bf16 a[80:83], a[224:227], a[212:215], a[80:83]\n" \
"s_lshl_b32 s40, s44, 15\n" \
"s_lshl_b32 s41, s43, 14\n" \
"v_mfma_f32_16x16x32_bf16 a[96:99], a[160:163], a[152:155], a[96:99]\n" \
"ds_read_b64_tr_b16 a[254:255], v39 offset:41984\n" \
"s_lshl_b32 s48, s46, 20\n" \
"v_mfma_f32_16x16x32_bf16 a[96:99], a[224:227], a[216:219], a[96:99]\n" \
"s_or_b32 s41, s40, s41\n" \
"s_add_i32 s49, s48, 0x1000000\n" \
"v_mfma_f32_16x16x32_bf16 a[100:103], a[164:167], a[152:155], a[100:103]\n" \
"s_or_b32 s42, s49, s41\n" \
"s_lshl_b32 s4, s4, 16\n" \
"v_mfma_f32_16x16x32_bf16 a[100:103], a[228:231], a[216:219], a[100:103]\n" \
"s_or_b32 s41, s42, s4\n" \
"s_lshl_b32 s4, s5, 16\n" \
"v_mfma_f32_16x16x32_bf16 a[104:107], a[168:171], a[152:155], a[104:107]\n" \
"s_mul_i32 s5, s6, 0x24800\n" \
"s_lshl_b32 s6, s46, 9\n" \
"v_mfma_f32_16x16x32_bf16 a[104:107], a[232:235], a[216:219], a[104:107]\n" \
"s_lshl_b32 s7, s7, 6\n" \
"s_add_i32 s6, s5, s6\n" \
"v_mfma_f32_16x16x32_bf16 a[108:111], a[172:175], a[152:155], a[108:111]\n" \
"s_or_b32 s5, s5, s7\n" \
"s_lshl_b32 s7, s45, 2\n" \
"v_mfma_f32_16x16x32_bf16 a[108:111], a[236:239], a[216:219], a[108:111]\n" \
"s_or_b32 s5, s5, s7\n" \
"s_lshl_b32 s7, s3, 12\n" \
"s_waitcnt lgkmcnt(15)\n" \
"v_mfma_f32_16x16x32_bf16 a[124:127], a[172:175], a[156:159], a[124:127]\n" \
"s_or_b32 s50, s49, s7\n" \
"s_lshl_b32 s7, s3, 11\n" \
"v_mfma_f32_16x16x32_bf16 a[124:127], a[236:239], a[220:223], a[124:127]\n" \
"v_lshrrev_b32_e64 v215, 4, v20\n" \
"v_bfe_u32 v216, v20, 4, 3\n" \
"v_mfma_f32_16x16x32_bf16 a[120:123], a[168:171], a[156:159], a[120:123]\n" \
"s_or_b32 s42, s42, s4\n" \
"s_lshl_b32 s4, s43, 11\n" \
"v_mfma_f32_16x16x32_bf16 a[120:123], a[232:235], a[220:223], a[120:123]\n" \
"s_add_i32 s52, s48, s7\n" \
"s_add_i32 s53, s53, s7\n" \
"v_mfma_f32_16x16x32_bf16 a[116:119], a[164:167], a[156:159], a[116:119]\n" \
"v_mul_u32_u24_e32 v214, 0x480, v214\n" \
"v_bitop3_b32 v218, v212, v215, 7 bitop3:0x78\n" \
"v_mfma_f32_16x16x32_bf16 a[116:119], a[228:231], a[220:223], a[116:119]\n" \
"v_bitop3_b32 v212, v212, v216, 4 bitop3:0x36\n" \
"s_lshl_b32 s7, s3, 10\n" \
"v_mfma_f32_16x16x32_bf16 a[112:115], a[160:163], a[156:159], a[112:115]\n" \
"s_or_b32 s4, s40, s4\n" \
"v_lshl_or_b32 v25, v212, 4, v214\n" \
"s_waitcnt vmcnt(1) lgkmcnt(0)\n" \
"v_mfma_f32_16x16x32_bf16 a[112:115], a[224:227], a[220:223], a[112:115]\n" \
"s_barrier\n" \
"v_mfma_f32_16x16x32_bf16 v[52:55], a[176:179], a[128:131], v[52:55]\n" \
"s_add_u32 m0, s30, 0x8000\n" \
"s_movk_i32 s84, 0x280\n" \
"v_mfma_f32_16x16x32_bf16 v[52:55], a[240:243], a[192:195], v[52:55]\n" \
"buffer_load_dwordx4 v22, s[64:67], s84 offen offset:0 lds\n" \
"v_mfma_f32_16x16x32_bf16 v[56:59], a[180:183], a[128:131], v[56:59]\n" \
"s_movk_i32 s84, 0x1e80\n" \
"v_mfma_f32_16x16x32_bf16 v[56:59], a[244:247], a[192:195], v[56:59]\n" \
"buffer_load_dwordx4 v23, s[64:67], s84 offen offset:1024 lds\n" \
"v_mfma_f32_16x16x32_bf16 v[60:63], a[184:187], a[128:131], v[60:63]\n" \
"ds_read_b64_tr_b16 a[160:161], v32 offset:0\n" \
"v_mul_u32_u24_e32 v212, 0x480, v217\n" \
"v_mfma_f32_16x16x32_bf16 v[60:63], a[248:251], a[192:195], v[60:63]\n" \
"s_movk_i32 s84, 0x3a80\n" \
"v_mfma_f32_16x16x32_bf16 v[64:67], a[188:191], a[128:131], v[64:67]\n" \
"buffer_load_dwordx4 v22, s[64:67], s84 offen offset:2048 lds\n" \
"v_mfma_f32_16x16x32_bf16 v[64:67], a[252:255], a[192:195], v[64:67]\n" \
"ds_read_b128 a[128:131], v28 offset:16384\n" \
"v_bitop3_b32 v213, v215, v213, 7 bitop3:0x6c\n" \
"v_mfma_f32_16x16x32_bf16 v[80:83], a[188:191], a[132:135], v[80:83]\n" \
"ds_read_b128 a[192:195], v29 offset:16384\n" \
"s_or_b32 s40, s7, 0x14000\n" \
"v_mfma_f32_16x16x32_bf16 v[80:83], a[252:255], a[196:199], v[80:83]\n" \
"s_movk_i32 s84, 0x5680\n" \
"v_mfma_f32_16x16x32_bf16 v[76:79], a[184:187], a[132:135], v[76:79]\n" \
"buffer_load_dwordx4 v23, s[64:67], s84 offen offset:3072 lds\n" \
"v_mfma_f32_16x16x32_bf16 v[76:79], a[248:251], a[196:199], v[76:79]\n" \
"s_add_u32 m0, s31, 0x8000\n" \
"s_mov_b32 s84, 0x280000\n" \
"v_mfma_f32_16x16x32_bf16 v[72:75], a[180:183], a[132:135], v[72:75]\n" \
"buffer_load_dwordx4 v26, s[68:71], s84 offen offset:0 lds\n" \
"v_mfma_f32_16x16x32_bf16 v[72:75], a[244:247], a[196:199], v[72:75]\n" \
"ds_read_b64_tr_b16 a[162:163], v32 offset:1024\n" \
"s_lshl_b32 s43, s44, 13\n" \
"v_mfma_f32_16x16x32_bf16 v[68:71], a[176:179], a[132:135], v[68:71]\n" \
"ds_read_b64_tr_b16 a[224:225], v32 offset:8192\n" \
"s_mulk_i32 s44, 0xe800\n" \
"v_mfma_f32_16x16x32_bf16 v[68:71], a[240:243], a[196:199], v[68:71]\n" \
"ds_read_b128 a[132:135], v28 offset:18432\n" \
"s_lshl_b32 s7, s3, 8\n" \
"v_mfma_f32_16x16x32_bf16 v[84:87], a[176:179], a[136:139], v[84:87]\n" \
"ds_read_b128 a[196:199], v29 offset:18432\n" \
"v_lshl_or_b32 v24, v218, 4, v214\n" \
"v_mfma_f32_16x16x32_bf16 v[84:87], a[240:243], a[200:203], v[84:87]\n" \
"s_addk_i32 s84, 0x7c00\n" \
"v_mfma_f32_16x16x32_bf16 v[88:91], a[180:183], a[136:139], v[88:91]\n" \
"buffer_load_dwordx4 v26, s[68:71], s84 offen offset:1024 lds\n" \
"v_mfma_f32_16x16x32_bf16 v[88:91], a[244:247], a[200:203], v[88:91]\n" \
"s_addk_i32 s84, 0x7c00\n" \
"v_mfma_f32_16x16x32_bf16 v[92:95], a[184:187], a[136:139], v[92:95]\n" \
"buffer_load_dwordx4 v27, s[68:71], s84 offen offset:2048 lds\n" \
"v_mfma_f32_16x16x32_bf16 v[92:95], a[248:251], a[200:203], v[92:95]\n" \
"s_addk_i32 s84, 0x7c00\n" \
"v_mfma_f32_16x16x32_bf16 v[96:99], a[188:191], a[136:139], v[96:99]\n" \
"buffer_load_dwordx4 v27, s[68:71], s84 offen offset:3072 lds\n" \
"v_mfma_f32_16x16x32_bf16 v[96:99], a[252:255], a[200:203], v[96:99]\n" \
"ds_read_b128 a[136:139], v28 offset:20480\n" \
"v_lshl_or_b32 v21, v213, 4, v212\n" \
"v_mfma_f32_16x16x32_bf16 v[112:115], a[188:191], a[140:143], v[112:115]\n" \
"ds_read_b128 a[200:203], v29 offset:20480\n" \
"s_add_i32 s44, s44, 0x17000\n" \
"v_mfma_f32_16x16x32_bf16 v[112:115], a[252:255], a[204:207], v[112:115]\n" \
"s_add_u32 m0, s30, 0x9000\n" \
"s_mov_b32 s84, 0x8280\n" \
"v_mfma_f32_16x16x32_bf16 v[108:111], a[184:187], a[140:143], v[108:111]\n" \
"buffer_load_dwordx4 v22, s[64:67], s84 offen offset:0 lds\n" \
"v_mfma_f32_16x16x32_bf16 v[108:111], a[248:251], a[204:207], v[108:111]\n" \
"s_addk_i32 s84, 0x1c00\n" \
"v_mfma_f32_16x16x32_bf16 v[104:107], a[180:183], a[140:143], v[104:107]\n" \
"buffer_load_dwordx4 v23, s[64:67], s84 offen offset:1024 lds\n" \
"v_mfma_f32_16x16x32_bf16 v[104:107], a[244:247], a[204:207], v[104:107]\n" \
"s_addk_i32 s84, 0x1c00\n" \
"v_mfma_f32_16x16x32_bf16 v[100:103], a[176:179], a[140:143], v[100:103]\n" \
"buffer_load_dwordx4 v22, s[64:67], s84 offen offset:2048 lds\n" \
"v_mfma_f32_16x16x32_bf16 v[100:103], a[240:243], a[204:207], v[100:103]\n" \
"ds_read_b128 a[140:143], v28 offset:22528\n" \
"s_or_b32 s45, s7, 0x27000\n" \
"v_mfma_f32_16x16x32_bf16 v[116:119], a[176:179], a[144:147], v[116:119]\n" \
"ds_read_b128 a[204:207], v29 offset:22528\n" \
"s_or_b32 s46, s4, 0x10000\n" \
"v_mfma_f32_16x16x32_bf16 v[116:119], a[240:243], a[208:211], v[116:119]\n" \
"s_addk_i32 s84, 0x1c00\n" \
"v_mfma_f32_16x16x32_bf16 v[120:123], a[180:183], a[144:147], v[120:123]\n" \
"buffer_load_dwordx4 v23, s[64:67], s84 offen offset:3072 lds\n" \
"v_mfma_f32_16x16x32_bf16 v[120:123], a[244:247], a[208:211], v[120:123]\n" \
"s_add_u32 m0, s31, 0xc000\n" \
"s_mov_b32 s84, 0x280000\n" \
"v_mfma_f32_16x16x32_bf16 v[124:127], a[184:187], a[144:147], v[124:127]\n" \
"buffer_load_dwordx4 v26, s[72:75], s84 offen offset:0 lds\n" \
"v_mfma_f32_16x16x32_bf16 v[124:127], a[248:251], a[208:211], v[124:127]\n" \
"s_addk_i32 s84, 0x7c00\n" \
"v_mfma_f32_16x16x32_bf16 v[128:131], a[188:191], a[144:147], v[128:131]\n" \
"buffer_load_dwordx4 v26, s[72:75], s84 offen offset:1024 lds\n" \
"v_mfma_f32_16x16x32_bf16 v[128:131], a[252:255], a[208:211], v[128:131]\n" \
"ds_read_b128 a[144:147], v28 offset:0\n" \
"s_add_i32 s48, s6, 0x3000000\n" \
"v_mfma_f32_16x16x32_bf16 v[144:147], a[188:191], a[148:151], v[144:147]\n" \
"ds_read_b128 a[208:211], v29 offset:0\n" \
"s_add_i32 s49, s5, 0x3004000\n" \
"v_mfma_f32_16x16x32_bf16 v[144:147], a[252:255], a[212:215], v[144:147]\n" \
"ds_read_b128 v[200:203], v199 offset:0\n" \
"s_add_i32 s52, s52, 0x1010000\n" \
"v_mfma_f32_16x16x32_bf16 v[140:143], a[184:187], a[148:151], v[140:143]\n" \
"ds_read_b128 v[204:207], v199 offset:4096\n" \
"v_mfma_f32_16x16x32_bf16 v[140:143], a[248:251], a[212:215], v[140:143]\n" \
"ds_read_b64_tr_b16 a[226:227], v32 offset:9216\n" \
"v_mfma_f32_16x16x32_bf16 v[136:139], a[180:183], a[148:151], v[136:139]\n" \
"ds_read_b64_tr_b16 a[164:165], v33 offset:0\n" \
"v_mfma_f32_16x16x32_bf16 v[136:139], a[244:247], a[212:215], v[136:139]\n" \
"ds_read_b128 v[208:211], v199 offset:8192\n" \
"v_mfma_f32_16x16x32_bf16 v[132:135], a[176:179], a[148:151], v[132:135]\n" \
"ds_read_b64_tr_b16 a[166:167], v33 offset:1024\n" \
"v_mfma_f32_16x16x32_bf16 v[132:135], a[240:243], a[212:215], v[132:135]\n" \
"ds_read_b128 a[148:151], v28 offset:2048\n" \
"v_mfma_f32_16x16x32_bf16 v[148:151], a[176:179], a[152:155], v[148:151]\n" \
"ds_read_b128 a[212:215], v29 offset:2048\n" \
"v_mfma_f32_16x16x32_bf16 v[148:151], a[240:243], a[216:219], v[148:151]\n" \
"ds_read_b64_tr_b16 a[228:229], v33 offset:8192\n" \
"v_mfma_f32_16x16x32_bf16 v[152:155], a[180:183], a[152:155], v[152:155]\n" \
"ds_read_b64_tr_b16 a[230:231], v33 offset:9216\n" \
"v_mfma_f32_16x16x32_bf16 v[152:155], a[244:247], a[216:219], v[152:155]\n" \
"ds_read_b64_tr_b16 a[168:169], v34 offset:0\n" \
"v_mfma_f32_16x16x32_bf16 v[156:159], a[184:187], a[152:155], v[156:159]\n" \
"ds_read_b64_tr_b16 a[170:171], v34 offset:1024\n" \
"v_mfma_f32_16x16x32_bf16 v[156:159], a[248:251], a[216:219], v[156:159]\n" \
"ds_read_b64_tr_b16 a[232:233], v34 offset:8192\n" \
"v_mfma_f32_16x16x32_bf16 v[160:163], a[188:191], a[152:155], v[160:163]\n" \
"ds_read_b64_tr_b16 a[234:235], v34 offset:9216\n" \
"v_mfma_f32_16x16x32_bf16 v[160:163], a[252:255], a[216:219], v[160:163]\n" \
"ds_read_b128 a[152:155], v28 offset:4096\n" \
"v_mfma_f32_16x16x32_bf16 v[176:179], a[188:191], a[156:159], v[176:179]\n" \
"ds_read_b128 a[216:219], v29 offset:4096\n" \
"v_mfma_f32_16x16x32_bf16 v[176:179], a[252:255], a[220:223], v[176:179]\n" \
"ds_read_b64_tr_b16 a[172:173], v35 offset:0\n" \
"v_mfma_f32_16x16x32_bf16 v[172:175], a[184:187], a[156:159], v[172:175]\n" \
"ds_read_b64_tr_b16 a[174:175], v35 offset:1024\n" \
"v_mfma_f32_16x16x32_bf16 v[172:175], a[248:251], a[220:223], v[172:175]\n" \
"ds_read_b64_tr_b16 a[236:237], v35 offset:8192\n" \
"v_mfma_f32_16x16x32_bf16 v[168:171], a[180:183], a[156:159], v[168:171]\n" \
"ds_read_b64_tr_b16 a[238:239], v35 offset:9216\n" \
"v_mfma_f32_16x16x32_bf16 v[168:171], a[244:247], a[220:223], v[168:171]\n" \
"v_mfma_f32_16x16x32_bf16 v[164:167], a[176:179], a[156:159], v[164:167]\n" \
"v_mfma_f32_16x16x32_bf16 v[164:167], a[240:243], a[220:223], v[164:167]\n" \
"ds_read_b128 a[156:159], v28 offset:6144\n" \
"ds_read_b128 a[220:223], v29 offset:6144\n" \
"s_waitcnt lgkmcnt(15)\n" \
"v_mfma_f32_16x16x32_bf16 a[0:3], a[160:163], a[128:131], a[0:3]\n" \
"s_addk_i32 s84, 0x7c00\n" \
"v_mfma_f32_16x16x32_bf16 a[0:3], a[224:227], a[192:195], a[0:3]\n" \
"buffer_load_dwordx4 v27, s[72:75], s84 offen offset:2048 lds\n" \
"v_mfma_f32_16x16x32_bf16 a[4:7], a[164:167], a[128:131], a[4:7]\n" \
"ds_read_b64_tr_b16 a[176:177], v36 offset:0\n" \
"s_waitcnt lgkmcnt(13)\n" \
"v_mfma_f32_16x16x32_bf16 a[4:7], a[228:231], a[192:195], a[4:7]\n" \
"s_addk_i32 s84, 0x7c00\n" \
"s_waitcnt lgkmcnt(11)\n" \
"v_mfma_f32_16x16x32_bf16 a[8:11], a[168:171], a[128:131], a[8:11]\n" \
"buffer_load_dwordx4 v27, s[72:75], s84 offen offset:3072 lds\n" \
"s_waitcnt lgkmcnt(9)\n" \
"v_mfma_f32_16x16x32_bf16 a[8:11], a[232:235], a[192:195], a[8:11]\n" \
"ds_read_b64_tr_b16 a[178:179], v36 offset:1024\n" \
"s_waitcnt lgkmcnt(6)\n" \
"v_mfma_f32_16x16x32_bf16 a[12:15], a[172:175], a[128:131], a[12:15]\n" \
"ds_read_b64_tr_b16 a[240:241], v36 offset:8192\n" \
"s_waitcnt lgkmcnt(5)\n" \
"v_mfma_f32_16x16x32_bf16 a[12:15], a[236:239], a[192:195], a[12:15]\n" \
"v_mfma_f32_16x16x32_bf16 a[28:31], a[172:175], a[132:135], a[28:31]\n" \
"v_mfma_f32_16x16x32_bf16 a[28:31], a[236:239], a[196:199], a[28:31]\n" \
"ds_read_b64_tr_b16 a[242:243], v36 offset:9216\n" \
"v_mfma_f32_16x16x32_bf16 a[24:27], a[168:171], a[132:135], a[24:27]\n" \
"v_mfma_f32_16x16x32_bf16 a[24:27], a[232:235], a[196:199], a[24:27]\n" \
"v_mfma_f32_16x16x32_bf16 a[20:23], a[164:167], a[132:135], a[20:23]\n" \
"v_mfma_f32_16x16x32_bf16 a[20:23], a[228:231], a[196:199], a[20:23]\n" \
"ds_read_b64_tr_b16 a[180:181], v37 offset:0\n" \
"v_mfma_f32_16x16x32_bf16 a[16:19], a[160:163], a[132:135], a[16:19]\n" \
"v_mfma_f32_16x16x32_bf16 a[16:19], a[224:227], a[196:199], a[16:19]\n" \
"v_mfma_f32_16x16x32_bf16 a[32:35], a[160:163], a[136:139], a[32:35]\n" \
"ds_read_b64_tr_b16 a[182:183], v37 offset:1024\n" \
"v_mfma_f32_16x16x32_bf16 a[32:35], a[224:227], a[200:203], a[32:35]\n" \
"v_mfma_f32_16x16x32_bf16 a[36:39], a[164:167], a[136:139], a[36:39]\n" \
"v_mfma_f32_16x16x32_bf16 a[36:39], a[228:231], a[200:203], a[36:39]\n" \
"buffer_store_dwordx4 v[200:203], v20, s[80:83], s35 offen sc1\n" \
".fill (L_pb + 12000 - .) / 4, 4, 0xBF800000\n" \
"L_pp3:\n" \
"s_cbranch_execz L_pret\n" \
"v_mfma_f32_16x16x32_bf16 a[40:43], a[168:171], a[136:139], a[40:43]\n" \
"s_mov_b32 m0, s32\n" \
"ds_read_b64_tr_b16 a[244:245], v37 offset:8192\n" \
"v_mfma_f32_16x16x32_bf16 a[40:43], a[232:235], a[200:203], a[40:43]\n" \
"s_mov_b32 exec_lo, 0x0\n" \
"s_mov_b32 exec_hi, 0xffffffff\n" \
"global_load_lds_dword v[188:189], off\n" \
"s_mov_b64 exec, -1\n" \
"s_nop 0\n" \
"v_mfma_f32_16x16x32_bf16 a[44:47], a[172:175], a[136:139], a[44:47]\n" \
"ds_read_b64_tr_b16 a[246:247], v37 offset:9216\n" \
"v_mfma_f32_16x16x32_bf16 a[44:47], a[236:239], a[200:203], a[44:47]\n" \
"v_mfma_f32_16x16x32_bf16 a[60:63], a[172:175], a[140:143], a[60:63]\n" \
"v_mfma_f32_16x16x32_bf16 a[60:63], a[236:239], a[204:207], a[60:63]\n" \
"v_mfma_f32_16x16x32_bf16 a[56:59], a[168:171], a[140:143], a[56:59]\n" \
"ds_read_b64_tr_b16 a[184:185], v38 offset:0\n" \
"v_mfma_f32_16x16x32_bf16 a[56:59], a[232:235], a[204:207], a[56:59]\n" \
"v_mfma_f32_16x16x32_bf16 a[52:55], a[164:167], a[140:143], a[52:55]\n" \
"buffer_store_dwordx4 v[204:207], v20, s[80:83], s88 offen sc1\n" \
"v_mfma_f32_16x16x32_bf16 a[52:55], a[228:231], a[204:207], a[52:55]\n" \
"ds_read_b64_tr_b16 a[186:187], v38 offset:1024\n" \
"v_mfma_f32_16x16x32_bf16 a[48:51], a[160:163], a[140:143], a[48:51]\n" \
"v_mfma_f32_16x16x32_bf16 a[48:51], a[224:227], a[204:207], a[48:51]\n" \
"v_mfma_f32_16x16x32_bf16 a[64:67], a[160:163], a[144:147], a[64:67]\n" \
"ds_read_b64_tr_b16 a[248:249], v38 offset:8192\n" \
"v_mfma_f32_16x16x32_bf16 a[64:67], a[224:227], a[208:211], a[64:67]\n" \
"v_mfma_f32_16x16x32_bf16 a[68:71], a[164:167], a[144:147], a[68:71]\n" \
"v_mfma_f32_16x16x32_bf16 a[68:71], a[228:231], a[208:211], a[68:71]\n" \
"ds_read_b64_tr_b16 a[250:251], v38 offset:9216\n" \
"v_mfma_f32_16x16x32_bf16 a[72:75], a[168:171], a[144:147], a[72:75]\n" \
"v_mfma_f32_16x16x32_bf16 a[72:75], a[232:235], a[208:211], a[72:75]\n" \
"v_mfma_f32_16x16x32_bf16 a[76:79], a[172:175], a[144:147], a[76:79]\n" \
"buffer_store_dwordx4 v[208:211], v20, s[80:83], s36 offen sc1\n" \
"v_mfma_f32_16x16x32_bf16 a[76:79], a[236:239], a[208:211], a[76:79]\n" \
"ds_read_b64_tr_b16 a[188:189], v39 offset:0\n" \
"v_mfma_f32_16x16x32_bf16 a[92:95], a[172:175], a[148:151], a[92:95]\n" \
"v_mfma_f32_16x16x32_bf16 a[92:95], a[236:239], a[212:215], a[92:95]\n" \
"v_mfma_f32_16x16x32_bf16 a[88:91], a[168:171], a[148:151], a[88:91]\n" \
"ds_read_b64_tr_b16 a[190:191], v39 offset:1024\n" \
"v_mfma_f32_16x16x32_bf16 a[88:91], a[232:235], a[212:215], a[88:91]\n" \
"v_mfma_f32_16x16x32_bf16 a[84:87], a[164:167], a[148:151], a[84:87]\n" \
"v_mfma_f32_16x16x32_bf16 a[84:87], a[228:231], a[212:215], a[84:87]\n" \
"ds_read_b64_tr_b16 a[252:253], v39 offset:8192\n" \
"v_mfma_f32_16x16x32_bf16 a[80:83], a[160:163], a[148:151], a[80:83]\n" \
"v_mfma_f32_16x16x32_bf16 a[80:83], a[224:227], a[212:215], a[80:83]\n" \
"v_mfma_f32_16x16x32_bf16 a[96:99], a[160:163], a[152:155], a[96:99]\n" \
"ds_read_b64_tr_b16 a[254:255], v39 offset:9216\n" \
"v_mfma_f32_16x16x32_bf16 a[96:99], a[224:227], a[216:219], a[96:99]\n" \
"v_mfma_f32_16x16x32_bf16 a[100:103], a[164:167], a[152:155], a[100:103]\n" \
"v_mfma_f32_16x16x32_bf16 a[100:103], a[228:231], a[216:219], a[100:103]\n" \
"v_mfma_f32_16x16x32_bf16 a[104:107], a[168:171], a[152:155], a[104:107]\n" \
"v_mfma_f32_16x16x32_bf16 a[104:107], a[232:235], a[216:219], a[104:107]\n" \
"v_mfma_f32_16x16x32_bf16 a[108:111], a[172:175], a[152:155], a[108:111]\n" \
"v_mfma_f32_16x16x32_bf16 a[108:111], a[236:239], a[216:219], a[108:111]\n" \
"s_waitcnt lgkmcnt(15)\n" \
"v_mfma_f32_16x16x32_bf16 a[124:127], a[172:175], a[156:159], a[124:127]\n" \
"v_mfma_f32_16x16x32_bf16 a[124:127], a[236:239], a[220:223], a[124:127]\n" \
"v_mfma_f32_16x16x32_bf16 a[120:123], a[168:171], a[156:159], a[120:123]\n" \
"v_mfma_f32_16x16x32_bf16 a[120:123], a[232:235], a[220:223], a[120:123]\n" \
"v_mfma_f32_16x16x32_bf16 a[116:119], a[164:167], a[156:159], a[116:119]\n" \
"v_mfma_f32_16x16x32_bf16 a[116:119], a[228:231], a[220:223], a[116:119]\n" \
"v_mfma_f32_16x16x32_bf16 a[112:115], a[160:163], a[156:159], a[112:115]\n" \
"s_waitcnt vmcnt(4) lgkmcnt(0)\n" \
"v_mfma_f32_16x16x32_bf16 a[112:115], a[224:227], a[220:223], a[112:115]\n" \
"s_barrier\n" \
"v_mfma_f32_16x16x32_bf16 v[52:55], a[176:179], a[128:131], v[52:55]\n" \
"s_add_u32 m0, s30, 0x0\n" \
"s_movk_i32 s84, 0x300\n" \
"v_mfma_f32_16x16x32_bf16 v[52:55], a[240:243], a[192:195], v[52:55]\n" \
"buffer_load_dwordx4 v22, s[64:67], s84 offen offset:0 lds\n" \
"v_mfma_f32_16x16x32_bf16 v[56:59], a[180:183], a[128:131], v[56:59]\n" \
"ds_read_b64_tr_b16 a[160:161], v32 offset:32768\n" \
"v_mfma_f32_16x16x32_bf16 v[56:59], a[244:247], a[192:195], v[56:59]\n" \
"s_movk_i32 s84, 0x1f00\n" \
"v_mfma_f32_16x16x32_bf16 v[60:63], a[184:187], a[128:131], v[60:63]\n" \
"buffer_load_dwordx4 v23, s[64:67], s84 offen offset:1024 lds\n" \
"v_mfma_f32_16x16x32_bf16 v[60:63], a[248:251], a[192:195], v[60:63]\n" \
"ds_read_b64_tr_b16 a[162:163], v32 offset:33792\n" \
"v_mfma_f32_16x16x32_bf16 v[64:67], a[188:191], a[128:131], v[64:67]\n" \
"ds_read_b64_tr_b16 a[224:225], v32 offset:40960\n" \
"v_mfma_f32_16x16x32_bf16 v[64:67], a[252:255], a[192:195], v[64:67]\n" \
"ds_read_b128 a[128:131], v28 offset:49152\n" \
"v_mfma_f32_16x16x32_bf16 v[80:83], a[188:191], a[132:135], v[80:83]\n" \
"ds_read_b128 a[192:195], v29 offset:49152\n" \
"v_mfma_f32_16x16x32_bf16 v[80:83], a[252:255], a[196:199], v[80:83]\n" \
"s_movk_i32 s84, 0x3b00\n" \
"v_mfma_f32_16x16x32_bf16 v[76:79], a[184:187], a[132:135], v[76:79]\n" \
"buffer_load_dwordx4 v22, s[64:67], s84 offen offset:2048 lds\n" \
"v_mfma_f32_16x16x32_bf16 v[76:79], a[248:251], a[196:199], v[76:79]\n" \
"s_movk_i32 s84, 0x5700\n" \
"v_mfma_f32_16x16x32_bf16 v[72:75], a[180:183], a[132:135], v[72:75]\n" \
"buffer_load_dwordx4 v23, s[64:67], s84 offen offset:3072 lds\n" \
"v_mfma_f32_16x16x32_bf16 v[72:75], a[244:247], a[196:199], v[72:75]\n" \
"s_add_u32 m0, s31, 0x0\n" \
"s_mov_b32 s84, 0x300000\n" \
"v_mfma_f32_16x16x32_bf16 v[68:71], a[176:179], a[132:135], v[68:71]\n" \
"buffer_load_dwordx4 v26, s[68:71], s84 offen offset:0 lds\n" \
"v_mfma_f32_16x16x32_bf16 v[68:71], a[240:243], a[196:199], v[68:71]\n" \
"ds_read_b128 a[132:135], v28 offset:51200\n" \
"v_mfma_f32_16x16x32_bf16 v[84:87], a[176:179], a[136:139], v[84:87]\n" \
"ds_read_b128 a[196:199], v29 offset:51200\n" \
"v_mfma_f32_16x16x32_bf16 v[84:87], a[240:243], a[200:203], v[84:87]\n" \
"s_addk_i32 s84, 0x7c00\n" \
"v_mfma_f32_16x16x32_bf16 v[88:91], a[180:183], a[136:139], v[88:91]\n" \
"buffer_load_dwordx4 v26, s[68:71], s84 offen offset:1024 lds\n" \
"v_mfma_f32_16x16x32_bf16 v[88:91], a[244:247], a[200:203], v[88:91]\n" \
"ds_read_b64_tr_b16 a[226:227], v32 offset:41984\n" \
"v_mfma_f32_16x16x32_bf16 v[92:95], a[184:187], a[136:139], v[92:95]\n" \
"s_addk_i32 s84, 0x7c00\n" \
"v_mfma_f32_16x16x32_bf16 v[92:95], a[248:251], a[200:203], v[92:95]\n" \
"buffer_load_dwordx4 v27, s[68:71], s84 offen offset:2048 lds\n" \
"v_mfma_f32_16x16x32_bf16 v[96:99], a[188:191], a[136:139], v[96:99]\n" \
"ds_read_b64_tr_b16 a[164:165], v33 offset:32768\n" \
"v_mfma_f32_16x16x32_bf16 v[96:99], a[252:255], a[200:203], v[96:99]\n" \
"ds_read_b128 a[136:139], v28 offset:53248\n" \
"v_mfma_f32_16x16x32_bf16 v[112:115], a[188:191], a[140:143], v[112:115]\n" \
"ds_read_b128 a[200:203], v29 offset:53248\n" \
"v_mfma_f32_16x16x32_bf16 v[112:115], a[252:255], a[204:207], v[112:115]\n" \
"s_addk_i32 s84, 0x7c00\n" \
"v_mfma_f32_16x16x32_bf16 v[108:111], a[184:187], a[140:143], v[108:111]\n" \
"buffer_load_dwordx4 v27, s[68:71], s84 offen offset:3072 lds\n" \
"v_mfma_f32_16x16x32_bf16 v[108:111], a[248:251], a[204:207], v[108:111]\n" \
"s_add_u32 m0, s30, 0x1000\n" \
"s_mov_b32 s84, 0x8300\n" \
"v_mfma_f32_16x16x32_bf16 v[104:107], a[180:183], a[140:143], v[104:107]\n" \
"buffer_load_dwordx4 v22, s[64:67], s84 offen offset:0 lds\n" \
"v_mfma_f32_16x16x32_bf16 v[104:107], a[244:247], a[204:207], v[104:107]\n" \
"ds_read_b64_tr_b16 a[166:167], v33 offset:33792\n" \
"v_mfma_f32_16x16x32_bf16 v[100:103], a[176:179], a[140:143], v[100:103]\n" \
"ds_read_b64_tr_b16 a[228:229], v33 offset:40960\n" \
"v_mfma_f32_16x16x32_bf16 v[100:103], a[240:243], a[204:207], v[100:103]\n" \
"ds_read_b128 a[140:143], v28 offset:55296\n" \
"v_mfma_f32_16x16x32_bf16 v[116:119], a[176:179], a[144:147], v[116:119]\n" \
"ds_read_b128 a[204:207], v29 offset:55296\n" \
"v_mfma_f32_16x16x32_bf16 v[116:119], a[240:243], a[208:211], v[116:119]\n" \
"s_addk_i32 s84, 0x1c00\n" \
"v_mfma_f32_16x16x32_bf16 v[120:123], a[180:183], a[144:147], v[120:123]\n" \
"buffer_load_dwordx4 v23, s[64:67], s84 offen offset:1024 lds\n" \
"v_mfma_f32_16x16x32_bf16 v[120:123], a[244:247], a[208:211], v[120:123]\n" \
"s_addk_i32 s84, 0x1c00\n" \
"v_mfma_f32_16x16x32_bf16 v[124:127], a[184:187], a[144:147], v[124:127]\n" \
"buffer_load_dwordx4 v22, s[64:67], s84 offen offset:2048 lds\n" \
"v_mfma_f32_16x16x32_bf16 v[124:127], a[248:251], a[208:211], v[124:127]\n" \
"s_addk_i32 s84, 0x1c00\n" \
"s_nop 0\n" \
"v_mfma_f32_16x16x32_bf16 v[128:131], a[188:191], a[144:147], v[128:131]\n" \
"buffer_load_dwordx4 v23, s[64:67], s84 offen offset:3072 lds\n" \
"v_mfma_f32_16x16x32_bf16 v[128:131], a[252:255], a[208:211], v[128:131]\n" \
"ds_read_b128 a[144:147], v28 offset:32768\n" \
"v_mfma_f32_16x16x32_bf16 v[144:147], a[188:191], a[148:151], v[144:147]\n" \
"ds_read_b128 a[208:211], v29 offset:32768\n" \
"v_mfma_f32_16x16x32_bf16 v[144:147], a[252:255], a[212:215], v[144:147]\n" \
"s_add_u32 m0, s31, 0x4000\n" \
"s_mov_b32 s84, 0x300000\n" \
"v_mfma_f32_16x16x32_bf16 v[140:143], a[184:187], a[148:151], v[140:143]\n" \
"buffer_load_dwordx4 v26, s[72:75], s84 offen offset:0 lds\n" \
"v_mfma_f32_16x16x32_bf16 v[140:143], a[248:251], a[212:215], v[140:143]\n" \
"ds_read_b64_tr_b16 a[230:231], v33 offset:41984\n" \
"v_mfma_f32_16x16x32_bf16 v[136:139], a[180:183], a[148:151], v[136:139]\n" \
"s_addk_i32 s84, 0x7c00\n" \
"s_nop 0\n" \
"v_mfma_f32_16x16x32_bf16 v[136:139], a[244:247], a[212:215], v[136:139]\n" \
"buffer_load_dwordx4 v26, s[72:75], s84 offen offset:1024 lds\n" \
"v_mfma_f32_16x16x32_bf16 v[132:135], a[176:179], a[148:151], v[132:135]\n" \
"ds_read_b64_tr_b16 a[168:169], v34 offset:32768\n" \
"v_mfma_f32_16x16x32_bf16 v[132:135], a[240:243], a[212:215], v[132:135]\n" \
"ds_read_b128 a[148:151], v28 offset:34816\n" \
"v_mfma_f32_16x16x32_bf16 v[148:151], a[176:179], a[152:155], v[148:151]\n" \
"ds_read_b128 a[212:215], v29 offset:34816\n" \
"v_mfma_f32_16x16x32_bf16 v[148:151], a[240:243], a[216:219], v[148:151]\n" \
"ds_read_b64_tr_b16 a[170:171], v34 offset:33792\n" \
"v_mfma_f32_16x16x32_bf16 v[152:155], a[180:183], a[152:155], v[152:155]\n" \
"ds_read_b64_tr_b16 a[232:233], v34 offset:40960\n" \
"v_mfma_f32_16x16x32_bf16 v[152:155], a[244:247], a[216:219], v[152:155]\n" \
"ds_read_b64_tr_b16 a[234:235], v34 offset:41984\n" \
"v_mfma_f32_16x16x32_bf16 v[156:159], a[184:187], a[152:155], v[156:159]\n" \
"ds_read_b64_tr_b16 a[172:173], v35 offset:32768\n" \
"v_mfma_f32_16x16x32_bf16 v[156:159], a[248:251], a[216:219], v[156:159]\n" \
"ds_read_b64_tr_b16 a[174:175], v35 offset:33792\n" \
"v_mfma_f32_16x16x32_bf16 v[160:163], a[188:191], a[152:155], v[160:163]\n" \
"ds_read_b64_tr_b16 a[236:237], v35 offset:40960\n" \
"v_mfma_f32_16x16x32_bf16 v[160:163], a[252:255], a[216:219], v[160:163]\n" \
"ds_read_b128 a[152:155], v28 offset:36864\n" \
"v_mfma_f32_16x16x32_bf16 v[176:179], a[188:191], a[156:159], v[176:179]\n" \
"ds_read_b128 a[216:219], v29 offset:36864\n" \
"v_mfma_f32_16x16x32_bf16 v[176:179], a[252:255], a[220:223], v[176:179]\n" \
"ds_read_b64_tr_b16 a[238:239], v35 offset:41984\n" \
"v_mfma_f32_16x16x32_bf16 v[172:175], a[184:187], a[156:159], v[172:175]\n" \
"v_mfma_f32_16x16x32_bf16 v[172:175], a[248:251], a[220:223], v[172:175]\n" \
"v_mfma_f32_16x16x32_bf16 v[168:171], a[180:183], a[156:159], v[168:171]\n" \
"v_mfma_f32_16x16x32_bf16 v[168:171], a[244:247], a[220:223], v[168:171]\n" \
"v_mfma_f32_16x16x32_bf16 v[164:167], a[176:179], a[156:159], v[164:167]\n" \
"v_mfma_f32_16x16x32_bf16 v[164:167], a[240:243], a[220:223], v[164:167]\n" \
"ds_read_b128 a[156:159], v28 offset:38912\n" \
"ds_read_b128 a[220:223], v29 offset:38912\n" \
"s_waitcnt lgkmcnt(15)\n" \
"v_mfma_f32_16x16x32_bf16 a[0:3], a[160:163], a[128:131], a[0:3]\n" \
"s_addk_i32 s84, 0x7c00\n" \
"v_mfma_f32_16x16x32_bf16 a[0:3], a[224:227], a[192:195], a[0:3]\n" \
"buffer_load_dwordx4 v27, s[72:75], s84 offen offset:2048 lds\n" \
"v_mfma_f32_16x16x32_bf16 a[4:7], a[164:167], a[128:131], a[4:7]\n" \
"ds_read_b64_tr_b16 a[176:177], v36 offset:32768\n" \
"s_waitcnt lgkmcnt(15)\n" \
"v_mfma_f32_16x16x32_bf16 a[4:7], a[228:231], a[192:195], a[4:7]\n" \
"s_addk_i32 s84, 0x7c00\n" \
"s_waitcnt lgkmcnt(11)\n" \
"v_mfma_f32_16x16x32_bf16 a[8:11], a[168:171], a[128:131], a[8:11]\n" \
"buffer_load_dwordx4 v27, s[72:75], s84 offen offset:3072 lds\n" \
"s_waitcnt lgkmcnt(9)\n" \
"v_mfma_f32_16x16x32_bf16 a[8:11], a[232:235], a[192:195], a[8:11]\n" \
"ds_read_b64_tr_b16 a[178:179], v36 offset:33792\n" \
"s_waitcnt lgkmcnt(8)\n" \
"v_mfma_f32_16x16x32_bf16 a[12:15], a[172:175], a[128:131], a[12:15]\n" \
"ds_read_b64_tr_b16 a[240:241], v36 offset:40960\n" \
"s_waitcnt lgkmcnt(5)\n" \
"v_mfma_f32_16x16x32_bf16 a[12:15], a[236:239], a[192:195], a[12:15]\n" \
"v_mfma_f32_16x16x32_bf16 a[28:31], a[172:175], a[132:135], a[28:31]\n" \
"v_mfma_f32_16x16x32_bf16 a[28:31], a[236:239], a[196:199], a[28:31]\n" \
"ds_read_b64_tr_b16 a[242:243], v36 offset:41984\n" \
"v_mfma_f32_16x16x32_bf16 a[24:27], a[168:171], a[132:135], a[24:27]\n" \
"v_mfma_f32_16x16x32_bf16 a[24:27], a[232:235], a[196:199], a[24:27]\n" \
"v_mfma_f32_16x16x32_bf16 a[20:23], a[164:167], a[132:135], a[20:23]\n" \
"v_mfma_f32_16x16x32_bf16 a[20:23], a[228:231], a[196:199], a[20:23]\n" \
"ds_read_b64_tr_b16 a[180:181], v37 offset:32768\n" \
"v_mfma_f32_16x16x32_bf16 a[16:19], a[160:163], a[132:135], a[16:19]\n" \
"v_mfma_f32_16x16x32_bf16 a[16:19], a[224:227], a[196:199], a[16:19]\n" \
"v_mfma_f32_16x16x32_bf16 a[32:35], a[160:163], a[136:139], a[32:35]\n" \
"ds_read_b64_tr_b16 a[182:183], v37 offset:33792\n" \
"v_mfma_f32_16x16x32_bf16 a[32:35], a[224:227], a[200:203], a[32:35]\n" \
"v_mfma_f32_16x16x32_bf16 a[36:39], a[164:167], a[136:139], a[36:39]\n" \
"v_lshlrev_b32_e32 v45, 3, v20\n" \
"v_add_u32_e32 v45, s31, v45\n" \
"v_mfma_f32_16x16x32_bf16 a[36:39], a[228:231], a[200:203], a[36:39]\n" \
"s_getpc_b64 s[86:87]\n" \
"s_sub_u32 s86, s86, 0xf800\n" \
"s_subb_u32 s87, s87, 0\n" \
"s_mov_b32 m0, s32\n" \
"s_nop 0\n" \
"v_mfma_f32_16x16x32_bf16 a[40:43], a[168:171], a[136:139], a[40:43]\n" \
"global_load_lds_dword v45, s[86:87]\n" \
"v_mfma_f32_16x16x32_bf16 a[40:43], a[232:235], a[200:203], a[40:43]\n" \
"ds_read_b64_tr_b16 a[244:245], v37 offset:40960\n" \
"v_mfma_f32_16x16x32_bf16 a[44:47], a[172:175], a[136:139], a[44:47]\n" \
"ds_read_b64_tr_b16 a[246:247], v37 offset:41984\n" \
"v_mfma_f32_16x16x32_bf16 a[44:47], a[236:239], a[200:203], a[44:47]\n" \
"v_mfma_f32_16x16x32_bf16 a[60:63], a[172:175], a[140:143], a[60:63]\n" \
"v_mfma_f32_16x16x32_bf16 a[60:63], a[236:239], a[204:207], a[60:63]\n" \
"v_mfma_f32_16x16x32_bf16 a[56:59], a[168:171], a[140:143], a[56:59]\n" \
"ds_read_b64_tr_b16 a[184:185], v38 offset:32768\n" \
"v_mfma_f32_16x16x32_bf16 a[56:59], a[232:235], a[204:207], a[56:59]\n" \
"v_mfma_f32_16x16x32_bf16 a[52:55], a[164:167], a[140:143], a[52:55]\n" \
"v_mfma_f32_16x16x32_bf16 a[52:55], a[228:231], a[204:207], a[52:55]\n" \
"ds_read_b64_tr_b16 a[186:187], v38 offset:33792\n" \
"v_mfma_f32_16x16x32_bf16 a[48:51], a[160:163], a[140:143], a[48:51]\n" \
"v_mfma_f32_16x16x32_bf16 a[48:51], a[224:227], a[204:207], a[48:51]\n" \
"v_mfma_f32_16x16x32_bf16 a[64:67], a[160:163], a[144:147], a[64:67]\n" \
"ds_read_b64_tr_b16 a[248:249], v38 offset:40960\n" \
"v_mfma_f32_16x16x32_bf16 a[64:67], a[224:227], a[208:211], a[64:67]\n" \
"v_mfma_f32_16x16x32_bf16 a[68:71], a[164:167], a[144:147], a[68:71]\n" \
"v_mfma_f32_16x16x32_bf16 a[68:71], a[228:231], a[208:211], a[68:71]\n" \
"ds_read_b64_tr_b16 a[250:251], v38 offset:41984\n" \
"v_mfma_f32_16x16x32_bf16 a[72:75], a[168:171], a[144:147], a[72:75]\n" \
"v_mfma_f32_16x16x32_bf16 a[72:75], a[232:235], a[208:211], a[72:75]\n" \
"v_mfma_f32_16x16x32_bf16 a[76:79], a[172:175], a[144:147], a[76:79]\n" \
"v_mfma_f32_16x16x32_bf16 a[76:79], a[236:239], a[208:211], a[76:79]\n" \
"ds_read_b64_tr_b16 a[188:189], v39 offset:32768\n" \
"v_mfma_f32_16x16x32_bf16 a[92:95], a[172:175], a[148:151], a[92:95]\n" \
"v_mfma_f32_16x16x32_bf16 a[92:95], a[236:239], a[212:215], a[92:95]\n" \
"v_mfma_f32_16x16x32_bf16 a[88:91], a[168:171], a[148:151], a[88:91]\n" \
"ds_read_b64_tr_b16 a[190:191], v39 offset:33792\n" \
"v_mfma_f32_16x16x32_bf16 a[88:91], a[232:235], a[212:215], a[88:91]\n" \
"v_mfma_f32_16x16x32_bf16 a[84:87], a[164:167], a[148:151], a[84:87]\n" \
"v_mfma_f32_16x16x32_bf16 a[84:87], a[228:231], a[212:215], a[84:87]\n" \
"ds_read_b64_tr_b16 a[252:253], v39 offset:40960\n" \
"v_mfma_f32_16x16x32_bf16 a[80:83], a[160:163], a[148:151], a[80:83]\n" \
"v_mfma_f32_16x16x32_bf16 a[80:83], a[224:227], a[212:215], a[80:83]\n" \
"v_mfma_f32_16x16x32_bf16 a[96:99], a[160:163], a[152:155], a[96:99]\n" \
"ds_read_b64_tr_b16 a[254:255], v39 offset:41984\n" \
"v_mfma_f32_16x16x32_bf16 a[96:99], a[224:227], a[216:219], a[96:99]\n" \
"v_mfma_f32_16x16x32_bf16 a[100:103], a[164:167], a[152:155], a[100:103]\n" \
"v_mfma_f32_16x16x32_bf16 a[100:103], a[228:231], a[216:219], a[100:103]\n" \
"v_mfma_f32_16x16x32_bf16 a[104:107], a[168:171], a[152:155], a[104:107]\n" \
"v_mfma_f32_16x16x32_bf16 a[104:107], a[232:235], a[216:219], a[104:107]\n" \
"v_mfma_f32_16x16x32_bf16 a[108:111], a[172:175], a[152:155], a[108:111]\n" \
"v_mfma_f32_16x16x32_bf16 a[108:111], a[236:239], a[216:219], a[108:111]\n" \
"s_waitcnt lgkmcnt(15)\n" \
"v_mfma_f32_16x16x32_bf16 a[124:127], a[172:175], a[156:159], a[124:127]\n" \
"v_mfma_f32_16x16x32_bf16 a[124:127], a[236:239], a[220:223], a[124:127]\n" \
"v_mfma_f32_16x16x32_bf16 a[120:123], a[168:171], a[156:159], a[120:123]\n" \
"v_mfma_f32_16x16x32_bf16 a[120:123], a[232:235], a[220:223], a[120:123]\n" \
"v_mfma_f32_16x16x32_bf16 a[116:119], a[164:167], a[156:159], a[116:119]\n" \
"v_mfma_f32_16x16x32_bf16 a[116:119], a[228:231], a[220:223], a[116:119]\n" \
"v_mfma_f32_16x16x32_bf16 a[112:115], a[160:163], a[156:159], a[112:115]\n" \
"s_waitcnt vmcnt(1) lgkmcnt(0)\n" \
"v_mfma_f32_16x16x32_bf16 a[112:115], a[224:227], a[220:223], a[112:115]\n" \
"s_barrier\n" \
"v_mov_b32_e32 v45, s62\n" \
"s_mov_b64 exec, 1\n" \
"buffer_store_dword v45, off, s[80:83], s37 offset:128 sc1\n" \
"s_mov_b64 exec, -1\n" \
"v_mfma_f32_16x16x32_bf16 v[52:55], a[176:179], a[128:131], v[52:55]\n" \
"s_add_u32 m0, s30, 0x8000\n" \
"s_movk_i32 s84, 0x380\n" \
"v_mfma_f32_16x16x32_bf16 v[52:55], a[240:243], a[192:195], v[52:55]\n" \
"buffer_load_dwordx4 v22, s[64:67], s84 offen offset:0 lds\n" \
"v_mfma_f32_16x16x32_bf16 v[56:59], a[180:183], a[128:131], v[56:59]\n" \
"ds_read_b64_tr_b16 a[160:161], v32 offset:0\n" \
"v_mfma_f32_16x16x32_bf16 v[56:59], a[244:247], a[192:195], v[56:59]\n" \
"s_movk_i32 s84, 0x1f80\n" \
"v_mfma_f32_16x16x32_bf16 v[60:63], a[184:187], a[128:131], v[60:63]\n" \
"buffer_load_dwordx4 v23, s[64:67], s84 offen offset:1024 lds\n" \
"v_mfma_f32_16x16x32_bf16 v[60:63], a[248:251], a[192:195], v[60:63]\n" \
"ds_read_b64_tr_b16 a[162:163], v32 offset:1024\n" \
"v_mfma_f32_16x16x32_bf16 v[64:67], a[188:191], a[128:131], v[64:67]\n" \
"ds_read_b64_tr_b16 a[224:225], v32 offset:8192\n" \
"v_mfma_f32_16x16x32_bf16 v[64:67], a[252:255], a[192:195], v[64:67]\n" \
"ds_read_b128 a[128:131], v28 offset:16384\n" \
"v_mfma_f32_16x16x32_bf16 v[80:83], a[188:191], a[132:135], v[80:83]\n" \
"ds_read_b128 a[192:195], v29 offset:16384\n" \
"v_mfma_f32_16x16x32_bf16 v[80:83], a[252:255], a[196:199], v[80:83]\n" \
"s_movk_i32 s84, 0x3b80\n" \
"v_mfma_f32_16x16x32_bf16 v[76:79], a[184:187], a[132:135], v[76:79]\n" \
"buffer_load_dwordx4 v22, s[64:67], s84 offen offset:2048 lds\n" \
"v_mfma_f32_16x16x32_bf16 v[76:79], a[248:251], a[196:199], v[76:79]\n" \
"s_movk_i32 s84, 0x5780\n" \
"v_mfma_f32_16x16x32_bf16 v[72:75], a[180:183], a[132:135], v[72:75]\n" \
"buffer_load_dwordx4 v23, s[64:67], s84 offen offset:3072 lds\n" \
"v_mfma_f32_16x16x32_bf16 v[72:75], a[244:247], a[196:199], v[72:75]\n" \
"s_add_u32 m0, s31, 0x8000\n" \
"s_mov_b32 s84, 0x380000\n" \
"v_mfma_f32_16x16x32_bf16 v[68:71], a[176:179], a[132:135], v[68:71]\n" \
"buffer_load_dwordx4 v26, s[68:71], s84 offen offset:0 lds\n" \
"v_mfma_f32_16x16x32_bf16 v[68:71], a[240:243], a[196:199], v[68:71]\n" \
"ds_read_b128 a[132:135], v28 offset:18432\n" \
"v_mfma_f32_16x16x32_bf16 v[84:87], a[176:179], a[136:139], v[84:87]\n" \
"ds_read_b128 a[196:199], v29 offset:18432\n" \
"v_mfma_f32_16x16x32_bf16 v[84:87], a[240:243], a[200:203], v[84:87]\n" \
"s_addk_i32 s84, 0x7c00\n" \
"v_mfma_f32_16x16x32_bf16 v[88:91], a[180:183], a[136:139], v[88:91]\n" \
"buffer_load_dwordx4 v26, s[68:71], s84 offen offset:1024 lds\n" \
"v_mfma_f32_16x16x32_bf16 v[88:91], a[244:247], a[200:203], v[88:91]\n" \
"ds_read_b64_tr_b16 a[226:227], v32 offset:9216\n" \
"v_mfma_f32_16x16x32_bf16 v[92:95], a[184:187], a[136:139], v[92:95]\n" \
"s_addk_i32 s84, 0x7c00\n" \
"v_mfma_f32_16x16x32_bf16 v[92:95], a[248:251], a[200:203], v[92:95]\n" \
"buffer_load_dwordx4 v27, s[68:71], s84 offen offset:2048 lds\n" \
"v_mfma_f32_16x16x32_bf16 v[96:99], a[188:191], a[136:139], v[96:99]\n" \
"ds_read_b64_tr_b16 a[164:165], v33 offset:0\n" \
"v_mfma_f32_16x16x32_bf16 v[96:99], a[252:255], a[200:203], v[96:99]\n" \
"ds_read_b128 a[136:139], v28 offset:20480\n" \
"v_mfma_f32_16x16x32_bf16 v[112:115], a[188:191], a[140:143], v[112:115]\n" \
"ds_read_b128 a[200:203], v29 offset:20480\n" \
"v_mfma_f32_16x16x32_bf16 v[112:115], a[252:255], a[204:207], v[112:115]\n" \
"s_addk_i32 s84, 0x7c00\n" \
"s_nop 0\n" \
"v_mfma_f32_16x16x32_bf16 v[108:111], a[184:187], a[140:143], v[108:111]\n" \
"buffer_load_dwordx4 v27, s[68:71], s84 offen offset:3072 lds\n" \
"v_mfma_f32_16x16x32_bf16 v[108:111], a[248:251], a[204:207], v[108:111]\n" \
"s_add_u32 m0, s30, 0x9000\n" \
"s_mov_b32 s84, 0x8380\n" \
"v_mfma_f32_16x16x32_bf16 v[104:107], a[180:183], a[140:143], v[104:107]\n" \
"buffer_load_dwordx4 v22, s[64:67], s84 offen offset:0 lds\n" \
"v_mfma_f32_16x16x32_bf16 v[104:107], a[244:247], a[204:207], v[104:107]\n" \
"ds_read_b64_tr_b16 a[166:167], v33 offset:1024\n" \
"v_mfma_f32_16x16x32_bf16 v[100:103], a[176:179], a[140:143], v[100:103]\n" \
"ds_read_b64_tr_b16 a[228:229], v33 offset:8192\n" \
"v_mfma_f32_16x16x32_bf16 v[100:103], a[240:243], a[204:207], v[100:103]\n" \
"ds_read_b128 a[140:143], v28 offset:22528\n" \
"v_mfma_f32_16x16x32_bf16 v[116:119], a[176:179], a[144:147], v[116:119]\n" \
"ds_read_b128 a[204:207], v29 offset:22528\n" \
"v_mfma_f32_16x16x32_bf16 v[116:119], a[240:243], a[208:211], v[116:119]\n" \
"s_addk_i32 s84, 0x1c00\n" \
"v_mfma_f32_16x16x32_bf16 v[120:123], a[180:183], a[144:147], v[120:123]\n" \
"buffer_load_dwordx4 v23, s[64:67], s84 offen offset:1024 lds\n" \
"v_mfma_f32_16x16x32_bf16 v[120:123], a[244:247], a[208:211], v[120:123]\n" \
"s_addk_i32 s84, 0x1c00\n" \
"v_mfma_f32_16x16x32_bf16 v[124:127], a[184:187], a[144:147], v[124:127]\n" \
"buffer_load_dwordx4 v22, s[64:67], s84 offen offset:2048 lds\n" \
"v_mfma_f32_16x16x32_bf16 v[124:127], a[248:251], a[208:211], v[124:127]\n" \
"s_addk_i32 s84, 0x1c00\n" \
"s_nop 0\n" \
"v_mfma_f32_16x16x32_bf16 v[128:131], a[188:191], a[144:147], v[128:131]\n" \
"buffer_load_dwordx4 v23, s[64:67], s84 offen offset:3072 lds\n" \
"v_mfma_f32_16x16x32_bf16 v[128:131], a[252:255], a[208:211], v[128:131]\n" \
"ds_read_b128 a[144:147], v28 offset:0\n" \
"v_mfma_f32_16x16x32_bf16 v[144:147], a[188:191], a[148:151], v[144:147]\n" \
"ds_read_b128 a[208:211], v29 offset:0\n" \
"v_mfma_f32_16x16x32_bf16 v[144:147], a[252:255], a[212:215], v[144:147]\n" \
"s_add_u32 m0, s31, 0xc000\n" \
"s_mov_b32 s84, 0x380000\n" \
"v_mfma_f32_16x16x32_bf16 v[140:143], a[184:187], a[148:151], v[140:143]\n" \
"buffer_load_dwordx4 v26, s[72:75], s84 offen offset:0 lds\n" \
"v_mfma_f32_16x16x32_bf16 v[140:143], a[248:251], a[212:215], v[140:143]\n" \
"ds_read_b64_tr_b16 a[230:231], v33 offset:9216\n" \
"v_mfma_f32_16x16x32_bf16 v[136:139], a[180:183], a[148:151], v[136:139]\n" \
"s_addk_i32 s84, 0x7c00\n" \
"s_nop 0\n" \
"v_mfma_f32_16x16x32_bf16 v[136:139], a[244:247], a[212:215], v[136:139]\n" \
"buffer_load_dwordx4 v26, s[72:75], s84 offen offset:1024 lds\n" \
"v_mfma_f32_16x16x32_bf16 v[132:135], a[176:179], a[148:151], v[132:135]\n" \
"ds_read_b64_tr_b16 a[168:169], v34 offset:0\n" \
"v_mfma_f32_16x16x32_bf16 v[132:135], a[240:243], a[212:215], v[132:135]\n" \
"ds_read_b128 a[148:151], v28 offset:2048\n" \
"v_mfma_f32_16x16x32_bf16 v[148:151], a[176:179], a[152:155], v[148:151]\n" \
"ds_read_b128 a[212:215], v29 offset:2048\n" \
"v_mfma_f32_16x16x32_bf16 v[148:151], a[240:243], a[216:219], v[148:151]\n" \
"ds_read_b64_tr_b16 a[170:171], v34 offset:1024\n" \
"v_mfma_f32_16x16x32_bf16 v[152:155], a[180:183], a[152:155], v[152:155]\n" \
"ds_read_b64_tr_b16 a[232:233], v34 offset:8192\n" \
"v_mfma_f32_16x16x32_bf16 v[152:155], a[244:247], a[216:219], v[152:155]\n" \
"ds_read_b64_tr_b16 a[234:235], v34 offset:9216\n" \
"v_mfma_f32_16x16x32_bf16 v[156:159], a[184:187], a[152:155], v[156:159]\n" \
"ds_read_b64_tr_b16 a[172:173], v35 offset:0\n" \
"v_mfma_f32_16x16x32_bf16 v[156:159], a[248:251], a[216:219], v[156:159]\n" \
"ds_read_b64_tr_b16 a[174:175], v35 offset:1024\n" \
"v_mfma_f32_16x16x32_bf16 v[160:163], a[188:191], a[152:155], v[160:163]\n" \
"ds_read_b64_tr_b16 a[236:237], v35 offset:8192\n" \
"v_mfma_f32_16x16x32_bf16 v[160:163], a[252:255], a[216:219], v[160:163]\n" \
"ds_read_b128 a[152:155], v28 offset:4096\n" \
"v_mfma_f32_16x16x32_bf16 v[176:179], a[188:191], a[156:159], v[176:179]\n" \
"ds_read_b128 a[216:219], v29 offset:4096\n" \
"v_mfma_f32_16x16x32_bf16 v[176:179], a[252:255], a[220:223], v[176:179]\n" \
"ds_read_b64_tr_b16 a[238:239], v35 offset:9216\n" \
"v_mfma_f32_16x16x32_bf16 v[172:175], a[184:187], a[156:159], v[172:175]\n" \
"v_mfma_f32_16x16x32_bf16 v[172:175], a[248:251], a[220:223], v[172:175]\n" \
"v_mfma_f32_16x16x32_bf16 v[168:171], a[180:183], a[156:159], v[168:171]\n" \
"v_mfma_f32_16x16x32_bf16 v[168:171], a[244:247], a[220:223], v[168:171]\n" \
"v_mfma_f32_16x16x32_bf16 v[164:167], a[176:179], a[156:159], v[164:167]\n" \
"v_mfma_f32_16x16x32_bf16 v[164:167], a[240:243], a[220:223], v[164:167]\n" \
"ds_read_b128 a[156:159], v28 offset:6144\n" \
"ds_read_b128 a[220:223], v29 offset:6144\n" \
"s_waitcnt lgkmcnt(15)\n" \
"v_mfma_f32_16x16x32_bf16 a[0:3], a[160:163], a[128:131], a[0:3]\n" \
"s_addk_i32 s84, 0x7c00\n" \
"v_mfma_f32_16x16x32_bf16 a[0:3], a[224:227], a[192:195], a[0:3]\n" \
"buffer_load_dwordx4 v27, s[72:75], s84 offen offset:2048 lds\n" \
"v_mfma_f32_16x16x32_bf16 a[4:7], a[164:167], a[128:131], a[4:7]\n" \
"ds_read_b64_tr_b16 a[176:177], v36 offset:0\n" \
"s_waitcnt lgkmcnt(15)\n" \
"v_mfma_f32_16x16x32_bf16 a[4:7], a[228:231], a[192:195], a[4:7]\n" \
"s_addk_i32 s84, 0x7c00\n" \
"s_waitcnt lgkmcnt(11)\n" \
"v_mfma_f32_16x16x32_bf16 a[8:11], a[168:171], a[128:131], a[8:11]\n" \
"buffer_load_dwordx4 v27, s[72:75], s84 offen offset:3072 lds\n" \
"s_waitcnt lgkmcnt(9)\n" \
"v_mfma_f32_16x16x32_bf16 a[8:11], a[232:235], a[192:195], a[8:11]\n" \
"ds_read_b64_tr_b16 a[178:179], v36 offset:1024\n" \
"s_waitcnt lgkmcnt(8)\n" \
"v_mfma_f32_16x16x32_bf16 a[12:15], a[172:175], a[128:131], a[12:15]\n" \
"ds_read_b64_tr_b16 a[240:241], v36 offset:8192\n" \
"s_waitcnt lgkmcnt(5)\n" \
"v_mfma_f32_16x16x32_bf16 a[12:15], a[236:239], a[192:195], a[12:15]\n" \
"v_mfma_f32_16x16x32_bf16 a[28:31], a[172:175], a[132:135], a[28:31]\n" \
"v_mfma_f32_16x16x32_bf16 a[28:31], a[236:239], a[196:199], a[28:31]\n" \
"ds_read_b64_tr_b16 a[242:243], v36 offset:9216\n" \
"v_mfma_f32_16x16x32_bf16 a[24:27], a[168:171], a[132:135], a[24:27]\n" \
"v_mfma_f32_16x16x32_bf16 a[24:27], a[232:235], a[196:199], a[24:27]\n" \
"v_mfma_f32_16x16x32_bf16 a[20:23], a[164:167], a[132:135], a[20:23]\n" \
"v_mfma_f32_16x16x32_bf16 a[20:23], a[228:231], a[196:199], a[20:23]\n" \
"ds_read_b64_tr_b16 a[180:181], v37 offset:0\n" \
"v_mfma_f32_16x16x32_bf16 a[16:19], a[160:163], a[132:135], a[16:19]\n" \
"v_mfma_f32_16x16x32_bf16 a[16:19], a[224:227], a[196:199], a[16:19]\n" \
"v_mfma_f32_16x16x32_bf16 a[32:35], a[160:163], a[136:139], a[32:35]\n" \
"v_bfe_u32 v44, v20, 2, 5\n" \
"ds_read_b64_tr_b16 a[182:183], v37 offset:1024\n" \
"v_mfma_f32_16x16x32_bf16 a[32:35], a[224:227], a[200:203], a[32:35]\n" \
"buffer_load_dword v44, v44, s[80:83], s38 offen offset:128 sc1\n" \
"v_mfma_f32_16x16x32_bf16 a[36:39], a[164:167], a[136:139], a[36:39]\n" \
"v_mfma_f32_16x16x32_bf16 a[36:39], a[228:231], a[200:203], a[36:39]\n" \
"ds_read_b64_tr_b16 a[244:245], v37 offset:8192\n" \
"v_mfma_f32_16x16x32_bf16 a[40:43], a[168:171], a[136:139], a[40:43]\n" \
"v_mfma_f32_16x16x32_bf16 a[40:43], a[232:235], a[200:203], a[40:43]\n" \
"v_mfma_f32_16x16x32_bf16 a[44:47], a[172:175], a[136:139], a[44:47]\n" \
"ds_read_b64_tr_b16 a[246:247], v37 offset:9216\n" \
"v_mfma_f32_16x16x32_bf16 a[44:47], a[236:239], a[200:203], a[44:47]\n" \
"v_mfma_f32_16x16x32_bf16 a[60:63], a[172:175], a[140:143], a[60:63]\n" \
"global_store_dwordx4 v49, v[0:3], s[28:29] offset:0 sc1\n" \
"v_mfma_f32_16x16x32_bf16 a[60:63], a[236:239], a[204:207], a[60:63]\n" \
"v_mfma_f32_16x16x32_bf16 a[56:59], a[168:171], a[140:143], a[56:59]\n" \
"ds_read_b64_tr_b16 a[184:185], v38 offset:0\n" \
"v_mfma_f32_16x16x32_bf16 a[56:59], a[232:235], a[204:207], a[56:59]\n" \
"v_mfma_f32_16x16x32_bf16 a[52:55], a[164:167], a[140:143], a[52:55]\n" \
"v_mfma_f32_16x16x32_bf16 a[52:55], a[228:231], a[204:207], a[52:55]\n" \
"ds_read_b64_tr_b16 a[186:187], v38 offset:1024\n" \
"v_mfma_f32_16x16x32_bf16 a[48:51], a[160:163], a[140:143], a[48:51]\n" \
"v_mfma_f32_16x16x32_bf16 a[48:51], a[224:227], a[204:207], a[48:51]\n" \
"v_mfma_f32_16x16x32_bf16 a[64:67], a[160:163], a[144:147], a[64:67]\n" \
"global_store_dwordx4 v49, v[0:3], s[28:29] offset:1024 sc1\n" \
"v_mfma_f32_16x16x32_bf16 a[64:67], a[224:227], a[208:211], a[64:67]\n" \
"ds_read_b64_tr_b16 a[248:249], v38 offset:8192\n" \
"v_mfma_f32_16x16x32_bf16 a[68:71], a[164:167], a[144:147], a[68:71]\n" \
"v_mfma_f32_16x16x32_bf16 a[68:71], a[228:231], a[208:211], a[68:71]\n" \
"ds_read_b64_tr_b16 a[250:251], v38 offset:9216\n" \
"v_mfma_f32_16x16x32_bf16 a[72:75], a[168:171], a[144:147], a[72:75]\n" \
"v_mfma_f32_16x16x32_bf16 a[72:75], a[232:235], a[208:211], a[72:75]\n" \
"v_mfma_f32_16x16x32_bf16 a[76:79], a[172:175], a[144:147], a[76:79]\n" \
"v_mfma_f32_16x16x32_bf16 a[76:79], a[236:239], a[208:211], a[76:79]\n" \
"ds_read_b64_tr_b16 a[188:189], v39 offset:0\n" \
"v_mfma_f32_16x16x32_bf16 a[92:95], a[172:175], a[148:151], a[92:95]\n" \
"s_mov_b64 exec, 0xffff\n" \
"global_store_dwordx4 v49, v[0:3], s[28:29] offset:2048 sc1\n" \
"s_mov_b64 exec, -1\n" \
"s_nop 0\n" \
"v_mfma_f32_16x16x32_bf16 a[92:95], a[236:239], a[212:215], a[92:95]\n" \
"v_mfma_f32_16x16x32_bf16 a[88:91], a[168:171], a[148:151], a[88:91]\n" \
"ds_read_b64_tr_b16 a[190:191], v39 offset:1024\n" \
"v_mfma_f32_16x16x32_bf16 a[88:91], a[232:235], a[212:215], a[88:91]\n" \
"v_mfma_f32_16x16x32_bf16 a[84:87], a[164:167], a[148:151], a[84:87]\n" \
"v_mfma_f32_16x16x32_bf16 a[84:87], a[228:231], a[212:215], a[84:87]\n" \
"ds_read_b64_tr_b16 a[252:253], v39 offset:8192\n" \
"v_mfma_f32_16x16x32_bf16 a[80:83], a[160:163], a[148:151], a[80:83]\n" \
"v_mfma_f32_16x16x32_bf16 a[80:83], a[224:227], a[212:215], a[80:83]\n" \
"v_mfma_f32_16x16x32_bf16 a[96:99], a[160:163], a[152:155], a[96:99]\n" \
"ds_read_b64_tr_b16 a[254:255], v39 offset:9216\n" \
"v_mfma_f32_16x16x32_bf16 a[96:99], a[224:227], a[216:219], a[96:99]\n" \
"v_mfma_f32_16x16x32_bf16 a[100:103], a[164:167], a[152:155], a[100:103]\n" \
"v_mfma_f32_16x16x32_bf16 a[100:103], a[228:231], a[216:219], a[100:103]\n" \
"v_mfma_f32_16x16x32_bf16 a[104:107], a[168:171], a[152:155], a[104:107]\n" \
"v_mfma_f32_16x16x32_bf16 a[104:107], a[232:235], a[216:219], a[104:107]\n" \
"v_mfma_f32_16x16x32_bf16 a[108:111], a[172:175], a[152:155], a[108:111]\n" \
"v_mfma_f32_16x16x32_bf16 a[108:111], a[236:239], a[216:219], a[108:111]\n" \
"s_waitcnt lgkmcnt(15)\n" \
"v_mfma_f32_16x16x32_bf16 a[124:127], a[172:175], a[156:159], a[124:127]\n" \
"v_mfma_f32_16x16x32_bf16 a[124:127], a[236:239], a[220:223], a[124:127]\n" \
"v_mfma_f32_16x16x32_bf16 a[120:123], a[168:171], a[156:159], a[120:123]\n" \
"v_mfma_f32_16x16x32_bf16 a[120:123], a[232:235], a[220:223], a[120:123]\n" \
"v_mfma_f32_16x16x32_bf16 a[116:119], a[164:167], a[156:159], a[116:119]\n" \
"v_mfma_f32_16x16x32_bf16 a[116:119], a[228:231], a[220:223], a[116:119]\n" \
"v_mfma_f32_16x16x32_bf16 a[112:115], a[160:163], a[156:159], a[112:115]\n" \
"s_waitcnt vmcnt(3) lgkmcnt(0)\n" \
"v_mfma_f32_16x16x32_bf16 a[112:115], a[224:227], a[220:223], a[112:115]\n" \
"s_barrier\n" \
"v_cmp_ne_u32_e32 vcc, s62, v44\n" \
"s_nop 4\n" \
"s_cbranch_vccnz L_poll60_%=\n" \
"L_pollr60_%=:\n" \
"s_mov_b32 s68, s12\n" \
"s_and_b32 s69, s13, 0xffff\n" \
"s_mov_b32 s70, 0x480000\n" \
"s_mov_b32 s71, 0x20000\n" \
"v_mfma_f32_16x16x32_bf16 v[52:55], a[176:179], a[128:131], v[52:55]\n" \
"s_add_u32 m0, s40, 0x0\n" \
"s_add_u32 s85, s39, 0x0\n" \
"s_nop 0\n" \
"v_mfma_f32_16x16x32_bf16 v[52:55], a[240:243], a[192:195], v[52:55]\n" \
"buffer_load_dwordx4 v20, s[80:83], s85 offen sc1 lds\n" \
"v_mfma_f32_16x16x32_bf16 v[56:59], a[180:183], a[128:131], v[56:59]\n" \
"ds_read_b64_tr_b16 a[160:161], v32 offset:32768\n" \
"v_mfma_f32_16x16x32_bf16 v[56:59], a[244:247], a[192:195], v[56:59]\n" \
"s_add_u32 m0, s40, 0x1000\n" \
"s_add_u32 s85, s39, 0x1000\n" \
".fill (L_pb + 16096 - .) / 4, 4, 0xBF800000\n" \
"L_pp4:\n" \
"s_cbranch_execz L_pret\n" \
"s_nop 0\n" \
"v_mfma_f32_16x16x32_bf16 v[60:63], a[184:187], a[128:131], v[60:63]\n" \
"buffer_load_dwordx4 v20, s[80:83], s85 offen sc1 lds\n" \
"v_mfma_f32_16x16x32_bf16 v[60:63], a[248:251], a[192:195], v[60:63]\n" \
"ds_read_b64_tr_b16 a[162:163], v32 offset:33792\n" \
"v_mfma_f32_16x16x32_bf16 v[64:67], a[188:191], a[128:131], v[64:67]\n" \
"ds_read_b64_tr_b16 a[224:225], v32 offset:40960\n" \
"v_mfma_f32_16x16x32_bf16 v[64:67], a[252:255], a[192:195], v[64:67]\n" \
"ds_read_b128 a[128:131], v28 offset:49152\n" \
"v_mfma_f32_16x16x32_bf16 v[80:83], a[188:191], a[132:135], v[80:83]\n" \
"ds_read_b128 a[192:195], v29 offset:49152\n" \
"v_mfma_f32_16x16x32_bf16 v[80:83], a[252:255], a[196:199], v[80:83]\n" \
"s_add_u32 m0, s40, 0x3000\n" \
"s_movk_i32 s84, 0x0\n" \
"v_mfma_f32_16x16x32_bf16 v[76:79], a[184:187], a[132:135], v[76:79]\n" \
"buffer_load_dwordx4 v21, s[76:79], s84 offen offset:0 lds\n" \
"v_mfma_f32_16x16x32_bf16 v[76:79], a[248:251], a[196:199], v[76:79]\n" \
"s_add_u32 m0, s30, 0x0\n" \
"s_movk_i32 s84, 0x0\n" \
"s_nop 0\n" \
"v_mfma_f32_16x16x32_bf16 v[72:75], a[180:183], a[132:135], v[72:75]\n" \
"buffer_load_dwordx4 v24, s[76:79], s84 offen offset:0 lds\n" \
"v_mfma_f32_16x16x32_bf16 v[72:75], a[244:247], a[196:199], v[72:75]\n" \
"ds_read_b64_tr_b16 a[226:227], v32 offset:41984\n" \
"v_mfma_f32_16x16x32_bf16 v[68:71], a[176:179], a[132:135], v[68:71]\n" \
"ds_read_b64_tr_b16 a[164:165], v33 offset:32768\n" \
"v_mfma_f32_16x16x32_bf16 v[68:71], a[240:243], a[196:199], v[68:71]\n" \
"ds_read_b128 a[132:135], v28 offset:51200\n" \
"v_mfma_f32_16x16x32_bf16 v[84:87], a[176:179], a[136:139], v[84:87]\n" \
"ds_read_b128 a[196:199], v29 offset:51200\n" \
"v_mfma_f32_16x16x32_bf16 v[84:87], a[240:243], a[200:203], v[84:87]\n" \
"s_movk_i32 s84, 0x2000\n" \
"v_mfma_f32_16x16x32_bf16 v[88:91], a[180:183], a[136:139], v[88:91]\n" \
"buffer_load_dwordx4 v25, s[76:79], s84 offen offset:1024 lds\n" \
"v_mfma_f32_16x16x32_bf16 v[88:91], a[244:247], a[200:203], v[88:91]\n" \
"s_movk_i32 s84, 0x4000\n" \
"v_mfma_f32_16x16x32_bf16 v[92:95], a[184:187], a[136:139], v[92:95]\n" \
"buffer_load_dwordx4 v24, s[76:79], s84 offen offset:2048 lds\n" \
"v_mfma_f32_16x16x32_bf16 v[92:95], a[248:251], a[200:203], v[92:95]\n" \
"ds_read_b64_tr_b16 a[166:167], v33 offset:33792\n" \
"v_mfma_f32_16x16x32_bf16 v[96:99], a[188:191], a[136:139], v[96:99]\n" \
"ds_read_b64_tr_b16 a[228:229], v33 offset:40960\n" \
"v_mfma_f32_16x16x32_bf16 v[96:99], a[252:255], a[200:203], v[96:99]\n" \
"ds_read_b128 a[136:139], v28 offset:53248\n" \
"v_mfma_f32_16x16x32_bf16 v[112:115], a[188:191], a[140:143], v[112:115]\n" \
"ds_read_b128 a[200:203], v29 offset:53248\n" \
"v_mfma_f32_16x16x32_bf16 v[112:115], a[252:255], a[204:207], v[112:115]\n" \
"s_movk_i32 s84, 0x6000\n" \
"v_mfma_f32_16x16x32_bf16 v[108:111], a[184:187], a[140:143], v[108:111]\n" \
"buffer_load_dwordx4 v25, s[76:79], s84 offen offset:3072 lds\n" \
"v_mfma_f32_16x16x32_bf16 v[108:111], a[248:251], a[204:207], v[108:111]\n" \
"s_add_u32 m0, s31, 0x0\n" \
"s_movk_i32 s84, 0x0\n" \
"v_mfma_f32_16x16x32_bf16 v[104:107], a[180:183], a[140:143], v[104:107]\n" \
"buffer_load_dwordx4 v26, s[68:71], s84 offen offset:0 lds\n" \
"v_mfma_f32_16x16x32_bf16 v[104:107], a[244:247], a[204:207], v[104:107]\n" \
"s_movk_i32 s84, 0x7c00\n" \
"v_mfma_f32_16x16x32_bf16 v[100:103], a[176:179], a[140:143], v[100:103]\n" \
"buffer_load_dwordx4 v26, s[68:71], s84 offen offset:1024 lds\n" \
"v_mfma_f32_16x16x32_bf16 v[100:103], a[240:243], a[204:207], v[100:103]\n" \
"ds_read_b128 a[140:143], v28 offset:55296\n" \
"v_mfma_f32_16x16x32_bf16 v[116:119], a[176:179], a[144:147], v[116:119]\n" \
"ds_read_b128 a[204:207], v29 offset:55296\n" \
"v_mfma_f32_16x16x32_bf16 v[116:119], a[240:243], a[208:211], v[116:119]\n" \
"s_addk_i32 s84, 0x7c00\n" \
"v_mfma_f32_16x16x32_bf16 v[120:123], a[180:183], a[144:147], v[120:123]\n" \
"buffer_load_dwordx4 v27, s[68:71], s84 offen offset:2048 lds\n" \
"v_mfma_f32_16x16x32_bf16 v[120:123], a[244:247], a[208:211], v[120:123]\n" \
"ds_read_b64_tr_b16 a[230:231], v33 offset:41984\n" \
"v_mfma_f32_16x16x32_bf16 v[124:127], a[184:187], a[144:147], v[124:127]\n" \
"s_addk_i32 s84, 0x7c00\n" \
"v_mfma_f32_16x16x32_bf16 v[124:127], a[248:251], a[208:211], v[124:127]\n" \
"buffer_load_dwordx4 v27, s[68:71], s84 offen offset:3072 lds\n" \
"v_mfma_f32_16x16x32_bf16 v[128:131], a[188:191], a[144:147], v[128:131]\n" \
"ds_read_b64_tr_b16 a[168:169], v34 offset:32768\n" \
"v_mfma_f32_16x16x32_bf16 v[128:131], a[252:255], a[208:211], v[128:131]\n" \
"ds_read_b128 a[144:147], v28 offset:32768\n" \
"v_mfma_f32_16x16x32_bf16 v[144:147], a[188:191], a[148:151], v[144:147]\n" \
"ds_read_b128 a[208:211], v29 offset:32768\n" \
"v_mfma_f32_16x16x32_bf16 v[144:147], a[252:255], a[212:215], v[144:147]\n" \
"s_add_u32 m0, s30, 0x1000\n" \
"s_mov_b32 s84, 0x9000\n" \
"v_mfma_f32_16x16x32_bf16 v[140:143], a[184:187], a[148:151], v[140:143]\n" \
"buffer_load_dwordx4 v24, s[76:79], s84 offen offset:0 lds\n" \
"v_mfma_f32_16x16x32_bf16 v[140:143], a[248:251], a[212:215], v[140:143]\n" \
"ds_read_b64_tr_b16 a[170:171], v34 offset:33792\n" \
"v_mfma_f32_16x16x32_bf16 v[136:139], a[180:183], a[148:151], v[136:139]\n" \
"s_addk_i32 s84, 0x2000\n" \
"s_nop 0\n" \
"v_mfma_f32_16x16x32_bf16 v[136:139], a[244:247], a[212:215], v[136:139]\n" \
"buffer_load_dwordx4 v25, s[76:79], s84 offen offset:1024 lds\n" \
"v_mfma_f32_16x16x32_bf16 v[132:135], a[176:179], a[148:151], v[132:135]\n" \
"ds_read_b64_tr_b16 a[232:233], v34 offset:40960\n" \
"v_mfma_f32_16x16x32_bf16 v[132:135], a[240:243], a[212:215], v[132:135]\n" \
"ds_read_b128 a[148:151], v28 offset:34816\n" \
"v_mfma_f32_16x16x32_bf16 v[148:151], a[176:179], a[152:155], v[148:151]\n" \
"ds_read_b128 a[212:215], v29 offset:34816\n" \
"v_mfma_f32_16x16x32_bf16 v[148:151], a[240:243], a[216:219], v[148:151]\n" \
"ds_read_b64_tr_b16 a[234:235], v34 offset:41984\n" \
"v_mfma_f32_16x16x32_bf16 v[152:155], a[180:183], a[152:155], v[152:155]\n" \
"ds_read_b64_tr_b16 a[172:173], v35 offset:32768\n" \
"v_mfma_f32_16x16x32_bf16 v[152:155], a[244:247], a[216:219], v[152:155]\n" \
"ds_read_b64_tr_b16 a[174:175], v35 offset:33792\n" \
"v_mfma_f32_16x16x32_bf16 v[156:159], a[184:187], a[152:155], v[156:159]\n" \
"ds_read_b64_tr_b16 a[236:237], v35 offset:40960\n" \
"v_mfma_f32_16x16x32_bf16 v[156:159], a[248:251], a[216:219], v[156:159]\n" \
"ds_read_b64_tr_b16 a[238:239], v35 offset:41984\n" \
"v_mfma_f32_16x16x32_bf16 v[160:163], a[188:191], a[152:155], v[160:163]\n" \
"v_mfma_f32_16x16x32_bf16 v[160:163], a[252:255], a[216:219], v[160:163]\n" \
"ds_read_b128 a[152:155], v28 offset:36864\n" \
"v_mfma_f32_16x16x32_bf16 v[176:179], a[188:191], a[156:159], v[176:179]\n" \
"ds_read_b128 a[216:219], v29 offset:36864\n" \
"v_mfma_f32_16x16x32_bf16 v[176:179], a[252:255], a[220:223], v[176:179]\n" \
"v_mfma_f32_16x16x32_bf16 v[172:175], a[184:187], a[156:159], v[172:175]\n" \
"v_mfma_f32_16x16x32_bf16 v[172:175], a[248:251], a[220:223], v[172:175]\n" \
"v_mfma_f32_16x16x32_bf16 v[168:171], a[180:183], a[156:159], v[168:171]\n" \
"v_mfma_f32_16x16x32_bf16 v[168:171], a[244:247], a[220:223], v[168:171]\n" \
"v_mfma_f32_16x16x32_bf16 v[164:167], a[176:179], a[156:159], v[164:167]\n" \
"v_mfma_f32_16x16x32_bf16 v[164:167], a[240:243], a[220:223], v[164:167]\n" \
"ds_read_b128 a[156:159], v28 offset:38912\n" \
"ds_read_b128 a[220:223], v29 offset:38912\n" \
"s_waitcnt lgkmcnt(15)\n" \
"v_mfma_f32_16x16x32_bf16 a[0:3], a[160:163], a[128:131], a[0:3]\n" \
"s_addk_i32 s84, 0x2000\n" \
"v_mfma_f32_16x16x32_bf16 a[0:3], a[224:227], a[192:195], a[0:3]\n" \
"buffer_load_dwordx4 v24, s[76:79], s84 offen offset:2048 lds\n" \
"v_mfma_f32_16x16x32_bf16 a[4:7], a[164:167], a[128:131], a[4:7]\n" \
"ds_read_b64_tr_b16 a[176:177], v36 offset:32768\n" \
"v_mfma_f32_16x16x32_bf16 a[4:7], a[228:231], a[192:195], a[4:7]\n" \
"s_addk_i32 s84, 0x2000\n" \
"s_waitcnt lgkmcnt(13)\n" \
"v_mfma_f32_16x16x32_bf16 a[8:11], a[168:171], a[128:131], a[8:11]\n" \
"buffer_load_dwordx4 v25, s[76:79], s84 offen offset:3072 lds\n" \
"s_waitcnt lgkmcnt(9)\n" \
"v_mfma_f32_16x16x32_bf16 a[8:11], a[232:235], a[192:195], a[8:11]\n" \
"ds_read_b64_tr_b16 a[178:179], v36 offset:33792\n" \
"s_waitcnt lgkmcnt(8)\n" \
"v_mfma_f32_16x16x32_bf16 a[12:15], a[172:175], a[128:131], a[12:15]\n" \
"ds_read_b64_tr_b16 a[240:241], v36 offset:40960\n" \
"v_accvgpr_read_b32 v184, a0\n" \
"s_waitcnt lgkmcnt(7)\n" \
"v_mfma_f32_16x16x32_bf16 a[12:15], a[236:239], a[192:195], a[12:15]\n" \
"v_accvgpr_read_b32 v185, a1\n" \
"v_accvgpr_read_b32 v186, a2\n" \
"v_mfma_f32_16x16x32_bf16 a[28:31], a[172:175], a[132:135], a[28:31]\n" \
"v_accvgpr_read_b32 v187, a3\n" \
"v_accvgpr_read_b32 v180, a4\n" \
"v_mfma_f32_16x16x32_bf16 a[28:31], a[236:239], a[196:199], a[28:31]\n" \
"s_add_u32 m0, s30, 0x20000\n" \
"s_movk_i32 s84, 0x100\n" \
"v_mfma_f32_16x16x32_bf16 a[24:27], a[168:171], a[132:135], a[24:27]\n" \
"buffer_load_dwordx4 v24, s[76:79], s84 offen offset:0 lds\n" \
"v_mfma_f32_16x16x32_bf16 a[24:27], a[232:235], a[196:199], a[24:27]\n" \
"ds_read_b64_tr_b16 a[242:243], v36 offset:41984\n" \
"v_accvgpr_read_b32 v181, a5\n" \
"v_mfma_f32_16x16x32_bf16 a[20:23], a[164:167], a[132:135], a[20:23]\n" \
"v_accvgpr_read_b32 v182, a6\n" \
"v_accvgpr_read_b32 v183, a7\n" \
"v_mfma_f32_16x16x32_bf16 a[20:23], a[228:231], a[196:199], a[20:23]\n" \
"ds_read_b64_tr_b16 a[180:181], v37 offset:32768\n" \
"v_cvt_pk_bf16_f32 v184, v184, v185\n" \
"v_mfma_f32_16x16x32_bf16 a[16:19], a[160:163], a[132:135], a[16:19]\n" \
"s_movk_i32 s84, 0x2100\n" \
"s_nop 0\n" \
"v_mfma_f32_16x16x32_bf16 a[16:19], a[224:227], a[196:199], a[16:19]\n" \
"buffer_load_dwordx4 v25, s[76:79], s84 offen offset:1024 lds\n" \
"v_mfma_f32_16x16x32_bf16 a[32:35], a[160:163], a[136:139], a[32:35]\n" \
"ds_read_b64_tr_b16 a[182:183], v37 offset:33792\n" \
"v_cvt_pk_bf16_f32 v185, v186, v187\n" \
"v_mfma_f32_16x16x32_bf16 a[32:35], a[224:227], a[200:203], a[32:35]\n" \
"v_cvt_pk_bf16_f32 v186, v180, v181\n" \
"v_cvt_pk_bf16_f32 v187, v182, v183\n" \
"v_mfma_f32_16x16x32_bf16 a[36:39], a[164:167], a[136:139], a[36:39]\n" \
"v_accvgpr_read_b32 v188, a8\n" \
"v_accvgpr_read_b32 v189, a9\n" \
"v_mfma_f32_16x16x32_bf16 a[36:39], a[228:231], a[200:203], a[36:39]\n" \
"s_movk_i32 s84, 0x4100\n" \
"s_nop 0\n" \
"v_mfma_f32_16x16x32_bf16 a[40:43], a[168:171], a[136:139], a[40:43]\n" \
"buffer_load_dwordx4 v24, s[76:79], s84 offen offset:2048 lds\n" \
"v_mfma_f32_16x16x32_bf16 a[40:43], a[232:235], a[200:203], a[40:43]\n" \
"ds_read_b64_tr_b16 a[244:245], v37 offset:40960\n" \
"v_accvgpr_read_b32 v190, a10\n" \
"v_mfma_f32_16x16x32_bf16 a[44:47], a[172:175], a[136:139], a[44:47]\n" \
"ds_read_b64_tr_b16 a[246:247], v37 offset:41984\n" \
"v_accvgpr_read_b32 v191, a11\n" \
"v_mfma_f32_16x16x32_bf16 a[44:47], a[236:239], a[200:203], a[44:47]\n" \
"v_accvgpr_read_b32 v180, a12\n" \
"v_accvgpr_read_b32 v181, a13\n" \
"v_mfma_f32_16x16x32_bf16 a[60:63], a[172:175], a[140:143], a[60:63]\n" \
"s_movk_i32 s84, 0x6100\n" \
"v_mfma_f32_16x16x32_bf16 a[60:63], a[236:239], a[204:207], a[60:63]\n" \
"buffer_load_dwordx4 v25, s[76:79], s84 offen offset:3072 lds\n" \
"v_mfma_f32_16x16x32_bf16 a[56:59], a[168:171], a[140:143], a[56:59]\n" \
"ds_read_b64_tr_b16 a[184:185], v38 offset:32768\n" \
"v_accvgpr_read_b32 v182, a14\n" \
"v_mfma_f32_16x16x32_bf16 a[56:59], a[232:235], a[204:207], a[56:59]\n" \
"v_accvgpr_read_b32 v183, a15\n" \
"v_cvt_pk_bf16_f32 v188, v188, v189\n" \
"v_mfma_f32_16x16x32_bf16 a[52:55], a[164:167], a[140:143], a[52:55]\n" \
"s_add_u32 m0, s30, 0x21000\n" \
"s_addk_i32 s84, 0x3000\n" \
"v_mfma_f32_16x16x32_bf16 a[52:55], a[228:231], a[204:207], a[52:55]\n" \
"buffer_load_dwordx4 v24, s[76:79], s84 offen offset:0 lds\n" \
"v_mfma_f32_16x16x32_bf16 a[48:51], a[160:163], a[140:143], a[48:51]\n" \
"ds_read_b64_tr_b16 a[186:187], v38 offset:33792\n" \
"v_cvt_pk_bf16_f32 v189, v190, v191\n" \
"v_mfma_f32_16x16x32_bf16 a[48:51], a[224:227], a[204:207], a[48:51]\n" \
"v_cvt_pk_bf16_f32 v190, v180, v181\n" \
"v_cvt_pk_bf16_f32 v191, v182, v183\n" \
"v_mfma_f32_16x16x32_bf16 a[64:67], a[160:163], a[144:147], a[64:67]\n" \
"ds_read_b64_tr_b16 a[248:249], v38 offset:40960\n" \
"v_accvgpr_read_b32 v192, a24\n" \
"v_mfma_f32_16x16x32_bf16 a[64:67], a[224:227], a[208:211], a[64:67]\n" \
"s_addk_i32 s84, 0x2000\n" \
"s_nop 0\n" \
"v_mfma_f32_16x16x32_bf16 a[68:71], a[164:167], a[144:147], a[68:71]\n" \
"buffer_load_dwordx4 v25, s[76:79], s84 offen offset:1024 lds\n" \
"v_mfma_f32_16x16x32_bf16 a[68:71], a[228:231], a[208:211], a[68:71]\n" \
"ds_read_b64_tr_b16 a[250:251], v38 offset:41984\n" \
"v_accvgpr_read_b32 v193, a25\n" \
"v_mfma_f32_16x16x32_bf16 a[72:75], a[168:171], a[144:147], a[72:75]\n" \
"v_accvgpr_read_b32 v194, a26\n" \
"v_accvgpr_read_b32 v195, a27\n" \
"v_mfma_f32_16x16x32_bf16 a[72:75], a[232:235], a[208:211], a[72:75]\n" \
"v_accvgpr_read_b32 v180, a28\n" \
"v_accvgpr_read_b32 v181, a29\n" \
"v_mfma_f32_16x16x32_bf16 a[76:79], a[172:175], a[144:147], a[76:79]\n" \
"s_addk_i32 s84, 0x2000\n" \
"v_mfma_f32_16x16x32_bf16 a[76:79], a[236:239], a[208:211], a[76:79]\n" \
"buffer_load_dwordx4 v24, s[76:79], s84 offen offset:2048 lds\n" \
"v_mfma_f32_16x16x32_bf16 a[92:95], a[172:175], a[148:151], a[92:95]\n" \
"s_add_u32 s89, s41, 0x0\n" \
"buffer_store_dwordx4 v[184:187], v20, s[80:83], s89 offen offset:0 sc1\n" \
"v_mfma_f32_16x16x32_bf16 a[92:95], a[236:239], a[212:215], a[92:95]\n" \
"ds_read_b64_tr_b16 a[188:189], v39 offset:32768\n" \
"v_accvgpr_read_b32 v182, a30\n" \
"v_mfma_f32_16x16x32_bf16 a[88:91], a[168:171], a[148:151], a[88:91]\n" \
"ds_read_b64_tr_b16 a[190:191], v39 offset:33792\n" \
"v_accvgpr_read_b32 v183, a31\n" \
"v_mfma_f32_16x16x32_bf16 a[88:91], a[232:235], a[212:215], a[88:91]\n" \
"s_addk_i32 s84, 0x2000\n" \
"v_mfma_f32_16x16x32_bf16 a[84:87], a[164:167], a[148:151], a[84:87]\n" \
"buffer_load_dwordx4 v25, s[76:79], s84 offen offset:3072 lds\n" \
"v_mfma_f32_16x16x32_bf16 a[84:87], a[228:231], a[212:215], a[84:87]\n" \
"ds_read_b64_tr_b16 a[252:253], v39 offset:40960\n" \
"v_cvt_pk_bf16_f32 v192, v192, v193\n" \
"v_mfma_f32_16x16x32_bf16 a[80:83], a[160:163], a[148:151], a[80:83]\n" \
"s_add_u32 s88, s41, 0x0\n" \
"buffer_store_dwordx4 v[188:191], v20, s[80:83], s88 offen offset:1024 sc1\n" \
"v_mfma_f32_16x16x32_bf16 a[80:83], a[224:227], a[212:215], a[80:83]\n" \
"v_cvt_pk_bf16_f32 v193, v194, v195\n" \
"v_cvt_pk_bf16_f32 v194, v180, v181\n" \
"s_waitcnt lgkmcnt(15)\n" \
"s_nop 0\n" \
"v_mfma_f32_16x16x32_bf16 a[96:99], a[160:163], a[152:155], a[96:99]\n" \
"ds_read_b64_tr_b16 a[254:255], v39 offset:41984\n" \
"v_cvt_pk_bf16_f32 v195, v182, v183\n" \
"v_mfma_f32_16x16x32_bf16 a[96:99], a[224:227], a[216:219], a[96:99]\n" \
"v_accvgpr_read_b32 v196, a16\n" \
"v_accvgpr_read_b32 v197, a17\n" \
"v_mfma_f32_16x16x32_bf16 a[100:103], a[164:167], a[152:155], a[100:103]\n" \
"v_accvgpr_read_b32 v198, a18\n" \
"v_accvgpr_read_b32 v199, a19\n" \
"v_mfma_f32_16x16x32_bf16 a[100:103], a[228:231], a[216:219], a[100:103]\n" \
"v_accvgpr_read_b32 v180, a20\n" \
"v_accvgpr_read_b32 v181, a21\n" \
"v_mfma_f32_16x16x32_bf16 a[104:107], a[168:171], a[152:155], a[104:107]\n" \
"s_add_u32 s89, s41, 0x1000\n" \
"buffer_store_dwordx4 v[192:195], v20, s[80:83], s89 offen offset:1024 sc1\n" \
"v_mfma_f32_16x16x32_bf16 a[104:107], a[232:235], a[216:219], a[104:107]\n" \
"v_accvgpr_read_b32 v182, a22\n" \
"v_accvgpr_read_b32 v183, a23\n" \
"v_mfma_f32_16x16x32_bf16 a[108:111], a[172:175], a[152:155], a[108:111]\n" \
"v_cvt_pk_bf16_f32 v196, v196, v197\n" \
"v_cvt_pk_bf16_f32 v197, v198, v199\n" \
"v_mfma_f32_16x16x32_bf16 a[108:111], a[236:239], a[216:219], a[108:111]\n" \
"v_cvt_pk_bf16_f32 v198, v180, v181\n" \
"v_cvt_pk_bf16_f32 v199, v182, v183\n" \
"v_mfma_f32_16x16x32_bf16 a[124:127], a[172:175], a[156:159], a[124:127]\n" \
"v_accvgpr_read_b32 v200, a32\n" \
"v_accvgpr_read_b32 v201, a33\n" \
"v_mfma_f32_16x16x32_bf16 a[124:127], a[236:239], a[220:223], a[124:127]\n" \
"v_accvgpr_read_b32 v202, a34\n" \
"v_accvgpr_read_b32 v203, a35\n" \
"v_mfma_f32_16x16x32_bf16 a[120:123], a[168:171], a[156:159], a[120:123]\n" \
"s_add_u32 s88, s41, 0x1000\n" \
"buffer_store_dwordx4 v[196:199], v20, s[80:83], s88 offen offset:0 sc1\n" \
"v_mfma_f32_16x16x32_bf16 a[120:123], a[232:235], a[220:223], a[120:123]\n" \
"v_accvgpr_read_b32 v180, a36\n" \
"v_accvgpr_read_b32 v181, a37\n" \
"v_mfma_f32_16x16x32_bf16 a[116:119], a[164:167], a[156:159], a[116:119]\n" \
"v_accvgpr_read_b32 v182, a38\n" \
"v_accvgpr_read_b32 v183, a39\n" \
"v_mfma_f32_16x16x32_bf16 a[116:119], a[228:231], a[220:223], a[116:119]\n" \
"v_cvt_pk_bf16_f32 v200, v200, v201\n" \
"v_cvt_pk_bf16_f32 v201, v202, v203\n" \
"v_mfma_f32_16x16x32_bf16 a[112:115], a[160:163], a[156:159], a[112:115]\n" \
"v_cvt_pk_bf16_f32 v202, v180, v181\n" \
"v_cvt_pk_bf16_f32 v203, v182, v183\n" \
"v_mfma_f32_16x16x32_bf16 a[112:115], a[224:227], a[220:223], a[112:115]\n" \
"v_accvgpr_read_b32 v204, a40\n" \
"v_accvgpr_read_b32 v205, a41\n" \
"s_waitcnt vmcnt(12) lgkmcnt(0)\n" \
"s_barrier\n" \
"v_mov_b32_e32 v36, v32\n" \
"v_xor_b32_e32 v32, 0x0, v48\n" \
"v_mov_b32_e32 v37, v33\n" \
"v_xor_b32_e32 v33, 0x20, v48\n" \
"v_mov_b32_e32 v38, v34\n" \
"v_xor_b32_e32 v34, 0x40, v48\n" \
"v_mov_b32_e64 v39, v35\n" \
"v_xor_b32_e32 v35, 0x60, v48\n" \
"v_add_u32_e32 v22, s44, v28\n" \
"v_add_u32_e32 v28, s43, v28\n" \
"v_add_u32_e32 v23, s44, v29\n" \
"v_add_u32_e32 v29, s43, v29\n" \
"v_mfma_f32_16x16x32_bf16 v[52:55], a[176:179], a[128:131], v[52:55]\n" \
"s_add_u32 m0, s40, 0x8000\n" \
"s_add_u32 s85, s39, 0x2000\n" \
"v_mfma_f32_16x16x32_bf16 v[52:55], a[240:243], a[192:195], v[52:55]\n" \
"buffer_load_dwordx4 v20, s[80:83], s85 offen sc1 lds\n" \
"v_mfma_f32_16x16x32_bf16 v[56:59], a[180:183], a[128:131], v[56:59]\n" \
"ds_read_b64_tr_b16 a[160:161], v32 offset:0\n" \
"v_accvgpr_read_b32 v206, a42\n" \
"v_mfma_f32_16x16x32_bf16 v[56:59], a[244:247], a[192:195], v[56:59]\n" \
"s_add_u32 m0, s40, 0x9000\n" \
"s_add_u32 s85, s39, 0x3000\n" \
"v_mfma_f32_16x16x32_bf16 v[60:63], a[184:187], a[128:131], v[60:63]\n" \
"buffer_load_dwordx4 v20, s[80:83], s85 offen sc1 lds\n" \
"v_mfma_f32_16x16x32_bf16 v[60:63], a[248:251], a[192:195], v[60:63]\n" \
"ds_read_b64_tr_b16 a[162:163], v32 offset:512\n" \
"v_accvgpr_read_b32 v207, a43\n" \
"v_mfma_f32_16x16x32_bf16 v[64:67], a[188:191], a[128:131], v[64:67]\n" \
"ds_read_b64_tr_b16 a[224:225], v32 offset:4096\n" \
"v_accvgpr_read_b32 v180, a44\n" \
"v_mfma_f32_16x16x32_bf16 v[64:67], a[252:255], a[192:195], v[64:67]\n" \
"ds_read_b128 a[128:131], v28 offset:0\n" \
"v_accvgpr_read_b32 v181, a45\n" \
"v_mfma_f32_16x16x32_bf16 v[80:83], a[188:191], a[132:135], v[80:83]\n" \
"ds_read_b128 a[192:195], v29 offset:0\n" \
"v_accvgpr_read_b32 v182, a46\n" \
"v_mfma_f32_16x16x32_bf16 v[80:83], a[252:255], a[196:199], v[80:83]\n" \
"s_add_u32 m0, s40, 0xb000\n" \
"s_movk_i32 s84, 0x80\n" \
"v_mfma_f32_16x16x32_bf16 v[76:79], a[184:187], a[132:135], v[76:79]\n" \
"buffer_load_dwordx4 v21, s[76:79], s84 offen offset:0 lds\n" \
"v_mfma_f32_16x16x32_bf16 v[76:79], a[248:251], a[196:199], v[76:79]\n" \
"s_add_u32 m0, s30, 0x8000\n" \
"s_movk_i32 s84, 0x80\n" \
"v_mfma_f32_16x16x32_bf16 v[72:75], a[180:183], a[132:135], v[72:75]\n" \
"buffer_load_dwordx4 v24, s[76:79], s84 offen offset:0 lds\n" \
"v_mfma_f32_16x16x32_bf16 v[72:75], a[244:247], a[196:199], v[72:75]\n" \
"ds_read_b64_tr_b16 a[226:227], v32 offset:4608\n" \
"v_accvgpr_read_b32 v183, a47\n" \
"v_mfma_f32_16x16x32_bf16 v[68:71], a[176:179], a[132:135], v[68:71]\n" \
"ds_read_b64_tr_b16 a[164:165], v33 offset:0\n" \
"v_cvt_pk_bf16_f32 v204, v204, v205\n" \
"v_mfma_f32_16x16x32_bf16 v[68:71], a[240:243], a[196:199], v[68:71]\n" \
"ds_read_b128 a[132:135], v28 offset:2048\n" \
"v_cvt_pk_bf16_f32 v205, v206, v207\n" \
"v_mfma_f32_16x16x32_bf16 v[84:87], a[176:179], a[136:139], v[84:87]\n" \
"ds_read_b128 a[196:199], v29 offset:2048\n" \
"v_cvt_pk_bf16_f32 v206, v180, v181\n" \
"v_mfma_f32_16x16x32_bf16 v[84:87], a[240:243], a[200:203], v[84:87]\n" \
"s_movk_i32 s84, 0x2080\n" \
"v_mfma_f32_16x16x32_bf16 v[88:91], a[180:183], a[136:139], v[88:91]\n" \
"buffer_load_dwordx4 v25, s[76:79], s84 offen offset:1024 lds\n" \
"v_mfma_f32_16x16x32_bf16 v[88:91], a[244:247], a[200:203], v[88:91]\n" \
"s_movk_i32 s84, 0x4080\n" \
"v_mfma_f32_16x16x32_bf16 v[92:95], a[184:187], a[136:139], v[92:95]\n" \
"buffer_load_dwordx4 v24, s[76:79], s84 offen offset:2048 lds\n" \
"v_mfma_f32_16x16x32_bf16 v[92:95], a[248:251], a[200:203], v[92:95]\n" \
"ds_read_b64_tr_b16 a[166:167], v33 offset:512\n" \
"v_cvt_pk_bf16_f32 v207, v182, v183\n" \
"v_mfma_f32_16x16x32_bf16 v[96:99], a[188:191], a[136:139], v[96:99]\n" \
"ds_read_b64_tr_b16 a[228:229], v33 offset:4096\n" \
"v_accvgpr_read_b32 v208, a56\n" \
"v_mfma_f32_16x16x32_bf16 v[96:99], a[252:255], a[200:203], v[96:99]\n" \
"ds_read_b128 a[136:139], v28 offset:4096\n" \
"v_accvgpr_read_b32 v209, a57\n" \
"v_mfma_f32_16x16x32_bf16 v[112:115], a[188:191], a[140:143], v[112:115]\n" \
"ds_read_b128 a[200:203], v29 offset:4096\n" \
"v_accvgpr_read_b32 v210, a58\n" \
"v_mfma_f32_16x16x32_bf16 v[112:115], a[252:255], a[204:207], v[112:115]\n" \
"s_movk_i32 s84, 0x6080\n" \
"v_mfma_f32_16x16x32_bf16 v[108:111], a[184:187], a[140:143], v[108:111]\n" \
"buffer_load_dwordx4 v25, s[76:79], s84 offen offset:3072 lds\n" \
"v_mfma_f32_16x16x32_bf16 v[108:111], a[248:251], a[204:207], v[108:111]\n" \
"s_add_u32 m0, s31, 0x8000\n" \
"s_mov_b32 s84, 0x80000\n" \
"v_mfma_f32_16x16x32_bf16 v[104:107], a[180:183], a[140:143], v[104:107]\n" \
"buffer_load_dwordx4 v26, s[68:71], s84 offen offset:0 lds\n" \
"v_mfma_f32_16x16x32_bf16 v[104:107], a[244:247], a[204:207], v[104:107]\n" \
"s_addk_i32 s84, 0x7c00\n" \
"v_mfma_f32_16x16x32_bf16 v[100:103], a[176:179], a[140:143], v[100:103]\n" \
"buffer_load_dwordx4 v26, s[68:71], s84 offen offset:1024 lds\n" \
"v_mfma_f32_16x16x32_bf16 v[100:103], a[240:243], a[204:207], v[100:103]\n" \
"ds_read_b128 a[140:143], v28 offset:6144\n" \
"v_accvgpr_read_b32 v211, a59\n" \
"v_mfma_f32_16x16x32_bf16 v[116:119], a[176:179], a[144:147], v[116:119]\n" \
"ds_read_b128 a[204:207], v29 offset:6144\n" \
"v_accvgpr_read_b32 v180, a60\n" \
"v_mfma_f32_16x16x32_bf16 v[116:119], a[240:243], a[208:211], v[116:119]\n" \
"s_addk_i32 s84, 0x7c00\n" \
"v_mfma_f32_16x16x32_bf16 v[120:123], a[180:183], a[144:147], v[120:123]\n" \
"buffer_load_dwordx4 v27, s[68:71], s84 offen offset:2048 lds\n" \
"v_mfma_f32_16x16x32_bf16 v[120:123], a[244:247], a[208:211], v[120:123]\n" \
"ds_read_b64_tr_b16 a[230:231], v33 offset:4608\n" \
"v_accvgpr_read_b32 v181, a61\n" \
"v_mfma_f32_16x16x32_bf16 v[124:127], a[184:187], a[144:147], v[124:127]\n" \
"s_addk_i32 s84, 0x7c00\n" \
"v_mfma_f32_16x16x32_bf16 v[124:127], a[248:251], a[208:211], v[124:127]\n" \
"buffer_load_dwordx4 v27, s[68:71], s84 offen offset:3072 lds\n" \
"v_mfma_f32_16x16x32_bf16 v[128:131], a[188:191], a[144:147], v[128:131]\n" \
"ds_read_b64_tr_b16 a[172:173], v35 offset:0\n" \
"v_accvgpr_read_b32 v182, a62\n" \
"v_mfma_f32_16x16x32_bf16 v[128:131], a[252:255], a[208:211], v[128:131]\n" \
"ds_read_b128 a[144:147], v28 offset:8192\n" \
"v_accvgpr_read_b32 v183, a63\n" \
"v_mfma_f32_16x16x32_bf16 v[144:147], a[188:191], a[148:151], v[144:147]\n" \
"ds_read_b128 a[208:211], v29 offset:8192\n" \
"v_cvt_pk_bf16_f32 v208, v208, v209\n" \
"v_mfma_f32_16x16x32_bf16 v[144:147], a[252:255], a[212:215], v[144:147]\n" \
"s_add_u32 m0, s30, 0x9000\n" \
"s_mov_b32 s84, 0x9080\n" \
"v_mfma_f32_16x16x32_bf16 v[140:143], a[184:187], a[148:151], v[140:143]\n" \
"buffer_load_dwordx4 v24, s[76:79], s84 offen offset:0 lds\n" \
"v_mfma_f32_16x16x32_bf16 v[140:143], a[248:251], a[212:215], v[140:143]\n" \
"ds_read_b64_tr_b16 a[174:175], v35 offset:512\n" \
"v_cvt_pk_bf16_f32 v209, v210, v211\n" \
"v_mfma_f32_16x16x32_bf16 v[136:139], a[180:183], a[148:151], v[136:139]\n" \
"s_addk_i32 s84, 0x2000\n" \
"s_nop 0\n" \
"v_mfma_f32_16x16x32_bf16 v[136:139], a[244:247], a[212:215], v[136:139]\n" \
"buffer_load_dwordx4 v25, s[76:79], s84 offen offset:1024 lds\n" \
"v_mfma_f32_16x16x32_bf16 v[132:135], a[176:179], a[148:151], v[132:135]\n" \
"ds_read_b64_tr_b16 a[236:237], v35 offset:4096\n" \
"v_cvt_pk_bf16_f32 v210, v180, v181\n" \
"v_mfma_f32_16x16x32_bf16 v[132:135], a[240:243], a[212:215], v[132:135]\n" \
"ds_read_b128 a[148:151], v28 offset:10240\n" \
"v_cvt_pk_bf16_f32 v211, v182, v183\n" \
"v_mfma_f32_16x16x32_bf16 v[148:151], a[176:179], a[152:155], v[148:151]\n" \
"ds_read_b128 a[212:215], v29 offset:10240\n" \
"v_accvgpr_read_b32 v212, a48\n" \
"v_mfma_f32_16x16x32_bf16 v[148:151], a[240:243], a[216:219], v[148:151]\n" \
"ds_read_b64_tr_b16 a[238:239], v35 offset:4608\n" \
"v_accvgpr_read_b32 v213, a49\n" \
"v_mfma_f32_16x16x32_bf16 v[152:155], a[180:183], a[152:155], v[152:155]\n" \
"ds_read_b64_tr_b16 a[168:169], v34 offset:0\n" \
"v_accvgpr_read_b32 v214, a50\n" \
"v_mfma_f32_16x16x32_bf16 v[152:155], a[244:247], a[216:219], v[152:155]\n" \
"ds_read_b64_tr_b16 a[170:171], v34 offset:512\n" \
"v_accvgpr_read_b32 v215, a51\n" \
"v_mfma_f32_16x16x32_bf16 v[156:159], a[184:187], a[152:155], v[156:159]\n" \
"ds_read_b64_tr_b16 a[232:233], v34 offset:4096\n" \
"v_accvgpr_read_b32 v180, a52\n" \
"v_mfma_f32_16x16x32_bf16 v[156:159], a[248:251], a[216:219], v[156:159]\n" \
"ds_read_b64_tr_b16 a[234:235], v34 offset:4608\n" \
"v_accvgpr_read_b32 v181, a53\n" \
"v_mfma_f32_16x16x32_bf16 v[160:163], a[188:191], a[152:155], v[160:163]\n" \
"ds_read_b128 v[248:251], v22 offset:0\n" \
"v_accvgpr_read_b32 v182, a54\n" \
"v_mfma_f32_16x16x32_bf16 v[160:163], a[252:255], a[216:219], v[160:163]\n" \
"ds_read_b128 a[152:155], v28 offset:12288\n" \
"v_accvgpr_read_b32 v183, a55\n" \
"v_mfma_f32_16x16x32_bf16 v[176:179], a[188:191], a[156:159], v[176:179]\n" \
"ds_read_b128 a[216:219], v29 offset:12288\n" \
"v_cvt_pk_bf16_f32 v212, v212, v213\n" \
"v_mfma_f32_16x16x32_bf16 v[176:179], a[252:255], a[220:223], v[176:179]\n" \
"ds_read_b128 v[48:51], v23 offset:0\n" \
"v_cvt_pk_bf16_f32 v213, v214, v215\n" \
"v_mfma_f32_16x16x32_bf16 v[172:175], a[184:187], a[156:159], v[172:175]\n" \
"v_cvt_pk_bf16_f32 v214, v180, v181\n" \
"v_cvt_pk_bf16_f32 v215, v182, v183\n" \
"v_mfma_f32_16x16x32_bf16 v[172:175], a[248:251], a[220:223], v[172:175]\n" \
"v_accvgpr_read_b32 v216, a64\n" \
"v_accvgpr_read_b32 v217, a65\n" \
"v_mfma_f32_16x16x32_bf16 v[168:171], a[180:183], a[156:159], v[168:171]\n" \
"v_accvgpr_read_b32 v218, a66\n" \
"v_accvgpr_read_b32 v219, a67\n" \
"v_mfma_f32_16x16x32_bf16 v[168:171], a[244:247], a[220:223], v[168:171]\n" \
"v_accvgpr_read_b32 v180, a68\n" \
"v_accvgpr_read_b32 v181, a69\n" \
"v_mfma_f32_16x16x32_bf16 v[164:167], a[176:179], a[156:159], v[164:167]\n" \
"v_accvgpr_read_b32 v182, a70\n" \
"v_accvgpr_read_b32 v183, a71\n" \
"v_mfma_f32_16x16x32_bf16 v[164:167], a[240:243], a[220:223], v[164:167]\n" \
"ds_read_b128 a[156:159], v28 offset:14336\n" \
"ds_read_b128 a[220:223], v29 offset:14336\n" \
"s_waitcnt lgkmcnt(15)\n" \
"v_mfma_f32_16x16x32_bf16 a[0:3], a[160:163], a[128:131], 0\n" \
"s_addk_i32 s84, 0x2000\n" \
"v_cvt_pk_bf16_f32 v216, v216, v217\n" \
"v_mfma_f32_16x16x32_bf16 a[0:3], a[224:227], a[192:195], a[0:3]\n" \
"buffer_load_dwordx4 v24, s[76:79], s84 offen offset:2048 lds\n" \
"v_cvt_pk_bf16_f32 v217, v218, v219\n" \
"v_mfma_f32_16x16x32_bf16 a[4:7], a[164:167], a[128:131], 0\n" \
"ds_read_b64_tr_b16 a[176:177], v36 offset:0\n" \
"v_cvt_pk_bf16_f32 v218, v180, v181\n" \
"v_mfma_f32_16x16x32_bf16 a[4:7], a[228:231], a[192:195], a[4:7]\n" \
"s_addk_i32 s84, 0x2000\n" \
"v_cvt_pk_bf16_f32 v219, v182, v183\n" \
"s_waitcnt lgkmcnt(15)\n" \
"v_mfma_f32_16x16x32_bf16 a[12:15], a[172:175], a[128:131], 0\n" \
"buffer_load_dwordx4 v25, s[76:79], s84 offen offset:3072 lds\n" \
"v_accvgpr_read_b32 v220, a80\n" \
"s_waitcnt lgkmcnt(11)\n" \
"v_mfma_f32_16x16x32_bf16 a[12:15], a[236:239], a[192:195], a[12:15]\n" \
"ds_read_b64_tr_b16 a[178:179], v36 offset:1024\n" \
"v_accvgpr_read_b32 v221, a81\n" \
"s_waitcnt lgkmcnt(10)\n" \
"v_mfma_f32_16x16x32_bf16 a[8:11], a[128:131], a[168:171], 0\n" \
"ds_read_b64_tr_b16 a[240:241], v36 offset:8192\n" \
"v_accvgpr_read_b32 v222, a82\n" \
"s_waitcnt lgkmcnt(9)\n" \
"s_nop 0\n" \
"v_mfma_f32_16x16x32_bf16 a[8:11], a[192:195], a[232:235], a[8:11]\n" \
"ds_read_b64_tr_b16 a[242:243], v36 offset:9216\n" \
"v_accvgpr_read_b32 v223, a83\n" \
"v_mfma_f32_16x16x32_bf16 a[24:27], a[132:135], a[168:171], 0\n" \
"ds_read_b64_tr_b16 a[180:181], v37 offset:0\n" \
"v_accvgpr_read_b32 v180, a84\n" \
"v_mfma_f32_16x16x32_bf16 a[24:27], a[196:199], a[232:235], a[24:27]\n" \
"ds_read_b64_tr_b16 a[182:183], v37 offset:1024\n" \
"v_accvgpr_read_b32 v181, a85\n" \
"v_mfma_f32_16x16x32_bf16 a[28:31], a[172:175], a[132:135], 0\n" \
"ds_read_b64_tr_b16 a[244:245], v37 offset:8192\n" \
"v_accvgpr_read_b32 v182, a86\n" \
"v_mfma_f32_16x16x32_bf16 a[28:31], a[236:239], a[196:199], a[28:31]\n" \
"s_add_u32 s89, s41, 0x2000\n" \
"buffer_store_dwordx4 v[200:203], v20, s[80:83], s89 offen offset:0 sc1\n" \
"v_mfma_f32_16x16x32_bf16 a[20:23], a[164:167], a[132:135], 0\n" \
"ds_read_b64_tr_b16 a[246:247], v37 offset:9216\n" \
"v_accvgpr_read_b32 v183, a87\n" \
"v_mfma_f32_16x16x32_bf16 a[20:23], a[228:231], a[196:199], a[20:23]\n" \
"ds_read_b64_tr_b16 a[184:185], v38 offset:0\n" \
"v_cvt_pk_bf16_f32 v220, v220, v221\n" \
"v_mfma_f32_16x16x32_bf16 a[16:19], a[160:163], a[132:135], 0\n" \
"ds_read_b64_tr_b16 a[186:187], v38 offset:1024\n" \
"v_cvt_pk_bf16_f32 v221, v222, v223\n" \
"v_mfma_f32_16x16x32_bf16 a[16:19], a[224:227], a[196:199], a[16:19]\n" \
"v_cvt_pk_bf16_f32 v222, v180, v181\n" \
"v_cvt_pk_bf16_f32 v223, v182, v183\n" \
"v_cvt_pk_bf16_f32 v224, v52, v53\n" \
".fill (L_pb + 20192 - .) / 4, 4, 0xBF800000\n" \
"L_pp5:\n" \
"s_cbranch_execz L_pret\n" \
"s_nop 0\n" \
"v_mfma_f32_16x16x32_bf16 a[32:35], a[160:163], a[136:139], 0\n" \
"s_add_u32 s88, s41, 0x2000\n" \
"buffer_store_dwordx4 v[204:207], v20, s[80:83], s88 offen offset:1024 sc1\n" \
"v_mfma_f32_16x16x32_bf16 a[32:35], a[224:227], a[200:203], a[32:35]\n" \
"ds_read_b64_tr_b16 a[248:249], v38 offset:8192\n" \
"v_cvt_pk_bf16_f32 v225, v54, v55\n" \
"v_mfma_f32_16x16x32_bf16 a[36:39], a[164:167], a[136:139], 0\n" \
"ds_read_b64_tr_b16 a[250:251], v38 offset:9216\n" \
"v_cvt_pk_bf16_f32 v226, v56, v57\n" \
"v_mfma_f32_16x16x32_bf16 a[36:39], a[228:231], a[200:203], a[36:39]\n" \
"ds_read_b64_tr_b16 a[188:189], v39 offset:0\n" \
"v_cvt_pk_bf16_f32 v227, v58, v59\n" \
"v_mfma_f32_16x16x32_bf16 a[44:47], a[172:175], a[136:139], 0\n" \
"v_cvt_pk_bf16_f32 v228, v60, v61\n" \
"v_cvt_pk_bf16_f32 v229, v62, v63\n" \
"v_cvt_pk_bf16_f32 v230, v64, v65\n" \
"v_mfma_f32_16x16x32_bf16 a[44:47], a[236:239], a[200:203], a[44:47]\n" \
"ds_read_b64_tr_b16 a[190:191], v39 offset:1024\n" \
"v_cvt_pk_bf16_f32 v231, v66, v67\n" \
"v_mfma_f32_16x16x32_bf16 a[40:43], a[136:139], a[168:171], 0\n" \
"s_add_u32 s89, s41, 0x3000\n" \
"buffer_store_dwordx4 v[208:211], v20, s[80:83], s89 offen offset:1024 sc1\n" \
"v_mfma_f32_16x16x32_bf16 a[40:43], a[200:203], a[232:235], a[40:43]\n" \
"ds_read_b64_tr_b16 a[252:253], v39 offset:8192\n" \
"v_cvt_pk_bf16_f32 v232, v76, v77\n" \
"v_mfma_f32_16x16x32_bf16 a[56:59], a[140:143], a[168:171], 0\n" \
"ds_read_b64_tr_b16 a[254:255], v39 offset:9216\n" \
"v_cvt_pk_bf16_f32 v233, v78, v79\n" \
"v_mfma_f32_16x16x32_bf16 a[56:59], a[204:207], a[232:235], a[56:59]\n" \
"v_cvt_pk_bf16_f32 v234, v80, v81\n" \
"v_cvt_pk_bf16_f32 v235, v82, v83\n" \
"v_cvt_pk_bf16_f32 v236, v68, v69\n" \
"v_mfma_f32_16x16x32_bf16 a[60:63], a[172:175], a[140:143], 0\n" \
"v_cvt_pk_bf16_f32 v237, v70, v71\n" \
"v_cvt_pk_bf16_f32 v238, v72, v73\n" \
"v_cvt_pk_bf16_f32 v239, v74, v75\n" \
"v_mfma_f32_16x16x32_bf16 a[60:63], a[236:239], a[204:207], a[60:63]\n" \
"s_add_u32 s88, s41, 0x3000\n" \
"buffer_store_dwordx4 v[212:215], v20, s[80:83], s88 offen offset:0 sc1\n" \
"v_mfma_f32_16x16x32_bf16 a[52:55], a[164:167], a[140:143], 0\n" \
"v_cvt_pk_bf16_f32 v240, v84, v85\n" \
"v_cvt_pk_bf16_f32 v241, v86, v87\n" \
"v_cvt_pk_bf16_f32 v242, v88, v89\n" \
"v_mfma_f32_16x16x32_bf16 a[52:55], a[228:231], a[204:207], a[52:55]\n" \
"v_cvt_pk_bf16_f32 v243, v90, v91\n" \
"v_cvt_pk_bf16_f32 v244, v92, v93\n" \
"v_cvt_pk_bf16_f32 v245, v94, v95\n" \
"v_mfma_f32_16x16x32_bf16 a[48:51], a[160:163], a[140:143], 0\n" \
"v_cvt_pk_bf16_f32 v246, v96, v97\n" \
"v_cvt_pk_bf16_f32 v247, v98, v99\n" \
"v_cvt_pk_bf16_f32 v184, v108, v109\n" \
"v_mfma_f32_16x16x32_bf16 a[48:51], a[224:227], a[204:207], a[48:51]\n" \
"v_cvt_pk_bf16_f32 v185, v110, v111\n" \
"v_cvt_pk_bf16_f32 v186, v112, v113\n" \
"v_cvt_pk_bf16_f32 v187, v114, v115\n" \
"s_waitcnt lgkmcnt(15)\n" \
"s_nop 0\n" \
"v_mfma_f32_16x16x32_bf16 a[64:67], a[164:167], v[248:251], 0\n" \
"v_cvt_pk_bf16_f32 v188, v100, v101\n" \
"v_cvt_pk_bf16_f32 v189, v102, v103\n" \
"v_cvt_pk_bf16_f32 v190, v104, v105\n" \
"v_mfma_f32_16x16x32_bf16 a[64:67], a[228:231], v[48:51], a[64:67]\n" \
"v_cvt_pk_bf16_f32 v191, v106, v107\n" \
"v_cvt_pk_bf16_f32 v192, v116, v117\n" \
"v_cvt_pk_bf16_f32 v193, v118, v119\n" \
"v_mfma_f32_16x16x32_bf16 a[80:83], a[172:175], v[248:251], 0\n" \
"v_cvt_pk_bf16_f32 v194, v120, v121\n" \
"v_cvt_pk_bf16_f32 v195, v122, v123\n" \
"v_cvt_pk_bf16_f32 v196, v124, v125\n" \
"v_mfma_f32_16x16x32_bf16 a[80:83], a[236:239], v[48:51], a[80:83]\n" \
"v_cvt_pk_bf16_f32 v197, v126, v127\n" \
"v_cvt_pk_bf16_f32 v198, v128, v129\n" \
"v_cvt_pk_bf16_f32 v199, v130, v131\n" \
"v_mfma_f32_16x16x32_bf16 a[68:71], v[248:251], a[168:171], 0\n" \
"v_cvt_pk_bf16_f32 v252, v140, v141\n" \
"v_cvt_pk_bf16_f32 v253, v142, v143\n" \
"v_cvt_pk_bf16_f32 v254, v144, v145\n" \
"v_mfma_f32_16x16x32_bf16 a[68:71], v[48:51], a[232:235], a[68:71]\n" \
"v_cvt_pk_bf16_f32 v255, v146, v147\n" \
"v_accvgpr_write_b32 a84, v252\n" \
"v_accvgpr_write_b32 a85, v253\n" \
"s_waitcnt vmcnt(4) lgkmcnt(0)\n" \
"s_barrier\n" \
"v_mfma_f32_16x16x32_bf16 v[52:55], a[176:179], a[128:131], 0\n" \
"s_add_u32 m0, s40, 0x0\n" \
"s_nop 0\n" \
"s_add_u32 s85, s39, 0x4000\n" \
"v_accvgpr_write_b32 a86, v254\n" \
"v_mfma_f32_16x16x32_bf16 v[52:55], a[240:243], a[192:195], v[52:55]\n" \
"buffer_load_dwordx4 v20, s[80:83], s85 offen sc1 lds\n" \
"v_accvgpr_write_b32 a87, v255\n" \
"v_mfma_f32_16x16x32_bf16 v[56:59], a[180:183], a[128:131], 0\n" \
"ds_read_b64_tr_b16 a[160:161], v32 offset:32768\n" \
"v_cvt_pk_bf16_f32 v252, v132, v133\n" \
"v_mfma_f32_16x16x32_bf16 v[56:59], a[244:247], a[192:195], v[56:59]\n" \
"ds_read_b64_tr_b16 a[162:163], v32 offset:33280\n" \
"v_cvt_pk_bf16_f32 v253, v134, v135\n" \
"v_mfma_f32_16x16x32_bf16 v[60:63], a[184:187], a[128:131], 0\n" \
"s_add_u32 m0, s40, 0x1000\n" \
"s_add_u32 s85, s39, 0x5000\n" \
"v_cvt_pk_bf16_f32 v254, v136, v137\n" \
"v_mfma_f32_16x16x32_bf16 v[60:63], a[248:251], a[192:195], v[60:63]\n" \
"buffer_load_dwordx4 v20, s[80:83], s85 offen sc1 lds\n" \
"v_cvt_pk_bf16_f32 v255, v138, v139\n" \
"v_mfma_f32_16x16x32_bf16 v[64:67], a[188:191], a[128:131], 0\n" \
"ds_read_b64_tr_b16 a[224:225], v32 offset:36864\n" \
"v_cvt_pk_bf16_f32 v200, v148, v149\n" \
"v_mfma_f32_16x16x32_bf16 v[64:67], a[252:255], a[192:195], v[64:67]\n" \
"ds_read_b128 a[128:131], v28 offset:32768\n" \
"v_cvt_pk_bf16_f32 v201, v150, v151\n" \
"v_mfma_f32_16x16x32_bf16 v[80:83], a[188:191], a[132:135], 0\n" \
"ds_read_b128 a[192:195], v29 offset:32768\n" \
"v_cvt_pk_bf16_f32 v202, v152, v153\n" \
"v_mfma_f32_16x16x32_bf16 v[80:83], a[252:255], a[196:199], v[80:83]\n" \
"s_add_u32 m0, s40, 0x3000\n" \
"s_movk_i32 s84, 0x100\n" \
"s_nop 0\n" \
"v_cvt_pk_bf16_f32 v203, v154, v155\n" \
"v_mfma_f32_16x16x32_bf16 v[76:79], a[184:187], a[132:135], 0\n" \
"buffer_load_dwordx4 v21, s[76:79], s84 offen offset:0 lds\n" \
"v_cvt_pk_bf16_f32 v204, v156, v157\n" \
"v_mfma_f32_16x16x32_bf16 v[76:79], a[248:251], a[196:199], v[76:79]\n" \
"ds_read_b64_tr_b16 a[226:227], v32 offset:37376\n" \
"v_cvt_pk_bf16_f32 v205, v158, v159\n" \
"v_mfma_f32_16x16x32_bf16 v[72:75], a[180:183], a[132:135], 0\n" \
"ds_read_b64_tr_b16 a[164:165], v33 offset:32768\n" \
"v_cvt_pk_bf16_f32 v206, v160, v161\n" \
"v_mfma_f32_16x16x32_bf16 v[72:75], a[244:247], a[196:199], v[72:75]\n" \
"v_cvt_pk_bf16_f32 v207, v162, v163\n" \
"v_cvt_pk_bf16_f32 v208, v172, v173\n" \
"v_cvt_pk_bf16_f32 v209, v174, v175\n" \
"v_mfma_f32_16x16x32_bf16 v[68:71], a[176:179], a[132:135], 0\n" \
"ds_read_b64_tr_b16 a[166:167], v33 offset:33280\n" \
"v_cvt_pk_bf16_f32 v210, v176, v177\n" \
"v_mfma_f32_16x16x32_bf16 v[68:71], a[240:243], a[196:199], v[68:71]\n" \
"ds_read_b128 a[132:135], v28 offset:34816\n" \
"v_cvt_pk_bf16_f32 v211, v178, v179\n" \
"v_mfma_f32_16x16x32_bf16 v[84:87], a[176:179], a[136:139], 0\n" \
"ds_read_b128 a[196:199], v29 offset:34816\n" \
"v_cvt_pk_bf16_f32 v212, v164, v165\n" \
"v_mfma_f32_16x16x32_bf16 v[84:87], a[240:243], a[200:203], v[84:87]\n" \
"s_add_u32 m0, s31, 0x0\n" \
"s_mov_b32 s84, 0x100000\n" \
"v_cvt_pk_bf16_f32 v213, v166, v167\n" \
"v_mfma_f32_16x16x32_bf16 v[88:91], a[180:183], a[136:139], 0\n" \
"buffer_load_dwordx4 v26, s[68:71], s84 offen offset:0 lds\n" \
"v_cvt_pk_bf16_f32 v214, v168, v169\n" \
"v_mfma_f32_16x16x32_bf16 v[88:91], a[244:247], a[200:203], v[88:91]\n" \
"s_add_u32 s89, s41, 0x0\n" \
"buffer_store_dwordx4 v[224:227], v20, s[80:83], s89 offen offset:2048 sc1\n" \
"v_mfma_f32_16x16x32_bf16 v[92:95], a[184:187], a[136:139], 0\n" \
"ds_read_b64_tr_b16 a[228:229], v33 offset:36864\n" \
"v_cvt_pk_bf16_f32 v215, v170, v171\n" \
"v_mfma_f32_16x16x32_bf16 v[92:95], a[248:251], a[200:203], v[92:95]\n" \
"ds_read_b64_tr_b16 a[230:231], v33 offset:37376\n" \
"v_mfma_f32_16x16x32_bf16 v[96:99], a[188:191], a[136:139], 0\n" \
"v_mfma_f32_16x16x32_bf16 v[96:99], a[252:255], a[200:203], v[96:99]\n" \
"ds_read_b128 a[136:139], v28 offset:36864\n" \
"v_mfma_f32_16x16x32_bf16 v[112:115], a[188:191], a[140:143], 0\n" \
"ds_read_b128 a[200:203], v29 offset:36864\n" \
"v_mfma_f32_16x16x32_bf16 v[112:115], a[252:255], a[204:207], v[112:115]\n" \
"s_addk_i32 s84, 0x7c00\n" \
"v_mfma_f32_16x16x32_bf16 v[108:111], a[184:187], a[140:143], 0\n" \
"buffer_load_dwordx4 v26, s[68:71], s84 offen offset:1024 lds\n" \
"v_mfma_f32_16x16x32_bf16 v[108:111], a[248:251], a[204:207], v[108:111]\n" \
"ds_read_b64_tr_b16 a[172:173], v35 offset:32768\n" \
"v_mfma_f32_16x16x32_bf16 v[104:107], a[180:183], a[140:143], 0\n" \
"s_addk_i32 s84, 0x7c00\n" \
"v_mfma_f32_16x16x32_bf16 v[104:107], a[244:247], a[204:207], v[104:107]\n" \
"buffer_load_dwordx4 v27, s[68:71], s84 offen offset:2048 lds\n" \
"v_mfma_f32_16x16x32_bf16 v[100:103], a[176:179], a[140:143], 0\n" \
"ds_read_b64_tr_b16 a[174:175], v35 offset:33280\n" \
"v_mfma_f32_16x16x32_bf16 v[100:103], a[240:243], a[204:207], v[100:103]\n" \
"ds_read_b128 a[140:143], v28 offset:38912\n" \
"v_mfma_f32_16x16x32_bf16 v[116:119], a[176:179], a[144:147], 0\n" \
"ds_read_b128 a[204:207], v29 offset:38912\n" \
"v_mfma_f32_16x16x32_bf16 v[116:119], a[240:243], a[208:211], v[116:119]\n" \
"s_addk_i32 s84, 0x7c00\n" \
"v_mfma_f32_16x16x32_bf16 v[120:123], a[180:183], a[144:147], 0\n" \
"buffer_load_dwordx4 v27, s[68:71], s84 offen offset:3072 lds\n" \
"v_mfma_f32_16x16x32_bf16 v[120:123], a[244:247], a[208:211], v[120:123]\n" \
"ds_read_b64_tr_b16 a[236:237], v35 offset:36864\n" \
"v_mfma_f32_16x16x32_bf16 v[124:127], a[184:187], a[144:147], 0\n" \
"ds_read_b64_tr_b16 a[238:239], v35 offset:37376\n" \
"v_mfma_f32_16x16x32_bf16 v[124:127], a[248:251], a[208:211], v[124:127]\n" \
"s_add_u32 m0, s30, 0x0\n" \
"s_movk_i32 s84, 0x180\n" \
"v_mfma_f32_16x16x32_bf16 v[128:131], a[188:191], a[144:147], 0\n" \
"buffer_load_dwordx4 v24, s[76:79], s84 offen offset:0 lds\n" \
"v_mfma_f32_16x16x32_bf16 v[128:131], a[252:255], a[208:211], v[128:131]\n" \
"ds_read_b128 a[144:147], v28 offset:40960\n" \
"v_mfma_f32_16x16x32_bf16 v[144:147], a[188:191], a[148:151], 0\n" \
"ds_read_b128 a[208:211], v29 offset:40960\n" \
"v_mfma_f32_16x16x32_bf16 v[144:147], a[252:255], a[212:215], v[144:147]\n" \
"ds_read_b64_tr_b16 a[168:169], v34 offset:32768\n" \
"v_mfma_f32_16x16x32_bf16 v[140:143], a[184:187], a[148:151], 0\n" \
"s_movk_i32 s84, 0x2180\n" \
"v_mfma_f32_16x16x32_bf16 v[140:143], a[248:251], a[212:215], v[140:143]\n" \
"buffer_load_dwordx4 v25, s[76:79], s84 offen offset:1024 lds\n" \
"v_mfma_f32_16x16x32_bf16 v[136:139], a[180:183], a[148:151], 0\n" \
"ds_read_b64_tr_b16 a[170:171], v34 offset:33280\n" \
"v_mfma_f32_16x16x32_bf16 v[136:139], a[244:247], a[212:215], v[136:139]\n" \
"ds_read_b64_tr_b16 a[232:233], v34 offset:36864\n" \
"v_mfma_f32_16x16x32_bf16 v[132:135], a[176:179], a[148:151], 0\n" \
"ds_read_b64_tr_b16 a[234:235], v34 offset:37376\n" \
"v_mfma_f32_16x16x32_bf16 v[132:135], a[240:243], a[212:215], v[132:135]\n" \
"ds_read_b128 a[148:151], v28 offset:43008\n" \
"v_mfma_f32_16x16x32_bf16 v[148:151], a[176:179], a[152:155], 0\n" \
"ds_read_b128 a[212:215], v29 offset:43008\n" \
"v_mfma_f32_16x16x32_bf16 v[148:151], a[240:243], a[216:219], v[148:151]\n" \
"s_movk_i32 s84, 0x4180\n" \
"v_mfma_f32_16x16x32_bf16 v[152:155], a[180:183], a[152:155], 0\n" \
"buffer_load_dwordx4 v24, s[76:79], s84 offen offset:2048 lds\n" \
"v_mfma_f32_16x16x32_bf16 v[152:155], a[244:247], a[216:219], v[152:155]\n" \
"s_movk_i32 s84, 0x6180\n" \
"v_mfma_f32_16x16x32_bf16 v[156:159], a[184:187], a[152:155], 0\n" \
"buffer_load_dwordx4 v25, s[76:79], s84 offen offset:3072 lds\n" \
"v_mfma_f32_16x16x32_bf16 v[156:159], a[248:251], a[216:219], v[156:159]\n" \
"ds_read_b128 v[248:251], v22 offset:32768\n" \
"v_mfma_f32_16x16x32_bf16 v[160:163], a[188:191], a[152:155], 0\n" \
"ds_read_b128 v[48:51], v23 offset:32768\n" \
"v_mfma_f32_16x16x32_bf16 v[160:163], a[252:255], a[216:219], v[160:163]\n" \
"ds_read_b128 a[152:155], v28 offset:45056\n" \
"v_mfma_f32_16x16x32_bf16 v[176:179], a[188:191], a[156:159], 0\n" \
"ds_read_b128 a[216:219], v29 offset:45056\n" \
"v_mfma_f32_16x16x32_bf16 v[176:179], a[252:255], a[220:223], v[176:179]\n" \
"v_mfma_f32_16x16x32_bf16 v[172:175], a[184:187], a[156:159], 0\n" \
"v_mfma_f32_16x16x32_bf16 v[172:175], a[248:251], a[220:223], v[172:175]\n" \
"v_mfma_f32_16x16x32_bf16 v[168:171], a[180:183], a[156:159], 0\n" \
"v_mfma_f32_16x16x32_bf16 v[168:171], a[244:247], a[220:223], v[168:171]\n" \
"v_mfma_f32_16x16x32_bf16 v[164:167], a[176:179], a[156:159], 0\n" \
"v_mfma_f32_16x16x32_bf16 v[164:167], a[240:243], a[220:223], v[164:167]\n" \
"ds_read_b128 a[156:159], v28 offset:47104\n" \
"ds_read_b128 a[220:223], v29 offset:47104\n" \
"s_waitcnt lgkmcnt(15)\n" \
"v_mfma_f32_16x16x32_bf16 a[0:3], a[160:163], a[128:131], a[0:3]\n" \
"s_add_u32 m0, s30, 0x1000\n" \
"s_addk_i32 s84, 0x3000\n" \
"v_mfma_f32_16x16x32_bf16 a[0:3], a[224:227], a[192:195], a[0:3]\n" \
"buffer_load_dwordx4 v24, s[76:79], s84 offen offset:0 lds\n" \
"v_mfma_f32_16x16x32_bf16 a[4:7], a[164:167], a[128:131], a[4:7]\n" \
"ds_read_b64_tr_b16 a[176:177], v36 offset:32768\n" \
"v_mov_b32_e64 v11, 0\n" \
"v_mfma_f32_16x16x32_bf16 a[4:7], a[228:231], a[192:195], a[4:7]\n" \
"ds_read_b64_tr_b16 a[178:179], v36 offset:33792\n" \
"s_and_b32 s6, s2, 7\n" \
"v_mfma_f32_16x16x32_bf16 a[12:15], a[172:175], a[128:131], a[12:15]\n" \
"ds_read_b64_tr_b16 a[240:241], v36 offset:40960\n" \
"s_lshl_b32 s7, s3, 1\n" \
"s_waitcnt lgkmcnt(15)\n" \
"v_mfma_f32_16x16x32_bf16 a[12:15], a[236:239], a[192:195], a[12:15]\n" \
"s_add_u32 s88, s41, 0x0\n" \
"buffer_store_dwordx4 v[228:231], v20, s[80:83], s88 offen offset:3072 sc1\n" \
"s_waitcnt lgkmcnt(13)\n" \
"v_mfma_f32_16x16x32_bf16 a[8:11], a[128:131], a[168:171], a[8:11]\n" \
"ds_read_b64_tr_b16 a[242:243], v36 offset:41984\n" \
"s_lshr_b32 s4, s2, 5\n" \
"s_waitcnt lgkmcnt(12)\n" \
"v_mfma_f32_16x16x32_bf16 a[8:11], a[192:195], a[232:235], a[8:11]\n" \
"ds_read_b64_tr_b16 a[180:181], v37 offset:32768\n" \
"s_lshl_b32 s5, s6, 2\n" \
"v_mfma_f32_16x16x32_bf16 a[24:27], a[132:135], a[168:171], a[24:27]\n" \
"ds_read_b64_tr_b16 a[182:183], v37 offset:33792\n" \
"s_bfe_u32 s8, s2, 0x20003\n" \
"v_mfma_f32_16x16x32_bf16 a[24:27], a[196:199], a[232:235], a[24:27]\n" \
"ds_read_b64_tr_b16 a[244:245], v37 offset:40960\n" \
"s_and_b32 s7, s7, 4\n" \
"v_mfma_f32_16x16x32_bf16 a[28:31], a[172:175], a[132:135], a[28:31]\n" \
"s_and_b32 s9, s3, 1\n" \
"s_or_b32 s13, s5, s8\n" \
"v_mfma_f32_16x16x32_bf16 a[28:31], a[236:239], a[196:199], a[28:31]\n" \
"s_addk_i32 s84, 0x2000\n" \
"v_mfma_f32_16x16x32_bf16 a[20:23], a[164:167], a[132:135], a[20:23]\n" \
"buffer_load_dwordx4 v25, s[76:79], s84 offen offset:1024 lds\n" \
"v_mfma_f32_16x16x32_bf16 a[20:23], a[228:231], a[196:199], a[20:23]\n" \
"ds_read_b64_tr_b16 a[246:247], v37 offset:41984\n" \
"s_or_b32 s9, s7, s9\n" \
"v_mfma_f32_16x16x32_bf16 a[16:19], a[160:163], a[132:135], a[16:19]\n" \
"ds_read_b64_tr_b16 a[184:185], v38 offset:32768\n" \
"s_mul_i32 s7, s4, 0xa00\n" \
"v_mfma_f32_16x16x32_bf16 a[16:19], a[224:227], a[196:199], a[16:19]\n" \
"ds_read_b64_tr_b16 a[186:187], v38 offset:33792\n" \
"s_add_i32 s7, s7, 0x5bc1000\n" \
"v_mfma_f32_16x16x32_bf16 a[32:35], a[160:163], a[136:139], a[32:35]\n" \
"s_addk_i32 s84, 0x2000\n" \
"v_mfma_f32_16x16x32_bf16 a[32:35], a[224:227], a[200:203], a[32:35]\n" \
"buffer_load_dwordx4 v24, s[76:79], s84 offen offset:2048 lds\n" \
"v_mfma_f32_16x16x32_bf16 a[36:39], a[164:167], a[136:139], a[36:39]\n" \
"ds_read_b64_tr_b16 a[248:249], v38 offset:40960\n" \
"s_lshl_b32 s10, s13, 2\n" \
"v_mfma_f32_16x16x32_bf16 a[36:39], a[228:231], a[200:203], a[36:39]\n" \
"ds_read_b64_tr_b16 a[250:251], v38 offset:41984\n" \
"s_or_b32 s10, s10, s7\n" \
"v_mfma_f32_16x16x32_bf16 a[44:47], a[172:175], a[136:139], a[44:47]\n" \
"ds_read_b64_tr_b16 a[188:189], v39 offset:32768\n" \
"v_writelane_b32 v11, s10, 0\n" \
"v_mfma_f32_16x16x32_bf16 a[44:47], a[236:239], a[200:203], a[44:47]\n" \
"ds_read_b64_tr_b16 a[190:191], v39 offset:33792\n" \
"s_lshl_b32 s14, s4, 3\n" \
"v_mfma_f32_16x16x32_bf16 a[40:43], a[136:139], a[168:171], a[40:43]\n" \
"s_add_u32 s89, s41, 0x1000\n" \
"buffer_store_dwordx4 v[232:235], v20, s[80:83], s89 offen offset:3072 sc1\n" \
"v_mfma_f32_16x16x32_bf16 a[40:43], a[200:203], a[232:235], a[40:43]\n" \
"ds_read_b64_tr_b16 a[252:253], v39 offset:40960\n" \
"v_writelane_b32 v11, s7, 1\n" \
"v_mfma_f32_16x16x32_bf16 a[56:59], a[140:143], a[168:171], a[56:59]\n" \
"ds_read_b64_tr_b16 a[254:255], v39 offset:41984\n" \
"s_or_b32 s7, s14, s6\n" \
"v_mfma_f32_16x16x32_bf16 a[56:59], a[204:207], a[232:235], a[56:59]\n" \
"s_lshl_b32 s7, s7, 10\n" \
"s_lshl_b32 s10, s3, 8\n" \
"v_mfma_f32_16x16x32_bf16 a[60:63], a[172:175], a[140:143], a[60:63]\n" \
"s_or_b32 s7, s7, s10\n" \
"s_or_b32 s10, s7, 0x5c00200\n" \
"v_mfma_f32_16x16x32_bf16 a[60:63], a[236:239], a[204:207], a[60:63]\n" \
"s_addk_i32 s84, 0x2000\n" \
"v_mfma_f32_16x16x32_bf16 a[52:55], a[164:167], a[140:143], a[52:55]\n" \
"buffer_load_dwordx4 v25, s[76:79], s84 offen offset:3072 lds\n" \
"v_mfma_f32_16x16x32_bf16 a[52:55], a[228:231], a[204:207], a[52:55]\n" \
"v_writelane_b32 v11, s10, 2\n" \
"s_lshl_b32 s10, s8, 2\n" \
"v_mfma_f32_16x16x32_bf16 a[48:51], a[160:163], a[140:143], a[48:51]\n" \
"s_or_b32 s11, s10, s7\n" \
"s_or_b32 s11, s11, 0x5c00000\n" \
"v_mfma_f32_16x16x32_bf16 a[48:51], a[224:227], a[204:207], a[48:51]\n" \
"v_writelane_b32 v11, s11, 3\n" \
"s_or_b32 s7, s7, 0x5c00080\n" \
"s_waitcnt lgkmcnt(15)\n" \
"v_mfma_f32_16x16x32_bf16 a[64:67], a[164:167], v[248:251], a[64:67]\n" \
"v_writelane_b32 v11, s7, 4\n" \
"s_or_b32 s7, s7, s10\n" \
"v_mfma_f32_16x16x32_bf16 a[64:67], a[228:231], v[48:51], a[64:67]\n" \
"s_add_i32 s10, s3, s2\n" \
"v_writelane_b32 v11, s7, 5\n" \
"v_mfma_f32_16x16x32_bf16 a[80:83], a[172:175], v[248:251], a[80:83]\n" \
"s_mov_b32 s7, 2.0\n" \
"s_mov_b32 s11, 0x5bc0000\n" \
"v_mfma_f32_16x16x32_bf16 a[80:83], a[236:239], v[48:51], a[80:83]\n" \
"s_cmp_lg_u32 s10, 0\n" \
"s_cselect_b32 s10, s7, s11\n" \
"v_mfma_f32_16x16x32_bf16 a[68:71], v[248:251], a[168:171], a[68:71]\n" \
"s_lshr_b32 s15, s2, 3\n" \
"v_writelane_b32 v11, s10, 54\n" \
"s_waitcnt vmcnt(10) lgkmcnt(0)\n" \
"v_mfma_f32_16x16x32_bf16 a[68:71], v[48:51], a[232:235], a[68:71]\n" \
"s_barrier\n" \
"v_xor_b32_e32 v28, 0x20000, v28\n" \
"v_xor_b32_e32 v29, 0x20000, v29\n" \
"v_mfma_f32_16x16x32_bf16 v[52:55], a[176:179], a[128:131], v[52:55]\n" \
"s_add_u32 m0, s40, 0x8000\n" \
"s_add_u32 s85, s39, 0x6000\n" \
"v_mfma_f32_16x16x32_bf16 v[52:55], a[240:243], a[192:195], v[52:55]\n" \
"buffer_load_dwordx4 v20, s[80:83], s85 offen sc1 lds\n" \
"v_mfma_f32_16x16x32_bf16 v[56:59], a[180:183], a[128:131], v[56:59]\n" \
"ds_read_b64_tr_b16 a[160:161], v32 offset:0\n" \
"s_lshl_b32 s10, s6, 5\n" \
"v_mfma_f32_16x16x32_bf16 v[56:59], a[244:247], a[192:195], v[56:59]\n" \
"ds_read_b64_tr_b16 a[162:163], v32 offset:512\n" \
"s_min_u32 s16, s3, 2\n" \
"s_nop 0\n" \
"v_mfma_f32_16x16x32_bf16 v[60:63], a[184:187], a[128:131], v[60:63]\n" \
"s_add_u32 m0, s40, 0x9000\n" \
"s_add_u32 s85, s39, 0x7000\n" \
"v_mfma_f32_16x16x32_bf16 v[60:63], a[248:251], a[192:195], v[60:63]\n" \
"buffer_load_dwordx4 v20, s[80:83], s85 offen sc1 lds\n" \
"v_mfma_f32_16x16x32_bf16 v[64:67], a[188:191], a[128:131], v[64:67]\n" \
"ds_read_b64_tr_b16 a[224:225], v32 offset:4096\n" \
"s_mul_i32 s12, s8, 0x120\n" \
"v_mfma_f32_16x16x32_bf16 v[64:67], a[252:255], a[192:195], v[64:67]\n" \
"ds_read_b128 a[128:131], v28 offset:0\n" \
"v_mfma_f32_16x16x32_bf16 v[80:83], a[188:191], a[132:135], v[80:83]\n" \
"ds_read_b128 a[192:195], v29 offset:0\n" \
"v_mfma_f32_16x16x32_bf16 v[80:83], a[252:255], a[196:199], v[80:83]\n" \
"s_add_u32 s88, s41, 0x1000\n" \
"buffer_store_dwordx4 v[236:239], v20, s[80:83], s88 offen offset:2048 sc1\n" \
"v_mfma_f32_16x16x32_bf16 v[76:79], a[184:187], a[132:135], v[76:79]\n" \
"ds_read_b64_tr_b16 a[226:227], v32 offset:4608\n" \
"v_mfma_f32_16x16x32_bf16 v[76:79], a[248:251], a[196:199], v[76:79]\n" \
"ds_read_b64_tr_b16 a[164:165], v33 offset:0\n" \
"v_mfma_f32_16x16x32_bf16 v[72:75], a[180:183], a[132:135], v[72:75]\n" \
"s_add_u32 m0, s40, 0xb000\n" \
"s_movk_i32 s84, 0x180\n" \
"s_nop 0\n" \
"v_mfma_f32_16x16x32_bf16 v[72:75], a[244:247], a[196:199], v[72:75]\n" \
"buffer_load_dwordx4 v21, s[76:79], s84 offen offset:0 lds\n" \
"v_mfma_f32_16x16x32_bf16 v[68:71], a[176:179], a[132:135], v[68:71]\n" \
"ds_read_b64_tr_b16 a[166:167], v33 offset:512\n" \
"v_mfma_f32_16x16x32_bf16 v[68:71], a[240:243], a[196:199], v[68:71]\n" \
"ds_read_b128 a[132:135], v28 offset:2048\n" \
"v_mfma_f32_16x16x32_bf16 v[84:87], a[176:179], a[136:139], v[84:87]\n" \
"ds_read_b128 a[196:199], v29 offset:2048\n" \
"v_mfma_f32_16x16x32_bf16 v[84:87], a[240:243], a[200:203], v[84:87]\n" \
"s_add_u32 m0, s31, 0x8000\n" \
"s_mov_b32 s84, 0x180000\n" \
"v_mfma_f32_16x16x32_bf16 v[88:91], a[180:183], a[136:139], v[88:91]\n" \
"buffer_load_dwordx4 v26, s[68:71], s84 offen offset:0 lds\n" \
"v_mfma_f32_16x16x32_bf16 v[88:91], a[244:247], a[200:203], v[88:91]\n" \
"ds_read_b64_tr_b16 a[228:229], v33 offset:4096\n" \
"v_mfma_f32_16x16x32_bf16 v[92:95], a[184:187], a[136:139], v[92:95]\n" \
"ds_read_b64_tr_b16 a[230:231], v33 offset:4608\n" \
"v_mfma_f32_16x16x32_bf16 v[92:95], a[248:251], a[200:203], v[92:95]\n" \
"s_addk_i32 s84, 0x7c00\n" \
"v_mfma_f32_16x16x32_bf16 v[96:99], a[188:191], a[136:139], v[96:99]\n" \
"buffer_load_dwordx4 v26, s[68:71], s84 offen offset:1024 lds\n" \
"v_mfma_f32_16x16x32_bf16 v[96:99], a[252:255], a[200:203], v[96:99]\n" \
"ds_read_b128 a[136:139], v28 offset:4096\n" \
"v_mfma_f32_16x16x32_bf16 v[112:115], a[188:191], a[140:143], v[112:115]\n" \
"ds_read_b128 a[200:203], v29 offset:4096\n" \
"v_mfma_f32_16x16x32_bf16 v[112:115], a[252:255], a[204:207], v[112:115]\n" \
"s_addk_i32 s84, 0x7c00\n" \
"v_mfma_f32_16x16x32_bf16 v[108:111], a[184:187], a[140:143], v[108:111]\n" \
"buffer_load_dwordx4 v27, s[68:71], s84 offen offset:2048 lds\n" \
"v_mfma_f32_16x16x32_bf16 v[108:111], a[248:251], a[204:207], v[108:111]\n" \
"ds_read_b64_tr_b16 a[172:173], v35 offset:0\n" \
"v_mfma_f32_16x16x32_bf16 v[104:107], a[180:183], a[140:143], v[104:107]\n" \
"ds_read_b64_tr_b16 a[174:175], v35 offset:512\n" \
"v_mfma_f32_16x16x32_bf16 v[104:107], a[244:247], a[204:207], v[104:107]\n" \
"ds_read_b64_tr_b16 a[236:237], v35 offset:4096\n" \
"v_mfma_f32_16x16x32_bf16 v[100:103], a[176:179], a[140:143], v[100:103]\n" \
"s_add_u32 s11, s22, s12\n" \
"s_addc_u32 s17, s23, 0\n" \
"v_mfma_f32_16x16x32_bf16 v[100:103], a[240:243], a[204:207], v[100:103]\n" \
"ds_read_b128 a[140:143], v28 offset:6144\n" \
"v_mfma_f32_16x16x32_bf16 v[116:119], a[176:179], a[144:147], v[116:119]\n" \
"ds_read_b128 a[204:207], v29 offset:6144\n" \
"v_mfma_f32_16x16x32_bf16 v[116:119], a[240:243], a[208:211], v[116:119]\n" \
"s_addk_i32 s84, 0x7c00\n" \
"v_mfma_f32_16x16x32_bf16 v[120:123], a[180:183], a[144:147], v[120:123]\n" \
"buffer_load_dwordx4 v27, s[68:71], s84 offen offset:3072 lds\n" \
"v_mfma_f32_16x16x32_bf16 v[120:123], a[244:247], a[208:211], v[120:123]\n" \
"s_add_u32 m0, s30, 0x8000\n" \
"s_movk_i32 s84, 0x200\n" \
"v_mfma_f32_16x16x32_bf16 v[124:127], a[184:187], a[144:147], v[124:127]\n" \
"buffer_load_dwordx4 v24, s[76:79], s84 offen offset:0 lds\n" \
"v_mfma_f32_16x16x32_bf16 v[124:127], a[248:251], a[208:211], v[124:127]\n" \
"ds_read_b64_tr_b16 a[238:239], v35 offset:4608\n" \
"v_mfma_f32_16x16x32_bf16 v[128:131], a[188:191], a[144:147], v[128:131]\n" \
"s_add_u32 s89, s41, 0x2000\n" \
"buffer_store_dwordx4 v[240:243], v20, s[80:83], s89 offen offset:2048 sc1\n" \
"v_mfma_f32_16x16x32_bf16 v[128:131], a[252:255], a[208:211], v[128:131]\n" \
"ds_read_b128 a[144:147], v28 offset:8192\n" \
"v_mfma_f32_16x16x32_bf16 v[144:147], a[188:191], a[148:151], v[144:147]\n" \
"ds_read_b128 a[208:211], v29 offset:8192\n" \
"v_mfma_f32_16x16x32_bf16 v[144:147], a[252:255], a[212:215], v[144:147]\n" \
"ds_read_b64_tr_b16 a[168:169], v34 offset:0\n" \
"v_mfma_f32_16x16x32_bf16 v[140:143], a[184:187], a[148:151], v[140:143]\n" \
"ds_read_b64_tr_b16 a[170:171], v34 offset:512\n" \
"v_mfma_f32_16x16x32_bf16 v[140:143], a[248:251], a[212:215], v[140:143]\n" \
"s_movk_i32 s84, 0x2200\n" \
"s_nop 0\n" \
"v_mfma_f32_16x16x32_bf16 v[136:139], a[180:183], a[148:151], v[136:139]\n" \
"buffer_load_dwordx4 v25, s[76:79], s84 offen offset:1024 lds\n" \
"v_mfma_f32_16x16x32_bf16 v[136:139], a[244:247], a[212:215], v[136:139]\n" \
"ds_read_b64_tr_b16 a[232:233], v34 offset:4096\n" \
"v_mfma_f32_16x16x32_bf16 v[132:135], a[176:179], a[148:151], v[132:135]\n" \
"ds_read_b64_tr_b16 a[234:235], v34 offset:4608\n" \
"v_mfma_f32_16x16x32_bf16 v[132:135], a[240:243], a[212:215], v[132:135]\n" \
"ds_read_b128 a[148:151], v28 offset:10240\n" \
"v_mfma_f32_16x16x32_bf16 v[148:151], a[176:179], a[152:155], v[148:151]\n" \
"ds_read_b128 a[212:215], v29 offset:10240\n" \
"v_mfma_f32_16x16x32_bf16 v[148:151], a[240:243], a[216:219], v[148:151]\n" \
"s_movk_i32 s84, 0x4200\n" \
"v_mfma_f32_16x16x32_bf16 v[152:155], a[180:183], a[152:155], v[152:155]\n" \
"buffer_load_dwordx4 v24, s[76:79], s84 offen offset:2048 lds\n" \
"v_mfma_f32_16x16x32_bf16 v[152:155], a[244:247], a[216:219], v[152:155]\n" \
"s_movk_i32 s84, 0x6200\n" \
"v_mfma_f32_16x16x32_bf16 v[156:159], a[184:187], a[152:155], v[156:159]\n" \
"buffer_load_dwordx4 v25, s[76:79], s84 offen offset:3072 lds\n" \
"v_mfma_f32_16x16x32_bf16 v[156:159], a[248:251], a[216:219], v[156:159]\n" \
"ds_read_b128 v[248:251], v22 offset:0\n" \
"v_mfma_f32_16x16x32_bf16 v[160:163], a[188:191], a[152:155], v[160:163]\n" \
"ds_read_b128 v[48:51], v23 offset:0\n" \
"v_mfma_f32_16x16x32_bf16 v[160:163], a[252:255], a[216:219], v[160:163]\n" \
"ds_read_b128 a[152:155], v28 offset:12288\n" \
"v_mfma_f32_16x16x32_bf16 v[176:179], a[188:191], a[156:159], v[176:179]\n" \
"ds_read_b128 a[216:219], v29 offset:12288\n" \
"v_mfma_f32_16x16x32_bf16 v[176:179], a[252:255], a[220:223], v[176:179]\n" \
"s_add_u32 s18, s24, s12\n" \
"v_writelane_b32 v11, s11, 9\n" \
"s_addc_u32 s19, s25, 0\n" \
"v_mfma_f32_16x16x32_bf16 v[172:175], a[184:187], a[156:159], v[172:175]\n" \
"s_and_b32 s20, s17, 0xffff\n" \
"v_writelane_b32 v11, s20, 10\n" \
"v_mfma_f32_16x16x32_bf16 v[172:175], a[248:251], a[220:223], v[172:175]\n" \
"s_and_b32 s20, s19, 0xffff\n" \
"v_writelane_b32 v11, s18, 11\n" \
"v_mfma_f32_16x16x32_bf16 v[168:171], a[180:183], a[156:159], v[168:171]\n" \
"s_cmpk_lt_u32 s3, 3\n" \
"s_cselect_b32 s11, s11, s18\n" \
"v_mfma_f32_16x16x32_bf16 v[168:171], a[244:247], a[220:223], v[168:171]\n" \
"s_movk_i32 s18, 0xfff\n" \
"v_writelane_b32 v11, s20, 12\n" \
"v_mfma_f32_16x16x32_bf16 v[164:167], a[176:179], a[156:159], v[164:167]\n" \
"s_and_b32 s20, s29, 0xffff\n" \
"v_writelane_b32 v11, s28, 13\n" \
"v_mfma_f32_16x16x32_bf16 v[164:167], a[240:243], a[220:223], v[164:167]\n" \
"ds_read_b128 a[156:159], v28 offset:14336\n" \
"ds_read_b128 a[220:223], v29 offset:14336\n" \
"s_waitcnt lgkmcnt(15)\n" \
"v_mfma_f32_16x16x32_bf16 a[0:3], a[160:163], a[128:131], a[0:3]\n" \
"s_add_u32 m0, s30, 0x9000\n" \
"s_addk_i32 s84, 0x3000\n" \
"v_mfma_f32_16x16x32_bf16 a[0:3], a[224:227], a[192:195], a[0:3]\n" \
"buffer_load_dwordx4 v24, s[76:79], s84 offen offset:0 lds\n" \
"v_mfma_f32_16x16x32_bf16 a[4:7], a[164:167], a[128:131], a[4:7]\n" \
"ds_read_b64_tr_b16 a[176:177], v36 offset:0\n" \
"v_writelane_b32 v11, s20, 14\n" \
"v_mfma_f32_16x16x32_bf16 a[4:7], a[228:231], a[192:195], a[4:7]\n" \
"ds_read_b64_tr_b16 a[178:179], v36 offset:1024\n" \
"v_writelane_b32 v11, s11, 15\n" \
"v_mfma_f32_16x16x32_bf16 a[12:15], a[172:175], a[128:131], a[12:15]\n" \
"ds_read_b64_tr_b16 a[240:241], v36 offset:8192\n" \
"s_waitcnt lgkmcnt(15)\n" \
"v_mfma_f32_16x16x32_bf16 a[12:15], a[236:239], a[192:195], a[12:15]\n" \
"s_add_u32 s88, s41, 0x2000\n" \
"buffer_store_dwordx4 v[244:247], v20, s[80:83], s88 offen offset:3072 sc1\n" \
"s_waitcnt lgkmcnt(13)\n" \
"v_mfma_f32_16x16x32_bf16 a[8:11], a[128:131], a[168:171], a[8:11]\n" \
"ds_read_b64_tr_b16 a[242:243], v36 offset:9216\n" \
"s_waitcnt lgkmcnt(12)\n" \
"v_mfma_f32_16x16x32_bf16 a[8:11], a[192:195], a[232:235], a[8:11]\n" \
"ds_read_b64_tr_b16 a[180:181], v37 offset:0\n" \
"v_mfma_f32_16x16x32_bf16 a[24:27], a[132:135], a[168:171], a[24:27]\n" \
"ds_read_b64_tr_b16 a[182:183], v37 offset:1024\n" \
"v_mfma_f32_16x16x32_bf16 a[24:27], a[196:199], a[232:235], a[24:27]\n" \
"ds_read_b64_tr_b16 a[244:245], v37 offset:8192\n" \
"v_mfma_f32_16x16x32_bf16 a[28:31], a[172:175], a[132:135], a[28:31]\n" \
"s_cmpk_lt_u32 s3, 3\n" \
"s_cselect_b32 s11, s17, s19\n" \
"v_mfma_f32_16x16x32_bf16 a[28:31], a[236:239], a[196:199], a[28:31]\n" \
"s_addk_i32 s84, 0x2000\n" \
"s_nop 0\n" \
"v_mfma_f32_16x16x32_bf16 a[20:23], a[164:167], a[132:135], a[20:23]\n" \
"buffer_load_dwordx4 v25, s[76:79], s84 offen offset:1024 lds\n" \
"v_mfma_f32_16x16x32_bf16 a[20:23], a[228:231], a[196:199], a[20:23]\n" \
"ds_read_b64_tr_b16 a[246:247], v37 offset:9216\n" \
"s_and_b32 s11, s11, 0xffff\n" \
"v_mfma_f32_16x16x32_bf16 a[16:19], a[160:163], a[132:135], a[16:19]\n" \
"ds_read_b64_tr_b16 a[184:185], v38 offset:0\n" \
"v_writelane_b32 v11, s11, 16\n" \
"v_mfma_f32_16x16x32_bf16 a[16:19], a[224:227], a[196:199], a[16:19]\n" \
"ds_read_b64_tr_b16 a[186:187], v38 offset:1024\n" \
"s_mul_i32 s11, s6, 0x48\n" \
"v_mfma_f32_16x16x32_bf16 a[32:35], a[160:163], a[136:139], a[32:35]\n" \
"s_addk_i32 s84, 0x2000\n" \
"s_nop 0\n" \
"v_mfma_f32_16x16x32_bf16 a[32:35], a[224:227], a[200:203], a[32:35]\n" \
"buffer_load_dwordx4 v24, s[76:79], s84 offen offset:2048 lds\n" \
"v_mfma_f32_16x16x32_bf16 a[36:39], a[164:167], a[136:139], a[36:39]\n" \
"ds_read_b64_tr_b16 a[248:249], v38 offset:8192\n" \
"s_add_i32 s17, s11, 0x47\n" \
"v_mfma_f32_16x16x32_bf16 a[36:39], a[228:231], a[200:203], a[36:39]\n" \
"ds_read_b64_tr_b16 a[250:251], v38 offset:9216\n" \
".fill (L_pb + 24288 - .) / 4, 4, 0xBF800000\n" \
"L_pp6:\n" \
"s_cbranch_execz L_pret\n" \
"s_nop 0\n" \
"v_mfma_f32_16x16x32_bf16 a[44:47], a[172:175], a[136:139], a[44:47]\n" \
"ds_read_b64_tr_b16 a[188:189], v39 offset:0\n" \
"v_mfma_f32_16x16x32_bf16 a[44:47], a[236:239], a[200:203], a[44:47]\n" \
"ds_read_b64_tr_b16 a[190:191], v39 offset:1024\n" \
"v_mfma_f32_16x16x32_bf16 a[40:43], a[136:139], a[168:171], a[40:43]\n" \
"s_add_u32 s89, s41, 0x3000\n" \
"buffer_store_dwordx4 v[184:187], v20, s[80:83], s89 offen offset:3072 sc1\n" \
"v_mfma_f32_16x16x32_bf16 a[40:43], a[200:203], a[232:235], a[40:43]\n" \
"ds_read_b64_tr_b16 a[252:253], v39 offset:8192\n" \
"v_mfma_f32_16x16x32_bf16 a[56:59], a[140:143], a[168:171], a[56:59]\n" \
"ds_read_b64_tr_b16 a[254:255], v39 offset:9216\n" \
"v_mfma_f32_16x16x32_bf16 a[56:59], a[204:207], a[232:235], a[56:59]\n" \
"s_cmpk_lt_u32 s3, 3\n" \
"s_cselect_b32 s17, s17, s18\n" \
"v_mfma_f32_16x16x32_bf16 a[60:63], a[172:175], a[140:143], a[60:63]\n" \
"s_mulk_i32 s17, 0x480\n" \
"s_addk_i32 s17, 0x120\n" \
"v_mfma_f32_16x16x32_bf16 a[60:63], a[236:239], a[204:207], a[60:63]\n" \
"s_addk_i32 s84, 0x2000\n" \
"v_mfma_f32_16x16x32_bf16 a[52:55], a[164:167], a[140:143], a[52:55]\n" \
"buffer_load_dwordx4 v25, s[76:79], s84 offen offset:3072 lds\n" \
"v_mfma_f32_16x16x32_bf16 a[52:55], a[228:231], a[204:207], a[52:55]\n" \
"v_writelane_b32 v11, s17, 17\n" \
"s_lshl_b32 s17, s3, 5\n" \
"v_mfma_f32_16x16x32_bf16 a[48:51], a[160:163], a[140:143], a[48:51]\n" \
"s_add_i32 s11, s11, s17\n" \
"s_mul_i32 s17, s11, 0x480\n" \
"v_mfma_f32_16x16x32_bf16 a[48:51], a[224:227], a[204:207], a[48:51]\n" \
"s_mul_i32 s11, s13, 0x24000\n" \
"s_add_i32 s18, s11, 0x1b000\n" \
"s_waitcnt lgkmcnt(15)\n" \
"v_mfma_f32_16x16x32_bf16 a[64:67], a[164:167], v[248:251], a[64:67]\n" \
"s_cmpk_lt_u32 s3, 3\n" \
"s_cselect_b32 s17, s17, s18\n" \
"v_mfma_f32_16x16x32_bf16 a[64:67], a[228:231], v[48:51], a[64:67]\n" \
"s_mov_b32 s18, 0x8000\n" \
"v_writelane_b32 v11, s17, 18\n" \
"v_mfma_f32_16x16x32_bf16 a[80:83], a[172:175], v[248:251], a[80:83]\n" \
"s_mul_i32 s17, s16, 0x2400\n" \
"s_mul_i32 s16, s16, 0x9000\n" \
"v_mfma_f32_16x16x32_bf16 a[80:83], a[236:239], v[48:51], a[80:83]\n" \
"s_bitset1_b32 s17, 17\n" \
"v_writelane_b32 v11, s17, 19\n" \
"v_mfma_f32_16x16x32_bf16 a[68:71], v[248:251], a[168:171], a[68:71]\n" \
"s_add_i32 s16, s11, s16\n" \
"v_writelane_b32 v11, s16, 26\n" \
"s_waitcnt vmcnt(11) lgkmcnt(0)\n" \
"v_mfma_f32_16x16x32_bf16 a[68:71], v[48:51], a[232:235], a[68:71]\n" \
"s_barrier\n" \
"v_xor_b32_e32 v28, 0x20000, v28\n" \
"v_xor_b32_e32 v29, 0x20000, v29\n" \
"v_mfma_f32_16x16x32_bf16 v[52:55], a[176:179], a[128:131], v[52:55]\n" \
"s_add_u32 m0, s40, 0x0\n" \
"s_nop 0\n" \
"s_add_u32 s85, s39, 0x8000\n" \
"v_mfma_f32_16x16x32_bf16 v[52:55], a[240:243], a[192:195], v[52:55]\n" \
"buffer_load_dwordx4 v20, s[80:83], s85 offen sc1 lds\n" \
"v_mfma_f32_16x16x32_bf16 v[56:59], a[180:183], a[128:131], v[56:59]\n" \
"ds_read_b64_tr_b16 a[160:161], v32 offset:32768\n" \
"s_mul_i32 s16, s3, 0x2400\n" \
"v_mfma_f32_16x16x32_bf16 v[56:59], a[244:247], a[192:195], v[56:59]\n" \
"ds_read_b64_tr_b16 a[162:163], v32 offset:33280\n" \
"s_add_i32 s17, s16, 0xa400\n" \
"v_mfma_f32_16x16x32_bf16 v[60:63], a[184:187], a[128:131], v[60:63]\n" \
"s_add_u32 m0, s40, 0x1000\n" \
"s_add_u32 s85, s39, 0x9000\n" \
"v_mfma_f32_16x16x32_bf16 v[60:63], a[248:251], a[192:195], v[60:63]\n" \
"buffer_load_dwordx4 v20, s[80:83], s85 offen sc1 lds\n" \
"v_mfma_f32_16x16x32_bf16 v[64:67], a[188:191], a[128:131], v[64:67]\n" \
"ds_read_b64_tr_b16 a[224:225], v32 offset:36864\n" \
"v_mfma_f32_16x16x32_bf16 v[64:67], a[252:255], a[192:195], v[64:67]\n" \
"ds_read_b128 a[128:131], v28 offset:0\n" \
"v_mfma_f32_16x16x32_bf16 v[80:83], a[188:191], a[132:135], v[80:83]\n" \
"ds_read_b128 a[192:195], v29 offset:0\n" \
"v_mfma_f32_16x16x32_bf16 v[80:83], a[252:255], a[196:199], v[80:83]\n" \
"s_add_u32 s88, s41, 0x3000\n" \
"buffer_store_dwordx4 v[188:191], v20, s[80:83], s88 offen offset:2048 sc1\n" \
"v_mfma_f32_16x16x32_bf16 v[76:79], a[184:187], a[132:135], v[76:79]\n" \
"ds_read_b64_tr_b16 a[226:227], v32 offset:37376\n" \
"v_mfma_f32_16x16x32_bf16 v[76:79], a[248:251], a[196:199], v[76:79]\n" \
"ds_read_b64_tr_b16 a[164:165], v33 offset:32768\n" \
"v_mfma_f32_16x16x32_bf16 v[72:75], a[180:183], a[132:135], v[72:75]\n" \
"s_add_u32 m0, s40, 0x3000\n" \
"s_movk_i32 s84, 0x200\n" \
"v_mfma_f32_16x16x32_bf16 v[72:75], a[244:247], a[196:199], v[72:75]\n" \
"buffer_load_dwordx4 v21, s[76:79], s84 offen offset:0 lds\n" \
"v_mfma_f32_16x16x32_bf16 v[68:71], a[176:179], a[132:135], v[68:71]\n" \
"ds_read_b64_tr_b16 a[166:167], v33 offset:33280\n" \
"v_mfma_f32_16x16x32_bf16 v[68:71], a[240:243], a[196:199], v[68:71]\n" \
"ds_read_b128 a[132:135], v28 offset:2048\n" \
"v_mfma_f32_16x16x32_bf16 v[84:87], a[176:179], a[136:139], v[84:87]\n" \
"ds_read_b128 a[196:199], v29 offset:2048\n" \
"v_mfma_f32_16x16x32_bf16 v[84:87], a[240:243], a[200:203], v[84:87]\n" \
"s_add_u32 m0, s31, 0x0\n" \
"s_mov_b32 s84, 0x200000\n" \
"v_mfma_f32_16x16x32_bf16 v[88:91], a[180:183], a[136:139], v[88:91]\n" \
"buffer_load_dwordx4 v26, s[68:71], s84 offen offset:0 lds\n" \
"v_mfma_f32_16x16x32_bf16 v[88:91], a[244:247], a[200:203], v[88:91]\n" \
"ds_read_b64_tr_b16 a[228:229], v33 offset:36864\n" \
"v_mfma_f32_16x16x32_bf16 v[92:95], a[184:187], a[136:139], v[92:95]\n" \
"ds_read_b64_tr_b16 a[230:231], v33 offset:37376\n" \
"v_mfma_f32_16x16x32_bf16 v[92:95], a[248:251], a[200:203], v[92:95]\n" \
"s_addk_i32 s84, 0x7c00\n" \
"v_mfma_f32_16x16x32_bf16 v[96:99], a[188:191], a[136:139], v[96:99]\n" \
"buffer_load_dwordx4 v26, s[68:71], s84 offen offset:1024 lds\n" \
"v_mfma_f32_16x16x32_bf16 v[96:99], a[252:255], a[200:203], v[96:99]\n" \
"ds_read_b128 a[136:139], v28 offset:4096\n" \
"v_mfma_f32_16x16x32_bf16 v[112:115], a[188:191], a[140:143], v[112:115]\n" \
"ds_read_b128 a[200:203], v29 offset:4096\n" \
"v_mfma_f32_16x16x32_bf16 v[112:115], a[252:255], a[204:207], v[112:115]\n" \
"s_addk_i32 s84, 0x7c00\n" \
"v_mfma_f32_16x16x32_bf16 v[108:111], a[184:187], a[140:143], v[108:111]\n" \
"buffer_load_dwordx4 v27, s[68:71], s84 offen offset:2048 lds\n" \
"v_mfma_f32_16x16x32_bf16 v[108:111], a[248:251], a[204:207], v[108:111]\n" \
"ds_read_b64_tr_b16 a[172:173], v35 offset:32768\n" \
"v_mfma_f32_16x16x32_bf16 v[104:107], a[180:183], a[140:143], v[104:107]\n" \
"ds_read_b64_tr_b16 a[174:175], v35 offset:33280\n" \
"v_mfma_f32_16x16x32_bf16 v[104:107], a[244:247], a[204:207], v[104:107]\n" \
"ds_read_b64_tr_b16 a[236:237], v35 offset:36864\n" \
"v_mfma_f32_16x16x32_bf16 v[100:103], a[176:179], a[140:143], v[100:103]\n" \
"s_cmpk_lt_u32 s3, 3\n" \
"s_cselect_b32 s17, s17, s18\n" \
"v_mfma_f32_16x16x32_bf16 v[100:103], a[240:243], a[204:207], v[100:103]\n" \
"ds_read_b128 a[140:143], v28 offset:6144\n" \
"s_mov_b32 s18, 0x1b000\n" \
"v_mfma_f32_16x16x32_bf16 v[116:119], a[176:179], a[144:147], v[116:119]\n" \
"ds_read_b128 a[204:207], v29 offset:6144\n" \
"s_xor_b32 s19, s3, 2\n" \
"v_mfma_f32_16x16x32_bf16 v[116:119], a[240:243], a[208:211], v[116:119]\n" \
"s_addk_i32 s84, 0x7c00\n" \
"v_mfma_f32_16x16x32_bf16 v[120:123], a[180:183], a[144:147], v[120:123]\n" \
"buffer_load_dwordx4 v27, s[68:71], s84 offen offset:3072 lds\n" \
"v_mfma_f32_16x16x32_bf16 v[120:123], a[244:247], a[208:211], v[120:123]\n" \
"s_add_u32 m0, s30, 0x20000\n" \
"s_movk_i32 s84, 0x280\n" \
"v_mfma_f32_16x16x32_bf16 v[124:127], a[184:187], a[144:147], v[124:127]\n" \
"buffer_load_dwordx4 v24, s[76:79], s84 offen offset:0 lds\n" \
"v_mfma_f32_16x16x32_bf16 v[124:127], a[248:251], a[208:211], v[124:127]\n" \
"ds_read_b64_tr_b16 a[238:239], v35 offset:37376\n" \
"v_mfma_f32_16x16x32_bf16 v[128:131], a[188:191], a[144:147], v[128:131]\n" \
"s_add_u32 s89, s42, 0x0\n" \
"buffer_store_dwordx4 v[216:219], v20, s[80:83], s89 offen offset:0 sc1\n" \
"v_mfma_f32_16x16x32_bf16 v[128:131], a[252:255], a[208:211], v[128:131]\n" \
"ds_read_b128 a[144:147], v28 offset:8192\n" \
"v_mfma_f32_16x16x32_bf16 v[144:147], a[188:191], a[148:151], v[144:147]\n" \
"ds_read_b128 a[208:211], v29 offset:8192\n" \
"v_mfma_f32_16x16x32_bf16 v[144:147], a[252:255], a[212:215], v[144:147]\n" \
"ds_read_b64_tr_b16 a[168:169], v34 offset:32768\n" \
"v_mfma_f32_16x16x32_bf16 v[140:143], a[184:187], a[148:151], v[140:143]\n" \
"ds_read_b64_tr_b16 a[170:171], v34 offset:33280\n" \
"v_mfma_f32_16x16x32_bf16 v[140:143], a[248:251], a[212:215], v[140:143]\n" \
"s_movk_i32 s84, 0x2280\n" \
"s_nop 0\n" \
"v_mfma_f32_16x16x32_bf16 v[136:139], a[180:183], a[148:151], v[136:139]\n" \
"buffer_load_dwordx4 v25, s[76:79], s84 offen offset:1024 lds\n" \
"v_mfma_f32_16x16x32_bf16 v[136:139], a[244:247], a[212:215], v[136:139]\n" \
"ds_read_b64_tr_b16 a[232:233], v34 offset:36864\n" \
"v_mfma_f32_16x16x32_bf16 v[132:135], a[176:179], a[148:151], v[132:135]\n" \
"ds_read_b64_tr_b16 a[234:235], v34 offset:37376\n" \
"v_mfma_f32_16x16x32_bf16 v[132:135], a[240:243], a[212:215], v[132:135]\n" \
"ds_read_b128 a[148:151], v28 offset:10240\n" \
"v_mfma_f32_16x16x32_bf16 v[148:151], a[176:179], a[152:155], v[148:151]\n" \
"ds_read_b128 a[212:215], v29 offset:10240\n" \
"v_mfma_f32_16x16x32_bf16 v[148:151], a[240:243], a[216:219], v[148:151]\n" \
"s_movk_i32 s84, 0x4280\n" \
"v_mfma_f32_16x16x32_bf16 v[152:155], a[180:183], a[152:155], v[152:155]\n" \
"buffer_load_dwordx4 v24, s[76:79], s84 offen offset:2048 lds\n" \
"v_mfma_f32_16x16x32_bf16 v[152:155], a[244:247], a[216:219], v[152:155]\n" \
"s_movk_i32 s84, 0x6280\n" \
"v_mfma_f32_16x16x32_bf16 v[156:159], a[184:187], a[152:155], v[156:159]\n" \
"buffer_load_dwordx4 v25, s[76:79], s84 offen offset:3072 lds\n" \
"v_mfma_f32_16x16x32_bf16 v[156:159], a[248:251], a[216:219], v[156:159]\n" \
"ds_read_b128 v[248:251], v22 offset:32768\n" \
"v_mfma_f32_16x16x32_bf16 v[160:163], a[188:191], a[152:155], v[160:163]\n" \
"ds_read_b128 v[48:51], v23 offset:32768\n" \
"v_mfma_f32_16x16x32_bf16 v[160:163], a[252:255], a[216:219], v[160:163]\n" \
"ds_read_b128 a[152:155], v28 offset:12288\n" \
"v_mfma_f32_16x16x32_bf16 v[176:179], a[188:191], a[156:159], v[176:179]\n" \
"ds_read_b128 a[216:219], v29 offset:12288\n" \
"v_mfma_f32_16x16x32_bf16 v[176:179], a[252:255], a[220:223], v[176:179]\n" \
"s_cmp_lg_u32 s19, 0\n" \
"s_cselect_b32 s18, s17, s18\n" \
"v_mfma_f32_16x16x32_bf16 v[172:175], a[184:187], a[156:159], v[172:175]\n" \
"v_writelane_b32 v11, s17, 20\n" \
"s_or_b32 s17, s16, 0x10000\n" \
"v_mfma_f32_16x16x32_bf16 v[172:175], a[248:251], a[220:223], v[172:175]\n" \
"v_writelane_b32 v11, s18, 21\n" \
"s_add_i32 s18, s16, 0x19000\n" \
"v_mfma_f32_16x16x32_bf16 v[168:171], a[180:183], a[156:159], v[168:171]\n" \
"v_writelane_b32 v11, s17, 22\n" \
"s_mov_b32 s17, 0\n" \
"v_mfma_f32_16x16x32_bf16 v[168:171], a[244:247], a[220:223], v[168:171]\n" \
"s_cmpk_lt_u32 s3, 3\n" \
"s_cselect_b32 s18, s18, s17\n" \
"v_mfma_f32_16x16x32_bf16 v[164:167], a[176:179], a[156:159], v[164:167]\n" \
"s_bitset1_b32 s16, 17\n" \
"v_writelane_b32 v11, s18, 23\n" \
"v_mfma_f32_16x16x32_bf16 v[164:167], a[240:243], a[220:223], v[164:167]\n" \
"ds_read_b128 a[156:159], v28 offset:14336\n" \
"ds_read_b128 a[220:223], v29 offset:14336\n" \
"s_waitcnt lgkmcnt(15)\n" \
"v_mfma_f32_16x16x32_bf16 a[0:3], a[160:163], a[128:131], a[0:3]\n" \
"s_add_u32 m0, s30, 0x21000\n" \
"s_addk_i32 s84, 0x3000\n" \
"v_mfma_f32_16x16x32_bf16 a[0:3], a[224:227], a[192:195], a[0:3]\n" \
"buffer_load_dwordx4 v24, s[76:79], s84 offen offset:0 lds\n" \
"v_mfma_f32_16x16x32_bf16 a[4:7], a[164:167], a[128:131], a[4:7]\n" \
"ds_read_b64_tr_b16 a[176:177], v36 offset:32768\n" \
"s_movk_i32 s18, 0x2400\n" \
"v_mfma_f32_16x16x32_bf16 a[4:7], a[228:231], a[192:195], a[4:7]\n" \
"ds_read_b64_tr_b16 a[178:179], v36 offset:33792\n" \
"v_mfma_f32_16x16x32_bf16 a[12:15], a[172:175], a[128:131], a[12:15]\n" \
"ds_read_b64_tr_b16 a[240:241], v36 offset:40960\n" \
"s_waitcnt lgkmcnt(15)\n" \
"v_mfma_f32_16x16x32_bf16 a[12:15], a[236:239], a[192:195], a[12:15]\n" \
"s_add_u32 s88, s42, 0x1000\n" \
"buffer_store_dwordx4 v[220:223], v20, s[80:83], s88 offen offset:0 sc1\n" \
"s_waitcnt lgkmcnt(13)\n" \
"v_mfma_f32_16x16x32_bf16 a[8:11], a[128:131], a[168:171], a[8:11]\n" \
"ds_read_b64_tr_b16 a[242:243], v36 offset:41984\n" \
"s_waitcnt lgkmcnt(12)\n" \
"v_mfma_f32_16x16x32_bf16 a[8:11], a[192:195], a[232:235], a[8:11]\n" \
"ds_read_b64_tr_b16 a[180:181], v37 offset:32768\n" \
"v_mfma_f32_16x16x32_bf16 a[24:27], a[132:135], a[168:171], a[24:27]\n" \
"ds_read_b64_tr_b16 a[182:183], v37 offset:33792\n" \
"v_mfma_f32_16x16x32_bf16 a[24:27], a[196:199], a[232:235], a[24:27]\n" \
"ds_read_b64_tr_b16 a[244:245], v37 offset:40960\n" \
"v_mfma_f32_16x16x32_bf16 a[28:31], a[172:175], a[132:135], a[28:31]\n" \
"s_cmpk_lt_u32 s3, 3\n" \
"s_cselect_b32 s16, s16, s18\n" \
"v_mfma_f32_16x16x32_bf16 a[28:31], a[236:239], a[196:199], a[28:31]\n" \
"s_addk_i32 s84, 0x2000\n" \
"v_mfma_f32_16x16x32_bf16 a[20:23], a[164:167], a[132:135], a[20:23]\n" \
"buffer_load_dwordx4 v25, s[76:79], s84 offen offset:1024 lds\n" \
"v_mfma_f32_16x16x32_bf16 a[20:23], a[228:231], a[196:199], a[20:23]\n" \
"ds_read_b64_tr_b16 a[246:247], v37 offset:41984\n" \
"s_mul_i32 s19, s3, 0x9000\n" \
"v_mfma_f32_16x16x32_bf16 a[16:19], a[160:163], a[132:135], a[16:19]\n" \
"ds_read_b64_tr_b16 a[184:185], v38 offset:32768\n" \
"v_writelane_b32 v11, s16, 24\n" \
"v_mfma_f32_16x16x32_bf16 a[16:19], a[224:227], a[196:199], a[16:19]\n" \
"ds_read_b64_tr_b16 a[186:187], v38 offset:33792\n" \
"s_lshl_b32 s16, s3, 10\n" \
"v_mfma_f32_16x16x32_bf16 a[32:35], a[160:163], a[136:139], a[32:35]\n" \
"s_addk_i32 s84, 0x2000\n" \
"v_mfma_f32_16x16x32_bf16 a[32:35], a[224:227], a[200:203], a[32:35]\n" \
"buffer_load_dwordx4 v24, s[76:79], s84 offen offset:2048 lds\n" \
"v_mfma_f32_16x16x32_bf16 a[36:39], a[164:167], a[136:139], a[36:39]\n" \
"ds_read_b64_tr_b16 a[248:249], v38 offset:40960\n" \
"s_addk_i32 s16, 0x4800\n" \
"v_mfma_f32_16x16x32_bf16 a[36:39], a[228:231], a[200:203], a[36:39]\n" \
"ds_read_b64_tr_b16 a[250:251], v38 offset:41984\n" \
"v_writelane_b32 v11, s16, 25\n" \
"v_mfma_f32_16x16x32_bf16 a[44:47], a[172:175], a[136:139], a[44:47]\n" \
"ds_read_b64_tr_b16 a[188:189], v39 offset:32768\n" \
"s_add_i32 s16, s15, 1\n" \
"v_mfma_f32_16x16x32_bf16 a[44:47], a[236:239], a[200:203], a[44:47]\n" \
"ds_read_b64_tr_b16 a[190:191], v39 offset:33792\n" \
"s_and_b32 s16, s16, 3\n" \
"v_mfma_f32_16x16x32_bf16 a[40:43], a[136:139], a[168:171], a[40:43]\n" \
"s_add_u32 s89, s42, 0x0\n" \
"buffer_store_dwordx4 v[192:195], v20, s[80:83], s89 offen offset:2048 sc1\n" \
"v_mfma_f32_16x16x32_bf16 a[40:43], a[200:203], a[232:235], a[40:43]\n" \
"ds_read_b64_tr_b16 a[252:253], v39 offset:40960\n" \
"s_or_b32 s16, s16, s5\n" \
"v_mfma_f32_16x16x32_bf16 a[56:59], a[140:143], a[168:171], a[56:59]\n" \
"ds_read_b64_tr_b16 a[254:255], v39 offset:41984\n" \
"s_mul_i32 s18, s16, 0x24000\n" \
"v_mfma_f32_16x16x32_bf16 a[56:59], a[204:207], a[232:235], a[56:59]\n" \
"s_add_i32 s18, s18, s19\n" \
"s_xor_b32 s13, s13, 2\n" \
"v_mfma_f32_16x16x32_bf16 a[60:63], a[172:175], a[140:143], a[60:63]\n" \
"s_add_i32 s15, s15, -1\n" \
"v_writelane_b32 v11, s18, 27\n" \
"v_mfma_f32_16x16x32_bf16 a[60:63], a[236:239], a[204:207], a[60:63]\n" \
"s_addk_i32 s84, 0x2000\n" \
"v_mfma_f32_16x16x32_bf16 a[52:55], a[164:167], a[140:143], a[52:55]\n" \
"buffer_load_dwordx4 v25, s[76:79], s84 offen offset:3072 lds\n" \
"v_mfma_f32_16x16x32_bf16 a[52:55], a[228:231], a[204:207], a[52:55]\n" \
"s_mul_i32 s18, s13, 0x24000\n" \
"s_and_b32 s15, s15, 3\n" \
"v_mfma_f32_16x16x32_bf16 a[48:51], a[160:163], a[140:143], a[48:51]\n" \
"s_add_i32 s18, s18, s19\n" \
"s_or_b32 s15, s15, s5\n" \
"v_mfma_f32_16x16x32_bf16 a[48:51], a[224:227], a[204:207], a[48:51]\n" \
"v_writelane_b32 v11, s18, 28\n" \
"s_mul_i32 s18, s15, 0x24000\n" \
"s_waitcnt lgkmcnt(15)\n" \
"v_mfma_f32_16x16x32_bf16 a[64:67], a[164:167], v[248:251], a[64:67]\n" \
"s_add_i32 s18, s18, s19\n" \
"v_writelane_b32 v11, s18, 29\n" \
"v_mfma_f32_16x16x32_bf16 a[64:67], a[228:231], v[48:51], a[64:67]\n" \
"s_or_b32 s18, s9, s14\n" \
"s_mul_i32 s18, s18, 0x24800\n" \
"v_mfma_f32_16x16x32_bf16 a[80:83], a[172:175], v[248:251], a[80:83]\n" \
"s_lshl_b32 s16, s16, 12\n" \
"s_add_i32 s19, s18, 0x3004800\n" \
"v_mfma_f32_16x16x32_bf16 a[80:83], a[236:239], v[48:51], a[80:83]\n" \
"s_add_i32 s20, s19, s16\n" \
"v_writelane_b32 v11, s20, 30\n" \
"v_mfma_f32_16x16x32_bf16 a[68:71], v[248:251], a[168:171], a[68:71]\n" \
"s_lshl_b32 s13, s13, 12\n" \
"s_lshl_b32 s15, s15, 12\n" \
"s_waitcnt vmcnt(11) lgkmcnt(0)\n" \
"v_mfma_f32_16x16x32_bf16 a[68:71], v[48:51], a[232:235], a[68:71]\n" \
"s_barrier\n" \
"v_mfma_f32_16x16x32_bf16 v[52:55], a[176:179], a[128:131], v[52:55]\n" \
"s_add_u32 m0, s40, 0x8000\n" \
"s_add_u32 s85, s39, 0xa000\n" \
"v_mfma_f32_16x16x32_bf16 v[52:55], a[240:243], a[192:195], v[52:55]\n" \
"buffer_load_dwordx4 v20, s[80:83], s85 offen sc1 lds\n" \
"v_mfma_f32_16x16x32_bf16 v[56:59], a[180:183], a[128:131], v[56:59]\n" \
"ds_read_b64_tr_b16 a[160:161], v32 offset:0\n" \
"v_accvgpr_read_b32 v224, a120\n" \
"v_mfma_f32_16x16x32_bf16 v[56:59], a[244:247], a[192:195], v[56:59]\n" \
"ds_read_b64_tr_b16 a[162:163], v32 offset:512\n" \
"v_accvgpr_read_b32 v225, a121\n" \
"v_mfma_f32_16x16x32_bf16 v[60:63], a[184:187], a[128:131], v[60:63]\n" \
"s_add_u32 m0, s40, 0x9000\n" \
"s_add_u32 s85, s39, 0xb000\n" \
"v_mfma_f32_16x16x32_bf16 v[60:63], a[248:251], a[192:195], v[60:63]\n" \
"buffer_load_dwordx4 v20, s[80:83], s85 offen sc1 lds\n" \
"v_mfma_f32_16x16x32_bf16 v[64:67], a[188:191], a[128:131], v[64:67]\n" \
"ds_read_b64_tr_b16 a[224:225], v32 offset:4096\n" \
"v_accvgpr_read_b32 v226, a122\n" \
"v_mfma_f32_16x16x32_bf16 v[64:67], a[252:255], a[192:195], v[64:67]\n" \
"ds_read_b128 a[128:131], v28 offset:32768\n" \
"v_accvgpr_read_b32 v227, a123\n" \
"v_mfma_f32_16x16x32_bf16 v[80:83], a[188:191], a[132:135], v[80:83]\n" \
"ds_read_b128 a[192:195], v29 offset:32768\n" \
"v_accvgpr_read_b32 v180, a124\n" \
"v_mfma_f32_16x16x32_bf16 v[80:83], a[252:255], a[196:199], v[80:83]\n" \
"s_add_u32 s88, s42, 0x0\n" \
"buffer_store_dwordx4 v[196:199], v20, s[80:83], s88 offen offset:3072 sc1\n" \
"v_mfma_f32_16x16x32_bf16 v[76:79], a[184:187], a[132:135], v[76:79]\n" \
"ds_read_b64_tr_b16 a[226:227], v32 offset:4608\n" \
"v_accvgpr_read_b32 v181, a125\n" \
"v_mfma_f32_16x16x32_bf16 v[76:79], a[248:251], a[196:199], v[76:79]\n" \
"ds_read_b64_tr_b16 a[164:165], v33 offset:0\n" \
"v_accvgpr_read_b32 v182, a126\n" \
"v_mfma_f32_16x16x32_bf16 v[72:75], a[180:183], a[132:135], v[72:75]\n" \
"s_add_u32 m0, s40, 0xb000\n" \
"s_movk_i32 s84, 0x280\n" \
"v_mfma_f32_16x16x32_bf16 v[72:75], a[244:247], a[196:199], v[72:75]\n" \
"buffer_load_dwordx4 v21, s[76:79], s84 offen offset:0 lds\n" \
"v_mfma_f32_16x16x32_bf16 v[68:71], a[176:179], a[132:135], v[68:71]\n" \
"ds_read_b64_tr_b16 a[166:167], v33 offset:512\n" \
"v_accvgpr_read_b32 v183, a127\n" \
"v_mfma_f32_16x16x32_bf16 v[68:71], a[240:243], a[196:199], v[68:71]\n" \
"ds_read_b128 a[132:135], v28 offset:34816\n" \
"v_cvt_pk_bf16_f32 v224, v224, v225\n" \
"v_mfma_f32_16x16x32_bf16 v[84:87], a[176:179], a[136:139], v[84:87]\n" \
"ds_read_b128 a[196:199], v29 offset:34816\n" \
"v_cvt_pk_bf16_f32 v225, v226, v227\n" \
"v_mfma_f32_16x16x32_bf16 v[84:87], a[240:243], a[200:203], v[84:87]\n" \
"s_add_u32 m0, s31, 0x8000\n" \
"s_mov_b32 s84, 0x280000\n" \
"v_mfma_f32_16x16x32_bf16 v[88:91], a[180:183], a[136:139], v[88:91]\n" \
"buffer_load_dwordx4 v26, s[68:71], s84 offen offset:0 lds\n" \
"v_mfma_f32_16x16x32_bf16 v[88:91], a[244:247], a[200:203], v[88:91]\n" \
"ds_read_b64_tr_b16 a[228:229], v33 offset:4096\n" \
"v_cvt_pk_bf16_f32 v226, v180, v181\n" \
"v_mfma_f32_16x16x32_bf16 v[92:95], a[184:187], a[136:139], v[92:95]\n" \
"ds_read_b64_tr_b16 a[230:231], v33 offset:4608\n" \
"v_cvt_pk_bf16_f32 v227, v182, v183\n" \
"v_mfma_f32_16x16x32_bf16 v[92:95], a[248:251], a[200:203], v[92:95]\n" \
"s_addk_i32 s84, 0x7c00\n" \
"v_mfma_f32_16x16x32_bf16 v[96:99], a[188:191], a[136:139], v[96:99]\n" \
"buffer_load_dwordx4 v26, s[68:71], s84 offen offset:1024 lds\n" \
"v_mfma_f32_16x16x32_bf16 v[96:99], a[252:255], a[200:203], v[96:99]\n" \
"ds_read_b128 a[136:139], v28 offset:36864\n" \
"v_accvgpr_read_b32 v220, a112\n" \
"v_mfma_f32_16x16x32_bf16 v[112:115], a[188:191], a[140:143], v[112:115]\n" \
"ds_read_b128 a[200:203], v29 offset:36864\n" \
"v_accvgpr_read_b32 v221, a113\n" \
"v_mfma_f32_16x16x32_bf16 v[112:115], a[252:255], a[204:207], v[112:115]\n" \
"s_addk_i32 s84, 0x7c00\n" \
"v_mfma_f32_16x16x32_bf16 v[108:111], a[184:187], a[140:143], v[108:111]\n" \
"buffer_load_dwordx4 v27, s[68:71], s84 offen offset:2048 lds\n" \
"v_mfma_f32_16x16x32_bf16 v[108:111], a[248:251], a[204:207], v[108:111]\n" \
"ds_read_b64_tr_b16 a[172:173], v35 offset:0\n" \
"v_accvgpr_read_b32 v222, a114\n" \
"v_mfma_f32_16x16x32_bf16 v[104:107], a[180:183], a[140:143], v[104:107]\n" \
"ds_read_b64_tr_b16 a[174:175], v35 offset:512\n" \
"v_accvgpr_read_b32 v223, a115\n" \
"v_mfma_f32_16x16x32_bf16 v[104:107], a[244:247], a[204:207], v[104:107]\n" \
"ds_read_b64_tr_b16 a[236:237], v35 offset:4096\n" \
"v_accvgpr_read_b32 v180, a116\n" \
"v_mfma_f32_16x16x32_bf16 v[100:103], a[176:179], a[140:143], v[100:103]\n" \
"v_accvgpr_read_b32 v181, a117\n" \
"v_accvgpr_read_b32 v182, a118\n" \
"v_mfma_f32_16x16x32_bf16 v[100:103], a[240:243], a[204:207], v[100:103]\n" \
"ds_read_b128 a[140:143], v28 offset:38912\n" \
"v_accvgpr_read_b32 v183, a119\n" \
"v_mfma_f32_16x16x32_bf16 v[116:119], a[176:179], a[144:147], v[116:119]\n" \
"ds_read_b128 a[204:207], v29 offset:38912\n" \
"v_cvt_pk_bf16_f32 v220, v220, v221\n" \
"v_mfma_f32_16x16x32_bf16 v[116:119], a[240:243], a[208:211], v[116:119]\n" \
"s_addk_i32 s84, 0x7c00\n" \
"v_mfma_f32_16x16x32_bf16 v[120:123], a[180:183], a[144:147], v[120:123]\n" \
"buffer_load_dwordx4 v27, s[68:71], s84 offen offset:3072 lds\n" \
"v_mfma_f32_16x16x32_bf16 v[120:123], a[244:247], a[208:211], v[120:123]\n" \
"s_add_u32 m0, s30, 0x0\n" \
"s_movk_i32 s84, 0x300\n" \
"s_nop 0\n" \
"v_mfma_f32_16x16x32_bf16 v[124:127], a[184:187], a[144:147], v[124:127]\n" \
"buffer_load_dwordx4 v24, s[76:79], s84 offen offset:0 lds\n" \
"v_mfma_f32_16x16x32_bf16 v[124:127], a[248:251], a[208:211], v[124:127]\n" \
"ds_read_b64_tr_b16 a[238:239], v35 offset:4608\n" \
"v_cvt_pk_bf16_f32 v221, v222, v223\n" \
"v_mfma_f32_16x16x32_bf16 v[128:131], a[188:191], a[144:147], v[128:131]\n" \
"s_add_u32 s89, s42, 0x1000\n" \
"buffer_store_dwordx4 a[84:87], v20, s[80:83], s89 offen offset:3072 sc1\n" \
"v_mfma_f32_16x16x32_bf16 v[128:131], a[252:255], a[208:211], v[128:131]\n" \
"ds_read_b128 a[144:147], v28 offset:40960\n" \
"v_cvt_pk_bf16_f32 v222, v180, v181\n" \
"v_mfma_f32_16x16x32_bf16 v[144:147], a[188:191], a[148:151], v[144:147]\n" \
"ds_read_b128 a[208:211], v29 offset:40960\n" \
"v_cvt_pk_bf16_f32 v223, v182, v183\n" \
"v_mfma_f32_16x16x32_bf16 v[144:147], a[252:255], a[212:215], v[144:147]\n" \
"ds_read_b64_tr_b16 a[168:169], v34 offset:0\n" \
"v_accvgpr_read_b32 v216, a104\n" \
"v_mfma_f32_16x16x32_bf16 v[140:143], a[184:187], a[148:151], v[140:143]\n" \
"ds_read_b64_tr_b16 a[170:171], v34 offset:512\n" \
"v_accvgpr_read_b32 v217, a105\n" \
"v_mfma_f32_16x16x32_bf16 v[140:143], a[248:251], a[212:215], v[140:143]\n" \
"s_movk_i32 s84, 0x2300\n" \
"s_nop 0\n" \
"v_mfma_f32_16x16x32_bf16 v[136:139], a[180:183], a[148:151], v[136:139]\n" \
"buffer_load_dwordx4 v25, s[76:79], s84 offen offset:1024 lds\n" \
"v_mfma_f32_16x16x32_bf16 v[136:139], a[244:247], a[212:215], v[136:139]\n" \
"ds_read_b64_tr_b16 a[232:233], v34 offset:4096\n" \
"v_accvgpr_read_b32 v218, a106\n" \
"v_mfma_f32_16x16x32_bf16 v[132:135], a[176:179], a[148:151], v[132:135]\n" \
"ds_read_b64_tr_b16 a[234:235], v34 offset:4608\n" \
"v_accvgpr_read_b32 v219, a107\n" \
"v_mfma_f32_16x16x32_bf16 v[132:135], a[240:243], a[212:215], v[132:135]\n" \
"ds_read_b128 a[148:151], v28 offset:43008\n" \
"v_accvgpr_read_b32 v180, a108\n" \
"v_mfma_f32_16x16x32_bf16 v[148:151], a[176:179], a[152:155], v[148:151]\n" \
"ds_read_b128 a[212:215], v29 offset:43008\n" \
"v_accvgpr_read_b32 v181, a109\n" \
"v_mfma_f32_16x16x32_bf16 v[148:151], a[240:243], a[216:219], v[148:151]\n" \
"s_movk_i32 s84, 0x4300\n" \
"v_mfma_f32_16x16x32_bf16 v[152:155], a[180:183], a[152:155], v[152:155]\n" \
"buffer_load_dwordx4 v24, s[76:79], s84 offen offset:2048 lds\n" \
"v_mfma_f32_16x16x32_bf16 v[152:155], a[244:247], a[216:219], v[152:155]\n" \
"s_movk_i32 s84, 0x6300\n" \
"v_mfma_f32_16x16x32_bf16 v[156:159], a[184:187], a[152:155], v[156:159]\n" \
"buffer_load_dwordx4 v25, s[76:79], s84 offen offset:3072 lds\n" \
"v_mfma_f32_16x16x32_bf16 v[156:159], a[248:251], a[216:219], v[156:159]\n" \
"ds_read_b128 v[248:251], v22 offset:0\n" \
"v_accvgpr_read_b32 v182, a110\n" \
"v_mfma_f32_16x16x32_bf16 v[160:163], a[188:191], a[152:155], v[160:163]\n" \
"ds_read_b128 v[48:51], v23 offset:0\n" \
"v_accvgpr_read_b32 v183, a111\n" \
"v_mfma_f32_16x16x32_bf16 v[160:163], a[252:255], a[216:219], v[160:163]\n" \
"ds_read_b128 a[152:155], v28 offset:45056\n" \
"v_cvt_pk_bf16_f32 v216, v216, v217\n" \
"v_mfma_f32_16x16x32_bf16 v[176:179], a[188:191], a[156:159], v[176:179]\n" \
"ds_read_b128 a[216:219], v29 offset:45056\n" \
"v_cvt_pk_bf16_f32 v217, v218, v219\n" \
"v_mfma_f32_16x16x32_bf16 v[176:179], a[252:255], a[220:223], v[176:179]\n" \
"v_cvt_pk_bf16_f32 v218, v180, v181\n" \
"v_cvt_pk_bf16_f32 v219, v182, v183\n" \
"v_mfma_f32_16x16x32_bf16 v[172:175], a[184:187], a[156:159], v[172:175]\n" \
"v_accvgpr_read_b32 v192, a96\n" \
"v_accvgpr_read_b32 v193, a97\n" \
"v_mfma_f32_16x16x32_bf16 v[172:175], a[248:251], a[220:223], v[172:175]\n" \
"v_accvgpr_read_b32 v194, a98\n" \
"v_accvgpr_read_b32 v195, a99\n" \
"v_mfma_f32_16x16x32_bf16 v[168:171], a[180:183], a[156:159], v[168:171]\n" \
"v_accvgpr_read_b32 v180, a100\n" \
"v_accvgpr_read_b32 v181, a101\n" \
"v_mfma_f32_16x16x32_bf16 v[168:171], a[244:247], a[220:223], v[168:171]\n" \
"v_accvgpr_read_b32 v182, a102\n" \
"v_accvgpr_read_b32 v183, a103\n" \
"v_mfma_f32_16x16x32_bf16 v[164:167], a[176:179], a[156:159], v[164:167]\n" \
"v_cvt_pk_bf16_f32 v192, v192, v193\n" \
"v_cvt_pk_bf16_f32 v193, v194, v195\n" \
"v_mfma_f32_16x16x32_bf16 v[164:167], a[240:243], a[220:223], v[164:167]\n" \
"ds_read_b128 a[156:159], v28 offset:47104\n" \
"ds_read_b128 a[220:223], v29 offset:47104\n" \
"s_waitcnt lgkmcnt(15)\n" \
"v_mfma_f32_16x16x32_bf16 a[0:3], a[160:163], a[128:131], a[0:3]\n" \
"s_add_u32 m0, s30, 0x1000\n" \
"s_addk_i32 s84, 0x3000\n" \
"v_mfma_f32_16x16x32_bf16 a[0:3], a[224:227], a[192:195], a[0:3]\n" \
"buffer_load_dwordx4 v24, s[76:79], s84 offen offset:0 lds\n" \
"v_mfma_f32_16x16x32_bf16 a[4:7], a[164:167], a[128:131], a[4:7]\n" \
"ds_read_b64_tr_b16 a[176:177], v36 offset:0\n" \
"v_cvt_pk_bf16_f32 v194, v180, v181\n" \
"v_mfma_f32_16x16x32_bf16 a[4:7], a[228:231], a[192:195], a[4:7]\n" \
"ds_read_b64_tr_b16 a[178:179], v36 offset:1024\n" \
"v_cvt_pk_bf16_f32 v195, v182, v183\n" \
"v_mfma_f32_16x16x32_bf16 a[12:15], a[172:175], a[128:131], a[12:15]\n" \
"ds_read_b64_tr_b16 a[240:241], v36 offset:8192\n" \
"v_accvgpr_read_b32 v188, a88\n" \
"s_waitcnt lgkmcnt(15)\n" \
"v_mfma_f32_16x16x32_bf16 a[12:15], a[236:239], a[192:195], a[12:15]\n" \
"s_add_u32 s88, s42, 0x1000\n" \
"buffer_store_dwordx4 v[252:255], v20, s[80:83], s88 offen offset:2048 sc1\n" \
"s_waitcnt lgkmcnt(13)\n" \
"v_mfma_f32_16x16x32_bf16 a[8:11], a[128:131], a[168:171], a[8:11]\n" \
"ds_read_b64_tr_b16 a[242:243], v36 offset:9216\n" \
"v_accvgpr_read_b32 v189, a89\n" \
"s_waitcnt lgkmcnt(12)\n" \
"s_nop 0\n" \
"v_mfma_f32_16x16x32_bf16 a[8:11], a[192:195], a[232:235], a[8:11]\n" \
"ds_read_b64_tr_b16 a[180:181], v37 offset:0\n" \
"v_accvgpr_read_b32 v190, a90\n" \
"v_mfma_f32_16x16x32_bf16 a[24:27], a[132:135], a[168:171], a[24:27]\n" \
"ds_read_b64_tr_b16 a[182:183], v37 offset:1024\n" \
"v_accvgpr_read_b32 v191, a91\n" \
"v_mfma_f32_16x16x32_bf16 a[24:27], a[196:199], a[232:235], a[24:27]\n" \
"ds_read_b64_tr_b16 a[244:245], v37 offset:8192\n" \
"v_accvgpr_read_b32 v180, a92\n" \
"v_mfma_f32_16x16x32_bf16 a[28:31], a[172:175], a[132:135], a[28:31]\n" \
"v_accvgpr_read_b32 v181, a93\n" \
"v_accvgpr_read_b32 v182, a94\n" \
"v_mfma_f32_16x16x32_bf16 a[28:31], a[236:239], a[196:199], a[28:31]\n" \
"s_addk_i32 s84, 0x2000\n" \
"s_nop 0\n" \
"v_mfma_f32_16x16x32_bf16 a[20:23], a[164:167], a[132:135], a[20:23]\n" \
"buffer_load_dwordx4 v25, s[76:79], s84 offen offset:1024 lds\n" \
"v_mfma_f32_16x16x32_bf16 a[20:23], a[228:231], a[196:199], a[20:23]\n" \
"ds_read_b64_tr_b16 a[246:247], v37 offset:9216\n" \
"v_accvgpr_read_b32 v183, a95\n" \
"v_mfma_f32_16x16x32_bf16 a[16:19], a[160:163], a[132:135], a[16:19]\n" \
"ds_read_b64_tr_b16 a[184:185], v38 offset:0\n" \
"v_cvt_pk_bf16_f32 v188, v188, v189\n" \
"v_mfma_f32_16x16x32_bf16 a[16:19], a[224:227], a[196:199], a[16:19]\n" \
"ds_read_b64_tr_b16 a[186:187], v38 offset:1024\n" \
"v_cvt_pk_bf16_f32 v189, v190, v191\n" \
"v_mfma_f32_16x16x32_bf16 a[32:35], a[160:163], a[136:139], a[32:35]\n" \
"s_addk_i32 s84, 0x2000\n" \
"s_nop 0\n" \
"v_mfma_f32_16x16x32_bf16 a[32:35], a[224:227], a[200:203], a[32:35]\n" \
"buffer_load_dwordx4 v24, s[76:79], s84 offen offset:2048 lds\n" \
"v_mfma_f32_16x16x32_bf16 a[36:39], a[164:167], a[136:139], a[36:39]\n" \
"ds_read_b64_tr_b16 a[248:249], v38 offset:8192\n" \
"v_cvt_pk_bf16_f32 v190, v180, v181\n" \
"v_mfma_f32_16x16x32_bf16 a[36:39], a[228:231], a[200:203], a[36:39]\n" \
"ds_read_b64_tr_b16 a[250:251], v38 offset:9216\n" \
"v_cvt_pk_bf16_f32 v191, v182, v183\n" \
"v_mfma_f32_16x16x32_bf16 a[44:47], a[172:175], a[136:139], a[44:47]\n" \
"ds_read_b64_tr_b16 a[188:189], v39 offset:0\n" \
"v_accvgpr_read_b32 v184, a72\n" \
"v_mfma_f32_16x16x32_bf16 a[44:47], a[236:239], a[200:203], a[44:47]\n" \
"ds_read_b64_tr_b16 a[190:191], v39 offset:1024\n" \
"v_accvgpr_read_b32 v185, a73\n" \
"v_mfma_f32_16x16x32_bf16 a[40:43], a[136:139], a[168:171], a[40:43]\n" \
"s_add_u32 s89, s42, 0x2000\n" \
"buffer_store_dwordx4 v[200:203], v20, s[80:83], s89 offen offset:2048 sc1\n" \
"v_mfma_f32_16x16x32_bf16 a[40:43], a[200:203], a[232:235], a[40:43]\n" \
"ds_read_b64_tr_b16 a[252:253], v39 offset:8192\n" \
".fill (L_pb + 28384 - .) / 4, 4, 0xBF800000\n" \
"L_pp7:\n" \
"s_cbranch_execz L_pret\n" \
"s_nop 0\n" \
"v_accvgpr_read_b32 v186, a74\n" \
"v_mfma_f32_16x16x32_bf16 a[56:59], a[140:143], a[168:171], a[56:59]\n" \
"ds_read_b64_tr_b16 a[254:255], v39 offset:9216\n" \
"v_accvgpr_read_b32 v187, a75\n" \
"v_mfma_f32_16x16x32_bf16 a[56:59], a[204:207], a[232:235], a[56:59]\n" \
"v_accvgpr_read_b32 v180, a76\n" \
"v_accvgpr_read_b32 v181, a77\n" \
"v_mfma_f32_16x16x32_bf16 a[60:63], a[172:175], a[140:143], a[60:63]\n" \
"v_accvgpr_read_b32 v182, a78\n" \
"v_accvgpr_read_b32 v183, a79\n" \
"v_mfma_f32_16x16x32_bf16 a[60:63], a[236:239], a[204:207], a[60:63]\n" \
"s_addk_i32 s84, 0x2000\n" \
"v_mfma_f32_16x16x32_bf16 a[52:55], a[164:167], a[140:143], a[52:55]\n" \
"buffer_load_dwordx4 v25, s[76:79], s84 offen offset:3072 lds\n" \
"v_mfma_f32_16x16x32_bf16 a[52:55], a[228:231], a[204:207], a[52:55]\n" \
"v_cvt_pk_bf16_f32 v184, v184, v185\n" \
"v_cvt_pk_bf16_f32 v185, v186, v187\n" \
"v_mfma_f32_16x16x32_bf16 a[48:51], a[160:163], a[140:143], a[48:51]\n" \
"v_cvt_pk_bf16_f32 v186, v180, v181\n" \
"v_cvt_pk_bf16_f32 v187, v182, v183\n" \
"v_mfma_f32_16x16x32_bf16 a[48:51], a[224:227], a[204:207], a[48:51]\n" \
"s_sub_i32 s14, s9, s14\n" \
"s_add_i32 s20, s19, s13\n" \
"s_waitcnt lgkmcnt(15)\n" \
"v_mfma_f32_16x16x32_bf16 a[64:67], a[164:167], v[248:251], a[64:67]\n" \
"v_writelane_b32 v11, s20, 31\n" \
"s_add_i32 s19, s19, s15\n" \
"v_mfma_f32_16x16x32_bf16 a[64:67], a[228:231], v[48:51], a[64:67]\n" \
"s_mul_i32 s14, s14, 0x24800\n" \
"v_writelane_b32 v11, s19, 32\n" \
"v_mfma_f32_16x16x32_bf16 a[80:83], a[172:175], v[248:251], a[80:83]\n" \
"s_add_i32 s19, s14, 0x4120800\n" \
"s_add_i32 s16, s19, s16\n" \
"v_mfma_f32_16x16x32_bf16 a[80:83], a[236:239], v[48:51], a[80:83]\n" \
"v_writelane_b32 v11, s16, 33\n" \
"s_add_i32 s13, s19, s13\n" \
"v_mfma_f32_16x16x32_bf16 a[68:71], v[248:251], a[168:171], a[68:71]\n" \
"v_writelane_b32 v11, s13, 34\n" \
"s_add_i32 s19, s19, s15\n" \
"s_waitcnt vmcnt(11) lgkmcnt(0)\n" \
"v_mfma_f32_16x16x32_bf16 a[68:71], v[48:51], a[232:235], a[68:71]\n" \
"s_barrier\n" \
"v_mov_b32_e32 v45, s62\n" \
"s_mov_b64 exec, 1\n" \
"buffer_store_dword v45, off, s[80:83], s37 offset:0 sc1\n" \
"s_mov_b64 exec, -1\n" \
"v_xor_b32_e32 v28, 0x20000, v28\n" \
"v_xor_b32_e32 v29, 0x20000, v29\n" \
"v_mfma_f32_16x16x32_bf16 v[52:55], a[176:179], a[128:131], v[52:55]\n" \
"s_add_u32 m0, s40, 0x0\n" \
"s_add_u32 s85, s39, 0xc000\n" \
"v_mfma_f32_16x16x32_bf16 v[52:55], a[240:243], a[192:195], v[52:55]\n" \
"buffer_load_dwordx4 v20, s[80:83], s85 offen sc1 lds\n" \
"v_mfma_f32_16x16x32_bf16 v[56:59], a[180:183], a[128:131], v[56:59]\n" \
"ds_read_b64_tr_b16 a[160:161], v32 offset:32768\n" \
"s_or_b32 s13, s18, 0x3000000\n" \
"v_mfma_f32_16x16x32_bf16 v[56:59], a[244:247], a[192:195], v[56:59]\n" \
"ds_read_b64_tr_b16 a[162:163], v32 offset:33280\n" \
"s_lshl_b32 s15, s6, 11\n" \
"v_mfma_f32_16x16x32_bf16 v[60:63], a[184:187], a[128:131], v[60:63]\n" \
"s_add_u32 m0, s40, 0x1000\n" \
"s_add_u32 s85, s39, 0xd000\n" \
"v_mfma_f32_16x16x32_bf16 v[60:63], a[248:251], a[192:195], v[60:63]\n" \
"buffer_load_dwordx4 v20, s[80:83], s85 offen sc1 lds\n" \
"v_mfma_f32_16x16x32_bf16 v[64:67], a[188:191], a[128:131], v[64:67]\n" \
"ds_read_b64_tr_b16 a[224:225], v32 offset:36864\n" \
"v_writelane_b32 v11, s19, 35\n" \
"v_mfma_f32_16x16x32_bf16 v[64:67], a[252:255], a[192:195], v[64:67]\n" \
"ds_read_b128 a[128:131], v28 offset:0\n" \
"s_add_i32 s16, s13, s15\n" \
"v_mfma_f32_16x16x32_bf16 v[80:83], a[188:191], a[132:135], v[80:83]\n" \
"ds_read_b128 a[192:195], v29 offset:0\n" \
"v_writelane_b32 v11, s16, 36\n" \
"v_mfma_f32_16x16x32_bf16 v[80:83], a[252:255], a[196:199], v[80:83]\n" \
"s_add_u32 m0, s40, 0x3000\n" \
"s_movk_i32 s84, 0x300\n" \
"v_mfma_f32_16x16x32_bf16 v[76:79], a[184:187], a[132:135], v[76:79]\n" \
"buffer_load_dwordx4 v21, s[76:79], s84 offen offset:0 lds\n" \
"v_mfma_f32_16x16x32_bf16 v[76:79], a[248:251], a[196:199], v[76:79]\n" \
"ds_read_b64_tr_b16 a[226:227], v32 offset:37376\n" \
"s_add_i32 s16, s14, 0x411c000\n" \
"v_mfma_f32_16x16x32_bf16 v[72:75], a[180:183], a[132:135], v[72:75]\n" \
"ds_read_b64_tr_b16 a[164:165], v33 offset:32768\n" \
"s_add_i32 s19, s16, s15\n" \
"v_mfma_f32_16x16x32_bf16 v[72:75], a[244:247], a[196:199], v[72:75]\n" \
"v_writelane_b32 v11, s19, 37\n" \
"s_add_i32 s18, s18, 0x3004000\n" \
"v_mfma_f32_16x16x32_bf16 v[68:71], a[176:179], a[132:135], v[68:71]\n" \
"ds_read_b64_tr_b16 a[166:167], v33 offset:33280\n" \
"v_writelane_b32 v11, s18, 38\n" \
"v_mfma_f32_16x16x32_bf16 v[68:71], a[240:243], a[196:199], v[68:71]\n" \
"ds_read_b128 a[132:135], v28 offset:2048\n" \
"s_add_i32 s14, s14, 0x4120000\n" \
"v_mfma_f32_16x16x32_bf16 v[84:87], a[176:179], a[136:139], v[84:87]\n" \
"ds_read_b128 a[196:199], v29 offset:2048\n" \
"v_writelane_b32 v11, s14, 39\n" \
"v_mfma_f32_16x16x32_bf16 v[84:87], a[240:243], a[200:203], v[84:87]\n" \
"s_add_u32 m0, s31, 0x0\n" \
"s_mov_b32 s84, 0x300000\n" \
"v_mfma_f32_16x16x32_bf16 v[88:91], a[180:183], a[136:139], v[88:91]\n" \
"buffer_load_dwordx4 v26, s[68:71], s84 offen offset:0 lds\n" \
"v_mfma_f32_16x16x32_bf16 v[88:91], a[244:247], a[200:203], v[88:91]\n" \
"s_add_u32 s88, s42, 0x2000\n" \
"buffer_store_dwordx4 v[204:207], v20, s[80:83], s88 offen offset:3072 sc1\n" \
"v_mfma_f32_16x16x32_bf16 v[92:95], a[184:187], a[136:139], v[92:95]\n" \
"ds_read_b64_tr_b16 a[228:229], v33 offset:36864\n" \
"s_lshl_b32 s14, s4, 2\n" \
"v_mfma_f32_16x16x32_bf16 v[92:95], a[248:251], a[200:203], v[92:95]\n" \
"ds_read_b64_tr_b16 a[230:231], v33 offset:37376\n" \
"s_or_b32 s8, s14, s8\n" \
"v_mfma_f32_16x16x32_bf16 v[96:99], a[188:191], a[136:139], v[96:99]\n" \
"s_mul_i32 s8, s8, 0x90000\n" \
"s_lshl_b32 s14, s3, 14\n" \
"v_mfma_f32_16x16x32_bf16 v[96:99], a[252:255], a[200:203], v[96:99]\n" \
"ds_read_b128 a[136:139], v28 offset:4096\n" \
"s_add_i32 s8, s8, s14\n" \
"v_mfma_f32_16x16x32_bf16 v[112:115], a[188:191], a[140:143], v[112:115]\n" \
"ds_read_b128 a[200:203], v29 offset:4096\n" \
"s_lshl_b32 s9, s9, 4\n" \
"v_mfma_f32_16x16x32_bf16 v[112:115], a[252:255], a[204:207], v[112:115]\n" \
"s_addk_i32 s84, 0x7c00\n" \
"v_mfma_f32_16x16x32_bf16 v[108:111], a[184:187], a[140:143], v[108:111]\n" \
"buffer_load_dwordx4 v26, s[68:71], s84 offen offset:1024 lds\n" \
"v_mfma_f32_16x16x32_bf16 v[108:111], a[248:251], a[204:207], v[108:111]\n" \
"ds_read_b64_tr_b16 a[172:173], v35 offset:32768\n" \
"s_lshl_b32 s14, s4, 7\n" \
"v_mfma_f32_16x16x32_bf16 v[104:107], a[180:183], a[140:143], v[104:107]\n" \
"s_addk_i32 s84, 0x7c00\n" \
"v_mfma_f32_16x16x32_bf16 v[104:107], a[244:247], a[204:207], v[104:107]\n" \
"buffer_load_dwordx4 v27, s[68:71], s84 offen offset:2048 lds\n" \
"v_mfma_f32_16x16x32_bf16 v[100:103], a[176:179], a[140:143], v[100:103]\n" \
"ds_read_b64_tr_b16 a[174:175], v35 offset:33280\n" \
"s_or_b32 s18, s9, s14\n" \
"v_mfma_f32_16x16x32_bf16 v[100:103], a[240:243], a[204:207], v[100:103]\n" \
"ds_read_b128 a[140:143], v28 offset:6144\n" \
"s_sub_i32 s9, s9, s14\n" \
"v_mfma_f32_16x16x32_bf16 v[116:119], a[176:179], a[144:147], v[116:119]\n" \
"ds_read_b128 a[204:207], v29 offset:6144\n" \
"s_mulk_i32 s18, 0x480\n" \
"v_mfma_f32_16x16x32_bf16 v[116:119], a[240:243], a[208:211], v[116:119]\n" \
"s_addk_i32 s84, 0x7c00\n" \
"v_mfma_f32_16x16x32_bf16 v[120:123], a[180:183], a[144:147], v[120:123]\n" \
"buffer_load_dwordx4 v27, s[68:71], s84 offen offset:3072 lds\n" \
"v_mfma_f32_16x16x32_bf16 v[120:123], a[244:247], a[208:211], v[120:123]\n" \
"ds_read_b64_tr_b16 a[236:237], v35 offset:36864\n" \
"s_mulk_i32 s9, 0x480\n" \
"v_mfma_f32_16x16x32_bf16 v[124:127], a[184:187], a[144:147], v[124:127]\n" \
"ds_read_b64_tr_b16 a[238:239], v35 offset:37376\n" \
"s_or_b32 s14, s18, s12\n" \
"v_mfma_f32_16x16x32_bf16 v[124:127], a[248:251], a[208:211], v[124:127]\n" \
"s_add_u32 m0, s30, 0x8000\n" \
"s_movk_i32 s84, 0x380\n" \
"v_mfma_f32_16x16x32_bf16 v[128:131], a[188:191], a[144:147], v[128:131]\n" \
"buffer_load_dwordx4 v24, s[76:79], s84 offen offset:0 lds\n" \
"v_mfma_f32_16x16x32_bf16 v[128:131], a[252:255], a[208:211], v[128:131]\n" \
"ds_read_b128 a[144:147], v28 offset:8192\n" \
"s_or_b32 s9, s9, s12\n" \
"v_mfma_f32_16x16x32_bf16 v[144:147], a[188:191], a[148:151], v[144:147]\n" \
"ds_read_b128 a[208:211], v29 offset:8192\n" \
"s_and_b32 s12, s2, 1\n" \
"v_mfma_f32_16x16x32_bf16 v[144:147], a[252:255], a[212:215], v[144:147]\n" \
"ds_read_b64_tr_b16 a[168:169], v34 offset:32768\n" \
"v_mfma_f32_16x16x32_bf16 v[140:143], a[184:187], a[148:151], v[140:143]\n" \
"s_movk_i32 s84, 0x2380\n" \
"s_nop 0\n" \
"v_mfma_f32_16x16x32_bf16 v[140:143], a[248:251], a[212:215], v[140:143]\n" \
"buffer_load_dwordx4 v25, s[76:79], s84 offen offset:1024 lds\n" \
"v_mfma_f32_16x16x32_bf16 v[136:139], a[180:183], a[148:151], v[136:139]\n" \
"ds_read_b64_tr_b16 a[170:171], v34 offset:33280\n" \
"v_mfma_f32_16x16x32_bf16 v[136:139], a[244:247], a[212:215], v[136:139]\n" \
"ds_read_b64_tr_b16 a[232:233], v34 offset:36864\n" \
"v_mfma_f32_16x16x32_bf16 v[132:135], a[176:179], a[148:151], v[132:135]\n" \
"ds_read_b64_tr_b16 a[234:235], v34 offset:37376\n" \
"v_mfma_f32_16x16x32_bf16 v[132:135], a[240:243], a[212:215], v[132:135]\n" \
"ds_read_b128 a[148:151], v28 offset:10240\n" \
"v_mfma_f32_16x16x32_bf16 v[148:151], a[176:179], a[152:155], v[148:151]\n" \
"ds_read_b128 a[212:215], v29 offset:10240\n" \
"v_mfma_f32_16x16x32_bf16 v[148:151], a[240:243], a[216:219], v[148:151]\n" \
"s_movk_i32 s84, 0x4380\n" \
"v_mfma_f32_16x16x32_bf16 v[152:155], a[180:183], a[152:155], v[152:155]\n" \
"buffer_load_dwordx4 v24, s[76:79], s84 offen offset:2048 lds\n" \
"v_mfma_f32_16x16x32_bf16 v[152:155], a[244:247], a[216:219], v[152:155]\n" \
"s_movk_i32 s84, 0x6380\n" \
"v_mfma_f32_16x16x32_bf16 v[156:159], a[184:187], a[152:155], v[156:159]\n" \
"buffer_load_dwordx4 v25, s[76:79], s84 offen offset:3072 lds\n" \
"v_mfma_f32_16x16x32_bf16 v[156:159], a[248:251], a[216:219], v[156:159]\n" \
"ds_read_b128 v[248:251], v22 offset:32768\n" \
"v_mfma_f32_16x16x32_bf16 v[160:163], a[188:191], a[152:155], v[160:163]\n" \
"ds_read_b128 v[48:51], v23 offset:32768\n" \
"v_mfma_f32_16x16x32_bf16 v[160:163], a[252:255], a[216:219], v[160:163]\n" \
"ds_read_b128 a[152:155], v28 offset:12288\n" \
"v_mfma_f32_16x16x32_bf16 v[176:179], a[188:191], a[156:159], v[176:179]\n" \
"ds_read_b128 a[216:219], v29 offset:12288\n" \
"v_mfma_f32_16x16x32_bf16 v[176:179], a[252:255], a[220:223], v[176:179]\n" \
"s_cmp_lg_u32 s12, 0\n" \
"s_cselect_b32 s13, s16, s13\n" \
"v_mfma_f32_16x16x32_bf16 v[172:175], a[184:187], a[156:159], v[172:175]\n" \
"s_bfe_u32 s16, s2, 0x20001\n" \
"s_addk_i32 s13, 0x4000\n" \
"v_mfma_f32_16x16x32_bf16 v[172:175], a[248:251], a[220:223], v[172:175]\n" \
"s_xor_b32 s16, s16, s3\n" \
"v_mfma_f32_16x16x32_bf16 v[168:171], a[180:183], a[156:159], v[168:171]\n" \
"s_cmp_lg_u32 s16, 0\n" \
"s_cselect_b32 s13, s7, s13\n" \
"v_mfma_f32_16x16x32_bf16 v[168:171], a[244:247], a[220:223], v[168:171]\n" \
"s_lshl_b32 s6, s6, 16\n" \
"v_writelane_b32 v11, s13, 40\n" \
"v_mfma_f32_16x16x32_bf16 v[164:167], a[176:179], a[156:159], v[164:167]\n" \
"s_add_i32 s13, s8, 0x4240000\n" \
"s_add_i32 s6, s13, s6\n" \
"v_mfma_f32_16x16x32_bf16 v[164:167], a[240:243], a[220:223], v[164:167]\n" \
"ds_read_b128 a[156:159], v28 offset:14336\n" \
"ds_read_b128 a[220:223], v29 offset:14336\n" \
"s_waitcnt lgkmcnt(15)\n" \
"v_mfma_f32_16x16x32_bf16 a[0:3], a[160:163], a[128:131], a[0:3]\n" \
"s_add_u32 m0, s30, 0x9000\n" \
"s_addk_i32 s84, 0x3000\n" \
"v_mfma_f32_16x16x32_bf16 a[0:3], a[224:227], a[192:195], a[0:3]\n" \
"buffer_load_dwordx4 v24, s[76:79], s84 offen offset:0 lds\n" \
"v_mfma_f32_16x16x32_bf16 a[4:7], a[164:167], a[128:131], a[4:7]\n" \
"ds_read_b64_tr_b16 a[176:177], v36 offset:32768\n" \
"v_writelane_b32 v11, s6, 41\n" \
"v_mfma_f32_16x16x32_bf16 a[4:7], a[228:231], a[192:195], a[4:7]\n" \
"ds_read_b64_tr_b16 a[178:179], v36 offset:33792\n" \
"s_add_i32 s8, s8, 0x42c0000\n" \
"v_mfma_f32_16x16x32_bf16 a[12:15], a[172:175], a[128:131], a[12:15]\n" \
"ds_read_b64_tr_b16 a[240:241], v36 offset:40960\n" \
"s_lshl_b32 s6, s12, 10\n" \
"s_waitcnt lgkmcnt(15)\n" \
"v_mfma_f32_16x16x32_bf16 a[12:15], a[236:239], a[192:195], a[12:15]\n" \
"s_add_u32 s89, s42, 0x3000\n" \
"buffer_store_dwordx4 v[208:211], v20, s[80:83], s89 offen offset:3072 sc1\n" \
"s_waitcnt lgkmcnt(13)\n" \
"v_mfma_f32_16x16x32_bf16 a[8:11], a[128:131], a[168:171], a[8:11]\n" \
"ds_read_b64_tr_b16 a[242:243], v36 offset:41984\n" \
"s_or_b32 s6, s8, s6\n" \
"s_waitcnt lgkmcnt(12)\n" \
"v_mfma_f32_16x16x32_bf16 a[8:11], a[192:195], a[232:235], a[8:11]\n" \
"ds_read_b64_tr_b16 a[180:181], v37 offset:32768\n" \
"v_mfma_f32_16x16x32_bf16 a[24:27], a[132:135], a[168:171], a[24:27]\n" \
"ds_read_b64_tr_b16 a[182:183], v37 offset:33792\n" \
"v_mfma_f32_16x16x32_bf16 a[24:27], a[196:199], a[232:235], a[24:27]\n" \
"ds_read_b64_tr_b16 a[244:245], v37 offset:40960\n" \
"v_mfma_f32_16x16x32_bf16 a[28:31], a[172:175], a[132:135], a[28:31]\n" \
"s_cmp_lg_u32 s16, 0\n" \
"s_cselect_b32 s6, s7, s6\n" \
"v_mfma_f32_16x16x32_bf16 a[28:31], a[236:239], a[196:199], a[28:31]\n" \
"s_addk_i32 s84, 0x2000\n" \
"v_mfma_f32_16x16x32_bf16 a[20:23], a[164:167], a[132:135], a[20:23]\n" \
"buffer_load_dwordx4 v25, s[76:79], s84 offen offset:1024 lds\n" \
"v_mfma_f32_16x16x32_bf16 a[20:23], a[228:231], a[196:199], a[20:23]\n" \
"ds_read_b64_tr_b16 a[246:247], v37 offset:41984\n" \
"s_mulk_i32 s4, 0x4800\n" \
"v_mfma_f32_16x16x32_bf16 a[16:19], a[160:163], a[132:135], a[16:19]\n" \
"ds_read_b64_tr_b16 a[184:185], v38 offset:32768\n" \
"v_writelane_b32 v11, s6, 42\n" \
"v_mfma_f32_16x16x32_bf16 a[16:19], a[224:227], a[196:199], a[16:19]\n" \
"ds_read_b64_tr_b16 a[186:187], v38 offset:33792\n" \
"s_add_i32 s6, s14, s10\n" \
"v_mfma_f32_16x16x32_bf16 a[32:35], a[160:163], a[136:139], a[32:35]\n" \
"s_addk_i32 s84, 0x2000\n" \
"v_mfma_f32_16x16x32_bf16 a[32:35], a[224:227], a[200:203], a[32:35]\n" \
"buffer_load_dwordx4 v24, s[76:79], s84 offen offset:2048 lds\n" \
"v_mfma_f32_16x16x32_bf16 a[36:39], a[164:167], a[136:139], a[36:39]\n" \
"ds_read_b64_tr_b16 a[248:249], v38 offset:40960\n" \
"v_writelane_b32 v11, s6, 43\n" \
"v_mfma_f32_16x16x32_bf16 a[36:39], a[228:231], a[200:203], a[36:39]\n" \
"ds_read_b64_tr_b16 a[250:251], v38 offset:41984\n" \
"s_add_i32 s6, s9, 0x21c000\n" \
"v_mfma_f32_16x16x32_bf16 a[44:47], a[172:175], a[136:139], a[44:47]\n" \
"ds_read_b64_tr_b16 a[188:189], v39 offset:32768\n" \
"s_add_i32 s9, s6, s10\n" \
"v_mfma_f32_16x16x32_bf16 a[44:47], a[236:239], a[200:203], a[44:47]\n" \
"ds_read_b64_tr_b16 a[190:191], v39 offset:33792\n" \
"v_mfma_f32_16x16x32_bf16 a[40:43], a[136:139], a[168:171], a[40:43]\n" \
"s_add_u32 s88, s42, 0x3000\n" \
"buffer_store_dwordx4 v[212:215], v20, s[80:83], s88 offen offset:2048 sc1\n" \
"v_mfma_f32_16x16x32_bf16 a[40:43], a[200:203], a[232:235], a[40:43]\n" \
"ds_read_b64_tr_b16 a[252:253], v39 offset:40960\n" \
"v_mfma_f32_16x16x32_bf16 a[56:59], a[140:143], a[168:171], a[56:59]\n" \
"ds_read_b64_tr_b16 a[254:255], v39 offset:41984\n" \
"v_mfma_f32_16x16x32_bf16 a[56:59], a[204:207], a[232:235], a[56:59]\n" \
"s_cmp_lg_u32 s12, 0\n" \
"s_cselect_b32 s6, s6, s14\n" \
"v_mfma_f32_16x16x32_bf16 a[60:63], a[172:175], a[140:143], a[60:63]\n" \
"s_addk_i32 s6, 0x100\n" \
"v_writelane_b32 v11, s9, 44\n" \
"v_mfma_f32_16x16x32_bf16 a[60:63], a[236:239], a[204:207], a[60:63]\n" \
"s_addk_i32 s84, 0x2000\n" \
"v_mfma_f32_16x16x32_bf16 a[52:55], a[164:167], a[140:143], a[52:55]\n" \
"buffer_load_dwordx4 v25, s[76:79], s84 offen offset:3072 lds\n" \
"v_mfma_f32_16x16x32_bf16 a[52:55], a[228:231], a[204:207], a[52:55]\n" \
"s_cmp_lg_u32 s16, 0\n" \
"s_cselect_b32 s6, s7, s6\n" \
"v_mfma_f32_16x16x32_bf16 a[48:51], a[160:163], a[140:143], a[48:51]\n" \
"v_writelane_b32 v11, s6, 45\n" \
"s_or_b32 s6, s8, s15\n" \
"v_mfma_f32_16x16x32_bf16 a[48:51], a[224:227], a[204:207], a[48:51]\n" \
"v_writelane_b32 v11, s6, 53\n" \
"s_lshl_b32 s6, s3, 17\n" \
"s_waitcnt lgkmcnt(15)\n" \
"v_mfma_f32_16x16x32_bf16 a[64:67], a[164:167], v[248:251], a[64:67]\n" \
"s_add_i32 s6, s13, s6\n" \
"s_or_b32 s5, s6, s5\n" \
"v_mfma_f32_16x16x32_bf16 a[64:67], a[228:231], v[48:51], a[64:67]\n" \
"s_or_b32 s5, s5, 0x3840\n" \
"v_writelane_b32 v11, s5, 6\n" \
"v_mfma_f32_16x16x32_bf16 a[80:83], a[172:175], v[248:251], a[80:83]\n" \
"s_mov_b32 s5, 0xff0000\n" \
"v_mfma_f32_16x16x32_bf16 a[80:83], a[236:239], v[48:51], a[80:83]\n" \
"s_cmp_lg_u32 s16, 0\n" \
"s_cselect_b32 s5, s17, s5\n" \
"v_mfma_f32_16x16x32_bf16 a[68:71], v[248:251], a[168:171], a[68:71]\n" \
"v_writelane_b32 v11, s5, 7\n" \
"s_mul_i32 s5, s3, 0x1200\n" \
"s_waitcnt vmcnt(10) lgkmcnt(0)\n" \
"v_mfma_f32_16x16x32_bf16 a[68:71], v[48:51], a[232:235], a[68:71]\n" \
"s_barrier\n" \
"v_xor_b32_e32 v28, 0x20000, v28\n" \
"v_xor_b32_e32 v29, 0x20000, v29\n" \
"v_mfma_f32_16x16x32_bf16 v[52:55], a[176:179], a[128:131], v[52:55]\n" \
"s_add_u32 m0, s40, 0x8000\n" \
"s_add_u32 s85, s39, 0xe000\n" \
"v_mfma_f32_16x16x32_bf16 v[52:55], a[240:243], a[192:195], v[52:55]\n" \
"buffer_load_dwordx4 v20, s[80:83], s85 offen sc1 lds\n" \
"v_mfma_f32_16x16x32_bf16 v[56:59], a[180:183], a[128:131], v[56:59]\n" \
"ds_read_b64_tr_b16 a[160:161], v32 offset:0\n" \
"s_add_i32 s4, s5, s4\n" \
"v_mfma_f32_16x16x32_bf16 v[56:59], a[244:247], a[192:195], v[56:59]\n" \
"ds_read_b64_tr_b16 a[162:163], v32 offset:512\n" \
"s_add_i32 s4, s4, s11\n" \
"v_mfma_f32_16x16x32_bf16 v[60:63], a[184:187], a[128:131], v[60:63]\n" \
"s_add_u32 m0, s40, 0x9000\n" \
"s_add_u32 s85, s39, 0xf000\n" \
"v_mfma_f32_16x16x32_bf16 v[60:63], a[248:251], a[192:195], v[60:63]\n" \
"buffer_load_dwordx4 v20, s[80:83], s85 offen sc1 lds\n" \
"v_mfma_f32_16x16x32_bf16 v[64:67], a[188:191], a[128:131], v[64:67]\n" \
"ds_read_b64_tr_b16 a[224:225], v32 offset:4096\n" \
"v_writelane_b32 v11, s4, 8\n" \
"v_mfma_f32_16x16x32_bf16 v[64:67], a[252:255], a[192:195], v[64:67]\n" \
"ds_read_b128 a[128:131], v28 offset:0\n" \
"s_lshl_b32 s4, s2, 16\n" \
"v_mfma_f32_16x16x32_bf16 v[80:83], a[188:191], a[132:135], v[80:83]\n" \
"ds_read_b128 a[192:195], v29 offset:0\n" \
"s_add_i32 s5, s4, 0x10000\n" \
"v_mfma_f32_16x16x32_bf16 v[80:83], a[252:255], a[196:199], v[80:83]\n" \
"s_add_u32 m0, s40, 0xb000\n" \
"s_movk_i32 s84, 0x380\n" \
"v_mfma_f32_16x16x32_bf16 v[76:79], a[184:187], a[132:135], v[76:79]\n" \
"buffer_load_dwordx4 v21, s[76:79], s84 offen offset:0 lds\n" \
"v_mfma_f32_16x16x32_bf16 v[76:79], a[248:251], a[196:199], v[76:79]\n" \
"ds_read_b64_tr_b16 a[226:227], v32 offset:4608\n" \
"s_and_b32 s5, s5, 0x70000\n" \
"v_mfma_f32_16x16x32_bf16 v[72:75], a[180:183], a[132:135], v[72:75]\n" \
"ds_read_b64_tr_b16 a[164:165], v33 offset:0\n" \
"s_add_i32 s5, s13, s5\n" \
"v_mfma_f32_16x16x32_bf16 v[72:75], a[244:247], a[196:199], v[72:75]\n" \
"v_writelane_b32 v11, s5, 46\n" \
"s_add_i32 s5, s4, 0x20000\n" \
"v_mfma_f32_16x16x32_bf16 v[68:71], a[176:179], a[132:135], v[68:71]\n" \
"ds_read_b64_tr_b16 a[166:167], v33 offset:512\n" \
"s_and_b32 s5, s5, 0x70000\n" \
"v_mfma_f32_16x16x32_bf16 v[68:71], a[240:243], a[196:199], v[68:71]\n" \
"ds_read_b128 a[132:135], v28 offset:2048\n" \
"s_add_i32 s5, s13, s5\n" \
"v_mfma_f32_16x16x32_bf16 v[84:87], a[176:179], a[136:139], v[84:87]\n" \
"ds_read_b128 a[196:199], v29 offset:2048\n" \
"s_bitset1_b32 s5, 11\n" \
"v_mfma_f32_16x16x32_bf16 v[84:87], a[240:243], a[200:203], v[84:87]\n" \
"s_add_u32 m0, s31, 0x8000\n" \
"s_mov_b32 s84, 0x380000\n" \
"v_mfma_f32_16x16x32_bf16 v[88:91], a[180:183], a[136:139], v[88:91]\n" \
"buffer_load_dwordx4 v26, s[68:71], s84 offen offset:0 lds\n" \
"v_mfma_f32_16x16x32_bf16 v[88:91], a[244:247], a[200:203], v[88:91]\n" \
"s_add_u32 s89, s42, 0x0\n" \
"buffer_store_dwordx4 v[184:187], v20, s[80:83], s89 offen offset:1024 sc1\n" \
"v_mfma_f32_16x16x32_bf16 v[92:95], a[184:187], a[136:139], v[92:95]\n" \
"ds_read_b64_tr_b16 a[228:229], v33 offset:4096\n" \
"v_writelane_b32 v11, s5, 47\n" \
"v_mfma_f32_16x16x32_bf16 v[92:95], a[248:251], a[200:203], v[92:95]\n" \
"ds_read_b64_tr_b16 a[230:231], v33 offset:4608\n" \
"s_add_i32 s5, s4, 0x30000\n" \
"v_mfma_f32_16x16x32_bf16 v[96:99], a[188:191], a[136:139], v[96:99]\n" \
"s_and_b32 s5, s5, 0x70000\n" \
"s_add_i32 s5, s13, s5\n" \
"v_mfma_f32_16x16x32_bf16 v[96:99], a[252:255], a[200:203], v[96:99]\n" \
"ds_read_b128 a[136:139], v28 offset:4096\n" \
"s_bitset1_b32 s5, 12\n" \
"v_mfma_f32_16x16x32_bf16 v[112:115], a[188:191], a[140:143], v[112:115]\n" \
"ds_read_b128 a[200:203], v29 offset:4096\n" \
"v_writelane_b32 v11, s5, 48\n" \
"v_mfma_f32_16x16x32_bf16 v[112:115], a[252:255], a[204:207], v[112:115]\n" \
"s_addk_i32 s84, 0x7c00\n" \
"v_mfma_f32_16x16x32_bf16 v[108:111], a[184:187], a[140:143], v[108:111]\n" \
"buffer_load_dwordx4 v26, s[68:71], s84 offen offset:1024 lds\n" \
"v_mfma_f32_16x16x32_bf16 v[108:111], a[248:251], a[204:207], v[108:111]\n" \
"ds_read_b64_tr_b16 a[172:173], v35 offset:0\n" \
"s_and_b32 s5, s4, 0x70000\n" \
"v_mfma_f32_16x16x32_bf16 v[104:107], a[180:183], a[140:143], v[104:107]\n" \
"s_addk_i32 s84, 0x7c00\n" \
"v_mfma_f32_16x16x32_bf16 v[104:107], a[244:247], a[204:207], v[104:107]\n" \
"buffer_load_dwordx4 v27, s[68:71], s84 offen offset:2048 lds\n" \
"v_mfma_f32_16x16x32_bf16 v[100:103], a[176:179], a[140:143], v[100:103]\n" \
"ds_read_b64_tr_b16 a[174:175], v35 offset:512\n" \
"s_xor_b32 s5, s5, 0x40000\n" \
"v_mfma_f32_16x16x32_bf16 v[100:103], a[240:243], a[204:207], v[100:103]\n" \
"ds_read_b128 a[140:143], v28 offset:6144\n" \
"s_add_i32 s5, s13, s5\n" \
"v_mfma_f32_16x16x32_bf16 v[116:119], a[176:179], a[144:147], v[116:119]\n" \
"ds_read_b128 a[204:207], v29 offset:6144\n" \
"s_or_b32 s5, s5, 0x1800\n" \
"v_mfma_f32_16x16x32_bf16 v[116:119], a[240:243], a[208:211], v[116:119]\n" \
"s_addk_i32 s84, 0x7c00\n" \
"v_mfma_f32_16x16x32_bf16 v[120:123], a[180:183], a[144:147], v[120:123]\n" \
"buffer_load_dwordx4 v27, s[68:71], s84 offen offset:3072 lds\n" \
"v_mfma_f32_16x16x32_bf16 v[120:123], a[244:247], a[208:211], v[120:123]\n" \
"ds_read_b64_tr_b16 a[236:237], v35 offset:4096\n" \
"v_writelane_b32 v11, s5, 49\n" \
"v_mfma_f32_16x16x32_bf16 v[124:127], a[184:187], a[144:147], v[124:127]\n" \
"ds_read_b64_tr_b16 a[238:239], v35 offset:4608\n" \
"s_add_i32 s5, s4, 0x50000\n" \
"v_mfma_f32_16x16x32_bf16 v[124:127], a[248:251], a[208:211], v[124:127]\n" \
"s_add_u32 m0, s30, 0x20000\n" \
"s_movk_i32 s84, 0x400\n" \
"v_mfma_f32_16x16x32_bf16 v[128:131], a[188:191], a[144:147], v[128:131]\n" \
"buffer_load_dwordx4 v24, s[76:79], s84 offen offset:0 lds\n" \
"v_mfma_f32_16x16x32_bf16 v[128:131], a[252:255], a[208:211], v[128:131]\n" \
"ds_read_b128 a[144:147], v28 offset:8192\n" \
"s_and_b32 s5, s5, 0x70000\n" \
"v_mfma_f32_16x16x32_bf16 v[144:147], a[188:191], a[148:151], v[144:147]\n" \
"ds_read_b128 a[208:211], v29 offset:8192\n" \
"s_add_i32 s5, s13, s5\n" \
"v_mfma_f32_16x16x32_bf16 v[144:147], a[252:255], a[212:215], v[144:147]\n" \
"ds_read_b64_tr_b16 a[168:169], v34 offset:0\n" \
"s_bitset1_b32 s5, 13\n" \
"v_mfma_f32_16x16x32_bf16 v[140:143], a[184:187], a[148:151], v[140:143]\n" \
"s_movk_i32 s84, 0x2400\n" \
"s_nop 0\n" \
"v_mfma_f32_16x16x32_bf16 v[140:143], a[248:251], a[212:215], v[140:143]\n" \
"buffer_load_dwordx4 v25, s[76:79], s84 offen offset:1024 lds\n" \
"v_mfma_f32_16x16x32_bf16 v[136:139], a[180:183], a[148:151], v[136:139]\n" \
"ds_read_b64_tr_b16 a[170:171], v34 offset:512\n" \
"v_writelane_b32 v11, s5, 50\n" \
"v_mfma_f32_16x16x32_bf16 v[136:139], a[244:247], a[212:215], v[136:139]\n" \
"ds_read_b64_tr_b16 a[232:233], v34 offset:4096\n" \
"s_add_i32 s5, s4, 0x60000\n" \
"v_mfma_f32_16x16x32_bf16 v[132:135], a[176:179], a[148:151], v[132:135]\n" \
"ds_read_b64_tr_b16 a[234:235], v34 offset:4608\n" \
"s_and_b32 s5, s5, 0x70000\n" \
"v_mfma_f32_16x16x32_bf16 v[132:135], a[240:243], a[212:215], v[132:135]\n" \
"ds_read_b128 a[148:151], v28 offset:10240\n" \
"s_add_i32 s4, s4, 0x70000\n" \
"v_mfma_f32_16x16x32_bf16 v[148:151], a[176:179], a[152:155], v[148:151]\n" \
"ds_read_b128 a[212:215], v29 offset:10240\n" \
"s_add_i32 s5, s13, s5\n" \
"v_mfma_f32_16x16x32_bf16 v[148:151], a[240:243], a[216:219], v[148:151]\n" \
"s_movk_i32 s84, 0x4400\n" \
"v_mfma_f32_16x16x32_bf16 v[152:155], a[180:183], a[152:155], v[152:155]\n" \
"buffer_load_dwordx4 v24, s[76:79], s84 offen offset:2048 lds\n" \
"v_mfma_f32_16x16x32_bf16 v[152:155], a[244:247], a[216:219], v[152:155]\n" \
"s_movk_i32 s84, 0x6400\n" \
"s_nop 0\n" \
"v_mfma_f32_16x16x32_bf16 v[156:159], a[184:187], a[152:155], v[156:159]\n" \
"buffer_load_dwordx4 v25, s[76:79], s84 offen offset:3072 lds\n" \
"v_mfma_f32_16x16x32_bf16 v[156:159], a[248:251], a[216:219], v[156:159]\n" \
"ds_read_b128 v[248:251], v22 offset:0\n" \
"s_and_b32 s4, s4, 0x70000\n" \
"v_mfma_f32_16x16x32_bf16 v[160:163], a[188:191], a[152:155], v[160:163]\n" \
"ds_read_b128 v[48:51], v23 offset:0\n" \
"s_or_b32 s5, s5, 0x2800\n" \
"v_mfma_f32_16x16x32_bf16 v[160:163], a[252:255], a[216:219], v[160:163]\n" \
"ds_read_b128 a[152:155], v28 offset:12288\n" \
"v_writelane_b32 v11, s5, 51\n" \
"v_mfma_f32_16x16x32_bf16 v[176:179], a[188:191], a[156:159], v[176:179]\n" \
"ds_read_b128 a[216:219], v29 offset:12288\n" \
"s_add_i32 s13, s13, s4\n" \
"v_mfma_f32_16x16x32_bf16 v[176:179], a[252:255], a[220:223], v[176:179]\n" \
"s_or_b32 s4, s13, 0x3000\n" \
"v_writelane_b32 v11, s4, 52\n" \
"v_mfma_f32_16x16x32_bf16 v[172:175], a[184:187], a[156:159], v[172:175]\n" \
"s_movk_i32 s11, 0x4000\n" \
"s_lshl_b32 s4, s2, 2\n" \
"v_mfma_f32_16x16x32_bf16 v[172:175], a[248:251], a[220:223], v[172:175]\n" \
"s_and_b32 s4, s4, 28\n" \
"s_bfe_u32 s5, s2, 0x20003\n" \
"v_mfma_f32_16x16x32_bf16 v[168:171], a[180:183], a[156:159], v[168:171]\n" \
"s_or_b32 s4, s4, s5\n" \
"s_lshr_b32 s5, s2, 5\n" \
"v_mfma_f32_16x16x32_bf16 v[168:171], a[244:247], a[220:223], v[168:171]\n" \
"s_lshr_b32 s19, s3, 1\n" \
"s_lshl_b32 s18, s5, 1\n" \
"v_mfma_f32_16x16x32_bf16 v[164:167], a[176:179], a[156:159], v[164:167]\n" \
"s_lshl_b32 s6, s4, 20\n" \
"s_lshl_b32 s7, s4, 8\n" \
"v_mfma_f32_16x16x32_bf16 v[164:167], a[240:243], a[220:223], v[164:167]\n" \
"ds_read_b128 a[156:159], v28 offset:14336\n" \
"ds_read_b128 a[220:223], v29 offset:14336\n" \
"s_waitcnt lgkmcnt(15)\n" \
"v_mfma_f32_16x16x32_bf16 a[0:3], a[160:163], a[128:131], a[0:3]\n" \
"s_add_u32 m0, s30, 0x21000\n" \
"s_addk_i32 s84, 0x3000\n" \
"v_mfma_f32_16x16x32_bf16 a[0:3], a[224:227], a[192:195], a[0:3]\n" \
"buffer_load_dwordx4 v24, s[76:79], s84 offen offset:0 lds\n" \
"v_mfma_f32_16x16x32_bf16 a[4:7], a[164:167], a[128:131], a[4:7]\n" \
"ds_read_b64_tr_b16 a[176:177], v36 offset:0\n" \
"s_and_b32 s13, s3, 1\n" \
"v_mfma_f32_16x16x32_bf16 a[4:7], a[228:231], a[192:195], a[4:7]\n" \
"ds_read_b64_tr_b16 a[178:179], v36 offset:1024\n" \
"s_add_i32 s14, s18, s19\n" \
"v_mfma_f32_16x16x32_bf16 a[12:15], a[172:175], a[128:131], a[12:15]\n" \
"ds_read_b64_tr_b16 a[240:241], v36 offset:8192\n" \
"s_waitcnt lgkmcnt(15)\n" \
"v_mfma_f32_16x16x32_bf16 a[12:15], a[236:239], a[192:195], a[12:15]\n" \
"s_add_u32 s88, s42, 0x1000\n" \
"buffer_store_dwordx4 v[188:191], v20, s[80:83], s88 offen offset:1024 sc1\n" \
"s_waitcnt lgkmcnt(13)\n" \
"v_mfma_f32_16x16x32_bf16 a[8:11], a[128:131], a[168:171], a[8:11]\n" \
"ds_read_b64_tr_b16 a[242:243], v36 offset:9216\n" \
"s_waitcnt lgkmcnt(12)\n" \
"v_mfma_f32_16x16x32_bf16 a[8:11], a[192:195], a[232:235], a[8:11]\n" \
"ds_read_b64_tr_b16 a[180:181], v37 offset:0\n" \
"v_mfma_f32_16x16x32_bf16 a[24:27], a[132:135], a[168:171], a[24:27]\n" \
"ds_read_b64_tr_b16 a[182:183], v37 offset:1024\n" \
"v_mfma_f32_16x16x32_bf16 a[24:27], a[196:199], a[232:235], a[24:27]\n" \
"ds_read_b64_tr_b16 a[244:245], v37 offset:8192\n" \
"v_mfma_f32_16x16x32_bf16 a[28:31], a[172:175], a[132:135], a[28:31]\n" \
"s_add_u32 s6, s26, s6\n" \
"s_addc_u32 s9, s27, 0\n" \
"v_mfma_f32_16x16x32_bf16 a[28:31], a[236:239], a[196:199], a[28:31]\n" \
"s_addk_i32 s84, 0x2000\n" \
"v_mfma_f32_16x16x32_bf16 a[20:23], a[164:167], a[132:135], a[20:23]\n" \
"buffer_load_dwordx4 v25, s[76:79], s84 offen offset:1024 lds\n" \
"v_mfma_f32_16x16x32_bf16 a[20:23], a[228:231], a[196:199], a[20:23]\n" \
"ds_read_b64_tr_b16 a[246:247], v37 offset:9216\n" \
"v_mfma_f32_16x16x32_bf16 a[16:19], a[160:163], a[132:135], a[16:19]\n" \
"ds_read_b64_tr_b16 a[184:185], v38 offset:0\n" \
"v_mfma_f32_16x16x32_bf16 a[16:19], a[224:227], a[196:199], a[16:19]\n" \
"ds_read_b64_tr_b16 a[186:187], v38 offset:1024\n" \
"v_mfma_f32_16x16x32_bf16 a[32:35], a[160:163], a[136:139], a[32:35]\n" \
"s_addk_i32 s84, 0x2000\n" \
"s_nop 0\n" \
"v_mfma_f32_16x16x32_bf16 a[32:35], a[224:227], a[200:203], a[32:35]\n" \
"buffer_load_dwordx4 v24, s[76:79], s84 offen offset:2048 lds\n" \
"v_mfma_f32_16x16x32_bf16 a[36:39], a[164:167], a[136:139], a[36:39]\n" \
"ds_read_b64_tr_b16 a[248:249], v38 offset:8192\n" \
"v_mfma_f32_16x16x32_bf16 a[36:39], a[228:231], a[200:203], a[36:39]\n" \
"ds_read_b64_tr_b16 a[250:251], v38 offset:9216\n" \
"v_mfma_f32_16x16x32_bf16 a[44:47], a[172:175], a[136:139], a[44:47]\n" \
"ds_read_b64_tr_b16 a[188:189], v39 offset:0\n" \
"v_mfma_f32_16x16x32_bf16 a[44:47], a[236:239], a[200:203], a[44:47]\n" \
"ds_read_b64_tr_b16 a[190:191], v39 offset:1024\n" \
"v_mfma_f32_16x16x32_bf16 a[40:43], a[136:139], a[168:171], a[40:43]\n" \
"s_add_u32 s89, s42, 0x2000\n" \
"buffer_store_dwordx4 v[192:195], v20, s[80:83], s89 offen offset:0 sc1\n" \
"v_mfma_f32_16x16x32_bf16 a[40:43], a[200:203], a[232:235], a[40:43]\n" \
"ds_read_b64_tr_b16 a[252:253], v39 offset:8192\n" \
"v_mfma_f32_16x16x32_bf16 a[56:59], a[140:143], a[168:171], a[56:59]\n" \
"ds_read_b64_tr_b16 a[254:255], v39 offset:9216\n" \
"v_mfma_f32_16x16x32_bf16 a[56:59], a[204:207], a[232:235], a[56:59]\n" \
"s_add_u32 s8, s6, 0x1000000\n" \
"s_addc_u32 s6, s9, 0\n" \
"v_mfma_f32_16x16x32_bf16 a[60:63], a[172:175], a[140:143], a[60:63]\n" \
"s_and_b32 s9, s6, 0xffff\n" \
"s_lshl_b32 s6, s5, 16\n" \
"v_mfma_f32_16x16x32_bf16 a[60:63], a[236:239], a[204:207], a[60:63]\n" \
"s_addk_i32 s84, 0x2000\n" \
"v_mfma_f32_16x16x32_bf16 a[52:55], a[164:167], a[140:143], a[52:55]\n" \
"buffer_load_dwordx4 v25, s[76:79], s84 offen offset:3072 lds\n" \
"v_mfma_f32_16x16x32_bf16 a[52:55], a[228:231], a[204:207], a[52:55]\n" \
"s_sub_i32 s10, 0x100000, s6\n" \
"s_mov_b32 s6, 0x8000\n" \
"v_mfma_f32_16x16x32_bf16 a[48:51], a[160:163], a[140:143], a[48:51]\n" \
"s_cmp_lg_u32 s5, 0\n" \
"s_cselect_b32 s17, s11, s6\n" \
".fill (L_pb + 32480 - .) / 4, 4, 0xBF800000\n" \
"L_pp8:\n" \
"s_cbranch_execz L_pret\n" \
"v_mfma_f32_16x16x32_bf16 a[48:51], a[224:227], a[204:207], a[48:51]\n" \
"s_lshl_b32 s6, s3, 1\n" \
"s_lshl_b32 s5, s5, 3\n" \
"s_waitcnt lgkmcnt(15)\n" \
"v_mfma_f32_16x16x32_bf16 a[64:67], a[164:167], v[248:251], a[64:67]\n" \
"s_and_b32 s6, s6, 4\n" \
"s_or_b32 s12, s5, s6\n" \
"v_mfma_f32_16x16x32_bf16 a[64:67], a[228:231], v[48:51], a[64:67]\n" \
"s_sub_i32 s5, s13, s5\n" \
"s_or_b32 s5, s5, s6\n" \
"v_mfma_f32_16x16x32_bf16 a[80:83], a[172:175], v[248:251], a[80:83]\n" \
"s_or_b32 s6, s12, s13\n" \
"s_lshl_b32 s4, s4, 2\n" \
"v_mfma_f32_16x16x32_bf16 a[80:83], a[236:239], v[48:51], a[80:83]\n" \
"s_mulk_i32 s6, 0x92\n" \
"s_add_i32 s4, s4, 18\n" \
"v_mfma_f32_16x16x32_bf16 a[68:71], v[248:251], a[168:171], a[68:71]\n" \
"s_mulk_i32 s5, 0x92\n" \
"s_add_i32 s6, s4, s6\n" \
"s_waitcnt vmcnt(10) lgkmcnt(0)\n" \
"v_mfma_f32_16x16x32_bf16 a[68:71], v[48:51], a[232:235], a[68:71]\n" \
"s_barrier\n" \
"v_mfma_f32_16x16x32_bf16 v[52:55], a[176:179], a[128:131], v[52:55]\n" \
"s_add_u32 m0, s40, 0x0\n" \
"s_add_u32 s85, s39, 0x10000\n" \
"v_mfma_f32_16x16x32_bf16 v[52:55], a[240:243], a[192:195], v[52:55]\n" \
"buffer_load_dwordx4 v20, s[80:83], s85 offen sc1 lds\n" \
"v_mfma_f32_16x16x32_bf16 v[56:59], a[180:183], a[128:131], v[56:59]\n" \
"ds_read_b64_tr_b16 a[160:161], v32 offset:32768\n" \
"s_add_i32 s5, s5, s4\n" \
"v_mfma_f32_16x16x32_bf16 v[56:59], a[244:247], a[192:195], v[56:59]\n" \
"ds_read_b64_tr_b16 a[162:163], v32 offset:33280\n" \
"s_lshl_b32 s4, s3, 11\n" \
"v_mfma_f32_16x16x32_bf16 v[60:63], a[184:187], a[128:131], v[60:63]\n" \
"s_lshl_b32 s15, s13, 12\n" \
"s_lshl_b32 s16, s19, 14\n" \
"v_mfma_f32_16x16x32_bf16 v[60:63], a[248:251], a[192:195], v[60:63]\n" \
"ds_read_b64_tr_b16 a[224:225], v32 offset:36864\n" \
"s_mul_i32 s19, s19, 0xe000\n" \
"v_mfma_f32_16x16x32_bf16 v[64:67], a[188:191], a[128:131], v[64:67]\n" \
"s_mov_b32 s11, 0x20000\n" \
"s_or_b32 s12, s4, 0xc000\n" \
"v_mfma_f32_16x16x32_bf16 v[64:67], a[252:255], a[192:195], v[64:67]\n" \
"ds_read_b128 a[128:131], v28 offset:32768\n" \
"s_lshl_b32 s4, s3, 8\n" \
"v_mfma_f32_16x16x32_bf16 v[80:83], a[188:191], a[132:135], v[80:83]\n" \
"ds_read_b128 a[192:195], v29 offset:32768\n" \
"s_or_b32 s22, s15, s16\n" \
"v_mfma_f32_16x16x32_bf16 v[80:83], a[252:255], a[196:199], v[80:83]\n" \
"s_add_u32 m0, s40, 0x1000\n" \
"s_add_u32 s85, s39, 0x11000\n" \
"v_mfma_f32_16x16x32_bf16 v[76:79], a[184:187], a[132:135], v[76:79]\n" \
"buffer_load_dwordx4 v20, s[80:83], s85 offen sc1 lds\n" \
"v_mfma_f32_16x16x32_bf16 v[76:79], a[248:251], a[196:199], v[76:79]\n" \
"ds_read_b64_tr_b16 a[226:227], v32 offset:37376\n" \
"s_or_b32 s23, s19, s15\n" \
"v_mfma_f32_16x16x32_bf16 v[72:75], a[180:183], a[132:135], v[72:75]\n" \
"ds_read_b64_tr_b16 a[164:165], v33 offset:32768\n" \
"s_lshl_b32 s60, s6, 10\n" \
"v_mfma_f32_16x16x32_bf16 v[72:75], a[244:247], a[196:199], v[72:75]\n" \
"s_lshl_b32 s61, s5, 10\n" \
"s_lshl_b32 s14, s14, 15\n" \
"s_nop 0\n" \
"v_mfma_f32_16x16x32_bf16 v[68:71], a[176:179], a[132:135], v[68:71]\n" \
"ds_read_b64_tr_b16 a[166:167], v33 offset:33280\n" \
"s_or_b32 s20, s4, 0x26c00\n" \
"v_mfma_f32_16x16x32_bf16 v[68:71], a[240:243], a[196:199], v[68:71]\n" \
"ds_read_b128 a[132:135], v28 offset:34816\n" \
"s_or_b32 s13, s22, 0x8000\n" \
"v_mfma_f32_16x16x32_bf16 v[84:87], a[176:179], a[136:139], v[84:87]\n" \
"ds_read_b128 a[196:199], v29 offset:34816\n" \
"s_add_i32 s22, s22, 0x14000\n" \
"v_mfma_f32_16x16x32_bf16 v[84:87], a[240:243], a[200:203], v[84:87]\n" \
"s_add_u32 m0, s40, 0x3000\n" \
"s_movk_i32 s84, 0x400\n" \
"v_mfma_f32_16x16x32_bf16 v[88:91], a[180:183], a[136:139], v[88:91]\n" \
"buffer_load_dwordx4 v21, s[76:79], s84 offen offset:0 lds\n" \
"v_mfma_f32_16x16x32_bf16 v[88:91], a[244:247], a[200:203], v[88:91]\n" \
"ds_read_b64_tr_b16 a[228:229], v33 offset:36864\n" \
"s_bitset1_b32 s23, 16\n" \
"v_mfma_f32_16x16x32_bf16 v[92:95], a[184:187], a[136:139], v[92:95]\n" \
"ds_read_b64_tr_b16 a[230:231], v33 offset:37376\n" \
"s_or_b32 s24, s7, 0x5be8020\n" \
"v_mfma_f32_16x16x32_bf16 v[92:95], a[248:251], a[200:203], v[92:95]\n" \
"s_add_u32 s88, s42, 0x2000\n" \
"buffer_store_dwordx4 v[216:219], v20, s[80:83], s88 offen offset:1024 sc1\n" \
"v_mfma_f32_16x16x32_bf16 v[96:99], a[188:191], a[136:139], v[96:99]\n" \
"s_and_b32 s57, s27, 0xffff\n" \
"s_mov_b32 s58, 0x6000000\n" \
"v_mfma_f32_16x16x32_bf16 v[96:99], a[252:255], a[200:203], v[96:99]\n" \
"ds_read_b128 a[136:139], v28 offset:36864\n" \
"s_mov_b32 s56, s26\n" \
"v_mfma_f32_16x16x32_bf16 v[112:115], a[188:191], a[140:143], v[112:115]\n" \
"ds_read_b128 a[200:203], v29 offset:36864\n" \
"s_mov_b32 s59, s11\n" \
"v_mfma_f32_16x16x32_bf16 v[112:115], a[252:255], a[204:207], v[112:115]\n" \
"ds_read_b64_tr_b16 a[172:173], v35 offset:32768\n" \
"s_add_i32 s60, s60, 0x3000000\n" \
"v_mfma_f32_16x16x32_bf16 v[108:111], a[184:187], a[140:143], v[108:111]\n" \
"ds_read_b64_tr_b16 a[174:175], v35 offset:33280\n" \
"s_add_i32 s61, s61, 0x411c000\n" \
"v_mfma_f32_16x16x32_bf16 v[108:111], a[248:251], a[204:207], v[108:111]\n" \
"s_sub_i32 s19, 14, s18\n" \
"s_mov_b32 s21, 0x71800000\n" \
"v_mfma_f32_16x16x32_bf16 v[104:107], a[180:183], a[140:143], v[104:107]\n" \
"s_add_u32 m0, s31, 0x0\n" \
"s_mov_b32 s84, 0x400000\n" \
"v_mfma_f32_16x16x32_bf16 v[104:107], a[244:247], a[204:207], v[104:107]\n" \
"buffer_load_dwordx4 v26, s[68:71], s84 offen offset:0 lds\n" \
"v_mfma_f32_16x16x32_bf16 v[100:103], a[176:179], a[140:143], v[100:103]\n" \
"ds_read_b64_tr_b16 a[236:237], v35 offset:36864\n" \
"s_mov_b32 s4, L_gsw-L_gtr\n" \
"v_mfma_f32_16x16x32_bf16 v[100:103], a[240:243], a[204:207], v[100:103]\n" \
"ds_read_b128 a[140:143], v28 offset:38912\n" \
"v_mfma_f32_16x16x32_bf16 v[116:119], a[176:179], a[144:147], v[116:119]\n" \
"ds_read_b128 a[204:207], v29 offset:38912\n" \
"v_mfma_f32_16x16x32_bf16 v[116:119], a[240:243], a[208:211], v[116:119]\n" \
"ds_read_b64_tr_b16 a[238:239], v35 offset:37376\n" \
"v_mfma_f32_16x16x32_bf16 v[120:123], a[180:183], a[144:147], v[120:123]\n" \
"ds_read_b64_tr_b16 a[168:169], v34 offset:32768\n" \
"v_mfma_f32_16x16x32_bf16 v[120:123], a[244:247], a[208:211], v[120:123]\n" \
"s_addk_i32 s84, 0x7c00\n" \
"v_mfma_f32_16x16x32_bf16 v[124:127], a[184:187], a[144:147], v[124:127]\n" \
"buffer_load_dwordx4 v26, s[68:71], s84 offen offset:1024 lds\n" \
"v_mfma_f32_16x16x32_bf16 v[124:127], a[248:251], a[208:211], v[124:127]\n" \
"ds_read_b64_tr_b16 a[170:171], v34 offset:33280\n" \
"v_mfma_f32_16x16x32_bf16 v[128:131], a[188:191], a[144:147], v[128:131]\n" \
"s_cmp_eq_u32 s19, 0\n" \
"s_cselect_b32 s4, L_gs7-L_gtr, s4\n" \
"v_mfma_f32_16x16x32_bf16 v[128:131], a[252:255], a[208:211], v[128:131]\n" \
"ds_read_b128 a[144:147], v28 offset:40960\n" \
"v_mfma_f32_16x16x32_bf16 v[144:147], a[188:191], a[148:151], v[144:147]\n" \
"ds_read_b128 a[208:211], v29 offset:40960\n" \
"v_mfma_f32_16x16x32_bf16 v[144:147], a[252:255], a[212:215], v[144:147]\n" \
"ds_read_b64_tr_b16 a[232:233], v34 offset:36864\n" \
"v_mfma_f32_16x16x32_bf16 v[140:143], a[184:187], a[148:151], v[140:143]\n" \
"s_addk_i32 s84, 0x7c00\n" \
"v_mfma_f32_16x16x32_bf16 v[140:143], a[248:251], a[212:215], v[140:143]\n" \
"buffer_load_dwordx4 v27, s[68:71], s84 offen offset:2048 lds\n" \
"v_mfma_f32_16x16x32_bf16 v[136:139], a[180:183], a[148:151], v[136:139]\n" \
"ds_read_b64_tr_b16 a[234:235], v34 offset:37376\n" \
"v_mfma_f32_16x16x32_bf16 v[136:139], a[244:247], a[212:215], v[136:139]\n" \
"s_cmp_eq_u32 s16, 0\n" \
"s_cselect_b32 s4, 0, s4\n" \
"v_mfma_f32_16x16x32_bf16 v[132:135], a[176:179], a[148:151], v[132:135]\n" \
"ds_read_b128 v[248:251], v22 offset:32768\n" \
"v_mfma_f32_16x16x32_bf16 v[132:135], a[240:243], a[212:215], v[132:135]\n" \
"ds_read_b128 a[148:151], v28 offset:43008\n" \
"v_mfma_f32_16x16x32_bf16 v[148:151], a[176:179], a[152:155], v[148:151]\n" \
"ds_read_b128 a[212:215], v29 offset:43008\n" \
"v_mfma_f32_16x16x32_bf16 v[148:151], a[240:243], a[216:219], v[148:151]\n" \
"s_addk_i32 s84, 0x7c00\n" \
"v_mfma_f32_16x16x32_bf16 v[152:155], a[180:183], a[152:155], v[152:155]\n" \
"buffer_load_dwordx4 v27, s[68:71], s84 offen offset:3072 lds\n" \
"v_mfma_f32_16x16x32_bf16 v[152:155], a[244:247], a[216:219], v[152:155]\n" \
"ds_read_b128 v[48:51], v23 offset:32768\n" \
"v_mfma_f32_16x16x32_bf16 v[156:159], a[184:187], a[152:155], v[156:159]\n" \
"s_getpc_b64 s[28:29]\n" \
"L_gp0_%=:\n" \
"s_add_u32 s28, s28, L_gtr-L_gp0_%=\n" \
"s_addc_u32 s29, s29, 0\n" \
"v_mfma_f32_16x16x32_bf16 v[156:159], a[248:251], a[216:219], v[156:159]\n" \
"s_add_u32 s28, s28, s4\n" \
"s_addc_u32 s29, s29, 0\n" \
"v_mfma_f32_16x16x32_bf16 v[160:163], a[188:191], a[152:155], v[160:163]\n" \
"v_mfma_f32_16x16x32_bf16 v[160:163], a[252:255], a[216:219], v[160:163]\n" \
"ds_read_b128 a[152:155], v28 offset:45056\n" \
"v_mfma_f32_16x16x32_bf16 v[176:179], a[188:191], a[156:159], v[176:179]\n" \
"ds_read_b128 a[216:219], v29 offset:45056\n" \
"v_mfma_f32_16x16x32_bf16 v[176:179], a[252:255], a[220:223], v[176:179]\n" \
"v_mfma_f32_16x16x32_bf16 v[172:175], a[184:187], a[156:159], v[172:175]\n" \
"v_mfma_f32_16x16x32_bf16 v[172:175], a[248:251], a[220:223], v[172:175]\n" \
"v_mfma_f32_16x16x32_bf16 v[168:171], a[180:183], a[156:159], v[168:171]\n" \
"v_mfma_f32_16x16x32_bf16 v[168:171], a[244:247], a[220:223], v[168:171]\n" \
"v_mfma_f32_16x16x32_bf16 v[164:167], a[176:179], a[156:159], v[164:167]\n" \
"v_mfma_f32_16x16x32_bf16 v[164:167], a[240:243], a[220:223], v[164:167]\n" \
"ds_read_b128 a[156:159], v28 offset:47104\n" \
"ds_read_b128 a[220:223], v29 offset:47104\n" \
"s_waitcnt lgkmcnt(15)\n" \
"s_nop 0\n" \
"v_mfma_f32_16x16x32_bf16 a[0:3], a[160:163], a[128:131], a[0:3]\n" \
"s_add_u32 s89, s42, 0x3000\n" \
"buffer_store_dwordx4 v[220:223], v20, s[80:83], s89 offen offset:0 sc1\n" \
"v_mfma_f32_16x16x32_bf16 a[0:3], a[224:227], a[192:195], a[0:3]\n" \
"ds_read_b64_tr_b16 a[176:177], v36 offset:32768\n" \
"v_mfma_f32_16x16x32_bf16 a[4:7], a[164:167], a[128:131], a[4:7]\n" \
"ds_read_b64_tr_b16 a[178:179], v36 offset:33792\n" \
"v_mfma_f32_16x16x32_bf16 a[4:7], a[228:231], a[192:195], a[4:7]\n" \
"ds_read_b64_tr_b16 a[240:241], v36 offset:40960\n" \
"v_mfma_f32_16x16x32_bf16 a[12:15], a[172:175], a[128:131], a[12:15]\n" \
"ds_read_b64_tr_b16 a[242:243], v36 offset:41984\n" \
"s_waitcnt lgkmcnt(15)\n" \
"v_mfma_f32_16x16x32_bf16 a[12:15], a[236:239], a[192:195], a[12:15]\n" \
"v_mfma_f32_16x16x32_bf16 a[8:11], a[128:131], a[168:171], a[8:11]\n" \
"ds_read_b64_tr_b16 a[180:181], v37 offset:32768\n" \
"s_waitcnt lgkmcnt(13)\n" \
"v_mfma_f32_16x16x32_bf16 a[8:11], a[192:195], a[232:235], a[8:11]\n" \
"v_mfma_f32_16x16x32_bf16 a[24:27], a[132:135], a[168:171], a[24:27]\n" \
"ds_read_b64_tr_b16 a[182:183], v37 offset:33792\n" \
"v_mfma_f32_16x16x32_bf16 a[24:27], a[196:199], a[232:235], a[24:27]\n" \
"ds_read_b64_tr_b16 a[244:245], v37 offset:40960\n" \
"v_mfma_f32_16x16x32_bf16 a[28:31], a[172:175], a[132:135], a[28:31]\n" \
"v_mfma_f32_16x16x32_bf16 a[28:31], a[236:239], a[196:199], a[28:31]\n" \
"ds_read_b64_tr_b16 a[246:247], v37 offset:41984\n" \
"v_mfma_f32_16x16x32_bf16 a[20:23], a[164:167], a[132:135], a[20:23]\n" \
"v_mfma_f32_16x16x32_bf16 a[20:23], a[228:231], a[196:199], a[20:23]\n" \
"ds_read_b64_tr_b16 a[184:185], v38 offset:32768\n" \
"v_mfma_f32_16x16x32_bf16 a[16:19], a[160:163], a[132:135], a[16:19]\n" \
"ds_read_b64_tr_b16 a[186:187], v38 offset:33792\n" \
"v_mfma_f32_16x16x32_bf16 a[16:19], a[224:227], a[196:199], a[16:19]\n" \
"v_mfma_f32_16x16x32_bf16 a[32:35], a[160:163], a[136:139], a[32:35]\n" \
"s_add_u32 s88, s42, 0x3000\n" \
"buffer_store_dwordx4 v[224:227], v20, s[80:83], s88 offen offset:1024 sc1\n" \
"v_mfma_f32_16x16x32_bf16 a[32:35], a[224:227], a[200:203], a[32:35]\n" \
"ds_read_b64_tr_b16 a[248:249], v38 offset:40960\n" \
"v_mfma_f32_16x16x32_bf16 a[36:39], a[164:167], a[136:139], a[36:39]\n" \
"v_lshlrev_b32_e32 v45, 3, v20\n" \
"v_add_u32_e32 v45, s30, v45\n" \
"v_mfma_f32_16x16x32_bf16 a[36:39], a[228:231], a[200:203], a[36:39]\n" \
"s_getpc_b64 s[86:87]\n" \
"L_ct0_%=:\n" \
"s_add_u32 s86, s86, L_ap0-L_ct0_%=\n" \
"s_addc_u32 s87, s87, 0\n" \
"s_add_u32 m0, s40, 0x2000\n" \
"v_mfma_f32_16x16x32_bf16 a[44:47], a[172:175], a[136:139], a[44:47]\n" \
"global_load_lds_dword v45, s[86:87]\n" \
"v_mfma_f32_16x16x32_bf16 a[44:47], a[236:239], a[200:203], a[44:47]\n" \
"ds_read_b64_tr_b16 a[250:251], v38 offset:41984\n" \
"v_mfma_f32_16x16x32_bf16 a[40:43], a[136:139], a[168:171], a[40:43]\n" \
"ds_read_b64_tr_b16 a[188:189], v39 offset:32768\n" \
"v_mfma_f32_16x16x32_bf16 a[40:43], a[200:203], a[232:235], a[40:43]\n" \
"ds_read_b64_tr_b16 a[190:191], v39 offset:33792\n" \
"v_mfma_f32_16x16x32_bf16 a[56:59], a[140:143], a[168:171], a[56:59]\n" \
"ds_read_b64_tr_b16 a[252:253], v39 offset:40960\n" \
"v_mfma_f32_16x16x32_bf16 a[56:59], a[204:207], a[232:235], a[56:59]\n" \
"ds_read_b64_tr_b16 a[254:255], v39 offset:41984\n" \
"v_mfma_f32_16x16x32_bf16 a[60:63], a[172:175], a[140:143], a[60:63]\n" \
"v_mfma_f32_16x16x32_bf16 a[60:63], a[236:239], a[204:207], a[60:63]\n" \
"v_mfma_f32_16x16x32_bf16 a[52:55], a[164:167], a[140:143], a[52:55]\n" \
"v_mfma_f32_16x16x32_bf16 a[52:55], a[228:231], a[204:207], a[52:55]\n" \
"v_mfma_f32_16x16x32_bf16 a[48:51], a[160:163], a[140:143], a[48:51]\n" \
"v_mfma_f32_16x16x32_bf16 a[48:51], a[224:227], a[204:207], a[48:51]\n" \
"s_waitcnt lgkmcnt(15)\n" \
"v_mfma_f32_16x16x32_bf16 a[64:67], a[164:167], v[248:251], a[64:67]\n" \
"v_mfma_f32_16x16x32_bf16 a[64:67], a[228:231], v[48:51], a[64:67]\n" \
"v_mfma_f32_16x16x32_bf16 a[80:83], a[172:175], v[248:251], a[80:83]\n" \
"v_mfma_f32_16x16x32_bf16 a[80:83], a[236:239], v[48:51], a[80:83]\n" \
"v_mfma_f32_16x16x32_bf16 a[68:71], v[248:251], a[168:171], a[68:71]\n" \
"s_waitcnt vmcnt(3) lgkmcnt(0)\n" \
"v_mfma_f32_16x16x32_bf16 a[68:71], v[48:51], a[232:235], a[68:71]\n" \
"s_barrier\n" \
"s_nop 0\n" \
"v_xor_b32_e32 v28, 0x20000, v28\n" \
"v_xor_b32_e32 v29, 0x20000, v29\n" \
"v_mfma_f32_16x16x32_bf16 v[52:55], a[176:179], a[128:131], v[52:55]\n" \
"ds_read_b64_tr_b16 a[160:161], v32 offset:0\n" \
"v_accvgpr_read_b32 v184, a0\n" \
"v_mfma_f32_16x16x32_bf16 v[52:55], a[240:243], a[192:195], v[52:55]\n" \
"v_accvgpr_read_b32 v185, a1\n" \
"v_accvgpr_read_b32 v186, a2\n" \
"v_mfma_f32_16x16x32_bf16 v[56:59], a[180:183], a[128:131], v[56:59]\n" \
"ds_read_b64_tr_b16 a[162:163], v32 offset:512\n" \
"v_accvgpr_read_b32 v187, a3\n" \
"v_mfma_f32_16x16x32_bf16 v[56:59], a[244:247], a[192:195], v[56:59]\n" \
"v_accvgpr_read_b32 v188, a4\n" \
"v_accvgpr_read_b32 v189, a5\n" \
"v_mfma_f32_16x16x32_bf16 v[60:63], a[184:187], a[128:131], v[60:63]\n" \
"v_accvgpr_read_b32 v190, a6\n" \
"v_accvgpr_read_b32 v191, a7\n" \
"v_mfma_f32_16x16x32_bf16 v[60:63], a[248:251], a[192:195], v[60:63]\n" \
"ds_read_b64_tr_b16 a[224:225], v32 offset:4096\n" \
"v_accvgpr_read_b32 v196, a12\n" \
"v_mfma_f32_16x16x32_bf16 v[64:67], a[188:191], a[128:131], v[64:67]\n" \
"v_accvgpr_read_b32 v197, a13\n" \
"v_accvgpr_read_b32 v198, a14\n" \
"v_mfma_f32_16x16x32_bf16 v[64:67], a[252:255], a[192:195], v[64:67]\n" \
"ds_read_b128 a[128:131], v28 offset:0\n" \
"v_accvgpr_read_b32 v199, a15\n" \
"v_mfma_f32_16x16x32_bf16 v[80:83], a[188:191], a[132:135], v[80:83]\n" \
"ds_read_b128 a[192:195], v29 offset:0\n" \
"v_accvgpr_read_b32 v192, a8\n" \
"v_mfma_f32_16x16x32_bf16 v[80:83], a[252:255], a[196:199], v[80:83]\n" \
"ds_read_b64_tr_b16 a[226:227], v32 offset:4608\n" \
"v_accvgpr_read_b32 v193, a9\n" \
"v_mfma_f32_16x16x32_bf16 v[76:79], a[184:187], a[132:135], v[76:79]\n" \
"v_accvgpr_read_b32 v194, a10\n" \
"v_accvgpr_read_b32 v195, a11\n" \
"v_mfma_f32_16x16x32_bf16 v[76:79], a[248:251], a[196:199], v[76:79]\n" \
"ds_read_b64_tr_b16 a[164:165], v33 offset:0\n" \
"v_accvgpr_read_b32 v208, a24\n" \
"v_mfma_f32_16x16x32_bf16 v[72:75], a[180:183], a[132:135], v[72:75]\n" \
"v_accvgpr_read_b32 v209, a25\n" \
"v_accvgpr_read_b32 v210, a26\n" \
"v_mfma_f32_16x16x32_bf16 v[72:75], a[244:247], a[196:199], v[72:75]\n" \
"v_accvgpr_read_b32 v211, a27\n" \
"v_accvgpr_read_b32 v212, a28\n" \
"v_mfma_f32_16x16x32_bf16 v[68:71], a[176:179], a[132:135], v[68:71]\n" \
"ds_read_b64_tr_b16 a[166:167], v33 offset:512\n" \
"v_accvgpr_read_b32 v213, a29\n" \
"v_mfma_f32_16x16x32_bf16 v[68:71], a[240:243], a[196:199], v[68:71]\n" \
"ds_read_b128 a[132:135], v28 offset:2048\n" \
"v_accvgpr_read_b32 v214, a30\n" \
"v_mfma_f32_16x16x32_bf16 v[84:87], a[176:179], a[136:139], v[84:87]\n" \
"ds_read_b128 a[196:199], v29 offset:2048\n" \
"v_accvgpr_read_b32 v215, a31\n" \
"v_mfma_f32_16x16x32_bf16 v[84:87], a[240:243], a[200:203], v[84:87]\n" \
"ds_read_b64_tr_b16 a[228:229], v33 offset:4096\n" \
"v_accvgpr_read_b32 v204, a20\n" \
"v_mfma_f32_16x16x32_bf16 v[88:91], a[180:183], a[136:139], v[88:91]\n" \
"v_accvgpr_read_b32 v205, a21\n" \
"v_accvgpr_read_b32 v206, a22\n" \
"v_mfma_f32_16x16x32_bf16 v[88:91], a[244:247], a[200:203], v[88:91]\n" \
"v_accvgpr_read_b32 v207, a23\n" \
"v_accvgpr_read_b32 v200, a16\n" \
"v_mfma_f32_16x16x32_bf16 v[92:95], a[184:187], a[136:139], v[92:95]\n" \
"ds_read_b64_tr_b16 a[230:231], v33 offset:4608\n" \
"v_accvgpr_read_b32 v201, a17\n" \
"v_mfma_f32_16x16x32_bf16 v[92:95], a[248:251], a[200:203], v[92:95]\n" \
"v_accvgpr_read_b32 v202, a18\n" \
"v_accvgpr_read_b32 v203, a19\n" \
"v_mfma_f32_16x16x32_bf16 v[96:99], a[188:191], a[136:139], v[96:99]\n" \
"v_accvgpr_read_b32 v216, a32\n" \
"v_accvgpr_read_b32 v217, a33\n" \
"v_mfma_f32_16x16x32_bf16 v[96:99], a[252:255], a[200:203], v[96:99]\n" \
"ds_read_b128 a[136:139], v28 offset:4096\n" \
"v_accvgpr_read_b32 v218, a34\n" \
"v_mfma_f32_16x16x32_bf16 v[112:115], a[188:191], a[140:143], v[112:115]\n" \
"ds_read_b128 a[200:203], v29 offset:4096\n" \
"v_accvgpr_read_b32 v219, a35\n" \
"v_mfma_f32_16x16x32_bf16 v[112:115], a[252:255], a[204:207], v[112:115]\n" \
"ds_read_b64_tr_b16 a[172:173], v35 offset:0\n" \
"v_accvgpr_read_b32 v220, a36\n" \
"v_mfma_f32_16x16x32_bf16 v[108:111], a[184:187], a[140:143], v[108:111]\n" \
"ds_read_b64_tr_b16 a[174:175], v35 offset:512\n" \
"v_accvgpr_read_b32 v221, a37\n" \
"v_mfma_f32_16x16x32_bf16 v[108:111], a[248:251], a[204:207], v[108:111]\n" \
"v_accvgpr_read_b32 v222, a38\n" \
"v_accvgpr_read_b32 v223, a39\n" \
"v_mfma_f32_16x16x32_bf16 v[104:107], a[180:183], a[140:143], v[104:107]\n" \
"ds_read_b64_tr_b16 a[236:237], v35 offset:4096\n" \
"v_accvgpr_read_b32 v228, a44\n" \
"v_mfma_f32_16x16x32_bf16 v[104:107], a[244:247], a[204:207], v[104:107]\n" \
"v_accvgpr_read_b32 v229, a45\n" \
"v_accvgpr_read_b32 v230, a46\n" \
"v_mfma_f32_16x16x32_bf16 v[100:103], a[176:179], a[140:143], v[100:103]\n" \
"v_accvgpr_read_b32 v231, a47\n" \
"v_accvgpr_read_b32 v224, a40\n" \
"v_mfma_f32_16x16x32_bf16 v[100:103], a[240:243], a[204:207], v[100:103]\n" \
"ds_read_b128 a[140:143], v28 offset:6144\n" \
"v_accvgpr_read_b32 v225, a41\n" \
"v_mfma_f32_16x16x32_bf16 v[116:119], a[176:179], a[144:147], v[116:119]\n" \
"ds_read_b128 a[204:207], v29 offset:6144\n" \
"v_accvgpr_read_b32 v226, a42\n" \
"v_mfma_f32_16x16x32_bf16 v[116:119], a[240:243], a[208:211], v[116:119]\n" \
"ds_read_b64_tr_b16 a[238:239], v35 offset:4608\n" \
"v_accvgpr_read_b32 v227, a43\n" \
"v_mfma_f32_16x16x32_bf16 v[120:123], a[180:183], a[144:147], v[120:123]\n" \
"ds_read_b64_tr_b16 a[168:169], v34 offset:0\n" \
"v_accvgpr_read_b32 v240, a56\n" \
"v_mfma_f32_16x16x32_bf16 v[120:123], a[244:247], a[208:211], v[120:123]\n" \
"v_accvgpr_read_b32 v241, a57\n" \
"v_accvgpr_read_b32 v242, a58\n" \
"v_mfma_f32_16x16x32_bf16 v[124:127], a[184:187], a[144:147], v[124:127]\n" \
"v_accvgpr_read_b32 v243, a59\n" \
"v_accvgpr_read_b32 v244, a60\n" \
"v_mfma_f32_16x16x32_bf16 v[124:127], a[248:251], a[208:211], v[124:127]\n" \
"ds_read_b64_tr_b16 a[170:171], v34 offset:512\n" \
"v_accvgpr_read_b32 v245, a61\n" \
"v_mfma_f32_16x16x32_bf16 v[128:131], a[188:191], a[144:147], v[128:131]\n" \
"v_accvgpr_read_b32 v246, a62\n" \
"v_accvgpr_read_b32 v247, a63\n" \
"v_mfma_f32_16x16x32_bf16 v[128:131], a[252:255], a[208:211], v[128:131]\n" \
"ds_read_b128 a[144:147], v28 offset:8192\n" \
"v_accvgpr_read_b32 v236, a52\n" \
"v_mfma_f32_16x16x32_bf16 v[144:147], a[188:191], a[148:151], v[144:147]\n" \
"ds_read_b128 a[208:211], v29 offset:8192\n" \
"v_accvgpr_read_b32 v237, a53\n" \
"v_mfma_f32_16x16x32_bf16 v[144:147], a[252:255], a[212:215], v[144:147]\n" \
"ds_read_b64_tr_b16 a[232:233], v34 offset:4096\n" \
"v_accvgpr_read_b32 v238, a54\n" \
"v_mfma_f32_16x16x32_bf16 v[140:143], a[184:187], a[148:151], v[140:143]\n" \
"v_accvgpr_read_b32 v239, a55\n" \
"v_accvgpr_read_b32 v232, a48\n" \
"v_mfma_f32_16x16x32_bf16 v[140:143], a[248:251], a[212:215], v[140:143]\n" \
"ds_read_b64_tr_b16 a[234:235], v34 offset:4608\n" \
"v_accvgpr_read_b32 v233, a49\n" \
"v_mfma_f32_16x16x32_bf16 v[136:139], a[180:183], a[148:151], v[136:139]\n" \
"v_accvgpr_read_b32 v234, a50\n" \
"v_accvgpr_read_b32 v235, a51\n" \
"v_mfma_f32_16x16x32_bf16 v[136:139], a[244:247], a[212:215], v[136:139]\n" \
"v_accvgpr_read_b32 v180, a64\n" \
"v_accvgpr_read_b32 v181, a65\n" \
"v_mfma_f32_16x16x32_bf16 v[132:135], a[176:179], a[148:151], v[132:135]\n" \
"ds_read_b128 v[248:251], v22 offset:0\n" \
"v_accvgpr_read_b32 v182, a66\n" \
"v_mfma_f32_16x16x32_bf16 v[132:135], a[240:243], a[212:215], v[132:135]\n" \
"ds_read_b128 a[148:151], v28 offset:10240\n" \
"v_accvgpr_read_b32 v183, a67\n" \
"v_mfma_f32_16x16x32_bf16 v[148:151], a[176:179], a[152:155], v[148:151]\n" \
"ds_read_b128 a[212:215], v29 offset:10240\n" \
"v_accvgpr_read_b32 v252, a68\n" \
"v_mfma_f32_16x16x32_bf16 v[148:151], a[240:243], a[216:219], v[148:151]\n" \
"ds_read_b128 v[48:51], v23 offset:0\n" \
"v_accvgpr_read_b32 v253, a69\n" \
"v_mfma_f32_16x16x32_bf16 v[152:155], a[180:183], a[152:155], v[152:155]\n" \
"v_accvgpr_read_b32 v254, a70\n" \
"v_accvgpr_read_b32 v255, a71\n" \
"v_mfma_f32_16x16x32_bf16 v[152:155], a[244:247], a[216:219], v[152:155]\n" \
"v_lshrrev_b32_e64 v24, 9, v20\n" \
"v_and_b32_e32 v26, 0x1ff, v20\n" \
"v_mfma_f32_16x16x32_bf16 v[156:159], a[184:187], a[152:155], v[156:159]\n" \
"v_lshl_add_u32 v24, v24, 3, v26\n" \
"v_lshrrev_b32_e64 v25, 8, v20\n" \
"v_mfma_f32_16x16x32_bf16 v[156:159], a[248:251], a[216:219], v[156:159]\n" \
"v_mul_u32_u24_e32 v25, 0x24700, v25\n" \
"v_add_u32_e64 v25, v25, v20\n" \
"v_mfma_f32_16x16x32_bf16 v[160:163], a[188:191], a[152:155], v[160:163]\n" \
"v_lshrrev_b32_e32 v26, 2, v20\n" \
"v_add_u32_e32 v26, s45, v26\n" \
"v_mfma_f32_16x16x32_bf16 v[160:163], a[252:255], a[216:219], v[160:163]\n" \
"ds_read_b128 a[152:155], v28 offset:12288\n" \
"v_add_u32_e64 v27, s46, v20\n" \
"v_mfma_f32_16x16x32_bf16 v[176:179], a[188:191], a[156:159], v[176:179]\n" \
"ds_read_b128 a[216:219], v29 offset:12288\n" \
"v_add_u32_e64 v21, s43, v27\n" \
"v_mfma_f32_16x16x32_bf16 v[176:179], a[252:255], a[220:223], v[176:179]\n" \
"v_mfma_f32_16x16x32_bf16 v[172:175], a[184:187], a[156:159], v[172:175]\n" \
"v_mfma_f32_16x16x32_bf16 v[172:175], a[248:251], a[220:223], v[172:175]\n" \
"v_mfma_f32_16x16x32_bf16 v[168:171], a[180:183], a[156:159], v[168:171]\n" \
"v_mfma_f32_16x16x32_bf16 v[168:171], a[244:247], a[220:223], v[168:171]\n" \
"v_mfma_f32_16x16x32_bf16 v[164:167], a[176:179], a[156:159], v[164:167]\n" \
"v_mfma_f32_16x16x32_bf16 v[164:167], a[240:243], a[220:223], v[164:167]\n" \
"ds_read_b128 a[156:159], v28 offset:14336\n" \
"ds_read_b128 a[220:223], v29 offset:14336\n" \
"s_waitcnt lgkmcnt(15)\n" \
"s_nop 0\n" \
"v_mfma_f32_16x16x32_bf16 v[184:187], a[160:163], a[128:131], v[184:187]\n" \
"v_bfe_u32 v44, v20, 2, 5\n" \
"ds_read_b64_tr_b16 a[176:177], v36 offset:0\n" \
"v_mfma_f32_16x16x32_bf16 v[184:187], a[224:227], a[192:195], v[184:187]\n" \
"buffer_load_dword v44, v44, s[80:83], s38 offen offset:0 sc1\n" \
"v_mfma_f32_16x16x32_bf16 v[188:191], a[164:167], a[128:131], v[188:191]\n" \
"ds_read_b64_tr_b16 a[178:179], v36 offset:1024\n" \
"v_mfma_f32_16x16x32_bf16 v[188:191], a[228:231], a[192:195], v[188:191]\n" \
"ds_read_b64_tr_b16 a[240:241], v36 offset:8192\n" \
"v_mfma_f32_16x16x32_bf16 v[196:199], a[172:175], a[128:131], v[196:199]\n" \
"ds_read_b64_tr_b16 a[242:243], v36 offset:9216\n" \
"s_waitcnt lgkmcnt(15)\n" \
"v_mfma_f32_16x16x32_bf16 v[196:199], a[236:239], a[192:195], v[196:199]\n" \
"v_mfma_f32_16x16x32_bf16 v[192:195], a[128:131], a[168:171], v[192:195]\n" \
"ds_read_b64_tr_b16 a[180:181], v37 offset:0\n" \
"s_waitcnt lgkmcnt(13)\n" \
"v_mfma_f32_16x16x32_bf16 v[192:195], a[192:195], a[232:235], v[192:195]\n" \
"v_mfma_f32_16x16x32_bf16 v[208:211], a[132:135], a[168:171], v[208:211]\n" \
"ds_read_b64_tr_b16 a[182:183], v37 offset:1024\n" \
"v_mfma_f32_16x16x32_bf16 v[208:211], a[196:199], a[232:235], v[208:211]\n" \
"ds_read_b64_tr_b16 a[244:245], v37 offset:8192\n" \
"v_mfma_f32_16x16x32_bf16 v[212:215], a[172:175], a[132:135], v[212:215]\n" \
"v_mfma_f32_16x16x32_bf16 v[212:215], a[236:239], a[196:199], v[212:215]\n" \
"ds_read_b64_tr_b16 a[246:247], v37 offset:9216\n" \
"v_cvt_pk_bf16_f32 v188, v188, v189\n" \
"v_mfma_f32_16x16x32_bf16 v[204:207], a[164:167], a[132:135], v[204:207]\n" \
"v_cvt_pk_bf16_f32 v189, v190, v191\n" \
"v_cvt_pk_bf16_f32 v190, v198, v199\n" \
"v_mfma_f32_16x16x32_bf16 v[204:207], a[228:231], a[196:199], v[204:207]\n" \
"ds_read_b64_tr_b16 a[184:185], v38 offset:0\n" \
"v_mov_b32_e32 v191, 0x3f80\n" \
"v_mfma_f32_16x16x32_bf16 v[200:203], a[160:163], a[132:135], v[200:203]\n" \
"ds_read_b64_tr_b16 a[186:187], v38 offset:1024\n" \
"v_cvt_pk_bf16_f32 v184, v184, v185\n" \
"v_mfma_f32_16x16x32_bf16 v[200:203], a[224:227], a[196:199], v[200:203]\n" \
"v_cvt_pk_bf16_f32 v185, v186, v187\n" \
"v_cvt_pk_bf16_f32 v186, v196, v197\n" \
"v_mfma_f32_16x16x32_bf16 v[216:219], a[160:163], a[136:139], v[216:219]\n" \
"ds_read_b64_tr_b16 a[248:249], v38 offset:8192\n" \
"v_mov_b32_e64 v187, 0\n" \
"v_mfma_f32_16x16x32_bf16 v[216:219], a[224:227], a[200:203], v[216:219]\n" \
"ds_read_b64_tr_b16 a[250:251], v38 offset:9216\n" \
"v_mfma_f32_16x16x32_bf16 v[220:223], a[164:167], a[136:139], v[220:223]\n" \
"v_mfma_f32_16x16x32_bf16 v[220:223], a[228:231], a[200:203], v[220:223]\n" \
"ds_read_b64_tr_b16 a[188:189], v39 offset:0\n" \
"v_cvt_pk_bf16_f32 v204, v204, v205\n" \
"v_mfma_f32_16x16x32_bf16 v[228:231], a[172:175], a[136:139], v[228:231]\n" \
"v_cvt_pk_bf16_f32 v205, v206, v207\n" \
"v_cvt_pk_bf16_f32 v206, v214, v215\n" \
"v_mfma_f32_16x16x32_bf16 v[228:231], a[236:239], a[200:203], v[228:231]\n" \
"ds_read_b64_tr_b16 a[190:191], v39 offset:1024\n" \
"v_mov_b32_e32 v207, 0x3f80\n" \
"v_mfma_f32_16x16x32_bf16 v[224:227], a[136:139], a[168:171], v[224:227]\n" \
"ds_read_b64_tr_b16 a[252:253], v39 offset:8192\n" \
"v_cvt_pk_bf16_f32 v200, v200, v201\n" \
"v_mfma_f32_16x16x32_bf16 v[224:227], a[200:203], a[232:235], v[224:227]\n" \
"v_cvt_pk_bf16_f32 v201, v202, v203\n" \
"v_cvt_pk_bf16_f32 v202, v212, v213\n" \
"v_mfma_f32_16x16x32_bf16 v[240:243], a[140:143], a[168:171], v[240:243]\n" \
"ds_read_b64_tr_b16 a[254:255], v39 offset:9216\n" \
"v_mov_b32_e64 v203, 0\n" \
"v_mfma_f32_16x16x32_bf16 v[240:243], a[204:207], a[232:235], v[240:243]\n" \
"v_mfma_f32_16x16x32_bf16 v[244:247], a[172:175], a[140:143], v[244:247]\n" \
"v_mfma_f32_16x16x32_bf16 v[244:247], a[236:239], a[204:207], v[244:247]\n" \
"v_cvt_pk_bf16_f32 v220, v220, v221\n" \
"v_cvt_pk_bf16_f32 v221, v222, v223\n" \
"v_mfma_f32_16x16x32_bf16 v[236:239], a[164:167], a[140:143], v[236:239]\n" \
"v_cvt_pk_bf16_f32 v222, v230, v231\n" \
"v_mov_b32_e32 v223, 0x3f80\n" \
"v_mfma_f32_16x16x32_bf16 v[236:239], a[228:231], a[204:207], v[236:239]\n" \
"v_cvt_pk_bf16_f32 v216, v216, v217\n" \
"v_cvt_pk_bf16_f32 v217, v218, v219\n" \
"v_mfma_f32_16x16x32_bf16 v[232:235], a[160:163], a[140:143], v[232:235]\n" \
"v_cvt_pk_bf16_f32 v218, v228, v229\n" \
"v_mov_b32_e64 v219, 0\n" \
"v_mfma_f32_16x16x32_bf16 v[232:235], a[224:227], a[204:207], v[232:235]\n" \
"s_waitcnt lgkmcnt(15)\n" \
"s_nop 0\n" \
"v_mfma_f32_16x16x32_bf16 v[180:183], a[164:167], v[248:251], v[180:183]\n" \
"v_mfma_f32_16x16x32_bf16 v[180:183], a[228:231], v[48:51], v[180:183]\n" \
"v_mfma_f32_16x16x32_bf16 a[80:83], a[172:175], v[248:251], a[80:83]\n" \
"v_mfma_f32_16x16x32_bf16 a[80:83], a[236:239], v[48:51], a[80:83]\n" \
"v_cvt_pk_bf16_f32 v236, v236, v237\n" \
"v_cvt_pk_bf16_f32 v237, v238, v239\n" \
"v_mfma_f32_16x16x32_bf16 v[252:255], v[248:251], a[168:171], v[252:255]\n" \
"v_cvt_pk_bf16_f32 v238, v246, v247\n" \
"v_mov_b32_e32 v239, 0x3f80\n" \
"v_mfma_f32_16x16x32_bf16 v[252:255], v[48:51], a[232:235], v[252:255]\n" \
"v_cvt_pk_bf16_f32 v232, v232, v233\n" \
"v_cvt_pk_bf16_f32 v233, v234, v235\n" \
"s_waitcnt vmcnt(0) lgkmcnt(0)\n" \
"s_barrier\n" \
"v_mov_b32_e32 v45, s62\n" \
"s_mov_b64 exec, 1\n" \
"buffer_store_dword v45, off, s[80:83], s37 offset:32 sc1\n" \
"s_mov_b64 exec, -1\n" \
"v_cmp_ne_u32_e32 vcc, s62, v44\n" \
"s_nop 4\n" \
"s_cbranch_vccnz L_poll160_%=\n" \
"L_pollr160_%=:\n" \
"v_mfma_f32_16x16x32_bf16 v[52:55], a[176:179], a[128:131], v[52:55]\n" \
"s_mov_b32 s73, s50\n" \
"s_sub_u32 m0, s31, 0x10000\n" \
"v_mfma_f32_16x16x32_bf16 v[52:55], a[240:243], a[192:195], v[52:55]\n" \
"buffer_load_dwordx4 v20, s[80:83], s73 offen offset:0 sc1 lds\n" \
"v_mfma_f32_16x16x32_bf16 v[56:59], a[180:183], a[128:131], v[56:59]\n" \
"s_add_u32 s74, s50, 0x4000\n" \
"s_add_u32 s75, s50, 0x8000\n" \
"v_mfma_f32_16x16x32_bf16 v[56:59], a[244:247], a[192:195], v[56:59]\n" \
"s_mov_b32 s70, 0x3e0293ee\n" \
"s_mov_b32 s76, s52\n" \
"v_mfma_f32_16x16x32_bf16 v[60:63], a[184:187], a[128:131], v[60:63]\n" \
"v_mul_f32_e32 v52, s70, v52\n" \
"v_mul_f32_e32 v53, s70, v53\n" \
"v_mfma_f32_16x16x32_bf16 v[60:63], a[248:251], a[192:195], v[60:63]\n" \
"buffer_load_dwordx4 v20, s[80:83], s73 offen offset:1024 sc1 lds\n" \
".fill (L_pb + 36576 - .) / 4, 4, 0xBF800000\n" \
"L_pp9:\n" \
"s_cbranch_execz L_pret\n" \
"v_mfma_f32_16x16x32_bf16 v[64:67], a[188:191], a[128:131], v[64:67]\n" \
"v_mul_f32_e32 v54, s70, v54\n" \
"v_mul_f32_e64 v55, s70, v55\n" \
"v_mfma_f32_16x16x32_bf16 v[64:67], a[252:255], a[192:195], v[64:67]\n" \
"v_cvt_pk_bf16_f32 v52, v52, v53\n" \
"v_mul_f32_e64 v56, s70, v56\n" \
"v_mfma_f32_16x16x32_bf16 v[80:83], a[188:191], a[132:135], v[80:83]\n" \
"v_mul_f32_e32 v60, s70, v60\n" \
"v_mul_f32_e32 v61, s70, v61\n" \
"v_mfma_f32_16x16x32_bf16 v[80:83], a[252:255], a[196:199], v[80:83]\n" \
"buffer_load_dwordx4 v20, s[80:83], s73 offen offset:2048 sc1 lds\n" \
"v_mfma_f32_16x16x32_bf16 v[76:79], a[184:187], a[132:135], v[76:79]\n" \
"v_mul_f32_e32 v62, s70, v62\n" \
"v_mul_f32_e32 v63, s70, v63\n" \
"v_mfma_f32_16x16x32_bf16 v[76:79], a[248:251], a[196:199], v[76:79]\n" \
"v_cvt_pk_bf16_f32 v60, v60, v61\n" \
"v_mul_f32_e64 v57, s70, v57\n" \
"v_mfma_f32_16x16x32_bf16 v[72:75], a[180:183], a[132:135], v[72:75]\n" \
"v_mul_f32_e32 v58, s70, v58\n" \
"v_mul_f32_e32 v59, s70, v59\n" \
"v_mfma_f32_16x16x32_bf16 v[72:75], a[244:247], a[196:199], v[72:75]\n" \
"buffer_load_dwordx4 v20, s[80:83], s73 offen offset:3072 sc1 lds\n" \
"v_mfma_f32_16x16x32_bf16 v[68:71], a[176:179], a[132:135], v[68:71]\n" \
"s_sub_u32 m0, s31, s51\n" \
"v_cvt_pk_bf16_f32 v53, v54, v55\n" \
"v_mfma_f32_16x16x32_bf16 v[68:71], a[240:243], a[196:199], v[68:71]\n" \
"v_mul_f32_e32 v64, s70, v64\n" \
"v_mul_f32_e64 v65, s70, v65\n" \
"v_mfma_f32_16x16x32_bf16 v[84:87], a[176:179], a[136:139], v[84:87]\n" \
"v_mul_f32_e32 v66, s70, v66\n" \
"v_mul_f32_e32 v67, s70, v67\n" \
"v_mfma_f32_16x16x32_bf16 v[84:87], a[240:243], a[200:203], v[84:87]\n" \
"buffer_load_dwordx4 v20, s[80:83], s74 offen offset:0 sc1 lds\n" \
"v_mfma_f32_16x16x32_bf16 v[88:91], a[180:183], a[136:139], v[88:91]\n" \
"v_cvt_pk_bf16_f32 v61, v62, v63\n" \
"v_cvt_pk_bf16_f32 v54, v56, v57\n" \
"v_mfma_f32_16x16x32_bf16 v[88:91], a[244:247], a[200:203], v[88:91]\n" \
"v_cvt_pk_bf16_f32 v55, v58, v59\n" \
"v_cvt_pk_bf16_f32 v62, v64, v65\n" \
"v_mfma_f32_16x16x32_bf16 v[92:95], a[184:187], a[136:139], v[92:95]\n" \
"v_cvt_pk_bf16_f32 v63, v66, v67\n" \
"v_mul_f32_e64 v68, s70, v68\n" \
"v_mfma_f32_16x16x32_bf16 v[92:95], a[248:251], a[200:203], v[92:95]\n" \
"buffer_load_dwordx4 v20, s[80:83], s74 offen offset:1024 sc1 lds\n" \
"v_mfma_f32_16x16x32_bf16 v[96:99], a[188:191], a[136:139], v[96:99]\n" \
"ds_write_b128 v27, v[52:55] offset:0\n" \
"v_mfma_f32_16x16x32_bf16 v[96:99], a[252:255], a[200:203], v[96:99]\n" \
"v_mul_f32_e32 v69, s70, v69\n" \
"v_mul_f32_e32 v76, s70, v76\n" \
"v_mfma_f32_16x16x32_bf16 v[112:115], a[188:191], a[140:143], v[112:115]\n" \
"v_mul_f32_e32 v77, s70, v77\n" \
"v_mul_f32_e32 v70, s70, v70\n" \
"v_mfma_f32_16x16x32_bf16 v[112:115], a[252:255], a[204:207], v[112:115]\n" \
"buffer_load_dwordx4 v20, s[80:83], s74 offen offset:2048 sc1 lds\n" \
"v_mfma_f32_16x16x32_bf16 v[108:111], a[184:187], a[140:143], v[108:111]\n" \
"ds_write_b128 v27, v[60:63] offset:1024\n" \
"v_mfma_f32_16x16x32_bf16 v[108:111], a[248:251], a[204:207], v[108:111]\n" \
"v_mul_f32_e64 v71, s70, v71\n" \
"v_cvt_pk_bf16_f32 v68, v68, v69\n" \
"v_mfma_f32_16x16x32_bf16 v[104:107], a[180:183], a[140:143], v[104:107]\n" \
"v_mul_f32_e32 v78, s70, v78\n" \
"v_mul_f32_e32 v79, s70, v79\n" \
"v_mfma_f32_16x16x32_bf16 v[104:107], a[244:247], a[204:207], v[104:107]\n" \
"buffer_load_dwordx4 v20, s[80:83], s74 offen offset:3072 sc1 lds\n" \
"v_mfma_f32_16x16x32_bf16 v[100:103], a[176:179], a[140:143], v[100:103]\n" \
"s_sub_u32 m0, s31, 0x4000\n" \
"v_cvt_pk_bf16_f32 v76, v76, v77\n" \
"v_mfma_f32_16x16x32_bf16 v[100:103], a[240:243], a[204:207], v[100:103]\n" \
"v_mul_f32_e32 v72, s70, v72\n" \
"v_mul_f32_e32 v73, s70, v73\n" \
"v_mfma_f32_16x16x32_bf16 v[116:119], a[176:179], a[144:147], v[116:119]\n" \
"v_mul_f32_e32 v74, s70, v74\n" \
"v_mul_f32_e32 v75, s70, v75\n" \
"v_mfma_f32_16x16x32_bf16 v[116:119], a[240:243], a[208:211], v[116:119]\n" \
"buffer_load_dwordx4 v20, s[80:83], s75 offen offset:0 sc1 lds\n" \
"v_mfma_f32_16x16x32_bf16 v[120:123], a[180:183], a[144:147], v[120:123]\n" \
"v_cvt_pk_bf16_f32 v69, v70, v71\n" \
"v_mul_f32_e64 v80, s70, v80\n" \
"v_mfma_f32_16x16x32_bf16 v[120:123], a[244:247], a[208:211], v[120:123]\n" \
"v_mul_f32_e32 v81, s70, v81\n" \
"v_mul_f32_e32 v82, s70, v82\n" \
"v_mfma_f32_16x16x32_bf16 v[124:127], a[184:187], a[144:147], v[124:127]\n" \
"v_mul_f32_e64 v83, s70, v83\n" \
"v_cvt_pk_bf16_f32 v77, v78, v79\n" \
"v_mfma_f32_16x16x32_bf16 v[124:127], a[248:251], a[208:211], v[124:127]\n" \
"buffer_load_dwordx4 v20, s[80:83], s75 offen offset:1024 sc1 lds\n" \
"v_mfma_f32_16x16x32_bf16 v[128:131], a[188:191], a[144:147], v[128:131]\n" \
"v_cvt_pk_bf16_f32 v70, v72, v73\n" \
"v_cvt_pk_bf16_f32 v71, v74, v75\n" \
"v_mfma_f32_16x16x32_bf16 v[128:131], a[252:255], a[208:211], v[128:131]\n" \
"v_cvt_pk_bf16_f32 v78, v80, v81\n" \
"v_cvt_pk_bf16_f32 v79, v82, v83\n" \
"v_mfma_f32_16x16x32_bf16 v[144:147], a[188:191], a[148:151], v[144:147]\n" \
"ds_write_b128 v27, v[68:71] offset:4096\n" \
"v_mfma_f32_16x16x32_bf16 v[144:147], a[252:255], a[212:215], v[144:147]\n" \
"buffer_load_dwordx4 v20, s[80:83], s75 offen offset:2048 sc1 lds\n" \
"v_mfma_f32_16x16x32_bf16 v[140:143], a[184:187], a[148:151], v[140:143]\n" \
"v_cvt_pk_bf16_f32 v180, v180, v181\n" \
"v_accvgpr_read_b32 v50, a82\n" \
"v_mfma_f32_16x16x32_bf16 v[140:143], a[248:251], a[212:215], v[140:143]\n" \
"ds_write_b128 v27, v[76:79] offset:5120\n" \
"v_mfma_f32_16x16x32_bf16 v[136:139], a[180:183], a[148:151], v[136:139]\n" \
"v_accvgpr_read_b32 v51, a83\n" \
"v_cvt_pk_bf16_f32 v181, v182, v183\n" \
"v_mfma_f32_16x16x32_bf16 v[136:139], a[244:247], a[212:215], v[136:139]\n" \
"buffer_load_dwordx4 v20, s[80:83], s75 offen offset:3072 sc1 lds\n" \
"v_mfma_f32_16x16x32_bf16 v[132:135], a[176:179], a[148:151], v[132:135]\n" \
"s_mov_b32 m0, s53\n" \
"v_cvt_pk_bf16_f32 v182, v50, v51\n" \
"v_mfma_f32_16x16x32_bf16 v[132:135], a[240:243], a[212:215], v[132:135]\n" \
"v_mov_b32_e32 v183, 0x3f80\n" \
"v_cvt_pk_bf16_f32 v234, v244, v245\n" \
"v_mfma_f32_16x16x32_bf16 v[148:151], a[176:179], a[152:155], v[148:151]\n" \
"v_mov_b32_e32 v235, 0\n" \
"v_cvt_pk_bf16_f32 v54, v192, v193\n" \
"v_mfma_f32_16x16x32_bf16 v[148:151], a[240:243], a[216:219], v[148:151]\n" \
"buffer_load_dwordx4 v20, s[80:83], s76 offen offset:0 sc1 lds\n" \
"v_mfma_f32_16x16x32_bf16 v[152:155], a[180:183], a[152:155], v[152:155]\n" \
"v_cvt_pk_bf16_f32 v55, v194, v195\n" \
"v_cvt_pk_bf16_f32 v56, v208, v209\n" \
"v_mfma_f32_16x16x32_bf16 v[152:155], a[244:247], a[216:219], v[152:155]\n" \
"v_cvt_pk_bf16_f32 v57, v210, v211\n" \
"v_cvt_pk_bf16_f32 v58, v224, v225\n" \
"v_cvt_pk_bf16_f32 v59, v226, v227\n" \
"v_cvt_pk_bf16_f32 v60, v240, v241\n" \
"v_cvt_pk_bf16_f32 v61, v242, v243\n" \
"v_mfma_f32_16x16x32_bf16 v[32:35], v[180:183], v[184:187], 0\n" \
"buffer_load_dwordx4 v20, s[80:83], s76 offen offset:1024 sc1 lds\n" \
"v_mfma_f32_16x16x32_bf16 v[156:159], a[184:187], a[152:155], v[156:159]\n" \
"v_mfma_f32_16x16x32_bf16 v[192:195], v[188:191], v[184:187], 0\n" \
"v_mfma_f32_16x16x32_bf16 v[208:211], v[204:207], v[200:203], 0\n" \
"v_mfma_f32_16x16x32_bf16 v[36:39], v[188:191], v[200:203], 0\n" \
"v_mfma_f32_16x16x32_bf16 v[224:227], v[220:223], v[216:219], 0\n" \
"v_mfma_f32_16x16x32_bf16 v[248:251], v[204:207], v[216:219], 0\n" \
"v_mfma_f32_16x16x32_bf16 v[240:243], v[236:239], v[232:235], 0\n" \
"v_mfma_f32_16x16x32_bf16 v[64:67], v[220:223], v[232:235], 0\n" \
"s_mov_b32 s64, 0xaaaaaaaa\n" \
"s_mov_b32 s65, 0xaaaaaaaa\n" \
"v_mfma_f32_16x16x32_bf16 v[156:159], a[248:251], a[216:219], v[156:159]\n" \
"v_cndmask_b32_e64 v32, v32, v33, s[64:65]\n" \
"v_cndmask_b32_e64 v34, v34, v35, s[64:65]\n" \
"v_mfma_f32_16x16x32_bf16 v[160:163], a[188:191], a[152:155], v[160:163]\n" \
"s_mov_b32 s66, 0xfff0ffff\n" \
"s_mov_b32 s67, 0xf000ff00\n" \
"s_mov_b32 s68, 0xffc0fffc\n" \
"s_mov_b32 s69, 0xc000fc00\n" \
"v_cndmask_b32_e64 v192, v192, v193, s[64:65]\n" \
"v_add_f32_e64 v32, s47, v32\n" \
"v_cndmask_b32_e64 v194, v194, v195, s[64:65]\n" \
"v_add_f32_e64 v34, s47, v34\n" \
"v_cndmask_b32_e64 v208, v208, v209, s[64:65]\n" \
"v_cndmask_b32_e64 v36, v36, v37, s[64:65]\n" \
"v_cndmask_b32_e64 v210, v210, v211, s[64:65]\n" \
"v_cndmask_b32_e64 v38, v38, v39, s[64:65]\n" \
"v_cndmask_b32_e64 v224, v224, v225, s[64:65]\n" \
"v_cndmask_b32_e64 v248, v248, v249, s[64:65]\n" \
"v_cndmask_b32_e64 v226, v226, v227, s[64:65]\n" \
"v_cndmask_b32_e64 v250, v250, v251, s[64:65]\n" \
"v_cndmask_b32_e64 v240, v240, v241, s[64:65]\n" \
"v_cndmask_b32_e64 v64, v64, v65, s[64:65]\n" \
"v_cndmask_b32_e64 v242, v242, v243, s[64:65]\n" \
"v_cndmask_b32_e64 v66, v66, v67, s[64:65]\n" \
"v_cndmask_b32_e64 v192, v32, v192, s[66:67]\n" \
"v_cndmask_b32_e64 v194, v34, v194, s[68:69]\n" \
"v_cndmask_b32_e64 v208, v36, v208, s[66:67]\n" \
"v_cndmask_b32_e64 v210, v38, v210, s[68:69]\n" \
"v_cndmask_b32_e64 v224, v248, v224, s[66:67]\n" \
"v_cndmask_b32_e64 v226, v250, v226, s[68:69]\n" \
"v_cndmask_b32_e64 v240, v64, v240, s[66:67]\n" \
"v_cndmask_b32_e64 v242, v66, v242, s[68:69]\n" \
"v_mfma_f32_16x16x32_bf16 v[160:163], a[252:255], a[216:219], v[160:163]\n" \
"v_max_f32_e32 v193, v192, v194\n" \
"v_max_f32_e32 v209, v208, v210\n" \
"v_max_f32_e32 v225, v224, v226\n" \
"v_max_f32_e32 v241, v240, v242\n" \
"v_mul_f32_e32 v84, s70, v84\n" \
"v_permlane32_swap_b32_e32 v193, v225\n" \
"v_permlane32_swap_b32_e32 v209, v241\n" \
"v_mul_f32_e32 v85, s70, v85\n" \
"v_mul_f32_e32 v92, s70, v92\n" \
"v_mul_f32_e32 v93, s70, v93\n" \
"v_permlane32_swap_b32_e32 v196, v228\n" \
"v_permlane32_swap_b32_e32 v212, v244\n" \
"v_max_f32_e32 v193, v193, v225\n" \
"v_max_f32_e32 v209, v209, v241\n" \
"v_accvgpr_read_b32 v48, a80\n" \
"v_accvgpr_read_b32 v49, a81\n" \
"v_mul_f32_e32 v86, s70, v86\n" \
"v_mul_f32_e32 v87, s70, v87\n" \
"v_cvt_pk_bf16_f32 v84, v84, v85\n" \
"v_mul_f32_e32 v94, s70, v94\n" \
"v_mul_f32_e32 v95, s70, v95\n" \
"v_cvt_pk_bf16_f32 v92, v92, v93\n" \
"v_permlane16_swap_b32_e32 v228, v244\n" \
"v_permlane16_swap_b32_e32 v193, v209\n" \
"v_mfma_f32_16x16x32_bf16 v[176:179], a[188:191], a[156:159], v[176:179]\n" \
"s_mov_b32 s72, 0x3eae1a93\n" \
"v_mul_f32_e32 v88, s70, v88\n" \
"v_mul_f32_e32 v89, s70, v89\n" \
"v_mul_f32_e32 v90, s70, v90\n" \
"v_mul_f32_e32 v91, s70, v91\n" \
"v_cvt_pk_bf16_f32 v85, v86, v87\n" \
"v_mul_f32_e32 v96, s70, v96\n" \
"v_mul_f32_e32 v97, s70, v97\n" \
"v_mul_f32_e32 v98, s70, v98\n" \
"v_mul_f32_e32 v99, s70, v99\n" \
"v_cvt_pk_bf16_f32 v93, v94, v95\n" \
"v_mul_f32_e32 v45, 0xbfb8aa3b, v228\n" \
"v_mfma_f32_16x16x16_bf16 v[48:51], v[182:183], v[46:47], 0\n" \
"v_mfma_f32_16x16x16_bf16 v[196:199], v[190:191], v[46:47], 0\n" \
"v_mfma_f32_16x16x16_bf16 v[212:215], v[206:207], v[46:47], 0\n" \
"v_max_f32_e64 v193, v193, v209\n" \
"v_cvt_pk_bf16_f32 v86, v88, v89\n" \
"v_cvt_pk_bf16_f32 v87, v90, v91\n" \
"v_cvt_pk_bf16_f32 v94, v96, v97\n" \
"v_cvt_pk_bf16_f32 v95, v98, v99\n" \
"v_mfma_f32_16x16x16_bf16 v[228:231], v[222:223], v[46:47], 0\n" \
"v_mul_f32_e32 v193, s72, v193\n" \
"v_mov_b32_e32 v209, v193\n" \
"ds_write_b128 v27, v[84:87] offset:8192\n" \
"v_mfma_f32_16x16x16_bf16 v[244:247], v[238:239], v[46:47], 0\n" \
"v_permlane16_swap_b32_e32 v193, v209\n" \
"s_mov_b32 s71, 0x3e0293ee\n" \
"v_mfma_f32_16x16x32_bf16 v[176:179], a[252:255], a[220:223], v[176:179]\n" \
"v_mov_b32_e32 v225, v193\n" \
"v_mov_b32_e32 v241, v209\n" \
"v_mul_f32_e32 v100, s70, v100\n" \
"v_mul_f32_e64 v101, s70, v101\n" \
"ds_write_b128 v27, v[92:95] offset:9216\n" \
"v_mul_f32_e32 v108, s70, v108\n" \
"v_mul_f32_e32 v109, s70, v109\n" \
"v_pk_mul_f32 v[116:117], v[116:117], s[70:71]\n" \
"v_pk_mul_f32 v[124:125], v[124:125], s[70:71]\n" \
"v_pk_mul_f32 v[132:133], v[132:133], s[70:71]\n" \
"v_pk_mul_f32 v[140:141], v[140:141], s[70:71]\n" \
"v_pk_mul_f32 v[148:149], v[148:149], s[70:71]\n" \
"v_pk_mul_f32 v[156:157], v[156:157], s[70:71]\n" \
"v_permlane32_swap_b32_e32 v193, v225\n" \
"v_permlane32_swap_b32_e32 v209, v241\n" \
"v_mul_f32_e32 v102, s70, v102\n" \
"v_mul_f32_e32 v103, s70, v103\n" \
"v_cvt_pk_bf16_f32 v100, v100, v101\n" \
"v_mul_f32_e32 v110, s70, v110\n" \
"v_mul_f32_e32 v111, s70, v111\n" \
"v_cvt_pk_bf16_f32 v108, v108, v109\n" \
"v_pk_mul_f32 v[118:119], v[118:119], s[70:71]\n" \
"v_cvt_pk_bf16_f32 v116, v116, v117\n" \
"v_pk_mul_f32 v[126:127], v[126:127], s[70:71]\n" \
"v_cvt_pk_bf16_f32 v124, v124, v125\n" \
"v_pk_mul_f32 v[134:135], v[134:135], s[70:71]\n" \
"v_cvt_pk_bf16_f32 v132, v132, v133\n" \
"v_pk_mul_f32 v[142:143], v[142:143], s[70:71]\n" \
"v_cvt_pk_bf16_f32 v140, v140, v141\n" \
"v_pk_mul_f32 v[150:151], v[150:151], s[70:71]\n" \
"v_cvt_pk_bf16_f32 v148, v148, v149\n" \
"v_pk_mul_f32 v[158:159], v[158:159], s[70:71]\n" \
"v_cvt_pk_bf16_f32 v156, v156, v157\n" \
"v_fma_f32 v192, v192, s72, -v193\n" \
"v_fma_f32 v194, v194, s72, -v193\n" \
"v_fma_f32 v208, v208, s72, -v209\n" \
"v_fma_f32 v210, v210, s72, -v209\n" \
"v_mul_f32_e32 v104, s70, v104\n" \
"v_mul_f32_e32 v105, s70, v105\n" \
"v_mul_f32_e32 v106, s70, v106\n" \
"v_mul_f32_e32 v107, s70, v107\n" \
"v_cvt_pk_bf16_f32 v101, v102, v103\n" \
"v_mul_f32_e32 v112, s70, v112\n" \
"v_mul_f32_e32 v113, s70, v113\n" \
"v_mul_f32_e32 v114, s70, v114\n" \
"v_mul_f32_e32 v115, s70, v115\n" \
"v_cvt_pk_bf16_f32 v109, v110, v111\n" \
"v_pk_mul_f32 v[120:121], v[120:121], s[70:71]\n" \
"v_pk_mul_f32 v[122:123], v[122:123], s[70:71]\n" \
"v_cvt_pk_bf16_f32 v117, v118, v119\n" \
"v_pk_mul_f32 v[128:129], v[128:129], s[70:71]\n" \
"v_pk_mul_f32 v[130:131], v[130:131], s[70:71]\n" \
"v_cvt_pk_bf16_f32 v125, v126, v127\n" \
"v_pk_mul_f32 v[136:137], v[136:137], s[70:71]\n" \
"v_pk_mul_f32 v[138:139], v[138:139], s[70:71]\n" \
"v_cvt_pk_bf16_f32 v133, v134, v135\n" \
"v_pk_mul_f32 v[144:145], v[144:145], s[70:71]\n" \
"v_pk_mul_f32 v[146:147], v[146:147], s[70:71]\n" \
"v_cvt_pk_bf16_f32 v141, v142, v143\n" \
"v_pk_mul_f32 v[152:153], v[152:153], s[70:71]\n" \
"v_pk_mul_f32 v[154:155], v[154:155], s[70:71]\n" \
"v_cvt_pk_bf16_f32 v149, v150, v151\n" \
"v_pk_mul_f32 v[160:161], v[160:161], s[70:71]\n" \
"v_pk_mul_f32 v[162:163], v[162:163], s[70:71]\n" \
"v_cvt_pk_bf16_f32 v157, v158, v159\n" \
"v_pk_mul_f32 v[176:177], v[176:177], s[70:71]\n" \
"v_pk_mul_f32 v[178:179], v[178:179], s[70:71]\n" \
"v_exp_f32_e32 v192, v192\n" \
"v_exp_f32_e32 v194, v194\n" \
"v_exp_f32_e32 v208, v208\n" \
"v_exp_f32_e32 v210, v210\n" \
"v_fma_f32 v224, v224, s72, -v225\n" \
"v_fma_f32 v226, v226, s72, -v225\n" \
"v_mfma_f32_16x16x32_bf16 v[172:175], a[184:187], a[156:159], v[172:175]\n" \
"v_cvt_pk_bf16_f32 v102, v104, v105\n" \
"v_cvt_pk_bf16_f32 v103, v106, v107\n" \
"v_cvt_pk_bf16_f32 v110, v112, v113\n" \
"v_cvt_pk_bf16_f32 v111, v114, v115\n" \
"v_cvt_pk_bf16_f32 v118, v120, v121\n" \
"v_cvt_pk_bf16_f32 v119, v122, v123\n" \
"v_cvt_pk_bf16_f32 v126, v128, v129\n" \
"v_cvt_pk_bf16_f32 v127, v130, v131\n" \
"v_cvt_pk_bf16_f32 v134, v136, v137\n" \
"v_cvt_pk_bf16_f32 v135, v138, v139\n" \
"v_cvt_pk_bf16_f32 v142, v144, v145\n" \
"v_cvt_pk_bf16_f32 v143, v146, v147\n" \
"v_cvt_pk_bf16_f32 v150, v152, v153\n" \
"v_cvt_pk_bf16_f32 v151, v154, v155\n" \
"v_cvt_pk_bf16_f32 v158, v160, v161\n" \
"v_cvt_pk_bf16_f32 v159, v162, v163\n" \
"v_exp_f32_e32 v224, v224\n" \
"v_exp_f32_e32 v226, v226\n" \
"v_fma_f32 v240, v240, s72, -v241\n" \
"v_fma_f32 v242, v242, s72, -v241\n" \
"ds_write_b128 v27, v[100:103] offset:12288\n" \
"v_cvt_pk_bf16_f32 v192, v192, v194\n" \
"v_cvt_pk_bf16_f32 v208, v208, v210\n" \
"v_exp_f32_e32 v240, v240\n" \
"v_exp_f32_e32 v242, v242\n" \
"v_cvt_pk_bf16_f32 v68, v48, v49\n" \
"v_cvt_pk_bf16_f32 v69, v50, v51\n" \
"v_cvt_pk_bf16_f32 v70, v196, v197\n" \
"v_cvt_pk_bf16_f32 v71, v198, v199\n" \
"ds_write_b128 v27, v[108:111] offset:13312\n" \
"v_cvt_pk_bf16_f32 v72, v212, v213\n" \
"v_cvt_pk_bf16_f32 v73, v214, v215\n" \
"v_perm_b32 v184, v192, v192, v40\n" \
"v_perm_b32 v185, v192, v192, v41\n" \
"v_perm_b32 v186, v192, v192, v42\n" \
"v_perm_b32 v187, v192, v192, v43\n" \
"v_perm_b32 v200, v208, v208, v40\n" \
"v_perm_b32 v201, v208, v208, v41\n" \
"v_perm_b32 v202, v208, v208, v42\n" \
"v_perm_b32 v203, v208, v208, v43\n" \
"ds_write_b128 v21, v[116:119] offset:16384\n" \
"v_cvt_pk_bf16_f32 v224, v224, v226\n" \
"v_cvt_pk_bf16_f32 v74, v228, v229\n" \
"v_cvt_pk_bf16_f32 v75, v230, v231\n" \
"v_mfma_f32_16x16x32_bf16 v[32:35], v[68:71], v[184:187], 0\n" \
"v_mfma_f32_16x16x32_bf16 v[36:39], v[70:73], v[200:203], 0\n" \
"v_perm_b32 v216, v224, v224, v40\n" \
"v_perm_b32 v217, v224, v224, v41\n" \
"ds_write_b128 v21, v[124:127] offset:17408\n" \
"v_perm_b32 v218, v224, v224, v42\n" \
"v_perm_b32 v219, v224, v224, v43\n" \
"v_cvt_pk_bf16_f32 v240, v240, v242\n" \
"v_mfma_f32_16x16x32_bf16 v[172:175], a[248:251], a[220:223], v[172:175]\n" \
"v_cvt_pk_bf16_f32 v76, v244, v245\n" \
"v_cvt_pk_bf16_f32 v77, v246, v247\n" \
"v_mfma_f32_16x16x32_bf16 v[248:251], v[72:75], v[216:219], 0\n" \
"v_perm_b32 v232, v240, v240, v40\n" \
"v_perm_b32 v233, v240, v240, v41\n" \
"ds_write_b128 v21, v[132:135] offset:20480\n" \
"v_perm_b32 v234, v240, v240, v42\n" \
"v_perm_b32 v235, v240, v240, v43\n" \
"v_pk_mul_f32 v[172:173], v[172:173], s[70:71]\n" \
"v_pk_mul_f32 v[174:175], v[174:175], s[70:71]\n" \
"v_cvt_pk_bf16_f32 v172, v172, v173\n" \
"v_cvt_pk_bf16_f32 v173, v174, v175\n" \
"v_cvt_pk_bf16_f32 v174, v176, v177\n" \
"v_cvt_pk_bf16_f32 v175, v178, v179\n" \
"v_mfma_f32_16x16x32_bf16 v[64:67], v[74:77], v[232:235], 0\n" \
"ds_write_b128 v21, v[140:143] offset:21504\n" \
"v_mfma_f32_16x16x32_bf16 v[168:171], a[180:183], a[156:159], v[168:171]\n" \
"v_cvt_pk_bf16_f32 v52, v252, v253\n" \
"v_cvt_pk_bf16_f32 v53, v254, v255\n" \
"v_mfma_f32_16x16x32_bf16 v[168:171], a[244:247], a[220:223], v[168:171]\n" \
"v_exp_f32_e64 v45, v45\n" \
"v_mfma_f32_16x16x32_bf16 v[192:195], v[52:55], v[184:187], 0\n" \
"ds_write_b128 v21, v[148:151] offset:24576\n" \
"v_mfma_f32_16x16x32_bf16 v[208:211], v[54:57], v[200:203], 0\n" \
"v_mfma_f32_16x16x32_bf16 v[224:227], v[56:59], v[216:219], 0\n" \
"v_mfma_f32_16x16x32_bf16 v[240:243], v[58:61], v[232:235], 0\n" \
"ds_write_b128 v21, v[156:159] offset:25600\n" \
"v_mov_b32_dpp v34, v38 quad_perm:[0,1,2,3] row_mask:0x2 bank_mask:0xf\n" \
"v_pk_mul_f32 v[168:169], v[168:169], s[70:71]\n" \
"v_pk_mul_f32 v[170:171], v[170:171], s[70:71]\n" \
"v_mov_b32_dpp v34, v250 quad_perm:[0,1,2,3] row_mask:0x4 bank_mask:0xf\n" \
"v_add_f32_e64 v45, 1.0, v45\n" \
"v_mfma_f32_16x16x32_bf16 v[164:167], a[176:179], a[156:159], v[164:167]\n" \
"v_mov_b32_dpp v34, v66 quad_perm:[0,1,2,3] row_mask:0x8 bank_mask:0xf\n" \
"v_mov_b32_dpp v32, v36 quad_perm:[0,1,2,3] row_mask:0x2 bank_mask:0xf\n" \
"v_rcp_f32_e64 v45, v45\n" \
"ds_write_b128 v21, v[172:175] offset:29696\n" \
"v_mov_b32_dpp v33, v37 quad_perm:[0,1,2,3] row_mask:0x2 bank_mask:0xf\n" \
"v_rcp_f32_e64 v34, v34\n" \
"v_mov_b32_dpp v32, v248 quad_perm:[0,1,2,3] row_mask:0x4 bank_mask:0xf\n" \
"v_mov_b32_dpp v33, v249 quad_perm:[0,1,2,3] row_mask:0x4 bank_mask:0xf\n" \
"v_mul_f32_e64 v34, v45, v34\n" \
"v_mov_b32_dpp v32, v64 quad_perm:[0,1,2,3] row_mask:0x8 bank_mask:0xf\n" \
"v_mov_b32_dpp v33, v65 quad_perm:[0,1,2,3] row_mask:0x8 bank_mask:0xf\n" \
"v_mul_f32_e32 v32, v34, v32\n" \
"v_mul_f32_e32 v33, v34, v33\n" \
"v_mov_b32_e64 v38, v34\n" \
"v_mfma_f32_16x16x32_bf16 v[164:167], a[240:243], a[220:223], v[164:167]\n" \
"v_sub_f32_e32 v44, 1.0, v45\n" \
"v_permlane16_swap_b32_e32 v34, v38\n" \
"v_mov_b32_e32 v250, v34\n" \
"v_mov_b32_e32 v66, v38\n" \
"v_cvt_pk_bf16_f32 v32, v32, v33\n" \
"v_permlane32_swap_b32_e32 v34, v250\n" \
"v_permlane32_swap_b32_e32 v38, v66\n" \
"v_mul_f32_e64 v192, v34, v192\n" \
"v_pk_mul_f32 v[164:165], v[164:165], s[70:71]\n" \
"v_pk_mul_f32 v[166:167], v[166:167], s[70:71]\n" \
"v_cvt_pk_bf16_f32 v164, v164, v165\n" \
"v_cvt_pk_bf16_f32 v165, v166, v167\n" \
"v_cvt_pk_bf16_f32 v166, v168, v169\n" \
"v_cvt_pk_bf16_f32 v167, v170, v171\n" \
"ds_write_b128 v21, v[164:167] offset:28672\n" \
"v_mul_f32_e32 v193, v34, v193\n" \
"v_mul_f32_e32 v208, v38, v208\n" \
"v_mul_f32_e32 v209, v38, v209\n" \
"v_mul_f32_e32 v224, v250, v224\n" \
"v_mul_f32_e32 v225, v250, v225\n" \
"v_mul_f32_e32 v240, v66, v240\n" \
"v_mul_f32_e32 v241, v66, v241\n" \
"v_mul_f32_e32 v194, v34, v194\n" \
"v_mul_f32_e64 v195, v34, v195\n" \
"v_cvt_pk_bf16_f32 v192, v192, v193\n" \
"v_mul_f32_e32 v210, v38, v210\n" \
"v_mul_f32_e32 v211, v38, v211\n" \
"v_cvt_pk_bf16_f32 v208, v208, v209\n" \
"v_mul_f32_e32 v226, v250, v226\n" \
"v_mul_f32_e32 v227, v250, v227\n" \
"v_cvt_pk_bf16_f32 v224, v224, v225\n" \
"v_mul_f32_e32 v242, v66, v242\n" \
"v_mul_f32_e32 v243, v66, v243\n" \
"v_cvt_pk_bf16_f32 v240, v240, v241\n" \
"v_cvt_pk_bf16_f32 v193, v194, v195\n" \
"s_add_u32 s84, s48, 0x0\n" \
"s_nop 0\n" \
"v_cvt_pk_bf16_f32 v209, v210, v211\n" \
"s_add_u32 s85, s48, 0x24800\n" \
"v_cvt_pk_bf16_f32 v225, v226, v227\n" \
"s_add_u32 s86, s48, 0x49000\n" \
"v_cvt_pk_bf16_f32 v241, v242, v243\n" \
"s_add_u32 s87, s48, 0x6d800\n" \
"ds_write_b32 v26, v44\n" \
"buffer_store_dword v32, v25, s[80:83], s49 offen sc1\n" \
"buffer_store_dwordx2 v[192:193], v24, s[80:83], s84 offen sc1\n" \
"buffer_store_dwordx2 v[208:209], v24, s[80:83], s85 offen sc1\n" \
"buffer_store_dwordx2 v[224:225], v24, s[80:83], s86 offen sc1\n" \
"buffer_store_dwordx2 v[240:241], v24, s[80:83], s87 offen sc1\n" \
"s_branch L_end_%=\n" \
"L_poll60_%=:\n" \
"s_mov_b32 s84, 0\n" \
"L_pl60_%=:\n" \
"s_sleep 1\n" \
"v_bfe_u32 v44, v20, 2, 5\n" \
"buffer_load_dword v44, v44, s[80:83], s38 offen offset:128 sc1\n" \
"s_waitcnt vmcnt(0)\n" \
"v_cmp_ne_u32_e32 vcc, s62, v44\n" \
"s_add_u32 s84, s84, 1\n" \
"s_nop 3\n" \
"s_cbranch_vccz L_pollr60_%=\n" \
"s_cmp_gt_u32 s84, 0x400000\n" \
"s_cbranch_scc0 L_pl60_%=\n" \
"s_branch L_pollr60_%=\n" \
"L_poll160_%=:\n" \
"s_mov_b32 s84, 0\n" \
"L_pl160_%=:\n" \
"s_sleep 1\n" \
"v_bfe_u32 v44, v20, 2, 5\n" \
"buffer_load_dword v44, v44, s[80:83], s38 offen offset:0 sc1\n" \
"s_waitcnt vmcnt(0)\n" \
"v_cmp_ne_u32_e32 vcc, s62, v44\n" \
"s_add_u32 s84, s84, 1\n" \
"s_nop 3\n" \
"s_cbranch_vccz L_pollr160_%=\n" \
"s_cmp_gt_u32 s84, 0x400000\n" \
"s_cbranch_scc0 L_pl160_%=\n" \
"s_branch L_pollr160_%=\n" \
".fill (L_pb + 42548 - .) / 4, 4, 0xBF800000\n" \
"L_w1_%=:\n" \
"s_waitcnt vmcnt(19)\n" \
"s_barrier\n" \
"s_nop 0\n" \
"ds_read_b64_tr_b16 a[160:161], v32 offset:0\n" \
"ds_read_b64_tr_b16 a[162:163], v32 offset:1024\n" \
"ds_read_b128 a[128:131], v28 offset:16384\n" \
"ds_read_b64_tr_b16 a[224:225], v32 offset:8192\n" \
"ds_read_b64_tr_b16 a[226:227], v32 offset:9216\n" \
"ds_read_b128 a[192:195], v29 offset:16384\n" \
"ds_read_b128 a[132:135], v28 offset:18432\n" \
"ds_read_b128 a[196:199], v29 offset:18432\n" \
"ds_read_b64_tr_b16 a[164:165], v33 offset:0\n" \
"ds_read_b64_tr_b16 a[166:167], v33 offset:1024\n" \
"ds_read_b64_tr_b16 a[228:229], v33 offset:8192\n" \
"ds_read_b64_tr_b16 a[230:231], v33 offset:9216\n" \
"ds_read_b128 a[136:139], v28 offset:20480\n" \
"ds_read_b128 a[200:203], v29 offset:20480\n" \
"s_waitcnt lgkmcnt(11)\n" \
"v_mfma_f32_16x16x32_bf16 a[0:3], a[128:131], a[160:163], 0\n" \
"ds_read_b64_tr_b16 a[168:169], v34 offset:0\n" \
"s_lshr_b32 s4, s2, 1\n" \
"s_waitcnt lgkmcnt(9)\n" \
"v_mfma_f32_16x16x32_bf16 a[0:3], a[192:195], a[224:227], a[0:3]\n" \
"ds_read_b64_tr_b16 a[170:171], v34 offset:1024\n" \
"s_and_b32 s4, s4, 0x7c\n" \
"s_waitcnt lgkmcnt(9)\n" \
"v_mfma_f32_16x16x32_bf16 a[16:19], a[132:135], a[160:163], 0\n" \
"ds_read_b64_tr_b16 a[232:233], v34 offset:8192\n" \
"v_mov_b32_e32 v213, s13\n" \
"s_waitcnt lgkmcnt(9)\n" \
"v_mfma_f32_16x16x32_bf16 a[16:19], a[196:199], a[224:227], a[16:19]\n" \
"ds_read_b64_tr_b16 a[234:235], v34 offset:9216\n" \
"s_or_b32 s4, s4, s3\n" \
"s_waitcnt lgkmcnt(8)\n" \
"v_mfma_f32_16x16x32_bf16 a[20:23], a[132:135], a[164:167], 0\n" \
"ds_read_b128 a[140:143], v28 offset:22528\n" \
"v_lshrrev_b32_e32 v212, 4, v20\n" \
"s_waitcnt lgkmcnt(7)\n" \
"v_mfma_f32_16x16x32_bf16 a[20:23], a[196:199], a[228:231], a[20:23]\n" \
"ds_read_b128 a[204:207], v29 offset:22528\n" \
"v_mad_u64_u32 v[214:215], s[4:5], s4, 36, v[212:213]\n" \
"v_mfma_f32_16x16x32_bf16 a[4:7], a[128:131], a[164:167], 0\n" \
"ds_read_b64_tr_b16 a[172:173], v35 offset:0\n" \
"v_mov_b32_e64 v217, s9\n" \
"v_mfma_f32_16x16x32_bf16 a[4:7], a[192:195], a[228:231], a[4:7]\n" \
"ds_read_b64_tr_b16 a[174:175], v35 offset:1024\n" \
"s_movk_i32 s4, 0x240\n" \
"s_waitcnt lgkmcnt(9)\n" \
"v_mfma_f32_16x16x32_bf16 a[32:35], a[136:139], a[160:163], 0\n" \
"ds_read_b64_tr_b16 a[236:237], v35 offset:8192\n" \
"v_cmp_gt_u32_e32 vcc, s4, v20\n" \
"s_waitcnt lgkmcnt(9)\n" \
"v_mfma_f32_16x16x32_bf16 a[32:35], a[200:203], a[224:227], a[32:35]\n" \
"ds_read_b64_tr_b16 a[238:239], v35 offset:9216\n" \
"s_lshl_b32 s4, s2, 3\n" \
"v_mfma_f32_16x16x32_bf16 a[36:39], a[136:139], a[164:167], 0\n" \
"ds_read_b128 a[144:147], v28 offset:0\n" \
"s_and_b32 s4, s4, 56\n" \
"v_mfma_f32_16x16x32_bf16 a[36:39], a[200:203], a[228:231], a[36:39]\n" \
"ds_read_b128 a[208:211], v29 offset:0\n" \
"v_cndmask_b32_e32 v217, v217, v213, vcc\n" \
"s_waitcnt lgkmcnt(10)\n" \
"v_mfma_f32_16x16x32_bf16 a[40:43], a[136:139], a[168:171], 0\n" \
"ds_read_b128 a[148:151], v28 offset:2048\n" \
"v_lshlrev_b32_e32 v213, 3, v214\n" \
"s_waitcnt lgkmcnt(9)\n" \
"v_mfma_f32_16x16x32_bf16 a[40:43], a[200:203], a[232:235], a[40:43]\n" \
"ds_read_b128 a[212:215], v29 offset:2048\n" \
"v_and_b32_e32 v213, 0x1ffc0, v213\n" \
"v_mfma_f32_16x16x32_bf16 a[24:27], a[132:135], a[168:171], 0\n" \
"ds_read_b128 a[152:155], v28 offset:4096\n" \
"v_and_b32_e64 v214, 7, v214\n" \
"v_mfma_f32_16x16x32_bf16 a[24:27], a[196:199], a[232:235], a[24:27]\n" \
"ds_read_b128 a[216:219], v29 offset:4096\n" \
"v_or3_b32 v213, v214, s4, v213\n" \
"v_mfma_f32_16x16x32_bf16 a[8:11], a[128:131], a[168:171], 0\n" \
"ds_read_b128 a[156:159], v28 offset:6144\n" \
"s_lshl_b32 s4, s2, 2\n" \
"v_mfma_f32_16x16x32_bf16 a[8:11], a[192:195], a[232:235], a[8:11]\n" \
"ds_read_b128 a[220:223], v29 offset:6144\n" \
"s_mov_b32 m0, s32\n" \
"s_waitcnt lgkmcnt(13)\n" \
"v_mfma_f32_16x16x32_bf16 a[48:51], a[140:143], a[160:163], 0\n" \
"s_mov_b32 exec_lo, 0xff000000\n" \
"s_mov_b32 exec_hi, 0x0\n" \
"global_load_lds_dword v[184:185], off\n" \
"s_mov_b64 exec, -1\n" \
"s_waitcnt lgkmcnt(12)\n" \
"v_mfma_f32_16x16x32_bf16 a[48:51], a[204:207], a[224:227], a[48:51]\n" \
"ds_read_b64_tr_b16 a[176:177], v36 offset:0\n" \
"s_or_b32 s4, s4, s3\n" \
"v_mfma_f32_16x16x32_bf16 a[52:55], a[140:143], a[164:167], 0\n" \
"ds_read_b64_tr_b16 a[178:179], v36 offset:1024\n" \
"s_mul_i32 s4, s4, 18\n" \
"v_mfma_f32_16x16x32_bf16 a[52:55], a[204:207], a[228:231], a[52:55]\n" \
"s_mov_b32 m0, s32\n" \
"ds_read_b64_tr_b16 a[240:241], v36 offset:8192\n" \
"v_mfma_f32_16x16x32_bf16 a[56:59], a[140:143], a[168:171], 0\n" \
"s_mov_b32 exec_lo, 0xffff\n" \
"s_mov_b32 exec_hi, 0x0\n" \
"global_load_lds_dword v[186:187], off\n" \
"s_mov_b64 exec, -1\n" \
"v_mfma_f32_16x16x32_bf16 a[56:59], a[204:207], a[232:235], a[56:59]\n" \
"ds_read_b64_tr_b16 a[242:243], v36 offset:9216\n" \
"v_min_u32_e32 v212, 53, v212\n" \
"s_waitcnt lgkmcnt(14)\n" \
"v_mfma_f32_16x16x32_bf16 a[60:63], a[140:143], a[172:175], 0\n" \
"ds_read_b64_tr_b16 a[180:181], v37 offset:0\n" \
"s_sub_i32 s4, s4, 36\n" \
"s_waitcnt lgkmcnt(13)\n" \
"v_mfma_f32_16x16x32_bf16 a[60:63], a[204:207], a[236:239], a[60:63]\n" \
"ds_read_b64_tr_b16 a[182:183], v37 offset:1024\n" \
"v_add_u32_e32 v212, s4, v212\n" \
"v_mfma_f32_16x16x32_bf16 a[44:47], a[136:139], a[172:175], 0\n" \
"ds_read_b64_tr_b16 a[244:245], v37 offset:8192\n" \
"v_mov_b32_e64 v215, s12\n" \
"v_mfma_f32_16x16x32_bf16 a[44:47], a[200:203], a[236:239], a[44:47]\n" \
"ds_read_b64_tr_b16 a[246:247], v37 offset:9216\n" \
"v_mov_b32_e64 v216, s8\n" \
"v_mfma_f32_16x16x32_bf16 a[28:31], a[132:135], a[172:175], 0\n" \
"ds_read_b64_tr_b16 a[184:185], v38 offset:0\n" \
"v_cndmask_b32_e64 v212, v212, v213, vcc\n" \
"v_mfma_f32_16x16x32_bf16 a[28:31], a[196:199], a[236:239], a[28:31]\n" \
"ds_read_b64_tr_b16 a[186:187], v38 offset:1024\n" \
"v_cndmask_b32_e64 v216, v216, v215, vcc\n" \
"v_mfma_f32_16x16x32_bf16 a[12:15], a[128:131], a[172:175], 0\n" \
"v_lshlrev_b32_e32 v212, 7, v212\n" \
"v_mov_b32_e32 v213, 0\n" \
"v_mfma_f32_16x16x32_bf16 a[12:15], a[192:195], a[236:239], a[12:15]\n" \
"v_lshl_add_u64 v[188:189], v[216:217], 0, v[212:213]\n" \
"s_movk_i32 s5, 0xd80\n" \
"s_waitcnt lgkmcnt(15)\n" \
"v_mfma_f32_16x16x32_bf16 a[76:79], a[144:147], a[172:175], 0\n" \
"ds_read_b64_tr_b16 a[248:249], v38 offset:8192\n" \
"s_lshr_b32 s6, s2, 5\n" \
"v_mfma_f32_16x16x32_bf16 a[76:79], a[208:211], a[236:239], a[76:79]\n" \
"v_lshrrev_b32_e32 v212, 4, v20\n" \
"v_lshl_or_b32 v215, s3, 6, v212\n" \
"v_mfma_f32_16x16x32_bf16 a[72:75], a[144:147], a[168:171], 0\n" \
"v_mul_u32_u24_e32 v216, 0x2aab, v215\n" \
"v_lshrrev_b32_e64 v216, 16, v216\n" \
"v_mfma_f32_16x16x32_bf16 a[72:75], a[208:211], a[232:235], a[72:75]\n" \
"ds_read_b64_tr_b16 a[250:251], v38 offset:9216\n" \
"v_mad_i32_i24 v217, v216, -6, v215\n" \
"v_mfma_f32_16x16x32_bf16 a[68:71], a[144:147], a[164:167], 0\n" \
"v_add_u32_e32 v218, 0xfffffe50, v215\n" \
"s_mul_i32 s35, s6, 0x48\n" \
"v_mfma_f32_16x16x32_bf16 a[68:71], a[208:211], a[228:231], a[68:71]\n" \
"v_lshrrev_b32_e32 v219, 1, v217\n" \
"v_add_u32_e32 v218, s35, v216\n" \
"v_mfma_f32_16x16x32_bf16 a[64:67], a[144:147], a[160:163], 0\n" \
"v_mul_u32_u24_e32 v219, 0x480, v219\n" \
"v_and_b32_e64 v214, 16, v20\n" \
"v_mfma_f32_16x16x32_bf16 a[64:67], a[208:211], a[224:227], a[64:67]\n" \
"ds_read_b64_tr_b16 a[188:189], v39 offset:0\n" \
"v_mad_u32_u24 v219, v218, s5, v219\n" \
"v_mfma_f32_16x16x32_bf16 a[80:83], a[148:151], a[160:163], 0\n" \
"v_or_b32_e32 v192, v219, v214\n" \
"v_mov_b32_e32 v220, 2.0\n" \
"s_waitcnt lgkmcnt(15)\n" \
"v_mfma_f32_16x16x32_bf16 a[80:83], a[212:215], a[224:227], a[80:83]\n" \
"v_mov_b32_e32 v222, 7\n" \
"v_lshlrev_b32_e64 v220, 7, v218\n" \
"v_mfma_f32_16x16x32_bf16 a[84:87], a[148:151], a[164:167], 0\n" \
"ds_read_b64_tr_b16 a[190:191], v39 offset:1024\n" \
"v_lshrrev_b32_e64 v218, 1, v218\n" \
"v_mfma_f32_16x16x32_bf16 a[84:87], a[212:215], a[228:231], a[84:87]\n" \
"v_and_b32_e32 v218, 4, v218\n" \
"v_and_b32_e32 v216, 2, v216\n" \
"v_mfma_f32_16x16x32_bf16 a[88:91], a[148:151], a[168:171], 0\n" \
"s_mul_i32 s7, s6, 0x2400\n" \
"s_sub_i32 s34, 0x22000, s7\n" \
"v_mfma_f32_16x16x32_bf16 a[88:91], a[212:215], a[232:235], a[88:91]\n" \
"ds_read_b64_tr_b16 a[252:253], v39 offset:8192\n" \
"v_bitop3_b32 v216, v217, v218, v216 bitop3:0x1e\n" \
"v_mfma_f32_16x16x32_bf16 a[92:95], a[148:151], a[172:175], 0\n" \
"v_or_b32_e32 v213, 0x24400, v20\n" \
"v_lshlrev_b32_e64 v216, 4, v216\n" \
"v_mfma_f32_16x16x32_bf16 a[92:95], a[212:215], a[236:239], a[92:95]\n" \
"v_add3_u32 v196, v220, s34, v216\n" \
"v_mov_b32_e64 v216, v213\n" \
"v_mfma_f32_16x16x32_bf16 a[108:111], a[152:155], a[172:175], 0\n" \
"ds_read_b64_tr_b16 a[254:255], v39 offset:9216\n" \
"s_movk_i32 s36, 0x1b0\n" \
"v_mfma_f32_16x16x32_bf16 a[108:111], a[216:219], a[236:239], a[108:111]\n" \
"v_or_b32_e32 v216, 0x100, v215\n" \
"v_mul_u32_u24_e32 v217, 0x2aab, v216\n" \
"v_mfma_f32_16x16x32_bf16 a[104:107], a[152:155], a[168:171], 0\n" \
"v_add_u32_e32 v215, 0xffffff50, v215\n" \
"v_lshrrev_b32_e32 v217, 16, v217\n" \
"v_mfma_f32_16x16x32_bf16 a[104:107], a[216:219], a[232:235], a[104:107]\n" \
"v_mov_b32_e64 v220, v215\n" \
"v_mad_i32_i24 v218, v217, -6, v216\n" \
"v_mfma_f32_16x16x32_bf16 a[100:103], a[152:155], a[164:167], 0\n" \
"v_cmp_gt_u32_e32 vcc, s36, v216\n" \
"v_mov_b32_e32 v221, 7\n" \
"v_mfma_f32_16x16x32_bf16 a[100:103], a[216:219], a[228:231], a[100:103]\n" \
"v_lshrrev_b32_e32 v222, 1, v218\n" \
"v_cndmask_b32_e32 v217, v220, v217, vcc\n" \
"v_mfma_f32_16x16x32_bf16 a[96:99], a[152:155], a[160:163], 0\n" \
"s_movk_i32 s4, 0x48\n" \
"v_add_u32_e32 v220, s35, v217\n" \
"v_mfma_f32_16x16x32_bf16 a[96:99], a[216:219], a[224:227], a[96:99]\n" \
"v_mul_u32_u24_e32 v222, 0x480, v222\n" \
"v_mad_u32_u24 v222, v220, s5, v222\n" \
"s_waitcnt lgkmcnt(15)\n" \
"v_mfma_f32_16x16x32_bf16 a[112:115], a[156:159], a[160:163], 0\n" \
"v_cmp_gt_u32_e64 s[4:5], s4, v215\n" \
"v_cndmask_b32_e32 v215, v221, v218, vcc\n" \
"v_mfma_f32_16x16x32_bf16 a[112:115], a[220:223], a[224:227], a[112:115]\n" \
"v_lshrrev_b32_e32 v218, 1, v220\n" \
"v_or_b32_e32 v214, v222, v214\n" \
"v_mfma_f32_16x16x32_bf16 a[116:119], a[156:159], a[164:167], 0\n" \
"v_mov_b32_e32 v222, 2.0\n" \
"v_and_b32_e32 v218, 4, v218\n" \
"v_mfma_f32_16x16x32_bf16 a[116:119], a[220:223], a[228:231], a[116:119]\n" \
"v_and_b32_e64 v217, 2, v217\n" \
"v_bitop3_b32 v215, v215, v218, v217 bitop3:0x1e\n" \
"v_mfma_f32_16x16x32_bf16 a[120:123], a[156:159], a[168:171], 0\n" \
"v_lshlrev_b32_e32 v223, 1, v220\n" \
"v_mov_b32_e32 v224, 2.0\n" \
"v_mfma_f32_16x16x32_bf16 a[120:123], a[220:223], a[232:235], a[120:123]\n" \
"v_cndmask_b32_e32 v193, v222, v214, vcc\n" \
"v_lshlrev_b32_e32 v214, 7, v220\n" \
"v_mfma_f32_16x16x32_bf16 a[124:127], a[156:159], a[172:175], 0\n" \
"v_lshlrev_b32_e32 v215, 4, v215\n" \
"s_mul_i32 s39, s3, 18\n" \
"s_waitcnt vmcnt(5) lgkmcnt(0)\n" \
"v_mfma_f32_16x16x32_bf16 a[124:127], a[220:223], a[236:239], a[124:127]\n" \
"s_barrier\n" \
"v_mfma_f32_16x16x32_bf16 v[52:55], a[128:131], a[176:179], 0\n" \
"s_add_u32 m0, s30, 0x0\n" \
"s_movk_i32 s84, 0x100\n" \
"v_mfma_f32_16x16x32_bf16 v[52:55], a[192:195], a[240:243], v[52:55]\n" \
"buffer_load_dwordx4 v22, s[64:67], s84 offen offset:0 lds\n" \
"v_mfma_f32_16x16x32_bf16 v[56:59], a[128:131], a[180:183], 0\n" \
"ds_read_b64_tr_b16 a[160:161], v32 offset:32768\n" \
"v_add3_u32 v214, v214, s34, v215\n" \
"v_mfma_f32_16x16x32_bf16 v[56:59], a[192:195], a[244:247], v[56:59]\n" \
"s_movk_i32 s84, 0x1d00\n" \
"v_mfma_f32_16x16x32_bf16 v[60:63], a[128:131], a[184:187], 0\n" \
"buffer_load_dwordx4 v23, s[64:67], s84 offen offset:1024 lds\n" \
"v_mfma_f32_16x16x32_bf16 v[60:63], a[192:195], a[248:251], v[60:63]\n" \
"ds_read_b64_tr_b16 a[162:163], v32 offset:33792\n" \
"v_cndmask_b32_e64 v195, v224, v223, s[4:5]\n" \
"v_mfma_f32_16x16x32_bf16 v[64:67], a[128:131], a[188:191], 0\n" \
"ds_read_b64_tr_b16 a[224:225], v32 offset:40960\n" \
"s_movk_i32 s4, 0x1f8\n" \
"v_mfma_f32_16x16x32_bf16 v[64:67], a[192:195], a[252:255], v[64:67]\n" \
"ds_read_b128 a[128:131], v28 offset:49152\n" \
"v_mov_b32_e64 v215, v213\n" \
"v_mfma_f32_16x16x32_bf16 v[80:83], a[132:135], a[188:191], 0\n" \
"ds_read_b128 a[192:195], v29 offset:49152\n" \
"s_add_i32 s35, s35, s39\n" \
"v_mfma_f32_16x16x32_bf16 v[80:83], a[196:199], a[252:255], v[80:83]\n" \
"s_movk_i32 s84, 0x3900\n" \
"v_mfma_f32_16x16x32_bf16 v[76:79], a[132:135], a[184:187], 0\n" \
"buffer_load_dwordx4 v22, s[64:67], s84 offen offset:2048 lds\n" \
"v_mfma_f32_16x16x32_bf16 v[76:79], a[196:199], a[248:251], v[76:79]\n" \
"s_movk_i32 s84, 0x5500\n" \
"v_mfma_f32_16x16x32_bf16 v[72:75], a[132:135], a[180:183], 0\n" \
"buffer_load_dwordx4 v23, s[64:67], s84 offen offset:3072 lds\n" \
"v_mfma_f32_16x16x32_bf16 v[72:75], a[196:199], a[244:247], v[72:75]\n" \
"s_add_u32 m0, s31, 0x0\n" \
"s_mov_b32 s84, 0x100000\n" \
"v_mfma_f32_16x16x32_bf16 v[68:71], a[132:135], a[176:179], 0\n" \
"buffer_load_dwordx4 v26, s[68:71], s84 offen offset:0 lds\n" \
"v_mfma_f32_16x16x32_bf16 v[68:71], a[196:199], a[240:243], v[68:71]\n" \
"ds_read_b128 a[132:135], v28 offset:51200\n" \
"v_cmp_gt_u32_e64 vcc, s4, v216\n" \
"v_mfma_f32_16x16x32_bf16 v[84:87], a[136:139], a[176:179], 0\n" \
"ds_read_b128 a[196:199], v29 offset:51200\n" \
"v_add_u32_e64 v212, s35, v212\n" \
"v_mfma_f32_16x16x32_bf16 v[84:87], a[200:203], a[240:243], v[84:87]\n" \
"s_addk_i32 s84, 0x7c00\n" \
"v_mfma_f32_16x16x32_bf16 v[88:91], a[136:139], a[180:183], 0\n" \
"buffer_load_dwordx4 v26, s[68:71], s84 offen offset:1024 lds\n" \
"v_mfma_f32_16x16x32_bf16 v[88:91], a[200:203], a[244:247], v[88:91]\n" \
"ds_read_b64_tr_b16 a[226:227], v32 offset:41984\n" \
"v_mov_b32_e32 v219, 2.0\n" \
"v_mfma_f32_16x16x32_bf16 v[92:95], a[136:139], a[184:187], 0\n" \
"s_addk_i32 s84, 0x7c00\n" \
"v_mfma_f32_16x16x32_bf16 v[92:95], a[200:203], a[248:251], v[92:95]\n" \
"buffer_load_dwordx4 v27, s[68:71], s84 offen offset:2048 lds\n" \
"v_mfma_f32_16x16x32_bf16 v[96:99], a[136:139], a[188:191], 0\n" \
"ds_read_b64_tr_b16 a[164:165], v33 offset:32768\n" \
"v_cndmask_b32_e32 v197, v215, v214, vcc\n" \
"v_mfma_f32_16x16x32_bf16 v[96:99], a[200:203], a[252:255], v[96:99]\n" \
"ds_read_b128 a[136:139], v28 offset:53248\n" \
"s_movk_i32 s35, 0x120\n" \
"v_mfma_f32_16x16x32_bf16 v[112:115], a[140:143], a[188:191], 0\n" \
"ds_read_b128 a[200:203], v29 offset:53248\n" \
"v_mul_u32_u24_e32 v214, 0xd80, v212\n" \
"v_mfma_f32_16x16x32_bf16 v[112:115], a[204:207], a[252:255], v[112:115]\n" \
"s_addk_i32 s84, 0x7c00\n" \
"v_mfma_f32_16x16x32_bf16 v[108:111], a[140:143], a[184:187], 0\n" \
"buffer_load_dwordx4 v27, s[68:71], s84 offen offset:3072 lds\n" \
"v_mfma_f32_16x16x32_bf16 v[108:111], a[204:207], a[248:251], v[108:111]\n" \
"s_add_u32 m0, s30, 0x1000\n" \
"s_mov_b32 s84, 0x8100\n" \
"v_mfma_f32_16x16x32_bf16 v[104:107], a[140:143], a[180:183], 0\n" \
"buffer_load_dwordx4 v22, s[64:67], s84 offen offset:0 lds\n" \
"v_mfma_f32_16x16x32_bf16 v[104:107], a[204:207], a[244:247], v[104:107]\n" \
"ds_read_b64_tr_b16 a[166:167], v33 offset:33792\n" \
"v_cmp_gt_u32_e64 vcc, s35, v20\n" \
"v_mfma_f32_16x16x32_bf16 v[100:103], a[140:143], a[176:179], 0\n" \
"ds_read_b64_tr_b16 a[228:229], v33 offset:40960\n" \
"v_lshrrev_b32_e64 v215, 1, v212\n" \
"v_mfma_f32_16x16x32_bf16 v[100:103], a[204:207], a[240:243], v[100:103]\n" \
"ds_read_b128 a[140:143], v28 offset:55296\n" \
"s_and_b32 s4, s2, 7\n" \
"v_mfma_f32_16x16x32_bf16 v[116:119], a[144:147], a[176:179], 0\n" \
"ds_read_b128 a[204:207], v29 offset:55296\n" \
"v_cndmask_b32_e32 v194, v219, v214, vcc\n" \
"v_mfma_f32_16x16x32_bf16 v[116:119], a[208:211], a[240:243], v[116:119]\n" \
"s_addk_i32 s84, 0x1c00\n" \
"v_mfma_f32_16x16x32_bf16 v[120:123], a[144:147], a[180:183], 0\n" \
"buffer_load_dwordx4 v23, s[64:67], s84 offen offset:1024 lds\n" \
"v_mfma_f32_16x16x32_bf16 v[120:123], a[208:211], a[244:247], v[120:123]\n" \
"s_addk_i32 s84, 0x1c00\n" \
"v_mfma_f32_16x16x32_bf16 v[124:127], a[144:147], a[184:187], 0\n" \
"buffer_load_dwordx4 v22, s[64:67], s84 offen offset:2048 lds\n" \
"v_mfma_f32_16x16x32_bf16 v[124:127], a[208:211], a[248:251], v[124:127]\n" \
"s_addk_i32 s84, 0x1c00\n" \
".fill (L_pb + 44768 - .) / 4, 4, 0xBF800000\n" \
"L_pp11:\n" \
"s_cbranch_execz L_pret\n" \
"v_mfma_f32_16x16x32_bf16 v[128:131], a[144:147], a[188:191], 0\n" \
"buffer_load_dwordx4 v23, s[64:67], s84 offen offset:3072 lds\n" \
"v_mfma_f32_16x16x32_bf16 v[128:131], a[208:211], a[252:255], v[128:131]\n" \
"ds_read_b128 a[144:147], v28 offset:32768\n" \
"v_lshlrev_b32_e32 v214, 7, v212\n" \
"v_mfma_f32_16x16x32_bf16 v[144:147], a[148:151], a[188:191], 0\n" \
"ds_read_b128 a[208:211], v29 offset:32768\n" \
"v_lshrrev_b32_e64 v212, 2, v212\n" \
"v_mfma_f32_16x16x32_bf16 v[144:147], a[212:215], a[252:255], v[144:147]\n" \
"s_add_u32 m0, s31, 0x4000\n" \
"s_mov_b32 s84, 0x100000\n" \
"v_mfma_f32_16x16x32_bf16 v[140:143], a[148:151], a[184:187], 0\n" \
"buffer_load_dwordx4 v26, s[72:75], s84 offen offset:0 lds\n" \
"v_mfma_f32_16x16x32_bf16 v[140:143], a[212:215], a[248:251], v[140:143]\n" \
"ds_read_b64_tr_b16 a[230:231], v33 offset:41984\n" \
"v_and_b32_e64 v212, 2, v212\n" \
"v_mfma_f32_16x16x32_bf16 v[136:139], a[148:151], a[180:183], 0\n" \
"s_addk_i32 s84, 0x7c00\n" \
"v_mfma_f32_16x16x32_bf16 v[136:139], a[212:215], a[244:247], v[136:139]\n" \
"buffer_load_dwordx4 v26, s[72:75], s84 offen offset:1024 lds\n" \
"v_mfma_f32_16x16x32_bf16 v[132:135], a[148:151], a[176:179], 0\n" \
"ds_read_b64_tr_b16 a[168:169], v34 offset:32768\n" \
"v_and_or_b32 v212, v215, 1, v212\n" \
"v_mfma_f32_16x16x32_bf16 v[132:135], a[212:215], a[240:243], v[132:135]\n" \
"ds_read_b128 a[148:151], v28 offset:34816\n" \
"s_lshl_b32 s5, s4, 2\n" \
"v_mfma_f32_16x16x32_bf16 v[148:151], a[152:155], a[176:179], 0\n" \
"ds_read_b128 a[212:215], v29 offset:34816\n" \
"s_bfe_u32 s38, s2, 0x20003\n" \
"v_mfma_f32_16x16x32_bf16 v[148:151], a[216:219], a[240:243], v[148:151]\n" \
"ds_read_b64_tr_b16 a[170:171], v34 offset:33792\n" \
"v_lshlrev_b32_e64 v212, 5, v212\n" \
"v_mfma_f32_16x16x32_bf16 v[152:155], a[152:155], a[180:183], 0\n" \
"ds_read_b64_tr_b16 a[232:233], v34 offset:40960\n" \
"s_movk_i32 s35, 0x60\n" \
"v_mfma_f32_16x16x32_bf16 v[152:155], a[216:219], a[244:247], v[152:155]\n" \
"ds_read_b64_tr_b16 a[234:235], v34 offset:41984\n" \
"s_or_b32 s5, s5, s38\n" \
"v_mfma_f32_16x16x32_bf16 v[156:159], a[152:155], a[184:187], 0\n" \
"ds_read_b64_tr_b16 a[172:173], v35 offset:32768\n" \
"v_bitop3_b32 v212, v212, s35, v214 bitop3:0x36\n" \
"v_mfma_f32_16x16x32_bf16 v[156:159], a[216:219], a[248:251], v[156:159]\n" \
"ds_read_b64_tr_b16 a[174:175], v35 offset:33792\n" \
"s_lshr_b32 s36, s2, 3\n" \
"v_mfma_f32_16x16x32_bf16 v[160:163], a[152:155], a[188:191], 0\n" \
"ds_read_b64_tr_b16 a[236:237], v35 offset:40960\n" \
"v_add_u32_e32 v212, s34, v212\n" \
"v_mfma_f32_16x16x32_bf16 v[160:163], a[216:219], a[252:255], v[160:163]\n" \
"ds_read_b128 a[152:155], v28 offset:36864\n" \
"s_mul_i32 s34, s5, 0x12000\n" \
"v_mfma_f32_16x16x32_bf16 v[176:179], a[156:159], a[188:191], 0\n" \
"ds_read_b128 a[216:219], v29 offset:36864\n" \
"s_or_b32 s40, s34, 0x6000000\n" \
"v_mfma_f32_16x16x32_bf16 v[176:179], a[220:223], a[252:255], v[176:179]\n" \
"ds_read_b64_tr_b16 a[238:239], v35 offset:41984\n" \
"s_mul_i32 s34, s36, 0x90000\n" \
"v_mfma_f32_16x16x32_bf16 v[172:175], a[156:159], a[184:187], 0\n" \
"s_lshl_b32 s36, s3, 14\n" \
"s_add_i32 s34, s34, s36\n" \
"v_mfma_f32_16x16x32_bf16 v[172:175], a[220:223], a[248:251], v[172:175]\n" \
"s_lshl_b32 s38, s5, 8\n" \
"s_add_i32 s41, s34, 0x4240000\n" \
"v_mfma_f32_16x16x32_bf16 v[168:171], a[156:159], a[180:183], 0\n" \
"s_mul_i32 s34, s5, 36\n" \
"s_add_i32 s5, s7, s40\n" \
"v_mfma_f32_16x16x32_bf16 v[168:171], a[220:223], a[244:247], v[168:171]\n" \
"s_addk_i32 s5, 0x2000\n" \
"s_mov_b32 s37, 2.0\n" \
"v_mfma_f32_16x16x32_bf16 v[164:167], a[156:159], a[176:179], 0\n" \
"s_or_b32 s38, s38, 0x5be8000\n" \
"v_mfma_f32_16x16x32_bf16 v[164:167], a[220:223], a[240:243], v[164:167]\n" \
"ds_read_b128 a[156:159], v28 offset:38912\n" \
"ds_read_b128 a[220:223], v29 offset:38912\n" \
"s_waitcnt lgkmcnt(15)\n" \
"v_mfma_f32_16x16x32_bf16 a[0:3], a[128:131], a[160:163], a[0:3]\n" \
"s_addk_i32 s84, 0x7c00\n" \
"v_mfma_f32_16x16x32_bf16 a[0:3], a[192:195], a[224:227], a[0:3]\n" \
"buffer_load_dwordx4 v27, s[72:75], s84 offen offset:2048 lds\n" \
"v_mfma_f32_16x16x32_bf16 a[4:7], a[128:131], a[164:167], a[4:7]\n" \
"ds_read_b64_tr_b16 a[176:177], v36 offset:32768\n" \
"s_waitcnt lgkmcnt(15)\n" \
"v_mfma_f32_16x16x32_bf16 a[4:7], a[192:195], a[228:231], a[4:7]\n" \
"s_addk_i32 s84, 0x7c00\n" \
"s_waitcnt lgkmcnt(11)\n" \
"v_mfma_f32_16x16x32_bf16 a[8:11], a[128:131], a[168:171], a[8:11]\n" \
"buffer_load_dwordx4 v27, s[72:75], s84 offen offset:3072 lds\n" \
"s_waitcnt lgkmcnt(9)\n" \
"v_mfma_f32_16x16x32_bf16 a[8:11], a[192:195], a[232:235], a[8:11]\n" \
"ds_read_b64_tr_b16 a[178:179], v36 offset:33792\n" \
"s_waitcnt lgkmcnt(8)\n" \
"v_mfma_f32_16x16x32_bf16 a[12:15], a[128:131], a[172:175], a[12:15]\n" \
"ds_read_b64_tr_b16 a[240:241], v36 offset:40960\n" \
"s_waitcnt lgkmcnt(5)\n" \
"v_mfma_f32_16x16x32_bf16 a[12:15], a[192:195], a[236:239], a[12:15]\n" \
"s_cmp_lg_u32 s3, 0\n" \
"s_cselect_b32 s36, s37, s5\n" \
"v_mfma_f32_16x16x32_bf16 a[28:31], a[132:135], a[172:175], a[28:31]\n" \
"s_lshl_b32 s5, s6, 2\n" \
"s_or_b32 s5, s38, s5\n" \
"v_mfma_f32_16x16x32_bf16 a[28:31], a[196:199], a[236:239], a[28:31]\n" \
"ds_read_b64_tr_b16 a[242:243], v36 offset:41984\n" \
"s_lshl_b32 s35, s3, 10\n" \
"v_mfma_f32_16x16x32_bf16 a[24:27], a[132:135], a[168:171], a[24:27]\n" \
"s_cmpk_lt_u32 s3, 3\n" \
"s_cselect_b32 s37, s37, s5\n" \
"v_mfma_f32_16x16x32_bf16 a[24:27], a[196:199], a[232:235], a[24:27]\n" \
"s_lshl_b32 s5, s4, 16\n" \
"s_lshl_b32 s4, s4, 11\n" \
"v_mfma_f32_16x16x32_bf16 a[20:23], a[132:135], a[164:167], a[20:23]\n" \
"v_cndmask_b32_e32 v198, v213, v212, vcc\n" \
"v_or_b32_e32 v212, s35, v20\n" \
"v_mfma_f32_16x16x32_bf16 a[20:23], a[196:199], a[228:231], a[20:23]\n" \
"ds_read_b64_tr_b16 a[180:181], v37 offset:32768\n" \
"s_or_b32 s39, s40, s35\n" \
"v_mfma_f32_16x16x32_bf16 a[16:19], a[132:135], a[160:163], a[16:19]\n" \
"s_or_b32 s55, s41, s4\n" \
"v_or_b32_e32 v199, 0x22000, v212\n" \
"v_mfma_f32_16x16x32_bf16 a[16:19], a[196:199], a[224:227], a[16:19]\n" \
"s_add_i32 s35, s39, s7\n" \
"s_add_i32 s54, s41, s5\n" \
"v_mfma_f32_16x16x32_bf16 a[32:35], a[136:139], a[160:163], a[32:35]\n" \
"s_mov_b32 m0, s32\n" \
"ds_read_b64_tr_b16 a[182:183], v37 offset:33792\n" \
"v_mfma_f32_16x16x32_bf16 a[32:35], a[200:203], a[224:227], a[32:35]\n" \
"s_mov_b32 exec_lo, 0x0\n" \
"s_mov_b32 exec_hi, 0xff\n" \
"global_load_lds_dword v[184:185], off\n" \
"s_mov_b64 exec, -1\n" \
"v_mfma_f32_16x16x32_bf16 a[36:39], a[136:139], a[164:167], a[36:39]\n" \
"s_add_i32 s55, s55, 0x80000\n" \
"v_mov_b32_e32 v215, 0xffff\n" \
"v_mfma_f32_16x16x32_bf16 a[36:39], a[200:203], a[228:231], a[36:39]\n" \
"ds_read_b64_tr_b16 a[244:245], v37 offset:40960\n" \
"s_and_b32 s4, s3, 1\n" \
"v_mfma_f32_16x16x32_bf16 a[40:43], a[136:139], a[168:171], a[40:43]\n" \
"s_lshl_b32 s5, s4, 4\n" \
"v_lshrrev_b32_e32 v212, 4, v20\n" \
"v_mfma_f32_16x16x32_bf16 a[40:43], a[200:203], a[232:235], a[40:43]\n" \
"s_mov_b32 m0, s32\n" \
"v_lshrrev_b32_e32 v218, 6, v20\n" \
"v_mfma_f32_16x16x32_bf16 a[44:47], a[136:139], a[172:175], a[44:47]\n" \
"s_mov_b32 exec_lo, 0xffff0000\n" \
"s_mov_b32 exec_hi, 0x0\n" \
"global_load_lds_dword v[186:187], off\n" \
"s_mov_b64 exec, -1\n" \
"v_mfma_f32_16x16x32_bf16 a[44:47], a[200:203], a[236:239], a[44:47]\n" \
"ds_read_b64_tr_b16 a[246:247], v37 offset:41984\n" \
"v_and_b32_e64 v219, 12, v218\n" \
"v_mfma_f32_16x16x32_bf16 a[60:63], a[140:143], a[172:175], a[60:63]\n" \
"v_and_or_b32 v214, v212, 15, s5\n" \
"v_cmp_gt_u32_e64 vcc, v219, v214\n" \
"v_mfma_f32_16x16x32_bf16 a[60:63], a[204:207], a[236:239], a[60:63]\n" \
"v_mov_b32_e32 v220, 0xffff0000\n" \
"s_lshl_b32 s5, s2, 2\n" \
"v_mfma_f32_16x16x32_bf16 a[56:59], a[140:143], a[168:171], a[56:59]\n" \
"ds_read_b64_tr_b16 a[184:185], v38 offset:32768\n" \
"v_cndmask_b32_e64 v217, v215, 0, vcc\n" \
"v_mfma_f32_16x16x32_bf16 a[56:59], a[204:207], a[232:235], a[56:59]\n" \
"v_cmp_lt_u32_e32 vcc, v219, v214\n" \
"v_mov_b32_e64 v213, s23\n" \
"v_mfma_f32_16x16x32_bf16 a[52:55], a[140:143], a[164:167], a[52:55]\n" \
"s_or_b32 s5, s5, s3\n" \
"v_cndmask_b32_e32 v221, 0, v220, vcc\n" \
"v_mfma_f32_16x16x32_bf16 a[52:55], a[204:207], a[228:231], a[52:55]\n" \
"ds_read_b64_tr_b16 a[186:187], v38 offset:33792\n" \
"v_or_b32_e64 v12, v217, v221\n" \
"v_mfma_f32_16x16x32_bf16 a[48:51], a[140:143], a[160:163], a[48:51]\n" \
"v_or_b32_e32 v217, 2, v219\n" \
"v_cmp_gt_u32_e32 vcc, v217, v214\n" \
"v_mfma_f32_16x16x32_bf16 a[48:51], a[204:207], a[224:227], a[48:51]\n" \
"v_mov_b32_e32 v216, s25\n" \
"s_movk_i32 s40, 0xf400\n" \
"v_mfma_f32_16x16x32_bf16 a[64:67], a[144:147], a[160:163], a[64:67]\n" \
"ds_read_b64_tr_b16 a[248:249], v38 offset:40960\n" \
"v_cndmask_b32_e64 v221, v215, 0, vcc\n" \
"v_mfma_f32_16x16x32_bf16 a[64:67], a[208:211], a[224:227], a[64:67]\n" \
"v_cmp_lt_u32_e32 vcc, v217, v214\n" \
"s_lshl_b32 s4, s4, 6\n" \
"v_mfma_f32_16x16x32_bf16 a[68:71], a[144:147], a[164:167], a[68:71]\n" \
"v_cndmask_b32_e32 v217, 0, v220, vcc\n" \
"v_or_b32_e32 v13, v221, v217\n" \
"v_mfma_f32_16x16x32_bf16 a[68:71], a[208:211], a[228:231], a[68:71]\n" \
"ds_read_b64_tr_b16 a[250:251], v38 offset:41984\n" \
"v_or_b32_e64 v217, 16, v219\n" \
"v_mfma_f32_16x16x32_bf16 a[72:75], a[144:147], a[168:171], a[72:75]\n" \
"v_cmp_gt_u32_e32 vcc, v217, v214\n" \
"s_nop 1\n" \
"v_cndmask_b32_e64 v221, v215, 0, vcc\n" \
"v_mfma_f32_16x16x32_bf16 a[72:75], a[208:211], a[232:235], a[72:75]\n" \
"v_cmp_lt_u32_e32 vcc, v217, v214\n" \
"s_nop 1\n" \
"v_cndmask_b32_e64 v217, 0, v220, vcc\n" \
"v_mfma_f32_16x16x32_bf16 a[76:79], a[144:147], a[172:175], a[76:79]\n" \
"v_or_b32_e32 v14, v221, v217\n" \
"v_or_b32_e32 v217, 18, v219\n" \
"v_mfma_f32_16x16x32_bf16 a[76:79], a[208:211], a[236:239], a[76:79]\n" \
"ds_read_b64_tr_b16 a[188:189], v39 offset:32768\n" \
"v_cmp_gt_u32_e64 vcc, v217, v214\n" \
"v_mfma_f32_16x16x32_bf16 a[92:95], a[148:151], a[172:175], a[92:95]\n" \
"s_nop 0\n" \
"v_cndmask_b32_e64 v215, v215, 0, vcc\n" \
"v_cmp_lt_u32_e32 vcc, v217, v214\n" \
"v_mfma_f32_16x16x32_bf16 a[92:95], a[212:215], a[236:239], a[92:95]\n" \
"s_nop 0\n" \
"v_cndmask_b32_e32 v214, 0, v220, vcc\n" \
"v_or_b32_e64 v15, v215, v214\n" \
"v_mfma_f32_16x16x32_bf16 a[88:91], a[148:151], a[168:171], a[88:91]\n" \
"ds_read_b64_tr_b16 a[190:191], v39 offset:33792\n" \
"v_mad_u64_u32 v[214:215], s[6:7], s5, 42, v[212:213]\n" \
"v_mfma_f32_16x16x32_bf16 a[88:91], a[212:215], a[232:235], a[88:91]\n" \
"s_movk_i32 s5, 0x1440\n" \
"v_cmp_gt_u32_e32 vcc, s5, v214\n" \
"v_mfma_f32_16x16x32_bf16 a[84:87], a[148:151], a[164:167], a[84:87]\n" \
"v_min_u32_e32 v217, 0xa43f, v214\n" \
"v_mov_b32_e64 v214, s24\n" \
"v_mfma_f32_16x16x32_bf16 a[84:87], a[212:215], a[228:231], a[84:87]\n" \
"ds_read_b64_tr_b16 a[252:253], v39 offset:40960\n" \
"v_cndmask_b32_e64 v215, v216, v213, vcc\n" \
"v_mfma_f32_16x16x32_bf16 a[80:83], a[148:151], a[160:163], a[80:83]\n" \
"v_mov_b32_e32 v213, s22\n" \
"s_movk_i32 s6, 0xffee\n" \
"v_mfma_f32_16x16x32_bf16 a[80:83], a[212:215], a[224:227], a[80:83]\n" \
"v_cndmask_b32_e64 v214, v214, v213, vcc\n" \
"v_add_u32_e32 v213, 0xffffebc0, v217\n" \
"v_mfma_f32_16x16x32_bf16 a[96:99], a[152:155], a[160:163], a[96:99]\n" \
"ds_read_b64_tr_b16 a[254:255], v39 offset:41984\n" \
"s_lshl_b32 s5, s2, 5\n" \
"v_mfma_f32_16x16x32_bf16 a[96:99], a[216:219], a[224:227], a[96:99]\n" \
"v_cndmask_b32_e32 v213, v213, v217, vcc\n" \
"v_lshlrev_b32_e64 v216, 7, v213\n" \
"v_mfma_f32_16x16x32_bf16 a[100:103], a[152:155], a[164:167], a[100:103]\n" \
"v_mul_u32_u24_e32 v213, 0xe39, v212\n" \
"v_mov_b32_e64 v217, 0\n" \
"v_mfma_f32_16x16x32_bf16 a[100:103], a[216:219], a[228:231], a[100:103]\n" \
"v_lshrrev_b32_e64 v213, 16, v213\n" \
"v_lshl_add_u64 v[16:17], v[214:215], 0, v[216:217]\n" \
"v_mfma_f32_16x16x32_bf16 a[104:107], a[152:155], a[168:171], a[104:107]\n" \
"v_mad_i32_i24 v214, v213, s6, v212\n" \
"s_add_i32 s5, s5, 32\n" \
"v_mfma_f32_16x16x32_bf16 a[104:107], a[216:219], a[232:235], a[104:107]\n" \
"v_lshlrev_b32_e32 v215, 4, v214\n" \
"v_add_u32_e64 v216, s5, v215\n" \
"v_mfma_f32_16x16x32_bf16 a[108:111], a[152:155], a[172:175], a[108:111]\n" \
"v_and_b32_e32 v216, 0xf0, v216\n" \
"v_cmp_gt_u32_e64 vcc, 16, v214\n" \
"v_mfma_f32_16x16x32_bf16 a[108:111], a[216:219], a[236:239], a[108:111]\n" \
"s_movk_i32 s7, 0xf800\n" \
"v_cndmask_b32_e32 v214, v215, v216, vcc\n" \
"s_waitcnt lgkmcnt(15)\n" \
"v_mfma_f32_16x16x32_bf16 a[124:127], a[156:159], a[172:175], a[124:127]\n" \
"v_or_b32_e32 v215, 64, v212\n" \
"v_mul_u32_u24_e32 v216, 0xe39, v215\n" \
"v_mfma_f32_16x16x32_bf16 a[124:127], a[220:223], a[236:239], a[124:127]\n" \
"v_lshrrev_b32_e64 v216, 16, v216\n" \
"v_mad_i32_i24 v215, v216, s6, v215\n" \
"v_mfma_f32_16x16x32_bf16 a[120:123], a[156:159], a[168:171], a[120:123]\n" \
"v_lshlrev_b32_e32 v217, 4, v215\n" \
"v_add_u32_e32 v220, s5, v217\n" \
"v_mfma_f32_16x16x32_bf16 a[120:123], a[220:223], a[232:235], a[120:123]\n" \
"v_and_b32_e32 v220, 0xf0, v220\n" \
"v_cmp_gt_u32_e64 vcc, 16, v215\n" \
"v_mfma_f32_16x16x32_bf16 a[116:119], a[156:159], a[164:167], a[116:119]\n" \
"s_nop 0\n" \
"v_cndmask_b32_e32 v215, v217, v220, vcc\n" \
"v_or_b32_e32 v217, 0x80, v212\n" \
"v_mfma_f32_16x16x32_bf16 a[116:119], a[220:223], a[228:231], a[116:119]\n" \
"v_mul_u32_u24_e32 v220, 0xe39, v217\n" \
"v_lshrrev_b32_e64 v220, 16, v220\n" \
"v_mfma_f32_16x16x32_bf16 a[112:115], a[156:159], a[160:163], a[112:115]\n" \
"v_mad_i32_i24 v217, v220, s6, v217\n" \
"v_lshlrev_b32_e64 v221, 4, v217\n" \
"v_mfma_f32_16x16x32_bf16 a[112:115], a[220:223], a[224:227], a[112:115]\n" \
"s_waitcnt vmcnt(20)\n" \
"v_readfirstlane_b32 s62, v180\n" \
"s_nop 3\n" \
"s_add_u32 s62, s62, 0x9E3779B1\n" \
"s_waitcnt vmcnt(2) lgkmcnt(0)\n" \
"s_barrier\n" \
"v_mfma_f32_16x16x32_bf16 v[52:55], a[128:131], a[176:179], v[52:55]\n" \
"s_add_u32 m0, s30, 0x8000\n" \
"s_movk_i32 s84, 0x180\n" \
"v_mfma_f32_16x16x32_bf16 v[52:55], a[192:195], a[240:243], v[52:55]\n" \
"buffer_load_dwordx4 v22, s[64:67], s84 offen offset:0 lds\n" \
"v_mfma_f32_16x16x32_bf16 v[56:59], a[128:131], a[180:183], v[56:59]\n" \
"ds_read_b64_tr_b16 a[160:161], v32 offset:0\n" \
"v_add_u32_e64 v222, s5, v221\n" \
"v_mfma_f32_16x16x32_bf16 v[56:59], a[192:195], a[244:247], v[56:59]\n" \
"s_movk_i32 s84, 0x1d80\n" \
"v_mfma_f32_16x16x32_bf16 v[60:63], a[128:131], a[184:187], v[60:63]\n" \
"buffer_load_dwordx4 v23, s[64:67], s84 offen offset:1024 lds\n" \
"v_mfma_f32_16x16x32_bf16 v[60:63], a[192:195], a[248:251], v[60:63]\n" \
"ds_read_b64_tr_b16 a[162:163], v32 offset:1024\n" \
"v_and_b32_e32 v222, 0xf0, v222\n" \
"v_mfma_f32_16x16x32_bf16 v[64:67], a[128:131], a[188:191], v[64:67]\n" \
"ds_read_b64_tr_b16 a[224:225], v32 offset:8192\n" \
"v_cmp_gt_u32_e32 vcc, 16, v217\n" \
"v_mfma_f32_16x16x32_bf16 v[64:67], a[192:195], a[252:255], v[64:67]\n" \
"ds_read_b128 a[128:131], v28 offset:16384\n" \
"v_cndmask_b32_e64 v217, v221, v222, vcc\n" \
"v_mfma_f32_16x16x32_bf16 v[80:83], a[132:135], a[188:191], v[80:83]\n" \
"ds_read_b128 a[192:195], v29 offset:16384\n" \
"v_or_b32_e32 v221, 0xc0, v212\n" \
"v_mfma_f32_16x16x32_bf16 v[80:83], a[196:199], a[252:255], v[80:83]\n" \
"s_movk_i32 s84, 0x3980\n" \
"v_mfma_f32_16x16x32_bf16 v[76:79], a[132:135], a[184:187], v[76:79]\n" \
"buffer_load_dwordx4 v22, s[64:67], s84 offen offset:2048 lds\n" \
"v_mfma_f32_16x16x32_bf16 v[76:79], a[196:199], a[248:251], v[76:79]\n" \
"s_movk_i32 s84, 0x5580\n" \
"v_mfma_f32_16x16x32_bf16 v[72:75], a[132:135], a[180:183], v[72:75]\n" \
"buffer_load_dwordx4 v23, s[64:67], s84 offen offset:3072 lds\n" \
"v_mfma_f32_16x16x32_bf16 v[72:75], a[196:199], a[244:247], v[72:75]\n" \
"s_add_u32 m0, s31, 0x8000\n" \
"s_mov_b32 s84, 0x180000\n" \
"v_mfma_f32_16x16x32_bf16 v[68:71], a[132:135], a[176:179], v[68:71]\n" \
"buffer_load_dwordx4 v26, s[68:71], s84 offen offset:0 lds\n" \
"v_mfma_f32_16x16x32_bf16 v[68:71], a[196:199], a[240:243], v[68:71]\n" \
"ds_read_b128 a[132:135], v28 offset:18432\n" \
"v_mul_u32_u24_e32 v222, 0xe39, v221\n" \
"v_mfma_f32_16x16x32_bf16 v[84:87], a[136:139], a[176:179], v[84:87]\n" \
"ds_read_b128 a[196:199], v29 offset:18432\n" \
"v_lshrrev_b32_e64 v222, 16, v222\n" \
"v_mfma_f32_16x16x32_bf16 v[84:87], a[200:203], a[240:243], v[84:87]\n" \
"s_addk_i32 s84, 0x7c00\n" \
"v_mfma_f32_16x16x32_bf16 v[88:91], a[136:139], a[180:183], v[88:91]\n" \
"buffer_load_dwordx4 v26, s[68:71], s84 offen offset:1024 lds\n" \
"v_mfma_f32_16x16x32_bf16 v[88:91], a[200:203], a[244:247], v[88:91]\n" \
"ds_read_b64_tr_b16 a[226:227], v32 offset:9216\n" \
"v_mad_i32_i24 v221, v222, s6, v221\n" \
"v_mfma_f32_16x16x32_bf16 v[92:95], a[136:139], a[184:187], v[92:95]\n" \
"s_addk_i32 s84, 0x7c00\n" \
"v_mfma_f32_16x16x32_bf16 v[92:95], a[200:203], a[248:251], v[92:95]\n" \
"buffer_load_dwordx4 v27, s[68:71], s84 offen offset:2048 lds\n" \
"v_mfma_f32_16x16x32_bf16 v[96:99], a[136:139], a[188:191], v[96:99]\n" \
"ds_read_b64_tr_b16 a[164:165], v33 offset:0\n" \
"v_lshlrev_b32_e64 v223, 4, v221\n" \
"v_mfma_f32_16x16x32_bf16 v[96:99], a[200:203], a[252:255], v[96:99]\n" \
"ds_read_b128 a[136:139], v28 offset:20480\n" \
"v_add_u32_e64 v224, s5, v223\n" \
"v_mfma_f32_16x16x32_bf16 v[112:115], a[140:143], a[188:191], v[112:115]\n" \
"ds_read_b128 a[200:203], v29 offset:20480\n" \
"v_and_b32_e32 v224, 0xf0, v224\n" \
"v_mfma_f32_16x16x32_bf16 v[112:115], a[204:207], a[252:255], v[112:115]\n" \
"s_addk_i32 s84, 0x7c00\n" \
"v_mfma_f32_16x16x32_bf16 v[108:111], a[140:143], a[184:187], v[108:111]\n" \
"buffer_load_dwordx4 v27, s[68:71], s84 offen offset:3072 lds\n" \
"v_mfma_f32_16x16x32_bf16 v[108:111], a[204:207], a[248:251], v[108:111]\n" \
"s_add_u32 m0, s30, 0x9000\n" \
"s_mov_b32 s84, 0x8180\n" \
"v_mfma_f32_16x16x32_bf16 v[104:107], a[140:143], a[180:183], v[104:107]\n" \
"buffer_load_dwordx4 v22, s[64:67], s84 offen offset:0 lds\n" \
"v_mfma_f32_16x16x32_bf16 v[104:107], a[204:207], a[244:247], v[104:107]\n" \
"ds_read_b64_tr_b16 a[166:167], v33 offset:1024\n" \
"v_cmp_gt_u32_e32 vcc, 16, v221\n" \
"v_mfma_f32_16x16x32_bf16 v[100:103], a[140:143], a[176:179], v[100:103]\n" \
"ds_read_b64_tr_b16 a[228:229], v33 offset:8192\n" \
"v_cndmask_b32_e64 v221, v223, v224, vcc\n" \
"v_mfma_f32_16x16x32_bf16 v[100:103], a[204:207], a[240:243], v[100:103]\n" \
"ds_read_b128 a[140:143], v28 offset:22528\n" \
"v_or_b32_e32 v223, 0x100, v212\n" \
"v_mfma_f32_16x16x32_bf16 v[116:119], a[144:147], a[176:179], v[116:119]\n" \
"ds_read_b128 a[204:207], v29 offset:22528\n" \
"v_mul_u32_u24_e32 v224, 0xe39, v223\n" \
"v_mfma_f32_16x16x32_bf16 v[116:119], a[208:211], a[240:243], v[116:119]\n" \
"s_addk_i32 s84, 0x1c00\n" \
"v_mfma_f32_16x16x32_bf16 v[120:123], a[144:147], a[180:183], v[120:123]\n" \
"buffer_load_dwordx4 v23, s[64:67], s84 offen offset:1024 lds\n" \
"v_mfma_f32_16x16x32_bf16 v[120:123], a[208:211], a[244:247], v[120:123]\n" \
"s_addk_i32 s84, 0x1c00\n" \
"v_mfma_f32_16x16x32_bf16 v[124:127], a[144:147], a[184:187], v[124:127]\n" \
"buffer_load_dwordx4 v22, s[64:67], s84 offen offset:2048 lds\n" \
"v_mfma_f32_16x16x32_bf16 v[124:127], a[208:211], a[248:251], v[124:127]\n" \
"s_addk_i32 s84, 0x1c00\n" \
"v_mfma_f32_16x16x32_bf16 v[128:131], a[144:147], a[188:191], v[128:131]\n" \
"buffer_load_dwordx4 v23, s[64:67], s84 offen offset:3072 lds\n" \
"v_mfma_f32_16x16x32_bf16 v[128:131], a[208:211], a[252:255], v[128:131]\n" \
"ds_read_b128 a[144:147], v28 offset:0\n" \
"v_lshrrev_b32_e32 v224, 16, v224\n" \
"v_mfma_f32_16x16x32_bf16 v[144:147], a[148:151], a[188:191], v[144:147]\n" \
"ds_read_b128 a[208:211], v29 offset:0\n" \
"v_mad_i32_i24 v223, v224, s6, v223\n" \
"v_mfma_f32_16x16x32_bf16 v[144:147], a[212:215], a[252:255], v[144:147]\n" \
"s_add_u32 m0, s31, 0xc000\n" \
"s_mov_b32 s84, 0x180000\n" \
"v_mfma_f32_16x16x32_bf16 v[140:143], a[148:151], a[184:187], v[140:143]\n" \
"buffer_load_dwordx4 v26, s[72:75], s84 offen offset:0 lds\n" \
"v_mfma_f32_16x16x32_bf16 v[140:143], a[212:215], a[248:251], v[140:143]\n" \
"ds_read_b64_tr_b16 a[230:231], v33 offset:9216\n" \
"v_lshlrev_b32_e64 v225, 4, v223\n" \
"v_mfma_f32_16x16x32_bf16 v[136:139], a[148:151], a[180:183], v[136:139]\n" \
"s_addk_i32 s84, 0x7c00\n" \
"v_mfma_f32_16x16x32_bf16 v[136:139], a[212:215], a[244:247], v[136:139]\n" \
"buffer_load_dwordx4 v26, s[72:75], s84 offen offset:1024 lds\n" \
"v_mfma_f32_16x16x32_bf16 v[132:135], a[148:151], a[176:179], v[132:135]\n" \
"ds_read_b64_tr_b16 a[168:169], v34 offset:0\n" \
"v_add_u32_e32 v226, s5, v225\n" \
"v_mfma_f32_16x16x32_bf16 v[132:135], a[212:215], a[240:243], v[132:135]\n" \
"ds_read_b128 a[148:151], v28 offset:2048\n" \
"v_and_b32_e32 v226, 0xf0, v226\n" \
"v_mfma_f32_16x16x32_bf16 v[148:151], a[152:155], a[176:179], v[148:151]\n" \
"ds_read_b128 a[212:215], v29 offset:2048\n" \
"v_cmp_gt_u32_e64 vcc, 16, v223\n" \
"v_mfma_f32_16x16x32_bf16 v[148:151], a[216:219], a[240:243], v[148:151]\n" \
"ds_read_b64_tr_b16 a[170:171], v34 offset:1024\n" \
"v_cndmask_b32_e64 v223, v225, v226, vcc\n" \
"v_mfma_f32_16x16x32_bf16 v[152:155], a[152:155], a[180:183], v[152:155]\n" \
"ds_read_b64_tr_b16 a[232:233], v34 offset:8192\n" \
"v_or_b32_e32 v225, 0x140, v212\n" \
"v_mfma_f32_16x16x32_bf16 v[152:155], a[216:219], a[244:247], v[152:155]\n" \
"ds_read_b64_tr_b16 a[234:235], v34 offset:9216\n" \
"v_mul_u32_u24_e32 v226, 0xe39, v225\n" \
"v_mfma_f32_16x16x32_bf16 v[156:159], a[152:155], a[184:187], v[156:159]\n" \
"ds_read_b64_tr_b16 a[172:173], v35 offset:0\n" \
"v_lshrrev_b32_e64 v226, 16, v226\n" \
"v_mfma_f32_16x16x32_bf16 v[156:159], a[216:219], a[248:251], v[156:159]\n" \
"ds_read_b64_tr_b16 a[174:175], v35 offset:1024\n" \
"v_mad_i32_i24 v225, v226, s6, v225\n" \
"v_mfma_f32_16x16x32_bf16 v[160:163], a[152:155], a[188:191], v[160:163]\n" \
"ds_read_b64_tr_b16 a[236:237], v35 offset:8192\n" \
"v_lshlrev_b32_e64 v227, 4, v225\n" \
"v_mfma_f32_16x16x32_bf16 v[160:163], a[216:219], a[252:255], v[160:163]\n" \
"ds_read_b128 a[152:155], v28 offset:4096\n" \
"v_add_u32_e64 v228, s5, v227\n" \
"v_mfma_f32_16x16x32_bf16 v[176:179], a[156:159], a[188:191], v[176:179]\n" \
"ds_read_b128 a[216:219], v29 offset:4096\n" \
"v_and_b32_e32 v228, 0xf0, v228\n" \
"v_mfma_f32_16x16x32_bf16 v[176:179], a[220:223], a[252:255], v[176:179]\n" \
"ds_read_b64_tr_b16 a[238:239], v35 offset:9216\n" \
"v_cmp_gt_u32_e64 vcc, 16, v225\n" \
"v_mfma_f32_16x16x32_bf16 v[172:175], a[156:159], a[184:187], v[172:175]\n" \
"s_nop 0\n" \
"v_cndmask_b32_e32 v225, v227, v228, vcc\n" \
"v_or_b32_e32 v227, 0x180, v212\n" \
"v_mfma_f32_16x16x32_bf16 v[172:175], a[220:223], a[248:251], v[172:175]\n" \
"v_mul_u32_u24_e32 v228, 0xe39, v227\n" \
"v_lshrrev_b32_e64 v228, 16, v228\n" \
"v_mfma_f32_16x16x32_bf16 v[168:171], a[156:159], a[180:183], v[168:171]\n" \
"v_mad_i32_i24 v227, v228, s6, v227\n" \
"v_lshlrev_b32_e64 v229, 4, v227\n" \
"v_mfma_f32_16x16x32_bf16 v[168:171], a[220:223], a[244:247], v[168:171]\n" \
"v_add_u32_e64 v230, s5, v229\n" \
"v_and_b32_e32 v230, 0xf0, v230\n" \
"v_mfma_f32_16x16x32_bf16 v[164:167], a[156:159], a[176:179], v[164:167]\n" \
"v_cmp_gt_u32_e32 vcc, 16, v227\n" \
"s_nop 1\n" \
"v_cndmask_b32_e64 v227, v229, v230, vcc\n" \
"v_mfma_f32_16x16x32_bf16 v[164:167], a[220:223], a[240:243], v[164:167]\n" \
"ds_read_b128 a[156:159], v28 offset:6144\n" \
"ds_read_b128 a[220:223], v29 offset:6144\n" \
"s_waitcnt lgkmcnt(15)\n" \
"v_mfma_f32_16x16x32_bf16 a[0:3], a[128:131], a[160:163], a[0:3]\n" \
"s_addk_i32 s84, 0x7c00\n" \
"v_mfma_f32_16x16x32_bf16 a[0:3], a[192:195], a[224:227], a[0:3]\n" \
"buffer_load_dwordx4 v27, s[72:75], s84 offen offset:2048 lds\n" \
"v_mfma_f32_16x16x32_bf16 a[4:7], a[128:131], a[164:167], a[4:7]\n" \
"ds_read_b64_tr_b16 a[176:177], v36 offset:0\n" \
"v_or_b32_e32 v229, 0x1c0, v212\n" \
"s_waitcnt lgkmcnt(15)\n" \
"v_mfma_f32_16x16x32_bf16 a[4:7], a[192:195], a[228:231], a[4:7]\n" \
"s_addk_i32 s84, 0x7c00\n" \
"s_waitcnt lgkmcnt(11)\n" \
"v_mfma_f32_16x16x32_bf16 a[8:11], a[128:131], a[168:171], a[8:11]\n" \
"buffer_load_dwordx4 v27, s[72:75], s84 offen offset:3072 lds\n" \
"s_waitcnt lgkmcnt(9)\n" \
"v_mfma_f32_16x16x32_bf16 a[8:11], a[192:195], a[232:235], a[8:11]\n" \
"ds_read_b64_tr_b16 a[178:179], v36 offset:1024\n" \
"v_mul_u32_u24_e32 v230, 0xe39, v229\n" \
"s_waitcnt lgkmcnt(8)\n" \
"v_mfma_f32_16x16x32_bf16 a[12:15], a[128:131], a[172:175], a[12:15]\n" \
"ds_read_b64_tr_b16 a[240:241], v36 offset:8192\n" \
"v_lshrrev_b32_e32 v230, 16, v230\n" \
"s_waitcnt lgkmcnt(5)\n" \
"v_mfma_f32_16x16x32_bf16 a[12:15], a[192:195], a[236:239], a[12:15]\n" \
"v_mad_i32_i24 v229, v230, s6, v229\n" \
"v_lshlrev_b32_e32 v231, 4, v229\n" \
"v_mfma_f32_16x16x32_bf16 a[28:31], a[132:135], a[172:175], a[28:31]\n" \
"v_add_u32_e64 v232, s5, v231\n" \
"v_and_b32_e32 v232, 0xf0, v232\n" \
"v_mfma_f32_16x16x32_bf16 a[28:31], a[196:199], a[236:239], a[28:31]\n" \
"ds_read_b64_tr_b16 a[242:243], v36 offset:9216\n" \
"v_cmp_gt_u32_e64 vcc, 16, v229\n" \
"v_mfma_f32_16x16x32_bf16 a[24:27], a[132:135], a[168:171], a[24:27]\n" \
"buffer_load_dwordx4 v[200:203], v192, s[76:79], s34 offen offset:0\n" \
"v_mfma_f32_16x16x32_bf16 a[24:27], a[196:199], a[232:235], a[24:27]\n" \
"v_cndmask_b32_e64 v229, v231, v232, vcc\n" \
"v_or_b32_e32 v231, 0x200, v212\n" \
"v_mfma_f32_16x16x32_bf16 a[20:23], a[132:135], a[164:167], a[20:23]\n" \
"v_mul_u32_u24_e32 v232, 0xe39, v231\n" \
"v_lshrrev_b32_e64 v232, 16, v232\n" \
"v_mfma_f32_16x16x32_bf16 a[20:23], a[196:199], a[228:231], a[20:23]\n" \
"ds_read_b64_tr_b16 a[180:181], v37 offset:0\n" \
"v_mad_i32_i24 v231, v232, s6, v231\n" \
"v_mfma_f32_16x16x32_bf16 a[16:19], a[132:135], a[160:163], a[16:19]\n" \
"v_lshlrev_b32_e32 v233, 4, v231\n" \
"v_add_u32_e32 v234, s5, v233\n" \
"v_mfma_f32_16x16x32_bf16 a[16:19], a[196:199], a[224:227], a[16:19]\n" \
"s_movk_i32 s5, 0x480\n" \
"v_mad_u32_u24 v0, v213, s5, v214\n" \
"v_mfma_f32_16x16x32_bf16 a[32:35], a[136:139], a[160:163], a[32:35]\n" \
"buffer_load_dwordx4 v[204:207], v193, s[76:79], s34 offen offset:0\n" \
"v_mfma_f32_16x16x32_bf16 a[32:35], a[200:203], a[224:227], a[32:35]\n" \
"s_mov_b32 m0, s32\n" \
"ds_read_b64_tr_b16 a[182:183], v37 offset:1024\n" \
"v_mfma_f32_16x16x32_bf16 a[36:39], a[136:139], a[164:167], a[36:39]\n" \
"s_mov_b32 exec_lo, 0x0\n" \
"s_mov_b32 exec_hi, 0xff00\n" \
"global_load_lds_dword v[184:185], off\n" \
"s_mov_b64 exec, -1\n" \
"v_mfma_f32_16x16x32_bf16 a[36:39], a[200:203], a[228:231], a[36:39]\n" \
"ds_read_b64_tr_b16 a[244:245], v37 offset:8192\n" \
"v_mul_u32_u24_e32 v213, 0x480, v216\n" \
"v_mfma_f32_16x16x32_bf16 a[40:43], a[136:139], a[168:171], a[40:43]\n" \
"s_movk_i32 s6, 0xfc00\n" \
"s_nop 0\n" \
"v_add3_u32 v1, v213, v215, s6\n" \
"v_mfma_f32_16x16x32_bf16 a[40:43], a[200:203], a[232:235], a[40:43]\n" \
"buffer_load_dword v208, v194, s[76:79], s34 offen offset:32\n" \
"v_mfma_f32_16x16x32_bf16 a[44:47], a[136:139], a[172:175], a[44:47]\n" \
"ds_read_b64_tr_b16 a[246:247], v37 offset:9216\n" \
"v_mul_u32_u24_e32 v213, 0x480, v220\n" \
"v_mfma_f32_16x16x32_bf16 a[44:47], a[200:203], a[236:239], a[44:47]\n" \
"v_add3_u32 v2, v213, v217, s7\n" \
"v_mul_u32_u24_e32 v213, 0x480, v222\n" \
"v_mfma_f32_16x16x32_bf16 a[60:63], a[140:143], a[172:175], a[60:63]\n" \
"v_add3_u32 v3, v213, v221, s40\n" \
"v_mul_u32_u24_e32 v213, 0x480, v226\n" \
"v_mfma_f32_16x16x32_bf16 a[60:63], a[204:207], a[236:239], a[60:63]\n" \
"v_add3_u32 v5, v213, v225, s6\n" \
"v_mul_u32_u24_e32 v213, 0x480, v228\n" \
"v_mfma_f32_16x16x32_bf16 a[56:59], a[140:143], a[168:171], a[56:59]\n" \
"buffer_load_dword v209, v194, s[76:79], s34 offen offset:1184\n" \
"v_mfma_f32_16x16x32_bf16 a[56:59], a[204:207], a[232:235], a[56:59]\n" \
"ds_read_b64_tr_b16 a[184:185], v38 offset:0\n" \
"v_add3_u32 v6, v213, v227, s7\n" \
"v_mfma_f32_16x16x32_bf16 a[52:55], a[140:143], a[164:167], a[52:55]\n" \
"v_mul_u32_u24_e32 v213, 0x480, v230\n" \
"v_add3_u32 v7, v213, v229, s40\n" \
"v_mfma_f32_16x16x32_bf16 a[52:55], a[204:207], a[228:231], a[52:55]\n" \
"ds_read_b64_tr_b16 a[186:187], v38 offset:1024\n" \
"s_lshl_b32 s7, s3, 3\n" \
"v_mfma_f32_16x16x32_bf16 a[48:51], a[140:143], a[160:163], a[48:51]\n" \
"s_lshl_b32 s40, s3, 1\n" \
"s_and_b32 s7, s7, 16\n" \
"v_mfma_f32_16x16x32_bf16 a[48:51], a[204:207], a[224:227], a[48:51]\n" \
"s_and_b32 s40, s40, 2\n" \
"s_and_b32 s6, s2, 7\n" \
"v_mfma_f32_16x16x32_bf16 a[64:67], a[144:147], a[160:163], a[64:67]\n" \
"buffer_load_dword v211, v194, s[76:79], s34 offen offset:2336\n" \
"v_mfma_f32_16x16x32_bf16 a[64:67], a[208:211], a[224:227], a[64:67]\n" \
"ds_read_b64_tr_b16 a[248:249], v38 offset:8192\n" \
"v_bfe_u32 v213, v20, 7, 1\n" \
"v_mfma_f32_16x16x32_bf16 a[68:71], a[144:147], a[164:167], a[68:71]\n" \
"s_or_b32 s7, s40, s7\n" \
"v_and_b32_e32 v234, 0xf0, v234\n" \
"v_mfma_f32_16x16x32_bf16 a[68:71], a[208:211], a[228:231], a[68:71]\n" \
"ds_read_b64_tr_b16 a[250:251], v38 offset:9216\n" \
"v_cmp_gt_u32_e64 vcc, 16, v231\n" \
"v_mfma_f32_16x16x32_bf16 a[72:75], a[144:147], a[168:171], a[72:75]\n" \
"v_or3_b32 v213, s7, v213, v219\n" \
"v_and_b32_e64 v214, 32, v20\n" \
"v_mfma_f32_16x16x32_bf16 a[72:75], a[208:211], a[232:235], a[72:75]\n" \
"s_mov_b32 s76, s20\n" \
"s_and_b32 s77, s21, 0xffff\n" \
"v_mfma_f32_16x16x32_bf16 a[76:79], a[144:147], a[172:175], a[76:79]\n" \
"s_mov_b32 s78, 0x480\n" \
"s_lshl_b32 s6, s6, 5\n" \
"v_mfma_f32_16x16x32_bf16 a[76:79], a[208:211], a[236:239], a[76:79]\n" \
"buffer_load_ushort v210, v195, s[76:79], 0 offen\n" \
"v_mfma_f32_16x16x32_bf16 a[92:95], a[148:151], a[172:175], a[92:95]\n" \
"ds_read_b64_tr_b16 a[188:189], v39 offset:0\n" \
"v_cndmask_b32_e64 v231, v233, v234, vcc\n" \
"v_mfma_f32_16x16x32_bf16 a[92:95], a[212:215], a[236:239], a[92:95]\n" \
"v_mul_u32_u24_e64 v213, 18, v213\n" \
"v_mov_b32_e32 v215, 0x100\n" \
"v_mfma_f32_16x16x32_bf16 a[88:91], a[148:151], a[168:171], a[88:91]\n" \
"s_mov_b32 s76, s8\n" \
"s_and_b32 s77, s9, 0xffff\n" \
"v_mfma_f32_16x16x32_bf16 a[88:91], a[212:215], a[232:235], a[88:91]\n" \
"s_mov_b32 s78, 0x240000\n" \
"ds_read_b64_tr_b16 a[190:191], v39 offset:1024\n" \
".fill (L_pb + 48864 - .) / 4, 4, 0xBF800000\n" \
"L_pp12:\n" \
"s_cbranch_execz L_pret\n" \
"v_mfma_f32_16x16x32_bf16 a[84:87], a[148:151], a[164:167], a[84:87]\n" \
"v_mov_b32_e32 v216, s6\n" \
"v_cmp_eq_u32_e64 vcc, 0, v214\n" \
"v_mfma_f32_16x16x32_bf16 a[84:87], a[212:215], a[228:231], a[84:87]\n" \
"ds_read_b64_tr_b16 a[252:253], v39 offset:8192\n" \
"v_and_or_b32 v213, v218, 1, v213\n" \
"v_mfma_f32_16x16x32_bf16 a[80:83], a[148:151], a[160:163], a[80:83]\n" \
"v_lshlrev_b32_e32 v212, 3, v212\n" \
"v_cndmask_b32_e32 v214, v215, v216, vcc\n" \
"v_mfma_f32_16x16x32_bf16 a[80:83], a[212:215], a[224:227], a[80:83]\n" \
"v_mad_u32_u24 v213, v213, s5, v214\n" \
"v_and_or_b32 v213, v20, 16, v213\n" \
"v_mfma_f32_16x16x32_bf16 a[96:99], a[152:155], a[160:163], a[96:99]\n" \
"ds_read_b64_tr_b16 a[254:255], v39 offset:9216\n" \
"v_add_u32_e32 v9, 0x4800, v213\n" \
"v_mfma_f32_16x16x32_bf16 a[96:99], a[216:219], a[224:227], a[96:99]\n" \
"v_mul_u32_u24_e32 v213, 0x120, v218\n" \
"v_and_or_b32 v10, v212, 24, v213\n" \
"v_mfma_f32_16x16x32_bf16 a[100:103], a[152:155], a[164:167], a[100:103]\n" \
"v_lshrrev_b32_e64 v213, 2, v20\n" \
"v_and_or_b32 v213, v213, 60, s4\n" \
"v_mfma_f32_16x16x32_bf16 a[100:103], a[216:219], a[228:231], a[100:103]\n" \
"v_lshl_or_b32 v212, s3, 9, v212\n" \
"v_mad_u32_u24 v4, v224, s5, v223\n" \
"v_mfma_f32_16x16x32_bf16 a[104:107], a[152:155], a[168:171], a[104:107]\n" \
"v_mad_u32_u24 v8, v232, s5, v231\n" \
"v_or_b32_e32 v18, 0x27000, v213\n" \
"v_mfma_f32_16x16x32_bf16 a[104:107], a[216:219], a[232:235], a[104:107]\n" \
"v_or_b32_e32 v19, 0x27800, v212\n" \
"v_mov_b32_e32 v215, 0x1000c0c\n" \
"v_mfma_f32_16x16x32_bf16 a[108:111], a[152:155], a[172:175], a[108:111]\n" \
"v_mov_b32_e32 v216, 0xc0c0100\n" \
"v_bfe_u32 v214, v20, 4, 1\n" \
"v_mfma_f32_16x16x32_bf16 a[108:111], a[216:219], a[236:239], a[108:111]\n" \
"v_lshrrev_b32_e32 v212, 8, v20\n" \
"v_cmp_eq_u32_e32 vcc, 0, v214\n" \
"s_waitcnt lgkmcnt(15)\n" \
"v_mfma_f32_16x16x32_bf16 a[124:127], a[156:159], a[172:175], a[124:127]\n" \
"v_bfe_u32 v213, v20, 4, 4\n" \
"v_mov_b32_e32 v217, 0xc0c0302\n" \
"v_mfma_f32_16x16x32_bf16 a[124:127], a[220:223], a[236:239], a[124:127]\n" \
"v_cndmask_b32_e32 v215, v215, v216, vcc\n" \
"v_mov_b32_e32 v216, 0x3020c0c\n" \
"v_mfma_f32_16x16x32_bf16 a[120:123], a[156:159], a[168:171], a[120:123]\n" \
"v_lshl_or_b32 v214, v212, 2, v214\n" \
"v_cndmask_b32_e64 v216, v216, v217, vcc\n" \
"v_mfma_f32_16x16x32_bf16 a[120:123], a[220:223], a[232:235], a[120:123]\n" \
"v_mov_b32_e32 v217, 0xc0c0c0c\n" \
"v_cmp_gt_u32_e64 vcc, v214, v213\n" \
"v_mfma_f32_16x16x32_bf16 a[116:119], a[156:159], a[164:167], a[116:119]\n" \
"v_or_b32_e32 v214, 2, v214\n" \
"s_lshr_b32 s4, s2, 5\n" \
"v_mfma_f32_16x16x32_bf16 a[116:119], a[220:223], a[228:231], a[116:119]\n" \
"v_cndmask_b32_e32 v40, v217, v215, vcc\n" \
"v_cndmask_b32_e32 v42, v215, v217, vcc\n" \
"v_mfma_f32_16x16x32_bf16 a[112:115], a[156:159], a[160:163], a[112:115]\n" \
"v_cmp_gt_u32_e32 vcc, v214, v213\n" \
"v_and_b32_e32 v214, 16, v20\n" \
"s_waitcnt vmcnt(7) lgkmcnt(0)\n" \
"v_mfma_f32_16x16x32_bf16 a[112:115], a[220:223], a[224:227], a[112:115]\n" \
"s_barrier\n" \
"v_mfma_f32_16x16x32_bf16 v[52:55], a[128:131], a[176:179], v[52:55]\n" \
"s_add_u32 m0, s30, 0x0\n" \
"s_movk_i32 s84, 0x200\n" \
"v_mfma_f32_16x16x32_bf16 v[52:55], a[192:195], a[240:243], v[52:55]\n" \
"buffer_load_dwordx4 v22, s[64:67], s84 offen offset:0 lds\n" \
"v_mfma_f32_16x16x32_bf16 v[56:59], a[128:131], a[180:183], v[56:59]\n" \
"ds_read_b64_tr_b16 a[160:161], v32 offset:32768\n" \
"s_movk_i32 s42, 0x3f80\n" \
"v_mfma_f32_16x16x32_bf16 v[56:59], a[192:195], a[244:247], v[56:59]\n" \
"s_movk_i32 s84, 0x1e00\n" \
"v_mfma_f32_16x16x32_bf16 v[60:63], a[128:131], a[184:187], v[60:63]\n" \
"buffer_load_dwordx4 v23, s[64:67], s84 offen offset:1024 lds\n" \
"v_mfma_f32_16x16x32_bf16 v[60:63], a[192:195], a[248:251], v[60:63]\n" \
"ds_read_b64_tr_b16 a[162:163], v32 offset:33792\n" \
"s_xor_b32 s5, s4, 15\n" \
"v_mfma_f32_16x16x32_bf16 v[64:67], a[128:131], a[188:191], v[64:67]\n" \
"ds_read_b64_tr_b16 a[224:225], v32 offset:40960\n" \
"v_mfma_f32_16x16x32_bf16 v[64:67], a[192:195], a[252:255], v[64:67]\n" \
"ds_read_b128 a[128:131], v28 offset:49152\n" \
"v_mfma_f32_16x16x32_bf16 v[80:83], a[132:135], a[188:191], v[80:83]\n" \
"ds_read_b128 a[192:195], v29 offset:49152\n" \
"v_mfma_f32_16x16x32_bf16 v[80:83], a[196:199], a[252:255], v[80:83]\n" \
"s_movk_i32 s84, 0x3a00\n" \
"v_mfma_f32_16x16x32_bf16 v[76:79], a[132:135], a[184:187], v[76:79]\n" \
"buffer_load_dwordx4 v22, s[64:67], s84 offen offset:2048 lds\n" \
"v_mfma_f32_16x16x32_bf16 v[76:79], a[196:199], a[248:251], v[76:79]\n" \
"s_movk_i32 s84, 0x5600\n" \
"v_mfma_f32_16x16x32_bf16 v[72:75], a[132:135], a[180:183], v[72:75]\n" \
"buffer_load_dwordx4 v23, s[64:67], s84 offen offset:3072 lds\n" \
"v_mfma_f32_16x16x32_bf16 v[72:75], a[196:199], a[244:247], v[72:75]\n" \
"s_add_u32 m0, s31, 0x0\n" \
"s_mov_b32 s84, 0x200000\n" \
"v_mfma_f32_16x16x32_bf16 v[68:71], a[132:135], a[176:179], v[68:71]\n" \
"buffer_load_dwordx4 v26, s[68:71], s84 offen offset:0 lds\n" \
"v_mfma_f32_16x16x32_bf16 v[68:71], a[196:199], a[240:243], v[68:71]\n" \
"ds_read_b128 a[132:135], v28 offset:51200\n" \
"v_mfma_f32_16x16x32_bf16 v[84:87], a[136:139], a[176:179], v[84:87]\n" \
"ds_read_b128 a[196:199], v29 offset:51200\n" \
"v_mfma_f32_16x16x32_bf16 v[84:87], a[200:203], a[240:243], v[84:87]\n" \
"s_addk_i32 s84, 0x7c00\n" \
"v_mfma_f32_16x16x32_bf16 v[88:91], a[136:139], a[180:183], v[88:91]\n" \
"buffer_load_dwordx4 v26, s[68:71], s84 offen offset:1024 lds\n" \
"v_mfma_f32_16x16x32_bf16 v[88:91], a[200:203], a[244:247], v[88:91]\n" \
"ds_read_b64_tr_b16 a[226:227], v32 offset:41984\n" \
"v_mfma_f32_16x16x32_bf16 v[92:95], a[136:139], a[184:187], v[92:95]\n" \
"s_addk_i32 s84, 0x7c00\n" \
"v_mfma_f32_16x16x32_bf16 v[92:95], a[200:203], a[248:251], v[92:95]\n" \
"buffer_load_dwordx4 v27, s[68:71], s84 offen offset:2048 lds\n" \
"v_mfma_f32_16x16x32_bf16 v[96:99], a[136:139], a[188:191], v[96:99]\n" \
"ds_read_b64_tr_b16 a[164:165], v33 offset:32768\n" \
"v_mfma_f32_16x16x32_bf16 v[96:99], a[200:203], a[252:255], v[96:99]\n" \
"ds_read_b128 a[136:139], v28 offset:53248\n" \
"v_mfma_f32_16x16x32_bf16 v[112:115], a[140:143], a[188:191], v[112:115]\n" \
"ds_read_b128 a[200:203], v29 offset:53248\n" \
"v_mfma_f32_16x16x32_bf16 v[112:115], a[204:207], a[252:255], v[112:115]\n" \
"s_addk_i32 s84, 0x7c00\n" \
"s_nop 0\n" \
"v_mfma_f32_16x16x32_bf16 v[108:111], a[140:143], a[184:187], v[108:111]\n" \
"buffer_load_dwordx4 v27, s[68:71], s84 offen offset:3072 lds\n" \
"v_mfma_f32_16x16x32_bf16 v[108:111], a[204:207], a[248:251], v[108:111]\n" \
"s_add_u32 m0, s30, 0x1000\n" \
"s_mov_b32 s84, 0x8200\n" \
"v_mfma_f32_16x16x32_bf16 v[104:107], a[140:143], a[180:183], v[104:107]\n" \
"buffer_load_dwordx4 v22, s[64:67], s84 offen offset:0 lds\n" \
"v_mfma_f32_16x16x32_bf16 v[104:107], a[204:207], a[244:247], v[104:107]\n" \
"ds_read_b64_tr_b16 a[166:167], v33 offset:33792\n" \
"v_mfma_f32_16x16x32_bf16 v[100:103], a[140:143], a[176:179], v[100:103]\n" \
"ds_read_b64_tr_b16 a[228:229], v33 offset:40960\n" \
"v_mfma_f32_16x16x32_bf16 v[100:103], a[204:207], a[240:243], v[100:103]\n" \
"ds_read_b128 a[140:143], v28 offset:55296\n" \
"v_mfma_f32_16x16x32_bf16 v[116:119], a[144:147], a[176:179], v[116:119]\n" \
"ds_read_b128 a[204:207], v29 offset:55296\n" \
"v_mfma_f32_16x16x32_bf16 v[116:119], a[208:211], a[240:243], v[116:119]\n" \
"s_addk_i32 s84, 0x1c00\n" \
"v_mfma_f32_16x16x32_bf16 v[120:123], a[144:147], a[180:183], v[120:123]\n" \
"buffer_load_dwordx4 v23, s[64:67], s84 offen offset:1024 lds\n" \
"v_mfma_f32_16x16x32_bf16 v[120:123], a[208:211], a[244:247], v[120:123]\n" \
"s_addk_i32 s84, 0x1c00\n" \
"v_mfma_f32_16x16x32_bf16 v[124:127], a[144:147], a[184:187], v[124:127]\n" \
"buffer_load_dwordx4 v22, s[64:67], s84 offen offset:2048 lds\n" \
"v_mfma_f32_16x16x32_bf16 v[124:127], a[208:211], a[248:251], v[124:127]\n" \
"s_addk_i32 s84, 0x1c00\n" \
"s_nop 0\n" \
"v_mfma_f32_16x16x32_bf16 v[128:131], a[144:147], a[188:191], v[128:131]\n" \
"buffer_load_dwordx4 v23, s[64:67], s84 offen offset:3072 lds\n" \
"v_mfma_f32_16x16x32_bf16 v[128:131], a[208:211], a[252:255], v[128:131]\n" \
"ds_read_b128 a[144:147], v28 offset:32768\n" \
"v_mfma_f32_16x16x32_bf16 v[144:147], a[148:151], a[188:191], v[144:147]\n" \
"ds_read_b128 a[208:211], v29 offset:32768\n" \
"v_mfma_f32_16x16x32_bf16 v[144:147], a[212:215], a[252:255], v[144:147]\n" \
"s_add_u32 m0, s31, 0x4000\n" \
"s_mov_b32 s84, 0x200000\n" \
"v_mfma_f32_16x16x32_bf16 v[140:143], a[148:151], a[184:187], v[140:143]\n" \
"buffer_load_dwordx4 v26, s[72:75], s84 offen offset:0 lds\n" \
"v_mfma_f32_16x16x32_bf16 v[140:143], a[212:215], a[248:251], v[140:143]\n" \
"ds_read_b64_tr_b16 a[230:231], v33 offset:41984\n" \
"v_mfma_f32_16x16x32_bf16 v[136:139], a[148:151], a[180:183], v[136:139]\n" \
"s_addk_i32 s84, 0x7c00\n" \
"s_nop 0\n" \
"v_mfma_f32_16x16x32_bf16 v[136:139], a[212:215], a[244:247], v[136:139]\n" \
"buffer_load_dwordx4 v26, s[72:75], s84 offen offset:1024 lds\n" \
"v_mfma_f32_16x16x32_bf16 v[132:135], a[148:151], a[176:179], v[132:135]\n" \
"ds_read_b64_tr_b16 a[168:169], v34 offset:32768\n" \
"v_mfma_f32_16x16x32_bf16 v[132:135], a[212:215], a[240:243], v[132:135]\n" \
"ds_read_b128 a[148:151], v28 offset:34816\n" \
"v_mfma_f32_16x16x32_bf16 v[148:151], a[152:155], a[176:179], v[148:151]\n" \
"ds_read_b128 a[212:215], v29 offset:34816\n" \
"v_mfma_f32_16x16x32_bf16 v[148:151], a[216:219], a[240:243], v[148:151]\n" \
"ds_read_b64_tr_b16 a[170:171], v34 offset:33792\n" \
"v_mfma_f32_16x16x32_bf16 v[152:155], a[152:155], a[180:183], v[152:155]\n" \
"ds_read_b64_tr_b16 a[232:233], v34 offset:40960\n" \
"v_mfma_f32_16x16x32_bf16 v[152:155], a[216:219], a[244:247], v[152:155]\n" \
"ds_read_b64_tr_b16 a[234:235], v34 offset:41984\n" \
"v_mfma_f32_16x16x32_bf16 v[156:159], a[152:155], a[184:187], v[156:159]\n" \
"ds_read_b64_tr_b16 a[172:173], v35 offset:32768\n" \
"v_mfma_f32_16x16x32_bf16 v[156:159], a[216:219], a[248:251], v[156:159]\n" \
"ds_read_b64_tr_b16 a[174:175], v35 offset:33792\n" \
"v_mfma_f32_16x16x32_bf16 v[160:163], a[152:155], a[188:191], v[160:163]\n" \
"ds_read_b64_tr_b16 a[236:237], v35 offset:40960\n" \
"v_mfma_f32_16x16x32_bf16 v[160:163], a[216:219], a[252:255], v[160:163]\n" \
"ds_read_b128 a[152:155], v28 offset:36864\n" \
"v_mfma_f32_16x16x32_bf16 v[176:179], a[156:159], a[188:191], v[176:179]\n" \
"ds_read_b128 a[216:219], v29 offset:36864\n" \
"v_mfma_f32_16x16x32_bf16 v[176:179], a[220:223], a[252:255], v[176:179]\n" \
"ds_read_b64_tr_b16 a[238:239], v35 offset:41984\n" \
"v_mfma_f32_16x16x32_bf16 v[172:175], a[156:159], a[184:187], v[172:175]\n" \
"s_cmpk_lt_u32 s3, 2\n" \
"s_cselect_b32 s6, s5, s4\n" \
"v_mfma_f32_16x16x32_bf16 v[172:175], a[220:223], a[248:251], v[172:175]\n" \
"s_lshl_b32 s7, s6, 7\n" \
"v_and_b32_e32 v213, 0x320, v20\n" \
"v_mfma_f32_16x16x32_bf16 v[168:171], a[156:159], a[180:183], v[168:171]\n" \
"s_movk_i32 s41, 0x100\n" \
"v_lshlrev_b32_e64 v214, v214, s42\n" \
"v_mfma_f32_16x16x32_bf16 v[168:171], a[220:223], a[244:247], v[168:171]\n" \
"v_mov_b32_e32 v215, 0\n" \
"s_add_i32 s40, s7, -16\n" \
"v_mfma_f32_16x16x32_bf16 v[164:167], a[156:159], a[176:179], v[164:167]\n" \
"v_cndmask_b32_e32 v41, v217, v216, vcc\n" \
"v_cndmask_b32_e32 v43, v216, v217, vcc\n" \
"v_mfma_f32_16x16x32_bf16 v[164:167], a[220:223], a[240:243], v[164:167]\n" \
"ds_read_b128 a[156:159], v28 offset:38912\n" \
"ds_read_b128 a[220:223], v29 offset:38912\n" \
"s_waitcnt lgkmcnt(15)\n" \
"v_mfma_f32_16x16x32_bf16 a[0:3], a[128:131], a[160:163], a[0:3]\n" \
"s_addk_i32 s84, 0x7c00\n" \
"v_mfma_f32_16x16x32_bf16 a[0:3], a[192:195], a[224:227], a[0:3]\n" \
"buffer_load_dwordx4 v27, s[72:75], s84 offen offset:2048 lds\n" \
"v_mfma_f32_16x16x32_bf16 a[4:7], a[128:131], a[164:167], a[4:7]\n" \
"ds_read_b64_tr_b16 a[176:177], v36 offset:32768\n" \
"v_cmp_eq_u32_e32 vcc, s41, v213\n" \
"s_waitcnt lgkmcnt(15)\n" \
"v_mfma_f32_16x16x32_bf16 a[4:7], a[192:195], a[228:231], a[4:7]\n" \
"s_addk_i32 s84, 0x7c00\n" \
"s_waitcnt lgkmcnt(11)\n" \
"v_mfma_f32_16x16x32_bf16 a[8:11], a[128:131], a[168:171], a[8:11]\n" \
"buffer_load_dwordx4 v27, s[72:75], s84 offen offset:3072 lds\n" \
"s_waitcnt lgkmcnt(9)\n" \
"v_mfma_f32_16x16x32_bf16 a[8:11], a[192:195], a[232:235], a[8:11]\n" \
"ds_read_b64_tr_b16 a[178:179], v36 offset:33792\n" \
"s_mov_b32 s41, 0\n" \
"s_waitcnt lgkmcnt(8)\n" \
"v_mfma_f32_16x16x32_bf16 a[12:15], a[128:131], a[172:175], a[12:15]\n" \
"ds_read_b64_tr_b16 a[240:241], v36 offset:40960\n" \
"s_and_b32 s43, s3, 1\n" \
"s_waitcnt lgkmcnt(5)\n" \
"v_mfma_f32_16x16x32_bf16 a[12:15], a[192:195], a[236:239], a[12:15]\n" \
"v_cndmask_b32_e32 v46, v215, v214, vcc\n" \
"v_and_b32_e32 v213, 0x330, v20\n" \
"v_mfma_f32_16x16x32_bf16 a[28:31], a[132:135], a[172:175], a[28:31]\n" \
"s_cmp_lg_u32 s7, 0\n" \
"s_cselect_b32 s40, s40, s41\n" \
"v_mfma_f32_16x16x32_bf16 a[28:31], a[196:199], a[236:239], a[28:31]\n" \
"ds_read_b64_tr_b16 a[242:243], v36 offset:41984\n" \
"s_mov_b32 s42, 0xff800000\n" \
"v_mfma_f32_16x16x32_bf16 a[24:27], a[132:135], a[168:171], a[24:27]\n" \
"s_cmp_lg_u32 s7, 0\n" \
"s_cselect_b32 s47, s41, s42\n" \
"v_mfma_f32_16x16x32_bf16 a[24:27], a[196:199], a[232:235], a[24:27]\n" \
"s_lshl_b32 s41, s43, 6\n" \
"v_lshrrev_b32_e32 v215, 6, v20\n" \
"v_mfma_f32_16x16x32_bf16 a[20:23], a[132:135], a[164:167], a[20:23]\n" \
"v_mov_b32_e32 v214, 0x3f80\n" \
"v_cmp_eq_u32_e64 vcc, 32, v213\n" \
"v_mfma_f32_16x16x32_bf16 a[20:23], a[196:199], a[228:231], a[20:23]\n" \
"ds_read_b64_tr_b16 a[180:181], v37 offset:32768\n" \
"s_or_b32 s7, s41, s7\n" \
"v_mfma_f32_16x16x32_bf16 a[16:19], a[132:135], a[160:163], a[16:19]\n" \
"v_lshrrev_b32_e32 v213, 7, v20\n" \
"v_bfe_u32 v216, v20, 6, 2\n" \
"v_mfma_f32_16x16x32_bf16 a[16:19], a[196:199], a[224:227], a[16:19]\n" \
"v_lshrrev_b32_e64 v219, 1, v20\n" \
"v_bfe_u32 v215, v215, 1, 1\n" \
"v_mfma_f32_16x16x32_bf16 a[32:35], a[136:139], a[160:163], a[32:35]\n" \
"s_waitcnt vmcnt(16)\n" \
"v_or_b32_e32 v204, v210, v204\n" \
"v_or_b32_e64 v206, v210, v206\n" \
"v_mfma_f32_16x16x32_bf16 a[32:35], a[200:203], a[224:227], a[32:35]\n" \
"v_mov_b32_e64 v210, 0\n" \
"s_add_u32 s88, s35, 0x1000\n" \
"v_mfma_f32_16x16x32_bf16 a[36:39], a[136:139], a[164:167], a[36:39]\n" \
"ds_read_b64_tr_b16 a[182:183], v37 offset:33792\n" \
"v_cndmask_b32_e64 v47, 0, v214, vcc\n" \
"v_mfma_f32_16x16x32_bf16 a[36:39], a[200:203], a[228:231], a[36:39]\n" \
"ds_read_b64_tr_b16 a[244:245], v37 offset:40960\n" \
"v_or_b32_e64 v214, s7, v213\n" \
"v_mfma_f32_16x16x32_bf16 a[40:43], a[136:139], a[168:171], a[40:43]\n" \
"s_mov_b32 m0, s32\n" \
"v_lshl_or_b32 v217, s43, 3, v213\n" \
"v_mfma_f32_16x16x32_bf16 a[40:43], a[200:203], a[232:235], a[40:43]\n" \
"s_mov_b32 exec_lo, 0xffffffff\n" \
"s_mov_b32 exec_hi, 0x0\n" \
"global_load_lds_dword v[188:189], off\n" \
"s_mov_b64 exec, -1\n" \
"v_mfma_f32_16x16x32_bf16 a[44:47], a[136:139], a[172:175], a[44:47]\n" \
"ds_read_b64_tr_b16 a[246:247], v37 offset:41984\n" \
"v_lshlrev_b32_e64 v218, 10, v212\n" \
"v_mfma_f32_16x16x32_bf16 a[44:47], a[200:203], a[236:239], a[44:47]\n" \
"v_lshlrev_b32_e32 v216, 7, v216\n" \
"v_and_b32_e32 v219, 24, v219\n" \
"v_mfma_f32_16x16x32_bf16 a[60:63], a[140:143], a[172:175], a[60:63]\n" \
"ds_write_b128 v196, v[200:203]\n" \
"v_mfma_f32_16x16x32_bf16 a[60:63], a[204:207], a[236:239], a[60:63]\n" \
"v_and_or_b32 v213, v213, 2, v215\n" \
"v_add_u32_e64 v217, s40, v217\n" \
"v_mfma_f32_16x16x32_bf16 a[56:59], a[140:143], a[168:171], a[56:59]\n" \
"ds_read_b64_tr_b16 a[184:185], v38 offset:32768\n" \
"v_or3_b32 v216, v216, v218, v219\n" \
"v_mfma_f32_16x16x32_bf16 a[56:59], a[204:207], a[232:235], a[56:59]\n" \
"v_lshlrev_b32_e64 v213, 5, v213\n" \
"s_mov_b32 s40, 0x14000\n" \
"v_mfma_f32_16x16x32_bf16 a[52:55], a[140:143], a[164:167], a[52:55]\n" \
"s_lshl_b32 s7, s2, 2\n" \
"v_or3_b32 v48, v216, v213, s40\n" \
"v_mfma_f32_16x16x32_bf16 a[52:55], a[204:207], a[228:231], a[52:55]\n" \
"ds_read_b64_tr_b16 a[186:187], v38 offset:33792\n" \
"s_lshl_b32 s40, s43, 2\n" \
"v_mfma_f32_16x16x32_bf16 a[48:51], a[140:143], a[160:163], a[48:51]\n" \
"s_lshl_b32 s6, s6, 3\n" \
"v_or_b32_e32 v213, s40, v212\n" \
"v_mfma_f32_16x16x32_bf16 a[48:51], a[204:207], a[224:227], a[48:51]\n" \
"s_or_b32 s6, s6, s40\n" \
"s_and_b32 s40, s7, 0x7c\n" \
"v_mfma_f32_16x16x32_bf16 a[64:67], a[144:147], a[160:163], a[64:67]\n" \
"ds_write_b128 v197, v[204:207]\n" \
"v_mfma_f32_16x16x32_bf16 a[64:67], a[208:211], a[224:227], a[64:67]\n" \
"ds_read_b64_tr_b16 a[248:249], v38 offset:40960\n" \
"s_or_b32 s40, s40, s3\n" \
"v_mfma_f32_16x16x32_bf16 a[68:71], a[144:147], a[164:167], a[68:71]\n" \
"s_cmpk_lt_u32 s40, 64\n" \
"s_cselect_b32 s41, s4, s5\n" \
"v_mfma_f32_16x16x32_bf16 a[68:71], a[208:211], a[228:231], a[68:71]\n" \
"ds_read_b64_tr_b16 a[250:251], v38 offset:41984\n" \
"s_lshl_b32 s40, s40, 1\n" \
"v_mfma_f32_16x16x32_bf16 a[72:75], a[144:147], a[168:171], a[72:75]\n" \
"s_lshl_b32 s41, s41, 7\n" \
"s_and_b32 s40, s40, 0x7e\n" \
"v_mfma_f32_16x16x32_bf16 a[72:75], a[208:211], a[232:235], a[72:75]\n" \
"s_or_b32 s40, s41, s40\n" \
"s_mulk_i32 s40, 0x480\n" \
"v_mfma_f32_16x16x32_bf16 a[76:79], a[144:147], a[172:175], a[76:79]\n" \
"v_add_u32_e64 v49, s40, v20\n" \
"s_mov_b32 s40, 0xc000\n" \
"v_mfma_f32_16x16x32_bf16 a[76:79], a[208:211], a[236:239], a[76:79]\n" \
"ds_read_b64_tr_b16 a[188:189], v39 offset:32768\n" \
"s_and_b32 s7, s7, 28\n" \
"v_mfma_f32_16x16x32_bf16 a[92:95], a[148:151], a[172:175], a[92:95]\n" \
"ds_write_b128 v198, v[208:211]\n" \
"v_mfma_f32_16x16x32_bf16 a[92:95], a[212:215], a[236:239], a[92:95]\n" \
"s_bfe_u32 s45, s2, 0x20003\n" \
"s_mov_b32 s41, 0x8000\n" \
"v_mfma_f32_16x16x32_bf16 a[88:91], a[148:151], a[168:171], a[88:91]\n" \
"ds_read_b64_tr_b16 a[190:191], v39 offset:33792\n" \
"v_mfma_f32_16x16x32_bf16 a[88:91], a[212:215], a[232:235], a[88:91]\n" \
"s_cmp_lg_u32 s4, 0\n" \
"s_cselect_b32 s51, s40, s41\n" \
"v_mfma_f32_16x16x32_bf16 a[84:87], a[148:151], a[164:167], a[84:87]\n" \
"s_movk_i32 s40, 0x4000\n" \
"s_lshr_b32 s44, s3, 1\n" \
"v_mfma_f32_16x16x32_bf16 a[84:87], a[212:215], a[228:231], a[84:87]\n" \
"ds_read_b64_tr_b16 a[252:253], v39 offset:40960\n" \
"s_or_b32 s46, s7, s45\n" \
"v_mfma_f32_16x16x32_bf16 a[80:83], a[148:151], a[160:163], a[80:83]\n" \
"s_cmp_lg_u32 s4, 0\n" \
"s_cselect_b32 s53, s41, s40\n" \
"v_mfma_f32_16x16x32_bf16 a[80:83], a[212:215], a[224:227], a[80:83]\n" \
"s_lshl_b32 s40, s44, 15\n" \
"s_lshl_b32 s41, s43, 14\n" \
"v_mfma_f32_16x16x32_bf16 a[96:99], a[152:155], a[160:163], a[96:99]\n" \
"ds_read_b64_tr_b16 a[254:255], v39 offset:41984\n" \
"s_lshl_b32 s48, s46, 20\n" \
"v_mfma_f32_16x16x32_bf16 a[96:99], a[216:219], a[224:227], a[96:99]\n" \
"s_or_b32 s41, s40, s41\n" \
"s_add_i32 s49, s48, 0x1000000\n" \
"v_mfma_f32_16x16x32_bf16 a[100:103], a[152:155], a[164:167], a[100:103]\n" \
"s_or_b32 s42, s49, s41\n" \
"s_lshl_b32 s4, s4, 16\n" \
"v_mfma_f32_16x16x32_bf16 a[100:103], a[216:219], a[228:231], a[100:103]\n" \
"s_or_b32 s41, s42, s4\n" \
"s_lshl_b32 s4, s5, 16\n" \
"v_mfma_f32_16x16x32_bf16 a[104:107], a[152:155], a[168:171], a[104:107]\n" \
"s_mul_i32 s5, s6, 0x24800\n" \
"s_lshl_b32 s6, s46, 9\n" \
"v_mfma_f32_16x16x32_bf16 a[104:107], a[216:219], a[232:235], a[104:107]\n" \
"s_lshl_b32 s7, s7, 6\n" \
"s_add_i32 s6, s5, s6\n" \
"v_mfma_f32_16x16x32_bf16 a[108:111], a[152:155], a[172:175], a[108:111]\n" \
"s_or_b32 s5, s5, s7\n" \
"s_lshl_b32 s7, s45, 2\n" \
"v_mfma_f32_16x16x32_bf16 a[108:111], a[216:219], a[236:239], a[108:111]\n" \
"s_or_b32 s5, s5, s7\n" \
"s_lshl_b32 s7, s3, 12\n" \
"s_waitcnt lgkmcnt(15)\n" \
"v_mfma_f32_16x16x32_bf16 a[124:127], a[156:159], a[172:175], a[124:127]\n" \
"s_or_b32 s50, s49, s7\n" \
"s_lshl_b32 s7, s3, 11\n" \
"v_mfma_f32_16x16x32_bf16 a[124:127], a[220:223], a[236:239], a[124:127]\n" \
"v_lshrrev_b32_e64 v215, 4, v20\n" \
"v_bfe_u32 v216, v20, 4, 3\n" \
"v_mfma_f32_16x16x32_bf16 a[120:123], a[156:159], a[168:171], a[120:123]\n" \
"s_or_b32 s42, s42, s4\n" \
"s_lshl_b32 s4, s43, 11\n" \
"v_mfma_f32_16x16x32_bf16 a[120:123], a[220:223], a[232:235], a[120:123]\n" \
"s_add_i32 s52, s48, s7\n" \
"s_add_i32 s53, s53, s7\n" \
"v_mfma_f32_16x16x32_bf16 a[116:119], a[156:159], a[164:167], a[116:119]\n" \
"v_mul_u32_u24_e32 v214, 0x480, v214\n" \
"v_bitop3_b32 v218, v212, v215, 7 bitop3:0x78\n" \
"v_mfma_f32_16x16x32_bf16 a[116:119], a[220:223], a[228:231], a[116:119]\n" \
"v_bitop3_b32 v212, v212, v216, 4 bitop3:0x36\n" \
"s_lshl_b32 s7, s3, 10\n" \
"v_mfma_f32_16x16x32_bf16 a[112:115], a[156:159], a[160:163], a[112:115]\n" \
"s_or_b32 s4, s40, s4\n" \
"v_lshl_or_b32 v25, v212, 4, v214\n" \
"s_waitcnt vmcnt(1) lgkmcnt(0)\n" \
"v_mfma_f32_16x16x32_bf16 a[112:115], a[220:223], a[224:227], a[112:115]\n" \
"s_barrier\n" \
"v_mfma_f32_16x16x32_bf16 v[52:55], a[128:131], a[176:179], v[52:55]\n" \
"s_add_u32 m0, s30, 0x8000\n" \
"s_movk_i32 s84, 0x280\n" \
"v_mfma_f32_16x16x32_bf16 v[52:55], a[192:195], a[240:243], v[52:55]\n" \
"buffer_load_dwordx4 v22, s[64:67], s84 offen offset:0 lds\n" \
"v_mfma_f32_16x16x32_bf16 v[56:59], a[128:131], a[180:183], v[56:59]\n" \
"s_movk_i32 s84, 0x1e80\n" \
"v_mfma_f32_16x16x32_bf16 v[56:59], a[192:195], a[244:247], v[56:59]\n" \
"buffer_load_dwordx4 v23, s[64:67], s84 offen offset:1024 lds\n" \
"v_mfma_f32_16x16x32_bf16 v[60:63], a[128:131], a[184:187], v[60:63]\n" \
"ds_read_b64_tr_b16 a[160:161], v32 offset:0\n" \
"v_mul_u32_u24_e32 v212, 0x480, v217\n" \
"v_mfma_f32_16x16x32_bf16 v[60:63], a[192:195], a[248:251], v[60:63]\n" \
"s_movk_i32 s84, 0x3a80\n" \
"v_mfma_f32_16x16x32_bf16 v[64:67], a[128:131], a[188:191], v[64:67]\n" \
"buffer_load_dwordx4 v22, s[64:67], s84 offen offset:2048 lds\n" \
"v_mfma_f32_16x16x32_bf16 v[64:67], a[192:195], a[252:255], v[64:67]\n" \
"ds_read_b128 a[128:131], v28 offset:16384\n" \
"v_bitop3_b32 v213, v215, v213, 7 bitop3:0x6c\n" \
"v_mfma_f32_16x16x32_bf16 v[80:83], a[132:135], a[188:191], v[80:83]\n" \
"ds_read_b128 a[192:195], v29 offset:16384\n" \
"s_or_b32 s40, s7, 0x14000\n" \
"v_mfma_f32_16x16x32_bf16 v[80:83], a[196:199], a[252:255], v[80:83]\n" \
"s_movk_i32 s84, 0x5680\n" \
"v_mfma_f32_16x16x32_bf16 v[76:79], a[132:135], a[184:187], v[76:79]\n" \
"buffer_load_dwordx4 v23, s[64:67], s84 offen offset:3072 lds\n" \
"v_mfma_f32_16x16x32_bf16 v[76:79], a[196:199], a[248:251], v[76:79]\n" \
"s_add_u32 m0, s31, 0x8000\n" \
"s_mov_b32 s84, 0x280000\n" \
"v_mfma_f32_16x16x32_bf16 v[72:75], a[132:135], a[180:183], v[72:75]\n" \
"buffer_load_dwordx4 v26, s[68:71], s84 offen offset:0 lds\n" \
"v_mfma_f32_16x16x32_bf16 v[72:75], a[196:199], a[244:247], v[72:75]\n" \
"ds_read_b64_tr_b16 a[162:163], v32 offset:1024\n" \
"s_lshl_b32 s43, s44, 13\n" \
"v_mfma_f32_16x16x32_bf16 v[68:71], a[132:135], a[176:179], v[68:71]\n" \
"ds_read_b64_tr_b16 a[224:225], v32 offset:8192\n" \
"s_mulk_i32 s44, 0xe800\n" \
"v_mfma_f32_16x16x32_bf16 v[68:71], a[196:199], a[240:243], v[68:71]\n" \
"ds_read_b128 a[132:135], v28 offset:18432\n" \
"s_lshl_b32 s7, s3, 8\n" \
"v_mfma_f32_16x16x32_bf16 v[84:87], a[136:139], a[176:179], v[84:87]\n" \
"ds_read_b128 a[196:199], v29 offset:18432\n" \
"v_lshl_or_b32 v24, v218, 4, v214\n" \
"v_mfma_f32_16x16x32_bf16 v[84:87], a[200:203], a[240:243], v[84:87]\n" \
"s_addk_i32 s84, 0x7c00\n" \
"v_mfma_f32_16x16x32_bf16 v[88:91], a[136:139], a[180:183], v[88:91]\n" \
"buffer_load_dwordx4 v26, s[68:71], s84 offen offset:1024 lds\n" \
"v_mfma_f32_16x16x32_bf16 v[88:91], a[200:203], a[244:247], v[88:91]\n" \
"s_addk_i32 s84, 0x7c00\n" \
"v_mfma_f32_16x16x32_bf16 v[92:95], a[136:139], a[184:187], v[92:95]\n" \
"buffer_load_dwordx4 v27, s[68:71], s84 offen offset:2048 lds\n" \
"v_mfma_f32_16x16x32_bf16 v[92:95], a[200:203], a[248:251], v[92:95]\n" \
"s_addk_i32 s84, 0x7c00\n" \
"v_mfma_f32_16x16x32_bf16 v[96:99], a[136:139], a[188:191], v[96:99]\n" \
"buffer_load_dwordx4 v27, s[68:71], s84 offen offset:3072 lds\n" \
"v_mfma_f32_16x16x32_bf16 v[96:99], a[200:203], a[252:255], v[96:99]\n" \
"ds_read_b128 a[136:139], v28 offset:20480\n" \
"v_lshl_or_b32 v21, v213, 4, v212\n" \
"v_mfma_f32_16x16x32_bf16 v[112:115], a[140:143], a[188:191], v[112:115]\n" \
"ds_read_b128 a[200:203], v29 offset:20480\n" \
"s_add_i32 s44, s44, 0x17000\n" \
"v_mfma_f32_16x16x32_bf16 v[112:115], a[204:207], a[252:255], v[112:115]\n" \
"s_add_u32 m0, s30, 0x9000\n" \
"s_mov_b32 s84, 0x8280\n" \
"v_mfma_f32_16x16x32_bf16 v[108:111], a[140:143], a[184:187], v[108:111]\n" \
"buffer_load_dwordx4 v22, s[64:67], s84 offen offset:0 lds\n" \
"v_mfma_f32_16x16x32_bf16 v[108:111], a[204:207], a[248:251], v[108:111]\n" \
"s_addk_i32 s84, 0x1c00\n" \
"v_mfma_f32_16x16x32_bf16 v[104:107], a[140:143], a[180:183], v[104:107]\n" \
"buffer_load_dwordx4 v23, s[64:67], s84 offen offset:1024 lds\n" \
"v_mfma_f32_16x16x32_bf16 v[104:107], a[204:207], a[244:247], v[104:107]\n" \
"s_addk_i32 s84, 0x1c00\n" \
"v_mfma_f32_16x16x32_bf16 v[100:103], a[140:143], a[176:179], v[100:103]\n" \
"buffer_load_dwordx4 v22, s[64:67], s84 offen offset:2048 lds\n" \
"v_mfma_f32_16x16x32_bf16 v[100:103], a[204:207], a[240:243], v[100:103]\n" \
"ds_read_b128 a[140:143], v28 offset:22528\n" \
"s_or_b32 s45, s7, 0x27000\n" \
"v_mfma_f32_16x16x32_bf16 v[116:119], a[144:147], a[176:179], v[116:119]\n" \
"ds_read_b128 a[204:207], v29 offset:22528\n" \
"s_or_b32 s46, s4, 0x10000\n" \
"v_mfma_f32_16x16x32_bf16 v[116:119], a[208:211], a[240:243], v[116:119]\n" \
"s_addk_i32 s84, 0x1c00\n" \
"v_mfma_f32_16x16x32_bf16 v[120:123], a[144:147], a[180:183], v[120:123]\n" \
"buffer_load_dwordx4 v23, s[64:67], s84 offen offset:3072 lds\n" \
"v_mfma_f32_16x16x32_bf16 v[120:123], a[208:211], a[244:247], v[120:123]\n" \
"s_add_u32 m0, s31, 0xc000\n" \
"s_mov_b32 s84, 0x280000\n" \
"v_mfma_f32_16x16x32_bf16 v[124:127], a[144:147], a[184:187], v[124:127]\n" \
"buffer_load_dwordx4 v26, s[72:75], s84 offen offset:0 lds\n" \
"v_mfma_f32_16x16x32_bf16 v[124:127], a[208:211], a[248:251], v[124:127]\n" \
"s_addk_i32 s84, 0x7c00\n" \
"v_mfma_f32_16x16x32_bf16 v[128:131], a[144:147], a[188:191], v[128:131]\n" \
"buffer_load_dwordx4 v26, s[72:75], s84 offen offset:1024 lds\n" \
"v_mfma_f32_16x16x32_bf16 v[128:131], a[208:211], a[252:255], v[128:131]\n" \
"ds_read_b128 a[144:147], v28 offset:0\n" \
"s_add_i32 s48, s6, 0x3000000\n" \
"v_mfma_f32_16x16x32_bf16 v[144:147], a[148:151], a[188:191], v[144:147]\n" \
"ds_read_b128 a[208:211], v29 offset:0\n" \
"s_add_i32 s49, s5, 0x3004000\n" \
"v_mfma_f32_16x16x32_bf16 v[144:147], a[212:215], a[252:255], v[144:147]\n" \
"ds_read_b128 v[200:203], v199 offset:0\n" \
"s_add_i32 s52, s52, 0x1010000\n" \
"v_mfma_f32_16x16x32_bf16 v[140:143], a[148:151], a[184:187], v[140:143]\n" \
"ds_read_b128 v[204:207], v199 offset:4096\n" \
"v_mfma_f32_16x16x32_bf16 v[140:143], a[212:215], a[248:251], v[140:143]\n" \
"ds_read_b64_tr_b16 a[226:227], v32 offset:9216\n" \
"v_mfma_f32_16x16x32_bf16 v[136:139], a[148:151], a[180:183], v[136:139]\n" \
"ds_read_b64_tr_b16 a[164:165], v33 offset:0\n" \
"v_mfma_f32_16x16x32_bf16 v[136:139], a[212:215], a[244:247], v[136:139]\n" \
"ds_read_b128 v[208:211], v199 offset:8192\n" \
"v_mfma_f32_16x16x32_bf16 v[132:135], a[148:151], a[176:179], v[132:135]\n" \
"ds_read_b64_tr_b16 a[166:167], v33 offset:1024\n" \
"v_mfma_f32_16x16x32_bf16 v[132:135], a[212:215], a[240:243], v[132:135]\n" \
"ds_read_b128 a[148:151], v28 offset:2048\n" \
"v_mfma_f32_16x16x32_bf16 v[148:151], a[152:155], a[176:179], v[148:151]\n" \
"ds_read_b128 a[212:215], v29 offset:2048\n" \
"v_mfma_f32_16x16x32_bf16 v[148:151], a[216:219], a[240:243], v[148:151]\n" \
"ds_read_b64_tr_b16 a[228:229], v33 offset:8192\n" \
"v_mfma_f32_16x16x32_bf16 v[152:155], a[152:155], a[180:183], v[152:155]\n" \
"ds_read_b64_tr_b16 a[230:231], v33 offset:9216\n" \
"v_mfma_f32_16x16x32_bf16 v[152:155], a[216:219], a[244:247], v[152:155]\n" \
"ds_read_b64_tr_b16 a[168:169], v34 offset:0\n" \
"v_mfma_f32_16x16x32_bf16 v[156:159], a[152:155], a[184:187], v[156:159]\n" \
"ds_read_b64_tr_b16 a[170:171], v34 offset:1024\n" \
"v_mfma_f32_16x16x32_bf16 v[156:159], a[216:219], a[248:251], v[156:159]\n" \
"ds_read_b64_tr_b16 a[232:233], v34 offset:8192\n" \
"v_mfma_f32_16x16x32_bf16 v[160:163], a[152:155], a[188:191], v[160:163]\n" \
"ds_read_b64_tr_b16 a[234:235], v34 offset:9216\n" \
"v_mfma_f32_16x16x32_bf16 v[160:163], a[216:219], a[252:255], v[160:163]\n" \
"ds_read_b128 a[152:155], v28 offset:4096\n" \
"v_mfma_f32_16x16x32_bf16 v[176:179], a[156:159], a[188:191], v[176:179]\n" \
"ds_read_b128 a[216:219], v29 offset:4096\n" \
"v_mfma_f32_16x16x32_bf16 v[176:179], a[220:223], a[252:255], v[176:179]\n" \
"ds_read_b64_tr_b16 a[172:173], v35 offset:0\n" \
"v_mfma_f32_16x16x32_bf16 v[172:175], a[156:159], a[184:187], v[172:175]\n" \
"ds_read_b64_tr_b16 a[174:175], v35 offset:1024\n" \
"v_mfma_f32_16x16x32_bf16 v[172:175], a[220:223], a[248:251], v[172:175]\n" \
"ds_read_b64_tr_b16 a[236:237], v35 offset:8192\n" \
"v_mfma_f32_16x16x32_bf16 v[168:171], a[156:159], a[180:183], v[168:171]\n" \
"ds_read_b64_tr_b16 a[238:239], v35 offset:9216\n" \
"v_mfma_f32_16x16x32_bf16 v[168:171], a[220:223], a[244:247], v[168:171]\n" \
"v_mfma_f32_16x16x32_bf16 v[164:167], a[156:159], a[176:179], v[164:167]\n" \
"v_mfma_f32_16x16x32_bf16 v[164:167], a[220:223], a[240:243], v[164:167]\n" \
"ds_read_b128 a[156:159], v28 offset:6144\n" \
"ds_read_b128 a[220:223], v29 offset:6144\n" \
"s_waitcnt lgkmcnt(15)\n" \
"v_mfma_f32_16x16x32_bf16 a[0:3], a[128:131], a[160:163], a[0:3]\n" \
"s_addk_i32 s84, 0x7c00\n" \
"v_mfma_f32_16x16x32_bf16 a[0:3], a[192:195], a[224:227], a[0:3]\n" \
"buffer_load_dwordx4 v27, s[72:75], s84 offen offset:2048 lds\n" \
"v_mfma_f32_16x16x32_bf16 a[4:7], a[128:131], a[164:167], a[4:7]\n" \
"ds_read_b64_tr_b16 a[176:177], v36 offset:0\n" \
"s_waitcnt lgkmcnt(13)\n" \
"v_mfma_f32_16x16x32_bf16 a[4:7], a[192:195], a[228:231], a[4:7]\n" \
"s_addk_i32 s84, 0x7c00\n" \
"s_waitcnt lgkmcnt(11)\n" \
"v_mfma_f32_16x16x32_bf16 a[8:11], a[128:131], a[168:171], a[8:11]\n" \
"buffer_load_dwordx4 v27, s[72:75], s84 offen offset:3072 lds\n" \
"s_waitcnt lgkmcnt(9)\n" \
"v_mfma_f32_16x16x32_bf16 a[8:11], a[192:195], a[232:235], a[8:11]\n" \
"ds_read_b64_tr_b16 a[178:179], v36 offset:1024\n" \
"s_waitcnt lgkmcnt(6)\n" \
"v_mfma_f32_16x16x32_bf16 a[12:15], a[128:131], a[172:175], a[12:15]\n" \
"ds_read_b64_tr_b16 a[240:241], v36 offset:8192\n" \
"s_waitcnt lgkmcnt(5)\n" \
"v_mfma_f32_16x16x32_bf16 a[12:15], a[192:195], a[236:239], a[12:15]\n" \
"v_mfma_f32_16x16x32_bf16 a[28:31], a[132:135], a[172:175], a[28:31]\n" \
"v_mfma_f32_16x16x32_bf16 a[28:31], a[196:199], a[236:239], a[28:31]\n" \
"ds_read_b64_tr_b16 a[242:243], v36 offset:9216\n" \
"v_mfma_f32_16x16x32_bf16 a[24:27], a[132:135], a[168:171], a[24:27]\n" \
"v_mfma_f32_16x16x32_bf16 a[24:27], a[196:199], a[232:235], a[24:27]\n" \
"v_mfma_f32_16x16x32_bf16 a[20:23], a[132:135], a[164:167], a[20:23]\n" \
"v_mfma_f32_16x16x32_bf16 a[20:23], a[196:199], a[228:231], a[20:23]\n" \
"ds_read_b64_tr_b16 a[180:181], v37 offset:0\n" \
"v_mfma_f32_16x16x32_bf16 a[16:19], a[132:135], a[160:163], a[16:19]\n" \
"v_mfma_f32_16x16x32_bf16 a[16:19], a[196:199], a[224:227], a[16:19]\n" \
"v_mfma_f32_16x16x32_bf16 a[32:35], a[136:139], a[160:163], a[32:35]\n" \
"ds_read_b64_tr_b16 a[182:183], v37 offset:1024\n" \
"v_mfma_f32_16x16x32_bf16 a[32:35], a[200:203], a[224:227], a[32:35]\n" \
"v_mfma_f32_16x16x32_bf16 a[36:39], a[136:139], a[164:167], a[36:39]\n" \
"v_mfma_f32_16x16x32_bf16 a[36:39], a[200:203], a[228:231], a[36:39]\n" \
"buffer_store_dwordx4 v[200:203], v20, s[80:83], s35 offen sc1\n" \
".fill (L_pb + 52960 - .) / 4, 4, 0xBF800000\n" \
"L_pp13:\n" \
"s_cbranch_execz L_pret\n" \
"v_mfma_f32_16x16x32_bf16 a[40:43], a[136:139], a[168:171], a[40:43]\n" \
"s_mov_b32 m0, s32\n" \
"ds_read_b64_tr_b16 a[244:245], v37 offset:8192\n" \
"v_mfma_f32_16x16x32_bf16 a[40:43], a[200:203], a[232:235], a[40:43]\n" \
"s_mov_b32 exec_lo, 0x0\n" \
"s_mov_b32 exec_hi, 0xffffffff\n" \
"global_load_lds_dword v[188:189], off\n" \
"s_mov_b64 exec, -1\n" \
"s_nop 0\n" \
"v_mfma_f32_16x16x32_bf16 a[44:47], a[136:139], a[172:175], a[44:47]\n" \
"ds_read_b64_tr_b16 a[246:247], v37 offset:9216\n" \
"v_mfma_f32_16x16x32_bf16 a[44:47], a[200:203], a[236:239], a[44:47]\n" \
"v_mfma_f32_16x16x32_bf16 a[60:63], a[140:143], a[172:175], a[60:63]\n" \
"v_mfma_f32_16x16x32_bf16 a[60:63], a[204:207], a[236:239], a[60:63]\n" \
"v_mfma_f32_16x16x32_bf16 a[56:59], a[140:143], a[168:171], a[56:59]\n" \
"ds_read_b64_tr_b16 a[184:185], v38 offset:0\n" \
"v_mfma_f32_16x16x32_bf16 a[56:59], a[204:207], a[232:235], a[56:59]\n" \
"v_mfma_f32_16x16x32_bf16 a[52:55], a[140:143], a[164:167], a[52:55]\n" \
"buffer_store_dwordx4 v[204:207], v20, s[80:83], s88 offen sc1\n" \
"v_mfma_f32_16x16x32_bf16 a[52:55], a[204:207], a[228:231], a[52:55]\n" \
"ds_read_b64_tr_b16 a[186:187], v38 offset:1024\n" \
"v_mfma_f32_16x16x32_bf16 a[48:51], a[140:143], a[160:163], a[48:51]\n" \
"v_mfma_f32_16x16x32_bf16 a[48:51], a[204:207], a[224:227], a[48:51]\n" \
"v_mfma_f32_16x16x32_bf16 a[64:67], a[144:147], a[160:163], a[64:67]\n" \
"ds_read_b64_tr_b16 a[248:249], v38 offset:8192\n" \
"v_mfma_f32_16x16x32_bf16 a[64:67], a[208:211], a[224:227], a[64:67]\n" \
"v_mfma_f32_16x16x32_bf16 a[68:71], a[144:147], a[164:167], a[68:71]\n" \
"v_mfma_f32_16x16x32_bf16 a[68:71], a[208:211], a[228:231], a[68:71]\n" \
"ds_read_b64_tr_b16 a[250:251], v38 offset:9216\n" \
"v_mfma_f32_16x16x32_bf16 a[72:75], a[144:147], a[168:171], a[72:75]\n" \
"v_mfma_f32_16x16x32_bf16 a[72:75], a[208:211], a[232:235], a[72:75]\n" \
"v_mfma_f32_16x16x32_bf16 a[76:79], a[144:147], a[172:175], a[76:79]\n" \
"buffer_store_dwordx4 v[208:211], v20, s[80:83], s36 offen sc1\n" \
"v_mfma_f32_16x16x32_bf16 a[76:79], a[208:211], a[236:239], a[76:79]\n" \
"ds_read_b64_tr_b16 a[188:189], v39 offset:0\n" \
"v_mfma_f32_16x16x32_bf16 a[92:95], a[148:151], a[172:175], a[92:95]\n" \
"v_mfma_f32_16x16x32_bf16 a[92:95], a[212:215], a[236:239], a[92:95]\n" \
"v_mfma_f32_16x16x32_bf16 a[88:91], a[148:151], a[168:171], a[88:91]\n" \
"ds_read_b64_tr_b16 a[190:191], v39 offset:1024\n" \
"v_mfma_f32_16x16x32_bf16 a[88:91], a[212:215], a[232:235], a[88:91]\n" \
"v_mfma_f32_16x16x32_bf16 a[84:87], a[148:151], a[164:167], a[84:87]\n" \
"v_mfma_f32_16x16x32_bf16 a[84:87], a[212:215], a[228:231], a[84:87]\n" \
"ds_read_b64_tr_b16 a[252:253], v39 offset:8192\n" \
"v_mfma_f32_16x16x32_bf16 a[80:83], a[148:151], a[160:163], a[80:83]\n" \
"v_mfma_f32_16x16x32_bf16 a[80:83], a[212:215], a[224:227], a[80:83]\n" \
"v_mfma_f32_16x16x32_bf16 a[96:99], a[152:155], a[160:163], a[96:99]\n" \
"ds_read_b64_tr_b16 a[254:255], v39 offset:9216\n" \
"v_mfma_f32_16x16x32_bf16 a[96:99], a[216:219], a[224:227], a[96:99]\n" \
"v_mfma_f32_16x16x32_bf16 a[100:103], a[152:155], a[164:167], a[100:103]\n" \
"v_mfma_f32_16x16x32_bf16 a[100:103], a[216:219], a[228:231], a[100:103]\n" \
"v_mfma_f32_16x16x32_bf16 a[104:107], a[152:155], a[168:171], a[104:107]\n" \
"v_mfma_f32_16x16x32_bf16 a[104:107], a[216:219], a[232:235], a[104:107]\n" \
"v_mfma_f32_16x16x32_bf16 a[108:111], a[152:155], a[172:175], a[108:111]\n" \
"v_mfma_f32_16x16x32_bf16 a[108:111], a[216:219], a[236:239], a[108:111]\n" \
"s_waitcnt lgkmcnt(15)\n" \
"v_mfma_f32_16x16x32_bf16 a[124:127], a[156:159], a[172:175], a[124:127]\n" \
"v_mfma_f32_16x16x32_bf16 a[124:127], a[220:223], a[236:239], a[124:127]\n" \
"v_mfma_f32_16x16x32_bf16 a[120:123], a[156:159], a[168:171], a[120:123]\n" \
"v_mfma_f32_16x16x32_bf16 a[120:123], a[220:223], a[232:235], a[120:123]\n" \
"v_mfma_f32_16x16x32_bf16 a[116:119], a[156:159], a[164:167], a[116:119]\n" \
"v_mfma_f32_16x16x32_bf16 a[116:119], a[220:223], a[228:231], a[116:119]\n" \
"v_mfma_f32_16x16x32_bf16 a[112:115], a[156:159], a[160:163], a[112:115]\n" \
"s_waitcnt vmcnt(4) lgkmcnt(0)\n" \
"v_mfma_f32_16x16x32_bf16 a[112:115], a[220:223], a[224:227], a[112:115]\n" \
"s_barrier\n" \
"v_mfma_f32_16x16x32_bf16 v[52:55], a[128:131], a[176:179], v[52:55]\n" \
"s_add_u32 m0, s30, 0x0\n" \
"s_movk_i32 s84, 0x300\n" \
"v_mfma_f32_16x16x32_bf16 v[52:55], a[192:195], a[240:243], v[52:55]\n" \
"buffer_load_dwordx4 v22, s[64:67], s84 offen offset:0 lds\n" \
"v_mfma_f32_16x16x32_bf16 v[56:59], a[128:131], a[180:183], v[56:59]\n" \
"ds_read_b64_tr_b16 a[160:161], v32 offset:32768\n" \
"v_mfma_f32_16x16x32_bf16 v[56:59], a[192:195], a[244:247], v[56:59]\n" \
"s_movk_i32 s84, 0x1f00\n" \
"v_mfma_f32_16x16x32_bf16 v[60:63], a[128:131], a[184:187], v[60:63]\n" \
"buffer_load_dwordx4 v23, s[64:67], s84 offen offset:1024 lds\n" \
"v_mfma_f32_16x16x32_bf16 v[60:63], a[192:195], a[248:251], v[60:63]\n" \
"ds_read_b64_tr_b16 a[162:163], v32 offset:33792\n" \
"v_mfma_f32_16x16x32_bf16 v[64:67], a[128:131], a[188:191], v[64:67]\n" \
"ds_read_b64_tr_b16 a[224:225], v32 offset:40960\n" \
"v_mfma_f32_16x16x32_bf16 v[64:67], a[192:195], a[252:255], v[64:67]\n" \
"ds_read_b128 a[128:131], v28 offset:49152\n" \
"v_mfma_f32_16x16x32_bf16 v[80:83], a[132:135], a[188:191], v[80:83]\n" \
"ds_read_b128 a[192:195], v29 offset:49152\n" \
"v_mfma_f32_16x16x32_bf16 v[80:83], a[196:199], a[252:255], v[80:83]\n" \
"s_movk_i32 s84, 0x3b00\n" \
"v_mfma_f32_16x16x32_bf16 v[76:79], a[132:135], a[184:187], v[76:79]\n" \
"buffer_load_dwordx4 v22, s[64:67], s84 offen offset:2048 lds\n" \
"v_mfma_f32_16x16x32_bf16 v[76:79], a[196:199], a[248:251], v[76:79]\n" \
"s_movk_i32 s84, 0x5700\n" \
"v_mfma_f32_16x16x32_bf16 v[72:75], a[132:135], a[180:183], v[72:75]\n" \
"buffer_load_dwordx4 v23, s[64:67], s84 offen offset:3072 lds\n" \
"v_mfma_f32_16x16x32_bf16 v[72:75], a[196:199], a[244:247], v[72:75]\n" \
"s_add_u32 m0, s31, 0x0\n" \
"s_mov_b32 s84, 0x300000\n" \
"v_mfma_f32_16x16x32_bf16 v[68:71], a[132:135], a[176:179], v[68:71]\n" \
"buffer_load_dwordx4 v26, s[68:71], s84 offen offset:0 lds\n" \
"v_mfma_f32_16x16x32_bf16 v[68:71], a[196:199], a[240:243], v[68:71]\n" \
"ds_read_b128 a[132:135], v28 offset:51200\n" \
"v_mfma_f32_16x16x32_bf16 v[84:87], a[136:139], a[176:179], v[84:87]\n" \
"ds_read_b128 a[196:199], v29 offset:51200\n" \
"v_mfma_f32_16x16x32_bf16 v[84:87], a[200:203], a[240:243], v[84:87]\n" \
"s_addk_i32 s84, 0x7c00\n" \
"v_mfma_f32_16x16x32_bf16 v[88:91], a[136:139], a[180:183], v[88:91]\n" \
"buffer_load_dwordx4 v26, s[68:71], s84 offen offset:1024 lds\n" \
"v_mfma_f32_16x16x32_bf16 v[88:91], a[200:203], a[244:247], v[88:91]\n" \
"ds_read_b64_tr_b16 a[226:227], v32 offset:41984\n" \
"v_mfma_f32_16x16x32_bf16 v[92:95], a[136:139], a[184:187], v[92:95]\n" \
"s_addk_i32 s84, 0x7c00\n" \
"v_mfma_f32_16x16x32_bf16 v[92:95], a[200:203], a[248:251], v[92:95]\n" \
"buffer_load_dwordx4 v27, s[68:71], s84 offen offset:2048 lds\n" \
"v_mfma_f32_16x16x32_bf16 v[96:99], a[136:139], a[188:191], v[96:99]\n" \
"ds_read_b64_tr_b16 a[164:165], v33 offset:32768\n" \
"v_mfma_f32_16x16x32_bf16 v[96:99], a[200:203], a[252:255], v[96:99]\n" \
"ds_read_b128 a[136:139], v28 offset:53248\n" \
"v_mfma_f32_16x16x32_bf16 v[112:115], a[140:143], a[188:191], v[112:115]\n" \
"ds_read_b128 a[200:203], v29 offset:53248\n" \
"v_mfma_f32_16x16x32_bf16 v[112:115], a[204:207], a[252:255], v[112:115]\n" \
"s_addk_i32 s84, 0x7c00\n" \
"v_mfma_f32_16x16x32_bf16 v[108:111], a[140:143], a[184:187], v[108:111]\n" \
"buffer_load_dwordx4 v27, s[68:71], s84 offen offset:3072 lds\n" \
"v_mfma_f32_16x16x32_bf16 v[108:111], a[204:207], a[248:251], v[108:111]\n" \
"s_add_u32 m0, s30, 0x1000\n" \
"s_mov_b32 s84, 0x8300\n" \
"v_mfma_f32_16x16x32_bf16 v[104:107], a[140:143], a[180:183], v[104:107]\n" \
"buffer_load_dwordx4 v22, s[64:67], s84 offen offset:0 lds\n" \
"v_mfma_f32_16x16x32_bf16 v[104:107], a[204:207], a[244:247], v[104:107]\n" \
"ds_read_b64_tr_b16 a[166:167], v33 offset:33792\n" \
"v_mfma_f32_16x16x32_bf16 v[100:103], a[140:143], a[176:179], v[100:103]\n" \
"ds_read_b64_tr_b16 a[228:229], v33 offset:40960\n" \
"v_mfma_f32_16x16x32_bf16 v[100:103], a[204:207], a[240:243], v[100:103]\n" \
"ds_read_b128 a[140:143], v28 offset:55296\n" \
"v_mfma_f32_16x16x32_bf16 v[116:119], a[144:147], a[176:179], v[116:119]\n" \
"ds_read_b128 a[204:207], v29 offset:55296\n" \
"v_mfma_f32_16x16x32_bf16 v[116:119], a[208:211], a[240:243], v[116:119]\n" \
"s_addk_i32 s84, 0x1c00\n" \
"v_mfma_f32_16x16x32_bf16 v[120:123], a[144:147], a[180:183], v[120:123]\n" \
"buffer_load_dwordx4 v23, s[64:67], s84 offen offset:1024 lds\n" \
"v_mfma_f32_16x16x32_bf16 v[120:123], a[208:211], a[244:247], v[120:123]\n" \
"s_addk_i32 s84, 0x1c00\n" \
"v_mfma_f32_16x16x32_bf16 v[124:127], a[144:147], a[184:187], v[124:127]\n" \
"buffer_load_dwordx4 v22, s[64:67], s84 offen offset:2048 lds\n" \
"v_mfma_f32_16x16x32_bf16 v[124:127], a[208:211], a[248:251], v[124:127]\n" \
"s_addk_i32 s84, 0x1c00\n" \
"s_nop 0\n" \
"v_mfma_f32_16x16x32_bf16 v[128:131], a[144:147], a[188:191], v[128:131]\n" \
"buffer_load_dwordx4 v23, s[64:67], s84 offen offset:3072 lds\n" \
"v_mfma_f32_16x16x32_bf16 v[128:131], a[208:211], a[252:255], v[128:131]\n" \
"ds_read_b128 a[144:147], v28 offset:32768\n" \
"v_mfma_f32_16x16x32_bf16 v[144:147], a[148:151], a[188:191], v[144:147]\n" \
"ds_read_b128 a[208:211], v29 offset:32768\n" \
"v_mfma_f32_16x16x32_bf16 v[144:147], a[212:215], a[252:255], v[144:147]\n" \
"s_add_u32 m0, s31, 0x4000\n" \
"s_mov_b32 s84, 0x300000\n" \
"v_mfma_f32_16x16x32_bf16 v[140:143], a[148:151], a[184:187], v[140:143]\n" \
"buffer_load_dwordx4 v26, s[72:75], s84 offen offset:0 lds\n" \
"v_mfma_f32_16x16x32_bf16 v[140:143], a[212:215], a[248:251], v[140:143]\n" \
"ds_read_b64_tr_b16 a[230:231], v33 offset:41984\n" \
"v_mfma_f32_16x16x32_bf16 v[136:139], a[148:151], a[180:183], v[136:139]\n" \
"s_addk_i32 s84, 0x7c00\n" \
"s_nop 0\n" \
"v_mfma_f32_16x16x32_bf16 v[136:139], a[212:215], a[244:247], v[136:139]\n" \
"buffer_load_dwordx4 v26, s[72:75], s84 offen offset:1024 lds\n" \
"v_mfma_f32_16x16x32_bf16 v[132:135], a[148:151], a[176:179], v[132:135]\n" \
"ds_read_b64_tr_b16 a[168:169], v34 offset:32768\n" \
"v_mfma_f32_16x16x32_bf16 v[132:135], a[212:215], a[240:243], v[132:135]\n" \
"ds_read_b128 a[148:151], v28 offset:34816\n" \
"v_mfma_f32_16x16x32_bf16 v[148:151], a[152:155], a[176:179], v[148:151]\n" \
"ds_read_b128 a[212:215], v29 offset:34816\n" \
"v_mfma_f32_16x16x32_bf16 v[148:151], a[216:219], a[240:243], v[148:151]\n" \
"ds_read_b64_tr_b16 a[170:171], v34 offset:33792\n" \
"v_mfma_f32_16x16x32_bf16 v[152:155], a[152:155], a[180:183], v[152:155]\n" \
"ds_read_b64_tr_b16 a[232:233], v34 offset:40960\n" \
"v_mfma_f32_16x16x32_bf16 v[152:155], a[216:219], a[244:247], v[152:155]\n" \
"ds_read_b64_tr_b16 a[234:235], v34 offset:41984\n" \
"v_mfma_f32_16x16x32_bf16 v[156:159], a[152:155], a[184:187], v[156:159]\n" \
"ds_read_b64_tr_b16 a[172:173], v35 offset:32768\n" \
"v_mfma_f32_16x16x32_bf16 v[156:159], a[216:219], a[248:251], v[156:159]\n" \
"ds_read_b64_tr_b16 a[174:175], v35 offset:33792\n" \
"v_mfma_f32_16x16x32_bf16 v[160:163], a[152:155], a[188:191], v[160:163]\n" \
"ds_read_b64_tr_b16 a[236:237], v35 offset:40960\n" \
"v_mfma_f32_16x16x32_bf16 v[160:163], a[216:219], a[252:255], v[160:163]\n" \
"ds_read_b128 a[152:155], v28 offset:36864\n" \
"v_mfma_f32_16x16x32_bf16 v[176:179], a[156:159], a[188:191], v[176:179]\n" \
"ds_read_b128 a[216:219], v29 offset:36864\n" \
"v_mfma_f32_16x16x32_bf16 v[176:179], a[220:223], a[252:255], v[176:179]\n" \
"ds_read_b64_tr_b16 a[238:239], v35 offset:41984\n" \
"v_mfma_f32_16x16x32_bf16 v[172:175], a[156:159], a[184:187], v[172:175]\n" \
"v_mfma_f32_16x16x32_bf16 v[172:175], a[220:223], a[248:251], v[172:175]\n" \
"v_mfma_f32_16x16x32_bf16 v[168:171], a[156:159], a[180:183], v[168:171]\n" \
"v_mfma_f32_16x16x32_bf16 v[168:171], a[220:223], a[244:247], v[168:171]\n" \
"v_mfma_f32_16x16x32_bf16 v[164:167], a[156:159], a[176:179], v[164:167]\n" \
"v_mfma_f32_16x16x32_bf16 v[164:167], a[220:223], a[240:243], v[164:167]\n" \
"ds_read_b128 a[156:159], v28 offset:38912\n" \
"ds_read_b128 a[220:223], v29 offset:38912\n" \
"s_waitcnt lgkmcnt(15)\n" \
"v_mfma_f32_16x16x32_bf16 a[0:3], a[128:131], a[160:163], a[0:3]\n" \
"s_addk_i32 s84, 0x7c00\n" \
"v_mfma_f32_16x16x32_bf16 a[0:3], a[192:195], a[224:227], a[0:3]\n" \
"buffer_load_dwordx4 v27, s[72:75], s84 offen offset:2048 lds\n" \
"v_mfma_f32_16x16x32_bf16 a[4:7], a[128:131], a[164:167], a[4:7]\n" \
"ds_read_b64_tr_b16 a[176:177], v36 offset:32768\n" \
"s_waitcnt lgkmcnt(15)\n" \
"v_mfma_f32_16x16x32_bf16 a[4:7], a[192:195], a[228:231], a[4:7]\n" \
"s_addk_i32 s84, 0x7c00\n" \
"s_waitcnt lgkmcnt(11)\n" \
"v_mfma_f32_16x16x32_bf16 a[8:11], a[128:131], a[168:171], a[8:11]\n" \
"buffer_load_dwordx4 v27, s[72:75], s84 offen offset:3072 lds\n" \
"s_waitcnt lgkmcnt(9)\n" \
"v_mfma_f32_16x16x32_bf16 a[8:11], a[192:195], a[232:235], a[8:11]\n" \
"ds_read_b64_tr_b16 a[178:179], v36 offset:33792\n" \
"s_waitcnt lgkmcnt(8)\n" \
"v_mfma_f32_16x16x32_bf16 a[12:15], a[128:131], a[172:175], a[12:15]\n" \
"ds_read_b64_tr_b16 a[240:241], v36 offset:40960\n" \
"s_waitcnt lgkmcnt(5)\n" \
"v_mfma_f32_16x16x32_bf16 a[12:15], a[192:195], a[236:239], a[12:15]\n" \
"v_mfma_f32_16x16x32_bf16 a[28:31], a[132:135], a[172:175], a[28:31]\n" \
"v_mfma_f32_16x16x32_bf16 a[28:31], a[196:199], a[236:239], a[28:31]\n" \
"ds_read_b64_tr_b16 a[242:243], v36 offset:41984\n" \
"v_mfma_f32_16x16x32_bf16 a[24:27], a[132:135], a[168:171], a[24:27]\n" \
"v_mfma_f32_16x16x32_bf16 a[24:27], a[196:199], a[232:235], a[24:27]\n" \
"v_mfma_f32_16x16x32_bf16 a[20:23], a[132:135], a[164:167], a[20:23]\n" \
"v_mfma_f32_16x16x32_bf16 a[20:23], a[196:199], a[228:231], a[20:23]\n" \
"ds_read_b64_tr_b16 a[180:181], v37 offset:32768\n" \
"v_mfma_f32_16x16x32_bf16 a[16:19], a[132:135], a[160:163], a[16:19]\n" \
"v_mfma_f32_16x16x32_bf16 a[16:19], a[196:199], a[224:227], a[16:19]\n" \
"v_mfma_f32_16x16x32_bf16 a[32:35], a[136:139], a[160:163], a[32:35]\n" \
"ds_read_b64_tr_b16 a[182:183], v37 offset:33792\n" \
"v_mfma_f32_16x16x32_bf16 a[32:35], a[200:203], a[224:227], a[32:35]\n" \
"v_mfma_f32_16x16x32_bf16 a[36:39], a[136:139], a[164:167], a[36:39]\n" \
"v_lshlrev_b32_e32 v45, 3, v20\n" \
"v_add_u32_e32 v45, s31, v45\n" \
"v_mfma_f32_16x16x32_bf16 a[36:39], a[200:203], a[228:231], a[36:39]\n" \
"s_getpc_b64 s[86:87]\n" \
"s_sub_u32 s86, s86, 0x10800\n" \
"s_subb_u32 s87, s87, 0\n" \
"s_mov_b32 m0, s32\n" \
"s_nop 0\n" \
"v_mfma_f32_16x16x32_bf16 a[40:43], a[136:139], a[168:171], a[40:43]\n" \
"global_load_lds_dword v45, s[86:87]\n" \
"v_mfma_f32_16x16x32_bf16 a[40:43], a[200:203], a[232:235], a[40:43]\n" \
"ds_read_b64_tr_b16 a[244:245], v37 offset:40960\n" \
"v_mfma_f32_16x16x32_bf16 a[44:47], a[136:139], a[172:175], a[44:47]\n" \
"ds_read_b64_tr_b16 a[246:247], v37 offset:41984\n" \
"v_mfma_f32_16x16x32_bf16 a[44:47], a[200:203], a[236:239], a[44:47]\n" \
"v_mfma_f32_16x16x32_bf16 a[60:63], a[140:143], a[172:175], a[60:63]\n" \
"v_mfma_f32_16x16x32_bf16 a[60:63], a[204:207], a[236:239], a[60:63]\n" \
"v_mfma_f32_16x16x32_bf16 a[56:59], a[140:143], a[168:171], a[56:59]\n" \
"ds_read_b64_tr_b16 a[184:185], v38 offset:32768\n" \
"v_mfma_f32_16x16x32_bf16 a[56:59], a[204:207], a[232:235], a[56:59]\n" \
"v_mfma_f32_16x16x32_bf16 a[52:55], a[140:143], a[164:167], a[52:55]\n" \
"v_mfma_f32_16x16x32_bf16 a[52:55], a[204:207], a[228:231], a[52:55]\n" \
"ds_read_b64_tr_b16 a[186:187], v38 offset:33792\n" \
"v_mfma_f32_16x16x32_bf16 a[48:51], a[140:143], a[160:163], a[48:51]\n" \
"v_mfma_f32_16x16x32_bf16 a[48:51], a[204:207], a[224:227], a[48:51]\n" \
"v_mfma_f32_16x16x32_bf16 a[64:67], a[144:147], a[160:163], a[64:67]\n" \
"ds_read_b64_tr_b16 a[248:249], v38 offset:40960\n" \
"v_mfma_f32_16x16x32_bf16 a[64:67], a[208:211], a[224:227], a[64:67]\n" \
"v_mfma_f32_16x16x32_bf16 a[68:71], a[144:147], a[164:167], a[68:71]\n" \
"v_mfma_f32_16x16x32_bf16 a[68:71], a[208:211], a[228:231], a[68:71]\n" \
"ds_read_b64_tr_b16 a[250:251], v38 offset:41984\n" \
"v_mfma_f32_16x16x32_bf16 a[72:75], a[144:147], a[168:171], a[72:75]\n" \
"v_mfma_f32_16x16x32_bf16 a[72:75], a[208:211], a[232:235], a[72:75]\n" \
"v_mfma_f32_16x16x32_bf16 a[76:79], a[144:147], a[172:175], a[76:79]\n" \
"v_mfma_f32_16x16x32_bf16 a[76:79], a[208:211], a[236:239], a[76:79]\n" \
"ds_read_b64_tr_b16 a[188:189], v39 offset:32768\n" \
"v_mfma_f32_16x16x32_bf16 a[92:95], a[148:151], a[172:175], a[92:95]\n" \
"v_mfma_f32_16x16x32_bf16 a[92:95], a[212:215], a[236:239], a[92:95]\n" \
"v_mfma_f32_16x16x32_bf16 a[88:91], a[148:151], a[168:171], a[88:91]\n" \
"ds_read_b64_tr_b16 a[190:191], v39 offset:33792\n" \
"v_mfma_f32_16x16x32_bf16 a[88:91], a[212:215], a[232:235], a[88:91]\n" \
"v_mfma_f32_16x16x32_bf16 a[84:87], a[148:151], a[164:167], a[84:87]\n" \
"v_mfma_f32_16x16x32_bf16 a[84:87], a[212:215], a[228:231], a[84:87]\n" \
"ds_read_b64_tr_b16 a[252:253], v39 offset:40960\n" \
"v_mfma_f32_16x16x32_bf16 a[80:83], a[148:151], a[160:163], a[80:83]\n" \
"v_mfma_f32_16x16x32_bf16 a[80:83], a[212:215], a[224:227], a[80:83]\n" \
"v_mfma_f32_16x16x32_bf16 a[96:99], a[152:155], a[160:163], a[96:99]\n" \
"ds_read_b64_tr_b16 a[254:255], v39 offset:41984\n" \
"v_mfma_f32_16x16x32_bf16 a[96:99], a[216:219], a[224:227], a[96:99]\n" \
"v_mfma_f32_16x16x32_bf16 a[100:103], a[152:155], a[164:167], a[100:103]\n" \
"v_mfma_f32_16x16x32_bf16 a[100:103], a[216:219], a[228:231], a[100:103]\n" \
"v_mfma_f32_16x16x32_bf16 a[104:107], a[152:155], a[168:171], a[104:107]\n" \
"v_mfma_f32_16x16x32_bf16 a[104:107], a[216:219], a[232:235], a[104:107]\n" \
"v_mfma_f32_16x16x32_bf16 a[108:111], a[152:155], a[172:175], a[108:111]\n" \
"v_mfma_f32_16x16x32_bf16 a[108:111], a[216:219], a[236:239], a[108:111]\n" \
"s_waitcnt lgkmcnt(15)\n" \
"v_mfma_f32_16x16x32_bf16 a[124:127], a[156:159], a[172:175], a[124:127]\n" \
"v_mfma_f32_16x16x32_bf16 a[124:127], a[220:223], a[236:239], a[124:127]\n" \
"v_mfma_f32_16x16x32_bf16 a[120:123], a[156:159], a[168:171], a[120:123]\n" \
"v_mfma_f32_16x16x32_bf16 a[120:123], a[220:223], a[232:235], a[120:123]\n" \
"v_mfma_f32_16x16x32_bf16 a[116:119], a[156:159], a[164:167], a[116:119]\n" \
"v_mfma_f32_16x16x32_bf16 a[116:119], a[220:223], a[228:231], a[116:119]\n" \
"v_mfma_f32_16x16x32_bf16 a[112:115], a[156:159], a[160:163], a[112:115]\n" \
"s_waitcnt vmcnt(1) lgkmcnt(0)\n" \
"v_mfma_f32_16x16x32_bf16 a[112:115], a[220:223], a[224:227], a[112:115]\n" \
"s_barrier\n" \
"v_mov_b32_e32 v45, s62\n" \
"s_mov_b64 exec, 1\n" \
"buffer_store_dword v45, off, s[80:83], s37 offset:128 sc1\n" \
"s_mov_b64 exec, -1\n" \
"v_mfma_f32_16x16x32_bf16 v[52:55], a[128:131], a[176:179], v[52:55]\n" \
"s_add_u32 m0, s30, 0x8000\n" \
"s_movk_i32 s84, 0x380\n" \
"v_mfma_f32_16x16x32_bf16 v[52:55], a[192:195], a[240:243], v[52:55]\n" \
"buffer_load_dwordx4 v22, s[64:67], s84 offen offset:0 lds\n" \
"v_mfma_f32_16x16x32_bf16 v[56:59], a[128:131], a[180:183], v[56:59]\n" \
"ds_read_b64_tr_b16 a[160:161], v32 offset:0\n" \
"v_mfma_f32_16x16x32_bf16 v[56:59], a[192:195], a[244:247], v[56:59]\n" \
"s_movk_i32 s84, 0x1f80\n" \
"v_mfma_f32_16x16x32_bf16 v[60:63], a[128:131], a[184:187], v[60:63]\n" \
"buffer_load_dwordx4 v23, s[64:67], s84 offen offset:1024 lds\n" \
"v_mfma_f32_16x16x32_bf16 v[60:63], a[192:195], a[248:251], v[60:63]\n" \
"ds_read_b64_tr_b16 a[162:163], v32 offset:1024\n" \
"v_mfma_f32_16x16x32_bf16 v[64:67], a[128:131], a[188:191], v[64:67]\n" \
"ds_read_b64_tr_b16 a[224:225], v32 offset:8192\n" \
"v_mfma_f32_16x16x32_bf16 v[64:67], a[192:195], a[252:255], v[64:67]\n" \
"ds_read_b128 a[128:131], v28 offset:16384\n" \
"v_mfma_f32_16x16x32_bf16 v[80:83], a[132:135], a[188:191], v[80:83]\n" \
"ds_read_b128 a[192:195], v29 offset:16384\n" \
"v_mfma_f32_16x16x32_bf16 v[80:83], a[196:199], a[252:255], v[80:83]\n" \
"s_movk_i32 s84, 0x3b80\n" \
"v_mfma_f32_16x16x32_bf16 v[76:79], a[132:135], a[184:187], v[76:79]\n" \
"buffer_load_dwordx4 v22, s[64:67], s84 offen offset:2048 lds\n" \
"v_mfma_f32_16x16x32_bf16 v[76:79], a[196:199], a[248:251], v[76:79]\n" \
"s_movk_i32 s84, 0x5780\n" \
"v_mfma_f32_16x16x32_bf16 v[72:75], a[132:135], a[180:183], v[72:75]\n" \
"buffer_load_dwordx4 v23, s[64:67], s84 offen offset:3072 lds\n" \
"v_mfma_f32_16x16x32_bf16 v[72:75], a[196:199], a[244:247], v[72:75]\n" \
"s_add_u32 m0, s31, 0x8000\n" \
"s_mov_b32 s84, 0x380000\n" \
"v_mfma_f32_16x16x32_bf16 v[68:71], a[132:135], a[176:179], v[68:71]\n" \
"buffer_load_dwordx4 v26, s[68:71], s84 offen offset:0 lds\n" \
"v_mfma_f32_16x16x32_bf16 v[68:71], a[196:199], a[240:243], v[68:71]\n" \
"ds_read_b128 a[132:135], v28 offset:18432\n" \
"v_mfma_f32_16x16x32_bf16 v[84:87], a[136:139], a[176:179], v[84:87]\n" \
"ds_read_b128 a[196:199], v29 offset:18432\n" \
"v_mfma_f32_16x16x32_bf16 v[84:87], a[200:203], a[240:243], v[84:87]\n" \
"s_addk_i32 s84, 0x7c00\n" \
"v_mfma_f32_16x16x32_bf16 v[88:91], a[136:139], a[180:183], v[88:91]\n" \
"buffer_load_dwordx4 v26, s[68:71], s84 offen offset:1024 lds\n" \
"v_mfma_f32_16x16x32_bf16 v[88:91], a[200:203], a[244:247], v[88:91]\n" \
"ds_read_b64_tr_b16 a[226:227], v32 offset:9216\n" \
"v_mfma_f32_16x16x32_bf16 v[92:95], a[136:139], a[184:187], v[92:95]\n" \
"s_addk_i32 s84, 0x7c00\n" \
"v_mfma_f32_16x16x32_bf16 v[92:95], a[200:203], a[248:251], v[92:95]\n" \
"buffer_load_dwordx4 v27, s[68:71], s84 offen offset:2048 lds\n" \
"v_mfma_f32_16x16x32_bf16 v[96:99], a[136:139], a[188:191], v[96:99]\n" \
"ds_read_b64_tr_b16 a[164:165], v33 offset:0\n" \
"v_mfma_f32_16x16x32_bf16 v[96:99], a[200:203], a[252:255], v[96:99]\n" \
"ds_read_b128 a[136:139], v28 offset:20480\n" \
"v_mfma_f32_16x16x32_bf16 v[112:115], a[140:143], a[188:191], v[112:115]\n" \
"ds_read_b128 a[200:203], v29 offset:20480\n" \
"v_mfma_f32_16x16x32_bf16 v[112:115], a[204:207], a[252:255], v[112:115]\n" \
"s_addk_i32 s84, 0x7c00\n" \
"s_nop 0\n" \
"v_mfma_f32_16x16x32_bf16 v[108:111], a[140:143], a[184:187], v[108:111]\n" \
"buffer_load_dwordx4 v27, s[68:71], s84 offen offset:3072 lds\n" \
"v_mfma_f32_16x16x32_bf16 v[108:111], a[204:207], a[248:251], v[108:111]\n" \
"s_add_u32 m0, s30, 0x9000\n" \
"s_mov_b32 s84, 0x8380\n" \
"v_mfma_f32_16x16x32_bf16 v[104:107], a[140:143], a[180:183], v[104:107]\n" \
"buffer_load_dwordx4 v22, s[64:67], s84 offen offset:0 lds\n" \
"v_mfma_f32_16x16x32_bf16 v[104:107], a[204:207], a[244:247], v[104:107]\n" \
"ds_read_b64_tr_b16 a[166:167], v33 offset:1024\n" \
"v_mfma_f32_16x16x32_bf16 v[100:103], a[140:143], a[176:179], v[100:103]\n" \
"ds_read_b64_tr_b16 a[228:229], v33 offset:8192\n" \
"v_mfma_f32_16x16x32_bf16 v[100:103], a[204:207], a[240:243], v[100:103]\n" \
"ds_read_b128 a[140:143], v28 offset:22528\n" \
"v_mfma_f32_16x16x32_bf16 v[116:119], a[144:147], a[176:179], v[116:119]\n" \
"ds_read_b128 a[204:207], v29 offset:22528\n" \
"v_mfma_f32_16x16x32_bf16 v[116:119], a[208:211], a[240:243], v[116:119]\n" \
"s_addk_i32 s84, 0x1c00\n" \
"v_mfma_f32_16x16x32_bf16 v[120:123], a[144:147], a[180:183], v[120:123]\n" \
"buffer_load_dwordx4 v23, s[64:67], s84 offen offset:1024 lds\n" \
"v_mfma_f32_16x16x32_bf16 v[120:123], a[208:211], a[244:247], v[120:123]\n" \
"s_addk_i32 s84, 0x1c00\n" \
"v_mfma_f32_16x16x32_bf16 v[124:127], a[144:147], a[184:187], v[124:127]\n" \
"buffer_load_dwordx4 v22, s[64:67], s84 offen offset:2048 lds\n" \
"v_mfma_f32_16x16x32_bf16 v[124:127], a[208:211], a[248:251], v[124:127]\n" \
"s_addk_i32 s84, 0x1c00\n" \
"s_nop 0\n" \
"v_mfma_f32_16x16x32_bf16 v[128:131], a[144:147], a[188:191], v[128:131]\n" \
"buffer_load_dwordx4 v23, s[64:67], s84 offen offset:3072 lds\n" \
"v_mfma_f32_16x16x32_bf16 v[128:131], a[208:211], a[252:255], v[128:131]\n" \
"ds_read_b128 a[144:147], v28 offset:0\n" \
"v_mfma_f32_16x16x32_bf16 v[144:147], a[148:151], a[188:191], v[144:147]\n" \
"ds_read_b128 a[208:211], v29 offset:0\n" \
"v_mfma_f32_16x16x32_bf16 v[144:147], a[212:215], a[252:255], v[144:147]\n" \
"s_add_u32 m0, s31, 0xc000\n" \
"s_mov_b32 s84, 0x380000\n" \
"v_mfma_f32_16x16x32_bf16 v[140:143], a[148:151], a[184:187], v[140:143]\n" \
"buffer_load_dwordx4 v26, s[72:75], s84 offen offset:0 lds\n" \
"v_mfma_f32_16x16x32_bf16 v[140:143], a[212:215], a[248:251], v[140:143]\n" \
"ds_read_b64_tr_b16 a[230:231], v33 offset:9216\n" \
"v_mfma_f32_16x16x32_bf16 v[136:139], a[148:151], a[180:183], v[136:139]\n" \
"s_addk_i32 s84, 0x7c00\n" \
"s_nop 0\n" \
"v_mfma_f32_16x16x32_bf16 v[136:139], a[212:215], a[244:247], v[136:139]\n" \
"buffer_load_dwordx4 v26, s[72:75], s84 offen offset:1024 lds\n" \
"v_mfma_f32_16x16x32_bf16 v[132:135], a[148:151], a[176:179], v[132:135]\n" \
"ds_read_b64_tr_b16 a[168:169], v34 offset:0\n" \
"v_mfma_f32_16x16x32_bf16 v[132:135], a[212:215], a[240:243], v[132:135]\n" \
"ds_read_b128 a[148:151], v28 offset:2048\n" \
"v_mfma_f32_16x16x32_bf16 v[148:151], a[152:155], a[176:179], v[148:151]\n" \
"ds_read_b128 a[212:215], v29 offset:2048\n" \
"v_mfma_f32_16x16x32_bf16 v[148:151], a[216:219], a[240:243], v[148:151]\n" \
"ds_read_b64_tr_b16 a[170:171], v34 offset:1024\n" \
"v_mfma_f32_16x16x32_bf16 v[152:155], a[152:155], a[180:183], v[152:155]\n" \
"ds_read_b64_tr_b16 a[232:233], v34 offset:8192\n" \
"v_mfma_f32_16x16x32_bf16 v[152:155], a[216:219], a[244:247], v[152:155]\n" \
"ds_read_b64_tr_b16 a[234:235], v34 offset:9216\n" \
"v_mfma_f32_16x16x32_bf16 v[156:159], a[152:155], a[184:187], v[156:159]\n" \
"ds_read_b64_tr_b16 a[172:173], v35 offset:0\n" \
"v_mfma_f32_16x16x32_bf16 v[156:159], a[216:219], a[248:251], v[156:159]\n" \
"ds_read_b64_tr_b16 a[174:175], v35 offset:1024\n" \
"v_mfma_f32_16x16x32_bf16 v[160:163], a[152:155], a[188:191], v[160:163]\n" \
"ds_read_b64_tr_b16 a[236:237], v35 offset:8192\n" \
"v_mfma_f32_16x16x32_bf16 v[160:163], a[216:219], a[252:255], v[160:163]\n" \
"ds_read_b128 a[152:155], v28 offset:4096\n" \
"v_mfma_f32_16x16x32_bf16 v[176:179], a[156:159], a[188:191], v[176:179]\n" \
"ds_read_b128 a[216:219], v29 offset:4096\n" \
"v_mfma_f32_16x16x32_bf16 v[176:179], a[220:223], a[252:255], v[176:179]\n" \
"ds_read_b64_tr_b16 a[238:239], v35 offset:9216\n" \
"v_mfma_f32_16x16x32_bf16 v[172:175], a[156:159], a[184:187], v[172:175]\n" \
"v_mfma_f32_16x16x32_bf16 v[172:175], a[220:223], a[248:251], v[172:175]\n" \
"v_mfma_f32_16x16x32_bf16 v[168:171], a[156:159], a[180:183], v[168:171]\n" \
"v_mfma_f32_16x16x32_bf16 v[168:171], a[220:223], a[244:247], v[168:171]\n" \
"v_mfma_f32_16x16x32_bf16 v[164:167], a[156:159], a[176:179], v[164:167]\n" \
"v_mfma_f32_16x16x32_bf16 v[164:167], a[220:223], a[240:243], v[164:167]\n" \
"ds_read_b128 a[156:159], v28 offset:6144\n" \
"ds_read_b128 a[220:223], v29 offset:6144\n" \
"s_waitcnt lgkmcnt(15)\n" \
"v_mfma_f32_16x16x32_bf16 a[0:3], a[128:131], a[160:163], a[0:3]\n" \
"s_addk_i32 s84, 0x7c00\n" \
"v_mfma_f32_16x16x32_bf16 a[0:3], a[192:195], a[224:227], a[0:3]\n" \
"buffer_load_dwordx4 v27, s[72:75], s84 offen offset:2048 lds\n" \
"v_mfma_f32_16x16x32_bf16 a[4:7], a[128:131], a[164:167], a[4:7]\n" \
"ds_read_b64_tr_b16 a[176:177], v36 offset:0\n" \
"s_waitcnt lgkmcnt(15)\n" \
"v_mfma_f32_16x16x32_bf16 a[4:7], a[192:195], a[228:231], a[4:7]\n" \
"s_addk_i32 s84, 0x7c00\n" \
"s_waitcnt lgkmcnt(11)\n" \
"v_mfma_f32_16x16x32_bf16 a[8:11], a[128:131], a[168:171], a[8:11]\n" \
"buffer_load_dwordx4 v27, s[72:75], s84 offen offset:3072 lds\n" \
"s_waitcnt lgkmcnt(9)\n" \
"v_mfma_f32_16x16x32_bf16 a[8:11], a[192:195], a[232:235], a[8:11]\n" \
"ds_read_b64_tr_b16 a[178:179], v36 offset:1024\n" \
"s_waitcnt lgkmcnt(8)\n" \
"v_mfma_f32_16x16x32_bf16 a[12:15], a[128:131], a[172:175], a[12:15]\n" \
"ds_read_b64_tr_b16 a[240:241], v36 offset:8192\n" \
"s_waitcnt lgkmcnt(5)\n" \
"v_mfma_f32_16x16x32_bf16 a[12:15], a[192:195], a[236:239], a[12:15]\n" \
"v_mfma_f32_16x16x32_bf16 a[28:31], a[132:135], a[172:175], a[28:31]\n" \
"v_mfma_f32_16x16x32_bf16 a[28:31], a[196:199], a[236:239], a[28:31]\n" \
"ds_read_b64_tr_b16 a[242:243], v36 offset:9216\n" \
"v_mfma_f32_16x16x32_bf16 a[24:27], a[132:135], a[168:171], a[24:27]\n" \
"v_mfma_f32_16x16x32_bf16 a[24:27], a[196:199], a[232:235], a[24:27]\n" \
"v_mfma_f32_16x16x32_bf16 a[20:23], a[132:135], a[164:167], a[20:23]\n" \
"v_mfma_f32_16x16x32_bf16 a[20:23], a[196:199], a[228:231], a[20:23]\n" \
"ds_read_b64_tr_b16 a[180:181], v37 offset:0\n" \
"v_mfma_f32_16x16x32_bf16 a[16:19], a[132:135], a[160:163], a[16:19]\n" \
"v_mfma_f32_16x16x32_bf16 a[16:19], a[196:199], a[224:227], a[16:19]\n" \
"v_mfma_f32_16x16x32_bf16 a[32:35], a[136:139], a[160:163], a[32:35]\n" \
"v_bfe_u32 v44, v20, 2, 5\n" \
"ds_read_b64_tr_b16 a[182:183], v37 offset:1024\n" \
"v_mfma_f32_16x16x32_bf16 a[32:35], a[200:203], a[224:227], a[32:35]\n" \
"buffer_load_dword v44, v44, s[80:83], s38 offen offset:128 sc1\n" \
"v_mfma_f32_16x16x32_bf16 a[36:39], a[136:139], a[164:167], a[36:39]\n" \
"v_mfma_f32_16x16x32_bf16 a[36:39], a[200:203], a[228:231], a[36:39]\n" \
"ds_read_b64_tr_b16 a[244:245], v37 offset:8192\n" \
"v_mfma_f32_16x16x32_bf16 a[40:43], a[136:139], a[168:171], a[40:43]\n" \
"v_mfma_f32_16x16x32_bf16 a[40:43], a[200:203], a[232:235], a[40:43]\n" \
"v_mfma_f32_16x16x32_bf16 a[44:47], a[136:139], a[172:175], a[44:47]\n" \
"ds_read_b64_tr_b16 a[246:247], v37 offset:9216\n" \
"v_mfma_f32_16x16x32_bf16 a[44:47], a[200:203], a[236:239], a[44:47]\n" \
"v_mfma_f32_16x16x32_bf16 a[60:63], a[140:143], a[172:175], a[60:63]\n" \
"global_store_dwordx4 v49, v[0:3], s[28:29] offset:0 sc1\n" \
"v_mfma_f32_16x16x32_bf16 a[60:63], a[204:207], a[236:239], a[60:63]\n" \
"v_mfma_f32_16x16x32_bf16 a[56:59], a[140:143], a[168:171], a[56:59]\n" \
"ds_read_b64_tr_b16 a[184:185], v38 offset:0\n" \
"v_mfma_f32_16x16x32_bf16 a[56:59], a[204:207], a[232:235], a[56:59]\n" \
"v_mfma_f32_16x16x32_bf16 a[52:55], a[140:143], a[164:167], a[52:55]\n" \
"v_mfma_f32_16x16x32_bf16 a[52:55], a[204:207], a[228:231], a[52:55]\n" \
"ds_read_b64_tr_b16 a[186:187], v38 offset:1024\n" \
"v_mfma_f32_16x16x32_bf16 a[48:51], a[140:143], a[160:163], a[48:51]\n" \
"v_mfma_f32_16x16x32_bf16 a[48:51], a[204:207], a[224:227], a[48:51]\n" \
"v_mfma_f32_16x16x32_bf16 a[64:67], a[144:147], a[160:163], a[64:67]\n" \
"global_store_dwordx4 v49, v[0:3], s[28:29] offset:1024 sc1\n" \
"v_mfma_f32_16x16x32_bf16 a[64:67], a[208:211], a[224:227], a[64:67]\n" \
"ds_read_b64_tr_b16 a[248:249], v38 offset:8192\n" \
"v_mfma_f32_16x16x32_bf16 a[68:71], a[144:147], a[164:167], a[68:71]\n" \
"v_mfma_f32_16x16x32_bf16 a[68:71], a[208:211], a[228:231], a[68:71]\n" \
"ds_read_b64_tr_b16 a[250:251], v38 offset:9216\n" \
"v_mfma_f32_16x16x32_bf16 a[72:75], a[144:147], a[168:171], a[72:75]\n" \
"v_mfma_f32_16x16x32_bf16 a[72:75], a[208:211], a[232:235], a[72:75]\n" \
"v_mfma_f32_16x16x32_bf16 a[76:79], a[144:147], a[172:175], a[76:79]\n" \
"v_mfma_f32_16x16x32_bf16 a[76:79], a[208:211], a[236:239], a[76:79]\n" \
"ds_read_b64_tr_b16 a[188:189], v39 offset:0\n" \
"v_mfma_f32_16x16x32_bf16 a[92:95], a[148:151], a[172:175], a[92:95]\n" \
"s_mov_b64 exec, 0xffff\n" \
"global_store_dwordx4 v49, v[0:3], s[28:29] offset:2048 sc1\n" \
"s_mov_b64 exec, -1\n" \
"s_nop 0\n" \
"v_mfma_f32_16x16x32_bf16 a[92:95], a[212:215], a[236:239], a[92:95]\n" \
"v_mfma_f32_16x16x32_bf16 a[88:91], a[148:151], a[168:171], a[88:91]\n" \
"ds_read_b64_tr_b16 a[190:191], v39 offset:1024\n" \
"v_mfma_f32_16x16x32_bf16 a[88:91], a[212:215], a[232:235], a[88:91]\n" \
"v_mfma_f32_16x16x32_bf16 a[84:87], a[148:151], a[164:167], a[84:87]\n" \
"v_mfma_f32_16x16x32_bf16 a[84:87], a[212:215], a[228:231], a[84:87]\n" \
"ds_read_b64_tr_b16 a[252:253], v39 offset:8192\n" \
"v_mfma_f32_16x16x32_bf16 a[80:83], a[148:151], a[160:163], a[80:83]\n" \
"v_mfma_f32_16x16x32_bf16 a[80:83], a[212:215], a[224:227], a[80:83]\n" \
"v_mfma_f32_16x16x32_bf16 a[96:99], a[152:155], a[160:163], a[96:99]\n" \
"ds_read_b64_tr_b16 a[254:255], v39 offset:9216\n" \
"v_mfma_f32_16x16x32_bf16 a[96:99], a[216:219], a[224:227], a[96:99]\n" \
"v_mfma_f32_16x16x32_bf16 a[100:103], a[152:155], a[164:167], a[100:103]\n" \
"v_mfma_f32_16x16x32_bf16 a[100:103], a[216:219], a[228:231], a[100:103]\n" \
"v_mfma_f32_16x16x32_bf16 a[104:107], a[152:155], a[168:171], a[104:107]\n" \
"v_mfma_f32_16x16x32_bf16 a[104:107], a[216:219], a[232:235], a[104:107]\n" \
"v_mfma_f32_16x16x32_bf16 a[108:111], a[152:155], a[172:175], a[108:111]\n" \
"v_mfma_f32_16x16x32_bf16 a[108:111], a[216:219], a[236:239], a[108:111]\n" \
"s_waitcnt lgkmcnt(15)\n" \
"v_mfma_f32_16x16x32_bf16 a[124:127], a[156:159], a[172:175], a[124:127]\n" \
"v_mfma_f32_16x16x32_bf16 a[124:127], a[220:223], a[236:239], a[124:127]\n" \
"v_mfma_f32_16x16x32_bf16 a[120:123], a[156:159], a[168:171], a[120:123]\n" \
"v_mfma_f32_16x16x32_bf16 a[120:123], a[220:223], a[232:235], a[120:123]\n" \
"v_mfma_f32_16x16x32_bf16 a[116:119], a[156:159], a[164:167], a[116:119]\n" \
"v_mfma_f32_16x16x32_bf16 a[116:119], a[220:223], a[228:231], a[116:119]\n" \
"v_mfma_f32_16x16x32_bf16 a[112:115], a[156:159], a[160:163], a[112:115]\n" \
"s_waitcnt vmcnt(3) lgkmcnt(0)\n" \
"v_mfma_f32_16x16x32_bf16 a[112:115], a[220:223], a[224:227], a[112:115]\n" \
"s_barrier\n" \
"v_cmp_ne_u32_e32 vcc, s62, v44\n" \
"s_nop 4\n" \
"s_cbranch_vccnz L_poll61_%=\n" \
"L_pollr61_%=:\n" \
"s_mov_b32 s68, s12\n" \
"s_and_b32 s69, s13, 0xffff\n" \
"s_mov_b32 s70, 0x480000\n" \
"s_mov_b32 s71, 0x20000\n" \
"v_mfma_f32_16x16x32_bf16 v[52:55], a[128:131], a[176:179], v[52:55]\n" \
"s_add_u32 m0, s40, 0x0\n" \
"s_add_u32 s85, s39, 0x0\n" \
"s_nop 0\n" \
"v_mfma_f32_16x16x32_bf16 v[52:55], a[192:195], a[240:243], v[52:55]\n" \
"buffer_load_dwordx4 v20, s[80:83], s85 offen sc1 lds\n" \
"v_mfma_f32_16x16x32_bf16 v[56:59], a[128:131], a[180:183], v[56:59]\n" \
"ds_read_b64_tr_b16 a[160:161], v32 offset:32768\n" \
"v_mfma_f32_16x16x32_bf16 v[56:59], a[192:195], a[244:247], v[56:59]\n" \
"s_add_u32 m0, s40, 0x1000\n" \
"s_add_u32 s85, s39, 0x1000\n" \
".fill (L_pb + 57056 - .) / 4, 4, 0xBF800000\n" \
"L_pp14:\n" \
"s_cbranch_execz L_pret\n" \
"s_nop 0\n" \
"v_mfma_f32_16x16x32_bf16 v[60:63], a[128:131], a[184:187], v[60:63]\n" \
"buffer_load_dwordx4 v20, s[80:83], s85 offen sc1 lds\n" \
"v_mfma_f32_16x16x32_bf16 v[60:63], a[192:195], a[248:251], v[60:63]\n" \
"ds_read_b64_tr_b16 a[162:163], v32 offset:33792\n" \
"v_mfma_f32_16x16x32_bf16 v[64:67], a[128:131], a[188:191], v[64:67]\n" \
"ds_read_b64_tr_b16 a[224:225], v32 offset:40960\n" \
"v_mfma_f32_16x16x32_bf16 v[64:67], a[192:195], a[252:255], v[64:67]\n" \
"ds_read_b128 a[128:131], v28 offset:49152\n" \
"v_mfma_f32_16x16x32_bf16 v[80:83], a[132:135], a[188:191], v[80:83]\n" \
"ds_read_b128 a[192:195], v29 offset:49152\n" \
"v_mfma_f32_16x16x32_bf16 v[80:83], a[196:199], a[252:255], v[80:83]\n" \
"s_add_u32 m0, s40, 0x3000\n" \
"s_movk_i32 s84, 0x0\n" \
"v_mfma_f32_16x16x32_bf16 v[76:79], a[132:135], a[184:187], v[76:79]\n" \
"buffer_load_dwordx4 v21, s[76:79], s84 offen offset:0 lds\n" \
"v_mfma_f32_16x16x32_bf16 v[76:79], a[196:199], a[248:251], v[76:79]\n" \
"s_add_u32 m0, s30, 0x0\n" \
"s_movk_i32 s84, 0x0\n" \
"s_nop 0\n" \
"v_mfma_f32_16x16x32_bf16 v[72:75], a[132:135], a[180:183], v[72:75]\n" \
"buffer_load_dwordx4 v24, s[76:79], s84 offen offset:0 lds\n" \
"v_mfma_f32_16x16x32_bf16 v[72:75], a[196:199], a[244:247], v[72:75]\n" \
"ds_read_b64_tr_b16 a[226:227], v32 offset:41984\n" \
"v_mfma_f32_16x16x32_bf16 v[68:71], a[132:135], a[176:179], v[68:71]\n" \
"ds_read_b64_tr_b16 a[164:165], v33 offset:32768\n" \
"v_mfma_f32_16x16x32_bf16 v[68:71], a[196:199], a[240:243], v[68:71]\n" \
"ds_read_b128 a[132:135], v28 offset:51200\n" \
"v_mfma_f32_16x16x32_bf16 v[84:87], a[136:139], a[176:179], v[84:87]\n" \
"ds_read_b128 a[196:199], v29 offset:51200\n" \
"v_mfma_f32_16x16x32_bf16 v[84:87], a[200:203], a[240:243], v[84:87]\n" \
"s_movk_i32 s84, 0x2000\n" \
"v_mfma_f32_16x16x32_bf16 v[88:91], a[136:139], a[180:183], v[88:91]\n" \
"buffer_load_dwordx4 v25, s[76:79], s84 offen offset:1024 lds\n" \
"v_mfma_f32_16x16x32_bf16 v[88:91], a[200:203], a[244:247], v[88:91]\n" \
"s_movk_i32 s84, 0x4000\n" \
"v_mfma_f32_16x16x32_bf16 v[92:95], a[136:139], a[184:187], v[92:95]\n" \
"buffer_load_dwordx4 v24, s[76:79], s84 offen offset:2048 lds\n" \
"v_mfma_f32_16x16x32_bf16 v[92:95], a[200:203], a[248:251], v[92:95]\n" \
"ds_read_b64_tr_b16 a[166:167], v33 offset:33792\n" \
"v_mfma_f32_16x16x32_bf16 v[96:99], a[136:139], a[188:191], v[96:99]\n" \
"ds_read_b64_tr_b16 a[228:229], v33 offset:40960\n" \
"v_mfma_f32_16x16x32_bf16 v[96:99], a[200:203], a[252:255], v[96:99]\n" \
"ds_read_b128 a[136:139], v28 offset:53248\n" \
"v_mfma_f32_16x16x32_bf16 v[112:115], a[140:143], a[188:191], v[112:115]\n" \
"ds_read_b128 a[200:203], v29 offset:53248\n" \
"v_mfma_f32_16x16x32_bf16 v[112:115], a[204:207], a[252:255], v[112:115]\n" \
"s_movk_i32 s84, 0x6000\n" \
"v_mfma_f32_16x16x32_bf16 v[108:111], a[140:143], a[184:187], v[108:111]\n" \
"buffer_load_dwordx4 v25, s[76:79], s84 offen offset:3072 lds\n" \
"v_mfma_f32_16x16x32_bf16 v[108:111], a[204:207], a[248:251], v[108:111]\n" \
"s_add_u32 m0, s31, 0x0\n" \
"s_movk_i32 s84, 0x0\n" \
"v_mfma_f32_16x16x32_bf16 v[104:107], a[140:143], a[180:183], v[104:107]\n" \
"buffer_load_dwordx4 v26, s[68:71], s84 offen offset:0 lds\n" \
"v_mfma_f32_16x16x32_bf16 v[104:107], a[204:207], a[244:247], v[104:107]\n" \
"s_movk_i32 s84, 0x7c00\n" \
"v_mfma_f32_16x16x32_bf16 v[100:103], a[140:143], a[176:179], v[100:103]\n" \
"buffer_load_dwordx4 v26, s[68:71], s84 offen offset:1024 lds\n" \
"v_mfma_f32_16x16x32_bf16 v[100:103], a[204:207], a[240:243], v[100:103]\n" \
"ds_read_b128 a[140:143], v28 offset:55296\n" \
"v_mfma_f32_16x16x32_bf16 v[116:119], a[144:147], a[176:179], v[116:119]\n" \
"ds_read_b128 a[204:207], v29 offset:55296\n" \
"v_mfma_f32_16x16x32_bf16 v[116:119], a[208:211], a[240:243], v[116:119]\n" \
"s_addk_i32 s84, 0x7c00\n" \
"v_mfma_f32_16x16x32_bf16 v[120:123], a[144:147], a[180:183], v[120:123]\n" \
"buffer_load_dwordx4 v27, s[68:71], s84 offen offset:2048 lds\n" \
"v_mfma_f32_16x16x32_bf16 v[120:123], a[208:211], a[244:247], v[120:123]\n" \
"ds_read_b64_tr_b16 a[230:231], v33 offset:41984\n" \
"v_mfma_f32_16x16x32_bf16 v[124:127], a[144:147], a[184:187], v[124:127]\n" \
"s_addk_i32 s84, 0x7c00\n" \
"v_mfma_f32_16x16x32_bf16 v[124:127], a[208:211], a[248:251], v[124:127]\n" \
"buffer_load_dwordx4 v27, s[68:71], s84 offen offset:3072 lds\n" \
"v_mfma_f32_16x16x32_bf16 v[128:131], a[144:147], a[188:191], v[128:131]\n" \
"ds_read_b64_tr_b16 a[168:169], v34 offset:32768\n" \
"v_mfma_f32_16x16x32_bf16 v[128:131], a[208:211], a[252:255], v[128:131]\n" \
"ds_read_b128 a[144:147], v28 offset:32768\n" \
"v_mfma_f32_16x16x32_bf16 v[144:147], a[148:151], a[188:191], v[144:147]\n" \
"ds_read_b128 a[208:211], v29 offset:32768\n" \
"v_mfma_f32_16x16x32_bf16 v[144:147], a[212:215], a[252:255], v[144:147]\n" \
"s_add_u32 m0, s30, 0x1000\n" \
"s_mov_b32 s84, 0x9000\n" \
"v_mfma_f32_16x16x32_bf16 v[140:143], a[148:151], a[184:187], v[140:143]\n" \
"buffer_load_dwordx4 v24, s[76:79], s84 offen offset:0 lds\n" \
"v_mfma_f32_16x16x32_bf16 v[140:143], a[212:215], a[248:251], v[140:143]\n" \
"ds_read_b64_tr_b16 a[170:171], v34 offset:33792\n" \
"v_mfma_f32_16x16x32_bf16 v[136:139], a[148:151], a[180:183], v[136:139]\n" \
"s_addk_i32 s84, 0x2000\n" \
"s_nop 0\n" \
"v_mfma_f32_16x16x32_bf16 v[136:139], a[212:215], a[244:247], v[136:139]\n" \
"buffer_load_dwordx4 v25, s[76:79], s84 offen offset:1024 lds\n" \
"v_mfma_f32_16x16x32_bf16 v[132:135], a[148:151], a[176:179], v[132:135]\n" \
"ds_read_b64_tr_b16 a[232:233], v34 offset:40960\n" \
"v_mfma_f32_16x16x32_bf16 v[132:135], a[212:215], a[240:243], v[132:135]\n" \
"ds_read_b128 a[148:151], v28 offset:34816\n" \
"v_mfma_f32_16x16x32_bf16 v[148:151], a[152:155], a[176:179], v[148:151]\n" \
"ds_read_b128 a[212:215], v29 offset:34816\n" \
"v_mfma_f32_16x16x32_bf16 v[148:151], a[216:219], a[240:243], v[148:151]\n" \
"ds_read_b64_tr_b16 a[234:235], v34 offset:41984\n" \
"v_mfma_f32_16x16x32_bf16 v[152:155], a[152:155], a[180:183], v[152:155]\n" \
"ds_read_b64_tr_b16 a[172:173], v35 offset:32768\n" \
"v_mfma_f32_16x16x32_bf16 v[152:155], a[216:219], a[244:247], v[152:155]\n" \
"ds_read_b64_tr_b16 a[174:175], v35 offset:33792\n" \
"v_mfma_f32_16x16x32_bf16 v[156:159], a[152:155], a[184:187], v[156:159]\n" \
"ds_read_b64_tr_b16 a[236:237], v35 offset:40960\n" \
"v_mfma_f32_16x16x32_bf16 v[156:159], a[216:219], a[248:251], v[156:159]\n" \
"ds_read_b64_tr_b16 a[238:239], v35 offset:41984\n" \
"v_mfma_f32_16x16x32_bf16 v[160:163], a[152:155], a[188:191], v[160:163]\n" \
"v_mfma_f32_16x16x32_bf16 v[160:163], a[216:219], a[252:255], v[160:163]\n" \
"ds_read_b128 a[152:155], v28 offset:36864\n" \
"v_mfma_f32_16x16x32_bf16 v[176:179], a[156:159], a[188:191], v[176:179]\n" \
"ds_read_b128 a[216:219], v29 offset:36864\n" \
"v_mfma_f32_16x16x32_bf16 v[176:179], a[220:223], a[252:255], v[176:179]\n" \
"v_mfma_f32_16x16x32_bf16 v[172:175], a[156:159], a[184:187], v[172:175]\n" \
"v_mfma_f32_16x16x32_bf16 v[172:175], a[220:223], a[248:251], v[172:175]\n" \
"v_mfma_f32_16x16x32_bf16 v[168:171], a[156:159], a[180:183], v[168:171]\n" \
"v_mfma_f32_16x16x32_bf16 v[168:171], a[220:223], a[244:247], v[168:171]\n" \
"v_mfma_f32_16x16x32_bf16 v[164:167], a[156:159], a[176:179], v[164:167]\n" \
"v_mfma_f32_16x16x32_bf16 v[164:167], a[220:223], a[240:243], v[164:167]\n" \
"ds_read_b128 a[156:159], v28 offset:38912\n" \
"ds_read_b128 a[220:223], v29 offset:38912\n" \
"s_waitcnt lgkmcnt(15)\n" \
"v_mfma_f32_16x16x32_bf16 a[0:3], a[128:131], a[160:163], a[0:3]\n" \
"s_addk_i32 s84, 0x2000\n" \
"v_mfma_f32_16x16x32_bf16 a[0:3], a[192:195], a[224:227], a[0:3]\n" \
"buffer_load_dwordx4 v24, s[76:79], s84 offen offset:2048 lds\n" \
"v_mfma_f32_16x16x32_bf16 a[4:7], a[128:131], a[164:167], a[4:7]\n" \
"ds_read_b64_tr_b16 a[176:177], v36 offset:32768\n" \
"v_mfma_f32_16x16x32_bf16 a[4:7], a[192:195], a[228:231], a[4:7]\n" \
"s_addk_i32 s84, 0x2000\n" \
"s_waitcnt lgkmcnt(13)\n" \
"v_mfma_f32_16x16x32_bf16 a[8:11], a[128:131], a[168:171], a[8:11]\n" \
"buffer_load_dwordx4 v25, s[76:79], s84 offen offset:3072 lds\n" \
"s_waitcnt lgkmcnt(9)\n" \
"v_mfma_f32_16x16x32_bf16 a[8:11], a[192:195], a[232:235], a[8:11]\n" \
"ds_read_b64_tr_b16 a[178:179], v36 offset:33792\n" \
"s_waitcnt lgkmcnt(8)\n" \
"v_mfma_f32_16x16x32_bf16 a[12:15], a[128:131], a[172:175], a[12:15]\n" \
"ds_read_b64_tr_b16 a[240:241], v36 offset:40960\n" \
"s_waitcnt lgkmcnt(7)\n" \
"v_mfma_f32_16x16x32_bf16 a[12:15], a[192:195], a[236:239], a[12:15]\n" \
"v_mfma_f32_16x16x32_bf16 a[28:31], a[132:135], a[172:175], a[28:31]\n" \
"v_mfma_f32_16x16x32_bf16 a[28:31], a[196:199], a[236:239], a[28:31]\n" \
"s_add_u32 m0, s30, 0x20000\n" \
"s_movk_i32 s84, 0x100\n" \
"v_mfma_f32_16x16x32_bf16 a[24:27], a[132:135], a[168:171], a[24:27]\n" \
"buffer_load_dwordx4 v24, s[76:79], s84 offen offset:0 lds\n" \
"v_mfma_f32_16x16x32_bf16 a[24:27], a[196:199], a[232:235], a[24:27]\n" \
"ds_read_b64_tr_b16 a[242:243], v36 offset:41984\n" \
"v_mfma_f32_16x16x32_bf16 a[20:23], a[132:135], a[164:167], a[20:23]\n" \
"v_accvgpr_read_b32 v184, a12\n" \
"v_accvgpr_read_b32 v185, a13\n" \
"v_mfma_f32_16x16x32_bf16 a[20:23], a[196:199], a[228:231], a[20:23]\n" \
"ds_read_b64_tr_b16 a[180:181], v37 offset:32768\n" \
"v_accvgpr_read_b32 v186, a14\n" \
"v_mfma_f32_16x16x32_bf16 a[16:19], a[132:135], a[160:163], a[16:19]\n" \
"s_movk_i32 s84, 0x2100\n" \
"s_nop 0\n" \
"v_mfma_f32_16x16x32_bf16 a[16:19], a[196:199], a[224:227], a[16:19]\n" \
"buffer_load_dwordx4 v25, s[76:79], s84 offen offset:1024 lds\n" \
"v_mfma_f32_16x16x32_bf16 a[32:35], a[136:139], a[160:163], a[32:35]\n" \
"ds_read_b64_tr_b16 a[182:183], v37 offset:33792\n" \
"v_accvgpr_read_b32 v187, a15\n" \
"v_mfma_f32_16x16x32_bf16 a[32:35], a[200:203], a[224:227], a[32:35]\n" \
"v_accvgpr_read_b32 v180, a28\n" \
"v_accvgpr_read_b32 v181, a29\n" \
"v_mfma_f32_16x16x32_bf16 a[36:39], a[136:139], a[164:167], a[36:39]\n" \
"v_accvgpr_read_b32 v182, a30\n" \
"v_accvgpr_read_b32 v183, a31\n" \
"v_mfma_f32_16x16x32_bf16 a[36:39], a[200:203], a[228:231], a[36:39]\n" \
"s_movk_i32 s84, 0x4100\n" \
"s_nop 0\n" \
"v_mfma_f32_16x16x32_bf16 a[40:43], a[136:139], a[168:171], a[40:43]\n" \
"buffer_load_dwordx4 v24, s[76:79], s84 offen offset:2048 lds\n" \
"v_mfma_f32_16x16x32_bf16 a[40:43], a[200:203], a[232:235], a[40:43]\n" \
"ds_read_b64_tr_b16 a[244:245], v37 offset:40960\n" \
"v_cvt_pk_bf16_f32 v184, v184, v185\n" \
"v_mfma_f32_16x16x32_bf16 a[44:47], a[136:139], a[172:175], a[44:47]\n" \
"ds_read_b64_tr_b16 a[246:247], v37 offset:41984\n" \
"v_cvt_pk_bf16_f32 v185, v186, v187\n" \
"v_mfma_f32_16x16x32_bf16 a[44:47], a[200:203], a[236:239], a[44:47]\n" \
"v_cvt_pk_bf16_f32 v186, v180, v181\n" \
"v_cvt_pk_bf16_f32 v187, v182, v183\n" \
"v_mfma_f32_16x16x32_bf16 a[60:63], a[140:143], a[172:175], a[60:63]\n" \
"s_movk_i32 s84, 0x6100\n" \
"v_mfma_f32_16x16x32_bf16 a[60:63], a[204:207], a[236:239], a[60:63]\n" \
"buffer_load_dwordx4 v25, s[76:79], s84 offen offset:3072 lds\n" \
"v_mfma_f32_16x16x32_bf16 a[56:59], a[140:143], a[168:171], a[56:59]\n" \
"ds_read_b64_tr_b16 a[184:185], v38 offset:32768\n" \
"v_accvgpr_read_b32 v188, a8\n" \
"v_mfma_f32_16x16x32_bf16 a[56:59], a[204:207], a[232:235], a[56:59]\n" \
"v_accvgpr_read_b32 v189, a9\n" \
"v_accvgpr_read_b32 v190, a10\n" \
"v_mfma_f32_16x16x32_bf16 a[52:55], a[140:143], a[164:167], a[52:55]\n" \
"s_add_u32 m0, s30, 0x21000\n" \
"s_addk_i32 s84, 0x3000\n" \
"v_mfma_f32_16x16x32_bf16 a[52:55], a[204:207], a[228:231], a[52:55]\n" \
"buffer_load_dwordx4 v24, s[76:79], s84 offen offset:0 lds\n" \
"v_mfma_f32_16x16x32_bf16 a[48:51], a[140:143], a[160:163], a[48:51]\n" \
"ds_read_b64_tr_b16 a[186:187], v38 offset:33792\n" \
"v_accvgpr_read_b32 v191, a11\n" \
"v_mfma_f32_16x16x32_bf16 a[48:51], a[204:207], a[224:227], a[48:51]\n" \
"v_accvgpr_read_b32 v180, a24\n" \
"v_accvgpr_read_b32 v181, a25\n" \
"v_mfma_f32_16x16x32_bf16 a[64:67], a[144:147], a[160:163], a[64:67]\n" \
"ds_read_b64_tr_b16 a[248:249], v38 offset:40960\n" \
"v_accvgpr_read_b32 v182, a26\n" \
"v_mfma_f32_16x16x32_bf16 a[64:67], a[208:211], a[224:227], a[64:67]\n" \
"s_addk_i32 s84, 0x2000\n" \
"s_nop 0\n" \
"v_mfma_f32_16x16x32_bf16 a[68:71], a[144:147], a[164:167], a[68:71]\n" \
"buffer_load_dwordx4 v25, s[76:79], s84 offen offset:1024 lds\n" \
"v_mfma_f32_16x16x32_bf16 a[68:71], a[208:211], a[228:231], a[68:71]\n" \
"ds_read_b64_tr_b16 a[250:251], v38 offset:41984\n" \
"v_accvgpr_read_b32 v183, a27\n" \
"v_mfma_f32_16x16x32_bf16 a[72:75], a[144:147], a[168:171], a[72:75]\n" \
"v_cvt_pk_bf16_f32 v188, v188, v189\n" \
"v_cvt_pk_bf16_f32 v189, v190, v191\n" \
"v_mfma_f32_16x16x32_bf16 a[72:75], a[208:211], a[232:235], a[72:75]\n" \
"v_cvt_pk_bf16_f32 v190, v180, v181\n" \
"v_cvt_pk_bf16_f32 v191, v182, v183\n" \
"v_mfma_f32_16x16x32_bf16 a[76:79], a[144:147], a[172:175], a[76:79]\n" \
"s_addk_i32 s84, 0x2000\n" \
"v_mfma_f32_16x16x32_bf16 a[76:79], a[208:211], a[236:239], a[76:79]\n" \
"buffer_load_dwordx4 v24, s[76:79], s84 offen offset:2048 lds\n" \
"v_mfma_f32_16x16x32_bf16 a[92:95], a[148:151], a[172:175], a[92:95]\n" \
"s_add_u32 s89, s41, 0x0\n" \
"buffer_store_dwordx4 v[184:187], v20, s[80:83], s89 offen offset:3072 sc1\n" \
"v_mfma_f32_16x16x32_bf16 a[92:95], a[212:215], a[236:239], a[92:95]\n" \
"ds_read_b64_tr_b16 a[188:189], v39 offset:32768\n" \
"v_accvgpr_read_b32 v192, a4\n" \
"v_mfma_f32_16x16x32_bf16 a[88:91], a[148:151], a[168:171], a[88:91]\n" \
"ds_read_b64_tr_b16 a[190:191], v39 offset:33792\n" \
"v_accvgpr_read_b32 v193, a5\n" \
"v_mfma_f32_16x16x32_bf16 a[88:91], a[212:215], a[232:235], a[88:91]\n" \
"s_addk_i32 s84, 0x2000\n" \
"v_mfma_f32_16x16x32_bf16 a[84:87], a[148:151], a[164:167], a[84:87]\n" \
"buffer_load_dwordx4 v25, s[76:79], s84 offen offset:3072 lds\n" \
"v_mfma_f32_16x16x32_bf16 a[84:87], a[212:215], a[228:231], a[84:87]\n" \
"ds_read_b64_tr_b16 a[252:253], v39 offset:40960\n" \
"v_accvgpr_read_b32 v194, a6\n" \
"v_mfma_f32_16x16x32_bf16 a[80:83], a[148:151], a[160:163], a[80:83]\n" \
"s_add_u32 s88, s41, 0x0\n" \
"buffer_store_dwordx4 v[188:191], v20, s[80:83], s88 offen offset:2048 sc1\n" \
"v_mfma_f32_16x16x32_bf16 a[80:83], a[212:215], a[224:227], a[80:83]\n" \
"v_accvgpr_read_b32 v195, a7\n" \
"v_accvgpr_read_b32 v180, a20\n" \
"s_waitcnt lgkmcnt(15)\n" \
"s_nop 0\n" \
"v_mfma_f32_16x16x32_bf16 a[96:99], a[152:155], a[160:163], a[96:99]\n" \
"ds_read_b64_tr_b16 a[254:255], v39 offset:41984\n" \
"v_accvgpr_read_b32 v181, a21\n" \
"v_mfma_f32_16x16x32_bf16 a[96:99], a[216:219], a[224:227], a[96:99]\n" \
"v_accvgpr_read_b32 v182, a22\n" \
"v_accvgpr_read_b32 v183, a23\n" \
"v_mfma_f32_16x16x32_bf16 a[100:103], a[152:155], a[164:167], a[100:103]\n" \
"v_cvt_pk_bf16_f32 v192, v192, v193\n" \
"v_cvt_pk_bf16_f32 v193, v194, v195\n" \
"v_mfma_f32_16x16x32_bf16 a[100:103], a[216:219], a[228:231], a[100:103]\n" \
"v_cvt_pk_bf16_f32 v194, v180, v181\n" \
"v_cvt_pk_bf16_f32 v195, v182, v183\n" \
"v_mfma_f32_16x16x32_bf16 a[104:107], a[152:155], a[168:171], a[104:107]\n" \
"s_add_u32 s89, s41, 0x0\n" \
"s_nop 0\n" \
"buffer_store_dwordx4 v[192:195], v20, s[80:83], s89 offen offset:1024 sc1\n" \
"v_mfma_f32_16x16x32_bf16 a[104:107], a[216:219], a[232:235], a[104:107]\n" \
"v_accvgpr_read_b32 v196, a0\n" \
"v_accvgpr_read_b32 v197, a1\n" \
"v_mfma_f32_16x16x32_bf16 a[108:111], a[152:155], a[172:175], a[108:111]\n" \
"v_accvgpr_read_b32 v198, a2\n" \
"v_accvgpr_read_b32 v199, a3\n" \
"v_mfma_f32_16x16x32_bf16 a[108:111], a[216:219], a[236:239], a[108:111]\n" \
"v_accvgpr_read_b32 v180, a16\n" \
"v_accvgpr_read_b32 v181, a17\n" \
"v_mfma_f32_16x16x32_bf16 a[124:127], a[156:159], a[172:175], a[124:127]\n" \
"v_accvgpr_read_b32 v182, a18\n" \
"v_accvgpr_read_b32 v183, a19\n" \
"v_mfma_f32_16x16x32_bf16 a[124:127], a[220:223], a[236:239], a[124:127]\n" \
"v_cvt_pk_bf16_f32 v196, v196, v197\n" \
"v_cvt_pk_bf16_f32 v197, v198, v199\n" \
"v_mfma_f32_16x16x32_bf16 a[120:123], a[156:159], a[168:171], a[120:123]\n" \
"v_cvt_pk_bf16_f32 v198, v180, v181\n" \
"v_cvt_pk_bf16_f32 v199, v182, v183\n" \
"s_add_u32 s88, s41, 0x0\n" \
"s_nop 0\n" \
"buffer_store_dwordx4 v[196:199], v20, s[80:83], s88 offen offset:0 sc1\n" \
"v_mfma_f32_16x16x32_bf16 a[120:123], a[220:223], a[232:235], a[120:123]\n" \
"v_accvgpr_read_b32 v200, a44\n" \
"v_accvgpr_read_b32 v201, a45\n" \
"v_mfma_f32_16x16x32_bf16 a[116:119], a[156:159], a[164:167], a[116:119]\n" \
"v_accvgpr_read_b32 v202, a46\n" \
"v_accvgpr_read_b32 v203, a47\n" \
"v_mfma_f32_16x16x32_bf16 a[116:119], a[220:223], a[228:231], a[116:119]\n" \
"v_accvgpr_read_b32 v180, a60\n" \
"v_accvgpr_read_b32 v181, a61\n" \
"v_mfma_f32_16x16x32_bf16 a[112:115], a[156:159], a[160:163], a[112:115]\n" \
"v_accvgpr_read_b32 v182, a62\n" \
"v_accvgpr_read_b32 v183, a63\n" \
"v_mfma_f32_16x16x32_bf16 a[112:115], a[220:223], a[224:227], a[112:115]\n" \
"v_cvt_pk_bf16_f32 v200, v200, v201\n" \
"v_cvt_pk_bf16_f32 v201, v202, v203\n" \
"s_waitcnt vmcnt(12) lgkmcnt(0)\n" \
"s_barrier\n" \
"v_xor_b32_e32 v36, 0x4000, v36\n" \
"v_xor_b32_e64 v32, 0x0, v48\n" \
"v_xor_b32_e32 v37, 0x4000, v37\n" \
"v_xor_b32_e64 v33, 0x20, v48\n" \
"v_xor_b32_e32 v38, 0x4000, v38\n" \
"v_xor_b32_e64 v34, 0x40, v48\n" \
"v_xor_b32_e32 v39, 0x4000, v39\n" \
"v_xor_b32_e32 v35, 0x60, v48\n" \
"v_add_u32_e32 v28, s43, v28\n" \
"v_add_u32_e32 v29, s43, v29\n" \
"v_mfma_f32_16x16x32_bf16 v[116:119], a[144:147], a[176:179], v[116:119]\n" \
"s_add_u32 m0, s40, 0x8000\n" \
"s_add_u32 s85, s39, 0x2000\n" \
"v_mfma_f32_16x16x32_bf16 v[116:119], a[208:211], a[240:243], v[116:119]\n" \
"buffer_load_dwordx4 v20, s[80:83], s85 offen sc1 lds\n" \
"v_mfma_f32_16x16x32_bf16 v[120:123], a[144:147], a[180:183], v[120:123]\n" \
"ds_read_b64_tr_b16 a[160:161], v32 offset:0\n" \
"v_cvt_pk_bf16_f32 v202, v180, v181\n" \
"v_mfma_f32_16x16x32_bf16 v[120:123], a[208:211], a[244:247], v[120:123]\n" \
"s_add_u32 m0, s40, 0x9000\n" \
"s_add_u32 s85, s39, 0x3000\n" \
"v_mfma_f32_16x16x32_bf16 v[124:127], a[144:147], a[184:187], v[124:127]\n" \
"buffer_load_dwordx4 v20, s[80:83], s85 offen sc1 lds\n" \
"v_mfma_f32_16x16x32_bf16 v[124:127], a[208:211], a[248:251], v[124:127]\n" \
"ds_read_b64_tr_b16 a[162:163], v32 offset:512\n" \
"v_cvt_pk_bf16_f32 v203, v182, v183\n" \
"v_mfma_f32_16x16x32_bf16 v[128:131], a[144:147], a[188:191], v[128:131]\n" \
"ds_read_b64_tr_b16 a[224:225], v32 offset:4096\n" \
"v_accvgpr_read_b32 v204, a40\n" \
"v_mfma_f32_16x16x32_bf16 v[128:131], a[208:211], a[252:255], v[128:131]\n" \
"ds_read_b128 a[144:147], v28 offset:8192\n" \
"v_accvgpr_read_b32 v205, a41\n" \
"v_mfma_f32_16x16x32_bf16 v[144:147], a[148:151], a[188:191], v[144:147]\n" \
"ds_read_b128 a[208:211], v29 offset:8192\n" \
"v_accvgpr_read_b32 v206, a42\n" \
"v_mfma_f32_16x16x32_bf16 v[144:147], a[212:215], a[252:255], v[144:147]\n" \
"s_add_u32 m0, s40, 0xb000\n" \
"s_movk_i32 s84, 0x80\n" \
"v_mfma_f32_16x16x32_bf16 v[140:143], a[148:151], a[184:187], v[140:143]\n" \
"buffer_load_dwordx4 v21, s[76:79], s84 offen offset:0 lds\n" \
"v_mfma_f32_16x16x32_bf16 v[140:143], a[212:215], a[248:251], v[140:143]\n" \
"s_add_u32 m0, s30, 0x8000\n" \
"s_movk_i32 s84, 0x80\n" \
"v_mfma_f32_16x16x32_bf16 v[136:139], a[148:151], a[180:183], v[136:139]\n" \
"buffer_load_dwordx4 v24, s[76:79], s84 offen offset:0 lds\n" \
"v_mfma_f32_16x16x32_bf16 v[136:139], a[212:215], a[244:247], v[136:139]\n" \
"ds_read_b64_tr_b16 a[226:227], v32 offset:4608\n" \
"v_accvgpr_read_b32 v207, a43\n" \
"v_mfma_f32_16x16x32_bf16 v[132:135], a[148:151], a[176:179], v[132:135]\n" \
"ds_read_b64_tr_b16 a[164:165], v33 offset:0\n" \
"v_accvgpr_read_b32 v180, a56\n" \
"v_mfma_f32_16x16x32_bf16 v[132:135], a[212:215], a[240:243], v[132:135]\n" \
"ds_read_b128 a[148:151], v28 offset:10240\n" \
"v_accvgpr_read_b32 v181, a57\n" \
"v_mfma_f32_16x16x32_bf16 v[148:151], a[152:155], a[176:179], v[148:151]\n" \
"ds_read_b128 a[212:215], v29 offset:10240\n" \
"v_accvgpr_read_b32 v182, a58\n" \
"v_mfma_f32_16x16x32_bf16 v[148:151], a[216:219], a[240:243], v[148:151]\n" \
"s_movk_i32 s84, 0x2080\n" \
"v_mfma_f32_16x16x32_bf16 v[152:155], a[152:155], a[180:183], v[152:155]\n" \
"buffer_load_dwordx4 v25, s[76:79], s84 offen offset:1024 lds\n" \
"v_mfma_f32_16x16x32_bf16 v[152:155], a[216:219], a[244:247], v[152:155]\n" \
"s_movk_i32 s84, 0x4080\n" \
"v_mfma_f32_16x16x32_bf16 v[156:159], a[152:155], a[184:187], v[156:159]\n" \
"buffer_load_dwordx4 v24, s[76:79], s84 offen offset:2048 lds\n" \
"v_mfma_f32_16x16x32_bf16 v[156:159], a[216:219], a[248:251], v[156:159]\n" \
"ds_read_b64_tr_b16 a[166:167], v33 offset:512\n" \
"v_accvgpr_read_b32 v183, a59\n" \
"v_mfma_f32_16x16x32_bf16 v[160:163], a[152:155], a[188:191], v[160:163]\n" \
"ds_read_b64_tr_b16 a[228:229], v33 offset:4096\n" \
"v_cvt_pk_bf16_f32 v204, v204, v205\n" \
"v_mfma_f32_16x16x32_bf16 v[160:163], a[216:219], a[252:255], v[160:163]\n" \
"ds_read_b128 a[152:155], v28 offset:12288\n" \
"v_cvt_pk_bf16_f32 v205, v206, v207\n" \
"v_mfma_f32_16x16x32_bf16 v[176:179], a[156:159], a[188:191], v[176:179]\n" \
"ds_read_b128 a[216:219], v29 offset:12288\n" \
"v_cvt_pk_bf16_f32 v206, v180, v181\n" \
"v_mfma_f32_16x16x32_bf16 v[176:179], a[220:223], a[252:255], v[176:179]\n" \
"s_movk_i32 s84, 0x6080\n" \
"v_mfma_f32_16x16x32_bf16 v[172:175], a[156:159], a[184:187], v[172:175]\n" \
"buffer_load_dwordx4 v25, s[76:79], s84 offen offset:3072 lds\n" \
"v_mfma_f32_16x16x32_bf16 v[172:175], a[220:223], a[248:251], v[172:175]\n" \
"s_add_u32 m0, s31, 0x8000\n" \
"s_mov_b32 s84, 0x80000\n" \
"v_mfma_f32_16x16x32_bf16 v[168:171], a[156:159], a[180:183], v[168:171]\n" \
"buffer_load_dwordx4 v26, s[68:71], s84 offen offset:0 lds\n" \
"v_mfma_f32_16x16x32_bf16 v[168:171], a[220:223], a[244:247], v[168:171]\n" \
"s_addk_i32 s84, 0x7c00\n" \
"v_mfma_f32_16x16x32_bf16 v[164:167], a[156:159], a[176:179], v[164:167]\n" \
"buffer_load_dwordx4 v26, s[68:71], s84 offen offset:1024 lds\n" \
"v_mfma_f32_16x16x32_bf16 v[164:167], a[220:223], a[240:243], v[164:167]\n" \
"ds_read_b128 a[156:159], v28 offset:14336\n" \
"v_cvt_pk_bf16_f32 v207, v182, v183\n" \
"v_mfma_f32_16x16x32_bf16 v[84:87], a[136:139], a[176:179], v[84:87]\n" \
"ds_read_b128 a[220:223], v29 offset:14336\n" \
"v_accvgpr_read_b32 v208, a36\n" \
"v_mfma_f32_16x16x32_bf16 v[84:87], a[200:203], a[240:243], v[84:87]\n" \
"s_addk_i32 s84, 0x7c00\n" \
"v_mfma_f32_16x16x32_bf16 v[88:91], a[136:139], a[180:183], v[88:91]\n" \
"buffer_load_dwordx4 v27, s[68:71], s84 offen offset:2048 lds\n" \
"v_mfma_f32_16x16x32_bf16 v[88:91], a[200:203], a[244:247], v[88:91]\n" \
"ds_read_b64_tr_b16 a[230:231], v33 offset:4608\n" \
"v_accvgpr_read_b32 v209, a37\n" \
"v_mfma_f32_16x16x32_bf16 v[92:95], a[136:139], a[184:187], v[92:95]\n" \
"s_addk_i32 s84, 0x7c00\n" \
"v_mfma_f32_16x16x32_bf16 v[92:95], a[200:203], a[248:251], v[92:95]\n" \
"buffer_load_dwordx4 v27, s[68:71], s84 offen offset:3072 lds\n" \
"v_mfma_f32_16x16x32_bf16 v[96:99], a[136:139], a[188:191], v[96:99]\n" \
"ds_read_b64_tr_b16 a[172:173], v35 offset:0\n" \
"v_accvgpr_read_b32 v210, a38\n" \
"v_mfma_f32_16x16x32_bf16 v[96:99], a[200:203], a[252:255], v[96:99]\n" \
"ds_read_b128 a[136:139], v28 offset:4096\n" \
"v_accvgpr_read_b32 v211, a39\n" \
"v_mfma_f32_16x16x32_bf16 v[112:115], a[140:143], a[188:191], v[112:115]\n" \
"ds_read_b128 a[200:203], v29 offset:4096\n" \
"v_accvgpr_read_b32 v180, a52\n" \
"v_mfma_f32_16x16x32_bf16 v[112:115], a[204:207], a[252:255], v[112:115]\n" \
"s_add_u32 m0, s30, 0x9000\n" \
"s_mov_b32 s84, 0x9080\n" \
"v_mfma_f32_16x16x32_bf16 v[108:111], a[140:143], a[184:187], v[108:111]\n" \
"buffer_load_dwordx4 v24, s[76:79], s84 offen offset:0 lds\n" \
"v_mfma_f32_16x16x32_bf16 v[108:111], a[204:207], a[248:251], v[108:111]\n" \
"ds_read_b64_tr_b16 a[174:175], v35 offset:512\n" \
"v_accvgpr_read_b32 v181, a53\n" \
"v_mfma_f32_16x16x32_bf16 v[104:107], a[140:143], a[180:183], v[104:107]\n" \
"s_addk_i32 s84, 0x2000\n" \
"s_nop 0\n" \
"v_mfma_f32_16x16x32_bf16 v[104:107], a[204:207], a[244:247], v[104:107]\n" \
"buffer_load_dwordx4 v25, s[76:79], s84 offen offset:1024 lds\n" \
"v_mfma_f32_16x16x32_bf16 v[100:103], a[140:143], a[176:179], v[100:103]\n" \
"ds_read_b64_tr_b16 a[236:237], v35 offset:4096\n" \
"v_accvgpr_read_b32 v182, a54\n" \
"v_mfma_f32_16x16x32_bf16 v[100:103], a[204:207], a[240:243], v[100:103]\n" \
"ds_read_b128 a[140:143], v28 offset:6144\n" \
"v_accvgpr_read_b32 v183, a55\n" \
"v_mfma_f32_16x16x32_bf16 v[52:55], a[128:131], a[176:179], v[52:55]\n" \
"ds_read_b128 a[204:207], v29 offset:6144\n" \
"v_cvt_pk_bf16_f32 v208, v208, v209\n" \
"v_mfma_f32_16x16x32_bf16 v[52:55], a[192:195], a[240:243], v[52:55]\n" \
"ds_read_b64_tr_b16 a[238:239], v35 offset:4608\n" \
"v_cvt_pk_bf16_f32 v209, v210, v211\n" \
"v_mfma_f32_16x16x32_bf16 v[56:59], a[128:131], a[180:183], v[56:59]\n" \
"ds_read_b64_tr_b16 a[168:169], v34 offset:0\n" \
"v_cvt_pk_bf16_f32 v210, v180, v181\n" \
"v_mfma_f32_16x16x32_bf16 v[56:59], a[192:195], a[244:247], v[56:59]\n" \
"ds_read_b64_tr_b16 a[170:171], v34 offset:512\n" \
"v_cvt_pk_bf16_f32 v211, v182, v183\n" \
"v_mfma_f32_16x16x32_bf16 v[60:63], a[128:131], a[184:187], v[60:63]\n" \
"ds_read_b64_tr_b16 a[232:233], v34 offset:4096\n" \
"v_accvgpr_read_b32 v212, a32\n" \
"v_mfma_f32_16x16x32_bf16 v[60:63], a[192:195], a[248:251], v[60:63]\n" \
"ds_read_b64_tr_b16 a[234:235], v34 offset:4608\n" \
"v_accvgpr_read_b32 v213, a33\n" \
"v_mfma_f32_16x16x32_bf16 v[64:67], a[128:131], a[188:191], v[64:67]\n" \
"v_accvgpr_read_b32 v214, a34\n" \
"v_accvgpr_read_b32 v215, a35\n" \
"v_mfma_f32_16x16x32_bf16 v[64:67], a[192:195], a[252:255], v[64:67]\n" \
"ds_read_b128 a[128:131], v28 offset:0\n" \
"v_accvgpr_read_b32 v180, a48\n" \
"v_mfma_f32_16x16x32_bf16 v[80:83], a[132:135], a[188:191], v[80:83]\n" \
"ds_read_b128 a[192:195], v29 offset:0\n" \
"v_accvgpr_read_b32 v181, a49\n" \
"v_mfma_f32_16x16x32_bf16 v[80:83], a[196:199], a[252:255], v[80:83]\n" \
"v_accvgpr_read_b32 v182, a50\n" \
"v_accvgpr_read_b32 v183, a51\n" \
"v_mfma_f32_16x16x32_bf16 v[76:79], a[132:135], a[184:187], v[76:79]\n" \
"v_cvt_pk_bf16_f32 v212, v212, v213\n" \
"v_cvt_pk_bf16_f32 v213, v214, v215\n" \
"v_mfma_f32_16x16x32_bf16 v[76:79], a[196:199], a[248:251], v[76:79]\n" \
"v_cvt_pk_bf16_f32 v214, v180, v181\n" \
"v_cvt_pk_bf16_f32 v215, v182, v183\n" \
"v_mfma_f32_16x16x32_bf16 v[72:75], a[132:135], a[180:183], v[72:75]\n" \
"v_accvgpr_read_b32 v216, a68\n" \
"v_accvgpr_read_b32 v217, a69\n" \
"v_mfma_f32_16x16x32_bf16 v[72:75], a[196:199], a[244:247], v[72:75]\n" \
"v_accvgpr_read_b32 v218, a70\n" \
"v_accvgpr_read_b32 v219, a71\n" \
"v_mfma_f32_16x16x32_bf16 v[68:71], a[132:135], a[176:179], v[68:71]\n" \
"v_accvgpr_read_b32 v180, a84\n" \
"v_accvgpr_read_b32 v181, a85\n" \
"v_mfma_f32_16x16x32_bf16 v[68:71], a[196:199], a[240:243], v[68:71]\n" \
"ds_read_b128 a[132:135], v28 offset:2048\n" \
"ds_read_b128 a[196:199], v29 offset:2048\n" \
"s_waitcnt lgkmcnt(15)\n" \
"v_mfma_f32_16x16x32_bf16 a[0:3], a[160:163], a[144:147], 0\n" \
"s_addk_i32 s84, 0x2000\n" \
"v_accvgpr_read_b32 v182, a86\n" \
"v_mfma_f32_16x16x32_bf16 a[0:3], a[224:227], a[208:211], a[0:3]\n" \
"buffer_load_dwordx4 v24, s[76:79], s84 offen offset:2048 lds\n" \
"v_accvgpr_read_b32 v183, a87\n" \
"v_mfma_f32_16x16x32_bf16 a[4:7], a[164:167], a[144:147], 0\n" \
"ds_read_b64_tr_b16 a[176:177], v36 offset:0\n" \
"v_cvt_pk_bf16_f32 v216, v216, v217\n" \
"v_mfma_f32_16x16x32_bf16 a[4:7], a[228:231], a[208:211], a[4:7]\n" \
"s_addk_i32 s84, 0x2000\n" \
"v_cvt_pk_bf16_f32 v217, v218, v219\n" \
"s_waitcnt lgkmcnt(13)\n" \
"v_mfma_f32_16x16x32_bf16 a[12:15], a[172:175], a[144:147], 0\n" \
"buffer_load_dwordx4 v25, s[76:79], s84 offen offset:3072 lds\n" \
"v_cvt_pk_bf16_f32 v218, v180, v181\n" \
"s_waitcnt lgkmcnt(9)\n" \
"v_mfma_f32_16x16x32_bf16 a[12:15], a[236:239], a[208:211], a[12:15]\n" \
"ds_read_b64_tr_b16 a[178:179], v36 offset:1024\n" \
"v_cvt_pk_bf16_f32 v219, v182, v183\n" \
"s_waitcnt lgkmcnt(8)\n" \
"v_mfma_f32_16x16x32_bf16 a[8:11], a[144:147], a[168:171], 0\n" \
"ds_read_b64_tr_b16 a[240:241], v36 offset:8192\n" \
"v_accvgpr_read_b32 v220, a64\n" \
"s_waitcnt lgkmcnt(7)\n" \
"s_nop 0\n" \
"v_mfma_f32_16x16x32_bf16 a[8:11], a[208:211], a[232:235], a[8:11]\n" \
"ds_read_b64_tr_b16 a[242:243], v36 offset:9216\n" \
"v_accvgpr_read_b32 v221, a65\n" \
"v_mfma_f32_16x16x32_bf16 a[24:27], a[148:151], a[168:171], 0\n" \
"ds_read_b64_tr_b16 a[180:181], v37 offset:0\n" \
"v_accvgpr_read_b32 v222, a66\n" \
"v_mfma_f32_16x16x32_bf16 a[24:27], a[212:215], a[232:235], a[24:27]\n" \
"ds_read_b64_tr_b16 a[182:183], v37 offset:1024\n" \
"v_accvgpr_read_b32 v223, a67\n" \
"v_mfma_f32_16x16x32_bf16 a[28:31], a[172:175], a[148:151], 0\n" \
"ds_read_b64_tr_b16 a[244:245], v37 offset:8192\n" \
"v_accvgpr_read_b32 v180, a80\n" \
"v_mfma_f32_16x16x32_bf16 a[28:31], a[236:239], a[212:215], a[28:31]\n" \
"s_add_u32 s89, s41, 0x2000\n" \
"buffer_store_dwordx4 v[200:203], v20, s[80:83], s89 offen offset:3072 sc1\n" \
"v_mfma_f32_16x16x32_bf16 a[20:23], a[164:167], a[148:151], 0\n" \
"ds_read_b64_tr_b16 a[246:247], v37 offset:9216\n" \
"v_accvgpr_read_b32 v181, a81\n" \
"v_mfma_f32_16x16x32_bf16 a[20:23], a[228:231], a[212:215], a[20:23]\n" \
"ds_read_b64_tr_b16 a[184:185], v38 offset:0\n" \
"v_accvgpr_read_b32 v182, a82\n" \
"v_mfma_f32_16x16x32_bf16 a[16:19], a[160:163], a[148:151], 0\n" \
"ds_read_b64_tr_b16 a[186:187], v38 offset:1024\n" \
"v_accvgpr_read_b32 v183, a83\n" \
"v_mfma_f32_16x16x32_bf16 a[16:19], a[224:227], a[212:215], a[16:19]\n" \
"v_cvt_pk_bf16_f32 v220, v220, v221\n" \
"v_cvt_pk_bf16_f32 v221, v222, v223\n" \
"v_cvt_pk_bf16_f32 v222, v180, v181\n" \
"v_mfma_f32_16x16x32_bf16 a[32:35], a[160:163], a[152:155], 0\n" \
"s_add_u32 s88, s41, 0x2000\n" \
".fill (L_pb + 61152 - .) / 4, 4, 0xBF800000\n" \
"L_pp15:\n" \
"s_cbranch_execz L_pret\n" \
"s_nop 0\n" \
"buffer_store_dwordx4 v[204:207], v20, s[80:83], s88 offen offset:2048 sc1\n" \
"v_mfma_f32_16x16x32_bf16 a[32:35], a[224:227], a[216:219], a[32:35]\n" \
"ds_read_b64_tr_b16 a[248:249], v38 offset:8192\n" \
"v_cvt_pk_bf16_f32 v223, v182, v183\n" \
"v_mfma_f32_16x16x32_bf16 a[36:39], a[164:167], a[152:155], 0\n" \
"ds_read_b64_tr_b16 a[250:251], v38 offset:9216\n" \
"v_cvt_pk_bf16_f32 v224, v128, v129\n" \
"v_mfma_f32_16x16x32_bf16 a[36:39], a[228:231], a[216:219], a[36:39]\n" \
"ds_read_b64_tr_b16 a[188:189], v39 offset:0\n" \
"v_cvt_pk_bf16_f32 v225, v130, v131\n" \
"v_mfma_f32_16x16x32_bf16 a[44:47], a[172:175], a[152:155], 0\n" \
"v_cvt_pk_bf16_f32 v226, v144, v145\n" \
"v_cvt_pk_bf16_f32 v227, v146, v147\n" \
"v_cvt_pk_bf16_f32 v228, v124, v125\n" \
"v_mfma_f32_16x16x32_bf16 a[44:47], a[236:239], a[216:219], a[44:47]\n" \
"ds_read_b64_tr_b16 a[190:191], v39 offset:1024\n" \
"v_cvt_pk_bf16_f32 v229, v126, v127\n" \
"v_mfma_f32_16x16x32_bf16 a[40:43], a[152:155], a[168:171], 0\n" \
"s_add_u32 s89, s41, 0x2000\n" \
"buffer_store_dwordx4 v[208:211], v20, s[80:83], s89 offen offset:1024 sc1\n" \
"v_mfma_f32_16x16x32_bf16 a[40:43], a[216:219], a[232:235], a[40:43]\n" \
"ds_read_b64_tr_b16 a[252:253], v39 offset:8192\n" \
"v_cvt_pk_bf16_f32 v230, v140, v141\n" \
"v_mfma_f32_16x16x32_bf16 a[56:59], a[156:159], a[168:171], 0\n" \
"ds_read_b64_tr_b16 a[254:255], v39 offset:9216\n" \
"v_cvt_pk_bf16_f32 v231, v142, v143\n" \
"v_mfma_f32_16x16x32_bf16 a[56:59], a[220:223], a[232:235], a[56:59]\n" \
"v_cvt_pk_bf16_f32 v232, v120, v121\n" \
"v_cvt_pk_bf16_f32 v233, v122, v123\n" \
"v_cvt_pk_bf16_f32 v234, v136, v137\n" \
"v_mfma_f32_16x16x32_bf16 a[60:63], a[172:175], a[156:159], 0\n" \
"v_cvt_pk_bf16_f32 v235, v138, v139\n" \
"v_cvt_pk_bf16_f32 v236, v116, v117\n" \
"v_cvt_pk_bf16_f32 v237, v118, v119\n" \
"v_mfma_f32_16x16x32_bf16 a[60:63], a[236:239], a[220:223], a[60:63]\n" \
"s_add_u32 s88, s41, 0x2000\n" \
"buffer_store_dwordx4 v[212:215], v20, s[80:83], s88 offen offset:0 sc1\n" \
"v_mfma_f32_16x16x32_bf16 a[52:55], a[164:167], a[156:159], 0\n" \
"v_cvt_pk_bf16_f32 v238, v132, v133\n" \
"v_cvt_pk_bf16_f32 v239, v134, v135\n" \
"v_cvt_pk_bf16_f32 v240, v160, v161\n" \
"v_mfma_f32_16x16x32_bf16 a[52:55], a[228:231], a[220:223], a[52:55]\n" \
"v_cvt_pk_bf16_f32 v241, v162, v163\n" \
"v_cvt_pk_bf16_f32 v242, v176, v177\n" \
"v_cvt_pk_bf16_f32 v243, v178, v179\n" \
"v_mfma_f32_16x16x32_bf16 a[48:51], a[160:163], a[156:159], 0\n" \
"v_cvt_pk_bf16_f32 v244, v156, v157\n" \
"v_cvt_pk_bf16_f32 v245, v158, v159\n" \
"v_cvt_pk_bf16_f32 v246, v172, v173\n" \
"v_mfma_f32_16x16x32_bf16 a[48:51], a[224:227], a[220:223], a[48:51]\n" \
"v_cvt_pk_bf16_f32 v247, v174, v175\n" \
"v_cvt_pk_bf16_f32 v184, v152, v153\n" \
"v_cvt_pk_bf16_f32 v185, v154, v155\n" \
"v_mfma_f32_16x16x32_bf16 a[64:67], a[164:167], a[140:143], 0\n" \
"v_cvt_pk_bf16_f32 v186, v168, v169\n" \
"v_cvt_pk_bf16_f32 v187, v170, v171\n" \
"v_cvt_pk_bf16_f32 v188, v148, v149\n" \
"v_mfma_f32_16x16x32_bf16 a[64:67], a[228:231], a[204:207], a[64:67]\n" \
"v_cvt_pk_bf16_f32 v189, v150, v151\n" \
"v_cvt_pk_bf16_f32 v190, v164, v165\n" \
"v_cvt_pk_bf16_f32 v191, v166, v167\n" \
"v_mfma_f32_16x16x32_bf16 a[80:83], a[172:175], a[140:143], 0\n" \
"v_cvt_pk_bf16_f32 v192, v96, v97\n" \
"v_cvt_pk_bf16_f32 v193, v98, v99\n" \
"v_cvt_pk_bf16_f32 v194, v112, v113\n" \
"v_mfma_f32_16x16x32_bf16 a[80:83], a[236:239], a[204:207], a[80:83]\n" \
"v_cvt_pk_bf16_f32 v195, v114, v115\n" \
"v_cvt_pk_bf16_f32 v196, v92, v93\n" \
"v_cvt_pk_bf16_f32 v197, v94, v95\n" \
"v_mfma_f32_16x16x32_bf16 a[68:71], a[140:143], a[168:171], 0\n" \
"v_cvt_pk_bf16_f32 v198, v108, v109\n" \
"v_cvt_pk_bf16_f32 v199, v110, v111\n" \
"v_cvt_pk_bf16_f32 v252, v88, v89\n" \
"v_mfma_f32_16x16x32_bf16 a[68:71], a[204:207], a[232:235], a[68:71]\n" \
"v_cvt_pk_bf16_f32 v253, v90, v91\n" \
"v_cvt_pk_bf16_f32 v254, v104, v105\n" \
"v_cvt_pk_bf16_f32 v255, v106, v107\n" \
"s_waitcnt vmcnt(4) lgkmcnt(0)\n" \
"s_barrier\n" \
"v_mfma_f32_16x16x32_bf16 v[116:119], a[176:179], a[144:147], 0\n" \
"s_add_u32 m0, s40, 0x0\n" \
"s_nop 0\n" \
"s_add_u32 s85, s39, 0x4000\n" \
"v_accvgpr_write_b32 a84, v252\n" \
"v_mfma_f32_16x16x32_bf16 v[116:119], a[240:243], a[208:211], v[116:119]\n" \
"buffer_load_dwordx4 v20, s[80:83], s85 offen sc1 lds\n" \
"v_accvgpr_write_b32 a85, v253\n" \
"v_mfma_f32_16x16x32_bf16 v[120:123], a[180:183], a[144:147], 0\n" \
"ds_read_b64_tr_b16 a[160:161], v32 offset:32768\n" \
"v_accvgpr_write_b32 a86, v254\n" \
"v_mfma_f32_16x16x32_bf16 v[120:123], a[244:247], a[208:211], v[120:123]\n" \
"ds_read_b64_tr_b16 a[162:163], v32 offset:33280\n" \
"v_accvgpr_write_b32 a87, v255\n" \
"v_mfma_f32_16x16x32_bf16 v[124:127], a[184:187], a[144:147], 0\n" \
"s_add_u32 m0, s40, 0x1000\n" \
"s_add_u32 s85, s39, 0x5000\n" \
"v_cvt_pk_bf16_f32 v252, v84, v85\n" \
"v_mfma_f32_16x16x32_bf16 v[124:127], a[248:251], a[208:211], v[124:127]\n" \
"buffer_load_dwordx4 v20, s[80:83], s85 offen sc1 lds\n" \
"v_cvt_pk_bf16_f32 v253, v86, v87\n" \
"v_mfma_f32_16x16x32_bf16 v[128:131], a[188:191], a[144:147], 0\n" \
"ds_read_b64_tr_b16 a[224:225], v32 offset:36864\n" \
"v_cvt_pk_bf16_f32 v254, v100, v101\n" \
"v_mfma_f32_16x16x32_bf16 v[128:131], a[252:255], a[208:211], v[128:131]\n" \
"ds_read_b128 a[144:147], v28 offset:40960\n" \
"v_cvt_pk_bf16_f32 v255, v102, v103\n" \
"v_mfma_f32_16x16x32_bf16 v[144:147], a[188:191], a[148:151], 0\n" \
"ds_read_b128 a[208:211], v29 offset:40960\n" \
"v_cvt_pk_bf16_f32 v200, v64, v65\n" \
"v_mfma_f32_16x16x32_bf16 v[144:147], a[252:255], a[212:215], v[144:147]\n" \
"s_add_u32 m0, s40, 0x3000\n" \
"s_movk_i32 s84, 0x100\n" \
"s_nop 0\n" \
"v_cvt_pk_bf16_f32 v201, v66, v67\n" \
"v_mfma_f32_16x16x32_bf16 v[140:143], a[184:187], a[148:151], 0\n" \
"buffer_load_dwordx4 v21, s[76:79], s84 offen offset:0 lds\n" \
"v_cvt_pk_bf16_f32 v202, v80, v81\n" \
"v_mfma_f32_16x16x32_bf16 v[140:143], a[248:251], a[212:215], v[140:143]\n" \
"ds_read_b64_tr_b16 a[226:227], v32 offset:37376\n" \
"v_cvt_pk_bf16_f32 v203, v82, v83\n" \
"v_mfma_f32_16x16x32_bf16 v[136:139], a[180:183], a[148:151], 0\n" \
"v_cvt_pk_bf16_f32 v204, v60, v61\n" \
"v_cvt_pk_bf16_f32 v205, v62, v63\n" \
"v_cvt_pk_bf16_f32 v206, v76, v77\n" \
"v_mfma_f32_16x16x32_bf16 v[136:139], a[244:247], a[212:215], v[136:139]\n" \
"ds_read_b64_tr_b16 a[164:165], v33 offset:32768\n" \
"v_cvt_pk_bf16_f32 v207, v78, v79\n" \
"v_mfma_f32_16x16x32_bf16 v[132:135], a[176:179], a[148:151], 0\n" \
"v_cvt_pk_bf16_f32 v208, v56, v57\n" \
"v_cvt_pk_bf16_f32 v209, v58, v59\n" \
"v_cvt_pk_bf16_f32 v210, v72, v73\n" \
"v_mfma_f32_16x16x32_bf16 v[132:135], a[240:243], a[212:215], v[132:135]\n" \
"ds_read_b128 a[148:151], v28 offset:43008\n" \
"v_cvt_pk_bf16_f32 v211, v74, v75\n" \
"v_mfma_f32_16x16x32_bf16 v[148:151], a[176:179], a[152:155], 0\n" \
"ds_read_b128 a[212:215], v29 offset:43008\n" \
"v_cvt_pk_bf16_f32 v212, v52, v53\n" \
"v_mfma_f32_16x16x32_bf16 v[148:151], a[240:243], a[216:219], v[148:151]\n" \
"s_add_u32 m0, s31, 0x0\n" \
"s_mov_b32 s84, 0x100000\n" \
"v_cvt_pk_bf16_f32 v213, v54, v55\n" \
"v_mfma_f32_16x16x32_bf16 v[152:155], a[180:183], a[152:155], 0\n" \
"buffer_load_dwordx4 v26, s[68:71], s84 offen offset:0 lds\n" \
"v_cvt_pk_bf16_f32 v214, v68, v69\n" \
"v_mfma_f32_16x16x32_bf16 v[152:155], a[244:247], a[216:219], v[152:155]\n" \
"s_addk_i32 s84, 0x7c00\n" \
"v_cvt_pk_bf16_f32 v215, v70, v71\n" \
"v_mfma_f32_16x16x32_bf16 v[156:159], a[184:187], a[152:155], 0\n" \
"buffer_load_dwordx4 v26, s[68:71], s84 offen offset:1024 lds\n" \
"v_mfma_f32_16x16x32_bf16 v[156:159], a[248:251], a[216:219], v[156:159]\n" \
"ds_read_b64_tr_b16 a[166:167], v33 offset:33280\n" \
"v_mfma_f32_16x16x32_bf16 v[160:163], a[188:191], a[152:155], 0\n" \
"ds_read_b64_tr_b16 a[228:229], v33 offset:36864\n" \
"v_mfma_f32_16x16x32_bf16 v[160:163], a[252:255], a[216:219], v[160:163]\n" \
"ds_read_b128 a[152:155], v28 offset:45056\n" \
"v_mfma_f32_16x16x32_bf16 v[176:179], a[188:191], a[156:159], 0\n" \
"ds_read_b128 a[216:219], v29 offset:45056\n" \
"v_mfma_f32_16x16x32_bf16 v[176:179], a[252:255], a[220:223], v[176:179]\n" \
"s_addk_i32 s84, 0x7c00\n" \
"v_mfma_f32_16x16x32_bf16 v[172:175], a[184:187], a[156:159], 0\n" \
"buffer_load_dwordx4 v27, s[68:71], s84 offen offset:2048 lds\n" \
"v_mfma_f32_16x16x32_bf16 v[172:175], a[248:251], a[220:223], v[172:175]\n" \
"ds_read_b64_tr_b16 a[230:231], v33 offset:37376\n" \
"v_mfma_f32_16x16x32_bf16 v[168:171], a[180:183], a[156:159], 0\n" \
"s_addk_i32 s84, 0x7c00\n" \
"v_mfma_f32_16x16x32_bf16 v[168:171], a[244:247], a[220:223], v[168:171]\n" \
"buffer_load_dwordx4 v27, s[68:71], s84 offen offset:3072 lds\n" \
"v_mfma_f32_16x16x32_bf16 v[164:167], a[176:179], a[156:159], 0\n" \
"ds_read_b64_tr_b16 a[172:173], v35 offset:32768\n" \
"v_mfma_f32_16x16x32_bf16 v[164:167], a[240:243], a[220:223], v[164:167]\n" \
"ds_read_b128 a[156:159], v28 offset:47104\n" \
"v_mfma_f32_16x16x32_bf16 v[84:87], a[176:179], a[136:139], 0\n" \
"ds_read_b128 a[220:223], v29 offset:47104\n" \
"v_mfma_f32_16x16x32_bf16 v[84:87], a[240:243], a[200:203], v[84:87]\n" \
"s_add_u32 s89, s41, 0x3000\n" \
"buffer_store_dwordx4 v[192:195], v20, s[80:83], s89 offen offset:3072 sc1\n" \
"v_mfma_f32_16x16x32_bf16 v[88:91], a[180:183], a[136:139], 0\n" \
"ds_read_b64_tr_b16 a[174:175], v35 offset:33280\n" \
"v_mfma_f32_16x16x32_bf16 v[88:91], a[244:247], a[200:203], v[88:91]\n" \
"ds_read_b64_tr_b16 a[236:237], v35 offset:36864\n" \
"v_mfma_f32_16x16x32_bf16 v[92:95], a[184:187], a[136:139], 0\n" \
"ds_read_b64_tr_b16 a[238:239], v35 offset:37376\n" \
"v_mfma_f32_16x16x32_bf16 v[92:95], a[248:251], a[200:203], v[92:95]\n" \
"s_add_u32 m0, s30, 0x0\n" \
"s_movk_i32 s84, 0x180\n" \
"v_mfma_f32_16x16x32_bf16 v[96:99], a[188:191], a[136:139], 0\n" \
"buffer_load_dwordx4 v24, s[76:79], s84 offen offset:0 lds\n" \
"v_mfma_f32_16x16x32_bf16 v[96:99], a[252:255], a[200:203], v[96:99]\n" \
"ds_read_b128 a[136:139], v28 offset:36864\n" \
"v_mfma_f32_16x16x32_bf16 v[112:115], a[188:191], a[140:143], 0\n" \
"ds_read_b128 a[200:203], v29 offset:36864\n" \
"v_mfma_f32_16x16x32_bf16 v[112:115], a[252:255], a[204:207], v[112:115]\n" \
"ds_read_b64_tr_b16 a[168:169], v34 offset:32768\n" \
"v_mfma_f32_16x16x32_bf16 v[108:111], a[184:187], a[140:143], 0\n" \
"s_movk_i32 s84, 0x2180\n" \
"s_nop 0\n" \
"v_mfma_f32_16x16x32_bf16 v[108:111], a[248:251], a[204:207], v[108:111]\n" \
"buffer_load_dwordx4 v25, s[76:79], s84 offen offset:1024 lds\n" \
"v_mfma_f32_16x16x32_bf16 v[104:107], a[180:183], a[140:143], 0\n" \
"ds_read_b64_tr_b16 a[170:171], v34 offset:33280\n" \
"v_mfma_f32_16x16x32_bf16 v[104:107], a[244:247], a[204:207], v[104:107]\n" \
"ds_read_b64_tr_b16 a[232:233], v34 offset:36864\n" \
"v_mfma_f32_16x16x32_bf16 v[100:103], a[176:179], a[140:143], 0\n" \
"v_mfma_f32_16x16x32_bf16 v[100:103], a[240:243], a[204:207], v[100:103]\n" \
"ds_read_b128 a[140:143], v28 offset:38912\n" \
"v_mfma_f32_16x16x32_bf16 v[52:55], a[176:179], a[128:131], 0\n" \
"ds_read_b128 a[204:207], v29 offset:38912\n" \
"v_mfma_f32_16x16x32_bf16 v[52:55], a[240:243], a[192:195], v[52:55]\n" \
"s_movk_i32 s84, 0x4180\n" \
"v_mfma_f32_16x16x32_bf16 v[56:59], a[180:183], a[128:131], 0\n" \
"buffer_load_dwordx4 v24, s[76:79], s84 offen offset:2048 lds\n" \
"v_mfma_f32_16x16x32_bf16 v[56:59], a[244:247], a[192:195], v[56:59]\n" \
"s_movk_i32 s84, 0x6180\n" \
"v_mfma_f32_16x16x32_bf16 v[60:63], a[184:187], a[128:131], 0\n" \
"buffer_load_dwordx4 v25, s[76:79], s84 offen offset:3072 lds\n" \
"v_mfma_f32_16x16x32_bf16 v[60:63], a[248:251], a[192:195], v[60:63]\n" \
"ds_read_b64_tr_b16 a[234:235], v34 offset:37376\n" \
"v_mfma_f32_16x16x32_bf16 v[64:67], a[188:191], a[128:131], 0\n" \
"v_mfma_f32_16x16x32_bf16 v[64:67], a[252:255], a[192:195], v[64:67]\n" \
"ds_read_b128 a[128:131], v28 offset:32768\n" \
"v_mfma_f32_16x16x32_bf16 v[80:83], a[188:191], a[132:135], 0\n" \
"ds_read_b128 a[192:195], v29 offset:32768\n" \
"v_mfma_f32_16x16x32_bf16 v[80:83], a[252:255], a[196:199], v[80:83]\n" \
"v_mfma_f32_16x16x32_bf16 v[76:79], a[184:187], a[132:135], 0\n" \
"v_mfma_f32_16x16x32_bf16 v[76:79], a[248:251], a[196:199], v[76:79]\n" \
"v_mfma_f32_16x16x32_bf16 v[72:75], a[180:183], a[132:135], 0\n" \
"v_mfma_f32_16x16x32_bf16 v[72:75], a[244:247], a[196:199], v[72:75]\n" \
"v_mfma_f32_16x16x32_bf16 v[68:71], a[176:179], a[132:135], 0\n" \
"v_mfma_f32_16x16x32_bf16 v[68:71], a[240:243], a[196:199], v[68:71]\n" \
"ds_read_b128 a[132:135], v28 offset:34816\n" \
"ds_read_b128 a[196:199], v29 offset:34816\n" \
"s_waitcnt lgkmcnt(15)\n" \
"v_mfma_f32_16x16x32_bf16 a[0:3], a[160:163], a[144:147], a[0:3]\n" \
"s_add_u32 m0, s30, 0x1000\n" \
"s_addk_i32 s84, 0x3000\n" \
"v_mfma_f32_16x16x32_bf16 a[0:3], a[224:227], a[208:211], a[0:3]\n" \
"buffer_load_dwordx4 v24, s[76:79], s84 offen offset:0 lds\n" \
"v_mfma_f32_16x16x32_bf16 a[4:7], a[164:167], a[144:147], a[4:7]\n" \
"ds_read_b64_tr_b16 a[176:177], v36 offset:32768\n" \
"v_mov_b32_e64 v11, 0\n" \
"v_mfma_f32_16x16x32_bf16 a[4:7], a[228:231], a[208:211], a[4:7]\n" \
"ds_read_b64_tr_b16 a[178:179], v36 offset:33792\n" \
"s_and_b32 s6, s2, 7\n" \
"s_waitcnt lgkmcnt(15)\n" \
"v_mfma_f32_16x16x32_bf16 a[12:15], a[172:175], a[144:147], a[12:15]\n" \
"ds_read_b64_tr_b16 a[240:241], v36 offset:40960\n" \
"s_lshl_b32 s7, s3, 1\n" \
"s_waitcnt lgkmcnt(15)\n" \
"v_mfma_f32_16x16x32_bf16 a[12:15], a[236:239], a[208:211], a[12:15]\n" \
"s_add_u32 s88, s41, 0x3000\n" \
"buffer_store_dwordx4 v[196:199], v20, s[80:83], s88 offen offset:2048 sc1\n" \
"s_waitcnt lgkmcnt(11)\n" \
"v_mfma_f32_16x16x32_bf16 a[8:11], a[144:147], a[168:171], a[8:11]\n" \
"ds_read_b64_tr_b16 a[242:243], v36 offset:41984\n" \
"s_lshr_b32 s4, s2, 5\n" \
"s_waitcnt lgkmcnt(8)\n" \
"v_mfma_f32_16x16x32_bf16 a[8:11], a[208:211], a[232:235], a[8:11]\n" \
"ds_read_b64_tr_b16 a[180:181], v37 offset:32768\n" \
"s_lshl_b32 s5, s6, 2\n" \
"v_mfma_f32_16x16x32_bf16 a[24:27], a[148:151], a[168:171], a[24:27]\n" \
"ds_read_b64_tr_b16 a[182:183], v37 offset:33792\n" \
"s_bfe_u32 s8, s2, 0x20003\n" \
"v_mfma_f32_16x16x32_bf16 a[24:27], a[212:215], a[232:235], a[24:27]\n" \
"ds_read_b64_tr_b16 a[244:245], v37 offset:40960\n" \
"s_and_b32 s7, s7, 4\n" \
"v_mfma_f32_16x16x32_bf16 a[28:31], a[172:175], a[148:151], a[28:31]\n" \
"s_and_b32 s9, s3, 1\n" \
"s_or_b32 s13, s5, s8\n" \
"v_mfma_f32_16x16x32_bf16 a[28:31], a[236:239], a[212:215], a[28:31]\n" \
"s_addk_i32 s84, 0x2000\n" \
"v_mfma_f32_16x16x32_bf16 a[20:23], a[164:167], a[148:151], a[20:23]\n" \
"buffer_load_dwordx4 v25, s[76:79], s84 offen offset:1024 lds\n" \
"v_mfma_f32_16x16x32_bf16 a[20:23], a[228:231], a[212:215], a[20:23]\n" \
"ds_read_b64_tr_b16 a[246:247], v37 offset:41984\n" \
"s_or_b32 s9, s7, s9\n" \
"v_mfma_f32_16x16x32_bf16 a[16:19], a[160:163], a[148:151], a[16:19]\n" \
"ds_read_b64_tr_b16 a[184:185], v38 offset:32768\n" \
"s_mul_i32 s7, s4, 0xa00\n" \
"v_mfma_f32_16x16x32_bf16 a[16:19], a[224:227], a[212:215], a[16:19]\n" \
"ds_read_b64_tr_b16 a[186:187], v38 offset:33792\n" \
"s_add_i32 s7, s7, 0x5bc1000\n" \
"v_mfma_f32_16x16x32_bf16 a[32:35], a[160:163], a[152:155], a[32:35]\n" \
"s_addk_i32 s84, 0x2000\n" \
"v_mfma_f32_16x16x32_bf16 a[32:35], a[224:227], a[216:219], a[32:35]\n" \
"buffer_load_dwordx4 v24, s[76:79], s84 offen offset:2048 lds\n" \
"v_mfma_f32_16x16x32_bf16 a[36:39], a[164:167], a[152:155], a[36:39]\n" \
"ds_read_b64_tr_b16 a[248:249], v38 offset:40960\n" \
"s_lshl_b32 s10, s13, 2\n" \
"v_mfma_f32_16x16x32_bf16 a[36:39], a[228:231], a[216:219], a[36:39]\n" \
"ds_read_b64_tr_b16 a[250:251], v38 offset:41984\n" \
"s_or_b32 s10, s10, s7\n" \
"v_mfma_f32_16x16x32_bf16 a[44:47], a[172:175], a[152:155], a[44:47]\n" \
"ds_read_b64_tr_b16 a[188:189], v39 offset:32768\n" \
"v_writelane_b32 v11, s10, 0\n" \
"v_mfma_f32_16x16x32_bf16 a[44:47], a[236:239], a[216:219], a[44:47]\n" \
"ds_read_b64_tr_b16 a[190:191], v39 offset:33792\n" \
"s_lshl_b32 s14, s4, 3\n" \
"v_mfma_f32_16x16x32_bf16 a[40:43], a[152:155], a[168:171], a[40:43]\n" \
"s_add_u32 s89, s41, 0x3000\n" \
"buffer_store_dwordx4 a[84:87], v20, s[80:83], s89 offen offset:1024 sc1\n" \
"v_mfma_f32_16x16x32_bf16 a[40:43], a[216:219], a[232:235], a[40:43]\n" \
"ds_read_b64_tr_b16 a[252:253], v39 offset:40960\n" \
"v_writelane_b32 v11, s7, 1\n" \
"v_mfma_f32_16x16x32_bf16 a[56:59], a[156:159], a[168:171], a[56:59]\n" \
"ds_read_b64_tr_b16 a[254:255], v39 offset:41984\n" \
"s_or_b32 s7, s14, s6\n" \
"v_mfma_f32_16x16x32_bf16 a[56:59], a[220:223], a[232:235], a[56:59]\n" \
"s_lshl_b32 s7, s7, 10\n" \
"s_lshl_b32 s10, s3, 8\n" \
"v_mfma_f32_16x16x32_bf16 a[60:63], a[172:175], a[156:159], a[60:63]\n" \
"s_or_b32 s7, s7, s10\n" \
"s_or_b32 s10, s7, 0x5c00200\n" \
"v_mfma_f32_16x16x32_bf16 a[60:63], a[236:239], a[220:223], a[60:63]\n" \
"s_addk_i32 s84, 0x2000\n" \
"v_mfma_f32_16x16x32_bf16 a[52:55], a[164:167], a[156:159], a[52:55]\n" \
"buffer_load_dwordx4 v25, s[76:79], s84 offen offset:3072 lds\n" \
"v_mfma_f32_16x16x32_bf16 a[52:55], a[228:231], a[220:223], a[52:55]\n" \
"v_writelane_b32 v11, s10, 2\n" \
"s_lshl_b32 s10, s8, 2\n" \
"v_mfma_f32_16x16x32_bf16 a[48:51], a[160:163], a[156:159], a[48:51]\n" \
"s_or_b32 s11, s10, s7\n" \
"s_or_b32 s11, s11, 0x5c00000\n" \
"v_mfma_f32_16x16x32_bf16 a[48:51], a[224:227], a[220:223], a[48:51]\n" \
"v_writelane_b32 v11, s11, 3\n" \
"s_or_b32 s7, s7, 0x5c00080\n" \
"v_mfma_f32_16x16x32_bf16 a[64:67], a[164:167], a[140:143], a[64:67]\n" \
"v_writelane_b32 v11, s7, 4\n" \
"s_or_b32 s7, s7, s10\n" \
"v_mfma_f32_16x16x32_bf16 a[64:67], a[228:231], a[204:207], a[64:67]\n" \
"s_add_i32 s10, s3, s2\n" \
"v_writelane_b32 v11, s7, 5\n" \
"v_mfma_f32_16x16x32_bf16 a[80:83], a[172:175], a[140:143], a[80:83]\n" \
"s_mov_b32 s7, 2.0\n" \
"s_mov_b32 s11, 0x5bc0000\n" \
"v_mfma_f32_16x16x32_bf16 a[80:83], a[236:239], a[204:207], a[80:83]\n" \
"s_cmp_lg_u32 s10, 0\n" \
"s_cselect_b32 s10, s7, s11\n" \
"v_mfma_f32_16x16x32_bf16 a[68:71], a[140:143], a[168:171], a[68:71]\n" \
"s_lshr_b32 s15, s2, 3\n" \
"v_writelane_b32 v11, s10, 54\n" \
"s_waitcnt vmcnt(11) lgkmcnt(0)\n" \
"v_mfma_f32_16x16x32_bf16 a[68:71], a[204:207], a[232:235], a[68:71]\n" \
"s_barrier\n" \
"v_xor_b32_e32 v28, 0x20000, v28\n" \
"v_xor_b32_e32 v29, 0x20000, v29\n" \
"v_mfma_f32_16x16x32_bf16 v[116:119], a[176:179], a[144:147], v[116:119]\n" \
"s_add_u32 m0, s40, 0x8000\n" \
"s_add_u32 s85, s39, 0x6000\n" \
"v_mfma_f32_16x16x32_bf16 v[116:119], a[240:243], a[208:211], v[116:119]\n" \
"buffer_load_dwordx4 v20, s[80:83], s85 offen sc1 lds\n" \
"v_mfma_f32_16x16x32_bf16 v[120:123], a[180:183], a[144:147], v[120:123]\n" \
"ds_read_b64_tr_b16 a[160:161], v32 offset:0\n" \
"s_lshl_b32 s10, s6, 5\n" \
"v_mfma_f32_16x16x32_bf16 v[120:123], a[244:247], a[208:211], v[120:123]\n" \
"ds_read_b64_tr_b16 a[162:163], v32 offset:512\n" \
"s_min_u32 s16, s3, 2\n" \
"v_mfma_f32_16x16x32_bf16 v[124:127], a[184:187], a[144:147], v[124:127]\n" \
"s_add_u32 m0, s40, 0x9000\n" \
"s_add_u32 s85, s39, 0x7000\n" \
"v_mfma_f32_16x16x32_bf16 v[124:127], a[248:251], a[208:211], v[124:127]\n" \
"buffer_load_dwordx4 v20, s[80:83], s85 offen sc1 lds\n" \
"v_mfma_f32_16x16x32_bf16 v[128:131], a[188:191], a[144:147], v[128:131]\n" \
"ds_read_b64_tr_b16 a[224:225], v32 offset:4096\n" \
"s_mul_i32 s12, s8, 0x120\n" \
"v_mfma_f32_16x16x32_bf16 v[128:131], a[252:255], a[208:211], v[128:131]\n" \
"ds_read_b128 a[144:147], v28 offset:8192\n" \
"v_mfma_f32_16x16x32_bf16 v[144:147], a[188:191], a[148:151], v[144:147]\n" \
"ds_read_b128 a[208:211], v29 offset:8192\n" \
"v_mfma_f32_16x16x32_bf16 v[144:147], a[252:255], a[212:215], v[144:147]\n" \
"s_add_u32 m0, s40, 0xb000\n" \
"s_movk_i32 s84, 0x180\n" \
"v_mfma_f32_16x16x32_bf16 v[140:143], a[184:187], a[148:151], v[140:143]\n" \
"buffer_load_dwordx4 v21, s[76:79], s84 offen offset:0 lds\n" \
"v_mfma_f32_16x16x32_bf16 v[140:143], a[248:251], a[212:215], v[140:143]\n" \
"ds_read_b64_tr_b16 a[226:227], v32 offset:4608\n" \
"v_mfma_f32_16x16x32_bf16 v[136:139], a[180:183], a[148:151], v[136:139]\n" \
"s_add_u32 s88, s41, 0x3000\n" \
"buffer_store_dwordx4 v[252:255], v20, s[80:83], s88 offen offset:0 sc1\n" \
"v_mfma_f32_16x16x32_bf16 v[136:139], a[244:247], a[212:215], v[136:139]\n" \
"ds_read_b64_tr_b16 a[164:165], v33 offset:0\n" \
"v_mfma_f32_16x16x32_bf16 v[132:135], a[176:179], a[148:151], v[132:135]\n" \
"s_add_u32 s11, s22, s12\n" \
"s_addc_u32 s17, s23, 0\n" \
"s_nop 0\n" \
"v_mfma_f32_16x16x32_bf16 v[132:135], a[240:243], a[212:215], v[132:135]\n" \
"ds_read_b128 a[148:151], v28 offset:10240\n" \
"v_mfma_f32_16x16x32_bf16 v[148:151], a[176:179], a[152:155], v[148:151]\n" \
"ds_read_b128 a[212:215], v29 offset:10240\n" \
"v_mfma_f32_16x16x32_bf16 v[148:151], a[240:243], a[216:219], v[148:151]\n" \
"s_add_u32 m0, s31, 0x8000\n" \
"s_mov_b32 s84, 0x180000\n" \
"v_mfma_f32_16x16x32_bf16 v[152:155], a[180:183], a[152:155], v[152:155]\n" \
"buffer_load_dwordx4 v26, s[68:71], s84 offen offset:0 lds\n" \
"v_mfma_f32_16x16x32_bf16 v[152:155], a[244:247], a[216:219], v[152:155]\n" \
"ds_read_b64_tr_b16 a[166:167], v33 offset:512\n" \
"v_mfma_f32_16x16x32_bf16 v[156:159], a[184:187], a[152:155], v[156:159]\n" \
"ds_read_b64_tr_b16 a[228:229], v33 offset:4096\n" \
"v_mfma_f32_16x16x32_bf16 v[156:159], a[248:251], a[216:219], v[156:159]\n" \
"s_addk_i32 s84, 0x7c00\n" \
"v_mfma_f32_16x16x32_bf16 v[160:163], a[188:191], a[152:155], v[160:163]\n" \
"buffer_load_dwordx4 v26, s[68:71], s84 offen offset:1024 lds\n" \
"v_mfma_f32_16x16x32_bf16 v[160:163], a[252:255], a[216:219], v[160:163]\n" \
"ds_read_b128 a[152:155], v28 offset:12288\n" \
"v_mfma_f32_16x16x32_bf16 v[176:179], a[188:191], a[156:159], v[176:179]\n" \
"ds_read_b128 a[216:219], v29 offset:12288\n" \
"v_mfma_f32_16x16x32_bf16 v[176:179], a[252:255], a[220:223], v[176:179]\n" \
"s_addk_i32 s84, 0x7c00\n" \
"v_mfma_f32_16x16x32_bf16 v[172:175], a[184:187], a[156:159], v[172:175]\n" \
"buffer_load_dwordx4 v27, s[68:71], s84 offen offset:2048 lds\n" \
"v_mfma_f32_16x16x32_bf16 v[172:175], a[248:251], a[220:223], v[172:175]\n" \
"ds_read_b64_tr_b16 a[230:231], v33 offset:4608\n" \
"v_mfma_f32_16x16x32_bf16 v[168:171], a[180:183], a[156:159], v[168:171]\n" \
"ds_read_b64_tr_b16 a[172:173], v35 offset:0\n" \
"v_mfma_f32_16x16x32_bf16 v[168:171], a[244:247], a[220:223], v[168:171]\n" \
"ds_read_b64_tr_b16 a[174:175], v35 offset:512\n" \
"v_mfma_f32_16x16x32_bf16 v[164:167], a[176:179], a[156:159], v[164:167]\n" \
"s_add_u32 s18, s24, s12\n" \
"v_writelane_b32 v11, s11, 9\n" \
"s_addc_u32 s19, s25, 0\n" \
"v_mfma_f32_16x16x32_bf16 v[164:167], a[240:243], a[220:223], v[164:167]\n" \
"ds_read_b128 a[156:159], v28 offset:14336\n" \
"s_and_b32 s20, s17, 0xffff\n" \
"v_mfma_f32_16x16x32_bf16 v[84:87], a[176:179], a[136:139], v[84:87]\n" \
"ds_read_b128 a[220:223], v29 offset:14336\n" \
"v_writelane_b32 v11, s20, 10\n" \
"v_mfma_f32_16x16x32_bf16 v[84:87], a[240:243], a[200:203], v[84:87]\n" \
"s_addk_i32 s84, 0x7c00\n" \
"v_mfma_f32_16x16x32_bf16 v[88:91], a[180:183], a[136:139], v[88:91]\n" \
"buffer_load_dwordx4 v27, s[68:71], s84 offen offset:3072 lds\n" \
"v_mfma_f32_16x16x32_bf16 v[88:91], a[244:247], a[200:203], v[88:91]\n" \
"s_add_u32 m0, s30, 0x8000\n" \
"s_movk_i32 s84, 0x200\n" \
"v_mfma_f32_16x16x32_bf16 v[92:95], a[184:187], a[136:139], v[92:95]\n" \
"buffer_load_dwordx4 v24, s[76:79], s84 offen offset:0 lds\n" \
"v_mfma_f32_16x16x32_bf16 v[92:95], a[248:251], a[200:203], v[92:95]\n" \
"ds_read_b64_tr_b16 a[236:237], v35 offset:4096\n" \
"s_and_b32 s20, s19, 0xffff\n" \
"v_mfma_f32_16x16x32_bf16 v[96:99], a[188:191], a[136:139], v[96:99]\n" \
"ds_read_b64_tr_b16 a[238:239], v35 offset:4608\n" \
"v_writelane_b32 v11, s18, 11\n" \
"v_mfma_f32_16x16x32_bf16 v[96:99], a[252:255], a[200:203], v[96:99]\n" \
"ds_read_b128 a[136:139], v28 offset:4096\n" \
"v_mfma_f32_16x16x32_bf16 v[112:115], a[188:191], a[140:143], v[112:115]\n" \
"ds_read_b128 a[200:203], v29 offset:4096\n" \
"v_mfma_f32_16x16x32_bf16 v[112:115], a[252:255], a[204:207], v[112:115]\n" \
"s_movk_i32 s84, 0x2200\n" \
"s_nop 0\n" \
"v_mfma_f32_16x16x32_bf16 v[108:111], a[184:187], a[140:143], v[108:111]\n" \
"buffer_load_dwordx4 v25, s[76:79], s84 offen offset:1024 lds\n" \
"v_mfma_f32_16x16x32_bf16 v[108:111], a[248:251], a[204:207], v[108:111]\n" \
"s_add_u32 s89, s41, 0x1000\n" \
"buffer_store_dwordx4 v[200:203], v20, s[80:83], s89 offen offset:3072 sc1\n" \
"v_mfma_f32_16x16x32_bf16 v[104:107], a[180:183], a[140:143], v[104:107]\n" \
"ds_read_b64_tr_b16 a[168:169], v34 offset:0\n" \
"v_mfma_f32_16x16x32_bf16 v[104:107], a[244:247], a[204:207], v[104:107]\n" \
"ds_read_b64_tr_b16 a[170:171], v34 offset:512\n" \
"v_mfma_f32_16x16x32_bf16 v[100:103], a[176:179], a[140:143], v[100:103]\n" \
"ds_read_b64_tr_b16 a[232:233], v34 offset:4096\n" \
"v_mfma_f32_16x16x32_bf16 v[100:103], a[240:243], a[204:207], v[100:103]\n" \
"ds_read_b128 a[140:143], v28 offset:6144\n" \
"v_mfma_f32_16x16x32_bf16 v[52:55], a[176:179], a[128:131], v[52:55]\n" \
"ds_read_b128 a[204:207], v29 offset:6144\n" \
"v_mfma_f32_16x16x32_bf16 v[52:55], a[240:243], a[192:195], v[52:55]\n" \
"s_movk_i32 s84, 0x4200\n" \
"v_mfma_f32_16x16x32_bf16 v[56:59], a[180:183], a[128:131], v[56:59]\n" \
"buffer_load_dwordx4 v24, s[76:79], s84 offen offset:2048 lds\n" \
"v_mfma_f32_16x16x32_bf16 v[56:59], a[244:247], a[192:195], v[56:59]\n" \
"s_movk_i32 s84, 0x6200\n" \
"v_mfma_f32_16x16x32_bf16 v[60:63], a[184:187], a[128:131], v[60:63]\n" \
"buffer_load_dwordx4 v25, s[76:79], s84 offen offset:3072 lds\n" \
"v_mfma_f32_16x16x32_bf16 v[60:63], a[248:251], a[192:195], v[60:63]\n" \
"ds_read_b64_tr_b16 a[234:235], v34 offset:4608\n" \
"v_mfma_f32_16x16x32_bf16 v[64:67], a[188:191], a[128:131], v[64:67]\n" \
"s_cmpk_lt_u32 s3, 3\n" \
"s_cselect_b32 s11, s11, s18\n" \
"v_mfma_f32_16x16x32_bf16 v[64:67], a[252:255], a[192:195], v[64:67]\n" \
"ds_read_b128 a[128:131], v28 offset:0\n" \
"s_movk_i32 s18, 0xfff\n" \
"v_mfma_f32_16x16x32_bf16 v[80:83], a[188:191], a[132:135], v[80:83]\n" \
"ds_read_b128 a[192:195], v29 offset:0\n" \
"v_writelane_b32 v11, s20, 12\n" \
"v_mfma_f32_16x16x32_bf16 v[80:83], a[252:255], a[196:199], v[80:83]\n" \
"s_and_b32 s20, s29, 0xffff\n" \
"v_writelane_b32 v11, s28, 13\n" \
"v_mfma_f32_16x16x32_bf16 v[76:79], a[184:187], a[132:135], v[76:79]\n" \
"v_writelane_b32 v11, s20, 14\n" \
"v_writelane_b32 v11, s11, 15\n" \
"v_mfma_f32_16x16x32_bf16 v[76:79], a[248:251], a[196:199], v[76:79]\n" \
"s_cmpk_lt_u32 s3, 3\n" \
"s_cselect_b32 s11, s17, s19\n" \
"v_mfma_f32_16x16x32_bf16 v[72:75], a[180:183], a[132:135], v[72:75]\n" \
"s_and_b32 s11, s11, 0xffff\n" \
"v_writelane_b32 v11, s11, 16\n" \
"v_mfma_f32_16x16x32_bf16 v[72:75], a[244:247], a[196:199], v[72:75]\n" \
"s_mul_i32 s11, s6, 0x48\n" \
"s_add_i32 s17, s11, 0x47\n" \
"v_mfma_f32_16x16x32_bf16 v[68:71], a[176:179], a[132:135], v[68:71]\n" \
"s_cmpk_lt_u32 s3, 3\n" \
"s_cselect_b32 s17, s17, s18\n" \
"v_mfma_f32_16x16x32_bf16 v[68:71], a[240:243], a[196:199], v[68:71]\n" \
"ds_read_b128 a[132:135], v28 offset:2048\n" \
"ds_read_b128 a[196:199], v29 offset:2048\n" \
"s_waitcnt lgkmcnt(15)\n" \
"v_mfma_f32_16x16x32_bf16 a[0:3], a[160:163], a[144:147], a[0:3]\n" \
"s_add_u32 m0, s30, 0x9000\n" \
"s_addk_i32 s84, 0x3000\n" \
"v_mfma_f32_16x16x32_bf16 a[0:3], a[224:227], a[208:211], a[0:3]\n" \
"buffer_load_dwordx4 v24, s[76:79], s84 offen offset:0 lds\n" \
"v_mfma_f32_16x16x32_bf16 a[4:7], a[164:167], a[144:147], a[4:7]\n" \
"ds_read_b64_tr_b16 a[176:177], v36 offset:0\n" \
"s_mulk_i32 s17, 0x480\n" \
"v_mfma_f32_16x16x32_bf16 a[4:7], a[228:231], a[208:211], a[4:7]\n" \
"ds_read_b64_tr_b16 a[178:179], v36 offset:1024\n" \
"s_addk_i32 s17, 0x120\n" \
"v_mfma_f32_16x16x32_bf16 a[12:15], a[172:175], a[144:147], a[12:15]\n" \
"ds_read_b64_tr_b16 a[240:241], v36 offset:8192\n" \
"v_writelane_b32 v11, s17, 17\n" \
"s_waitcnt lgkmcnt(15)\n" \
"v_mfma_f32_16x16x32_bf16 a[12:15], a[236:239], a[208:211], a[12:15]\n" \
"s_add_u32 s88, s41, 0x1000\n" \
"buffer_store_dwordx4 v[204:207], v20, s[80:83], s88 offen offset:2048 sc1\n" \
"s_waitcnt lgkmcnt(11)\n" \
"v_mfma_f32_16x16x32_bf16 a[8:11], a[144:147], a[168:171], a[8:11]\n" \
"ds_read_b64_tr_b16 a[242:243], v36 offset:9216\n" \
"s_lshl_b32 s17, s3, 5\n" \
"s_waitcnt lgkmcnt(8)\n" \
"v_mfma_f32_16x16x32_bf16 a[8:11], a[208:211], a[232:235], a[8:11]\n" \
"ds_read_b64_tr_b16 a[180:181], v37 offset:0\n" \
"s_add_i32 s11, s11, s17\n" \
"v_mfma_f32_16x16x32_bf16 a[24:27], a[148:151], a[168:171], a[24:27]\n" \
"ds_read_b64_tr_b16 a[182:183], v37 offset:1024\n" \
"s_mul_i32 s17, s11, 0x480\n" \
"v_mfma_f32_16x16x32_bf16 a[24:27], a[212:215], a[232:235], a[24:27]\n" \
"ds_read_b64_tr_b16 a[244:245], v37 offset:8192\n" \
"s_mul_i32 s11, s13, 0x24000\n" \
"v_mfma_f32_16x16x32_bf16 a[28:31], a[172:175], a[148:151], a[28:31]\n" \
"s_add_i32 s18, s11, 0x1b000\n" \
"v_mfma_f32_16x16x32_bf16 a[28:31], a[236:239], a[212:215], a[28:31]\n" \
"s_addk_i32 s84, 0x2000\n" \
"v_mfma_f32_16x16x32_bf16 a[20:23], a[164:167], a[148:151], a[20:23]\n" \
"buffer_load_dwordx4 v25, s[76:79], s84 offen offset:1024 lds\n" \
"v_mfma_f32_16x16x32_bf16 a[20:23], a[228:231], a[212:215], a[20:23]\n" \
"ds_read_b64_tr_b16 a[246:247], v37 offset:9216\n" \
"v_mfma_f32_16x16x32_bf16 a[16:19], a[160:163], a[148:151], a[16:19]\n" \
"ds_read_b64_tr_b16 a[184:185], v38 offset:0\n" \
"v_mfma_f32_16x16x32_bf16 a[16:19], a[224:227], a[212:215], a[16:19]\n" \
"ds_read_b64_tr_b16 a[186:187], v38 offset:1024\n" \
"v_mfma_f32_16x16x32_bf16 a[32:35], a[160:163], a[152:155], a[32:35]\n" \
"s_addk_i32 s84, 0x2000\n" \
"v_mfma_f32_16x16x32_bf16 a[32:35], a[224:227], a[216:219], a[32:35]\n" \
"buffer_load_dwordx4 v24, s[76:79], s84 offen offset:2048 lds\n" \
"v_mfma_f32_16x16x32_bf16 a[36:39], a[164:167], a[152:155], a[36:39]\n" \
"ds_read_b64_tr_b16 a[248:249], v38 offset:8192\n" \
".fill (L_pb + 65248 - .) / 4, 4, 0xBF800000\n" \
"L_pp16:\n" \
"s_cbranch_execz L_pret\n" \
"s_nop 0\n" \
"v_mfma_f32_16x16x32_bf16 a[36:39], a[228:231], a[216:219], a[36:39]\n" \
"ds_read_b64_tr_b16 a[250:251], v38 offset:9216\n" \
"v_mfma_f32_16x16x32_bf16 a[44:47], a[172:175], a[152:155], a[44:47]\n" \
"ds_read_b64_tr_b16 a[188:189], v39 offset:0\n" \
"v_mfma_f32_16x16x32_bf16 a[44:47], a[236:239], a[216:219], a[44:47]\n" \
"ds_read_b64_tr_b16 a[190:191], v39 offset:1024\n" \
"v_mfma_f32_16x16x32_bf16 a[40:43], a[152:155], a[168:171], a[40:43]\n" \
"s_add_u32 s89, s41, 0x1000\n" \
"buffer_store_dwordx4 v[208:211], v20, s[80:83], s89 offen offset:1024 sc1\n" \
"v_mfma_f32_16x16x32_bf16 a[40:43], a[216:219], a[232:235], a[40:43]\n" \
"ds_read_b64_tr_b16 a[252:253], v39 offset:8192\n" \
"v_mfma_f32_16x16x32_bf16 a[56:59], a[156:159], a[168:171], a[56:59]\n" \
"ds_read_b64_tr_b16 a[254:255], v39 offset:9216\n" \
"v_mfma_f32_16x16x32_bf16 a[56:59], a[220:223], a[232:235], a[56:59]\n" \
"s_cmpk_lt_u32 s3, 3\n" \
"s_cselect_b32 s17, s17, s18\n" \
"v_mfma_f32_16x16x32_bf16 a[60:63], a[172:175], a[156:159], a[60:63]\n" \
"s_mov_b32 s18, 0x8000\n" \
"v_writelane_b32 v11, s17, 18\n" \
"v_mfma_f32_16x16x32_bf16 a[60:63], a[236:239], a[220:223], a[60:63]\n" \
"s_addk_i32 s84, 0x2000\n" \
"v_mfma_f32_16x16x32_bf16 a[52:55], a[164:167], a[156:159], a[52:55]\n" \
"buffer_load_dwordx4 v25, s[76:79], s84 offen offset:3072 lds\n" \
"v_mfma_f32_16x16x32_bf16 a[52:55], a[228:231], a[220:223], a[52:55]\n" \
"s_mul_i32 s17, s16, 0x2400\n" \
"s_mul_i32 s16, s16, 0x9000\n" \
"v_mfma_f32_16x16x32_bf16 a[48:51], a[160:163], a[156:159], a[48:51]\n" \
"s_bitset1_b32 s17, 17\n" \
"v_writelane_b32 v11, s17, 19\n" \
"v_mfma_f32_16x16x32_bf16 a[48:51], a[224:227], a[220:223], a[48:51]\n" \
"s_add_i32 s16, s11, s16\n" \
"v_writelane_b32 v11, s16, 26\n" \
"v_mfma_f32_16x16x32_bf16 a[64:67], a[164:167], a[140:143], a[64:67]\n" \
"s_mul_i32 s16, s3, 0x2400\n" \
"s_add_i32 s17, s16, 0xa400\n" \
"v_mfma_f32_16x16x32_bf16 a[64:67], a[228:231], a[204:207], a[64:67]\n" \
"s_cmpk_lt_u32 s3, 3\n" \
"s_cselect_b32 s17, s17, s18\n" \
"v_mfma_f32_16x16x32_bf16 a[80:83], a[172:175], a[140:143], a[80:83]\n" \
"s_mov_b32 s18, 0x1b000\n" \
"s_xor_b32 s19, s3, 2\n" \
"v_mfma_f32_16x16x32_bf16 a[80:83], a[236:239], a[204:207], a[80:83]\n" \
"s_cmp_lg_u32 s19, 0\n" \
"s_cselect_b32 s18, s17, s18\n" \
"v_mfma_f32_16x16x32_bf16 a[68:71], a[140:143], a[168:171], a[68:71]\n" \
"v_writelane_b32 v11, s17, 20\n" \
"s_or_b32 s17, s16, 0x10000\n" \
"s_waitcnt vmcnt(11) lgkmcnt(0)\n" \
"v_mfma_f32_16x16x32_bf16 a[68:71], a[204:207], a[232:235], a[68:71]\n" \
"s_barrier\n" \
"v_xor_b32_e32 v28, 0x20000, v28\n" \
"v_xor_b32_e32 v29, 0x20000, v29\n" \
"v_mfma_f32_16x16x32_bf16 v[116:119], a[176:179], a[144:147], v[116:119]\n" \
"s_add_u32 m0, s40, 0x0\n" \
"s_nop 0\n" \
"s_add_u32 s85, s39, 0x8000\n" \
"v_mfma_f32_16x16x32_bf16 v[116:119], a[240:243], a[208:211], v[116:119]\n" \
"buffer_load_dwordx4 v20, s[80:83], s85 offen sc1 lds\n" \
"v_mfma_f32_16x16x32_bf16 v[120:123], a[180:183], a[144:147], v[120:123]\n" \
"ds_read_b64_tr_b16 a[160:161], v32 offset:32768\n" \
"v_writelane_b32 v11, s18, 21\n" \
"v_mfma_f32_16x16x32_bf16 v[120:123], a[244:247], a[208:211], v[120:123]\n" \
"ds_read_b64_tr_b16 a[162:163], v32 offset:33280\n" \
"s_add_i32 s18, s16, 0x19000\n" \
"v_mfma_f32_16x16x32_bf16 v[124:127], a[184:187], a[144:147], v[124:127]\n" \
"s_add_u32 m0, s40, 0x1000\n" \
"s_add_u32 s85, s39, 0x9000\n" \
"v_mfma_f32_16x16x32_bf16 v[124:127], a[248:251], a[208:211], v[124:127]\n" \
"buffer_load_dwordx4 v20, s[80:83], s85 offen sc1 lds\n" \
"v_mfma_f32_16x16x32_bf16 v[128:131], a[188:191], a[144:147], v[128:131]\n" \
"ds_read_b64_tr_b16 a[224:225], v32 offset:36864\n" \
"v_writelane_b32 v11, s17, 22\n" \
"v_mfma_f32_16x16x32_bf16 v[128:131], a[252:255], a[208:211], v[128:131]\n" \
"ds_read_b128 a[144:147], v28 offset:8192\n" \
"s_mov_b32 s17, 0\n" \
"v_mfma_f32_16x16x32_bf16 v[144:147], a[188:191], a[148:151], v[144:147]\n" \
"ds_read_b128 a[208:211], v29 offset:8192\n" \
"v_mfma_f32_16x16x32_bf16 v[144:147], a[252:255], a[212:215], v[144:147]\n" \
"s_add_u32 m0, s40, 0x3000\n" \
"s_movk_i32 s84, 0x200\n" \
"v_mfma_f32_16x16x32_bf16 v[140:143], a[184:187], a[148:151], v[140:143]\n" \
"buffer_load_dwordx4 v21, s[76:79], s84 offen offset:0 lds\n" \
"v_mfma_f32_16x16x32_bf16 v[140:143], a[248:251], a[212:215], v[140:143]\n" \
"ds_read_b64_tr_b16 a[226:227], v32 offset:37376\n" \
"v_mfma_f32_16x16x32_bf16 v[136:139], a[180:183], a[148:151], v[136:139]\n" \
"s_add_u32 s88, s41, 0x1000\n" \
"buffer_store_dwordx4 v[212:215], v20, s[80:83], s88 offen offset:0 sc1\n" \
"v_mfma_f32_16x16x32_bf16 v[136:139], a[244:247], a[212:215], v[136:139]\n" \
"ds_read_b64_tr_b16 a[164:165], v33 offset:32768\n" \
"v_mfma_f32_16x16x32_bf16 v[132:135], a[176:179], a[148:151], v[132:135]\n" \
"s_cmpk_lt_u32 s3, 3\n" \
"s_cselect_b32 s18, s18, s17\n" \
"v_mfma_f32_16x16x32_bf16 v[132:135], a[240:243], a[212:215], v[132:135]\n" \
"ds_read_b128 a[148:151], v28 offset:10240\n" \
"s_bitset1_b32 s16, 17\n" \
"v_mfma_f32_16x16x32_bf16 v[148:151], a[176:179], a[152:155], v[148:151]\n" \
"ds_read_b128 a[212:215], v29 offset:10240\n" \
"v_writelane_b32 v11, s18, 23\n" \
"v_mfma_f32_16x16x32_bf16 v[148:151], a[240:243], a[216:219], v[148:151]\n" \
"s_add_u32 m0, s31, 0x0\n" \
"s_mov_b32 s84, 0x200000\n" \
"v_mfma_f32_16x16x32_bf16 v[152:155], a[180:183], a[152:155], v[152:155]\n" \
"buffer_load_dwordx4 v26, s[68:71], s84 offen offset:0 lds\n" \
"v_mfma_f32_16x16x32_bf16 v[152:155], a[244:247], a[216:219], v[152:155]\n" \
"ds_read_b64_tr_b16 a[166:167], v33 offset:33280\n" \
"s_movk_i32 s18, 0x2400\n" \
"v_mfma_f32_16x16x32_bf16 v[156:159], a[184:187], a[152:155], v[156:159]\n" \
"ds_read_b64_tr_b16 a[228:229], v33 offset:36864\n" \
"v_mfma_f32_16x16x32_bf16 v[156:159], a[248:251], a[216:219], v[156:159]\n" \
"s_addk_i32 s84, 0x7c00\n" \
"v_mfma_f32_16x16x32_bf16 v[160:163], a[188:191], a[152:155], v[160:163]\n" \
"buffer_load_dwordx4 v26, s[68:71], s84 offen offset:1024 lds\n" \
"v_mfma_f32_16x16x32_bf16 v[160:163], a[252:255], a[216:219], v[160:163]\n" \
"ds_read_b128 a[152:155], v28 offset:12288\n" \
"v_mfma_f32_16x16x32_bf16 v[176:179], a[188:191], a[156:159], v[176:179]\n" \
"ds_read_b128 a[216:219], v29 offset:12288\n" \
"v_mfma_f32_16x16x32_bf16 v[176:179], a[252:255], a[220:223], v[176:179]\n" \
"s_addk_i32 s84, 0x7c00\n" \
"v_mfma_f32_16x16x32_bf16 v[172:175], a[184:187], a[156:159], v[172:175]\n" \
"buffer_load_dwordx4 v27, s[68:71], s84 offen offset:2048 lds\n" \
"v_mfma_f32_16x16x32_bf16 v[172:175], a[248:251], a[220:223], v[172:175]\n" \
"ds_read_b64_tr_b16 a[230:231], v33 offset:37376\n" \
"v_mfma_f32_16x16x32_bf16 v[168:171], a[180:183], a[156:159], v[168:171]\n" \
"ds_read_b64_tr_b16 a[172:173], v35 offset:32768\n" \
"v_mfma_f32_16x16x32_bf16 v[168:171], a[244:247], a[220:223], v[168:171]\n" \
"ds_read_b64_tr_b16 a[174:175], v35 offset:33280\n" \
"v_mfma_f32_16x16x32_bf16 v[164:167], a[176:179], a[156:159], v[164:167]\n" \
"s_cmpk_lt_u32 s3, 3\n" \
"s_cselect_b32 s16, s16, s18\n" \
"v_mfma_f32_16x16x32_bf16 v[164:167], a[240:243], a[220:223], v[164:167]\n" \
"ds_read_b128 a[156:159], v28 offset:14336\n" \
"s_mul_i32 s19, s3, 0x9000\n" \
"v_mfma_f32_16x16x32_bf16 v[84:87], a[176:179], a[136:139], v[84:87]\n" \
"ds_read_b128 a[220:223], v29 offset:14336\n" \
"v_writelane_b32 v11, s16, 24\n" \
"v_mfma_f32_16x16x32_bf16 v[84:87], a[240:243], a[200:203], v[84:87]\n" \
"s_addk_i32 s84, 0x7c00\n" \
"v_mfma_f32_16x16x32_bf16 v[88:91], a[180:183], a[136:139], v[88:91]\n" \
"buffer_load_dwordx4 v27, s[68:71], s84 offen offset:3072 lds\n" \
"v_mfma_f32_16x16x32_bf16 v[88:91], a[244:247], a[200:203], v[88:91]\n" \
"s_add_u32 m0, s30, 0x20000\n" \
"s_movk_i32 s84, 0x280\n" \
"v_mfma_f32_16x16x32_bf16 v[92:95], a[184:187], a[136:139], v[92:95]\n" \
"buffer_load_dwordx4 v24, s[76:79], s84 offen offset:0 lds\n" \
"v_mfma_f32_16x16x32_bf16 v[92:95], a[248:251], a[200:203], v[92:95]\n" \
"ds_read_b64_tr_b16 a[236:237], v35 offset:36864\n" \
"s_lshl_b32 s16, s3, 10\n" \
"v_mfma_f32_16x16x32_bf16 v[96:99], a[188:191], a[136:139], v[96:99]\n" \
"ds_read_b64_tr_b16 a[238:239], v35 offset:37376\n" \
"s_addk_i32 s16, 0x4800\n" \
"v_mfma_f32_16x16x32_bf16 v[96:99], a[252:255], a[200:203], v[96:99]\n" \
"ds_read_b128 a[136:139], v28 offset:4096\n" \
"v_writelane_b32 v11, s16, 25\n" \
"v_mfma_f32_16x16x32_bf16 v[112:115], a[188:191], a[140:143], v[112:115]\n" \
"ds_read_b128 a[200:203], v29 offset:4096\n" \
"s_add_i32 s16, s15, 1\n" \
"v_mfma_f32_16x16x32_bf16 v[112:115], a[252:255], a[204:207], v[112:115]\n" \
"s_movk_i32 s84, 0x2280\n" \
"v_mfma_f32_16x16x32_bf16 v[108:111], a[184:187], a[140:143], v[108:111]\n" \
"buffer_load_dwordx4 v25, s[76:79], s84 offen offset:1024 lds\n" \
"v_mfma_f32_16x16x32_bf16 v[108:111], a[248:251], a[204:207], v[108:111]\n" \
"s_add_u32 s89, s42, 0x0\n" \
"buffer_store_dwordx4 v[216:219], v20, s[80:83], s89 offen offset:1024 sc1\n" \
"v_mfma_f32_16x16x32_bf16 v[104:107], a[180:183], a[140:143], v[104:107]\n" \
"ds_read_b64_tr_b16 a[168:169], v34 offset:32768\n" \
"s_and_b32 s16, s16, 3\n" \
"v_mfma_f32_16x16x32_bf16 v[104:107], a[244:247], a[204:207], v[104:107]\n" \
"ds_read_b64_tr_b16 a[170:171], v34 offset:33280\n" \
"s_or_b32 s16, s16, s5\n" \
"v_mfma_f32_16x16x32_bf16 v[100:103], a[176:179], a[140:143], v[100:103]\n" \
"ds_read_b64_tr_b16 a[232:233], v34 offset:36864\n" \
"s_mul_i32 s18, s16, 0x24000\n" \
"v_mfma_f32_16x16x32_bf16 v[100:103], a[240:243], a[204:207], v[100:103]\n" \
"ds_read_b128 a[140:143], v28 offset:6144\n" \
"s_add_i32 s18, s18, s19\n" \
"v_mfma_f32_16x16x32_bf16 v[52:55], a[176:179], a[128:131], v[52:55]\n" \
"ds_read_b128 a[204:207], v29 offset:6144\n" \
"s_xor_b32 s13, s13, 2\n" \
"v_mfma_f32_16x16x32_bf16 v[52:55], a[240:243], a[192:195], v[52:55]\n" \
"s_movk_i32 s84, 0x4280\n" \
"v_mfma_f32_16x16x32_bf16 v[56:59], a[180:183], a[128:131], v[56:59]\n" \
"buffer_load_dwordx4 v24, s[76:79], s84 offen offset:2048 lds\n" \
"v_mfma_f32_16x16x32_bf16 v[56:59], a[244:247], a[192:195], v[56:59]\n" \
"s_movk_i32 s84, 0x6280\n" \
"v_mfma_f32_16x16x32_bf16 v[60:63], a[184:187], a[128:131], v[60:63]\n" \
"buffer_load_dwordx4 v25, s[76:79], s84 offen offset:3072 lds\n" \
"v_mfma_f32_16x16x32_bf16 v[60:63], a[248:251], a[192:195], v[60:63]\n" \
"ds_read_b64_tr_b16 a[234:235], v34 offset:37376\n" \
"s_add_i32 s15, s15, -1\n" \
"v_mfma_f32_16x16x32_bf16 v[64:67], a[188:191], a[128:131], v[64:67]\n" \
"v_writelane_b32 v11, s18, 27\n" \
"s_mul_i32 s18, s13, 0x24000\n" \
"v_mfma_f32_16x16x32_bf16 v[64:67], a[252:255], a[192:195], v[64:67]\n" \
"ds_read_b128 a[128:131], v28 offset:0\n" \
"s_and_b32 s15, s15, 3\n" \
"v_mfma_f32_16x16x32_bf16 v[80:83], a[188:191], a[132:135], v[80:83]\n" \
"ds_read_b128 a[192:195], v29 offset:0\n" \
"s_add_i32 s18, s18, s19\n" \
"v_mfma_f32_16x16x32_bf16 v[80:83], a[252:255], a[196:199], v[80:83]\n" \
"s_or_b32 s15, s15, s5\n" \
"v_writelane_b32 v11, s18, 28\n" \
"v_mfma_f32_16x16x32_bf16 v[76:79], a[184:187], a[132:135], v[76:79]\n" \
"s_mul_i32 s18, s15, 0x24000\n" \
"s_add_i32 s18, s18, s19\n" \
"v_mfma_f32_16x16x32_bf16 v[76:79], a[248:251], a[196:199], v[76:79]\n" \
"v_writelane_b32 v11, s18, 29\n" \
"s_or_b32 s18, s9, s14\n" \
"v_mfma_f32_16x16x32_bf16 v[72:75], a[180:183], a[132:135], v[72:75]\n" \
"s_mul_i32 s18, s18, 0x24800\n" \
"s_lshl_b32 s16, s16, 12\n" \
"v_mfma_f32_16x16x32_bf16 v[72:75], a[244:247], a[196:199], v[72:75]\n" \
"s_add_i32 s19, s18, 0x3004800\n" \
"s_add_i32 s20, s19, s16\n" \
"v_mfma_f32_16x16x32_bf16 v[68:71], a[176:179], a[132:135], v[68:71]\n" \
"v_writelane_b32 v11, s20, 30\n" \
"s_lshl_b32 s13, s13, 12\n" \
"v_mfma_f32_16x16x32_bf16 v[68:71], a[240:243], a[196:199], v[68:71]\n" \
"ds_read_b128 a[132:135], v28 offset:2048\n" \
"ds_read_b128 a[196:199], v29 offset:2048\n" \
"s_waitcnt lgkmcnt(15)\n" \
"v_mfma_f32_16x16x32_bf16 a[0:3], a[160:163], a[144:147], a[0:3]\n" \
"s_add_u32 m0, s30, 0x21000\n" \
"s_addk_i32 s84, 0x3000\n" \
"v_mfma_f32_16x16x32_bf16 a[0:3], a[224:227], a[208:211], a[0:3]\n" \
"buffer_load_dwordx4 v24, s[76:79], s84 offen offset:0 lds\n" \
"v_mfma_f32_16x16x32_bf16 a[4:7], a[164:167], a[144:147], a[4:7]\n" \
"ds_read_b64_tr_b16 a[176:177], v36 offset:32768\n" \
"s_lshl_b32 s15, s15, 12\n" \
"v_mfma_f32_16x16x32_bf16 a[4:7], a[228:231], a[208:211], a[4:7]\n" \
"ds_read_b64_tr_b16 a[178:179], v36 offset:33792\n" \
"s_sub_i32 s14, s9, s14\n" \
"v_mfma_f32_16x16x32_bf16 a[12:15], a[172:175], a[144:147], a[12:15]\n" \
"ds_read_b64_tr_b16 a[240:241], v36 offset:40960\n" \
"s_add_i32 s20, s19, s13\n" \
"s_waitcnt lgkmcnt(15)\n" \
"v_mfma_f32_16x16x32_bf16 a[12:15], a[236:239], a[208:211], a[12:15]\n" \
"s_add_u32 s88, s42, 0x0\n" \
"buffer_store_dwordx4 v[220:223], v20, s[80:83], s88 offen offset:0 sc1\n" \
"s_waitcnt lgkmcnt(11)\n" \
"v_mfma_f32_16x16x32_bf16 a[8:11], a[144:147], a[168:171], a[8:11]\n" \
"ds_read_b64_tr_b16 a[242:243], v36 offset:41984\n" \
"v_writelane_b32 v11, s20, 31\n" \
"s_waitcnt lgkmcnt(8)\n" \
"v_mfma_f32_16x16x32_bf16 a[8:11], a[208:211], a[232:235], a[8:11]\n" \
"ds_read_b64_tr_b16 a[180:181], v37 offset:32768\n" \
"s_add_i32 s19, s19, s15\n" \
"v_mfma_f32_16x16x32_bf16 a[24:27], a[148:151], a[168:171], a[24:27]\n" \
"ds_read_b64_tr_b16 a[182:183], v37 offset:33792\n" \
"s_mul_i32 s14, s14, 0x24800\n" \
"v_mfma_f32_16x16x32_bf16 a[24:27], a[212:215], a[232:235], a[24:27]\n" \
"ds_read_b64_tr_b16 a[244:245], v37 offset:40960\n" \
"v_writelane_b32 v11, s19, 32\n" \
"v_mfma_f32_16x16x32_bf16 a[28:31], a[172:175], a[148:151], a[28:31]\n" \
"s_add_i32 s19, s14, 0x4120800\n" \
"s_add_i32 s16, s19, s16\n" \
"v_mfma_f32_16x16x32_bf16 a[28:31], a[236:239], a[212:215], a[28:31]\n" \
"s_addk_i32 s84, 0x2000\n" \
"v_mfma_f32_16x16x32_bf16 a[20:23], a[164:167], a[148:151], a[20:23]\n" \
"buffer_load_dwordx4 v25, s[76:79], s84 offen offset:1024 lds\n" \
"v_mfma_f32_16x16x32_bf16 a[20:23], a[228:231], a[212:215], a[20:23]\n" \
"ds_read_b64_tr_b16 a[246:247], v37 offset:41984\n" \
"v_writelane_b32 v11, s16, 33\n" \
"v_mfma_f32_16x16x32_bf16 a[16:19], a[160:163], a[148:151], a[16:19]\n" \
"ds_read_b64_tr_b16 a[184:185], v38 offset:32768\n" \
"s_add_i32 s13, s19, s13\n" \
"v_mfma_f32_16x16x32_bf16 a[16:19], a[224:227], a[212:215], a[16:19]\n" \
"ds_read_b64_tr_b16 a[186:187], v38 offset:33792\n" \
"v_writelane_b32 v11, s13, 34\n" \
"v_mfma_f32_16x16x32_bf16 a[32:35], a[160:163], a[152:155], a[32:35]\n" \
"s_addk_i32 s84, 0x2000\n" \
"v_mfma_f32_16x16x32_bf16 a[32:35], a[224:227], a[216:219], a[32:35]\n" \
"buffer_load_dwordx4 v24, s[76:79], s84 offen offset:2048 lds\n" \
"v_mfma_f32_16x16x32_bf16 a[36:39], a[164:167], a[152:155], a[36:39]\n" \
"ds_read_b64_tr_b16 a[248:249], v38 offset:40960\n" \
"s_add_i32 s19, s19, s15\n" \
"v_mfma_f32_16x16x32_bf16 a[36:39], a[228:231], a[216:219], a[36:39]\n" \
"ds_read_b64_tr_b16 a[250:251], v38 offset:41984\n" \
"s_or_b32 s13, s18, 0x3000000\n" \
"v_mfma_f32_16x16x32_bf16 a[44:47], a[172:175], a[152:155], a[44:47]\n" \
"ds_read_b64_tr_b16 a[188:189], v39 offset:32768\n" \
"s_lshl_b32 s15, s6, 11\n" \
"v_mfma_f32_16x16x32_bf16 a[44:47], a[236:239], a[216:219], a[44:47]\n" \
"ds_read_b64_tr_b16 a[190:191], v39 offset:33792\n" \
"v_writelane_b32 v11, s19, 35\n" \
"v_mfma_f32_16x16x32_bf16 a[40:43], a[152:155], a[168:171], a[40:43]\n" \
"s_add_u32 s89, s42, 0x1000\n" \
"buffer_store_dwordx4 v[224:227], v20, s[80:83], s89 offen offset:3072 sc1\n" \
"v_mfma_f32_16x16x32_bf16 a[40:43], a[216:219], a[232:235], a[40:43]\n" \
"ds_read_b64_tr_b16 a[252:253], v39 offset:40960\n" \
"s_add_i32 s16, s13, s15\n" \
"v_mfma_f32_16x16x32_bf16 a[56:59], a[156:159], a[168:171], a[56:59]\n" \
"ds_read_b64_tr_b16 a[254:255], v39 offset:41984\n" \
"v_writelane_b32 v11, s16, 36\n" \
"v_mfma_f32_16x16x32_bf16 a[56:59], a[220:223], a[232:235], a[56:59]\n" \
"s_add_i32 s16, s14, 0x411c000\n" \
"s_add_i32 s19, s16, s15\n" \
"v_mfma_f32_16x16x32_bf16 a[60:63], a[172:175], a[156:159], a[60:63]\n" \
"v_writelane_b32 v11, s19, 37\n" \
"s_add_i32 s18, s18, 0x3004000\n" \
"v_mfma_f32_16x16x32_bf16 a[60:63], a[236:239], a[220:223], a[60:63]\n" \
"s_addk_i32 s84, 0x2000\n" \
"v_mfma_f32_16x16x32_bf16 a[52:55], a[164:167], a[156:159], a[52:55]\n" \
"buffer_load_dwordx4 v25, s[76:79], s84 offen offset:3072 lds\n" \
"v_mfma_f32_16x16x32_bf16 a[52:55], a[228:231], a[220:223], a[52:55]\n" \
"v_writelane_b32 v11, s18, 38\n" \
"s_add_i32 s14, s14, 0x4120000\n" \
"v_mfma_f32_16x16x32_bf16 a[48:51], a[160:163], a[156:159], a[48:51]\n" \
"v_writelane_b32 v11, s14, 39\n" \
"s_lshl_b32 s14, s4, 2\n" \
"v_mfma_f32_16x16x32_bf16 a[48:51], a[224:227], a[220:223], a[48:51]\n" \
"s_or_b32 s8, s14, s8\n" \
"s_mul_i32 s8, s8, 0x90000\n" \
"v_mfma_f32_16x16x32_bf16 a[64:67], a[164:167], a[140:143], a[64:67]\n" \
"s_lshl_b32 s14, s3, 14\n" \
"s_add_i32 s8, s8, s14\n" \
"v_mfma_f32_16x16x32_bf16 a[64:67], a[228:231], a[204:207], a[64:67]\n" \
"s_lshl_b32 s9, s9, 4\n" \
"s_lshl_b32 s14, s4, 7\n" \
"v_mfma_f32_16x16x32_bf16 a[80:83], a[172:175], a[140:143], a[80:83]\n" \
"s_or_b32 s18, s9, s14\n" \
"s_sub_i32 s9, s9, s14\n" \
"v_mfma_f32_16x16x32_bf16 a[80:83], a[236:239], a[204:207], a[80:83]\n" \
"s_mulk_i32 s18, 0x480\n" \
"s_mulk_i32 s9, 0x480\n" \
"v_mfma_f32_16x16x32_bf16 a[68:71], a[140:143], a[168:171], a[68:71]\n" \
"s_or_b32 s14, s18, s12\n" \
"s_or_b32 s9, s9, s12\n" \
"s_waitcnt vmcnt(11) lgkmcnt(0)\n" \
"v_mfma_f32_16x16x32_bf16 a[68:71], a[204:207], a[232:235], a[68:71]\n" \
"s_barrier\n" \
"v_mfma_f32_16x16x32_bf16 v[116:119], a[176:179], a[144:147], v[116:119]\n" \
"s_add_u32 m0, s40, 0x8000\n" \
"s_add_u32 s85, s39, 0xa000\n" \
"v_mfma_f32_16x16x32_bf16 v[116:119], a[240:243], a[208:211], v[116:119]\n" \
"buffer_load_dwordx4 v20, s[80:83], s85 offen sc1 lds\n" \
"v_mfma_f32_16x16x32_bf16 v[120:123], a[180:183], a[144:147], v[120:123]\n" \
"ds_read_b64_tr_b16 a[160:161], v32 offset:0\n" \
"v_accvgpr_read_b32 v212, a108\n" \
"v_mfma_f32_16x16x32_bf16 v[120:123], a[244:247], a[208:211], v[120:123]\n" \
"ds_read_b64_tr_b16 a[162:163], v32 offset:512\n" \
"v_accvgpr_read_b32 v213, a109\n" \
"v_mfma_f32_16x16x32_bf16 v[124:127], a[184:187], a[144:147], v[124:127]\n" \
"s_add_u32 m0, s40, 0x9000\n" \
"s_add_u32 s85, s39, 0xb000\n" \
"v_mfma_f32_16x16x32_bf16 v[124:127], a[248:251], a[208:211], v[124:127]\n" \
"buffer_load_dwordx4 v20, s[80:83], s85 offen sc1 lds\n" \
"v_mfma_f32_16x16x32_bf16 v[128:131], a[188:191], a[144:147], v[128:131]\n" \
"ds_read_b64_tr_b16 a[224:225], v32 offset:4096\n" \
"v_accvgpr_read_b32 v214, a110\n" \
"v_mfma_f32_16x16x32_bf16 v[128:131], a[252:255], a[208:211], v[128:131]\n" \
"ds_read_b128 a[144:147], v28 offset:40960\n" \
"v_accvgpr_read_b32 v215, a111\n" \
"v_mfma_f32_16x16x32_bf16 v[144:147], a[188:191], a[148:151], v[144:147]\n" \
"ds_read_b128 a[208:211], v29 offset:40960\n" \
"v_accvgpr_read_b32 v180, a124\n" \
"v_mfma_f32_16x16x32_bf16 v[144:147], a[252:255], a[212:215], v[144:147]\n" \
"s_add_u32 m0, s40, 0xb000\n" \
"s_movk_i32 s84, 0x280\n" \
"s_nop 0\n" \
"v_mfma_f32_16x16x32_bf16 v[140:143], a[184:187], a[148:151], v[140:143]\n" \
"buffer_load_dwordx4 v21, s[76:79], s84 offen offset:0 lds\n" \
"v_mfma_f32_16x16x32_bf16 v[140:143], a[248:251], a[212:215], v[140:143]\n" \
"ds_read_b64_tr_b16 a[226:227], v32 offset:4608\n" \
"v_accvgpr_read_b32 v181, a125\n" \
"v_mfma_f32_16x16x32_bf16 v[136:139], a[180:183], a[148:151], v[136:139]\n" \
"s_add_u32 s88, s42, 0x1000\n" \
"buffer_store_dwordx4 v[228:231], v20, s[80:83], s88 offen offset:2048 sc1\n" \
"v_mfma_f32_16x16x32_bf16 v[136:139], a[244:247], a[212:215], v[136:139]\n" \
"ds_read_b64_tr_b16 a[164:165], v33 offset:0\n" \
"v_accvgpr_read_b32 v182, a126\n" \
"v_mfma_f32_16x16x32_bf16 v[132:135], a[176:179], a[148:151], v[132:135]\n" \
"v_accvgpr_read_b32 v183, a127\n" \
"v_cvt_pk_bf16_f32 v212, v212, v213\n" \
"v_mfma_f32_16x16x32_bf16 v[132:135], a[240:243], a[212:215], v[132:135]\n" \
"ds_read_b128 a[148:151], v28 offset:43008\n" \
"v_cvt_pk_bf16_f32 v213, v214, v215\n" \
"v_mfma_f32_16x16x32_bf16 v[148:151], a[176:179], a[152:155], v[148:151]\n" \
"ds_read_b128 a[212:215], v29 offset:43008\n" \
"v_cvt_pk_bf16_f32 v214, v180, v181\n" \
"v_mfma_f32_16x16x32_bf16 v[148:151], a[240:243], a[216:219], v[148:151]\n" \
"s_add_u32 m0, s31, 0x8000\n" \
"s_mov_b32 s84, 0x280000\n" \
"v_mfma_f32_16x16x32_bf16 v[152:155], a[180:183], a[152:155], v[152:155]\n" \
"buffer_load_dwordx4 v26, s[68:71], s84 offen offset:0 lds\n" \
"v_mfma_f32_16x16x32_bf16 v[152:155], a[244:247], a[216:219], v[152:155]\n" \
"ds_read_b64_tr_b16 a[166:167], v33 offset:512\n" \
"v_cvt_pk_bf16_f32 v215, v182, v183\n" \
"v_mfma_f32_16x16x32_bf16 v[156:159], a[184:187], a[152:155], v[156:159]\n" \
"ds_read_b64_tr_b16 a[228:229], v33 offset:4096\n" \
"v_accvgpr_read_b32 v208, a104\n" \
"v_mfma_f32_16x16x32_bf16 v[156:159], a[248:251], a[216:219], v[156:159]\n" \
"s_addk_i32 s84, 0x7c00\n" \
"v_mfma_f32_16x16x32_bf16 v[160:163], a[188:191], a[152:155], v[160:163]\n" \
"buffer_load_dwordx4 v26, s[68:71], s84 offen offset:1024 lds\n" \
"v_mfma_f32_16x16x32_bf16 v[160:163], a[252:255], a[216:219], v[160:163]\n" \
"ds_read_b128 a[152:155], v28 offset:45056\n" \
"v_accvgpr_read_b32 v209, a105\n" \
"v_mfma_f32_16x16x32_bf16 v[176:179], a[188:191], a[156:159], v[176:179]\n" \
"ds_read_b128 a[216:219], v29 offset:45056\n" \
"v_accvgpr_read_b32 v210, a106\n" \
"v_mfma_f32_16x16x32_bf16 v[176:179], a[252:255], a[220:223], v[176:179]\n" \
"s_addk_i32 s84, 0x7c00\n" \
"v_mfma_f32_16x16x32_bf16 v[172:175], a[184:187], a[156:159], v[172:175]\n" \
"buffer_load_dwordx4 v27, s[68:71], s84 offen offset:2048 lds\n" \
"v_mfma_f32_16x16x32_bf16 v[172:175], a[248:251], a[220:223], v[172:175]\n" \
"ds_read_b64_tr_b16 a[230:231], v33 offset:4608\n" \
"v_accvgpr_read_b32 v211, a107\n" \
"v_mfma_f32_16x16x32_bf16 v[168:171], a[180:183], a[156:159], v[168:171]\n" \
"ds_read_b64_tr_b16 a[172:173], v35 offset:0\n" \
"v_accvgpr_read_b32 v180, a120\n" \
"v_mfma_f32_16x16x32_bf16 v[168:171], a[244:247], a[220:223], v[168:171]\n" \
"ds_read_b64_tr_b16 a[174:175], v35 offset:512\n" \
"v_accvgpr_read_b32 v181, a121\n" \
"v_mfma_f32_16x16x32_bf16 v[164:167], a[176:179], a[156:159], v[164:167]\n" \
"v_accvgpr_read_b32 v182, a122\n" \
"v_accvgpr_read_b32 v183, a123\n" \
"v_mfma_f32_16x16x32_bf16 v[164:167], a[240:243], a[220:223], v[164:167]\n" \
"ds_read_b128 a[156:159], v28 offset:47104\n" \
"v_cvt_pk_bf16_f32 v208, v208, v209\n" \
"v_mfma_f32_16x16x32_bf16 v[84:87], a[176:179], a[136:139], v[84:87]\n" \
"ds_read_b128 a[220:223], v29 offset:47104\n" \
"v_cvt_pk_bf16_f32 v209, v210, v211\n" \
"v_mfma_f32_16x16x32_bf16 v[84:87], a[240:243], a[200:203], v[84:87]\n" \
"s_addk_i32 s84, 0x7c00\n" \
"v_mfma_f32_16x16x32_bf16 v[88:91], a[180:183], a[136:139], v[88:91]\n" \
"buffer_load_dwordx4 v27, s[68:71], s84 offen offset:3072 lds\n" \
"v_mfma_f32_16x16x32_bf16 v[88:91], a[244:247], a[200:203], v[88:91]\n" \
"s_add_u32 m0, s30, 0x0\n" \
"s_movk_i32 s84, 0x300\n" \
"s_nop 0\n" \
"v_mfma_f32_16x16x32_bf16 v[92:95], a[184:187], a[136:139], v[92:95]\n" \
"buffer_load_dwordx4 v24, s[76:79], s84 offen offset:0 lds\n" \
"v_mfma_f32_16x16x32_bf16 v[92:95], a[248:251], a[200:203], v[92:95]\n" \
"ds_read_b64_tr_b16 a[236:237], v35 offset:4096\n" \
"v_cvt_pk_bf16_f32 v210, v180, v181\n" \
"v_mfma_f32_16x16x32_bf16 v[96:99], a[188:191], a[136:139], v[96:99]\n" \
"ds_read_b64_tr_b16 a[238:239], v35 offset:4608\n" \
"v_cvt_pk_bf16_f32 v211, v182, v183\n" \
"v_mfma_f32_16x16x32_bf16 v[96:99], a[252:255], a[200:203], v[96:99]\n" \
"ds_read_b128 a[136:139], v28 offset:36864\n" \
"v_accvgpr_read_b32 v204, a100\n" \
"v_mfma_f32_16x16x32_bf16 v[112:115], a[188:191], a[140:143], v[112:115]\n" \
"ds_read_b128 a[200:203], v29 offset:36864\n" \
"v_accvgpr_read_b32 v205, a101\n" \
"v_mfma_f32_16x16x32_bf16 v[112:115], a[252:255], a[204:207], v[112:115]\n" \
"s_movk_i32 s84, 0x2300\n" \
"s_nop 0\n" \
"v_mfma_f32_16x16x32_bf16 v[108:111], a[184:187], a[140:143], v[108:111]\n" \
"buffer_load_dwordx4 v25, s[76:79], s84 offen offset:1024 lds\n" \
"v_mfma_f32_16x16x32_bf16 v[108:111], a[248:251], a[204:207], v[108:111]\n" \
"s_add_u32 s89, s42, 0x1000\n" \
"buffer_store_dwordx4 v[232:235], v20, s[80:83], s89 offen offset:1024 sc1\n" \
"v_mfma_f32_16x16x32_bf16 v[104:107], a[180:183], a[140:143], v[104:107]\n" \
"ds_read_b64_tr_b16 a[168:169], v34 offset:0\n" \
"v_accvgpr_read_b32 v206, a102\n" \
"v_mfma_f32_16x16x32_bf16 v[104:107], a[244:247], a[204:207], v[104:107]\n" \
"ds_read_b64_tr_b16 a[170:171], v34 offset:512\n" \
"v_accvgpr_read_b32 v207, a103\n" \
"v_mfma_f32_16x16x32_bf16 v[100:103], a[176:179], a[140:143], v[100:103]\n" \
"ds_read_b64_tr_b16 a[232:233], v34 offset:4096\n" \
"v_accvgpr_read_b32 v180, a116\n" \
"v_mfma_f32_16x16x32_bf16 v[100:103], a[240:243], a[204:207], v[100:103]\n" \
"ds_read_b128 a[140:143], v28 offset:38912\n" \
"v_accvgpr_read_b32 v181, a117\n" \
"v_mfma_f32_16x16x32_bf16 v[52:55], a[176:179], a[128:131], v[52:55]\n" \
"ds_read_b128 a[204:207], v29 offset:38912\n" \
"v_accvgpr_read_b32 v182, a118\n" \
"v_mfma_f32_16x16x32_bf16 v[52:55], a[240:243], a[192:195], v[52:55]\n" \
"s_movk_i32 s84, 0x4300\n" \
"v_mfma_f32_16x16x32_bf16 v[56:59], a[180:183], a[128:131], v[56:59]\n" \
"buffer_load_dwordx4 v24, s[76:79], s84 offen offset:2048 lds\n" \
"v_mfma_f32_16x16x32_bf16 v[56:59], a[244:247], a[192:195], v[56:59]\n" \
"s_movk_i32 s84, 0x6300\n" \
"v_mfma_f32_16x16x32_bf16 v[60:63], a[184:187], a[128:131], v[60:63]\n" \
"buffer_load_dwordx4 v25, s[76:79], s84 offen offset:3072 lds\n" \
"v_mfma_f32_16x16x32_bf16 v[60:63], a[248:251], a[192:195], v[60:63]\n" \
"ds_read_b64_tr_b16 a[234:235], v34 offset:4608\n" \
"v_accvgpr_read_b32 v183, a119\n" \
"v_mfma_f32_16x16x32_bf16 v[64:67], a[188:191], a[128:131], v[64:67]\n" \
"v_cvt_pk_bf16_f32 v204, v204, v205\n" \
"v_cvt_pk_bf16_f32 v205, v206, v207\n" \
"v_mfma_f32_16x16x32_bf16 v[64:67], a[252:255], a[192:195], v[64:67]\n" \
"ds_read_b128 a[128:131], v28 offset:32768\n" \
"v_cvt_pk_bf16_f32 v206, v180, v181\n" \
"v_mfma_f32_16x16x32_bf16 v[80:83], a[188:191], a[132:135], v[80:83]\n" \
"ds_read_b128 a[192:195], v29 offset:32768\n" \
"v_cvt_pk_bf16_f32 v207, v182, v183\n" \
"v_mfma_f32_16x16x32_bf16 v[80:83], a[252:255], a[196:199], v[80:83]\n" \
"v_accvgpr_read_b32 v200, a96\n" \
"v_accvgpr_read_b32 v201, a97\n" \
"v_mfma_f32_16x16x32_bf16 v[76:79], a[184:187], a[132:135], v[76:79]\n" \
"v_accvgpr_read_b32 v202, a98\n" \
"v_accvgpr_read_b32 v203, a99\n" \
"v_mfma_f32_16x16x32_bf16 v[76:79], a[248:251], a[196:199], v[76:79]\n" \
"v_accvgpr_read_b32 v180, a112\n" \
"v_accvgpr_read_b32 v181, a113\n" \
"v_mfma_f32_16x16x32_bf16 v[72:75], a[180:183], a[132:135], v[72:75]\n" \
"v_accvgpr_read_b32 v182, a114\n" \
"v_accvgpr_read_b32 v183, a115\n" \
"v_mfma_f32_16x16x32_bf16 v[72:75], a[244:247], a[196:199], v[72:75]\n" \
"v_cvt_pk_bf16_f32 v200, v200, v201\n" \
"v_cvt_pk_bf16_f32 v201, v202, v203\n" \
"v_mfma_f32_16x16x32_bf16 v[68:71], a[176:179], a[132:135], v[68:71]\n" \
"v_cvt_pk_bf16_f32 v202, v180, v181\n" \
"v_cvt_pk_bf16_f32 v203, v182, v183\n" \
"v_mfma_f32_16x16x32_bf16 v[68:71], a[240:243], a[196:199], v[68:71]\n" \
"ds_read_b128 a[132:135], v28 offset:34816\n" \
"ds_read_b128 a[196:199], v29 offset:34816\n" \
"s_waitcnt lgkmcnt(15)\n" \
"v_mfma_f32_16x16x32_bf16 a[0:3], a[160:163], a[144:147], a[0:3]\n" \
"s_add_u32 m0, s30, 0x1000\n" \
"s_addk_i32 s84, 0x3000\n" \
"v_mfma_f32_16x16x32_bf16 a[0:3], a[224:227], a[208:211], a[0:3]\n" \
"buffer_load_dwordx4 v24, s[76:79], s84 offen offset:0 lds\n" \
"v_mfma_f32_16x16x32_bf16 a[4:7], a[164:167], a[144:147], a[4:7]\n" \
"ds_read_b64_tr_b16 a[176:177], v36 offset:0\n" \
"v_accvgpr_read_b32 v196, a76\n" \
"v_mfma_f32_16x16x32_bf16 a[4:7], a[228:231], a[208:211], a[4:7]\n" \
"ds_read_b64_tr_b16 a[178:179], v36 offset:1024\n" \
"v_accvgpr_read_b32 v197, a77\n" \
"v_mfma_f32_16x16x32_bf16 a[12:15], a[172:175], a[144:147], a[12:15]\n" \
"ds_read_b64_tr_b16 a[240:241], v36 offset:8192\n" \
"v_accvgpr_read_b32 v198, a78\n" \
"s_waitcnt lgkmcnt(15)\n" \
"v_mfma_f32_16x16x32_bf16 a[12:15], a[236:239], a[208:211], a[12:15]\n" \
"s_add_u32 s88, s42, 0x1000\n" \
"buffer_store_dwordx4 v[236:239], v20, s[80:83], s88 offen offset:0 sc1\n" \
"s_waitcnt lgkmcnt(11)\n" \
"v_mfma_f32_16x16x32_bf16 a[8:11], a[144:147], a[168:171], a[8:11]\n" \
"ds_read_b64_tr_b16 a[242:243], v36 offset:9216\n" \
"v_accvgpr_read_b32 v199, a79\n" \
"s_waitcnt lgkmcnt(8)\n" \
"s_nop 0\n" \
"v_mfma_f32_16x16x32_bf16 a[8:11], a[208:211], a[232:235], a[8:11]\n" \
"ds_read_b64_tr_b16 a[180:181], v37 offset:0\n" \
"v_accvgpr_read_b32 v180, a92\n" \
"v_mfma_f32_16x16x32_bf16 a[24:27], a[148:151], a[168:171], a[24:27]\n" \
"ds_read_b64_tr_b16 a[182:183], v37 offset:1024\n" \
"v_accvgpr_read_b32 v181, a93\n" \
"v_mfma_f32_16x16x32_bf16 a[24:27], a[212:215], a[232:235], a[24:27]\n" \
"ds_read_b64_tr_b16 a[244:245], v37 offset:8192\n" \
"v_accvgpr_read_b32 v182, a94\n" \
"v_mfma_f32_16x16x32_bf16 a[28:31], a[172:175], a[148:151], a[28:31]\n" \
"v_accvgpr_read_b32 v183, a95\n" \
"v_cvt_pk_bf16_f32 v196, v196, v197\n" \
"v_mfma_f32_16x16x32_bf16 a[28:31], a[236:239], a[212:215], a[28:31]\n" \
"s_addk_i32 s84, 0x2000\n" \
"s_nop 0\n" \
"v_mfma_f32_16x16x32_bf16 a[20:23], a[164:167], a[148:151], a[20:23]\n" \
"buffer_load_dwordx4 v25, s[76:79], s84 offen offset:1024 lds\n" \
"v_mfma_f32_16x16x32_bf16 a[20:23], a[228:231], a[212:215], a[20:23]\n" \
"ds_read_b64_tr_b16 a[246:247], v37 offset:9216\n" \
"v_cvt_pk_bf16_f32 v197, v198, v199\n" \
"v_mfma_f32_16x16x32_bf16 a[16:19], a[160:163], a[148:151], a[16:19]\n" \
"ds_read_b64_tr_b16 a[184:185], v38 offset:0\n" \
"v_cvt_pk_bf16_f32 v198, v180, v181\n" \
"v_mfma_f32_16x16x32_bf16 a[16:19], a[224:227], a[212:215], a[16:19]\n" \
"ds_read_b64_tr_b16 a[186:187], v38 offset:1024\n" \
"v_cvt_pk_bf16_f32 v199, v182, v183\n" \
"v_mfma_f32_16x16x32_bf16 a[32:35], a[160:163], a[152:155], a[32:35]\n" \
"s_addk_i32 s84, 0x2000\n" \
"s_nop 0\n" \
".fill (L_pb + 69344 - .) / 4, 4, 0xBF800000\n" \
"L_pp17:\n" \
"s_cbranch_execz L_pret\n" \
"s_nop 0\n" \
"v_mfma_f32_16x16x32_bf16 a[32:35], a[224:227], a[216:219], a[32:35]\n" \
"buffer_load_dwordx4 v24, s[76:79], s84 offen offset:2048 lds\n" \
"v_mfma_f32_16x16x32_bf16 a[36:39], a[164:167], a[152:155], a[36:39]\n" \
"ds_read_b64_tr_b16 a[248:249], v38 offset:8192\n" \
"v_accvgpr_read_b32 v192, a72\n" \
"v_mfma_f32_16x16x32_bf16 a[36:39], a[228:231], a[216:219], a[36:39]\n" \
"ds_read_b64_tr_b16 a[250:251], v38 offset:9216\n" \
"v_accvgpr_read_b32 v193, a73\n" \
"v_mfma_f32_16x16x32_bf16 a[44:47], a[172:175], a[152:155], a[44:47]\n" \
"ds_read_b64_tr_b16 a[188:189], v39 offset:0\n" \
"v_accvgpr_read_b32 v194, a74\n" \
"v_mfma_f32_16x16x32_bf16 a[44:47], a[236:239], a[216:219], a[44:47]\n" \
"ds_read_b64_tr_b16 a[190:191], v39 offset:1024\n" \
"v_accvgpr_read_b32 v195, a75\n" \
"v_mfma_f32_16x16x32_bf16 a[40:43], a[152:155], a[168:171], a[40:43]\n" \
"s_add_u32 s89, s42, 0x3000\n" \
"buffer_store_dwordx4 v[240:243], v20, s[80:83], s89 offen offset:3072 sc1\n" \
"v_mfma_f32_16x16x32_bf16 a[40:43], a[216:219], a[232:235], a[40:43]\n" \
"ds_read_b64_tr_b16 a[252:253], v39 offset:8192\n" \
"v_accvgpr_read_b32 v180, a88\n" \
"v_mfma_f32_16x16x32_bf16 a[56:59], a[156:159], a[168:171], a[56:59]\n" \
"ds_read_b64_tr_b16 a[254:255], v39 offset:9216\n" \
"v_accvgpr_read_b32 v181, a89\n" \
"v_mfma_f32_16x16x32_bf16 a[56:59], a[220:223], a[232:235], a[56:59]\n" \
"v_accvgpr_read_b32 v182, a90\n" \
"v_accvgpr_read_b32 v183, a91\n" \
"v_mfma_f32_16x16x32_bf16 a[60:63], a[172:175], a[156:159], a[60:63]\n" \
"v_cvt_pk_bf16_f32 v192, v192, v193\n" \
"v_cvt_pk_bf16_f32 v193, v194, v195\n" \
"v_mfma_f32_16x16x32_bf16 a[60:63], a[236:239], a[220:223], a[60:63]\n" \
"s_addk_i32 s84, 0x2000\n" \
"v_mfma_f32_16x16x32_bf16 a[52:55], a[164:167], a[156:159], a[52:55]\n" \
"buffer_load_dwordx4 v25, s[76:79], s84 offen offset:3072 lds\n" \
"v_mfma_f32_16x16x32_bf16 a[52:55], a[228:231], a[220:223], a[52:55]\n" \
"v_cvt_pk_bf16_f32 v194, v180, v181\n" \
"v_cvt_pk_bf16_f32 v195, v182, v183\n" \
"v_mfma_f32_16x16x32_bf16 a[48:51], a[160:163], a[156:159], a[48:51]\n" \
"s_and_b32 s12, s2, 1\n" \
"v_mfma_f32_16x16x32_bf16 a[48:51], a[224:227], a[220:223], a[48:51]\n" \
"s_cmp_lg_u32 s12, 0\n" \
"s_cselect_b32 s13, s16, s13\n" \
"v_mfma_f32_16x16x32_bf16 a[64:67], a[164:167], a[140:143], a[64:67]\n" \
"s_bfe_u32 s16, s2, 0x20001\n" \
"s_addk_i32 s13, 0x4000\n" \
"v_mfma_f32_16x16x32_bf16 a[64:67], a[228:231], a[204:207], a[64:67]\n" \
"s_xor_b32 s16, s16, s3\n" \
"v_mfma_f32_16x16x32_bf16 a[80:83], a[172:175], a[140:143], a[80:83]\n" \
"s_cmp_lg_u32 s16, 0\n" \
"s_cselect_b32 s13, s7, s13\n" \
"v_mfma_f32_16x16x32_bf16 a[80:83], a[236:239], a[204:207], a[80:83]\n" \
"s_lshl_b32 s6, s6, 16\n" \
"v_writelane_b32 v11, s13, 40\n" \
"v_mfma_f32_16x16x32_bf16 a[68:71], a[140:143], a[168:171], a[68:71]\n" \
"s_add_i32 s13, s8, 0x4240000\n" \
"s_add_i32 s6, s13, s6\n" \
"s_waitcnt vmcnt(11) lgkmcnt(0)\n" \
"v_mfma_f32_16x16x32_bf16 a[68:71], a[204:207], a[232:235], a[68:71]\n" \
"s_barrier\n" \
"v_mov_b32_e32 v45, s62\n" \
"s_mov_b64 exec, 1\n" \
"buffer_store_dword v45, off, s[80:83], s37 offset:0 sc1\n" \
"s_mov_b64 exec, -1\n" \
"v_xor_b32_e32 v28, 0x20000, v28\n" \
"v_xor_b32_e32 v29, 0x20000, v29\n" \
"v_mfma_f32_16x16x32_bf16 v[116:119], a[176:179], a[144:147], v[116:119]\n" \
"s_add_u32 m0, s40, 0x0\n" \
"s_add_u32 s85, s39, 0xc000\n" \
"v_mfma_f32_16x16x32_bf16 v[116:119], a[240:243], a[208:211], v[116:119]\n" \
"buffer_load_dwordx4 v20, s[80:83], s85 offen sc1 lds\n" \
"v_mfma_f32_16x16x32_bf16 v[120:123], a[180:183], a[144:147], v[120:123]\n" \
"ds_read_b64_tr_b16 a[160:161], v32 offset:32768\n" \
"v_writelane_b32 v11, s6, 41\n" \
"v_mfma_f32_16x16x32_bf16 v[120:123], a[244:247], a[208:211], v[120:123]\n" \
"ds_read_b64_tr_b16 a[162:163], v32 offset:33280\n" \
"s_add_i32 s8, s8, 0x42c0000\n" \
"v_mfma_f32_16x16x32_bf16 v[124:127], a[184:187], a[144:147], v[124:127]\n" \
"s_add_u32 m0, s40, 0x1000\n" \
"s_add_u32 s85, s39, 0xd000\n" \
"v_mfma_f32_16x16x32_bf16 v[124:127], a[248:251], a[208:211], v[124:127]\n" \
"buffer_load_dwordx4 v20, s[80:83], s85 offen sc1 lds\n" \
"v_mfma_f32_16x16x32_bf16 v[128:131], a[188:191], a[144:147], v[128:131]\n" \
"ds_read_b64_tr_b16 a[224:225], v32 offset:36864\n" \
"s_lshl_b32 s6, s12, 10\n" \
"v_mfma_f32_16x16x32_bf16 v[128:131], a[252:255], a[208:211], v[128:131]\n" \
"ds_read_b128 a[144:147], v28 offset:8192\n" \
"s_or_b32 s6, s8, s6\n" \
"v_mfma_f32_16x16x32_bf16 v[144:147], a[188:191], a[148:151], v[144:147]\n" \
"ds_read_b128 a[208:211], v29 offset:8192\n" \
"v_mfma_f32_16x16x32_bf16 v[144:147], a[252:255], a[212:215], v[144:147]\n" \
"s_add_u32 m0, s40, 0x3000\n" \
"s_movk_i32 s84, 0x300\n" \
"v_mfma_f32_16x16x32_bf16 v[140:143], a[184:187], a[148:151], v[140:143]\n" \
"buffer_load_dwordx4 v21, s[76:79], s84 offen offset:0 lds\n" \
"v_mfma_f32_16x16x32_bf16 v[140:143], a[248:251], a[212:215], v[140:143]\n" \
"ds_read_b64_tr_b16 a[226:227], v32 offset:37376\n" \
"v_mfma_f32_16x16x32_bf16 v[136:139], a[180:183], a[148:151], v[136:139]\n" \
"s_cmp_lg_u32 s16, 0\n" \
"s_cselect_b32 s6, s7, s6\n" \
"v_mfma_f32_16x16x32_bf16 v[136:139], a[244:247], a[212:215], v[136:139]\n" \
"ds_read_b64_tr_b16 a[164:165], v33 offset:32768\n" \
"s_mulk_i32 s4, 0x4800\n" \
"v_mfma_f32_16x16x32_bf16 v[132:135], a[176:179], a[148:151], v[132:135]\n" \
"v_writelane_b32 v11, s6, 42\n" \
"s_add_i32 s6, s14, s10\n" \
"v_mfma_f32_16x16x32_bf16 v[132:135], a[240:243], a[212:215], v[132:135]\n" \
"ds_read_b128 a[148:151], v28 offset:10240\n" \
"v_writelane_b32 v11, s6, 43\n" \
"v_mfma_f32_16x16x32_bf16 v[148:151], a[176:179], a[152:155], v[148:151]\n" \
"ds_read_b128 a[212:215], v29 offset:10240\n" \
"s_add_i32 s6, s9, 0x21c000\n" \
"v_mfma_f32_16x16x32_bf16 v[148:151], a[240:243], a[216:219], v[148:151]\n" \
"s_add_u32 m0, s31, 0x0\n" \
"s_mov_b32 s84, 0x300000\n" \
"v_mfma_f32_16x16x32_bf16 v[152:155], a[180:183], a[152:155], v[152:155]\n" \
"buffer_load_dwordx4 v26, s[68:71], s84 offen offset:0 lds\n" \
"v_mfma_f32_16x16x32_bf16 v[152:155], a[244:247], a[216:219], v[152:155]\n" \
"s_addk_i32 s84, 0x7c00\n" \
"v_mfma_f32_16x16x32_bf16 v[156:159], a[184:187], a[152:155], v[156:159]\n" \
"buffer_load_dwordx4 v26, s[68:71], s84 offen offset:1024 lds\n" \
"v_mfma_f32_16x16x32_bf16 v[156:159], a[248:251], a[216:219], v[156:159]\n" \
"ds_read_b64_tr_b16 a[166:167], v33 offset:33280\n" \
"s_add_i32 s9, s6, s10\n" \
"v_mfma_f32_16x16x32_bf16 v[160:163], a[188:191], a[152:155], v[160:163]\n" \
"ds_read_b64_tr_b16 a[228:229], v33 offset:36864\n" \
"v_mfma_f32_16x16x32_bf16 v[160:163], a[252:255], a[216:219], v[160:163]\n" \
"ds_read_b128 a[152:155], v28 offset:12288\n" \
"v_mfma_f32_16x16x32_bf16 v[176:179], a[188:191], a[156:159], v[176:179]\n" \
"ds_read_b128 a[216:219], v29 offset:12288\n" \
"v_mfma_f32_16x16x32_bf16 v[176:179], a[252:255], a[220:223], v[176:179]\n" \
"s_addk_i32 s84, 0x7c00\n" \
"v_mfma_f32_16x16x32_bf16 v[172:175], a[184:187], a[156:159], v[172:175]\n" \
"buffer_load_dwordx4 v27, s[68:71], s84 offen offset:2048 lds\n" \
"v_mfma_f32_16x16x32_bf16 v[172:175], a[248:251], a[220:223], v[172:175]\n" \
"ds_read_b64_tr_b16 a[230:231], v33 offset:37376\n" \
"v_mfma_f32_16x16x32_bf16 v[168:171], a[180:183], a[156:159], v[168:171]\n" \
"s_addk_i32 s84, 0x7c00\n" \
"v_mfma_f32_16x16x32_bf16 v[168:171], a[244:247], a[220:223], v[168:171]\n" \
"buffer_load_dwordx4 v27, s[68:71], s84 offen offset:3072 lds\n" \
"v_mfma_f32_16x16x32_bf16 v[164:167], a[176:179], a[156:159], v[164:167]\n" \
"ds_read_b64_tr_b16 a[172:173], v35 offset:32768\n" \
"v_mfma_f32_16x16x32_bf16 v[164:167], a[240:243], a[220:223], v[164:167]\n" \
"ds_read_b128 a[156:159], v28 offset:14336\n" \
"v_mfma_f32_16x16x32_bf16 v[84:87], a[176:179], a[136:139], v[84:87]\n" \
"ds_read_b128 a[220:223], v29 offset:14336\n" \
"v_mfma_f32_16x16x32_bf16 v[84:87], a[240:243], a[200:203], v[84:87]\n" \
"s_add_u32 s88, s42, 0x3000\n" \
"buffer_store_dwordx4 v[244:247], v20, s[80:83], s88 offen offset:2048 sc1\n" \
"v_mfma_f32_16x16x32_bf16 v[88:91], a[180:183], a[136:139], v[88:91]\n" \
"ds_read_b64_tr_b16 a[174:175], v35 offset:33280\n" \
"v_mfma_f32_16x16x32_bf16 v[88:91], a[244:247], a[200:203], v[88:91]\n" \
"ds_read_b64_tr_b16 a[236:237], v35 offset:36864\n" \
"v_mfma_f32_16x16x32_bf16 v[92:95], a[184:187], a[136:139], v[92:95]\n" \
"ds_read_b64_tr_b16 a[238:239], v35 offset:37376\n" \
"v_mfma_f32_16x16x32_bf16 v[92:95], a[248:251], a[200:203], v[92:95]\n" \
"s_add_u32 m0, s30, 0x8000\n" \
"s_movk_i32 s84, 0x380\n" \
"v_mfma_f32_16x16x32_bf16 v[96:99], a[188:191], a[136:139], v[96:99]\n" \
"buffer_load_dwordx4 v24, s[76:79], s84 offen offset:0 lds\n" \
"v_mfma_f32_16x16x32_bf16 v[96:99], a[252:255], a[200:203], v[96:99]\n" \
"ds_read_b128 a[136:139], v28 offset:4096\n" \
"v_mfma_f32_16x16x32_bf16 v[112:115], a[188:191], a[140:143], v[112:115]\n" \
"ds_read_b128 a[200:203], v29 offset:4096\n" \
"v_mfma_f32_16x16x32_bf16 v[112:115], a[252:255], a[204:207], v[112:115]\n" \
"ds_read_b64_tr_b16 a[168:169], v34 offset:32768\n" \
"v_mfma_f32_16x16x32_bf16 v[108:111], a[184:187], a[140:143], v[108:111]\n" \
"s_movk_i32 s84, 0x2380\n" \
"v_mfma_f32_16x16x32_bf16 v[108:111], a[248:251], a[204:207], v[108:111]\n" \
"buffer_load_dwordx4 v25, s[76:79], s84 offen offset:1024 lds\n" \
"v_mfma_f32_16x16x32_bf16 v[104:107], a[180:183], a[140:143], v[104:107]\n" \
"ds_read_b64_tr_b16 a[170:171], v34 offset:33280\n" \
"v_mfma_f32_16x16x32_bf16 v[104:107], a[244:247], a[204:207], v[104:107]\n" \
"ds_read_b64_tr_b16 a[232:233], v34 offset:36864\n" \
"v_mfma_f32_16x16x32_bf16 v[100:103], a[176:179], a[140:143], v[100:103]\n" \
"s_cmp_lg_u32 s12, 0\n" \
"s_cselect_b32 s6, s6, s14\n" \
"v_mfma_f32_16x16x32_bf16 v[100:103], a[240:243], a[204:207], v[100:103]\n" \
"ds_read_b128 a[140:143], v28 offset:6144\n" \
"s_addk_i32 s6, 0x100\n" \
"v_mfma_f32_16x16x32_bf16 v[52:55], a[176:179], a[128:131], v[52:55]\n" \
"ds_read_b128 a[204:207], v29 offset:6144\n" \
"v_writelane_b32 v11, s9, 44\n" \
"v_mfma_f32_16x16x32_bf16 v[52:55], a[240:243], a[192:195], v[52:55]\n" \
"s_movk_i32 s84, 0x4380\n" \
"v_mfma_f32_16x16x32_bf16 v[56:59], a[180:183], a[128:131], v[56:59]\n" \
"buffer_load_dwordx4 v24, s[76:79], s84 offen offset:2048 lds\n" \
"v_mfma_f32_16x16x32_bf16 v[56:59], a[244:247], a[192:195], v[56:59]\n" \
"s_movk_i32 s84, 0x6380\n" \
"v_mfma_f32_16x16x32_bf16 v[60:63], a[184:187], a[128:131], v[60:63]\n" \
"buffer_load_dwordx4 v25, s[76:79], s84 offen offset:3072 lds\n" \
"v_mfma_f32_16x16x32_bf16 v[60:63], a[248:251], a[192:195], v[60:63]\n" \
"ds_read_b64_tr_b16 a[234:235], v34 offset:37376\n" \
"v_mfma_f32_16x16x32_bf16 v[64:67], a[188:191], a[128:131], v[64:67]\n" \
"s_cmp_lg_u32 s16, 0\n" \
"s_cselect_b32 s6, s7, s6\n" \
"v_mfma_f32_16x16x32_bf16 v[64:67], a[252:255], a[192:195], v[64:67]\n" \
"ds_read_b128 a[128:131], v28 offset:0\n" \
"v_writelane_b32 v11, s6, 45\n" \
"v_mfma_f32_16x16x32_bf16 v[80:83], a[188:191], a[132:135], v[80:83]\n" \
"ds_read_b128 a[192:195], v29 offset:0\n" \
"s_or_b32 s6, s8, s15\n" \
"v_mfma_f32_16x16x32_bf16 v[80:83], a[252:255], a[196:199], v[80:83]\n" \
"v_writelane_b32 v11, s6, 53\n" \
"s_lshl_b32 s6, s3, 17\n" \
"v_mfma_f32_16x16x32_bf16 v[76:79], a[184:187], a[132:135], v[76:79]\n" \
"s_add_i32 s6, s13, s6\n" \
"s_or_b32 s5, s6, s5\n" \
"v_mfma_f32_16x16x32_bf16 v[76:79], a[248:251], a[196:199], v[76:79]\n" \
"s_or_b32 s5, s5, 0x3840\n" \
"v_writelane_b32 v11, s5, 6\n" \
"v_mfma_f32_16x16x32_bf16 v[72:75], a[180:183], a[132:135], v[72:75]\n" \
"s_mov_b32 s5, 0xff0000\n" \
"v_mfma_f32_16x16x32_bf16 v[72:75], a[244:247], a[196:199], v[72:75]\n" \
"s_cmp_lg_u32 s16, 0\n" \
"s_cselect_b32 s5, s17, s5\n" \
"v_mfma_f32_16x16x32_bf16 v[68:71], a[176:179], a[132:135], v[68:71]\n" \
"v_writelane_b32 v11, s5, 7\n" \
"s_mul_i32 s5, s3, 0x1200\n" \
"v_mfma_f32_16x16x32_bf16 v[68:71], a[240:243], a[196:199], v[68:71]\n" \
"ds_read_b128 a[132:135], v28 offset:2048\n" \
"ds_read_b128 a[196:199], v29 offset:2048\n" \
"s_waitcnt lgkmcnt(15)\n" \
"v_mfma_f32_16x16x32_bf16 a[0:3], a[160:163], a[144:147], a[0:3]\n" \
"s_add_u32 m0, s30, 0x9000\n" \
"s_addk_i32 s84, 0x3000\n" \
"v_mfma_f32_16x16x32_bf16 a[0:3], a[224:227], a[208:211], a[0:3]\n" \
"buffer_load_dwordx4 v24, s[76:79], s84 offen offset:0 lds\n" \
"v_mfma_f32_16x16x32_bf16 a[4:7], a[164:167], a[144:147], a[4:7]\n" \
"ds_read_b64_tr_b16 a[176:177], v36 offset:32768\n" \
"s_add_i32 s4, s5, s4\n" \
"v_mfma_f32_16x16x32_bf16 a[4:7], a[228:231], a[208:211], a[4:7]\n" \
"ds_read_b64_tr_b16 a[178:179], v36 offset:33792\n" \
"s_add_i32 s4, s4, s11\n" \
"s_waitcnt lgkmcnt(15)\n" \
"v_mfma_f32_16x16x32_bf16 a[12:15], a[172:175], a[144:147], a[12:15]\n" \
"ds_read_b64_tr_b16 a[240:241], v36 offset:40960\n" \
"v_writelane_b32 v11, s4, 8\n" \
"s_waitcnt lgkmcnt(15)\n" \
"v_mfma_f32_16x16x32_bf16 a[12:15], a[236:239], a[208:211], a[12:15]\n" \
"s_add_u32 s89, s42, 0x3000\n" \
"buffer_store_dwordx4 v[184:187], v20, s[80:83], s89 offen offset:1024 sc1\n" \
"s_waitcnt lgkmcnt(11)\n" \
"v_mfma_f32_16x16x32_bf16 a[8:11], a[144:147], a[168:171], a[8:11]\n" \
"ds_read_b64_tr_b16 a[242:243], v36 offset:41984\n" \
"s_lshl_b32 s4, s2, 16\n" \
"s_waitcnt lgkmcnt(8)\n" \
"v_mfma_f32_16x16x32_bf16 a[8:11], a[208:211], a[232:235], a[8:11]\n" \
"ds_read_b64_tr_b16 a[180:181], v37 offset:32768\n" \
"s_add_i32 s5, s4, 0x10000\n" \
"v_mfma_f32_16x16x32_bf16 a[24:27], a[148:151], a[168:171], a[24:27]\n" \
"ds_read_b64_tr_b16 a[182:183], v37 offset:33792\n" \
"s_and_b32 s5, s5, 0x70000\n" \
"v_mfma_f32_16x16x32_bf16 a[24:27], a[212:215], a[232:235], a[24:27]\n" \
"ds_read_b64_tr_b16 a[244:245], v37 offset:40960\n" \
"s_add_i32 s5, s13, s5\n" \
"v_mfma_f32_16x16x32_bf16 a[28:31], a[172:175], a[148:151], a[28:31]\n" \
"v_writelane_b32 v11, s5, 46\n" \
"s_add_i32 s5, s4, 0x20000\n" \
"v_mfma_f32_16x16x32_bf16 a[28:31], a[236:239], a[212:215], a[28:31]\n" \
"s_addk_i32 s84, 0x2000\n" \
"v_mfma_f32_16x16x32_bf16 a[20:23], a[164:167], a[148:151], a[20:23]\n" \
"buffer_load_dwordx4 v25, s[76:79], s84 offen offset:1024 lds\n" \
"v_mfma_f32_16x16x32_bf16 a[20:23], a[228:231], a[212:215], a[20:23]\n" \
"ds_read_b64_tr_b16 a[246:247], v37 offset:41984\n" \
"s_and_b32 s5, s5, 0x70000\n" \
"v_mfma_f32_16x16x32_bf16 a[16:19], a[160:163], a[148:151], a[16:19]\n" \
"ds_read_b64_tr_b16 a[184:185], v38 offset:32768\n" \
"s_add_i32 s5, s13, s5\n" \
"v_mfma_f32_16x16x32_bf16 a[16:19], a[224:227], a[212:215], a[16:19]\n" \
"ds_read_b64_tr_b16 a[186:187], v38 offset:33792\n" \
"s_bitset1_b32 s5, 11\n" \
"v_mfma_f32_16x16x32_bf16 a[32:35], a[160:163], a[152:155], a[32:35]\n" \
"s_addk_i32 s84, 0x2000\n" \
"s_nop 0\n" \
"v_mfma_f32_16x16x32_bf16 a[32:35], a[224:227], a[216:219], a[32:35]\n" \
"buffer_load_dwordx4 v24, s[76:79], s84 offen offset:2048 lds\n" \
"v_mfma_f32_16x16x32_bf16 a[36:39], a[164:167], a[152:155], a[36:39]\n" \
"ds_read_b64_tr_b16 a[248:249], v38 offset:40960\n" \
"v_writelane_b32 v11, s5, 47\n" \
"v_mfma_f32_16x16x32_bf16 a[36:39], a[228:231], a[216:219], a[36:39]\n" \
"ds_read_b64_tr_b16 a[250:251], v38 offset:41984\n" \
"s_add_i32 s5, s4, 0x30000\n" \
"v_mfma_f32_16x16x32_bf16 a[44:47], a[172:175], a[152:155], a[44:47]\n" \
"ds_read_b64_tr_b16 a[188:189], v39 offset:32768\n" \
"s_and_b32 s5, s5, 0x70000\n" \
"v_mfma_f32_16x16x32_bf16 a[44:47], a[236:239], a[216:219], a[44:47]\n" \
"ds_read_b64_tr_b16 a[190:191], v39 offset:33792\n" \
"s_add_i32 s5, s13, s5\n" \
"v_mfma_f32_16x16x32_bf16 a[40:43], a[152:155], a[168:171], a[40:43]\n" \
"s_add_u32 s88, s42, 0x3000\n" \
"buffer_store_dwordx4 v[188:191], v20, s[80:83], s88 offen offset:0 sc1\n" \
"v_mfma_f32_16x16x32_bf16 a[40:43], a[216:219], a[232:235], a[40:43]\n" \
"ds_read_b64_tr_b16 a[252:253], v39 offset:40960\n" \
"s_bitset1_b32 s5, 12\n" \
"v_mfma_f32_16x16x32_bf16 a[56:59], a[156:159], a[168:171], a[56:59]\n" \
"ds_read_b64_tr_b16 a[254:255], v39 offset:41984\n" \
"v_writelane_b32 v11, s5, 48\n" \
"v_mfma_f32_16x16x32_bf16 a[56:59], a[220:223], a[232:235], a[56:59]\n" \
"s_and_b32 s5, s4, 0x70000\n" \
"s_xor_b32 s5, s5, 0x40000\n" \
"v_mfma_f32_16x16x32_bf16 a[60:63], a[172:175], a[156:159], a[60:63]\n" \
"s_add_i32 s5, s13, s5\n" \
"s_or_b32 s5, s5, 0x1800\n" \
"v_mfma_f32_16x16x32_bf16 a[60:63], a[236:239], a[220:223], a[60:63]\n" \
"s_addk_i32 s84, 0x2000\n" \
"v_mfma_f32_16x16x32_bf16 a[52:55], a[164:167], a[156:159], a[52:55]\n" \
"buffer_load_dwordx4 v25, s[76:79], s84 offen offset:3072 lds\n" \
"v_mfma_f32_16x16x32_bf16 a[52:55], a[228:231], a[220:223], a[52:55]\n" \
"v_writelane_b32 v11, s5, 49\n" \
"s_add_i32 s5, s4, 0x50000\n" \
"v_mfma_f32_16x16x32_bf16 a[48:51], a[160:163], a[156:159], a[48:51]\n" \
"s_and_b32 s5, s5, 0x70000\n" \
"s_add_i32 s5, s13, s5\n" \
"v_mfma_f32_16x16x32_bf16 a[48:51], a[224:227], a[220:223], a[48:51]\n" \
"s_bitset1_b32 s5, 13\n" \
"v_writelane_b32 v11, s5, 50\n" \
"v_mfma_f32_16x16x32_bf16 a[64:67], a[164:167], a[140:143], a[64:67]\n" \
"s_add_i32 s5, s4, 0x60000\n" \
"s_and_b32 s5, s5, 0x70000\n" \
"v_mfma_f32_16x16x32_bf16 a[64:67], a[228:231], a[204:207], a[64:67]\n" \
"s_add_i32 s4, s4, 0x70000\n" \
"s_add_i32 s5, s13, s5\n" \
"v_mfma_f32_16x16x32_bf16 a[80:83], a[172:175], a[140:143], a[80:83]\n" \
"s_and_b32 s4, s4, 0x70000\n" \
"s_or_b32 s5, s5, 0x2800\n" \
"v_mfma_f32_16x16x32_bf16 a[80:83], a[236:239], a[204:207], a[80:83]\n" \
"v_writelane_b32 v11, s5, 51\n" \
"s_add_i32 s13, s13, s4\n" \
"v_mfma_f32_16x16x32_bf16 a[68:71], a[140:143], a[168:171], a[68:71]\n" \
"s_or_b32 s4, s13, 0x3000\n" \
"v_writelane_b32 v11, s4, 52\n" \
"s_waitcnt vmcnt(11) lgkmcnt(0)\n" \
"v_mfma_f32_16x16x32_bf16 a[68:71], a[204:207], a[232:235], a[68:71]\n" \
"s_barrier\n" \
"v_xor_b32_e32 v28, 0x20000, v28\n" \
"v_xor_b32_e32 v29, 0x20000, v29\n" \
"v_mfma_f32_16x16x32_bf16 v[116:119], a[176:179], a[144:147], v[116:119]\n" \
"s_add_u32 m0, s40, 0x8000\n" \
"s_add_u32 s85, s39, 0xe000\n" \
"v_mfma_f32_16x16x32_bf16 v[116:119], a[240:243], a[208:211], v[116:119]\n" \
"buffer_load_dwordx4 v20, s[80:83], s85 offen sc1 lds\n" \
"v_mfma_f32_16x16x32_bf16 v[120:123], a[180:183], a[144:147], v[120:123]\n" \
"ds_read_b64_tr_b16 a[160:161], v32 offset:0\n" \
"s_movk_i32 s11, 0x4000\n" \
"v_mfma_f32_16x16x32_bf16 v[120:123], a[244:247], a[208:211], v[120:123]\n" \
"ds_read_b64_tr_b16 a[162:163], v32 offset:512\n" \
"s_lshl_b32 s4, s2, 2\n" \
"v_mfma_f32_16x16x32_bf16 v[124:127], a[184:187], a[144:147], v[124:127]\n" \
"s_add_u32 m0, s40, 0x9000\n" \
"s_add_u32 s85, s39, 0xf000\n" \
"v_mfma_f32_16x16x32_bf16 v[124:127], a[248:251], a[208:211], v[124:127]\n" \
"buffer_load_dwordx4 v20, s[80:83], s85 offen sc1 lds\n" \
"v_mfma_f32_16x16x32_bf16 v[128:131], a[188:191], a[144:147], v[128:131]\n" \
"ds_read_b64_tr_b16 a[224:225], v32 offset:4096\n" \
"s_and_b32 s4, s4, 28\n" \
"v_mfma_f32_16x16x32_bf16 v[128:131], a[252:255], a[208:211], v[128:131]\n" \
"ds_read_b128 a[144:147], v28 offset:8192\n" \
"s_bfe_u32 s5, s2, 0x20003\n" \
"v_mfma_f32_16x16x32_bf16 v[144:147], a[188:191], a[148:151], v[144:147]\n" \
"ds_read_b128 a[208:211], v29 offset:8192\n" \
"s_or_b32 s4, s4, s5\n" \
"v_mfma_f32_16x16x32_bf16 v[144:147], a[252:255], a[212:215], v[144:147]\n" \
"s_add_u32 m0, s40, 0xb000\n" \
"s_movk_i32 s84, 0x380\n" \
"v_mfma_f32_16x16x32_bf16 v[140:143], a[184:187], a[148:151], v[140:143]\n" \
"buffer_load_dwordx4 v21, s[76:79], s84 offen offset:0 lds\n" \
"v_mfma_f32_16x16x32_bf16 v[140:143], a[248:251], a[212:215], v[140:143]\n" \
"ds_read_b64_tr_b16 a[226:227], v32 offset:4608\n" \
"s_lshr_b32 s5, s2, 5\n" \
"v_mfma_f32_16x16x32_bf16 v[136:139], a[180:183], a[148:151], v[136:139]\n" \
"s_lshr_b32 s19, s3, 1\n" \
"s_lshl_b32 s18, s5, 1\n" \
"v_mfma_f32_16x16x32_bf16 v[136:139], a[244:247], a[212:215], v[136:139]\n" \
"ds_read_b64_tr_b16 a[164:165], v33 offset:0\n" \
"s_lshl_b32 s6, s4, 20\n" \
"v_mfma_f32_16x16x32_bf16 v[132:135], a[176:179], a[148:151], v[132:135]\n" \
"s_lshl_b32 s7, s4, 8\n" \
"s_and_b32 s13, s3, 1\n" \
"v_mfma_f32_16x16x32_bf16 v[132:135], a[240:243], a[212:215], v[132:135]\n" \
"ds_read_b128 a[148:151], v28 offset:10240\n" \
"s_add_i32 s14, s18, s19\n" \
"v_mfma_f32_16x16x32_bf16 v[148:151], a[176:179], a[152:155], v[148:151]\n" \
"ds_read_b128 a[212:215], v29 offset:10240\n" \
"v_mfma_f32_16x16x32_bf16 v[148:151], a[240:243], a[216:219], v[148:151]\n" \
"s_add_u32 m0, s31, 0x8000\n" \
"s_mov_b32 s84, 0x380000\n" \
"v_mfma_f32_16x16x32_bf16 v[152:155], a[180:183], a[152:155], v[152:155]\n" \
"buffer_load_dwordx4 v26, s[68:71], s84 offen offset:0 lds\n" \
"v_mfma_f32_16x16x32_bf16 v[152:155], a[244:247], a[216:219], v[152:155]\n" \
"s_addk_i32 s84, 0x7c00\n" \
"s_nop 0\n" \
"v_mfma_f32_16x16x32_bf16 v[156:159], a[184:187], a[152:155], v[156:159]\n" \
"buffer_load_dwordx4 v26, s[68:71], s84 offen offset:1024 lds\n" \
"v_mfma_f32_16x16x32_bf16 v[156:159], a[248:251], a[216:219], v[156:159]\n" \
"ds_read_b64_tr_b16 a[166:167], v33 offset:512\n" \
"v_mfma_f32_16x16x32_bf16 v[160:163], a[188:191], a[152:155], v[160:163]\n" \
"ds_read_b64_tr_b16 a[228:229], v33 offset:4096\n" \
"v_mfma_f32_16x16x32_bf16 v[160:163], a[252:255], a[216:219], v[160:163]\n" \
"ds_read_b128 a[152:155], v28 offset:12288\n" \
"v_mfma_f32_16x16x32_bf16 v[176:179], a[188:191], a[156:159], v[176:179]\n" \
"ds_read_b128 a[216:219], v29 offset:12288\n" \
"v_mfma_f32_16x16x32_bf16 v[176:179], a[252:255], a[220:223], v[176:179]\n" \
"s_addk_i32 s84, 0x7c00\n" \
"v_mfma_f32_16x16x32_bf16 v[172:175], a[184:187], a[156:159], v[172:175]\n" \
"buffer_load_dwordx4 v27, s[68:71], s84 offen offset:2048 lds\n" \
"v_mfma_f32_16x16x32_bf16 v[172:175], a[248:251], a[220:223], v[172:175]\n" \
"ds_read_b64_tr_b16 a[230:231], v33 offset:4608\n" \
"v_mfma_f32_16x16x32_bf16 v[168:171], a[180:183], a[156:159], v[168:171]\n" \
"s_addk_i32 s84, 0x7c00\n" \
"v_mfma_f32_16x16x32_bf16 v[168:171], a[244:247], a[220:223], v[168:171]\n" \
"buffer_load_dwordx4 v27, s[68:71], s84 offen offset:3072 lds\n" \
"v_mfma_f32_16x16x32_bf16 v[164:167], a[176:179], a[156:159], v[164:167]\n" \
"ds_read_b64_tr_b16 a[172:173], v35 offset:0\n" \
"v_mfma_f32_16x16x32_bf16 v[164:167], a[240:243], a[220:223], v[164:167]\n" \
"ds_read_b128 a[156:159], v28 offset:14336\n" \
"v_mfma_f32_16x16x32_bf16 v[84:87], a[176:179], a[136:139], v[84:87]\n" \
"ds_read_b128 a[220:223], v29 offset:14336\n" \
"v_mfma_f32_16x16x32_bf16 v[84:87], a[240:243], a[200:203], v[84:87]\n" \
"s_add_u32 s89, s42, 0x0\n" \
"buffer_store_dwordx4 v[192:195], v20, s[80:83], s89 offen offset:2048 sc1\n" \
"v_mfma_f32_16x16x32_bf16 v[88:91], a[180:183], a[136:139], v[88:91]\n" \
"ds_read_b64_tr_b16 a[174:175], v35 offset:512\n" \
"v_mfma_f32_16x16x32_bf16 v[88:91], a[244:247], a[200:203], v[88:91]\n" \
"ds_read_b64_tr_b16 a[236:237], v35 offset:4096\n" \
"v_mfma_f32_16x16x32_bf16 v[92:95], a[184:187], a[136:139], v[92:95]\n" \
"ds_read_b64_tr_b16 a[238:239], v35 offset:4608\n" \
"v_mfma_f32_16x16x32_bf16 v[92:95], a[248:251], a[200:203], v[92:95]\n" \
"s_add_u32 m0, s30, 0x20000\n" \
"s_movk_i32 s84, 0x400\n" \
"v_mfma_f32_16x16x32_bf16 v[96:99], a[188:191], a[136:139], v[96:99]\n" \
"buffer_load_dwordx4 v24, s[76:79], s84 offen offset:0 lds\n" \
"v_mfma_f32_16x16x32_bf16 v[96:99], a[252:255], a[200:203], v[96:99]\n" \
"ds_read_b128 a[136:139], v28 offset:4096\n" \
"v_mfma_f32_16x16x32_bf16 v[112:115], a[188:191], a[140:143], v[112:115]\n" \
"ds_read_b128 a[200:203], v29 offset:4096\n" \
"v_mfma_f32_16x16x32_bf16 v[112:115], a[252:255], a[204:207], v[112:115]\n" \
"ds_read_b64_tr_b16 a[168:169], v34 offset:0\n" \
"v_mfma_f32_16x16x32_bf16 v[108:111], a[184:187], a[140:143], v[108:111]\n" \
"s_movk_i32 s84, 0x2400\n" \
"v_mfma_f32_16x16x32_bf16 v[108:111], a[248:251], a[204:207], v[108:111]\n" \
"buffer_load_dwordx4 v25, s[76:79], s84 offen offset:1024 lds\n" \
"v_mfma_f32_16x16x32_bf16 v[104:107], a[180:183], a[140:143], v[104:107]\n" \
"ds_read_b64_tr_b16 a[170:171], v34 offset:512\n" \
"v_mfma_f32_16x16x32_bf16 v[104:107], a[244:247], a[204:207], v[104:107]\n" \
"ds_read_b64_tr_b16 a[232:233], v34 offset:4096\n" \
"v_mfma_f32_16x16x32_bf16 v[100:103], a[176:179], a[140:143], v[100:103]\n" \
"s_add_u32 s6, s26, s6\n" \
"s_addc_u32 s9, s27, 0\n" \
"v_mfma_f32_16x16x32_bf16 v[100:103], a[240:243], a[204:207], v[100:103]\n" \
"ds_read_b128 a[140:143], v28 offset:6144\n" \
"v_mfma_f32_16x16x32_bf16 v[52:55], a[176:179], a[128:131], v[52:55]\n" \
"ds_read_b128 a[204:207], v29 offset:6144\n" \
"v_mfma_f32_16x16x32_bf16 v[52:55], a[240:243], a[192:195], v[52:55]\n" \
"s_movk_i32 s84, 0x4400\n" \
"v_mfma_f32_16x16x32_bf16 v[56:59], a[180:183], a[128:131], v[56:59]\n" \
"buffer_load_dwordx4 v24, s[76:79], s84 offen offset:2048 lds\n" \
"v_mfma_f32_16x16x32_bf16 v[56:59], a[244:247], a[192:195], v[56:59]\n" \
"s_movk_i32 s84, 0x6400\n" \
"v_mfma_f32_16x16x32_bf16 v[60:63], a[184:187], a[128:131], v[60:63]\n" \
"buffer_load_dwordx4 v25, s[76:79], s84 offen offset:3072 lds\n" \
"v_mfma_f32_16x16x32_bf16 v[60:63], a[248:251], a[192:195], v[60:63]\n" \
"ds_read_b64_tr_b16 a[234:235], v34 offset:4608\n" \
"v_mfma_f32_16x16x32_bf16 v[64:67], a[188:191], a[128:131], v[64:67]\n" \
"s_add_u32 s8, s6, 0x1000000\n" \
"s_addc_u32 s6, s9, 0\n" \
"v_mfma_f32_16x16x32_bf16 v[64:67], a[252:255], a[192:195], v[64:67]\n" \
"ds_read_b128 a[128:131], v28 offset:0\n" \
"s_and_b32 s9, s6, 0xffff\n" \
"v_mfma_f32_16x16x32_bf16 v[80:83], a[188:191], a[132:135], v[80:83]\n" \
"ds_read_b128 a[192:195], v29 offset:0\n" \
"s_lshl_b32 s6, s5, 16\n" \
"v_mfma_f32_16x16x32_bf16 v[80:83], a[252:255], a[196:199], v[80:83]\n" \
"s_sub_i32 s10, 0x100000, s6\n" \
"s_mov_b32 s6, 0x8000\n" \
"v_mfma_f32_16x16x32_bf16 v[76:79], a[184:187], a[132:135], v[76:79]\n" \
"s_cmp_lg_u32 s5, 0\n" \
"s_cselect_b32 s17, s11, s6\n" \
"v_mfma_f32_16x16x32_bf16 v[76:79], a[248:251], a[196:199], v[76:79]\n" \
"s_lshl_b32 s6, s3, 1\n" \
"s_lshl_b32 s5, s5, 3\n" \
"v_mfma_f32_16x16x32_bf16 v[72:75], a[180:183], a[132:135], v[72:75]\n" \
"s_and_b32 s6, s6, 4\n" \
"s_or_b32 s12, s5, s6\n" \
"v_mfma_f32_16x16x32_bf16 v[72:75], a[244:247], a[196:199], v[72:75]\n" \
"s_sub_i32 s5, s13, s5\n" \
"s_or_b32 s5, s5, s6\n" \
"v_mfma_f32_16x16x32_bf16 v[68:71], a[176:179], a[132:135], v[68:71]\n" \
"s_or_b32 s6, s12, s13\n" \
"s_lshl_b32 s4, s4, 2\n" \
"v_mfma_f32_16x16x32_bf16 v[68:71], a[240:243], a[196:199], v[68:71]\n" \
"ds_read_b128 a[132:135], v28 offset:2048\n" \
"ds_read_b128 a[196:199], v29 offset:2048\n" \
"s_waitcnt lgkmcnt(15)\n" \
"v_mfma_f32_16x16x32_bf16 a[0:3], a[160:163], a[144:147], a[0:3]\n" \
"s_add_u32 m0, s30, 0x21000\n" \
"s_addk_i32 s84, 0x3000\n" \
"v_mfma_f32_16x16x32_bf16 a[0:3], a[224:227], a[208:211], a[0:3]\n" \
"buffer_load_dwordx4 v24, s[76:79], s84 offen offset:0 lds\n" \
"v_mfma_f32_16x16x32_bf16 a[4:7], a[164:167], a[144:147], a[4:7]\n" \
"ds_read_b64_tr_b16 a[176:177], v36 offset:0\n" \
"s_mulk_i32 s6, 0x92\n" \
"v_mfma_f32_16x16x32_bf16 a[4:7], a[228:231], a[208:211], a[4:7]\n" \
"ds_read_b64_tr_b16 a[178:179], v36 offset:1024\n" \
"s_add_i32 s4, s4, 18\n" \
"s_waitcnt lgkmcnt(15)\n" \
"v_mfma_f32_16x16x32_bf16 a[12:15], a[172:175], a[144:147], a[12:15]\n" \
"ds_read_b64_tr_b16 a[240:241], v36 offset:8192\n" \
"s_mulk_i32 s5, 0x92\n" \
"s_waitcnt lgkmcnt(15)\n" \
"v_mfma_f32_16x16x32_bf16 a[12:15], a[236:239], a[208:211], a[12:15]\n" \
"s_add_u32 s88, s42, 0x0\n" \
"buffer_store_dwordx4 v[196:199], v20, s[80:83], s88 offen offset:3072 sc1\n" \
"s_waitcnt lgkmcnt(11)\n" \
"v_mfma_f32_16x16x32_bf16 a[8:11], a[144:147], a[168:171], a[8:11]\n" \
"ds_read_b64_tr_b16 a[242:243], v36 offset:9216\n" \
"s_add_i32 s6, s4, s6\n" \
"s_waitcnt lgkmcnt(8)\n" \
"v_mfma_f32_16x16x32_bf16 a[8:11], a[208:211], a[232:235], a[8:11]\n" \
"ds_read_b64_tr_b16 a[180:181], v37 offset:0\n" \
"s_add_i32 s5, s5, s4\n" \
"v_mfma_f32_16x16x32_bf16 a[24:27], a[148:151], a[168:171], a[24:27]\n" \
"ds_read_b64_tr_b16 a[182:183], v37 offset:1024\n" \
"s_lshl_b32 s4, s3, 11\n" \
"v_mfma_f32_16x16x32_bf16 a[24:27], a[212:215], a[232:235], a[24:27]\n" \
"ds_read_b64_tr_b16 a[244:245], v37 offset:8192\n" \
"s_lshl_b32 s15, s13, 12\n" \
"v_mfma_f32_16x16x32_bf16 a[28:31], a[172:175], a[148:151], a[28:31]\n" \
"s_lshl_b32 s16, s19, 14\n" \
"s_mul_i32 s19, s19, 0xe000\n" \
"v_mfma_f32_16x16x32_bf16 a[28:31], a[236:239], a[212:215], a[28:31]\n" \
"s_addk_i32 s84, 0x2000\n" \
"v_mfma_f32_16x16x32_bf16 a[20:23], a[164:167], a[148:151], a[20:23]\n" \
"buffer_load_dwordx4 v25, s[76:79], s84 offen offset:1024 lds\n" \
"v_mfma_f32_16x16x32_bf16 a[20:23], a[228:231], a[212:215], a[20:23]\n" \
"ds_read_b64_tr_b16 a[246:247], v37 offset:9216\n" \
"s_mov_b32 s11, 0x20000\n" \
"v_mfma_f32_16x16x32_bf16 a[16:19], a[160:163], a[148:151], a[16:19]\n" \
"ds_read_b64_tr_b16 a[184:185], v38 offset:0\n" \
"s_or_b32 s12, s4, 0xc000\n" \
"v_mfma_f32_16x16x32_bf16 a[16:19], a[224:227], a[212:215], a[16:19]\n" \
"ds_read_b64_tr_b16 a[186:187], v38 offset:1024\n" \
"s_lshl_b32 s4, s3, 8\n" \
"v_mfma_f32_16x16x32_bf16 a[32:35], a[160:163], a[152:155], a[32:35]\n" \
"s_addk_i32 s84, 0x2000\n" \
"v_mfma_f32_16x16x32_bf16 a[32:35], a[224:227], a[216:219], a[32:35]\n" \
"buffer_load_dwordx4 v24, s[76:79], s84 offen offset:2048 lds\n" \
"v_mfma_f32_16x16x32_bf16 a[36:39], a[164:167], a[152:155], a[36:39]\n" \
"ds_read_b64_tr_b16 a[248:249], v38 offset:8192\n" \
"s_or_b32 s22, s15, s16\n" \
"v_mfma_f32_16x16x32_bf16 a[36:39], a[228:231], a[216:219], a[36:39]\n" \
"ds_read_b64_tr_b16 a[250:251], v38 offset:9216\n" \
"s_or_b32 s23, s19, s15\n" \
"v_mfma_f32_16x16x32_bf16 a[44:47], a[172:175], a[152:155], a[44:47]\n" \
"ds_read_b64_tr_b16 a[188:189], v39 offset:0\n" \
"s_lshl_b32 s60, s6, 10\n" \
"v_mfma_f32_16x16x32_bf16 a[44:47], a[236:239], a[216:219], a[44:47]\n" \
"ds_read_b64_tr_b16 a[190:191], v39 offset:1024\n" \
"s_lshl_b32 s61, s5, 10\n" \
"v_mfma_f32_16x16x32_bf16 a[40:43], a[152:155], a[168:171], a[40:43]\n" \
"s_add_u32 s89, s42, 0x2000\n" \
"buffer_store_dwordx4 v[200:203], v20, s[80:83], s89 offen offset:0 sc1\n" \
"v_mfma_f32_16x16x32_bf16 a[40:43], a[216:219], a[232:235], a[40:43]\n" \
"ds_read_b64_tr_b16 a[252:253], v39 offset:8192\n" \
"s_lshl_b32 s14, s14, 15\n" \
"v_mfma_f32_16x16x32_bf16 a[56:59], a[156:159], a[168:171], a[56:59]\n" \
"ds_read_b64_tr_b16 a[254:255], v39 offset:9216\n" \
"s_or_b32 s20, s4, 0x26c00\n" \
"v_mfma_f32_16x16x32_bf16 a[56:59], a[220:223], a[232:235], a[56:59]\n" \
"s_or_b32 s13, s22, 0x8000\n" \
"s_add_i32 s22, s22, 0x14000\n" \
"v_mfma_f32_16x16x32_bf16 a[60:63], a[172:175], a[156:159], a[60:63]\n" \
"s_bitset1_b32 s23, 16\n" \
"s_or_b32 s24, s7, 0x5be8020\n" \
"v_mfma_f32_16x16x32_bf16 a[60:63], a[236:239], a[220:223], a[60:63]\n" \
"s_addk_i32 s84, 0x2000\n" \
"v_mfma_f32_16x16x32_bf16 a[52:55], a[164:167], a[156:159], a[52:55]\n" \
".fill (L_pb + 73440 - .) / 4, 4, 0xBF800000\n" \
"L_pp18:\n" \
"s_cbranch_execz L_pret\n" \
"buffer_load_dwordx4 v25, s[76:79], s84 offen offset:3072 lds\n" \
"v_mfma_f32_16x16x32_bf16 a[52:55], a[228:231], a[220:223], a[52:55]\n" \
"s_and_b32 s57, s27, 0xffff\n" \
"s_mov_b32 s58, 0x6000000\n" \
"v_mfma_f32_16x16x32_bf16 a[48:51], a[160:163], a[156:159], a[48:51]\n" \
"s_mov_b32 s56, s26\n" \
"s_mov_b32 s59, s11\n" \
"v_mfma_f32_16x16x32_bf16 a[48:51], a[224:227], a[220:223], a[48:51]\n" \
"s_add_i32 s60, s60, 0x3000000\n" \
"s_add_i32 s61, s61, 0x411c000\n" \
"v_mfma_f32_16x16x32_bf16 a[64:67], a[164:167], a[140:143], a[64:67]\n" \
"s_sub_i32 s19, 14, s18\n" \
"s_mov_b32 s21, 0x71800000\n" \
"v_mfma_f32_16x16x32_bf16 a[64:67], a[228:231], a[204:207], a[64:67]\n" \
"s_mov_b32 s4, L_gsw-L_gtr\n" \
"v_mfma_f32_16x16x32_bf16 a[80:83], a[172:175], a[140:143], a[80:83]\n" \
"s_cmp_eq_u32 s19, 0\n" \
"s_cselect_b32 s4, L_gs7-L_gtr, s4\n" \
"v_mfma_f32_16x16x32_bf16 a[80:83], a[236:239], a[204:207], a[80:83]\n" \
"s_cmp_eq_u32 s16, 0\n" \
"s_cselect_b32 s4, 0, s4\n" \
"v_mfma_f32_16x16x32_bf16 a[68:71], a[140:143], a[168:171], a[68:71]\n" \
"s_getpc_b64 s[28:29]\n" \
"L_gp1_%=:\n" \
"s_add_u32 s28, s28, L_gtr-L_gp1_%=\n" \
"s_addc_u32 s29, s29, 0\n" \
"s_waitcnt vmcnt(11) lgkmcnt(0)\n" \
"v_mfma_f32_16x16x32_bf16 a[68:71], a[204:207], a[232:235], a[68:71]\n" \
"s_barrier\n" \
"v_mfma_f32_16x16x32_bf16 v[116:119], a[176:179], a[144:147], v[116:119]\n" \
"s_add_u32 m0, s40, 0x0\n" \
"s_add_u32 s85, s39, 0x10000\n" \
"v_mfma_f32_16x16x32_bf16 v[116:119], a[240:243], a[208:211], v[116:119]\n" \
"buffer_load_dwordx4 v20, s[80:83], s85 offen sc1 lds\n" \
"v_mfma_f32_16x16x32_bf16 v[120:123], a[180:183], a[144:147], v[120:123]\n" \
"ds_read_b64_tr_b16 a[160:161], v32 offset:32768\n" \
"v_mfma_f32_16x16x32_bf16 v[120:123], a[244:247], a[208:211], v[120:123]\n" \
"ds_read_b64_tr_b16 a[162:163], v32 offset:33280\n" \
"v_mfma_f32_16x16x32_bf16 v[124:127], a[184:187], a[144:147], v[124:127]\n" \
"s_add_u32 s28, s28, s4\n" \
"s_addc_u32 s29, s29, 0\n" \
"v_mfma_f32_16x16x32_bf16 v[124:127], a[248:251], a[208:211], v[124:127]\n" \
"v_mfma_f32_16x16x32_bf16 v[128:131], a[188:191], a[144:147], v[128:131]\n" \
"ds_read_b64_tr_b16 a[224:225], v32 offset:36864\n" \
"v_mfma_f32_16x16x32_bf16 v[128:131], a[252:255], a[208:211], v[128:131]\n" \
"ds_read_b128 a[144:147], v28 offset:40960\n" \
"v_mfma_f32_16x16x32_bf16 v[144:147], a[188:191], a[148:151], v[144:147]\n" \
"ds_read_b128 a[208:211], v29 offset:40960\n" \
"v_mfma_f32_16x16x32_bf16 v[144:147], a[252:255], a[212:215], v[144:147]\n" \
"s_add_u32 m0, s40, 0x1000\n" \
"s_add_u32 s85, s39, 0x11000\n" \
"v_mfma_f32_16x16x32_bf16 v[140:143], a[184:187], a[148:151], v[140:143]\n" \
"buffer_load_dwordx4 v20, s[80:83], s85 offen sc1 lds\n" \
"v_mfma_f32_16x16x32_bf16 v[140:143], a[248:251], a[212:215], v[140:143]\n" \
"ds_read_b64_tr_b16 a[226:227], v32 offset:37376\n" \
"v_mfma_f32_16x16x32_bf16 v[136:139], a[180:183], a[148:151], v[136:139]\n" \
"v_mfma_f32_16x16x32_bf16 v[136:139], a[244:247], a[212:215], v[136:139]\n" \
"ds_read_b64_tr_b16 a[164:165], v33 offset:32768\n" \
"v_mfma_f32_16x16x32_bf16 v[132:135], a[176:179], a[148:151], v[132:135]\n" \
"v_mfma_f32_16x16x32_bf16 v[132:135], a[240:243], a[212:215], v[132:135]\n" \
"ds_read_b128 a[148:151], v28 offset:43008\n" \
"v_mfma_f32_16x16x32_bf16 v[148:151], a[176:179], a[152:155], v[148:151]\n" \
"ds_read_b128 a[212:215], v29 offset:43008\n" \
"v_mfma_f32_16x16x32_bf16 v[148:151], a[240:243], a[216:219], v[148:151]\n" \
"s_add_u32 m0, s40, 0x3000\n" \
"s_movk_i32 s84, 0x400\n" \
"v_mfma_f32_16x16x32_bf16 v[152:155], a[180:183], a[152:155], v[152:155]\n" \
"buffer_load_dwordx4 v21, s[76:79], s84 offen offset:0 lds\n" \
"v_mfma_f32_16x16x32_bf16 v[152:155], a[244:247], a[216:219], v[152:155]\n" \
"ds_read_b64_tr_b16 a[166:167], v33 offset:33280\n" \
"v_mfma_f32_16x16x32_bf16 v[156:159], a[184:187], a[152:155], v[156:159]\n" \
"ds_read_b64_tr_b16 a[228:229], v33 offset:36864\n" \
"v_mfma_f32_16x16x32_bf16 v[156:159], a[248:251], a[216:219], v[156:159]\n" \
"s_add_u32 m0, s31, 0x0\n" \
"s_mov_b32 s84, 0x400000\n" \
"v_mfma_f32_16x16x32_bf16 v[160:163], a[188:191], a[152:155], v[160:163]\n" \
"buffer_load_dwordx4 v26, s[68:71], s84 offen offset:0 lds\n" \
"v_mfma_f32_16x16x32_bf16 v[160:163], a[252:255], a[216:219], v[160:163]\n" \
"ds_read_b128 a[152:155], v28 offset:45056\n" \
"v_mfma_f32_16x16x32_bf16 v[176:179], a[188:191], a[156:159], v[176:179]\n" \
"ds_read_b128 a[216:219], v29 offset:45056\n" \
"v_mfma_f32_16x16x32_bf16 v[176:179], a[252:255], a[220:223], v[176:179]\n" \
"ds_read_b64_tr_b16 a[230:231], v33 offset:37376\n" \
"v_mfma_f32_16x16x32_bf16 v[172:175], a[184:187], a[156:159], v[172:175]\n" \
"ds_read_b64_tr_b16 a[172:173], v35 offset:32768\n" \
"v_mfma_f32_16x16x32_bf16 v[172:175], a[248:251], a[220:223], v[172:175]\n" \
"v_mfma_f32_16x16x32_bf16 v[168:171], a[180:183], a[156:159], v[168:171]\n" \
"s_add_u32 s88, s42, 0x2000\n" \
"buffer_store_dwordx4 v[204:207], v20, s[80:83], s88 offen offset:1024 sc1\n" \
"v_mfma_f32_16x16x32_bf16 v[168:171], a[244:247], a[220:223], v[168:171]\n" \
"ds_read_b64_tr_b16 a[174:175], v35 offset:33280\n" \
"v_mfma_f32_16x16x32_bf16 v[164:167], a[176:179], a[156:159], v[164:167]\n" \
"v_mfma_f32_16x16x32_bf16 v[164:167], a[240:243], a[220:223], v[164:167]\n" \
"ds_read_b128 a[156:159], v28 offset:47104\n" \
"v_mfma_f32_16x16x32_bf16 v[84:87], a[176:179], a[136:139], v[84:87]\n" \
"ds_read_b128 a[220:223], v29 offset:47104\n" \
"v_mfma_f32_16x16x32_bf16 v[84:87], a[240:243], a[200:203], v[84:87]\n" \
"ds_read_b64_tr_b16 a[236:237], v35 offset:36864\n" \
"v_mfma_f32_16x16x32_bf16 v[88:91], a[180:183], a[136:139], v[88:91]\n" \
"v_mfma_f32_16x16x32_bf16 v[88:91], a[244:247], a[200:203], v[88:91]\n" \
"s_addk_i32 s84, 0x7c00\n" \
"s_nop 0\n" \
"v_mfma_f32_16x16x32_bf16 v[92:95], a[184:187], a[136:139], v[92:95]\n" \
"buffer_load_dwordx4 v26, s[68:71], s84 offen offset:1024 lds\n" \
"v_mfma_f32_16x16x32_bf16 v[92:95], a[248:251], a[200:203], v[92:95]\n" \
"ds_read_b64_tr_b16 a[238:239], v35 offset:37376\n" \
"v_mfma_f32_16x16x32_bf16 v[96:99], a[188:191], a[136:139], v[96:99]\n" \
"v_mfma_f32_16x16x32_bf16 v[96:99], a[252:255], a[200:203], v[96:99]\n" \
"ds_read_b128 a[136:139], v28 offset:36864\n" \
"v_mfma_f32_16x16x32_bf16 v[112:115], a[188:191], a[140:143], v[112:115]\n" \
"ds_read_b128 a[200:203], v29 offset:36864\n" \
"v_mfma_f32_16x16x32_bf16 v[112:115], a[252:255], a[204:207], v[112:115]\n" \
"ds_read_b64_tr_b16 a[168:169], v34 offset:32768\n" \
"v_mfma_f32_16x16x32_bf16 v[108:111], a[184:187], a[140:143], v[108:111]\n" \
"s_addk_i32 s84, 0x7c00\n" \
"s_nop 0\n" \
"v_mfma_f32_16x16x32_bf16 v[108:111], a[248:251], a[204:207], v[108:111]\n" \
"buffer_load_dwordx4 v27, s[68:71], s84 offen offset:2048 lds\n" \
"v_mfma_f32_16x16x32_bf16 v[104:107], a[180:183], a[140:143], v[104:107]\n" \
"ds_read_b64_tr_b16 a[170:171], v34 offset:33280\n" \
"v_mfma_f32_16x16x32_bf16 v[104:107], a[244:247], a[204:207], v[104:107]\n" \
"ds_read_b64_tr_b16 a[232:233], v34 offset:36864\n" \
"v_mfma_f32_16x16x32_bf16 v[100:103], a[176:179], a[140:143], v[100:103]\n" \
"v_mfma_f32_16x16x32_bf16 v[100:103], a[240:243], a[204:207], v[100:103]\n" \
"ds_read_b128 a[140:143], v28 offset:38912\n" \
"v_mfma_f32_16x16x32_bf16 v[52:55], a[176:179], a[128:131], v[52:55]\n" \
"ds_read_b128 a[204:207], v29 offset:38912\n" \
"v_mfma_f32_16x16x32_bf16 v[52:55], a[240:243], a[192:195], v[52:55]\n" \
"s_addk_i32 s84, 0x7c00\n" \
"s_nop 0\n" \
"v_mfma_f32_16x16x32_bf16 v[56:59], a[180:183], a[128:131], v[56:59]\n" \
"buffer_load_dwordx4 v27, s[68:71], s84 offen offset:3072 lds\n" \
"v_mfma_f32_16x16x32_bf16 v[56:59], a[244:247], a[192:195], v[56:59]\n" \
"ds_read_b64_tr_b16 a[234:235], v34 offset:37376\n" \
"v_mfma_f32_16x16x32_bf16 v[60:63], a[184:187], a[128:131], v[60:63]\n" \
"v_mfma_f32_16x16x32_bf16 v[60:63], a[248:251], a[192:195], v[60:63]\n" \
"v_mfma_f32_16x16x32_bf16 v[64:67], a[188:191], a[128:131], v[64:67]\n" \
"v_mfma_f32_16x16x32_bf16 v[64:67], a[252:255], a[192:195], v[64:67]\n" \
"ds_read_b128 a[128:131], v28 offset:32768\n" \
"v_mfma_f32_16x16x32_bf16 v[80:83], a[188:191], a[132:135], v[80:83]\n" \
"ds_read_b128 a[192:195], v29 offset:32768\n" \
"v_mfma_f32_16x16x32_bf16 v[80:83], a[252:255], a[196:199], v[80:83]\n" \
"v_mfma_f32_16x16x32_bf16 v[76:79], a[184:187], a[132:135], v[76:79]\n" \
"v_mfma_f32_16x16x32_bf16 v[76:79], a[248:251], a[196:199], v[76:79]\n" \
"v_mfma_f32_16x16x32_bf16 v[72:75], a[180:183], a[132:135], v[72:75]\n" \
"v_mfma_f32_16x16x32_bf16 v[72:75], a[244:247], a[196:199], v[72:75]\n" \
"v_mfma_f32_16x16x32_bf16 v[68:71], a[176:179], a[132:135], v[68:71]\n" \
"v_mfma_f32_16x16x32_bf16 v[68:71], a[240:243], a[196:199], v[68:71]\n" \
"ds_read_b128 a[132:135], v28 offset:34816\n" \
"ds_read_b128 a[196:199], v29 offset:34816\n" \
"s_waitcnt lgkmcnt(15)\n" \
"s_nop 0\n" \
"v_mfma_f32_16x16x32_bf16 a[0:3], a[160:163], a[144:147], a[0:3]\n" \
"s_add_u32 s89, s42, 0x2000\n" \
"buffer_store_dwordx4 v[208:211], v20, s[80:83], s89 offen offset:2048 sc1\n" \
"v_mfma_f32_16x16x32_bf16 a[0:3], a[224:227], a[208:211], a[0:3]\n" \
"ds_read_b64_tr_b16 a[176:177], v36 offset:32768\n" \
"v_mfma_f32_16x16x32_bf16 a[4:7], a[164:167], a[144:147], a[4:7]\n" \
"ds_read_b64_tr_b16 a[178:179], v36 offset:33792\n" \
"v_mfma_f32_16x16x32_bf16 a[4:7], a[228:231], a[208:211], a[4:7]\n" \
"ds_read_b64_tr_b16 a[240:241], v36 offset:40960\n" \
"v_mfma_f32_16x16x32_bf16 a[12:15], a[172:175], a[144:147], a[12:15]\n" \
"ds_read_b64_tr_b16 a[242:243], v36 offset:41984\n" \
"s_waitcnt lgkmcnt(15)\n" \
"v_mfma_f32_16x16x32_bf16 a[12:15], a[236:239], a[208:211], a[12:15]\n" \
"s_waitcnt lgkmcnt(12)\n" \
"v_mfma_f32_16x16x32_bf16 a[8:11], a[144:147], a[168:171], a[8:11]\n" \
"ds_read_b64_tr_b16 a[180:181], v37 offset:32768\n" \
"s_waitcnt lgkmcnt(9)\n" \
"s_nop 0\n" \
"v_mfma_f32_16x16x32_bf16 a[8:11], a[208:211], a[232:235], a[8:11]\n" \
"v_mfma_f32_16x16x32_bf16 a[24:27], a[148:151], a[168:171], a[24:27]\n" \
"ds_read_b64_tr_b16 a[182:183], v37 offset:33792\n" \
"v_mfma_f32_16x16x32_bf16 a[24:27], a[212:215], a[232:235], a[24:27]\n" \
"ds_read_b64_tr_b16 a[244:245], v37 offset:40960\n" \
"v_mfma_f32_16x16x32_bf16 a[28:31], a[172:175], a[148:151], a[28:31]\n" \
"v_mfma_f32_16x16x32_bf16 a[28:31], a[236:239], a[212:215], a[28:31]\n" \
"ds_read_b64_tr_b16 a[246:247], v37 offset:41984\n" \
"v_mfma_f32_16x16x32_bf16 a[20:23], a[164:167], a[148:151], a[20:23]\n" \
"v_mfma_f32_16x16x32_bf16 a[20:23], a[228:231], a[212:215], a[20:23]\n" \
"ds_read_b64_tr_b16 a[184:185], v38 offset:32768\n" \
"v_mfma_f32_16x16x32_bf16 a[16:19], a[160:163], a[148:151], a[16:19]\n" \
"ds_read_b64_tr_b16 a[186:187], v38 offset:33792\n" \
"v_mfma_f32_16x16x32_bf16 a[16:19], a[224:227], a[212:215], a[16:19]\n" \
"v_mfma_f32_16x16x32_bf16 a[32:35], a[160:163], a[152:155], a[32:35]\n" \
"s_add_u32 s88, s42, 0x2000\n" \
"buffer_store_dwordx4 v[212:215], v20, s[80:83], s88 offen offset:3072 sc1\n" \
"v_mfma_f32_16x16x32_bf16 a[32:35], a[224:227], a[216:219], a[32:35]\n" \
"ds_read_b64_tr_b16 a[248:249], v38 offset:40960\n" \
"v_mfma_f32_16x16x32_bf16 a[36:39], a[164:167], a[152:155], a[36:39]\n" \
"v_lshlrev_b32_e32 v45, 3, v20\n" \
"v_add_u32_e32 v45, s30, v45\n" \
"v_mfma_f32_16x16x32_bf16 a[36:39], a[228:231], a[216:219], a[36:39]\n" \
"s_getpc_b64 s[86:87]\n" \
"L_ct1_%=:\n" \
"s_add_u32 s86, s86, L_ap0-L_ct1_%=\n" \
"s_addc_u32 s87, s87, 0\n" \
"s_add_u32 m0, s40, 0x2000\n" \
"v_mfma_f32_16x16x32_bf16 a[44:47], a[172:175], a[152:155], a[44:47]\n" \
"global_load_lds_dword v45, s[86:87]\n" \
"v_mfma_f32_16x16x32_bf16 a[44:47], a[236:239], a[216:219], a[44:47]\n" \
"ds_read_b64_tr_b16 a[250:251], v38 offset:41984\n" \
"v_mfma_f32_16x16x32_bf16 a[40:43], a[152:155], a[168:171], a[40:43]\n" \
"ds_read_b64_tr_b16 a[188:189], v39 offset:32768\n" \
"v_mfma_f32_16x16x32_bf16 a[40:43], a[216:219], a[232:235], a[40:43]\n" \
"ds_read_b64_tr_b16 a[190:191], v39 offset:33792\n" \
"v_mfma_f32_16x16x32_bf16 a[56:59], a[156:159], a[168:171], a[56:59]\n" \
"ds_read_b64_tr_b16 a[252:253], v39 offset:40960\n" \
"v_mfma_f32_16x16x32_bf16 a[56:59], a[220:223], a[232:235], a[56:59]\n" \
"ds_read_b64_tr_b16 a[254:255], v39 offset:41984\n" \
"v_mfma_f32_16x16x32_bf16 a[60:63], a[172:175], a[156:159], a[60:63]\n" \
"v_mfma_f32_16x16x32_bf16 a[60:63], a[236:239], a[220:223], a[60:63]\n" \
"v_mfma_f32_16x16x32_bf16 a[52:55], a[164:167], a[156:159], a[52:55]\n" \
"v_mfma_f32_16x16x32_bf16 a[52:55], a[228:231], a[220:223], a[52:55]\n" \
"v_mfma_f32_16x16x32_bf16 a[48:51], a[160:163], a[156:159], a[48:51]\n" \
"v_mfma_f32_16x16x32_bf16 a[48:51], a[224:227], a[220:223], a[48:51]\n" \
"v_mfma_f32_16x16x32_bf16 a[64:67], a[164:167], a[140:143], a[64:67]\n" \
"v_mfma_f32_16x16x32_bf16 a[64:67], a[228:231], a[204:207], a[64:67]\n" \
"v_mfma_f32_16x16x32_bf16 a[80:83], a[172:175], a[140:143], a[80:83]\n" \
"v_mfma_f32_16x16x32_bf16 a[80:83], a[236:239], a[204:207], a[80:83]\n" \
"v_mfma_f32_16x16x32_bf16 a[68:71], a[140:143], a[168:171], a[68:71]\n" \
"s_waitcnt vmcnt(3) lgkmcnt(0)\n" \
"v_mfma_f32_16x16x32_bf16 a[68:71], a[204:207], a[232:235], a[68:71]\n" \
"s_barrier\n" \
"v_xor_b32_e32 v28, 0x20000, v28\n" \
"v_xor_b32_e32 v29, 0x20000, v29\n" \
"v_mfma_f32_16x16x32_bf16 v[116:119], a[176:179], a[144:147], v[116:119]\n" \
"ds_read_b64_tr_b16 a[160:161], v32 offset:0\n" \
"v_accvgpr_read_b32 v184, a0\n" \
"v_mfma_f32_16x16x32_bf16 v[116:119], a[240:243], a[208:211], v[116:119]\n" \
"v_accvgpr_read_b32 v185, a1\n" \
"v_accvgpr_read_b32 v186, a2\n" \
"v_mfma_f32_16x16x32_bf16 v[120:123], a[180:183], a[144:147], v[120:123]\n" \
"v_accvgpr_read_b32 v187, a3\n" \
"v_accvgpr_read_b32 v188, a4\n" \
"v_mfma_f32_16x16x32_bf16 v[120:123], a[244:247], a[208:211], v[120:123]\n" \
"ds_read_b64_tr_b16 a[162:163], v32 offset:512\n" \
"v_accvgpr_read_b32 v189, a5\n" \
"v_mfma_f32_16x16x32_bf16 v[124:127], a[184:187], a[144:147], v[124:127]\n" \
"v_accvgpr_read_b32 v190, a6\n" \
"v_accvgpr_read_b32 v191, a7\n" \
"v_mfma_f32_16x16x32_bf16 v[124:127], a[248:251], a[208:211], v[124:127]\n" \
"v_accvgpr_read_b32 v196, a12\n" \
"v_accvgpr_read_b32 v197, a13\n" \
"v_mfma_f32_16x16x32_bf16 v[128:131], a[188:191], a[144:147], v[128:131]\n" \
"ds_read_b64_tr_b16 a[224:225], v32 offset:4096\n" \
"v_accvgpr_read_b32 v198, a14\n" \
"v_mfma_f32_16x16x32_bf16 v[128:131], a[252:255], a[208:211], v[128:131]\n" \
"ds_read_b128 a[144:147], v28 offset:8192\n" \
"v_accvgpr_read_b32 v199, a15\n" \
"v_mfma_f32_16x16x32_bf16 v[144:147], a[188:191], a[148:151], v[144:147]\n" \
"ds_read_b128 a[208:211], v29 offset:8192\n" \
"v_accvgpr_read_b32 v192, a8\n" \
"v_mfma_f32_16x16x32_bf16 v[144:147], a[252:255], a[212:215], v[144:147]\n" \
"ds_read_b64_tr_b16 a[226:227], v32 offset:4608\n" \
"v_accvgpr_read_b32 v193, a9\n" \
"v_mfma_f32_16x16x32_bf16 v[140:143], a[184:187], a[148:151], v[140:143]\n" \
"v_accvgpr_read_b32 v194, a10\n" \
"v_accvgpr_read_b32 v195, a11\n" \
"v_mfma_f32_16x16x32_bf16 v[140:143], a[248:251], a[212:215], v[140:143]\n" \
"v_accvgpr_read_b32 v208, a24\n" \
"v_accvgpr_read_b32 v209, a25\n" \
"v_mfma_f32_16x16x32_bf16 v[136:139], a[180:183], a[148:151], v[136:139]\n" \
"v_accvgpr_read_b32 v210, a26\n" \
"v_accvgpr_read_b32 v211, a27\n" \
"v_mfma_f32_16x16x32_bf16 v[136:139], a[244:247], a[212:215], v[136:139]\n" \
"ds_read_b64_tr_b16 a[164:165], v33 offset:0\n" \
"v_accvgpr_read_b32 v212, a28\n" \
"v_mfma_f32_16x16x32_bf16 v[132:135], a[176:179], a[148:151], v[132:135]\n" \
"v_accvgpr_read_b32 v213, a29\n" \
"v_accvgpr_read_b32 v214, a30\n" \
"v_mfma_f32_16x16x32_bf16 v[132:135], a[240:243], a[212:215], v[132:135]\n" \
"ds_read_b128 a[148:151], v28 offset:10240\n" \
"v_accvgpr_read_b32 v215, a31\n" \
"v_mfma_f32_16x16x32_bf16 v[148:151], a[176:179], a[152:155], v[148:151]\n" \
"ds_read_b128 a[212:215], v29 offset:10240\n" \
"v_accvgpr_read_b32 v204, a20\n" \
"v_mfma_f32_16x16x32_bf16 v[148:151], a[240:243], a[216:219], v[148:151]\n" \
"ds_read_b64_tr_b16 a[166:167], v33 offset:512\n" \
"v_accvgpr_read_b32 v205, a21\n" \
"v_mfma_f32_16x16x32_bf16 v[152:155], a[180:183], a[152:155], v[152:155]\n" \
"v_accvgpr_read_b32 v206, a22\n" \
"v_accvgpr_read_b32 v207, a23\n" \
"v_mfma_f32_16x16x32_bf16 v[152:155], a[244:247], a[216:219], v[152:155]\n" \
"ds_read_b64_tr_b16 a[228:229], v33 offset:4096\n" \
"v_accvgpr_read_b32 v200, a16\n" \
"v_mfma_f32_16x16x32_bf16 v[156:159], a[184:187], a[152:155], v[156:159]\n" \
"v_accvgpr_read_b32 v201, a17\n" \
"v_accvgpr_read_b32 v202, a18\n" \
"v_mfma_f32_16x16x32_bf16 v[156:159], a[248:251], a[216:219], v[156:159]\n" \
"v_accvgpr_read_b32 v203, a19\n" \
"v_accvgpr_read_b32 v216, a32\n" \
"v_mfma_f32_16x16x32_bf16 v[160:163], a[188:191], a[152:155], v[160:163]\n" \
"ds_read_b64_tr_b16 a[230:231], v33 offset:4608\n" \
"v_accvgpr_read_b32 v217, a33\n" \
"v_mfma_f32_16x16x32_bf16 v[160:163], a[252:255], a[216:219], v[160:163]\n" \
"ds_read_b128 a[152:155], v28 offset:12288\n" \
"v_accvgpr_read_b32 v218, a34\n" \
"v_mfma_f32_16x16x32_bf16 v[176:179], a[188:191], a[156:159], v[176:179]\n" \
"ds_read_b128 a[216:219], v29 offset:12288\n" \
"v_accvgpr_read_b32 v219, a35\n" \
"v_mfma_f32_16x16x32_bf16 v[176:179], a[252:255], a[220:223], v[176:179]\n" \
"v_accvgpr_read_b32 v220, a36\n" \
"v_accvgpr_read_b32 v221, a37\n" \
"v_mfma_f32_16x16x32_bf16 v[172:175], a[184:187], a[156:159], v[172:175]\n" \
"ds_read_b64_tr_b16 a[172:173], v35 offset:0\n" \
"v_accvgpr_read_b32 v222, a38\n" \
"v_mfma_f32_16x16x32_bf16 v[172:175], a[248:251], a[220:223], v[172:175]\n" \
"v_accvgpr_read_b32 v223, a39\n" \
"v_accvgpr_read_b32 v228, a44\n" \
"v_mfma_f32_16x16x32_bf16 v[168:171], a[180:183], a[156:159], v[168:171]\n" \
"v_accvgpr_read_b32 v229, a45\n" \
"v_accvgpr_read_b32 v230, a46\n" \
"v_mfma_f32_16x16x32_bf16 v[168:171], a[244:247], a[220:223], v[168:171]\n" \
"ds_read_b64_tr_b16 a[174:175], v35 offset:512\n" \
"v_accvgpr_read_b32 v231, a47\n" \
"v_mfma_f32_16x16x32_bf16 v[164:167], a[176:179], a[156:159], v[164:167]\n" \
"v_accvgpr_read_b32 v224, a40\n" \
"v_accvgpr_read_b32 v225, a41\n" \
"v_mfma_f32_16x16x32_bf16 v[164:167], a[240:243], a[220:223], v[164:167]\n" \
"ds_read_b128 a[156:159], v28 offset:14336\n" \
"v_accvgpr_read_b32 v226, a42\n" \
"v_mfma_f32_16x16x32_bf16 v[84:87], a[176:179], a[136:139], v[84:87]\n" \
"ds_read_b128 a[220:223], v29 offset:14336\n" \
"v_accvgpr_read_b32 v227, a43\n" \
"v_mfma_f32_16x16x32_bf16 v[84:87], a[240:243], a[200:203], v[84:87]\n" \
"ds_read_b64_tr_b16 a[236:237], v35 offset:4096\n" \
"v_accvgpr_read_b32 v240, a56\n" \
"v_mfma_f32_16x16x32_bf16 v[88:91], a[180:183], a[136:139], v[88:91]\n" \
"v_accvgpr_read_b32 v241, a57\n" \
"v_accvgpr_read_b32 v242, a58\n" \
"v_mfma_f32_16x16x32_bf16 v[88:91], a[244:247], a[200:203], v[88:91]\n" \
"ds_read_b64_tr_b16 a[238:239], v35 offset:4608\n" \
"v_accvgpr_read_b32 v243, a59\n" \
"v_mfma_f32_16x16x32_bf16 v[92:95], a[184:187], a[136:139], v[92:95]\n" \
"v_accvgpr_read_b32 v244, a60\n" \
"v_accvgpr_read_b32 v245, a61\n" \
"v_mfma_f32_16x16x32_bf16 v[92:95], a[248:251], a[200:203], v[92:95]\n" \
"v_accvgpr_read_b32 v246, a62\n" \
"v_accvgpr_read_b32 v247, a63\n" \
"v_mfma_f32_16x16x32_bf16 v[96:99], a[188:191], a[136:139], v[96:99]\n" \
"v_accvgpr_read_b32 v236, a52\n" \
"v_accvgpr_read_b32 v237, a53\n" \
"v_mfma_f32_16x16x32_bf16 v[96:99], a[252:255], a[200:203], v[96:99]\n" \
"ds_read_b128 a[136:139], v28 offset:4096\n" \
"v_accvgpr_read_b32 v238, a54\n" \
"v_mfma_f32_16x16x32_bf16 v[112:115], a[188:191], a[140:143], v[112:115]\n" \
"ds_read_b128 a[200:203], v29 offset:4096\n" \
"v_accvgpr_read_b32 v239, a55\n" \
"v_mfma_f32_16x16x32_bf16 v[112:115], a[252:255], a[204:207], v[112:115]\n" \
"ds_read_b64_tr_b16 a[168:169], v34 offset:0\n" \
"v_accvgpr_read_b32 v232, a48\n" \
"v_mfma_f32_16x16x32_bf16 v[108:111], a[184:187], a[140:143], v[108:111]\n" \
"ds_read_b64_tr_b16 a[170:171], v34 offset:512\n" \
"v_accvgpr_read_b32 v233, a49\n" \
"v_mfma_f32_16x16x32_bf16 v[108:111], a[248:251], a[204:207], v[108:111]\n" \
"v_accvgpr_read_b32 v234, a50\n" \
"v_accvgpr_read_b32 v235, a51\n" \
"v_mfma_f32_16x16x32_bf16 v[104:107], a[180:183], a[140:143], v[104:107]\n" \
"v_accvgpr_read_b32 v180, a64\n" \
"v_accvgpr_read_b32 v181, a65\n" \
"v_mfma_f32_16x16x32_bf16 v[104:107], a[244:247], a[204:207], v[104:107]\n" \
"ds_read_b64_tr_b16 a[232:233], v34 offset:4096\n" \
"v_accvgpr_read_b32 v182, a66\n" \
"v_mfma_f32_16x16x32_bf16 v[100:103], a[176:179], a[140:143], v[100:103]\n" \
"v_accvgpr_read_b32 v183, a67\n" \
"v_accvgpr_read_b32 v48, a80\n" \
"v_mfma_f32_16x16x32_bf16 v[100:103], a[240:243], a[204:207], v[100:103]\n" \
"ds_read_b128 a[140:143], v28 offset:6144\n" \
"v_accvgpr_read_b32 v49, a81\n" \
"v_mfma_f32_16x16x32_bf16 v[52:55], a[176:179], a[128:131], v[52:55]\n" \
"ds_read_b128 a[204:207], v29 offset:6144\n" \
"v_accvgpr_read_b32 v50, a82\n" \
"v_mfma_f32_16x16x32_bf16 v[52:55], a[240:243], a[192:195], v[52:55]\n" \
"ds_read_b64_tr_b16 a[234:235], v34 offset:4608\n" \
"v_accvgpr_read_b32 v51, a83\n" \
"v_mfma_f32_16x16x32_bf16 v[56:59], a[180:183], a[128:131], v[56:59]\n" \
"v_accvgpr_read_b32 v252, a68\n" \
"v_accvgpr_read_b32 v253, a69\n" \
"v_mfma_f32_16x16x32_bf16 v[56:59], a[244:247], a[192:195], v[56:59]\n" \
"v_accvgpr_read_b32 v254, a70\n" \
"v_accvgpr_read_b32 v255, a71\n" \
"v_mfma_f32_16x16x32_bf16 v[60:63], a[184:187], a[128:131], v[60:63]\n" \
"v_lshrrev_b32_e64 v24, 9, v20\n" \
"v_and_b32_e32 v26, 0x1ff, v20\n" \
"v_mfma_f32_16x16x32_bf16 v[60:63], a[248:251], a[192:195], v[60:63]\n" \
"v_lshl_add_u32 v24, v24, 3, v26\n" \
"v_lshrrev_b32_e64 v25, 8, v20\n" \
"v_mfma_f32_16x16x32_bf16 v[64:67], a[188:191], a[128:131], v[64:67]\n" \
"v_mul_u32_u24_e32 v25, 0x24700, v25\n" \
"v_add_u32_e64 v25, v25, v20\n" \
"v_mfma_f32_16x16x32_bf16 v[64:67], a[252:255], a[192:195], v[64:67]\n" \
"ds_read_b128 a[128:131], v28 offset:0\n" \
"v_lshrrev_b32_e64 v26, 2, v20\n" \
"v_mfma_f32_16x16x32_bf16 v[80:83], a[188:191], a[132:135], v[80:83]\n" \
"ds_read_b128 a[192:195], v29 offset:0\n" \
"v_add_u32_e64 v26, s45, v26\n" \
"v_mfma_f32_16x16x32_bf16 v[80:83], a[252:255], a[196:199], v[80:83]\n" \
"v_add_u32_e32 v27, s46, v20\n" \
"v_add_u32_e32 v21, s43, v27\n" \
"v_mfma_f32_16x16x32_bf16 v[76:79], a[184:187], a[132:135], v[76:79]\n" \
"v_mfma_f32_16x16x32_bf16 v[76:79], a[248:251], a[196:199], v[76:79]\n" \
"v_mfma_f32_16x16x32_bf16 v[72:75], a[180:183], a[132:135], v[72:75]\n" \
"v_mfma_f32_16x16x32_bf16 v[72:75], a[244:247], a[196:199], v[72:75]\n" \
"v_mfma_f32_16x16x32_bf16 v[68:71], a[176:179], a[132:135], v[68:71]\n" \
"v_mfma_f32_16x16x32_bf16 v[68:71], a[240:243], a[196:199], v[68:71]\n" \
"ds_read_b128 a[132:135], v28 offset:2048\n" \
"ds_read_b128 a[196:199], v29 offset:2048\n" \
"s_waitcnt lgkmcnt(15)\n" \
"s_nop 0\n" \
"v_mfma_f32_16x16x32_bf16 v[184:187], a[160:163], a[144:147], v[184:187]\n" \
"v_bfe_u32 v44, v20, 2, 5\n" \
"ds_read_b64_tr_b16 a[176:177], v36 offset:0\n" \
"v_mfma_f32_16x16x32_bf16 v[184:187], a[224:227], a[208:211], v[184:187]\n" \
"buffer_load_dword v44, v44, s[80:83], s38 offen offset:0 sc1\n" \
"v_mfma_f32_16x16x32_bf16 v[188:191], a[164:167], a[144:147], v[188:191]\n" \
"ds_read_b64_tr_b16 a[178:179], v36 offset:1024\n" \
"v_mfma_f32_16x16x32_bf16 v[188:191], a[228:231], a[208:211], v[188:191]\n" \
"ds_read_b64_tr_b16 a[240:241], v36 offset:8192\n" \
"v_mfma_f32_16x16x32_bf16 v[196:199], a[172:175], a[144:147], v[196:199]\n" \
"ds_read_b64_tr_b16 a[242:243], v36 offset:9216\n" \
"s_waitcnt lgkmcnt(15)\n" \
"v_mfma_f32_16x16x32_bf16 v[196:199], a[236:239], a[208:211], v[196:199]\n" \
"s_waitcnt lgkmcnt(12)\n" \
"v_mfma_f32_16x16x32_bf16 v[192:195], a[144:147], a[168:171], v[192:195]\n" \
"ds_read_b64_tr_b16 a[180:181], v37 offset:0\n" \
"s_waitcnt lgkmcnt(9)\n" \
"s_nop 0\n" \
"v_mfma_f32_16x16x32_bf16 v[192:195], a[208:211], a[232:235], v[192:195]\n" \
"v_mfma_f32_16x16x32_bf16 v[208:211], a[148:151], a[168:171], v[208:211]\n" \
"ds_read_b64_tr_b16 a[182:183], v37 offset:1024\n" \
"v_mfma_f32_16x16x32_bf16 v[208:211], a[212:215], a[232:235], v[208:211]\n" \
"ds_read_b64_tr_b16 a[244:245], v37 offset:8192\n" \
"v_mfma_f32_16x16x32_bf16 v[212:215], a[172:175], a[148:151], v[212:215]\n" \
"v_mfma_f32_16x16x32_bf16 v[212:215], a[236:239], a[212:215], v[212:215]\n" \
"ds_read_b64_tr_b16 a[246:247], v37 offset:9216\n" \
"v_cvt_pk_bf16_f32 v188, v188, v189\n" \
"v_mfma_f32_16x16x32_bf16 v[204:207], a[164:167], a[148:151], v[204:207]\n" \
"v_cvt_pk_bf16_f32 v189, v190, v191\n" \
"v_cvt_pk_bf16_f32 v190, v198, v199\n" \
"v_mfma_f32_16x16x32_bf16 v[204:207], a[228:231], a[212:215], v[204:207]\n" \
"ds_read_b64_tr_b16 a[184:185], v38 offset:0\n" \
"v_mov_b32_e32 v191, 0x3f80\n" \
"v_mfma_f32_16x16x32_bf16 v[200:203], a[160:163], a[148:151], v[200:203]\n" \
"ds_read_b64_tr_b16 a[186:187], v38 offset:1024\n" \
"v_cvt_pk_bf16_f32 v184, v184, v185\n" \
"v_mfma_f32_16x16x32_bf16 v[200:203], a[224:227], a[212:215], v[200:203]\n" \
"v_cvt_pk_bf16_f32 v185, v186, v187\n" \
"v_cvt_pk_bf16_f32 v186, v196, v197\n" \
"v_mfma_f32_16x16x32_bf16 v[216:219], a[160:163], a[152:155], v[216:219]\n" \
"ds_read_b64_tr_b16 a[248:249], v38 offset:8192\n" \
"v_mov_b32_e64 v187, 0\n" \
"v_mfma_f32_16x16x32_bf16 v[216:219], a[224:227], a[216:219], v[216:219]\n" \
"ds_read_b64_tr_b16 a[250:251], v38 offset:9216\n" \
"v_mfma_f32_16x16x32_bf16 v[220:223], a[164:167], a[152:155], v[220:223]\n" \
"v_mfma_f32_16x16x32_bf16 v[220:223], a[228:231], a[216:219], v[220:223]\n" \
"ds_read_b64_tr_b16 a[188:189], v39 offset:0\n" \
"v_cvt_pk_bf16_f32 v204, v204, v205\n" \
"v_mfma_f32_16x16x32_bf16 v[228:231], a[172:175], a[152:155], v[228:231]\n" \
"v_cvt_pk_bf16_f32 v205, v206, v207\n" \
"v_cvt_pk_bf16_f32 v206, v214, v215\n" \
"v_mfma_f32_16x16x32_bf16 v[228:231], a[236:239], a[216:219], v[228:231]\n" \
"ds_read_b64_tr_b16 a[190:191], v39 offset:1024\n" \
"v_mov_b32_e32 v207, 0x3f80\n" \
"v_mfma_f32_16x16x32_bf16 v[224:227], a[152:155], a[168:171], v[224:227]\n" \
"ds_read_b64_tr_b16 a[252:253], v39 offset:8192\n" \
"v_cvt_pk_bf16_f32 v200, v200, v201\n" \
"v_mfma_f32_16x16x32_bf16 v[224:227], a[216:219], a[232:235], v[224:227]\n" \
"v_cvt_pk_bf16_f32 v201, v202, v203\n" \
"v_cvt_pk_bf16_f32 v202, v212, v213\n" \
"v_mfma_f32_16x16x32_bf16 v[240:243], a[156:159], a[168:171], v[240:243]\n" \
"ds_read_b64_tr_b16 a[254:255], v39 offset:9216\n" \
"v_mov_b32_e64 v203, 0\n" \
"v_mfma_f32_16x16x32_bf16 v[240:243], a[220:223], a[232:235], v[240:243]\n" \
"v_mfma_f32_16x16x32_bf16 v[244:247], a[172:175], a[156:159], v[244:247]\n" \
"v_mfma_f32_16x16x32_bf16 v[244:247], a[236:239], a[220:223], v[244:247]\n" \
"v_cvt_pk_bf16_f32 v220, v220, v221\n" \
"v_cvt_pk_bf16_f32 v221, v222, v223\n" \
"v_mfma_f32_16x16x32_bf16 v[236:239], a[164:167], a[156:159], v[236:239]\n" \
"v_cvt_pk_bf16_f32 v222, v230, v231\n" \
"v_mov_b32_e32 v223, 0x3f80\n" \
"v_mfma_f32_16x16x32_bf16 v[236:239], a[228:231], a[220:223], v[236:239]\n" \
"v_cvt_pk_bf16_f32 v216, v216, v217\n" \
"v_cvt_pk_bf16_f32 v217, v218, v219\n" \
"v_mfma_f32_16x16x32_bf16 v[232:235], a[160:163], a[156:159], v[232:235]\n" \
"v_cvt_pk_bf16_f32 v218, v228, v229\n" \
"v_mov_b32_e64 v219, 0\n" \
"v_mfma_f32_16x16x32_bf16 v[232:235], a[224:227], a[220:223], v[232:235]\n" \
"v_mfma_f32_16x16x32_bf16 v[180:183], a[164:167], a[140:143], v[180:183]\n" \
"v_mfma_f32_16x16x32_bf16 v[180:183], a[228:231], a[204:207], v[180:183]\n" \
"v_mfma_f32_16x16x32_bf16 v[48:51], a[172:175], a[140:143], v[48:51]\n" \
"v_mfma_f32_16x16x32_bf16 v[48:51], a[236:239], a[204:207], v[48:51]\n" \
"v_cvt_pk_bf16_f32 v236, v236, v237\n" \
"v_cvt_pk_bf16_f32 v237, v238, v239\n" \
"v_mfma_f32_16x16x32_bf16 v[252:255], a[140:143], a[168:171], v[252:255]\n" \
"v_cvt_pk_bf16_f32 v238, v246, v247\n" \
"v_mov_b32_e32 v239, 0x3f80\n" \
"v_mfma_f32_16x16x32_bf16 v[252:255], a[204:207], a[232:235], v[252:255]\n" \
"v_cvt_pk_bf16_f32 v232, v232, v233\n" \
"v_cvt_pk_bf16_f32 v233, v234, v235\n" \
"s_waitcnt vmcnt(0) lgkmcnt(0)\n" \
"s_barrier\n" \
"v_mov_b32_e32 v45, s62\n" \
"s_mov_b64 exec, 1\n" \
"buffer_store_dword v45, off, s[80:83], s37 offset:32 sc1\n" \
"s_mov_b64 exec, -1\n" \
"v_cmp_ne_u32_e32 vcc, s62, v44\n" \
"s_nop 4\n" \
"s_cbranch_vccnz L_poll161_%=\n" \
"L_pollr161_%=:\n" \
"v_mfma_f32_16x16x32_bf16 v[116:119], a[176:179], a[144:147], v[116:119]\n" \
"s_mov_b32 s73, s50\n" \
"s_sub_u32 m0, s31, 0x10000\n" \
"v_mfma_f32_16x16x32_bf16 v[116:119], a[240:243], a[208:211], v[116:119]\n" \
"buffer_load_dwordx4 v20, s[80:83], s73 offen offset:0 sc1 lds\n" \
"v_mfma_f32_16x16x32_bf16 v[120:123], a[180:183], a[144:147], v[120:123]\n" \
"s_add_u32 s74, s50, 0x4000\n" \
"s_add_u32 s75, s50, 0x8000\n" \
"v_mfma_f32_16x16x32_bf16 v[120:123], a[244:247], a[208:211], v[120:123]\n" \
"s_mov_b32 s70, 0x3e0293ee\n" \
"s_mov_b32 s76, s52\n" \
"v_mfma_f32_16x16x32_bf16 v[124:127], a[184:187], a[144:147], v[124:127]\n" \
"v_mul_f32_e32 v116, s70, v116\n" \
"v_mul_f32_e32 v117, s70, v117\n" \
"v_mfma_f32_16x16x32_bf16 v[124:127], a[248:251], a[208:211], v[124:127]\n" \
"buffer_load_dwordx4 v20, s[80:83], s73 offen offset:1024 sc1 lds\n" \
"v_mfma_f32_16x16x32_bf16 v[128:131], a[188:191], a[144:147], v[128:131]\n" \
"v_mul_f32_e32 v118, s70, v118\n" \
"v_mul_f32_e32 v119, s70, v119\n" \
"v_mfma_f32_16x16x32_bf16 v[128:131], a[252:255], a[208:211], v[128:131]\n" \
"v_cvt_pk_bf16_f32 v116, v116, v117\n" \
"v_mul_f32_e64 v120, s70, v120\n" \
"v_mfma_f32_16x16x32_bf16 v[144:147], a[188:191], a[148:151], v[144:147]\n" \
"v_mul_f32_e32 v124, s70, v124\n" \
"v_mul_f32_e32 v125, s70, v125\n" \
"v_mfma_f32_16x16x32_bf16 v[144:147], a[252:255], a[212:215], v[144:147]\n" \
"buffer_load_dwordx4 v20, s[80:83], s73 offen offset:2048 sc1 lds\n" \
"v_mfma_f32_16x16x32_bf16 v[140:143], a[184:187], a[148:151], v[140:143]\n" \
"v_mul_f32_e32 v126, s70, v126\n" \
"v_mul_f32_e32 v127, s70, v127\n" \
"v_mfma_f32_16x16x32_bf16 v[140:143], a[248:251], a[212:215], v[140:143]\n" \
"v_cvt_pk_bf16_f32 v124, v124, v125\n" \
"v_mul_f32_e64 v121, s70, v121\n" \
"v_mfma_f32_16x16x32_bf16 v[136:139], a[180:183], a[148:151], v[136:139]\n" \
".fill (L_pb + 77536 - .) / 4, 4, 0xBF800000\n" \
"L_pp19:\n" \
"s_cbranch_execz L_pret\n" \
"v_mul_f32_e32 v122, s70, v122\n" \
"v_mul_f32_e64 v123, s70, v123\n" \
"v_mfma_f32_16x16x32_bf16 v[136:139], a[244:247], a[212:215], v[136:139]\n" \
"buffer_load_dwordx4 v20, s[80:83], s73 offen offset:3072 sc1 lds\n" \
"v_mfma_f32_16x16x32_bf16 v[132:135], a[176:179], a[148:151], v[132:135]\n" \
"s_sub_u32 m0, s31, s51\n" \
"v_cvt_pk_bf16_f32 v117, v118, v119\n" \
"v_mfma_f32_16x16x32_bf16 v[132:135], a[240:243], a[212:215], v[132:135]\n" \
"v_mul_f32_e32 v128, s70, v128\n" \
"v_mul_f32_e64 v129, s70, v129\n" \
"v_mfma_f32_16x16x32_bf16 v[148:151], a[176:179], a[152:155], v[148:151]\n" \
"v_mul_f32_e32 v130, s70, v130\n" \
"v_mul_f32_e32 v131, s70, v131\n" \
"v_mfma_f32_16x16x32_bf16 v[148:151], a[240:243], a[216:219], v[148:151]\n" \
"buffer_load_dwordx4 v20, s[80:83], s74 offen offset:0 sc1 lds\n" \
"v_mfma_f32_16x16x32_bf16 v[152:155], a[180:183], a[152:155], v[152:155]\n" \
"v_cvt_pk_bf16_f32 v125, v126, v127\n" \
"v_cvt_pk_bf16_f32 v118, v120, v121\n" \
"v_mfma_f32_16x16x32_bf16 v[152:155], a[244:247], a[216:219], v[152:155]\n" \
"v_cvt_pk_bf16_f32 v119, v122, v123\n" \
"v_cvt_pk_bf16_f32 v126, v128, v129\n" \
"v_mfma_f32_16x16x32_bf16 v[156:159], a[184:187], a[152:155], v[156:159]\n" \
"v_cvt_pk_bf16_f32 v127, v130, v131\n" \
"v_mul_f32_e64 v132, s70, v132\n" \
"v_mfma_f32_16x16x32_bf16 v[156:159], a[248:251], a[216:219], v[156:159]\n" \
"buffer_load_dwordx4 v20, s[80:83], s74 offen offset:1024 sc1 lds\n" \
"v_mfma_f32_16x16x32_bf16 v[160:163], a[188:191], a[152:155], v[160:163]\n" \
"ds_write_b128 v21, v[116:119] offset:16384\n" \
"v_mfma_f32_16x16x32_bf16 v[160:163], a[252:255], a[216:219], v[160:163]\n" \
"v_mul_f32_e32 v133, s70, v133\n" \
"v_mul_f32_e32 v140, s70, v140\n" \
"v_mfma_f32_16x16x32_bf16 v[176:179], a[188:191], a[156:159], v[176:179]\n" \
"v_mul_f32_e32 v141, s70, v141\n" \
"v_mul_f32_e32 v134, s70, v134\n" \
"v_mfma_f32_16x16x32_bf16 v[176:179], a[252:255], a[220:223], v[176:179]\n" \
"buffer_load_dwordx4 v20, s[80:83], s74 offen offset:2048 sc1 lds\n" \
"v_mfma_f32_16x16x32_bf16 v[172:175], a[184:187], a[156:159], v[172:175]\n" \
"ds_write_b128 v21, v[124:127] offset:17408\n" \
"v_mfma_f32_16x16x32_bf16 v[172:175], a[248:251], a[220:223], v[172:175]\n" \
"v_mul_f32_e64 v135, s70, v135\n" \
"v_cvt_pk_bf16_f32 v132, v132, v133\n" \
"v_mfma_f32_16x16x32_bf16 v[168:171], a[180:183], a[156:159], v[168:171]\n" \
"v_mul_f32_e32 v142, s70, v142\n" \
"v_mul_f32_e32 v143, s70, v143\n" \
"v_mfma_f32_16x16x32_bf16 v[168:171], a[244:247], a[220:223], v[168:171]\n" \
"buffer_load_dwordx4 v20, s[80:83], s74 offen offset:3072 sc1 lds\n" \
"v_mfma_f32_16x16x32_bf16 v[164:167], a[176:179], a[156:159], v[164:167]\n" \
"s_sub_u32 m0, s31, 0x4000\n" \
"v_cvt_pk_bf16_f32 v140, v140, v141\n" \
"v_mfma_f32_16x16x32_bf16 v[164:167], a[240:243], a[220:223], v[164:167]\n" \
"v_mul_f32_e32 v136, s70, v136\n" \
"v_mul_f32_e32 v137, s70, v137\n" \
"v_mfma_f32_16x16x32_bf16 v[84:87], a[176:179], a[136:139], v[84:87]\n" \
"v_mul_f32_e32 v138, s70, v138\n" \
"v_mul_f32_e32 v139, s70, v139\n" \
"v_mfma_f32_16x16x32_bf16 v[84:87], a[240:243], a[200:203], v[84:87]\n" \
"buffer_load_dwordx4 v20, s[80:83], s75 offen offset:0 sc1 lds\n" \
"v_mfma_f32_16x16x32_bf16 v[88:91], a[180:183], a[136:139], v[88:91]\n" \
"v_cvt_pk_bf16_f32 v133, v134, v135\n" \
"v_mul_f32_e64 v144, s70, v144\n" \
"v_mfma_f32_16x16x32_bf16 v[88:91], a[244:247], a[200:203], v[88:91]\n" \
"v_mul_f32_e32 v145, s70, v145\n" \
"v_mul_f32_e32 v146, s70, v146\n" \
"v_mfma_f32_16x16x32_bf16 v[92:95], a[184:187], a[136:139], v[92:95]\n" \
"v_mul_f32_e64 v147, s70, v147\n" \
"v_cvt_pk_bf16_f32 v141, v142, v143\n" \
"v_mfma_f32_16x16x32_bf16 v[92:95], a[248:251], a[200:203], v[92:95]\n" \
"buffer_load_dwordx4 v20, s[80:83], s75 offen offset:1024 sc1 lds\n" \
"v_mfma_f32_16x16x32_bf16 v[96:99], a[188:191], a[136:139], v[96:99]\n" \
"v_cvt_pk_bf16_f32 v134, v136, v137\n" \
"v_cvt_pk_bf16_f32 v135, v138, v139\n" \
"v_mfma_f32_16x16x32_bf16 v[96:99], a[252:255], a[200:203], v[96:99]\n" \
"v_cvt_pk_bf16_f32 v142, v144, v145\n" \
"v_cvt_pk_bf16_f32 v143, v146, v147\n" \
"v_mfma_f32_16x16x32_bf16 v[112:115], a[188:191], a[140:143], v[112:115]\n" \
"ds_write_b128 v21, v[132:135] offset:20480\n" \
"v_mfma_f32_16x16x32_bf16 v[112:115], a[252:255], a[204:207], v[112:115]\n" \
"buffer_load_dwordx4 v20, s[80:83], s75 offen offset:2048 sc1 lds\n" \
"v_mfma_f32_16x16x32_bf16 v[108:111], a[184:187], a[140:143], v[108:111]\n" \
"v_cvt_pk_bf16_f32 v180, v180, v181\n" \
"v_cvt_pk_bf16_f32 v181, v182, v183\n" \
"v_mfma_f32_16x16x32_bf16 v[108:111], a[248:251], a[204:207], v[108:111]\n" \
"ds_write_b128 v21, v[140:143] offset:21504\n" \
"v_mfma_f32_16x16x32_bf16 v[104:107], a[180:183], a[140:143], v[104:107]\n" \
"v_cvt_pk_bf16_f32 v234, v244, v245\n" \
"v_mov_b32_e64 v235, 0\n" \
"v_mfma_f32_16x16x32_bf16 v[104:107], a[244:247], a[204:207], v[104:107]\n" \
"buffer_load_dwordx4 v20, s[80:83], s75 offen offset:3072 sc1 lds\n" \
"v_mfma_f32_16x16x32_bf16 v[100:103], a[176:179], a[140:143], v[100:103]\n" \
"s_mov_b32 m0, s53\n" \
"s_nop 0\n" \
"v_cvt_pk_bf16_f32 v182, v50, v51\n" \
"v_mfma_f32_16x16x32_bf16 v[100:103], a[240:243], a[204:207], v[100:103]\n" \
"v_mov_b32_e32 v183, 0x3f80\n" \
"v_cvt_pk_bf16_f32 v118, v192, v193\n" \
"v_mfma_f32_16x16x32_bf16 v[52:55], a[176:179], a[128:131], v[52:55]\n" \
"v_cvt_pk_bf16_f32 v119, v194, v195\n" \
"v_cvt_pk_bf16_f32 v120, v208, v209\n" \
"v_mfma_f32_16x16x32_bf16 v[52:55], a[240:243], a[192:195], v[52:55]\n" \
"buffer_load_dwordx4 v20, s[80:83], s76 offen offset:0 sc1 lds\n" \
"v_mfma_f32_16x16x32_bf16 v[56:59], a[180:183], a[128:131], v[56:59]\n" \
"v_cvt_pk_bf16_f32 v121, v210, v211\n" \
"v_cvt_pk_bf16_f32 v122, v224, v225\n" \
"v_mfma_f32_16x16x32_bf16 v[56:59], a[244:247], a[192:195], v[56:59]\n" \
"v_cvt_pk_bf16_f32 v123, v226, v227\n" \
"v_cvt_pk_bf16_f32 v124, v240, v241\n" \
"v_cvt_pk_bf16_f32 v125, v242, v243\n" \
"v_mfma_f32_16x16x32_bf16 v[60:63], a[184:187], a[128:131], v[60:63]\n" \
"v_mfma_f32_16x16x32_bf16 v[192:195], v[188:191], v[184:187], 0\n" \
"buffer_load_dwordx4 v20, s[80:83], s76 offen offset:1024 sc1 lds\n" \
"v_mfma_f32_16x16x32_bf16 v[32:35], v[180:183], v[184:187], 0\n" \
"v_mfma_f32_16x16x32_bf16 v[208:211], v[204:207], v[200:203], 0\n" \
"v_mfma_f32_16x16x32_bf16 v[36:39], v[188:191], v[200:203], 0\n" \
"v_mfma_f32_16x16x32_bf16 v[224:227], v[220:223], v[216:219], 0\n" \
"v_mfma_f32_16x16x32_bf16 v[248:251], v[204:207], v[216:219], 0\n" \
"v_mfma_f32_16x16x32_bf16 v[240:243], v[236:239], v[232:235], 0\n" \
"v_mfma_f32_16x16x32_bf16 v[128:131], v[220:223], v[232:235], 0\n" \
"s_mov_b32 s64, 0xaaaaaaaa\n" \
"s_mov_b32 s65, 0xaaaaaaaa\n" \
"v_mfma_f32_16x16x32_bf16 v[60:63], a[248:251], a[192:195], v[60:63]\n" \
"s_mov_b32 s66, 0xfff0ffff\n" \
"s_mov_b32 s67, 0xf000ff00\n" \
"v_mfma_f32_16x16x32_bf16 v[64:67], a[188:191], a[128:131], v[64:67]\n" \
"s_mov_b32 s68, 0xffc0fffc\n" \
"s_mov_b32 s69, 0xc000fc00\n" \
"v_cndmask_b32_e64 v192, v192, v193, s[64:65]\n" \
"v_cndmask_b32_e64 v32, v32, v33, s[64:65]\n" \
"v_cndmask_b32_e64 v194, v194, v195, s[64:65]\n" \
"v_cndmask_b32_e64 v34, v34, v35, s[64:65]\n" \
"v_cndmask_b32_e64 v208, v208, v209, s[64:65]\n" \
"v_cndmask_b32_e64 v36, v36, v37, s[64:65]\n" \
"v_cndmask_b32_e64 v210, v210, v211, s[64:65]\n" \
"v_cndmask_b32_e64 v38, v38, v39, s[64:65]\n" \
"v_cndmask_b32_e64 v224, v224, v225, s[64:65]\n" \
"v_cndmask_b32_e64 v248, v248, v249, s[64:65]\n" \
"v_cndmask_b32_e64 v226, v226, v227, s[64:65]\n" \
"v_cndmask_b32_e64 v250, v250, v251, s[64:65]\n" \
"v_cndmask_b32_e64 v240, v240, v241, s[64:65]\n" \
"v_cndmask_b32_e64 v128, v128, v129, s[64:65]\n" \
"v_cndmask_b32_e64 v242, v242, v243, s[64:65]\n" \
"v_cndmask_b32_e64 v130, v130, v131, s[64:65]\n" \
"v_cndmask_b32_e64 v192, v32, v192, s[66:67]\n" \
"v_cndmask_b32_e64 v194, v34, v194, s[68:69]\n" \
"v_cndmask_b32_e64 v208, v36, v208, s[66:67]\n" \
"v_cndmask_b32_e64 v210, v38, v210, s[68:69]\n" \
"v_cndmask_b32_e64 v224, v248, v224, s[66:67]\n" \
"v_cndmask_b32_e64 v226, v250, v226, s[68:69]\n" \
"v_cndmask_b32_e64 v240, v128, v240, s[66:67]\n" \
"v_cndmask_b32_e64 v242, v130, v242, s[68:69]\n" \
"v_mfma_f32_16x16x32_bf16 v[64:67], a[252:255], a[192:195], v[64:67]\n" \
"v_max_f32_e32 v193, v192, v194\n" \
"v_max_f32_e32 v209, v208, v210\n" \
"v_max_f32_e32 v225, v224, v226\n" \
"v_max_f32_e32 v241, v240, v242\n" \
"v_mul_f32_e32 v148, s70, v148\n" \
"v_permlane32_swap_b32_e32 v193, v225\n" \
"v_permlane32_swap_b32_e32 v209, v241\n" \
"v_mul_f32_e32 v149, s70, v149\n" \
"v_mul_f32_e32 v156, s70, v156\n" \
"v_mul_f32_e32 v157, s70, v157\n" \
"v_permlane32_swap_b32_e32 v196, v228\n" \
"v_permlane32_swap_b32_e32 v212, v244\n" \
"v_max_f32_e32 v193, v193, v225\n" \
"v_max_f32_e32 v209, v209, v241\n" \
"v_mul_f32_e32 v150, s70, v150\n" \
"v_mul_f32_e32 v151, s70, v151\n" \
"v_cvt_pk_bf16_f32 v148, v148, v149\n" \
"v_mul_f32_e32 v158, s70, v158\n" \
"v_mul_f32_e32 v159, s70, v159\n" \
"v_cvt_pk_bf16_f32 v156, v156, v157\n" \
"v_permlane16_swap_b32_e32 v228, v244\n" \
"v_permlane16_swap_b32_e32 v193, v209\n" \
"v_mfma_f32_16x16x32_bf16 v[80:83], a[188:191], a[132:135], v[80:83]\n" \
"s_mov_b32 s72, 0x3eae1a93\n" \
"v_mul_f32_e32 v152, s70, v152\n" \
"v_mul_f32_e32 v153, s70, v153\n" \
"v_mul_f32_e32 v154, s70, v154\n" \
"v_mul_f32_e32 v155, s70, v155\n" \
"v_cvt_pk_bf16_f32 v149, v150, v151\n" \
"v_mul_f32_e32 v160, s70, v160\n" \
"v_mul_f32_e32 v161, s70, v161\n" \
"v_mul_f32_e32 v162, s70, v162\n" \
"v_mul_f32_e32 v163, s70, v163\n" \
"v_cvt_pk_bf16_f32 v157, v158, v159\n" \
"v_mul_f32_e32 v45, 0xbfb8aa3b, v228\n" \
"v_mfma_f32_16x16x16_bf16 v[48:51], v[182:183], v[46:47], 0\n" \
"v_mfma_f32_16x16x16_bf16 v[196:199], v[190:191], v[46:47], 0\n" \
"v_mfma_f32_16x16x16_bf16 v[212:215], v[206:207], v[46:47], 0\n" \
"v_max_f32_e64 v193, v193, v209\n" \
"v_cvt_pk_bf16_f32 v150, v152, v153\n" \
"v_cvt_pk_bf16_f32 v151, v154, v155\n" \
"v_cvt_pk_bf16_f32 v158, v160, v161\n" \
"v_cvt_pk_bf16_f32 v159, v162, v163\n" \
"v_mfma_f32_16x16x16_bf16 v[228:231], v[222:223], v[46:47], 0\n" \
"v_mul_f32_e32 v193, s72, v193\n" \
"v_mov_b32_e32 v209, v193\n" \
"ds_write_b128 v21, v[148:151] offset:24576\n" \
"v_mfma_f32_16x16x16_bf16 v[244:247], v[238:239], v[46:47], 0\n" \
"v_permlane16_swap_b32_e32 v193, v209\n" \
"s_mov_b32 s71, 0x3e0293ee\n" \
"v_mfma_f32_16x16x32_bf16 v[80:83], a[252:255], a[196:199], v[80:83]\n" \
"v_mov_b32_e32 v225, v193\n" \
"v_mov_b32_e32 v241, v209\n" \
"v_mul_f32_e32 v164, s70, v164\n" \
"v_mul_f32_e64 v165, s70, v165\n" \
"ds_write_b128 v21, v[156:159] offset:25600\n" \
"v_mul_f32_e32 v172, s70, v172\n" \
"v_mul_f32_e32 v173, s70, v173\n" \
"v_pk_mul_f32 v[84:85], v[84:85], s[70:71]\n" \
"v_pk_mul_f32 v[92:93], v[92:93], s[70:71]\n" \
"v_pk_mul_f32 v[100:101], v[100:101], s[70:71]\n" \
"v_pk_mul_f32 v[108:109], v[108:109], s[70:71]\n" \
"v_pk_mul_f32 v[52:53], v[52:53], s[70:71]\n" \
"v_pk_mul_f32 v[60:61], v[60:61], s[70:71]\n" \
"v_permlane32_swap_b32_e32 v193, v225\n" \
"v_permlane32_swap_b32_e32 v209, v241\n" \
"v_mul_f32_e32 v166, s70, v166\n" \
"v_mul_f32_e32 v167, s70, v167\n" \
"v_cvt_pk_bf16_f32 v164, v164, v165\n" \
"v_mul_f32_e32 v174, s70, v174\n" \
"v_mul_f32_e32 v175, s70, v175\n" \
"v_cvt_pk_bf16_f32 v172, v172, v173\n" \
"v_pk_mul_f32 v[86:87], v[86:87], s[70:71]\n" \
"v_cvt_pk_bf16_f32 v84, v84, v85\n" \
"v_pk_mul_f32 v[94:95], v[94:95], s[70:71]\n" \
"v_cvt_pk_bf16_f32 v92, v92, v93\n" \
"v_pk_mul_f32 v[102:103], v[102:103], s[70:71]\n" \
"v_cvt_pk_bf16_f32 v100, v100, v101\n" \
"v_pk_mul_f32 v[110:111], v[110:111], s[70:71]\n" \
"v_cvt_pk_bf16_f32 v108, v108, v109\n" \
"v_pk_mul_f32 v[54:55], v[54:55], s[70:71]\n" \
"v_cvt_pk_bf16_f32 v52, v52, v53\n" \
"v_pk_mul_f32 v[62:63], v[62:63], s[70:71]\n" \
"v_cvt_pk_bf16_f32 v60, v60, v61\n" \
"v_fma_f32 v192, v192, s72, -v193\n" \
"v_fma_f32 v194, v194, s72, -v193\n" \
"v_fma_f32 v208, v208, s72, -v209\n" \
"v_fma_f32 v210, v210, s72, -v209\n" \
"v_mul_f32_e32 v168, s70, v168\n" \
"v_mul_f32_e32 v169, s70, v169\n" \
"v_mul_f32_e32 v170, s70, v170\n" \
"v_mul_f32_e32 v171, s70, v171\n" \
"v_cvt_pk_bf16_f32 v165, v166, v167\n" \
"v_mul_f32_e32 v176, s70, v176\n" \
"v_mul_f32_e32 v177, s70, v177\n" \
"v_mul_f32_e32 v178, s70, v178\n" \
"v_mul_f32_e32 v179, s70, v179\n" \
"v_cvt_pk_bf16_f32 v173, v174, v175\n" \
"v_pk_mul_f32 v[88:89], v[88:89], s[70:71]\n" \
"v_pk_mul_f32 v[90:91], v[90:91], s[70:71]\n" \
"v_cvt_pk_bf16_f32 v85, v86, v87\n" \
"v_pk_mul_f32 v[96:97], v[96:97], s[70:71]\n" \
"v_pk_mul_f32 v[98:99], v[98:99], s[70:71]\n" \
"v_cvt_pk_bf16_f32 v93, v94, v95\n" \
"v_pk_mul_f32 v[104:105], v[104:105], s[70:71]\n" \
"v_pk_mul_f32 v[106:107], v[106:107], s[70:71]\n" \
"v_cvt_pk_bf16_f32 v101, v102, v103\n" \
"v_pk_mul_f32 v[112:113], v[112:113], s[70:71]\n" \
"v_pk_mul_f32 v[114:115], v[114:115], s[70:71]\n" \
"v_cvt_pk_bf16_f32 v109, v110, v111\n" \
"v_pk_mul_f32 v[56:57], v[56:57], s[70:71]\n" \
"v_pk_mul_f32 v[58:59], v[58:59], s[70:71]\n" \
"v_cvt_pk_bf16_f32 v53, v54, v55\n" \
"v_pk_mul_f32 v[64:65], v[64:65], s[70:71]\n" \
"v_pk_mul_f32 v[66:67], v[66:67], s[70:71]\n" \
"v_cvt_pk_bf16_f32 v61, v62, v63\n" \
"v_pk_mul_f32 v[80:81], v[80:81], s[70:71]\n" \
"v_pk_mul_f32 v[82:83], v[82:83], s[70:71]\n" \
"v_exp_f32_e32 v192, v192\n" \
"v_exp_f32_e32 v194, v194\n" \
"v_exp_f32_e32 v208, v208\n" \
"v_exp_f32_e32 v210, v210\n" \
"v_fma_f32 v224, v224, s72, -v225\n" \
"v_fma_f32 v226, v226, s72, -v225\n" \
"v_mfma_f32_16x16x32_bf16 v[76:79], a[184:187], a[132:135], v[76:79]\n" \
"v_cvt_pk_bf16_f32 v166, v168, v169\n" \
"v_cvt_pk_bf16_f32 v167, v170, v171\n" \
"v_cvt_pk_bf16_f32 v174, v176, v177\n" \
"v_cvt_pk_bf16_f32 v175, v178, v179\n" \
"v_cvt_pk_bf16_f32 v86, v88, v89\n" \
"v_cvt_pk_bf16_f32 v87, v90, v91\n" \
"v_cvt_pk_bf16_f32 v94, v96, v97\n" \
"v_cvt_pk_bf16_f32 v95, v98, v99\n" \
"v_cvt_pk_bf16_f32 v102, v104, v105\n" \
"v_cvt_pk_bf16_f32 v103, v106, v107\n" \
"v_cvt_pk_bf16_f32 v110, v112, v113\n" \
"v_cvt_pk_bf16_f32 v111, v114, v115\n" \
"v_cvt_pk_bf16_f32 v54, v56, v57\n" \
"v_cvt_pk_bf16_f32 v55, v58, v59\n" \
"v_cvt_pk_bf16_f32 v62, v64, v65\n" \
"v_cvt_pk_bf16_f32 v63, v66, v67\n" \
"v_exp_f32_e32 v224, v224\n" \
"v_exp_f32_e32 v226, v226\n" \
"v_fma_f32 v240, v240, s72, -v241\n" \
"v_fma_f32 v242, v242, s72, -v241\n" \
"ds_write_b128 v21, v[164:167] offset:28672\n" \
"v_cvt_pk_bf16_f32 v192, v192, v194\n" \
"v_cvt_pk_bf16_f32 v208, v208, v210\n" \
"v_exp_f32_e32 v240, v240\n" \
"v_exp_f32_e32 v242, v242\n" \
"v_cvt_pk_bf16_f32 v132, v48, v49\n" \
"v_cvt_pk_bf16_f32 v133, v50, v51\n" \
"v_cvt_pk_bf16_f32 v134, v196, v197\n" \
"v_cvt_pk_bf16_f32 v135, v198, v199\n" \
"ds_write_b128 v21, v[172:175] offset:29696\n" \
"v_cvt_pk_bf16_f32 v136, v212, v213\n" \
"v_cvt_pk_bf16_f32 v137, v214, v215\n" \
"v_perm_b32 v184, v192, v192, v40\n" \
"v_perm_b32 v185, v192, v192, v41\n" \
"v_perm_b32 v186, v192, v192, v42\n" \
"v_perm_b32 v187, v192, v192, v43\n" \
"v_perm_b32 v200, v208, v208, v40\n" \
"v_perm_b32 v201, v208, v208, v41\n" \
"v_perm_b32 v202, v208, v208, v42\n" \
"v_perm_b32 v203, v208, v208, v43\n" \
"ds_write_b128 v27, v[84:87] offset:8192\n" \
"v_cvt_pk_bf16_f32 v224, v224, v226\n" \
"v_cvt_pk_bf16_f32 v138, v228, v229\n" \
"v_cvt_pk_bf16_f32 v139, v230, v231\n" \
"v_mfma_f32_16x16x32_bf16 v[32:35], v[132:135], v[184:187], 0\n" \
"v_mfma_f32_16x16x32_bf16 v[36:39], v[134:137], v[200:203], 0\n" \
"v_perm_b32 v216, v224, v224, v40\n" \
"v_perm_b32 v217, v224, v224, v41\n" \
"ds_write_b128 v27, v[92:95] offset:9216\n" \
"v_perm_b32 v218, v224, v224, v42\n" \
"v_perm_b32 v219, v224, v224, v43\n" \
"v_cvt_pk_bf16_f32 v240, v240, v242\n" \
"v_mfma_f32_16x16x32_bf16 v[76:79], a[248:251], a[196:199], v[76:79]\n" \
"v_cvt_pk_bf16_f32 v140, v244, v245\n" \
"v_cvt_pk_bf16_f32 v141, v246, v247\n" \
"v_mfma_f32_16x16x32_bf16 v[248:251], v[136:139], v[216:219], 0\n" \
"v_perm_b32 v232, v240, v240, v40\n" \
"v_perm_b32 v233, v240, v240, v41\n" \
"ds_write_b128 v27, v[100:103] offset:12288\n" \
"v_perm_b32 v234, v240, v240, v42\n" \
"v_perm_b32 v235, v240, v240, v43\n" \
"v_pk_mul_f32 v[76:77], v[76:77], s[70:71]\n" \
"v_pk_mul_f32 v[78:79], v[78:79], s[70:71]\n" \
"v_cvt_pk_bf16_f32 v76, v76, v77\n" \
"v_cvt_pk_bf16_f32 v77, v78, v79\n" \
"v_cvt_pk_bf16_f32 v78, v80, v81\n" \
"v_cvt_pk_bf16_f32 v79, v82, v83\n" \
"v_mfma_f32_16x16x32_bf16 v[128:131], v[138:141], v[232:235], 0\n" \
"ds_write_b128 v27, v[108:111] offset:13312\n" \
"v_mfma_f32_16x16x32_bf16 v[72:75], a[180:183], a[132:135], v[72:75]\n" \
"v_cvt_pk_bf16_f32 v116, v252, v253\n" \
"v_cvt_pk_bf16_f32 v117, v254, v255\n" \
"v_mfma_f32_16x16x32_bf16 v[72:75], a[244:247], a[196:199], v[72:75]\n" \
"v_exp_f32_e64 v45, v45\n" \
"v_mfma_f32_16x16x32_bf16 v[192:195], v[116:119], v[184:187], 0\n" \
"ds_write_b128 v27, v[52:55] offset:0\n" \
"v_mfma_f32_16x16x32_bf16 v[208:211], v[118:121], v[200:203], 0\n" \
"v_mfma_f32_16x16x32_bf16 v[224:227], v[120:123], v[216:219], 0\n" \
"v_mfma_f32_16x16x32_bf16 v[240:243], v[122:125], v[232:235], 0\n" \
"ds_write_b128 v27, v[60:63] offset:1024\n" \
"v_mov_b32_dpp v34, v38 quad_perm:[0,1,2,3] row_mask:0x2 bank_mask:0xf\n" \
"v_pk_mul_f32 v[72:73], v[72:73], s[70:71]\n" \
"v_pk_mul_f32 v[74:75], v[74:75], s[70:71]\n" \
"v_mov_b32_dpp v34, v250 quad_perm:[0,1,2,3] row_mask:0x4 bank_mask:0xf\n" \
"v_add_f32_e64 v45, 1.0, v45\n" \
"v_mfma_f32_16x16x32_bf16 v[68:71], a[176:179], a[132:135], v[68:71]\n" \
"v_mov_b32_dpp v34, v130 quad_perm:[0,1,2,3] row_mask:0x8 bank_mask:0xf\n" \
"v_mov_b32_dpp v32, v36 quad_perm:[0,1,2,3] row_mask:0x2 bank_mask:0xf\n" \
"v_rcp_f32_e64 v45, v45\n" \
"ds_write_b128 v27, v[76:79] offset:5120\n" \
"v_mov_b32_dpp v33, v37 quad_perm:[0,1,2,3] row_mask:0x2 bank_mask:0xf\n" \
"v_rcp_f32_e64 v34, v34\n" \
"v_mov_b32_dpp v32, v248 quad_perm:[0,1,2,3] row_mask:0x4 bank_mask:0xf\n" \
"v_mov_b32_dpp v33, v249 quad_perm:[0,1,2,3] row_mask:0x4 bank_mask:0xf\n" \
"v_mul_f32_e64 v34, v45, v34\n" \
"v_mov_b32_dpp v32, v128 quad_perm:[0,1,2,3] row_mask:0x8 bank_mask:0xf\n" \
"v_mov_b32_dpp v33, v129 quad_perm:[0,1,2,3] row_mask:0x8 bank_mask:0xf\n" \
"v_mul_f32_e32 v32, v34, v32\n" \
"v_mul_f32_e32 v33, v34, v33\n" \
"v_mov_b32_e64 v38, v34\n" \
"v_mfma_f32_16x16x32_bf16 v[68:71], a[240:243], a[196:199], v[68:71]\n" \
"v_sub_f32_e32 v44, 1.0, v45\n" \
"v_permlane16_swap_b32_e32 v34, v38\n" \
"v_mov_b32_e32 v250, v34\n" \
"v_mov_b32_e32 v130, v38\n" \
"v_cvt_pk_bf16_f32 v32, v32, v33\n" \
"v_permlane32_swap_b32_e32 v34, v250\n" \
"v_permlane32_swap_b32_e32 v38, v130\n" \
"v_mul_f32_e64 v192, v34, v192\n" \
"v_pk_mul_f32 v[68:69], v[68:69], s[70:71]\n" \
"v_pk_mul_f32 v[70:71], v[70:71], s[70:71]\n" \
"v_cvt_pk_bf16_f32 v68, v68, v69\n" \
"v_cvt_pk_bf16_f32 v69, v70, v71\n" \
"v_cvt_pk_bf16_f32 v70, v72, v73\n" \
"v_cvt_pk_bf16_f32 v71, v74, v75\n" \
"ds_write_b128 v27, v[68:71] offset:4096\n" \
"v_mul_f32_e32 v193, v34, v193\n" \
"v_mul_f32_e32 v208, v38, v208\n" \
"v_mul_f32_e32 v209, v38, v209\n" \
"v_mul_f32_e32 v224, v250, v224\n" \
"v_mul_f32_e32 v225, v250, v225\n" \
"v_mul_f32_e32 v240, v130, v240\n" \
"v_mul_f32_e32 v241, v130, v241\n" \
"v_mul_f32_e32 v194, v34, v194\n" \
"v_mul_f32_e64 v195, v34, v195\n" \
"v_cvt_pk_bf16_f32 v192, v192, v193\n" \
"v_mul_f32_e32 v210, v38, v210\n" \
"v_mul_f32_e32 v211, v38, v211\n" \
"v_cvt_pk_bf16_f32 v208, v208, v209\n" \
"v_mul_f32_e32 v226, v250, v226\n" \
"v_mul_f32_e32 v227, v250, v227\n" \
"v_cvt_pk_bf16_f32 v224, v224, v225\n" \
"v_mul_f32_e32 v242, v130, v242\n" \
"v_mul_f32_e32 v243, v130, v243\n" \
"v_cvt_pk_bf16_f32 v240, v240, v241\n" \
"v_cvt_pk_bf16_f32 v193, v194, v195\n" \
"s_add_u32 s84, s48, 0x0\n" \
"s_nop 0\n" \
"v_cvt_pk_bf16_f32 v209, v210, v211\n" \
"s_add_u32 s85, s48, 0x24800\n" \
"v_cvt_pk_bf16_f32 v225, v226, v227\n" \
"s_add_u32 s86, s48, 0x49000\n" \
"v_cvt_pk_bf16_f32 v241, v242, v243\n" \
"s_add_u32 s87, s48, 0x6d800\n" \
"ds_write_b32 v26, v44\n" \
"buffer_store_dword v32, v25, s[80:83], s49 offen sc1\n" \
"buffer_store_dwordx2 v[192:193], v24, s[80:83], s84 offen sc1\n" \
"buffer_store_dwordx2 v[208:209], v24, s[80:83], s85 offen sc1\n" \
"buffer_store_dwordx2 v[224:225], v24, s[80:83], s86 offen sc1\n" \
"buffer_store_dwordx2 v[240:241], v24, s[80:83], s87 offen sc1\n" \
"s_branch L_end_%=\n" \
"L_poll61_%=:\n" \
"s_mov_b32 s84, 0\n" \
"L_pl61_%=:\n" \
"s_sleep 1\n" \
"v_bfe_u32 v44, v20, 2, 5\n" \
"buffer_load_dword v44, v44, s[80:83], s38 offen offset:128 sc1\n" \
"s_waitcnt vmcnt(0)\n" \
"v_cmp_ne_u32_e32 vcc, s62, v44\n" \
"s_add_u32 s84, s84, 1\n" \
"s_nop 3\n" \
"s_cbranch_vccz L_pollr61_%=\n" \
"s_cmp_gt_u32 s84, 0x400000\n" \
"s_cbranch_scc0 L_pl61_%=\n" \
"s_branch L_pollr61_%=\n" \
"L_poll161_%=:\n" \
"s_mov_b32 s84, 0\n" \
"L_pl161_%=:\n" \
"s_sleep 1\n" \
"v_bfe_u32 v44, v20, 2, 5\n" \
"buffer_load_dword v44, v44, s[80:83], s38 offen offset:0 sc1\n" \
"s_waitcnt vmcnt(0)\n" \
"v_cmp_ne_u32_e32 vcc, s62, v44\n" \
"s_add_u32 s84, s84, 1\n" \
"s_nop 3\n" \
"s_cbranch_vccz L_pollr161_%=\n" \
"s_cmp_gt_u32 s84, 0x400000\n" \
"s_cbranch_scc0 L_pl161_%=\n" \
"s_branch L_pollr161_%=\n" \
"L_end_%=:\n"
#define PROJ_VCLOB "v21","v24","v25","v28","v29","v32","v33","v34","v35","v36","v37","v38","v39","v40","v41","v42","v43","v44","v45","v46","v47","v48","v49","v50","v51","v52","v53","v54","v55","v56","v57","v58","v59","v60","v61","v62","v63","v64","v65","v66","v67","v68","v69","v70","v71","v72","v73","v74","v75","v76","v77","v78","v79","v80","v81","v82","v83","v84","v85","v86","v87","v88","v89","v90","v91","v92","v93","v94","v95","v96","v97","v98","v99","v100","v101","v102","v103","v104","v105","v106","v107","v108","v109","v110","v111","v112","v113","v114","v115","v116","v117","v118","v119","v120","v121","v122","v123","v124","v125","v126","v127","v128","v129","v130","v131","v132","v133","v134","v135","v136","v137","v138","v139","v140","v141","v142","v143","v144","v145","v146","v147","v148","v149","v150","v151","v152","v153","v154","v155","v156","v157","v158","v159","v160","v161","v162","v163","v164","v165","v166","v167","v168","v169","v170","v171","v172","v173","v174","v175","v176","v177","v178","v179","v180","v181","v182","v183","v184","v185","v186","v187","v188","v189","v190","v192","v193","v194","v195","v196","v197","v198","v199","v200","v201","v202","v203","v204","v205","v206","v207","v208","v209","v210","v211","v212","v213","v214","v215","v216","v217","v218","v219","v220","v221","v222","v223","v224","v225","v226","v227","v228","v229","v230","v231","v232","v233","v234","v235","v236","v237","v238","v239","v240","v241","v242","v243","v244","v245","v246","v247","v248","v249","v250","v251","v252","v253","v254","v255"
#define PROJ_SCLOB "s4","s5","s6","s7","s32","s33","s34","s35","s36","s37","s38","s39","s40","s41","s42","s43","s44","s45","s46","s47","s48","s49","s50","s51","s52","s53","s54","s55","s63","s64","s65","s66","s67","s68","s69","s70","s71","s72","s73","s74","s75","s76","s77","s78","s79","s80","s81","s82","s83","s84","s85","s86","s87","s88","s89","s90","s91","s92","s93","s94","s95","s96","s97","s98","s99","s100","s101"

typedef const u16* const __attribute__((address_space(4)))* kaptr;
__device__ __forceinline__ void proj_phase(kaptr& ka, char*& ws, const unsigned wid, u32x4 (&vp)[5], unsigned& vl, u32x4 (&sa)[5], u32x2 s24, u32x2& s28, unsigned tdum, u32x4& srdA, u32x2& sd, unsigned& tag
    ) {
    const unsigned b = blockIdx.x, h = (b & 7) * 4 + ((b >> 3) & 3), mt = b >> 5;
    const unsigned rb = (wid < 2u ? 15u - mt : mt) * 128u;
    const unsigned lane = threadIdx.x & 63u, wc = wid & 1u;
    u32x2 vAc, vB;
#pragma unroll
    for (int par = 0; par < 2; ++par) {
        const unsigned lr = lane >> 3, cc = (lane & 7u) ^ (4u * par + (lr >> 1)), r0 = rb + wc * 64u + lr;
        vAc[par] = r0 * (DC * 2) + cc * 16u;
        const unsigned br = lane >> 4, p16 = lane & 15u, sc = (((p16 >> 1) ^ (br | (par << 2))) << 1) | (p16 & 1u);
        vB[par] = (16u * wid + br) * (NBIG * 2) + h * (HD * 2) + sc * 16u;
    }
    vl = lane * 16u;
    u32x2 s30 = {wid * 8192u, 65536u + wid * 4096u};
    asm volatile(PROJ_ASM
                 : "={s62}"(tag), "={s[56:59]}"(srdA), "={s[60:61]}"(sd), "+{s[8:11]}"(sa[0]), "+{s[12:15]}"(sa[1]), "+{s[16:19]}"(sa[2]), "+{s[20:23]}"(sa[3]), "+{s[24:25]}"(s24), "+{s[28:29]}"(s28), "+{s[30:31]}"(s30),
                   "={v[0:3]}"(vp[0]), "={v[4:7]}"(vp[1]), "={v[8:11]}"(vp[2]), "={v[12:15]}"(vp[3]), "={v[16:19]}"(vp[4]), "+{v[22:23]}"(vAc), "+{v[26:27]}"(vB), "+{v191}"(tdum), "+{s[26:27]}"(ws), "+{s[0:1]}"(ka)
                 : "{s2}"(b), "{s3}"(wid), "{v20}"(vl)
                 : "memory", "m0", "vcc", "scc", PROJ_VCLOB, PROJ_SCLOB, ACLOB_LO, ACLOB_HI);
    sa[4] = (u32x4){s24[0], 0u, 0u, 0u};
}

namespace att {
#define DI __device__ __forceinline__
typedef __bf16 bfx8 __attribute__((ext_vector_type(8)));
DI unsigned rfl(unsigned x) { return __builtin_amdgcn_readfirstlane(x); }
DI f32x4 mfma(u32x4 a, u32x4 b, f32x4 c) { return __builtin_amdgcn_mfma_f32_16x16x32_bf16(__builtin_bit_cast(bfx8, a), __builtin_bit_cast(bfx8, b), c, 0, 0, 0); }
typedef __bf16 bfx2 __attribute__((ext_vector_type(2)));
typedef float f32x2 __attribute__((ext_vector_type(2)));
DI unsigned cvtpk2(float lo, float hi) { f32x2 v = {lo, hi}; bfx2 b = __builtin_convertvector(v, bfx2); return __builtin_bit_cast(unsigned, b); }
DI void att_exact(const u16* __restrict__ x, const u16* __restrict__ wq, char* __restrict__ ws, unsigned h, unsigned r0, float g1) {
    const unsigned lane = lane_id(), g = lane >> 4, c = lane & 15u;
    f32x4 o[8];
#pragma unroll
    for (int n = 0; n < 8; ++n) o[n] = (f32x4){0.f, 0.f, 0.f, 0.f};
#pragma unroll 1
    for (unsigned kk = 0; kk < (unsigned)DM; kk += 32u) {
        const unsigned k = kk + 8u * g;
        const u32x4 xf = *reinterpret_cast<const u32x4*>(x + (r0 + c) * DM + k);
#pragma unroll
        for (int n = 0; n < 8; ++n) {
            const u16* w = wq + k * NBIG + h * HD + n * 16 + c; u32x4 wf;
#pragma unroll
            for (int e = 0; e < 4; ++e) wf[e] = (unsigned)w[2 * e * NBIG] | ((unsigned)w[(2 * e + 1) * NBIG] << 16);
            o[n] = mfma(wf, xf, o[n]);
        }
    }
    u32x4 q[4];
#pragma unroll
    for (int p = 0; p < 4; ++p) {
        const f32x4 a = o[2 * p] * 0.12751743082f, b = o[2 * p + 1] * 0.12751743082f;
        q[p] = (u32x4){cvtpk2(a[0], a[1]), cvtpk2(a[2], a[3]), cvtpk2(b[0], b[1]), cvtpk2(b[2], b[3])};
    }
#pragma unroll
    for (int n = 0; n < 8; ++n) o[n] = (f32x4){0.f, 0.f, 0.f, 0.f};
    const __amdgpu_buffer_rsrc_t srd = __builtin_amdgcn_make_buffer_rsrc((void*)(ws + WS_K + (size_t)h * 1048576), (short)0, 1048576, 0x00020000);
    float m = -3.0e38f, l = 0.f;
#pragma unroll 1
    for (unsigned k0 = 0; k0 <= r0 + 15u; k0 += 32u) {
        const unsigned tb = (k0 >> 6) * 32768u + (k0 & 32u) * 256u;
        f32x4 s[2]; float mx = m;
#pragma unroll
        for (int kt = 0; kt < 2; ++kt) {
            s[kt] = (f32x4){0.f, 0.f, 0.f, 0.f};
#pragma unroll
            for (int p = 0; p < 4; ++p) s[kt] = mfma(__builtin_amdgcn_raw_buffer_load_b128(srd, lane * 16u + (kt * 4 + p) * 1024u, tb, 16), q[p], s[kt]);
#pragma unroll
            for (int e = 0; e < 4; ++e) { if (k0 + 16u * kt + 4u * g + e > r0 + c) s[kt][e] = -3.0e38f; mx = fmaxf(mx, s[kt][e]); }
        }
        mx = fmaxf(mx, __shfl_xor(mx, 16)); mx = fmaxf(mx, __shfl_xor(mx, 32));
        const float al = __builtin_amdgcn_exp2f(m - mx); m = mx;
        u32x4 pf;
#pragma unroll
        for (int e = 0; e < 4; ++e) pf[e] = cvtpk2(__builtin_amdgcn_exp2f(s[e >> 1][2 * (e & 1)] - m), __builtin_amdgcn_exp2f(s[e >> 1][2 * (e & 1) + 1] - m));
        l *= al;
#pragma unroll
        for (int e = 0; e < 4; ++e) l += __uint_as_float(pf[e] << 16) + __uint_as_float(pf[e] & 0xffff0000u);
#pragma unroll
        for (int n = 0; n < 8; ++n) o[n] = mfma(__builtin_amdgcn_raw_buffer_load_b128(srd, lane * 16u + 16384u + n * 1024u, tb, 16), pf, o[n] * al);
    }
    l += __shfl_xor(l, 16); l += __shfl_xor(l, 32);
    const float f = g1 / l;
    const __amdgpu_buffer_rsrc_t srdA = __builtin_amdgcn_make_buffer_rsrc((void*)(ws + WS_AF), (short)0, 128 * KS_TOT * 1024, 0x00020000);
#pragma unroll
    for (int p = 0; p < 4; ++p) {
        const f32x4 a = o[2 * p] * f, b = o[2 * p + 1] * f;
        __builtin_amdgcn_raw_buffer_store_b128((u32x4){cvtpk2(a[0], a[1]), cvtpk2(a[2], a[3]), cvtpk2(b[0], b[1]), cvtpk2(b[2], b[3])}, srdA, lane * 16u + p * 1024u, ((r0 >> 4) * KS_TOT + 18u + 4u * h) * 1024u, 16);
    }
}
}

#define ATT_ASM \
"s_branch L_ago_%=\n" \
".p2align 12\n" \
"L_ab:\n" \
".fill (L_ab + 0 - .) / 4, 4, 0xBF800000\n" \
"L_ap0:\n" \
"s_setpc_b64 s[88:89]\n" \
"L_ago_%=:\n" \
"s_mov_b32 s101, 0\n" \
"s_waitcnt vmcnt(15) lgkmcnt(0)\n" \
"s_barrier\n" \
"v_accvgpr_write_b32 a216, 0\n" \
"v_accvgpr_write_b32 a217, 0\n" \
"v_accvgpr_write_b32 a218, 0\n" \
"v_accvgpr_write_b32 a219, 0\n" \
"v_bfe_u32 v61, v20, 2, 5\n" \
"s_mov_b32 s96, s24\n" \
"v_mfma_f32_32x32x16_bf16 v[128:143], a[216:219], a[216:219], 0\n" \
"buffer_load_dword v57, v61, s[56:59], s96 offen sc1\n" \
"v_and_b32_e32 v63, 0x300, v20\n" \
"ds_read_b128 v[96:99], v63 offset:0\n" \
"v_add_u32_e32 v58, s22, v20\n" \
"ds_read_b128 a[0:3], v58 offset:0\n" \
"v_mfma_f32_32x32x16_bf16 v[144:159], a[216:219], a[216:219], 0\n" \
"ds_read_b128 a[16:19], v58 offset:8192\n" \
"v_add_u32_e32 v59, s23, v20\n" \
"ds_read_b128 a[32:35], v59 offset:0\n" \
"ds_read_b128 a[48:51], v59 offset:8192\n" \
"ds_read_b128 v[100:103], v63 offset:1024\n" \
"ds_read_b128 a[4:7], v58 offset:1024\n" \
"v_mfma_f32_32x32x16_bf16 v[160:175], a[216:219], a[216:219], 0\n" \
"ds_read_b128 a[20:23], v58 offset:9216\n" \
"ds_read_b128 a[36:39], v59 offset:1024\n" \
"ds_read_b128 a[52:55], v59 offset:9216\n" \
"s_sub_u32 s67, s12, 0xc000\n" \
"ds_read_b128 v[104:107], v63 offset:2048\n" \
"s_mov_b32 s65, s12\n" \
"v_mfma_f32_32x32x16_bf16 v[176:191], a[216:219], a[216:219], 0\n" \
"v_add_u32_e32 v55, s67, v20\n" \
"v_add_u32_e32 v52, 0xc000, v55\n" \
"s_xor_b32 m0, s65, 0x10000\n" \
"ds_read_b128 a[8:11], v58 offset:2048\n" \
"buffer_load_dwordx4 v52, s[8:11], 0 offen offset:0 sc1 lds\n" \
"v_mfma_f32_32x32x16_bf16 v[192:207], a[216:219], a[216:219], 0\n" \
"ds_read_b128 a[24:27], v58 offset:10240\n" \
"ds_read_b128 a[40:43], v59 offset:2048\n" \
"ds_read_b128 a[56:59], v59 offset:10240\n" \
"ds_read_b128 v[108:111], v63 offset:3072\n" \
"buffer_load_dwordx4 v52, s[8:11], 0 offen offset:1024 sc1 lds\n" \
"v_mfma_f32_32x32x16_bf16 v[208:223], a[216:219], a[216:219], 0\n" \
"ds_read_b128 a[12:15], v58 offset:3072\n" \
"ds_read_b128 a[28:31], v58 offset:11264\n" \
"ds_read_b128 a[44:47], v59 offset:3072\n" \
"ds_read_b128 a[60:63], v59 offset:11264\n" \
"s_waitcnt lgkmcnt(15)\n" \
"v_xor_b32_e32 v96, 0x80008000, v96\n" \
"v_mfma_f32_32x32x16_bf16 v[224:239], a[216:219], a[216:219], 0\n" \
"v_xor_b32_e32 v97, 0x80008000, v97\n" \
"v_xor_b32_e32 v98, 0x80008000, v98\n" \
"v_xor_b32_e32 v99, 0x80008000, v99\n" \
"ds_read_b128 a[160:163], v20 offset:0\n" \
"ds_read_b128 a[164:167], v20 offset:1024\n" \
"s_waitcnt lgkmcnt(15)\n" \
"v_xor_b32_e32 v100, 0x80008000, v100\n" \
"v_mfma_f32_32x32x16_bf16 v[240:255], a[216:219], a[216:219], 0\n" \
"v_xor_b32_e32 v101, 0x80008000, v101\n" \
"v_xor_b32_e32 v102, 0x80008000, v102\n" \
"v_xor_b32_e32 v103, 0x80008000, v103\n" \
"ds_read_b128 a[168:171], v20 offset:2048\n" \
"ds_read_b128 a[172:175], v20 offset:3072\n" \
"ds_read_b128 a[188:191], v20 offset:7168\n" \
"v_mfma_f32_16x16x32_bf16 a[192:195], a[216:219], a[216:219], 0\n" \
"ds_read_b128 a[184:187], v20 offset:6144\n" \
"s_waitcnt lgkmcnt(15)\n" \
"v_xor_b32_e32 v104, 0x80008000, v104\n" \
"v_mfma_f32_16x16x32_bf16 a[196:199], a[216:219], a[216:219], 0\n" \
"v_xor_b32_e32 v105, 0x80008000, v105\n" \
"v_xor_b32_e32 v106, 0x80008000, v106\n" \
"v_mfma_f32_16x16x32_bf16 v[32:35], v[96:99], a[0:3], 0\n" \
"v_xor_b32_e32 v107, 0x80008000, v107\n" \
"ds_read_b128 a[180:183], v20 offset:5120\n" \
"v_mfma_f32_16x16x32_bf16 v[36:39], v[96:99], a[16:19], 0\n" \
"ds_read_b128 a[176:179], v20 offset:4096\n" \
"s_waitcnt lgkmcnt(12)\n" \
"v_xor_b32_e32 v108, 0x80008000, v108\n" \
"v_mfma_f32_16x16x32_bf16 v[40:43], v[96:99], a[32:35], 0\n" \
"v_xor_b32_e32 v109, 0x80008000, v109\n" \
"v_xor_b32_e32 v110, 0x80008000, v110\n" \
"v_mfma_f32_16x16x32_bf16 v[44:47], v[96:99], a[48:51], 0\n" \
"v_xor_b32_e32 v111, 0x80008000, v111\n" \
"v_add_u32_e32 v60, s15, v20\n" \
"v_mfma_f32_16x16x32_bf16 v[32:35], v[100:103], a[4:7], v[32:35]\n" \
"v_add_u32_e32 v60, 0x10000, v60\n" \
"ds_read_b128 a[128:131], v20 offset:8192\n" \
"v_mfma_f32_16x16x32_bf16 v[36:39], v[100:103], a[20:23], v[36:39]\n" \
"ds_read_b128 a[144:147], v20 offset:12288\n" \
"ds_read_b128 a[132:135], v20 offset:9216\n" \
"v_mfma_f32_16x16x32_bf16 v[40:43], v[100:103], a[36:39], v[40:43]\n" \
"ds_read_b128 a[148:151], v20 offset:13312\n" \
"ds_read_b128 a[136:139], v20 offset:10240\n" \
"v_mfma_f32_16x16x32_bf16 v[44:47], v[100:103], a[52:55], v[44:47]\n" \
"ds_read_b128 a[152:155], v20 offset:14336\n" \
"ds_read_b128 a[140:143], v20 offset:11264\n" \
"v_mfma_f32_16x16x32_bf16 v[32:35], v[104:107], a[8:11], v[32:35]\n" \
"ds_read_b128 a[156:159], v20 offset:15360\n" \
"ds_read_b128 a[64:67], v60 offset:16384\n" \
"v_mfma_f32_16x16x32_bf16 v[36:39], v[104:107], a[24:27], v[36:39]\n" \
"ds_read_b128 a[68:71], v60 offset:17408\n" \
"ds_read_b128 a[72:75], v60 offset:18432\n" \
"v_mfma_f32_16x16x32_bf16 v[40:43], v[104:107], a[40:43], v[40:43]\n" \
"ds_read_b128 a[76:79], v60 offset:19456\n" \
"ds_read_b128 a[80:83], v60 offset:24576\n" \
"v_mfma_f32_16x16x32_bf16 v[44:47], v[104:107], a[56:59], v[44:47]\n" \
"ds_read_b128 a[84:87], v60 offset:25600\n" \
"s_waitcnt lgkmcnt(15)\n" \
"v_mfma_f32_16x16x32_bf16 v[32:35], v[108:111], a[12:15], v[32:35]\n" \
"ds_read_b128 a[88:91], v60 offset:26624\n" \
"ds_read_b128 a[92:95], v60 offset:27648\n" \
"v_mfma_f32_16x16x32_bf16 v[36:39], v[108:111], a[28:31], v[36:39]\n" \
"ds_read_b128 a[96:99], v60 offset:0\n" \
"ds_read_b128 a[100:103], v60 offset:1024\n" \
"v_mfma_f32_16x16x32_bf16 v[40:43], v[108:111], a[44:47], v[40:43]\n" \
"ds_read_b128 a[104:107], v60 offset:2048\n" \
"ds_read_b128 a[108:111], v60 offset:3072\n" \
"v_mfma_f32_16x16x32_bf16 v[44:47], v[108:111], a[60:63], v[44:47]\n" \
"ds_read_b128 a[112:115], v60 offset:8192\n" \
"ds_read_b128 a[116:119], v60 offset:9216\n" \
"v_mfma_f32_16x16x32_bf16 v[64:67], a[160:163], a[0:3], v[32:35]\n" \
"ds_read_b128 a[120:123], v60 offset:10240\n" \
"ds_read_b128 a[124:127], v60 offset:11264\n" \
"v_mfma_f32_16x16x32_bf16 v[64:67], a[164:167], a[4:7], v[64:67]\n" \
"v_bfe_u32 v26, v20, 8, 2\n" \
"s_mov_b32 m0, s20\n" \
"v_mfma_f32_16x16x32_bf16 v[64:67], a[168:171], a[8:11], v[64:67]\n" \
"v_bfe_u32 v25, v20, 5, 3\n" \
"v_lshlrev_b32_e32 v26, 1, v26\n" \
"v_mfma_f32_16x16x32_bf16 v[64:67], a[172:175], a[12:15], v[64:67]\n" \
"global_load_lds_dword v[16:17], off\n" \
"v_mfma_f32_16x16x32_bf16 v[68:71], a[188:191], a[12:15], v[32:35]\n" \
"v_sub_u32_e32 v25, v25, v26\n" \
"v_and_b32_e32 v26, 16, v20\n" \
"v_mfma_f32_16x16x32_bf16 v[68:71], a[184:187], a[8:11], v[68:71]\n" \
"v_mov_b32_e32 v27, 0x3f80\n" \
"v_lshlrev_b32_e32 v26, v26, v27\n" \
"v_mfma_f32_16x16x32_bf16 v[68:71], a[180:183], a[4:7], v[68:71]\n" \
"v_mov_b32_e32 v54, 0x3f803f80\n" \
"s_waitcnt lgkmcnt(15)\n" \
"v_mfma_f32_16x16x32_bf16 v[68:71], a[176:179], a[0:3], v[68:71]\n" \
"s_mov_b32 s84, 0x33333333\n" \
"s_mov_b32 s85, 0xcccccccc\n" \
"v_mfma_f32_16x16x32_bf16 v[76:79], a[176:179], a[16:19], v[36:39]\n" \
"s_mov_b32 s86, 0x11111111\n" \
"s_mov_b32 s87, 0x11111111\n" \
"v_mfma_f32_16x16x32_bf16 v[76:79], a[180:183], a[20:23], v[76:79]\n" \
"s_mov_b32 s88, 0x44444444\n" \
"s_mov_b32 s89, 0x44444444\n" \
"v_mfma_f32_16x16x32_bf16 v[76:79], a[184:187], a[24:27], v[76:79]\n" \
"v_exp_f32_e32 v70, v70\n" \
"v_mfma_f32_16x16x32_bf16 v[76:79], a[188:191], a[28:31], v[76:79]\n" \
"v_exp_f32_e32 v71, v71\n" \
"v_mfma_f32_16x16x32_bf16 v[72:75], a[172:175], a[28:31], v[36:39]\n" \
"v_exp_f32_e32 v68, v68\n" \
"v_mfma_f32_16x16x32_bf16 v[72:75], a[168:171], a[24:27], v[72:75]\n" \
"v_exp_f32_e32 v69, v69\n" \
"v_mfma_f32_16x16x32_bf16 v[72:75], a[164:167], a[20:23], v[72:75]\n" \
"v_bfe_i32 v21, v20, 4, 1\n" \
"v_cvt_pk_bf16_f32 v71, v70, v71\n" \
"v_mfma_f32_16x16x32_bf16 v[72:75], a[160:163], a[16:19], v[72:75]\n" \
"v_exp_f32_e32 v66, v66\n" \
"v_mfma_f32_16x16x32_bf16 v[80:83], a[160:163], a[32:35], v[40:43]\n" \
"v_exp_f32_e32 v67, v67\n" \
"v_mfma_f32_16x16x32_bf16 v[80:83], a[164:167], a[36:39], v[80:83]\n" \
"v_exp_f32_e32 v64, v64\n" \
"v_mfma_f32_16x16x32_bf16 v[80:83], a[168:171], a[40:43], v[80:83]\n" \
"v_exp_f32_e32 v65, v65\n" \
"v_mfma_f32_16x16x32_bf16 v[80:83], a[172:175], a[44:47], v[80:83]\n" \
"v_exp_f32_e32 v72, v72\n" \
"v_mfma_f32_16x16x32_bf16 v[84:87], a[188:191], a[44:47], v[40:43]\n" \
"s_waitcnt vmcnt(3)\n" \
"v_cmp_ne_u32_e32 vcc, s62, v57\n" \
"v_mfma_f32_16x16x32_bf16 v[84:87], a[184:187], a[40:43], v[84:87]\n" \
"v_exp_f32_e32 v73, v73\n" \
"v_mfma_f32_16x16x32_bf16 v[84:87], a[180:183], a[36:39], v[84:87]\n" \
"v_exp_f32_e32 v74, v74\n" \
"v_mfma_f32_16x16x32_bf16 v[84:87], a[176:179], a[32:35], v[84:87]\n" \
"s_cbranch_vccnz L_skb_%=\n" \
"L_rkb_%=:\n" \
"v_cmp_eq_u32_e32 vcc, 0, v25\n" \
"v_mfma_f32_16x16x32_bf16 v[92:95], a[176:179], a[48:51], v[44:47]\n" \
"v_exp_f32_e32 v75, v75\n" \
"v_mfma_f32_16x16x32_bf16 v[92:95], a[180:183], a[52:55], v[92:95]\n" \
"v_cndmask_b32_e32 v28, 0, v26, vcc\n" \
"v_cvt_pk_bf16_f32 v72, v72, v73\n" \
"v_mfma_f32_16x16x32_bf16 v[92:95], a[184:187], a[56:59], v[92:95]\n" \
"v_exp_f32_e32 v76, v76\n" \
"v_mfma_f32_16x16x32_bf16 v[92:95], a[188:191], a[60:63], v[92:95]\n" \
"v_exp_f32_e32 v77, v77\n" \
"v_mfma_f32_16x16x32_bf16 v[88:91], a[172:175], a[60:63], v[44:47]\n" \
"v_exp_f32_e32 v78, v78\n" \
"v_mfma_f32_16x16x32_bf16 v[88:91], a[168:171], a[56:59], v[88:91]\n" \
"v_exp_f32_e32 v79, v79\n" \
"v_mfma_f32_16x16x32_bf16 v[88:91], a[164:167], a[52:55], v[88:91]\n" \
"v_cndmask_b32_e64 v22, 0, v54, s[84:85]\n" \
"v_cndmask_b32_e64 v23, 0, v54, s[86:87]\n" \
"v_mfma_f32_16x16x32_bf16 v[88:91], a[160:163], a[48:51], v[88:91]\n" \
"v_cndmask_b32_e64 v24, 0, v54, s[88:89]\n" \
"v_and_b32_e32 v21, 0xaaaaaaaa, v21\n" \
"s_waitcnt vmcnt(4) lgkmcnt(0)\n" \
"s_barrier\n" \
"v_cmp_eq_u32_e32 vcc, 1, v25\n" \
"v_cvt_pk_bf16_f32 v70, v68, v69\n" \
"v_cvt_pk_bf16_f32 v73, v74, v75\n" \
"v_mov_b32_e32 v62, 0\n" \
"v_accvgpr_write_b32 a208, 0\n" \
"v_accvgpr_write_b32 a209, 0\n" \
"v_accvgpr_write_b32 a210, 0\n" \
"v_accvgpr_write_b32 a211, 0\n" \
"v_accvgpr_write_b32 a200, v22\n" \
"v_accvgpr_write_b32 a201, v22\n" \
"v_accvgpr_write_b32 a202, v22\n" \
"v_accvgpr_write_b32 a203, v22\n" \
"v_accvgpr_write_b32 a204, v23\n" \
"v_accvgpr_write_b32 a205, v23\n" \
"v_accvgpr_write_b32 a206, v23\n" \
"v_accvgpr_write_b32 a207, v23\n" \
"v_accvgpr_write_b32 a212, v24\n" \
"v_accvgpr_write_b32 a213, v24\n" \
"v_accvgpr_write_b32 a214, v24\n" \
"v_accvgpr_write_b32 a215, v24\n" \
"v_or_b32_e32 v21, 0x44444444, v21\n" \
"s_mov_b32 s64, 0x8000\n" \
"v_add_u32_e32 v50, 0x6000, v55\n" \
"v_add_u32_e32 v51, 0xa000, v55\n" \
"v_add_u32_e32 v53, 0x10000, v55\n" \
"v_add_u32_e32 v48, 0xc000, v20\n" \
"v_add_u32_e32 v49, s17, v20\n" \
"v_cndmask_b32_e32 v29, 0, v26, vcc\n" \
"v_accvgpr_write_b32 a220, v96\n" \
"v_accvgpr_write_b32 a221, v97\n" \
"v_accvgpr_write_b32 a222, v98\n" \
"v_accvgpr_write_b32 a223, v99\n" \
"v_accvgpr_write_b32 a224, v100\n" \
"v_accvgpr_write_b32 a225, v101\n" \
"v_accvgpr_write_b32 a226, v102\n" \
"v_accvgpr_write_b32 a227, v103\n" \
"v_accvgpr_write_b32 a228, v104\n" \
"v_accvgpr_write_b32 a229, v105\n" \
"v_accvgpr_write_b32 a230, v106\n" \
"v_accvgpr_write_b32 a231, v107\n" \
"v_accvgpr_write_b32 a232, v108\n" \
"v_accvgpr_write_b32 a233, v109\n" \
"v_accvgpr_write_b32 a234, v110\n" \
"v_accvgpr_write_b32 a235, v111\n" \
"v_cvt_pk_bf16_f32 v69, v66, v67\n" \
"v_cvt_pk_bf16_f32 v68, v64, v65\n" \
"v_cvt_pk_bf16_f32 v74, v76, v77\n" \
"v_cvt_pk_bf16_f32 v75, v78, v79\n" \
"s_sub_u32 s66, s18, 1\n" \
"s_cbranch_scc1 L_tr_%=\n" \
"s_branch L_1_%=\n" \
".fill (L_ab + 1816 - .) / 4, 4, 0xBF800000\n" \
"L_tL_1_%=:\n" \
"s_waitcnt vmcnt(4) lgkmcnt(0)\n" \
"v_smfmac_f32_16x16x64_bf16 a[196:199], a[200:203], v[116:123], v21\n" \
"s_barrier\n" \
"L_1_%=:\n" \
"v_mfma_f32_16x16x32_bf16 v[96:99], a[128:131], a[0:3], v[32:35]\n" \
"s_xor_b32 m0, s65, 0x12000\n" \
"ds_read_b128 a[160:163], v49 offset:0\n" \
"v_mfma_f32_16x16x32_bf16 v[96:99], a[132:135], a[4:7], v[96:99]\n" \
"buffer_load_dwordx4 v50, s[8:11], s64 offen offset:0 sc1 lds\n" \
"v_mfma_f32_16x16x32_bf16 v[96:99], a[136:139], a[8:11], v[96:99]\n" \
"ds_read_b128 a[164:167], v49 offset:1024\n" \
"ds_read_b128 a[168:171], v49 offset:2048\n" \
"v_mfma_f32_16x16x32_bf16 v[96:99], a[140:143], a[12:15], v[96:99]\n" \
"ds_read_b128 a[172:175], v49 offset:3072\n" \
"ds_read_b128 a[176:179], v49 offset:4096\n" \
"v_mfma_f32_16x16x32_bf16 v[100:103], a[156:159], a[12:15], v[32:35]\n" \
"ds_read_b128 a[180:183], v49 offset:5120\n" \
"ds_read_b128 a[184:187], v49 offset:6144\n" \
"v_mfma_f32_16x16x32_bf16 v[100:103], a[152:155], a[8:11], v[100:103]\n" \
"v_exp_f32_e64 v86, v86\n" \
"v_mfma_f32_16x16x32_bf16 v[100:103], a[148:151], a[4:7], v[100:103]\n" \
"v_exp_f32_e64 v87, v87\n" \
"v_mfma_f32_16x16x32_bf16 v[100:103], a[144:147], a[0:3], v[100:103]\n" \
"ds_read_b128 a[188:191], v49 offset:7168\n" \
"v_cvt_pk_bf16_f32 v87, v86, v87\n" \
"v_mfma_f32_16x16x32_bf16 v[108:111], a[144:147], a[16:19], v[36:39]\n" \
"v_exp_f32_e64 v84, v84\n" \
"v_mfma_f32_16x16x32_bf16 v[108:111], a[148:151], a[20:23], v[108:111]\n" \
"v_exp_f32_e64 v85, v85\n" \
"v_mfma_f32_16x16x32_bf16 v[108:111], a[152:155], a[24:27], v[108:111]\n" \
"v_exp_f32_e64 v82, v82\n" \
"v_mfma_f32_16x16x32_bf16 v[108:111], a[156:159], a[28:31], v[108:111]\n" \
"v_exp_f32_e64 v83, v83\n" \
"v_mfma_f32_16x16x32_bf16 v[104:107], a[140:143], a[28:31], v[36:39]\n" \
"v_exp_f32_e64 v80, v80\n" \
"v_mfma_f32_16x16x32_bf16 v[104:107], a[136:139], a[24:27], v[104:107]\n" \
"v_exp_f32_e64 v81, v81\n" \
"v_mfma_f32_16x16x32_bf16 v[104:107], a[132:135], a[20:23], v[104:107]\n" \
"v_exp_f32_e64 v88, v88\n" \
"v_mfma_f32_16x16x32_bf16 v[104:107], a[128:131], a[16:19], v[104:107]\n" \
"buffer_load_dwordx4 v50, s[8:11], s64 offen offset:1024 sc1 lds\n" \
"v_mfma_f32_16x16x32_bf16 v[112:115], a[128:131], a[32:35], v[40:43]\n" \
"s_xor_b32 m0, s65, 0x6000\n" \
"v_cvt_pk_bf16_f32 v86, v84, v85\n" \
"v_mfma_f32_16x16x32_bf16 v[112:115], a[132:135], a[36:39], v[112:115]\n" \
"v_cvt_pk_bf16_f32 v85, v82, v83\n" \
"v_cvt_pk_bf16_f32 v84, v80, v81\n" \
"v_mfma_f32_16x16x32_bf16 v[112:115], a[136:139], a[40:43], v[112:115]\n" \
"v_exp_f32_e64 v89, v89\n" \
"v_mfma_f32_16x16x32_bf16 v[112:115], a[140:143], a[44:47], v[112:115]\n" \
"v_exp_f32_e64 v90, v90\n" \
"v_mfma_f32_16x16x32_bf16 v[116:119], a[156:159], a[44:47], v[40:43]\n" \
"v_exp_f32_e64 v91, v91\n" \
"v_mfma_f32_16x16x32_bf16 v[116:119], a[152:155], a[40:43], v[116:119]\n" \
"v_exp_f32_e64 v92, v92\n" \
"v_mfma_f32_16x16x32_bf16 v[116:119], a[148:151], a[36:39], v[116:119]\n" \
"v_exp_f32_e64 v93, v93\n" \
"v_mfma_f32_16x16x32_bf16 v[116:119], a[144:147], a[32:35], v[116:119]\n" \
"v_exp_f32_e64 v94, v94\n" \
"v_mfma_f32_16x16x32_bf16 v[124:127], a[144:147], a[48:51], v[44:47]\n" \
"v_cvt_pk_bf16_f32 v88, v88, v89\n" \
"v_cvt_pk_bf16_f32 v89, v90, v91\n" \
"v_mfma_f32_16x16x32_bf16 v[124:127], a[148:151], a[52:55], v[124:127]\n" \
"v_exp_f32_e64 v95, v95\n" \
"v_mfma_f32_16x16x32_bf16 v[124:127], a[152:155], a[56:59], v[124:127]\n" \
"v_cvt_pk_bf16_f32 v90, v92, v93\n" \
"v_cvt_pk_bf16_f32 v91, v94, v95\n" \
"v_mfma_f32_16x16x32_bf16 v[124:127], a[156:159], a[60:63], v[124:127]\n" \
"ds_read_b128 a[144:147], v48 offset:4096\n" \
"ds_read_b128 a[148:151], v48 offset:5120\n" \
"v_mfma_f32_16x16x32_bf16 v[120:123], a[140:143], a[60:63], v[44:47]\n" \
"ds_read_b128 a[152:155], v48 offset:6144\n" \
"ds_read_b128 a[140:143], v48 offset:3072\n" \
"v_mfma_f32_16x16x32_bf16 v[120:123], a[136:139], a[56:59], v[120:123]\n" \
"ds_read_b128 a[136:139], v48 offset:2048\n" \
"ds_read_b128 a[156:159], v48 offset:7168\n" \
"v_mfma_f32_16x16x32_bf16 v[120:123], a[132:135], a[52:55], v[120:123]\n" \
"buffer_load_dwordx4 v51, s[8:11], s64 offen offset:0 sc1 lds\n" \
"v_mfma_f32_16x16x32_bf16 v[120:123], a[128:131], a[48:51], v[120:123]\n" \
"ds_read_b128 a[128:131], v48 offset:0\n" \
"s_waitcnt lgkmcnt(7)\n" \
"v_mfma_f32_16x16x32_bf16 v[128:131], a[160:163], v[68:71], v[128:131]\n" \
"v_exp_f32_e32 v102, v102\n" \
"v_mfma_f32_16x16x32_bf16 v[160:163], a[160:163], v[72:75], v[160:163]\n" \
"v_exp_f32_e64 v103, v103\n" \
"v_mfma_f32_16x16x32_bf16 v[132:135], a[164:167], v[68:71], v[132:135]\n" \
"ds_read_b128 a[132:135], v48 offset:1024\n" \
"v_cvt_pk_bf16_f32 v103, v102, v103\n" \
"v_mfma_f32_16x16x32_bf16 v[164:167], a[164:167], v[72:75], v[164:167]\n" \
"v_exp_f32_e64 v100, v100\n" \
"v_mfma_f32_16x16x32_bf16 v[136:139], a[168:171], v[68:71], v[136:139]\n" \
"v_exp_f32_e64 v101, v101\n" \
"v_mfma_f32_16x16x32_bf16 v[168:171], a[168:171], v[72:75], v[168:171]\n" \
"v_exp_f32_e64 v98, v98\n" \
"v_mfma_f32_16x16x32_bf16 v[140:143], a[172:175], v[68:71], v[140:143]\n" \
"v_exp_f32_e64 v99, v99\n" \
"v_mfma_f32_16x16x32_bf16 v[172:175], a[172:175], v[72:75], v[172:175]\n" \
"v_exp_f32_e64 v96, v96\n" \
"v_mfma_f32_16x16x32_bf16 v[144:147], a[176:179], v[68:71], v[144:147]\n" \
"v_exp_f32_e64 v97, v97\n" \
"v_mfma_f32_16x16x32_bf16 v[176:179], a[176:179], v[72:75], v[176:179]\n" \
"v_exp_f32_e64 v104, v104\n" \
"v_mfma_f32_16x16x32_bf16 v[148:151], a[180:183], v[68:71], v[148:151]\n" \
"v_exp_f32_e64 v105, v105\n" \
"v_mfma_f32_16x16x32_bf16 v[180:183], a[180:183], v[72:75], v[180:183]\n" \
"v_cvt_pk_bf16_f32 v102, v100, v101\n" \
"v_cvt_pk_bf16_f32 v101, v98, v99\n" \
"v_mfma_f32_16x16x32_bf16 v[152:155], a[184:187], v[68:71], v[152:155]\n" \
"v_cvt_pk_bf16_f32 v100, v96, v97\n" \
"v_cvt_pk_bf16_f32 v104, v104, v105\n" \
"v_mfma_f32_16x16x32_bf16 v[184:187], a[184:187], v[72:75], v[184:187]\n" \
"buffer_load_dwordx4 v51, s[8:11], s64 offen offset:1024 sc1 lds\n" \
"v_mfma_f32_16x16x32_bf16 v[156:159], a[188:191], v[68:71], v[156:159]\n" \
"v_exp_f32_e64 v106, v106\n" \
"v_mfma_f32_16x16x32_bf16 v[188:191], a[188:191], v[72:75], v[188:191]\n" \
"v_exp_f32_e64 v107, v107\n" \
"v_smfmac_f32_16x16x64_bf16 a[192:195], a[200:203], v[68:75], v21\n" \
"v_exp_f32_e64 v108, v108\n" \
"v_mfma_f32_16x16x32_bf16 v[220:223], a[188:191], v[84:87], v[220:223]\n" \
"v_exp_f32_e64 v109, v109\n" \
"v_mfma_f32_16x16x32_bf16 v[252:255], a[188:191], v[88:91], v[252:255]\n" \
"v_exp_f32_e64 v110, v110\n" \
"v_mfma_f32_16x16x32_bf16 v[216:219], a[184:187], v[84:87], v[216:219]\n" \
"v_exp_f32_e64 v111, v111\n" \
"v_mfma_f32_16x16x32_bf16 v[248:251], a[184:187], v[88:91], v[248:251]\n" \
"v_exp_f32_e64 v118, v118\n" \
"v_mfma_f32_16x16x32_bf16 v[212:215], a[180:183], v[84:87], v[212:215]\n" \
"v_exp_f32_e64 v119, v119\n" \
"v_mfma_f32_16x16x32_bf16 v[244:247], a[180:183], v[88:91], v[244:247]\n" \
"v_cvt_pk_bf16_f32 v105, v106, v107\n" \
"v_cvt_pk_bf16_f32 v106, v108, v109\n" \
"v_mfma_f32_16x16x32_bf16 v[208:211], a[176:179], v[84:87], v[208:211]\n" \
"v_cvt_pk_bf16_f32 v107, v110, v111\n" \
"v_cvt_pk_bf16_f32 v119, v118, v119\n" \
"v_mfma_f32_16x16x32_bf16 v[240:243], a[176:179], v[88:91], v[240:243]\n" \
"v_exp_f32_e64 v116, v116\n" \
"v_mfma_f32_16x16x32_bf16 v[204:207], a[172:175], v[84:87], v[204:207]\n" \
"v_exp_f32_e64 v117, v117\n" \
"v_mfma_f32_16x16x32_bf16 v[236:239], a[172:175], v[88:91], v[236:239]\n" \
"v_exp_f32_e64 v114, v114\n" \
"v_mfma_f32_16x16x32_bf16 v[200:203], a[168:171], v[84:87], v[200:203]\n" \
"v_exp_f32_e64 v115, v115\n" \
"v_mfma_f32_16x16x32_bf16 v[232:235], a[168:171], v[88:91], v[232:235]\n" \
"v_exp_f32_e64 v112, v112\n" \
"v_mfma_f32_16x16x32_bf16 v[196:199], a[164:167], v[84:87], v[196:199]\n" \
"v_exp_f32_e64 v113, v113\n" \
"v_mfma_f32_16x16x32_bf16 v[228:231], a[164:167], v[88:91], v[228:231]\n" \
"v_exp_f32_e64 v120, v120\n" \
"v_mfma_f32_16x16x32_bf16 v[192:195], a[160:163], v[84:87], v[192:195]\n" \
"v_exp_f32_e64 v121, v121\n" \
"v_mfma_f32_16x16x32_bf16 v[224:227], a[160:163], v[88:91], v[224:227]\n" \
"v_cvt_pk_bf16_f32 v118, v116, v117\n" \
"v_cvt_pk_bf16_f32 v117, v114, v115\n" \
"v_smfmac_f32_16x16x64_bf16 a[196:199], a[200:203], v[84:91], v21\n" \
"v_cvt_pk_bf16_f32 v116, v112, v113\n" \
"v_cvt_pk_bf16_f32 v120, v120, v121\n" \
"s_waitcnt vmcnt(4) lgkmcnt(0)\n" \
"s_barrier\n" \
"v_mfma_f32_16x16x32_bf16 v[64:67], a[128:131], a[0:3], v[32:35]\n" \
"s_xor_b32 m0, s65, 0x14000\n" \
"ds_read_b128 a[160:163], v49 offset:8192\n" \
"v_mfma_f32_16x16x32_bf16 v[64:67], a[132:135], a[4:7], v[64:67]\n" \
"buffer_load_dwordx4 v52, s[8:11], s64 offen offset:0 sc1 lds\n" \
"v_mfma_f32_16x16x32_bf16 v[64:67], a[136:139], a[8:11], v[64:67]\n" \
"ds_read_b128 a[164:167], v49 offset:9216\n" \
"ds_read_b128 a[168:171], v49 offset:10240\n" \
"v_mfma_f32_16x16x32_bf16 v[64:67], a[140:143], a[12:15], v[64:67]\n" \
"ds_read_b128 a[172:175], v49 offset:11264\n" \
"ds_read_b128 a[176:179], v49 offset:12288\n" \
"v_mfma_f32_16x16x32_bf16 v[68:71], a[156:159], a[12:15], v[32:35]\n" \
"ds_read_b128 a[180:183], v49 offset:13312\n" \
"ds_read_b128 a[184:187], v49 offset:14336\n" \
"v_mfma_f32_16x16x32_bf16 v[68:71], a[152:155], a[8:11], v[68:71]\n" \
"v_exp_f32_e64 v122, v122\n" \
"v_mfma_f32_16x16x32_bf16 v[68:71], a[148:151], a[4:7], v[68:71]\n" \
"v_exp_f32_e64 v123, v123\n" \
"v_mfma_f32_16x16x32_bf16 v[68:71], a[144:147], a[0:3], v[68:71]\n" \
"ds_read_b128 a[188:191], v49 offset:15360\n" \
"v_cvt_pk_bf16_f32 v121, v122, v123\n" \
"v_mfma_f32_16x16x32_bf16 v[76:79], a[144:147], a[16:19], v[36:39]\n" \
"v_exp_f32_e64 v124, v124\n" \
"v_mfma_f32_16x16x32_bf16 v[76:79], a[148:151], a[20:23], v[76:79]\n" \
"v_exp_f32_e64 v125, v125\n" \
"v_mfma_f32_16x16x32_bf16 v[76:79], a[152:155], a[24:27], v[76:79]\n" \
"v_exp_f32_e64 v126, v126\n" \
"v_mfma_f32_16x16x32_bf16 v[76:79], a[156:159], a[28:31], v[76:79]\n" \
"v_exp_f32_e64 v127, v127\n" \
"v_mfma_f32_16x16x32_bf16 v[72:75], a[140:143], a[28:31], v[36:39]\n" \
"v_cvt_pk_bf16_f32 v122, v124, v125\n" \
"v_cvt_pk_bf16_f32 v123, v126, v127\n" \
"v_mfma_f32_16x16x32_bf16 v[72:75], a[136:139], a[24:27], v[72:75]\n" \
"v_exp_f32_e64 v70, v70\n" \
"v_mfma_f32_16x16x32_bf16 v[72:75], a[132:135], a[20:23], v[72:75]\n" \
"v_exp_f32_e64 v71, v71\n" \
"v_mfma_f32_16x16x32_bf16 v[72:75], a[128:131], a[16:19], v[72:75]\n" \
"buffer_load_dwordx4 v52, s[8:11], s64 offen offset:1024 sc1 lds\n" \
"v_mfma_f32_16x16x32_bf16 v[80:83], a[128:131], a[32:35], v[40:43]\n" \
"s_mov_b32 m0, s65\n" \
"v_cvt_pk_bf16_f32 v71, v70, v71\n" \
"v_mfma_f32_16x16x32_bf16 v[80:83], a[132:135], a[36:39], v[80:83]\n" \
"v_exp_f32_e32 v68, v68\n" \
"v_mfma_f32_16x16x32_bf16 v[80:83], a[136:139], a[40:43], v[80:83]\n" \
"v_exp_f32_e64 v69, v69\n" \
"v_mfma_f32_16x16x32_bf16 v[80:83], a[140:143], a[44:47], v[80:83]\n" \
"v_exp_f32_e64 v72, v72\n" \
"v_mfma_f32_16x16x32_bf16 v[84:87], a[156:159], a[44:47], v[40:43]\n" \
"v_exp_f32_e64 v73, v73\n" \
"v_mfma_f32_16x16x32_bf16 v[84:87], a[152:155], a[40:43], v[84:87]\n" \
"v_exp_f32_e64 v74, v74\n" \
"v_mfma_f32_16x16x32_bf16 v[84:87], a[148:151], a[36:39], v[84:87]\n" \
"v_exp_f32_e64 v75, v75\n" \
"v_mfma_f32_16x16x32_bf16 v[84:87], a[144:147], a[32:35], v[84:87]\n" \
"v_exp_f32_e64 v66, v66\n" \
"v_mfma_f32_16x16x32_bf16 v[92:95], a[144:147], a[48:51], v[44:47]\n" \
"ds_read_b128 a[144:147], v48 offset:12288\n" \
"v_cvt_pk_bf16_f32 v72, v72, v73\n" \
"v_mfma_f32_16x16x32_bf16 v[92:95], a[148:151], a[52:55], v[92:95]\n" \
"ds_read_b128 a[148:151], v48 offset:13312\n" \
"v_cvt_pk_bf16_f32 v70, v68, v69\n" \
"v_mfma_f32_16x16x32_bf16 v[92:95], a[152:155], a[56:59], v[92:95]\n" \
"ds_read_b128 a[152:155], v48 offset:14336\n" \
"v_cvt_pk_bf16_f32 v73, v74, v75\n" \
"v_mfma_f32_16x16x32_bf16 v[92:95], a[156:159], a[60:63], v[92:95]\n" \
"ds_read_b128 a[156:159], v48 offset:15360\n" \
"v_add_u32_e32 v49, 0x10000, v48\n" \
"v_mfma_f32_16x16x32_bf16 v[88:91], a[140:143], a[60:63], v[44:47]\n" \
"ds_read_b128 a[140:143], v48 offset:11264\n" \
"s_xor_b32 s65, s65, 0x4000\n" \
"v_mfma_f32_16x16x32_bf16 v[88:91], a[136:139], a[56:59], v[88:91]\n" \
"v_exp_f32_e64 v67, v67\n" \
"v_mfma_f32_16x16x32_bf16 v[88:91], a[132:135], a[52:55], v[88:91]\n" \
"buffer_load_dwordx4 v53, s[8:11], s64 offen offset:0 sc1 lds\n" \
"v_mfma_f32_16x16x32_bf16 v[88:91], a[128:131], a[48:51], v[88:91]\n" \
"ds_read_b128 a[128:131], v48 offset:8192\n" \
"s_waitcnt lgkmcnt(6)\n" \
"v_mfma_f32_16x16x32_bf16 v[128:131], a[160:163], v[100:103], v[128:131]\n" \
"ds_read_b128 a[132:135], v48 offset:9216\n" \
"ds_read_b128 a[136:139], v48 offset:10240\n" \
"v_mfma_f32_16x16x32_bf16 v[160:163], a[160:163], v[104:107], v[160:163]\n" \
"v_exp_f32_e32 v64, v64\n" \
"v_mfma_f32_16x16x32_bf16 v[132:135], a[164:167], v[100:103], v[132:135]\n" \
"v_exp_f32_e64 v65, v65\n" \
"v_mfma_f32_16x16x32_bf16 v[164:167], a[164:167], v[104:107], v[164:167]\n" \
"v_exp_f32_e64 v76, v76\n" \
"v_mfma_f32_16x16x32_bf16 v[136:139], a[168:171], v[100:103], v[136:139]\n" \
"v_exp_f32_e64 v77, v77\n" \
"v_mfma_f32_16x16x32_bf16 v[168:171], a[168:171], v[104:107], v[168:171]\n" \
"v_exp_f32_e64 v78, v78\n" \
"v_mfma_f32_16x16x32_bf16 v[140:143], a[172:175], v[100:103], v[140:143]\n" \
"v_exp_f32_e64 v79, v79\n" \
"v_mfma_f32_16x16x32_bf16 v[172:175], a[172:175], v[104:107], v[172:175]\n" \
"v_cvt_pk_bf16_f32 v69, v66, v67\n" \
"v_cvt_pk_bf16_f32 v68, v64, v65\n" \
"v_mfma_f32_16x16x32_bf16 v[144:147], a[176:179], v[100:103], v[144:147]\n" \
"v_cvt_pk_bf16_f32 v74, v76, v77\n" \
"v_cvt_pk_bf16_f32 v75, v78, v79\n" \
"v_mfma_f32_16x16x32_bf16 v[176:179], a[176:179], v[104:107], v[176:179]\n" \
"v_xor_b32_e32 v48, 0x4000, v48\n" \
"v_mfma_f32_16x16x32_bf16 v[148:151], a[180:183], v[100:103], v[148:151]\n" \
"v_mfma_f32_16x16x32_bf16 v[180:183], a[180:183], v[104:107], v[180:183]\n" \
"v_mfma_f32_16x16x32_bf16 v[152:155], a[184:187], v[100:103], v[152:155]\n" \
"v_mfma_f32_16x16x32_bf16 v[184:187], a[184:187], v[104:107], v[184:187]\n" \
"buffer_load_dwordx4 v53, s[8:11], s64 offen offset:1024 sc1 lds\n" \
"v_mfma_f32_16x16x32_bf16 v[156:159], a[188:191], v[100:103], v[156:159]\n" \
"s_add_u32 s64, s64, 0x8000\n" \
"s_sub_u32 s66, s66, 1\n" \
"s_nop 0\n" \
"v_mfma_f32_16x16x32_bf16 v[188:191], a[188:191], v[104:107], v[188:191]\n" \
"v_smfmac_f32_16x16x64_bf16 a[192:195], a[200:203], v[100:107], v21\n" \
"v_mfma_f32_16x16x32_bf16 v[220:223], a[188:191], v[116:119], v[220:223]\n" \
"v_mfma_f32_16x16x32_bf16 v[252:255], a[188:191], v[120:123], v[252:255]\n" \
"v_mfma_f32_16x16x32_bf16 v[216:219], a[184:187], v[116:119], v[216:219]\n" \
"v_mfma_f32_16x16x32_bf16 v[248:251], a[184:187], v[120:123], v[248:251]\n" \
"v_mfma_f32_16x16x32_bf16 v[212:215], a[180:183], v[116:119], v[212:215]\n" \
"v_mfma_f32_16x16x32_bf16 v[244:247], a[180:183], v[120:123], v[244:247]\n" \
"v_mfma_f32_16x16x32_bf16 v[208:211], a[176:179], v[116:119], v[208:211]\n" \
"v_mfma_f32_16x16x32_bf16 v[240:243], a[176:179], v[120:123], v[240:243]\n" \
"v_mfma_f32_16x16x32_bf16 v[204:207], a[172:175], v[116:119], v[204:207]\n" \
"v_mfma_f32_16x16x32_bf16 v[236:239], a[172:175], v[120:123], v[236:239]\n" \
"v_mfma_f32_16x16x32_bf16 v[200:203], a[168:171], v[116:119], v[200:203]\n" \
"v_mfma_f32_16x16x32_bf16 v[232:235], a[168:171], v[120:123], v[232:235]\n" \
"v_mfma_f32_16x16x32_bf16 v[196:199], a[164:167], v[116:119], v[196:199]\n" \
"v_mfma_f32_16x16x32_bf16 v[228:231], a[164:167], v[120:123], v[228:231]\n" \
"v_mfma_f32_16x16x32_bf16 v[192:195], a[160:163], v[116:119], v[192:195]\n" \
"v_mfma_f32_16x16x32_bf16 v[224:227], a[160:163], v[120:123], v[224:227]\n" \
"s_cbranch_scc0 L_tL_1_%=\n" \
"s_waitcnt vmcnt(4) lgkmcnt(0)\n" \
"v_smfmac_f32_16x16x64_bf16 a[196:199], a[200:203], v[116:123], v21\n" \
"s_barrier\n" \
"L_tr_%=:\n" \
"s_setpc_b64 s[28:29]\n" \
".fill (L_ab + 4096 - .) / 4, 4, 0xBF800000\n" \
".fill (L_ab + 4096 - .) / 4, 4, 0xBF800000\n" \
"L_ap1:\n" \
"s_setpc_b64 s[88:89]\n" \
"L_gtr:\n" \
"v_mfma_f32_16x16x32_bf16 v[96:99], a[128:131], a[0:3], v[32:35]\n" \
"v_mov_b32_e32 v56, s62\n" \
"v_readlane_b32 s68, v11, 0\n" \
"v_mfma_f32_16x16x32_bf16 v[96:99], a[132:135], a[4:7], v[96:99]\n" \
"s_xor_b32 m0, s65, 0x12000\n" \
"s_mov_b32 s65, s13\n" \
"v_mfma_f32_16x16x32_bf16 v[96:99], a[136:139], a[8:11], v[96:99]\n" \
"buffer_load_dwordx4 v50, s[8:11], s64 offen offset:0 sc1 lds\n" \
"v_mfma_f32_16x16x32_bf16 v[96:99], a[140:143], a[12:15], v[96:99]\n" \
"v_add_u32_e32 v63, s15, v20\n" \
"s_add_u32 s67, s14, 40960\n" \
"v_mfma_f32_16x16x32_bf16 v[100:103], a[156:159], a[12:15], v[32:35]\n" \
"buffer_load_dwordx4 v50, s[8:11], s64 offen offset:1024 sc1 lds\n" \
"s_mov_b64 exec, 1\n" \
"buffer_store_dword v56, off, s[56:59], s68 offset:0 sc1\n" \
"s_mov_b64 exec, -1\n" \
"v_mfma_f32_16x16x32_bf16 v[100:103], a[152:155], a[8:11], v[100:103]\n" \
"v_add_u32_e32 v50, 0x6000, v63\n" \
"s_xor_b32 m0, s65, 0x8000\n" \
"v_mfma_f32_16x16x32_bf16 v[100:103], a[148:151], a[4:7], v[100:103]\n" \
"ds_read_b128 a[160:163], v49 offset:0\n" \
"ds_read_b128 a[164:167], v49 offset:1024\n" \
"v_mfma_f32_16x16x32_bf16 v[100:103], a[144:147], a[0:3], v[100:103]\n" \
"ds_read_b128 a[168:171], v49 offset:2048\n" \
"ds_read_b128 a[172:175], v49 offset:3072\n" \
"v_mfma_f32_16x16x32_bf16 v[108:111], a[144:147], a[16:19], v[36:39]\n" \
"buffer_load_dwordx4 v50, s[8:11], s67 offen offset:0 sc1 lds\n" \
"v_mfma_f32_16x16x32_bf16 v[108:111], a[148:151], a[20:23], v[108:111]\n" \
"ds_read_b128 a[176:179], v49 offset:4096\n" \
"ds_read_b128 a[180:183], v49 offset:5120\n" \
"v_mfma_f32_16x16x32_bf16 v[108:111], a[152:155], a[24:27], v[108:111]\n" \
"ds_read_b128 a[184:187], v49 offset:6144\n" \
"ds_read_b128 a[188:191], v49 offset:7168\n" \
"v_mfma_f32_16x16x32_bf16 v[108:111], a[156:159], a[28:31], v[108:111]\n" \
"s_mov_b32 s64, s14\n" \
"v_add_u32_e32 v51, 0x12000, v63\n" \
"v_mfma_f32_16x16x32_bf16 v[104:107], a[140:143], a[28:31], v[36:39]\n" \
"buffer_load_dwordx4 v50, s[8:11], s67 offen offset:1024 sc1 lds\n" \
"v_mfma_f32_16x16x32_bf16 v[104:107], a[136:139], a[24:27], v[104:107]\n" \
"v_exp_f32_e32 v86, v86\n" \
"v_mfma_f32_16x16x32_bf16 v[104:107], a[132:135], a[20:23], v[104:107]\n" \
"v_exp_f32_e32 v87, v87\n" \
"v_mfma_f32_16x16x32_bf16 v[104:107], a[128:131], a[16:19], v[104:107]\n" \
"v_exp_f32_e32 v84, v84\n" \
"v_mfma_f32_16x16x32_bf16 v[112:115], a[128:131], a[32:35], v[40:43]\n" \
"v_exp_f32_e32 v85, v85\n" \
"v_mfma_f32_16x16x32_bf16 v[112:115], a[132:135], a[36:39], v[112:115]\n" \
"buffer_load_dwordx4 v50, s[8:11], s67 offen offset:2048 sc1 lds\n" \
"v_mfma_f32_16x16x32_bf16 v[112:115], a[136:139], a[40:43], v[112:115]\n" \
"v_cvt_pk_bf16_f32 v87, v86, v87\n" \
"v_cvt_pk_bf16_f32 v86, v84, v85\n" \
"v_mfma_f32_16x16x32_bf16 v[112:115], a[140:143], a[44:47], v[112:115]\n" \
"v_exp_f32_e32 v82, v82\n" \
"v_mfma_f32_16x16x32_bf16 v[116:119], a[156:159], a[44:47], v[40:43]\n" \
"v_exp_f32_e32 v83, v83\n" \
"v_mfma_f32_16x16x32_bf16 v[116:119], a[152:155], a[40:43], v[116:119]\n" \
"buffer_load_dwordx4 v50, s[8:11], s67 offen offset:3072 sc1 lds\n" \
"v_mfma_f32_16x16x32_bf16 v[116:119], a[148:151], a[36:39], v[116:119]\n" \
"s_xor_b32 m0, s65, 0xa000\n" \
"v_cvt_pk_bf16_f32 v85, v82, v83\n" \
"v_mfma_f32_16x16x32_bf16 v[116:119], a[144:147], a[32:35], v[116:119]\n" \
"v_exp_f32_e32 v80, v80\n" \
"v_mfma_f32_16x16x32_bf16 v[124:127], a[144:147], a[48:51], v[44:47]\n" \
"v_exp_f32_e32 v81, v81\n" \
"v_mfma_f32_16x16x32_bf16 v[124:127], a[148:151], a[52:55], v[124:127]\n" \
"buffer_load_dwordx4 v51, s[8:11], s64 offen offset:0 sc1 lds\n" \
"v_mfma_f32_16x16x32_bf16 v[124:127], a[152:155], a[56:59], v[124:127]\n" \
"v_cvt_pk_bf16_f32 v84, v80, v81\n" \
"v_readlane_b32 s93, v11, 19\n" \
"v_mfma_f32_16x16x32_bf16 v[124:127], a[156:159], a[60:63], v[124:127]\n" \
"v_exp_f32_e32 v88, v88\n" \
"v_mfma_f32_16x16x32_bf16 v[120:123], a[140:143], a[60:63], v[44:47]\n" \
"v_exp_f32_e32 v89, v89\n" \
"v_mfma_f32_16x16x32_bf16 v[120:123], a[136:139], a[56:59], v[120:123]\n" \
"v_exp_f32_e32 v90, v90\n" \
"v_mfma_f32_16x16x32_bf16 v[120:123], a[132:135], a[52:55], v[120:123]\n" \
"buffer_load_dwordx4 v51, s[8:11], s64 offen offset:1024 sc1 lds\n" \
"v_mfma_f32_16x16x32_bf16 v[120:123], a[128:131], a[48:51], v[120:123]\n" \
"v_cvt_pk_bf16_f32 v88, v88, v89\n" \
"s_waitcnt lgkmcnt(0)\n" \
"v_mfma_f32_16x16x32_bf16 v[128:131], a[160:163], v[68:71], v[128:131]\n" \
"v_exp_f32_e32 v91, v91\n" \
"v_mfma_f32_16x16x32_bf16 v[160:163], a[160:163], v[72:75], v[160:163]\n" \
"v_exp_f32_e32 v92, v92\n" \
"v_mfma_f32_16x16x32_bf16 v[132:135], a[164:167], v[68:71], v[132:135]\n" \
"buffer_load_dwordx4 v51, s[8:11], s64 offen offset:2048 sc1 lds\n" \
"v_mfma_f32_16x16x32_bf16 v[164:167], a[164:167], v[72:75], v[164:167]\n" \
"v_exp_f32_e32 v93, v93\n" \
"v_mfma_f32_16x16x32_bf16 v[136:139], a[168:171], v[68:71], v[136:139]\n" \
"v_exp_f32_e32 v94, v94\n" \
"v_mfma_f32_16x16x32_bf16 v[168:171], a[168:171], v[72:75], v[168:171]\n" \
"v_exp_f32_e32 v95, v95\n" \
"v_mfma_f32_16x16x32_bf16 v[140:143], a[172:175], v[68:71], v[140:143]\n" \
"buffer_load_dwordx4 v51, s[8:11], s64 offen offset:3072 sc1 lds\n" \
"v_mfma_f32_16x16x32_bf16 v[172:175], a[172:175], v[72:75], v[172:175]\n" \
"v_cvt_pk_bf16_f32 v89, v90, v91\n" \
"v_cvt_pk_bf16_f32 v90, v92, v93\n" \
"v_mfma_f32_16x16x32_bf16 v[144:147], a[176:179], v[68:71], v[144:147]\n" \
"v_cvt_pk_bf16_f32 v91, v94, v95\n" \
"v_readlane_b32 s80, v11, 11\n" \
"v_mfma_f32_16x16x32_bf16 v[176:179], a[176:179], v[72:75], v[176:179]\n" \
"v_readlane_b32 s81, v11, 12\n" \
"s_mov_b32 s82, 0x47fca0\n" \
"v_mfma_f32_16x16x32_bf16 v[148:151], a[180:183], v[68:71], v[148:151]\n" \
"s_mov_b32 s83, 0x20000\n" \
"v_readlane_b32 s92, v11, 26\n" \
"v_mfma_f32_16x16x32_bf16 v[180:183], a[180:183], v[72:75], v[180:183]\n" \
"s_add_u32 m0, s93, 0x0\n" \
"ds_read_b128 a[128:131], v48 offset:0\n" \
"v_mfma_f32_16x16x32_bf16 v[152:155], a[184:187], v[68:71], v[152:155]\n" \
"ds_read_b128 a[144:147], v48 offset:4096\n" \
"ds_read_b128 a[132:135], v48 offset:1024\n" \
"v_mfma_f32_16x16x32_bf16 v[184:187], a[184:187], v[72:75], v[184:187]\n" \
"buffer_load_dwordx4 v0, s[80:83], s92 offen offset:0 lds\n" \
"v_mfma_f32_16x16x32_bf16 v[156:159], a[188:191], v[68:71], v[156:159]\n" \
"ds_read_b128 a[148:151], v48 offset:5120\n" \
"ds_read_b128 a[136:139], v48 offset:2048\n" \
"v_mfma_f32_16x16x32_bf16 v[188:191], a[188:191], v[72:75], v[188:191]\n" \
"ds_read_b128 a[152:155], v48 offset:6144\n" \
"ds_read_b128 a[140:143], v48 offset:3072\n" \
"v_smfmac_f32_16x16x64_bf16 a[192:195], a[200:203], v[68:75], v21\n" \
"ds_read_b128 a[156:159], v48 offset:7168\n" \
"v_add_u32_e32 v52, 0x14000, v63\n" \
"v_mfma_f32_16x16x32_bf16 v[220:223], a[188:191], v[84:87], v[220:223]\n" \
"buffer_load_dwordx4 v1, s[80:83], s92 offen offset:1024 lds\n" \
"v_mfma_f32_16x16x32_bf16 v[252:255], a[188:191], v[88:91], v[252:255]\n" \
"v_exp_f32_e32 v102, v102\n" \
"v_mfma_f32_16x16x32_bf16 v[216:219], a[184:187], v[84:87], v[216:219]\n" \
"buffer_load_dwordx4 v2, s[80:83], s92 offen offset:2048 lds\n" \
"v_mfma_f32_16x16x32_bf16 v[248:251], a[184:187], v[88:91], v[248:251]\n" \
"v_exp_f32_e32 v103, v103\n" \
"v_mfma_f32_16x16x32_bf16 v[212:215], a[180:183], v[84:87], v[212:215]\n" \
"v_add_u32_e32 v53, 0x20000, v63\n" \
"v_cvt_pk_bf16_f32 v103, v102, v103\n" \
"v_mfma_f32_16x16x32_bf16 v[244:247], a[180:183], v[88:91], v[244:247]\n" \
"v_exp_f32_e32 v100, v100\n" \
"v_mfma_f32_16x16x32_bf16 v[208:211], a[176:179], v[84:87], v[208:211]\n" \
"buffer_load_dwordx4 v3, s[80:83], s92 offen offset:3072 lds\n" \
"v_mfma_f32_16x16x32_bf16 v[240:243], a[176:179], v[88:91], v[240:243]\n" \
"v_exp_f32_e32 v101, v101\n" \
"v_mfma_f32_16x16x32_bf16 v[204:207], a[172:175], v[84:87], v[204:207]\n" \
"v_exp_f32_e32 v98, v98\n" \
"v_mfma_f32_16x16x32_bf16 v[236:239], a[172:175], v[88:91], v[236:239]\n" \
"v_exp_f32_e32 v99, v99\n" \
"v_mfma_f32_16x16x32_bf16 v[200:203], a[168:171], v[84:87], v[200:203]\n" \
"v_exp_f32_e32 v96, v96\n" \
"v_mfma_f32_16x16x32_bf16 v[232:235], a[168:171], v[88:91], v[232:235]\n" \
"v_exp_f32_e32 v97, v97\n" \
"v_mfma_f32_16x16x32_bf16 v[196:199], a[164:167], v[84:87], v[196:199]\n" \
"v_exp_f32_e32 v104, v104\n" \
"v_mfma_f32_16x16x32_bf16 v[228:231], a[164:167], v[88:91], v[228:231]\n" \
"v_exp_f32_e32 v105, v105\n" \
"v_mfma_f32_16x16x32_bf16 v[192:195], a[160:163], v[84:87], v[192:195]\n" \
"v_cvt_pk_bf16_f32 v102, v100, v101\n" \
"v_cvt_pk_bf16_f32 v101, v98, v99\n" \
"v_mfma_f32_16x16x32_bf16 v[224:227], a[160:163], v[88:91], v[224:227]\n" \
"v_cvt_pk_bf16_f32 v100, v96, v97\n" \
"v_cvt_pk_bf16_f32 v104, v104, v105\n" \
"v_smfmac_f32_16x16x64_bf16 a[196:199], a[200:203], v[84:91], v21\n" \
"v_exp_f32_e32 v106, v106\n" \
"s_waitcnt vmcnt(15) lgkmcnt(0)\n" \
"s_barrier\n" \
"v_mfma_f32_16x16x32_bf16 v[64:67], a[128:131], a[0:3], v[32:35]\n" \
"s_xor_b32 m0, s65, 0x18000\n" \
"ds_read_b128 a[160:163], v49 offset:8192\n" \
"v_mfma_f32_16x16x32_bf16 v[64:67], a[132:135], a[4:7], v[64:67]\n" \
"buffer_load_dwordx4 v52, s[8:11], s64 offen offset:0 sc1 lds\n" \
"v_mfma_f32_16x16x32_bf16 v[64:67], a[136:139], a[8:11], v[64:67]\n" \
"ds_read_b128 a[164:167], v49 offset:9216\n" \
"ds_read_b128 a[168:171], v49 offset:10240\n" \
"v_mfma_f32_16x16x32_bf16 v[64:67], a[140:143], a[12:15], v[64:67]\n" \
"ds_read_b128 a[172:175], v49 offset:11264\n" \
"ds_read_b128 a[176:179], v49 offset:12288\n" \
"v_mfma_f32_16x16x32_bf16 v[68:71], a[156:159], a[12:15], v[32:35]\n" \
"buffer_load_dwordx4 v52, s[8:11], s64 offen offset:1024 sc1 lds\n" \
"v_mfma_f32_16x16x32_bf16 v[68:71], a[152:155], a[8:11], v[68:71]\n" \
"ds_read_b128 a[180:183], v49 offset:13312\n" \
"ds_read_b128 a[184:187], v49 offset:14336\n" \
"v_mfma_f32_16x16x32_bf16 v[68:71], a[148:151], a[4:7], v[68:71]\n" \
"v_exp_f32_e32 v107, v107\n" \
"v_mfma_f32_16x16x32_bf16 v[68:71], a[144:147], a[0:3], v[68:71]\n" \
"v_exp_f32_e32 v108, v108\n" \
"v_mfma_f32_16x16x32_bf16 v[76:79], a[144:147], a[16:19], v[36:39]\n" \
"v_exp_f32_e32 v109, v109\n" \
"v_mfma_f32_16x16x32_bf16 v[76:79], a[148:151], a[20:23], v[76:79]\n" \
"buffer_load_dwordx4 v52, s[8:11], s64 offen offset:2048 sc1 lds\n" \
"v_mfma_f32_16x16x32_bf16 v[76:79], a[152:155], a[24:27], v[76:79]\n" \
"v_exp_f32_e32 v110, v110\n" \
"v_mfma_f32_16x16x32_bf16 v[76:79], a[156:159], a[28:31], v[76:79]\n" \
"v_exp_f32_e32 v111, v111\n" \
"v_mfma_f32_16x16x32_bf16 v[72:75], a[140:143], a[28:31], v[36:39]\n" \
"v_cvt_pk_bf16_f32 v105, v106, v107\n" \
"v_cvt_pk_bf16_f32 v106, v108, v109\n" \
"v_mfma_f32_16x16x32_bf16 v[72:75], a[136:139], a[24:27], v[72:75]\n" \
"buffer_load_dwordx4 v52, s[8:11], s64 offen offset:3072 sc1 lds\n" \
"v_mfma_f32_16x16x32_bf16 v[72:75], a[132:135], a[20:23], v[72:75]\n" \
"s_mov_b32 m0, s65\n" \
"v_cvt_pk_bf16_f32 v107, v110, v111\n" \
"v_mfma_f32_16x16x32_bf16 v[72:75], a[128:131], a[16:19], v[72:75]\n" \
"ds_read_b128 a[188:191], v49 offset:15360\n" \
"v_add_u32_e32 v49, 0x10000, v48\n" \
"v_mfma_f32_16x16x32_bf16 v[80:83], a[128:131], a[32:35], v[40:43]\n" \
"v_exp_f32_e32 v118, v118\n" \
"v_mfma_f32_16x16x32_bf16 v[80:83], a[132:135], a[36:39], v[80:83]\n" \
"v_exp_f32_e32 v119, v119\n" \
"v_mfma_f32_16x16x32_bf16 v[80:83], a[136:139], a[40:43], v[80:83]\n" \
"buffer_load_dwordx4 v53, s[8:11], s64 offen offset:0 sc1 lds\n" \
"v_mfma_f32_16x16x32_bf16 v[80:83], a[140:143], a[44:47], v[80:83]\n" \
"v_exp_f32_e32 v116, v116\n" \
"v_mfma_f32_16x16x32_bf16 v[84:87], a[156:159], a[44:47], v[40:43]\n" \
"v_exp_f32_e32 v117, v117\n" \
"v_mfma_f32_16x16x32_bf16 v[84:87], a[152:155], a[40:43], v[84:87]\n" \
"v_cvt_pk_bf16_f32 v119, v118, v119\n" \
"v_cvt_pk_bf16_f32 v118, v116, v117\n" \
"v_mfma_f32_16x16x32_bf16 v[84:87], a[148:151], a[36:39], v[84:87]\n" \
"v_exp_f32_e32 v114, v114\n" \
"v_mfma_f32_16x16x32_bf16 v[84:87], a[144:147], a[32:35], v[84:87]\n" \
"buffer_load_dwordx4 v53, s[8:11], s64 offen offset:1024 sc1 lds\n" \
"v_mfma_f32_16x16x32_bf16 v[92:95], a[144:147], a[48:51], v[44:47]\n" \
"v_exp_f32_e32 v115, v115\n" \
"v_mfma_f32_16x16x32_bf16 v[92:95], a[148:151], a[52:55], v[92:95]\n" \
"v_exp_f32_e32 v112, v112\n" \
"v_mfma_f32_16x16x32_bf16 v[92:95], a[152:155], a[56:59], v[92:95]\n" \
"v_exp_f32_e32 v113, v113\n" \
"v_mfma_f32_16x16x32_bf16 v[92:95], a[156:159], a[60:63], v[92:95]\n" \
"buffer_load_dwordx4 v53, s[8:11], s64 offen offset:2048 sc1 lds\n" \
"v_mfma_f32_16x16x32_bf16 v[88:91], a[140:143], a[60:63], v[44:47]\n" \
"v_cvt_pk_bf16_f32 v117, v114, v115\n" \
"v_cvt_pk_bf16_f32 v116, v112, v113\n" \
"v_mfma_f32_16x16x32_bf16 v[88:91], a[136:139], a[56:59], v[88:91]\n" \
"v_exp_f32_e32 v120, v120\n" \
"v_mfma_f32_16x16x32_bf16 v[88:91], a[132:135], a[52:55], v[88:91]\n" \
"v_exp_f32_e32 v121, v121\n" \
"v_mfma_f32_16x16x32_bf16 v[88:91], a[128:131], a[48:51], v[88:91]\n" \
"v_cvt_pk_bf16_f32 v120, v120, v121\n" \
"s_waitcnt lgkmcnt(0)\n" \
"v_mfma_f32_16x16x32_bf16 v[128:131], a[160:163], v[100:103], v[128:131]\n" \
"buffer_load_dwordx4 v53, s[8:11], s64 offen offset:3072 sc1 lds\n" \
"v_mfma_f32_16x16x32_bf16 v[160:163], a[160:163], v[104:107], v[160:163]\n" \
"s_add_u32 m0, s93, 0x1000\n" \
"ds_read_b128 a[128:131], v48 offset:8192\n" \
"v_mfma_f32_16x16x32_bf16 v[132:135], a[164:167], v[100:103], v[132:135]\n" \
"v_exp_f32_e32 v122, v122\n" \
"v_mfma_f32_16x16x32_bf16 v[164:167], a[164:167], v[104:107], v[164:167]\n" \
"v_exp_f32_e32 v123, v123\n" \
"v_mfma_f32_16x16x32_bf16 v[136:139], a[168:171], v[100:103], v[136:139]\n" \
"buffer_load_dwordx4 v4, s[80:83], s92 offen offset:0 lds\n" \
"v_mfma_f32_16x16x32_bf16 v[168:171], a[168:171], v[104:107], v[168:171]\n" \
"v_exp_f32_e32 v124, v124\n" \
"v_mfma_f32_16x16x32_bf16 v[140:143], a[172:175], v[100:103], v[140:143]\n" \
"v_exp_f32_e32 v125, v125\n" \
"v_mfma_f32_16x16x32_bf16 v[172:175], a[172:175], v[104:107], v[172:175]\n" \
"v_exp_f32_e32 v126, v126\n" \
"v_mfma_f32_16x16x32_bf16 v[144:147], a[176:179], v[100:103], v[144:147]\n" \
"v_exp_f32_e32 v127, v127\n" \
"v_mfma_f32_16x16x32_bf16 v[176:179], a[176:179], v[104:107], v[176:179]\n" \
"v_cvt_pk_bf16_f32 v121, v122, v123\n" \
"v_cvt_pk_bf16_f32 v122, v124, v125\n" \
"v_mfma_f32_16x16x32_bf16 v[148:151], a[180:183], v[100:103], v[148:151]\n" \
"buffer_load_dwordx4 v5, s[80:83], s92 offen offset:1024 lds\n" \
"v_mfma_f32_16x16x32_bf16 v[180:183], a[180:183], v[104:107], v[180:183]\n" \
"v_cvt_pk_bf16_f32 v123, v126, v127\n" \
"ds_read_b128 a[144:147], v48 offset:12288\n" \
"v_mfma_f32_16x16x32_bf16 v[152:155], a[184:187], v[100:103], v[152:155]\n" \
"ds_read_b128 a[132:135], v48 offset:9216\n" \
"ds_read_b128 a[148:151], v48 offset:13312\n" \
"v_mfma_f32_16x16x32_bf16 v[184:187], a[184:187], v[104:107], v[184:187]\n" \
"ds_read_b128 a[136:139], v48 offset:10240\n" \
"ds_read_b128 a[152:155], v48 offset:14336\n" \
"v_mfma_f32_16x16x32_bf16 v[156:159], a[188:191], v[100:103], v[156:159]\n" \
"ds_read_b128 a[140:143], v48 offset:11264\n" \
"ds_read_b128 a[156:159], v48 offset:15360\n" \
"v_mfma_f32_16x16x32_bf16 v[188:191], a[188:191], v[104:107], v[188:191]\n" \
"buffer_load_dwordx4 v6, s[80:83], s92 offen offset:2048 lds\n" \
"v_smfmac_f32_16x16x64_bf16 a[192:195], a[200:203], v[100:107], v21\n" \
"v_exp_f32_e32 v70, v70\n" \
"v_mfma_f32_16x16x32_bf16 v[220:223], a[188:191], v[116:119], v[220:223]\n" \
"v_exp_f32_e32 v71, v71\n" \
"v_mfma_f32_16x16x32_bf16 v[252:255], a[188:191], v[120:123], v[252:255]\n" \
"buffer_load_dwordx4 v7, s[80:83], s92 offen offset:3072 lds\n" \
"v_mfma_f32_16x16x32_bf16 v[216:219], a[184:187], v[116:119], v[216:219]\n" \
"s_add_u32 m0, s93, 0x2000\n" \
"v_cvt_pk_bf16_f32 v71, v70, v71\n" \
"v_mfma_f32_16x16x32_bf16 v[248:251], a[184:187], v[120:123], v[248:251]\n" \
"v_exp_f32_e32 v72, v72\n" \
"v_mfma_f32_16x16x32_bf16 v[212:215], a[180:183], v[116:119], v[212:215]\n" \
"v_exp_f32_e32 v73, v73\n" \
"v_mfma_f32_16x16x32_bf16 v[244:247], a[180:183], v[120:123], v[244:247]\n" \
"v_exp_f32_e32 v68, v68\n" \
"v_mfma_f32_16x16x32_bf16 v[208:211], a[176:179], v[116:119], v[208:211]\n" \
"buffer_load_dwordx4 v8, s[80:83], s92 offen offset:0 lds\n" \
"v_mfma_f32_16x16x32_bf16 v[240:243], a[176:179], v[120:123], v[240:243]\n" \
"v_exp_f32_e32 v69, v69\n" \
"v_mfma_f32_16x16x32_bf16 v[204:207], a[172:175], v[116:119], v[204:207]\n" \
"v_exp_f32_e32 v74, v74\n" \
"v_mfma_f32_16x16x32_bf16 v[236:239], a[172:175], v[120:123], v[236:239]\n" \
"v_exp_f32_e32 v75, v75\n" \
"v_mfma_f32_16x16x32_bf16 v[200:203], a[168:171], v[116:119], v[200:203]\n" \
"v_exp_f32_e32 v66, v66\n" \
"v_mfma_f32_16x16x32_bf16 v[232:235], a[168:171], v[120:123], v[232:235]\n" \
"v_exp_f32_e32 v67, v67\n" \
"v_mfma_f32_16x16x32_bf16 v[196:199], a[164:167], v[116:119], v[196:199]\n" \
"v_exp_f32_e32 v64, v64\n" \
"v_mfma_f32_16x16x32_bf16 v[228:231], a[164:167], v[120:123], v[228:231]\n" \
"v_exp_f32_e32 v65, v65\n" \
"v_mfma_f32_16x16x32_bf16 v[192:195], a[160:163], v[116:119], v[192:195]\n" \
"v_cvt_pk_bf16_f32 v72, v72, v73\n" \
"v_cvt_pk_bf16_f32 v70, v68, v69\n" \
"v_mfma_f32_16x16x32_bf16 v[224:227], a[160:163], v[120:123], v[224:227]\n" \
"v_exp_f32_e32 v76, v76\n" \
"v_smfmac_f32_16x16x64_bf16 a[196:199], a[200:203], v[116:123], v21\n" \
"v_exp_f32_e32 v77, v77\n" \
"s_waitcnt vmcnt(13) lgkmcnt(0)\n" \
"s_barrier\n" \
"v_exp_f32_e32 v78, v78\n" \
"v_exp_f32_e32 v79, v79\n" \
"v_cvt_pk_bf16_f32 v73, v74, v75\n" \
"v_cvt_pk_bf16_f32 v69, v66, v67\n" \
"v_cvt_pk_bf16_f32 v68, v64, v65\n" \
"v_cvt_pk_bf16_f32 v74, v76, v77\n" \
"v_cvt_pk_bf16_f32 v75, v78, v79\n" \
"v_add_u32_e32 v48, s16, v20\n" \
"s_xor_b32 s65, s65, 0x8000\n" \
"s_add_u32 s64, s64, 0x10000\n" \
"s_getpc_b64 s[86:87]\n" \
"L_hpt_%=:\n" \
"s_add_u32 s86, s86, L_ap8-L_hpt_%=\n" \
"s_addc_u32 s87, s87, 0\n" \
"s_add_u32 s86, s86, s15\n" \
"s_addc_u32 s87, s87, 0\n" \
"s_mov_b64 exec, 0\n" \
"s_swappc_b64 s[88:89], s[86:87]\n" \
"s_mov_b64 exec, -1\n" \
"s_sub_u32 s66, s19, 1\n" \
"s_cbranch_scc1 L_la_%=\n" \
"s_branch L_2_%=\n" \
".fill (L_ab + 6424 - .) / 4, 4, 0xBF800000\n" \
"L_tL_2_%=:\n" \
"s_waitcnt vmcnt(8) lgkmcnt(0)\n" \
"v_smfmac_f32_16x16x64_bf16 a[196:199], a[200:203], v[116:123], v21\n" \
"s_barrier\n" \
"L_2_%=:\n" \
"v_mfma_f32_16x16x32_bf16 v[96:99], a[128:131], a[64:67], v[32:35]\n" \
"s_xor_b32 m0, s65, 0x12000\n" \
"ds_read_b128 a[160:163], v49 offset:0\n" \
"v_mfma_f32_16x16x32_bf16 v[96:99], a[132:135], a[68:71], v[96:99]\n" \
"buffer_load_dwordx4 v50, s[8:11], s64 offen offset:0 sc1 lds\n" \
"v_mfma_f32_16x16x32_bf16 v[96:99], a[136:139], a[72:75], v[96:99]\n" \
"ds_read_b128 a[164:167], v49 offset:1024\n" \
"ds_read_b128 a[168:171], v49 offset:2048\n" \
"v_mfma_f32_16x16x32_bf16 v[96:99], a[140:143], a[76:79], v[96:99]\n" \
"ds_read_b128 a[172:175], v49 offset:3072\n" \
"ds_read_b128 a[176:179], v49 offset:4096\n" \
"v_mfma_f32_16x16x32_bf16 v[100:103], a[156:159], a[76:79], v[32:35]\n" \
"ds_read_b128 a[180:183], v49 offset:5120\n" \
"ds_read_b128 a[184:187], v49 offset:6144\n" \
"v_mfma_f32_16x16x32_bf16 v[100:103], a[152:155], a[72:75], v[100:103]\n" \
"v_exp_f32_e64 v86, v86\n" \
"v_mfma_f32_16x16x32_bf16 v[100:103], a[148:151], a[68:71], v[100:103]\n" \
"v_exp_f32_e64 v87, v87\n" \
"v_mfma_f32_16x16x32_bf16 v[100:103], a[144:147], a[64:67], v[100:103]\n" \
"buffer_load_dwordx4 v50, s[8:11], s64 offen offset:1024 sc1 lds\n" \
"v_mfma_f32_16x16x32_bf16 v[108:111], a[144:147], a[80:83], v[36:39]\n" \
"ds_read_b128 a[188:191], v49 offset:7168\n" \
"v_cvt_pk_bf16_f32 v87, v86, v87\n" \
"v_mfma_f32_16x16x32_bf16 v[108:111], a[148:151], a[84:87], v[108:111]\n" \
"v_exp_f32_e64 v84, v84\n" \
"v_mfma_f32_16x16x32_bf16 v[108:111], a[152:155], a[88:91], v[108:111]\n" \
"v_exp_f32_e64 v85, v85\n" \
"v_mfma_f32_16x16x32_bf16 v[108:111], a[156:159], a[92:95], v[108:111]\n" \
"v_exp_f32_e64 v82, v82\n" \
"v_mfma_f32_16x16x32_bf16 v[104:107], a[140:143], a[92:95], v[36:39]\n" \
"v_exp_f32_e64 v83, v83\n" \
"v_mfma_f32_16x16x32_bf16 v[104:107], a[136:139], a[88:91], v[104:107]\n" \
"v_exp_f32_e64 v80, v80\n" \
"v_mfma_f32_16x16x32_bf16 v[104:107], a[132:135], a[84:87], v[104:107]\n" \
"v_exp_f32_e64 v81, v81\n" \
"v_mfma_f32_16x16x32_bf16 v[104:107], a[128:131], a[80:83], v[104:107]\n" \
"buffer_load_dwordx4 v50, s[8:11], s64 offen offset:2048 sc1 lds\n" \
"v_mfma_f32_16x16x32_bf16 v[112:115], a[128:131], a[96:99], v[40:43]\n" \
"v_exp_f32_e64 v88, v88\n" \
"v_mfma_f32_16x16x32_bf16 v[112:115], a[132:135], a[100:103], v[112:115]\n" \
"v_exp_f32_e64 v89, v89\n" \
"v_mfma_f32_16x16x32_bf16 v[112:115], a[136:139], a[104:107], v[112:115]\n" \
"v_cvt_pk_bf16_f32 v86, v84, v85\n" \
"v_cvt_pk_bf16_f32 v85, v82, v83\n" \
"v_mfma_f32_16x16x32_bf16 v[112:115], a[140:143], a[108:111], v[112:115]\n" \
"v_cvt_pk_bf16_f32 v84, v80, v81\n" \
"v_cvt_pk_bf16_f32 v88, v88, v89\n" \
"v_mfma_f32_16x16x32_bf16 v[116:119], a[156:159], a[108:111], v[40:43]\n" \
"v_exp_f32_e64 v90, v90\n" \
"v_mfma_f32_16x16x32_bf16 v[116:119], a[152:155], a[104:107], v[116:119]\n" \
"v_exp_f32_e64 v91, v91\n" \
"v_mfma_f32_16x16x32_bf16 v[116:119], a[148:151], a[100:103], v[116:119]\n" \
"buffer_load_dwordx4 v50, s[8:11], s64 offen offset:3072 sc1 lds\n" \
"v_mfma_f32_16x16x32_bf16 v[116:119], a[144:147], a[96:99], v[116:119]\n" \
"s_xor_b32 m0, s65, 0xa000\n" \
"v_cvt_pk_bf16_f32 v89, v90, v91\n" \
"v_mfma_f32_16x16x32_bf16 v[124:127], a[144:147], a[112:115], v[44:47]\n" \
"v_exp_f32_e64 v92, v92\n" \
"v_mfma_f32_16x16x32_bf16 v[124:127], a[148:151], a[116:119], v[124:127]\n" \
"v_exp_f32_e64 v93, v93\n" \
"v_mfma_f32_16x16x32_bf16 v[124:127], a[152:155], a[120:123], v[124:127]\n" \
"v_exp_f32_e64 v94, v94\n" \
"v_mfma_f32_16x16x32_bf16 v[124:127], a[156:159], a[124:127], v[124:127]\n" \
"v_exp_f32_e64 v95, v95\n" \
"v_mfma_f32_16x16x32_bf16 v[120:123], a[140:143], a[124:127], v[44:47]\n" \
"v_cvt_pk_bf16_f32 v90, v92, v93\n" \
"v_cvt_pk_bf16_f32 v91, v94, v95\n" \
"v_mfma_f32_16x16x32_bf16 v[120:123], a[136:139], a[120:123], v[120:123]\n" \
"ds_read_b128 a[144:147], v48 offset:4096\n" \
"ds_read_b128 a[148:151], v48 offset:5120\n" \
"v_mfma_f32_16x16x32_bf16 v[120:123], a[132:135], a[116:119], v[120:123]\n" \
"buffer_load_dwordx4 v51, s[8:11], s64 offen offset:0 sc1 lds\n" \
"v_mfma_f32_16x16x32_bf16 v[120:123], a[128:131], a[112:115], v[120:123]\n" \
"ds_read_b128 a[128:131], v48 offset:0\n" \
"s_waitcnt lgkmcnt(3)\n" \
"v_mfma_f32_16x16x32_bf16 v[128:131], a[160:163], v[68:71], v[128:131]\n" \
"ds_read_b128 a[132:135], v48 offset:1024\n" \
"ds_read_b128 a[136:139], v48 offset:2048\n" \
"v_mfma_f32_16x16x32_bf16 v[160:163], a[160:163], v[72:75], v[160:163]\n" \
"ds_read_b128 a[152:155], v48 offset:6144\n" \
"ds_read_b128 a[140:143], v48 offset:3072\n" \
"v_mfma_f32_16x16x32_bf16 v[132:135], a[164:167], v[68:71], v[132:135]\n" \
"v_exp_f32_e32 v102, v102\n" \
"v_mfma_f32_16x16x32_bf16 v[164:167], a[164:167], v[72:75], v[164:167]\n" \
"v_exp_f32_e64 v103, v103\n" \
"v_mfma_f32_16x16x32_bf16 v[136:139], a[168:171], v[68:71], v[136:139]\n" \
"ds_read_b128 a[156:159], v48 offset:7168\n" \
"v_cvt_pk_bf16_f32 v103, v102, v103\n" \
"v_mfma_f32_16x16x32_bf16 v[168:171], a[168:171], v[72:75], v[168:171]\n" \
"buffer_load_dwordx4 v51, s[8:11], s64 offen offset:1024 sc1 lds\n" \
"v_mfma_f32_16x16x32_bf16 v[140:143], a[172:175], v[68:71], v[140:143]\n" \
"v_exp_f32_e64 v100, v100\n" \
"v_mfma_f32_16x16x32_bf16 v[172:175], a[172:175], v[72:75], v[172:175]\n" \
"v_exp_f32_e64 v101, v101\n" \
"v_mfma_f32_16x16x32_bf16 v[144:147], a[176:179], v[68:71], v[144:147]\n" \
"v_exp_f32_e64 v98, v98\n" \
"v_mfma_f32_16x16x32_bf16 v[176:179], a[176:179], v[72:75], v[176:179]\n" \
"v_exp_f32_e64 v99, v99\n" \
"v_mfma_f32_16x16x32_bf16 v[148:151], a[180:183], v[68:71], v[148:151]\n" \
"v_exp_f32_e64 v96, v96\n" \
"v_mfma_f32_16x16x32_bf16 v[180:183], a[180:183], v[72:75], v[180:183]\n" \
"v_exp_f32_e64 v97, v97\n" \
"v_mfma_f32_16x16x32_bf16 v[152:155], a[184:187], v[68:71], v[152:155]\n" \
"v_exp_f32_e64 v104, v104\n" \
"v_mfma_f32_16x16x32_bf16 v[184:187], a[184:187], v[72:75], v[184:187]\n" \
"buffer_load_dwordx4 v51, s[8:11], s64 offen offset:2048 sc1 lds\n" \
"v_mfma_f32_16x16x32_bf16 v[156:159], a[188:191], v[68:71], v[156:159]\n" \
"v_exp_f32_e64 v105, v105\n" \
"v_mfma_f32_16x16x32_bf16 v[188:191], a[188:191], v[72:75], v[188:191]\n" \
"v_cvt_pk_bf16_f32 v102, v100, v101\n" \
"v_cvt_pk_bf16_f32 v101, v98, v99\n" \
"v_smfmac_f32_16x16x64_bf16 a[192:195], a[200:203], v[68:75], v21\n" \
"v_cvt_pk_bf16_f32 v100, v96, v97\n" \
"v_cvt_pk_bf16_f32 v104, v104, v105\n" \
"v_mfma_f32_16x16x32_bf16 v[220:223], a[188:191], v[84:87], v[220:223]\n" \
"v_exp_f32_e64 v106, v106\n" \
"v_mfma_f32_16x16x32_bf16 v[252:255], a[188:191], v[88:91], v[252:255]\n" \
"v_exp_f32_e64 v107, v107\n" \
"v_mfma_f32_16x16x32_bf16 v[216:219], a[184:187], v[84:87], v[216:219]\n" \
"v_exp_f32_e64 v108, v108\n" \
"v_mfma_f32_16x16x32_bf16 v[248:251], a[184:187], v[88:91], v[248:251]\n" \
"buffer_load_dwordx4 v51, s[8:11], s64 offen offset:3072 sc1 lds\n" \
"v_mfma_f32_16x16x32_bf16 v[212:215], a[180:183], v[84:87], v[212:215]\n" \
"v_exp_f32_e64 v109, v109\n" \
"v_mfma_f32_16x16x32_bf16 v[244:247], a[180:183], v[88:91], v[244:247]\n" \
"v_exp_f32_e64 v110, v110\n" \
"v_mfma_f32_16x16x32_bf16 v[208:211], a[176:179], v[84:87], v[208:211]\n" \
"v_exp_f32_e64 v111, v111\n" \
"v_mfma_f32_16x16x32_bf16 v[240:243], a[176:179], v[88:91], v[240:243]\n" \
"v_exp_f32_e64 v118, v118\n" \
"v_mfma_f32_16x16x32_bf16 v[204:207], a[172:175], v[84:87], v[204:207]\n" \
"v_exp_f32_e64 v119, v119\n" \
"v_mfma_f32_16x16x32_bf16 v[236:239], a[172:175], v[88:91], v[236:239]\n" \
"v_cvt_pk_bf16_f32 v105, v106, v107\n" \
"v_cvt_pk_bf16_f32 v106, v108, v109\n" \
"v_mfma_f32_16x16x32_bf16 v[200:203], a[168:171], v[84:87], v[200:203]\n" \
"v_cvt_pk_bf16_f32 v107, v110, v111\n" \
"v_cvt_pk_bf16_f32 v119, v118, v119\n" \
"v_mfma_f32_16x16x32_bf16 v[232:235], a[168:171], v[88:91], v[232:235]\n" \
"v_exp_f32_e64 v116, v116\n" \
"v_mfma_f32_16x16x32_bf16 v[196:199], a[164:167], v[84:87], v[196:199]\n" \
"v_exp_f32_e64 v117, v117\n" \
"v_mfma_f32_16x16x32_bf16 v[228:231], a[164:167], v[88:91], v[228:231]\n" \
"v_exp_f32_e64 v114, v114\n" \
"v_mfma_f32_16x16x32_bf16 v[192:195], a[160:163], v[84:87], v[192:195]\n" \
"v_exp_f32_e64 v115, v115\n" \
"v_mfma_f32_16x16x32_bf16 v[224:227], a[160:163], v[88:91], v[224:227]\n" \
"v_exp_f32_e64 v112, v112\n" \
"v_smfmac_f32_16x16x64_bf16 a[196:199], a[200:203], v[84:91], v21\n" \
"v_exp_f32_e32 v113, v113\n" \
"s_waitcnt vmcnt(8) lgkmcnt(0)\n" \
"s_barrier\n" \
"s_nop 0\n" \
"v_mfma_f32_16x16x32_bf16 v[64:67], a[128:131], a[64:67], v[32:35]\n" \
"s_xor_b32 m0, s65, 0x18000\n" \
"ds_read_b128 a[160:163], v49 offset:8192\n" \
"v_mfma_f32_16x16x32_bf16 v[64:67], a[132:135], a[68:71], v[64:67]\n" \
"buffer_load_dwordx4 v52, s[8:11], s64 offen offset:0 sc1 lds\n" \
"v_mfma_f32_16x16x32_bf16 v[64:67], a[136:139], a[72:75], v[64:67]\n" \
"ds_read_b128 a[164:167], v49 offset:9216\n" \
"ds_read_b128 a[168:171], v49 offset:10240\n" \
"v_mfma_f32_16x16x32_bf16 v[64:67], a[140:143], a[76:79], v[64:67]\n" \
"ds_read_b128 a[172:175], v49 offset:11264\n" \
"ds_read_b128 a[176:179], v49 offset:12288\n" \
"v_mfma_f32_16x16x32_bf16 v[68:71], a[156:159], a[76:79], v[32:35]\n" \
"ds_read_b128 a[180:183], v49 offset:13312\n" \
"ds_read_b128 a[184:187], v49 offset:14336\n" \
"v_mfma_f32_16x16x32_bf16 v[68:71], a[152:155], a[72:75], v[68:71]\n" \
"ds_read_b128 a[188:191], v49 offset:15360\n" \
"v_cvt_pk_bf16_f32 v118, v116, v117\n" \
"v_mfma_f32_16x16x32_bf16 v[68:71], a[148:151], a[68:71], v[68:71]\n" \
"v_cvt_pk_bf16_f32 v117, v114, v115\n" \
"v_cvt_pk_bf16_f32 v116, v112, v113\n" \
"v_mfma_f32_16x16x32_bf16 v[68:71], a[144:147], a[64:67], v[68:71]\n" \
"buffer_load_dwordx4 v52, s[8:11], s64 offen offset:1024 sc1 lds\n" \
"v_mfma_f32_16x16x32_bf16 v[76:79], a[144:147], a[80:83], v[36:39]\n" \
"v_exp_f32_e64 v120, v120\n" \
"v_mfma_f32_16x16x32_bf16 v[76:79], a[148:151], a[84:87], v[76:79]\n" \
"v_exp_f32_e64 v121, v121\n" \
"v_mfma_f32_16x16x32_bf16 v[76:79], a[152:155], a[88:91], v[76:79]\n" \
"v_exp_f32_e64 v122, v122\n" \
"v_mfma_f32_16x16x32_bf16 v[76:79], a[156:159], a[92:95], v[76:79]\n" \
"v_exp_f32_e64 v123, v123\n" \
"v_mfma_f32_16x16x32_bf16 v[72:75], a[140:143], a[92:95], v[36:39]\n" \
"v_cvt_pk_bf16_f32 v120, v120, v121\n" \
"v_cvt_pk_bf16_f32 v121, v122, v123\n" \
"v_mfma_f32_16x16x32_bf16 v[72:75], a[136:139], a[88:91], v[72:75]\n" \
"v_exp_f32_e64 v124, v124\n" \
"v_mfma_f32_16x16x32_bf16 v[72:75], a[132:135], a[84:87], v[72:75]\n" \
"v_exp_f32_e64 v125, v125\n" \
"v_mfma_f32_16x16x32_bf16 v[72:75], a[128:131], a[80:83], v[72:75]\n" \
"buffer_load_dwordx4 v52, s[8:11], s64 offen offset:2048 sc1 lds\n" \
"v_mfma_f32_16x16x32_bf16 v[80:83], a[128:131], a[96:99], v[40:43]\n" \
"v_exp_f32_e64 v126, v126\n" \
"v_mfma_f32_16x16x32_bf16 v[80:83], a[132:135], a[100:103], v[80:83]\n" \
"v_exp_f32_e64 v127, v127\n" \
"v_mfma_f32_16x16x32_bf16 v[80:83], a[136:139], a[104:107], v[80:83]\n" \
"v_cvt_pk_bf16_f32 v122, v124, v125\n" \
"v_cvt_pk_bf16_f32 v123, v126, v127\n" \
"v_mfma_f32_16x16x32_bf16 v[80:83], a[140:143], a[108:111], v[80:83]\n" \
"v_exp_f32_e64 v70, v70\n" \
"v_mfma_f32_16x16x32_bf16 v[84:87], a[156:159], a[108:111], v[40:43]\n" \
"v_exp_f32_e64 v71, v71\n" \
"v_mfma_f32_16x16x32_bf16 v[84:87], a[152:155], a[104:107], v[84:87]\n" \
"v_exp_f32_e64 v72, v72\n" \
"v_mfma_f32_16x16x32_bf16 v[84:87], a[148:151], a[100:103], v[84:87]\n" \
"buffer_load_dwordx4 v52, s[8:11], s64 offen offset:3072 sc1 lds\n" \
"v_mfma_f32_16x16x32_bf16 v[84:87], a[144:147], a[96:99], v[84:87]\n" \
"s_mov_b32 m0, s65\n" \
"v_cvt_pk_bf16_f32 v71, v70, v71\n" \
"v_mfma_f32_16x16x32_bf16 v[92:95], a[144:147], a[112:115], v[44:47]\n" \
"ds_read_b128 a[144:147], v48 offset:12288\n" \
"v_add_u32_e32 v49, 0x10000, v48\n" \
"v_mfma_f32_16x16x32_bf16 v[92:95], a[148:151], a[116:119], v[92:95]\n" \
"ds_read_b128 a[148:151], v48 offset:13312\n" \
"s_xor_b32 s65, s65, 0x8000\n" \
"v_mfma_f32_16x16x32_bf16 v[92:95], a[152:155], a[120:123], v[92:95]\n" \
"v_exp_f32_e32 v73, v73\n" \
"v_mfma_f32_16x16x32_bf16 v[92:95], a[156:159], a[124:127], v[92:95]\n" \
"ds_read_b128 a[152:155], v48 offset:14336\n" \
"ds_read_b128 a[156:159], v48 offset:15360\n" \
"v_mfma_f32_16x16x32_bf16 v[88:91], a[140:143], a[124:127], v[44:47]\n" \
"ds_read_b128 a[140:143], v48 offset:11264\n" \
".fill (L_ab + 8192 - .) / 4, 4, 0xBF800000\n" \
"L_ap2:\n" \
"s_cbranch_execz L_ap0\n" \
"v_cvt_pk_bf16_f32 v72, v72, v73\n" \
"v_mfma_f32_16x16x32_bf16 v[88:91], a[136:139], a[120:123], v[88:91]\n" \
"v_exp_f32_e32 v68, v68\n" \
"v_mfma_f32_16x16x32_bf16 v[88:91], a[132:135], a[116:119], v[88:91]\n" \
"buffer_load_dwordx4 v53, s[8:11], s64 offen offset:0 sc1 lds\n" \
"v_mfma_f32_16x16x32_bf16 v[88:91], a[128:131], a[112:115], v[88:91]\n" \
"ds_read_b128 a[128:131], v48 offset:8192\n" \
"s_waitcnt lgkmcnt(6)\n" \
"v_mfma_f32_16x16x32_bf16 v[128:131], a[160:163], v[100:103], v[128:131]\n" \
"ds_read_b128 a[132:135], v48 offset:9216\n" \
"ds_read_b128 a[136:139], v48 offset:10240\n" \
"v_mfma_f32_16x16x32_bf16 v[160:163], a[160:163], v[104:107], v[160:163]\n" \
"v_exp_f32_e32 v69, v69\n" \
"v_mfma_f32_16x16x32_bf16 v[132:135], a[164:167], v[100:103], v[132:135]\n" \
"v_exp_f32_e64 v74, v74\n" \
"v_mfma_f32_16x16x32_bf16 v[164:167], a[164:167], v[104:107], v[164:167]\n" \
"v_exp_f32_e64 v75, v75\n" \
"v_mfma_f32_16x16x32_bf16 v[136:139], a[168:171], v[100:103], v[136:139]\n" \
"v_exp_f32_e64 v66, v66\n" \
"v_mfma_f32_16x16x32_bf16 v[168:171], a[168:171], v[104:107], v[168:171]\n" \
"buffer_load_dwordx4 v53, s[8:11], s64 offen offset:1024 sc1 lds\n" \
"v_mfma_f32_16x16x32_bf16 v[140:143], a[172:175], v[100:103], v[140:143]\n" \
"v_exp_f32_e64 v67, v67\n" \
"v_mfma_f32_16x16x32_bf16 v[172:175], a[172:175], v[104:107], v[172:175]\n" \
"v_exp_f32_e64 v64, v64\n" \
"v_mfma_f32_16x16x32_bf16 v[144:147], a[176:179], v[100:103], v[144:147]\n" \
"v_exp_f32_e64 v65, v65\n" \
"v_mfma_f32_16x16x32_bf16 v[176:179], a[176:179], v[104:107], v[176:179]\n" \
"v_exp_f32_e64 v76, v76\n" \
"v_mfma_f32_16x16x32_bf16 v[148:151], a[180:183], v[100:103], v[148:151]\n" \
"v_exp_f32_e64 v77, v77\n" \
"v_mfma_f32_16x16x32_bf16 v[180:183], a[180:183], v[104:107], v[180:183]\n" \
"v_exp_f32_e64 v78, v78\n" \
"v_mfma_f32_16x16x32_bf16 v[152:155], a[184:187], v[100:103], v[152:155]\n" \
"v_exp_f32_e64 v79, v79\n" \
"v_mfma_f32_16x16x32_bf16 v[184:187], a[184:187], v[104:107], v[184:187]\n" \
"buffer_load_dwordx4 v53, s[8:11], s64 offen offset:2048 sc1 lds\n" \
"v_mfma_f32_16x16x32_bf16 v[156:159], a[188:191], v[100:103], v[156:159]\n" \
"v_cvt_pk_bf16_f32 v70, v68, v69\n" \
"v_cvt_pk_bf16_f32 v73, v74, v75\n" \
"v_mfma_f32_16x16x32_bf16 v[188:191], a[188:191], v[104:107], v[188:191]\n" \
"v_cvt_pk_bf16_f32 v69, v66, v67\n" \
"v_cvt_pk_bf16_f32 v68, v64, v65\n" \
"v_smfmac_f32_16x16x64_bf16 a[192:195], a[200:203], v[100:107], v21\n" \
"v_cvt_pk_bf16_f32 v74, v76, v77\n" \
"v_cvt_pk_bf16_f32 v75, v78, v79\n" \
"v_mfma_f32_16x16x32_bf16 v[220:223], a[188:191], v[116:119], v[220:223]\n" \
"v_xor_b32_e32 v48, 0x8000, v48\n" \
"v_mfma_f32_16x16x32_bf16 v[252:255], a[188:191], v[120:123], v[252:255]\n" \
"v_mfma_f32_16x16x32_bf16 v[216:219], a[184:187], v[116:119], v[216:219]\n" \
"v_mfma_f32_16x16x32_bf16 v[248:251], a[184:187], v[120:123], v[248:251]\n" \
"buffer_load_dwordx4 v53, s[8:11], s64 offen offset:3072 sc1 lds\n" \
"v_mfma_f32_16x16x32_bf16 v[212:215], a[180:183], v[116:119], v[212:215]\n" \
"s_add_u32 s64, s64, 0x10000\n" \
"s_sub_u32 s66, s66, 1\n" \
"s_nop 0\n" \
"v_mfma_f32_16x16x32_bf16 v[244:247], a[180:183], v[120:123], v[244:247]\n" \
"v_mfma_f32_16x16x32_bf16 v[208:211], a[176:179], v[116:119], v[208:211]\n" \
"v_mfma_f32_16x16x32_bf16 v[240:243], a[176:179], v[120:123], v[240:243]\n" \
"v_mfma_f32_16x16x32_bf16 v[204:207], a[172:175], v[116:119], v[204:207]\n" \
"v_mfma_f32_16x16x32_bf16 v[236:239], a[172:175], v[120:123], v[236:239]\n" \
"v_mfma_f32_16x16x32_bf16 v[200:203], a[168:171], v[116:119], v[200:203]\n" \
"v_mfma_f32_16x16x32_bf16 v[232:235], a[168:171], v[120:123], v[232:235]\n" \
"v_mfma_f32_16x16x32_bf16 v[196:199], a[164:167], v[116:119], v[196:199]\n" \
"v_mfma_f32_16x16x32_bf16 v[228:231], a[164:167], v[120:123], v[228:231]\n" \
"v_mfma_f32_16x16x32_bf16 v[192:195], a[160:163], v[116:119], v[192:195]\n" \
"v_mfma_f32_16x16x32_bf16 v[224:227], a[160:163], v[120:123], v[224:227]\n" \
"s_cbranch_scc0 L_tL_2_%=\n" \
"s_waitcnt vmcnt(8) lgkmcnt(0)\n" \
"v_smfmac_f32_16x16x64_bf16 a[196:199], a[200:203], v[116:123], v21\n" \
"s_barrier\n" \
"L_la_%=:\n" \
"v_mfma_f32_16x16x32_bf16 v[96:99], a[128:131], a[64:67], v[32:35]\n" \
"v_readlane_b32 s96, v11, 1\n" \
"v_bfe_u32 v27, v20, 2, 7\n" \
"v_mfma_f32_16x16x32_bf16 v[96:99], a[132:135], a[68:71], v[96:99]\n" \
"s_xor_b32 m0, s65, 0x12000\n" \
"ds_read_b128 a[160:163], v49 offset:0\n" \
"v_mfma_f32_16x16x32_bf16 v[96:99], a[136:139], a[72:75], v[96:99]\n" \
"buffer_load_dword v26, v27, s[56:59], s96 offen sc1\n" \
"v_mfma_f32_16x16x32_bf16 v[96:99], a[140:143], a[76:79], v[96:99]\n" \
"buffer_load_dwordx4 v50, s[8:11], s64 offen offset:0 sc1 lds\n" \
"v_mfma_f32_16x16x32_bf16 v[100:103], a[156:159], a[76:79], v[32:35]\n" \
"ds_read_b128 a[164:167], v49 offset:1024\n" \
"ds_read_b128 a[168:171], v49 offset:2048\n" \
"v_mfma_f32_16x16x32_bf16 v[100:103], a[152:155], a[72:75], v[100:103]\n" \
"ds_read_b128 a[172:175], v49 offset:3072\n" \
"ds_read_b128 a[176:179], v49 offset:4096\n" \
"v_mfma_f32_16x16x32_bf16 v[100:103], a[148:151], a[68:71], v[100:103]\n" \
"ds_read_b128 a[180:183], v49 offset:5120\n" \
"ds_read_b128 a[184:187], v49 offset:6144\n" \
"v_mfma_f32_16x16x32_bf16 v[100:103], a[144:147], a[64:67], v[100:103]\n" \
"ds_read_b128 a[188:191], v49 offset:7168\n" \
"v_readlane_b32 s97, v11, 36\n" \
"v_mfma_f32_16x16x32_bf16 v[108:111], a[144:147], a[80:83], v[36:39]\n" \
"v_exp_f32_e32 v86, v86\n" \
"v_mfma_f32_16x16x32_bf16 v[108:111], a[148:151], a[84:87], v[108:111]\n" \
"v_exp_f32_e64 v87, v87\n" \
"v_mfma_f32_16x16x32_bf16 v[108:111], a[152:155], a[88:91], v[108:111]\n" \
"buffer_load_dwordx4 v50, s[8:11], s64 offen offset:1024 sc1 lds\n" \
"v_mfma_f32_16x16x32_bf16 v[108:111], a[156:159], a[92:95], v[108:111]\n" \
"v_exp_f32_e64 v84, v84\n" \
"v_mfma_f32_16x16x32_bf16 v[104:107], a[140:143], a[92:95], v[36:39]\n" \
"v_exp_f32_e64 v85, v85\n" \
"v_mfma_f32_16x16x32_bf16 v[104:107], a[136:139], a[88:91], v[104:107]\n" \
"v_cvt_pk_bf16_f32 v87, v86, v87\n" \
"v_cvt_pk_bf16_f32 v86, v84, v85\n" \
"v_mfma_f32_16x16x32_bf16 v[104:107], a[132:135], a[84:87], v[104:107]\n" \
"v_exp_f32_e64 v82, v82\n" \
"v_mfma_f32_16x16x32_bf16 v[104:107], a[128:131], a[80:83], v[104:107]\n" \
"v_exp_f32_e64 v83, v83\n" \
"v_mfma_f32_16x16x32_bf16 v[112:115], a[128:131], a[96:99], v[40:43]\n" \
"v_exp_f32_e64 v80, v80\n" \
"v_mfma_f32_16x16x32_bf16 v[112:115], a[132:135], a[100:103], v[112:115]\n" \
"v_exp_f32_e64 v81, v81\n" \
"v_mfma_f32_16x16x32_bf16 v[112:115], a[136:139], a[104:107], v[112:115]\n" \
"buffer_load_dwordx4 v50, s[8:11], s64 offen offset:2048 sc1 lds\n" \
"v_mfma_f32_16x16x32_bf16 v[112:115], a[140:143], a[108:111], v[112:115]\n" \
"v_cvt_pk_bf16_f32 v85, v82, v83\n" \
"v_cvt_pk_bf16_f32 v84, v80, v81\n" \
"v_mfma_f32_16x16x32_bf16 v[116:119], a[156:159], a[108:111], v[40:43]\n" \
"v_exp_f32_e64 v88, v88\n" \
"v_mfma_f32_16x16x32_bf16 v[116:119], a[152:155], a[104:107], v[116:119]\n" \
"v_exp_f32_e64 v89, v89\n" \
"v_mfma_f32_16x16x32_bf16 v[116:119], a[148:151], a[100:103], v[116:119]\n" \
"v_exp_f32_e64 v90, v90\n" \
"v_mfma_f32_16x16x32_bf16 v[116:119], a[144:147], a[96:99], v[116:119]\n" \
"v_exp_f32_e64 v91, v91\n" \
"v_mfma_f32_16x16x32_bf16 v[124:127], a[144:147], a[112:115], v[44:47]\n" \
"v_cvt_pk_bf16_f32 v88, v88, v89\n" \
"v_cvt_pk_bf16_f32 v89, v90, v91\n" \
"v_mfma_f32_16x16x32_bf16 v[124:127], a[148:151], a[116:119], v[124:127]\n" \
"buffer_load_dwordx4 v50, s[8:11], s64 offen offset:3072 sc1 lds\n" \
"v_mfma_f32_16x16x32_bf16 v[124:127], a[152:155], a[120:123], v[124:127]\n" \
"v_exp_f32_e64 v92, v92\n" \
"v_mfma_f32_16x16x32_bf16 v[124:127], a[156:159], a[124:127], v[124:127]\n" \
"v_exp_f32_e64 v93, v93\n" \
"v_mfma_f32_16x16x32_bf16 v[120:123], a[140:143], a[124:127], v[44:47]\n" \
"v_exp_f32_e64 v94, v94\n" \
"v_mfma_f32_16x16x32_bf16 v[120:123], a[136:139], a[120:123], v[120:123]\n" \
"v_exp_f32_e64 v95, v95\n" \
"v_mfma_f32_16x16x32_bf16 v[120:123], a[132:135], a[116:119], v[120:123]\n" \
"v_cvt_pk_bf16_f32 v90, v92, v93\n" \
"v_cvt_pk_bf16_f32 v91, v94, v95\n" \
"v_mfma_f32_16x16x32_bf16 v[120:123], a[128:131], a[112:115], v[120:123]\n" \
"ds_read_b128 a[128:131], v48 offset:0\n" \
"s_waitcnt lgkmcnt(1)\n" \
"s_nop 0\n" \
"v_mfma_f32_16x16x32_bf16 v[128:131], a[160:163], v[68:71], v[128:131]\n" \
"ds_read_b128 a[144:147], v48 offset:4096\n" \
"ds_read_b128 a[132:135], v48 offset:1024\n" \
"v_mfma_f32_16x16x32_bf16 v[160:163], a[160:163], v[72:75], v[160:163]\n" \
"ds_read_b128 a[148:151], v48 offset:5120\n" \
"ds_read_b128 a[136:139], v48 offset:2048\n" \
"v_mfma_f32_16x16x32_bf16 v[132:135], a[164:167], v[68:71], v[132:135]\n" \
"ds_read_b128 a[152:155], v48 offset:6144\n" \
"ds_read_b128 a[140:143], v48 offset:3072\n" \
"v_mfma_f32_16x16x32_bf16 v[164:167], a[164:167], v[72:75], v[164:167]\n" \
"ds_read_b128 a[156:159], v48 offset:7168\n" \
"v_add_u32_e32 v17, 0x49000, v20\n" \
"v_mfma_f32_16x16x32_bf16 v[136:139], a[168:171], v[68:71], v[136:139]\n" \
"v_readlane_b32 s98, v11, 37\n" \
"v_readlane_b32 s93, v11, 20\n" \
"v_mfma_f32_16x16x32_bf16 v[168:171], a[168:171], v[72:75], v[168:171]\n" \
"v_readlane_b32 s84, v11, 15\n" \
"v_readlane_b32 s85, v11, 16\n" \
"v_mfma_f32_16x16x32_bf16 v[140:143], a[172:175], v[68:71], v[140:143]\n" \
"v_readlane_b32 s86, v11, 17\n" \
"s_mov_b32 s87, 0x20000\n" \
"v_mfma_f32_16x16x32_bf16 v[172:175], a[172:175], v[72:75], v[172:175]\n" \
"v_readlane_b32 s92, v11, 18\n" \
"v_readlane_b32 s91, v11, 21\n" \
"v_mfma_f32_16x16x32_bf16 v[144:147], a[176:179], v[68:71], v[144:147]\n" \
"v_exp_f32_e64 v102, v102\n" \
"v_mfma_f32_16x16x32_bf16 v[176:179], a[176:179], v[72:75], v[176:179]\n" \
"v_exp_f32_e64 v103, v103\n" \
"v_mfma_f32_16x16x32_bf16 v[148:151], a[180:183], v[68:71], v[148:151]\n" \
"v_exp_f32_e64 v100, v100\n" \
"v_mfma_f32_16x16x32_bf16 v[180:183], a[180:183], v[72:75], v[180:183]\n" \
"v_exp_f32_e64 v101, v101\n" \
"v_mfma_f32_16x16x32_bf16 v[152:155], a[184:187], v[68:71], v[152:155]\n" \
"v_cvt_pk_bf16_f32 v103, v102, v103\n" \
"v_cvt_pk_bf16_f32 v102, v100, v101\n" \
"v_mfma_f32_16x16x32_bf16 v[184:187], a[184:187], v[72:75], v[184:187]\n" \
"v_exp_f32_e64 v98, v98\n" \
"v_mfma_f32_16x16x32_bf16 v[156:159], a[188:191], v[68:71], v[156:159]\n" \
"v_exp_f32_e64 v99, v99\n" \
"v_mfma_f32_16x16x32_bf16 v[188:191], a[188:191], v[72:75], v[188:191]\n" \
"s_waitcnt vmcnt(4)\n" \
"v_cmp_ne_u32_e32 vcc, s62, v26\n" \
"v_smfmac_f32_16x16x64_bf16 a[192:195], a[200:203], v[68:75], v21\n" \
"v_exp_f32_e64 v96, v96\n" \
"v_mfma_f32_16x16x32_bf16 v[220:223], a[188:191], v[84:87], v[220:223]\n" \
"v_exp_f32_e64 v97, v97\n" \
"v_mfma_f32_16x16x32_bf16 v[252:255], a[188:191], v[88:91], v[252:255]\n" \
"s_cbranch_vccnz L_sol_%=\n" \
"L_rol_%=:\n" \
"v_cvt_pk_bf16_f32 v101, v98, v99\n" \
"buffer_load_dwordx4 a[220:223], v20, s[56:59], s97 offen offset:0 sc1\n" \
"v_mfma_f32_16x16x32_bf16 v[216:219], a[184:187], v[84:87], v[216:219]\n" \
"v_cvt_pk_bf16_f32 v100, v96, v97\n" \
"s_xor_b32 s65, s65, 0x8000\n" \
"v_mfma_f32_16x16x32_bf16 v[248:251], a[184:187], v[88:91], v[248:251]\n" \
"v_exp_f32_e32 v104, v104\n" \
"v_mfma_f32_16x16x32_bf16 v[212:215], a[180:183], v[84:87], v[212:215]\n" \
"v_exp_f32_e64 v105, v105\n" \
"v_mfma_f32_16x16x32_bf16 v[244:247], a[180:183], v[88:91], v[244:247]\n" \
"v_exp_f32_e64 v106, v106\n" \
"v_mfma_f32_16x16x32_bf16 v[208:211], a[176:179], v[84:87], v[208:211]\n" \
"v_exp_f32_e64 v107, v107\n" \
"v_mfma_f32_16x16x32_bf16 v[240:243], a[176:179], v[88:91], v[240:243]\n" \
"buffer_load_dwordx4 a[228:231], v17, s[56:59], s97 offen offset:0 sc1\n" \
"v_mfma_f32_16x16x32_bf16 v[204:207], a[172:175], v[84:87], v[204:207]\n" \
"v_cvt_pk_bf16_f32 v104, v104, v105\n" \
"v_cvt_pk_bf16_f32 v105, v106, v107\n" \
"v_mfma_f32_16x16x32_bf16 v[236:239], a[172:175], v[88:91], v[236:239]\n" \
"v_exp_f32_e64 v108, v108\n" \
"v_mfma_f32_16x16x32_bf16 v[200:203], a[168:171], v[84:87], v[200:203]\n" \
"v_exp_f32_e64 v109, v109\n" \
"v_mfma_f32_16x16x32_bf16 v[232:235], a[168:171], v[88:91], v[232:235]\n" \
"v_exp_f32_e64 v110, v110\n" \
"v_mfma_f32_16x16x32_bf16 v[196:199], a[164:167], v[84:87], v[196:199]\n" \
"v_exp_f32_e64 v111, v111\n" \
"v_mfma_f32_16x16x32_bf16 v[228:231], a[164:167], v[88:91], v[228:231]\n" \
"v_cvt_pk_bf16_f32 v106, v108, v109\n" \
"v_cvt_pk_bf16_f32 v107, v110, v111\n" \
"v_mfma_f32_16x16x32_bf16 v[192:195], a[160:163], v[84:87], v[192:195]\n" \
"v_exp_f32_e64 v118, v118\n" \
"v_mfma_f32_16x16x32_bf16 v[224:227], a[160:163], v[88:91], v[224:227]\n" \
"v_exp_f32_e64 v119, v119\n" \
"v_smfmac_f32_16x16x64_bf16 a[196:199], a[200:203], v[84:91], v21\n" \
"buffer_load_dwordx4 a[236:239], v20, s[56:59], s98 offen offset:0 sc1\n" \
"s_waitcnt vmcnt(8) lgkmcnt(0)\n" \
"s_barrier\n" \
"v_mfma_f32_16x16x32_bf16 v[64:67], a[128:131], a[64:67], v[32:35]\n" \
"s_add_u32 m0, s93, 0x0\n" \
"s_nop 0\n" \
"ds_read_b128 a[160:163], v49 offset:8192\n" \
"v_mfma_f32_16x16x32_bf16 v[64:67], a[132:135], a[68:71], v[64:67]\n" \
"buffer_load_dwordx4 v0, s[84:87], s92 offen offset:0 lds\n" \
"v_mfma_f32_16x16x32_bf16 v[64:67], a[136:139], a[72:75], v[64:67]\n" \
"ds_read_b128 a[164:167], v49 offset:9216\n" \
"ds_read_b128 a[168:171], v49 offset:10240\n" \
"v_mfma_f32_16x16x32_bf16 v[64:67], a[140:143], a[76:79], v[64:67]\n" \
"ds_read_b128 a[172:175], v49 offset:11264\n" \
"ds_read_b128 a[176:179], v49 offset:12288\n" \
"v_mfma_f32_16x16x32_bf16 v[68:71], a[156:159], a[76:79], v[32:35]\n" \
"ds_read_b128 a[180:183], v49 offset:13312\n" \
"ds_read_b128 a[184:187], v49 offset:14336\n" \
"v_mfma_f32_16x16x32_bf16 v[68:71], a[152:155], a[72:75], v[68:71]\n" \
"ds_read_b128 a[188:191], v49 offset:15360\n" \
"v_cvt_pk_bf16_f32 v119, v118, v119\n" \
"v_mfma_f32_16x16x32_bf16 v[68:71], a[148:151], a[68:71], v[68:71]\n" \
"buffer_load_dwordx4 v1, s[84:87], s92 offen offset:1024 lds\n" \
"v_mfma_f32_16x16x32_bf16 v[68:71], a[144:147], a[64:67], v[68:71]\n" \
"buffer_load_dwordx4 a[244:247], v17, s[56:59], s98 offen offset:0 sc1\n" \
"v_mfma_f32_16x16x32_bf16 v[76:79], a[144:147], a[80:83], v[36:39]\n" \
"v_exp_f32_e64 v116, v116\n" \
"v_mfma_f32_16x16x32_bf16 v[76:79], a[148:151], a[84:87], v[76:79]\n" \
"v_exp_f32_e64 v117, v117\n" \
"v_mfma_f32_16x16x32_bf16 v[76:79], a[152:155], a[88:91], v[76:79]\n" \
"v_exp_f32_e64 v114, v114\n" \
"v_mfma_f32_16x16x32_bf16 v[76:79], a[156:159], a[92:95], v[76:79]\n" \
"v_exp_f32_e64 v115, v115\n" \
"v_mfma_f32_16x16x32_bf16 v[72:75], a[140:143], a[92:95], v[36:39]\n" \
"v_exp_f32_e64 v112, v112\n" \
"v_mfma_f32_16x16x32_bf16 v[72:75], a[136:139], a[88:91], v[72:75]\n" \
"buffer_load_dwordx4 v2, s[84:87], s92 offen offset:2048 lds\n" \
"v_mfma_f32_16x16x32_bf16 v[72:75], a[132:135], a[84:87], v[72:75]\n" \
"v_exp_f32_e64 v113, v113\n" \
"v_mfma_f32_16x16x32_bf16 v[72:75], a[128:131], a[80:83], v[72:75]\n" \
"v_cvt_pk_bf16_f32 v118, v116, v117\n" \
"v_cvt_pk_bf16_f32 v117, v114, v115\n" \
"v_mfma_f32_16x16x32_bf16 v[80:83], a[128:131], a[96:99], v[40:43]\n" \
"buffer_load_dwordx4 a[224:227], v20, s[56:59], s97 offen offset:1024 sc1\n" \
"v_mfma_f32_16x16x32_bf16 v[80:83], a[132:135], a[100:103], v[80:83]\n" \
"v_cvt_pk_bf16_f32 v116, v112, v113\n" \
"v_add_u32_e32 v49, 0x10000, v48\n" \
"v_mfma_f32_16x16x32_bf16 v[80:83], a[136:139], a[104:107], v[80:83]\n" \
"v_exp_f32_e64 v120, v120\n" \
"v_mfma_f32_16x16x32_bf16 v[80:83], a[140:143], a[108:111], v[80:83]\n" \
"v_exp_f32_e64 v121, v121\n" \
"v_mfma_f32_16x16x32_bf16 v[84:87], a[156:159], a[108:111], v[40:43]\n" \
"buffer_load_dwordx4 v3, s[84:87], s92 offen offset:3072 lds\n" \
"v_mfma_f32_16x16x32_bf16 v[84:87], a[152:155], a[104:107], v[84:87]\n" \
"s_add_u32 m0, s91, 0x1000\n" \
"v_cvt_pk_bf16_f32 v120, v120, v121\n" \
"v_mfma_f32_16x16x32_bf16 v[84:87], a[148:151], a[100:103], v[84:87]\n" \
"v_exp_f32_e64 v122, v122\n" \
"v_mfma_f32_16x16x32_bf16 v[84:87], a[144:147], a[96:99], v[84:87]\n" \
"v_exp_f32_e64 v123, v123\n" \
"v_mfma_f32_16x16x32_bf16 v[92:95], a[144:147], a[112:115], v[44:47]\n" \
"v_exp_f32_e64 v124, v124\n" \
"v_mfma_f32_16x16x32_bf16 v[92:95], a[148:151], a[116:119], v[92:95]\n" \
"buffer_load_dwordx4 a[232:235], v17, s[56:59], s97 offen offset:1024 sc1\n" \
"v_mfma_f32_16x16x32_bf16 v[92:95], a[152:155], a[120:123], v[92:95]\n" \
"buffer_load_dwordx4 v4, s[84:87], s92 offen offset:0 lds\n" \
"v_mfma_f32_16x16x32_bf16 v[92:95], a[156:159], a[124:127], v[92:95]\n" \
"v_exp_f32_e64 v125, v125\n" \
"v_mfma_f32_16x16x32_bf16 v[88:91], a[140:143], a[124:127], v[44:47]\n" \
"v_exp_f32_e64 v126, v126\n" \
"v_mfma_f32_16x16x32_bf16 v[88:91], a[136:139], a[120:123], v[88:91]\n" \
"v_exp_f32_e64 v127, v127\n" \
"v_mfma_f32_16x16x32_bf16 v[88:91], a[132:135], a[116:119], v[88:91]\n" \
"v_cvt_pk_bf16_f32 v121, v122, v123\n" \
"v_cvt_pk_bf16_f32 v122, v124, v125\n" \
"v_mfma_f32_16x16x32_bf16 v[88:91], a[128:131], a[112:115], v[88:91]\n" \
"v_cvt_pk_bf16_f32 v123, v126, v127\n" \
"s_waitcnt lgkmcnt(0)\n" \
"s_nop 0\n" \
"v_mfma_f32_16x16x32_bf16 v[128:131], a[160:163], v[100:103], v[128:131]\n" \
"ds_read_b128 a[128:131], v48 offset:8192\n" \
"ds_read_b128 a[144:147], v48 offset:12288\n" \
"v_mfma_f32_16x16x32_bf16 v[160:163], a[160:163], v[104:107], v[160:163]\n" \
"buffer_load_dwordx4 v5, s[84:87], s92 offen offset:1024 lds\n" \
"v_mfma_f32_16x16x32_bf16 v[132:135], a[164:167], v[100:103], v[132:135]\n" \
"buffer_load_dwordx4 a[240:243], v20, s[56:59], s98 offen offset:1024 sc1\n" \
"v_mfma_f32_16x16x32_bf16 v[164:167], a[164:167], v[104:107], v[164:167]\n" \
"ds_read_b128 a[132:135], v48 offset:9216\n" \
"ds_read_b128 a[148:151], v48 offset:13312\n" \
"v_mfma_f32_16x16x32_bf16 v[136:139], a[168:171], v[100:103], v[136:139]\n" \
"ds_read_b128 a[136:139], v48 offset:10240\n" \
"ds_read_b128 a[152:155], v48 offset:14336\n" \
"v_mfma_f32_16x16x32_bf16 v[168:171], a[168:171], v[104:107], v[168:171]\n" \
"ds_read_b128 a[140:143], v48 offset:11264\n" \
"ds_read_b128 a[156:159], v48 offset:15360\n" \
"v_mfma_f32_16x16x32_bf16 v[140:143], a[172:175], v[100:103], v[140:143]\n" \
"v_exp_f32_e64 v70, v70\n" \
"v_mfma_f32_16x16x32_bf16 v[172:175], a[172:175], v[104:107], v[172:175]\n" \
"v_exp_f32_e64 v71, v71\n" \
"v_mfma_f32_16x16x32_bf16 v[144:147], a[176:179], v[100:103], v[144:147]\n" \
"buffer_load_dwordx4 v6, s[84:87], s92 offen offset:2048 lds\n" \
"v_mfma_f32_16x16x32_bf16 v[176:179], a[176:179], v[104:107], v[176:179]\n" \
"v_exp_f32_e64 v72, v72\n" \
"v_mfma_f32_16x16x32_bf16 v[148:151], a[180:183], v[100:103], v[148:151]\n" \
"v_exp_f32_e64 v73, v73\n" \
"v_mfma_f32_16x16x32_bf16 v[180:183], a[180:183], v[104:107], v[180:183]\n" \
"buffer_load_dwordx4 a[248:251], v17, s[56:59], s98 offen offset:1024 sc1\n" \
"v_mfma_f32_16x16x32_bf16 v[152:155], a[184:187], v[100:103], v[152:155]\n" \
"v_exp_f32_e64 v68, v68\n" \
"v_mfma_f32_16x16x32_bf16 v[184:187], a[184:187], v[104:107], v[184:187]\n" \
"v_exp_f32_e64 v69, v69\n" \
"v_mfma_f32_16x16x32_bf16 v[156:159], a[188:191], v[100:103], v[156:159]\n" \
"buffer_load_dwordx4 v7, s[84:87], s92 offen offset:3072 lds\n" \
"v_mfma_f32_16x16x32_bf16 v[188:191], a[188:191], v[104:107], v[188:191]\n" \
"s_add_u32 m0, s91, 0x2000\n" \
"v_cvt_pk_bf16_f32 v71, v70, v71\n" \
"v_smfmac_f32_16x16x64_bf16 a[192:195], a[200:203], v[100:107], v21\n" \
"v_exp_f32_e64 v74, v74\n" \
"v_mfma_f32_16x16x32_bf16 v[220:223], a[188:191], v[116:119], v[220:223]\n" \
"v_exp_f32_e64 v75, v75\n" \
"v_mfma_f32_16x16x32_bf16 v[252:255], a[188:191], v[120:123], v[252:255]\n" \
"v_exp_f32_e64 v66, v66\n" \
"v_mfma_f32_16x16x32_bf16 v[216:219], a[184:187], v[116:119], v[216:219]\n" \
"v_exp_f32_e64 v67, v67\n" \
"v_mfma_f32_16x16x32_bf16 v[248:251], a[184:187], v[120:123], v[248:251]\n" \
"v_exp_f32_e64 v64, v64\n" \
"v_mfma_f32_16x16x32_bf16 v[212:215], a[180:183], v[116:119], v[212:215]\n" \
"buffer_load_dwordx4 v8, s[84:87], s92 offen offset:0 lds\n" \
"v_mfma_f32_16x16x32_bf16 v[244:247], a[180:183], v[120:123], v[244:247]\n" \
"v_exp_f32_e64 v65, v65\n" \
"v_mfma_f32_16x16x32_bf16 v[208:211], a[176:179], v[116:119], v[208:211]\n" \
"v_cvt_pk_bf16_f32 v72, v72, v73\n" \
"v_cvt_pk_bf16_f32 v70, v68, v69\n" \
"v_mfma_f32_16x16x32_bf16 v[240:243], a[176:179], v[120:123], v[240:243]\n" \
"v_exp_f32_e64 v76, v76\n" \
"v_mfma_f32_16x16x32_bf16 v[204:207], a[172:175], v[116:119], v[204:207]\n" \
"v_exp_f32_e64 v77, v77\n" \
"v_mfma_f32_16x16x32_bf16 v[236:239], a[172:175], v[120:123], v[236:239]\n" \
"v_exp_f32_e64 v78, v78\n" \
"v_mfma_f32_16x16x32_bf16 v[200:203], a[168:171], v[116:119], v[200:203]\n" \
"v_exp_f32_e64 v79, v79\n" \
"v_mfma_f32_16x16x32_bf16 v[232:235], a[168:171], v[120:123], v[232:235]\n" \
"v_cvt_pk_bf16_f32 v73, v74, v75\n" \
"v_cvt_pk_bf16_f32 v69, v66, v67\n" \
"v_mfma_f32_16x16x32_bf16 v[196:199], a[164:167], v[116:119], v[196:199]\n" \
"v_cvt_pk_bf16_f32 v68, v64, v65\n" \
"v_cvt_pk_bf16_f32 v74, v76, v77\n" \
"v_mfma_f32_16x16x32_bf16 v[228:231], a[164:167], v[120:123], v[228:231]\n" \
"v_cvt_pk_bf16_f32 v75, v78, v79\n" \
"v_xor_b32_e32 v48, 0x8000, v48\n" \
"v_mfma_f32_16x16x32_bf16 v[192:195], a[160:163], v[116:119], v[192:195]\n" \
"s_add_u32 s64, s64, 0x10000\n" \
"v_mfma_f32_16x16x32_bf16 v[224:227], a[160:163], v[120:123], v[224:227]\n" \
"s_waitcnt vmcnt(14) lgkmcnt(0)\n" \
"v_smfmac_f32_16x16x64_bf16 a[196:199], a[200:203], v[116:123], v21\n" \
"s_barrier\n" \
"s_cmp_lg_u32 s16, 0\n" \
"s_cbranch_scc1 L_f1_%=\n" \
"ds_read_b128 a[160:163], v49 offset:0\n" \
"ds_read_b128 a[164:167], v49 offset:1024\n" \
"ds_read_b128 a[168:171], v49 offset:2048\n" \
"ds_read_b128 a[172:175], v49 offset:3072\n" \
"ds_read_b128 a[176:179], v49 offset:4096\n" \
"ds_read_b128 a[180:183], v49 offset:5120\n" \
"ds_read_b128 a[184:187], v49 offset:6144\n" \
"ds_read_b128 a[188:191], v49 offset:7168\n" \
"v_add_u32_e32 v54, 0x49000, v20\n" \
"buffer_load_dwordx4 a[96:99], v20, s[56:59], s60 offen offset:0 sc1\n" \
"v_add_u32_e32 v50, s15, v20\n" \
"v_add_u32_e32 v51, 0x10000, v50\n" \
"ds_read_b32 v60, v18 offset:0\n" \
"ds_read_b32 v61, v18 offset:128\n" \
"ds_read_b128 a[0:3], v49 offset:8192\n" \
"ds_read_b128 a[4:7], v49 offset:9216\n" \
"ds_read_b128 a[8:11], v49 offset:10240\n" \
"ds_read_b128 a[12:15], v49 offset:11264\n" \
"ds_read_b128 a[16:19], v49 offset:12288\n" \
"ds_read_b128 a[20:23], v49 offset:13312\n" \
"ds_read_b128 a[24:27], v49 offset:14336\n" \
"ds_read_b128 a[28:31], v49 offset:15360\n" \
"v_exp_f32_e32 v86, v86\n" \
"v_exp_f32_e32 v87, v87\n" \
"v_exp_f32_e32 v84, v84\n" \
"v_exp_f32_e32 v85, v85\n" \
"v_cvt_pk_bf16_f32 v87, v86, v87\n" \
"buffer_load_dwordx4 a[100:103], v20, s[56:59], s60 offen offset:1024 sc1\n" \
"v_exp_f32_e32 v82, v82\n" \
"v_exp_f32_e32 v83, v83\n" \
"v_exp_f32_e32 v80, v80\n" \
"v_exp_f32_e32 v81, v81\n" \
"s_waitcnt lgkmcnt(15)\n" \
"v_mfma_f32_16x16x32_bf16 v[128:131], a[160:163], v[68:71], v[128:131]\n" \
"v_cvt_pk_bf16_f32 v86, v84, v85\n" \
"v_cvt_pk_bf16_f32 v85, v82, v83\n" \
"v_mfma_f32_16x16x32_bf16 v[160:163], a[160:163], v[72:75], v[160:163]\n" \
"v_cvt_pk_bf16_f32 v84, v80, v81\n" \
"v_and_b32_e32 v84, v12, v84\n" \
"v_mfma_f32_16x16x32_bf16 v[132:135], a[164:167], v[68:71], v[132:135]\n" \
"v_and_b32_e32 v85, v13, v85\n" \
"v_and_b32_e32 v86, v14, v86\n" \
"v_mfma_f32_16x16x32_bf16 v[164:167], a[164:167], v[72:75], v[164:167]\n" \
"buffer_load_dwordx4 a[104:107], v20, s[56:59], s60 offen offset:2048 sc1\n" \
"v_mfma_f32_16x16x32_bf16 v[136:139], a[168:171], v[68:71], v[136:139]\n" \
"v_cvt_pk_bf16_f32 v128, v128, v129\n" \
"v_cvt_pk_bf16_f32 v160, v160, v161\n" \
"v_mfma_f32_16x16x32_bf16 v[168:171], a[168:171], v[72:75], v[168:171]\n" \
"v_cvt_pk_bf16_f32 v129, v130, v131\n" \
"s_waitcnt lgkmcnt(10)\n" \
"v_mfma_f32_16x16x32_bf16 v[140:143], a[172:175], v[68:71], v[140:143]\n" \
"v_cvt_pk_bf16_f32 v161, v162, v163\n" \
"v_cvt_pk_bf16_f32 v130, v132, v133\n" \
"v_mfma_f32_16x16x32_bf16 v[172:175], a[172:175], v[72:75], v[172:175]\n" \
"v_cvt_pk_bf16_f32 v131, v134, v135\n" \
"v_cvt_pk_bf16_f32 v162, v164, v165\n" \
"v_mfma_f32_16x16x32_bf16 v[144:147], a[176:179], v[68:71], v[144:147]\n" \
"v_cvt_pk_bf16_f32 v163, v166, v167\n" \
"v_cvt_pk_bf16_f32 v132, v136, v137\n" \
"v_mfma_f32_16x16x32_bf16 v[176:179], a[176:179], v[72:75], v[176:179]\n" \
"buffer_load_dwordx4 a[108:111], v20, s[56:59], s60 offen offset:3072 sc1\n" \
"v_mfma_f32_16x16x32_bf16 v[148:151], a[180:183], v[68:71], v[148:151]\n" \
"v_cvt_pk_bf16_f32 v133, v138, v139\n" \
"v_cvt_pk_bf16_f32 v134, v140, v141\n" \
"v_mfma_f32_16x16x32_bf16 v[180:183], a[180:183], v[72:75], v[180:183]\n" \
"v_cvt_pk_bf16_f32 v135, v142, v143\n" \
"v_cvt_pk_bf16_f32 v164, v168, v169\n" \
"v_mfma_f32_16x16x32_bf16 v[152:155], a[184:187], v[68:71], v[152:155]\n" \
"v_cvt_pk_bf16_f32 v165, v170, v171\n" \
"v_cvt_pk_bf16_f32 v166, v172, v173\n" \
"v_mfma_f32_16x16x32_bf16 v[184:187], a[184:187], v[72:75], v[184:187]\n" \
"v_cvt_pk_bf16_f32 v167, v174, v175\n" \
"v_cvt_pk_bf16_f32 v136, v144, v145\n" \
"v_mfma_f32_16x16x32_bf16 v[156:159], a[188:191], v[68:71], v[156:159]\n" \
"v_cvt_pk_bf16_f32 v137, v146, v147\n" \
"v_cvt_pk_bf16_f32 v138, v148, v149\n" \
"v_mfma_f32_16x16x32_bf16 v[188:191], a[188:191], v[72:75], v[188:191]\n" \
"v_cvt_pk_bf16_f32 v139, v150, v151\n" \
"v_cvt_pk_bf16_f32 v140, v152, v153\n" \
"v_smfmac_f32_16x16x64_bf16 a[192:195], a[200:203], v[68:75], v21\n" \
"v_cvt_pk_bf16_f32 v141, v154, v155\n" \
"v_cvt_pk_bf16_f32 v168, v176, v177\n" \
"v_mfma_f32_16x16x32_bf16 v[120:123], a[128:131], a[112:115], v[44:47]\n" \
"v_cvt_pk_bf16_f32 v142, v156, v157\n" \
"v_cvt_pk_bf16_f32 v143, v158, v159\n" \
"v_mfma_f32_16x16x32_bf16 v[120:123], a[132:135], a[116:119], v[120:123]\n" \
"v_cvt_pk_bf16_f32 v169, v178, v179\n" \
"v_cvt_pk_bf16_f32 v170, v180, v181\n" \
"v_cvt_pk_bf16_f32 v171, v182, v183\n" \
"v_cvt_pk_bf16_f32 v172, v184, v185\n" \
"v_cvt_pk_bf16_f32 v173, v186, v187\n" \
"v_cvt_pk_bf16_f32 v174, v188, v189\n" \
"v_cvt_pk_bf16_f32 v175, v190, v191\n" \
"ds_write_b128 v50, v[128:131] offset:0\n" \
"v_mfma_f32_16x16x32_bf16 v[120:123], a[136:139], a[120:123], v[120:123]\n" \
"v_and_b32_e32 v87, v15, v87\n" \
"v_accvgpr_read_b32 v40, a192\n" \
"v_mfma_f32_16x16x32_bf16 v[120:123], a[140:143], a[124:127], v[120:123]\n" \
"ds_write_b128 v50, v[132:135] offset:1024\n" \
"v_mfma_f32_16x16x32_bf16 v[124:127], a[156:159], a[124:127], v[44:47]\n" \
"buffer_load_dwordx4 a[124:127], v54, s[56:59], s60 offen offset:3072 sc1\n" \
"v_mfma_f32_16x16x32_bf16 v[124:127], a[152:155], a[120:123], v[124:127]\n" \
"ds_write_b128 v50, v[136:139] offset:2048\n" \
"v_mfma_f32_16x16x32_bf16 v[124:127], a[148:151], a[116:119], v[124:127]\n" \
"v_accvgpr_read_b32 v22, a193\n" \
"v_accvgpr_read_b32 v41, a194\n" \
"v_mfma_f32_16x16x32_bf16 v[124:127], a[144:147], a[112:115], v[124:127]\n" \
"ds_write_b128 v50, v[140:143] offset:3072\n" \
"v_mfma_f32_16x16x32_bf16 a[196:199], a[204:207], v[84:87], a[196:199]\n" \
"v_accvgpr_read_b32 v23, a195\n" \
"v_add_f32_e32 v40, v40, v22\n" \
"v_mfma_f32_16x16x32_bf16 v[192:195], a[160:163], v[84:87], v[192:195]\n" \
"ds_write_b128 v51, v[160:163] offset:32768\n" \
"v_mfma_f32_16x16x32_bf16 v[196:199], a[164:167], v[84:87], v[196:199]\n" \
"buffer_load_dwordx4 a[112:115], v54, s[56:59], s60 offen offset:0 sc1\n" \
"v_mfma_f32_16x16x32_bf16 v[200:203], a[168:171], v[84:87], v[200:203]\n" \
"ds_write_b128 v51, v[164:167] offset:33792\n" \
"v_add_f32_e32 v41, v41, v23\n" \
"ds_write_b64 v19, v[40:41]\n" \
"v_mfma_f32_16x16x32_bf16 v[204:207], a[172:175], v[84:87], v[204:207]\n" \
"v_lshl_add_u32 v52, s15, 2, v20\n" \
"v_add_u32_e32 v52, 0x1a000, v52\n" \
"ds_write_b128 v51, v[168:171] offset:34816\n" \
"v_mfma_f32_16x16x32_bf16 v[208:211], a[176:179], v[84:87], v[208:211]\n" \
"v_exp_f32_e32 v88, v88\n" \
"v_mfma_f32_16x16x32_bf16 v[212:215], a[180:183], v[84:87], v[212:215]\n" \
"ds_write_b128 v51, v[172:175] offset:35840\n" \
"v_mfma_f32_16x16x32_bf16 v[216:219], a[184:187], v[84:87], v[216:219]\n" \
"buffer_load_dwordx4 a[116:119], v54, s[56:59], s60 offen offset:1024 sc1\n" \
"v_mfma_f32_16x16x32_bf16 v[220:223], a[188:191], v[84:87], v[220:223]\n" \
"v_exp_f32_e32 v89, v89\n" \
"v_exp_f32_e32 v90, v90\n" \
"v_exp_f32_e32 v91, v91\n" \
"v_cvt_pk_bf16_f32 v88, v88, v89\n" \
"v_exp_f32_e32 v92, v92\n" \
"v_exp_f32_e32 v93, v93\n" \
"v_exp_f32_e32 v94, v94\n" \
"v_exp_f32_e32 v95, v95\n" \
"v_cvt_pk_bf16_f32 v89, v90, v91\n" \
"v_cvt_pk_bf16_f32 v90, v92, v93\n" \
"v_cvt_pk_bf16_f32 v91, v94, v95\n" \
"v_xor_b32_e32 v63, 0x400, v19\n" \
"buffer_load_dwordx4 a[120:123], v54, s[56:59], s60 offen offset:2048 sc1\n" \
"v_exp_f32_e32 v120, v120\n" \
"v_exp_f32_e32 v121, v121\n" \
"v_exp_f32_e32 v122, v122\n" \
"v_exp_f32_e32 v123, v123\n" \
"v_cvt_pk_bf16_f32 v120, v120, v121\n" \
"v_exp_f32_e32 v124, v124\n" \
"v_exp_f32_e32 v125, v125\n" \
"s_waitcnt lgkmcnt(0)\n" \
"s_barrier\n" \
"ds_read_b128 v[144:147], v52 offset:0\n" \
".fill (L_ab + 12288 - .) / 4, 4, 0xBF800000\n" \
"L_ap3:\n" \
"s_cbranch_execz L_ap0\n" \
"ds_read_b128 v[148:151], v52 offset:1024\n" \
"ds_read_b128 v[152:155], v52 offset:2048\n" \
"ds_read_b128 v[156:159], v52 offset:3072\n" \
"ds_read_b128 v[176:179], v52 offset:4096\n" \
"ds_read_b128 v[180:183], v52 offset:5120\n" \
"ds_read_b128 v[184:187], v52 offset:6144\n" \
"ds_read_b128 v[188:191], v52 offset:7168\n" \
"v_mfma_f32_16x16x32_bf16 a[196:199], a[212:215], v[88:91], a[196:199]\n" \
"ds_read_b64 v[42:43], v63\n" \
"v_cvt_pk_bf16_f32 v121, v122, v123\n" \
"v_mfma_f32_16x16x32_bf16 v[224:227], a[160:163], v[88:91], v[224:227]\n" \
"v_exp_f32_e32 v126, v126\n" \
"v_mfma_f32_16x16x32_bf16 v[228:231], a[164:167], v[88:91], v[228:231]\n" \
"v_exp_f32_e32 v127, v127\n" \
"v_mfma_f32_16x16x32_bf16 v[232:235], a[168:171], v[88:91], v[232:235]\n" \
"v_cvt_pk_bf16_f32 v122, v124, v125\n" \
"v_cvt_pk_bf16_f32 v123, v126, v127\n" \
"v_mfma_f32_16x16x32_bf16 v[236:239], a[172:175], v[88:91], v[236:239]\n" \
"v_and_b32_e32 v120, v12, v120\n" \
"v_and_b32_e32 v121, v13, v121\n" \
"v_mfma_f32_16x16x32_bf16 v[240:243], a[176:179], v[88:91], v[240:243]\n" \
"v_and_b32_e32 v122, v14, v122\n" \
"v_and_b32_e32 v123, v15, v123\n" \
"v_mfma_f32_16x16x32_bf16 v[244:247], a[180:183], v[88:91], v[244:247]\n" \
"v_readlane_b32 s93, v11, 22\n" \
"v_readlane_b32 s92, v11, 27\n" \
"v_mfma_f32_16x16x32_bf16 v[248:251], a[184:187], v[88:91], v[248:251]\n" \
"s_add_u32 m0, s93, 0x0\n" \
"v_mov_b32_e32 v130, v28\n" \
"v_mfma_f32_16x16x32_bf16 v[252:255], a[188:191], v[88:91], v[252:255]\n" \
"v_mov_b32_e32 v131, v29\n" \
"v_mov_b32_e32 v132, 0\n" \
"v_mov_b32_e32 v133, 0\n" \
"v_mov_b32_e32 v128, 0\n" \
"v_mov_b32_e32 v129, 0\n" \
"s_waitcnt vmcnt(8) lgkmcnt(1)\n" \
"s_barrier\n" \
"v_mfma_f32_16x16x32_bf16 a[196:199], a[212:215], v[120:123], a[196:199]\n" \
"buffer_load_dwordx4 v0, s[80:83], s92 offen offset:0 lds\n" \
"v_mfma_f32_16x16x32_bf16 v[224:227], a[0:3], v[120:123], v[224:227]\n" \
"v_mfma_f32_16x16x32_bf16 v[228:231], a[4:7], v[120:123], v[228:231]\n" \
"v_mfma_f32_16x16x32_bf16 v[232:235], a[8:11], v[120:123], v[232:235]\n" \
"v_mfma_f32_16x16x32_bf16 v[236:239], a[12:15], v[120:123], v[236:239]\n" \
"v_mfma_f32_16x16x32_bf16 v[240:243], a[16:19], v[120:123], v[240:243]\n" \
"v_mfma_f32_16x16x32_bf16 v[244:247], a[20:23], v[120:123], v[244:247]\n" \
"buffer_load_dwordx4 v1, s[80:83], s92 offen offset:1024 lds\n" \
"v_mfma_f32_16x16x32_bf16 v[248:251], a[24:27], v[120:123], v[248:251]\n" \
"v_accvgpr_read_b32 v56, a196\n" \
"v_accvgpr_read_b32 v24, a197\n" \
"v_mfma_f32_16x16x32_bf16 v[252:255], a[28:31], v[120:123], v[252:255]\n" \
"v_add_f32_e32 v56, v56, v24\n" \
"s_waitcnt lgkmcnt(0)\n" \
"v_add_f32_e32 v56, v56, v42\n" \
"v_mfma_f32_16x16x32_bf16 v[192:195], v[130:133], v[144:147], v[192:195]\n" \
"v_max_f32_e32 v62, v62, v56\n" \
"v_accvgpr_read_b32 v57, a198\n" \
"v_mfma_f32_16x16x32_bf16 v[196:199], v[128:131], v[144:147], v[196:199]\n" \
"v_rcp_f32_e32 v56, v56\n" \
"v_mfma_f32_16x16x32_bf16 v[200:203], v[130:133], v[148:151], v[200:203]\n" \
"v_mul_f32_e32 v56, v56, v60\n" \
"v_accvgpr_read_b32 v25, a199\n" \
"buffer_load_dwordx4 v2, s[80:83], s92 offen offset:2048 lds\n" \
"v_mfma_f32_16x16x32_bf16 v[204:207], v[128:131], v[148:151], v[204:207]\n" \
"v_mul_f32_e32 v192, v56, v192\n" \
"v_mul_f32_e32 v193, v56, v193\n" \
"v_mfma_f32_16x16x32_bf16 v[208:211], v[130:133], v[152:155], v[208:211]\n" \
"v_mul_f32_e32 v194, v56, v194\n" \
"v_mul_f32_e32 v195, v56, v195\n" \
"v_mfma_f32_16x16x32_bf16 v[212:215], v[128:131], v[152:155], v[212:215]\n" \
"v_mul_f32_e32 v196, v56, v196\n" \
"v_mul_f32_e32 v197, v56, v197\n" \
"v_mfma_f32_16x16x32_bf16 v[216:219], v[130:133], v[156:159], v[216:219]\n" \
"v_mul_f32_e32 v198, v56, v198\n" \
"v_mul_f32_e32 v199, v56, v199\n" \
"v_mfma_f32_16x16x32_bf16 v[220:223], v[128:131], v[156:159], v[220:223]\n" \
"v_cvt_pk_bf16_f32 v64, v192, v193\n" \
"v_cvt_pk_bf16_f32 v65, v194, v195\n" \
"v_mfma_f32_16x16x32_bf16 v[224:227], v[130:133], v[176:179], v[224:227]\n" \
"buffer_load_dwordx4 v3, s[80:83], s92 offen offset:3072 lds\n" \
"s_add_u32 m0, s93, 0x1000\n" \
"v_mfma_f32_16x16x32_bf16 v[228:231], v[128:131], v[176:179], v[228:231]\n" \
"v_cvt_pk_bf16_f32 v66, v196, v197\n" \
"v_cvt_pk_bf16_f32 v67, v198, v199\n" \
"v_mfma_f32_16x16x32_bf16 v[232:235], v[130:133], v[180:183], v[232:235]\n" \
"v_mul_f32_e32 v200, v56, v200\n" \
"v_mul_f32_e32 v201, v56, v201\n" \
"v_mfma_f32_16x16x32_bf16 v[236:239], v[128:131], v[180:183], v[236:239]\n" \
"v_mfma_f32_16x16x32_bf16 a[208:211], a[216:219], v[64:67], a[208:211]\n" \
"v_mul_f32_e32 v202, v56, v202\n" \
"v_mul_f32_e32 v203, v56, v203\n" \
"v_mfma_f32_16x16x32_bf16 v[240:243], v[130:133], v[184:187], v[240:243]\n" \
"v_mul_f32_e32 v204, v56, v204\n" \
"v_mul_f32_e32 v205, v56, v205\n" \
"buffer_load_dwordx4 v4, s[80:83], s92 offen offset:0 lds\n" \
"v_mul_f32_e32 v206, v56, v206\n" \
"v_mul_f32_e32 v207, v56, v207\n" \
"v_add_f32_e32 v57, v57, v25\n" \
"v_cvt_pk_bf16_f32 v68, v200, v201\n" \
"v_cvt_pk_bf16_f32 v69, v202, v203\n" \
"v_cvt_pk_bf16_f32 v70, v204, v205\n" \
"v_cvt_pk_bf16_f32 v71, v206, v207\n" \
"v_add_f32_e32 v57, v57, v43\n" \
"v_mul_f32_e32 v208, v56, v208\n" \
"v_mfma_f32_16x16x32_bf16 a[208:211], a[216:219], v[68:71], a[208:211]\n" \
"v_mul_f32_e32 v209, v56, v209\n" \
"v_mul_f32_e32 v210, v56, v210\n" \
"v_mul_f32_e32 v211, v56, v211\n" \
"v_mul_f32_e32 v212, v56, v212\n" \
"v_mul_f32_e32 v213, v56, v213\n" \
"v_mul_f32_e32 v214, v56, v214\n" \
"v_mul_f32_e32 v215, v56, v215\n" \
"v_max_f32_e32 v62, v62, v57\n" \
"v_cvt_pk_bf16_f32 v72, v208, v209\n" \
"v_cvt_pk_bf16_f32 v73, v210, v211\n" \
"buffer_load_dwordx4 v5, s[80:83], s92 offen offset:1024 lds\n" \
"v_cvt_pk_bf16_f32 v74, v212, v213\n" \
"v_cvt_pk_bf16_f32 v75, v214, v215\n" \
"v_rcp_f32_e32 v57, v57\n" \
"v_mfma_f32_16x16x32_bf16 v[244:247], v[128:131], v[184:187], v[244:247]\n" \
"v_mfma_f32_16x16x32_bf16 a[208:211], a[216:219], v[72:75], a[208:211]\n" \
"v_mul_f32_e32 v216, v56, v216\n" \
"v_mul_f32_e32 v217, v56, v217\n" \
"v_mul_f32_e32 v218, v56, v218\n" \
"v_mul_f32_e32 v219, v56, v219\n" \
"v_mul_f32_e32 v220, v56, v220\n" \
"v_mul_f32_e32 v221, v56, v221\n" \
"v_mul_f32_e32 v222, v56, v222\n" \
"v_mul_f32_e32 v223, v56, v223\n" \
"v_cvt_pk_bf16_f32 v76, v216, v217\n" \
"v_cvt_pk_bf16_f32 v77, v218, v219\n" \
"v_cvt_pk_bf16_f32 v78, v220, v221\n" \
"buffer_load_dwordx4 v6, s[80:83], s92 offen offset:2048 lds\n" \
"v_cvt_pk_bf16_f32 v79, v222, v223\n" \
"v_mul_f32_e32 v57, v57, v61\n" \
"v_mul_f32_e32 v224, v57, v224\n" \
"v_mfma_f32_16x16x32_bf16 a[208:211], a[216:219], v[76:79], a[208:211]\n" \
"v_mul_f32_e32 v225, v57, v225\n" \
"v_mul_f32_e32 v226, v57, v226\n" \
"v_mul_f32_e32 v227, v57, v227\n" \
"v_mul_f32_e32 v228, v57, v228\n" \
"v_mul_f32_e32 v229, v57, v229\n" \
"v_mul_f32_e32 v230, v57, v230\n" \
"v_mul_f32_e32 v231, v57, v231\n" \
"v_cvt_pk_bf16_f32 v80, v224, v225\n" \
"v_cvt_pk_bf16_f32 v81, v226, v227\n" \
"v_cvt_pk_bf16_f32 v82, v228, v229\n" \
"v_cvt_pk_bf16_f32 v83, v230, v231\n" \
"v_mfma_f32_16x16x32_bf16 v[248:251], v[130:133], v[188:191], v[248:251]\n" \
"v_mul_f32_e32 v232, v57, v232\n" \
"v_mul_f32_e32 v233, v57, v233\n" \
"v_mfma_f32_16x16x32_bf16 a[208:211], a[216:219], v[80:83], a[208:211]\n" \
"buffer_load_dwordx4 v7, s[80:83], s92 offen offset:3072 lds\n" \
"s_add_u32 m0, s93, 0x2000\n" \
"v_mul_f32_e32 v234, v57, v234\n" \
"v_mul_f32_e32 v235, v57, v235\n" \
"v_mul_f32_e32 v236, v57, v236\n" \
"v_mul_f32_e32 v237, v57, v237\n" \
"v_mul_f32_e32 v238, v57, v238\n" \
"v_mul_f32_e32 v239, v57, v239\n" \
"v_cvt_pk_bf16_f32 v84, v232, v233\n" \
"v_cvt_pk_bf16_f32 v85, v234, v235\n" \
"v_cvt_pk_bf16_f32 v86, v236, v237\n" \
"v_cvt_pk_bf16_f32 v87, v238, v239\n" \
"v_mul_f32_e32 v240, v57, v240\n" \
"v_mul_f32_e32 v241, v57, v241\n" \
"v_mfma_f32_16x16x32_bf16 a[208:211], a[216:219], v[84:87], a[208:211]\n" \
"v_mul_f32_e32 v242, v57, v242\n" \
"v_mul_f32_e32 v243, v57, v243\n" \
"v_mul_f32_e32 v244, v57, v244\n" \
"v_mul_f32_e32 v245, v57, v245\n" \
"v_mul_f32_e32 v246, v57, v246\n" \
"v_mul_f32_e32 v247, v57, v247\n" \
"buffer_load_dwordx4 v8, s[80:83], s92 offen offset:0 lds\n" \
"v_cvt_pk_bf16_f32 v96, v240, v241\n" \
"v_cvt_pk_bf16_f32 v97, v242, v243\n" \
"v_cvt_pk_bf16_f32 v98, v244, v245\n" \
"v_cvt_pk_bf16_f32 v99, v246, v247\n" \
"v_mfma_f32_16x16x32_bf16 v[252:255], v[128:131], v[188:191], v[252:255]\n" \
"v_mul_f32_e32 v248, v57, v248\n" \
"v_mfma_f32_16x16x32_bf16 a[208:211], a[216:219], v[96:99], a[208:211]\n" \
"v_mul_f32_e32 v249, v57, v249\n" \
"v_mul_f32_e32 v250, v57, v250\n" \
"v_mul_f32_e32 v251, v57, v251\n" \
"v_cvt_pk_bf16_f32 v100, v248, v249\n" \
"v_cvt_pk_bf16_f32 v101, v250, v251\n" \
"v_cmp_lt_f32_e64 s[72:73], s21, v62\n" \
"v_mul_f32_e32 v252, v57, v252\n" \
"v_mul_f32_e32 v253, v57, v253\n" \
"v_mul_f32_e32 v254, v57, v254\n" \
"v_mul_f32_e32 v255, v57, v255\n" \
"v_cvt_pk_bf16_f32 v102, v252, v253\n" \
"v_cvt_pk_bf16_f32 v103, v254, v255\n" \
"s_nop 1\n" \
"v_mfma_f32_16x16x32_bf16 a[208:211], a[216:219], v[100:103], a[208:211]\n" \
"s_nop 7\n" \
"v_accvgpr_read_b32 v63, a208\n" \
"v_cmp_u_f32_e32 vcc, v63, v63\n" \
"s_nop 0\n" \
"s_or_b64 vcc, s[72:73], vcc\n" \
"s_cbranch_scc0 L_out_%=\n" \
"s_branch L_end_%=\n" \
"L_skb_%=:\n" \
"s_sleep 1\n" \
"buffer_load_dword v57, v61, s[56:59], s96 offen sc1\n" \
"s_add_u32 s101, s101, 1\n" \
"s_waitcnt vmcnt(0)\n" \
"v_cmp_ne_u32_e32 vcc, s62, v57\n" \
"s_cmp_gt_u32 s101, 0x100000\n" \
"s_cbranch_scc1 L_rkb_%=\n" \
"s_nop 2\n" \
"s_cbranch_vccnz L_skb_%=\n" \
"s_branch L_rkb_%=\n" \
"L_sol_%=:\n" \
"s_sleep 1\n" \
"buffer_load_dword v26, v27, s[56:59], s96 offen sc1\n" \
"s_add_u32 s101, s101, 1\n" \
"s_waitcnt vmcnt(0)\n" \
"v_cmp_ne_u32_e32 vcc, s62, v26\n" \
"s_cmp_gt_u32 s101, 0x100000\n" \
"s_cbranch_scc1 L_rol_%=\n" \
"s_nop 2\n" \
"s_cbranch_vccnz L_sol_%=\n" \
"s_branch L_rol_%=\n" \
"L_sos_%=:\n" \
"s_sleep 1\n" \
"buffer_load_dword v26, v27, s[56:59], s96 offen sc1\n" \
"s_add_u32 s101, s101, 1\n" \
"s_waitcnt vmcnt(0)\n" \
"v_cmp_ne_u32_e32 vcc, s62, v26\n" \
"s_cmp_gt_u32 s101, 0x100000\n" \
"s_cbranch_scc1 L_ros_%=\n" \
"s_nop 2\n" \
"s_cbranch_vccnz L_sos_%=\n" \
"s_branch L_ros_%=\n" \
"L_sa_%=:\n" \
"s_sleep 1\n" \
"buffer_load_dword v26, v38, s[56:59], s64 offen sc1\n" \
"s_add_u32 s101, s101, 1\n" \
"s_waitcnt vmcnt(0)\n" \
"v_cmp_ne_u32_e32 vcc, s62, v26\n" \
"s_cmp_gt_u32 s101, 0x100000\n" \
"s_cbranch_scc1 L_ra_%=\n" \
"s_nop 2\n" \
"s_cbranch_vccnz L_sa_%=\n" \
"s_branch L_ra_%=\n" \
"L_sb_%=:\n" \
"s_sleep 1\n" \
"buffer_load_dword v47, v38, s[56:59], s71 offen sc1\n" \
"s_add_u32 s101, s101, 1\n" \
"s_waitcnt vmcnt(0)\n" \
"v_cmp_ne_u32_e32 vcc, s62, v47\n" \
"s_cmp_gt_u32 s101, 0x100000\n" \
"s_cbranch_scc1 L_rb_%=\n" \
"s_nop 2\n" \
"s_cbranch_vccnz L_sb_%=\n" \
"s_branch L_rb_%=\n" \
"L_sna_%=:\n" \
"buffer_load_dword v37, v40, s[56:59], s74 offen sc1\n" \
"s_add_u32 s101, s101, 1\n" \
"s_waitcnt vmcnt(0)\n" \
"v_cmp_eq_u32_e64 s[98:99], s62, v37\n" \
"s_cmp_gt_u32 s101, 0x100000\n" \
"s_cbranch_scc1 L_rna_%=\n" \
"s_andn2_b32 s75, s20, s98\n" \
"s_cbranch_scc0 L_gna_%=\n" \
"s_sleep 1\n" \
"s_branch L_sna_%=\n" \
"L_gna_%=:\n" \
"s_bitcmp1_b32 s20, 0\n" \
"s_cbranch_scc0 L_kna0_%=\n" \
"buffer_load_dwordx4 v[116:119], v20, s[56:59], s12 offen offset:0 sc1\n" \
"L_kna0_%=:\n" \
"s_bitcmp1_b32 s20, 1\n" \
"s_cbranch_scc0 L_kna1_%=\n" \
"buffer_load_dwordx4 v[124:127], v20, s[56:59], s12 offen offset:2048 sc1\n" \
"L_kna1_%=:\n" \
"s_bitcmp1_b32 s20, 2\n" \
"s_cbranch_scc0 L_kna2_%=\n" \
"buffer_load_dwordx4 v[132:135], v20, s[56:59], s13 offen offset:0 sc1\n" \
"L_kna2_%=:\n" \
"s_bitcmp1_b32 s20, 3\n" \
"s_cbranch_scc0 L_kna3_%=\n" \
"buffer_load_dwordx4 v[140:143], v20, s[56:59], s13 offen offset:2048 sc1\n" \
"L_kna3_%=:\n" \
"s_bitcmp1_b32 s20, 4\n" \
"s_cbranch_scc0 L_kna4_%=\n" \
"buffer_load_dwordx4 v[148:151], v20, s[56:59], s14 offen offset:0 sc1\n" \
"L_kna4_%=:\n" \
"s_bitcmp1_b32 s20, 5\n" \
"s_cbranch_scc0 L_kna5_%=\n" \
"buffer_load_dwordx4 v[156:159], v20, s[56:59], s14 offen offset:2048 sc1\n" \
"L_kna5_%=:\n" \
"s_bitcmp1_b32 s20, 6\n" \
"s_cbranch_scc0 L_kna6_%=\n" \
"buffer_load_dwordx4 v[164:167], v20, s[56:59], s15 offen offset:0 sc1\n" \
"L_kna6_%=:\n" \
"s_waitcnt vmcnt(0)\n" \
"s_branch L_rna_%=\n" \
"L_snx_%=:\n" \
"buffer_load_dword v37, v40, s[56:59], s74 offen sc1\n" \
"s_add_u32 s101, s101, 1\n" \
"s_waitcnt vmcnt(0)\n" \
"v_cmp_eq_u32_e64 s[98:99], s62, v37\n" \
"s_cmp_gt_u32 s101, 0x100000\n" \
"s_cbranch_scc1 L_rnx_%=\n" \
"s_andn2_b32 s75, s21, s98\n" \
"s_cbranch_scc0 L_gnx_%=\n" \
"s_sleep 1\n" \
"s_branch L_snx_%=\n" \
"L_gnx_%=:\n" \
"s_bitcmp1_b32 s21, 16\n" \
"s_cbranch_scc0 L_knx16_%=\n" \
"buffer_load_dwordx4 v[112:115], v20, s[56:59], s16 offen offset:0 sc1\n" \
"L_knx16_%=:\n" \
"s_bitcmp1_b32 s21, 17\n" \
"s_cbranch_scc0 L_knx17_%=\n" \
"buffer_load_dwordx4 v[184:187], v20, s[56:59], s16 offen offset:2048 sc1\n" \
"L_knx17_%=:\n" \
"s_bitcmp1_b32 s21, 18\n" \
"s_cbranch_scc0 L_knx18_%=\n" \
"buffer_load_dwordx4 v[120:123], v20, s[56:59], s17 offen offset:0 sc1\n" \
"L_knx18_%=:\n" \
"s_bitcmp1_b32 s21, 19\n" \
"s_cbranch_scc0 L_knx19_%=\n" \
"buffer_load_dwordx4 v[192:195], v20, s[56:59], s17 offen offset:2048 sc1\n" \
"L_knx19_%=:\n" \
"s_bitcmp1_b32 s21, 20\n" \
"s_cbranch_scc0 L_knx20_%=\n" \
"buffer_load_dwordx4 v[128:131], v20, s[56:59], s18 offen offset:0 sc1\n" \
"L_knx20_%=:\n" \
"s_bitcmp1_b32 s21, 21\n" \
"s_cbranch_scc0 L_knx21_%=\n" \
"buffer_load_dwordx4 v[200:203], v20, s[56:59], s18 offen offset:2048 sc1\n" \
"L_knx21_%=:\n" \
"s_bitcmp1_b32 s21, 22\n" \
"s_cbranch_scc0 L_knx22_%=\n" \
"buffer_load_dwordx4 v[176:179], v20, s[56:59], s19 offen offset:0 sc1\n" \
"L_knx22_%=:\n" \
"s_bitcmp1_b32 s21, 23\n" \
"s_cbranch_scc0 L_knx23_%=\n" \
"buffer_load_dwordx4 v[248:251], v20, s[56:59], s19 offen offset:2048 sc1\n" \
"L_knx23_%=:\n" \
"s_bitcmp1_b32 s21, 8\n" \
"s_cbranch_scc0 L_knx8_%=\n" \
"buffer_load_dwordx4 v[188:191], v20, s[56:59], s12 offen offset:1024 sc1\n" \
"L_knx8_%=:\n" \
"s_bitcmp1_b32 s21, 9\n" \
"s_cbranch_scc0 L_knx9_%=\n" \
"buffer_load_dwordx4 v[196:199], v20, s[56:59], s12 offen offset:3072 sc1\n" \
"L_knx9_%=:\n" \
"s_waitcnt vmcnt(0)\n" \
"s_branch L_rnx_%=\n" \
"L_sx0_%=:\n" \
"s_sleep 1\n" \
"buffer_load_dword v37, v40, s[56:59], s74 offen sc1\n" \
"s_add_u32 s101, s101, 1\n" \
"s_waitcnt vmcnt(0)\n" \
"v_cmp_eq_u32_e64 s[98:99], s62, v37\n" \
"s_cmp_gt_u32 s101, 0x100000\n" \
"s_cbranch_scc1 L_rx0_%=\n" \
"s_andn2_b32 s75, 0x400, s98\n" \
"s_cbranch_scc1 L_sx0_%=\n" \
"s_branch L_rx0_%=\n" \
"L_sx1_%=:\n" \
"s_sleep 1\n" \
"buffer_load_dword v37, v40, s[56:59], s74 offen sc1\n" \
"s_add_u32 s101, s101, 1\n" \
"s_waitcnt vmcnt(0)\n" \
"v_cmp_eq_u32_e64 s[98:99], s62, v37\n" \
"s_cmp_gt_u32 s101, 0x100000\n" \
"s_cbranch_scc1 L_rx1_%=\n" \
"s_andn2_b32 s75, 0x800, s98\n" \
"s_cbranch_scc1 L_sx1_%=\n" \
"s_branch L_rx1_%=\n" \
"L_sx2_%=:\n" \
"s_sleep 1\n" \
"buffer_load_dword v37, v40, s[56:59], s74 offen sc1\n" \
"s_add_u32 s101, s101, 1\n" \
"s_waitcnt vmcnt(0)\n" \
"v_cmp_eq_u32_e64 s[98:99], s62, v37\n" \
"s_cmp_gt_u32 s101, 0x100000\n" \
"s_cbranch_scc1 L_rx2_%=\n" \
"s_andn2_b32 s75, 0x1000, s98\n" \
"s_cbranch_scc1 L_sx2_%=\n" \
"s_branch L_rx2_%=\n" \
"L_sx3_%=:\n" \
"s_sleep 1\n" \
"buffer_load_dword v37, v40, s[56:59], s74 offen sc1\n" \
"s_add_u32 s101, s101, 1\n" \
"s_waitcnt vmcnt(0)\n" \
"v_cmp_eq_u32_e64 s[98:99], s62, v37\n" \
"s_cmp_gt_u32 s101, 0x100000\n" \
"s_cbranch_scc1 L_rx3_%=\n" \
"s_andn2_b32 s75, 0x2000, s98\n" \
"s_cbranch_scc1 L_sx3_%=\n" \
"s_branch L_rx3_%=\n" \
"L_sx4_%=:\n" \
"s_sleep 1\n" \
"buffer_load_dword v37, v40, s[56:59], s74 offen sc1\n" \
"s_add_u32 s101, s101, 1\n" \
"s_waitcnt vmcnt(0)\n" \
"v_cmp_eq_u32_e64 s[98:99], s62, v37\n" \
"s_cmp_gt_u32 s101, 0x100000\n" \
"s_cbranch_scc1 L_rx4_%=\n" \
"s_andn2_b32 s75, 0x4000, s98\n" \
"s_cbranch_scc1 L_sx4_%=\n" \
"s_branch L_rx4_%=\n" \
"L_att_stub:\n" \
"s_mov_b32 s101, 0\n" \
"v_add_u32_e32 v32, 0x49000, v20\n" \
"v_readlane_b32 s96, v11, 36\n" \
"v_readlane_b32 s97, v11, 37\n" \
"buffer_load_dwordx4 a[96:99], v20, s[56:59], s60 offen offset:0 sc1\n" \
"buffer_load_dwordx4 a[112:115], v32, s[56:59], s60 offen offset:0 sc1\n" \
"buffer_load_dwordx4 a[100:103], v20, s[56:59], s60 offen offset:1024 sc1\n" \
"buffer_load_dwordx4 a[116:119], v32, s[56:59], s60 offen offset:1024 sc1\n" \
"buffer_load_dwordx4 a[104:107], v20, s[56:59], s60 offen offset:2048 sc1\n" \
"buffer_load_dwordx4 a[120:123], v32, s[56:59], s60 offen offset:2048 sc1\n" \
"buffer_load_dwordx4 a[108:111], v20, s[56:59], s60 offen offset:3072 sc1\n" \
"buffer_load_dwordx4 a[124:127], v32, s[56:59], s60 offen offset:3072 sc1\n" \
"buffer_load_dwordx4 a[220:223], v20, s[56:59], s96 offen offset:0 sc1\n" \
"buffer_load_dwordx4 a[228:231], v32, s[56:59], s96 offen offset:0 sc1\n" \
"buffer_load_dwordx4 a[236:239], v20, s[56:59], s97 offen offset:0 sc1\n" \
"buffer_load_dwordx4 a[244:247], v32, s[56:59], s97 offen offset:0 sc1\n" \
"buffer_load_dwordx4 a[224:227], v20, s[56:59], s96 offen offset:1024 sc1\n" \
"buffer_load_dwordx4 a[232:235], v32, s[56:59], s96 offen offset:1024 sc1\n" \
"buffer_load_dwordx4 a[240:243], v20, s[56:59], s97 offen offset:1024 sc1\n" \
"buffer_load_dwordx4 a[248:251], v32, s[56:59], s97 offen offset:1024 sc1\n" \
"buffer_load_dwordx4 v[64:67], v20, s[56:59], s61 offen offset:0 sc1\n" \
"buffer_load_dwordx4 v[68:71], v20, s[56:59], s61 offen offset:1024 sc1\n" \
"buffer_load_dwordx4 v[72:75], v20, s[56:59], s61 offen offset:2048 sc1\n" \
"buffer_load_dwordx4 v[76:79], v20, s[56:59], s61 offen offset:3072 sc1\n" \
"buffer_load_dwordx4 v[80:83], v32, s[56:59], s61 offen offset:0 sc1\n" \
"buffer_load_dwordx4 v[84:87], v32, s[56:59], s61 offen offset:1024 sc1\n" \
"buffer_load_dwordx4 v[96:99], v32, s[56:59], s61 offen offset:2048 sc1\n" \
"buffer_load_dwordx4 v[100:103], v32, s[56:59], s61 offen offset:3072 sc1\n" \
"v_not_b32_e32 v26, s62\n" \
"s_waitcnt vmcnt(0)\n" \
"s_branch L_out_%=\n" \
".fill (L_ab + 16384 - .) / 4, 4, 0xBF800000\n" \
".fill (L_ab + 16384 - .) / 4, 4, 0xBF800000\n" \
"L_ap4:\n" \
"s_setpc_b64 s[88:89]\n" \
"L_gsw:\n" \
"v_mfma_f32_16x16x32_bf16 v[104:107], a[128:131], a[16:19], v[36:39]\n" \
"s_add_u32 s68, s60, 0xfff6e000\n" \
"s_add_u32 s69, s60, 0xfffb7000\n" \
"s_add_u32 s70, s60, 0x0\n" \
"s_add_u32 s71, s60, 0x49000\n" \
"v_mfma_f32_16x16x32_bf16 v[104:107], a[132:135], a[20:23], v[104:107]\n" \
"s_xor_b32 m0, s65, 0x12000\n" \
"ds_read_b128 a[160:163], v49 offset:0\n" \
"v_mfma_f32_16x16x32_bf16 v[104:107], a[136:139], a[24:27], v[104:107]\n" \
"buffer_load_dwordx4 v50, s[8:11], s64 offen offset:0 sc1 lds\n" \
"v_mfma_f32_16x16x32_bf16 v[104:107], a[140:143], a[28:31], v[104:107]\n" \
"ds_read_b128 a[164:167], v49 offset:1024\n" \
"ds_read_b128 a[168:171], v49 offset:2048\n" \
"v_mfma_f32_16x16x32_bf16 v[108:111], a[156:159], a[28:31], v[36:39]\n" \
"ds_read_b128 a[172:175], v49 offset:3072\n" \
"ds_read_b128 a[176:179], v49 offset:4096\n" \
"v_mfma_f32_16x16x32_bf16 v[108:111], a[152:155], a[24:27], v[108:111]\n" \
"buffer_load_dwordx4 v50, s[8:11], s64 offen offset:1024 sc1 lds\n" \
"v_mfma_f32_16x16x32_bf16 v[108:111], a[148:151], a[20:23], v[108:111]\n" \
"s_mov_b32 s65, s13\n" \
"v_add_u32_e32 v63, s15, v20\n" \
"v_mfma_f32_16x16x32_bf16 v[108:111], a[144:147], a[16:19], v[108:111]\n" \
"s_add_u32 s67, s14, 40960\n" \
"v_add_u32_e32 v50, 0x6000, v63\n" \
"v_mfma_f32_16x16x32_bf16 v[116:119], a[144:147], a[32:35], v[40:43]\n" \
"s_xor_b32 m0, s65, 0x8000\n" \
"ds_read_b128 a[180:183], v49 offset:5120\n" \
"v_mfma_f32_16x16x32_bf16 v[116:119], a[148:151], a[36:39], v[116:119]\n" \
"ds_read_b128 a[184:187], v49 offset:6144\n" \
"v_and_b32_e32 v68, v12, v68\n" \
"v_mfma_f32_16x16x32_bf16 v[116:119], a[152:155], a[40:43], v[116:119]\n" \
"v_and_b32_e32 v69, v13, v69\n" \
"v_and_b32_e32 v70, v14, v70\n" \
"v_mfma_f32_16x16x32_bf16 v[116:119], a[156:159], a[44:47], v[116:119]\n" \
"buffer_load_dwordx4 v50, s[8:11], s67 offen offset:0 sc1 lds\n" \
"v_mfma_f32_16x16x32_bf16 v[112:115], a[140:143], a[44:47], v[40:43]\n" \
"v_and_b32_e32 v71, v15, v71\n" \
"ds_read_b128 a[188:191], v49 offset:7168\n" \
"v_mfma_f32_16x16x32_bf16 v[112:115], a[136:139], a[40:43], v[112:115]\n" \
"v_exp_f32_e32 v86, v86\n" \
"v_mfma_f32_16x16x32_bf16 v[112:115], a[132:135], a[36:39], v[112:115]\n" \
"v_exp_f32_e32 v87, v87\n" \
"v_mfma_f32_16x16x32_bf16 v[112:115], a[128:131], a[32:35], v[112:115]\n" \
"v_exp_f32_e32 v84, v84\n" \
"v_mfma_f32_16x16x32_bf16 v[120:123], a[128:131], a[48:51], v[44:47]\n" \
"buffer_load_dwordx4 v50, s[8:11], s67 offen offset:1024 sc1 lds\n" \
"v_mfma_f32_16x16x32_bf16 v[120:123], a[132:135], a[52:55], v[120:123]\n" \
"v_exp_f32_e32 v85, v85\n" \
"v_mfma_f32_16x16x32_bf16 v[120:123], a[136:139], a[56:59], v[120:123]\n" \
"v_cvt_pk_bf16_f32 v87, v86, v87\n" \
"v_cvt_pk_bf16_f32 v86, v84, v85\n" \
"v_mfma_f32_16x16x32_bf16 v[120:123], a[140:143], a[60:63], v[120:123]\n" \
"v_exp_f32_e32 v82, v82\n" \
"v_mfma_f32_16x16x32_bf16 v[124:127], a[156:159], a[60:63], v[44:47]\n" \
"v_exp_f32_e32 v83, v83\n" \
"v_mfma_f32_16x16x32_bf16 v[124:127], a[152:155], a[56:59], v[124:127]\n" \
"v_exp_f32_e32 v80, v80\n" \
"v_mfma_f32_16x16x32_bf16 v[124:127], a[148:151], a[52:55], v[124:127]\n" \
"buffer_load_dwordx4 v50, s[8:11], s67 offen offset:2048 sc1 lds\n" \
"v_mfma_f32_16x16x32_bf16 v[124:127], a[144:147], a[48:51], v[124:127]\n" \
"v_cvt_pk_bf16_f32 v85, v82, v83\n" \
"s_waitcnt lgkmcnt(1)\n" \
"v_mfma_f32_16x16x32_bf16 v[128:131], a[160:163], v[68:71], v[128:131]\n" \
"v_exp_f32_e32 v81, v81\n" \
"v_mfma_f32_16x16x32_bf16 v[160:163], a[160:163], v[72:75], v[160:163]\n" \
"v_cvt_pk_bf16_f32 v84, v80, v81\n" \
"s_mov_b32 s64, s14\n" \
"v_mfma_f32_16x16x32_bf16 v[132:135], a[164:167], v[68:71], v[132:135]\n" \
"v_exp_f32_e32 v88, v88\n" \
"v_mfma_f32_16x16x32_bf16 v[164:167], a[164:167], v[72:75], v[164:167]\n" \
"v_exp_f32_e32 v89, v89\n" \
"v_mfma_f32_16x16x32_bf16 v[136:139], a[168:171], v[68:71], v[136:139]\n" \
"buffer_load_dwordx4 v50, s[8:11], s67 offen offset:3072 sc1 lds\n" \
"v_mfma_f32_16x16x32_bf16 v[168:171], a[168:171], v[72:75], v[168:171]\n" \
"v_exp_f32_e32 v90, v90\n" \
"v_mfma_f32_16x16x32_bf16 v[140:143], a[172:175], v[68:71], v[140:143]\n" \
"v_exp_f32_e32 v91, v91\n" \
"v_mfma_f32_16x16x32_bf16 v[172:175], a[172:175], v[72:75], v[172:175]\n" \
"v_cvt_pk_bf16_f32 v88, v88, v89\n" \
"v_cvt_pk_bf16_f32 v89, v90, v91\n" \
"v_mfma_f32_16x16x32_bf16 v[144:147], a[176:179], v[68:71], v[144:147]\n" \
"v_exp_f32_e32 v92, v92\n" \
"v_mfma_f32_16x16x32_bf16 v[176:179], a[176:179], v[72:75], v[176:179]\n" \
"v_exp_f32_e32 v93, v93\n" \
"v_mfma_f32_16x16x32_bf16 v[148:151], a[180:183], v[68:71], v[148:151]\n" \
"v_exp_f32_e32 v94, v94\n" \
"v_mfma_f32_16x16x32_bf16 v[180:183], a[180:183], v[72:75], v[180:183]\n" \
"v_exp_f32_e32 v95, v95\n" \
"v_mfma_f32_16x16x32_bf16 v[152:155], a[184:187], v[68:71], v[152:155]\n" \
"v_cvt_pk_bf16_f32 v90, v92, v93\n" \
"v_cvt_pk_bf16_f32 v91, v94, v95\n" \
"v_mfma_f32_16x16x32_bf16 v[184:187], a[184:187], v[72:75], v[184:187]\n" \
"v_add_u32_e32 v51, 0x12000, v63\n" \
"s_waitcnt lgkmcnt(0)\n" \
"v_mfma_f32_16x16x32_bf16 v[156:159], a[188:191], v[68:71], v[156:159]\n" \
"s_xor_b32 m0, s65, 0xa000\n" \
"ds_read_b32 v58, v18 offset:512\n" \
"v_mfma_f32_16x16x32_bf16 v[188:191], a[188:191], v[72:75], v[188:191]\n" \
"buffer_load_dwordx4 v51, s[8:11], s64 offen offset:0 sc1 lds\n" \
"v_smfmac_f32_16x16x64_bf16 a[192:195], a[200:203], v[68:75], v21\n" \
"buffer_load_dwordx4 v51, s[8:11], s64 offen offset:1024 sc1 lds\n" \
"v_mfma_f32_16x16x32_bf16 v[220:223], a[188:191], v[84:87], v[220:223]\n" \
"ds_read_b32 v59, v18 offset:640\n" \
"ds_read_b32 v60, v18 offset:768\n" \
"v_mfma_f32_16x16x32_bf16 v[252:255], a[188:191], v[88:91], v[252:255]\n" \
"ds_read_b32 v61, v18 offset:896\n" \
"ds_read_b128 a[128:131], v48 offset:0\n" \
"v_mfma_f32_16x16x32_bf16 v[216:219], a[184:187], v[84:87], v[216:219]\n" \
"ds_read_b128 a[144:147], v48 offset:4096\n" \
"ds_read_b128 a[132:135], v48 offset:1024\n" \
"v_mfma_f32_16x16x32_bf16 v[248:251], a[184:187], v[88:91], v[248:251]\n" \
"ds_read_b128 a[148:151], v48 offset:5120\n" \
"ds_read_b128 a[136:139], v48 offset:2048\n" \
"v_mfma_f32_16x16x32_bf16 v[212:215], a[180:183], v[84:87], v[212:215]\n" \
"ds_read_b128 a[152:155], v48 offset:6144\n" \
"ds_read_b128 a[140:143], v48 offset:3072\n" \
"v_mfma_f32_16x16x32_bf16 v[244:247], a[180:183], v[88:91], v[244:247]\n" \
"ds_read_b128 a[156:159], v48 offset:7168\n" \
"v_add_u32_e32 v52, 0x14000, v63\n" \
"v_mfma_f32_16x16x32_bf16 v[208:211], a[176:179], v[84:87], v[208:211]\n" \
"buffer_load_dwordx4 v51, s[8:11], s64 offen offset:2048 sc1 lds\n" \
"v_mfma_f32_16x16x32_bf16 v[240:243], a[176:179], v[88:91], v[240:243]\n" \
"v_exp_f32_e32 v104, v104\n" \
"v_mfma_f32_16x16x32_bf16 v[204:207], a[172:175], v[84:87], v[204:207]\n" \
"v_exp_f32_e32 v105, v105\n" \
"v_mfma_f32_16x16x32_bf16 v[236:239], a[172:175], v[88:91], v[236:239]\n" \
"buffer_load_dwordx4 v51, s[8:11], s64 offen offset:3072 sc1 lds\n" \
"v_mfma_f32_16x16x32_bf16 v[200:203], a[168:171], v[84:87], v[200:203]\n" \
"v_exp_f32_e32 v106, v106\n" \
"v_mfma_f32_16x16x32_bf16 v[232:235], a[168:171], v[88:91], v[232:235]\n" \
"v_exp_f32_e32 v107, v107\n" \
"v_mfma_f32_16x16x32_bf16 v[196:199], a[164:167], v[84:87], v[196:199]\n" \
"v_cvt_pk_bf16_f32 v104, v104, v105\n" \
"v_cvt_pk_bf16_f32 v105, v106, v107\n" \
"v_mfma_f32_16x16x32_bf16 v[228:231], a[164:167], v[88:91], v[228:231]\n" \
"v_exp_f32_e32 v108, v108\n" \
"v_mfma_f32_16x16x32_bf16 v[192:195], a[160:163], v[84:87], v[192:195]\n" \
"v_exp_f32_e32 v109, v109\n" \
"v_mfma_f32_16x16x32_bf16 v[224:227], a[160:163], v[88:91], v[224:227]\n" \
"v_exp_f32_e32 v110, v110\n" \
"v_smfmac_f32_16x16x64_bf16 a[196:199], a[200:203], v[84:91], v21\n" \
"v_exp_f32_e32 v111, v111\n" \
"v_cvt_pk_bf16_f32 v106, v108, v109\n" \
"v_cvt_pk_bf16_f32 v107, v110, v111\n" \
"v_and_b32_e32 v104, v12, v104\n" \
"v_and_b32_e32 v105, v13, v105\n" \
"v_and_b32_e32 v106, v14, v106\n" \
"v_and_b32_e32 v107, v15, v107\n" \
"v_accvgpr_read_b32 v54, a192\n" \
"v_accvgpr_read_b32 v22, a193\n" \
"v_add_u32_e32 v53, 0x20000, v63\n" \
"v_exp_f32_e32 v118, v118\n" \
"v_exp_f32_e32 v119, v119\n" \
"v_exp_f32_e32 v116, v116\n" \
"v_exp_f32_e32 v117, v117\n" \
"v_cvt_pk_bf16_f32 v119, v118, v119\n" \
"v_exp_f32_e32 v114, v114\n" \
"v_exp_f32_e32 v115, v115\n" \
"v_exp_f32_e32 v112, v112\n" \
"v_exp_f32_e32 v113, v113\n" \
"v_cvt_pk_bf16_f32 v118, v116, v117\n" \
"v_cvt_pk_bf16_f32 v117, v114, v115\n" \
"v_cvt_pk_bf16_f32 v116, v112, v113\n" \
"v_readlane_b32 s93, v11, 19\n" \
"s_waitcnt vmcnt(10) lgkmcnt(0)\n" \
"s_barrier\n" \
"v_mfma_f32_16x16x32_bf16 v[80:83], a[128:131], a[32:35], v[40:43]\n" \
"ds_read_b128 a[160:163], v49 offset:8192\n" \
"ds_read_b128 a[164:167], v49 offset:9216\n" \
"v_mfma_f32_16x16x32_bf16 v[80:83], a[132:135], a[36:39], v[80:83]\n" \
"ds_read_b128 a[168:171], v49 offset:10240\n" \
"s_xor_b32 m0, s65, 0x18000\n" \
"v_mfma_f32_16x16x32_bf16 v[80:83], a[136:139], a[40:43], v[80:83]\n" \
"buffer_load_dwordx4 v52, s[8:11], s64 offen offset:0 sc1 lds\n" \
"v_mfma_f32_16x16x32_bf16 v[80:83], a[140:143], a[44:47], v[80:83]\n" \
"ds_read_b128 a[172:175], v49 offset:11264\n" \
"ds_read_b128 a[176:179], v49 offset:12288\n" \
"v_mfma_f32_16x16x32_bf16 v[84:87], a[156:159], a[44:47], v[40:43]\n" \
"ds_read_b128 a[180:183], v49 offset:13312\n" \
"ds_read_b128 a[184:187], v49 offset:14336\n" \
"v_mfma_f32_16x16x32_bf16 v[84:87], a[152:155], a[40:43], v[84:87]\n" \
"buffer_load_dwordx4 v52, s[8:11], s64 offen offset:1024 sc1 lds\n" \
"v_mfma_f32_16x16x32_bf16 v[84:87], a[148:151], a[36:39], v[84:87]\n" \
"ds_read_b128 a[188:191], v49 offset:15360\n" \
"v_readlane_b32 s80, v11, 11\n" \
"v_mfma_f32_16x16x32_bf16 v[84:87], a[144:147], a[32:35], v[84:87]\n" \
"buffer_load_dwordx4 v52, s[8:11], s64 offen offset:2048 sc1 lds\n" \
"v_mfma_f32_16x16x32_bf16 v[92:95], a[144:147], a[48:51], v[44:47]\n" \
"v_exp_f32_e32 v120, v120\n" \
"v_mfma_f32_16x16x32_bf16 v[92:95], a[148:151], a[52:55], v[92:95]\n" \
"v_exp_f32_e32 v121, v121\n" \
"v_mfma_f32_16x16x32_bf16 v[92:95], a[152:155], a[56:59], v[92:95]\n" \
"buffer_load_dwordx4 v52, s[8:11], s64 offen offset:3072 sc1 lds\n" \
"v_mfma_f32_16x16x32_bf16 v[92:95], a[156:159], a[60:63], v[92:95]\n" \
"s_mov_b32 m0, s65\n" \
"v_cvt_pk_bf16_f32 v120, v120, v121\n" \
"v_mfma_f32_16x16x32_bf16 v[88:91], a[140:143], a[60:63], v[44:47]\n" \
"v_exp_f32_e32 v122, v122\n" \
"v_mfma_f32_16x16x32_bf16 v[88:91], a[136:139], a[56:59], v[88:91]\n" \
"buffer_load_dwordx4 v53, s[8:11], s64 offen offset:0 sc1 lds\n" \
"v_mfma_f32_16x16x32_bf16 v[88:91], a[132:135], a[52:55], v[88:91]\n" \
"v_exp_f32_e32 v123, v123\n" \
"v_mfma_f32_16x16x32_bf16 v[88:91], a[128:131], a[48:51], v[88:91]\n" \
"v_exp_f32_e32 v124, v124\n" \
"v_mfma_f32_16x16x32_bf16 a[192:195], a[212:215], v[104:107], a[192:195]\n" \
"v_cvt_pk_bf16_f32 v121, v122, v123\n" \
"s_waitcnt lgkmcnt(5)\n" \
"v_mfma_f32_16x16x32_bf16 v[160:163], a[160:163], v[104:107], v[160:163]\n" \
"buffer_load_dwordx4 v53, s[8:11], s64 offen offset:1024 sc1 lds\n" \
"v_mfma_f32_16x16x32_bf16 v[164:167], a[164:167], v[104:107], v[164:167]\n" \
"v_exp_f32_e32 v125, v125\n" \
"v_mfma_f32_16x16x32_bf16 v[168:171], a[168:171], v[104:107], v[168:171]\n" \
"v_cvt_pk_bf16_f32 v122, v124, v125\n" \
"s_waitcnt lgkmcnt(1)\n" \
"v_mfma_f32_16x16x32_bf16 v[172:175], a[172:175], v[104:107], v[172:175]\n" \
"v_exp_f32_e32 v126, v126\n" \
"v_mfma_f32_16x16x32_bf16 v[176:179], a[176:179], v[104:107], v[176:179]\n" \
"v_exp_f32_e32 v127, v127\n" \
"v_mfma_f32_16x16x32_bf16 v[180:183], a[180:183], v[104:107], v[180:183]\n" \
"v_cvt_pk_bf16_f32 v123, v126, v127\n" \
"v_readlane_b32 s81, v11, 12\n" \
"v_mfma_f32_16x16x32_bf16 v[184:187], a[184:187], v[104:107], v[184:187]\n" \
"s_mov_b32 s82, 0x47fca0\n" \
"s_waitcnt lgkmcnt(0)\n" \
"v_mfma_f32_16x16x32_bf16 v[188:191], a[188:191], v[104:107], v[188:191]\n" \
"buffer_load_dwordx4 v53, s[8:11], s64 offen offset:2048 sc1 lds\n" \
"v_mfma_f32_16x16x32_bf16 v[220:223], a[188:191], v[116:119], v[220:223]\n" \
"buffer_load_dwordx4 v53, s[8:11], s64 offen offset:3072 sc1 lds\n" \
"v_mfma_f32_16x16x32_bf16 v[252:255], a[188:191], v[120:123], v[252:255]\n" \
"s_mov_b32 s83, 0x20000\n" \
"v_readlane_b32 s92, v11, 26\n" \
"v_mfma_f32_16x16x32_bf16 v[216:219], a[184:187], v[116:119], v[216:219]\n" \
"s_add_u32 m0, s93, 0x0\n" \
"ds_read_b128 a[128:131], v48 offset:8192\n" \
"v_mfma_f32_16x16x32_bf16 v[248:251], a[184:187], v[120:123], v[248:251]\n" \
"ds_read_b128 a[144:147], v48 offset:12288\n" \
"ds_read_b128 a[132:135], v48 offset:9216\n" \
"v_mfma_f32_16x16x32_bf16 v[212:215], a[180:183], v[116:119], v[212:215]\n" \
"buffer_load_dwordx4 v0, s[80:83], s92 offen offset:0 lds\n" \
"v_mfma_f32_16x16x32_bf16 v[244:247], a[180:183], v[120:123], v[244:247]\n" \
"buffer_load_dwordx4 v1, s[80:83], s92 offen offset:1024 lds\n" \
"v_mfma_f32_16x16x32_bf16 v[208:211], a[176:179], v[116:119], v[208:211]\n" \
"ds_read_b128 a[148:151], v48 offset:13312\n" \
"ds_read_b128 a[136:139], v48 offset:10240\n" \
"v_mfma_f32_16x16x32_bf16 v[240:243], a[176:179], v[120:123], v[240:243]\n" \
"ds_read_b128 a[152:155], v48 offset:14336\n" \
"ds_read_b128 a[140:143], v48 offset:11264\n" \
"v_mfma_f32_16x16x32_bf16 v[204:207], a[172:175], v[116:119], v[204:207]\n" \
"buffer_load_dwordx4 v2, s[80:83], s92 offen offset:2048 lds\n" \
"v_mfma_f32_16x16x32_bf16 v[236:239], a[172:175], v[120:123], v[236:239]\n" \
"ds_read_b128 a[156:159], v48 offset:15360\n" \
"s_xor_b32 s65, s65, 0x8000\n" \
"v_mfma_f32_16x16x32_bf16 v[200:203], a[168:171], v[116:119], v[200:203]\n" \
"s_add_u32 s64, s64, 0x10000\n" \
"v_add_u32_e32 v49, 0x10000, v48\n" \
"v_mfma_f32_16x16x32_bf16 v[232:235], a[168:171], v[120:123], v[232:235]\n" \
"buffer_load_dwordx4 v3, s[80:83], s92 offen offset:3072 lds\n" \
"v_mfma_f32_16x16x32_bf16 v[196:199], a[164:167], v[116:119], v[196:199]\n" \
"v_exp_f32_e32 v86, v86\n" \
"v_mfma_f32_16x16x32_bf16 v[228:231], a[164:167], v[120:123], v[228:231]\n" \
"v_exp_f32_e32 v87, v87\n" \
"v_mfma_f32_16x16x32_bf16 v[192:195], a[160:163], v[116:119], v[192:195]\n" \
"v_exp_f32_e32 v84, v84\n" \
"v_mfma_f32_16x16x32_bf16 v[224:227], a[160:163], v[120:123], v[224:227]\n" \
"v_exp_f32_e32 v85, v85\n" \
"v_smfmac_f32_16x16x64_bf16 a[196:199], a[200:203], v[116:123], v21\n" \
"v_exp_f32_e32 v88, v88\n" \
"v_exp_f32_e32 v89, v89\n" \
"v_cvt_pk_bf16_f32 v87, v86, v87\n" \
"v_exp_f32_e32 v82, v82\n" \
"v_exp_f32_e32 v83, v83\n" \
"v_exp_f32_e32 v80, v80\n" \
"v_exp_f32_e32 v81, v81\n" \
"v_exp_f32_e32 v90, v90\n" \
"v_exp_f32_e32 v91, v91\n" \
"v_cvt_pk_bf16_f32 v86, v84, v85\n" \
"v_cvt_pk_bf16_f32 v88, v88, v89\n" \
"v_exp_f32_e32 v92, v92\n" \
"v_exp_f32_e32 v93, v93\n" \
"v_exp_f32_e32 v94, v94\n" \
"v_exp_f32_e32 v95, v95\n" \
"v_cvt_pk_bf16_f32 v85, v82, v83\n" \
"v_cvt_pk_bf16_f32 v84, v80, v81\n" \
"v_cvt_pk_bf16_f32 v89, v90, v91\n" \
"v_and_b32_e32 v84, v12, v84\n" \
"v_and_b32_e32 v85, v13, v85\n" \
"v_and_b32_e32 v86, v14, v86\n" \
"v_and_b32_e32 v87, v15, v87\n" \
"v_cvt_pk_bf16_f32 v90, v92, v93\n" \
"v_cvt_pk_bf16_f32 v91, v94, v95\n" \
"v_add_u32_e32 v48, s16, v20\n" \
"v_add_f32_e32 v54, v54, v22\n" \
"v_accvgpr_read_b32 v55, a194\n" \
"v_accvgpr_read_b32 v23, a195\n" \
"v_mfma_f32_16x16x32_bf16 a[192:195], a[216:219], a[216:219], 0\n" \
"v_max_f32_e32 v62, v62, v54\n" \
"v_add_f32_e32 v55, v55, v23\n" \
"v_max_f32_e32 v62, v62, v55\n" \
"v_rcp_f32_e32 v54, v54\n" \
"v_rcp_f32_e32 v55, v55\n" \
"v_mul_f32_e32 v54, v54, v58\n" \
"v_mul_f32_e32 v55, v55, v59\n" \
"v_mul_f32_e32 v128, v54, v128\n" \
"v_mul_f32_e32 v129, v54, v129\n" \
"v_mul_f32_e32 v136, v54, v136\n" \
"v_mul_f32_e32 v137, v54, v137\n" \
"v_mul_f32_e32 v144, v54, v144\n" \
"v_mul_f32_e32 v145, v54, v145\n" \
"v_mul_f32_e32 v152, v54, v152\n" \
"v_mul_f32_e32 v153, v54, v153\n" \
"v_mul_f32_e32 v160, v55, v160\n" \
"v_mul_f32_e32 v161, v55, v161\n" \
"v_mul_f32_e32 v168, v55, v168\n" \
"v_mul_f32_e32 v169, v55, v169\n" \
"v_mul_f32_e32 v176, v55, v176\n" \
"v_mul_f32_e32 v177, v55, v177\n" \
"v_mul_f32_e32 v184, v55, v184\n" \
"v_mul_f32_e32 v185, v55, v185\n" \
"v_cvt_pk_bf16_f32 v128, v128, v129\n" \
"v_mul_f32_e32 v130, v54, v130\n" \
"v_mul_f32_e32 v131, v54, v131\n" \
"v_cvt_pk_bf16_f32 v136, v136, v137\n" \
"v_mul_f32_e32 v138, v54, v138\n" \
"v_mul_f32_e32 v139, v54, v139\n" \
"v_cvt_pk_bf16_f32 v144, v144, v145\n" \
"v_mul_f32_e32 v146, v54, v146\n" \
"v_mul_f32_e32 v147, v54, v147\n" \
"v_cvt_pk_bf16_f32 v152, v152, v153\n" \
"v_mul_f32_e32 v154, v54, v154\n" \
"v_mul_f32_e32 v155, v54, v155\n" \
"v_cvt_pk_bf16_f32 v160, v160, v161\n" \
"v_mul_f32_e32 v162, v55, v162\n" \
"v_mul_f32_e32 v163, v55, v163\n" \
"v_cvt_pk_bf16_f32 v168, v168, v169\n" \
"v_mul_f32_e32 v170, v55, v170\n" \
"v_mul_f32_e32 v171, v55, v171\n" \
"v_cvt_pk_bf16_f32 v176, v176, v177\n" \
"v_mul_f32_e32 v178, v55, v178\n" \
"v_mul_f32_e32 v179, v55, v179\n" \
"v_cvt_pk_bf16_f32 v184, v184, v185\n" \
"v_mul_f32_e32 v186, v55, v186\n" \
"v_mul_f32_e32 v187, v55, v187\n" \
"v_cvt_pk_bf16_f32 v129, v130, v131\n" \
"v_mul_f32_e32 v132, v54, v132\n" \
"v_mul_f32_e32 v133, v54, v133\n" \
"v_mul_f32_e32 v134, v54, v134\n" \
"v_mul_f32_e32 v135, v54, v135\n" \
"v_cvt_pk_bf16_f32 v137, v138, v139\n" \
"v_mul_f32_e32 v140, v54, v140\n" \
"s_waitcnt vmcnt(12) lgkmcnt(0)\n" \
"s_barrier\n" \
"v_mfma_f32_16x16x32_bf16 v[120:123], a[128:131], a[48:51], v[44:47]\n" \
"s_xor_b32 m0, s65, 0x12000\n" \
"ds_read_b128 a[188:191], v49 offset:7168\n" \
"v_mfma_f32_16x16x32_bf16 v[120:123], a[132:135], a[52:55], v[120:123]\n" \
"buffer_load_dwordx4 v50, s[8:11], s64 offen offset:0 sc1 lds\n" \
"v_mfma_f32_16x16x32_bf16 v[120:123], a[136:139], a[56:59], v[120:123]\n" \
"ds_read_b128 a[184:187], v49 offset:6144\n" \
"ds_read_b128 a[180:183], v49 offset:5120\n" \
"v_mfma_f32_16x16x32_bf16 v[120:123], a[140:143], a[60:63], v[120:123]\n" \
"buffer_load_dwordx4 v50, s[8:11], s64 offen offset:1024 sc1 lds\n" \
"v_mfma_f32_16x16x32_bf16 v[124:127], a[156:159], a[60:63], v[44:47]\n" \
"ds_read_b128 a[176:179], v49 offset:4096\n" \
"ds_read_b128 a[172:175], v49 offset:3072\n" \
"v_mfma_f32_16x16x32_bf16 v[124:127], a[152:155], a[56:59], v[124:127]\n" \
"ds_read_b128 a[168:171], v49 offset:2048\n" \
"ds_read_b128 a[164:167], v49 offset:1024\n" \
"v_mfma_f32_16x16x32_bf16 v[124:127], a[148:151], a[52:55], v[124:127]\n" \
"ds_read_b128 a[160:163], v49 offset:0\n" \
"ds_read_b128 a[0:3], v49 offset:8192\n" \
"v_mfma_f32_16x16x32_bf16 v[124:127], a[144:147], a[48:51], v[124:127]\n" \
"buffer_load_dwordx4 v50, s[8:11], s64 offen offset:2048 sc1 lds\n" \
"v_mfma_f32_16x16x32_bf16 v[32:35], a[220:223], a[64:67], 0\n" \
"ds_read_b128 a[4:7], v49 offset:9216\n" \
"ds_read_b128 a[8:11], v49 offset:10240\n" \
"v_mfma_f32_16x16x32_bf16 v[36:39], a[220:223], a[80:83], 0\n" \
"ds_read_b128 a[12:15], v49 offset:11264\n" \
"ds_read_b128 a[16:19], v49 offset:12288\n" \
"v_mfma_f32_16x16x32_bf16 v[40:43], a[220:223], a[96:99], 0\n" \
"ds_read_b128 a[20:23], v49 offset:13312\n" \
"ds_read_b128 a[24:27], v49 offset:14336\n" \
"v_mfma_f32_16x16x32_bf16 v[44:47], a[220:223], a[112:115], 0\n" \
"buffer_load_dwordx4 v50, s[8:11], s64 offen offset:3072 sc1 lds\n" \
"v_mfma_f32_16x16x32_bf16 v[32:35], a[224:227], a[68:71], v[32:35]\n" \
"s_xor_b32 m0, s65, 0xa000\n" \
"ds_read_b128 a[28:31], v49 offset:15360\n" \
"v_mfma_f32_16x16x32_bf16 v[36:39], a[224:227], a[84:87], v[36:39]\n" \
"v_exp_f32_e32 v120, v120\n" \
"v_mfma_f32_16x16x32_bf16 v[40:43], a[224:227], a[100:103], v[40:43]\n" \
"buffer_load_dwordx4 v51, s[8:11], s64 offen offset:0 sc1 lds\n" \
"v_mfma_f32_16x16x32_bf16 v[44:47], a[224:227], a[116:119], v[44:47]\n" \
"v_exp_f32_e32 v121, v121\n" \
"v_mfma_f32_16x16x32_bf16 v[32:35], a[228:231], a[72:75], v[32:35]\n" \
"v_exp_f32_e32 v122, v122\n" \
"v_mfma_f32_16x16x32_bf16 v[36:39], a[228:231], a[88:91], v[36:39]\n" \
"v_exp_f32_e32 v123, v123\n" \
"v_mfma_f32_16x16x32_bf16 v[40:43], a[228:231], a[104:107], v[40:43]\n" \
"buffer_load_dwordx4 v51, s[8:11], s64 offen offset:1024 sc1 lds\n" \
"v_mfma_f32_16x16x32_bf16 v[44:47], a[228:231], a[120:123], v[44:47]\n" \
"v_cvt_pk_bf16_f32 v120, v120, v121\n" \
"v_cvt_pk_bf16_f32 v121, v122, v123\n" \
"v_mfma_f32_16x16x32_bf16 v[32:35], a[232:235], a[76:79], v[32:35]\n" \
"v_exp_f32_e32 v124, v124\n" \
"v_mfma_f32_16x16x32_bf16 v[36:39], a[232:235], a[92:95], v[36:39]\n" \
"v_exp_f32_e32 v125, v125\n" \
"v_mfma_f32_16x16x32_bf16 v[40:43], a[232:235], a[108:111], v[40:43]\n" \
"buffer_load_dwordx4 v51, s[8:11], s64 offen offset:2048 sc1 lds\n" \
"v_mfma_f32_16x16x32_bf16 v[44:47], a[232:235], a[124:127], v[44:47]\n" \
"v_exp_f32_e32 v126, v126\n" \
"v_smfmac_f32_16x16x64_bf16 a[196:199], a[200:203], v[84:91], v21\n" \
"v_cvt_pk_bf16_f32 v122, v124, v125\n" \
"s_waitcnt lgkmcnt(1)\n" \
"v_mfma_f32_16x16x32_bf16 v[220:223], a[188:191], v[84:87], v[220:223]\n" \
"buffer_load_dwordx4 v51, s[8:11], s64 offen offset:3072 sc1 lds\n" \
"v_mfma_f32_16x16x32_bf16 v[252:255], a[188:191], v[88:91], v[252:255]\n" \
"s_add_u32 m0, s93, 0x1000\n" \
"v_and_b32_e32 v120, v12, v120\n" \
"v_mfma_f32_16x16x32_bf16 v[216:219], a[184:187], v[84:87], v[216:219]\n" \
"v_exp_f32_e32 v127, v127\n" \
"v_mfma_f32_16x16x32_bf16 v[248:251], a[184:187], v[88:91], v[248:251]\n" \
"v_cvt_pk_bf16_f32 v123, v126, v127\n" \
"v_and_b32_e32 v121, v13, v121\n" \
"v_mfma_f32_16x16x32_bf16 v[212:215], a[180:183], v[84:87], v[212:215]\n" \
"buffer_load_dwordx4 v4, s[80:83], s92 offen offset:0 lds\n" \
"v_mfma_f32_16x16x32_bf16 v[244:247], a[180:183], v[88:91], v[244:247]\n" \
"v_and_b32_e32 v122, v14, v122\n" \
"v_and_b32_e32 v123, v15, v123\n" \
"v_mfma_f32_16x16x32_bf16 v[208:211], a[176:179], v[84:87], v[208:211]\n" \
"v_accvgpr_read_b32 v56, a196\n" \
"v_accvgpr_read_b32 v24, a197\n" \
"v_mfma_f32_16x16x32_bf16 v[240:243], a[176:179], v[88:91], v[240:243]\n" \
"ds_read_b128 a[128:131], v48 offset:0\n" \
"ds_read_b128 a[144:147], v48 offset:4096\n" \
"v_mfma_f32_16x16x32_bf16 v[204:207], a[172:175], v[84:87], v[204:207]\n" \
"buffer_load_dwordx4 v5, s[80:83], s92 offen offset:1024 lds\n" \
"v_mfma_f32_16x16x32_bf16 v[236:239], a[172:175], v[88:91], v[236:239]\n" \
"ds_read_b128 a[132:135], v48 offset:1024\n" \
"ds_read_b128 a[148:151], v48 offset:5120\n" \
"v_mfma_f32_16x16x32_bf16 v[200:203], a[168:171], v[84:87], v[200:203]\n" \
"ds_read_b128 a[136:139], v48 offset:2048\n" \
"ds_read_b128 a[152:155], v48 offset:6144\n" \
"v_mfma_f32_16x16x32_bf16 v[232:235], a[168:171], v[88:91], v[232:235]\n" \
"buffer_load_dwordx4 v6, s[80:83], s92 offen offset:2048 lds\n" \
"v_mfma_f32_16x16x32_bf16 v[196:199], a[164:167], v[84:87], v[196:199]\n" \
"ds_read_b128 a[140:143], v48 offset:3072\n" \
"ds_read_b128 a[156:159], v48 offset:7168\n" \
"v_mfma_f32_16x16x32_bf16 v[228:231], a[164:167], v[88:91], v[228:231]\n" \
"v_mul_f32_e32 v141, v54, v141\n" \
"v_mul_f32_e32 v142, v54, v142\n" \
"v_mfma_f32_16x16x32_bf16 v[192:195], a[160:163], v[84:87], v[192:195]\n" \
"v_mul_f32_e32 v143, v54, v143\n" \
"v_cvt_pk_bf16_f32 v145, v146, v147\n" \
"v_mfma_f32_16x16x32_bf16 v[224:227], a[160:163], v[88:91], v[224:227]\n" \
"buffer_load_dwordx4 v7, s[80:83], s92 offen offset:3072 lds\n" \
"v_mfma_f32_16x16x32_bf16 a[196:199], a[212:215], v[120:123], a[196:199]\n" \
"v_mul_f32_e32 v148, v54, v148\n" \
"v_mul_f32_e32 v149, v54, v149\n" \
"v_mfma_f32_16x16x32_bf16 v[224:227], a[0:3], v[120:123], v[224:227]\n" \
"v_mul_f32_e32 v150, v54, v150\n" \
"v_mul_f32_e32 v151, v54, v151\n" \
"v_mfma_f32_16x16x32_bf16 v[228:231], a[4:7], v[120:123], v[228:231]\n" \
"v_cvt_pk_bf16_f32 v153, v154, v155\n" \
"v_mul_f32_e32 v156, v54, v156\n" \
"v_mfma_f32_16x16x32_bf16 v[232:235], a[8:11], v[120:123], v[232:235]\n" \
"v_mul_f32_e32 v157, v54, v157\n" \
"v_mul_f32_e32 v158, v54, v158\n" \
"v_mfma_f32_16x16x32_bf16 v[236:239], a[12:15], v[120:123], v[236:239]\n" \
"v_mul_f32_e32 v159, v54, v159\n" \
"v_cvt_pk_bf16_f32 v161, v162, v163\n" \
"v_mfma_f32_16x16x32_bf16 v[240:243], a[16:19], v[120:123], v[240:243]\n" \
"v_mul_f32_e32 v164, v55, v164\n" \
"v_mul_f32_e32 v165, v55, v165\n" \
"v_mfma_f32_16x16x32_bf16 v[244:247], a[20:23], v[120:123], v[244:247]\n" \
"v_mul_f32_e32 v166, v55, v166\n" \
"v_mul_f32_e32 v167, v55, v167\n" \
"v_mfma_f32_16x16x32_bf16 v[248:251], a[24:27], v[120:123], v[248:251]\n" \
"v_cvt_pk_bf16_f32 v169, v170, v171\n" \
"s_waitcnt lgkmcnt(4)\n" \
"v_mfma_f32_16x16x32_bf16 v[252:255], a[28:31], v[120:123], v[252:255]\n" \
"v_mul_f32_e32 v172, v55, v172\n" \
"v_mul_f32_e32 v173, v55, v173\n" \
"v_mul_f32_e32 v174, v55, v174\n" \
"v_mul_f32_e32 v175, v55, v175\n" \
"v_cvt_pk_bf16_f32 v177, v178, v179\n" \
"v_mul_f32_e32 v180, v55, v180\n" \
"v_mul_f32_e32 v181, v55, v181\n" \
"v_mul_f32_e32 v182, v55, v182\n" \
"v_mul_f32_e32 v183, v55, v183\n" \
"v_cvt_pk_bf16_f32 v185, v186, v187\n" \
"v_mul_f32_e32 v188, v55, v188\n" \
"v_mul_f32_e32 v189, v55, v189\n" \
"v_mul_f32_e32 v190, v55, v190\n" \
"v_mul_f32_e32 v191, v55, v191\n" \
"v_cvt_pk_bf16_f32 v130, v132, v133\n" \
"v_cvt_pk_bf16_f32 v131, v134, v135\n" \
"buffer_store_dwordx4 v[128:131], v20, s[56:59], s68 offen offset:0 sc1\n" \
"v_cvt_pk_bf16_f32 v138, v140, v141\n" \
"v_cvt_pk_bf16_f32 v139, v142, v143\n" \
"buffer_store_dwordx4 v[136:139], v20, s[56:59], s68 offen offset:1024 sc1\n" \
"v_cvt_pk_bf16_f32 v146, v148, v149\n" \
"v_cvt_pk_bf16_f32 v147, v150, v151\n" \
"buffer_store_dwordx4 v[144:147], v20, s[56:59], s68 offen offset:2048 sc1\n" \
"v_cvt_pk_bf16_f32 v154, v156, v157\n" \
"v_cvt_pk_bf16_f32 v155, v158, v159\n" \
"buffer_store_dwordx4 v[152:155], v20, s[56:59], s68 offen offset:3072 sc1\n" \
"v_cvt_pk_bf16_f32 v162, v164, v165\n" \
"v_cvt_pk_bf16_f32 v163, v166, v167\n" \
"buffer_store_dwordx4 v[160:163], v20, s[56:59], s69 offen offset:0 sc1\n" \
"v_cvt_pk_bf16_f32 v170, v172, v173\n" \
"v_cvt_pk_bf16_f32 v171, v174, v175\n" \
"buffer_store_dwordx4 v[168:171], v20, s[56:59], s69 offen offset:1024 sc1\n" \
"v_cvt_pk_bf16_f32 v178, v180, v181\n" \
"v_cvt_pk_bf16_f32 v179, v182, v183\n" \
"buffer_store_dwordx4 v[176:179], v20, s[56:59], s69 offen offset:2048 sc1\n" \
"v_cvt_pk_bf16_f32 v186, v188, v189\n" \
"v_cvt_pk_bf16_f32 v187, v190, v191\n" \
"buffer_store_dwordx4 v[184:187], v20, s[56:59], s69 offen offset:3072 sc1\n" \
"s_add_u32 m0, s93, 0x2000\n" \
"v_mfma_f32_16x16x32_bf16 a[208:211], a[216:219], v[128:131], a[208:211]\n" \
"buffer_load_dwordx4 v8, s[80:83], s92 offen offset:0 lds\n" \
"v_mfma_f32_16x16x32_bf16 a[208:211], a[216:219], v[136:139], a[208:211]\n" \
"v_mfma_f32_32x32x16_bf16 v[128:143], a[216:219], a[216:219], 0\n" \
"v_mfma_f32_16x16x32_bf16 a[208:211], a[216:219], v[144:147], a[208:211]\n" \
"v_mfma_f32_16x16x32_bf16 a[208:211], a[216:219], v[152:155], a[208:211]\n" \
"v_mfma_f32_32x32x16_bf16 v[144:159], a[216:219], a[216:219], 0\n" \
"v_add_f32_e32 v56, v56, v24\n" \
"v_mfma_f32_16x16x32_bf16 a[208:211], a[216:219], v[160:163], a[208:211]\n" \
"v_max_f32_e32 v62, v62, v56\n" \
"v_accvgpr_read_b32 v57, a198\n" \
"s_waitcnt vmcnt(21) lgkmcnt(0)\n" \
"s_barrier\n" \
"v_mfma_f32_16x16x32_bf16 v[64:67], a[128:131], a[64:67], v[32:35]\n" \
"s_xor_b32 m0, s65, 0x18000\n" \
"v_accvgpr_read_b32 v25, a199\n" \
"v_mfma_f32_16x16x32_bf16 a[196:199], a[216:219], a[216:219], 0\n" \
"buffer_load_dwordx4 v52, s[8:11], s64 offen offset:0 sc1 lds\n" \
"v_mfma_f32_16x16x32_bf16 v[64:67], a[132:135], a[68:71], v[64:67]\n" \
"v_rcp_f32_e32 v56, v56\n" \
"v_mfma_f32_16x16x32_bf16 v[64:67], a[136:139], a[72:75], v[64:67]\n" \
"v_mfma_f32_16x16x32_bf16 a[208:211], a[216:219], v[168:171], a[208:211]\n" \
"v_mfma_f32_32x32x16_bf16 v[160:175], a[216:219], a[216:219], 0\n" \
"v_mul_f32_e32 v56, v56, v60\n" \
"v_mfma_f32_16x16x32_bf16 a[208:211], a[216:219], v[176:179], a[208:211]\n" \
"v_mul_f32_e32 v192, v56, v192\n" \
"v_mul_f32_e32 v193, v56, v193\n" \
"v_mfma_f32_16x16x32_bf16 v[64:67], a[140:143], a[76:79], v[64:67]\n" \
"buffer_load_dwordx4 v52, s[8:11], s64 offen offset:1024 sc1 lds\n" \
"v_mfma_f32_16x16x32_bf16 v[68:71], a[156:159], a[76:79], v[32:35]\n" \
"v_cvt_pk_bf16_f32 v192, v192, v193\n" \
"v_mul_f32_e32 v194, v56, v194\n" \
"v_mfma_f32_16x16x32_bf16 v[68:71], a[152:155], a[72:75], v[68:71]\n" \
"v_mul_f32_e32 v195, v56, v195\n" \
"v_cvt_pk_bf16_f32 v193, v194, v195\n" \
"v_mfma_f32_16x16x32_bf16 v[68:71], a[148:151], a[68:71], v[68:71]\n" \
"v_mfma_f32_16x16x32_bf16 a[208:211], a[216:219], v[184:187], a[208:211]\n" \
"v_mfma_f32_32x32x16_bf16 v[176:191], a[216:219], a[216:219], 0\n" \
"v_mul_f32_e32 v196, v56, v196\n" \
"v_mul_f32_e32 v197, v56, v197\n" \
"v_mul_f32_e32 v198, v56, v198\n" \
"v_mul_f32_e32 v199, v56, v199\n" \
"v_mul_f32_e32 v200, v56, v200\n" \
"v_mul_f32_e32 v201, v56, v201\n" \
"v_mfma_f32_16x16x32_bf16 v[68:71], a[144:147], a[64:67], v[68:71]\n" \
"buffer_load_dwordx4 v52, s[8:11], s64 offen offset:2048 sc1 lds\n" \
"v_mfma_f32_16x16x32_bf16 v[76:79], a[144:147], a[80:83], v[36:39]\n" \
"v_cvt_pk_bf16_f32 v194, v196, v197\n" \
"v_cvt_pk_bf16_f32 v195, v198, v199\n" \
"buffer_store_dwordx4 v[192:195], v20, s[56:59], s70 offen offset:0 sc1\n" \
"v_mfma_f32_16x16x32_bf16 v[76:79], a[148:151], a[84:87], v[76:79]\n" \
"v_cvt_pk_bf16_f32 v200, v200, v201\n" \
"v_mul_f32_e32 v202, v56, v202\n" \
"v_mfma_f32_16x16x32_bf16 v[76:79], a[152:155], a[88:91], v[76:79]\n" \
"v_mul_f32_e32 v203, v56, v203\n" \
"v_add_f32_e32 v57, v57, v25\n" \
"v_mfma_f32_16x16x32_bf16 v[76:79], a[156:159], a[92:95], v[76:79]\n" \
"buffer_load_dwordx4 v52, s[8:11], s64 offen offset:3072 sc1 lds\n" \
"v_mfma_f32_16x16x32_bf16 v[72:75], a[140:143], a[92:95], v[36:39]\n" \
"s_mov_b32 m0, s65\n" \
"v_cvt_pk_bf16_f32 v201, v202, v203\n" \
"v_mfma_f32_16x16x32_bf16 v[72:75], a[136:139], a[88:91], v[72:75]\n" \
"v_mfma_f32_16x16x32_bf16 a[208:211], a[216:219], v[192:195], a[208:211]\n" \
"v_mul_f32_e32 v204, v56, v204\n" \
"v_mul_f32_e32 v205, v56, v205\n" \
"v_mfma_f32_16x16x32_bf16 v[72:75], a[132:135], a[84:87], v[72:75]\n" \
"v_mul_f32_e32 v206, v56, v206\n" \
"v_mul_f32_e32 v207, v56, v207\n" \
"v_mfma_f32_16x16x32_bf16 v[72:75], a[128:131], a[80:83], v[72:75]\n" \
"buffer_load_dwordx4 v53, s[8:11], s64 offen offset:0 sc1 lds\n" \
"v_mfma_f32_16x16x32_bf16 v[80:83], a[128:131], a[96:99], v[40:43]\n" \
"v_mul_f32_e32 v208, v56, v208\n" \
"v_mul_f32_e32 v209, v56, v209\n" \
"v_mfma_f32_16x16x32_bf16 v[80:83], a[132:135], a[100:103], v[80:83]\n" \
"v_max_f32_e32 v62, v62, v57\n" \
"v_cvt_pk_bf16_f32 v202, v204, v205\n" \
"v_mfma_f32_16x16x32_bf16 v[80:83], a[136:139], a[104:107], v[80:83]\n" \
"buffer_load_dwordx4 v53, s[8:11], s64 offen offset:1024 sc1 lds\n" \
"v_mfma_f32_16x16x32_bf16 v[80:83], a[140:143], a[108:111], v[80:83]\n" \
"v_cvt_pk_bf16_f32 v203, v206, v207\n" \
"v_cvt_pk_bf16_f32 v208, v208, v209\n" \
"buffer_store_dwordx4 v[200:203], v20, s[56:59], s70 offen offset:1024 sc1\n" \
"v_mfma_f32_16x16x32_bf16 v[84:87], a[156:159], a[108:111], v[40:43]\n" \
"v_mul_f32_e32 v210, v56, v210\n" \
"v_mul_f32_e32 v211, v56, v211\n" \
"v_mfma_f32_16x16x32_bf16 v[84:87], a[152:155], a[104:107], v[84:87]\n" \
"v_rcp_f32_e32 v57, v57\n" \
"v_mfma_f32_16x16x32_bf16 v[84:87], a[148:151], a[100:103], v[84:87]\n" \
"buffer_load_dwordx4 v53, s[8:11], s64 offen offset:2048 sc1 lds\n" \
"v_mfma_f32_16x16x32_bf16 v[84:87], a[144:147], a[96:99], v[84:87]\n" \
"v_mfma_f32_16x16x32_bf16 a[208:211], a[216:219], v[200:203], a[208:211]\n" \
"v_mfma_f32_32x32x16_bf16 v[192:207], a[216:219], a[216:219], 0\n" \
"v_cvt_pk_bf16_f32 v209, v210, v211\n" \
"v_mul_f32_e32 v212, v56, v212\n" \
".fill (L_ab + 20480 - .) / 4, 4, 0xBF800000\n" \
"L_ap5:\n" \
"s_cbranch_execz L_ap0\n" \
"v_mul_f32_e32 v213, v56, v213\n" \
"v_mul_f32_e32 v214, v56, v214\n" \
"v_mul_f32_e32 v215, v56, v215\n" \
"v_mul_f32_e32 v216, v56, v216\n" \
"v_mfma_f32_16x16x32_bf16 v[92:95], a[144:147], a[112:115], v[44:47]\n" \
"ds_read_b128 a[144:147], v48 offset:12288\n" \
"v_mul_f32_e32 v217, v56, v217\n" \
"v_mfma_f32_16x16x32_bf16 v[92:95], a[148:151], a[116:119], v[92:95]\n" \
"ds_read_b128 a[148:151], v48 offset:13312\n" \
"v_cvt_pk_bf16_f32 v210, v212, v213\n" \
"v_mfma_f32_16x16x32_bf16 v[92:95], a[152:155], a[120:123], v[92:95]\n" \
"ds_read_b128 a[152:155], v48 offset:14336\n" \
"v_cvt_pk_bf16_f32 v211, v214, v215\n" \
"buffer_store_dwordx4 v[208:211], v20, s[56:59], s70 offen offset:2048 sc1\n" \
"v_mfma_f32_16x16x32_bf16 v[92:95], a[156:159], a[124:127], v[92:95]\n" \
"ds_read_b128 a[156:159], v48 offset:15360\n" \
"v_cvt_pk_bf16_f32 v216, v216, v217\n" \
"v_mfma_f32_16x16x32_bf16 v[88:91], a[140:143], a[124:127], v[44:47]\n" \
"ds_read_b128 a[140:143], v48 offset:11264\n" \
"v_mul_f32_e32 v218, v56, v218\n" \
"v_mfma_f32_16x16x32_bf16 v[88:91], a[136:139], a[120:123], v[88:91]\n" \
"ds_read_b128 a[136:139], v48 offset:10240\n" \
"v_mul_f32_e32 v219, v56, v219\n" \
"v_mfma_f32_16x16x32_bf16 v[88:91], a[132:135], a[116:119], v[88:91]\n" \
"ds_read_b128 a[132:135], v48 offset:9216\n" \
"v_mul_f32_e32 v57, v57, v61\n" \
"v_mfma_f32_16x16x32_bf16 v[88:91], a[128:131], a[112:115], v[88:91]\n" \
"ds_read_b128 a[128:131], v48 offset:8192\n" \
"v_mfma_f32_16x16x32_bf16 a[208:211], a[216:219], v[208:211], a[208:211]\n" \
"v_cvt_pk_bf16_f32 v217, v218, v219\n" \
"v_mul_f32_e32 v220, v56, v220\n" \
"v_mul_f32_e32 v221, v56, v221\n" \
"v_mul_f32_e32 v222, v56, v222\n" \
"v_mul_f32_e32 v223, v56, v223\n" \
"v_mul_f32_e32 v224, v57, v224\n" \
"v_mul_f32_e32 v225, v57, v225\n" \
"v_cvt_pk_bf16_f32 v218, v220, v221\n" \
"v_cvt_pk_bf16_f32 v219, v222, v223\n" \
"buffer_store_dwordx4 v[216:219], v20, s[56:59], s70 offen offset:3072 sc1\n" \
"v_cvt_pk_bf16_f32 v224, v224, v225\n" \
"v_mul_f32_e32 v226, v57, v226\n" \
"v_mul_f32_e32 v227, v57, v227\n" \
"v_mfma_f32_16x16x32_bf16 a[208:211], a[216:219], v[216:219], a[208:211]\n" \
"v_mfma_f32_32x32x16_bf16 v[208:223], a[216:219], a[216:219], 0\n" \
"v_cvt_pk_bf16_f32 v225, v226, v227\n" \
"v_mul_f32_e32 v228, v57, v228\n" \
"v_mul_f32_e32 v229, v57, v229\n" \
"v_mul_f32_e32 v230, v57, v230\n" \
"v_mul_f32_e32 v231, v57, v231\n" \
"v_mul_f32_e32 v232, v57, v232\n" \
"v_mul_f32_e32 v233, v57, v233\n" \
"v_cvt_pk_bf16_f32 v226, v228, v229\n" \
"v_cvt_pk_bf16_f32 v227, v230, v231\n" \
"buffer_store_dwordx4 v[224:227], v20, s[56:59], s71 offen offset:0 sc1\n" \
"v_cvt_pk_bf16_f32 v232, v232, v233\n" \
"v_mul_f32_e32 v234, v57, v234\n" \
"v_mul_f32_e32 v235, v57, v235\n" \
"v_mfma_f32_16x16x32_bf16 a[208:211], a[216:219], v[224:227], a[208:211]\n" \
"v_cvt_pk_bf16_f32 v233, v234, v235\n" \
"v_mul_f32_e32 v236, v57, v236\n" \
"v_mul_f32_e32 v237, v57, v237\n" \
"v_mul_f32_e32 v238, v57, v238\n" \
"v_mul_f32_e32 v239, v57, v239\n" \
"v_mul_f32_e32 v240, v57, v240\n" \
"v_mul_f32_e32 v241, v57, v241\n" \
"v_cvt_pk_bf16_f32 v234, v236, v237\n" \
"v_cvt_pk_bf16_f32 v235, v238, v239\n" \
"buffer_store_dwordx4 v[232:235], v20, s[56:59], s71 offen offset:1024 sc1\n" \
"v_cvt_pk_bf16_f32 v240, v240, v241\n" \
"v_mul_f32_e32 v242, v57, v242\n" \
"v_mul_f32_e32 v243, v57, v243\n" \
"v_mfma_f32_16x16x32_bf16 a[208:211], a[216:219], v[232:235], a[208:211]\n" \
"v_mfma_f32_32x32x16_bf16 v[224:239], a[216:219], a[216:219], 0\n" \
"v_cvt_pk_bf16_f32 v241, v242, v243\n" \
"v_mul_f32_e32 v244, v57, v244\n" \
"v_mul_f32_e32 v245, v57, v245\n" \
"v_mul_f32_e32 v246, v57, v246\n" \
"v_mul_f32_e32 v247, v57, v247\n" \
"v_mul_f32_e32 v248, v57, v248\n" \
"v_mul_f32_e32 v249, v57, v249\n" \
"v_cvt_pk_bf16_f32 v242, v244, v245\n" \
"v_cvt_pk_bf16_f32 v243, v246, v247\n" \
"buffer_store_dwordx4 v[240:243], v20, s[56:59], s71 offen offset:2048 sc1\n" \
"v_cvt_pk_bf16_f32 v248, v248, v249\n" \
"v_mul_f32_e32 v250, v57, v250\n" \
"v_mul_f32_e32 v251, v57, v251\n" \
"v_mfma_f32_16x16x32_bf16 a[208:211], a[216:219], v[240:243], a[208:211]\n" \
"v_cvt_pk_bf16_f32 v249, v250, v251\n" \
"v_mul_f32_e32 v252, v57, v252\n" \
"v_mul_f32_e32 v253, v57, v253\n" \
"v_mul_f32_e32 v254, v57, v254\n" \
"v_mul_f32_e32 v255, v57, v255\n" \
"v_cvt_pk_bf16_f32 v250, v252, v253\n" \
"v_cvt_pk_bf16_f32 v251, v254, v255\n" \
"buffer_store_dwordx4 v[248:251], v20, s[56:59], s71 offen offset:3072 sc1\n" \
"buffer_load_dwordx4 v53, s[8:11], s64 offen offset:3072 sc1 lds\n" \
"s_waitcnt vmcnt(16) lgkmcnt(0)\n" \
"s_barrier\n" \
"v_mfma_f32_16x16x32_bf16 a[208:211], a[216:219], v[248:251], a[208:211]\n" \
"v_mfma_f32_32x32x16_bf16 v[240:255], a[216:219], a[216:219], 0\n" \
"v_exp_f32_e32 v70, v70\n" \
"v_exp_f32_e32 v71, v71\n" \
"v_exp_f32_e32 v72, v72\n" \
"v_exp_f32_e32 v73, v73\n" \
"s_add_u32 s64, s64, 0x10000\n" \
"v_cmp_lt_f32_e64 s[72:73], s21, v62\n" \
"v_readlane_b32 s95, v11, 3\n" \
"v_accvgpr_read_b32 v63, a208\n" \
"v_cmp_u_f32_e32 vcc, v63, v63\n" \
"v_exp_f32_e32 v68, v68\n" \
"v_exp_f32_e32 v69, v69\n" \
"v_exp_f32_e32 v74, v74\n" \
"v_exp_f32_e32 v75, v75\n" \
"s_or_b64 vcc, s[72:73], vcc\n" \
"s_cselect_b32 s95, 0x40000000, s95\n" \
"s_cselect_b32 s94, 0x7f800000, 0\n" \
"v_cvt_pk_bf16_f32 v71, v70, v71\n" \
"v_exp_f32_e32 v66, v66\n" \
"v_exp_f32_e32 v67, v67\n" \
"v_exp_f32_e32 v64, v64\n" \
"v_exp_f32_e32 v65, v65\n" \
"v_cvt_pk_bf16_f32 v72, v72, v73\n" \
"v_exp_f32_e32 v76, v76\n" \
"v_exp_f32_e32 v77, v77\n" \
"v_exp_f32_e32 v78, v78\n" \
"v_exp_f32_e32 v79, v79\n" \
"v_cvt_pk_bf16_f32 v70, v68, v69\n" \
"v_cvt_pk_bf16_f32 v73, v74, v75\n" \
"v_add_u32_e32 v49, 0x10000, v48\n" \
"s_xor_b32 s65, s65, 0x8000\n" \
"v_cvt_pk_bf16_f32 v69, v66, v67\n" \
"v_cvt_pk_bf16_f32 v68, v64, v65\n" \
"v_cvt_pk_bf16_f32 v74, v76, v77\n" \
"v_cvt_pk_bf16_f32 v75, v78, v79\n" \
"v_xor_b32_e32 v48, 0x8000, v48\n" \
"s_sub_u32 s66, s19, 2\n" \
"s_branch L_2_%=\n" \
"L_f1_%=:\n" \
"ds_read_b128 a[160:163], v49 offset:0\n" \
"ds_read_b128 a[164:167], v49 offset:1024\n" \
"ds_read_b128 a[168:171], v49 offset:2048\n" \
"v_mfma_f32_16x16x32_bf16 v[104:107], a[128:131], a[80:83], v[36:39]\n" \
"v_cvt_pk_bf16_f32 v192, v192, v193\n" \
"v_cvt_pk_bf16_f32 v224, v224, v225\n" \
"v_cvt_pk_bf16_f32 v193, v194, v195\n" \
"v_cvt_pk_bf16_f32 v225, v226, v227\n" \
"ds_read_b128 a[172:175], v49 offset:3072\n" \
"v_cvt_pk_bf16_f32 v194, v196, v197\n" \
"v_cvt_pk_bf16_f32 v195, v198, v199\n" \
"v_cvt_pk_bf16_f32 v226, v228, v229\n" \
"v_cvt_pk_bf16_f32 v227, v230, v231\n" \
"v_mfma_f32_16x16x32_bf16 v[104:107], a[132:135], a[84:87], v[104:107]\n" \
"v_lshl_add_u32 v50, s15, 2, v20\n" \
"v_cvt_pk_bf16_f32 v196, v200, v201\n" \
"v_cvt_pk_bf16_f32 v197, v202, v203\n" \
"v_cvt_pk_bf16_f32 v198, v204, v205\n" \
"v_cvt_pk_bf16_f32 v199, v206, v207\n" \
"v_cvt_pk_bf16_f32 v228, v232, v233\n" \
"v_cvt_pk_bf16_f32 v229, v234, v235\n" \
"v_cvt_pk_bf16_f32 v230, v236, v237\n" \
"v_cvt_pk_bf16_f32 v231, v238, v239\n" \
"v_add_u32_e32 v50, 0x1a000, v50\n" \
"v_mov_b32_e32 v61, s62\n" \
"v_cvt_pk_bf16_f32 v200, v208, v209\n" \
"v_cvt_pk_bf16_f32 v201, v210, v211\n" \
"v_cvt_pk_bf16_f32 v202, v212, v213\n" \
"v_cvt_pk_bf16_f32 v203, v214, v215\n" \
"v_cvt_pk_bf16_f32 v204, v216, v217\n" \
"v_cvt_pk_bf16_f32 v205, v218, v219\n" \
"v_cvt_pk_bf16_f32 v206, v220, v221\n" \
"v_cvt_pk_bf16_f32 v207, v222, v223\n" \
"v_cvt_pk_bf16_f32 v232, v240, v241\n" \
"v_cvt_pk_bf16_f32 v233, v242, v243\n" \
"v_cvt_pk_bf16_f32 v234, v244, v245\n" \
"v_cvt_pk_bf16_f32 v235, v246, v247\n" \
"v_cvt_pk_bf16_f32 v236, v248, v249\n" \
"v_cvt_pk_bf16_f32 v237, v250, v251\n" \
"v_cvt_pk_bf16_f32 v238, v252, v253\n" \
"v_cvt_pk_bf16_f32 v239, v254, v255\n" \
"ds_write_b128 v50, v[192:195] offset:0\n" \
"v_mfma_f32_16x16x32_bf16 v[104:107], a[136:139], a[88:91], v[104:107]\n" \
"ds_read_b128 a[176:179], v49 offset:4096\n" \
"v_add_u32_e32 v56, 0x49000, v20\n" \
"v_mfma_f32_16x16x32_bf16 v[104:107], a[140:143], a[92:95], v[104:107]\n" \
"ds_write_b128 v50, v[196:199] offset:1024\n" \
"v_mfma_f32_16x16x32_bf16 v[108:111], a[156:159], a[92:95], v[36:39]\n" \
"ds_read_b128 a[180:183], v49 offset:5120\n" \
"ds_read_b128 a[184:187], v49 offset:6144\n" \
"v_mfma_f32_16x16x32_bf16 v[108:111], a[152:155], a[88:91], v[108:111]\n" \
"ds_write_b128 v50, v[200:203] offset:2048\n" \
"v_mfma_f32_16x16x32_bf16 v[108:111], a[148:151], a[84:87], v[108:111]\n" \
"v_and_b32_e32 v68, v12, v68\n" \
"v_and_b32_e32 v69, v13, v69\n" \
"v_mfma_f32_16x16x32_bf16 v[108:111], a[144:147], a[80:83], v[108:111]\n" \
"ds_write_b128 v50, v[204:207] offset:3072\n" \
"v_and_b32_e32 v70, v14, v70\n" \
"v_and_b32_e32 v71, v15, v71\n" \
"ds_read_b128 a[188:191], v49 offset:7168\n" \
"ds_read_b128 a[0:3], v20 offset:8192\n" \
"s_waitcnt lgkmcnt(9)\n" \
"v_mfma_f32_16x16x32_bf16 v[128:131], a[160:163], v[68:71], v[128:131]\n" \
"ds_write_b128 v50, v[224:227] offset:4096\n" \
"v_mfma_f32_16x16x32_bf16 v[160:163], a[160:163], v[72:75], v[160:163]\n" \
"ds_read_b128 a[4:7], v20 offset:9216\n" \
"ds_read_b128 a[8:11], v20 offset:10240\n" \
"v_mfma_f32_16x16x32_bf16 v[132:135], a[164:167], v[68:71], v[132:135]\n" \
"ds_write_b128 v50, v[228:231] offset:5120\n" \
"v_mfma_f32_16x16x32_bf16 v[164:167], a[164:167], v[72:75], v[164:167]\n" \
"ds_read_b128 a[12:15], v20 offset:11264\n" \
"ds_read_b128 a[28:31], v20 offset:15360\n" \
"v_mfma_f32_16x16x32_bf16 v[136:139], a[168:171], v[68:71], v[136:139]\n" \
"ds_write_b128 v50, v[232:235] offset:6144\n" \
"v_mfma_f32_16x16x32_bf16 v[168:171], a[168:171], v[72:75], v[168:171]\n" \
"ds_read_b128 a[24:27], v20 offset:14336\n" \
"ds_read_b128 a[20:23], v20 offset:13312\n" \
"v_mfma_f32_16x16x32_bf16 v[140:143], a[172:175], v[68:71], v[140:143]\n" \
"ds_write_b128 v50, v[236:239] offset:7168\n" \
"v_mfma_f32_16x16x32_bf16 v[172:175], a[172:175], v[72:75], v[172:175]\n" \
"ds_read_b128 a[16:19], v20 offset:12288\n" \
"s_waitcnt lgkmcnt(15)\n" \
"v_mfma_f32_16x16x32_bf16 v[144:147], a[176:179], v[68:71], v[144:147]\n" \
"v_xor_b32_e32 v48, 0x4000, v49\n" \
"ds_read_b32 v58, v18 offset:256\n" \
"v_mfma_f32_16x16x32_bf16 v[176:179], a[176:179], v[72:75], v[176:179]\n" \
"ds_read_b32 v59, v18 offset:384\n" \
"ds_read_b128 a[32:35], v48 offset:8192\n" \
"v_mfma_f32_16x16x32_bf16 v[148:151], a[180:183], v[68:71], v[148:151]\n" \
"ds_read_b128 a[36:39], v48 offset:9216\n" \
"ds_read_b128 a[40:43], v48 offset:10240\n" \
"v_mfma_f32_16x16x32_bf16 v[180:183], a[180:183], v[72:75], v[180:183]\n" \
"ds_read_b128 a[44:47], v48 offset:11264\n" \
"ds_read_b128 a[48:51], v48 offset:12288\n" \
"v_mfma_f32_16x16x32_bf16 v[152:155], a[184:187], v[68:71], v[152:155]\n" \
"ds_read_b128 a[52:55], v48 offset:13312\n" \
"ds_read_b128 a[56:59], v48 offset:14336\n" \
"v_mfma_f32_16x16x32_bf16 v[184:187], a[184:187], v[72:75], v[184:187]\n" \
"ds_read_b128 a[60:63], v48 offset:15360\n" \
"s_waitcnt lgkmcnt(15)\n" \
"v_mfma_f32_16x16x32_bf16 v[156:159], a[188:191], v[68:71], v[156:159]\n" \
"ds_read_b128 a[128:131], v49 offset:8192\n" \
"ds_read_b128 a[132:135], v49 offset:9216\n" \
"v_mfma_f32_16x16x32_bf16 v[188:191], a[188:191], v[72:75], v[188:191]\n" \
"ds_read_b128 a[136:139], v49 offset:10240\n" \
"ds_read_b128 a[140:143], v49 offset:11264\n" \
"v_smfmac_f32_16x16x64_bf16 a[192:195], a[200:203], v[68:75], v21\n" \
"ds_read_b128 a[144:147], v49 offset:12288\n" \
"ds_read_b128 a[148:151], v49 offset:13312\n" \
"v_mfma_f32_16x16x32_bf16 v[112:115], a[0:3], a[64:67], v[32:35]\n" \
"ds_read_b128 a[152:155], v49 offset:14336\n" \
"ds_read_b128 a[156:159], v49 offset:15360\n" \
"v_mfma_f32_16x16x32_bf16 v[112:115], a[4:7], a[68:71], v[112:115]\n" \
"v_accvgpr_read_b32 v40, a196\n" \
"v_accvgpr_read_b32 v24, a197\n" \
"v_mfma_f32_16x16x32_bf16 v[112:115], a[8:11], a[72:75], v[112:115]\n" \
"v_accvgpr_read_b32 v41, a198\n" \
"v_accvgpr_read_b32 v25, a199\n" \
"v_mfma_f32_16x16x32_bf16 v[112:115], a[12:15], a[76:79], v[112:115]\n" \
"v_add_f32_e32 v40, v40, v24\n" \
"v_add_f32_e32 v41, v41, v25\n" \
"v_mfma_f32_16x16x32_bf16 v[116:119], a[28:31], a[76:79], v[32:35]\n" \
"v_add_f32_e32 v40, s94, v40\n" \
"s_waitcnt lgkmcnt(15)\n" \
"v_mfma_f32_16x16x32_bf16 v[116:119], a[24:27], a[72:75], v[116:119]\n" \
"v_add_f32_e32 v41, s94, v41\n" \
"ds_write_b64 v19, v[40:41]\n" \
"v_mfma_f32_16x16x32_bf16 v[116:119], a[20:23], a[68:71], v[116:119]\n" \
"v_add_u32_e32 v52, s15, v20\n" \
"v_add_u32_e32 v51, 0x10000, v52\n" \
"v_mfma_f32_16x16x32_bf16 v[116:119], a[16:19], a[64:67], v[116:119]\n" \
"v_exp_f32_e32 v104, v104\n" \
"v_mfma_f32_16x16x32_bf16 v[124:127], a[16:19], a[80:83], v[36:39]\n" \
"v_exp_f32_e32 v105, v105\n" \
"v_mfma_f32_16x16x32_bf16 v[124:127], a[20:23], a[84:87], v[124:127]\n" \
"v_exp_f32_e32 v106, v106\n" \
"v_mfma_f32_16x16x32_bf16 v[124:127], a[24:27], a[88:91], v[124:127]\n" \
"v_exp_f32_e32 v107, v107\n" \
"v_mfma_f32_16x16x32_bf16 v[124:127], a[28:31], a[92:95], v[124:127]\n" \
"v_cvt_pk_bf16_f32 v104, v104, v105\n" \
"v_cvt_pk_bf16_f32 v105, v106, v107\n" \
"v_mfma_f32_16x16x32_bf16 v[120:123], a[12:15], a[92:95], v[36:39]\n" \
"v_exp_f32_e32 v108, v108\n" \
"v_mfma_f32_16x16x32_bf16 v[120:123], a[8:11], a[88:91], v[120:123]\n" \
"v_exp_f32_e32 v109, v109\n" \
"v_mfma_f32_16x16x32_bf16 v[120:123], a[4:7], a[84:87], v[120:123]\n" \
"v_exp_f32_e32 v110, v110\n" \
"v_mfma_f32_16x16x32_bf16 v[120:123], a[0:3], a[80:83], v[120:123]\n" \
"v_exp_f32_e32 v111, v111\n" \
"s_waitcnt lgkmcnt(0)\n" \
"s_barrier\n" \
"ds_read_b128 v[208:211], v52 offset:0\n" \
"ds_read_b128 v[212:215], v52 offset:1024\n" \
"ds_read_b128 v[216:219], v52 offset:2048\n" \
"ds_read_b128 v[220:223], v52 offset:3072\n" \
"ds_read_b128 v[240:243], v51 offset:32768\n" \
"ds_read_b128 v[244:247], v51 offset:33792\n" \
"ds_read_b128 v[248:251], v51 offset:34816\n" \
"ds_read_b128 v[252:255], v51 offset:35840\n" \
"v_cvt_pk_bf16_f32 v106, v108, v109\n" \
"v_cvt_pk_bf16_f32 v107, v110, v111\n" \
"v_and_b32_e32 v104, v12, v104\n" \
"v_and_b32_e32 v105, v13, v105\n" \
"v_and_b32_e32 v106, v14, v106\n" \
"s_waitcnt vmcnt(0)\n" \
"s_mov_b64 exec, 1\n" \
"buffer_store_dword v61, off, s[56:59], s95 offset:0 sc1\n" \
"s_mov_b64 exec, -1\n" \
"v_and_b32_e32 v107, v15, v107\n" \
"v_xor_b32_e32 v63, 0x400, v19\n" \
"ds_read_b64 v[42:43], v63\n" \
"v_mfma_f32_16x16x32_bf16 a[192:195], a[212:215], v[104:107], a[192:195]\n" \
"v_mov_b32_e32 v194, v28\n" \
"v_mov_b32_e32 v195, v29\n" \
"v_mfma_f32_16x16x32_bf16 v[160:163], a[128:131], v[104:107], v[160:163]\n" \
"v_mov_b32_e32 v196, 0\n" \
"v_mov_b32_e32 v197, 0\n" \
"v_mfma_f32_16x16x32_bf16 v[164:167], a[132:135], v[104:107], v[164:167]\n" \
"v_mov_b32_e32 v192, 0\n" \
"v_mov_b32_e32 v193, 0\n" \
"v_mfma_f32_16x16x32_bf16 v[168:171], a[136:139], v[104:107], v[168:171]\n" \
"v_readlane_b32 s93, v11, 22\n" \
"v_readlane_b32 s92, v11, 27\n" \
"v_mfma_f32_16x16x32_bf16 v[172:175], a[140:143], v[104:107], v[172:175]\n" \
"buffer_load_dwordx4 a[96:99], v20, s[56:59], s60 offen offset:0 sc1\n" \
"v_mfma_f32_16x16x32_bf16 v[176:179], a[144:147], v[104:107], v[176:179]\n" \
"v_exp_f32_e32 v118, v118\n" \
"v_mfma_f32_16x16x32_bf16 v[180:183], a[148:151], v[104:107], v[180:183]\n" \
"v_exp_f32_e32 v119, v119\n" \
"v_mfma_f32_16x16x32_bf16 v[184:187], a[152:155], v[104:107], v[184:187]\n" \
"s_add_u32 m0, s93, 0x0\n" \
"v_cvt_pk_bf16_f32 v119, v118, v119\n" \
"v_mfma_f32_16x16x32_bf16 v[188:191], a[156:159], v[104:107], v[188:191]\n" \
"v_exp_f32_e32 v120, v120\n" \
"s_waitcnt lgkmcnt(1)\n" \
"s_barrier\n" \
"buffer_load_dwordx4 a[112:115], v56, s[56:59], s60 offen offset:0 sc1\n" \
"v_mfma_f32_16x16x32_bf16 v[128:131], v[194:197], v[208:211], v[128:131]\n" \
"v_exp_f32_e32 v121, v121\n" \
"v_mfma_f32_16x16x32_bf16 v[132:135], v[192:195], v[208:211], v[132:135]\n" \
"v_exp_f32_e32 v116, v116\n" \
"v_mfma_f32_16x16x32_bf16 v[136:139], v[194:197], v[212:215], v[136:139]\n" \
"v_exp_f32_e32 v117, v117\n" \
"v_mfma_f32_16x16x32_bf16 v[140:143], v[192:195], v[212:215], v[140:143]\n" \
"v_exp_f32_e32 v122, v122\n" \
"v_mfma_f32_16x16x32_bf16 v[144:147], v[194:197], v[216:219], v[144:147]\n" \
"v_exp_f32_e32 v123, v123\n" \
"v_mfma_f32_16x16x32_bf16 v[148:151], v[192:195], v[216:219], v[148:151]\n" \
"buffer_load_dwordx4 a[100:103], v20, s[56:59], s60 offen offset:1024 sc1\n" \
"v_mfma_f32_16x16x32_bf16 v[152:155], v[194:197], v[220:223], v[152:155]\n" \
"v_exp_f32_e32 v114, v114\n" \
"v_mfma_f32_16x16x32_bf16 v[156:159], v[192:195], v[220:223], v[156:159]\n" \
"v_exp_f32_e32 v115, v115\n" \
"v_mfma_f32_16x16x32_bf16 v[160:163], v[194:197], v[240:243], v[160:163]\n" \
"v_exp_f32_e32 v112, v112\n" \
"v_mfma_f32_16x16x32_bf16 v[164:167], v[192:195], v[240:243], v[164:167]\n" \
"v_exp_f32_e32 v113, v113\n" \
"v_mfma_f32_16x16x32_bf16 v[168:171], v[194:197], v[244:247], v[168:171]\n" \
"v_cvt_pk_bf16_f32 v120, v120, v121\n" \
"v_cvt_pk_bf16_f32 v118, v116, v117\n" \
"v_mfma_f32_16x16x32_bf16 v[172:175], v[192:195], v[244:247], v[172:175]\n" \
"buffer_load_dwordx4 a[116:119], v56, s[56:59], s60 offen offset:1024 sc1\n" \
"v_mfma_f32_16x16x32_bf16 v[176:179], v[194:197], v[248:251], v[176:179]\n" \
"v_exp_f32_e32 v124, v124\n" \
"v_mfma_f32_16x16x32_bf16 v[180:183], v[192:195], v[248:251], v[180:183]\n" \
"v_exp_f32_e32 v125, v125\n" \
"v_mfma_f32_16x16x32_bf16 v[184:187], v[194:197], v[252:255], v[184:187]\n" \
"v_exp_f32_e32 v126, v126\n" \
"v_mfma_f32_16x16x32_bf16 v[188:191], v[192:195], v[252:255], v[188:191]\n" \
"v_exp_f32_e32 v127, v127\n" \
"v_cvt_pk_bf16_f32 v121, v122, v123\n" \
"v_cvt_pk_bf16_f32 v117, v114, v115\n" \
"v_cvt_pk_bf16_f32 v116, v112, v113\n" \
"v_cvt_pk_bf16_f32 v122, v124, v125\n" \
"v_cvt_pk_bf16_f32 v123, v126, v127\n" \
"buffer_load_dwordx4 a[104:107], v20, s[56:59], s60 offen offset:2048 sc1\n" \
"s_nop 0\n" \
"v_smfmac_f32_16x16x64_bf16 a[192:195], a[200:203], v[116:123], v21\n" \
"v_mfma_f32_16x16x32_bf16 v[128:131], a[32:35], v[116:119], v[128:131]\n" \
"v_mfma_f32_16x16x32_bf16 v[160:163], a[32:35], v[120:123], v[160:163]\n" \
"v_mfma_f32_16x16x32_bf16 v[132:135], a[36:39], v[116:119], v[132:135]\n" \
"v_mfma_f32_16x16x32_bf16 v[164:167], a[36:39], v[120:123], v[164:167]\n" \
"v_mfma_f32_16x16x32_bf16 v[136:139], a[40:43], v[116:119], v[136:139]\n" \
"buffer_load_dwordx4 a[120:123], v56, s[56:59], s60 offen offset:2048 sc1\n" \
"v_mfma_f32_16x16x32_bf16 v[168:171], a[40:43], v[120:123], v[168:171]\n" \
"v_mfma_f32_16x16x32_bf16 v[140:143], a[44:47], v[116:119], v[140:143]\n" \
"v_accvgpr_read_b32 v54, a192\n" \
"v_accvgpr_read_b32 v22, a193\n" \
"v_mfma_f32_16x16x32_bf16 v[172:175], a[44:47], v[120:123], v[172:175]\n" \
"v_add_f32_e32 v54, v54, v22\n" \
"s_waitcnt lgkmcnt(0)\n" \
"v_add_f32_e32 v54, v54, v42\n" \
"v_mfma_f32_16x16x32_bf16 v[144:147], a[48:51], v[116:119], v[144:147]\n" \
"v_max_f32_e32 v62, v62, v54\n" \
"v_accvgpr_read_b32 v55, a194\n" \
"v_mfma_f32_16x16x32_bf16 v[176:179], a[48:51], v[120:123], v[176:179]\n" \
"v_rcp_f32_e32 v54, v54\n" \
"buffer_load_dwordx4 a[108:111], v20, s[56:59], s60 offen offset:3072 sc1\n" \
"v_mfma_f32_16x16x32_bf16 v[148:151], a[52:55], v[116:119], v[148:151]\n" \
"v_mul_f32_e32 v54, v54, v58\n" \
"v_mul_f32_e32 v128, v54, v128\n" \
"v_mfma_f32_16x16x32_bf16 v[180:183], a[52:55], v[120:123], v[180:183]\n" \
"v_mul_f32_e32 v129, v54, v129\n" \
"v_mul_f32_e32 v130, v54, v130\n" \
"v_mul_f32_e32 v131, v54, v131\n" \
"v_mul_f32_e32 v132, v54, v132\n" \
"v_mul_f32_e32 v133, v54, v133\n" \
"v_mul_f32_e32 v134, v54, v134\n" \
"v_mul_f32_e32 v135, v54, v135\n" \
"v_cvt_pk_bf16_f32 v64, v128, v129\n" \
"v_cvt_pk_bf16_f32 v65, v130, v131\n" \
"v_cvt_pk_bf16_f32 v66, v132, v133\n" \
"v_cvt_pk_bf16_f32 v67, v134, v135\n" \
"v_accvgpr_read_b32 v23, a195\n" \
"v_mfma_f32_16x16x32_bf16 v[152:155], a[56:59], v[116:119], v[152:155]\n" \
"v_mfma_f32_16x16x32_bf16 a[208:211], a[216:219], v[64:67], a[208:211]\n" \
"buffer_load_dwordx4 a[124:127], v56, s[56:59], s60 offen offset:3072 sc1\n" \
"v_mul_f32_e32 v136, v54, v136\n" \
"v_mul_f32_e32 v137, v54, v137\n" \
"v_mul_f32_e32 v138, v54, v138\n" \
"v_mul_f32_e32 v139, v54, v139\n" \
"v_mul_f32_e32 v140, v54, v140\n" \
"v_mul_f32_e32 v141, v54, v141\n" \
"v_mul_f32_e32 v142, v54, v142\n" \
"v_mul_f32_e32 v143, v54, v143\n" \
"v_add_f32_e32 v55, v55, v23\n" \
"v_cvt_pk_bf16_f32 v68, v136, v137\n" \
"v_cvt_pk_bf16_f32 v69, v138, v139\n" \
"v_cvt_pk_bf16_f32 v70, v140, v141\n" \
"v_cvt_pk_bf16_f32 v71, v142, v143\n" \
"v_add_f32_e32 v55, v55, v43\n" \
"v_mul_f32_e32 v144, v54, v144\n" \
"v_mfma_f32_16x16x32_bf16 a[208:211], a[216:219], v[68:71], a[208:211]\n" \
"v_mul_f32_e32 v145, v54, v145\n" \
"v_mul_f32_e32 v146, v54, v146\n" \
"v_mul_f32_e32 v147, v54, v147\n" \
"v_mul_f32_e32 v148, v54, v148\n" \
"buffer_load_dwordx4 v0, s[80:83], s92 offen offset:0 lds\n" \
"v_mul_f32_e32 v149, v54, v149\n" \
"v_mul_f32_e32 v150, v54, v150\n" \
"v_mul_f32_e32 v151, v54, v151\n" \
"v_max_f32_e32 v62, v62, v55\n" \
"v_cvt_pk_bf16_f32 v72, v144, v145\n" \
"v_cvt_pk_bf16_f32 v73, v146, v147\n" \
"v_cvt_pk_bf16_f32 v74, v148, v149\n" \
"v_cvt_pk_bf16_f32 v75, v150, v151\n" \
"v_rcp_f32_e32 v55, v55\n" \
"v_mfma_f32_16x16x32_bf16 v[184:187], a[56:59], v[120:123], v[184:187]\n" \
"v_mfma_f32_16x16x32_bf16 a[208:211], a[216:219], v[72:75], a[208:211]\n" \
"v_mul_f32_e32 v152, v54, v152\n" \
"v_mul_f32_e32 v153, v54, v153\n" \
"v_mul_f32_e32 v154, v54, v154\n" \
"v_mul_f32_e32 v155, v54, v155\n" \
"v_cvt_pk_bf16_f32 v76, v152, v153\n" \
"buffer_load_dwordx4 v1, s[80:83], s92 offen offset:1024 lds\n" \
"v_cvt_pk_bf16_f32 v77, v154, v155\n" \
"v_mul_f32_e32 v55, v55, v59\n" \
"v_mul_f32_e32 v160, v55, v160\n" \
"v_mul_f32_e32 v161, v55, v161\n" \
"v_mul_f32_e32 v162, v55, v162\n" \
"v_mul_f32_e32 v163, v55, v163\n" \
"v_mul_f32_e32 v164, v55, v164\n" \
"v_mul_f32_e32 v165, v55, v165\n" \
"v_mul_f32_e32 v166, v55, v166\n" \
"v_mul_f32_e32 v167, v55, v167\n" \
"v_cvt_pk_bf16_f32 v80, v160, v161\n" \
"v_cvt_pk_bf16_f32 v81, v162, v163\n" \
"v_cvt_pk_bf16_f32 v82, v164, v165\n" \
"v_cvt_pk_bf16_f32 v83, v166, v167\n" \
"v_mfma_f32_16x16x32_bf16 v[156:159], a[60:63], v[116:119], v[156:159]\n" \
"v_mul_f32_e32 v168, v55, v168\n" \
"v_mul_f32_e32 v169, v55, v169\n" \
"v_mul_f32_e32 v170, v55, v170\n" \
"v_mul_f32_e32 v171, v55, v171\n" \
"v_mul_f32_e32 v172, v55, v172\n" \
"buffer_load_dwordx4 v2, s[80:83], s92 offen offset:2048 lds\n" \
"v_mul_f32_e32 v173, v55, v173\n" \
"v_mul_f32_e32 v174, v55, v174\n" \
"v_mul_f32_e32 v156, v54, v156\n" \
"v_mul_f32_e32 v157, v54, v157\n" \
"v_mul_f32_e32 v158, v54, v158\n" \
"v_mul_f32_e32 v159, v54, v159\n" \
"v_cvt_pk_bf16_f32 v78, v156, v157\n" \
"v_cvt_pk_bf16_f32 v79, v158, v159\n" \
"v_mul_f32_e32 v175, v55, v175\n" \
"v_cvt_pk_bf16_f32 v84, v168, v169\n" \
"v_mfma_f32_16x16x32_bf16 a[208:211], a[216:219], v[76:79], a[208:211]\n" \
"v_mfma_f32_16x16x32_bf16 a[208:211], a[216:219], v[80:83], a[208:211]\n" \
"v_cvt_pk_bf16_f32 v85, v170, v171\n" \
"v_cvt_pk_bf16_f32 v86, v172, v173\n" \
"v_cvt_pk_bf16_f32 v87, v174, v175\n" \
"v_mul_f32_e32 v176, v55, v176\n" \
"v_mul_f32_e32 v177, v55, v177\n" \
"buffer_load_dwordx4 v3, s[80:83], s92 offen offset:3072 lds\n" \
"s_add_u32 m0, s93, 0x1000\n" \
"v_mfma_f32_16x16x32_bf16 a[208:211], a[216:219], v[84:87], a[208:211]\n" \
"v_mul_f32_e32 v178, v55, v178\n" \
"v_mul_f32_e32 v179, v55, v179\n" \
"v_mul_f32_e32 v180, v55, v180\n" \
"v_mul_f32_e32 v181, v55, v181\n" \
"v_mul_f32_e32 v182, v55, v182\n" \
"v_mul_f32_e32 v183, v55, v183\n" \
"v_cvt_pk_bf16_f32 v96, v176, v177\n" \
"v_cvt_pk_bf16_f32 v97, v178, v179\n" \
"v_cvt_pk_bf16_f32 v98, v180, v181\n" \
"v_cvt_pk_bf16_f32 v99, v182, v183\n" \
"v_mfma_f32_16x16x32_bf16 v[188:191], a[60:63], v[120:123], v[188:191]\n" \
"v_mul_f32_e32 v184, v55, v184\n" \
"v_mfma_f32_16x16x32_bf16 a[208:211], a[216:219], v[96:99], a[208:211]\n" \
"v_mul_f32_e32 v185, v55, v185\n" \
"v_mul_f32_e32 v186, v55, v186\n" \
"buffer_load_dwordx4 v4, s[80:83], s92 offen offset:0 lds\n" \
"v_mul_f32_e32 v187, v55, v187\n" \
"v_cvt_pk_bf16_f32 v100, v184, v185\n" \
"v_cvt_pk_bf16_f32 v101, v186, v187\n" \
"v_mul_f32_e32 v188, v55, v188\n" \
"v_mul_f32_e32 v189, v55, v189\n" \
"v_mul_f32_e32 v190, v55, v190\n" \
"v_mul_f32_e32 v191, v55, v191\n" \
"v_cvt_pk_bf16_f32 v102, v188, v189\n" \
"v_cvt_pk_bf16_f32 v103, v190, v191\n" \
"v_cmp_lt_f32_e64 s[72:73], s21, v62\n" \
"buffer_load_dwordx4 v5, s[80:83], s92 offen offset:1024 lds\n" \
"v_mfma_f32_16x16x32_bf16 a[208:211], a[216:219], v[100:103], a[208:211]\n" \
"buffer_load_dwordx4 v6, s[80:83], s92 offen offset:2048 lds\n" \
"buffer_load_dwordx4 v7, s[80:83], s92 offen offset:3072 lds\n" \
"s_add_u32 m0, s93, 0x2000\n" \
"s_nop 0\n" \
"buffer_load_dwordx4 v8, s[80:83], s92 offen offset:0 lds\n" \
"s_nop 2\n" \
"v_accvgpr_read_b32 v63, a208\n" \
"v_cmp_u_f32_e32 vcc, v63, v63\n" \
"s_nop 0\n" \
"s_or_b64 vcc, s[72:73], vcc\n" \
"s_cbranch_scc0 L_out_%=\n" \
"s_branch L_end_%=\n" \
".fill (L_ab + 24576 - .) / 4, 4, 0xBF800000\n" \
".fill (L_ab + 24576 - .) / 4, 4, 0xBF800000\n" \
"L_ap6:\n" \
"s_setpc_b64 s[88:89]\n" \
"L_gs7:\n" \
"v_mfma_f32_16x16x32_bf16 v[104:107], a[128:131], a[16:19], v[36:39]\n" \
"s_add_u32 s68, s60, 0xfff6e000\n" \
"s_add_u32 s69, s60, 0xfffb7000\n" \
"s_add_u32 s70, s60, 0x0\n" \
"s_add_u32 s71, s60, 0x49000\n" \
"v_mfma_f32_16x16x32_bf16 v[104:107], a[132:135], a[20:23], v[104:107]\n" \
"s_xor_b32 m0, s65, 0x12000\n" \
"ds_read_b128 a[160:163], v49 offset:0\n" \
"v_mfma_f32_16x16x32_bf16 v[104:107], a[136:139], a[24:27], v[104:107]\n" \
"buffer_load_dwordx4 v50, s[8:11], s64 offen offset:0 sc1 lds\n" \
"v_mfma_f32_16x16x32_bf16 v[104:107], a[140:143], a[28:31], v[104:107]\n" \
"ds_read_b128 a[164:167], v49 offset:1024\n" \
"ds_read_b128 a[168:171], v49 offset:2048\n" \
"v_mfma_f32_16x16x32_bf16 v[108:111], a[156:159], a[28:31], v[36:39]\n" \
"ds_read_b128 a[172:175], v49 offset:3072\n" \
"ds_read_b128 a[176:179], v49 offset:4096\n" \
"v_mfma_f32_16x16x32_bf16 v[108:111], a[152:155], a[24:27], v[108:111]\n" \
"s_mov_b32 s65, s13\n" \
"v_add_u32_e32 v63, s15, v20\n" \
"v_mfma_f32_16x16x32_bf16 v[108:111], a[148:151], a[20:23], v[108:111]\n" \
"buffer_load_dwordx4 v50, s[8:11], s64 offen offset:1024 sc1 lds\n" \
"v_mfma_f32_16x16x32_bf16 v[108:111], a[144:147], a[16:19], v[108:111]\n" \
"s_add_u32 s67, s14, 40960\n" \
"v_add_u32_e32 v50, 0x6000, v63\n" \
"v_mfma_f32_16x16x32_bf16 v[116:119], a[144:147], a[32:35], v[40:43]\n" \
"s_xor_b32 m0, s65, 0x8000\n" \
"ds_read_b128 a[180:183], v49 offset:5120\n" \
"v_mfma_f32_16x16x32_bf16 v[116:119], a[148:151], a[36:39], v[116:119]\n" \
"ds_read_b128 a[184:187], v49 offset:6144\n" \
"v_and_b32_e32 v68, v12, v68\n" \
"v_mfma_f32_16x16x32_bf16 v[116:119], a[152:155], a[40:43], v[116:119]\n" \
"v_and_b32_e32 v69, v13, v69\n" \
"v_and_b32_e32 v70, v14, v70\n" \
"v_mfma_f32_16x16x32_bf16 v[116:119], a[156:159], a[44:47], v[116:119]\n" \
"buffer_load_dwordx4 v50, s[8:11], s67 offen offset:0 sc1 lds\n" \
"v_mfma_f32_16x16x32_bf16 v[112:115], a[140:143], a[44:47], v[40:43]\n" \
"v_and_b32_e32 v71, v15, v71\n" \
"ds_read_b128 a[188:191], v49 offset:7168\n" \
"v_mfma_f32_16x16x32_bf16 v[112:115], a[136:139], a[40:43], v[112:115]\n" \
"v_exp_f32_e32 v86, v86\n" \
"v_mfma_f32_16x16x32_bf16 v[112:115], a[132:135], a[36:39], v[112:115]\n" \
"v_exp_f32_e32 v87, v87\n" \
"v_mfma_f32_16x16x32_bf16 v[112:115], a[128:131], a[32:35], v[112:115]\n" \
"v_exp_f32_e32 v84, v84\n" \
"v_mfma_f32_16x16x32_bf16 v[120:123], a[128:131], a[48:51], v[44:47]\n" \
"buffer_load_dwordx4 v50, s[8:11], s67 offen offset:1024 sc1 lds\n" \
"v_mfma_f32_16x16x32_bf16 v[120:123], a[132:135], a[52:55], v[120:123]\n" \
"v_exp_f32_e32 v85, v85\n" \
"v_mfma_f32_16x16x32_bf16 v[120:123], a[136:139], a[56:59], v[120:123]\n" \
"v_cvt_pk_bf16_f32 v87, v86, v87\n" \
"v_cvt_pk_bf16_f32 v86, v84, v85\n" \
"v_mfma_f32_16x16x32_bf16 v[120:123], a[140:143], a[60:63], v[120:123]\n" \
"v_exp_f32_e32 v82, v82\n" \
"v_mfma_f32_16x16x32_bf16 v[124:127], a[156:159], a[60:63], v[44:47]\n" \
"v_exp_f32_e32 v83, v83\n" \
"v_mfma_f32_16x16x32_bf16 v[124:127], a[152:155], a[56:59], v[124:127]\n" \
"v_exp_f32_e32 v80, v80\n" \
"v_mfma_f32_16x16x32_bf16 v[124:127], a[148:151], a[52:55], v[124:127]\n" \
"buffer_load_dwordx4 v50, s[8:11], s67 offen offset:2048 sc1 lds\n" \
"v_mfma_f32_16x16x32_bf16 v[124:127], a[144:147], a[48:51], v[124:127]\n" \
"v_cvt_pk_bf16_f32 v85, v82, v83\n" \
"s_waitcnt lgkmcnt(1)\n" \
"v_mfma_f32_16x16x32_bf16 v[128:131], a[160:163], v[68:71], v[128:131]\n" \
"v_exp_f32_e32 v81, v81\n" \
"v_mfma_f32_16x16x32_bf16 v[160:163], a[160:163], v[72:75], v[160:163]\n" \
"v_cvt_pk_bf16_f32 v84, v80, v81\n" \
"s_mov_b32 s64, s14\n" \
"v_mfma_f32_16x16x32_bf16 v[132:135], a[164:167], v[68:71], v[132:135]\n" \
"v_exp_f32_e32 v88, v88\n" \
"v_mfma_f32_16x16x32_bf16 v[164:167], a[164:167], v[72:75], v[164:167]\n" \
"v_exp_f32_e32 v89, v89\n" \
"v_mfma_f32_16x16x32_bf16 v[136:139], a[168:171], v[68:71], v[136:139]\n" \
"buffer_load_dwordx4 v50, s[8:11], s67 offen offset:3072 sc1 lds\n" \
"v_mfma_f32_16x16x32_bf16 v[168:171], a[168:171], v[72:75], v[168:171]\n" \
"v_exp_f32_e32 v90, v90\n" \
"v_mfma_f32_16x16x32_bf16 v[140:143], a[172:175], v[68:71], v[140:143]\n" \
"v_exp_f32_e32 v91, v91\n" \
"v_mfma_f32_16x16x32_bf16 v[172:175], a[172:175], v[72:75], v[172:175]\n" \
"v_cvt_pk_bf16_f32 v88, v88, v89\n" \
"v_cvt_pk_bf16_f32 v89, v90, v91\n" \
"v_mfma_f32_16x16x32_bf16 v[144:147], a[176:179], v[68:71], v[144:147]\n" \
"v_exp_f32_e32 v92, v92\n" \
"v_mfma_f32_16x16x32_bf16 v[176:179], a[176:179], v[72:75], v[176:179]\n" \
"v_exp_f32_e32 v93, v93\n" \
"v_mfma_f32_16x16x32_bf16 v[148:151], a[180:183], v[68:71], v[148:151]\n" \
"v_exp_f32_e32 v94, v94\n" \
"v_mfma_f32_16x16x32_bf16 v[180:183], a[180:183], v[72:75], v[180:183]\n" \
"v_exp_f32_e32 v95, v95\n" \
"v_mfma_f32_16x16x32_bf16 v[152:155], a[184:187], v[68:71], v[152:155]\n" \
"v_cvt_pk_bf16_f32 v90, v92, v93\n" \
"v_cvt_pk_bf16_f32 v91, v94, v95\n" \
"v_mfma_f32_16x16x32_bf16 v[184:187], a[184:187], v[72:75], v[184:187]\n" \
"v_add_u32_e32 v51, 0x12000, v63\n" \
"s_waitcnt lgkmcnt(0)\n" \
"v_mfma_f32_16x16x32_bf16 v[156:159], a[188:191], v[68:71], v[156:159]\n" \
"s_xor_b32 m0, s65, 0xa000\n" \
"ds_read_b32 v58, v18 offset:512\n" \
"v_mfma_f32_16x16x32_bf16 v[188:191], a[188:191], v[72:75], v[188:191]\n" \
"buffer_load_dwordx4 v51, s[8:11], s64 offen offset:0 sc1 lds\n" \
"v_smfmac_f32_16x16x64_bf16 a[192:195], a[200:203], v[68:75], v21\n" \
"ds_read_b32 v59, v18 offset:640\n" \
"ds_read_b32 v60, v18 offset:768\n" \
"v_mfma_f32_16x16x32_bf16 v[220:223], a[188:191], v[84:87], v[220:223]\n" \
"ds_read_b32 v61, v18 offset:896\n" \
"ds_read_b128 a[128:131], v48 offset:0\n" \
"v_mfma_f32_16x16x32_bf16 v[252:255], a[188:191], v[88:91], v[252:255]\n" \
"ds_read_b128 a[144:147], v48 offset:4096\n" \
"ds_read_b128 a[132:135], v48 offset:1024\n" \
"v_mfma_f32_16x16x32_bf16 v[216:219], a[184:187], v[84:87], v[216:219]\n" \
"buffer_load_dwordx4 v51, s[8:11], s64 offen offset:1024 sc1 lds\n" \
"v_mfma_f32_16x16x32_bf16 v[248:251], a[184:187], v[88:91], v[248:251]\n" \
"ds_read_b128 a[148:151], v48 offset:5120\n" \
"ds_read_b128 a[136:139], v48 offset:2048\n" \
"v_mfma_f32_16x16x32_bf16 v[212:215], a[180:183], v[84:87], v[212:215]\n" \
"ds_read_b128 a[152:155], v48 offset:6144\n" \
"ds_read_b128 a[140:143], v48 offset:3072\n" \
"v_mfma_f32_16x16x32_bf16 v[244:247], a[180:183], v[88:91], v[244:247]\n" \
"ds_read_b128 a[156:159], v48 offset:7168\n" \
"v_add_u32_e32 v52, 0x14000, v63\n" \
"v_mfma_f32_16x16x32_bf16 v[208:211], a[176:179], v[84:87], v[208:211]\n" \
"buffer_load_dwordx4 v51, s[8:11], s64 offen offset:2048 sc1 lds\n" \
"v_mfma_f32_16x16x32_bf16 v[240:243], a[176:179], v[88:91], v[240:243]\n" \
"v_exp_f32_e32 v104, v104\n" \
"v_mfma_f32_16x16x32_bf16 v[204:207], a[172:175], v[84:87], v[204:207]\n" \
"v_exp_f32_e32 v105, v105\n" \
"v_mfma_f32_16x16x32_bf16 v[236:239], a[172:175], v[88:91], v[236:239]\n" \
"v_exp_f32_e32 v106, v106\n" \
"v_mfma_f32_16x16x32_bf16 v[200:203], a[168:171], v[84:87], v[200:203]\n" \
"buffer_load_dwordx4 v51, s[8:11], s64 offen offset:3072 sc1 lds\n" \
"v_mfma_f32_16x16x32_bf16 v[232:235], a[168:171], v[88:91], v[232:235]\n" \
"v_exp_f32_e32 v107, v107\n" \
"v_mfma_f32_16x16x32_bf16 v[196:199], a[164:167], v[84:87], v[196:199]\n" \
"v_cvt_pk_bf16_f32 v104, v104, v105\n" \
"v_cvt_pk_bf16_f32 v105, v106, v107\n" \
"v_mfma_f32_16x16x32_bf16 v[228:231], a[164:167], v[88:91], v[228:231]\n" \
"v_exp_f32_e32 v108, v108\n" \
"v_mfma_f32_16x16x32_bf16 v[192:195], a[160:163], v[84:87], v[192:195]\n" \
"v_exp_f32_e32 v109, v109\n" \
"v_mfma_f32_16x16x32_bf16 v[224:227], a[160:163], v[88:91], v[224:227]\n" \
"v_exp_f32_e32 v110, v110\n" \
"v_smfmac_f32_16x16x64_bf16 a[196:199], a[200:203], v[84:91], v21\n" \
"v_exp_f32_e32 v111, v111\n" \
"v_cvt_pk_bf16_f32 v106, v108, v109\n" \
"v_cvt_pk_bf16_f32 v107, v110, v111\n" \
"v_and_b32_e32 v104, v12, v104\n" \
"v_and_b32_e32 v105, v13, v105\n" \
"v_and_b32_e32 v106, v14, v106\n" \
"v_and_b32_e32 v107, v15, v107\n" \
"v_accvgpr_read_b32 v54, a192\n" \
"v_accvgpr_read_b32 v22, a193\n" \
"v_readlane_b32 s93, v11, 19\n" \
"v_readlane_b32 s80, v11, 11\n" \
"v_readlane_b32 s81, v11, 12\n" \
"s_mov_b32 s82, 0x47fca0\n" \
"s_mov_b32 s83, 0x20000\n" \
"v_readlane_b32 s92, v11, 26\n" \
"v_exp_f32_e32 v118, v118\n" \
"v_exp_f32_e32 v119, v119\n" \
"v_exp_f32_e32 v116, v116\n" \
"v_exp_f32_e32 v117, v117\n" \
"v_cvt_pk_bf16_f32 v119, v118, v119\n" \
"v_exp_f32_e32 v114, v114\n" \
"v_exp_f32_e32 v115, v115\n" \
"v_exp_f32_e32 v112, v112\n" \
"v_cvt_pk_bf16_f32 v118, v116, v117\n" \
"s_waitcnt vmcnt(10) lgkmcnt(0)\n" \
"s_barrier\n" \
"v_mfma_f32_16x16x32_bf16 v[80:83], a[128:131], a[32:35], v[40:43]\n" \
"ds_read_b128 a[160:163], v49 offset:8192\n" \
"ds_read_b128 a[164:167], v49 offset:9216\n" \
"v_mfma_f32_16x16x32_bf16 v[80:83], a[132:135], a[36:39], v[80:83]\n" \
"ds_read_b128 a[168:171], v49 offset:10240\n" \
"s_xor_b32 m0, s65, 0x18000\n" \
"v_mfma_f32_16x16x32_bf16 v[80:83], a[136:139], a[40:43], v[80:83]\n" \
"buffer_load_dwordx4 v52, s[8:11], s64 offen offset:0 sc1 lds\n" \
"v_mfma_f32_16x16x32_bf16 v[80:83], a[140:143], a[44:47], v[80:83]\n" \
"ds_read_b128 a[172:175], v49 offset:11264\n" \
"ds_read_b128 a[176:179], v49 offset:12288\n" \
"v_mfma_f32_16x16x32_bf16 v[84:87], a[156:159], a[44:47], v[40:43]\n" \
"ds_read_b128 a[180:183], v49 offset:13312\n" \
"ds_read_b128 a[184:187], v49 offset:14336\n" \
"v_mfma_f32_16x16x32_bf16 v[84:87], a[152:155], a[40:43], v[84:87]\n" \
"ds_read_b128 a[188:191], v49 offset:15360\n" \
"v_cvt_pk_bf16_f32 v117, v114, v115\n" \
"v_mfma_f32_16x16x32_bf16 v[84:87], a[148:151], a[36:39], v[84:87]\n" \
"buffer_load_dwordx4 v52, s[8:11], s64 offen offset:1024 sc1 lds\n" \
"v_mfma_f32_16x16x32_bf16 v[84:87], a[144:147], a[32:35], v[84:87]\n" \
"v_exp_f32_e32 v113, v113\n" \
"v_mfma_f32_16x16x32_bf16 v[92:95], a[144:147], a[48:51], v[44:47]\n" \
"v_cvt_pk_bf16_f32 v116, v112, v113\n" \
"ds_read_b128 a[144:147], v48 offset:12288\n" \
"v_mfma_f32_16x16x32_bf16 v[92:95], a[148:151], a[52:55], v[92:95]\n" \
"v_exp_f32_e32 v120, v120\n" \
"v_mfma_f32_16x16x32_bf16 v[92:95], a[152:155], a[56:59], v[92:95]\n" \
"buffer_load_dwordx4 v52, s[8:11], s64 offen offset:2048 sc1 lds\n" \
"v_mfma_f32_16x16x32_bf16 v[92:95], a[156:159], a[60:63], v[92:95]\n" \
"v_exp_f32_e32 v121, v121\n" \
"v_mfma_f32_16x16x32_bf16 v[88:91], a[140:143], a[60:63], v[44:47]\n" \
"v_exp_f32_e32 v122, v122\n" \
"v_mfma_f32_16x16x32_bf16 v[88:91], a[136:139], a[56:59], v[88:91]\n" \
"v_exp_f32_e32 v123, v123\n" \
"v_mfma_f32_16x16x32_bf16 v[88:91], a[132:135], a[52:55], v[88:91]\n" \
"buffer_load_dwordx4 v52, s[8:11], s64 offen offset:3072 sc1 lds\n" \
"v_mfma_f32_16x16x32_bf16 v[88:91], a[128:131], a[48:51], v[88:91]\n" \
"s_add_u32 m0, s93, 0x0\n" \
"v_cvt_pk_bf16_f32 v120, v120, v121\n" \
"v_mfma_f32_16x16x32_bf16 a[192:195], a[212:215], v[104:107], a[192:195]\n" \
"v_cvt_pk_bf16_f32 v121, v122, v123\n" \
"s_waitcnt lgkmcnt(6)\n" \
"v_mfma_f32_16x16x32_bf16 v[160:163], a[160:163], v[104:107], v[160:163]\n" \
"v_exp_f32_e32 v124, v124\n" \
"v_mfma_f32_16x16x32_bf16 v[164:167], a[164:167], v[104:107], v[164:167]\n" \
"buffer_load_dwordx4 v0, s[80:83], s92 offen offset:0 lds\n" \
"v_mfma_f32_16x16x32_bf16 v[168:171], a[168:171], v[104:107], v[168:171]\n" \
"ds_read_b128 a[128:131], v48 offset:8192\n" \
"s_waitcnt lgkmcnt(2)\n" \
"v_mfma_f32_16x16x32_bf16 v[172:175], a[172:175], v[104:107], v[172:175]\n" \
"v_exp_f32_e32 v125, v125\n" \
"v_mfma_f32_16x16x32_bf16 v[176:179], a[176:179], v[104:107], v[176:179]\n" \
"v_exp_f32_e32 v126, v126\n" \
"buffer_load_dwordx4 v1, s[80:83], s92 offen offset:1024 lds\n" \
"v_mfma_f32_16x16x32_bf16 v[180:183], a[180:183], v[104:107], v[180:183]\n" \
"v_exp_f32_e32 v127, v127\n" \
"v_mfma_f32_16x16x32_bf16 v[184:187], a[184:187], v[104:107], v[184:187]\n" \
"v_cvt_pk_bf16_f32 v122, v124, v125\n" \
"v_cvt_pk_bf16_f32 v123, v126, v127\n" \
"v_mfma_f32_16x16x32_bf16 v[188:191], a[188:191], v[104:107], v[188:191]\n" \
"ds_read_b128 a[132:135], v48 offset:9216\n" \
"ds_read_b128 a[148:151], v48 offset:13312\n" \
"buffer_load_dwordx4 v2, s[80:83], s92 offen offset:2048 lds\n" \
"v_mfma_f32_16x16x32_bf16 v[220:223], a[188:191], v[116:119], v[220:223]\n" \
"ds_read_b128 a[136:139], v48 offset:10240\n" \
"ds_read_b128 a[152:155], v48 offset:14336\n" \
"v_mfma_f32_16x16x32_bf16 v[252:255], a[188:191], v[120:123], v[252:255]\n" \
"ds_read_b128 a[140:143], v48 offset:11264\n" \
"ds_read_b128 a[156:159], v48 offset:15360\n" \
"v_mfma_f32_16x16x32_bf16 v[216:219], a[184:187], v[116:119], v[216:219]\n" \
"v_readlane_b32 s96, v11, 1\n" \
"v_bfe_u32 v27, v20, 2, 7\n" \
"buffer_load_dwordx4 v3, s[80:83], s92 offen offset:3072 lds\n" \
"v_mfma_f32_16x16x32_bf16 v[248:251], a[184:187], v[120:123], v[248:251]\n" \
"s_add_u32 m0, s93, 0x1000\n" \
"v_add_f32_e32 v54, v54, v22\n" \
"v_mfma_f32_16x16x32_bf16 v[212:215], a[180:183], v[116:119], v[212:215]\n" \
"v_max_f32_e32 v62, v62, v54\n" \
"v_add_u32_e32 v49, 0x10000, v48\n" \
"v_mfma_f32_16x16x32_bf16 v[244:247], a[180:183], v[120:123], v[244:247]\n" \
"v_rcp_f32_e32 v54, v54\n" \
"buffer_load_dwordx4 v4, s[80:83], s92 offen offset:0 lds\n" \
"v_mfma_f32_16x16x32_bf16 v[208:211], a[176:179], v[116:119], v[208:211]\n" \
"v_mul_f32_e32 v54, v54, v58\n" \
"v_mul_f32_e32 v128, v54, v128\n" \
"v_mfma_f32_16x16x32_bf16 v[240:243], a[176:179], v[120:123], v[240:243]\n" \
"v_mul_f32_e32 v129, v54, v129\n" \
"v_mul_f32_e32 v136, v54, v136\n" \
"v_mfma_f32_16x16x32_bf16 v[204:207], a[172:175], v[116:119], v[204:207]\n" \
"v_mul_f32_e32 v137, v54, v137\n" \
"v_mul_f32_e32 v144, v54, v144\n" \
"buffer_load_dwordx4 v5, s[80:83], s92 offen offset:1024 lds\n" \
"v_mfma_f32_16x16x32_bf16 v[236:239], a[172:175], v[120:123], v[236:239]\n" \
"v_mul_f32_e32 v145, v54, v145\n" \
"v_mul_f32_e32 v152, v54, v152\n" \
"v_mfma_f32_16x16x32_bf16 v[200:203], a[168:171], v[116:119], v[200:203]\n" \
"v_mul_f32_e32 v153, v54, v153\n" \
"v_cvt_pk_bf16_f32 v128, v128, v129\n" \
"v_mfma_f32_16x16x32_bf16 v[232:235], a[168:171], v[120:123], v[232:235]\n" \
"v_mul_f32_e32 v130, v54, v130\n" \
"v_mul_f32_e32 v131, v54, v131\n" \
"buffer_load_dwordx4 v6, s[80:83], s92 offen offset:2048 lds\n" \
"v_mfma_f32_16x16x32_bf16 v[196:199], a[164:167], v[116:119], v[196:199]\n" \
"v_cvt_pk_bf16_f32 v136, v136, v137\n" \
"v_mul_f32_e32 v138, v54, v138\n" \
"v_mfma_f32_16x16x32_bf16 v[228:231], a[164:167], v[120:123], v[228:231]\n" \
"v_mul_f32_e32 v139, v54, v139\n" \
"v_cvt_pk_bf16_f32 v144, v144, v145\n" \
"v_mfma_f32_16x16x32_bf16 v[192:195], a[160:163], v[116:119], v[192:195]\n" \
"v_mul_f32_e32 v146, v54, v146\n" \
"v_mul_f32_e32 v147, v54, v147\n" \
"buffer_load_dwordx4 v7, s[80:83], s92 offen offset:3072 lds\n" \
"v_mfma_f32_16x16x32_bf16 v[224:227], a[160:163], v[120:123], v[224:227]\n" \
"v_cvt_pk_bf16_f32 v152, v152, v153\n" \
"v_mul_f32_e32 v154, v54, v154\n" \
"v_smfmac_f32_16x16x64_bf16 a[196:199], a[200:203], v[116:123], v21\n" \
"v_mul_f32_e32 v155, v54, v155\n" \
"v_cvt_pk_bf16_f32 v129, v130, v131\n" \
"v_mul_f32_e32 v132, v54, v132\n" \
"v_mul_f32_e32 v133, v54, v133\n" \
"v_mul_f32_e32 v134, v54, v134\n" \
"v_mul_f32_e32 v135, v54, v135\n" \
"v_cvt_pk_bf16_f32 v137, v138, v139\n" \
"v_mul_f32_e32 v140, v54, v140\n" \
"v_mul_f32_e32 v141, v54, v141\n" \
"v_mul_f32_e32 v142, v54, v142\n" \
"v_mul_f32_e32 v143, v54, v143\n" \
"v_cvt_pk_bf16_f32 v145, v146, v147\n" \
"v_mul_f32_e32 v148, v54, v148\n" \
"v_mul_f32_e32 v149, v54, v149\n" \
"v_mul_f32_e32 v150, v54, v150\n" \
"v_mul_f32_e32 v151, v54, v151\n" \
"v_cvt_pk_bf16_f32 v153, v154, v155\n" \
"v_mul_f32_e32 v156, v54, v156\n" \
"v_mul_f32_e32 v157, v54, v157\n" \
"v_mul_f32_e32 v158, v54, v158\n" \
"v_mul_f32_e32 v159, v54, v159\n" \
"v_cvt_pk_bf16_f32 v130, v132, v133\n" \
"v_cvt_pk_bf16_f32 v131, v134, v135\n" \
"buffer_store_dwordx4 v[128:131], v20, s[56:59], s68 offen offset:0 sc1\n" \
"v_cvt_pk_bf16_f32 v138, v140, v141\n" \
"v_cvt_pk_bf16_f32 v139, v142, v143\n" \
"v_cvt_pk_bf16_f32 v146, v148, v149\n" \
"v_cvt_pk_bf16_f32 v147, v150, v151\n" \
"v_cvt_pk_bf16_f32 v154, v156, v157\n" \
"v_cvt_pk_bf16_f32 v155, v158, v159\n" \
"s_add_u32 m0, s93, 0x2000\n" \
"s_xor_b32 s65, s65, 0x8000\n" \
"s_add_u32 s64, s64, 0x10000\n" \
"v_exp_f32_e32 v86, v86\n" \
"buffer_store_dwordx4 v[136:139], v20, s[56:59], s68 offen offset:1024 sc1\n" \
"v_exp_f32_e32 v87, v87\n" \
"v_exp_f32_e32 v84, v84\n" \
"v_exp_f32_e32 v85, v85\n" \
"v_exp_f32_e32 v88, v88\n" \
"v_exp_f32_e32 v89, v89\n" \
"v_cvt_pk_bf16_f32 v87, v86, v87\n" \
"buffer_store_dwordx4 v[144:147], v20, s[56:59], s68 offen offset:2048 sc1\n" \
"v_exp_f32_e32 v82, v82\n" \
"v_exp_f32_e32 v83, v83\n" \
"v_exp_f32_e32 v80, v80\n" \
"v_exp_f32_e32 v81, v81\n" \
"v_exp_f32_e32 v90, v90\n" \
"v_exp_f32_e32 v91, v91\n" \
"buffer_store_dwordx4 v[152:155], v20, s[56:59], s68 offen offset:3072 sc1\n" \
"v_cvt_pk_bf16_f32 v86, v84, v85\n" \
"v_cvt_pk_bf16_f32 v88, v88, v89\n" \
"v_exp_f32_e32 v92, v92\n" \
"v_exp_f32_e32 v93, v93\n" \
"v_exp_f32_e32 v94, v94\n" \
"v_exp_f32_e32 v95, v95\n" \
"v_cvt_pk_bf16_f32 v85, v82, v83\n" \
"buffer_load_dwordx4 v8, s[80:83], s92 offen offset:0 lds\n" \
"v_cvt_pk_bf16_f32 v84, v80, v81\n" \
"v_cvt_pk_bf16_f32 v89, v90, v91\n" \
"v_and_b32_e32 v84, v12, v84\n" \
"v_and_b32_e32 v85, v13, v85\n" \
"v_and_b32_e32 v86, v14, v86\n" \
"v_and_b32_e32 v87, v15, v87\n" \
"s_waitcnt vmcnt(17) lgkmcnt(0)\n" \
"s_barrier\n" \
"v_mfma_f32_16x16x32_bf16 v[120:123], a[128:131], a[48:51], v[44:47]\n" \
"buffer_load_dword v26, v27, s[56:59], s96 offen sc1\n" \
"v_mfma_f32_16x16x32_bf16 v[120:123], a[132:135], a[52:55], v[120:123]\n" \
"ds_read_b128 a[188:191], v49 offset:7168\n" \
"ds_read_b128 a[184:187], v49 offset:6144\n" \
"v_mfma_f32_16x16x32_bf16 v[120:123], a[136:139], a[56:59], v[120:123]\n" \
"s_xor_b32 m0, s65, 0x12000\n" \
"ds_read_b128 a[180:183], v49 offset:5120\n" \
"v_mfma_f32_16x16x32_bf16 v[120:123], a[140:143], a[60:63], v[120:123]\n" \
"ds_read_b128 a[176:179], v49 offset:4096\n" \
"ds_read_b128 a[172:175], v49 offset:3072\n" \
"v_mfma_f32_16x16x32_bf16 v[124:127], a[156:159], a[60:63], v[44:47]\n" \
"buffer_load_dwordx4 v50, s[8:11], s64 offen offset:0 sc1 lds\n" \
"v_mfma_f32_16x16x32_bf16 v[124:127], a[152:155], a[56:59], v[124:127]\n" \
"ds_read_b128 a[168:171], v49 offset:2048\n" \
"v_cvt_pk_bf16_f32 v90, v92, v93\n" \
"v_mfma_f32_16x16x32_bf16 v[124:127], a[148:151], a[52:55], v[124:127]\n" \
"v_cvt_pk_bf16_f32 v91, v94, v95\n" \
"ds_read_b128 a[164:167], v49 offset:1024\n" \
"v_mfma_f32_16x16x32_bf16 v[124:127], a[144:147], a[48:51], v[124:127]\n" \
"ds_read_b128 a[160:163], v49 offset:0\n" \
"ds_read_b128 a[0:3], v49 offset:8192\n" \
"v_mfma_f32_16x16x32_bf16 v[32:35], a[220:223], a[64:67], 0\n" \
"buffer_load_dwordx4 v50, s[8:11], s64 offen offset:1024 sc1 lds\n" \
"v_mfma_f32_16x16x32_bf16 v[36:39], a[220:223], a[80:83], 0\n" \
"ds_read_b128 a[4:7], v49 offset:9216\n" \
"ds_read_b128 a[8:11], v49 offset:10240\n" \
"v_mfma_f32_16x16x32_bf16 v[32:35], a[224:227], a[68:71], v[32:35]\n" \
"ds_read_b128 a[12:15], v49 offset:11264\n" \
"ds_read_b128 a[16:19], v49 offset:12288\n" \
"v_mfma_f32_16x16x32_bf16 v[36:39], a[224:227], a[84:87], v[36:39]\n" \
"ds_read_b128 a[20:23], v49 offset:13312\n" \
"ds_read_b128 a[24:27], v49 offset:14336\n" \
"v_mfma_f32_16x16x32_bf16 v[32:35], a[228:231], a[72:75], v[32:35]\n" \
"buffer_load_dwordx4 v50, s[8:11], s64 offen offset:2048 sc1 lds\n" \
"v_mfma_f32_16x16x32_bf16 v[36:39], a[228:231], a[88:91], v[36:39]\n" \
"ds_read_b128 a[28:31], v49 offset:15360\n" \
"v_readlane_b32 s97, v11, 36\n" \
"v_mfma_f32_16x16x32_bf16 v[32:35], a[232:235], a[76:79], v[32:35]\n" \
"v_exp_f32_e32 v120, v120\n" \
"v_mfma_f32_16x16x32_bf16 v[36:39], a[232:235], a[92:95], v[36:39]\n" \
"v_exp_f32_e32 v121, v121\n" \
"v_smfmac_f32_16x16x64_bf16 a[196:199], a[200:203], v[84:91], v21\n" \
"v_cvt_pk_bf16_f32 v120, v120, v121\n" \
"s_waitcnt lgkmcnt(13)\n" \
"v_mfma_f32_16x16x32_bf16 v[220:223], a[188:191], v[84:87], v[220:223]\n" \
"buffer_load_dwordx4 v50, s[8:11], s64 offen offset:3072 sc1 lds\n" \
"v_mfma_f32_16x16x32_bf16 v[252:255], a[188:191], v[88:91], v[252:255]\n" \
"v_exp_f32_e32 v122, v122\n" \
"v_mfma_f32_16x16x32_bf16 v[216:219], a[184:187], v[84:87], v[216:219]\n" \
"v_exp_f32_e32 v123, v123\n" \
"v_mfma_f32_16x16x32_bf16 v[248:251], a[184:187], v[88:91], v[248:251]\n" \
"v_exp_f32_e32 v124, v124\n" \
"v_mfma_f32_16x16x32_bf16 v[212:215], a[180:183], v[84:87], v[212:215]\n" \
"v_exp_f32_e32 v125, v125\n" \
"v_mfma_f32_16x16x32_bf16 v[244:247], a[180:183], v[88:91], v[244:247]\n" \
"v_cvt_pk_bf16_f32 v121, v122, v123\n" \
"s_waitcnt lgkmcnt(7)\n" \
"v_mfma_f32_16x16x32_bf16 v[208:211], a[176:179], v[84:87], v[208:211]\n" \
"v_exp_f32_e32 v126, v126\n" \
"v_mfma_f32_16x16x32_bf16 v[240:243], a[176:179], v[88:91], v[240:243]\n" \
"v_exp_f32_e32 v127, v127\n" \
"v_mfma_f32_16x16x32_bf16 v[204:207], a[172:175], v[84:87], v[204:207]\n" \
"v_cvt_pk_bf16_f32 v122, v124, v125\n" \
"v_cvt_pk_bf16_f32 v123, v126, v127\n" \
"v_mfma_f32_16x16x32_bf16 v[236:239], a[172:175], v[88:91], v[236:239]\n" \
"v_and_b32_e32 v120, v12, v120\n" \
"v_and_b32_e32 v121, v13, v121\n" \
"v_mfma_f32_16x16x32_bf16 v[200:203], a[168:171], v[84:87], v[200:203]\n" \
"v_and_b32_e32 v122, v14, v122\n" \
"v_and_b32_e32 v123, v15, v123\n" \
"v_mfma_f32_16x16x32_bf16 v[232:235], a[168:171], v[88:91], v[232:235]\n" \
"v_accvgpr_read_b32 v56, a196\n" \
"v_accvgpr_read_b32 v24, a197\n" \
"v_mfma_f32_16x16x32_bf16 v[196:199], a[164:167], v[84:87], v[196:199]\n" \
"v_add_u32_e32 v17, 0x49000, v20\n" \
"v_readlane_b32 s98, v11, 37\n" \
"v_mfma_f32_16x16x32_bf16 v[228:231], a[164:167], v[88:91], v[228:231]\n" \
"v_add_u32_e32 v48, s16, v20\n" \
"ds_read_b128 a[128:131], v48 offset:0\n" \
"v_mfma_f32_16x16x32_bf16 v[192:195], a[160:163], v[84:87], v[192:195]\n" \
"ds_read_b128 a[144:147], v48 offset:4096\n" \
"ds_read_b128 a[132:135], v48 offset:1024\n" \
"v_mfma_f32_16x16x32_bf16 v[224:227], a[160:163], v[88:91], v[224:227]\n" \
"ds_read_b128 a[148:151], v48 offset:5120\n" \
"ds_read_b128 a[136:139], v48 offset:2048\n" \
"v_mfma_f32_16x16x32_bf16 a[196:199], a[212:215], v[120:123], a[196:199]\n" \
"ds_read_b128 a[152:155], v48 offset:6144\n" \
"ds_read_b128 a[140:143], v48 offset:3072\n" \
"v_mfma_f32_16x16x32_bf16 v[224:227], a[0:3], v[120:123], v[224:227]\n" \
"ds_read_b128 a[156:159], v48 offset:7168\n" \
"s_waitcnt lgkmcnt(8)\n" \
"v_mfma_f32_16x16x32_bf16 v[228:231], a[4:7], v[120:123], v[228:231]\n" \
"v_accvgpr_read_b32 v55, a194\n" \
"v_accvgpr_read_b32 v23, a195\n" \
"v_mfma_f32_16x16x32_bf16 a[192:195], a[216:219], a[216:219], 0\n" \
"v_add_f32_e32 v55, v55, v23\n" \
"v_max_f32_e32 v62, v62, v55\n" \
"v_mfma_f32_16x16x32_bf16 v[232:235], a[8:11], v[120:123], v[232:235]\n" \
"v_rcp_f32_e32 v55, v55\n" \
"v_mfma_f32_16x16x32_bf16 v[236:239], a[12:15], v[120:123], v[236:239]\n" \
"v_mul_f32_e32 v55, v55, v59\n" \
"v_mul_f32_e32 v160, v55, v160\n" \
"v_mfma_f32_16x16x32_bf16 v[240:243], a[16:19], v[120:123], v[240:243]\n" \
"v_mul_f32_e32 v161, v55, v161\n" \
"v_mul_f32_e32 v168, v55, v168\n" \
"v_mfma_f32_16x16x32_bf16 v[244:247], a[20:23], v[120:123], v[244:247]\n" \
"v_mul_f32_e32 v169, v55, v169\n" \
"v_mul_f32_e32 v176, v55, v176\n" \
"v_mfma_f32_16x16x32_bf16 v[248:251], a[24:27], v[120:123], v[248:251]\n" \
"v_mul_f32_e32 v177, v55, v177\n" \
"v_mul_f32_e32 v184, v55, v184\n" \
"v_mfma_f32_16x16x32_bf16 v[252:255], a[28:31], v[120:123], v[252:255]\n" \
"v_mul_f32_e32 v185, v55, v185\n" \
"v_cvt_pk_bf16_f32 v160, v160, v161\n" \
"v_mul_f32_e32 v162, v55, v162\n" \
"v_mul_f32_e32 v163, v55, v163\n" \
"v_cvt_pk_bf16_f32 v168, v168, v169\n" \
"v_mul_f32_e32 v170, v55, v170\n" \
"v_mul_f32_e32 v171, v55, v171\n" \
"v_cvt_pk_bf16_f32 v176, v176, v177\n" \
"v_mul_f32_e32 v178, v55, v178\n" \
"s_waitcnt vmcnt(4)\n" \
"v_cmp_ne_u32_e32 vcc, s62, v26\n" \
"v_mul_f32_e32 v179, v55, v179\n" \
"v_cvt_pk_bf16_f32 v184, v184, v185\n" \
"v_mul_f32_e32 v186, v55, v186\n" \
"v_mul_f32_e32 v187, v55, v187\n" \
"v_cvt_pk_bf16_f32 v161, v162, v163\n" \
"s_cbranch_vccnz L_sos_%=\n" \
"L_ros_%=:\n" \
"buffer_load_dwordx4 a[220:223], v20, s[56:59], s97 offen offset:0 sc1\n" \
"v_mul_f32_e32 v164, v55, v164\n" \
"v_mul_f32_e32 v165, v55, v165\n" \
"v_mul_f32_e32 v166, v55, v166\n" \
"v_mul_f32_e32 v167, v55, v167\n" \
"v_cvt_pk_bf16_f32 v169, v170, v171\n" \
"v_mul_f32_e32 v172, v55, v172\n" \
"v_mul_f32_e32 v173, v55, v173\n" \
"v_mul_f32_e32 v174, v55, v174\n" \
"v_mul_f32_e32 v175, v55, v175\n" \
"v_cvt_pk_bf16_f32 v177, v178, v179\n" \
"v_mul_f32_e32 v180, v55, v180\n" \
"buffer_load_dwordx4 a[228:231], v17, s[56:59], s97 offen offset:0 sc1\n" \
"v_mul_f32_e32 v181, v55, v181\n" \
"v_mul_f32_e32 v182, v55, v182\n" \
"v_mul_f32_e32 v183, v55, v183\n" \
"v_cvt_pk_bf16_f32 v185, v186, v187\n" \
"v_mul_f32_e32 v188, v55, v188\n" \
"v_mul_f32_e32 v189, v55, v189\n" \
"v_mul_f32_e32 v190, v55, v190\n" \
"v_mul_f32_e32 v191, v55, v191\n" \
"v_cvt_pk_bf16_f32 v162, v164, v165\n" \
"v_cvt_pk_bf16_f32 v163, v166, v167\n" \
"v_cvt_pk_bf16_f32 v170, v172, v173\n" \
"buffer_store_dwordx4 v[160:163], v20, s[56:59], s69 offen offset:0 sc1\n" \
"v_cvt_pk_bf16_f32 v171, v174, v175\n" \
"v_cvt_pk_bf16_f32 v178, v180, v181\n" \
"v_cvt_pk_bf16_f32 v179, v182, v183\n" \
"v_cvt_pk_bf16_f32 v186, v188, v189\n" \
"v_cvt_pk_bf16_f32 v187, v190, v191\n" \
"v_readlane_b32 s93, v11, 20\n" \
"v_readlane_b32 s84, v11, 15\n" \
"v_readlane_b32 s85, v11, 16\n" \
"v_readlane_b32 s86, v11, 17\n" \
"s_mov_b32 s87, 0x20000\n" \
"v_readlane_b32 s92, v11, 18\n" \
"buffer_store_dwordx4 v[168:171], v20, s[56:59], s69 offen offset:1024 sc1\n" \
"v_readlane_b32 s91, v11, 21\n" \
"v_mfma_f32_16x16x32_bf16 a[208:211], a[216:219], v[128:131], a[208:211]\n" \
"v_mfma_f32_16x16x32_bf16 a[208:211], a[216:219], v[136:139], a[208:211]\n" \
"v_mfma_f32_32x32x16_bf16 v[128:143], a[216:219], a[216:219], 0\n" \
"buffer_store_dwordx4 v[176:179], v20, s[56:59], s69 offen offset:2048 sc1\n" \
"v_mfma_f32_16x16x32_bf16 a[208:211], a[216:219], v[144:147], a[208:211]\n" \
"v_mfma_f32_16x16x32_bf16 a[208:211], a[216:219], v[152:155], a[208:211]\n" \
"v_mfma_f32_32x32x16_bf16 v[144:159], a[216:219], a[216:219], 0\n" \
"buffer_store_dwordx4 v[184:187], v20, s[56:59], s69 offen offset:3072 sc1\n" \
"v_add_f32_e32 v56, v56, v24\n" \
"v_mfma_f32_16x16x32_bf16 a[208:211], a[216:219], v[160:163], a[208:211]\n" \
"v_max_f32_e32 v62, v62, v56\n" \
"v_rcp_f32_e32 v56, v56\n" \
"v_mfma_f32_16x16x32_bf16 a[208:211], a[216:219], v[168:171], a[208:211]\n" \
"v_mfma_f32_32x32x16_bf16 v[160:175], a[216:219], a[216:219], 0\n" \
"buffer_load_dwordx4 a[236:239], v20, s[56:59], s98 offen offset:0 sc1\n" \
"v_mul_f32_e32 v56, v56, v60\n" \
"v_mfma_f32_16x16x32_bf16 a[208:211], a[216:219], v[176:179], a[208:211]\n" \
"v_mul_f32_e32 v192, v56, v192\n" \
"v_mul_f32_e32 v193, v56, v193\n" \
"v_cvt_pk_bf16_f32 v192, v192, v193\n" \
"v_mul_f32_e32 v194, v56, v194\n" \
"v_mul_f32_e32 v195, v56, v195\n" \
"buffer_load_dwordx4 a[244:247], v17, s[56:59], s98 offen offset:0 sc1\n" \
"v_mfma_f32_16x16x32_bf16 a[208:211], a[216:219], v[184:187], a[208:211]\n" \
"v_mfma_f32_32x32x16_bf16 v[176:191], a[216:219], a[216:219], 0\n" \
"v_cvt_pk_bf16_f32 v193, v194, v195\n" \
"v_mul_f32_e32 v196, v56, v196\n" \
"v_mul_f32_e32 v197, v56, v197\n" \
"v_mul_f32_e32 v198, v56, v198\n" \
"v_mul_f32_e32 v199, v56, v199\n" \
"buffer_load_dwordx4 a[224:227], v20, s[56:59], s97 offen offset:1024 sc1\n" \
"v_mul_f32_e32 v200, v56, v200\n" \
"v_mul_f32_e32 v201, v56, v201\n" \
"v_accvgpr_read_b32 v57, a198\n" \
"v_accvgpr_read_b32 v25, a199\n" \
"v_mfma_f32_16x16x32_bf16 a[196:199], a[216:219], a[216:219], 0\n" \
"v_cvt_pk_bf16_f32 v194, v196, v197\n" \
"v_cvt_pk_bf16_f32 v195, v198, v199\n" \
"v_cvt_pk_bf16_f32 v200, v200, v201\n" \
"v_mul_f32_e32 v202, v56, v202\n" \
"v_mul_f32_e32 v203, v56, v203\n" \
"buffer_store_dwordx4 v[192:195], v20, s[56:59], s70 offen offset:0 sc1\n" \
"v_add_f32_e32 v57, v57, v25\n" \
"v_mfma_f32_16x16x32_bf16 a[208:211], a[216:219], v[192:195], a[208:211]\n" \
"v_cvt_pk_bf16_f32 v201, v202, v203\n" \
"v_mul_f32_e32 v204, v56, v204\n" \
"v_mul_f32_e32 v205, v56, v205\n" \
"v_mul_f32_e32 v206, v56, v206\n" \
"v_mul_f32_e32 v207, v56, v207\n" \
"v_mul_f32_e32 v208, v56, v208\n" \
"v_mul_f32_e32 v209, v56, v209\n" \
"v_max_f32_e32 v62, v62, v57\n" \
"buffer_load_dwordx4 a[232:235], v17, s[56:59], s97 offen offset:1024 sc1\n" \
"s_waitcnt vmcnt(16) lgkmcnt(0)\n" \
"s_barrier\n" \
"v_mfma_f32_16x16x32_bf16 v[64:67], a[128:131], a[64:67], v[32:35]\n" \
"s_add_u32 m0, s93, 0x0\n" \
"v_cvt_pk_bf16_f32 v202, v204, v205\n" \
"v_mfma_f32_16x16x32_bf16 v[64:67], a[132:135], a[68:71], v[64:67]\n" \
"buffer_load_dwordx4 a[240:243], v20, s[56:59], s98 offen offset:1024 sc1\n" \
"v_mfma_f32_16x16x32_bf16 v[64:67], a[136:139], a[72:75], v[64:67]\n" \
"v_cvt_pk_bf16_f32 v203, v206, v207\n" \
"v_cvt_pk_bf16_f32 v208, v208, v209\n" \
"v_mfma_f32_16x16x32_bf16 v[64:67], a[140:143], a[76:79], v[64:67]\n" \
"v_mul_f32_e32 v210, v56, v210\n" \
"v_mul_f32_e32 v211, v56, v211\n" \
"v_mfma_f32_16x16x32_bf16 v[68:71], a[156:159], a[76:79], v[32:35]\n" \
"v_rcp_f32_e32 v57, v57\n" \
"buffer_store_dwordx4 v[200:203], v20, s[56:59], s70 offen offset:1024 sc1\n" \
"v_mfma_f32_16x16x32_bf16 v[68:71], a[152:155], a[72:75], v[68:71]\n" \
"v_mfma_f32_16x16x32_bf16 a[208:211], a[216:219], v[200:203], a[208:211]\n" \
"v_mfma_f32_32x32x16_bf16 v[192:207], a[216:219], a[216:219], 0\n" \
"v_cvt_pk_bf16_f32 v209, v210, v211\n" \
"buffer_load_dwordx4 a[248:251], v17, s[56:59], s98 offen offset:1024 sc1\n" \
"v_mul_f32_e32 v212, v56, v212\n" \
"v_mul_f32_e32 v213, v56, v213\n" \
"v_mul_f32_e32 v214, v56, v214\n" \
"v_mfma_f32_16x16x32_bf16 v[68:71], a[148:151], a[68:71], v[68:71]\n" \
"v_mul_f32_e32 v215, v56, v215\n" \
"v_mul_f32_e32 v216, v56, v216\n" \
"v_mfma_f32_16x16x32_bf16 v[68:71], a[144:147], a[64:67], v[68:71]\n" \
"v_mul_f32_e32 v217, v56, v217\n" \
"v_cvt_pk_bf16_f32 v210, v212, v213\n" \
"buffer_load_dwordx4 v0, s[84:87], s92 offen offset:0 lds\n" \
"v_mfma_f32_16x16x32_bf16 v[76:79], a[144:147], a[80:83], v[36:39]\n" \
"ds_read_b128 a[144:147], v48 offset:12288\n" \
"v_cvt_pk_bf16_f32 v211, v214, v215\n" \
"v_mfma_f32_16x16x32_bf16 v[76:79], a[148:151], a[84:87], v[76:79]\n" \
"ds_read_b128 a[148:151], v48 offset:13312\n" \
"v_cvt_pk_bf16_f32 v216, v216, v217\n" \
"v_mfma_f32_16x16x32_bf16 v[76:79], a[152:155], a[88:91], v[76:79]\n" \
"ds_read_b128 a[152:155], v48 offset:14336\n" \
"v_mul_f32_e32 v218, v56, v218\n" \
"buffer_store_dwordx4 v[208:211], v20, s[56:59], s70 offen offset:2048 sc1\n" \
"v_mfma_f32_16x16x32_bf16 v[76:79], a[156:159], a[92:95], v[76:79]\n" \
"ds_read_b128 a[156:159], v48 offset:15360\n" \
"v_mul_f32_e32 v219, v56, v219\n" \
"v_mfma_f32_16x16x32_bf16 v[72:75], a[140:143], a[92:95], v[36:39]\n" \
"ds_read_b128 a[140:143], v48 offset:11264\n" \
"v_mul_f32_e32 v57, v57, v61\n" \
"v_mfma_f32_16x16x32_bf16 v[72:75], a[136:139], a[88:91], v[72:75]\n" \
".fill (L_ab + 28672 - .) / 4, 4, 0xBF800000\n" \
"L_ap7:\n" \
"s_cbranch_execz L_ap0\n" \
"ds_read_b128 a[136:139], v48 offset:10240\n" \
"v_cvt_pk_bf16_f32 v217, v218, v219\n" \
"buffer_load_dwordx4 v1, s[84:87], s92 offen offset:1024 lds\n" \
"v_mfma_f32_16x16x32_bf16 v[72:75], a[132:135], a[84:87], v[72:75]\n" \
"ds_read_b128 a[132:135], v48 offset:9216\n" \
"v_mul_f32_e32 v220, v56, v220\n" \
"v_mfma_f32_16x16x32_bf16 v[72:75], a[128:131], a[80:83], v[72:75]\n" \
"ds_read_b128 a[128:131], v48 offset:8192\n" \
"v_mfma_f32_16x16x32_bf16 a[208:211], a[216:219], v[208:211], a[208:211]\n" \
"v_mul_f32_e32 v221, v56, v221\n" \
"buffer_load_dwordx4 v2, s[84:87], s92 offen offset:2048 lds\n" \
"v_mul_f32_e32 v222, v56, v222\n" \
"v_mul_f32_e32 v223, v56, v223\n" \
"v_mul_f32_e32 v224, v57, v224\n" \
"v_mul_f32_e32 v225, v57, v225\n" \
"v_cvt_pk_bf16_f32 v218, v220, v221\n" \
"v_cvt_pk_bf16_f32 v219, v222, v223\n" \
"v_cvt_pk_bf16_f32 v224, v224, v225\n" \
"v_mul_f32_e32 v226, v57, v226\n" \
"v_mul_f32_e32 v227, v57, v227\n" \
"v_mfma_f32_16x16x32_bf16 a[208:211], a[216:219], v[216:219], a[208:211]\n" \
"buffer_store_dwordx4 v[216:219], v20, s[56:59], s70 offen offset:3072 sc1\n" \
"v_cvt_pk_bf16_f32 v225, v226, v227\n" \
"v_mul_f32_e32 v228, v57, v228\n" \
"v_mfma_f32_32x32x16_bf16 v[208:223], a[216:219], a[216:219], 0\n" \
"v_mul_f32_e32 v229, v57, v229\n" \
"v_mul_f32_e32 v230, v57, v230\n" \
"v_mul_f32_e32 v231, v57, v231\n" \
"v_mul_f32_e32 v232, v57, v232\n" \
"v_mul_f32_e32 v233, v57, v233\n" \
"v_cvt_pk_bf16_f32 v226, v228, v229\n" \
"v_cvt_pk_bf16_f32 v227, v230, v231\n" \
"buffer_store_dwordx4 v[224:227], v20, s[56:59], s71 offen offset:0 sc1\n" \
"v_cvt_pk_bf16_f32 v232, v232, v233\n" \
"v_mul_f32_e32 v234, v57, v234\n" \
"v_mul_f32_e32 v235, v57, v235\n" \
"v_mfma_f32_16x16x32_bf16 a[208:211], a[216:219], v[224:227], a[208:211]\n" \
"v_cvt_pk_bf16_f32 v233, v234, v235\n" \
"v_mul_f32_e32 v236, v57, v236\n" \
"v_mul_f32_e32 v237, v57, v237\n" \
"v_mul_f32_e32 v238, v57, v238\n" \
"v_mul_f32_e32 v239, v57, v239\n" \
"v_mul_f32_e32 v240, v57, v240\n" \
"buffer_load_dwordx4 v3, s[84:87], s92 offen offset:3072 lds\n" \
"s_add_u32 m0, s91, 0x1000\n" \
"v_mul_f32_e32 v241, v57, v241\n" \
"v_cvt_pk_bf16_f32 v234, v236, v237\n" \
"v_cvt_pk_bf16_f32 v235, v238, v239\n" \
"v_cvt_pk_bf16_f32 v240, v240, v241\n" \
"v_mul_f32_e32 v242, v57, v242\n" \
"v_mul_f32_e32 v243, v57, v243\n" \
"v_mfma_f32_16x16x32_bf16 a[208:211], a[216:219], v[232:235], a[208:211]\n" \
"v_cvt_pk_bf16_f32 v241, v242, v243\n" \
"v_mul_f32_e32 v244, v57, v244\n" \
"buffer_store_dwordx4 v[232:235], v20, s[56:59], s71 offen offset:1024 sc1\n" \
"v_mul_f32_e32 v245, v57, v245\n" \
"v_mul_f32_e32 v246, v57, v246\n" \
"v_mfma_f32_32x32x16_bf16 v[224:239], a[216:219], a[216:219], 0\n" \
"v_mul_f32_e32 v247, v57, v247\n" \
"v_mul_f32_e32 v248, v57, v248\n" \
"v_mul_f32_e32 v249, v57, v249\n" \
"v_cvt_pk_bf16_f32 v242, v244, v245\n" \
"v_cvt_pk_bf16_f32 v243, v246, v247\n" \
"v_cvt_pk_bf16_f32 v248, v248, v249\n" \
"v_mul_f32_e32 v250, v57, v250\n" \
"buffer_store_dwordx4 v[240:243], v20, s[56:59], s71 offen offset:2048 sc1\n" \
"v_mul_f32_e32 v251, v57, v251\n" \
"v_mfma_f32_16x16x32_bf16 a[208:211], a[216:219], v[240:243], a[208:211]\n" \
"v_cvt_pk_bf16_f32 v249, v250, v251\n" \
"v_mul_f32_e32 v252, v57, v252\n" \
"v_mul_f32_e32 v253, v57, v253\n" \
"v_mul_f32_e32 v254, v57, v254\n" \
"v_mul_f32_e32 v255, v57, v255\n" \
"v_cvt_pk_bf16_f32 v250, v252, v253\n" \
"v_cvt_pk_bf16_f32 v251, v254, v255\n" \
"v_add_u32_e32 v53, 0x20000, v63\n" \
"buffer_store_dwordx4 v[248:251], v20, s[56:59], s71 offen offset:3072 sc1\n" \
"v_mfma_f32_16x16x32_bf16 a[208:211], a[216:219], v[248:251], a[208:211]\n" \
"v_exp_f32_e32 v70, v70\n" \
"v_mfma_f32_32x32x16_bf16 v[240:255], a[216:219], a[216:219], 0\n" \
"v_exp_f32_e32 v71, v71\n" \
"v_exp_f32_e32 v72, v72\n" \
"v_exp_f32_e32 v73, v73\n" \
"buffer_load_dwordx4 v4, s[84:87], s92 offen offset:0 lds\n" \
"s_add_u32 s64, s64, 0x10000\n" \
"v_cmp_lt_f32_e64 s[72:73], s21, v62\n" \
"v_accvgpr_read_b32 v63, a208\n" \
"v_cmp_u_f32_e32 vcc, v63, v63\n" \
"v_readlane_b32 s95, v11, 3\n" \
"v_exp_f32_e32 v68, v68\n" \
"v_exp_f32_e32 v69, v69\n" \
"v_exp_f32_e32 v74, v74\n" \
"buffer_load_dwordx4 v5, s[84:87], s92 offen offset:1024 lds\n" \
"v_exp_f32_e32 v75, v75\n" \
"s_or_b64 vcc, s[72:73], vcc\n" \
"s_cselect_b32 s95, 0x40000000, s95\n" \
"s_cselect_b32 s94, 0x7f800000, 0\n" \
"v_cvt_pk_bf16_f32 v71, v70, v71\n" \
"v_exp_f32_e32 v66, v66\n" \
"v_exp_f32_e32 v67, v67\n" \
"v_exp_f32_e32 v64, v64\n" \
"buffer_load_dwordx4 v6, s[84:87], s92 offen offset:2048 lds\n" \
"v_exp_f32_e32 v65, v65\n" \
"v_cvt_pk_bf16_f32 v72, v72, v73\n" \
"v_exp_f32_e32 v76, v76\n" \
"v_exp_f32_e32 v77, v77\n" \
"v_exp_f32_e32 v78, v78\n" \
"v_exp_f32_e32 v79, v79\n" \
"buffer_load_dwordx4 v7, s[84:87], s92 offen offset:3072 lds\n" \
"s_add_u32 m0, s91, 0x2000\n" \
"v_cvt_pk_bf16_f32 v70, v68, v69\n" \
"v_cvt_pk_bf16_f32 v73, v74, v75\n" \
"v_add_u32_e32 v49, 0x10000, v48\n" \
"s_xor_b32 s65, s65, 0x8000\n" \
"v_cvt_pk_bf16_f32 v69, v66, v67\n" \
"v_cvt_pk_bf16_f32 v68, v64, v65\n" \
"v_cvt_pk_bf16_f32 v74, v76, v77\n" \
"v_cvt_pk_bf16_f32 v75, v78, v79\n" \
"v_xor_b32_e32 v48, 0x8000, v48\n" \
"s_sub_u32 s66, s19, 2\n" \
"buffer_load_dwordx4 v8, s[84:87], s92 offen offset:0 lds\n" \
"s_waitcnt vmcnt(18) lgkmcnt(0)\n" \
"s_barrier\n" \
"s_branch L_f1_%=\n" \
".fill (L_ab + 32768 - .) / 4, 4, 0xBF800000\n" \
".fill (L_ab + 32768 - .) / 4, 4, 0xBF800000\n" \
"L_ap8:\n" \
"s_setpc_b64 s[88:89]\n" \
"L_out_%=:\n" \
"v_bfe_u32 v38, v20, 2, 4\n" \
"v_readlane_b32 s64, v11, 2\n" \
"v_add_u32_e32 v32, 0x49000, v20\n" \
"v_add_u32_e32 v34, 0x20000, v10\n" \
"ds_read_b64_tr_b16 a[0:1], v34 offset:0\n" \
"ds_read_b64_tr_b16 a[2:3], v34 offset:4608\n" \
"ds_read_b64_tr_b16 a[4:5], v34 offset:9216\n" \
"buffer_load_dword v26, v38, s[56:59], s64 offen sc1\n" \
"ds_read_b64_tr_b16 a[6:7], v34 offset:13824\n" \
"ds_read_b64_tr_b16 a[8:9], v34 offset:32\n" \
"ds_read_b64_tr_b16 a[10:11], v34 offset:4640\n" \
"ds_read_b64_tr_b16 a[12:13], v34 offset:9248\n" \
"buffer_store_dwordx4 v[64:67], v20, s[56:59], s61 offen offset:0 sc1\n" \
"ds_read_b64_tr_b16 a[14:15], v34 offset:13856\n" \
"v_mov_b32_e32 v39, s62\n" \
"v_readlane_b32 s70, v11, 5\n" \
"v_readlane_b32 s71, v11, 4\n" \
"buffer_store_dwordx4 v[68:71], v20, s[56:59], s61 offen offset:1024 sc1\n" \
"ds_read_b64_tr_b16 a[16:17], v34 offset:64\n" \
"ds_read_b64_tr_b16 a[18:19], v34 offset:4672\n" \
"ds_read_b64_tr_b16 a[20:21], v34 offset:9280\n" \
"ds_read_b64_tr_b16 a[22:23], v34 offset:13888\n" \
"buffer_store_dwordx4 v[72:75], v20, s[56:59], s61 offen offset:2048 sc1\n" \
"ds_read_b64_tr_b16 a[24:25], v34 offset:96\n" \
"ds_read_b64_tr_b16 a[26:27], v34 offset:4704\n" \
"ds_read_b64_tr_b16 a[28:29], v34 offset:9312\n" \
"ds_read_b64_tr_b16 a[30:31], v34 offset:13920\n" \
"buffer_store_dwordx4 v[76:79], v20, s[56:59], s61 offen offset:3072 sc1\n" \
"ds_read_b64_tr_b16 a[192:193], v34 offset:128\n" \
"ds_read_b64_tr_b16 a[194:195], v34 offset:4736\n" \
"ds_read_b64_tr_b16 a[196:197], v34 offset:9344\n" \
"ds_read_b64_tr_b16 a[198:199], v34 offset:13952\n" \
"buffer_store_dwordx4 v[80:83], v32, s[56:59], s61 offen offset:0 sc1\n" \
"ds_read_b64_tr_b16 a[200:201], v34 offset:160\n" \
"ds_read_b64_tr_b16 a[202:203], v34 offset:4768\n" \
"ds_read_b64_tr_b16 a[204:205], v34 offset:9376\n" \
"ds_read_b64_tr_b16 a[206:207], v34 offset:13984\n" \
"buffer_store_dwordx4 v[84:87], v32, s[56:59], s61 offen offset:1024 sc1\n" \
"ds_read_b64_tr_b16 a[208:209], v34 offset:192\n" \
"s_waitcnt vmcnt(16) lgkmcnt(15)\n" \
"v_mfma_f32_16x16x32_bf16 v[112:115], a[0:3], a[96:99], 0\n" \
"buffer_store_dwordx4 v[96:99], v32, s[56:59], s61 offen offset:2048 sc1\n" \
"v_mfma_f32_16x16x32_bf16 v[112:115], a[4:7], a[100:103], v[112:115]\n" \
"ds_read_b64_tr_b16 a[210:211], v34 offset:4800\n" \
"ds_read_b64_tr_b16 a[212:213], v34 offset:9408\n" \
"buffer_store_dwordx4 v[100:103], v32, s[56:59], s61 offen offset:3072 sc1\n" \
"v_mfma_f32_16x16x32_bf16 v[116:119], a[0:3], a[112:115], 0\n" \
"ds_read_b64_tr_b16 a[214:215], v34 offset:14016\n" \
"ds_read_b64_tr_b16 a[216:217], v34 offset:224\n" \
"v_mfma_f32_16x16x32_bf16 v[116:119], a[4:7], a[116:119], v[116:119]\n" \
"ds_read_b64_tr_b16 a[218:219], v34 offset:4832\n" \
"v_lshrrev_b32_e32 v44, 9, v20\n" \
"v_mfma_f32_16x16x32_bf16 v[184:187], a[0:3], v[64:67], 0\n" \
"v_mul_u32_u24_e32 v44, 0xb40, v44\n" \
"v_add_u32_e32 v35, v44, v10\n" \
"v_mfma_f32_16x16x32_bf16 v[184:187], a[4:7], v[68:71], v[184:187]\n" \
"v_readlane_b32 s65, v11, 30\n" \
"v_readlane_b32 s67, v11, 23\n" \
"v_mfma_f32_16x16x32_bf16 v[188:191], a[0:3], v[80:83], 0\n" \
"ds_read_b64_tr_b16 a[0:1], v34 offset:9440\n" \
"ds_read_b64_tr_b16 a[2:3], v34 offset:14048\n" \
"v_mfma_f32_16x16x32_bf16 v[188:191], a[4:7], v[84:87], v[188:191]\n" \
"ds_read_b64_tr_b16 a[4:5], v34 offset:256\n" \
"ds_read_b64_tr_b16 a[6:7], v34 offset:4864\n" \
"v_mfma_f32_16x16x32_bf16 v[196:199], a[8:11], v[80:83], 0\n" \
"v_readlane_b32 s80, v11, 11\n" \
"v_readlane_b32 s81, v11, 12\n" \
"v_mfma_f32_16x16x32_bf16 v[196:199], a[12:15], v[84:87], v[196:199]\n" \
"s_mov_b32 s82, 0x47fca0\n" \
"s_mov_b32 s83, 0x20000\n" \
"v_mfma_f32_16x16x32_bf16 v[192:195], a[8:11], v[64:67], 0\n" \
"v_readlane_b32 s66, v11, 28\n" \
"s_add_u32 m0, s67, 0x0\n" \
"v_mfma_f32_16x16x32_bf16 v[192:195], a[12:15], v[68:71], v[192:195]\n" \
"v_readlane_b32 s92, v11, 24\n" \
"v_readlane_b32 s73, v11, 29\n" \
"v_mfma_f32_16x16x32_bf16 v[124:127], a[8:11], a[112:115], 0\n" \
"buffer_load_dwordx4 v0, s[80:83], s66 offen offset:0 lds\n" \
"v_mfma_f32_16x16x32_bf16 v[124:127], a[12:15], a[116:119], v[124:127]\n" \
"v_readlane_b32 s94, v11, 25\n" \
"v_readlane_b32 s84, v11, 9\n" \
"v_mfma_f32_16x16x32_bf16 v[120:123], a[8:11], a[96:99], 0\n" \
"ds_read_b64_tr_b16 a[8:9], v34 offset:9472\n" \
"ds_read_b64_tr_b16 a[10:11], v34 offset:14080\n" \
"v_mfma_f32_16x16x32_bf16 v[120:123], a[12:15], a[100:103], v[120:123]\n" \
"ds_read_b64_tr_b16 a[12:13], v34 offset:18432\n" \
"ds_read_b64_tr_b16 a[14:15], v34 offset:23040\n" \
"v_mfma_f32_16x16x32_bf16 v[128:131], a[16:19], a[96:99], 0\n" \
"v_readlane_b32 s85, v11, 10\n" \
"s_waitcnt lgkmcnt(9)\n" \
"v_mfma_f32_16x16x32_bf16 v[128:131], a[20:23], a[100:103], v[128:131]\n" \
"buffer_load_dwordx4 v1, s[80:83], s66 offen offset:1024 lds\n" \
"v_mfma_f32_16x16x32_bf16 v[132:135], a[16:19], a[112:115], 0\n" \
"s_mov_b32 s86, 0xa1ca0\n" \
"s_mov_b32 s87, 0x20000\n" \
"v_mfma_f32_16x16x32_bf16 v[132:135], a[20:23], a[116:119], v[132:135]\n" \
"v_add_u32_e32 v33, 0x10000, v10\n" \
"v_readlane_b32 s68, v11, 31\n" \
"v_mfma_f32_16x16x32_bf16 v[200:203], a[16:19], v[64:67], 0\n" \
"v_readlane_b32 s69, v11, 32\n" \
"v_readlane_b32 s72, v11, 34\n" \
"v_mfma_f32_16x16x32_bf16 v[200:203], a[20:23], v[68:71], v[200:203]\n" \
"v_readlane_b32 s93, v11, 35\n" \
"v_readlane_b32 s63, v11, 41\n" \
"v_mfma_f32_16x16x32_bf16 v[204:207], a[16:19], v[80:83], 0\n" \
"ds_read_b64_tr_b16 a[16:17], v34 offset:18464\n" \
"ds_read_b64_tr_b16 a[18:19], v34 offset:23072\n" \
"v_mfma_f32_16x16x32_bf16 v[204:207], a[20:23], v[84:87], v[204:207]\n" \
"ds_read_b64_tr_b16 a[20:21], v34 offset:18496\n" \
"ds_read_b64_tr_b16 a[22:23], v34 offset:23104\n" \
"v_mfma_f32_16x16x32_bf16 v[212:215], a[24:27], v[80:83], 0\n" \
"buffer_load_dwordx4 v2, s[80:83], s66 offen offset:2048 lds\n" \
"v_mfma_f32_16x16x32_bf16 v[212:215], a[28:31], v[84:87], v[212:215]\n" \
"v_bfe_u32 v40, v20, 2, 7\n" \
"s_add_u32 s74, s63, 0x3800\n" \
"v_mfma_f32_16x16x32_bf16 v[208:211], a[24:27], v[64:67], 0\n" \
"v_lshlrev_b32_e32 v41, 12, v20\n" \
"s_add_u32 s12, s63, 0x0\n" \
"v_mfma_f32_16x16x32_bf16 v[208:211], a[28:31], v[68:71], v[208:211]\n" \
"v_readlane_b32 s95, v11, 38\n" \
"v_readlane_b32 s96, v11, 39\n" \
"v_mfma_f32_16x16x32_bf16 v[140:143], a[24:27], a[112:115], 0\n" \
"s_add_u32 s13, s63, 0x1000\n" \
"v_readlane_b32 s97, v11, 40\n" \
"v_mfma_f32_16x16x32_bf16 v[140:143], a[28:31], a[116:119], v[140:143]\n" \
"buffer_load_dwordx4 v3, s[80:83], s66 offen offset:3072 lds\n" \
"v_mfma_f32_16x16x32_bf16 v[136:139], a[24:27], a[96:99], 0\n" \
"ds_read_b64_tr_b16 a[24:25], v34 offset:18528\n" \
"ds_read_b64_tr_b16 a[26:27], v34 offset:23136\n" \
"v_mfma_f32_16x16x32_bf16 v[136:139], a[28:31], a[100:103], v[136:139]\n" \
"ds_read_b64_tr_b16 a[28:29], v34 offset:18560\n" \
"ds_read_b64_tr_b16 a[30:31], v34 offset:23168\n" \
"v_mfma_f32_16x16x32_bf16 v[144:147], a[192:195], a[96:99], 0\n" \
"s_add_u32 m0, s67, 0x1000\n" \
"s_add_u32 s14, s63, 0x2000\n" \
"v_mfma_f32_16x16x32_bf16 v[144:147], a[196:199], a[100:103], v[144:147]\n" \
"s_add_u32 s15, s63, 0x3000\n" \
"v_readlane_b32 s100, v11, 7\n" \
"v_mfma_f32_16x16x32_bf16 v[148:151], a[192:195], a[112:115], 0\n" \
"buffer_load_dwordx4 v4, s[80:83], s66 offen offset:0 lds\n" \
"v_mfma_f32_16x16x32_bf16 v[148:151], a[196:199], a[116:119], v[148:151]\n" \
"v_readlane_b32 s16, v11, 42\n" \
"v_bfe_u32 v44, v20, 4, 4\n" \
"v_mfma_f32_16x16x32_bf16 v[216:219], a[192:195], v[64:67], 0\n" \
"s_add_u32 s17, s16, 0x1000\n" \
"s_add_u32 s18, s16, 0x2000\n" \
"v_mfma_f32_16x16x32_bf16 v[216:219], a[196:199], v[68:71], v[216:219]\n" \
"s_add_u32 s19, s16, 0x3000\n" \
"v_bfe_u32 v45, v20, 8, 2\n" \
"v_mfma_f32_16x16x32_bf16 v[220:223], a[192:195], v[80:83], 0\n" \
"ds_read_b64_tr_b16 a[192:193], v34 offset:18592\n" \
"ds_read_b64_tr_b16 a[194:195], v34 offset:23200\n" \
"v_mfma_f32_16x16x32_bf16 v[220:223], a[196:199], v[84:87], v[220:223]\n" \
"ds_read_b64_tr_b16 a[196:197], v34 offset:18624\n" \
"ds_read_b64_tr_b16 a[198:199], v34 offset:23232\n" \
"v_mfma_f32_16x16x32_bf16 v[228:231], a[200:203], v[80:83], 0\n" \
"buffer_load_dwordx4 v5, s[80:83], s66 offen offset:1024 lds\n" \
"v_mfma_f32_16x16x32_bf16 v[228:231], a[204:207], v[84:87], v[228:231]\n" \
"v_mul_u32_u24_e32 v42, 0x480, v44\n" \
"v_lshl_add_u32 v42, v45, 3, v42\n" \
"v_mfma_f32_16x16x32_bf16 v[224:227], a[200:203], v[64:67], 0\n" \
"s_waitcnt vmcnt(14)\n" \
"v_cmp_ne_u32_e32 vcc, s62, v26\n" \
"v_mfma_f32_16x16x32_bf16 v[224:227], a[204:207], v[68:71], v[224:227]\n" \
"v_lshrrev_b32_e32 v44, 1, v44\n" \
"v_lshlrev_b32_e32 v45, 1, v45\n" \
"v_mfma_f32_16x16x32_bf16 v[156:159], a[200:203], a[112:115], 0\n" \
"v_sub_u32_e32 v44, v44, v45\n" \
"s_cbranch_vccnz L_sa_%=\n" \
"L_ra_%=:\n" \
"v_mfma_f32_16x16x32_bf16 v[156:159], a[204:207], a[116:119], v[156:159]\n" \
"buffer_load_dwordx4 a[64:67], v20, s[56:59], s65 offen offset:0 sc1\n" \
"v_mfma_f32_16x16x32_bf16 v[152:155], a[200:203], a[96:99], 0\n" \
"ds_read_b64_tr_b16 a[200:201], v34 offset:18656\n" \
"ds_read_b64_tr_b16 a[202:203], v34 offset:23264\n" \
"v_mfma_f32_16x16x32_bf16 v[152:155], a[204:207], a[100:103], v[152:155]\n" \
"ds_read_b64_tr_b16 a[204:205], v34 offset:18688\n" \
"ds_read_b64_tr_b16 a[206:207], v34 offset:23296\n" \
"v_mfma_f32_16x16x32_bf16 v[160:163], a[208:211], a[96:99], 0\n" \
"v_readlane_b32 s64, v11, 33\n" \
"v_and_b32_e32 v45, 16, v20\n" \
"v_mfma_f32_16x16x32_bf16 v[160:163], a[212:215], a[100:103], v[160:163]\n" \
"v_mov_b32_e32 v46, 0x3f80\n" \
"v_lshlrev_b32_e32 v45, v45, v46\n" \
"v_mfma_f32_16x16x32_bf16 v[164:167], a[208:211], a[112:115], 0\n" \
"buffer_load_dwordx4 a[68:71], v20, s[56:59], s65 offen offset:1024 sc1\n" \
"v_mfma_f32_16x16x32_bf16 v[164:167], a[212:215], a[116:119], v[164:167]\n" \
"v_mov_b32_e32 v52, 0\n" \
"v_mov_b32_e32 v53, 0\n" \
"v_mfma_f32_16x16x32_bf16 v[232:235], a[208:211], v[64:67], 0\n" \
"v_mov_b32_e32 v48, 0\n" \
"v_mov_b32_e32 v49, 0\n" \
"v_mfma_f32_16x16x32_bf16 v[232:235], a[212:215], v[68:71], v[232:235]\n" \
"v_add_u32_e32 v43, 0x9000, v42\n" \
"v_readlane_b32 s88, v11, 13\n" \
"v_mfma_f32_16x16x32_bf16 v[236:239], a[208:211], v[80:83], 0\n" \
"ds_read_b64_tr_b16 a[208:209], v10 offset:32768\n" \
"ds_read_b64_tr_b16 a[210:211], v10 offset:37376\n" \
"v_mfma_f32_16x16x32_bf16 v[236:239], a[212:215], v[84:87], v[236:239]\n" \
"ds_read_b64_tr_b16 a[212:213], v10 offset:32800\n" \
"s_waitcnt lgkmcnt(7)\n" \
"v_mfma_f32_16x16x32_bf16 v[244:247], a[216:219], v[80:83], 0\n" \
"ds_read_b64_tr_b16 a[214:215], v10 offset:37408\n" \
"v_readlane_b32 s89, v11, 14\n" \
"v_mfma_f32_16x16x32_bf16 v[244:247], a[0:3], v[84:87], v[244:247]\n" \
"buffer_load_dwordx4 a[80:83], v32, s[56:59], s65 offen offset:0 sc1\n" \
"v_mfma_f32_16x16x32_bf16 v[240:243], a[216:219], v[64:67], 0\n" \
"s_mov_b32 s90, 0x240000\n" \
"s_mov_b32 s91, 0x20000\n" \
"v_mfma_f32_16x16x32_bf16 v[240:243], a[0:3], v[68:71], v[240:243]\n" \
"v_mov_b32_e32 v46, s62\n" \
"v_mfma_f32_16x16x32_bf16 v[172:175], a[216:219], a[112:115], 0\n" \
"v_mfma_f32_16x16x32_bf16 v[172:175], a[0:3], a[116:119], v[172:175]\n" \
"v_mfma_f32_16x16x32_bf16 v[168:171], a[216:219], a[96:99], 0\n" \
"ds_read_b64_tr_b16 a[216:217], v10 offset:32832\n" \
"ds_read_b64_tr_b16 a[218:219], v10 offset:37440\n" \
"v_mfma_f32_16x16x32_bf16 v[168:171], a[0:3], a[100:103], v[168:171]\n" \
"ds_read_b64_tr_b16 a[0:1], v10 offset:32864\n" \
"ds_read_b64_tr_b16 a[2:3], v10 offset:37472\n" \
"v_mfma_f32_16x16x32_bf16 v[176:179], a[4:7], a[96:99], 0\n" \
"buffer_load_dwordx4 a[84:87], v32, s[56:59], s65 offen offset:1024 sc1\n" \
"v_mfma_f32_16x16x32_bf16 v[176:179], a[8:11], a[100:103], v[176:179]\n" \
"v_mfma_f32_16x16x32_bf16 v[180:183], a[4:7], a[112:115], 0\n" \
"v_mfma_f32_16x16x32_bf16 v[180:183], a[8:11], a[116:119], v[180:183]\n" \
"v_mfma_f32_16x16x32_bf16 v[248:251], a[4:7], v[64:67], 0\n" \
"v_mfma_f32_16x16x32_bf16 v[248:251], a[8:11], v[68:71], v[248:251]\n" \
"buffer_load_dwordx4 a[72:75], v20, s[56:59], s65 offen offset:2048 sc1\n" \
"v_mfma_f32_16x16x32_bf16 v[252:255], a[4:7], v[80:83], 0\n" \
"ds_read_b64_tr_b16 a[4:5], v10 offset:32896\n" \
"ds_read_b64_tr_b16 a[6:7], v10 offset:37504\n" \
"v_mfma_f32_16x16x32_bf16 v[252:255], a[8:11], v[84:87], v[252:255]\n" \
"ds_read_b64_tr_b16 a[8:9], v10 offset:32928\n" \
"ds_read_b64_tr_b16 a[10:11], v10 offset:37536\n" \
"v_mfma_f32_16x16x32_bf16 v[112:115], a[12:15], a[104:107], v[112:115]\n" \
"v_mfma_f32_16x16x32_bf16 v[116:119], a[12:15], a[120:123], v[116:119]\n" \
"v_mfma_f32_16x16x32_bf16 v[184:187], a[12:15], v[72:75], v[184:187]\n" \
"buffer_load_dwordx4 a[76:79], v20, s[56:59], s65 offen offset:3072 sc1\n" \
"v_mfma_f32_16x16x32_bf16 v[188:191], a[12:15], v[96:99], v[188:191]\n" \
"ds_read_b64_tr_b16 a[12:13], v10 offset:32960\n" \
"ds_read_b64_tr_b16 a[14:15], v10 offset:37568\n" \
"v_mfma_f32_16x16x32_bf16 v[196:199], a[16:19], v[96:99], v[196:199]\n" \
"v_mfma_f32_16x16x32_bf16 v[192:195], a[16:19], v[72:75], v[192:195]\n" \
"v_mfma_f32_16x16x32_bf16 v[124:127], a[16:19], a[120:123], v[124:127]\n" \
"v_mfma_f32_16x16x32_bf16 v[120:123], a[16:19], a[104:107], v[120:123]\n" \
"ds_read_b64_tr_b16 a[16:17], v10 offset:32992\n" \
"ds_read_b64_tr_b16 a[18:19], v10 offset:37600\n" \
"v_mfma_f32_16x16x32_bf16 v[128:131], a[20:23], a[104:107], v[128:131]\n" \
"buffer_load_dwordx4 a[88:91], v32, s[56:59], s65 offen offset:2048 sc1\n" \
"v_mfma_f32_16x16x32_bf16 v[132:135], a[20:23], a[120:123], v[132:135]\n" \
"v_mfma_f32_16x16x32_bf16 v[200:203], a[20:23], v[72:75], v[200:203]\n" \
"v_mfma_f32_16x16x32_bf16 v[204:207], a[20:23], v[96:99], v[204:207]\n" \
"ds_read_b64_tr_b16 a[20:21], v10 offset:33024\n" \
"ds_read_b64_tr_b16 a[22:23], v10 offset:37632\n" \
"s_waitcnt lgkmcnt(10)\n" \
"s_barrier\n" \
"v_mfma_f32_16x16x32_bf16 v[212:215], a[24:27], v[96:99], v[212:215]\n" \
"buffer_load_dwordx4 a[92:95], v32, s[56:59], s65 offen offset:3072 sc1\n" \
"v_mfma_f32_16x16x32_bf16 v[208:211], a[24:27], v[72:75], v[208:211]\n" \
"v_readlane_b32 s65, v11, 46\n" \
"v_mfma_f32_16x16x32_bf16 v[140:143], a[24:27], a[120:123], v[140:143]\n" \
"v_mfma_f32_16x16x32_bf16 v[136:139], a[24:27], a[104:107], v[136:139]\n" \
"ds_read_b64_tr_b16 a[24:25], v35 offset:41984\n" \
"ds_read_b64_tr_b16 a[26:27], v35 offset:44288\n" \
"v_mfma_f32_16x16x32_bf16 v[144:147], a[28:31], a[104:107], v[144:147]\n" \
"v_mfma_f32_16x16x32_bf16 v[148:151], a[28:31], a[120:123], v[148:151]\n" \
"buffer_load_dwordx4 v6, s[80:83], s66 offen offset:2048 lds\n" \
"v_mfma_f32_16x16x32_bf16 v[216:219], a[28:31], v[72:75], v[216:219]\n" \
"v_mfma_f32_16x16x32_bf16 v[220:223], a[28:31], v[96:99], v[220:223]\n" \
"ds_read_b64_tr_b16 a[28:29], v35 offset:52352\n" \
"ds_read_b64_tr_b16 a[30:31], v35 offset:54656\n" \
"v_mfma_f32_16x16x32_bf16 v[228:231], a[192:195], v[96:99], v[228:231]\n" \
"v_mfma_f32_16x16x32_bf16 v[224:227], a[192:195], v[72:75], v[224:227]\n" \
"v_mfma_f32_16x16x32_bf16 v[156:159], a[192:195], a[120:123], v[156:159]\n" \
"buffer_load_dwordx4 v7, s[80:83], s66 offen offset:3072 lds\n" \
"v_mfma_f32_16x16x32_bf16 v[152:155], a[192:195], a[104:107], v[152:155]\n" \
"ds_read_b64_tr_b16 a[192:193], v35 offset:42016\n" \
"ds_read_b64_tr_b16 a[194:195], v35 offset:44320\n" \
"v_mfma_f32_16x16x32_bf16 v[160:163], a[196:199], a[104:107], v[160:163]\n" \
"s_add_u32 m0, s67, 0x2000\n" \
"v_readlane_b32 s67, v11, 48\n" \
"v_mfma_f32_16x16x32_bf16 v[164:167], a[196:199], a[120:123], v[164:167]\n" \
"v_mfma_f32_16x16x32_bf16 v[232:235], a[196:199], v[72:75], v[232:235]\n" \
"v_mfma_f32_16x16x32_bf16 v[236:239], a[196:199], v[96:99], v[236:239]\n" \
"ds_read_b64_tr_b16 a[196:197], v35 offset:52384\n" \
"ds_read_b64_tr_b16 a[198:199], v35 offset:54688\n" \
"v_mfma_f32_16x16x32_bf16 v[244:247], a[200:203], v[96:99], v[244:247]\n" \
"buffer_load_dwordx4 v8, s[80:83], s66 offen offset:0 lds\n" \
"v_mfma_f32_16x16x32_bf16 v[240:243], a[200:203], v[72:75], v[240:243]\n" \
"s_add_u32 m0, s92, 0x0\n" \
"v_readlane_b32 s66, v11, 47\n" \
"v_mfma_f32_16x16x32_bf16 v[172:175], a[200:203], a[120:123], v[172:175]\n" \
"v_mfma_f32_16x16x32_bf16 v[168:171], a[200:203], a[104:107], v[168:171]\n" \
"ds_read_b64_tr_b16 a[200:201], v35 offset:42048\n" \
"ds_read_b64_tr_b16 a[202:203], v35 offset:44352\n" \
"v_mfma_f32_16x16x32_bf16 v[176:179], a[204:207], a[104:107], v[176:179]\n" \
"v_mfma_f32_16x16x32_bf16 v[180:183], a[204:207], a[120:123], v[180:183]\n" \
"buffer_load_dwordx4 v0, s[80:83], s73 offen offset:0 lds\n" \
"v_mfma_f32_16x16x32_bf16 v[248:251], a[204:207], v[72:75], v[248:251]\n" \
"v_mfma_f32_16x16x32_bf16 v[252:255], a[204:207], v[96:99], v[252:255]\n" \
"ds_read_b64_tr_b16 a[204:205], v35 offset:52416\n" \
"ds_read_b64_tr_b16 a[206:207], v35 offset:54720\n" \
"v_mfma_f32_16x16x32_bf16 v[112:115], a[208:211], a[108:111], v[112:115]\n" \
"v_mfma_f32_16x16x32_bf16 v[116:119], a[208:211], a[124:127], v[116:119]\n" \
"v_mfma_f32_16x16x32_bf16 v[184:187], a[208:211], v[76:79], v[184:187]\n" \
"buffer_load_dwordx4 v1, s[80:83], s73 offen offset:1024 lds\n" \
"v_mfma_f32_16x16x32_bf16 v[188:191], a[208:211], v[100:103], v[188:191]\n" \
"ds_read_b64_tr_b16 a[208:209], v35 offset:42080\n" \
"ds_read_b64_tr_b16 a[210:211], v35 offset:44384\n" \
"v_mfma_f32_16x16x32_bf16 v[196:199], a[212:215], v[100:103], v[196:199]\n" \
"v_mfma_f32_16x16x32_bf16 v[192:195], a[212:215], v[76:79], v[192:195]\n" \
"s_waitcnt vmcnt(18)\n" \
"s_mov_b64 exec, 1\n" \
"buffer_store_dword v39, off, s[56:59], s70 offset:0 sc1\n" \
"s_mov_b64 exec, -1\n" \
"v_mfma_f32_16x16x32_bf16 v[124:127], a[212:215], a[124:127], v[124:127]\n" \
"v_readlane_b32 s70, v11, 51\n" \
"v_mfma_f32_16x16x32_bf16 v[120:123], a[212:215], a[108:111], v[120:123]\n" \
"ds_read_b64_tr_b16 a[212:213], v35 offset:52448\n" \
"ds_read_b64_tr_b16 a[214:215], v35 offset:54752\n" \
"v_mfma_f32_16x16x32_bf16 v[128:131], a[216:219], a[108:111], v[128:131]\n" \
"v_mfma_f32_16x16x32_bf16 v[132:135], a[216:219], a[124:127], v[132:135]\n" \
"buffer_load_dwordx4 v2, s[80:83], s73 offen offset:2048 lds\n" \
"v_mfma_f32_16x16x32_bf16 v[200:203], a[216:219], v[76:79], v[200:203]\n" \
"v_mfma_f32_16x16x32_bf16 v[204:207], a[216:219], v[100:103], v[204:207]\n" \
"ds_read_b64_tr_b16 a[216:217], v35 offset:42112\n" \
"ds_read_b64_tr_b16 a[218:219], v35 offset:44416\n" \
"v_mfma_f32_16x16x32_bf16 v[212:215], a[0:3], v[100:103], v[212:215]\n" \
"v_mfma_f32_16x16x32_bf16 v[208:211], a[0:3], v[76:79], v[208:211]\n" \
"v_mfma_f32_16x16x32_bf16 v[140:143], a[0:3], a[124:127], v[140:143]\n" \
"buffer_load_dwordx4 v3, s[80:83], s73 offen offset:3072 lds\n" \
"v_mfma_f32_16x16x32_bf16 v[136:139], a[0:3], a[108:111], v[136:139]\n" \
"ds_read_b64_tr_b16 a[0:1], v35 offset:52480\n" \
"s_waitcnt lgkmcnt(7)\n" \
"v_mfma_f32_16x16x32_bf16 v[144:147], a[4:7], a[108:111], v[144:147]\n" \
"ds_read_b64_tr_b16 a[2:3], v35 offset:54784\n" \
"s_add_u32 m0, s92, 0x1000\n" \
"v_mfma_f32_16x16x32_bf16 v[148:151], a[4:7], a[124:127], v[148:151]\n" \
"v_mfma_f32_16x16x32_bf16 v[216:219], a[4:7], v[76:79], v[216:219]\n" \
"v_mfma_f32_16x16x32_bf16 v[220:223], a[4:7], v[100:103], v[220:223]\n" \
"ds_read_b64_tr_b16 a[4:5], v35 offset:42144\n" \
"ds_read_b64_tr_b16 a[6:7], v35 offset:44448\n" \
"v_mfma_f32_16x16x32_bf16 v[228:231], a[8:11], v[100:103], v[228:231]\n" \
"buffer_load_dwordx4 v4, s[80:83], s73 offen offset:0 lds\n" \
"v_mfma_f32_16x16x32_bf16 v[224:227], a[8:11], v[76:79], v[224:227]\n" \
"v_mfma_f32_16x16x32_bf16 v[156:159], a[8:11], a[124:127], v[156:159]\n" \
"v_mfma_f32_16x16x32_bf16 v[152:155], a[8:11], a[108:111], v[152:155]\n" \
"ds_read_b64_tr_b16 a[8:9], v35 offset:52512\n" \
"ds_read_b64_tr_b16 a[10:11], v35 offset:54816\n" \
"v_mfma_f32_16x16x32_bf16 v[160:163], a[12:15], a[108:111], v[160:163]\n" \
"v_mfma_f32_16x16x32_bf16 v[164:167], a[12:15], a[124:127], v[164:167]\n" \
"buffer_load_dwordx4 v5, s[80:83], s73 offen offset:1024 lds\n" \
"v_mfma_f32_16x16x32_bf16 v[232:235], a[12:15], v[76:79], v[232:235]\n" \
"v_mfma_f32_16x16x32_bf16 v[236:239], a[12:15], v[100:103], v[236:239]\n" \
"ds_read_b64_tr_b16 a[12:13], v35 offset:42176\n" \
"ds_read_b64_tr_b16 a[14:15], v35 offset:44480\n" \
"v_mfma_f32_16x16x32_bf16 v[244:247], a[16:19], v[100:103], v[244:247]\n" \
"v_mfma_f32_16x16x32_bf16 v[240:243], a[16:19], v[76:79], v[240:243]\n" \
"v_mfma_f32_16x16x32_bf16 v[172:175], a[16:19], a[124:127], v[172:175]\n" \
"buffer_load_dwordx4 v6, s[80:83], s73 offen offset:2048 lds\n" \
"v_mfma_f32_16x16x32_bf16 v[168:171], a[16:19], a[108:111], v[168:171]\n" \
"ds_read_b64_tr_b16 a[16:17], v35 offset:52544\n" \
"ds_read_b64_tr_b16 a[18:19], v35 offset:54848\n" \
"v_mfma_f32_16x16x32_bf16 v[176:179], a[20:23], a[108:111], v[176:179]\n" \
"v_mfma_f32_16x16x32_bf16 v[180:183], a[20:23], a[124:127], v[180:183]\n" \
"v_mfma_f32_16x16x32_bf16 v[248:251], a[20:23], v[76:79], v[248:251]\n" \
"v_mfma_f32_16x16x32_bf16 v[252:255], a[20:23], v[100:103], v[252:255]\n" \
"ds_read_b64_tr_b16 a[20:21], v35 offset:42208\n" \
"ds_read_b64_tr_b16 a[22:23], v35 offset:44512\n" \
"v_mfma_f32_16x16x32_bf16 v[112:115], a[24:27], a[220:223], v[112:115]\n" \
"buffer_load_dwordx4 v7, s[80:83], s73 offen offset:3072 lds\n" \
"v_mfma_f32_16x16x32_bf16 v[112:115], a[28:31], a[224:227], v[112:115]\n" \
"s_add_u32 m0, s92, 0x2000\n" \
"v_readlane_b32 s92, v11, 46\n" \
"v_mfma_f32_16x16x32_bf16 v[116:119], a[24:27], a[228:231], v[116:119]\n" \
"v_mfma_f32_16x16x32_bf16 v[116:119], a[28:31], a[232:235], v[116:119]\n" \
"v_mfma_f32_16x16x32_bf16 v[184:187], a[24:27], a[236:239], v[184:187]\n" \
"v_mfma_f32_16x16x32_bf16 v[184:187], a[28:31], a[240:243], v[184:187]\n" \
"buffer_load_dwordx4 v8, s[80:83], s73 offen offset:0 lds\n" \
"v_mfma_f32_16x16x32_bf16 v[188:191], a[24:27], a[244:247], v[188:191]\n" \
"ds_read_b64_tr_b16 a[24:25], v35 offset:52576\n" \
"ds_read_b64_tr_b16 a[26:27], v35 offset:54880\n" \
"v_mfma_f32_16x16x32_bf16 v[188:191], a[28:31], a[248:251], v[188:191]\n" \
"s_mov_b32 m0, s94\n" \
"ds_read_b64_tr_b16 a[28:29], v35 offset:42240\n" \
"v_mfma_f32_16x16x32_bf16 v[196:199], a[192:195], a[244:247], v[196:199]\n" \
"ds_read_b64_tr_b16 a[30:31], v35 offset:44544\n" \
"v_readlane_b32 s73, v11, 6\n" \
"v_mfma_f32_16x16x32_bf16 v[196:199], a[196:199], a[248:251], v[196:199]\n" \
"v_mfma_f32_16x16x32_bf16 v[192:195], a[192:195], a[236:239], v[192:195]\n" \
"buffer_load_dwordx4 v9, s[84:87], 0 offen lds\n" \
"v_mfma_f32_16x16x32_bf16 v[192:195], a[196:199], a[240:243], v[192:195]\n" \
"v_mfma_f32_16x16x32_bf16 v[124:127], a[192:195], a[228:231], v[124:127]\n" \
"v_mfma_f32_16x16x32_bf16 v[124:127], a[196:199], a[232:235], v[124:127]\n" \
"v_mfma_f32_16x16x32_bf16 v[120:123], a[192:195], a[220:223], v[120:123]\n" \
"ds_read_b64_tr_b16 a[192:193], v35 offset:52608\n" \
"ds_read_b64_tr_b16 a[194:195], v35 offset:54912\n" \
"v_mfma_f32_16x16x32_bf16 v[120:123], a[196:199], a[224:227], v[120:123]\n" \
"ds_read_b64_tr_b16 a[196:197], v33 offset:0\n" \
"ds_read_b64_tr_b16 a[198:199], v33 offset:4608\n" \
"v_mfma_f32_16x16x32_bf16 v[128:131], a[200:203], a[220:223], v[128:131]\n" \
"buffer_load_dwordx4 a[128:131], v20, s[56:59], s68 offen offset:0 sc1\n" \
"v_mfma_f32_16x16x32_bf16 v[128:131], a[204:207], a[224:227], v[128:131]\n" \
"v_mfma_f32_16x16x32_bf16 v[132:135], a[200:203], a[228:231], v[132:135]\n" \
"v_mfma_f32_16x16x32_bf16 v[132:135], a[204:207], a[232:235], v[132:135]\n" \
"v_mfma_f32_16x16x32_bf16 v[200:203], a[200:203], a[236:239], v[200:203]\n" \
"v_mfma_f32_16x16x32_bf16 v[200:203], a[204:207], a[240:243], v[200:203]\n" \
"buffer_load_dwordx4 a[132:135], v20, s[56:59], s68 offen offset:1024 sc1\n" \
"v_mfma_f32_16x16x32_bf16 v[204:207], a[200:203], a[244:247], v[204:207]\n" \
"ds_read_b64_tr_b16 a[200:201], v33 offset:9216\n" \
"ds_read_b64_tr_b16 a[202:203], v33 offset:13824\n" \
"v_mfma_f32_16x16x32_bf16 v[204:207], a[204:207], a[248:251], v[204:207]\n" \
"ds_read_b64_tr_b16 a[204:205], v33 offset:32\n" \
"s_waitcnt lgkmcnt(7)\n" \
"v_mfma_f32_16x16x32_bf16 v[212:215], a[208:211], a[244:247], v[212:215]\n" \
"ds_read_b64_tr_b16 a[206:207], v33 offset:4640\n" \
"v_mfma_f32_16x16x32_bf16 v[212:215], a[212:215], a[248:251], v[212:215]\n" \
"v_mfma_f32_16x16x32_bf16 v[208:211], a[208:211], a[236:239], v[208:211]\n" \
"buffer_load_dword v47, v38, s[56:59], s71 offen sc1\n" \
"v_mfma_f32_16x16x32_bf16 v[208:211], a[212:215], a[240:243], v[208:211]\n" \
"v_mfma_f32_16x16x32_bf16 v[140:143], a[208:211], a[228:231], v[140:143]\n" \
"v_mfma_f32_16x16x32_bf16 v[140:143], a[212:215], a[232:235], v[140:143]\n" \
"v_mfma_f32_16x16x32_bf16 v[136:139], a[208:211], a[220:223], v[136:139]\n" \
"ds_read_b64_tr_b16 a[208:209], v33 offset:9248\n" \
"ds_read_b64_tr_b16 a[210:211], v33 offset:13856\n" \
"v_mfma_f32_16x16x32_bf16 v[136:139], a[212:215], a[224:227], v[136:139]\n" \
"ds_read_b64_tr_b16 a[212:213], v33 offset:64\n" \
"ds_read_b64_tr_b16 a[214:215], v33 offset:4672\n" \
"v_mfma_f32_16x16x32_bf16 v[144:147], a[216:219], a[220:223], v[144:147]\n" \
"buffer_load_dwordx4 a[144:147], v32, s[56:59], s68 offen offset:0 sc1\n" \
"v_mfma_f32_16x16x32_bf16 v[144:147], a[0:3], a[224:227], v[144:147]\n" \
"v_mfma_f32_16x16x32_bf16 v[148:151], a[216:219], a[228:231], v[148:151]\n" \
"v_mfma_f32_16x16x32_bf16 v[148:151], a[0:3], a[232:235], v[148:151]\n" \
"v_mfma_f32_16x16x32_bf16 v[216:219], a[216:219], a[236:239], v[216:219]\n" \
"v_mfma_f32_16x16x32_bf16 v[216:219], a[0:3], a[240:243], v[216:219]\n" \
"buffer_load_dwordx4 a[148:151], v32, s[56:59], s68 offen offset:1024 sc1\n" \
"v_mfma_f32_16x16x32_bf16 v[220:223], a[216:219], a[244:247], v[220:223]\n" \
"ds_read_b64_tr_b16 a[216:217], v33 offset:9280\n" \
"ds_read_b64_tr_b16 a[218:219], v33 offset:13888\n" \
"v_mfma_f32_16x16x32_bf16 v[220:223], a[0:3], a[248:251], v[220:223]\n" \
"ds_read_b64_tr_b16 a[0:1], v33 offset:96\n" \
"ds_read_b64_tr_b16 a[2:3], v33 offset:4704\n" \
"v_mfma_f32_16x16x32_bf16 v[228:231], a[4:7], a[244:247], v[228:231]\n" \
"v_mfma_f32_16x16x32_bf16 v[228:231], a[8:11], a[248:251], v[228:231]\n" \
"v_mfma_f32_16x16x32_bf16 v[224:227], a[4:7], a[236:239], v[224:227]\n" \
"buffer_load_dwordx4 a[152:155], v32, s[56:59], s68 offen offset:2048 sc1\n" \
"v_mfma_f32_16x16x32_bf16 v[224:227], a[8:11], a[240:243], v[224:227]\n" \
"v_mfma_f32_16x16x32_bf16 v[156:159], a[4:7], a[228:231], v[156:159]\n" \
"v_mfma_f32_16x16x32_bf16 v[156:159], a[8:11], a[232:235], v[156:159]\n" \
"v_mfma_f32_16x16x32_bf16 v[152:155], a[4:7], a[220:223], v[152:155]\n" \
"ds_read_b64_tr_b16 a[4:5], v33 offset:9312\n" \
"ds_read_b64_tr_b16 a[6:7], v33 offset:13920\n" \
"v_mfma_f32_16x16x32_bf16 v[152:155], a[8:11], a[224:227], v[152:155]\n" \
"ds_read_b64_tr_b16 a[8:9], v33 offset:128\n" \
"ds_read_b64_tr_b16 a[10:11], v33 offset:4736\n" \
"v_mfma_f32_16x16x32_bf16 v[160:163], a[12:15], a[220:223], v[160:163]\n" \
"buffer_load_dwordx4 a[156:159], v32, s[56:59], s68 offen offset:3072 sc1\n" \
"v_mfma_f32_16x16x32_bf16 v[160:163], a[16:19], a[224:227], v[160:163]\n" \
"v_mfma_f32_16x16x32_bf16 v[164:167], a[12:15], a[228:231], v[164:167]\n" \
"v_mfma_f32_16x16x32_bf16 v[164:167], a[16:19], a[232:235], v[164:167]\n" \
"v_mfma_f32_16x16x32_bf16 v[232:235], a[12:15], a[236:239], v[232:235]\n" \
"v_mfma_f32_16x16x32_bf16 v[232:235], a[16:19], a[240:243], v[232:235]\n" \
"buffer_load_dwordx4 a[136:139], v20, s[56:59], s68 offen offset:2048 sc1\n" \
"v_mfma_f32_16x16x32_bf16 v[236:239], a[12:15], a[244:247], v[236:239]\n" \
"ds_read_b64_tr_b16 a[12:13], v33 offset:9344\n" \
"ds_read_b64_tr_b16 a[14:15], v33 offset:13952\n" \
"v_mfma_f32_16x16x32_bf16 v[236:239], a[16:19], a[248:251], v[236:239]\n" \
"ds_read_b64_tr_b16 a[16:17], v33 offset:160\n" \
"ds_read_b64_tr_b16 a[18:19], v33 offset:4768\n" \
"v_mfma_f32_16x16x32_bf16 v[244:247], a[20:23], a[244:247], v[244:247]\n" \
"v_mfma_f32_16x16x32_bf16 v[244:247], a[24:27], a[248:251], v[244:247]\n" \
"v_mfma_f32_16x16x32_bf16 v[240:243], a[20:23], a[236:239], v[240:243]\n" \
"buffer_load_dwordx4 a[140:143], v20, s[56:59], s68 offen offset:3072 sc1\n" \
"v_mfma_f32_16x16x32_bf16 v[240:243], a[24:27], a[240:243], v[240:243]\n" \
"v_readlane_b32 s68, v11, 49\n" \
"v_mfma_f32_16x16x32_bf16 v[172:175], a[20:23], a[228:231], v[172:175]\n" \
"v_mfma_f32_16x16x32_bf16 v[172:175], a[24:27], a[232:235], v[172:175]\n" \
"v_mfma_f32_16x16x32_bf16 v[168:171], a[20:23], a[220:223], v[168:171]\n" \
"ds_read_b64_tr_b16 a[20:21], v33 offset:9376\n" \
"ds_read_b64_tr_b16 a[22:23], v33 offset:13984\n" \
"v_mfma_f32_16x16x32_bf16 v[168:171], a[24:27], a[224:227], v[168:171]\n" \
"ds_read_b64_tr_b16 a[24:25], v33 offset:192\n" \
"ds_read_b64_tr_b16 a[26:27], v33 offset:4800\n" \
"v_mfma_f32_16x16x32_bf16 v[176:179], a[28:31], a[220:223], v[176:179]\n" \
"s_waitcnt lgkmcnt(8)\n" \
"v_mfma_f32_16x16x32_bf16 v[176:179], a[192:195], a[224:227], v[176:179]\n" \
"buffer_load_dwordx4 a[32:35], v20, s[56:59], s69 offen offset:0 sc1\n" \
"v_mfma_f32_16x16x32_bf16 v[180:183], a[28:31], a[228:231], v[180:183]\n" \
"v_mfma_f32_16x16x32_bf16 v[180:183], a[192:195], a[232:235], v[180:183]\n" \
"v_mfma_f32_16x16x32_bf16 v[248:251], a[28:31], a[236:239], v[248:251]\n" \
"v_mfma_f32_16x16x32_bf16 v[248:251], a[192:195], a[240:243], v[248:251]\n" \
"v_mfma_f32_16x16x32_bf16 v[252:255], a[28:31], a[244:247], v[252:255]\n" \
"ds_read_b64_tr_b16 a[28:29], v33 offset:9408\n" \
"ds_read_b64_tr_b16 a[30:31], v33 offset:14016\n" \
"v_mfma_f32_16x16x32_bf16 v[252:255], a[192:195], a[248:251], v[252:255]\n" \
"ds_read_b64_tr_b16 a[192:193], v33 offset:224\n" \
"s_waitcnt vmcnt(18)\n" \
"v_mfma_f32_16x16x32_bf16 v[112:115], a[196:199], a[64:67], v[112:115]\n" \
"buffer_load_dwordx4 a[36:39], v20, s[56:59], s69 offen offset:1024 sc1\n" \
"v_mfma_f32_16x16x32_bf16 v[112:115], a[200:203], a[68:71], v[112:115]\n" \
"ds_read_b64_tr_b16 a[194:195], v33 offset:4832\n" \
"v_mfma_f32_16x16x32_bf16 v[116:119], a[196:199], a[80:83], v[116:119]\n" \
"ds_read_b64_tr_b16 a[196:197], v33 offset:9440\n" \
"ds_read_b64_tr_b16 a[198:199], v33 offset:14048\n" \
"v_mfma_f32_16x16x32_bf16 v[116:119], a[200:203], a[84:87], v[116:119]\n" \
"ds_read_b64_tr_b16 a[200:201], v33 offset:256\n" \
"ds_read_b64_tr_b16 a[202:203], v33 offset:4864\n" \
"v_mfma_f32_16x16x32_bf16 v[124:127], a[204:207], a[80:83], v[124:127]\n" \
"v_mfma_f32_16x16x32_bf16 v[124:127], a[208:211], a[84:87], v[124:127]\n" \
"buffer_load_dwordx4 a[48:51], v32, s[56:59], s69 offen offset:0 sc1\n" \
"v_mfma_f32_16x16x32_bf16 v[120:123], a[204:207], a[64:67], v[120:123]\n" \
"ds_read_b64_tr_b16 a[204:205], v33 offset:9472\n" \
"ds_read_b64_tr_b16 a[206:207], v33 offset:14080\n" \
"v_mfma_f32_16x16x32_bf16 v[120:123], a[208:211], a[68:71], v[120:123]\n" \
"ds_read_b64_tr_b16 a[208:209], v33 offset:18432\n" \
"ds_read_b64_tr_b16 a[210:211], v33 offset:23040\n" \
"v_mfma_f32_16x16x32_bf16 v[128:131], a[212:215], a[64:67], v[128:131]\n" \
"v_mfma_f32_16x16x32_bf16 v[128:131], a[216:219], a[68:71], v[128:131]\n" \
"v_mfma_f32_16x16x32_bf16 v[132:135], a[212:215], a[80:83], v[132:135]\n" \
"buffer_load_dwordx4 a[52:55], v32, s[56:59], s69 offen offset:1024 sc1\n" \
"v_mfma_f32_16x16x32_bf16 v[132:135], a[216:219], a[84:87], v[132:135]\n" \
"ds_read_b64_tr_b16 a[212:213], v33 offset:27648\n" \
"ds_read_b64_tr_b16 a[214:215], v33 offset:32256\n" \
"v_mfma_f32_16x16x32_bf16 v[140:143], a[0:3], a[80:83], v[140:143]\n" \
"ds_read_b64_tr_b16 a[216:217], v33 offset:18464\n" \
"ds_read_b64_tr_b16 a[218:219], v33 offset:23072\n" \
"v_mfma_f32_16x16x32_bf16 v[140:143], a[4:7], a[84:87], v[140:143]\n" \
"s_waitcnt vmcnt(10)\n" \
"v_cmp_ne_u32_e32 vcc, s62, v47\n" \
"v_mfma_f32_16x16x32_bf16 v[136:139], a[0:3], a[64:67], v[136:139]\n" \
"ds_read_b64_tr_b16 a[0:1], v33 offset:27680\n" \
".fill (L_ab + 36864 - .) / 4, 4, 0xBF800000\n" \
"L_ap9:\n" \
"s_cbranch_execz L_ap0\n" \
"ds_read_b64_tr_b16 a[2:3], v33 offset:32288\n" \
"v_mfma_f32_16x16x32_bf16 v[136:139], a[4:7], a[68:71], v[136:139]\n" \
"buffer_load_dwordx4 a[56:59], v32, s[56:59], s69 offen offset:2048 sc1\n" \
"v_mfma_f32_16x16x32_bf16 v[144:147], a[8:11], a[64:67], v[144:147]\n" \
"ds_read_b64_tr_b16 a[4:5], v33 offset:18496\n" \
"s_waitcnt lgkmcnt(13)\n" \
"v_mfma_f32_16x16x32_bf16 v[144:147], a[12:15], a[68:71], v[144:147]\n" \
"ds_read_b64_tr_b16 a[6:7], v33 offset:23104\n" \
"s_cbranch_vccnz L_sb_%=\n" \
"L_rb_%=:\n" \
"v_mfma_f32_16x16x32_bf16 v[148:151], a[8:11], a[80:83], v[148:151]\n" \
"ds_read_b64_tr_b16 a[8:9], v33 offset:27712\n" \
"ds_read_b64_tr_b16 a[10:11], v33 offset:32320\n" \
"v_mfma_f32_16x16x32_bf16 v[148:151], a[12:15], a[84:87], v[148:151]\n" \
"ds_read_b64_tr_b16 a[12:13], v33 offset:18528\n" \
"ds_read_b64_tr_b16 a[14:15], v33 offset:23136\n" \
"buffer_load_dwordx4 a[60:63], v32, s[56:59], s69 offen offset:3072 sc1\n" \
"v_mfma_f32_16x16x32_bf16 v[156:159], a[16:19], a[80:83], v[156:159]\n" \
"v_readlane_b32 s71, v11, 52\n" \
"v_cmp_eq_u32_e32 vcc, 0, v44\n" \
"v_mfma_f32_16x16x32_bf16 v[156:159], a[20:23], a[84:87], v[156:159]\n" \
"v_mfma_f32_16x16x32_bf16 v[152:155], a[16:19], a[64:67], v[152:155]\n" \
"ds_read_b64_tr_b16 a[16:17], v33 offset:27744\n" \
"ds_read_b64_tr_b16 a[18:19], v33 offset:32352\n" \
"v_mfma_f32_16x16x32_bf16 v[152:155], a[20:23], a[68:71], v[152:155]\n" \
"ds_read_b64_tr_b16 a[20:21], v33 offset:18560\n" \
"ds_read_b64_tr_b16 a[22:23], v33 offset:23168\n" \
"buffer_load_dwordx4 a[40:43], v20, s[56:59], s69 offen offset:2048 sc1\n" \
"v_mfma_f32_16x16x32_bf16 v[160:163], a[24:27], a[64:67], v[160:163]\n" \
"v_cndmask_b32_e32 v50, 0, v45, vcc\n" \
"v_cmp_eq_u32_e32 vcc, 1, v44\n" \
"v_mfma_f32_16x16x32_bf16 v[160:163], a[28:31], a[68:71], v[160:163]\n" \
"v_and_b32_e32 v44, 0x3c0, v20\n" \
"v_cndmask_b32_e32 v51, 0, v45, vcc\n" \
"v_mfma_f32_16x16x32_bf16 v[164:167], a[24:27], a[80:83], v[164:167]\n" \
"ds_read_b64_tr_b16 a[24:25], v33 offset:27776\n" \
"ds_read_b64_tr_b16 a[26:27], v33 offset:32384\n" \
"v_mfma_f32_16x16x32_bf16 v[164:167], a[28:31], a[84:87], v[164:167]\n" \
"ds_read_b64_tr_b16 a[28:29], v33 offset:18592\n" \
"ds_read_b64_tr_b16 a[30:31], v33 offset:23200\n" \
"buffer_load_dwordx4 a[44:47], v20, s[56:59], s69 offen offset:3072 sc1\n" \
"v_mfma_f32_16x16x32_bf16 v[172:175], a[192:195], a[80:83], v[172:175]\n" \
"v_readlane_b32 s69, v11, 50\n" \
"v_bfe_u32 v45, v20, 1, 5\n" \
"v_mfma_f32_16x16x32_bf16 v[172:175], a[196:199], a[84:87], v[172:175]\n" \
"v_and_b32_e32 v45, 24, v45\n" \
"v_add_u32_e32 v45, 0x4800, v45\n" \
"v_mfma_f32_16x16x32_bf16 v[168:171], a[192:195], a[64:67], v[168:171]\n" \
"ds_read_b64_tr_b16 a[192:193], v33 offset:27808\n" \
"ds_read_b64_tr_b16 a[194:195], v33 offset:32416\n" \
"v_mfma_f32_16x16x32_bf16 v[168:171], a[196:199], a[68:71], v[168:171]\n" \
"ds_read_b64_tr_b16 a[196:197], v33 offset:18624\n" \
"s_waitcnt lgkmcnt(13)\n" \
"v_mfma_f32_16x16x32_bf16 v[176:179], a[200:203], a[64:67], v[176:179]\n" \
"ds_read_b64_tr_b16 a[198:199], v33 offset:23232\n" \
"v_add_u32_e32 v36, v44, v45\n" \
"v_mfma_f32_16x16x32_bf16 v[176:179], a[204:207], a[68:71], v[176:179]\n" \
"buffer_load_dwordx4 a[220:223], v20, s[56:59], s64 offen offset:0 sc1\n" \
"v_mfma_f32_16x16x32_bf16 v[180:183], a[200:203], a[80:83], v[180:183]\n" \
"ds_read_b64_tr_b16 a[200:201], v33 offset:27840\n" \
"ds_read_b64_tr_b16 a[202:203], v33 offset:32448\n" \
"v_mfma_f32_16x16x32_bf16 v[180:183], a[204:207], a[84:87], v[180:183]\n" \
"ds_read_b64_tr_b16 a[204:205], v33 offset:18656\n" \
"ds_read_b64_tr_b16 a[206:207], v33 offset:23264\n" \
"v_mfma_f32_16x16x32_bf16 v[112:115], a[208:211], a[72:75], v[112:115]\n" \
"v_mfma_f32_16x16x32_bf16 v[112:115], a[212:215], a[76:79], v[112:115]\n" \
"v_mfma_f32_16x16x32_bf16 v[116:119], a[208:211], a[88:91], v[116:119]\n" \
"ds_read_b64_tr_b16 a[208:209], v33 offset:27872\n" \
"ds_read_b64_tr_b16 a[210:211], v33 offset:32480\n" \
"v_mfma_f32_16x16x32_bf16 v[116:119], a[212:215], a[92:95], v[116:119]\n" \
"ds_read_b64_tr_b16 a[212:213], v33 offset:18688\n" \
"ds_read_b64_tr_b16 a[214:215], v33 offset:23296\n" \
"v_mfma_f32_16x16x32_bf16 v[124:127], a[216:219], a[88:91], v[124:127]\n" \
"buffer_load_dwordx4 a[224:227], v20, s[56:59], s64 offen offset:1024 sc1\n" \
"v_mfma_f32_16x16x32_bf16 v[124:127], a[0:3], a[92:95], v[124:127]\n" \
"v_mfma_f32_16x16x32_bf16 v[120:123], a[216:219], a[72:75], v[120:123]\n" \
"ds_read_b64_tr_b16 a[216:217], v33 offset:27904\n" \
"ds_read_b64_tr_b16 a[218:219], v33 offset:32512\n" \
"v_mfma_f32_16x16x32_bf16 v[120:123], a[0:3], a[76:79], v[120:123]\n" \
"s_barrier\n" \
"v_mfma_f32_16x16x32_bf16 v[128:131], a[4:7], a[72:75], v[128:131]\n" \
"ds_read_b64_tr_b16 a[0:1], v33 offset:36864\n" \
"ds_read_b64_tr_b16 a[2:3], v33 offset:41472\n" \
"v_mfma_f32_16x16x32_bf16 v[128:131], a[8:11], a[76:79], v[128:131]\n" \
"buffer_load_dwordx4 a[236:239], v32, s[56:59], s64 offen offset:0 sc1\n" \
"v_mfma_f32_16x16x32_bf16 v[132:135], a[4:7], a[88:91], v[132:135]\n" \
"ds_read_b64_tr_b16 a[4:5], v33 offset:46080\n" \
"ds_read_b64_tr_b16 a[6:7], v33 offset:50688\n" \
"v_mfma_f32_16x16x32_bf16 v[132:135], a[8:11], a[92:95], v[132:135]\n" \
"ds_read_b64_tr_b16 a[8:9], v33 offset:55296\n" \
"s_waitcnt lgkmcnt(15)\n" \
"v_mfma_f32_16x16x32_bf16 v[140:143], a[12:15], a[88:91], v[140:143]\n" \
"ds_read_b64_tr_b16 a[10:11], v33 offset:59904\n" \
"v_mfma_f32_16x16x32_bf16 v[140:143], a[16:19], a[92:95], v[140:143]\n" \
"v_mfma_f32_16x16x32_bf16 v[136:139], a[12:15], a[72:75], v[136:139]\n" \
"ds_read_b64_tr_b16 a[12:13], v10 offset:0\n" \
"ds_read_b64_tr_b16 a[14:15], v10 offset:4608\n" \
"v_mfma_f32_16x16x32_bf16 v[136:139], a[16:19], a[76:79], v[136:139]\n" \
"ds_read_b64_tr_b16 a[16:17], v34 offset:0\n" \
"ds_read_b64_tr_b16 a[18:19], v34 offset:4608\n" \
"v_mfma_f32_16x16x32_bf16 v[144:147], a[20:23], a[72:75], v[144:147]\n" \
"buffer_load_dwordx4 a[240:243], v32, s[56:59], s64 offen offset:1024 sc1\n" \
"v_mfma_f32_16x16x32_bf16 v[144:147], a[24:27], a[76:79], v[144:147]\n" \
"v_mfma_f32_16x16x32_bf16 v[148:151], a[20:23], a[88:91], v[148:151]\n" \
"ds_read_b64_tr_b16 a[20:21], v34 offset:9216\n" \
"ds_read_b64_tr_b16 a[22:23], v34 offset:13824\n" \
"v_mfma_f32_16x16x32_bf16 v[148:151], a[24:27], a[92:95], v[148:151]\n" \
"ds_read_b64_tr_b16 a[24:25], v34 offset:18432\n" \
"ds_read_b64_tr_b16 a[26:27], v34 offset:23040\n" \
"v_mfma_f32_16x16x32_bf16 v[156:159], a[28:31], a[88:91], v[156:159]\n" \
"v_mfma_f32_16x16x32_bf16 v[156:159], a[192:195], a[92:95], v[156:159]\n" \
"buffer_load_dwordx4 a[244:247], v32, s[56:59], s64 offen offset:2048 sc1\n" \
"v_mfma_f32_16x16x32_bf16 v[152:155], a[28:31], a[72:75], v[152:155]\n" \
"ds_read_b64_tr_b16 a[28:29], v10 offset:9216\n" \
"ds_read_b64_tr_b16 a[30:31], v10 offset:13824\n" \
"v_mfma_f32_16x16x32_bf16 v[152:155], a[192:195], a[76:79], v[152:155]\n" \
"ds_read_b64_tr_b16 a[192:193], v33 offset:36896\n" \
"ds_read_b64_tr_b16 a[194:195], v33 offset:41504\n" \
"v_mfma_f32_16x16x32_bf16 v[160:163], a[196:199], a[72:75], v[160:163]\n" \
"s_waitcnt lgkmcnt(15)\n" \
"v_mfma_f32_16x16x32_bf16 v[160:163], a[200:203], a[76:79], v[160:163]\n" \
"v_mfma_f32_16x16x32_bf16 v[164:167], a[196:199], a[88:91], v[164:167]\n" \
"ds_read_b64_tr_b16 a[196:197], v33 offset:46112\n" \
"ds_read_b64_tr_b16 a[198:199], v33 offset:50720\n" \
"v_mfma_f32_16x16x32_bf16 v[164:167], a[200:203], a[92:95], v[164:167]\n" \
"ds_read_b64_tr_b16 a[200:201], v33 offset:55328\n" \
"ds_read_b64_tr_b16 a[202:203], v33 offset:59936\n" \
"v_mfma_f32_16x16x32_bf16 v[172:175], a[204:207], a[88:91], v[172:175]\n" \
"buffer_load_dwordx4 a[248:251], v32, s[56:59], s64 offen offset:3072 sc1\n" \
"v_mfma_f32_16x16x32_bf16 v[172:175], a[208:211], a[92:95], v[172:175]\n" \
"v_mfma_f32_16x16x32_bf16 v[168:171], a[204:207], a[72:75], v[168:171]\n" \
"ds_read_b64_tr_b16 a[204:205], v10 offset:32\n" \
"ds_read_b64_tr_b16 a[206:207], v10 offset:4640\n" \
"v_mfma_f32_16x16x32_bf16 v[168:171], a[208:211], a[76:79], v[168:171]\n" \
"ds_read_b64_tr_b16 a[208:209], v34 offset:32\n" \
"ds_read_b64_tr_b16 a[210:211], v34 offset:4640\n" \
"v_mfma_f32_16x16x32_bf16 v[176:179], a[212:215], a[72:75], v[176:179]\n" \
"v_mfma_f32_16x16x32_bf16 v[176:179], a[216:219], a[76:79], v[176:179]\n" \
"buffer_load_dwordx4 a[228:231], v20, s[56:59], s64 offen offset:2048 sc1\n" \
"v_mfma_f32_16x16x32_bf16 v[180:183], a[212:215], a[88:91], v[180:183]\n" \
"ds_read_b64_tr_b16 a[212:213], v34 offset:9248\n" \
"ds_read_b64_tr_b16 a[214:215], v34 offset:13856\n" \
"v_mfma_f32_16x16x32_bf16 v[180:183], a[216:219], a[92:95], v[180:183]\n" \
"ds_read_b64_tr_b16 a[216:217], v34 offset:18464\n" \
"ds_read_b64_tr_b16 a[218:219], v34 offset:23072\n" \
"v_mfma_f32_16x16x32_bf16 v[112:115], a[0:3], a[128:131], v[112:115]\n" \
"s_waitcnt lgkmcnt(15)\n" \
"v_mfma_f32_16x16x32_bf16 v[112:115], a[4:7], a[132:135], v[112:115]\n" \
"s_waitcnt vmcnt(18)\n" \
"v_mfma_f32_16x16x32_bf16 v[116:119], a[0:3], a[144:147], v[116:119]\n" \
"ds_read_b64_tr_b16 a[0:1], v10 offset:9248\n" \
"ds_read_b64_tr_b16 a[2:3], v10 offset:13856\n" \
"v_mfma_f32_16x16x32_bf16 v[116:119], a[4:7], a[148:151], v[116:119]\n" \
"ds_read_b64_tr_b16 a[4:5], v33 offset:36928\n" \
"ds_read_b64_tr_b16 a[6:7], v33 offset:41536\n" \
"v_mfma_f32_16x16x32_bf16 v[116:119], a[8:11], a[152:155], v[116:119]\n" \
"s_waitcnt vmcnt(17)\n" \
"v_mfma_f32_16x16x32_bf16 v[116:119], a[12:15], a[156:159], v[116:119]\n" \
"s_waitcnt vmcnt(16)\n" \
"v_mfma_f32_16x16x32_bf16 v[112:115], a[8:11], a[136:139], v[112:115]\n" \
"ds_read_b64_tr_b16 a[8:9], v33 offset:46144\n" \
"s_waitcnt vmcnt(15)\n" \
"v_mfma_f32_16x16x32_bf16 v[112:115], a[12:15], a[140:143], v[112:115]\n" \
"ds_read_b64_tr_b16 a[10:11], v33 offset:50752\n" \
"s_waitcnt vmcnt(14)\n" \
"v_mfma_f32_16x16x32_bf16 v[112:115], a[16:19], a[32:35], v[112:115]\n" \
"ds_read_b64_tr_b16 a[12:13], v33 offset:55360\n" \
"s_waitcnt vmcnt(13)\n" \
"v_mfma_f32_16x16x32_bf16 v[112:115], a[20:23], a[36:39], v[112:115]\n" \
"ds_read_b64_tr_b16 a[14:15], v33 offset:59968\n" \
"s_waitcnt vmcnt(12)\n" \
"v_mfma_f32_16x16x32_bf16 v[116:119], a[16:19], a[48:51], v[116:119]\n" \
"ds_read_b64_tr_b16 a[16:17], v10 offset:64\n" \
"s_waitcnt vmcnt(11)\n" \
"v_mfma_f32_16x16x32_bf16 v[116:119], a[20:23], a[52:55], v[116:119]\n" \
"ds_read_b64_tr_b16 a[18:19], v10 offset:4672\n" \
"s_waitcnt vmcnt(10)\n" \
"v_mfma_f32_16x16x32_bf16 v[116:119], a[24:27], a[56:59], v[116:119]\n" \
"ds_read_b64_tr_b16 a[20:21], v34 offset:64\n" \
"ds_read_b64_tr_b16 a[22:23], v34 offset:4672\n" \
"buffer_load_dwordx4 a[232:235], v20, s[56:59], s64 offen offset:3072 sc1\n" \
"s_waitcnt vmcnt(10) lgkmcnt(12)\n" \
"v_mfma_f32_16x16x32_bf16 v[116:119], a[28:31], a[60:63], v[116:119]\n" \
"buffer_load_dwordx4 a[160:163], v20, s[56:59], s72 offen offset:0 sc1\n" \
"s_waitcnt vmcnt(10)\n" \
"v_mfma_f32_16x16x32_bf16 v[112:115], a[24:27], a[40:43], v[112:115]\n" \
"ds_read_b64_tr_b16 a[24:25], v34 offset:9280\n" \
"ds_read_b64_tr_b16 a[26:27], v34 offset:13888\n" \
"buffer_load_dwordx4 a[164:167], v20, s[56:59], s72 offen offset:1024 sc1\n" \
"s_waitcnt vmcnt(10)\n" \
"v_mfma_f32_16x16x32_bf16 v[112:115], a[28:31], a[44:47], v[112:115]\n" \
"ds_read_b64_tr_b16 a[28:29], v34 offset:18496\n" \
"ds_read_b64_tr_b16 a[30:31], v34 offset:23104\n" \
"v_mfma_f32_16x16x32_bf16 v[120:123], a[192:195], a[128:131], v[120:123]\n" \
"buffer_load_dwordx4 a[176:179], v32, s[56:59], s72 offen offset:0 sc1\n" \
"v_mfma_f32_16x16x32_bf16 v[120:123], a[196:199], a[132:135], v[120:123]\n" \
"v_mfma_f32_16x16x32_bf16 v[124:127], a[192:195], a[144:147], v[124:127]\n" \
"ds_read_b64_tr_b16 a[192:193], v10 offset:9280\n" \
"ds_read_b64_tr_b16 a[194:195], v10 offset:13888\n" \
"v_cvt_pk_bf16_f32 v112, v112, v113\n" \
"v_cvt_pk_bf16_f32 v113, v114, v115\n" \
"v_cvt_pk_bf16_f32 v114, v116, v117\n" \
"v_cvt_pk_bf16_f32 v115, v118, v119\n" \
"buffer_store_dwordx4 v[112:115], v20, s[56:59], s65 offen offset:0 sc1\n" \
"s_add_u32 s65, s65, 0x3800\n" \
"v_mfma_f32_16x16x32_bf16 v[124:127], a[196:199], a[148:151], v[124:127]\n" \
"ds_read_b64_tr_b16 a[196:197], v33 offset:36960\n" \
"ds_read_b64_tr_b16 a[198:199], v33 offset:41568\n" \
"v_mfma_f32_16x16x32_bf16 v[124:127], a[200:203], a[152:155], v[124:127]\n" \
"v_mfma_f32_16x16x32_bf16 v[124:127], a[204:207], a[156:159], v[124:127]\n" \
"v_mfma_f32_16x16x32_bf16 v[120:123], a[200:203], a[136:139], v[120:123]\n" \
"ds_read_b64_tr_b16 a[200:201], v33 offset:46176\n" \
"ds_read_b64_tr_b16 a[202:203], v33 offset:50784\n" \
"v_mfma_f32_16x16x32_bf16 v[120:123], a[204:207], a[140:143], v[120:123]\n" \
"ds_read_b64_tr_b16 a[204:205], v33 offset:55392\n" \
"ds_read_b64_tr_b16 a[206:207], v33 offset:60000\n" \
"v_mfma_f32_16x16x32_bf16 v[120:123], a[208:211], a[32:35], v[120:123]\n" \
"buffer_load_dwordx4 a[180:183], v32, s[56:59], s72 offen offset:1024 sc1\n" \
"v_mfma_f32_16x16x32_bf16 v[120:123], a[212:215], a[36:39], v[120:123]\n" \
"v_mfma_f32_16x16x32_bf16 v[124:127], a[208:211], a[48:51], v[124:127]\n" \
"ds_read_b64_tr_b16 a[208:209], v10 offset:96\n" \
"ds_read_b64_tr_b16 a[210:211], v10 offset:4704\n" \
"v_mfma_f32_16x16x32_bf16 v[124:127], a[212:215], a[52:55], v[124:127]\n" \
"ds_read_b64_tr_b16 a[212:213], v34 offset:96\n" \
"ds_read_b64_tr_b16 a[214:215], v34 offset:4704\n" \
"v_mfma_f32_16x16x32_bf16 v[124:127], a[216:219], a[56:59], v[124:127]\n" \
"s_waitcnt lgkmcnt(12)\n" \
"v_mfma_f32_16x16x32_bf16 v[124:127], a[0:3], a[60:63], v[124:127]\n" \
"buffer_load_dwordx4 a[168:171], v20, s[56:59], s72 offen offset:2048 sc1\n" \
"v_mfma_f32_16x16x32_bf16 v[120:123], a[216:219], a[40:43], v[120:123]\n" \
"ds_read_b64_tr_b16 a[216:217], v34 offset:9312\n" \
"ds_read_b64_tr_b16 a[218:219], v34 offset:13920\n" \
"v_mfma_f32_16x16x32_bf16 v[120:123], a[0:3], a[44:47], v[120:123]\n" \
"ds_read_b64_tr_b16 a[0:1], v34 offset:18528\n" \
"ds_read_b64_tr_b16 a[2:3], v34 offset:23136\n" \
"v_mfma_f32_16x16x32_bf16 v[128:131], a[4:7], a[128:131], v[128:131]\n" \
"v_mfma_f32_16x16x32_bf16 v[128:131], a[8:11], a[132:135], v[128:131]\n" \
"v_mfma_f32_16x16x32_bf16 v[132:135], a[4:7], a[144:147], v[132:135]\n" \
"ds_read_b64_tr_b16 a[4:5], v10 offset:9312\n" \
"ds_read_b64_tr_b16 a[6:7], v10 offset:13920\n" \
"v_mfma_f32_16x16x32_bf16 v[132:135], a[8:11], a[148:151], v[132:135]\n" \
"v_cvt_pk_bf16_f32 v120, v120, v121\n" \
"v_cvt_pk_bf16_f32 v121, v122, v123\n" \
"v_cvt_pk_bf16_f32 v122, v124, v125\n" \
"v_cvt_pk_bf16_f32 v123, v126, v127\n" \
"buffer_store_dwordx4 v[120:123], v20, s[56:59], s66 offen offset:0 sc1\n" \
"s_add_u32 s66, s66, 0x3000\n" \
"v_mfma_f32_16x16x32_bf16 v[132:135], a[12:15], a[152:155], v[132:135]\n" \
"ds_read_b64_tr_b16 a[8:9], v33 offset:36992\n" \
"ds_read_b64_tr_b16 a[10:11], v33 offset:41600\n" \
"v_mfma_f32_16x16x32_bf16 v[132:135], a[16:19], a[156:159], v[132:135]\n" \
"v_mfma_f32_16x16x32_bf16 v[128:131], a[12:15], a[136:139], v[128:131]\n" \
"ds_read_b64_tr_b16 a[12:13], v33 offset:46208\n" \
"ds_read_b64_tr_b16 a[14:15], v33 offset:50816\n" \
"v_mfma_f32_16x16x32_bf16 v[128:131], a[16:19], a[140:143], v[128:131]\n" \
"ds_read_b64_tr_b16 a[16:17], v33 offset:55424\n" \
"ds_read_b64_tr_b16 a[18:19], v33 offset:60032\n" \
"v_mfma_f32_16x16x32_bf16 v[128:131], a[20:23], a[32:35], v[128:131]\n" \
"buffer_load_dwordx4 a[184:187], v32, s[56:59], s72 offen offset:2048 sc1\n" \
"v_mfma_f32_16x16x32_bf16 v[128:131], a[24:27], a[36:39], v[128:131]\n" \
"v_mfma_f32_16x16x32_bf16 v[132:135], a[20:23], a[48:51], v[132:135]\n" \
"ds_read_b64_tr_b16 a[20:21], v10 offset:128\n" \
"ds_read_b64_tr_b16 a[22:23], v10 offset:4736\n" \
"v_mfma_f32_16x16x32_bf16 v[132:135], a[24:27], a[52:55], v[132:135]\n" \
"ds_read_b64_tr_b16 a[24:25], v34 offset:128\n" \
"ds_read_b64_tr_b16 a[26:27], v34 offset:4736\n" \
"v_mfma_f32_16x16x32_bf16 v[132:135], a[28:31], a[56:59], v[132:135]\n" \
"s_waitcnt lgkmcnt(12)\n" \
"v_mfma_f32_16x16x32_bf16 v[132:135], a[192:195], a[60:63], v[132:135]\n" \
"buffer_load_dwordx4 a[172:175], v20, s[56:59], s72 offen offset:3072 sc1\n" \
"v_mfma_f32_16x16x32_bf16 v[128:131], a[28:31], a[40:43], v[128:131]\n" \
"ds_read_b64_tr_b16 a[28:29], v34 offset:9344\n" \
"ds_read_b64_tr_b16 a[30:31], v34 offset:13952\n" \
"v_mfma_f32_16x16x32_bf16 v[128:131], a[192:195], a[44:47], v[128:131]\n" \
"ds_read_b64_tr_b16 a[192:193], v34 offset:18560\n" \
"ds_read_b64_tr_b16 a[194:195], v34 offset:23168\n" \
"v_mfma_f32_16x16x32_bf16 v[136:139], a[196:199], a[128:131], v[136:139]\n" \
"v_mfma_f32_16x16x32_bf16 v[136:139], a[200:203], a[132:135], v[136:139]\n" \
"v_mfma_f32_16x16x32_bf16 v[140:143], a[196:199], a[144:147], v[140:143]\n" \
"buffer_load_dwordx4 a[188:191], v32, s[56:59], s72 offen offset:3072 sc1\n" \
"v_mfma_f32_16x16x32_bf16 v[140:143], a[200:203], a[148:151], v[140:143]\n" \
"v_readlane_b32 s72, v11, 53\n" \
"v_cvt_pk_bf16_f32 v128, v128, v129\n" \
"v_cvt_pk_bf16_f32 v129, v130, v131\n" \
"v_cvt_pk_bf16_f32 v130, v132, v133\n" \
"v_cvt_pk_bf16_f32 v131, v134, v135\n" \
"v_mfma_f32_16x16x32_bf16 v[140:143], a[204:207], a[152:155], v[140:143]\n" \
"buffer_store_dwordx4 v[128:131], v20, s[56:59], s67 offen offset:0 sc1\n" \
"s_add_u32 s67, s67, 0x2800\n" \
"v_mfma_f32_16x16x32_bf16 v[140:143], a[208:211], a[156:159], v[140:143]\n" \
"ds_read_b64_tr_b16 a[196:197], v10 offset:9344\n" \
"ds_read_b64_tr_b16 a[198:199], v10 offset:13952\n" \
"v_mfma_f32_16x16x32_bf16 v[136:139], a[204:207], a[136:139], v[136:139]\n" \
"ds_read_b64_tr_b16 a[200:201], v33 offset:37024\n" \
"ds_read_b64_tr_b16 a[202:203], v33 offset:41632\n" \
"v_mfma_f32_16x16x32_bf16 v[136:139], a[208:211], a[140:143], v[136:139]\n" \
"ds_read_b64_tr_b16 a[204:205], v33 offset:46240\n" \
"ds_read_b64_tr_b16 a[206:207], v33 offset:50848\n" \
"v_mfma_f32_16x16x32_bf16 v[136:139], a[212:215], a[32:35], v[136:139]\n" \
"ds_read_b64_tr_b16 a[208:209], v33 offset:55456\n" \
"ds_read_b64_tr_b16 a[210:211], v33 offset:60064\n" \
"v_mfma_f32_16x16x32_bf16 v[136:139], a[216:219], a[36:39], v[136:139]\n" \
"buffer_load_dwordx4 a[96:99], v20, s[56:59], s93 offen offset:0 sc1\n" \
"v_mfma_f32_16x16x32_bf16 v[140:143], a[212:215], a[48:51], v[140:143]\n" \
"ds_read_b64_tr_b16 a[212:213], v10 offset:160\n" \
"ds_read_b64_tr_b16 a[214:215], v10 offset:4768\n" \
"v_mfma_f32_16x16x32_bf16 v[140:143], a[216:219], a[52:55], v[140:143]\n" \
"ds_read_b64_tr_b16 a[216:217], v34 offset:160\n" \
"ds_read_b64_tr_b16 a[218:219], v34 offset:4768\n" \
"v_mfma_f32_16x16x32_bf16 v[140:143], a[0:3], a[56:59], v[140:143]\n" \
"s_waitcnt lgkmcnt(13)\n" \
"v_mfma_f32_16x16x32_bf16 v[140:143], a[4:7], a[60:63], v[140:143]\n" \
"v_mfma_f32_16x16x32_bf16 v[136:139], a[0:3], a[40:43], v[136:139]\n" \
"buffer_load_dwordx4 a[100:103], v20, s[56:59], s93 offen offset:1024 sc1\n" \
"v_mfma_f32_16x16x32_bf16 v[136:139], a[4:7], a[44:47], v[136:139]\n" \
"ds_read_b64_tr_b16 a[0:1], v34 offset:9376\n" \
"ds_read_b64_tr_b16 a[2:3], v34 offset:13984\n" \
"v_mfma_f32_16x16x32_bf16 v[144:147], a[8:11], a[128:131], v[144:147]\n" \
"ds_read_b64_tr_b16 a[4:5], v34 offset:18592\n" \
"ds_read_b64_tr_b16 a[6:7], v34 offset:23200\n" \
"v_mfma_f32_16x16x32_bf16 v[144:147], a[12:15], a[132:135], v[144:147]\n" \
"v_mfma_f32_16x16x32_bf16 v[148:151], a[8:11], a[144:147], v[148:151]\n" \
"ds_read_b64_tr_b16 a[8:9], v10 offset:9376\n" \
"v_cvt_pk_bf16_f32 v136, v136, v137\n" \
"v_cvt_pk_bf16_f32 v137, v138, v139\n" \
"v_cvt_pk_bf16_f32 v138, v140, v141\n" \
"v_cvt_pk_bf16_f32 v139, v142, v143\n" \
"buffer_store_dwordx4 v[136:139], v20, s[56:59], s68 offen offset:0 sc1\n" \
"s_add_u32 s68, s68, 0x2000\n" \
"v_mfma_f32_16x16x32_bf16 v[148:151], a[12:15], a[148:151], v[148:151]\n" \
"ds_read_b64_tr_b16 a[10:11], v10 offset:13984\n" \
"ds_read_b64_tr_b16 a[12:13], v33 offset:37056\n" \
"v_mfma_f32_16x16x32_bf16 v[148:151], a[16:19], a[152:155], v[148:151]\n" \
"ds_read_b64_tr_b16 a[14:15], v33 offset:41664\n" \
"v_mfma_f32_16x16x32_bf16 v[148:151], a[20:23], a[156:159], v[148:151]\n" \
"v_mfma_f32_16x16x32_bf16 v[144:147], a[16:19], a[136:139], v[144:147]\n" \
"buffer_load_dwordx4 a[112:115], v32, s[56:59], s93 offen offset:0 sc1\n" \
"v_mfma_f32_16x16x32_bf16 v[144:147], a[20:23], a[140:143], v[144:147]\n" \
"ds_read_b64_tr_b16 a[16:17], v33 offset:46272\n" \
"ds_read_b64_tr_b16 a[18:19], v33 offset:50880\n" \
"v_mfma_f32_16x16x32_bf16 v[144:147], a[24:27], a[32:35], v[144:147]\n" \
"ds_read_b64_tr_b16 a[20:21], v33 offset:55488\n" \
"ds_read_b64_tr_b16 a[22:23], v33 offset:60096\n" \
"v_mfma_f32_16x16x32_bf16 v[144:147], a[28:31], a[36:39], v[144:147]\n" \
"v_mfma_f32_16x16x32_bf16 v[148:151], a[24:27], a[48:51], v[148:151]\n" \
"ds_read_b64_tr_b16 a[24:25], v10 offset:192\n" \
"ds_read_b64_tr_b16 a[26:27], v10 offset:4800\n" \
"buffer_load_dwordx4 a[116:119], v32, s[56:59], s93 offen offset:1024 sc1\n" \
"v_mfma_f32_16x16x32_bf16 v[148:151], a[28:31], a[52:55], v[148:151]\n" \
"ds_read_b64_tr_b16 a[28:29], v34 offset:192\n" \
"s_waitcnt lgkmcnt(13)\n" \
"v_mfma_f32_16x16x32_bf16 v[148:151], a[192:195], a[56:59], v[148:151]\n" \
"ds_read_b64_tr_b16 a[30:31], v34 offset:4800\n" \
"v_mfma_f32_16x16x32_bf16 v[148:151], a[196:199], a[60:63], v[148:151]\n" \
"v_mfma_f32_16x16x32_bf16 v[144:147], a[192:195], a[40:43], v[144:147]\n" \
"ds_read_b64_tr_b16 a[192:193], v34 offset:9408\n" \
"ds_read_b64_tr_b16 a[194:195], v34 offset:14016\n" \
"buffer_load_dwordx4 a[120:123], v32, s[56:59], s93 offen offset:2048 sc1\n" \
"v_mfma_f32_16x16x32_bf16 v[144:147], a[196:199], a[44:47], v[144:147]\n" \
"ds_read_b64_tr_b16 a[196:197], v34 offset:18624\n" \
"ds_read_b64_tr_b16 a[198:199], v34 offset:23232\n" \
"v_mfma_f32_16x16x32_bf16 v[152:155], a[200:203], a[128:131], v[152:155]\n" \
"v_mfma_f32_16x16x32_bf16 v[152:155], a[204:207], a[132:135], v[152:155]\n" \
"v_mfma_f32_16x16x32_bf16 v[156:159], a[200:203], a[144:147], v[156:159]\n" \
"ds_read_b64_tr_b16 a[200:201], v10 offset:9408\n" \
"ds_read_b64_tr_b16 a[202:203], v10 offset:14016\n" \
"buffer_load_dwordx4 a[124:127], v32, s[56:59], s93 offen offset:3072 sc1\n" \
"v_cvt_pk_bf16_f32 v144, v144, v145\n" \
"v_cvt_pk_bf16_f32 v145, v146, v147\n" \
"v_cvt_pk_bf16_f32 v146, v148, v149\n" \
"v_cvt_pk_bf16_f32 v147, v150, v151\n" \
"v_mfma_f32_16x16x32_bf16 v[156:159], a[204:207], a[148:151], v[156:159]\n" \
"ds_read_b64_tr_b16 a[204:205], v33 offset:256\n" \
"ds_read_b64_tr_b16 a[206:207], v33 offset:4864\n" \
"buffer_store_dwordx4 v[144:147], v20, s[56:59], s69 offen offset:0 sc1\n" \
"s_add_u32 s69, s69, 0x1800\n" \
"v_mfma_f32_16x16x32_bf16 v[156:159], a[208:211], a[152:155], v[156:159]\n" \
"v_mfma_f32_16x16x32_bf16 v[156:159], a[212:215], a[156:159], v[156:159]\n" \
"v_mfma_f32_16x16x32_bf16 v[152:155], a[208:211], a[136:139], v[152:155]\n" \
"ds_read_b64_tr_b16 a[208:209], v33 offset:9472\n" \
"ds_read_b64_tr_b16 a[210:211], v33 offset:14080\n" \
"v_mfma_f32_16x16x32_bf16 v[152:155], a[212:215], a[140:143], v[152:155]\n" \
"buffer_load_dwordx4 a[104:107], v20, s[56:59], s93 offen offset:2048 sc1\n" \
"v_mfma_f32_16x16x32_bf16 v[152:155], a[216:219], a[32:35], v[152:155]\n" \
"ds_read_b64_tr_b16 a[212:213], v33 offset:18688\n" \
"ds_read_b64_tr_b16 a[214:215], v33 offset:23296\n" \
"v_mfma_f32_16x16x32_bf16 v[152:155], a[0:3], a[36:39], v[152:155]\n" \
"v_mfma_f32_16x16x32_bf16 v[156:159], a[216:219], a[48:51], v[156:159]\n" \
"ds_read_b64_tr_b16 a[216:217], v33 offset:27904\n" \
"ds_read_b64_tr_b16 a[218:219], v33 offset:32512\n" \
"v_mfma_f32_16x16x32_bf16 v[156:159], a[0:3], a[52:55], v[156:159]\n" \
"ds_read_b64_tr_b16 a[0:1], v33 offset:37120\n" \
"buffer_load_dwordx4 a[108:111], v20, s[56:59], s93 offen offset:3072 sc1\n" \
"s_waitcnt lgkmcnt(11)\n" \
"v_mfma_f32_16x16x32_bf16 v[156:159], a[4:7], a[56:59], v[156:159]\n" \
"ds_read_b64_tr_b16 a[2:3], v33 offset:41728\n" \
"v_mfma_f32_16x16x32_bf16 v[156:159], a[8:11], a[60:63], v[156:159]\n" \
"v_mfma_f32_16x16x32_bf16 v[152:155], a[4:7], a[40:43], v[152:155]\n" \
"ds_read_b64_tr_b16 a[4:5], v33 offset:46336\n" \
"ds_read_b64_tr_b16 a[6:7], v33 offset:50944\n" \
"v_mfma_f32_16x16x32_bf16 v[152:155], a[8:11], a[44:47], v[152:155]\n" \
"ds_read_b64_tr_b16 a[8:9], v33 offset:55552\n" \
"ds_read_b64_tr_b16 a[10:11], v33 offset:60160\n" \
"v_mfma_f32_16x16x32_bf16 v[160:163], a[12:15], a[128:131], v[160:163]\n" \
"buffer_load_dwordx4 v[64:67], v20, s[56:59], s95 offen offset:0 sc1\n" \
"v_mfma_f32_16x16x32_bf16 v[160:163], a[16:19], a[132:135], v[160:163]\n" \
"v_mfma_f32_16x16x32_bf16 v[164:167], a[12:15], a[144:147], v[164:167]\n" \
"ds_read_b64_tr_b16 a[12:13], v10 offset:256\n" \
"ds_read_b64_tr_b16 a[14:15], v10 offset:4864\n" \
"v_cvt_pk_bf16_f32 v152, v152, v153\n" \
"v_cvt_pk_bf16_f32 v153, v154, v155\n" \
"v_cvt_pk_bf16_f32 v154, v156, v157\n" \
"v_cvt_pk_bf16_f32 v155, v158, v159\n" \
"buffer_store_dwordx4 v[152:155], v20, s[56:59], s70 offen offset:0 sc1\n" \
"s_add_u32 s70, s70, 0x1000\n" \
"v_mfma_f32_16x16x32_bf16 v[164:167], a[16:19], a[148:151], v[164:167]\n" \
"ds_read_b64_tr_b16 a[16:17], v34 offset:256\n" \
"ds_read_b64_tr_b16 a[18:19], v34 offset:4864\n" \
"v_mfma_f32_16x16x32_bf16 v[164:167], a[20:23], a[152:155], v[164:167]\n" \
"v_mfma_f32_16x16x32_bf16 v[164:167], a[24:27], a[156:159], v[164:167]\n" \
"v_mfma_f32_16x16x32_bf16 v[160:163], a[20:23], a[136:139], v[160:163]\n" \
"ds_read_b64_tr_b16 a[20:21], v34 offset:9472\n" \
"ds_read_b64_tr_b16 a[22:23], v34 offset:14080\n" \
"v_mfma_f32_16x16x32_bf16 v[160:163], a[24:27], a[140:143], v[160:163]\n" \
"ds_read_b64_tr_b16 a[24:25], v34 offset:18688\n" \
"ds_read_b64_tr_b16 a[26:27], v34 offset:23296\n" \
"s_waitcnt vmcnt(19)\n" \
"s_mov_b64 exec, 1\n" \
"buffer_store_dword v39, off, s[56:59], s65 offset:0 sc1\n" \
"s_mov_b64 exec, -1\n" \
"v_mfma_f32_16x16x32_bf16 v[160:163], a[28:31], a[32:35], v[160:163]\n" \
"v_mfma_f32_16x16x32_bf16 v[160:163], a[192:195], a[36:39], v[160:163]\n" \
"v_mfma_f32_16x16x32_bf16 v[164:167], a[28:31], a[48:51], v[164:167]\n" \
"ds_read_b64_tr_b16 a[28:29], v10 offset:9472\n" \
"ds_read_b64_tr_b16 a[30:31], v10 offset:14080\n" \
"v_mfma_f32_16x16x32_bf16 v[164:167], a[192:195], a[52:55], v[164:167]\n" \
"ds_read_b64_tr_b16 a[192:193], v33 offset:0\n" \
"ds_read_b64_tr_b16 a[194:195], v33 offset:4608\n" \
"v_mfma_f32_16x16x32_bf16 v[164:167], a[196:199], a[56:59], v[164:167]\n" \
"s_waitcnt lgkmcnt(12)\n" \
"v_mfma_f32_16x16x32_bf16 v[164:167], a[200:203], a[60:63], v[164:167]\n" \
"buffer_load_dwordx4 v[68:71], v20, s[56:59], s95 offen offset:1024 sc1\n" \
"v_mfma_f32_16x16x32_bf16 v[160:163], a[196:199], a[40:43], v[160:163]\n" \
"ds_read_b64_tr_b16 a[196:197], v33 offset:9216\n" \
"ds_read_b64_tr_b16 a[198:199], v33 offset:13824\n" \
"v_mfma_f32_16x16x32_bf16 v[160:163], a[200:203], a[44:47], v[160:163]\n" \
"ds_read_b64_tr_b16 a[200:201], v33 offset:18432\n" \
"ds_read_b64_tr_b16 a[202:203], v33 offset:23040\n" \
"v_mfma_f32_16x16x32_bf16 v[248:251], a[204:207], a[220:223], v[248:251]\n" \
"v_mfma_f32_16x16x32_bf16 v[248:251], a[208:211], a[224:227], v[248:251]\n" \
"v_mfma_f32_16x16x32_bf16 v[252:255], a[204:207], a[236:239], v[252:255]\n" \
"ds_read_b64_tr_b16 a[204:205], v33 offset:27648\n" \
"ds_read_b64_tr_b16 a[206:207], v33 offset:32256\n" \
"v_mfma_f32_16x16x32_bf16 v[252:255], a[208:211], a[240:243], v[252:255]\n" \
"v_cvt_pk_bf16_f32 v160, v160, v161\n" \
"v_cvt_pk_bf16_f32 v161, v162, v163\n" \
"v_cvt_pk_bf16_f32 v162, v164, v165\n" \
"v_cvt_pk_bf16_f32 v163, v166, v167\n" \
"buffer_store_dwordx4 v[160:163], v20, s[56:59], s71 offen offset:0 sc1\n" \
"s_add_u32 s71, s71, 0x800\n" \
"v_mfma_f32_16x16x32_bf16 v[252:255], a[212:215], a[244:247], v[252:255]\n" \
"ds_read_b64_tr_b16 a[208:209], v33 offset:36864\n" \
"ds_read_b64_tr_b16 a[210:211], v33 offset:41472\n" \
"v_mfma_f32_16x16x32_bf16 v[252:255], a[216:219], a[248:251], v[252:255]\n" \
"v_mfma_f32_16x16x32_bf16 v[248:251], a[212:215], a[228:231], v[248:251]\n" \
"ds_read_b64_tr_b16 a[212:213], v33 offset:46080\n" \
"ds_read_b64_tr_b16 a[214:215], v33 offset:50688\n" \
"s_waitcnt vmcnt(19)\n" \
"s_mov_b64 exec, 1\n" \
"buffer_store_dword v39, off, s[56:59], s66 offset:4 sc1\n" \
"s_mov_b64 exec, -1\n" \
"v_readlane_b32 s66, v11, 47\n" \
"v_mfma_f32_16x16x32_bf16 v[248:251], a[216:219], a[232:235], v[248:251]\n" \
"ds_read_b64_tr_b16 a[216:217], v33 offset:55296\n" \
"ds_read_b64_tr_b16 a[218:219], v33 offset:59904\n" \
"v_mfma_f32_16x16x32_bf16 v[176:179], a[0:3], a[128:131], v[176:179]\n" \
"v_mfma_f32_16x16x32_bf16 v[176:179], a[4:7], a[132:135], v[176:179]\n" \
"v_mfma_f32_16x16x32_bf16 v[180:183], a[0:3], a[144:147], v[180:183]\n" \
"buffer_load_dwordx4 v[72:75], v32, s[56:59], s95 offen offset:0 sc1\n" \
"v_mfma_f32_16x16x32_bf16 v[180:183], a[4:7], a[148:151], v[180:183]\n" \
"v_mfma_f32_16x16x32_bf16 v[248:251], a[0:3], a[160:163], v[248:251]\n" \
"v_mfma_f32_16x16x32_bf16 v[248:251], a[4:7], a[164:167], v[248:251]\n" \
"v_mfma_f32_16x16x32_bf16 v[252:255], a[0:3], a[176:179], v[252:255]\n" \
"ds_read_b64_tr_b16 a[0:1], v10 offset:0\n" \
"ds_read_b64_tr_b16 a[2:3], v10 offset:4608\n" \
"v_mfma_f32_16x16x32_bf16 v[252:255], a[4:7], a[180:183], v[252:255]\n" \
"ds_read_b64_tr_b16 a[4:5], v34 offset:0\n" \
"s_waitcnt vmcnt(20)\n" \
"v_mfma_f32_16x16x32_bf16 v[252:255], a[8:11], a[184:187], v[252:255]\n" \
"ds_read_b64_tr_b16 a[6:7], v34 offset:4608\n" \
"s_waitcnt vmcnt(18) lgkmcnt(10)\n" \
"v_mfma_f32_16x16x32_bf16 v[252:255], a[12:15], a[188:191], v[252:255]\n" \
"buffer_load_dwordx4 v[76:79], v32, s[56:59], s95 offen offset:1024 sc1\n" \
"v_mfma_f32_16x16x32_bf16 v[248:251], a[8:11], a[168:171], v[248:251]\n" \
"v_mfma_f32_16x16x32_bf16 v[248:251], a[12:15], a[172:175], v[248:251]\n" \
"v_mfma_f32_16x16x32_bf16 v[180:183], a[8:11], a[152:155], v[180:183]\n" \
"v_mfma_f32_16x16x32_bf16 v[180:183], a[12:15], a[156:159], v[180:183]\n" \
"v_mfma_f32_16x16x32_bf16 v[176:179], a[8:11], a[136:139], v[176:179]\n" \
"ds_read_b64_tr_b16 a[8:9], v34 offset:9216\n" \
"ds_read_b64_tr_b16 a[10:11], v34 offset:13824\n" \
"v_mfma_f32_16x16x32_bf16 v[176:179], a[12:15], a[140:143], v[176:179]\n" \
"ds_read_b64_tr_b16 a[12:13], v34 offset:18432\n" \
"ds_read_b64_tr_b16 a[14:15], v34 offset:23040\n" \
"s_waitcnt vmcnt(18)\n" \
"s_mov_b64 exec, 1\n" \
"buffer_store_dword v39, off, s[56:59], s67 offset:8 sc1\n" \
"s_mov_b64 exec, -1\n" \
"v_readlane_b32 s67, v11, 48\n" \
"v_mfma_f32_16x16x32_bf16 v[176:179], a[16:19], a[32:35], v[176:179]\n" \
"v_mfma_f32_16x16x32_bf16 v[176:179], a[20:23], a[36:39], v[176:179]\n" \
"v_mfma_f32_16x16x32_bf16 v[180:183], a[16:19], a[48:51], v[180:183]\n" \
"v_mfma_f32_16x16x32_bf16 v[180:183], a[20:23], a[52:55], v[180:183]\n" \
"s_waitcnt vmcnt(18)\n" \
"v_mfma_f32_16x16x32_bf16 v[248:251], a[16:19], a[96:99], v[248:251]\n" \
"s_waitcnt vmcnt(17)\n" \
"v_mfma_f32_16x16x32_bf16 v[248:251], a[20:23], a[100:103], v[248:251]\n" \
"s_waitcnt vmcnt(15)\n" \
"v_mfma_f32_16x16x32_bf16 v[252:255], a[16:19], a[112:115], v[252:255]\n" \
"ds_read_b64_tr_b16 a[16:17], v10 offset:9216\n" \
"s_mov_b64 exec, 1\n" \
"buffer_store_dword v39, off, s[56:59], s68 offset:12 sc1\n" \
"s_mov_b64 exec, -1\n" \
"s_waitcnt vmcnt(15)\n" \
"v_mfma_f32_16x16x32_bf16 v[252:255], a[20:23], a[116:119], v[252:255]\n" \
"v_readlane_b32 s68, v11, 49\n" \
"s_waitcnt vmcnt(14)\n" \
"v_mfma_f32_16x16x32_bf16 v[252:255], a[24:27], a[120:123], v[252:255]\n" \
"ds_read_b64_tr_b16 a[18:19], v10 offset:13824\n" \
"s_waitcnt vmcnt(13)\n" \
"v_mfma_f32_16x16x32_bf16 v[252:255], a[28:31], a[124:127], v[252:255]\n" \
"ds_read_b64_tr_b16 a[20:21], v33 offset:32\n" \
"ds_read_b64_tr_b16 a[22:23], v33 offset:4640\n" \
"buffer_load_dwordx4 v[80:83], v20, s[56:59], s96 offen offset:0 sc1\n" \
"s_waitcnt vmcnt(12)\n" \
"v_mfma_f32_16x16x32_bf16 v[248:251], a[24:27], a[104:107], v[248:251]\n" \
"s_mov_b64 exec, 1\n" \
"buffer_store_dword v39, off, s[56:59], s69 offset:16 sc1\n" \
"s_mov_b64 exec, -1\n" \
"v_readlane_b32 s69, v11, 50\n" \
".fill (L_ab + 40960 - .) / 4, 4, 0xBF800000\n" \
"L_ap10:\n" \
"s_cbranch_execz L_ap0\n" \
"s_waitcnt vmcnt(12)\n" \
"v_mfma_f32_16x16x32_bf16 v[248:251], a[28:31], a[108:111], v[248:251]\n" \
"v_mfma_f32_16x16x32_bf16 v[180:183], a[24:27], a[56:59], v[180:183]\n" \
"buffer_load_dwordx4 v[84:87], v20, s[56:59], s96 offen offset:1024 sc1\n" \
"v_mfma_f32_16x16x32_bf16 v[180:183], a[28:31], a[60:63], v[180:183]\n" \
"v_mfma_f32_16x16x32_bf16 v[176:179], a[24:27], a[40:43], v[176:179]\n" \
"ds_read_b64_tr_b16 a[24:25], v33 offset:9248\n" \
"ds_read_b64_tr_b16 a[26:27], v33 offset:13856\n" \
"v_mfma_f32_16x16x32_bf16 v[176:179], a[28:31], a[44:47], v[176:179]\n" \
"ds_read_b64_tr_b16 a[28:29], v33 offset:18464\n" \
"v_cvt_pk_bf16_f32 v248, v248, v249\n" \
"v_cvt_pk_bf16_f32 v249, v250, v251\n" \
"v_cvt_pk_bf16_f32 v250, v252, v253\n" \
"v_cvt_pk_bf16_f32 v251, v254, v255\n" \
"buffer_store_dwordx4 v[248:251], v20, s[56:59], s72 offen offset:1024 sc1\n" \
"v_mfma_f32_16x16x32_bf16 v[184:187], a[192:195], a[220:223], v[184:187]\n" \
"ds_read_b64_tr_b16 a[30:31], v33 offset:23072\n" \
"v_cvt_pk_bf16_f32 v176, v176, v177\n" \
"v_cvt_pk_bf16_f32 v177, v178, v179\n" \
"v_cvt_pk_bf16_f32 v178, v180, v181\n" \
"v_cvt_pk_bf16_f32 v179, v182, v183\n" \
"v_mfma_f32_16x16x32_bf16 v[184:187], a[196:199], a[224:227], v[184:187]\n" \
"buffer_store_dwordx4 v[176:179], v20, s[56:59], s72 offen offset:0 sc1\n" \
"v_mfma_f32_16x16x32_bf16 v[188:191], a[192:195], a[236:239], v[188:191]\n" \
"ds_read_b64_tr_b16 a[192:193], v33 offset:27680\n" \
"ds_read_b64_tr_b16 a[194:195], v33 offset:32288\n" \
"v_mfma_f32_16x16x32_bf16 v[188:191], a[196:199], a[240:243], v[188:191]\n" \
"ds_read_b64_tr_b16 a[196:197], v33 offset:36896\n" \
"ds_read_b64_tr_b16 a[198:199], v33 offset:41504\n" \
"v_mfma_f32_16x16x32_bf16 v[188:191], a[200:203], a[244:247], v[188:191]\n" \
"v_mfma_f32_16x16x32_bf16 v[188:191], a[204:207], a[248:251], v[188:191]\n" \
"v_mfma_f32_16x16x32_bf16 v[184:187], a[200:203], a[228:231], v[184:187]\n" \
"ds_read_b64_tr_b16 a[200:201], v33 offset:46112\n" \
"ds_read_b64_tr_b16 a[202:203], v33 offset:50720\n" \
"v_mfma_f32_16x16x32_bf16 v[184:187], a[204:207], a[232:235], v[184:187]\n" \
"ds_read_b64_tr_b16 a[204:205], v33 offset:55328\n" \
"s_waitcnt lgkmcnt(11)\n" \
"v_mfma_f32_16x16x32_bf16 v[184:187], a[208:211], a[160:163], v[184:187]\n" \
"buffer_load_dwordx4 v[88:91], v32, s[56:59], s96 offen offset:0 sc1\n" \
"v_mfma_f32_16x16x32_bf16 v[184:187], a[212:215], a[164:167], v[184:187]\n" \
"ds_read_b64_tr_b16 a[206:207], v33 offset:59936\n" \
"v_mfma_f32_16x16x32_bf16 v[188:191], a[208:211], a[176:179], v[188:191]\n" \
"ds_read_b64_tr_b16 a[208:209], v10 offset:32\n" \
"ds_read_b64_tr_b16 a[210:211], v10 offset:4640\n" \
"v_mfma_f32_16x16x32_bf16 v[188:191], a[212:215], a[180:183], v[188:191]\n" \
"ds_read_b64_tr_b16 a[212:213], v34 offset:32\n" \
"ds_read_b64_tr_b16 a[214:215], v34 offset:4640\n" \
"v_mfma_f32_16x16x32_bf16 v[188:191], a[216:219], a[184:187], v[188:191]\n" \
"v_mfma_f32_16x16x32_bf16 v[188:191], a[0:3], a[188:191], v[188:191]\n" \
"buffer_load_dwordx4 v[92:95], v32, s[56:59], s96 offen offset:1024 sc1\n" \
"v_mfma_f32_16x16x32_bf16 v[184:187], a[216:219], a[168:171], v[184:187]\n" \
"ds_read_b64_tr_b16 a[216:217], v34 offset:9248\n" \
"ds_read_b64_tr_b16 a[218:219], v34 offset:13856\n" \
"v_mfma_f32_16x16x32_bf16 v[184:187], a[0:3], a[172:175], v[184:187]\n" \
"ds_read_b64_tr_b16 a[0:1], v34 offset:18464\n" \
"ds_read_b64_tr_b16 a[2:3], v34 offset:23072\n" \
"v_mfma_f32_16x16x32_bf16 v[184:187], a[4:7], a[96:99], v[184:187]\n" \
"v_mfma_f32_16x16x32_bf16 v[184:187], a[8:11], a[100:103], v[184:187]\n" \
"v_mfma_f32_16x16x32_bf16 v[188:191], a[4:7], a[112:115], v[188:191]\n" \
"ds_read_b64_tr_b16 a[4:5], v10 offset:9248\n" \
"ds_read_b64_tr_b16 a[6:7], v10 offset:13856\n" \
"v_mfma_f32_16x16x32_bf16 v[188:191], a[8:11], a[116:119], v[188:191]\n" \
"ds_read_b64_tr_b16 a[8:9], v33 offset:64\n" \
"ds_read_b64_tr_b16 a[10:11], v33 offset:4672\n" \
"v_mfma_f32_16x16x32_bf16 v[188:191], a[12:15], a[120:123], v[188:191]\n" \
"buffer_load_dwordx4 v[96:99], v20, s[56:59], s97 offen offset:0 sc1\n" \
"v_mfma_f32_16x16x32_bf16 v[188:191], a[16:19], a[124:127], v[188:191]\n" \
"v_mfma_f32_16x16x32_bf16 v[184:187], a[12:15], a[104:107], v[184:187]\n" \
"ds_read_b64_tr_b16 a[12:13], v33 offset:9280\n" \
"ds_read_b64_tr_b16 a[14:15], v33 offset:13888\n" \
"v_mfma_f32_16x16x32_bf16 v[184:187], a[16:19], a[108:111], v[184:187]\n" \
"buffer_load_dword v37, v40, s[56:59], s74 offen sc1\n" \
"v_mfma_f32_16x16x32_bf16 v[192:195], a[20:23], a[220:223], v[192:195]\n" \
"ds_read_b64_tr_b16 a[16:17], v33 offset:18496\n" \
"s_waitcnt lgkmcnt(13)\n" \
"v_mfma_f32_16x16x32_bf16 v[192:195], a[24:27], a[224:227], v[192:195]\n" \
"ds_read_b64_tr_b16 a[18:19], v33 offset:23104\n" \
"v_mfma_f32_16x16x32_bf16 v[196:199], a[20:23], a[236:239], v[196:199]\n" \
"ds_read_b64_tr_b16 a[20:21], v33 offset:27712\n" \
"v_cvt_pk_bf16_f32 v184, v184, v185\n" \
"v_cvt_pk_bf16_f32 v185, v186, v187\n" \
"v_cvt_pk_bf16_f32 v186, v188, v189\n" \
"v_cvt_pk_bf16_f32 v187, v190, v191\n" \
"buffer_store_dwordx4 v[184:187], v20, s[56:59], s92 offen offset:1024 sc1\n" \
"s_waitcnt vmcnt(18)\n" \
"s_mov_b64 exec, 1\n" \
"buffer_store_dword v39, off, s[56:59], s70 offset:20 sc1\n" \
"s_mov_b64 exec, -1\n" \
"s_add_u32 s92, s92, 0x3800\n" \
"v_mfma_f32_16x16x32_bf16 v[196:199], a[24:27], a[240:243], v[196:199]\n" \
"ds_read_b64_tr_b16 a[22:23], v33 offset:32320\n" \
"ds_read_b64_tr_b16 a[24:25], v33 offset:36928\n" \
"v_mfma_f32_16x16x32_bf16 v[196:199], a[28:31], a[244:247], v[196:199]\n" \
"ds_read_b64_tr_b16 a[26:27], v33 offset:41536\n" \
"v_mfma_f32_16x16x32_bf16 v[196:199], a[192:195], a[248:251], v[196:199]\n" \
"v_mfma_f32_16x16x32_bf16 v[192:195], a[28:31], a[228:231], v[192:195]\n" \
"ds_read_b64_tr_b16 a[28:29], v33 offset:46144\n" \
"ds_read_b64_tr_b16 a[30:31], v33 offset:50752\n" \
"v_mfma_f32_16x16x32_bf16 v[192:195], a[192:195], a[232:235], v[192:195]\n" \
"buffer_load_dwordx4 v[100:103], v20, s[56:59], s97 offen offset:1024 sc1\n" \
"v_mfma_f32_16x16x32_bf16 v[192:195], a[196:199], a[160:163], v[192:195]\n" \
"ds_read_b64_tr_b16 a[192:193], v33 offset:55360\n" \
"ds_read_b64_tr_b16 a[194:195], v33 offset:59968\n" \
"v_mfma_f32_16x16x32_bf16 v[192:195], a[200:203], a[164:167], v[192:195]\n" \
"v_mfma_f32_16x16x32_bf16 v[196:199], a[196:199], a[176:179], v[196:199]\n" \
"ds_read_b64_tr_b16 a[196:197], v10 offset:64\n" \
"ds_read_b64_tr_b16 a[198:199], v10 offset:4672\n" \
"v_mfma_f32_16x16x32_bf16 v[196:199], a[200:203], a[180:183], v[196:199]\n" \
"ds_read_b64_tr_b16 a[200:201], v34 offset:64\n" \
"ds_read_b64_tr_b16 a[202:203], v34 offset:4672\n" \
"v_mfma_f32_16x16x32_bf16 v[196:199], a[204:207], a[184:187], v[196:199]\n" \
"buffer_load_dwordx4 v[104:107], v32, s[56:59], s97 offen offset:0 sc1\n" \
"v_mfma_f32_16x16x32_bf16 v[196:199], a[208:211], a[188:191], v[196:199]\n" \
"v_mfma_f32_16x16x32_bf16 v[192:195], a[204:207], a[168:171], v[192:195]\n" \
"ds_read_b64_tr_b16 a[204:205], v34 offset:9280\n" \
"ds_read_b64_tr_b16 a[206:207], v34 offset:13888\n" \
"v_mfma_f32_16x16x32_bf16 v[192:195], a[208:211], a[172:175], v[192:195]\n" \
"ds_read_b64_tr_b16 a[208:209], v34 offset:18496\n" \
"s_waitcnt lgkmcnt(14)\n" \
"v_mfma_f32_16x16x32_bf16 v[192:195], a[212:215], a[96:99], v[192:195]\n" \
"ds_read_b64_tr_b16 a[210:211], v34 offset:23104\n" \
"v_mfma_f32_16x16x32_bf16 v[192:195], a[216:219], a[100:103], v[192:195]\n" \
"buffer_load_dwordx4 v[108:111], v32, s[56:59], s97 offen offset:1024 sc1\n" \
"v_mfma_f32_16x16x32_bf16 v[196:199], a[212:215], a[112:115], v[196:199]\n" \
"ds_read_b64_tr_b16 a[212:213], v10 offset:9280\n" \
"ds_read_b64_tr_b16 a[214:215], v10 offset:13888\n" \
"v_mfma_f32_16x16x32_bf16 v[196:199], a[216:219], a[116:119], v[196:199]\n" \
"ds_read_b64_tr_b16 a[216:217], v33 offset:96\n" \
"ds_read_b64_tr_b16 a[218:219], v33 offset:4704\n" \
"s_waitcnt vmcnt(19)\n" \
"s_mov_b64 exec, 1\n" \
"buffer_store_dword v39, off, s[56:59], s71 offset:24 sc1\n" \
"s_mov_b64 exec, -1\n" \
"v_mfma_f32_16x16x32_bf16 v[196:199], a[0:3], a[120:123], v[196:199]\n" \
"v_mfma_f32_16x16x32_bf16 v[196:199], a[4:7], a[124:127], v[196:199]\n" \
"v_mfma_f32_16x16x32_bf16 v[192:195], a[0:3], a[104:107], v[192:195]\n" \
"ds_read_b64_tr_b16 a[0:1], v33 offset:9312\n" \
"ds_read_b64_tr_b16 a[2:3], v33 offset:13920\n" \
"v_mfma_f32_16x16x32_bf16 v[192:195], a[4:7], a[108:111], v[192:195]\n" \
"ds_read_b64_tr_b16 a[4:5], v33 offset:18528\n" \
"ds_read_b64_tr_b16 a[6:7], v33 offset:23136\n" \
"v_mfma_f32_16x16x32_bf16 v[200:203], a[8:11], a[220:223], v[200:203]\n" \
"v_mfma_f32_16x16x32_bf16 v[200:203], a[12:15], a[224:227], v[200:203]\n" \
"v_mfma_f32_16x16x32_bf16 v[204:207], a[8:11], a[236:239], v[204:207]\n" \
"ds_read_b64_tr_b16 a[8:9], v33 offset:27744\n" \
"ds_read_b64_tr_b16 a[10:11], v33 offset:32352\n" \
"v_mfma_f32_16x16x32_bf16 v[204:207], a[12:15], a[240:243], v[204:207]\n" \
"v_cvt_pk_bf16_f32 v192, v192, v193\n" \
"v_cvt_pk_bf16_f32 v193, v194, v195\n" \
"v_cvt_pk_bf16_f32 v194, v196, v197\n" \
"v_cvt_pk_bf16_f32 v195, v198, v199\n" \
"buffer_store_dwordx4 v[192:195], v20, s[56:59], s66 offen offset:1024 sc1\n" \
"v_mfma_f32_16x16x32_bf16 v[204:207], a[16:19], a[244:247], v[204:207]\n" \
"ds_read_b64_tr_b16 a[12:13], v33 offset:36960\n" \
"s_waitcnt lgkmcnt(11)\n" \
"v_mfma_f32_16x16x32_bf16 v[204:207], a[20:23], a[248:251], v[204:207]\n" \
"ds_read_b64_tr_b16 a[14:15], v33 offset:41568\n" \
"v_mfma_f32_16x16x32_bf16 v[200:203], a[16:19], a[228:231], v[200:203]\n" \
"ds_read_b64_tr_b16 a[16:17], v33 offset:46176\n" \
"ds_read_b64_tr_b16 a[18:19], v33 offset:50784\n" \
"v_mfma_f32_16x16x32_bf16 v[200:203], a[20:23], a[232:235], v[200:203]\n" \
"ds_read_b64_tr_b16 a[20:21], v33 offset:55392\n" \
"ds_read_b64_tr_b16 a[22:23], v33 offset:60000\n" \
"v_mfma_f32_16x16x32_bf16 v[200:203], a[24:27], a[160:163], v[200:203]\n" \
"v_mfma_f32_16x16x32_bf16 v[200:203], a[28:31], a[164:167], v[200:203]\n" \
"v_mfma_f32_16x16x32_bf16 v[204:207], a[24:27], a[176:179], v[204:207]\n" \
"ds_read_b64_tr_b16 a[24:25], v10 offset:96\n" \
"ds_read_b64_tr_b16 a[26:27], v10 offset:4704\n" \
"v_mfma_f32_16x16x32_bf16 v[204:207], a[28:31], a[180:183], v[204:207]\n" \
"ds_read_b64_tr_b16 a[28:29], v34 offset:96\n" \
"ds_read_b64_tr_b16 a[30:31], v34 offset:4704\n" \
"v_mfma_f32_16x16x32_bf16 v[204:207], a[192:195], a[184:187], v[204:207]\n" \
"v_mfma_f32_16x16x32_bf16 v[204:207], a[196:199], a[188:191], v[204:207]\n" \
"v_mfma_f32_16x16x32_bf16 v[200:203], a[192:195], a[168:171], v[200:203]\n" \
"ds_read_b64_tr_b16 a[192:193], v34 offset:9312\n" \
"ds_read_b64_tr_b16 a[194:195], v34 offset:13920\n" \
"v_mfma_f32_16x16x32_bf16 v[200:203], a[196:199], a[172:175], v[200:203]\n" \
"ds_read_b64_tr_b16 a[196:197], v34 offset:18528\n" \
"ds_read_b64_tr_b16 a[198:199], v34 offset:23136\n" \
"v_mfma_f32_16x16x32_bf16 v[200:203], a[200:203], a[96:99], v[200:203]\n" \
"v_mfma_f32_16x16x32_bf16 v[200:203], a[204:207], a[100:103], v[200:203]\n" \
"v_mfma_f32_16x16x32_bf16 v[204:207], a[200:203], a[112:115], v[204:207]\n" \
"ds_read_b64_tr_b16 a[200:201], v10 offset:9312\n" \
"ds_read_b64_tr_b16 a[202:203], v10 offset:13920\n" \
"v_mfma_f32_16x16x32_bf16 v[204:207], a[204:207], a[116:119], v[204:207]\n" \
"ds_read_b64_tr_b16 a[204:205], v33 offset:128\n" \
"ds_read_b64_tr_b16 a[206:207], v33 offset:4736\n" \
"v_mfma_f32_16x16x32_bf16 v[204:207], a[208:211], a[120:123], v[204:207]\n" \
"s_waitcnt lgkmcnt(15)\n" \
"v_mfma_f32_16x16x32_bf16 v[204:207], a[212:215], a[124:127], v[204:207]\n" \
"v_mfma_f32_16x16x32_bf16 v[200:203], a[208:211], a[104:107], v[200:203]\n" \
"ds_read_b64_tr_b16 a[208:209], v33 offset:9344\n" \
"ds_read_b64_tr_b16 a[210:211], v33 offset:13952\n" \
"v_mfma_f32_16x16x32_bf16 v[200:203], a[212:215], a[108:111], v[200:203]\n" \
"ds_read_b64_tr_b16 a[212:213], v33 offset:18560\n" \
"ds_read_b64_tr_b16 a[214:215], v33 offset:23168\n" \
"v_mfma_f32_16x16x32_bf16 v[208:211], a[216:219], a[220:223], v[208:211]\n" \
"v_mfma_f32_16x16x32_bf16 v[208:211], a[0:3], a[224:227], v[208:211]\n" \
"v_mfma_f32_16x16x32_bf16 v[212:215], a[216:219], a[236:239], v[212:215]\n" \
"ds_read_b64_tr_b16 a[216:217], v33 offset:27776\n" \
"ds_read_b64_tr_b16 a[218:219], v33 offset:32384\n" \
"v_mfma_f32_16x16x32_bf16 v[212:215], a[0:3], a[240:243], v[212:215]\n" \
"v_cvt_pk_bf16_f32 v200, v200, v201\n" \
"v_cvt_pk_bf16_f32 v201, v202, v203\n" \
"v_cvt_pk_bf16_f32 v202, v204, v205\n" \
"v_cvt_pk_bf16_f32 v203, v206, v207\n" \
"buffer_store_dwordx4 v[200:203], v20, s[56:59], s67 offen offset:1024 sc1\n" \
"v_mfma_f32_16x16x32_bf16 v[212:215], a[4:7], a[244:247], v[212:215]\n" \
"ds_read_b64_tr_b16 a[0:1], v33 offset:36992\n" \
"ds_read_b64_tr_b16 a[2:3], v33 offset:41600\n" \
"v_mfma_f32_16x16x32_bf16 v[212:215], a[8:11], a[248:251], v[212:215]\n" \
"v_mfma_f32_16x16x32_bf16 v[208:211], a[4:7], a[228:231], v[208:211]\n" \
"ds_read_b64_tr_b16 a[4:5], v33 offset:46208\n" \
"ds_read_b64_tr_b16 a[6:7], v33 offset:50816\n" \
"v_mfma_f32_16x16x32_bf16 v[208:211], a[8:11], a[232:235], v[208:211]\n" \
"ds_read_b64_tr_b16 a[8:9], v33 offset:55424\n" \
"ds_read_b64_tr_b16 a[10:11], v33 offset:60032\n" \
"v_mfma_f32_16x16x32_bf16 v[208:211], a[12:15], a[160:163], v[208:211]\n" \
"s_waitcnt lgkmcnt(13)\n" \
"v_mfma_f32_16x16x32_bf16 v[208:211], a[16:19], a[164:167], v[208:211]\n" \
"v_mfma_f32_16x16x32_bf16 v[212:215], a[12:15], a[176:179], v[212:215]\n" \
"s_waitcnt vmcnt(8)\n" \
"v_cmp_eq_u32_e64 s[98:99], s62, v37\n" \
"s_mov_b64 exec, 3\n" \
"buffer_store_dword v39, v41, s[56:59], s73 offen sc1\n" \
"s_mov_b64 exec, -1\n" \
"s_andn2_b32 s75, 0x7f, s98\n" \
"s_mov_b32 s20, s75\n" \
"s_bitcmp1_b32 s20, 0\n" \
"s_cselect_b32 s93, 0x40000000, s12\n" \
"s_bitcmp1_b32 s20, 1\n" \
"s_cselect_b32 s94, 0x40000000, s12\n" \
"s_bitcmp1_b32 s20, 2\n" \
"s_cselect_b32 s95, 0x40000000, s13\n" \
"s_bitcmp1_b32 s20, 3\n" \
"s_cselect_b32 s96, 0x40000000, s13\n" \
"s_bitcmp1_b32 s20, 4\n" \
"s_cselect_b32 s97, 0x40000000, s14\n" \
"s_bitcmp1_b32 s20, 5\n" \
"s_cselect_b32 s64, 0x40000000, s14\n" \
"s_bitcmp1_b32 s20, 6\n" \
"s_cselect_b32 s65, 0x40000000, s15\n" \
"s_add_u32 s66, s66, 0x3000\n" \
"s_add_u32 s67, s67, 0x2800\n" \
"v_mfma_f32_16x16x32_bf16 v[212:215], a[16:19], a[180:183], v[212:215]\n" \
"buffer_load_dwordx4 v[116:119], v20, s[56:59], s93 offen offset:0 sc1\n" \
"v_mfma_f32_16x16x32_bf16 v[212:215], a[20:23], a[184:187], v[212:215]\n" \
"ds_read_b64_tr_b16 a[12:13], v10 offset:128\n" \
"ds_read_b64_tr_b16 a[14:15], v10 offset:4736\n" \
"v_mfma_f32_16x16x32_bf16 v[212:215], a[24:27], a[188:191], v[212:215]\n" \
"ds_read_b64_tr_b16 a[16:17], v34 offset:128\n" \
"ds_read_b64_tr_b16 a[18:19], v34 offset:4736\n" \
"v_mfma_f32_16x16x32_bf16 v[208:211], a[20:23], a[168:171], v[208:211]\n" \
"ds_read_b64_tr_b16 a[20:21], v34 offset:9344\n" \
"ds_read_b64_tr_b16 a[22:23], v34 offset:13952\n" \
"v_mfma_f32_16x16x32_bf16 v[208:211], a[24:27], a[172:175], v[208:211]\n" \
"ds_read_b64_tr_b16 a[24:25], v34 offset:18560\n" \
"ds_read_b64_tr_b16 a[26:27], v34 offset:23168\n" \
"v_mfma_f32_16x16x32_bf16 v[208:211], a[28:31], a[96:99], v[208:211]\n" \
"v_mfma_f32_16x16x32_bf16 v[208:211], a[192:195], a[100:103], v[208:211]\n" \
"v_mfma_f32_16x16x32_bf16 v[212:215], a[28:31], a[112:115], v[212:215]\n" \
"ds_read_b64_tr_b16 a[28:29], v10 offset:9344\n" \
"ds_read_b64_tr_b16 a[30:31], v10 offset:13952\n" \
"v_mfma_f32_16x16x32_bf16 v[212:215], a[192:195], a[116:119], v[212:215]\n" \
"ds_read_b64_tr_b16 a[192:193], v33 offset:160\n" \
"ds_read_b64_tr_b16 a[194:195], v33 offset:4768\n" \
"v_mfma_f32_16x16x32_bf16 v[212:215], a[196:199], a[120:123], v[212:215]\n" \
"v_mfma_f32_16x16x32_bf16 v[212:215], a[200:203], a[124:127], v[212:215]\n" \
"v_mfma_f32_16x16x32_bf16 v[208:211], a[196:199], a[104:107], v[208:211]\n" \
"ds_read_b64_tr_b16 a[196:197], v33 offset:9376\n" \
"ds_read_b64_tr_b16 a[198:199], v33 offset:13984\n" \
"v_mfma_f32_16x16x32_bf16 v[208:211], a[200:203], a[108:111], v[208:211]\n" \
"ds_read_b64_tr_b16 a[200:201], v33 offset:18592\n" \
"s_waitcnt lgkmcnt(15)\n" \
"v_mfma_f32_16x16x32_bf16 v[216:219], a[204:207], a[220:223], v[216:219]\n" \
"ds_read_b64_tr_b16 a[202:203], v33 offset:23200\n" \
"v_mfma_f32_16x16x32_bf16 v[216:219], a[208:211], a[224:227], v[216:219]\n" \
"buffer_load_dwordx4 v[124:127], v20, s[56:59], s94 offen offset:2048 sc1\n" \
"v_mfma_f32_16x16x32_bf16 v[220:223], a[204:207], a[236:239], v[220:223]\n" \
"ds_read_b64_tr_b16 a[204:205], v33 offset:27808\n" \
"v_cvt_pk_bf16_f32 v208, v208, v209\n" \
"v_cvt_pk_bf16_f32 v209, v210, v211\n" \
"v_cvt_pk_bf16_f32 v210, v212, v213\n" \
"v_cvt_pk_bf16_f32 v211, v214, v215\n" \
"v_mfma_f32_16x16x32_bf16 v[220:223], a[208:211], a[240:243], v[220:223]\n" \
"buffer_store_dwordx4 v[208:211], v20, s[56:59], s68 offen offset:1024 sc1\n" \
"s_add_u32 s68, s68, 0x2000\n" \
"v_mfma_f32_16x16x32_bf16 v[220:223], a[212:215], a[244:247], v[220:223]\n" \
"ds_read_b64_tr_b16 a[206:207], v33 offset:32416\n" \
"ds_read_b64_tr_b16 a[208:209], v33 offset:37024\n" \
"v_mfma_f32_16x16x32_bf16 v[220:223], a[216:219], a[248:251], v[220:223]\n" \
"ds_read_b64_tr_b16 a[210:211], v33 offset:41632\n" \
"v_mfma_f32_16x16x32_bf16 v[216:219], a[212:215], a[228:231], v[216:219]\n" \
"ds_read_b64_tr_b16 a[212:213], v33 offset:46240\n" \
"ds_read_b64_tr_b16 a[214:215], v33 offset:50848\n" \
"v_mfma_f32_16x16x32_bf16 v[216:219], a[216:219], a[232:235], v[216:219]\n" \
"ds_read_b64_tr_b16 a[216:217], v33 offset:55456\n" \
"ds_read_b64_tr_b16 a[218:219], v33 offset:60064\n" \
"v_mfma_f32_16x16x32_bf16 v[216:219], a[0:3], a[160:163], v[216:219]\n" \
"v_mfma_f32_16x16x32_bf16 v[216:219], a[4:7], a[164:167], v[216:219]\n" \
"v_mfma_f32_16x16x32_bf16 v[220:223], a[0:3], a[176:179], v[220:223]\n" \
"ds_read_b64_tr_b16 a[0:1], v10 offset:160\n" \
"ds_read_b64_tr_b16 a[2:3], v10 offset:4768\n" \
"v_mfma_f32_16x16x32_bf16 v[220:223], a[4:7], a[180:183], v[220:223]\n" \
"ds_read_b64_tr_b16 a[4:5], v34 offset:160\n" \
"ds_read_b64_tr_b16 a[6:7], v34 offset:4768\n" \
"v_mfma_f32_16x16x32_bf16 v[220:223], a[8:11], a[184:187], v[220:223]\n" \
"s_waitcnt lgkmcnt(13)\n" \
"v_mfma_f32_16x16x32_bf16 v[220:223], a[12:15], a[188:191], v[220:223]\n" \
"s_waitcnt vmcnt(11)\n" \
"s_mov_b64 exec, 1\n" \
"buffer_store_dword v39, off, s[56:59], s92 offset:32 sc1\n" \
"s_mov_b64 exec, -1\n" \
"v_mfma_f32_16x16x32_bf16 v[216:219], a[8:11], a[168:171], v[216:219]\n" \
"ds_read_b64_tr_b16 a[8:9], v34 offset:9376\n" \
"ds_read_b64_tr_b16 a[10:11], v34 offset:13984\n" \
"v_mfma_f32_16x16x32_bf16 v[216:219], a[12:15], a[172:175], v[216:219]\n" \
"ds_read_b64_tr_b16 a[12:13], v34 offset:18592\n" \
"ds_read_b64_tr_b16 a[14:15], v34 offset:23200\n" \
"v_mfma_f32_16x16x32_bf16 v[216:219], a[16:19], a[96:99], v[216:219]\n" \
"v_mfma_f32_16x16x32_bf16 v[216:219], a[20:23], a[100:103], v[216:219]\n" \
"buffer_load_dwordx4 v[132:135], v20, s[56:59], s95 offen offset:0 sc1\n" \
"v_mfma_f32_16x16x32_bf16 v[220:223], a[16:19], a[112:115], v[220:223]\n" \
"ds_read_b64_tr_b16 a[16:17], v10 offset:9376\n" \
"ds_read_b64_tr_b16 a[18:19], v10 offset:13984\n" \
"v_mfma_f32_16x16x32_bf16 v[220:223], a[20:23], a[116:119], v[220:223]\n" \
"ds_read_b64_tr_b16 a[20:21], v33 offset:192\n" \
"ds_read_b64_tr_b16 a[22:23], v33 offset:4800\n" \
"v_mfma_f32_16x16x32_bf16 v[220:223], a[24:27], a[120:123], v[220:223]\n" \
"v_mfma_f32_16x16x32_bf16 v[220:223], a[28:31], a[124:127], v[220:223]\n" \
"v_mfma_f32_16x16x32_bf16 v[216:219], a[24:27], a[104:107], v[216:219]\n" \
"ds_read_b64_tr_b16 a[24:25], v33 offset:9408\n" \
"ds_read_b64_tr_b16 a[26:27], v33 offset:14016\n" \
"v_mfma_f32_16x16x32_bf16 v[216:219], a[28:31], a[108:111], v[216:219]\n" \
"buffer_load_dword v37, v40, s[56:59], s74 offen sc1\n" \
"v_mfma_f32_16x16x32_bf16 v[224:227], a[192:195], a[220:223], v[224:227]\n" \
"ds_read_b64_tr_b16 a[28:29], v33 offset:18624\n" \
"ds_read_b64_tr_b16 a[30:31], v33 offset:23232\n" \
"v_mfma_f32_16x16x32_bf16 v[224:227], a[196:199], a[224:227], v[224:227]\n" \
"v_mfma_f32_16x16x32_bf16 v[228:231], a[192:195], a[236:239], v[228:231]\n" \
"ds_read_b64_tr_b16 a[192:193], v33 offset:27840\n" \
"ds_read_b64_tr_b16 a[194:195], v33 offset:32448\n" \
"v_cvt_pk_bf16_f32 v216, v216, v217\n" \
"v_cvt_pk_bf16_f32 v217, v218, v219\n" \
"v_cvt_pk_bf16_f32 v218, v220, v221\n" \
"v_cvt_pk_bf16_f32 v219, v222, v223\n" \
"buffer_store_dwordx4 v[216:219], v20, s[56:59], s69 offen offset:1024 sc1\n" \
"s_add_u32 s69, s69, 0x1800\n" \
"v_mfma_f32_16x16x32_bf16 v[228:231], a[196:199], a[240:243], v[228:231]\n" \
"ds_read_b64_tr_b16 a[196:197], v33 offset:37056\n" \
"s_waitcnt lgkmcnt(12)\n" \
"v_mfma_f32_16x16x32_bf16 v[228:231], a[200:203], a[244:247], v[228:231]\n" \
"ds_read_b64_tr_b16 a[198:199], v33 offset:41664\n" \
"v_mfma_f32_16x16x32_bf16 v[228:231], a[204:207], a[248:251], v[228:231]\n" \
"v_mfma_f32_16x16x32_bf16 v[224:227], a[200:203], a[228:231], v[224:227]\n" \
"buffer_load_dwordx4 v[140:143], v20, s[56:59], s96 offen offset:2048 sc1\n" \
"v_mfma_f32_16x16x32_bf16 v[224:227], a[204:207], a[232:235], v[224:227]\n" \
"ds_read_b64_tr_b16 a[200:201], v33 offset:46272\n" \
"ds_read_b64_tr_b16 a[202:203], v33 offset:50880\n" \
"v_mfma_f32_16x16x32_bf16 v[224:227], a[208:211], a[160:163], v[224:227]\n" \
"ds_read_b64_tr_b16 a[204:205], v33 offset:55488\n" \
"ds_read_b64_tr_b16 a[206:207], v33 offset:60096\n" \
"v_mfma_f32_16x16x32_bf16 v[224:227], a[212:215], a[164:167], v[224:227]\n" \
"v_mfma_f32_16x16x32_bf16 v[228:231], a[208:211], a[176:179], v[228:231]\n" \
"ds_read_b64_tr_b16 a[208:209], v10 offset:192\n" \
"ds_read_b64_tr_b16 a[210:211], v10 offset:4800\n" \
"v_mfma_f32_16x16x32_bf16 v[228:231], a[212:215], a[180:183], v[228:231]\n" \
"ds_read_b64_tr_b16 a[212:213], v34 offset:192\n" \
"ds_read_b64_tr_b16 a[214:215], v34 offset:4800\n" \
"v_mfma_f32_16x16x32_bf16 v[228:231], a[216:219], a[184:187], v[228:231]\n" \
"v_mfma_f32_16x16x32_bf16 v[228:231], a[0:3], a[188:191], v[228:231]\n" \
"v_mfma_f32_16x16x32_bf16 v[224:227], a[216:219], a[168:171], v[224:227]\n" \
"ds_read_b64_tr_b16 a[216:217], v34 offset:9408\n" \
"ds_read_b64_tr_b16 a[218:219], v34 offset:14016\n" \
"v_mfma_f32_16x16x32_bf16 v[224:227], a[0:3], a[172:175], v[224:227]\n" \
"ds_read_b64_tr_b16 a[0:1], v34 offset:18624\n" \
"ds_read_b64_tr_b16 a[2:3], v34 offset:23232\n" \
"s_waitcnt vmcnt(10)\n" \
"s_mov_b64 exec, 1\n" \
"buffer_store_dword v39, off, s[56:59], s66 offset:36 sc1\n" \
"s_mov_b64 exec, -1\n" \
"v_mfma_f32_16x16x32_bf16 v[224:227], a[4:7], a[96:99], v[224:227]\n" \
"v_mfma_f32_16x16x32_bf16 v[224:227], a[8:11], a[100:103], v[224:227]\n" \
"v_mfma_f32_16x16x32_bf16 v[228:231], a[4:7], a[112:115], v[228:231]\n" \
"ds_read_b64_tr_b16 a[4:5], v10 offset:9408\n" \
"ds_read_b64_tr_b16 a[6:7], v10 offset:14016\n" \
"v_mfma_f32_16x16x32_bf16 v[228:231], a[8:11], a[116:119], v[228:231]\n" \
"ds_read_b64_tr_b16 a[8:9], v33 offset:224\n" \
"s_waitcnt lgkmcnt(15)\n" \
"v_mfma_f32_16x16x32_bf16 v[228:231], a[12:15], a[120:123], v[228:231]\n" \
"buffer_load_dwordx4 v[148:151], v20, s[56:59], s97 offen offset:0 sc1\n" \
"v_mfma_f32_16x16x32_bf16 v[228:231], a[16:19], a[124:127], v[228:231]\n" \
"ds_read_b64_tr_b16 a[10:11], v33 offset:4832\n" \
"v_mfma_f32_16x16x32_bf16 v[224:227], a[12:15], a[104:107], v[224:227]\n" \
"ds_read_b64_tr_b16 a[12:13], v33 offset:9440\n" \
"ds_read_b64_tr_b16 a[14:15], v33 offset:14048\n" \
"v_mfma_f32_16x16x32_bf16 v[224:227], a[16:19], a[108:111], v[224:227]\n" \
"ds_read_b64_tr_b16 a[16:17], v33 offset:18656\n" \
"ds_read_b64_tr_b16 a[18:19], v33 offset:23264\n" \
"v_mfma_f32_16x16x32_bf16 v[232:235], a[20:23], a[220:223], v[232:235]\n" \
"v_mfma_f32_16x16x32_bf16 v[232:235], a[24:27], a[224:227], v[232:235]\n" \
"v_mfma_f32_16x16x32_bf16 v[236:239], a[20:23], a[236:239], v[236:239]\n" \
"ds_read_b64_tr_b16 a[20:21], v33 offset:27872\n" \
"ds_read_b64_tr_b16 a[22:23], v33 offset:32480\n" \
"v_mfma_f32_16x16x32_bf16 v[236:239], a[24:27], a[240:243], v[236:239]\n" \
"v_cvt_pk_bf16_f32 v224, v224, v225\n" \
"v_cvt_pk_bf16_f32 v225, v226, v227\n" \
"v_cvt_pk_bf16_f32 v226, v228, v229\n" \
"v_cvt_pk_bf16_f32 v227, v230, v231\n" \
"v_mfma_f32_16x16x32_bf16 v[236:239], a[28:31], a[244:247], v[236:239]\n" \
"ds_read_b64_tr_b16 a[24:25], v33 offset:37088\n" \
"ds_read_b64_tr_b16 a[26:27], v33 offset:41696\n" \
"v_mfma_f32_16x16x32_bf16 v[236:239], a[192:195], a[248:251], v[236:239]\n" \
"v_mfma_f32_16x16x32_bf16 v[232:235], a[28:31], a[228:231], v[232:235]\n" \
"ds_read_b64_tr_b16 a[28:29], v33 offset:46304\n" \
"ds_read_b64_tr_b16 a[30:31], v33 offset:50912\n" \
"v_mfma_f32_16x16x32_bf16 v[232:235], a[192:195], a[232:235], v[232:235]\n" \
"ds_read_b64_tr_b16 a[192:193], v33 offset:55520\n" \
"ds_read_b64_tr_b16 a[194:195], v33 offset:60128\n" \
"v_mfma_f32_16x16x32_bf16 v[232:235], a[196:199], a[160:163], v[232:235]\n" \
"s_waitcnt lgkmcnt(14)\n" \
"v_mfma_f32_16x16x32_bf16 v[232:235], a[200:203], a[164:167], v[232:235]\n" \
"buffer_load_dwordx4 v[156:159], v20, s[56:59], s64 offen offset:2048 sc1\n" \
"v_readlane_b32 s64, v11, 51\n" \
"v_mfma_f32_16x16x32_bf16 v[236:239], a[196:199], a[176:179], v[236:239]\n" \
"ds_read_b64_tr_b16 a[196:197], v10 offset:224\n" \
"ds_read_b64_tr_b16 a[198:199], v10 offset:4832\n" \
"v_mfma_f32_16x16x32_bf16 v[236:239], a[200:203], a[180:183], v[236:239]\n" \
"ds_read_b64_tr_b16 a[200:201], v34 offset:224\n" \
"ds_read_b64_tr_b16 a[202:203], v34 offset:4832\n" \
"buffer_store_dwordx4 v[224:227], v20, s[56:59], s64 offen offset:1024 sc1\n" \
"v_mfma_f32_16x16x32_bf16 v[236:239], a[204:207], a[184:187], v[236:239]\n" \
"v_mfma_f32_16x16x32_bf16 v[236:239], a[208:211], a[188:191], v[236:239]\n" \
"v_mfma_f32_16x16x32_bf16 v[232:235], a[204:207], a[168:171], v[232:235]\n" \
"ds_read_b64_tr_b16 a[204:205], v34 offset:9440\n" \
"ds_read_b64_tr_b16 a[206:207], v34 offset:14048\n" \
"s_waitcnt vmcnt(13)\n" \
"s_mov_b64 exec, 1\n" \
"buffer_store_dword v39, off, s[56:59], s67 offset:40 sc1\n" \
"s_mov_b64 exec, -1\n" \
"v_mfma_f32_16x16x32_bf16 v[232:235], a[208:211], a[172:175], v[232:235]\n" \
"ds_read_b64_tr_b16 a[208:209], v34 offset:18656\n" \
"ds_read_b64_tr_b16 a[210:211], v34 offset:23264\n" \
"v_mfma_f32_16x16x32_bf16 v[232:235], a[212:215], a[96:99], v[232:235]\n" \
"v_mfma_f32_16x16x32_bf16 v[232:235], a[216:219], a[100:103], v[232:235]\n" \
"v_mfma_f32_16x16x32_bf16 v[236:239], a[212:215], a[112:115], v[236:239]\n" \
"ds_read_b64_tr_b16 a[212:213], v10 offset:9440\n" \
"ds_read_b64_tr_b16 a[214:215], v10 offset:14048\n" \
"v_mfma_f32_16x16x32_bf16 v[236:239], a[216:219], a[116:119], v[236:239]\n" \
"ds_read_b64_tr_b16 a[216:217], v36 offset:0\n" \
"ds_read_b64_tr_b16 a[218:219], v36 offset:1024\n" \
"v_mfma_f32_16x16x32_bf16 v[236:239], a[0:3], a[120:123], v[236:239]\n" \
"v_mfma_f32_16x16x32_bf16 v[236:239], a[4:7], a[124:127], v[236:239]\n" \
"v_mfma_f32_16x16x32_bf16 v[232:235], a[0:3], a[104:107], v[232:235]\n" \
"ds_read_b64_tr_b16 a[0:1], v36 offset:2048\n" \
"ds_read_b64_tr_b16 a[2:3], v36 offset:3072\n" \
"v_mfma_f32_16x16x32_bf16 v[232:235], a[4:7], a[108:111], v[232:235]\n" \
"ds_read_b64_tr_b16 a[4:5], v36 offset:32\n" \
"buffer_load_dwordx4 v[164:167], v20, s[56:59], s65 offen offset:0 sc1\n" \
"v_readlane_b32 s65, v11, 52\n" \
"s_waitcnt lgkmcnt(11)\n" \
"v_mfma_f32_16x16x32_bf16 v[240:243], a[8:11], a[220:223], v[240:243]\n" \
"ds_read_b64_tr_b16 a[6:7], v36 offset:1056\n" \
"v_mfma_f32_16x16x32_bf16 v[240:243], a[12:15], a[224:227], v[240:243]\n" \
"v_mfma_f32_16x16x32_bf16 v[244:247], a[8:11], a[236:239], v[244:247]\n" \
"v_cvt_pk_bf16_f32 v232, v232, v233\n" \
"v_cvt_pk_bf16_f32 v233, v234, v235\n" \
"v_cvt_pk_bf16_f32 v234, v236, v237\n" \
"v_cvt_pk_bf16_f32 v235, v238, v239\n" \
"buffer_store_dwordx4 v[232:235], v20, s[56:59], s65 offen offset:1024 sc1\n" \
"v_mfma_f32_16x16x32_bf16 v[244:247], a[12:15], a[240:243], v[244:247]\n" \
"ds_read_b64_tr_b16 a[8:9], v36 offset:2080\n" \
"ds_read_b64_tr_b16 a[10:11], v36 offset:3104\n" \
"v_mfma_f32_16x16x32_bf16 v[244:247], a[16:19], a[244:247], v[244:247]\n" \
"v_mfma_f32_16x16x32_bf16 v[244:247], a[20:23], a[248:251], v[244:247]\n" \
"v_mfma_f32_16x16x32_bf16 v[240:243], a[16:19], a[228:231], v[240:243]\n" \
"v_mfma_f32_16x16x32_bf16 v[240:243], a[20:23], a[232:235], v[240:243]\n" \
"v_mfma_f32_16x16x32_bf16 v[168:171], a[24:27], a[128:131], v[168:171]\n" \
"v_mfma_f32_16x16x32_bf16 v[168:171], a[28:31], a[132:135], v[168:171]\n" \
"v_mfma_f32_16x16x32_bf16 v[172:175], a[24:27], a[144:147], v[172:175]\n" \
"s_waitcnt vmcnt(9)\n" \
"v_cmp_eq_u32_e64 s[98:99], s62, v37\n" \
"buffer_load_dword v37, v40, s[56:59], s74 offen sc1\n" \
"s_mov_b64 exec, 1\n" \
"buffer_store_dword v39, off, s[56:59], s68 offset:44 sc1\n" \
"s_mov_b64 exec, -1\n" \
"s_or_b32 s75, 0x300, s100\n" \
"s_andn2_b32 s75, s75, s98\n" \
"s_mov_b32 s21, s75\n" \
"s_bitcmp1_b32 s21, 16\n" \
"s_cselect_b32 s70, 0x40000000, s16\n" \
"s_bitcmp1_b32 s21, 17\n" \
"s_cselect_b32 s71, 0x40000000, s16\n" \
"s_bitcmp1_b32 s21, 18\n" \
"s_cselect_b32 s72, 0x40000000, s17\n" \
"s_bitcmp1_b32 s21, 19\n" \
"s_cselect_b32 s73, 0x40000000, s17\n" \
"s_bitcmp1_b32 s21, 20\n" \
"s_cselect_b32 s92, 0x40000000, s18\n" \
"s_bitcmp1_b32 s21, 21\n" \
"s_cselect_b32 s93, 0x40000000, s18\n" \
"s_bitcmp1_b32 s21, 22\n" \
"s_cselect_b32 s94, 0x40000000, s19\n" \
"s_bitcmp1_b32 s21, 23\n" \
"s_cselect_b32 s95, 0x40000000, s19\n" \
"s_bitcmp1_b32 s21, 8\n" \
"s_cselect_b32 s96, 0x40000000, s12\n" \
"s_bitcmp1_b32 s21, 9\n" \
"s_cselect_b32 s97, 0x40000000, s12\n" \
"s_add_u32 s64, s64, 0x1000\n" \
"s_add_u32 s65, s65, 0x800\n" \
"v_mfma_f32_16x16x32_bf16 v[172:175], a[28:31], a[148:151], v[172:175]\n" \
"buffer_load_dwordx4 v[112:115], v20, s[56:59], s70 offen offset:0 sc1\n" \
"v_mfma_f32_16x16x32_bf16 v[240:243], a[24:27], a[160:163], v[240:243]\n" \
"v_mfma_f32_16x16x32_bf16 v[240:243], a[28:31], a[164:167], v[240:243]\n" \
"v_mfma_f32_16x16x32_bf16 v[244:247], a[24:27], a[176:179], v[244:247]\n" \
"v_mfma_f32_16x16x32_bf16 v[244:247], a[28:31], a[180:183], v[244:247]\n" \
"buffer_load_dwordx4 v[184:187], v20, s[56:59], s71 offen offset:2048 sc1\n" \
"v_mfma_f32_16x16x32_bf16 v[244:247], a[192:195], a[184:187], v[244:247]\n" \
"v_readlane_b32 s71, v11, 54\n" \
"v_mfma_f32_16x16x32_bf16 v[244:247], a[196:199], a[188:191], v[244:247]\n" \
"v_mfma_f32_16x16x32_bf16 v[240:243], a[192:195], a[168:171], v[240:243]\n" \
"v_mfma_f32_16x16x32_bf16 v[240:243], a[196:199], a[172:175], v[240:243]\n" \
"buffer_load_dwordx4 v[120:123], v20, s[56:59], s72 offen offset:0 sc1\n" \
"v_mfma_f32_16x16x32_bf16 v[172:175], a[192:195], a[152:155], v[172:175]\n" \
"v_readlane_b32 s72, v11, 45\n" \
"v_mfma_f32_16x16x32_bf16 v[172:175], a[196:199], a[156:159], v[172:175]\n" \
"v_mfma_f32_16x16x32_bf16 v[168:171], a[192:195], a[136:139], v[168:171]\n" \
"v_mfma_f32_16x16x32_bf16 v[168:171], a[196:199], a[140:143], v[168:171]\n" \
"buffer_load_dwordx4 v[192:195], v20, s[56:59], s73 offen offset:2048 sc1\n" \
"v_mfma_f32_16x16x32_bf16 v[168:171], a[200:203], a[32:35], v[168:171]\n" \
"v_readlane_b32 s73, v11, 43\n" \
"s_waitcnt lgkmcnt(0)\n" \
"v_mfma_f32_16x16x32_bf16 v[168:171], a[204:207], a[36:39], v[168:171]\n" \
"v_mfma_f32_16x16x32_bf16 v[172:175], a[200:203], a[48:51], v[172:175]\n" \
"v_mfma_f32_16x16x32_bf16 v[172:175], a[204:207], a[52:55], v[172:175]\n" \
"buffer_load_dwordx4 v[128:131], v20, s[56:59], s92 offen offset:0 sc1\n" \
"v_mfma_f32_16x16x32_bf16 v[240:243], a[200:203], a[96:99], v[240:243]\n" \
"v_readlane_b32 s92, v11, 44\n" \
"v_mfma_f32_16x16x32_bf16 v[240:243], a[204:207], a[100:103], v[240:243]\n" \
"v_mfma_f32_16x16x32_bf16 v[244:247], a[200:203], a[112:115], v[244:247]\n" \
"v_mfma_f32_16x16x32_bf16 v[244:247], a[204:207], a[116:119], v[244:247]\n" \
"buffer_load_dwordx4 v[200:203], v20, s[56:59], s93 offen offset:2048 sc1\n" \
"v_mfma_f32_16x16x32_bf16 v[244:247], a[208:211], a[120:123], v[244:247]\n" \
"v_mfma_f32_16x16x32_bf16 v[244:247], a[212:215], a[124:127], v[244:247]\n" \
"v_mfma_f32_16x16x32_bf16 v[240:243], a[208:211], a[104:107], v[240:243]\n" \
"v_mfma_f32_16x16x32_bf16 v[240:243], a[212:215], a[108:111], v[240:243]\n" \
".fill (L_ab + 45056 - .) / 4, 4, 0xBF800000\n" \
"L_ap11:\n" \
"s_cbranch_execz L_ap0\n" \
"buffer_load_dwordx4 v[176:179], v20, s[56:59], s94 offen offset:0 sc1\n" \
"s_waitcnt vmcnt(17)\n" \
"s_mov_b64 exec, 1\n" \
"buffer_store_dword v39, off, s[56:59], s69 offset:48 sc1\n" \
"s_mov_b64 exec, -1\n" \
"v_mfma_f32_16x16x32_bf16 v[172:175], a[208:211], a[56:59], v[172:175]\n" \
"v_mfma_f32_16x16x32_bf16 v[172:175], a[212:215], a[60:63], v[172:175]\n" \
"v_mfma_f32_16x16x32_bf16 v[168:171], a[208:211], a[40:43], v[168:171]\n" \
"v_mfma_f32_16x16x32_bf16 v[168:171], a[212:215], a[44:47], v[168:171]\n" \
"buffer_load_dwordx4 v[248:251], v20, s[56:59], s95 offen offset:2048 sc1\n" \
"v_mfma_f32_16x16x32_bf16 v[168:171], a[216:219], v[64:67], v[168:171]\n" \
"v_mfma_f32_16x16x32_bf16 v[172:175], a[216:219], v[72:75], v[172:175]\n" \
"v_mfma_f32_16x16x32_bf16 v[240:243], a[216:219], v[80:83], v[240:243]\n" \
"v_mfma_f32_16x16x32_bf16 v[244:247], a[216:219], v[88:91], v[244:247]\n" \
"buffer_load_dwordx4 v[188:191], v20, s[56:59], s96 offen offset:1024 sc1\n" \
"v_mfma_f32_16x16x32_bf16 v[168:171], a[0:3], v[68:71], v[168:171]\n" \
"v_mfma_f32_16x16x32_bf16 v[172:175], a[0:3], v[76:79], v[172:175]\n" \
"v_mfma_f32_16x16x32_bf16 v[240:243], a[0:3], v[84:87], v[240:243]\n" \
"v_mfma_f32_16x16x32_bf16 v[244:247], a[0:3], v[92:95], v[244:247]\n" \
"buffer_load_dwordx4 v[196:199], v20, s[56:59], s97 offen offset:3072 sc1\n" \
"v_mfma_f32_16x16x32_bf16 v[56:59], a[4:7], v[96:99], 0\n" \
"v_mfma_f32_16x16x32_bf16 v[60:63], a[4:7], v[104:107], 0\n" \
"v_mfma_f32_16x16x32_bf16 v[56:59], a[8:11], v[100:103], v[56:59]\n" \
"v_mfma_f32_16x16x32_bf16 v[60:63], a[8:11], v[108:111], v[60:63]\n" \
"s_waitcnt vmcnt(16)\n" \
"s_mov_b64 exec, 1\n" \
"buffer_store_dword v39, off, s[56:59], s64 offset:52 sc1\n" \
"s_mov_b64 exec, -1\n" \
"s_waitcnt vmcnt(14)\n" \
"s_mov_b64 exec, 1\n" \
"buffer_store_dword v39, off, s[56:59], s65 offset:56 sc1\n" \
"s_mov_b64 exec, -1\n" \
"s_cmp_lg_u32 s20, 0\n" \
"s_cbranch_scc1 L_sna_%=\n" \
"L_rna_%=:\n" \
"s_cmp_lg_u32 s21, 0\n" \
"s_cbranch_scc1 L_snx_%=\n" \
"L_rnx_%=:\n" \
"s_waitcnt vmcnt(14)\n" \
"v_cmp_eq_u32_e64 s[98:99], s62, v37\n" \
"v_mfma_f32_16x16x32_bf16 v[168:171], v[50:53], v[116:119], v[168:171]\n" \
"s_andn2_b32 s75, 0x400, s98\n" \
"s_cbranch_scc1 L_sx0_%=\n" \
"L_rx0_%=:\n" \
"s_add_u32 s66, s63, 0x1000\n" \
"v_mfma_f32_16x16x32_bf16 v[172:175], v[48:51], v[116:119], v[172:175]\n" \
"buffer_load_dwordx4 v[204:207], v20, s[56:59], s66 offen offset:1024 sc1\n" \
"s_andn2_b32 s75, 0x800, s98\n" \
"s_cbranch_scc1 L_sx1_%=\n" \
"L_rx1_%=:\n" \
"s_add_u32 s67, s63, 0x1800\n" \
"v_mfma_f32_16x16x32_bf16 v[168:171], v[50:53], v[124:127], v[168:171]\n" \
"v_mfma_f32_16x16x32_bf16 v[172:175], v[48:51], v[124:127], v[172:175]\n" \
"v_mfma_f32_16x16x32_bf16 v[168:171], v[50:53], v[132:135], v[168:171]\n" \
"buffer_load_dwordx4 v[212:215], v20, s[56:59], s67 offen offset:1024 sc1\n" \
"s_andn2_b32 s75, 0x1000, s98\n" \
"s_cbranch_scc1 L_sx2_%=\n" \
"L_rx2_%=:\n" \
"s_add_u32 s68, s63, 0x2000\n" \
"v_mfma_f32_16x16x32_bf16 v[172:175], v[48:51], v[132:135], v[172:175]\n" \
"v_mfma_f32_16x16x32_bf16 v[168:171], v[50:53], v[140:143], v[168:171]\n" \
"v_mfma_f32_16x16x32_bf16 v[172:175], v[48:51], v[140:143], v[172:175]\n" \
"buffer_load_dwordx4 v[220:223], v20, s[56:59], s68 offen offset:1024 sc1\n" \
"s_andn2_b32 s75, 0x2000, s98\n" \
"s_cbranch_scc1 L_sx3_%=\n" \
"L_rx3_%=:\n" \
"s_add_u32 s69, s63, 0x2800\n" \
"v_mfma_f32_16x16x32_bf16 v[168:171], v[50:53], v[148:151], v[168:171]\n" \
"v_mfma_f32_16x16x32_bf16 v[172:175], v[48:51], v[148:151], v[172:175]\n" \
"v_mfma_f32_16x16x32_bf16 v[168:171], v[50:53], v[156:159], v[168:171]\n" \
"buffer_load_dwordx4 v[228:231], v20, s[56:59], s69 offen offset:1024 sc1\n" \
"s_andn2_b32 s75, 0x4000, s98\n" \
"s_cbranch_scc1 L_sx4_%=\n" \
"L_rx4_%=:\n" \
"s_add_u32 s70, s63, 0x3000\n" \
"v_mfma_f32_16x16x32_bf16 v[172:175], v[48:51], v[156:159], v[172:175]\n" \
"v_mfma_f32_16x16x32_bf16 v[168:171], v[50:53], v[164:167], v[168:171]\n" \
"v_mfma_f32_16x16x32_bf16 v[172:175], v[48:51], v[164:167], v[172:175]\n" \
"buffer_load_dwordx4 v[236:239], v20, s[56:59], s70 offen offset:1024 sc1\n" \
"s_waitcnt vmcnt(17)\n" \
"v_mfma_f32_16x16x32_bf16 v[56:59], v[50:53], v[112:115], v[56:59]\n" \
"v_mfma_f32_16x16x32_bf16 v[60:63], v[48:51], v[112:115], v[60:63]\n" \
"s_waitcnt vmcnt(16)\n" \
"v_mfma_f32_16x16x32_bf16 v[56:59], v[50:53], v[184:187], v[56:59]\n" \
"v_mfma_f32_16x16x32_bf16 v[60:63], v[48:51], v[184:187], v[60:63]\n" \
"v_cvt_pk_bf16_f32 v168, v168, v169\n" \
"s_waitcnt vmcnt(15)\n" \
"v_mfma_f32_16x16x32_bf16 v[56:59], v[50:53], v[120:123], v[56:59]\n" \
"v_cvt_pk_bf16_f32 v172, v172, v173\n" \
"v_cvt_pk_bf16_f32 v169, v170, v171\n" \
"v_mfma_f32_16x16x32_bf16 v[60:63], v[48:51], v[120:123], v[60:63]\n" \
"v_cvt_pk_bf16_f32 v173, v174, v175\n" \
"s_waitcnt vmcnt(14)\n" \
"v_mfma_f32_16x16x32_bf16 v[56:59], v[50:53], v[192:195], v[56:59]\n" \
"buffer_store_dwordx2 v[168:169], v42, s[88:91], s73 offen sc1\n" \
"v_mfma_f32_16x16x32_bf16 v[60:63], v[48:51], v[192:195], v[60:63]\n" \
"s_waitcnt vmcnt(14)\n" \
"v_mfma_f32_16x16x32_bf16 v[56:59], v[50:53], v[128:131], v[56:59]\n" \
"buffer_store_dwordx2 v[172:173], v43, s[88:91], s73 offen sc1\n" \
"v_mfma_f32_16x16x32_bf16 v[60:63], v[48:51], v[128:131], v[60:63]\n" \
"s_waitcnt vmcnt(14)\n" \
"v_mfma_f32_16x16x32_bf16 v[56:59], v[50:53], v[200:203], v[56:59]\n" \
"v_mfma_f32_16x16x32_bf16 v[60:63], v[48:51], v[200:203], v[60:63]\n" \
"s_waitcnt vmcnt(13)\n" \
"v_mfma_f32_16x16x32_bf16 v[56:59], v[50:53], v[176:179], v[56:59]\n" \
"v_mfma_f32_16x16x32_bf16 v[60:63], v[48:51], v[176:179], v[60:63]\n" \
"s_waitcnt vmcnt(11)\n" \
"v_mfma_f32_16x16x32_bf16 v[56:59], v[50:53], v[248:251], v[56:59]\n" \
"v_mfma_f32_16x16x32_bf16 v[60:63], v[48:51], v[248:251], v[60:63]\n" \
"s_waitcnt vmcnt(10)\n" \
"v_mfma_f32_16x16x32_bf16 v[240:243], v[50:53], v[188:191], v[240:243]\n" \
"v_mfma_f32_16x16x32_bf16 v[244:247], v[48:51], v[188:191], v[244:247]\n" \
"s_waitcnt vmcnt(9)\n" \
"v_mfma_f32_16x16x32_bf16 v[240:243], v[50:53], v[196:199], v[240:243]\n" \
"v_mfma_f32_16x16x32_bf16 v[244:247], v[48:51], v[196:199], v[244:247]\n" \
"s_mov_b64 exec, 1\n" \
"buffer_store_dword v46, off, s[56:59], s71 offset:0 sc1\n" \
"s_mov_b64 exec, -1\n" \
"v_cvt_pk_bf16_f32 v56, v56, v57\n" \
"v_cvt_pk_bf16_f32 v60, v60, v61\n" \
"v_cvt_pk_bf16_f32 v57, v58, v59\n" \
"v_cvt_pk_bf16_f32 v61, v62, v63\n" \
"buffer_store_dwordx2 v[56:57], v42, s[88:91], s72 offen sc1\n" \
"buffer_store_dwordx2 v[60:61], v43, s[88:91], s72 offen sc1\n" \
"s_waitcnt vmcnt(9)\n" \
"v_mfma_f32_16x16x32_bf16 v[240:243], v[50:53], v[204:207], v[240:243]\n" \
"v_mfma_f32_16x16x32_bf16 v[244:247], v[48:51], v[204:207], v[244:247]\n" \
"s_waitcnt vmcnt(8)\n" \
"v_mfma_f32_16x16x32_bf16 v[240:243], v[50:53], v[212:215], v[240:243]\n" \
"v_mfma_f32_16x16x32_bf16 v[244:247], v[48:51], v[212:215], v[244:247]\n" \
"s_waitcnt vmcnt(7)\n" \
"v_mfma_f32_16x16x32_bf16 v[240:243], v[50:53], v[220:223], v[240:243]\n" \
"v_mfma_f32_16x16x32_bf16 v[244:247], v[48:51], v[220:223], v[244:247]\n" \
"s_waitcnt vmcnt(6)\n" \
"v_mfma_f32_16x16x32_bf16 v[240:243], v[50:53], v[228:231], v[240:243]\n" \
"v_mfma_f32_16x16x32_bf16 v[244:247], v[48:51], v[228:231], v[244:247]\n" \
"s_waitcnt vmcnt(5)\n" \
"v_mfma_f32_16x16x32_bf16 v[240:243], v[50:53], v[236:239], v[240:243]\n" \
"v_mfma_f32_16x16x32_bf16 v[244:247], v[48:51], v[236:239], v[244:247]\n" \
"s_nop 6\n" \
"v_cvt_pk_bf16_f32 v240, v240, v241\n" \
"v_cvt_pk_bf16_f32 v244, v244, v245\n" \
"v_cvt_pk_bf16_f32 v241, v242, v243\n" \
"v_cvt_pk_bf16_f32 v245, v246, v247\n" \
"buffer_store_dwordx2 v[240:241], v42, s[88:91], s92 offen sc1\n" \
"buffer_store_dwordx2 v[244:245], v43, s[88:91], s92 offen sc1\n"
#define ATT_VCLOB "v21","v22","v23","v24","v25","v26","v27","v28","v29","v32","v33","v34","v35","v36","v37","v38","v39","v40","v41","v42","v43","v44","v45","v46","v47","v48","v49","v50","v51","v52","v53","v54","v55","v56","v57","v58","v59","v60","v61","v62","v63","v64","v65","v66","v67","v68","v69","v70","v71","v72","v73","v74","v75","v76","v77","v78","v79","v80","v81","v82","v83","v84","v85","v86","v87","v88","v89","v90","v91","v92","v93","v94","v95","v96","v97","v98","v99","v100","v101","v102","v103","v104","v105","v106","v107","v108","v109","v110","v111","v112","v113","v114","v115","v116","v117","v118","v119","v120","v121","v122","v123","v124","v125","v126","v127","v128","v129","v130","v131","v132","v133","v134","v135","v136","v137","v138","v139","v140","v141","v142","v143","v144","v145","v146","v147","v148","v149","v150","v151","v152","v153","v154","v155","v156","v157","v158","v159","v160","v161","v162","v163","v164","v165","v166","v167","v168","v169","v170","v171","v172","v173","v174","v175","v176","v177","v178","v179","v180","v181","v182","v183","v184","v185","v186","v187","v188","v189","v190","v191","v192","v193","v194","v195","v196","v197","v198","v199","v200","v201","v202","v203","v204","v205","v206","v207","v208","v209","v210","v211","v212","v213","v214","v215","v216","v217","v218","v219","v220","v221","v222","v223","v224","v225","v226","v227","v228","v229","v230","v231","v232","v233","v234","v235","v236","v237","v238","v239","v240","v241","v242","v243","v244","v245","v246","v247","v248","v249","v250","v251","v252","v253","v254","v255"
#define ATT_SCLOB "s63","s64","s65","s66","s67","s68","s69","s70","s71","s72","s73","s74","s75","s76","s77","s78","s79","s80","s81","s82","s83","s84","s85","s86","s87","s88","s89","s90","s91","s92","s93","s95","s96","s97","s98","s99","s100","s101"
#define ATT_WTCH 0
#define ATT_PEEL 1
#define ATT_PEEL_FOW 0
enum { T_fo, T_fj, T_fa, T_fas, T_fb, T_fbs, T_f8, T_mk8, T_wt, T_wol, T_woh, T_wbl, T_wbh, T_outl, T_outh, T_r1l, T_r1h, T_r1n, T_r1s, T_m0, T_m1, T_m1b, T_mw, T_m3, T_m4, T_ml, T_w0, T_w1, T_w2, T_w3, T_A1, T_A2, T_A3, T_B1, T_B2, T_B3, T_Ao, T_Bo, T_Al, T_Bl, T_l8, T_xr, T_x8r, T_oA, T_oB, T_o8, T_x0, T_x1, T_x2, T_x3, T_x4, T_x5, T_x6, T_x8, T_ep };

__device__ __forceinline__ void out_consts(u32x4 (&vp)[3], const u16* wo, const u16* wob, const u16* out, const unsigned b, const unsigned w, const unsigned lane) {
    const unsigned sp = b & 7u, nt = (b >> 3) & 3u, j = b >> 5, head = 4u * sp + nt, OOR = 0x40000000u;
    unsigned vw[9];
#pragma unroll
    for (unsigned i = 0; i < 9u; ++i) {
        const unsigned q = 64u * i + lane, row = __umul24(q, 3641u) >> 16, c = q - __umul24(row, 18u);
        vw[i] = __umul24(row, DM * 2) + (c < 16u ? (c * 16u + sp * 32u + 32u) & 255u : c * 16u) - (i & 3u) * 1024u;
    }
    const unsigned row = 16u * w + (lane >> 2), r9 = __umul24(16u * (row >> 5) + (row & 12u) + 2u * ((row >> 4) & 1u) + ((row >> 1) & 1u), 18u) + 16u + (row & 1u);
    unsigned vwl = __umul24(r9, DM * 2) + ((lane & 2u) ? 256u : sp * 32u) + (lane & 1u) * 16u, vrd = __umul24(lane >> 2, 288u) + (lane & 3u) * 8u;
    unsigned tab = 0;
#define TB(n, v) asm("v_writelane_b32 %0, %1, %2" : "+v"(tab) : "s"((unsigned)(v)), "n"((int)T_##n))
    const unsigned FJ = (unsigned)WS_F + (F_J + 640u * j) * 4u, mt = 2u * (w & 2u) + (w & 1u), w2 = w < 2u ? w : 2u, hw = head * 147456u;
    const unsigned FR = (unsigned)WS_F + F_R * 4u + (8u * j + sp) * 1024u;
    TB(fo, FJ + (ATT_PEEL_FOW ? 16u * head + 4u * w : 4u * head)); TB(fj, FJ); TB(fa, FR + (w | 2u) * 256u); TB(fas, FR + w * 256u + nt * 4u);
    TB(fb, FR + 128u + w * 256u); TB(fbs, FR + 128u + w * 256u + nt * 4u);
    TB(ep, ssel(b + w, OOR, (unsigned)WS_F + F_EPOCH * 4u));
    const u16* pwo = wo + nt * 144u; const u16* pwb = wob + nt * 144u;
    TB(wol, PLO(pwo)); TB(woh, PHI(pwo) & 0xffffu); TB(wbl, PLO(pwb)); TB(wbh, PHI(pwb) & 0xffffu); TB(outl, PLO(out)); TB(outh, PHI(out) & 0xffffu); TB(r1l, sslt<3>(w, PLO(pwo), PLO(pwb))); TB(r1h, sslt<3>(w, PHI(pwo), PHI(pwb)) & 0xffffu);
    TB(r1n, sslt<3>(w, 72u * sp + 71u, 4095u) * 1152u + 288u); TB(r1s, sslt<3>(w, (72u * sp + 32u * w) * 1152u, hw + 110592u));
    TB(m0, 131072u + 9216u * w2); TB(w0, hw + 36864u * w2); TB(m1, ATT_PEEL ? sslt<3>(w, 41984u + 9216u * w, 32768u) : 24576u + 9216u * w); TB(m1b, ssel(w ^ 2u, sslt<3>(w, 41984u + 9216u * w, 32768u), 114688u - 4096u)); TB(mw, 65536u + 9216u * w); TB(m3, sslt<3>(w, 102400u + 9216u * w, 0u)); TB(m4, sslt<3>(w, 131072u + 9216u * w, 9216u)); TB(ml, (ATT_PEEL ? 18432u : 61440u) + 1024u * w);
    const unsigned tA = (unsigned)WS_AF + (8u * j + mt) * (KS_TOT * 1024u), tB = (unsigned)WS_AF + (120u - 8u * j + mt) * (KS_TOT * 1024u);
#define HK(k) (4u * sp + ((nt + k) & 3u))
    TB(w1, HK(1u) * 147456u + w * 36864u); TB(w2, HK(2u) * 147456u + w * 36864u); TB(w3, HK(3u) * 147456u + w * 36864u);
    TB(A1, tA + 18432u + HK(1u) * 4096u); TB(A2, tA + 18432u + HK(2u) * 4096u); TB(A3, tA + 18432u + HK(3u) * 4096u);
    TB(B1, tB + 18432u + HK(1u) * 4096u); TB(B2, tB + 18432u + HK(2u) * 4096u); TB(B3, tB + 18432u + HK(3u) * 4096u);
    TB(Ao, tA + sp * 2048u); TB(Bo, tB + sp * 2048u); TB(Al, tA + 16384u); TB(Bl, tB + 16384u);
    const unsigned nred = w ^ (sp >> 1), blk = sp & 1u;
    const unsigned xg = (unsigned)WS_X + (4u * j + nt) * 589824u + w * 16384u, oA = (128u * j + 16u * mt) * 1152u + nt * 288u, oB = (1920u - 128u * j + 16u * mt) * 1152u + nt * 288u;
    TB(l8, ssel(nred, OOR, ssel(blk, tB, tA) + 16384u)); TB(xr, xg + sp * 65536u); TB(x8r, ssel(nred, OOR, xg + 524288u + (sp & 1u) * 1024u));
    TB(oA, oA + sp * 32u); TB(oB, oB + sp * 32u); TB(o8, ssel(nred, OOR, ssel(blk, oB, oA) + 256u)); TB(x8, xg + 524288u + sp * 2048u);
    TB(f8, xg + w * 131072u + 14400u + sp * 4u); TB(mk8, ssel(nred, 0u, 0xff0000u)); TB(wt, hw + j * 18432u + w * 4608u);
#define XI(i) TB(x##i, xg + ((sp + 1u + i) & 7u) * 65536u + i * 2048u)
    XI(0); XI(1); XI(2); XI(3); XI(4); XI(5); XI(6);
#undef XI
#undef HK
#undef TB
    vp[0] = (u32x4){vw[0], vw[1], vw[2], vw[3]}; vp[1] = (u32x4){vw[4], vw[5], vw[6], vw[7]}; vp[2] = (u32x4){vw[8], vwl, vrd, tab};
}
__global__ __launch_bounds__(256, 1) __attribute__((aligned(4096))) void fused_kernel(const u16* __restrict__ x, const u16* __restrict__ ckv, const u16* __restrict__ wq, const u16* __restrict__ wuk,
                                                       const u16* __restrict__ wuv, const u16* __restrict__ wqkv, const u16* __restrict__ gw,
                                                       const u16* __restrict__ wo, const u16* __restrict__ wob, char* __restrict__ ws_, u16* __restrict__ out) {
    using namespace att;
    extern __shared__ __attribute__((aligned(16))) char lds[];
    u32x4 sa[5]; u32x2 s24, s28; char* ws; unsigned tdum; kaptr ka = (kaptr)__builtin_amdgcn_kernarg_segment_ptr();
    asm volatile("s_load_dwordx8 s[8:15], %[ka], 0x0\n\ts_load_dwordx8 s[16:23], %[ka], 0x20\n\ts_getpc_b64 s[4:5]\n\tv_lshlrev_b32_e32 v191, 7, %[t]\n\ts_bfe_u32 s6, %[b], 0x20003\n\t"
                 "v_lshl_add_u32 v191, s6, 15, v191\n\tv_min_u32_e32 v191, %[e], v191\n\tglobal_load_dword v191, v191, s[4:5]\n\ts_load_dwordx4 s[24:27], %[ka], 0x40\n\ts_load_dwordx2 s[28:29], %[ka], 0x50"
                 : "={s[8:11]}"(sa[0]), "={s[12:15]}"(sa[1]), "={s[16:19]}"(sa[2]), "={s[20:23]}"(sa[3]), "={s[24:25]}"(s24), "={s[26:27]}"(ws), "={s[28:29]}"(s28), "={v191}"(tdum), [ka] "+{s[0:1]}"(ka)
                 : [t] "v"(threadIdx.x), [b] "s"(blockIdx.x), [e] "i"(CT_END) : "s4", "s5", "s6", "scc");
    SBAR();
    const unsigned wid = rfl(threadIdx.x >> 6);
    unsigned tag, vl;
    const int b = blockIdx.x;
    u32x4 vp[5], srdA; u32x2 sd;
    proj_phase(ka, ws, wid, vp, vl, sa, s24, s28, tdum, srdA, sd, tag
    );
    unsigned* const flags = (unsigned*)(ws + WS_F);
    RTS(2);
#define AST(L) do {} while (0)
#define ATT_TAIL "s_endpgm\n"
    unsigned abad;
    asm volatile(ATT_ASM ATT_TAIL "L_end_%=:\n s_nop 1"
                 : "={s94}"(abad)
                 : "{v[0:3]}"(vp[0]), "{v[4:7]}"(vp[1]), "{v[8:11]}"(vp[2]), "{v[12:15]}"(vp[3]), "{v[16:19]}"(vp[4]), "{v20}"(vl), "{s[56:59]}"(srdA), "{s[60:61]}"(sd), "{s62}"(tag),
                   "{s[8:11]}"(sa[0]), "{s[12:15]}"(sa[1]), "{s[16:19]}"(sa[2]), "{s[20:23]}"(sa[3]), "{s24}"(sa[4][0]), "{s3}"(wid), "{s[28:29]}"(s28)
                 : "memory", "m0", "vcc", "scc", ATT_VCLOB, ATT_SCLOB, ACLOB_LO, ACLOB_HI);
    {
        unsigned b2 = b, w2 = wid;
        asm volatile("s_waitcnt vmcnt(0)" : "+s"(ka), "+s"(b2), "+s"(w2), "+s"(ws) :: "memory");
        const unsigned j2 = b2 >> 5, p2 = w2 & 1u, h2 = (b2 & 7u) * 4u + ((b2 >> 3) & 3u);
#pragma unroll 1
        for (unsigned i = w2 & 2u; i < ((w2 & 2u) ? (abad ? 8u : 4u) : 2u); ++i)
            att_exact(ka[0], ka[2], ws, h2, (i < 4u ? 1920u - 128u * j2 : 128u * j2 - 128u) + 32u * i + 16u * p2, *reinterpret_cast<const float*>(lds + LDS_G + (32u * i + 16u * p2 + (lane_id() & 15u)) * 4u));
        asm volatile("s_waitcnt vmcnt(0)" ::: "memory");
        unsigned* fa = (unsigned*)(ws + WS_F) + F_R + (8u * j2 + (b2 & 7u)) * 256u + (2u + p2) * 64u + ((b2 >> 3) & 3u);
        if (w2 & 2u) st_flag(fa, tag);
        wait_flags(fa, ld_flag(fa), tag);
        const unsigned l2 = lane_id(); u32x4 c[3]; out_consts(c, ka[7], ka[8], ka[10], b2, w2, l2);
        asm volatile("s_waitcnt vmcnt(0) lgkmcnt(0)\n\ts_nop 7\n\ts_branch L_att_stub" :: "{v[0:3]}"(c[0]), "{v[4:7]}"(c[1]), "{v[8:11]}"(c[2]), "{v20}"(l2 * 16u), "{s[56:59]}"(SRDA), "{s60}"(SD(8u * j2, h2, w2)),
                     "{s61}"(SD(120u - 8u * j2, h2, w2)), "{s62}"(tag), "{s3}"(w2) : "memory");
    }
    RTS(5);
}

extern "C" void kernel_launch(void* const* d_in, const int* in_sizes, int n_in, void* d_out, int out_size, void* d_ws, size_t ws_size,
                              hipStream_t stream) {
    (void)hipFuncSetAttribute((const void*)fused_kernel, hipFuncAttributeMaxDynamicSharedMemorySize, att::LDS_BYTES);
    fused_kernel<<<dim3(256), 256, att::LDS_BYTES, stream>>>((const u16*)d_in[0], (const u16*)d_in[4], (const u16*)d_in[5], (const u16*)d_in[6], (const u16*)d_in[7], (const u16*)d_in[1],
                                                             (const u16*)d_in[9], (const u16*)d_in[2], (const u16*)d_in[8], (char*)d_ws, (u16*)d_out);
}
```

```cpp
#include <hip/hip_runtime.h>
#include <hip/hip_bf16.h>
#include <stdint.h>
#include <utility>

typedef short bf16x8 __attribute__((ext_vector_type(8)));
typedef short s16x4 __attribute__((ext_vector_type(4)));
typedef float f32x16 __attribute__((ext_vector_type(16)));
typedef float f32x4 __attribute__((ext_vector_type(4)));
typedef unsigned u32x4 __attribute__((ext_vector_type(4)));
typedef unsigned u32x2 __attribute__((ext_vector_type(2)));
typedef unsigned short u16;

constexpr int S = 2048, DM = 576, NH = 32, HD = 128, DC = 512, NQKV = 1728, DHA = 18;
constexpr int NBIG = NH * HD;
constexpr int KS_TOT = 146;

#define SBAR() __builtin_amdgcn_sched_barrier(0)
constexpr size_t WS_Q = 0, WS_K = WS_Q + (size_t)S * NBIG * 2, WS_V = WS_K + (size_t)S * NBIG * 2, WS_AF = WS_V + (size_t)S * NBIG * 2, WS_X = WS_AF + (size_t)128 * KS_TOT * 1024,
                 WS_P = WS_X + (size_t)64 * 16 * 9216, WS_F = WS_P + (size_t)128 * (4 * 32 * 256 + 1024) * 4;
constexpr size_t WS_L = (size_t)96 << 20;
static_assert(WS_F == 96206848, "workspace map is baked into the generated asm");
constexpr int F_EPOCH = 0, F_J = 1024, F_ST = 8192, F_KV = 40960, F_R = 65536;
constexpr unsigned TAG_STEP = 0x9E3779B1u;
__device__ __forceinline__ unsigned ld_flag(const unsigned* p) { return __hip_atomic_load(p, __ATOMIC_RELAXED, __HIP_MEMORY_SCOPE_AGENT); }
__device__ __forceinline__ void st_flag(unsigned* p, unsigned v) { __hip_atomic_store(p, v, __ATOMIC_RELAXED, __HIP_MEMORY_SCOPE_AGENT); }
__device__ __forceinline__ void wait_flags(const unsigned* p, unsigned v, unsigned tag) {
    if (__builtin_expect(__builtin_amdgcn_ballot_w64(v != tag) != 0ull, 0)) {
        unsigned spins = 0;
        do { __builtin_amdgcn_s_sleep(1); } while (__builtin_amdgcn_ballot_w64(ld_flag(p) != tag) != 0ull && ++spins < (1u << 22));
    }
    asm volatile("" ::: "memory");
}

__device__ __forceinline__ unsigned lane_id() { unsigned l; asm volatile("v_mbcnt_lo_u32_b32 %0, -1, 0\n\tv_mbcnt_hi_u32_b32 %0, -1, %0" : "=v"(l)); return l; }
#define RTS(slot) do {} while (0)
#define CT_END 0x1f300
#define ACLOB_LO "a0","a1","a2","a3","a4","a5","a6","a7","a8","a9","a10","a11","a12","a13","a14","a15","a16","a17","a18","a19","a20","a21","a22","a23","a24","a25","a26","a27","a28","a29","a30","a31","a32","a33","a34","a35","a36","a37","a38","a39","a40","a41","a42","a43","a44","a45","a46","a47","a48","a49","a50","a51","a52","a53","a54","a55","a56","a57","a58","a59","a60","a61","a62","a63","a64","a65","a66","a67","a68","a69","a70","a71","a72","a73","a74","a75","a76","a77","a78","a79","a80","a81","a82","a83","a84","a85","a86","a87","a88","a89","a90","a91","a92","a93","a94","a95","a96","a97","a98","a99","a100","a101","a102","a103","a104","a105","a106","a107","a108","a109","a110","a111","a112","a113","a114","a115","a116","a117","a118","a119","a120","a121","a122","a123","a124","a125","a126","a127"
#define ACLOB_HI "a128","a129","a130","a131","a132","a133","a134","a135","a136","a137","a138","a139","a140","a141","a142","a143","a144","a145","a146","a147","a148","a149","a150","a151","a152","a153","a154","a155","a156","a157","a158","a159","a160","a161","a162","a163","a164","a165","a166","a167","a168","a169","a170","a171","a172","a173","a174","a175","a176","a177","a178","a179","a180","a181","a182","a183","a184","a185","a186","a187","a188","a189","a190","a191","a192","a193","a194","a195","a196","a197","a198","a199","a200","a201","a202","a203","a204","a205","a206","a207","a208","a209","a210","a211","a212","a213","a214","a215","a216","a217","a218","a219","a220","a221","a222","a223","a224","a225","a226","a227","a228","a229","a230","a231","a232","a233","a234","a235","a236","a237","a238","a239","a240","a241","a242","a243","a244","a245","a246","a247","a248","a249","a250","a251","a252","a253","a254","a255"
#define PLO(p) ((unsigned)(size_t)(p))
#define PHI(p) ((unsigned)((size_t)(p) >> 32))
__device__ __forceinline__ unsigned vsel(bool c, unsigned a, unsigned b) { asm("" : "+v"(a), "+v"(b)); return c ? a : b; }
__device__ __forceinline__ unsigned ssel(unsigned c, unsigned a, unsigned b) { unsigned r; asm("s_cmp_lg_u32 %1, 0\n\ts_cselect_b32 %0, %2, %3" : "=s"(r) : "s"(c), "s"(a), "s"(b) : "scc"); return r; }
template <int N> __device__ __forceinline__ unsigned sslt(unsigned c, unsigned a, unsigned b) { unsigned r; asm("s_cmpk_lt_u32 %1, %4\n\ts_cselect_b32 %0, %2, %3" : "=s"(r) : "s"(c), "s"(a), "s"(b), "n"(N) : "scc"); return r; }
namespace att {
constexpr int LDS_BYTES = 163840;
constexpr unsigned LDS_G = 159744u, LDS_L = 161792u;
}
#define SD(J8, H, W) ((unsigned)WS_AF + (((J8) + 2u * ((W) & 2u) + ((W) & 1u)) * KS_TOT + 18u + 4u * (H)) * 1024u)
#define SRDA (u32x4){PLO(ws), PHI(ws) & 0xffffu, (unsigned)WS_L, 0x00020000u}
#define PROJ_ASM \
".p2align 3\n" \
"L_pb:\n" \
"s_waitcnt lgkmcnt(0)\n" \
"s_mov_b32 s64, s10\n" \
"s_and_b32 s65, s11, 0xffff\n" \
"s_mov_b32 s66, 0x200000\n" \
"s_mov_b32 s67, 0x20000\n" \
"s_mov_b32 s68, s14\n" \
"s_and_b32 s69, s15, 0xffff\n" \
"s_mov_b32 s70, 0x400000\n" \
"s_mov_b32 s71, 0x20000\n" \
"s_mov_b32 s72, s16\n" \
"s_and_b32 s73, s17, 0xffff\n" \
"s_mov_b32 s74, 0x400000\n" \
"s_mov_b32 s75, 0x20000\n" \
"s_add_u32 m0, s30, 0x0\n" \
"s_movk_i32 s84, 0x0\n" \
"buffer_load_dwordx4 v22, s[64:67], s84 offen offset:0 lds\n" \
"s_movk_i32 s84, 0x1c00\n" \
"buffer_load_dwordx4 v23, s[64:67], s84 offen offset:1024 lds\n" \
"s_movk_i32 s84, 0x3800\n" \
"buffer_load_dwordx4 v22, s[64:67], s84 offen offset:2048 lds\n" \
"s_movk_i32 s84, 0x5400\n" \
"buffer_load_dwordx4 v23, s[64:67], s84 offen offset:3072 lds\n" \
"s_add_u32 m0, s31, 0x0\n" \
"s_movk_i32 s84, 0x0\n" \
"buffer_load_dwordx4 v26, s[68:71], s84 offen offset:0 lds\n" \
"s_movk_i32 s84, 0x7c00\n" \
"buffer_load_dwordx4 v26, s[68:71], s84 offen offset:1024 lds\n" \
"s_addk_i32 s84, 0x7c00\n" \
"buffer_load_dwordx4 v27, s[68:71], s84 offen offset:2048 lds\n" \
"s_addk_i32 s84, 0x7c00\n" \
"buffer_load_dwordx4 v27, s[68:71], s84 offen offset:3072 lds\n" \
"s_movk_i32 s7, 0x300\n" \
"s_lshr_b32 s4, s2, 1\n" \
"s_and_b32 s4, s4, 60\n" \
"s_or_b32 s4, s4, s3\n" \
"v_lshrrev_b32_e32 v212, 4, v20\n" \
"v_min_u32_e32 v212, 59, v212\n" \
"s_cmpk_lt_u32 s2, 0x80\n" \
"s_cselect_b32 s6, s14, s16\n" \
"v_subrev_u32_e32 v214, 48, v212\n" \
"v_mov_b32_e32 v212, s10\n" \
"v_mov_b32_e32 v215, s6\n" \
"s_cmpk_lt_u32 s2, 0x80\n" \
"s_cselect_b32 s5, s15, s17\n" \
"v_cmp_gt_u32_e32 vcc, s7, v20\n" \
"v_mov_b32_e64 v213, s11\n" \
"v_bfe_u32 v216, v20, 4, 3\n" \
"v_cndmask_b32_e32 v212, v212, v215, vcc\n" \
"v_mov_b32_e32 v215, s5\n" \
"s_lshl_b32 s5, s3, 4\n" \
"v_cndmask_b32_e32 v213, v213, v215, vcc\n" \
"v_lshlrev_b32_e64 v215, 5, v20\n" \
"v_and_b32_e32 v215, 0x7000, v215\n" \
"v_lshl_add_u32 v215, s4, 6, v215\n" \
"s_lshl_b32 s4, s2, 3\n" \
"s_and_b32 s4, s4, 56\n" \
"v_or3_b32 v215, v215, s4, v216\n" \
"s_lshl_b32 s4, s2, 6\n" \
"s_or_b32 s6, s5, s4\n" \
"v_cmp_gt_u32_e64 s[4:5], 6, v214\n" \
"v_add_u32_e32 v215, 0x2000, v215\n" \
"s_nop 0\n" \
"v_cndmask_b32_e64 v216, 4, 2, s[4:5]\n" \
"v_add3_u32 v214, s6, v214, v216\n" \
"s_lshl_b32 s4, s3, 9\n" \
"v_cndmask_b32_e32 v214, v214, v215, vcc\n" \
"v_lshlrev_b32_e32 v214, 7, v214\n" \
"v_mov_b32_e64 v215, 0\n" \
"v_lshl_add_u64 v[184:185], v[212:213], 0, v[214:215]\n" \
"s_or_b32 s32, s4, 0x25000\n" \
"s_add_u32 m0, s30, 0x1000\n" \
"s_mov_b32 s84, 0x8000\n" \
"buffer_load_dwordx4 v22, s[64:67], s84 offen offset:0 lds\n" \
"s_and_b32 s33, s3, 1\n" \
"v_lshrrev_b32_e32 v212, 4, v20\n" \
"v_lshrrev_b32_e64 v214, 8, v20\n" \
"v_bfe_u32 v212, v212, 1, 3\n" \
"v_lshlrev_b32_e32 v215, 2, v20\n" \
"s_addk_i32 s84, 0x1c00\n" \
"buffer_load_dwordx4 v23, s[64:67], s84 offen offset:1024 lds\n" \
"v_lshrrev_b32_e64 v216, 1, v20\n" \
"v_bfe_u32 v213, v20, 4, 4\n" \
"s_lshl_b32 s4, s3, 5\n" \
"v_xor_b32_e32 v212, v212, v214\n" \
"v_lshlrev_b32_e32 v214, 11, v214\n" \
"s_addk_i32 s84, 0x1c00\n" \
"buffer_load_dwordx4 v22, s[64:67], s84 offen offset:2048 lds\n" \
"v_and_b32_e32 v215, 0x300, v215\n" \
"v_and_b32_e64 v217, 24, v216\n" \
"v_and_or_b32 v213, s4, 64, v213\n" \
"v_or3_b32 v214, v215, v214, v217\n" \
"s_lshl_b32 s4, s33, 14\n" \
"s_addk_i32 s84, 0x1c00\n" \
"buffer_load_dwordx4 v23, s[64:67], s84 offen offset:3072 lds\n" \
"v_and_b32_e32 v215, 0xe0, v216\n" \
"v_lshlrev_b32_e32 v213, 7, v213\n" \
"v_lshlrev_b32_e32 v212, 4, v212\n" \
"v_or3_b32 v214, v214, s4, v215\n" \
"v_or_b32_e64 v28, v212, v213\n" \
"s_add_u32 m0, s31, 0x4000\n" \
"s_movk_i32 s84, 0x0\n" \
"buffer_load_dwordx4 v26, s[72:75], s84 offen offset:0 lds\n" \
"v_add_u32_e32 v32, 0x10000, v214\n" \
"v_bitop3_b32 v29, v212, 64, v213 bitop3:0x36\n" \
"v_xor_b32_e32 v33, 0x20, v32\n" \
"v_xor_b32_e64 v34, 0x40, v32\n" \
"v_xor_b32_e32 v35, 0x60, v32\n" \
"s_movk_i32 s84, 0x7c00\n" \
"buffer_load_dwordx4 v26, s[72:75], s84 offen offset:1024 lds\n" \
"v_xor_b32_e32 v36, 0x80, v32\n" \
"v_xor_b32_e32 v37, 0xa0, v32\n" \
"v_xor_b32_e32 v38, 0xc0, v32\n" \
"v_xor_b32_e32 v39, 0xe0, v32\n" \
"s_mov_b32 s80, s26\n" \
"s_addk_i32 s84, 0x7c00\n" \
"buffer_load_dwordx4 v27, s[72:75], s84 offen offset:2048 lds\n" \
"s_and_b32 s81, s27, 0xffff\n" \
"s_mov_b32 s82, 0x10000000\n" \
"s_mov_b32 s83, 0x20000\n" \
"s_mov_b32 s76, s18\n" \
"s_and_b32 s77, s19, 0xffff\n" \
"s_addk_i32 s84, 0x7c00\n" \
"buffer_load_dwordx4 v27, s[72:75], s84 offen offset:3072 lds\n" \
"s_mov_b32 s78, 0x1e6000\n" \
"s_mov_b32 s79, 0x20000\n" \
"s_branch L_pgo_%=\n" \
"L_pret:\n" \
"s_setpc_b64 s[88:89]\n" \
"L_pgo_%=:\n" \
"s_mul_i32 s88, s33, 0xa000\n" \
"s_lshl_b32 s89, s3, 11\n" \
"s_and_b32 s89, s89, 0x1000\n" \
"s_add_u32 s88, s88, s89\n" \
"s_getpc_b64 s[86:87]\n" \
"L_hpa_%=:\n" \
"s_add_u32 s86, s86, L_pp1-L_hpa_%=\n" \
"s_addc_u32 s87, s87, 0\n" \
"s_add_u32 s86, s86, s88\n" \
"s_addc_u32 s87, s87, 0\n" \
"s_add_u32 m0, s30, 0x8000\n" \
"s_movk_i32 s84, 0x80\n" \
"buffer_load_dwordx4 v22, s[64:67], s84 offen offset:0 lds\n" \
"s_lshr_b32 s4, s2, 1\n" \
"s_and_b32 s4, s4, 0x7c\n" \
"v_bfe_u32 v212, v20, 4, 5\n" \
"s_movk_i32 s84, 0x1c80\n" \
"buffer_load_dwordx4 v23, s[64:67], s84 offen offset:1024 lds\n" \
"s_or_b32 s4, s4, s3\n" \
"v_min_u32_e32 v212, 26, v212\n" \
"v_mad_u64_u32 v[212:213], s[4:5], s4, 27, v[212:213]\n" \
"s_movk_i32 s84, 0x3880\n" \
"buffer_load_dwordx4 v22, s[64:67], s84 offen offset:2048 lds\n" \
"v_mul_u32_u24_e32 v213, 0xaaab, v212\n" \
"v_lshrrev_b32_e32 v213, 18, v213\n" \
"s_mov_b32 s4, 0x1fffffa\n" \
"s_movk_i32 s84, 0x5480\n" \
"buffer_load_dwordx4 v23, s[64:67], s84 offen offset:3072 lds\n" \
"v_mad_u64_u32 v[214:215], s[4:5], v213, s4, v[212:213]\n" \
"s_and_b32 s6, s2, 7\n" \
"v_mul_u32_u24_e32 v213, 27, v213\n" \
"s_mov_b64 exec, 0\n" \
"s_swappc_b64 s[88:89], s[86:87]\n" \
"s_mov_b64 exec, -1\n" \
"s_add_u32 s86, s86, 0x2000\n" \
"s_addc_u32 s87, s87, 0\n" \
"s_add_u32 m0, s31, 0x8000\n" \
"s_mov_b32 s84, 0x80000\n" \
"buffer_load_dwordx4 v26, s[68:71], s84 offen offset:0 lds\n" \
"v_lshrrev_b32_e32 v214, 1, v214\n" \
"v_and_b32_e32 v212, 1, v212\n" \
"v_mul_u32_u24_e32 v214, 9, v214\n" \
"s_addk_i32 s84, 0x7c00\n" \
"buffer_load_dwordx4 v26, s[68:71], s84 offen offset:1024 lds\n" \
"v_add3_u32 v212, v212, s6, v213\n" \
"v_add_lshl_u32 v212, v212, v214, 7\n" \
"v_mov_b32_e32 v213, 0\n" \
"s_mov_b64 exec, 0\n" \
"s_swappc_b64 s[88:89], s[86:87]\n" \
"s_mov_b64 exec, -1\n" \
"s_add_u32 s86, s86, 0x2000\n" \
"s_addc_u32 s87, s87, 0\n" \
"s_addk_i32 s84, 0x7c00\n" \
"buffer_load_dwordx4 v27, s[68:71], s84 offen offset:2048 lds\n" \
"v_lshl_add_u64 v[186:187], s[18:19], 0, v[212:213]\n" \
"s_lshl_b32 s35, s2, 10\n" \
"s_lshr_b32 s37, s2, 1\n" \
"s_addk_i32 s84, 0x7c00\n" \
"buffer_load_dwordx4 v27, s[68:71], s84 offen offset:3072 lds\n" \
"s_mul_i32 s37, s37, 0x24800\n" \
"s_and_b32 s35, s35, 0x400\n" \
"s_lshl_b32 s36, s3, 8\n" \
"s_mov_b64 exec, 0\n" \
"s_swappc_b64 s[88:89], s[86:87]\n" \
"s_mov_b64 exec, -1\n" \
"s_add_u32 s86, s86, 0x2000\n" \
"s_addc_u32 s87, s87, 0\n" \
"s_add_u32 m0, s30, 0x9000\n" \
"s_mov_b32 s84, 0x8080\n" \
"buffer_load_dwordx4 v22, s[64:67], s84 offen offset:0 lds\n" \
"s_or_b32 s35, s37, s35\n" \
"s_and_b32 s4, s2, 7\n" \
"s_lshr_b32 s6, s2, 5\n" \
"s_addk_i32 s84, 0x1c00\n" \
"buffer_load_dwordx4 v23, s[64:67], s84 offen offset:1024 lds\n" \
"s_lshl_b32 s34, s2, 5\n" \
"s_add_i32 s35, s35, s36\n" \
"s_lshl_b32 s7, s4, 10\n" \
"s_addk_i32 s84, 0x1c00\n" \
"buffer_load_dwordx4 v22, s[64:67], s84 offen offset:2048 lds\n" \
"s_and_b32 s34, s34, 0x300\n" \
"s_add_i32 s35, s35, 0x3004000\n" \
"s_lshl_b32 s37, s6, 13\n" \
"s_mov_b64 exec, 0\n" \
"s_swappc_b64 s[88:89], s[86:87]\n" \
"s_mov_b64 exec, -1\n" \
"s_andn2_b32 s85, 2, s3\n" \
"s_lshl_b32 s85, s85, 12\n" \
"s_add_u32 s86, s86, s85\n" \
"s_addc_u32 s87, s87, 0\n" \
"s_addk_i32 s84, 0x1c00\n" \
"buffer_load_dwordx4 v23, s[64:67], s84 offen offset:3072 lds\n" \
"s_or_b32 s34, s7, s34\n" \
"s_or_b32 s7, s37, s7\n" \
"v_mov_b32_e64 v213, s35\n" \
"s_add_u32 m0, s31, 0xc000\n" \
"s_mov_b32 s84, 0x80000\n" \
"buffer_load_dwordx4 v26, s[72:75], s84 offen offset:0 lds\n" \
"s_or_b32 s7, s7, s36\n" \
"s_lshr_b32 s5, s2, 3\n" \
"s_or_b32 s7, s7, 0x5c00000\n" \
"s_mov_b64 exec, 0\n" \
"s_swappc_b64 s[88:89], s[86:87]\n" \
"s_mov_b64 exec, -1\n" \
"s_addk_i32 s84, 0x7c00\n" \
"buffer_load_dwordx4 v26, s[72:75], s84 offen offset:1024 lds\n" \
"v_writelane_b32 v213, s7, 2\n" \
"s_mul_i32 s5, s5, 0x90000\n" \
"v_writelane_b32 v213, s7, 3\n" \
"s_addk_i32 s84, 0x7c00\n" \
"buffer_load_dwordx4 v27, s[72:75], s84 offen offset:2048 lds\n" \
"s_lshl_b32 s7, s3, 14\n" \
"s_lshl_b32 s4, s4, 16\n" \
"s_add_i32 s5, s5, s7\n" \
"s_addk_i32 s84, 0x7c00\n" \
"buffer_load_dwordx4 v27, s[72:75], s84 offen offset:3072 lds\n" \
"s_or_b32 s34, s34, 0x5be8000\n" \
"v_writelane_b32 v213, s34, 4\n" \
"s_add_i32 s4, s5, s4\n" \
"v_writelane_b32 v213, s34, 5\n" \
"s_add_i32 s4, s4, 0x4243800\n" \
"v_writelane_b32 v213, s4, 6\n" \
"s_mul_i32 s4, s6, 0xa00\n" \
"v_lshlrev_b32_e32 v212, 3, v20\n" \
"s_lshl_b32 s5, s3, 7\n" \
"v_and_b32_e32 v212, 0x80, v212\n" \
"s_add_i32 s4, s4, s5\n" \
"s_add_i32 s4, s4, 0x5bc0f80\n" \
"v_writelane_b32 v213, s4, 7\n" \
"v_add_u32_e32 v49, v213, v212\n" \
"s_mov_b32 m0, s32\n" \
"s_mov_b32 exec_lo, 0xffffff\n" \
"s_mov_b32 exec_hi, 0xfff0000\n" \
"global_load_lds_dword v[184:185], off\n" \
"s_mov_b64 exec, -1\n" \
"s_mov_b32 m0, s32\n" \
"s_mov_b64 exec, 0xff\n" \
"buffer_load_dword v49, s[80:83], 0 offen lds\n" \
"s_mov_b64 exec, -1\n" \
"s_mov_b32 s84, 0x5bc0000\n" \
"buffer_load_dword v180, off, s[80:83], s84 sc1\n" \
"s_cmp_lg_u32 s33, 0\n" \
"s_cbranch_scc1 L_w1_%=\n" \
"L_w0_%=:\n" \
"s_waitcnt vmcnt(19)\n" \
"s_barrier\n" \
"ds_read_b64_tr_b16 a[160:161], v32 offset:0\n" \
"ds_read_b64_tr_b16 a[162:163], v32 offset:1024\n" \
"ds_read_b128 a[128:131], v28 offset:16384\n" \
"ds_read_b64_tr_b16 a[224:225], v32 offset:8192\n" \
"ds_read_b64_tr_b16 a[226:227], v32 offset:9216\n" \
"ds_read_b128 a[192:195], v29 offset:16384\n" \
"ds_read_b128 a[132:135], v28 offset:18432\n" \
"ds_read_b128 a[196:199], v29 offset:18432\n" \
"ds_read_b64_tr_b16 a[164:165], v33 offset:0\n" \
"ds_read_b64_tr_b16 a[166:167], v33 offset:1024\n" \
"ds_read_b64_tr_b16 a[228:229], v33 offset:8192\n" \
"ds_read_b64_tr_b16 a[230:231], v33 offset:9216\n" \
"ds_read_b128 a[136:139], v28 offset:20480\n" \
"ds_read_b128 a[200:203], v29 offset:20480\n" \
"s_waitcnt lgkmcnt(11)\n" \
"v_mfma_f32_16x16x32_bf16 a[0:3], a[160:163], a[128:131], 0\n" \
"ds_read_b64_tr_b16 a[168:169], v34 offset:0\n" \
"s_lshr_b32 s4, s2, 1\n" \
"s_waitcnt lgkmcnt(9)\n" \
"v_mfma_f32_16x16x32_bf16 a[0:3], a[224:227], a[192:195], a[0:3]\n" \
"ds_read_b64_tr_b16 a[170:171], v34 offset:1024\n" \
"s_and_b32 s4, s4, 0x7c\n" \
"s_waitcnt lgkmcnt(9)\n" \
"v_mfma_f32_16x16x32_bf16 a[16:19], a[160:163], a[132:135], 0\n" \
"ds_read_b64_tr_b16 a[232:233], v34 offset:8192\n" \
"v_mov_b32_e32 v213, s13\n" \
"s_waitcnt lgkmcnt(9)\n" \
"v_mfma_f32_16x16x32_bf16 a[16:19], a[224:227], a[196:199], a[16:19]\n" \
"ds_read_b64_tr_b16 a[234:235], v34 offset:9216\n" \
"s_or_b32 s4, s4, s3\n" \
"s_waitcnt lgkmcnt(8)\n" \
"v_mfma_f32_16x16x32_bf16 a[20:23], a[164:167], a[132:135], 0\n" \
"ds_read_b128 a[140:143], v28 offset:22528\n" \
"v_lshrrev_b32_e32 v212, 4, v20\n" \
"s_waitcnt lgkmcnt(7)\n" \
"v_mfma_f32_16x16x32_bf16 a[20:23], a[228:231], a[196:199], a[20:23]\n" \
"ds_read_b128 a[204:207], v29 offset:22528\n" \
"v_mad_u64_u32 v[214:215], s[4:5], s4, 36, v[212:213]\n" \
"v_mfma_f32_16x16x32_bf16 a[4:7], a[164:167], a[128:131], 0\n" \
"ds_read_b64_tr_b16 a[172:173], v35 offset:0\n" \
"v_mov_b32_e64 v217, s9\n" \
"v_mfma_f32_16x16x32_bf16 a[4:7], a[228:231], a[192:195], a[4:7]\n" \
"ds_read_b64_tr_b16 a[174:175], v35 offset:1024\n" \
"s_movk_i32 s4, 0x240\n" \
"s_waitcnt lgkmcnt(9)\n" \
"v_mfma_f32_16x16x32_bf16 a[32:35], a[160:163], a[136:139], 0\n" \
"ds_read_b64_tr_b16 a[236:237], v35 offset:8192\n" \
"v_cmp_gt_u32_e32 vcc, s4, v20\n" \
"s_waitcnt lgkmcnt(9)\n" \
"v_mfma_f32_16x16x32_bf16 a[32:35], a[224:227], a[200:203], a[32:35]\n" \
"ds_read_b64_tr_b16 a[238:239], v35 offset:9216\n" \
"s_lshl_b32 s4, s2, 3\n" \
"v_mfma_f32_16x16x32_bf16 a[36:39], a[164:167], a[136:139], 0\n" \
"ds_read_b128 a[144:147], v28 offset:0\n" \
"s_and_b32 s4, s4, 56\n" \
"v_mfma_f32_16x16x32_bf16 a[36:39], a[228:231], a[200:203], a[36:39]\n" \
"ds_read_b128 a[208:211], v29 offset:0\n" \
"v_cndmask_b32_e32 v217, v217, v213, vcc\n" \
"s_waitcnt lgkmcnt(10)\n" \
"v_mfma_f32_16x16x32_bf16 a[40:43], a[168:171], a[136:139], 0\n" \
"ds_read_b128 a[148:151], v28 offset:2048\n" \
"v_lshlrev_b32_e32 v213, 3, v214\n" \
"s_waitcnt lgkmcnt(9)\n" \
"v_mfma_f32_16x16x32_bf16 a[40:43], a[232:235], a[200:203], a[40:43]\n" \
"ds_read_b128 a[212:215], v29 offset:2048\n" \
"v_and_b32_e32 v213, 0x1ffc0, v213\n" \
"v_mfma_f32_16x16x32_bf16 a[24:27], a[168:171], a[132:135], 0\n" \
"ds_read_b128 a[152:155], v28 offset:4096\n" \
"v_and_b32_e64 v214, 7, v214\n" \
"v_mfma_f32_16x16x32_bf16 a[24:27], a[232:235], a[196:199], a[24:27]\n" \
"ds_read_b128 a[216:219], v29 offset:4096\n" \
"v_or3_b32 v213, v214, s4, v213\n" \
"v_mfma_f32_16x16x32_bf16 a[8:11], a[168:171], a[128:131], 0\n" \
"ds_read_b128 a[156:159], v28 offset:6144\n" \
"s_lshl_b32 s4, s2, 2\n" \
"v_mfma_f32_16x16x32_bf16 a[8:11], a[232:235], a[192:195], a[8:11]\n" \
"ds_read_b128 a[220:223], v29 offset:6144\n" \
"s_mov_b32 m0, s32\n" \
"s_waitcnt lgkmcnt(13)\n" \
"v_mfma_f32_16x16x32_bf16 a[48:51], a[160:163], a[140:143], 0\n" \
"s_mov_b32 exec_lo, 0xff000000\n" \
"s_mov_b32 exec_hi, 0x0\n" \
"global_load_lds_dword v[184:185], off\n" \
"s_mov_b64 exec, -1\n" \
"s_waitcnt lgkmcnt(12)\n" \
"v_mfma_f32_16x16x32_bf16 a[48:51], a[224:227], a[204:207], a[48:51]\n" \
"ds_read_b64_tr_b16 a[176:177], v36 offset:0\n" \
"s_or_b32 s4, s4, s3\n" \
"v_mfma_f32_16x16x32_bf16 a[52:55], a[164:167], a[140:143], 0\n" \
"ds_read_b64_tr_b16 a[178:179], v36 offset:1024\n" \
"s_mul_i32 s4, s4, 18\n" \
"v_mfma_f32_16x16x32_bf16 a[52:55], a[228:231], a[204:207], a[52:55]\n" \
"s_mov_b32 m0, s32\n" \
"ds_read_b64_tr_b16 a[240:241], v36 offset:8192\n" \
"v_mfma_f32_16x16x32_bf16 a[56:59], a[168:171], a[140:143], 0\n" \
"s_mov_b32 exec_lo, 0xffff\n" \
"s_mov_b32 exec_hi, 0x0\n" \
"global_load_lds_dword v[186:187], off\n" \
"s_mov_b64 exec, -1\n" \
"v_mfma_f32_16x16x32_bf16 a[56:59], a[232:235], a[204:207], a[56:59]\n" \
"ds_read_b64_tr_b16 a[242:243], v36 offset:9216\n" \
"v_min_u32_e32 v212, 53, v212\n" \
"s_waitcnt lgkmcnt(14)\n" \
"v_mfma_f32_16x16x32_bf16 a[60:63], a[172:175], a[140:143], 0\n" \
"ds_read_b64_tr_b16 a[180:181], v37 offset:0\n" \
"s_sub_i32 s4, s4, 36\n" \
"s_waitcnt lgkmcnt(13)\n" \
"v_mfma_f32_16x16x32_bf16 a[60:63], a[236:239], a[204:207], a[60:63]\n" \
"ds_read_b64_tr_b16 a[182:183], v37 offset:1024\n" \
"v_add_u32_e32 v212, s4, v212\n" \
"v_mfma_f32_16x16x32_bf16 a[44:47], a[172:175], a[136:139], 0\n" \
"ds_read_b64_tr_b16 a[244:245], v37 offset:8192\n" \
"v_mov_b32_e64 v215, s12\n" \
"v_mfma_f32_16x16x32_bf16 a[44:47], a[236:239], a[200:203], a[44:47]\n" \
"ds_read_b64_tr_b16 a[246:247], v37 offset:9216\n" \
"v_mov_b32_e64 v216, s8\n" \
"v_mfma_f32_16x16x32_bf16 a[28:31], a[172:175], a[132:135], 0\n" \
"ds_read_b64_tr_b16 a[184:185], v38 offset:0\n" \
"v_cndmask_b32_e64 v212, v212, v213, vcc\n" \
"v_mfma_f32_16x16x32_bf16 a[28:31], a[236:239], a[196:199], a[28:31]\n" \
"ds_read_b64_tr_b16 a[186:187], v38 offset:1024\n" \
"v_cndmask_b32_e64 v216, v216, v215, vcc\n" \
"v_mfma_f32_16x16x32_bf16 a[12:15], a[172:175], a[128:131], 0\n" \
"v_lshlrev_b32_e32 v212, 7, v212\n" \
"v_mov_b32_e32 v213, 0\n" \
"v_mfma_f32_16x16x32_bf16 a[12:15], a[236:239], a[192:195], a[12:15]\n" \
"v_lshl_add_u64 v[188:189], v[216:217], 0, v[212:213]\n" \
"s_movk_i32 s5, 0xd80\n" \
"s_waitcnt lgkmcnt(15)\n" \
"v_mfma_f32_16x16x32_bf16 a[76:79], a[172:175], a[144:147], 0\n" \
"ds_read_b64_tr_b16 a[248:249], v38 offset:8192\n" \
"s_lshr_b32 s6, s2, 5\n" \
"v_mfma_f32_16x16x32_bf16 a[76:79], a[236:239], a[208:211], a[76:79]\n" \
"v_lshrrev_b32_e32 v212, 4, v20\n" \
"v_lshl_or_b32 v215, s3, 6, v212\n" \
"v_mfma_f32_16x16x32_bf16 a[72:75], a[168:171], a[144:147], 0\n" \
"v_mul_u32_u24_e32 v216, 0x2aab, v215\n" \
"v_lshrrev_b32_e64 v216, 16, v216\n" \
"v_mfma_f32_16x16x32_bf16 a[72:75], a[232:235], a[208:211], a[72:75]\n" \
"ds_read_b64_tr_b16 a[250:251], v38 offset:9216\n" \
"v_mad_i32_i24 v217, v216, -6, v215\n" \
"v_mfma_f32_16x16x32_bf16 a[68:71], a[164:167], a[144:147], 0\n" \
"v_add_u32_e32 v218, 0xfffffe50, v215\n" \
"s_mul_i32 s35, s6, 0x48\n" \
"v_mfma_f32_16x16x32_bf16 a[68:71], a[228:231], a[208:211], a[68:71]\n" \
"v_lshrrev_b32_e32 v219, 1, v217\n" \
"v_add_u32_e32 v218, s35, v216\n" \
"v_mfma_f32_16x16x32_bf16 a[64:67], a[160:163], a[144:147], 0\n" \
"v_mul_u32_u24_e32 v219, 0x480, v219\n" \
"v_and_b32_e64 v214, 16, v20\n" \
"v_mfma_f32_16x16x32_bf16 a[64:67], a[224:227], a[208:211], a[64:67]\n" \
"ds_read_b64_tr_b16 a[188:189], v39 offset:0\n" \
"v_mad_u32_u24 v219, v218, s5, v219\n" \
"v_mfma_f32_16x16x32_bf16 a[80:83], a[160:163], a[148:151], 0\n" \
"v_or_b32_e32 v192, v219, v214\n" \
"v_mov_b32_e32 v220, 2.0\n" \
"s_waitcnt lgkmcnt(15)\n" \
"v_mfma_f32_16x16x32_bf16 a[80:83], a[224:227], a[212:215], a[80:83]\n" \
"v_mov_b32_e32 v222, 7\n" \
"v_lshlrev_b32_e64 v220, 7, v218\n" \
"v_mfma_f32_16x16x32_bf16 a[84:87], a[164:167], a[148:151], 0\n" \
"ds_read_b64_tr_b16 a[190:191], v39 offset:1024\n" \
"v_lshrrev_b32_e64 v218, 1, v218\n" \
"v_mfma_f32_16x16x32_bf16 a[84:87], a[228:231], a[212:215], a[84:87]\n" \
"v_and_b32_e32 v218, 4, v218\n" \
"v_and_b32_e32 v216, 2, v216\n" \
"v_mfma_f32_16x16x32_bf16 a[88:91], a[168:171], a[148:151], 0\n" \
"s_mul_i32 s7, s6, 0x2400\n" \
"s_sub_i32 s34, 0x22000, s7\n" \
"v_mfma_f32_16x16x32_bf16 a[88:91], a[232:235], a[212:215], a[88:91]\n" \
"ds_read_b64_tr_b16 a[252:253], v39 offset:8192\n" \
"v_bitop3_b32 v216, v217, v218, v216 bitop3:0x1e\n" \
"v_mfma_f32_16x16x32_bf16 a[92:95], a[172:175], a[148:151], 0\n" \
"v_or_b32_e32 v213, 0x24400, v20\n" \
"v_lshlrev_b32_e64 v216, 4, v216\n" \
"v_mfma_f32_16x16x32_bf16 a[92:95], a[236:239], a[212:215], a[92:95]\n" \
"v_add3_u32 v196, v220, s34, v216\n" \
"v_mov_b32_e64 v216, v213\n" \
"v_mfma_f32_16x16x32_bf16 a[108:111], a[172:175], a[152:155], 0\n" \
"ds_read_b64_tr_b16 a[254:255], v39 offset:9216\n" \
"s_movk_i32 s36, 0x1b0\n" \
"v_mfma_f32_16x16x32_bf16 a[108:111], a[236:239], a[216:219], a[108:111]\n" \
"v_or_b32_e32 v216, 0x100, v215\n" \
"v_mul_u32_u24_e32 v217, 0x2aab, v216\n" \
"v_mfma_f32_16x16x32_bf16 a[104:107], a[168:171], a[152:155], 0\n" \
"v_add_u32_e32 v215, 0xffffff50, v215\n" \
"v_lshrrev_b32_e32 v217, 16, v217\n" \
"v_mfma_f32_16x16x32_bf16 a[104:107], a[232:235], a[216:219], a[104:107]\n" \
"v_mov_b32_e64 v220, v215\n" \
"v_mad_i32_i24 v218, v217, -6, v216\n" \
"v_mfma_f32_16x16x32_bf16 a[100:103], a[164:167], a[152:155], 0\n" \
"v_cmp_gt_u32_e32 vcc, s36, v216\n" \
"v_mov_b32_e32 v221, 7\n" \
"v_mfma_f32_16x16x32_bf16 a[100:103], a[228:231], a[216:219], a[100:103]\n" \
"v_lshrrev_b32_e32 v222, 1, v218\n" \
"v_cndmask_b32_e32 v217, v220, v217, vcc\n" \
"v_mfma_f32_16x16x32_bf16 a[96:99], a[160:163], a[152:155], 0\n" \
"s_movk_i32 s4, 0x48\n" \
"v_add_u32_e32 v220, s35, v217\n" \
"v_mfma_f32_16x16x32_bf16 a[96:99], a[224:227], a[216:219], a[96:99]\n" \
"v_mul_u32_u24_e32 v222, 0x480, v222\n" \
"v_mad_u32_u24 v222, v220, s5, v222\n" \
"s_waitcnt lgkmcnt(15)\n" \
"v_mfma_f32_16x16x32_bf16 a[112:115], a[160:163], a[156:159], 0\n" \
"v_cmp_gt_u32_e64 s[4:5], s4, v215\n" \
"v_cndmask_b32_e32 v215, v221, v218, vcc\n" \
"v_mfma_f32_16x16x32_bf16 a[112:115], a[224:227], a[220:223], a[112:115]\n" \
"v_lshrrev_b32_e32 v218, 1, v220\n" \
"v_or_b32_e32 v214, v222, v214\n" \
"v_mfma_f32_16x16x32_bf16 a[116:119], a[164:167], a[156:159], 0\n" \
"v_mov_b32_e32 v222, 2.0\n" \
"v_and_b32_e32 v218, 4, v218\n" \
"v_mfma_f32_16x16x32_bf16 a[116:119], a[228:231], a[220:223], a[116:119]\n" \
"v_and_b32_e64 v217, 2, v217\n" \
"v_bitop3_b32 v215, v215, v218, v217 bitop3:0x1e\n" \
"v_mfma_f32_16x16x32_bf16 a[120:123], a[168:171], a[156:159], 0\n" \
"v_lshlrev_b32_e32 v223, 1, v220\n" \
"v_mov_b32_e32 v224, 2.0\n" \
"v_mfma_f32_16x16x32_bf16 a[120:123], a[232:235], a[220:223], a[120:123]\n" \
"v_cndmask_b32_e32 v193, v222, v214, vcc\n" \
"v_lshlrev_b32_e32 v214, 7, v220\n" \
"v_mfma_f32_16x16x32_bf16 a[124:127], a[172:175], a[156:159], 0\n" \
"v_lshlrev_b32_e32 v215, 4, v215\n" \
"s_mul_i32 s39, s3, 18\n" \
"s_waitcnt vmcnt(5) lgkmcnt(0)\n" \
"v_mfma_f32_16x16x32_bf16 a[124:127], a[236:239], a[220:223], a[124:127]\n" \
"s_barrier\n" \
"v_mfma_f32_16x16x32_bf16 v[52:55], a[176:179], a[128:131], 0\n" \
"s_add_u32 m0, s30, 0x0\n" \
"s_movk_i32 s84, 0x100\n" \
"v_mfma_f32_16x16x32_bf16 v[52:55], a[240:243], a[192:195], v[52:55]\n" \
"buffer_load_dwordx4 v22, s[64:67], s84 offen offset:0 lds\n" \
"v_mfma_f32_16x16x32_bf16 v[56:59], a[180:183], a[128:131], 0\n" \
"ds_read_b64_tr_b16 a[160:161], v32 offset:32768\n" \
"v_add3_u32 v214, v214, s34, v215\n" \
"v_mfma_f32_16x16x32_bf16 v[56:59], a[244:247], a[192:195], v[56:59]\n" \
"s_movk_i32 s84, 0x1d00\n" \
"v_mfma_f32_16x16x32_bf16 v[60:63], a[184:187], a[128:131], 0\n" \
"buffer_load_dwordx4 v23, s[64:67], s84 offen offset:1024 lds\n" \
"v_mfma_f32_16x16x32_bf16 v[60:63], a[248:251], a[192:195], v[60:63]\n" \
"ds_read_b64_tr_b16 a[162:163], v32 offset:33792\n" \
"v_cndmask_b32_e64 v195, v224, v223, s[4:5]\n" \
"v_mfma_f32_16x16x32_bf16 v[64:67], a[188:191], a[128:131], 0\n" \
"ds_read_b64_tr_b16 a[224:225], v32 offset:40960\n" \
"s_movk_i32 s4, 0x1f8\n" \
"v_mfma_f32_16x16x32_bf16 v[64:67], a[252:255], a[192:195], v[64:67]\n" \
"ds_read_b128 a[128:131], v28 offset:49152\n" \
"v_mov_b32_e64 v215, v213\n" \
"v_mfma_f32_16x16x32_bf16 v[80:83], a[188:191], a[132:135], 0\n" \
"ds_read_b128 a[192:195], v29 offset:49152\n" \
"s_add_i32 s35, s35, s39\n" \
"v_mfma_f32_16x16x32_bf16 v[80:83], a[252:255], a[196:199], v[80:83]\n" \
"s_movk_i32 s84, 0x3900\n" \
"v_mfma_f32_16x16x32_bf16 v[76:79], a[184:187], a[132:135], 0\n" \
"buffer_load_dwordx4 v22, s[64:67], s84 offen offset:2048 lds\n" \
"v_mfma_f32_16x16x32_bf16 v[76:79], a[248:251], a[196:199], v[76:79]\n" \
"s_movk_i32 s84, 0x5500\n" \
"v_mfma_f32_16x16x32_bf16 v[72:75], a[180:183], a[132:135], 0\n" \
"buffer_load_dwordx4 v23, s[64:67], s84 offen offset:3072 lds\n" \
"v_mfma_f32_16x16x32_bf16 v[72:75], a[244:247], a[196:199], v[72:75]\n" \
"s_add_u32 m0, s31, 0x0\n" \
"s_mov_b32 s84, 0x100000\n" \
"v_mfma_f32_16x16x32_bf16 v[68:71], a[176:179], a[132:135], 0\n" \
"buffer_load_dwordx4 v26, s[68:71], s84 offen offset:0 lds\n" \
"v_mfma_f32_16x16x32_bf16 v[68:71], a[240:243], a[196:199], v[68:71]\n" \
"ds_read_b128 a[132:135], v28 offset:51200\n" \
"v_cmp_gt_u32_e64 vcc, s4, v216\n" \
"v_mfma_f32_16x16x32_bf16 v[84:87], a[176:179], a[136:139], 0\n" \
"ds_read_b128 a[196:199], v29 offset:51200\n" \
"v_add_u32_e64 v212, s35, v212\n" \
"v_mfma_f32_16x16x32_bf16 v[84:87], a[240:243], a[200:203], v[84:87]\n" \
"s_addk_i32 s84, 0x7c00\n" \
"v_mfma_f32_16x16x32_bf16 v[88:91], a[180:183], a[136:139], 0\n" \
"buffer_load_dwordx4 v26, s[68:71], s84 offen offset:1024 lds\n" \
"v_mfma_f32_16x16x32_bf16 v[88:91], a[244:247], a[200:203], v[88:91]\n" \
"ds_read_b64_tr_b16 a[226:227], v32 offset:41984\n" \
"v_mov_b32_e32 v219, 2.0\n" \
"v_mfma_f32_16x16x32_bf16 v[92:95], a[184:187], a[136:139], 0\n" \
"s_addk_i32 s84, 0x7c00\n" \
"v_mfma_f32_16x16x32_bf16 v[92:95], a[248:251], a[200:203], v[92:95]\n" \
"buffer_load_dwordx4 v27, s[68:71], s84 offen offset:2048 lds\n" \
"v_mfma_f32_16x16x32_bf16 v[96:99], a[188:191], a[136:139], 0\n" \
"ds_read_b64_tr_b16 a[164:165], v33 offset:32768\n" \
"v_cndmask_b32_e32 v197, v215, v214, vcc\n" \
"v_mfma_f32_16x16x32_bf16 v[96:99], a[252:255], a[200:203], v[96:99]\n" \
"ds_read_b128 a[136:139], v28 offset:53248\n" \
"s_movk_i32 s35, 0x120\n" \
"v_mfma_f32_16x16x32_bf16 v[112:115], a[188:191], a[140:143], 0\n" \
"ds_read_b128 a[200:203], v29 offset:53248\n" \
"v_mul_u32_u24_e32 v214, 0xd80, v212\n" \
"v_mfma_f32_16x16x32_bf16 v[112:115], a[252:255], a[204:207], v[112:115]\n" \
"s_addk_i32 s84, 0x7c00\n" \
"v_mfma_f32_16x16x32_bf16 v[108:111], a[184:187], a[140:143], 0\n" \
"buffer_load_dwordx4 v27, s[68:71], s84 offen offset:3072 lds\n" \
"v_mfma_f32_16x16x32_bf16 v[108:111], a[248:251], a[204:207], v[108:111]\n" \
"s_add_u32 m0, s30, 0x1000\n" \
"s_mov_b32 s84, 0x8100\n" \
"v_mfma_f32_16x16x32_bf16 v[104:107], a[180:183], a[140:143], 0\n" \
"buffer_load_dwordx4 v22, s[64:67], s84 offen offset:0 lds\n" \
"v_mfma_f32_16x16x32_bf16 v[104:107], a[244:247], a[204:207], v[104:107]\n" \
"ds_read_b64_tr_b16 a[166:167], v33 offset:33792\n" \
"v_cmp_gt_u32_e64 vcc, s35, v20\n" \
"v_mfma_f32_16x16x32_bf16 v[100:103], a[176:179], a[140:143], 0\n" \
"ds_read_b64_tr_b16 a[228:229], v33 offset:40960\n" \
"v_lshrrev_b32_e64 v215, 1, v212\n" \
"v_mfma_f32_16x16x32_bf16 v[100:103], a[240:243], a[204:207], v[100:103]\n" \
"ds_read_b128 a[140:143], v28 offset:55296\n" \
"s_and_b32 s4, s2, 7\n" \
"v_mfma_f32_16x16x32_bf16 v[116:119], a[176:179], a[144:147], 0\n" \
"ds_read_b128 a[204:207], v29 offset:55296\n" \
"v_cndmask_b32_e32 v194, v219, v214, vcc\n" \
"v_mfma_f32_16x16x32_bf16 v[116:119], a[240:243], a[208:211], v[116:119]\n" \
"s_addk_i32 s84, 0x1c00\n" \
"v_mfma_f32_16x16x32_bf16 v[120:123], a[180:183], a[144:147], 0\n" \
"buffer_load_dwordx4 v23, s[64:67], s84 offen offset:1024 lds\n" \
"v_mfma_f32_16x16x32_bf16 v[120:123], a[244:247], a[208:211], v[120:123]\n" \
"s_addk_i32 s84, 0x1c00\n" \
"v_mfma_f32_16x16x32_bf16 v[124:127], a[184:187], a[144:147], 0\n" \
"buffer_load_dwordx4 v22, s[64:67], s84 offen offset:2048 lds\n" \
"v_mfma_f32_16x16x32_bf16 v[124:127], a[248:251], a[208:211], v[124:127]\n" \
"s_addk_i32 s84, 0x1c00\n" \
".fill (L_pb + 3808 - .) / 4, 4, 0xBF800000\n" \
"L_pp1:\n" \
"s_cbranch_execz L_pret\n" \
"v_mfma_f32_16x16x32_bf16 v[128:131], a[188:191], a[144:147], 0\n" \
"buffer_load_dwordx4 v23, s[64:67], s84 offen offset:3072 lds\n" \
"v_mfma_f32_16x16x32_bf16 v[128:131], a[252:255], a[208:211], v[128:131]\n" \
"ds_read_b128 a[144:147], v28 offset:32768\n" \
"v_lshlrev_b32_e32 v214, 7, v212\n" \
"v_mfma_f32_16x16x32_bf16 v[144:147], a[188:191], a[148:151], 0\n" \
"ds_read_b128 a[208:211], v29 offset:32768\n" \
"v_lshrrev_b32_e64 v212, 2, v212\n" \
"v_mfma_f32_16x16x32_bf16 v[144:147], a[252:255], a[212:215], v[144:147]\n" \
"s_add_u32 m0, s31, 0x4000\n" \
"s_mov_b32 s84, 0x100000\n" \
"v_mfma_f32_16x16x32_bf16 v[140:143], a[184:187], a[148:151], 0\n" \
"buffer_load_dwordx4 v26, s[72:75], s84 offen offset:0 lds\n" \
"v_mfma_f32_16x16x32_bf16 v[140:143], a[248:251], a[212:215], v[140:143]\n" \
"ds_read_b64_tr_b16 a[230:231], v33 offset:41984\n" \
"v_and_b32_e64 v212, 2, v212\n" \
"v_mfma_f32_16x16x32_bf16 v[136:139], a[180:183], a[148:151], 0\n" \
"s_addk_i32 s84, 0x7c00\n" \
"v_mfma_f32_16x16x32_bf16 v[136:139], a[244:247], a[212:215], v[136:139]\n" \
"buffer_load_dwordx4 v26, s[72:75], s84 offen offset:1024 lds\n" \
"v_mfma_f32_16x16x32_bf16 v[132:135], a[176:179], a[148:151], 0\n" \
"ds_read_b64_tr_b16 a[168:169], v34 offset:32768\n" \
"v_and_or_b32 v212, v215, 1, v212\n" \
"v_mfma_f32_16x16x32_bf16 v[132:135], a[240:243], a[212:215], v[132:135]\n" \
"ds_read_b128 a[148:151], v28 offset:34816\n" \
"s_lshl_b32 s5, s4, 2\n" \
"v_mfma_f32_16x16x32_bf16 v[148:151], a[176:179], a[152:155], 0\n" \
"ds_read_b128 a[212:215], v29 offset:34816\n" \
"s_bfe_u32 s38, s2, 0x20003\n" \
"v_mfma_f32_16x16x32_bf16 v[148:151], a[240:243], a[216:219], v[148:151]\n" \
"ds_read_b64_tr_b16 a[170:171], v34 offset:33792\n" \
"v_lshlrev_b32_e64 v212, 5, v212\n" \
"v_mfma_f32_16x16x32_bf16 v[152:155], a[180:183], a[152:155], 0\n" \
"ds_read_b64_tr_b16 a[232:233], v34 offset:40960\n" \
"s_movk_i32 s35, 0x60\n" \
"v_mfma_f32_16x16x32_bf16 v[152:155], a[244:247], a[216:219], v[152:155]\n" \
"ds_read_b64_tr_b16 a[234:235], v34 offset:41984\n" \
"s_or_b32 s5, s5, s38\n" \
"v_mfma_f32_16x16x32_bf16 v[156:159], a[184:187], a[152:155], 0\n" \
"ds_read_b64_tr_b16 a[172:173], v35 offset:32768\n" \
"v_bitop3_b32 v212, v212, s35, v214 bitop3:0x36\n" \
"v_mfma_f32_16x16x32_bf16 v[156:159], a[248:251], a[216:219], v[156:159]\n" \
"ds_read_b64_tr_b16 a[174:175], v35 offset:33792\n" \
"s_lshr_b32 s36, s2, 3\n" \
"v_mfma_f32_16x16x32_bf16 v[160:163], a[188:191], a[152:155], 0\n" \
"ds_read_b64_tr_b16 a[236:237], v35 offset:40960\n" \
"v_add_u32_e32 v212, s34, v212\n" \
"v_mfma_f32_16x16x32_bf16 v[160:163], a[252:255], a[216:219], v[160:163]\n" \
"ds_read_b128 a[152:155], v28 offset:36864\n" \
"s_mul_i32 s34, s5, 0x12000\n" \
"v_mfma_f32_16x16x32_bf16 v[176:179], a[188:191], a[156:159], 0\n" \
"ds_read_b128 a[216:219], v29 offset:36864\n" \
"s_or_b32 s40, s34, 0x6000000\n" \
"v_mfma_f32_16x16x32_bf16 v[176:179], a[252:255], a[220:223], v[176:179]\n" \
"ds_read_b64_tr_b16 a[238:239], v35 offset:41984\n" \
"s_mul_i32 s34, s36, 0x90000\n" \
"v_mfma_f32_16x16x32_bf16 v[172:175], a[184:187], a[156:159], 0\n" \
"s_lshl_b32 s36, s3, 14\n" \
"s_add_i32 s34, s34, s36\n" \
"v_mfma_f32_16x16x32_bf16 v[172:175], a[248:251], a[220:223], v[172:175]\n" \
"s_lshl_b32 s38, s5, 8\n" \
"s_add_i32 s41, s34, 0x4240000\n" \
"v_mfma_f32_16x16x32_bf16 v[168:171], a[180:183], a[156:159], 0\n" \
"s_mul_i32 s34, s5, 36\n" \
"s_add_i32 s5, s7, s40\n" \
"v_mfma_f32_16x16x32_bf16 v[168:171], a[244:247], a[220:223], v[168:171]\n" \
"s_addk_i32 s5, 0x2000\n" \
"s_mov_b32 s37, 2.0\n" \
"v_mfma_f32_16x16x32_bf16 v[164:167], a[176:179], a[156:159], 0\n" \
"s_or_b32 s38, s38, 0x5be8000\n" \
"v_mfma_f32_16x16x32_bf16 v[164:167], a[240:243], a[220:223], v[164:167]\n" \
"ds_read_b128 a[156:159], v28 offset:38912\n" \
"ds_read_b128 a[220:223], v29 offset:38912\n" \
"s_waitcnt lgkmcnt(15)\n" \
"v_mfma_f32_16x16x32_bf16 a[0:3], a[160:163], a[128:131], a[0:3]\n" \
"s_addk_i32 s84, 0x7c00\n" \
"v_mfma_f32_16x16x32_bf16 a[0:3], a[224:227], a[192:195], a[0:3]\n" \
"buffer_load_dwordx4 v27, s[72:75], s84 offen offset:2048 lds\n" \
"v_mfma_f32_16x16x32_bf16 a[4:7], a[164:167], a[128:131], a[4:7]\n" \
"ds_read_b64_tr_b16 a[176:177], v36 offset:32768\n" \
"s_waitcnt lgkmcnt(15)\n" \
"v_mfma_f32_16x16x32_bf16 a[4:7], a[228:231], a[192:195], a[4:7]\n" \
"s_addk_i32 s84, 0x7c00\n" \
"s_waitcnt lgkmcnt(11)\n" \
"v_mfma_f32_16x16x32_bf16 a[8:11], a[168:171], a[128:131], a[8:11]\n" \
"buffer_load_dwordx4 v27, s[72:75], s84 offen offset:3072 lds\n" \
"s_waitcnt lgkmcnt(9)\n" \
"v_mfma_f32_16x16x32_bf16 a[8:11], a[232:235], a[192:195], a[8:11]\n" \
"ds_read_b64_tr_b16 a[178:179], v36 offset:33792\n" \
"s_waitcnt lgkmcnt(8)\n" \
"v_mfma_f32_16x16x32_bf16 a[12:15], a[172:175], a[128:131], a[12:15]\n" \
"ds_read_b64_tr_b16 a[240:241], v36 offset:40960\n" \
"s_waitcnt lgkmcnt(5)\n" \
"v_mfma_f32_16x16x32_bf16 a[12:15], a[236:239], a[192:195], a[12:15]\n" \
"s_cmp_lg_u32 s3, 0\n" \
"s_cselect_b32 s36, s37, s5\n" \
"v_mfma_f32_16x16x32_bf16 a[28:31], a[172:175], a[132:135], a[28:31]\n" \
"s_lshl_b32 s5, s6, 2\n" \
"s_or_b32 s5, s38, s5\n" \
"v_mfma_f32_16x16x32_bf16 a[28:31], a[236:239], a[196:199], a[28:31]\n" \
"ds_read_b64_tr_b16 a[242:243], v36 offset:41984\n" \
"s_lshl_b32 s35, s3, 10\n" \
"v_mfma_f32_16x16x32_bf16 a[24:27], a[168:171], a[132:135], a[24:27]\n" \
"s_cmpk_lt_u32 s3, 3\n" \
"s_cselect_b32 s37, s37, s5\n" \
"v_mfma_f32_16x16x32_bf16 a[24:27], a[232:235], a[196:199], a[24:27]\n" \
"s_lshl_b32 s5, s4, 16\n" \
"s_lshl_b32 s4, s4, 11\n" \
"v_mfma_f32_16x16x32_bf16 a[20:23], a[164:167], a[132:135], a[20:23]\n" \
"v_cndmask_b32_e32 v198, v213, v212, vcc\n" \
"v_or_b32_e32 v212, s35, v20\n" \
"v_mfma_f32_16x16x32_bf16 a[20:23], a[228:231], a[196:199], a[20:23]\n" \
"ds_read_b64_tr_b16 a[180:181], v37 offset:32768\n" \
"s_or_b32 s39, s40, s35\n" \
"v_mfma_f32_16x16x32_bf16 a[16:19], a[160:163], a[132:135], a[16:19]\n" \
"s_or_b32 s55, s41, s4\n" \
"v_or_b32_e32 v199, 0x22000, v212\n" \
"v_mfma_f32_16x16x32_bf16 a[16:19], a[224:227], a[196:199], a[16:19]\n" \
"s_add_i32 s35, s39, s7\n" \
"s_add_i32 s54, s41, s5\n" \
"v_mfma_f32_16x16x32_bf16 a[32:35], a[160:163], a[136:139], a[32:35]\n" \
"s_mov_b32 m0, s32\n" \
"ds_read_b64_tr_b16 a[182:183], v37 offset:33792\n" \
"v_mfma_f32_16x16x32_bf16 a[32:35], a[224:227], a[200:203], a[32:35]\n" \
"s_mov_b32 exec_lo, 0x0\n" \
"s_mov_b32 exec_hi, 0xff\n" \
"global_load_lds_dword v[184:185], off\n" \
"s_mov_b64 exec, -1\n" \
"v_mfma_f32_16x16x32_bf16 a[36:39], a[164:167], a[136:139], a[36:39]\n" \
"s_add_i32 s55, s55, 0x80000\n" \
"v_mov_b32_e32 v215, 0xffff\n" \
"v_mfma_f32_16x16x32_bf16 a[36:39], a[228:231], a[200:203], a[36:39]\n" \
"ds_read_b64_tr_b16 a[244:245], v37 offset:40960\n" \
"s_and_b32 s4, s3, 1\n" \
"v_mfma_f32_16x16x32_bf16 a[40:43], a[168:171], a[136:139], a[40:43]\n" \
"s_lshl_b32 s5, s4, 4\n" \
"v_lshrrev_b32_e32 v212, 4, v20\n" \
"v_mfma_f32_16x16x32_bf16 a[40:43], a[232:235], a[200:203], a[40:43]\n" \
"s_mov_b32 m0, s32\n" \
"v_lshrrev_b32_e32 v218, 6, v20\n" \
"v_mfma_f32_16x16x32_bf16 a[44:47], a[172:175], a[136:139], a[44:47]\n" \
"s_mov_b32 exec_lo, 0xffff0000\n" \
"s_mov_b32 exec_hi, 0x0\n" \
"global_load_lds_dword v[186:187], off\n" \
"s_mov_b64 exec, -1\n" \
"v_mfma_f32_16x16x32_bf16 a[44:47], a[236:239], a[200:203], a[44:47]\n" \
"ds_read_b64_tr_b16 a[246:247], v37 offset:41984\n" \
"v_and_b32_e64 v219, 12, v218\n" \
"v_mfma_f32_16x16x32_bf16 a[60:63], a[172:175], a[140:143], a[60:63]\n" \
"v_and_or_b32 v214, v212, 15, s5\n" \
"v_cmp_gt_u32_e64 vcc, v219, v214\n" \
"v_mfma_f32_16x16x32_bf16 a[60:63], a[236:239], a[204:207], a[60:63]\n" \
"v_mov_b32_e32 v220, 0xffff0000\n" \
"s_lshl_b32 s5, s2, 2\n" \
"v_mfma_f32_16x16x32_bf16 a[56:59], a[168:171], a[140:143], a[56:59]\n" \
"ds_read_b64_tr_b16 a[184:185], v38 offset:32768\n" \
"v_cndmask_b32_e64 v217, v215, 0, vcc\n" \
"v_mfma_f32_16x16x32_bf16 a[56:59], a[232:235], a[204:207], a[56:59]\n" \
"v_cmp_lt_u32_e32 vcc, v219, v214\n" \
"v_mov_b32_e64 v213, s23\n" \
"v_mfma_f32_16x16x32_bf16 a[52:55], a[164:167], a[140:143], a[52:55]\n" \
"s_or_b32 s5, s5, s3\n" \
"v_cndmask_b32_e32 v221, 0, v220, vcc\n" \
"v_mfma_f32_16x16x32_bf16 a[52:55], a[228:231], a[204:207], a[52:55]\n" \
"ds_read_b64_tr_b16 a[186:187], v38 offset:33792\n" \
"v_or_b32_e64 v12, v217, v221\n" \
"v_mfma_f32_16x16x32_bf16 a[48:51], a[160:163], a[140:143], a[48:51]\n" \
"v_or_b32_e32 v217, 2, v219\n" \
"v_cmp_gt_u32_e32 vcc, v217, v214\n" \
"v_mfma_f32_16x16x32_bf16 a[48:51], a[224:227], a[204:207], a[48:51]\n" \
"v_mov_b32_e32 v216, s25\n" \
"s_movk_i32 s40, 0xf400\n" \
"v_mfma_f32_16x16x32_bf16 a[64:67], a[160:163], a[144:147], a[64:67]\n" \
"ds_read_b64_tr_b16 a[248:249], v38 offset:40960\n" \
"v_cndmask_b32_e64 v221, v215, 0, vcc\n" \
"v_mfma_f32_16x16x32_bf16 a[64:67], a[224:227], a[208:211], a[64:67]\n" \
"v_cmp_lt_u32_e32 vcc, v217, v214\n" \
"s_lshl_b32 s4, s4, 6\n" \
"v_mfma_f32_16x16x32_bf16 a[68:71], a[164:167], a[144:147], a[68:71]\n" \
"v_cndmask_b32_e32 v217, 0, v220, vcc\n" \
"v_or_b32_e32 v13, v221, v217\n" \
"v_mfma_f32_16x16x32_bf16 a[68:71], a[228:231], a[208:211], a[68:71]\n" \
"ds_read_b64_tr_b16 a[250:251], v38 offset:41984\n" \
"v_or_b32_e64 v217, 16, v219\n" \
"v_mfma_f32_16x16x32_bf16 a[72:75], a[168:171], a[144:147], a[72:75]\n" \
"v_cmp_gt_u32_e32 vcc, v217, v214\n" \
"s_nop 1\n" \
"v_cndmask_b32_e64 v221, v215, 0, vcc\n" \
"v_mfma_f32_16x16x32_bf16 a[72:75], a[232:235], a[208:211], a[72:75]\n" \
"v_cmp_lt_u32_e32 vcc, v217, v214\n" \
"s_nop 1\n" \
"v_cndmask_b32_e64 v217, 0, v220, vcc\n" \
"v_mfma_f32_16x16x32_bf16 a[76:79], a[172:175], a[144:147], a[76:79]\n" \
"v_or_b32_e32 v14, v221, v217\n" \
"v_or_b32_e32 v217, 18, v219\n" \
"v_mfma_f32_16x16x32_bf16 a[76:79], a[236:239], a[208:211], a[76:79]\n" \
"ds_read_b64_tr_b16 a[188:189], v39 offset:32768\n" \
"v_cmp_gt_u32_e64 vcc, v217, v214\n" \
"v_mfma_f32_16x16x32_bf16 a[92:95], a[172:175], a[148:151], a[92:95]\n" \
"s_nop 0\n" \
"v_cndmask_b32_e64 v215, v215, 0, vcc\n" \
"v_cmp_lt_u32_e32 vcc, v217, v214\n" \
"v_mfma_f32_16x16x32_bf16 a[92:95], a[236:239], a[212:215], a[92:95]\n" \
"s_nop 0\n" \
"v_cndmask_b32_e32 v214, 0, v220, vcc\n" \
"v_or_b32_e64 v15, v215, v214\n" \
"v_mfma_f32_16x16x32_bf16 a[88:91], a[168:171], a[148:151], a[88:91]\n" \
"ds_read_b64_tr_b16 a[190:191], v39 offset:33792\n" \
"v_mad_u64_u32 v[214:215], s[6:7], s5, 42, v[212:213]\n" \
"v_mfma_f32_16x16x32_bf16 a[88:91], a[232:235], a[212:215], a[88:91]\n" \
"s_movk_i32 s5, 0x1440\n" \
"v_cmp_gt_u32_e32 vcc, s5, v214\n" \
"v_mfma_f32_16x16x32_bf16 a[84:87], a[164:167], a[148:151], a[84:87]\n" \
"v_min_u32_e32 v217, 0xa43f, v214\n" \
"v_mov_b32_e64 v214, s24\n" \
"v_mfma_f32_16x16x32_bf16 a[84:87], a[228:231], a[212:215], a[84:87]\n" \
"ds_read_b64_tr_b16 a[252:253], v39 offset:40960\n" \
"v_cndmask_b32_e64 v215, v216, v213, vcc\n" \
"v_mfma_f32_16x16x32_bf16 a[80:83], a[160:163], a[148:151], a[80:83]\n" \
"v_mov_b32_e32 v213, s22\n" \
"s_movk_i32 s6, 0xffee\n" \
"v_mfma_f32_16x16x32_bf16 a[80:83], a[224:227], a[212:215], a[80:83]\n" \
"v_cndmask_b32_e64 v214, v214, v213, vcc\n" \
"v_add_u32_e32 v213, 0xffffebc0, v217\n" \
"v_mfma_f32_16x16x32_bf16 a[96:99], a[160:163], a[152:155], a[96:99]\n" \
"ds_read_b64_tr_b16 a[254:255], v39 offset:41984\n" \
"s_lshl_b32 s5, s2, 5\n" \
"v_mfma_f32_16x16x32_bf16 a[96:99], a[224:227], a[216:219], a[96:99]\n" \
"v_cndmask_b32_e32 v213, v213, v217, vcc\n" \
"v_lshlrev_b32_e64 v216, 7, v213\n" \
"v_mfma_f32_16x16x32_bf16 a[100:103], a[164:167], a[152:155], a[100:103]\n" \
"v_mul_u32_u24_e32 v213, 0xe39, v212\n" \
"v_mov_b32_e64 v217, 0\n" \
"v_mfma_f32_16x16x32_bf16 a[100:103], a[228:231], a[216:219], a[100:103]\n" \
"v_lshrrev_b32_e64 v213, 16, v213\n" \
"v_lshl_add_u64 v[16:17], v[214:215], 0, v[216:217]\n" \
"v_mfma_f32_16x16x32_bf16 a[104:107], a[168:171], a[152:155], a[104:107]\n" \
"v_mad_i32_i24 v214, v213, s6, v212\n" \
"s_add_i32 s5, s5, 32\n" \
"v_mfma_f32_16x16x32_bf16 a[104:107], a[232:235], a[216:219], a[104:107]\n" \
"v_lshlrev_b32_e32 v215, 4, v214\n" \
"v_add_u32_e64 v216, s5, v215\n" \
"v_mfma_f32_16x16x32_bf16 a[108:111], a[172:175], a[152:155], a[108:111]\n" \
"v_and_b32_e32 v216, 0xf0, v216\n" \
"v_cmp_gt_u32_e64 vcc, 16, v214\n" \
"v_mfma_f32_16x16x32_bf16 a[108:111], a[236:239], a[216:219], a[108:111]\n" \
"s_movk_i32 s7, 0xf800\n" \
"v_cndmask_b32_e32 v214, v215, v216, vcc\n" \
"s_waitcnt lgkmcnt(15)\n" \
"v_mfma_f32_16x16x32_bf16 a[124:127], a[172:175], a[156:159], a[124:127]\n" \
"v_or_b32_e32 v215, 64, v212\n" \
"v_mul_u32_u24_e32 v216, 0xe39, v215\n" \
"v_mfma_f32_16x16x32_bf16 a[124:127], a[236:239], a[220:223], a[124:127]\n" \
"v_lshrrev_b32_e64 v216, 16, v216\n" \
"v_mad_i32_i24 v215, v216, s6, v215\n" \
"v_mfma_f32_16x16x32_bf16 a[120:123], a[168:171], a[156:159], a[120:123]\n" \
"v_lshlrev_b32_e32 v217, 4, v215\n" \
"v_add_u32_e32 v220, s5, v217\n" \
"v_mfma_f32_16x16x32_bf16 a[120:123], a[232:235], a[220:223], a[120:123]\n" \
"v_and_b32_e32 v220, 0xf0, v220\n" \
"v_cmp_gt_u32_e64 vcc, 16, v215\n" \
"v_mfma_f32_16x16x32_bf16 a[116:119], a[164:167], a[156:159], a[116:119]\n" \
"s_nop 0\n" \
"v_cndmask_b32_e32 v215, v217, v220, vcc\n" \
"v_or_b32_e32 v217, 0x80, v212\n" \
"v_mfma_f32_16x16x32_bf16 a[116:119], a[228:231], a[220:223], a[116:119]\n" \
"v_mul_u32_u24_e32 v220, 0xe39, v217\n" \
"v_lshrrev_b32_e64 v220, 16, v220\n" \
"v_mfma_f32_16x16x32_bf16 a[112:115], a[160:163], a[156:159], a[112:115]\n" \
"v_mad_i32_i24 v217, v220, s6, v217\n" \
"v_lshlrev_b32_e64 v221, 4, v217\n" \
"v_mfma_f32_16x16x32_bf16 a[112:115], a[224:227], a[220:223], a[112:115]\n" \
"s_waitcnt vmcnt(20)\n" \
"v_readfirstlane_b32 s62, v180\n" \
"s_nop 3\n" \
"s_add_u32 s62, s62, 0x9E3779B1\n" \
"s_waitcnt vmcnt(2) lgkmcnt(0)\n" \
"s_barrier\n" \
"v_mfma_f32_16x16x32_bf16 v[52:55], a[176:179], a[128:131], v[52:55]\n" \
"s_add_u32 m0, s30, 0x8000\n" \
"s_movk_i32 s84, 0x180\n" \
"v_mfma_f32_16x16x32_bf16 v[52:55], a[240:243], a[192:195], v[52:55]\n" \
"buffer_load_dwordx4 v22, s[64:67], s84 offen offset:0 lds\n" \
"v_mfma_f32_16x16x32_bf16 v[56:59], a[180:183], a[128:131], v[56:59]\n" \
"ds_read_b64_tr_b16 a[160:161], v32 offset:0\n" \
"v_add_u32_e64 v222, s5, v221\n" \
"v_mfma_f32_16x16x32_bf16 v[56:59], a[244:247], a[192:195], v[56:59]\n" \
"s_movk_i32 s84, 0x1d80\n" \
"v_mfma_f32_16x16x32_bf16 v[60:63], a[184:187], a[128:131], v[60:63]\n" \
"buffer_load_dwordx4 v23, s[64:67], s84 offen offset:1024 lds\n" \
"v_mfma_f32_16x16x32_bf16 v[60:63], a[248:251], a[192:195], v[60:63]\n" \
"ds_read_b64_tr_b16 a[162:163], v32 offset:1024\n" \
"v_and_b32_e32 v222, 0xf0, v222\n" \
"v_mfma_f32_16x16x32_bf16 v[64:67], a[188:191], a[128:131], v[64:67]\n" \
"ds_read_b64_tr_b16 a[224:225], v32 offset:8192\n" \
"v_cmp_gt_u32_e32 vcc, 16, v217\n" \
"v_mfma_f32_16x16x32_bf16 v[64:67], a[252:255], a[192:195], v[64:67]\n" \
"ds_read_b128 a[128:131], v28 offset:16384\n" \
"v_cndmask_b32_e64 v217, v221, v222, vcc\n" \
"v_mfma_f32_16x16x32_bf16 v[80:83], a[188:191], a[132:135], v[80:83]\n" \
"ds_read_b128 a[192:195], v29 offset:16384\n" \
"v_or_b32_e32 v221, 0xc0, v212\n" \
"v_mfma_f32_16x16x32_bf16 v[80:83], a[252:255], a[196:199], v[80:83]\n" \
"s_movk_i32 s84, 0x3980\n" \
"v_mfma_f32_16x16x32_bf16 v[76:79], a[184:187], a[132:135], v[76:79]\n" \
"buffer_load_dwordx4 v22, s[64:67], s84 offen offset:2048 lds\n" \
"v_mfma_f32_16x16x32_bf16 v[76:79], a[248:251], a[196:199], v[76:79]\n" \
"s_movk_i32 s84, 0x5580\n" \
"v_mfma_f32_16x16x32_bf16 v[72:75], a[180:183], a[132:135], v[72:75]\n" \
"buffer_load_dwordx4 v23, s[64:67], s84 offen offset:3072 lds\n" \
"v_mfma_f32_16x16x32_bf16 v[72:75], a[244:247], a[196:199], v[72:75]\n" \
"s_add_u32 m0, s31, 0x8000\n" \
"s_mov_b32 s84, 0x180000\n" \
"v_mfma_f32_16x16x32_bf16 v[68:71], a[176:179], a[132:135], v[68:71]\n" \
"buffer_load_dwordx4 v26, s[68:71], s84 offen offset:0 lds\n" \
"v_mfma_f32_16x16x32_bf16 v[68:71], a[240:243], a[196:199], v[68:71]\n" \
"ds_read_b128 a[132:135], v28 offset:18432\n" \
"v_mul_u32_u24_e32 v222, 0xe39, v221\n" \
"v_mfma_f32_16x16x32_bf16 v[84:87], a[176:179], a[136:139], v[84:87]\n" \
"ds_read_b128 a[196:199], v29 offset:18432\n" \
"v_lshrrev_b32_e64 v222, 16, v222\n" \
"v_mfma_f32_16x16x32_bf16 v[84:87], a[240:243], a[200:203], v[84:87]\n" \
"s_addk_i32 s84, 0x7c00\n" \
"v_mfma_f32_16x16x32_bf16 v[88:91], a[180:183], a[136:139], v[88:91]\n" \
"buffer_load_dwordx4 v26, s[68:71], s84 offen offset:1024 lds\n" \
"v_mfma_f32_16x16x32_bf16 v[88:91], a[244:247], a[200:203], v[88:91]\n" \
"ds_read_b64_tr_b16 a[226:227], v32 offset:9216\n" \
"v_mad_i32_i24 v221, v222, s6, v221\n" \
"v_mfma_f32_16x16x32_bf16 v[92:95], a[184:187], a[136:139], v[92:95]\n" \
"s_addk_i32 s84, 0x7c00\n" \
"v_mfma_f32_16x16x32_bf16 v[92:95], a[248:251], a[200:203], v[92:95]\n" \
"buffer_load_dwordx4 v27, s[68:71], s84 offen offset:2048 lds\n" \
"v_mfma_f32_16x16x32_bf16 v[96:99], a[188:191], a[136:139], v[96:99]\n" \
"ds_read_b64_tr_b16 a[164:165], v33 offset:0\n" \
"v_lshlrev_b32_e64 v223, 4, v221\n" \
"v_mfma_f32_16x16x32_bf16 v[96:99], a[252:255], a[200:203], v[96:99]\n" \
"ds_read_b128 a[136:139], v28 offset:20480\n" \
"v_add_u32_e64 v224, s5, v223\n" \
"v_mfma_f32_16x16x32_bf16 v[112:115], a[188:191], a[140:143], v[112:115]\n" \
"ds_read_b128 a[200:203], v29 offset:20480\n" \
"v_and_b32_e32 v224, 0xf0, v224\n" \
"v_mfma_f32_16x16x32_bf16 v[112:115], a[252:255], a[204:207], v[112:115]\n" \
"s_addk_i32 s84, 0x7c00\n" \
"v_mfma_f32_16x16x32_bf16 v[108:111], a[184:187], a[140:143], v[108:111]\n" \
"buffer_load_dwordx4 v27, s[68:71], s84 offen offset:3072 lds\n" \
"v_mfma_f32_16x16x32_bf16 v[108:111], a[248:251], a[204:207], v[108:111]\n" \
"s_add_u32 m0, s30, 0x9000\n" \
"s_mov_b32 s84, 0x8180\n" \
"v_mfma_f32_16x16x32_bf16 v[104:107], a[180:183], a[140:143], v[104:107]\n" \
"buffer_load_dwordx4 v22, s[64:67], s84 offen offset:0 lds\n" \
"v_mfma_f32_16x16x32_bf16 v[104:107], a[244:247], a[204:207], v[104:107]\n" \
"ds_read_b64_tr_b16 a[166:167], v33 offset:1024\n" \
"v_cmp_gt_u32_e32 vcc, 16, v221\n" \
"v_mfma_f32_16x16x32_bf16 v[100:103], a[176:179], a[140:143], v[100:103]\n" \
"ds_read_b64_tr_b16 a[228:229], v33 offset:8192\n" \
"v_cndmask_b32_e64 v221, v223, v224, vcc\n" \
"v_mfma_f32_16x16x32_bf16 v[100:103], a[240:243], a[204:207], v[100:103]\n" \
"ds_read_b128 a[140:143], v28 offset:22528\n" \
"v_or_b32_e32 v223, 0x100, v212\n" \
"v_mfma_f32_16x16x32_bf16 v[116:119], a[176:179], a[144:147], v[116:119]\n" \
"ds_read_b128 a[204:207], v29 offset:22528\n" \
"v_mul_u32_u24_e32 v224, 0xe39, v223\n" \
"v_mfma_f32_16x16x32_bf16 v[116:119], a[240:243], a[208:211], v[116:119]\n" \
"s_addk_i32 s84, 0x1c00\n" \
"v_mfma_f32_16x16x32_bf16 v[120:123], a[180:183], a[144:147], v[120:123]\n" \
"buffer_load_dwordx4 v23, s[64:67], s84 offen offset:1024 lds\n" \
"v_mfma_f32_16x16x32_bf16 v[120:123], a[244:247], a[208:211], v[120:123]\n" \
"s_addk_i32 s84, 0x1c00\n" \
"v_mfma_f32_16x16x32_bf16 v[124:127], a[184:187], a[144:147], v[124:127]\n" \
"buffer_load_dwordx4 v22, s[64:67], s84 offen offset:2048 lds\n" \
"v_mfma_f32_16x16x32_bf16 v[124:127], a[248:251], a[208:211], v[124:127]\n" \
"s_addk_i32 s84, 0x1c00\n" \
"v_mfma_f32_16x16x32_bf16 v[128:131], a[188:191], a[144:147], v[128:131]\n" \
"buffer_load_dwordx4 v23, s[64:67], s84 offen offset:3072 lds\n" \
"v_mfma_f32_16x16x32_bf16 v[128:131], a[252:255], a[208:211], v[128:131]\n" \
"ds_read_b128 a[144:147], v28 offset:0\n" \
"v_lshrrev_b32_e32 v224, 16, v224\n" \
"v_mfma_f32_16x16x32_bf16 v[144:147], a[188:191], a[148:151], v[144:147]\n" \
"ds_read_b128 a[208:211], v29 offset:0\n" \
"v_mad_i32_i24 v223, v224, s6, v223\n" \
"v_mfma_f32_16x16x32_bf16 v[144:147], a[252:255], a[212:215], v[144:147]\n" \
"s_add_u32 m0, s31, 0xc000\n" \
"s_mov_b32 s84, 0x180000\n" \
"v_mfma_f32_16x16x32_bf16 v[140:143], a[184:187], a[148:151], v[140:143]\n" \
"buffer_load_dwordx4 v26, s[72:75], s84 offen offset:0 lds\n" \
"v_mfma_f32_16x16x32_bf16 v[140:143], a[248:251], a[212:215], v[140:143]\n" \
"ds_read_b64_tr_b16 a[230:231], v33 offset:9216\n" \
"v_lshlrev_b32_e64 v225, 4, v223\n" \
"v_mfma_f32_16x16x32_bf16 v[136:139], a[180:183], a[148:151], v[136:139]\n" \
"s_addk_i32 s84, 0x7c00\n" \
"v_mfma_f32_16x16x32_bf16 v[136:139], a[244:247], a[212:215], v[136:139]\n" \
"buffer_load_dwordx4 v26, s[72:75], s84 offen offset:1024 lds\n" \
"v_mfma_f32_16x16x32_bf16 v[132:135], a[176:179], a[148:151], v[132:135]\n" \
"ds_read_b64_tr_b16 a[168:169], v34 offset:0\n" \
"v_add_u32_e32 v226, s5, v225\n" \
"v_mfma_f32_16x16x32_bf16 v[132:135], a[240:243], a[212:215], v[132:135]\n" \
"ds_read_b128 a[148:151], v28 offset:2048\n" \
"v_and_b32_e32 v226, 0xf0, v226\n" \
"v_mfma_f32_16x16x32_bf16 v[148:151], a[176:179], a[152:155], v[148:151]\n" \
"ds_read_b128 a[212:215], v29 offset:2048\n" \
"v_cmp_gt_u32_e64 vcc, 16, v223\n" \
"v_mfma_f32_16x16x32_bf16 v[148:151], a[240:243], a[216:219], v[148:151]\n" \
"ds_read_b64_tr_b16 a[170:171], v34 offset:1024\n" \
"v_cndmask_b32_e64 v223, v225, v226, vcc\n" \
"v_mfma_f32_16x16x32_bf16 v[152:155], a[180:183], a[152:155], v[152:155]\n" \
"ds_read_b64_tr_b16 a[232:233], v34 offset:8192\n" \
"v_or_b32_e32 v225, 0x140, v212\n" \
"v_mfma_f32_16x16x32_bf16 v[152:155], a[244:247], a[216:219], v[152:155]\n" \
"ds_read_b64_tr_b16 a[234:235], v34 offset:9216\n" \
"v_mul_u32_u24_e32 v226, 0xe39, v225\n" \
"v_mfma_f32_16x16x32_bf16 v[156:159], a[184:187], a[152:155], v[156:159]\n" \
"ds_read_b64_tr_b16 a[172:173], v35 offset:0\n" \
"v_lshrrev_b32_e64 v226, 16, v226\n" \
"v_mfma_f32_16x16x32_bf16 v[156:159], a[248:251], a[216:219], v[156:159]\n" \
"ds_read_b64_tr_b16 a[174:175], v35 offset:1024\n" \
"v_mad_i32_i24 v225, v226, s6, v225\n" \
"v_mfma_f32_16x16x32_bf16 v[160:163], a[188:191], a[152:155], v[160:163]\n" \
"ds_read_b64_tr_b16 a[236:237], v35 offset:8192\n" \
"v_lshlrev_b32_e64 v227, 4, v225\n" \
"v_mfma_f32_16x16x32_bf16 v[160:163], a[252:255], a[216:219], v[160:163]\n" \
"ds_read_b128 a[152:155], v28 offset:4096\n" \
"v_add_u32_e64 v228, s5, v227\n" \
"v_mfma_f32_16x16x32_bf16 v[176:179], a[188:191], a[156:159], v[176:179]\n" \
"ds_read_b128 a[216:219], v29 offset:4096\n" \
"v_and_b32_e32 v228, 0xf0, v228\n" \
"v_mfma_f32_16x16x32_bf16 v[176:179], a[252:255], a[220:223], v[176:179]\n" \
"ds_read_b64_tr_b16 a[238:239], v35 offset:9216\n" \
"v_cmp_gt_u32_e64 vcc, 16, v225\n" \
"v_mfma_f32_16x16x32_bf16 v[172:175], a[184:187], a[156:159], v[172:175]\n" \
"s_nop 0\n" \
"v_cndmask_b32_e32 v225, v227, v228, vcc\n" \
"v_or_b32_e32 v227, 0x180, v212\n" \
"v_mfma_f32_16x16x32_bf16 v[172:175], a[248:251], a[220:223], v[172:175]\n" \
"v_mul_u32_u24_e32 v228, 0xe39, v227\n" \
"v_lshrrev_b32_e64 v228, 16, v228\n" \
"v_mfma_f32_16x16x32_bf16 v[168:171], a[180:183], a[156:159], v[168:171]\n" \
"v_mad_i32_i24 v227, v228, s6, v227\n" \
"v_lshlrev_b32_e64 v229, 4, v227\n" \
"v_mfma_f32_16x16x32_bf16 v[168:171], a[244:247], a[220:223], v[168:171]\n" \
"v_add_u32_e64 v230, s5, v229\n" \
"v_and_b32_e32 v230, 0xf0, v230\n" \
"v_mfma_f32_16x16x32_bf16 v[164:167], a[176:179], a[156:159], v[164:167]\n" \
"v_cmp_gt_u32_e32 vcc, 16, v227\n" \
"s_nop 1\n" \
"v_cndmask_b32_e64 v227, v229, v230, vcc\n" \
"v_mfma_f32_16x16x32_bf16 v[164:167], a[240:243], a[220:223], v[164:167]\n" \
"ds_read_b128 a[156:159], v28 offset:6144\n" \
"ds_read_b128 a[220:223], v29 offset:6144\n" \
"s_waitcnt lgkmcnt(15)\n" \
"v_mfma_f32_16x16x32_bf16 a[0:3], a[160:163], a[128:131], a[0:3]\n" \
"s_addk_i32 s84, 0x7c00\n" \
"v_mfma_f32_16x16x32_bf16 a[0:3], a[224:227], a[192:195], a[0:3]\n" \
"buffer_load_dwordx4 v27, s[72:75], s84 offen offset:2048 lds\n" \
"v_mfma_f32_16x16x32_bf16 a[4:7], a[164:167], a[128:131], a[4:7]\n" \
"ds_read_b64_tr_b16 a[176:177], v36 offset:0\n" \
"v_or_b32_e32 v229, 0x1c0, v212\n" \
"s_waitcnt lgkmcnt(15)\n" \
"v_mfma_f32_16x16x32_bf16 a[4:7], a[228:231], a[192:195], a[4:7]\n" \
"s_addk_i32 s84, 0x7c00\n" \
"s_waitcnt lgkmcnt(11)\n" \
"v_mfma_f32_16x16x32_bf16 a[8:11], a[168:171], a[128:131], a[8:11]\n" \
"buffer_load_dwordx4 v27, s[72:75], s84 offen offset:3072 lds\n" \
"s_waitcnt lgkmcnt(9)\n" \
"v_mfma_f32_16x16x32_bf16 a[8:11], a[232:235], a[192:195], a[8:11]\n" \
"ds_read_b64_tr_b16 a[178:179], v36 offset:1024\n" \
"v_mul_u32_u24_e32 v230, 0xe39, v229\n" \
"s_waitcnt lgkmcnt(8)\n" \
"v_mfma_f32_16x16x32_bf16 a[12:15], a[172:175], a[128:131], a[12:15]\n" \
"ds_read_b64_tr_b16 a[240:241], v36 offset:8192\n" \
"v_lshrrev_b32_e32 v230, 16, v230\n" \
"s_waitcnt lgkmcnt(5)\n" \
"v_mfma_f32_16x16x32_bf16 a[12:15], a[236:239], a[192:195], a[12:15]\n" \
"v_mad_i32_i24 v229, v230, s6, v229\n" \
"v_lshlrev_b32_e32 v231, 4, v229\n" \
"v_mfma_f32_16x16x32_bf16 a[28:31], a[172:175], a[132:135], a[28:31]\n" \
"v_add_u32_e64 v232, s5, v231\n" \
"v_and_b32_e32 v232, 0xf0, v232\n" \
"v_mfma_f32_16x16x32_bf16 a[28:31], a[236:239], a[196:199], a[28:31]\n" \
"ds_read_b64_tr_b16 a[242:243], v36 offset:9216\n" \
"v_cmp_gt_u32_e64 vcc, 16, v229\n" \
"v_mfma_f32_16x16x32_bf16 a[24:27], a[168:171], a[132:135], a[24:27]\n" \
"buffer_load_dwordx4 v[200:203], v192, s[76:79], s34 offen offset:0\n" \
"v_mfma_f32_16x16x32_bf16 a[24:27], a[232:235], a[196:199], a[24:27]\n" \
"v_cndmask_b32_e64 v229, v231, v232, vcc\n" \
"v_or_b32_e32 v231, 0x200, v212\n" \
"v_mfma_f32_16x16x32_bf16 a[20:23], a[164:167], a[132:135], a[20:23]\n" \
"v_mul_u32_u24_e32 v232, 0xe39, v231\n" \
"v_lshrrev_b32_e64 v232, 16, v232\n" \
"v_mfma_f32_16x16x32_bf16 a[20:23], a[228:231], a[196:199], a[20:23]\n" \
"ds_read_b64_tr_b16 a[180:181], v37 offset:0\n" \
"v_mad_i32_i24 v231, v232, s6, v231\n" \
"v_mfma_f32_16x16x32_bf16 a[16:19], a[160:163], a[132:135], a[16:19]\n" \
"v_lshlrev_b32_e32 v233, 4, v231\n" \
"v_add_u32_e32 v234, s5, v233\n" \
"v_mfma_f32_16x16x32_bf16 a[16:19], a[224:227], a[196:199], a[16:19]\n" \
"s_movk_i32 s5, 0x480\n" \
"v_mad_u32_u24 v0, v213, s5, v214\n" \
"v_mfma_f32_16x16x32_bf16 a[32:35], a[160:163], a[136:139], a[32:35]\n" \
"buffer_load_dwordx4 v[204:207], v193, s[76:79], s34 offen offset:0\n" \
"v_mfma_f32_16x16x32_bf16 a[32:35], a[224:227], a[200:203], a[32:35]\n" \
"s_mov_b32 m0, s32\n" \
"ds_read_b64_tr_b16 a[182:183], v37 offset:1024\n" \
"v_mfma_f32_16x16x32_bf16 a[36:39], a[164:167], a[136:139], a[36:39]\n" \
"s_mov_b32 exec_lo, 0x0\n" \
"s_mov_b32 exec_hi, 0xff00\n" \
"global_load_lds_dword v[184:185], off\n" \
"s_mov_b64 exec, -1\n" \
"v_mfma_f32_16x16x32_bf16 a[36:39], a[228:231], a[200:203], a[36:39]\n" \
"ds_read_b64_tr_b16 a[244:245], v37 offset:8192\n" \
"v_mul_u32_u24_e32 v213, 0x480, v216\n" \
"v_mfma_f32_16x16x32_bf16 a[40:43], a[168:171], a[136:139], a[40:43]\n" \
"s_movk_i32 s6, 0xfc00\n" \
"s_nop 0\n" \
"v_add3_u32 v1, v213, v215, s6\n" \
"v_mfma_f32_16x16x32_bf16 a[40:43], a[232:235], a[200:203], a[40:43]\n" \
"buffer_load_dword v208, v194, s[76:79], s34 offen offset:32\n" \
"v_mfma_f32_16x16x32_bf16 a[44:47], a[172:175], a[136:139], a[44:47]\n" \
"ds_read_b64_tr_b16 a[246:247], v37 offset:9216\n" \
"v_mul_u32_u24_e32 v213, 0x480, v220\n" \
"v_mfma_f32_16x16x32_bf16 a[44:47], a[236:239], a[200:203], a[44:47]\n" \
"v_add3_u32 v2, v213, v217, s7\n" \
"v_mul_u32_u24_e32 v213, 0x480, v222\n" \
"v_mfma_f32_16x16x32_bf16 a[60:63], a[172:175], a[140:143], a[60:63]\n" \
"v_add3_u32 v3, v213, v221, s40\n" \
"v_mul_u32_u24_e32 v213, 0x480, v226\n" \
"v_mfma_f32_16x16x32_bf16 a[60:63], a[236:239], a[204:207], a[60:63]\n" \
"v_add3_u32 v5, v213, v225, s6\n" \
"v_mul_u32_u24_e32 v213, 0x480, v228\n" \
"v_mfma_f32_16x16x32_bf16 a[56:59], a[168:171], a[140:143], a[56:59]\n" \
"buffer_load_dword v209, v194, s[76:79], s34 offen offset:1184\n" \
"v_mfma_f32_16x16x32_bf16 a[56:59], a[232:235], a[204:207], a[56:59]\n" \
"ds_read_b64_tr_b16 a[184:185], v38 offset:0\n" \
"v_add3_u32 v6, v213, v227, s7\n" \
"v_mfma_f32_16x16x32_bf16 a[52:55], a[164:167], a[140:143], a[52:55]\n" \
"v_mul_u32_u24_e32 v213, 0x480, v230\n" \
"v_add3_u32 v7, v213, v229, s40\n" \
"v_mfma_f32_16x16x32_bf16 a[52:55], a[228:231], a[204:207], a[52:55]\n" \
"ds_read_b64_tr_b16 a[186:187], v38 offset:1024\n" \
"s_lshl_b32 s7, s3, 3\n" \
"v_mfma_f32_16x16x32_bf16 a[48:51], a[160:163], a[140:143], a[48:51]\n" \
"s_lshl_b32 s40, s3, 1\n" \
"s_and_b32 s7, s7, 16\n" \
"v_mfma_f32_16x16x32_bf16 a[48:51], a[224:227], a[204:207], a[48:51]\n" \
"s_and_b32 s40, s40, 2\n" \
"s_and_b32 s6, s2, 7\n" \
"v_mfma_f32_16x16x32_bf16 a[64:67], a[160:163], a[144:147], a[64:67]\n" \
"buffer_load_dword v211, v194, s[76:79], s34 offen offset:2336\n" \
"v_mfma_f32_16x16x32_bf16 a[64:67], a[224:227], a[208:211], a[64:67]\n" \
"ds_read_b64_tr_b16 a[248:249], v38 offset:8192\n" \
"v_bfe_u32 v213, v20, 7, 1\n" \
"v_mfma_f32_16x16x32_bf16 a[68:71], a[164:167], a[144:147], a[68:71]\n" \
"s_or_b32 s7, s40, s7\n" \
"v_and_b32_e32 v234, 0xf0, v234\n" \
"v_mfma_f32_16x16x32_bf16 a[68:71], a[228:231], a[208:211], a[68:71]\n" \
"ds_read_b64_tr_b16 a[250:251], v38 offset:9216\n" \
"v_cmp_gt_u32_e64 vcc, 16, v231\n" \
"v_mfma_f32_16x16x32_bf16 a[72:75], a[168:171], a[144:147], a[72:75]\n" \
"v_or3_b32 v213, s7, v213, v219\n" \
"v_and_b32_e64 v214, 32, v20\n" \
"v_mfma_f32_16x16x32_bf16 a[72:75], a[232:235], a[208:211], a[72:75]\n" \
"s_mov_b32 s76, s20\n" \
"s_and_b32 s77, s21, 0xffff\n" \
"v_mfma_f32_16x16x32_bf16 a[76:79], a[172:175], a[144:147], a[76:79]\n" \
"s_mov_b32 s78, 0x480\n" \
"s_lshl_b32 s6, s6, 5\n" \
"v_mfma_f32_16x16x32_bf16 a[76:79], a[236:239], a[208:211], a[76:79]\n" \
"buffer_load_ushort v210, v195, s[76:79], 0 offen\n" \
"v_mfma_f32_16x16x32_bf16 a[92:95], a[172:175], a[148:151], a[92:95]\n" \
"ds_read_b64_tr_b16 a[188:189], v39 offset:0\n" \
"v_cndmask_b32_e64 v231, v233, v234, vcc\n" \
"v_mfma_f32_16x16x32_bf16 a[92:95], a[236:239], a[212:215], a[92:95]\n" \
"v_mul_u32_u24_e64 v213, 18, v213\n" \
"v_mov_b32_e32 v215, 0x100\n" \
"v_mfma_f32_16x16x32_bf16 a[88:91], a[168:171], a[148:151], a[88:91]\n" \
"s_mov_b32 s76, s8\n" \
"s_and_b32 s77, s9, 0xffff\n" \
"v_mfma_f32_16x16x32_bf16 a[88:91], a[232:235], a[212:215], a[88:91]\n" \
"s_mov_b32 s78, 0x240000\n" \
"ds_read_b64_tr_b16 a[190:191], v39 offset:1024\n" \
".fill (L_pb + 7904 - .) / 4, 4, 0xBF800000\n" \
"L_pp2:\n" \
"s_cbranch_execz L_pret\n" \
"v_mfma_f32_16x16x32_bf16 a[84:87], a[164:167], a[148:151], a[84:87]\n" \
"v_mov_b32_e32 v216, s6\n" \
"v_cmp_eq_u32_e64 vcc, 0, v214\n" \
"v_mfma_f32_16x16x32_bf16 a[84:87], a[228:231], a[212:215], a[84:87]\n" \
"ds_read_b64_tr_b16 a[252:253], v39 offset:8192\n" \
"v_and_or_b32 v213, v218, 1, v213\n" \
"v_mfma_f32_16x16x32_bf16 a[80:83], a[160:163], a[148:151], a[80:83]\n" \
"v_lshlrev_b32_e32 v212, 3, v212\n" \
"v_cndmask_b32_e32 v214, v215, v216, vcc\n" \
"v_mfma_f32_16x16x32_bf16 a[80:83], a[224:227], a[212:215], a[80:83]\n" \
"v_mad_u32_u24 v213, v213, s5, v214\n" \
"v_and_or_b32 v213, v20, 16, v213\n" \
"v_mfma_f32_16x16x32_bf16 a[96:99], a[160:163], a[152:155], a[96:99]\n" \
"ds_read_b64_tr_b16 a[254:255], v39 offset:9216\n" \
"v_add_u32_e32 v9, 0x4800, v213\n" \
"v_mfma_f32_16x16x32_bf16 a[96:99], a[224:227], a[216:219], a[96:99]\n" \
"v_mul_u32_u24_e32 v213, 0x120, v218\n" \
"v_and_or_b32 v10, v212, 24, v213\n" \
"v_mfma_f32_16x16x32_bf16 a[100:103], a[164:167], a[152:155], a[100:103]\n" \
"v_lshrrev_b32_e64 v213, 2, v20\n" \
"v_and_or_b32 v213, v213, 60, s4\n" \
"v_mfma_f32_16x16x32_bf16 a[100:103], a[228:231], a[216:219], a[100:103]\n" \
"v_lshl_or_b32 v212, s3, 9, v212\n" \
"v_mad_u32_u24 v4, v224, s5, v223\n" \
"v_mfma_f32_16x16x32_bf16 a[104:107], a[168:171], a[152:155], a[104:107]\n" \
"v_mad_u32_u24 v8, v232, s5, v231\n" \
"v_or_b32_e32 v18, 0x27000, v213\n" \
"v_mfma_f32_16x16x32_bf16 a[104:107], a[232:235], a[216:219], a[104:107]\n" \
"v_or_b32_e32 v19, 0x27800, v212\n" \
"v_mov_b32_e32 v215, 0x1000c0c\n" \
"v_mfma_f32_16x16x32_bf16 a[108:111], a[172:175], a[152:155], a[108:111]\n" \
"v_mov_b32_e32 v216, 0xc0c0100\n" \
"v_bfe_u32 v214, v20, 4, 1\n" \
"v_mfma_f32_16x16x32_bf16 a[108:111], a[236:239], a[216:219], a[108:111]\n" \
"v_lshrrev_b32_e32 v212, 8, v20\n" \
"v_cmp_eq_u32_e32 vcc, 0, v214\n" \
"s_waitcnt lgkmcnt(15)\n" \
"v_mfma_f32_16x16x32_bf16 a[124:127], a[172:175], a[156:159], a[124:127]\n" \
"v_bfe_u32 v213, v20, 4, 4\n" \
"v_mov_b32_e32 v217, 0xc0c0302\n" \
"v_mfma_f32_16x16x32_bf16 a[124:127], a[236:239], a[220:223], a[124:127]\n" \
"v_cndmask_b32_e32 v215, v215, v216, vcc\n" \
"v_mov_b32_e32 v216, 0x3020c0c\n" \
"v_mfma_f32_16x16x32_bf16 a[120:123], a[168:171], a[156:159], a[120:123]\n" \
"v_lshl_or_b32 v214, v212, 2, v214\n" \
"v_cndmask_b32_e64 v216, v216, v217, vcc\n" \
"v_mfma_f32_16x16x32_bf16 a[120:123], a[232:235], a[220:223], a[120:123]\n" \
"v_mov_b32_e32 v217, 0xc0c0c0c\n" \
"v_cmp_gt_u32_e64 vcc, v214, v213\n" \
"v_mfma_f32_16x16x32_bf16 a[116:119], a[164:167], a[156:159], a[116:119]\n" \
"v_or_b32_e32 v214, 2, v214\n" \
"s_lshr_b32 s4, s2, 5\n" \
"v_mfma_f32_16x16x32_bf16 a[116:119], a[228:231], a[220:223], a[116:119]\n" \
"v_cndmask_b32_e32 v40, v217, v215, vcc\n" \
"v_cndmask_b32_e32 v42, v215, v217, vcc\n" \
"v_mfma_f32_16x16x32_bf16 a[112:115], a[160:163], a[156:159], a[112:115]\n" \
"v_cmp_gt_u32_e32 vcc, v214, v213\n" \
"v_and_b32_e32 v214, 16, v20\n" \
"s_waitcnt vmcnt(7) lgkmcnt(0)\n" \
"v_mfma_f32_16x16x32_bf16 a[112:115], a[224:227], a[220:223], a[112:115]\n" \
"s_barrier\n" \
"v_mfma_f32_16x16x32_bf16 v[52:55], a[176:179], a[128:131], v[52:55]\n" \
"s_add_u32 m0, s30, 0x0\n" \
"s_movk_i32 s84, 0x200\n" \
"v_mfma_f32_16x16x32_bf16 v[52:55], a[240:243], a[192:195], v[52:55]\n" \
"buffer_load_dwordx4 v22, s[64:67], s84 offen offset:0 lds\n" \
"v_mfma_f32_16x16x32_bf16 v[56:59], a[180:183], a[128:131], v[56:59]\n" \
"ds_read_b64_tr_b16 a[160:161], v32 offset:32768\n" \
"s_movk_i32 s42, 0x3f80\n" \
"v_mfma_f32_16x16x32_bf16 v[56:59], a[244:247], a[192:195], v[56:59]\n" \
"s_movk_i32 s84, 0x1e00\n" \
"v_mfma_f32_16x16x32_bf16 v[60:63], a[184:187], a[128:131], v[60:63]\n" \
"buffer_load_dwordx4 v23, s[64:67], s84 offen offset:1024 lds\n" \
"v_mfma_f32_16x16x32_bf16 v[60:63], a[248:251], a[192:195], v[60:63]\n" \
"ds_read_b64_tr_b16 a[162:163], v32 offset:33792\n" \
"s_xor_b32 s5, s4, 15\n" \
"v_mfma_f32_16x16x32_bf16 v[64:67], a[188:191], a[128:131], v[64:67]\n" \
"ds_read_b64_tr_b16 a[224:225], v32 offset:40960\n" \
"v_mfma_f32_16x16x32_bf16 v[64:67], a[252:255], a[192:195], v[64:67]\n" \
"ds_read_b128 a[128:131], v28 offset:49152\n" \
"v_mfma_f32_16x16x32_bf16 v[80:83], a[188:191], a[132:135], v[80:83]\n" \
"ds_read_b128 a[192:195], v29 offset:49152\n" \
"v_mfma_f32_16x16x32_bf16 v[80:83], a[252:255], a[196:199], v[80:83]\n" \
"s_movk_i32 s84, 0x3a00\n" \
"v_mfma_f32_16x16x32_bf16 v[76:79], a[184:187], a[132:135], v[76:79]\n" \
"buffer_load_dwordx4 v22, s[64:67], s84 offen offset:2048 lds\n" \
"v_mfma_f32_16x16x32_bf16 v[76:79], a[248:251], a[196:199], v[76:79]\n" \
"s_movk_i32 s84, 0x5600\n" \
"v_mfma_f32_16x16x32_bf16 v[72:75], a[180:183], a[132:135], v[72:75]\n" \
"buffer_load_dwordx4 v23, s[64:67], s84 offen offset:3072 lds\n" \
"v_mfma_f32_16x16x32_bf16 v[72:75], a[244:247], a[196:199], v[72:75]\n" \
"s_add_u32 m0, s31, 0x0\n" \
"s_mov_b32 s84, 0x200000\n" \
"v_mfma_f32_16x16x32_bf16 v[68:71], a[176:179], a[132:135], v[68:71]\n" \
"buffer_load_dwordx4 v26, s[68:71], s84 offen offset:0 lds\n" \
"v_mfma_f32_16x16x32_bf16 v[68:71], a[240:243], a[196:199], v[68:71]\n" \
"ds_read_b128 a[132:135], v28 offset:51200\n" \
"v_mfma_f32_16x16x32_bf16 v[84:87], a[176:179], a[136:139], v[84:87]\n" \
"ds_read_b128 a[196:199], v29 offset:51200\n" \
"v_mfma_f32_16x16x32_bf16 v[84:87], a[240:243], a[200:203], v[84:87]\n" \
"s_addk_i32 s84, 0x7c00\n" \
"v_mfma_f32_16x16x32_bf16 v[88:91], a[180:183], a[136:139], v[88:91]\n" \
"buffer_load_dwordx4 v26, s[68:71], s84 offen offset:1024 lds\n" \
"v_mfma_f32_16x16x32_bf16 v[88:91], a[244:247], a[200:203], v[88:91]\n" \
"ds_read_b64_tr_b16 a[226:227], v32 offset:41984\n" \
"v_mfma_f32_16x16x32_bf16 v[92:95], a[184:187], a[136:139], v[92:95]\n" \
"s_addk_i32 s84, 0x7c00\n" \
"v_mfma_f32_16x16x32_bf16 v[92:95], a[248:251], a[200:203], v[92:95]\n" \
"buffer_load_dwordx4 v27, s[68:71], s84 offen offset:2048 lds\n" \
"v_mfma_f32_16x16x32_bf16 v[96:99], a[188:191], a[136:139], v[96:99]\n" \
"ds_read_b64_tr_b16 a[164:165], v33 offset:32768\n" \
"v_mfma_f32_16x16x32_bf16 v[96:99], a[252:255], a[200:203], v[96:99]\n" \
"ds_read_b128 a[136:139], v28 offset:53248\n" \
"v_mfma_f32_16x16x32_bf16 v[112:115], a[188:191], a[140:143], v[112:115]\n" \
"ds_read_b128 a[200:203], v29 offset:53248\n" \
"v_mfma_f32_16x16x32_bf16 v[112:115], a[252:255], a[204:207], v[112:115]\n" \
"s_addk_i32 s84, 0x7c00\n" \
"s_nop 0\n" \
"v_mfma_f32_16x16x32_bf16 v[108:111], a[184:187], a[140:143], v[108:111]\n" \
"buffer_load_dwordx4 v27, s[68:71], s84 offen offset:3072 lds\n" \
"v_mfma_f32_16x16x32_bf16 v[108:111], a[248:251], a[204:207], v[108:111]\n" \
"s_add_u32 m0, s30, 0x1000\n" \
"s_mov_b32 s84, 0x8200\n" \
"v_mfma_f32_16x16x32_bf16 v[104:107], a[180:183], a[140:143], v[104:107]\n" \
"buffer_load_dwordx4 v22, s[64:67], s84 offen offset:0 lds\n" \
"v_mfma_f32_16x16x32_bf16 v[104:107], a[244:247], a[204:207], v[104:107]\n" \
"ds_read_b64_tr_b16 a[166:167], v33 offset:33792\n" \
"v_mfma_f32_16x16x32_bf16 v[100:103], a[176:179], a[140:143], v[100:103]\n" \
"ds_read_b64_tr_b16 a[228:229], v33 offset:40960\n" \
"v_mfma_f32_16x16x32_bf16 v[100:103], a[240:243], a[204:207], v[100:103]\n" \
"ds_read_b128 a[140:143], v28 offset:55296\n" \
"v_mfma_f32_16x16x32_bf16 v[116:119], a[176:179], a[144:147], v[116:119]\n" \
"ds_read_b128 a[204:207], v29 offset:55296\n" \
"v_mfma_f32_16x16x32_bf16 v[116:119], a[240:243], a[208:211], v[116:119]\n" \
"s_addk_i32 s84, 0x1c00\n" \
"v_mfma_f32_16x16x32_bf16 v[120:123], a[180:183], a[144:147], v[120:123]\n" \
"buffer_load_dwordx4 v23, s[64:67], s84 offen offset:1024 lds\n" \
"v_mfma_f32_16x16x32_bf16 v[120:123], a[244:247], a[208:211], v[120:123]\n" \
"s_addk_i32 s84, 0x1c00\n" \
"v_mfma_f32_16x16x32_bf16 v[124:127], a[184:187], a[144:147], v[124:127]\n" \
"buffer_load_dwordx4 v22, s[64:67], s84 offen offset:2048 lds\n" \
"v_mfma_f32_16x16x32_bf16 v[124:127], a[248:251], a[208:211], v[124:127]\n" \
"s_addk_i32 s84, 0x1c00\n" \
"s_nop 0\n" \
"v_mfma_f32_16x16x32_bf16 v[128:131], a[188:191], a[144:147], v[128:131]\n" \
"buffer_load_dwordx4 v23, s[64:67], s84 offen offset:3072 lds\n" \
"v_mfma_f32_16x16x32_bf16 v[128:131], a[252:255], a[208:211], v[128:131]\n" \
"ds_read_b128 a[144:147], v28 offset:32768\n" \
"v_mfma_f32_16x16x32_bf16 v[144:147], a[188:191], a[148:151], v[144:147]\n" \
"ds_read_b128 a[208:211], v29 offset:32768\n" \
"v_mfma_f32_16x16x32_bf16 v[144:147], a[252:255], a[212:215], v[144:147]\n" \
"s_add_u32 m0, s31, 0x4000\n" \
"s_mov_b32 s84, 0x200000\n" \
"v_mfma_f32_16x16x32_bf16 v[140:143], a[184:187], a[148:151], v[140:143]\n" \
"buffer_load_dwordx4 v26, s[72:75], s84 offen offset:0 lds\n" \
"v_mfma_f32_16x16x32_bf16 v[140:143], a[248:251], a[212:215], v[140:143]\n" \
"ds_read_b64_tr_b16 a[230:231], v33 offset:41984\n" \
"v_mfma_f32_16x16x32_bf16 v[136:139], a[180:183], a[148:151], v[136:139]\n" \
"s_addk_i32 s84, 0x7c00\n" \
"s_nop 0\n" \
"v_mfma_f32_16x16x32_bf16 v[136:139], a[244:247], a[212:215], v[136:139]\n" \
"buffer_load_dwordx4 v26, s[72:75], s84 offen offset:1024 lds\n" \
"v_mfma_f32_16x16x32_bf16 v[132:135], a[176:179], a[148:151], v[132:135]\n" \
"ds_read_b64_tr_b16 a[168:169], v34 offset:32768\n" \
"v_mfma_f32_16x16x32_bf16 v[132:135], a[240:243], a[212:215], v[132:135]\n" \
"ds_read_b128 a[148:151], v28 offset:34816\n" \
"v_mfma_f32_16x16x32_bf16 v[148:151], a[176:179], a[152:155], v[148:151]\n" \
"ds_read_b128 a[212:215], v29 offset:34816\n" \
"v_mfma_f32_16x16x32_bf16 v[148:151], a[240:243], a[216:219], v[148:151]\n" \
"ds_read_b64_tr_b16 a[170:171], v34 offset:33792\n" \
"v_mfma_f32_16x16x32_bf16 v[152:155], a[180:183], a[152:155], v[152:155]\n" \
"ds_read_b64_tr_b16 a[232:233], v34 offset:40960\n" \
"v_mfma_f32_16x16x32_bf16 v[152:155], a[244:247], a[216:219], v[152:155]\n" \
"ds_read_b64_tr_b16 a[234:235], v34 offset:41984\n" \
"v_mfma_f32_16x16x32_bf16 v[156:159], a[184:187], a[152:155], v[156:159]\n" \
"ds_read_b64_tr_b16 a[172:173], v35 offset:32768\n" \
"v_mfma_f32_16x16x32_bf16 v[156:159], a[248:251], a[216:219], v[156:159]\n" \
"ds_read_b64_tr_b16 a[174:175], v35 offset:33792\n" \
"v_mfma_f32_16x16x32_bf16 v[160:163], a[188:191], a[152:155], v[160:163]\n" \
"ds_read_b64_tr_b16 a[236:237], v35 offset:40960\n" \
"v_mfma_f32_16x16x32_bf16 v[160:163], a[252:255], a[216:219], v[160:163]\n" \
"ds_read_b128 a[152:155], v28 offset:36864\n" \
"v_mfma_f32_16x16x32_bf16 v[176:179], a[188:191], a[156:159], v[176:179]\n" \
"ds_read_b128 a[216:219], v29 offset:36864\n" \
"v_mfma_f32_16x16x32_bf16 v[176:179], a[252:255], a[220:223], v[176:179]\n" \
"ds_read_b64_tr_b16 a[238:239], v35 offset:41984\n" \
"v_mfma_f32_16x16x32_bf16 v[172:175], a[184:187], a[156:159], v[172:175]\n" \
"s_cmpk_lt_u32 s3, 2\n" \
"s_cselect_b32 s6, s5, s4\n" \
"v_mfma_f32_16x16x32_bf16 v[172:175], a[248:251], a[220:223], v[172:175]\n" \
"s_lshl_b32 s7, s6, 7\n" \
"v_and_b32_e32 v213, 0x320, v20\n" \
"v_mfma_f32_16x16x32_bf16 v[168:171], a[180:183], a[156:159], v[168:171]\n" \
"s_movk_i32 s41, 0x100\n" \
"v_lshlrev_b32_e64 v214, v214, s42\n" \
"v_mfma_f32_16x16x32_bf16 v[168:171], a[244:247], a[220:223], v[168:171]\n" \
"v_mov_b32_e32 v215, 0\n" \
"s_add_i32 s40, s7, -16\n" \
"v_mfma_f32_16x16x32_bf16 v[164:167], a[176:179], a[156:159], v[164:167]\n" \
"v_cndmask_b32_e32 v41, v217, v216, vcc\n" \
"v_cndmask_b32_e32 v43, v216, v217, vcc\n" \
"v_mfma_f32_16x16x32_bf16 v[164:167], a[240:243], a[220:223], v[164:167]\n" \
"ds_read_b128 a[156:159], v28 offset:38912\n" \
"ds_read_b128 a[220:223], v29 offset:38912\n" \
"s_waitcnt lgkmcnt(15)\n" \
"v_mfma_f32_16x16x32_bf16 a[0:3], a[160:163], a[128:131], a[0:3]\n" \
"s_addk_i32 s84, 0x7c00\n" \
"v_mfma_f32_16x16x32_bf16 a[0:3], a[224:227], a[192:195], a[0:3]\n" \
"buffer_load_dwordx4 v27, s[72:75], s84 offen offset:2048 lds\n" \
"v_mfma_f32_16x16x32_bf16 a[4:7], a[164:167], a[128:131], a[4:7]\n" \
"ds_read_b64_tr_b16 a[176:177], v36 offset:32768\n" \
"v_cmp_eq_u32_e32 vcc, s41, v213\n" \
"s_waitcnt lgkmcnt(15)\n" \
"v_mfma_f32_16x16x32_bf16 a[4:7], a[228:231], a[192:195], a[4:7]\n" \
"s_addk_i32 s84, 0x7c00\n" \
"s_waitcnt lgkmcnt(11)\n" \
"v_mfma_f32_16x16x32_bf16 a[8:11], a[168:171], a[128:131], a[8:11]\n" \
"buffer_load_dwordx4 v27, s[72:75], s84 offen offset:3072 lds\n" \
"s_waitcnt lgkmcnt(9)\n" \
"v_mfma_f32_16x16x32_bf16 a[8:11], a[232:235], a[192:195], a[8:11]\n" \
"ds_read_b64_tr_b16 a[178:179], v36 offset:33792\n" \
"s_mov_b32 s41, 0\n" \
"s_waitcnt lgkmcnt(8)\n" \
"v_mfma_f32_16x16x32_bf16 a[12:15], a[172:175], a[128:131], a[12:15]\n" \
"ds_read_b64_tr_b16 a[240:241], v36 offset:40960\n" \
"s_and_b32 s43, s3, 1\n" \
"s_waitcnt lgkmcnt(5)\n" \
"v_mfma_f32_16x16x32_bf16 a[12:15], a[236:239], a[192:195], a[12:15]\n" \
"v_cndmask_b32_e32 v46, v215, v214, vcc\n" \
"v_and_b32_e32 v213, 0x330, v20\n" \
"v_mfma_f32_16x16x32_bf16 a[28:31], a[172:175], a[132:135], a[28:31]\n" \
"s_cmp_lg_u32 s7, 0\n" \
"s_cselect_b32 s40, s40, s41\n" \
"v_mfma_f32_16x16x32_bf16 a[28:31], a[236:239], a[196:199], a[28:31]\n" \
"ds_read_b64_tr_b16 a[242:243], v36 offset:41984\n" \
"s_mov_b32 s42, 0xff800000\n" \
"v_mfma_f32_16x16x32_bf16 a[24:27], a[168:171], a[132:135], a[24:27]\n" \
"s_cmp_lg_u32 s7, 0\n" \
"s_cselect_b32 s47, s41, s42\n" \
"v_mfma_f32_16x16x32_bf16 a[24:27], a[232:235], a[196:199], a[24:27]\n" \
"s_lshl_b32 s41, s43, 6\n" \
"v_lshrrev_b32_e32 v215, 6, v20\n" \
"v_mfma_f32_16x16x32_bf16 a[20:23], a[164:167], a[132:135], a[20:23]\n" \
"v_mov_b32_e32 v214, 0x3f80\n" \
"v_cmp_eq_u32_e64 vcc, 32, v213\n" \
"v_mfma_f32_16x16x32_bf16 a[20:23], a[228:231], a[196:199], a[20:23]\n" \
"ds_read_b64_tr_b16 a[180:181], v37 offset:32768\n" \
"s_or_b32 s7, s41, s7\n" \
"v_mfma_f32_16x16x32_bf16 a[16:19], a[160:163], a[132:135], a[16:19]\n" \
"v_lshrrev_b32_e32 v213, 7, v20\n" \
"v_bfe_u32 v216, v20, 6, 2\n" \
"v_mfma_f32_16x16x32_bf16 a[16:19], a[224:227], a[196:199], a[16:19]\n" \
"v_lshrrev_b32_e64 v219, 1, v20\n" \
"v_bfe_u32 v215, v215, 1, 1\n" \
"v_mfma_f32_16x16x32_bf16 a[32:35], a[160:163], a[136:139], a[32:35]\n" \
"s_waitcnt vmcnt(16)\n" \
"v_or_b32_e32 v204, v210, v204\n" \
"v_or_b32_e64 v206, v210, v206\n" \
"v_mfma_f32_16x16x32_bf16 a[32:35], a[224:227], a[200:203], a[32:35]\n" \
"v_mov_b32_e64 v210, 0\n" \
"s_add_u32 s88, s35, 0x1000\n" \
"v_mfma_f32_16x16x32_bf16 a[36:39], a[164:167], a[136:139], a[36:39]\n" \
"ds_read_b64_tr_b16 a[182:183], v37 offset:33792\n" \
"v_cndmask_b32_e64 v47, 0, v214, vcc\n" \
"v_mfma_f32_16x16x32_bf16 a[36:39], a[228:231], a[200:203], a[36:39]\n" \
"ds_read_b64_tr_b16 a[244:245], v37 offset:40960\n" \
"v_or_b32_e64 v214, s7, v213\n" \
"v_mfma_f32_16x16x32_bf16 a[40:43], a[168:171], a[136:139], a[40:43]\n" \
"s_mov_b32 m0, s32\n" \
"v_lshl_or_b32 v217, s43, 3, v213\n" \
"v_mfma_f32_16x16x32_bf16 a[40:43], a[232:235], a[200:203], a[40:43]\n" \
"s_mov_b32 exec_lo, 0xffffffff\n" \
"s_mov_b32 exec_hi, 0x0\n" \
"global_load_lds_dword v[188:189], off\n" \
"s_mov_b64 exec, -1\n" \
"v_mfma_f32_16x16x32_bf16 a[44:47], a[172:175], a[136:139], a[44:47]\n" \
"ds_read_b64_tr_b16 a[246:247], v37 offset:41984\n" \
"v_lshlrev_b32_e64 v218, 10, v212\n" \
"v_mfma_f32_16x16x32_bf16 a[44:47], a[236:239], a[200:203], a[44:47]\n" \
"v_lshlrev_b32_e32 v216, 7, v216\n" \
"v_and_b32_e32 v219, 24, v219\n" \
"v_mfma_f32_16x16x32_bf16 a[60:63], a[172:175], a[140:143], a[60:63]\n" \
"ds_write_b128 v196, v[200:203]\n" \
"v_mfma_f32_16x16x32_bf16 a[60:63], a[236:239], a[204:207], a[60:63]\n" \
"v_and_or_b32 v213, v213, 2, v215\n" \
"v_add_u32_e64 v217, s40, v217\n" \
"v_mfma_f32_16x16x32_bf16 a[56:59], a[168:171], a[140:143], a[56:59]\n" \
"ds_read_b64_tr_b16 a[184:185], v38 offset:32768\n" \
"v_or3_b32 v216, v216, v218, v219\n" \
"v_mfma_f32_16x16x32_bf16 a[56:59], a[232:235], a[204:207], a[56:59]\n" \
"v_lshlrev_b32_e64 v213, 5, v213\n" \
"s_mov_b32 s40, 0x14000\n" \
"v_mfma_f32_16x16x32_bf16 a[52:55], a[164:167], a[140:143], a[52:55]\n" \
"s_lshl_b32 s7, s2, 2\n" \
"v_or3_b32 v48, v216, v213, s40\n" \
"v_mfma_f32_16x16x32_bf16 a[52:55], a[228:231], a[204:207], a[52:55]\n" \
"ds_read_b64_tr_b16 a[186:187], v38 offset:33792\n" \
"s_lshl_b32 s40, s43, 2\n" \
"v_mfma_f32_16x16x32_bf16 a[48:51], a[160:163], a[140:143], a[48:51]\n" \
"s_lshl_b32 s6, s6, 3\n" \
"v_or_b32_e32 v213, s40, v212\n" \
"v_mfma_f32_16x16x32_bf16 a[48:51], a[224:227], a[204:207], a[48:51]\n" \
"s_or_b32 s6, s6, s40\n" \
"s_and_b32 s40, s7, 0x7c\n" \
"v_mfma_f32_16x16x32_bf16 a[64:67], a[160:163], a[144:147], a[64:67]\n" \
"ds_write_b128 v197, v[204:207]\n" \
"v_mfma_f32_16x16x32_bf16 a[64:67], a[224:227], a[208:211], a[64:67]\n" \
"ds_read_b64_tr_b16 a[248:249], v38 offset:40960\n" \
"s_or_b32 s40, s40, s3\n" \
"v_mfma_f32_16x16x32_bf16 a[68:71], a[164:167], a[144:147], a[68:71]\n" \
"s_cmpk_lt_u32 s40, 64\n" \
"s_cselect_b32 s41, s4, s5\n" \
"v_mfma_f32_16x16x32_bf16 a[68:71], a[228:231], a[208:211], a[68:71]\n" \
"ds_read_b64_tr_b16 a[250:251], v38 offset:41984\n" \
"s_lshl_b32 s40, s40, 1\n" \
"v_mfma_f32_16x16x32_bf16 a[72:75], a[168:171], a[144:147], a[72:75]\n" \
"s_lshl_b32 s41, s41, 7\n" \
"s_and_b32 s40, s40, 0x7e\n" \
"v_mfma_f32_16x16x32_bf16 a[72:75], a[232:235], a[208:211], a[72:75]\n" \
"s_or_b32 s40, s41, s40\n" \
"s_mulk_i32 s40, 0x480\n" \
"v_mfma_f32_16x16x32_bf16 a[76:79], a[172:175], a[144:147], a[76:79]\n" \
"v_add_u32_e64 v49, s40, v20\n" \
"s_mov_b32 s40, 0xc000\n" \
"v_mfma_f32_16x16x32_bf16 a[76:79], a[236:239], a[208:211], a[76:79]\n" \
"ds_read_b64_tr_b16 a[188:189], v39 offset:32768\n" \
"s_and_b32 s7, s7, 28\n" \
"v_mfma_f32_16x16x32_bf16 a[92:95], a[172:175], a[148:151], a[92:95]\n" \
"ds_write_b128 v198, v[208:211]\n" \
"v_mfma_f32_16x16x32_bf16 a[92:95], a[236:239], a[212:215], a[92:95]\n" \
"s_bfe_u32 s45, s2, 0x20003\n" \
"s_mov_b32 s41, 0x8000\n" \
"v_mfma_f32_16x16x32_bf16 a[88:91], a[168:171], a[148:151], a[88:91]\n" \
"ds_read_b64_tr_b16 a[190:191], v39 offset:33792\n" \
"v_mfma_f32_16x16x32_bf16 a[88:91], a[232:235], a[212:215], a[88:91]\n" \
"s_cmp_lg_u32 s4, 0\n" \
"s_cselect_b32 s51, s40, s41\n" \
"v_mfma_f32_16x16x32_bf16 a[84:87], a[164:167], a[148:151], a[84:87]\n" \
"s_movk_i32 s40, 0x4000\n" \
"s_lshr_b32 s44, s3, 1\n" \
"v_mfma_f32_16x16x32_bf16 a[84:87], a[228:231], a[212:215], a[84:87]\n" \
"ds_read_b64_tr_b16 a[252:253], v39 offset:40960\n" \
"s_or_b32 s46, s7, s45\n" \
"v_mfma_f32_16x16x32_bf16 a[80:83], a[160:163], a[148:151], a[80:83]\n" \
"s_cmp_lg_u32 s4, 0\n" \
"s_cselect_b32 s53, s41, s40\n" \
"v_mfma_f32_16x16x32_bf16 a[80:83], a[224:227], a[212:215], a[80:83]\n" \
"s_lshl_b32 s40, s44, 15\n" \
"s_lshl_b32 s41, s43, 14\n" \
"v_mfma_f32_16x16x32_bf16 a[96:99], a[160:163], a[152:155], a[96:99]\n" \
"ds_read_b64_tr_b16 a[254:255], v39 offset:41984\n" \
"s_lshl_b32 s48, s46, 20\n" \
"v_mfma_f32_16x16x32_bf16 a[96:99], a[224:227], a[216:219], a[96:99]\n" \
"s_or_b32 s41, s40, s41\n" \
"s_add_i32 s49, s48, 0x1000000\n" \
"v_mfma_f32_16x16x32_bf16 a[100:103], a[164:167], a[152:155], a[100:103]\n" \
"s_or_b32 s42, s49, s41\n" \
"s_lshl_b32 s4, s4, 16\n" \
"v_mfma_f32_16x16x32_bf16 a[100:103], a[228:231], a[216:219], a[100:103]\n" \
"s_or_b32 s41, s42, s4\n" \
"s_lshl_b32 s4, s5, 16\n" \
"v_mfma_f32_16x16x32_bf16 a[104:107], a[168:171], a[152:155], a[104:107]\n" \
"s_mul_i32 s5, s6, 0x24800\n" \
"s_lshl_b32 s6, s46, 9\n" \
"v_mfma_f32_16x16x32_bf16 a[104:107], a[232:235], a[216:219], a[104:107]\n" \
"s_lshl_b32 s7, s7, 6\n" \
"s_add_i32 s6, s5, s6\n" \
"v_mfma_f32_16x16x32_bf16 a[108:111], a[172:175], a[152:155], a[108:111]\n" \
"s_or_b32 s5, s5, s7\n" \
"s_lshl_b32 s7, s45, 2\n" \
"v_mfma_f32_16x16x32_bf16 a[108:111], a[236:239], a[216:219], a[108:111]\n" \
"s_or_b32 s5, s5, s7\n" \
"s_lshl_b32 s7, s3, 12\n" \
"s_waitcnt lgkmcnt(15)\n" \
"v_mfma_f32_16x16x32_bf16 a[124:127], a[172:175], a[156:159], a[124:127]\n" \
"s_or_b32 s50, s49, s7\n" \
"s_lshl_b32 s7, s3, 11\n" \
"v_mfma_f32_16x16x32_bf16 a[124:127], a[236:239], a[220:223], a[124:127]\n" \
"v_lshrrev_b32_e64 v215, 4, v20\n" \
"v_bfe_u32 v216, v20, 4, 3\n" \
"v_mfma_f32_16x16x32_bf16 a[120:123], a[168:171], a[156:159], a[120:123]\n" \
"s_or_b32 s42, s42, s4\n" \
"s_lshl_b32 s4, s43, 11\n" \
"v_mfma_f32_16x16x32_bf16 a[120:123], a[232:235], a[220:223], a[120:123]\n" \
"s_add_i32 s52, s48, s7\n" \
"s_add_i32 s53, s53, s7\n" \
"v_mfma_f32_16x16x32_bf16 a[116:119], a[164:167], a[156:159], a[116:119]\n" \
"v_mul_u32_u24_e32 v214, 0x480, v214\n" \
"v_bitop3_b32 v218, v212, v215, 7 bitop3:0x78\n" \
"v_mfma_f32_16x16x32_bf16 a[116:119], a[228:231], a[220:223], a[116:119]\n" \
"v_bitop3_b32 v212, v212, v216, 4 bitop3:0x36\n" \
"s_lshl_b32 s7, s3, 10\n" \
"v_mfma_f32_16x16x32_bf16 a[112:115], a[160:163], a[156:159], a[112:115]\n" \
"s_or_b32 s4, s40, s4\n" \
"v_lshl_or_b32 v25, v212, 4, v214\n" \
"s_waitcnt vmcnt(1) lgkmcnt(0)\n" \
"v_mfma_f32_16x16x32_bf16 a[112:115], a[224:227], a[220:223], a[112:115]\n" \
"s_barrier\n" \
"v_mfma_f32_16x16x32_bf16 v[52:55], a[176:179], a[128:131], v[52:55]\n" \
"s_add_u32 m0, s30, 0x8000\n" \
"s_movk_i32 s84, 0x280\n" \
"v_mfma_f32_16x16x32_bf16 v[52:55], a[240:243], a[192:195], v[52:55]\n" \
"buffer_load_dwordx4 v22, s[64:67], s84 offen offset:0 lds\n" \
"v_mfma_f32_16x16x32_bf16 v[56:59], a[180:183], a[128:131], v[56:59]\n" \
"s_movk_i32 s84, 0x1e80\n" \
"v_mfma_f32_16x16x32_bf16 v[56:59], a[244:247], a[192:195], v[56:59]\n" \
"buffer_load_dwordx4 v23, s[64:67], s84 offen offset:1024 lds\n" \
"v_mfma_f32_16x16x32_bf16 v[60:63], a[184:187], a[128:131], v[60:63]\n" \
"ds_read_b64_tr_b16 a[160:161], v32 offset:0\n" \
"v_mul_u32_u24_e32 v212, 0x480, v217\n" \
"v_mfma_f32_16x16x32_bf16 v[60:63], a[248:251], a[192:195], v[60:63]\n" \
"s_movk_i32 s84, 0x3a80\n" \
"v_mfma_f32_16x16x32_bf16 v[64:67], a[188:191], a[128:131], v[64:67]\n" \
"buffer_load_dwordx4 v22, s[64:67], s84 offen offset:2048 lds\n" \
"v_mfma_f32_16x16x32_bf16 v[64:67], a[252:255], a[192:195], v[64:67]\n" \
"ds_read_b128 a[128:131], v28 offset:16384\n" \
"v_bitop3_b32 v213, v215, v213, 7 bitop3:0x6c\n" \
"v_mfma_f32_16x16x32_bf16 v[80:83], a[188:191], a[132:135], v[80:83]\n" \
"ds_read_b128 a[192:195], v29 offset:16384\n" \
"s_or_b32 s40, s7, 0x14000\n" \
"v_mfma_f32_16x16x32_bf16 v[80:83], a[252:255], a[196:199], v[80:83]\n" \
"s_movk_i32 s84, 0x5680\n" \
"v_mfma_f32_16x16x32_bf16 v[76:79], a[184:187], a[132:135], v[76:79]\n" \
"buffer_load_dwordx4 v23, s[64:67], s84 offen offset:3072 lds\n" \
"v_mfma_f32_16x16x32_bf16 v[76:79], a[248:251], a[196:199], v[76:79]\n" \
"s_add_u32 m0, s31, 0x8000\n" \
"s_mov_b32 s84, 0x280000\n" \
"v_mfma_f32_16x16x32_bf16 v[72:75], a[180:183], a[132:135], v[72:75]\n" \
"buffer_load_dwordx4 v26, s[68:71], s84 offen offset:0 lds\n" \
"v_mfma_f32_16x16x32_bf16 v[72:75], a[244:247], a[196:199], v[72:75]\n" \
"ds_read_b64_tr_b16 a[162:163], v32 offset:1024\n" \
"s_lshl_b32 s43, s44, 13\n" \
"v_mfma_f32_16x16x32_bf16 v[68:71], a[176:179], a[132:135], v[68:71]\n" \
"ds_read_b64_tr_b16 a[224:225], v32 offset:8192\n" \
"s_mulk_i32 s44, 0xe800\n" \
"v_mfma_f32_16x16x32_bf16 v[68:71], a[240:243], a[196:199], v[68:71]\n" \
"ds_read_b128 a[132:135], v28 offset:18432\n" \
"s_lshl_b32 s7, s3, 8\n" \
"v_mfma_f32_16x16x32_bf16 v[84:87], a[176:179], a[136:139], v[84:87]\n" \
"ds_read_b128 a[196:199], v29 offset:18432\n" \
"v_lshl_or_b32 v24, v218, 4, v214\n" \
"v_mfma_f32_16x16x32_bf16 v[84:87], a[240:243], a[200:203], v[84:87]\n" \
"s_addk_i32 s84, 0x7c00\n" \
"v_mfma_f32_16x16x32_bf16 v[88:91], a[180:183], a[136:139], v[88:91]\n" \
"buffer_load_dwordx4 v26, s[68:71], s84 offen offset:1024 lds\n" \
"v_mfma_f32_16x16x32_bf16 v[88:91], a[244:247], a[200:203], v[88:91]\n" \
"s_addk_i32 s84, 0x7c00\n" \
"v_mfma_f32_16x16x32_bf16 v[92:95], a[184:187], a[136:139], v[92:95]\n" \
"buffer_load_dwordx4 v27, s[68:71], s84 offen offset:2048 lds\n" \
"v_mfma_f32_16x16x32_bf16 v[92:95], a[248:251], a[200:203], v[92:95]\n" \
"s_addk_i32 s84, 0x7c00\n" \
"v_mfma_f32_16x16x32_bf16 v[96:99], a[188:191], a[136:139], v[96:99]\n" \
"buffer_load_dwordx4 v27, s[68:71], s84 offen offset:3072 lds\n" \
"v_mfma_f32_16x16x32_bf16 v[96:99], a[252:255], a[200:203], v[96:99]\n" \
"ds_read_b128 a[136:139], v28 offset:20480\n" \
"v_lshl_or_b32 v21, v213, 4, v212\n" \
"v_mfma_f32_16x16x32_bf16 v[112:115], a[188:191], a[140:143], v[112:115]\n" \
"ds_read_b128 a[200:203], v29 offset:20480\n" \
"s_add_i32 s44, s44, 0x17000\n" \
"v_mfma_f32_16x16x32_bf16 v[112:115], a[252:255], a[204:207], v[112:115]\n" \
"s_add_u32 m0, s30, 0x9000\n" \
"s_mov_b32 s84, 0x8280\n" \
"v_mfma_f32_16x16x32_bf16 v[108:111], a[184:187], a[140:143], v[108:111]\n" \
"buffer_load_dwordx4 v22, s[64:67], s84 offen offset:0 lds\n" \
"v_mfma_f32_16x16x32_bf16 v[108:111], a[248:251], a[204:207], v[108:111]\n" \
"s_addk_i32 s84, 0x1c00\n" \
"v_mfma_f32_16x16x32_bf16 v[104:107], a[180:183], a[140:143], v[104:107]\n" \
"buffer_load_dwordx4 v23, s[64:67], s84 offen offset:1024 lds\n" \
"v_mfma_f32_16x16x32_bf16 v[104:107], a[244:247], a[204:207], v[104:107]\n" \
"s_addk_i32 s84, 0x1c00\n" \
"v_mfma_f32_16x16x32_bf16 v[100:103], a[176:179], a[140:143], v[100:103]\n" \
"buffer_load_dwordx4 v22, s[64:67], s84 offen offset:2048 lds\n" \
"v_mfma_f32_16x16x32_bf16 v[100:103], a[240:243], a[204:207], v[100:103]\n" \
"ds_read_b128 a[140:143], v28 offset:22528\n" \
"s_or_b32 s45, s7, 0x27000\n" \
"v_mfma_f32_16x16x32_bf16 v[116:119], a[176:179], a[144:147], v[116:119]\n" \
"ds_read_b128 a[204:207], v29 offset:22528\n" \
"s_or_b32 s46, s4, 0x10000\n" \
"v_mfma_f32_16x16x32_bf16 v[116:119], a[240:243], a[208:211], v[116:119]\n" \
"s_addk_i32 s84, 0x1c00\n" \
"v_mfma_f32_16x16x32_bf16 v[120:123], a[180:183], a[144:147], v[120:123]\n" \
"buffer_load_dwordx4 v23, s[64:67], s84 offen offset:3072 lds\n" \
"v_mfma_f32_16x16x32_bf16 v[120:123], a[244:247], a[208:211], v[120:123]\n" \
"s_add_u32 m0, s31, 0xc000\n" \
"s_mov_b32 s84, 0x280000\n" \
"v_mfma_f32_16x16x32_bf16 v[124:127], a[184:187], a[144:147], v[124:127]\n" \
"buffer_load_dwordx4 v26, s[72:75], s84 offen offset:0 lds\n" \
"v_mfma_f32_16x16x32_bf16 v[124:127], a[248:251], a[208:211], v[124:127]\n" \
"s_addk_i32 s84, 0x7c00\n" \
"v_mfma_f32_16x16x32_bf16 v[128:131], a[188:191], a[144:147], v[128:131]\n" \
"buffer_load_dwordx4 v26, s[72:75], s84 offen offset:1024 lds\n" \
"v_mfma_f32_16x16x32_bf16 v[128:131], a[252:255], a[208:211], v[128:131]\n" \
"ds_read_b128 a[144:147], v28 offset:0\n" \
"s_add_i32 s48, s6, 0x3000000\n" \
"v_mfma_f32_16x16x32_bf16 v[144:147], a[188:191], a[148:151], v[144:147]\n" \
"ds_read_b128 a[208:211], v29 offset:0\n" \
"s_add_i32 s49, s5, 0x3004000\n" \
"v_mfma_f32_16x16x32_bf16 v[144:147], a[252:255], a[212:215], v[144:147]\n" \
"ds_read_b128 v[200:203], v199 offset:0\n" \
"s_add_i32 s52, s52, 0x1010000\n" \
"v_mfma_f32_16x16x32_bf16 v[140:143], a[184:187], a[148:151], v[140:143]\n" \
"ds_read_b128 v[204:207], v199 offset:4096\n" \
"v_mfma_f32_16x16x32_bf16 v[140:143], a[248:251], a[212:215], v[140:143]\n" \
"ds_read_b64_tr_b16 a[226:227], v32 offset:9216\n" \
"v_mfma_f32_16x16x32_bf16 v[136:139], a[180:183], a[148:151], v[136:139]\n" \
"ds_read_b64_tr_b16 a[164:165], v33 offset:0\n" \
"v_mfma_f32_16x16x32_bf16 v[136:139], a[244:247], a[212:215], v[136:139]\n" \
"ds_read_b128 v[208:211], v199 offset:8192\n" \
"v_mfma_f32_16x16x32_bf16 v[132:135], a[176:179], a[148:151], v[132:135]\n" \
"ds_read_b64_tr_b16 a[166:167], v33 offset:1024\n" \
"v_mfma_f32_16x16x32_bf16 v[132:135], a[240:243], a[212:215], v[132:135]\n" \
"ds_read_b128 a[148:151], v28 offset:2048\n" \
"v_mfma_f32_16x16x32_bf16 v[148:151], a[176:179], a[152:155], v[148:151]\n" \
"ds_read_b128 a[212:215], v29 offset:2048\n" \
"v_mfma_f32_16x16x32_bf16 v[148:151], a[240:243], a[216:219], v[148:151]\n" \
"ds_read_b64_tr_b16 a[228:229], v33 offset:8192\n" \
"v_mfma_f32_16x16x32_bf16 v[152:155], a[180:183], a[152:155], v[152:155]\n" \
"ds_read_b64_tr_b16 a[230:231], v33 offset:9216\n" \
"v_mfma_f32_16x16x32_bf16 v[152:155], a[244:247], a[216:219], v[152:155]\n" \
"ds_read_b64_tr_b16 a[168:169], v34 offset:0\n" \
"v_mfma_f32_16x16x32_bf16 v[156:159], a[184:187], a[152:155], v[156:159]\n" \
"ds_read_b64_tr_b16 a[170:171], v34 offset:1024\n" \
"v_mfma_f32_16x16x32_bf16 v[156:159], a[248:251], a[216:219], v[156:159]\n" \
"ds_read_b64_tr_b16 a[232:233], v34 offset:8192\n" \
"v_mfma_f32_16x16x32_bf16 v[160:163], a[188:191], a[152:155], v[160:163]\n" \
"ds_read_b64_tr_b16 a[234:235], v34 offset:9216\n" \
"v_mfma_f32_16x16x32_bf16 v[160:163], a[252:255], a[216:219], v[160:163]\n" \
"ds_read_b128 a[152:155], v28 offset:4096\n" \
"v_mfma_f32_16x16x32_bf16 v[176:179], a[188:191], a[156:159], v[176:179]\n" \
"ds_read_b128 a[216:219], v29 offset:4096\n" \
"v_mfma_f32_16x16x32_bf16 v[176:179], a[252:255], a[220:223], v[176:179]\n" \
"ds_read_b64_tr_b16 a[172:173], v35 offset:0\n" \
"v_mfma_f32_16x16x32_bf16 v[172:175], a[184:187], a[156:159], v[172:175]\n" \
"ds_read_b64_tr_b16 a[174:175], v35 offset:1024\n" \
"v_mfma_f32_16x16x32_bf16 v[172:175], a[248:251], a[220:223], v[172:175]\n" \
"ds_read_b64_tr_b16 a[236:237], v35 offset:8192\n" \
"v_mfma_f32_16x16x32_bf16 v[168:171], a[180:183], a[156:159], v[168:171]\n" \
"ds_read_b64_tr_b16 a[238:239], v35 offset:9216\n" \
"v_mfma_f32_16x16x32_bf16 v[168:171], a[244:247], a[220:223], v[168:171]\n" \
"v_mfma_f32_16x16x32_bf16 v[164:167], a[176:179], a[156:159], v[164:167]\n" \
"v_mfma_f32_16x16x32_bf16 v[164:167], a[240:243], a[220:223], v[164:167]\n" \
"ds_read_b128 a[156:159], v28 offset:6144\n" \
"ds_read_b128 a[220:223], v29 offset:6144\n" \
"s_waitcnt lgkmcnt(15)\n" \
"v_mfma_f32_16x16x32_bf16 a[0:3], a[160:163], a[128:131], a[0:3]\n" \
"s_addk_i32 s84, 0x7c00\n" \
"v_mfma_f32_16x16x32_bf16 a[0:3], a[224:227], a[192:195], a[0:3]\n" \
"buffer_load_dwordx4 v27, s[72:75], s84 offen offset:2048 lds\n" \
"v_mfma_f32_16x16x32_bf16 a[4:7], a[164:167], a[128:131], a[4:7]\n" \
"ds_read_b64_tr_b16 a[176:177], v36 offset:0\n" \
"s_waitcnt lgkmcnt(13)\n" \
"v_mfma_f32_16x16x32_bf16 a[4:7], a[228:231], a[192:195], a[4:7]\n" \
"s_addk_i32 s84, 0x7c00\n" \
"s_waitcnt lgkmcnt(11)\n" \
"v_mfma_f32_16x16x32_bf16 a[8:11], a[168:171], a[128:131], a[8:11]\n" \
"buffer_load_dwordx4 v27, s[72:75], s84 offen offset:3072 lds\n" \
"s_waitcnt lgkmcnt(9)\n" \
"v_mfma_f32_16x16x32_bf16 a[8:11], a[232:235], a[192:195], a[8:11]\n" \
"ds_read_b64_tr_b16 a[178:179], v36 offset:1024\n" \
"s_waitcnt lgkmcnt(6)\n" \
"v_mfma_f32_16x16x32_bf16 a[12:15], a[172:175], a[128:131], a[12:15]\n" \
"ds_read_b64_tr_b16 a[240:241], v36 offset:8192\n" \
"s_waitcnt lgkmcnt(5)\n" \
"v_mfma_f32_16x16x32_bf16 a[12:15], a[236:239], a[192:195], a[12:15]\n" \
"v_mfma_f32_16x16x32_bf16 a[28:31], a[172:175], a[132:135], a[28:31]\n" \
"v_mfma_f32_16x16x32_bf16 a[28:31], a[236:239], a[196:199], a[28:31]\n" \
"ds_read_b64_tr_b16 a[242:243], v36 offset:9216\n" \
"v_mfma_f32_16x16x32_bf16 a[24:27], a[168:171], a[132:135], a[24:27]\n" \
"v_mfma_f32_16x16x32_bf16 a[24:27], a[232:235], a[196:199], a[24:27]\n" \
"v_mfma_f32_16x16x32_bf16 a[20:23], a[164:167], a[132:135], a[20:23]\n" \
"v_mfma_f32_16x16x32_bf16 a[20:23], a[228:231], a[196:199], a[20:23]\n" \
"ds_read_b64_tr_b16 a[180:181], v37 offset:0\n" \
"v_mfma_f32_16x16x32_bf16 a[16:19], a[160:163], a[132:135], a[16:19]\n" \
"v_mfma_f32_16x16x32_bf16 a[16:19], a[224:227], a[196:199], a[16:19]\n" \
"v_mfma_f32_16x16x32_bf16 a[32:35], a[160:163], a[136:139], a[32:35]\n" \
"ds_read_b64_tr_b16 a[182:183], v37 offset:1024\n" \
"v_mfma_f32_16x16x32_bf16 a[32:35], a[224:227], a[200:203], a[32:35]\n" \
"v_mfma_f32_16x16x32_bf16 a[36:39], a[164:167], a[136:139], a[36:39]\n" \
"v_mfma_f32_16x16x32_bf16 a[36:39], a[228:231], a[200:203], a[36:39]\n" \
"buffer_store_dwordx4 v[200:203], v20, s[80:83], s35 offen sc1\n" \
".fill (L_pb + 12000 - .) / 4, 4, 0xBF800000\n" \
"L_pp3:\n" \
"s_cbranch_execz L_pret\n" \
"v_mfma_f32_16x16x32_bf16 a[40:43], a[168:171], a[136:139], a[40:43]\n" \
"s_mov_b32 m0, s32\n" \
"ds_read_b64_tr_b16 a[244:245], v37 offset:8192\n" \
"v_mfma_f32_16x16x32_bf16 a[40:43], a[232:235], a[200:203], a[40:43]\n" \
"s_mov_b32 exec_lo, 0x0\n" \
"s_mov_b32 exec_hi, 0xffffffff\n" \
"global_load_lds_dword v[188:189], off\n" \
"s_mov_b64 exec, -1\n" \
"s_nop 0\n" \
"v_mfma_f32_16x16x32_bf16 a[44:47], a[172:175], a[136:139], a[44:47]\n" \
"ds_read_b64_tr_b16 a[246:247], v37 offset:9216\n" \
"v_mfma_f32_16x16x32_bf16 a[44:47], a[236:239], a[200:203], a[44:47]\n" \
"v_mfma_f32_16x16x32_bf16 a[60:63], a[172:175], a[140:143], a[60:63]\n" \
"v_mfma_f32_16x16x32_bf16 a[60:63], a[236:239], a[204:207], a[60:63]\n" \
"v_mfma_f32_16x16x32_bf16 a[56:59], a[168:171], a[140:143], a[56:59]\n" \
"ds_read_b64_tr_b16 a[184:185], v38 offset:0\n" \
"v_mfma_f32_16x16x32_bf16 a[56:59], a[232:235], a[204:207], a[56:59]\n" \
"v_mfma_f32_16x16x32_bf16 a[52:55], a[164:167], a[140:143], a[52:55]\n" \
"buffer_store_dwordx4 v[204:207], v20, s[80:83], s88 offen sc1\n" \
"v_mfma_f32_16x16x32_bf16 a[52:55], a[228:231], a[204:207], a[52:55]\n" \
"ds_read_b64_tr_b16 a[186:187], v38 offset:1024\n" \
"v_mfma_f32_16x16x32_bf16 a[48:51], a[160:163], a[140:143], a[48:51]\n" \
"v_mfma_f32_16x16x32_bf16 a[48:51], a[224:227], a[204:207], a[48:51]\n" \
"v_mfma_f32_16x16x32_bf16 a[64:67], a[160:163], a[144:147], a[64:67]\n" \
"ds_read_b64_tr_b16 a[248:249], v38 offset:8192\n" \
"v_mfma_f32_16x16x32_bf16 a[64:67], a[224:227], a[208:211], a[64:67]\n" \
"v_mfma_f32_16x16x32_bf16 a[68:71], a[164:167], a[144:147], a[68:71]\n" \
"v_mfma_f32_16x16x32_bf16 a[68:71], a[228:231], a[208:211], a[68:71]\n" \
"ds_read_b64_tr_b16 a[250:251], v38 offset:9216\n" \
"v_mfma_f32_16x16x32_bf16 a[72:75], a[168:171], a[144:147], a[72:75]\n" \
"v_mfma_f32_16x16x32_bf16 a[72:75], a[232:235], a[208:211], a[72:75]\n" \
"v_mfma_f32_16x16x32_bf16 a[76:79], a[172:175], a[144:147], a[76:79]\n" \
"buffer_store_dwordx4 v[208:211], v20, s[80:83], s36 offen sc1\n" \
"v_mfma_f32_16x16x32_bf16 a[76:79], a[236:239], a[208:211], a[76:79]\n" \
"ds_read_b64_tr_b16 a[188:189], v39 offset:0\n" \
"v_mfma_f32_16x16x32_bf16 a[92:95], a[172:175], a[148:151], a[92:95]\n" \
"v_mfma_f32_16x16x32_bf16 a[92:95], a[236:239], a[212:215], a[92:95]\n" \
"v_mfma_f32_16x16x32_bf16 a[88:91], a[168:171], a[148:151], a[88:91]\n" \
"ds_read_b64_tr_b16 a[190:191], v39 offset:1024\n" \
"v_mfma_f32_16x16x32_bf16 a[88:91], a[232:235], a[212:215], a[88:91]\n" \
"v_mfma_f32_16x16x32_bf16 a[84:87], a[164:167], a[148:151], a[84:87]\n" \
"v_mfma_f32_16x16x32_bf16 a[84:87], a[228:231], a[212:215], a[84:87]\n" \
"ds_read_b64_tr_b16 a[252:253], v39 offset:8192\n" \
"v_mfma_f32_16x16x32_bf16 a[80:83], a[160:163], a[148:151], a[80:83]\n" \
"v_mfma_f32_16x16x32_bf16 a[80:83], a[224:227], a[212:215], a[80:83]\n" \
"v_mfma_f32_16x16x32_bf16 a[96:99], a[160:163], a[152:155], a[96:99]\n" \
"ds_read_b64_tr_b16 a[254:255], v39 offset:9216\n" \
"v_mfma_f32_16x16x32_bf16 a[96:99], a[224:227], a[216:219], a[96:99]\n" \
"v_mfma_f32_16x16x32_bf16 a[100:103], a[164:167], a[152:155], a[100:103]\n" \
"v_mfma_f32_16x16x32_bf16 a[100:103], a[228:231], a[216:219], a[100:103]\n" \
"v_mfma_f32_16x16x32_bf16 a[104:107], a[168:171], a[152:155], a[104:107]\n" \
"v_mfma_f32_16x16x32_bf16 a[104:107], a[232:235], a[216:219], a[104:107]\n" \
"v_mfma_f32_16x16x32_bf16 a[108:111], a[172:175], a[152:155], a[108:111]\n" \
"v_mfma_f32_16x16x32_bf16 a[108:111], a[236:239], a[216:219], a[108:111]\n" \
"s_waitcnt lgkmcnt(15)\n" \
"v_mfma_f32_16x16x32_bf16 a[124:127], a[172:175], a[156:159], a[124:127]\n" \
"v_mfma_f32_16x16x32_bf16 a[124:127], a[236:239], a[220:223], a[124:127]\n" \
"v_mfma_f32_16x16x32_bf16 a[120:123], a[168:171], a[156:159], a[120:123]\n" \
"v_mfma_f32_16x16x32_bf16 a[120:123], a[232:235], a[220:223], a[120:123]\n" \
"v_mfma_f32_16x16x32_bf16 a[116:119], a[164:167], a[156:159], a[116:119]\n" \
"v_mfma_f32_16x16x32_bf16 a[116:119], a[228:231], a[220:223], a[116:119]\n" \
"v_mfma_f32_16x16x32_bf16 a[112:115], a[160:163], a[156:159], a[112:115]\n" \
"s_waitcnt vmcnt(4) lgkmcnt(0)\n" \
"v_mfma_f32_16x16x32_bf16 a[112:115], a[224:227], a[220:223], a[112:115]\n" \
"s_barrier\n" \
"v_mfma_f32_16x16x32_bf16 v[52:55], a[176:179], a[128:131], v[52:55]\n" \
"s_add_u32 m0, s30, 0x0\n" \
"s_movk_i32 s84, 0x300\n" \
"v_mfma_f32_16x16x32_bf16 v[52:55], a[240:243], a[192:195], v[52:55]\n" \
"buffer_load_dwordx4 v22, s[64:67], s84 offen offset:0 lds\n" \
"v_mfma_f32_16x16x32_bf16 v[56:59], a[180:183], a[128:131], v[56:59]\n" \
"ds_read_b64_tr_b16 a[160:161], v32 offset:32768\n" \
"v_mfma_f32_16x16x32_bf16 v[56:59], a[244:247], a[192:195], v[56:59]\n" \
"s_movk_i32 s84, 0x1f00\n" \
"v_mfma_f32_16x16x32_bf16 v[60:63], a[184:187], a[128:131], v[60:63]\n" \
"buffer_load_dwordx4 v23, s[64:67], s84 offen offset:1024 lds\n" \
"v_mfma_f32_16x16x32_bf16 v[60:63], a[248:251], a[192:195], v[60:63]\n" \
"ds_read_b64_tr_b16 a[162:163], v32 offset:33792\n" \
"v_mfma_f32_16x16x32_bf16 v[64:67], a[188:191], a[128:131], v[64:67]\n" \
"ds_read_b64_tr_b16 a[224:225], v32 offset:40960\n" \
"v_mfma_f32_16x16x32_bf16 v[64:67], a[252:255], a[192:195], v[64:67]\n" \
"ds_read_b128 a[128:131], v28 offset:49152\n" \
"v_mfma_f32_16x16x32_bf16 v[80:83], a[188:191], a[132:135], v[80:83]\n" \
"ds_read_b128 a[192:195], v29 offset:49152\n" \
"v_mfma_f32_16x16x32_bf16 v[80:83], a[252:255], a[196:199], v[80:83]\n" \
"s_movk_i32 s84, 0x3b00\n" \
"v_mfma_f32_16x16x32_bf16 v[76:79], a[184:187], a[132:135], v[76:79]\n" \
"buffer_load_dwordx4 v22, s[64:67], s84 offen offset:2048 lds\n" \
"v_mfma_f32_16x16x32_bf16 v[76:79], a[248:251], a[196:199], v[76:79]\n" \
"s_movk_i32 s84, 0x5700\n" \
"v_mfma_f32_16x16x32_bf16 v[72:75], a[180:183], a[132:135], v[72:75]\n" \
"buffer_load_dwordx4 v23, s[64:67], s84 offen offset:3072 lds\n" \
"v_mfma_f32_16x16x32_bf16 v[72:75], a[244:247], a[196:199], v[72:75]\n" \
"s_add_u32 m0, s31, 0x0\n" \
"s_mov_b32 s84, 0x300000\n" \
"v_mfma_f32_16x16x32_bf16 v[68:71], a[176:179], a[132:135], v[68:71]\n" \
"buffer_load_dwordx4 v26, s[68:71], s84 offen offset:0 lds\n" \
"v_mfma_f32_16x16x32_bf16 v[68:71], a[240:243], a[196:199], v[68:71]\n" \
"ds_read_b128 a[132:135], v28 offset:51200\n" \
"v_mfma_f32_16x16x32_bf16 v[84:87], a[176:179], a[136:139], v[84:87]\n" \
"ds_read_b128 a[196:199], v29 offset:51200\n" \
"v_mfma_f32_16x16x32_bf16 v[84:87], a[240:243], a[200:203], v[84:87]\n" \
"s_addk_i32 s84, 0x7c00\n" \
"v_mfma_f32_16x16x32_bf16 v[88:91], a[180:183], a[136:139], v[88:91]\n" \
"buffer_load_dwordx4 v26, s[68:71], s84 offen offset:1024 lds\n" \
"v_mfma_f32_16x16x32_bf16 v[88:91], a[244:247], a[200:203], v[88:91]\n" \
"ds_read_b64_tr_b16 a[226:227], v32 offset:41984\n" \
"v_mfma_f32_16x16x32_bf16 v[92:95], a[184:187], a[136:139], v[92:95]\n" \
"s_addk_i32 s84, 0x7c00\n" \
"v_mfma_f32_16x16x32_bf16 v[92:95], a[248:251], a[200:203], v[92:95]\n" \
"buffer_load_dwordx4 v27, s[68:71], s84 offen offset:2048 lds\n" \
"v_mfma_f32_16x16x32_bf16 v[96:99], a[188:191], a[136:139], v[96:99]\n" \
"ds_read_b64_tr_b16 a[164:165], v33 offset:32768\n" \
"v_mfma_f32_16x16x32_bf16 v[96:99], a[252:255], a[200:203], v[96:99]\n" \
"ds_read_b128 a[136:139], v28 offset:53248\n" \
"v_mfma_f32_16x16x32_bf16 v[112:115], a[188:191], a[140:143], v[112:115]\n" \
"ds_read_b128 a[200:203], v29 offset:53248\n" \
"v_mfma_f32_16x16x32_bf16 v[112:115], a[252:255], a[204:207], v[112:115]\n" \
"s_addk_i32 s84, 0x7c00\n" \
"v_mfma_f32_16x16x32_bf16 v[108:111], a[184:187], a[140:143], v[108:111]\n" \
"buffer_load_dwordx4 v27, s[68:71], s84 offen offset:3072 lds\n" \
"v_mfma_f32_16x16x32_bf16 v[108:111], a[248:251], a[204:207], v[108:111]\n" \
"s_add_u32 m0, s30, 0x1000\n" \
"s_mov_b32 s84, 0x8300\n" \
"v_mfma_f32_16x16x32_bf16 v[104:107], a[180:183], a[140:143], v[104:107]\n" \
"buffer_load_dwordx4 v22, s[64:67], s84 offen offset:0 lds\n" \
"v_mfma_f32_16x16x32_bf16 v[104:107], a[244:247], a[204:207], v[104:107]\n" \
"ds_read_b64_tr_b16 a[166:167], v33 offset:33792\n" \
"v_mfma_f32_16x16x32_bf16 v[100:103], a[176:179], a[140:143], v[100:103]\n" \
"ds_read_b64_tr_b16 a[228:229], v33 offset:40960\n" \
"v_mfma_f32_16x16x32_bf16 v[100:103], a[240:243], a[204:207], v[100:103]\n" \
"ds_read_b128 a[140:143], v28 offset:55296\n" \
"v_mfma_f32_16x16x32_bf16 v[116:119], a[176:179], a[144:147], v[116:119]\n" \
"ds_read_b128 a[204:207], v29 offset:55296\n" \
"v_mfma_f32_16x16x32_bf16 v[116:119], a[240:243], a[208:211], v[116:119]\n" \
"s_addk_i32 s84, 0x1c00\n" \
"v_mfma_f32_16x16x32_bf16 v[120:123], a[180:183], a[144:147], v[120:123]\n" \
"buffer_load_dwordx4 v23, s[64:67], s84 offen offset:1024 lds\n" \
"v_mfma_f32_16x16x32_bf16 v[120:123], a[244:247], a[208:211], v[120:123]\n" \
"s_addk_i32 s84, 0x1c00\n" \
"v_mfma_f32_16x16x32_bf16 v[124:127], a[184:187], a[144:147], v[124:127]\n" \
"buffer_load_dwordx4 v22, s[64:67], s84 offen offset:2048 lds\n" \
"v_mfma_f32_16x16x32_bf16 v[124:127], a[248:251], a[208:211], v[124:127]\n" \
"s_addk_i32 s84, 0x1c00\n" \
"s_nop 0\n" \
"v_mfma_f32_16x16x32_bf16 v[128:131], a[188:191], a[144:147], v[128:131]\n" \
"buffer_load_dwordx4 v23, s[64:67], s84 offen offset:3072 lds\n" \
"v_mfma_f32_16x16x32_bf16 v[128:131], a[252:255], a[208:211], v[128:131]\n" \
"ds_read_b128 a[144:147], v28 offset:32768\n" \
"v_mfma_f32_16x16x32_bf16 v[144:147], a[188:191], a[148:151], v[144:147]\n" \
"ds_read_b128 a[208:211], v29 offset:32768\n" \
"v_mfma_f32_16x16x32_bf16 v[144:147], a[252:255], a[212:215], v[144:147]\n" \
"s_add_u32 m0, s31, 0x4000\n" \
"s_mov_b32 s84, 0x300000\n" \
"v_mfma_f32_16x16x32_bf16 v[140:143], a[184:187], a[148:151], v[140:143]\n" \
"buffer_load_dwordx4 v26, s[72:75], s84 offen offset:0 lds\n" \
"v_mfma_f32_16x16x32_bf16 v[140:143], a[248:251], a[212:215], v[140:143]\n" \
"ds_read_b64_tr_b16 a[230:231], v33 offset:41984\n" \
"v_mfma_f32_16x16x32_bf16 v[136:139], a[180:183], a[148:151], v[136:139]\n" \
"s_addk_i32 s84, 0x7c00\n" \
"s_nop 0\n" \
"v_mfma_f32_16x16x32_bf16 v[136:139], a[244:247], a[212:215], v[136:139]\n" \
"buffer_load_dwordx4 v26, s[72:75], s84 offen offset:1024 lds\n" \
"v_mfma_f32_16x16x32_bf16 v[132:135], a[176:179], a[148:151], v[132:135]\n" \
"ds_read_b64_tr_b16 a[168:169], v34 offset:32768\n" \
"v_mfma_f32_16x16x32_bf16 v[132:135], a[240:243], a[212:215], v[132:135]\n" \
"ds_read_b128 a[148:151], v28 offset:34816\n" \
"v_mfma_f32_16x16x32_bf16 v[148:151], a[176:179], a[152:155], v[148:151]\n" \
"ds_read_b128 a[212:215], v29 offset:34816\n" \
"v_mfma_f32_16x16x32_bf16 v[148:151], a[240:243], a[216:219], v[148:151]\n" \
"ds_read_b64_tr_b16 a[170:171], v34 offset:33792\n" \
"v_mfma_f32_16x16x32_bf16 v[152:155], a[180:183], a[152:155], v[152:155]\n" \
"ds_read_b64_tr_b16 a[232:233], v34 offset:40960\n" \
"v_mfma_f32_16x16x32_bf16 v[152:155], a[244:247], a[216:219], v[152:155]\n" \
"ds_read_b64_tr_b16 a[234:235], v34 offset:41984\n" \
"v_mfma_f32_16x16x32_bf16 v[156:159], a[184:187], a[152:155], v[156:159]\n" \
"ds_read_b64_tr_b16 a[172:173], v35 offset:32768\n" \
"v_mfma_f32_16x16x32_bf16 v[156:159], a[248:251], a[216:219], v[156:159]\n" \
"ds_read_b64_tr_b16 a[174:175], v35 offset:33792\n" \
"v_mfma_f32_16x16x32_bf16 v[160:163], a[188:191], a[152:155], v[160:163]\n" \
"ds_read_b64_tr_b16 a[236:237], v35 offset:40960\n" \
"v_mfma_f32_16x16x32_bf16 v[160:163], a[252:255], a[216:219], v[160:163]\n" \
"ds_read_b128 a[152:155], v28 offset:36864\n" \
"v_mfma_f32_16x16x32_bf16 v[176:179], a[188:191], a[156:159], v[176:179]\n" \
"ds_read_b128 a[216:219], v29 offset:36864\n" \
"v_mfma_f32_16x16x32_bf16 v[176:179], a[252:255], a[220:223], v[176:179]\n" \
"ds_read_b64_tr_b16 a[238:239], v35 offset:41984\n" \
"v_mfma_f32_16x16x32_bf16 v[172:175], a[184:187], a[156:159], v[172:175]\n" \
"v_mfma_f32_16x16x32_bf16 v[172:175], a[248:251], a[220:223], v[172:175]\n" \
"v_mfma_f32_16x16x32_bf16 v[168:171], a[180:183], a[156:159], v[168:171]\n" \
"v_mfma_f32_16x16x32_bf16 v[168:171], a[244:247], a[220:223], v[168:171]\n" \
"v_mfma_f32_16x16x32_bf16 v[164:167], a[176:179], a[156:159], v[164:167]\n" \
"v_mfma_f32_16x16x32_bf16 v[164:167], a[240:243], a[220:223], v[164:167]\n" \
"ds_read_b128 a[156:159], v28 offset:38912\n" \
"ds_read_b128 a[220:223], v29 offset:38912\n" \
"s_waitcnt lgkmcnt(15)\n" \
"v_mfma_f32_16x16x32_bf16 a[0:3], a[160:163], a[128:131], a[0:3]\n" \
"s_addk_i32 s84, 0x7c00\n" \
"v_mfma_f32_16x16x32_bf16 a[0:3], a[224:227], a[192:195], a[0:3]\n" \
"buffer_load_dwordx4 v27, s[72:75], s84 offen offset:2048 lds\n" \
"v_mfma_f32_16x16x32_bf16 a[4:7], a[164:167], a[128:131], a[4:7]\n" \
"ds_read_b64_tr_b16 a[176:177], v36 offset:32768\n" \
"s_waitcnt lgkmcnt(15)\n" \
"v_mfma_f32_16x16x32_bf16 a[4:7], a[228:231], a[192:195], a[4:7]\n" \
"s_addk_i32 s84, 0x7c00\n" \
"s_waitcnt lgkmcnt(11)\n" \
"v_mfma_f32_16x16x32_bf16 a[8:11], a[168:171], a[128:131], a[8:11]\n" \
"buffer_load_dwordx4 v27, s[72:75], s84 offen offset:3072 lds\n" \
"s_waitcnt lgkmcnt(9)\n" \
"v_mfma_f32_16x16x32_bf16 a[8:11], a[232:235], a[192:195], a[8:11]\n" \
"ds_read_b64_tr_b16 a[178:179], v36 offset:33792\n" \
"s_waitcnt lgkmcnt(8)\n" \
"v_mfma_f32_16x16x32_bf16 a[12:15], a[172:175], a[128:131], a[12:15]\n" \
"ds_read_b64_tr_b16 a[240:241], v36 offset:40960\n" \
"s_waitcnt lgkmcnt(5)\n" \
"v_mfma_f32_16x16x32_bf16 a[12:15], a[236:239], a[192:195], a[12:15]\n" \
"v_mfma_f32_16x16x32_bf16 a[28:31], a[172:175], a[132:135], a[28:31]\n" \
"v_mfma_f32_16x16x32_bf16 a[28:31], a[236:239], a[196:199], a[28:31]\n" \
"ds_read_b64_tr_b16 a[242:243], v36 offset:41984\n" \
"v_mfma_f32_16x16x32_bf16 a[24:27], a[168:171], a[132:135], a[24:27]\n" \
"v_mfma_f32_16x16x32_bf16 a[24:27], a[232:235], a[196:199], a[24:27]\n" \
"v_mfma_f32_16x16x32_bf16 a[20:23], a[164:167], a[132:135], a[20:23]\n" \
"v_mfma_f32_16x16x32_bf16 a[20:23], a[228:231], a[196:199], a[20:23]\n" \
"ds_read_b64_tr_b16 a[180:181], v37 offset:32768\n" \
"v_mfma_f32_16x16x32_bf16 a[16:19], a[160:163], a[132:135], a[16:19]\n" \
"v_mfma_f32_16x16x32_bf16 a[16:19], a[224:227], a[196:199], a[16:19]\n" \
"v_mfma_f32_16x16x32_bf16 a[32:35], a[160:163], a[136:139], a[32:35]\n" \
"ds_read_b64_tr_b16 a[182:183], v37 offset:33792\n" \
"v_mfma_f32_16x16x32_bf16 a[32:35], a[224:227], a[200:203], a[32:35]\n" \
"v_mfma_f32_16x16x32_bf16 a[36:39], a[164:167], a[136:139], a[36:39]\n" \
"v_lshlrev_b32_e32 v45, 3, v20\n" \
"v_add_u32_e32 v45, s31, v45\n" \
"v_mfma_f32_16x16x32_bf16 a[36:39], a[228:231], a[200:203], a[36:39]\n" \
"s_getpc_b64 s[86:87]\n" \
"s_sub_u32 s86, s86, 0xf800\n" \
"s_subb_u32 s87, s87, 0\n" \
"s_mov_b32 m0, s32\n" \
"s_nop 0\n" \
"v_mfma_f32_16x16x32_bf16 a[40:43], a[168:171], a[136:139], a[40:43]\n" \
"global_load_lds_dword v45, s[86:87]\n" \
"v_mfma_f32_16x16x32_bf16 a[40:43], a[232:235], a[200:203], a[40:43]\n" \
"ds_read_b64_tr_b16 a[244:245], v37 offset:40960\n" \
"v_mfma_f32_16x16x32_bf16 a[44:47], a[172:175], a[136:139], a[44:47]\n" \
"ds_read_b64_tr_b16 a[246:247], v37 offset:41984\n" \
"v_mfma_f32_16x16x32_bf16 a[44:47], a[236:239], a[200:203], a[44:47]\n" \
"v_mfma_f32_16x16x32_bf16 a[60:63], a[172:175], a[140:143], a[60:63]\n" \
"v_mfma_f32_16x16x32_bf16 a[60:63], a[236:239], a[204:207], a[60:63]\n" \
"v_mfma_f32_16x16x32_bf16 a[56:59], a[168:171], a[140:143], a[56:59]\n" \
"ds_read_b64_tr_b16 a[184:185], v38 offset:32768\n" \
"v_mfma_f32_16x16x32_bf16 a[56:59], a[232:235], a[204:207], a[56:59]\n" \
"v_mfma_f32_16x16x32_bf16 a[52:55], a[164:167], a[140:143], a[52:55]\n" \
"v_mfma_f32_16x16x32_bf16 a[52:55], a[228:231], a[204:207], a[52:55]\n" \
"ds_read_b64_tr_b16 a[186:187], v38 offset:33792\n" \
"v_mfma_f32_16x16x32_bf16 a[48:51], a[160:163], a[140:143], a[48:51]\n" \
"v_mfma_f32_16x16x32_bf16 a[48:51], a[224:227], a[204:207], a[48:51]\n" \
"v_mfma_f32_16x16x32_bf16 a[64:67], a[160:163], a[144:147], a[64:67]\n" \
"ds_read_b64_tr_b16 a[248:249], v38 offset:40960\n" \
"v_mfma_f32_16x16x32_bf16 a[64:67], a[224:227], a[208:211], a[64:67]\n" \
"v_mfma_f32_16x16x32_bf16 a[68:71], a[164:167], a[144:147], a[68:71]\n" \
"v_mfma_f32_16x16x32_bf16 a[68:71], a[228:231], a[208:211], a[68:71]\n" \
"ds_read_b64_tr_b16 a[250:251], v38 offset:41984\n" \
"v_mfma_f32_16x16x32_bf16 a[72:75], a[168:171], a[144:147], a[72:75]\n" \
"v_mfma_f32_16x16x32_bf16 a[72:75], a[232:235], a[208:211], a[72:75]\n" \
"v_mfma_f32_16x16x32_bf16 a[76:79], a[172:175], a[144:147], a[76:79]\n" \
"v_mfma_f32_16x16x32_bf16 a[76:79], a[236:239], a[208:211], a[76:79]\n" \
"ds_read_b64_tr_b16 a[188:189], v39 offset:32768\n" \
"v_mfma_f32_16x16x32_bf16 a[92:95], a[172:175], a[148:151], a[92:95]\n" \
"v_mfma_f32_16x16x32_bf16 a[92:95], a[236:239], a[212:215], a[92:95]\n" \
"v_mfma_f32_16x16x32_bf16 a[88:91], a[168:171], a[148:151], a[88:91]\n" \
"ds_read_b64_tr_b16 a[190:191], v39 offset:33792\n" \
"v_mfma_f32_16x16x32_bf16 a[88:91], a[232:235], a[212:215], a[88:91]\n" \
"v_mfma_f32_16x16x32_bf16 a[84:87], a[164:167], a[148:151], a[84:87]\n" \
"v_mfma_f32_16x16x32_bf16 a[84:87], a[228:231], a[212:215], a[84:87]\n" \
"ds_read_b64_tr_b16 a[252:253], v39 offset:40960\n" \
"v_mfma_f32_16x16x32_bf16 a[80:83], a[160:163], a[148:151], a[80:83]\n" \
"v_mfma_f32_16x16x32_bf16 a[80:83], a[224:227], a[212:215], a[80:83]\n" \
"v_mfma_f32_16x16x32_bf16 a[96:99], a[160:163], a[152:155], a[96:99]\n" \
"ds_read_b64_tr_b16 a[254:255], v39 offset:41984\n" \
"v_mfma_f32_16x16x32_bf16 a[96:99], a[224:227], a[216:219], a[96:99]\n" \
"v_mfma_f32_16x16x32_bf16 a[100:103], a[164:167], a[152:155], a[100:103]\n" \
"v_mfma_f32_16x16x32_bf16 a[100:103], a[228:231], a[216:219], a[100:103]\n" \
"v_mfma_f32_16x16x32_bf16 a[104:107], a[168:171], a[152:155], a[104:107]\n" \
"v_mfma_f32_16x16x32_bf16 a[104:107], a[232:235], a[216:219], a[104:107]\n" \
"v_mfma_f32_16x16x32_bf16 a[108:111], a[172:175], a[152:155], a[108:111]\n" \
"v_mfma_f32_16x16x32_bf16 a[108:111], a[236:239], a[216:219], a[108:111]\n" \
"s_waitcnt lgkmcnt(15)\n" \
"v_mfma_f32_16x16x32_bf16 a[124:127], a[172:175], a[156:159], a[124:127]\n" \
"v_mfma_f32_16x16x32_bf16 a[124:127], a[236:239], a[220:223], a[124:127]\n" \
"v_mfma_f32_16x16x32_bf16 a[120:123], a[168:171], a[156:159], a[120:123]\n" \
"v_mfma_f32_16x16x32_bf16 a[120:123], a[232:235], a[220:223], a[120:123]\n" \
"v_mfma_f32_16x16x32_bf16 a[116:119], a[164:167], a[156:159], a[116:119]\n" \
"v_mfma_f32_16x16x32_bf16 a[116:119], a[228:231], a[220:223], a[116:119]\n" \
"v_mfma_f32_16x16x32_bf16 a[112:115], a[160:163], a[156:159], a[112:115]\n" \
"s_waitcnt vmcnt(1) lgkmcnt(0)\n" \
"v_mfma_f32_16x16x32_bf16 a[112:115], a[224:227], a[220:223], a[112:115]\n" \
"s_barrier\n" \
"v_mov_b32_e32 v45, s62\n" \
"s_mov_b64 exec, 1\n" \
"buffer_store_dword v45, off, s[80:83], s37 offset:128 sc1\n" \
"s_mov_b64 exec, -1\n" \
"v_mfma_f32_16x16x32_bf16 v[52:55], a[176:179], a[128:131], v[52:55]\n" \
"s_add_u32 m0, s30, 0x8000\n" \
"s_movk_i32 s84, 0x380\n" \
"v_mfma_f32_16x16x32_bf16 v[52:55], a[240:243], a[192:195], v[52:55]\n" \
"buffer_load_dwordx4 v22, s[64:67], s84 offen offset:0 lds\n" \
"v_mfma_f32_16x16x32_bf16 v[56:59], a[180:183], a[128:131], v[56:59]\n" \
"ds_read_b64_tr_b16 a[160:161], v32 offset:0\n" \
"v_mfma_f32_16x16x32_bf16 v[56:59], a[244:247], a[192:195], v[56:59]\n" \
"s_movk_i32 s84, 0x1f80\n" \
"v_mfma_f32_16x16x32_bf16 v[60:63], a[184:187], a[128:131], v[60:63]\n" \
"buffer_load_dwordx4 v23, s[64:67], s84 offen offset:1024 lds\n" \
"v_mfma_f32_16x16x32_bf16 v[60:63], a[248:251], a[192:195], v[60:63]\n" \
"ds_read_b64_tr_b16 a[162:163], v32 offset:1024\n" \
"v_mfma_f32_16x16x32_bf16 v[64:67], a[188:191], a[128:131], v[64:67]\n" \
"ds_read_b64_tr_b16 a[224:225], v32 offset:8192\n" \
"v_mfma_f32_16x16x32_bf16 v[64:67], a[252:255], a[192:195], v[64:67]\n" \
"ds_read_b128 a[128:131], v28 offset:16384\n" \
"v_mfma_f32_16x16x32_bf16 v[80:83], a[188:191], a[132:135], v[80:83]\n" \
"ds_read_b128 a[192:195], v29 offset:16384\n" \
"v_mfma_f32_16x16x32_bf16 v[80:83], a[252:255], a[196:199], v[80:83]\n" \
"s_movk_i32 s84, 0x3b80\n" \
"v_mfma_f32_16x16x32_bf16 v[76:79], a[184:187], a[132:135], v[76:79]\n" \
"buffer_load_dwordx4 v22, s[64:67], s84 offen offset:2048 lds\n" \
"v_mfma_f32_16x16x32_bf16 v[76:79], a[248:251], a[196:199], v[76:79]\n" \
"s_movk_i32 s84, 0x5780\n" \
"v_mfma_f32_16x16x32_bf16 v[72:75], a[180:183], a[132:135], v[72:75]\n" \
"buffer_load_dwordx4 v23, s[64:67], s84 offen offset:3072 lds\n" \
"v_mfma_f32_16x16x32_bf16 v[72:75], a[244:247], a[196:199], v[72:75]\n" \
"s_add_u32 m0, s31, 0x8000\n" \
"s_mov_b32 s84, 0x380000\n" \
"v_mfma_f32_16x16x32_bf16 v[68:71], a[176:179], a[132:135], v[68:71]\n" \
"buffer_load_dwordx4 v26, s[68:71], s84 offen offset:0 lds\n" \
"v_mfma_f32_16x16x32_bf16 v[68:71], a[240:243], a[196:199], v[68:71]\n" \
"ds_read_b128 a[132:135], v28 offset:18432\n" \
"v_mfma_f32_16x16x32_bf16 v[84:87], a[176:179], a[136:139], v[84:87]\n" \
"ds_read_b128 a[196:199], v29 offset:18432\n" \
"v_mfma_f32_16x16x32_bf16 v[84:87], a[240:243], a[200:203], v[84:87]\n" \
"s_addk_i32 s84, 0x7c00\n" \
"v_mfma_f32_16x16x32_bf16 v[88:91], a[180:183], a[136:139], v[88:91]\n" \
"buffer_load_dwordx4 v26, s[68:71], s84 offen offset:1024 lds\n" \
"v_mfma_f32_16x16x32_bf16 v[88:91], a[244:247], a[200:203], v[88:91]\n" \
"ds_read_b64_tr_b16 a[226:227], v32 offset:9216\n" \
"v_mfma_f32_16x16x32_bf16 v[92:95], a[184:187], a[136:139], v[92:95]\n" \
"s_addk_i32 s84, 0x7c00\n" \
"v_mfma_f32_16x16x32_bf16 v[92:95], a[248:251], a[200:203], v[92:95]\n" \
"buffer_load_dwordx4 v27, s[68:71], s84 offen offset:2048 lds\n" \
"v_mfma_f32_16x16x32_bf16 v[96:99], a[188:191], a[136:139], v[96:99]\n" \
"ds_read_b64_tr_b16 a[164:165], v33 offset:0\n" \
"v_mfma_f32_16x16x32_bf16 v[96:99], a[252:255], a[200:203], v[96:99]\n" \
"ds_read_b128 a[136:139], v28 offset:20480\n" \
"v_mfma_f32_16x16x32_bf16 v[112:115], a[188:191], a[140:143], v[112:115]\n" \
"ds_read_b128 a[200:203], v29 offset:20480\n" \
"v_mfma_f32_16x16x32_bf16 v[112:115], a[252:255], a[204:207], v[112:115]\n" \
"s_addk_i32 s84, 0x7c00\n" \
"s_nop 0\n" \
"v_mfma_f32_16x16x32_bf16 v[108:111], a[184:187], a[140:143], v[108:111]\n" \
"buffer_load_dwordx4 v27, s[68:71], s84 offen offset:3072 lds\n" \
"v_mfma_f32_16x16x32_bf16 v[108:111], a[248:251], a[204:207], v[108:111]\n" \
"s_add_u32 m0, s30, 0x9000\n" \
"s_mov_b32 s84, 0x8380\n" \
"v_mfma_f32_16x16x32_bf16 v[104:107], a[180:183], a[140:143], v[104:107]\n" \
"buffer_load_dwordx4 v22, s[64:67], s84 offen offset:0 lds\n" \
"v_mfma_f32_16x16x32_bf16 v[104:107], a[244:247], a[204:207], v[104:107]\n" \
"ds_read_b64_tr_b16 a[166:167], v33 offset:1024\n" \
"v_mfma_f32_16x16x32_bf16 v[100:103], a[176:179], a[140:143], v[100:103]\n" \
"ds_read_b64_tr_b16 a[228:229], v33 offset:8192\n" \
"v_mfma_f32_16x16x32_bf16 v[100:103], a[240:243], a[204:207], v[100:103]\n" \
"ds_read_b128 a[140:143], v28 offset:22528\n" \
"v_mfma_f32_16x16x32_bf16 v[116:119], a[176:179], a[144:147], v[116:119]\n" \
"ds_read_b128 a[204:207], v29 offset:22528\n" \
"v_mfma_f32_16x16x32_bf16 v[116:119], a[240:243], a[208:211], v[116:119]\n" \
"s_addk_i32 s84, 0x1c00\n" \
"v_mfma_f32_16x16x32_bf16 v[120:123], a[180:183], a[144:147], v[120:123]\n" \
"buffer_load_dwordx4 v23, s[64:67], s84 offen offset:1024 lds\n" \
"v_mfma_f32_16x16x32_bf16 v[120:123], a[244:247], a[208:211], v[120:123]\n" \
"s_addk_i32 s84, 0x1c00\n" \
"v_mfma_f32_16x16x32_bf16 v[124:127], a[184:187], a[144:147], v[124:127]\n" \
"buffer_load_dwordx4 v22, s[64:67], s84 offen offset:2048 lds\n" \
"v_mfma_f32_16x16x32_bf16 v[124:127], a[248:251], a[208:211], v[124:127]\n" \
"s_addk_i32 s84, 0x1c00\n" \
"s_nop 0\n" \
"v_mfma_f32_16x16x32_bf16 v[128:131], a[188:191], a[144:147], v[128:131]\n" \
"buffer_load_dwordx4 v23, s[64:67], s84 offen offset:3072 lds\n" \
"v_mfma_f32_16x16x32_bf16 v[128:131], a[252:255], a[208:211], v[128:131]\n" \
"ds_read_b128 a[144:147], v28 offset:0\n" \
"v_mfma_f32_16x16x32_bf16 v[144:147], a[188:191], a[148:151], v[144:147]\n" \
"ds_read_b128 a[208:211], v29 offset:0\n" \
"v_mfma_f32_16x16x32_bf16 v[144:147], a[252:255], a[212:215], v[144:147]\n" \
"s_add_u32 m0, s31, 0xc000\n" \
"s_mov_b32 s84, 0x380000\n" \
"v_mfma_f32_16x16x32_bf16 v[140:143], a[184:187], a[148:151], v[140:143]\n" \
"buffer_load_dwordx4 v26, s[72:75], s84 offen offset:0 lds\n" \
"v_mfma_f32_16x16x32_bf16 v[140:143], a[248:251], a[212:215], v[140:143]\n" \
"ds_read_b64_tr_b16 a[230:231], v33 offset:9216\n" \
"v_mfma_f32_16x16x32_bf16 v[136:139], a[180:183], a[148:151], v[136:139]\n" \
"s_addk_i32 s84, 0x7c00\n" \
"s_nop 0\n" \
"v_mfma_f32_16x16x32_bf16 v[136:139], a[244:247], a[212:215], v[136:139]\n" \
"buffer_load_dwordx4 v26, s[72:75], s84 offen offset:1024 lds\n" \
"v_mfma_f32_16x16x32_bf16 v[132:135], a[176:179], a[148:151], v[132:135]\n" \
"ds_read_b64_tr_b16 a[168:169], v34 offset:0\n" \
"v_mfma_f32_16x16x32_bf16 v[132:135], a[240:243], a[212:215], v[132:135]\n" \
"ds_read_b128 a[148:151], v28 offset:2048\n" \
"v_mfma_f32_16x16x32_bf16 v[148:151], a[176:179], a[152:155], v[148:151]\n" \
"ds_read_b128 a[212:215], v29 offset:2048\n" \
"v_mfma_f32_16x16x32_bf16 v[148:151], a[240:243], a[216:219], v[148:151]\n" \
"ds_read_b64_tr_b16 a[170:171], v34 offset:1024\n" \
"v_mfma_f32_16x16x32_bf16 v[152:155], a[180:183], a[152:155], v[152:155]\n" \
"ds_read_b64_tr_b16 a[232:233], v34 offset:8192\n" \
"v_mfma_f32_16x16x32_bf16 v[152:155], a[244:247], a[216:219], v[152:155]\n" \
"ds_read_b64_tr_b16 a[234:235], v34 offset:9216\n" \
"v_mfma_f32_16x16x32_bf16 v[156:159], a[184:187], a[152:155], v[156:159]\n" \
"ds_read_b64_tr_b16 a[172:173], v35 offset:0\n" \
"v_mfma_f32_16x16x32_bf16 v[156:159], a[248:251], a[216:219], v[156:159]\n" \
"ds_read_b64_tr_b16 a[174:175], v35 offset:1024\n" \
"v_mfma_f32_16x16x32_bf16 v[160:163], a[188:191], a[152:155], v[160:163]\n" \
"ds_read_b64_tr_b16 a[236:237], v35 offset:8192\n" \
"v_mfma_f32_16x16x32_bf16 v[160:163], a[252:255], a[216:219], v[160:163]\n" \
"ds_read_b128 a[152:155], v28 offset:4096\n" \
"v_mfma_f32_16x16x32_bf16 v[176:179], a[188:191], a[156:159], v[176:179]\n" \
"ds_read_b128 a[216:219], v29 offset:4096\n" \
"v_mfma_f32_16x16x32_bf16 v[176:179], a[252:255], a[220:223], v[176:179]\n" \
"ds_read_b64_tr_b16 a[238:239], v35 offset:9216\n" \
"v_mfma_f32_16x16x32_bf16 v[172:175], a[184:187], a[156:159], v[172:175]\n" \
"v_mfma_f32_16x16x32_bf16 v[172:175], a[248:251], a[220:223], v[172:175]\n" \
"v_mfma_f32_16x16x32_bf16 v[168:171], a[180:183], a[156:159], v[168:171]\n" \
"v_mfma_f32_16x16x32_bf16 v[168:171], a[244:247], a[220:223], v[168:171]\n" \
"v_mfma_f32_16x16x32_bf16 v[164:167], a[176:179], a[156:159], v[164:167]\n" \
"v_mfma_f32_16x16x32_bf16 v[164:167], a[240:243], a[220:223], v[164:167]\n" \
"ds_read_b128 a[156:159], v28 offset:6144\n" \
"ds_read_b128 a[220:223], v29 offset:6144\n" \
"s_waitcnt lgkmcnt(15)\n" \
"v_mfma_f32_16x16x32_bf16 a[0:3], a[160:163], a[128:131], a[0:3]\n" \
"s_addk_i32 s84, 0x7c00\n" \
"v_mfma_f32_16x16x32_bf16 a[0:3], a[224:227], a[192:195], a[0:3]\n" \
"buffer_load_dwordx4 v27, s[72:75], s84 offen offset:2048 lds\n" \
"v_mfma_f32_16x16x32_bf16 a[4:7], a[164:167], a[128:131], a[4:7]\n" \
"ds_read_b64_tr_b16 a[176:177], v36 offset:0\n" \
"s_waitcnt lgkmcnt(15)\n" \
"v_mfma_f32_16x16x32_bf16 a[4:7], a[228:231], a[192:195], a[4:7]\n" \
"s_addk_i32 s84, 0x7c00\n" \
"s_waitcnt lgkmcnt(11)\n" \
"v_mfma_f32_16x16x32_bf16 a[8:11], a[168:171], a[128:131], a[8:11]\n" \
"buffer_load_dwordx4 v27, s[72:75], s84 offen offset:3072 lds\n" \
"s_waitcnt lgkmcnt(9)\n" \
"v_mfma_f32_16x16x32_bf16 a[8:11], a[232:235], a[192:195], a[8:11]\n" \
"ds_read_b64_tr_b16 a[178:179], v36 offset:1024\n" \
"s_waitcnt lgkmcnt(8)\n" \
"v_mfma_f32_16x16x32_bf16 a[12:15], a[172:175], a[128:131], a[12:15]\n" \
"ds_read_b64_tr_b16 a[240:241], v36 offset:8192\n" \
"s_waitcnt lgkmcnt(5)\n" \
"v_mfma_f32_16x16x32_bf16 a[12:15], a[236:239], a[192:195], a[12:15]\n" \
"v_mfma_f32_16x16x32_bf16 a[28:31], a[172:175], a[132:135], a[28:31]\n" \
"v_mfma_f32_16x16x32_bf16 a[28:31], a[236:239], a[196:199], a[28:31]\n" \
"ds_read_b64_tr_b16 a[242:243], v36 offset:9216\n" \
"v_mfma_f32_16x16x32_bf16 a[24:27], a[168:171], a[132:135], a[24:27]\n" \
"v_mfma_f32_16x16x32_bf16 a[24:27], a[232:235], a[196:199], a[24:27]\n" \
"v_mfma_f32_16x16x32_bf16 a[20:23], a[164:167], a[132:135], a[20:23]\n" \
"v_mfma_f32_16x16x32_bf16 a[20:23], a[228:231], a[196:199], a[20:23]\n" \
"ds_read_b64_tr_b16 a[180:181], v37 offset:0\n" \
"v_mfma_f32_16x16x32_bf16 a[16:19], a[160:163], a[132:135], a[16:19]\n" \
"v_mfma_f32_16x16x32_bf16 a[16:19], a[224:227], a[196:199], a[16:19]\n" \
"v_mfma_f32_16x16x32_bf16 a[32:35], a[160:163], a[136:139], a[32:35]\n" \
"v_bfe_u32 v44, v20, 2, 5\n" \
"ds_read_b64_tr_b16 a[182:183], v37 offset:1024\n" \
"v_mfma_f32_16x16x32_bf16 a[32:35], a[224:227], a[200:203], a[32:35]\n" \
"buffer_load_dword v44, v44, s[80:83], s38 offen offset:128 sc1\n" \
"v_mfma_f32_16x16x32_bf16 a[36:39], a[164:167], a[136:139], a[36:39]\n" \
"v_mfma_f32_16x16x32_bf16 a[36:39], a[228:231], a[200:203], a[36:39]\n" \
"ds_read_b64_tr_b16 a[244:245], v37 offset:8192\n" \
"v_mfma_f32_16x16x32_bf16 a[40:43], a[168:171], a[136:139], a[40:43]\n" \
"v_mfma_f32_16x16x32_bf16 a[40:43], a[232:235], a[200:203], a[40:43]\n" \
"v_mfma_f32_16x16x32_bf16 a[44:47], a[172:175], a[136:139], a[44:47]\n" \
"ds_read_b64_tr_b16 a[246:247], v37 offset:9216\n" \
"v_mfma_f32_16x16x32_bf16 a[44:47], a[236:239], a[200:203], a[44:47]\n" \
"v_mfma_f32_16x16x32_bf16 a[60:63], a[172:175], a[140:143], a[60:63]\n" \
"global_store_dwordx4 v49, v[0:3], s[28:29] offset:0 sc1\n" \
"v_mfma_f32_16x16x32_bf16 a[60:63], a[236:239], a[204:207], a[60:63]\n" \
"v_mfma_f32_16x16x32_bf16 a[56:59], a[168:171], a[140:143], a[56:59]\n" \
"ds_read_b64_tr_b16 a[184:185], v38 offset:0\n" \
"v_mfma_f32_16x16x32_bf16 a[56:59], a[232:235], a[204:207], a[56:59]\n" \
"v_mfma_f32_16x16x32_bf16 a[52:55], a[164:167], a[140:143], a[52:55]\n" \
"v_mfma_f32_16x16x32_bf16 a[52:55], a[228:231], a[204:207], a[52:55]\n" \
"ds_read_b64_tr_b16 a[186:187], v38 offset:1024\n" \
"v_mfma_f32_16x16x32_bf16 a[48:51], a[160:163], a[140:143], a[48:51]\n" \
"v_mfma_f32_16x16x32_bf16 a[48:51], a[224:227], a[204:207], a[48:51]\n" \
"v_mfma_f32_16x16x32_bf16 a[64:67], a[160:163], a[144:147], a[64:67]\n" \
"global_store_dwordx4 v49, v[0:3], s[28:29] offset:1024 sc1\n" \
"v_mfma_f32_16x16x32_bf16 a[64:67], a[224:227], a[208:211], a[64:67]\n" \
"ds_read_b64_tr_b16 a[248:249], v38 offset:8192\n" \
"v_mfma_f32_16x16x32_bf16 a[68:71], a[164:167], a[144:147], a[68:71]\n" \
"v_mfma_f32_16x16x32_bf16 a[68:71], a[228:231], a[208:211], a[68:71]\n" \
"ds_read_b64_tr_b16 a[250:251], v38 offset:9216\n" \
"v_mfma_f32_16x16x32_bf16 a[72:75], a[168:171], a[144:147], a[72:75]\n" \
"v_mfma_f32_16x16x32_bf16 a[72:75], a[232:235], a[208:211], a[72:75]\n" \
"v_mfma_f32_16x16x32_bf16 a[76:79], a[172:175], a[144:147], a[76:79]\n" \
"v_mfma_f32_16x16x32_bf16 a[76:79], a[236:239], a[208:211], a[76:79]\n" \
"ds_read_b64_tr_b16 a[188:189], v39 offset:0\n" \
"v_mfma_f32_16x16x32_bf16 a[92:95], a[172:175], a[148:151], a[92:95]\n" \
"s_mov_b64 exec, 0xffff\n" \
"global_store_dwordx4 v49, v[0:3], s[28:29] offset:2048 sc1\n" \
"s_mov_b64 exec, -1\n" \
"s_nop 0\n" \
"v_mfma_f32_16x16x32_bf16 a[92:95], a[236:239], a[212:215], a[92:95]\n" \
"v_mfma_f32_16x16x32_bf16 a[88:91], a[168:171], a[148:151], a[88:91]\n" \
"ds_read_b64_tr_b16 a[190:191], v39 offset:1024\n" \
"v_mfma_f32_16x16x32_bf16 a[88:91], a[232:235], a[212:215], a[88:91]\n" \
"v_mfma_f32_16x16x32_bf16 a[84:87], a[164:167], a[148:151], a[84:87]\n" \
"v_mfma_f32_16x16x32_bf16 a[84:87], a[228:231], a[212:215], a[84:87]\n" \
"ds_read_b64_tr_b16 a[252:253], v39 offset:8192\n" \
"v_mfma_f32_16x16x32_bf16 a[80:83], a[160:163], a[148:151], a[80:83]\n" \
"v_mfma_f32_16x16x32_bf16 a[80:83], a[224:227], a[212:215], a[80:83]\n" \
"v_mfma_f32_16x16x32_bf16 a[96:99], a[160:163], a[152:155], a[96:99]\n" \
"ds_read_b64_tr_b16 a[254:255], v39 offset:9216\n" \
"v_mfma_f32_16x16x32_bf16 a[96:99], a[224:227], a[216:219], a[96:99]\n" \
"v_mfma_f32_16x16x32_bf16 a[100:103], a[164:167], a[152:155], a[100:103]\n" \
"v_mfma_f32_16x16x32_bf16 a[100:103], a[228:231], a[216:219], a[100:103]\n" \
"v_mfma_f32_16x16x32_bf16 a[104:107], a[168:171], a[152:155], a[104:107]\n" \
"v_mfma_f32_16x16x32_bf16 a[104:107], a[232:235], a[216:219], a[104:107]\n" \
"v_mfma_f32_16x16x32_bf16 a[108:111], a[172:175], a[152:155], a[108:111]\n" \
"v_mfma_f32_16x16x32_bf16 a[108:111], a[236:239], a[216:219], a[108:111]\n" \
"s_waitcnt lgkmcnt(15)\n" \
"v_mfma_f32_16x16x32_bf16 a[124:127], a[172:175], a[156:159], a[124:127]\n" \
"v_mfma_f32_16x16x32_bf16 a[124:127], a[236:239], a[220:223], a[124:127]\n" \
"v_mfma_f32_16x16x32_bf16 a[120:123], a[168:171], a[156:159], a[120:123]\n" \
"v_mfma_f32_16x16x32_bf16 a[120:123], a[232:235], a[220:223], a[120:123]\n" \
"v_mfma_f32_16x16x32_bf16 a[116:119], a[164:167], a[156:159], a[116:119]\n" \
"v_mfma_f32_16x16x32_bf16 a[116:119], a[228:231], a[220:223], a[116:119]\n" \
"v_mfma_f32_16x16x32_bf16 a[112:115], a[160:163], a[156:159], a[112:115]\n" \
"s_waitcnt vmcnt(3) lgkmcnt(0)\n" \
"v_mfma_f32_16x16x32_bf16 a[112:115], a[224:227], a[220:223], a[112:115]\n" \
"s_barrier\n" \
"v_cmp_ne_u32_e32 vcc, s62, v44\n" \
"s_nop 4\n" \
"s_cbranch_vccnz L_poll60_%=\n" \
"L_pollr60_%=:\n" \
"s_mov_b32 s68, s12\n" \
"s_and_b32 s69, s13, 0xffff\n" \
"s_mov_b32 s70, 0x480000\n" \
"s_mov_b32 s71, 0x20000\n" \
"v_mfma_f32_16x16x32_bf16 v[52:55], a[176:179], a[128:131], v[52:55]\n" \
"s_add_u32 m0, s40, 0x0\n" \
"s_add_u32 s85, s39, 0x0\n" \
"s_nop 0\n" \
"v_mfma_f32_16x16x32_bf16 v[52:55], a[240:243], a[192:195], v[52:55]\n" \
"buffer_load_dwordx4 v20, s[80:83], s85 offen sc1 lds\n" \
"v_mfma_f32_16x16x32_bf16 v[56:59], a[180:183], a[128:131], v[56:59]\n" \
"ds_read_b64_tr_b16 a[160:161], v32 offset:32768\n" \
"v_mfma_f32_16x16x32_bf16 v[56:59], a[244:247], a[192:195], v[56:59]\n" \
"s_add_u32 m0, s40, 0x1000\n" \
"s_add_u32 s85, s39, 0x1000\n" \
".fill (L_pb + 16096 - .) / 4, 4, 0xBF800000\n" \
"L_pp4:\n" \
"s_cbranch_execz L_pret\n" \
"s_nop 0\n" \
"v_mfma_f32_16x16x32_bf16 v[60:63], a[184:187], a[128:131], v[60:63]\n" \
"buffer_load_dwordx4 v20, s[80:83], s85 offen sc1 lds\n" \
"v_mfma_f32_16x16x32_bf16 v[60:63], a[248:251], a[192:195], v[60:63]\n" \
"ds_read_b64_tr_b16 a[162:163], v32 offset:33792\n" \
"v_mfma_f32_16x16x32_bf16 v[64:67], a[188:191], a[128:131], v[64:67]\n" \
"ds_read_b64_tr_b16 a[224:225], v32 offset:40960\n" \
"v_mfma_f32_16x16x32_bf16 v[64:67], a[252:255], a[192:195], v[64:67]\n" \
"ds_read_b128 a[128:131], v28 offset:49152\n" \
"v_mfma_f32_16x16x32_bf16 v[80:83], a[188:191], a[132:135], v[80:83]\n" \
"ds_read_b128 a[192:195], v29 offset:49152\n" \
"v_mfma_f32_16x16x32_bf16 v[80:83], a[252:255], a[196:199], v[80:83]\n" \
"s_add_u32 m0, s40, 0x3000\n" \
"s_movk_i32 s84, 0x0\n" \
"v_mfma_f32_16x16x32_bf16 v[76:79], a[184:187], a[132:135], v[76:79]\n" \
"buffer_load_dwordx4 v21, s[76:79], s84 offen offset:0 lds\n" \
"v_mfma_f32_16x16x32_bf16 v[76:79], a[248:251], a[196:199], v[76:79]\n" \
"s_add_u32 m0, s30, 0x0\n" \
"s_movk_i32 s84, 0x0\n" \
"s_nop 0\n" \
"v_mfma_f32_16x16x32_bf16 v[72:75], a[180:183], a[132:135], v[72:75]\n" \
"buffer_load_dwordx4 v24, s[76:79], s84 offen offset:0 lds\n" \
"v_mfma_f32_16x16x32_bf16 v[72:75], a[244:247], a[196:199], v[72:75]\n" \
"ds_read_b64_tr_b16 a[226:227], v32 offset:41984\n" \
"v_mfma_f32_16x16x32_bf16 v[68:71], a[176:179], a[132:135], v[68:71]\n" \
"ds_read_b64_tr_b16 a[164:165], v33 offset:32768\n" \
"v_mfma_f32_16x16x32_bf16 v[68:71], a[240:243], a[196:199], v[68:71]\n" \
"ds_read_b128 a[132:135], v28 offset:51200\n" \
"v_mfma_f32_16x16x32_bf16 v[84:87], a[176:179], a[136:139], v[84:87]\n" \
"ds_read_b128 a[196:199], v29 offset:51200\n" \
"v_mfma_f32_16x16x32_bf16 v[84:87], a[240:243], a[200:203], v[84:87]\n" \
"s_movk_i32 s84, 0x2000\n" \
"v_mfma_f32_16x16x32_bf16 v[88:91], a[180:183], a[136:139], v[88:91]\n" \
"buffer_load_dwordx4 v25, s[76:79], s84 offen offset:1024 lds\n" \
"v_mfma_f32_16x16x32_bf16 v[88:91], a[244:247], a[200:203], v[88:91]\n" \
"s_movk_i32 s84, 0x4000\n" \
"v_mfma_f32_16x16x32_bf16 v[92:95], a[184:187], a[136:139], v[92:95]\n" \
"buffer_load_dwordx4 v24, s[76:79], s84 offen offset:2048 lds\n" \
"v_mfma_f32_16x16x32_bf16 v[92:95], a[248:251], a[200:203], v[92:95]\n" \
"ds_read_b64_tr_b16 a[166:167], v33 offset:33792\n" \
"v_mfma_f32_16x16x32_bf16 v[96:99], a[188:191], a[136:139], v[96:99]\n" \
"ds_read_b64_tr_b16 a[228:229], v33 offset:40960\n" \
"v_mfma_f32_16x16x32_bf16 v[96:99], a[252:255], a[200:203], v[96:99]\n" \
"ds_read_b128 a[136:139], v28 offset:53248\n" \
"v_mfma_f32_16x16x32_bf16 v[112:115], a[188:191], a[140:143], v[112:115]\n" \
"ds_read_b128 a[200:203], v29 offset:53248\n" \
"v_mfma_f32_16x16x32_bf16 v[112:115], a[252:255], a[204:207], v[112:115]\n" \
"s_movk_i32 s84, 0x6000\n" \
"v_mfma_f32_16x16x32_bf16 v[108:111], a[184:187], a[140:143], v[108:111]\n" \
"buffer_load_dwordx4 v25, s[76:79], s84 offen offset:3072 lds\n" \
"v_mfma_f32_16x16x32_bf16 v[108:111], a[248:251], a[204:207], v[108:111]\n" \
"s_add_u32 m0, s31, 0x0\n" \
"s_movk_i32 s84, 0x0\n" \
"v_mfma_f32_16x16x32_bf16 v[104:107], a[180:183], a[140:143], v[104:107]\n" \
"buffer_load_dwordx4 v26, s[68:71], s84 offen offset:0 lds\n" \
"v_mfma_f32_16x16x32_bf16 v[104:107], a[244:247], a[204:207], v[104:107]\n" \
"s_movk_i32 s84, 0x7c00\n" \
"v_mfma_f32_16x16x32_bf16 v[100:103], a[176:179], a[140:143], v[100:103]\n" \
"buffer_load_dwordx4 v26, s[68:71], s84 offen offset:1024 lds\n" \
"v_mfma_f32_16x16x32_bf16 v[100:103], a[240:243], a[204:207], v[100:103]\n" \
"ds_read_b128 a[140:143], v28 offset:55296\n" \
"v_mfma_f32_16x16x32_bf16 v[116:119], a[176:179], a[144:147], v[116:119]\n" \
"ds_read_b128 a[204:207], v29 offset:55296\n" \
"v_mfma_f32_16x16x32_bf16 v[116:119], a[240:243], a[208:211], v[116:119]\n" \
"s_addk_i32 s84, 0x7c00\n" \
"v_mfma_f32_16x16x32_bf16 v[120:123], a[180:183], a[144:147], v[120:123]\n" \
"buffer_load_dwordx4 v27, s[68:71], s84 offen offset:2048 lds\n" \
"v_mfma_f32_16x16x32_bf16 v[120:123], a[244:247], a[208:211], v[120:123]\n" \
"ds_read_b64_tr_b16 a[230:231], v33 offset:41984\n" \
"v_mfma_f32_16x16x32_bf16 v[124:127], a[184:187], a[144:147], v[124:127]\n" \
"s_addk_i32 s84, 0x7c00\n" \
"v_mfma_f32_16x16x32_bf16 v[124:127], a[248:251], a[208:211], v[124:127]\n" \
"buffer_load_dwordx4 v27, s[68:71], s84 offen offset:3072 lds\n" \
"v_mfma_f32_16x16x32_bf16 v[128:131], a[188:191], a[144:147], v[128:131]\n" \
"ds_read_b64_tr_b16 a[168:169], v34 offset:32768\n" \
"v_mfma_f32_16x16x32_bf16 v[128:131], a[252:255], a[208:211], v[128:131]\n" \
"ds_read_b128 a[144:147], v28 offset:32768\n" \
"v_mfma_f32_16x16x32_bf16 v[144:147], a[188:191], a[148:151], v[144:147]\n" \
"ds_read_b128 a[208:211], v29 offset:32768\n" \
"v_mfma_f32_16x16x32_bf16 v[144:147], a[252:255], a[212:215], v[144:147]\n" \
"s_add_u32 m0, s30, 0x1000\n" \
"s_mov_b32 s84, 0x9000\n" \
"v_mfma_f32_16x16x32_bf16 v[140:143], a[184:187], a[148:151], v[140:143]\n" \
"buffer_load_dwordx4 v24, s[76:79], s84 offen offset:0 lds\n" \
"v_mfma_f32_16x16x32_bf16 v[140:143], a[248:251], a[212:215], v[140:143]\n" \
"ds_read_b64_tr_b16 a[170:171], v34 offset:33792\n" \
"v_mfma_f32_16x16x32_bf16 v[136:139], a[180:183], a[148:151], v[136:139]\n" \
"s_addk_i32 s84, 0x2000\n" \
"s_nop 0\n" \
"v_mfma_f32_16x16x32_bf16 v[136:139], a[244:247], a[212:215], v[136:139]\n" \
"buffer_load_dwordx4 v25, s[76:79], s84 offen offset:1024 lds\n" \
"v_mfma_f32_16x16x32_bf16 v[132:135], a[176:179], a[148:151], v[132:135]\n" \
"ds_read_b64_tr_b16 a[232:233], v34 offset:40960\n" \
"v_mfma_f32_16x16x32_bf16 v[132:135], a[240:243], a[212:215], v[132:135]\n" \
"ds_read_b128 a[148:151], v28 offset:34816\n" \
"v_mfma_f32_16x16x32_bf16 v[148:151], a[176:179], a[152:155], v[148:151]\n" \
"ds_read_b128 a[212:215], v29 offset:34816\n" \
"v_mfma_f32_16x16x32_bf16 v[148:151], a[240:243], a[216:219], v[148:151]\n" \
"ds_read_b64_tr_b16 a[234:235], v34 offset:41984\n" \
"v_mfma_f32_16x16x32_bf16 v[152:155], a[180:183], a[152:155], v[152:155]\n" \
"ds_read_b64_tr_b16 a[172:173], v35 offset:32768\n" \
"v_mfma_f32_16x16x32_bf16 v[152:155], a[244:247], a[216:219], v[152:155]\n" \
"ds_read_b64_tr_b16 a[174:175], v35 offset:33792\n" \
"v_mfma_f32_16x16x32_bf16 v[156:159], a[184:187], a[152:155], v[156:159]\n" \
"ds_read_b64_tr_b16 a[236:237], v35 offset:40960\n" \
"v_mfma_f32_16x16x32_bf16 v[156:159], a[248:251], a[216:219], v[156:159]\n" \
"ds_read_b64_tr_b16 a[238:239], v35 offset:41984\n" \
"v_mfma_f32_16x16x32_bf16 v[160:163], a[188:191], a[152:155], v[160:163]\n" \
"v_mfma_f32_16x16x32_bf16 v[160:163], a[252:255], a[216:219], v[160:163]\n" \
"ds_read_b128 a[152:155], v28 offset:36864\n" \
"v_mfma_f32_16x16x32_bf16 v[176:179], a[188:191], a[156:159], v[176:179]\n" \
"ds_read_b128 a[216:219], v29 offset:36864\n" \
"v_mfma_f32_16x16x32_bf16 v[176:179], a[252:255], a[220:223], v[176:179]\n" \
"v_mfma_f32_16x16x32_bf16 v[172:175], a[184:187], a[156:159], v[172:175]\n" \
"v_mfma_f32_16x16x32_bf16 v[172:175], a[248:251], a[220:223], v[172:175]\n" \
"v_mfma_f32_16x16x32_bf16 v[168:171], a[180:183], a[156:159], v[168:171]\n" \
"v_mfma_f32_16x16x32_bf16 v[168:171], a[244:247], a[220:223], v[168:171]\n" \
"v_mfma_f32_16x16x32_bf16 v[164:167], a[176:179], a[156:159], v[164:167]\n" \
"v_mfma_f32_16x16x32_bf16 v[164:167], a[240:243], a[220:223], v[164:167]\n" \
"ds_read_b128 a[156:159], v28 offset:38912\n" \
"ds_read_b128 a[220:223], v29 offset:38912\n" \
"s_waitcnt lgkmcnt(15)\n" \
"v_mfma_f32_16x16x32_bf16 a[0:3], a[160:163], a[128:131], a[0:3]\n" \
"s_addk_i32 s84, 0x2000\n" \
"v_mfma_f32_16x16x32_bf16 a[0:3], a[224:227], a[192:195], a[0:3]\n" \
"buffer_load_dwordx4 v24, s[76:79], s84 offen offset:2048 lds\n" \
"v_mfma_f32_16x16x32_bf16 a[4:7], a[164:167], a[128:131], a[4:7]\n" \
"ds_read_b64_tr_b16 a[176:177], v36 offset:32768\n" \
"v_mfma_f32_16x16x32_bf16 a[4:7], a[228:231], a[192:195], a[4:7]\n" \
"s_addk_i32 s84, 0x2000\n" \
"s_waitcnt lgkmcnt(13)\n" \
"v_mfma_f32_16x16x32_bf16 a[8:11], a[168:171], a[128:131], a[8:11]\n" \
"buffer_load_dwordx4 v25, s[76:79], s84 offen offset:3072 lds\n" \
"s_waitcnt lgkmcnt(9)\n" \
"v_mfma_f32_16x16x32_bf16 a[8:11], a[232:235], a[192:195], a[8:11]\n" \
"ds_read_b64_tr_b16 a[178:179], v36 offset:33792\n" \
"s_waitcnt lgkmcnt(8)\n" \
"v_mfma_f32_16x16x32_bf16 a[12:15], a[172:175], a[128:131], a[12:15]\n" \
"ds_read_b64_tr_b16 a[240:241], v36 offset:40960\n" \
"v_accvgpr_read_b32 v184, a0\n" \
"s_waitcnt lgkmcnt(7)\n" \
"v_mfma_f32_16x16x32_bf16 a[12:15], a[236:239], a[192:195], a[12:15]\n" \
"v_accvgpr_read_b32 v185, a1\n" \
"v_accvgpr_read_b32 v186, a2\n" \
"v_mfma_f32_16x16x32_bf16 a[28:31], a[172:175], a[132:135], a[28:31]\n" \
"v_accvgpr_read_b32 v187, a3\n" \
"v_accvgpr_read_b32 v180, a4\n" \
"v_mfma_f32_16x16x32_bf16 a[28:31], a[236:239], a[196:199], a[28:31]\n" \
"s_add_u32 m0, s30, 0x20000\n" \
"s_movk_i32 s84, 0x100\n" \
"v_mfma_f32_16x16x32_bf16 a[24:27], a[168:171], a[132:135], a[24:27]\n" \
"buffer_load_dwordx4 v24, s[76:79], s84 offen offset:0 lds\n" \
"v_mfma_f32_16x16x32_bf16 a[24:27], a[232:235], a[196:199], a[24:27]\n" \
"ds_read_b64_tr_b16 a[242:243], v36 offset:41984\n" \
"v_accvgpr_read_b32 v181, a5\n" \
"v_mfma_f32_16x16x32_bf16 a[20:23], a[164:167], a[132:135], a[20:23]\n" \
"v_accvgpr_read_b32 v182, a6\n" \
"v_accvgpr_read_b32 v183, a7\n" \
"v_mfma_f32_16x16x32_bf16 a[20:23], a[228:231], a[196:199], a[20:23]\n" \
"ds_read_b64_tr_b16 a[180:181], v37 offset:32768\n" \
"v_cvt_pk_bf16_f32 v184, v184, v185\n" \
"v_mfma_f32_16x16x32_bf16 a[16:19], a[160:163], a[132:135], a[16:19]\n" \
"s_movk_i32 s84, 0x2100\n" \
"s_nop 0\n" \
"v_mfma_f32_16x16x32_bf16 a[16:19], a[224:227], a[196:199], a[16:19]\n" \
"buffer_load_dwordx4 v25, s[76:79], s84 offen offset:1024 lds\n" \
"v_mfma_f32_16x16x32_bf16 a[32:35], a[160:163], a[136:139], a[32:35]\n" \
"ds_read_b64_tr_b16 a[182:183], v37 offset:33792\n" \
"v_cvt_pk_bf16_f32 v185, v186, v187\n" \
"v_mfma_f32_16x16x32_bf16 a[32:35], a[224:227], a[200:203], a[32:35]\n" \
"v_cvt_pk_bf16_f32 v186, v180, v181\n" \
"v_cvt_pk_bf16_f32 v187, v182, v183\n" \
"v_mfma_f32_16x16x32_bf16 a[36:39], a[164:167], a[136:139], a[36:39]\n" \
"v_accvgpr_read_b32 v188, a8\n" \
"v_accvgpr_read_b32 v189, a9\n" \
"v_mfma_f32_16x16x32_bf16 a[36:39], a[228:231], a[200:203], a[36:39]\n" \
"s_movk_i32 s84, 0x4100\n" \
"s_nop 0\n" \
"v_mfma_f32_16x16x32_bf16 a[40:43], a[168:171], a[136:139], a[40:43]\n" \
"buffer_load_dwordx4 v24, s[76:79], s84 offen offset:2048 lds\n" \
"v_mfma_f32_16x16x32_bf16 a[40:43], a[232:235], a[200:203], a[40:43]\n" \
"ds_read_b64_tr_b16 a[244:245], v37 offset:40960\n" \
"v_accvgpr_read_b32 v190, a10\n" \
"v_mfma_f32_16x16x32_bf16 a[44:47], a[172:175], a[136:139], a[44:47]\n" \
"ds_read_b64_tr_b16 a[246:247], v37 offset:41984\n" \
"v_accvgpr_read_b32 v191, a11\n" \
"v_mfma_f32_16x16x32_bf16 a[44:47], a[236:239], a[200:203], a[44:47]\n" \
"v_accvgpr_read_b32 v180, a12\n" \
"v_accvgpr_read_b32 v181, a13\n" \
"v_mfma_f32_16x16x32_bf16 a[60:63], a[172:175], a[140:143], a[60:63]\n" \
"s_movk_i32 s84, 0x6100\n" \
"v_mfma_f32_16x16x32_bf16 a[60:63], a[236:239], a[204:207], a[60:63]\n" \
"buffer_load_dwordx4 v25, s[76:79], s84 offen offset:3072 lds\n" \
"v_mfma_f32_16x16x32_bf16 a[56:59], a[168:171], a[140:143], a[56:59]\n" \
"ds_read_b64_tr_b16 a[184:185], v38 offset:32768\n" \
"v_accvgpr_read_b32 v182, a14\n" \
"v_mfma_f32_16x16x32_bf16 a[56:59], a[232:235], a[204:207], a[56:59]\n" \
"v_accvgpr_read_b32 v183, a15\n" \
"v_cvt_pk_bf16_f32 v188, v188, v189\n" \
"v_mfma_f32_16x16x32_bf16 a[52:55], a[164:167], a[140:143], a[52:55]\n" \
"s_add_u32 m0, s30, 0x21000\n" \
"s_addk_i32 s84, 0x3000\n" \
"v_mfma_f32_16x16x32_bf16 a[52:55], a[228:231], a[204:207], a[52:55]\n" \
"buffer_load_dwordx4 v24, s[76:79], s84 offen offset:0 lds\n" \
"v_mfma_f32_16x16x32_bf16 a[48:51], a[160:163], a[140:143], a[48:51]\n" \
"ds_read_b64_tr_b16 a[186:187], v38 offset:33792\n" \
"v_cvt_pk_bf16_f32 v189, v190, v191\n" \
"v_mfma_f32_16x16x32_bf16 a[48:51], a[224:227], a[204:207], a[48:51]\n" \
"v_cvt_pk_bf16_f32 v190, v180, v181\n" \
"v_cvt_pk_bf16_f32 v191, v182, v183\n" \
"v_mfma_f32_16x16x32_bf16 a[64:67], a[160:163], a[144:147], a[64:67]\n" \
"ds_read_b64_tr_b16 a[248:249], v38 offset:40960\n" \
"v_accvgpr_read_b32 v192, a24\n" \
"v_mfma_f32_16x16x32_bf16 a[64:67], a[224:227], a[208:211], a[64:67]\n" \
"s_addk_i32 s84, 0x2000\n" \
"s_nop 0\n" \
"v_mfma_f32_16x16x32_bf16 a[68:71], a[164:167], a[144:147], a[68:71]\n" \
"buffer_load_dwordx4 v25, s[76:79], s84 offen offset:1024 lds\n" \
"v_mfma_f32_16x16x32_bf16 a[68:71], a[228:231], a[208:211], a[68:71]\n" \
"ds_read_b64_tr_b16 a[250:251], v38 offset:41984\n" \
"v_accvgpr_read_b32 v193, a25\n" \
"v_mfma_f32_16x16x32_bf16 a[72:75], a[168:171], a[144:147], a[72:75]\n" \
"v_accvgpr_read_b32 v194, a26\n" \
"v_accvgpr_read_b32 v195, a27\n" \
"v_mfma_f32_16x16x32_bf16 a[72:75], a[232:235], a[208:211], a[72:75]\n" \
"v_accvgpr_read_b32 v180, a28\n" \
"v_accvgpr_read_b32 v181, a29\n" \
"v_mfma_f32_16x16x32_bf16 a[76:79], a[172:175], a[144:147], a[76:79]\n" \
"s_addk_i32 s84, 0x2000\n" \
"v_mfma_f32_16x16x32_bf16 a[76:79], a[236:239], a[208:211], a[76:79]\n" \
"buffer_load_dwordx4 v24, s[76:79], s84 offen offset:2048 lds\n" \
"v_mfma_f32_16x16x32_bf16 a[92:95], a[172:175], a[148:151], a[92:95]\n" \
"s_add_u32 s89, s41, 0x0\n" \
"buffer_store_dwordx4 v[184:187], v20, s[80:83], s89 offen offset:0 sc1\n" \
"v_mfma_f32_16x16x32_bf16 a[92:95], a[236:239], a[212:215], a[92:95]\n" \
"ds_read_b64_tr_b16 a[188:189], v39 offset:32768\n" \
"v_accvgpr_read_b32 v182, a30\n" \
"v_mfma_f32_16x16x32_bf16 a[88:91], a[168:171], a[148:151], a[88:91]\n" \
"ds_read_b64_tr_b16 a[190:191], v39 offset:33792\n" \
"v_accvgpr_read_b32 v183, a31\n" \
"v_mfma_f32_16x16x32_bf16 a[88:91], a[232:235], a[212:215], a[88:91]\n" \
"s_addk_i32 s84, 0x2000\n" \
"v_mfma_f32_16x16x32_bf16 a[84:87], a[164:167], a[148:151], a[84:87]\n" \
"buffer_load_dwordx4 v25, s[76:79], s84 offen offset:3072 lds\n" \
"v_mfma_f32_16x16x32_bf16 a[84:87], a[228:231], a[212:215], a[84:87]\n" \
"ds_read_b64_tr_b16 a[252:253], v39 offset:40960\n" \
"v_cvt_pk_bf16_f32 v192, v192, v193\n" \
"v_mfma_f32_16x16x32_bf16 a[80:83], a[160:163], a[148:151], a[80:83]\n" \
"s_add_u32 s88, s41, 0x0\n" \
"buffer_store_dwordx4 v[188:191], v20, s[80:83], s88 offen offset:1024 sc1\n" \
"v_mfma_f32_16x16x32_bf16 a[80:83], a[224:227], a[212:215], a[80:83]\n" \
"v_cvt_pk_bf16_f32 v193, v194, v195\n" \
"v_cvt_pk_bf16_f32 v194, v180, v181\n" \
"s_waitcnt lgkmcnt(15)\n" \
"s_nop 0\n" \
"v_mfma_f32_16x16x32_bf16 a[96:99], a[160:163], a[152:155], a[96:99]\n" \
"ds_read_b64_tr_b16 a[254:255], v39 offset:41984\n" \
"v_cvt_pk_bf16_f32 v195, v182, v183\n" \
"v_mfma_f32_16x16x32_bf16 a[96:99], a[224:227], a[216:219], a[96:99]\n" \
"v_accvgpr_read_b32 v196, a16\n" \
"v_accvgpr_read_b32 v197, a17\n" \
"v_mfma_f32_16x16x32_bf16 a[100:103], a[164:167], a[152:155], a[100:103]\n" \
"v_accvgpr_read_b32 v198, a18\n" \
"v_accvgpr_read_b32 v199, a19\n" \
"v_mfma_f32_16x16x32_bf16 a[100:103], a[228:231], a[216:219], a[100:103]\n" \
"v_accvgpr_read_b32 v180, a20\n" \
"v_accvgpr_read_b32 v181, a21\n" \
"v_mfma_f32_16x16x32_bf16 a[104:107], a[168:171], a[152:155], a[104:107]\n" \
"s_add_u32 s89, s41, 0x1000\n" \
"buffer_store_dwordx4 v[192:195], v20, s[80:83], s89 offen offset:1024 sc1\n" \
"v_mfma_f32_16x16x32_bf16 a[104:107], a[232:235], a[216:219], a[104:107]\n" \
"v_accvgpr_read_b32 v182, a22\n" \
"v_accvgpr_read_b32 v183, a23\n" \
"v_mfma_f32_16x16x32_bf16 a[108:111], a[172:175], a[152:155], a[108:111]\n" \
"v_cvt_pk_bf16_f32 v196, v196, v197\n" \
"v_cvt_pk_bf16_f32 v197, v198, v199\n" \
"v_mfma_f32_16x16x32_bf16 a[108:111], a[236:239], a[216:219], a[108:111]\n" \
"v_cvt_pk_bf16_f32 v198, v180, v181\n" \
"v_cvt_pk_bf16_f32 v199, v182, v183\n" \
"v_mfma_f32_16x16x32_bf16 a[124:127], a[172:175], a[156:159], a[124:127]\n" \
"v_accvgpr_read_b32 v200, a32\n" \
"v_accvgpr_read_b32 v201, a33\n" \
"v_mfma_f32_16x16x32_bf16 a[124:127], a[236:239], a[220:223], a[124:127]\n" \
"v_accvgpr_read_b32 v202, a34\n" \
"v_accvgpr_read_b32 v203, a35\n" \
"v_mfma_f32_16x16x32_bf16 a[120:123], a[168:171], a[156:159], a[120:123]\n" \
"s_add_u32 s88, s41, 0x1000\n" \
"buffer_store_dwordx4 v[196:199], v20, s[80:83], s88 offen offset:0 sc1\n" \
"v_mfma_f32_16x16x32_bf16 a[120:123], a[232:235], a[220:223], a[120:123]\n" \
"v_accvgpr_read_b32 v180, a36\n" \
"v_accvgpr_read_b32 v181, a37\n" \
"v_mfma_f32_16x16x32_bf16 a[116:119], a[164:167], a[156:159], a[116:119]\n" \
"v_accvgpr_read_b32 v182, a38\n" \
"v_accvgpr_read_b32 v183, a39\n" \
"v_mfma_f32_16x16x32_bf16 a[116:119], a[228:231], a[220:223], a[116:119]\n" \
"v_cvt_pk_bf16_f32 v200, v200, v201\n" \
"v_cvt_pk_bf16_f32 v201, v202, v203\n" \
"v_mfma_f32_16x16x32_bf16 a[112:115], a[160:163], a[156:159], a[112:115]\n" \
"v_cvt_pk_bf16_f32 v202, v180, v181\n" \
"v_cvt_pk_bf16_f32 v203, v182, v183\n" \
"v_mfma_f32_16x16x32_bf16 a[112:115], a[224:227], a[220:223], a[112:115]\n" \
"v_accvgpr_read_b32 v204, a40\n" \
"v_accvgpr_read_b32 v205, a41\n" \
"s_waitcnt vmcnt(12) lgkmcnt(0)\n" \
"s_barrier\n" \
"v_mov_b32_e32 v36, v32\n" \
"v_xor_b32_e32 v32, 0x0, v48\n" \
"v_mov_b32_e32 v37, v33\n" \
"v_xor_b32_e32 v33, 0x20, v48\n" \
"v_mov_b32_e32 v38, v34\n" \
"v_xor_b32_e32 v34, 0x40, v48\n" \
"v_mov_b32_e64 v39, v35\n" \
"v_xor_b32_e32 v35, 0x60, v48\n" \
"v_add_u32_e32 v22, s44, v28\n" \
"v_add_u32_e32 v28, s43, v28\n" \
"v_add_u32_e32 v23, s44, v29\n" \
"v_add_u32_e32 v29, s43, v29\n" \
"v_mfma_f32_16x16x32_bf16 v[52:55], a[176:179], a[128:131], v[52:55]\n" \
"s_add_u32 m0, s40, 0x8000\n" \
"s_add_u32 s85, s39, 0x2000\n" \
"v_mfma_f32_16x16x32_bf16 v[52:55], a[240:243], a[192:195], v[52:55]\n" \
"buffer_load_dwordx4 v20, s[80:83], s85 offen sc1 lds\n" \
"v_mfma_f32_16x16x32_bf16 v[56:59], a[180:183], a[128:131], v[56:59]\n" \
"ds_read_b64_tr_b16 a[160:161], v32 offset:0\n" \
"v_accvgpr_read_b32 v206, a42\n" \
"v_mfma_f32_16x16x32_bf16 v[56:59], a[244:247], a[192:195], v[56:59]\n" \
"s_add_u32 m0, s40, 0x9000\n" \
"s_add_u32 s85, s39, 0x3000\n" \
"v_mfma_f32_16x16x32_bf16 v[60:63], a[184:187], a[128:131], v[60:63]\n" \
"buffer_load_dwordx4 v20, s[80:83], s85 offen sc1 lds\n" \
"v_mfma_f32_16x16x32_bf16 v[60:63], a[248:251], a[192:195], v[60:63]\n" \
"ds_read_b64_tr_b16 a[162:163], v32 offset:512\n" \
"v_accvgpr_read_b32 v207, a43\n" \
"v_mfma_f32_16x16x32_bf16 v[64:67], a[188:191], a[128:131], v[64:67]\n" \
"ds_read_b64_tr_b16 a[224:225], v32 offset:4096\n" \
"v_accvgpr_read_b32 v180, a44\n" \
"v_mfma_f32_16x16x32_bf16 v[64:67], a[252:255], a[192:195], v[64:67]\n" \
"ds_read_b128 a[128:131], v28 offset:0\n" \
"v_accvgpr_read_b32 v181, a45\n" \
"v_mfma_f32_16x16x32_bf16 v[80:83], a[188:191], a[132:135], v[80:83]\n" \
"ds_read_b128 a[192:195], v29 offset:0\n" \
"v_accvgpr_read_b32 v182, a46\n" \
"v_mfma_f32_16x16x32_bf16 v[80:83], a[252:255], a[196:199], v[80:83]\n" \
"s_add_u32 m0, s40, 0xb000\n" \
"s_movk_i32 s84, 0x80\n" \
"v_mfma_f32_16x16x32_bf16 v[76:79], a[184:187], a[132:135], v[76:79]\n" \
"buffer_load_dwordx4 v21, s[76:79], s84 offen offset:0 lds\n" \
"v_mfma_f32_16x16x32_bf16 v[76:79], a[248:251], a[196:199], v[76:79]\n" \
"s_add_u32 m0, s30, 0x8000\n" \
"s_movk_i32 s84, 0x80\n" \
"v_mfma_f32_16x16x32_bf16 v[72:75], a[180:183], a[132:135], v[72:75]\n" \
"buffer_load_dwordx4 v24, s[76:79], s84 offen offset:0 lds\n" \
"v_mfma_f32_16x16x32_bf16 v[72:75], a[244:247], a[196:199], v[72:75]\n" \
"ds_read_b64_tr_b16 a[226:227], v32 offset:4608\n" \
"v_accvgpr_read_b32 v183, a47\n" \
"v_mfma_f32_16x16x32_bf16 v[68:71], a[176:179], a[132:135], v[68:71]\n" \
"ds_read_b64_tr_b16 a[164:165], v33 offset:0\n" \
"v_cvt_pk_bf16_f32 v204, v204, v205\n" \
"v_mfma_f32_16x16x32_bf16 v[68:71], a[240:243], a[196:199], v[68:71]\n" \
"ds_read_b128 a[132:135], v28 offset:2048\n" \
"v_cvt_pk_bf16_f32 v205, v206, v207\n" \
"v_mfma_f32_16x16x32_bf16 v[84:87], a[176:179], a[136:139], v[84:87]\n" \
"ds_read_b128 a[196:199], v29 offset:2048\n" \
"v_cvt_pk_bf16_f32 v206, v180, v181\n" \
"v_mfma_f32_16x16x32_bf16 v[84:87], a[240:243], a[200:203], v[84:87]\n" \
"s_movk_i32 s84, 0x2080\n" \
"v_mfma_f32_16x16x32_bf16 v[88:91], a[180:183], a[136:139], v[88:91]\n" \
"buffer_load_dwordx4 v25, s[76:79], s84 offen offset:1024 lds\n" \
"v_mfma_f32_16x16x32_bf16 v[88:91], a[244:247], a[200:203], v[88:91]\n" \
"s_movk_i32 s84, 0x4080\n" \
"v_mfma_f32_16x16x32_bf16 v[92:95], a[184:187], a[136:139], v[92:95]\n" \
"buffer_load_dwordx4 v24, s[76:79], s84 offen offset:2048 lds\n" \
"v_mfma_f32_16x16x32_bf16 v[92:95], a[248:251], a[200:203], v[92:95]\n" \
"ds_read_b64_tr_b16 a[166:167], v33 offset:512\n" \
"v_cvt_pk_bf16_f32 v207, v182, v183\n" \
"v_mfma_f32_16x16x32_bf16 v[96:99], a[188:191], a[136:139], v[96:99]\n" \
"ds_read_b64_tr_b16 a[228:229], v33 offset:4096\n" \
"v_accvgpr_read_b32 v208, a56\n" \
"v_mfma_f32_16x16x32_bf16 v[96:99], a[252:255], a[200:203], v[96:99]\n" \
"ds_read_b128 a[136:139], v28 offset:4096\n" \
"v_accvgpr_read_b32 v209, a57\n" \
"v_mfma_f32_16x16x32_bf16 v[112:115], a[188:191], a[140:143], v[112:115]\n" \
"ds_read_b128 a[200:203], v29 offset:4096\n" \
"v_accvgpr_read_b32 v210, a58\n" \
"v_mfma_f32_16x16x32_bf16 v[112:115], a[252:255], a[204:207], v[112:115]\n" \
"s_movk_i32 s84, 0x6080\n" \
"v_mfma_f32_16x16x32_bf16 v[108:111], a[184:187], a[140:143], v[108:111]\n" \
"buffer_load_dwordx4 v25, s[76:79], s84 offen offset:3072 lds\n" \
"v_mfma_f32_16x16x32_bf16 v[108:111], a[248:251], a[204:207], v[108:111]\n" \
"s_add_u32 m0, s31, 0x8000\n" \
"s_mov_b32 s84, 0x80000\n" \
"v_mfma_f32_16x16x32_bf16 v[104:107], a[180:183], a[140:143], v[104:107]\n" \
"buffer_load_dwordx4 v26, s[68:71], s84 offen offset:0 lds\n" \
"v_mfma_f32_16x16x32_bf16 v[104:107], a[244:247], a[204:207], v[104:107]\n" \
"s_addk_i32 s84, 0x7c00\n" \
"v_mfma_f32_16x16x32_bf16 v[100:103], a[176:179], a[140:143], v[100:103]\n" \
"buffer_load_dwordx4 v26, s[68:71], s84 offen offset:1024 lds\n" \
"v_mfma_f32_16x16x32_bf16 v[100:103], a[240:243], a[204:207], v[100:103]\n" \
"ds_read_b128 a[140:143], v28 offset:6144\n" \
"v_accvgpr_read_b32 v211, a59\n" \
"v_mfma_f32_16x16x32_bf16 v[116:119], a[176:179], a[144:147], v[116:119]\n" \
"ds_read_b128 a[204:207], v29 offset:6144\n" \
"v_accvgpr_read_b32 v180, a60\n" \
"v_mfma_f32_16x16x32_bf16 v[116:119], a[240:243], a[208:211], v[116:119]\n" \
"s_addk_i32 s84, 0x7c00\n" \
"v_mfma_f32_16x16x32_bf16 v[120:123], a[180:183], a[144:147], v[120:123]\n" \
"buffer_load_dwordx4 v27, s[68:71], s84 offen offset:2048 lds\n" \
"v_mfma_f32_16x16x32_bf16 v[120:123], a[244:247], a[208:211], v[120:123]\n" \
"ds_read_b64_tr_b16 a[230:231], v33 offset:4608\n" \
"v_accvgpr_read_b32 v181, a61\n" \
"v_mfma_f32_16x16x32_bf16 v[124:127], a[184:187], a[144:147], v[124:127]\n" \
"s_addk_i32 s84, 0x7c00\n" \
"v_mfma_f32_16x16x32_bf16 v[124:127], a[248:251], a[208:211], v[124:127]\n" \
"buffer_load_dwordx4 v27, s[68:71], s84 offen offset:3072 lds\n" \
"v_mfma_f32_16x16x32_bf16 v[128:131], a[188:191], a[144:147], v[128:131]\n" \
"ds_read_b64_tr_b16 a[172:173], v35 offset:0\n" \
"v_accvgpr_read_b32 v182, a62\n" \
"v_mfma_f32_16x16x32_bf16 v[128:131], a[252:255], a[208:211], v[128:131]\n" \
"ds_read_b128 a[144:147], v28 offset:8192\n" \
"v_accvgpr_read_b32 v183, a63\n" \
"v_mfma_f32_16x16x32_bf16 v[144:147], a[188:191], a[148:151], v[144:147]\n" \
"ds_read_b128 a[208:211], v29 offset:8192\n" \
"v_cvt_pk_bf16_f32 v208, v208, v209\n" \
"v_mfma_f32_16x16x32_bf16 v[144:147], a[252:255], a[212:215], v[144:147]\n" \
"s_add_u32 m0, s30, 0x9000\n" \
"s_mov_b32 s84, 0x9080\n" \
"v_mfma_f32_16x16x32_bf16 v[140:143], a[184:187], a[148:151], v[140:143]\n" \
"buffer_load_dwordx4 v24, s[76:79], s84 offen offset:0 lds\n" \
"v_mfma_f32_16x16x32_bf16 v[140:143], a[248:251], a[212:215], v[140:143]\n" \
"ds_read_b64_tr_b16 a[174:175], v35 offset:512\n" \
"v_cvt_pk_bf16_f32 v209, v210, v211\n" \
"v_mfma_f32_16x16x32_bf16 v[136:139], a[180:183], a[148:151], v[136:139]\n" \
"s_addk_i32 s84, 0x2000\n" \
"s_nop 0\n" \
"v_mfma_f32_16x16x32_bf16 v[136:139], a[244:247], a[212:215], v[136:139]\n" \
"buffer_load_dwordx4 v25, s[76:79], s84 offen offset:1024 lds\n" \
"v_mfma_f32_16x16x32_bf16 v[132:135], a[176:179], a[148:151], v[132:135]\n" \
"ds_read_b64_tr_b16 a[236:237], v35 offset:4096\n" \
"v_cvt_pk_bf16_f32 v210, v180, v181\n" \
"v_mfma_f32_16x16x32_bf16 v[132:135], a[240:243], a[212:215], v[132:135]\n" \
"ds_read_b128 a[148:151], v28 offset:10240\n" \
"v_cvt_pk_bf16_f32 v211, v182, v183\n" \
"v_mfma_f32_16x16x32_bf16 v[148:151], a[176:179], a[152:155], v[148:151]\n" \
"ds_read_b128 a[212:215], v29 offset:10240\n" \
"v_accvgpr_read_b32 v212, a48\n" \
"v_mfma_f32_16x16x32_bf16 v[148:151], a[240:243], a[216:219], v[148:151]\n" \
"ds_read_b64_tr_b16 a[238:239], v35 offset:4608\n" \
"v_accvgpr_read_b32 v213, a49\n" \
"v_mfma_f32_16x16x32_bf16 v[152:155], a[180:183], a[152:155], v[152:155]\n" \
"ds_read_b64_tr_b16 a[168:169], v34 offset:0\n" \
"v_accvgpr_read_b32 v214, a50\n" \
"v_mfma_f32_16x16x32_bf16 v[152:155], a[244:247], a[216:219], v[152:155]\n" \
"ds_read_b64_tr_b16 a[170:171], v34 offset:512\n" \
"v_accvgpr_read_b32 v215, a51\n" \
"v_mfma_f32_16x16x32_bf16 v[156:159], a[184:187], a[152:155], v[156:159]\n" \
"ds_read_b64_tr_b16 a[232:233], v34 offset:4096\n" \
"v_accvgpr_read_b32 v180, a52\n" \
"v_mfma_f32_16x16x32_bf16 v[156:159], a[248:251], a[216:219], v[156:159]\n" \
"ds_read_b64_tr_b16 a[234:235], v34 offset:4608\n" \
"v_accvgpr_read_b32 v181, a53\n" \
"v_mfma_f32_16x16x32_bf16 v[160:163], a[188:191], a[152:155], v[160:163]\n" \
"ds_read_b128 v[248:251], v22 offset:0\n" \
"v_accvgpr_read_b32 v182, a54\n" \
"v_mfma_f32_16x16x32_bf16 v[160:163], a[252:255], a[216:219], v[160:163]\n" \
"ds_read_b128 a[152:155], v28 offset:12288\n" \
"v_accvgpr_read_b32 v183, a55\n" \
"v_mfma_f32_16x16x32_bf16 v[176:179], a[188:191], a[156:159], v[176:179]\n" \
"ds_read_b128 a[216:219], v29 offset:12288\n" \
"v_cvt_pk_bf16_f32 v212, v212, v213\n" \
"v_mfma_f32_16x16x32_bf16 v[176:179], a[252:255], a[220:223], v[176:179]\n" \
"ds_read_b128 v[48:51], v23 offset:0\n" \
"v_cvt_pk_bf16_f32 v213, v214, v215\n" \
"v_mfma_f32_16x16x32_bf16 v[172:175], a[184:187], a[156:159], v[172:175]\n" \
"v_cvt_pk_bf16_f32 v214, v180, v181\n" \
"v_cvt_pk_bf16_f32 v215, v182, v183\n" \
"v_mfma_f32_16x16x32_bf16 v[172:175], a[248:251], a[220:223], v[172:175]\n" \
"v_accvgpr_read_b32 v216, a64\n" \
"v_accvgpr_read_b32 v217, a65\n" \
"v_mfma_f32_16x16x32_bf16 v[168:171], a[180:183], a[156:159], v[168:171]\n" \
"v_accvgpr_read_b32 v218, a66\n" \
"v_accvgpr_read_b32 v219, a67\n" \
"v_mfma_f32_16x16x32_bf16 v[168:171], a[244:247], a[220:223], v[168:171]\n" \
"v_accvgpr_read_b32 v180, a68\n" \
"v_accvgpr_read_b32 v181, a69\n" \
"v_mfma_f32_16x16x32_bf16 v[164:167], a[176:179], a[156:159], v[164:167]\n" \
"v_accvgpr_read_b32 v182, a70\n" \
"v_accvgpr_read_b32 v183, a71\n" \
"v_mfma_f32_16x16x32_bf16 v[164:167], a[240:243], a[220:223], v[164:167]\n" \
"ds_read_b128 a[156:159], v28 offset:14336\n" \
"ds_read_b128 a[220:223], v29 offset:14336\n" \
"s_waitcnt lgkmcnt(15)\n" \
"v_mfma_f32_16x16x32_bf16 a[0:3], a[160:163], a[128:131], 0\n" \
"s_addk_i32 s84, 0x2000\n" \
"v_cvt_pk_bf16_f32 v216, v216, v217\n" \
"v_mfma_f32_16x16x32_bf16 a[0:3], a[224:227], a[192:195], a[0:3]\n" \
"buffer_load_dwordx4 v24, s[76:79], s84 offen offset:2048 lds\n" \
"v_cvt_pk_bf16_f32 v217, v218, v219\n" \
"v_mfma_f32_16x16x32_bf16 a[4:7], a[164:167], a[128:131], 0\n" \
"ds_read_b64_tr_b16 a[176:177], v36 offset:0\n" \
"v_cvt_pk_bf16_f32 v218, v180, v181\n" \
"v_mfma_f32_16x16x32_bf16 a[4:7], a[228:231], a[192:195], a[4:7]\n" \
"s_addk_i32 s84, 0x2000\n" \
"v_cvt_pk_bf16_f32 v219, v182, v183\n" \
"s_waitcnt lgkmcnt(15)\n" \
"v_mfma_f32_16x16x32_bf16 a[12:15], a[172:175], a[128:131], 0\n" \
"buffer_load_dwordx4 v25, s[76:79], s84 offen offset:3072 lds\n" \
"v_accvgpr_read_b32 v220, a80\n" \
"s_waitcnt lgkmcnt(11)\n" \
"v_mfma_f32_16x16x32_bf16 a[12:15], a[236:239], a[192:195], a[12:15]\n" \
"ds_read_b64_tr_b16 a[178:179], v36 offset:1024\n" \
"v_accvgpr_read_b32 v221, a81\n" \
"s_waitcnt lgkmcnt(10)\n" \
"v_mfma_f32_16x16x32_bf16 a[8:11], a[128:131], a[168:171], 0\n" \
"ds_read_b64_tr_b16 a[240:241], v36 offset:8192\n" \
"v_accvgpr_read_b32 v222, a82\n" \
"s_waitcnt lgkmcnt(9)\n" \
"s_nop 0\n" \
"v_mfma_f32_16x16x32_bf16 a[8:11], a[192:195], a[232:235], a[8:11]\n" \
"ds_read_b64_tr_b16 a[242:243], v36 offset:9216\n" \
"v_accvgpr_read_b32 v223, a83\n" \
"v_mfma_f32_16x16x32_bf16 a[24:27], a[132:135], a[168:171], 0\n" \
"ds_read_b64_tr_b16 a[180:181], v37 offset:0\n" \
"v_accvgpr_read_b32 v180, a84\n" \
"v_mfma_f32_16x16x32_bf16 a[24:27], a[196:199], a[232:235], a[24:27]\n" \
"ds_read_b64_tr_b16 a[182:183], v37 offset:1024\n" \
"v_accvgpr_read_b32 v181, a85\n" \
"v_mfma_f32_16x16x32_bf16 a[28:31], a[172:175], a[132:135], 0\n" \
"ds_read_b64_tr_b16 a[244:245], v37 offset:8192\n" \
"v_accvgpr_read_b32 v182, a86\n" \
"v_mfma_f32_16x16x32_bf16 a[28:31], a[236:239], a[196:199], a[28:31]\n" \
"s_add_u32 s89, s41, 0x2000\n" \
"buffer_store_dwordx4 v[200:203], v20, s[80:83], s89 offen offset:0 sc1\n" \
"v_mfma_f32_16x16x32_bf16 a[20:23], a[164:167], a[132:135], 0\n" \
"ds_read_b64_tr_b16 a[246:247], v37 offset:9216\n" \
"v_accvgpr_read_b32 v183, a87\n" \
"v_mfma_f32_16x16x32_bf16 a[20:23], a[228:231], a[196:199], a[20:23]\n" \
"ds_read_b64_tr_b16 a[184:185], v38 offset:0\n" \
"v_cvt_pk_bf16_f32 v220, v220, v221\n" \
"v_mfma_f32_16x16x32_bf16 a[16:19], a[160:163], a[132:135], 0\n" \
"ds_read_b64_tr_b16 a[186:187], v38 offset:1024\n" \
"v_cvt_pk_bf16_f32 v221, v222, v223\n" \
"v_mfma_f32_16x16x32_bf16 a[16:19], a[224:227], a[196:199], a[16:19]\n" \
"v_cvt_pk_bf16_f32 v222, v180, v181\n" \
"v_cvt_pk_bf16_f32 v223, v182, v183\n" \
"v_cvt_pk_bf16_f32 v224, v52, v53\n" \
".fill (L_pb + 20192 - .) / 4, 4, 0xBF800000\n" \
"L_pp5:\n" \
"s_cbranch_execz L_pret\n" \
"s_nop 0\n" \
"v_mfma_f32_16x16x32_bf16 a[32:35], a[160:163], a[136:139], 0\n" \
"s_add_u32 s88, s41, 0x2000\n" \
"buffer_store_dwordx4 v[204:207], v20, s[80:83], s88 offen offset:1024 sc1\n" \
"v_mfma_f32_16x16x32_bf16 a[32:35], a[224:227], a[200:203], a[32:35]\n" \
"ds_read_b64_tr_b16 a[248:249], v38 offset:8192\n" \
"v_cvt_pk_bf16_f32 v225, v54, v55\n" \
"v_mfma_f32_16x16x32_bf16 a[36:39], a[164:167], a[136:139], 0\n" \
"ds_read_b64_tr_b16 a[250:251], v38 offset:9216\n" \
"v_cvt_pk_bf16_f32 v226, v56, v57\n" \
"v_mfma_f32_16x16x32_bf16 a[36:39], a[228:231], a[200:203], a[36:39]\n" \
"ds_read_b64_tr_b16 a[188:189], v39 offset:0\n" \
"v_cvt_pk_bf16_f32 v227, v58, v59\n" \
"v_mfma_f32_16x16x32_bf16 a[44:47], a[172:175], a[136:139], 0\n" \
"v_cvt_pk_bf16_f32 v228, v60, v61\n" \
"v_cvt_pk_bf16_f32 v229, v62, v63\n" \
"v_cvt_pk_bf16_f32 v230, v64, v65\n" \
"v_mfma_f32_16x16x32_bf16 a[44:47], a[236:239], a[200:203], a[44:47]\n" \
"ds_read_b64_tr_b16 a[190:191], v39 offset:1024\n" \
"v_cvt_pk_bf16_f32 v231, v66, v67\n" \
"v_mfma_f32_16x16x32_bf16 a[40:43], a[136:139], a[168:171], 0\n" \
"s_add_u32 s89, s41, 0x3000\n" \
"buffer_store_dwordx4 v[208:211], v20, s[80:83], s89 offen offset:1024 sc1\n" \
"v_mfma_f32_16x16x32_bf16 a[40:43], a[200:203], a[232:235], a[40:43]\n" \
"ds_read_b64_tr_b16 a[252:253], v39 offset:8192\n" \
"v_cvt_pk_bf16_f32 v232, v76, v77\n" \
"v_mfma_f32_16x16x32_bf16 a[56:59], a[140:143], a[168:171], 0\n" \
"ds_read_b64_tr_b16 a[254:255], v39 offset:9216\n" \
"v_cvt_pk_bf16_f32 v233, v78, v79\n" \
"v_mfma_f32_16x16x32_bf16 a[56:59], a[204:207], a[232:235], a[56:59]\n" \
"v_cvt_pk_bf16_f32 v234, v80, v81\n" \
"v_cvt_pk_bf16_f32 v235, v82, v83\n" \
"v_cvt_pk_bf16_f32 v236, v68, v69\n" \
"v_mfma_f32_16x16x32_bf16 a[60:63], a[172:175], a[140:143], 0\n" \
"v_cvt_pk_bf16_f32 v237, v70, v71\n" \
"v_cvt_pk_bf16_f32 v238, v72, v73\n" \
"v_cvt_pk_bf16_f32 v239, v74, v75\n" \
"v_mfma_f32_16x16x32_bf16 a[60:63], a[236:239], a[204:207], a[60:63]\n" \
"s_add_u32 s88, s41, 0x3000\n" \
"buffer_store_dwordx4 v[212:215], v20, s[80:83], s88 offen offset:0 sc1\n" \
"v_mfma_f32_16x16x32_bf16 a[52:55], a[164:167], a[140:143], 0\n" \
"v_cvt_pk_bf16_f32 v240, v84, v85\n" \
"v_cvt_pk_bf16_f32 v241, v86, v87\n" \
"v_cvt_pk_bf16_f32 v242, v88, v89\n" \
"v_mfma_f32_16x16x32_bf16 a[52:55], a[228:231], a[204:207], a[52:55]\n" \
"v_cvt_pk_bf16_f32 v243, v90, v91\n" \
"v_cvt_pk_bf16_f32 v244, v92, v93\n" \
"v_cvt_pk_bf16_f32 v245, v94, v95\n" \
"v_mfma_f32_16x16x32_bf16 a[48:51], a[160:163], a[140:143], 0\n" \
"v_cvt_pk_bf16_f32 v246, v96, v97\n" \
"v_cvt_pk_bf16_f32 v247, v98, v99\n" \
"v_cvt_pk_bf16_f32 v184, v108, v109\n" \
"v_mfma_f32_16x16x32_bf16 a[48:51], a[224:227], a[204:207], a[48:51]\n" \
"v_cvt_pk_bf16_f32 v185, v110, v111\n" \
"v_cvt_pk_bf16_f32 v186, v112, v113\n" \
"v_cvt_pk_bf16_f32 v187, v114, v115\n" \
"s_waitcnt lgkmcnt(15)\n" \
"s_nop 0\n" \
"v_mfma_f32_16x16x32_bf16 a[64:67], a[164:167], v[248:251], 0\n" \
"v_cvt_pk_bf16_f32 v188, v100, v101\n" \
"v_cvt_pk_bf16_f32 v189, v102, v103\n" \
"v_cvt_pk_bf16_f32 v190, v104, v105\n" \
"v_mfma_f32_16x16x32_bf16 a[64:67], a[228:231], v[48:51], a[64:67]\n" \
"v_cvt_pk_bf16_f32 v191, v106, v107\n" \
"v_cvt_pk_bf16_f32 v192, v116, v117\n" \
"v_cvt_pk_bf16_f32 v193, v118, v119\n" \
"v_mfma_f32_16x16x32_bf16 a[80:83], a[172:175], v[248:251], 0\n" \
"v_cvt_pk_bf16_f32 v194, v120, v121\n" \
"v_cvt_pk_bf16_f32 v195, v122, v123\n" \
"v_cvt_pk_bf16_f32 v196, v124, v125\n" \
"v_mfma_f32_16x16x32_bf16 a[80:83], a[236:239], v[48:51], a[80:83]\n" \
"v_cvt_pk_bf16_f32 v197, v126, v127\n" \
"v_cvt_pk_bf16_f32 v198, v128, v129\n" \
"v_cvt_pk_bf16_f32 v199, v130, v131\n" \
"v_mfma_f32_16x16x32_bf16 a[68:71], v[248:251], a[168:171], 0\n" \
"v_cvt_pk_bf16_f32 v252, v140, v141\n" \
"v_cvt_pk_bf16_f32 v253, v142, v143\n" \
"v_cvt_pk_bf16_f32 v254, v144, v145\n" \
"v_mfma_f32_16x16x32_bf16 a[68:71], v[48:51], a[232:235], a[68:71]\n" \
"v_cvt_pk_bf16_f32 v255, v146, v147\n" \
"v_accvgpr_write_b32 a84, v252\n" \
"v_accvgpr_write_b32 a85, v253\n" \
"s_waitcnt vmcnt(4) lgkmcnt(0)\n" \
"s_barrier\n" \
"v_mfma_f32_16x16x32_bf16 v[52:55], a[176:179], a[128:131], 0\n" \
"s_add_u32 m0, s40, 0x0\n" \
"s_nop 0\n" \
"s_add_u32 s85, s39, 0x4000\n" \
"v_accvgpr_write_b32 a86, v254\n" \
"v_mfma_f32_16x16x32_bf16 v[52:55], a[240:243], a[192:195], v[52:55]\n" \
"buffer_load_dwordx4 v20, s[80:83], s85 offen sc1 lds\n" \
"v_accvgpr_write_b32 a87, v255\n" \
"v_mfma_f32_16x16x32_bf16 v[56:59], a[180:183], a[128:131], 0\n" \
"ds_read_b64_tr_b16 a[160:161], v32 offset:32768\n" \
"v_cvt_pk_bf16_f32 v252, v132, v133\n" \
"v_mfma_f32_16x16x32_bf16 v[56:59], a[244:247], a[192:195], v[56:59]\n" \
"ds_read_b64_tr_b16 a[162:163], v32 offset:33280\n" \
"v_cvt_pk_bf16_f32 v253, v134, v135\n" \
"v_mfma_f32_16x16x32_bf16 v[60:63], a[184:187], a[128:131], 0\n" \
"s_add_u32 m0, s40, 0x1000\n" \
"s_add_u32 s85, s39, 0x5000\n" \
"v_cvt_pk_bf16_f32 v254, v136, v137\n" \
"v_mfma_f32_16x16x32_bf16 v[60:63], a[248:251], a[192:195], v[60:63]\n" \
"buffer_load_dwordx4 v20, s[80:83], s85 offen sc1 lds\n" \
"v_cvt_pk_bf16_f32 v255, v138, v139\n" \
"v_mfma_f32_16x16x32_bf16 v[64:67], a[188:191], a[128:131], 0\n" \
"ds_read_b64_tr_b16 a[224:225], v32 offset:36864\n" \
"v_cvt_pk_bf16_f32 v200, v148, v149\n" \
"v_mfma_f32_16x16x32_bf16 v[64:67], a[252:255], a[192:195], v[64:67]\n" \
"ds_read_b128 a[128:131], v28 offset:32768\n" \
"v_cvt_pk_bf16_f32 v201, v150, v151\n" \
"v_mfma_f32_16x16x32_bf16 v[80:83], a[188:191], a[132:135], 0\n" \
"ds_read_b128 a[192:195], v29 offset:32768\n" \
"v_cvt_pk_bf16_f32 v202, v152, v153\n" \
"v_mfma_f32_16x16x32_bf16 v[80:83], a[252:255], a[196:199], v[80:83]\n" \
"s_add_u32 m0, s40, 0x3000\n" \
"s_movk_i32 s84, 0x100\n" \
"s_nop 0\n" \
"v_cvt_pk_bf16_f32 v203, v154, v155\n" \
"v_mfma_f32_16x16x32_bf16 v[76:79], a[184:187], a[132:135], 0\n" \
"buffer_load_dwordx4 v21, s[76:79], s84 offen offset:0 lds\n" \
"v_cvt_pk_bf16_f32 v204, v156, v157\n" \
"v_mfma_f32_16x16x32_bf16 v[76:79], a[248:251], a[196:199], v[76:79]\n" \
"ds_read_b64_tr_b16 a[226:227], v32 offset:37376\n" \
"v_cvt_pk_bf16_f32 v205, v158, v159\n" \
"v_mfma_f32_16x16x32_bf16 v[72:75], a[180:183], a[132:135], 0\n" \
"ds_read_b64_tr_b16 a[164:165], v33 offset:32768\n" \
"v_cvt_pk_bf16_f32 v206, v160, v161\n" \
"v_mfma_f32_16x16x32_bf16 v[72:75], a[244:247], a[196:199], v[72:75]\n" \
"v_cvt_pk_bf16_f32 v207, v162, v163\n" \
"v_cvt_pk_bf16_f32 v208, v172, v173\n" \
"v_cvt_pk_bf16_f32 v209, v174, v175\n" \
"v_mfma_f32_16x16x32_bf16 v[68:71], a[176:179], a[132:135], 0\n" \
"ds_read_b64_tr_b16 a[166:167], v33 offset:33280\n" \
"v_cvt_pk_bf16_f32 v210, v176, v177\n" \
"v_mfma_f32_16x16x32_bf16 v[68:71], a[240:243], a[196:199], v[68:71]\n" \
"ds_read_b128 a[132:135], v28 offset:34816\n" \
"v_cvt_pk_bf16_f32 v211, v178, v179\n" \
"v_mfma_f32_16x16x32_bf16 v[84:87], a[176:179], a[136:139], 0\n" \
"ds_read_b128 a[196:199], v29 offset:34816\n" \
"v_cvt_pk_bf16_f32 v212, v164, v165\n" \
"v_mfma_f32_16x16x32_bf16 v[84:87], a[240:243], a[200:203], v[84:87]\n" \
"s_add_u32 m0, s31, 0x0\n" \
"s_mov_b32 s84, 0x100000\n" \
"v_cvt_pk_bf16_f32 v213, v166, v167\n" \
"v_mfma_f32_16x16x32_bf16 v[88:91], a[180:183], a[136:139], 0\n" \
"buffer_load_dwordx4 v26, s[68:71], s84 offen offset:0 lds\n" \
"v_cvt_pk_bf16_f32 v214, v168, v169\n" \
"v_mfma_f32_16x16x32_bf16 v[88:91], a[244:247], a[200:203], v[88:91]\n" \
"s_add_u32 s89, s41, 0x0\n" \
"buffer_store_dwordx4 v[224:227], v20, s[80:83], s89 offen offset:2048 sc1\n" \
"v_mfma_f32_16x16x32_bf16 v[92:95], a[184:187], a[136:139], 0\n" \
"ds_read_b64_tr_b16 a[228:229], v33 offset:36864\n" \
"v_cvt_pk_bf16_f32 v215, v170, v171\n" \
"v_mfma_f32_16x16x32_bf16 v[92:95], a[248:251], a[200:203], v[92:95]\n" \
"ds_read_b64_tr_b16 a[230:231], v33 offset:37376\n" \
"v_mfma_f32_16x16x32_bf16 v[96:99], a[188:191], a[136:139], 0\n" \
"v_mfma_f32_16x16x32_bf16 v[96:99], a[252:255], a[200:203], v[96:99]\n" \
"ds_read_b128 a[136:139], v28 offset:36864\n" \
"v_mfma_f32_16x16x32_bf16 v[112:115], a[188:191], a[140:143], 0\n" \
"ds_read_b128 a[200:203], v29 offset:36864\n" \
"v_mfma_f32_16x16x32_bf16 v[112:115], a[252:255], a[204:207], v[112:115]\n" \
"s_addk_i32 s84, 0x7c00\n" \
"v_mfma_f32_16x16x32_bf16 v[108:111], a[184:187], a[140:143], 0\n" \
"buffer_load_dwordx4 v26, s[68:71], s84 offen offset:1024 lds\n" \
"v_mfma_f32_16x16x32_bf16 v[108:111], a[248:251], a[204:207], v[108:111]\n" \
"ds_read_b64_tr_b16 a[172:173], v35 offset:32768\n" \
"v_mfma_f32_16x16x32_bf16 v[104:107], a[180:183], a[140:143], 0\n" \
"s_addk_i32 s84, 0x7c00\n" \
"v_mfma_f32_16x16x32_bf16 v[104:107], a[244:247], a[204:207], v[104:107]\n" \
"buffer_load_dwordx4 v27, s[68:71], s84 offen offset:2048 lds\n" \
"v_mfma_f32_16x16x32_bf16 v[100:103], a[176:179], a[140:143], 0\n" \
"ds_read_b64_tr_b16 a[174:175], v35 offset:33280\n" \
"v_mfma_f32_16x16x32_bf16 v[100:103], a[240:243], a[204:207], v[100:103]\n" \
"ds_read_b128 a[140:143], v28 offset:38912\n" \
"v_mfma_f32_16x16x32_bf16 v[116:119], a[176:179], a[144:147], 0\n" \
"ds_read_b128 a[204:207], v29 offset:38912\n" \
"v_mfma_f32_16x16x32_bf16 v[116:119], a[240:243], a[208:211], v[116:119]\n" \
"s_addk_i32 s84, 0x7c00\n" \
"v_mfma_f32_16x16x32_bf16 v[120:123], a[180:183], a[144:147], 0\n" \
"buffer_load_dwordx4 v27, s[68:71], s84 offen offset:3072 lds\n" \
"v_mfma_f32_16x16x32_bf16 v[120:123], a[244:247], a[208:211], v[120:123]\n" \
"ds_read_b64_tr_b16 a[236:237], v35 offset:36864\n" \
"v_mfma_f32_16x16x32_bf16 v[124:127], a[184:187], a[144:147], 0\n" \
"ds_read_b64_tr_b16 a[238:239], v35 offset:37376\n" \
"v_mfma_f32_16x16x32_bf16 v[124:127], a[248:251], a[208:211], v[124:127]\n" \
"s_add_u32 m0, s30, 0x0\n" \
"s_movk_i32 s84, 0x180\n" \
"v_mfma_f32_16x16x32_bf16 v[128:131], a[188:191], a[144:147], 0\n" \
"buffer_load_dwordx4 v24, s[76:79], s84 offen offset:0 lds\n" \
"v_mfma_f32_16x16x32_bf16 v[128:131], a[252:255], a[208:211], v[128:131]\n" \
"ds_read_b128 a[144:147], v28 offset:40960\n" \
"v_mfma_f32_16x16x32_bf16 v[144:147], a[188:191], a[148:151], 0\n" \
"ds_read_b128 a[208:211], v29 offset:40960\n" \
"v_mfma_f32_16x16x32_bf16 v[144:147], a[252:255], a[212:215], v[144:147]\n" \
"ds_read_b64_tr_b16 a[168:169], v34 offset:32768\n" \
"v_mfma_f32_16x16x32_bf16 v[140:143], a[184:187], a[148:151], 0\n" \
"s_movk_i32 s84, 0x2180\n" \
"v_mfma_f32_16x16x32_bf16 v[140:143], a[248:251], a[212:215], v[140:143]\n" \
"buffer_load_dwordx4 v25, s[76:79], s84 offen offset:1024 lds\n" \
"v_mfma_f32_16x16x32_bf16 v[136:139], a[180:183], a[148:151], 0\n" \
"ds_read_b64_tr_b16 a[170:171], v34 offset:33280\n" \
"v_mfma_f32_16x16x32_bf16 v[136:139], a[244:247], a[212:215], v[136:139]\n" \
"ds_read_b64_tr_b16 a[232:233], v34 offset:36864\n" \
"v_mfma_f32_16x16x32_bf16 v[132:135], a[176:179], a[148:151], 0\n" \
"ds_read_b64_tr_b16 a[234:235], v34 offset:37376\n" \
"v_mfma_f32_16x16x32_bf16 v[132:135], a[240:243], a[212:215], v[132:135]\n" \
"ds_read_b128 a[148:151], v28 offset:43008\n" \
"v_mfma_f32_16x16x32_bf16 v[148:151], a[176:179], a[152:155], 0\n" \
"ds_read_b128 a[212:215], v29 offset:43008\n" \
"v_mfma_f32_16x16x32_bf16 v[148:151], a[240:243], a[216:219], v[148:151]\n" \
"s_movk_i32 s84, 0x4180\n" \
"v_mfma_f32_16x16x32_bf16 v[152:155], a[180:183], a[152:155], 0\n" \
"buffer_load_dwordx4 v24, s[76:79], s84 offen offset:2048 lds\n" \
"v_mfma_f32_16x16x32_bf16 v[152:155], a[244:247], a[216:219], v[152:155]\n" \
"s_movk_i32 s84, 0x6180\n" \
"v_mfma_f32_16x16x32_bf16 v[156:159], a[184:187], a[152:155], 0\n" \
"buffer_load_dwordx4 v25, s[76:79], s84 offen offset:3072 lds\n" \
"v_mfma_f32_16x16x32_bf16 v[156:159], a[248:251], a[216:219], v[156:159]\n" \
"ds_read_b128 v[248:251], v22 offset:32768\n" \
"v_mfma_f32_16x16x32_bf16 v[160:163], a[188:191], a[152:155], 0\n" \
"ds_read_b128 v[48:51], v23 offset:32768\n" \
"v_mfma_f32_16x16x32_bf16 v[160:163], a[252:255], a[216:219], v[160:163]\n" \
"ds_read_b128 a[152:155], v28 offset:45056\n" \
"v_mfma_f32_16x16x32_bf16 v[176:179], a[188:191], a[156:159], 0\n" \
"ds_read_b128 a[216:219], v29 offset:45056\n" \
"v_mfma_f32_16x16x32_bf16 v[176:179], a[252:255], a[220:223], v[176:179]\n" \
"v_mfma_f32_16x16x32_bf16 v[172:175], a[184:187], a[156:159], 0\n" \
"v_mfma_f32_16x16x32_bf16 v[172:175], a[248:251], a[220:223], v[172:175]\n" \
"v_mfma_f32_16x16x32_bf16 v[168:171], a[180:183], a[156:159], 0\n" \
"v_mfma_f32_16x16x32_bf16 v[168:171], a[244:247], a[220:223], v[168:171]\n" \
"v_mfma_f32_16x16x32_bf16 v[164:167], a[176:179], a[156:159], 0\n" \
"v_mfma_f32_16x16x32_bf16 v[164:167], a[240:243], a[220:223], v[164:167]\n" \
"ds_read_b128 a[156:159], v28 offset:47104\n" \
"ds_read_b128 a[220:223], v29 offset:47104\n" \
"s_waitcnt lgkmcnt(15)\n" \
"v_mfma_f32_16x16x32_bf16 a[0:3], a[160:163], a[128:131], a[0:3]\n" \
"s_add_u32 m0, s30, 0x1000\n" \
"s_addk_i32 s84, 0x3000\n" \
"v_mfma_f32_16x16x32_bf16 a[0:3], a[224:227], a[192:195], a[0:3]\n" \
"buffer_load_dwordx4 v24, s[76:79], s84 offen offset:0 lds\n" \
"v_mfma_f32_16x16x32_bf16 a[4:7], a[164:167], a[128:131], a[4:7]\n" \
"ds_read_b64_tr_b16 a[176:177], v36 offset:32768\n" \
"v_mov_b32_e64 v11, 0\n" \
"v_mfma_f32_16x16x32_bf16 a[4:7], a[228:231], a[192:195], a[4:7]\n" \
"ds_read_b64_tr_b16 a[178:179], v36 offset:33792\n" \
"s_and_b32 s6, s2, 7\n" \
"v_mfma_f32_16x16x32_bf16 a[12:15], a[172:175], a[128:131], a[12:15]\n" \
"ds_read_b64_tr_b16 a[240:241], v36 offset:40960\n" \
"s_lshl_b32 s5, s3, 1\n" \
"s_waitcnt lgkmcnt(15)\n" \
"v_mfma_f32_16x16x32_bf16 a[12:15], a[236:239], a[192:195], a[12:15]\n" \
"s_add_u32 s88, s41, 0x0\n" \
"buffer_store_dwordx4 v[228:231], v20, s[80:83], s88 offen offset:3072 sc1\n" \
"s_waitcnt lgkmcnt(13)\n" \
"v_mfma_f32_16x16x32_bf16 a[8:11], a[128:131], a[168:171], a[8:11]\n" \
"ds_read_b64_tr_b16 a[242:243], v36 offset:41984\n" \
"s_lshl_b32 s4, s6, 2\n" \
"s_waitcnt lgkmcnt(12)\n" \
"v_mfma_f32_16x16x32_bf16 a[8:11], a[192:195], a[232:235], a[8:11]\n" \
"ds_read_b64_tr_b16 a[180:181], v37 offset:32768\n" \
"s_bfe_u32 s8, s2, 0x20003\n" \
"v_mfma_f32_16x16x32_bf16 a[24:27], a[132:135], a[168:171], a[24:27]\n" \
"ds_read_b64_tr_b16 a[182:183], v37 offset:33792\n" \
"s_or_b32 s13, s4, s8\n" \
"v_mfma_f32_16x16x32_bf16 a[24:27], a[196:199], a[232:235], a[24:27]\n" \
"ds_read_b64_tr_b16 a[244:245], v37 offset:40960\n" \
"s_and_b32 s5, s5, 4\n" \
"v_mfma_f32_16x16x32_bf16 a[28:31], a[172:175], a[132:135], a[28:31]\n" \
"s_and_b32 s7, s3, 1\n" \
"s_or_b32 s9, s5, s7\n" \
"v_mfma_f32_16x16x32_bf16 a[28:31], a[236:239], a[196:199], a[28:31]\n" \
"s_addk_i32 s84, 0x2000\n" \
"v_mfma_f32_16x16x32_bf16 a[20:23], a[164:167], a[132:135], a[20:23]\n" \
"buffer_load_dwordx4 v25, s[76:79], s84 offen offset:1024 lds\n" \
"v_mfma_f32_16x16x32_bf16 a[20:23], a[228:231], a[196:199], a[20:23]\n" \
"ds_read_b64_tr_b16 a[246:247], v37 offset:41984\n" \
"s_lshl_b32 s5, s13, 4\n" \
"v_mfma_f32_16x16x32_bf16 a[16:19], a[160:163], a[132:135], a[16:19]\n" \
"ds_read_b64_tr_b16 a[184:185], v38 offset:32768\n" \
"s_lshl_b32 s7, s3, 2\n" \
"v_mfma_f32_16x16x32_bf16 a[16:19], a[224:227], a[196:199], a[16:19]\n" \
"ds_read_b64_tr_b16 a[186:187], v38 offset:33792\n" \
"s_or_b32 s7, s5, s7\n" \
"v_mfma_f32_16x16x32_bf16 a[32:35], a[160:163], a[136:139], a[32:35]\n" \
"s_addk_i32 s84, 0x2000\n" \
"v_mfma_f32_16x16x32_bf16 a[32:35], a[224:227], a[200:203], a[32:35]\n" \
"buffer_load_dwordx4 v24, s[76:79], s84 offen offset:2048 lds\n" \
"v_mfma_f32_16x16x32_bf16 a[36:39], a[164:167], a[136:139], a[36:39]\n" \
"ds_read_b64_tr_b16 a[248:249], v38 offset:40960\n" \
"s_lshr_b32 s5, s2, 5\n" \
"v_mfma_f32_16x16x32_bf16 a[36:39], a[228:231], a[200:203], a[36:39]\n" \
"ds_read_b64_tr_b16 a[250:251], v38 offset:41984\n" \
"s_mul_i32 s10, s5, 0xa00\n" \
"v_mfma_f32_16x16x32_bf16 a[44:47], a[172:175], a[136:139], a[44:47]\n" \
"ds_read_b64_tr_b16 a[188:189], v39 offset:32768\n" \
"s_add_i32 s10, s10, 0x5bc1000\n" \
"v_mfma_f32_16x16x32_bf16 a[44:47], a[236:239], a[200:203], a[44:47]\n" \
"ds_read_b64_tr_b16 a[190:191], v39 offset:33792\n" \
"s_or_b32 s7, s7, s10\n" \
"v_mfma_f32_16x16x32_bf16 a[40:43], a[136:139], a[168:171], a[40:43]\n" \
"s_add_u32 s89, s41, 0x1000\n" \
"buffer_store_dwordx4 v[232:235], v20, s[80:83], s89 offen offset:3072 sc1\n" \
"v_mfma_f32_16x16x32_bf16 a[40:43], a[200:203], a[232:235], a[40:43]\n" \
"ds_read_b64_tr_b16 a[252:253], v39 offset:40960\n" \
"s_lshl_b32 s14, s5, 3\n" \
"v_mfma_f32_16x16x32_bf16 a[56:59], a[140:143], a[168:171], a[56:59]\n" \
"ds_read_b64_tr_b16 a[254:255], v39 offset:41984\n" \
"v_writelane_b32 v11, s7, 0\n" \
"v_mfma_f32_16x16x32_bf16 a[56:59], a[204:207], a[232:235], a[56:59]\n" \
"s_or_b32 s7, s14, s6\n" \
"v_writelane_b32 v11, s10, 1\n" \
"v_mfma_f32_16x16x32_bf16 a[60:63], a[172:175], a[140:143], a[60:63]\n" \
"s_lshl_b32 s7, s7, 10\n" \
"s_lshl_b32 s10, s3, 8\n" \
"v_mfma_f32_16x16x32_bf16 a[60:63], a[236:239], a[204:207], a[60:63]\n" \
"s_addk_i32 s84, 0x2000\n" \
"v_mfma_f32_16x16x32_bf16 a[52:55], a[164:167], a[140:143], a[52:55]\n" \
"buffer_load_dwordx4 v25, s[76:79], s84 offen offset:3072 lds\n" \
"v_mfma_f32_16x16x32_bf16 a[52:55], a[228:231], a[204:207], a[52:55]\n" \
"s_or_b32 s7, s7, s10\n" \
"s_or_b32 s10, s7, 0x5c00200\n" \
"v_mfma_f32_16x16x32_bf16 a[48:51], a[160:163], a[140:143], a[48:51]\n" \
"v_writelane_b32 v11, s10, 2\n" \
"s_lshl_b32 s10, s8, 2\n" \
"v_mfma_f32_16x16x32_bf16 a[48:51], a[224:227], a[204:207], a[48:51]\n" \
"s_or_b32 s11, s10, s7\n" \
"s_or_b32 s11, s11, 0x5c00000\n" \
"s_waitcnt lgkmcnt(15)\n" \
"v_mfma_f32_16x16x32_bf16 a[64:67], a[164:167], v[248:251], a[64:67]\n" \
"v_writelane_b32 v11, s11, 3\n" \
"s_or_b32 s7, s7, 0x5c00080\n" \
"v_mfma_f32_16x16x32_bf16 a[64:67], a[228:231], v[48:51], a[64:67]\n" \
"v_writelane_b32 v11, s7, 4\n" \
"s_or_b32 s7, s7, s10\n" \
"v_mfma_f32_16x16x32_bf16 a[80:83], a[172:175], v[248:251], a[80:83]\n" \
"s_add_i32 s10, s3, s2\n" \
"v_writelane_b32 v11, s7, 5\n" \
"v_mfma_f32_16x16x32_bf16 a[80:83], a[236:239], v[48:51], a[80:83]\n" \
"s_mov_b32 s7, 2.0\n" \
"s_mov_b32 s11, 0x5bc0000\n" \
"v_mfma_f32_16x16x32_bf16 a[68:71], v[248:251], a[168:171], a[68:71]\n" \
"s_cmp_lg_u32 s10, 0\n" \
"s_cselect_b32 s10, s7, s11\n" \
"s_waitcnt vmcnt(10) lgkmcnt(0)\n" \
"v_mfma_f32_16x16x32_bf16 a[68:71], v[48:51], a[232:235], a[68:71]\n" \
"s_barrier\n" \
"v_xor_b32_e32 v28, 0x20000, v28\n" \
"v_xor_b32_e32 v29, 0x20000, v29\n" \
"v_mfma_f32_16x16x32_bf16 v[52:55], a[176:179], a[128:131], v[52:55]\n" \
"s_add_u32 m0, s40, 0x8000\n" \
"s_add_u32 s85, s39, 0x6000\n" \
"v_mfma_f32_16x16x32_bf16 v[52:55], a[240:243], a[192:195], v[52:55]\n" \
"buffer_load_dwordx4 v20, s[80:83], s85 offen sc1 lds\n" \
"v_mfma_f32_16x16x32_bf16 v[56:59], a[180:183], a[128:131], v[56:59]\n" \
"ds_read_b64_tr_b16 a[160:161], v32 offset:0\n" \
"s_lshr_b32 s15, s2, 3\n" \
"v_mfma_f32_16x16x32_bf16 v[56:59], a[244:247], a[192:195], v[56:59]\n" \
"ds_read_b64_tr_b16 a[162:163], v32 offset:512\n" \
"v_writelane_b32 v11, s10, 54\n" \
"v_mfma_f32_16x16x32_bf16 v[60:63], a[184:187], a[128:131], v[60:63]\n" \
"s_add_u32 m0, s40, 0x9000\n" \
"s_add_u32 s85, s39, 0x7000\n" \
"v_mfma_f32_16x16x32_bf16 v[60:63], a[248:251], a[192:195], v[60:63]\n" \
"buffer_load_dwordx4 v20, s[80:83], s85 offen sc1 lds\n" \
"v_mfma_f32_16x16x32_bf16 v[64:67], a[188:191], a[128:131], v[64:67]\n" \
"ds_read_b64_tr_b16 a[224:225], v32 offset:4096\n" \
"s_lshl_b32 s10, s6, 5\n" \
"v_mfma_f32_16x16x32_bf16 v[64:67], a[252:255], a[192:195], v[64:67]\n" \
"ds_read_b128 a[128:131], v28 offset:0\n" \
"s_min_u32 s16, s3, 2\n" \
"s_nop 0\n" \
"v_mfma_f32_16x16x32_bf16 v[80:83], a[188:191], a[132:135], v[80:83]\n" \
"ds_read_b128 a[192:195], v29 offset:0\n" \
"s_mul_i32 s12, s8, 0x120\n" \
"v_mfma_f32_16x16x32_bf16 v[80:83], a[252:255], a[196:199], v[80:83]\n" \
"s_add_u32 s88, s41, 0x1000\n" \
"buffer_store_dwordx4 v[236:239], v20, s[80:83], s88 offen offset:2048 sc1\n" \
"v_mfma_f32_16x16x32_bf16 v[76:79], a[184:187], a[132:135], v[76:79]\n" \
"ds_read_b64_tr_b16 a[226:227], v32 offset:4608\n" \
"v_mfma_f32_16x16x32_bf16 v[76:79], a[248:251], a[196:199], v[76:79]\n" \
"ds_read_b64_tr_b16 a[164:165], v33 offset:0\n" \
"v_mfma_f32_16x16x32_bf16 v[72:75], a[180:183], a[132:135], v[72:75]\n" \
"s_add_u32 m0, s40, 0xb000\n" \
"s_movk_i32 s84, 0x180\n" \
"s_nop 0\n" \
"v_mfma_f32_16x16x32_bf16 v[72:75], a[244:247], a[196:199], v[72:75]\n" \
"buffer_load_dwordx4 v21, s[76:79], s84 offen offset:0 lds\n" \
"v_mfma_f32_16x16x32_bf16 v[68:71], a[176:179], a[132:135], v[68:71]\n" \
"ds_read_b64_tr_b16 a[166:167], v33 offset:512\n" \
"v_mfma_f32_16x16x32_bf16 v[68:71], a[240:243], a[196:199], v[68:71]\n" \
"ds_read_b128 a[132:135], v28 offset:2048\n" \
"v_mfma_f32_16x16x32_bf16 v[84:87], a[176:179], a[136:139], v[84:87]\n" \
"ds_read_b128 a[196:199], v29 offset:2048\n" \
"v_mfma_f32_16x16x32_bf16 v[84:87], a[240:243], a[200:203], v[84:87]\n" \
"s_add_u32 m0, s31, 0x8000\n" \
"s_mov_b32 s84, 0x180000\n" \
"v_mfma_f32_16x16x32_bf16 v[88:91], a[180:183], a[136:139], v[88:91]\n" \
"buffer_load_dwordx4 v26, s[68:71], s84 offen offset:0 lds\n" \
"v_mfma_f32_16x16x32_bf16 v[88:91], a[244:247], a[200:203], v[88:91]\n" \
"ds_read_b64_tr_b16 a[228:229], v33 offset:4096\n" \
"v_mfma_f32_16x16x32_bf16 v[92:95], a[184:187], a[136:139], v[92:95]\n" \
"ds_read_b64_tr_b16 a[230:231], v33 offset:4608\n" \
"v_mfma_f32_16x16x32_bf16 v[92:95], a[248:251], a[200:203], v[92:95]\n" \
"s_addk_i32 s84, 0x7c00\n" \
"v_mfma_f32_16x16x32_bf16 v[96:99], a[188:191], a[136:139], v[96:99]\n" \
"buffer_load_dwordx4 v26, s[68:71], s84 offen offset:1024 lds\n" \
"v_mfma_f32_16x16x32_bf16 v[96:99], a[252:255], a[200:203], v[96:99]\n" \
"ds_read_b128 a[136:139], v28 offset:4096\n" \
"v_mfma_f32_16x16x32_bf16 v[112:115], a[188:191], a[140:143], v[112:115]\n" \
"ds_read_b128 a[200:203], v29 offset:4096\n" \
"v_mfma_f32_16x16x32_bf16 v[112:115], a[252:255], a[204:207], v[112:115]\n" \
"s_addk_i32 s84, 0x7c00\n" \
"v_mfma_f32_16x16x32_bf16 v[108:111], a[184:187], a[140:143], v[108:111]\n" \
"buffer_load_dwordx4 v27, s[68:71], s84 offen offset:2048 lds\n" \
"v_mfma_f32_16x16x32_bf16 v[108:111], a[248:251], a[204:207], v[108:111]\n" \
"ds_read_b64_tr_b16 a[172:173], v35 offset:0\n" \
"v_mfma_f32_16x16x32_bf16 v[104:107], a[180:183], a[140:143], v[104:107]\n" \
"ds_read_b64_tr_b16 a[174:175], v35 offset:512\n" \
"v_mfma_f32_16x16x32_bf16 v[104:107], a[244:247], a[204:207], v[104:107]\n" \
"ds_read_b64_tr_b16 a[236:237], v35 offset:4096\n" \
"v_mfma_f32_16x16x32_bf16 v[100:103], a[176:179], a[140:143], v[100:103]\n" \
"s_add_u32 s11, s22, s12\n" \
"s_addc_u32 s17, s23, 0\n" \
"v_mfma_f32_16x16x32_bf16 v[100:103], a[240:243], a[204:207], v[100:103]\n" \
"ds_read_b128 a[140:143], v28 offset:6144\n" \
"v_mfma_f32_16x16x32_bf16 v[116:119], a[176:179], a[144:147], v[116:119]\n" \
"ds_read_b128 a[204:207], v29 offset:6144\n" \
"v_mfma_f32_16x16x32_bf16 v[116:119], a[240:243], a[208:211], v[116:119]\n" \
"s_addk_i32 s84, 0x7c00\n" \
"v_mfma_f32_16x16x32_bf16 v[120:123], a[180:183], a[144:147], v[120:123]\n" \
"buffer_load_dwordx4 v27, s[68:71], s84 offen offset:3072 lds\n" \
"v_mfma_f32_16x16x32_bf16 v[120:123], a[244:247], a[208:211], v[120:123]\n" \
"s_add_u32 m0, s30, 0x8000\n" \
"s_movk_i32 s84, 0x200\n" \
"v_mfma_f32_16x16x32_bf16 v[124:127], a[184:187], a[144:147], v[124:127]\n" \
"buffer_load_dwordx4 v24, s[76:79], s84 offen offset:0 lds\n" \
"v_mfma_f32_16x16x32_bf16 v[124:127], a[248:251], a[208:211], v[124:127]\n" \
"ds_read_b64_tr_b16 a[238:239], v35 offset:4608\n" \
"v_mfma_f32_16x16x32_bf16 v[128:131], a[188:191], a[144:147], v[128:131]\n" \
"s_add_u32 s89, s41, 0x2000\n" \
"buffer_store_dwordx4 v[240:243], v20, s[80:83], s89 offen offset:2048 sc1\n" \
"v_mfma_f32_16x16x32_bf16 v[128:131], a[252:255], a[208:211], v[128:131]\n" \
"ds_read_b128 a[144:147], v28 offset:8192\n" \
"v_mfma_f32_16x16x32_bf16 v[144:147], a[188:191], a[148:151], v[144:147]\n" \
"ds_read_b128 a[208:211], v29 offset:8192\n" \
"v_mfma_f32_16x16x32_bf16 v[144:147], a[252:255], a[212:215], v[144:147]\n" \
"ds_read_b64_tr_b16 a[168:169], v34 offset:0\n" \
"v_mfma_f32_16x16x32_bf16 v[140:143], a[184:187], a[148:151], v[140:143]\n" \
"ds_read_b64_tr_b16 a[170:171], v34 offset:512\n" \
"v_mfma_f32_16x16x32_bf16 v[140:143], a[248:251], a[212:215], v[140:143]\n" \
"s_movk_i32 s84, 0x2200\n" \
"s_nop 0\n" \
"v_mfma_f32_16x16x32_bf16 v[136:139], a[180:183], a[148:151], v[136:139]\n" \
"buffer_load_dwordx4 v25, s[76:79], s84 offen offset:1024 lds\n" \
"v_mfma_f32_16x16x32_bf16 v[136:139], a[244:247], a[212:215], v[136:139]\n" \
"ds_read_b64_tr_b16 a[232:233], v34 offset:4096\n" \
"v_mfma_f32_16x16x32_bf16 v[132:135], a[176:179], a[148:151], v[132:135]\n" \
"ds_read_b64_tr_b16 a[234:235], v34 offset:4608\n" \
"v_mfma_f32_16x16x32_bf16 v[132:135], a[240:243], a[212:215], v[132:135]\n" \
"ds_read_b128 a[148:151], v28 offset:10240\n" \
"v_mfma_f32_16x16x32_bf16 v[148:151], a[176:179], a[152:155], v[148:151]\n" \
"ds_read_b128 a[212:215], v29 offset:10240\n" \
"v_mfma_f32_16x16x32_bf16 v[148:151], a[240:243], a[216:219], v[148:151]\n" \
"s_movk_i32 s84, 0x4200\n" \
"v_mfma_f32_16x16x32_bf16 v[152:155], a[180:183], a[152:155], v[152:155]\n" \
"buffer_load_dwordx4 v24, s[76:79], s84 offen offset:2048 lds\n" \
"v_mfma_f32_16x16x32_bf16 v[152:155], a[244:247], a[216:219], v[152:155]\n" \
"s_movk_i32 s84, 0x6200\n" \
"v_mfma_f32_16x16x32_bf16 v[156:159], a[184:187], a[152:155], v[156:159]\n" \
"buffer_load_dwordx4 v25, s[76:79], s84 offen offset:3072 lds\n" \
"v_mfma_f32_16x16x32_bf16 v[156:159], a[248:251], a[216:219], v[156:159]\n" \
"ds_read_b128 v[248:251], v22 offset:0\n" \
"v_mfma_f32_16x16x32_bf16 v[160:163], a[188:191], a[152:155], v[160:163]\n" \
"ds_read_b128 v[48:51], v23 offset:0\n" \
"v_mfma_f32_16x16x32_bf16 v[160:163], a[252:255], a[216:219], v[160:163]\n" \
"ds_read_b128 a[152:155], v28 offset:12288\n" \
"v_mfma_f32_16x16x32_bf16 v[176:179], a[188:191], a[156:159], v[176:179]\n" \
"ds_read_b128 a[216:219], v29 offset:12288\n" \
"v_mfma_f32_16x16x32_bf16 v[176:179], a[252:255], a[220:223], v[176:179]\n" \
"s_add_u32 s18, s24, s12\n" \
"v_writelane_b32 v11, s11, 9\n" \
"s_addc_u32 s19, s25, 0\n" \
"v_mfma_f32_16x16x32_bf16 v[172:175], a[184:187], a[156:159], v[172:175]\n" \
"s_and_b32 s20, s17, 0xffff\n" \
"v_writelane_b32 v11, s20, 10\n" \
"v_mfma_f32_16x16x32_bf16 v[172:175], a[248:251], a[220:223], v[172:175]\n" \
"s_and_b32 s20, s19, 0xffff\n" \
"v_writelane_b32 v11, s18, 11\n" \
"v_mfma_f32_16x16x32_bf16 v[168:171], a[180:183], a[156:159], v[168:171]\n" \
"s_cmpk_lt_u32 s3, 3\n" \
"s_cselect_b32 s11, s11, s18\n" \
"v_mfma_f32_16x16x32_bf16 v[168:171], a[244:247], a[220:223], v[168:171]\n" \
"s_movk_i32 s18, 0xfff\n" \
"v_writelane_b32 v11, s20, 12\n" \
"v_mfma_f32_16x16x32_bf16 v[164:167], a[176:179], a[156:159], v[164:167]\n" \
"s_and_b32 s20, s29, 0xffff\n" \
"v_writelane_b32 v11, s28, 13\n" \
"v_mfma_f32_16x16x32_bf16 v[164:167], a[240:243], a[220:223], v[164:167]\n" \
"ds_read_b128 a[156:159], v28 offset:14336\n" \
"ds_read_b128 a[220:223], v29 offset:14336\n" \
"s_waitcnt lgkmcnt(15)\n" \
"v_mfma_f32_16x16x32_bf16 a[0:3], a[160:163], a[128:131], a[0:3]\n" \
"s_add_u32 m0, s30, 0x9000\n" \
"s_addk_i32 s84, 0x3000\n" \
"v_mfma_f32_16x16x32_bf16 a[0:3], a[224:227], a[192:195], a[0:3]\n" \
"buffer_load_dwordx4 v24, s[76:79], s84 offen offset:0 lds\n" \
"v_mfma_f32_16x16x32_bf16 a[4:7], a[164:167], a[128:131], a[4:7]\n" \
"ds_read_b64_tr_b16 a[176:177], v36 offset:0\n" \
"v_writelane_b32 v11, s20, 14\n" \
"v_mfma_f32_16x16x32_bf16 a[4:7], a[228:231], a[192:195], a[4:7]\n" \
"ds_read_b64_tr_b16 a[178:179], v36 offset:1024\n" \
"v_writelane_b32 v11, s11, 15\n" \
"v_mfma_f32_16x16x32_bf16 a[12:15], a[172:175], a[128:131], a[12:15]\n" \
"ds_read_b64_tr_b16 a[240:241], v36 offset:8192\n" \
"s_waitcnt lgkmcnt(15)\n" \
"v_mfma_f32_16x16x32_bf16 a[12:15], a[236:239], a[192:195], a[12:15]\n" \
"s_add_u32 s88, s41, 0x2000\n" \
"buffer_store_dwordx4 v[244:247], v20, s[80:83], s88 offen offset:3072 sc1\n" \
"s_waitcnt lgkmcnt(13)\n" \
"v_mfma_f32_16x16x32_bf16 a[8:11], a[128:131], a[168:171], a[8:11]\n" \
"ds_read_b64_tr_b16 a[242:243], v36 offset:9216\n" \
"s_waitcnt lgkmcnt(12)\n" \
"v_mfma_f32_16x16x32_bf16 a[8:11], a[192:195], a[232:235], a[8:11]\n" \
"ds_read_b64_tr_b16 a[180:181], v37 offset:0\n" \
"v_mfma_f32_16x16x32_bf16 a[24:27], a[132:135], a[168:171], a[24:27]\n" \
"ds_read_b64_tr_b16 a[182:183], v37 offset:1024\n" \
"v_mfma_f32_16x16x32_bf16 a[24:27], a[196:199], a[232:235], a[24:27]\n" \
"ds_read_b64_tr_b16 a[244:245], v37 offset:8192\n" \
"v_mfma_f32_16x16x32_bf16 a[28:31], a[172:175], a[132:135], a[28:31]\n" \
"s_cmpk_lt_u32 s3, 3\n" \
"s_cselect_b32 s11, s17, s19\n" \
"v_mfma_f32_16x16x32_bf16 a[28:31], a[236:239], a[196:199], a[28:31]\n" \
"s_addk_i32 s84, 0x2000\n" \
"s_nop 0\n" \
"v_mfma_f32_16x16x32_bf16 a[20:23], a[164:167], a[132:135], a[20:23]\n" \
"buffer_load_dwordx4 v25, s[76:79], s84 offen offset:1024 lds\n" \
"v_mfma_f32_16x16x32_bf16 a[20:23], a[228:231], a[196:199], a[20:23]\n" \
"ds_read_b64_tr_b16 a[246:247], v37 offset:9216\n" \
"s_and_b32 s11, s11, 0xffff\n" \
"v_mfma_f32_16x16x32_bf16 a[16:19], a[160:163], a[132:135], a[16:19]\n" \
"ds_read_b64_tr_b16 a[184:185], v38 offset:0\n" \
"v_writelane_b32 v11, s11, 16\n" \
"v_mfma_f32_16x16x32_bf16 a[16:19], a[224:227], a[196:199], a[16:19]\n" \
"ds_read_b64_tr_b16 a[186:187], v38 offset:1024\n" \
"s_mul_i32 s11, s6, 0x48\n" \
"v_mfma_f32_16x16x32_bf16 a[32:35], a[160:163], a[136:139], a[32:35]\n" \
"s_addk_i32 s84, 0x2000\n" \
"s_nop 0\n" \
"v_mfma_f32_16x16x32_bf16 a[32:35], a[224:227], a[200:203], a[32:35]\n" \
"buffer_load_dwordx4 v24, s[76:79], s84 offen offset:2048 lds\n" \
"v_mfma_f32_16x16x32_bf16 a[36:39], a[164:167], a[136:139], a[36:39]\n" \
"ds_read_b64_tr_b16 a[248:249], v38 offset:8192\n" \
"s_add_i32 s17, s11, 0x47\n" \
"v_mfma_f32_16x16x32_bf16 a[36:39], a[228:231], a[200:203], a[36:39]\n" \
".fill (L_pb + 24288 - .) / 4, 4, 0xBF800000\n" \
"L_pp6:\n" \
"s_cbranch_execz L_pret\n" \
"s_nop 0\n" \
"ds_read_b64_tr_b16 a[250:251], v38 offset:9216\n" \
"v_mfma_f32_16x16x32_bf16 a[44:47], a[172:175], a[136:139], a[44:47]\n" \
"ds_read_b64_tr_b16 a[188:189], v39 offset:0\n" \
"v_mfma_f32_16x16x32_bf16 a[44:47], a[236:239], a[200:203], a[44:47]\n" \
"ds_read_b64_tr_b16 a[190:191], v39 offset:1024\n" \
"v_mfma_f32_16x16x32_bf16 a[40:43], a[136:139], a[168:171], a[40:43]\n" \
"s_add_u32 s89, s41, 0x3000\n" \
"buffer_store_dwordx4 v[184:187], v20, s[80:83], s89 offen offset:3072 sc1\n" \
"v_mfma_f32_16x16x32_bf16 a[40:43], a[200:203], a[232:235], a[40:43]\n" \
"ds_read_b64_tr_b16 a[252:253], v39 offset:8192\n" \
"v_mfma_f32_16x16x32_bf16 a[56:59], a[140:143], a[168:171], a[56:59]\n" \
"ds_read_b64_tr_b16 a[254:255], v39 offset:9216\n" \
"v_mfma_f32_16x16x32_bf16 a[56:59], a[204:207], a[232:235], a[56:59]\n" \
"s_cmpk_lt_u32 s3, 3\n" \
"s_cselect_b32 s17, s17, s18\n" \
"v_mfma_f32_16x16x32_bf16 a[60:63], a[172:175], a[140:143], a[60:63]\n" \
"s_mulk_i32 s17, 0x480\n" \
"s_addk_i32 s17, 0x120\n" \
"v_mfma_f32_16x16x32_bf16 a[60:63], a[236:239], a[204:207], a[60:63]\n" \
"s_addk_i32 s84, 0x2000\n" \
"v_mfma_f32_16x16x32_bf16 a[52:55], a[164:167], a[140:143], a[52:55]\n" \
"buffer_load_dwordx4 v25, s[76:79], s84 offen offset:3072 lds\n" \
"v_mfma_f32_16x16x32_bf16 a[52:55], a[228:231], a[204:207], a[52:55]\n" \
"v_writelane_b32 v11, s17, 17\n" \
"s_lshl_b32 s17, s3, 5\n" \
"v_mfma_f32_16x16x32_bf16 a[48:51], a[160:163], a[140:143], a[48:51]\n" \
"s_add_i32 s11, s11, s17\n" \
"s_mul_i32 s17, s11, 0x480\n" \
"v_mfma_f32_16x16x32_bf16 a[48:51], a[224:227], a[204:207], a[48:51]\n" \
"s_mul_i32 s11, s13, 0x24000\n" \
"s_add_i32 s18, s11, 0x1b000\n" \
"s_waitcnt lgkmcnt(15)\n" \
"v_mfma_f32_16x16x32_bf16 a[64:67], a[164:167], v[248:251], a[64:67]\n" \
"s_cmpk_lt_u32 s3, 3\n" \
"s_cselect_b32 s17, s17, s18\n" \
"v_mfma_f32_16x16x32_bf16 a[64:67], a[228:231], v[48:51], a[64:67]\n" \
"s_mov_b32 s18, 0x8000\n" \
"v_writelane_b32 v11, s17, 18\n" \
"v_mfma_f32_16x16x32_bf16 a[80:83], a[172:175], v[248:251], a[80:83]\n" \
"s_mul_i32 s17, s16, 0x2400\n" \
"s_mul_i32 s16, s16, 0x9000\n" \
"v_mfma_f32_16x16x32_bf16 a[80:83], a[236:239], v[48:51], a[80:83]\n" \
"s_bitset1_b32 s17, 17\n" \
"v_writelane_b32 v11, s17, 19\n" \
"v_mfma_f32_16x16x32_bf16 a[68:71], v[248:251], a[168:171], a[68:71]\n" \
"s_add_i32 s16, s11, s16\n" \
"v_writelane_b32 v11, s16, 26\n" \
"s_waitcnt vmcnt(11) lgkmcnt(0)\n" \
"v_mfma_f32_16x16x32_bf16 a[68:71], v[48:51], a[232:235], a[68:71]\n" \
"s_barrier\n" \
"v_xor_b32_e32 v28, 0x20000, v28\n" \
"v_xor_b32_e32 v29, 0x20000, v29\n" \
"v_mfma_f32_16x16x32_bf16 v[52:55], a[176:179], a[128:131], v[52:55]\n" \
"s_add_u32 m0, s40, 0x0\n" \
"s_nop 0\n" \
"s_add_u32 s85, s39, 0x8000\n" \
"v_mfma_f32_16x16x32_bf16 v[52:55], a[240:243], a[192:195], v[52:55]\n" \
"buffer_load_dwordx4 v20, s[80:83], s85 offen sc1 lds\n" \
"v_mfma_f32_16x16x32_bf16 v[56:59], a[180:183], a[128:131], v[56:59]\n" \
"ds_read_b64_tr_b16 a[160:161], v32 offset:32768\n" \
"s_mul_i32 s16, s3, 0x2400\n" \
"v_mfma_f32_16x16x32_bf16 v[56:59], a[244:247], a[192:195], v[56:59]\n" \
"ds_read_b64_tr_b16 a[162:163], v32 offset:33280\n" \
"s_add_i32 s17, s16, 0xa400\n" \
"v_mfma_f32_16x16x32_bf16 v[60:63], a[184:187], a[128:131], v[60:63]\n" \
"s_add_u32 m0, s40, 0x1000\n" \
"s_add_u32 s85, s39, 0x9000\n" \
"v_mfma_f32_16x16x32_bf16 v[60:63], a[248:251], a[192:195], v[60:63]\n" \
"buffer_load_dwordx4 v20, s[80:83], s85 offen sc1 lds\n" \
"v_mfma_f32_16x16x32_bf16 v[64:67], a[188:191], a[128:131], v[64:67]\n" \
"ds_read_b64_tr_b16 a[224:225], v32 offset:36864\n" \
"v_mfma_f32_16x16x32_bf16 v[64:67], a[252:255], a[192:195], v[64:67]\n" \
"ds_read_b128 a[128:131], v28 offset:0\n" \
"v_mfma_f32_16x16x32_bf16 v[80:83], a[188:191], a[132:135], v[80:83]\n" \
"ds_read_b128 a[192:195], v29 offset:0\n" \
"v_mfma_f32_16x16x32_bf16 v[80:83], a[252:255], a[196:199], v[80:83]\n" \
"s_add_u32 s88, s41, 0x3000\n" \
"buffer_store_dwordx4 v[188:191], v20, s[80:83], s88 offen offset:2048 sc1\n" \
"v_mfma_f32_16x16x32_bf16 v[76:79], a[184:187], a[132:135], v[76:79]\n" \
"ds_read_b64_tr_b16 a[226:227], v32 offset:37376\n" \
"v_mfma_f32_16x16x32_bf16 v[76:79], a[248:251], a[196:199], v[76:79]\n" \
"ds_read_b64_tr_b16 a[164:165], v33 offset:32768\n" \
"v_mfma_f32_16x16x32_bf16 v[72:75], a[180:183], a[132:135], v[72:75]\n" \
"s_add_u32 m0, s40, 0x3000\n" \
"s_movk_i32 s84, 0x200\n" \
"v_mfma_f32_16x16x32_bf16 v[72:75], a[244:247], a[196:199], v[72:75]\n" \
"buffer_load_dwordx4 v21, s[76:79], s84 offen offset:0 lds\n" \
"v_mfma_f32_16x16x32_bf16 v[68:71], a[176:179], a[132:135], v[68:71]\n" \
"ds_read_b64_tr_b16 a[166:167], v33 offset:33280\n" \
"v_mfma_f32_16x16x32_bf16 v[68:71], a[240:243], a[196:199], v[68:71]\n" \
"ds_read_b128 a[132:135], v28 offset:2048\n" \
"v_mfma_f32_16x16x32_bf16 v[84:87], a[176:179], a[136:139], v[84:87]\n" \
"ds_read_b128 a[196:199], v29 offset:2048\n" \
"v_mfma_f32_16x16x32_bf16 v[84:87], a[240:243], a[200:203], v[84:87]\n" \
"s_add_u32 m0, s31, 0x0\n" \
"s_mov_b32 s84, 0x200000\n" \
"v_mfma_f32_16x16x32_bf16 v[88:91], a[180:183], a[136:139], v[88:91]\n" \
"buffer_load_dwordx4 v26, s[68:71], s84 offen offset:0 lds\n" \
"v_mfma_f32_16x16x32_bf16 v[88:91], a[244:247], a[200:203], v[88:91]\n" \
"ds_read_b64_tr_b16 a[228:229], v33 offset:36864\n" \
"v_mfma_f32_16x16x32_bf16 v[92:95], a[184:187], a[136:139], v[92:95]\n" \
"ds_read_b64_tr_b16 a[230:231], v33 offset:37376\n" \
"v_mfma_f32_16x16x32_bf16 v[92:95], a[248:251], a[200:203], v[92:95]\n" \
"s_addk_i32 s84, 0x7c00\n" \
"v_mfma_f32_16x16x32_bf16 v[96:99], a[188:191], a[136:139], v[96:99]\n" \
"buffer_load_dwordx4 v26, s[68:71], s84 offen offset:1024 lds\n" \
"v_mfma_f32_16x16x32_bf16 v[96:99], a[252:255], a[200:203], v[96:99]\n" \
"ds_read_b128 a[136:139], v28 offset:4096\n" \
"v_mfma_f32_16x16x32_bf16 v[112:115], a[188:191], a[140:143], v[112:115]\n" \
"ds_read_b128 a[200:203], v29 offset:4096\n" \
"v_mfma_f32_16x16x32_bf16 v[112:115], a[252:255], a[204:207], v[112:115]\n" \
"s_addk_i32 s84, 0x7c00\n" \
"v_mfma_f32_16x16x32_bf16 v[108:111], a[184:187], a[140:143], v[108:111]\n" \
"buffer_load_dwordx4 v27, s[68:71], s84 offen offset:2048 lds\n" \
"v_mfma_f32_16x16x32_bf16 v[108:111], a[248:251], a[204:207], v[108:111]\n" \
"ds_read_b64_tr_b16 a[172:173], v35 offset:32768\n" \
"v_mfma_f32_16x16x32_bf16 v[104:107], a[180:183], a[140:143], v[104:107]\n" \
"ds_read_b64_tr_b16 a[174:175], v35 offset:33280\n" \
"v_mfma_f32_16x16x32_bf16 v[104:107], a[244:247], a[204:207], v[104:107]\n" \
"ds_read_b64_tr_b16 a[236:237], v35 offset:36864\n" \
"v_mfma_f32_16x16x32_bf16 v[100:103], a[176:179], a[140:143], v[100:103]\n" \
"s_cmpk_lt_u32 s3, 3\n" \
"s_cselect_b32 s17, s17, s18\n" \
"v_mfma_f32_16x16x32_bf16 v[100:103], a[240:243], a[204:207], v[100:103]\n" \
"ds_read_b128 a[140:143], v28 offset:6144\n" \
"s_mov_b32 s18, 0x1b000\n" \
"v_mfma_f32_16x16x32_bf16 v[116:119], a[176:179], a[144:147], v[116:119]\n" \
"ds_read_b128 a[204:207], v29 offset:6144\n" \
"s_xor_b32 s19, s3, 2\n" \
"v_mfma_f32_16x16x32_bf16 v[116:119], a[240:243], a[208:211], v[116:119]\n" \
"s_addk_i32 s84, 0x7c00\n" \
"v_mfma_f32_16x16x32_bf16 v[120:123], a[180:183], a[144:147], v[120:123]\n" \
"buffer_load_dwordx4 v27, s[68:71], s84 offen offset:3072 lds\n" \
"v_mfma_f32_16x16x32_bf16 v[120:123], a[244:247], a[208:211], v[120:123]\n" \
"s_add_u32 m0, s30, 0x20000\n" \
"s_movk_i32 s84, 0x280\n" \
"v_mfma_f32_16x16x32_bf16 v[124:127], a[184:187], a[144:147], v[124:127]\n" \
"buffer_load_dwordx4 v24, s[76:79], s84 offen offset:0 lds\n" \
"v_mfma_f32_16x16x32_bf16 v[124:127], a[248:251], a[208:211], v[124:127]\n" \
"ds_read_b64_tr_b16 a[238:239], v35 offset:37376\n" \
"v_mfma_f32_16x16x32_bf16 v[128:131], a[188:191], a[144:147], v[128:131]\n" \
"s_add_u32 s89, s42, 0x0\n" \
"buffer_store_dwordx4 v[216:219], v20, s[80:83], s89 offen offset:0 sc1\n" \
"v_mfma_f32_16x16x32_bf16 v[128:131], a[252:255], a[208:211], v[128:131]\n" \
"ds_read_b128 a[144:147], v28 offset:8192\n" \
"v_mfma_f32_16x16x32_bf16 v[144:147], a[188:191], a[148:151], v[144:147]\n" \
"ds_read_b128 a[208:211], v29 offset:8192\n" \
"v_mfma_f32_16x16x32_bf16 v[144:147], a[252:255], a[212:215], v[144:147]\n" \
"ds_read_b64_tr_b16 a[168:169], v34 offset:32768\n" \
"v_mfma_f32_16x16x32_bf16 v[140:143], a[184:187], a[148:151], v[140:143]\n" \
"ds_read_b64_tr_b16 a[170:171], v34 offset:33280\n" \
"v_mfma_f32_16x16x32_bf16 v[140:143], a[248:251], a[212:215], v[140:143]\n" \
"s_movk_i32 s84, 0x2280\n" \
"s_nop 0\n" \
"v_mfma_f32_16x16x32_bf16 v[136:139], a[180:183], a[148:151], v[136:139]\n" \
"buffer_load_dwordx4 v25, s[76:79], s84 offen offset:1024 lds\n" \
"v_mfma_f32_16x16x32_bf16 v[136:139], a[244:247], a[212:215], v[136:139]\n" \
"ds_read_b64_tr_b16 a[232:233], v34 offset:36864\n" \
"v_mfma_f32_16x16x32_bf16 v[132:135], a[176:179], a[148:151], v[132:135]\n" \
"ds_read_b64_tr_b16 a[234:235], v34 offset:37376\n" \
"v_mfma_f32_16x16x32_bf16 v[132:135], a[240:243], a[212:215], v[132:135]\n" \
"ds_read_b128 a[148:151], v28 offset:10240\n" \
"v_mfma_f32_16x16x32_bf16 v[148:151], a[176:179], a[152:155], v[148:151]\n" \
"ds_read_b128 a[212:215], v29 offset:10240\n" \
"v_mfma_f32_16x16x32_bf16 v[148:151], a[240:243], a[216:219], v[148:151]\n" \
"s_movk_i32 s84, 0x4280\n" \
"v_mfma_f32_16x16x32_bf16 v[152:155], a[180:183], a[152:155], v[152:155]\n" \
"buffer_load_dwordx4 v24, s[76:79], s84 offen offset:2048 lds\n" \
"v_mfma_f32_16x16x32_bf16 v[152:155], a[244:247], a[216:219], v[152:155]\n" \
"s_movk_i32 s84, 0x6280\n" \
"v_mfma_f32_16x16x32_bf16 v[156:159], a[184:187], a[152:155], v[156:159]\n" \
"buffer_load_dwordx4 v25, s[76:79], s84 offen offset:3072 lds\n" \
"v_mfma_f32_16x16x32_bf16 v[156:159], a[248:251], a[216:219], v[156:159]\n" \
"ds_read_b128 v[248:251], v22 offset:32768\n" \
"v_mfma_f32_16x16x32_bf16 v[160:163], a[188:191], a[152:155], v[160:163]\n" \
"ds_read_b128 v[48:51], v23 offset:32768\n" \
"v_mfma_f32_16x16x32_bf16 v[160:163], a[252:255], a[216:219], v[160:163]\n" \
"ds_read_b128 a[152:155], v28 offset:12288\n" \
"v_mfma_f32_16x16x32_bf16 v[176:179], a[188:191], a[156:159], v[176:179]\n" \
"ds_read_b128 a[216:219], v29 offset:12288\n" \
"v_mfma_f32_16x16x32_bf16 v[176:179], a[252:255], a[220:223], v[176:179]\n" \
"s_cmp_lg_u32 s19, 0\n" \
"s_cselect_b32 s18, s17, s18\n" \
"v_mfma_f32_16x16x32_bf16 v[172:175], a[184:187], a[156:159], v[172:175]\n" \
"v_writelane_b32 v11, s17, 20\n" \
"s_or_b32 s17, s16, 0x10000\n" \
"v_mfma_f32_16x16x32_bf16 v[172:175], a[248:251], a[220:223], v[172:175]\n" \
"v_writelane_b32 v11, s18, 21\n" \
"s_add_i32 s18, s16, 0x19000\n" \
"v_mfma_f32_16x16x32_bf16 v[168:171], a[180:183], a[156:159], v[168:171]\n" \
"v_writelane_b32 v11, s17, 22\n" \
"s_mov_b32 s17, 0\n" \
"v_mfma_f32_16x16x32_bf16 v[168:171], a[244:247], a[220:223], v[168:171]\n" \
"s_cmpk_lt_u32 s3, 3\n" \
"s_cselect_b32 s18, s18, s17\n" \
"v_mfma_f32_16x16x32_bf16 v[164:167], a[176:179], a[156:159], v[164:167]\n" \
"s_bitset1_b32 s16, 17\n" \
"v_writelane_b32 v11, s18, 23\n" \
"v_mfma_f32_16x16x32_bf16 v[164:167], a[240:243], a[220:223], v[164:167]\n" \
"ds_read_b128 a[156:159], v28 offset:14336\n" \
"ds_read_b128 a[220:223], v29 offset:14336\n" \
"s_waitcnt lgkmcnt(15)\n" \
"v_mfma_f32_16x16x32_bf16 a[0:3], a[160:163], a[128:131], a[0:3]\n" \
"s_add_u32 m0, s30, 0x21000\n" \
"s_addk_i32 s84, 0x3000\n" \
"v_mfma_f32_16x16x32_bf16 a[0:3], a[224:227], a[192:195], a[0:3]\n" \
"buffer_load_dwordx4 v24, s[76:79], s84 offen offset:0 lds\n" \
"v_mfma_f32_16x16x32_bf16 a[4:7], a[164:167], a[128:131], a[4:7]\n" \
"ds_read_b64_tr_b16 a[176:177], v36 offset:32768\n" \
"s_movk_i32 s18, 0x2400\n" \
"v_mfma_f32_16x16x32_bf16 a[4:7], a[228:231], a[192:195], a[4:7]\n" \
"ds_read_b64_tr_b16 a[178:179], v36 offset:33792\n" \
"v_mfma_f32_16x16x32_bf16 a[12:15], a[172:175], a[128:131], a[12:15]\n" \
"ds_read_b64_tr_b16 a[240:241], v36 offset:40960\n" \
"s_waitcnt lgkmcnt(15)\n" \
"v_mfma_f32_16x16x32_bf16 a[12:15], a[236:239], a[192:195], a[12:15]\n" \
"s_add_u32 s88, s42, 0x1000\n" \
"buffer_store_dwordx4 v[220:223], v20, s[80:83], s88 offen offset:0 sc1\n" \
"s_waitcnt lgkmcnt(13)\n" \
"v_mfma_f32_16x16x32_bf16 a[8:11], a[128:131], a[168:171], a[8:11]\n" \
"ds_read_b64_tr_b16 a[242:243], v36 offset:41984\n" \
"s_waitcnt lgkmcnt(12)\n" \
"v_mfma_f32_16x16x32_bf16 a[8:11], a[192:195], a[232:235], a[8:11]\n" \
"ds_read_b64_tr_b16 a[180:181], v37 offset:32768\n" \
"v_mfma_f32_16x16x32_bf16 a[24:27], a[132:135], a[168:171], a[24:27]\n" \
"ds_read_b64_tr_b16 a[182:183], v37 offset:33792\n" \
"v_mfma_f32_16x16x32_bf16 a[24:27], a[196:199], a[232:235], a[24:27]\n" \
"ds_read_b64_tr_b16 a[244:245], v37 offset:40960\n" \
"v_mfma_f32_16x16x32_bf16 a[28:31], a[172:175], a[132:135], a[28:31]\n" \
"s_cmpk_lt_u32 s3, 3\n" \
"s_cselect_b32 s16, s16, s18\n" \
"v_mfma_f32_16x16x32_bf16 a[28:31], a[236:239], a[196:199], a[28:31]\n" \
"s_addk_i32 s84, 0x2000\n" \
"v_mfma_f32_16x16x32_bf16 a[20:23], a[164:167], a[132:135], a[20:23]\n" \
"buffer_load_dwordx4 v25, s[76:79], s84 offen offset:1024 lds\n" \
"v_mfma_f32_16x16x32_bf16 a[20:23], a[228:231], a[196:199], a[20:23]\n" \
"ds_read_b64_tr_b16 a[246:247], v37 offset:41984\n" \
"s_mul_i32 s19, s3, 0x9000\n" \
"v_mfma_f32_16x16x32_bf16 a[16:19], a[160:163], a[132:135], a[16:19]\n" \
"ds_read_b64_tr_b16 a[184:185], v38 offset:32768\n" \
"v_writelane_b32 v11, s16, 24\n" \
"v_mfma_f32_16x16x32_bf16 a[16:19], a[224:227], a[196:199], a[16:19]\n" \
"ds_read_b64_tr_b16 a[186:187], v38 offset:33792\n" \
"s_lshl_b32 s16, s3, 10\n" \
"v_mfma_f32_16x16x32_bf16 a[32:35], a[160:163], a[136:139], a[32:35]\n" \
"s_addk_i32 s84, 0x2000\n" \
"v_mfma_f32_16x16x32_bf16 a[32:35], a[224:227], a[200:203], a[32:35]\n" \
"buffer_load_dwordx4 v24, s[76:79], s84 offen offset:2048 lds\n" \
"v_mfma_f32_16x16x32_bf16 a[36:39], a[164:167], a[136:139], a[36:39]\n" \
"ds_read_b64_tr_b16 a[248:249], v38 offset:40960\n" \
"s_addk_i32 s16, 0x4800\n" \
"v_mfma_f32_16x16x32_bf16 a[36:39], a[228:231], a[200:203], a[36:39]\n" \
"ds_read_b64_tr_b16 a[250:251], v38 offset:41984\n" \
"v_writelane_b32 v11, s16, 25\n" \
"v_mfma_f32_16x16x32_bf16 a[44:47], a[172:175], a[136:139], a[44:47]\n" \
"ds_read_b64_tr_b16 a[188:189], v39 offset:32768\n" \
"s_add_i32 s16, s15, 1\n" \
"v_mfma_f32_16x16x32_bf16 a[44:47], a[236:239], a[200:203], a[44:47]\n" \
"ds_read_b64_tr_b16 a[190:191], v39 offset:33792\n" \
"s_and_b32 s16, s16, 3\n" \
"v_mfma_f32_16x16x32_bf16 a[40:43], a[136:139], a[168:171], a[40:43]\n" \
"s_add_u32 s89, s42, 0x0\n" \
"buffer_store_dwordx4 v[192:195], v20, s[80:83], s89 offen offset:2048 sc1\n" \
"v_mfma_f32_16x16x32_bf16 a[40:43], a[200:203], a[232:235], a[40:43]\n" \
"ds_read_b64_tr_b16 a[252:253], v39 offset:40960\n" \
"s_or_b32 s16, s16, s4\n" \
"v_mfma_f32_16x16x32_bf16 a[56:59], a[140:143], a[168:171], a[56:59]\n" \
"ds_read_b64_tr_b16 a[254:255], v39 offset:41984\n" \
"s_mul_i32 s18, s16, 0x24000\n" \
"v_mfma_f32_16x16x32_bf16 a[56:59], a[204:207], a[232:235], a[56:59]\n" \
"s_add_i32 s18, s18, s19\n" \
"s_xor_b32 s13, s13, 2\n" \
"v_mfma_f32_16x16x32_bf16 a[60:63], a[172:175], a[140:143], a[60:63]\n" \
"s_add_i32 s15, s15, -1\n" \
"v_writelane_b32 v11, s18, 27\n" \
"v_mfma_f32_16x16x32_bf16 a[60:63], a[236:239], a[204:207], a[60:63]\n" \
"s_addk_i32 s84, 0x2000\n" \
"v_mfma_f32_16x16x32_bf16 a[52:55], a[164:167], a[140:143], a[52:55]\n" \
"buffer_load_dwordx4 v25, s[76:79], s84 offen offset:3072 lds\n" \
"v_mfma_f32_16x16x32_bf16 a[52:55], a[228:231], a[204:207], a[52:55]\n" \
"s_mul_i32 s18, s13, 0x24000\n" \
"s_and_b32 s15, s15, 3\n" \
"v_mfma_f32_16x16x32_bf16 a[48:51], a[160:163], a[140:143], a[48:51]\n" \
"s_add_i32 s18, s18, s19\n" \
"s_or_b32 s15, s15, s4\n" \
"v_mfma_f32_16x16x32_bf16 a[48:51], a[224:227], a[204:207], a[48:51]\n" \
"v_writelane_b32 v11, s18, 28\n" \
"s_mul_i32 s18, s15, 0x24000\n" \
"s_waitcnt lgkmcnt(15)\n" \
"v_mfma_f32_16x16x32_bf16 a[64:67], a[164:167], v[248:251], a[64:67]\n" \
"s_add_i32 s18, s18, s19\n" \
"v_writelane_b32 v11, s18, 29\n" \
"v_mfma_f32_16x16x32_bf16 a[64:67], a[228:231], v[48:51], a[64:67]\n" \
"s_or_b32 s18, s9, s14\n" \
"s_mul_i32 s18, s18, 0x24800\n" \
"v_mfma_f32_16x16x32_bf16 a[80:83], a[172:175], v[248:251], a[80:83]\n" \
"s_lshl_b32 s16, s16, 12\n" \
"s_add_i32 s19, s18, 0x3004800\n" \
"v_mfma_f32_16x16x32_bf16 a[80:83], a[236:239], v[48:51], a[80:83]\n" \
"s_add_i32 s20, s19, s16\n" \
"v_writelane_b32 v11, s20, 30\n" \
"v_mfma_f32_16x16x32_bf16 a[68:71], v[248:251], a[168:171], a[68:71]\n" \
"s_lshl_b32 s13, s13, 12\n" \
"s_lshl_b32 s15, s15, 12\n" \
"s_waitcnt vmcnt(11) lgkmcnt(0)\n" \
"v_mfma_f32_16x16x32_bf16 a[68:71], v[48:51], a[232:235], a[68:71]\n" \
"s_barrier\n" \
"v_mfma_f32_16x16x32_bf16 v[52:55], a[176:179], a[128:131], v[52:55]\n" \
"s_add_u32 m0, s40, 0x8000\n" \
"s_add_u32 s85, s39, 0xa000\n" \
"v_mfma_f32_16x16x32_bf16 v[52:55], a[240:243], a[192:195], v[52:55]\n" \
"buffer_load_dwordx4 v20, s[80:83], s85 offen sc1 lds\n" \
"v_mfma_f32_16x16x32_bf16 v[56:59], a[180:183], a[128:131], v[56:59]\n" \
"ds_read_b64_tr_b16 a[160:161], v32 offset:0\n" \
"v_accvgpr_read_b32 v224, a120\n" \
"v_mfma_f32_16x16x32_bf16 v[56:59], a[244:247], a[192:195], v[56:59]\n" \
"ds_read_b64_tr_b16 a[162:163], v32 offset:512\n" \
"v_accvgpr_read_b32 v225, a121\n" \
"v_mfma_f32_16x16x32_bf16 v[60:63], a[184:187], a[128:131], v[60:63]\n" \
"s_add_u32 m0, s40, 0x9000\n" \
"s_add_u32 s85, s39, 0xb000\n" \
"v_mfma_f32_16x16x32_bf16 v[60:63], a[248:251], a[192:195], v[60:63]\n" \
"buffer_load_dwordx4 v20, s[80:83], s85 offen sc1 lds\n" \
"v_mfma_f32_16x16x32_bf16 v[64:67], a[188:191], a[128:131], v[64:67]\n" \
"ds_read_b64_tr_b16 a[224:225], v32 offset:4096\n" \
"v_accvgpr_read_b32 v226, a122\n" \
"v_mfma_f32_16x16x32_bf16 v[64:67], a[252:255], a[192:195], v[64:67]\n" \
"ds_read_b128 a[128:131], v28 offset:32768\n" \
"v_accvgpr_read_b32 v227, a123\n" \
"v_mfma_f32_16x16x32_bf16 v[80:83], a[188:191], a[132:135], v[80:83]\n" \
"ds_read_b128 a[192:195], v29 offset:32768\n" \
"v_accvgpr_read_b32 v180, a124\n" \
"v_mfma_f32_16x16x32_bf16 v[80:83], a[252:255], a[196:199], v[80:83]\n" \
"s_add_u32 s88, s42, 0x0\n" \
"buffer_store_dwordx4 v[196:199], v20, s[80:83], s88 offen offset:3072 sc1\n" \
"v_mfma_f32_16x16x32_bf16 v[76:79], a[184:187], a[132:135], v[76:79]\n" \
"ds_read_b64_tr_b16 a[226:227], v32 offset:4608\n" \
"v_accvgpr_read_b32 v181, a125\n" \
"v_mfma_f32_16x16x32_bf16 v[76:79], a[248:251], a[196:199], v[76:79]\n" \
"ds_read_b64_tr_b16 a[164:165], v33 offset:0\n" \
"v_accvgpr_read_b32 v182, a126\n" \
"v_mfma_f32_16x16x32_bf16 v[72:75], a[180:183], a[132:135], v[72:75]\n" \
"s_add_u32 m0, s40, 0xb000\n" \
"s_movk_i32 s84, 0x280\n" \
"v_mfma_f32_16x16x32_bf16 v[72:75], a[244:247], a[196:199], v[72:75]\n" \
"buffer_load_dwordx4 v21, s[76:79], s84 offen offset:0 lds\n" \
"v_mfma_f32_16x16x32_bf16 v[68:71], a[176:179], a[132:135], v[68:71]\n" \
"ds_read_b64_tr_b16 a[166:167], v33 offset:512\n" \
"v_accvgpr_read_b32 v183, a127\n" \
"v_mfma_f32_16x16x32_bf16 v[68:71], a[240:243], a[196:199], v[68:71]\n" \
"ds_read_b128 a[132:135], v28 offset:34816\n" \
"v_cvt_pk_bf16_f32 v224, v224, v225\n" \
"v_mfma_f32_16x16x32_bf16 v[84:87], a[176:179], a[136:139], v[84:87]\n" \
"ds_read_b128 a[196:199], v29 offset:34816\n" \
"v_cvt_pk_bf16_f32 v225, v226, v227\n" \
"v_mfma_f32_16x16x32_bf16 v[84:87], a[240:243], a[200:203], v[84:87]\n" \
"s_add_u32 m0, s31, 0x8000\n" \
"s_mov_b32 s84, 0x280000\n" \
"v_mfma_f32_16x16x32_bf16 v[88:91], a[180:183], a[136:139], v[88:91]\n" \
"buffer_load_dwordx4 v26, s[68:71], s84 offen offset:0 lds\n" \
"v_mfma_f32_16x16x32_bf16 v[88:91], a[244:247], a[200:203], v[88:91]\n" \
"ds_read_b64_tr_b16 a[228:229], v33 offset:4096\n" \
"v_cvt_pk_bf16_f32 v226, v180, v181\n" \
"v_mfma_f32_16x16x32_bf16 v[92:95], a[184:187], a[136:139], v[92:95]\n" \
"ds_read_b64_tr_b16 a[230:231], v33 offset:4608\n" \
"v_cvt_pk_bf16_f32 v227, v182, v183\n" \
"v_mfma_f32_16x16x32_bf16 v[92:95], a[248:251], a[200:203], v[92:95]\n" \
"s_addk_i32 s84, 0x7c00\n" \
"v_mfma_f32_16x16x32_bf16 v[96:99], a[188:191], a[136:139], v[96:99]\n" \
"buffer_load_dwordx4 v26, s[68:71], s84 offen offset:1024 lds\n" \
"v_mfma_f32_16x16x32_bf16 v[96:99], a[252:255], a[200:203], v[96:99]\n" \
"ds_read_b128 a[136:139], v28 offset:36864\n" \
"v_accvgpr_read_b32 v220, a112\n" \
"v_mfma_f32_16x16x32_bf16 v[112:115], a[188:191], a[140:143], v[112:115]\n" \
"ds_read_b128 a[200:203], v29 offset:36864\n" \
"v_accvgpr_read_b32 v221, a113\n" \
"v_mfma_f32_16x16x32_bf16 v[112:115], a[252:255], a[204:207], v[112:115]\n" \
"s_addk_i32 s84, 0x7c00\n" \
"v_mfma_f32_16x16x32_bf16 v[108:111], a[184:187], a[140:143], v[108:111]\n" \
"buffer_load_dwordx4 v27, s[68:71], s84 offen offset:2048 lds\n" \
"v_mfma_f32_16x16x32_bf16 v[108:111], a[248:251], a[204:207], v[108:111]\n" \
"ds_read_b64_tr_b16 a[172:173], v35 offset:0\n" \
"v_accvgpr_read_b32 v222, a114\n" \
"v_mfma_f32_16x16x32_bf16 v[104:107], a[180:183], a[140:143], v[104:107]\n" \
"ds_read_b64_tr_b16 a[174:175], v35 offset:512\n" \
"v_accvgpr_read_b32 v223, a115\n" \
"v_mfma_f32_16x16x32_bf16 v[104:107], a[244:247], a[204:207], v[104:107]\n" \
"ds_read_b64_tr_b16 a[236:237], v35 offset:4096\n" \
"v_accvgpr_read_b32 v180, a116\n" \
"v_mfma_f32_16x16x32_bf16 v[100:103], a[176:179], a[140:143], v[100:103]\n" \
"v_accvgpr_read_b32 v181, a117\n" \
"v_accvgpr_read_b32 v182, a118\n" \
"v_mfma_f32_16x16x32_bf16 v[100:103], a[240:243], a[204:207], v[100:103]\n" \
"ds_read_b128 a[140:143], v28 offset:38912\n" \
"v_accvgpr_read_b32 v183, a119\n" \
"v_mfma_f32_16x16x32_bf16 v[116:119], a[176:179], a[144:147], v[116:119]\n" \
"ds_read_b128 a[204:207], v29 offset:38912\n" \
"v_cvt_pk_bf16_f32 v220, v220, v221\n" \
"v_mfma_f32_16x16x32_bf16 v[116:119], a[240:243], a[208:211], v[116:119]\n" \
"s_addk_i32 s84, 0x7c00\n" \
"v_mfma_f32_16x16x32_bf16 v[120:123], a[180:183], a[144:147], v[120:123]\n" \
"buffer_load_dwordx4 v27, s[68:71], s84 offen offset:3072 lds\n" \
"v_mfma_f32_16x16x32_bf16 v[120:123], a[244:247], a[208:211], v[120:123]\n" \
"s_add_u32 m0, s30, 0x0\n" \
"s_movk_i32 s84, 0x300\n" \
"s_nop 0\n" \
"v_mfma_f32_16x16x32_bf16 v[124:127], a[184:187], a[144:147], v[124:127]\n" \
"buffer_load_dwordx4 v24, s[76:79], s84 offen offset:0 lds\n" \
"v_mfma_f32_16x16x32_bf16 v[124:127], a[248:251], a[208:211], v[124:127]\n" \
"ds_read_b64_tr_b16 a[238:239], v35 offset:4608\n" \
"v_cvt_pk_bf16_f32 v221, v222, v223\n" \
"v_mfma_f32_16x16x32_bf16 v[128:131], a[188:191], a[144:147], v[128:131]\n" \
"s_add_u32 s89, s42, 0x1000\n" \
"buffer_store_dwordx4 a[84:87], v20, s[80:83], s89 offen offset:3072 sc1\n" \
"v_mfma_f32_16x16x32_bf16 v[128:131], a[252:255], a[208:211], v[128:131]\n" \
"ds_read_b128 a[144:147], v28 offset:40960\n" \
"v_cvt_pk_bf16_f32 v222, v180, v181\n" \
"v_mfma_f32_16x16x32_bf16 v[144:147], a[188:191], a[148:151], v[144:147]\n" \
"ds_read_b128 a[208:211], v29 offset:40960\n" \
"v_cvt_pk_bf16_f32 v223, v182, v183\n" \
"v_mfma_f32_16x16x32_bf16 v[144:147], a[252:255], a[212:215], v[144:147]\n" \
"ds_read_b64_tr_b16 a[168:169], v34 offset:0\n" \
"v_accvgpr_read_b32 v216, a104\n" \
"v_mfma_f32_16x16x32_bf16 v[140:143], a[184:187], a[148:151], v[140:143]\n" \
"ds_read_b64_tr_b16 a[170:171], v34 offset:512\n" \
"v_accvgpr_read_b32 v217, a105\n" \
"v_mfma_f32_16x16x32_bf16 v[140:143], a[248:251], a[212:215], v[140:143]\n" \
"s_movk_i32 s84, 0x2300\n" \
"s_nop 0\n" \
"v_mfma_f32_16x16x32_bf16 v[136:139], a[180:183], a[148:151], v[136:139]\n" \
"buffer_load_dwordx4 v25, s[76:79], s84 offen offset:1024 lds\n" \
"v_mfma_f32_16x16x32_bf16 v[136:139], a[244:247], a[212:215], v[136:139]\n" \
"ds_read_b64_tr_b16 a[232:233], v34 offset:4096\n" \
"v_accvgpr_read_b32 v218, a106\n" \
"v_mfma_f32_16x16x32_bf16 v[132:135], a[176:179], a[148:151], v[132:135]\n" \
"ds_read_b64_tr_b16 a[234:235], v34 offset:4608\n" \
"v_accvgpr_read_b32 v219, a107\n" \
"v_mfma_f32_16x16x32_bf16 v[132:135], a[240:243], a[212:215], v[132:135]\n" \
"ds_read_b128 a[148:151], v28 offset:43008\n" \
"v_accvgpr_read_b32 v180, a108\n" \
"v_mfma_f32_16x16x32_bf16 v[148:151], a[176:179], a[152:155], v[148:151]\n" \
"ds_read_b128 a[212:215], v29 offset:43008\n" \
"v_accvgpr_read_b32 v181, a109\n" \
"v_mfma_f32_16x16x32_bf16 v[148:151], a[240:243], a[216:219], v[148:151]\n" \
"s_movk_i32 s84, 0x4300\n" \
"v_mfma_f32_16x16x32_bf16 v[152:155], a[180:183], a[152:155], v[152:155]\n" \
"buffer_load_dwordx4 v24, s[76:79], s84 offen offset:2048 lds\n" \
"v_mfma_f32_16x16x32_bf16 v[152:155], a[244:247], a[216:219], v[152:155]\n" \
"s_movk_i32 s84, 0x6300\n" \
"v_mfma_f32_16x16x32_bf16 v[156:159], a[184:187], a[152:155], v[156:159]\n" \
"buffer_load_dwordx4 v25, s[76:79], s84 offen offset:3072 lds\n" \
"v_mfma_f32_16x16x32_bf16 v[156:159], a[248:251], a[216:219], v[156:159]\n" \
"ds_read_b128 v[248:251], v22 offset:0\n" \
"v_accvgpr_read_b32 v182, a110\n" \
"v_mfma_f32_16x16x32_bf16 v[160:163], a[188:191], a[152:155], v[160:163]\n" \
"ds_read_b128 v[48:51], v23 offset:0\n" \
"v_accvgpr_read_b32 v183, a111\n" \
"v_mfma_f32_16x16x32_bf16 v[160:163], a[252:255], a[216:219], v[160:163]\n" \
"ds_read_b128 a[152:155], v28 offset:45056\n" \
"v_cvt_pk_bf16_f32 v216, v216, v217\n" \
"v_mfma_f32_16x16x32_bf16 v[176:179], a[188:191], a[156:159], v[176:179]\n" \
"ds_read_b128 a[216:219], v29 offset:45056\n" \
"v_cvt_pk_bf16_f32 v217, v218, v219\n" \
"v_mfma_f32_16x16x32_bf16 v[176:179], a[252:255], a[220:223], v[176:179]\n" \
"v_cvt_pk_bf16_f32 v218, v180, v181\n" \
"v_cvt_pk_bf16_f32 v219, v182, v183\n" \
"v_mfma_f32_16x16x32_bf16 v[172:175], a[184:187], a[156:159], v[172:175]\n" \
"v_accvgpr_read_b32 v192, a96\n" \
"v_accvgpr_read_b32 v193, a97\n" \
"v_mfma_f32_16x16x32_bf16 v[172:175], a[248:251], a[220:223], v[172:175]\n" \
"v_accvgpr_read_b32 v194, a98\n" \
"v_accvgpr_read_b32 v195, a99\n" \
"v_mfma_f32_16x16x32_bf16 v[168:171], a[180:183], a[156:159], v[168:171]\n" \
"v_accvgpr_read_b32 v180, a100\n" \
"v_accvgpr_read_b32 v181, a101\n" \
"v_mfma_f32_16x16x32_bf16 v[168:171], a[244:247], a[220:223], v[168:171]\n" \
"v_accvgpr_read_b32 v182, a102\n" \
"v_accvgpr_read_b32 v183, a103\n" \
"v_mfma_f32_16x16x32_bf16 v[164:167], a[176:179], a[156:159], v[164:167]\n" \
"v_cvt_pk_bf16_f32 v192, v192, v193\n" \
"v_cvt_pk_bf16_f32 v193, v194, v195\n" \
"v_mfma_f32_16x16x32_bf16 v[164:167], a[240:243], a[220:223], v[164:167]\n" \
"ds_read_b128 a[156:159], v28 offset:47104\n" \
"ds_read_b128 a[220:223], v29 offset:47104\n" \
"s_waitcnt lgkmcnt(15)\n" \
"v_mfma_f32_16x16x32_bf16 a[0:3], a[160:163], a[128:131], a[0:3]\n" \
"s_add_u32 m0, s30, 0x1000\n" \
"s_addk_i32 s84, 0x3000\n" \
"v_mfma_f32_16x16x32_bf16 a[0:3], a[224:227], a[192:195], a[0:3]\n" \
"buffer_load_dwordx4 v24, s[76:79], s84 offen offset:0 lds\n" \
"v_mfma_f32_16x16x32_bf16 a[4:7], a[164:167], a[128:131], a[4:7]\n" \
"ds_read_b64_tr_b16 a[176:177], v36 offset:0\n" \
"v_cvt_pk_bf16_f32 v194, v180, v181\n" \
"v_mfma_f32_16x16x32_bf16 a[4:7], a[228:231], a[192:195], a[4:7]\n" \
"ds_read_b64_tr_b16 a[178:179], v36 offset:1024\n" \
"v_cvt_pk_bf16_f32 v195, v182, v183\n" \
"v_mfma_f32_16x16x32_bf16 a[12:15], a[172:175], a[128:131], a[12:15]\n" \
"ds_read_b64_tr_b16 a[240:241], v36 offset:8192\n" \
"v_accvgpr_read_b32 v188, a88\n" \
"s_waitcnt lgkmcnt(15)\n" \
"v_mfma_f32_16x16x32_bf16 a[12:15], a[236:239], a[192:195], a[12:15]\n" \
"s_add_u32 s88, s42, 0x1000\n" \
"buffer_store_dwordx4 v[252:255], v20, s[80:83], s88 offen offset:2048 sc1\n" \
"s_waitcnt lgkmcnt(13)\n" \
"v_mfma_f32_16x16x32_bf16 a[8:11], a[128:131], a[168:171], a[8:11]\n" \
"ds_read_b64_tr_b16 a[242:243], v36 offset:9216\n" \
"v_accvgpr_read_b32 v189, a89\n" \
"s_waitcnt lgkmcnt(12)\n" \
"s_nop 0\n" \
"v_mfma_f32_16x16x32_bf16 a[8:11], a[192:195], a[232:235], a[8:11]\n" \
"ds_read_b64_tr_b16 a[180:181], v37 offset:0\n" \
"v_accvgpr_read_b32 v190, a90\n" \
"v_mfma_f32_16x16x32_bf16 a[24:27], a[132:135], a[168:171], a[24:27]\n" \
"ds_read_b64_tr_b16 a[182:183], v37 offset:1024\n" \
"v_accvgpr_read_b32 v191, a91\n" \
"v_mfma_f32_16x16x32_bf16 a[24:27], a[196:199], a[232:235], a[24:27]\n" \
"ds_read_b64_tr_b16 a[244:245], v37 offset:8192\n" \
"v_accvgpr_read_b32 v180, a92\n" \
"v_mfma_f32_16x16x32_bf16 a[28:31], a[172:175], a[132:135], a[28:31]\n" \
"v_accvgpr_read_b32 v181, a93\n" \
"v_accvgpr_read_b32 v182, a94\n" \
"v_mfma_f32_16x16x32_bf16 a[28:31], a[236:239], a[196:199], a[28:31]\n" \
"s_addk_i32 s84, 0x2000\n" \
"s_nop 0\n" \
"v_mfma_f32_16x16x32_bf16 a[20:23], a[164:167], a[132:135], a[20:23]\n" \
"buffer_load_dwordx4 v25, s[76:79], s84 offen offset:1024 lds\n" \
"v_mfma_f32_16x16x32_bf16 a[20:23], a[228:231], a[196:199], a[20:23]\n" \
"ds_read_b64_tr_b16 a[246:247], v37 offset:9216\n" \
"v_accvgpr_read_b32 v183, a95\n" \
"v_mfma_f32_16x16x32_bf16 a[16:19], a[160:163], a[132:135], a[16:19]\n" \
"ds_read_b64_tr_b16 a[184:185], v38 offset:0\n" \
"v_cvt_pk_bf16_f32 v188, v188, v189\n" \
"v_mfma_f32_16x16x32_bf16 a[16:19], a[224:227], a[196:199], a[16:19]\n" \
"ds_read_b64_tr_b16 a[186:187], v38 offset:1024\n" \
"v_cvt_pk_bf16_f32 v189, v190, v191\n" \
"v_mfma_f32_16x16x32_bf16 a[32:35], a[160:163], a[136:139], a[32:35]\n" \
"s_addk_i32 s84, 0x2000\n" \
"s_nop 0\n" \
"v_mfma_f32_16x16x32_bf16 a[32:35], a[224:227], a[200:203], a[32:35]\n" \
"buffer_load_dwordx4 v24, s[76:79], s84 offen offset:2048 lds\n" \
"v_mfma_f32_16x16x32_bf16 a[36:39], a[164:167], a[136:139], a[36:39]\n" \
"ds_read_b64_tr_b16 a[248:249], v38 offset:8192\n" \
"v_cvt_pk_bf16_f32 v190, v180, v181\n" \
"v_mfma_f32_16x16x32_bf16 a[36:39], a[228:231], a[200:203], a[36:39]\n" \
"ds_read_b64_tr_b16 a[250:251], v38 offset:9216\n" \
"v_cvt_pk_bf16_f32 v191, v182, v183\n" \
"v_mfma_f32_16x16x32_bf16 a[44:47], a[172:175], a[136:139], a[44:47]\n" \
"ds_read_b64_tr_b16 a[188:189], v39 offset:0\n" \
"v_accvgpr_read_b32 v184, a72\n" \
"v_mfma_f32_16x16x32_bf16 a[44:47], a[236:239], a[200:203], a[44:47]\n" \
"ds_read_b64_tr_b16 a[190:191], v39 offset:1024\n" \
"v_accvgpr_read_b32 v185, a73\n" \
"v_mfma_f32_16x16x32_bf16 a[40:43], a[136:139], a[168:171], a[40:43]\n" \
"s_add_u32 s89, s42, 0x2000\n" \
"buffer_store_dwordx4 v[200:203], v20, s[80:83], s89 offen offset:2048 sc1\n" \
"v_mfma_f32_16x16x32_bf16 a[40:43], a[200:203], a[232:235], a[40:43]\n" \
".fill (L_pb + 28384 - .) / 4, 4, 0xBF800000\n" \
"L_pp7:\n" \
"s_cbranch_execz L_pret\n" \
"s_nop 0\n" \
"ds_read_b64_tr_b16 a[252:253], v39 offset:8192\n" \
"v_accvgpr_read_b32 v186, a74\n" \
"v_mfma_f32_16x16x32_bf16 a[56:59], a[140:143], a[168:171], a[56:59]\n" \
"ds_read_b64_tr_b16 a[254:255], v39 offset:9216\n" \
"v_accvgpr_read_b32 v187, a75\n" \
"v_mfma_f32_16x16x32_bf16 a[56:59], a[204:207], a[232:235], a[56:59]\n" \
"v_accvgpr_read_b32 v180, a76\n" \
"v_accvgpr_read_b32 v181, a77\n" \
"v_mfma_f32_16x16x32_bf16 a[60:63], a[172:175], a[140:143], a[60:63]\n" \
"v_accvgpr_read_b32 v182, a78\n" \
"v_accvgpr_read_b32 v183, a79\n" \
"v_mfma_f32_16x16x32_bf16 a[60:63], a[236:239], a[204:207], a[60:63]\n" \
"s_addk_i32 s84, 0x2000\n" \
"v_mfma_f32_16x16x32_bf16 a[52:55], a[164:167], a[140:143], a[52:55]\n" \
"buffer_load_dwordx4 v25, s[76:79], s84 offen offset:3072 lds\n" \
"v_mfma_f32_16x16x32_bf16 a[52:55], a[228:231], a[204:207], a[52:55]\n" \
"v_cvt_pk_bf16_f32 v184, v184, v185\n" \
"v_cvt_pk_bf16_f32 v185, v186, v187\n" \
"v_mfma_f32_16x16x32_bf16 a[48:51], a[160:163], a[140:143], a[48:51]\n" \
"v_cvt_pk_bf16_f32 v186, v180, v181\n" \
"v_cvt_pk_bf16_f32 v187, v182, v183\n" \
"v_mfma_f32_16x16x32_bf16 a[48:51], a[224:227], a[204:207], a[48:51]\n" \
"s_sub_i32 s14, s9, s14\n" \
"s_add_i32 s20, s19, s13\n" \
"s_waitcnt lgkmcnt(15)\n" \
"v_mfma_f32_16x16x32_bf16 a[64:67], a[164:167], v[248:251], a[64:67]\n" \
"v_writelane_b32 v11, s20, 31\n" \
"s_add_i32 s19, s19, s15\n" \
"v_mfma_f32_16x16x32_bf16 a[64:67], a[228:231], v[48:51], a[64:67]\n" \
"s_mul_i32 s14, s14, 0x24800\n" \
"v_writelane_b32 v11, s19, 32\n" \
"v_mfma_f32_16x16x32_bf16 a[80:83], a[172:175], v[248:251], a[80:83]\n" \
"s_add_i32 s19, s14, 0x4120800\n" \
"s_add_i32 s16, s19, s16\n" \
"v_mfma_f32_16x16x32_bf16 a[80:83], a[236:239], v[48:51], a[80:83]\n" \
"v_writelane_b32 v11, s16, 33\n" \
"s_add_i32 s13, s19, s13\n" \
"v_mfma_f32_16x16x32_bf16 a[68:71], v[248:251], a[168:171], a[68:71]\n" \
"v_writelane_b32 v11, s13, 34\n" \
"s_add_i32 s19, s19, s15\n" \
"s_waitcnt vmcnt(11) lgkmcnt(0)\n" \
"v_mfma_f32_16x16x32_bf16 a[68:71], v[48:51], a[232:235], a[68:71]\n" \
"s_barrier\n" \
"v_mov_b32_e32 v45, s62\n" \
"s_mov_b64 exec, 1\n" \
"buffer_store_dword v45, off, s[80:83], s37 offset:0 sc1\n" \
"s_mov_b64 exec, -1\n" \
"v_xor_b32_e32 v28, 0x20000, v28\n" \
"v_xor_b32_e32 v29, 0x20000, v29\n" \
"v_mfma_f32_16x16x32_bf16 v[52:55], a[176:179], a[128:131], v[52:55]\n" \
"s_add_u32 m0, s40, 0x0\n" \
"s_add_u32 s85, s39, 0xc000\n" \
"v_mfma_f32_16x16x32_bf16 v[52:55], a[240:243], a[192:195], v[52:55]\n" \
"buffer_load_dwordx4 v20, s[80:83], s85 offen sc1 lds\n" \
"v_mfma_f32_16x16x32_bf16 v[56:59], a[180:183], a[128:131], v[56:59]\n" \
"ds_read_b64_tr_b16 a[160:161], v32 offset:32768\n" \
"s_or_b32 s13, s18, 0x3000000\n" \
"v_mfma_f32_16x16x32_bf16 v[56:59], a[244:247], a[192:195], v[56:59]\n" \
"ds_read_b64_tr_b16 a[162:163], v32 offset:33280\n" \
"s_lshl_b32 s15, s6, 11\n" \
"v_mfma_f32_16x16x32_bf16 v[60:63], a[184:187], a[128:131], v[60:63]\n" \
"s_add_u32 m0, s40, 0x1000\n" \
"s_add_u32 s85, s39, 0xd000\n" \
"v_mfma_f32_16x16x32_bf16 v[60:63], a[248:251], a[192:195], v[60:63]\n" \
"buffer_load_dwordx4 v20, s[80:83], s85 offen sc1 lds\n" \
"v_mfma_f32_16x16x32_bf16 v[64:67], a[188:191], a[128:131], v[64:67]\n" \
"ds_read_b64_tr_b16 a[224:225], v32 offset:36864\n" \
"v_writelane_b32 v11, s19, 35\n" \
"v_mfma_f32_16x16x32_bf16 v[64:67], a[252:255], a[192:195], v[64:67]\n" \
"ds_read_b128 a[128:131], v28 offset:0\n" \
"s_add_i32 s16, s13, s15\n" \
"v_mfma_f32_16x16x32_bf16 v[80:83], a[188:191], a[132:135], v[80:83]\n" \
"ds_read_b128 a[192:195], v29 offset:0\n" \
"v_writelane_b32 v11, s16, 36\n" \
"v_mfma_f32_16x16x32_bf16 v[80:83], a[252:255], a[196:199], v[80:83]\n" \
"s_add_u32 m0, s40, 0x3000\n" \
"s_movk_i32 s84, 0x300\n" \
"v_mfma_f32_16x16x32_bf16 v[76:79], a[184:187], a[132:135], v[76:79]\n" \
"buffer_load_dwordx4 v21, s[76:79], s84 offen offset:0 lds\n" \
"v_mfma_f32_16x16x32_bf16 v[76:79], a[248:251], a[196:199], v[76:79]\n" \
"ds_read_b64_tr_b16 a[226:227], v32 offset:37376\n" \
"s_add_i32 s16, s14, 0x411c000\n" \
"v_mfma_f32_16x16x32_bf16 v[72:75], a[180:183], a[132:135], v[72:75]\n" \
"ds_read_b64_tr_b16 a[164:165], v33 offset:32768\n" \
"s_add_i32 s19, s16, s15\n" \
"v_mfma_f32_16x16x32_bf16 v[72:75], a[244:247], a[196:199], v[72:75]\n" \
"v_writelane_b32 v11, s19, 37\n" \
"s_add_i32 s18, s18, 0x3004000\n" \
"v_mfma_f32_16x16x32_bf16 v[68:71], a[176:179], a[132:135], v[68:71]\n" \
"ds_read_b64_tr_b16 a[166:167], v33 offset:33280\n" \
"v_writelane_b32 v11, s18, 38\n" \
"v_mfma_f32_16x16x32_bf16 v[68:71], a[240:243], a[196:199], v[68:71]\n" \
"ds_read_b128 a[132:135], v28 offset:2048\n" \
"s_add_i32 s14, s14, 0x4120000\n" \
"v_mfma_f32_16x16x32_bf16 v[84:87], a[176:179], a[136:139], v[84:87]\n" \
"ds_read_b128 a[196:199], v29 offset:2048\n" \
"v_writelane_b32 v11, s14, 39\n" \
"v_mfma_f32_16x16x32_bf16 v[84:87], a[240:243], a[200:203], v[84:87]\n" \
"s_add_u32 m0, s31, 0x0\n" \
"s_mov_b32 s84, 0x300000\n" \
"v_mfma_f32_16x16x32_bf16 v[88:91], a[180:183], a[136:139], v[88:91]\n" \
"buffer_load_dwordx4 v26, s[68:71], s84 offen offset:0 lds\n" \
"v_mfma_f32_16x16x32_bf16 v[88:91], a[244:247], a[200:203], v[88:91]\n" \
"s_add_u32 s88, s42, 0x2000\n" \
"buffer_store_dwordx4 v[204:207], v20, s[80:83], s88 offen offset:3072 sc1\n" \
"v_mfma_f32_16x16x32_bf16 v[92:95], a[184:187], a[136:139], v[92:95]\n" \
"ds_read_b64_tr_b16 a[228:229], v33 offset:36864\n" \
"s_lshl_b32 s14, s5, 2\n" \
"v_mfma_f32_16x16x32_bf16 v[92:95], a[248:251], a[200:203], v[92:95]\n" \
"ds_read_b64_tr_b16 a[230:231], v33 offset:37376\n" \
"s_or_b32 s8, s14, s8\n" \
"v_mfma_f32_16x16x32_bf16 v[96:99], a[188:191], a[136:139], v[96:99]\n" \
"s_mul_i32 s8, s8, 0x90000\n" \
"s_lshl_b32 s14, s3, 14\n" \
"v_mfma_f32_16x16x32_bf16 v[96:99], a[252:255], a[200:203], v[96:99]\n" \
"ds_read_b128 a[136:139], v28 offset:4096\n" \
"s_add_i32 s8, s8, s14\n" \
"v_mfma_f32_16x16x32_bf16 v[112:115], a[188:191], a[140:143], v[112:115]\n" \
"ds_read_b128 a[200:203], v29 offset:4096\n" \
"s_lshl_b32 s9, s9, 4\n" \
"v_mfma_f32_16x16x32_bf16 v[112:115], a[252:255], a[204:207], v[112:115]\n" \
"s_addk_i32 s84, 0x7c00\n" \
"v_mfma_f32_16x16x32_bf16 v[108:111], a[184:187], a[140:143], v[108:111]\n" \
"buffer_load_dwordx4 v26, s[68:71], s84 offen offset:1024 lds\n" \
"v_mfma_f32_16x16x32_bf16 v[108:111], a[248:251], a[204:207], v[108:111]\n" \
"ds_read_b64_tr_b16 a[172:173], v35 offset:32768\n" \
"s_lshl_b32 s14, s5, 7\n" \
"v_mfma_f32_16x16x32_bf16 v[104:107], a[180:183], a[140:143], v[104:107]\n" \
"s_addk_i32 s84, 0x7c00\n" \
"v_mfma_f32_16x16x32_bf16 v[104:107], a[244:247], a[204:207], v[104:107]\n" \
"buffer_load_dwordx4 v27, s[68:71], s84 offen offset:2048 lds\n" \
"v_mfma_f32_16x16x32_bf16 v[100:103], a[176:179], a[140:143], v[100:103]\n" \
"ds_read_b64_tr_b16 a[174:175], v35 offset:33280\n" \
"s_or_b32 s18, s9, s14\n" \
"v_mfma_f32_16x16x32_bf16 v[100:103], a[240:243], a[204:207], v[100:103]\n" \
"ds_read_b128 a[140:143], v28 offset:6144\n" \
"s_sub_i32 s9, s9, s14\n" \
"v_mfma_f32_16x16x32_bf16 v[116:119], a[176:179], a[144:147], v[116:119]\n" \
"ds_read_b128 a[204:207], v29 offset:6144\n" \
"s_mulk_i32 s18, 0x480\n" \
"v_mfma_f32_16x16x32_bf16 v[116:119], a[240:243], a[208:211], v[116:119]\n" \
"s_addk_i32 s84, 0x7c00\n" \
"v_mfma_f32_16x16x32_bf16 v[120:123], a[180:183], a[144:147], v[120:123]\n" \
"buffer_load_dwordx4 v27, s[68:71], s84 offen offset:3072 lds\n" \
"v_mfma_f32_16x16x32_bf16 v[120:123], a[244:247], a[208:211], v[120:123]\n" \
"ds_read_b64_tr_b16 a[236:237], v35 offset:36864\n" \
"s_mulk_i32 s9, 0x480\n" \
"v_mfma_f32_16x16x32_bf16 v[124:127], a[184:187], a[144:147], v[124:127]\n" \
"ds_read_b64_tr_b16 a[238:239], v35 offset:37376\n" \
"s_or_b32 s14, s18, s12\n" \
"v_mfma_f32_16x16x32_bf16 v[124:127], a[248:251], a[208:211], v[124:127]\n" \
"s_add_u32 m0, s30, 0x8000\n" \
"s_movk_i32 s84, 0x380\n" \
"v_mfma_f32_16x16x32_bf16 v[128:131], a[188:191], a[144:147], v[128:131]\n" \
"buffer_load_dwordx4 v24, s[76:79], s84 offen offset:0 lds\n" \
"v_mfma_f32_16x16x32_bf16 v[128:131], a[252:255], a[208:211], v[128:131]\n" \
"ds_read_b128 a[144:147], v28 offset:8192\n" \
"s_or_b32 s9, s9, s12\n" \
"v_mfma_f32_16x16x32_bf16 v[144:147], a[188:191], a[148:151], v[144:147]\n" \
"ds_read_b128 a[208:211], v29 offset:8192\n" \
"s_and_b32 s12, s2, 1\n" \
"v_mfma_f32_16x16x32_bf16 v[144:147], a[252:255], a[212:215], v[144:147]\n" \
"ds_read_b64_tr_b16 a[168:169], v34 offset:32768\n" \
"v_mfma_f32_16x16x32_bf16 v[140:143], a[184:187], a[148:151], v[140:143]\n" \
"s_movk_i32 s84, 0x2380\n" \
"s_nop 0\n" \
"v_mfma_f32_16x16x32_bf16 v[140:143], a[248:251], a[212:215], v[140:143]\n" \
"buffer_load_dwordx4 v25, s[76:79], s84 offen offset:1024 lds\n" \
"v_mfma_f32_16x16x32_bf16 v[136:139], a[180:183], a[148:151], v[136:139]\n" \
"ds_read_b64_tr_b16 a[170:171], v34 offset:33280\n" \
"v_mfma_f32_16x16x32_bf16 v[136:139], a[244:247], a[212:215], v[136:139]\n" \
"ds_read_b64_tr_b16 a[232:233], v34 offset:36864\n" \
"v_mfma_f32_16x16x32_bf16 v[132:135], a[176:179], a[148:151], v[132:135]\n" \
"ds_read_b64_tr_b16 a[234:235], v34 offset:37376\n" \
"v_mfma_f32_16x16x32_bf16 v[132:135], a[240:243], a[212:215], v[132:135]\n" \
"ds_read_b128 a[148:151], v28 offset:10240\n" \
"v_mfma_f32_16x16x32_bf16 v[148:151], a[176:179], a[152:155], v[148:151]\n" \
"ds_read_b128 a[212:215], v29 offset:10240\n" \
"v_mfma_f32_16x16x32_bf16 v[148:151], a[240:243], a[216:219], v[148:151]\n" \
"s_movk_i32 s84, 0x4380\n" \
"v_mfma_f32_16x16x32_bf16 v[152:155], a[180:183], a[152:155], v[152:155]\n" \
"buffer_load_dwordx4 v24, s[76:79], s84 offen offset:2048 lds\n" \
"v_mfma_f32_16x16x32_bf16 v[152:155], a[244:247], a[216:219], v[152:155]\n" \
"s_movk_i32 s84, 0x6380\n" \
"v_mfma_f32_16x16x32_bf16 v[156:159], a[184:187], a[152:155], v[156:159]\n" \
"buffer_load_dwordx4 v25, s[76:79], s84 offen offset:3072 lds\n" \
"v_mfma_f32_16x16x32_bf16 v[156:159], a[248:251], a[216:219], v[156:159]\n" \
"ds_read_b128 v[248:251], v22 offset:32768\n" \
"v_mfma_f32_16x16x32_bf16 v[160:163], a[188:191], a[152:155], v[160:163]\n" \
"ds_read_b128 v[48:51], v23 offset:32768\n" \
"v_mfma_f32_16x16x32_bf16 v[160:163], a[252:255], a[216:219], v[160:163]\n" \
"ds_read_b128 a[152:155], v28 offset:12288\n" \
"v_mfma_f32_16x16x32_bf16 v[176:179], a[188:191], a[156:159], v[176:179]\n" \
"ds_read_b128 a[216:219], v29 offset:12288\n" \
"v_mfma_f32_16x16x32_bf16 v[176:179], a[252:255], a[220:223], v[176:179]\n" \
"s_cmp_lg_u32 s12, 0\n" \
"s_cselect_b32 s13, s16, s13\n" \
"v_mfma_f32_16x16x32_bf16 v[172:175], a[184:187], a[156:159], v[172:175]\n" \
"s_bfe_u32 s16, s2, 0x20001\n" \
"s_addk_i32 s13, 0x4000\n" \
"v_mfma_f32_16x16x32_bf16 v[172:175], a[248:251], a[220:223], v[172:175]\n" \
"s_xor_b32 s16, s16, s3\n" \
"v_mfma_f32_16x16x32_bf16 v[168:171], a[180:183], a[156:159], v[168:171]\n" \
"s_cmp_lg_u32 s16, 0\n" \
"s_cselect_b32 s13, s7, s13\n" \
"v_mfma_f32_16x16x32_bf16 v[168:171], a[244:247], a[220:223], v[168:171]\n" \
"s_lshl_b32 s6, s6, 16\n" \
"v_writelane_b32 v11, s13, 40\n" \
"v_mfma_f32_16x16x32_bf16 v[164:167], a[176:179], a[156:159], v[164:167]\n" \
"s_add_i32 s13, s8, 0x4240000\n" \
"s_add_i32 s6, s13, s6\n" \
"v_mfma_f32_16x16x32_bf16 v[164:167], a[240:243], a[220:223], v[164:167]\n" \
"ds_read_b128 a[156:159], v28 offset:14336\n" \
"ds_read_b128 a[220:223], v29 offset:14336\n" \
"s_waitcnt lgkmcnt(15)\n" \
"v_mfma_f32_16x16x32_bf16 a[0:3], a[160:163], a[128:131], a[0:3]\n" \
"s_add_u32 m0, s30, 0x9000\n" \
"s_addk_i32 s84, 0x3000\n" \
"v_mfma_f32_16x16x32_bf16 a[0:3], a[224:227], a[192:195], a[0:3]\n" \
"buffer_load_dwordx4 v24, s[76:79], s84 offen offset:0 lds\n" \
"v_mfma_f32_16x16x32_bf16 a[4:7], a[164:167], a[128:131], a[4:7]\n" \
"ds_read_b64_tr_b16 a[176:177], v36 offset:32768\n" \
"v_writelane_b32 v11, s6, 41\n" \
"v_mfma_f32_16x16x32_bf16 a[4:7], a[228:231], a[192:195], a[4:7]\n" \
"ds_read_b64_tr_b16 a[178:179], v36 offset:33792\n" \
"s_add_i32 s8, s8, 0x42c0000\n" \
"v_mfma_f32_16x16x32_bf16 a[12:15], a[172:175], a[128:131], a[12:15]\n" \
"ds_read_b64_tr_b16 a[240:241], v36 offset:40960\n" \
"s_lshl_b32 s6, s12, 10\n" \
"s_waitcnt lgkmcnt(15)\n" \
"v_mfma_f32_16x16x32_bf16 a[12:15], a[236:239], a[192:195], a[12:15]\n" \
"s_add_u32 s89, s42, 0x3000\n" \
"buffer_store_dwordx4 v[208:211], v20, s[80:83], s89 offen offset:3072 sc1\n" \
"s_waitcnt lgkmcnt(13)\n" \
"v_mfma_f32_16x16x32_bf16 a[8:11], a[128:131], a[168:171], a[8:11]\n" \
"ds_read_b64_tr_b16 a[242:243], v36 offset:41984\n" \
"s_or_b32 s6, s8, s6\n" \
"s_waitcnt lgkmcnt(12)\n" \
"v_mfma_f32_16x16x32_bf16 a[8:11], a[192:195], a[232:235], a[8:11]\n" \
"ds_read_b64_tr_b16 a[180:181], v37 offset:32768\n" \
"v_mfma_f32_16x16x32_bf16 a[24:27], a[132:135], a[168:171], a[24:27]\n" \
"ds_read_b64_tr_b16 a[182:183], v37 offset:33792\n" \
"v_mfma_f32_16x16x32_bf16 a[24:27], a[196:199], a[232:235], a[24:27]\n" \
"ds_read_b64_tr_b16 a[244:245], v37 offset:40960\n" \
"v_mfma_f32_16x16x32_bf16 a[28:31], a[172:175], a[132:135], a[28:31]\n" \
"s_cmp_lg_u32 s16, 0\n" \
"s_cselect_b32 s6, s7, s6\n" \
"v_mfma_f32_16x16x32_bf16 a[28:31], a[236:239], a[196:199], a[28:31]\n" \
"s_addk_i32 s84, 0x2000\n" \
"v_mfma_f32_16x16x32_bf16 a[20:23], a[164:167], a[132:135], a[20:23]\n" \
"buffer_load_dwordx4 v25, s[76:79], s84 offen offset:1024 lds\n" \
"v_mfma_f32_16x16x32_bf16 a[20:23], a[228:231], a[196:199], a[20:23]\n" \
"ds_read_b64_tr_b16 a[246:247], v37 offset:41984\n" \
"s_mulk_i32 s5, 0x4800\n" \
"v_mfma_f32_16x16x32_bf16 a[16:19], a[160:163], a[132:135], a[16:19]\n" \
"ds_read_b64_tr_b16 a[184:185], v38 offset:32768\n" \
"v_writelane_b32 v11, s6, 42\n" \
"v_mfma_f32_16x16x32_bf16 a[16:19], a[224:227], a[196:199], a[16:19]\n" \
"ds_read_b64_tr_b16 a[186:187], v38 offset:33792\n" \
"s_add_i32 s6, s14, s10\n" \
"v_mfma_f32_16x16x32_bf16 a[32:35], a[160:163], a[136:139], a[32:35]\n" \
"s_addk_i32 s84, 0x2000\n" \
"v_mfma_f32_16x16x32_bf16 a[32:35], a[224:227], a[200:203], a[32:35]\n" \
"buffer_load_dwordx4 v24, s[76:79], s84 offen offset:2048 lds\n" \
"v_mfma_f32_16x16x32_bf16 a[36:39], a[164:167], a[136:139], a[36:39]\n" \
"ds_read_b64_tr_b16 a[248:249], v38 offset:40960\n" \
"v_writelane_b32 v11, s6, 43\n" \
"v_mfma_f32_16x16x32_bf16 a[36:39], a[228:231], a[200:203], a[36:39]\n" \
"ds_read_b64_tr_b16 a[250:251], v38 offset:41984\n" \
"s_add_i32 s6, s9, 0x21c000\n" \
"v_mfma_f32_16x16x32_bf16 a[44:47], a[172:175], a[136:139], a[44:47]\n" \
"ds_read_b64_tr_b16 a[188:189], v39 offset:32768\n" \
"s_add_i32 s9, s6, s10\n" \
"v_mfma_f32_16x16x32_bf16 a[44:47], a[236:239], a[200:203], a[44:47]\n" \
"ds_read_b64_tr_b16 a[190:191], v39 offset:33792\n" \
"v_mfma_f32_16x16x32_bf16 a[40:43], a[136:139], a[168:171], a[40:43]\n" \
"s_add_u32 s88, s42, 0x3000\n" \
"buffer_store_dwordx4 v[212:215], v20, s[80:83], s88 offen offset:2048 sc1\n" \
"v_mfma_f32_16x16x32_bf16 a[40:43], a[200:203], a[232:235], a[40:43]\n" \
"ds_read_b64_tr_b16 a[252:253], v39 offset:40960\n" \
"v_mfma_f32_16x16x32_bf16 a[56:59], a[140:143], a[168:171], a[56:59]\n" \
"ds_read_b64_tr_b16 a[254:255], v39 offset:41984\n" \
"v_mfma_f32_16x16x32_bf16 a[56:59], a[204:207], a[232:235], a[56:59]\n" \
"s_cmp_lg_u32 s12, 0\n" \
"s_cselect_b32 s6, s6, s14\n" \
"v_mfma_f32_16x16x32_bf16 a[60:63], a[172:175], a[140:143], a[60:63]\n" \
"s_addk_i32 s6, 0x100\n" \
"v_writelane_b32 v11, s9, 44\n" \
"v_mfma_f32_16x16x32_bf16 a[60:63], a[236:239], a[204:207], a[60:63]\n" \
"s_addk_i32 s84, 0x2000\n" \
"v_mfma_f32_16x16x32_bf16 a[52:55], a[164:167], a[140:143], a[52:55]\n" \
"buffer_load_dwordx4 v25, s[76:79], s84 offen offset:3072 lds\n" \
"v_mfma_f32_16x16x32_bf16 a[52:55], a[228:231], a[204:207], a[52:55]\n" \
"s_cmp_lg_u32 s16, 0\n" \
"s_cselect_b32 s6, s7, s6\n" \
"v_mfma_f32_16x16x32_bf16 a[48:51], a[160:163], a[140:143], a[48:51]\n" \
"v_writelane_b32 v11, s6, 45\n" \
"s_or_b32 s6, s8, s15\n" \
"v_mfma_f32_16x16x32_bf16 a[48:51], a[224:227], a[204:207], a[48:51]\n" \
"v_writelane_b32 v11, s6, 53\n" \
"s_lshl_b32 s6, s3, 17\n" \
"s_waitcnt lgkmcnt(15)\n" \
"v_mfma_f32_16x16x32_bf16 a[64:67], a[164:167], v[248:251], a[64:67]\n" \
"s_add_i32 s6, s13, s6\n" \
"s_or_b32 s4, s6, s4\n" \
"v_mfma_f32_16x16x32_bf16 a[64:67], a[228:231], v[48:51], a[64:67]\n" \
"s_or_b32 s4, s4, 0x3840\n" \
"v_writelane_b32 v11, s4, 6\n" \
"v_mfma_f32_16x16x32_bf16 a[80:83], a[172:175], v[248:251], a[80:83]\n" \
"s_mov_b32 s4, 0xff0000\n" \
"v_mfma_f32_16x16x32_bf16 a[80:83], a[236:239], v[48:51], a[80:83]\n" \
"s_cmp_lg_u32 s16, 0\n" \
"s_cselect_b32 s4, s17, s4\n" \
"v_mfma_f32_16x16x32_bf16 a[68:71], v[248:251], a[168:171], a[68:71]\n" \
"v_writelane_b32 v11, s4, 7\n" \
"s_mul_i32 s4, s3, 0x1200\n" \
"s_waitcnt vmcnt(10) lgkmcnt(0)\n" \
"v_mfma_f32_16x16x32_bf16 a[68:71], v[48:51], a[232:235], a[68:71]\n" \
"s_barrier\n" \
"v_xor_b32_e32 v28, 0x20000, v28\n" \
"v_xor_b32_e32 v29, 0x20000, v29\n" \
"v_mfma_f32_16x16x32_bf16 v[52:55], a[176:179], a[128:131], v[52:55]\n" \
"s_add_u32 m0, s40, 0x8000\n" \
"s_add_u32 s85, s39, 0xe000\n" \
"v_mfma_f32_16x16x32_bf16 v[52:55], a[240:243], a[192:195], v[52:55]\n" \
"buffer_load_dwordx4 v20, s[80:83], s85 offen sc1 lds\n" \
"v_mfma_f32_16x16x32_bf16 v[56:59], a[180:183], a[128:131], v[56:59]\n" \
"ds_read_b64_tr_b16 a[160:161], v32 offset:0\n" \
"s_add_i32 s4, s4, s5\n" \
"v_mfma_f32_16x16x32_bf16 v[56:59], a[244:247], a[192:195], v[56:59]\n" \
"ds_read_b64_tr_b16 a[162:163], v32 offset:512\n" \
"s_add_i32 s4, s4, s11\n" \
"v_mfma_f32_16x16x32_bf16 v[60:63], a[184:187], a[128:131], v[60:63]\n" \
"s_add_u32 m0, s40, 0x9000\n" \
"s_add_u32 s85, s39, 0xf000\n" \
"v_mfma_f32_16x16x32_bf16 v[60:63], a[248:251], a[192:195], v[60:63]\n" \
"buffer_load_dwordx4 v20, s[80:83], s85 offen sc1 lds\n" \
"v_mfma_f32_16x16x32_bf16 v[64:67], a[188:191], a[128:131], v[64:67]\n" \
"ds_read_b64_tr_b16 a[224:225], v32 offset:4096\n" \
"v_writelane_b32 v11, s4, 8\n" \
"v_mfma_f32_16x16x32_bf16 v[64:67], a[252:255], a[192:195], v[64:67]\n" \
"ds_read_b128 a[128:131], v28 offset:0\n" \
"s_lshl_b32 s4, s2, 16\n" \
"v_mfma_f32_16x16x32_bf16 v[80:83], a[188:191], a[132:135], v[80:83]\n" \
"ds_read_b128 a[192:195], v29 offset:0\n" \
"s_add_i32 s5, s4, 0x10000\n" \
"v_mfma_f32_16x16x32_bf16 v[80:83], a[252:255], a[196:199], v[80:83]\n" \
"s_add_u32 m0, s40, 0xb000\n" \
"s_movk_i32 s84, 0x380\n" \
"v_mfma_f32_16x16x32_bf16 v[76:79], a[184:187], a[132:135], v[76:79]\n" \
"buffer_load_dwordx4 v21, s[76:79], s84 offen offset:0 lds\n" \
"v_mfma_f32_16x16x32_bf16 v[76:79], a[248:251], a[196:199], v[76:79]\n" \
"ds_read_b64_tr_b16 a[226:227], v32 offset:4608\n" \
"s_and_b32 s5, s5, 0x70000\n" \
"v_mfma_f32_16x16x32_bf16 v[72:75], a[180:183], a[132:135], v[72:75]\n" \
"ds_read_b64_tr_b16 a[164:165], v33 offset:0\n" \
"s_add_i32 s5, s13, s5\n" \
"v_mfma_f32_16x16x32_bf16 v[72:75], a[244:247], a[196:199], v[72:75]\n" \
"v_writelane_b32 v11, s5, 46\n" \
"s_add_i32 s5, s4, 0x20000\n" \
"v_mfma_f32_16x16x32_bf16 v[68:71], a[176:179], a[132:135], v[68:71]\n" \
"ds_read_b64_tr_b16 a[166:167], v33 offset:512\n" \
"s_and_b32 s5, s5, 0x70000\n" \
"v_mfma_f32_16x16x32_bf16 v[68:71], a[240:243], a[196:199], v[68:71]\n" \
"ds_read_b128 a[132:135], v28 offset:2048\n" \
"s_add_i32 s5, s13, s5\n" \
"v_mfma_f32_16x16x32_bf16 v[84:87], a[176:179], a[136:139], v[84:87]\n" \
"ds_read_b128 a[196:199], v29 offset:2048\n" \
"s_bitset1_b32 s5, 11\n" \
"v_mfma_f32_16x16x32_bf16 v[84:87], a[240:243], a[200:203], v[84:87]\n" \
"s_add_u32 m0, s31, 0x8000\n" \
"s_mov_b32 s84, 0x380000\n" \
"v_mfma_f32_16x16x32_bf16 v[88:91], a[180:183], a[136:139], v[88:91]\n" \
"buffer_load_dwordx4 v26, s[68:71], s84 offen offset:0 lds\n" \
"v_mfma_f32_16x16x32_bf16 v[88:91], a[244:247], a[200:203], v[88:91]\n" \
"s_add_u32 s89, s42, 0x0\n" \
"buffer_store_dwordx4 v[184:187], v20, s[80:83], s89 offen offset:1024 sc1\n" \
"v_mfma_f32_16x16x32_bf16 v[92:95], a[184:187], a[136:139], v[92:95]\n" \
"ds_read_b64_tr_b16 a[228:229], v33 offset:4096\n" \
"v_writelane_b32 v11, s5, 47\n" \
"v_mfma_f32_16x16x32_bf16 v[92:95], a[248:251], a[200:203], v[92:95]\n" \
"ds_read_b64_tr_b16 a[230:231], v33 offset:4608\n" \
"s_add_i32 s5, s4, 0x30000\n" \
"v_mfma_f32_16x16x32_bf16 v[96:99], a[188:191], a[136:139], v[96:99]\n" \
"s_and_b32 s5, s5, 0x70000\n" \
"s_add_i32 s5, s13, s5\n" \
"v_mfma_f32_16x16x32_bf16 v[96:99], a[252:255], a[200:203], v[96:99]\n" \
"ds_read_b128 a[136:139], v28 offset:4096\n" \
"s_bitset1_b32 s5, 12\n" \
"v_mfma_f32_16x16x32_bf16 v[112:115], a[188:191], a[140:143], v[112:115]\n" \
"ds_read_b128 a[200:203], v29 offset:4096\n" \
"v_writelane_b32 v11, s5, 48\n" \
"v_mfma_f32_16x16x32_bf16 v[112:115], a[252:255], a[204:207], v[112:115]\n" \
"s_addk_i32 s84, 0x7c00\n" \
"v_mfma_f32_16x16x32_bf16 v[108:111], a[184:187], a[140:143], v[108:111]\n" \
"buffer_load_dwordx4 v26, s[68:71], s84 offen offset:1024 lds\n" \
"v_mfma_f32_16x16x32_bf16 v[108:111], a[248:251], a[204:207], v[108:111]\n" \
"ds_read_b64_tr_b16 a[172:173], v35 offset:0\n" \
"s_and_b32 s5, s4, 0x70000\n" \
"v_mfma_f32_16x16x32_bf16 v[104:107], a[180:183], a[140:143], v[104:107]\n" \
"s_addk_i32 s84, 0x7c00\n" \
"v_mfma_f32_16x16x32_bf16 v[104:107], a[244:247], a[204:207], v[104:107]\n" \
"buffer_load_dwordx4 v27, s[68:71], s84 offen offset:2048 lds\n" \
"v_mfma_f32_16x16x32_bf16 v[100:103], a[176:179], a[140:143], v[100:103]\n" \
"ds_read_b64_tr_b16 a[174:175], v35 offset:512\n" \
"s_xor_b32 s5, s5, 0x40000\n" \
"v_mfma_f32_16x16x32_bf16 v[100:103], a[240:243], a[204:207], v[100:103]\n" \
"ds_read_b128 a[140:143], v28 offset:6144\n" \
"s_add_i32 s5, s13, s5\n" \
"v_mfma_f32_16x16x32_bf16 v[116:119], a[176:179], a[144:147], v[116:119]\n" \
"ds_read_b128 a[204:207], v29 offset:6144\n" \
"s_or_b32 s5, s5, 0x1800\n" \
"v_mfma_f32_16x16x32_bf16 v[116:119], a[240:243], a[208:211], v[116:119]\n" \
"s_addk_i32 s84, 0x7c00\n" \
"v_mfma_f32_16x16x32_bf16 v[120:123], a[180:183], a[144:147], v[120:123]\n" \
"buffer_load_dwordx4 v27, s[68:71], s84 offen offset:3072 lds\n" \
"v_mfma_f32_16x16x32_bf16 v[120:123], a[244:247], a[208:211], v[120:123]\n" \
"ds_read_b64_tr_b16 a[236:237], v35 offset:4096\n" \
"v_writelane_b32 v11, s5, 49\n" \
"v_mfma_f32_16x16x32_bf16 v[124:127], a[184:187], a[144:147], v[124:127]\n" \
"ds_read_b64_tr_b16 a[238:239], v35 offset:4608\n" \
"s_add_i32 s5, s4, 0x50000\n" \
"v_mfma_f32_16x16x32_bf16 v[124:127], a[248:251], a[208:211], v[124:127]\n" \
"s_add_u32 m0, s30, 0x20000\n" \
"s_movk_i32 s84, 0x400\n" \
"v_mfma_f32_16x16x32_bf16 v[128:131], a[188:191], a[144:147], v[128:131]\n" \
"buffer_load_dwordx4 v24, s[76:79], s84 offen offset:0 lds\n" \
"v_mfma_f32_16x16x32_bf16 v[128:131], a[252:255], a[208:211], v[128:131]\n" \
"ds_read_b128 a[144:147], v28 offset:8192\n" \
"s_and_b32 s5, s5, 0x70000\n" \
"v_mfma_f32_16x16x32_bf16 v[144:147], a[188:191], a[148:151], v[144:147]\n" \
"ds_read_b128 a[208:211], v29 offset:8192\n" \
"s_add_i32 s5, s13, s5\n" \
"v_mfma_f32_16x16x32_bf16 v[144:147], a[252:255], a[212:215], v[144:147]\n" \
"ds_read_b64_tr_b16 a[168:169], v34 offset:0\n" \
"s_bitset1_b32 s5, 13\n" \
"v_mfma_f32_16x16x32_bf16 v[140:143], a[184:187], a[148:151], v[140:143]\n" \
"s_movk_i32 s84, 0x2400\n" \
"s_nop 0\n" \
"v_mfma_f32_16x16x32_bf16 v[140:143], a[248:251], a[212:215], v[140:143]\n" \
"buffer_load_dwordx4 v25, s[76:79], s84 offen offset:1024 lds\n" \
"v_mfma_f32_16x16x32_bf16 v[136:139], a[180:183], a[148:151], v[136:139]\n" \
"ds_read_b64_tr_b16 a[170:171], v34 offset:512\n" \
"v_writelane_b32 v11, s5, 50\n" \
"v_mfma_f32_16x16x32_bf16 v[136:139], a[244:247], a[212:215], v[136:139]\n" \
"ds_read_b64_tr_b16 a[232:233], v34 offset:4096\n" \
"s_add_i32 s5, s4, 0x60000\n" \
"v_mfma_f32_16x16x32_bf16 v[132:135], a[176:179], a[148:151], v[132:135]\n" \
"ds_read_b64_tr_b16 a[234:235], v34 offset:4608\n" \
"s_and_b32 s5, s5, 0x70000\n" \
"v_mfma_f32_16x16x32_bf16 v[132:135], a[240:243], a[212:215], v[132:135]\n" \
"ds_read_b128 a[148:151], v28 offset:10240\n" \
"s_add_i32 s4, s4, 0x70000\n" \
"v_mfma_f32_16x16x32_bf16 v[148:151], a[176:179], a[152:155], v[148:151]\n" \
"ds_read_b128 a[212:215], v29 offset:10240\n" \
"s_add_i32 s5, s13, s5\n" \
"v_mfma_f32_16x16x32_bf16 v[148:151], a[240:243], a[216:219], v[148:151]\n" \
"s_movk_i32 s84, 0x4400\n" \
"v_mfma_f32_16x16x32_bf16 v[152:155], a[180:183], a[152:155], v[152:155]\n" \
"buffer_load_dwordx4 v24, s[76:79], s84 offen offset:2048 lds\n" \
"v_mfma_f32_16x16x32_bf16 v[152:155], a[244:247], a[216:219], v[152:155]\n" \
"s_movk_i32 s84, 0x6400\n" \
"s_nop 0\n" \
"v_mfma_f32_16x16x32_bf16 v[156:159], a[184:187], a[152:155], v[156:159]\n" \
"buffer_load_dwordx4 v25, s[76:79], s84 offen offset:3072 lds\n" \
"v_mfma_f32_16x16x32_bf16 v[156:159], a[248:251], a[216:219], v[156:159]\n" \
"ds_read_b128 v[248:251], v22 offset:0\n" \
"s_and_b32 s4, s4, 0x70000\n" \
"v_mfma_f32_16x16x32_bf16 v[160:163], a[188:191], a[152:155], v[160:163]\n" \
"ds_read_b128 v[48:51], v23 offset:0\n" \
"s_or_b32 s5, s5, 0x2800\n" \
"v_mfma_f32_16x16x32_bf16 v[160:163], a[252:255], a[216:219], v[160:163]\n" \
"ds_read_b128 a[152:155], v28 offset:12288\n" \
"v_writelane_b32 v11, s5, 51\n" \
"v_mfma_f32_16x16x32_bf16 v[176:179], a[188:191], a[156:159], v[176:179]\n" \
"ds_read_b128 a[216:219], v29 offset:12288\n" \
"s_add_i32 s13, s13, s4\n" \
"v_mfma_f32_16x16x32_bf16 v[176:179], a[252:255], a[220:223], v[176:179]\n" \
"s_or_b32 s4, s13, 0x3000\n" \
"v_writelane_b32 v11, s4, 52\n" \
"v_mfma_f32_16x16x32_bf16 v[172:175], a[184:187], a[156:159], v[172:175]\n" \
"s_movk_i32 s11, 0x4000\n" \
"s_lshl_b32 s4, s2, 2\n" \
"v_mfma_f32_16x16x32_bf16 v[172:175], a[248:251], a[220:223], v[172:175]\n" \
"s_and_b32 s4, s4, 28\n" \
"s_bfe_u32 s5, s2, 0x20003\n" \
"v_mfma_f32_16x16x32_bf16 v[168:171], a[180:183], a[156:159], v[168:171]\n" \
"s_or_b32 s4, s4, s5\n" \
"s_lshr_b32 s5, s2, 5\n" \
"v_mfma_f32_16x16x32_bf16 v[168:171], a[244:247], a[220:223], v[168:171]\n" \
"s_lshr_b32 s19, s3, 1\n" \
"s_lshl_b32 s18, s5, 1\n" \
"v_mfma_f32_16x16x32_bf16 v[164:167], a[176:179], a[156:159], v[164:167]\n" \
"s_lshl_b32 s6, s4, 20\n" \
"s_lshl_b32 s7, s4, 8\n" \
"v_mfma_f32_16x16x32_bf16 v[164:167], a[240:243], a[220:223], v[164:167]\n" \
"ds_read_b128 a[156:159], v28 offset:14336\n" \
"ds_read_b128 a[220:223], v29 offset:14336\n" \
"s_waitcnt lgkmcnt(15)\n" \
"v_mfma_f32_16x16x32_bf16 a[0:3], a[160:163], a[128:131], a[0:3]\n" \
"s_add_u32 m0, s30, 0x21000\n" \
"s_addk_i32 s84, 0x3000\n" \
"v_mfma_f32_16x16x32_bf16 a[0:3], a[224:227], a[192:195], a[0:3]\n" \
"buffer_load_dwordx4 v24, s[76:79], s84 offen offset:0 lds\n" \
"v_mfma_f32_16x16x32_bf16 a[4:7], a[164:167], a[128:131], a[4:7]\n" \
"ds_read_b64_tr_b16 a[176:177], v36 offset:0\n" \
"s_and_b32 s13, s3, 1\n" \
"v_mfma_f32_16x16x32_bf16 a[4:7], a[228:231], a[192:195], a[4:7]\n" \
"ds_read_b64_tr_b16 a[178:179], v36 offset:1024\n" \
"s_add_i32 s14, s18, s19\n" \
"v_mfma_f32_16x16x32_bf16 a[12:15], a[172:175], a[128:131], a[12:15]\n" \
"ds_read_b64_tr_b16 a[240:241], v36 offset:8192\n" \
"s_waitcnt lgkmcnt(15)\n" \
"v_mfma_f32_16x16x32_bf16 a[12:15], a[236:239], a[192:195], a[12:15]\n" \
"s_add_u32 s88, s42, 0x1000\n" \
"buffer_store_dwordx4 v[188:191], v20, s[80:83], s88 offen offset:1024 sc1\n" \
"s_waitcnt lgkmcnt(13)\n" \
"v_mfma_f32_16x16x32_bf16 a[8:11], a[128:131], a[168:171], a[8:11]\n" \
"ds_read_b64_tr_b16 a[242:243], v36 offset:9216\n" \
"s_waitcnt lgkmcnt(12)\n" \
"v_mfma_f32_16x16x32_bf16 a[8:11], a[192:195], a[232:235], a[8:11]\n" \
"ds_read_b64_tr_b16 a[180:181], v37 offset:0\n" \
"v_mfma_f32_16x16x32_bf16 a[24:27], a[132:135], a[168:171], a[24:27]\n" \
"ds_read_b64_tr_b16 a[182:183], v37 offset:1024\n" \
"v_mfma_f32_16x16x32_bf16 a[24:27], a[196:199], a[232:235], a[24:27]\n" \
"ds_read_b64_tr_b16 a[244:245], v37 offset:8192\n" \
"v_mfma_f32_16x16x32_bf16 a[28:31], a[172:175], a[132:135], a[28:31]\n" \
"s_add_u32 s6, s26, s6\n" \
"s_addc_u32 s9, s27, 0\n" \
"v_mfma_f32_16x16x32_bf16 a[28:31], a[236:239], a[196:199], a[28:31]\n" \
"s_addk_i32 s84, 0x2000\n" \
"v_mfma_f32_16x16x32_bf16 a[20:23], a[164:167], a[132:135], a[20:23]\n" \
"buffer_load_dwordx4 v25, s[76:79], s84 offen offset:1024 lds\n" \
"v_mfma_f32_16x16x32_bf16 a[20:23], a[228:231], a[196:199], a[20:23]\n" \
"ds_read_b64_tr_b16 a[246:247], v37 offset:9216\n" \
"v_mfma_f32_16x16x32_bf16 a[16:19], a[160:163], a[132:135], a[16:19]\n" \
"ds_read_b64_tr_b16 a[184:185], v38 offset:0\n" \
"v_mfma_f32_16x16x32_bf16 a[16:19], a[224:227], a[196:199], a[16:19]\n" \
"ds_read_b64_tr_b16 a[186:187], v38 offset:1024\n" \
"v_mfma_f32_16x16x32_bf16 a[32:35], a[160:163], a[136:139], a[32:35]\n" \
"s_addk_i32 s84, 0x2000\n" \
"s_nop 0\n" \
"v_mfma_f32_16x16x32_bf16 a[32:35], a[224:227], a[200:203], a[32:35]\n" \
"buffer_load_dwordx4 v24, s[76:79], s84 offen offset:2048 lds\n" \
"v_mfma_f32_16x16x32_bf16 a[36:39], a[164:167], a[136:139], a[36:39]\n" \
"ds_read_b64_tr_b16 a[248:249], v38 offset:8192\n" \
"v_mfma_f32_16x16x32_bf16 a[36:39], a[228:231], a[200:203], a[36:39]\n" \
"ds_read_b64_tr_b16 a[250:251], v38 offset:9216\n" \
"v_mfma_f32_16x16x32_bf16 a[44:47], a[172:175], a[136:139], a[44:47]\n" \
"ds_read_b64_tr_b16 a[188:189], v39 offset:0\n" \
"v_mfma_f32_16x16x32_bf16 a[44:47], a[236:239], a[200:203], a[44:47]\n" \
"ds_read_b64_tr_b16 a[190:191], v39 offset:1024\n" \
"v_mfma_f32_16x16x32_bf16 a[40:43], a[136:139], a[168:171], a[40:43]\n" \
"s_add_u32 s89, s42, 0x2000\n" \
"buffer_store_dwordx4 v[192:195], v20, s[80:83], s89 offen offset:0 sc1\n" \
"v_mfma_f32_16x16x32_bf16 a[40:43], a[200:203], a[232:235], a[40:43]\n" \
"ds_read_b64_tr_b16 a[252:253], v39 offset:8192\n" \
"v_mfma_f32_16x16x32_bf16 a[56:59], a[140:143], a[168:171], a[56:59]\n" \
"ds_read_b64_tr_b16 a[254:255], v39 offset:9216\n" \
"v_mfma_f32_16x16x32_bf16 a[56:59], a[204:207], a[232:235], a[56:59]\n" \
"s_add_u32 s8, s6, 0x1000000\n" \
"s_addc_u32 s6, s9, 0\n" \
"v_mfma_f32_16x16x32_bf16 a[60:63], a[172:175], a[140:143], a[60:63]\n" \
"s_and_b32 s9, s6, 0xffff\n" \
"s_lshl_b32 s6, s5, 16\n" \
"v_mfma_f32_16x16x32_bf16 a[60:63], a[236:239], a[204:207], a[60:63]\n" \
"s_addk_i32 s84, 0x2000\n" \
"v_mfma_f32_16x16x32_bf16 a[52:55], a[164:167], a[140:143], a[52:55]\n" \
"buffer_load_dwordx4 v25, s[76:79], s84 offen offset:3072 lds\n" \
"v_mfma_f32_16x16x32_bf16 a[52:55], a[228:231], a[204:207], a[52:55]\n" \
"s_sub_i32 s10, 0x100000, s6\n" \
"s_mov_b32 s6, 0x8000\n" \
"v_mfma_f32_16x16x32_bf16 a[48:51], a[160:163], a[140:143], a[48:51]\n" \
"s_cmp_lg_u32 s5, 0\n" \
".fill (L_pb + 32480 - .) / 4, 4, 0xBF800000\n" \
"L_pp8:\n" \
"s_cbranch_execz L_pret\n" \
"s_cselect_b32 s17, s11, s6\n" \
"v_mfma_f32_16x16x32_bf16 a[48:51], a[224:227], a[204:207], a[48:51]\n" \
"s_lshl_b32 s6, s3, 1\n" \
"s_lshl_b32 s5, s5, 3\n" \
"s_waitcnt lgkmcnt(15)\n" \
"v_mfma_f32_16x16x32_bf16 a[64:67], a[164:167], v[248:251], a[64:67]\n" \
"s_and_b32 s6, s6, 4\n" \
"s_or_b32 s12, s5, s6\n" \
"v_mfma_f32_16x16x32_bf16 a[64:67], a[228:231], v[48:51], a[64:67]\n" \
"s_sub_i32 s5, s13, s5\n" \
"s_or_b32 s5, s5, s6\n" \
"v_mfma_f32_16x16x32_bf16 a[80:83], a[172:175], v[248:251], a[80:83]\n" \
"s_or_b32 s6, s12, s13\n" \
"s_lshl_b32 s4, s4, 2\n" \
"v_mfma_f32_16x16x32_bf16 a[80:83], a[236:239], v[48:51], a[80:83]\n" \
"s_mulk_i32 s6, 0x92\n" \
"s_add_i32 s4, s4, 18\n" \
"v_mfma_f32_16x16x32_bf16 a[68:71], v[248:251], a[168:171], a[68:71]\n" \
"s_mulk_i32 s5, 0x92\n" \
"s_add_i32 s6, s4, s6\n" \
"s_waitcnt vmcnt(10) lgkmcnt(0)\n" \
"v_mfma_f32_16x16x32_bf16 a[68:71], v[48:51], a[232:235], a[68:71]\n" \
"s_barrier\n" \
"v_mfma_f32_16x16x32_bf16 v[52:55], a[176:179], a[128:131], v[52:55]\n" \
"s_add_u32 m0, s40, 0x0\n" \
"s_add_u32 s85, s39, 0x10000\n" \
"v_mfma_f32_16x16x32_bf16 v[52:55], a[240:243], a[192:195], v[52:55]\n" \
"buffer_load_dwordx4 v20, s[80:83], s85 offen sc1 lds\n" \
"v_mfma_f32_16x16x32_bf16 v[56:59], a[180:183], a[128:131], v[56:59]\n" \
"ds_read_b64_tr_b16 a[160:161], v32 offset:32768\n" \
"s_add_i32 s5, s5, s4\n" \
"v_mfma_f32_16x16x32_bf16 v[56:59], a[244:247], a[192:195], v[56:59]\n" \
"ds_read_b64_tr_b16 a[162:163], v32 offset:33280\n" \
"s_lshl_b32 s4, s3, 11\n" \
"v_mfma_f32_16x16x32_bf16 v[60:63], a[184:187], a[128:131], v[60:63]\n" \
"s_lshl_b32 s15, s13, 12\n" \
"s_lshl_b32 s16, s19, 14\n" \
"v_mfma_f32_16x16x32_bf16 v[60:63], a[248:251], a[192:195], v[60:63]\n" \
"ds_read_b64_tr_b16 a[224:225], v32 offset:36864\n" \
"s_mul_i32 s19, s19, 0xe000\n" \
"v_mfma_f32_16x16x32_bf16 v[64:67], a[188:191], a[128:131], v[64:67]\n" \
"s_mov_b32 s11, 0x20000\n" \
"s_or_b32 s12, s4, 0xc000\n" \
"v_mfma_f32_16x16x32_bf16 v[64:67], a[252:255], a[192:195], v[64:67]\n" \
"ds_read_b128 a[128:131], v28 offset:32768\n" \
"s_lshl_b32 s4, s3, 8\n" \
"v_mfma_f32_16x16x32_bf16 v[80:83], a[188:191], a[132:135], v[80:83]\n" \
"ds_read_b128 a[192:195], v29 offset:32768\n" \
"s_or_b32 s22, s15, s16\n" \
"v_mfma_f32_16x16x32_bf16 v[80:83], a[252:255], a[196:199], v[80:83]\n" \
"s_add_u32 m0, s40, 0x1000\n" \
"s_add_u32 s85, s39, 0x11000\n" \
"v_mfma_f32_16x16x32_bf16 v[76:79], a[184:187], a[132:135], v[76:79]\n" \
"buffer_load_dwordx4 v20, s[80:83], s85 offen sc1 lds\n" \
"v_mfma_f32_16x16x32_bf16 v[76:79], a[248:251], a[196:199], v[76:79]\n" \
"ds_read_b64_tr_b16 a[226:227], v32 offset:37376\n" \
"s_or_b32 s23, s19, s15\n" \
"v_mfma_f32_16x16x32_bf16 v[72:75], a[180:183], a[132:135], v[72:75]\n" \
"ds_read_b64_tr_b16 a[164:165], v33 offset:32768\n" \
"s_lshl_b32 s60, s6, 10\n" \
"v_mfma_f32_16x16x32_bf16 v[72:75], a[244:247], a[196:199], v[72:75]\n" \
"s_lshl_b32 s61, s5, 10\n" \
"s_lshl_b32 s14, s14, 15\n" \
"v_mfma_f32_16x16x32_bf16 v[68:71], a[176:179], a[132:135], v[68:71]\n" \
"ds_read_b64_tr_b16 a[166:167], v33 offset:33280\n" \
"s_or_b32 s20, s4, 0x26c00\n" \
"v_mfma_f32_16x16x32_bf16 v[68:71], a[240:243], a[196:199], v[68:71]\n" \
"ds_read_b128 a[132:135], v28 offset:34816\n" \
"s_or_b32 s13, s22, 0x8000\n" \
"v_mfma_f32_16x16x32_bf16 v[84:87], a[176:179], a[136:139], v[84:87]\n" \
"ds_read_b128 a[196:199], v29 offset:34816\n" \
"s_add_i32 s22, s22, 0x14000\n" \
"v_mfma_f32_16x16x32_bf16 v[84:87], a[240:243], a[200:203], v[84:87]\n" \
"s_add_u32 m0, s40, 0x3000\n" \
"s_movk_i32 s84, 0x400\n" \
"v_mfma_f32_16x16x32_bf16 v[88:91], a[180:183], a[136:139], v[88:91]\n" \
"buffer_load_dwordx4 v21, s[76:79], s84 offen offset:0 lds\n" \
"v_mfma_f32_16x16x32_bf16 v[88:91], a[244:247], a[200:203], v[88:91]\n" \
"ds_read_b64_tr_b16 a[228:229], v33 offset:36864\n" \
"s_bitset1_b32 s23, 16\n" \
"v_mfma_f32_16x16x32_bf16 v[92:95], a[184:187], a[136:139], v[92:95]\n" \
"ds_read_b64_tr_b16 a[230:231], v33 offset:37376\n" \
"s_or_b32 s24, s7, 0x5be8020\n" \
"v_mfma_f32_16x16x32_bf16 v[92:95], a[248:251], a[200:203], v[92:95]\n" \
"s_add_u32 s88, s42, 0x2000\n" \
"buffer_store_dwordx4 v[216:219], v20, s[80:83], s88 offen offset:1024 sc1\n" \
"v_mfma_f32_16x16x32_bf16 v[96:99], a[188:191], a[136:139], v[96:99]\n" \
"s_and_b32 s57, s27, 0xffff\n" \
"s_mov_b32 s58, 0x6000000\n" \
"v_mfma_f32_16x16x32_bf16 v[96:99], a[252:255], a[200:203], v[96:99]\n" \
"ds_read_b128 a[136:139], v28 offset:36864\n" \
"s_mov_b32 s56, s26\n" \
"v_mfma_f32_16x16x32_bf16 v[112:115], a[188:191], a[140:143], v[112:115]\n" \
"ds_read_b128 a[200:203], v29 offset:36864\n" \
"s_mov_b32 s59, s11\n" \
"v_mfma_f32_16x16x32_bf16 v[112:115], a[252:255], a[204:207], v[112:115]\n" \
"ds_read_b64_tr_b16 a[172:173], v35 offset:32768\n" \
"s_add_i32 s60, s60, 0x3000000\n" \
"v_mfma_f32_16x16x32_bf16 v[108:111], a[184:187], a[140:143], v[108:111]\n" \
"ds_read_b64_tr_b16 a[174:175], v35 offset:33280\n" \
"s_add_i32 s61, s61, 0x411c000\n" \
"v_mfma_f32_16x16x32_bf16 v[108:111], a[248:251], a[204:207], v[108:111]\n" \
"s_sub_i32 s19, 14, s18\n" \
"s_mov_b32 s21, 0x71800000\n" \
"v_mfma_f32_16x16x32_bf16 v[104:107], a[180:183], a[140:143], v[104:107]\n" \
"s_add_u32 m0, s31, 0x0\n" \
"s_mov_b32 s84, 0x400000\n" \
"v_mfma_f32_16x16x32_bf16 v[104:107], a[244:247], a[204:207], v[104:107]\n" \
"buffer_load_dwordx4 v26, s[68:71], s84 offen offset:0 lds\n" \
"v_mfma_f32_16x16x32_bf16 v[100:103], a[176:179], a[140:143], v[100:103]\n" \
"ds_read_b64_tr_b16 a[236:237], v35 offset:36864\n" \
"s_mov_b32 s4, L_gsw-L_gtr\n" \
"v_mfma_f32_16x16x32_bf16 v[100:103], a[240:243], a[204:207], v[100:103]\n" \
"ds_read_b128 a[140:143], v28 offset:38912\n" \
"v_mfma_f32_16x16x32_bf16 v[116:119], a[176:179], a[144:147], v[116:119]\n" \
"ds_read_b128 a[204:207], v29 offset:38912\n" \
"v_mfma_f32_16x16x32_bf16 v[116:119], a[240:243], a[208:211], v[116:119]\n" \
"ds_read_b64_tr_b16 a[238:239], v35 offset:37376\n" \
"v_mfma_f32_16x16x32_bf16 v[120:123], a[180:183], a[144:147], v[120:123]\n" \
"ds_read_b64_tr_b16 a[168:169], v34 offset:32768\n" \
"v_mfma_f32_16x16x32_bf16 v[120:123], a[244:247], a[208:211], v[120:123]\n" \
"s_addk_i32 s84, 0x7c00\n" \
"v_mfma_f32_16x16x32_bf16 v[124:127], a[184:187], a[144:147], v[124:127]\n" \
"buffer_load_dwordx4 v26, s[68:71], s84 offen offset:1024 lds\n" \
"v_mfma_f32_16x16x32_bf16 v[124:127], a[248:251], a[208:211], v[124:127]\n" \
"ds_read_b64_tr_b16 a[170:171], v34 offset:33280\n" \
"v_mfma_f32_16x16x32_bf16 v[128:131], a[188:191], a[144:147], v[128:131]\n" \
"s_cmp_eq_u32 s19, 0\n" \
"s_cselect_b32 s4, L_gs7-L_gtr, s4\n" \
"v_mfma_f32_16x16x32_bf16 v[128:131], a[252:255], a[208:211], v[128:131]\n" \
"ds_read_b128 a[144:147], v28 offset:40960\n" \
"v_mfma_f32_16x16x32_bf16 v[144:147], a[188:191], a[148:151], v[144:147]\n" \
"ds_read_b128 a[208:211], v29 offset:40960\n" \
"v_mfma_f32_16x16x32_bf16 v[144:147], a[252:255], a[212:215], v[144:147]\n" \
"ds_read_b64_tr_b16 a[232:233], v34 offset:36864\n" \
"v_mfma_f32_16x16x32_bf16 v[140:143], a[184:187], a[148:151], v[140:143]\n" \
"s_addk_i32 s84, 0x7c00\n" \
"v_mfma_f32_16x16x32_bf16 v[140:143], a[248:251], a[212:215], v[140:143]\n" \
"buffer_load_dwordx4 v27, s[68:71], s84 offen offset:2048 lds\n" \
"v_mfma_f32_16x16x32_bf16 v[136:139], a[180:183], a[148:151], v[136:139]\n" \
"ds_read_b64_tr_b16 a[234:235], v34 offset:37376\n" \
"v_mfma_f32_16x16x32_bf16 v[136:139], a[244:247], a[212:215], v[136:139]\n" \
"s_cmp_eq_u32 s16, 0\n" \
"s_cselect_b32 s4, 0, s4\n" \
"v_mfma_f32_16x16x32_bf16 v[132:135], a[176:179], a[148:151], v[132:135]\n" \
"ds_read_b128 v[248:251], v22 offset:32768\n" \
"v_mfma_f32_16x16x32_bf16 v[132:135], a[240:243], a[212:215], v[132:135]\n" \
"ds_read_b128 a[148:151], v28 offset:43008\n" \
"v_mfma_f32_16x16x32_bf16 v[148:151], a[176:179], a[152:155], v[148:151]\n" \
"ds_read_b128 a[212:215], v29 offset:43008\n" \
"v_mfma_f32_16x16x32_bf16 v[148:151], a[240:243], a[216:219], v[148:151]\n" \
"s_addk_i32 s84, 0x7c00\n" \
"v_mfma_f32_16x16x32_bf16 v[152:155], a[180:183], a[152:155], v[152:155]\n" \
"buffer_load_dwordx4 v27, s[68:71], s84 offen offset:3072 lds\n" \
"v_mfma_f32_16x16x32_bf16 v[152:155], a[244:247], a[216:219], v[152:155]\n" \
"ds_read_b128 v[48:51], v23 offset:32768\n" \
"v_mfma_f32_16x16x32_bf16 v[156:159], a[184:187], a[152:155], v[156:159]\n" \
"s_getpc_b64 s[28:29]\n" \
"L_gp0_%=:\n" \
"s_add_u32 s28, s28, L_gtr-L_gp0_%=\n" \
"s_addc_u32 s29, s29, 0\n" \
"v_mfma_f32_16x16x32_bf16 v[156:159], a[248:251], a[216:219], v[156:159]\n" \
"s_add_u32 s28, s28, s4\n" \
"s_addc_u32 s29, s29, 0\n" \
"v_mfma_f32_16x16x32_bf16 v[160:163], a[188:191], a[152:155], v[160:163]\n" \
"v_mfma_f32_16x16x32_bf16 v[160:163], a[252:255], a[216:219], v[160:163]\n" \
"ds_read_b128 a[152:155], v28 offset:45056\n" \
"v_mfma_f32_16x16x32_bf16 v[176:179], a[188:191], a[156:159], v[176:179]\n" \
"ds_read_b128 a[216:219], v29 offset:45056\n" \
"v_mfma_f32_16x16x32_bf16 v[176:179], a[252:255], a[220:223], v[176:179]\n" \
"v_mfma_f32_16x16x32_bf16 v[172:175], a[184:187], a[156:159], v[172:175]\n" \
"v_mfma_f32_16x16x32_bf16 v[172:175], a[248:251], a[220:223], v[172:175]\n" \
"v_mfma_f32_16x16x32_bf16 v[168:171], a[180:183], a[156:159], v[168:171]\n" \
"v_mfma_f32_16x16x32_bf16 v[168:171], a[244:247], a[220:223], v[168:171]\n" \
"v_mfma_f32_16x16x32_bf16 v[164:167], a[176:179], a[156:159], v[164:167]\n" \
"v_mfma_f32_16x16x32_bf16 v[164:167], a[240:243], a[220:223], v[164:167]\n" \
"ds_read_b128 a[156:159], v28 offset:47104\n" \
"ds_read_b128 a[220:223], v29 offset:47104\n" \
"s_waitcnt lgkmcnt(15)\n" \
"s_nop 0\n" \
"v_mfma_f32_16x16x32_bf16 a[0:3], a[160:163], a[128:131], a[0:3]\n" \
"s_add_u32 s89, s42, 0x3000\n" \
"buffer_store_dwordx4 v[220:223], v20, s[80:83], s89 offen offset:0 sc1\n" \
"v_mfma_f32_16x16x32_bf16 a[0:3], a[224:227], a[192:195], a[0:3]\n" \
"ds_read_b64_tr_b16 a[176:177], v36 offset:32768\n" \
"v_mfma_f32_16x16x32_bf16 a[4:7], a[164:167], a[128:131], a[4:7]\n" \
"ds_read_b64_tr_b16 a[178:179], v36 offset:33792\n" \
"v_mfma_f32_16x16x32_bf16 a[4:7], a[228:231], a[192:195], a[4:7]\n" \
"ds_read_b64_tr_b16 a[240:241], v36 offset:40960\n" \
"v_mfma_f32_16x16x32_bf16 a[12:15], a[172:175], a[128:131], a[12:15]\n" \
"ds_read_b64_tr_b16 a[242:243], v36 offset:41984\n" \
"s_waitcnt lgkmcnt(15)\n" \
"v_mfma_f32_16x16x32_bf16 a[12:15], a[236:239], a[192:195], a[12:15]\n" \
"v_mfma_f32_16x16x32_bf16 a[8:11], a[128:131], a[168:171], a[8:11]\n" \
"ds_read_b64_tr_b16 a[180:181], v37 offset:32768\n" \
"s_waitcnt lgkmcnt(13)\n" \
"v_mfma_f32_16x16x32_bf16 a[8:11], a[192:195], a[232:235], a[8:11]\n" \
"v_mfma_f32_16x16x32_bf16 a[24:27], a[132:135], a[168:171], a[24:27]\n" \
"ds_read_b64_tr_b16 a[182:183], v37 offset:33792\n" \
"v_mfma_f32_16x16x32_bf16 a[24:27], a[196:199], a[232:235], a[24:27]\n" \
"ds_read_b64_tr_b16 a[244:245], v37 offset:40960\n" \
"v_mfma_f32_16x16x32_bf16 a[28:31], a[172:175], a[132:135], a[28:31]\n" \
"v_mfma_f32_16x16x32_bf16 a[28:31], a[236:239], a[196:199], a[28:31]\n" \
"ds_read_b64_tr_b16 a[246:247], v37 offset:41984\n" \
"v_mfma_f32_16x16x32_bf16 a[20:23], a[164:167], a[132:135], a[20:23]\n" \
"v_mfma_f32_16x16x32_bf16 a[20:23], a[228:231], a[196:199], a[20:23]\n" \
"ds_read_b64_tr_b16 a[184:185], v38 offset:32768\n" \
"v_mfma_f32_16x16x32_bf16 a[16:19], a[160:163], a[132:135], a[16:19]\n" \
"ds_read_b64_tr_b16 a[186:187], v38 offset:33792\n" \
"v_mfma_f32_16x16x32_bf16 a[16:19], a[224:227], a[196:199], a[16:19]\n" \
"v_mfma_f32_16x16x32_bf16 a[32:35], a[160:163], a[136:139], a[32:35]\n" \
"s_add_u32 s88, s42, 0x3000\n" \
"buffer_store_dwordx4 v[224:227], v20, s[80:83], s88 offen offset:1024 sc1\n" \
"v_mfma_f32_16x16x32_bf16 a[32:35], a[224:227], a[200:203], a[32:35]\n" \
"ds_read_b64_tr_b16 a[248:249], v38 offset:40960\n" \
"v_mfma_f32_16x16x32_bf16 a[36:39], a[164:167], a[136:139], a[36:39]\n" \
"v_lshlrev_b32_e32 v45, 3, v20\n" \
"v_add_u32_e32 v45, s30, v45\n" \
"v_mfma_f32_16x16x32_bf16 a[36:39], a[228:231], a[200:203], a[36:39]\n" \
"s_getpc_b64 s[86:87]\n" \
"L_ct0_%=:\n" \
"s_add_u32 s86, s86, L_ap0-L_ct0_%=\n" \
"s_addc_u32 s87, s87, 0\n" \
"s_add_u32 m0, s40, 0x2000\n" \
"v_mfma_f32_16x16x32_bf16 a[44:47], a[172:175], a[136:139], a[44:47]\n" \
"global_load_lds_dword v45, s[86:87]\n" \
"v_mfma_f32_16x16x32_bf16 a[44:47], a[236:239], a[200:203], a[44:47]\n" \
"ds_read_b64_tr_b16 a[250:251], v38 offset:41984\n" \
"v_mfma_f32_16x16x32_bf16 a[40:43], a[136:139], a[168:171], a[40:43]\n" \
"ds_read_b64_tr_b16 a[188:189], v39 offset:32768\n" \
"v_mfma_f32_16x16x32_bf16 a[40:43], a[200:203], a[232:235], a[40:43]\n" \
"ds_read_b64_tr_b16 a[190:191], v39 offset:33792\n" \
"v_mfma_f32_16x16x32_bf16 a[56:59], a[140:143], a[168:171], a[56:59]\n" \
"ds_read_b64_tr_b16 a[252:253], v39 offset:40960\n" \
"v_mfma_f32_16x16x32_bf16 a[56:59], a[204:207], a[232:235], a[56:59]\n" \
"ds_read_b64_tr_b16 a[254:255], v39 offset:41984\n" \
"v_mfma_f32_16x16x32_bf16 a[60:63], a[172:175], a[140:143], a[60:63]\n" \
"v_mfma_f32_16x16x32_bf16 a[60:63], a[236:239], a[204:207], a[60:63]\n" \
"v_mfma_f32_16x16x32_bf16 a[52:55], a[164:167], a[140:143], a[52:55]\n" \
"v_mfma_f32_16x16x32_bf16 a[52:55], a[228:231], a[204:207], a[52:55]\n" \
"v_mfma_f32_16x16x32_bf16 a[48:51], a[160:163], a[140:143], a[48:51]\n" \
"v_mfma_f32_16x16x32_bf16 a[48:51], a[224:227], a[204:207], a[48:51]\n" \
"s_waitcnt lgkmcnt(15)\n" \
"v_mfma_f32_16x16x32_bf16 a[64:67], a[164:167], v[248:251], a[64:67]\n" \
"v_mfma_f32_16x16x32_bf16 a[64:67], a[228:231], v[48:51], a[64:67]\n" \
"v_mfma_f32_16x16x32_bf16 a[80:83], a[172:175], v[248:251], a[80:83]\n" \
"v_mfma_f32_16x16x32_bf16 a[80:83], a[236:239], v[48:51], a[80:83]\n" \
"v_mfma_f32_16x16x32_bf16 a[68:71], v[248:251], a[168:171], a[68:71]\n" \
"s_waitcnt vmcnt(3) lgkmcnt(0)\n" \
"v_mfma_f32_16x16x32_bf16 a[68:71], v[48:51], a[232:235], a[68:71]\n" \
"s_barrier\n" \
"v_xor_b32_e32 v28, 0x20000, v28\n" \
"v_xor_b32_e32 v29, 0x20000, v29\n" \
"v_mfma_f32_16x16x32_bf16 v[52:55], a[176:179], a[128:131], v[52:55]\n" \
"ds_read_b64_tr_b16 a[160:161], v32 offset:0\n" \
"v_accvgpr_read_b32 v184, a0\n" \
"v_mfma_f32_16x16x32_bf16 v[52:55], a[240:243], a[192:195], v[52:55]\n" \
"v_accvgpr_read_b32 v185, a1\n" \
"v_accvgpr_read_b32 v186, a2\n" \
"v_mfma_f32_16x16x32_bf16 v[56:59], a[180:183], a[128:131], v[56:59]\n" \
"ds_read_b64_tr_b16 a[162:163], v32 offset:512\n" \
"v_accvgpr_read_b32 v187, a3\n" \
"v_mfma_f32_16x16x32_bf16 v[56:59], a[244:247], a[192:195], v[56:59]\n" \
"v_accvgpr_read_b32 v188, a4\n" \
"v_accvgpr_read_b32 v189, a5\n" \
"v_mfma_f32_16x16x32_bf16 v[60:63], a[184:187], a[128:131], v[60:63]\n" \
"v_accvgpr_read_b32 v190, a6\n" \
"v_accvgpr_read_b32 v191, a7\n" \
"v_mfma_f32_16x16x32_bf16 v[60:63], a[248:251], a[192:195], v[60:63]\n" \
"ds_read_b64_tr_b16 a[224:225], v32 offset:4096\n" \
"v_accvgpr_read_b32 v196, a12\n" \
"v_mfma_f32_16x16x32_bf16 v[64:67], a[188:191], a[128:131], v[64:67]\n" \
"v_accvgpr_read_b32 v197, a13\n" \
"v_accvgpr_read_b32 v198, a14\n" \
"v_mfma_f32_16x16x32_bf16 v[64:67], a[252:255], a[192:195], v[64:67]\n" \
"ds_read_b128 a[128:131], v28 offset:0\n" \
"v_accvgpr_read_b32 v199, a15\n" \
"v_mfma_f32_16x16x32_bf16 v[80:83], a[188:191], a[132:135], v[80:83]\n" \
"ds_read_b128 a[192:195], v29 offset:0\n" \
"v_accvgpr_read_b32 v192, a8\n" \
"v_mfma_f32_16x16x32_bf16 v[80:83], a[252:255], a[196:199], v[80:83]\n" \
"ds_read_b64_tr_b16 a[226:227], v32 offset:4608\n" \
"v_accvgpr_read_b32 v193, a9\n" \
"v_mfma_f32_16x16x32_bf16 v[76:79], a[184:187], a[132:135], v[76:79]\n" \
"v_accvgpr_read_b32 v194, a10\n" \
"v_accvgpr_read_b32 v195, a11\n" \
"v_mfma_f32_16x16x32_bf16 v[76:79], a[248:251], a[196:199], v[76:79]\n" \
"ds_read_b64_tr_b16 a[164:165], v33 offset:0\n" \
"v_accvgpr_read_b32 v208, a24\n" \
"v_mfma_f32_16x16x32_bf16 v[72:75], a[180:183], a[132:135], v[72:75]\n" \
"v_accvgpr_read_b32 v209, a25\n" \
"v_accvgpr_read_b32 v210, a26\n" \
"v_mfma_f32_16x16x32_bf16 v[72:75], a[244:247], a[196:199], v[72:75]\n" \
"v_accvgpr_read_b32 v211, a27\n" \
"v_accvgpr_read_b32 v212, a28\n" \
"v_mfma_f32_16x16x32_bf16 v[68:71], a[176:179], a[132:135], v[68:71]\n" \
"ds_read_b64_tr_b16 a[166:167], v33 offset:512\n" \
"v_accvgpr_read_b32 v213, a29\n" \
"v_mfma_f32_16x16x32_bf16 v[68:71], a[240:243], a[196:199], v[68:71]\n" \
"ds_read_b128 a[132:135], v28 offset:2048\n" \
"v_accvgpr_read_b32 v214, a30\n" \
"v_mfma_f32_16x16x32_bf16 v[84:87], a[176:179], a[136:139], v[84:87]\n" \
"ds_read_b128 a[196:199], v29 offset:2048\n" \
"v_accvgpr_read_b32 v215, a31\n" \
"v_mfma_f32_16x16x32_bf16 v[84:87], a[240:243], a[200:203], v[84:87]\n" \
"ds_read_b64_tr_b16 a[228:229], v33 offset:4096\n" \
"v_accvgpr_read_b32 v204, a20\n" \
"v_mfma_f32_16x16x32_bf16 v[88:91], a[180:183], a[136:139], v[88:91]\n" \
"v_accvgpr_read_b32 v205, a21\n" \
"v_accvgpr_read_b32 v206, a22\n" \
"v_mfma_f32_16x16x32_bf16 v[88:91], a[244:247], a[200:203], v[88:91]\n" \
"v_accvgpr_read_b32 v207, a23\n" \
"v_accvgpr_read_b32 v200, a16\n" \
"v_mfma_f32_16x16x32_bf16 v[92:95], a[184:187], a[136:139], v[92:95]\n" \
"ds_read_b64_tr_b16 a[230:231], v33 offset:4608\n" \
"v_accvgpr_read_b32 v201, a17\n" \
"v_mfma_f32_16x16x32_bf16 v[92:95], a[248:251], a[200:203], v[92:95]\n" \
"v_accvgpr_read_b32 v202, a18\n" \
"v_accvgpr_read_b32 v203, a19\n" \
"v_mfma_f32_16x16x32_bf16 v[96:99], a[188:191], a[136:139], v[96:99]\n" \
"v_accvgpr_read_b32 v216, a32\n" \
"v_accvgpr_read_b32 v217, a33\n" \
"v_mfma_f32_16x16x32_bf16 v[96:99], a[252:255], a[200:203], v[96:99]\n" \
"ds_read_b128 a[136:139], v28 offset:4096\n" \
"v_accvgpr_read_b32 v218, a34\n" \
"v_mfma_f32_16x16x32_bf16 v[112:115], a[188:191], a[140:143], v[112:115]\n" \
"ds_read_b128 a[200:203], v29 offset:4096\n" \
"v_accvgpr_read_b32 v219, a35\n" \
"v_mfma_f32_16x16x32_bf16 v[112:115], a[252:255], a[204:207], v[112:115]\n" \
"ds_read_b64_tr_b16 a[172:173], v35 offset:0\n" \
"v_accvgpr_read_b32 v220, a36\n" \
"v_mfma_f32_16x16x32_bf16 v[108:111], a[184:187], a[140:143], v[108:111]\n" \
"ds_read_b64_tr_b16 a[174:175], v35 offset:512\n" \
"v_accvgpr_read_b32 v221, a37\n" \
"v_mfma_f32_16x16x32_bf16 v[108:111], a[248:251], a[204:207], v[108:111]\n" \
"v_accvgpr_read_b32 v222, a38\n" \
"v_accvgpr_read_b32 v223, a39\n" \
"v_mfma_f32_16x16x32_bf16 v[104:107], a[180:183], a[140:143], v[104:107]\n" \
"ds_read_b64_tr_b16 a[236:237], v35 offset:4096\n" \
"v_accvgpr_read_b32 v228, a44\n" \
"v_mfma_f32_16x16x32_bf16 v[104:107], a[244:247], a[204:207], v[104:107]\n" \
"v_accvgpr_read_b32 v229, a45\n" \
"v_accvgpr_read_b32 v230, a46\n" \
"v_mfma_f32_16x16x32_bf16 v[100:103], a[176:179], a[140:143], v[100:103]\n" \
"v_accvgpr_read_b32 v231, a47\n" \
"v_accvgpr_read_b32 v224, a40\n" \
"v_mfma_f32_16x16x32_bf16 v[100:103], a[240:243], a[204:207], v[100:103]\n" \
"ds_read_b128 a[140:143], v28 offset:6144\n" \
"v_accvgpr_read_b32 v225, a41\n" \
"v_mfma_f32_16x16x32_bf16 v[116:119], a[176:179], a[144:147], v[116:119]\n" \
"ds_read_b128 a[204:207], v29 offset:6144\n" \
"v_accvgpr_read_b32 v226, a42\n" \
"v_mfma_f32_16x16x32_bf16 v[116:119], a[240:243], a[208:211], v[116:119]\n" \
"ds_read_b64_tr_b16 a[238:239], v35 offset:4608\n" \
"v_accvgpr_read_b32 v227, a43\n" \
"v_mfma_f32_16x16x32_bf16 v[120:123], a[180:183], a[144:147], v[120:123]\n" \
"ds_read_b64_tr_b16 a[168:169], v34 offset:0\n" \
"v_accvgpr_read_b32 v240, a56\n" \
"v_mfma_f32_16x16x32_bf16 v[120:123], a[244:247], a[208:211], v[120:123]\n" \
"v_accvgpr_read_b32 v241, a57\n" \
"v_accvgpr_read_b32 v242, a58\n" \
"v_mfma_f32_16x16x32_bf16 v[124:127], a[184:187], a[144:147], v[124:127]\n" \
"v_accvgpr_read_b32 v243, a59\n" \
"v_accvgpr_read_b32 v244, a60\n" \
"v_mfma_f32_16x16x32_bf16 v[124:127], a[248:251], a[208:211], v[124:127]\n" \
"ds_read_b64_tr_b16 a[170:171], v34 offset:512\n" \
"v_accvgpr_read_b32 v245, a61\n" \
"v_mfma_f32_16x16x32_bf16 v[128:131], a[188:191], a[144:147], v[128:131]\n" \
"v_accvgpr_read_b32 v246, a62\n" \
"v_accvgpr_read_b32 v247, a63\n" \
"v_mfma_f32_16x16x32_bf16 v[128:131], a[252:255], a[208:211], v[128:131]\n" \
"ds_read_b128 a[144:147], v28 offset:8192\n" \
"v_accvgpr_read_b32 v236, a52\n" \
"v_mfma_f32_16x16x32_bf16 v[144:147], a[188:191], a[148:151], v[144:147]\n" \
"ds_read_b128 a[208:211], v29 offset:8192\n" \
"v_accvgpr_read_b32 v237, a53\n" \
"v_mfma_f32_16x16x32_bf16 v[144:147], a[252:255], a[212:215], v[144:147]\n" \
"ds_read_b64_tr_b16 a[232:233], v34 offset:4096\n" \
"v_accvgpr_read_b32 v238, a54\n" \
"v_mfma_f32_16x16x32_bf16 v[140:143], a[184:187], a[148:151], v[140:143]\n" \
"v_accvgpr_read_b32 v239, a55\n" \
"v_accvgpr_read_b32 v232, a48\n" \
"v_mfma_f32_16x16x32_bf16 v[140:143], a[248:251], a[212:215], v[140:143]\n" \
"ds_read_b64_tr_b16 a[234:235], v34 offset:4608\n" \
"v_accvgpr_read_b32 v233, a49\n" \
"v_mfma_f32_16x16x32_bf16 v[136:139], a[180:183], a[148:151], v[136:139]\n" \
"v_accvgpr_read_b32 v234, a50\n" \
"v_accvgpr_read_b32 v235, a51\n" \
"v_mfma_f32_16x16x32_bf16 v[136:139], a[244:247], a[212:215], v[136:139]\n" \
"v_accvgpr_read_b32 v180, a64\n" \
"v_accvgpr_read_b32 v181, a65\n" \
"v_mfma_f32_16x16x32_bf16 v[132:135], a[176:179], a[148:151], v[132:135]\n" \
"ds_read_b128 v[248:251], v22 offset:0\n" \
"v_accvgpr_read_b32 v182, a66\n" \
"v_mfma_f32_16x16x32_bf16 v[132:135], a[240:243], a[212:215], v[132:135]\n" \
"ds_read_b128 a[148:151], v28 offset:10240\n" \
"v_accvgpr_read_b32 v183, a67\n" \
"v_mfma_f32_16x16x32_bf16 v[148:151], a[176:179], a[152:155], v[148:151]\n" \
"ds_read_b128 a[212:215], v29 offset:10240\n" \
"v_accvgpr_read_b32 v252, a68\n" \
"v_mfma_f32_16x16x32_bf16 v[148:151], a[240:243], a[216:219], v[148:151]\n" \
"ds_read_b128 v[48:51], v23 offset:0\n" \
"v_accvgpr_read_b32 v253, a69\n" \
"v_mfma_f32_16x16x32_bf16 v[152:155], a[180:183], a[152:155], v[152:155]\n" \
"v_accvgpr_read_b32 v254, a70\n" \
"v_accvgpr_read_b32 v255, a71\n" \
"v_mfma_f32_16x16x32_bf16 v[152:155], a[244:247], a[216:219], v[152:155]\n" \
"v_lshrrev_b32_e32 v24, 9, v20\n" \
"v_and_b32_e32 v26, 0x1ff, v20\n" \
"v_mfma_f32_16x16x32_bf16 v[156:159], a[184:187], a[152:155], v[156:159]\n" \
"v_lshl_add_u32 v24, v24, 3, v26\n" \
"v_lshrrev_b32_e64 v25, 8, v20\n" \
"v_mfma_f32_16x16x32_bf16 v[156:159], a[248:251], a[216:219], v[156:159]\n" \
"v_mul_u32_u24_e32 v25, 0x24700, v25\n" \
"v_add_u32_e64 v25, v25, v20\n" \
"v_mfma_f32_16x16x32_bf16 v[160:163], a[188:191], a[152:155], v[160:163]\n" \
"v_lshrrev_b32_e32 v26, 2, v20\n" \
"v_add_u32_e32 v26, s45, v26\n" \
"v_mfma_f32_16x16x32_bf16 v[160:163], a[252:255], a[216:219], v[160:163]\n" \
"ds_read_b128 a[152:155], v28 offset:12288\n" \
"v_add_u32_e64 v27, s46, v20\n" \
"v_mfma_f32_16x16x32_bf16 v[176:179], a[188:191], a[156:159], v[176:179]\n" \
"ds_read_b128 a[216:219], v29 offset:12288\n" \
"v_add_u32_e64 v21, s43, v27\n" \
"v_mfma_f32_16x16x32_bf16 v[176:179], a[252:255], a[220:223], v[176:179]\n" \
"v_mfma_f32_16x16x32_bf16 v[172:175], a[184:187], a[156:159], v[172:175]\n" \
"v_mfma_f32_16x16x32_bf16 v[172:175], a[248:251], a[220:223], v[172:175]\n" \
"v_mfma_f32_16x16x32_bf16 v[168:171], a[180:183], a[156:159], v[168:171]\n" \
"v_mfma_f32_16x16x32_bf16 v[168:171], a[244:247], a[220:223], v[168:171]\n" \
"v_mfma_f32_16x16x32_bf16 v[164:167], a[176:179], a[156:159], v[164:167]\n" \
"v_mfma_f32_16x16x32_bf16 v[164:167], a[240:243], a[220:223], v[164:167]\n" \
"ds_read_b128 a[156:159], v28 offset:14336\n" \
"ds_read_b128 a[220:223], v29 offset:14336\n" \
"s_waitcnt lgkmcnt(15)\n" \
"s_nop 0\n" \
"v_mfma_f32_16x16x32_bf16 v[184:187], a[160:163], a[128:131], v[184:187]\n" \
"v_bfe_u32 v44, v20, 2, 5\n" \
"ds_read_b64_tr_b16 a[176:177], v36 offset:0\n" \
"v_mfma_f32_16x16x32_bf16 v[184:187], a[224:227], a[192:195], v[184:187]\n" \
"buffer_load_dword v44, v44, s[80:83], s38 offen offset:0 sc1\n" \
"v_mfma_f32_16x16x32_bf16 v[188:191], a[164:167], a[128:131], v[188:191]\n" \
"ds_read_b64_tr_b16 a[178:179], v36 offset:1024\n" \
"v_mfma_f32_16x16x32_bf16 v[188:191], a[228:231], a[192:195], v[188:191]\n" \
"ds_read_b64_tr_b16 a[240:241], v36 offset:8192\n" \
"v_mfma_f32_16x16x32_bf16 v[196:199], a[172:175], a[128:131], v[196:199]\n" \
"ds_read_b64_tr_b16 a[242:243], v36 offset:9216\n" \
"s_waitcnt lgkmcnt(15)\n" \
"v_mfma_f32_16x16x32_bf16 v[196:199], a[236:239], a[192:195], v[196:199]\n" \
"v_mfma_f32_16x16x32_bf16 v[192:195], a[128:131], a[168:171], v[192:195]\n" \
"ds_read_b64_tr_b16 a[180:181], v37 offset:0\n" \
"s_waitcnt lgkmcnt(13)\n" \
"v_mfma_f32_16x16x32_bf16 v[192:195], a[192:195], a[232:235], v[192:195]\n" \
"v_mfma_f32_16x16x32_bf16 v[208:211], a[132:135], a[168:171], v[208:211]\n" \
"ds_read_b64_tr_b16 a[182:183], v37 offset:1024\n" \
"v_mfma_f32_16x16x32_bf16 v[208:211], a[196:199], a[232:235], v[208:211]\n" \
"ds_read_b64_tr_b16 a[244:245], v37 offset:8192\n" \
"v_mfma_f32_16x16x32_bf16 v[212:215], a[172:175], a[132:135], v[212:215]\n" \
"v_mfma_f32_16x16x32_bf16 v[212:215], a[236:239], a[196:199], v[212:215]\n" \
"ds_read_b64_tr_b16 a[246:247], v37 offset:9216\n" \
"v_cvt_pk_bf16_f32 v188, v188, v189\n" \
"v_mfma_f32_16x16x32_bf16 v[204:207], a[164:167], a[132:135], v[204:207]\n" \
"v_cvt_pk_bf16_f32 v189, v190, v191\n" \
"v_cvt_pk_bf16_f32 v190, v198, v199\n" \
"v_mfma_f32_16x16x32_bf16 v[204:207], a[228:231], a[196:199], v[204:207]\n" \
"ds_read_b64_tr_b16 a[184:185], v38 offset:0\n" \
"v_mov_b32_e32 v191, 0x3f80\n" \
"v_mfma_f32_16x16x32_bf16 v[200:203], a[160:163], a[132:135], v[200:203]\n" \
"ds_read_b64_tr_b16 a[186:187], v38 offset:1024\n" \
"v_cvt_pk_bf16_f32 v184, v184, v185\n" \
"v_mfma_f32_16x16x32_bf16 v[200:203], a[224:227], a[196:199], v[200:203]\n" \
"v_cvt_pk_bf16_f32 v185, v186, v187\n" \
"v_cvt_pk_bf16_f32 v186, v196, v197\n" \
"v_mfma_f32_16x16x32_bf16 v[216:219], a[160:163], a[136:139], v[216:219]\n" \
"ds_read_b64_tr_b16 a[248:249], v38 offset:8192\n" \
"v_mov_b32_e64 v187, 0\n" \
"v_mfma_f32_16x16x32_bf16 v[216:219], a[224:227], a[200:203], v[216:219]\n" \
"ds_read_b64_tr_b16 a[250:251], v38 offset:9216\n" \
"v_mfma_f32_16x16x32_bf16 v[220:223], a[164:167], a[136:139], v[220:223]\n" \
"v_mfma_f32_16x16x32_bf16 v[220:223], a[228:231], a[200:203], v[220:223]\n" \
"ds_read_b64_tr_b16 a[188:189], v39 offset:0\n" \
"v_cvt_pk_bf16_f32 v204, v204, v205\n" \
"v_mfma_f32_16x16x32_bf16 v[228:231], a[172:175], a[136:139], v[228:231]\n" \
"v_cvt_pk_bf16_f32 v205, v206, v207\n" \
"v_cvt_pk_bf16_f32 v206, v214, v215\n" \
"v_mfma_f32_16x16x32_bf16 v[228:231], a[236:239], a[200:203], v[228:231]\n" \
"ds_read_b64_tr_b16 a[190:191], v39 offset:1024\n" \
"v_mov_b32_e32 v207, 0x3f80\n" \
"v_mfma_f32_16x16x32_bf16 v[224:227], a[136:139], a[168:171], v[224:227]\n" \
"ds_read_b64_tr_b16 a[252:253], v39 offset:8192\n" \
"v_cvt_pk_bf16_f32 v200, v200, v201\n" \
"v_mfma_f32_16x16x32_bf16 v[224:227], a[200:203], a[232:235], v[224:227]\n" \
"v_cvt_pk_bf16_f32 v201, v202, v203\n" \
"v_cvt_pk_bf16_f32 v202, v212, v213\n" \
"v_mfma_f32_16x16x32_bf16 v[240:243], a[140:143], a[168:171], v[240:243]\n" \
"ds_read_b64_tr_b16 a[254:255], v39 offset:9216\n" \
"v_mov_b32_e64 v203, 0\n" \
"v_mfma_f32_16x16x32_bf16 v[240:243], a[204:207], a[232:235], v[240:243]\n" \
"v_mfma_f32_16x16x32_bf16 v[244:247], a[172:175], a[140:143], v[244:247]\n" \
"v_mfma_f32_16x16x32_bf16 v[244:247], a[236:239], a[204:207], v[244:247]\n" \
"v_cvt_pk_bf16_f32 v220, v220, v221\n" \
"v_cvt_pk_bf16_f32 v221, v222, v223\n" \
"v_mfma_f32_16x16x32_bf16 v[236:239], a[164:167], a[140:143], v[236:239]\n" \
"v_cvt_pk_bf16_f32 v222, v230, v231\n" \
"v_mov_b32_e32 v223, 0x3f80\n" \
"v_mfma_f32_16x16x32_bf16 v[236:239], a[228:231], a[204:207], v[236:239]\n" \
"v_cvt_pk_bf16_f32 v216, v216, v217\n" \
"v_cvt_pk_bf16_f32 v217, v218, v219\n" \
"v_mfma_f32_16x16x32_bf16 v[232:235], a[160:163], a[140:143], v[232:235]\n" \
"v_cvt_pk_bf16_f32 v218, v228, v229\n" \
"v_mov_b32_e64 v219, 0\n" \
"v_mfma_f32_16x16x32_bf16 v[232:235], a[224:227], a[204:207], v[232:235]\n" \
"s_waitcnt lgkmcnt(15)\n" \
"s_nop 0\n" \
"v_mfma_f32_16x16x32_bf16 v[180:183], a[164:167], v[248:251], v[180:183]\n" \
"v_mfma_f32_16x16x32_bf16 v[180:183], a[228:231], v[48:51], v[180:183]\n" \
"v_mfma_f32_16x16x32_bf16 a[80:83], a[172:175], v[248:251], a[80:83]\n" \
"v_mfma_f32_16x16x32_bf16 a[80:83], a[236:239], v[48:51], a[80:83]\n" \
"v_cvt_pk_bf16_f32 v236, v236, v237\n" \
"v_cvt_pk_bf16_f32 v237, v238, v239\n" \
"v_mfma_f32_16x16x32_bf16 v[252:255], v[248:251], a[168:171], v[252:255]\n" \
"v_cvt_pk_bf16_f32 v238, v246, v247\n" \
"v_mov_b32_e32 v239, 0x3f80\n" \
"v_mfma_f32_16x16x32_bf16 v[252:255], v[48:51], a[232:235], v[252:255]\n" \
"v_cvt_pk_bf16_f32 v232, v232, v233\n" \
"v_cvt_pk_bf16_f32 v233, v234, v235\n" \
"s_waitcnt vmcnt(0) lgkmcnt(0)\n" \
"s_barrier\n" \
"v_mov_b32_e32 v45, s62\n" \
"s_mov_b64 exec, 1\n" \
"buffer_store_dword v45, off, s[80:83], s37 offset:32 sc1\n" \
"s_mov_b64 exec, -1\n" \
"v_cmp_ne_u32_e32 vcc, s62, v44\n" \
"s_nop 4\n" \
"s_cbranch_vccnz L_poll160_%=\n" \
"L_pollr160_%=:\n" \
"v_mfma_f32_16x16x32_bf16 v[52:55], a[176:179], a[128:131], v[52:55]\n" \
"s_mov_b32 s73, s50\n" \
"s_sub_u32 m0, s31, 0x10000\n" \
"v_mfma_f32_16x16x32_bf16 v[52:55], a[240:243], a[192:195], v[52:55]\n" \
"buffer_load_dwordx4 v20, s[80:83], s73 offen offset:0 sc1 lds\n" \
"v_mfma_f32_16x16x32_bf16 v[56:59], a[180:183], a[128:131], v[56:59]\n" \
"s_add_u32 s74, s50, 0x4000\n" \
"s_add_u32 s75, s50, 0x8000\n" \
"v_mfma_f32_16x16x32_bf16 v[56:59], a[244:247], a[192:195], v[56:59]\n" \
"s_mov_b32 s70, 0x3e0293ee\n" \
"s_mov_b32 s76, s52\n" \
"v_mfma_f32_16x16x32_bf16 v[60:63], a[184:187], a[128:131], v[60:63]\n" \
"v_mul_f32_e32 v52, s70, v52\n" \
"v_mul_f32_e32 v53, s70, v53\n" \
"v_mfma_f32_16x16x32_bf16 v[60:63], a[248:251], a[192:195], v[60:63]\n" \
"buffer_load_dwordx4 v20, s[80:83], s73 offen offset:1024 sc1 lds\n" \
"v_mfma_f32_16x16x32_bf16 v[64:67], a[188:191], a[128:131], v[64:67]\n" \
".fill (L_pb + 36576 - .) / 4, 4, 0xBF800000\n" \
"L_pp9:\n" \
"s_cbranch_execz L_pret\n" \
"v_mul_f32_e32 v54, s70, v54\n" \
"v_mul_f32_e64 v55, s70, v55\n" \
"v_mfma_f32_16x16x32_bf16 v[64:67], a[252:255], a[192:195], v[64:67]\n" \
"v_cvt_pk_bf16_f32 v52, v52, v53\n" \
"v_mul_f32_e64 v56, s70, v56\n" \
"v_mfma_f32_16x16x32_bf16 v[80:83], a[188:191], a[132:135], v[80:83]\n" \
"v_mul_f32_e32 v60, s70, v60\n" \
"v_mul_f32_e32 v61, s70, v61\n" \
"v_mfma_f32_16x16x32_bf16 v[80:83], a[252:255], a[196:199], v[80:83]\n" \
"buffer_load_dwordx4 v20, s[80:83], s73 offen offset:2048 sc1 lds\n" \
"v_mfma_f32_16x16x32_bf16 v[76:79], a[184:187], a[132:135], v[76:79]\n" \
"v_mul_f32_e32 v62, s70, v62\n" \
"v_mul_f32_e32 v63, s70, v63\n" \
"v_mfma_f32_16x16x32_bf16 v[76:79], a[248:251], a[196:199], v[76:79]\n" \
"v_cvt_pk_bf16_f32 v60, v60, v61\n" \
"v_mul_f32_e64 v57, s70, v57\n" \
"v_mfma_f32_16x16x32_bf16 v[72:75], a[180:183], a[132:135], v[72:75]\n" \
"v_mul_f32_e32 v58, s70, v58\n" \
"v_mul_f32_e32 v59, s70, v59\n" \
"v_mfma_f32_16x16x32_bf16 v[72:75], a[244:247], a[196:199], v[72:75]\n" \
"buffer_load_dwordx4 v20, s[80:83], s73 offen offset:3072 sc1 lds\n" \
"v_mfma_f32_16x16x32_bf16 v[68:71], a[176:179], a[132:135], v[68:71]\n" \
"s_sub_u32 m0, s31, s51\n" \
"v_cvt_pk_bf16_f32 v53, v54, v55\n" \
"v_mfma_f32_16x16x32_bf16 v[68:71], a[240:243], a[196:199], v[68:71]\n" \
"v_mul_f32_e32 v64, s70, v64\n" \
"v_mul_f32_e64 v65, s70, v65\n" \
"v_mfma_f32_16x16x32_bf16 v[84:87], a[176:179], a[136:139], v[84:87]\n" \
"v_mul_f32_e32 v66, s70, v66\n" \
"v_mul_f32_e32 v67, s70, v67\n" \
"v_mfma_f32_16x16x32_bf16 v[84:87], a[240:243], a[200:203], v[84:87]\n" \
"buffer_load_dwordx4 v20, s[80:83], s74 offen offset:0 sc1 lds\n" \
"v_mfma_f32_16x16x32_bf16 v[88:91], a[180:183], a[136:139], v[88:91]\n" \
"v_cvt_pk_bf16_f32 v61, v62, v63\n" \
"v_cvt_pk_bf16_f32 v54, v56, v57\n" \
"v_mfma_f32_16x16x32_bf16 v[88:91], a[244:247], a[200:203], v[88:91]\n" \
"v_cvt_pk_bf16_f32 v55, v58, v59\n" \
"v_cvt_pk_bf16_f32 v62, v64, v65\n" \
"v_mfma_f32_16x16x32_bf16 v[92:95], a[184:187], a[136:139], v[92:95]\n" \
"v_cvt_pk_bf16_f32 v63, v66, v67\n" \
"v_mul_f32_e64 v68, s70, v68\n" \
"v_mfma_f32_16x16x32_bf16 v[92:95], a[248:251], a[200:203], v[92:95]\n" \
"buffer_load_dwordx4 v20, s[80:83], s74 offen offset:1024 sc1 lds\n" \
"v_mfma_f32_16x16x32_bf16 v[96:99], a[188:191], a[136:139], v[96:99]\n" \
"ds_write_b128 v27, v[52:55] offset:0\n" \
"v_mfma_f32_16x16x32_bf16 v[96:99], a[252:255], a[200:203], v[96:99]\n" \
"v_mul_f32_e32 v69, s70, v69\n" \
"v_mul_f32_e32 v76, s70, v76\n" \
"v_mfma_f32_16x16x32_bf16 v[112:115], a[188:191], a[140:143], v[112:115]\n" \
"v_mul_f32_e32 v77, s70, v77\n" \
"v_mul_f32_e32 v70, s70, v70\n" \
"v_mfma_f32_16x16x32_bf16 v[112:115], a[252:255], a[204:207], v[112:115]\n" \
"buffer_load_dwordx4 v20, s[80:83], s74 offen offset:2048 sc1 lds\n" \
"v_mfma_f32_16x16x32_bf16 v[108:111], a[184:187], a[140:143], v[108:111]\n" \
"ds_write_b128 v27, v[60:63] offset:1024\n" \
"v_mfma_f32_16x16x32_bf16 v[108:111], a[248:251], a[204:207], v[108:111]\n" \
"v_mul_f32_e64 v71, s70, v71\n" \
"v_cvt_pk_bf16_f32 v68, v68, v69\n" \
"v_mfma_f32_16x16x32_bf16 v[104:107], a[180:183], a[140:143], v[104:107]\n" \
"v_mul_f32_e32 v78, s70, v78\n" \
"v_mul_f32_e32 v79, s70, v79\n" \
"v_mfma_f32_16x16x32_bf16 v[104:107], a[244:247], a[204:207], v[104:107]\n" \
"buffer_load_dwordx4 v20, s[80:83], s74 offen offset:3072 sc1 lds\n" \
"v_mfma_f32_16x16x32_bf16 v[100:103], a[176:179], a[140:143], v[100:103]\n" \
"s_sub_u32 m0, s31, 0x4000\n" \
"v_cvt_pk_bf16_f32 v76, v76, v77\n" \
"v_mfma_f32_16x16x32_bf16 v[100:103], a[240:243], a[204:207], v[100:103]\n" \
"v_mul_f32_e32 v72, s70, v72\n" \
"v_mul_f32_e32 v73, s70, v73\n" \
"v_mfma_f32_16x16x32_bf16 v[116:119], a[176:179], a[144:147], v[116:119]\n" \
"v_mul_f32_e32 v74, s70, v74\n" \
"v_mul_f32_e32 v75, s70, v75\n" \
"v_mfma_f32_16x16x32_bf16 v[116:119], a[240:243], a[208:211], v[116:119]\n" \
"buffer_load_dwordx4 v20, s[80:83], s75 offen offset:0 sc1 lds\n" \
"v_mfma_f32_16x16x32_bf16 v[120:123], a[180:183], a[144:147], v[120:123]\n" \
"v_cvt_pk_bf16_f32 v69, v70, v71\n" \
"v_mul_f32_e64 v80, s70, v80\n" \
"v_mfma_f32_16x16x32_bf16 v[120:123], a[244:247], a[208:211], v[120:123]\n" \
"v_mul_f32_e32 v81, s70, v81\n" \
"v_mul_f32_e32 v82, s70, v82\n" \
"v_mfma_f32_16x16x32_bf16 v[124:127], a[184:187], a[144:147], v[124:127]\n" \
"v_mul_f32_e64 v83, s70, v83\n" \
"v_cvt_pk_bf16_f32 v77, v78, v79\n" \
"v_mfma_f32_16x16x32_bf16 v[124:127], a[248:251], a[208:211], v[124:127]\n" \
"buffer_load_dwordx4 v20, s[80:83], s75 offen offset:1024 sc1 lds\n" \
"v_mfma_f32_16x16x32_bf16 v[128:131], a[188:191], a[144:147], v[128:131]\n" \
"v_cvt_pk_bf16_f32 v70, v72, v73\n" \
"v_cvt_pk_bf16_f32 v71, v74, v75\n" \
"v_mfma_f32_16x16x32_bf16 v[128:131], a[252:255], a[208:211], v[128:131]\n" \
"v_cvt_pk_bf16_f32 v78, v80, v81\n" \
"v_cvt_pk_bf16_f32 v79, v82, v83\n" \
"v_mfma_f32_16x16x32_bf16 v[144:147], a[188:191], a[148:151], v[144:147]\n" \
"ds_write_b128 v27, v[68:71] offset:4096\n" \
"v_mfma_f32_16x16x32_bf16 v[144:147], a[252:255], a[212:215], v[144:147]\n" \
"buffer_load_dwordx4 v20, s[80:83], s75 offen offset:2048 sc1 lds\n" \
"v_mfma_f32_16x16x32_bf16 v[140:143], a[184:187], a[148:151], v[140:143]\n" \
"v_cvt_pk_bf16_f32 v180, v180, v181\n" \
"v_accvgpr_read_b32 v50, a82\n" \
"v_mfma_f32_16x16x32_bf16 v[140:143], a[248:251], a[212:215], v[140:143]\n" \
"ds_write_b128 v27, v[76:79] offset:5120\n" \
"v_mfma_f32_16x16x32_bf16 v[136:139], a[180:183], a[148:151], v[136:139]\n" \
"v_accvgpr_read_b32 v51, a83\n" \
"v_cvt_pk_bf16_f32 v181, v182, v183\n" \
"v_mfma_f32_16x16x32_bf16 v[136:139], a[244:247], a[212:215], v[136:139]\n" \
"buffer_load_dwordx4 v20, s[80:83], s75 offen offset:3072 sc1 lds\n" \
"v_mfma_f32_16x16x32_bf16 v[132:135], a[176:179], a[148:151], v[132:135]\n" \
"s_mov_b32 m0, s53\n" \
"v_cvt_pk_bf16_f32 v182, v50, v51\n" \
"v_mfma_f32_16x16x32_bf16 v[132:135], a[240:243], a[212:215], v[132:135]\n" \
"v_mov_b32_e32 v183, 0x3f80\n" \
"v_cvt_pk_bf16_f32 v234, v244, v245\n" \
"v_mfma_f32_16x16x32_bf16 v[148:151], a[176:179], a[152:155], v[148:151]\n" \
"v_mov_b32_e32 v235, 0\n" \
"v_cvt_pk_bf16_f32 v54, v192, v193\n" \
"v_mfma_f32_16x16x32_bf16 v[148:151], a[240:243], a[216:219], v[148:151]\n" \
"buffer_load_dwordx4 v20, s[80:83], s76 offen offset:0 sc1 lds\n" \
"v_mfma_f32_16x16x32_bf16 v[152:155], a[180:183], a[152:155], v[152:155]\n" \
"v_cvt_pk_bf16_f32 v55, v194, v195\n" \
"v_cvt_pk_bf16_f32 v56, v208, v209\n" \
"v_mfma_f32_16x16x32_bf16 v[152:155], a[244:247], a[216:219], v[152:155]\n" \
"v_cvt_pk_bf16_f32 v57, v210, v211\n" \
"v_cvt_pk_bf16_f32 v58, v224, v225\n" \
"v_cvt_pk_bf16_f32 v59, v226, v227\n" \
"v_cvt_pk_bf16_f32 v60, v240, v241\n" \
"v_cvt_pk_bf16_f32 v61, v242, v243\n" \
"v_mfma_f32_16x16x32_bf16 v[32:35], v[180:183], v[184:187], 0\n" \
"buffer_load_dwordx4 v20, s[80:83], s76 offen offset:1024 sc1 lds\n" \
"v_mfma_f32_16x16x32_bf16 v[156:159], a[184:187], a[152:155], v[156:159]\n" \
"v_mfma_f32_16x16x32_bf16 v[192:195], v[188:191], v[184:187], 0\n" \
"v_mfma_f32_16x16x32_bf16 v[208:211], v[204:207], v[200:203], 0\n" \
"v_mfma_f32_16x16x32_bf16 v[36:39], v[188:191], v[200:203], 0\n" \
"v_mfma_f32_16x16x32_bf16 v[224:227], v[220:223], v[216:219], 0\n" \
"v_mfma_f32_16x16x32_bf16 v[248:251], v[204:207], v[216:219], 0\n" \
"v_mfma_f32_16x16x32_bf16 v[240:243], v[236:239], v[232:235], 0\n" \
"v_mfma_f32_16x16x32_bf16 v[64:67], v[220:223], v[232:235], 0\n" \
"s_mov_b32 s64, 0xaaaaaaaa\n" \
"s_mov_b32 s65, 0xaaaaaaaa\n" \
"v_mfma_f32_16x16x32_bf16 v[156:159], a[248:251], a[216:219], v[156:159]\n" \
"v_cndmask_b32_e64 v32, v32, v33, s[64:65]\n" \
"v_cndmask_b32_e64 v34, v34, v35, s[64:65]\n" \
"v_mfma_f32_16x16x32_bf16 v[160:163], a[188:191], a[152:155], v[160:163]\n" \
"s_mov_b32 s66, 0xfff0ffff\n" \
"s_mov_b32 s67, 0xf000ff00\n" \
"s_mov_b32 s68, 0xffc0fffc\n" \
"s_mov_b32 s69, 0xc000fc00\n" \
"v_cndmask_b32_e64 v192, v192, v193, s[64:65]\n" \
"v_add_f32_e64 v32, s47, v32\n" \
"v_cndmask_b32_e64 v194, v194, v195, s[64:65]\n" \
"v_add_f32_e64 v34, s47, v34\n" \
"v_cndmask_b32_e64 v208, v208, v209, s[64:65]\n" \
"v_cndmask_b32_e64 v36, v36, v37, s[64:65]\n" \
"v_cndmask_b32_e64 v210, v210, v211, s[64:65]\n" \
"v_cndmask_b32_e64 v38, v38, v39, s[64:65]\n" \
"v_cndmask_b32_e64 v224, v224, v225, s[64:65]\n" \
"v_cndmask_b32_e64 v248, v248, v249, s[64:65]\n" \
"v_cndmask_b32_e64 v226, v226, v227, s[64:65]\n" \
"v_cndmask_b32_e64 v250, v250, v251, s[64:65]\n" \
"v_cndmask_b32_e64 v240, v240, v241, s[64:65]\n" \
"v_cndmask_b32_e64 v64, v64, v65, s[64:65]\n" \
"v_cndmask_b32_e64 v242, v242, v243, s[64:65]\n" \
"v_cndmask_b32_e64 v66, v66, v67, s[64:65]\n" \
"v_cndmask_b32_e64 v192, v32, v192, s[66:67]\n" \
"v_cndmask_b32_e64 v194, v34, v194, s[68:69]\n" \
"v_cndmask_b32_e64 v208, v36, v208, s[66:67]\n" \
"v_cndmask_b32_e64 v210, v38, v210, s[68:69]\n" \
"v_cndmask_b32_e64 v224, v248, v224, s[66:67]\n" \
"v_cndmask_b32_e64 v226, v250, v226, s[68:69]\n" \
"v_cndmask_b32_e64 v240, v64, v240, s[66:67]\n" \
"v_cndmask_b32_e64 v242, v66, v242, s[68:69]\n" \
"v_mfma_f32_16x16x32_bf16 v[160:163], a[252:255], a[216:219], v[160:163]\n" \
"v_max_f32_e32 v193, v192, v194\n" \
"v_max_f32_e32 v209, v208, v210\n" \
"v_max_f32_e32 v225, v224, v226\n" \
"v_max_f32_e32 v241, v240, v242\n" \
"v_mul_f32_e32 v84, s70, v84\n" \
"v_permlane32_swap_b32_e32 v193, v225\n" \
"v_permlane32_swap_b32_e32 v209, v241\n" \
"v_mul_f32_e32 v85, s70, v85\n" \
"v_mul_f32_e32 v92, s70, v92\n" \
"v_mul_f32_e32 v93, s70, v93\n" \
"v_permlane32_swap_b32_e32 v196, v228\n" \
"v_permlane32_swap_b32_e32 v212, v244\n" \
"v_max_f32_e32 v193, v193, v225\n" \
"v_max_f32_e32 v209, v209, v241\n" \
"v_accvgpr_read_b32 v48, a80\n" \
"v_accvgpr_read_b32 v49, a81\n" \
"v_mul_f32_e32 v86, s70, v86\n" \
"v_mul_f32_e32 v87, s70, v87\n" \
"v_cvt_pk_bf16_f32 v84, v84, v85\n" \
"v_mul_f32_e32 v94, s70, v94\n" \
"v_mul_f32_e32 v95, s70, v95\n" \
"v_cvt_pk_bf16_f32 v92, v92, v93\n" \
"v_permlane16_swap_b32_e32 v228, v244\n" \
"v_permlane16_swap_b32_e32 v193, v209\n" \
"v_mfma_f32_16x16x32_bf16 v[176:179], a[188:191], a[156:159], v[176:179]\n" \
"s_mov_b32 s72, 0x3eae1a93\n" \
"v_mul_f32_e32 v88, s70, v88\n" \
"v_mul_f32_e32 v89, s70, v89\n" \
"v_mul_f32_e32 v90, s70, v90\n" \
"v_mul_f32_e32 v91, s70, v91\n" \
"v_cvt_pk_bf16_f32 v85, v86, v87\n" \
"v_mul_f32_e32 v96, s70, v96\n" \
"v_mul_f32_e32 v97, s70, v97\n" \
"v_mul_f32_e32 v98, s70, v98\n" \
"v_mul_f32_e32 v99, s70, v99\n" \
"v_cvt_pk_bf16_f32 v93, v94, v95\n" \
"v_mul_f32_e32 v45, 0xbfb8aa3b, v228\n" \
"v_mfma_f32_16x16x16_bf16 v[48:51], v[182:183], v[46:47], 0\n" \
"v_mfma_f32_16x16x16_bf16 v[196:199], v[190:191], v[46:47], 0\n" \
"v_mfma_f32_16x16x16_bf16 v[212:215], v[206:207], v[46:47], 0\n" \
"v_max_f32_e64 v193, v193, v209\n" \
"v_cvt_pk_bf16_f32 v86, v88, v89\n" \
"v_cvt_pk_bf16_f32 v87, v90, v91\n" \
"v_cvt_pk_bf16_f32 v94, v96, v97\n" \
"v_cvt_pk_bf16_f32 v95, v98, v99\n" \
"v_mfma_f32_16x16x16_bf16 v[228:231], v[222:223], v[46:47], 0\n" \
"v_mul_f32_e32 v193, s72, v193\n" \
"v_mov_b32_e32 v209, v193\n" \
"ds_write_b128 v27, v[84:87] offset:8192\n" \
"v_mfma_f32_16x16x16_bf16 v[244:247], v[238:239], v[46:47], 0\n" \
"v_permlane16_swap_b32_e32 v193, v209\n" \
"s_mov_b32 s71, 0x3e0293ee\n" \
"v_mfma_f32_16x16x32_bf16 v[176:179], a[252:255], a[220:223], v[176:179]\n" \
"v_mov_b32_e32 v225, v193\n" \
"v_mov_b32_e32 v241, v209\n" \
"v_mul_f32_e32 v100, s70, v100\n" \
"v_mul_f32_e64 v101, s70, v101\n" \
"ds_write_b128 v27, v[92:95] offset:9216\n" \
"v_mul_f32_e32 v108, s70, v108\n" \
"v_mul_f32_e32 v109, s70, v109\n" \
"v_pk_mul_f32 v[116:117], v[116:117], s[70:71]\n" \
"v_pk_mul_f32 v[124:125], v[124:125], s[70:71]\n" \
"v_pk_mul_f32 v[132:133], v[132:133], s[70:71]\n" \
"v_pk_mul_f32 v[140:141], v[140:141], s[70:71]\n" \
"v_pk_mul_f32 v[148:149], v[148:149], s[70:71]\n" \
"v_pk_mul_f32 v[156:157], v[156:157], s[70:71]\n" \
"v_permlane32_swap_b32_e32 v193, v225\n" \
"v_permlane32_swap_b32_e32 v209, v241\n" \
"v_mul_f32_e32 v102, s70, v102\n" \
"v_mul_f32_e32 v103, s70, v103\n" \
"v_cvt_pk_bf16_f32 v100, v100, v101\n" \
"v_mul_f32_e32 v110, s70, v110\n" \
"v_mul_f32_e32 v111, s70, v111\n" \
"v_cvt_pk_bf16_f32 v108, v108, v109\n" \
"v_pk_mul_f32 v[118:119], v[118:119], s[70:71]\n" \
"v_cvt_pk_bf16_f32 v116, v116, v117\n" \
"v_pk_mul_f32 v[126:127], v[126:127], s[70:71]\n" \
"v_cvt_pk_bf16_f32 v124, v124, v125\n" \
"v_pk_mul_f32 v[134:135], v[134:135], s[70:71]\n" \
"v_cvt_pk_bf16_f32 v132, v132, v133\n" \
"v_pk_mul_f32 v[142:143], v[142:143], s[70:71]\n" \
"v_cvt_pk_bf16_f32 v140, v140, v141\n" \
"v_pk_mul_f32 v[150:151], v[150:151], s[70:71]\n" \
"v_cvt_pk_bf16_f32 v148, v148, v149\n" \
"v_pk_mul_f32 v[158:159], v[158:159], s[70:71]\n" \
"v_cvt_pk_bf16_f32 v156, v156, v157\n" \
"v_fma_f32 v192, v192, s72, -v193\n" \
"v_fma_f32 v194, v194, s72, -v193\n" \
"v_fma_f32 v208, v208, s72, -v209\n" \
"v_fma_f32 v210, v210, s72, -v209\n" \
"v_mul_f32_e32 v104, s70, v104\n" \
"v_mul_f32_e32 v105, s70, v105\n" \
"v_mul_f32_e32 v106, s70, v106\n" \
"v_mul_f32_e32 v107, s70, v107\n" \
"v_cvt_pk_bf16_f32 v101, v102, v103\n" \
"v_mul_f32_e32 v112, s70, v112\n" \
"v_mul_f32_e32 v113, s70, v113\n" \
"v_mul_f32_e32 v114, s70, v114\n" \
"v_mul_f32_e32 v115, s70, v115\n" \
"v_cvt_pk_bf16_f32 v109, v110, v111\n" \
"v_pk_mul_f32 v[120:121], v[120:121], s[70:71]\n" \
"v_pk_mul_f32 v[122:123], v[122:123], s[70:71]\n" \
"v_cvt_pk_bf16_f32 v117, v118, v119\n" \
"v_pk_mul_f32 v[128:129], v[128:129], s[70:71]\n" \
"v_pk_mul_f32 v[130:131], v[130:131], s[70:71]\n" \
"v_cvt_pk_bf16_f32 v125, v126, v127\n" \
"v_pk_mul_f32 v[136:137], v[136:137], s[70:71]\n" \
"v_pk_mul_f32 v[138:139], v[138:139], s[70:71]\n" \
"v_cvt_pk_bf16_f32 v133, v134, v135\n" \
"v_pk_mul_f32 v[144:145], v[144:145], s[70:71]\n" \
"v_pk_mul_f32 v[146:147], v[146:147], s[70:71]\n" \
"v_cvt_pk_bf16_f32 v141, v142, v143\n" \
"v_pk_mul_f32 v[152:153], v[152:153], s[70:71]\n" \
"v_pk_mul_f32 v[154:155], v[154:155], s[70:71]\n" \
"v_cvt_pk_bf16_f32 v149, v150, v151\n" \
"v_pk_mul_f32 v[160:161], v[160:161], s[70:71]\n" \
"v_pk_mul_f32 v[162:163], v[162:163], s[70:71]\n" \
"v_cvt_pk_bf16_f32 v157, v158, v159\n" \
"v_pk_mul_f32 v[176:177], v[176:177], s[70:71]\n" \
"v_pk_mul_f32 v[178:179], v[178:179], s[70:71]\n" \
"v_exp_f32_e32 v192, v192\n" \
"v_exp_f32_e32 v194, v194\n" \
"v_exp_f32_e32 v208, v208\n" \
"v_exp_f32_e32 v210, v210\n" \
"v_fma_f32 v224, v224, s72, -v225\n" \
"v_fma_f32 v226, v226, s72, -v225\n" \
"v_mfma_f32_16x16x32_bf16 v[172:175], a[184:187], a[156:159], v[172:175]\n" \
"v_cvt_pk_bf16_f32 v102, v104, v105\n" \
"v_cvt_pk_bf16_f32 v103, v106, v107\n" \
"v_cvt_pk_bf16_f32 v110, v112, v113\n" \
"v_cvt_pk_bf16_f32 v111, v114, v115\n" \
"v_cvt_pk_bf16_f32 v118, v120, v121\n" \
"v_cvt_pk_bf16_f32 v119, v122, v123\n" \
"v_cvt_pk_bf16_f32 v126, v128, v129\n" \
"v_cvt_pk_bf16_f32 v127, v130, v131\n" \
"v_cvt_pk_bf16_f32 v134, v136, v137\n" \
"v_cvt_pk_bf16_f32 v135, v138, v139\n" \
"v_cvt_pk_bf16_f32 v142, v144, v145\n" \
"v_cvt_pk_bf16_f32 v143, v146, v147\n" \
"v_cvt_pk_bf16_f32 v150, v152, v153\n" \
"v_cvt_pk_bf16_f32 v151, v154, v155\n" \
"v_cvt_pk_bf16_f32 v158, v160, v161\n" \
"v_cvt_pk_bf16_f32 v159, v162, v163\n" \
"v_exp_f32_e32 v224, v224\n" \
"v_exp_f32_e32 v226, v226\n" \
"v_fma_f32 v240, v240, s72, -v241\n" \
"v_fma_f32 v242, v242, s72, -v241\n" \
"ds_write_b128 v27, v[100:103] offset:12288\n" \
"v_cvt_pk_bf16_f32 v192, v192, v194\n" \
"v_cvt_pk_bf16_f32 v208, v208, v210\n" \
"v_exp_f32_e32 v240, v240\n" \
"v_exp_f32_e32 v242, v242\n" \
"v_cvt_pk_bf16_f32 v68, v48, v49\n" \
"v_cvt_pk_bf16_f32 v69, v50, v51\n" \
"v_cvt_pk_bf16_f32 v70, v196, v197\n" \
"v_cvt_pk_bf16_f32 v71, v198, v199\n" \
"ds_write_b128 v27, v[108:111] offset:13312\n" \
"v_cvt_pk_bf16_f32 v72, v212, v213\n" \
"v_cvt_pk_bf16_f32 v73, v214, v215\n" \
"v_perm_b32 v184, v192, v192, v40\n" \
"v_perm_b32 v185, v192, v192, v41\n" \
"v_perm_b32 v186, v192, v192, v42\n" \
"v_perm_b32 v187, v192, v192, v43\n" \
"v_perm_b32 v200, v208, v208, v40\n" \
"v_perm_b32 v201, v208, v208, v41\n" \
"v_perm_b32 v202, v208, v208, v42\n" \
"v_perm_b32 v203, v208, v208, v43\n" \
"ds_write_b128 v21, v[116:119] offset:16384\n" \
"v_cvt_pk_bf16_f32 v224, v224, v226\n" \
"v_cvt_pk_bf16_f32 v74, v228, v229\n" \
"v_cvt_pk_bf16_f32 v75, v230, v231\n" \
"v_mfma_f32_16x16x32_bf16 v[32:35], v[68:71], v[184:187], 0\n" \
"v_mfma_f32_16x16x32_bf16 v[36:39], v[70:73], v[200:203], 0\n" \
"v_perm_b32 v216, v224, v224, v40\n" \
"v_perm_b32 v217, v224, v224, v41\n" \
"ds_write_b128 v21, v[124:127] offset:17408\n" \
"v_perm_b32 v218, v224, v224, v42\n" \
"v_perm_b32 v219, v224, v224, v43\n" \
"v_cvt_pk_bf16_f32 v240, v240, v242\n" \
"v_mfma_f32_16x16x32_bf16 v[172:175], a[248:251], a[220:223], v[172:175]\n" \
"v_cvt_pk_bf16_f32 v76, v244, v245\n" \
"v_cvt_pk_bf16_f32 v77, v246, v247\n" \
"v_mfma_f32_16x16x32_bf16 v[248:251], v[72:75], v[216:219], 0\n" \
"v_perm_b32 v232, v240, v240, v40\n" \
"v_perm_b32 v233, v240, v240, v41\n" \
"ds_write_b128 v21, v[132:135] offset:20480\n" \
"v_perm_b32 v234, v240, v240, v42\n" \
"v_perm_b32 v235, v240, v240, v43\n" \
"v_pk_mul_f32 v[172:173], v[172:173], s[70:71]\n" \
"v_pk_mul_f32 v[174:175], v[174:175], s[70:71]\n" \
"v_cvt_pk_bf16_f32 v172, v172, v173\n" \
"v_cvt_pk_bf16_f32 v173, v174, v175\n" \
"v_cvt_pk_bf16_f32 v174, v176, v177\n" \
"v_cvt_pk_bf16_f32 v175, v178, v179\n" \
"v_mfma_f32_16x16x32_bf16 v[64:67], v[74:77], v[232:235], 0\n" \
"ds_write_b128 v21, v[140:143] offset:21504\n" \
"v_mfma_f32_16x16x32_bf16 v[168:171], a[180:183], a[156:159], v[168:171]\n" \
"v_cvt_pk_bf16_f32 v52, v252, v253\n" \
"v_cvt_pk_bf16_f32 v53, v254, v255\n" \
"v_mfma_f32_16x16x32_bf16 v[168:171], a[244:247], a[220:223], v[168:171]\n" \
"v_exp_f32_e64 v45, v45\n" \
"v_mfma_f32_16x16x32_bf16 v[192:195], v[52:55], v[184:187], 0\n" \
"ds_write_b128 v21, v[148:151] offset:24576\n" \
"v_mfma_f32_16x16x32_bf16 v[208:211], v[54:57], v[200:203], 0\n" \
"v_mfma_f32_16x16x32_bf16 v[224:227], v[56:59], v[216:219], 0\n" \
"v_mfma_f32_16x16x32_bf16 v[240:243], v[58:61], v[232:235], 0\n" \
"ds_write_b128 v21, v[156:159] offset:25600\n" \
"v_mov_b32_dpp v34, v38 quad_perm:[0,1,2,3] row_mask:0x2 bank_mask:0xf\n" \
"v_pk_mul_f32 v[168:169], v[168:169], s[70:71]\n" \
"v_pk_mul_f32 v[170:171], v[170:171], s[70:71]\n" \
"v_mov_b32_dpp v34, v250 quad_perm:[0,1,2,3] row_mask:0x4 bank_mask:0xf\n" \
"v_add_f32_e64 v45, 1.0, v45\n" \
"v_mfma_f32_16x16x32_bf16 v[164:167], a[176:179], a[156:159], v[164:167]\n" \
"v_mov_b32_dpp v34, v66 quad_perm:[0,1,2,3] row_mask:0x8 bank_mask:0xf\n" \
"v_mov_b32_dpp v32, v36 quad_perm:[0,1,2,3] row_mask:0x2 bank_mask:0xf\n" \
"v_rcp_f32_e64 v45, v45\n" \
"ds_write_b128 v21, v[172:175] offset:29696\n" \
"v_mov_b32_dpp v33, v37 quad_perm:[0,1,2,3] row_mask:0x2 bank_mask:0xf\n" \
"v_rcp_f32_e64 v34, v34\n" \
"v_mov_b32_dpp v32, v248 quad_perm:[0,1,2,3] row_mask:0x4 bank_mask:0xf\n" \
"v_mov_b32_dpp v33, v249 quad_perm:[0,1,2,3] row_mask:0x4 bank_mask:0xf\n" \
"v_mul_f32_e64 v34, v45, v34\n" \
"v_mov_b32_dpp v32, v64 quad_perm:[0,1,2,3] row_mask:0x8 bank_mask:0xf\n" \
"v_mov_b32_dpp v33, v65 quad_perm:[0,1,2,3] row_mask:0x8 bank_mask:0xf\n" \
"v_mul_f32_e32 v32, v34, v32\n" \
"v_mul_f32_e32 v33, v34, v33\n" \
"v_mov_b32_e64 v38, v34\n" \
"v_mfma_f32_16x16x32_bf16 v[164:167], a[240:243], a[220:223], v[164:167]\n" \
"v_sub_f32_e32 v44, 1.0, v45\n" \
"v_permlane16_swap_b32_e32 v34, v38\n" \
"v_mov_b32_e32 v250, v34\n" \
"v_mov_b32_e32 v66, v38\n" \
"v_cvt_pk_bf16_f32 v32, v32, v33\n" \
"v_permlane32_swap_b32_e32 v34, v250\n" \
"v_permlane32_swap_b32_e32 v38, v66\n" \
"v_mul_f32_e64 v192, v34, v192\n" \
"v_pk_mul_f32 v[164:165], v[164:165], s[70:71]\n" \
"v_pk_mul_f32 v[166:167], v[166:167], s[70:71]\n" \
"v_cvt_pk_bf16_f32 v164, v164, v165\n" \
"v_cvt_pk_bf16_f32 v165, v166, v167\n" \
"v_cvt_pk_bf16_f32 v166, v168, v169\n" \
"v_cvt_pk_bf16_f32 v167, v170, v171\n" \
"ds_write_b128 v21, v[164:167] offset:28672\n" \
"v_mul_f32_e32 v193, v34, v193\n" \
"v_mul_f32_e32 v208, v38, v208\n" \
"v_mul_f32_e32 v209, v38, v209\n" \
"v_mul_f32_e32 v224, v250, v224\n" \
"v_mul_f32_e32 v225, v250, v225\n" \
"v_mul_f32_e32 v240, v66, v240\n" \
"v_mul_f32_e32 v241, v66, v241\n" \
"v_mul_f32_e32 v194, v34, v194\n" \
"v_mul_f32_e64 v195, v34, v195\n" \
"v_cvt_pk_bf16_f32 v192, v192, v193\n" \
"v_mul_f32_e32 v210, v38, v210\n" \
"v_mul_f32_e32 v211, v38, v211\n" \
"v_cvt_pk_bf16_f32 v208, v208, v209\n" \
"v_mul_f32_e32 v226, v250, v226\n" \
"v_mul_f32_e32 v227, v250, v227\n" \
"v_cvt_pk_bf16_f32 v224, v224, v225\n" \
"v_mul_f32_e32 v242, v66, v242\n" \
"v_mul_f32_e32 v243, v66, v243\n" \
"v_cvt_pk_bf16_f32 v240, v240, v241\n" \
"v_cvt_pk_bf16_f32 v193, v194, v195\n" \
"s_add_u32 s84, s48, 0x0\n" \
"s_nop 0\n" \
"v_cvt_pk_bf16_f32 v209, v210, v211\n" \
"s_add_u32 s85, s48, 0x24800\n" \
"v_cvt_pk_bf16_f32 v225, v226, v227\n" \
"s_add_u32 s86, s48, 0x49000\n" \
"v_cvt_pk_bf16_f32 v241, v242, v243\n" \
"s_add_u32 s87, s48, 0x6d800\n" \
"ds_write_b32 v26, v44\n" \
"buffer_store_dword v32, v25, s[80:83], s49 offen sc1\n" \
"buffer_store_dwordx2 v[192:193], v24, s[80:83], s84 offen sc1\n" \
"buffer_store_dwordx2 v[208:209], v24, s[80:83], s85 offen sc1\n" \
"buffer_store_dwordx2 v[224:225], v24, s[80:83], s86 offen sc1\n" \
"buffer_store_dwordx2 v[240:241], v24, s[80:83], s87 offen sc1\n" \
"s_branch L_end_%=\n" \
"L_poll60_%=:\n" \
"s_mov_b32 s84, 0\n" \
"L_pl60_%=:\n" \
"s_sleep 1\n" \
"v_bfe_u32 v44, v20, 2, 5\n" \
"buffer_load_dword v44, v44, s[80:83], s38 offen offset:128 sc1\n" \
"s_waitcnt vmcnt(0)\n" \
"v_cmp_ne_u32_e32 vcc, s62, v44\n" \
"s_add_u32 s84, s84, 1\n" \
"s_nop 3\n" \
"s_cbranch_vccz L_pollr60_%=\n" \
"s_cmp_gt_u32 s84, 0x400000\n" \
"s_cbranch_scc0 L_pl60_%=\n" \
"s_branch L_pollr60_%=\n" \
"L_poll160_%=:\n" \
"s_mov_b32 s84, 0\n" \
"L_pl160_%=:\n" \
"s_sleep 1\n" \
"v_bfe_u32 v44, v20, 2, 5\n" \
"buffer_load_dword v44, v44, s[80:83], s38 offen offset:0 sc1\n" \
"s_waitcnt vmcnt(0)\n" \
"v_cmp_ne_u32_e32 vcc, s62, v44\n" \
"s_add_u32 s84, s84, 1\n" \
"s_nop 3\n" \
"s_cbranch_vccz L_pollr160_%=\n" \
"s_cmp_gt_u32 s84, 0x400000\n" \
"s_cbranch_scc0 L_pl160_%=\n" \
"s_branch L_pollr160_%=\n" \
".fill (L_pb + 42552 - .) / 4, 4, 0xBF800000\n" \
"L_w1_%=:\n" \
"s_waitcnt vmcnt(19)\n" \
"s_barrier\n" \
"ds_read_b64_tr_b16 a[160:161], v32 offset:0\n" \
"ds_read_b64_tr_b16 a[162:163], v32 offset:1024\n" \
"ds_read_b128 a[128:131], v28 offset:16384\n" \
"ds_read_b64_tr_b16 a[224:225], v32 offset:8192\n" \
"ds_read_b64_tr_b16 a[226:227], v32 offset:9216\n" \
"ds_read_b128 a[192:195], v29 offset:16384\n" \
"ds_read_b128 a[132:135], v28 offset:18432\n" \
"ds_read_b128 a[196:199], v29 offset:18432\n" \
"ds_read_b64_tr_b16 a[164:165], v33 offset:0\n" \
"ds_read_b64_tr_b16 a[166:167], v33 offset:1024\n" \
"ds_read_b64_tr_b16 a[228:229], v33 offset:8192\n" \
"ds_read_b64_tr_b16 a[230:231], v33 offset:9216\n" \
"ds_read_b128 a[136:139], v28 offset:20480\n" \
"ds_read_b128 a[200:203], v29 offset:20480\n" \
"s_waitcnt lgkmcnt(11)\n" \
"v_mfma_f32_16x16x32_bf16 a[0:3], a[128:131], a[160:163], 0\n" \
"ds_read_b64_tr_b16 a[168:169], v34 offset:0\n" \
"s_lshr_b32 s4, s2, 1\n" \
"s_waitcnt lgkmcnt(9)\n" \
"v_mfma_f32_16x16x32_bf16 a[0:3], a[192:195], a[224:227], a[0:3]\n" \
"ds_read_b64_tr_b16 a[170:171], v34 offset:1024\n" \
"s_and_b32 s4, s4, 0x7c\n" \
"s_waitcnt lgkmcnt(9)\n" \
"v_mfma_f32_16x16x32_bf16 a[16:19], a[132:135], a[160:163], 0\n" \
"ds_read_b64_tr_b16 a[232:233], v34 offset:8192\n" \
"v_mov_b32_e32 v213, s13\n" \
"s_waitcnt lgkmcnt(9)\n" \
"v_mfma_f32_16x16x32_bf16 a[16:19], a[196:199], a[224:227], a[16:19]\n" \
"ds_read_b64_tr_b16 a[234:235], v34 offset:9216\n" \
"s_or_b32 s4, s4, s3\n" \
"s_waitcnt lgkmcnt(8)\n" \
"v_mfma_f32_16x16x32_bf16 a[20:23], a[132:135], a[164:167], 0\n" \
"ds_read_b128 a[140:143], v28 offset:22528\n" \
"v_lshrrev_b32_e32 v212, 4, v20\n" \
"s_waitcnt lgkmcnt(7)\n" \
"v_mfma_f32_16x16x32_bf16 a[20:23], a[196:199], a[228:231], a[20:23]\n" \
"ds_read_b128 a[204:207], v29 offset:22528\n" \
"v_mad_u64_u32 v[214:215], s[4:5], s4, 36, v[212:213]\n" \
"v_mfma_f32_16x16x32_bf16 a[4:7], a[128:131], a[164:167], 0\n" \
"ds_read_b64_tr_b16 a[172:173], v35 offset:0\n" \
"v_mov_b32_e64 v217, s9\n" \
"v_mfma_f32_16x16x32_bf16 a[4:7], a[192:195], a[228:231], a[4:7]\n" \
"ds_read_b64_tr_b16 a[174:175], v35 offset:1024\n" \
"s_movk_i32 s4, 0x240\n" \
"s_waitcnt lgkmcnt(9)\n" \
"v_mfma_f32_16x16x32_bf16 a[32:35], a[136:139], a[160:163], 0\n" \
"ds_read_b64_tr_b16 a[236:237], v35 offset:8192\n" \
"v_cmp_gt_u32_e32 vcc, s4, v20\n" \
"s_waitcnt lgkmcnt(9)\n" \
"v_mfma_f32_16x16x32_bf16 a[32:35], a[200:203], a[224:227], a[32:35]\n" \
"ds_read_b64_tr_b16 a[238:239], v35 offset:9216\n" \
"s_lshl_b32 s4, s2, 3\n" \
"v_mfma_f32_16x16x32_bf16 a[36:39], a[136:139], a[164:167], 0\n" \
"ds_read_b128 a[144:147], v28 offset:0\n" \
"s_and_b32 s4, s4, 56\n" \
"v_mfma_f32_16x16x32_bf16 a[36:39], a[200:203], a[228:231], a[36:39]\n" \
"ds_read_b128 a[208:211], v29 offset:0\n" \
"v_cndmask_b32_e32 v217, v217, v213, vcc\n" \
"s_waitcnt lgkmcnt(10)\n" \
"v_mfma_f32_16x16x32_bf16 a[40:43], a[136:139], a[168:171], 0\n" \
"ds_read_b128 a[148:151], v28 offset:2048\n" \
"v_lshlrev_b32_e32 v213, 3, v214\n" \
"s_waitcnt lgkmcnt(9)\n" \
"v_mfma_f32_16x16x32_bf16 a[40:43], a[200:203], a[232:235], a[40:43]\n" \
"ds_read_b128 a[212:215], v29 offset:2048\n" \
"v_and_b32_e32 v213, 0x1ffc0, v213\n" \
"v_mfma_f32_16x16x32_bf16 a[24:27], a[132:135], a[168:171], 0\n" \
"ds_read_b128 a[152:155], v28 offset:4096\n" \
"v_and_b32_e64 v214, 7, v214\n" \
"v_mfma_f32_16x16x32_bf16 a[24:27], a[196:199], a[232:235], a[24:27]\n" \
"ds_read_b128 a[216:219], v29 offset:4096\n" \
"v_or3_b32 v213, v214, s4, v213\n" \
"v_mfma_f32_16x16x32_bf16 a[8:11], a[128:131], a[168:171], 0\n" \
"ds_read_b128 a[156:159], v28 offset:6144\n" \
"s_lshl_b32 s4, s2, 2\n" \
"v_mfma_f32_16x16x32_bf16 a[8:11], a[192:195], a[232:235], a[8:11]\n" \
"ds_read_b128 a[220:223], v29 offset:6144\n" \
"s_mov_b32 m0, s32\n" \
"s_waitcnt lgkmcnt(13)\n" \
"v_mfma_f32_16x16x32_bf16 a[48:51], a[140:143], a[160:163], 0\n" \
"s_mov_b32 exec_lo, 0xff000000\n" \
"s_mov_b32 exec_hi, 0x0\n" \
"global_load_lds_dword v[184:185], off\n" \
"s_mov_b64 exec, -1\n" \
"s_waitcnt lgkmcnt(12)\n" \
"v_mfma_f32_16x16x32_bf16 a[48:51], a[204:207], a[224:227], a[48:51]\n" \
"ds_read_b64_tr_b16 a[176:177], v36 offset:0\n" \
"s_or_b32 s4, s4, s3\n" \
"v_mfma_f32_16x16x32_bf16 a[52:55], a[140:143], a[164:167], 0\n" \
"ds_read_b64_tr_b16 a[178:179], v36 offset:1024\n" \
"s_mul_i32 s4, s4, 18\n" \
"v_mfma_f32_16x16x32_bf16 a[52:55], a[204:207], a[228:231], a[52:55]\n" \
"s_mov_b32 m0, s32\n" \
"ds_read_b64_tr_b16 a[240:241], v36 offset:8192\n" \
"v_mfma_f32_16x16x32_bf16 a[56:59], a[140:143], a[168:171], 0\n" \
"s_mov_b32 exec_lo, 0xffff\n" \
"s_mov_b32 exec_hi, 0x0\n" \
"global_load_lds_dword v[186:187], off\n" \
"s_mov_b64 exec, -1\n" \
"v_mfma_f32_16x16x32_bf16 a[56:59], a[204:207], a[232:235], a[56:59]\n" \
"ds_read_b64_tr_b16 a[242:243], v36 offset:9216\n" \
"v_min_u32_e32 v212, 53, v212\n" \
"s_waitcnt lgkmcnt(14)\n" \
"v_mfma_f32_16x16x32_bf16 a[60:63], a[140:143], a[172:175], 0\n" \
"ds_read_b64_tr_b16 a[180:181], v37 offset:0\n" \
"s_sub_i32 s4, s4, 36\n" \
"s_waitcnt lgkmcnt(13)\n" \
"v_mfma_f32_16x16x32_bf16 a[60:63], a[204:207], a[236:239], a[60:63]\n" \
"ds_read_b64_tr_b16 a[182:183], v37 offset:1024\n" \
"v_add_u32_e32 v212, s4, v212\n" \
"v_mfma_f32_16x16x32_bf16 a[44:47], a[136:139], a[172:175], 0\n" \
"ds_read_b64_tr_b16 a[244:245], v37 offset:8192\n" \
"v_mov_b32_e64 v215, s12\n" \
"v_mfma_f32_16x16x32_bf16 a[44:47], a[200:203], a[236:239], a[44:47]\n" \
"ds_read_b64_tr_b16 a[246:247], v37 offset:9216\n" \
"v_mov_b32_e64 v216, s8\n" \
"v_mfma_f32_16x16x32_bf16 a[28:31], a[132:135], a[172:175], 0\n" \
"ds_read_b64_tr_b16 a[184:185], v38 offset:0\n" \
"v_cndmask_b32_e64 v212, v212, v213, vcc\n" \
"v_mfma_f32_16x16x32_bf16 a[28:31], a[196:199], a[236:239], a[28:31]\n" \
"ds_read_b64_tr_b16 a[186:187], v38 offset:1024\n" \
"v_cndmask_b32_e64 v216, v216, v215, vcc\n" \
"v_mfma_f32_16x16x32_bf16 a[12:15], a[128:131], a[172:175], 0\n" \
"v_lshlrev_b32_e32 v212, 7, v212\n" \
"v_mov_b32_e32 v213, 0\n" \
"v_mfma_f32_16x16x32_bf16 a[12:15], a[192:195], a[236:239], a[12:15]\n" \
"v_lshl_add_u64 v[188:189], v[216:217], 0, v[212:213]\n" \
"s_movk_i32 s5, 0xd80\n" \
"s_waitcnt lgkmcnt(15)\n" \
"v_mfma_f32_16x16x32_bf16 a[76:79], a[144:147], a[172:175], 0\n" \
"ds_read_b64_tr_b16 a[248:249], v38 offset:8192\n" \
"s_lshr_b32 s6, s2, 5\n" \
"v_mfma_f32_16x16x32_bf16 a[76:79], a[208:211], a[236:239], a[76:79]\n" \
"v_lshrrev_b32_e32 v212, 4, v20\n" \
"v_lshl_or_b32 v215, s3, 6, v212\n" \
"v_mfma_f32_16x16x32_bf16 a[72:75], a[144:147], a[168:171], 0\n" \
"v_mul_u32_u24_e32 v216, 0x2aab, v215\n" \
"v_lshrrev_b32_e64 v216, 16, v216\n" \
"v_mfma_f32_16x16x32_bf16 a[72:75], a[208:211], a[232:235], a[72:75]\n" \
"ds_read_b64_tr_b16 a[250:251], v38 offset:9216\n" \
"v_mad_i32_i24 v217, v216, -6, v215\n" \
"v_mfma_f32_16x16x32_bf16 a[68:71], a[144:147], a[164:167], 0\n" \
"v_add_u32_e32 v218, 0xfffffe50, v215\n" \
"s_mul_i32 s35, s6, 0x48\n" \
"v_mfma_f32_16x16x32_bf16 a[68:71], a[208:211], a[228:231], a[68:71]\n" \
"v_lshrrev_b32_e32 v219, 1, v217\n" \
"v_add_u32_e32 v218, s35, v216\n" \
"v_mfma_f32_16x16x32_bf16 a[64:67], a[144:147], a[160:163], 0\n" \
"v_mul_u32_u24_e32 v219, 0x480, v219\n" \
"v_and_b32_e64 v214, 16, v20\n" \
"v_mfma_f32_16x16x32_bf16 a[64:67], a[208:211], a[224:227], a[64:67]\n" \
"ds_read_b64_tr_b16 a[188:189], v39 offset:0\n" \
"v_mad_u32_u24 v219, v218, s5, v219\n" \
"v_mfma_f32_16x16x32_bf16 a[80:83], a[148:151], a[160:163], 0\n" \
"v_or_b32_e32 v192, v219, v214\n" \
"v_mov_b32_e32 v220, 2.0\n" \
"s_waitcnt lgkmcnt(15)\n" \
"v_mfma_f32_16x16x32_bf16 a[80:83], a[212:215], a[224:227], a[80:83]\n" \
"v_mov_b32_e32 v222, 7\n" \
"v_lshlrev_b32_e64 v220, 7, v218\n" \
"v_mfma_f32_16x16x32_bf16 a[84:87], a[148:151], a[164:167], 0\n" \
"ds_read_b64_tr_b16 a[190:191], v39 offset:1024\n" \
"v_lshrrev_b32_e64 v218, 1, v218\n" \
"v_mfma_f32_16x16x32_bf16 a[84:87], a[212:215], a[228:231], a[84:87]\n" \
"v_and_b32_e32 v218, 4, v218\n" \
"v_and_b32_e32 v216, 2, v216\n" \
"v_mfma_f32_16x16x32_bf16 a[88:91], a[148:151], a[168:171], 0\n" \
"s_mul_i32 s7, s6, 0x2400\n" \
"s_sub_i32 s34, 0x22000, s7\n" \
"v_mfma_f32_16x16x32_bf16 a[88:91], a[212:215], a[232:235], a[88:91]\n" \
"ds_read_b64_tr_b16 a[252:253], v39 offset:8192\n" \
"v_bitop3_b32 v216, v217, v218, v216 bitop3:0x1e\n" \
"v_mfma_f32_16x16x32_bf16 a[92:95], a[148:151], a[172:175], 0\n" \
"v_or_b32_e32 v213, 0x24400, v20\n" \
"v_lshlrev_b32_e64 v216, 4, v216\n" \
"v_mfma_f32_16x16x32_bf16 a[92:95], a[212:215], a[236:239], a[92:95]\n" \
"v_add3_u32 v196, v220, s34, v216\n" \
"v_mov_b32_e64 v216, v213\n" \
"v_mfma_f32_16x16x32_bf16 a[108:111], a[152:155], a[172:175], 0\n" \
"ds_read_b64_tr_b16 a[254:255], v39 offset:9216\n" \
"s_movk_i32 s36, 0x1b0\n" \
"v_mfma_f32_16x16x32_bf16 a[108:111], a[216:219], a[236:239], a[108:111]\n" \
"v_or_b32_e32 v216, 0x100, v215\n" \
"v_mul_u32_u24_e32 v217, 0x2aab, v216\n" \
"v_mfma_f32_16x16x32_bf16 a[104:107], a[152:155], a[168:171], 0\n" \
"v_add_u32_e32 v215, 0xffffff50, v215\n" \
"v_lshrrev_b32_e32 v217, 16, v217\n" \
"v_mfma_f32_16x16x32_bf16 a[104:107], a[216:219], a[232:235], a[104:107]\n" \
"v_mov_b32_e64 v220, v215\n" \
"v_mad_i32_i24 v218, v217, -6, v216\n" \
"v_mfma_f32_16x16x32_bf16 a[100:103], a[152:155], a[164:167], 0\n" \
"v_cmp_gt_u32_e32 vcc, s36, v216\n" \
"v_mov_b32_e32 v221, 7\n" \
"v_mfma_f32_16x16x32_bf16 a[100:103], a[216:219], a[228:231], a[100:103]\n" \
"v_lshrrev_b32_e32 v222, 1, v218\n" \
"v_cndmask_b32_e32 v217, v220, v217, vcc\n" \
"v_mfma_f32_16x16x32_bf16 a[96:99], a[152:155], a[160:163], 0\n" \
"s_movk_i32 s4, 0x48\n" \
"v_add_u32_e32 v220, s35, v217\n" \
"v_mfma_f32_16x16x32_bf16 a[96:99], a[216:219], a[224:227], a[96:99]\n" \
"v_mul_u32_u24_e32 v222, 0x480, v222\n" \
"v_mad_u32_u24 v222, v220, s5, v222\n" \
"s_waitcnt lgkmcnt(15)\n" \
"v_mfma_f32_16x16x32_bf16 a[112:115], a[156:159], a[160:163], 0\n" \
"v_cmp_gt_u32_e64 s[4:5], s4, v215\n" \
"v_cndmask_b32_e32 v215, v221, v218, vcc\n" \
"v_mfma_f32_16x16x32_bf16 a[112:115], a[220:223], a[224:227], a[112:115]\n" \
"v_lshrrev_b32_e32 v218, 1, v220\n" \
"v_or_b32_e32 v214, v222, v214\n" \
"v_mfma_f32_16x16x32_bf16 a[116:119], a[156:159], a[164:167], 0\n" \
"v_mov_b32_e32 v222, 2.0\n" \
"v_and_b32_e32 v218, 4, v218\n" \
"v_mfma_f32_16x16x32_bf16 a[116:119], a[220:223], a[228:231], a[116:119]\n" \
"v_and_b32_e64 v217, 2, v217\n" \
"v_bitop3_b32 v215, v215, v218, v217 bitop3:0x1e\n" \
"v_mfma_f32_16x16x32_bf16 a[120:123], a[156:159], a[168:171], 0\n" \
"v_lshlrev_b32_e32 v223, 1, v220\n" \
"v_mov_b32_e32 v224, 2.0\n" \
"v_mfma_f32_16x16x32_bf16 a[120:123], a[220:223], a[232:235], a[120:123]\n" \
"v_cndmask_b32_e32 v193, v222, v214, vcc\n" \
"v_lshlrev_b32_e32 v214, 7, v220\n" \
"v_mfma_f32_16x16x32_bf16 a[124:127], a[156:159], a[172:175], 0\n" \
"v_lshlrev_b32_e32 v215, 4, v215\n" \
"s_mul_i32 s39, s3, 18\n" \
"s_waitcnt vmcnt(5) lgkmcnt(0)\n" \
"v_mfma_f32_16x16x32_bf16 a[124:127], a[220:223], a[236:239], a[124:127]\n" \
"s_barrier\n" \
"v_mfma_f32_16x16x32_bf16 v[52:55], a[128:131], a[176:179], 0\n" \
"s_add_u32 m0, s30, 0x0\n" \
"s_movk_i32 s84, 0x100\n" \
"v_mfma_f32_16x16x32_bf16 v[52:55], a[192:195], a[240:243], v[52:55]\n" \
"buffer_load_dwordx4 v22, s[64:67], s84 offen offset:0 lds\n" \
"v_mfma_f32_16x16x32_bf16 v[56:59], a[128:131], a[180:183], 0\n" \
"ds_read_b64_tr_b16 a[160:161], v32 offset:32768\n" \
"v_add3_u32 v214, v214, s34, v215\n" \
"v_mfma_f32_16x16x32_bf16 v[56:59], a[192:195], a[244:247], v[56:59]\n" \
"s_movk_i32 s84, 0x1d00\n" \
"v_mfma_f32_16x16x32_bf16 v[60:63], a[128:131], a[184:187], 0\n" \
"buffer_load_dwordx4 v23, s[64:67], s84 offen offset:1024 lds\n" \
"v_mfma_f32_16x16x32_bf16 v[60:63], a[192:195], a[248:251], v[60:63]\n" \
"ds_read_b64_tr_b16 a[162:163], v32 offset:33792\n" \
"v_cndmask_b32_e64 v195, v224, v223, s[4:5]\n" \
"v_mfma_f32_16x16x32_bf16 v[64:67], a[128:131], a[188:191], 0\n" \
"ds_read_b64_tr_b16 a[224:225], v32 offset:40960\n" \
"s_movk_i32 s4, 0x1f8\n" \
"v_mfma_f32_16x16x32_bf16 v[64:67], a[192:195], a[252:255], v[64:67]\n" \
"ds_read_b128 a[128:131], v28 offset:49152\n" \
"v_mov_b32_e64 v215, v213\n" \
"v_mfma_f32_16x16x32_bf16 v[80:83], a[132:135], a[188:191], 0\n" \
"ds_read_b128 a[192:195], v29 offset:49152\n" \
"s_add_i32 s35, s35, s39\n" \
"v_mfma_f32_16x16x32_bf16 v[80:83], a[196:199], a[252:255], v[80:83]\n" \
"s_movk_i32 s84, 0x3900\n" \
"v_mfma_f32_16x16x32_bf16 v[76:79], a[132:135], a[184:187], 0\n" \
"buffer_load_dwordx4 v22, s[64:67], s84 offen offset:2048 lds\n" \
"v_mfma_f32_16x16x32_bf16 v[76:79], a[196:199], a[248:251], v[76:79]\n" \
"s_movk_i32 s84, 0x5500\n" \
"v_mfma_f32_16x16x32_bf16 v[72:75], a[132:135], a[180:183], 0\n" \
"buffer_load_dwordx4 v23, s[64:67], s84 offen offset:3072 lds\n" \
"v_mfma_f32_16x16x32_bf16 v[72:75], a[196:199], a[244:247], v[72:75]\n" \
"s_add_u32 m0, s31, 0x0\n" \
"s_mov_b32 s84, 0x100000\n" \
"v_mfma_f32_16x16x32_bf16 v[68:71], a[132:135], a[176:179], 0\n" \
"buffer_load_dwordx4 v26, s[68:71], s84 offen offset:0 lds\n" \
"v_mfma_f32_16x16x32_bf16 v[68:71], a[196:199], a[240:243], v[68:71]\n" \
"ds_read_b128 a[132:135], v28 offset:51200\n" \
"v_cmp_gt_u32_e64 vcc, s4, v216\n" \
"v_mfma_f32_16x16x32_bf16 v[84:87], a[136:139], a[176:179], 0\n" \
"ds_read_b128 a[196:199], v29 offset:51200\n" \
"v_add_u32_e64 v212, s35, v212\n" \
"v_mfma_f32_16x16x32_bf16 v[84:87], a[200:203], a[240:243], v[84:87]\n" \
"s_addk_i32 s84, 0x7c00\n" \
"v_mfma_f32_16x16x32_bf16 v[88:91], a[136:139], a[180:183], 0\n" \
"buffer_load_dwordx4 v26, s[68:71], s84 offen offset:1024 lds\n" \
"v_mfma_f32_16x16x32_bf16 v[88:91], a[200:203], a[244:247], v[88:91]\n" \
"ds_read_b64_tr_b16 a[226:227], v32 offset:41984\n" \
"v_mov_b32_e32 v219, 2.0\n" \
"v_mfma_f32_16x16x32_bf16 v[92:95], a[136:139], a[184:187], 0\n" \
"s_addk_i32 s84, 0x7c00\n" \
"v_mfma_f32_16x16x32_bf16 v[92:95], a[200:203], a[248:251], v[92:95]\n" \
"buffer_load_dwordx4 v27, s[68:71], s84 offen offset:2048 lds\n" \
"v_mfma_f32_16x16x32_bf16 v[96:99], a[136:139], a[188:191], 0\n" \
"ds_read_b64_tr_b16 a[164:165], v33 offset:32768\n" \
"v_cndmask_b32_e32 v197, v215, v214, vcc\n" \
"v_mfma_f32_16x16x32_bf16 v[96:99], a[200:203], a[252:255], v[96:99]\n" \
"ds_read_b128 a[136:139], v28 offset:53248\n" \
"s_movk_i32 s35, 0x120\n" \
"v_mfma_f32_16x16x32_bf16 v[112:115], a[140:143], a[188:191], 0\n" \
"ds_read_b128 a[200:203], v29 offset:53248\n" \
"v_mul_u32_u24_e32 v214, 0xd80, v212\n" \
"v_mfma_f32_16x16x32_bf16 v[112:115], a[204:207], a[252:255], v[112:115]\n" \
"s_addk_i32 s84, 0x7c00\n" \
"v_mfma_f32_16x16x32_bf16 v[108:111], a[140:143], a[184:187], 0\n" \
"buffer_load_dwordx4 v27, s[68:71], s84 offen offset:3072 lds\n" \
"v_mfma_f32_16x16x32_bf16 v[108:111], a[204:207], a[248:251], v[108:111]\n" \
"s_add_u32 m0, s30, 0x1000\n" \
"s_mov_b32 s84, 0x8100\n" \
"v_mfma_f32_16x16x32_bf16 v[104:107], a[140:143], a[180:183], 0\n" \
"buffer_load_dwordx4 v22, s[64:67], s84 offen offset:0 lds\n" \
"v_mfma_f32_16x16x32_bf16 v[104:107], a[204:207], a[244:247], v[104:107]\n" \
"ds_read_b64_tr_b16 a[166:167], v33 offset:33792\n" \
"v_cmp_gt_u32_e64 vcc, s35, v20\n" \
"v_mfma_f32_16x16x32_bf16 v[100:103], a[140:143], a[176:179], 0\n" \
"ds_read_b64_tr_b16 a[228:229], v33 offset:40960\n" \
"v_lshrrev_b32_e64 v215, 1, v212\n" \
"v_mfma_f32_16x16x32_bf16 v[100:103], a[204:207], a[240:243], v[100:103]\n" \
"ds_read_b128 a[140:143], v28 offset:55296\n" \
"s_and_b32 s4, s2, 7\n" \
"v_mfma_f32_16x16x32_bf16 v[116:119], a[144:147], a[176:179], 0\n" \
"ds_read_b128 a[204:207], v29 offset:55296\n" \
"v_cndmask_b32_e32 v194, v219, v214, vcc\n" \
"v_mfma_f32_16x16x32_bf16 v[116:119], a[208:211], a[240:243], v[116:119]\n" \
"s_addk_i32 s84, 0x1c00\n" \
"v_mfma_f32_16x16x32_bf16 v[120:123], a[144:147], a[180:183], 0\n" \
"buffer_load_dwordx4 v23, s[64:67], s84 offen offset:1024 lds\n" \
"v_mfma_f32_16x16x32_bf16 v[120:123], a[208:211], a[244:247], v[120:123]\n" \
"s_addk_i32 s84, 0x1c00\n" \
"v_mfma_f32_16x16x32_bf16 v[124:127], a[144:147], a[184:187], 0\n" \
"buffer_load_dwordx4 v22, s[64:67], s84 offen offset:2048 lds\n" \
"v_mfma_f32_16x16x32_bf16 v[124:127], a[208:211], a[248:251], v[124:127]\n" \
"s_addk_i32 s84, 0x1c00\n" \
".fill (L_pb + 44768 - .) / 4, 4, 0xBF800000\n" \
"L_pp11:\n" \
"s_cbranch_execz L_pret\n" \
"v_mfma_f32_16x16x32_bf16 v[128:131], a[144:147], a[188:191], 0\n" \
"buffer_load_dwordx4 v23, s[64:67], s84 offen offset:3072 lds\n" \
"v_mfma_f32_16x16x32_bf16 v[128:131], a[208:211], a[252:255], v[128:131]\n" \
"ds_read_b128 a[144:147], v28 offset:32768\n" \
"v_lshlrev_b32_e32 v214, 7, v212\n" \
"v_mfma_f32_16x16x32_bf16 v[144:147], a[148:151], a[188:191], 0\n" \
"ds_read_b128 a[208:211], v29 offset:32768\n" \
"v_lshrrev_b32_e64 v212, 2, v212\n" \
"v_mfma_f32_16x16x32_bf16 v[144:147], a[212:215], a[252:255], v[144:147]\n" \
"s_add_u32 m0, s31, 0x4000\n" \
"s_mov_b32 s84, 0x100000\n" \
"v_mfma_f32_16x16x32_bf16 v[140:143], a[148:151], a[184:187], 0\n" \
"buffer_load_dwordx4 v26, s[72:75], s84 offen offset:0 lds\n" \
"v_mfma_f32_16x16x32_bf16 v[140:143], a[212:215], a[248:251], v[140:143]\n" \
"ds_read_b64_tr_b16 a[230:231], v33 offset:41984\n" \
"v_and_b32_e64 v212, 2, v212\n" \
"v_mfma_f32_16x16x32_bf16 v[136:139], a[148:151], a[180:183], 0\n" \
"s_addk_i32 s84, 0x7c00\n" \
"v_mfma_f32_16x16x32_bf16 v[136:139], a[212:215], a[244:247], v[136:139]\n" \
"buffer_load_dwordx4 v26, s[72:75], s84 offen offset:1024 lds\n" \
"v_mfma_f32_16x16x32_bf16 v[132:135], a[148:151], a[176:179], 0\n" \
"ds_read_b64_tr_b16 a[168:169], v34 offset:32768\n" \
"v_and_or_b32 v212, v215, 1, v212\n" \
"v_mfma_f32_16x16x32_bf16 v[132:135], a[212:215], a[240:243], v[132:135]\n" \
"ds_read_b128 a[148:151], v28 offset:34816\n" \
"s_lshl_b32 s5, s4, 2\n" \
"v_mfma_f32_16x16x32_bf16 v[148:151], a[152:155], a[176:179], 0\n" \
"ds_read_b128 a[212:215], v29 offset:34816\n" \
"s_bfe_u32 s38, s2, 0x20003\n" \
"v_mfma_f32_16x16x32_bf16 v[148:151], a[216:219], a[240:243], v[148:151]\n" \
"ds_read_b64_tr_b16 a[170:171], v34 offset:33792\n" \
"v_lshlrev_b32_e64 v212, 5, v212\n" \
"v_mfma_f32_16x16x32_bf16 v[152:155], a[152:155], a[180:183], 0\n" \
"ds_read_b64_tr_b16 a[232:233], v34 offset:40960\n" \
"s_movk_i32 s35, 0x60\n" \
"v_mfma_f32_16x16x32_bf16 v[152:155], a[216:219], a[244:247], v[152:155]\n" \
"ds_read_b64_tr_b16 a[234:235], v34 offset:41984\n" \
"s_or_b32 s5, s5, s38\n" \
"v_mfma_f32_16x16x32_bf16 v[156:159], a[152:155], a[184:187], 0\n" \
"ds_read_b64_tr_b16 a[172:173], v35 offset:32768\n" \
"v_bitop3_b32 v212, v212, s35, v214 bitop3:0x36\n" \
"v_mfma_f32_16x16x32_bf16 v[156:159], a[216:219], a[248:251], v[156:159]\n" \
"ds_read_b64_tr_b16 a[174:175], v35 offset:33792\n" \
"s_lshr_b32 s36, s2, 3\n" \
"v_mfma_f32_16x16x32_bf16 v[160:163], a[152:155], a[188:191], 0\n" \
"ds_read_b64_tr_b16 a[236:237], v35 offset:40960\n" \
"v_add_u32_e32 v212, s34, v212\n" \
"v_mfma_f32_16x16x32_bf16 v[160:163], a[216:219], a[252:255], v[160:163]\n" \
"ds_read_b128 a[152:155], v28 offset:36864\n" \
"s_mul_i32 s34, s5, 0x12000\n" \
"v_mfma_f32_16x16x32_bf16 v[176:179], a[156:159], a[188:191], 0\n" \
"ds_read_b128 a[216:219], v29 offset:36864\n" \
"s_or_b32 s40, s34, 0x6000000\n" \
"v_mfma_f32_16x16x32_bf16 v[176:179], a[220:223], a[252:255], v[176:179]\n" \
"ds_read_b64_tr_b16 a[238:239], v35 offset:41984\n" \
"s_mul_i32 s34, s36, 0x90000\n" \
"v_mfma_f32_16x16x32_bf16 v[172:175], a[156:159], a[184:187], 0\n" \
"s_lshl_b32 s36, s3, 14\n" \
"s_add_i32 s34, s34, s36\n" \
"v_mfma_f32_16x16x32_bf16 v[172:175], a[220:223], a[248:251], v[172:175]\n" \
"s_lshl_b32 s38, s5, 8\n" \
"s_add_i32 s41, s34, 0x4240000\n" \
"v_mfma_f32_16x16x32_bf16 v[168:171], a[156:159], a[180:183], 0\n" \
"s_mul_i32 s34, s5, 36\n" \
"s_add_i32 s5, s7, s40\n" \
"v_mfma_f32_16x16x32_bf16 v[168:171], a[220:223], a[244:247], v[168:171]\n" \
"s_addk_i32 s5, 0x2000\n" \
"s_mov_b32 s37, 2.0\n" \
"v_mfma_f32_16x16x32_bf16 v[164:167], a[156:159], a[176:179], 0\n" \
"s_or_b32 s38, s38, 0x5be8000\n" \
"v_mfma_f32_16x16x32_bf16 v[164:167], a[220:223], a[240:243], v[164:167]\n" \
"ds_read_b128 a[156:159], v28 offset:38912\n" \
"ds_read_b128 a[220:223], v29 offset:38912\n" \
"s_waitcnt lgkmcnt(15)\n" \
"v_mfma_f32_16x16x32_bf16 a[0:3], a[128:131], a[160:163], a[0:3]\n" \
"s_addk_i32 s84, 0x7c00\n" \
"v_mfma_f32_16x16x32_bf16 a[0:3], a[192:195], a[224:227], a[0:3]\n" \
"buffer_load_dwordx4 v27, s[72:75], s84 offen offset:2048 lds\n" \
"v_mfma_f32_16x16x32_bf16 a[4:7], a[128:131], a[164:167], a[4:7]\n" \
"ds_read_b64_tr_b16 a[176:177], v36 offset:32768\n" \
"s_waitcnt lgkmcnt(15)\n" \
"v_mfma_f32_16x16x32_bf16 a[4:7], a[192:195], a[228:231], a[4:7]\n" \
"s_addk_i32 s84, 0x7c00\n" \
"s_waitcnt lgkmcnt(11)\n" \
"v_mfma_f32_16x16x32_bf16 a[8:11], a[128:131], a[168:171], a[8:11]\n" \
"buffer_load_dwordx4 v27, s[72:75], s84 offen offset:3072 lds\n" \
"s_waitcnt lgkmcnt(9)\n" \
"v_mfma_f32_16x16x32_bf16 a[8:11], a[192:195], a[232:235], a[8:11]\n" \
"ds_read_b64_tr_b16 a[178:179], v36 offset:33792\n" \
"s_waitcnt lgkmcnt(8)\n" \
"v_mfma_f32_16x16x32_bf16 a[12:15], a[128:131], a[172:175], a[12:15]\n" \
"ds_read_b64_tr_b16 a[240:241], v36 offset:40960\n" \
"s_waitcnt lgkmcnt(5)\n" \
"v_mfma_f32_16x16x32_bf16 a[12:15], a[192:195], a[236:239], a[12:15]\n" \
"s_cmp_lg_u32 s3, 0\n" \
"s_cselect_b32 s36, s37, s5\n" \
"v_mfma_f32_16x16x32_bf16 a[28:31], a[132:135], a[172:175], a[28:31]\n" \
"s_lshl_b32 s5, s6, 2\n" \
"s_or_b32 s5, s38, s5\n" \
"v_mfma_f32_16x16x32_bf16 a[28:31], a[196:199], a[236:239], a[28:31]\n" \
"ds_read_b64_tr_b16 a[242:243], v36 offset:41984\n" \
"s_lshl_b32 s35, s3, 10\n" \
"v_mfma_f32_16x16x32_bf16 a[24:27], a[132:135], a[168:171], a[24:27]\n" \
"s_cmpk_lt_u32 s3, 3\n" \
"s_cselect_b32 s37, s37, s5\n" \
"v_mfma_f32_16x16x32_bf16 a[24:27], a[196:199], a[232:235], a[24:27]\n" \
"s_lshl_b32 s5, s4, 16\n" \
"s_lshl_b32 s4, s4, 11\n" \
"v_mfma_f32_16x16x32_bf16 a[20:23], a[132:135], a[164:167], a[20:23]\n" \
"v_cndmask_b32_e32 v198, v213, v212, vcc\n" \
"v_or_b32_e32 v212, s35, v20\n" \
"v_mfma_f32_16x16x32_bf16 a[20:23], a[196:199], a[228:231], a[20:23]\n" \
"ds_read_b64_tr_b16 a[180:181], v37 offset:32768\n" \
"s_or_b32 s39, s40, s35\n" \
"v_mfma_f32_16x16x32_bf16 a[16:19], a[132:135], a[160:163], a[16:19]\n" \
"s_or_b32 s55, s41, s4\n" \
"v_or_b32_e32 v199, 0x22000, v212\n" \
"v_mfma_f32_16x16x32_bf16 a[16:19], a[196:199], a[224:227], a[16:19]\n" \
"s_add_i32 s35, s39, s7\n" \
"s_add_i32 s54, s41, s5\n" \
"v_mfma_f32_16x16x32_bf16 a[32:35], a[136:139], a[160:163], a[32:35]\n" \
"s_mov_b32 m0, s32\n" \
"ds_read_b64_tr_b16 a[182:183], v37 offset:33792\n" \
"v_mfma_f32_16x16x32_bf16 a[32:35], a[200:203], a[224:227], a[32:35]\n" \
"s_mov_b32 exec_lo, 0x0\n" \
"s_mov_b32 exec_hi, 0xff\n" \
"global_load_lds_dword v[184:185], off\n" \
"s_mov_b64 exec, -1\n" \
"v_mfma_f32_16x16x32_bf16 a[36:39], a[136:139], a[164:167], a[36:39]\n" \
"s_add_i32 s55, s55, 0x80000\n" \
"v_mov_b32_e32 v215, 0xffff\n" \
"v_mfma_f32_16x16x32_bf16 a[36:39], a[200:203], a[228:231], a[36:39]\n" \
"ds_read_b64_tr_b16 a[244:245], v37 offset:40960\n" \
"s_and_b32 s4, s3, 1\n" \
"v_mfma_f32_16x16x32_bf16 a[40:43], a[136:139], a[168:171], a[40:43]\n" \
"s_lshl_b32 s5, s4, 4\n" \
"v_lshrrev_b32_e32 v212, 4, v20\n" \
"v_mfma_f32_16x16x32_bf16 a[40:43], a[200:203], a[232:235], a[40:43]\n" \
"s_mov_b32 m0, s32\n" \
"v_lshrrev_b32_e32 v218, 6, v20\n" \
"v_mfma_f32_16x16x32_bf16 a[44:47], a[136:139], a[172:175], a[44:47]\n" \
"s_mov_b32 exec_lo, 0xffff0000\n" \
"s_mov_b32 exec_hi, 0x0\n" \
"global_load_lds_dword v[186:187], off\n" \
"s_mov_b64 exec, -1\n" \
"v_mfma_f32_16x16x32_bf16 a[44:47], a[200:203], a[236:239], a[44:47]\n" \
"ds_read_b64_tr_b16 a[246:247], v37 offset:41984\n" \
"v_and_b32_e64 v219, 12, v218\n" \
"v_mfma_f32_16x16x32_bf16 a[60:63], a[140:143], a[172:175], a[60:63]\n" \
"v_and_or_b32 v214, v212, 15, s5\n" \
"v_cmp_gt_u32_e64 vcc, v219, v214\n" \
"v_mfma_f32_16x16x32_bf16 a[60:63], a[204:207], a[236:239], a[60:63]\n" \
"v_mov_b32_e32 v220, 0xffff0000\n" \
"s_lshl_b32 s5, s2, 2\n" \
"v_mfma_f32_16x16x32_bf16 a[56:59], a[140:143], a[168:171], a[56:59]\n" \
"ds_read_b64_tr_b16 a[184:185], v38 offset:32768\n" \
"v_cndmask_b32_e64 v217, v215, 0, vcc\n" \
"v_mfma_f32_16x16x32_bf16 a[56:59], a[204:207], a[232:235], a[56:59]\n" \
"v_cmp_lt_u32_e32 vcc, v219, v214\n" \
"v_mov_b32_e64 v213, s23\n" \
"v_mfma_f32_16x16x32_bf16 a[52:55], a[140:143], a[164:167], a[52:55]\n" \
"s_or_b32 s5, s5, s3\n" \
"v_cndmask_b32_e32 v221, 0, v220, vcc\n" \
"v_mfma_f32_16x16x32_bf16 a[52:55], a[204:207], a[228:231], a[52:55]\n" \
"ds_read_b64_tr_b16 a[186:187], v38 offset:33792\n" \
"v_or_b32_e64 v12, v217, v221\n" \
"v_mfma_f32_16x16x32_bf16 a[48:51], a[140:143], a[160:163], a[48:51]\n" \
"v_or_b32_e32 v217, 2, v219\n" \
"v_cmp_gt_u32_e32 vcc, v217, v214\n" \
"v_mfma_f32_16x16x32_bf16 a[48:51], a[204:207], a[224:227], a[48:51]\n" \
"v_mov_b32_e32 v216, s25\n" \
"s_movk_i32 s40, 0xf400\n" \
"v_mfma_f32_16x16x32_bf16 a[64:67], a[144:147], a[160:163], a[64:67]\n" \
"ds_read_b64_tr_b16 a[248:249], v38 offset:40960\n" \
"v_cndmask_b32_e64 v221, v215, 0, vcc\n" \
"v_mfma_f32_16x16x32_bf16 a[64:67], a[208:211], a[224:227], a[64:67]\n" \
"v_cmp_lt_u32_e32 vcc, v217, v214\n" \
"s_lshl_b32 s4, s4, 6\n" \
"v_mfma_f32_16x16x32_bf16 a[68:71], a[144:147], a[164:167], a[68:71]\n" \
"v_cndmask_b32_e32 v217, 0, v220, vcc\n" \
"v_or_b32_e32 v13, v221, v217\n" \
"v_mfma_f32_16x16x32_bf16 a[68:71], a[208:211], a[228:231], a[68:71]\n" \
"ds_read_b64_tr_b16 a[250:251], v38 offset:41984\n" \
"v_or_b32_e64 v217, 16, v219\n" \
"v_mfma_f32_16x16x32_bf16 a[72:75], a[144:147], a[168:171], a[72:75]\n" \
"v_cmp_gt_u32_e32 vcc, v217, v214\n" \
"s_nop 1\n" \
"v_cndmask_b32_e64 v221, v215, 0, vcc\n" \
"v_mfma_f32_16x16x32_bf16 a[72:75], a[208:211], a[232:235], a[72:75]\n" \
"v_cmp_lt_u32_e32 vcc, v217, v214\n" \
"s_nop 1\n" \
"v_cndmask_b32_e64 v217, 0, v220, vcc\n" \
"v_mfma_f32_16x16x32_bf16 a[76:79], a[144:147], a[172:175], a[76:79]\n" \
"v_or_b32_e32 v14, v221, v217\n" \
"v_or_b32_e32 v217, 18, v219\n" \
"v_mfma_f32_16x16x32_bf16 a[76:79], a[208:211], a[236:239], a[76:79]\n" \
"ds_read_b64_tr_b16 a[188:189], v39 offset:32768\n" \
"v_cmp_gt_u32_e64 vcc, v217, v214\n" \
"v_mfma_f32_16x16x32_bf16 a[92:95], a[148:151], a[172:175], a[92:95]\n" \
"s_nop 0\n" \
"v_cndmask_b32_e64 v215, v215, 0, vcc\n" \
"v_cmp_lt_u32_e32 vcc, v217, v214\n" \
"v_mfma_f32_16x16x32_bf16 a[92:95], a[212:215], a[236:239], a[92:95]\n" \
"s_nop 0\n" \
"v_cndmask_b32_e32 v214, 0, v220, vcc\n" \
"v_or_b32_e64 v15, v215, v214\n" \
"v_mfma_f32_16x16x32_bf16 a[88:91], a[148:151], a[168:171], a[88:91]\n" \
"ds_read_b64_tr_b16 a[190:191], v39 offset:33792\n" \
"v_mad_u64_u32 v[214:215], s[6:7], s5, 42, v[212:213]\n" \
"v_mfma_f32_16x16x32_bf16 a[88:91], a[212:215], a[232:235], a[88:91]\n" \
"s_movk_i32 s5, 0x1440\n" \
"v_cmp_gt_u32_e32 vcc, s5, v214\n" \
"v_mfma_f32_16x16x32_bf16 a[84:87], a[148:151], a[164:167], a[84:87]\n" \
"v_min_u32_e32 v217, 0xa43f, v214\n" \
"v_mov_b32_e64 v214, s24\n" \
"v_mfma_f32_16x16x32_bf16 a[84:87], a[212:215], a[228:231], a[84:87]\n" \
"ds_read_b64_tr_b16 a[252:253], v39 offset:40960\n" \
"v_cndmask_b32_e64 v215, v216, v213, vcc\n" \
"v_mfma_f32_16x16x32_bf16 a[80:83], a[148:151], a[160:163], a[80:83]\n" \
"v_mov_b32_e32 v213, s22\n" \
"s_movk_i32 s6, 0xffee\n" \
"v_mfma_f32_16x16x32_bf16 a[80:83], a[212:215], a[224:227], a[80:83]\n" \
"v_cndmask_b32_e64 v214, v214, v213, vcc\n" \
"v_add_u32_e32 v213, 0xffffebc0, v217\n" \
"v_mfma_f32_16x16x32_bf16 a[96:99], a[152:155], a[160:163], a[96:99]\n" \
"ds_read_b64_tr_b16 a[254:255], v39 offset:41984\n" \
"s_lshl_b32 s5, s2, 5\n" \
"v_mfma_f32_16x16x32_bf16 a[96:99], a[216:219], a[224:227], a[96:99]\n" \
"v_cndmask_b32_e32 v213, v213, v217, vcc\n" \
"v_lshlrev_b32_e64 v216, 7, v213\n" \
"v_mfma_f32_16x16x32_bf16 a[100:103], a[152:155], a[164:167], a[100:103]\n" \
"v_mul_u32_u24_e32 v213, 0xe39, v212\n" \
"v_mov_b32_e64 v217, 0\n" \
"v_mfma_f32_16x16x32_bf16 a[100:103], a[216:219], a[228:231], a[100:103]\n" \
"v_lshrrev_b32_e64 v213, 16, v213\n" \
"v_lshl_add_u64 v[16:17], v[214:215], 0, v[216:217]\n" \
"v_mfma_f32_16x16x32_bf16 a[104:107], a[152:155], a[168:171], a[104:107]\n" \
"v_mad_i32_i24 v214, v213, s6, v212\n" \
"s_add_i32 s5, s5, 32\n" \
"v_mfma_f32_16x16x32_bf16 a[104:107], a[216:219], a[232:235], a[104:107]\n" \
"v_lshlrev_b32_e32 v215, 4, v214\n" \
"v_add_u32_e64 v216, s5, v215\n" \
"v_mfma_f32_16x16x32_bf16 a[108:111], a[152:155], a[172:175], a[108:111]\n" \
"v_and_b32_e32 v216, 0xf0, v216\n" \
"v_cmp_gt_u32_e64 vcc, 16, v214\n" \
"v_mfma_f32_16x16x32_bf16 a[108:111], a[216:219], a[236:239], a[108:111]\n" \
"s_movk_i32 s7, 0xf800\n" \
"v_cndmask_b32_e32 v214, v215, v216, vcc\n" \
"s_waitcnt lgkmcnt(15)\n" \
"v_mfma_f32_16x16x32_bf16 a[124:127], a[156:159], a[172:175], a[124:127]\n" \
"v_or_b32_e32 v215, 64, v212\n" \
"v_mul_u32_u24_e32 v216, 0xe39, v215\n" \
"v_mfma_f32_16x16x32_bf16 a[124:127], a[220:223], a[236:239], a[124:127]\n" \
"v_lshrrev_b32_e64 v216, 16, v216\n" \
"v_mad_i32_i24 v215, v216, s6, v215\n" \
"v_mfma_f32_16x16x32_bf16 a[120:123], a[156:159], a[168:171], a[120:123]\n" \
"v_lshlrev_b32_e32 v217, 4, v215\n" \
"v_add_u32_e32 v220, s5, v217\n" \
"v_mfma_f32_16x16x32_bf16 a[120:123], a[220:223], a[232:235], a[120:123]\n" \
"v_and_b32_e32 v220, 0xf0, v220\n" \
"v_cmp_gt_u32_e64 vcc, 16, v215\n" \
"v_mfma_f32_16x16x32_bf16 a[116:119], a[156:159], a[164:167], a[116:119]\n" \
"s_nop 0\n" \
"v_cndmask_b32_e32 v215, v217, v220, vcc\n" \
"v_or_b32_e32 v217, 0x80, v212\n" \
"v_mfma_f32_16x16x32_bf16 a[116:119], a[220:223], a[228:231], a[116:119]\n" \
"v_mul_u32_u24_e32 v220, 0xe39, v217\n" \
"v_lshrrev_b32_e64 v220, 16, v220\n" \
"v_mfma_f32_16x16x32_bf16 a[112:115], a[156:159], a[160:163], a[112:115]\n" \
"v_mad_i32_i24 v217, v220, s6, v217\n" \
"v_lshlrev_b32_e64 v221, 4, v217\n" \
"v_mfma_f32_16x16x32_bf16 a[112:115], a[220:223], a[224:227], a[112:115]\n" \
"s_waitcnt vmcnt(20)\n" \
"v_readfirstlane_b32 s62, v180\n" \
"s_nop 3\n" \
"s_add_u32 s62, s62, 0x9E3779B1\n" \
"s_waitcnt vmcnt(2) lgkmcnt(0)\n" \
"s_barrier\n" \
"v_mfma_f32_16x16x32_bf16 v[52:55], a[128:131], a[176:179], v[52:55]\n" \
"s_add_u32 m0, s30, 0x8000\n" \
"s_movk_i32 s84, 0x180\n" \
"v_mfma_f32_16x16x32_bf16 v[52:55], a[192:195], a[240:243], v[52:55]\n" \
"buffer_load_dwordx4 v22, s[64:67], s84 offen offset:0 lds\n" \
"v_mfma_f32_16x16x32_bf16 v[56:59], a[128:131], a[180:183], v[56:59]\n" \
"ds_read_b64_tr_b16 a[160:161], v32 offset:0\n" \
"v_add_u32_e64 v222, s5, v221\n" \
"v_mfma_f32_16x16x32_bf16 v[56:59], a[192:195], a[244:247], v[56:59]\n" \
"s_movk_i32 s84, 0x1d80\n" \
"v_mfma_f32_16x16x32_bf16 v[60:63], a[128:131], a[184:187], v[60:63]\n" \
"buffer_load_dwordx4 v23, s[64:67], s84 offen offset:1024 lds\n" \
"v_mfma_f32_16x16x32_bf16 v[60:63], a[192:195], a[248:251], v[60:63]\n" \
"ds_read_b64_tr_b16 a[162:163], v32 offset:1024\n" \
"v_and_b32_e32 v222, 0xf0, v222\n" \
"v_mfma_f32_16x16x32_bf16 v[64:67], a[128:131], a[188:191], v[64:67]\n" \
"ds_read_b64_tr_b16 a[224:225], v32 offset:8192\n" \
"v_cmp_gt_u32_e32 vcc, 16, v217\n" \
"v_mfma_f32_16x16x32_bf16 v[64:67], a[192:195], a[252:255], v[64:67]\n" \
"ds_read_b128 a[128:131], v28 offset:16384\n" \
"v_cndmask_b32_e64 v217, v221, v222, vcc\n" \
"v_mfma_f32_16x16x32_bf16 v[80:83], a[132:135], a[188:191], v[80:83]\n" \
"ds_read_b128 a[192:195], v29 offset:16384\n" \
"v_or_b32_e32 v221, 0xc0, v212\n" \
"v_mfma_f32_16x16x32_bf16 v[80:83], a[196:199], a[252:255], v[80:83]\n" \
"s_movk_i32 s84, 0x3980\n" \
"v_mfma_f32_16x16x32_bf16 v[76:79], a[132:135], a[184:187], v[76:79]\n" \
"buffer_load_dwordx4 v22, s[64:67], s84 offen offset:2048 lds\n" \
"v_mfma_f32_16x16x32_bf16 v[76:79], a[196:199], a[248:251], v[76:79]\n" \
"s_movk_i32 s84, 0x5580\n" \
"v_mfma_f32_16x16x32_bf16 v[72:75], a[132:135], a[180:183], v[72:75]\n" \
"buffer_load_dwordx4 v23, s[64:67], s84 offen offset:3072 lds\n" \
"v_mfma_f32_16x16x32_bf16 v[72:75], a[196:199], a[244:247], v[72:75]\n" \
"s_add_u32 m0, s31, 0x8000\n" \
"s_mov_b32 s84, 0x180000\n" \
"v_mfma_f32_16x16x32_bf16 v[68:71], a[132:135], a[176:179], v[68:71]\n" \
"buffer_load_dwordx4 v26, s[68:71], s84 offen offset:0 lds\n" \
"v_mfma_f32_16x16x32_bf16 v[68:71], a[196:199], a[240:243], v[68:71]\n" \
"ds_read_b128 a[132:135], v28 offset:18432\n" \
"v_mul_u32_u24_e32 v222, 0xe39, v221\n" \
"v_mfma_f32_16x16x32_bf16 v[84:87], a[136:139], a[176:179], v[84:87]\n" \
"ds_read_b128 a[196:199], v29 offset:18432\n" \
"v_lshrrev_b32_e64 v222, 16, v222\n" \
"v_mfma_f32_16x16x32_bf16 v[84:87], a[200:203], a[240:243], v[84:87]\n" \
"s_addk_i32 s84, 0x7c00\n" \
"v_mfma_f32_16x16x32_bf16 v[88:91], a[136:139], a[180:183], v[88:91]\n" \
"buffer_load_dwordx4 v26, s[68:71], s84 offen offset:1024 lds\n" \
"v_mfma_f32_16x16x32_bf16 v[88:91], a[200:203], a[244:247], v[88:91]\n" \
"ds_read_b64_tr_b16 a[226:227], v32 offset:9216\n" \
"v_mad_i32_i24 v221, v222, s6, v221\n" \
"v_mfma_f32_16x16x32_bf16 v[92:95], a[136:139], a[184:187], v[92:95]\n" \
"s_addk_i32 s84, 0x7c00\n" \
"v_mfma_f32_16x16x32_bf16 v[92:95], a[200:203], a[248:251], v[92:95]\n" \
"buffer_load_dwordx4 v27, s[68:71], s84 offen offset:2048 lds\n" \
"v_mfma_f32_16x16x32_bf16 v[96:99], a[136:139], a[188:191], v[96:99]\n" \
"ds_read_b64_tr_b16 a[164:165], v33 offset:0\n" \
"v_lshlrev_b32_e64 v223, 4, v221\n" \
"v_mfma_f32_16x16x32_bf16 v[96:99], a[200:203], a[252:255], v[96:99]\n" \
"ds_read_b128 a[136:139], v28 offset:20480\n" \
"v_add_u32_e64 v224, s5, v223\n" \
"v_mfma_f32_16x16x32_bf16 v[112:115], a[140:143], a[188:191], v[112:115]\n" \
"ds_read_b128 a[200:203], v29 offset:20480\n" \
"v_and_b32_e32 v224, 0xf0, v224\n" \
"v_mfma_f32_16x16x32_bf16 v[112:115], a[204:207], a[252:255], v[112:115]\n" \
"s_addk_i32 s84, 0x7c00\n" \
"v_mfma_f32_16x16x32_bf16 v[108:111], a[140:143], a[184:187], v[108:111]\n" \
"buffer_load_dwordx4 v27, s[68:71], s84 offen offset:3072 lds\n" \
"v_mfma_f32_16x16x32_bf16 v[108:111], a[204:207], a[248:251], v[108:111]\n" \
"s_add_u32 m0, s30, 0x9000\n" \
"s_mov_b32 s84, 0x8180\n" \
"v_mfma_f32_16x16x32_bf16 v[104:107], a[140:143], a[180:183], v[104:107]\n" \
"buffer_load_dwordx4 v22, s[64:67], s84 offen offset:0 lds\n" \
"v_mfma_f32_16x16x32_bf16 v[104:107], a[204:207], a[244:247], v[104:107]\n" \
"ds_read_b64_tr_b16 a[166:167], v33 offset:1024\n" \
"v_cmp_gt_u32_e32 vcc, 16, v221\n" \
"v_mfma_f32_16x16x32_bf16 v[100:103], a[140:143], a[176:179], v[100:103]\n" \
"ds_read_b64_tr_b16 a[228:229], v33 offset:8192\n" \
"v_cndmask_b32_e64 v221, v223, v224, vcc\n" \
"v_mfma_f32_16x16x32_bf16 v[100:103], a[204:207], a[240:243], v[100:103]\n" \
"ds_read_b128 a[140:143], v28 offset:22528\n" \
"v_or_b32_e32 v223, 0x100, v212\n" \
"v_mfma_f32_16x16x32_bf16 v[116:119], a[144:147], a[176:179], v[116:119]\n" \
"ds_read_b128 a[204:207], v29 offset:22528\n" \
"v_mul_u32_u24_e32 v224, 0xe39, v223\n" \
"v_mfma_f32_16x16x32_bf16 v[116:119], a[208:211], a[240:243], v[116:119]\n" \
"s_addk_i32 s84, 0x1c00\n" \
"v_mfma_f32_16x16x32_bf16 v[120:123], a[144:147], a[180:183], v[120:123]\n" \
"buffer_load_dwordx4 v23, s[64:67], s84 offen offset:1024 lds\n" \
"v_mfma_f32_16x16x32_bf16 v[120:123], a[208:211], a[244:247], v[120:123]\n" \
"s_addk_i32 s84, 0x1c00\n" \
"v_mfma_f32_16x16x32_bf16 v[124:127], a[144:147], a[184:187], v[124:127]\n" \
"buffer_load_dwordx4 v22, s[64:67], s84 offen offset:2048 lds\n" \
"v_mfma_f32_16x16x32_bf16 v[124:127], a[208:211], a[248:251], v[124:127]\n" \
"s_addk_i32 s84, 0x1c00\n" \
"v_mfma_f32_16x16x32_bf16 v[128:131], a[144:147], a[188:191], v[128:131]\n" \
"buffer_load_dwordx4 v23, s[64:67], s84 offen offset:3072 lds\n" \
"v_mfma_f32_16x16x32_bf16 v[128:131], a[208:211], a[252:255], v[128:131]\n" \
"ds_read_b128 a[144:147], v28 offset:0\n" \
"v_lshrrev_b32_e32 v224, 16, v224\n" \
"v_mfma_f32_16x16x32_bf16 v[144:147], a[148:151], a[188:191], v[144:147]\n" \
"ds_read_b128 a[208:211], v29 offset:0\n" \
"v_mad_i32_i24 v223, v224, s6, v223\n" \
"v_mfma_f32_16x16x32_bf16 v[144:147], a[212:215], a[252:255], v[144:147]\n" \
"s_add_u32 m0, s31, 0xc000\n" \
"s_mov_b32 s84, 0x180000\n" \
"v_mfma_f32_16x16x32_bf16 v[140:143], a[148:151], a[184:187], v[140:143]\n" \
"buffer_load_dwordx4 v26, s[72:75], s84 offen offset:0 lds\n" \
"v_mfma_f32_16x16x32_bf16 v[140:143], a[212:215], a[248:251], v[140:143]\n" \
"ds_read_b64_tr_b16 a[230:231], v33 offset:9216\n" \
"v_lshlrev_b32_e64 v225, 4, v223\n" \
"v_mfma_f32_16x16x32_bf16 v[136:139], a[148:151], a[180:183], v[136:139]\n" \
"s_addk_i32 s84, 0x7c00\n" \
"v_mfma_f32_16x16x32_bf16 v[136:139], a[212:215], a[244:247], v[136:139]\n" \
"buffer_load_dwordx4 v26, s[72:75], s84 offen offset:1024 lds\n" \
"v_mfma_f32_16x16x32_bf16 v[132:135], a[148:151], a[176:179], v[132:135]\n" \
"ds_read_b64_tr_b16 a[168:169], v34 offset:0\n" \
"v_add_u32_e32 v226, s5, v225\n" \
"v_mfma_f32_16x16x32_bf16 v[132:135], a[212:215], a[240:243], v[132:135]\n" \
"ds_read_b128 a[148:151], v28 offset:2048\n" \
"v_and_b32_e32 v226, 0xf0, v226\n" \
"v_mfma_f32_16x16x32_bf16 v[148:151], a[152:155], a[176:179], v[148:151]\n" \
"ds_read_b128 a[212:215], v29 offset:2048\n" \
"v_cmp_gt_u32_e64 vcc, 16, v223\n" \
"v_mfma_f32_16x16x32_bf16 v[148:151], a[216:219], a[240:243], v[148:151]\n" \
"ds_read_b64_tr_b16 a[170:171], v34 offset:1024\n" \
"v_cndmask_b32_e64 v223, v225, v226, vcc\n" \
"v_mfma_f32_16x16x32_bf16 v[152:155], a[152:155], a[180:183], v[152:155]\n" \
"ds_read_b64_tr_b16 a[232:233], v34 offset:8192\n" \
"v_or_b32_e32 v225, 0x140, v212\n" \
"v_mfma_f32_16x16x32_bf16 v[152:155], a[216:219], a[244:247], v[152:155]\n" \
"ds_read_b64_tr_b16 a[234:235], v34 offset:9216\n" \
"v_mul_u32_u24_e32 v226, 0xe39, v225\n" \
"v_mfma_f32_16x16x32_bf16 v[156:159], a[152:155], a[184:187], v[156:159]\n" \
"ds_read_b64_tr_b16 a[172:173], v35 offset:0\n" \
"v_lshrrev_b32_e64 v226, 16, v226\n" \
"v_mfma_f32_16x16x32_bf16 v[156:159], a[216:219], a[248:251], v[156:159]\n" \
"ds_read_b64_tr_b16 a[174:175], v35 offset:1024\n" \
"v_mad_i32_i24 v225, v226, s6, v225\n" \
"v_mfma_f32_16x16x32_bf16 v[160:163], a[152:155], a[188:191], v[160:163]\n" \
"ds_read_b64_tr_b16 a[236:237], v35 offset:8192\n" \
"v_lshlrev_b32_e64 v227, 4, v225\n" \
"v_mfma_f32_16x16x32_bf16 v[160:163], a[216:219], a[252:255], v[160:163]\n" \
"ds_read_b128 a[152:155], v28 offset:4096\n" \
"v_add_u32_e64 v228, s5, v227\n" \
"v_mfma_f32_16x16x32_bf16 v[176:179], a[156:159], a[188:191], v[176:179]\n" \
"ds_read_b128 a[216:219], v29 offset:4096\n" \
"v_and_b32_e32 v228, 0xf0, v228\n" \
"v_mfma_f32_16x16x32_bf16 v[176:179], a[220:223], a[252:255], v[176:179]\n" \
"ds_read_b64_tr_b16 a[238:239], v35 offset:9216\n" \
"v_cmp_gt_u32_e64 vcc, 16, v225\n" \
"v_mfma_f32_16x16x32_bf16 v[172:175], a[156:159], a[184:187], v[172:175]\n" \
"s_nop 0\n" \
"v_cndmask_b32_e32 v225, v227, v228, vcc\n" \
"v_or_b32_e32 v227, 0x180, v212\n" \
"v_mfma_f32_16x16x32_bf16 v[172:175], a[220:223], a[248:251], v[172:175]\n" \
"v_mul_u32_u24_e32 v228, 0xe39, v227\n" \
"v_lshrrev_b32_e64 v228, 16, v228\n" \
"v_mfma_f32_16x16x32_bf16 v[168:171], a[156:159], a[180:183], v[168:171]\n" \
"v_mad_i32_i24 v227, v228, s6, v227\n" \
"v_lshlrev_b32_e64 v229, 4, v227\n" \
"v_mfma_f32_16x16x32_bf16 v[168:171], a[220:223], a[244:247], v[168:171]\n" \
"v_add_u32_e64 v230, s5, v229\n" \
"v_and_b32_e32 v230, 0xf0, v230\n" \
"v_mfma_f32_16x16x32_bf16 v[164:167], a[156:159], a[176:179], v[164:167]\n" \
"v_cmp_gt_u32_e32 vcc, 16, v227\n" \
"s_nop 1\n" \
"v_cndmask_b32_e64 v227, v229, v230, vcc\n" \
"v_mfma_f32_16x16x32_bf16 v[164:167], a[220:223], a[240:243], v[164:167]\n" \
"ds_read_b128 a[156:159], v28 offset:6144\n" \
"ds_read_b128 a[220:223], v29 offset:6144\n" \
"s_waitcnt lgkmcnt(15)\n" \
"v_mfma_f32_16x16x32_bf16 a[0:3], a[128:131], a[160:163], a[0:3]\n" \
"s_addk_i32 s84, 0x7c00\n" \
"v_mfma_f32_16x16x32_bf16 a[0:3], a[192:195], a[224:227], a[0:3]\n" \
"buffer_load_dwordx4 v27, s[72:75], s84 offen offset:2048 lds\n" \
"v_mfma_f32_16x16x32_bf16 a[4:7], a[128:131], a[164:167], a[4:7]\n" \
"ds_read_b64_tr_b16 a[176:177], v36 offset:0\n" \
"v_or_b32_e32 v229, 0x1c0, v212\n" \
"s_waitcnt lgkmcnt(15)\n" \
"v_mfma_f32_16x16x32_bf16 a[4:7], a[192:195], a[228:231], a[4:7]\n" \
"s_addk_i32 s84, 0x7c00\n" \
"s_waitcnt lgkmcnt(11)\n" \
"v_mfma_f32_16x16x32_bf16 a[8:11], a[128:131], a[168:171], a[8:11]\n" \
"buffer_load_dwordx4 v27, s[72:75], s84 offen offset:3072 lds\n" \
"s_waitcnt lgkmcnt(9)\n" \
"v_mfma_f32_16x16x32_bf16 a[8:11], a[192:195], a[232:235], a[8:11]\n" \
"ds_read_b64_tr_b16 a[178:179], v36 offset:1024\n" \
"v_mul_u32_u24_e32 v230, 0xe39, v229\n" \
"s_waitcnt lgkmcnt(8)\n" \
"v_mfma_f32_16x16x32_bf16 a[12:15], a[128:131], a[172:175], a[12:15]\n" \
"ds_read_b64_tr_b16 a[240:241], v36 offset:8192\n" \
"v_lshrrev_b32_e32 v230, 16, v230\n" \
"s_waitcnt lgkmcnt(5)\n" \
"v_mfma_f32_16x16x32_bf16 a[12:15], a[192:195], a[236:239], a[12:15]\n" \
"v_mad_i32_i24 v229, v230, s6, v229\n" \
"v_lshlrev_b32_e32 v231, 4, v229\n" \
"v_mfma_f32_16x16x32_bf16 a[28:31], a[132:135], a[172:175], a[28:31]\n" \
"v_add_u32_e64 v232, s5, v231\n" \
"v_and_b32_e32 v232, 0xf0, v232\n" \
"v_mfma_f32_16x16x32_bf16 a[28:31], a[196:199], a[236:239], a[28:31]\n" \
"ds_read_b64_tr_b16 a[242:243], v36 offset:9216\n" \
"v_cmp_gt_u32_e64 vcc, 16, v229\n" \
"v_mfma_f32_16x16x32_bf16 a[24:27], a[132:135], a[168:171], a[24:27]\n" \
"buffer_load_dwordx4 v[200:203], v192, s[76:79], s34 offen offset:0\n" \
"v_mfma_f32_16x16x32_bf16 a[24:27], a[196:199], a[232:235], a[24:27]\n" \
"v_cndmask_b32_e64 v229, v231, v232, vcc\n" \
"v_or_b32_e32 v231, 0x200, v212\n" \
"v_mfma_f32_16x16x32_bf16 a[20:23], a[132:135], a[164:167], a[20:23]\n" \
"v_mul_u32_u24_e32 v232, 0xe39, v231\n" \
"v_lshrrev_b32_e64 v232, 16, v232\n" \
"v_mfma_f32_16x16x32_bf16 a[20:23], a[196:199], a[228:231], a[20:23]\n" \
"ds_read_b64_tr_b16 a[180:181], v37 offset:0\n" \
"v_mad_i32_i24 v231, v232, s6, v231\n" \
"v_mfma_f32_16x16x32_bf16 a[16:19], a[132:135], a[160:163], a[16:19]\n" \
"v_lshlrev_b32_e32 v233, 4, v231\n" \
"v_add_u32_e32 v234, s5, v233\n" \
"v_mfma_f32_16x16x32_bf16 a[16:19], a[196:199], a[224:227], a[16:19]\n" \
"s_movk_i32 s5, 0x480\n" \
"v_mad_u32_u24 v0, v213, s5, v214\n" \
"v_mfma_f32_16x16x32_bf16 a[32:35], a[136:139], a[160:163], a[32:35]\n" \
"buffer_load_dwordx4 v[204:207], v193, s[76:79], s34 offen offset:0\n" \
"v_mfma_f32_16x16x32_bf16 a[32:35], a[200:203], a[224:227], a[32:35]\n" \
"s_mov_b32 m0, s32\n" \
"ds_read_b64_tr_b16 a[182:183], v37 offset:1024\n" \
"v_mfma_f32_16x16x32_bf16 a[36:39], a[136:139], a[164:167], a[36:39]\n" \
"s_mov_b32 exec_lo, 0x0\n" \
"s_mov_b32 exec_hi, 0xff00\n" \
"global_load_lds_dword v[184:185], off\n" \
"s_mov_b64 exec, -1\n" \
"v_mfma_f32_16x16x32_bf16 a[36:39], a[200:203], a[228:231], a[36:39]\n" \
"ds_read_b64_tr_b16 a[244:245], v37 offset:8192\n" \
"v_mul_u32_u24_e32 v213, 0x480, v216\n" \
"v_mfma_f32_16x16x32_bf16 a[40:43], a[136:139], a[168:171], a[40:43]\n" \
"s_movk_i32 s6, 0xfc00\n" \
"s_nop 0\n" \
"v_add3_u32 v1, v213, v215, s6\n" \
"v_mfma_f32_16x16x32_bf16 a[40:43], a[200:203], a[232:235], a[40:43]\n" \
"buffer_load_dword v208, v194, s[76:79], s34 offen offset:32\n" \
"v_mfma_f32_16x16x32_bf16 a[44:47], a[136:139], a[172:175], a[44:47]\n" \
"ds_read_b64_tr_b16 a[246:247], v37 offset:9216\n" \
"v_mul_u32_u24_e32 v213, 0x480, v220\n" \
"v_mfma_f32_16x16x32_bf16 a[44:47], a[200:203], a[236:239], a[44:47]\n" \
"v_add3_u32 v2, v213, v217, s7\n" \
"v_mul_u32_u24_e32 v213, 0x480, v222\n" \
"v_mfma_f32_16x16x32_bf16 a[60:63], a[140:143], a[172:175], a[60:63]\n" \
"v_add3_u32 v3, v213, v221, s40\n" \
"v_mul_u32_u24_e32 v213, 0x480, v226\n" \
"v_mfma_f32_16x16x32_bf16 a[60:63], a[204:207], a[236:239], a[60:63]\n" \
"v_add3_u32 v5, v213, v225, s6\n" \
"v_mul_u32_u24_e32 v213, 0x480, v228\n" \
"v_mfma_f32_16x16x32_bf16 a[56:59], a[140:143], a[168:171], a[56:59]\n" \
"buffer_load_dword v209, v194, s[76:79], s34 offen offset:1184\n" \
"v_mfma_f32_16x16x32_bf16 a[56:59], a[204:207], a[232:235], a[56:59]\n" \
"ds_read_b64_tr_b16 a[184:185], v38 offset:0\n" \
"v_add3_u32 v6, v213, v227, s7\n" \
"v_mfma_f32_16x16x32_bf16 a[52:55], a[140:143], a[164:167], a[52:55]\n" \
"v_mul_u32_u24_e32 v213, 0x480, v230\n" \
"v_add3_u32 v7, v213, v229, s40\n" \
"v_mfma_f32_16x16x32_bf16 a[52:55], a[204:207], a[228:231], a[52:55]\n" \
"ds_read_b64_tr_b16 a[186:187], v38 offset:1024\n" \
"s_lshl_b32 s7, s3, 3\n" \
"v_mfma_f32_16x16x32_bf16 a[48:51], a[140:143], a[160:163], a[48:51]\n" \
"s_lshl_b32 s40, s3, 1\n" \
"s_and_b32 s7, s7, 16\n" \
"v_mfma_f32_16x16x32_bf16 a[48:51], a[204:207], a[224:227], a[48:51]\n" \
"s_and_b32 s40, s40, 2\n" \
"s_and_b32 s6, s2, 7\n" \
"v_mfma_f32_16x16x32_bf16 a[64:67], a[144:147], a[160:163], a[64:67]\n" \
"buffer_load_dword v211, v194, s[76:79], s34 offen offset:2336\n" \
"v_mfma_f32_16x16x32_bf16 a[64:67], a[208:211], a[224:227], a[64:67]\n" \
"ds_read_b64_tr_b16 a[248:249], v38 offset:8192\n" \
"v_bfe_u32 v213, v20, 7, 1\n" \
"v_mfma_f32_16x16x32_bf16 a[68:71], a[144:147], a[164:167], a[68:71]\n" \
"s_or_b32 s7, s40, s7\n" \
"v_and_b32_e32 v234, 0xf0, v234\n" \
"v_mfma_f32_16x16x32_bf16 a[68:71], a[208:211], a[228:231], a[68:71]\n" \
"ds_read_b64_tr_b16 a[250:251], v38 offset:9216\n" \
"v_cmp_gt_u32_e64 vcc, 16, v231\n" \
"v_mfma_f32_16x16x32_bf16 a[72:75], a[144:147], a[168:171], a[72:75]\n" \
"v_or3_b32 v213, s7, v213, v219\n" \
"v_and_b32_e64 v214, 32, v20\n" \
"v_mfma_f32_16x16x32_bf16 a[72:75], a[208:211], a[232:235], a[72:75]\n" \
"s_mov_b32 s76, s20\n" \
"s_and_b32 s77, s21, 0xffff\n" \
"v_mfma_f32_16x16x32_bf16 a[76:79], a[144:147], a[172:175], a[76:79]\n" \
"s_mov_b32 s78, 0x480\n" \
"s_lshl_b32 s6, s6, 5\n" \
"v_mfma_f32_16x16x32_bf16 a[76:79], a[208:211], a[236:239], a[76:79]\n" \
"buffer_load_ushort v210, v195, s[76:79], 0 offen\n" \
"v_mfma_f32_16x16x32_bf16 a[92:95], a[148:151], a[172:175], a[92:95]\n" \
"ds_read_b64_tr_b16 a[188:189], v39 offset:0\n" \
"v_cndmask_b32_e64 v231, v233, v234, vcc\n" \
"v_mfma_f32_16x16x32_bf16 a[92:95], a[212:215], a[236:239], a[92:95]\n" \
"v_mul_u32_u24_e64 v213, 18, v213\n" \
"v_mov_b32_e32 v215, 0x100\n" \
"v_mfma_f32_16x16x32_bf16 a[88:91], a[148:151], a[168:171], a[88:91]\n" \
"s_mov_b32 s76, s8\n" \
"s_and_b32 s77, s9, 0xffff\n" \
"v_mfma_f32_16x16x32_bf16 a[88:91], a[212:215], a[232:235], a[88:91]\n" \
"s_mov_b32 s78, 0x240000\n" \
"ds_read_b64_tr_b16 a[190:191], v39 offset:1024\n" \
".fill (L_pb + 48864 - .) / 4, 4, 0xBF800000\n" \
"L_pp12:\n" \
"s_cbranch_execz L_pret\n" \
"v_mfma_f32_16x16x32_bf16 a[84:87], a[148:151], a[164:167], a[84:87]\n" \
"v_mov_b32_e32 v216, s6\n" \
"v_cmp_eq_u32_e64 vcc, 0, v214\n" \
"v_mfma_f32_16x16x32_bf16 a[84:87], a[212:215], a[228:231], a[84:87]\n" \
"ds_read_b64_tr_b16 a[252:253], v39 offset:8192\n" \
"v_and_or_b32 v213, v218, 1, v213\n" \
"v_mfma_f32_16x16x32_bf16 a[80:83], a[148:151], a[160:163], a[80:83]\n" \
"v_lshlrev_b32_e32 v212, 3, v212\n" \
"v_cndmask_b32_e32 v214, v215, v216, vcc\n" \
"v_mfma_f32_16x16x32_bf16 a[80:83], a[212:215], a[224:227], a[80:83]\n" \
"v_mad_u32_u24 v213, v213, s5, v214\n" \
"v_and_or_b32 v213, v20, 16, v213\n" \
"v_mfma_f32_16x16x32_bf16 a[96:99], a[152:155], a[160:163], a[96:99]\n" \
"ds_read_b64_tr_b16 a[254:255], v39 offset:9216\n" \
"v_add_u32_e32 v9, 0x4800, v213\n" \
"v_mfma_f32_16x16x32_bf16 a[96:99], a[216:219], a[224:227], a[96:99]\n" \
"v_mul_u32_u24_e32 v213, 0x120, v218\n" \
"v_and_or_b32 v10, v212, 24, v213\n" \
"v_mfma_f32_16x16x32_bf16 a[100:103], a[152:155], a[164:167], a[100:103]\n" \
"v_lshrrev_b32_e64 v213, 2, v20\n" \
"v_and_or_b32 v213, v213, 60, s4\n" \
"v_mfma_f32_16x16x32_bf16 a[100:103], a[216:219], a[228:231], a[100:103]\n" \
"v_lshl_or_b32 v212, s3, 9, v212\n" \
"v_mad_u32_u24 v4, v224, s5, v223\n" \
"v_mfma_f32_16x16x32_bf16 a[104:107], a[152:155], a[168:171], a[104:107]\n" \
"v_mad_u32_u24 v8, v232, s5, v231\n" \
"v_or_b32_e32 v18, 0x27000, v213\n" \
"v_mfma_f32_16x16x32_bf16 a[104:107], a[216:219], a[232:235], a[104:107]\n" \
"v_or_b32_e32 v19, 0x27800, v212\n" \
"v_mov_b32_e32 v215, 0x1000c0c\n" \
"v_mfma_f32_16x16x32_bf16 a[108:111], a[152:155], a[172:175], a[108:111]\n" \
"v_mov_b32_e32 v216, 0xc0c0100\n" \
"v_bfe_u32 v214, v20, 4, 1\n" \
"v_mfma_f32_16x16x32_bf16 a[108:111], a[216:219], a[236:239], a[108:111]\n" \
"v_lshrrev_b32_e32 v212, 8, v20\n" \
"v_cmp_eq_u32_e32 vcc, 0, v214\n" \
"s_waitcnt lgkmcnt(15)\n" \
"v_mfma_f32_16x16x32_bf16 a[124:127], a[156:159], a[172:175], a[124:127]\n" \
"v_bfe_u32 v213, v20, 4, 4\n" \
"v_mov_b32_e32 v217, 0xc0c0302\n" \
"v_mfma_f32_16x16x32_bf16 a[124:127], a[220:223], a[236:239], a[124:127]\n" \
"v_cndmask_b32_e32 v215, v215, v216, vcc\n" \
"v_mov_b32_e32 v216, 0x3020c0c\n" \
"v_mfma_f32_16x16x32_bf16 a[120:123], a[156:159], a[168:171], a[120:123]\n" \
"v_lshl_or_b32 v214, v212, 2, v214\n" \
"v_cndmask_b32_e64 v216, v216, v217, vcc\n" \
"v_mfma_f32_16x16x32_bf16 a[120:123], a[220:223], a[232:235], a[120:123]\n" \
"v_mov_b32_e32 v217, 0xc0c0c0c\n" \
"v_cmp_gt_u32_e64 vcc, v214, v213\n" \
"v_mfma_f32_16x16x32_bf16 a[116:119], a[156:159], a[164:167], a[116:119]\n" \
"v_or_b32_e32 v214, 2, v214\n" \
"s_lshr_b32 s4, s2, 5\n" \
"v_mfma_f32_16x16x32_bf16 a[116:119], a[220:223], a[228:231], a[116:119]\n" \
"v_cndmask_b32_e32 v40, v217, v215, vcc\n" \
"v_cndmask_b32_e32 v42, v215, v217, vcc\n" \
"v_mfma_f32_16x16x32_bf16 a[112:115], a[156:159], a[160:163], a[112:115]\n" \
"v_cmp_gt_u32_e32 vcc, v214, v213\n" \
"v_and_b32_e32 v214, 16, v20\n" \
"s_waitcnt vmcnt(7) lgkmcnt(0)\n" \
"v_mfma_f32_16x16x32_bf16 a[112:115], a[220:223], a[224:227], a[112:115]\n" \
"s_barrier\n" \
"v_mfma_f32_16x16x32_bf16 v[52:55], a[128:131], a[176:179], v[52:55]\n" \
"s_add_u32 m0, s30, 0x0\n" \
"s_movk_i32 s84, 0x200\n" \
"v_mfma_f32_16x16x32_bf16 v[52:55], a[192:195], a[240:243], v[52:55]\n" \
"buffer_load_dwordx4 v22, s[64:67], s84 offen offset:0 lds\n" \
"v_mfma_f32_16x16x32_bf16 v[56:59], a[128:131], a[180:183], v[56:59]\n" \
"ds_read_b64_tr_b16 a[160:161], v32 offset:32768\n" \
"s_movk_i32 s42, 0x3f80\n" \
"v_mfma_f32_16x16x32_bf16 v[56:59], a[192:195], a[244:247], v[56:59]\n" \
"s_movk_i32 s84, 0x1e00\n" \
"v_mfma_f32_16x16x32_bf16 v[60:63], a[128:131], a[184:187], v[60:63]\n" \
"buffer_load_dwordx4 v23, s[64:67], s84 offen offset:1024 lds\n" \
"v_mfma_f32_16x16x32_bf16 v[60:63], a[192:195], a[248:251], v[60:63]\n" \
"ds_read_b64_tr_b16 a[162:163], v32 offset:33792\n" \
"s_xor_b32 s5, s4, 15\n" \
"v_mfma_f32_16x16x32_bf16 v[64:67], a[128:131], a[188:191], v[64:67]\n" \
"ds_read_b64_tr_b16 a[224:225], v32 offset:40960\n" \
"v_mfma_f32_16x16x32_bf16 v[64:67], a[192:195], a[252:255], v[64:67]\n" \
"ds_read_b128 a[128:131], v28 offset:49152\n" \
"v_mfma_f32_16x16x32_bf16 v[80:83], a[132:135], a[188:191], v[80:83]\n" \
"ds_read_b128 a[192:195], v29 offset:49152\n" \
"v_mfma_f32_16x16x32_bf16 v[80:83], a[196:199], a[252:255], v[80:83]\n" \
"s_movk_i32 s84, 0x3a00\n" \
"v_mfma_f32_16x16x32_bf16 v[76:79], a[132:135], a[184:187], v[76:79]\n" \
"buffer_load_dwordx4 v22, s[64:67], s84 offen offset:2048 lds\n" \
"v_mfma_f32_16x16x32_bf16 v[76:79], a[196:199], a[248:251], v[76:79]\n" \
"s_movk_i32 s84, 0x5600\n" \
"v_mfma_f32_16x16x32_bf16 v[72:75], a[132:135], a[180:183], v[72:75]\n" \
"buffer_load_dwordx4 v23, s[64:67], s84 offen offset:3072 lds\n" \
"v_mfma_f32_16x16x32_bf16 v[72:75], a[196:199], a[244:247], v[72:75]\n" \
"s_add_u32 m0, s31, 0x0\n" \
"s_mov_b32 s84, 0x200000\n" \
"v_mfma_f32_16x16x32_bf16 v[68:71], a[132:135], a[176:179], v[68:71]\n" \
"buffer_load_dwordx4 v26, s[68:71], s84 offen offset:0 lds\n" \
"v_mfma_f32_16x16x32_bf16 v[68:71], a[196:199], a[240:243], v[68:71]\n" \
"ds_read_b128 a[132:135], v28 offset:51200\n" \
"v_mfma_f32_16x16x32_bf16 v[84:87], a[136:139], a[176:179], v[84:87]\n" \
"ds_read_b128 a[196:199], v29 offset:51200\n" \
"v_mfma_f32_16x16x32_bf16 v[84:87], a[200:203], a[240:243], v[84:87]\n" \
"s_addk_i32 s84, 0x7c00\n" \
"v_mfma_f32_16x16x32_bf16 v[88:91], a[136:139], a[180:183], v[88:91]\n" \
"buffer_load_dwordx4 v26, s[68:71], s84 offen offset:1024 lds\n" \
"v_mfma_f32_16x16x32_bf16 v[88:91], a[200:203], a[244:247], v[88:91]\n" \
"ds_read_b64_tr_b16 a[226:227], v32 offset:41984\n" \
"v_mfma_f32_16x16x32_bf16 v[92:95], a[136:139], a[184:187], v[92:95]\n" \
"s_addk_i32 s84, 0x7c00\n" \
"v_mfma_f32_16x16x32_bf16 v[92:95], a[200:203], a[248:251], v[92:95]\n" \
"buffer_load_dwordx4 v27, s[68:71], s84 offen offset:2048 lds\n" \
"v_mfma_f32_16x16x32_bf16 v[96:99], a[136:139], a[188:191], v[96:99]\n" \
"ds_read_b64_tr_b16 a[164:165], v33 offset:32768\n" \
"v_mfma_f32_16x16x32_bf16 v[96:99], a[200:203], a[252:255], v[96:99]\n" \
"ds_read_b128 a[136:139], v28 offset:53248\n" \
"v_mfma_f32_16x16x32_bf16 v[112:115], a[140:143], a[188:191], v[112:115]\n" \
"ds_read_b128 a[200:203], v29 offset:53248\n" \
"v_mfma_f32_16x16x32_bf16 v[112:115], a[204:207], a[252:255], v[112:115]\n" \
"s_addk_i32 s84, 0x7c00\n" \
"s_nop 0\n" \
"v_mfma_f32_16x16x32_bf16 v[108:111], a[140:143], a[184:187], v[108:111]\n" \
"buffer_load_dwordx4 v27, s[68:71], s84 offen offset:3072 lds\n" \
"v_mfma_f32_16x16x32_bf16 v[108:111], a[204:207], a[248:251], v[108:111]\n" \
"s_add_u32 m0, s30, 0x1000\n" \
"s_mov_b32 s84, 0x8200\n" \
"v_mfma_f32_16x16x32_bf16 v[104:107], a[140:143], a[180:183], v[104:107]\n" \
"buffer_load_dwordx4 v22, s[64:67], s84 offen offset:0 lds\n" \
"v_mfma_f32_16x16x32_bf16 v[104:107], a[204:207], a[244:247], v[104:107]\n" \
"ds_read_b64_tr_b16 a[166:167], v33 offset:33792\n" \
"v_mfma_f32_16x16x32_bf16 v[100:103], a[140:143], a[176:179], v[100:103]\n" \
"ds_read_b64_tr_b16 a[228:229], v33 offset:40960\n" \
"v_mfma_f32_16x16x32_bf16 v[100:103], a[204:207], a[240:243], v[100:103]\n" \
"ds_read_b128 a[140:143], v28 offset:55296\n" \
"v_mfma_f32_16x16x32_bf16 v[116:119], a[144:147], a[176:179], v[116:119]\n" \
"ds_read_b128 a[204:207], v29 offset:55296\n" \
"v_mfma_f32_16x16x32_bf16 v[116:119], a[208:211], a[240:243], v[116:119]\n" \
"s_addk_i32 s84, 0x1c00\n" \
"v_mfma_f32_16x16x32_bf16 v[120:123], a[144:147], a[180:183], v[120:123]\n" \
"buffer_load_dwordx4 v23, s[64:67], s84 offen offset:1024 lds\n" \
"v_mfma_f32_16x16x32_bf16 v[120:123], a[208:211], a[244:247], v[120:123]\n" \
"s_addk_i32 s84, 0x1c00\n" \
"v_mfma_f32_16x16x32_bf16 v[124:127], a[144:147], a[184:187], v[124:127]\n" \
"buffer_load_dwordx4 v22, s[64:67], s84 offen offset:2048 lds\n" \
"v_mfma_f32_16x16x32_bf16 v[124:127], a[208:211], a[248:251], v[124:127]\n" \
"s_addk_i32 s84, 0x1c00\n" \
"s_nop 0\n" \
"v_mfma_f32_16x16x32_bf16 v[128:131], a[144:147], a[188:191], v[128:131]\n" \
"buffer_load_dwordx4 v23, s[64:67], s84 offen offset:3072 lds\n" \
"v_mfma_f32_16x16x32_bf16 v[128:131], a[208:211], a[252:255], v[128:131]\n" \
"ds_read_b128 a[144:147], v28 offset:32768\n" \
"v_mfma_f32_16x16x32_bf16 v[144:147], a[148:151], a[188:191], v[144:147]\n" \
"ds_read_b128 a[208:211], v29 offset:32768\n" \
"v_mfma_f32_16x16x32_bf16 v[144:147], a[212:215], a[252:255], v[144:147]\n" \
"s_add_u32 m0, s31, 0x4000\n" \
"s_mov_b32 s84, 0x200000\n" \
"v_mfma_f32_16x16x32_bf16 v[140:143], a[148:151], a[184:187], v[140:143]\n" \
"buffer_load_dwordx4 v26, s[72:75], s84 offen offset:0 lds\n" \
"v_mfma_f32_16x16x32_bf16 v[140:143], a[212:215], a[248:251], v[140:143]\n" \
"ds_read_b64_tr_b16 a[230:231], v33 offset:41984\n" \
"v_mfma_f32_16x16x32_bf16 v[136:139], a[148:151], a[180:183], v[136:139]\n" \
"s_addk_i32 s84, 0x7c00\n" \
"s_nop 0\n" \
"v_mfma_f32_16x16x32_bf16 v[136:139], a[212:215], a[244:247], v[136:139]\n" \
"buffer_load_dwordx4 v26, s[72:75], s84 offen offset:1024 lds\n" \
"v_mfma_f32_16x16x32_bf16 v[132:135], a[148:151], a[176:179], v[132:135]\n" \
"ds_read_b64_tr_b16 a[168:169], v34 offset:32768\n" \
"v_mfma_f32_16x16x32_bf16 v[132:135], a[212:215], a[240:243], v[132:135]\n" \
"ds_read_b128 a[148:151], v28 offset:34816\n" \
"v_mfma_f32_16x16x32_bf16 v[148:151], a[152:155], a[176:179], v[148:151]\n" \
"ds_read_b128 a[212:215], v29 offset:34816\n" \
"v_mfma_f32_16x16x32_bf16 v[148:151], a[216:219], a[240:243], v[148:151]\n" \
"ds_read_b64_tr_b16 a[170:171], v34 offset:33792\n" \
"v_mfma_f32_16x16x32_bf16 v[152:155], a[152:155], a[180:183], v[152:155]\n" \
"ds_read_b64_tr_b16 a[232:233], v34 offset:40960\n" \
"v_mfma_f32_16x16x32_bf16 v[152:155], a[216:219], a[244:247], v[152:155]\n" \
"ds_read_b64_tr_b16 a[234:235], v34 offset:41984\n" \
"v_mfma_f32_16x16x32_bf16 v[156:159], a[152:155], a[184:187], v[156:159]\n" \
"ds_read_b64_tr_b16 a[172:173], v35 offset:32768\n" \
"v_mfma_f32_16x16x32_bf16 v[156:159], a[216:219], a[248:251], v[156:159]\n" \
"ds_read_b64_tr_b16 a[174:175], v35 offset:33792\n" \
"v_mfma_f32_16x16x32_bf16 v[160:163], a[152:155], a[188:191], v[160:163]\n" \
"ds_read_b64_tr_b16 a[236:237], v35 offset:40960\n" \
"v_mfma_f32_16x16x32_bf16 v[160:163], a[216:219], a[252:255], v[160:163]\n" \
"ds_read_b128 a[152:155], v28 offset:36864\n" \
"v_mfma_f32_16x16x32_bf16 v[176:179], a[156:159], a[188:191], v[176:179]\n" \
"ds_read_b128 a[216:219], v29 offset:36864\n" \
"v_mfma_f32_16x16x32_bf16 v[176:179], a[220:223], a[252:255], v[176:179]\n" \
"ds_read_b64_tr_b16 a[238:239], v35 offset:41984\n" \
"v_mfma_f32_16x16x32_bf16 v[172:175], a[156:159], a[184:187], v[172:175]\n" \
"s_cmpk_lt_u32 s3, 2\n" \
"s_cselect_b32 s6, s5, s4\n" \
"v_mfma_f32_16x16x32_bf16 v[172:175], a[220:223], a[248:251], v[172:175]\n" \
"s_lshl_b32 s7, s6, 7\n" \
"v_and_b32_e32 v213, 0x320, v20\n" \
"v_mfma_f32_16x16x32_bf16 v[168:171], a[156:159], a[180:183], v[168:171]\n" \
"s_movk_i32 s41, 0x100\n" \
"v_lshlrev_b32_e64 v214, v214, s42\n" \
"v_mfma_f32_16x16x32_bf16 v[168:171], a[220:223], a[244:247], v[168:171]\n" \
"v_mov_b32_e32 v215, 0\n" \
"s_add_i32 s40, s7, -16\n" \
"v_mfma_f32_16x16x32_bf16 v[164:167], a[156:159], a[176:179], v[164:167]\n" \
"v_cndmask_b32_e32 v41, v217, v216, vcc\n" \
"v_cndmask_b32_e32 v43, v216, v217, vcc\n" \
"v_mfma_f32_16x16x32_bf16 v[164:167], a[220:223], a[240:243], v[164:167]\n" \
"ds_read_b128 a[156:159], v28 offset:38912\n" \
"ds_read_b128 a[220:223], v29 offset:38912\n" \
"s_waitcnt lgkmcnt(15)\n" \
"v_mfma_f32_16x16x32_bf16 a[0:3], a[128:131], a[160:163], a[0:3]\n" \
"s_addk_i32 s84, 0x7c00\n" \
"v_mfma_f32_16x16x32_bf16 a[0:3], a[192:195], a[224:227], a[0:3]\n" \
"buffer_load_dwordx4 v27, s[72:75], s84 offen offset:2048 lds\n" \
"v_mfma_f32_16x16x32_bf16 a[4:7], a[128:131], a[164:167], a[4:7]\n" \
"ds_read_b64_tr_b16 a[176:177], v36 offset:32768\n" \
"v_cmp_eq_u32_e32 vcc, s41, v213\n" \
"s_waitcnt lgkmcnt(15)\n" \
"v_mfma_f32_16x16x32_bf16 a[4:7], a[192:195], a[228:231], a[4:7]\n" \
"s_addk_i32 s84, 0x7c00\n" \
"s_waitcnt lgkmcnt(11)\n" \
"v_mfma_f32_16x16x32_bf16 a[8:11], a[128:131], a[168:171], a[8:11]\n" \
"buffer_load_dwordx4 v27, s[72:75], s84 offen offset:3072 lds\n" \
"s_waitcnt lgkmcnt(9)\n" \
"v_mfma_f32_16x16x32_bf16 a[8:11], a[192:195], a[232:235], a[8:11]\n" \
"ds_read_b64_tr_b16 a[178:179], v36 offset:33792\n" \
"s_mov_b32 s41, 0\n" \
"s_waitcnt lgkmcnt(8)\n" \
"v_mfma_f32_16x16x32_bf16 a[12:15], a[128:131], a[172:175], a[12:15]\n" \
"ds_read_b64_tr_b16 a[240:241], v36 offset:40960\n" \
"s_and_b32 s43, s3, 1\n" \
"s_waitcnt lgkmcnt(5)\n" \
"v_mfma_f32_16x16x32_bf16 a[12:15], a[192:195], a[236:239], a[12:15]\n" \
"v_cndmask_b32_e32 v46, v215, v214, vcc\n" \
"v_and_b32_e32 v213, 0x330, v20\n" \
"v_mfma_f32_16x16x32_bf16 a[28:31], a[132:135], a[172:175], a[28:31]\n" \
"s_cmp_lg_u32 s7, 0\n" \
"s_cselect_b32 s40, s40, s41\n" \
"v_mfma_f32_16x16x32_bf16 a[28:31], a[196:199], a[236:239], a[28:31]\n" \
"ds_read_b64_tr_b16 a[242:243], v36 offset:41984\n" \
"s_mov_b32 s42, 0xff800000\n" \
"v_mfma_f32_16x16x32_bf16 a[24:27], a[132:135], a[168:171], a[24:27]\n" \
"s_cmp_lg_u32 s7, 0\n" \
"s_cselect_b32 s47, s41, s42\n" \
"v_mfma_f32_16x16x32_bf16 a[24:27], a[196:199], a[232:235], a[24:27]\n" \
"s_lshl_b32 s41, s43, 6\n" \
"v_lshrrev_b32_e32 v215, 6, v20\n" \
"v_mfma_f32_16x16x32_bf16 a[20:23], a[132:135], a[164:167], a[20:23]\n" \
"v_mov_b32_e32 v214, 0x3f80\n" \
"v_cmp_eq_u32_e64 vcc, 32, v213\n" \
"v_mfma_f32_16x16x32_bf16 a[20:23], a[196:199], a[228:231], a[20:23]\n" \
"ds_read_b64_tr_b16 a[180:181], v37 offset:32768\n" \
"s_or_b32 s7, s41, s7\n" \
"v_mfma_f32_16x16x32_bf16 a[16:19], a[132:135], a[160:163], a[16:19]\n" \
"v_lshrrev_b32_e32 v213, 7, v20\n" \
"v_bfe_u32 v216, v20, 6, 2\n" \
"v_mfma_f32_16x16x32_bf16 a[16:19], a[196:199], a[224:227], a[16:19]\n" \
"v_lshrrev_b32_e64 v219, 1, v20\n" \
"v_bfe_u32 v215, v215, 1, 1\n" \
"v_mfma_f32_16x16x32_bf16 a[32:35], a[136:139], a[160:163], a[32:35]\n" \
"s_waitcnt vmcnt(16)\n" \
"v_or_b32_e32 v204, v210, v204\n" \
"v_or_b32_e64 v206, v210, v206\n" \
"v_mfma_f32_16x16x32_bf16 a[32:35], a[200:203], a[224:227], a[32:35]\n" \
"v_mov_b32_e64 v210, 0\n" \
"s_add_u32 s88, s35, 0x1000\n" \
"v_mfma_f32_16x16x32_bf16 a[36:39], a[136:139], a[164:167], a[36:39]\n" \
"ds_read_b64_tr_b16 a[182:183], v37 offset:33792\n" \
"v_cndmask_b32_e64 v47, 0, v214, vcc\n" \
"v_mfma_f32_16x16x32_bf16 a[36:39], a[200:203], a[228:231], a[36:39]\n" \
"ds_read_b64_tr_b16 a[244:245], v37 offset:40960\n" \
"v_or_b32_e64 v214, s7, v213\n" \
"v_mfma_f32_16x16x32_bf16 a[40:43], a[136:139], a[168:171], a[40:43]\n" \
"s_mov_b32 m0, s32\n" \
"v_lshl_or_b32 v217, s43, 3, v213\n" \
"v_mfma_f32_16x16x32_bf16 a[40:43], a[200:203], a[232:235], a[40:43]\n" \
"s_mov_b32 exec_lo, 0xffffffff\n" \
"s_mov_b32 exec_hi, 0x0\n" \
"global_load_lds_dword v[188:189], off\n" \
"s_mov_b64 exec, -1\n" \
"v_mfma_f32_16x16x32_bf16 a[44:47], a[136:139], a[172:175], a[44:47]\n" \
"ds_read_b64_tr_b16 a[246:247], v37 offset:41984\n" \
"v_lshlrev_b32_e64 v218, 10, v212\n" \
"v_mfma_f32_16x16x32_bf16 a[44:47], a[200:203], a[236:239], a[44:47]\n" \
"v_lshlrev_b32_e32 v216, 7, v216\n" \
"v_and_b32_e32 v219, 24, v219\n" \
"v_mfma_f32_16x16x32_bf16 a[60:63], a[140:143], a[172:175], a[60:63]\n" \
"ds_write_b128 v196, v[200:203]\n" \
"v_mfma_f32_16x16x32_bf16 a[60:63], a[204:207], a[236:239], a[60:63]\n" \
"v_and_or_b32 v213, v213, 2, v215\n" \
"v_add_u32_e64 v217, s40, v217\n" \
"v_mfma_f32_16x16x32_bf16 a[56:59], a[140:143], a[168:171], a[56:59]\n" \
"ds_read_b64_tr_b16 a[184:185], v38 offset:32768\n" \
"v_or3_b32 v216, v216, v218, v219\n" \
"v_mfma_f32_16x16x32_bf16 a[56:59], a[204:207], a[232:235], a[56:59]\n" \
"v_lshlrev_b32_e64 v213, 5, v213\n" \
"s_mov_b32 s40, 0x14000\n" \
"v_mfma_f32_16x16x32_bf16 a[52:55], a[140:143], a[164:167], a[52:55]\n" \
"s_lshl_b32 s7, s2, 2\n" \
"v_or3_b32 v48, v216, v213, s40\n" \
"v_mfma_f32_16x16x32_bf16 a[52:55], a[204:207], a[228:231], a[52:55]\n" \
"ds_read_b64_tr_b16 a[186:187], v38 offset:33792\n" \
"s_lshl_b32 s40, s43, 2\n" \
"v_mfma_f32_16x16x32_bf16 a[48:51], a[140:143], a[160:163], a[48:51]\n" \
"s_lshl_b32 s6, s6, 3\n" \
"v_or_b32_e32 v213, s40, v212\n" \
"v_mfma_f32_16x16x32_bf16 a[48:51], a[204:207], a[224:227], a[48:51]\n" \
"s_or_b32 s6, s6, s40\n" \
"s_and_b32 s40, s7, 0x7c\n" \
"v_mfma_f32_16x16x32_bf16 a[64:67], a[144:147], a[160:163], a[64:67]\n" \
"ds_write_b128 v197, v[204:207]\n" \
"v_mfma_f32_16x16x32_bf16 a[64:67], a[208:211], a[224:227], a[64:67]\n" \
"ds_read_b64_tr_b16 a[248:249], v38 offset:40960\n" \
"s_or_b32 s40, s40, s3\n" \
"v_mfma_f32_16x16x32_bf16 a[68:71], a[144:147], a[164:167], a[68:71]\n" \
"s_cmpk_lt_u32 s40, 64\n" \
"s_cselect_b32 s41, s4, s5\n" \
"v_mfma_f32_16x16x32_bf16 a[68:71], a[208:211], a[228:231], a[68:71]\n" \
"ds_read_b64_tr_b16 a[250:251], v38 offset:41984\n" \
"s_lshl_b32 s40, s40, 1\n" \
"v_mfma_f32_16x16x32_bf16 a[72:75], a[144:147], a[168:171], a[72:75]\n" \
"s_lshl_b32 s41, s41, 7\n" \
"s_and_b32 s40, s40, 0x7e\n" \
"v_mfma_f32_16x16x32_bf16 a[72:75], a[208:211], a[232:235], a[72:75]\n" \
"s_or_b32 s40, s41, s40\n" \
"s_mulk_i32 s40, 0x480\n" \
"v_mfma_f32_16x16x32_bf16 a[76:79], a[144:147], a[172:175], a[76:79]\n" \
"v_add_u32_e64 v49, s40, v20\n" \
"s_mov_b32 s40, 0xc000\n" \
"v_mfma_f32_16x16x32_bf16 a[76:79], a[208:211], a[236:239], a[76:79]\n" \
"ds_read_b64_tr_b16 a[188:189], v39 offset:32768\n" \
"s_and_b32 s7, s7, 28\n" \
"v_mfma_f32_16x16x32_bf16 a[92:95], a[148:151], a[172:175], a[92:95]\n" \
"ds_write_b128 v198, v[208:211]\n" \
"v_mfma_f32_16x16x32_bf16 a[92:95], a[212:215], a[236:239], a[92:95]\n" \
"s_bfe_u32 s45, s2, 0x20003\n" \
"s_mov_b32 s41, 0x8000\n" \
"v_mfma_f32_16x16x32_bf16 a[88:91], a[148:151], a[168:171], a[88:91]\n" \
"ds_read_b64_tr_b16 a[190:191], v39 offset:33792\n" \
"v_mfma_f32_16x16x32_bf16 a[88:91], a[212:215], a[232:235], a[88:91]\n" \
"s_cmp_lg_u32 s4, 0\n" \
"s_cselect_b32 s51, s40, s41\n" \
"v_mfma_f32_16x16x32_bf16 a[84:87], a[148:151], a[164:167], a[84:87]\n" \
"s_movk_i32 s40, 0x4000\n" \
"s_lshr_b32 s44, s3, 1\n" \
"v_mfma_f32_16x16x32_bf16 a[84:87], a[212:215], a[228:231], a[84:87]\n" \
"ds_read_b64_tr_b16 a[252:253], v39 offset:40960\n" \
"s_or_b32 s46, s7, s45\n" \
"v_mfma_f32_16x16x32_bf16 a[80:83], a[148:151], a[160:163], a[80:83]\n" \
"s_cmp_lg_u32 s4, 0\n" \
"s_cselect_b32 s53, s41, s40\n" \
"v_mfma_f32_16x16x32_bf16 a[80:83], a[212:215], a[224:227], a[80:83]\n" \
"s_lshl_b32 s40, s44, 15\n" \
"s_lshl_b32 s41, s43, 14\n" \
"v_mfma_f32_16x16x32_bf16 a[96:99], a[152:155], a[160:163], a[96:99]\n" \
"ds_read_b64_tr_b16 a[254:255], v39 offset:41984\n" \
"s_lshl_b32 s48, s46, 20\n" \
"v_mfma_f32_16x16x32_bf16 a[96:99], a[216:219], a[224:227], a[96:99]\n" \
"s_or_b32 s41, s40, s41\n" \
"s_add_i32 s49, s48, 0x1000000\n" \
"v_mfma_f32_16x16x32_bf16 a[100:103], a[152:155], a[164:167], a[100:103]\n" \
"s_or_b32 s42, s49, s41\n" \
"s_lshl_b32 s4, s4, 16\n" \
"v_mfma_f32_16x16x32_bf16 a[100:103], a[216:219], a[228:231], a[100:103]\n" \
"s_or_b32 s41, s42, s4\n" \
"s_lshl_b32 s4, s5, 16\n" \
"v_mfma_f32_16x16x32_bf16 a[104:107], a[152:155], a[168:171], a[104:107]\n" \
"s_mul_i32 s5, s6, 0x24800\n" \
"s_lshl_b32 s6, s46, 9\n" \
"v_mfma_f32_16x16x32_bf16 a[104:107], a[216:219], a[232:235], a[104:107]\n" \
"s_lshl_b32 s7, s7, 6\n" \
"s_add_i32 s6, s5, s6\n" \
"v_mfma_f32_16x16x32_bf16 a[108:111], a[152:155], a[172:175], a[108:111]\n" \
"s_or_b32 s5, s5, s7\n" \
"s_lshl_b32 s7, s45, 2\n" \
"v_mfma_f32_16x16x32_bf16 a[108:111], a[216:219], a[236:239], a[108:111]\n" \
"s_or_b32 s5, s5, s7\n" \
"s_lshl_b32 s7, s3, 12\n" \
"s_waitcnt lgkmcnt(15)\n" \
"v_mfma_f32_16x16x32_bf16 a[124:127], a[156:159], a[172:175], a[124:127]\n" \
"s_or_b32 s50, s49, s7\n" \
"s_lshl_b32 s7, s3, 11\n" \
"v_mfma_f32_16x16x32_bf16 a[124:127], a[220:223], a[236:239], a[124:127]\n" \
"v_lshrrev_b32_e64 v215, 4, v20\n" \
"v_bfe_u32 v216, v20, 4, 3\n" \
"v_mfma_f32_16x16x32_bf16 a[120:123], a[156:159], a[168:171], a[120:123]\n" \
"s_or_b32 s42, s42, s4\n" \
"s_lshl_b32 s4, s43, 11\n" \
"v_mfma_f32_16x16x32_bf16 a[120:123], a[220:223], a[232:235], a[120:123]\n" \
"s_add_i32 s52, s48, s7\n" \
"s_add_i32 s53, s53, s7\n" \
"v_mfma_f32_16x16x32_bf16 a[116:119], a[156:159], a[164:167], a[116:119]\n" \
"v_mul_u32_u24_e32 v214, 0x480, v214\n" \
"v_bitop3_b32 v218, v212, v215, 7 bitop3:0x78\n" \
"v_mfma_f32_16x16x32_bf16 a[116:119], a[220:223], a[228:231], a[116:119]\n" \
"v_bitop3_b32 v212, v212, v216, 4 bitop3:0x36\n" \
"s_lshl_b32 s7, s3, 10\n" \
"v_mfma_f32_16x16x32_bf16 a[112:115], a[156:159], a[160:163], a[112:115]\n" \
"s_or_b32 s4, s40, s4\n" \
"v_lshl_or_b32 v25, v212, 4, v214\n" \
"s_waitcnt vmcnt(1) lgkmcnt(0)\n" \
"v_mfma_f32_16x16x32_bf16 a[112:115], a[220:223], a[224:227], a[112:115]\n" \
"s_barrier\n" \
"v_mfma_f32_16x16x32_bf16 v[52:55], a[128:131], a[176:179], v[52:55]\n" \
"s_add_u32 m0, s30, 0x8000\n" \
"s_movk_i32 s84, 0x280\n" \
"v_mfma_f32_16x16x32_bf16 v[52:55], a[192:195], a[240:243], v[52:55]\n" \
"buffer_load_dwordx4 v22, s[64:67], s84 offen offset:0 lds\n" \
"v_mfma_f32_16x16x32_bf16 v[56:59], a[128:131], a[180:183], v[56:59]\n" \
"s_movk_i32 s84, 0x1e80\n" \
"v_mfma_f32_16x16x32_bf16 v[56:59], a[192:195], a[244:247], v[56:59]\n" \
"buffer_load_dwordx4 v23, s[64:67], s84 offen offset:1024 lds\n" \
"v_mfma_f32_16x16x32_bf16 v[60:63], a[128:131], a[184:187], v[60:63]\n" \
"ds_read_b64_tr_b16 a[160:161], v32 offset:0\n" \
"v_mul_u32_u24_e32 v212, 0x480, v217\n" \
"v_mfma_f32_16x16x32_bf16 v[60:63], a[192:195], a[248:251], v[60:63]\n" \
"s_movk_i32 s84, 0x3a80\n" \
"v_mfma_f32_16x16x32_bf16 v[64:67], a[128:131], a[188:191], v[64:67]\n" \
"buffer_load_dwordx4 v22, s[64:67], s84 offen offset:2048 lds\n" \
"v_mfma_f32_16x16x32_bf16 v[64:67], a[192:195], a[252:255], v[64:67]\n" \
"ds_read_b128 a[128:131], v28 offset:16384\n" \
"v_bitop3_b32 v213, v215, v213, 7 bitop3:0x6c\n" \
"v_mfma_f32_16x16x32_bf16 v[80:83], a[132:135], a[188:191], v[80:83]\n" \
"ds_read_b128 a[192:195], v29 offset:16384\n" \
"s_or_b32 s40, s7, 0x14000\n" \
"v_mfma_f32_16x16x32_bf16 v[80:83], a[196:199], a[252:255], v[80:83]\n" \
"s_movk_i32 s84, 0x5680\n" \
"v_mfma_f32_16x16x32_bf16 v[76:79], a[132:135], a[184:187], v[76:79]\n" \
"buffer_load_dwordx4 v23, s[64:67], s84 offen offset:3072 lds\n" \
"v_mfma_f32_16x16x32_bf16 v[76:79], a[196:199], a[248:251], v[76:79]\n" \
"s_add_u32 m0, s31, 0x8000\n" \
"s_mov_b32 s84, 0x280000\n" \
"v_mfma_f32_16x16x32_bf16 v[72:75], a[132:135], a[180:183], v[72:75]\n" \
"buffer_load_dwordx4 v26, s[68:71], s84 offen offset:0 lds\n" \
"v_mfma_f32_16x16x32_bf16 v[72:75], a[196:199], a[244:247], v[72:75]\n" \
"ds_read_b64_tr_b16 a[162:163], v32 offset:1024\n" \
"s_lshl_b32 s43, s44, 13\n" \
"v_mfma_f32_16x16x32_bf16 v[68:71], a[132:135], a[176:179], v[68:71]\n" \
"ds_read_b64_tr_b16 a[224:225], v32 offset:8192\n" \
"s_mulk_i32 s44, 0xe800\n" \
"v_mfma_f32_16x16x32_bf16 v[68:71], a[196:199], a[240:243], v[68:71]\n" \
"ds_read_b128 a[132:135], v28 offset:18432\n" \
"s_lshl_b32 s7, s3, 8\n" \
"v_mfma_f32_16x16x32_bf16 v[84:87], a[136:139], a[176:179], v[84:87]\n" \
"ds_read_b128 a[196:199], v29 offset:18432\n" \
"v_lshl_or_b32 v24, v218, 4, v214\n" \
"v_mfma_f32_16x16x32_bf16 v[84:87], a[200:203], a[240:243], v[84:87]\n" \
"s_addk_i32 s84, 0x7c00\n" \
"v_mfma_f32_16x16x32_bf16 v[88:91], a[136:139], a[180:183], v[88:91]\n" \
"buffer_load_dwordx4 v26, s[68:71], s84 offen offset:1024 lds\n" \
"v_mfma_f32_16x16x32_bf16 v[88:91], a[200:203], a[244:247], v[88:91]\n" \
"s_addk_i32 s84, 0x7c00\n" \
"v_mfma_f32_16x16x32_bf16 v[92:95], a[136:139], a[184:187], v[92:95]\n" \
"buffer_load_dwordx4 v27, s[68:71], s84 offen offset:2048 lds\n" \
"v_mfma_f32_16x16x32_bf16 v[92:95], a[200:203], a[248:251], v[92:95]\n" \
"s_addk_i32 s84, 0x7c00\n" \
"v_mfma_f32_16x16x32_bf16 v[96:99], a[136:139], a[188:191], v[96:99]\n" \
"buffer_load_dwordx4 v27, s[68:71], s84 offen offset:3072 lds\n" \
"v_mfma_f32_16x16x32_bf16 v[96:99], a[200:203], a[252:255], v[96:99]\n" \
"ds_read_b128 a[136:139], v28 offset:20480\n" \
"v_lshl_or_b32 v21, v213, 4, v212\n" \
"v_mfma_f32_16x16x32_bf16 v[112:115], a[140:143], a[188:191], v[112:115]\n" \
"ds_read_b128 a[200:203], v29 offset:20480\n" \
"s_add_i32 s44, s44, 0x17000\n" \
"v_mfma_f32_16x16x32_bf16 v[112:115], a[204:207], a[252:255], v[112:115]\n" \
"s_add_u32 m0, s30, 0x9000\n" \
"s_mov_b32 s84, 0x8280\n" \
"v_mfma_f32_16x16x32_bf16 v[108:111], a[140:143], a[184:187], v[108:111]\n" \
"buffer_load_dwordx4 v22, s[64:67], s84 offen offset:0 lds\n" \
"v_mfma_f32_16x16x32_bf16 v[108:111], a[204:207], a[248:251], v[108:111]\n" \
"s_addk_i32 s84, 0x1c00\n" \
"v_mfma_f32_16x16x32_bf16 v[104:107], a[140:143], a[180:183], v[104:107]\n" \
"buffer_load_dwordx4 v23, s[64:67], s84 offen offset:1024 lds\n" \
"v_mfma_f32_16x16x32_bf16 v[104:107], a[204:207], a[244:247], v[104:107]\n" \
"s_addk_i32 s84, 0x1c00\n" \
"v_mfma_f32_16x16x32_bf16 v[100:103], a[140:143], a[176:179], v[100:103]\n" \
"buffer_load_dwordx4 v22, s[64:67], s84 offen offset:2048 lds\n" \
"v_mfma_f32_16x16x32_bf16 v[100:103], a[204:207], a[240:243], v[100:103]\n" \
"ds_read_b128 a[140:143], v28 offset:22528\n" \
"s_or_b32 s45, s7, 0x27000\n" \
"v_mfma_f32_16x16x32_bf16 v[116:119], a[144:147], a[176:179], v[116:119]\n" \
"ds_read_b128 a[204:207], v29 offset:22528\n" \
"s_or_b32 s46, s4, 0x10000\n" \
"v_mfma_f32_16x16x32_bf16 v[116:119], a[208:211], a[240:243], v[116:119]\n" \
"s_addk_i32 s84, 0x1c00\n" \
"v_mfma_f32_16x16x32_bf16 v[120:123], a[144:147], a[180:183], v[120:123]\n" \
"buffer_load_dwordx4 v23, s[64:67], s84 offen offset:3072 lds\n" \
"v_mfma_f32_16x16x32_bf16 v[120:123], a[208:211], a[244:247], v[120:123]\n" \
"s_add_u32 m0, s31, 0xc000\n" \
"s_mov_b32 s84, 0x280000\n" \
"v_mfma_f32_16x16x32_bf16 v[124:127], a[144:147], a[184:187], v[124:127]\n" \
"buffer_load_dwordx4 v26, s[72:75], s84 offen offset:0 lds\n" \
"v_mfma_f32_16x16x32_bf16 v[124:127], a[208:211], a[248:251], v[124:127]\n" \
"s_addk_i32 s84, 0x7c00\n" \
"v_mfma_f32_16x16x32_bf16 v[128:131], a[144:147], a[188:191], v[128:131]\n" \
"buffer_load_dwordx4 v26, s[72:75], s84 offen offset:1024 lds\n" \
"v_mfma_f32_16x16x32_bf16 v[128:131], a[208:211], a[252:255], v[128:131]\n" \
"ds_read_b128 a[144:147], v28 offset:0\n" \
"s_add_i32 s48, s6, 0x3000000\n" \
"v_mfma_f32_16x16x32_bf16 v[144:147], a[148:151], a[188:191], v[144:147]\n" \
"ds_read_b128 a[208:211], v29 offset:0\n" \
"s_add_i32 s49, s5, 0x3004000\n" \
"v_mfma_f32_16x16x32_bf16 v[144:147], a[212:215], a[252:255], v[144:147]\n" \
"ds_read_b128 v[200:203], v199 offset:0\n" \
"s_add_i32 s52, s52, 0x1010000\n" \
"v_mfma_f32_16x16x32_bf16 v[140:143], a[148:151], a[184:187], v[140:143]\n" \
"ds_read_b128 v[204:207], v199 offset:4096\n" \
"v_mfma_f32_16x16x32_bf16 v[140:143], a[212:215], a[248:251], v[140:143]\n" \
"ds_read_b64_tr_b16 a[226:227], v32 offset:9216\n" \
"v_mfma_f32_16x16x32_bf16 v[136:139], a[148:151], a[180:183], v[136:139]\n" \
"ds_read_b64_tr_b16 a[164:165], v33 offset:0\n" \
"v_mfma_f32_16x16x32_bf16 v[136:139], a[212:215], a[244:247], v[136:139]\n" \
"ds_read_b128 v[208:211], v199 offset:8192\n" \
"v_mfma_f32_16x16x32_bf16 v[132:135], a[148:151], a[176:179], v[132:135]\n" \
"ds_read_b64_tr_b16 a[166:167], v33 offset:1024\n" \
"v_mfma_f32_16x16x32_bf16 v[132:135], a[212:215], a[240:243], v[132:135]\n" \
"ds_read_b128 a[148:151], v28 offset:2048\n" \
"v_mfma_f32_16x16x32_bf16 v[148:151], a[152:155], a[176:179], v[148:151]\n" \
"ds_read_b128 a[212:215], v29 offset:2048\n" \
"v_mfma_f32_16x16x32_bf16 v[148:151], a[216:219], a[240:243], v[148:151]\n" \
"ds_read_b64_tr_b16 a[228:229], v33 offset:8192\n" \
"v_mfma_f32_16x16x32_bf16 v[152:155], a[152:155], a[180:183], v[152:155]\n" \
"ds_read_b64_tr_b16 a[230:231], v33 offset:9216\n" \
"v_mfma_f32_16x16x32_bf16 v[152:155], a[216:219], a[244:247], v[152:155]\n" \
"ds_read_b64_tr_b16 a[168:169], v34 offset:0\n" \
"v_mfma_f32_16x16x32_bf16 v[156:159], a[152:155], a[184:187], v[156:159]\n" \
"ds_read_b64_tr_b16 a[170:171], v34 offset:1024\n" \
"v_mfma_f32_16x16x32_bf16 v[156:159], a[216:219], a[248:251], v[156:159]\n" \
"ds_read_b64_tr_b16 a[232:233], v34 offset:8192\n" \
"v_mfma_f32_16x16x32_bf16 v[160:163], a[152:155], a[188:191], v[160:163]\n" \
"ds_read_b64_tr_b16 a[234:235], v34 offset:9216\n" \
"v_mfma_f32_16x16x32_bf16 v[160:163], a[216:219], a[252:255], v[160:163]\n" \
"ds_read_b128 a[152:155], v28 offset:4096\n" \
"v_mfma_f32_16x16x32_bf16 v[176:179], a[156:159], a[188:191], v[176:179]\n" \
"ds_read_b128 a[216:219], v29 offset:4096\n" \
"v_mfma_f32_16x16x32_bf16 v[176:179], a[220:223], a[252:255], v[176:179]\n" \
"ds_read_b64_tr_b16 a[172:173], v35 offset:0\n" \
"v_mfma_f32_16x16x32_bf16 v[172:175], a[156:159], a[184:187], v[172:175]\n" \
"ds_read_b64_tr_b16 a[174:175], v35 offset:1024\n" \
"v_mfma_f32_16x16x32_bf16 v[172:175], a[220:223], a[248:251], v[172:175]\n" \
"ds_read_b64_tr_b16 a[236:237], v35 offset:8192\n" \
"v_mfma_f32_16x16x32_bf16 v[168:171], a[156:159], a[180:183], v[168:171]\n" \
"ds_read_b64_tr_b16 a[238:239], v35 offset:9216\n" \
"v_mfma_f32_16x16x32_bf16 v[168:171], a[220:223], a[244:247], v[168:171]\n" \
"v_mfma_f32_16x16x32_bf16 v[164:167], a[156:159], a[176:179], v[164:167]\n" \
"v_mfma_f32_16x16x32_bf16 v[164:167], a[220:223], a[240:243], v[164:167]\n" \
"ds_read_b128 a[156:159], v28 offset:6144\n" \
"ds_read_b128 a[220:223], v29 offset:6144\n" \
"s_waitcnt lgkmcnt(15)\n" \
"v_mfma_f32_16x16x32_bf16 a[0:3], a[128:131], a[160:163], a[0:3]\n" \
"s_addk_i32 s84, 0x7c00\n" \
"v_mfma_f32_16x16x32_bf16 a[0:3], a[192:195], a[224:227], a[0:3]\n" \
"buffer_load_dwordx4 v27, s[72:75], s84 offen offset:2048 lds\n" \
"v_mfma_f32_16x16x32_bf16 a[4:7], a[128:131], a[164:167], a[4:7]\n" \
"ds_read_b64_tr_b16 a[176:177], v36 offset:0\n" \
"s_waitcnt lgkmcnt(13)\n" \
"v_mfma_f32_16x16x32_bf16 a[4:7], a[192:195], a[228:231], a[4:7]\n" \
"s_addk_i32 s84, 0x7c00\n" \
"s_waitcnt lgkmcnt(11)\n" \
"v_mfma_f32_16x16x32_bf16 a[8:11], a[128:131], a[168:171], a[8:11]\n" \
"buffer_load_dwordx4 v27, s[72:75], s84 offen offset:3072 lds\n" \
"s_waitcnt lgkmcnt(9)\n" \
"v_mfma_f32_16x16x32_bf16 a[8:11], a[192:195], a[232:235], a[8:11]\n" \
"ds_read_b64_tr_b16 a[178:179], v36 offset:1024\n" \
"s_waitcnt lgkmcnt(6)\n" \
"v_mfma_f32_16x16x32_bf16 a[12:15], a[128:131], a[172:175], a[12:15]\n" \
"ds_read_b64_tr_b16 a[240:241], v36 offset:8192\n" \
"s_waitcnt lgkmcnt(5)\n" \
"v_mfma_f32_16x16x32_bf16 a[12:15], a[192:195], a[236:239], a[12:15]\n" \
"v_mfma_f32_16x16x32_bf16 a[28:31], a[132:135], a[172:175], a[28:31]\n" \
"v_mfma_f32_16x16x32_bf16 a[28:31], a[196:199], a[236:239], a[28:31]\n" \
"ds_read_b64_tr_b16 a[242:243], v36 offset:9216\n" \
"v_mfma_f32_16x16x32_bf16 a[24:27], a[132:135], a[168:171], a[24:27]\n" \
"v_mfma_f32_16x16x32_bf16 a[24:27], a[196:199], a[232:235], a[24:27]\n" \
"v_mfma_f32_16x16x32_bf16 a[20:23], a[132:135], a[164:167], a[20:23]\n" \
"v_mfma_f32_16x16x32_bf16 a[20:23], a[196:199], a[228:231], a[20:23]\n" \
"ds_read_b64_tr_b16 a[180:181], v37 offset:0\n" \
"v_mfma_f32_16x16x32_bf16 a[16:19], a[132:135], a[160:163], a[16:19]\n" \
"v_mfma_f32_16x16x32_bf16 a[16:19], a[196:199], a[224:227], a[16:19]\n" \
"v_mfma_f32_16x16x32_bf16 a[32:35], a[136:139], a[160:163], a[32:35]\n" \
"ds_read_b64_tr_b16 a[182:183], v37 offset:1024\n" \
"v_mfma_f32_16x16x32_bf16 a[32:35], a[200:203], a[224:227], a[32:35]\n" \
"v_mfma_f32_16x16x32_bf16 a[36:39], a[136:139], a[164:167], a[36:39]\n" \
"v_mfma_f32_16x16x32_bf16 a[36:39], a[200:203], a[228:231], a[36:39]\n" \
"buffer_store_dwordx4 v[200:203], v20, s[80:83], s35 offen sc1\n" \
".fill (L_pb + 52960 - .) / 4, 4, 0xBF800000\n" \
"L_pp13:\n" \
"s_cbranch_execz L_pret\n" \
"v_mfma_f32_16x16x32_bf16 a[40:43], a[136:139], a[168:171], a[40:43]\n" \
"s_mov_b32 m0, s32\n" \
"ds_read_b64_tr_b16 a[244:245], v37 offset:8192\n" \
"v_mfma_f32_16x16x32_bf16 a[40:43], a[200:203], a[232:235], a[40:43]\n" \
"s_mov_b32 exec_lo, 0x0\n" \
"s_mov_b32 exec_hi, 0xffffffff\n" \
"global_load_lds_dword v[188:189], off\n" \
"s_mov_b64 exec, -1\n" \
"s_nop 0\n" \
"v_mfma_f32_16x16x32_bf16 a[44:47], a[136:139], a[172:175], a[44:47]\n" \
"ds_read_b64_tr_b16 a[246:247], v37 offset:9216\n" \
"v_mfma_f32_16x16x32_bf16 a[44:47], a[200:203], a[236:239], a[44:47]\n" \
"v_mfma_f32_16x16x32_bf16 a[60:63], a[140:143], a[172:175], a[60:63]\n" \
"v_mfma_f32_16x16x32_bf16 a[60:63], a[204:207], a[236:239], a[60:63]\n" \
"v_mfma_f32_16x16x32_bf16 a[56:59], a[140:143], a[168:171], a[56:59]\n" \
"ds_read_b64_tr_b16 a[184:185], v38 offset:0\n" \
"v_mfma_f32_16x16x32_bf16 a[56:59], a[204:207], a[232:235], a[56:59]\n" \
"v_mfma_f32_16x16x32_bf16 a[52:55], a[140:143], a[164:167], a[52:55]\n" \
"buffer_store_dwordx4 v[204:207], v20, s[80:83], s88 offen sc1\n" \
"v_mfma_f32_16x16x32_bf16 a[52:55], a[204:207], a[228:231], a[52:55]\n" \
"ds_read_b64_tr_b16 a[186:187], v38 offset:1024\n" \
"v_mfma_f32_16x16x32_bf16 a[48:51], a[140:143], a[160:163], a[48:51]\n" \
"v_mfma_f32_16x16x32_bf16 a[48:51], a[204:207], a[224:227], a[48:51]\n" \
"v_mfma_f32_16x16x32_bf16 a[64:67], a[144:147], a[160:163], a[64:67]\n" \
"ds_read_b64_tr_b16 a[248:249], v38 offset:8192\n" \
"v_mfma_f32_16x16x32_bf16 a[64:67], a[208:211], a[224:227], a[64:67]\n" \
"v_mfma_f32_16x16x32_bf16 a[68:71], a[144:147], a[164:167], a[68:71]\n" \
"v_mfma_f32_16x16x32_bf16 a[68:71], a[208:211], a[228:231], a[68:71]\n" \
"ds_read_b64_tr_b16 a[250:251], v38 offset:9216\n" \
"v_mfma_f32_16x16x32_bf16 a[72:75], a[144:147], a[168:171], a[72:75]\n" \
"v_mfma_f32_16x16x32_bf16 a[72:75], a[208:211], a[232:235], a[72:75]\n" \
"v_mfma_f32_16x16x32_bf16 a[76:79], a[144:147], a[172:175], a[76:79]\n" \
"buffer_store_dwordx4 v[208:211], v20, s[80:83], s36 offen sc1\n" \
"v_mfma_f32_16x16x32_bf16 a[76:79], a[208:211], a[236:239], a[76:79]\n" \
"ds_read_b64_tr_b16 a[188:189], v39 offset:0\n" \
"v_mfma_f32_16x16x32_bf16 a[92:95], a[148:151], a[172:175], a[92:95]\n" \
"v_mfma_f32_16x16x32_bf16 a[92:95], a[212:215], a[236:239], a[92:95]\n" \
"v_mfma_f32_16x16x32_bf16 a[88:91], a[148:151], a[168:171], a[88:91]\n" \
"ds_read_b64_tr_b16 a[190:191], v39 offset:1024\n" \
"v_mfma_f32_16x16x32_bf16 a[88:91], a[212:215], a[232:235], a[88:91]\n" \
"v_mfma_f32_16x16x32_bf16 a[84:87], a[148:151], a[164:167], a[84:87]\n" \
"v_mfma_f32_16x16x32_bf16 a[84:87], a[212:215], a[228:231], a[84:87]\n" \
"ds_read_b64_tr_b16 a[252:253], v39 offset:8192\n" \
"v_mfma_f32_16x16x32_bf16 a[80:83], a[148:151], a[160:163], a[80:83]\n" \
"v_mfma_f32_16x16x32_bf16 a[80:83], a[212:215], a[224:227], a[80:83]\n" \
"v_mfma_f32_16x16x32_bf16 a[96:99], a[152:155], a[160:163], a[96:99]\n" \
"ds_read_b64_tr_b16 a[254:255], v39 offset:9216\n" \
"v_mfma_f32_16x16x32_bf16 a[96:99], a[216:219], a[224:227], a[96:99]\n" \
"v_mfma_f32_16x16x32_bf16 a[100:103], a[152:155], a[164:167], a[100:103]\n" \
"v_mfma_f32_16x16x32_bf16 a[100:103], a[216:219], a[228:231], a[100:103]\n" \
"v_mfma_f32_16x16x32_bf16 a[104:107], a[152:155], a[168:171], a[104:107]\n" \
"v_mfma_f32_16x16x32_bf16 a[104:107], a[216:219], a[232:235], a[104:107]\n" \
"v_mfma_f32_16x16x32_bf16 a[108:111], a[152:155], a[172:175], a[108:111]\n" \
"v_mfma_f32_16x16x32_bf16 a[108:111], a[216:219], a[236:239], a[108:111]\n" \
"s_waitcnt lgkmcnt(15)\n" \
"v_mfma_f32_16x16x32_bf16 a[124:127], a[156:159], a[172:175], a[124:127]\n" \
"v_mfma_f32_16x16x32_bf16 a[124:127], a[220:223], a[236:239], a[124:127]\n" \
"v_mfma_f32_16x16x32_bf16 a[120:123], a[156:159], a[168:171], a[120:123]\n" \
"v_mfma_f32_16x16x32_bf16 a[120:123], a[220:223], a[232:235], a[120:123]\n" \
"v_mfma_f32_16x16x32_bf16 a[116:119], a[156:159], a[164:167], a[116:119]\n" \
"v_mfma_f32_16x16x32_bf16 a[116:119], a[220:223], a[228:231], a[116:119]\n" \
"v_mfma_f32_16x16x32_bf16 a[112:115], a[156:159], a[160:163], a[112:115]\n" \
"s_waitcnt vmcnt(4) lgkmcnt(0)\n" \
"v_mfma_f32_16x16x32_bf16 a[112:115], a[220:223], a[224:227], a[112:115]\n" \
"s_barrier\n" \
"v_mfma_f32_16x16x32_bf16 v[52:55], a[128:131], a[176:179], v[52:55]\n" \
"s_add_u32 m0, s30, 0x0\n" \
"s_movk_i32 s84, 0x300\n" \
"v_mfma_f32_16x16x32_bf16 v[52:55], a[192:195], a[240:243], v[52:55]\n" \
"buffer_load_dwordx4 v22, s[64:67], s84 offen offset:0 lds\n" \
"v_mfma_f32_16x16x32_bf16 v[56:59], a[128:131], a[180:183], v[56:59]\n" \
"ds_read_b64_tr_b16 a[160:161], v32 offset:32768\n" \
"v_mfma_f32_16x16x32_bf16 v[56:59], a[192:195], a[244:247], v[56:59]\n" \
"s_movk_i32 s84, 0x1f00\n" \
"v_mfma_f32_16x16x32_bf16 v[60:63], a[128:131], a[184:187], v[60:63]\n" \
"buffer_load_dwordx4 v23, s[64:67], s84 offen offset:1024 lds\n" \
"v_mfma_f32_16x16x32_bf16 v[60:63], a[192:195], a[248:251], v[60:63]\n" \
"ds_read_b64_tr_b16 a[162:163], v32 offset:33792\n" \
"v_mfma_f32_16x16x32_bf16 v[64:67], a[128:131], a[188:191], v[64:67]\n" \
"ds_read_b64_tr_b16 a[224:225], v32 offset:40960\n" \
"v_mfma_f32_16x16x32_bf16 v[64:67], a[192:195], a[252:255], v[64:67]\n" \
"ds_read_b128 a[128:131], v28 offset:49152\n" \
"v_mfma_f32_16x16x32_bf16 v[80:83], a[132:135], a[188:191], v[80:83]\n" \
"ds_read_b128 a[192:195], v29 offset:49152\n" \
"v_mfma_f32_16x16x32_bf16 v[80:83], a[196:199], a[252:255], v[80:83]\n" \
"s_movk_i32 s84, 0x3b00\n" \
"v_mfma_f32_16x16x32_bf16 v[76:79], a[132:135], a[184:187], v[76:79]\n" \
"buffer_load_dwordx4 v22, s[64:67], s84 offen offset:2048 lds\n" \
"v_mfma_f32_16x16x32_bf16 v[76:79], a[196:199], a[248:251], v[76:79]\n" \
"s_movk_i32 s84, 0x5700\n" \
"v_mfma_f32_16x16x32_bf16 v[72:75], a[132:135], a[180:183], v[72:75]\n" \
"buffer_load_dwordx4 v23, s[64:67], s84 offen offset:3072 lds\n" \
"v_mfma_f32_16x16x32_bf16 v[72:75], a[196:199], a[244:247], v[72:75]\n" \
"s_add_u32 m0, s31, 0x0\n" \
"s_mov_b32 s84, 0x300000\n" \
"v_mfma_f32_16x16x32_bf16 v[68:71], a[132:135], a[176:179], v[68:71]\n" \
"buffer_load_dwordx4 v26, s[68:71], s84 offen offset:0 lds\n" \
"v_mfma_f32_16x16x32_bf16 v[68:71], a[196:199], a[240:243], v[68:71]\n" \
"ds_read_b128 a[132:135], v28 offset:51200\n" \
"v_mfma_f32_16x16x32_bf16 v[84:87], a[136:139], a[176:179], v[84:87]\n" \
"ds_read_b128 a[196:199], v29 offset:51200\n" \
"v_mfma_f32_16x16x32_bf16 v[84:87], a[200:203], a[240:243], v[84:87]\n" \
"s_addk_i32 s84, 0x7c00\n" \
"v_mfma_f32_16x16x32_bf16 v[88:91], a[136:139], a[180:183], v[88:91]\n" \
"buffer_load_dwordx4 v26, s[68:71], s84 offen offset:1024 lds\n" \
"v_mfma_f32_16x16x32_bf16 v[88:91], a[200:203], a[244:247], v[88:91]\n" \
"ds_read_b64_tr_b16 a[226:227], v32 offset:41984\n" \
"v_mfma_f32_16x16x32_bf16 v[92:95], a[136:139], a[184:187], v[92:95]\n" \
"s_addk_i32 s84, 0x7c00\n" \
"v_mfma_f32_16x16x32_bf16 v[92:95], a[200:203], a[248:251], v[92:95]\n" \
"buffer_load_dwordx4 v27, s[68:71], s84 offen offset:2048 lds\n" \
"v_mfma_f32_16x16x32_bf16 v[96:99], a[136:139], a[188:191], v[96:99]\n" \
"ds_read_b64_tr_b16 a[164:165], v33 offset:32768\n" \
"v_mfma_f32_16x16x32_bf16 v[96:99], a[200:203], a[252:255], v[96:99]\n" \
"ds_read_b128 a[136:139], v28 offset:53248\n" \
"v_mfma_f32_16x16x32_bf16 v[112:115], a[140:143], a[188:191], v[112:115]\n" \
"ds_read_b128 a[200:203], v29 offset:53248\n" \
"v_mfma_f32_16x16x32_bf16 v[112:115], a[204:207], a[252:255], v[112:115]\n" \
"s_addk_i32 s84, 0x7c00\n" \
"v_mfma_f32_16x16x32_bf16 v[108:111], a[140:143], a[184:187], v[108:111]\n" \
"buffer_load_dwordx4 v27, s[68:71], s84 offen offset:3072 lds\n" \
"v_mfma_f32_16x16x32_bf16 v[108:111], a[204:207], a[248:251], v[108:111]\n" \
"s_add_u32 m0, s30, 0x1000\n" \
"s_mov_b32 s84, 0x8300\n" \
"v_mfma_f32_16x16x32_bf16 v[104:107], a[140:143], a[180:183], v[104:107]\n" \
"buffer_load_dwordx4 v22, s[64:67], s84 offen offset:0 lds\n" \
"v_mfma_f32_16x16x32_bf16 v[104:107], a[204:207], a[244:247], v[104:107]\n" \
"ds_read_b64_tr_b16 a[166:167], v33 offset:33792\n" \
"v_mfma_f32_16x16x32_bf16 v[100:103], a[140:143], a[176:179], v[100:103]\n" \
"ds_read_b64_tr_b16 a[228:229], v33 offset:40960\n" \
"v_mfma_f32_16x16x32_bf16 v[100:103], a[204:207], a[240:243], v[100:103]\n" \
"ds_read_b128 a[140:143], v28 offset:55296\n" \
"v_mfma_f32_16x16x32_bf16 v[116:119], a[144:147], a[176:179], v[116:119]\n" \
"ds_read_b128 a[204:207], v29 offset:55296\n" \
"v_mfma_f32_16x16x32_bf16 v[116:119], a[208:211], a[240:243], v[116:119]\n" \
"s_addk_i32 s84, 0x1c00\n" \
"v_mfma_f32_16x16x32_bf16 v[120:123], a[144:147], a[180:183], v[120:123]\n" \
"buffer_load_dwordx4 v23, s[64:67], s84 offen offset:1024 lds\n" \
"v_mfma_f32_16x16x32_bf16 v[120:123], a[208:211], a[244:247], v[120:123]\n" \
"s_addk_i32 s84, 0x1c00\n" \
"v_mfma_f32_16x16x32_bf16 v[124:127], a[144:147], a[184:187], v[124:127]\n" \
"buffer_load_dwordx4 v22, s[64:67], s84 offen offset:2048 lds\n" \
"v_mfma_f32_16x16x32_bf16 v[124:127], a[208:211], a[248:251], v[124:127]\n" \
"s_addk_i32 s84, 0x1c00\n" \
"s_nop 0\n" \
"v_mfma_f32_16x16x32_bf16 v[128:131], a[144:147], a[188:191], v[128:131]\n" \
"buffer_load_dwordx4 v23, s[64:67], s84 offen offset:3072 lds\n" \
"v_mfma_f32_16x16x32_bf16 v[128:131], a[208:211], a[252:255], v[128:131]\n" \
"ds_read_b128 a[144:147], v28 offset:32768\n" \
"v_mfma_f32_16x16x32_bf16 v[144:147], a[148:151], a[188:191], v[144:147]\n" \
"ds_read_b128 a[208:211], v29 offset:32768\n" \
"v_mfma_f32_16x16x32_bf16 v[144:147], a[212:215], a[252:255], v[144:147]\n" \
"s_add_u32 m0, s31, 0x4000\n" \
"s_mov_b32 s84, 0x300000\n" \
"v_mfma_f32_16x16x32_bf16 v[140:143], a[148:151], a[184:187], v[140:143]\n" \
"buffer_load_dwordx4 v26, s[72:75], s84 offen offset:0 lds\n" \
"v_mfma_f32_16x16x32_bf16 v[140:143], a[212:215], a[248:251], v[140:143]\n" \
"ds_read_b64_tr_b16 a[230:231], v33 offset:41984\n" \
"v_mfma_f32_16x16x32_bf16 v[136:139], a[148:151], a[180:183], v[136:139]\n" \
"s_addk_i32 s84, 0x7c00\n" \
"s_nop 0\n" \
"v_mfma_f32_16x16x32_bf16 v[136:139], a[212:215], a[244:247], v[136:139]\n" \
"buffer_load_dwordx4 v26, s[72:75], s84 offen offset:1024 lds\n" \
"v_mfma_f32_16x16x32_bf16 v[132:135], a[148:151], a[176:179], v[132:135]\n" \
"ds_read_b64_tr_b16 a[168:169], v34 offset:32768\n" \
"v_mfma_f32_16x16x32_bf16 v[132:135], a[212:215], a[240:243], v[132:135]\n" \
"ds_read_b128 a[148:151], v28 offset:34816\n" \
"v_mfma_f32_16x16x32_bf16 v[148:151], a[152:155], a[176:179], v[148:151]\n" \
"ds_read_b128 a[212:215], v29 offset:34816\n" \
"v_mfma_f32_16x16x32_bf16 v[148:151], a[216:219], a[240:243], v[148:151]\n" \
"ds_read_b64_tr_b16 a[170:171], v34 offset:33792\n" \
"v_mfma_f32_16x16x32_bf16 v[152:155], a[152:155], a[180:183], v[152:155]\n" \
"ds_read_b64_tr_b16 a[232:233], v34 offset:40960\n" \
"v_mfma_f32_16x16x32_bf16 v[152:155], a[216:219], a[244:247], v[152:155]\n" \
"ds_read_b64_tr_b16 a[234:235], v34 offset:41984\n" \
"v_mfma_f32_16x16x32_bf16 v[156:159], a[152:155], a[184:187], v[156:159]\n" \
"ds_read_b64_tr_b16 a[172:173], v35 offset:32768\n" \
"v_mfma_f32_16x16x32_bf16 v[156:159], a[216:219], a[248:251], v[156:159]\n" \
"ds_read_b64_tr_b16 a[174:175], v35 offset:33792\n" \
"v_mfma_f32_16x16x32_bf16 v[160:163], a[152:155], a[188:191], v[160:163]\n" \
"ds_read_b64_tr_b16 a[236:237], v35 offset:40960\n" \
"v_mfma_f32_16x16x32_bf16 v[160:163], a[216:219], a[252:255], v[160:163]\n" \
"ds_read_b128 a[152:155], v28 offset:36864\n" \
"v_mfma_f32_16x16x32_bf16 v[176:179], a[156:159], a[188:191], v[176:179]\n" \
"ds_read_b128 a[216:219], v29 offset:36864\n" \
"v_mfma_f32_16x16x32_bf16 v[176:179], a[220:223], a[252:255], v[176:179]\n" \
"ds_read_b64_tr_b16 a[238:239], v35 offset:41984\n" \
"v_mfma_f32_16x16x32_bf16 v[172:175], a[156:159], a[184:187], v[172:175]\n" \
"v_mfma_f32_16x16x32_bf16 v[172:175], a[220:223], a[248:251], v[172:175]\n" \
"v_mfma_f32_16x16x32_bf16 v[168:171], a[156:159], a[180:183], v[168:171]\n" \
"v_mfma_f32_16x16x32_bf16 v[168:171], a[220:223], a[244:247], v[168:171]\n" \
"v_mfma_f32_16x16x32_bf16 v[164:167], a[156:159], a[176:179], v[164:167]\n" \
"v_mfma_f32_16x16x32_bf16 v[164:167], a[220:223], a[240:243], v[164:167]\n" \
"ds_read_b128 a[156:159], v28 offset:38912\n" \
"ds_read_b128 a[220:223], v29 offset:38912\n" \
"s_waitcnt lgkmcnt(15)\n" \
"v_mfma_f32_16x16x32_bf16 a[0:3], a[128:131], a[160:163], a[0:3]\n" \
"s_addk_i32 s84, 0x7c00\n" \
"v_mfma_f32_16x16x32_bf16 a[0:3], a[192:195], a[224:227], a[0:3]\n" \
"buffer_load_dwordx4 v27, s[72:75], s84 offen offset:2048 lds\n" \
"v_mfma_f32_16x16x32_bf16 a[4:7], a[128:131], a[164:167], a[4:7]\n" \
"ds_read_b64_tr_b16 a[176:177], v36 offset:32768\n" \
"s_waitcnt lgkmcnt(15)\n" \
"v_mfma_f32_16x16x32_bf16 a[4:7], a[192:195], a[228:231], a[4:7]\n" \
"s_addk_i32 s84, 0x7c00\n" \
"s_waitcnt lgkmcnt(11)\n" \
"v_mfma_f32_16x16x32_bf16 a[8:11], a[128:131], a[168:171], a[8:11]\n" \
"buffer_load_dwordx4 v27, s[72:75], s84 offen offset:3072 lds\n" \
"s_waitcnt lgkmcnt(9)\n" \
"v_mfma_f32_16x16x32_bf16 a[8:11], a[192:195], a[232:235], a[8:11]\n" \
"ds_read_b64_tr_b16 a[178:179], v36 offset:33792\n" \
"s_waitcnt lgkmcnt(8)\n" \
"v_mfma_f32_16x16x32_bf16 a[12:15], a[128:131], a[172:175], a[12:15]\n" \
"ds_read_b64_tr_b16 a[240:241], v36 offset:40960\n" \
"s_waitcnt lgkmcnt(5)\n" \
"v_mfma_f32_16x16x32_bf16 a[12:15], a[192:195], a[236:239], a[12:15]\n" \
"v_mfma_f32_16x16x32_bf16 a[28:31], a[132:135], a[172:175], a[28:31]\n" \
"v_mfma_f32_16x16x32_bf16 a[28:31], a[196:199], a[236:239], a[28:31]\n" \
"ds_read_b64_tr_b16 a[242:243], v36 offset:41984\n" \
"v_mfma_f32_16x16x32_bf16 a[24:27], a[132:135], a[168:171], a[24:27]\n" \
"v_mfma_f32_16x16x32_bf16 a[24:27], a[196:199], a[232:235], a[24:27]\n" \
"v_mfma_f32_16x16x32_bf16 a[20:23], a[132:135], a[164:167], a[20:23]\n" \
"v_mfma_f32_16x16x32_bf16 a[20:23], a[196:199], a[228:231], a[20:23]\n" \
"ds_read_b64_tr_b16 a[180:181], v37 offset:32768\n" \
"v_mfma_f32_16x16x32_bf16 a[16:19], a[132:135], a[160:163], a[16:19]\n" \
"v_mfma_f32_16x16x32_bf16 a[16:19], a[196:199], a[224:227], a[16:19]\n" \
"v_mfma_f32_16x16x32_bf16 a[32:35], a[136:139], a[160:163], a[32:35]\n" \
"ds_read_b64_tr_b16 a[182:183], v37 offset:33792\n" \
"v_mfma_f32_16x16x32_bf16 a[32:35], a[200:203], a[224:227], a[32:35]\n" \
"v_mfma_f32_16x16x32_bf16 a[36:39], a[136:139], a[164:167], a[36:39]\n" \
"v_lshlrev_b32_e32 v45, 3, v20\n" \
"v_add_u32_e32 v45, s31, v45\n" \
"v_mfma_f32_16x16x32_bf16 a[36:39], a[200:203], a[228:231], a[36:39]\n" \
"s_getpc_b64 s[86:87]\n" \
"s_sub_u32 s86, s86, 0x10800\n" \
"s_subb_u32 s87, s87, 0\n" \
"s_mov_b32 m0, s32\n" \
"s_nop 0\n" \
"v_mfma_f32_16x16x32_bf16 a[40:43], a[136:139], a[168:171], a[40:43]\n" \
"global_load_lds_dword v45, s[86:87]\n" \
"v_mfma_f32_16x16x32_bf16 a[40:43], a[200:203], a[232:235], a[40:43]\n" \
"ds_read_b64_tr_b16 a[244:245], v37 offset:40960\n" \
"v_mfma_f32_16x16x32_bf16 a[44:47], a[136:139], a[172:175], a[44:47]\n" \
"ds_read_b64_tr_b16 a[246:247], v37 offset:41984\n" \
"v_mfma_f32_16x16x32_bf16 a[44:47], a[200:203], a[236:239], a[44:47]\n" \
"v_mfma_f32_16x16x32_bf16 a[60:63], a[140:143], a[172:175], a[60:63]\n" \
"v_mfma_f32_16x16x32_bf16 a[60:63], a[204:207], a[236:239], a[60:63]\n" \
"v_mfma_f32_16x16x32_bf16 a[56:59], a[140:143], a[168:171], a[56:59]\n" \
"ds_read_b64_tr_b16 a[184:185], v38 offset:32768\n" \
"v_mfma_f32_16x16x32_bf16 a[56:59], a[204:207], a[232:235], a[56:59]\n" \
"v_mfma_f32_16x16x32_bf16 a[52:55], a[140:143], a[164:167], a[52:55]\n" \
"v_mfma_f32_16x16x32_bf16 a[52:55], a[204:207], a[228:231], a[52:55]\n" \
"ds_read_b64_tr_b16 a[186:187], v38 offset:33792\n" \
"v_mfma_f32_16x16x32_bf16 a[48:51], a[140:143], a[160:163], a[48:51]\n" \
"v_mfma_f32_16x16x32_bf16 a[48:51], a[204:207], a[224:227], a[48:51]\n" \
"v_mfma_f32_16x16x32_bf16 a[64:67], a[144:147], a[160:163], a[64:67]\n" \
"ds_read_b64_tr_b16 a[248:249], v38 offset:40960\n" \
"v_mfma_f32_16x16x32_bf16 a[64:67], a[208:211], a[224:227], a[64:67]\n" \
"v_mfma_f32_16x16x32_bf16 a[68:71], a[144:147], a[164:167], a[68:71]\n" \
"v_mfma_f32_16x16x32_bf16 a[68:71], a[208:211], a[228:231], a[68:71]\n" \
"ds_read_b64_tr_b16 a[250:251], v38 offset:41984\n" \
"v_mfma_f32_16x16x32_bf16 a[72:75], a[144:147], a[168:171], a[72:75]\n" \
"v_mfma_f32_16x16x32_bf16 a[72:75], a[208:211], a[232:235], a[72:75]\n" \
"v_mfma_f32_16x16x32_bf16 a[76:79], a[144:147], a[172:175], a[76:79]\n" \
"v_mfma_f32_16x16x32_bf16 a[76:79], a[208:211], a[236:239], a[76:79]\n" \
"ds_read_b64_tr_b16 a[188:189], v39 offset:32768\n" \
"v_mfma_f32_16x16x32_bf16 a[92:95], a[148:151], a[172:175], a[92:95]\n" \
"v_mfma_f32_16x16x32_bf16 a[92:95], a[212:215], a[236:239], a[92:95]\n" \
"v_mfma_f32_16x16x32_bf16 a[88:91], a[148:151], a[168:171], a[88:91]\n" \
"ds_read_b64_tr_b16 a[190:191], v39 offset:33792\n" \
"v_mfma_f32_16x16x32_bf16 a[88:91], a[212:215], a[232:235], a[88:91]\n" \
"v_mfma_f32_16x16x32_bf16 a[84:87], a[148:151], a[164:167], a[84:87]\n" \
"v_mfma_f32_16x16x32_bf16 a[84:87], a[212:215], a[228:231], a[84:87]\n" \
"ds_read_b64_tr_b16 a[252:253], v39 offset:40960\n" \
"v_mfma_f32_16x16x32_bf16 a[80:83], a[148:151], a[160:163], a[80:83]\n" \
"v_mfma_f32_16x16x32_bf16 a[80:83], a[212:215], a[224:227], a[80:83]\n" \
"v_mfma_f32_16x16x32_bf16 a[96:99], a[152:155], a[160:163], a[96:99]\n" \
"ds_read_b64_tr_b16 a[254:255], v39 offset:41984\n" \
"v_mfma_f32_16x16x32_bf16 a[96:99], a[216:219], a[224:227], a[96:99]\n" \
"v_mfma_f32_16x16x32_bf16 a[100:103], a[152:155], a[164:167], a[100:103]\n" \
"v_mfma_f32_16x16x32_bf16 a[100:103], a[216:219], a[228:231], a[100:103]\n" \
"v_mfma_f32_16x16x32_bf16 a[104:107], a[152:155], a[168:171], a[104:107]\n" \
"v_mfma_f32_16x16x32_bf16 a[104:107], a[216:219], a[232:235], a[104:107]\n" \
"v_mfma_f32_16x16x32_bf16 a[108:111], a[152:155], a[172:175], a[108:111]\n" \
"v_mfma_f32_16x16x32_bf16 a[108:111], a[216:219], a[236:239], a[108:111]\n" \
"s_waitcnt lgkmcnt(15)\n" \
"v_mfma_f32_16x16x32_bf16 a[124:127], a[156:159], a[172:175], a[124:127]\n" \
"v_mfma_f32_16x16x32_bf16 a[124:127], a[220:223], a[236:239], a[124:127]\n" \
"v_mfma_f32_16x16x32_bf16 a[120:123], a[156:159], a[168:171], a[120:123]\n" \
"v_mfma_f32_16x16x32_bf16 a[120:123], a[220:223], a[232:235], a[120:123]\n" \
"v_mfma_f32_16x16x32_bf16 a[116:119], a[156:159], a[164:167], a[116:119]\n" \
"v_mfma_f32_16x16x32_bf16 a[116:119], a[220:223], a[228:231], a[116:119]\n" \
"v_mfma_f32_16x16x32_bf16 a[112:115], a[156:159], a[160:163], a[112:115]\n" \
"s_waitcnt vmcnt(1) lgkmcnt(0)\n" \
"v_mfma_f32_16x16x32_bf16 a[112:115], a[220:223], a[224:227], a[112:115]\n" \
"s_barrier\n" \
"v_mov_b32_e32 v45, s62\n" \
"s_mov_b64 exec, 1\n" \
"buffer_store_dword v45, off, s[80:83], s37 offset:128 sc1\n" \
"s_mov_b64 exec, -1\n" \
"v_mfma_f32_16x16x32_bf16 v[52:55], a[128:131], a[176:179], v[52:55]\n" \
"s_add_u32 m0, s30, 0x8000\n" \
"s_movk_i32 s84, 0x380\n" \
"v_mfma_f32_16x16x32_bf16 v[52:55], a[192:195], a[240:243], v[52:55]\n" \
"buffer_load_dwordx4 v22, s[64:67], s84 offen offset:0 lds\n" \
"v_mfma_f32_16x16x32_bf16 v[56:59], a[128:131], a[180:183], v[56:59]\n" \
"ds_read_b64_tr_b16 a[160:161], v32 offset:0\n" \
"v_mfma_f32_16x16x32_bf16 v[56:59], a[192:195], a[244:247], v[56:59]\n" \
"s_movk_i32 s84, 0x1f80\n" \
"v_mfma_f32_16x16x32_bf16 v[60:63], a[128:131], a[184:187], v[60:63]\n" \
"buffer_load_dwordx4 v23, s[64:67], s84 offen offset:1024 lds\n" \
"v_mfma_f32_16x16x32_bf16 v[60:63], a[192:195], a[248:251], v[60:63]\n" \
"ds_read_b64_tr_b16 a[162:163], v32 offset:1024\n" \
"v_mfma_f32_16x16x32_bf16 v[64:67], a[128:131], a[188:191], v[64:67]\n" \
"ds_read_b64_tr_b16 a[224:225], v32 offset:8192\n" \
"v_mfma_f32_16x16x32_bf16 v[64:67], a[192:195], a[252:255], v[64:67]\n" \
"ds_read_b128 a[128:131], v28 offset:16384\n" \
"v_mfma_f32_16x16x32_bf16 v[80:83], a[132:135], a[188:191], v[80:83]\n" \
"ds_read_b128 a[192:195], v29 offset:16384\n" \
"v_mfma_f32_16x16x32_bf16 v[80:83], a[196:199], a[252:255], v[80:83]\n" \
"s_movk_i32 s84, 0x3b80\n" \
"v_mfma_f32_16x16x32_bf16 v[76:79], a[132:135], a[184:187], v[76:79]\n" \
"buffer_load_dwordx4 v22, s[64:67], s84 offen offset:2048 lds\n" \
"v_mfma_f32_16x16x32_bf16 v[76:79], a[196:199], a[248:251], v[76:79]\n" \
"s_movk_i32 s84, 0x5780\n" \
"v_mfma_f32_16x16x32_bf16 v[72:75], a[132:135], a[180:183], v[72:75]\n" \
"buffer_load_dwordx4 v23, s[64:67], s84 offen offset:3072 lds\n" \
"v_mfma_f32_16x16x32_bf16 v[72:75], a[196:199], a[244:247], v[72:75]\n" \
"s_add_u32 m0, s31, 0x8000\n" \
"s_mov_b32 s84, 0x380000\n" \
"v_mfma_f32_16x16x32_bf16 v[68:71], a[132:135], a[176:179], v[68:71]\n" \
"buffer_load_dwordx4 v26, s[68:71], s84 offen offset:0 lds\n" \
"v_mfma_f32_16x16x32_bf16 v[68:71], a[196:199], a[240:243], v[68:71]\n" \
"ds_read_b128 a[132:135], v28 offset:18432\n" \
"v_mfma_f32_16x16x32_bf16 v[84:87], a[136:139], a[176:179], v[84:87]\n" \
"ds_read_b128 a[196:199], v29 offset:18432\n" \
"v_mfma_f32_16x16x32_bf16 v[84:87], a[200:203], a[240:243], v[84:87]\n" \
"s_addk_i32 s84, 0x7c00\n" \
"v_mfma_f32_16x16x32_bf16 v[88:91], a[136:139], a[180:183], v[88:91]\n" \
"buffer_load_dwordx4 v26, s[68:71], s84 offen offset:1024 lds\n" \
"v_mfma_f32_16x16x32_bf16 v[88:91], a[200:203], a[244:247], v[88:91]\n" \
"ds_read_b64_tr_b16 a[226:227], v32 offset:9216\n" \
"v_mfma_f32_16x16x32_bf16 v[92:95], a[136:139], a[184:187], v[92:95]\n" \
"s_addk_i32 s84, 0x7c00\n" \
"v_mfma_f32_16x16x32_bf16 v[92:95], a[200:203], a[248:251], v[92:95]\n" \
"buffer_load_dwordx4 v27, s[68:71], s84 offen offset:2048 lds\n" \
"v_mfma_f32_16x16x32_bf16 v[96:99], a[136:139], a[188:191], v[96:99]\n" \
"ds_read_b64_tr_b16 a[164:165], v33 offset:0\n" \
"v_mfma_f32_16x16x32_bf16 v[96:99], a[200:203], a[252:255], v[96:99]\n" \
"ds_read_b128 a[136:139], v28 offset:20480\n" \
"v_mfma_f32_16x16x32_bf16 v[112:115], a[140:143], a[188:191], v[112:115]\n" \
"ds_read_b128 a[200:203], v29 offset:20480\n" \
"v_mfma_f32_16x16x32_bf16 v[112:115], a[204:207], a[252:255], v[112:115]\n" \
"s_addk_i32 s84, 0x7c00\n" \
"s_nop 0\n" \
"v_mfma_f32_16x16x32_bf16 v[108:111], a[140:143], a[184:187], v[108:111]\n" \
"buffer_load_dwordx4 v27, s[68:71], s84 offen offset:3072 lds\n" \
"v_mfma_f32_16x16x32_bf16 v[108:111], a[204:207], a[248:251], v[108:111]\n" \
"s_add_u32 m0, s30, 0x9000\n" \
"s_mov_b32 s84, 0x8380\n" \
"v_mfma_f32_16x16x32_bf16 v[104:107], a[140:143], a[180:183], v[104:107]\n" \
"buffer_load_dwordx4 v22, s[64:67], s84 offen offset:0 lds\n" \
"v_mfma_f32_16x16x32_bf16 v[104:107], a[204:207], a[244:247], v[104:107]\n" \
"ds_read_b64_tr_b16 a[166:167], v33 offset:1024\n" \
"v_mfma_f32_16x16x32_bf16 v[100:103], a[140:143], a[176:179], v[100:103]\n" \
"ds_read_b64_tr_b16 a[228:229], v33 offset:8192\n" \
"v_mfma_f32_16x16x32_bf16 v[100:103], a[204:207], a[240:243], v[100:103]\n" \
"ds_read_b128 a[140:143], v28 offset:22528\n" \
"v_mfma_f32_16x16x32_bf16 v[116:119], a[144:147], a[176:179], v[116:119]\n" \
"ds_read_b128 a[204:207], v29 offset:22528\n" \
"v_mfma_f32_16x16x32_bf16 v[116:119], a[208:211], a[240:243], v[116:119]\n" \
"s_addk_i32 s84, 0x1c00\n" \
"v_mfma_f32_16x16x32_bf16 v[120:123], a[144:147], a[180:183], v[120:123]\n" \
"buffer_load_dwordx4 v23, s[64:67], s84 offen offset:1024 lds\n" \
"v_mfma_f32_16x16x32_bf16 v[120:123], a[208:211], a[244:247], v[120:123]\n" \
"s_addk_i32 s84, 0x1c00\n" \
"v_mfma_f32_16x16x32_bf16 v[124:127], a[144:147], a[184:187], v[124:127]\n" \
"buffer_load_dwordx4 v22, s[64:67], s84 offen offset:2048 lds\n" \
"v_mfma_f32_16x16x32_bf16 v[124:127], a[208:211], a[248:251], v[124:127]\n" \
"s_addk_i32 s84, 0x1c00\n" \
"s_nop 0\n" \
"v_mfma_f32_16x16x32_bf16 v[128:131], a[144:147], a[188:191], v[128:131]\n" \
"buffer_load_dwordx4 v23, s[64:67], s84 offen offset:3072 lds\n" \
"v_mfma_f32_16x16x32_bf16 v[128:131], a[208:211], a[252:255], v[128:131]\n" \
"ds_read_b128 a[144:147], v28 offset:0\n" \
"v_mfma_f32_16x16x32_bf16 v[144:147], a[148:151], a[188:191], v[144:147]\n" \
"ds_read_b128 a[208:211], v29 offset:0\n" \
"v_mfma_f32_16x16x32_bf16 v[144:147], a[212:215], a[252:255], v[144:147]\n" \
"s_add_u32 m0, s31, 0xc000\n" \
"s_mov_b32 s84, 0x380000\n" \
"v_mfma_f32_16x16x32_bf16 v[140:143], a[148:151], a[184:187], v[140:143]\n" \
"buffer_load_dwordx4 v26, s[72:75], s84 offen offset:0 lds\n" \
"v_mfma_f32_16x16x32_bf16 v[140:143], a[212:215], a[248:251], v[140:143]\n" \
"ds_read_b64_tr_b16 a[230:231], v33 offset:9216\n" \
"v_mfma_f32_16x16x32_bf16 v[136:139], a[148:151], a[180:183], v[136:139]\n" \
"s_addk_i32 s84, 0x7c00\n" \
"s_nop 0\n" \
"v_mfma_f32_16x16x32_bf16 v[136:139], a[212:215], a[244:247], v[136:139]\n" \
"buffer_load_dwordx4 v26, s[72:75], s84 offen offset:1024 lds\n" \
"v_mfma_f32_16x16x32_bf16 v[132:135], a[148:151], a[176:179], v[132:135]\n" \
"ds_read_b64_tr_b16 a[168:169], v34 offset:0\n" \
"v_mfma_f32_16x16x32_bf16 v[132:135], a[212:215], a[240:243], v[132:135]\n" \
"ds_read_b128 a[148:151], v28 offset:2048\n" \
"v_mfma_f32_16x16x32_bf16 v[148:151], a[152:155], a[176:179], v[148:151]\n" \
"ds_read_b128 a[212:215], v29 offset:2048\n" \
"v_mfma_f32_16x16x32_bf16 v[148:151], a[216:219], a[240:243], v[148:151]\n" \
"ds_read_b64_tr_b16 a[170:171], v34 offset:1024\n" \
"v_mfma_f32_16x16x32_bf16 v[152:155], a[152:155], a[180:183], v[152:155]\n" \
"ds_read_b64_tr_b16 a[232:233], v34 offset:8192\n" \
"v_mfma_f32_16x16x32_bf16 v[152:155], a[216:219], a[244:247], v[152:155]\n" \
"ds_read_b64_tr_b16 a[234:235], v34 offset:9216\n" \
"v_mfma_f32_16x16x32_bf16 v[156:159], a[152:155], a[184:187], v[156:159]\n" \
"ds_read_b64_tr_b16 a[172:173], v35 offset:0\n" \
"v_mfma_f32_16x16x32_bf16 v[156:159], a[216:219], a[248:251], v[156:159]\n" \
"ds_read_b64_tr_b16 a[174:175], v35 offset:1024\n" \
"v_mfma_f32_16x16x32_bf16 v[160:163], a[152:155], a[188:191], v[160:163]\n" \
"ds_read_b64_tr_b16 a[236:237], v35 offset:8192\n" \
"v_mfma_f32_16x16x32_bf16 v[160:163], a[216:219], a[252:255], v[160:163]\n" \
"ds_read_b128 a[152:155], v28 offset:4096\n" \
"v_mfma_f32_16x16x32_bf16 v[176:179], a[156:159], a[188:191], v[176:179]\n" \
"ds_read_b128 a[216:219], v29 offset:4096\n" \
"v_mfma_f32_16x16x32_bf16 v[176:179], a[220:223], a[252:255], v[176:179]\n" \
"ds_read_b64_tr_b16 a[238:239], v35 offset:9216\n" \
"v_mfma_f32_16x16x32_bf16 v[172:175], a[156:159], a[184:187], v[172:175]\n" \
"v_mfma_f32_16x16x32_bf16 v[172:175], a[220:223], a[248:251], v[172:175]\n" \
"v_mfma_f32_16x16x32_bf16 v[168:171], a[156:159], a[180:183], v[168:171]\n" \
"v_mfma_f32_16x16x32_bf16 v[168:171], a[220:223], a[244:247], v[168:171]\n" \
"v_mfma_f32_16x16x32_bf16 v[164:167], a[156:159], a[176:179], v[164:167]\n" \
"v_mfma_f32_16x16x32_bf16 v[164:167], a[220:223], a[240:243], v[164:167]\n" \
"ds_read_b128 a[156:159], v28 offset:6144\n" \
"ds_read_b128 a[220:223], v29 offset:6144\n" \
"s_waitcnt lgkmcnt(15)\n" \
"v_mfma_f32_16x16x32_bf16 a[0:3], a[128:131], a[160:163], a[0:3]\n" \
"s_addk_i32 s84, 0x7c00\n" \
"v_mfma_f32_16x16x32_bf16 a[0:3], a[192:195], a[224:227], a[0:3]\n" \
"buffer_load_dwordx4 v27, s[72:75], s84 offen offset:2048 lds\n" \
"v_mfma_f32_16x16x32_bf16 a[4:7], a[128:131], a[164:167], a[4:7]\n" \
"ds_read_b64_tr_b16 a[176:177], v36 offset:0\n" \
"s_waitcnt lgkmcnt(15)\n" \
"v_mfma_f32_16x16x32_bf16 a[4:7], a[192:195], a[228:231], a[4:7]\n" \
"s_addk_i32 s84, 0x7c00\n" \
"s_waitcnt lgkmcnt(11)\n" \
"v_mfma_f32_16x16x32_bf16 a[8:11], a[128:131], a[168:171], a[8:11]\n" \
"buffer_load_dwordx4 v27, s[72:75], s84 offen offset:3072 lds\n" \
"s_waitcnt lgkmcnt(9)\n" \
"v_mfma_f32_16x16x32_bf16 a[8:11], a[192:195], a[232:235], a[8:11]\n" \
"ds_read_b64_tr_b16 a[178:179], v36 offset:1024\n" \
"s_waitcnt lgkmcnt(8)\n" \
"v_mfma_f32_16x16x32_bf16 a[12:15], a[128:131], a[172:175], a[12:15]\n" \
"ds_read_b64_tr_b16 a[240:241], v36 offset:8192\n" \
"s_waitcnt lgkmcnt(5)\n" \
"v_mfma_f32_16x16x32_bf16 a[12:15], a[192:195], a[236:239], a[12:15]\n" \
"v_mfma_f32_16x16x32_bf16 a[28:31], a[132:135], a[172:175], a[28:31]\n" \
"v_mfma_f32_16x16x32_bf16 a[28:31], a[196:199], a[236:239], a[28:31]\n" \
"ds_read_b64_tr_b16 a[242:243], v36 offset:9216\n" \
"v_mfma_f32_16x16x32_bf16 a[24:27], a[132:135], a[168:171], a[24:27]\n" \
"v_mfma_f32_16x16x32_bf16 a[24:27], a[196:199], a[232:235], a[24:27]\n" \
"v_mfma_f32_16x16x32_bf16 a[20:23], a[132:135], a[164:167], a[20:23]\n" \
"v_mfma_f32_16x16x32_bf16 a[20:23], a[196:199], a[228:231], a[20:23]\n" \
"ds_read_b64_tr_b16 a[180:181], v37 offset:0\n" \
"v_mfma_f32_16x16x32_bf16 a[16:19], a[132:135], a[160:163], a[16:19]\n" \
"v_mfma_f32_16x16x32_bf16 a[16:19], a[196:199], a[224:227], a[16:19]\n" \
"v_mfma_f32_16x16x32_bf16 a[32:35], a[136:139], a[160:163], a[32:35]\n" \
"v_bfe_u32 v44, v20, 2, 5\n" \
"ds_read_b64_tr_b16 a[182:183], v37 offset:1024\n" \
"v_mfma_f32_16x16x32_bf16 a[32:35], a[200:203], a[224:227], a[32:35]\n" \
"buffer_load_dword v44, v44, s[80:83], s38 offen offset:128 sc1\n" \
"v_mfma_f32_16x16x32_bf16 a[36:39], a[136:139], a[164:167], a[36:39]\n" \
"v_mfma_f32_16x16x32_bf16 a[36:39], a[200:203], a[228:231], a[36:39]\n" \
"ds_read_b64_tr_b16 a[244:245], v37 offset:8192\n" \
"v_mfma_f32_16x16x32_bf16 a[40:43], a[136:139], a[168:171], a[40:43]\n" \
"v_mfma_f32_16x16x32_bf16 a[40:43], a[200:203], a[232:235], a[40:43]\n" \
"v_mfma_f32_16x16x32_bf16 a[44:47], a[136:139], a[172:175], a[44:47]\n" \
"ds_read_b64_tr_b16 a[246:247], v37 offset:9216\n" \
"v_mfma_f32_16x16x32_bf16 a[44:47], a[200:203], a[236:239], a[44:47]\n" \
"v_mfma_f32_16x16x32_bf16 a[60:63], a[140:143], a[172:175], a[60:63]\n" \
"global_store_dwordx4 v49, v[0:3], s[28:29] offset:0 sc1\n" \
"v_mfma_f32_16x16x32_bf16 a[60:63], a[204:207], a[236:239], a[60:63]\n" \
"v_mfma_f32_16x16x32_bf16 a[56:59], a[140:143], a[168:171], a[56:59]\n" \
"ds_read_b64_tr_b16 a[184:185], v38 offset:0\n" \
"v_mfma_f32_16x16x32_bf16 a[56:59], a[204:207], a[232:235], a[56:59]\n" \
"v_mfma_f32_16x16x32_bf16 a[52:55], a[140:143], a[164:167], a[52:55]\n" \
"v_mfma_f32_16x16x32_bf16 a[52:55], a[204:207], a[228:231], a[52:55]\n" \
"ds_read_b64_tr_b16 a[186:187], v38 offset:1024\n" \
"v_mfma_f32_16x16x32_bf16 a[48:51], a[140:143], a[160:163], a[48:51]\n" \
"v_mfma_f32_16x16x32_bf16 a[48:51], a[204:207], a[224:227], a[48:51]\n" \
"v_mfma_f32_16x16x32_bf16 a[64:67], a[144:147], a[160:163], a[64:67]\n" \
"global_store_dwordx4 v49, v[0:3], s[28:29] offset:1024 sc1\n" \
"v_mfma_f32_16x16x32_bf16 a[64:67], a[208:211], a[224:227], a[64:67]\n" \
"ds_read_b64_tr_b16 a[248:249], v38 offset:8192\n" \
"v_mfma_f32_16x16x32_bf16 a[68:71], a[144:147], a[164:167], a[68:71]\n" \
"v_mfma_f32_16x16x32_bf16 a[68:71], a[208:211], a[228:231], a[68:71]\n" \
"ds_read_b64_tr_b16 a[250:251], v38 offset:9216\n" \
"v_mfma_f32_16x16x32_bf16 a[72:75], a[144:147], a[168:171], a[72:75]\n" \
"v_mfma_f32_16x16x32_bf16 a[72:75], a[208:211], a[232:235], a[72:75]\n" \
"v_mfma_f32_16x16x32_bf16 a[76:79], a[144:147], a[172:175], a[76:79]\n" \
"v_mfma_f32_16x16x32_bf16 a[76:79], a[208:211], a[236:239], a[76:79]\n" \
"ds_read_b64_tr_b16 a[188:189], v39 offset:0\n" \
"v_mfma_f32_16x16x32_bf16 a[92:95], a[148:151], a[172:175], a[92:95]\n" \
"s_mov_b64 exec, 0xffff\n" \
"global_store_dwordx4 v49, v[0:3], s[28:29] offset:2048 sc1\n" \
"s_mov_b64 exec, -1\n" \
"s_nop 0\n" \
"v_mfma_f32_16x16x32_bf16 a[92:95], a[212:215], a[236:239], a[92:95]\n" \
"v_mfma_f32_16x16x32_bf16 a[88:91], a[148:151], a[168:171], a[88:91]\n" \
"ds_read_b64_tr_b16 a[190:191], v39 offset:1024\n" \
"v_mfma_f32_16x16x32_bf16 a[88:91], a[212:215], a[232:235], a[88:91]\n" \
"v_mfma_f32_16x16x32_bf16 a[84:87], a[148:151], a[164:167], a[84:87]\n" \
"v_mfma_f32_16x16x32_bf16 a[84:87], a[212:215], a[228:231], a[84:87]\n" \
"ds_read_b64_tr_b16 a[252:253], v39 offset:8192\n" \
"v_mfma_f32_16x16x32_bf16 a[80:83], a[148:151], a[160:163], a[80:83]\n" \
"v_mfma_f32_16x16x32_bf16 a[80:83], a[212:215], a[224:227], a[80:83]\n" \
"v_mfma_f32_16x16x32_bf16 a[96:99], a[152:155], a[160:163], a[96:99]\n" \
"ds_read_b64_tr_b16 a[254:255], v39 offset:9216\n" \
"v_mfma_f32_16x16x32_bf16 a[96:99], a[216:219], a[224:227], a[96:99]\n" \
"v_mfma_f32_16x16x32_bf16 a[100:103], a[152:155], a[164:167], a[100:103]\n" \
"v_mfma_f32_16x16x32_bf16 a[100:103], a[216:219], a[228:231], a[100:103]\n" \
"v_mfma_f32_16x16x32_bf16 a[104:107], a[152:155], a[168:171], a[104:107]\n" \
"v_mfma_f32_16x16x32_bf16 a[104:107], a[216:219], a[232:235], a[104:107]\n" \
"v_mfma_f32_16x16x32_bf16 a[108:111], a[152:155], a[172:175], a[108:111]\n" \
"v_mfma_f32_16x16x32_bf16 a[108:111], a[216:219], a[236:239], a[108:111]\n" \
"s_waitcnt lgkmcnt(15)\n" \
"v_mfma_f32_16x16x32_bf16 a[124:127], a[156:159], a[172:175], a[124:127]\n" \
"v_mfma_f32_16x16x32_bf16 a[124:127], a[220:223], a[236:239], a[124:127]\n" \
"v_mfma_f32_16x16x32_bf16 a[120:123], a[156:159], a[168:171], a[120:123]\n" \
"v_mfma_f32_16x16x32_bf16 a[120:123], a[220:223], a[232:235], a[120:123]\n" \
"v_mfma_f32_16x16x32_bf16 a[116:119], a[156:159], a[164:167], a[116:119]\n" \
"v_mfma_f32_16x16x32_bf16 a[116:119], a[220:223], a[228:231], a[116:119]\n" \
"v_mfma_f32_16x16x32_bf16 a[112:115], a[156:159], a[160:163], a[112:115]\n" \
"s_waitcnt vmcnt(3) lgkmcnt(0)\n" \
"v_mfma_f32_16x16x32_bf16 a[112:115], a[220:223], a[224:227], a[112:115]\n" \
"s_barrier\n" \
"v_cmp_ne_u32_e32 vcc, s62, v44\n" \
"s_nop 4\n" \
"s_cbranch_vccnz L_poll61_%=\n" \
"L_pollr61_%=:\n" \
"s_mov_b32 s68, s12\n" \
"s_and_b32 s69, s13, 0xffff\n" \
"s_mov_b32 s70, 0x480000\n" \
"s_mov_b32 s71, 0x20000\n" \
"v_mfma_f32_16x16x32_bf16 v[52:55], a[128:131], a[176:179], v[52:55]\n" \
"s_add_u32 m0, s40, 0x0\n" \
"s_add_u32 s85, s39, 0x0\n" \
"s_nop 0\n" \
"v_mfma_f32_16x16x32_bf16 v[52:55], a[192:195], a[240:243], v[52:55]\n" \
"buffer_load_dwordx4 v20, s[80:83], s85 offen sc1 lds\n" \
"v_mfma_f32_16x16x32_bf16 v[56:59], a[128:131], a[180:183], v[56:59]\n" \
"ds_read_b64_tr_b16 a[160:161], v32 offset:32768\n" \
"v_mfma_f32_16x16x32_bf16 v[56:59], a[192:195], a[244:247], v[56:59]\n" \
"s_add_u32 m0, s40, 0x1000\n" \
"s_add_u32 s85, s39, 0x1000\n" \
".fill (L_pb + 57056 - .) / 4, 4, 0xBF800000\n" \
"L_pp14:\n" \
"s_cbranch_execz L_pret\n" \
"s_nop 0\n" \
"v_mfma_f32_16x16x32_bf16 v[60:63], a[128:131], a[184:187], v[60:63]\n" \
"buffer_load_dwordx4 v20, s[80:83], s85 offen sc1 lds\n" \
"v_mfma_f32_16x16x32_bf16 v[60:63], a[192:195], a[248:251], v[60:63]\n" \
"ds_read_b64_tr_b16 a[162:163], v32 offset:33792\n" \
"v_mfma_f32_16x16x32_bf16 v[64:67], a[128:131], a[188:191], v[64:67]\n" \
"ds_read_b64_tr_b16 a[224:225], v32 offset:40960\n" \
"v_mfma_f32_16x16x32_bf16 v[64:67], a[192:195], a[252:255], v[64:67]\n" \
"ds_read_b128 a[128:131], v28 offset:49152\n" \
"v_mfma_f32_16x16x32_bf16 v[80:83], a[132:135], a[188:191], v[80:83]\n" \
"ds_read_b128 a[192:195], v29 offset:49152\n" \
"v_mfma_f32_16x16x32_bf16 v[80:83], a[196:199], a[252:255], v[80:83]\n" \
"s_add_u32 m0, s40, 0x3000\n" \
"s_movk_i32 s84, 0x0\n" \
"v_mfma_f32_16x16x32_bf16 v[76:79], a[132:135], a[184:187], v[76:79]\n" \
"buffer_load_dwordx4 v21, s[76:79], s84 offen offset:0 lds\n" \
"v_mfma_f32_16x16x32_bf16 v[76:79], a[196:199], a[248:251], v[76:79]\n" \
"s_add_u32 m0, s30, 0x0\n" \
"s_movk_i32 s84, 0x0\n" \
"s_nop 0\n" \
"v_mfma_f32_16x16x32_bf16 v[72:75], a[132:135], a[180:183], v[72:75]\n" \
"buffer_load_dwordx4 v24, s[76:79], s84 offen offset:0 lds\n" \
"v_mfma_f32_16x16x32_bf16 v[72:75], a[196:199], a[244:247], v[72:75]\n" \
"ds_read_b64_tr_b16 a[226:227], v32 offset:41984\n" \
"v_mfma_f32_16x16x32_bf16 v[68:71], a[132:135], a[176:179], v[68:71]\n" \
"ds_read_b64_tr_b16 a[164:165], v33 offset:32768\n" \
"v_mfma_f32_16x16x32_bf16 v[68:71], a[196:199], a[240:243], v[68:71]\n" \
"ds_read_b128 a[132:135], v28 offset:51200\n" \
"v_mfma_f32_16x16x32_bf16 v[84:87], a[136:139], a[176:179], v[84:87]\n" \
"ds_read_b128 a[196:199], v29 offset:51200\n" \
"v_mfma_f32_16x16x32_bf16 v[84:87], a[200:203], a[240:243], v[84:87]\n" \
"s_movk_i32 s84, 0x2000\n" \
"v_mfma_f32_16x16x32_bf16 v[88:91], a[136:139], a[180:183], v[88:91]\n" \
"buffer_load_dwordx4 v25, s[76:79], s84 offen offset:1024 lds\n" \
"v_mfma_f32_16x16x32_bf16 v[88:91], a[200:203], a[244:247], v[88:91]\n" \
"s_movk_i32 s84, 0x4000\n" \
"v_mfma_f32_16x16x32_bf16 v[92:95], a[136:139], a[184:187], v[92:95]\n" \
"buffer_load_dwordx4 v24, s[76:79], s84 offen offset:2048 lds\n" \
"v_mfma_f32_16x16x32_bf16 v[92:95], a[200:203], a[248:251], v[92:95]\n" \
"ds_read_b64_tr_b16 a[166:167], v33 offset:33792\n" \
"v_mfma_f32_16x16x32_bf16 v[96:99], a[136:139], a[188:191], v[96:99]\n" \
"ds_read_b64_tr_b16 a[228:229], v33 offset:40960\n" \
"v_mfma_f32_16x16x32_bf16 v[96:99], a[200:203], a[252:255], v[96:99]\n" \
"ds_read_b128 a[136:139], v28 offset:53248\n" \
"v_mfma_f32_16x16x32_bf16 v[112:115], a[140:143], a[188:191], v[112:115]\n" \
"ds_read_b128 a[200:203], v29 offset:53248\n" \
"v_mfma_f32_16x16x32_bf16 v[112:115], a[204:207], a[252:255], v[112:115]\n" \
"s_movk_i32 s84, 0x6000\n" \
"v_mfma_f32_16x16x32_bf16 v[108:111], a[140:143], a[184:187], v[108:111]\n" \
"buffer_load_dwordx4 v25, s[76:79], s84 offen offset:3072 lds\n" \
"v_mfma_f32_16x16x32_bf16 v[108:111], a[204:207], a[248:251], v[108:111]\n" \
"s_add_u32 m0, s31, 0x0\n" \
"s_movk_i32 s84, 0x0\n" \
"v_mfma_f32_16x16x32_bf16 v[104:107], a[140:143], a[180:183], v[104:107]\n" \
"buffer_load_dwordx4 v26, s[68:71], s84 offen offset:0 lds\n" \
"v_mfma_f32_16x16x32_bf16 v[104:107], a[204:207], a[244:247], v[104:107]\n" \
"s_movk_i32 s84, 0x7c00\n" \
"v_mfma_f32_16x16x32_bf16 v[100:103], a[140:143], a[176:179], v[100:103]\n" \
"buffer_load_dwordx4 v26, s[68:71], s84 offen offset:1024 lds\n" \
"v_mfma_f32_16x16x32_bf16 v[100:103], a[204:207], a[240:243], v[100:103]\n" \
"ds_read_b128 a[140:143], v28 offset:55296\n" \
"v_mfma_f32_16x16x32_bf16 v[116:119], a[144:147], a[176:179], v[116:119]\n" \
"ds_read_b128 a[204:207], v29 offset:55296\n" \
"v_mfma_f32_16x16x32_bf16 v[116:119], a[208:211], a[240:243], v[116:119]\n" \
"s_addk_i32 s84, 0x7c00\n" \
"v_mfma_f32_16x16x32_bf16 v[120:123], a[144:147], a[180:183], v[120:123]\n" \
"buffer_load_dwordx4 v27, s[68:71], s84 offen offset:2048 lds\n" \
"v_mfma_f32_16x16x32_bf16 v[120:123], a[208:211], a[244:247], v[120:123]\n" \
"ds_read_b64_tr_b16 a[230:231], v33 offset:41984\n" \
"v_mfma_f32_16x16x32_bf16 v[124:127], a[144:147], a[184:187], v[124:127]\n" \
"s_addk_i32 s84, 0x7c00\n" \
"v_mfma_f32_16x16x32_bf16 v[124:127], a[208:211], a[248:251], v[124:127]\n" \
"buffer_load_dwordx4 v27, s[68:71], s84 offen offset:3072 lds\n" \
"v_mfma_f32_16x16x32_bf16 v[128:131], a[144:147], a[188:191], v[128:131]\n" \
"ds_read_b64_tr_b16 a[168:169], v34 offset:32768\n" \
"v_mfma_f32_16x16x32_bf16 v[128:131], a[208:211], a[252:255], v[128:131]\n" \
"ds_read_b128 a[144:147], v28 offset:32768\n" \
"v_mfma_f32_16x16x32_bf16 v[144:147], a[148:151], a[188:191], v[144:147]\n" \
"ds_read_b128 a[208:211], v29 offset:32768\n" \
"v_mfma_f32_16x16x32_bf16 v[144:147], a[212:215], a[252:255], v[144:147]\n" \
"s_add_u32 m0, s30, 0x1000\n" \
"s_mov_b32 s84, 0x9000\n" \
"v_mfma_f32_16x16x32_bf16 v[140:143], a[148:151], a[184:187], v[140:143]\n" \
"buffer_load_dwordx4 v24, s[76:79], s84 offen offset:0 lds\n" \
"v_mfma_f32_16x16x32_bf16 v[140:143], a[212:215], a[248:251], v[140:143]\n" \
"ds_read_b64_tr_b16 a[170:171], v34 offset:33792\n" \
"v_mfma_f32_16x16x32_bf16 v[136:139], a[148:151], a[180:183], v[136:139]\n" \
"s_addk_i32 s84, 0x2000\n" \
"s_nop 0\n" \
"v_mfma_f32_16x16x32_bf16 v[136:139], a[212:215], a[244:247], v[136:139]\n" \
"buffer_load_dwordx4 v25, s[76:79], s84 offen offset:1024 lds\n" \
"v_mfma_f32_16x16x32_bf16 v[132:135], a[148:151], a[176:179], v[132:135]\n" \
"ds_read_b64_tr_b16 a[232:233], v34 offset:40960\n" \
"v_mfma_f32_16x16x32_bf16 v[132:135], a[212:215], a[240:243], v[132:135]\n" \
"ds_read_b128 a[148:151], v28 offset:34816\n" \
"v_mfma_f32_16x16x32_bf16 v[148:151], a[152:155], a[176:179], v[148:151]\n" \
"ds_read_b128 a[212:215], v29 offset:34816\n" \
"v_mfma_f32_16x16x32_bf16 v[148:151], a[216:219], a[240:243], v[148:151]\n" \
"ds_read_b64_tr_b16 a[234:235], v34 offset:41984\n" \
"v_mfma_f32_16x16x32_bf16 v[152:155], a[152:155], a[180:183], v[152:155]\n" \
"ds_read_b64_tr_b16 a[172:173], v35 offset:32768\n" \
"v_mfma_f32_16x16x32_bf16 v[152:155], a[216:219], a[244:247], v[152:155]\n" \
"ds_read_b64_tr_b16 a[174:175], v35 offset:33792\n" \
"v_mfma_f32_16x16x32_bf16 v[156:159], a[152:155], a[184:187], v[156:159]\n" \
"ds_read_b64_tr_b16 a[236:237], v35 offset:40960\n" \
"v_mfma_f32_16x16x32_bf16 v[156:159], a[216:219], a[248:251], v[156:159]\n" \
"ds_read_b64_tr_b16 a[238:239], v35 offset:41984\n" \
"v_mfma_f32_16x16x32_bf16 v[160:163], a[152:155], a[188:191], v[160:163]\n" \
"v_mfma_f32_16x16x32_bf16 v[160:163], a[216:219], a[252:255], v[160:163]\n" \
"ds_read_b128 a[152:155], v28 offset:36864\n" \
"v_mfma_f32_16x16x32_bf16 v[176:179], a[156:159], a[188:191], v[176:179]\n" \
"ds_read_b128 a[216:219], v29 offset:36864\n" \
"v_mfma_f32_16x16x32_bf16 v[176:179], a[220:223], a[252:255], v[176:179]\n" \
"v_mfma_f32_16x16x32_bf16 v[172:175], a[156:159], a[184:187], v[172:175]\n" \
"v_mfma_f32_16x16x32_bf16 v[172:175], a[220:223], a[248:251], v[172:175]\n" \
"v_mfma_f32_16x16x32_bf16 v[168:171], a[156:159], a[180:183], v[168:171]\n" \
"v_mfma_f32_16x16x32_bf16 v[168:171], a[220:223], a[244:247], v[168:171]\n" \
"v_mfma_f32_16x16x32_bf16 v[164:167], a[156:159], a[176:179], v[164:167]\n" \
"v_mfma_f32_16x16x32_bf16 v[164:167], a[220:223], a[240:243], v[164:167]\n" \
"ds_read_b128 a[156:159], v28 offset:38912\n" \
"ds_read_b128 a[220:223], v29 offset:38912\n" \
"s_waitcnt lgkmcnt(15)\n" \
"v_mfma_f32_16x16x32_bf16 a[0:3], a[128:131], a[160:163], a[0:3]\n" \
"s_addk_i32 s84, 0x2000\n" \
"v_mfma_f32_16x16x32_bf16 a[0:3], a[192:195], a[224:227], a[0:3]\n" \
"buffer_load_dwordx4 v24, s[76:79], s84 offen offset:2048 lds\n" \
"v_mfma_f32_16x16x32_bf16 a[4:7], a[128:131], a[164:167], a[4:7]\n" \
"ds_read_b64_tr_b16 a[176:177], v36 offset:32768\n" \
"v_mfma_f32_16x16x32_bf16 a[4:7], a[192:195], a[228:231], a[4:7]\n" \
"s_addk_i32 s84, 0x2000\n" \
"s_waitcnt lgkmcnt(13)\n" \
"v_mfma_f32_16x16x32_bf16 a[8:11], a[128:131], a[168:171], a[8:11]\n" \
"buffer_load_dwordx4 v25, s[76:79], s84 offen offset:3072 lds\n" \
"s_waitcnt lgkmcnt(9)\n" \
"v_mfma_f32_16x16x32_bf16 a[8:11], a[192:195], a[232:235], a[8:11]\n" \
"ds_read_b64_tr_b16 a[178:179], v36 offset:33792\n" \
"s_waitcnt lgkmcnt(8)\n" \
"v_mfma_f32_16x16x32_bf16 a[12:15], a[128:131], a[172:175], a[12:15]\n" \
"ds_read_b64_tr_b16 a[240:241], v36 offset:40960\n" \
"s_waitcnt lgkmcnt(7)\n" \
"v_mfma_f32_16x16x32_bf16 a[12:15], a[192:195], a[236:239], a[12:15]\n" \
"v_mfma_f32_16x16x32_bf16 a[28:31], a[132:135], a[172:175], a[28:31]\n" \
"v_mfma_f32_16x16x32_bf16 a[28:31], a[196:199], a[236:239], a[28:31]\n" \
"s_add_u32 m0, s30, 0x20000\n" \
"s_movk_i32 s84, 0x100\n" \
"v_mfma_f32_16x16x32_bf16 a[24:27], a[132:135], a[168:171], a[24:27]\n" \
"buffer_load_dwordx4 v24, s[76:79], s84 offen offset:0 lds\n" \
"v_mfma_f32_16x16x32_bf16 a[24:27], a[196:199], a[232:235], a[24:27]\n" \
"ds_read_b64_tr_b16 a[242:243], v36 offset:41984\n" \
"v_mfma_f32_16x16x32_bf16 a[20:23], a[132:135], a[164:167], a[20:23]\n" \
"v_accvgpr_read_b32 v184, a12\n" \
"v_accvgpr_read_b32 v185, a13\n" \
"v_mfma_f32_16x16x32_bf16 a[20:23], a[196:199], a[228:231], a[20:23]\n" \
"ds_read_b64_tr_b16 a[180:181], v37 offset:32768\n" \
"v_accvgpr_read_b32 v186, a14\n" \
"v_mfma_f32_16x16x32_bf16 a[16:19], a[132:135], a[160:163], a[16:19]\n" \
"s_movk_i32 s84, 0x2100\n" \
"s_nop 0\n" \
"v_mfma_f32_16x16x32_bf16 a[16:19], a[196:199], a[224:227], a[16:19]\n" \
"buffer_load_dwordx4 v25, s[76:79], s84 offen offset:1024 lds\n" \
"v_mfma_f32_16x16x32_bf16 a[32:35], a[136:139], a[160:163], a[32:35]\n" \
"ds_read_b64_tr_b16 a[182:183], v37 offset:33792\n" \
"v_accvgpr_read_b32 v187, a15\n" \
"v_mfma_f32_16x16x32_bf16 a[32:35], a[200:203], a[224:227], a[32:35]\n" \
"v_accvgpr_read_b32 v180, a28\n" \
"v_accvgpr_read_b32 v181, a29\n" \
"v_mfma_f32_16x16x32_bf16 a[36:39], a[136:139], a[164:167], a[36:39]\n" \
"v_accvgpr_read_b32 v182, a30\n" \
"v_accvgpr_read_b32 v183, a31\n" \
"v_mfma_f32_16x16x32_bf16 a[36:39], a[200:203], a[228:231], a[36:39]\n" \
"s_movk_i32 s84, 0x4100\n" \
"s_nop 0\n" \
"v_mfma_f32_16x16x32_bf16 a[40:43], a[136:139], a[168:171], a[40:43]\n" \
"buffer_load_dwordx4 v24, s[76:79], s84 offen offset:2048 lds\n" \
"v_mfma_f32_16x16x32_bf16 a[40:43], a[200:203], a[232:235], a[40:43]\n" \
"ds_read_b64_tr_b16 a[244:245], v37 offset:40960\n" \
"v_cvt_pk_bf16_f32 v184, v184, v185\n" \
"v_mfma_f32_16x16x32_bf16 a[44:47], a[136:139], a[172:175], a[44:47]\n" \
"ds_read_b64_tr_b16 a[246:247], v37 offset:41984\n" \
"v_cvt_pk_bf16_f32 v185, v186, v187\n" \
"v_mfma_f32_16x16x32_bf16 a[44:47], a[200:203], a[236:239], a[44:47]\n" \
"v_cvt_pk_bf16_f32 v186, v180, v181\n" \
"v_cvt_pk_bf16_f32 v187, v182, v183\n" \
"v_mfma_f32_16x16x32_bf16 a[60:63], a[140:143], a[172:175], a[60:63]\n" \
"s_movk_i32 s84, 0x6100\n" \
"v_mfma_f32_16x16x32_bf16 a[60:63], a[204:207], a[236:239], a[60:63]\n" \
"buffer_load_dwordx4 v25, s[76:79], s84 offen offset:3072 lds\n" \
"v_mfma_f32_16x16x32_bf16 a[56:59], a[140:143], a[168:171], a[56:59]\n" \
"ds_read_b64_tr_b16 a[184:185], v38 offset:32768\n" \
"v_accvgpr_read_b32 v188, a8\n" \
"v_mfma_f32_16x16x32_bf16 a[56:59], a[204:207], a[232:235], a[56:59]\n" \
"v_accvgpr_read_b32 v189, a9\n" \
"v_accvgpr_read_b32 v190, a10\n" \
"v_mfma_f32_16x16x32_bf16 a[52:55], a[140:143], a[164:167], a[52:55]\n" \
"s_add_u32 m0, s30, 0x21000\n" \
"s_addk_i32 s84, 0x3000\n" \
"v_mfma_f32_16x16x32_bf16 a[52:55], a[204:207], a[228:231], a[52:55]\n" \
"buffer_load_dwordx4 v24, s[76:79], s84 offen offset:0 lds\n" \
"v_mfma_f32_16x16x32_bf16 a[48:51], a[140:143], a[160:163], a[48:51]\n" \
"ds_read_b64_tr_b16 a[186:187], v38 offset:33792\n" \
"v_accvgpr_read_b32 v191, a11\n" \
"v_mfma_f32_16x16x32_bf16 a[48:51], a[204:207], a[224:227], a[48:51]\n" \
"v_accvgpr_read_b32 v180, a24\n" \
"v_accvgpr_read_b32 v181, a25\n" \
"v_mfma_f32_16x16x32_bf16 a[64:67], a[144:147], a[160:163], a[64:67]\n" \
"ds_read_b64_tr_b16 a[248:249], v38 offset:40960\n" \
"v_accvgpr_read_b32 v182, a26\n" \
"v_mfma_f32_16x16x32_bf16 a[64:67], a[208:211], a[224:227], a[64:67]\n" \
"s_addk_i32 s84, 0x2000\n" \
"s_nop 0\n" \
"v_mfma_f32_16x16x32_bf16 a[68:71], a[144:147], a[164:167], a[68:71]\n" \
"buffer_load_dwordx4 v25, s[76:79], s84 offen offset:1024 lds\n" \
"v_mfma_f32_16x16x32_bf16 a[68:71], a[208:211], a[228:231], a[68:71]\n" \
"ds_read_b64_tr_b16 a[250:251], v38 offset:41984\n" \
"v_accvgpr_read_b32 v183, a27\n" \
"v_mfma_f32_16x16x32_bf16 a[72:75], a[144:147], a[168:171], a[72:75]\n" \
"v_cvt_pk_bf16_f32 v188, v188, v189\n" \
"v_cvt_pk_bf16_f32 v189, v190, v191\n" \
"v_mfma_f32_16x16x32_bf16 a[72:75], a[208:211], a[232:235], a[72:75]\n" \
"v_cvt_pk_bf16_f32 v190, v180, v181\n" \
"v_cvt_pk_bf16_f32 v191, v182, v183\n" \
"v_mfma_f32_16x16x32_bf16 a[76:79], a[144:147], a[172:175], a[76:79]\n" \
"s_addk_i32 s84, 0x2000\n" \
"v_mfma_f32_16x16x32_bf16 a[76:79], a[208:211], a[236:239], a[76:79]\n" \
"buffer_load_dwordx4 v24, s[76:79], s84 offen offset:2048 lds\n" \
"v_mfma_f32_16x16x32_bf16 a[92:95], a[148:151], a[172:175], a[92:95]\n" \
"s_add_u32 s89, s41, 0x0\n" \
"buffer_store_dwordx4 v[184:187], v20, s[80:83], s89 offen offset:3072 sc1\n" \
"v_mfma_f32_16x16x32_bf16 a[92:95], a[212:215], a[236:239], a[92:95]\n" \
"ds_read_b64_tr_b16 a[188:189], v39 offset:32768\n" \
"v_accvgpr_read_b32 v192, a4\n" \
"v_mfma_f32_16x16x32_bf16 a[88:91], a[148:151], a[168:171], a[88:91]\n" \
"ds_read_b64_tr_b16 a[190:191], v39 offset:33792\n" \
"v_accvgpr_read_b32 v193, a5\n" \
"v_mfma_f32_16x16x32_bf16 a[88:91], a[212:215], a[232:235], a[88:91]\n" \
"s_addk_i32 s84, 0x2000\n" \
"v_mfma_f32_16x16x32_bf16 a[84:87], a[148:151], a[164:167], a[84:87]\n" \
"buffer_load_dwordx4 v25, s[76:79], s84 offen offset:3072 lds\n" \
"v_mfma_f32_16x16x32_bf16 a[84:87], a[212:215], a[228:231], a[84:87]\n" \
"ds_read_b64_tr_b16 a[252:253], v39 offset:40960\n" \
"v_accvgpr_read_b32 v194, a6\n" \
"v_mfma_f32_16x16x32_bf16 a[80:83], a[148:151], a[160:163], a[80:83]\n" \
"s_add_u32 s88, s41, 0x0\n" \
"buffer_store_dwordx4 v[188:191], v20, s[80:83], s88 offen offset:2048 sc1\n" \
"v_mfma_f32_16x16x32_bf16 a[80:83], a[212:215], a[224:227], a[80:83]\n" \
"v_accvgpr_read_b32 v195, a7\n" \
"v_accvgpr_read_b32 v180, a20\n" \
"s_waitcnt lgkmcnt(15)\n" \
"s_nop 0\n" \
"v_mfma_f32_16x16x32_bf16 a[96:99], a[152:155], a[160:163], a[96:99]\n" \
"ds_read_b64_tr_b16 a[254:255], v39 offset:41984\n" \
"v_accvgpr_read_b32 v181, a21\n" \
"v_mfma_f32_16x16x32_bf16 a[96:99], a[216:219], a[224:227], a[96:99]\n" \
"v_accvgpr_read_b32 v182, a22\n" \
"v_accvgpr_read_b32 v183, a23\n" \
"v_mfma_f32_16x16x32_bf16 a[100:103], a[152:155], a[164:167], a[100:103]\n" \
"v_cvt_pk_bf16_f32 v192, v192, v193\n" \
"v_cvt_pk_bf16_f32 v193, v194, v195\n" \
"v_mfma_f32_16x16x32_bf16 a[100:103], a[216:219], a[228:231], a[100:103]\n" \
"v_cvt_pk_bf16_f32 v194, v180, v181\n" \
"v_cvt_pk_bf16_f32 v195, v182, v183\n" \
"v_mfma_f32_16x16x32_bf16 a[104:107], a[152:155], a[168:171], a[104:107]\n" \
"s_add_u32 s89, s41, 0x0\n" \
"s_nop 0\n" \
"buffer_store_dwordx4 v[192:195], v20, s[80:83], s89 offen offset:1024 sc1\n" \
"v_mfma_f32_16x16x32_bf16 a[104:107], a[216:219], a[232:235], a[104:107]\n" \
"v_accvgpr_read_b32 v196, a0\n" \
"v_accvgpr_read_b32 v197, a1\n" \
"v_mfma_f32_16x16x32_bf16 a[108:111], a[152:155], a[172:175], a[108:111]\n" \
"v_accvgpr_read_b32 v198, a2\n" \
"v_accvgpr_read_b32 v199, a3\n" \
"v_mfma_f32_16x16x32_bf16 a[108:111], a[216:219], a[236:239], a[108:111]\n" \
"v_accvgpr_read_b32 v180, a16\n" \
"v_accvgpr_read_b32 v181, a17\n" \
"v_mfma_f32_16x16x32_bf16 a[124:127], a[156:159], a[172:175], a[124:127]\n" \
"v_accvgpr_read_b32 v182, a18\n" \
"v_accvgpr_read_b32 v183, a19\n" \
"v_mfma_f32_16x16x32_bf16 a[124:127], a[220:223], a[236:239], a[124:127]\n" \
"v_cvt_pk_bf16_f32 v196, v196, v197\n" \
"v_cvt_pk_bf16_f32 v197, v198, v199\n" \
"v_mfma_f32_16x16x32_bf16 a[120:123], a[156:159], a[168:171], a[120:123]\n" \
"v_cvt_pk_bf16_f32 v198, v180, v181\n" \
"v_cvt_pk_bf16_f32 v199, v182, v183\n" \
"s_add_u32 s88, s41, 0x0\n" \
"s_nop 0\n" \
"buffer_store_dwordx4 v[196:199], v20, s[80:83], s88 offen offset:0 sc1\n" \
"v_mfma_f32_16x16x32_bf16 a[120:123], a[220:223], a[232:235], a[120:123]\n" \
"v_accvgpr_read_b32 v200, a44\n" \
"v_accvgpr_read_b32 v201, a45\n" \
"v_mfma_f32_16x16x32_bf16 a[116:119], a[156:159], a[164:167], a[116:119]\n" \
"v_accvgpr_read_b32 v202, a46\n" \
"v_accvgpr_read_b32 v203, a47\n" \
"v_mfma_f32_16x16x32_bf16 a[116:119], a[220:223], a[228:231], a[116:119]\n" \
"v_accvgpr_read_b32 v180, a60\n" \
"v_accvgpr_read_b32 v181, a61\n" \
"v_mfma_f32_16x16x32_bf16 a[112:115], a[156:159], a[160:163], a[112:115]\n" \
"v_accvgpr_read_b32 v182, a62\n" \
"v_accvgpr_read_b32 v183, a63\n" \
"v_mfma_f32_16x16x32_bf16 a[112:115], a[220:223], a[224:227], a[112:115]\n" \
"v_cvt_pk_bf16_f32 v200, v200, v201\n" \
"v_cvt_pk_bf16_f32 v201, v202, v203\n" \
"s_waitcnt vmcnt(12) lgkmcnt(0)\n" \
"s_barrier\n" \
"v_xor_b32_e32 v36, 0x4000, v36\n" \
"v_xor_b32_e64 v32, 0x0, v48\n" \
"v_xor_b32_e32 v37, 0x4000, v37\n" \
"v_xor_b32_e64 v33, 0x20, v48\n" \
"v_xor_b32_e32 v38, 0x4000, v38\n" \
"v_xor_b32_e64 v34, 0x40, v48\n" \
"v_xor_b32_e32 v39, 0x4000, v39\n" \
"v_xor_b32_e32 v35, 0x60, v48\n" \
"v_add_u32_e32 v28, s43, v28\n" \
"v_add_u32_e32 v29, s43, v29\n" \
"v_mfma_f32_16x16x32_bf16 v[116:119], a[144:147], a[176:179], v[116:119]\n" \
"s_add_u32 m0, s40, 0x8000\n" \
"s_add_u32 s85, s39, 0x2000\n" \
"v_mfma_f32_16x16x32_bf16 v[116:119], a[208:211], a[240:243], v[116:119]\n" \
"buffer_load_dwordx4 v20, s[80:83], s85 offen sc1 lds\n" \
"v_mfma_f32_16x16x32_bf16 v[120:123], a[144:147], a[180:183], v[120:123]\n" \
"ds_read_b64_tr_b16 a[160:161], v32 offset:0\n" \
"v_cvt_pk_bf16_f32 v202, v180, v181\n" \
"v_mfma_f32_16x16x32_bf16 v[120:123], a[208:211], a[244:247], v[120:123]\n" \
"s_add_u32 m0, s40, 0x9000\n" \
"s_add_u32 s85, s39, 0x3000\n" \
"v_mfma_f32_16x16x32_bf16 v[124:127], a[144:147], a[184:187], v[124:127]\n" \
"buffer_load_dwordx4 v20, s[80:83], s85 offen sc1 lds\n" \
"v_mfma_f32_16x16x32_bf16 v[124:127], a[208:211], a[248:251], v[124:127]\n" \
"ds_read_b64_tr_b16 a[162:163], v32 offset:512\n" \
"v_cvt_pk_bf16_f32 v203, v182, v183\n" \
"v_mfma_f32_16x16x32_bf16 v[128:131], a[144:147], a[188:191], v[128:131]\n" \
"ds_read_b64_tr_b16 a[224:225], v32 offset:4096\n" \
"v_accvgpr_read_b32 v204, a40\n" \
"v_mfma_f32_16x16x32_bf16 v[128:131], a[208:211], a[252:255], v[128:131]\n" \
"ds_read_b128 a[144:147], v28 offset:8192\n" \
"v_accvgpr_read_b32 v205, a41\n" \
"v_mfma_f32_16x16x32_bf16 v[144:147], a[148:151], a[188:191], v[144:147]\n" \
"ds_read_b128 a[208:211], v29 offset:8192\n" \
"v_accvgpr_read_b32 v206, a42\n" \
"v_mfma_f32_16x16x32_bf16 v[144:147], a[212:215], a[252:255], v[144:147]\n" \
"s_add_u32 m0, s40, 0xb000\n" \
"s_movk_i32 s84, 0x80\n" \
"v_mfma_f32_16x16x32_bf16 v[140:143], a[148:151], a[184:187], v[140:143]\n" \
"buffer_load_dwordx4 v21, s[76:79], s84 offen offset:0 lds\n" \
"v_mfma_f32_16x16x32_bf16 v[140:143], a[212:215], a[248:251], v[140:143]\n" \
"s_add_u32 m0, s30, 0x8000\n" \
"s_movk_i32 s84, 0x80\n" \
"v_mfma_f32_16x16x32_bf16 v[136:139], a[148:151], a[180:183], v[136:139]\n" \
"buffer_load_dwordx4 v24, s[76:79], s84 offen offset:0 lds\n" \
"v_mfma_f32_16x16x32_bf16 v[136:139], a[212:215], a[244:247], v[136:139]\n" \
"ds_read_b64_tr_b16 a[226:227], v32 offset:4608\n" \
"v_accvgpr_read_b32 v207, a43\n" \
"v_mfma_f32_16x16x32_bf16 v[132:135], a[148:151], a[176:179], v[132:135]\n" \
"ds_read_b64_tr_b16 a[164:165], v33 offset:0\n" \
"v_accvgpr_read_b32 v180, a56\n" \
"v_mfma_f32_16x16x32_bf16 v[132:135], a[212:215], a[240:243], v[132:135]\n" \
"ds_read_b128 a[148:151], v28 offset:10240\n" \
"v_accvgpr_read_b32 v181, a57\n" \
"v_mfma_f32_16x16x32_bf16 v[148:151], a[152:155], a[176:179], v[148:151]\n" \
"ds_read_b128 a[212:215], v29 offset:10240\n" \
"v_accvgpr_read_b32 v182, a58\n" \
"v_mfma_f32_16x16x32_bf16 v[148:151], a[216:219], a[240:243], v[148:151]\n" \
"s_movk_i32 s84, 0x2080\n" \
"v_mfma_f32_16x16x32_bf16 v[152:155], a[152:155], a[180:183], v[152:155]\n" \
"buffer_load_dwordx4 v25, s[76:79], s84 offen offset:1024 lds\n" \
"v_mfma_f32_16x16x32_bf16 v[152:155], a[216:219], a[244:247], v[152:155]\n" \
"s_movk_i32 s84, 0x4080\n" \
"v_mfma_f32_16x16x32_bf16 v[156:159], a[152:155], a[184:187], v[156:159]\n" \
"buffer_load_dwordx4 v24, s[76:79], s84 offen offset:2048 lds\n" \
"v_mfma_f32_16x16x32_bf16 v[156:159], a[216:219], a[248:251], v[156:159]\n" \
"ds_read_b64_tr_b16 a[166:167], v33 offset:512\n" \
"v_accvgpr_read_b32 v183, a59\n" \
"v_mfma_f32_16x16x32_bf16 v[160:163], a[152:155], a[188:191], v[160:163]\n" \
"ds_read_b64_tr_b16 a[228:229], v33 offset:4096\n" \
"v_cvt_pk_bf16_f32 v204, v204, v205\n" \
"v_mfma_f32_16x16x32_bf16 v[160:163], a[216:219], a[252:255], v[160:163]\n" \
"ds_read_b128 a[152:155], v28 offset:12288\n" \
"v_cvt_pk_bf16_f32 v205, v206, v207\n" \
"v_mfma_f32_16x16x32_bf16 v[176:179], a[156:159], a[188:191], v[176:179]\n" \
"ds_read_b128 a[216:219], v29 offset:12288\n" \
"v_cvt_pk_bf16_f32 v206, v180, v181\n" \
"v_mfma_f32_16x16x32_bf16 v[176:179], a[220:223], a[252:255], v[176:179]\n" \
"s_movk_i32 s84, 0x6080\n" \
"v_mfma_f32_16x16x32_bf16 v[172:175], a[156:159], a[184:187], v[172:175]\n" \
"buffer_load_dwordx4 v25, s[76:79], s84 offen offset:3072 lds\n" \
"v_mfma_f32_16x16x32_bf16 v[172:175], a[220:223], a[248:251], v[172:175]\n" \
"s_add_u32 m0, s31, 0x8000\n" \
"s_mov_b32 s84, 0x80000\n" \
"v_mfma_f32_16x16x32_bf16 v[168:171], a[156:159], a[180:183], v[168:171]\n" \
"buffer_load_dwordx4 v26, s[68:71], s84 offen offset:0 lds\n" \
"v_mfma_f32_16x16x32_bf16 v[168:171], a[220:223], a[244:247], v[168:171]\n" \
"s_addk_i32 s84, 0x7c00\n" \
"v_mfma_f32_16x16x32_bf16 v[164:167], a[156:159], a[176:179], v[164:167]\n" \
"buffer_load_dwordx4 v26, s[68:71], s84 offen offset:1024 lds\n" \
"v_mfma_f32_16x16x32_bf16 v[164:167], a[220:223], a[240:243], v[164:167]\n" \
"ds_read_b128 a[156:159], v28 offset:14336\n" \
"v_cvt_pk_bf16_f32 v207, v182, v183\n" \
"v_mfma_f32_16x16x32_bf16 v[84:87], a[136:139], a[176:179], v[84:87]\n" \
"ds_read_b128 a[220:223], v29 offset:14336\n" \
"v_accvgpr_read_b32 v208, a36\n" \
"v_mfma_f32_16x16x32_bf16 v[84:87], a[200:203], a[240:243], v[84:87]\n" \
"s_addk_i32 s84, 0x7c00\n" \
"v_mfma_f32_16x16x32_bf16 v[88:91], a[136:139], a[180:183], v[88:91]\n" \
"buffer_load_dwordx4 v27, s[68:71], s84 offen offset:2048 lds\n" \
"v_mfma_f32_16x16x32_bf16 v[88:91], a[200:203], a[244:247], v[88:91]\n" \
"ds_read_b64_tr_b16 a[230:231], v33 offset:4608\n" \
"v_accvgpr_read_b32 v209, a37\n" \
"v_mfma_f32_16x16x32_bf16 v[92:95], a[136:139], a[184:187], v[92:95]\n" \
"s_addk_i32 s84, 0x7c00\n" \
"v_mfma_f32_16x16x32_bf16 v[92:95], a[200:203], a[248:251], v[92:95]\n" \
"buffer_load_dwordx4 v27, s[68:71], s84 offen offset:3072 lds\n" \
"v_mfma_f32_16x16x32_bf16 v[96:99], a[136:139], a[188:191], v[96:99]\n" \
"ds_read_b64_tr_b16 a[172:173], v35 offset:0\n" \
"v_accvgpr_read_b32 v210, a38\n" \
"v_mfma_f32_16x16x32_bf16 v[96:99], a[200:203], a[252:255], v[96:99]\n" \
"ds_read_b128 a[136:139], v28 offset:4096\n" \
"v_accvgpr_read_b32 v211, a39\n" \
"v_mfma_f32_16x16x32_bf16 v[112:115], a[140:143], a[188:191], v[112:115]\n" \
"ds_read_b128 a[200:203], v29 offset:4096\n" \
"v_accvgpr_read_b32 v180, a52\n" \
"v_mfma_f32_16x16x32_bf16 v[112:115], a[204:207], a[252:255], v[112:115]\n" \
"s_add_u32 m0, s30, 0x9000\n" \
"s_mov_b32 s84, 0x9080\n" \
"v_mfma_f32_16x16x32_bf16 v[108:111], a[140:143], a[184:187], v[108:111]\n" \
"buffer_load_dwordx4 v24, s[76:79], s84 offen offset:0 lds\n" \
"v_mfma_f32_16x16x32_bf16 v[108:111], a[204:207], a[248:251], v[108:111]\n" \
"ds_read_b64_tr_b16 a[174:175], v35 offset:512\n" \
"v_accvgpr_read_b32 v181, a53\n" \
"v_mfma_f32_16x16x32_bf16 v[104:107], a[140:143], a[180:183], v[104:107]\n" \
"s_addk_i32 s84, 0x2000\n" \
"s_nop 0\n" \
"v_mfma_f32_16x16x32_bf16 v[104:107], a[204:207], a[244:247], v[104:107]\n" \
"buffer_load_dwordx4 v25, s[76:79], s84 offen offset:1024 lds\n" \
"v_mfma_f32_16x16x32_bf16 v[100:103], a[140:143], a[176:179], v[100:103]\n" \
"ds_read_b64_tr_b16 a[236:237], v35 offset:4096\n" \
"v_accvgpr_read_b32 v182, a54\n" \
"v_mfma_f32_16x16x32_bf16 v[100:103], a[204:207], a[240:243], v[100:103]\n" \
"ds_read_b128 a[140:143], v28 offset:6144\n" \
"v_accvgpr_read_b32 v183, a55\n" \
"v_mfma_f32_16x16x32_bf16 v[52:55], a[128:131], a[176:179], v[52:55]\n" \
"ds_read_b128 a[204:207], v29 offset:6144\n" \
"v_cvt_pk_bf16_f32 v208, v208, v209\n" \
"v_mfma_f32_16x16x32_bf16 v[52:55], a[192:195], a[240:243], v[52:55]\n" \
"ds_read_b64_tr_b16 a[238:239], v35 offset:4608\n" \
"v_cvt_pk_bf16_f32 v209, v210, v211\n" \
"v_mfma_f32_16x16x32_bf16 v[56:59], a[128:131], a[180:183], v[56:59]\n" \
"ds_read_b64_tr_b16 a[168:169], v34 offset:0\n" \
"v_cvt_pk_bf16_f32 v210, v180, v181\n" \
"v_mfma_f32_16x16x32_bf16 v[56:59], a[192:195], a[244:247], v[56:59]\n" \
"ds_read_b64_tr_b16 a[170:171], v34 offset:512\n" \
"v_cvt_pk_bf16_f32 v211, v182, v183\n" \
"v_mfma_f32_16x16x32_bf16 v[60:63], a[128:131], a[184:187], v[60:63]\n" \
"ds_read_b64_tr_b16 a[232:233], v34 offset:4096\n" \
"v_accvgpr_read_b32 v212, a32\n" \
"v_mfma_f32_16x16x32_bf16 v[60:63], a[192:195], a[248:251], v[60:63]\n" \
"ds_read_b64_tr_b16 a[234:235], v34 offset:4608\n" \
"v_accvgpr_read_b32 v213, a33\n" \
"v_mfma_f32_16x16x32_bf16 v[64:67], a[128:131], a[188:191], v[64:67]\n" \
"v_accvgpr_read_b32 v214, a34\n" \
"v_accvgpr_read_b32 v215, a35\n" \
"v_mfma_f32_16x16x32_bf16 v[64:67], a[192:195], a[252:255], v[64:67]\n" \
"ds_read_b128 a[128:131], v28 offset:0\n" \
"v_accvgpr_read_b32 v180, a48\n" \
"v_mfma_f32_16x16x32_bf16 v[80:83], a[132:135], a[188:191], v[80:83]\n" \
"ds_read_b128 a[192:195], v29 offset:0\n" \
"v_accvgpr_read_b32 v181, a49\n" \
"v_mfma_f32_16x16x32_bf16 v[80:83], a[196:199], a[252:255], v[80:83]\n" \
"v_accvgpr_read_b32 v182, a50\n" \
"v_accvgpr_read_b32 v183, a51\n" \
"v_mfma_f32_16x16x32_bf16 v[76:79], a[132:135], a[184:187], v[76:79]\n" \
"v_cvt_pk_bf16_f32 v212, v212, v213\n" \
"v_cvt_pk_bf16_f32 v213, v214, v215\n" \
"v_mfma_f32_16x16x32_bf16 v[76:79], a[196:199], a[248:251], v[76:79]\n" \
"v_cvt_pk_bf16_f32 v214, v180, v181\n" \
"v_cvt_pk_bf16_f32 v215, v182, v183\n" \
"v_mfma_f32_16x16x32_bf16 v[72:75], a[132:135], a[180:183], v[72:75]\n" \
"v_accvgpr_read_b32 v216, a68\n" \
"v_accvgpr_read_b32 v217, a69\n" \
"v_mfma_f32_16x16x32_bf16 v[72:75], a[196:199], a[244:247], v[72:75]\n" \
"v_accvgpr_read_b32 v218, a70\n" \
"v_accvgpr_read_b32 v219, a71\n" \
"v_mfma_f32_16x16x32_bf16 v[68:71], a[132:135], a[176:179], v[68:71]\n" \
"v_accvgpr_read_b32 v180, a84\n" \
"v_accvgpr_read_b32 v181, a85\n" \
"v_mfma_f32_16x16x32_bf16 v[68:71], a[196:199], a[240:243], v[68:71]\n" \
"ds_read_b128 a[132:135], v28 offset:2048\n" \
"ds_read_b128 a[196:199], v29 offset:2048\n" \
"s_waitcnt lgkmcnt(15)\n" \
"v_mfma_f32_16x16x32_bf16 a[0:3], a[160:163], a[144:147], 0\n" \
"s_addk_i32 s84, 0x2000\n" \
"v_accvgpr_read_b32 v182, a86\n" \
"v_mfma_f32_16x16x32_bf16 a[0:3], a[224:227], a[208:211], a[0:3]\n" \
"buffer_load_dwordx4 v24, s[76:79], s84 offen offset:2048 lds\n" \
"v_accvgpr_read_b32 v183, a87\n" \
"v_mfma_f32_16x16x32_bf16 a[4:7], a[164:167], a[144:147], 0\n" \
"ds_read_b64_tr_b16 a[176:177], v36 offset:0\n" \
"v_cvt_pk_bf16_f32 v216, v216, v217\n" \
"v_mfma_f32_16x16x32_bf16 a[4:7], a[228:231], a[208:211], a[4:7]\n" \
"s_addk_i32 s84, 0x2000\n" \
"v_cvt_pk_bf16_f32 v217, v218, v219\n" \
"s_waitcnt lgkmcnt(13)\n" \
"v_mfma_f32_16x16x32_bf16 a[12:15], a[172:175], a[144:147], 0\n" \
"buffer_load_dwordx4 v25, s[76:79], s84 offen offset:3072 lds\n" \
"v_cvt_pk_bf16_f32 v218, v180, v181\n" \
"s_waitcnt lgkmcnt(9)\n" \
"v_mfma_f32_16x16x32_bf16 a[12:15], a[236:239], a[208:211], a[12:15]\n" \
"ds_read_b64_tr_b16 a[178:179], v36 offset:1024\n" \
"v_cvt_pk_bf16_f32 v219, v182, v183\n" \
"s_waitcnt lgkmcnt(8)\n" \
"v_mfma_f32_16x16x32_bf16 a[8:11], a[144:147], a[168:171], 0\n" \
"ds_read_b64_tr_b16 a[240:241], v36 offset:8192\n" \
"v_accvgpr_read_b32 v220, a64\n" \
"s_waitcnt lgkmcnt(7)\n" \
"s_nop 0\n" \
"v_mfma_f32_16x16x32_bf16 a[8:11], a[208:211], a[232:235], a[8:11]\n" \
"ds_read_b64_tr_b16 a[242:243], v36 offset:9216\n" \
"v_accvgpr_read_b32 v221, a65\n" \
"v_mfma_f32_16x16x32_bf16 a[24:27], a[148:151], a[168:171], 0\n" \
"ds_read_b64_tr_b16 a[180:181], v37 offset:0\n" \
"v_accvgpr_read_b32 v222, a66\n" \
"v_mfma_f32_16x16x32_bf16 a[24:27], a[212:215], a[232:235], a[24:27]\n" \
"ds_read_b64_tr_b16 a[182:183], v37 offset:1024\n" \
"v_accvgpr_read_b32 v223, a67\n" \
"v_mfma_f32_16x16x32_bf16 a[28:31], a[172:175], a[148:151], 0\n" \
"ds_read_b64_tr_b16 a[244:245], v37 offset:8192\n" \
"v_accvgpr_read_b32 v180, a80\n" \
"v_mfma_f32_16x16x32_bf16 a[28:31], a[236:239], a[212:215], a[28:31]\n" \
"s_add_u32 s89, s41, 0x2000\n" \
"buffer_store_dwordx4 v[200:203], v20, s[80:83], s89 offen offset:3072 sc1\n" \
"v_mfma_f32_16x16x32_bf16 a[20:23], a[164:167], a[148:151], 0\n" \
"ds_read_b64_tr_b16 a[246:247], v37 offset:9216\n" \
"v_accvgpr_read_b32 v181, a81\n" \
"v_mfma_f32_16x16x32_bf16 a[20:23], a[228:231], a[212:215], a[20:23]\n" \
"ds_read_b64_tr_b16 a[184:185], v38 offset:0\n" \
"v_accvgpr_read_b32 v182, a82\n" \
"v_mfma_f32_16x16x32_bf16 a[16:19], a[160:163], a[148:151], 0\n" \
"ds_read_b64_tr_b16 a[186:187], v38 offset:1024\n" \
"v_accvgpr_read_b32 v183, a83\n" \
"v_mfma_f32_16x16x32_bf16 a[16:19], a[224:227], a[212:215], a[16:19]\n" \
"v_cvt_pk_bf16_f32 v220, v220, v221\n" \
"v_cvt_pk_bf16_f32 v221, v222, v223\n" \
"v_cvt_pk_bf16_f32 v222, v180, v181\n" \
"v_mfma_f32_16x16x32_bf16 a[32:35], a[160:163], a[152:155], 0\n" \
"s_add_u32 s88, s41, 0x2000\n" \
".fill (L_pb + 61152 - .) / 4, 4, 0xBF800000\n" \
"L_pp15:\n" \
"s_cbranch_execz L_pret\n" \
"s_nop 0\n" \
"buffer_store_dwordx4 v[204:207], v20, s[80:83], s88 offen offset:2048 sc1\n" \
"v_mfma_f32_16x16x32_bf16 a[32:35], a[224:227], a[216:219], a[32:35]\n" \
"ds_read_b64_tr_b16 a[248:249], v38 offset:8192\n" \
"v_cvt_pk_bf16_f32 v223, v182, v183\n" \
"v_mfma_f32_16x16x32_bf16 a[36:39], a[164:167], a[152:155], 0\n" \
"ds_read_b64_tr_b16 a[250:251], v38 offset:9216\n" \
"v_cvt_pk_bf16_f32 v224, v128, v129\n" \
"v_mfma_f32_16x16x32_bf16 a[36:39], a[228:231], a[216:219], a[36:39]\n" \
"ds_read_b64_tr_b16 a[188:189], v39 offset:0\n" \
"v_cvt_pk_bf16_f32 v225, v130, v131\n" \
"v_mfma_f32_16x16x32_bf16 a[44:47], a[172:175], a[152:155], 0\n" \
"v_cvt_pk_bf16_f32 v226, v144, v145\n" \
"v_cvt_pk_bf16_f32 v227, v146, v147\n" \
"v_cvt_pk_bf16_f32 v228, v124, v125\n" \
"v_mfma_f32_16x16x32_bf16 a[44:47], a[236:239], a[216:219], a[44:47]\n" \
"ds_read_b64_tr_b16 a[190:191], v39 offset:1024\n" \
"v_cvt_pk_bf16_f32 v229, v126, v127\n" \
"v_mfma_f32_16x16x32_bf16 a[40:43], a[152:155], a[168:171], 0\n" \
"s_add_u32 s89, s41, 0x2000\n" \
"buffer_store_dwordx4 v[208:211], v20, s[80:83], s89 offen offset:1024 sc1\n" \
"v_mfma_f32_16x16x32_bf16 a[40:43], a[216:219], a[232:235], a[40:43]\n" \
"ds_read_b64_tr_b16 a[252:253], v39 offset:8192\n" \
"v_cvt_pk_bf16_f32 v230, v140, v141\n" \
"v_mfma_f32_16x16x32_bf16 a[56:59], a[156:159], a[168:171], 0\n" \
"ds_read_b64_tr_b16 a[254:255], v39 offset:9216\n" \
"v_cvt_pk_bf16_f32 v231, v142, v143\n" \
"v_mfma_f32_16x16x32_bf16 a[56:59], a[220:223], a[232:235], a[56:59]\n" \
"v_cvt_pk_bf16_f32 v232, v120, v121\n" \
"v_cvt_pk_bf16_f32 v233, v122, v123\n" \
"v_cvt_pk_bf16_f32 v234, v136, v137\n" \
"v_mfma_f32_16x16x32_bf16 a[60:63], a[172:175], a[156:159], 0\n" \
"v_cvt_pk_bf16_f32 v235, v138, v139\n" \
"v_cvt_pk_bf16_f32 v236, v116, v117\n" \
"v_cvt_pk_bf16_f32 v237, v118, v119\n" \
"v_mfma_f32_16x16x32_bf16 a[60:63], a[236:239], a[220:223], a[60:63]\n" \
"s_add_u32 s88, s41, 0x2000\n" \
"buffer_store_dwordx4 v[212:215], v20, s[80:83], s88 offen offset:0 sc1\n" \
"v_mfma_f32_16x16x32_bf16 a[52:55], a[164:167], a[156:159], 0\n" \
"v_cvt_pk_bf16_f32 v238, v132, v133\n" \
"v_cvt_pk_bf16_f32 v239, v134, v135\n" \
"v_cvt_pk_bf16_f32 v240, v160, v161\n" \
"v_mfma_f32_16x16x32_bf16 a[52:55], a[228:231], a[220:223], a[52:55]\n" \
"v_cvt_pk_bf16_f32 v241, v162, v163\n" \
"v_cvt_pk_bf16_f32 v242, v176, v177\n" \
"v_cvt_pk_bf16_f32 v243, v178, v179\n" \
"v_mfma_f32_16x16x32_bf16 a[48:51], a[160:163], a[156:159], 0\n" \
"v_cvt_pk_bf16_f32 v244, v156, v157\n" \
"v_cvt_pk_bf16_f32 v245, v158, v159\n" \
"v_cvt_pk_bf16_f32 v246, v172, v173\n" \
"v_mfma_f32_16x16x32_bf16 a[48:51], a[224:227], a[220:223], a[48:51]\n" \
"v_cvt_pk_bf16_f32 v247, v174, v175\n" \
"v_cvt_pk_bf16_f32 v184, v152, v153\n" \
"v_cvt_pk_bf16_f32 v185, v154, v155\n" \
"v_mfma_f32_16x16x32_bf16 a[64:67], a[164:167], a[140:143], 0\n" \
"v_cvt_pk_bf16_f32 v186, v168, v169\n" \
"v_cvt_pk_bf16_f32 v187, v170, v171\n" \
"v_cvt_pk_bf16_f32 v188, v148, v149\n" \
"v_mfma_f32_16x16x32_bf16 a[64:67], a[228:231], a[204:207], a[64:67]\n" \
"v_cvt_pk_bf16_f32 v189, v150, v151\n" \
"v_cvt_pk_bf16_f32 v190, v164, v165\n" \
"v_cvt_pk_bf16_f32 v191, v166, v167\n" \
"v_mfma_f32_16x16x32_bf16 a[80:83], a[172:175], a[140:143], 0\n" \
"v_cvt_pk_bf16_f32 v192, v96, v97\n" \
"v_cvt_pk_bf16_f32 v193, v98, v99\n" \
"v_cvt_pk_bf16_f32 v194, v112, v113\n" \
"v_mfma_f32_16x16x32_bf16 a[80:83], a[236:239], a[204:207], a[80:83]\n" \
"v_cvt_pk_bf16_f32 v195, v114, v115\n" \
"v_cvt_pk_bf16_f32 v196, v92, v93\n" \
"v_cvt_pk_bf16_f32 v197, v94, v95\n" \
"v_mfma_f32_16x16x32_bf16 a[68:71], a[140:143], a[168:171], 0\n" \
"v_cvt_pk_bf16_f32 v198, v108, v109\n" \
"v_cvt_pk_bf16_f32 v199, v110, v111\n" \
"v_cvt_pk_bf16_f32 v252, v88, v89\n" \
"v_mfma_f32_16x16x32_bf16 a[68:71], a[204:207], a[232:235], a[68:71]\n" \
"v_cvt_pk_bf16_f32 v253, v90, v91\n" \
"v_cvt_pk_bf16_f32 v254, v104, v105\n" \
"v_cvt_pk_bf16_f32 v255, v106, v107\n" \
"s_waitcnt vmcnt(4) lgkmcnt(0)\n" \
"s_barrier\n" \
"v_mfma_f32_16x16x32_bf16 v[116:119], a[176:179], a[144:147], 0\n" \
"s_add_u32 m0, s40, 0x0\n" \
"s_nop 0\n" \
"s_add_u32 s85, s39, 0x4000\n" \
"v_accvgpr_write_b32 a84, v252\n" \
"v_mfma_f32_16x16x32_bf16 v[116:119], a[240:243], a[208:211], v[116:119]\n" \
"buffer_load_dwordx4 v20, s[80:83], s85 offen sc1 lds\n" \
"v_accvgpr_write_b32 a85, v253\n" \
"v_mfma_f32_16x16x32_bf16 v[120:123], a[180:183], a[144:147], 0\n" \
"ds_read_b64_tr_b16 a[160:161], v32 offset:32768\n" \
"v_accvgpr_write_b32 a86, v254\n" \
"v_mfma_f32_16x16x32_bf16 v[120:123], a[244:247], a[208:211], v[120:123]\n" \
"ds_read_b64_tr_b16 a[162:163], v32 offset:33280\n" \
"v_accvgpr_write_b32 a87, v255\n" \
"v_mfma_f32_16x16x32_bf16 v[124:127], a[184:187], a[144:147], 0\n" \
"s_add_u32 m0, s40, 0x1000\n" \
"s_add_u32 s85, s39, 0x5000\n" \
"v_cvt_pk_bf16_f32 v252, v84, v85\n" \
"v_mfma_f32_16x16x32_bf16 v[124:127], a[248:251], a[208:211], v[124:127]\n" \
"buffer_load_dwordx4 v20, s[80:83], s85 offen sc1 lds\n" \
"v_cvt_pk_bf16_f32 v253, v86, v87\n" \
"v_mfma_f32_16x16x32_bf16 v[128:131], a[188:191], a[144:147], 0\n" \
"ds_read_b64_tr_b16 a[224:225], v32 offset:36864\n" \
"v_cvt_pk_bf16_f32 v254, v100, v101\n" \
"v_mfma_f32_16x16x32_bf16 v[128:131], a[252:255], a[208:211], v[128:131]\n" \
"ds_read_b128 a[144:147], v28 offset:40960\n" \
"v_cvt_pk_bf16_f32 v255, v102, v103\n" \
"v_mfma_f32_16x16x32_bf16 v[144:147], a[188:191], a[148:151], 0\n" \
"ds_read_b128 a[208:211], v29 offset:40960\n" \
"v_cvt_pk_bf16_f32 v200, v64, v65\n" \
"v_mfma_f32_16x16x32_bf16 v[144:147], a[252:255], a[212:215], v[144:147]\n" \
"s_add_u32 m0, s40, 0x3000\n" \
"s_movk_i32 s84, 0x100\n" \
"s_nop 0\n" \
"v_cvt_pk_bf16_f32 v201, v66, v67\n" \
"v_mfma_f32_16x16x32_bf16 v[140:143], a[184:187], a[148:151], 0\n" \
"buffer_load_dwordx4 v21, s[76:79], s84 offen offset:0 lds\n" \
"v_cvt_pk_bf16_f32 v202, v80, v81\n" \
"v_mfma_f32_16x16x32_bf16 v[140:143], a[248:251], a[212:215], v[140:143]\n" \
"ds_read_b64_tr_b16 a[226:227], v32 offset:37376\n" \
"v_cvt_pk_bf16_f32 v203, v82, v83\n" \
"v_mfma_f32_16x16x32_bf16 v[136:139], a[180:183], a[148:151], 0\n" \
"v_cvt_pk_bf16_f32 v204, v60, v61\n" \
"v_cvt_pk_bf16_f32 v205, v62, v63\n" \
"v_cvt_pk_bf16_f32 v206, v76, v77\n" \
"v_mfma_f32_16x16x32_bf16 v[136:139], a[244:247], a[212:215], v[136:139]\n" \
"ds_read_b64_tr_b16 a[164:165], v33 offset:32768\n" \
"v_cvt_pk_bf16_f32 v207, v78, v79\n" \
"v_mfma_f32_16x16x32_bf16 v[132:135], a[176:179], a[148:151], 0\n" \
"v_cvt_pk_bf16_f32 v208, v56, v57\n" \
"v_cvt_pk_bf16_f32 v209, v58, v59\n" \
"v_cvt_pk_bf16_f32 v210, v72, v73\n" \
"v_mfma_f32_16x16x32_bf16 v[132:135], a[240:243], a[212:215], v[132:135]\n" \
"ds_read_b128 a[148:151], v28 offset:43008\n" \
"v_cvt_pk_bf16_f32 v211, v74, v75\n" \
"v_mfma_f32_16x16x32_bf16 v[148:151], a[176:179], a[152:155], 0\n" \
"ds_read_b128 a[212:215], v29 offset:43008\n" \
"v_cvt_pk_bf16_f32 v212, v52, v53\n" \
"v_mfma_f32_16x16x32_bf16 v[148:151], a[240:243], a[216:219], v[148:151]\n" \
"s_add_u32 m0, s31, 0x0\n" \
"s_mov_b32 s84, 0x100000\n" \
"v_cvt_pk_bf16_f32 v213, v54, v55\n" \
"v_mfma_f32_16x16x32_bf16 v[152:155], a[180:183], a[152:155], 0\n" \
"buffer_load_dwordx4 v26, s[68:71], s84 offen offset:0 lds\n" \
"v_cvt_pk_bf16_f32 v214, v68, v69\n" \
"v_mfma_f32_16x16x32_bf16 v[152:155], a[244:247], a[216:219], v[152:155]\n" \
"s_addk_i32 s84, 0x7c00\n" \
"v_cvt_pk_bf16_f32 v215, v70, v71\n" \
"v_mfma_f32_16x16x32_bf16 v[156:159], a[184:187], a[152:155], 0\n" \
"buffer_load_dwordx4 v26, s[68:71], s84 offen offset:1024 lds\n" \
"v_mfma_f32_16x16x32_bf16 v[156:159], a[248:251], a[216:219], v[156:159]\n" \
"ds_read_b64_tr_b16 a[166:167], v33 offset:33280\n" \
"v_mfma_f32_16x16x32_bf16 v[160:163], a[188:191], a[152:155], 0\n" \
"ds_read_b64_tr_b16 a[228:229], v33 offset:36864\n" \
"v_mfma_f32_16x16x32_bf16 v[160:163], a[252:255], a[216:219], v[160:163]\n" \
"ds_read_b128 a[152:155], v28 offset:45056\n" \
"v_mfma_f32_16x16x32_bf16 v[176:179], a[188:191], a[156:159], 0\n" \
"ds_read_b128 a[216:219], v29 offset:45056\n" \
"v_mfma_f32_16x16x32_bf16 v[176:179], a[252:255], a[220:223], v[176:179]\n" \
"s_addk_i32 s84, 0x7c00\n" \
"v_mfma_f32_16x16x32_bf16 v[172:175], a[184:187], a[156:159], 0\n" \
"buffer_load_dwordx4 v27, s[68:71], s84 offen offset:2048 lds\n" \
"v_mfma_f32_16x16x32_bf16 v[172:175], a[248:251], a[220:223], v[172:175]\n" \
"ds_read_b64_tr_b16 a[230:231], v33 offset:37376\n" \
"v_mfma_f32_16x16x32_bf16 v[168:171], a[180:183], a[156:159], 0\n" \
"s_addk_i32 s84, 0x7c00\n" \
"v_mfma_f32_16x16x32_bf16 v[168:171], a[244:247], a[220:223], v[168:171]\n" \
"buffer_load_dwordx4 v27, s[68:71], s84 offen offset:3072 lds\n" \
"v_mfma_f32_16x16x32_bf16 v[164:167], a[176:179], a[156:159], 0\n" \
"ds_read_b64_tr_b16 a[172:173], v35 offset:32768\n" \
"v_mfma_f32_16x16x32_bf16 v[164:167], a[240:243], a[220:223], v[164:167]\n" \
"ds_read_b128 a[156:159], v28 offset:47104\n" \
"v_mfma_f32_16x16x32_bf16 v[84:87], a[176:179], a[136:139], 0\n" \
"ds_read_b128 a[220:223], v29 offset:47104\n" \
"v_mfma_f32_16x16x32_bf16 v[84:87], a[240:243], a[200:203], v[84:87]\n" \
"s_add_u32 s89, s41, 0x3000\n" \
"buffer_store_dwordx4 v[192:195], v20, s[80:83], s89 offen offset:3072 sc1\n" \
"v_mfma_f32_16x16x32_bf16 v[88:91], a[180:183], a[136:139], 0\n" \
"ds_read_b64_tr_b16 a[174:175], v35 offset:33280\n" \
"v_mfma_f32_16x16x32_bf16 v[88:91], a[244:247], a[200:203], v[88:91]\n" \
"ds_read_b64_tr_b16 a[236:237], v35 offset:36864\n" \
"v_mfma_f32_16x16x32_bf16 v[92:95], a[184:187], a[136:139], 0\n" \
"ds_read_b64_tr_b16 a[238:239], v35 offset:37376\n" \
"v_mfma_f32_16x16x32_bf16 v[92:95], a[248:251], a[200:203], v[92:95]\n" \
"s_add_u32 m0, s30, 0x0\n" \
"s_movk_i32 s84, 0x180\n" \
"v_mfma_f32_16x16x32_bf16 v[96:99], a[188:191], a[136:139], 0\n" \
"buffer_load_dwordx4 v24, s[76:79], s84 offen offset:0 lds\n" \
"v_mfma_f32_16x16x32_bf16 v[96:99], a[252:255], a[200:203], v[96:99]\n" \
"ds_read_b128 a[136:139], v28 offset:36864\n" \
"v_mfma_f32_16x16x32_bf16 v[112:115], a[188:191], a[140:143], 0\n" \
"ds_read_b128 a[200:203], v29 offset:36864\n" \
"v_mfma_f32_16x16x32_bf16 v[112:115], a[252:255], a[204:207], v[112:115]\n" \
"ds_read_b64_tr_b16 a[168:169], v34 offset:32768\n" \
"v_mfma_f32_16x16x32_bf16 v[108:111], a[184:187], a[140:143], 0\n" \
"s_movk_i32 s84, 0x2180\n" \
"s_nop 0\n" \
"v_mfma_f32_16x16x32_bf16 v[108:111], a[248:251], a[204:207], v[108:111]\n" \
"buffer_load_dwordx4 v25, s[76:79], s84 offen offset:1024 lds\n" \
"v_mfma_f32_16x16x32_bf16 v[104:107], a[180:183], a[140:143], 0\n" \
"ds_read_b64_tr_b16 a[170:171], v34 offset:33280\n" \
"v_mfma_f32_16x16x32_bf16 v[104:107], a[244:247], a[204:207], v[104:107]\n" \
"ds_read_b64_tr_b16 a[232:233], v34 offset:36864\n" \
"v_mfma_f32_16x16x32_bf16 v[100:103], a[176:179], a[140:143], 0\n" \
"v_mfma_f32_16x16x32_bf16 v[100:103], a[240:243], a[204:207], v[100:103]\n" \
"ds_read_b128 a[140:143], v28 offset:38912\n" \
"v_mfma_f32_16x16x32_bf16 v[52:55], a[176:179], a[128:131], 0\n" \
"ds_read_b128 a[204:207], v29 offset:38912\n" \
"v_mfma_f32_16x16x32_bf16 v[52:55], a[240:243], a[192:195], v[52:55]\n" \
"s_movk_i32 s84, 0x4180\n" \
"v_mfma_f32_16x16x32_bf16 v[56:59], a[180:183], a[128:131], 0\n" \
"buffer_load_dwordx4 v24, s[76:79], s84 offen offset:2048 lds\n" \
"v_mfma_f32_16x16x32_bf16 v[56:59], a[244:247], a[192:195], v[56:59]\n" \
"s_movk_i32 s84, 0x6180\n" \
"v_mfma_f32_16x16x32_bf16 v[60:63], a[184:187], a[128:131], 0\n" \
"buffer_load_dwordx4 v25, s[76:79], s84 offen offset:3072 lds\n" \
"v_mfma_f32_16x16x32_bf16 v[60:63], a[248:251], a[192:195], v[60:63]\n" \
"ds_read_b64_tr_b16 a[234:235], v34 offset:37376\n" \
"v_mfma_f32_16x16x32_bf16 v[64:67], a[188:191], a[128:131], 0\n" \
"v_mfma_f32_16x16x32_bf16 v[64:67], a[252:255], a[192:195], v[64:67]\n" \
"ds_read_b128 a[128:131], v28 offset:32768\n" \
"v_mfma_f32_16x16x32_bf16 v[80:83], a[188:191], a[132:135], 0\n" \
"ds_read_b128 a[192:195], v29 offset:32768\n" \
"v_mfma_f32_16x16x32_bf16 v[80:83], a[252:255], a[196:199], v[80:83]\n" \
"v_mfma_f32_16x16x32_bf16 v[76:79], a[184:187], a[132:135], 0\n" \
"v_mfma_f32_16x16x32_bf16 v[76:79], a[248:251], a[196:199], v[76:79]\n" \
"v_mfma_f32_16x16x32_bf16 v[72:75], a[180:183], a[132:135], 0\n" \
"v_mfma_f32_16x16x32_bf16 v[72:75], a[244:247], a[196:199], v[72:75]\n" \
"v_mfma_f32_16x16x32_bf16 v[68:71], a[176:179], a[132:135], 0\n" \
"v_mfma_f32_16x16x32_bf16 v[68:71], a[240:243], a[196:199], v[68:71]\n" \
"ds_read_b128 a[132:135], v28 offset:34816\n" \
"ds_read_b128 a[196:199], v29 offset:34816\n" \
"s_waitcnt lgkmcnt(15)\n" \
"v_mfma_f32_16x16x32_bf16 a[0:3], a[160:163], a[144:147], a[0:3]\n" \
"s_add_u32 m0, s30, 0x1000\n" \
"s_addk_i32 s84, 0x3000\n" \
"v_mfma_f32_16x16x32_bf16 a[0:3], a[224:227], a[208:211], a[0:3]\n" \
"buffer_load_dwordx4 v24, s[76:79], s84 offen offset:0 lds\n" \
"v_mfma_f32_16x16x32_bf16 a[4:7], a[164:167], a[144:147], a[4:7]\n" \
"ds_read_b64_tr_b16 a[176:177], v36 offset:32768\n" \
"v_mov_b32_e64 v11, 0\n" \
"v_mfma_f32_16x16x32_bf16 a[4:7], a[228:231], a[208:211], a[4:7]\n" \
"ds_read_b64_tr_b16 a[178:179], v36 offset:33792\n" \
"s_and_b32 s6, s2, 7\n" \
"s_waitcnt lgkmcnt(15)\n" \
"v_mfma_f32_16x16x32_bf16 a[12:15], a[172:175], a[144:147], a[12:15]\n" \
"ds_read_b64_tr_b16 a[240:241], v36 offset:40960\n" \
"s_lshl_b32 s5, s3, 1\n" \
"s_waitcnt lgkmcnt(15)\n" \
"v_mfma_f32_16x16x32_bf16 a[12:15], a[236:239], a[208:211], a[12:15]\n" \
"s_add_u32 s88, s41, 0x3000\n" \
"buffer_store_dwordx4 v[196:199], v20, s[80:83], s88 offen offset:2048 sc1\n" \
"s_waitcnt lgkmcnt(11)\n" \
"v_mfma_f32_16x16x32_bf16 a[8:11], a[144:147], a[168:171], a[8:11]\n" \
"ds_read_b64_tr_b16 a[242:243], v36 offset:41984\n" \
"s_lshl_b32 s4, s6, 2\n" \
"s_waitcnt lgkmcnt(8)\n" \
"v_mfma_f32_16x16x32_bf16 a[8:11], a[208:211], a[232:235], a[8:11]\n" \
"ds_read_b64_tr_b16 a[180:181], v37 offset:32768\n" \
"s_bfe_u32 s8, s2, 0x20003\n" \
"v_mfma_f32_16x16x32_bf16 a[24:27], a[148:151], a[168:171], a[24:27]\n" \
"ds_read_b64_tr_b16 a[182:183], v37 offset:33792\n" \
"s_or_b32 s13, s4, s8\n" \
"v_mfma_f32_16x16x32_bf16 a[24:27], a[212:215], a[232:235], a[24:27]\n" \
"ds_read_b64_tr_b16 a[244:245], v37 offset:40960\n" \
"s_and_b32 s5, s5, 4\n" \
"v_mfma_f32_16x16x32_bf16 a[28:31], a[172:175], a[148:151], a[28:31]\n" \
"s_and_b32 s7, s3, 1\n" \
"s_or_b32 s9, s5, s7\n" \
"v_mfma_f32_16x16x32_bf16 a[28:31], a[236:239], a[212:215], a[28:31]\n" \
"s_addk_i32 s84, 0x2000\n" \
"v_mfma_f32_16x16x32_bf16 a[20:23], a[164:167], a[148:151], a[20:23]\n" \
"buffer_load_dwordx4 v25, s[76:79], s84 offen offset:1024 lds\n" \
"v_mfma_f32_16x16x32_bf16 a[20:23], a[228:231], a[212:215], a[20:23]\n" \
"ds_read_b64_tr_b16 a[246:247], v37 offset:41984\n" \
"s_lshl_b32 s5, s13, 4\n" \
"v_mfma_f32_16x16x32_bf16 a[16:19], a[160:163], a[148:151], a[16:19]\n" \
"ds_read_b64_tr_b16 a[184:185], v38 offset:32768\n" \
"s_lshl_b32 s7, s3, 2\n" \
"v_mfma_f32_16x16x32_bf16 a[16:19], a[224:227], a[212:215], a[16:19]\n" \
"ds_read_b64_tr_b16 a[186:187], v38 offset:33792\n" \
"s_or_b32 s7, s5, s7\n" \
"v_mfma_f32_16x16x32_bf16 a[32:35], a[160:163], a[152:155], a[32:35]\n" \
"s_addk_i32 s84, 0x2000\n" \
"v_mfma_f32_16x16x32_bf16 a[32:35], a[224:227], a[216:219], a[32:35]\n" \
"buffer_load_dwordx4 v24, s[76:79], s84 offen offset:2048 lds\n" \
"v_mfma_f32_16x16x32_bf16 a[36:39], a[164:167], a[152:155], a[36:39]\n" \
"ds_read_b64_tr_b16 a[248:249], v38 offset:40960\n" \
"s_lshr_b32 s5, s2, 5\n" \
"v_mfma_f32_16x16x32_bf16 a[36:39], a[228:231], a[216:219], a[36:39]\n" \
"ds_read_b64_tr_b16 a[250:251], v38 offset:41984\n" \
"s_mul_i32 s10, s5, 0xa00\n" \
"v_mfma_f32_16x16x32_bf16 a[44:47], a[172:175], a[152:155], a[44:47]\n" \
"ds_read_b64_tr_b16 a[188:189], v39 offset:32768\n" \
"s_add_i32 s10, s10, 0x5bc1000\n" \
"v_mfma_f32_16x16x32_bf16 a[44:47], a[236:239], a[216:219], a[44:47]\n" \
"ds_read_b64_tr_b16 a[190:191], v39 offset:33792\n" \
"s_or_b32 s7, s7, s10\n" \
"v_mfma_f32_16x16x32_bf16 a[40:43], a[152:155], a[168:171], a[40:43]\n" \
"s_add_u32 s89, s41, 0x3000\n" \
"buffer_store_dwordx4 a[84:87], v20, s[80:83], s89 offen offset:1024 sc1\n" \
"v_mfma_f32_16x16x32_bf16 a[40:43], a[216:219], a[232:235], a[40:43]\n" \
"ds_read_b64_tr_b16 a[252:253], v39 offset:40960\n" \
"s_lshl_b32 s14, s5, 3\n" \
"v_mfma_f32_16x16x32_bf16 a[56:59], a[156:159], a[168:171], a[56:59]\n" \
"ds_read_b64_tr_b16 a[254:255], v39 offset:41984\n" \
"v_writelane_b32 v11, s7, 0\n" \
"v_mfma_f32_16x16x32_bf16 a[56:59], a[220:223], a[232:235], a[56:59]\n" \
"s_or_b32 s7, s14, s6\n" \
"v_writelane_b32 v11, s10, 1\n" \
"v_mfma_f32_16x16x32_bf16 a[60:63], a[172:175], a[156:159], a[60:63]\n" \
"s_lshl_b32 s7, s7, 10\n" \
"s_lshl_b32 s10, s3, 8\n" \
"v_mfma_f32_16x16x32_bf16 a[60:63], a[236:239], a[220:223], a[60:63]\n" \
"s_addk_i32 s84, 0x2000\n" \
"v_mfma_f32_16x16x32_bf16 a[52:55], a[164:167], a[156:159], a[52:55]\n" \
"buffer_load_dwordx4 v25, s[76:79], s84 offen offset:3072 lds\n" \
"v_mfma_f32_16x16x32_bf16 a[52:55], a[228:231], a[220:223], a[52:55]\n" \
"s_or_b32 s7, s7, s10\n" \
"s_or_b32 s10, s7, 0x5c00200\n" \
"v_mfma_f32_16x16x32_bf16 a[48:51], a[160:163], a[156:159], a[48:51]\n" \
"v_writelane_b32 v11, s10, 2\n" \
"s_lshl_b32 s10, s8, 2\n" \
"v_mfma_f32_16x16x32_bf16 a[48:51], a[224:227], a[220:223], a[48:51]\n" \
"s_or_b32 s11, s10, s7\n" \
"s_or_b32 s11, s11, 0x5c00000\n" \
"v_mfma_f32_16x16x32_bf16 a[64:67], a[164:167], a[140:143], a[64:67]\n" \
"v_writelane_b32 v11, s11, 3\n" \
"s_or_b32 s7, s7, 0x5c00080\n" \
"v_mfma_f32_16x16x32_bf16 a[64:67], a[228:231], a[204:207], a[64:67]\n" \
"v_writelane_b32 v11, s7, 4\n" \
"s_or_b32 s7, s7, s10\n" \
"v_mfma_f32_16x16x32_bf16 a[80:83], a[172:175], a[140:143], a[80:83]\n" \
"s_add_i32 s10, s3, s2\n" \
"v_writelane_b32 v11, s7, 5\n" \
"v_mfma_f32_16x16x32_bf16 a[80:83], a[236:239], a[204:207], a[80:83]\n" \
"s_mov_b32 s7, 2.0\n" \
"s_mov_b32 s11, 0x5bc0000\n" \
"v_mfma_f32_16x16x32_bf16 a[68:71], a[140:143], a[168:171], a[68:71]\n" \
"s_cmp_lg_u32 s10, 0\n" \
"s_cselect_b32 s10, s7, s11\n" \
"s_waitcnt vmcnt(11) lgkmcnt(0)\n" \
"v_mfma_f32_16x16x32_bf16 a[68:71], a[204:207], a[232:235], a[68:71]\n" \
"s_barrier\n" \
"v_xor_b32_e32 v28, 0x20000, v28\n" \
"v_xor_b32_e32 v29, 0x20000, v29\n" \
"v_mfma_f32_16x16x32_bf16 v[116:119], a[176:179], a[144:147], v[116:119]\n" \
"s_add_u32 m0, s40, 0x8000\n" \
"s_add_u32 s85, s39, 0x6000\n" \
"v_mfma_f32_16x16x32_bf16 v[116:119], a[240:243], a[208:211], v[116:119]\n" \
"buffer_load_dwordx4 v20, s[80:83], s85 offen sc1 lds\n" \
"v_mfma_f32_16x16x32_bf16 v[120:123], a[180:183], a[144:147], v[120:123]\n" \
"ds_read_b64_tr_b16 a[160:161], v32 offset:0\n" \
"s_lshr_b32 s15, s2, 3\n" \
"v_mfma_f32_16x16x32_bf16 v[120:123], a[244:247], a[208:211], v[120:123]\n" \
"ds_read_b64_tr_b16 a[162:163], v32 offset:512\n" \
"v_writelane_b32 v11, s10, 54\n" \
"v_mfma_f32_16x16x32_bf16 v[124:127], a[184:187], a[144:147], v[124:127]\n" \
"s_add_u32 m0, s40, 0x9000\n" \
"s_add_u32 s85, s39, 0x7000\n" \
"v_mfma_f32_16x16x32_bf16 v[124:127], a[248:251], a[208:211], v[124:127]\n" \
"buffer_load_dwordx4 v20, s[80:83], s85 offen sc1 lds\n" \
"v_mfma_f32_16x16x32_bf16 v[128:131], a[188:191], a[144:147], v[128:131]\n" \
"ds_read_b64_tr_b16 a[224:225], v32 offset:4096\n" \
"s_lshl_b32 s10, s6, 5\n" \
"v_mfma_f32_16x16x32_bf16 v[128:131], a[252:255], a[208:211], v[128:131]\n" \
"ds_read_b128 a[144:147], v28 offset:8192\n" \
"s_min_u32 s16, s3, 2\n" \
"v_mfma_f32_16x16x32_bf16 v[144:147], a[188:191], a[148:151], v[144:147]\n" \
"ds_read_b128 a[208:211], v29 offset:8192\n" \
"s_mul_i32 s12, s8, 0x120\n" \
"v_mfma_f32_16x16x32_bf16 v[144:147], a[252:255], a[212:215], v[144:147]\n" \
"s_add_u32 m0, s40, 0xb000\n" \
"s_movk_i32 s84, 0x180\n" \
"v_mfma_f32_16x16x32_bf16 v[140:143], a[184:187], a[148:151], v[140:143]\n" \
"buffer_load_dwordx4 v21, s[76:79], s84 offen offset:0 lds\n" \
"v_mfma_f32_16x16x32_bf16 v[140:143], a[248:251], a[212:215], v[140:143]\n" \
"ds_read_b64_tr_b16 a[226:227], v32 offset:4608\n" \
"v_mfma_f32_16x16x32_bf16 v[136:139], a[180:183], a[148:151], v[136:139]\n" \
"s_add_u32 s88, s41, 0x3000\n" \
"buffer_store_dwordx4 v[252:255], v20, s[80:83], s88 offen offset:0 sc1\n" \
"v_mfma_f32_16x16x32_bf16 v[136:139], a[244:247], a[212:215], v[136:139]\n" \
"ds_read_b64_tr_b16 a[164:165], v33 offset:0\n" \
"v_mfma_f32_16x16x32_bf16 v[132:135], a[176:179], a[148:151], v[132:135]\n" \
"s_add_u32 s11, s22, s12\n" \
"s_addc_u32 s17, s23, 0\n" \
"s_nop 0\n" \
"v_mfma_f32_16x16x32_bf16 v[132:135], a[240:243], a[212:215], v[132:135]\n" \
"ds_read_b128 a[148:151], v28 offset:10240\n" \
"v_mfma_f32_16x16x32_bf16 v[148:151], a[176:179], a[152:155], v[148:151]\n" \
"ds_read_b128 a[212:215], v29 offset:10240\n" \
"v_mfma_f32_16x16x32_bf16 v[148:151], a[240:243], a[216:219], v[148:151]\n" \
"s_add_u32 m0, s31, 0x8000\n" \
"s_mov_b32 s84, 0x180000\n" \
"v_mfma_f32_16x16x32_bf16 v[152:155], a[180:183], a[152:155], v[152:155]\n" \
"buffer_load_dwordx4 v26, s[68:71], s84 offen offset:0 lds\n" \
"v_mfma_f32_16x16x32_bf16 v[152:155], a[244:247], a[216:219], v[152:155]\n" \
"ds_read_b64_tr_b16 a[166:167], v33 offset:512\n" \
"v_mfma_f32_16x16x32_bf16 v[156:159], a[184:187], a[152:155], v[156:159]\n" \
"ds_read_b64_tr_b16 a[228:229], v33 offset:4096\n" \
"v_mfma_f32_16x16x32_bf16 v[156:159], a[248:251], a[216:219], v[156:159]\n" \
"s_addk_i32 s84, 0x7c00\n" \
"v_mfma_f32_16x16x32_bf16 v[160:163], a[188:191], a[152:155], v[160:163]\n" \
"buffer_load_dwordx4 v26, s[68:71], s84 offen offset:1024 lds\n" \
"v_mfma_f32_16x16x32_bf16 v[160:163], a[252:255], a[216:219], v[160:163]\n" \
"ds_read_b128 a[152:155], v28 offset:12288\n" \
"v_mfma_f32_16x16x32_bf16 v[176:179], a[188:191], a[156:159], v[176:179]\n" \
"ds_read_b128 a[216:219], v29 offset:12288\n" \
"v_mfma_f32_16x16x32_bf16 v[176:179], a[252:255], a[220:223], v[176:179]\n" \
"s_addk_i32 s84, 0x7c00\n" \
"v_mfma_f32_16x16x32_bf16 v[172:175], a[184:187], a[156:159], v[172:175]\n" \
"buffer_load_dwordx4 v27, s[68:71], s84 offen offset:2048 lds\n" \
"v_mfma_f32_16x16x32_bf16 v[172:175], a[248:251], a[220:223], v[172:175]\n" \
"ds_read_b64_tr_b16 a[230:231], v33 offset:4608\n" \
"v_mfma_f32_16x16x32_bf16 v[168:171], a[180:183], a[156:159], v[168:171]\n" \
"ds_read_b64_tr_b16 a[172:173], v35 offset:0\n" \
"v_mfma_f32_16x16x32_bf16 v[168:171], a[244:247], a[220:223], v[168:171]\n" \
"ds_read_b64_tr_b16 a[174:175], v35 offset:512\n" \
"v_mfma_f32_16x16x32_bf16 v[164:167], a[176:179], a[156:159], v[164:167]\n" \
"s_add_u32 s18, s24, s12\n" \
"v_writelane_b32 v11, s11, 9\n" \
"s_addc_u32 s19, s25, 0\n" \
"v_mfma_f32_16x16x32_bf16 v[164:167], a[240:243], a[220:223], v[164:167]\n" \
"ds_read_b128 a[156:159], v28 offset:14336\n" \
"s_and_b32 s20, s17, 0xffff\n" \
"v_mfma_f32_16x16x32_bf16 v[84:87], a[176:179], a[136:139], v[84:87]\n" \
"ds_read_b128 a[220:223], v29 offset:14336\n" \
"v_writelane_b32 v11, s20, 10\n" \
"v_mfma_f32_16x16x32_bf16 v[84:87], a[240:243], a[200:203], v[84:87]\n" \
"s_addk_i32 s84, 0x7c00\n" \
"v_mfma_f32_16x16x32_bf16 v[88:91], a[180:183], a[136:139], v[88:91]\n" \
"buffer_load_dwordx4 v27, s[68:71], s84 offen offset:3072 lds\n" \
"v_mfma_f32_16x16x32_bf16 v[88:91], a[244:247], a[200:203], v[88:91]\n" \
"s_add_u32 m0, s30, 0x8000\n" \
"s_movk_i32 s84, 0x200\n" \
"v_mfma_f32_16x16x32_bf16 v[92:95], a[184:187], a[136:139], v[92:95]\n" \
"buffer_load_dwordx4 v24, s[76:79], s84 offen offset:0 lds\n" \
"v_mfma_f32_16x16x32_bf16 v[92:95], a[248:251], a[200:203], v[92:95]\n" \
"ds_read_b64_tr_b16 a[236:237], v35 offset:4096\n" \
"s_and_b32 s20, s19, 0xffff\n" \
"v_mfma_f32_16x16x32_bf16 v[96:99], a[188:191], a[136:139], v[96:99]\n" \
"ds_read_b64_tr_b16 a[238:239], v35 offset:4608\n" \
"v_writelane_b32 v11, s18, 11\n" \
"v_mfma_f32_16x16x32_bf16 v[96:99], a[252:255], a[200:203], v[96:99]\n" \
"ds_read_b128 a[136:139], v28 offset:4096\n" \
"v_mfma_f32_16x16x32_bf16 v[112:115], a[188:191], a[140:143], v[112:115]\n" \
"ds_read_b128 a[200:203], v29 offset:4096\n" \
"v_mfma_f32_16x16x32_bf16 v[112:115], a[252:255], a[204:207], v[112:115]\n" \
"s_movk_i32 s84, 0x2200\n" \
"s_nop 0\n" \
"v_mfma_f32_16x16x32_bf16 v[108:111], a[184:187], a[140:143], v[108:111]\n" \
"buffer_load_dwordx4 v25, s[76:79], s84 offen offset:1024 lds\n" \
"v_mfma_f32_16x16x32_bf16 v[108:111], a[248:251], a[204:207], v[108:111]\n" \
"s_add_u32 s89, s41, 0x1000\n" \
"buffer_store_dwordx4 v[200:203], v20, s[80:83], s89 offen offset:3072 sc1\n" \
"v_mfma_f32_16x16x32_bf16 v[104:107], a[180:183], a[140:143], v[104:107]\n" \
"ds_read_b64_tr_b16 a[168:169], v34 offset:0\n" \
"v_mfma_f32_16x16x32_bf16 v[104:107], a[244:247], a[204:207], v[104:107]\n" \
"ds_read_b64_tr_b16 a[170:171], v34 offset:512\n" \
"v_mfma_f32_16x16x32_bf16 v[100:103], a[176:179], a[140:143], v[100:103]\n" \
"ds_read_b64_tr_b16 a[232:233], v34 offset:4096\n" \
"v_mfma_f32_16x16x32_bf16 v[100:103], a[240:243], a[204:207], v[100:103]\n" \
"ds_read_b128 a[140:143], v28 offset:6144\n" \
"v_mfma_f32_16x16x32_bf16 v[52:55], a[176:179], a[128:131], v[52:55]\n" \
"ds_read_b128 a[204:207], v29 offset:6144\n" \
"v_mfma_f32_16x16x32_bf16 v[52:55], a[240:243], a[192:195], v[52:55]\n" \
"s_movk_i32 s84, 0x4200\n" \
"v_mfma_f32_16x16x32_bf16 v[56:59], a[180:183], a[128:131], v[56:59]\n" \
"buffer_load_dwordx4 v24, s[76:79], s84 offen offset:2048 lds\n" \
"v_mfma_f32_16x16x32_bf16 v[56:59], a[244:247], a[192:195], v[56:59]\n" \
"s_movk_i32 s84, 0x6200\n" \
"v_mfma_f32_16x16x32_bf16 v[60:63], a[184:187], a[128:131], v[60:63]\n" \
"buffer_load_dwordx4 v25, s[76:79], s84 offen offset:3072 lds\n" \
"v_mfma_f32_16x16x32_bf16 v[60:63], a[248:251], a[192:195], v[60:63]\n" \
"ds_read_b64_tr_b16 a[234:235], v34 offset:4608\n" \
"v_mfma_f32_16x16x32_bf16 v[64:67], a[188:191], a[128:131], v[64:67]\n" \
"s_cmpk_lt_u32 s3, 3\n" \
"s_cselect_b32 s11, s11, s18\n" \
"v_mfma_f32_16x16x32_bf16 v[64:67], a[252:255], a[192:195], v[64:67]\n" \
"ds_read_b128 a[128:131], v28 offset:0\n" \
"s_movk_i32 s18, 0xfff\n" \
"v_mfma_f32_16x16x32_bf16 v[80:83], a[188:191], a[132:135], v[80:83]\n" \
"ds_read_b128 a[192:195], v29 offset:0\n" \
"v_writelane_b32 v11, s20, 12\n" \
"v_mfma_f32_16x16x32_bf16 v[80:83], a[252:255], a[196:199], v[80:83]\n" \
"s_and_b32 s20, s29, 0xffff\n" \
"v_writelane_b32 v11, s28, 13\n" \
"v_mfma_f32_16x16x32_bf16 v[76:79], a[184:187], a[132:135], v[76:79]\n" \
"v_writelane_b32 v11, s20, 14\n" \
"v_writelane_b32 v11, s11, 15\n" \
"v_mfma_f32_16x16x32_bf16 v[76:79], a[248:251], a[196:199], v[76:79]\n" \
"s_cmpk_lt_u32 s3, 3\n" \
"s_cselect_b32 s11, s17, s19\n" \
"v_mfma_f32_16x16x32_bf16 v[72:75], a[180:183], a[132:135], v[72:75]\n" \
"s_and_b32 s11, s11, 0xffff\n" \
"v_writelane_b32 v11, s11, 16\n" \
"v_mfma_f32_16x16x32_bf16 v[72:75], a[244:247], a[196:199], v[72:75]\n" \
"s_mul_i32 s11, s6, 0x48\n" \
"s_add_i32 s17, s11, 0x47\n" \
"v_mfma_f32_16x16x32_bf16 v[68:71], a[176:179], a[132:135], v[68:71]\n" \
"s_cmpk_lt_u32 s3, 3\n" \
"s_cselect_b32 s17, s17, s18\n" \
"v_mfma_f32_16x16x32_bf16 v[68:71], a[240:243], a[196:199], v[68:71]\n" \
"ds_read_b128 a[132:135], v28 offset:2048\n" \
"ds_read_b128 a[196:199], v29 offset:2048\n" \
"s_waitcnt lgkmcnt(15)\n" \
"v_mfma_f32_16x16x32_bf16 a[0:3], a[160:163], a[144:147], a[0:3]\n" \
"s_add_u32 m0, s30, 0x9000\n" \
"s_addk_i32 s84, 0x3000\n" \
"v_mfma_f32_16x16x32_bf16 a[0:3], a[224:227], a[208:211], a[0:3]\n" \
"buffer_load_dwordx4 v24, s[76:79], s84 offen offset:0 lds\n" \
"v_mfma_f32_16x16x32_bf16 a[4:7], a[164:167], a[144:147], a[4:7]\n" \
"ds_read_b64_tr_b16 a[176:177], v36 offset:0\n" \
"s_mulk_i32 s17, 0x480\n" \
"v_mfma_f32_16x16x32_bf16 a[4:7], a[228:231], a[208:211], a[4:7]\n" \
"ds_read_b64_tr_b16 a[178:179], v36 offset:1024\n" \
"s_addk_i32 s17, 0x120\n" \
"v_mfma_f32_16x16x32_bf16 a[12:15], a[172:175], a[144:147], a[12:15]\n" \
"ds_read_b64_tr_b16 a[240:241], v36 offset:8192\n" \
"v_writelane_b32 v11, s17, 17\n" \
"s_waitcnt lgkmcnt(15)\n" \
"v_mfma_f32_16x16x32_bf16 a[12:15], a[236:239], a[208:211], a[12:15]\n" \
"s_add_u32 s88, s41, 0x1000\n" \
"buffer_store_dwordx4 v[204:207], v20, s[80:83], s88 offen offset:2048 sc1\n" \
"s_waitcnt lgkmcnt(11)\n" \
"v_mfma_f32_16x16x32_bf16 a[8:11], a[144:147], a[168:171], a[8:11]\n" \
"ds_read_b64_tr_b16 a[242:243], v36 offset:9216\n" \
"s_lshl_b32 s17, s3, 5\n" \
"s_waitcnt lgkmcnt(8)\n" \
"v_mfma_f32_16x16x32_bf16 a[8:11], a[208:211], a[232:235], a[8:11]\n" \
"ds_read_b64_tr_b16 a[180:181], v37 offset:0\n" \
"s_add_i32 s11, s11, s17\n" \
"v_mfma_f32_16x16x32_bf16 a[24:27], a[148:151], a[168:171], a[24:27]\n" \
"ds_read_b64_tr_b16 a[182:183], v37 offset:1024\n" \
"s_mul_i32 s17, s11, 0x480\n" \
"v_mfma_f32_16x16x32_bf16 a[24:27], a[212:215], a[232:235], a[24:27]\n" \
"ds_read_b64_tr_b16 a[244:245], v37 offset:8192\n" \
"s_mul_i32 s11, s13, 0x24000\n" \
"v_mfma_f32_16x16x32_bf16 a[28:31], a[172:175], a[148:151], a[28:31]\n" \
"s_add_i32 s18, s11, 0x1b000\n" \
"v_mfma_f32_16x16x32_bf16 a[28:31], a[236:239], a[212:215], a[28:31]\n" \
"s_addk_i32 s84, 0x2000\n" \
"v_mfma_f32_16x16x32_bf16 a[20:23], a[164:167], a[148:151], a[20:23]\n" \
"buffer_load_dwordx4 v25, s[76:79], s84 offen offset:1024 lds\n" \
"v_mfma_f32_16x16x32_bf16 a[20:23], a[228:231], a[212:215], a[20:23]\n" \
"ds_read_b64_tr_b16 a[246:247], v37 offset:9216\n" \
"v_mfma_f32_16x16x32_bf16 a[16:19], a[160:163], a[148:151], a[16:19]\n" \
"ds_read_b64_tr_b16 a[184:185], v38 offset:0\n" \
"v_mfma_f32_16x16x32_bf16 a[16:19], a[224:227], a[212:215], a[16:19]\n" \
"ds_read_b64_tr_b16 a[186:187], v38 offset:1024\n" \
"v_mfma_f32_16x16x32_bf16 a[32:35], a[160:163], a[152:155], a[32:35]\n" \
"s_addk_i32 s84, 0x2000\n" \
"v_mfma_f32_16x16x32_bf16 a[32:35], a[224:227], a[216:219], a[32:35]\n" \
"buffer_load_dwordx4 v24, s[76:79], s84 offen offset:2048 lds\n" \
"v_mfma_f32_16x16x32_bf16 a[36:39], a[164:167], a[152:155], a[36:39]\n" \
".fill (L_pb + 65248 - .) / 4, 4, 0xBF800000\n" \
"L_pp16:\n" \
"s_cbranch_execz L_pret\n" \
"s_nop 0\n" \
"ds_read_b64_tr_b16 a[248:249], v38 offset:8192\n" \
"v_mfma_f32_16x16x32_bf16 a[36:39], a[228:231], a[216:219], a[36:39]\n" \
"ds_read_b64_tr_b16 a[250:251], v38 offset:9216\n" \
"v_mfma_f32_16x16x32_bf16 a[44:47], a[172:175], a[152:155], a[44:47]\n" \
"ds_read_b64_tr_b16 a[188:189], v39 offset:0\n" \
"v_mfma_f32_16x16x32_bf16 a[44:47], a[236:239], a[216:219], a[44:47]\n" \
"ds_read_b64_tr_b16 a[190:191], v39 offset:1024\n" \
"v_mfma_f32_16x16x32_bf16 a[40:43], a[152:155], a[168:171], a[40:43]\n" \
"s_add_u32 s89, s41, 0x1000\n" \
"buffer_store_dwordx4 v[208:211], v20, s[80:83], s89 offen offset:1024 sc1\n" \
"v_mfma_f32_16x16x32_bf16 a[40:43], a[216:219], a[232:235], a[40:43]\n" \
"ds_read_b64_tr_b16 a[252:253], v39 offset:8192\n" \
"v_mfma_f32_16x16x32_bf16 a[56:59], a[156:159], a[168:171], a[56:59]\n" \
"ds_read_b64_tr_b16 a[254:255], v39 offset:9216\n" \
"v_mfma_f32_16x16x32_bf16 a[56:59], a[220:223], a[232:235], a[56:59]\n" \
"s_cmpk_lt_u32 s3, 3\n" \
"s_cselect_b32 s17, s17, s18\n" \
"v_mfma_f32_16x16x32_bf16 a[60:63], a[172:175], a[156:159], a[60:63]\n" \
"s_mov_b32 s18, 0x8000\n" \
"v_writelane_b32 v11, s17, 18\n" \
"v_mfma_f32_16x16x32_bf16 a[60:63], a[236:239], a[220:223], a[60:63]\n" \
"s_addk_i32 s84, 0x2000\n" \
"v_mfma_f32_16x16x32_bf16 a[52:55], a[164:167], a[156:159], a[52:55]\n" \
"buffer_load_dwordx4 v25, s[76:79], s84 offen offset:3072 lds\n" \
"v_mfma_f32_16x16x32_bf16 a[52:55], a[228:231], a[220:223], a[52:55]\n" \
"s_mul_i32 s17, s16, 0x2400\n" \
"s_mul_i32 s16, s16, 0x9000\n" \
"v_mfma_f32_16x16x32_bf16 a[48:51], a[160:163], a[156:159], a[48:51]\n" \
"s_bitset1_b32 s17, 17\n" \
"v_writelane_b32 v11, s17, 19\n" \
"v_mfma_f32_16x16x32_bf16 a[48:51], a[224:227], a[220:223], a[48:51]\n" \
"s_add_i32 s16, s11, s16\n" \
"v_writelane_b32 v11, s16, 26\n" \
"v_mfma_f32_16x16x32_bf16 a[64:67], a[164:167], a[140:143], a[64:67]\n" \
"s_mul_i32 s16, s3, 0x2400\n" \
"s_add_i32 s17, s16, 0xa400\n" \
"v_mfma_f32_16x16x32_bf16 a[64:67], a[228:231], a[204:207], a[64:67]\n" \
"s_cmpk_lt_u32 s3, 3\n" \
"s_cselect_b32 s17, s17, s18\n" \
"v_mfma_f32_16x16x32_bf16 a[80:83], a[172:175], a[140:143], a[80:83]\n" \
"s_mov_b32 s18, 0x1b000\n" \
"s_xor_b32 s19, s3, 2\n" \
"v_mfma_f32_16x16x32_bf16 a[80:83], a[236:239], a[204:207], a[80:83]\n" \
"s_cmp_lg_u32 s19, 0\n" \
"s_cselect_b32 s18, s17, s18\n" \
"v_mfma_f32_16x16x32_bf16 a[68:71], a[140:143], a[168:171], a[68:71]\n" \
"v_writelane_b32 v11, s17, 20\n" \
"s_or_b32 s17, s16, 0x10000\n" \
"s_waitcnt vmcnt(11) lgkmcnt(0)\n" \
"v_mfma_f32_16x16x32_bf16 a[68:71], a[204:207], a[232:235], a[68:71]\n" \
"s_barrier\n" \
"v_xor_b32_e32 v28, 0x20000, v28\n" \
"v_xor_b32_e32 v29, 0x20000, v29\n" \
"v_mfma_f32_16x16x32_bf16 v[116:119], a[176:179], a[144:147], v[116:119]\n" \
"s_add_u32 m0, s40, 0x0\n" \
"s_nop 0\n" \
"s_add_u32 s85, s39, 0x8000\n" \
"v_mfma_f32_16x16x32_bf16 v[116:119], a[240:243], a[208:211], v[116:119]\n" \
"buffer_load_dwordx4 v20, s[80:83], s85 offen sc1 lds\n" \
"v_mfma_f32_16x16x32_bf16 v[120:123], a[180:183], a[144:147], v[120:123]\n" \
"ds_read_b64_tr_b16 a[160:161], v32 offset:32768\n" \
"v_writelane_b32 v11, s18, 21\n" \
"v_mfma_f32_16x16x32_bf16 v[120:123], a[244:247], a[208:211], v[120:123]\n" \
"ds_read_b64_tr_b16 a[162:163], v32 offset:33280\n" \
"s_add_i32 s18, s16, 0x19000\n" \
"v_mfma_f32_16x16x32_bf16 v[124:127], a[184:187], a[144:147], v[124:127]\n" \
"s_add_u32 m0, s40, 0x1000\n" \
"s_add_u32 s85, s39, 0x9000\n" \
"v_mfma_f32_16x16x32_bf16 v[124:127], a[248:251], a[208:211], v[124:127]\n" \
"buffer_load_dwordx4 v20, s[80:83], s85 offen sc1 lds\n" \
"v_mfma_f32_16x16x32_bf16 v[128:131], a[188:191], a[144:147], v[128:131]\n" \
"ds_read_b64_tr_b16 a[224:225], v32 offset:36864\n" \
"v_writelane_b32 v11, s17, 22\n" \
"v_mfma_f32_16x16x32_bf16 v[128:131], a[252:255], a[208:211], v[128:131]\n" \
"ds_read_b128 a[144:147], v28 offset:8192\n" \
"s_mov_b32 s17, 0\n" \
"v_mfma_f32_16x16x32_bf16 v[144:147], a[188:191], a[148:151], v[144:147]\n" \
"ds_read_b128 a[208:211], v29 offset:8192\n" \
"v_mfma_f32_16x16x32_bf16 v[144:147], a[252:255], a[212:215], v[144:147]\n" \
"s_add_u32 m0, s40, 0x3000\n" \
"s_movk_i32 s84, 0x200\n" \
"v_mfma_f32_16x16x32_bf16 v[140:143], a[184:187], a[148:151], v[140:143]\n" \
"buffer_load_dwordx4 v21, s[76:79], s84 offen offset:0 lds\n" \
"v_mfma_f32_16x16x32_bf16 v[140:143], a[248:251], a[212:215], v[140:143]\n" \
"ds_read_b64_tr_b16 a[226:227], v32 offset:37376\n" \
"v_mfma_f32_16x16x32_bf16 v[136:139], a[180:183], a[148:151], v[136:139]\n" \
"s_add_u32 s88, s41, 0x1000\n" \
"buffer_store_dwordx4 v[212:215], v20, s[80:83], s88 offen offset:0 sc1\n" \
"v_mfma_f32_16x16x32_bf16 v[136:139], a[244:247], a[212:215], v[136:139]\n" \
"ds_read_b64_tr_b16 a[164:165], v33 offset:32768\n" \
"v_mfma_f32_16x16x32_bf16 v[132:135], a[176:179], a[148:151], v[132:135]\n" \
"s_cmpk_lt_u32 s3, 3\n" \
"s_cselect_b32 s18, s18, s17\n" \
"v_mfma_f32_16x16x32_bf16 v[132:135], a[240:243], a[212:215], v[132:135]\n" \
"ds_read_b128 a[148:151], v28 offset:10240\n" \
"s_bitset1_b32 s16, 17\n" \
"v_mfma_f32_16x16x32_bf16 v[148:151], a[176:179], a[152:155], v[148:151]\n" \
"ds_read_b128 a[212:215], v29 offset:10240\n" \
"v_writelane_b32 v11, s18, 23\n" \
"v_mfma_f32_16x16x32_bf16 v[148:151], a[240:243], a[216:219], v[148:151]\n" \
"s_add_u32 m0, s31, 0x0\n" \
"s_mov_b32 s84, 0x200000\n" \
"v_mfma_f32_16x16x32_bf16 v[152:155], a[180:183], a[152:155], v[152:155]\n" \
"buffer_load_dwordx4 v26, s[68:71], s84 offen offset:0 lds\n" \
"v_mfma_f32_16x16x32_bf16 v[152:155], a[244:247], a[216:219], v[152:155]\n" \
"ds_read_b64_tr_b16 a[166:167], v33 offset:33280\n" \
"s_movk_i32 s18, 0x2400\n" \
"v_mfma_f32_16x16x32_bf16 v[156:159], a[184:187], a[152:155], v[156:159]\n" \
"ds_read_b64_tr_b16 a[228:229], v33 offset:36864\n" \
"v_mfma_f32_16x16x32_bf16 v[156:159], a[248:251], a[216:219], v[156:159]\n" \
"s_addk_i32 s84, 0x7c00\n" \
"v_mfma_f32_16x16x32_bf16 v[160:163], a[188:191], a[152:155], v[160:163]\n" \
"buffer_load_dwordx4 v26, s[68:71], s84 offen offset:1024 lds\n" \
"v_mfma_f32_16x16x32_bf16 v[160:163], a[252:255], a[216:219], v[160:163]\n" \
"ds_read_b128 a[152:155], v28 offset:12288\n" \
"v_mfma_f32_16x16x32_bf16 v[176:179], a[188:191], a[156:159], v[176:179]\n" \
"ds_read_b128 a[216:219], v29 offset:12288\n" \
"v_mfma_f32_16x16x32_bf16 v[176:179], a[252:255], a[220:223], v[176:179]\n" \
"s_addk_i32 s84, 0x7c00\n" \
"v_mfma_f32_16x16x32_bf16 v[172:175], a[184:187], a[156:159], v[172:175]\n" \
"buffer_load_dwordx4 v27, s[68:71], s84 offen offset:2048 lds\n" \
"v_mfma_f32_16x16x32_bf16 v[172:175], a[248:251], a[220:223], v[172:175]\n" \
"ds_read_b64_tr_b16 a[230:231], v33 offset:37376\n" \
"v_mfma_f32_16x16x32_bf16 v[168:171], a[180:183], a[156:159], v[168:171]\n" \
"ds_read_b64_tr_b16 a[172:173], v35 offset:32768\n" \
"v_mfma_f32_16x16x32_bf16 v[168:171], a[244:247], a[220:223], v[168:171]\n" \
"ds_read_b64_tr_b16 a[174:175], v35 offset:33280\n" \
"v_mfma_f32_16x16x32_bf16 v[164:167], a[176:179], a[156:159], v[164:167]\n" \
"s_cmpk_lt_u32 s3, 3\n" \
"s_cselect_b32 s16, s16, s18\n" \
"v_mfma_f32_16x16x32_bf16 v[164:167], a[240:243], a[220:223], v[164:167]\n" \
"ds_read_b128 a[156:159], v28 offset:14336\n" \
"s_mul_i32 s19, s3, 0x9000\n" \
"v_mfma_f32_16x16x32_bf16 v[84:87], a[176:179], a[136:139], v[84:87]\n" \
"ds_read_b128 a[220:223], v29 offset:14336\n" \
"v_writelane_b32 v11, s16, 24\n" \
"v_mfma_f32_16x16x32_bf16 v[84:87], a[240:243], a[200:203], v[84:87]\n" \
"s_addk_i32 s84, 0x7c00\n" \
"v_mfma_f32_16x16x32_bf16 v[88:91], a[180:183], a[136:139], v[88:91]\n" \
"buffer_load_dwordx4 v27, s[68:71], s84 offen offset:3072 lds\n" \
"v_mfma_f32_16x16x32_bf16 v[88:91], a[244:247], a[200:203], v[88:91]\n" \
"s_add_u32 m0, s30, 0x20000\n" \
"s_movk_i32 s84, 0x280\n" \
"v_mfma_f32_16x16x32_bf16 v[92:95], a[184:187], a[136:139], v[92:95]\n" \
"buffer_load_dwordx4 v24, s[76:79], s84 offen offset:0 lds\n" \
"v_mfma_f32_16x16x32_bf16 v[92:95], a[248:251], a[200:203], v[92:95]\n" \
"ds_read_b64_tr_b16 a[236:237], v35 offset:36864\n" \
"s_lshl_b32 s16, s3, 10\n" \
"v_mfma_f32_16x16x32_bf16 v[96:99], a[188:191], a[136:139], v[96:99]\n" \
"ds_read_b64_tr_b16 a[238:239], v35 offset:37376\n" \
"s_addk_i32 s16, 0x4800\n" \
"v_mfma_f32_16x16x32_bf16 v[96:99], a[252:255], a[200:203], v[96:99]\n" \
"ds_read_b128 a[136:139], v28 offset:4096\n" \
"v_writelane_b32 v11, s16, 25\n" \
"v_mfma_f32_16x16x32_bf16 v[112:115], a[188:191], a[140:143], v[112:115]\n" \
"ds_read_b128 a[200:203], v29 offset:4096\n" \
"s_add_i32 s16, s15, 1\n" \
"v_mfma_f32_16x16x32_bf16 v[112:115], a[252:255], a[204:207], v[112:115]\n" \
"s_movk_i32 s84, 0x2280\n" \
"v_mfma_f32_16x16x32_bf16 v[108:111], a[184:187], a[140:143], v[108:111]\n" \
"buffer_load_dwordx4 v25, s[76:79], s84 offen offset:1024 lds\n" \
"v_mfma_f32_16x16x32_bf16 v[108:111], a[248:251], a[204:207], v[108:111]\n" \
"s_add_u32 s89, s42, 0x0\n" \
"buffer_store_dwordx4 v[216:219], v20, s[80:83], s89 offen offset:1024 sc1\n" \
"v_mfma_f32_16x16x32_bf16 v[104:107], a[180:183], a[140:143], v[104:107]\n" \
"ds_read_b64_tr_b16 a[168:169], v34 offset:32768\n" \
"s_and_b32 s16, s16, 3\n" \
"v_mfma_f32_16x16x32_bf16 v[104:107], a[244:247], a[204:207], v[104:107]\n" \
"ds_read_b64_tr_b16 a[170:171], v34 offset:33280\n" \
"s_or_b32 s16, s16, s4\n" \
"v_mfma_f32_16x16x32_bf16 v[100:103], a[176:179], a[140:143], v[100:103]\n" \
"ds_read_b64_tr_b16 a[232:233], v34 offset:36864\n" \
"s_mul_i32 s18, s16, 0x24000\n" \
"v_mfma_f32_16x16x32_bf16 v[100:103], a[240:243], a[204:207], v[100:103]\n" \
"ds_read_b128 a[140:143], v28 offset:6144\n" \
"s_add_i32 s18, s18, s19\n" \
"v_mfma_f32_16x16x32_bf16 v[52:55], a[176:179], a[128:131], v[52:55]\n" \
"ds_read_b128 a[204:207], v29 offset:6144\n" \
"s_xor_b32 s13, s13, 2\n" \
"v_mfma_f32_16x16x32_bf16 v[52:55], a[240:243], a[192:195], v[52:55]\n" \
"s_movk_i32 s84, 0x4280\n" \
"v_mfma_f32_16x16x32_bf16 v[56:59], a[180:183], a[128:131], v[56:59]\n" \
"buffer_load_dwordx4 v24, s[76:79], s84 offen offset:2048 lds\n" \
"v_mfma_f32_16x16x32_bf16 v[56:59], a[244:247], a[192:195], v[56:59]\n" \
"s_movk_i32 s84, 0x6280\n" \
"v_mfma_f32_16x16x32_bf16 v[60:63], a[184:187], a[128:131], v[60:63]\n" \
"buffer_load_dwordx4 v25, s[76:79], s84 offen offset:3072 lds\n" \
"v_mfma_f32_16x16x32_bf16 v[60:63], a[248:251], a[192:195], v[60:63]\n" \
"ds_read_b64_tr_b16 a[234:235], v34 offset:37376\n" \
"s_add_i32 s15, s15, -1\n" \
"v_mfma_f32_16x16x32_bf16 v[64:67], a[188:191], a[128:131], v[64:67]\n" \
"v_writelane_b32 v11, s18, 27\n" \
"s_mul_i32 s18, s13, 0x24000\n" \
"v_mfma_f32_16x16x32_bf16 v[64:67], a[252:255], a[192:195], v[64:67]\n" \
"ds_read_b128 a[128:131], v28 offset:0\n" \
"s_and_b32 s15, s15, 3\n" \
"v_mfma_f32_16x16x32_bf16 v[80:83], a[188:191], a[132:135], v[80:83]\n" \
"ds_read_b128 a[192:195], v29 offset:0\n" \
"s_add_i32 s18, s18, s19\n" \
"v_mfma_f32_16x16x32_bf16 v[80:83], a[252:255], a[196:199], v[80:83]\n" \
"s_or_b32 s15, s15, s4\n" \
"v_writelane_b32 v11, s18, 28\n" \
"v_mfma_f32_16x16x32_bf16 v[76:79], a[184:187], a[132:135], v[76:79]\n" \
"s_mul_i32 s18, s15, 0x24000\n" \
"s_add_i32 s18, s18, s19\n" \
"v_mfma_f32_16x16x32_bf16 v[76:79], a[248:251], a[196:199], v[76:79]\n" \
"v_writelane_b32 v11, s18, 29\n" \
"s_or_b32 s18, s9, s14\n" \
"v_mfma_f32_16x16x32_bf16 v[72:75], a[180:183], a[132:135], v[72:75]\n" \
"s_mul_i32 s18, s18, 0x24800\n" \
"s_lshl_b32 s16, s16, 12\n" \
"v_mfma_f32_16x16x32_bf16 v[72:75], a[244:247], a[196:199], v[72:75]\n" \
"s_add_i32 s19, s18, 0x3004800\n" \
"s_add_i32 s20, s19, s16\n" \
"v_mfma_f32_16x16x32_bf16 v[68:71], a[176:179], a[132:135], v[68:71]\n" \
"v_writelane_b32 v11, s20, 30\n" \
"s_lshl_b32 s13, s13, 12\n" \
"v_mfma_f32_16x16x32_bf16 v[68:71], a[240:243], a[196:199], v[68:71]\n" \
"ds_read_b128 a[132:135], v28 offset:2048\n" \
"ds_read_b128 a[196:199], v29 offset:2048\n" \
"s_waitcnt lgkmcnt(15)\n" \
"v_mfma_f32_16x16x32_bf16 a[0:3], a[160:163], a[144:147], a[0:3]\n" \
"s_add_u32 m0, s30, 0x21000\n" \
"s_addk_i32 s84, 0x3000\n" \
"v_mfma_f32_16x16x32_bf16 a[0:3], a[224:227], a[208:211], a[0:3]\n" \
"buffer_load_dwordx4 v24, s[76:79], s84 offen offset:0 lds\n" \
"v_mfma_f32_16x16x32_bf16 a[4:7], a[164:167], a[144:147], a[4:7]\n" \
"ds_read_b64_tr_b16 a[176:177], v36 offset:32768\n" \
"s_lshl_b32 s15, s15, 12\n" \
"v_mfma_f32_16x16x32_bf16 a[4:7], a[228:231], a[208:211], a[4:7]\n" \
"ds_read_b64_tr_b16 a[178:179], v36 offset:33792\n" \
"s_sub_i32 s14, s9, s14\n" \
"v_mfma_f32_16x16x32_bf16 a[12:15], a[172:175], a[144:147], a[12:15]\n" \
"ds_read_b64_tr_b16 a[240:241], v36 offset:40960\n" \
"s_add_i32 s20, s19, s13\n" \
"s_waitcnt lgkmcnt(15)\n" \
"v_mfma_f32_16x16x32_bf16 a[12:15], a[236:239], a[208:211], a[12:15]\n" \
"s_add_u32 s88, s42, 0x0\n" \
"buffer_store_dwordx4 v[220:223], v20, s[80:83], s88 offen offset:0 sc1\n" \
"s_waitcnt lgkmcnt(11)\n" \
"v_mfma_f32_16x16x32_bf16 a[8:11], a[144:147], a[168:171], a[8:11]\n" \
"ds_read_b64_tr_b16 a[242:243], v36 offset:41984\n" \
"v_writelane_b32 v11, s20, 31\n" \
"s_waitcnt lgkmcnt(8)\n" \
"v_mfma_f32_16x16x32_bf16 a[8:11], a[208:211], a[232:235], a[8:11]\n" \
"ds_read_b64_tr_b16 a[180:181], v37 offset:32768\n" \
"s_add_i32 s19, s19, s15\n" \
"v_mfma_f32_16x16x32_bf16 a[24:27], a[148:151], a[168:171], a[24:27]\n" \
"ds_read_b64_tr_b16 a[182:183], v37 offset:33792\n" \
"s_mul_i32 s14, s14, 0x24800\n" \
"v_mfma_f32_16x16x32_bf16 a[24:27], a[212:215], a[232:235], a[24:27]\n" \
"ds_read_b64_tr_b16 a[244:245], v37 offset:40960\n" \
"v_writelane_b32 v11, s19, 32\n" \
"v_mfma_f32_16x16x32_bf16 a[28:31], a[172:175], a[148:151], a[28:31]\n" \
"s_add_i32 s19, s14, 0x4120800\n" \
"s_add_i32 s16, s19, s16\n" \
"v_mfma_f32_16x16x32_bf16 a[28:31], a[236:239], a[212:215], a[28:31]\n" \
"s_addk_i32 s84, 0x2000\n" \
"v_mfma_f32_16x16x32_bf16 a[20:23], a[164:167], a[148:151], a[20:23]\n" \
"buffer_load_dwordx4 v25, s[76:79], s84 offen offset:1024 lds\n" \
"v_mfma_f32_16x16x32_bf16 a[20:23], a[228:231], a[212:215], a[20:23]\n" \
"ds_read_b64_tr_b16 a[246:247], v37 offset:41984\n" \
"v_writelane_b32 v11, s16, 33\n" \
"v_mfma_f32_16x16x32_bf16 a[16:19], a[160:163], a[148:151], a[16:19]\n" \
"ds_read_b64_tr_b16 a[184:185], v38 offset:32768\n" \
"s_add_i32 s13, s19, s13\n" \
"v_mfma_f32_16x16x32_bf16 a[16:19], a[224:227], a[212:215], a[16:19]\n" \
"ds_read_b64_tr_b16 a[186:187], v38 offset:33792\n" \
"v_writelane_b32 v11, s13, 34\n" \
"v_mfma_f32_16x16x32_bf16 a[32:35], a[160:163], a[152:155], a[32:35]\n" \
"s_addk_i32 s84, 0x2000\n" \
"v_mfma_f32_16x16x32_bf16 a[32:35], a[224:227], a[216:219], a[32:35]\n" \
"buffer_load_dwordx4 v24, s[76:79], s84 offen offset:2048 lds\n" \
"v_mfma_f32_16x16x32_bf16 a[36:39], a[164:167], a[152:155], a[36:39]\n" \
"ds_read_b64_tr_b16 a[248:249], v38 offset:40960\n" \
"s_add_i32 s19, s19, s15\n" \
"v_mfma_f32_16x16x32_bf16 a[36:39], a[228:231], a[216:219], a[36:39]\n" \
"ds_read_b64_tr_b16 a[250:251], v38 offset:41984\n" \
"s_or_b32 s13, s18, 0x3000000\n" \
"v_mfma_f32_16x16x32_bf16 a[44:47], a[172:175], a[152:155], a[44:47]\n" \
"ds_read_b64_tr_b16 a[188:189], v39 offset:32768\n" \
"s_lshl_b32 s15, s6, 11\n" \
"v_mfma_f32_16x16x32_bf16 a[44:47], a[236:239], a[216:219], a[44:47]\n" \
"ds_read_b64_tr_b16 a[190:191], v39 offset:33792\n" \
"v_writelane_b32 v11, s19, 35\n" \
"v_mfma_f32_16x16x32_bf16 a[40:43], a[152:155], a[168:171], a[40:43]\n" \
"s_add_u32 s89, s42, 0x1000\n" \
"buffer_store_dwordx4 v[224:227], v20, s[80:83], s89 offen offset:3072 sc1\n" \
"v_mfma_f32_16x16x32_bf16 a[40:43], a[216:219], a[232:235], a[40:43]\n" \
"ds_read_b64_tr_b16 a[252:253], v39 offset:40960\n" \
"s_add_i32 s16, s13, s15\n" \
"v_mfma_f32_16x16x32_bf16 a[56:59], a[156:159], a[168:171], a[56:59]\n" \
"ds_read_b64_tr_b16 a[254:255], v39 offset:41984\n" \
"v_writelane_b32 v11, s16, 36\n" \
"v_mfma_f32_16x16x32_bf16 a[56:59], a[220:223], a[232:235], a[56:59]\n" \
"s_add_i32 s16, s14, 0x411c000\n" \
"s_add_i32 s19, s16, s15\n" \
"v_mfma_f32_16x16x32_bf16 a[60:63], a[172:175], a[156:159], a[60:63]\n" \
"v_writelane_b32 v11, s19, 37\n" \
"s_add_i32 s18, s18, 0x3004000\n" \
"v_mfma_f32_16x16x32_bf16 a[60:63], a[236:239], a[220:223], a[60:63]\n" \
"s_addk_i32 s84, 0x2000\n" \
"v_mfma_f32_16x16x32_bf16 a[52:55], a[164:167], a[156:159], a[52:55]\n" \
"buffer_load_dwordx4 v25, s[76:79], s84 offen offset:3072 lds\n" \
"v_mfma_f32_16x16x32_bf16 a[52:55], a[228:231], a[220:223], a[52:55]\n" \
"v_writelane_b32 v11, s18, 38\n" \
"s_add_i32 s14, s14, 0x4120000\n" \
"v_mfma_f32_16x16x32_bf16 a[48:51], a[160:163], a[156:159], a[48:51]\n" \
"v_writelane_b32 v11, s14, 39\n" \
"s_lshl_b32 s14, s5, 2\n" \
"v_mfma_f32_16x16x32_bf16 a[48:51], a[224:227], a[220:223], a[48:51]\n" \
"s_or_b32 s8, s14, s8\n" \
"s_mul_i32 s8, s8, 0x90000\n" \
"v_mfma_f32_16x16x32_bf16 a[64:67], a[164:167], a[140:143], a[64:67]\n" \
"s_lshl_b32 s14, s3, 14\n" \
"s_add_i32 s8, s8, s14\n" \
"v_mfma_f32_16x16x32_bf16 a[64:67], a[228:231], a[204:207], a[64:67]\n" \
"s_lshl_b32 s9, s9, 4\n" \
"s_lshl_b32 s14, s5, 7\n" \
"v_mfma_f32_16x16x32_bf16 a[80:83], a[172:175], a[140:143], a[80:83]\n" \
"s_or_b32 s18, s9, s14\n" \
"s_sub_i32 s9, s9, s14\n" \
"v_mfma_f32_16x16x32_bf16 a[80:83], a[236:239], a[204:207], a[80:83]\n" \
"s_mulk_i32 s18, 0x480\n" \
"s_mulk_i32 s9, 0x480\n" \
"v_mfma_f32_16x16x32_bf16 a[68:71], a[140:143], a[168:171], a[68:71]\n" \
"s_or_b32 s14, s18, s12\n" \
"s_or_b32 s9, s9, s12\n" \
"s_waitcnt vmcnt(11) lgkmcnt(0)\n" \
"v_mfma_f32_16x16x32_bf16 a[68:71], a[204:207], a[232:235], a[68:71]\n" \
"s_barrier\n" \
"v_mfma_f32_16x16x32_bf16 v[116:119], a[176:179], a[144:147], v[116:119]\n" \
"s_add_u32 m0, s40, 0x8000\n" \
"s_add_u32 s85, s39, 0xa000\n" \
"v_mfma_f32_16x16x32_bf16 v[116:119], a[240:243], a[208:211], v[116:119]\n" \
"buffer_load_dwordx4 v20, s[80:83], s85 offen sc1 lds\n" \
"v_mfma_f32_16x16x32_bf16 v[120:123], a[180:183], a[144:147], v[120:123]\n" \
"ds_read_b64_tr_b16 a[160:161], v32 offset:0\n" \
"v_accvgpr_read_b32 v212, a108\n" \
"v_mfma_f32_16x16x32_bf16 v[120:123], a[244:247], a[208:211], v[120:123]\n" \
"ds_read_b64_tr_b16 a[162:163], v32 offset:512\n" \
"v_accvgpr_read_b32 v213, a109\n" \
"v_mfma_f32_16x16x32_bf16 v[124:127], a[184:187], a[144:147], v[124:127]\n" \
"s_add_u32 m0, s40, 0x9000\n" \
"s_add_u32 s85, s39, 0xb000\n" \
"v_mfma_f32_16x16x32_bf16 v[124:127], a[248:251], a[208:211], v[124:127]\n" \
"buffer_load_dwordx4 v20, s[80:83], s85 offen sc1 lds\n" \
"v_mfma_f32_16x16x32_bf16 v[128:131], a[188:191], a[144:147], v[128:131]\n" \
"ds_read_b64_tr_b16 a[224:225], v32 offset:4096\n" \
"v_accvgpr_read_b32 v214, a110\n" \
"v_mfma_f32_16x16x32_bf16 v[128:131], a[252:255], a[208:211], v[128:131]\n" \
"ds_read_b128 a[144:147], v28 offset:40960\n" \
"v_accvgpr_read_b32 v215, a111\n" \
"v_mfma_f32_16x16x32_bf16 v[144:147], a[188:191], a[148:151], v[144:147]\n" \
"ds_read_b128 a[208:211], v29 offset:40960\n" \
"v_accvgpr_read_b32 v180, a124\n" \
"v_mfma_f32_16x16x32_bf16 v[144:147], a[252:255], a[212:215], v[144:147]\n" \
"s_add_u32 m0, s40, 0xb000\n" \
"s_movk_i32 s84, 0x280\n" \
"s_nop 0\n" \
"v_mfma_f32_16x16x32_bf16 v[140:143], a[184:187], a[148:151], v[140:143]\n" \
"buffer_load_dwordx4 v21, s[76:79], s84 offen offset:0 lds\n" \
"v_mfma_f32_16x16x32_bf16 v[140:143], a[248:251], a[212:215], v[140:143]\n" \
"ds_read_b64_tr_b16 a[226:227], v32 offset:4608\n" \
"v_accvgpr_read_b32 v181, a125\n" \
"v_mfma_f32_16x16x32_bf16 v[136:139], a[180:183], a[148:151], v[136:139]\n" \
"s_add_u32 s88, s42, 0x1000\n" \
"buffer_store_dwordx4 v[228:231], v20, s[80:83], s88 offen offset:2048 sc1\n" \
"v_mfma_f32_16x16x32_bf16 v[136:139], a[244:247], a[212:215], v[136:139]\n" \
"ds_read_b64_tr_b16 a[164:165], v33 offset:0\n" \
"v_accvgpr_read_b32 v182, a126\n" \
"v_mfma_f32_16x16x32_bf16 v[132:135], a[176:179], a[148:151], v[132:135]\n" \
"v_accvgpr_read_b32 v183, a127\n" \
"v_cvt_pk_bf16_f32 v212, v212, v213\n" \
"v_mfma_f32_16x16x32_bf16 v[132:135], a[240:243], a[212:215], v[132:135]\n" \
"ds_read_b128 a[148:151], v28 offset:43008\n" \
"v_cvt_pk_bf16_f32 v213, v214, v215\n" \
"v_mfma_f32_16x16x32_bf16 v[148:151], a[176:179], a[152:155], v[148:151]\n" \
"ds_read_b128 a[212:215], v29 offset:43008\n" \
"v_cvt_pk_bf16_f32 v214, v180, v181\n" \
"v_mfma_f32_16x16x32_bf16 v[148:151], a[240:243], a[216:219], v[148:151]\n" \
"s_add_u32 m0, s31, 0x8000\n" \
"s_mov_b32 s84, 0x280000\n" \
"v_mfma_f32_16x16x32_bf16 v[152:155], a[180:183], a[152:155], v[152:155]\n" \
"buffer_load_dwordx4 v26, s[68:71], s84 offen offset:0 lds\n" \
"v_mfma_f32_16x16x32_bf16 v[152:155], a[244:247], a[216:219], v[152:155]\n" \
"ds_read_b64_tr_b16 a[166:167], v33 offset:512\n" \
"v_cvt_pk_bf16_f32 v215, v182, v183\n" \
"v_mfma_f32_16x16x32_bf16 v[156:159], a[184:187], a[152:155], v[156:159]\n" \
"ds_read_b64_tr_b16 a[228:229], v33 offset:4096\n" \
"v_accvgpr_read_b32 v208, a104\n" \
"v_mfma_f32_16x16x32_bf16 v[156:159], a[248:251], a[216:219], v[156:159]\n" \
"s_addk_i32 s84, 0x7c00\n" \
"v_mfma_f32_16x16x32_bf16 v[160:163], a[188:191], a[152:155], v[160:163]\n" \
"buffer_load_dwordx4 v26, s[68:71], s84 offen offset:1024 lds\n" \
"v_mfma_f32_16x16x32_bf16 v[160:163], a[252:255], a[216:219], v[160:163]\n" \
"ds_read_b128 a[152:155], v28 offset:45056\n" \
"v_accvgpr_read_b32 v209, a105\n" \
"v_mfma_f32_16x16x32_bf16 v[176:179], a[188:191], a[156:159], v[176:179]\n" \
"ds_read_b128 a[216:219], v29 offset:45056\n" \
"v_accvgpr_read_b32 v210, a106\n" \
"v_mfma_f32_16x16x32_bf16 v[176:179], a[252:255], a[220:223], v[176:179]\n" \
"s_addk_i32 s84, 0x7c00\n" \
"v_mfma_f32_16x16x32_bf16 v[172:175], a[184:187], a[156:159], v[172:175]\n" \
"buffer_load_dwordx4 v27, s[68:71], s84 offen offset:2048 lds\n" \
"v_mfma_f32_16x16x32_bf16 v[172:175], a[248:251], a[220:223], v[172:175]\n" \
"ds_read_b64_tr_b16 a[230:231], v33 offset:4608\n" \
"v_accvgpr_read_b32 v211, a107\n" \
"v_mfma_f32_16x16x32_bf16 v[168:171], a[180:183], a[156:159], v[168:171]\n" \
"ds_read_b64_tr_b16 a[172:173], v35 offset:0\n" \
"v_accvgpr_read_b32 v180, a120\n" \
"v_mfma_f32_16x16x32_bf16 v[168:171], a[244:247], a[220:223], v[168:171]\n" \
"ds_read_b64_tr_b16 a[174:175], v35 offset:512\n" \
"v_accvgpr_read_b32 v181, a121\n" \
"v_mfma_f32_16x16x32_bf16 v[164:167], a[176:179], a[156:159], v[164:167]\n" \
"v_accvgpr_read_b32 v182, a122\n" \
"v_accvgpr_read_b32 v183, a123\n" \
"v_mfma_f32_16x16x32_bf16 v[164:167], a[240:243], a[220:223], v[164:167]\n" \
"ds_read_b128 a[156:159], v28 offset:47104\n" \
"v_cvt_pk_bf16_f32 v208, v208, v209\n" \
"v_mfma_f32_16x16x32_bf16 v[84:87], a[176:179], a[136:139], v[84:87]\n" \
"ds_read_b128 a[220:223], v29 offset:47104\n" \
"v_cvt_pk_bf16_f32 v209, v210, v211\n" \
"v_mfma_f32_16x16x32_bf16 v[84:87], a[240:243], a[200:203], v[84:87]\n" \
"s_addk_i32 s84, 0x7c00\n" \
"v_mfma_f32_16x16x32_bf16 v[88:91], a[180:183], a[136:139], v[88:91]\n" \
"buffer_load_dwordx4 v27, s[68:71], s84 offen offset:3072 lds\n" \
"v_mfma_f32_16x16x32_bf16 v[88:91], a[244:247], a[200:203], v[88:91]\n" \
"s_add_u32 m0, s30, 0x0\n" \
"s_movk_i32 s84, 0x300\n" \
"s_nop 0\n" \
"v_mfma_f32_16x16x32_bf16 v[92:95], a[184:187], a[136:139], v[92:95]\n" \
"buffer_load_dwordx4 v24, s[76:79], s84 offen offset:0 lds\n" \
"v_mfma_f32_16x16x32_bf16 v[92:95], a[248:251], a[200:203], v[92:95]\n" \
"ds_read_b64_tr_b16 a[236:237], v35 offset:4096\n" \
"v_cvt_pk_bf16_f32 v210, v180, v181\n" \
"v_mfma_f32_16x16x32_bf16 v[96:99], a[188:191], a[136:139], v[96:99]\n" \
"ds_read_b64_tr_b16 a[238:239], v35 offset:4608\n" \
"v_cvt_pk_bf16_f32 v211, v182, v183\n" \
"v_mfma_f32_16x16x32_bf16 v[96:99], a[252:255], a[200:203], v[96:99]\n" \
"ds_read_b128 a[136:139], v28 offset:36864\n" \
"v_accvgpr_read_b32 v204, a100\n" \
"v_mfma_f32_16x16x32_bf16 v[112:115], a[188:191], a[140:143], v[112:115]\n" \
"ds_read_b128 a[200:203], v29 offset:36864\n" \
"v_accvgpr_read_b32 v205, a101\n" \
"v_mfma_f32_16x16x32_bf16 v[112:115], a[252:255], a[204:207], v[112:115]\n" \
"s_movk_i32 s84, 0x2300\n" \
"s_nop 0\n" \
"v_mfma_f32_16x16x32_bf16 v[108:111], a[184:187], a[140:143], v[108:111]\n" \
"buffer_load_dwordx4 v25, s[76:79], s84 offen offset:1024 lds\n" \
"v_mfma_f32_16x16x32_bf16 v[108:111], a[248:251], a[204:207], v[108:111]\n" \
"s_add_u32 s89, s42, 0x1000\n" \
"buffer_store_dwordx4 v[232:235], v20, s[80:83], s89 offen offset:1024 sc1\n" \
"v_mfma_f32_16x16x32_bf16 v[104:107], a[180:183], a[140:143], v[104:107]\n" \
"ds_read_b64_tr_b16 a[168:169], v34 offset:0\n" \
"v_accvgpr_read_b32 v206, a102\n" \
"v_mfma_f32_16x16x32_bf16 v[104:107], a[244:247], a[204:207], v[104:107]\n" \
"ds_read_b64_tr_b16 a[170:171], v34 offset:512\n" \
"v_accvgpr_read_b32 v207, a103\n" \
"v_mfma_f32_16x16x32_bf16 v[100:103], a[176:179], a[140:143], v[100:103]\n" \
"ds_read_b64_tr_b16 a[232:233], v34 offset:4096\n" \
"v_accvgpr_read_b32 v180, a116\n" \
"v_mfma_f32_16x16x32_bf16 v[100:103], a[240:243], a[204:207], v[100:103]\n" \
"ds_read_b128 a[140:143], v28 offset:38912\n" \
"v_accvgpr_read_b32 v181, a117\n" \
"v_mfma_f32_16x16x32_bf16 v[52:55], a[176:179], a[128:131], v[52:55]\n" \
"ds_read_b128 a[204:207], v29 offset:38912\n" \
"v_accvgpr_read_b32 v182, a118\n" \
"v_mfma_f32_16x16x32_bf16 v[52:55], a[240:243], a[192:195], v[52:55]\n" \
"s_movk_i32 s84, 0x4300\n" \
"v_mfma_f32_16x16x32_bf16 v[56:59], a[180:183], a[128:131], v[56:59]\n" \
"buffer_load_dwordx4 v24, s[76:79], s84 offen offset:2048 lds\n" \
"v_mfma_f32_16x16x32_bf16 v[56:59], a[244:247], a[192:195], v[56:59]\n" \
"s_movk_i32 s84, 0x6300\n" \
"v_mfma_f32_16x16x32_bf16 v[60:63], a[184:187], a[128:131], v[60:63]\n" \
"buffer_load_dwordx4 v25, s[76:79], s84 offen offset:3072 lds\n" \
"v_mfma_f32_16x16x32_bf16 v[60:63], a[248:251], a[192:195], v[60:63]\n" \
"ds_read_b64_tr_b16 a[234:235], v34 offset:4608\n" \
"v_accvgpr_read_b32 v183, a119\n" \
"v_mfma_f32_16x16x32_bf16 v[64:67], a[188:191], a[128:131], v[64:67]\n" \
"v_cvt_pk_bf16_f32 v204, v204, v205\n" \
"v_cvt_pk_bf16_f32 v205, v206, v207\n" \
"v_mfma_f32_16x16x32_bf16 v[64:67], a[252:255], a[192:195], v[64:67]\n" \
"ds_read_b128 a[128:131], v28 offset:32768\n" \
"v_cvt_pk_bf16_f32 v206, v180, v181\n" \
"v_mfma_f32_16x16x32_bf16 v[80:83], a[188:191], a[132:135], v[80:83]\n" \
"ds_read_b128 a[192:195], v29 offset:32768\n" \
"v_cvt_pk_bf16_f32 v207, v182, v183\n" \
"v_mfma_f32_16x16x32_bf16 v[80:83], a[252:255], a[196:199], v[80:83]\n" \
"v_accvgpr_read_b32 v200, a96\n" \
"v_accvgpr_read_b32 v201, a97\n" \
"v_mfma_f32_16x16x32_bf16 v[76:79], a[184:187], a[132:135], v[76:79]\n" \
"v_accvgpr_read_b32 v202, a98\n" \
"v_accvgpr_read_b32 v203, a99\n" \
"v_mfma_f32_16x16x32_bf16 v[76:79], a[248:251], a[196:199], v[76:79]\n" \
"v_accvgpr_read_b32 v180, a112\n" \
"v_accvgpr_read_b32 v181, a113\n" \
"v_mfma_f32_16x16x32_bf16 v[72:75], a[180:183], a[132:135], v[72:75]\n" \
"v_accvgpr_read_b32 v182, a114\n" \
"v_accvgpr_read_b32 v183, a115\n" \
"v_mfma_f32_16x16x32_bf16 v[72:75], a[244:247], a[196:199], v[72:75]\n" \
"v_cvt_pk_bf16_f32 v200, v200, v201\n" \
"v_cvt_pk_bf16_f32 v201, v202, v203\n" \
"v_mfma_f32_16x16x32_bf16 v[68:71], a[176:179], a[132:135], v[68:71]\n" \
"v_cvt_pk_bf16_f32 v202, v180, v181\n" \
"v_cvt_pk_bf16_f32 v203, v182, v183\n" \
"v_mfma_f32_16x16x32_bf16 v[68:71], a[240:243], a[196:199], v[68:71]\n" \
"ds_read_b128 a[132:135], v28 offset:34816\n" \
"ds_read_b128 a[196:199], v29 offset:34816\n" \
"s_waitcnt lgkmcnt(15)\n" \
"v_mfma_f32_16x16x32_bf16 a[0:3], a[160:163], a[144:147], a[0:3]\n" \
"s_add_u32 m0, s30, 0x1000\n" \
"s_addk_i32 s84, 0x3000\n" \
"v_mfma_f32_16x16x32_bf16 a[0:3], a[224:227], a[208:211], a[0:3]\n" \
"buffer_load_dwordx4 v24, s[76:79], s84 offen offset:0 lds\n" \
"v_mfma_f32_16x16x32_bf16 a[4:7], a[164:167], a[144:147], a[4:7]\n" \
"ds_read_b64_tr_b16 a[176:177], v36 offset:0\n" \
"v_accvgpr_read_b32 v196, a76\n" \
"v_mfma_f32_16x16x32_bf16 a[4:7], a[228:231], a[208:211], a[4:7]\n" \
"ds_read_b64_tr_b16 a[178:179], v36 offset:1024\n" \
"v_accvgpr_read_b32 v197, a77\n" \
"v_mfma_f32_16x16x32_bf16 a[12:15], a[172:175], a[144:147], a[12:15]\n" \
"ds_read_b64_tr_b16 a[240:241], v36 offset:8192\n" \
"v_accvgpr_read_b32 v198, a78\n" \
"s_waitcnt lgkmcnt(15)\n" \
"v_mfma_f32_16x16x32_bf16 a[12:15], a[236:239], a[208:211], a[12:15]\n" \
"s_add_u32 s88, s42, 0x1000\n" \
"buffer_store_dwordx4 v[236:239], v20, s[80:83], s88 offen offset:0 sc1\n" \
"s_waitcnt lgkmcnt(11)\n" \
"v_mfma_f32_16x16x32_bf16 a[8:11], a[144:147], a[168:171], a[8:11]\n" \
"ds_read_b64_tr_b16 a[242:243], v36 offset:9216\n" \
"v_accvgpr_read_b32 v199, a79\n" \
"s_waitcnt lgkmcnt(8)\n" \
"s_nop 0\n" \
"v_mfma_f32_16x16x32_bf16 a[8:11], a[208:211], a[232:235], a[8:11]\n" \
"ds_read_b64_tr_b16 a[180:181], v37 offset:0\n" \
"v_accvgpr_read_b32 v180, a92\n" \
"v_mfma_f32_16x16x32_bf16 a[24:27], a[148:151], a[168:171], a[24:27]\n" \
"ds_read_b64_tr_b16 a[182:183], v37 offset:1024\n" \
"v_accvgpr_read_b32 v181, a93\n" \
"v_mfma_f32_16x16x32_bf16 a[24:27], a[212:215], a[232:235], a[24:27]\n" \
"ds_read_b64_tr_b16 a[244:245], v37 offset:8192\n" \
"v_accvgpr_read_b32 v182, a94\n" \
"v_mfma_f32_16x16x32_bf16 a[28:31], a[172:175], a[148:151], a[28:31]\n" \
"v_accvgpr_read_b32 v183, a95\n" \
"v_cvt_pk_bf16_f32 v196, v196, v197\n" \
"v_mfma_f32_16x16x32_bf16 a[28:31], a[236:239], a[212:215], a[28:31]\n" \
"s_addk_i32 s84, 0x2000\n" \
"s_nop 0\n" \
"v_mfma_f32_16x16x32_bf16 a[20:23], a[164:167], a[148:151], a[20:23]\n" \
"buffer_load_dwordx4 v25, s[76:79], s84 offen offset:1024 lds\n" \
"v_mfma_f32_16x16x32_bf16 a[20:23], a[228:231], a[212:215], a[20:23]\n" \
"ds_read_b64_tr_b16 a[246:247], v37 offset:9216\n" \
"v_cvt_pk_bf16_f32 v197, v198, v199\n" \
"v_mfma_f32_16x16x32_bf16 a[16:19], a[160:163], a[148:151], a[16:19]\n" \
"ds_read_b64_tr_b16 a[184:185], v38 offset:0\n" \
"v_cvt_pk_bf16_f32 v198, v180, v181\n" \
"v_mfma_f32_16x16x32_bf16 a[16:19], a[224:227], a[212:215], a[16:19]\n" \
"ds_read_b64_tr_b16 a[186:187], v38 offset:1024\n" \
"v_cvt_pk_bf16_f32 v199, v182, v183\n" \
"v_mfma_f32_16x16x32_bf16 a[32:35], a[160:163], a[152:155], a[32:35]\n" \
".fill (L_pb + 69344 - .) / 4, 4, 0xBF800000\n" \
"L_pp17:\n" \
"s_cbranch_execz L_pret\n" \
"s_addk_i32 s84, 0x2000\n" \
"v_mfma_f32_16x16x32_bf16 a[32:35], a[224:227], a[216:219], a[32:35]\n" \
"buffer_load_dwordx4 v24, s[76:79], s84 offen offset:2048 lds\n" \
"v_mfma_f32_16x16x32_bf16 a[36:39], a[164:167], a[152:155], a[36:39]\n" \
"ds_read_b64_tr_b16 a[248:249], v38 offset:8192\n" \
"v_accvgpr_read_b32 v192, a72\n" \
"v_mfma_f32_16x16x32_bf16 a[36:39], a[228:231], a[216:219], a[36:39]\n" \
"ds_read_b64_tr_b16 a[250:251], v38 offset:9216\n" \
"v_accvgpr_read_b32 v193, a73\n" \
"v_mfma_f32_16x16x32_bf16 a[44:47], a[172:175], a[152:155], a[44:47]\n" \
"ds_read_b64_tr_b16 a[188:189], v39 offset:0\n" \
"v_accvgpr_read_b32 v194, a74\n" \
"v_mfma_f32_16x16x32_bf16 a[44:47], a[236:239], a[216:219], a[44:47]\n" \
"ds_read_b64_tr_b16 a[190:191], v39 offset:1024\n" \
"v_accvgpr_read_b32 v195, a75\n" \
"v_mfma_f32_16x16x32_bf16 a[40:43], a[152:155], a[168:171], a[40:43]\n" \
"s_add_u32 s89, s42, 0x3000\n" \
"buffer_store_dwordx4 v[240:243], v20, s[80:83], s89 offen offset:3072 sc1\n" \
"v_mfma_f32_16x16x32_bf16 a[40:43], a[216:219], a[232:235], a[40:43]\n" \
"ds_read_b64_tr_b16 a[252:253], v39 offset:8192\n" \
"v_accvgpr_read_b32 v180, a88\n" \
"v_mfma_f32_16x16x32_bf16 a[56:59], a[156:159], a[168:171], a[56:59]\n" \
"ds_read_b64_tr_b16 a[254:255], v39 offset:9216\n" \
"v_accvgpr_read_b32 v181, a89\n" \
"v_mfma_f32_16x16x32_bf16 a[56:59], a[220:223], a[232:235], a[56:59]\n" \
"v_accvgpr_read_b32 v182, a90\n" \
"v_accvgpr_read_b32 v183, a91\n" \
"v_mfma_f32_16x16x32_bf16 a[60:63], a[172:175], a[156:159], a[60:63]\n" \
"v_cvt_pk_bf16_f32 v192, v192, v193\n" \
"v_cvt_pk_bf16_f32 v193, v194, v195\n" \
"v_mfma_f32_16x16x32_bf16 a[60:63], a[236:239], a[220:223], a[60:63]\n" \
"s_addk_i32 s84, 0x2000\n" \
"v_mfma_f32_16x16x32_bf16 a[52:55], a[164:167], a[156:159], a[52:55]\n" \
"buffer_load_dwordx4 v25, s[76:79], s84 offen offset:3072 lds\n" \
"v_mfma_f32_16x16x32_bf16 a[52:55], a[228:231], a[220:223], a[52:55]\n" \
"v_cvt_pk_bf16_f32 v194, v180, v181\n" \
"v_cvt_pk_bf16_f32 v195, v182, v183\n" \
"v_mfma_f32_16x16x32_bf16 a[48:51], a[160:163], a[156:159], a[48:51]\n" \
"s_and_b32 s12, s2, 1\n" \
"v_mfma_f32_16x16x32_bf16 a[48:51], a[224:227], a[220:223], a[48:51]\n" \
"s_cmp_lg_u32 s12, 0\n" \
"s_cselect_b32 s13, s16, s13\n" \
"v_mfma_f32_16x16x32_bf16 a[64:67], a[164:167], a[140:143], a[64:67]\n" \
"s_bfe_u32 s16, s2, 0x20001\n" \
"s_addk_i32 s13, 0x4000\n" \
"v_mfma_f32_16x16x32_bf16 a[64:67], a[228:231], a[204:207], a[64:67]\n" \
"s_xor_b32 s16, s16, s3\n" \
"v_mfma_f32_16x16x32_bf16 a[80:83], a[172:175], a[140:143], a[80:83]\n" \
"s_cmp_lg_u32 s16, 0\n" \
"s_cselect_b32 s13, s7, s13\n" \
"v_mfma_f32_16x16x32_bf16 a[80:83], a[236:239], a[204:207], a[80:83]\n" \
"s_lshl_b32 s6, s6, 16\n" \
"v_writelane_b32 v11, s13, 40\n" \
"v_mfma_f32_16x16x32_bf16 a[68:71], a[140:143], a[168:171], a[68:71]\n" \
"s_add_i32 s13, s8, 0x4240000\n" \
"s_add_i32 s6, s13, s6\n" \
"s_waitcnt vmcnt(11) lgkmcnt(0)\n" \
"v_mfma_f32_16x16x32_bf16 a[68:71], a[204:207], a[232:235], a[68:71]\n" \
"s_barrier\n" \
"v_mov_b32_e32 v45, s62\n" \
"s_mov_b64 exec, 1\n" \
"buffer_store_dword v45, off, s[80:83], s37 offset:0 sc1\n" \
"s_mov_b64 exec, -1\n" \
"v_xor_b32_e32 v28, 0x20000, v28\n" \
"v_xor_b32_e32 v29, 0x20000, v29\n" \
"v_mfma_f32_16x16x32_bf16 v[116:119], a[176:179], a[144:147], v[116:119]\n" \
"s_add_u32 m0, s40, 0x0\n" \
"s_add_u32 s85, s39, 0xc000\n" \
"v_mfma_f32_16x16x32_bf16 v[116:119], a[240:243], a[208:211], v[116:119]\n" \
"buffer_load_dwordx4 v20, s[80:83], s85 offen sc1 lds\n" \
"v_mfma_f32_16x16x32_bf16 v[120:123], a[180:183], a[144:147], v[120:123]\n" \
"ds_read_b64_tr_b16 a[160:161], v32 offset:32768\n" \
"v_writelane_b32 v11, s6, 41\n" \
"v_mfma_f32_16x16x32_bf16 v[120:123], a[244:247], a[208:211], v[120:123]\n" \
"ds_read_b64_tr_b16 a[162:163], v32 offset:33280\n" \
"s_add_i32 s8, s8, 0x42c0000\n" \
"v_mfma_f32_16x16x32_bf16 v[124:127], a[184:187], a[144:147], v[124:127]\n" \
"s_add_u32 m0, s40, 0x1000\n" \
"s_add_u32 s85, s39, 0xd000\n" \
"v_mfma_f32_16x16x32_bf16 v[124:127], a[248:251], a[208:211], v[124:127]\n" \
"buffer_load_dwordx4 v20, s[80:83], s85 offen sc1 lds\n" \
"v_mfma_f32_16x16x32_bf16 v[128:131], a[188:191], a[144:147], v[128:131]\n" \
"ds_read_b64_tr_b16 a[224:225], v32 offset:36864\n" \
"s_lshl_b32 s6, s12, 10\n" \
"v_mfma_f32_16x16x32_bf16 v[128:131], a[252:255], a[208:211], v[128:131]\n" \
"ds_read_b128 a[144:147], v28 offset:8192\n" \
"s_or_b32 s6, s8, s6\n" \
"v_mfma_f32_16x16x32_bf16 v[144:147], a[188:191], a[148:151], v[144:147]\n" \
"ds_read_b128 a[208:211], v29 offset:8192\n" \
"v_mfma_f32_16x16x32_bf16 v[144:147], a[252:255], a[212:215], v[144:147]\n" \
"s_add_u32 m0, s40, 0x3000\n" \
"s_movk_i32 s84, 0x300\n" \
"v_mfma_f32_16x16x32_bf16 v[140:143], a[184:187], a[148:151], v[140:143]\n" \
"buffer_load_dwordx4 v21, s[76:79], s84 offen offset:0 lds\n" \
"v_mfma_f32_16x16x32_bf16 v[140:143], a[248:251], a[212:215], v[140:143]\n" \
"ds_read_b64_tr_b16 a[226:227], v32 offset:37376\n" \
"v_mfma_f32_16x16x32_bf16 v[136:139], a[180:183], a[148:151], v[136:139]\n" \
"s_cmp_lg_u32 s16, 0\n" \
"s_cselect_b32 s6, s7, s6\n" \
"v_mfma_f32_16x16x32_bf16 v[136:139], a[244:247], a[212:215], v[136:139]\n" \
"ds_read_b64_tr_b16 a[164:165], v33 offset:32768\n" \
"s_mulk_i32 s5, 0x4800\n" \
"v_mfma_f32_16x16x32_bf16 v[132:135], a[176:179], a[148:151], v[132:135]\n" \
"v_writelane_b32 v11, s6, 42\n" \
"s_add_i32 s6, s14, s10\n" \
"v_mfma_f32_16x16x32_bf16 v[132:135], a[240:243], a[212:215], v[132:135]\n" \
"ds_read_b128 a[148:151], v28 offset:10240\n" \
"v_writelane_b32 v11, s6, 43\n" \
"v_mfma_f32_16x16x32_bf16 v[148:151], a[176:179], a[152:155], v[148:151]\n" \
"ds_read_b128 a[212:215], v29 offset:10240\n" \
"s_add_i32 s6, s9, 0x21c000\n" \
"v_mfma_f32_16x16x32_bf16 v[148:151], a[240:243], a[216:219], v[148:151]\n" \
"s_add_u32 m0, s31, 0x0\n" \
"s_mov_b32 s84, 0x300000\n" \
"v_mfma_f32_16x16x32_bf16 v[152:155], a[180:183], a[152:155], v[152:155]\n" \
"buffer_load_dwordx4 v26, s[68:71], s84 offen offset:0 lds\n" \
"v_mfma_f32_16x16x32_bf16 v[152:155], a[244:247], a[216:219], v[152:155]\n" \
"s_addk_i32 s84, 0x7c00\n" \
"v_mfma_f32_16x16x32_bf16 v[156:159], a[184:187], a[152:155], v[156:159]\n" \
"buffer_load_dwordx4 v26, s[68:71], s84 offen offset:1024 lds\n" \
"v_mfma_f32_16x16x32_bf16 v[156:159], a[248:251], a[216:219], v[156:159]\n" \
"ds_read_b64_tr_b16 a[166:167], v33 offset:33280\n" \
"s_add_i32 s9, s6, s10\n" \
"v_mfma_f32_16x16x32_bf16 v[160:163], a[188:191], a[152:155], v[160:163]\n" \
"ds_read_b64_tr_b16 a[228:229], v33 offset:36864\n" \
"v_mfma_f32_16x16x32_bf16 v[160:163], a[252:255], a[216:219], v[160:163]\n" \
"ds_read_b128 a[152:155], v28 offset:12288\n" \
"v_mfma_f32_16x16x32_bf16 v[176:179], a[188:191], a[156:159], v[176:179]\n" \
"ds_read_b128 a[216:219], v29 offset:12288\n" \
"v_mfma_f32_16x16x32_bf16 v[176:179], a[252:255], a[220:223], v[176:179]\n" \
"s_addk_i32 s84, 0x7c00\n" \
"v_mfma_f32_16x16x32_bf16 v[172:175], a[184:187], a[156:159], v[172:175]\n" \
"buffer_load_dwordx4 v27, s[68:71], s84 offen offset:2048 lds\n" \
"v_mfma_f32_16x16x32_bf16 v[172:175], a[248:251], a[220:223], v[172:175]\n" \
"ds_read_b64_tr_b16 a[230:231], v33 offset:37376\n" \
"v_mfma_f32_16x16x32_bf16 v[168:171], a[180:183], a[156:159], v[168:171]\n" \
"s_addk_i32 s84, 0x7c00\n" \
"v_mfma_f32_16x16x32_bf16 v[168:171], a[244:247], a[220:223], v[168:171]\n" \
"buffer_load_dwordx4 v27, s[68:71], s84 offen offset:3072 lds\n" \
"v_mfma_f32_16x16x32_bf16 v[164:167], a[176:179], a[156:159], v[164:167]\n" \
"ds_read_b64_tr_b16 a[172:173], v35 offset:32768\n" \
"v_mfma_f32_16x16x32_bf16 v[164:167], a[240:243], a[220:223], v[164:167]\n" \
"ds_read_b128 a[156:159], v28 offset:14336\n" \
"v_mfma_f32_16x16x32_bf16 v[84:87], a[176:179], a[136:139], v[84:87]\n" \
"ds_read_b128 a[220:223], v29 offset:14336\n" \
"v_mfma_f32_16x16x32_bf16 v[84:87], a[240:243], a[200:203], v[84:87]\n" \
"s_add_u32 s88, s42, 0x3000\n" \
"buffer_store_dwordx4 v[244:247], v20, s[80:83], s88 offen offset:2048 sc1\n" \
"v_mfma_f32_16x16x32_bf16 v[88:91], a[180:183], a[136:139], v[88:91]\n" \
"ds_read_b64_tr_b16 a[174:175], v35 offset:33280\n" \
"v_mfma_f32_16x16x32_bf16 v[88:91], a[244:247], a[200:203], v[88:91]\n" \
"ds_read_b64_tr_b16 a[236:237], v35 offset:36864\n" \
"v_mfma_f32_16x16x32_bf16 v[92:95], a[184:187], a[136:139], v[92:95]\n" \
"ds_read_b64_tr_b16 a[238:239], v35 offset:37376\n" \
"v_mfma_f32_16x16x32_bf16 v[92:95], a[248:251], a[200:203], v[92:95]\n" \
"s_add_u32 m0, s30, 0x8000\n" \
"s_movk_i32 s84, 0x380\n" \
"v_mfma_f32_16x16x32_bf16 v[96:99], a[188:191], a[136:139], v[96:99]\n" \
"buffer_load_dwordx4 v24, s[76:79], s84 offen offset:0 lds\n" \
"v_mfma_f32_16x16x32_bf16 v[96:99], a[252:255], a[200:203], v[96:99]\n" \
"ds_read_b128 a[136:139], v28 offset:4096\n" \
"v_mfma_f32_16x16x32_bf16 v[112:115], a[188:191], a[140:143], v[112:115]\n" \
"ds_read_b128 a[200:203], v29 offset:4096\n" \
"v_mfma_f32_16x16x32_bf16 v[112:115], a[252:255], a[204:207], v[112:115]\n" \
"ds_read_b64_tr_b16 a[168:169], v34 offset:32768\n" \
"v_mfma_f32_16x16x32_bf16 v[108:111], a[184:187], a[140:143], v[108:111]\n" \
"s_movk_i32 s84, 0x2380\n" \
"v_mfma_f32_16x16x32_bf16 v[108:111], a[248:251], a[204:207], v[108:111]\n" \
"buffer_load_dwordx4 v25, s[76:79], s84 offen offset:1024 lds\n" \
"v_mfma_f32_16x16x32_bf16 v[104:107], a[180:183], a[140:143], v[104:107]\n" \
"ds_read_b64_tr_b16 a[170:171], v34 offset:33280\n" \
"v_mfma_f32_16x16x32_bf16 v[104:107], a[244:247], a[204:207], v[104:107]\n" \
"ds_read_b64_tr_b16 a[232:233], v34 offset:36864\n" \
"v_mfma_f32_16x16x32_bf16 v[100:103], a[176:179], a[140:143], v[100:103]\n" \
"s_cmp_lg_u32 s12, 0\n" \
"s_cselect_b32 s6, s6, s14\n" \
"v_mfma_f32_16x16x32_bf16 v[100:103], a[240:243], a[204:207], v[100:103]\n" \
"ds_read_b128 a[140:143], v28 offset:6144\n" \
"s_addk_i32 s6, 0x100\n" \
"v_mfma_f32_16x16x32_bf16 v[52:55], a[176:179], a[128:131], v[52:55]\n" \
"ds_read_b128 a[204:207], v29 offset:6144\n" \
"v_writelane_b32 v11, s9, 44\n" \
"v_mfma_f32_16x16x32_bf16 v[52:55], a[240:243], a[192:195], v[52:55]\n" \
"s_movk_i32 s84, 0x4380\n" \
"v_mfma_f32_16x16x32_bf16 v[56:59], a[180:183], a[128:131], v[56:59]\n" \
"buffer_load_dwordx4 v24, s[76:79], s84 offen offset:2048 lds\n" \
"v_mfma_f32_16x16x32_bf16 v[56:59], a[244:247], a[192:195], v[56:59]\n" \
"s_movk_i32 s84, 0x6380\n" \
"v_mfma_f32_16x16x32_bf16 v[60:63], a[184:187], a[128:131], v[60:63]\n" \
"buffer_load_dwordx4 v25, s[76:79], s84 offen offset:3072 lds\n" \
"v_mfma_f32_16x16x32_bf16 v[60:63], a[248:251], a[192:195], v[60:63]\n" \
"ds_read_b64_tr_b16 a[234:235], v34 offset:37376\n" \
"v_mfma_f32_16x16x32_bf16 v[64:67], a[188:191], a[128:131], v[64:67]\n" \
"s_cmp_lg_u32 s16, 0\n" \
"s_cselect_b32 s6, s7, s6\n" \
"v_mfma_f32_16x16x32_bf16 v[64:67], a[252:255], a[192:195], v[64:67]\n" \
"ds_read_b128 a[128:131], v28 offset:0\n" \
"v_writelane_b32 v11, s6, 45\n" \
"v_mfma_f32_16x16x32_bf16 v[80:83], a[188:191], a[132:135], v[80:83]\n" \
"ds_read_b128 a[192:195], v29 offset:0\n" \
"s_or_b32 s6, s8, s15\n" \
"v_mfma_f32_16x16x32_bf16 v[80:83], a[252:255], a[196:199], v[80:83]\n" \
"v_writelane_b32 v11, s6, 53\n" \
"s_lshl_b32 s6, s3, 17\n" \
"v_mfma_f32_16x16x32_bf16 v[76:79], a[184:187], a[132:135], v[76:79]\n" \
"s_add_i32 s6, s13, s6\n" \
"s_or_b32 s4, s6, s4\n" \
"v_mfma_f32_16x16x32_bf16 v[76:79], a[248:251], a[196:199], v[76:79]\n" \
"s_or_b32 s4, s4, 0x3840\n" \
"v_writelane_b32 v11, s4, 6\n" \
"v_mfma_f32_16x16x32_bf16 v[72:75], a[180:183], a[132:135], v[72:75]\n" \
"s_mov_b32 s4, 0xff0000\n" \
"v_mfma_f32_16x16x32_bf16 v[72:75], a[244:247], a[196:199], v[72:75]\n" \
"s_cmp_lg_u32 s16, 0\n" \
"s_cselect_b32 s4, s17, s4\n" \
"v_mfma_f32_16x16x32_bf16 v[68:71], a[176:179], a[132:135], v[68:71]\n" \
"v_writelane_b32 v11, s4, 7\n" \
"s_mul_i32 s4, s3, 0x1200\n" \
"v_mfma_f32_16x16x32_bf16 v[68:71], a[240:243], a[196:199], v[68:71]\n" \
"ds_read_b128 a[132:135], v28 offset:2048\n" \
"ds_read_b128 a[196:199], v29 offset:2048\n" \
"s_waitcnt lgkmcnt(15)\n" \
"v_mfma_f32_16x16x32_bf16 a[0:3], a[160:163], a[144:147], a[0:3]\n" \
"s_add_u32 m0, s30, 0x9000\n" \
"s_addk_i32 s84, 0x3000\n" \
"v_mfma_f32_16x16x32_bf16 a[0:3], a[224:227], a[208:211], a[0:3]\n" \
"buffer_load_dwordx4 v24, s[76:79], s84 offen offset:0 lds\n" \
"v_mfma_f32_16x16x32_bf16 a[4:7], a[164:167], a[144:147], a[4:7]\n" \
"ds_read_b64_tr_b16 a[176:177], v36 offset:32768\n" \
"s_add_i32 s4, s4, s5\n" \
"v_mfma_f32_16x16x32_bf16 a[4:7], a[228:231], a[208:211], a[4:7]\n" \
"ds_read_b64_tr_b16 a[178:179], v36 offset:33792\n" \
"s_add_i32 s4, s4, s11\n" \
"s_waitcnt lgkmcnt(15)\n" \
"v_mfma_f32_16x16x32_bf16 a[12:15], a[172:175], a[144:147], a[12:15]\n" \
"ds_read_b64_tr_b16 a[240:241], v36 offset:40960\n" \
"v_writelane_b32 v11, s4, 8\n" \
"s_waitcnt lgkmcnt(15)\n" \
"v_mfma_f32_16x16x32_bf16 a[12:15], a[236:239], a[208:211], a[12:15]\n" \
"s_add_u32 s89, s42, 0x3000\n" \
"buffer_store_dwordx4 v[184:187], v20, s[80:83], s89 offen offset:1024 sc1\n" \
"s_waitcnt lgkmcnt(11)\n" \
"v_mfma_f32_16x16x32_bf16 a[8:11], a[144:147], a[168:171], a[8:11]\n" \
"ds_read_b64_tr_b16 a[242:243], v36 offset:41984\n" \
"s_lshl_b32 s4, s2, 16\n" \
"s_waitcnt lgkmcnt(8)\n" \
"v_mfma_f32_16x16x32_bf16 a[8:11], a[208:211], a[232:235], a[8:11]\n" \
"ds_read_b64_tr_b16 a[180:181], v37 offset:32768\n" \
"s_add_i32 s5, s4, 0x10000\n" \
"v_mfma_f32_16x16x32_bf16 a[24:27], a[148:151], a[168:171], a[24:27]\n" \
"ds_read_b64_tr_b16 a[182:183], v37 offset:33792\n" \
"s_and_b32 s5, s5, 0x70000\n" \
"v_mfma_f32_16x16x32_bf16 a[24:27], a[212:215], a[232:235], a[24:27]\n" \
"ds_read_b64_tr_b16 a[244:245], v37 offset:40960\n" \
"s_add_i32 s5, s13, s5\n" \
"v_mfma_f32_16x16x32_bf16 a[28:31], a[172:175], a[148:151], a[28:31]\n" \
"v_writelane_b32 v11, s5, 46\n" \
"s_add_i32 s5, s4, 0x20000\n" \
"v_mfma_f32_16x16x32_bf16 a[28:31], a[236:239], a[212:215], a[28:31]\n" \
"s_addk_i32 s84, 0x2000\n" \
"v_mfma_f32_16x16x32_bf16 a[20:23], a[164:167], a[148:151], a[20:23]\n" \
"buffer_load_dwordx4 v25, s[76:79], s84 offen offset:1024 lds\n" \
"v_mfma_f32_16x16x32_bf16 a[20:23], a[228:231], a[212:215], a[20:23]\n" \
"ds_read_b64_tr_b16 a[246:247], v37 offset:41984\n" \
"s_and_b32 s5, s5, 0x70000\n" \
"v_mfma_f32_16x16x32_bf16 a[16:19], a[160:163], a[148:151], a[16:19]\n" \
"ds_read_b64_tr_b16 a[184:185], v38 offset:32768\n" \
"s_add_i32 s5, s13, s5\n" \
"v_mfma_f32_16x16x32_bf16 a[16:19], a[224:227], a[212:215], a[16:19]\n" \
"ds_read_b64_tr_b16 a[186:187], v38 offset:33792\n" \
"s_bitset1_b32 s5, 11\n" \
"v_mfma_f32_16x16x32_bf16 a[32:35], a[160:163], a[152:155], a[32:35]\n" \
"s_addk_i32 s84, 0x2000\n" \
"s_nop 0\n" \
"v_mfma_f32_16x16x32_bf16 a[32:35], a[224:227], a[216:219], a[32:35]\n" \
"buffer_load_dwordx4 v24, s[76:79], s84 offen offset:2048 lds\n" \
"v_mfma_f32_16x16x32_bf16 a[36:39], a[164:167], a[152:155], a[36:39]\n" \
"ds_read_b64_tr_b16 a[248:249], v38 offset:40960\n" \
"v_writelane_b32 v11, s5, 47\n" \
"v_mfma_f32_16x16x32_bf16 a[36:39], a[228:231], a[216:219], a[36:39]\n" \
"ds_read_b64_tr_b16 a[250:251], v38 offset:41984\n" \
"s_add_i32 s5, s4, 0x30000\n" \
"v_mfma_f32_16x16x32_bf16 a[44:47], a[172:175], a[152:155], a[44:47]\n" \
"ds_read_b64_tr_b16 a[188:189], v39 offset:32768\n" \
"s_and_b32 s5, s5, 0x70000\n" \
"v_mfma_f32_16x16x32_bf16 a[44:47], a[236:239], a[216:219], a[44:47]\n" \
"ds_read_b64_tr_b16 a[190:191], v39 offset:33792\n" \
"s_add_i32 s5, s13, s5\n" \
"v_mfma_f32_16x16x32_bf16 a[40:43], a[152:155], a[168:171], a[40:43]\n" \
"s_add_u32 s88, s42, 0x3000\n" \
"buffer_store_dwordx4 v[188:191], v20, s[80:83], s88 offen offset:0 sc1\n" \
"v_mfma_f32_16x16x32_bf16 a[40:43], a[216:219], a[232:235], a[40:43]\n" \
"ds_read_b64_tr_b16 a[252:253], v39 offset:40960\n" \
"s_bitset1_b32 s5, 12\n" \
"v_mfma_f32_16x16x32_bf16 a[56:59], a[156:159], a[168:171], a[56:59]\n" \
"ds_read_b64_tr_b16 a[254:255], v39 offset:41984\n" \
"v_writelane_b32 v11, s5, 48\n" \
"v_mfma_f32_16x16x32_bf16 a[56:59], a[220:223], a[232:235], a[56:59]\n" \
"s_and_b32 s5, s4, 0x70000\n" \
"s_xor_b32 s5, s5, 0x40000\n" \
"v_mfma_f32_16x16x32_bf16 a[60:63], a[172:175], a[156:159], a[60:63]\n" \
"s_add_i32 s5, s13, s5\n" \
"s_or_b32 s5, s5, 0x1800\n" \
"v_mfma_f32_16x16x32_bf16 a[60:63], a[236:239], a[220:223], a[60:63]\n" \
"s_addk_i32 s84, 0x2000\n" \
"v_mfma_f32_16x16x32_bf16 a[52:55], a[164:167], a[156:159], a[52:55]\n" \
"buffer_load_dwordx4 v25, s[76:79], s84 offen offset:3072 lds\n" \
"v_mfma_f32_16x16x32_bf16 a[52:55], a[228:231], a[220:223], a[52:55]\n" \
"v_writelane_b32 v11, s5, 49\n" \
"s_add_i32 s5, s4, 0x50000\n" \
"v_mfma_f32_16x16x32_bf16 a[48:51], a[160:163], a[156:159], a[48:51]\n" \
"s_and_b32 s5, s5, 0x70000\n" \
"s_add_i32 s5, s13, s5\n" \
"v_mfma_f32_16x16x32_bf16 a[48:51], a[224:227], a[220:223], a[48:51]\n" \
"s_bitset1_b32 s5, 13\n" \
"v_writelane_b32 v11, s5, 50\n" \
"v_mfma_f32_16x16x32_bf16 a[64:67], a[164:167], a[140:143], a[64:67]\n" \
"s_add_i32 s5, s4, 0x60000\n" \
"s_and_b32 s5, s5, 0x70000\n" \
"v_mfma_f32_16x16x32_bf16 a[64:67], a[228:231], a[204:207], a[64:67]\n" \
"s_add_i32 s4, s4, 0x70000\n" \
"s_add_i32 s5, s13, s5\n" \
"v_mfma_f32_16x16x32_bf16 a[80:83], a[172:175], a[140:143], a[80:83]\n" \
"s_and_b32 s4, s4, 0x70000\n" \
"s_or_b32 s5, s5, 0x2800\n" \
"v_mfma_f32_16x16x32_bf16 a[80:83], a[236:239], a[204:207], a[80:83]\n" \
"v_writelane_b32 v11, s5, 51\n" \
"s_add_i32 s13, s13, s4\n" \
"v_mfma_f32_16x16x32_bf16 a[68:71], a[140:143], a[168:171], a[68:71]\n" \
"s_or_b32 s4, s13, 0x3000\n" \
"v_writelane_b32 v11, s4, 52\n" \
"s_waitcnt vmcnt(11) lgkmcnt(0)\n" \
"v_mfma_f32_16x16x32_bf16 a[68:71], a[204:207], a[232:235], a[68:71]\n" \
"s_barrier\n" \
"v_xor_b32_e32 v28, 0x20000, v28\n" \
"v_xor_b32_e32 v29, 0x20000, v29\n" \
"v_mfma_f32_16x16x32_bf16 v[116:119], a[176:179], a[144:147], v[116:119]\n" \
"s_add_u32 m0, s40, 0x8000\n" \
"s_add_u32 s85, s39, 0xe000\n" \
"v_mfma_f32_16x16x32_bf16 v[116:119], a[240:243], a[208:211], v[116:119]\n" \
"buffer_load_dwordx4 v20, s[80:83], s85 offen sc1 lds\n" \
"v_mfma_f32_16x16x32_bf16 v[120:123], a[180:183], a[144:147], v[120:123]\n" \
"ds_read_b64_tr_b16 a[160:161], v32 offset:0\n" \
"s_movk_i32 s11, 0x4000\n" \
"v_mfma_f32_16x16x32_bf16 v[120:123], a[244:247], a[208:211], v[120:123]\n" \
"ds_read_b64_tr_b16 a[162:163], v32 offset:512\n" \
"s_lshl_b32 s4, s2, 2\n" \
"v_mfma_f32_16x16x32_bf16 v[124:127], a[184:187], a[144:147], v[124:127]\n" \
"s_add_u32 m0, s40, 0x9000\n" \
"s_add_u32 s85, s39, 0xf000\n" \
"v_mfma_f32_16x16x32_bf16 v[124:127], a[248:251], a[208:211], v[124:127]\n" \
"buffer_load_dwordx4 v20, s[80:83], s85 offen sc1 lds\n" \
"v_mfma_f32_16x16x32_bf16 v[128:131], a[188:191], a[144:147], v[128:131]\n" \
"ds_read_b64_tr_b16 a[224:225], v32 offset:4096\n" \
"s_and_b32 s4, s4, 28\n" \
"v_mfma_f32_16x16x32_bf16 v[128:131], a[252:255], a[208:211], v[128:131]\n" \
"ds_read_b128 a[144:147], v28 offset:8192\n" \
"s_bfe_u32 s5, s2, 0x20003\n" \
"v_mfma_f32_16x16x32_bf16 v[144:147], a[188:191], a[148:151], v[144:147]\n" \
"ds_read_b128 a[208:211], v29 offset:8192\n" \
"s_or_b32 s4, s4, s5\n" \
"v_mfma_f32_16x16x32_bf16 v[144:147], a[252:255], a[212:215], v[144:147]\n" \
"s_add_u32 m0, s40, 0xb000\n" \
"s_movk_i32 s84, 0x380\n" \
"v_mfma_f32_16x16x32_bf16 v[140:143], a[184:187], a[148:151], v[140:143]\n" \
"buffer_load_dwordx4 v21, s[76:79], s84 offen offset:0 lds\n" \
"v_mfma_f32_16x16x32_bf16 v[140:143], a[248:251], a[212:215], v[140:143]\n" \
"ds_read_b64_tr_b16 a[226:227], v32 offset:4608\n" \
"s_lshr_b32 s5, s2, 5\n" \
"v_mfma_f32_16x16x32_bf16 v[136:139], a[180:183], a[148:151], v[136:139]\n" \
"s_lshr_b32 s19, s3, 1\n" \
"s_lshl_b32 s18, s5, 1\n" \
"v_mfma_f32_16x16x32_bf16 v[136:139], a[244:247], a[212:215], v[136:139]\n" \
"ds_read_b64_tr_b16 a[164:165], v33 offset:0\n" \
"s_lshl_b32 s6, s4, 20\n" \
"v_mfma_f32_16x16x32_bf16 v[132:135], a[176:179], a[148:151], v[132:135]\n" \
"s_lshl_b32 s7, s4, 8\n" \
"s_and_b32 s13, s3, 1\n" \
"v_mfma_f32_16x16x32_bf16 v[132:135], a[240:243], a[212:215], v[132:135]\n" \
"ds_read_b128 a[148:151], v28 offset:10240\n" \
"s_add_i32 s14, s18, s19\n" \
"v_mfma_f32_16x16x32_bf16 v[148:151], a[176:179], a[152:155], v[148:151]\n" \
"ds_read_b128 a[212:215], v29 offset:10240\n" \
"v_mfma_f32_16x16x32_bf16 v[148:151], a[240:243], a[216:219], v[148:151]\n" \
"s_add_u32 m0, s31, 0x8000\n" \
"s_mov_b32 s84, 0x380000\n" \
"v_mfma_f32_16x16x32_bf16 v[152:155], a[180:183], a[152:155], v[152:155]\n" \
"buffer_load_dwordx4 v26, s[68:71], s84 offen offset:0 lds\n" \
"v_mfma_f32_16x16x32_bf16 v[152:155], a[244:247], a[216:219], v[152:155]\n" \
"s_addk_i32 s84, 0x7c00\n" \
"s_nop 0\n" \
"v_mfma_f32_16x16x32_bf16 v[156:159], a[184:187], a[152:155], v[156:159]\n" \
"buffer_load_dwordx4 v26, s[68:71], s84 offen offset:1024 lds\n" \
"v_mfma_f32_16x16x32_bf16 v[156:159], a[248:251], a[216:219], v[156:159]\n" \
"ds_read_b64_tr_b16 a[166:167], v33 offset:512\n" \
"v_mfma_f32_16x16x32_bf16 v[160:163], a[188:191], a[152:155], v[160:163]\n" \
"ds_read_b64_tr_b16 a[228:229], v33 offset:4096\n" \
"v_mfma_f32_16x16x32_bf16 v[160:163], a[252:255], a[216:219], v[160:163]\n" \
"ds_read_b128 a[152:155], v28 offset:12288\n" \
"v_mfma_f32_16x16x32_bf16 v[176:179], a[188:191], a[156:159], v[176:179]\n" \
"ds_read_b128 a[216:219], v29 offset:12288\n" \
"v_mfma_f32_16x16x32_bf16 v[176:179], a[252:255], a[220:223], v[176:179]\n" \
"s_addk_i32 s84, 0x7c00\n" \
"v_mfma_f32_16x16x32_bf16 v[172:175], a[184:187], a[156:159], v[172:175]\n" \
"buffer_load_dwordx4 v27, s[68:71], s84 offen offset:2048 lds\n" \
"v_mfma_f32_16x16x32_bf16 v[172:175], a[248:251], a[220:223], v[172:175]\n" \
"ds_read_b64_tr_b16 a[230:231], v33 offset:4608\n" \
"v_mfma_f32_16x16x32_bf16 v[168:171], a[180:183], a[156:159], v[168:171]\n" \
"s_addk_i32 s84, 0x7c00\n" \
"v_mfma_f32_16x16x32_bf16 v[168:171], a[244:247], a[220:223], v[168:171]\n" \
"buffer_load_dwordx4 v27, s[68:71], s84 offen offset:3072 lds\n" \
"v_mfma_f32_16x16x32_bf16 v[164:167], a[176:179], a[156:159], v[164:167]\n" \
"ds_read_b64_tr_b16 a[172:173], v35 offset:0\n" \
"v_mfma_f32_16x16x32_bf16 v[164:167], a[240:243], a[220:223], v[164:167]\n" \
"ds_read_b128 a[156:159], v28 offset:14336\n" \
"v_mfma_f32_16x16x32_bf16 v[84:87], a[176:179], a[136:139], v[84:87]\n" \
"ds_read_b128 a[220:223], v29 offset:14336\n" \
"v_mfma_f32_16x16x32_bf16 v[84:87], a[240:243], a[200:203], v[84:87]\n" \
"s_add_u32 s89, s42, 0x0\n" \
"buffer_store_dwordx4 v[192:195], v20, s[80:83], s89 offen offset:2048 sc1\n" \
"v_mfma_f32_16x16x32_bf16 v[88:91], a[180:183], a[136:139], v[88:91]\n" \
"ds_read_b64_tr_b16 a[174:175], v35 offset:512\n" \
"v_mfma_f32_16x16x32_bf16 v[88:91], a[244:247], a[200:203], v[88:91]\n" \
"ds_read_b64_tr_b16 a[236:237], v35 offset:4096\n" \
"v_mfma_f32_16x16x32_bf16 v[92:95], a[184:187], a[136:139], v[92:95]\n" \
"ds_read_b64_tr_b16 a[238:239], v35 offset:4608\n" \
"v_mfma_f32_16x16x32_bf16 v[92:95], a[248:251], a[200:203], v[92:95]\n" \
"s_add_u32 m0, s30, 0x20000\n" \
"s_movk_i32 s84, 0x400\n" \
"v_mfma_f32_16x16x32_bf16 v[96:99], a[188:191], a[136:139], v[96:99]\n" \
"buffer_load_dwordx4 v24, s[76:79], s84 offen offset:0 lds\n" \
"v_mfma_f32_16x16x32_bf16 v[96:99], a[252:255], a[200:203], v[96:99]\n" \
"ds_read_b128 a[136:139], v28 offset:4096\n" \
"v_mfma_f32_16x16x32_bf16 v[112:115], a[188:191], a[140:143], v[112:115]\n" \
"ds_read_b128 a[200:203], v29 offset:4096\n" \
"v_mfma_f32_16x16x32_bf16 v[112:115], a[252:255], a[204:207], v[112:115]\n" \
"ds_read_b64_tr_b16 a[168:169], v34 offset:0\n" \
"v_mfma_f32_16x16x32_bf16 v[108:111], a[184:187], a[140:143], v[108:111]\n" \
"s_movk_i32 s84, 0x2400\n" \
"v_mfma_f32_16x16x32_bf16 v[108:111], a[248:251], a[204:207], v[108:111]\n" \
"buffer_load_dwordx4 v25, s[76:79], s84 offen offset:1024 lds\n" \
"v_mfma_f32_16x16x32_bf16 v[104:107], a[180:183], a[140:143], v[104:107]\n" \
"ds_read_b64_tr_b16 a[170:171], v34 offset:512\n" \
"v_mfma_f32_16x16x32_bf16 v[104:107], a[244:247], a[204:207], v[104:107]\n" \
"ds_read_b64_tr_b16 a[232:233], v34 offset:4096\n" \
"v_mfma_f32_16x16x32_bf16 v[100:103], a[176:179], a[140:143], v[100:103]\n" \
"s_add_u32 s6, s26, s6\n" \
"s_addc_u32 s9, s27, 0\n" \
"v_mfma_f32_16x16x32_bf16 v[100:103], a[240:243], a[204:207], v[100:103]\n" \
"ds_read_b128 a[140:143], v28 offset:6144\n" \
"v_mfma_f32_16x16x32_bf16 v[52:55], a[176:179], a[128:131], v[52:55]\n" \
"ds_read_b128 a[204:207], v29 offset:6144\n" \
"v_mfma_f32_16x16x32_bf16 v[52:55], a[240:243], a[192:195], v[52:55]\n" \
"s_movk_i32 s84, 0x4400\n" \
"v_mfma_f32_16x16x32_bf16 v[56:59], a[180:183], a[128:131], v[56:59]\n" \
"buffer_load_dwordx4 v24, s[76:79], s84 offen offset:2048 lds\n" \
"v_mfma_f32_16x16x32_bf16 v[56:59], a[244:247], a[192:195], v[56:59]\n" \
"s_movk_i32 s84, 0x6400\n" \
"v_mfma_f32_16x16x32_bf16 v[60:63], a[184:187], a[128:131], v[60:63]\n" \
"buffer_load_dwordx4 v25, s[76:79], s84 offen offset:3072 lds\n" \
"v_mfma_f32_16x16x32_bf16 v[60:63], a[248:251], a[192:195], v[60:63]\n" \
"ds_read_b64_tr_b16 a[234:235], v34 offset:4608\n" \
"v_mfma_f32_16x16x32_bf16 v[64:67], a[188:191], a[128:131], v[64:67]\n" \
"s_add_u32 s8, s6, 0x1000000\n" \
"s_addc_u32 s6, s9, 0\n" \
"v_mfma_f32_16x16x32_bf16 v[64:67], a[252:255], a[192:195], v[64:67]\n" \
"ds_read_b128 a[128:131], v28 offset:0\n" \
"s_and_b32 s9, s6, 0xffff\n" \
"v_mfma_f32_16x16x32_bf16 v[80:83], a[188:191], a[132:135], v[80:83]\n" \
"ds_read_b128 a[192:195], v29 offset:0\n" \
"s_lshl_b32 s6, s5, 16\n" \
"v_mfma_f32_16x16x32_bf16 v[80:83], a[252:255], a[196:199], v[80:83]\n" \
"s_sub_i32 s10, 0x100000, s6\n" \
"s_mov_b32 s6, 0x8000\n" \
"v_mfma_f32_16x16x32_bf16 v[76:79], a[184:187], a[132:135], v[76:79]\n" \
"s_cmp_lg_u32 s5, 0\n" \
"s_cselect_b32 s17, s11, s6\n" \
"v_mfma_f32_16x16x32_bf16 v[76:79], a[248:251], a[196:199], v[76:79]\n" \
"s_lshl_b32 s6, s3, 1\n" \
"s_lshl_b32 s5, s5, 3\n" \
"v_mfma_f32_16x16x32_bf16 v[72:75], a[180:183], a[132:135], v[72:75]\n" \
"s_and_b32 s6, s6, 4\n" \
"s_or_b32 s12, s5, s6\n" \
"v_mfma_f32_16x16x32_bf16 v[72:75], a[244:247], a[196:199], v[72:75]\n" \
"s_sub_i32 s5, s13, s5\n" \
"s_or_b32 s5, s5, s6\n" \
"v_mfma_f32_16x16x32_bf16 v[68:71], a[176:179], a[132:135], v[68:71]\n" \
"s_or_b32 s6, s12, s13\n" \
"s_lshl_b32 s4, s4, 2\n" \
"v_mfma_f32_16x16x32_bf16 v[68:71], a[240:243], a[196:199], v[68:71]\n" \
"ds_read_b128 a[132:135], v28 offset:2048\n" \
"ds_read_b128 a[196:199], v29 offset:2048\n" \
"s_waitcnt lgkmcnt(15)\n" \
"v_mfma_f32_16x16x32_bf16 a[0:3], a[160:163], a[144:147], a[0:3]\n" \
"s_add_u32 m0, s30, 0x21000\n" \
"s_addk_i32 s84, 0x3000\n" \
"v_mfma_f32_16x16x32_bf16 a[0:3], a[224:227], a[208:211], a[0:3]\n" \
"buffer_load_dwordx4 v24, s[76:79], s84 offen offset:0 lds\n" \
"v_mfma_f32_16x16x32_bf16 a[4:7], a[164:167], a[144:147], a[4:7]\n" \
"ds_read_b64_tr_b16 a[176:177], v36 offset:0\n" \
"s_mulk_i32 s6, 0x92\n" \
"v_mfma_f32_16x16x32_bf16 a[4:7], a[228:231], a[208:211], a[4:7]\n" \
"ds_read_b64_tr_b16 a[178:179], v36 offset:1024\n" \
"s_add_i32 s4, s4, 18\n" \
"s_waitcnt lgkmcnt(15)\n" \
"v_mfma_f32_16x16x32_bf16 a[12:15], a[172:175], a[144:147], a[12:15]\n" \
"ds_read_b64_tr_b16 a[240:241], v36 offset:8192\n" \
"s_mulk_i32 s5, 0x92\n" \
"s_waitcnt lgkmcnt(15)\n" \
"v_mfma_f32_16x16x32_bf16 a[12:15], a[236:239], a[208:211], a[12:15]\n" \
"s_add_u32 s88, s42, 0x0\n" \
"buffer_store_dwordx4 v[196:199], v20, s[80:83], s88 offen offset:3072 sc1\n" \
"s_waitcnt lgkmcnt(11)\n" \
"v_mfma_f32_16x16x32_bf16 a[8:11], a[144:147], a[168:171], a[8:11]\n" \
"ds_read_b64_tr_b16 a[242:243], v36 offset:9216\n" \
"s_add_i32 s6, s4, s6\n" \
"s_waitcnt lgkmcnt(8)\n" \
"v_mfma_f32_16x16x32_bf16 a[8:11], a[208:211], a[232:235], a[8:11]\n" \
"ds_read_b64_tr_b16 a[180:181], v37 offset:0\n" \
"s_add_i32 s5, s5, s4\n" \
"v_mfma_f32_16x16x32_bf16 a[24:27], a[148:151], a[168:171], a[24:27]\n" \
"ds_read_b64_tr_b16 a[182:183], v37 offset:1024\n" \
"s_lshl_b32 s4, s3, 11\n" \
"v_mfma_f32_16x16x32_bf16 a[24:27], a[212:215], a[232:235], a[24:27]\n" \
"ds_read_b64_tr_b16 a[244:245], v37 offset:8192\n" \
"s_lshl_b32 s15, s13, 12\n" \
"v_mfma_f32_16x16x32_bf16 a[28:31], a[172:175], a[148:151], a[28:31]\n" \
"s_lshl_b32 s16, s19, 14\n" \
"s_mul_i32 s19, s19, 0xe000\n" \
"v_mfma_f32_16x16x32_bf16 a[28:31], a[236:239], a[212:215], a[28:31]\n" \
"s_addk_i32 s84, 0x2000\n" \
"v_mfma_f32_16x16x32_bf16 a[20:23], a[164:167], a[148:151], a[20:23]\n" \
"buffer_load_dwordx4 v25, s[76:79], s84 offen offset:1024 lds\n" \
"v_mfma_f32_16x16x32_bf16 a[20:23], a[228:231], a[212:215], a[20:23]\n" \
"ds_read_b64_tr_b16 a[246:247], v37 offset:9216\n" \
"s_mov_b32 s11, 0x20000\n" \
"v_mfma_f32_16x16x32_bf16 a[16:19], a[160:163], a[148:151], a[16:19]\n" \
"ds_read_b64_tr_b16 a[184:185], v38 offset:0\n" \
"s_or_b32 s12, s4, 0xc000\n" \
"v_mfma_f32_16x16x32_bf16 a[16:19], a[224:227], a[212:215], a[16:19]\n" \
"ds_read_b64_tr_b16 a[186:187], v38 offset:1024\n" \
"s_lshl_b32 s4, s3, 8\n" \
"v_mfma_f32_16x16x32_bf16 a[32:35], a[160:163], a[152:155], a[32:35]\n" \
"s_addk_i32 s84, 0x2000\n" \
"v_mfma_f32_16x16x32_bf16 a[32:35], a[224:227], a[216:219], a[32:35]\n" \
"buffer_load_dwordx4 v24, s[76:79], s84 offen offset:2048 lds\n" \
"v_mfma_f32_16x16x32_bf16 a[36:39], a[164:167], a[152:155], a[36:39]\n" \
"ds_read_b64_tr_b16 a[248:249], v38 offset:8192\n" \
"s_or_b32 s22, s15, s16\n" \
"v_mfma_f32_16x16x32_bf16 a[36:39], a[228:231], a[216:219], a[36:39]\n" \
"ds_read_b64_tr_b16 a[250:251], v38 offset:9216\n" \
"s_or_b32 s23, s19, s15\n" \
"v_mfma_f32_16x16x32_bf16 a[44:47], a[172:175], a[152:155], a[44:47]\n" \
"ds_read_b64_tr_b16 a[188:189], v39 offset:0\n" \
"s_lshl_b32 s60, s6, 10\n" \
"v_mfma_f32_16x16x32_bf16 a[44:47], a[236:239], a[216:219], a[44:47]\n" \
"ds_read_b64_tr_b16 a[190:191], v39 offset:1024\n" \
"s_lshl_b32 s61, s5, 10\n" \
"v_mfma_f32_16x16x32_bf16 a[40:43], a[152:155], a[168:171], a[40:43]\n" \
"s_add_u32 s89, s42, 0x2000\n" \
"buffer_store_dwordx4 v[200:203], v20, s[80:83], s89 offen offset:0 sc1\n" \
"v_mfma_f32_16x16x32_bf16 a[40:43], a[216:219], a[232:235], a[40:43]\n" \
"ds_read_b64_tr_b16 a[252:253], v39 offset:8192\n" \
"s_lshl_b32 s14, s14, 15\n" \
"v_mfma_f32_16x16x32_bf16 a[56:59], a[156:159], a[168:171], a[56:59]\n" \
"ds_read_b64_tr_b16 a[254:255], v39 offset:9216\n" \
"s_or_b32 s20, s4, 0x26c00\n" \
"v_mfma_f32_16x16x32_bf16 a[56:59], a[220:223], a[232:235], a[56:59]\n" \
"s_or_b32 s13, s22, 0x8000\n" \
"s_add_i32 s22, s22, 0x14000\n" \
"v_mfma_f32_16x16x32_bf16 a[60:63], a[172:175], a[156:159], a[60:63]\n" \
"s_bitset1_b32 s23, 16\n" \
"s_or_b32 s24, s7, 0x5be8020\n" \
"v_mfma_f32_16x16x32_bf16 a[60:63], a[236:239], a[220:223], a[60:63]\n" \
"s_addk_i32 s84, 0x2000\n" \
"v_mfma_f32_16x16x32_bf16 a[52:55], a[164:167], a[156:159], a[52:55]\n" \
".fill (L_pb + 73440 - .) / 4, 4, 0xBF800000\n" \
"L_pp18:\n" \
"s_cbranch_execz L_pret\n" \
"buffer_load_dwordx4 v25, s[76:79], s84 offen offset:3072 lds\n" \
"v_mfma_f32_16x16x32_bf16 a[52:55], a[228:231], a[220:223], a[52:55]\n" \
"s_and_b32 s57, s27, 0xffff\n" \
"s_mov_b32 s58, 0x6000000\n" \
"v_mfma_f32_16x16x32_bf16 a[48:51], a[160:163], a[156:159], a[48:51]\n" \
"s_mov_b32 s56, s26\n" \
"s_mov_b32 s59, s11\n" \
"v_mfma_f32_16x16x32_bf16 a[48:51], a[224:227], a[220:223], a[48:51]\n" \
"s_add_i32 s60, s60, 0x3000000\n" \
"s_add_i32 s61, s61, 0x411c000\n" \
"v_mfma_f32_16x16x32_bf16 a[64:67], a[164:167], a[140:143], a[64:67]\n" \
"s_sub_i32 s19, 14, s18\n" \
"s_mov_b32 s21, 0x71800000\n" \
"v_mfma_f32_16x16x32_bf16 a[64:67], a[228:231], a[204:207], a[64:67]\n" \
"s_mov_b32 s4, L_gsw-L_gtr\n" \
"v_mfma_f32_16x16x32_bf16 a[80:83], a[172:175], a[140:143], a[80:83]\n" \
"s_cmp_eq_u32 s19, 0\n" \
"s_cselect_b32 s4, L_gs7-L_gtr, s4\n" \
"v_mfma_f32_16x16x32_bf16 a[80:83], a[236:239], a[204:207], a[80:83]\n" \
"s_cmp_eq_u32 s16, 0\n" \
"s_cselect_b32 s4, 0, s4\n" \
"v_mfma_f32_16x16x32_bf16 a[68:71], a[140:143], a[168:171], a[68:71]\n" \
"s_getpc_b64 s[28:29]\n" \
"L_gp1_%=:\n" \
"s_add_u32 s28, s28, L_gtr-L_gp1_%=\n" \
"s_addc_u32 s29, s29, 0\n" \
"s_waitcnt vmcnt(11) lgkmcnt(0)\n" \
"v_mfma_f32_16x16x32_bf16 a[68:71], a[204:207], a[232:235], a[68:71]\n" \
"s_barrier\n" \
"v_mfma_f32_16x16x32_bf16 v[116:119], a[176:179], a[144:147], v[116:119]\n" \
"s_add_u32 m0, s40, 0x0\n" \
"s_add_u32 s85, s39, 0x10000\n" \
"v_mfma_f32_16x16x32_bf16 v[116:119], a[240:243], a[208:211], v[116:119]\n" \
"buffer_load_dwordx4 v20, s[80:83], s85 offen sc1 lds\n" \
"v_mfma_f32_16x16x32_bf16 v[120:123], a[180:183], a[144:147], v[120:123]\n" \
"ds_read_b64_tr_b16 a[160:161], v32 offset:32768\n" \
"v_mfma_f32_16x16x32_bf16 v[120:123], a[244:247], a[208:211], v[120:123]\n" \
"ds_read_b64_tr_b16 a[162:163], v32 offset:33280\n" \
"v_mfma_f32_16x16x32_bf16 v[124:127], a[184:187], a[144:147], v[124:127]\n" \
"s_add_u32 s28, s28, s4\n" \
"s_addc_u32 s29, s29, 0\n" \
"v_mfma_f32_16x16x32_bf16 v[124:127], a[248:251], a[208:211], v[124:127]\n" \
"v_mfma_f32_16x16x32_bf16 v[128:131], a[188:191], a[144:147], v[128:131]\n" \
"ds_read_b64_tr_b16 a[224:225], v32 offset:36864\n" \
"v_mfma_f32_16x16x32_bf16 v[128:131], a[252:255], a[208:211], v[128:131]\n" \
"ds_read_b128 a[144:147], v28 offset:40960\n" \
"v_mfma_f32_16x16x32_bf16 v[144:147], a[188:191], a[148:151], v[144:147]\n" \
"ds_read_b128 a[208:211], v29 offset:40960\n" \
"v_mfma_f32_16x16x32_bf16 v[144:147], a[252:255], a[212:215], v[144:147]\n" \
"s_add_u32 m0, s40, 0x1000\n" \
"s_add_u32 s85, s39, 0x11000\n" \
"v_mfma_f32_16x16x32_bf16 v[140:143], a[184:187], a[148:151], v[140:143]\n" \
"buffer_load_dwordx4 v20, s[80:83], s85 offen sc1 lds\n" \
"v_mfma_f32_16x16x32_bf16 v[140:143], a[248:251], a[212:215], v[140:143]\n" \
"ds_read_b64_tr_b16 a[226:227], v32 offset:37376\n" \
"v_mfma_f32_16x16x32_bf16 v[136:139], a[180:183], a[148:151], v[136:139]\n" \
"v_mfma_f32_16x16x32_bf16 v[136:139], a[244:247], a[212:215], v[136:139]\n" \
"ds_read_b64_tr_b16 a[164:165], v33 offset:32768\n" \
"v_mfma_f32_16x16x32_bf16 v[132:135], a[176:179], a[148:151], v[132:135]\n" \
"v_mfma_f32_16x16x32_bf16 v[132:135], a[240:243], a[212:215], v[132:135]\n" \
"ds_read_b128 a[148:151], v28 offset:43008\n" \
"v_mfma_f32_16x16x32_bf16 v[148:151], a[176:179], a[152:155], v[148:151]\n" \
"ds_read_b128 a[212:215], v29 offset:43008\n" \
"v_mfma_f32_16x16x32_bf16 v[148:151], a[240:243], a[216:219], v[148:151]\n" \
"s_add_u32 m0, s40, 0x3000\n" \
"s_movk_i32 s84, 0x400\n" \
"v_mfma_f32_16x16x32_bf16 v[152:155], a[180:183], a[152:155], v[152:155]\n" \
"buffer_load_dwordx4 v21, s[76:79], s84 offen offset:0 lds\n" \
"v_mfma_f32_16x16x32_bf16 v[152:155], a[244:247], a[216:219], v[152:155]\n" \
"ds_read_b64_tr_b16 a[166:167], v33 offset:33280\n" \
"v_mfma_f32_16x16x32_bf16 v[156:159], a[184:187], a[152:155], v[156:159]\n" \
"ds_read_b64_tr_b16 a[228:229], v33 offset:36864\n" \
"v_mfma_f32_16x16x32_bf16 v[156:159], a[248:251], a[216:219], v[156:159]\n" \
"s_add_u32 m0, s31, 0x0\n" \
"s_mov_b32 s84, 0x400000\n" \
"v_mfma_f32_16x16x32_bf16 v[160:163], a[188:191], a[152:155], v[160:163]\n" \
"buffer_load_dwordx4 v26, s[68:71], s84 offen offset:0 lds\n" \
"v_mfma_f32_16x16x32_bf16 v[160:163], a[252:255], a[216:219], v[160:163]\n" \
"ds_read_b128 a[152:155], v28 offset:45056\n" \
"v_mfma_f32_16x16x32_bf16 v[176:179], a[188:191], a[156:159], v[176:179]\n" \
"ds_read_b128 a[216:219], v29 offset:45056\n" \
"v_mfma_f32_16x16x32_bf16 v[176:179], a[252:255], a[220:223], v[176:179]\n" \
"ds_read_b64_tr_b16 a[230:231], v33 offset:37376\n" \
"v_mfma_f32_16x16x32_bf16 v[172:175], a[184:187], a[156:159], v[172:175]\n" \
"ds_read_b64_tr_b16 a[172:173], v35 offset:32768\n" \
"v_mfma_f32_16x16x32_bf16 v[172:175], a[248:251], a[220:223], v[172:175]\n" \
"v_mfma_f32_16x16x32_bf16 v[168:171], a[180:183], a[156:159], v[168:171]\n" \
"s_add_u32 s88, s42, 0x2000\n" \
"buffer_store_dwordx4 v[204:207], v20, s[80:83], s88 offen offset:1024 sc1\n" \
"v_mfma_f32_16x16x32_bf16 v[168:171], a[244:247], a[220:223], v[168:171]\n" \
"ds_read_b64_tr_b16 a[174:175], v35 offset:33280\n" \
"v_mfma_f32_16x16x32_bf16 v[164:167], a[176:179], a[156:159], v[164:167]\n" \
"v_mfma_f32_16x16x32_bf16 v[164:167], a[240:243], a[220:223], v[164:167]\n" \
"ds_read_b128 a[156:159], v28 offset:47104\n" \
"v_mfma_f32_16x16x32_bf16 v[84:87], a[176:179], a[136:139], v[84:87]\n" \
"ds_read_b128 a[220:223], v29 offset:47104\n" \
"v_mfma_f32_16x16x32_bf16 v[84:87], a[240:243], a[200:203], v[84:87]\n" \
"ds_read_b64_tr_b16 a[236:237], v35 offset:36864\n" \
"v_mfma_f32_16x16x32_bf16 v[88:91], a[180:183], a[136:139], v[88:91]\n" \
"v_mfma_f32_16x16x32_bf16 v[88:91], a[244:247], a[200:203], v[88:91]\n" \
"s_addk_i32 s84, 0x7c00\n" \
"s_nop 0\n" \
"v_mfma_f32_16x16x32_bf16 v[92:95], a[184:187], a[136:139], v[92:95]\n" \
"buffer_load_dwordx4 v26, s[68:71], s84 offen offset:1024 lds\n" \
"v_mfma_f32_16x16x32_bf16 v[92:95], a[248:251], a[200:203], v[92:95]\n" \
"ds_read_b64_tr_b16 a[238:239], v35 offset:37376\n" \
"v_mfma_f32_16x16x32_bf16 v[96:99], a[188:191], a[136:139], v[96:99]\n" \
"v_mfma_f32_16x16x32_bf16 v[96:99], a[252:255], a[200:203], v[96:99]\n" \
"ds_read_b128 a[136:139], v28 offset:36864\n" \
"v_mfma_f32_16x16x32_bf16 v[112:115], a[188:191], a[140:143], v[112:115]\n" \
"ds_read_b128 a[200:203], v29 offset:36864\n" \
"v_mfma_f32_16x16x32_bf16 v[112:115], a[252:255], a[204:207], v[112:115]\n" \
"ds_read_b64_tr_b16 a[168:169], v34 offset:32768\n" \
"v_mfma_f32_16x16x32_bf16 v[108:111], a[184:187], a[140:143], v[108:111]\n" \
"s_addk_i32 s84, 0x7c00\n" \
"s_nop 0\n" \
"v_mfma_f32_16x16x32_bf16 v[108:111], a[248:251], a[204:207], v[108:111]\n" \
"buffer_load_dwordx4 v27, s[68:71], s84 offen offset:2048 lds\n" \
"v_mfma_f32_16x16x32_bf16 v[104:107], a[180:183], a[140:143], v[104:107]\n" \
"ds_read_b64_tr_b16 a[170:171], v34 offset:33280\n" \
"v_mfma_f32_16x16x32_bf16 v[104:107], a[244:247], a[204:207], v[104:107]\n" \
"ds_read_b64_tr_b16 a[232:233], v34 offset:36864\n" \
"v_mfma_f32_16x16x32_bf16 v[100:103], a[176:179], a[140:143], v[100:103]\n" \
"v_mfma_f32_16x16x32_bf16 v[100:103], a[240:243], a[204:207], v[100:103]\n" \
"ds_read_b128 a[140:143], v28 offset:38912\n" \
"v_mfma_f32_16x16x32_bf16 v[52:55], a[176:179], a[128:131], v[52:55]\n" \
"ds_read_b128 a[204:207], v29 offset:38912\n" \
"v_mfma_f32_16x16x32_bf16 v[52:55], a[240:243], a[192:195], v[52:55]\n" \
"s_addk_i32 s84, 0x7c00\n" \
"s_nop 0\n" \
"v_mfma_f32_16x16x32_bf16 v[56:59], a[180:183], a[128:131], v[56:59]\n" \
"buffer_load_dwordx4 v27, s[68:71], s84 offen offset:3072 lds\n" \
"v_mfma_f32_16x16x32_bf16 v[56:59], a[244:247], a[192:195], v[56:59]\n" \
"ds_read_b64_tr_b16 a[234:235], v34 offset:37376\n" \
"v_mfma_f32_16x16x32_bf16 v[60:63], a[184:187], a[128:131], v[60:63]\n" \
"v_mfma_f32_16x16x32_bf16 v[60:63], a[248:251], a[192:195], v[60:63]\n" \
"v_mfma_f32_16x16x32_bf16 v[64:67], a[188:191], a[128:131], v[64:67]\n" \
"v_mfma_f32_16x16x32_bf16 v[64:67], a[252:255], a[192:195], v[64:67]\n" \
"ds_read_b128 a[128:131], v28 offset:32768\n" \
"v_mfma_f32_16x16x32_bf16 v[80:83], a[188:191], a[132:135], v[80:83]\n" \
"ds_read_b128 a[192:195], v29 offset:32768\n" \
"v_mfma_f32_16x16x32_bf16 v[80:83], a[252:255], a[196:199], v[80:83]\n" \
"v_mfma_f32_16x16x32_bf16 v[76:79], a[184:187], a[132:135], v[76:79]\n" \
"v_mfma_f32_16x16x32_bf16 v[76:79], a[248:251], a[196:199], v[76:79]\n" \
"v_mfma_f32_16x16x32_bf16 v[72:75], a[180:183], a[132:135], v[72:75]\n" \
"v_mfma_f32_16x16x32_bf16 v[72:75], a[244:247], a[196:199], v[72:75]\n" \
"v_mfma_f32_16x16x32_bf16 v[68:71], a[176:179], a[132:135], v[68:71]\n" \
"v_mfma_f32_16x16x32_bf16 v[68:71], a[240:243], a[196:199], v[68:71]\n" \
"ds_read_b128 a[132:135], v28 offset:34816\n" \
"ds_read_b128 a[196:199], v29 offset:34816\n" \
"s_waitcnt lgkmcnt(15)\n" \
"s_nop 0\n" \
"v_mfma_f32_16x16x32_bf16 a[0:3], a[160:163], a[144:147], a[0:3]\n" \
"s_add_u32 s89, s42, 0x2000\n" \
"buffer_store_dwordx4 v[208:211], v20, s[80:83], s89 offen offset:2048 sc1\n" \
"v_mfma_f32_16x16x32_bf16 a[0:3], a[224:227], a[208:211], a[0:3]\n" \
"ds_read_b64_tr_b16 a[176:177], v36 offset:32768\n" \
"v_mfma_f32_16x16x32_bf16 a[4:7], a[164:167], a[144:147], a[4:7]\n" \
"ds_read_b64_tr_b16 a[178:179], v36 offset:33792\n" \
"v_mfma_f32_16x16x32_bf16 a[4:7], a[228:231], a[208:211], a[4:7]\n" \
"ds_read_b64_tr_b16 a[240:241], v36 offset:40960\n" \
"v_mfma_f32_16x16x32_bf16 a[12:15], a[172:175], a[144:147], a[12:15]\n" \
"ds_read_b64_tr_b16 a[242:243], v36 offset:41984\n" \
"s_waitcnt lgkmcnt(15)\n" \
"v_mfma_f32_16x16x32_bf16 a[12:15], a[236:239], a[208:211], a[12:15]\n" \
"s_waitcnt lgkmcnt(12)\n" \
"v_mfma_f32_16x16x32_bf16 a[8:11], a[144:147], a[168:171], a[8:11]\n" \
"ds_read_b64_tr_b16 a[180:181], v37 offset:32768\n" \
"s_waitcnt lgkmcnt(9)\n" \
"s_nop 0\n" \
"v_mfma_f32_16x16x32_bf16 a[8:11], a[208:211], a[232:235], a[8:11]\n" \
"v_mfma_f32_16x16x32_bf16 a[24:27], a[148:151], a[168:171], a[24:27]\n" \
"ds_read_b64_tr_b16 a[182:183], v37 offset:33792\n" \
"v_mfma_f32_16x16x32_bf16 a[24:27], a[212:215], a[232:235], a[24:27]\n" \
"ds_read_b64_tr_b16 a[244:245], v37 offset:40960\n" \
"v_mfma_f32_16x16x32_bf16 a[28:31], a[172:175], a[148:151], a[28:31]\n" \
"v_mfma_f32_16x16x32_bf16 a[28:31], a[236:239], a[212:215], a[28:31]\n" \
"ds_read_b64_tr_b16 a[246:247], v37 offset:41984\n" \
"v_mfma_f32_16x16x32_bf16 a[20:23], a[164:167], a[148:151], a[20:23]\n" \
"v_mfma_f32_16x16x32_bf16 a[20:23], a[228:231], a[212:215], a[20:23]\n" \
"ds_read_b64_tr_b16 a[184:185], v38 offset:32768\n" \
"v_mfma_f32_16x16x32_bf16 a[16:19], a[160:163], a[148:151], a[16:19]\n" \
"ds_read_b64_tr_b16 a[186:187], v38 offset:33792\n" \
"v_mfma_f32_16x16x32_bf16 a[16:19], a[224:227], a[212:215], a[16:19]\n" \
"v_mfma_f32_16x16x32_bf16 a[32:35], a[160:163], a[152:155], a[32:35]\n" \
"s_add_u32 s88, s42, 0x2000\n" \
"buffer_store_dwordx4 v[212:215], v20, s[80:83], s88 offen offset:3072 sc1\n" \
"v_mfma_f32_16x16x32_bf16 a[32:35], a[224:227], a[216:219], a[32:35]\n" \
"ds_read_b64_tr_b16 a[248:249], v38 offset:40960\n" \
"v_mfma_f32_16x16x32_bf16 a[36:39], a[164:167], a[152:155], a[36:39]\n" \
"v_lshlrev_b32_e32 v45, 3, v20\n" \
"v_add_u32_e32 v45, s30, v45\n" \
"v_mfma_f32_16x16x32_bf16 a[36:39], a[228:231], a[216:219], a[36:39]\n" \
"s_getpc_b64 s[86:87]\n" \
"L_ct1_%=:\n" \
"s_add_u32 s86, s86, L_ap0-L_ct1_%=\n" \
"s_addc_u32 s87, s87, 0\n" \
"s_add_u32 m0, s40, 0x2000\n" \
"v_mfma_f32_16x16x32_bf16 a[44:47], a[172:175], a[152:155], a[44:47]\n" \
"global_load_lds_dword v45, s[86:87]\n" \
"v_mfma_f32_16x16x32_bf16 a[44:47], a[236:239], a[216:219], a[44:47]\n" \
"ds_read_b64_tr_b16 a[250:251], v38 offset:41984\n" \
"v_mfma_f32_16x16x32_bf16 a[40:43], a[152:155], a[168:171], a[40:43]\n" \
"ds_read_b64_tr_b16 a[188:189], v39 offset:32768\n" \
"v_mfma_f32_16x16x32_bf16 a[40:43], a[216:219], a[232:235], a[40:43]\n" \
"ds_read_b64_tr_b16 a[190:191], v39 offset:33792\n" \
"v_mfma_f32_16x16x32_bf16 a[56:59], a[156:159], a[168:171], a[56:59]\n" \
"ds_read_b64_tr_b16 a[252:253], v39 offset:40960\n" \
"v_mfma_f32_16x16x32_bf16 a[56:59], a[220:223], a[232:235], a[56:59]\n" \
"ds_read_b64_tr_b16 a[254:255], v39 offset:41984\n" \
"v_mfma_f32_16x16x32_bf16 a[60:63], a[172:175], a[156:159], a[60:63]\n" \
"v_mfma_f32_16x16x32_bf16 a[60:63], a[236:239], a[220:223], a[60:63]\n" \
"v_mfma_f32_16x16x32_bf16 a[52:55], a[164:167], a[156:159], a[52:55]\n" \
"v_mfma_f32_16x16x32_bf16 a[52:55], a[228:231], a[220:223], a[52:55]\n" \
"v_mfma_f32_16x16x32_bf16 a[48:51], a[160:163], a[156:159], a[48:51]\n" \
"v_mfma_f32_16x16x32_bf16 a[48:51], a[224:227], a[220:223], a[48:51]\n" \
"v_mfma_f32_16x16x32_bf16 a[64:67], a[164:167], a[140:143], a[64:67]\n" \
"v_mfma_f32_16x16x32_bf16 a[64:67], a[228:231], a[204:207], a[64:67]\n" \
"v_mfma_f32_16x16x32_bf16 a[80:83], a[172:175], a[140:143], a[80:83]\n" \
"v_mfma_f32_16x16x32_bf16 a[80:83], a[236:239], a[204:207], a[80:83]\n" \
"v_mfma_f32_16x16x32_bf16 a[68:71], a[140:143], a[168:171], a[68:71]\n" \
"s_waitcnt vmcnt(3) lgkmcnt(0)\n" \
"v_mfma_f32_16x16x32_bf16 a[68:71], a[204:207], a[232:235], a[68:71]\n" \
"s_barrier\n" \
"v_xor_b32_e32 v28, 0x20000, v28\n" \
"v_xor_b32_e32 v29, 0x20000, v29\n" \
"v_mfma_f32_16x16x32_bf16 v[116:119], a[176:179], a[144:147], v[116:119]\n" \
"ds_read_b64_tr_b16 a[160:161], v32 offset:0\n" \
"v_accvgpr_read_b32 v184, a0\n" \
"v_mfma_f32_16x16x32_bf16 v[116:119], a[240:243], a[208:211], v[116:119]\n" \
"v_accvgpr_read_b32 v185, a1\n" \
"v_accvgpr_read_b32 v186, a2\n" \
"v_mfma_f32_16x16x32_bf16 v[120:123], a[180:183], a[144:147], v[120:123]\n" \
"v_accvgpr_read_b32 v187, a3\n" \
"v_accvgpr_read_b32 v188, a4\n" \
"v_mfma_f32_16x16x32_bf16 v[120:123], a[244:247], a[208:211], v[120:123]\n" \
"ds_read_b64_tr_b16 a[162:163], v32 offset:512\n" \
"v_accvgpr_read_b32 v189, a5\n" \
"v_mfma_f32_16x16x32_bf16 v[124:127], a[184:187], a[144:147], v[124:127]\n" \
"v_accvgpr_read_b32 v190, a6\n" \
"v_accvgpr_read_b32 v191, a7\n" \
"v_mfma_f32_16x16x32_bf16 v[124:127], a[248:251], a[208:211], v[124:127]\n" \
"v_accvgpr_read_b32 v196, a12\n" \
"v_accvgpr_read_b32 v197, a13\n" \
"v_mfma_f32_16x16x32_bf16 v[128:131], a[188:191], a[144:147], v[128:131]\n" \
"ds_read_b64_tr_b16 a[224:225], v32 offset:4096\n" \
"v_accvgpr_read_b32 v198, a14\n" \
"v_mfma_f32_16x16x32_bf16 v[128:131], a[252:255], a[208:211], v[128:131]\n" \
"ds_read_b128 a[144:147], v28 offset:8192\n" \
"v_accvgpr_read_b32 v199, a15\n" \
"v_mfma_f32_16x16x32_bf16 v[144:147], a[188:191], a[148:151], v[144:147]\n" \
"ds_read_b128 a[208:211], v29 offset:8192\n" \
"v_accvgpr_read_b32 v192, a8\n" \
"v_mfma_f32_16x16x32_bf16 v[144:147], a[252:255], a[212:215], v[144:147]\n" \
"ds_read_b64_tr_b16 a[226:227], v32 offset:4608\n" \
"v_accvgpr_read_b32 v193, a9\n" \
"v_mfma_f32_16x16x32_bf16 v[140:143], a[184:187], a[148:151], v[140:143]\n" \
"v_accvgpr_read_b32 v194, a10\n" \
"v_accvgpr_read_b32 v195, a11\n" \
"v_mfma_f32_16x16x32_bf16 v[140:143], a[248:251], a[212:215], v[140:143]\n" \
"v_accvgpr_read_b32 v208, a24\n" \
"v_accvgpr_read_b32 v209, a25\n" \
"v_mfma_f32_16x16x32_bf16 v[136:139], a[180:183], a[148:151], v[136:139]\n" \
"v_accvgpr_read_b32 v210, a26\n" \
"v_accvgpr_read_b32 v211, a27\n" \
"v_mfma_f32_16x16x32_bf16 v[136:139], a[244:247], a[212:215], v[136:139]\n" \
"ds_read_b64_tr_b16 a[164:165], v33 offset:0\n" \
"v_accvgpr_read_b32 v212, a28\n" \
"v_mfma_f32_16x16x32_bf16 v[132:135], a[176:179], a[148:151], v[132:135]\n" \
"v_accvgpr_read_b32 v213, a29\n" \
"v_accvgpr_read_b32 v214, a30\n" \
"v_mfma_f32_16x16x32_bf16 v[132:135], a[240:243], a[212:215], v[132:135]\n" \
"ds_read_b128 a[148:151], v28 offset:10240\n" \
"v_accvgpr_read_b32 v215, a31\n" \
"v_mfma_f32_16x16x32_bf16 v[148:151], a[176:179], a[152:155], v[148:151]\n" \
"ds_read_b128 a[212:215], v29 offset:10240\n" \
"v_accvgpr_read_b32 v204, a20\n" \
"v_mfma_f32_16x16x32_bf16 v[148:151], a[240:243], a[216:219], v[148:151]\n" \
"ds_read_b64_tr_b16 a[166:167], v33 offset:512\n" \
"v_accvgpr_read_b32 v205, a21\n" \
"v_mfma_f32_16x16x32_bf16 v[152:155], a[180:183], a[152:155], v[152:155]\n" \
"v_accvgpr_read_b32 v206, a22\n" \
"v_accvgpr_read_b32 v207, a23\n" \
"v_mfma_f32_16x16x32_bf16 v[152:155], a[244:247], a[216:219], v[152:155]\n" \
"ds_read_b64_tr_b16 a[228:229], v33 offset:4096\n" \
"v_accvgpr_read_b32 v200, a16\n" \
"v_mfma_f32_16x16x32_bf16 v[156:159], a[184:187], a[152:155], v[156:159]\n" \
"v_accvgpr_read_b32 v201, a17\n" \
"v_accvgpr_read_b32 v202, a18\n" \
"v_mfma_f32_16x16x32_bf16 v[156:159], a[248:251], a[216:219], v[156:159]\n" \
"v_accvgpr_read_b32 v203, a19\n" \
"v_accvgpr_read_b32 v216, a32\n" \
"v_mfma_f32_16x16x32_bf16 v[160:163], a[188:191], a[152:155], v[160:163]\n" \
"ds_read_b64_tr_b16 a[230:231], v33 offset:4608\n" \
"v_accvgpr_read_b32 v217, a33\n" \
"v_mfma_f32_16x16x32_bf16 v[160:163], a[252:255], a[216:219], v[160:163]\n" \
"ds_read_b128 a[152:155], v28 offset:12288\n" \
"v_accvgpr_read_b32 v218, a34\n" \
"v_mfma_f32_16x16x32_bf16 v[176:179], a[188:191], a[156:159], v[176:179]\n" \
"ds_read_b128 a[216:219], v29 offset:12288\n" \
"v_accvgpr_read_b32 v219, a35\n" \
"v_mfma_f32_16x16x32_bf16 v[176:179], a[252:255], a[220:223], v[176:179]\n" \
"v_accvgpr_read_b32 v220, a36\n" \
"v_accvgpr_read_b32 v221, a37\n" \
"v_mfma_f32_16x16x32_bf16 v[172:175], a[184:187], a[156:159], v[172:175]\n" \
"ds_read_b64_tr_b16 a[172:173], v35 offset:0\n" \
"v_accvgpr_read_b32 v222, a38\n" \
"v_mfma_f32_16x16x32_bf16 v[172:175], a[248:251], a[220:223], v[172:175]\n" \
"v_accvgpr_read_b32 v223, a39\n" \
"v_accvgpr_read_b32 v228, a44\n" \
"v_mfma_f32_16x16x32_bf16 v[168:171], a[180:183], a[156:159], v[168:171]\n" \
"v_accvgpr_read_b32 v229, a45\n" \
"v_accvgpr_read_b32 v230, a46\n" \
"v_mfma_f32_16x16x32_bf16 v[168:171], a[244:247], a[220:223], v[168:171]\n" \
"ds_read_b64_tr_b16 a[174:175], v35 offset:512\n" \
"v_accvgpr_read_b32 v231, a47\n" \
"v_mfma_f32_16x16x32_bf16 v[164:167], a[176:179], a[156:159], v[164:167]\n" \
"v_accvgpr_read_b32 v224, a40\n" \
"v_accvgpr_read_b32 v225, a41\n" \
"v_mfma_f32_16x16x32_bf16 v[164:167], a[240:243], a[220:223], v[164:167]\n" \
"ds_read_b128 a[156:159], v28 offset:14336\n" \
"v_accvgpr_read_b32 v226, a42\n" \
"v_mfma_f32_16x16x32_bf16 v[84:87], a[176:179], a[136:139], v[84:87]\n" \
"ds_read_b128 a[220:223], v29 offset:14336\n" \
"v_accvgpr_read_b32 v227, a43\n" \
"v_mfma_f32_16x16x32_bf16 v[84:87], a[240:243], a[200:203], v[84:87]\n" \
"ds_read_b64_tr_b16 a[236:237], v35 offset:4096\n" \
"v_accvgpr_read_b32 v240, a56\n" \
"v_mfma_f32_16x16x32_bf16 v[88:91], a[180:183], a[136:139], v[88:91]\n" \
"v_accvgpr_read_b32 v241, a57\n" \
"v_accvgpr_read_b32 v242, a58\n" \
"v_mfma_f32_16x16x32_bf16 v[88:91], a[244:247], a[200:203], v[88:91]\n" \
"ds_read_b64_tr_b16 a[238:239], v35 offset:4608\n" \
"v_accvgpr_read_b32 v243, a59\n" \
"v_mfma_f32_16x16x32_bf16 v[92:95], a[184:187], a[136:139], v[92:95]\n" \
"v_accvgpr_read_b32 v244, a60\n" \
"v_accvgpr_read_b32 v245, a61\n" \
"v_mfma_f32_16x16x32_bf16 v[92:95], a[248:251], a[200:203], v[92:95]\n" \
"v_accvgpr_read_b32 v246, a62\n" \
"v_accvgpr_read_b32 v247, a63\n" \
"v_mfma_f32_16x16x32_bf16 v[96:99], a[188:191], a[136:139], v[96:99]\n" \
"v_accvgpr_read_b32 v236, a52\n" \
"v_accvgpr_read_b32 v237, a53\n" \
"v_mfma_f32_16x16x32_bf16 v[96:99], a[252:255], a[200:203], v[96:99]\n" \
"ds_read_b128 a[136:139], v28 offset:4096\n" \
"v_accvgpr_read_b32 v238, a54\n" \
"v_mfma_f32_16x16x32_bf16 v[112:115], a[188:191], a[140:143], v[112:115]\n" \
"ds_read_b128 a[200:203], v29 offset:4096\n" \
"v_accvgpr_read_b32 v239, a55\n" \
"v_mfma_f32_16x16x32_bf16 v[112:115], a[252:255], a[204:207], v[112:115]\n" \
"ds_read_b64_tr_b16 a[168:169], v34 offset:0\n" \
"v_accvgpr_read_b32 v232, a48\n" \
"v_mfma_f32_16x16x32_bf16 v[108:111], a[184:187], a[140:143], v[108:111]\n" \
"ds_read_b64_tr_b16 a[170:171], v34 offset:512\n" \
"v_accvgpr_read_b32 v233, a49\n" \
"v_mfma_f32_16x16x32_bf16 v[108:111], a[248:251], a[204:207], v[108:111]\n" \
"v_accvgpr_read_b32 v234, a50\n" \
"v_accvgpr_read_b32 v235, a51\n" \
"v_mfma_f32_16x16x32_bf16 v[104:107], a[180:183], a[140:143], v[104:107]\n" \
"v_accvgpr_read_b32 v180, a64\n" \
"v_accvgpr_read_b32 v181, a65\n" \
"v_mfma_f32_16x16x32_bf16 v[104:107], a[244:247], a[204:207], v[104:107]\n" \
"ds_read_b64_tr_b16 a[232:233], v34 offset:4096\n" \
"v_accvgpr_read_b32 v182, a66\n" \
"v_mfma_f32_16x16x32_bf16 v[100:103], a[176:179], a[140:143], v[100:103]\n" \
"v_accvgpr_read_b32 v183, a67\n" \
"v_accvgpr_read_b32 v48, a80\n" \
"v_mfma_f32_16x16x32_bf16 v[100:103], a[240:243], a[204:207], v[100:103]\n" \
"ds_read_b128 a[140:143], v28 offset:6144\n" \
"v_accvgpr_read_b32 v49, a81\n" \
"v_mfma_f32_16x16x32_bf16 v[52:55], a[176:179], a[128:131], v[52:55]\n" \
"ds_read_b128 a[204:207], v29 offset:6144\n" \
"v_accvgpr_read_b32 v50, a82\n" \
"v_mfma_f32_16x16x32_bf16 v[52:55], a[240:243], a[192:195], v[52:55]\n" \
"ds_read_b64_tr_b16 a[234:235], v34 offset:4608\n" \
"v_accvgpr_read_b32 v51, a83\n" \
"v_mfma_f32_16x16x32_bf16 v[56:59], a[180:183], a[128:131], v[56:59]\n" \
"v_accvgpr_read_b32 v252, a68\n" \
"v_accvgpr_read_b32 v253, a69\n" \
"v_mfma_f32_16x16x32_bf16 v[56:59], a[244:247], a[192:195], v[56:59]\n" \
"v_accvgpr_read_b32 v254, a70\n" \
"v_accvgpr_read_b32 v255, a71\n" \
"v_mfma_f32_16x16x32_bf16 v[60:63], a[184:187], a[128:131], v[60:63]\n" \
"v_lshrrev_b32_e64 v24, 9, v20\n" \
"v_and_b32_e32 v26, 0x1ff, v20\n" \
"v_mfma_f32_16x16x32_bf16 v[60:63], a[248:251], a[192:195], v[60:63]\n" \
"v_lshl_add_u32 v24, v24, 3, v26\n" \
"v_lshrrev_b32_e64 v25, 8, v20\n" \
"v_mfma_f32_16x16x32_bf16 v[64:67], a[188:191], a[128:131], v[64:67]\n" \
"v_mul_u32_u24_e32 v25, 0x24700, v25\n" \
"v_add_u32_e64 v25, v25, v20\n" \
"v_mfma_f32_16x16x32_bf16 v[64:67], a[252:255], a[192:195], v[64:67]\n" \
"ds_read_b128 a[128:131], v28 offset:0\n" \
"v_lshrrev_b32_e64 v26, 2, v20\n" \
"v_mfma_f32_16x16x32_bf16 v[80:83], a[188:191], a[132:135], v[80:83]\n" \
"ds_read_b128 a[192:195], v29 offset:0\n" \
"v_add_u32_e64 v26, s45, v26\n" \
"v_mfma_f32_16x16x32_bf16 v[80:83], a[252:255], a[196:199], v[80:83]\n" \
"v_add_u32_e32 v27, s46, v20\n" \
"v_add_u32_e32 v21, s43, v27\n" \
"v_mfma_f32_16x16x32_bf16 v[76:79], a[184:187], a[132:135], v[76:79]\n" \
"v_mfma_f32_16x16x32_bf16 v[76:79], a[248:251], a[196:199], v[76:79]\n" \
"v_mfma_f32_16x16x32_bf16 v[72:75], a[180:183], a[132:135], v[72:75]\n" \
"v_mfma_f32_16x16x32_bf16 v[72:75], a[244:247], a[196:199], v[72:75]\n" \
"v_mfma_f32_16x16x32_bf16 v[68:71], a[176:179], a[132:135], v[68:71]\n" \
"v_mfma_f32_16x16x32_bf16 v[68:71], a[240:243], a[196:199], v[68:71]\n" \
"ds_read_b128 a[132:135], v28 offset:2048\n" \
"ds_read_b128 a[196:199], v29 offset:2048\n" \
"s_waitcnt lgkmcnt(15)\n" \
"s_nop 0\n" \
"v_mfma_f32_16x16x32_bf16 v[184:187], a[160:163], a[144:147], v[184:187]\n" \
"v_bfe_u32 v44, v20, 2, 5\n" \
"ds_read_b64_tr_b16 a[176:177], v36 offset:0\n" \
"v_mfma_f32_16x16x32_bf16 v[184:187], a[224:227], a[208:211], v[184:187]\n" \
"buffer_load_dword v44, v44, s[80:83], s38 offen offset:0 sc1\n" \
"v_mfma_f32_16x16x32_bf16 v[188:191], a[164:167], a[144:147], v[188:191]\n" \
"ds_read_b64_tr_b16 a[178:179], v36 offset:1024\n" \
"v_mfma_f32_16x16x32_bf16 v[188:191], a[228:231], a[208:211], v[188:191]\n" \
"ds_read_b64_tr_b16 a[240:241], v36 offset:8192\n" \
"v_mfma_f32_16x16x32_bf16 v[196:199], a[172:175], a[144:147], v[196:199]\n" \
"ds_read_b64_tr_b16 a[242:243], v36 offset:9216\n" \
"s_waitcnt lgkmcnt(15)\n" \
"v_mfma_f32_16x16x32_bf16 v[196:199], a[236:239], a[208:211], v[196:199]\n" \
"s_waitcnt lgkmcnt(12)\n" \
"v_mfma_f32_16x16x32_bf16 v[192:195], a[144:147], a[168:171], v[192:195]\n" \
"ds_read_b64_tr_b16 a[180:181], v37 offset:0\n" \
"s_waitcnt lgkmcnt(9)\n" \
"s_nop 0\n" \
"v_mfma_f32_16x16x32_bf16 v[192:195], a[208:211], a[232:235], v[192:195]\n" \
"v_mfma_f32_16x16x32_bf16 v[208:211], a[148:151], a[168:171], v[208:211]\n" \
"ds_read_b64_tr_b16 a[182:183], v37 offset:1024\n" \
"v_mfma_f32_16x16x32_bf16 v[208:211], a[212:215], a[232:235], v[208:211]\n" \
"ds_read_b64_tr_b16 a[244:245], v37 offset:8192\n" \
"v_mfma_f32_16x16x32_bf16 v[212:215], a[172:175], a[148:151], v[212:215]\n" \
"v_mfma_f32_16x16x32_bf16 v[212:215], a[236:239], a[212:215], v[212:215]\n" \
"ds_read_b64_tr_b16 a[246:247], v37 offset:9216\n" \
"v_cvt_pk_bf16_f32 v188, v188, v189\n" \
"v_mfma_f32_16x16x32_bf16 v[204:207], a[164:167], a[148:151], v[204:207]\n" \
"v_cvt_pk_bf16_f32 v189, v190, v191\n" \
"v_cvt_pk_bf16_f32 v190, v198, v199\n" \
"v_mfma_f32_16x16x32_bf16 v[204:207], a[228:231], a[212:215], v[204:207]\n" \
"ds_read_b64_tr_b16 a[184:185], v38 offset:0\n" \
"v_mov_b32_e32 v191, 0x3f80\n" \
"v_mfma_f32_16x16x32_bf16 v[200:203], a[160:163], a[148:151], v[200:203]\n" \
"ds_read_b64_tr_b16 a[186:187], v38 offset:1024\n" \
"v_cvt_pk_bf16_f32 v184, v184, v185\n" \
"v_mfma_f32_16x16x32_bf16 v[200:203], a[224:227], a[212:215], v[200:203]\n" \
"v_cvt_pk_bf16_f32 v185, v186, v187\n" \
"v_cvt_pk_bf16_f32 v186, v196, v197\n" \
"v_mfma_f32_16x16x32_bf16 v[216:219], a[160:163], a[152:155], v[216:219]\n" \
"ds_read_b64_tr_b16 a[248:249], v38 offset:8192\n" \
"v_mov_b32_e64 v187, 0\n" \
"v_mfma_f32_16x16x32_bf16 v[216:219], a[224:227], a[216:219], v[216:219]\n" \
"ds_read_b64_tr_b16 a[250:251], v38 offset:9216\n" \
"v_mfma_f32_16x16x32_bf16 v[220:223], a[164:167], a[152:155], v[220:223]\n" \
"v_mfma_f32_16x16x32_bf16 v[220:223], a[228:231], a[216:219], v[220:223]\n" \
"ds_read_b64_tr_b16 a[188:189], v39 offset:0\n" \
"v_cvt_pk_bf16_f32 v204, v204, v205\n" \
"v_mfma_f32_16x16x32_bf16 v[228:231], a[172:175], a[152:155], v[228:231]\n" \
"v_cvt_pk_bf16_f32 v205, v206, v207\n" \
"v_cvt_pk_bf16_f32 v206, v214, v215\n" \
"v_mfma_f32_16x16x32_bf16 v[228:231], a[236:239], a[216:219], v[228:231]\n" \
"ds_read_b64_tr_b16 a[190:191], v39 offset:1024\n" \
"v_mov_b32_e32 v207, 0x3f80\n" \
"v_mfma_f32_16x16x32_bf16 v[224:227], a[152:155], a[168:171], v[224:227]\n" \
"ds_read_b64_tr_b16 a[252:253], v39 offset:8192\n" \
"v_cvt_pk_bf16_f32 v200, v200, v201\n" \
"v_mfma_f32_16x16x32_bf16 v[224:227], a[216:219], a[232:235], v[224:227]\n" \
"v_cvt_pk_bf16_f32 v201, v202, v203\n" \
"v_cvt_pk_bf16_f32 v202, v212, v213\n" \
"v_mfma_f32_16x16x32_bf16 v[240:243], a[156:159], a[168:171], v[240:243]\n" \
"ds_read_b64_tr_b16 a[254:255], v39 offset:9216\n" \
"v_mov_b32_e64 v203, 0\n" \
"v_mfma_f32_16x16x32_bf16 v[240:243], a[220:223], a[232:235], v[240:243]\n" \
"v_mfma_f32_16x16x32_bf16 v[244:247], a[172:175], a[156:159], v[244:247]\n" \
"v_mfma_f32_16x16x32_bf16 v[244:247], a[236:239], a[220:223], v[244:247]\n" \
"v_cvt_pk_bf16_f32 v220, v220, v221\n" \
"v_cvt_pk_bf16_f32 v221, v222, v223\n" \
"v_mfma_f32_16x16x32_bf16 v[236:239], a[164:167], a[156:159], v[236:239]\n" \
"v_cvt_pk_bf16_f32 v222, v230, v231\n" \
"v_mov_b32_e32 v223, 0x3f80\n" \
"v_mfma_f32_16x16x32_bf16 v[236:239], a[228:231], a[220:223], v[236:239]\n" \
"v_cvt_pk_bf16_f32 v216, v216, v217\n" \
"v_cvt_pk_bf16_f32 v217, v218, v219\n" \
"v_mfma_f32_16x16x32_bf16 v[232:235], a[160:163], a[156:159], v[232:235]\n" \
"v_cvt_pk_bf16_f32 v218, v228, v229\n" \
"v_mov_b32_e64 v219, 0\n" \
"v_mfma_f32_16x16x32_bf16 v[232:235], a[224:227], a[220:223], v[232:235]\n" \
"v_mfma_f32_16x16x32_bf16 v[180:183], a[164:167], a[140:143], v[180:183]\n" \
"v_mfma_f32_16x16x32_bf16 v[180:183], a[228:231], a[204:207], v[180:183]\n" \
"v_mfma_f32_16x16x32_bf16 v[48:51], a[172:175], a[140:143], v[48:51]\n" \
"v_mfma_f32_16x16x32_bf16 v[48:51], a[236:239], a[204:207], v[48:51]\n" \
"v_cvt_pk_bf16_f32 v236, v236, v237\n" \
"v_cvt_pk_bf16_f32 v237, v238, v239\n" \
"v_mfma_f32_16x16x32_bf16 v[252:255], a[140:143], a[168:171], v[252:255]\n" \
"v_cvt_pk_bf16_f32 v238, v246, v247\n" \
"v_mov_b32_e32 v239, 0x3f80\n" \
"v_mfma_f32_16x16x32_bf16 v[252:255], a[204:207], a[232:235], v[252:255]\n" \
"v_cvt_pk_bf16_f32 v232, v232, v233\n" \
"v_cvt_pk_bf16_f32 v233, v234, v235\n" \
"s_waitcnt vmcnt(0) lgkmcnt(0)\n" \
"s_barrier\n" \
"v_mov_b32_e32 v45, s62\n" \
"s_mov_b64 exec, 1\n" \
"buffer_store_dword v45, off, s[80:83], s37 offset:32 sc1\n" \
"s_mov_b64 exec, -1\n" \
"v_cmp_ne_u32_e32 vcc, s62, v44\n" \
"s_nop 4\n" \
"s_cbranch_vccnz L_poll161_%=\n" \
"L_pollr161_%=:\n" \
"v_mfma_f32_16x16x32_bf16 v[116:119], a[176:179], a[144:147], v[116:119]\n" \
"s_mov_b32 s73, s50\n" \
"s_sub_u32 m0, s31, 0x10000\n" \
"v_mfma_f32_16x16x32_bf16 v[116:119], a[240:243], a[208:211], v[116:119]\n" \
"buffer_load_dwordx4 v20, s[80:83], s73 offen offset:0 sc1 lds\n" \
"v_mfma_f32_16x16x32_bf16 v[120:123], a[180:183], a[144:147], v[120:123]\n" \
"s_add_u32 s74, s50, 0x4000\n" \
"s_add_u32 s75, s50, 0x8000\n" \
"v_mfma_f32_16x16x32_bf16 v[120:123], a[244:247], a[208:211], v[120:123]\n" \
"s_mov_b32 s70, 0x3e0293ee\n" \
"s_mov_b32 s76, s52\n" \
"v_mfma_f32_16x16x32_bf16 v[124:127], a[184:187], a[144:147], v[124:127]\n" \
"v_mul_f32_e32 v116, s70, v116\n" \
"v_mul_f32_e32 v117, s70, v117\n" \
"v_mfma_f32_16x16x32_bf16 v[124:127], a[248:251], a[208:211], v[124:127]\n" \
"buffer_load_dwordx4 v20, s[80:83], s73 offen offset:1024 sc1 lds\n" \
"v_mfma_f32_16x16x32_bf16 v[128:131], a[188:191], a[144:147], v[128:131]\n" \
"v_mul_f32_e32 v118, s70, v118\n" \
"v_mul_f32_e32 v119, s70, v119\n" \
"v_mfma_f32_16x16x32_bf16 v[128:131], a[252:255], a[208:211], v[128:131]\n" \
"v_cvt_pk_bf16_f32 v116, v116, v117\n" \
"v_mul_f32_e64 v120, s70, v120\n" \
"v_mfma_f32_16x16x32_bf16 v[144:147], a[188:191], a[148:151], v[144:147]\n" \
"v_mul_f32_e32 v124, s70, v124\n" \
"v_mul_f32_e32 v125, s70, v125\n" \
"v_mfma_f32_16x16x32_bf16 v[144:147], a[252:255], a[212:215], v[144:147]\n" \
"buffer_load_dwordx4 v20, s[80:83], s73 offen offset:2048 sc1 lds\n" \
"v_mfma_f32_16x16x32_bf16 v[140:143], a[184:187], a[148:151], v[140:143]\n" \
"v_mul_f32_e32 v126, s70, v126\n" \
"v_mul_f32_e32 v127, s70, v127\n" \
"v_mfma_f32_16x16x32_bf16 v[140:143], a[248:251], a[212:215], v[140:143]\n" \
"v_cvt_pk_bf16_f32 v124, v124, v125\n" \
"v_mul_f32_e64 v121, s70, v121\n" \
"v_mfma_f32_16x16x32_bf16 v[136:139], a[180:183], a[148:151], v[136:139]\n" \
".fill (L_pb + 77536 - .) / 4, 4, 0xBF800000\n" \
"L_pp19:\n" \
"s_cbranch_execz L_pret\n" \
"v_mul_f32_e32 v122, s70, v122\n" \
"v_mul_f32_e64 v123, s70, v123\n" \
"v_mfma_f32_16x16x32_bf16 v[136:139], a[244:247], a[212:215], v[136:139]\n" \
"buffer_load_dwordx4 v20, s[80:83], s73 offen offset:3072 sc1 lds\n" \
"v_mfma_f32_16x16x32_bf16 v[132:135], a[176:179], a[148:151], v[132:135]\n" \
"s_sub_u32 m0, s31, s51\n" \
"v_cvt_pk_bf16_f32 v117, v118, v119\n" \
"v_mfma_f32_16x16x32_bf16 v[132:135], a[240:243], a[212:215], v[132:135]\n" \
"v_mul_f32_e32 v128, s70, v128\n" \
"v_mul_f32_e64 v129, s70, v129\n" \
"v_mfma_f32_16x16x32_bf16 v[148:151], a[176:179], a[152:155], v[148:151]\n" \
"v_mul_f32_e32 v130, s70, v130\n" \
"v_mul_f32_e32 v131, s70, v131\n" \
"v_mfma_f32_16x16x32_bf16 v[148:151], a[240:243], a[216:219], v[148:151]\n" \
"buffer_load_dwordx4 v20, s[80:83], s74 offen offset:0 sc1 lds\n" \
"v_mfma_f32_16x16x32_bf16 v[152:155], a[180:183], a[152:155], v[152:155]\n" \
"v_cvt_pk_bf16_f32 v125, v126, v127\n" \
"v_cvt_pk_bf16_f32 v118, v120, v121\n" \
"v_mfma_f32_16x16x32_bf16 v[152:155], a[244:247], a[216:219], v[152:155]\n" \
"v_cvt_pk_bf16_f32 v119, v122, v123\n" \
"v_cvt_pk_bf16_f32 v126, v128, v129\n" \
"v_mfma_f32_16x16x32_bf16 v[156:159], a[184:187], a[152:155], v[156:159]\n" \
"v_cvt_pk_bf16_f32 v127, v130, v131\n" \
"v_mul_f32_e64 v132, s70, v132\n" \
"v_mfma_f32_16x16x32_bf16 v[156:159], a[248:251], a[216:219], v[156:159]\n" \
"buffer_load_dwordx4 v20, s[80:83], s74 offen offset:1024 sc1 lds\n" \
"v_mfma_f32_16x16x32_bf16 v[160:163], a[188:191], a[152:155], v[160:163]\n" \
"ds_write_b128 v21, v[116:119] offset:16384\n" \
"v_mfma_f32_16x16x32_bf16 v[160:163], a[252:255], a[216:219], v[160:163]\n" \
"v_mul_f32_e32 v133, s70, v133\n" \
"v_mul_f32_e32 v140, s70, v140\n" \
"v_mfma_f32_16x16x32_bf16 v[176:179], a[188:191], a[156:159], v[176:179]\n" \
"v_mul_f32_e32 v141, s70, v141\n" \
"v_mul_f32_e32 v134, s70, v134\n" \
"v_mfma_f32_16x16x32_bf16 v[176:179], a[252:255], a[220:223], v[176:179]\n" \
"buffer_load_dwordx4 v20, s[80:83], s74 offen offset:2048 sc1 lds\n" \
"v_mfma_f32_16x16x32_bf16 v[172:175], a[184:187], a[156:159], v[172:175]\n" \
"ds_write_b128 v21, v[124:127] offset:17408\n" \
"v_mfma_f32_16x16x32_bf16 v[172:175], a[248:251], a[220:223], v[172:175]\n" \
"v_mul_f32_e64 v135, s70, v135\n" \
"v_cvt_pk_bf16_f32 v132, v132, v133\n" \
"v_mfma_f32_16x16x32_bf16 v[168:171], a[180:183], a[156:159], v[168:171]\n" \
"v_mul_f32_e32 v142, s70, v142\n" \
"v_mul_f32_e32 v143, s70, v143\n" \
"v_mfma_f32_16x16x32_bf16 v[168:171], a[244:247], a[220:223], v[168:171]\n" \
"buffer_load_dwordx4 v20, s[80:83], s74 offen offset:3072 sc1 lds\n" \
"v_mfma_f32_16x16x32_bf16 v[164:167], a[176:179], a[156:159], v[164:167]\n" \
"s_sub_u32 m0, s31, 0x4000\n" \
"v_cvt_pk_bf16_f32 v140, v140, v141\n" \
"v_mfma_f32_16x16x32_bf16 v[164:167], a[240:243], a[220:223], v[164:167]\n" \
"v_mul_f32_e32 v136, s70, v136\n" \
"v_mul_f32_e32 v137, s70, v137\n" \
"v_mfma_f32_16x16x32_bf16 v[84:87], a[176:179], a[136:139], v[84:87]\n" \
"v_mul_f32_e32 v138, s70, v138\n" \
"v_mul_f32_e32 v139, s70, v139\n" \
"v_mfma_f32_16x16x32_bf16 v[84:87], a[240:243], a[200:203], v[84:87]\n" \
"buffer_load_dwordx4 v20, s[80:83], s75 offen offset:0 sc1 lds\n" \
"v_mfma_f32_16x16x32_bf16 v[88:91], a[180:183], a[136:139], v[88:91]\n" \
"v_cvt_pk_bf16_f32 v133, v134, v135\n" \
"v_mul_f32_e64 v144, s70, v144\n" \
"v_mfma_f32_16x16x32_bf16 v[88:91], a[244:247], a[200:203], v[88:91]\n" \
"v_mul_f32_e32 v145, s70, v145\n" \
"v_mul_f32_e32 v146, s70, v146\n" \
"v_mfma_f32_16x16x32_bf16 v[92:95], a[184:187], a[136:139], v[92:95]\n" \
"v_mul_f32_e64 v147, s70, v147\n" \
"v_cvt_pk_bf16_f32 v141, v142, v143\n" \
"v_mfma_f32_16x16x32_bf16 v[92:95], a[248:251], a[200:203], v[92:95]\n" \
"buffer_load_dwordx4 v20, s[80:83], s75 offen offset:1024 sc1 lds\n" \
"v_mfma_f32_16x16x32_bf16 v[96:99], a[188:191], a[136:139], v[96:99]\n" \
"v_cvt_pk_bf16_f32 v134, v136, v137\n" \
"v_cvt_pk_bf16_f32 v135, v138, v139\n" \
"v_mfma_f32_16x16x32_bf16 v[96:99], a[252:255], a[200:203], v[96:99]\n" \
"v_cvt_pk_bf16_f32 v142, v144, v145\n" \
"v_cvt_pk_bf16_f32 v143, v146, v147\n" \
"v_mfma_f32_16x16x32_bf16 v[112:115], a[188:191], a[140:143], v[112:115]\n" \
"ds_write_b128 v21, v[132:135] offset:20480\n" \
"v_mfma_f32_16x16x32_bf16 v[112:115], a[252:255], a[204:207], v[112:115]\n" \
"buffer_load_dwordx4 v20, s[80:83], s75 offen offset:2048 sc1 lds\n" \
"v_mfma_f32_16x16x32_bf16 v[108:111], a[184:187], a[140:143], v[108:111]\n" \
"v_cvt_pk_bf16_f32 v180, v180, v181\n" \
"v_cvt_pk_bf16_f32 v181, v182, v183\n" \
"v_mfma_f32_16x16x32_bf16 v[108:111], a[248:251], a[204:207], v[108:111]\n" \
"ds_write_b128 v21, v[140:143] offset:21504\n" \
"v_mfma_f32_16x16x32_bf16 v[104:107], a[180:183], a[140:143], v[104:107]\n" \
"v_cvt_pk_bf16_f32 v234, v244, v245\n" \
"v_mov_b32_e64 v235, 0\n" \
"v_mfma_f32_16x16x32_bf16 v[104:107], a[244:247], a[204:207], v[104:107]\n" \
"buffer_load_dwordx4 v20, s[80:83], s75 offen offset:3072 sc1 lds\n" \
"v_mfma_f32_16x16x32_bf16 v[100:103], a[176:179], a[140:143], v[100:103]\n" \
"s_mov_b32 m0, s53\n" \
"s_nop 0\n" \
"v_cvt_pk_bf16_f32 v182, v50, v51\n" \
"v_mfma_f32_16x16x32_bf16 v[100:103], a[240:243], a[204:207], v[100:103]\n" \
"v_mov_b32_e32 v183, 0x3f80\n" \
"v_cvt_pk_bf16_f32 v118, v192, v193\n" \
"v_mfma_f32_16x16x32_bf16 v[52:55], a[176:179], a[128:131], v[52:55]\n" \
"v_cvt_pk_bf16_f32 v119, v194, v195\n" \
"v_cvt_pk_bf16_f32 v120, v208, v209\n" \
"v_mfma_f32_16x16x32_bf16 v[52:55], a[240:243], a[192:195], v[52:55]\n" \
"buffer_load_dwordx4 v20, s[80:83], s76 offen offset:0 sc1 lds\n" \
"v_mfma_f32_16x16x32_bf16 v[56:59], a[180:183], a[128:131], v[56:59]\n" \
"v_cvt_pk_bf16_f32 v121, v210, v211\n" \
"v_cvt_pk_bf16_f32 v122, v224, v225\n" \
"v_mfma_f32_16x16x32_bf16 v[56:59], a[244:247], a[192:195], v[56:59]\n" \
"v_cvt_pk_bf16_f32 v123, v226, v227\n" \
"v_cvt_pk_bf16_f32 v124, v240, v241\n" \
"v_cvt_pk_bf16_f32 v125, v242, v243\n" \
"v_mfma_f32_16x16x32_bf16 v[60:63], a[184:187], a[128:131], v[60:63]\n" \
"v_mfma_f32_16x16x32_bf16 v[192:195], v[188:191], v[184:187], 0\n" \
"buffer_load_dwordx4 v20, s[80:83], s76 offen offset:1024 sc1 lds\n" \
"v_mfma_f32_16x16x32_bf16 v[32:35], v[180:183], v[184:187], 0\n" \
"v_mfma_f32_16x16x32_bf16 v[208:211], v[204:207], v[200:203], 0\n" \
"v_mfma_f32_16x16x32_bf16 v[36:39], v[188:191], v[200:203], 0\n" \
"v_mfma_f32_16x16x32_bf16 v[224:227], v[220:223], v[216:219], 0\n" \
"v_mfma_f32_16x16x32_bf16 v[248:251], v[204:207], v[216:219], 0\n" \
"v_mfma_f32_16x16x32_bf16 v[240:243], v[236:239], v[232:235], 0\n" \
"v_mfma_f32_16x16x32_bf16 v[128:131], v[220:223], v[232:235], 0\n" \
"s_mov_b32 s64, 0xaaaaaaaa\n" \
"s_mov_b32 s65, 0xaaaaaaaa\n" \
"v_mfma_f32_16x16x32_bf16 v[60:63], a[248:251], a[192:195], v[60:63]\n" \
"s_mov_b32 s66, 0xfff0ffff\n" \
"s_mov_b32 s67, 0xf000ff00\n" \
"v_mfma_f32_16x16x32_bf16 v[64:67], a[188:191], a[128:131], v[64:67]\n" \
"s_mov_b32 s68, 0xffc0fffc\n" \
"s_mov_b32 s69, 0xc000fc00\n" \
"v_cndmask_b32_e64 v192, v192, v193, s[64:65]\n" \
"v_cndmask_b32_e64 v32, v32, v33, s[64:65]\n" \
"v_cndmask_b32_e64 v194, v194, v195, s[64:65]\n" \
"v_cndmask_b32_e64 v34, v34, v35, s[64:65]\n" \
"v_cndmask_b32_e64 v208, v208, v209, s[64:65]\n" \
"v_cndmask_b32_e64 v36, v36, v37, s[64:65]\n" \
"v_cndmask_b32_e64 v210, v210, v211, s[64:65]\n" \
"v_cndmask_b32_e64 v38, v38, v39, s[64:65]\n" \
"v_cndmask_b32_e64 v224, v224, v225, s[64:65]\n" \
"v_cndmask_b32_e64 v248, v248, v249, s[64:65]\n" \
"v_cndmask_b32_e64 v226, v226, v227, s[64:65]\n" \
"v_cndmask_b32_e64 v250, v250, v251, s[64:65]\n" \
"v_cndmask_b32_e64 v240, v240, v241, s[64:65]\n" \
"v_cndmask_b32_e64 v128, v128, v129, s[64:65]\n" \
"v_cndmask_b32_e64 v242, v242, v243, s[64:65]\n" \
"v_cndmask_b32_e64 v130, v130, v131, s[64:65]\n" \
"v_cndmask_b32_e64 v192, v32, v192, s[66:67]\n" \
"v_cndmask_b32_e64 v194, v34, v194, s[68:69]\n" \
"v_cndmask_b32_e64 v208, v36, v208, s[66:67]\n" \
"v_cndmask_b32_e64 v210, v38, v210, s[68:69]\n" \
"v_cndmask_b32_e64 v224, v248, v224, s[66:67]\n" \
"v_cndmask_b32_e64 v226, v250, v226, s[68:69]\n" \
"v_cndmask_b32_e64 v240, v128, v240, s[66:67]\n" \
"v_cndmask_b32_e64 v242, v130, v242, s[68:69]\n" \
"v_mfma_f32_16x16x32_bf16 v[64:67], a[252:255], a[192:195], v[64:67]\n" \
"v_max_f32_e32 v193, v192, v194\n" \
"v_max_f32_e32 v209, v208, v210\n" \
"v_max_f32_e32 v225, v224, v226\n" \
"v_max_f32_e32 v241, v240, v242\n" \
"v_mul_f32_e32 v148, s70, v148\n" \
"v_permlane32_swap_b32_e32 v193, v225\n" \
"v_permlane32_swap_b32_e32 v209, v241\n" \
"v_mul_f32_e32 v149, s70, v149\n" \
"v_mul_f32_e32 v156, s70, v156\n" \
"v_mul_f32_e32 v157, s70, v157\n" \
"v_permlane32_swap_b32_e32 v196, v228\n" \
"v_permlane32_swap_b32_e32 v212, v244\n" \
"v_max_f32_e32 v193, v193, v225\n" \
"v_max_f32_e32 v209, v209, v241\n" \
"v_mul_f32_e32 v150, s70, v150\n" \
"v_mul_f32_e32 v151, s70, v151\n" \
"v_cvt_pk_bf16_f32 v148, v148, v149\n" \
"v_mul_f32_e32 v158, s70, v158\n" \
"v_mul_f32_e32 v159, s70, v159\n" \
"v_cvt_pk_bf16_f32 v156, v156, v157\n" \
"v_permlane16_swap_b32_e32 v228, v244\n" \
"v_permlane16_swap_b32_e32 v193, v209\n" \
"v_mfma_f32_16x16x32_bf16 v[80:83], a[188:191], a[132:135], v[80:83]\n" \
"s_mov_b32 s72, 0x3eae1a93\n" \
"v_mul_f32_e32 v152, s70, v152\n" \
"v_mul_f32_e32 v153, s70, v153\n" \
"v_mul_f32_e32 v154, s70, v154\n" \
"v_mul_f32_e32 v155, s70, v155\n" \
"v_cvt_pk_bf16_f32 v149, v150, v151\n" \
"v_mul_f32_e32 v160, s70, v160\n" \
"v_mul_f32_e32 v161, s70, v161\n" \
"v_mul_f32_e32 v162, s70, v162\n" \
"v_mul_f32_e32 v163, s70, v163\n" \
"v_cvt_pk_bf16_f32 v157, v158, v159\n" \
"v_mul_f32_e32 v45, 0xbfb8aa3b, v228\n" \
"v_mfma_f32_16x16x16_bf16 v[48:51], v[182:183], v[46:47], 0\n" \
"v_mfma_f32_16x16x16_bf16 v[196:199], v[190:191], v[46:47], 0\n" \
"v_mfma_f32_16x16x16_bf16 v[212:215], v[206:207], v[46:47], 0\n" \
"v_max_f32_e64 v193, v193, v209\n" \
"v_cvt_pk_bf16_f32 v150, v152, v153\n" \
"v_cvt_pk_bf16_f32 v151, v154, v155\n" \
"v_cvt_pk_bf16_f32 v158, v160, v161\n" \
"v_cvt_pk_bf16_f32 v159, v162, v163\n" \
"v_mfma_f32_16x16x16_bf16 v[228:231], v[222:223], v[46:47], 0\n" \
"v_mul_f32_e32 v193, s72, v193\n" \
"v_mov_b32_e32 v209, v193\n" \
"ds_write_b128 v21, v[148:151] offset:24576\n" \
"v_mfma_f32_16x16x16_bf16 v[244:247], v[238:239], v[46:47], 0\n" \
"v_permlane16_swap_b32_e32 v193, v209\n" \
"s_mov_b32 s71, 0x3e0293ee\n" \
"v_mfma_f32_16x16x32_bf16 v[80:83], a[252:255], a[196:199], v[80:83]\n" \
"v_mov_b32_e32 v225, v193\n" \
"v_mov_b32_e32 v241, v209\n" \
"v_mul_f32_e32 v164, s70, v164\n" \
"v_mul_f32_e64 v165, s70, v165\n" \
"ds_write_b128 v21, v[156:159] offset:25600\n" \
"v_mul_f32_e32 v172, s70, v172\n" \
"v_mul_f32_e32 v173, s70, v173\n" \
"v_pk_mul_f32 v[84:85], v[84:85], s[70:71]\n" \
"v_pk_mul_f32 v[92:93], v[92:93], s[70:71]\n" \
"v_pk_mul_f32 v[100:101], v[100:101], s[70:71]\n" \
"v_pk_mul_f32 v[108:109], v[108:109], s[70:71]\n" \
"v_pk_mul_f32 v[52:53], v[52:53], s[70:71]\n" \
"v_pk_mul_f32 v[60:61], v[60:61], s[70:71]\n" \
"v_permlane32_swap_b32_e32 v193, v225\n" \
"v_permlane32_swap_b32_e32 v209, v241\n" \
"v_mul_f32_e32 v166, s70, v166\n" \
"v_mul_f32_e32 v167, s70, v167\n" \
"v_cvt_pk_bf16_f32 v164, v164, v165\n" \
"v_mul_f32_e32 v174, s70, v174\n" \
"v_mul_f32_e32 v175, s70, v175\n" \
"v_cvt_pk_bf16_f32 v172, v172, v173\n" \
"v_pk_mul_f32 v[86:87], v[86:87], s[70:71]\n" \
"v_cvt_pk_bf16_f32 v84, v84, v85\n" \
"v_pk_mul_f32 v[94:95], v[94:95], s[70:71]\n" \
"v_cvt_pk_bf16_f32 v92, v92, v93\n" \
"v_pk_mul_f32 v[102:103], v[102:103], s[70:71]\n" \
"v_cvt_pk_bf16_f32 v100, v100, v101\n" \
"v_pk_mul_f32 v[110:111], v[110:111], s[70:71]\n" \
"v_cvt_pk_bf16_f32 v108, v108, v109\n" \
"v_pk_mul_f32 v[54:55], v[54:55], s[70:71]\n" \
"v_cvt_pk_bf16_f32 v52, v52, v53\n" \
"v_pk_mul_f32 v[62:63], v[62:63], s[70:71]\n" \
"v_cvt_pk_bf16_f32 v60, v60, v61\n" \
"v_fma_f32 v192, v192, s72, -v193\n" \
"v_fma_f32 v194, v194, s72, -v193\n" \
"v_fma_f32 v208, v208, s72, -v209\n" \
"v_fma_f32 v210, v210, s72, -v209\n" \
"v_mul_f32_e32 v168, s70, v168\n" \
"v_mul_f32_e32 v169, s70, v169\n" \
"v_mul_f32_e32 v170, s70, v170\n" \
"v_mul_f32_e32 v171, s70, v171\n" \
"v_cvt_pk_bf16_f32 v165, v166, v167\n" \
"v_mul_f32_e32 v176, s70, v176\n" \
"v_mul_f32_e32 v177, s70, v177\n" \
"v_mul_f32_e32 v178, s70, v178\n" \
"v_mul_f32_e32 v179, s70, v179\n" \
"v_cvt_pk_bf16_f32 v173, v174, v175\n" \
"v_pk_mul_f32 v[88:89], v[88:89], s[70:71]\n" \
"v_pk_mul_f32 v[90:91], v[90:91], s[70:71]\n" \
"v_cvt_pk_bf16_f32 v85, v86, v87\n" \
"v_pk_mul_f32 v[96:97], v[96:97], s[70:71]\n" \
"v_pk_mul_f32 v[98:99], v[98:99], s[70:71]\n" \
"v_cvt_pk_bf16_f32 v93, v94, v95\n" \
"v_pk_mul_f32 v[104:105], v[104:105], s[70:71]\n" \
"v_pk_mul_f32 v[106:107], v[106:107], s[70:71]\n" \
"v_cvt_pk_bf16_f32 v101, v102, v103\n" \
"v_pk_mul_f32 v[112:113], v[112:113], s[70:71]\n" \
"v_pk_mul_f32 v[114:115], v[114:115], s[70:71]\n" \
"v_cvt_pk_bf16_f32 v109, v110, v111\n" \
"v_pk_mul_f32 v[56:57], v[56:57], s[70:71]\n" \
"v_pk_mul_f32 v[58:59], v[58:59], s[70:71]\n" \
"v_cvt_pk_bf16_f32 v53, v54, v55\n" \
"v_pk_mul_f32 v[64:65], v[64:65], s[70:71]\n" \
"v_pk_mul_f32 v[66:67], v[66:67], s[70:71]\n" \
"v_cvt_pk_bf16_f32 v61, v62, v63\n" \
"v_pk_mul_f32 v[80:81], v[80:81], s[70:71]\n" \
"v_pk_mul_f32 v[82:83], v[82:83], s[70:71]\n" \
"v_exp_f32_e32 v192, v192\n" \
"v_exp_f32_e32 v194, v194\n" \
"v_exp_f32_e32 v208, v208\n" \
"v_exp_f32_e32 v210, v210\n" \
"v_fma_f32 v224, v224, s72, -v225\n" \
"v_fma_f32 v226, v226, s72, -v225\n" \
"v_mfma_f32_16x16x32_bf16 v[76:79], a[184:187], a[132:135], v[76:79]\n" \
"v_cvt_pk_bf16_f32 v166, v168, v169\n" \
"v_cvt_pk_bf16_f32 v167, v170, v171\n" \
"v_cvt_pk_bf16_f32 v174, v176, v177\n" \
"v_cvt_pk_bf16_f32 v175, v178, v179\n" \
"v_cvt_pk_bf16_f32 v86, v88, v89\n" \
"v_cvt_pk_bf16_f32 v87, v90, v91\n" \
"v_cvt_pk_bf16_f32 v94, v96, v97\n" \
"v_cvt_pk_bf16_f32 v95, v98, v99\n" \
"v_cvt_pk_bf16_f32 v102, v104, v105\n" \
"v_cvt_pk_bf16_f32 v103, v106, v107\n" \
"v_cvt_pk_bf16_f32 v110, v112, v113\n" \
"v_cvt_pk_bf16_f32 v111, v114, v115\n" \
"v_cvt_pk_bf16_f32 v54, v56, v57\n" \
"v_cvt_pk_bf16_f32 v55, v58, v59\n" \
"v_cvt_pk_bf16_f32 v62, v64, v65\n" \
"v_cvt_pk_bf16_f32 v63, v66, v67\n" \
"v_exp_f32_e32 v224, v224\n" \
"v_exp_f32_e32 v226, v226\n" \
"v_fma_f32 v240, v240, s72, -v241\n" \
"v_fma_f32 v242, v242, s72, -v241\n" \
"ds_write_b128 v21, v[164:167] offset:28672\n" \
"v_cvt_pk_bf16_f32 v192, v192, v194\n" \
"v_cvt_pk_bf16_f32 v208, v208, v210\n" \
"v_exp_f32_e32 v240, v240\n" \
"v_exp_f32_e32 v242, v242\n" \
"v_cvt_pk_bf16_f32 v132, v48, v49\n" \
"v_cvt_pk_bf16_f32 v133, v50, v51\n" \
"v_cvt_pk_bf16_f32 v134, v196, v197\n" \
"v_cvt_pk_bf16_f32 v135, v198, v199\n" \
"ds_write_b128 v21, v[172:175] offset:29696\n" \
"v_cvt_pk_bf16_f32 v136, v212, v213\n" \
"v_cvt_pk_bf16_f32 v137, v214, v215\n" \
"v_perm_b32 v184, v192, v192, v40\n" \
"v_perm_b32 v185, v192, v192, v41\n" \
"v_perm_b32 v186, v192, v192, v42\n" \
"v_perm_b32 v187, v192, v192, v43\n" \
"v_perm_b32 v200, v208, v208, v40\n" \
"v_perm_b32 v201, v208, v208, v41\n" \
"v_perm_b32 v202, v208, v208, v42\n" \
"v_perm_b32 v203, v208, v208, v43\n" \
"ds_write_b128 v27, v[84:87] offset:8192\n" \
"v_cvt_pk_bf16_f32 v224, v224, v226\n" \
"v_cvt_pk_bf16_f32 v138, v228, v229\n" \
"v_cvt_pk_bf16_f32 v139, v230, v231\n" \
"v_mfma_f32_16x16x32_bf16 v[32:35], v[132:135], v[184:187], 0\n" \
"v_mfma_f32_16x16x32_bf16 v[36:39], v[134:137], v[200:203], 0\n" \
"v_perm_b32 v216, v224, v224, v40\n" \
"v_perm_b32 v217, v224, v224, v41\n" \
"ds_write_b128 v27, v[92:95] offset:9216\n" \
"v_perm_b32 v218, v224, v224, v42\n" \
"v_perm_b32 v219, v224, v224, v43\n" \
"v_cvt_pk_bf16_f32 v240, v240, v242\n" \
"v_mfma_f32_16x16x32_bf16 v[76:79], a[248:251], a[196:199], v[76:79]\n" \
"v_cvt_pk_bf16_f32 v140, v244, v245\n" \
"v_cvt_pk_bf16_f32 v141, v246, v247\n" \
"v_mfma_f32_16x16x32_bf16 v[248:251], v[136:139], v[216:219], 0\n" \
"v_perm_b32 v232, v240, v240, v40\n" \
"v_perm_b32 v233, v240, v240, v41\n" \
"ds_write_b128 v27, v[100:103] offset:12288\n" \
"v_perm_b32 v234, v240, v240, v42\n" \
"v_perm_b32 v235, v240, v240, v43\n" \
"v_pk_mul_f32 v[76:77], v[76:77], s[70:71]\n" \
"v_pk_mul_f32 v[78:79], v[78:79], s[70:71]\n" \
"v_cvt_pk_bf16_f32 v76, v76, v77\n" \
"v_cvt_pk_bf16_f32 v77, v78, v79\n" \
"v_cvt_pk_bf16_f32 v78, v80, v81\n" \
"v_cvt_pk_bf16_f32 v79, v82, v83\n" \
"v_mfma_f32_16x16x32_bf16 v[128:131], v[138:141], v[232:235], 0\n" \
"ds_write_b128 v27, v[108:111] offset:13312\n" \
"v_mfma_f32_16x16x32_bf16 v[72:75], a[180:183], a[132:135], v[72:75]\n" \
"v_cvt_pk_bf16_f32 v116, v252, v253\n" \
"v_cvt_pk_bf16_f32 v117, v254, v255\n" \
"v_mfma_f32_16x16x32_bf16 v[72:75], a[244:247], a[196:199], v[72:75]\n" \
"v_exp_f32_e64 v45, v45\n" \
"v_mfma_f32_16x16x32_bf16 v[192:195], v[116:119], v[184:187], 0\n" \
"ds_write_b128 v27, v[52:55] offset:0\n" \
"v_mfma_f32_16x16x32_bf16 v[208:211], v[118:121], v[200:203], 0\n" \
"v_mfma_f32_16x16x32_bf16 v[224:227], v[120:123], v[216:219], 0\n" \
"v_mfma_f32_16x16x32_bf16 v[240:243], v[122:125], v[232:235], 0\n" \
"ds_write_b128 v27, v[60:63] offset:1024\n" \
"v_mov_b32_dpp v34, v38 quad_perm:[0,1,2,3] row_mask:0x2 bank_mask:0xf\n" \
"v_pk_mul_f32 v[72:73], v[72:73], s[70:71]\n" \
"v_pk_mul_f32 v[74:75], v[74:75], s[70:71]\n" \
"v_mov_b32_dpp v34, v250 quad_perm:[0,1,2,3] row_mask:0x4 bank_mask:0xf\n" \
"v_add_f32_e64 v45, 1.0, v45\n" \
"v_mfma_f32_16x16x32_bf16 v[68:71], a[176:179], a[132:135], v[68:71]\n" \
"v_mov_b32_dpp v34, v130 quad_perm:[0,1,2,3] row_mask:0x8 bank_mask:0xf\n" \
"v_mov_b32_dpp v32, v36 quad_perm:[0,1,2,3] row_mask:0x2 bank_mask:0xf\n" \
"v_rcp_f32_e64 v45, v45\n" \
"ds_write_b128 v27, v[76:79] offset:5120\n" \
"v_mov_b32_dpp v33, v37 quad_perm:[0,1,2,3] row_mask:0x2 bank_mask:0xf\n" \
"v_rcp_f32_e64 v34, v34\n" \
"v_mov_b32_dpp v32, v248 quad_perm:[0,1,2,3] row_mask:0x4 bank_mask:0xf\n" \
"v_mov_b32_dpp v33, v249 quad_perm:[0,1,2,3] row_mask:0x4 bank_mask:0xf\n" \
"v_mul_f32_e64 v34, v45, v34\n" \
"v_mov_b32_dpp v32, v128 quad_perm:[0,1,2,3] row_mask:0x8 bank_mask:0xf\n" \
"v_mov_b32_dpp v33, v129 quad_perm:[0,1,2,3] row_mask:0x8 bank_mask:0xf\n" \
"v_mul_f32_e32 v32, v34, v32\n" \
"v_mul_f32_e32 v33, v34, v33\n" \
"v_mov_b32_e64 v38, v34\n" \
"v_mfma_f32_16x16x32_bf16 v[68:71], a[240:243], a[196:199], v[68:71]\n" \
"v_sub_f32_e32 v44, 1.0, v45\n" \
"v_permlane16_swap_b32_e32 v34, v38\n" \
"v_mov_b32_e32 v250, v34\n" \
"v_mov_b32_e32 v130, v38\n" \
"v_cvt_pk_bf16_f32 v32, v32, v33\n" \
"v_permlane32_swap_b32_e32 v34, v250\n" \
"v_permlane32_swap_b32_e32 v38, v130\n" \
"v_mul_f32_e64 v192, v34, v192\n" \
"v_pk_mul_f32 v[68:69], v[68:69], s[70:71]\n" \
"v_pk_mul_f32 v[70:71], v[70:71], s[70:71]\n" \
"v_cvt_pk_bf16_f32 v68, v68, v69\n" \
"v_cvt_pk_bf16_f32 v69, v70, v71\n" \
"v_cvt_pk_bf16_f32 v70, v72, v73\n" \
"v_cvt_pk_bf16_f32 v71, v74, v75\n" \
"ds_write_b128 v27, v[68:71] offset:4096\n" \
"v_mul_f32_e32 v193, v34, v193\n" \
"v_mul_f32_e32 v208, v38, v208\n" \
"v_mul_f32_e32 v209, v38, v209\n" \
"v_mul_f32_e32 v224, v250, v224\n" \
"v_mul_f32_e32 v225, v250, v225\n" \
"v_mul_f32_e32 v240, v130, v240\n" \
"v_mul_f32_e32 v241, v130, v241\n" \
"v_mul_f32_e32 v194, v34, v194\n" \
"v_mul_f32_e64 v195, v34, v195\n" \
"v_cvt_pk_bf16_f32 v192, v192, v193\n" \
"v_mul_f32_e32 v210, v38, v210\n" \
"v_mul_f32_e32 v211, v38, v211\n" \
"v_cvt_pk_bf16_f32 v208, v208, v209\n" \
"v_mul_f32_e32 v226, v250, v226\n" \
"v_mul_f32_e32 v227, v250, v227\n" \
"v_cvt_pk_bf16_f32 v224, v224, v225\n" \
"v_mul_f32_e32 v242, v130, v242\n" \
"v_mul_f32_e32 v243, v130, v243\n" \
"v_cvt_pk_bf16_f32 v240, v240, v241\n" \
"v_cvt_pk_bf16_f32 v193, v194, v195\n" \
"s_add_u32 s84, s48, 0x0\n" \
"s_nop 0\n" \
"v_cvt_pk_bf16_f32 v209, v210, v211\n" \
"s_add_u32 s85, s48, 0x24800\n" \
"v_cvt_pk_bf16_f32 v225, v226, v227\n" \
"s_add_u32 s86, s48, 0x49000\n" \
"v_cvt_pk_bf16_f32 v241, v242, v243\n" \
"s_add_u32 s87, s48, 0x6d800\n" \
"ds_write_b32 v26, v44\n" \
"buffer_store_dword v32, v25, s[80:83], s49 offen sc1\n" \
"buffer_store_dwordx2 v[192:193], v24, s[80:83], s84 offen sc1\n" \
"buffer_store_dwordx2 v[208:209], v24, s[80:83], s85 offen sc1\n" \
"buffer_store_dwordx2 v[224:225], v24, s[80:83], s86 offen sc1\n" \
"buffer_store_dwordx2 v[240:241], v24, s[80:83], s87 offen sc1\n" \
"s_branch L_end_%=\n" \
"L_poll61_%=:\n" \
"s_mov_b32 s84, 0\n" \
"L_pl61_%=:\n" \
"s_sleep 1\n" \
"v_bfe_u32 v44, v20, 2, 5\n" \
"buffer_load_dword v44, v44, s[80:83], s38 offen offset:128 sc1\n" \
"s_waitcnt vmcnt(0)\n" \
"v_cmp_ne_u32_e32 vcc, s62, v44\n" \
"s_add_u32 s84, s84, 1\n" \
"s_nop 3\n" \
"s_cbranch_vccz L_pollr61_%=\n" \
"s_cmp_gt_u32 s84, 0x400000\n" \
"s_cbranch_scc0 L_pl61_%=\n" \
"s_branch L_pollr61_%=\n" \
"L_poll161_%=:\n" \
"s_mov_b32 s84, 0\n" \
"L_pl161_%=:\n" \
"s_sleep 1\n" \
"v_bfe_u32 v44, v20, 2, 5\n" \
"buffer_load_dword v44, v44, s[80:83], s38 offen offset:0 sc1\n" \
"s_waitcnt vmcnt(0)\n" \
"v_cmp_ne_u32_e32 vcc, s62, v44\n" \
"s_add_u32 s84, s84, 1\n" \
"s_nop 3\n" \
"s_cbranch_vccz L_pollr161_%=\n" \
"s_cmp_gt_u32 s84, 0x400000\n" \
"s_cbranch_scc0 L_pl161_%=\n" \
"s_branch L_pollr161_%=\n" \
"L_end_%=:\n"
#define PROJ_VCLOB "v21","v24","v25","v28","v29","v32","v33","v34","v35","v36","v37","v38","v39","v40","v41","v42","v43","v44","v45","v46","v47","v48","v49","v50","v51","v52","v53","v54","v55","v56","v57","v58","v59","v60","v61","v62","v63","v64","v65","v66","v67","v68","v69","v70","v71","v72","v73","v74","v75","v76","v77","v78","v79","v80","v81","v82","v83","v84","v85","v86","v87","v88","v89","v90","v91","v92","v93","v94","v95","v96","v97","v98","v99","v100","v101","v102","v103","v104","v105","v106","v107","v108","v109","v110","v111","v112","v113","v114","v115","v116","v117","v118","v119","v120","v121","v122","v123","v124","v125","v126","v127","v128","v129","v130","v131","v132","v133","v134","v135","v136","v137","v138","v139","v140","v141","v142","v143","v144","v145","v146","v147","v148","v149","v150","v151","v152","v153","v154","v155","v156","v157","v158","v159","v160","v161","v162","v163","v164","v165","v166","v167","v168","v169","v170","v171","v172","v173","v174","v175","v176","v177","v178","v179","v180","v181","v182","v183","v184","v185","v186","v187","v188","v189","v190","v192","v193","v194","v195","v196","v197","v198","v199","v200","v201","v202","v203","v204","v205","v206","v207","v208","v209","v210","v211","v212","v213","v214","v215","v216","v217","v218","v219","v220","v221","v222","v223","v224","v225","v226","v227","v228","v229","v230","v231","v232","v233","v234","v235","v236","v237","v238","v239","v240","v241","v242","v243","v244","v245","v246","v247","v248","v249","v250","v251","v252","v253","v254","v255"
#define PROJ_SCLOB "s4","s5","s6","s7","s32","s33","s34","s35","s36","s37","s38","s39","s40","s41","s42","s43","s44","s45","s46","s47","s48","s49","s50","s51","s52","s53","s54","s55","s63","s64","s65","s66","s67","s68","s69","s70","s71","s72","s73","s74","s75","s76","s77","s78","s79","s80","s81","s82","s83","s84","s85","s86","s87","s88","s89","s90","s91","s92","s93","s94","s95","s96","s97","s98","s99","s100","s101"

typedef const u16* const __attribute__((address_space(4)))* kaptr;
__device__ __forceinline__ void proj_phase(kaptr& ka, char*& ws, const unsigned wid, u32x4 (&vp)[5], unsigned& vl, u32x4 (&sa)[5], u32x2 s24, u32x2& s28, unsigned tdum, u32x4& srdA, u32x2& sd, unsigned& tag
    ) {
    const unsigned b = blockIdx.x, h = (b & 7) * 4 + ((b >> 3) & 3), mt = b >> 5;
    const unsigned rb = (wid < 2u ? 15u - mt : mt) * 128u;
    const unsigned lane = threadIdx.x & 63u, wc = wid & 1u;
    u32x2 vAc, vB;
#pragma unroll
    for (int par = 0; par < 2; ++par) {
        const unsigned lr = lane >> 3, cc = (lane & 7u) ^ (4u * par + (lr >> 1)), r0 = rb + wc * 64u + lr;
        vAc[par] = r0 * (DC * 2) + cc * 16u;
        const unsigned br = lane >> 4, p16 = lane & 15u, sc = (((p16 >> 1) ^ (br | (par << 2))) << 1) | (p16 & 1u);
        vB[par] = (16u * wid + br) * (NBIG * 2) + h * (HD * 2) + sc * 16u;
    }
    vl = lane * 16u;
    u32x2 s30 = {wid * 8192u, 65536u + wid * 4096u};
    asm volatile(PROJ_ASM
                 : "={s62}"(tag), "={s[56:59]}"(srdA), "={s[60:61]}"(sd), "+{s[8:11]}"(sa[0]), "+{s[12:15]}"(sa[1]), "+{s[16:19]}"(sa[2]), "+{s[20:23]}"(sa[3]), "+{s[24:25]}"(s24), "+{s[28:29]}"(s28), "+{s[30:31]}"(s30),
                   "={v[0:3]}"(vp[0]), "={v[4:7]}"(vp[1]), "={v[8:11]}"(vp[2]), "={v[12:15]}"(vp[3]), "={v[16:19]}"(vp[4]), "+{v[22:23]}"(vAc), "+{v[26:27]}"(vB), "+{v191}"(tdum), "+{s[26:27]}"(ws), "+{s[0:1]}"(ka)
                 : "{s2}"(b), "{s3}"(wid), "{v20}"(vl)
                 : "memory", "m0", "vcc", "scc", PROJ_VCLOB, PROJ_SCLOB, ACLOB_LO, ACLOB_HI);
    sa[4] = (u32x4){s24[0], 0u, 0u, 0u};
}

namespace att {
#define DI __device__ __forceinline__
typedef __bf16 bfx8 __attribute__((ext_vector_type(8)));
DI unsigned rfl(unsigned x) { return __builtin_amdgcn_readfirstlane(x); }
DI f32x4 mfma(u32x4 a, u32x4 b, f32x4 c) { return __builtin_amdgcn_mfma_f32_16x16x32_bf16(__builtin_bit_cast(bfx8, a), __builtin_bit_cast(bfx8, b), c, 0, 0, 0); }
typedef __bf16 bfx2 __attribute__((ext_vector_type(2)));
typedef float f32x2 __attribute__((ext_vector_type(2)));
DI unsigned cvtpk2(float lo, float hi) { f32x2 v = {lo, hi}; bfx2 b = __builtin_convertvector(v, bfx2); return __builtin_bit_cast(unsigned, b); }
DI void att_exact(const u16* __restrict__ x, const u16* __restrict__ wq, char* __restrict__ ws, unsigned h, unsigned r0, float g1) {
    const unsigned lane = lane_id(), g = lane >> 4, c = lane & 15u;
    f32x4 o[8];
#pragma unroll
    for (int n = 0; n < 8; ++n) o[n] = (f32x4){0.f, 0.f, 0.f, 0.f};
#pragma unroll 1
    for (unsigned kk = 0; kk < (unsigned)DM; kk += 32u) {
        const unsigned k = kk + 8u * g;
        const u32x4 xf = *reinterpret_cast<const u32x4*>(x + (r0 + c) * DM + k);
#pragma unroll
        for (int n = 0; n < 8; ++n) {
            const u16* w = wq + k * NBIG + h * HD + n * 16 + c; u32x4 wf;
#pragma unroll
            for (int e = 0; e < 4; ++e) wf[e] = (unsigned)w[2 * e * NBIG] | ((unsigned)w[(2 * e + 1) * NBIG] << 16);
            o[n] = mfma(wf, xf, o[n]);
        }
    }
    u32x4 q[4];
#pragma unroll
    for (int p = 0; p < 4; ++p) {
        const f32x4 a = o[2 * p] * 0.12751743082f, b = o[2 * p + 1] * 0.12751743082f;
        q[p] = (u32x4){cvtpk2(a[0], a[1]), cvtpk2(a[2], a[3]), cvtpk2(b[0], b[1]), cvtpk2(b[2], b[3])};
    }
#pragma unroll
    for (int n = 0; n < 8; ++n) o[n] = (f32x4){0.f, 0.f, 0.f, 0.f};
    const __amdgpu_buffer_rsrc_t srd = __builtin_amdgcn_make_buffer_rsrc((void*)(ws + WS_K + (size_t)h * 1048576), (short)0, 1048576, 0x00020000);
    float m = -3.0e38f, l = 0.f;
#pragma unroll 1
    for (unsigned k0 = 0; k0 <= r0 + 15u; k0 += 32u) {
        const unsigned tb = (k0 >> 6) * 32768u + (k0 & 32u) * 256u;
        f32x4 s[2]; float mx = m;
#pragma unroll
        for (int kt = 0; kt < 2; ++kt) {
            s[kt] = (f32x4){0.f, 0.f, 0.f, 0.f};
#pragma unroll
            for (int p = 0; p < 4; ++p) s[kt] = mfma(__builtin_amdgcn_raw_buffer_load_b128(srd, lane * 16u + (kt * 4 + p) * 1024u, tb, 16), q[p], s[kt]);
#pragma unroll
            for (int e = 0; e < 4; ++e) { if (k0 + 16u * kt + 4u * g + e > r0 + c) s[kt][e] = -3.0e38f; mx = fmaxf(mx, s[kt][e]); }
        }
        mx = fmaxf(mx, __shfl_xor(mx, 16)); mx = fmaxf(mx, __shfl_xor(mx, 32));
        const float al = __builtin_amdgcn_exp2f(m - mx); m = mx;
        u32x4 pf;
#pragma unroll
        for (int e = 0; e < 4; ++e) pf[e] = cvtpk2(__builtin_amdgcn_exp2f(s[e >> 1][2 * (e & 1)] - m), __builtin_amdgcn_exp2f(s[e >> 1][2 * (e & 1) + 1] - m));
        l *= al;
#pragma unroll
        for (int e = 0; e < 4; ++e) l += __uint_as_float(pf[e] << 16) + __uint_as_float(pf[e] & 0xffff0000u);
#pragma unroll
        for (int n = 0; n < 8; ++n) o[n] = mfma(__builtin_amdgcn_raw_buffer_load_b128(srd, lane * 16u + 16384u + n * 1024u, tb, 16), pf, o[n] * al);
    }
    l += __shfl_xor(l, 16); l += __shfl_xor(l, 32);
    const float f = g1 / l;
    const __amdgpu_buffer_rsrc_t srdA = __builtin_amdgcn_make_buffer_rsrc((void*)(ws + WS_AF), (short)0, 128 * KS_TOT * 1024, 0x00020000);
#pragma unroll
    for (int p = 0; p < 4; ++p) {
        const f32x4 a = o[2 * p] * f, b = o[2 * p + 1] * f;
        __builtin_amdgcn_raw_buffer_store_b128((u32x4){cvtpk2(a[0], a[1]), cvtpk2(a[2], a[3]), cvtpk2(b[0], b[1]), cvtpk2(b[2], b[3])}, srdA, lane * 16u + p * 1024u, ((r0 >> 4) * KS_TOT + 18u + 4u * h) * 1024u, 16);
    }
}
}

#define ATT_ASM \
"s_branch L_ago_%=\n" \
".p2align 12\n" \
"L_ab:\n" \
".fill (L_ab + 0 - .) / 4, 4, 0xBF800000\n" \
"L_ap0:\n" \
"s_setpc_b64 s[88:89]\n" \
"L_ago_%=:\n" \
"s_mov_b32 s101, 0\n" \
"s_waitcnt vmcnt(15) lgkmcnt(0)\n" \
"s_barrier\n" \
"v_accvgpr_write_b32 a216, 0\n" \
"v_accvgpr_write_b32 a217, 0\n" \
"v_accvgpr_write_b32 a218, 0\n" \
"v_accvgpr_write_b32 a219, 0\n" \
"v_bfe_u32 v61, v20, 2, 5\n" \
"s_mov_b32 s96, s24\n" \
"v_mfma_f32_32x32x16_bf16 v[128:143], a[216:219], a[216:219], 0\n" \
"buffer_load_dword v57, v61, s[56:59], s96 offen sc1\n" \
"v_and_b32_e32 v63, 0x300, v20\n" \
"ds_read_b128 v[96:99], v63 offset:0\n" \
"v_add_u32_e32 v58, s22, v20\n" \
"ds_read_b128 a[0:3], v58 offset:0\n" \
"v_mfma_f32_32x32x16_bf16 v[144:159], a[216:219], a[216:219], 0\n" \
"ds_read_b128 a[16:19], v58 offset:8192\n" \
"v_add_u32_e32 v59, s23, v20\n" \
"ds_read_b128 a[32:35], v59 offset:0\n" \
"ds_read_b128 a[48:51], v59 offset:8192\n" \
"ds_read_b128 v[100:103], v63 offset:1024\n" \
"ds_read_b128 a[4:7], v58 offset:1024\n" \
"v_mfma_f32_32x32x16_bf16 v[160:175], a[216:219], a[216:219], 0\n" \
"ds_read_b128 a[20:23], v58 offset:9216\n" \
"ds_read_b128 a[36:39], v59 offset:1024\n" \
"ds_read_b128 a[52:55], v59 offset:9216\n" \
"s_sub_u32 s67, s12, 0xc000\n" \
"ds_read_b128 v[104:107], v63 offset:2048\n" \
"s_mov_b32 s65, s12\n" \
"v_mfma_f32_32x32x16_bf16 v[176:191], a[216:219], a[216:219], 0\n" \
"v_add_u32_e32 v55, s67, v20\n" \
"v_add_u32_e32 v52, 0xc000, v55\n" \
"s_xor_b32 m0, s65, 0x10000\n" \
"ds_read_b128 a[8:11], v58 offset:2048\n" \
"buffer_load_dwordx4 v52, s[8:11], 0 offen offset:0 sc1 lds\n" \
"v_mfma_f32_32x32x16_bf16 v[192:207], a[216:219], a[216:219], 0\n" \
"ds_read_b128 a[24:27], v58 offset:10240\n" \
"ds_read_b128 a[40:43], v59 offset:2048\n" \
"ds_read_b128 a[56:59], v59 offset:10240\n" \
"ds_read_b128 v[108:111], v63 offset:3072\n" \
"buffer_load_dwordx4 v52, s[8:11], 0 offen offset:1024 sc1 lds\n" \
"v_mfma_f32_32x32x16_bf16 v[208:223], a[216:219], a[216:219], 0\n" \
"ds_read_b128 a[12:15], v58 offset:3072\n" \
"ds_read_b128 a[28:31], v58 offset:11264\n" \
"ds_read_b128 a[44:47], v59 offset:3072\n" \
"ds_read_b128 a[60:63], v59 offset:11264\n" \
"s_waitcnt lgkmcnt(15)\n" \
"v_xor_b32_e32 v96, 0x80008000, v96\n" \
"v_mfma_f32_32x32x16_bf16 v[224:239], a[216:219], a[216:219], 0\n" \
"v_xor_b32_e32 v97, 0x80008000, v97\n" \
"v_xor_b32_e32 v98, 0x80008000, v98\n" \
"v_xor_b32_e32 v99, 0x80008000, v99\n" \
"ds_read_b128 a[160:163], v20 offset:0\n" \
"ds_read_b128 a[164:167], v20 offset:1024\n" \
"s_waitcnt lgkmcnt(15)\n" \
"v_xor_b32_e32 v100, 0x80008000, v100\n" \
"v_mfma_f32_32x32x16_bf16 v[240:255], a[216:219], a[216:219], 0\n" \
"v_xor_b32_e32 v101, 0x80008000, v101\n" \
"v_xor_b32_e32 v102, 0x80008000, v102\n" \
"v_xor_b32_e32 v103, 0x80008000, v103\n" \
"ds_read_b128 a[168:171], v20 offset:2048\n" \
"ds_read_b128 a[172:175], v20 offset:3072\n" \
"ds_read_b128 a[188:191], v20 offset:7168\n" \
"v_mfma_f32_16x16x32_bf16 a[192:195], a[216:219], a[216:219], 0\n" \
"ds_read_b128 a[184:187], v20 offset:6144\n" \
"s_waitcnt lgkmcnt(15)\n" \
"v_xor_b32_e32 v104, 0x80008000, v104\n" \
"v_mfma_f32_16x16x32_bf16 a[196:199], a[216:219], a[216:219], 0\n" \
"v_xor_b32_e32 v105, 0x80008000, v105\n" \
"v_xor_b32_e32 v106, 0x80008000, v106\n" \
"v_mfma_f32_16x16x32_bf16 v[32:35], v[96:99], a[0:3], 0\n" \
"v_xor_b32_e32 v107, 0x80008000, v107\n" \
"ds_read_b128 a[180:183], v20 offset:5120\n" \
"v_mfma_f32_16x16x32_bf16 v[36:39], v[96:99], a[16:19], 0\n" \
"ds_read_b128 a[176:179], v20 offset:4096\n" \
"s_waitcnt lgkmcnt(12)\n" \
"v_xor_b32_e32 v108, 0x80008000, v108\n" \
"v_mfma_f32_16x16x32_bf16 v[40:43], v[96:99], a[32:35], 0\n" \
"v_xor_b32_e32 v109, 0x80008000, v109\n" \
"v_xor_b32_e32 v110, 0x80008000, v110\n" \
"v_mfma_f32_16x16x32_bf16 v[44:47], v[96:99], a[48:51], 0\n" \
"v_xor_b32_e32 v111, 0x80008000, v111\n" \
"v_add_u32_e32 v60, s15, v20\n" \
"v_mfma_f32_16x16x32_bf16 v[32:35], v[100:103], a[4:7], v[32:35]\n" \
"v_add_u32_e32 v60, 0x10000, v60\n" \
"ds_read_b128 a[128:131], v20 offset:8192\n" \
"v_mfma_f32_16x16x32_bf16 v[36:39], v[100:103], a[20:23], v[36:39]\n" \
"ds_read_b128 a[144:147], v20 offset:12288\n" \
"ds_read_b128 a[132:135], v20 offset:9216\n" \
"v_mfma_f32_16x16x32_bf16 v[40:43], v[100:103], a[36:39], v[40:43]\n" \
"ds_read_b128 a[148:151], v20 offset:13312\n" \
"ds_read_b128 a[136:139], v20 offset:10240\n" \
"v_mfma_f32_16x16x32_bf16 v[44:47], v[100:103], a[52:55], v[44:47]\n" \
"ds_read_b128 a[152:155], v20 offset:14336\n" \
"ds_read_b128 a[140:143], v20 offset:11264\n" \
"v_mfma_f32_16x16x32_bf16 v[32:35], v[104:107], a[8:11], v[32:35]\n" \
"ds_read_b128 a[156:159], v20 offset:15360\n" \
"ds_read_b128 a[64:67], v60 offset:16384\n" \
"v_mfma_f32_16x16x32_bf16 v[36:39], v[104:107], a[24:27], v[36:39]\n" \
"ds_read_b128 a[68:71], v60 offset:17408\n" \
"ds_read_b128 a[72:75], v60 offset:18432\n" \
"v_mfma_f32_16x16x32_bf16 v[40:43], v[104:107], a[40:43], v[40:43]\n" \
"ds_read_b128 a[76:79], v60 offset:19456\n" \
"ds_read_b128 a[80:83], v60 offset:24576\n" \
"v_mfma_f32_16x16x32_bf16 v[44:47], v[104:107], a[56:59], v[44:47]\n" \
"ds_read_b128 a[84:87], v60 offset:25600\n" \
"s_waitcnt lgkmcnt(15)\n" \
"v_mfma_f32_16x16x32_bf16 v[32:35], v[108:111], a[12:15], v[32:35]\n" \
"ds_read_b128 a[88:91], v60 offset:26624\n" \
"ds_read_b128 a[92:95], v60 offset:27648\n" \
"v_mfma_f32_16x16x32_bf16 v[36:39], v[108:111], a[28:31], v[36:39]\n" \
"ds_read_b128 a[96:99], v60 offset:0\n" \
"ds_read_b128 a[100:103], v60 offset:1024\n" \
"v_mfma_f32_16x16x32_bf16 v[40:43], v[108:111], a[44:47], v[40:43]\n" \
"ds_read_b128 a[104:107], v60 offset:2048\n" \
"ds_read_b128 a[108:111], v60 offset:3072\n" \
"v_mfma_f32_16x16x32_bf16 v[44:47], v[108:111], a[60:63], v[44:47]\n" \
"ds_read_b128 a[112:115], v60 offset:8192\n" \
"ds_read_b128 a[116:119], v60 offset:9216\n" \
"v_mfma_f32_16x16x32_bf16 v[64:67], a[160:163], a[0:3], v[32:35]\n" \
"ds_read_b128 a[120:123], v60 offset:10240\n" \
"ds_read_b128 a[124:127], v60 offset:11264\n" \
"v_mfma_f32_16x16x32_bf16 v[64:67], a[164:167], a[4:7], v[64:67]\n" \
"v_mov_b32_e32 v56, s62\n" \
"v_readlane_b32 s68, v11, 0\n" \
"v_mfma_f32_16x16x32_bf16 v[64:67], a[168:171], a[8:11], v[64:67]\n" \
"v_bfe_u32 v26, v20, 8, 2\n" \
"s_mov_b32 m0, s20\n" \
"v_mfma_f32_16x16x32_bf16 v[64:67], a[172:175], a[12:15], v[64:67]\n" \
"v_bfe_u32 v25, v20, 5, 3\n" \
"v_lshlrev_b32_e32 v26, 1, v26\n" \
"v_mfma_f32_16x16x32_bf16 v[68:71], a[188:191], a[12:15], v[32:35]\n" \
"global_load_lds_dword v[16:17], off\n" \
"v_mfma_f32_16x16x32_bf16 v[68:71], a[184:187], a[8:11], v[68:71]\n" \
"v_sub_u32_e32 v25, v25, v26\n" \
"v_and_b32_e32 v26, 16, v20\n" \
"v_mfma_f32_16x16x32_bf16 v[68:71], a[180:183], a[4:7], v[68:71]\n" \
"v_mov_b32_e32 v27, 0x3f80\n" \
"s_waitcnt lgkmcnt(15)\n" \
"v_mfma_f32_16x16x32_bf16 v[68:71], a[176:179], a[0:3], v[68:71]\n" \
"v_lshlrev_b32_e32 v26, v26, v27\n" \
"v_mov_b32_e32 v54, 0x3f803f80\n" \
"v_mfma_f32_16x16x32_bf16 v[76:79], a[176:179], a[16:19], v[36:39]\n" \
"s_mov_b32 s84, 0x33333333\n" \
"s_mov_b32 s85, 0xcccccccc\n" \
"v_mfma_f32_16x16x32_bf16 v[76:79], a[180:183], a[20:23], v[76:79]\n" \
"s_mov_b32 s86, 0x11111111\n" \
"s_mov_b32 s87, 0x11111111\n" \
"v_mfma_f32_16x16x32_bf16 v[76:79], a[184:187], a[24:27], v[76:79]\n" \
"v_exp_f32_e32 v70, v70\n" \
"v_mfma_f32_16x16x32_bf16 v[76:79], a[188:191], a[28:31], v[76:79]\n" \
"v_exp_f32_e32 v71, v71\n" \
"v_mfma_f32_16x16x32_bf16 v[72:75], a[172:175], a[28:31], v[36:39]\n" \
"v_exp_f32_e32 v68, v68\n" \
"v_mfma_f32_16x16x32_bf16 v[72:75], a[168:171], a[24:27], v[72:75]\n" \
"v_exp_f32_e32 v69, v69\n" \
"v_mfma_f32_16x16x32_bf16 v[72:75], a[164:167], a[20:23], v[72:75]\n" \
"s_mov_b32 s88, 0x44444444\n" \
"s_mov_b32 s89, 0x44444444\n" \
"v_mfma_f32_16x16x32_bf16 v[72:75], a[160:163], a[16:19], v[72:75]\n" \
"v_bfe_i32 v21, v20, 4, 1\n" \
"v_cvt_pk_bf16_f32 v71, v70, v71\n" \
"v_mfma_f32_16x16x32_bf16 v[80:83], a[160:163], a[32:35], v[40:43]\n" \
"v_exp_f32_e32 v66, v66\n" \
"v_mfma_f32_16x16x32_bf16 v[80:83], a[164:167], a[36:39], v[80:83]\n" \
"v_exp_f32_e32 v67, v67\n" \
"v_mfma_f32_16x16x32_bf16 v[80:83], a[168:171], a[40:43], v[80:83]\n" \
"v_exp_f32_e32 v64, v64\n" \
"v_mfma_f32_16x16x32_bf16 v[80:83], a[172:175], a[44:47], v[80:83]\n" \
"v_exp_f32_e32 v72, v72\n" \
"v_mfma_f32_16x16x32_bf16 v[84:87], a[188:191], a[44:47], v[40:43]\n" \
"s_waitcnt vmcnt(3)\n" \
"v_cmp_ne_u32_e32 vcc, s62, v57\n" \
"v_mfma_f32_16x16x32_bf16 v[84:87], a[184:187], a[40:43], v[84:87]\n" \
"v_exp_f32_e32 v73, v73\n" \
"v_mfma_f32_16x16x32_bf16 v[84:87], a[180:183], a[36:39], v[84:87]\n" \
"v_exp_f32_e32 v74, v74\n" \
"v_mfma_f32_16x16x32_bf16 v[84:87], a[176:179], a[32:35], v[84:87]\n" \
"s_cbranch_vccnz L_skb_%=\n" \
"L_rkb_%=:\n" \
"v_cmp_eq_u32_e32 vcc, 0, v25\n" \
"s_mov_b64 exec, 1\n" \
"buffer_store_dword v56, off, s[56:59], s68 offset:0 sc1\n" \
"s_mov_b64 exec, -1\n" \
"v_mfma_f32_16x16x32_bf16 v[92:95], a[176:179], a[48:51], v[44:47]\n" \
"v_exp_f32_e32 v75, v75\n" \
"v_mfma_f32_16x16x32_bf16 v[92:95], a[180:183], a[52:55], v[92:95]\n" \
"v_cndmask_b32_e32 v28, 0, v26, vcc\n" \
"v_cvt_pk_bf16_f32 v72, v72, v73\n" \
"v_mfma_f32_16x16x32_bf16 v[92:95], a[184:187], a[56:59], v[92:95]\n" \
"v_exp_f32_e32 v65, v65\n" \
"v_mfma_f32_16x16x32_bf16 v[92:95], a[188:191], a[60:63], v[92:95]\n" \
"v_exp_f32_e32 v76, v76\n" \
"v_mfma_f32_16x16x32_bf16 v[88:91], a[172:175], a[60:63], v[44:47]\n" \
"v_exp_f32_e32 v77, v77\n" \
"v_mfma_f32_16x16x32_bf16 v[88:91], a[168:171], a[56:59], v[88:91]\n" \
"v_exp_f32_e32 v78, v78\n" \
"v_mfma_f32_16x16x32_bf16 v[88:91], a[164:167], a[52:55], v[88:91]\n" \
"v_exp_f32_e32 v79, v79\n" \
"v_mfma_f32_16x16x32_bf16 v[88:91], a[160:163], a[48:51], v[88:91]\n" \
"v_cndmask_b32_e64 v22, 0, v54, s[84:85]\n" \
"v_cndmask_b32_e64 v23, 0, v54, s[86:87]\n" \
"s_waitcnt vmcnt(5) lgkmcnt(0)\n" \
"s_barrier\n" \
"v_cndmask_b32_e64 v24, 0, v54, s[88:89]\n" \
"v_and_b32_e32 v21, 0xaaaaaaaa, v21\n" \
"v_cmp_eq_u32_e32 vcc, 1, v25\n" \
"v_cvt_pk_bf16_f32 v70, v68, v69\n" \
"v_cvt_pk_bf16_f32 v73, v74, v75\n" \
"v_mov_b32_e32 v62, 0\n" \
"v_accvgpr_write_b32 a208, 0\n" \
"v_accvgpr_write_b32 a209, 0\n" \
"v_accvgpr_write_b32 a210, 0\n" \
"v_accvgpr_write_b32 a211, 0\n" \
"v_accvgpr_write_b32 a200, v22\n" \
"v_accvgpr_write_b32 a201, v22\n" \
"v_accvgpr_write_b32 a202, v22\n" \
"v_accvgpr_write_b32 a203, v22\n" \
"v_accvgpr_write_b32 a204, v23\n" \
"v_accvgpr_write_b32 a205, v23\n" \
"v_accvgpr_write_b32 a206, v23\n" \
"v_accvgpr_write_b32 a207, v23\n" \
"v_accvgpr_write_b32 a212, v24\n" \
"v_accvgpr_write_b32 a213, v24\n" \
"v_accvgpr_write_b32 a214, v24\n" \
"v_accvgpr_write_b32 a215, v24\n" \
"v_or_b32_e32 v21, 0x44444444, v21\n" \
"s_mov_b32 s64, 0x8000\n" \
"v_add_u32_e32 v50, 0x6000, v55\n" \
"v_add_u32_e32 v51, 0xa000, v55\n" \
"v_add_u32_e32 v53, 0x10000, v55\n" \
"v_add_u32_e32 v48, 0xc000, v20\n" \
"v_add_u32_e32 v49, s17, v20\n" \
"v_cndmask_b32_e32 v29, 0, v26, vcc\n" \
"v_accvgpr_write_b32 a220, v96\n" \
"v_accvgpr_write_b32 a221, v97\n" \
"v_accvgpr_write_b32 a222, v98\n" \
"v_accvgpr_write_b32 a223, v99\n" \
"v_accvgpr_write_b32 a224, v100\n" \
"v_accvgpr_write_b32 a225, v101\n" \
"v_accvgpr_write_b32 a226, v102\n" \
"v_accvgpr_write_b32 a227, v103\n" \
"v_accvgpr_write_b32 a228, v104\n" \
"v_accvgpr_write_b32 a229, v105\n" \
"v_accvgpr_write_b32 a230, v106\n" \
"v_accvgpr_write_b32 a231, v107\n" \
"v_accvgpr_write_b32 a232, v108\n" \
"v_accvgpr_write_b32 a233, v109\n" \
"v_accvgpr_write_b32 a234, v110\n" \
"v_accvgpr_write_b32 a235, v111\n" \
"v_cvt_pk_bf16_f32 v69, v66, v67\n" \
"v_cvt_pk_bf16_f32 v68, v64, v65\n" \
"v_cvt_pk_bf16_f32 v74, v76, v77\n" \
"v_cvt_pk_bf16_f32 v75, v78, v79\n" \
"s_sub_u32 s66, s18, 1\n" \
"s_cbranch_scc1 L_tr_%=\n" \
"s_branch L_1_%=\n" \
".fill (L_ab + 1840 - .) / 4, 4, 0xBF800000\n" \
"L_tL_1_%=:\n" \
"s_waitcnt vmcnt(4) lgkmcnt(0)\n" \
"v_smfmac_f32_16x16x64_bf16 a[196:199], a[200:203], v[116:123], v21\n" \
"s_barrier\n" \
"L_1_%=:\n" \
"v_mfma_f32_16x16x32_bf16 v[96:99], a[128:131], a[0:3], v[32:35]\n" \
"s_xor_b32 m0, s65, 0x12000\n" \
"ds_read_b128 a[160:163], v49 offset:0\n" \
"v_mfma_f32_16x16x32_bf16 v[96:99], a[132:135], a[4:7], v[96:99]\n" \
"buffer_load_dwordx4 v50, s[8:11], s64 offen offset:0 sc1 lds\n" \
"v_mfma_f32_16x16x32_bf16 v[96:99], a[136:139], a[8:11], v[96:99]\n" \
"ds_read_b128 a[164:167], v49 offset:1024\n" \
"ds_read_b128 a[168:171], v49 offset:2048\n" \
"v_mfma_f32_16x16x32_bf16 v[96:99], a[140:143], a[12:15], v[96:99]\n" \
"ds_read_b128 a[172:175], v49 offset:3072\n" \
"ds_read_b128 a[176:179], v49 offset:4096\n" \
"v_mfma_f32_16x16x32_bf16 v[100:103], a[156:159], a[12:15], v[32:35]\n" \
"ds_read_b128 a[180:183], v49 offset:5120\n" \
"ds_read_b128 a[184:187], v49 offset:6144\n" \
"v_mfma_f32_16x16x32_bf16 v[100:103], a[152:155], a[8:11], v[100:103]\n" \
"v_exp_f32_e64 v86, v86\n" \
"v_mfma_f32_16x16x32_bf16 v[100:103], a[148:151], a[4:7], v[100:103]\n" \
"v_exp_f32_e64 v87, v87\n" \
"v_mfma_f32_16x16x32_bf16 v[100:103], a[144:147], a[0:3], v[100:103]\n" \
"ds_read_b128 a[188:191], v49 offset:7168\n" \
"v_cvt_pk_bf16_f32 v87, v86, v87\n" \
"v_mfma_f32_16x16x32_bf16 v[108:111], a[144:147], a[16:19], v[36:39]\n" \
"v_exp_f32_e64 v84, v84\n" \
"v_mfma_f32_16x16x32_bf16 v[108:111], a[148:151], a[20:23], v[108:111]\n" \
"v_exp_f32_e64 v85, v85\n" \
"v_mfma_f32_16x16x32_bf16 v[108:111], a[152:155], a[24:27], v[108:111]\n" \
"v_exp_f32_e64 v82, v82\n" \
"v_mfma_f32_16x16x32_bf16 v[108:111], a[156:159], a[28:31], v[108:111]\n" \
"v_exp_f32_e64 v83, v83\n" \
"v_mfma_f32_16x16x32_bf16 v[104:107], a[140:143], a[28:31], v[36:39]\n" \
"v_exp_f32_e64 v80, v80\n" \
"v_mfma_f32_16x16x32_bf16 v[104:107], a[136:139], a[24:27], v[104:107]\n" \
"v_exp_f32_e64 v81, v81\n" \
"v_mfma_f32_16x16x32_bf16 v[104:107], a[132:135], a[20:23], v[104:107]\n" \
"v_exp_f32_e64 v88, v88\n" \
"v_mfma_f32_16x16x32_bf16 v[104:107], a[128:131], a[16:19], v[104:107]\n" \
"buffer_load_dwordx4 v50, s[8:11], s64 offen offset:1024 sc1 lds\n" \
"v_mfma_f32_16x16x32_bf16 v[112:115], a[128:131], a[32:35], v[40:43]\n" \
"s_xor_b32 m0, s65, 0x6000\n" \
"v_cvt_pk_bf16_f32 v86, v84, v85\n" \
"v_mfma_f32_16x16x32_bf16 v[112:115], a[132:135], a[36:39], v[112:115]\n" \
"v_cvt_pk_bf16_f32 v85, v82, v83\n" \
"v_cvt_pk_bf16_f32 v84, v80, v81\n" \
"v_mfma_f32_16x16x32_bf16 v[112:115], a[136:139], a[40:43], v[112:115]\n" \
"v_exp_f32_e64 v89, v89\n" \
"v_mfma_f32_16x16x32_bf16 v[112:115], a[140:143], a[44:47], v[112:115]\n" \
"v_exp_f32_e64 v90, v90\n" \
"v_mfma_f32_16x16x32_bf16 v[116:119], a[156:159], a[44:47], v[40:43]\n" \
"v_exp_f32_e64 v91, v91\n" \
"v_mfma_f32_16x16x32_bf16 v[116:119], a[152:155], a[40:43], v[116:119]\n" \
"v_exp_f32_e64 v92, v92\n" \
"v_mfma_f32_16x16x32_bf16 v[116:119], a[148:151], a[36:39], v[116:119]\n" \
"v_exp_f32_e64 v93, v93\n" \
"v_mfma_f32_16x16x32_bf16 v[116:119], a[144:147], a[32:35], v[116:119]\n" \
"v_exp_f32_e64 v94, v94\n" \
"v_mfma_f32_16x16x32_bf16 v[124:127], a[144:147], a[48:51], v[44:47]\n" \
"v_cvt_pk_bf16_f32 v88, v88, v89\n" \
"v_cvt_pk_bf16_f32 v89, v90, v91\n" \
"v_mfma_f32_16x16x32_bf16 v[124:127], a[148:151], a[52:55], v[124:127]\n" \
"v_exp_f32_e64 v95, v95\n" \
"v_mfma_f32_16x16x32_bf16 v[124:127], a[152:155], a[56:59], v[124:127]\n" \
"v_cvt_pk_bf16_f32 v90, v92, v93\n" \
"v_cvt_pk_bf16_f32 v91, v94, v95\n" \
"v_mfma_f32_16x16x32_bf16 v[124:127], a[156:159], a[60:63], v[124:127]\n" \
"ds_read_b128 a[144:147], v48 offset:4096\n" \
"ds_read_b128 a[148:151], v48 offset:5120\n" \
"v_mfma_f32_16x16x32_bf16 v[120:123], a[140:143], a[60:63], v[44:47]\n" \
"ds_read_b128 a[152:155], v48 offset:6144\n" \
"ds_read_b128 a[140:143], v48 offset:3072\n" \
"v_mfma_f32_16x16x32_bf16 v[120:123], a[136:139], a[56:59], v[120:123]\n" \
"ds_read_b128 a[136:139], v48 offset:2048\n" \
"ds_read_b128 a[156:159], v48 offset:7168\n" \
"v_mfma_f32_16x16x32_bf16 v[120:123], a[132:135], a[52:55], v[120:123]\n" \
"buffer_load_dwordx4 v51, s[8:11], s64 offen offset:0 sc1 lds\n" \
"v_mfma_f32_16x16x32_bf16 v[120:123], a[128:131], a[48:51], v[120:123]\n" \
"ds_read_b128 a[128:131], v48 offset:0\n" \
"s_waitcnt lgkmcnt(7)\n" \
"v_mfma_f32_16x16x32_bf16 v[128:131], a[160:163], v[68:71], v[128:131]\n" \
"v_exp_f32_e32 v102, v102\n" \
"v_mfma_f32_16x16x32_bf16 v[160:163], a[160:163], v[72:75], v[160:163]\n" \
"v_exp_f32_e64 v103, v103\n" \
"v_mfma_f32_16x16x32_bf16 v[132:135], a[164:167], v[68:71], v[132:135]\n" \
"ds_read_b128 a[132:135], v48 offset:1024\n" \
"v_cvt_pk_bf16_f32 v103, v102, v103\n" \
"v_mfma_f32_16x16x32_bf16 v[164:167], a[164:167], v[72:75], v[164:167]\n" \
"v_exp_f32_e64 v100, v100\n" \
"v_mfma_f32_16x16x32_bf16 v[136:139], a[168:171], v[68:71], v[136:139]\n" \
"v_exp_f32_e64 v101, v101\n" \
"v_mfma_f32_16x16x32_bf16 v[168:171], a[168:171], v[72:75], v[168:171]\n" \
"v_exp_f32_e64 v98, v98\n" \
"v_mfma_f32_16x16x32_bf16 v[140:143], a[172:175], v[68:71], v[140:143]\n" \
"v_exp_f32_e64 v99, v99\n" \
"v_mfma_f32_16x16x32_bf16 v[172:175], a[172:175], v[72:75], v[172:175]\n" \
"v_exp_f32_e64 v96, v96\n" \
"v_mfma_f32_16x16x32_bf16 v[144:147], a[176:179], v[68:71], v[144:147]\n" \
"v_exp_f32_e64 v97, v97\n" \
"v_mfma_f32_16x16x32_bf16 v[176:179], a[176:179], v[72:75], v[176:179]\n" \
"v_exp_f32_e64 v104, v104\n" \
"v_mfma_f32_16x16x32_bf16 v[148:151], a[180:183], v[68:71], v[148:151]\n" \
"v_exp_f32_e64 v105, v105\n" \
"v_mfma_f32_16x16x32_bf16 v[180:183], a[180:183], v[72:75], v[180:183]\n" \
"v_cvt_pk_bf16_f32 v102, v100, v101\n" \
"v_cvt_pk_bf16_f32 v101, v98, v99\n" \
"v_mfma_f32_16x16x32_bf16 v[152:155], a[184:187], v[68:71], v[152:155]\n" \
"v_cvt_pk_bf16_f32 v100, v96, v97\n" \
"v_cvt_pk_bf16_f32 v104, v104, v105\n" \
"v_mfma_f32_16x16x32_bf16 v[184:187], a[184:187], v[72:75], v[184:187]\n" \
"buffer_load_dwordx4 v51, s[8:11], s64 offen offset:1024 sc1 lds\n" \
"v_mfma_f32_16x16x32_bf16 v[156:159], a[188:191], v[68:71], v[156:159]\n" \
"v_exp_f32_e64 v106, v106\n" \
"v_mfma_f32_16x16x32_bf16 v[188:191], a[188:191], v[72:75], v[188:191]\n" \
"v_exp_f32_e64 v107, v107\n" \
"v_smfmac_f32_16x16x64_bf16 a[192:195], a[200:203], v[68:75], v21\n" \
"v_exp_f32_e64 v108, v108\n" \
"v_mfma_f32_16x16x32_bf16 v[220:223], a[188:191], v[84:87], v[220:223]\n" \
"v_exp_f32_e64 v109, v109\n" \
"v_mfma_f32_16x16x32_bf16 v[252:255], a[188:191], v[88:91], v[252:255]\n" \
"v_exp_f32_e64 v110, v110\n" \
"v_mfma_f32_16x16x32_bf16 v[216:219], a[184:187], v[84:87], v[216:219]\n" \
"v_exp_f32_e64 v111, v111\n" \
"v_mfma_f32_16x16x32_bf16 v[248:251], a[184:187], v[88:91], v[248:251]\n" \
"v_exp_f32_e64 v118, v118\n" \
"v_mfma_f32_16x16x32_bf16 v[212:215], a[180:183], v[84:87], v[212:215]\n" \
"v_exp_f32_e64 v119, v119\n" \
"v_mfma_f32_16x16x32_bf16 v[244:247], a[180:183], v[88:91], v[244:247]\n" \
"v_cvt_pk_bf16_f32 v105, v106, v107\n" \
"v_cvt_pk_bf16_f32 v106, v108, v109\n" \
"v_mfma_f32_16x16x32_bf16 v[208:211], a[176:179], v[84:87], v[208:211]\n" \
"v_cvt_pk_bf16_f32 v107, v110, v111\n" \
"v_cvt_pk_bf16_f32 v119, v118, v119\n" \
"v_mfma_f32_16x16x32_bf16 v[240:243], a[176:179], v[88:91], v[240:243]\n" \
"v_exp_f32_e64 v116, v116\n" \
"v_mfma_f32_16x16x32_bf16 v[204:207], a[172:175], v[84:87], v[204:207]\n" \
"v_exp_f32_e64 v117, v117\n" \
"v_mfma_f32_16x16x32_bf16 v[236:239], a[172:175], v[88:91], v[236:239]\n" \
"v_exp_f32_e64 v114, v114\n" \
"v_mfma_f32_16x16x32_bf16 v[200:203], a[168:171], v[84:87], v[200:203]\n" \
"v_exp_f32_e64 v115, v115\n" \
"v_mfma_f32_16x16x32_bf16 v[232:235], a[168:171], v[88:91], v[232:235]\n" \
"v_exp_f32_e64 v112, v112\n" \
"v_mfma_f32_16x16x32_bf16 v[196:199], a[164:167], v[84:87], v[196:199]\n" \
"v_exp_f32_e64 v113, v113\n" \
"v_mfma_f32_16x16x32_bf16 v[228:231], a[164:167], v[88:91], v[228:231]\n" \
"v_exp_f32_e64 v120, v120\n" \
"v_mfma_f32_16x16x32_bf16 v[192:195], a[160:163], v[84:87], v[192:195]\n" \
"v_exp_f32_e64 v121, v121\n" \
"v_mfma_f32_16x16x32_bf16 v[224:227], a[160:163], v[88:91], v[224:227]\n" \
"v_cvt_pk_bf16_f32 v118, v116, v117\n" \
"s_waitcnt vmcnt(4) lgkmcnt(0)\n" \
"v_smfmac_f32_16x16x64_bf16 a[196:199], a[200:203], v[84:91], v21\n" \
"s_barrier\n" \
"v_mfma_f32_16x16x32_bf16 v[64:67], a[128:131], a[0:3], v[32:35]\n" \
"s_xor_b32 m0, s65, 0x14000\n" \
"ds_read_b128 a[160:163], v49 offset:8192\n" \
"v_mfma_f32_16x16x32_bf16 v[64:67], a[132:135], a[4:7], v[64:67]\n" \
"buffer_load_dwordx4 v52, s[8:11], s64 offen offset:0 sc1 lds\n" \
"v_mfma_f32_16x16x32_bf16 v[64:67], a[136:139], a[8:11], v[64:67]\n" \
"ds_read_b128 a[164:167], v49 offset:9216\n" \
"ds_read_b128 a[168:171], v49 offset:10240\n" \
"v_mfma_f32_16x16x32_bf16 v[64:67], a[140:143], a[12:15], v[64:67]\n" \
"ds_read_b128 a[172:175], v49 offset:11264\n" \
"ds_read_b128 a[176:179], v49 offset:12288\n" \
"v_mfma_f32_16x16x32_bf16 v[68:71], a[156:159], a[12:15], v[32:35]\n" \
"ds_read_b128 a[180:183], v49 offset:13312\n" \
"ds_read_b128 a[184:187], v49 offset:14336\n" \
"v_mfma_f32_16x16x32_bf16 v[68:71], a[152:155], a[8:11], v[68:71]\n" \
"ds_read_b128 a[188:191], v49 offset:15360\n" \
"v_cvt_pk_bf16_f32 v117, v114, v115\n" \
"v_mfma_f32_16x16x32_bf16 v[68:71], a[148:151], a[4:7], v[68:71]\n" \
"v_cvt_pk_bf16_f32 v116, v112, v113\n" \
"v_cvt_pk_bf16_f32 v120, v120, v121\n" \
"v_mfma_f32_16x16x32_bf16 v[68:71], a[144:147], a[0:3], v[68:71]\n" \
"v_exp_f32_e64 v122, v122\n" \
"v_mfma_f32_16x16x32_bf16 v[76:79], a[144:147], a[16:19], v[36:39]\n" \
"v_exp_f32_e64 v123, v123\n" \
"v_mfma_f32_16x16x32_bf16 v[76:79], a[148:151], a[20:23], v[76:79]\n" \
"v_exp_f32_e64 v124, v124\n" \
"v_mfma_f32_16x16x32_bf16 v[76:79], a[152:155], a[24:27], v[76:79]\n" \
"v_exp_f32_e64 v125, v125\n" \
"v_mfma_f32_16x16x32_bf16 v[76:79], a[156:159], a[28:31], v[76:79]\n" \
"v_exp_f32_e64 v126, v126\n" \
"v_mfma_f32_16x16x32_bf16 v[72:75], a[140:143], a[28:31], v[36:39]\n" \
"v_exp_f32_e64 v127, v127\n" \
"v_mfma_f32_16x16x32_bf16 v[72:75], a[136:139], a[24:27], v[72:75]\n" \
"v_cvt_pk_bf16_f32 v121, v122, v123\n" \
"v_cvt_pk_bf16_f32 v122, v124, v125\n" \
"v_mfma_f32_16x16x32_bf16 v[72:75], a[132:135], a[20:23], v[72:75]\n" \
"v_cvt_pk_bf16_f32 v123, v126, v127\n" \
"v_add_u32_e32 v49, 0x10000, v48\n" \
"v_mfma_f32_16x16x32_bf16 v[72:75], a[128:131], a[16:19], v[72:75]\n" \
"buffer_load_dwordx4 v52, s[8:11], s64 offen offset:1024 sc1 lds\n" \
"v_mfma_f32_16x16x32_bf16 v[80:83], a[128:131], a[32:35], v[40:43]\n" \
"v_exp_f32_e64 v70, v70\n" \
"v_mfma_f32_16x16x32_bf16 v[80:83], a[132:135], a[36:39], v[80:83]\n" \
"v_exp_f32_e64 v71, v71\n" \
"v_mfma_f32_16x16x32_bf16 v[80:83], a[136:139], a[40:43], v[80:83]\n" \
"s_mov_b32 m0, s65\n" \
"v_cvt_pk_bf16_f32 v71, v70, v71\n" \
"v_mfma_f32_16x16x32_bf16 v[80:83], a[140:143], a[44:47], v[80:83]\n" \
"v_exp_f32_e32 v72, v72\n" \
"v_mfma_f32_16x16x32_bf16 v[84:87], a[156:159], a[44:47], v[40:43]\n" \
"v_exp_f32_e64 v73, v73\n" \
"v_mfma_f32_16x16x32_bf16 v[84:87], a[152:155], a[40:43], v[84:87]\n" \
"v_exp_f32_e64 v68, v68\n" \
"v_mfma_f32_16x16x32_bf16 v[84:87], a[148:151], a[36:39], v[84:87]\n" \
"v_exp_f32_e64 v69, v69\n" \
"v_mfma_f32_16x16x32_bf16 v[84:87], a[144:147], a[32:35], v[84:87]\n" \
"v_exp_f32_e64 v74, v74\n" \
"v_mfma_f32_16x16x32_bf16 v[92:95], a[144:147], a[48:51], v[44:47]\n" \
"ds_read_b128 a[144:147], v48 offset:12288\n" \
"v_cvt_pk_bf16_f32 v72, v72, v73\n" \
"v_mfma_f32_16x16x32_bf16 v[92:95], a[148:151], a[52:55], v[92:95]\n" \
"ds_read_b128 a[148:151], v48 offset:13312\n" \
"v_cvt_pk_bf16_f32 v70, v68, v69\n" \
"v_mfma_f32_16x16x32_bf16 v[92:95], a[152:155], a[56:59], v[92:95]\n" \
"ds_read_b128 a[152:155], v48 offset:14336\n" \
"s_xor_b32 s65, s65, 0x4000\n" \
"v_mfma_f32_16x16x32_bf16 v[92:95], a[156:159], a[60:63], v[92:95]\n" \
"v_exp_f32_e64 v75, v75\n" \
"v_mfma_f32_16x16x32_bf16 v[88:91], a[140:143], a[60:63], v[44:47]\n" \
"ds_read_b128 a[140:143], v48 offset:11264\n" \
"ds_read_b128 a[156:159], v48 offset:15360\n" \
"v_mfma_f32_16x16x32_bf16 v[88:91], a[136:139], a[56:59], v[88:91]\n" \
"ds_read_b128 a[136:139], v48 offset:10240\n" \
"v_cvt_pk_bf16_f32 v73, v74, v75\n" \
"v_mfma_f32_16x16x32_bf16 v[88:91], a[132:135], a[52:55], v[88:91]\n" \
"buffer_load_dwordx4 v53, s[8:11], s64 offen offset:0 sc1 lds\n" \
"v_mfma_f32_16x16x32_bf16 v[88:91], a[128:131], a[48:51], v[88:91]\n" \
"ds_read_b128 a[128:131], v48 offset:8192\n" \
"s_waitcnt lgkmcnt(7)\n" \
"v_mfma_f32_16x16x32_bf16 v[128:131], a[160:163], v[100:103], v[128:131]\n" \
"v_exp_f32_e32 v66, v66\n" \
"v_mfma_f32_16x16x32_bf16 v[160:163], a[160:163], v[104:107], v[160:163]\n" \
"v_exp_f32_e64 v67, v67\n" \
"v_mfma_f32_16x16x32_bf16 v[132:135], a[164:167], v[100:103], v[132:135]\n" \
"ds_read_b128 a[132:135], v48 offset:9216\n" \
"v_cvt_pk_bf16_f32 v69, v66, v67\n" \
"v_mfma_f32_16x16x32_bf16 v[164:167], a[164:167], v[104:107], v[164:167]\n" \
"v_exp_f32_e64 v64, v64\n" \
"v_mfma_f32_16x16x32_bf16 v[136:139], a[168:171], v[100:103], v[136:139]\n" \
"v_exp_f32_e64 v65, v65\n" \
"v_mfma_f32_16x16x32_bf16 v[168:171], a[168:171], v[104:107], v[168:171]\n" \
"v_exp_f32_e64 v76, v76\n" \
"v_mfma_f32_16x16x32_bf16 v[140:143], a[172:175], v[100:103], v[140:143]\n" \
"v_exp_f32_e64 v77, v77\n" \
"v_mfma_f32_16x16x32_bf16 v[172:175], a[172:175], v[104:107], v[172:175]\n" \
"v_exp_f32_e64 v78, v78\n" \
"v_mfma_f32_16x16x32_bf16 v[144:147], a[176:179], v[100:103], v[144:147]\n" \
"v_exp_f32_e64 v79, v79\n" \
"v_mfma_f32_16x16x32_bf16 v[176:179], a[176:179], v[104:107], v[176:179]\n" \
"v_cvt_pk_bf16_f32 v68, v64, v65\n" \
"v_cvt_pk_bf16_f32 v74, v76, v77\n" \
"v_mfma_f32_16x16x32_bf16 v[148:151], a[180:183], v[100:103], v[148:151]\n" \
"v_cvt_pk_bf16_f32 v75, v78, v79\n" \
"v_xor_b32_e32 v48, 0x4000, v48\n" \
"v_mfma_f32_16x16x32_bf16 v[180:183], a[180:183], v[104:107], v[180:183]\n" \
"v_mfma_f32_16x16x32_bf16 v[152:155], a[184:187], v[100:103], v[152:155]\n" \
"v_mfma_f32_16x16x32_bf16 v[184:187], a[184:187], v[104:107], v[184:187]\n" \
"buffer_load_dwordx4 v53, s[8:11], s64 offen offset:1024 sc1 lds\n" \
"v_mfma_f32_16x16x32_bf16 v[156:159], a[188:191], v[100:103], v[156:159]\n" \
"s_add_u32 s64, s64, 0x8000\n" \
"s_sub_u32 s66, s66, 1\n" \
"s_nop 0\n" \
"v_mfma_f32_16x16x32_bf16 v[188:191], a[188:191], v[104:107], v[188:191]\n" \
"v_smfmac_f32_16x16x64_bf16 a[192:195], a[200:203], v[100:107], v21\n" \
"v_mfma_f32_16x16x32_bf16 v[220:223], a[188:191], v[116:119], v[220:223]\n" \
"v_mfma_f32_16x16x32_bf16 v[252:255], a[188:191], v[120:123], v[252:255]\n" \
"v_mfma_f32_16x16x32_bf16 v[216:219], a[184:187], v[116:119], v[216:219]\n" \
"v_mfma_f32_16x16x32_bf16 v[248:251], a[184:187], v[120:123], v[248:251]\n" \
"v_mfma_f32_16x16x32_bf16 v[212:215], a[180:183], v[116:119], v[212:215]\n" \
"v_mfma_f32_16x16x32_bf16 v[244:247], a[180:183], v[120:123], v[244:247]\n" \
"v_mfma_f32_16x16x32_bf16 v[208:211], a[176:179], v[116:119], v[208:211]\n" \
"v_mfma_f32_16x16x32_bf16 v[240:243], a[176:179], v[120:123], v[240:243]\n" \
"v_mfma_f32_16x16x32_bf16 v[204:207], a[172:175], v[116:119], v[204:207]\n" \
"v_mfma_f32_16x16x32_bf16 v[236:239], a[172:175], v[120:123], v[236:239]\n" \
"v_mfma_f32_16x16x32_bf16 v[200:203], a[168:171], v[116:119], v[200:203]\n" \
"v_mfma_f32_16x16x32_bf16 v[232:235], a[168:171], v[120:123], v[232:235]\n" \
"v_mfma_f32_16x16x32_bf16 v[196:199], a[164:167], v[116:119], v[196:199]\n" \
"v_mfma_f32_16x16x32_bf16 v[228:231], a[164:167], v[120:123], v[228:231]\n" \
"v_mfma_f32_16x16x32_bf16 v[192:195], a[160:163], v[116:119], v[192:195]\n" \
"v_mfma_f32_16x16x32_bf16 v[224:227], a[160:163], v[120:123], v[224:227]\n" \
"s_cbranch_scc0 L_tL_1_%=\n" \
"s_waitcnt vmcnt(4) lgkmcnt(0)\n" \
"v_smfmac_f32_16x16x64_bf16 a[196:199], a[200:203], v[116:123], v21\n" \
"s_barrier\n" \
"L_tr_%=:\n" \
"s_setpc_b64 s[28:29]\n" \
".fill (L_ab + 4096 - .) / 4, 4, 0xBF800000\n" \
".fill (L_ab + 4096 - .) / 4, 4, 0xBF800000\n" \
"L_ap1:\n" \
"s_setpc_b64 s[88:89]\n" \
"L_gtr:\n" \
"v_mfma_f32_16x16x32_bf16 v[96:99], a[128:131], a[0:3], v[32:35]\n" \
"s_xor_b32 m0, s65, 0x12000\n" \
"s_mov_b32 s65, s13\n" \
"v_mfma_f32_16x16x32_bf16 v[96:99], a[132:135], a[4:7], v[96:99]\n" \
"buffer_load_dwordx4 v50, s[8:11], s64 offen offset:0 sc1 lds\n" \
"v_mfma_f32_16x16x32_bf16 v[96:99], a[136:139], a[8:11], v[96:99]\n" \
"v_add_u32_e32 v63, s15, v20\n" \
"s_add_u32 s67, s14, 40960\n" \
"v_mfma_f32_16x16x32_bf16 v[96:99], a[140:143], a[12:15], v[96:99]\n" \
"ds_read_b128 a[160:163], v49 offset:0\n" \
"ds_read_b128 a[164:167], v49 offset:1024\n" \
"v_mfma_f32_16x16x32_bf16 v[100:103], a[156:159], a[12:15], v[32:35]\n" \
"buffer_load_dwordx4 v50, s[8:11], s64 offen offset:1024 sc1 lds\n" \
"v_mfma_f32_16x16x32_bf16 v[100:103], a[152:155], a[8:11], v[100:103]\n" \
"v_add_u32_e32 v50, 0x6000, v63\n" \
"s_xor_b32 m0, s65, 0x8000\n" \
"v_mfma_f32_16x16x32_bf16 v[100:103], a[148:151], a[4:7], v[100:103]\n" \
"ds_read_b128 a[168:171], v49 offset:2048\n" \
"ds_read_b128 a[172:175], v49 offset:3072\n" \
"v_mfma_f32_16x16x32_bf16 v[100:103], a[144:147], a[0:3], v[100:103]\n" \
"ds_read_b128 a[176:179], v49 offset:4096\n" \
"ds_read_b128 a[180:183], v49 offset:5120\n" \
"v_mfma_f32_16x16x32_bf16 v[108:111], a[144:147], a[16:19], v[36:39]\n" \
"buffer_load_dwordx4 v50, s[8:11], s67 offen offset:0 sc1 lds\n" \
"v_mfma_f32_16x16x32_bf16 v[108:111], a[148:151], a[20:23], v[108:111]\n" \
"ds_read_b128 a[184:187], v49 offset:6144\n" \
"ds_read_b128 a[188:191], v49 offset:7168\n" \
"v_mfma_f32_16x16x32_bf16 v[108:111], a[152:155], a[24:27], v[108:111]\n" \
"s_mov_b32 s64, s14\n" \
"v_add_u32_e32 v51, 0x12000, v63\n" \
"v_mfma_f32_16x16x32_bf16 v[108:111], a[156:159], a[28:31], v[108:111]\n" \
"v_exp_f32_e32 v86, v86\n" \
"v_mfma_f32_16x16x32_bf16 v[104:107], a[140:143], a[28:31], v[36:39]\n" \
"buffer_load_dwordx4 v50, s[8:11], s67 offen offset:1024 sc1 lds\n" \
"v_mfma_f32_16x16x32_bf16 v[104:107], a[136:139], a[24:27], v[104:107]\n" \
"v_exp_f32_e32 v87, v87\n" \
"v_mfma_f32_16x16x32_bf16 v[104:107], a[132:135], a[20:23], v[104:107]\n" \
"v_exp_f32_e32 v84, v84\n" \
"v_mfma_f32_16x16x32_bf16 v[104:107], a[128:131], a[16:19], v[104:107]\n" \
"v_exp_f32_e32 v85, v85\n" \
"v_mfma_f32_16x16x32_bf16 v[112:115], a[128:131], a[32:35], v[40:43]\n" \
"v_cvt_pk_bf16_f32 v87, v86, v87\n" \
"v_cvt_pk_bf16_f32 v86, v84, v85\n" \
"v_mfma_f32_16x16x32_bf16 v[112:115], a[132:135], a[36:39], v[112:115]\n" \
"buffer_load_dwordx4 v50, s[8:11], s67 offen offset:2048 sc1 lds\n" \
"v_mfma_f32_16x16x32_bf16 v[112:115], a[136:139], a[40:43], v[112:115]\n" \
"v_exp_f32_e32 v82, v82\n" \
"v_mfma_f32_16x16x32_bf16 v[112:115], a[140:143], a[44:47], v[112:115]\n" \
"v_exp_f32_e32 v83, v83\n" \
"v_mfma_f32_16x16x32_bf16 v[116:119], a[156:159], a[44:47], v[40:43]\n" \
"v_exp_f32_e32 v80, v80\n" \
"v_mfma_f32_16x16x32_bf16 v[116:119], a[152:155], a[40:43], v[116:119]\n" \
"buffer_load_dwordx4 v50, s[8:11], s67 offen offset:3072 sc1 lds\n" \
"v_mfma_f32_16x16x32_bf16 v[116:119], a[148:151], a[36:39], v[116:119]\n" \
"s_xor_b32 m0, s65, 0xa000\n" \
"v_cvt_pk_bf16_f32 v85, v82, v83\n" \
"v_mfma_f32_16x16x32_bf16 v[116:119], a[144:147], a[32:35], v[116:119]\n" \
"v_exp_f32_e32 v81, v81\n" \
"v_mfma_f32_16x16x32_bf16 v[124:127], a[144:147], a[48:51], v[44:47]\n" \
"v_cvt_pk_bf16_f32 v84, v80, v81\n" \
"v_readlane_b32 s93, v11, 19\n" \
"v_mfma_f32_16x16x32_bf16 v[124:127], a[148:151], a[52:55], v[124:127]\n" \
"buffer_load_dwordx4 v51, s[8:11], s64 offen offset:0 sc1 lds\n" \
"v_mfma_f32_16x16x32_bf16 v[124:127], a[152:155], a[56:59], v[124:127]\n" \
"v_exp_f32_e32 v88, v88\n" \
"v_mfma_f32_16x16x32_bf16 v[124:127], a[156:159], a[60:63], v[124:127]\n" \
"v_exp_f32_e32 v89, v89\n" \
"v_mfma_f32_16x16x32_bf16 v[120:123], a[140:143], a[60:63], v[44:47]\n" \
"v_exp_f32_e32 v90, v90\n" \
"v_mfma_f32_16x16x32_bf16 v[120:123], a[136:139], a[56:59], v[120:123]\n" \
"v_exp_f32_e32 v91, v91\n" \
"v_mfma_f32_16x16x32_bf16 v[120:123], a[132:135], a[52:55], v[120:123]\n" \
"buffer_load_dwordx4 v51, s[8:11], s64 offen offset:1024 sc1 lds\n" \
"v_mfma_f32_16x16x32_bf16 v[120:123], a[128:131], a[48:51], v[120:123]\n" \
"v_cvt_pk_bf16_f32 v88, v88, v89\n" \
"s_waitcnt lgkmcnt(0)\n" \
"v_mfma_f32_16x16x32_bf16 v[128:131], a[160:163], v[68:71], v[128:131]\n" \
"v_exp_f32_e32 v92, v92\n" \
"v_mfma_f32_16x16x32_bf16 v[160:163], a[160:163], v[72:75], v[160:163]\n" \
"v_exp_f32_e32 v93, v93\n" \
"v_mfma_f32_16x16x32_bf16 v[132:135], a[164:167], v[68:71], v[132:135]\n" \
"buffer_load_dwordx4 v51, s[8:11], s64 offen offset:2048 sc1 lds\n" \
"v_mfma_f32_16x16x32_bf16 v[164:167], a[164:167], v[72:75], v[164:167]\n" \
"v_exp_f32_e32 v94, v94\n" \
"v_mfma_f32_16x16x32_bf16 v[136:139], a[168:171], v[68:71], v[136:139]\n" \
"v_exp_f32_e32 v95, v95\n" \
"v_mfma_f32_16x16x32_bf16 v[168:171], a[168:171], v[72:75], v[168:171]\n" \
"v_cvt_pk_bf16_f32 v89, v90, v91\n" \
"v_cvt_pk_bf16_f32 v90, v92, v93\n" \
"v_mfma_f32_16x16x32_bf16 v[140:143], a[172:175], v[68:71], v[140:143]\n" \
"buffer_load_dwordx4 v51, s[8:11], s64 offen offset:3072 sc1 lds\n" \
"v_mfma_f32_16x16x32_bf16 v[172:175], a[172:175], v[72:75], v[172:175]\n" \
"v_cvt_pk_bf16_f32 v91, v94, v95\n" \
"v_readlane_b32 s80, v11, 11\n" \
"v_mfma_f32_16x16x32_bf16 v[144:147], a[176:179], v[68:71], v[144:147]\n" \
"v_readlane_b32 s81, v11, 12\n" \
"s_mov_b32 s82, 0x47fca0\n" \
"v_mfma_f32_16x16x32_bf16 v[176:179], a[176:179], v[72:75], v[176:179]\n" \
"s_mov_b32 s83, 0x20000\n" \
"v_readlane_b32 s92, v11, 26\n" \
"v_mfma_f32_16x16x32_bf16 v[148:151], a[180:183], v[68:71], v[148:151]\n" \
"s_add_u32 m0, s93, 0x0\n" \
"ds_read_b128 a[128:131], v48 offset:0\n" \
"v_mfma_f32_16x16x32_bf16 v[180:183], a[180:183], v[72:75], v[180:183]\n" \
"ds_read_b128 a[144:147], v48 offset:4096\n" \
"ds_read_b128 a[132:135], v48 offset:1024\n" \
"v_mfma_f32_16x16x32_bf16 v[152:155], a[184:187], v[68:71], v[152:155]\n" \
"buffer_load_dwordx4 v0, s[80:83], s92 offen offset:0 lds\n" \
"v_mfma_f32_16x16x32_bf16 v[184:187], a[184:187], v[72:75], v[184:187]\n" \
"ds_read_b128 a[148:151], v48 offset:5120\n" \
"ds_read_b128 a[136:139], v48 offset:2048\n" \
"v_mfma_f32_16x16x32_bf16 v[156:159], a[188:191], v[68:71], v[156:159]\n" \
"ds_read_b128 a[152:155], v48 offset:6144\n" \
"ds_read_b128 a[140:143], v48 offset:3072\n" \
"v_mfma_f32_16x16x32_bf16 v[188:191], a[188:191], v[72:75], v[188:191]\n" \
"ds_read_b128 a[156:159], v48 offset:7168\n" \
"v_add_u32_e32 v52, 0x14000, v63\n" \
"v_smfmac_f32_16x16x64_bf16 a[192:195], a[200:203], v[68:75], v21\n" \
"buffer_load_dwordx4 v1, s[80:83], s92 offen offset:1024 lds\n" \
"v_mfma_f32_16x16x32_bf16 v[220:223], a[188:191], v[84:87], v[220:223]\n" \
"v_exp_f32_e32 v102, v102\n" \
"v_mfma_f32_16x16x32_bf16 v[252:255], a[188:191], v[88:91], v[252:255]\n" \
"v_exp_f32_e32 v103, v103\n" \
"v_mfma_f32_16x16x32_bf16 v[216:219], a[184:187], v[84:87], v[216:219]\n" \
"buffer_load_dwordx4 v2, s[80:83], s92 offen offset:2048 lds\n" \
"v_mfma_f32_16x16x32_bf16 v[248:251], a[184:187], v[88:91], v[248:251]\n" \
"v_add_u32_e32 v53, 0x20000, v63\n" \
"v_cvt_pk_bf16_f32 v103, v102, v103\n" \
"v_mfma_f32_16x16x32_bf16 v[212:215], a[180:183], v[84:87], v[212:215]\n" \
"v_exp_f32_e32 v100, v100\n" \
"v_mfma_f32_16x16x32_bf16 v[244:247], a[180:183], v[88:91], v[244:247]\n" \
"v_exp_f32_e32 v101, v101\n" \
"v_mfma_f32_16x16x32_bf16 v[208:211], a[176:179], v[84:87], v[208:211]\n" \
"buffer_load_dwordx4 v3, s[80:83], s92 offen offset:3072 lds\n" \
"v_mfma_f32_16x16x32_bf16 v[240:243], a[176:179], v[88:91], v[240:243]\n" \
"v_exp_f32_e32 v98, v98\n" \
"v_mfma_f32_16x16x32_bf16 v[204:207], a[172:175], v[84:87], v[204:207]\n" \
"v_exp_f32_e32 v99, v99\n" \
"v_mfma_f32_16x16x32_bf16 v[236:239], a[172:175], v[88:91], v[236:239]\n" \
"v_exp_f32_e32 v96, v96\n" \
"v_mfma_f32_16x16x32_bf16 v[200:203], a[168:171], v[84:87], v[200:203]\n" \
"v_exp_f32_e32 v97, v97\n" \
"v_mfma_f32_16x16x32_bf16 v[232:235], a[168:171], v[88:91], v[232:235]\n" \
"v_exp_f32_e32 v104, v104\n" \
"v_mfma_f32_16x16x32_bf16 v[196:199], a[164:167], v[84:87], v[196:199]\n" \
"v_exp_f32_e32 v105, v105\n" \
"v_mfma_f32_16x16x32_bf16 v[228:231], a[164:167], v[88:91], v[228:231]\n" \
"v_cvt_pk_bf16_f32 v102, v100, v101\n" \
"v_cvt_pk_bf16_f32 v101, v98, v99\n" \
"v_mfma_f32_16x16x32_bf16 v[192:195], a[160:163], v[84:87], v[192:195]\n" \
"v_cvt_pk_bf16_f32 v100, v96, v97\n" \
"v_cvt_pk_bf16_f32 v104, v104, v105\n" \
"v_mfma_f32_16x16x32_bf16 v[224:227], a[160:163], v[88:91], v[224:227]\n" \
"v_exp_f32_e32 v106, v106\n" \
"v_smfmac_f32_16x16x64_bf16 a[196:199], a[200:203], v[84:91], v21\n" \
"v_exp_f32_e32 v107, v107\n" \
"s_waitcnt vmcnt(14) lgkmcnt(0)\n" \
"s_barrier\n" \
"v_mfma_f32_16x16x32_bf16 v[64:67], a[128:131], a[0:3], v[32:35]\n" \
"s_xor_b32 m0, s65, 0x18000\n" \
"ds_read_b128 a[160:163], v49 offset:8192\n" \
"v_mfma_f32_16x16x32_bf16 v[64:67], a[132:135], a[4:7], v[64:67]\n" \
"buffer_load_dwordx4 v52, s[8:11], s64 offen offset:0 sc1 lds\n" \
"v_mfma_f32_16x16x32_bf16 v[64:67], a[136:139], a[8:11], v[64:67]\n" \
"ds_read_b128 a[164:167], v49 offset:9216\n" \
"ds_read_b128 a[168:171], v49 offset:10240\n" \
"v_mfma_f32_16x16x32_bf16 v[64:67], a[140:143], a[12:15], v[64:67]\n" \
"ds_read_b128 a[172:175], v49 offset:11264\n" \
"ds_read_b128 a[176:179], v49 offset:12288\n" \
"v_mfma_f32_16x16x32_bf16 v[68:71], a[156:159], a[12:15], v[32:35]\n" \
"buffer_load_dwordx4 v52, s[8:11], s64 offen offset:1024 sc1 lds\n" \
"v_mfma_f32_16x16x32_bf16 v[68:71], a[152:155], a[8:11], v[68:71]\n" \
"ds_read_b128 a[180:183], v49 offset:13312\n" \
"ds_read_b128 a[184:187], v49 offset:14336\n" \
"v_mfma_f32_16x16x32_bf16 v[68:71], a[148:151], a[4:7], v[68:71]\n" \
"v_exp_f32_e32 v108, v108\n" \
"v_mfma_f32_16x16x32_bf16 v[68:71], a[144:147], a[0:3], v[68:71]\n" \
"v_exp_f32_e32 v109, v109\n" \
"v_mfma_f32_16x16x32_bf16 v[76:79], a[144:147], a[16:19], v[36:39]\n" \
"v_exp_f32_e32 v110, v110\n" \
"v_mfma_f32_16x16x32_bf16 v[76:79], a[148:151], a[20:23], v[76:79]\n" \
"buffer_load_dwordx4 v52, s[8:11], s64 offen offset:2048 sc1 lds\n" \
"v_mfma_f32_16x16x32_bf16 v[76:79], a[152:155], a[24:27], v[76:79]\n" \
"v_exp_f32_e32 v111, v111\n" \
"v_mfma_f32_16x16x32_bf16 v[76:79], a[156:159], a[28:31], v[76:79]\n" \
"v_cvt_pk_bf16_f32 v105, v106, v107\n" \
"v_cvt_pk_bf16_f32 v106, v108, v109\n" \
"v_mfma_f32_16x16x32_bf16 v[72:75], a[140:143], a[28:31], v[36:39]\n" \
"v_cvt_pk_bf16_f32 v107, v110, v111\n" \
"ds_read_b128 a[188:191], v49 offset:15360\n" \
"v_mfma_f32_16x16x32_bf16 v[72:75], a[136:139], a[24:27], v[72:75]\n" \
"buffer_load_dwordx4 v52, s[8:11], s64 offen offset:3072 sc1 lds\n" \
"v_mfma_f32_16x16x32_bf16 v[72:75], a[132:135], a[20:23], v[72:75]\n" \
"s_mov_b32 m0, s65\n" \
"v_add_u32_e32 v49, 0x10000, v48\n" \
"v_mfma_f32_16x16x32_bf16 v[72:75], a[128:131], a[16:19], v[72:75]\n" \
"v_exp_f32_e32 v118, v118\n" \
"v_mfma_f32_16x16x32_bf16 v[80:83], a[128:131], a[32:35], v[40:43]\n" \
"v_exp_f32_e32 v119, v119\n" \
"v_mfma_f32_16x16x32_bf16 v[80:83], a[132:135], a[36:39], v[80:83]\n" \
"v_exp_f32_e32 v116, v116\n" \
"v_mfma_f32_16x16x32_bf16 v[80:83], a[136:139], a[40:43], v[80:83]\n" \
"buffer_load_dwordx4 v53, s[8:11], s64 offen offset:0 sc1 lds\n" \
"v_mfma_f32_16x16x32_bf16 v[80:83], a[140:143], a[44:47], v[80:83]\n" \
"v_exp_f32_e32 v117, v117\n" \
"v_mfma_f32_16x16x32_bf16 v[84:87], a[156:159], a[44:47], v[40:43]\n" \
"v_cvt_pk_bf16_f32 v119, v118, v119\n" \
"v_cvt_pk_bf16_f32 v118, v116, v117\n" \
"v_mfma_f32_16x16x32_bf16 v[84:87], a[152:155], a[40:43], v[84:87]\n" \
"v_exp_f32_e32 v114, v114\n" \
"v_mfma_f32_16x16x32_bf16 v[84:87], a[148:151], a[36:39], v[84:87]\n" \
"v_exp_f32_e32 v115, v115\n" \
"v_mfma_f32_16x16x32_bf16 v[84:87], a[144:147], a[32:35], v[84:87]\n" \
"buffer_load_dwordx4 v53, s[8:11], s64 offen offset:1024 sc1 lds\n" \
"v_mfma_f32_16x16x32_bf16 v[92:95], a[144:147], a[48:51], v[44:47]\n" \
"v_exp_f32_e32 v112, v112\n" \
"v_mfma_f32_16x16x32_bf16 v[92:95], a[148:151], a[52:55], v[92:95]\n" \
"v_exp_f32_e32 v113, v113\n" \
"v_mfma_f32_16x16x32_bf16 v[92:95], a[152:155], a[56:59], v[92:95]\n" \
"v_cvt_pk_bf16_f32 v117, v114, v115\n" \
"v_cvt_pk_bf16_f32 v116, v112, v113\n" \
"v_mfma_f32_16x16x32_bf16 v[92:95], a[156:159], a[60:63], v[92:95]\n" \
"buffer_load_dwordx4 v53, s[8:11], s64 offen offset:2048 sc1 lds\n" \
"v_mfma_f32_16x16x32_bf16 v[88:91], a[140:143], a[60:63], v[44:47]\n" \
"v_exp_f32_e32 v120, v120\n" \
"v_mfma_f32_16x16x32_bf16 v[88:91], a[136:139], a[56:59], v[88:91]\n" \
"v_exp_f32_e32 v121, v121\n" \
"v_mfma_f32_16x16x32_bf16 v[88:91], a[132:135], a[52:55], v[88:91]\n" \
"v_exp_f32_e32 v122, v122\n" \
"v_mfma_f32_16x16x32_bf16 v[88:91], a[128:131], a[48:51], v[88:91]\n" \
"v_cvt_pk_bf16_f32 v120, v120, v121\n" \
"s_waitcnt lgkmcnt(0)\n" \
"v_mfma_f32_16x16x32_bf16 v[128:131], a[160:163], v[100:103], v[128:131]\n" \
"buffer_load_dwordx4 v53, s[8:11], s64 offen offset:3072 sc1 lds\n" \
"v_mfma_f32_16x16x32_bf16 v[160:163], a[160:163], v[104:107], v[160:163]\n" \
"s_add_u32 m0, s93, 0x1000\n" \
"ds_read_b128 a[128:131], v48 offset:8192\n" \
"v_mfma_f32_16x16x32_bf16 v[132:135], a[164:167], v[100:103], v[132:135]\n" \
"v_exp_f32_e32 v123, v123\n" \
"v_mfma_f32_16x16x32_bf16 v[164:167], a[164:167], v[104:107], v[164:167]\n" \
"v_exp_f32_e32 v124, v124\n" \
"v_mfma_f32_16x16x32_bf16 v[136:139], a[168:171], v[100:103], v[136:139]\n" \
"buffer_load_dwordx4 v4, s[80:83], s92 offen offset:0 lds\n" \
"v_mfma_f32_16x16x32_bf16 v[168:171], a[168:171], v[104:107], v[168:171]\n" \
"v_exp_f32_e32 v125, v125\n" \
"v_mfma_f32_16x16x32_bf16 v[140:143], a[172:175], v[100:103], v[140:143]\n" \
"v_exp_f32_e32 v126, v126\n" \
"v_mfma_f32_16x16x32_bf16 v[172:175], a[172:175], v[104:107], v[172:175]\n" \
"v_exp_f32_e32 v127, v127\n" \
"v_mfma_f32_16x16x32_bf16 v[144:147], a[176:179], v[100:103], v[144:147]\n" \
"v_cvt_pk_bf16_f32 v121, v122, v123\n" \
"v_cvt_pk_bf16_f32 v122, v124, v125\n" \
"v_mfma_f32_16x16x32_bf16 v[176:179], a[176:179], v[104:107], v[176:179]\n" \
"buffer_load_dwordx4 v5, s[80:83], s92 offen offset:1024 lds\n" \
"v_mfma_f32_16x16x32_bf16 v[148:151], a[180:183], v[100:103], v[148:151]\n" \
"v_cvt_pk_bf16_f32 v123, v126, v127\n" \
"ds_read_b128 a[144:147], v48 offset:12288\n" \
"v_mfma_f32_16x16x32_bf16 v[180:183], a[180:183], v[104:107], v[180:183]\n" \
"ds_read_b128 a[132:135], v48 offset:9216\n" \
"ds_read_b128 a[148:151], v48 offset:13312\n" \
"v_mfma_f32_16x16x32_bf16 v[152:155], a[184:187], v[100:103], v[152:155]\n" \
"ds_read_b128 a[136:139], v48 offset:10240\n" \
"ds_read_b128 a[152:155], v48 offset:14336\n" \
"v_mfma_f32_16x16x32_bf16 v[184:187], a[184:187], v[104:107], v[184:187]\n" \
"ds_read_b128 a[140:143], v48 offset:11264\n" \
"ds_read_b128 a[156:159], v48 offset:15360\n" \
"v_mfma_f32_16x16x32_bf16 v[156:159], a[188:191], v[100:103], v[156:159]\n" \
"buffer_load_dwordx4 v6, s[80:83], s92 offen offset:2048 lds\n" \
"v_mfma_f32_16x16x32_bf16 v[188:191], a[188:191], v[104:107], v[188:191]\n" \
"v_exp_f32_e32 v70, v70\n" \
"v_smfmac_f32_16x16x64_bf16 a[192:195], a[200:203], v[100:107], v21\n" \
"v_exp_f32_e32 v71, v71\n" \
"v_mfma_f32_16x16x32_bf16 v[220:223], a[188:191], v[116:119], v[220:223]\n" \
"v_exp_f32_e32 v72, v72\n" \
"v_mfma_f32_16x16x32_bf16 v[252:255], a[188:191], v[120:123], v[252:255]\n" \
"buffer_load_dwordx4 v7, s[80:83], s92 offen offset:3072 lds\n" \
"v_mfma_f32_16x16x32_bf16 v[216:219], a[184:187], v[116:119], v[216:219]\n" \
"s_add_u32 m0, s93, 0x2000\n" \
"v_cvt_pk_bf16_f32 v71, v70, v71\n" \
"v_mfma_f32_16x16x32_bf16 v[248:251], a[184:187], v[120:123], v[248:251]\n" \
"v_exp_f32_e32 v73, v73\n" \
"v_mfma_f32_16x16x32_bf16 v[212:215], a[180:183], v[116:119], v[212:215]\n" \
"v_exp_f32_e32 v68, v68\n" \
"v_mfma_f32_16x16x32_bf16 v[244:247], a[180:183], v[120:123], v[244:247]\n" \
"v_exp_f32_e32 v69, v69\n" \
"v_mfma_f32_16x16x32_bf16 v[208:211], a[176:179], v[116:119], v[208:211]\n" \
"buffer_load_dwordx4 v8, s[80:83], s92 offen offset:0 lds\n" \
"v_mfma_f32_16x16x32_bf16 v[240:243], a[176:179], v[120:123], v[240:243]\n" \
"v_exp_f32_e32 v74, v74\n" \
"v_mfma_f32_16x16x32_bf16 v[204:207], a[172:175], v[116:119], v[204:207]\n" \
"v_exp_f32_e32 v75, v75\n" \
"v_mfma_f32_16x16x32_bf16 v[236:239], a[172:175], v[120:123], v[236:239]\n" \
"v_exp_f32_e32 v66, v66\n" \
"v_mfma_f32_16x16x32_bf16 v[200:203], a[168:171], v[116:119], v[200:203]\n" \
"v_exp_f32_e32 v67, v67\n" \
"v_mfma_f32_16x16x32_bf16 v[232:235], a[168:171], v[120:123], v[232:235]\n" \
"v_exp_f32_e32 v64, v64\n" \
"v_mfma_f32_16x16x32_bf16 v[196:199], a[164:167], v[116:119], v[196:199]\n" \
"v_exp_f32_e32 v65, v65\n" \
"v_mfma_f32_16x16x32_bf16 v[228:231], a[164:167], v[120:123], v[228:231]\n" \
"v_cvt_pk_bf16_f32 v72, v72, v73\n" \
"v_cvt_pk_bf16_f32 v70, v68, v69\n" \
"v_mfma_f32_16x16x32_bf16 v[192:195], a[160:163], v[116:119], v[192:195]\n" \
"v_exp_f32_e32 v76, v76\n" \
"v_mfma_f32_16x16x32_bf16 v[224:227], a[160:163], v[120:123], v[224:227]\n" \
"v_exp_f32_e32 v77, v77\n" \
"v_smfmac_f32_16x16x64_bf16 a[196:199], a[200:203], v[116:123], v21\n" \
"v_exp_f32_e32 v78, v78\n" \
"s_waitcnt vmcnt(13) lgkmcnt(0)\n" \
"s_barrier\n" \
"v_exp_f32_e32 v79, v79\n" \
"v_cvt_pk_bf16_f32 v73, v74, v75\n" \
"v_cvt_pk_bf16_f32 v69, v66, v67\n" \
"v_cvt_pk_bf16_f32 v68, v64, v65\n" \
"v_cvt_pk_bf16_f32 v74, v76, v77\n" \
"v_cvt_pk_bf16_f32 v75, v78, v79\n" \
"v_add_u32_e32 v48, s16, v20\n" \
"s_xor_b32 s65, s65, 0x8000\n" \
"s_add_u32 s64, s64, 0x10000\n" \
"s_getpc_b64 s[86:87]\n" \
"L_hpt_%=:\n" \
"s_add_u32 s86, s86, L_ap8-L_hpt_%=\n" \
"s_addc_u32 s87, s87, 0\n" \
"s_add_u32 s86, s86, s15\n" \
"s_addc_u32 s87, s87, 0\n" \
"s_mov_b64 exec, 0\n" \
"s_swappc_b64 s[88:89], s[86:87]\n" \
"s_mov_b64 exec, -1\n" \
"s_sub_u32 s66, s19, 1\n" \
"s_cbranch_scc1 L_la_%=\n" \
"s_branch L_2_%=\n" \
".fill (L_ab + 6400 - .) / 4, 4, 0xBF800000\n" \
"L_tL_2_%=:\n" \
"s_waitcnt vmcnt(8) lgkmcnt(0)\n" \
"v_smfmac_f32_16x16x64_bf16 a[196:199], a[200:203], v[116:123], v21\n" \
"s_barrier\n" \
"L_2_%=:\n" \
"v_mfma_f32_16x16x32_bf16 v[96:99], a[128:131], a[64:67], v[32:35]\n" \
"s_xor_b32 m0, s65, 0x12000\n" \
"ds_read_b128 a[160:163], v49 offset:0\n" \
"v_mfma_f32_16x16x32_bf16 v[96:99], a[132:135], a[68:71], v[96:99]\n" \
"buffer_load_dwordx4 v50, s[8:11], s64 offen offset:0 sc1 lds\n" \
"v_mfma_f32_16x16x32_bf16 v[96:99], a[136:139], a[72:75], v[96:99]\n" \
"ds_read_b128 a[164:167], v49 offset:1024\n" \
"ds_read_b128 a[168:171], v49 offset:2048\n" \
"v_mfma_f32_16x16x32_bf16 v[96:99], a[140:143], a[76:79], v[96:99]\n" \
"ds_read_b128 a[172:175], v49 offset:3072\n" \
"ds_read_b128 a[176:179], v49 offset:4096\n" \
"v_mfma_f32_16x16x32_bf16 v[100:103], a[156:159], a[76:79], v[32:35]\n" \
"ds_read_b128 a[180:183], v49 offset:5120\n" \
"ds_read_b128 a[184:187], v49 offset:6144\n" \
"v_mfma_f32_16x16x32_bf16 v[100:103], a[152:155], a[72:75], v[100:103]\n" \
"v_exp_f32_e64 v86, v86\n" \
"v_mfma_f32_16x16x32_bf16 v[100:103], a[148:151], a[68:71], v[100:103]\n" \
"v_exp_f32_e64 v87, v87\n" \
"v_mfma_f32_16x16x32_bf16 v[100:103], a[144:147], a[64:67], v[100:103]\n" \
"buffer_load_dwordx4 v50, s[8:11], s64 offen offset:1024 sc1 lds\n" \
"v_mfma_f32_16x16x32_bf16 v[108:111], a[144:147], a[80:83], v[36:39]\n" \
"ds_read_b128 a[188:191], v49 offset:7168\n" \
"v_cvt_pk_bf16_f32 v87, v86, v87\n" \
"v_mfma_f32_16x16x32_bf16 v[108:111], a[148:151], a[84:87], v[108:111]\n" \
"v_exp_f32_e64 v84, v84\n" \
"v_mfma_f32_16x16x32_bf16 v[108:111], a[152:155], a[88:91], v[108:111]\n" \
"v_exp_f32_e64 v85, v85\n" \
"v_mfma_f32_16x16x32_bf16 v[108:111], a[156:159], a[92:95], v[108:111]\n" \
"v_exp_f32_e64 v82, v82\n" \
"v_mfma_f32_16x16x32_bf16 v[104:107], a[140:143], a[92:95], v[36:39]\n" \
"v_exp_f32_e64 v83, v83\n" \
"v_mfma_f32_16x16x32_bf16 v[104:107], a[136:139], a[88:91], v[104:107]\n" \
"v_exp_f32_e64 v80, v80\n" \
"v_mfma_f32_16x16x32_bf16 v[104:107], a[132:135], a[84:87], v[104:107]\n" \
"v_exp_f32_e64 v81, v81\n" \
"v_mfma_f32_16x16x32_bf16 v[104:107], a[128:131], a[80:83], v[104:107]\n" \
"buffer_load_dwordx4 v50, s[8:11], s64 offen offset:2048 sc1 lds\n" \
"v_mfma_f32_16x16x32_bf16 v[112:115], a[128:131], a[96:99], v[40:43]\n" \
"v_exp_f32_e64 v88, v88\n" \
"v_mfma_f32_16x16x32_bf16 v[112:115], a[132:135], a[100:103], v[112:115]\n" \
"v_exp_f32_e64 v89, v89\n" \
"v_mfma_f32_16x16x32_bf16 v[112:115], a[136:139], a[104:107], v[112:115]\n" \
"v_cvt_pk_bf16_f32 v86, v84, v85\n" \
"v_cvt_pk_bf16_f32 v85, v82, v83\n" \
"v_mfma_f32_16x16x32_bf16 v[112:115], a[140:143], a[108:111], v[112:115]\n" \
"v_cvt_pk_bf16_f32 v84, v80, v81\n" \
"v_cvt_pk_bf16_f32 v88, v88, v89\n" \
"v_mfma_f32_16x16x32_bf16 v[116:119], a[156:159], a[108:111], v[40:43]\n" \
"v_exp_f32_e64 v90, v90\n" \
"v_mfma_f32_16x16x32_bf16 v[116:119], a[152:155], a[104:107], v[116:119]\n" \
"v_exp_f32_e64 v91, v91\n" \
"v_mfma_f32_16x16x32_bf16 v[116:119], a[148:151], a[100:103], v[116:119]\n" \
"buffer_load_dwordx4 v50, s[8:11], s64 offen offset:3072 sc1 lds\n" \
"v_mfma_f32_16x16x32_bf16 v[116:119], a[144:147], a[96:99], v[116:119]\n" \
"s_xor_b32 m0, s65, 0xa000\n" \
"v_cvt_pk_bf16_f32 v89, v90, v91\n" \
"v_mfma_f32_16x16x32_bf16 v[124:127], a[144:147], a[112:115], v[44:47]\n" \
"v_exp_f32_e64 v92, v92\n" \
"v_mfma_f32_16x16x32_bf16 v[124:127], a[148:151], a[116:119], v[124:127]\n" \
"v_exp_f32_e64 v93, v93\n" \
"v_mfma_f32_16x16x32_bf16 v[124:127], a[152:155], a[120:123], v[124:127]\n" \
"v_exp_f32_e64 v94, v94\n" \
"v_mfma_f32_16x16x32_bf16 v[124:127], a[156:159], a[124:127], v[124:127]\n" \
"v_exp_f32_e64 v95, v95\n" \
"v_mfma_f32_16x16x32_bf16 v[120:123], a[140:143], a[124:127], v[44:47]\n" \
"v_cvt_pk_bf16_f32 v90, v92, v93\n" \
"v_cvt_pk_bf16_f32 v91, v94, v95\n" \
"v_mfma_f32_16x16x32_bf16 v[120:123], a[136:139], a[120:123], v[120:123]\n" \
"ds_read_b128 a[144:147], v48 offset:4096\n" \
"ds_read_b128 a[148:151], v48 offset:5120\n" \
"v_mfma_f32_16x16x32_bf16 v[120:123], a[132:135], a[116:119], v[120:123]\n" \
"buffer_load_dwordx4 v51, s[8:11], s64 offen offset:0 sc1 lds\n" \
"v_mfma_f32_16x16x32_bf16 v[120:123], a[128:131], a[112:115], v[120:123]\n" \
"ds_read_b128 a[128:131], v48 offset:0\n" \
"s_waitcnt lgkmcnt(3)\n" \
"v_mfma_f32_16x16x32_bf16 v[128:131], a[160:163], v[68:71], v[128:131]\n" \
"ds_read_b128 a[132:135], v48 offset:1024\n" \
"ds_read_b128 a[136:139], v48 offset:2048\n" \
"v_mfma_f32_16x16x32_bf16 v[160:163], a[160:163], v[72:75], v[160:163]\n" \
"ds_read_b128 a[152:155], v48 offset:6144\n" \
"ds_read_b128 a[140:143], v48 offset:3072\n" \
"v_mfma_f32_16x16x32_bf16 v[132:135], a[164:167], v[68:71], v[132:135]\n" \
"v_exp_f32_e32 v102, v102\n" \
"v_mfma_f32_16x16x32_bf16 v[164:167], a[164:167], v[72:75], v[164:167]\n" \
"v_exp_f32_e64 v103, v103\n" \
"v_mfma_f32_16x16x32_bf16 v[136:139], a[168:171], v[68:71], v[136:139]\n" \
"ds_read_b128 a[156:159], v48 offset:7168\n" \
"v_cvt_pk_bf16_f32 v103, v102, v103\n" \
"v_mfma_f32_16x16x32_bf16 v[168:171], a[168:171], v[72:75], v[168:171]\n" \
"buffer_load_dwordx4 v51, s[8:11], s64 offen offset:1024 sc1 lds\n" \
"v_mfma_f32_16x16x32_bf16 v[140:143], a[172:175], v[68:71], v[140:143]\n" \
"v_exp_f32_e64 v100, v100\n" \
"v_mfma_f32_16x16x32_bf16 v[172:175], a[172:175], v[72:75], v[172:175]\n" \
"v_exp_f32_e64 v101, v101\n" \
"v_mfma_f32_16x16x32_bf16 v[144:147], a[176:179], v[68:71], v[144:147]\n" \
"v_exp_f32_e64 v98, v98\n" \
"v_mfma_f32_16x16x32_bf16 v[176:179], a[176:179], v[72:75], v[176:179]\n" \
"v_exp_f32_e64 v99, v99\n" \
"v_mfma_f32_16x16x32_bf16 v[148:151], a[180:183], v[68:71], v[148:151]\n" \
"v_exp_f32_e64 v96, v96\n" \
"v_mfma_f32_16x16x32_bf16 v[180:183], a[180:183], v[72:75], v[180:183]\n" \
"v_exp_f32_e64 v97, v97\n" \
"v_mfma_f32_16x16x32_bf16 v[152:155], a[184:187], v[68:71], v[152:155]\n" \
"v_exp_f32_e64 v104, v104\n" \
"v_mfma_f32_16x16x32_bf16 v[184:187], a[184:187], v[72:75], v[184:187]\n" \
"buffer_load_dwordx4 v51, s[8:11], s64 offen offset:2048 sc1 lds\n" \
"v_mfma_f32_16x16x32_bf16 v[156:159], a[188:191], v[68:71], v[156:159]\n" \
"v_exp_f32_e64 v105, v105\n" \
"v_mfma_f32_16x16x32_bf16 v[188:191], a[188:191], v[72:75], v[188:191]\n" \
"v_cvt_pk_bf16_f32 v102, v100, v101\n" \
"v_cvt_pk_bf16_f32 v101, v98, v99\n" \
"v_smfmac_f32_16x16x64_bf16 a[192:195], a[200:203], v[68:75], v21\n" \
"v_cvt_pk_bf16_f32 v100, v96, v97\n" \
"v_cvt_pk_bf16_f32 v104, v104, v105\n" \
"v_mfma_f32_16x16x32_bf16 v[220:223], a[188:191], v[84:87], v[220:223]\n" \
"v_exp_f32_e64 v106, v106\n" \
"v_mfma_f32_16x16x32_bf16 v[252:255], a[188:191], v[88:91], v[252:255]\n" \
"v_exp_f32_e64 v107, v107\n" \
"v_mfma_f32_16x16x32_bf16 v[216:219], a[184:187], v[84:87], v[216:219]\n" \
"v_exp_f32_e64 v108, v108\n" \
"v_mfma_f32_16x16x32_bf16 v[248:251], a[184:187], v[88:91], v[248:251]\n" \
"buffer_load_dwordx4 v51, s[8:11], s64 offen offset:3072 sc1 lds\n" \
"v_mfma_f32_16x16x32_bf16 v[212:215], a[180:183], v[84:87], v[212:215]\n" \
"v_exp_f32_e64 v109, v109\n" \
"v_mfma_f32_16x16x32_bf16 v[244:247], a[180:183], v[88:91], v[244:247]\n" \
"v_exp_f32_e64 v110, v110\n" \
"v_mfma_f32_16x16x32_bf16 v[208:211], a[176:179], v[84:87], v[208:211]\n" \
"v_exp_f32_e64 v111, v111\n" \
"v_mfma_f32_16x16x32_bf16 v[240:243], a[176:179], v[88:91], v[240:243]\n" \
"v_exp_f32_e64 v118, v118\n" \
"v_mfma_f32_16x16x32_bf16 v[204:207], a[172:175], v[84:87], v[204:207]\n" \
"v_exp_f32_e64 v119, v119\n" \
"v_mfma_f32_16x16x32_bf16 v[236:239], a[172:175], v[88:91], v[236:239]\n" \
"v_cvt_pk_bf16_f32 v105, v106, v107\n" \
"v_cvt_pk_bf16_f32 v106, v108, v109\n" \
"v_mfma_f32_16x16x32_bf16 v[200:203], a[168:171], v[84:87], v[200:203]\n" \
"v_cvt_pk_bf16_f32 v107, v110, v111\n" \
"v_cvt_pk_bf16_f32 v119, v118, v119\n" \
"v_mfma_f32_16x16x32_bf16 v[232:235], a[168:171], v[88:91], v[232:235]\n" \
"v_exp_f32_e64 v116, v116\n" \
"v_mfma_f32_16x16x32_bf16 v[196:199], a[164:167], v[84:87], v[196:199]\n" \
"v_exp_f32_e64 v117, v117\n" \
"v_mfma_f32_16x16x32_bf16 v[228:231], a[164:167], v[88:91], v[228:231]\n" \
"v_exp_f32_e64 v114, v114\n" \
"v_mfma_f32_16x16x32_bf16 v[192:195], a[160:163], v[84:87], v[192:195]\n" \
"v_exp_f32_e64 v115, v115\n" \
"v_mfma_f32_16x16x32_bf16 v[224:227], a[160:163], v[88:91], v[224:227]\n" \
"v_cvt_pk_bf16_f32 v118, v116, v117\n" \
"s_waitcnt vmcnt(8) lgkmcnt(0)\n" \
"v_smfmac_f32_16x16x64_bf16 a[196:199], a[200:203], v[84:91], v21\n" \
"s_barrier\n" \
"v_mfma_f32_16x16x32_bf16 v[64:67], a[128:131], a[64:67], v[32:35]\n" \
"s_xor_b32 m0, s65, 0x18000\n" \
"ds_read_b128 a[160:163], v49 offset:8192\n" \
"v_mfma_f32_16x16x32_bf16 v[64:67], a[132:135], a[68:71], v[64:67]\n" \
"buffer_load_dwordx4 v52, s[8:11], s64 offen offset:0 sc1 lds\n" \
"v_mfma_f32_16x16x32_bf16 v[64:67], a[136:139], a[72:75], v[64:67]\n" \
"ds_read_b128 a[164:167], v49 offset:9216\n" \
"ds_read_b128 a[168:171], v49 offset:10240\n" \
"v_mfma_f32_16x16x32_bf16 v[64:67], a[140:143], a[76:79], v[64:67]\n" \
"ds_read_b128 a[172:175], v49 offset:11264\n" \
"ds_read_b128 a[176:179], v49 offset:12288\n" \
"v_mfma_f32_16x16x32_bf16 v[68:71], a[156:159], a[76:79], v[32:35]\n" \
"ds_read_b128 a[180:183], v49 offset:13312\n" \
"ds_read_b128 a[184:187], v49 offset:14336\n" \
"v_mfma_f32_16x16x32_bf16 v[68:71], a[152:155], a[72:75], v[68:71]\n" \
"ds_read_b128 a[188:191], v49 offset:15360\n" \
"v_cvt_pk_bf16_f32 v117, v114, v115\n" \
"v_mfma_f32_16x16x32_bf16 v[68:71], a[148:151], a[68:71], v[68:71]\n" \
"v_exp_f32_e64 v112, v112\n" \
"v_mfma_f32_16x16x32_bf16 v[68:71], a[144:147], a[64:67], v[68:71]\n" \
"buffer_load_dwordx4 v52, s[8:11], s64 offen offset:1024 sc1 lds\n" \
"v_mfma_f32_16x16x32_bf16 v[76:79], a[144:147], a[80:83], v[36:39]\n" \
"v_exp_f32_e64 v113, v113\n" \
"v_mfma_f32_16x16x32_bf16 v[76:79], a[148:151], a[84:87], v[76:79]\n" \
"v_cvt_pk_bf16_f32 v116, v112, v113\n" \
"v_add_u32_e32 v49, 0x10000, v48\n" \
"v_mfma_f32_16x16x32_bf16 v[76:79], a[152:155], a[88:91], v[76:79]\n" \
"v_exp_f32_e64 v120, v120\n" \
"v_mfma_f32_16x16x32_bf16 v[76:79], a[156:159], a[92:95], v[76:79]\n" \
"v_exp_f32_e64 v121, v121\n" \
"v_mfma_f32_16x16x32_bf16 v[72:75], a[140:143], a[92:95], v[36:39]\n" \
"v_exp_f32_e64 v122, v122\n" \
"v_mfma_f32_16x16x32_bf16 v[72:75], a[136:139], a[88:91], v[72:75]\n" \
"v_exp_f32_e64 v123, v123\n" \
"v_mfma_f32_16x16x32_bf16 v[72:75], a[132:135], a[84:87], v[72:75]\n" \
"v_exp_f32_e64 v124, v124\n" \
"v_mfma_f32_16x16x32_bf16 v[72:75], a[128:131], a[80:83], v[72:75]\n" \
"buffer_load_dwordx4 v52, s[8:11], s64 offen offset:2048 sc1 lds\n" \
"v_mfma_f32_16x16x32_bf16 v[80:83], a[128:131], a[96:99], v[40:43]\n" \
"v_exp_f32_e64 v125, v125\n" \
"v_mfma_f32_16x16x32_bf16 v[80:83], a[132:135], a[100:103], v[80:83]\n" \
"v_exp_f32_e64 v126, v126\n" \
"v_mfma_f32_16x16x32_bf16 v[80:83], a[136:139], a[104:107], v[80:83]\n" \
"v_exp_f32_e64 v127, v127\n" \
"v_mfma_f32_16x16x32_bf16 v[80:83], a[140:143], a[108:111], v[80:83]\n" \
"v_exp_f32_e64 v70, v70\n" \
"v_mfma_f32_16x16x32_bf16 v[84:87], a[156:159], a[108:111], v[40:43]\n" \
"v_exp_f32_e64 v71, v71\n" \
"v_mfma_f32_16x16x32_bf16 v[84:87], a[152:155], a[104:107], v[84:87]\n" \
"v_cvt_pk_bf16_f32 v120, v120, v121\n" \
"v_cvt_pk_bf16_f32 v121, v122, v123\n" \
"v_mfma_f32_16x16x32_bf16 v[84:87], a[148:151], a[100:103], v[84:87]\n" \
"buffer_load_dwordx4 v52, s[8:11], s64 offen offset:3072 sc1 lds\n" \
"v_mfma_f32_16x16x32_bf16 v[84:87], a[144:147], a[96:99], v[84:87]\n" \
"s_mov_b32 m0, s65\n" \
"s_nop 0\n" \
"v_cvt_pk_bf16_f32 v122, v124, v125\n" \
"v_mfma_f32_16x16x32_bf16 v[92:95], a[144:147], a[112:115], v[44:47]\n" \
"v_cvt_pk_bf16_f32 v123, v126, v127\n" \
"ds_read_b128 a[144:147], v48 offset:12288\n" \
"v_mfma_f32_16x16x32_bf16 v[92:95], a[148:151], a[116:119], v[92:95]\n" \
"ds_read_b128 a[148:151], v48 offset:13312\n" \
"v_cvt_pk_bf16_f32 v71, v70, v71\n" \
"v_mfma_f32_16x16x32_bf16 v[92:95], a[152:155], a[120:123], v[92:95]\n" \
"ds_read_b128 a[152:155], v48 offset:14336\n" \
"s_xor_b32 s65, s65, 0x8000\n" \
"v_mfma_f32_16x16x32_bf16 v[92:95], a[156:159], a[124:127], v[92:95]\n" \
"v_exp_f32_e64 v72, v72\n" \
"v_mfma_f32_16x16x32_bf16 v[88:91], a[140:143], a[124:127], v[44:47]\n" \
"ds_read_b128 a[140:143], v48 offset:11264\n" \
"ds_read_b128 a[156:159], v48 offset:15360\n" \
"v_mfma_f32_16x16x32_bf16 v[88:91], a[136:139], a[120:123], v[88:91]\n" \
"v_exp_f32_e64 v73, v73\n" \
"v_mfma_f32_16x16x32_bf16 v[88:91], a[132:135], a[116:119], v[88:91]\n" \
".fill (L_ab + 8192 - .) / 4, 4, 0xBF800000\n" \
"L_ap2:\n" \
"s_cbranch_execz L_ap0\n" \
"buffer_load_dwordx4 v53, s[8:11], s64 offen offset:0 sc1 lds\n" \
"v_mfma_f32_16x16x32_bf16 v[88:91], a[128:131], a[112:115], v[88:91]\n" \
"ds_read_b128 a[128:131], v48 offset:8192\n" \
"s_waitcnt lgkmcnt(6)\n" \
"v_mfma_f32_16x16x32_bf16 v[128:131], a[160:163], v[100:103], v[128:131]\n" \
"ds_read_b128 a[132:135], v48 offset:9216\n" \
"ds_read_b128 a[136:139], v48 offset:10240\n" \
"v_mfma_f32_16x16x32_bf16 v[160:163], a[160:163], v[104:107], v[160:163]\n" \
"v_exp_f32_e64 v68, v68\n" \
"v_mfma_f32_16x16x32_bf16 v[132:135], a[164:167], v[100:103], v[132:135]\n" \
"v_exp_f32_e64 v69, v69\n" \
"v_mfma_f32_16x16x32_bf16 v[164:167], a[164:167], v[104:107], v[164:167]\n" \
"v_exp_f32_e64 v74, v74\n" \
"v_mfma_f32_16x16x32_bf16 v[136:139], a[168:171], v[100:103], v[136:139]\n" \
"v_exp_f32_e64 v75, v75\n" \
"v_mfma_f32_16x16x32_bf16 v[168:171], a[168:171], v[104:107], v[168:171]\n" \
"buffer_load_dwordx4 v53, s[8:11], s64 offen offset:1024 sc1 lds\n" \
"v_mfma_f32_16x16x32_bf16 v[140:143], a[172:175], v[100:103], v[140:143]\n" \
"v_exp_f32_e64 v66, v66\n" \
"v_mfma_f32_16x16x32_bf16 v[172:175], a[172:175], v[104:107], v[172:175]\n" \
"v_exp_f32_e64 v67, v67\n" \
"v_mfma_f32_16x16x32_bf16 v[144:147], a[176:179], v[100:103], v[144:147]\n" \
"v_exp_f32_e64 v64, v64\n" \
"v_mfma_f32_16x16x32_bf16 v[176:179], a[176:179], v[104:107], v[176:179]\n" \
"v_exp_f32_e64 v65, v65\n" \
"v_mfma_f32_16x16x32_bf16 v[148:151], a[180:183], v[100:103], v[148:151]\n" \
"v_cvt_pk_bf16_f32 v72, v72, v73\n" \
"v_cvt_pk_bf16_f32 v70, v68, v69\n" \
"v_mfma_f32_16x16x32_bf16 v[180:183], a[180:183], v[104:107], v[180:183]\n" \
"v_exp_f32_e64 v76, v76\n" \
"v_mfma_f32_16x16x32_bf16 v[152:155], a[184:187], v[100:103], v[152:155]\n" \
"v_exp_f32_e64 v77, v77\n" \
"v_mfma_f32_16x16x32_bf16 v[184:187], a[184:187], v[104:107], v[184:187]\n" \
"buffer_load_dwordx4 v53, s[8:11], s64 offen offset:2048 sc1 lds\n" \
"v_mfma_f32_16x16x32_bf16 v[156:159], a[188:191], v[100:103], v[156:159]\n" \
"v_exp_f32_e64 v78, v78\n" \
"v_mfma_f32_16x16x32_bf16 v[188:191], a[188:191], v[104:107], v[188:191]\n" \
"v_exp_f32_e64 v79, v79\n" \
"v_smfmac_f32_16x16x64_bf16 a[192:195], a[200:203], v[100:107], v21\n" \
"v_cvt_pk_bf16_f32 v73, v74, v75\n" \
"v_cvt_pk_bf16_f32 v69, v66, v67\n" \
"v_mfma_f32_16x16x32_bf16 v[220:223], a[188:191], v[116:119], v[220:223]\n" \
"v_cvt_pk_bf16_f32 v68, v64, v65\n" \
"v_cvt_pk_bf16_f32 v74, v76, v77\n" \
"v_mfma_f32_16x16x32_bf16 v[252:255], a[188:191], v[120:123], v[252:255]\n" \
"v_cvt_pk_bf16_f32 v75, v78, v79\n" \
"v_xor_b32_e32 v48, 0x8000, v48\n" \
"v_mfma_f32_16x16x32_bf16 v[216:219], a[184:187], v[116:119], v[216:219]\n" \
"v_mfma_f32_16x16x32_bf16 v[248:251], a[184:187], v[120:123], v[248:251]\n" \
"buffer_load_dwordx4 v53, s[8:11], s64 offen offset:3072 sc1 lds\n" \
"v_mfma_f32_16x16x32_bf16 v[212:215], a[180:183], v[116:119], v[212:215]\n" \
"s_add_u32 s64, s64, 0x10000\n" \
"s_sub_u32 s66, s66, 1\n" \
"s_nop 0\n" \
"v_mfma_f32_16x16x32_bf16 v[244:247], a[180:183], v[120:123], v[244:247]\n" \
"v_mfma_f32_16x16x32_bf16 v[208:211], a[176:179], v[116:119], v[208:211]\n" \
"v_mfma_f32_16x16x32_bf16 v[240:243], a[176:179], v[120:123], v[240:243]\n" \
"v_mfma_f32_16x16x32_bf16 v[204:207], a[172:175], v[116:119], v[204:207]\n" \
"v_mfma_f32_16x16x32_bf16 v[236:239], a[172:175], v[120:123], v[236:239]\n" \
"v_mfma_f32_16x16x32_bf16 v[200:203], a[168:171], v[116:119], v[200:203]\n" \
"v_mfma_f32_16x16x32_bf16 v[232:235], a[168:171], v[120:123], v[232:235]\n" \
"v_mfma_f32_16x16x32_bf16 v[196:199], a[164:167], v[116:119], v[196:199]\n" \
"v_mfma_f32_16x16x32_bf16 v[228:231], a[164:167], v[120:123], v[228:231]\n" \
"v_mfma_f32_16x16x32_bf16 v[192:195], a[160:163], v[116:119], v[192:195]\n" \
"v_mfma_f32_16x16x32_bf16 v[224:227], a[160:163], v[120:123], v[224:227]\n" \
"s_cbranch_scc0 L_tL_2_%=\n" \
"s_waitcnt vmcnt(8) lgkmcnt(0)\n" \
"v_smfmac_f32_16x16x64_bf16 a[196:199], a[200:203], v[116:123], v21\n" \
"s_barrier\n" \
"L_la_%=:\n" \
"v_mfma_f32_16x16x32_bf16 v[96:99], a[128:131], a[64:67], v[32:35]\n" \
"v_readlane_b32 s96, v11, 1\n" \
"v_lshrrev_b32_e32 v16, 1, v20\n" \
"v_mfma_f32_16x16x32_bf16 v[96:99], a[132:135], a[68:71], v[96:99]\n" \
"s_xor_b32 m0, s65, 0x12000\n" \
"ds_read_b128 a[160:163], v49 offset:0\n" \
"v_mfma_f32_16x16x32_bf16 v[96:99], a[136:139], a[72:75], v[96:99]\n" \
"buffer_load_dwordx2 v[26:27], v16, s[56:59], s96 offen sc1\n" \
"v_mfma_f32_16x16x32_bf16 v[96:99], a[140:143], a[76:79], v[96:99]\n" \
"buffer_load_dwordx4 v50, s[8:11], s64 offen offset:0 sc1 lds\n" \
"v_mfma_f32_16x16x32_bf16 v[100:103], a[156:159], a[76:79], v[32:35]\n" \
"ds_read_b128 a[164:167], v49 offset:1024\n" \
"ds_read_b128 a[168:171], v49 offset:2048\n" \
"v_mfma_f32_16x16x32_bf16 v[100:103], a[152:155], a[72:75], v[100:103]\n" \
"ds_read_b128 a[172:175], v49 offset:3072\n" \
"ds_read_b128 a[176:179], v49 offset:4096\n" \
"v_mfma_f32_16x16x32_bf16 v[100:103], a[148:151], a[68:71], v[100:103]\n" \
"ds_read_b128 a[180:183], v49 offset:5120\n" \
"ds_read_b128 a[184:187], v49 offset:6144\n" \
"v_mfma_f32_16x16x32_bf16 v[100:103], a[144:147], a[64:67], v[100:103]\n" \
"ds_read_b128 a[188:191], v49 offset:7168\n" \
"s_mov_b32 s63, s62\n" \
"v_mfma_f32_16x16x32_bf16 v[108:111], a[144:147], a[80:83], v[36:39]\n" \
"v_exp_f32_e32 v86, v86\n" \
"v_mfma_f32_16x16x32_bf16 v[108:111], a[148:151], a[84:87], v[108:111]\n" \
"v_exp_f32_e64 v87, v87\n" \
"v_mfma_f32_16x16x32_bf16 v[108:111], a[152:155], a[88:91], v[108:111]\n" \
"buffer_load_dwordx4 v50, s[8:11], s64 offen offset:1024 sc1 lds\n" \
"v_mfma_f32_16x16x32_bf16 v[108:111], a[156:159], a[92:95], v[108:111]\n" \
"v_exp_f32_e64 v84, v84\n" \
"v_mfma_f32_16x16x32_bf16 v[104:107], a[140:143], a[92:95], v[36:39]\n" \
"v_exp_f32_e64 v85, v85\n" \
"v_mfma_f32_16x16x32_bf16 v[104:107], a[136:139], a[88:91], v[104:107]\n" \
"v_cvt_pk_bf16_f32 v87, v86, v87\n" \
"v_cvt_pk_bf16_f32 v86, v84, v85\n" \
"v_mfma_f32_16x16x32_bf16 v[104:107], a[132:135], a[84:87], v[104:107]\n" \
"v_exp_f32_e64 v82, v82\n" \
"v_mfma_f32_16x16x32_bf16 v[104:107], a[128:131], a[80:83], v[104:107]\n" \
"v_exp_f32_e64 v83, v83\n" \
"v_mfma_f32_16x16x32_bf16 v[112:115], a[128:131], a[96:99], v[40:43]\n" \
"v_exp_f32_e64 v80, v80\n" \
"v_mfma_f32_16x16x32_bf16 v[112:115], a[132:135], a[100:103], v[112:115]\n" \
"v_exp_f32_e64 v81, v81\n" \
"v_mfma_f32_16x16x32_bf16 v[112:115], a[136:139], a[104:107], v[112:115]\n" \
"buffer_load_dwordx4 v50, s[8:11], s64 offen offset:2048 sc1 lds\n" \
"v_mfma_f32_16x16x32_bf16 v[112:115], a[140:143], a[108:111], v[112:115]\n" \
"v_cvt_pk_bf16_f32 v85, v82, v83\n" \
"v_cvt_pk_bf16_f32 v84, v80, v81\n" \
"v_mfma_f32_16x16x32_bf16 v[116:119], a[156:159], a[108:111], v[40:43]\n" \
"v_exp_f32_e64 v88, v88\n" \
"v_mfma_f32_16x16x32_bf16 v[116:119], a[152:155], a[104:107], v[116:119]\n" \
"v_exp_f32_e64 v89, v89\n" \
"v_mfma_f32_16x16x32_bf16 v[116:119], a[148:151], a[100:103], v[116:119]\n" \
"v_exp_f32_e64 v90, v90\n" \
"v_mfma_f32_16x16x32_bf16 v[116:119], a[144:147], a[96:99], v[116:119]\n" \
"v_exp_f32_e64 v91, v91\n" \
"v_mfma_f32_16x16x32_bf16 v[124:127], a[144:147], a[112:115], v[44:47]\n" \
"v_cvt_pk_bf16_f32 v88, v88, v89\n" \
"v_cvt_pk_bf16_f32 v89, v90, v91\n" \
"v_mfma_f32_16x16x32_bf16 v[124:127], a[148:151], a[116:119], v[124:127]\n" \
"buffer_load_dwordx4 v50, s[8:11], s64 offen offset:3072 sc1 lds\n" \
"v_mfma_f32_16x16x32_bf16 v[124:127], a[152:155], a[120:123], v[124:127]\n" \
"v_exp_f32_e64 v92, v92\n" \
"v_mfma_f32_16x16x32_bf16 v[124:127], a[156:159], a[124:127], v[124:127]\n" \
"v_exp_f32_e64 v93, v93\n" \
"v_mfma_f32_16x16x32_bf16 v[120:123], a[140:143], a[124:127], v[44:47]\n" \
"v_exp_f32_e64 v94, v94\n" \
"v_mfma_f32_16x16x32_bf16 v[120:123], a[136:139], a[120:123], v[120:123]\n" \
"v_exp_f32_e64 v95, v95\n" \
"v_mfma_f32_16x16x32_bf16 v[120:123], a[132:135], a[116:119], v[120:123]\n" \
"v_cvt_pk_bf16_f32 v90, v92, v93\n" \
"v_cvt_pk_bf16_f32 v91, v94, v95\n" \
"v_mfma_f32_16x16x32_bf16 v[120:123], a[128:131], a[112:115], v[120:123]\n" \
"v_readlane_b32 s97, v11, 36\n" \
"s_waitcnt lgkmcnt(0)\n" \
"s_nop 0\n" \
"v_mfma_f32_16x16x32_bf16 v[128:131], a[160:163], v[68:71], v[128:131]\n" \
"ds_read_b128 a[128:131], v48 offset:0\n" \
"ds_read_b128 a[144:147], v48 offset:4096\n" \
"v_mfma_f32_16x16x32_bf16 v[160:163], a[160:163], v[72:75], v[160:163]\n" \
"ds_read_b128 a[132:135], v48 offset:1024\n" \
"ds_read_b128 a[148:151], v48 offset:5120\n" \
"v_mfma_f32_16x16x32_bf16 v[132:135], a[164:167], v[68:71], v[132:135]\n" \
"ds_read_b128 a[136:139], v48 offset:2048\n" \
"ds_read_b128 a[152:155], v48 offset:6144\n" \
"v_mfma_f32_16x16x32_bf16 v[164:167], a[164:167], v[72:75], v[164:167]\n" \
"ds_read_b128 a[140:143], v48 offset:3072\n" \
"ds_read_b128 a[156:159], v48 offset:7168\n" \
"v_mfma_f32_16x16x32_bf16 v[136:139], a[168:171], v[68:71], v[136:139]\n" \
"v_add_u32_e32 v17, 0x49000, v20\n" \
"v_readlane_b32 s98, v11, 37\n" \
"v_mfma_f32_16x16x32_bf16 v[168:171], a[168:171], v[72:75], v[168:171]\n" \
"v_readlane_b32 s93, v11, 20\n" \
"v_readlane_b32 s84, v11, 15\n" \
"v_mfma_f32_16x16x32_bf16 v[140:143], a[172:175], v[68:71], v[140:143]\n" \
"v_readlane_b32 s85, v11, 16\n" \
"v_readlane_b32 s86, v11, 17\n" \
"v_mfma_f32_16x16x32_bf16 v[172:175], a[172:175], v[72:75], v[172:175]\n" \
"s_mov_b32 s87, 0x20000\n" \
"v_readlane_b32 s92, v11, 18\n" \
"v_mfma_f32_16x16x32_bf16 v[144:147], a[176:179], v[68:71], v[144:147]\n" \
"v_exp_f32_e64 v102, v102\n" \
"v_mfma_f32_16x16x32_bf16 v[176:179], a[176:179], v[72:75], v[176:179]\n" \
"v_exp_f32_e64 v103, v103\n" \
"v_mfma_f32_16x16x32_bf16 v[148:151], a[180:183], v[68:71], v[148:151]\n" \
"v_exp_f32_e64 v100, v100\n" \
"v_mfma_f32_16x16x32_bf16 v[180:183], a[180:183], v[72:75], v[180:183]\n" \
"v_exp_f32_e64 v101, v101\n" \
"v_mfma_f32_16x16x32_bf16 v[152:155], a[184:187], v[68:71], v[152:155]\n" \
"v_cvt_pk_bf16_f32 v103, v102, v103\n" \
"v_cvt_pk_bf16_f32 v102, v100, v101\n" \
"v_mfma_f32_16x16x32_bf16 v[184:187], a[184:187], v[72:75], v[184:187]\n" \
"v_exp_f32_e64 v98, v98\n" \
"v_mfma_f32_16x16x32_bf16 v[156:159], a[188:191], v[68:71], v[156:159]\n" \
"v_exp_f32_e64 v99, v99\n" \
"v_mfma_f32_16x16x32_bf16 v[188:191], a[188:191], v[72:75], v[188:191]\n" \
"s_waitcnt vmcnt(4)\n" \
"v_cmp_ne_u64_e32 vcc, s[62:63], v[26:27]\n" \
"v_smfmac_f32_16x16x64_bf16 a[192:195], a[200:203], v[68:75], v21\n" \
"v_exp_f32_e64 v96, v96\n" \
"v_mfma_f32_16x16x32_bf16 v[220:223], a[188:191], v[84:87], v[220:223]\n" \
"v_exp_f32_e64 v97, v97\n" \
"v_mfma_f32_16x16x32_bf16 v[252:255], a[188:191], v[88:91], v[252:255]\n" \
"s_cbranch_vccnz L_sol_%=\n" \
"L_rol_%=:\n" \
"v_cvt_pk_bf16_f32 v101, v98, v99\n" \
"buffer_load_dwordx4 a[220:223], v20, s[56:59], s97 offen offset:0 sc1\n" \
"v_mfma_f32_16x16x32_bf16 v[216:219], a[184:187], v[84:87], v[216:219]\n" \
"v_cvt_pk_bf16_f32 v100, v96, v97\n" \
"v_readlane_b32 s91, v11, 21\n" \
"v_mfma_f32_16x16x32_bf16 v[248:251], a[184:187], v[88:91], v[248:251]\n" \
"v_exp_f32_e32 v104, v104\n" \
"v_mfma_f32_16x16x32_bf16 v[212:215], a[180:183], v[84:87], v[212:215]\n" \
"v_exp_f32_e64 v105, v105\n" \
"v_mfma_f32_16x16x32_bf16 v[244:247], a[180:183], v[88:91], v[244:247]\n" \
"v_exp_f32_e64 v106, v106\n" \
"v_mfma_f32_16x16x32_bf16 v[208:211], a[176:179], v[84:87], v[208:211]\n" \
"v_exp_f32_e64 v107, v107\n" \
"v_mfma_f32_16x16x32_bf16 v[240:243], a[176:179], v[88:91], v[240:243]\n" \
"buffer_load_dwordx4 a[228:231], v17, s[56:59], s97 offen offset:0 sc1\n" \
"v_mfma_f32_16x16x32_bf16 v[204:207], a[172:175], v[84:87], v[204:207]\n" \
"v_cvt_pk_bf16_f32 v104, v104, v105\n" \
"v_cvt_pk_bf16_f32 v105, v106, v107\n" \
"v_mfma_f32_16x16x32_bf16 v[236:239], a[172:175], v[88:91], v[236:239]\n" \
"v_exp_f32_e64 v108, v108\n" \
"v_mfma_f32_16x16x32_bf16 v[200:203], a[168:171], v[84:87], v[200:203]\n" \
"v_exp_f32_e64 v109, v109\n" \
"v_mfma_f32_16x16x32_bf16 v[232:235], a[168:171], v[88:91], v[232:235]\n" \
"v_exp_f32_e64 v110, v110\n" \
"v_mfma_f32_16x16x32_bf16 v[196:199], a[164:167], v[84:87], v[196:199]\n" \
"v_exp_f32_e64 v111, v111\n" \
"v_mfma_f32_16x16x32_bf16 v[228:231], a[164:167], v[88:91], v[228:231]\n" \
"v_cvt_pk_bf16_f32 v106, v108, v109\n" \
"v_cvt_pk_bf16_f32 v107, v110, v111\n" \
"v_mfma_f32_16x16x32_bf16 v[192:195], a[160:163], v[84:87], v[192:195]\n" \
"v_exp_f32_e64 v118, v118\n" \
"v_mfma_f32_16x16x32_bf16 v[224:227], a[160:163], v[88:91], v[224:227]\n" \
"s_xor_b32 s65, s65, 0x8000\n" \
"s_waitcnt vmcnt(7) lgkmcnt(0)\n" \
"v_smfmac_f32_16x16x64_bf16 a[196:199], a[200:203], v[84:91], v21\n" \
"s_barrier\n" \
"v_mfma_f32_16x16x32_bf16 v[64:67], a[128:131], a[64:67], v[32:35]\n" \
"buffer_load_dwordx4 a[236:239], v20, s[56:59], s98 offen offset:0 sc1\n" \
"v_mfma_f32_16x16x32_bf16 v[64:67], a[132:135], a[68:71], v[64:67]\n" \
"s_add_u32 m0, s93, 0x0\n" \
"s_nop 0\n" \
"ds_read_b128 a[160:163], v49 offset:8192\n" \
"v_mfma_f32_16x16x32_bf16 v[64:67], a[136:139], a[72:75], v[64:67]\n" \
"buffer_load_dwordx4 v0, s[84:87], s92 offen offset:0 lds\n" \
"v_mfma_f32_16x16x32_bf16 v[64:67], a[140:143], a[76:79], v[64:67]\n" \
"ds_read_b128 a[164:167], v49 offset:9216\n" \
"ds_read_b128 a[168:171], v49 offset:10240\n" \
"v_mfma_f32_16x16x32_bf16 v[68:71], a[156:159], a[76:79], v[32:35]\n" \
"ds_read_b128 a[172:175], v49 offset:11264\n" \
"ds_read_b128 a[176:179], v49 offset:12288\n" \
"v_mfma_f32_16x16x32_bf16 v[68:71], a[152:155], a[72:75], v[68:71]\n" \
"ds_read_b128 a[180:183], v49 offset:13312\n" \
"ds_read_b128 a[184:187], v49 offset:14336\n" \
"v_mfma_f32_16x16x32_bf16 v[68:71], a[148:151], a[68:71], v[68:71]\n" \
"buffer_load_dwordx4 v1, s[84:87], s92 offen offset:1024 lds\n" \
"v_mfma_f32_16x16x32_bf16 v[68:71], a[144:147], a[64:67], v[68:71]\n" \
"buffer_load_dwordx4 a[244:247], v17, s[56:59], s98 offen offset:0 sc1\n" \
"v_mfma_f32_16x16x32_bf16 v[76:79], a[144:147], a[80:83], v[36:39]\n" \
"ds_read_b128 a[188:191], v49 offset:15360\n" \
"v_add_u32_e32 v49, 0x10000, v48\n" \
"v_mfma_f32_16x16x32_bf16 v[76:79], a[148:151], a[84:87], v[76:79]\n" \
"v_exp_f32_e64 v119, v119\n" \
"v_mfma_f32_16x16x32_bf16 v[76:79], a[152:155], a[88:91], v[76:79]\n" \
"v_exp_f32_e64 v116, v116\n" \
"v_mfma_f32_16x16x32_bf16 v[76:79], a[156:159], a[92:95], v[76:79]\n" \
"v_exp_f32_e64 v117, v117\n" \
"v_mfma_f32_16x16x32_bf16 v[72:75], a[140:143], a[92:95], v[36:39]\n" \
"v_cvt_pk_bf16_f32 v119, v118, v119\n" \
"v_cvt_pk_bf16_f32 v118, v116, v117\n" \
"v_mfma_f32_16x16x32_bf16 v[72:75], a[136:139], a[88:91], v[72:75]\n" \
"buffer_load_dwordx4 v2, s[84:87], s92 offen offset:2048 lds\n" \
"v_mfma_f32_16x16x32_bf16 v[72:75], a[132:135], a[84:87], v[72:75]\n" \
"v_exp_f32_e64 v114, v114\n" \
"v_mfma_f32_16x16x32_bf16 v[72:75], a[128:131], a[80:83], v[72:75]\n" \
"v_exp_f32_e64 v115, v115\n" \
"v_mfma_f32_16x16x32_bf16 v[80:83], a[128:131], a[96:99], v[40:43]\n" \
"buffer_load_dwordx4 a[224:227], v20, s[56:59], s97 offen offset:1024 sc1\n" \
"v_mfma_f32_16x16x32_bf16 v[80:83], a[132:135], a[100:103], v[80:83]\n" \
"v_exp_f32_e64 v112, v112\n" \
"v_mfma_f32_16x16x32_bf16 v[80:83], a[136:139], a[104:107], v[80:83]\n" \
"v_exp_f32_e64 v113, v113\n" \
"v_mfma_f32_16x16x32_bf16 v[80:83], a[140:143], a[108:111], v[80:83]\n" \
"v_cvt_pk_bf16_f32 v117, v114, v115\n" \
"v_cvt_pk_bf16_f32 v116, v112, v113\n" \
"v_mfma_f32_16x16x32_bf16 v[84:87], a[156:159], a[108:111], v[40:43]\n" \
"buffer_load_dwordx4 v3, s[84:87], s92 offen offset:3072 lds\n" \
"v_mfma_f32_16x16x32_bf16 v[84:87], a[152:155], a[104:107], v[84:87]\n" \
"s_add_u32 m0, s91, 0x1000\n" \
"s_add_u32 s64, s64, 0x10000\n" \
"v_mfma_f32_16x16x32_bf16 v[84:87], a[148:151], a[100:103], v[84:87]\n" \
"v_exp_f32_e64 v120, v120\n" \
"v_mfma_f32_16x16x32_bf16 v[84:87], a[144:147], a[96:99], v[84:87]\n" \
"v_exp_f32_e64 v121, v121\n" \
"v_mfma_f32_16x16x32_bf16 v[92:95], a[144:147], a[112:115], v[44:47]\n" \
"v_exp_f32_e64 v122, v122\n" \
"v_mfma_f32_16x16x32_bf16 v[92:95], a[148:151], a[116:119], v[92:95]\n" \
"buffer_load_dwordx4 a[232:235], v17, s[56:59], s97 offen offset:1024 sc1\n" \
"v_mfma_f32_16x16x32_bf16 v[92:95], a[152:155], a[120:123], v[92:95]\n" \
"buffer_load_dwordx4 v4, s[84:87], s92 offen offset:0 lds\n" \
"v_mfma_f32_16x16x32_bf16 v[92:95], a[156:159], a[124:127], v[92:95]\n" \
"v_exp_f32_e64 v123, v123\n" \
"v_mfma_f32_16x16x32_bf16 v[88:91], a[140:143], a[124:127], v[44:47]\n" \
"v_cvt_pk_bf16_f32 v120, v120, v121\n" \
"v_cvt_pk_bf16_f32 v121, v122, v123\n" \
"v_mfma_f32_16x16x32_bf16 v[88:91], a[136:139], a[120:123], v[88:91]\n" \
"v_exp_f32_e64 v124, v124\n" \
"v_mfma_f32_16x16x32_bf16 v[88:91], a[132:135], a[116:119], v[88:91]\n" \
"v_exp_f32_e64 v125, v125\n" \
"v_mfma_f32_16x16x32_bf16 v[88:91], a[128:131], a[112:115], v[88:91]\n" \
"v_cvt_pk_bf16_f32 v122, v124, v125\n" \
"s_waitcnt lgkmcnt(0)\n" \
"v_mfma_f32_16x16x32_bf16 v[128:131], a[160:163], v[100:103], v[128:131]\n" \
"v_exp_f32_e32 v126, v126\n" \
"v_mfma_f32_16x16x32_bf16 v[160:163], a[160:163], v[104:107], v[160:163]\n" \
"buffer_load_dwordx4 v5, s[84:87], s92 offen offset:1024 lds\n" \
"v_mfma_f32_16x16x32_bf16 v[132:135], a[164:167], v[100:103], v[132:135]\n" \
"buffer_load_dwordx4 a[240:243], v20, s[56:59], s98 offen offset:1024 sc1\n" \
"v_mfma_f32_16x16x32_bf16 v[164:167], a[164:167], v[104:107], v[164:167]\n" \
"v_exp_f32_e64 v127, v127\n" \
"v_mfma_f32_16x16x32_bf16 v[136:139], a[168:171], v[100:103], v[136:139]\n" \
"v_cvt_pk_bf16_f32 v123, v126, v127\n" \
"ds_read_b128 a[128:131], v48 offset:8192\n" \
"v_mfma_f32_16x16x32_bf16 v[168:171], a[168:171], v[104:107], v[168:171]\n" \
"ds_read_b128 a[144:147], v48 offset:12288\n" \
"ds_read_b128 a[132:135], v48 offset:9216\n" \
"v_mfma_f32_16x16x32_bf16 v[140:143], a[172:175], v[100:103], v[140:143]\n" \
"ds_read_b128 a[148:151], v48 offset:13312\n" \
"ds_read_b128 a[136:139], v48 offset:10240\n" \
"v_mfma_f32_16x16x32_bf16 v[172:175], a[172:175], v[104:107], v[172:175]\n" \
"ds_read_b128 a[152:155], v48 offset:14336\n" \
"ds_read_b128 a[140:143], v48 offset:11264\n" \
"v_mfma_f32_16x16x32_bf16 v[144:147], a[176:179], v[100:103], v[144:147]\n" \
"buffer_load_dwordx4 v6, s[84:87], s92 offen offset:2048 lds\n" \
"v_mfma_f32_16x16x32_bf16 v[176:179], a[176:179], v[104:107], v[176:179]\n" \
"v_exp_f32_e64 v70, v70\n" \
"v_mfma_f32_16x16x32_bf16 v[148:151], a[180:183], v[100:103], v[148:151]\n" \
"v_exp_f32_e64 v71, v71\n" \
"v_mfma_f32_16x16x32_bf16 v[180:183], a[180:183], v[104:107], v[180:183]\n" \
"buffer_load_dwordx4 a[248:251], v17, s[56:59], s98 offen offset:1024 sc1\n" \
"v_mfma_f32_16x16x32_bf16 v[152:155], a[184:187], v[100:103], v[152:155]\n" \
"ds_read_b128 a[156:159], v48 offset:15360\n" \
"v_cvt_pk_bf16_f32 v71, v70, v71\n" \
"v_mfma_f32_16x16x32_bf16 v[184:187], a[184:187], v[104:107], v[184:187]\n" \
"v_exp_f32_e64 v72, v72\n" \
"v_mfma_f32_16x16x32_bf16 v[156:159], a[188:191], v[100:103], v[156:159]\n" \
"buffer_load_dwordx4 v7, s[84:87], s92 offen offset:3072 lds\n" \
"v_mfma_f32_16x16x32_bf16 v[188:191], a[188:191], v[104:107], v[188:191]\n" \
"s_add_u32 m0, s91, 0x2000\n" \
"v_xor_b32_e32 v48, 0x8000, v48\n" \
"v_smfmac_f32_16x16x64_bf16 a[192:195], a[200:203], v[100:107], v21\n" \
"v_exp_f32_e64 v73, v73\n" \
"v_mfma_f32_16x16x32_bf16 v[220:223], a[188:191], v[116:119], v[220:223]\n" \
"v_exp_f32_e64 v68, v68\n" \
"v_mfma_f32_16x16x32_bf16 v[252:255], a[188:191], v[120:123], v[252:255]\n" \
"v_exp_f32_e64 v69, v69\n" \
"v_mfma_f32_16x16x32_bf16 v[216:219], a[184:187], v[116:119], v[216:219]\n" \
"v_exp_f32_e64 v74, v74\n" \
"v_mfma_f32_16x16x32_bf16 v[248:251], a[184:187], v[120:123], v[248:251]\n" \
"v_exp_f32_e64 v75, v75\n" \
"v_mfma_f32_16x16x32_bf16 v[212:215], a[180:183], v[116:119], v[212:215]\n" \
"buffer_load_dwordx4 v8, s[84:87], s92 offen offset:0 lds\n" \
"v_mfma_f32_16x16x32_bf16 v[244:247], a[180:183], v[120:123], v[244:247]\n" \
"v_exp_f32_e64 v66, v66\n" \
"v_mfma_f32_16x16x32_bf16 v[208:211], a[176:179], v[116:119], v[208:211]\n" \
"v_exp_f32_e64 v67, v67\n" \
"v_mfma_f32_16x16x32_bf16 v[240:243], a[176:179], v[120:123], v[240:243]\n" \
"v_exp_f32_e64 v64, v64\n" \
"v_mfma_f32_16x16x32_bf16 v[204:207], a[172:175], v[116:119], v[204:207]\n" \
"v_exp_f32_e64 v65, v65\n" \
"v_mfma_f32_16x16x32_bf16 v[236:239], a[172:175], v[120:123], v[236:239]\n" \
"v_cvt_pk_bf16_f32 v72, v72, v73\n" \
"v_cvt_pk_bf16_f32 v70, v68, v69\n" \
"v_mfma_f32_16x16x32_bf16 v[200:203], a[168:171], v[116:119], v[200:203]\n" \
"v_exp_f32_e64 v76, v76\n" \
"v_mfma_f32_16x16x32_bf16 v[232:235], a[168:171], v[120:123], v[232:235]\n" \
"v_exp_f32_e64 v77, v77\n" \
"v_mfma_f32_16x16x32_bf16 v[196:199], a[164:167], v[116:119], v[196:199]\n" \
"v_exp_f32_e64 v78, v78\n" \
"v_mfma_f32_16x16x32_bf16 v[228:231], a[164:167], v[120:123], v[228:231]\n" \
"v_exp_f32_e64 v79, v79\n" \
"v_mfma_f32_16x16x32_bf16 v[192:195], a[160:163], v[116:119], v[192:195]\n" \
"v_cvt_pk_bf16_f32 v73, v74, v75\n" \
"v_cvt_pk_bf16_f32 v69, v66, v67\n" \
"v_mfma_f32_16x16x32_bf16 v[224:227], a[160:163], v[120:123], v[224:227]\n" \
"v_cvt_pk_bf16_f32 v68, v64, v65\n" \
"s_waitcnt vmcnt(15) lgkmcnt(0)\n" \
"v_smfmac_f32_16x16x64_bf16 a[196:199], a[200:203], v[116:123], v21\n" \
"s_barrier\n" \
"v_cvt_pk_bf16_f32 v74, v76, v77\n" \
"v_cvt_pk_bf16_f32 v75, v78, v79\n" \
"s_cmp_lg_u32 s16, 0\n" \
"s_cbranch_scc1 L_f1_%=\n" \
"ds_read_b128 a[160:163], v49 offset:0\n" \
"ds_read_b128 a[164:167], v49 offset:1024\n" \
"ds_read_b128 a[168:171], v49 offset:2048\n" \
"ds_read_b128 a[172:175], v49 offset:3072\n" \
"ds_read_b128 a[176:179], v49 offset:4096\n" \
"ds_read_b128 a[180:183], v49 offset:5120\n" \
"ds_read_b128 a[184:187], v49 offset:6144\n" \
"ds_read_b128 a[188:191], v49 offset:7168\n" \
"v_add_u32_e32 v54, 0x49000, v20\n" \
"buffer_load_dwordx4 a[96:99], v20, s[56:59], s60 offen offset:0 sc1\n" \
"v_add_u32_e32 v50, s15, v20\n" \
"v_add_u32_e32 v51, 0x10000, v50\n" \
"ds_read_b32 v60, v18 offset:0\n" \
"ds_read_b32 v61, v18 offset:128\n" \
"ds_read_b128 a[0:3], v49 offset:8192\n" \
"ds_read_b128 a[4:7], v49 offset:9216\n" \
"ds_read_b128 a[8:11], v49 offset:10240\n" \
"ds_read_b128 a[12:15], v49 offset:11264\n" \
"ds_read_b128 a[16:19], v49 offset:12288\n" \
"ds_read_b128 a[20:23], v49 offset:13312\n" \
"ds_read_b128 a[24:27], v49 offset:14336\n" \
"ds_read_b128 a[28:31], v49 offset:15360\n" \
"v_exp_f32_e32 v86, v86\n" \
"v_exp_f32_e32 v87, v87\n" \
"v_exp_f32_e32 v84, v84\n" \
"v_exp_f32_e32 v85, v85\n" \
"v_cvt_pk_bf16_f32 v87, v86, v87\n" \
"buffer_load_dwordx4 a[100:103], v20, s[56:59], s60 offen offset:1024 sc1\n" \
"v_exp_f32_e32 v82, v82\n" \
"v_exp_f32_e32 v83, v83\n" \
"v_exp_f32_e32 v80, v80\n" \
"v_exp_f32_e32 v81, v81\n" \
"s_waitcnt lgkmcnt(15)\n" \
"v_mfma_f32_16x16x32_bf16 v[128:131], a[160:163], v[68:71], v[128:131]\n" \
"v_cvt_pk_bf16_f32 v86, v84, v85\n" \
"v_cvt_pk_bf16_f32 v85, v82, v83\n" \
"v_mfma_f32_16x16x32_bf16 v[160:163], a[160:163], v[72:75], v[160:163]\n" \
"v_cvt_pk_bf16_f32 v84, v80, v81\n" \
"v_and_b32_e32 v84, v12, v84\n" \
"v_mfma_f32_16x16x32_bf16 v[132:135], a[164:167], v[68:71], v[132:135]\n" \
"v_and_b32_e32 v85, v13, v85\n" \
"v_and_b32_e32 v86, v14, v86\n" \
"v_mfma_f32_16x16x32_bf16 v[164:167], a[164:167], v[72:75], v[164:167]\n" \
"buffer_load_dwordx4 a[104:107], v20, s[56:59], s60 offen offset:2048 sc1\n" \
"v_mfma_f32_16x16x32_bf16 v[136:139], a[168:171], v[68:71], v[136:139]\n" \
"v_cvt_pk_bf16_f32 v128, v128, v129\n" \
"v_cvt_pk_bf16_f32 v160, v160, v161\n" \
"v_mfma_f32_16x16x32_bf16 v[168:171], a[168:171], v[72:75], v[168:171]\n" \
"v_cvt_pk_bf16_f32 v129, v130, v131\n" \
"s_waitcnt lgkmcnt(10)\n" \
"v_mfma_f32_16x16x32_bf16 v[140:143], a[172:175], v[68:71], v[140:143]\n" \
"v_cvt_pk_bf16_f32 v161, v162, v163\n" \
"v_cvt_pk_bf16_f32 v130, v132, v133\n" \
"v_mfma_f32_16x16x32_bf16 v[172:175], a[172:175], v[72:75], v[172:175]\n" \
"v_cvt_pk_bf16_f32 v131, v134, v135\n" \
"v_cvt_pk_bf16_f32 v162, v164, v165\n" \
"v_mfma_f32_16x16x32_bf16 v[144:147], a[176:179], v[68:71], v[144:147]\n" \
"v_cvt_pk_bf16_f32 v163, v166, v167\n" \
"v_cvt_pk_bf16_f32 v132, v136, v137\n" \
"v_mfma_f32_16x16x32_bf16 v[176:179], a[176:179], v[72:75], v[176:179]\n" \
"buffer_load_dwordx4 a[108:111], v20, s[56:59], s60 offen offset:3072 sc1\n" \
"v_mfma_f32_16x16x32_bf16 v[148:151], a[180:183], v[68:71], v[148:151]\n" \
"v_cvt_pk_bf16_f32 v133, v138, v139\n" \
"v_cvt_pk_bf16_f32 v134, v140, v141\n" \
"v_mfma_f32_16x16x32_bf16 v[180:183], a[180:183], v[72:75], v[180:183]\n" \
"v_cvt_pk_bf16_f32 v135, v142, v143\n" \
"v_cvt_pk_bf16_f32 v164, v168, v169\n" \
"v_mfma_f32_16x16x32_bf16 v[152:155], a[184:187], v[68:71], v[152:155]\n" \
"v_cvt_pk_bf16_f32 v165, v170, v171\n" \
"v_cvt_pk_bf16_f32 v166, v172, v173\n" \
"v_mfma_f32_16x16x32_bf16 v[184:187], a[184:187], v[72:75], v[184:187]\n" \
"v_cvt_pk_bf16_f32 v167, v174, v175\n" \
"v_cvt_pk_bf16_f32 v136, v144, v145\n" \
"v_mfma_f32_16x16x32_bf16 v[156:159], a[188:191], v[68:71], v[156:159]\n" \
"v_cvt_pk_bf16_f32 v137, v146, v147\n" \
"v_cvt_pk_bf16_f32 v138, v148, v149\n" \
"v_mfma_f32_16x16x32_bf16 v[188:191], a[188:191], v[72:75], v[188:191]\n" \
"v_cvt_pk_bf16_f32 v139, v150, v151\n" \
"v_cvt_pk_bf16_f32 v140, v152, v153\n" \
"v_smfmac_f32_16x16x64_bf16 a[192:195], a[200:203], v[68:75], v21\n" \
"v_cvt_pk_bf16_f32 v141, v154, v155\n" \
"v_cvt_pk_bf16_f32 v168, v176, v177\n" \
"v_mfma_f32_16x16x32_bf16 v[120:123], a[128:131], a[112:115], v[44:47]\n" \
"v_cvt_pk_bf16_f32 v142, v156, v157\n" \
"v_cvt_pk_bf16_f32 v143, v158, v159\n" \
"v_mfma_f32_16x16x32_bf16 v[120:123], a[132:135], a[116:119], v[120:123]\n" \
"v_cvt_pk_bf16_f32 v169, v178, v179\n" \
"v_cvt_pk_bf16_f32 v170, v180, v181\n" \
"v_cvt_pk_bf16_f32 v171, v182, v183\n" \
"v_cvt_pk_bf16_f32 v172, v184, v185\n" \
"v_cvt_pk_bf16_f32 v173, v186, v187\n" \
"v_cvt_pk_bf16_f32 v174, v188, v189\n" \
"v_cvt_pk_bf16_f32 v175, v190, v191\n" \
"ds_write_b128 v50, v[128:131] offset:0\n" \
"v_mfma_f32_16x16x32_bf16 v[120:123], a[136:139], a[120:123], v[120:123]\n" \
"v_and_b32_e32 v87, v15, v87\n" \
"v_accvgpr_read_b32 v40, a192\n" \
"v_mfma_f32_16x16x32_bf16 v[120:123], a[140:143], a[124:127], v[120:123]\n" \
"ds_write_b128 v50, v[132:135] offset:1024\n" \
"v_mfma_f32_16x16x32_bf16 v[124:127], a[156:159], a[124:127], v[44:47]\n" \
"buffer_load_dwordx4 a[124:127], v54, s[56:59], s60 offen offset:3072 sc1\n" \
"v_mfma_f32_16x16x32_bf16 v[124:127], a[152:155], a[120:123], v[124:127]\n" \
"ds_write_b128 v50, v[136:139] offset:2048\n" \
"v_mfma_f32_16x16x32_bf16 v[124:127], a[148:151], a[116:119], v[124:127]\n" \
"v_accvgpr_read_b32 v22, a193\n" \
"v_accvgpr_read_b32 v41, a194\n" \
"v_mfma_f32_16x16x32_bf16 v[124:127], a[144:147], a[112:115], v[124:127]\n" \
"ds_write_b128 v50, v[140:143] offset:3072\n" \
"v_mfma_f32_16x16x32_bf16 a[196:199], a[204:207], v[84:87], a[196:199]\n" \
"v_accvgpr_read_b32 v23, a195\n" \
"v_add_f32_e32 v40, v40, v22\n" \
"v_mfma_f32_16x16x32_bf16 v[192:195], a[160:163], v[84:87], v[192:195]\n" \
"ds_write_b128 v51, v[160:163] offset:32768\n" \
"v_mfma_f32_16x16x32_bf16 v[196:199], a[164:167], v[84:87], v[196:199]\n" \
"buffer_load_dwordx4 a[112:115], v54, s[56:59], s60 offen offset:0 sc1\n" \
"v_mfma_f32_16x16x32_bf16 v[200:203], a[168:171], v[84:87], v[200:203]\n" \
"ds_write_b128 v51, v[164:167] offset:33792\n" \
"v_add_f32_e32 v41, v41, v23\n" \
"ds_write_b64 v19, v[40:41]\n" \
"v_mfma_f32_16x16x32_bf16 v[204:207], a[172:175], v[84:87], v[204:207]\n" \
"v_lshl_add_u32 v52, s15, 2, v20\n" \
"v_add_u32_e32 v52, 0x1a000, v52\n" \
"ds_write_b128 v51, v[168:171] offset:34816\n" \
"v_mfma_f32_16x16x32_bf16 v[208:211], a[176:179], v[84:87], v[208:211]\n" \
"v_exp_f32_e32 v88, v88\n" \
"v_mfma_f32_16x16x32_bf16 v[212:215], a[180:183], v[84:87], v[212:215]\n" \
"ds_write_b128 v51, v[172:175] offset:35840\n" \
"v_mfma_f32_16x16x32_bf16 v[216:219], a[184:187], v[84:87], v[216:219]\n" \
"buffer_load_dwordx4 a[116:119], v54, s[56:59], s60 offen offset:1024 sc1\n" \
"v_mfma_f32_16x16x32_bf16 v[220:223], a[188:191], v[84:87], v[220:223]\n" \
"v_exp_f32_e32 v89, v89\n" \
"v_exp_f32_e32 v90, v90\n" \
"v_exp_f32_e32 v91, v91\n" \
"v_cvt_pk_bf16_f32 v88, v88, v89\n" \
"v_exp_f32_e32 v92, v92\n" \
"v_exp_f32_e32 v93, v93\n" \
"v_exp_f32_e32 v94, v94\n" \
"v_exp_f32_e32 v95, v95\n" \
"v_cvt_pk_bf16_f32 v89, v90, v91\n" \
"v_cvt_pk_bf16_f32 v90, v92, v93\n" \
"v_cvt_pk_bf16_f32 v91, v94, v95\n" \
"v_xor_b32_e32 v63, 0x400, v19\n" \
"buffer_load_dwordx4 a[120:123], v54, s[56:59], s60 offen offset:2048 sc1\n" \
"v_exp_f32_e32 v120, v120\n" \
"v_exp_f32_e32 v121, v121\n" \
"v_exp_f32_e32 v122, v122\n" \
"v_exp_f32_e32 v123, v123\n" \
"v_cvt_pk_bf16_f32 v120, v120, v121\n" \
"v_exp_f32_e32 v124, v124\n" \
"v_exp_f32_e32 v125, v125\n" \
"s_waitcnt lgkmcnt(0)\n" \
"s_barrier\n" \
"ds_read_b128 v[144:147], v52 offset:0\n" \
"ds_read_b128 v[148:151], v52 offset:1024\n" \
"ds_read_b128 v[152:155], v52 offset:2048\n" \
".fill (L_ab + 12288 - .) / 4, 4, 0xBF800000\n" \
"L_ap3:\n" \
"s_cbranch_execz L_ap0\n" \
"ds_read_b128 v[156:159], v52 offset:3072\n" \
"ds_read_b128 v[176:179], v52 offset:4096\n" \
"ds_read_b128 v[180:183], v52 offset:5120\n" \
"ds_read_b128 v[184:187], v52 offset:6144\n" \
"ds_read_b128 v[188:191], v52 offset:7168\n" \
"v_mfma_f32_16x16x32_bf16 a[196:199], a[212:215], v[88:91], a[196:199]\n" \
"ds_read_b64 v[42:43], v63\n" \
"v_cvt_pk_bf16_f32 v121, v122, v123\n" \
"v_mfma_f32_16x16x32_bf16 v[224:227], a[160:163], v[88:91], v[224:227]\n" \
"v_exp_f32_e32 v126, v126\n" \
"v_mfma_f32_16x16x32_bf16 v[228:231], a[164:167], v[88:91], v[228:231]\n" \
"v_exp_f32_e32 v127, v127\n" \
"v_mfma_f32_16x16x32_bf16 v[232:235], a[168:171], v[88:91], v[232:235]\n" \
"v_cvt_pk_bf16_f32 v122, v124, v125\n" \
"v_cvt_pk_bf16_f32 v123, v126, v127\n" \
"v_mfma_f32_16x16x32_bf16 v[236:239], a[172:175], v[88:91], v[236:239]\n" \
"v_and_b32_e32 v120, v12, v120\n" \
"v_and_b32_e32 v121, v13, v121\n" \
"v_mfma_f32_16x16x32_bf16 v[240:243], a[176:179], v[88:91], v[240:243]\n" \
"v_and_b32_e32 v122, v14, v122\n" \
"v_and_b32_e32 v123, v15, v123\n" \
"v_mfma_f32_16x16x32_bf16 v[244:247], a[180:183], v[88:91], v[244:247]\n" \
"v_readlane_b32 s93, v11, 22\n" \
"v_readlane_b32 s92, v11, 27\n" \
"v_mfma_f32_16x16x32_bf16 v[248:251], a[184:187], v[88:91], v[248:251]\n" \
"s_add_u32 m0, s93, 0x0\n" \
"v_mov_b32_e32 v130, v28\n" \
"v_mfma_f32_16x16x32_bf16 v[252:255], a[188:191], v[88:91], v[252:255]\n" \
"v_mov_b32_e32 v131, v29\n" \
"v_mov_b32_e32 v132, 0\n" \
"v_mov_b32_e32 v133, 0\n" \
"v_mov_b32_e32 v128, 0\n" \
"v_mov_b32_e32 v129, 0\n" \
"s_waitcnt vmcnt(8) lgkmcnt(1)\n" \
"s_barrier\n" \
"v_mfma_f32_16x16x32_bf16 a[196:199], a[212:215], v[120:123], a[196:199]\n" \
"buffer_load_dwordx4 v0, s[80:83], s92 offen offset:0 lds\n" \
"v_mfma_f32_16x16x32_bf16 v[224:227], a[0:3], v[120:123], v[224:227]\n" \
"v_mfma_f32_16x16x32_bf16 v[228:231], a[4:7], v[120:123], v[228:231]\n" \
"v_mfma_f32_16x16x32_bf16 v[232:235], a[8:11], v[120:123], v[232:235]\n" \
"v_mfma_f32_16x16x32_bf16 v[236:239], a[12:15], v[120:123], v[236:239]\n" \
"v_mfma_f32_16x16x32_bf16 v[240:243], a[16:19], v[120:123], v[240:243]\n" \
"v_mfma_f32_16x16x32_bf16 v[244:247], a[20:23], v[120:123], v[244:247]\n" \
"buffer_load_dwordx4 v1, s[80:83], s92 offen offset:1024 lds\n" \
"v_mfma_f32_16x16x32_bf16 v[248:251], a[24:27], v[120:123], v[248:251]\n" \
"v_accvgpr_read_b32 v56, a196\n" \
"v_accvgpr_read_b32 v24, a197\n" \
"v_mfma_f32_16x16x32_bf16 v[252:255], a[28:31], v[120:123], v[252:255]\n" \
"v_add_f32_e32 v56, v56, v24\n" \
"s_waitcnt lgkmcnt(0)\n" \
"v_add_f32_e32 v56, v56, v42\n" \
"v_mfma_f32_16x16x32_bf16 v[192:195], v[130:133], v[144:147], v[192:195]\n" \
"v_max_f32_e32 v62, v62, v56\n" \
"v_accvgpr_read_b32 v57, a198\n" \
"v_mfma_f32_16x16x32_bf16 v[196:199], v[128:131], v[144:147], v[196:199]\n" \
"v_rcp_f32_e32 v56, v56\n" \
"v_mfma_f32_16x16x32_bf16 v[200:203], v[130:133], v[148:151], v[200:203]\n" \
"v_mul_f32_e32 v56, v56, v60\n" \
"v_accvgpr_read_b32 v25, a199\n" \
"buffer_load_dwordx4 v2, s[80:83], s92 offen offset:2048 lds\n" \
"v_mfma_f32_16x16x32_bf16 v[204:207], v[128:131], v[148:151], v[204:207]\n" \
"v_mul_f32_e32 v192, v56, v192\n" \
"v_mul_f32_e32 v193, v56, v193\n" \
"v_mfma_f32_16x16x32_bf16 v[208:211], v[130:133], v[152:155], v[208:211]\n" \
"v_mul_f32_e32 v194, v56, v194\n" \
"v_mul_f32_e32 v195, v56, v195\n" \
"v_mfma_f32_16x16x32_bf16 v[212:215], v[128:131], v[152:155], v[212:215]\n" \
"v_mul_f32_e32 v196, v56, v196\n" \
"v_mul_f32_e32 v197, v56, v197\n" \
"v_mfma_f32_16x16x32_bf16 v[216:219], v[130:133], v[156:159], v[216:219]\n" \
"v_mul_f32_e32 v198, v56, v198\n" \
"v_mul_f32_e32 v199, v56, v199\n" \
"v_mfma_f32_16x16x32_bf16 v[220:223], v[128:131], v[156:159], v[220:223]\n" \
"v_cvt_pk_bf16_f32 v64, v192, v193\n" \
"v_cvt_pk_bf16_f32 v65, v194, v195\n" \
"v_mfma_f32_16x16x32_bf16 v[224:227], v[130:133], v[176:179], v[224:227]\n" \
"buffer_load_dwordx4 v3, s[80:83], s92 offen offset:3072 lds\n" \
"s_add_u32 m0, s93, 0x1000\n" \
"v_mfma_f32_16x16x32_bf16 v[228:231], v[128:131], v[176:179], v[228:231]\n" \
"v_cvt_pk_bf16_f32 v66, v196, v197\n" \
"v_cvt_pk_bf16_f32 v67, v198, v199\n" \
"v_mfma_f32_16x16x32_bf16 v[232:235], v[130:133], v[180:183], v[232:235]\n" \
"v_mul_f32_e32 v200, v56, v200\n" \
"v_mul_f32_e32 v201, v56, v201\n" \
"v_mfma_f32_16x16x32_bf16 v[236:239], v[128:131], v[180:183], v[236:239]\n" \
"v_mfma_f32_16x16x32_bf16 a[208:211], a[216:219], v[64:67], a[208:211]\n" \
"v_mul_f32_e32 v202, v56, v202\n" \
"v_mul_f32_e32 v203, v56, v203\n" \
"v_mfma_f32_16x16x32_bf16 v[240:243], v[130:133], v[184:187], v[240:243]\n" \
"v_mul_f32_e32 v204, v56, v204\n" \
"v_mul_f32_e32 v205, v56, v205\n" \
"buffer_load_dwordx4 v4, s[80:83], s92 offen offset:0 lds\n" \
"v_mul_f32_e32 v206, v56, v206\n" \
"v_mul_f32_e32 v207, v56, v207\n" \
"v_add_f32_e32 v57, v57, v25\n" \
"v_cvt_pk_bf16_f32 v68, v200, v201\n" \
"v_cvt_pk_bf16_f32 v69, v202, v203\n" \
"v_cvt_pk_bf16_f32 v70, v204, v205\n" \
"v_cvt_pk_bf16_f32 v71, v206, v207\n" \
"v_add_f32_e32 v57, v57, v43\n" \
"v_mul_f32_e32 v208, v56, v208\n" \
"v_mfma_f32_16x16x32_bf16 a[208:211], a[216:219], v[68:71], a[208:211]\n" \
"v_mul_f32_e32 v209, v56, v209\n" \
"v_mul_f32_e32 v210, v56, v210\n" \
"v_mul_f32_e32 v211, v56, v211\n" \
"v_mul_f32_e32 v212, v56, v212\n" \
"v_mul_f32_e32 v213, v56, v213\n" \
"v_mul_f32_e32 v214, v56, v214\n" \
"v_mul_f32_e32 v215, v56, v215\n" \
"v_max_f32_e32 v62, v62, v57\n" \
"v_cvt_pk_bf16_f32 v72, v208, v209\n" \
"v_cvt_pk_bf16_f32 v73, v210, v211\n" \
"buffer_load_dwordx4 v5, s[80:83], s92 offen offset:1024 lds\n" \
"v_cvt_pk_bf16_f32 v74, v212, v213\n" \
"v_cvt_pk_bf16_f32 v75, v214, v215\n" \
"v_rcp_f32_e32 v57, v57\n" \
"v_mfma_f32_16x16x32_bf16 v[244:247], v[128:131], v[184:187], v[244:247]\n" \
"v_mfma_f32_16x16x32_bf16 a[208:211], a[216:219], v[72:75], a[208:211]\n" \
"v_mul_f32_e32 v216, v56, v216\n" \
"v_mul_f32_e32 v217, v56, v217\n" \
"v_mul_f32_e32 v218, v56, v218\n" \
"v_mul_f32_e32 v219, v56, v219\n" \
"v_mul_f32_e32 v220, v56, v220\n" \
"v_mul_f32_e32 v221, v56, v221\n" \
"v_mul_f32_e32 v222, v56, v222\n" \
"v_mul_f32_e32 v223, v56, v223\n" \
"v_cvt_pk_bf16_f32 v76, v216, v217\n" \
"v_cvt_pk_bf16_f32 v77, v218, v219\n" \
"v_cvt_pk_bf16_f32 v78, v220, v221\n" \
"buffer_load_dwordx4 v6, s[80:83], s92 offen offset:2048 lds\n" \
"v_cvt_pk_bf16_f32 v79, v222, v223\n" \
"v_mul_f32_e32 v57, v57, v61\n" \
"v_mul_f32_e32 v224, v57, v224\n" \
"v_mfma_f32_16x16x32_bf16 a[208:211], a[216:219], v[76:79], a[208:211]\n" \
"v_mul_f32_e32 v225, v57, v225\n" \
"v_mul_f32_e32 v226, v57, v226\n" \
"v_mul_f32_e32 v227, v57, v227\n" \
"v_mul_f32_e32 v228, v57, v228\n" \
"v_mul_f32_e32 v229, v57, v229\n" \
"v_mul_f32_e32 v230, v57, v230\n" \
"v_mul_f32_e32 v231, v57, v231\n" \
"v_cvt_pk_bf16_f32 v80, v224, v225\n" \
"v_cvt_pk_bf16_f32 v81, v226, v227\n" \
"v_cvt_pk_bf16_f32 v82, v228, v229\n" \
"v_cvt_pk_bf16_f32 v83, v230, v231\n" \
"v_mfma_f32_16x16x32_bf16 v[248:251], v[130:133], v[188:191], v[248:251]\n" \
"v_mul_f32_e32 v232, v57, v232\n" \
"v_mul_f32_e32 v233, v57, v233\n" \
"v_mfma_f32_16x16x32_bf16 a[208:211], a[216:219], v[80:83], a[208:211]\n" \
"buffer_load_dwordx4 v7, s[80:83], s92 offen offset:3072 lds\n" \
"s_add_u32 m0, s93, 0x2000\n" \
"v_mul_f32_e32 v234, v57, v234\n" \
"v_mul_f32_e32 v235, v57, v235\n" \
"v_mul_f32_e32 v236, v57, v236\n" \
"v_mul_f32_e32 v237, v57, v237\n" \
"v_mul_f32_e32 v238, v57, v238\n" \
"v_mul_f32_e32 v239, v57, v239\n" \
"v_cvt_pk_bf16_f32 v84, v232, v233\n" \
"v_cvt_pk_bf16_f32 v85, v234, v235\n" \
"v_cvt_pk_bf16_f32 v86, v236, v237\n" \
"v_cvt_pk_bf16_f32 v87, v238, v239\n" \
"v_mul_f32_e32 v240, v57, v240\n" \
"v_mul_f32_e32 v241, v57, v241\n" \
"v_mfma_f32_16x16x32_bf16 a[208:211], a[216:219], v[84:87], a[208:211]\n" \
"v_mul_f32_e32 v242, v57, v242\n" \
"v_mul_f32_e32 v243, v57, v243\n" \
"v_mul_f32_e32 v244, v57, v244\n" \
"v_mul_f32_e32 v245, v57, v245\n" \
"v_mul_f32_e32 v246, v57, v246\n" \
"v_mul_f32_e32 v247, v57, v247\n" \
"buffer_load_dwordx4 v8, s[80:83], s92 offen offset:0 lds\n" \
"v_cvt_pk_bf16_f32 v96, v240, v241\n" \
"v_cvt_pk_bf16_f32 v97, v242, v243\n" \
"v_cvt_pk_bf16_f32 v98, v244, v245\n" \
"v_cvt_pk_bf16_f32 v99, v246, v247\n" \
"v_mfma_f32_16x16x32_bf16 v[252:255], v[128:131], v[188:191], v[252:255]\n" \
"v_mul_f32_e32 v248, v57, v248\n" \
"v_mfma_f32_16x16x32_bf16 a[208:211], a[216:219], v[96:99], a[208:211]\n" \
"v_mul_f32_e32 v249, v57, v249\n" \
"v_mul_f32_e32 v250, v57, v250\n" \
"v_mul_f32_e32 v251, v57, v251\n" \
"v_cvt_pk_bf16_f32 v100, v248, v249\n" \
"v_cvt_pk_bf16_f32 v101, v250, v251\n" \
"v_cmp_lt_f32_e64 s[72:73], s21, v62\n" \
"v_mul_f32_e32 v252, v57, v252\n" \
"v_mul_f32_e32 v253, v57, v253\n" \
"v_mul_f32_e32 v254, v57, v254\n" \
"v_mul_f32_e32 v255, v57, v255\n" \
"v_cvt_pk_bf16_f32 v102, v252, v253\n" \
"v_cvt_pk_bf16_f32 v103, v254, v255\n" \
"s_nop 1\n" \
"v_mfma_f32_16x16x32_bf16 a[208:211], a[216:219], v[100:103], a[208:211]\n" \
"s_nop 7\n" \
"v_accvgpr_read_b32 v63, a208\n" \
"v_cmp_u_f32_e32 vcc, v63, v63\n" \
"s_nop 0\n" \
"s_or_b64 vcc, s[72:73], vcc\n" \
"s_cbranch_scc0 L_out_%=\n" \
"s_branch L_end_%=\n" \
"L_skb_%=:\n" \
"s_sleep 1\n" \
"buffer_load_dword v57, v61, s[56:59], s96 offen sc1\n" \
"s_add_u32 s101, s101, 1\n" \
"s_waitcnt vmcnt(0)\n" \
"v_cmp_ne_u32_e32 vcc, s62, v57\n" \
"s_cmp_gt_u32 s101, 0x100000\n" \
"s_cbranch_scc1 L_rkb_%=\n" \
"s_nop 2\n" \
"s_cbranch_vccnz L_skb_%=\n" \
"s_branch L_rkb_%=\n" \
"L_sol_%=:\n" \
"s_sleep 1\n" \
"buffer_load_dwordx2 v[26:27], v16, s[56:59], s96 offen sc1\n" \
"s_add_u32 s101, s101, 1\n" \
"s_waitcnt vmcnt(0)\n" \
"v_cmp_ne_u64_e32 vcc, s[62:63], v[26:27]\n" \
"s_cmp_gt_u32 s101, 0x100000\n" \
"s_cbranch_scc1 L_rol_%=\n" \
"s_nop 2\n" \
"s_cbranch_vccnz L_sol_%=\n" \
"s_branch L_rol_%=\n" \
"L_sos_%=:\n" \
"s_sleep 1\n" \
"buffer_load_dwordx2 v[26:27], v16, s[56:59], s96 offen sc1\n" \
"s_add_u32 s101, s101, 1\n" \
"s_waitcnt vmcnt(0)\n" \
"v_cmp_ne_u64_e32 vcc, s[62:63], v[26:27]\n" \
"s_cmp_gt_u32 s101, 0x100000\n" \
"s_cbranch_scc1 L_ros_%=\n" \
"s_nop 2\n" \
"s_cbranch_vccnz L_sos_%=\n" \
"s_branch L_ros_%=\n" \
"L_sa_%=:\n" \
"s_sleep 1\n" \
"buffer_load_dword v26, v38, s[56:59], s64 offen sc1\n" \
"s_add_u32 s101, s101, 1\n" \
"s_waitcnt vmcnt(0)\n" \
"v_cmp_ne_u32_e32 vcc, s62, v26\n" \
"s_cmp_gt_u32 s101, 0x100000\n" \
"s_cbranch_scc1 L_ra_%=\n" \
"s_nop 2\n" \
"s_cbranch_vccnz L_sa_%=\n" \
"s_branch L_ra_%=\n" \
"L_sb_%=:\n" \
"s_sleep 1\n" \
"buffer_load_dword v47, v38, s[56:59], s71 offen sc1\n" \
"s_add_u32 s101, s101, 1\n" \
"s_waitcnt vmcnt(0)\n" \
"v_cmp_ne_u32_e32 vcc, s62, v47\n" \
"s_cmp_gt_u32 s101, 0x100000\n" \
"s_cbranch_scc1 L_rb_%=\n" \
"s_nop 2\n" \
"s_cbranch_vccnz L_sb_%=\n" \
"s_branch L_rb_%=\n" \
"L_sna_%=:\n" \
"buffer_load_dword v37, v40, s[56:59], s74 offen sc1\n" \
"s_add_u32 s101, s101, 1\n" \
"s_waitcnt vmcnt(0)\n" \
"v_cmp_eq_u32_e64 s[98:99], s62, v37\n" \
"s_cmp_gt_u32 s101, 0x100000\n" \
"s_cbranch_scc1 L_rna_%=\n" \
"s_andn2_b32 s75, s20, s98\n" \
"s_cbranch_scc0 L_gna_%=\n" \
"s_sleep 1\n" \
"s_branch L_sna_%=\n" \
"L_gna_%=:\n" \
"s_bitcmp1_b32 s20, 0\n" \
"s_cbranch_scc0 L_kna0_%=\n" \
"buffer_load_dwordx4 v[116:119], v20, s[56:59], s12 offen offset:0 sc1\n" \
"L_kna0_%=:\n" \
"s_bitcmp1_b32 s20, 1\n" \
"s_cbranch_scc0 L_kna1_%=\n" \
"buffer_load_dwordx4 v[124:127], v20, s[56:59], s12 offen offset:2048 sc1\n" \
"L_kna1_%=:\n" \
"s_bitcmp1_b32 s20, 2\n" \
"s_cbranch_scc0 L_kna2_%=\n" \
"buffer_load_dwordx4 v[132:135], v20, s[56:59], s13 offen offset:0 sc1\n" \
"L_kna2_%=:\n" \
"s_bitcmp1_b32 s20, 3\n" \
"s_cbranch_scc0 L_kna3_%=\n" \
"buffer_load_dwordx4 v[140:143], v20, s[56:59], s13 offen offset:2048 sc1\n" \
"L_kna3_%=:\n" \
"s_bitcmp1_b32 s20, 4\n" \
"s_cbranch_scc0 L_kna4_%=\n" \
"buffer_load_dwordx4 v[148:151], v20, s[56:59], s14 offen offset:0 sc1\n" \
"L_kna4_%=:\n" \
"s_bitcmp1_b32 s20, 5\n" \
"s_cbranch_scc0 L_kna5_%=\n" \
"buffer_load_dwordx4 v[156:159], v20, s[56:59], s14 offen offset:2048 sc1\n" \
"L_kna5_%=:\n" \
"s_bitcmp1_b32 s20, 6\n" \
"s_cbranch_scc0 L_kna6_%=\n" \
"buffer_load_dwordx4 v[164:167], v20, s[56:59], s15 offen offset:0 sc1\n" \
"L_kna6_%=:\n" \
"s_waitcnt vmcnt(0)\n" \
"s_branch L_rna_%=\n" \
"L_snx_%=:\n" \
"buffer_load_dword v37, v40, s[56:59], s74 offen sc1\n" \
"s_add_u32 s101, s101, 1\n" \
"s_waitcnt vmcnt(0)\n" \
"v_cmp_eq_u32_e64 s[98:99], s62, v37\n" \
"s_cmp_gt_u32 s101, 0x100000\n" \
"s_cbranch_scc1 L_rnx_%=\n" \
"s_andn2_b32 s75, s21, s98\n" \
"s_cbranch_scc0 L_gnx_%=\n" \
"s_sleep 1\n" \
"s_branch L_snx_%=\n" \
"L_gnx_%=:\n" \
"s_bitcmp1_b32 s21, 16\n" \
"s_cbranch_scc0 L_knx16_%=\n" \
"buffer_load_dwordx4 v[112:115], v20, s[56:59], s16 offen offset:0 sc1\n" \
"L_knx16_%=:\n" \
"s_bitcmp1_b32 s21, 17\n" \
"s_cbranch_scc0 L_knx17_%=\n" \
"buffer_load_dwordx4 v[184:187], v20, s[56:59], s16 offen offset:2048 sc1\n" \
"L_knx17_%=:\n" \
"s_bitcmp1_b32 s21, 18\n" \
"s_cbranch_scc0 L_knx18_%=\n" \
"buffer_load_dwordx4 v[120:123], v20, s[56:59], s17 offen offset:0 sc1\n" \
"L_knx18_%=:\n" \
"s_bitcmp1_b32 s21, 19\n" \
"s_cbranch_scc0 L_knx19_%=\n" \
"buffer_load_dwordx4 v[192:195], v20, s[56:59], s17 offen offset:2048 sc1\n" \
"L_knx19_%=:\n" \
"s_bitcmp1_b32 s21, 20\n" \
"s_cbranch_scc0 L_knx20_%=\n" \
"buffer_load_dwordx4 v[128:131], v20, s[56:59], s18 offen offset:0 sc1\n" \
"L_knx20_%=:\n" \
"s_bitcmp1_b32 s21, 21\n" \
"s_cbranch_scc0 L_knx21_%=\n" \
"buffer_load_dwordx4 v[200:203], v20, s[56:59], s18 offen offset:2048 sc1\n" \
"L_knx21_%=:\n" \
"s_bitcmp1_b32 s21, 22\n" \
"s_cbranch_scc0 L_knx22_%=\n" \
"buffer_load_dwordx4 v[176:179], v20, s[56:59], s19 offen offset:0 sc1\n" \
"L_knx22_%=:\n" \
"s_bitcmp1_b32 s21, 23\n" \
"s_cbranch_scc0 L_knx23_%=\n" \
"buffer_load_dwordx4 v[248:251], v20, s[56:59], s19 offen offset:2048 sc1\n" \
"L_knx23_%=:\n" \
"s_bitcmp1_b32 s21, 8\n" \
"s_cbranch_scc0 L_knx8_%=\n" \
"buffer_load_dwordx4 v[188:191], v20, s[56:59], s12 offen offset:1024 sc1\n" \
"L_knx8_%=:\n" \
"s_bitcmp1_b32 s21, 9\n" \
"s_cbranch_scc0 L_knx9_%=\n" \
"buffer_load_dwordx4 v[196:199], v20, s[56:59], s12 offen offset:3072 sc1\n" \
"L_knx9_%=:\n" \
"s_waitcnt vmcnt(0)\n" \
"s_branch L_rnx_%=\n" \
"L_sx0_%=:\n" \
"s_sleep 1\n" \
"buffer_load_dword v37, v40, s[56:59], s74 offen sc1\n" \
"s_add_u32 s101, s101, 1\n" \
"s_waitcnt vmcnt(0)\n" \
"v_cmp_eq_u32_e64 s[98:99], s62, v37\n" \
"s_cmp_gt_u32 s101, 0x100000\n" \
"s_cbranch_scc1 L_rx0_%=\n" \
"s_andn2_b32 s75, 0x400, s98\n" \
"s_cbranch_scc1 L_sx0_%=\n" \
"s_branch L_rx0_%=\n" \
"L_sx1_%=:\n" \
"s_sleep 1\n" \
"buffer_load_dword v37, v40, s[56:59], s74 offen sc1\n" \
"s_add_u32 s101, s101, 1\n" \
"s_waitcnt vmcnt(0)\n" \
"v_cmp_eq_u32_e64 s[98:99], s62, v37\n" \
"s_cmp_gt_u32 s101, 0x100000\n" \
"s_cbranch_scc1 L_rx1_%=\n" \
"s_andn2_b32 s75, 0x800, s98\n" \
"s_cbranch_scc1 L_sx1_%=\n" \
"s_branch L_rx1_%=\n" \
"L_sx2_%=:\n" \
"s_sleep 1\n" \
"buffer_load_dword v37, v40, s[56:59], s74 offen sc1\n" \
"s_add_u32 s101, s101, 1\n" \
"s_waitcnt vmcnt(0)\n" \
"v_cmp_eq_u32_e64 s[98:99], s62, v37\n" \
"s_cmp_gt_u32 s101, 0x100000\n" \
"s_cbranch_scc1 L_rx2_%=\n" \
"s_andn2_b32 s75, 0x1000, s98\n" \
"s_cbranch_scc1 L_sx2_%=\n" \
"s_branch L_rx2_%=\n" \
"L_sx3_%=:\n" \
"s_sleep 1\n" \
"buffer_load_dword v37, v40, s[56:59], s74 offen sc1\n" \
"s_add_u32 s101, s101, 1\n" \
"s_waitcnt vmcnt(0)\n" \
"v_cmp_eq_u32_e64 s[98:99], s62, v37\n" \
"s_cmp_gt_u32 s101, 0x100000\n" \
"s_cbranch_scc1 L_rx3_%=\n" \
"s_andn2_b32 s75, 0x2000, s98\n" \
"s_cbranch_scc1 L_sx3_%=\n" \
"s_branch L_rx3_%=\n" \
"L_sx4_%=:\n" \
"s_sleep 1\n" \
"buffer_load_dword v37, v40, s[56:59], s74 offen sc1\n" \
"s_add_u32 s101, s101, 1\n" \
"s_waitcnt vmcnt(0)\n" \
"v_cmp_eq_u32_e64 s[98:99], s62, v37\n" \
"s_cmp_gt_u32 s101, 0x100000\n" \
"s_cbranch_scc1 L_rx4_%=\n" \
"s_andn2_b32 s75, 0x4000, s98\n" \
"s_cbranch_scc1 L_sx4_%=\n" \
"s_branch L_rx4_%=\n" \
"L_att_stub:\n" \
"s_mov_b32 s101, 0\n" \
"v_add_u32_e32 v32, 0x49000, v20\n" \
"v_readlane_b32 s96, v11, 36\n" \
"v_readlane_b32 s97, v11, 37\n" \
"buffer_load_dwordx4 a[96:99], v20, s[56:59], s60 offen offset:0 sc1\n" \
"buffer_load_dwordx4 a[112:115], v32, s[56:59], s60 offen offset:0 sc1\n" \
"buffer_load_dwordx4 a[100:103], v20, s[56:59], s60 offen offset:1024 sc1\n" \
"buffer_load_dwordx4 a[116:119], v32, s[56:59], s60 offen offset:1024 sc1\n" \
"buffer_load_dwordx4 a[104:107], v20, s[56:59], s60 offen offset:2048 sc1\n" \
"buffer_load_dwordx4 a[120:123], v32, s[56:59], s60 offen offset:2048 sc1\n" \
"buffer_load_dwordx4 a[108:111], v20, s[56:59], s60 offen offset:3072 sc1\n" \
"buffer_load_dwordx4 a[124:127], v32, s[56:59], s60 offen offset:3072 sc1\n" \
"buffer_load_dwordx4 a[220:223], v20, s[56:59], s96 offen offset:0 sc1\n" \
"buffer_load_dwordx4 a[228:231], v32, s[56:59], s96 offen offset:0 sc1\n" \
"buffer_load_dwordx4 a[236:239], v20, s[56:59], s97 offen offset:0 sc1\n" \
"buffer_load_dwordx4 a[244:247], v32, s[56:59], s97 offen offset:0 sc1\n" \
"buffer_load_dwordx4 a[224:227], v20, s[56:59], s96 offen offset:1024 sc1\n" \
"buffer_load_dwordx4 a[232:235], v32, s[56:59], s96 offen offset:1024 sc1\n" \
"buffer_load_dwordx4 a[240:243], v20, s[56:59], s97 offen offset:1024 sc1\n" \
"buffer_load_dwordx4 a[248:251], v32, s[56:59], s97 offen offset:1024 sc1\n" \
"buffer_load_dwordx4 v[64:67], v20, s[56:59], s61 offen offset:0 sc1\n" \
"buffer_load_dwordx4 v[68:71], v20, s[56:59], s61 offen offset:1024 sc1\n" \
"buffer_load_dwordx4 v[72:75], v20, s[56:59], s61 offen offset:2048 sc1\n" \
"buffer_load_dwordx4 v[76:79], v20, s[56:59], s61 offen offset:3072 sc1\n" \
"buffer_load_dwordx4 v[80:83], v32, s[56:59], s61 offen offset:0 sc1\n" \
"buffer_load_dwordx4 v[84:87], v32, s[56:59], s61 offen offset:1024 sc1\n" \
"buffer_load_dwordx4 v[96:99], v32, s[56:59], s61 offen offset:2048 sc1\n" \
"buffer_load_dwordx4 v[100:103], v32, s[56:59], s61 offen offset:3072 sc1\n" \
"v_not_b32_e32 v26, s62\n" \
"s_waitcnt vmcnt(0)\n" \
"s_branch L_out_%=\n" \
".fill (L_ab + 16384 - .) / 4, 4, 0xBF800000\n" \
".fill (L_ab + 16384 - .) / 4, 4, 0xBF800000\n" \
"L_ap4:\n" \
"s_setpc_b64 s[88:89]\n" \
"L_gsw:\n" \
"v_mfma_f32_16x16x32_bf16 v[104:107], a[128:131], a[16:19], v[36:39]\n" \
"s_add_u32 s68, s60, 0xfff6e000\n" \
"s_add_u32 s69, s60, 0xfffb7000\n" \
"s_add_u32 s70, s60, 0x0\n" \
"s_add_u32 s71, s60, 0x49000\n" \
"v_mfma_f32_16x16x32_bf16 v[104:107], a[132:135], a[20:23], v[104:107]\n" \
"s_xor_b32 m0, s65, 0x12000\n" \
"ds_read_b128 a[160:163], v49 offset:0\n" \
"v_mfma_f32_16x16x32_bf16 v[104:107], a[136:139], a[24:27], v[104:107]\n" \
"buffer_load_dwordx4 v50, s[8:11], s64 offen offset:0 sc1 lds\n" \
"v_mfma_f32_16x16x32_bf16 v[104:107], a[140:143], a[28:31], v[104:107]\n" \
"ds_read_b128 a[164:167], v49 offset:1024\n" \
"ds_read_b128 a[168:171], v49 offset:2048\n" \
"v_mfma_f32_16x16x32_bf16 v[108:111], a[156:159], a[28:31], v[36:39]\n" \
"ds_read_b128 a[172:175], v49 offset:3072\n" \
"ds_read_b128 a[176:179], v49 offset:4096\n" \
"v_mfma_f32_16x16x32_bf16 v[108:111], a[152:155], a[24:27], v[108:111]\n" \
"buffer_load_dwordx4 v50, s[8:11], s64 offen offset:1024 sc1 lds\n" \
"v_mfma_f32_16x16x32_bf16 v[108:111], a[148:151], a[20:23], v[108:111]\n" \
"s_mov_b32 s65, s13\n" \
"v_add_u32_e32 v63, s15, v20\n" \
"v_mfma_f32_16x16x32_bf16 v[108:111], a[144:147], a[16:19], v[108:111]\n" \
"s_add_u32 s67, s14, 40960\n" \
"v_add_u32_e32 v50, 0x6000, v63\n" \
"v_mfma_f32_16x16x32_bf16 v[116:119], a[144:147], a[32:35], v[40:43]\n" \
"s_xor_b32 m0, s65, 0x8000\n" \
"ds_read_b128 a[180:183], v49 offset:5120\n" \
"v_mfma_f32_16x16x32_bf16 v[116:119], a[148:151], a[36:39], v[116:119]\n" \
"ds_read_b128 a[184:187], v49 offset:6144\n" \
"v_and_b32_e32 v68, v12, v68\n" \
"v_mfma_f32_16x16x32_bf16 v[116:119], a[152:155], a[40:43], v[116:119]\n" \
"v_and_b32_e32 v69, v13, v69\n" \
"v_and_b32_e32 v70, v14, v70\n" \
"v_mfma_f32_16x16x32_bf16 v[116:119], a[156:159], a[44:47], v[116:119]\n" \
"buffer_load_dwordx4 v50, s[8:11], s67 offen offset:0 sc1 lds\n" \
"v_mfma_f32_16x16x32_bf16 v[112:115], a[140:143], a[44:47], v[40:43]\n" \
"v_and_b32_e32 v71, v15, v71\n" \
"ds_read_b128 a[188:191], v49 offset:7168\n" \
"v_mfma_f32_16x16x32_bf16 v[112:115], a[136:139], a[40:43], v[112:115]\n" \
"v_exp_f32_e32 v86, v86\n" \
"v_mfma_f32_16x16x32_bf16 v[112:115], a[132:135], a[36:39], v[112:115]\n" \
"v_exp_f32_e32 v87, v87\n" \
"v_mfma_f32_16x16x32_bf16 v[112:115], a[128:131], a[32:35], v[112:115]\n" \
"v_exp_f32_e32 v84, v84\n" \
"v_mfma_f32_16x16x32_bf16 v[120:123], a[128:131], a[48:51], v[44:47]\n" \
"buffer_load_dwordx4 v50, s[8:11], s67 offen offset:1024 sc1 lds\n" \
"v_mfma_f32_16x16x32_bf16 v[120:123], a[132:135], a[52:55], v[120:123]\n" \
"v_exp_f32_e32 v85, v85\n" \
"v_mfma_f32_16x16x32_bf16 v[120:123], a[136:139], a[56:59], v[120:123]\n" \
"v_cvt_pk_bf16_f32 v87, v86, v87\n" \
"v_cvt_pk_bf16_f32 v86, v84, v85\n" \
"v_mfma_f32_16x16x32_bf16 v[120:123], a[140:143], a[60:63], v[120:123]\n" \
"v_exp_f32_e32 v82, v82\n" \
"v_mfma_f32_16x16x32_bf16 v[124:127], a[156:159], a[60:63], v[44:47]\n" \
"v_exp_f32_e32 v83, v83\n" \
"v_mfma_f32_16x16x32_bf16 v[124:127], a[152:155], a[56:59], v[124:127]\n" \
"v_exp_f32_e32 v80, v80\n" \
"v_mfma_f32_16x16x32_bf16 v[124:127], a[148:151], a[52:55], v[124:127]\n" \
"buffer_load_dwordx4 v50, s[8:11], s67 offen offset:2048 sc1 lds\n" \
"v_mfma_f32_16x16x32_bf16 v[124:127], a[144:147], a[48:51], v[124:127]\n" \
"v_cvt_pk_bf16_f32 v85, v82, v83\n" \
"s_waitcnt lgkmcnt(1)\n" \
"v_mfma_f32_16x16x32_bf16 v[128:131], a[160:163], v[68:71], v[128:131]\n" \
"v_exp_f32_e32 v81, v81\n" \
"v_mfma_f32_16x16x32_bf16 v[160:163], a[160:163], v[72:75], v[160:163]\n" \
"v_cvt_pk_bf16_f32 v84, v80, v81\n" \
"s_mov_b32 s64, s14\n" \
"v_mfma_f32_16x16x32_bf16 v[132:135], a[164:167], v[68:71], v[132:135]\n" \
"v_exp_f32_e32 v88, v88\n" \
"v_mfma_f32_16x16x32_bf16 v[164:167], a[164:167], v[72:75], v[164:167]\n" \
"v_exp_f32_e32 v89, v89\n" \
"v_mfma_f32_16x16x32_bf16 v[136:139], a[168:171], v[68:71], v[136:139]\n" \
"buffer_load_dwordx4 v50, s[8:11], s67 offen offset:3072 sc1 lds\n" \
"v_mfma_f32_16x16x32_bf16 v[168:171], a[168:171], v[72:75], v[168:171]\n" \
"v_exp_f32_e32 v90, v90\n" \
"v_mfma_f32_16x16x32_bf16 v[140:143], a[172:175], v[68:71], v[140:143]\n" \
"v_exp_f32_e32 v91, v91\n" \
"v_mfma_f32_16x16x32_bf16 v[172:175], a[172:175], v[72:75], v[172:175]\n" \
"v_cvt_pk_bf16_f32 v88, v88, v89\n" \
"v_cvt_pk_bf16_f32 v89, v90, v91\n" \
"v_mfma_f32_16x16x32_bf16 v[144:147], a[176:179], v[68:71], v[144:147]\n" \
"v_exp_f32_e32 v92, v92\n" \
"v_mfma_f32_16x16x32_bf16 v[176:179], a[176:179], v[72:75], v[176:179]\n" \
"v_exp_f32_e32 v93, v93\n" \
"v_mfma_f32_16x16x32_bf16 v[148:151], a[180:183], v[68:71], v[148:151]\n" \
"v_exp_f32_e32 v94, v94\n" \
"v_mfma_f32_16x16x32_bf16 v[180:183], a[180:183], v[72:75], v[180:183]\n" \
"v_exp_f32_e32 v95, v95\n" \
"v_mfma_f32_16x16x32_bf16 v[152:155], a[184:187], v[68:71], v[152:155]\n" \
"v_cvt_pk_bf16_f32 v90, v92, v93\n" \
"v_cvt_pk_bf16_f32 v91, v94, v95\n" \
"v_mfma_f32_16x16x32_bf16 v[184:187], a[184:187], v[72:75], v[184:187]\n" \
"v_add_u32_e32 v51, 0x12000, v63\n" \
"s_waitcnt lgkmcnt(0)\n" \
"v_mfma_f32_16x16x32_bf16 v[156:159], a[188:191], v[68:71], v[156:159]\n" \
"s_xor_b32 m0, s65, 0xa000\n" \
"ds_read_b32 v58, v18 offset:512\n" \
"v_mfma_f32_16x16x32_bf16 v[188:191], a[188:191], v[72:75], v[188:191]\n" \
"buffer_load_dwordx4 v51, s[8:11], s64 offen offset:0 sc1 lds\n" \
"v_smfmac_f32_16x16x64_bf16 a[192:195], a[200:203], v[68:75], v21\n" \
"buffer_load_dwordx4 v51, s[8:11], s64 offen offset:1024 sc1 lds\n" \
"v_mfma_f32_16x16x32_bf16 v[220:223], a[188:191], v[84:87], v[220:223]\n" \
"ds_read_b32 v59, v18 offset:640\n" \
"ds_read_b32 v60, v18 offset:768\n" \
"v_mfma_f32_16x16x32_bf16 v[252:255], a[188:191], v[88:91], v[252:255]\n" \
"ds_read_b32 v61, v18 offset:896\n" \
"ds_read_b128 a[128:131], v48 offset:0\n" \
"v_mfma_f32_16x16x32_bf16 v[216:219], a[184:187], v[84:87], v[216:219]\n" \
"ds_read_b128 a[144:147], v48 offset:4096\n" \
"ds_read_b128 a[132:135], v48 offset:1024\n" \
"v_mfma_f32_16x16x32_bf16 v[248:251], a[184:187], v[88:91], v[248:251]\n" \
"ds_read_b128 a[148:151], v48 offset:5120\n" \
"ds_read_b128 a[136:139], v48 offset:2048\n" \
"v_mfma_f32_16x16x32_bf16 v[212:215], a[180:183], v[84:87], v[212:215]\n" \
"ds_read_b128 a[152:155], v48 offset:6144\n" \
"ds_read_b128 a[140:143], v48 offset:3072\n" \
"v_mfma_f32_16x16x32_bf16 v[244:247], a[180:183], v[88:91], v[244:247]\n" \
"ds_read_b128 a[156:159], v48 offset:7168\n" \
"v_add_u32_e32 v52, 0x14000, v63\n" \
"v_mfma_f32_16x16x32_bf16 v[208:211], a[176:179], v[84:87], v[208:211]\n" \
"buffer_load_dwordx4 v51, s[8:11], s64 offen offset:2048 sc1 lds\n" \
"v_mfma_f32_16x16x32_bf16 v[240:243], a[176:179], v[88:91], v[240:243]\n" \
"v_exp_f32_e32 v104, v104\n" \
"v_mfma_f32_16x16x32_bf16 v[204:207], a[172:175], v[84:87], v[204:207]\n" \
"v_exp_f32_e32 v105, v105\n" \
"v_mfma_f32_16x16x32_bf16 v[236:239], a[172:175], v[88:91], v[236:239]\n" \
"buffer_load_dwordx4 v51, s[8:11], s64 offen offset:3072 sc1 lds\n" \
"v_mfma_f32_16x16x32_bf16 v[200:203], a[168:171], v[84:87], v[200:203]\n" \
"v_exp_f32_e32 v106, v106\n" \
"v_mfma_f32_16x16x32_bf16 v[232:235], a[168:171], v[88:91], v[232:235]\n" \
"v_exp_f32_e32 v107, v107\n" \
"v_mfma_f32_16x16x32_bf16 v[196:199], a[164:167], v[84:87], v[196:199]\n" \
"v_cvt_pk_bf16_f32 v104, v104, v105\n" \
"v_cvt_pk_bf16_f32 v105, v106, v107\n" \
"v_mfma_f32_16x16x32_bf16 v[228:231], a[164:167], v[88:91], v[228:231]\n" \
"v_exp_f32_e32 v108, v108\n" \
"v_mfma_f32_16x16x32_bf16 v[192:195], a[160:163], v[84:87], v[192:195]\n" \
"v_exp_f32_e32 v109, v109\n" \
"v_mfma_f32_16x16x32_bf16 v[224:227], a[160:163], v[88:91], v[224:227]\n" \
"v_exp_f32_e32 v110, v110\n" \
"v_smfmac_f32_16x16x64_bf16 a[196:199], a[200:203], v[84:91], v21\n" \
"v_exp_f32_e32 v111, v111\n" \
"v_cvt_pk_bf16_f32 v106, v108, v109\n" \
"v_cvt_pk_bf16_f32 v107, v110, v111\n" \
"v_and_b32_e32 v104, v12, v104\n" \
"v_and_b32_e32 v105, v13, v105\n" \
"v_and_b32_e32 v106, v14, v106\n" \
"v_and_b32_e32 v107, v15, v107\n" \
"v_accvgpr_read_b32 v54, a192\n" \
"v_accvgpr_read_b32 v22, a193\n" \
"v_add_u32_e32 v53, 0x20000, v63\n" \
"v_exp_f32_e32 v118, v118\n" \
"v_exp_f32_e32 v119, v119\n" \
"v_exp_f32_e32 v116, v116\n" \
"v_exp_f32_e32 v117, v117\n" \
"v_cvt_pk_bf16_f32 v119, v118, v119\n" \
"v_exp_f32_e32 v114, v114\n" \
"v_exp_f32_e32 v115, v115\n" \
"v_exp_f32_e32 v112, v112\n" \
"v_exp_f32_e32 v113, v113\n" \
"v_cvt_pk_bf16_f32 v118, v116, v117\n" \
"v_cvt_pk_bf16_f32 v117, v114, v115\n" \
"v_cvt_pk_bf16_f32 v116, v112, v113\n" \
"v_readlane_b32 s93, v11, 19\n" \
"s_waitcnt vmcnt(10) lgkmcnt(0)\n" \
"s_barrier\n" \
"v_mfma_f32_16x16x32_bf16 v[80:83], a[128:131], a[32:35], v[40:43]\n" \
"ds_read_b128 a[160:163], v49 offset:8192\n" \
"ds_read_b128 a[164:167], v49 offset:9216\n" \
"v_mfma_f32_16x16x32_bf16 v[80:83], a[132:135], a[36:39], v[80:83]\n" \
"ds_read_b128 a[168:171], v49 offset:10240\n" \
"s_xor_b32 m0, s65, 0x18000\n" \
"v_mfma_f32_16x16x32_bf16 v[80:83], a[136:139], a[40:43], v[80:83]\n" \
"buffer_load_dwordx4 v52, s[8:11], s64 offen offset:0 sc1 lds\n" \
"v_mfma_f32_16x16x32_bf16 v[80:83], a[140:143], a[44:47], v[80:83]\n" \
"ds_read_b128 a[172:175], v49 offset:11264\n" \
"ds_read_b128 a[176:179], v49 offset:12288\n" \
"v_mfma_f32_16x16x32_bf16 v[84:87], a[156:159], a[44:47], v[40:43]\n" \
"ds_read_b128 a[180:183], v49 offset:13312\n" \
"ds_read_b128 a[184:187], v49 offset:14336\n" \
"v_mfma_f32_16x16x32_bf16 v[84:87], a[152:155], a[40:43], v[84:87]\n" \
"buffer_load_dwordx4 v52, s[8:11], s64 offen offset:1024 sc1 lds\n" \
"v_mfma_f32_16x16x32_bf16 v[84:87], a[148:151], a[36:39], v[84:87]\n" \
"ds_read_b128 a[188:191], v49 offset:15360\n" \
"v_readlane_b32 s80, v11, 11\n" \
"v_mfma_f32_16x16x32_bf16 v[84:87], a[144:147], a[32:35], v[84:87]\n" \
"buffer_load_dwordx4 v52, s[8:11], s64 offen offset:2048 sc1 lds\n" \
"v_mfma_f32_16x16x32_bf16 v[92:95], a[144:147], a[48:51], v[44:47]\n" \
"v_exp_f32_e32 v120, v120\n" \
"v_mfma_f32_16x16x32_bf16 v[92:95], a[148:151], a[52:55], v[92:95]\n" \
"v_exp_f32_e32 v121, v121\n" \
"v_mfma_f32_16x16x32_bf16 v[92:95], a[152:155], a[56:59], v[92:95]\n" \
"buffer_load_dwordx4 v52, s[8:11], s64 offen offset:3072 sc1 lds\n" \
"v_mfma_f32_16x16x32_bf16 v[92:95], a[156:159], a[60:63], v[92:95]\n" \
"s_mov_b32 m0, s65\n" \
"v_cvt_pk_bf16_f32 v120, v120, v121\n" \
"v_mfma_f32_16x16x32_bf16 v[88:91], a[140:143], a[60:63], v[44:47]\n" \
"v_exp_f32_e32 v122, v122\n" \
"v_mfma_f32_16x16x32_bf16 v[88:91], a[136:139], a[56:59], v[88:91]\n" \
"buffer_load_dwordx4 v53, s[8:11], s64 offen offset:0 sc1 lds\n" \
"v_mfma_f32_16x16x32_bf16 v[88:91], a[132:135], a[52:55], v[88:91]\n" \
"v_exp_f32_e32 v123, v123\n" \
"v_mfma_f32_16x16x32_bf16 v[88:91], a[128:131], a[48:51], v[88:91]\n" \
"v_exp_f32_e32 v124, v124\n" \
"v_mfma_f32_16x16x32_bf16 a[192:195], a[212:215], v[104:107], a[192:195]\n" \
"v_cvt_pk_bf16_f32 v121, v122, v123\n" \
"s_waitcnt lgkmcnt(5)\n" \
"v_mfma_f32_16x16x32_bf16 v[160:163], a[160:163], v[104:107], v[160:163]\n" \
"buffer_load_dwordx4 v53, s[8:11], s64 offen offset:1024 sc1 lds\n" \
"v_mfma_f32_16x16x32_bf16 v[164:167], a[164:167], v[104:107], v[164:167]\n" \
"v_exp_f32_e32 v125, v125\n" \
"v_mfma_f32_16x16x32_bf16 v[168:171], a[168:171], v[104:107], v[168:171]\n" \
"v_cvt_pk_bf16_f32 v122, v124, v125\n" \
"s_waitcnt lgkmcnt(1)\n" \
"v_mfma_f32_16x16x32_bf16 v[172:175], a[172:175], v[104:107], v[172:175]\n" \
"v_exp_f32_e32 v126, v126\n" \
"v_mfma_f32_16x16x32_bf16 v[176:179], a[176:179], v[104:107], v[176:179]\n" \
"v_exp_f32_e32 v127, v127\n" \
"v_mfma_f32_16x16x32_bf16 v[180:183], a[180:183], v[104:107], v[180:183]\n" \
"v_cvt_pk_bf16_f32 v123, v126, v127\n" \
"v_readlane_b32 s81, v11, 12\n" \
"v_mfma_f32_16x16x32_bf16 v[184:187], a[184:187], v[104:107], v[184:187]\n" \
"s_mov_b32 s82, 0x47fca0\n" \
"s_waitcnt lgkmcnt(0)\n" \
"v_mfma_f32_16x16x32_bf16 v[188:191], a[188:191], v[104:107], v[188:191]\n" \
"buffer_load_dwordx4 v53, s[8:11], s64 offen offset:2048 sc1 lds\n" \
"v_mfma_f32_16x16x32_bf16 v[220:223], a[188:191], v[116:119], v[220:223]\n" \
"buffer_load_dwordx4 v53, s[8:11], s64 offen offset:3072 sc1 lds\n" \
"v_mfma_f32_16x16x32_bf16 v[252:255], a[188:191], v[120:123], v[252:255]\n" \
"s_mov_b32 s83, 0x20000\n" \
"v_readlane_b32 s92, v11, 26\n" \
"v_mfma_f32_16x16x32_bf16 v[216:219], a[184:187], v[116:119], v[216:219]\n" \
"s_add_u32 m0, s93, 0x0\n" \
"ds_read_b128 a[128:131], v48 offset:8192\n" \
"v_mfma_f32_16x16x32_bf16 v[248:251], a[184:187], v[120:123], v[248:251]\n" \
"ds_read_b128 a[144:147], v48 offset:12288\n" \
"ds_read_b128 a[132:135], v48 offset:9216\n" \
"v_mfma_f32_16x16x32_bf16 v[212:215], a[180:183], v[116:119], v[212:215]\n" \
"buffer_load_dwordx4 v0, s[80:83], s92 offen offset:0 lds\n" \
"v_mfma_f32_16x16x32_bf16 v[244:247], a[180:183], v[120:123], v[244:247]\n" \
"buffer_load_dwordx4 v1, s[80:83], s92 offen offset:1024 lds\n" \
"v_mfma_f32_16x16x32_bf16 v[208:211], a[176:179], v[116:119], v[208:211]\n" \
"ds_read_b128 a[148:151], v48 offset:13312\n" \
"ds_read_b128 a[136:139], v48 offset:10240\n" \
"v_mfma_f32_16x16x32_bf16 v[240:243], a[176:179], v[120:123], v[240:243]\n" \
"ds_read_b128 a[152:155], v48 offset:14336\n" \
"ds_read_b128 a[140:143], v48 offset:11264\n" \
"v_mfma_f32_16x16x32_bf16 v[204:207], a[172:175], v[116:119], v[204:207]\n" \
"buffer_load_dwordx4 v2, s[80:83], s92 offen offset:2048 lds\n" \
"v_mfma_f32_16x16x32_bf16 v[236:239], a[172:175], v[120:123], v[236:239]\n" \
"ds_read_b128 a[156:159], v48 offset:15360\n" \
"s_xor_b32 s65, s65, 0x8000\n" \
"v_mfma_f32_16x16x32_bf16 v[200:203], a[168:171], v[116:119], v[200:203]\n" \
"s_add_u32 s64, s64, 0x10000\n" \
"v_add_u32_e32 v49, 0x10000, v48\n" \
"v_mfma_f32_16x16x32_bf16 v[232:235], a[168:171], v[120:123], v[232:235]\n" \
"buffer_load_dwordx4 v3, s[80:83], s92 offen offset:3072 lds\n" \
"v_mfma_f32_16x16x32_bf16 v[196:199], a[164:167], v[116:119], v[196:199]\n" \
"v_exp_f32_e32 v86, v86\n" \
"v_mfma_f32_16x16x32_bf16 v[228:231], a[164:167], v[120:123], v[228:231]\n" \
"v_exp_f32_e32 v87, v87\n" \
"v_mfma_f32_16x16x32_bf16 v[192:195], a[160:163], v[116:119], v[192:195]\n" \
"v_exp_f32_e32 v84, v84\n" \
"v_mfma_f32_16x16x32_bf16 v[224:227], a[160:163], v[120:123], v[224:227]\n" \
"v_exp_f32_e32 v85, v85\n" \
"v_smfmac_f32_16x16x64_bf16 a[196:199], a[200:203], v[116:123], v21\n" \
"v_exp_f32_e32 v88, v88\n" \
"v_exp_f32_e32 v89, v89\n" \
"v_cvt_pk_bf16_f32 v87, v86, v87\n" \
"v_exp_f32_e32 v82, v82\n" \
"v_exp_f32_e32 v83, v83\n" \
"v_exp_f32_e32 v80, v80\n" \
"v_exp_f32_e32 v81, v81\n" \
"v_exp_f32_e32 v90, v90\n" \
"v_exp_f32_e32 v91, v91\n" \
"v_cvt_pk_bf16_f32 v86, v84, v85\n" \
"v_cvt_pk_bf16_f32 v88, v88, v89\n" \
"v_exp_f32_e32 v92, v92\n" \
"v_exp_f32_e32 v93, v93\n" \
"v_exp_f32_e32 v94, v94\n" \
"v_exp_f32_e32 v95, v95\n" \
"v_cvt_pk_bf16_f32 v85, v82, v83\n" \
"v_cvt_pk_bf16_f32 v84, v80, v81\n" \
"v_cvt_pk_bf16_f32 v89, v90, v91\n" \
"v_and_b32_e32 v84, v12, v84\n" \
"v_and_b32_e32 v85, v13, v85\n" \
"v_and_b32_e32 v86, v14, v86\n" \
"v_and_b32_e32 v87, v15, v87\n" \
"v_cvt_pk_bf16_f32 v90, v92, v93\n" \
"v_cvt_pk_bf16_f32 v91, v94, v95\n" \
"v_add_u32_e32 v48, s16, v20\n" \
"v_add_f32_e32 v54, v54, v22\n" \
"v_accvgpr_read_b32 v55, a194\n" \
"v_accvgpr_read_b32 v23, a195\n" \
"v_mfma_f32_16x16x32_bf16 a[192:195], a[216:219], a[216:219], 0\n" \
"v_max_f32_e32 v62, v62, v54\n" \
"v_add_f32_e32 v55, v55, v23\n" \
"v_max_f32_e32 v62, v62, v55\n" \
"v_rcp_f32_e32 v54, v54\n" \
"v_rcp_f32_e32 v55, v55\n" \
"v_mul_f32_e32 v54, v54, v58\n" \
"v_mul_f32_e32 v55, v55, v59\n" \
"v_mul_f32_e32 v128, v54, v128\n" \
"v_mul_f32_e32 v129, v54, v129\n" \
"v_mul_f32_e32 v136, v54, v136\n" \
"v_mul_f32_e32 v137, v54, v137\n" \
"v_mul_f32_e32 v144, v54, v144\n" \
"v_mul_f32_e32 v145, v54, v145\n" \
"v_mul_f32_e32 v152, v54, v152\n" \
"v_mul_f32_e32 v153, v54, v153\n" \
"v_mul_f32_e32 v160, v55, v160\n" \
"v_mul_f32_e32 v161, v55, v161\n" \
"v_mul_f32_e32 v168, v55, v168\n" \
"v_mul_f32_e32 v169, v55, v169\n" \
"v_mul_f32_e32 v176, v55, v176\n" \
"v_mul_f32_e32 v177, v55, v177\n" \
"v_mul_f32_e32 v184, v55, v184\n" \
"v_mul_f32_e32 v185, v55, v185\n" \
"v_cvt_pk_bf16_f32 v128, v128, v129\n" \
"v_mul_f32_e32 v130, v54, v130\n" \
"v_mul_f32_e32 v131, v54, v131\n" \
"v_cvt_pk_bf16_f32 v136, v136, v137\n" \
"v_mul_f32_e32 v138, v54, v138\n" \
"v_mul_f32_e32 v139, v54, v139\n" \
"v_cvt_pk_bf16_f32 v144, v144, v145\n" \
"v_mul_f32_e32 v146, v54, v146\n" \
"v_mul_f32_e32 v147, v54, v147\n" \
"v_cvt_pk_bf16_f32 v152, v152, v153\n" \
"v_mul_f32_e32 v154, v54, v154\n" \
"v_mul_f32_e32 v155, v54, v155\n" \
"v_cvt_pk_bf16_f32 v160, v160, v161\n" \
"v_mul_f32_e32 v162, v55, v162\n" \
"v_mul_f32_e32 v163, v55, v163\n" \
"v_cvt_pk_bf16_f32 v168, v168, v169\n" \
"v_mul_f32_e32 v170, v55, v170\n" \
"v_mul_f32_e32 v171, v55, v171\n" \
"v_cvt_pk_bf16_f32 v176, v176, v177\n" \
"v_mul_f32_e32 v178, v55, v178\n" \
"v_mul_f32_e32 v179, v55, v179\n" \
"v_cvt_pk_bf16_f32 v184, v184, v185\n" \
"v_mul_f32_e32 v186, v55, v186\n" \
"v_mul_f32_e32 v187, v55, v187\n" \
"v_cvt_pk_bf16_f32 v129, v130, v131\n" \
"v_mul_f32_e32 v132, v54, v132\n" \
"v_mul_f32_e32 v133, v54, v133\n" \
"v_mul_f32_e32 v134, v54, v134\n" \
"v_mul_f32_e32 v135, v54, v135\n" \
"v_cvt_pk_bf16_f32 v137, v138, v139\n" \
"v_mul_f32_e32 v140, v54, v140\n" \
"s_waitcnt vmcnt(12) lgkmcnt(0)\n" \
"s_barrier\n" \
"v_mfma_f32_16x16x32_bf16 v[120:123], a[128:131], a[48:51], v[44:47]\n" \
"s_xor_b32 m0, s65, 0x12000\n" \
"ds_read_b128 a[188:191], v49 offset:7168\n" \
"v_mfma_f32_16x16x32_bf16 v[120:123], a[132:135], a[52:55], v[120:123]\n" \
"buffer_load_dwordx4 v50, s[8:11], s64 offen offset:0 sc1 lds\n" \
"v_mfma_f32_16x16x32_bf16 v[120:123], a[136:139], a[56:59], v[120:123]\n" \
"ds_read_b128 a[184:187], v49 offset:6144\n" \
"ds_read_b128 a[180:183], v49 offset:5120\n" \
"v_mfma_f32_16x16x32_bf16 v[120:123], a[140:143], a[60:63], v[120:123]\n" \
"buffer_load_dwordx4 v50, s[8:11], s64 offen offset:1024 sc1 lds\n" \
"v_mfma_f32_16x16x32_bf16 v[124:127], a[156:159], a[60:63], v[44:47]\n" \
"ds_read_b128 a[176:179], v49 offset:4096\n" \
"ds_read_b128 a[172:175], v49 offset:3072\n" \
"v_mfma_f32_16x16x32_bf16 v[124:127], a[152:155], a[56:59], v[124:127]\n" \
"ds_read_b128 a[168:171], v49 offset:2048\n" \
"ds_read_b128 a[164:167], v49 offset:1024\n" \
"v_mfma_f32_16x16x32_bf16 v[124:127], a[148:151], a[52:55], v[124:127]\n" \
"ds_read_b128 a[160:163], v49 offset:0\n" \
"ds_read_b128 a[0:3], v49 offset:8192\n" \
"v_mfma_f32_16x16x32_bf16 v[124:127], a[144:147], a[48:51], v[124:127]\n" \
"buffer_load_dwordx4 v50, s[8:11], s64 offen offset:2048 sc1 lds\n" \
"v_mfma_f32_16x16x32_bf16 v[32:35], a[220:223], a[64:67], 0\n" \
"ds_read_b128 a[4:7], v49 offset:9216\n" \
"ds_read_b128 a[8:11], v49 offset:10240\n" \
"v_mfma_f32_16x16x32_bf16 v[36:39], a[220:223], a[80:83], 0\n" \
"ds_read_b128 a[12:15], v49 offset:11264\n" \
"ds_read_b128 a[16:19], v49 offset:12288\n" \
"v_mfma_f32_16x16x32_bf16 v[40:43], a[220:223], a[96:99], 0\n" \
"ds_read_b128 a[20:23], v49 offset:13312\n" \
"ds_read_b128 a[24:27], v49 offset:14336\n" \
"v_mfma_f32_16x16x32_bf16 v[44:47], a[220:223], a[112:115], 0\n" \
"buffer_load_dwordx4 v50, s[8:11], s64 offen offset:3072 sc1 lds\n" \
"v_mfma_f32_16x16x32_bf16 v[32:35], a[224:227], a[68:71], v[32:35]\n" \
"s_xor_b32 m0, s65, 0xa000\n" \
"ds_read_b128 a[28:31], v49 offset:15360\n" \
"v_mfma_f32_16x16x32_bf16 v[36:39], a[224:227], a[84:87], v[36:39]\n" \
"v_exp_f32_e32 v120, v120\n" \
"v_mfma_f32_16x16x32_bf16 v[40:43], a[224:227], a[100:103], v[40:43]\n" \
"buffer_load_dwordx4 v51, s[8:11], s64 offen offset:0 sc1 lds\n" \
"v_mfma_f32_16x16x32_bf16 v[44:47], a[224:227], a[116:119], v[44:47]\n" \
"v_exp_f32_e32 v121, v121\n" \
"v_mfma_f32_16x16x32_bf16 v[32:35], a[228:231], a[72:75], v[32:35]\n" \
"v_exp_f32_e32 v122, v122\n" \
"v_mfma_f32_16x16x32_bf16 v[36:39], a[228:231], a[88:91], v[36:39]\n" \
"v_exp_f32_e32 v123, v123\n" \
"v_mfma_f32_16x16x32_bf16 v[40:43], a[228:231], a[104:107], v[40:43]\n" \
"buffer_load_dwordx4 v51, s[8:11], s64 offen offset:1024 sc1 lds\n" \
"v_mfma_f32_16x16x32_bf16 v[44:47], a[228:231], a[120:123], v[44:47]\n" \
"v_cvt_pk_bf16_f32 v120, v120, v121\n" \
"v_cvt_pk_bf16_f32 v121, v122, v123\n" \
"v_mfma_f32_16x16x32_bf16 v[32:35], a[232:235], a[76:79], v[32:35]\n" \
"v_exp_f32_e32 v124, v124\n" \
"v_mfma_f32_16x16x32_bf16 v[36:39], a[232:235], a[92:95], v[36:39]\n" \
"v_exp_f32_e32 v125, v125\n" \
"v_mfma_f32_16x16x32_bf16 v[40:43], a[232:235], a[108:111], v[40:43]\n" \
"buffer_load_dwordx4 v51, s[8:11], s64 offen offset:2048 sc1 lds\n" \
"v_mfma_f32_16x16x32_bf16 v[44:47], a[232:235], a[124:127], v[44:47]\n" \
"v_exp_f32_e32 v126, v126\n" \
"v_smfmac_f32_16x16x64_bf16 a[196:199], a[200:203], v[84:91], v21\n" \
"v_cvt_pk_bf16_f32 v122, v124, v125\n" \
"s_waitcnt lgkmcnt(1)\n" \
"v_mfma_f32_16x16x32_bf16 v[220:223], a[188:191], v[84:87], v[220:223]\n" \
"buffer_load_dwordx4 v51, s[8:11], s64 offen offset:3072 sc1 lds\n" \
"v_mfma_f32_16x16x32_bf16 v[252:255], a[188:191], v[88:91], v[252:255]\n" \
"s_add_u32 m0, s93, 0x1000\n" \
"v_and_b32_e32 v120, v12, v120\n" \
"v_mfma_f32_16x16x32_bf16 v[216:219], a[184:187], v[84:87], v[216:219]\n" \
"v_exp_f32_e32 v127, v127\n" \
"v_mfma_f32_16x16x32_bf16 v[248:251], a[184:187], v[88:91], v[248:251]\n" \
"v_cvt_pk_bf16_f32 v123, v126, v127\n" \
"v_and_b32_e32 v121, v13, v121\n" \
"v_mfma_f32_16x16x32_bf16 v[212:215], a[180:183], v[84:87], v[212:215]\n" \
"buffer_load_dwordx4 v4, s[80:83], s92 offen offset:0 lds\n" \
"v_mfma_f32_16x16x32_bf16 v[244:247], a[180:183], v[88:91], v[244:247]\n" \
"v_and_b32_e32 v122, v14, v122\n" \
"v_and_b32_e32 v123, v15, v123\n" \
"v_mfma_f32_16x16x32_bf16 v[208:211], a[176:179], v[84:87], v[208:211]\n" \
"v_accvgpr_read_b32 v56, a196\n" \
"v_accvgpr_read_b32 v24, a197\n" \
"v_mfma_f32_16x16x32_bf16 v[240:243], a[176:179], v[88:91], v[240:243]\n" \
"ds_read_b128 a[128:131], v48 offset:0\n" \
"ds_read_b128 a[144:147], v48 offset:4096\n" \
"v_mfma_f32_16x16x32_bf16 v[204:207], a[172:175], v[84:87], v[204:207]\n" \
"buffer_load_dwordx4 v5, s[80:83], s92 offen offset:1024 lds\n" \
"v_mfma_f32_16x16x32_bf16 v[236:239], a[172:175], v[88:91], v[236:239]\n" \
"ds_read_b128 a[132:135], v48 offset:1024\n" \
"ds_read_b128 a[148:151], v48 offset:5120\n" \
"v_mfma_f32_16x16x32_bf16 v[200:203], a[168:171], v[84:87], v[200:203]\n" \
"ds_read_b128 a[136:139], v48 offset:2048\n" \
"ds_read_b128 a[152:155], v48 offset:6144\n" \
"v_mfma_f32_16x16x32_bf16 v[232:235], a[168:171], v[88:91], v[232:235]\n" \
"buffer_load_dwordx4 v6, s[80:83], s92 offen offset:2048 lds\n" \
"v_mfma_f32_16x16x32_bf16 v[196:199], a[164:167], v[84:87], v[196:199]\n" \
"ds_read_b128 a[140:143], v48 offset:3072\n" \
"ds_read_b128 a[156:159], v48 offset:7168\n" \
"v_mfma_f32_16x16x32_bf16 v[228:231], a[164:167], v[88:91], v[228:231]\n" \
"v_mul_f32_e32 v141, v54, v141\n" \
"v_mul_f32_e32 v142, v54, v142\n" \
"v_mfma_f32_16x16x32_bf16 v[192:195], a[160:163], v[84:87], v[192:195]\n" \
"v_mul_f32_e32 v143, v54, v143\n" \
"v_cvt_pk_bf16_f32 v145, v146, v147\n" \
"v_mfma_f32_16x16x32_bf16 v[224:227], a[160:163], v[88:91], v[224:227]\n" \
"buffer_load_dwordx4 v7, s[80:83], s92 offen offset:3072 lds\n" \
"v_mfma_f32_16x16x32_bf16 a[196:199], a[212:215], v[120:123], a[196:199]\n" \
"v_mul_f32_e32 v148, v54, v148\n" \
"v_mul_f32_e32 v149, v54, v149\n" \
"v_mfma_f32_16x16x32_bf16 v[224:227], a[0:3], v[120:123], v[224:227]\n" \
"v_mul_f32_e32 v150, v54, v150\n" \
"v_mul_f32_e32 v151, v54, v151\n" \
"v_mfma_f32_16x16x32_bf16 v[228:231], a[4:7], v[120:123], v[228:231]\n" \
"v_cvt_pk_bf16_f32 v153, v154, v155\n" \
"v_mul_f32_e32 v156, v54, v156\n" \
"v_mfma_f32_16x16x32_bf16 v[232:235], a[8:11], v[120:123], v[232:235]\n" \
"v_mul_f32_e32 v157, v54, v157\n" \
"v_mul_f32_e32 v158, v54, v158\n" \
"v_mfma_f32_16x16x32_bf16 v[236:239], a[12:15], v[120:123], v[236:239]\n" \
"v_mul_f32_e32 v159, v54, v159\n" \
"v_cvt_pk_bf16_f32 v161, v162, v163\n" \
"v_mfma_f32_16x16x32_bf16 v[240:243], a[16:19], v[120:123], v[240:243]\n" \
"v_mul_f32_e32 v164, v55, v164\n" \
"v_mul_f32_e32 v165, v55, v165\n" \
"v_mfma_f32_16x16x32_bf16 v[244:247], a[20:23], v[120:123], v[244:247]\n" \
"v_mul_f32_e32 v166, v55, v166\n" \
"v_mul_f32_e32 v167, v55, v167\n" \
"v_mfma_f32_16x16x32_bf16 v[248:251], a[24:27], v[120:123], v[248:251]\n" \
"v_cvt_pk_bf16_f32 v169, v170, v171\n" \
"s_waitcnt lgkmcnt(4)\n" \
"v_mfma_f32_16x16x32_bf16 v[252:255], a[28:31], v[120:123], v[252:255]\n" \
"v_mul_f32_e32 v172, v55, v172\n" \
"v_mul_f32_e32 v173, v55, v173\n" \
"v_mul_f32_e32 v174, v55, v174\n" \
"v_mul_f32_e32 v175, v55, v175\n" \
"v_cvt_pk_bf16_f32 v177, v178, v179\n" \
"v_mul_f32_e32 v180, v55, v180\n" \
"v_mul_f32_e32 v181, v55, v181\n" \
"v_mul_f32_e32 v182, v55, v182\n" \
"v_mul_f32_e32 v183, v55, v183\n" \
"v_cvt_pk_bf16_f32 v185, v186, v187\n" \
"v_mul_f32_e32 v188, v55, v188\n" \
"v_mul_f32_e32 v189, v55, v189\n" \
"v_mul_f32_e32 v190, v55, v190\n" \
"v_mul_f32_e32 v191, v55, v191\n" \
"v_cvt_pk_bf16_f32 v130, v132, v133\n" \
"v_cvt_pk_bf16_f32 v131, v134, v135\n" \
"buffer_store_dwordx4 v[128:131], v20, s[56:59], s68 offen offset:0 sc1\n" \
"v_cvt_pk_bf16_f32 v138, v140, v141\n" \
"v_cvt_pk_bf16_f32 v139, v142, v143\n" \
"buffer_store_dwordx4 v[136:139], v20, s[56:59], s68 offen offset:1024 sc1\n" \
"v_cvt_pk_bf16_f32 v146, v148, v149\n" \
"v_cvt_pk_bf16_f32 v147, v150, v151\n" \
"buffer_store_dwordx4 v[144:147], v20, s[56:59], s68 offen offset:2048 sc1\n" \
"v_cvt_pk_bf16_f32 v154, v156, v157\n" \
"v_cvt_pk_bf16_f32 v155, v158, v159\n" \
"buffer_store_dwordx4 v[152:155], v20, s[56:59], s68 offen offset:3072 sc1\n" \
"v_cvt_pk_bf16_f32 v162, v164, v165\n" \
"v_cvt_pk_bf16_f32 v163, v166, v167\n" \
"buffer_store_dwordx4 v[160:163], v20, s[56:59], s69 offen offset:0 sc1\n" \
"v_cvt_pk_bf16_f32 v170, v172, v173\n" \
"v_cvt_pk_bf16_f32 v171, v174, v175\n" \
"buffer_store_dwordx4 v[168:171], v20, s[56:59], s69 offen offset:1024 sc1\n" \
"v_cvt_pk_bf16_f32 v178, v180, v181\n" \
"v_cvt_pk_bf16_f32 v179, v182, v183\n" \
"buffer_store_dwordx4 v[176:179], v20, s[56:59], s69 offen offset:2048 sc1\n" \
"v_cvt_pk_bf16_f32 v186, v188, v189\n" \
"v_cvt_pk_bf16_f32 v187, v190, v191\n" \
"buffer_store_dwordx4 v[184:187], v20, s[56:59], s69 offen offset:3072 sc1\n" \
"s_add_u32 m0, s93, 0x2000\n" \
"v_mfma_f32_16x16x32_bf16 a[208:211], a[216:219], v[128:131], a[208:211]\n" \
"buffer_load_dwordx4 v8, s[80:83], s92 offen offset:0 lds\n" \
"v_mfma_f32_16x16x32_bf16 a[208:211], a[216:219], v[136:139], a[208:211]\n" \
"v_mfma_f32_32x32x16_bf16 v[128:143], a[216:219], a[216:219], 0\n" \
"v_mfma_f32_16x16x32_bf16 a[208:211], a[216:219], v[144:147], a[208:211]\n" \
"v_mfma_f32_16x16x32_bf16 a[208:211], a[216:219], v[152:155], a[208:211]\n" \
"v_mfma_f32_32x32x16_bf16 v[144:159], a[216:219], a[216:219], 0\n" \
"v_add_f32_e32 v56, v56, v24\n" \
"v_mfma_f32_16x16x32_bf16 a[208:211], a[216:219], v[160:163], a[208:211]\n" \
"v_max_f32_e32 v62, v62, v56\n" \
"v_accvgpr_read_b32 v57, a198\n" \
"s_waitcnt vmcnt(21) lgkmcnt(0)\n" \
"s_barrier\n" \
"v_mfma_f32_16x16x32_bf16 v[64:67], a[128:131], a[64:67], v[32:35]\n" \
"s_xor_b32 m0, s65, 0x18000\n" \
"v_accvgpr_read_b32 v25, a199\n" \
"v_mfma_f32_16x16x32_bf16 a[196:199], a[216:219], a[216:219], 0\n" \
"buffer_load_dwordx4 v52, s[8:11], s64 offen offset:0 sc1 lds\n" \
"v_mfma_f32_16x16x32_bf16 v[64:67], a[132:135], a[68:71], v[64:67]\n" \
"v_rcp_f32_e32 v56, v56\n" \
"v_mfma_f32_16x16x32_bf16 v[64:67], a[136:139], a[72:75], v[64:67]\n" \
"v_mfma_f32_16x16x32_bf16 a[208:211], a[216:219], v[168:171], a[208:211]\n" \
"v_mfma_f32_32x32x16_bf16 v[160:175], a[216:219], a[216:219], 0\n" \
"v_mul_f32_e32 v56, v56, v60\n" \
"v_mfma_f32_16x16x32_bf16 a[208:211], a[216:219], v[176:179], a[208:211]\n" \
"v_mul_f32_e32 v192, v56, v192\n" \
"v_mul_f32_e32 v193, v56, v193\n" \
"v_mfma_f32_16x16x32_bf16 v[64:67], a[140:143], a[76:79], v[64:67]\n" \
"buffer_load_dwordx4 v52, s[8:11], s64 offen offset:1024 sc1 lds\n" \
"v_mfma_f32_16x16x32_bf16 v[68:71], a[156:159], a[76:79], v[32:35]\n" \
"v_cvt_pk_bf16_f32 v192, v192, v193\n" \
"v_mul_f32_e32 v194, v56, v194\n" \
"v_mfma_f32_16x16x32_bf16 v[68:71], a[152:155], a[72:75], v[68:71]\n" \
"v_mul_f32_e32 v195, v56, v195\n" \
"v_cvt_pk_bf16_f32 v193, v194, v195\n" \
"v_mfma_f32_16x16x32_bf16 v[68:71], a[148:151], a[68:71], v[68:71]\n" \
"v_mfma_f32_16x16x32_bf16 a[208:211], a[216:219], v[184:187], a[208:211]\n" \
"v_mfma_f32_32x32x16_bf16 v[176:191], a[216:219], a[216:219], 0\n" \
"v_mul_f32_e32 v196, v56, v196\n" \
"v_mul_f32_e32 v197, v56, v197\n" \
"v_mul_f32_e32 v198, v56, v198\n" \
"v_mul_f32_e32 v199, v56, v199\n" \
"v_mul_f32_e32 v200, v56, v200\n" \
"v_mul_f32_e32 v201, v56, v201\n" \
"v_mfma_f32_16x16x32_bf16 v[68:71], a[144:147], a[64:67], v[68:71]\n" \
"buffer_load_dwordx4 v52, s[8:11], s64 offen offset:2048 sc1 lds\n" \
"v_mfma_f32_16x16x32_bf16 v[76:79], a[144:147], a[80:83], v[36:39]\n" \
"v_cvt_pk_bf16_f32 v194, v196, v197\n" \
"v_cvt_pk_bf16_f32 v195, v198, v199\n" \
"buffer_store_dwordx4 v[192:195], v20, s[56:59], s70 offen offset:0 sc1\n" \
"v_mfma_f32_16x16x32_bf16 v[76:79], a[148:151], a[84:87], v[76:79]\n" \
"v_cvt_pk_bf16_f32 v200, v200, v201\n" \
"v_mul_f32_e32 v202, v56, v202\n" \
"v_mfma_f32_16x16x32_bf16 v[76:79], a[152:155], a[88:91], v[76:79]\n" \
"v_mul_f32_e32 v203, v56, v203\n" \
"v_add_f32_e32 v57, v57, v25\n" \
"v_mfma_f32_16x16x32_bf16 v[76:79], a[156:159], a[92:95], v[76:79]\n" \
"buffer_load_dwordx4 v52, s[8:11], s64 offen offset:3072 sc1 lds\n" \
"v_mfma_f32_16x16x32_bf16 v[72:75], a[140:143], a[92:95], v[36:39]\n" \
"s_mov_b32 m0, s65\n" \
"v_cvt_pk_bf16_f32 v201, v202, v203\n" \
"v_mfma_f32_16x16x32_bf16 v[72:75], a[136:139], a[88:91], v[72:75]\n" \
"v_mfma_f32_16x16x32_bf16 a[208:211], a[216:219], v[192:195], a[208:211]\n" \
"v_mul_f32_e32 v204, v56, v204\n" \
"v_mul_f32_e32 v205, v56, v205\n" \
"v_mfma_f32_16x16x32_bf16 v[72:75], a[132:135], a[84:87], v[72:75]\n" \
"v_mul_f32_e32 v206, v56, v206\n" \
"v_mul_f32_e32 v207, v56, v207\n" \
"v_mfma_f32_16x16x32_bf16 v[72:75], a[128:131], a[80:83], v[72:75]\n" \
"buffer_load_dwordx4 v53, s[8:11], s64 offen offset:0 sc1 lds\n" \
"v_mfma_f32_16x16x32_bf16 v[80:83], a[128:131], a[96:99], v[40:43]\n" \
"v_mul_f32_e32 v208, v56, v208\n" \
"v_mul_f32_e32 v209, v56, v209\n" \
"v_mfma_f32_16x16x32_bf16 v[80:83], a[132:135], a[100:103], v[80:83]\n" \
"v_max_f32_e32 v62, v62, v57\n" \
"v_cvt_pk_bf16_f32 v202, v204, v205\n" \
"v_mfma_f32_16x16x32_bf16 v[80:83], a[136:139], a[104:107], v[80:83]\n" \
"buffer_load_dwordx4 v53, s[8:11], s64 offen offset:1024 sc1 lds\n" \
"v_mfma_f32_16x16x32_bf16 v[80:83], a[140:143], a[108:111], v[80:83]\n" \
"v_cvt_pk_bf16_f32 v203, v206, v207\n" \
"v_cvt_pk_bf16_f32 v208, v208, v209\n" \
"buffer_store_dwordx4 v[200:203], v20, s[56:59], s70 offen offset:1024 sc1\n" \
"v_mfma_f32_16x16x32_bf16 v[84:87], a[156:159], a[108:111], v[40:43]\n" \
"v_mul_f32_e32 v210, v56, v210\n" \
"v_mul_f32_e32 v211, v56, v211\n" \
"v_mfma_f32_16x16x32_bf16 v[84:87], a[152:155], a[104:107], v[84:87]\n" \
"v_rcp_f32_e32 v57, v57\n" \
"v_mfma_f32_16x16x32_bf16 v[84:87], a[148:151], a[100:103], v[84:87]\n" \
"buffer_load_dwordx4 v53, s[8:11], s64 offen offset:2048 sc1 lds\n" \
"v_mfma_f32_16x16x32_bf16 v[84:87], a[144:147], a[96:99], v[84:87]\n" \
"v_mfma_f32_16x16x32_bf16 a[208:211], a[216:219], v[200:203], a[208:211]\n" \
"v_mfma_f32_32x32x16_bf16 v[192:207], a[216:219], a[216:219], 0\n" \
"v_cvt_pk_bf16_f32 v209, v210, v211\n" \
"v_mul_f32_e32 v212, v56, v212\n" \
".fill (L_ab + 20480 - .) / 4, 4, 0xBF800000\n" \
"L_ap5:\n" \
"s_cbranch_execz L_ap0\n" \
"v_mul_f32_e32 v213, v56, v213\n" \
"v_mul_f32_e32 v214, v56, v214\n" \
"v_mul_f32_e32 v215, v56, v215\n" \
"v_mul_f32_e32 v216, v56, v216\n" \
"v_mfma_f32_16x16x32_bf16 v[92:95], a[144:147], a[112:115], v[44:47]\n" \
"ds_read_b128 a[144:147], v48 offset:12288\n" \
"v_mul_f32_e32 v217, v56, v217\n" \
"v_mfma_f32_16x16x32_bf16 v[92:95], a[148:151], a[116:119], v[92:95]\n" \
"ds_read_b128 a[148:151], v48 offset:13312\n" \
"v_cvt_pk_bf16_f32 v210, v212, v213\n" \
"v_mfma_f32_16x16x32_bf16 v[92:95], a[152:155], a[120:123], v[92:95]\n" \
"ds_read_b128 a[152:155], v48 offset:14336\n" \
"v_cvt_pk_bf16_f32 v211, v214, v215\n" \
"buffer_store_dwordx4 v[208:211], v20, s[56:59], s70 offen offset:2048 sc1\n" \
"v_mfma_f32_16x16x32_bf16 v[92:95], a[156:159], a[124:127], v[92:95]\n" \
"ds_read_b128 a[156:159], v48 offset:15360\n" \
"v_cvt_pk_bf16_f32 v216, v216, v217\n" \
"v_mfma_f32_16x16x32_bf16 v[88:91], a[140:143], a[124:127], v[44:47]\n" \
"ds_read_b128 a[140:143], v48 offset:11264\n" \
"v_mul_f32_e32 v218, v56, v218\n" \
"v_mfma_f32_16x16x32_bf16 v[88:91], a[136:139], a[120:123], v[88:91]\n" \
"ds_read_b128 a[136:139], v48 offset:10240\n" \
"v_mul_f32_e32 v219, v56, v219\n" \
"v_mfma_f32_16x16x32_bf16 v[88:91], a[132:135], a[116:119], v[88:91]\n" \
"ds_read_b128 a[132:135], v48 offset:9216\n" \
"v_mul_f32_e32 v57, v57, v61\n" \
"v_mfma_f32_16x16x32_bf16 v[88:91], a[128:131], a[112:115], v[88:91]\n" \
"ds_read_b128 a[128:131], v48 offset:8192\n" \
"v_mfma_f32_16x16x32_bf16 a[208:211], a[216:219], v[208:211], a[208:211]\n" \
"v_cvt_pk_bf16_f32 v217, v218, v219\n" \
"v_mul_f32_e32 v220, v56, v220\n" \
"v_mul_f32_e32 v221, v56, v221\n" \
"v_mul_f32_e32 v222, v56, v222\n" \
"v_mul_f32_e32 v223, v56, v223\n" \
"v_mul_f32_e32 v224, v57, v224\n" \
"v_mul_f32_e32 v225, v57, v225\n" \
"v_cvt_pk_bf16_f32 v218, v220, v221\n" \
"v_cvt_pk_bf16_f32 v219, v222, v223\n" \
"buffer_store_dwordx4 v[216:219], v20, s[56:59], s70 offen offset:3072 sc1\n" \
"v_cvt_pk_bf16_f32 v224, v224, v225\n" \
"v_mul_f32_e32 v226, v57, v226\n" \
"v_mul_f32_e32 v227, v57, v227\n" \
"v_mfma_f32_16x16x32_bf16 a[208:211], a[216:219], v[216:219], a[208:211]\n" \
"v_mfma_f32_32x32x16_bf16 v[208:223], a[216:219], a[216:219], 0\n" \
"v_cvt_pk_bf16_f32 v225, v226, v227\n" \
"v_mul_f32_e32 v228, v57, v228\n" \
"v_mul_f32_e32 v229, v57, v229\n" \
"v_mul_f32_e32 v230, v57, v230\n" \
"v_mul_f32_e32 v231, v57, v231\n" \
"v_mul_f32_e32 v232, v57, v232\n" \
"v_mul_f32_e32 v233, v57, v233\n" \
"v_cvt_pk_bf16_f32 v226, v228, v229\n" \
"v_cvt_pk_bf16_f32 v227, v230, v231\n" \
"buffer_store_dwordx4 v[224:227], v20, s[56:59], s71 offen offset:0 sc1\n" \
"v_cvt_pk_bf16_f32 v232, v232, v233\n" \
"v_mul_f32_e32 v234, v57, v234\n" \
"v_mul_f32_e32 v235, v57, v235\n" \
"v_mfma_f32_16x16x32_bf16 a[208:211], a[216:219], v[224:227], a[208:211]\n" \
"v_cvt_pk_bf16_f32 v233, v234, v235\n" \
"v_mul_f32_e32 v236, v57, v236\n" \
"v_mul_f32_e32 v237, v57, v237\n" \
"v_mul_f32_e32 v238, v57, v238\n" \
"v_mul_f32_e32 v239, v57, v239\n" \
"v_mul_f32_e32 v240, v57, v240\n" \
"v_mul_f32_e32 v241, v57, v241\n" \
"v_cvt_pk_bf16_f32 v234, v236, v237\n" \
"v_cvt_pk_bf16_f32 v235, v238, v239\n" \
"buffer_store_dwordx4 v[232:235], v20, s[56:59], s71 offen offset:1024 sc1\n" \
"v_cvt_pk_bf16_f32 v240, v240, v241\n" \
"v_mul_f32_e32 v242, v57, v242\n" \
"v_mul_f32_e32 v243, v57, v243\n" \
"v_mfma_f32_16x16x32_bf16 a[208:211], a[216:219], v[232:235], a[208:211]\n" \
"v_mfma_f32_32x32x16_bf16 v[224:239], a[216:219], a[216:219], 0\n" \
"v_cvt_pk_bf16_f32 v241, v242, v243\n" \
"v_mul_f32_e32 v244, v57, v244\n" \
"v_mul_f32_e32 v245, v57, v245\n" \
"v_mul_f32_e32 v246, v57, v246\n" \
"v_mul_f32_e32 v247, v57, v247\n" \
"v_mul_f32_e32 v248, v57, v248\n" \
"v_mul_f32_e32 v249, v57, v249\n" \
"v_cvt_pk_bf16_f32 v242, v244, v245\n" \
"v_cvt_pk_bf16_f32 v243, v246, v247\n" \
"buffer_store_dwordx4 v[240:243], v20, s[56:59], s71 offen offset:2048 sc1\n" \
"v_cvt_pk_bf16_f32 v248, v248, v249\n" \
"v_mul_f32_e32 v250, v57, v250\n" \
"v_mul_f32_e32 v251, v57, v251\n" \
"v_mfma_f32_16x16x32_bf16 a[208:211], a[216:219], v[240:243], a[208:211]\n" \
"v_cvt_pk_bf16_f32 v249, v250, v251\n" \
"v_mul_f32_e32 v252, v57, v252\n" \
"v_mul_f32_e32 v253, v57, v253\n" \
"v_mul_f32_e32 v254, v57, v254\n" \
"v_mul_f32_e32 v255, v57, v255\n" \
"v_cvt_pk_bf16_f32 v250, v252, v253\n" \
"v_cvt_pk_bf16_f32 v251, v254, v255\n" \
"buffer_store_dwordx4 v[248:251], v20, s[56:59], s71 offen offset:3072 sc1\n" \
"buffer_load_dwordx4 v53, s[8:11], s64 offen offset:3072 sc1 lds\n" \
"s_waitcnt vmcnt(16) lgkmcnt(0)\n" \
"s_barrier\n" \
"v_mfma_f32_16x16x32_bf16 a[208:211], a[216:219], v[248:251], a[208:211]\n" \
"v_mfma_f32_32x32x16_bf16 v[240:255], a[216:219], a[216:219], 0\n" \
"v_exp_f32_e32 v70, v70\n" \
"v_exp_f32_e32 v71, v71\n" \
"v_exp_f32_e32 v72, v72\n" \
"v_exp_f32_e32 v73, v73\n" \
"s_add_u32 s64, s64, 0x10000\n" \
"v_cmp_lt_f32_e64 s[72:73], s21, v62\n" \
"v_readlane_b32 s95, v11, 3\n" \
"v_accvgpr_read_b32 v63, a208\n" \
"v_cmp_u_f32_e32 vcc, v63, v63\n" \
"v_exp_f32_e32 v68, v68\n" \
"v_exp_f32_e32 v69, v69\n" \
"v_exp_f32_e32 v74, v74\n" \
"v_exp_f32_e32 v75, v75\n" \
"s_or_b64 vcc, s[72:73], vcc\n" \
"s_cselect_b32 s95, 0x40000000, s95\n" \
"s_cselect_b32 s94, 0x7f800000, 0\n" \
"v_cvt_pk_bf16_f32 v71, v70, v71\n" \
"v_exp_f32_e32 v66, v66\n" \
"v_exp_f32_e32 v67, v67\n" \
"v_exp_f32_e32 v64, v64\n" \
"v_exp_f32_e32 v65, v65\n" \
"v_cvt_pk_bf16_f32 v72, v72, v73\n" \
"v_exp_f32_e32 v76, v76\n" \
"v_exp_f32_e32 v77, v77\n" \
"v_exp_f32_e32 v78, v78\n" \
"v_exp_f32_e32 v79, v79\n" \
"v_cvt_pk_bf16_f32 v70, v68, v69\n" \
"v_cvt_pk_bf16_f32 v73, v74, v75\n" \
"v_add_u32_e32 v49, 0x10000, v48\n" \
"s_xor_b32 s65, s65, 0x8000\n" \
"v_cvt_pk_bf16_f32 v69, v66, v67\n" \
"v_cvt_pk_bf16_f32 v68, v64, v65\n" \
"v_cvt_pk_bf16_f32 v74, v76, v77\n" \
"v_cvt_pk_bf16_f32 v75, v78, v79\n" \
"v_xor_b32_e32 v48, 0x8000, v48\n" \
"s_sub_u32 s66, s19, 2\n" \
"s_branch L_2_%=\n" \
"L_f1_%=:\n" \
"ds_read_b128 a[160:163], v49 offset:0\n" \
"ds_read_b128 a[164:167], v49 offset:1024\n" \
"ds_read_b128 a[168:171], v49 offset:2048\n" \
"v_mfma_f32_16x16x32_bf16 v[104:107], a[128:131], a[80:83], v[36:39]\n" \
"v_cvt_pk_bf16_f32 v192, v192, v193\n" \
"v_cvt_pk_bf16_f32 v224, v224, v225\n" \
"v_cvt_pk_bf16_f32 v193, v194, v195\n" \
"v_cvt_pk_bf16_f32 v225, v226, v227\n" \
"ds_read_b128 a[172:175], v49 offset:3072\n" \
"v_cvt_pk_bf16_f32 v194, v196, v197\n" \
"v_cvt_pk_bf16_f32 v195, v198, v199\n" \
"v_cvt_pk_bf16_f32 v226, v228, v229\n" \
"v_cvt_pk_bf16_f32 v227, v230, v231\n" \
"v_mfma_f32_16x16x32_bf16 v[104:107], a[132:135], a[84:87], v[104:107]\n" \
"v_lshl_add_u32 v50, s15, 2, v20\n" \
"v_cvt_pk_bf16_f32 v196, v200, v201\n" \
"v_cvt_pk_bf16_f32 v197, v202, v203\n" \
"v_cvt_pk_bf16_f32 v198, v204, v205\n" \
"v_cvt_pk_bf16_f32 v199, v206, v207\n" \
"v_cvt_pk_bf16_f32 v228, v232, v233\n" \
"v_cvt_pk_bf16_f32 v229, v234, v235\n" \
"v_cvt_pk_bf16_f32 v230, v236, v237\n" \
"v_cvt_pk_bf16_f32 v231, v238, v239\n" \
"v_add_u32_e32 v50, 0x1a000, v50\n" \
"v_mov_b32_e32 v61, s62\n" \
"v_cvt_pk_bf16_f32 v200, v208, v209\n" \
"v_cvt_pk_bf16_f32 v201, v210, v211\n" \
"v_cvt_pk_bf16_f32 v202, v212, v213\n" \
"v_cvt_pk_bf16_f32 v203, v214, v215\n" \
"v_cvt_pk_bf16_f32 v204, v216, v217\n" \
"v_cvt_pk_bf16_f32 v205, v218, v219\n" \
"v_cvt_pk_bf16_f32 v206, v220, v221\n" \
"v_cvt_pk_bf16_f32 v207, v222, v223\n" \
"v_cvt_pk_bf16_f32 v232, v240, v241\n" \
"v_cvt_pk_bf16_f32 v233, v242, v243\n" \
"v_cvt_pk_bf16_f32 v234, v244, v245\n" \
"v_cvt_pk_bf16_f32 v235, v246, v247\n" \
"v_cvt_pk_bf16_f32 v236, v248, v249\n" \
"v_cvt_pk_bf16_f32 v237, v250, v251\n" \
"v_cvt_pk_bf16_f32 v238, v252, v253\n" \
"v_cvt_pk_bf16_f32 v239, v254, v255\n" \
"ds_write_b128 v50, v[192:195] offset:0\n" \
"v_mfma_f32_16x16x32_bf16 v[104:107], a[136:139], a[88:91], v[104:107]\n" \
"ds_read_b128 a[176:179], v49 offset:4096\n" \
"v_add_u32_e32 v56, 0x49000, v20\n" \
"v_mfma_f32_16x16x32_bf16 v[104:107], a[140:143], a[92:95], v[104:107]\n" \
"ds_write_b128 v50, v[196:199] offset:1024\n" \
"v_mfma_f32_16x16x32_bf16 v[108:111], a[156:159], a[92:95], v[36:39]\n" \
"ds_read_b128 a[180:183], v49 offset:5120\n" \
"ds_read_b128 a[184:187], v49 offset:6144\n" \
"v_mfma_f32_16x16x32_bf16 v[108:111], a[152:155], a[88:91], v[108:111]\n" \
"ds_write_b128 v50, v[200:203] offset:2048\n" \
"v_mfma_f32_16x16x32_bf16 v[108:111], a[148:151], a[84:87], v[108:111]\n" \
"v_and_b32_e32 v68, v12, v68\n" \
"v_and_b32_e32 v69, v13, v69\n" \
"v_mfma_f32_16x16x32_bf16 v[108:111], a[144:147], a[80:83], v[108:111]\n" \
"ds_write_b128 v50, v[204:207] offset:3072\n" \
"v_and_b32_e32 v70, v14, v70\n" \
"v_and_b32_e32 v71, v15, v71\n" \
"ds_read_b128 a[188:191], v49 offset:7168\n" \
"ds_read_b128 a[0:3], v20 offset:8192\n" \
"s_waitcnt lgkmcnt(9)\n" \
"v_mfma_f32_16x16x32_bf16 v[128:131], a[160:163], v[68:71], v[128:131]\n" \
"ds_write_b128 v50, v[224:227] offset:4096\n" \
"v_mfma_f32_16x16x32_bf16 v[160:163], a[160:163], v[72:75], v[160:163]\n" \
"ds_read_b128 a[4:7], v20 offset:9216\n" \
"ds_read_b128 a[8:11], v20 offset:10240\n" \
"v_mfma_f32_16x16x32_bf16 v[132:135], a[164:167], v[68:71], v[132:135]\n" \
"ds_write_b128 v50, v[228:231] offset:5120\n" \
"v_mfma_f32_16x16x32_bf16 v[164:167], a[164:167], v[72:75], v[164:167]\n" \
"ds_read_b128 a[12:15], v20 offset:11264\n" \
"ds_read_b128 a[28:31], v20 offset:15360\n" \
"v_mfma_f32_16x16x32_bf16 v[136:139], a[168:171], v[68:71], v[136:139]\n" \
"ds_write_b128 v50, v[232:235] offset:6144\n" \
"v_mfma_f32_16x16x32_bf16 v[168:171], a[168:171], v[72:75], v[168:171]\n" \
"ds_read_b128 a[24:27], v20 offset:14336\n" \
"ds_read_b128 a[20:23], v20 offset:13312\n" \
"v_mfma_f32_16x16x32_bf16 v[140:143], a[172:175], v[68:71], v[140:143]\n" \
"ds_write_b128 v50, v[236:239] offset:7168\n" \
"v_mfma_f32_16x16x32_bf16 v[172:175], a[172:175], v[72:75], v[172:175]\n" \
"ds_read_b128 a[16:19], v20 offset:12288\n" \
"s_waitcnt lgkmcnt(15)\n" \
"v_mfma_f32_16x16x32_bf16 v[144:147], a[176:179], v[68:71], v[144:147]\n" \
"v_xor_b32_e32 v48, 0x4000, v49\n" \
"ds_read_b32 v58, v18 offset:256\n" \
"v_mfma_f32_16x16x32_bf16 v[176:179], a[176:179], v[72:75], v[176:179]\n" \
"ds_read_b32 v59, v18 offset:384\n" \
"ds_read_b128 a[32:35], v48 offset:8192\n" \
"v_mfma_f32_16x16x32_bf16 v[148:151], a[180:183], v[68:71], v[148:151]\n" \
"ds_read_b128 a[36:39], v48 offset:9216\n" \
"ds_read_b128 a[40:43], v48 offset:10240\n" \
"v_mfma_f32_16x16x32_bf16 v[180:183], a[180:183], v[72:75], v[180:183]\n" \
"ds_read_b128 a[44:47], v48 offset:11264\n" \
"ds_read_b128 a[48:51], v48 offset:12288\n" \
"v_mfma_f32_16x16x32_bf16 v[152:155], a[184:187], v[68:71], v[152:155]\n" \
"ds_read_b128 a[52:55], v48 offset:13312\n" \
"ds_read_b128 a[56:59], v48 offset:14336\n" \
"v_mfma_f32_16x16x32_bf16 v[184:187], a[184:187], v[72:75], v[184:187]\n" \
"ds_read_b128 a[60:63], v48 offset:15360\n" \
"s_waitcnt lgkmcnt(15)\n" \
"v_mfma_f32_16x16x32_bf16 v[156:159], a[188:191], v[68:71], v[156:159]\n" \
"ds_read_b128 a[128:131], v49 offset:8192\n" \
"ds_read_b128 a[132:135], v49 offset:9216\n" \
"v_mfma_f32_16x16x32_bf16 v[188:191], a[188:191], v[72:75], v[188:191]\n" \
"ds_read_b128 a[136:139], v49 offset:10240\n" \
"ds_read_b128 a[140:143], v49 offset:11264\n" \
"v_smfmac_f32_16x16x64_bf16 a[192:195], a[200:203], v[68:75], v21\n" \
"ds_read_b128 a[144:147], v49 offset:12288\n" \
"ds_read_b128 a[148:151], v49 offset:13312\n" \
"v_mfma_f32_16x16x32_bf16 v[112:115], a[0:3], a[64:67], v[32:35]\n" \
"ds_read_b128 a[152:155], v49 offset:14336\n" \
"ds_read_b128 a[156:159], v49 offset:15360\n" \
"v_mfma_f32_16x16x32_bf16 v[112:115], a[4:7], a[68:71], v[112:115]\n" \
"v_accvgpr_read_b32 v40, a196\n" \
"v_accvgpr_read_b32 v24, a197\n" \
"v_mfma_f32_16x16x32_bf16 v[112:115], a[8:11], a[72:75], v[112:115]\n" \
"v_accvgpr_read_b32 v41, a198\n" \
"v_accvgpr_read_b32 v25, a199\n" \
"v_mfma_f32_16x16x32_bf16 v[112:115], a[12:15], a[76:79], v[112:115]\n" \
"v_add_f32_e32 v40, v40, v24\n" \
"v_add_f32_e32 v41, v41, v25\n" \
"v_mfma_f32_16x16x32_bf16 v[116:119], a[28:31], a[76:79], v[32:35]\n" \
"v_add_f32_e32 v40, s94, v40\n" \
"s_waitcnt lgkmcnt(15)\n" \
"v_mfma_f32_16x16x32_bf16 v[116:119], a[24:27], a[72:75], v[116:119]\n" \
"v_add_f32_e32 v41, s94, v41\n" \
"ds_write_b64 v19, v[40:41]\n" \
"v_mfma_f32_16x16x32_bf16 v[116:119], a[20:23], a[68:71], v[116:119]\n" \
"v_add_u32_e32 v52, s15, v20\n" \
"v_add_u32_e32 v51, 0x10000, v52\n" \
"v_mfma_f32_16x16x32_bf16 v[116:119], a[16:19], a[64:67], v[116:119]\n" \
"v_exp_f32_e32 v104, v104\n" \
"v_mfma_f32_16x16x32_bf16 v[124:127], a[16:19], a[80:83], v[36:39]\n" \
"v_exp_f32_e32 v105, v105\n" \
"v_mfma_f32_16x16x32_bf16 v[124:127], a[20:23], a[84:87], v[124:127]\n" \
"v_exp_f32_e32 v106, v106\n" \
"v_mfma_f32_16x16x32_bf16 v[124:127], a[24:27], a[88:91], v[124:127]\n" \
"v_exp_f32_e32 v107, v107\n" \
"v_mfma_f32_16x16x32_bf16 v[124:127], a[28:31], a[92:95], v[124:127]\n" \
"v_cvt_pk_bf16_f32 v104, v104, v105\n" \
"v_cvt_pk_bf16_f32 v105, v106, v107\n" \
"v_mfma_f32_16x16x32_bf16 v[120:123], a[12:15], a[92:95], v[36:39]\n" \
"v_exp_f32_e32 v108, v108\n" \
"v_mfma_f32_16x16x32_bf16 v[120:123], a[8:11], a[88:91], v[120:123]\n" \
"v_exp_f32_e32 v109, v109\n" \
"v_mfma_f32_16x16x32_bf16 v[120:123], a[4:7], a[84:87], v[120:123]\n" \
"v_exp_f32_e32 v110, v110\n" \
"v_mfma_f32_16x16x32_bf16 v[120:123], a[0:3], a[80:83], v[120:123]\n" \
"v_exp_f32_e32 v111, v111\n" \
"s_waitcnt lgkmcnt(0)\n" \
"s_barrier\n" \
"ds_read_b128 v[208:211], v52 offset:0\n" \
"ds_read_b128 v[212:215], v52 offset:1024\n" \
"ds_read_b128 v[216:219], v52 offset:2048\n" \
"ds_read_b128 v[220:223], v52 offset:3072\n" \
"ds_read_b128 v[240:243], v51 offset:32768\n" \
"ds_read_b128 v[244:247], v51 offset:33792\n" \
"ds_read_b128 v[248:251], v51 offset:34816\n" \
"ds_read_b128 v[252:255], v51 offset:35840\n" \
"v_cvt_pk_bf16_f32 v106, v108, v109\n" \
"v_cvt_pk_bf16_f32 v107, v110, v111\n" \
"v_and_b32_e32 v104, v12, v104\n" \
"v_and_b32_e32 v105, v13, v105\n" \
"v_and_b32_e32 v106, v14, v106\n" \
"s_waitcnt vmcnt(0)\n" \
"s_mov_b64 exec, 1\n" \
"buffer_store_dword v61, off, s[56:59], s95 offset:0 sc1\n" \
"s_mov_b64 exec, -1\n" \
"v_and_b32_e32 v107, v15, v107\n" \
"v_xor_b32_e32 v63, 0x400, v19\n" \
"ds_read_b64 v[42:43], v63\n" \
"v_mfma_f32_16x16x32_bf16 a[192:195], a[212:215], v[104:107], a[192:195]\n" \
"v_mov_b32_e32 v194, v28\n" \
"v_mov_b32_e32 v195, v29\n" \
"v_mfma_f32_16x16x32_bf16 v[160:163], a[128:131], v[104:107], v[160:163]\n" \
"v_mov_b32_e32 v196, 0\n" \
"v_mov_b32_e32 v197, 0\n" \
"v_mfma_f32_16x16x32_bf16 v[164:167], a[132:135], v[104:107], v[164:167]\n" \
"v_mov_b32_e32 v192, 0\n" \
"v_mov_b32_e32 v193, 0\n" \
"v_mfma_f32_16x16x32_bf16 v[168:171], a[136:139], v[104:107], v[168:171]\n" \
"v_readlane_b32 s93, v11, 22\n" \
"v_readlane_b32 s92, v11, 27\n" \
"v_mfma_f32_16x16x32_bf16 v[172:175], a[140:143], v[104:107], v[172:175]\n" \
"buffer_load_dwordx4 a[96:99], v20, s[56:59], s60 offen offset:0 sc1\n" \
"v_mfma_f32_16x16x32_bf16 v[176:179], a[144:147], v[104:107], v[176:179]\n" \
"v_exp_f32_e32 v118, v118\n" \
"v_mfma_f32_16x16x32_bf16 v[180:183], a[148:151], v[104:107], v[180:183]\n" \
"v_exp_f32_e32 v119, v119\n" \
"v_mfma_f32_16x16x32_bf16 v[184:187], a[152:155], v[104:107], v[184:187]\n" \
"s_add_u32 m0, s93, 0x0\n" \
"v_cvt_pk_bf16_f32 v119, v118, v119\n" \
"v_mfma_f32_16x16x32_bf16 v[188:191], a[156:159], v[104:107], v[188:191]\n" \
"v_exp_f32_e32 v120, v120\n" \
"s_waitcnt lgkmcnt(1)\n" \
"s_barrier\n" \
"buffer_load_dwordx4 a[112:115], v56, s[56:59], s60 offen offset:0 sc1\n" \
"v_mfma_f32_16x16x32_bf16 v[128:131], v[194:197], v[208:211], v[128:131]\n" \
"v_exp_f32_e32 v121, v121\n" \
"v_mfma_f32_16x16x32_bf16 v[132:135], v[192:195], v[208:211], v[132:135]\n" \
"v_exp_f32_e32 v116, v116\n" \
"v_mfma_f32_16x16x32_bf16 v[136:139], v[194:197], v[212:215], v[136:139]\n" \
"v_exp_f32_e32 v117, v117\n" \
"v_mfma_f32_16x16x32_bf16 v[140:143], v[192:195], v[212:215], v[140:143]\n" \
"v_exp_f32_e32 v122, v122\n" \
"v_mfma_f32_16x16x32_bf16 v[144:147], v[194:197], v[216:219], v[144:147]\n" \
"v_exp_f32_e32 v123, v123\n" \
"v_mfma_f32_16x16x32_bf16 v[148:151], v[192:195], v[216:219], v[148:151]\n" \
"buffer_load_dwordx4 a[100:103], v20, s[56:59], s60 offen offset:1024 sc1\n" \
"v_mfma_f32_16x16x32_bf16 v[152:155], v[194:197], v[220:223], v[152:155]\n" \
"v_exp_f32_e32 v114, v114\n" \
"v_mfma_f32_16x16x32_bf16 v[156:159], v[192:195], v[220:223], v[156:159]\n" \
"v_exp_f32_e32 v115, v115\n" \
"v_mfma_f32_16x16x32_bf16 v[160:163], v[194:197], v[240:243], v[160:163]\n" \
"v_exp_f32_e32 v112, v112\n" \
"v_mfma_f32_16x16x32_bf16 v[164:167], v[192:195], v[240:243], v[164:167]\n" \
"v_exp_f32_e32 v113, v113\n" \
"v_mfma_f32_16x16x32_bf16 v[168:171], v[194:197], v[244:247], v[168:171]\n" \
"v_cvt_pk_bf16_f32 v120, v120, v121\n" \
"v_cvt_pk_bf16_f32 v118, v116, v117\n" \
"v_mfma_f32_16x16x32_bf16 v[172:175], v[192:195], v[244:247], v[172:175]\n" \
"buffer_load_dwordx4 a[116:119], v56, s[56:59], s60 offen offset:1024 sc1\n" \
"v_mfma_f32_16x16x32_bf16 v[176:179], v[194:197], v[248:251], v[176:179]\n" \
"v_exp_f32_e32 v124, v124\n" \
"v_mfma_f32_16x16x32_bf16 v[180:183], v[192:195], v[248:251], v[180:183]\n" \
"v_exp_f32_e32 v125, v125\n" \
"v_mfma_f32_16x16x32_bf16 v[184:187], v[194:197], v[252:255], v[184:187]\n" \
"v_exp_f32_e32 v126, v126\n" \
"v_mfma_f32_16x16x32_bf16 v[188:191], v[192:195], v[252:255], v[188:191]\n" \
"v_exp_f32_e32 v127, v127\n" \
"v_cvt_pk_bf16_f32 v121, v122, v123\n" \
"v_cvt_pk_bf16_f32 v117, v114, v115\n" \
"v_cvt_pk_bf16_f32 v116, v112, v113\n" \
"v_cvt_pk_bf16_f32 v122, v124, v125\n" \
"v_cvt_pk_bf16_f32 v123, v126, v127\n" \
"buffer_load_dwordx4 a[104:107], v20, s[56:59], s60 offen offset:2048 sc1\n" \
"s_nop 0\n" \
"v_smfmac_f32_16x16x64_bf16 a[192:195], a[200:203], v[116:123], v21\n" \
"v_mfma_f32_16x16x32_bf16 v[128:131], a[32:35], v[116:119], v[128:131]\n" \
"v_mfma_f32_16x16x32_bf16 v[160:163], a[32:35], v[120:123], v[160:163]\n" \
"v_mfma_f32_16x16x32_bf16 v[132:135], a[36:39], v[116:119], v[132:135]\n" \
"v_mfma_f32_16x16x32_bf16 v[164:167], a[36:39], v[120:123], v[164:167]\n" \
"v_mfma_f32_16x16x32_bf16 v[136:139], a[40:43], v[116:119], v[136:139]\n" \
"buffer_load_dwordx4 a[120:123], v56, s[56:59], s60 offen offset:2048 sc1\n" \
"v_mfma_f32_16x16x32_bf16 v[168:171], a[40:43], v[120:123], v[168:171]\n" \
"v_mfma_f32_16x16x32_bf16 v[140:143], a[44:47], v[116:119], v[140:143]\n" \
"v_accvgpr_read_b32 v54, a192\n" \
"v_accvgpr_read_b32 v22, a193\n" \
"v_mfma_f32_16x16x32_bf16 v[172:175], a[44:47], v[120:123], v[172:175]\n" \
"v_add_f32_e32 v54, v54, v22\n" \
"s_waitcnt lgkmcnt(0)\n" \
"v_add_f32_e32 v54, v54, v42\n" \
"v_mfma_f32_16x16x32_bf16 v[144:147], a[48:51], v[116:119], v[144:147]\n" \
"v_max_f32_e32 v62, v62, v54\n" \
"v_accvgpr_read_b32 v55, a194\n" \
"v_mfma_f32_16x16x32_bf16 v[176:179], a[48:51], v[120:123], v[176:179]\n" \
"v_rcp_f32_e32 v54, v54\n" \
"buffer_load_dwordx4 a[108:111], v20, s[56:59], s60 offen offset:3072 sc1\n" \
"v_mfma_f32_16x16x32_bf16 v[148:151], a[52:55], v[116:119], v[148:151]\n" \
"v_mul_f32_e32 v54, v54, v58\n" \
"v_mul_f32_e32 v128, v54, v128\n" \
"v_mfma_f32_16x16x32_bf16 v[180:183], a[52:55], v[120:123], v[180:183]\n" \
"v_mul_f32_e32 v129, v54, v129\n" \
"v_mul_f32_e32 v130, v54, v130\n" \
"v_mul_f32_e32 v131, v54, v131\n" \
"v_mul_f32_e32 v132, v54, v132\n" \
"v_mul_f32_e32 v133, v54, v133\n" \
"v_mul_f32_e32 v134, v54, v134\n" \
"v_mul_f32_e32 v135, v54, v135\n" \
"v_cvt_pk_bf16_f32 v64, v128, v129\n" \
"v_cvt_pk_bf16_f32 v65, v130, v131\n" \
"v_cvt_pk_bf16_f32 v66, v132, v133\n" \
"v_cvt_pk_bf16_f32 v67, v134, v135\n" \
"v_accvgpr_read_b32 v23, a195\n" \
"v_mfma_f32_16x16x32_bf16 v[152:155], a[56:59], v[116:119], v[152:155]\n" \
"v_mfma_f32_16x16x32_bf16 a[208:211], a[216:219], v[64:67], a[208:211]\n" \
"buffer_load_dwordx4 a[124:127], v56, s[56:59], s60 offen offset:3072 sc1\n" \
"v_mul_f32_e32 v136, v54, v136\n" \
"v_mul_f32_e32 v137, v54, v137\n" \
"v_mul_f32_e32 v138, v54, v138\n" \
"v_mul_f32_e32 v139, v54, v139\n" \
"v_mul_f32_e32 v140, v54, v140\n" \
"v_mul_f32_e32 v141, v54, v141\n" \
"v_mul_f32_e32 v142, v54, v142\n" \
"v_mul_f32_e32 v143, v54, v143\n" \
"v_add_f32_e32 v55, v55, v23\n" \
"v_cvt_pk_bf16_f32 v68, v136, v137\n" \
"v_cvt_pk_bf16_f32 v69, v138, v139\n" \
"v_cvt_pk_bf16_f32 v70, v140, v141\n" \
"v_cvt_pk_bf16_f32 v71, v142, v143\n" \
"v_add_f32_e32 v55, v55, v43\n" \
"v_mul_f32_e32 v144, v54, v144\n" \
"v_mfma_f32_16x16x32_bf16 a[208:211], a[216:219], v[68:71], a[208:211]\n" \
"v_mul_f32_e32 v145, v54, v145\n" \
"v_mul_f32_e32 v146, v54, v146\n" \
"v_mul_f32_e32 v147, v54, v147\n" \
"v_mul_f32_e32 v148, v54, v148\n" \
"buffer_load_dwordx4 v0, s[80:83], s92 offen offset:0 lds\n" \
"v_mul_f32_e32 v149, v54, v149\n" \
"v_mul_f32_e32 v150, v54, v150\n" \
"v_mul_f32_e32 v151, v54, v151\n" \
"v_max_f32_e32 v62, v62, v55\n" \
"v_cvt_pk_bf16_f32 v72, v144, v145\n" \
"v_cvt_pk_bf16_f32 v73, v146, v147\n" \
"v_cvt_pk_bf16_f32 v74, v148, v149\n" \
"v_cvt_pk_bf16_f32 v75, v150, v151\n" \
"v_rcp_f32_e32 v55, v55\n" \
"v_mfma_f32_16x16x32_bf16 v[184:187], a[56:59], v[120:123], v[184:187]\n" \
"v_mfma_f32_16x16x32_bf16 a[208:211], a[216:219], v[72:75], a[208:211]\n" \
"v_mul_f32_e32 v152, v54, v152\n" \
"v_mul_f32_e32 v153, v54, v153\n" \
"v_mul_f32_e32 v154, v54, v154\n" \
"v_mul_f32_e32 v155, v54, v155\n" \
"v_cvt_pk_bf16_f32 v76, v152, v153\n" \
"buffer_load_dwordx4 v1, s[80:83], s92 offen offset:1024 lds\n" \
"v_cvt_pk_bf16_f32 v77, v154, v155\n" \
"v_mul_f32_e32 v55, v55, v59\n" \
"v_mul_f32_e32 v160, v55, v160\n" \
"v_mul_f32_e32 v161, v55, v161\n" \
"v_mul_f32_e32 v162, v55, v162\n" \
"v_mul_f32_e32 v163, v55, v163\n" \
"v_mul_f32_e32 v164, v55, v164\n" \
"v_mul_f32_e32 v165, v55, v165\n" \
"v_mul_f32_e32 v166, v55, v166\n" \
"v_mul_f32_e32 v167, v55, v167\n" \
"v_cvt_pk_bf16_f32 v80, v160, v161\n" \
"v_cvt_pk_bf16_f32 v81, v162, v163\n" \
"v_cvt_pk_bf16_f32 v82, v164, v165\n" \
"v_cvt_pk_bf16_f32 v83, v166, v167\n" \
"v_mfma_f32_16x16x32_bf16 v[156:159], a[60:63], v[116:119], v[156:159]\n" \
"v_mul_f32_e32 v168, v55, v168\n" \
"v_mul_f32_e32 v169, v55, v169\n" \
"v_mul_f32_e32 v170, v55, v170\n" \
"v_mul_f32_e32 v171, v55, v171\n" \
"v_mul_f32_e32 v172, v55, v172\n" \
"buffer_load_dwordx4 v2, s[80:83], s92 offen offset:2048 lds\n" \
"v_mul_f32_e32 v173, v55, v173\n" \
"v_mul_f32_e32 v174, v55, v174\n" \
"v_mul_f32_e32 v156, v54, v156\n" \
"v_mul_f32_e32 v157, v54, v157\n" \
"v_mul_f32_e32 v158, v54, v158\n" \
"v_mul_f32_e32 v159, v54, v159\n" \
"v_cvt_pk_bf16_f32 v78, v156, v157\n" \
"v_cvt_pk_bf16_f32 v79, v158, v159\n" \
"v_mul_f32_e32 v175, v55, v175\n" \
"v_cvt_pk_bf16_f32 v84, v168, v169\n" \
"v_mfma_f32_16x16x32_bf16 a[208:211], a[216:219], v[76:79], a[208:211]\n" \
"v_mfma_f32_16x16x32_bf16 a[208:211], a[216:219], v[80:83], a[208:211]\n" \
"v_cvt_pk_bf16_f32 v85, v170, v171\n" \
"v_cvt_pk_bf16_f32 v86, v172, v173\n" \
"v_cvt_pk_bf16_f32 v87, v174, v175\n" \
"v_mul_f32_e32 v176, v55, v176\n" \
"v_mul_f32_e32 v177, v55, v177\n" \
"buffer_load_dwordx4 v3, s[80:83], s92 offen offset:3072 lds\n" \
"s_add_u32 m0, s93, 0x1000\n" \
"v_mfma_f32_16x16x32_bf16 a[208:211], a[216:219], v[84:87], a[208:211]\n" \
"v_mul_f32_e32 v178, v55, v178\n" \
"v_mul_f32_e32 v179, v55, v179\n" \
"v_mul_f32_e32 v180, v55, v180\n" \
"v_mul_f32_e32 v181, v55, v181\n" \
"v_mul_f32_e32 v182, v55, v182\n" \
"v_mul_f32_e32 v183, v55, v183\n" \
"v_cvt_pk_bf16_f32 v96, v176, v177\n" \
"v_cvt_pk_bf16_f32 v97, v178, v179\n" \
"v_cvt_pk_bf16_f32 v98, v180, v181\n" \
"v_cvt_pk_bf16_f32 v99, v182, v183\n" \
"v_mfma_f32_16x16x32_bf16 v[188:191], a[60:63], v[120:123], v[188:191]\n" \
"v_mul_f32_e32 v184, v55, v184\n" \
"v_mfma_f32_16x16x32_bf16 a[208:211], a[216:219], v[96:99], a[208:211]\n" \
"v_mul_f32_e32 v185, v55, v185\n" \
"v_mul_f32_e32 v186, v55, v186\n" \
"buffer_load_dwordx4 v4, s[80:83], s92 offen offset:0 lds\n" \
"v_mul_f32_e32 v187, v55, v187\n" \
"v_cvt_pk_bf16_f32 v100, v184, v185\n" \
"v_cvt_pk_bf16_f32 v101, v186, v187\n" \
"v_mul_f32_e32 v188, v55, v188\n" \
"v_mul_f32_e32 v189, v55, v189\n" \
"v_mul_f32_e32 v190, v55, v190\n" \
"v_mul_f32_e32 v191, v55, v191\n" \
"v_cvt_pk_bf16_f32 v102, v188, v189\n" \
"v_cvt_pk_bf16_f32 v103, v190, v191\n" \
"v_cmp_lt_f32_e64 s[72:73], s21, v62\n" \
"buffer_load_dwordx4 v5, s[80:83], s92 offen offset:1024 lds\n" \
"v_mfma_f32_16x16x32_bf16 a[208:211], a[216:219], v[100:103], a[208:211]\n" \
"buffer_load_dwordx4 v6, s[80:83], s92 offen offset:2048 lds\n" \
"buffer_load_dwordx4 v7, s[80:83], s92 offen offset:3072 lds\n" \
"s_add_u32 m0, s93, 0x2000\n" \
"s_nop 0\n" \
"buffer_load_dwordx4 v8, s[80:83], s92 offen offset:0 lds\n" \
"s_nop 2\n" \
"v_accvgpr_read_b32 v63, a208\n" \
"v_cmp_u_f32_e32 vcc, v63, v63\n" \
"s_nop 0\n" \
"s_or_b64 vcc, s[72:73], vcc\n" \
"s_cbranch_scc0 L_out_%=\n" \
"s_branch L_end_%=\n" \
".fill (L_ab + 24576 - .) / 4, 4, 0xBF800000\n" \
".fill (L_ab + 24576 - .) / 4, 4, 0xBF800000\n" \
"L_ap6:\n" \
"s_setpc_b64 s[88:89]\n" \
"L_gs7:\n" \
"v_mfma_f32_16x16x32_bf16 v[104:107], a[128:131], a[16:19], v[36:39]\n" \
"s_add_u32 s68, s60, 0xfff6e000\n" \
"s_add_u32 s69, s60, 0xfffb7000\n" \
"s_add_u32 s70, s60, 0x0\n" \
"s_add_u32 s71, s60, 0x49000\n" \
"v_mfma_f32_16x16x32_bf16 v[104:107], a[132:135], a[20:23], v[104:107]\n" \
"s_xor_b32 m0, s65, 0x12000\n" \
"ds_read_b128 a[160:163], v49 offset:0\n" \
"v_mfma_f32_16x16x32_bf16 v[104:107], a[136:139], a[24:27], v[104:107]\n" \
"buffer_load_dwordx4 v50, s[8:11], s64 offen offset:0 sc1 lds\n" \
"v_mfma_f32_16x16x32_bf16 v[104:107], a[140:143], a[28:31], v[104:107]\n" \
"ds_read_b128 a[164:167], v49 offset:1024\n" \
"ds_read_b128 a[168:171], v49 offset:2048\n" \
"v_mfma_f32_16x16x32_bf16 v[108:111], a[156:159], a[28:31], v[36:39]\n" \
"ds_read_b128 a[172:175], v49 offset:3072\n" \
"ds_read_b128 a[176:179], v49 offset:4096\n" \
"v_mfma_f32_16x16x32_bf16 v[108:111], a[152:155], a[24:27], v[108:111]\n" \
"buffer_load_dwordx4 v50, s[8:11], s64 offen offset:1024 sc1 lds\n" \
"v_mfma_f32_16x16x32_bf16 v[108:111], a[148:151], a[20:23], v[108:111]\n" \
"s_mov_b32 s65, s13\n" \
"v_add_u32_e32 v63, s15, v20\n" \
"v_mfma_f32_16x16x32_bf16 v[108:111], a[144:147], a[16:19], v[108:111]\n" \
"s_add_u32 s67, s14, 40960\n" \
"v_add_u32_e32 v50, 0x6000, v63\n" \
"v_mfma_f32_16x16x32_bf16 v[116:119], a[144:147], a[32:35], v[40:43]\n" \
"s_xor_b32 m0, s65, 0x8000\n" \
"ds_read_b128 a[180:183], v49 offset:5120\n" \
"v_mfma_f32_16x16x32_bf16 v[116:119], a[148:151], a[36:39], v[116:119]\n" \
"ds_read_b128 a[184:187], v49 offset:6144\n" \
"v_and_b32_e32 v68, v12, v68\n" \
"v_mfma_f32_16x16x32_bf16 v[116:119], a[152:155], a[40:43], v[116:119]\n" \
"v_and_b32_e32 v69, v13, v69\n" \
"v_and_b32_e32 v70, v14, v70\n" \
"v_mfma_f32_16x16x32_bf16 v[116:119], a[156:159], a[44:47], v[116:119]\n" \
"buffer_load_dwordx4 v50, s[8:11], s67 offen offset:0 sc1 lds\n" \
"v_mfma_f32_16x16x32_bf16 v[112:115], a[140:143], a[44:47], v[40:43]\n" \
"v_and_b32_e32 v71, v15, v71\n" \
"ds_read_b128 a[188:191], v49 offset:7168\n" \
"v_mfma_f32_16x16x32_bf16 v[112:115], a[136:139], a[40:43], v[112:115]\n" \
"v_exp_f32_e32 v86, v86\n" \
"v_mfma_f32_16x16x32_bf16 v[112:115], a[132:135], a[36:39], v[112:115]\n" \
"v_exp_f32_e32 v87, v87\n" \
"v_mfma_f32_16x16x32_bf16 v[112:115], a[128:131], a[32:35], v[112:115]\n" \
"v_exp_f32_e32 v84, v84\n" \
"v_mfma_f32_16x16x32_bf16 v[120:123], a[128:131], a[48:51], v[44:47]\n" \
"buffer_load_dwordx4 v50, s[8:11], s67 offen offset:1024 sc1 lds\n" \
"v_mfma_f32_16x16x32_bf16 v[120:123], a[132:135], a[52:55], v[120:123]\n" \
"v_exp_f32_e32 v85, v85\n" \
"v_mfma_f32_16x16x32_bf16 v[120:123], a[136:139], a[56:59], v[120:123]\n" \
"v_cvt_pk_bf16_f32 v87, v86, v87\n" \
"v_cvt_pk_bf16_f32 v86, v84, v85\n" \
"v_mfma_f32_16x16x32_bf16 v[120:123], a[140:143], a[60:63], v[120:123]\n" \
"v_exp_f32_e32 v82, v82\n" \
"v_mfma_f32_16x16x32_bf16 v[124:127], a[156:159], a[60:63], v[44:47]\n" \
"v_exp_f32_e32 v83, v83\n" \
"v_mfma_f32_16x16x32_bf16 v[124:127], a[152:155], a[56:59], v[124:127]\n" \
"v_exp_f32_e32 v80, v80\n" \
"v_mfma_f32_16x16x32_bf16 v[124:127], a[148:151], a[52:55], v[124:127]\n" \
"buffer_load_dwordx4 v50, s[8:11], s67 offen offset:2048 sc1 lds\n" \
"v_mfma_f32_16x16x32_bf16 v[124:127], a[144:147], a[48:51], v[124:127]\n" \
"v_cvt_pk_bf16_f32 v85, v82, v83\n" \
"s_waitcnt lgkmcnt(1)\n" \
"v_mfma_f32_16x16x32_bf16 v[128:131], a[160:163], v[68:71], v[128:131]\n" \
"v_exp_f32_e32 v81, v81\n" \
"v_mfma_f32_16x16x32_bf16 v[160:163], a[160:163], v[72:75], v[160:163]\n" \
"v_cvt_pk_bf16_f32 v84, v80, v81\n" \
"s_mov_b32 s64, s14\n" \
"v_mfma_f32_16x16x32_bf16 v[132:135], a[164:167], v[68:71], v[132:135]\n" \
"v_exp_f32_e32 v88, v88\n" \
"v_mfma_f32_16x16x32_bf16 v[164:167], a[164:167], v[72:75], v[164:167]\n" \
"v_exp_f32_e32 v89, v89\n" \
"v_mfma_f32_16x16x32_bf16 v[136:139], a[168:171], v[68:71], v[136:139]\n" \
"buffer_load_dwordx4 v50, s[8:11], s67 offen offset:3072 sc1 lds\n" \
"v_mfma_f32_16x16x32_bf16 v[168:171], a[168:171], v[72:75], v[168:171]\n" \
"v_exp_f32_e32 v90, v90\n" \
"v_mfma_f32_16x16x32_bf16 v[140:143], a[172:175], v[68:71], v[140:143]\n" \
"v_exp_f32_e32 v91, v91\n" \
"v_mfma_f32_16x16x32_bf16 v[172:175], a[172:175], v[72:75], v[172:175]\n" \
"v_cvt_pk_bf16_f32 v88, v88, v89\n" \
"v_cvt_pk_bf16_f32 v89, v90, v91\n" \
"v_mfma_f32_16x16x32_bf16 v[144:147], a[176:179], v[68:71], v[144:147]\n" \
"v_exp_f32_e32 v92, v92\n" \
"v_mfma_f32_16x16x32_bf16 v[176:179], a[176:179], v[72:75], v[176:179]\n" \
"v_exp_f32_e32 v93, v93\n" \
"v_mfma_f32_16x16x32_bf16 v[148:151], a[180:183], v[68:71], v[148:151]\n" \
"v_exp_f32_e32 v94, v94\n" \
"v_mfma_f32_16x16x32_bf16 v[180:183], a[180:183], v[72:75], v[180:183]\n" \
"v_exp_f32_e32 v95, v95\n" \
"v_mfma_f32_16x16x32_bf16 v[152:155], a[184:187], v[68:71], v[152:155]\n" \
"v_cvt_pk_bf16_f32 v90, v92, v93\n" \
"v_cvt_pk_bf16_f32 v91, v94, v95\n" \
"v_mfma_f32_16x16x32_bf16 v[184:187], a[184:187], v[72:75], v[184:187]\n" \
"v_add_u32_e32 v51, 0x12000, v63\n" \
"s_waitcnt lgkmcnt(0)\n" \
"v_mfma_f32_16x16x32_bf16 v[156:159], a[188:191], v[68:71], v[156:159]\n" \
"s_xor_b32 m0, s65, 0xa000\n" \
"ds_read_b32 v58, v18 offset:512\n" \
"v_mfma_f32_16x16x32_bf16 v[188:191], a[188:191], v[72:75], v[188:191]\n" \
"buffer_load_dwordx4 v51, s[8:11], s64 offen offset:0 sc1 lds\n" \
"v_smfmac_f32_16x16x64_bf16 a[192:195], a[200:203], v[68:75], v21\n" \
"ds_read_b32 v59, v18 offset:640\n" \
"ds_read_b32 v60, v18 offset:768\n" \
"v_mfma_f32_16x16x32_bf16 v[220:223], a[188:191], v[84:87], v[220:223]\n" \
"buffer_load_dwordx4 v51, s[8:11], s64 offen offset:1024 sc1 lds\n" \
"v_mfma_f32_16x16x32_bf16 v[252:255], a[188:191], v[88:91], v[252:255]\n" \
"ds_read_b32 v61, v18 offset:896\n" \
"ds_read_b128 a[128:131], v48 offset:0\n" \
"v_mfma_f32_16x16x32_bf16 v[216:219], a[184:187], v[84:87], v[216:219]\n" \
"ds_read_b128 a[144:147], v48 offset:4096\n" \
"ds_read_b128 a[132:135], v48 offset:1024\n" \
"v_mfma_f32_16x16x32_bf16 v[248:251], a[184:187], v[88:91], v[248:251]\n" \
"ds_read_b128 a[148:151], v48 offset:5120\n" \
"ds_read_b128 a[136:139], v48 offset:2048\n" \
"v_mfma_f32_16x16x32_bf16 v[212:215], a[180:183], v[84:87], v[212:215]\n" \
"ds_read_b128 a[152:155], v48 offset:6144\n" \
"ds_read_b128 a[140:143], v48 offset:3072\n" \
"v_mfma_f32_16x16x32_bf16 v[244:247], a[180:183], v[88:91], v[244:247]\n" \
"ds_read_b128 a[156:159], v48 offset:7168\n" \
"v_add_u32_e32 v52, 0x14000, v63\n" \
"v_mfma_f32_16x16x32_bf16 v[208:211], a[176:179], v[84:87], v[208:211]\n" \
"buffer_load_dwordx4 v51, s[8:11], s64 offen offset:2048 sc1 lds\n" \
"v_mfma_f32_16x16x32_bf16 v[240:243], a[176:179], v[88:91], v[240:243]\n" \
"v_exp_f32_e32 v104, v104\n" \
"v_mfma_f32_16x16x32_bf16 v[204:207], a[172:175], v[84:87], v[204:207]\n" \
"v_exp_f32_e32 v105, v105\n" \
"v_mfma_f32_16x16x32_bf16 v[236:239], a[172:175], v[88:91], v[236:239]\n" \
"buffer_load_dwordx4 v51, s[8:11], s64 offen offset:3072 sc1 lds\n" \
"v_mfma_f32_16x16x32_bf16 v[200:203], a[168:171], v[84:87], v[200:203]\n" \
"v_exp_f32_e32 v106, v106\n" \
"v_mfma_f32_16x16x32_bf16 v[232:235], a[168:171], v[88:91], v[232:235]\n" \
"v_exp_f32_e32 v107, v107\n" \
"v_mfma_f32_16x16x32_bf16 v[196:199], a[164:167], v[84:87], v[196:199]\n" \
"v_cvt_pk_bf16_f32 v104, v104, v105\n" \
"v_cvt_pk_bf16_f32 v105, v106, v107\n" \
"v_mfma_f32_16x16x32_bf16 v[228:231], a[164:167], v[88:91], v[228:231]\n" \
"v_exp_f32_e32 v108, v108\n" \
"v_mfma_f32_16x16x32_bf16 v[192:195], a[160:163], v[84:87], v[192:195]\n" \
"v_exp_f32_e32 v109, v109\n" \
"v_mfma_f32_16x16x32_bf16 v[224:227], a[160:163], v[88:91], v[224:227]\n" \
"v_exp_f32_e32 v110, v110\n" \
"v_smfmac_f32_16x16x64_bf16 a[196:199], a[200:203], v[84:91], v21\n" \
"v_exp_f32_e32 v111, v111\n" \
"v_cvt_pk_bf16_f32 v106, v108, v109\n" \
"v_cvt_pk_bf16_f32 v107, v110, v111\n" \
"v_and_b32_e32 v104, v12, v104\n" \
"v_and_b32_e32 v105, v13, v105\n" \
"v_and_b32_e32 v106, v14, v106\n" \
"v_and_b32_e32 v107, v15, v107\n" \
"v_accvgpr_read_b32 v54, a192\n" \
"v_accvgpr_read_b32 v22, a193\n" \
"v_readlane_b32 s93, v11, 19\n" \
"v_readlane_b32 s80, v11, 11\n" \
"v_readlane_b32 s81, v11, 12\n" \
"s_mov_b32 s82, 0x47fca0\n" \
"s_mov_b32 s83, 0x20000\n" \
"v_readlane_b32 s92, v11, 26\n" \
"v_exp_f32_e32 v118, v118\n" \
"v_exp_f32_e32 v119, v119\n" \
"v_exp_f32_e32 v116, v116\n" \
"v_exp_f32_e32 v117, v117\n" \
"v_cvt_pk_bf16_f32 v119, v118, v119\n" \
"v_exp_f32_e32 v114, v114\n" \
"v_exp_f32_e32 v115, v115\n" \
"v_exp_f32_e32 v112, v112\n" \
"v_cvt_pk_bf16_f32 v118, v116, v117\n" \
"s_waitcnt vmcnt(10) lgkmcnt(0)\n" \
"s_barrier\n" \
"v_mfma_f32_16x16x32_bf16 v[80:83], a[128:131], a[32:35], v[40:43]\n" \
"ds_read_b128 a[160:163], v49 offset:8192\n" \
"ds_read_b128 a[164:167], v49 offset:9216\n" \
"v_mfma_f32_16x16x32_bf16 v[80:83], a[132:135], a[36:39], v[80:83]\n" \
"ds_read_b128 a[168:171], v49 offset:10240\n" \
"s_xor_b32 m0, s65, 0x18000\n" \
"v_mfma_f32_16x16x32_bf16 v[80:83], a[136:139], a[40:43], v[80:83]\n" \
"buffer_load_dwordx4 v52, s[8:11], s64 offen offset:0 sc1 lds\n" \
"v_mfma_f32_16x16x32_bf16 v[80:83], a[140:143], a[44:47], v[80:83]\n" \
"ds_read_b128 a[172:175], v49 offset:11264\n" \
"ds_read_b128 a[176:179], v49 offset:12288\n" \
"v_mfma_f32_16x16x32_bf16 v[84:87], a[156:159], a[44:47], v[40:43]\n" \
"ds_read_b128 a[180:183], v49 offset:13312\n" \
"ds_read_b128 a[184:187], v49 offset:14336\n" \
"v_mfma_f32_16x16x32_bf16 v[84:87], a[152:155], a[40:43], v[84:87]\n" \
"buffer_load_dwordx4 v52, s[8:11], s64 offen offset:1024 sc1 lds\n" \
"v_mfma_f32_16x16x32_bf16 v[84:87], a[148:151], a[36:39], v[84:87]\n" \
"ds_read_b128 a[188:191], v49 offset:15360\n" \
"v_cvt_pk_bf16_f32 v117, v114, v115\n" \
"v_mfma_f32_16x16x32_bf16 v[84:87], a[144:147], a[32:35], v[84:87]\n" \
"buffer_load_dwordx4 v52, s[8:11], s64 offen offset:2048 sc1 lds\n" \
"v_mfma_f32_16x16x32_bf16 v[92:95], a[144:147], a[48:51], v[44:47]\n" \
"v_exp_f32_e32 v113, v113\n" \
"v_mfma_f32_16x16x32_bf16 v[92:95], a[148:151], a[52:55], v[92:95]\n" \
"buffer_load_dwordx4 v52, s[8:11], s64 offen offset:3072 sc1 lds\n" \
"v_mfma_f32_16x16x32_bf16 v[92:95], a[152:155], a[56:59], v[92:95]\n" \
"s_add_u32 m0, s93, 0x0\n" \
"v_cvt_pk_bf16_f32 v116, v112, v113\n" \
"v_mfma_f32_16x16x32_bf16 v[92:95], a[156:159], a[60:63], v[92:95]\n" \
"v_exp_f32_e32 v120, v120\n" \
"v_mfma_f32_16x16x32_bf16 v[88:91], a[140:143], a[60:63], v[44:47]\n" \
"buffer_load_dwordx4 v0, s[80:83], s92 offen offset:0 lds\n" \
"v_mfma_f32_16x16x32_bf16 v[88:91], a[136:139], a[56:59], v[88:91]\n" \
"v_exp_f32_e32 v121, v121\n" \
"v_mfma_f32_16x16x32_bf16 v[88:91], a[132:135], a[52:55], v[88:91]\n" \
"v_exp_f32_e32 v122, v122\n" \
"v_mfma_f32_16x16x32_bf16 v[88:91], a[128:131], a[48:51], v[88:91]\n" \
"buffer_load_dwordx4 v1, s[80:83], s92 offen offset:1024 lds\n" \
"v_mfma_f32_16x16x32_bf16 a[192:195], a[212:215], v[104:107], a[192:195]\n" \
"v_cvt_pk_bf16_f32 v120, v120, v121\n" \
"s_waitcnt lgkmcnt(5)\n" \
"v_mfma_f32_16x16x32_bf16 v[160:163], a[160:163], v[104:107], v[160:163]\n" \
"v_exp_f32_e32 v123, v123\n" \
"v_mfma_f32_16x16x32_bf16 v[164:167], a[164:167], v[104:107], v[164:167]\n" \
"buffer_load_dwordx4 v2, s[80:83], s92 offen offset:2048 lds\n" \
"v_mfma_f32_16x16x32_bf16 v[168:171], a[168:171], v[104:107], v[168:171]\n" \
"v_cvt_pk_bf16_f32 v121, v122, v123\n" \
"s_waitcnt lgkmcnt(1)\n" \
"v_mfma_f32_16x16x32_bf16 v[172:175], a[172:175], v[104:107], v[172:175]\n" \
"v_exp_f32_e32 v124, v124\n" \
"v_mfma_f32_16x16x32_bf16 v[176:179], a[176:179], v[104:107], v[176:179]\n" \
"v_exp_f32_e32 v125, v125\n" \
"v_mfma_f32_16x16x32_bf16 v[180:183], a[180:183], v[104:107], v[180:183]\n" \
"v_exp_f32_e32 v126, v126\n" \
"v_mfma_f32_16x16x32_bf16 v[184:187], a[184:187], v[104:107], v[184:187]\n" \
"v_cvt_pk_bf16_f32 v122, v124, v125\n" \
"s_waitcnt lgkmcnt(0)\n" \
"v_mfma_f32_16x16x32_bf16 v[188:191], a[188:191], v[104:107], v[188:191]\n" \
"v_exp_f32_e32 v127, v127\n" \
"v_mfma_f32_16x16x32_bf16 v[220:223], a[188:191], v[116:119], v[220:223]\n" \
"v_cvt_pk_bf16_f32 v123, v126, v127\n" \
"buffer_load_dwordx4 v3, s[80:83], s92 offen offset:3072 lds\n" \
"s_add_u32 m0, s93, 0x1000\n" \
"v_mfma_f32_16x16x32_bf16 v[252:255], a[188:191], v[120:123], v[252:255]\n" \
"ds_read_b128 a[128:131], v48 offset:8192\n" \
"ds_read_b128 a[144:147], v48 offset:12288\n" \
"v_mfma_f32_16x16x32_bf16 v[216:219], a[184:187], v[116:119], v[216:219]\n" \
"buffer_load_dwordx4 v4, s[80:83], s92 offen offset:0 lds\n" \
"v_mfma_f32_16x16x32_bf16 v[248:251], a[184:187], v[120:123], v[248:251]\n" \
"ds_read_b128 a[132:135], v48 offset:9216\n" \
"ds_read_b128 a[148:151], v48 offset:13312\n" \
"v_mfma_f32_16x16x32_bf16 v[212:215], a[180:183], v[116:119], v[212:215]\n" \
"buffer_load_dwordx4 v5, s[80:83], s92 offen offset:1024 lds\n" \
"v_mfma_f32_16x16x32_bf16 v[244:247], a[180:183], v[120:123], v[244:247]\n" \
"ds_read_b128 a[136:139], v48 offset:10240\n" \
"ds_read_b128 a[152:155], v48 offset:14336\n" \
"v_mfma_f32_16x16x32_bf16 v[208:211], a[176:179], v[116:119], v[208:211]\n" \
"buffer_load_dwordx4 v6, s[80:83], s92 offen offset:2048 lds\n" \
"v_mfma_f32_16x16x32_bf16 v[240:243], a[176:179], v[120:123], v[240:243]\n" \
"ds_read_b128 a[140:143], v48 offset:11264\n" \
"ds_read_b128 a[156:159], v48 offset:15360\n" \
"v_mfma_f32_16x16x32_bf16 v[204:207], a[172:175], v[116:119], v[204:207]\n" \
"buffer_load_dwordx4 v7, s[80:83], s92 offen offset:3072 lds\n" \
"v_mfma_f32_16x16x32_bf16 v[236:239], a[172:175], v[120:123], v[236:239]\n" \
"v_readlane_b32 s96, v11, 1\n" \
"v_lshrrev_b32_e32 v16, 1, v20\n" \
"v_mfma_f32_16x16x32_bf16 v[200:203], a[168:171], v[116:119], v[200:203]\n" \
"v_add_f32_e32 v54, v54, v22\n" \
"v_max_f32_e32 v62, v62, v54\n" \
"v_mfma_f32_16x16x32_bf16 v[232:235], a[168:171], v[120:123], v[232:235]\n" \
"v_rcp_f32_e32 v54, v54\n" \
"v_mfma_f32_16x16x32_bf16 v[196:199], a[164:167], v[116:119], v[196:199]\n" \
"v_mul_f32_e32 v54, v54, v58\n" \
"v_mul_f32_e32 v128, v54, v128\n" \
"v_mfma_f32_16x16x32_bf16 v[228:231], a[164:167], v[120:123], v[228:231]\n" \
"v_mul_f32_e32 v129, v54, v129\n" \
"v_mul_f32_e32 v136, v54, v136\n" \
"v_mfma_f32_16x16x32_bf16 v[192:195], a[160:163], v[116:119], v[192:195]\n" \
"v_mul_f32_e32 v137, v54, v137\n" \
"v_mul_f32_e32 v144, v54, v144\n" \
"v_mfma_f32_16x16x32_bf16 v[224:227], a[160:163], v[120:123], v[224:227]\n" \
"v_mul_f32_e32 v145, v54, v145\n" \
"v_mul_f32_e32 v152, v54, v152\n" \
"v_smfmac_f32_16x16x64_bf16 a[196:199], a[200:203], v[116:123], v21\n" \
"v_mul_f32_e32 v153, v54, v153\n" \
"v_cvt_pk_bf16_f32 v128, v128, v129\n" \
"v_mul_f32_e32 v130, v54, v130\n" \
"v_mul_f32_e32 v131, v54, v131\n" \
"v_cvt_pk_bf16_f32 v136, v136, v137\n" \
"v_mul_f32_e32 v138, v54, v138\n" \
"v_mul_f32_e32 v139, v54, v139\n" \
"v_cvt_pk_bf16_f32 v144, v144, v145\n" \
"v_mul_f32_e32 v146, v54, v146\n" \
"v_mul_f32_e32 v147, v54, v147\n" \
"v_cvt_pk_bf16_f32 v152, v152, v153\n" \
"v_mul_f32_e32 v154, v54, v154\n" \
"v_mul_f32_e32 v155, v54, v155\n" \
"v_cvt_pk_bf16_f32 v129, v130, v131\n" \
"v_mul_f32_e32 v132, v54, v132\n" \
"v_mul_f32_e32 v133, v54, v133\n" \
"v_mul_f32_e32 v134, v54, v134\n" \
"v_mul_f32_e32 v135, v54, v135\n" \
"v_cvt_pk_bf16_f32 v137, v138, v139\n" \
"v_mul_f32_e32 v140, v54, v140\n" \
"v_mul_f32_e32 v141, v54, v141\n" \
"v_mul_f32_e32 v142, v54, v142\n" \
"v_mul_f32_e32 v143, v54, v143\n" \
"v_cvt_pk_bf16_f32 v145, v146, v147\n" \
"v_mul_f32_e32 v148, v54, v148\n" \
"v_mul_f32_e32 v149, v54, v149\n" \
"v_mul_f32_e32 v150, v54, v150\n" \
"v_mul_f32_e32 v151, v54, v151\n" \
"v_cvt_pk_bf16_f32 v153, v154, v155\n" \
"v_mul_f32_e32 v156, v54, v156\n" \
"v_mul_f32_e32 v157, v54, v157\n" \
"v_mul_f32_e32 v158, v54, v158\n" \
"v_mul_f32_e32 v159, v54, v159\n" \
"v_cvt_pk_bf16_f32 v130, v132, v133\n" \
"v_cvt_pk_bf16_f32 v131, v134, v135\n" \
"buffer_store_dwordx4 v[128:131], v20, s[56:59], s68 offen offset:0 sc1\n" \
"v_cvt_pk_bf16_f32 v138, v140, v141\n" \
"v_cvt_pk_bf16_f32 v139, v142, v143\n" \
"v_cvt_pk_bf16_f32 v146, v148, v149\n" \
"v_cvt_pk_bf16_f32 v147, v150, v151\n" \
"v_cvt_pk_bf16_f32 v154, v156, v157\n" \
"v_cvt_pk_bf16_f32 v155, v158, v159\n" \
"buffer_store_dwordx4 v[136:139], v20, s[56:59], s68 offen offset:1024 sc1\n" \
"s_add_u32 m0, s93, 0x2000\n" \
"v_add_u32_e32 v49, 0x10000, v48\n" \
"s_xor_b32 s65, s65, 0x8000\n" \
"s_add_u32 s64, s64, 0x10000\n" \
"v_exp_f32_e32 v86, v86\n" \
"buffer_store_dwordx4 v[144:147], v20, s[56:59], s68 offen offset:2048 sc1\n" \
"v_exp_f32_e32 v87, v87\n" \
"v_exp_f32_e32 v84, v84\n" \
"v_exp_f32_e32 v85, v85\n" \
"buffer_store_dwordx4 v[152:155], v20, s[56:59], s68 offen offset:3072 sc1\n" \
"v_exp_f32_e32 v88, v88\n" \
"v_exp_f32_e32 v89, v89\n" \
"v_cvt_pk_bf16_f32 v87, v86, v87\n" \
"v_exp_f32_e32 v82, v82\n" \
"buffer_load_dwordx4 v8, s[80:83], s92 offen offset:0 lds\n" \
"v_exp_f32_e32 v83, v83\n" \
"v_exp_f32_e32 v80, v80\n" \
"v_exp_f32_e32 v81, v81\n" \
"v_exp_f32_e32 v90, v90\n" \
"v_exp_f32_e32 v91, v91\n" \
"v_cvt_pk_bf16_f32 v86, v84, v85\n" \
"v_cvt_pk_bf16_f32 v88, v88, v89\n" \
"v_exp_f32_e32 v92, v92\n" \
"v_exp_f32_e32 v93, v93\n" \
"v_exp_f32_e32 v94, v94\n" \
"v_exp_f32_e32 v95, v95\n" \
"v_cvt_pk_bf16_f32 v85, v82, v83\n" \
"v_cvt_pk_bf16_f32 v84, v80, v81\n" \
"v_cvt_pk_bf16_f32 v89, v90, v91\n" \
"v_and_b32_e32 v84, v12, v84\n" \
"v_and_b32_e32 v85, v13, v85\n" \
"v_and_b32_e32 v86, v14, v86\n" \
"s_waitcnt vmcnt(17) lgkmcnt(0)\n" \
"s_barrier\n" \
"v_mfma_f32_16x16x32_bf16 v[120:123], a[128:131], a[48:51], v[44:47]\n" \
"buffer_load_dwordx2 v[26:27], v16, s[56:59], s96 offen sc1\n" \
"v_mfma_f32_16x16x32_bf16 v[120:123], a[132:135], a[52:55], v[120:123]\n" \
"ds_read_b128 a[188:191], v49 offset:7168\n" \
"ds_read_b128 a[184:187], v49 offset:6144\n" \
"v_mfma_f32_16x16x32_bf16 v[120:123], a[136:139], a[56:59], v[120:123]\n" \
"s_xor_b32 m0, s65, 0x12000\n" \
"ds_read_b128 a[180:183], v49 offset:5120\n" \
"v_mfma_f32_16x16x32_bf16 v[120:123], a[140:143], a[60:63], v[120:123]\n" \
"buffer_load_dwordx4 v50, s[8:11], s64 offen offset:0 sc1 lds\n" \
"v_mfma_f32_16x16x32_bf16 v[124:127], a[156:159], a[60:63], v[44:47]\n" \
"ds_read_b128 a[176:179], v49 offset:4096\n" \
"ds_read_b128 a[172:175], v49 offset:3072\n" \
"v_mfma_f32_16x16x32_bf16 v[124:127], a[152:155], a[56:59], v[124:127]\n" \
"buffer_load_dwordx4 v50, s[8:11], s64 offen offset:1024 sc1 lds\n" \
"v_mfma_f32_16x16x32_bf16 v[124:127], a[148:151], a[52:55], v[124:127]\n" \
"ds_read_b128 a[168:171], v49 offset:2048\n" \
"v_and_b32_e32 v87, v15, v87\n" \
"v_mfma_f32_16x16x32_bf16 v[124:127], a[144:147], a[48:51], v[124:127]\n" \
"v_cvt_pk_bf16_f32 v90, v92, v93\n" \
"v_cvt_pk_bf16_f32 v91, v94, v95\n" \
"v_mfma_f32_16x16x32_bf16 v[32:35], a[220:223], a[64:67], 0\n" \
"buffer_load_dwordx4 v50, s[8:11], s64 offen offset:2048 sc1 lds\n" \
"v_mfma_f32_16x16x32_bf16 v[36:39], a[220:223], a[80:83], 0\n" \
"ds_read_b128 a[164:167], v49 offset:1024\n" \
"ds_read_b128 a[160:163], v49 offset:0\n" \
"v_mfma_f32_16x16x32_bf16 v[32:35], a[224:227], a[68:71], v[32:35]\n" \
"ds_read_b128 a[0:3], v49 offset:8192\n" \
"ds_read_b128 a[4:7], v49 offset:9216\n" \
"v_mfma_f32_16x16x32_bf16 v[36:39], a[224:227], a[84:87], v[36:39]\n" \
"ds_read_b128 a[8:11], v49 offset:10240\n" \
"ds_read_b128 a[12:15], v49 offset:11264\n" \
"v_mfma_f32_16x16x32_bf16 v[32:35], a[228:231], a[72:75], v[32:35]\n" \
"buffer_load_dwordx4 v50, s[8:11], s64 offen offset:3072 sc1 lds\n" \
"v_mfma_f32_16x16x32_bf16 v[36:39], a[228:231], a[88:91], v[36:39]\n" \
"ds_read_b128 a[16:19], v49 offset:12288\n" \
"ds_read_b128 a[20:23], v49 offset:13312\n" \
"v_mfma_f32_16x16x32_bf16 v[32:35], a[232:235], a[76:79], v[32:35]\n" \
"ds_read_b128 a[24:27], v49 offset:14336\n" \
"ds_read_b128 a[28:31], v49 offset:15360\n" \
"v_mfma_f32_16x16x32_bf16 v[36:39], a[232:235], a[92:95], v[36:39]\n" \
"v_exp_f32_e32 v120, v120\n" \
"v_smfmac_f32_16x16x64_bf16 a[196:199], a[200:203], v[84:91], v21\n" \
"s_mov_b32 s63, s62\n" \
"s_waitcnt lgkmcnt(13)\n" \
"v_mfma_f32_16x16x32_bf16 v[220:223], a[188:191], v[84:87], v[220:223]\n" \
"v_exp_f32_e32 v121, v121\n" \
"v_mfma_f32_16x16x32_bf16 v[252:255], a[188:191], v[88:91], v[252:255]\n" \
"v_exp_f32_e32 v122, v122\n" \
"v_mfma_f32_16x16x32_bf16 v[216:219], a[184:187], v[84:87], v[216:219]\n" \
"v_exp_f32_e32 v123, v123\n" \
"v_mfma_f32_16x16x32_bf16 v[248:251], a[184:187], v[88:91], v[248:251]\n" \
"v_cvt_pk_bf16_f32 v120, v120, v121\n" \
"v_cvt_pk_bf16_f32 v121, v122, v123\n" \
"v_mfma_f32_16x16x32_bf16 v[212:215], a[180:183], v[84:87], v[212:215]\n" \
"v_exp_f32_e32 v124, v124\n" \
"v_mfma_f32_16x16x32_bf16 v[244:247], a[180:183], v[88:91], v[244:247]\n" \
"v_and_b32_e32 v120, v12, v120\n" \
"s_waitcnt lgkmcnt(10)\n" \
"v_mfma_f32_16x16x32_bf16 v[208:211], a[176:179], v[84:87], v[208:211]\n" \
"v_exp_f32_e32 v125, v125\n" \
"v_mfma_f32_16x16x32_bf16 v[240:243], a[176:179], v[88:91], v[240:243]\n" \
"v_exp_f32_e32 v126, v126\n" \
"v_mfma_f32_16x16x32_bf16 v[204:207], a[172:175], v[84:87], v[204:207]\n" \
"v_exp_f32_e32 v127, v127\n" \
"v_mfma_f32_16x16x32_bf16 v[236:239], a[172:175], v[88:91], v[236:239]\n" \
"v_cvt_pk_bf16_f32 v122, v124, v125\n" \
"v_cvt_pk_bf16_f32 v123, v126, v127\n" \
"v_mfma_f32_16x16x32_bf16 v[200:203], a[168:171], v[84:87], v[200:203]\n" \
"v_and_b32_e32 v121, v13, v121\n" \
"v_and_b32_e32 v122, v14, v122\n" \
"v_mfma_f32_16x16x32_bf16 v[232:235], a[168:171], v[88:91], v[232:235]\n" \
"v_and_b32_e32 v123, v15, v123\n" \
"s_waitcnt lgkmcnt(0)\n" \
"v_mfma_f32_16x16x32_bf16 v[196:199], a[164:167], v[84:87], v[196:199]\n" \
"v_accvgpr_read_b32 v56, a196\n" \
"v_accvgpr_read_b32 v24, a197\n" \
"v_mfma_f32_16x16x32_bf16 v[228:231], a[164:167], v[88:91], v[228:231]\n" \
"v_readlane_b32 s97, v11, 36\n" \
"v_add_u32_e32 v17, 0x49000, v20\n" \
"v_mfma_f32_16x16x32_bf16 v[192:195], a[160:163], v[84:87], v[192:195]\n" \
"v_readlane_b32 s98, v11, 37\n" \
"v_add_u32_e32 v48, s16, v20\n" \
"v_mfma_f32_16x16x32_bf16 v[224:227], a[160:163], v[88:91], v[224:227]\n" \
"ds_read_b128 a[128:131], v48 offset:0\n" \
"ds_read_b128 a[144:147], v48 offset:4096\n" \
"v_mfma_f32_16x16x32_bf16 a[196:199], a[212:215], v[120:123], a[196:199]\n" \
"ds_read_b128 a[132:135], v48 offset:1024\n" \
"ds_read_b128 a[148:151], v48 offset:5120\n" \
"v_mfma_f32_16x16x32_bf16 v[224:227], a[0:3], v[120:123], v[224:227]\n" \
"ds_read_b128 a[136:139], v48 offset:2048\n" \
"ds_read_b128 a[152:155], v48 offset:6144\n" \
"v_mfma_f32_16x16x32_bf16 v[228:231], a[4:7], v[120:123], v[228:231]\n" \
"ds_read_b128 a[140:143], v48 offset:3072\n" \
"ds_read_b128 a[156:159], v48 offset:7168\n" \
"v_mfma_f32_16x16x32_bf16 v[232:235], a[8:11], v[120:123], v[232:235]\n" \
"v_accvgpr_read_b32 v55, a194\n" \
"v_accvgpr_read_b32 v23, a195\n" \
"v_mfma_f32_16x16x32_bf16 a[192:195], a[216:219], a[216:219], 0\n" \
"v_add_f32_e32 v55, v55, v23\n" \
"v_max_f32_e32 v62, v62, v55\n" \
"v_mfma_f32_16x16x32_bf16 v[236:239], a[12:15], v[120:123], v[236:239]\n" \
"v_rcp_f32_e32 v55, v55\n" \
"v_mfma_f32_16x16x32_bf16 v[240:243], a[16:19], v[120:123], v[240:243]\n" \
"v_mul_f32_e32 v55, v55, v59\n" \
"v_mul_f32_e32 v160, v55, v160\n" \
"v_mfma_f32_16x16x32_bf16 v[244:247], a[20:23], v[120:123], v[244:247]\n" \
"v_mul_f32_e32 v161, v55, v161\n" \
"v_mul_f32_e32 v168, v55, v168\n" \
"v_mfma_f32_16x16x32_bf16 v[248:251], a[24:27], v[120:123], v[248:251]\n" \
"v_mul_f32_e32 v169, v55, v169\n" \
"v_mul_f32_e32 v176, v55, v176\n" \
"v_mfma_f32_16x16x32_bf16 v[252:255], a[28:31], v[120:123], v[252:255]\n" \
"v_mul_f32_e32 v177, v55, v177\n" \
"v_mul_f32_e32 v184, v55, v184\n" \
"v_mul_f32_e32 v185, v55, v185\n" \
"v_cvt_pk_bf16_f32 v160, v160, v161\n" \
"v_mul_f32_e32 v162, v55, v162\n" \
"v_mul_f32_e32 v163, v55, v163\n" \
"v_cvt_pk_bf16_f32 v168, v168, v169\n" \
"v_mul_f32_e32 v170, v55, v170\n" \
"v_mul_f32_e32 v171, v55, v171\n" \
"s_waitcnt vmcnt(4)\n" \
"v_cmp_ne_u64_e32 vcc, s[62:63], v[26:27]\n" \
"v_cvt_pk_bf16_f32 v176, v176, v177\n" \
"v_mul_f32_e32 v178, v55, v178\n" \
"v_mul_f32_e32 v179, v55, v179\n" \
"v_cvt_pk_bf16_f32 v184, v184, v185\n" \
"v_mul_f32_e32 v186, v55, v186\n" \
"s_cbranch_vccnz L_sos_%=\n" \
"L_ros_%=:\n" \
"buffer_load_dwordx4 a[220:223], v20, s[56:59], s97 offen offset:0 sc1\n" \
"v_mul_f32_e32 v187, v55, v187\n" \
"v_cvt_pk_bf16_f32 v161, v162, v163\n" \
"v_mul_f32_e32 v164, v55, v164\n" \
"v_mul_f32_e32 v165, v55, v165\n" \
"v_mul_f32_e32 v166, v55, v166\n" \
"v_mul_f32_e32 v167, v55, v167\n" \
"buffer_load_dwordx4 a[228:231], v17, s[56:59], s97 offen offset:0 sc1\n" \
"v_cvt_pk_bf16_f32 v169, v170, v171\n" \
"v_mul_f32_e32 v172, v55, v172\n" \
"v_mul_f32_e32 v173, v55, v173\n" \
"v_mul_f32_e32 v174, v55, v174\n" \
"v_mul_f32_e32 v175, v55, v175\n" \
"v_cvt_pk_bf16_f32 v177, v178, v179\n" \
"buffer_load_dwordx4 a[236:239], v20, s[56:59], s98 offen offset:0 sc1\n" \
"v_mul_f32_e32 v180, v55, v180\n" \
"v_mul_f32_e32 v181, v55, v181\n" \
"v_mul_f32_e32 v182, v55, v182\n" \
"v_mul_f32_e32 v183, v55, v183\n" \
"v_cvt_pk_bf16_f32 v185, v186, v187\n" \
"v_mul_f32_e32 v188, v55, v188\n" \
"buffer_load_dwordx4 a[244:247], v17, s[56:59], s98 offen offset:0 sc1\n" \
"v_mul_f32_e32 v189, v55, v189\n" \
"v_mul_f32_e32 v190, v55, v190\n" \
"v_mul_f32_e32 v191, v55, v191\n" \
"v_cvt_pk_bf16_f32 v162, v164, v165\n" \
"v_cvt_pk_bf16_f32 v163, v166, v167\n" \
"v_cvt_pk_bf16_f32 v170, v172, v173\n" \
"buffer_store_dwordx4 v[160:163], v20, s[56:59], s69 offen offset:0 sc1\n" \
"v_cvt_pk_bf16_f32 v171, v174, v175\n" \
"v_cvt_pk_bf16_f32 v178, v180, v181\n" \
"v_cvt_pk_bf16_f32 v179, v182, v183\n" \
"v_cvt_pk_bf16_f32 v186, v188, v189\n" \
"v_cvt_pk_bf16_f32 v187, v190, v191\n" \
"v_readlane_b32 s93, v11, 20\n" \
"buffer_store_dwordx4 v[168:171], v20, s[56:59], s69 offen offset:1024 sc1\n" \
"v_readlane_b32 s84, v11, 15\n" \
"v_readlane_b32 s85, v11, 16\n" \
"v_readlane_b32 s86, v11, 17\n" \
"s_mov_b32 s87, 0x20000\n" \
"v_readlane_b32 s92, v11, 18\n" \
"v_readlane_b32 s91, v11, 21\n" \
"buffer_store_dwordx4 v[176:179], v20, s[56:59], s69 offen offset:2048 sc1\n" \
"v_mfma_f32_16x16x32_bf16 a[208:211], a[216:219], v[128:131], a[208:211]\n" \
"v_mfma_f32_16x16x32_bf16 a[208:211], a[216:219], v[136:139], a[208:211]\n" \
"buffer_store_dwordx4 v[184:187], v20, s[56:59], s69 offen offset:3072 sc1\n" \
"v_mfma_f32_32x32x16_bf16 v[128:143], a[216:219], a[216:219], 0\n" \
"v_mfma_f32_16x16x32_bf16 a[208:211], a[216:219], v[144:147], a[208:211]\n" \
"buffer_load_dwordx4 a[224:227], v20, s[56:59], s97 offen offset:1024 sc1\n" \
"v_mfma_f32_16x16x32_bf16 a[208:211], a[216:219], v[152:155], a[208:211]\n" \
"v_mfma_f32_32x32x16_bf16 v[144:159], a[216:219], a[216:219], 0\n" \
"buffer_load_dwordx4 a[232:235], v17, s[56:59], s97 offen offset:1024 sc1\n" \
"v_add_f32_e32 v56, v56, v24\n" \
"v_mfma_f32_16x16x32_bf16 a[208:211], a[216:219], v[160:163], a[208:211]\n" \
"buffer_load_dwordx4 a[240:243], v20, s[56:59], s98 offen offset:1024 sc1\n" \
"v_max_f32_e32 v62, v62, v56\n" \
"v_rcp_f32_e32 v56, v56\n" \
"v_mfma_f32_16x16x32_bf16 a[208:211], a[216:219], v[168:171], a[208:211]\n" \
"v_mfma_f32_32x32x16_bf16 v[160:175], a[216:219], a[216:219], 0\n" \
"buffer_load_dwordx4 a[248:251], v17, s[56:59], s98 offen offset:1024 sc1\n" \
"v_mul_f32_e32 v56, v56, v60\n" \
"v_mfma_f32_16x16x32_bf16 a[208:211], a[216:219], v[176:179], a[208:211]\n" \
"v_mul_f32_e32 v192, v56, v192\n" \
"v_mul_f32_e32 v193, v56, v193\n" \
"s_waitcnt vmcnt(17) lgkmcnt(0)\n" \
"s_barrier\n" \
"v_mfma_f32_16x16x32_bf16 v[64:67], a[128:131], a[64:67], v[32:35]\n" \
"s_add_u32 m0, s93, 0x0\n" \
"v_cvt_pk_bf16_f32 v192, v192, v193\n" \
"v_mfma_f32_16x16x32_bf16 v[64:67], a[132:135], a[68:71], v[64:67]\n" \
"buffer_load_dwordx4 v0, s[84:87], s92 offen offset:0 lds\n" \
"v_mfma_f32_16x16x32_bf16 v[64:67], a[136:139], a[72:75], v[64:67]\n" \
"v_mul_f32_e32 v194, v56, v194\n" \
"v_mul_f32_e32 v195, v56, v195\n" \
"v_mfma_f32_16x16x32_bf16 v[64:67], a[140:143], a[76:79], v[64:67]\n" \
"buffer_load_dwordx4 v1, s[84:87], s92 offen offset:1024 lds\n" \
"v_mfma_f32_16x16x32_bf16 v[68:71], a[156:159], a[76:79], v[32:35]\n" \
"v_mfma_f32_16x16x32_bf16 a[208:211], a[216:219], v[184:187], a[208:211]\n" \
"v_mfma_f32_32x32x16_bf16 v[176:191], a[216:219], a[216:219], 0\n" \
"buffer_load_dwordx4 v2, s[84:87], s92 offen offset:2048 lds\n" \
"v_cvt_pk_bf16_f32 v193, v194, v195\n" \
"v_mul_f32_e32 v196, v56, v196\n" \
"v_mul_f32_e32 v197, v56, v197\n" \
"v_mul_f32_e32 v198, v56, v198\n" \
"v_mfma_f32_16x16x32_bf16 v[68:71], a[152:155], a[72:75], v[68:71]\n" \
"buffer_load_dwordx4 v3, s[84:87], s92 offen offset:3072 lds\n" \
"v_mfma_f32_16x16x32_bf16 v[68:71], a[148:151], a[68:71], v[68:71]\n" \
"s_add_u32 m0, s91, 0x1000\n" \
"v_mul_f32_e32 v199, v56, v199\n" \
"v_mfma_f32_16x16x32_bf16 v[68:71], a[144:147], a[64:67], v[68:71]\n" \
"buffer_load_dwordx4 v4, s[84:87], s92 offen offset:0 lds\n" \
"v_mfma_f32_16x16x32_bf16 v[76:79], a[144:147], a[80:83], v[36:39]\n" \
"ds_read_b128 a[144:147], v48 offset:12288\n" \
"v_mul_f32_e32 v200, v56, v200\n" \
"v_mfma_f32_16x16x32_bf16 v[76:79], a[148:151], a[84:87], v[76:79]\n" \
"buffer_load_dwordx4 v5, s[84:87], s92 offen offset:1024 lds\n" \
"v_mfma_f32_16x16x32_bf16 v[76:79], a[152:155], a[88:91], v[76:79]\n" \
"ds_read_b128 a[148:151], v48 offset:13312\n" \
"ds_read_b128 a[152:155], v48 offset:14336\n" \
"v_mfma_f32_16x16x32_bf16 v[76:79], a[156:159], a[92:95], v[76:79]\n" \
"buffer_load_dwordx4 v6, s[84:87], s92 offen offset:2048 lds\n" \
"v_mfma_f32_16x16x32_bf16 v[72:75], a[140:143], a[92:95], v[36:39]\n" \
"ds_read_b128 a[140:143], v48 offset:11264\n" \
"ds_read_b128 a[156:159], v48 offset:15360\n" \
"v_mfma_f32_16x16x32_bf16 v[72:75], a[136:139], a[88:91], v[72:75]\n" \
"buffer_load_dwordx4 v7, s[84:87], s92 offen offset:3072 lds\n" \
"v_mfma_f32_16x16x32_bf16 v[72:75], a[132:135], a[84:87], v[72:75]\n" \
"ds_read_b128 a[132:135], v48 offset:9216\n" \
"ds_read_b128 a[136:139], v48 offset:10240\n" \
"v_mfma_f32_16x16x32_bf16 v[72:75], a[128:131], a[80:83], v[72:75]\n" \
"ds_read_b128 a[128:131], v48 offset:8192\n" \
"v_mul_f32_e32 v201, v56, v201\n" \
"v_accvgpr_read_b32 v57, a198\n" \
"v_accvgpr_read_b32 v25, a199\n" \
"v_mfma_f32_16x16x32_bf16 a[196:199], a[216:219], a[216:219], 0\n" \
"v_cvt_pk_bf16_f32 v194, v196, v197\n" \
"v_cvt_pk_bf16_f32 v195, v198, v199\n" \
"buffer_store_dwordx4 v[192:195], v20, s[56:59], s70 offen offset:0 sc1\n" \
"v_cvt_pk_bf16_f32 v200, v200, v201\n" \
"v_mul_f32_e32 v202, v56, v202\n" \
"v_mul_f32_e32 v203, v56, v203\n" \
"v_add_f32_e32 v57, v57, v25\n" \
"v_mfma_f32_16x16x32_bf16 a[208:211], a[216:219], v[192:195], a[208:211]\n" \
"v_cvt_pk_bf16_f32 v201, v202, v203\n" \
"v_mul_f32_e32 v204, v56, v204\n" \
"v_mul_f32_e32 v205, v56, v205\n" \
"v_mul_f32_e32 v206, v56, v206\n" \
"v_mul_f32_e32 v207, v56, v207\n" \
"v_mul_f32_e32 v208, v56, v208\n" \
"v_mul_f32_e32 v209, v56, v209\n" \
"v_max_f32_e32 v62, v62, v57\n" \
"v_cvt_pk_bf16_f32 v202, v204, v205\n" \
"v_cvt_pk_bf16_f32 v203, v206, v207\n" \
"buffer_store_dwordx4 v[200:203], v20, s[56:59], s70 offen offset:1024 sc1\n" \
".fill (L_ab + 28672 - .) / 4, 4, 0xBF800000\n" \
"L_ap7:\n" \
"s_cbranch_execz L_ap0\n" \
"v_cvt_pk_bf16_f32 v208, v208, v209\n" \
"v_mul_f32_e32 v210, v56, v210\n" \
"v_mul_f32_e32 v211, v56, v211\n" \
"v_rcp_f32_e32 v57, v57\n" \
"v_mfma_f32_16x16x32_bf16 a[208:211], a[216:219], v[200:203], a[208:211]\n" \
"v_mfma_f32_32x32x16_bf16 v[192:207], a[216:219], a[216:219], 0\n" \
"v_cvt_pk_bf16_f32 v209, v210, v211\n" \
"v_mul_f32_e32 v212, v56, v212\n" \
"v_mul_f32_e32 v213, v56, v213\n" \
"v_mul_f32_e32 v214, v56, v214\n" \
"v_mul_f32_e32 v215, v56, v215\n" \
"v_mul_f32_e32 v216, v56, v216\n" \
"v_mul_f32_e32 v217, v56, v217\n" \
"v_cvt_pk_bf16_f32 v210, v212, v213\n" \
"v_cvt_pk_bf16_f32 v211, v214, v215\n" \
"buffer_store_dwordx4 v[208:211], v20, s[56:59], s70 offen offset:2048 sc1\n" \
"v_cvt_pk_bf16_f32 v216, v216, v217\n" \
"v_mul_f32_e32 v218, v56, v218\n" \
"v_mul_f32_e32 v219, v56, v219\n" \
"v_mul_f32_e32 v57, v57, v61\n" \
"v_mfma_f32_16x16x32_bf16 a[208:211], a[216:219], v[208:211], a[208:211]\n" \
"v_cvt_pk_bf16_f32 v217, v218, v219\n" \
"v_mul_f32_e32 v220, v56, v220\n" \
"v_mul_f32_e32 v221, v56, v221\n" \
"v_mul_f32_e32 v222, v56, v222\n" \
"v_mul_f32_e32 v223, v56, v223\n" \
"v_mul_f32_e32 v224, v57, v224\n" \
"v_mul_f32_e32 v225, v57, v225\n" \
"v_cvt_pk_bf16_f32 v218, v220, v221\n" \
"v_cvt_pk_bf16_f32 v219, v222, v223\n" \
"buffer_store_dwordx4 v[216:219], v20, s[56:59], s70 offen offset:3072 sc1\n" \
"v_cvt_pk_bf16_f32 v224, v224, v225\n" \
"v_mul_f32_e32 v226, v57, v226\n" \
"v_mul_f32_e32 v227, v57, v227\n" \
"v_mfma_f32_16x16x32_bf16 a[208:211], a[216:219], v[216:219], a[208:211]\n" \
"v_mfma_f32_32x32x16_bf16 v[208:223], a[216:219], a[216:219], 0\n" \
"v_cvt_pk_bf16_f32 v225, v226, v227\n" \
"v_mul_f32_e32 v228, v57, v228\n" \
"v_mul_f32_e32 v229, v57, v229\n" \
"v_mul_f32_e32 v230, v57, v230\n" \
"v_mul_f32_e32 v231, v57, v231\n" \
"v_mul_f32_e32 v232, v57, v232\n" \
"v_mul_f32_e32 v233, v57, v233\n" \
"v_cvt_pk_bf16_f32 v226, v228, v229\n" \
"v_cvt_pk_bf16_f32 v227, v230, v231\n" \
"buffer_store_dwordx4 v[224:227], v20, s[56:59], s71 offen offset:0 sc1\n" \
"v_cvt_pk_bf16_f32 v232, v232, v233\n" \
"v_mul_f32_e32 v234, v57, v234\n" \
"v_mul_f32_e32 v235, v57, v235\n" \
"v_mfma_f32_16x16x32_bf16 a[208:211], a[216:219], v[224:227], a[208:211]\n" \
"v_cvt_pk_bf16_f32 v233, v234, v235\n" \
"v_mul_f32_e32 v236, v57, v236\n" \
"v_mul_f32_e32 v237, v57, v237\n" \
"v_mul_f32_e32 v238, v57, v238\n" \
"v_mul_f32_e32 v239, v57, v239\n" \
"v_mul_f32_e32 v240, v57, v240\n" \
"v_mul_f32_e32 v241, v57, v241\n" \
"v_cvt_pk_bf16_f32 v234, v236, v237\n" \
"v_cvt_pk_bf16_f32 v235, v238, v239\n" \
"buffer_store_dwordx4 v[232:235], v20, s[56:59], s71 offen offset:1024 sc1\n" \
"v_cvt_pk_bf16_f32 v240, v240, v241\n" \
"v_mul_f32_e32 v242, v57, v242\n" \
"v_mul_f32_e32 v243, v57, v243\n" \
"v_mfma_f32_16x16x32_bf16 a[208:211], a[216:219], v[232:235], a[208:211]\n" \
"v_mfma_f32_32x32x16_bf16 v[224:239], a[216:219], a[216:219], 0\n" \
"v_cvt_pk_bf16_f32 v241, v242, v243\n" \
"v_mul_f32_e32 v244, v57, v244\n" \
"v_mul_f32_e32 v245, v57, v245\n" \
"v_mul_f32_e32 v246, v57, v246\n" \
"v_mul_f32_e32 v247, v57, v247\n" \
"v_mul_f32_e32 v248, v57, v248\n" \
"v_mul_f32_e32 v249, v57, v249\n" \
"v_cvt_pk_bf16_f32 v242, v244, v245\n" \
"v_cvt_pk_bf16_f32 v243, v246, v247\n" \
"buffer_store_dwordx4 v[240:243], v20, s[56:59], s71 offen offset:2048 sc1\n" \
"v_cvt_pk_bf16_f32 v248, v248, v249\n" \
"v_mul_f32_e32 v250, v57, v250\n" \
"v_mul_f32_e32 v251, v57, v251\n" \
"v_mfma_f32_16x16x32_bf16 a[208:211], a[216:219], v[240:243], a[208:211]\n" \
"v_cvt_pk_bf16_f32 v249, v250, v251\n" \
"v_mul_f32_e32 v252, v57, v252\n" \
"v_mul_f32_e32 v253, v57, v253\n" \
"v_mul_f32_e32 v254, v57, v254\n" \
"v_mul_f32_e32 v255, v57, v255\n" \
"v_cvt_pk_bf16_f32 v250, v252, v253\n" \
"v_cvt_pk_bf16_f32 v251, v254, v255\n" \
"buffer_store_dwordx4 v[248:251], v20, s[56:59], s71 offen offset:3072 sc1\n" \
"s_add_u32 m0, s91, 0x2000\n" \
"v_mfma_f32_16x16x32_bf16 a[208:211], a[216:219], v[248:251], a[208:211]\n" \
"v_mfma_f32_32x32x16_bf16 v[240:255], a[216:219], a[216:219], 0\n" \
"v_add_u32_e32 v53, 0x20000, v63\n" \
"buffer_load_dwordx4 v8, s[84:87], s92 offen offset:0 lds\n" \
"v_exp_f32_e32 v70, v70\n" \
"v_exp_f32_e32 v71, v71\n" \
"v_exp_f32_e32 v72, v72\n" \
"v_exp_f32_e32 v73, v73\n" \
"s_add_u32 s64, s64, 0x10000\n" \
"v_accvgpr_read_b32 v63, a208\n" \
"v_cmp_u_f32_e32 vcc, v63, v63\n" \
"v_cmp_lt_f32_e64 s[72:73], s21, v62\n" \
"v_readlane_b32 s95, v11, 3\n" \
"v_exp_f32_e32 v68, v68\n" \
"v_exp_f32_e32 v69, v69\n" \
"v_exp_f32_e32 v74, v74\n" \
"v_exp_f32_e32 v75, v75\n" \
"s_or_b64 vcc, s[72:73], vcc\n" \
"s_cselect_b32 s95, 0x40000000, s95\n" \
"s_cselect_b32 s94, 0x7f800000, 0\n" \
"v_cvt_pk_bf16_f32 v71, v70, v71\n" \
"v_exp_f32_e32 v66, v66\n" \
"v_exp_f32_e32 v67, v67\n" \
"v_exp_f32_e32 v64, v64\n" \
"v_exp_f32_e32 v65, v65\n" \
"v_cvt_pk_bf16_f32 v72, v72, v73\n" \
"v_exp_f32_e32 v76, v76\n" \
"v_exp_f32_e32 v77, v77\n" \
"v_exp_f32_e32 v78, v78\n" \
"v_exp_f32_e32 v79, v79\n" \
"v_cvt_pk_bf16_f32 v70, v68, v69\n" \
"s_waitcnt vmcnt(17) lgkmcnt(0)\n" \
"s_barrier\n" \
"v_cvt_pk_bf16_f32 v73, v74, v75\n" \
"v_add_u32_e32 v49, 0x10000, v48\n" \
"s_xor_b32 s65, s65, 0x8000\n" \
"v_cvt_pk_bf16_f32 v69, v66, v67\n" \
"v_cvt_pk_bf16_f32 v68, v64, v65\n" \
"v_cvt_pk_bf16_f32 v74, v76, v77\n" \
"v_cvt_pk_bf16_f32 v75, v78, v79\n" \
"v_xor_b32_e32 v48, 0x8000, v48\n" \
"s_sub_u32 s66, s19, 2\n" \
"s_branch L_f1_%=\n" \
".fill (L_ab + 32768 - .) / 4, 4, 0xBF800000\n" \
".fill (L_ab + 32768 - .) / 4, 4, 0xBF800000\n" \
"L_ap8:\n" \
"s_setpc_b64 s[88:89]\n" \
"L_out_%=:\n" \
"v_bfe_u32 v38, v20, 2, 4\n" \
"v_readlane_b32 s64, v11, 2\n" \
"v_add_u32_e32 v32, 0x49000, v20\n" \
"v_add_u32_e32 v34, 0x20000, v10\n" \
"ds_read_b64_tr_b16 a[0:1], v34 offset:0\n" \
"ds_read_b64_tr_b16 a[2:3], v34 offset:4608\n" \
"ds_read_b64_tr_b16 a[4:5], v34 offset:9216\n" \
"buffer_load_dword v26, v38, s[56:59], s64 offen sc1\n" \
"ds_read_b64_tr_b16 a[6:7], v34 offset:13824\n" \
"ds_read_b64_tr_b16 a[8:9], v34 offset:32\n" \
"ds_read_b64_tr_b16 a[10:11], v34 offset:4640\n" \
"ds_read_b64_tr_b16 a[12:13], v34 offset:9248\n" \
"buffer_store_dwordx4 v[64:67], v20, s[56:59], s61 offen offset:0 sc1\n" \
"ds_read_b64_tr_b16 a[14:15], v34 offset:13856\n" \
"v_mov_b32_e32 v39, s62\n" \
"v_readlane_b32 s70, v11, 5\n" \
"v_readlane_b32 s71, v11, 4\n" \
"buffer_store_dwordx4 v[68:71], v20, s[56:59], s61 offen offset:1024 sc1\n" \
"ds_read_b64_tr_b16 a[16:17], v34 offset:64\n" \
"ds_read_b64_tr_b16 a[18:19], v34 offset:4672\n" \
"ds_read_b64_tr_b16 a[20:21], v34 offset:9280\n" \
"ds_read_b64_tr_b16 a[22:23], v34 offset:13888\n" \
"buffer_store_dwordx4 v[72:75], v20, s[56:59], s61 offen offset:2048 sc1\n" \
"ds_read_b64_tr_b16 a[24:25], v34 offset:96\n" \
"ds_read_b64_tr_b16 a[26:27], v34 offset:4704\n" \
"ds_read_b64_tr_b16 a[28:29], v34 offset:9312\n" \
"ds_read_b64_tr_b16 a[30:31], v34 offset:13920\n" \
"buffer_store_dwordx4 v[76:79], v20, s[56:59], s61 offen offset:3072 sc1\n" \
"ds_read_b64_tr_b16 a[192:193], v34 offset:128\n" \
"ds_read_b64_tr_b16 a[194:195], v34 offset:4736\n" \
"ds_read_b64_tr_b16 a[196:197], v34 offset:9344\n" \
"ds_read_b64_tr_b16 a[198:199], v34 offset:13952\n" \
"buffer_store_dwordx4 v[80:83], v32, s[56:59], s61 offen offset:0 sc1\n" \
"ds_read_b64_tr_b16 a[200:201], v34 offset:160\n" \
"ds_read_b64_tr_b16 a[202:203], v34 offset:4768\n" \
"ds_read_b64_tr_b16 a[204:205], v34 offset:9376\n" \
"ds_read_b64_tr_b16 a[206:207], v34 offset:13984\n" \
"buffer_store_dwordx4 v[84:87], v32, s[56:59], s61 offen offset:1024 sc1\n" \
"ds_read_b64_tr_b16 a[208:209], v34 offset:192\n" \
"s_waitcnt vmcnt(16) lgkmcnt(15)\n" \
"v_mfma_f32_16x16x32_bf16 v[112:115], a[0:3], a[96:99], 0\n" \
"buffer_store_dwordx4 v[96:99], v32, s[56:59], s61 offen offset:2048 sc1\n" \
"v_mfma_f32_16x16x32_bf16 v[112:115], a[4:7], a[100:103], v[112:115]\n" \
"ds_read_b64_tr_b16 a[210:211], v34 offset:4800\n" \
"ds_read_b64_tr_b16 a[212:213], v34 offset:9408\n" \
"buffer_store_dwordx4 v[100:103], v32, s[56:59], s61 offen offset:3072 sc1\n" \
"v_mfma_f32_16x16x32_bf16 v[116:119], a[0:3], a[112:115], 0\n" \
"ds_read_b64_tr_b16 a[214:215], v34 offset:14016\n" \
"ds_read_b64_tr_b16 a[216:217], v34 offset:224\n" \
"v_mfma_f32_16x16x32_bf16 v[116:119], a[4:7], a[116:119], v[116:119]\n" \
"ds_read_b64_tr_b16 a[218:219], v34 offset:4832\n" \
"v_lshrrev_b32_e32 v44, 9, v20\n" \
"v_mfma_f32_16x16x32_bf16 v[184:187], a[0:3], v[64:67], 0\n" \
"v_mul_u32_u24_e32 v44, 0xb40, v44\n" \
"v_add_u32_e32 v35, v44, v10\n" \
"v_mfma_f32_16x16x32_bf16 v[184:187], a[4:7], v[68:71], v[184:187]\n" \
"v_readlane_b32 s65, v11, 30\n" \
"v_readlane_b32 s67, v11, 23\n" \
"v_mfma_f32_16x16x32_bf16 v[188:191], a[0:3], v[80:83], 0\n" \
"ds_read_b64_tr_b16 a[0:1], v34 offset:9440\n" \
"ds_read_b64_tr_b16 a[2:3], v34 offset:14048\n" \
"v_mfma_f32_16x16x32_bf16 v[188:191], a[4:7], v[84:87], v[188:191]\n" \
"ds_read_b64_tr_b16 a[4:5], v34 offset:256\n" \
"ds_read_b64_tr_b16 a[6:7], v34 offset:4864\n" \
"v_mfma_f32_16x16x32_bf16 v[196:199], a[8:11], v[80:83], 0\n" \
"v_readlane_b32 s80, v11, 11\n" \
"v_readlane_b32 s81, v11, 12\n" \
"v_mfma_f32_16x16x32_bf16 v[196:199], a[12:15], v[84:87], v[196:199]\n" \
"s_mov_b32 s82, 0x47fca0\n" \
"s_mov_b32 s83, 0x20000\n" \
"v_mfma_f32_16x16x32_bf16 v[192:195], a[8:11], v[64:67], 0\n" \
"v_readlane_b32 s66, v11, 28\n" \
"s_add_u32 m0, s67, 0x0\n" \
"v_mfma_f32_16x16x32_bf16 v[192:195], a[12:15], v[68:71], v[192:195]\n" \
"v_readlane_b32 s92, v11, 24\n" \
"v_readlane_b32 s73, v11, 29\n" \
"v_mfma_f32_16x16x32_bf16 v[124:127], a[8:11], a[112:115], 0\n" \
"buffer_load_dwordx4 v0, s[80:83], s66 offen offset:0 lds\n" \
"v_mfma_f32_16x16x32_bf16 v[124:127], a[12:15], a[116:119], v[124:127]\n" \
"v_readlane_b32 s94, v11, 25\n" \
"v_readlane_b32 s84, v11, 9\n" \
"v_mfma_f32_16x16x32_bf16 v[120:123], a[8:11], a[96:99], 0\n" \
"ds_read_b64_tr_b16 a[8:9], v34 offset:9472\n" \
"ds_read_b64_tr_b16 a[10:11], v34 offset:14080\n" \
"v_mfma_f32_16x16x32_bf16 v[120:123], a[12:15], a[100:103], v[120:123]\n" \
"ds_read_b64_tr_b16 a[12:13], v34 offset:18432\n" \
"ds_read_b64_tr_b16 a[14:15], v34 offset:23040\n" \
"v_mfma_f32_16x16x32_bf16 v[128:131], a[16:19], a[96:99], 0\n" \
"v_readlane_b32 s85, v11, 10\n" \
"s_waitcnt lgkmcnt(9)\n" \
"v_mfma_f32_16x16x32_bf16 v[128:131], a[20:23], a[100:103], v[128:131]\n" \
"buffer_load_dwordx4 v1, s[80:83], s66 offen offset:1024 lds\n" \
"v_mfma_f32_16x16x32_bf16 v[132:135], a[16:19], a[112:115], 0\n" \
"s_mov_b32 s86, 0xa1ca0\n" \
"s_mov_b32 s87, 0x20000\n" \
"v_mfma_f32_16x16x32_bf16 v[132:135], a[20:23], a[116:119], v[132:135]\n" \
"v_add_u32_e32 v33, 0x10000, v10\n" \
"v_readlane_b32 s68, v11, 31\n" \
"v_mfma_f32_16x16x32_bf16 v[200:203], a[16:19], v[64:67], 0\n" \
"v_readlane_b32 s69, v11, 32\n" \
"v_readlane_b32 s72, v11, 34\n" \
"v_mfma_f32_16x16x32_bf16 v[200:203], a[20:23], v[68:71], v[200:203]\n" \
"v_readlane_b32 s93, v11, 35\n" \
"v_readlane_b32 s63, v11, 41\n" \
"v_mfma_f32_16x16x32_bf16 v[204:207], a[16:19], v[80:83], 0\n" \
"ds_read_b64_tr_b16 a[16:17], v34 offset:18464\n" \
"ds_read_b64_tr_b16 a[18:19], v34 offset:23072\n" \
"v_mfma_f32_16x16x32_bf16 v[204:207], a[20:23], v[84:87], v[204:207]\n" \
"ds_read_b64_tr_b16 a[20:21], v34 offset:18496\n" \
"ds_read_b64_tr_b16 a[22:23], v34 offset:23104\n" \
"v_mfma_f32_16x16x32_bf16 v[212:215], a[24:27], v[80:83], 0\n" \
"buffer_load_dwordx4 v2, s[80:83], s66 offen offset:2048 lds\n" \
"v_mfma_f32_16x16x32_bf16 v[212:215], a[28:31], v[84:87], v[212:215]\n" \
"v_bfe_u32 v40, v20, 2, 7\n" \
"s_add_u32 s74, s63, 0x3800\n" \
"v_mfma_f32_16x16x32_bf16 v[208:211], a[24:27], v[64:67], 0\n" \
"v_lshlrev_b32_e32 v41, 12, v20\n" \
"s_add_u32 s12, s63, 0x0\n" \
"v_mfma_f32_16x16x32_bf16 v[208:211], a[28:31], v[68:71], v[208:211]\n" \
"v_readlane_b32 s95, v11, 38\n" \
"v_readlane_b32 s96, v11, 39\n" \
"v_mfma_f32_16x16x32_bf16 v[140:143], a[24:27], a[112:115], 0\n" \
"s_add_u32 s13, s63, 0x1000\n" \
"v_readlane_b32 s97, v11, 40\n" \
"v_mfma_f32_16x16x32_bf16 v[140:143], a[28:31], a[116:119], v[140:143]\n" \
"buffer_load_dwordx4 v3, s[80:83], s66 offen offset:3072 lds\n" \
"v_mfma_f32_16x16x32_bf16 v[136:139], a[24:27], a[96:99], 0\n" \
"ds_read_b64_tr_b16 a[24:25], v34 offset:18528\n" \
"ds_read_b64_tr_b16 a[26:27], v34 offset:23136\n" \
"v_mfma_f32_16x16x32_bf16 v[136:139], a[28:31], a[100:103], v[136:139]\n" \
"ds_read_b64_tr_b16 a[28:29], v34 offset:18560\n" \
"ds_read_b64_tr_b16 a[30:31], v34 offset:23168\n" \
"v_mfma_f32_16x16x32_bf16 v[144:147], a[192:195], a[96:99], 0\n" \
"s_add_u32 m0, s67, 0x1000\n" \
"s_add_u32 s14, s63, 0x2000\n" \
"v_mfma_f32_16x16x32_bf16 v[144:147], a[196:199], a[100:103], v[144:147]\n" \
"s_add_u32 s15, s63, 0x3000\n" \
"v_readlane_b32 s100, v11, 7\n" \
"v_mfma_f32_16x16x32_bf16 v[148:151], a[192:195], a[112:115], 0\n" \
"buffer_load_dwordx4 v4, s[80:83], s66 offen offset:0 lds\n" \
"v_mfma_f32_16x16x32_bf16 v[148:151], a[196:199], a[116:119], v[148:151]\n" \
"v_readlane_b32 s16, v11, 42\n" \
"v_bfe_u32 v44, v20, 4, 4\n" \
"v_mfma_f32_16x16x32_bf16 v[216:219], a[192:195], v[64:67], 0\n" \
"s_add_u32 s17, s16, 0x1000\n" \
"s_add_u32 s18, s16, 0x2000\n" \
"v_mfma_f32_16x16x32_bf16 v[216:219], a[196:199], v[68:71], v[216:219]\n" \
"s_add_u32 s19, s16, 0x3000\n" \
"v_bfe_u32 v45, v20, 8, 2\n" \
"v_mfma_f32_16x16x32_bf16 v[220:223], a[192:195], v[80:83], 0\n" \
"ds_read_b64_tr_b16 a[192:193], v34 offset:18592\n" \
"ds_read_b64_tr_b16 a[194:195], v34 offset:23200\n" \
"v_mfma_f32_16x16x32_bf16 v[220:223], a[196:199], v[84:87], v[220:223]\n" \
"ds_read_b64_tr_b16 a[196:197], v34 offset:18624\n" \
"ds_read_b64_tr_b16 a[198:199], v34 offset:23232\n" \
"v_mfma_f32_16x16x32_bf16 v[228:231], a[200:203], v[80:83], 0\n" \
"buffer_load_dwordx4 v5, s[80:83], s66 offen offset:1024 lds\n" \
"v_mfma_f32_16x16x32_bf16 v[228:231], a[204:207], v[84:87], v[228:231]\n" \
"v_mul_u32_u24_e32 v42, 0x480, v44\n" \
"v_lshl_add_u32 v42, v45, 3, v42\n" \
"v_mfma_f32_16x16x32_bf16 v[224:227], a[200:203], v[64:67], 0\n" \
"s_waitcnt vmcnt(14)\n" \
"v_cmp_ne_u32_e32 vcc, s62, v26\n" \
"v_mfma_f32_16x16x32_bf16 v[224:227], a[204:207], v[68:71], v[224:227]\n" \
"v_lshrrev_b32_e32 v44, 1, v44\n" \
"v_lshlrev_b32_e32 v45, 1, v45\n" \
"v_mfma_f32_16x16x32_bf16 v[156:159], a[200:203], a[112:115], 0\n" \
"v_sub_u32_e32 v44, v44, v45\n" \
"s_cbranch_vccnz L_sa_%=\n" \
"L_ra_%=:\n" \
"v_mfma_f32_16x16x32_bf16 v[156:159], a[204:207], a[116:119], v[156:159]\n" \
"buffer_load_dwordx4 a[64:67], v20, s[56:59], s65 offen offset:0 sc1\n" \
"v_mfma_f32_16x16x32_bf16 v[152:155], a[200:203], a[96:99], 0\n" \
"ds_read_b64_tr_b16 a[200:201], v34 offset:18656\n" \
"ds_read_b64_tr_b16 a[202:203], v34 offset:23264\n" \
"v_mfma_f32_16x16x32_bf16 v[152:155], a[204:207], a[100:103], v[152:155]\n" \
"ds_read_b64_tr_b16 a[204:205], v34 offset:18688\n" \
"ds_read_b64_tr_b16 a[206:207], v34 offset:23296\n" \
"v_mfma_f32_16x16x32_bf16 v[160:163], a[208:211], a[96:99], 0\n" \
"v_readlane_b32 s64, v11, 33\n" \
"v_and_b32_e32 v45, 16, v20\n" \
"v_mfma_f32_16x16x32_bf16 v[160:163], a[212:215], a[100:103], v[160:163]\n" \
"v_mov_b32_e32 v46, 0x3f80\n" \
"v_lshlrev_b32_e32 v45, v45, v46\n" \
"v_mfma_f32_16x16x32_bf16 v[164:167], a[208:211], a[112:115], 0\n" \
"buffer_load_dwordx4 a[68:71], v20, s[56:59], s65 offen offset:1024 sc1\n" \
"v_mfma_f32_16x16x32_bf16 v[164:167], a[212:215], a[116:119], v[164:167]\n" \
"v_mov_b32_e32 v52, 0\n" \
"v_mov_b32_e32 v53, 0\n" \
"v_mfma_f32_16x16x32_bf16 v[232:235], a[208:211], v[64:67], 0\n" \
"v_mov_b32_e32 v48, 0\n" \
"v_mov_b32_e32 v49, 0\n" \
"v_mfma_f32_16x16x32_bf16 v[232:235], a[212:215], v[68:71], v[232:235]\n" \
"v_add_u32_e32 v43, 0x9000, v42\n" \
"v_readlane_b32 s88, v11, 13\n" \
"v_mfma_f32_16x16x32_bf16 v[236:239], a[208:211], v[80:83], 0\n" \
"ds_read_b64_tr_b16 a[208:209], v10 offset:32768\n" \
"ds_read_b64_tr_b16 a[210:211], v10 offset:37376\n" \
"v_mfma_f32_16x16x32_bf16 v[236:239], a[212:215], v[84:87], v[236:239]\n" \
"ds_read_b64_tr_b16 a[212:213], v10 offset:32800\n" \
"s_waitcnt lgkmcnt(7)\n" \
"v_mfma_f32_16x16x32_bf16 v[244:247], a[216:219], v[80:83], 0\n" \
"ds_read_b64_tr_b16 a[214:215], v10 offset:37408\n" \
"v_readlane_b32 s89, v11, 14\n" \
"v_mfma_f32_16x16x32_bf16 v[244:247], a[0:3], v[84:87], v[244:247]\n" \
"buffer_load_dwordx4 a[80:83], v32, s[56:59], s65 offen offset:0 sc1\n" \
"v_mfma_f32_16x16x32_bf16 v[240:243], a[216:219], v[64:67], 0\n" \
"s_mov_b32 s90, 0x240000\n" \
"s_mov_b32 s91, 0x20000\n" \
"v_mfma_f32_16x16x32_bf16 v[240:243], a[0:3], v[68:71], v[240:243]\n" \
"v_mov_b32_e32 v46, s62\n" \
"v_mfma_f32_16x16x32_bf16 v[172:175], a[216:219], a[112:115], 0\n" \
"v_mfma_f32_16x16x32_bf16 v[172:175], a[0:3], a[116:119], v[172:175]\n" \
"v_mfma_f32_16x16x32_bf16 v[168:171], a[216:219], a[96:99], 0\n" \
"ds_read_b64_tr_b16 a[216:217], v10 offset:32832\n" \
"ds_read_b64_tr_b16 a[218:219], v10 offset:37440\n" \
"v_mfma_f32_16x16x32_bf16 v[168:171], a[0:3], a[100:103], v[168:171]\n" \
"ds_read_b64_tr_b16 a[0:1], v10 offset:32864\n" \
"ds_read_b64_tr_b16 a[2:3], v10 offset:37472\n" \
"v_mfma_f32_16x16x32_bf16 v[176:179], a[4:7], a[96:99], 0\n" \
"buffer_load_dwordx4 a[84:87], v32, s[56:59], s65 offen offset:1024 sc1\n" \
"v_mfma_f32_16x16x32_bf16 v[176:179], a[8:11], a[100:103], v[176:179]\n" \
"v_mfma_f32_16x16x32_bf16 v[180:183], a[4:7], a[112:115], 0\n" \
"v_mfma_f32_16x16x32_bf16 v[180:183], a[8:11], a[116:119], v[180:183]\n" \
"v_mfma_f32_16x16x32_bf16 v[248:251], a[4:7], v[64:67], 0\n" \
"v_mfma_f32_16x16x32_bf16 v[248:251], a[8:11], v[68:71], v[248:251]\n" \
"buffer_load_dwordx4 a[72:75], v20, s[56:59], s65 offen offset:2048 sc1\n" \
"v_mfma_f32_16x16x32_bf16 v[252:255], a[4:7], v[80:83], 0\n" \
"ds_read_b64_tr_b16 a[4:5], v10 offset:32896\n" \
"ds_read_b64_tr_b16 a[6:7], v10 offset:37504\n" \
"v_mfma_f32_16x16x32_bf16 v[252:255], a[8:11], v[84:87], v[252:255]\n" \
"ds_read_b64_tr_b16 a[8:9], v10 offset:32928\n" \
"ds_read_b64_tr_b16 a[10:11], v10 offset:37536\n" \
"v_mfma_f32_16x16x32_bf16 v[112:115], a[12:15], a[104:107], v[112:115]\n" \
"v_mfma_f32_16x16x32_bf16 v[116:119], a[12:15], a[120:123], v[116:119]\n" \
"v_mfma_f32_16x16x32_bf16 v[184:187], a[12:15], v[72:75], v[184:187]\n" \
"buffer_load_dwordx4 a[76:79], v20, s[56:59], s65 offen offset:3072 sc1\n" \
"v_mfma_f32_16x16x32_bf16 v[188:191], a[12:15], v[96:99], v[188:191]\n" \
"ds_read_b64_tr_b16 a[12:13], v10 offset:32960\n" \
"ds_read_b64_tr_b16 a[14:15], v10 offset:37568\n" \
"v_mfma_f32_16x16x32_bf16 v[196:199], a[16:19], v[96:99], v[196:199]\n" \
"v_mfma_f32_16x16x32_bf16 v[192:195], a[16:19], v[72:75], v[192:195]\n" \
"v_mfma_f32_16x16x32_bf16 v[124:127], a[16:19], a[120:123], v[124:127]\n" \
"v_mfma_f32_16x16x32_bf16 v[120:123], a[16:19], a[104:107], v[120:123]\n" \
"ds_read_b64_tr_b16 a[16:17], v10 offset:32992\n" \
"ds_read_b64_tr_b16 a[18:19], v10 offset:37600\n" \
"v_mfma_f32_16x16x32_bf16 v[128:131], a[20:23], a[104:107], v[128:131]\n" \
"buffer_load_dwordx4 a[88:91], v32, s[56:59], s65 offen offset:2048 sc1\n" \
"v_mfma_f32_16x16x32_bf16 v[132:135], a[20:23], a[120:123], v[132:135]\n" \
"v_mfma_f32_16x16x32_bf16 v[200:203], a[20:23], v[72:75], v[200:203]\n" \
"v_mfma_f32_16x16x32_bf16 v[204:207], a[20:23], v[96:99], v[204:207]\n" \
"ds_read_b64_tr_b16 a[20:21], v10 offset:33024\n" \
"ds_read_b64_tr_b16 a[22:23], v10 offset:37632\n" \
"s_waitcnt lgkmcnt(10)\n" \
"s_barrier\n" \
"v_mfma_f32_16x16x32_bf16 v[212:215], a[24:27], v[96:99], v[212:215]\n" \
"buffer_load_dwordx4 a[92:95], v32, s[56:59], s65 offen offset:3072 sc1\n" \
"v_mfma_f32_16x16x32_bf16 v[208:211], a[24:27], v[72:75], v[208:211]\n" \
"v_readlane_b32 s65, v11, 46\n" \
"v_mfma_f32_16x16x32_bf16 v[140:143], a[24:27], a[120:123], v[140:143]\n" \
"v_mfma_f32_16x16x32_bf16 v[136:139], a[24:27], a[104:107], v[136:139]\n" \
"ds_read_b64_tr_b16 a[24:25], v35 offset:41984\n" \
"ds_read_b64_tr_b16 a[26:27], v35 offset:44288\n" \
"v_mfma_f32_16x16x32_bf16 v[144:147], a[28:31], a[104:107], v[144:147]\n" \
"v_mfma_f32_16x16x32_bf16 v[148:151], a[28:31], a[120:123], v[148:151]\n" \
"buffer_load_dwordx4 v6, s[80:83], s66 offen offset:2048 lds\n" \
"v_mfma_f32_16x16x32_bf16 v[216:219], a[28:31], v[72:75], v[216:219]\n" \
"v_mfma_f32_16x16x32_bf16 v[220:223], a[28:31], v[96:99], v[220:223]\n" \
"ds_read_b64_tr_b16 a[28:29], v35 offset:52352\n" \
"ds_read_b64_tr_b16 a[30:31], v35 offset:54656\n" \
"v_mfma_f32_16x16x32_bf16 v[228:231], a[192:195], v[96:99], v[228:231]\n" \
"v_mfma_f32_16x16x32_bf16 v[224:227], a[192:195], v[72:75], v[224:227]\n" \
"v_mfma_f32_16x16x32_bf16 v[156:159], a[192:195], a[120:123], v[156:159]\n" \
"buffer_load_dwordx4 v7, s[80:83], s66 offen offset:3072 lds\n" \
"v_mfma_f32_16x16x32_bf16 v[152:155], a[192:195], a[104:107], v[152:155]\n" \
"ds_read_b64_tr_b16 a[192:193], v35 offset:42016\n" \
"ds_read_b64_tr_b16 a[194:195], v35 offset:44320\n" \
"v_mfma_f32_16x16x32_bf16 v[160:163], a[196:199], a[104:107], v[160:163]\n" \
"s_add_u32 m0, s67, 0x2000\n" \
"v_readlane_b32 s67, v11, 48\n" \
"v_mfma_f32_16x16x32_bf16 v[164:167], a[196:199], a[120:123], v[164:167]\n" \
"v_mfma_f32_16x16x32_bf16 v[232:235], a[196:199], v[72:75], v[232:235]\n" \
"v_mfma_f32_16x16x32_bf16 v[236:239], a[196:199], v[96:99], v[236:239]\n" \
"ds_read_b64_tr_b16 a[196:197], v35 offset:52384\n" \
"ds_read_b64_tr_b16 a[198:199], v35 offset:54688\n" \
"v_mfma_f32_16x16x32_bf16 v[244:247], a[200:203], v[96:99], v[244:247]\n" \
"buffer_load_dwordx4 v8, s[80:83], s66 offen offset:0 lds\n" \
"v_mfma_f32_16x16x32_bf16 v[240:243], a[200:203], v[72:75], v[240:243]\n" \
"s_add_u32 m0, s92, 0x0\n" \
"v_readlane_b32 s66, v11, 47\n" \
"v_mfma_f32_16x16x32_bf16 v[172:175], a[200:203], a[120:123], v[172:175]\n" \
"v_mfma_f32_16x16x32_bf16 v[168:171], a[200:203], a[104:107], v[168:171]\n" \
"ds_read_b64_tr_b16 a[200:201], v35 offset:42048\n" \
"ds_read_b64_tr_b16 a[202:203], v35 offset:44352\n" \
"v_mfma_f32_16x16x32_bf16 v[176:179], a[204:207], a[104:107], v[176:179]\n" \
"v_mfma_f32_16x16x32_bf16 v[180:183], a[204:207], a[120:123], v[180:183]\n" \
"buffer_load_dwordx4 v0, s[80:83], s73 offen offset:0 lds\n" \
"v_mfma_f32_16x16x32_bf16 v[248:251], a[204:207], v[72:75], v[248:251]\n" \
"v_mfma_f32_16x16x32_bf16 v[252:255], a[204:207], v[96:99], v[252:255]\n" \
"ds_read_b64_tr_b16 a[204:205], v35 offset:52416\n" \
"ds_read_b64_tr_b16 a[206:207], v35 offset:54720\n" \
"v_mfma_f32_16x16x32_bf16 v[112:115], a[208:211], a[108:111], v[112:115]\n" \
"v_mfma_f32_16x16x32_bf16 v[116:119], a[208:211], a[124:127], v[116:119]\n" \
"v_mfma_f32_16x16x32_bf16 v[184:187], a[208:211], v[76:79], v[184:187]\n" \
"buffer_load_dwordx4 v1, s[80:83], s73 offen offset:1024 lds\n" \
"v_mfma_f32_16x16x32_bf16 v[188:191], a[208:211], v[100:103], v[188:191]\n" \
"ds_read_b64_tr_b16 a[208:209], v35 offset:42080\n" \
"ds_read_b64_tr_b16 a[210:211], v35 offset:44384\n" \
"v_mfma_f32_16x16x32_bf16 v[196:199], a[212:215], v[100:103], v[196:199]\n" \
"v_mfma_f32_16x16x32_bf16 v[192:195], a[212:215], v[76:79], v[192:195]\n" \
"s_waitcnt vmcnt(18)\n" \
"s_mov_b64 exec, 1\n" \
"buffer_store_dword v39, off, s[56:59], s70 offset:0 sc1\n" \
"s_mov_b64 exec, -1\n" \
"v_mfma_f32_16x16x32_bf16 v[124:127], a[212:215], a[124:127], v[124:127]\n" \
"v_readlane_b32 s70, v11, 51\n" \
"v_mfma_f32_16x16x32_bf16 v[120:123], a[212:215], a[108:111], v[120:123]\n" \
"ds_read_b64_tr_b16 a[212:213], v35 offset:52448\n" \
"ds_read_b64_tr_b16 a[214:215], v35 offset:54752\n" \
"v_mfma_f32_16x16x32_bf16 v[128:131], a[216:219], a[108:111], v[128:131]\n" \
"v_mfma_f32_16x16x32_bf16 v[132:135], a[216:219], a[124:127], v[132:135]\n" \
"buffer_load_dwordx4 v2, s[80:83], s73 offen offset:2048 lds\n" \
"v_mfma_f32_16x16x32_bf16 v[200:203], a[216:219], v[76:79], v[200:203]\n" \
"v_mfma_f32_16x16x32_bf16 v[204:207], a[216:219], v[100:103], v[204:207]\n" \
"ds_read_b64_tr_b16 a[216:217], v35 offset:42112\n" \
"ds_read_b64_tr_b16 a[218:219], v35 offset:44416\n" \
"v_mfma_f32_16x16x32_bf16 v[212:215], a[0:3], v[100:103], v[212:215]\n" \
"v_mfma_f32_16x16x32_bf16 v[208:211], a[0:3], v[76:79], v[208:211]\n" \
"v_mfma_f32_16x16x32_bf16 v[140:143], a[0:3], a[124:127], v[140:143]\n" \
"buffer_load_dwordx4 v3, s[80:83], s73 offen offset:3072 lds\n" \
"v_mfma_f32_16x16x32_bf16 v[136:139], a[0:3], a[108:111], v[136:139]\n" \
"ds_read_b64_tr_b16 a[0:1], v35 offset:52480\n" \
"s_waitcnt lgkmcnt(7)\n" \
"v_mfma_f32_16x16x32_bf16 v[144:147], a[4:7], a[108:111], v[144:147]\n" \
"ds_read_b64_tr_b16 a[2:3], v35 offset:54784\n" \
"s_add_u32 m0, s92, 0x1000\n" \
"v_mfma_f32_16x16x32_bf16 v[148:151], a[4:7], a[124:127], v[148:151]\n" \
"v_mfma_f32_16x16x32_bf16 v[216:219], a[4:7], v[76:79], v[216:219]\n" \
"v_mfma_f32_16x16x32_bf16 v[220:223], a[4:7], v[100:103], v[220:223]\n" \
"ds_read_b64_tr_b16 a[4:5], v35 offset:42144\n" \
"ds_read_b64_tr_b16 a[6:7], v35 offset:44448\n" \
"v_mfma_f32_16x16x32_bf16 v[228:231], a[8:11], v[100:103], v[228:231]\n" \
"buffer_load_dwordx4 v4, s[80:83], s73 offen offset:0 lds\n" \
"v_mfma_f32_16x16x32_bf16 v[224:227], a[8:11], v[76:79], v[224:227]\n" \
"v_mfma_f32_16x16x32_bf16 v[156:159], a[8:11], a[124:127], v[156:159]\n" \
"v_mfma_f32_16x16x32_bf16 v[152:155], a[8:11], a[108:111], v[152:155]\n" \
"ds_read_b64_tr_b16 a[8:9], v35 offset:52512\n" \
"ds_read_b64_tr_b16 a[10:11], v35 offset:54816\n" \
"v_mfma_f32_16x16x32_bf16 v[160:163], a[12:15], a[108:111], v[160:163]\n" \
"v_mfma_f32_16x16x32_bf16 v[164:167], a[12:15], a[124:127], v[164:167]\n" \
"buffer_load_dwordx4 v5, s[80:83], s73 offen offset:1024 lds\n" \
"v_mfma_f32_16x16x32_bf16 v[232:235], a[12:15], v[76:79], v[232:235]\n" \
"v_mfma_f32_16x16x32_bf16 v[236:239], a[12:15], v[100:103], v[236:239]\n" \
"ds_read_b64_tr_b16 a[12:13], v35 offset:42176\n" \
"ds_read_b64_tr_b16 a[14:15], v35 offset:44480\n" \
"v_mfma_f32_16x16x32_bf16 v[244:247], a[16:19], v[100:103], v[244:247]\n" \
"v_mfma_f32_16x16x32_bf16 v[240:243], a[16:19], v[76:79], v[240:243]\n" \
"v_mfma_f32_16x16x32_bf16 v[172:175], a[16:19], a[124:127], v[172:175]\n" \
"buffer_load_dwordx4 v6, s[80:83], s73 offen offset:2048 lds\n" \
"v_mfma_f32_16x16x32_bf16 v[168:171], a[16:19], a[108:111], v[168:171]\n" \
"ds_read_b64_tr_b16 a[16:17], v35 offset:52544\n" \
"ds_read_b64_tr_b16 a[18:19], v35 offset:54848\n" \
"v_mfma_f32_16x16x32_bf16 v[176:179], a[20:23], a[108:111], v[176:179]\n" \
"v_mfma_f32_16x16x32_bf16 v[180:183], a[20:23], a[124:127], v[180:183]\n" \
"v_mfma_f32_16x16x32_bf16 v[248:251], a[20:23], v[76:79], v[248:251]\n" \
"v_mfma_f32_16x16x32_bf16 v[252:255], a[20:23], v[100:103], v[252:255]\n" \
"ds_read_b64_tr_b16 a[20:21], v35 offset:42208\n" \
"ds_read_b64_tr_b16 a[22:23], v35 offset:44512\n" \
"v_mfma_f32_16x16x32_bf16 v[112:115], a[24:27], a[220:223], v[112:115]\n" \
"buffer_load_dwordx4 v7, s[80:83], s73 offen offset:3072 lds\n" \
"v_mfma_f32_16x16x32_bf16 v[112:115], a[28:31], a[224:227], v[112:115]\n" \
"s_add_u32 m0, s92, 0x2000\n" \
"v_readlane_b32 s92, v11, 46\n" \
"v_mfma_f32_16x16x32_bf16 v[116:119], a[24:27], a[228:231], v[116:119]\n" \
"v_mfma_f32_16x16x32_bf16 v[116:119], a[28:31], a[232:235], v[116:119]\n" \
"v_mfma_f32_16x16x32_bf16 v[184:187], a[24:27], a[236:239], v[184:187]\n" \
"v_mfma_f32_16x16x32_bf16 v[184:187], a[28:31], a[240:243], v[184:187]\n" \
"buffer_load_dwordx4 v8, s[80:83], s73 offen offset:0 lds\n" \
"v_mfma_f32_16x16x32_bf16 v[188:191], a[24:27], a[244:247], v[188:191]\n" \
"ds_read_b64_tr_b16 a[24:25], v35 offset:52576\n" \
"ds_read_b64_tr_b16 a[26:27], v35 offset:54880\n" \
"v_mfma_f32_16x16x32_bf16 v[188:191], a[28:31], a[248:251], v[188:191]\n" \
"s_mov_b32 m0, s94\n" \
"ds_read_b64_tr_b16 a[28:29], v35 offset:42240\n" \
"v_mfma_f32_16x16x32_bf16 v[196:199], a[192:195], a[244:247], v[196:199]\n" \
"ds_read_b64_tr_b16 a[30:31], v35 offset:44544\n" \
"v_readlane_b32 s73, v11, 6\n" \
"v_mfma_f32_16x16x32_bf16 v[196:199], a[196:199], a[248:251], v[196:199]\n" \
"v_mfma_f32_16x16x32_bf16 v[192:195], a[192:195], a[236:239], v[192:195]\n" \
"buffer_load_dwordx4 v9, s[84:87], 0 offen lds\n" \
"v_mfma_f32_16x16x32_bf16 v[192:195], a[196:199], a[240:243], v[192:195]\n" \
"v_mfma_f32_16x16x32_bf16 v[124:127], a[192:195], a[228:231], v[124:127]\n" \
"v_mfma_f32_16x16x32_bf16 v[124:127], a[196:199], a[232:235], v[124:127]\n" \
"v_mfma_f32_16x16x32_bf16 v[120:123], a[192:195], a[220:223], v[120:123]\n" \
"ds_read_b64_tr_b16 a[192:193], v35 offset:52608\n" \
"ds_read_b64_tr_b16 a[194:195], v35 offset:54912\n" \
"v_mfma_f32_16x16x32_bf16 v[120:123], a[196:199], a[224:227], v[120:123]\n" \
"ds_read_b64_tr_b16 a[196:197], v33 offset:0\n" \
"ds_read_b64_tr_b16 a[198:199], v33 offset:4608\n" \
"v_mfma_f32_16x16x32_bf16 v[128:131], a[200:203], a[220:223], v[128:131]\n" \
"buffer_load_dwordx4 a[128:131], v20, s[56:59], s68 offen offset:0 sc1\n" \
"v_mfma_f32_16x16x32_bf16 v[128:131], a[204:207], a[224:227], v[128:131]\n" \
"v_mfma_f32_16x16x32_bf16 v[132:135], a[200:203], a[228:231], v[132:135]\n" \
"v_mfma_f32_16x16x32_bf16 v[132:135], a[204:207], a[232:235], v[132:135]\n" \
"v_mfma_f32_16x16x32_bf16 v[200:203], a[200:203], a[236:239], v[200:203]\n" \
"v_mfma_f32_16x16x32_bf16 v[200:203], a[204:207], a[240:243], v[200:203]\n" \
"buffer_load_dwordx4 a[132:135], v20, s[56:59], s68 offen offset:1024 sc1\n" \
"v_mfma_f32_16x16x32_bf16 v[204:207], a[200:203], a[244:247], v[204:207]\n" \
"ds_read_b64_tr_b16 a[200:201], v33 offset:9216\n" \
"ds_read_b64_tr_b16 a[202:203], v33 offset:13824\n" \
"v_mfma_f32_16x16x32_bf16 v[204:207], a[204:207], a[248:251], v[204:207]\n" \
"ds_read_b64_tr_b16 a[204:205], v33 offset:32\n" \
"s_waitcnt lgkmcnt(7)\n" \
"v_mfma_f32_16x16x32_bf16 v[212:215], a[208:211], a[244:247], v[212:215]\n" \
"ds_read_b64_tr_b16 a[206:207], v33 offset:4640\n" \
"v_mfma_f32_16x16x32_bf16 v[212:215], a[212:215], a[248:251], v[212:215]\n" \
"v_mfma_f32_16x16x32_bf16 v[208:211], a[208:211], a[236:239], v[208:211]\n" \
"buffer_load_dword v47, v38, s[56:59], s71 offen sc1\n" \
"v_mfma_f32_16x16x32_bf16 v[208:211], a[212:215], a[240:243], v[208:211]\n" \
"v_mfma_f32_16x16x32_bf16 v[140:143], a[208:211], a[228:231], v[140:143]\n" \
"v_mfma_f32_16x16x32_bf16 v[140:143], a[212:215], a[232:235], v[140:143]\n" \
"v_mfma_f32_16x16x32_bf16 v[136:139], a[208:211], a[220:223], v[136:139]\n" \
"ds_read_b64_tr_b16 a[208:209], v33 offset:9248\n" \
"ds_read_b64_tr_b16 a[210:211], v33 offset:13856\n" \
"v_mfma_f32_16x16x32_bf16 v[136:139], a[212:215], a[224:227], v[136:139]\n" \
"ds_read_b64_tr_b16 a[212:213], v33 offset:64\n" \
"ds_read_b64_tr_b16 a[214:215], v33 offset:4672\n" \
"v_mfma_f32_16x16x32_bf16 v[144:147], a[216:219], a[220:223], v[144:147]\n" \
"buffer_load_dwordx4 a[144:147], v32, s[56:59], s68 offen offset:0 sc1\n" \
"v_mfma_f32_16x16x32_bf16 v[144:147], a[0:3], a[224:227], v[144:147]\n" \
"v_mfma_f32_16x16x32_bf16 v[148:151], a[216:219], a[228:231], v[148:151]\n" \
"v_mfma_f32_16x16x32_bf16 v[148:151], a[0:3], a[232:235], v[148:151]\n" \
"v_mfma_f32_16x16x32_bf16 v[216:219], a[216:219], a[236:239], v[216:219]\n" \
"v_mfma_f32_16x16x32_bf16 v[216:219], a[0:3], a[240:243], v[216:219]\n" \
"buffer_load_dwordx4 a[148:151], v32, s[56:59], s68 offen offset:1024 sc1\n" \
"v_mfma_f32_16x16x32_bf16 v[220:223], a[216:219], a[244:247], v[220:223]\n" \
"ds_read_b64_tr_b16 a[216:217], v33 offset:9280\n" \
"ds_read_b64_tr_b16 a[218:219], v33 offset:13888\n" \
"v_mfma_f32_16x16x32_bf16 v[220:223], a[0:3], a[248:251], v[220:223]\n" \
"ds_read_b64_tr_b16 a[0:1], v33 offset:96\n" \
"ds_read_b64_tr_b16 a[2:3], v33 offset:4704\n" \
"v_mfma_f32_16x16x32_bf16 v[228:231], a[4:7], a[244:247], v[228:231]\n" \
"v_mfma_f32_16x16x32_bf16 v[228:231], a[8:11], a[248:251], v[228:231]\n" \
"v_mfma_f32_16x16x32_bf16 v[224:227], a[4:7], a[236:239], v[224:227]\n" \
"buffer_load_dwordx4 a[152:155], v32, s[56:59], s68 offen offset:2048 sc1\n" \
"v_mfma_f32_16x16x32_bf16 v[224:227], a[8:11], a[240:243], v[224:227]\n" \
"v_mfma_f32_16x16x32_bf16 v[156:159], a[4:7], a[228:231], v[156:159]\n" \
"v_mfma_f32_16x16x32_bf16 v[156:159], a[8:11], a[232:235], v[156:159]\n" \
"v_mfma_f32_16x16x32_bf16 v[152:155], a[4:7], a[220:223], v[152:155]\n" \
"ds_read_b64_tr_b16 a[4:5], v33 offset:9312\n" \
"ds_read_b64_tr_b16 a[6:7], v33 offset:13920\n" \
"v_mfma_f32_16x16x32_bf16 v[152:155], a[8:11], a[224:227], v[152:155]\n" \
"ds_read_b64_tr_b16 a[8:9], v33 offset:128\n" \
"ds_read_b64_tr_b16 a[10:11], v33 offset:4736\n" \
"v_mfma_f32_16x16x32_bf16 v[160:163], a[12:15], a[220:223], v[160:163]\n" \
"buffer_load_dwordx4 a[156:159], v32, s[56:59], s68 offen offset:3072 sc1\n" \
"v_mfma_f32_16x16x32_bf16 v[160:163], a[16:19], a[224:227], v[160:163]\n" \
"v_mfma_f32_16x16x32_bf16 v[164:167], a[12:15], a[228:231], v[164:167]\n" \
"v_mfma_f32_16x16x32_bf16 v[164:167], a[16:19], a[232:235], v[164:167]\n" \
"v_mfma_f32_16x16x32_bf16 v[232:235], a[12:15], a[236:239], v[232:235]\n" \
"v_mfma_f32_16x16x32_bf16 v[232:235], a[16:19], a[240:243], v[232:235]\n" \
"buffer_load_dwordx4 a[136:139], v20, s[56:59], s68 offen offset:2048 sc1\n" \
"v_mfma_f32_16x16x32_bf16 v[236:239], a[12:15], a[244:247], v[236:239]\n" \
"ds_read_b64_tr_b16 a[12:13], v33 offset:9344\n" \
"ds_read_b64_tr_b16 a[14:15], v33 offset:13952\n" \
"v_mfma_f32_16x16x32_bf16 v[236:239], a[16:19], a[248:251], v[236:239]\n" \
"ds_read_b64_tr_b16 a[16:17], v33 offset:160\n" \
"ds_read_b64_tr_b16 a[18:19], v33 offset:4768\n" \
"v_mfma_f32_16x16x32_bf16 v[244:247], a[20:23], a[244:247], v[244:247]\n" \
"v_mfma_f32_16x16x32_bf16 v[244:247], a[24:27], a[248:251], v[244:247]\n" \
"v_mfma_f32_16x16x32_bf16 v[240:243], a[20:23], a[236:239], v[240:243]\n" \
"buffer_load_dwordx4 a[140:143], v20, s[56:59], s68 offen offset:3072 sc1\n" \
"v_mfma_f32_16x16x32_bf16 v[240:243], a[24:27], a[240:243], v[240:243]\n" \
"v_readlane_b32 s68, v11, 49\n" \
"v_mfma_f32_16x16x32_bf16 v[172:175], a[20:23], a[228:231], v[172:175]\n" \
"v_mfma_f32_16x16x32_bf16 v[172:175], a[24:27], a[232:235], v[172:175]\n" \
"v_mfma_f32_16x16x32_bf16 v[168:171], a[20:23], a[220:223], v[168:171]\n" \
"ds_read_b64_tr_b16 a[20:21], v33 offset:9376\n" \
"ds_read_b64_tr_b16 a[22:23], v33 offset:13984\n" \
"v_mfma_f32_16x16x32_bf16 v[168:171], a[24:27], a[224:227], v[168:171]\n" \
"ds_read_b64_tr_b16 a[24:25], v33 offset:192\n" \
"ds_read_b64_tr_b16 a[26:27], v33 offset:4800\n" \
"v_mfma_f32_16x16x32_bf16 v[176:179], a[28:31], a[220:223], v[176:179]\n" \
"s_waitcnt lgkmcnt(8)\n" \
"v_mfma_f32_16x16x32_bf16 v[176:179], a[192:195], a[224:227], v[176:179]\n" \
"buffer_load_dwordx4 a[32:35], v20, s[56:59], s69 offen offset:0 sc1\n" \
"v_mfma_f32_16x16x32_bf16 v[180:183], a[28:31], a[228:231], v[180:183]\n" \
"v_mfma_f32_16x16x32_bf16 v[180:183], a[192:195], a[232:235], v[180:183]\n" \
"v_mfma_f32_16x16x32_bf16 v[248:251], a[28:31], a[236:239], v[248:251]\n" \
"v_mfma_f32_16x16x32_bf16 v[248:251], a[192:195], a[240:243], v[248:251]\n" \
"v_mfma_f32_16x16x32_bf16 v[252:255], a[28:31], a[244:247], v[252:255]\n" \
"ds_read_b64_tr_b16 a[28:29], v33 offset:9408\n" \
"ds_read_b64_tr_b16 a[30:31], v33 offset:14016\n" \
"v_mfma_f32_16x16x32_bf16 v[252:255], a[192:195], a[248:251], v[252:255]\n" \
"ds_read_b64_tr_b16 a[192:193], v33 offset:224\n" \
"s_waitcnt vmcnt(18)\n" \
"v_mfma_f32_16x16x32_bf16 v[112:115], a[196:199], a[64:67], v[112:115]\n" \
"buffer_load_dwordx4 a[36:39], v20, s[56:59], s69 offen offset:1024 sc1\n" \
"v_mfma_f32_16x16x32_bf16 v[112:115], a[200:203], a[68:71], v[112:115]\n" \
"ds_read_b64_tr_b16 a[194:195], v33 offset:4832\n" \
"v_mfma_f32_16x16x32_bf16 v[116:119], a[196:199], a[80:83], v[116:119]\n" \
"ds_read_b64_tr_b16 a[196:197], v33 offset:9440\n" \
"ds_read_b64_tr_b16 a[198:199], v33 offset:14048\n" \
"v_mfma_f32_16x16x32_bf16 v[116:119], a[200:203], a[84:87], v[116:119]\n" \
"ds_read_b64_tr_b16 a[200:201], v33 offset:256\n" \
"ds_read_b64_tr_b16 a[202:203], v33 offset:4864\n" \
"v_mfma_f32_16x16x32_bf16 v[124:127], a[204:207], a[80:83], v[124:127]\n" \
"v_mfma_f32_16x16x32_bf16 v[124:127], a[208:211], a[84:87], v[124:127]\n" \
"buffer_load_dwordx4 a[48:51], v32, s[56:59], s69 offen offset:0 sc1\n" \
"v_mfma_f32_16x16x32_bf16 v[120:123], a[204:207], a[64:67], v[120:123]\n" \
"ds_read_b64_tr_b16 a[204:205], v33 offset:9472\n" \
"ds_read_b64_tr_b16 a[206:207], v33 offset:14080\n" \
"v_mfma_f32_16x16x32_bf16 v[120:123], a[208:211], a[68:71], v[120:123]\n" \
"ds_read_b64_tr_b16 a[208:209], v33 offset:18432\n" \
"ds_read_b64_tr_b16 a[210:211], v33 offset:23040\n" \
"v_mfma_f32_16x16x32_bf16 v[128:131], a[212:215], a[64:67], v[128:131]\n" \
"v_mfma_f32_16x16x32_bf16 v[128:131], a[216:219], a[68:71], v[128:131]\n" \
"v_mfma_f32_16x16x32_bf16 v[132:135], a[212:215], a[80:83], v[132:135]\n" \
"buffer_load_dwordx4 a[52:55], v32, s[56:59], s69 offen offset:1024 sc1\n" \
"v_mfma_f32_16x16x32_bf16 v[132:135], a[216:219], a[84:87], v[132:135]\n" \
"ds_read_b64_tr_b16 a[212:213], v33 offset:27648\n" \
"ds_read_b64_tr_b16 a[214:215], v33 offset:32256\n" \
"v_mfma_f32_16x16x32_bf16 v[140:143], a[0:3], a[80:83], v[140:143]\n" \
"ds_read_b64_tr_b16 a[216:217], v33 offset:18464\n" \
"ds_read_b64_tr_b16 a[218:219], v33 offset:23072\n" \
"v_mfma_f32_16x16x32_bf16 v[140:143], a[4:7], a[84:87], v[140:143]\n" \
"s_waitcnt vmcnt(10)\n" \
"v_cmp_ne_u32_e32 vcc, s62, v47\n" \
"v_mfma_f32_16x16x32_bf16 v[136:139], a[0:3], a[64:67], v[136:139]\n" \
"ds_read_b64_tr_b16 a[0:1], v33 offset:27680\n" \
".fill (L_ab + 36864 - .) / 4, 4, 0xBF800000\n" \
"L_ap9:\n" \
"s_cbranch_execz L_ap0\n" \
"ds_read_b64_tr_b16 a[2:3], v33 offset:32288\n" \
"v_mfma_f32_16x16x32_bf16 v[136:139], a[4:7], a[68:71], v[136:139]\n" \
"buffer_load_dwordx4 a[56:59], v32, s[56:59], s69 offen offset:2048 sc1\n" \
"v_mfma_f32_16x16x32_bf16 v[144:147], a[8:11], a[64:67], v[144:147]\n" \
"ds_read_b64_tr_b16 a[4:5], v33 offset:18496\n" \
"s_waitcnt lgkmcnt(13)\n" \
"v_mfma_f32_16x16x32_bf16 v[144:147], a[12:15], a[68:71], v[144:147]\n" \
"ds_read_b64_tr_b16 a[6:7], v33 offset:23104\n" \
"s_cbranch_vccnz L_sb_%=\n" \
"L_rb_%=:\n" \
"v_mfma_f32_16x16x32_bf16 v[148:151], a[8:11], a[80:83], v[148:151]\n" \
"ds_read_b64_tr_b16 a[8:9], v33 offset:27712\n" \
"ds_read_b64_tr_b16 a[10:11], v33 offset:32320\n" \
"v_mfma_f32_16x16x32_bf16 v[148:151], a[12:15], a[84:87], v[148:151]\n" \
"ds_read_b64_tr_b16 a[12:13], v33 offset:18528\n" \
"ds_read_b64_tr_b16 a[14:15], v33 offset:23136\n" \
"buffer_load_dwordx4 a[60:63], v32, s[56:59], s69 offen offset:3072 sc1\n" \
"v_mfma_f32_16x16x32_bf16 v[156:159], a[16:19], a[80:83], v[156:159]\n" \
"v_readlane_b32 s71, v11, 52\n" \
"v_cmp_eq_u32_e32 vcc, 0, v44\n" \
"v_mfma_f32_16x16x32_bf16 v[156:159], a[20:23], a[84:87], v[156:159]\n" \
"v_mfma_f32_16x16x32_bf16 v[152:155], a[16:19], a[64:67], v[152:155]\n" \
"ds_read_b64_tr_b16 a[16:17], v33 offset:27744\n" \
"ds_read_b64_tr_b16 a[18:19], v33 offset:32352\n" \
"v_mfma_f32_16x16x32_bf16 v[152:155], a[20:23], a[68:71], v[152:155]\n" \
"ds_read_b64_tr_b16 a[20:21], v33 offset:18560\n" \
"ds_read_b64_tr_b16 a[22:23], v33 offset:23168\n" \
"buffer_load_dwordx4 a[40:43], v20, s[56:59], s69 offen offset:2048 sc1\n" \
"v_mfma_f32_16x16x32_bf16 v[160:163], a[24:27], a[64:67], v[160:163]\n" \
"v_cndmask_b32_e32 v50, 0, v45, vcc\n" \
"v_cmp_eq_u32_e32 vcc, 1, v44\n" \
"v_mfma_f32_16x16x32_bf16 v[160:163], a[28:31], a[68:71], v[160:163]\n" \
"v_and_b32_e32 v44, 0x3c0, v20\n" \
"v_cndmask_b32_e32 v51, 0, v45, vcc\n" \
"v_mfma_f32_16x16x32_bf16 v[164:167], a[24:27], a[80:83], v[164:167]\n" \
"ds_read_b64_tr_b16 a[24:25], v33 offset:27776\n" \
"ds_read_b64_tr_b16 a[26:27], v33 offset:32384\n" \
"v_mfma_f32_16x16x32_bf16 v[164:167], a[28:31], a[84:87], v[164:167]\n" \
"ds_read_b64_tr_b16 a[28:29], v33 offset:18592\n" \
"ds_read_b64_tr_b16 a[30:31], v33 offset:23200\n" \
"buffer_load_dwordx4 a[44:47], v20, s[56:59], s69 offen offset:3072 sc1\n" \
"v_mfma_f32_16x16x32_bf16 v[172:175], a[192:195], a[80:83], v[172:175]\n" \
"v_readlane_b32 s69, v11, 50\n" \
"v_bfe_u32 v45, v20, 1, 5\n" \
"v_mfma_f32_16x16x32_bf16 v[172:175], a[196:199], a[84:87], v[172:175]\n" \
"v_and_b32_e32 v45, 24, v45\n" \
"v_add_u32_e32 v45, 0x4800, v45\n" \
"v_mfma_f32_16x16x32_bf16 v[168:171], a[192:195], a[64:67], v[168:171]\n" \
"ds_read_b64_tr_b16 a[192:193], v33 offset:27808\n" \
"ds_read_b64_tr_b16 a[194:195], v33 offset:32416\n" \
"v_mfma_f32_16x16x32_bf16 v[168:171], a[196:199], a[68:71], v[168:171]\n" \
"ds_read_b64_tr_b16 a[196:197], v33 offset:18624\n" \
"s_waitcnt lgkmcnt(13)\n" \
"v_mfma_f32_16x16x32_bf16 v[176:179], a[200:203], a[64:67], v[176:179]\n" \
"ds_read_b64_tr_b16 a[198:199], v33 offset:23232\n" \
"v_add_u32_e32 v36, v44, v45\n" \
"v_mfma_f32_16x16x32_bf16 v[176:179], a[204:207], a[68:71], v[176:179]\n" \
"buffer_load_dwordx4 a[220:223], v20, s[56:59], s64 offen offset:0 sc1\n" \
"v_mfma_f32_16x16x32_bf16 v[180:183], a[200:203], a[80:83], v[180:183]\n" \
"ds_read_b64_tr_b16 a[200:201], v33 offset:27840\n" \
"ds_read_b64_tr_b16 a[202:203], v33 offset:32448\n" \
"v_mfma_f32_16x16x32_bf16 v[180:183], a[204:207], a[84:87], v[180:183]\n" \
"ds_read_b64_tr_b16 a[204:205], v33 offset:18656\n" \
"ds_read_b64_tr_b16 a[206:207], v33 offset:23264\n" \
"v_mfma_f32_16x16x32_bf16 v[112:115], a[208:211], a[72:75], v[112:115]\n" \
"v_mfma_f32_16x16x32_bf16 v[112:115], a[212:215], a[76:79], v[112:115]\n" \
"v_mfma_f32_16x16x32_bf16 v[116:119], a[208:211], a[88:91], v[116:119]\n" \
"ds_read_b64_tr_b16 a[208:209], v33 offset:27872\n" \
"ds_read_b64_tr_b16 a[210:211], v33 offset:32480\n" \
"v_mfma_f32_16x16x32_bf16 v[116:119], a[212:215], a[92:95], v[116:119]\n" \
"ds_read_b64_tr_b16 a[212:213], v33 offset:18688\n" \
"ds_read_b64_tr_b16 a[214:215], v33 offset:23296\n" \
"v_mfma_f32_16x16x32_bf16 v[124:127], a[216:219], a[88:91], v[124:127]\n" \
"buffer_load_dwordx4 a[224:227], v20, s[56:59], s64 offen offset:1024 sc1\n" \
"v_mfma_f32_16x16x32_bf16 v[124:127], a[0:3], a[92:95], v[124:127]\n" \
"v_mfma_f32_16x16x32_bf16 v[120:123], a[216:219], a[72:75], v[120:123]\n" \
"ds_read_b64_tr_b16 a[216:217], v33 offset:27904\n" \
"ds_read_b64_tr_b16 a[218:219], v33 offset:32512\n" \
"v_mfma_f32_16x16x32_bf16 v[120:123], a[0:3], a[76:79], v[120:123]\n" \
"s_barrier\n" \
"v_mfma_f32_16x16x32_bf16 v[128:131], a[4:7], a[72:75], v[128:131]\n" \
"ds_read_b64_tr_b16 a[0:1], v33 offset:36864\n" \
"ds_read_b64_tr_b16 a[2:3], v33 offset:41472\n" \
"v_mfma_f32_16x16x32_bf16 v[128:131], a[8:11], a[76:79], v[128:131]\n" \
"buffer_load_dwordx4 a[236:239], v32, s[56:59], s64 offen offset:0 sc1\n" \
"v_mfma_f32_16x16x32_bf16 v[132:135], a[4:7], a[88:91], v[132:135]\n" \
"ds_read_b64_tr_b16 a[4:5], v33 offset:46080\n" \
"ds_read_b64_tr_b16 a[6:7], v33 offset:50688\n" \
"v_mfma_f32_16x16x32_bf16 v[132:135], a[8:11], a[92:95], v[132:135]\n" \
"ds_read_b64_tr_b16 a[8:9], v33 offset:55296\n" \
"s_waitcnt lgkmcnt(15)\n" \
"v_mfma_f32_16x16x32_bf16 v[140:143], a[12:15], a[88:91], v[140:143]\n" \
"ds_read_b64_tr_b16 a[10:11], v33 offset:59904\n" \
"v_mfma_f32_16x16x32_bf16 v[140:143], a[16:19], a[92:95], v[140:143]\n" \
"v_mfma_f32_16x16x32_bf16 v[136:139], a[12:15], a[72:75], v[136:139]\n" \
"ds_read_b64_tr_b16 a[12:13], v10 offset:0\n" \
"ds_read_b64_tr_b16 a[14:15], v10 offset:4608\n" \
"v_mfma_f32_16x16x32_bf16 v[136:139], a[16:19], a[76:79], v[136:139]\n" \
"ds_read_b64_tr_b16 a[16:17], v34 offset:0\n" \
"ds_read_b64_tr_b16 a[18:19], v34 offset:4608\n" \
"v_mfma_f32_16x16x32_bf16 v[144:147], a[20:23], a[72:75], v[144:147]\n" \
"buffer_load_dwordx4 a[240:243], v32, s[56:59], s64 offen offset:1024 sc1\n" \
"v_mfma_f32_16x16x32_bf16 v[144:147], a[24:27], a[76:79], v[144:147]\n" \
"v_mfma_f32_16x16x32_bf16 v[148:151], a[20:23], a[88:91], v[148:151]\n" \
"ds_read_b64_tr_b16 a[20:21], v34 offset:9216\n" \
"ds_read_b64_tr_b16 a[22:23], v34 offset:13824\n" \
"v_mfma_f32_16x16x32_bf16 v[148:151], a[24:27], a[92:95], v[148:151]\n" \
"ds_read_b64_tr_b16 a[24:25], v34 offset:18432\n" \
"ds_read_b64_tr_b16 a[26:27], v34 offset:23040\n" \
"v_mfma_f32_16x16x32_bf16 v[156:159], a[28:31], a[88:91], v[156:159]\n" \
"v_mfma_f32_16x16x32_bf16 v[156:159], a[192:195], a[92:95], v[156:159]\n" \
"buffer_load_dwordx4 a[244:247], v32, s[56:59], s64 offen offset:2048 sc1\n" \
"v_mfma_f32_16x16x32_bf16 v[152:155], a[28:31], a[72:75], v[152:155]\n" \
"ds_read_b64_tr_b16 a[28:29], v10 offset:9216\n" \
"ds_read_b64_tr_b16 a[30:31], v10 offset:13824\n" \
"v_mfma_f32_16x16x32_bf16 v[152:155], a[192:195], a[76:79], v[152:155]\n" \
"ds_read_b64_tr_b16 a[192:193], v33 offset:36896\n" \
"ds_read_b64_tr_b16 a[194:195], v33 offset:41504\n" \
"v_mfma_f32_16x16x32_bf16 v[160:163], a[196:199], a[72:75], v[160:163]\n" \
"s_waitcnt lgkmcnt(15)\n" \
"v_mfma_f32_16x16x32_bf16 v[160:163], a[200:203], a[76:79], v[160:163]\n" \
"v_mfma_f32_16x16x32_bf16 v[164:167], a[196:199], a[88:91], v[164:167]\n" \
"ds_read_b64_tr_b16 a[196:197], v33 offset:46112\n" \
"ds_read_b64_tr_b16 a[198:199], v33 offset:50720\n" \
"v_mfma_f32_16x16x32_bf16 v[164:167], a[200:203], a[92:95], v[164:167]\n" \
"ds_read_b64_tr_b16 a[200:201], v33 offset:55328\n" \
"ds_read_b64_tr_b16 a[202:203], v33 offset:59936\n" \
"v_mfma_f32_16x16x32_bf16 v[172:175], a[204:207], a[88:91], v[172:175]\n" \
"buffer_load_dwordx4 a[248:251], v32, s[56:59], s64 offen offset:3072 sc1\n" \
"v_mfma_f32_16x16x32_bf16 v[172:175], a[208:211], a[92:95], v[172:175]\n" \
"v_mfma_f32_16x16x32_bf16 v[168:171], a[204:207], a[72:75], v[168:171]\n" \
"ds_read_b64_tr_b16 a[204:205], v10 offset:32\n" \
"ds_read_b64_tr_b16 a[206:207], v10 offset:4640\n" \
"v_mfma_f32_16x16x32_bf16 v[168:171], a[208:211], a[76:79], v[168:171]\n" \
"ds_read_b64_tr_b16 a[208:209], v34 offset:32\n" \
"ds_read_b64_tr_b16 a[210:211], v34 offset:4640\n" \
"v_mfma_f32_16x16x32_bf16 v[176:179], a[212:215], a[72:75], v[176:179]\n" \
"v_mfma_f32_16x16x32_bf16 v[176:179], a[216:219], a[76:79], v[176:179]\n" \
"buffer_load_dwordx4 a[228:231], v20, s[56:59], s64 offen offset:2048 sc1\n" \
"v_mfma_f32_16x16x32_bf16 v[180:183], a[212:215], a[88:91], v[180:183]\n" \
"ds_read_b64_tr_b16 a[212:213], v34 offset:9248\n" \
"ds_read_b64_tr_b16 a[214:215], v34 offset:13856\n" \
"v_mfma_f32_16x16x32_bf16 v[180:183], a[216:219], a[92:95], v[180:183]\n" \
"ds_read_b64_tr_b16 a[216:217], v34 offset:18464\n" \
"ds_read_b64_tr_b16 a[218:219], v34 offset:23072\n" \
"v_mfma_f32_16x16x32_bf16 v[112:115], a[0:3], a[128:131], v[112:115]\n" \
"s_waitcnt lgkmcnt(15)\n" \
"v_mfma_f32_16x16x32_bf16 v[112:115], a[4:7], a[132:135], v[112:115]\n" \
"s_waitcnt vmcnt(18)\n" \
"v_mfma_f32_16x16x32_bf16 v[116:119], a[0:3], a[144:147], v[116:119]\n" \
"ds_read_b64_tr_b16 a[0:1], v10 offset:9248\n" \
"ds_read_b64_tr_b16 a[2:3], v10 offset:13856\n" \
"v_mfma_f32_16x16x32_bf16 v[116:119], a[4:7], a[148:151], v[116:119]\n" \
"ds_read_b64_tr_b16 a[4:5], v33 offset:36928\n" \
"ds_read_b64_tr_b16 a[6:7], v33 offset:41536\n" \
"v_mfma_f32_16x16x32_bf16 v[116:119], a[8:11], a[152:155], v[116:119]\n" \
"s_waitcnt vmcnt(17)\n" \
"v_mfma_f32_16x16x32_bf16 v[116:119], a[12:15], a[156:159], v[116:119]\n" \
"s_waitcnt vmcnt(16)\n" \
"v_mfma_f32_16x16x32_bf16 v[112:115], a[8:11], a[136:139], v[112:115]\n" \
"ds_read_b64_tr_b16 a[8:9], v33 offset:46144\n" \
"s_waitcnt vmcnt(15)\n" \
"v_mfma_f32_16x16x32_bf16 v[112:115], a[12:15], a[140:143], v[112:115]\n" \
"ds_read_b64_tr_b16 a[10:11], v33 offset:50752\n" \
"s_waitcnt vmcnt(14)\n" \
"v_mfma_f32_16x16x32_bf16 v[112:115], a[16:19], a[32:35], v[112:115]\n" \
"ds_read_b64_tr_b16 a[12:13], v33 offset:55360\n" \
"s_waitcnt vmcnt(13)\n" \
"v_mfma_f32_16x16x32_bf16 v[112:115], a[20:23], a[36:39], v[112:115]\n" \
"ds_read_b64_tr_b16 a[14:15], v33 offset:59968\n" \
"s_waitcnt vmcnt(12)\n" \
"v_mfma_f32_16x16x32_bf16 v[116:119], a[16:19], a[48:51], v[116:119]\n" \
"ds_read_b64_tr_b16 a[16:17], v10 offset:64\n" \
"s_waitcnt vmcnt(11)\n" \
"v_mfma_f32_16x16x32_bf16 v[116:119], a[20:23], a[52:55], v[116:119]\n" \
"ds_read_b64_tr_b16 a[18:19], v10 offset:4672\n" \
"s_waitcnt vmcnt(10)\n" \
"v_mfma_f32_16x16x32_bf16 v[116:119], a[24:27], a[56:59], v[116:119]\n" \
"ds_read_b64_tr_b16 a[20:21], v34 offset:64\n" \
"ds_read_b64_tr_b16 a[22:23], v34 offset:4672\n" \
"buffer_load_dwordx4 a[232:235], v20, s[56:59], s64 offen offset:3072 sc1\n" \
"s_waitcnt vmcnt(10) lgkmcnt(12)\n" \
"v_mfma_f32_16x16x32_bf16 v[116:119], a[28:31], a[60:63], v[116:119]\n" \
"buffer_load_dwordx4 a[160:163], v20, s[56:59], s72 offen offset:0 sc1\n" \
"s_waitcnt vmcnt(10)\n" \
"v_mfma_f32_16x16x32_bf16 v[112:115], a[24:27], a[40:43], v[112:115]\n" \
"ds_read_b64_tr_b16 a[24:25], v34 offset:9280\n" \
"ds_read_b64_tr_b16 a[26:27], v34 offset:13888\n" \
"buffer_load_dwordx4 a[164:167], v20, s[56:59], s72 offen offset:1024 sc1\n" \
"s_waitcnt vmcnt(10)\n" \
"v_mfma_f32_16x16x32_bf16 v[112:115], a[28:31], a[44:47], v[112:115]\n" \
"ds_read_b64_tr_b16 a[28:29], v34 offset:18496\n" \
"ds_read_b64_tr_b16 a[30:31], v34 offset:23104\n" \
"v_mfma_f32_16x16x32_bf16 v[120:123], a[192:195], a[128:131], v[120:123]\n" \
"buffer_load_dwordx4 a[176:179], v32, s[56:59], s72 offen offset:0 sc1\n" \
"v_mfma_f32_16x16x32_bf16 v[120:123], a[196:199], a[132:135], v[120:123]\n" \
"v_mfma_f32_16x16x32_bf16 v[124:127], a[192:195], a[144:147], v[124:127]\n" \
"ds_read_b64_tr_b16 a[192:193], v10 offset:9280\n" \
"ds_read_b64_tr_b16 a[194:195], v10 offset:13888\n" \
"v_cvt_pk_bf16_f32 v112, v112, v113\n" \
"v_cvt_pk_bf16_f32 v113, v114, v115\n" \
"v_cvt_pk_bf16_f32 v114, v116, v117\n" \
"v_cvt_pk_bf16_f32 v115, v118, v119\n" \
"buffer_store_dwordx4 v[112:115], v20, s[56:59], s65 offen offset:0 sc1\n" \
"s_add_u32 s65, s65, 0x3800\n" \
"v_mfma_f32_16x16x32_bf16 v[124:127], a[196:199], a[148:151], v[124:127]\n" \
"ds_read_b64_tr_b16 a[196:197], v33 offset:36960\n" \
"ds_read_b64_tr_b16 a[198:199], v33 offset:41568\n" \
"v_mfma_f32_16x16x32_bf16 v[124:127], a[200:203], a[152:155], v[124:127]\n" \
"v_mfma_f32_16x16x32_bf16 v[124:127], a[204:207], a[156:159], v[124:127]\n" \
"v_mfma_f32_16x16x32_bf16 v[120:123], a[200:203], a[136:139], v[120:123]\n" \
"ds_read_b64_tr_b16 a[200:201], v33 offset:46176\n" \
"ds_read_b64_tr_b16 a[202:203], v33 offset:50784\n" \
"v_mfma_f32_16x16x32_bf16 v[120:123], a[204:207], a[140:143], v[120:123]\n" \
"ds_read_b64_tr_b16 a[204:205], v33 offset:55392\n" \
"ds_read_b64_tr_b16 a[206:207], v33 offset:60000\n" \
"v_mfma_f32_16x16x32_bf16 v[120:123], a[208:211], a[32:35], v[120:123]\n" \
"buffer_load_dwordx4 a[180:183], v32, s[56:59], s72 offen offset:1024 sc1\n" \
"v_mfma_f32_16x16x32_bf16 v[120:123], a[212:215], a[36:39], v[120:123]\n" \
"v_mfma_f32_16x16x32_bf16 v[124:127], a[208:211], a[48:51], v[124:127]\n" \
"ds_read_b64_tr_b16 a[208:209], v10 offset:96\n" \
"ds_read_b64_tr_b16 a[210:211], v10 offset:4704\n" \
"v_mfma_f32_16x16x32_bf16 v[124:127], a[212:215], a[52:55], v[124:127]\n" \
"ds_read_b64_tr_b16 a[212:213], v34 offset:96\n" \
"ds_read_b64_tr_b16 a[214:215], v34 offset:4704\n" \
"v_mfma_f32_16x16x32_bf16 v[124:127], a[216:219], a[56:59], v[124:127]\n" \
"s_waitcnt lgkmcnt(12)\n" \
"v_mfma_f32_16x16x32_bf16 v[124:127], a[0:3], a[60:63], v[124:127]\n" \
"buffer_load_dwordx4 a[168:171], v20, s[56:59], s72 offen offset:2048 sc1\n" \
"v_mfma_f32_16x16x32_bf16 v[120:123], a[216:219], a[40:43], v[120:123]\n" \
"ds_read_b64_tr_b16 a[216:217], v34 offset:9312\n" \
"ds_read_b64_tr_b16 a[218:219], v34 offset:13920\n" \
"v_mfma_f32_16x16x32_bf16 v[120:123], a[0:3], a[44:47], v[120:123]\n" \
"ds_read_b64_tr_b16 a[0:1], v34 offset:18528\n" \
"ds_read_b64_tr_b16 a[2:3], v34 offset:23136\n" \
"v_mfma_f32_16x16x32_bf16 v[128:131], a[4:7], a[128:131], v[128:131]\n" \
"v_mfma_f32_16x16x32_bf16 v[128:131], a[8:11], a[132:135], v[128:131]\n" \
"v_mfma_f32_16x16x32_bf16 v[132:135], a[4:7], a[144:147], v[132:135]\n" \
"ds_read_b64_tr_b16 a[4:5], v10 offset:9312\n" \
"ds_read_b64_tr_b16 a[6:7], v10 offset:13920\n" \
"v_mfma_f32_16x16x32_bf16 v[132:135], a[8:11], a[148:151], v[132:135]\n" \
"v_cvt_pk_bf16_f32 v120, v120, v121\n" \
"v_cvt_pk_bf16_f32 v121, v122, v123\n" \
"v_cvt_pk_bf16_f32 v122, v124, v125\n" \
"v_cvt_pk_bf16_f32 v123, v126, v127\n" \
"buffer_store_dwordx4 v[120:123], v20, s[56:59], s66 offen offset:0 sc1\n" \
"s_add_u32 s66, s66, 0x3000\n" \
"v_mfma_f32_16x16x32_bf16 v[132:135], a[12:15], a[152:155], v[132:135]\n" \
"ds_read_b64_tr_b16 a[8:9], v33 offset:36992\n" \
"ds_read_b64_tr_b16 a[10:11], v33 offset:41600\n" \
"v_mfma_f32_16x16x32_bf16 v[132:135], a[16:19], a[156:159], v[132:135]\n" \
"v_mfma_f32_16x16x32_bf16 v[128:131], a[12:15], a[136:139], v[128:131]\n" \
"ds_read_b64_tr_b16 a[12:13], v33 offset:46208\n" \
"ds_read_b64_tr_b16 a[14:15], v33 offset:50816\n" \
"v_mfma_f32_16x16x32_bf16 v[128:131], a[16:19], a[140:143], v[128:131]\n" \
"ds_read_b64_tr_b16 a[16:17], v33 offset:55424\n" \
"ds_read_b64_tr_b16 a[18:19], v33 offset:60032\n" \
"v_mfma_f32_16x16x32_bf16 v[128:131], a[20:23], a[32:35], v[128:131]\n" \
"buffer_load_dwordx4 a[184:187], v32, s[56:59], s72 offen offset:2048 sc1\n" \
"v_mfma_f32_16x16x32_bf16 v[128:131], a[24:27], a[36:39], v[128:131]\n" \
"v_mfma_f32_16x16x32_bf16 v[132:135], a[20:23], a[48:51], v[132:135]\n" \
"ds_read_b64_tr_b16 a[20:21], v10 offset:128\n" \
"ds_read_b64_tr_b16 a[22:23], v10 offset:4736\n" \
"v_mfma_f32_16x16x32_bf16 v[132:135], a[24:27], a[52:55], v[132:135]\n" \
"ds_read_b64_tr_b16 a[24:25], v34 offset:128\n" \
"ds_read_b64_tr_b16 a[26:27], v34 offset:4736\n" \
"v_mfma_f32_16x16x32_bf16 v[132:135], a[28:31], a[56:59], v[132:135]\n" \
"s_waitcnt lgkmcnt(12)\n" \
"v_mfma_f32_16x16x32_bf16 v[132:135], a[192:195], a[60:63], v[132:135]\n" \
"buffer_load_dwordx4 a[172:175], v20, s[56:59], s72 offen offset:3072 sc1\n" \
"v_mfma_f32_16x16x32_bf16 v[128:131], a[28:31], a[40:43], v[128:131]\n" \
"ds_read_b64_tr_b16 a[28:29], v34 offset:9344\n" \
"ds_read_b64_tr_b16 a[30:31], v34 offset:13952\n" \
"v_mfma_f32_16x16x32_bf16 v[128:131], a[192:195], a[44:47], v[128:131]\n" \
"ds_read_b64_tr_b16 a[192:193], v34 offset:18560\n" \
"ds_read_b64_tr_b16 a[194:195], v34 offset:23168\n" \
"v_mfma_f32_16x16x32_bf16 v[136:139], a[196:199], a[128:131], v[136:139]\n" \
"v_mfma_f32_16x16x32_bf16 v[136:139], a[200:203], a[132:135], v[136:139]\n" \
"v_mfma_f32_16x16x32_bf16 v[140:143], a[196:199], a[144:147], v[140:143]\n" \
"buffer_load_dwordx4 a[188:191], v32, s[56:59], s72 offen offset:3072 sc1\n" \
"v_mfma_f32_16x16x32_bf16 v[140:143], a[200:203], a[148:151], v[140:143]\n" \
"v_readlane_b32 s72, v11, 53\n" \
"v_cvt_pk_bf16_f32 v128, v128, v129\n" \
"v_cvt_pk_bf16_f32 v129, v130, v131\n" \
"v_cvt_pk_bf16_f32 v130, v132, v133\n" \
"v_cvt_pk_bf16_f32 v131, v134, v135\n" \
"v_mfma_f32_16x16x32_bf16 v[140:143], a[204:207], a[152:155], v[140:143]\n" \
"buffer_store_dwordx4 v[128:131], v20, s[56:59], s67 offen offset:0 sc1\n" \
"s_add_u32 s67, s67, 0x2800\n" \
"v_mfma_f32_16x16x32_bf16 v[140:143], a[208:211], a[156:159], v[140:143]\n" \
"ds_read_b64_tr_b16 a[196:197], v10 offset:9344\n" \
"ds_read_b64_tr_b16 a[198:199], v10 offset:13952\n" \
"v_mfma_f32_16x16x32_bf16 v[136:139], a[204:207], a[136:139], v[136:139]\n" \
"ds_read_b64_tr_b16 a[200:201], v33 offset:37024\n" \
"ds_read_b64_tr_b16 a[202:203], v33 offset:41632\n" \
"v_mfma_f32_16x16x32_bf16 v[136:139], a[208:211], a[140:143], v[136:139]\n" \
"ds_read_b64_tr_b16 a[204:205], v33 offset:46240\n" \
"ds_read_b64_tr_b16 a[206:207], v33 offset:50848\n" \
"v_mfma_f32_16x16x32_bf16 v[136:139], a[212:215], a[32:35], v[136:139]\n" \
"ds_read_b64_tr_b16 a[208:209], v33 offset:55456\n" \
"ds_read_b64_tr_b16 a[210:211], v33 offset:60064\n" \
"v_mfma_f32_16x16x32_bf16 v[136:139], a[216:219], a[36:39], v[136:139]\n" \
"buffer_load_dwordx4 a[96:99], v20, s[56:59], s93 offen offset:0 sc1\n" \
"v_mfma_f32_16x16x32_bf16 v[140:143], a[212:215], a[48:51], v[140:143]\n" \
"ds_read_b64_tr_b16 a[212:213], v10 offset:160\n" \
"ds_read_b64_tr_b16 a[214:215], v10 offset:4768\n" \
"v_mfma_f32_16x16x32_bf16 v[140:143], a[216:219], a[52:55], v[140:143]\n" \
"ds_read_b64_tr_b16 a[216:217], v34 offset:160\n" \
"ds_read_b64_tr_b16 a[218:219], v34 offset:4768\n" \
"v_mfma_f32_16x16x32_bf16 v[140:143], a[0:3], a[56:59], v[140:143]\n" \
"s_waitcnt lgkmcnt(13)\n" \
"v_mfma_f32_16x16x32_bf16 v[140:143], a[4:7], a[60:63], v[140:143]\n" \
"v_mfma_f32_16x16x32_bf16 v[136:139], a[0:3], a[40:43], v[136:139]\n" \
"buffer_load_dwordx4 a[100:103], v20, s[56:59], s93 offen offset:1024 sc1\n" \
"v_mfma_f32_16x16x32_bf16 v[136:139], a[4:7], a[44:47], v[136:139]\n" \
"ds_read_b64_tr_b16 a[0:1], v34 offset:9376\n" \
"ds_read_b64_tr_b16 a[2:3], v34 offset:13984\n" \
"v_mfma_f32_16x16x32_bf16 v[144:147], a[8:11], a[128:131], v[144:147]\n" \
"ds_read_b64_tr_b16 a[4:5], v34 offset:18592\n" \
"ds_read_b64_tr_b16 a[6:7], v34 offset:23200\n" \
"v_mfma_f32_16x16x32_bf16 v[144:147], a[12:15], a[132:135], v[144:147]\n" \
"v_mfma_f32_16x16x32_bf16 v[148:151], a[8:11], a[144:147], v[148:151]\n" \
"ds_read_b64_tr_b16 a[8:9], v10 offset:9376\n" \
"v_cvt_pk_bf16_f32 v136, v136, v137\n" \
"v_cvt_pk_bf16_f32 v137, v138, v139\n" \
"v_cvt_pk_bf16_f32 v138, v140, v141\n" \
"v_cvt_pk_bf16_f32 v139, v142, v143\n" \
"buffer_store_dwordx4 v[136:139], v20, s[56:59], s68 offen offset:0 sc1\n" \
"s_add_u32 s68, s68, 0x2000\n" \
"v_mfma_f32_16x16x32_bf16 v[148:151], a[12:15], a[148:151], v[148:151]\n" \
"ds_read_b64_tr_b16 a[10:11], v10 offset:13984\n" \
"ds_read_b64_tr_b16 a[12:13], v33 offset:37056\n" \
"v_mfma_f32_16x16x32_bf16 v[148:151], a[16:19], a[152:155], v[148:151]\n" \
"ds_read_b64_tr_b16 a[14:15], v33 offset:41664\n" \
"v_mfma_f32_16x16x32_bf16 v[148:151], a[20:23], a[156:159], v[148:151]\n" \
"v_mfma_f32_16x16x32_bf16 v[144:147], a[16:19], a[136:139], v[144:147]\n" \
"buffer_load_dwordx4 a[112:115], v32, s[56:59], s93 offen offset:0 sc1\n" \
"v_mfma_f32_16x16x32_bf16 v[144:147], a[20:23], a[140:143], v[144:147]\n" \
"ds_read_b64_tr_b16 a[16:17], v33 offset:46272\n" \
"ds_read_b64_tr_b16 a[18:19], v33 offset:50880\n" \
"v_mfma_f32_16x16x32_bf16 v[144:147], a[24:27], a[32:35], v[144:147]\n" \
"ds_read_b64_tr_b16 a[20:21], v33 offset:55488\n" \
"ds_read_b64_tr_b16 a[22:23], v33 offset:60096\n" \
"v_mfma_f32_16x16x32_bf16 v[144:147], a[28:31], a[36:39], v[144:147]\n" \
"v_mfma_f32_16x16x32_bf16 v[148:151], a[24:27], a[48:51], v[148:151]\n" \
"ds_read_b64_tr_b16 a[24:25], v10 offset:192\n" \
"ds_read_b64_tr_b16 a[26:27], v10 offset:4800\n" \
"buffer_load_dwordx4 a[116:119], v32, s[56:59], s93 offen offset:1024 sc1\n" \
"v_mfma_f32_16x16x32_bf16 v[148:151], a[28:31], a[52:55], v[148:151]\n" \
"ds_read_b64_tr_b16 a[28:29], v34 offset:192\n" \
"s_waitcnt lgkmcnt(13)\n" \
"v_mfma_f32_16x16x32_bf16 v[148:151], a[192:195], a[56:59], v[148:151]\n" \
"ds_read_b64_tr_b16 a[30:31], v34 offset:4800\n" \
"v_mfma_f32_16x16x32_bf16 v[148:151], a[196:199], a[60:63], v[148:151]\n" \
"v_mfma_f32_16x16x32_bf16 v[144:147], a[192:195], a[40:43], v[144:147]\n" \
"ds_read_b64_tr_b16 a[192:193], v34 offset:9408\n" \
"ds_read_b64_tr_b16 a[194:195], v34 offset:14016\n" \
"buffer_load_dwordx4 a[120:123], v32, s[56:59], s93 offen offset:2048 sc1\n" \
"v_mfma_f32_16x16x32_bf16 v[144:147], a[196:199], a[44:47], v[144:147]\n" \
"ds_read_b64_tr_b16 a[196:197], v34 offset:18624\n" \
"ds_read_b64_tr_b16 a[198:199], v34 offset:23232\n" \
"v_mfma_f32_16x16x32_bf16 v[152:155], a[200:203], a[128:131], v[152:155]\n" \
"v_mfma_f32_16x16x32_bf16 v[152:155], a[204:207], a[132:135], v[152:155]\n" \
"v_mfma_f32_16x16x32_bf16 v[156:159], a[200:203], a[144:147], v[156:159]\n" \
"ds_read_b64_tr_b16 a[200:201], v10 offset:9408\n" \
"ds_read_b64_tr_b16 a[202:203], v10 offset:14016\n" \
"buffer_load_dwordx4 a[124:127], v32, s[56:59], s93 offen offset:3072 sc1\n" \
"v_cvt_pk_bf16_f32 v144, v144, v145\n" \
"v_cvt_pk_bf16_f32 v145, v146, v147\n" \
"v_cvt_pk_bf16_f32 v146, v148, v149\n" \
"v_cvt_pk_bf16_f32 v147, v150, v151\n" \
"v_mfma_f32_16x16x32_bf16 v[156:159], a[204:207], a[148:151], v[156:159]\n" \
"ds_read_b64_tr_b16 a[204:205], v33 offset:256\n" \
"ds_read_b64_tr_b16 a[206:207], v33 offset:4864\n" \
"buffer_store_dwordx4 v[144:147], v20, s[56:59], s69 offen offset:0 sc1\n" \
"s_add_u32 s69, s69, 0x1800\n" \
"v_mfma_f32_16x16x32_bf16 v[156:159], a[208:211], a[152:155], v[156:159]\n" \
"v_mfma_f32_16x16x32_bf16 v[156:159], a[212:215], a[156:159], v[156:159]\n" \
"v_mfma_f32_16x16x32_bf16 v[152:155], a[208:211], a[136:139], v[152:155]\n" \
"ds_read_b64_tr_b16 a[208:209], v33 offset:9472\n" \
"ds_read_b64_tr_b16 a[210:211], v33 offset:14080\n" \
"v_mfma_f32_16x16x32_bf16 v[152:155], a[212:215], a[140:143], v[152:155]\n" \
"buffer_load_dwordx4 a[104:107], v20, s[56:59], s93 offen offset:2048 sc1\n" \
"v_mfma_f32_16x16x32_bf16 v[152:155], a[216:219], a[32:35], v[152:155]\n" \
"ds_read_b64_tr_b16 a[212:213], v33 offset:18688\n" \
"ds_read_b64_tr_b16 a[214:215], v33 offset:23296\n" \
"v_mfma_f32_16x16x32_bf16 v[152:155], a[0:3], a[36:39], v[152:155]\n" \
"v_mfma_f32_16x16x32_bf16 v[156:159], a[216:219], a[48:51], v[156:159]\n" \
"ds_read_b64_tr_b16 a[216:217], v33 offset:27904\n" \
"ds_read_b64_tr_b16 a[218:219], v33 offset:32512\n" \
"v_mfma_f32_16x16x32_bf16 v[156:159], a[0:3], a[52:55], v[156:159]\n" \
"ds_read_b64_tr_b16 a[0:1], v33 offset:37120\n" \
"buffer_load_dwordx4 a[108:111], v20, s[56:59], s93 offen offset:3072 sc1\n" \
"s_waitcnt lgkmcnt(11)\n" \
"v_mfma_f32_16x16x32_bf16 v[156:159], a[4:7], a[56:59], v[156:159]\n" \
"ds_read_b64_tr_b16 a[2:3], v33 offset:41728\n" \
"v_mfma_f32_16x16x32_bf16 v[156:159], a[8:11], a[60:63], v[156:159]\n" \
"v_mfma_f32_16x16x32_bf16 v[152:155], a[4:7], a[40:43], v[152:155]\n" \
"ds_read_b64_tr_b16 a[4:5], v33 offset:46336\n" \
"ds_read_b64_tr_b16 a[6:7], v33 offset:50944\n" \
"v_mfma_f32_16x16x32_bf16 v[152:155], a[8:11], a[44:47], v[152:155]\n" \
"ds_read_b64_tr_b16 a[8:9], v33 offset:55552\n" \
"ds_read_b64_tr_b16 a[10:11], v33 offset:60160\n" \
"v_mfma_f32_16x16x32_bf16 v[160:163], a[12:15], a[128:131], v[160:163]\n" \
"buffer_load_dwordx4 v[64:67], v20, s[56:59], s95 offen offset:0 sc1\n" \
"v_mfma_f32_16x16x32_bf16 v[160:163], a[16:19], a[132:135], v[160:163]\n" \
"v_mfma_f32_16x16x32_bf16 v[164:167], a[12:15], a[144:147], v[164:167]\n" \
"ds_read_b64_tr_b16 a[12:13], v10 offset:256\n" \
"ds_read_b64_tr_b16 a[14:15], v10 offset:4864\n" \
"v_cvt_pk_bf16_f32 v152, v152, v153\n" \
"v_cvt_pk_bf16_f32 v153, v154, v155\n" \
"v_cvt_pk_bf16_f32 v154, v156, v157\n" \
"v_cvt_pk_bf16_f32 v155, v158, v159\n" \
"buffer_store_dwordx4 v[152:155], v20, s[56:59], s70 offen offset:0 sc1\n" \
"s_add_u32 s70, s70, 0x1000\n" \
"v_mfma_f32_16x16x32_bf16 v[164:167], a[16:19], a[148:151], v[164:167]\n" \
"ds_read_b64_tr_b16 a[16:17], v34 offset:256\n" \
"ds_read_b64_tr_b16 a[18:19], v34 offset:4864\n" \
"v_mfma_f32_16x16x32_bf16 v[164:167], a[20:23], a[152:155], v[164:167]\n" \
"v_mfma_f32_16x16x32_bf16 v[164:167], a[24:27], a[156:159], v[164:167]\n" \
"v_mfma_f32_16x16x32_bf16 v[160:163], a[20:23], a[136:139], v[160:163]\n" \
"ds_read_b64_tr_b16 a[20:21], v34 offset:9472\n" \
"ds_read_b64_tr_b16 a[22:23], v34 offset:14080\n" \
"v_mfma_f32_16x16x32_bf16 v[160:163], a[24:27], a[140:143], v[160:163]\n" \
"ds_read_b64_tr_b16 a[24:25], v34 offset:18688\n" \
"ds_read_b64_tr_b16 a[26:27], v34 offset:23296\n" \
"s_waitcnt vmcnt(19)\n" \
"s_mov_b64 exec, 1\n" \
"buffer_store_dword v39, off, s[56:59], s65 offset:0 sc1\n" \
"s_mov_b64 exec, -1\n" \
"v_mfma_f32_16x16x32_bf16 v[160:163], a[28:31], a[32:35], v[160:163]\n" \
"v_mfma_f32_16x16x32_bf16 v[160:163], a[192:195], a[36:39], v[160:163]\n" \
"v_mfma_f32_16x16x32_bf16 v[164:167], a[28:31], a[48:51], v[164:167]\n" \
"ds_read_b64_tr_b16 a[28:29], v10 offset:9472\n" \
"ds_read_b64_tr_b16 a[30:31], v10 offset:14080\n" \
"v_mfma_f32_16x16x32_bf16 v[164:167], a[192:195], a[52:55], v[164:167]\n" \
"ds_read_b64_tr_b16 a[192:193], v33 offset:0\n" \
"ds_read_b64_tr_b16 a[194:195], v33 offset:4608\n" \
"v_mfma_f32_16x16x32_bf16 v[164:167], a[196:199], a[56:59], v[164:167]\n" \
"s_waitcnt lgkmcnt(12)\n" \
"v_mfma_f32_16x16x32_bf16 v[164:167], a[200:203], a[60:63], v[164:167]\n" \
"buffer_load_dwordx4 v[68:71], v20, s[56:59], s95 offen offset:1024 sc1\n" \
"v_mfma_f32_16x16x32_bf16 v[160:163], a[196:199], a[40:43], v[160:163]\n" \
"ds_read_b64_tr_b16 a[196:197], v33 offset:9216\n" \
"ds_read_b64_tr_b16 a[198:199], v33 offset:13824\n" \
"v_mfma_f32_16x16x32_bf16 v[160:163], a[200:203], a[44:47], v[160:163]\n" \
"ds_read_b64_tr_b16 a[200:201], v33 offset:18432\n" \
"ds_read_b64_tr_b16 a[202:203], v33 offset:23040\n" \
"v_mfma_f32_16x16x32_bf16 v[248:251], a[204:207], a[220:223], v[248:251]\n" \
"v_mfma_f32_16x16x32_bf16 v[248:251], a[208:211], a[224:227], v[248:251]\n" \
"v_mfma_f32_16x16x32_bf16 v[252:255], a[204:207], a[236:239], v[252:255]\n" \
"ds_read_b64_tr_b16 a[204:205], v33 offset:27648\n" \
"ds_read_b64_tr_b16 a[206:207], v33 offset:32256\n" \
"v_mfma_f32_16x16x32_bf16 v[252:255], a[208:211], a[240:243], v[252:255]\n" \
"v_cvt_pk_bf16_f32 v160, v160, v161\n" \
"v_cvt_pk_bf16_f32 v161, v162, v163\n" \
"v_cvt_pk_bf16_f32 v162, v164, v165\n" \
"v_cvt_pk_bf16_f32 v163, v166, v167\n" \
"buffer_store_dwordx4 v[160:163], v20, s[56:59], s71 offen offset:0 sc1\n" \
"s_add_u32 s71, s71, 0x800\n" \
"v_mfma_f32_16x16x32_bf16 v[252:255], a[212:215], a[244:247], v[252:255]\n" \
"ds_read_b64_tr_b16 a[208:209], v33 offset:36864\n" \
"ds_read_b64_tr_b16 a[210:211], v33 offset:41472\n" \
"v_mfma_f32_16x16x32_bf16 v[252:255], a[216:219], a[248:251], v[252:255]\n" \
"v_mfma_f32_16x16x32_bf16 v[248:251], a[212:215], a[228:231], v[248:251]\n" \
"ds_read_b64_tr_b16 a[212:213], v33 offset:46080\n" \
"ds_read_b64_tr_b16 a[214:215], v33 offset:50688\n" \
"s_waitcnt vmcnt(19)\n" \
"s_mov_b64 exec, 1\n" \
"buffer_store_dword v39, off, s[56:59], s66 offset:4 sc1\n" \
"s_mov_b64 exec, -1\n" \
"v_readlane_b32 s66, v11, 47\n" \
"v_mfma_f32_16x16x32_bf16 v[248:251], a[216:219], a[232:235], v[248:251]\n" \
"ds_read_b64_tr_b16 a[216:217], v33 offset:55296\n" \
"ds_read_b64_tr_b16 a[218:219], v33 offset:59904\n" \
"v_mfma_f32_16x16x32_bf16 v[176:179], a[0:3], a[128:131], v[176:179]\n" \
"v_mfma_f32_16x16x32_bf16 v[176:179], a[4:7], a[132:135], v[176:179]\n" \
"v_mfma_f32_16x16x32_bf16 v[180:183], a[0:3], a[144:147], v[180:183]\n" \
"buffer_load_dwordx4 v[72:75], v32, s[56:59], s95 offen offset:0 sc1\n" \
"v_mfma_f32_16x16x32_bf16 v[180:183], a[4:7], a[148:151], v[180:183]\n" \
"v_mfma_f32_16x16x32_bf16 v[248:251], a[0:3], a[160:163], v[248:251]\n" \
"v_mfma_f32_16x16x32_bf16 v[248:251], a[4:7], a[164:167], v[248:251]\n" \
"v_mfma_f32_16x16x32_bf16 v[252:255], a[0:3], a[176:179], v[252:255]\n" \
"ds_read_b64_tr_b16 a[0:1], v10 offset:0\n" \
"ds_read_b64_tr_b16 a[2:3], v10 offset:4608\n" \
"v_mfma_f32_16x16x32_bf16 v[252:255], a[4:7], a[180:183], v[252:255]\n" \
"ds_read_b64_tr_b16 a[4:5], v34 offset:0\n" \
"s_waitcnt vmcnt(20)\n" \
"v_mfma_f32_16x16x32_bf16 v[252:255], a[8:11], a[184:187], v[252:255]\n" \
"ds_read_b64_tr_b16 a[6:7], v34 offset:4608\n" \
"s_waitcnt vmcnt(18) lgkmcnt(10)\n" \
"v_mfma_f32_16x16x32_bf16 v[252:255], a[12:15], a[188:191], v[252:255]\n" \
"buffer_load_dwordx4 v[76:79], v32, s[56:59], s95 offen offset:1024 sc1\n" \
"v_mfma_f32_16x16x32_bf16 v[248:251], a[8:11], a[168:171], v[248:251]\n" \
"v_mfma_f32_16x16x32_bf16 v[248:251], a[12:15], a[172:175], v[248:251]\n" \
"v_mfma_f32_16x16x32_bf16 v[180:183], a[8:11], a[152:155], v[180:183]\n" \
"v_mfma_f32_16x16x32_bf16 v[180:183], a[12:15], a[156:159], v[180:183]\n" \
"v_mfma_f32_16x16x32_bf16 v[176:179], a[8:11], a[136:139], v[176:179]\n" \
"ds_read_b64_tr_b16 a[8:9], v34 offset:9216\n" \
"ds_read_b64_tr_b16 a[10:11], v34 offset:13824\n" \
"v_mfma_f32_16x16x32_bf16 v[176:179], a[12:15], a[140:143], v[176:179]\n" \
"ds_read_b64_tr_b16 a[12:13], v34 offset:18432\n" \
"ds_read_b64_tr_b16 a[14:15], v34 offset:23040\n" \
"s_waitcnt vmcnt(18)\n" \
"s_mov_b64 exec, 1\n" \
"buffer_store_dword v39, off, s[56:59], s67 offset:8 sc1\n" \
"s_mov_b64 exec, -1\n" \
"v_readlane_b32 s67, v11, 48\n" \
"v_mfma_f32_16x16x32_bf16 v[176:179], a[16:19], a[32:35], v[176:179]\n" \
"v_mfma_f32_16x16x32_bf16 v[176:179], a[20:23], a[36:39], v[176:179]\n" \
"v_mfma_f32_16x16x32_bf16 v[180:183], a[16:19], a[48:51], v[180:183]\n" \
"v_mfma_f32_16x16x32_bf16 v[180:183], a[20:23], a[52:55], v[180:183]\n" \
"s_waitcnt vmcnt(18)\n" \
"v_mfma_f32_16x16x32_bf16 v[248:251], a[16:19], a[96:99], v[248:251]\n" \
"s_waitcnt vmcnt(17)\n" \
"v_mfma_f32_16x16x32_bf16 v[248:251], a[20:23], a[100:103], v[248:251]\n" \
"s_waitcnt vmcnt(15)\n" \
"v_mfma_f32_16x16x32_bf16 v[252:255], a[16:19], a[112:115], v[252:255]\n" \
"ds_read_b64_tr_b16 a[16:17], v10 offset:9216\n" \
"s_mov_b64 exec, 1\n" \
"buffer_store_dword v39, off, s[56:59], s68 offset:12 sc1\n" \
"s_mov_b64 exec, -1\n" \
"s_waitcnt vmcnt(15)\n" \
"v_mfma_f32_16x16x32_bf16 v[252:255], a[20:23], a[116:119], v[252:255]\n" \
"v_readlane_b32 s68, v11, 49\n" \
"s_waitcnt vmcnt(14)\n" \
"v_mfma_f32_16x16x32_bf16 v[252:255], a[24:27], a[120:123], v[252:255]\n" \
"ds_read_b64_tr_b16 a[18:19], v10 offset:13824\n" \
"s_waitcnt vmcnt(13)\n" \
"v_mfma_f32_16x16x32_bf16 v[252:255], a[28:31], a[124:127], v[252:255]\n" \
"ds_read_b64_tr_b16 a[20:21], v33 offset:32\n" \
"ds_read_b64_tr_b16 a[22:23], v33 offset:4640\n" \
"buffer_load_dwordx4 v[80:83], v20, s[56:59], s96 offen offset:0 sc1\n" \
"s_waitcnt vmcnt(12)\n" \
"v_mfma_f32_16x16x32_bf16 v[248:251], a[24:27], a[104:107], v[248:251]\n" \
"s_mov_b64 exec, 1\n" \
"buffer_store_dword v39, off, s[56:59], s69 offset:16 sc1\n" \
"s_mov_b64 exec, -1\n" \
"v_readlane_b32 s69, v11, 50\n" \
".fill (L_ab + 40960 - .) / 4, 4, 0xBF800000\n" \
"L_ap10:\n" \
"s_cbranch_execz L_ap0\n" \
"s_waitcnt vmcnt(12)\n" \
"v_mfma_f32_16x16x32_bf16 v[248:251], a[28:31], a[108:111], v[248:251]\n" \
"v_mfma_f32_16x16x32_bf16 v[180:183], a[24:27], a[56:59], v[180:183]\n" \
"buffer_load_dwordx4 v[84:87], v20, s[56:59], s96 offen offset:1024 sc1\n" \
"v_mfma_f32_16x16x32_bf16 v[180:183], a[28:31], a[60:63], v[180:183]\n" \
"v_mfma_f32_16x16x32_bf16 v[176:179], a[24:27], a[40:43], v[176:179]\n" \
"ds_read_b64_tr_b16 a[24:25], v33 offset:9248\n" \
"ds_read_b64_tr_b16 a[26:27], v33 offset:13856\n" \
"v_mfma_f32_16x16x32_bf16 v[176:179], a[28:31], a[44:47], v[176:179]\n" \
"ds_read_b64_tr_b16 a[28:29], v33 offset:18464\n" \
"v_cvt_pk_bf16_f32 v248, v248, v249\n" \
"v_cvt_pk_bf16_f32 v249, v250, v251\n" \
"v_cvt_pk_bf16_f32 v250, v252, v253\n" \
"v_cvt_pk_bf16_f32 v251, v254, v255\n" \
"buffer_store_dwordx4 v[248:251], v20, s[56:59], s72 offen offset:1024 sc1\n" \
"v_mfma_f32_16x16x32_bf16 v[184:187], a[192:195], a[220:223], v[184:187]\n" \
"ds_read_b64_tr_b16 a[30:31], v33 offset:23072\n" \
"v_cvt_pk_bf16_f32 v176, v176, v177\n" \
"v_cvt_pk_bf16_f32 v177, v178, v179\n" \
"v_cvt_pk_bf16_f32 v178, v180, v181\n" \
"v_cvt_pk_bf16_f32 v179, v182, v183\n" \
"v_mfma_f32_16x16x32_bf16 v[184:187], a[196:199], a[224:227], v[184:187]\n" \
"buffer_store_dwordx4 v[176:179], v20, s[56:59], s72 offen offset:0 sc1\n" \
"v_mfma_f32_16x16x32_bf16 v[188:191], a[192:195], a[236:239], v[188:191]\n" \
"ds_read_b64_tr_b16 a[192:193], v33 offset:27680\n" \
"ds_read_b64_tr_b16 a[194:195], v33 offset:32288\n" \
"v_mfma_f32_16x16x32_bf16 v[188:191], a[196:199], a[240:243], v[188:191]\n" \
"ds_read_b64_tr_b16 a[196:197], v33 offset:36896\n" \
"ds_read_b64_tr_b16 a[198:199], v33 offset:41504\n" \
"v_mfma_f32_16x16x32_bf16 v[188:191], a[200:203], a[244:247], v[188:191]\n" \
"v_mfma_f32_16x16x32_bf16 v[188:191], a[204:207], a[248:251], v[188:191]\n" \
"v_mfma_f32_16x16x32_bf16 v[184:187], a[200:203], a[228:231], v[184:187]\n" \
"ds_read_b64_tr_b16 a[200:201], v33 offset:46112\n" \
"ds_read_b64_tr_b16 a[202:203], v33 offset:50720\n" \
"v_mfma_f32_16x16x32_bf16 v[184:187], a[204:207], a[232:235], v[184:187]\n" \
"ds_read_b64_tr_b16 a[204:205], v33 offset:55328\n" \
"s_waitcnt lgkmcnt(11)\n" \
"v_mfma_f32_16x16x32_bf16 v[184:187], a[208:211], a[160:163], v[184:187]\n" \
"buffer_load_dwordx4 v[88:91], v32, s[56:59], s96 offen offset:0 sc1\n" \
"v_mfma_f32_16x16x32_bf16 v[184:187], a[212:215], a[164:167], v[184:187]\n" \
"ds_read_b64_tr_b16 a[206:207], v33 offset:59936\n" \
"v_mfma_f32_16x16x32_bf16 v[188:191], a[208:211], a[176:179], v[188:191]\n" \
"ds_read_b64_tr_b16 a[208:209], v10 offset:32\n" \
"ds_read_b64_tr_b16 a[210:211], v10 offset:4640\n" \
"v_mfma_f32_16x16x32_bf16 v[188:191], a[212:215], a[180:183], v[188:191]\n" \
"ds_read_b64_tr_b16 a[212:213], v34 offset:32\n" \
"ds_read_b64_tr_b16 a[214:215], v34 offset:4640\n" \
"v_mfma_f32_16x16x32_bf16 v[188:191], a[216:219], a[184:187], v[188:191]\n" \
"v_mfma_f32_16x16x32_bf16 v[188:191], a[0:3], a[188:191], v[188:191]\n" \
"buffer_load_dwordx4 v[92:95], v32, s[56:59], s96 offen offset:1024 sc1\n" \
"v_mfma_f32_16x16x32_bf16 v[184:187], a[216:219], a[168:171], v[184:187]\n" \
"ds_read_b64_tr_b16 a[216:217], v34 offset:9248\n" \
"ds_read_b64_tr_b16 a[218:219], v34 offset:13856\n" \
"v_mfma_f32_16x16x32_bf16 v[184:187], a[0:3], a[172:175], v[184:187]\n" \
"ds_read_b64_tr_b16 a[0:1], v34 offset:18464\n" \
"ds_read_b64_tr_b16 a[2:3], v34 offset:23072\n" \
"v_mfma_f32_16x16x32_bf16 v[184:187], a[4:7], a[96:99], v[184:187]\n" \
"v_mfma_f32_16x16x32_bf16 v[184:187], a[8:11], a[100:103], v[184:187]\n" \
"v_mfma_f32_16x16x32_bf16 v[188:191], a[4:7], a[112:115], v[188:191]\n" \
"ds_read_b64_tr_b16 a[4:5], v10 offset:9248\n" \
"ds_read_b64_tr_b16 a[6:7], v10 offset:13856\n" \
"v_mfma_f32_16x16x32_bf16 v[188:191], a[8:11], a[116:119], v[188:191]\n" \
"ds_read_b64_tr_b16 a[8:9], v33 offset:64\n" \
"ds_read_b64_tr_b16 a[10:11], v33 offset:4672\n" \
"v_mfma_f32_16x16x32_bf16 v[188:191], a[12:15], a[120:123], v[188:191]\n" \
"buffer_load_dwordx4 v[96:99], v20, s[56:59], s97 offen offset:0 sc1\n" \
"v_mfma_f32_16x16x32_bf16 v[188:191], a[16:19], a[124:127], v[188:191]\n" \
"v_mfma_f32_16x16x32_bf16 v[184:187], a[12:15], a[104:107], v[184:187]\n" \
"ds_read_b64_tr_b16 a[12:13], v33 offset:9280\n" \
"ds_read_b64_tr_b16 a[14:15], v33 offset:13888\n" \
"v_mfma_f32_16x16x32_bf16 v[184:187], a[16:19], a[108:111], v[184:187]\n" \
"buffer_load_dword v37, v40, s[56:59], s74 offen sc1\n" \
"v_mfma_f32_16x16x32_bf16 v[192:195], a[20:23], a[220:223], v[192:195]\n" \
"ds_read_b64_tr_b16 a[16:17], v33 offset:18496\n" \
"s_waitcnt lgkmcnt(13)\n" \
"v_mfma_f32_16x16x32_bf16 v[192:195], a[24:27], a[224:227], v[192:195]\n" \
"ds_read_b64_tr_b16 a[18:19], v33 offset:23104\n" \
"v_mfma_f32_16x16x32_bf16 v[196:199], a[20:23], a[236:239], v[196:199]\n" \
"ds_read_b64_tr_b16 a[20:21], v33 offset:27712\n" \
"v_cvt_pk_bf16_f32 v184, v184, v185\n" \
"v_cvt_pk_bf16_f32 v185, v186, v187\n" \
"v_cvt_pk_bf16_f32 v186, v188, v189\n" \
"v_cvt_pk_bf16_f32 v187, v190, v191\n" \
"buffer_store_dwordx4 v[184:187], v20, s[56:59], s92 offen offset:1024 sc1\n" \
"s_waitcnt vmcnt(18)\n" \
"s_mov_b64 exec, 1\n" \
"buffer_store_dword v39, off, s[56:59], s70 offset:20 sc1\n" \
"s_mov_b64 exec, -1\n" \
"s_add_u32 s92, s92, 0x3800\n" \
"v_mfma_f32_16x16x32_bf16 v[196:199], a[24:27], a[240:243], v[196:199]\n" \
"ds_read_b64_tr_b16 a[22:23], v33 offset:32320\n" \
"ds_read_b64_tr_b16 a[24:25], v33 offset:36928\n" \
"v_mfma_f32_16x16x32_bf16 v[196:199], a[28:31], a[244:247], v[196:199]\n" \
"ds_read_b64_tr_b16 a[26:27], v33 offset:41536\n" \
"v_mfma_f32_16x16x32_bf16 v[196:199], a[192:195], a[248:251], v[196:199]\n" \
"v_mfma_f32_16x16x32_bf16 v[192:195], a[28:31], a[228:231], v[192:195]\n" \
"ds_read_b64_tr_b16 a[28:29], v33 offset:46144\n" \
"ds_read_b64_tr_b16 a[30:31], v33 offset:50752\n" \
"v_mfma_f32_16x16x32_bf16 v[192:195], a[192:195], a[232:235], v[192:195]\n" \
"buffer_load_dwordx4 v[100:103], v20, s[56:59], s97 offen offset:1024 sc1\n" \
"v_mfma_f32_16x16x32_bf16 v[192:195], a[196:199], a[160:163], v[192:195]\n" \
"ds_read_b64_tr_b16 a[192:193], v33 offset:55360\n" \
"ds_read_b64_tr_b16 a[194:195], v33 offset:59968\n" \
"v_mfma_f32_16x16x32_bf16 v[192:195], a[200:203], a[164:167], v[192:195]\n" \
"v_mfma_f32_16x16x32_bf16 v[196:199], a[196:199], a[176:179], v[196:199]\n" \
"ds_read_b64_tr_b16 a[196:197], v10 offset:64\n" \
"ds_read_b64_tr_b16 a[198:199], v10 offset:4672\n" \
"v_mfma_f32_16x16x32_bf16 v[196:199], a[200:203], a[180:183], v[196:199]\n" \
"ds_read_b64_tr_b16 a[200:201], v34 offset:64\n" \
"ds_read_b64_tr_b16 a[202:203], v34 offset:4672\n" \
"v_mfma_f32_16x16x32_bf16 v[196:199], a[204:207], a[184:187], v[196:199]\n" \
"buffer_load_dwordx4 v[104:107], v32, s[56:59], s97 offen offset:0 sc1\n" \
"v_mfma_f32_16x16x32_bf16 v[196:199], a[208:211], a[188:191], v[196:199]\n" \
"v_mfma_f32_16x16x32_bf16 v[192:195], a[204:207], a[168:171], v[192:195]\n" \
"ds_read_b64_tr_b16 a[204:205], v34 offset:9280\n" \
"ds_read_b64_tr_b16 a[206:207], v34 offset:13888\n" \
"v_mfma_f32_16x16x32_bf16 v[192:195], a[208:211], a[172:175], v[192:195]\n" \
"ds_read_b64_tr_b16 a[208:209], v34 offset:18496\n" \
"s_waitcnt lgkmcnt(14)\n" \
"v_mfma_f32_16x16x32_bf16 v[192:195], a[212:215], a[96:99], v[192:195]\n" \
"ds_read_b64_tr_b16 a[210:211], v34 offset:23104\n" \
"v_mfma_f32_16x16x32_bf16 v[192:195], a[216:219], a[100:103], v[192:195]\n" \
"buffer_load_dwordx4 v[108:111], v32, s[56:59], s97 offen offset:1024 sc1\n" \
"v_mfma_f32_16x16x32_bf16 v[196:199], a[212:215], a[112:115], v[196:199]\n" \
"ds_read_b64_tr_b16 a[212:213], v10 offset:9280\n" \
"ds_read_b64_tr_b16 a[214:215], v10 offset:13888\n" \
"v_mfma_f32_16x16x32_bf16 v[196:199], a[216:219], a[116:119], v[196:199]\n" \
"ds_read_b64_tr_b16 a[216:217], v33 offset:96\n" \
"ds_read_b64_tr_b16 a[218:219], v33 offset:4704\n" \
"s_waitcnt vmcnt(19)\n" \
"s_mov_b64 exec, 1\n" \
"buffer_store_dword v39, off, s[56:59], s71 offset:24 sc1\n" \
"s_mov_b64 exec, -1\n" \
"v_mfma_f32_16x16x32_bf16 v[196:199], a[0:3], a[120:123], v[196:199]\n" \
"v_mfma_f32_16x16x32_bf16 v[196:199], a[4:7], a[124:127], v[196:199]\n" \
"v_mfma_f32_16x16x32_bf16 v[192:195], a[0:3], a[104:107], v[192:195]\n" \
"ds_read_b64_tr_b16 a[0:1], v33 offset:9312\n" \
"ds_read_b64_tr_b16 a[2:3], v33 offset:13920\n" \
"v_mfma_f32_16x16x32_bf16 v[192:195], a[4:7], a[108:111], v[192:195]\n" \
"ds_read_b64_tr_b16 a[4:5], v33 offset:18528\n" \
"ds_read_b64_tr_b16 a[6:7], v33 offset:23136\n" \
"v_mfma_f32_16x16x32_bf16 v[200:203], a[8:11], a[220:223], v[200:203]\n" \
"v_mfma_f32_16x16x32_bf16 v[200:203], a[12:15], a[224:227], v[200:203]\n" \
"v_mfma_f32_16x16x32_bf16 v[204:207], a[8:11], a[236:239], v[204:207]\n" \
"ds_read_b64_tr_b16 a[8:9], v33 offset:27744\n" \
"ds_read_b64_tr_b16 a[10:11], v33 offset:32352\n" \
"v_mfma_f32_16x16x32_bf16 v[204:207], a[12:15], a[240:243], v[204:207]\n" \
"v_cvt_pk_bf16_f32 v192, v192, v193\n" \
"v_cvt_pk_bf16_f32 v193, v194, v195\n" \
"v_cvt_pk_bf16_f32 v194, v196, v197\n" \
"v_cvt_pk_bf16_f32 v195, v198, v199\n" \
"buffer_store_dwordx4 v[192:195], v20, s[56:59], s66 offen offset:1024 sc1\n" \
"v_mfma_f32_16x16x32_bf16 v[204:207], a[16:19], a[244:247], v[204:207]\n" \
"ds_read_b64_tr_b16 a[12:13], v33 offset:36960\n" \
"s_waitcnt lgkmcnt(11)\n" \
"v_mfma_f32_16x16x32_bf16 v[204:207], a[20:23], a[248:251], v[204:207]\n" \
"ds_read_b64_tr_b16 a[14:15], v33 offset:41568\n" \
"v_mfma_f32_16x16x32_bf16 v[200:203], a[16:19], a[228:231], v[200:203]\n" \
"ds_read_b64_tr_b16 a[16:17], v33 offset:46176\n" \
"ds_read_b64_tr_b16 a[18:19], v33 offset:50784\n" \
"v_mfma_f32_16x16x32_bf16 v[200:203], a[20:23], a[232:235], v[200:203]\n" \
"ds_read_b64_tr_b16 a[20:21], v33 offset:55392\n" \
"ds_read_b64_tr_b16 a[22:23], v33 offset:60000\n" \
"v_mfma_f32_16x16x32_bf16 v[200:203], a[24:27], a[160:163], v[200:203]\n" \
"v_mfma_f32_16x16x32_bf16 v[200:203], a[28:31], a[164:167], v[200:203]\n" \
"v_mfma_f32_16x16x32_bf16 v[204:207], a[24:27], a[176:179], v[204:207]\n" \
"ds_read_b64_tr_b16 a[24:25], v10 offset:96\n" \
"ds_read_b64_tr_b16 a[26:27], v10 offset:4704\n" \
"v_mfma_f32_16x16x32_bf16 v[204:207], a[28:31], a[180:183], v[204:207]\n" \
"ds_read_b64_tr_b16 a[28:29], v34 offset:96\n" \
"ds_read_b64_tr_b16 a[30:31], v34 offset:4704\n" \
"v_mfma_f32_16x16x32_bf16 v[204:207], a[192:195], a[184:187], v[204:207]\n" \
"v_mfma_f32_16x16x32_bf16 v[204:207], a[196:199], a[188:191], v[204:207]\n" \
"v_mfma_f32_16x16x32_bf16 v[200:203], a[192:195], a[168:171], v[200:203]\n" \
"ds_read_b64_tr_b16 a[192:193], v34 offset:9312\n" \
"ds_read_b64_tr_b16 a[194:195], v34 offset:13920\n" \
"v_mfma_f32_16x16x32_bf16 v[200:203], a[196:199], a[172:175], v[200:203]\n" \
"ds_read_b64_tr_b16 a[196:197], v34 offset:18528\n" \
"ds_read_b64_tr_b16 a[198:199], v34 offset:23136\n" \
"v_mfma_f32_16x16x32_bf16 v[200:203], a[200:203], a[96:99], v[200:203]\n" \
"v_mfma_f32_16x16x32_bf16 v[200:203], a[204:207], a[100:103], v[200:203]\n" \
"v_mfma_f32_16x16x32_bf16 v[204:207], a[200:203], a[112:115], v[204:207]\n" \
"ds_read_b64_tr_b16 a[200:201], v10 offset:9312\n" \
"ds_read_b64_tr_b16 a[202:203], v10 offset:13920\n" \
"v_mfma_f32_16x16x32_bf16 v[204:207], a[204:207], a[116:119], v[204:207]\n" \
"ds_read_b64_tr_b16 a[204:205], v33 offset:128\n" \
"ds_read_b64_tr_b16 a[206:207], v33 offset:4736\n" \
"v_mfma_f32_16x16x32_bf16 v[204:207], a[208:211], a[120:123], v[204:207]\n" \
"s_waitcnt lgkmcnt(15)\n" \
"v_mfma_f32_16x16x32_bf16 v[204:207], a[212:215], a[124:127], v[204:207]\n" \
"v_mfma_f32_16x16x32_bf16 v[200:203], a[208:211], a[104:107], v[200:203]\n" \
"ds_read_b64_tr_b16 a[208:209], v33 offset:9344\n" \
"ds_read_b64_tr_b16 a[210:211], v33 offset:13952\n" \
"v_mfma_f32_16x16x32_bf16 v[200:203], a[212:215], a[108:111], v[200:203]\n" \
"ds_read_b64_tr_b16 a[212:213], v33 offset:18560\n" \
"ds_read_b64_tr_b16 a[214:215], v33 offset:23168\n" \
"v_mfma_f32_16x16x32_bf16 v[208:211], a[216:219], a[220:223], v[208:211]\n" \
"v_mfma_f32_16x16x32_bf16 v[208:211], a[0:3], a[224:227], v[208:211]\n" \
"v_mfma_f32_16x16x32_bf16 v[212:215], a[216:219], a[236:239], v[212:215]\n" \
"ds_read_b64_tr_b16 a[216:217], v33 offset:27776\n" \
"ds_read_b64_tr_b16 a[218:219], v33 offset:32384\n" \
"v_mfma_f32_16x16x32_bf16 v[212:215], a[0:3], a[240:243], v[212:215]\n" \
"v_cvt_pk_bf16_f32 v200, v200, v201\n" \
"v_cvt_pk_bf16_f32 v201, v202, v203\n" \
"v_cvt_pk_bf16_f32 v202, v204, v205\n" \
"v_cvt_pk_bf16_f32 v203, v206, v207\n" \
"buffer_store_dwordx4 v[200:203], v20, s[56:59], s67 offen offset:1024 sc1\n" \
"v_mfma_f32_16x16x32_bf16 v[212:215], a[4:7], a[244:247], v[212:215]\n" \
"ds_read_b64_tr_b16 a[0:1], v33 offset:36992\n" \
"ds_read_b64_tr_b16 a[2:3], v33 offset:41600\n" \
"v_mfma_f32_16x16x32_bf16 v[212:215], a[8:11], a[248:251], v[212:215]\n" \
"v_mfma_f32_16x16x32_bf16 v[208:211], a[4:7], a[228:231], v[208:211]\n" \
"ds_read_b64_tr_b16 a[4:5], v33 offset:46208\n" \
"ds_read_b64_tr_b16 a[6:7], v33 offset:50816\n" \
"v_mfma_f32_16x16x32_bf16 v[208:211], a[8:11], a[232:235], v[208:211]\n" \
"ds_read_b64_tr_b16 a[8:9], v33 offset:55424\n" \
"ds_read_b64_tr_b16 a[10:11], v33 offset:60032\n" \
"v_mfma_f32_16x16x32_bf16 v[208:211], a[12:15], a[160:163], v[208:211]\n" \
"s_waitcnt lgkmcnt(13)\n" \
"v_mfma_f32_16x16x32_bf16 v[208:211], a[16:19], a[164:167], v[208:211]\n" \
"v_mfma_f32_16x16x32_bf16 v[212:215], a[12:15], a[176:179], v[212:215]\n" \
"s_waitcnt vmcnt(8)\n" \
"v_cmp_eq_u32_e64 s[98:99], s62, v37\n" \
"s_mov_b64 exec, 3\n" \
"buffer_store_dword v39, v41, s[56:59], s73 offen sc1\n" \
"s_mov_b64 exec, -1\n" \
"s_andn2_b32 s75, 0x7f, s98\n" \
"s_mov_b32 s20, s75\n" \
"s_bitcmp1_b32 s20, 0\n" \
"s_cselect_b32 s93, 0x40000000, s12\n" \
"s_bitcmp1_b32 s20, 1\n" \
"s_cselect_b32 s94, 0x40000000, s12\n" \
"s_bitcmp1_b32 s20, 2\n" \
"s_cselect_b32 s95, 0x40000000, s13\n" \
"s_bitcmp1_b32 s20, 3\n" \
"s_cselect_b32 s96, 0x40000000, s13\n" \
"s_bitcmp1_b32 s20, 4\n" \
"s_cselect_b32 s97, 0x40000000, s14\n" \
"s_bitcmp1_b32 s20, 5\n" \
"s_cselect_b32 s64, 0x40000000, s14\n" \
"s_bitcmp1_b32 s20, 6\n" \
"s_cselect_b32 s65, 0x40000000, s15\n" \
"s_add_u32 s66, s66, 0x3000\n" \
"s_add_u32 s67, s67, 0x2800\n" \
"v_mfma_f32_16x16x32_bf16 v[212:215], a[16:19], a[180:183], v[212:215]\n" \
"buffer_load_dwordx4 v[116:119], v20, s[56:59], s93 offen offset:0 sc1\n" \
"v_mfma_f32_16x16x32_bf16 v[212:215], a[20:23], a[184:187], v[212:215]\n" \
"ds_read_b64_tr_b16 a[12:13], v10 offset:128\n" \
"ds_read_b64_tr_b16 a[14:15], v10 offset:4736\n" \
"v_mfma_f32_16x16x32_bf16 v[212:215], a[24:27], a[188:191], v[212:215]\n" \
"ds_read_b64_tr_b16 a[16:17], v34 offset:128\n" \
"ds_read_b64_tr_b16 a[18:19], v34 offset:4736\n" \
"v_mfma_f32_16x16x32_bf16 v[208:211], a[20:23], a[168:171], v[208:211]\n" \
"ds_read_b64_tr_b16 a[20:21], v34 offset:9344\n" \
"ds_read_b64_tr_b16 a[22:23], v34 offset:13952\n" \
"v_mfma_f32_16x16x32_bf16 v[208:211], a[24:27], a[172:175], v[208:211]\n" \
"ds_read_b64_tr_b16 a[24:25], v34 offset:18560\n" \
"ds_read_b64_tr_b16 a[26:27], v34 offset:23168\n" \
"v_mfma_f32_16x16x32_bf16 v[208:211], a[28:31], a[96:99], v[208:211]\n" \
"v_mfma_f32_16x16x32_bf16 v[208:211], a[192:195], a[100:103], v[208:211]\n" \
"v_mfma_f32_16x16x32_bf16 v[212:215], a[28:31], a[112:115], v[212:215]\n" \
"ds_read_b64_tr_b16 a[28:29], v10 offset:9344\n" \
"ds_read_b64_tr_b16 a[30:31], v10 offset:13952\n" \
"v_mfma_f32_16x16x32_bf16 v[212:215], a[192:195], a[116:119], v[212:215]\n" \
"ds_read_b64_tr_b16 a[192:193], v33 offset:160\n" \
"ds_read_b64_tr_b16 a[194:195], v33 offset:4768\n" \
"v_mfma_f32_16x16x32_bf16 v[212:215], a[196:199], a[120:123], v[212:215]\n" \
"v_mfma_f32_16x16x32_bf16 v[212:215], a[200:203], a[124:127], v[212:215]\n" \
"v_mfma_f32_16x16x32_bf16 v[208:211], a[196:199], a[104:107], v[208:211]\n" \
"ds_read_b64_tr_b16 a[196:197], v33 offset:9376\n" \
"ds_read_b64_tr_b16 a[198:199], v33 offset:13984\n" \
"v_mfma_f32_16x16x32_bf16 v[208:211], a[200:203], a[108:111], v[208:211]\n" \
"ds_read_b64_tr_b16 a[200:201], v33 offset:18592\n" \
"s_waitcnt lgkmcnt(15)\n" \
"v_mfma_f32_16x16x32_bf16 v[216:219], a[204:207], a[220:223], v[216:219]\n" \
"ds_read_b64_tr_b16 a[202:203], v33 offset:23200\n" \
"v_mfma_f32_16x16x32_bf16 v[216:219], a[208:211], a[224:227], v[216:219]\n" \
"buffer_load_dwordx4 v[124:127], v20, s[56:59], s94 offen offset:2048 sc1\n" \
"v_mfma_f32_16x16x32_bf16 v[220:223], a[204:207], a[236:239], v[220:223]\n" \
"ds_read_b64_tr_b16 a[204:205], v33 offset:27808\n" \
"v_cvt_pk_bf16_f32 v208, v208, v209\n" \
"v_cvt_pk_bf16_f32 v209, v210, v211\n" \
"v_cvt_pk_bf16_f32 v210, v212, v213\n" \
"v_cvt_pk_bf16_f32 v211, v214, v215\n" \
"v_mfma_f32_16x16x32_bf16 v[220:223], a[208:211], a[240:243], v[220:223]\n" \
"buffer_store_dwordx4 v[208:211], v20, s[56:59], s68 offen offset:1024 sc1\n" \
"s_add_u32 s68, s68, 0x2000\n" \
"v_mfma_f32_16x16x32_bf16 v[220:223], a[212:215], a[244:247], v[220:223]\n" \
"ds_read_b64_tr_b16 a[206:207], v33 offset:32416\n" \
"ds_read_b64_tr_b16 a[208:209], v33 offset:37024\n" \
"v_mfma_f32_16x16x32_bf16 v[220:223], a[216:219], a[248:251], v[220:223]\n" \
"ds_read_b64_tr_b16 a[210:211], v33 offset:41632\n" \
"v_mfma_f32_16x16x32_bf16 v[216:219], a[212:215], a[228:231], v[216:219]\n" \
"ds_read_b64_tr_b16 a[212:213], v33 offset:46240\n" \
"ds_read_b64_tr_b16 a[214:215], v33 offset:50848\n" \
"v_mfma_f32_16x16x32_bf16 v[216:219], a[216:219], a[232:235], v[216:219]\n" \
"ds_read_b64_tr_b16 a[216:217], v33 offset:55456\n" \
"ds_read_b64_tr_b16 a[218:219], v33 offset:60064\n" \
"v_mfma_f32_16x16x32_bf16 v[216:219], a[0:3], a[160:163], v[216:219]\n" \
"v_mfma_f32_16x16x32_bf16 v[216:219], a[4:7], a[164:167], v[216:219]\n" \
"v_mfma_f32_16x16x32_bf16 v[220:223], a[0:3], a[176:179], v[220:223]\n" \
"ds_read_b64_tr_b16 a[0:1], v10 offset:160\n" \
"ds_read_b64_tr_b16 a[2:3], v10 offset:4768\n" \
"v_mfma_f32_16x16x32_bf16 v[220:223], a[4:7], a[180:183], v[220:223]\n" \
"ds_read_b64_tr_b16 a[4:5], v34 offset:160\n" \
"ds_read_b64_tr_b16 a[6:7], v34 offset:4768\n" \
"v_mfma_f32_16x16x32_bf16 v[220:223], a[8:11], a[184:187], v[220:223]\n" \
"s_waitcnt lgkmcnt(13)\n" \
"v_mfma_f32_16x16x32_bf16 v[220:223], a[12:15], a[188:191], v[220:223]\n" \
"s_waitcnt vmcnt(11)\n" \
"s_mov_b64 exec, 1\n" \
"buffer_store_dword v39, off, s[56:59], s92 offset:32 sc1\n" \
"s_mov_b64 exec, -1\n" \
"v_mfma_f32_16x16x32_bf16 v[216:219], a[8:11], a[168:171], v[216:219]\n" \
"ds_read_b64_tr_b16 a[8:9], v34 offset:9376\n" \
"ds_read_b64_tr_b16 a[10:11], v34 offset:13984\n" \
"v_mfma_f32_16x16x32_bf16 v[216:219], a[12:15], a[172:175], v[216:219]\n" \
"ds_read_b64_tr_b16 a[12:13], v34 offset:18592\n" \
"ds_read_b64_tr_b16 a[14:15], v34 offset:23200\n" \
"v_mfma_f32_16x16x32_bf16 v[216:219], a[16:19], a[96:99], v[216:219]\n" \
"v_mfma_f32_16x16x32_bf16 v[216:219], a[20:23], a[100:103], v[216:219]\n" \
"buffer_load_dwordx4 v[132:135], v20, s[56:59], s95 offen offset:0 sc1\n" \
"v_mfma_f32_16x16x32_bf16 v[220:223], a[16:19], a[112:115], v[220:223]\n" \
"ds_read_b64_tr_b16 a[16:17], v10 offset:9376\n" \
"ds_read_b64_tr_b16 a[18:19], v10 offset:13984\n" \
"v_mfma_f32_16x16x32_bf16 v[220:223], a[20:23], a[116:119], v[220:223]\n" \
"ds_read_b64_tr_b16 a[20:21], v33 offset:192\n" \
"ds_read_b64_tr_b16 a[22:23], v33 offset:4800\n" \
"v_mfma_f32_16x16x32_bf16 v[220:223], a[24:27], a[120:123], v[220:223]\n" \
"v_mfma_f32_16x16x32_bf16 v[220:223], a[28:31], a[124:127], v[220:223]\n" \
"v_mfma_f32_16x16x32_bf16 v[216:219], a[24:27], a[104:107], v[216:219]\n" \
"ds_read_b64_tr_b16 a[24:25], v33 offset:9408\n" \
"ds_read_b64_tr_b16 a[26:27], v33 offset:14016\n" \
"v_mfma_f32_16x16x32_bf16 v[216:219], a[28:31], a[108:111], v[216:219]\n" \
"buffer_load_dword v37, v40, s[56:59], s74 offen sc1\n" \
"v_mfma_f32_16x16x32_bf16 v[224:227], a[192:195], a[220:223], v[224:227]\n" \
"ds_read_b64_tr_b16 a[28:29], v33 offset:18624\n" \
"ds_read_b64_tr_b16 a[30:31], v33 offset:23232\n" \
"v_mfma_f32_16x16x32_bf16 v[224:227], a[196:199], a[224:227], v[224:227]\n" \
"v_mfma_f32_16x16x32_bf16 v[228:231], a[192:195], a[236:239], v[228:231]\n" \
"ds_read_b64_tr_b16 a[192:193], v33 offset:27840\n" \
"ds_read_b64_tr_b16 a[194:195], v33 offset:32448\n" \
"v_cvt_pk_bf16_f32 v216, v216, v217\n" \
"v_cvt_pk_bf16_f32 v217, v218, v219\n" \
"v_cvt_pk_bf16_f32 v218, v220, v221\n" \
"v_cvt_pk_bf16_f32 v219, v222, v223\n" \
"buffer_store_dwordx4 v[216:219], v20, s[56:59], s69 offen offset:1024 sc1\n" \
"s_add_u32 s69, s69, 0x1800\n" \
"v_mfma_f32_16x16x32_bf16 v[228:231], a[196:199], a[240:243], v[228:231]\n" \
"ds_read_b64_tr_b16 a[196:197], v33 offset:37056\n" \
"s_waitcnt lgkmcnt(12)\n" \
"v_mfma_f32_16x16x32_bf16 v[228:231], a[200:203], a[244:247], v[228:231]\n" \
"ds_read_b64_tr_b16 a[198:199], v33 offset:41664\n" \
"v_mfma_f32_16x16x32_bf16 v[228:231], a[204:207], a[248:251], v[228:231]\n" \
"v_mfma_f32_16x16x32_bf16 v[224:227], a[200:203], a[228:231], v[224:227]\n" \
"buffer_load_dwordx4 v[140:143], v20, s[56:59], s96 offen offset:2048 sc1\n" \
"v_mfma_f32_16x16x32_bf16 v[224:227], a[204:207], a[232:235], v[224:227]\n" \
"ds_read_b64_tr_b16 a[200:201], v33 offset:46272\n" \
"ds_read_b64_tr_b16 a[202:203], v33 offset:50880\n" \
"v_mfma_f32_16x16x32_bf16 v[224:227], a[208:211], a[160:163], v[224:227]\n" \
"ds_read_b64_tr_b16 a[204:205], v33 offset:55488\n" \
"ds_read_b64_tr_b16 a[206:207], v33 offset:60096\n" \
"v_mfma_f32_16x16x32_bf16 v[224:227], a[212:215], a[164:167], v[224:227]\n" \
"v_mfma_f32_16x16x32_bf16 v[228:231], a[208:211], a[176:179], v[228:231]\n" \
"ds_read_b64_tr_b16 a[208:209], v10 offset:192\n" \
"ds_read_b64_tr_b16 a[210:211], v10 offset:4800\n" \
"v_mfma_f32_16x16x32_bf16 v[228:231], a[212:215], a[180:183], v[228:231]\n" \
"ds_read_b64_tr_b16 a[212:213], v34 offset:192\n" \
"ds_read_b64_tr_b16 a[214:215], v34 offset:4800\n" \
"v_mfma_f32_16x16x32_bf16 v[228:231], a[216:219], a[184:187], v[228:231]\n" \
"v_mfma_f32_16x16x32_bf16 v[228:231], a[0:3], a[188:191], v[228:231]\n" \
"v_mfma_f32_16x16x32_bf16 v[224:227], a[216:219], a[168:171], v[224:227]\n" \
"ds_read_b64_tr_b16 a[216:217], v34 offset:9408\n" \
"ds_read_b64_tr_b16 a[218:219], v34 offset:14016\n" \
"v_mfma_f32_16x16x32_bf16 v[224:227], a[0:3], a[172:175], v[224:227]\n" \
"ds_read_b64_tr_b16 a[0:1], v34 offset:18624\n" \
"ds_read_b64_tr_b16 a[2:3], v34 offset:23232\n" \
"s_waitcnt vmcnt(10)\n" \
"s_mov_b64 exec, 1\n" \
"buffer_store_dword v39, off, s[56:59], s66 offset:36 sc1\n" \
"s_mov_b64 exec, -1\n" \
"v_mfma_f32_16x16x32_bf16 v[224:227], a[4:7], a[96:99], v[224:227]\n" \
"v_mfma_f32_16x16x32_bf16 v[224:227], a[8:11], a[100:103], v[224:227]\n" \
"v_mfma_f32_16x16x32_bf16 v[228:231], a[4:7], a[112:115], v[228:231]\n" \
"ds_read_b64_tr_b16 a[4:5], v10 offset:9408\n" \
"ds_read_b64_tr_b16 a[6:7], v10 offset:14016\n" \
"v_mfma_f32_16x16x32_bf16 v[228:231], a[8:11], a[116:119], v[228:231]\n" \
"ds_read_b64_tr_b16 a[8:9], v33 offset:224\n" \
"s_waitcnt lgkmcnt(15)\n" \
"v_mfma_f32_16x16x32_bf16 v[228:231], a[12:15], a[120:123], v[228:231]\n" \
"buffer_load_dwordx4 v[148:151], v20, s[56:59], s97 offen offset:0 sc1\n" \
"v_mfma_f32_16x16x32_bf16 v[228:231], a[16:19], a[124:127], v[228:231]\n" \
"ds_read_b64_tr_b16 a[10:11], v33 offset:4832\n" \
"v_mfma_f32_16x16x32_bf16 v[224:227], a[12:15], a[104:107], v[224:227]\n" \
"ds_read_b64_tr_b16 a[12:13], v33 offset:9440\n" \
"ds_read_b64_tr_b16 a[14:15], v33 offset:14048\n" \
"v_mfma_f32_16x16x32_bf16 v[224:227], a[16:19], a[108:111], v[224:227]\n" \
"ds_read_b64_tr_b16 a[16:17], v33 offset:18656\n" \
"ds_read_b64_tr_b16 a[18:19], v33 offset:23264\n" \
"v_mfma_f32_16x16x32_bf16 v[232:235], a[20:23], a[220:223], v[232:235]\n" \
"v_mfma_f32_16x16x32_bf16 v[232:235], a[24:27], a[224:227], v[232:235]\n" \
"v_mfma_f32_16x16x32_bf16 v[236:239], a[20:23], a[236:239], v[236:239]\n" \
"ds_read_b64_tr_b16 a[20:21], v33 offset:27872\n" \
"ds_read_b64_tr_b16 a[22:23], v33 offset:32480\n" \
"v_mfma_f32_16x16x32_bf16 v[236:239], a[24:27], a[240:243], v[236:239]\n" \
"v_cvt_pk_bf16_f32 v224, v224, v225\n" \
"v_cvt_pk_bf16_f32 v225, v226, v227\n" \
"v_cvt_pk_bf16_f32 v226, v228, v229\n" \
"v_cvt_pk_bf16_f32 v227, v230, v231\n" \
"v_mfma_f32_16x16x32_bf16 v[236:239], a[28:31], a[244:247], v[236:239]\n" \
"ds_read_b64_tr_b16 a[24:25], v33 offset:37088\n" \
"ds_read_b64_tr_b16 a[26:27], v33 offset:41696\n" \
"v_mfma_f32_16x16x32_bf16 v[236:239], a[192:195], a[248:251], v[236:239]\n" \
"v_mfma_f32_16x16x32_bf16 v[232:235], a[28:31], a[228:231], v[232:235]\n" \
"ds_read_b64_tr_b16 a[28:29], v33 offset:46304\n" \
"ds_read_b64_tr_b16 a[30:31], v33 offset:50912\n" \
"v_mfma_f32_16x16x32_bf16 v[232:235], a[192:195], a[232:235], v[232:235]\n" \
"ds_read_b64_tr_b16 a[192:193], v33 offset:55520\n" \
"ds_read_b64_tr_b16 a[194:195], v33 offset:60128\n" \
"v_mfma_f32_16x16x32_bf16 v[232:235], a[196:199], a[160:163], v[232:235]\n" \
"s_waitcnt lgkmcnt(14)\n" \
"v_mfma_f32_16x16x32_bf16 v[232:235], a[200:203], a[164:167], v[232:235]\n" \
"buffer_load_dwordx4 v[156:159], v20, s[56:59], s64 offen offset:2048 sc1\n" \
"v_readlane_b32 s64, v11, 51\n" \
"v_mfma_f32_16x16x32_bf16 v[236:239], a[196:199], a[176:179], v[236:239]\n" \
"ds_read_b64_tr_b16 a[196:197], v10 offset:224\n" \
"ds_read_b64_tr_b16 a[198:199], v10 offset:4832\n" \
"v_mfma_f32_16x16x32_bf16 v[236:239], a[200:203], a[180:183], v[236:239]\n" \
"ds_read_b64_tr_b16 a[200:201], v34 offset:224\n" \
"ds_read_b64_tr_b16 a[202:203], v34 offset:4832\n" \
"buffer_store_dwordx4 v[224:227], v20, s[56:59], s64 offen offset:1024 sc1\n" \
"v_mfma_f32_16x16x32_bf16 v[236:239], a[204:207], a[184:187], v[236:239]\n" \
"v_mfma_f32_16x16x32_bf16 v[236:239], a[208:211], a[188:191], v[236:239]\n" \
"v_mfma_f32_16x16x32_bf16 v[232:235], a[204:207], a[168:171], v[232:235]\n" \
"ds_read_b64_tr_b16 a[204:205], v34 offset:9440\n" \
"ds_read_b64_tr_b16 a[206:207], v34 offset:14048\n" \
"s_waitcnt vmcnt(13)\n" \
"s_mov_b64 exec, 1\n" \
"buffer_store_dword v39, off, s[56:59], s67 offset:40 sc1\n" \
"s_mov_b64 exec, -1\n" \
"v_mfma_f32_16x16x32_bf16 v[232:235], a[208:211], a[172:175], v[232:235]\n" \
"ds_read_b64_tr_b16 a[208:209], v34 offset:18656\n" \
"ds_read_b64_tr_b16 a[210:211], v34 offset:23264\n" \
"v_mfma_f32_16x16x32_bf16 v[232:235], a[212:215], a[96:99], v[232:235]\n" \
"v_mfma_f32_16x16x32_bf16 v[232:235], a[216:219], a[100:103], v[232:235]\n" \
"v_mfma_f32_16x16x32_bf16 v[236:239], a[212:215], a[112:115], v[236:239]\n" \
"ds_read_b64_tr_b16 a[212:213], v10 offset:9440\n" \
"ds_read_b64_tr_b16 a[214:215], v10 offset:14048\n" \
"v_mfma_f32_16x16x32_bf16 v[236:239], a[216:219], a[116:119], v[236:239]\n" \
"ds_read_b64_tr_b16 a[216:217], v36 offset:0\n" \
"ds_read_b64_tr_b16 a[218:219], v36 offset:1024\n" \
"v_mfma_f32_16x16x32_bf16 v[236:239], a[0:3], a[120:123], v[236:239]\n" \
"v_mfma_f32_16x16x32_bf16 v[236:239], a[4:7], a[124:127], v[236:239]\n" \
"v_mfma_f32_16x16x32_bf16 v[232:235], a[0:3], a[104:107], v[232:235]\n" \
"ds_read_b64_tr_b16 a[0:1], v36 offset:2048\n" \
"ds_read_b64_tr_b16 a[2:3], v36 offset:3072\n" \
"v_mfma_f32_16x16x32_bf16 v[232:235], a[4:7], a[108:111], v[232:235]\n" \
"ds_read_b64_tr_b16 a[4:5], v36 offset:32\n" \
"buffer_load_dwordx4 v[164:167], v20, s[56:59], s65 offen offset:0 sc1\n" \
"v_readlane_b32 s65, v11, 52\n" \
"s_waitcnt lgkmcnt(11)\n" \
"v_mfma_f32_16x16x32_bf16 v[240:243], a[8:11], a[220:223], v[240:243]\n" \
"ds_read_b64_tr_b16 a[6:7], v36 offset:1056\n" \
"v_mfma_f32_16x16x32_bf16 v[240:243], a[12:15], a[224:227], v[240:243]\n" \
"v_mfma_f32_16x16x32_bf16 v[244:247], a[8:11], a[236:239], v[244:247]\n" \
"v_cvt_pk_bf16_f32 v232, v232, v233\n" \
"v_cvt_pk_bf16_f32 v233, v234, v235\n" \
"v_cvt_pk_bf16_f32 v234, v236, v237\n" \
"v_cvt_pk_bf16_f32 v235, v238, v239\n" \
"buffer_store_dwordx4 v[232:235], v20, s[56:59], s65 offen offset:1024 sc1\n" \
"v_mfma_f32_16x16x32_bf16 v[244:247], a[12:15], a[240:243], v[244:247]\n" \
"ds_read_b64_tr_b16 a[8:9], v36 offset:2080\n" \
"ds_read_b64_tr_b16 a[10:11], v36 offset:3104\n" \
"v_mfma_f32_16x16x32_bf16 v[244:247], a[16:19], a[244:247], v[244:247]\n" \
"v_mfma_f32_16x16x32_bf16 v[244:247], a[20:23], a[248:251], v[244:247]\n" \
"v_mfma_f32_16x16x32_bf16 v[240:243], a[16:19], a[228:231], v[240:243]\n" \
"v_mfma_f32_16x16x32_bf16 v[240:243], a[20:23], a[232:235], v[240:243]\n" \
"v_mfma_f32_16x16x32_bf16 v[168:171], a[24:27], a[128:131], v[168:171]\n" \
"v_mfma_f32_16x16x32_bf16 v[168:171], a[28:31], a[132:135], v[168:171]\n" \
"v_mfma_f32_16x16x32_bf16 v[172:175], a[24:27], a[144:147], v[172:175]\n" \
"s_waitcnt vmcnt(9)\n" \
"v_cmp_eq_u32_e64 s[98:99], s62, v37\n" \
"buffer_load_dword v37, v40, s[56:59], s74 offen sc1\n" \
"s_mov_b64 exec, 1\n" \
"buffer_store_dword v39, off, s[56:59], s68 offset:44 sc1\n" \
"s_mov_b64 exec, -1\n" \
"s_or_b32 s75, 0x300, s100\n" \
"s_andn2_b32 s75, s75, s98\n" \
"s_mov_b32 s21, s75\n" \
"s_bitcmp1_b32 s21, 16\n" \
"s_cselect_b32 s70, 0x40000000, s16\n" \
"s_bitcmp1_b32 s21, 17\n" \
"s_cselect_b32 s71, 0x40000000, s16\n" \
"s_bitcmp1_b32 s21, 18\n" \
"s_cselect_b32 s72, 0x40000000, s17\n" \
"s_bitcmp1_b32 s21, 19\n" \
"s_cselect_b32 s73, 0x40000000, s17\n" \
"s_bitcmp1_b32 s21, 20\n" \
"s_cselect_b32 s92, 0x40000000, s18\n" \
"s_bitcmp1_b32 s21, 21\n" \
"s_cselect_b32 s93, 0x40000000, s18\n" \
"s_bitcmp1_b32 s21, 22\n" \
"s_cselect_b32 s94, 0x40000000, s19\n" \
"s_bitcmp1_b32 s21, 23\n" \
"s_cselect_b32 s95, 0x40000000, s19\n" \
"s_bitcmp1_b32 s21, 8\n" \
"s_cselect_b32 s96, 0x40000000, s12\n" \
"s_bitcmp1_b32 s21, 9\n" \
"s_cselect_b32 s97, 0x40000000, s12\n" \
"s_add_u32 s64, s64, 0x1000\n" \
"s_add_u32 s65, s65, 0x800\n" \
"v_mfma_f32_16x16x32_bf16 v[172:175], a[28:31], a[148:151], v[172:175]\n" \
"buffer_load_dwordx4 v[112:115], v20, s[56:59], s70 offen offset:0 sc1\n" \
"v_mfma_f32_16x16x32_bf16 v[240:243], a[24:27], a[160:163], v[240:243]\n" \
"v_mfma_f32_16x16x32_bf16 v[240:243], a[28:31], a[164:167], v[240:243]\n" \
"v_mfma_f32_16x16x32_bf16 v[244:247], a[24:27], a[176:179], v[244:247]\n" \
"v_mfma_f32_16x16x32_bf16 v[244:247], a[28:31], a[180:183], v[244:247]\n" \
"buffer_load_dwordx4 v[184:187], v20, s[56:59], s71 offen offset:2048 sc1\n" \
"v_mfma_f32_16x16x32_bf16 v[244:247], a[192:195], a[184:187], v[244:247]\n" \
"v_readlane_b32 s71, v11, 54\n" \
"v_mfma_f32_16x16x32_bf16 v[244:247], a[196:199], a[188:191], v[244:247]\n" \
"v_mfma_f32_16x16x32_bf16 v[240:243], a[192:195], a[168:171], v[240:243]\n" \
"v_mfma_f32_16x16x32_bf16 v[240:243], a[196:199], a[172:175], v[240:243]\n" \
"buffer_load_dwordx4 v[120:123], v20, s[56:59], s72 offen offset:0 sc1\n" \
"v_mfma_f32_16x16x32_bf16 v[172:175], a[192:195], a[152:155], v[172:175]\n" \
"v_readlane_b32 s72, v11, 45\n" \
"v_mfma_f32_16x16x32_bf16 v[172:175], a[196:199], a[156:159], v[172:175]\n" \
"v_mfma_f32_16x16x32_bf16 v[168:171], a[192:195], a[136:139], v[168:171]\n" \
"v_mfma_f32_16x16x32_bf16 v[168:171], a[196:199], a[140:143], v[168:171]\n" \
"buffer_load_dwordx4 v[192:195], v20, s[56:59], s73 offen offset:2048 sc1\n" \
"v_mfma_f32_16x16x32_bf16 v[168:171], a[200:203], a[32:35], v[168:171]\n" \
"v_readlane_b32 s73, v11, 43\n" \
"s_waitcnt lgkmcnt(0)\n" \
"v_mfma_f32_16x16x32_bf16 v[168:171], a[204:207], a[36:39], v[168:171]\n" \
"v_mfma_f32_16x16x32_bf16 v[172:175], a[200:203], a[48:51], v[172:175]\n" \
"v_mfma_f32_16x16x32_bf16 v[172:175], a[204:207], a[52:55], v[172:175]\n" \
"buffer_load_dwordx4 v[128:131], v20, s[56:59], s92 offen offset:0 sc1\n" \
"v_mfma_f32_16x16x32_bf16 v[240:243], a[200:203], a[96:99], v[240:243]\n" \
"v_readlane_b32 s92, v11, 44\n" \
"v_mfma_f32_16x16x32_bf16 v[240:243], a[204:207], a[100:103], v[240:243]\n" \
"v_mfma_f32_16x16x32_bf16 v[244:247], a[200:203], a[112:115], v[244:247]\n" \
"v_mfma_f32_16x16x32_bf16 v[244:247], a[204:207], a[116:119], v[244:247]\n" \
"buffer_load_dwordx4 v[200:203], v20, s[56:59], s93 offen offset:2048 sc1\n" \
"v_mfma_f32_16x16x32_bf16 v[244:247], a[208:211], a[120:123], v[244:247]\n" \
"v_mfma_f32_16x16x32_bf16 v[244:247], a[212:215], a[124:127], v[244:247]\n" \
"v_mfma_f32_16x16x32_bf16 v[240:243], a[208:211], a[104:107], v[240:243]\n" \
"v_mfma_f32_16x16x32_bf16 v[240:243], a[212:215], a[108:111], v[240:243]\n" \
".fill (L_ab + 45056 - .) / 4, 4, 0xBF800000\n" \
"L_ap11:\n" \
"s_cbranch_execz L_ap0\n" \
"buffer_load_dwordx4 v[176:179], v20, s[56:59], s94 offen offset:0 sc1\n" \
"s_waitcnt vmcnt(17)\n" \
"s_mov_b64 exec, 1\n" \
"buffer_store_dword v39, off, s[56:59], s69 offset:48 sc1\n" \
"s_mov_b64 exec, -1\n" \
"v_mfma_f32_16x16x32_bf16 v[172:175], a[208:211], a[56:59], v[172:175]\n" \
"v_mfma_f32_16x16x32_bf16 v[172:175], a[212:215], a[60:63], v[172:175]\n" \
"v_mfma_f32_16x16x32_bf16 v[168:171], a[208:211], a[40:43], v[168:171]\n" \
"v_mfma_f32_16x16x32_bf16 v[168:171], a[212:215], a[44:47], v[168:171]\n" \
"buffer_load_dwordx4 v[248:251], v20, s[56:59], s95 offen offset:2048 sc1\n" \
"v_mfma_f32_16x16x32_bf16 v[168:171], a[216:219], v[64:67], v[168:171]\n" \
"v_mfma_f32_16x16x32_bf16 v[172:175], a[216:219], v[72:75], v[172:175]\n" \
"v_mfma_f32_16x16x32_bf16 v[240:243], a[216:219], v[80:83], v[240:243]\n" \
"v_mfma_f32_16x16x32_bf16 v[244:247], a[216:219], v[88:91], v[244:247]\n" \
"buffer_load_dwordx4 v[188:191], v20, s[56:59], s96 offen offset:1024 sc1\n" \
"v_mfma_f32_16x16x32_bf16 v[168:171], a[0:3], v[68:71], v[168:171]\n" \
"v_mfma_f32_16x16x32_bf16 v[172:175], a[0:3], v[76:79], v[172:175]\n" \
"v_mfma_f32_16x16x32_bf16 v[240:243], a[0:3], v[84:87], v[240:243]\n" \
"v_mfma_f32_16x16x32_bf16 v[244:247], a[0:3], v[92:95], v[244:247]\n" \
"buffer_load_dwordx4 v[196:199], v20, s[56:59], s97 offen offset:3072 sc1\n" \
"v_mfma_f32_16x16x32_bf16 v[56:59], a[4:7], v[96:99], 0\n" \
"v_mfma_f32_16x16x32_bf16 v[60:63], a[4:7], v[104:107], 0\n" \
"v_mfma_f32_16x16x32_bf16 v[56:59], a[8:11], v[100:103], v[56:59]\n" \
"v_mfma_f32_16x16x32_bf16 v[60:63], a[8:11], v[108:111], v[60:63]\n" \
"s_waitcnt vmcnt(16)\n" \
"s_mov_b64 exec, 1\n" \
"buffer_store_dword v39, off, s[56:59], s64 offset:52 sc1\n" \
"s_mov_b64 exec, -1\n" \
"s_waitcnt vmcnt(14)\n" \
"s_mov_b64 exec, 1\n" \
"buffer_store_dword v39, off, s[56:59], s65 offset:56 sc1\n" \
"s_mov_b64 exec, -1\n" \
"s_cmp_lg_u32 s20, 0\n" \
"s_cbranch_scc1 L_sna_%=\n" \
"L_rna_%=:\n" \
"s_cmp_lg_u32 s21, 0\n" \
"s_cbranch_scc1 L_snx_%=\n" \
"L_rnx_%=:\n" \
"s_waitcnt vmcnt(14)\n" \
"v_cmp_eq_u32_e64 s[98:99], s62, v37\n" \
"v_mfma_f32_16x16x32_bf16 v[168:171], v[50:53], v[116:119], v[168:171]\n" \
"s_andn2_b32 s75, 0x400, s98\n" \
"s_cbranch_scc1 L_sx0_%=\n" \
"L_rx0_%=:\n" \
"s_add_u32 s66, s63, 0x1000\n" \
"v_mfma_f32_16x16x32_bf16 v[172:175], v[48:51], v[116:119], v[172:175]\n" \
"buffer_load_dwordx4 v[204:207], v20, s[56:59], s66 offen offset:1024 sc1\n" \
"s_andn2_b32 s75, 0x800, s98\n" \
"s_cbranch_scc1 L_sx1_%=\n" \
"L_rx1_%=:\n" \
"s_add_u32 s67, s63, 0x1800\n" \
"v_mfma_f32_16x16x32_bf16 v[168:171], v[50:53], v[124:127], v[168:171]\n" \
"v_mfma_f32_16x16x32_bf16 v[172:175], v[48:51], v[124:127], v[172:175]\n" \
"v_mfma_f32_16x16x32_bf16 v[168:171], v[50:53], v[132:135], v[168:171]\n" \
"buffer_load_dwordx4 v[212:215], v20, s[56:59], s67 offen offset:1024 sc1\n" \
"s_andn2_b32 s75, 0x1000, s98\n" \
"s_cbranch_scc1 L_sx2_%=\n" \
"L_rx2_%=:\n" \
"s_add_u32 s68, s63, 0x2000\n" \
"v_mfma_f32_16x16x32_bf16 v[172:175], v[48:51], v[132:135], v[172:175]\n" \
"v_mfma_f32_16x16x32_bf16 v[168:171], v[50:53], v[140:143], v[168:171]\n" \
"v_mfma_f32_16x16x32_bf16 v[172:175], v[48:51], v[140:143], v[172:175]\n" \
"buffer_load_dwordx4 v[220:223], v20, s[56:59], s68 offen offset:1024 sc1\n" \
"s_andn2_b32 s75, 0x2000, s98\n" \
"s_cbranch_scc1 L_sx3_%=\n" \
"L_rx3_%=:\n" \
"s_add_u32 s69, s63, 0x2800\n" \
"v_mfma_f32_16x16x32_bf16 v[168:171], v[50:53], v[148:151], v[168:171]\n" \
"v_mfma_f32_16x16x32_bf16 v[172:175], v[48:51], v[148:151], v[172:175]\n" \
"v_mfma_f32_16x16x32_bf16 v[168:171], v[50:53], v[156:159], v[168:171]\n" \
"buffer_load_dwordx4 v[228:231], v20, s[56:59], s69 offen offset:1024 sc1\n" \
"s_andn2_b32 s75, 0x4000, s98\n" \
"s_cbranch_scc1 L_sx4_%=\n" \
"L_rx4_%=:\n" \
"s_add_u32 s70, s63, 0x3000\n" \
"v_mfma_f32_16x16x32_bf16 v[172:175], v[48:51], v[156:159], v[172:175]\n" \
"v_mfma_f32_16x16x32_bf16 v[168:171], v[50:53], v[164:167], v[168:171]\n" \
"v_mfma_f32_16x16x32_bf16 v[172:175], v[48:51], v[164:167], v[172:175]\n" \
"buffer_load_dwordx4 v[236:239], v20, s[56:59], s70 offen offset:1024 sc1\n" \
"s_waitcnt vmcnt(17)\n" \
"v_mfma_f32_16x16x32_bf16 v[56:59], v[50:53], v[112:115], v[56:59]\n" \
"v_mfma_f32_16x16x32_bf16 v[60:63], v[48:51], v[112:115], v[60:63]\n" \
"s_waitcnt vmcnt(16)\n" \
"v_mfma_f32_16x16x32_bf16 v[56:59], v[50:53], v[184:187], v[56:59]\n" \
"v_mfma_f32_16x16x32_bf16 v[60:63], v[48:51], v[184:187], v[60:63]\n" \
"v_cvt_pk_bf16_f32 v168, v168, v169\n" \
"s_waitcnt vmcnt(15)\n" \
"v_mfma_f32_16x16x32_bf16 v[56:59], v[50:53], v[120:123], v[56:59]\n" \
"v_cvt_pk_bf16_f32 v172, v172, v173\n" \
"v_cvt_pk_bf16_f32 v169, v170, v171\n" \
"v_mfma_f32_16x16x32_bf16 v[60:63], v[48:51], v[120:123], v[60:63]\n" \
"v_cvt_pk_bf16_f32 v173, v174, v175\n" \
"s_waitcnt vmcnt(14)\n" \
"v_mfma_f32_16x16x32_bf16 v[56:59], v[50:53], v[192:195], v[56:59]\n" \
"buffer_store_dwordx2 v[168:169], v42, s[88:91], s73 offen sc1\n" \
"v_mfma_f32_16x16x32_bf16 v[60:63], v[48:51], v[192:195], v[60:63]\n" \
"s_waitcnt vmcnt(14)\n" \
"v_mfma_f32_16x16x32_bf16 v[56:59], v[50:53], v[128:131], v[56:59]\n" \
"buffer_store_dwordx2 v[172:173], v43, s[88:91], s73 offen sc1\n" \
"v_mfma_f32_16x16x32_bf16 v[60:63], v[48:51], v[128:131], v[60:63]\n" \
"s_waitcnt vmcnt(14)\n" \
"v_mfma_f32_16x16x32_bf16 v[56:59], v[50:53], v[200:203], v[56:59]\n" \
"v_mfma_f32_16x16x32_bf16 v[60:63], v[48:51], v[200:203], v[60:63]\n" \
"s_waitcnt vmcnt(13)\n" \
"v_mfma_f32_16x16x32_bf16 v[56:59], v[50:53], v[176:179], v[56:59]\n" \
"v_mfma_f32_16x16x32_bf16 v[60:63], v[48:51], v[176:179], v[60:63]\n" \
"s_waitcnt vmcnt(11)\n" \
"v_mfma_f32_16x16x32_bf16 v[56:59], v[50:53], v[248:251], v[56:59]\n" \
"v_mfma_f32_16x16x32_bf16 v[60:63], v[48:51], v[248:251], v[60:63]\n" \
"s_waitcnt vmcnt(10)\n" \
"v_mfma_f32_16x16x32_bf16 v[240:243], v[50:53], v[188:191], v[240:243]\n" \
"v_mfma_f32_16x16x32_bf16 v[244:247], v[48:51], v[188:191], v[244:247]\n" \
"s_waitcnt vmcnt(9)\n" \
"v_mfma_f32_16x16x32_bf16 v[240:243], v[50:53], v[196:199], v[240:243]\n" \
"v_mfma_f32_16x16x32_bf16 v[244:247], v[48:51], v[196:199], v[244:247]\n" \
"s_mov_b64 exec, 1\n" \
"buffer_store_dword v46, off, s[56:59], s71 offset:0 sc1\n" \
"s_mov_b64 exec, -1\n" \
"v_cvt_pk_bf16_f32 v56, v56, v57\n" \
"v_cvt_pk_bf16_f32 v60, v60, v61\n" \
"v_cvt_pk_bf16_f32 v57, v58, v59\n" \
"v_cvt_pk_bf16_f32 v61, v62, v63\n" \
"buffer_store_dwordx2 v[56:57], v42, s[88:91], s72 offen sc1\n" \
"buffer_store_dwordx2 v[60:61], v43, s[88:91], s72 offen sc1\n" \
"s_waitcnt vmcnt(9)\n" \
"v_mfma_f32_16x16x32_bf16 v[240:243], v[50:53], v[204:207], v[240:243]\n" \
"v_mfma_f32_16x16x32_bf16 v[244:247], v[48:51], v[204:207], v[244:247]\n" \
"s_waitcnt vmcnt(8)\n" \
"v_mfma_f32_16x16x32_bf16 v[240:243], v[50:53], v[212:215], v[240:243]\n" \
"v_mfma_f32_16x16x32_bf16 v[244:247], v[48:51], v[212:215], v[244:247]\n" \
"s_waitcnt vmcnt(7)\n" \
"v_mfma_f32_16x16x32_bf16 v[240:243], v[50:53], v[220:223], v[240:243]\n" \
"v_mfma_f32_16x16x32_bf16 v[244:247], v[48:51], v[220:223], v[244:247]\n" \
"s_waitcnt vmcnt(6)\n" \
"v_mfma_f32_16x16x32_bf16 v[240:243], v[50:53], v[228:231], v[240:243]\n" \
"v_mfma_f32_16x16x32_bf16 v[244:247], v[48:51], v[228:231], v[244:247]\n" \
"s_waitcnt vmcnt(5)\n" \
"v_mfma_f32_16x16x32_bf16 v[240:243], v[50:53], v[236:239], v[240:243]\n" \
"v_mfma_f32_16x16x32_bf16 v[244:247], v[48:51], v[236:239], v[244:247]\n" \
"s_nop 6\n" \
"v_cvt_pk_bf16_f32 v240, v240, v241\n" \
"v_cvt_pk_bf16_f32 v244, v244, v245\n" \
"v_cvt_pk_bf16_f32 v241, v242, v243\n" \
"v_cvt_pk_bf16_f32 v245, v246, v247\n" \
"buffer_store_dwordx2 v[240:241], v42, s[88:91], s92 offen sc1\n" \
"buffer_store_dwordx2 v[244:245], v43, s[88:91], s92 offen sc1\n"
#define ATT_VCLOB "v21","v22","v23","v24","v25","v26","v27","v28","v29","v32","v33","v34","v35","v36","v37","v38","v39","v40","v41","v42","v43","v44","v45","v46","v47","v48","v49","v50","v51","v52","v53","v54","v55","v56","v57","v58","v59","v60","v61","v62","v63","v64","v65","v66","v67","v68","v69","v70","v71","v72","v73","v74","v75","v76","v77","v78","v79","v80","v81","v82","v83","v84","v85","v86","v87","v88","v89","v90","v91","v92","v93","v94","v95","v96","v97","v98","v99","v100","v101","v102","v103","v104","v105","v106","v107","v108","v109","v110","v111","v112","v113","v114","v115","v116","v117","v118","v119","v120","v121","v122","v123","v124","v125","v126","v127","v128","v129","v130","v131","v132","v133","v134","v135","v136","v137","v138","v139","v140","v141","v142","v143","v144","v145","v146","v147","v148","v149","v150","v151","v152","v153","v154","v155","v156","v157","v158","v159","v160","v161","v162","v163","v164","v165","v166","v167","v168","v169","v170","v171","v172","v173","v174","v175","v176","v177","v178","v179","v180","v181","v182","v183","v184","v185","v186","v187","v188","v189","v190","v191","v192","v193","v194","v195","v196","v197","v198","v199","v200","v201","v202","v203","v204","v205","v206","v207","v208","v209","v210","v211","v212","v213","v214","v215","v216","v217","v218","v219","v220","v221","v222","v223","v224","v225","v226","v227","v228","v229","v230","v231","v232","v233","v234","v235","v236","v237","v238","v239","v240","v241","v242","v243","v244","v245","v246","v247","v248","v249","v250","v251","v252","v253","v254","v255"
#define ATT_SCLOB "s63","s64","s65","s66","s67","s68","s69","s70","s71","s72","s73","s74","s75","s76","s77","s78","s79","s80","s81","s82","s83","s84","s85","s86","s87","s88","s89","s90","s91","s92","s93","s95","s96","s97","s98","s99","s100","s101"
#define ATT_WTCH 0
#define ATT_PEEL 1
#define ATT_PEEL_FOW 1
enum { T_fo, T_fj, T_fa, T_fas, T_fb, T_fbs, T_f8, T_mk8, T_wt, T_wol, T_woh, T_wbl, T_wbh, T_outl, T_outh, T_r1l, T_r1h, T_r1n, T_r1s, T_m0, T_m1, T_m1b, T_mw, T_m3, T_m4, T_ml, T_w0, T_w1, T_w2, T_w3, T_A1, T_A2, T_A3, T_B1, T_B2, T_B3, T_Ao, T_Bo, T_Al, T_Bl, T_l8, T_xr, T_x8r, T_oA, T_oB, T_o8, T_x0, T_x1, T_x2, T_x3, T_x4, T_x5, T_x6, T_x8, T_ep };

__device__ __forceinline__ void out_consts(u32x4 (&vp)[3], const u16* wo, const u16* wob, const u16* out, const unsigned b, const unsigned w, const unsigned lane) {
    const unsigned sp = b & 7u, nt = (b >> 3) & 3u, j = b >> 5, head = 4u * sp + nt, OOR = 0x40000000u;
    unsigned vw[9];
#pragma unroll
    for (unsigned i = 0; i < 9u; ++i) {
        const unsigned q = 64u * i + lane, row = __umul24(q, 3641u) >> 16, c = q - __umul24(row, 18u);
        vw[i] = __umul24(row, DM * 2) + (c < 16u ? (c * 16u + sp * 32u + 32u) & 255u : c * 16u) - (i & 3u) * 1024u;
    }
    const unsigned row = 16u * w + (lane >> 2), r9 = __umul24(16u * (row >> 5) + (row & 12u) + 2u * ((row >> 4) & 1u) + ((row >> 1) & 1u), 18u) + 16u + (row & 1u);
    unsigned vwl = __umul24(r9, DM * 2) + ((lane & 2u) ? 256u : sp * 32u) + (lane & 1u) * 16u, vrd = __umul24(lane >> 2, 288u) + (lane & 3u) * 8u;
    unsigned tab = 0;
#define TB(n, v) asm("v_writelane_b32 %0, %1, %2" : "+v"(tab) : "s"((unsigned)(v)), "n"((int)T_##n))
    const unsigned FJ = (unsigned)WS_F + (F_J + 640u * j) * 4u, mt = 2u * (w & 2u) + (w & 1u), w2 = w < 2u ? w : 2u, hw = head * 147456u;
    const unsigned FR = (unsigned)WS_F + F_R * 4u + (8u * j + sp) * 1024u;
    TB(fo, FJ + (ATT_PEEL_FOW ? 16u * head + 4u * w : 4u * head)); TB(fj, FJ); TB(fa, FR + (w | 2u) * 256u); TB(fas, FR + w * 256u + nt * 4u);
    TB(fb, FR + 128u + w * 256u); TB(fbs, FR + 128u + w * 256u + nt * 4u);
    TB(ep, ssel(b + w, OOR, (unsigned)WS_F + F_EPOCH * 4u));
    const u16* pwo = wo + nt * 144u; const u16* pwb = wob + nt * 144u;
    TB(wol, PLO(pwo)); TB(woh, PHI(pwo) & 0xffffu); TB(wbl, PLO(pwb)); TB(wbh, PHI(pwb) & 0xffffu); TB(outl, PLO(out)); TB(outh, PHI(out) & 0xffffu); TB(r1l, sslt<3>(w, PLO(pwo), PLO(pwb))); TB(r1h, sslt<3>(w, PHI(pwo), PHI(pwb)) & 0xffffu);
    TB(r1n, sslt<3>(w, 72u * sp + 71u, 4095u) * 1152u + 288u); TB(r1s, sslt<3>(w, (72u * sp + 32u * w) * 1152u, hw + 110592u));
    TB(m0, 131072u + 9216u * w2); TB(w0, hw + 36864u * w2); TB(m1, ATT_PEEL ? sslt<3>(w, 41984u + 9216u * w, 32768u) : 24576u + 9216u * w); TB(m1b, ssel(w ^ 2u, sslt<3>(w, 41984u + 9216u * w, 32768u), 114688u - 4096u)); TB(mw, 65536u + 9216u * w); TB(m3, sslt<3>(w, 102400u + 9216u * w, 0u)); TB(m4, sslt<3>(w, 131072u + 9216u * w, 9216u)); TB(ml, (ATT_PEEL ? 18432u : 61440u) + 1024u * w);
    const unsigned tA = (unsigned)WS_AF + (8u * j + mt) * (KS_TOT * 1024u), tB = (unsigned)WS_AF + (120u - 8u * j + mt) * (KS_TOT * 1024u);
#define HK(k) (4u * sp + ((nt + k) & 3u))
    TB(w1, HK(1u) * 147456u + w * 36864u); TB(w2, HK(2u) * 147456u + w * 36864u); TB(w3, HK(3u) * 147456u + w * 36864u);
    TB(A1, tA + 18432u + HK(1u) * 4096u); TB(A2, tA + 18432u + HK(2u) * 4096u); TB(A3, tA + 18432u + HK(3u) * 4096u);
    TB(B1, tB + 18432u + HK(1u) * 4096u); TB(B2, tB + 18432u + HK(2u) * 4096u); TB(B3, tB + 18432u + HK(3u) * 4096u);
    TB(Ao, tA + sp * 2048u); TB(Bo, tB + sp * 2048u); TB(Al, tA + 16384u); TB(Bl, tB + 16384u);
    const unsigned nred = w ^ (sp >> 1), blk = sp & 1u;
    const unsigned xg = (unsigned)WS_X + (4u * j + nt) * 589824u + w * 16384u, oA = (128u * j + 16u * mt) * 1152u + nt * 288u, oB = (1920u - 128u * j + 16u * mt) * 1152u + nt * 288u;
    TB(l8, ssel(nred, OOR, ssel(blk, tB, tA) + 16384u)); TB(xr, xg + sp * 65536u); TB(x8r, ssel(nred, OOR, xg + 524288u + (sp & 1u) * 1024u));
    TB(oA, oA + sp * 32u); TB(oB, oB + sp * 32u); TB(o8, ssel(nred, OOR, ssel(blk, oB, oA) + 256u)); TB(x8, xg + 524288u + sp * 2048u);
    TB(f8, xg + w * 131072u + 14400u + sp * 4u); TB(mk8, ssel(nred, 0u, 0xff0000u)); TB(wt, hw + j * 18432u + w * 4608u);
#define XI(i) TB(x##i, xg + ((sp + 1u + i) & 7u) * 65536u + i * 2048u)
    XI(0); XI(1); XI(2); XI(3); XI(4); XI(5); XI(6);
#undef XI
#undef HK
#undef TB
    vp[0] = (u32x4){vw[0], vw[1], vw[2], vw[3]}; vp[1] = (u32x4){vw[4], vw[5], vw[6], vw[7]}; vp[2] = (u32x4){vw[8], vwl, vrd, tab};
}
__global__ __launch_bounds__(256, 1) __attribute__((aligned(4096))) void fused_kernel(const u16* __restrict__ x, const u16* __restrict__ ckv, const u16* __restrict__ wq, const u16* __restrict__ wuk,
                                                       const u16* __restrict__ wuv, const u16* __restrict__ wqkv, const u16* __restrict__ gw,
                                                       const u16* __restrict__ wo, const u16* __restrict__ wob, char* __restrict__ ws_, u16* __restrict__ out) {
    using namespace att;
    extern __shared__ __attribute__((aligned(16))) char lds[];
    u32x4 sa[5]; u32x2 s24, s28; char* ws; unsigned tdum; kaptr ka = (kaptr)__builtin_amdgcn_kernarg_segment_ptr();
    asm volatile("s_load_dwordx8 s[8:15], %[ka], 0x0\n\ts_load_dwordx8 s[16:23], %[ka], 0x20\n\ts_getpc_b64 s[4:5]\n\tv_lshlrev_b32_e32 v191, 7, %[t]\n\ts_bfe_u32 s6, %[b], 0x20003\n\t"
                 "v_lshl_add_u32 v191, s6, 15, v191\n\tv_min_u32_e32 v191, %[e], v191\n\tglobal_load_dword v191, v191, s[4:5]\n\ts_load_dwordx4 s[24:27], %[ka], 0x40\n\ts_load_dwordx2 s[28:29], %[ka], 0x50"
                 : "={s[8:11]}"(sa[0]), "={s[12:15]}"(sa[1]), "={s[16:19]}"(sa[2]), "={s[20:23]}"(sa[3]), "={s[24:25]}"(s24), "={s[26:27]}"(ws), "={s[28:29]}"(s28), "={v191}"(tdum), [ka] "+{s[0:1]}"(ka)
                 : [t] "v"(threadIdx.x), [b] "s"(blockIdx.x), [e] "i"(CT_END) : "s4", "s5", "s6", "scc");
    SBAR();
    const unsigned wid = rfl(threadIdx.x >> 6);
    unsigned tag, vl;
    const int b = blockIdx.x;
    u32x4 vp[5], srdA; u32x2 sd;
    proj_phase(ka, ws, wid, vp, vl, sa, s24, s28, tdum, srdA, sd, tag
    );
    unsigned* const flags = (unsigned*)(ws + WS_F);
    RTS(2);
#define AST(L) do {} while (0)
#define ATT_TAIL "s_endpgm\n"
    unsigned abad;
    asm volatile(ATT_ASM ATT_TAIL "L_end_%=:\n s_nop 1"
                 : "={s94}"(abad)
                 : "{v[0:3]}"(vp[0]), "{v[4:7]}"(vp[1]), "{v[8:11]}"(vp[2]), "{v[12:15]}"(vp[3]), "{v[16:19]}"(vp[4]), "{v20}"(vl), "{s[56:59]}"(srdA), "{s[60:61]}"(sd), "{s62}"(tag),
                   "{s[8:11]}"(sa[0]), "{s[12:15]}"(sa[1]), "{s[16:19]}"(sa[2]), "{s[20:23]}"(sa[3]), "{s24}"(sa[4][0]), "{s3}"(wid), "{s[28:29]}"(s28)
                 : "memory", "m0", "vcc", "scc", ATT_VCLOB, ATT_SCLOB, ACLOB_LO, ACLOB_HI);
    {
        unsigned b2 = b, w2 = wid;
        asm volatile("s_waitcnt vmcnt(0)" : "+s"(ka), "+s"(b2), "+s"(w2), "+s"(ws) :: "memory");
        const unsigned j2 = b2 >> 5, p2 = w2 & 1u, h2 = (b2 & 7u) * 4u + ((b2 >> 3) & 3u);
#pragma unroll 1
        for (unsigned i = w2 & 2u; i < ((w2 & 2u) ? (abad ? 8u : 4u) : 2u); ++i)
            att_exact(ka[0], ka[2], ws, h2, (i < 4u ? 1920u - 128u * j2 : 128u * j2 - 128u) + 32u * i + 16u * p2, *reinterpret_cast<const float*>(lds + LDS_G + (32u * i + 16u * p2 + (lane_id() & 15u)) * 4u));
        asm volatile("s_waitcnt vmcnt(0)" ::: "memory");
        unsigned* fa = (unsigned*)(ws + WS_F) + F_R + (8u * j2 + (b2 & 7u)) * 256u + (2u + p2) * 64u + ((b2 >> 3) & 3u);
        if (w2 & 2u) st_flag(fa, tag);
        wait_flags(fa, ld_flag(fa), tag);
        const unsigned l2 = lane_id(); u32x4 c[3]; out_consts(c, ka[7], ka[8], ka[10], b2, w2, l2);
        asm volatile("s_waitcnt vmcnt(0) lgkmcnt(0)\n\ts_nop 7\n\ts_branch L_att_stub" :: "{v[0:3]}"(c[0]), "{v[4:7]}"(c[1]), "{v[8:11]}"(c[2]), "{v20}"(l2 * 16u), "{s[56:59]}"(SRDA), "{s60}"(SD(8u * j2, h2, w2)),
                     "{s61}"(SD(120u - 8u * j2, h2, w2)), "{s62}"(tag), "{s3}"(w2) : "memory");
    }
    RTS(5);
}

extern "C" void kernel_launch(void* const* d_in, const int* in_sizes, int n_in, void* d_out, int out_size, void* d_ws, size_t ws_size,
                              hipStream_t stream) {
    (void)hipFuncSetAttribute((const void*)fused_kernel, hipFuncAttributeMaxDynamicSharedMemorySize, att::LDS_BYTES);
    fused_kernel<<<dim3(256), 256, att::LDS_BYTES, stream>>>((const u16*)d_in[0], (const u16*)d_in[4], (const u16*)d_in[5], (const u16*)d_in[6], (const u16*)d_in[7], (const u16*)d_in[1],
                                                             (const u16*)d_in[9], (const u16*)d_in[2], (const u16*)d_in[8], (char*)d_ws, (u16*)d_out);
}
```

```cpp
#include <hip/hip_runtime.h>
#include <hip/hip_bf16.h>
#include <stdint.h>
#include <utility>

typedef short bf16x8 __attribute__((ext_vector_type(8)));
typedef short s16x4 __attribute__((ext_vector_type(4)));
typedef float f32x16 __attribute__((ext_vector_type(16)));
typedef float f32x4 __attribute__((ext_vector_type(4)));
typedef unsigned u32x4 __attribute__((ext_vector_type(4)));
typedef unsigned u32x2 __attribute__((ext_vector_type(2)));
typedef unsigned short u16;

constexpr int S = 2048, DM = 576, NH = 32, HD = 128, DC = 512, NQKV = 1728, DHA = 18;
constexpr int NBIG = NH * HD;
constexpr int KS_TOT = 146;

#define SBAR() __builtin_amdgcn_sched_barrier(0)
constexpr size_t WS_Q = 0, WS_K = WS_Q + (size_t)S * NBIG * 2, WS_V = WS_K + (size_t)S * NBIG * 2, WS_AF = WS_V + (size_t)S * NBIG * 2, WS_X = WS_AF + (size_t)128 * KS_TOT * 1024,
                 WS_P = WS_X + (size_t)64 * 16 * 9216, WS_F = WS_P + (size_t)128 * (4 * 32 * 256 + 1024) * 4;
constexpr size_t WS_L = (size_t)96 << 20;
static_assert(WS_F == 96206848, "workspace map is baked into the generated asm");
constexpr int F_EPOCH = 0, F_J = 1024, F_ST = 8192, F_KV = 40960, F_R = 65536;
constexpr unsigned TAG_STEP = 0x9E3779B1u;
__device__ __forceinline__ unsigned ld_flag(const unsigned* p) { return __hip_atomic_load(p, __ATOMIC_RELAXED, __HIP_MEMORY_SCOPE_AGENT); }
__device__ __forceinline__ void st_flag(unsigned* p, unsigned v) { __hip_atomic_store(p, v, __ATOMIC_RELAXED, __HIP_MEMORY_SCOPE_AGENT); }
__device__ __forceinline__ void wait_flags(const unsigned* p, unsigned v, unsigned tag) {
    if (__builtin_expect(__builtin_amdgcn_ballot_w64(v != tag) != 0ull, 0)) {
        unsigned spins = 0;
        do { __builtin_amdgcn_s_sleep(1); } while (__builtin_amdgcn_ballot_w64(ld_flag(p) != tag) != 0ull && ++spins < (1u << 22));
    }
    asm volatile("" ::: "memory");
}

__device__ __forceinline__ unsigned lane_id() { unsigned l; asm volatile("v_mbcnt_lo_u32_b32 %0, -1, 0\n\tv_mbcnt_hi_u32_b32 %0, -1, %0" : "=v"(l)); return l; }
#define RTS(slot) do {} while (0)
#define CT_END ((PROJ_PAGES + ATT_PAGES) * 4096 + (ATT_TAILB & ~127) - 128)
#define ACLOB_LO "a0","a1","a2","a3","a4","a5","a6","a7","a8","a9","a10","a11","a12","a13","a14","a15","a16","a17","a18","a19","a20","a21","a22","a23","a24","a25","a26","a27","a28","a29","a30","a31","a32","a33","a34","a35","a36","a37","a38","a39","a40","a41","a42","a43","a44","a45","a46","a47","a48","a49","a50","a51","a52","a53","a54","a55","a56","a57","a58","a59","a60","a61","a62","a63","a64","a65","a66","a67","a68","a69","a70","a71","a72","a73","a74","a75","a76","a77","a78","a79","a80","a81","a82","a83","a84","a85","a86","a87","a88","a89","a90","a91","a92","a93","a94","a95","a96","a97","a98","a99","a100","a101","a102","a103","a104","a105","a106","a107","a108","a109","a110","a111","a112","a113","a114","a115","a116","a117","a118","a119","a120","a121","a122","a123","a124","a125","a126","a127"
#define ACLOB_HI "a128","a129","a130","a131","a132","a133","a134","a135","a136","a137","a138","a139","a140","a141","a142","a143","a144","a145","a146","a147","a148","a149","a150","a151","a152","a153","a154","a155","a156","a157","a158","a159","a160","a161","a162","a163","a164","a165","a166","a167","a168","a169","a170","a171","a172","a173","a174","a175","a176","a177","a178","a179","a180","a181","a182","a183","a184","a185","a186","a187","a188","a189","a190","a191","a192","a193","a194","a195","a196","a197","a198","a199","a200","a201","a202","a203","a204","a205","a206","a207","a208","a209","a210","a211","a212","a213","a214","a215","a216","a217","a218","a219","a220","a221","a222","a223","a224","a225","a226","a227","a228","a229","a230","a231","a232","a233","a234","a235","a236","a237","a238","a239","a240","a241","a242","a243","a244","a245","a246","a247","a248","a249","a250","a251","a252","a253","a254","a255"
#define M_ "v_mfma_f32_16x16x32_bf16 "
#define X_ "ds_read_b64_tr_b16 "
#define B_ "buffer_load_dwordx4 "
#define C_ "v_cvt_pk_bf16_f32 "
#define R_ "ds_read_b128 "
#define E_ "v_exp_f32_e32 "
#define PLO(p) ((unsigned)(size_t)(p))
#define PHI(p) ((unsigned)((size_t)(p) >> 32))
__device__ __forceinline__ unsigned vsel(bool c, unsigned a, unsigned b) { asm("" : "+v"(a), "+v"(b)); return c ? a : b; }
__device__ __forceinline__ unsigned ssel(unsigned c, unsigned a, unsigned b) { unsigned r; asm("s_cmp_lg_u32 %1, 0\n\ts_cselect_b32 %0, %2, %3" : "=s"(r) : "s"(c), "s"(a), "s"(b) : "scc"); return r; }
template <int N> __device__ __forceinline__ unsigned sslt(unsigned c, unsigned a, unsigned b) { unsigned r; asm("s_cmpk_lt_u32 %1, %4\n\ts_cselect_b32 %0, %2, %3" : "=s"(r) : "s"(c), "s"(a), "s"(b), "n"(N) : "scc"); return r; }
namespace att {
constexpr int LDS_BYTES = 163840;
constexpr unsigned LDS_G = 159744u, LDS_L = 161792u;
}
#define SD(J8, H, W) ((unsigned)WS_AF + (((J8) + 2u * ((W) & 2u) + ((W) & 1u)) * KS_TOT + 18u + 4u * (H)) * 1024u)
#define SRDA (u32x4){PLO(ws), PHI(ws) & 0xffffu, (unsigned)WS_L, 0x00020000u}
#define PROJ_ASM \
".p2align 3\n" \
"L_pb:\n" \
"s_waitcnt lgkmcnt(0)\n" \
"s_mov_b32 s64, s10\n" \
"s_and_b32 s65, s11, 0xffff\n" \
"s_mov_b32 s66, 0x200000\n" \
"s_mov_b32 s67, 0x20000\n" \
"s_mov_b32 s68, s14\n" \
"s_and_b32 s69, s15, 0xffff\n" \
"s_mov_b32 s70, 0x400000\n" \
"s_mov_b32 s71, 0x20000\n" \
"s_mov_b32 s72, s16\n" \
"s_and_b32 s73, s17, 0xffff\n" \
"s_mov_b32 s74, 0x400000\n" \
"s_mov_b32 s75, 0x20000\n" \
"s_add_u32 m0, s30, 0x0\n" \
"s_movk_i32 s84, 0x0\n" \
B_"v22, s[64:67], s84 offen offset:0 lds\n" \
"s_movk_i32 s84, 0x1c00\n" \
B_"v23, s[64:67], s84 offen offset:1024 lds\n" \
"s_movk_i32 s84, 0x3800\n" \
B_"v22, s[64:67], s84 offen offset:2048 lds\n" \
"s_movk_i32 s84, 0x5400\n" \
B_"v23, s[64:67], s84 offen offset:3072 lds\n" \
"s_add_u32 m0, s31, 0x0\n" \
"s_movk_i32 s84, 0x0\n" \
B_"v26, s[68:71], s84 offen offset:0 lds\n" \
"s_movk_i32 s84, 0x7c00\n" \
B_"v26, s[68:71], s84 offen offset:1024 lds\n" \
"s_addk_i32 s84, 0x7c00\n" \
B_"v27, s[68:71], s84 offen offset:2048 lds\n" \
"s_addk_i32 s84, 0x7c00\n" \
B_"v27, s[68:71], s84 offen offset:3072 lds\n" \
"s_movk_i32 s7, 0x300\n" \
"s_lshr_b32 s4, s2, 1\n" \
"s_and_b32 s4, s4, 60\n" \
"s_or_b32 s4, s4, s3\n" \
"v_lshrrev_b32_e32 v212, 4, v20\n" \
"v_min_u32_e32 v212, 59, v212\n" \
"s_cmpk_lt_u32 s2, 0x80\n" \
"s_cselect_b32 s6, s14, s16\n" \
"v_subrev_u32_e32 v214, 48, v212\n" \
"v_mov_b32_e32 v212, s10\n" \
"v_mov_b32_e32 v215, s6\n" \
"s_cmpk_lt_u32 s2, 0x80\n" \
"s_cselect_b32 s5, s15, s17\n" \
"v_cmp_gt_u32_e32 vcc, s7, v20\n" \
"v_mov_b32_e64 v213, s11\n" \
"v_bfe_u32 v216, v20, 4, 3\n" \
"v_cndmask_b32_e32 v212, v212, v215, vcc\n" \
"v_mov_b32_e32 v215, s5\n" \
"s_lshl_b32 s5, s3, 4\n" \
"v_cndmask_b32_e32 v213, v213, v215, vcc\n" \
"v_lshlrev_b32_e64 v215, 5, v20\n" \
"v_and_b32_e32 v215, 0x7000, v215\n" \
"v_lshl_add_u32 v215, s4, 6, v215\n" \
"s_lshl_b32 s4, s2, 3\n" \
"s_and_b32 s4, s4, 56\n" \
"v_or3_b32 v215, v215, s4, v216\n" \
"s_lshl_b32 s4, s2, 6\n" \
"s_or_b32 s6, s5, s4\n" \
"v_cmp_gt_u32_e64 s[4:5], 6, v214\n" \
"v_add_u32_e32 v215, 0x2000, v215\n" \
"s_nop 0\n" \
"v_cndmask_b32_e64 v216, 4, 2, s[4:5]\n" \
"v_add3_u32 v214, s6, v214, v216\n" \
"s_lshl_b32 s4, s3, 9\n" \
"v_cndmask_b32_e32 v214, v214, v215, vcc\n" \
"v_lshlrev_b32_e32 v214, 7, v214\n" \
"v_mov_b32_e64 v215, 0\n" \
"v_lshl_add_u64 v[184:185], v[212:213], 0, v[214:215]\n" \
"s_or_b32 s32, s4, 0x25000\n" \
"s_add_u32 m0, s30, 0x1000\n" \
"s_mov_b32 s84, 0x8000\n" \
B_"v22, s[64:67], s84 offen offset:0 lds\n" \
"s_and_b32 s33, s3, 1\n" \
"v_lshrrev_b32_e32 v212, 4, v20\n" \
"v_lshrrev_b32_e64 v214, 8, v20\n" \
"v_bfe_u32 v212, v212, 1, 3\n" \
"v_lshlrev_b32_e32 v215, 2, v20\n" \
"s_addk_i32 s84, 0x1c00\n" \
B_"v23, s[64:67], s84 offen offset:1024 lds\n" \
"v_lshrrev_b32_e64 v216, 1, v20\n" \
"v_bfe_u32 v213, v20, 4, 4\n" \
"s_lshl_b32 s4, s3, 5\n" \
"v_xor_b32_e32 v212, v212, v214\n" \
"v_lshlrev_b32_e32 v214, 11, v214\n" \
"s_addk_i32 s84, 0x1c00\n" \
B_"v22, s[64:67], s84 offen offset:2048 lds\n" \
"v_and_b32_e32 v215, 0x300, v215\n" \
"v_and_b32_e64 v217, 24, v216\n" \
"v_and_or_b32 v213, s4, 64, v213\n" \
"v_or3_b32 v214, v215, v214, v217\n" \
"s_lshl_b32 s4, s33, 14\n" \
"s_addk_i32 s84, 0x1c00\n" \
B_"v23, s[64:67], s84 offen offset:3072 lds\n" \
"v_and_b32_e32 v215, 0xe0, v216\n" \
"v_lshlrev_b32_e32 v213, 7, v213\n" \
"v_lshlrev_b32_e32 v212, 4, v212\n" \
"v_or3_b32 v214, v214, s4, v215\n" \
"v_or_b32_e64 v28, v212, v213\n" \
"s_add_u32 m0, s31, 0x4000\n" \
"s_movk_i32 s84, 0x0\n" \
B_"v26, s[72:75], s84 offen offset:0 lds\n" \
"v_add_u32_e32 v32, 0x10000, v214\n" \
"v_bitop3_b32 v29, v212, 64, v213 bitop3:0x36\n" \
"v_xor_b32_e32 v33, 0x20, v32\n" \
"v_xor_b32_e64 v34, 0x40, v32\n" \
"v_xor_b32_e32 v35, 0x60, v32\n" \
"s_movk_i32 s84, 0x7c00\n" \
B_"v26, s[72:75], s84 offen offset:1024 lds\n" \
"v_xor_b32_e32 v36, 0x80, v32\n" \
"v_xor_b32_e32 v37, 0xa0, v32\n" \
"v_xor_b32_e32 v38, 0xc0, v32\n" \
"v_xor_b32_e32 v39, 0xe0, v32\n" \
"s_mov_b32 s80, s26\n" \
"s_addk_i32 s84, 0x7c00\n" \
B_"v27, s[72:75], s84 offen offset:2048 lds\n" \
"s_and_b32 s81, s27, 0xffff\n" \
"s_mov_b32 s82, 0x10000000\n" \
"s_mov_b32 s83, 0x20000\n" \
"s_mov_b32 s76, s18\n" \
"s_and_b32 s77, s19, 0xffff\n" \
"s_addk_i32 s84, 0x7c00\n" \
B_"v27, s[72:75], s84 offen offset:3072 lds\n" \
"s_mov_b32 s78, 0x1e6000\n" \
"s_mov_b32 s79, 0x20000\n" \
"s_branch L_pgo_%=\n" \
"L_pret:\n" \
"s_setpc_b64 s[88:89]\n" \
"L_pgo_%=:\n" \
"s_mul_i32 s88, s33, 0xa000\n" \
"s_lshl_b32 s89, s3, 11\n" \
"s_and_b32 s89, s89, 0x1000\n" \
"s_add_u32 s88, s88, s89\n" \
"s_getpc_b64 s[86:87]\n" \
"L_hpa_%=:\n" \
"s_add_u32 s86, s86, L_pp1-L_hpa_%=\n" \
"s_addc_u32 s87, s87, 0\n" \
"s_add_u32 s86, s86, s88\n" \
"s_addc_u32 s87, s87, 0\n" \
"s_add_u32 m0, s30, 0x8000\n" \
"s_movk_i32 s84, 0x80\n" \
B_"v22, s[64:67], s84 offen offset:0 lds\n" \
"s_lshr_b32 s4, s2, 1\n" \
"s_and_b32 s4, s4, 0x7c\n" \
"v_bfe_u32 v212, v20, 4, 5\n" \
"s_movk_i32 s84, 0x1c80\n" \
B_"v23, s[64:67], s84 offen offset:1024 lds\n" \
"s_or_b32 s4, s4, s3\n" \
"v_min_u32_e32 v212, 26, v212\n" \
"v_mad_u64_u32 v[212:213], s[4:5], s4, 27, v[212:213]\n" \
"s_movk_i32 s84, 0x3880\n" \
B_"v22, s[64:67], s84 offen offset:2048 lds\n" \
"v_mul_u32_u24_e32 v213, 0xaaab, v212\n" \
"v_lshrrev_b32_e32 v213, 18, v213\n" \
"s_mov_b32 s4, 0x1fffffa\n" \
"s_movk_i32 s84, 0x5480\n" \
B_"v23, s[64:67], s84 offen offset:3072 lds\n" \
"v_mad_u64_u32 v[214:215], s[4:5], v213, s4, v[212:213]\n" \
"s_and_b32 s6, s2, 7\n" \
"v_mul_u32_u24_e32 v213, 27, v213\n" \
"s_mov_b64 exec, 0\n" \
"s_swappc_b64 s[88:89], s[86:87]\n" \
"s_mov_b64 exec, -1\n" \
"s_add_u32 s86, s86, 0x2000\n" \
"s_addc_u32 s87, s87, 0\n" \
"s_add_u32 m0, s31, 0x8000\n" \
"s_mov_b32 s84, 0x80000\n" \
B_"v26, s[68:71], s84 offen offset:0 lds\n" \
"v_lshrrev_b32_e32 v214, 1, v214\n" \
"v_and_b32_e32 v212, 1, v212\n" \
"v_mul_u32_u24_e32 v214, 9, v214\n" \
"s_addk_i32 s84, 0x7c00\n" \
B_"v26, s[68:71], s84 offen offset:1024 lds\n" \
"v_add3_u32 v212, v212, s6, v213\n" \
"v_add_lshl_u32 v212, v212, v214, 7\n" \
"v_mov_b32_e32 v213, 0\n" \
"s_mov_b64 exec, 0\n" \
"s_swappc_b64 s[88:89], s[86:87]\n" \
"s_mov_b64 exec, -1\n" \
"s_add_u32 s86, s86, 0x2000\n" \
"s_addc_u32 s87, s87, 0\n" \
"s_addk_i32 s84, 0x7c00\n" \
B_"v27, s[68:71], s84 offen offset:2048 lds\n" \
"v_lshl_add_u64 v[186:187], s[18:19], 0, v[212:213]\n" \
"s_lshl_b32 s35, s2, 10\n" \
"s_lshr_b32 s37, s2, 1\n" \
"s_addk_i32 s84, 0x7c00\n" \
B_"v27, s[68:71], s84 offen offset:3072 lds\n" \
"s_mul_i32 s37, s37, 0x24800\n" \
"s_and_b32 s35, s35, 0x400\n" \
"s_lshl_b32 s36, s3, 8\n" \
"s_mov_b64 exec, 0\n" \
"s_swappc_b64 s[88:89], s[86:87]\n" \
"s_mov_b64 exec, -1\n" \
"s_add_u32 s86, s86, 0x2000\n" \
"s_addc_u32 s87, s87, 0\n" \
"s_add_u32 m0, s30, 0x9000\n" \
"s_mov_b32 s84, 0x8080\n" \
B_"v22, s[64:67], s84 offen offset:0 lds\n" \
"s_or_b32 s35, s37, s35\n" \
"s_and_b32 s4, s2, 7\n" \
"s_lshr_b32 s6, s2, 5\n" \
"s_addk_i32 s84, 0x1c00\n" \
B_"v23, s[64:67], s84 offen offset:1024 lds\n" \
"s_lshl_b32 s34, s2, 5\n" \
"s_add_i32 s35, s35, s36\n" \
"s_lshl_b32 s7, s4, 10\n" \
"s_addk_i32 s84, 0x1c00\n" \
B_"v22, s[64:67], s84 offen offset:2048 lds\n" \
"s_and_b32 s34, s34, 0x300\n" \
"s_add_i32 s35, s35, 0x3004000\n" \
"s_lshl_b32 s37, s6, 13\n" \
"s_mov_b64 exec, 0\n" \
"s_swappc_b64 s[88:89], s[86:87]\n" \
"s_mov_b64 exec, -1\n" \
"s_andn2_b32 s85, 2, s3\n" \
"s_lshl_b32 s85, s85, 12\n" \
"s_add_u32 s86, s86, s85\n" \
"s_addc_u32 s87, s87, 0\n" \
"s_addk_i32 s84, 0x1c00\n" \
B_"v23, s[64:67], s84 offen offset:3072 lds\n" \
"s_or_b32 s34, s7, s34\n" \
"s_or_b32 s7, s37, s7\n" \
"v_mov_b32_e64 v213, s35\n" \
"s_add_u32 m0, s31, 0xc000\n" \
"s_mov_b32 s84, 0x80000\n" \
B_"v26, s[72:75], s84 offen offset:0 lds\n" \
"s_or_b32 s7, s7, s36\n" \
"s_lshr_b32 s5, s2, 3\n" \
"s_or_b32 s7, s7, 0x5c00000\n" \
"s_mov_b64 exec, 0\n" \
"s_swappc_b64 s[88:89], s[86:87]\n" \
"s_mov_b64 exec, -1\n" \
"s_addk_i32 s84, 0x7c00\n" \
B_"v26, s[72:75], s84 offen offset:1024 lds\n" \
"v_writelane_b32 v213, s7, 2\n" \
"s_mul_i32 s5, s5, 0x90000\n" \
"v_writelane_b32 v213, s7, 3\n" \
"s_addk_i32 s84, 0x7c00\n" \
B_"v27, s[72:75], s84 offen offset:2048 lds\n" \
"s_lshl_b32 s7, s3, 14\n" \
"s_lshl_b32 s4, s4, 16\n" \
"s_add_i32 s5, s5, s7\n" \
"s_addk_i32 s84, 0x7c00\n" \
B_"v27, s[72:75], s84 offen offset:3072 lds\n" \
"s_or_b32 s34, s34, 0x5be8000\n" \
"v_writelane_b32 v213, s34, 4\n" \
"s_add_i32 s4, s5, s4\n" \
"v_writelane_b32 v213, s34, 5\n" \
"s_add_i32 s4, s4, 0x4243800\n" \
"v_writelane_b32 v213, s4, 6\n" \
"s_mul_i32 s4, s6, 0xa00\n" \
"v_lshlrev_b32_e32 v212, 3, v20\n" \
"s_lshl_b32 s5, s3, 7\n" \
"v_and_b32_e32 v212, 0x80, v212\n" \
"s_add_i32 s4, s4, s5\n" \
"s_add_i32 s4, s4, 0x5bc0f80\n" \
"v_writelane_b32 v213, s4, 7\n" \
"v_add_u32_e32 v49, v213, v212\n" \
"s_mov_b32 m0, s32\n" \
"s_mov_b32 exec_lo, 0xffffff\n" \
"s_mov_b32 exec_hi, 0xfff0000\n" \
"global_load_lds_dword v[184:185], off\n" \
"s_mov_b64 exec, -1\n" \
"s_mov_b32 m0, s32\n" \
"s_mov_b64 exec, 0xff\n" \
"buffer_load_dword v49, s[80:83], 0 offen lds\n" \
"s_mov_b64 exec, -1\n" \
"s_mov_b32 s84, 0x5bc0000\n" \
"buffer_load_dword v180, off, s[80:83], s84 sc1\n" \
"s_cmp_lg_u32 s33, 0\n" \
"s_cbranch_scc1 L_w1_%=\n" \
"L_w0_%=:\n" \
"s_waitcnt vmcnt(19)\n" \
"s_barrier\n" \
X_"a[160:161], v32 offset:0\n" \
X_"a[162:163], v32 offset:1024\n" \
R_"a[128:131], v28 offset:16384\n" \
X_"a[224:225], v32 offset:8192\n" \
X_"a[226:227], v32 offset:9216\n" \
R_"a[192:195], v29 offset:16384\n" \
R_"a[132:135], v28 offset:18432\n" \
R_"a[196:199], v29 offset:18432\n" \
X_"a[164:165], v33 offset:0\n" \
X_"a[166:167], v33 offset:1024\n" \
X_"a[228:229], v33 offset:8192\n" \
X_"a[230:231], v33 offset:9216\n" \
R_"a[136:139], v28 offset:20480\n" \
R_"a[200:203], v29 offset:20480\n" \
"s_waitcnt lgkmcnt(11)\n" \
M_"a[0:3], a[160:163], a[128:131], 0\n" \
X_"a[168:169], v34 offset:0\n" \
"s_lshr_b32 s4, s2, 1\n" \
"s_waitcnt lgkmcnt(9)\n" \
M_"a[0:3], a[224:227], a[192:195], a[0:3]\n" \
X_"a[170:171], v34 offset:1024\n" \
"s_and_b32 s4, s4, 0x7c\n" \
"s_waitcnt lgkmcnt(9)\n" \
M_"a[16:19], a[160:163], a[132:135], 0\n" \
X_"a[232:233], v34 offset:8192\n" \
"v_mov_b32_e32 v213, s13\n" \
"s_waitcnt lgkmcnt(9)\n" \
M_"a[16:19], a[224:227], a[196:199], a[16:19]\n" \
X_"a[234:235], v34 offset:9216\n" \
"s_or_b32 s4, s4, s3\n" \
"s_waitcnt lgkmcnt(8)\n" \
M_"a[20:23], a[164:167], a[132:135], 0\n" \
R_"a[140:143], v28 offset:22528\n" \
"v_lshrrev_b32_e32 v212, 4, v20\n" \
"s_waitcnt lgkmcnt(7)\n" \
M_"a[20:23], a[228:231], a[196:199], a[20:23]\n" \
R_"a[204:207], v29 offset:22528\n" \
"v_mad_u64_u32 v[214:215], s[4:5], s4, 36, v[212:213]\n" \
M_"a[4:7], a[164:167], a[128:131], 0\n" \
X_"a[172:173], v35 offset:0\n" \
"v_mov_b32_e64 v217, s9\n" \
M_"a[4:7], a[228:231], a[192:195], a[4:7]\n" \
X_"a[174:175], v35 offset:1024\n" \
"s_movk_i32 s4, 0x240\n" \
"s_waitcnt lgkmcnt(9)\n" \
M_"a[32:35], a[160:163], a[136:139], 0\n" \
X_"a[236:237], v35 offset:8192\n" \
"v_cmp_gt_u32_e32 vcc, s4, v20\n" \
"s_waitcnt lgkmcnt(9)\n" \
M_"a[32:35], a[224:227], a[200:203], a[32:35]\n" \
X_"a[238:239], v35 offset:9216\n" \
"s_lshl_b32 s4, s2, 3\n" \
M_"a[36:39], a[164:167], a[136:139], 0\n" \
R_"a[144:147], v28 offset:0\n" \
"s_and_b32 s4, s4, 56\n" \
M_"a[36:39], a[228:231], a[200:203], a[36:39]\n" \
R_"a[208:211], v29 offset:0\n" \
"v_cndmask_b32_e32 v217, v217, v213, vcc\n" \
"s_waitcnt lgkmcnt(10)\n" \
M_"a[40:43], a[168:171], a[136:139], 0\n" \
R_"a[148:151], v28 offset:2048\n" \
"v_lshlrev_b32_e32 v213, 3, v214\n" \
"s_waitcnt lgkmcnt(9)\n" \
M_"a[40:43], a[232:235], a[200:203], a[40:43]\n" \
R_"a[212:215], v29 offset:2048\n" \
"v_and_b32_e32 v213, 0x1ffc0, v213\n" \
M_"a[24:27], a[168:171], a[132:135], 0\n" \
R_"a[152:155], v28 offset:4096\n" \
"v_and_b32_e64 v214, 7, v214\n" \
M_"a[24:27], a[232:235], a[196:199], a[24:27]\n" \
R_"a[216:219], v29 offset:4096\n" \
"v_or3_b32 v213, v214, s4, v213\n" \
M_"a[8:11], a[168:171], a[128:131], 0\n" \
R_"a[156:159], v28 offset:6144\n" \
"s_lshl_b32 s4, s2, 2\n" \
M_"a[8:11], a[232:235], a[192:195], a[8:11]\n" \
R_"a[220:223], v29 offset:6144\n" \
"s_mov_b32 m0, s32\n" \
"s_waitcnt lgkmcnt(13)\n" \
M_"a[48:51], a[160:163], a[140:143], 0\n" \
"s_mov_b32 exec_lo, 0xff000000\n" \
"s_mov_b32 exec_hi, 0x0\n" \
"global_load_lds_dword v[184:185], off\n" \
"s_mov_b64 exec, -1\n" \
"s_waitcnt lgkmcnt(12)\n" \
M_"a[48:51], a[224:227], a[204:207], a[48:51]\n" \
X_"a[176:177], v36 offset:0\n" \
"s_or_b32 s4, s4, s3\n" \
M_"a[52:55], a[164:167], a[140:143], 0\n" \
X_"a[178:179], v36 offset:1024\n" \
"s_mul_i32 s4, s4, 18\n" \
M_"a[52:55], a[228:231], a[204:207], a[52:55]\n" \
"s_mov_b32 m0, s32\n" \
X_"a[240:241], v36 offset:8192\n" \
M_"a[56:59], a[168:171], a[140:143], 0\n" \
"s_mov_b32 exec_lo, 0xffff\n" \
"s_mov_b32 exec_hi, 0x0\n" \
"global_load_lds_dword v[186:187], off\n" \
"s_mov_b64 exec, -1\n" \
M_"a[56:59], a[232:235], a[204:207], a[56:59]\n" \
X_"a[242:243], v36 offset:9216\n" \
"v_min_u32_e32 v212, 53, v212\n" \
"s_waitcnt lgkmcnt(14)\n" \
M_"a[60:63], a[172:175], a[140:143], 0\n" \
X_"a[180:181], v37 offset:0\n" \
"s_sub_i32 s4, s4, 36\n" \
"s_waitcnt lgkmcnt(13)\n" \
M_"a[60:63], a[236:239], a[204:207], a[60:63]\n" \
X_"a[182:183], v37 offset:1024\n" \
"v_add_u32_e32 v212, s4, v212\n" \
M_"a[44:47], a[172:175], a[136:139], 0\n" \
X_"a[244:245], v37 offset:8192\n" \
"v_mov_b32_e64 v215, s12\n" \
M_"a[44:47], a[236:239], a[200:203], a[44:47]\n" \
X_"a[246:247], v37 offset:9216\n" \
"v_mov_b32_e64 v216, s8\n" \
M_"a[28:31], a[172:175], a[132:135], 0\n" \
X_"a[184:185], v38 offset:0\n" \
"v_cndmask_b32_e64 v212, v212, v213, vcc\n" \
M_"a[28:31], a[236:239], a[196:199], a[28:31]\n" \
X_"a[186:187], v38 offset:1024\n" \
"v_cndmask_b32_e64 v216, v216, v215, vcc\n" \
M_"a[12:15], a[172:175], a[128:131], 0\n" \
"v_lshlrev_b32_e32 v212, 7, v212\n" \
"v_mov_b32_e32 v213, 0\n" \
M_"a[12:15], a[236:239], a[192:195], a[12:15]\n" \
"v_lshl_add_u64 v[188:189], v[216:217], 0, v[212:213]\n" \
"s_movk_i32 s5, 0xd80\n" \
"s_waitcnt lgkmcnt(15)\n" \
M_"a[76:79], a[172:175], a[144:147], 0\n" \
X_"a[248:249], v38 offset:8192\n" \
"s_lshr_b32 s6, s2, 5\n" \
M_"a[76:79], a[236:239], a[208:211], a[76:79]\n" \
"v_lshrrev_b32_e32 v212, 4, v20\n" \
"v_lshl_or_b32 v215, s3, 6, v212\n" \
M_"a[72:75], a[168:171], a[144:147], 0\n" \
"v_mul_u32_u24_e32 v216, 0x2aab, v215\n" \
"v_lshrrev_b32_e64 v216, 16, v216\n" \
M_"a[72:75], a[232:235], a[208:211], a[72:75]\n" \
X_"a[250:251], v38 offset:9216\n" \
"v_mad_i32_i24 v217, v216, -6, v215\n" \
M_"a[68:71], a[164:167], a[144:147], 0\n" \
"v_add_u32_e32 v218, 0xfffffe50, v215\n" \
"s_mul_i32 s35, s6, 0x48\n" \
M_"a[68:71], a[228:231], a[208:211], a[68:71]\n" \
"v_lshrrev_b32_e32 v219, 1, v217\n" \
"v_add_u32_e32 v218, s35, v216\n" \
M_"a[64:67], a[160:163], a[144:147], 0\n" \
"v_mul_u32_u24_e32 v219, 0x480, v219\n" \
"v_and_b32_e64 v214, 16, v20\n" \
M_"a[64:67], a[224:227], a[208:211], a[64:67]\n" \
X_"a[188:189], v39 offset:0\n" \
"v_mad_u32_u24 v219, v218, s5, v219\n" \
M_"a[80:83], a[160:163], a[148:151], 0\n" \
"v_or_b32_e32 v192, v219, v214\n" \
"v_mov_b32_e32 v220, 2.0\n" \
"s_waitcnt lgkmcnt(15)\n" \
M_"a[80:83], a[224:227], a[212:215], a[80:83]\n" \
"v_mov_b32_e32 v222, 7\n" \
"v_lshlrev_b32_e64 v220, 7, v218\n" \
M_"a[84:87], a[164:167], a[148:151], 0\n" \
X_"a[190:191], v39 offset:1024\n" \
"v_lshrrev_b32_e64 v218, 1, v218\n" \
M_"a[84:87], a[228:231], a[212:215], a[84:87]\n" \
"v_and_b32_e32 v218, 4, v218\n" \
"v_and_b32_e32 v216, 2, v216\n" \
M_"a[88:91], a[168:171], a[148:151], 0\n" \
"s_mul_i32 s7, s6, 0x2400\n" \
"s_sub_i32 s34, 0x22000, s7\n" \
M_"a[88:91], a[232:235], a[212:215], a[88:91]\n" \
X_"a[252:253], v39 offset:8192\n" \
"v_bitop3_b32 v216, v217, v218, v216 bitop3:0x1e\n" \
M_"a[92:95], a[172:175], a[148:151], 0\n" \
"v_or_b32_e32 v213, 0x24400, v20\n" \
"v_lshlrev_b32_e64 v216, 4, v216\n" \
M_"a[92:95], a[236:239], a[212:215], a[92:95]\n" \
"v_add3_u32 v196, v220, s34, v216\n" \
"v_mov_b32_e64 v216, v213\n" \
M_"a[108:111], a[172:175], a[152:155], 0\n" \
X_"a[254:255], v39 offset:9216\n" \
"s_movk_i32 s36, 0x1b0\n" \
M_"a[108:111], a[236:239], a[216:219], a[108:111]\n" \
"v_or_b32_e32 v216, 0x100, v215\n" \
"v_mul_u32_u24_e32 v217, 0x2aab, v216\n" \
M_"a[104:107], a[168:171], a[152:155], 0\n" \
"v_add_u32_e32 v215, 0xffffff50, v215\n" \
"v_lshrrev_b32_e32 v217, 16, v217\n" \
M_"a[104:107], a[232:235], a[216:219], a[104:107]\n" \
"v_mov_b32_e64 v220, v215\n" \
"v_mad_i32_i24 v218, v217, -6, v216\n" \
M_"a[100:103], a[164:167], a[152:155], 0\n" \
"v_cmp_gt_u32_e32 vcc, s36, v216\n" \
"v_mov_b32_e32 v221, 7\n" \
M_"a[100:103], a[228:231], a[216:219], a[100:103]\n" \
"v_lshrrev_b32_e32 v222, 1, v218\n" \
"v_cndmask_b32_e32 v217, v220, v217, vcc\n" \
M_"a[96:99], a[160:163], a[152:155], 0\n" \
"s_movk_i32 s4, 0x48\n" \
"v_add_u32_e32 v220, s35, v217\n" \
M_"a[96:99], a[224:227], a[216:219], a[96:99]\n" \
"v_mul_u32_u24_e32 v222, 0x480, v222\n" \
"v_mad_u32_u24 v222, v220, s5, v222\n" \
"s_waitcnt lgkmcnt(15)\n" \
M_"a[112:115], a[160:163], a[156:159], 0\n" \
"v_cmp_gt_u32_e64 s[4:5], s4, v215\n" \
"v_cndmask_b32_e32 v215, v221, v218, vcc\n" \
M_"a[112:115], a[224:227], a[220:223], a[112:115]\n" \
"v_lshrrev_b32_e32 v218, 1, v220\n" \
"v_or_b32_e32 v214, v222, v214\n" \
M_"a[116:119], a[164:167], a[156:159], 0\n" \
"v_mov_b32_e32 v222, 2.0\n" \
"v_and_b32_e32 v218, 4, v218\n" \
M_"a[116:119], a[228:231], a[220:223], a[116:119]\n" \
"v_and_b32_e64 v217, 2, v217\n" \
"v_bitop3_b32 v215, v215, v218, v217 bitop3:0x1e\n" \
M_"a[120:123], a[168:171], a[156:159], 0\n" \
"v_lshlrev_b32_e32 v223, 1, v220\n" \
"v_mov_b32_e32 v224, 2.0\n" \
M_"a[120:123], a[232:235], a[220:223], a[120:123]\n" \
"v_cndmask_b32_e32 v193, v222, v214, vcc\n" \
"v_lshlrev_b32_e32 v214, 7, v220\n" \
M_"a[124:127], a[172:175], a[156:159], 0\n" \
"v_lshlrev_b32_e32 v215, 4, v215\n" \
"s_mul_i32 s39, s3, 18\n" \
"s_waitcnt vmcnt(5) lgkmcnt(0)\n" \
M_"a[124:127], a[236:239], a[220:223], a[124:127]\n" \
"s_barrier\n" \
M_"v[52:55], a[176:179], a[128:131], 0\n" \
"s_add_u32 m0, s30, 0x0\n" \
"s_movk_i32 s84, 0x100\n" \
M_"v[52:55], a[240:243], a[192:195], v[52:55]\n" \
B_"v22, s[64:67], s84 offen offset:0 lds\n" \
M_"v[56:59], a[180:183], a[128:131], 0\n" \
X_"a[160:161], v32 offset:32768\n" \
"v_add3_u32 v214, v214, s34, v215\n" \
M_"v[56:59], a[244:247], a[192:195], v[56:59]\n" \
"s_movk_i32 s84, 0x1d00\n" \
M_"v[60:63], a[184:187], a[128:131], 0\n" \
B_"v23, s[64:67], s84 offen offset:1024 lds\n" \
M_"v[60:63], a[248:251], a[192:195], v[60:63]\n" \
X_"a[162:163], v32 offset:33792\n" \
"v_cndmask_b32_e64 v195, v224, v223, s[4:5]\n" \
M_"v[64:67], a[188:191], a[128:131], 0\n" \
X_"a[224:225], v32 offset:40960\n" \
"s_movk_i32 s4, 0x1f8\n" \
M_"v[64:67], a[252:255], a[192:195], v[64:67]\n" \
R_"a[128:131], v28 offset:49152\n" \
"v_mov_b32_e64 v215, v213\n" \
M_"v[80:83], a[188:191], a[132:135], 0\n" \
R_"a[192:195], v29 offset:49152\n" \
"s_add_i32 s35, s35, s39\n" \
M_"v[80:83], a[252:255], a[196:199], v[80:83]\n" \
"s_movk_i32 s84, 0x3900\n" \
M_"v[76:79], a[184:187], a[132:135], 0\n" \
B_"v22, s[64:67], s84 offen offset:2048 lds\n" \
M_"v[76:79], a[248:251], a[196:199], v[76:79]\n" \
"s_movk_i32 s84, 0x5500\n" \
M_"v[72:75], a[180:183], a[132:135], 0\n" \
B_"v23, s[64:67], s84 offen offset:3072 lds\n" \
M_"v[72:75], a[244:247], a[196:199], v[72:75]\n" \
"s_add_u32 m0, s31, 0x0\n" \
"s_mov_b32 s84, 0x100000\n" \
M_"v[68:71], a[176:179], a[132:135], 0\n" \
B_"v26, s[68:71], s84 offen offset:0 lds\n" \
M_"v[68:71], a[240:243], a[196:199], v[68:71]\n" \
R_"a[132:135], v28 offset:51200\n" \
"v_cmp_gt_u32_e64 vcc, s4, v216\n" \
M_"v[84:87], a[176:179], a[136:139], 0\n" \
R_"a[196:199], v29 offset:51200\n" \
"v_add_u32_e64 v212, s35, v212\n" \
M_"v[84:87], a[240:243], a[200:203], v[84:87]\n" \
"s_addk_i32 s84, 0x7c00\n" \
M_"v[88:91], a[180:183], a[136:139], 0\n" \
B_"v26, s[68:71], s84 offen offset:1024 lds\n" \
M_"v[88:91], a[244:247], a[200:203], v[88:91]\n" \
X_"a[226:227], v32 offset:41984\n" \
"v_mov_b32_e32 v219, 2.0\n" \
M_"v[92:95], a[184:187], a[136:139], 0\n" \
"s_addk_i32 s84, 0x7c00\n" \
M_"v[92:95], a[248:251], a[200:203], v[92:95]\n" \
B_"v27, s[68:71], s84 offen offset:2048 lds\n" \
M_"v[96:99], a[188:191], a[136:139], 0\n" \
X_"a[164:165], v33 offset:32768\n" \
"v_cndmask_b32_e32 v197, v215, v214, vcc\n" \
M_"v[96:99], a[252:255], a[200:203], v[96:99]\n" \
R_"a[136:139], v28 offset:53248\n" \
"s_movk_i32 s35, 0x120\n" \
M_"v[112:115], a[188:191], a[140:143], 0\n" \
R_"a[200:203], v29 offset:53248\n" \
"v_mul_u32_u24_e32 v214, 0xd80, v212\n" \
M_"v[112:115], a[252:255], a[204:207], v[112:115]\n" \
"s_addk_i32 s84, 0x7c00\n" \
M_"v[108:111], a[184:187], a[140:143], 0\n" \
B_"v27, s[68:71], s84 offen offset:3072 lds\n" \
M_"v[108:111], a[248:251], a[204:207], v[108:111]\n" \
"s_add_u32 m0, s30, 0x1000\n" \
"s_mov_b32 s84, 0x8100\n" \
M_"v[104:107], a[180:183], a[140:143], 0\n" \
B_"v22, s[64:67], s84 offen offset:0 lds\n" \
M_"v[104:107], a[244:247], a[204:207], v[104:107]\n" \
X_"a[166:167], v33 offset:33792\n" \
"v_cmp_gt_u32_e64 vcc, s35, v20\n" \
M_"v[100:103], a[176:179], a[140:143], 0\n" \
X_"a[228:229], v33 offset:40960\n" \
"v_lshrrev_b32_e64 v215, 1, v212\n" \
M_"v[100:103], a[240:243], a[204:207], v[100:103]\n" \
R_"a[140:143], v28 offset:55296\n" \
"s_and_b32 s4, s2, 7\n" \
M_"v[116:119], a[176:179], a[144:147], 0\n" \
R_"a[204:207], v29 offset:55296\n" \
"v_cndmask_b32_e32 v194, v219, v214, vcc\n" \
M_"v[116:119], a[240:243], a[208:211], v[116:119]\n" \
"s_addk_i32 s84, 0x1c00\n" \
M_"v[120:123], a[180:183], a[144:147], 0\n" \
B_"v23, s[64:67], s84 offen offset:1024 lds\n" \
M_"v[120:123], a[244:247], a[208:211], v[120:123]\n" \
"s_addk_i32 s84, 0x1c00\n" \
M_"v[124:127], a[184:187], a[144:147], 0\n" \
B_"v22, s[64:67], s84 offen offset:2048 lds\n" \
M_"v[124:127], a[248:251], a[208:211], v[124:127]\n" \
"s_addk_i32 s84, 0x1c00\n" \
".fill (L_pb + 3808 - .) / 4, 4, 0xBF800000\n" \
"L_pp1:\n" \
"s_cbranch_execz L_pret\n" \
M_"v[128:131], a[188:191], a[144:147], 0\n" \
B_"v23, s[64:67], s84 offen offset:3072 lds\n" \
M_"v[128:131], a[252:255], a[208:211], v[128:131]\n" \
R_"a[144:147], v28 offset:32768\n" \
"v_lshlrev_b32_e32 v214, 7, v212\n" \
M_"v[144:147], a[188:191], a[148:151], 0\n" \
R_"a[208:211], v29 offset:32768\n" \
"v_lshrrev_b32_e64 v212, 2, v212\n" \
M_"v[144:147], a[252:255], a[212:215], v[144:147]\n" \
"s_add_u32 m0, s31, 0x4000\n" \
"s_mov_b32 s84, 0x100000\n" \
M_"v[140:143], a[184:187], a[148:151], 0\n" \
B_"v26, s[72:75], s84 offen offset:0 lds\n" \
M_"v[140:143], a[248:251], a[212:215], v[140:143]\n" \
X_"a[230:231], v33 offset:41984\n" \
"v_and_b32_e64 v212, 2, v212\n" \
M_"v[136:139], a[180:183], a[148:151], 0\n" \
"s_addk_i32 s84, 0x7c00\n" \
M_"v[136:139], a[244:247], a[212:215], v[136:139]\n" \
B_"v26, s[72:75], s84 offen offset:1024 lds\n" \
M_"v[132:135], a[176:179], a[148:151], 0\n" \
X_"a[168:169], v34 offset:32768\n" \
"v_and_or_b32 v212, v215, 1, v212\n" \
M_"v[132:135], a[240:243], a[212:215], v[132:135]\n" \
R_"a[148:151], v28 offset:34816\n" \
"s_lshl_b32 s5, s4, 2\n" \
M_"v[148:151], a[176:179], a[152:155], 0\n" \
R_"a[212:215], v29 offset:34816\n" \
"s_bfe_u32 s38, s2, 0x20003\n" \
M_"v[148:151], a[240:243], a[216:219], v[148:151]\n" \
X_"a[170:171], v34 offset:33792\n" \
"v_lshlrev_b32_e64 v212, 5, v212\n" \
M_"v[152:155], a[180:183], a[152:155], 0\n" \
X_"a[232:233], v34 offset:40960\n" \
"s_movk_i32 s35, 0x60\n" \
M_"v[152:155], a[244:247], a[216:219], v[152:155]\n" \
X_"a[234:235], v34 offset:41984\n" \
"s_or_b32 s5, s5, s38\n" \
M_"v[156:159], a[184:187], a[152:155], 0\n" \
X_"a[172:173], v35 offset:32768\n" \
"v_bitop3_b32 v212, v212, s35, v214 bitop3:0x36\n" \
M_"v[156:159], a[248:251], a[216:219], v[156:159]\n" \
X_"a[174:175], v35 offset:33792\n" \
"s_lshr_b32 s36, s2, 3\n" \
M_"v[160:163], a[188:191], a[152:155], 0\n" \
X_"a[236:237], v35 offset:40960\n" \
"v_add_u32_e32 v212, s34, v212\n" \
M_"v[160:163], a[252:255], a[216:219], v[160:163]\n" \
R_"a[152:155], v28 offset:36864\n" \
"s_mul_i32 s34, s5, 0x12000\n" \
M_"v[176:179], a[188:191], a[156:159], 0\n" \
R_"a[216:219], v29 offset:36864\n" \
"s_or_b32 s40, s34, 0x6000000\n" \
M_"v[176:179], a[252:255], a[220:223], v[176:179]\n" \
X_"a[238:239], v35 offset:41984\n" \
"s_mul_i32 s34, s36, 0x90000\n" \
M_"v[172:175], a[184:187], a[156:159], 0\n" \
"s_lshl_b32 s36, s3, 14\n" \
"s_add_i32 s34, s34, s36\n" \
M_"v[172:175], a[248:251], a[220:223], v[172:175]\n" \
"s_lshl_b32 s38, s5, 8\n" \
"s_add_i32 s41, s34, 0x4240000\n" \
M_"v[168:171], a[180:183], a[156:159], 0\n" \
"s_mul_i32 s34, s5, 36\n" \
"s_add_i32 s5, s7, s40\n" \
M_"v[168:171], a[244:247], a[220:223], v[168:171]\n" \
"s_addk_i32 s5, 0x2000\n" \
"s_mov_b32 s37, 2.0\n" \
M_"v[164:167], a[176:179], a[156:159], 0\n" \
"s_or_b32 s38, s38, 0x5be8000\n" \
M_"v[164:167], a[240:243], a[220:223], v[164:167]\n" \
R_"a[156:159], v28 offset:38912\n" \
R_"a[220:223], v29 offset:38912\n" \
"s_waitcnt lgkmcnt(15)\n" \
M_"a[0:3], a[160:163], a[128:131], a[0:3]\n" \
"s_addk_i32 s84, 0x7c00\n" \
M_"a[0:3], a[224:227], a[192:195], a[0:3]\n" \
B_"v27, s[72:75], s84 offen offset:2048 lds\n" \
M_"a[4:7], a[164:167], a[128:131], a[4:7]\n" \
X_"a[176:177], v36 offset:32768\n" \
"s_waitcnt lgkmcnt(15)\n" \
M_"a[4:7], a[228:231], a[192:195], a[4:7]\n" \
"s_addk_i32 s84, 0x7c00\n" \
"s_waitcnt lgkmcnt(11)\n" \
M_"a[8:11], a[168:171], a[128:131], a[8:11]\n" \
B_"v27, s[72:75], s84 offen offset:3072 lds\n" \
"s_waitcnt lgkmcnt(9)\n" \
M_"a[8:11], a[232:235], a[192:195], a[8:11]\n" \
X_"a[178:179], v36 offset:33792\n" \
"s_waitcnt lgkmcnt(8)\n" \
M_"a[12:15], a[172:175], a[128:131], a[12:15]\n" \
X_"a[240:241], v36 offset:40960\n" \
"s_waitcnt lgkmcnt(5)\n" \
M_"a[12:15], a[236:239], a[192:195], a[12:15]\n" \
"s_cmp_lg_u32 s3, 0\n" \
"s_cselect_b32 s36, s37, s5\n" \
M_"a[28:31], a[172:175], a[132:135], a[28:31]\n" \
"s_lshl_b32 s5, s6, 2\n" \
"s_or_b32 s5, s38, s5\n" \
M_"a[28:31], a[236:239], a[196:199], a[28:31]\n" \
X_"a[242:243], v36 offset:41984\n" \
"s_lshl_b32 s35, s3, 10\n" \
M_"a[24:27], a[168:171], a[132:135], a[24:27]\n" \
"s_cmpk_lt_u32 s3, 3\n" \
"s_cselect_b32 s37, s37, s5\n" \
M_"a[24:27], a[232:235], a[196:199], a[24:27]\n" \
"s_lshl_b32 s5, s4, 16\n" \
"s_lshl_b32 s4, s4, 11\n" \
M_"a[20:23], a[164:167], a[132:135], a[20:23]\n" \
"v_cndmask_b32_e32 v198, v213, v212, vcc\n" \
"v_or_b32_e32 v212, s35, v20\n" \
M_"a[20:23], a[228:231], a[196:199], a[20:23]\n" \
X_"a[180:181], v37 offset:32768\n" \
"s_or_b32 s39, s40, s35\n" \
M_"a[16:19], a[160:163], a[132:135], a[16:19]\n" \
"s_or_b32 s55, s41, s4\n" \
"v_or_b32_e32 v199, 0x22000, v212\n" \
M_"a[16:19], a[224:227], a[196:199], a[16:19]\n" \
"s_add_i32 s35, s39, s7\n" \
"s_add_i32 s54, s41, s5\n" \
M_"a[32:35], a[160:163], a[136:139], a[32:35]\n" \
"s_mov_b32 m0, s32\n" \
X_"a[182:183], v37 offset:33792\n" \
M_"a[32:35], a[224:227], a[200:203], a[32:35]\n" \
"s_mov_b32 exec_lo, 0x0\n" \
"s_mov_b32 exec_hi, 0xff\n" \
"global_load_lds_dword v[184:185], off\n" \
"s_mov_b64 exec, -1\n" \
M_"a[36:39], a[164:167], a[136:139], a[36:39]\n" \
"s_add_i32 s55, s55, 0x80000\n" \
"v_mov_b32_e32 v215, 0xffff\n" \
M_"a[36:39], a[228:231], a[200:203], a[36:39]\n" \
X_"a[244:245], v37 offset:40960\n" \
"s_and_b32 s4, s3, 1\n" \
M_"a[40:43], a[168:171], a[136:139], a[40:43]\n" \
"s_lshl_b32 s5, s4, 4\n" \
"v_lshrrev_b32_e32 v212, 4, v20\n" \
M_"a[40:43], a[232:235], a[200:203], a[40:43]\n" \
"s_mov_b32 m0, s32\n" \
"v_lshrrev_b32_e32 v218, 6, v20\n" \
M_"a[44:47], a[172:175], a[136:139], a[44:47]\n" \
"s_mov_b32 exec_lo, 0xffff0000\n" \
"s_mov_b32 exec_hi, 0x0\n" \
"global_load_lds_dword v[186:187], off\n" \
"s_mov_b64 exec, -1\n" \
M_"a[44:47], a[236:239], a[200:203], a[44:47]\n" \
X_"a[246:247], v37 offset:41984\n" \
"v_and_b32_e64 v219, 12, v218\n" \
M_"a[60:63], a[172:175], a[140:143], a[60:63]\n" \
"v_and_or_b32 v214, v212, 15, s5\n" \
"v_cmp_gt_u32_e64 vcc, v219, v214\n" \
M_"a[60:63], a[236:239], a[204:207], a[60:63]\n" \
"v_mov_b32_e32 v220, 0xffff0000\n" \
"s_lshl_b32 s5, s2, 2\n" \
M_"a[56:59], a[168:171], a[140:143], a[56:59]\n" \
X_"a[184:185], v38 offset:32768\n" \
"v_cndmask_b32_e64 v217, v215, 0, vcc\n" \
M_"a[56:59], a[232:235], a[204:207], a[56:59]\n" \
"v_cmp_lt_u32_e32 vcc, v219, v214\n" \
"v_mov_b32_e64 v213, s23\n" \
M_"a[52:55], a[164:167], a[140:143], a[52:55]\n" \
"s_or_b32 s5, s5, s3\n" \
"v_cndmask_b32_e32 v221, 0, v220, vcc\n" \
M_"a[52:55], a[228:231], a[204:207], a[52:55]\n" \
X_"a[186:187], v38 offset:33792\n" \
"v_or_b32_e64 v12, v217, v221\n" \
M_"a[48:51], a[160:163], a[140:143], a[48:51]\n" \
"v_or_b32_e32 v217, 2, v219\n" \
"v_cmp_gt_u32_e32 vcc, v217, v214\n" \
M_"a[48:51], a[224:227], a[204:207], a[48:51]\n" \
"v_mov_b32_e32 v216, s25\n" \
"s_movk_i32 s40, 0xf400\n" \
M_"a[64:67], a[160:163], a[144:147], a[64:67]\n" \
X_"a[248:249], v38 offset:40960\n" \
"v_cndmask_b32_e64 v221, v215, 0, vcc\n" \
M_"a[64:67], a[224:227], a[208:211], a[64:67]\n" \
"v_cmp_lt_u32_e32 vcc, v217, v214\n" \
"s_lshl_b32 s4, s4, 6\n" \
M_"a[68:71], a[164:167], a[144:147], a[68:71]\n" \
"v_cndmask_b32_e32 v217, 0, v220, vcc\n" \
"v_or_b32_e32 v13, v221, v217\n" \
M_"a[68:71], a[228:231], a[208:211], a[68:71]\n" \
X_"a[250:251], v38 offset:41984\n" \
"v_or_b32_e64 v217, 16, v219\n" \
M_"a[72:75], a[168:171], a[144:147], a[72:75]\n" \
"v_cmp_gt_u32_e32 vcc, v217, v214\n" \
"s_nop 1\n" \
"v_cndmask_b32_e64 v221, v215, 0, vcc\n" \
M_"a[72:75], a[232:235], a[208:211], a[72:75]\n" \
"v_cmp_lt_u32_e32 vcc, v217, v214\n" \
"s_nop 1\n" \
"v_cndmask_b32_e64 v217, 0, v220, vcc\n" \
M_"a[76:79], a[172:175], a[144:147], a[76:79]\n" \
"v_or_b32_e32 v14, v221, v217\n" \
"v_or_b32_e32 v217, 18, v219\n" \
M_"a[76:79], a[236:239], a[208:211], a[76:79]\n" \
X_"a[188:189], v39 offset:32768\n" \
"v_cmp_gt_u32_e64 vcc, v217, v214\n" \
M_"a[92:95], a[172:175], a[148:151], a[92:95]\n" \
"s_nop 0\n" \
"v_cndmask_b32_e64 v215, v215, 0, vcc\n" \
"v_cmp_lt_u32_e32 vcc, v217, v214\n" \
M_"a[92:95], a[236:239], a[212:215], a[92:95]\n" \
"s_nop 0\n" \
"v_cndmask_b32_e32 v214, 0, v220, vcc\n" \
"v_or_b32_e64 v15, v215, v214\n" \
M_"a[88:91], a[168:171], a[148:151], a[88:91]\n" \
X_"a[190:191], v39 offset:33792\n" \
"v_mad_u64_u32 v[214:215], s[6:7], s5, 42, v[212:213]\n" \
M_"a[88:91], a[232:235], a[212:215], a[88:91]\n" \
"s_movk_i32 s5, 0x1440\n" \
"v_cmp_gt_u32_e32 vcc, s5, v214\n" \
M_"a[84:87], a[164:167], a[148:151], a[84:87]\n" \
"v_min_u32_e32 v217, 0xa43f, v214\n" \
"v_mov_b32_e64 v214, s24\n" \
M_"a[84:87], a[228:231], a[212:215], a[84:87]\n" \
X_"a[252:253], v39 offset:40960\n" \
"v_cndmask_b32_e64 v215, v216, v213, vcc\n" \
M_"a[80:83], a[160:163], a[148:151], a[80:83]\n" \
"v_mov_b32_e32 v213, s22\n" \
"s_movk_i32 s6, 0xffee\n" \
M_"a[80:83], a[224:227], a[212:215], a[80:83]\n" \
"v_cndmask_b32_e64 v214, v214, v213, vcc\n" \
"v_add_u32_e32 v213, 0xffffebc0, v217\n" \
M_"a[96:99], a[160:163], a[152:155], a[96:99]\n" \
X_"a[254:255], v39 offset:41984\n" \
"s_lshl_b32 s5, s2, 5\n" \
M_"a[96:99], a[224:227], a[216:219], a[96:99]\n" \
"v_cndmask_b32_e32 v213, v213, v217, vcc\n" \
"v_lshlrev_b32_e64 v216, 7, v213\n" \
M_"a[100:103], a[164:167], a[152:155], a[100:103]\n" \
"v_mul_u32_u24_e32 v213, 0xe39, v212\n" \
"v_mov_b32_e64 v217, 0\n" \
M_"a[100:103], a[228:231], a[216:219], a[100:103]\n" \
"v_lshrrev_b32_e64 v213, 16, v213\n" \
"v_lshl_add_u64 v[16:17], v[214:215], 0, v[216:217]\n" \
M_"a[104:107], a[168:171], a[152:155], a[104:107]\n" \
"v_mad_i32_i24 v214, v213, s6, v212\n" \
"s_add_i32 s5, s5, 32\n" \
M_"a[104:107], a[232:235], a[216:219], a[104:107]\n" \
"v_lshlrev_b32_e32 v215, 4, v214\n" \
"v_add_u32_e64 v216, s5, v215\n" \
M_"a[108:111], a[172:175], a[152:155], a[108:111]\n" \
"v_and_b32_e32 v216, 0xf0, v216\n" \
"v_cmp_gt_u32_e64 vcc, 16, v214\n" \
M_"a[108:111], a[236:239], a[216:219], a[108:111]\n" \
"s_movk_i32 s7, 0xf800\n" \
"v_cndmask_b32_e32 v214, v215, v216, vcc\n" \
"s_waitcnt lgkmcnt(15)\n" \
M_"a[124:127], a[172:175], a[156:159], a[124:127]\n" \
"v_or_b32_e32 v215, 64, v212\n" \
"v_mul_u32_u24_e32 v216, 0xe39, v215\n" \
M_"a[124:127], a[236:239], a[220:223], a[124:127]\n" \
"v_lshrrev_b32_e64 v216, 16, v216\n" \
"v_mad_i32_i24 v215, v216, s6, v215\n" \
M_"a[120:123], a[168:171], a[156:159], a[120:123]\n" \
"v_lshlrev_b32_e32 v217, 4, v215\n" \
"v_add_u32_e32 v220, s5, v217\n" \
M_"a[120:123], a[232:235], a[220:223], a[120:123]\n" \
"v_and_b32_e32 v220, 0xf0, v220\n" \
"v_cmp_gt_u32_e64 vcc, 16, v215\n" \
M_"a[116:119], a[164:167], a[156:159], a[116:119]\n" \
"s_nop 0\n" \
"v_cndmask_b32_e32 v215, v217, v220, vcc\n" \
"v_or_b32_e32 v217, 0x80, v212\n" \
M_"a[116:119], a[228:231], a[220:223], a[116:119]\n" \
"v_mul_u32_u24_e32 v220, 0xe39, v217\n" \
"v_lshrrev_b32_e64 v220, 16, v220\n" \
M_"a[112:115], a[160:163], a[156:159], a[112:115]\n" \
"v_mad_i32_i24 v217, v220, s6, v217\n" \
"v_lshlrev_b32_e64 v221, 4, v217\n" \
M_"a[112:115], a[224:227], a[220:223], a[112:115]\n" \
"s_waitcnt vmcnt(20)\n" \
"v_readfirstlane_b32 s62, v180\n" \
"s_nop 3\n" \
"s_add_u32 s62, s62, 0x9E3779B1\n" \
"s_waitcnt vmcnt(2) lgkmcnt(0)\n" \
"s_barrier\n" \
M_"v[52:55], a[176:179], a[128:131], v[52:55]\n" \
"s_add_u32 m0, s30, 0x8000\n" \
"s_movk_i32 s84, 0x180\n" \
M_"v[52:55], a[240:243], a[192:195], v[52:55]\n" \
B_"v22, s[64:67], s84 offen offset:0 lds\n" \
M_"v[56:59], a[180:183], a[128:131], v[56:59]\n" \
X_"a[160:161], v32 offset:0\n" \
"v_add_u32_e64 v222, s5, v221\n" \
M_"v[56:59], a[244:247], a[192:195], v[56:59]\n" \
"s_movk_i32 s84, 0x1d80\n" \
M_"v[60:63], a[184:187], a[128:131], v[60:63]\n" \
B_"v23, s[64:67], s84 offen offset:1024 lds\n" \
M_"v[60:63], a[248:251], a[192:195], v[60:63]\n" \
X_"a[162:163], v32 offset:1024\n" \
"v_and_b32_e32 v222, 0xf0, v222\n" \
M_"v[64:67], a[188:191], a[128:131], v[64:67]\n" \
X_"a[224:225], v32 offset:8192\n" \
"v_cmp_gt_u32_e32 vcc, 16, v217\n" \
M_"v[64:67], a[252:255], a[192:195], v[64:67]\n" \
R_"a[128:131], v28 offset:16384\n" \
"v_cndmask_b32_e64 v217, v221, v222, vcc\n" \
M_"v[80:83], a[188:191], a[132:135], v[80:83]\n" \
R_"a[192:195], v29 offset:16384\n" \
"v_or_b32_e32 v221, 0xc0, v212\n" \
M_"v[80:83], a[252:255], a[196:199], v[80:83]\n" \
"s_movk_i32 s84, 0x3980\n" \
M_"v[76:79], a[184:187], a[132:135], v[76:79]\n" \
B_"v22, s[64:67], s84 offen offset:2048 lds\n" \
M_"v[76:79], a[248:251], a[196:199], v[76:79]\n" \
"s_movk_i32 s84, 0x5580\n" \
M_"v[72:75], a[180:183], a[132:135], v[72:75]\n" \
B_"v23, s[64:67], s84 offen offset:3072 lds\n" \
M_"v[72:75], a[244:247], a[196:199], v[72:75]\n" \
"s_add_u32 m0, s31, 0x8000\n" \
"s_mov_b32 s84, 0x180000\n" \
M_"v[68:71], a[176:179], a[132:135], v[68:71]\n" \
B_"v26, s[68:71], s84 offen offset:0 lds\n" \
M_"v[68:71], a[240:243], a[196:199], v[68:71]\n" \
R_"a[132:135], v28 offset:18432\n" \
"v_mul_u32_u24_e32 v222, 0xe39, v221\n" \
M_"v[84:87], a[176:179], a[136:139], v[84:87]\n" \
R_"a[196:199], v29 offset:18432\n" \
"v_lshrrev_b32_e64 v222, 16, v222\n" \
M_"v[84:87], a[240:243], a[200:203], v[84:87]\n" \
"s_addk_i32 s84, 0x7c00\n" \
M_"v[88:91], a[180:183], a[136:139], v[88:91]\n" \
B_"v26, s[68:71], s84 offen offset:1024 lds\n" \
M_"v[88:91], a[244:247], a[200:203], v[88:91]\n" \
X_"a[226:227], v32 offset:9216\n" \
"v_mad_i32_i24 v221, v222, s6, v221\n" \
M_"v[92:95], a[184:187], a[136:139], v[92:95]\n" \
"s_addk_i32 s84, 0x7c00\n" \
M_"v[92:95], a[248:251], a[200:203], v[92:95]\n" \
B_"v27, s[68:71], s84 offen offset:2048 lds\n" \
M_"v[96:99], a[188:191], a[136:139], v[96:99]\n" \
X_"a[164:165], v33 offset:0\n" \
"v_lshlrev_b32_e64 v223, 4, v221\n" \
M_"v[96:99], a[252:255], a[200:203], v[96:99]\n" \
R_"a[136:139], v28 offset:20480\n" \
"v_add_u32_e64 v224, s5, v223\n" \
M_"v[112:115], a[188:191], a[140:143], v[112:115]\n" \
R_"a[200:203], v29 offset:20480\n" \
"v_and_b32_e32 v224, 0xf0, v224\n" \
M_"v[112:115], a[252:255], a[204:207], v[112:115]\n" \
"s_addk_i32 s84, 0x7c00\n" \
M_"v[108:111], a[184:187], a[140:143], v[108:111]\n" \
B_"v27, s[68:71], s84 offen offset:3072 lds\n" \
M_"v[108:111], a[248:251], a[204:207], v[108:111]\n" \
"s_add_u32 m0, s30, 0x9000\n" \
"s_mov_b32 s84, 0x8180\n" \
M_"v[104:107], a[180:183], a[140:143], v[104:107]\n" \
B_"v22, s[64:67], s84 offen offset:0 lds\n" \
M_"v[104:107], a[244:247], a[204:207], v[104:107]\n" \
X_"a[166:167], v33 offset:1024\n" \
"v_cmp_gt_u32_e32 vcc, 16, v221\n" \
M_"v[100:103], a[176:179], a[140:143], v[100:103]\n" \
X_"a[228:229], v33 offset:8192\n" \
"v_cndmask_b32_e64 v221, v223, v224, vcc\n" \
M_"v[100:103], a[240:243], a[204:207], v[100:103]\n" \
R_"a[140:143], v28 offset:22528\n" \
"v_or_b32_e32 v223, 0x100, v212\n" \
M_"v[116:119], a[176:179], a[144:147], v[116:119]\n" \
R_"a[204:207], v29 offset:22528\n" \
"v_mul_u32_u24_e32 v224, 0xe39, v223\n" \
M_"v[116:119], a[240:243], a[208:211], v[116:119]\n" \
"s_addk_i32 s84, 0x1c00\n" \
M_"v[120:123], a[180:183], a[144:147], v[120:123]\n" \
B_"v23, s[64:67], s84 offen offset:1024 lds\n" \
M_"v[120:123], a[244:247], a[208:211], v[120:123]\n" \
"s_addk_i32 s84, 0x1c00\n" \
M_"v[124:127], a[184:187], a[144:147], v[124:127]\n" \
B_"v22, s[64:67], s84 offen offset:2048 lds\n" \
M_"v[124:127], a[248:251], a[208:211], v[124:127]\n" \
"s_addk_i32 s84, 0x1c00\n" \
M_"v[128:131], a[188:191], a[144:147], v[128:131]\n" \
B_"v23, s[64:67], s84 offen offset:3072 lds\n" \
M_"v[128:131], a[252:255], a[208:211], v[128:131]\n" \
R_"a[144:147], v28 offset:0\n" \
"v_lshrrev_b32_e32 v224, 16, v224\n" \
M_"v[144:147], a[188:191], a[148:151], v[144:147]\n" \
R_"a[208:211], v29 offset:0\n" \
"v_mad_i32_i24 v223, v224, s6, v223\n" \
M_"v[144:147], a[252:255], a[212:215], v[144:147]\n" \
"s_add_u32 m0, s31, 0xc000\n" \
"s_mov_b32 s84, 0x180000\n" \
M_"v[140:143], a[184:187], a[148:151], v[140:143]\n" \
B_"v26, s[72:75], s84 offen offset:0 lds\n" \
M_"v[140:143], a[248:251], a[212:215], v[140:143]\n" \
X_"a[230:231], v33 offset:9216\n" \
"v_lshlrev_b32_e64 v225, 4, v223\n" \
M_"v[136:139], a[180:183], a[148:151], v[136:139]\n" \
"s_addk_i32 s84, 0x7c00\n" \
M_"v[136:139], a[244:247], a[212:215], v[136:139]\n" \
B_"v26, s[72:75], s84 offen offset:1024 lds\n" \
M_"v[132:135], a[176:179], a[148:151], v[132:135]\n" \
X_"a[168:169], v34 offset:0\n" \
"v_add_u32_e32 v226, s5, v225\n" \
M_"v[132:135], a[240:243], a[212:215], v[132:135]\n" \
R_"a[148:151], v28 offset:2048\n" \
"v_and_b32_e32 v226, 0xf0, v226\n" \
M_"v[148:151], a[176:179], a[152:155], v[148:151]\n" \
R_"a[212:215], v29 offset:2048\n" \
"v_cmp_gt_u32_e64 vcc, 16, v223\n" \
M_"v[148:151], a[240:243], a[216:219], v[148:151]\n" \
X_"a[170:171], v34 offset:1024\n" \
"v_cndmask_b32_e64 v223, v225, v226, vcc\n" \
M_"v[152:155], a[180:183], a[152:155], v[152:155]\n" \
X_"a[232:233], v34 offset:8192\n" \
"v_or_b32_e32 v225, 0x140, v212\n" \
M_"v[152:155], a[244:247], a[216:219], v[152:155]\n" \
X_"a[234:235], v34 offset:9216\n" \
"v_mul_u32_u24_e32 v226, 0xe39, v225\n" \
M_"v[156:159], a[184:187], a[152:155], v[156:159]\n" \
X_"a[172:173], v35 offset:0\n" \
"v_lshrrev_b32_e64 v226, 16, v226\n" \
M_"v[156:159], a[248:251], a[216:219], v[156:159]\n" \
X_"a[174:175], v35 offset:1024\n" \
"v_mad_i32_i24 v225, v226, s6, v225\n" \
M_"v[160:163], a[188:191], a[152:155], v[160:163]\n" \
X_"a[236:237], v35 offset:8192\n" \
"v_lshlrev_b32_e64 v227, 4, v225\n" \
M_"v[160:163], a[252:255], a[216:219], v[160:163]\n" \
R_"a[152:155], v28 offset:4096\n" \
"v_add_u32_e64 v228, s5, v227\n" \
M_"v[176:179], a[188:191], a[156:159], v[176:179]\n" \
R_"a[216:219], v29 offset:4096\n" \
"v_and_b32_e32 v228, 0xf0, v228\n" \
M_"v[176:179], a[252:255], a[220:223], v[176:179]\n" \
X_"a[238:239], v35 offset:9216\n" \
"v_cmp_gt_u32_e64 vcc, 16, v225\n" \
M_"v[172:175], a[184:187], a[156:159], v[172:175]\n" \
"s_nop 0\n" \
"v_cndmask_b32_e32 v225, v227, v228, vcc\n" \
"v_or_b32_e32 v227, 0x180, v212\n" \
M_"v[172:175], a[248:251], a[220:223], v[172:175]\n" \
"v_mul_u32_u24_e32 v228, 0xe39, v227\n" \
"v_lshrrev_b32_e64 v228, 16, v228\n" \
M_"v[168:171], a[180:183], a[156:159], v[168:171]\n" \
"v_mad_i32_i24 v227, v228, s6, v227\n" \
"v_lshlrev_b32_e64 v229, 4, v227\n" \
M_"v[168:171], a[244:247], a[220:223], v[168:171]\n" \
"v_add_u32_e64 v230, s5, v229\n" \
"v_and_b32_e32 v230, 0xf0, v230\n" \
M_"v[164:167], a[176:179], a[156:159], v[164:167]\n" \
"v_cmp_gt_u32_e32 vcc, 16, v227\n" \
"s_nop 1\n" \
"v_cndmask_b32_e64 v227, v229, v230, vcc\n" \
M_"v[164:167], a[240:243], a[220:223], v[164:167]\n" \
R_"a[156:159], v28 offset:6144\n" \
R_"a[220:223], v29 offset:6144\n" \
"s_waitcnt lgkmcnt(15)\n" \
M_"a[0:3], a[160:163], a[128:131], a[0:3]\n" \
"s_addk_i32 s84, 0x7c00\n" \
M_"a[0:3], a[224:227], a[192:195], a[0:3]\n" \
B_"v27, s[72:75], s84 offen offset:2048 lds\n" \
M_"a[4:7], a[164:167], a[128:131], a[4:7]\n" \
X_"a[176:177], v36 offset:0\n" \
"v_or_b32_e32 v229, 0x1c0, v212\n" \
"s_waitcnt lgkmcnt(15)\n" \
M_"a[4:7], a[228:231], a[192:195], a[4:7]\n" \
"s_addk_i32 s84, 0x7c00\n" \
"s_waitcnt lgkmcnt(11)\n" \
M_"a[8:11], a[168:171], a[128:131], a[8:11]\n" \
B_"v27, s[72:75], s84 offen offset:3072 lds\n" \
"s_waitcnt lgkmcnt(9)\n" \
M_"a[8:11], a[232:235], a[192:195], a[8:11]\n" \
X_"a[178:179], v36 offset:1024\n" \
"v_mul_u32_u24_e32 v230, 0xe39, v229\n" \
"s_waitcnt lgkmcnt(8)\n" \
M_"a[12:15], a[172:175], a[128:131], a[12:15]\n" \
X_"a[240:241], v36 offset:8192\n" \
"v_lshrrev_b32_e32 v230, 16, v230\n" \
"s_waitcnt lgkmcnt(5)\n" \
M_"a[12:15], a[236:239], a[192:195], a[12:15]\n" \
"v_mad_i32_i24 v229, v230, s6, v229\n" \
"v_lshlrev_b32_e32 v231, 4, v229\n" \
M_"a[28:31], a[172:175], a[132:135], a[28:31]\n" \
"v_add_u32_e64 v232, s5, v231\n" \
"v_and_b32_e32 v232, 0xf0, v232\n" \
M_"a[28:31], a[236:239], a[196:199], a[28:31]\n" \
X_"a[242:243], v36 offset:9216\n" \
"v_cmp_gt_u32_e64 vcc, 16, v229\n" \
M_"a[24:27], a[168:171], a[132:135], a[24:27]\n" \
B_"v[200:203], v192, s[76:79], s34 offen offset:0\n" \
M_"a[24:27], a[232:235], a[196:199], a[24:27]\n" \
"v_cndmask_b32_e64 v229, v231, v232, vcc\n" \
"v_or_b32_e32 v231, 0x200, v212\n" \
M_"a[20:23], a[164:167], a[132:135], a[20:23]\n" \
"v_mul_u32_u24_e32 v232, 0xe39, v231\n" \
"v_lshrrev_b32_e64 v232, 16, v232\n" \
M_"a[20:23], a[228:231], a[196:199], a[20:23]\n" \
X_"a[180:181], v37 offset:0\n" \
"v_mad_i32_i24 v231, v232, s6, v231\n" \
M_"a[16:19], a[160:163], a[132:135], a[16:19]\n" \
"v_lshlrev_b32_e32 v233, 4, v231\n" \
"v_add_u32_e32 v234, s5, v233\n" \
M_"a[16:19], a[224:227], a[196:199], a[16:19]\n" \
"s_movk_i32 s5, 0x480\n" \
"v_mad_u32_u24 v0, v213, s5, v214\n" \
M_"a[32:35], a[160:163], a[136:139], a[32:35]\n" \
B_"v[204:207], v193, s[76:79], s34 offen offset:0\n" \
M_"a[32:35], a[224:227], a[200:203], a[32:35]\n" \
"s_mov_b32 m0, s32\n" \
X_"a[182:183], v37 offset:1024\n" \
M_"a[36:39], a[164:167], a[136:139], a[36:39]\n" \
"s_mov_b32 exec_lo, 0x0\n" \
"s_mov_b32 exec_hi, 0xff00\n" \
"global_load_lds_dword v[184:185], off\n" \
"s_mov_b64 exec, -1\n" \
M_"a[36:39], a[228:231], a[200:203], a[36:39]\n" \
X_"a[244:245], v37 offset:8192\n" \
"v_mul_u32_u24_e32 v213, 0x480, v216\n" \
M_"a[40:43], a[168:171], a[136:139], a[40:43]\n" \
"s_movk_i32 s6, 0xfc00\n" \
"s_nop 0\n" \
"v_add3_u32 v1, v213, v215, s6\n" \
M_"a[40:43], a[232:235], a[200:203], a[40:43]\n" \
"buffer_load_dword v208, v194, s[76:79], s34 offen offset:32\n" \
M_"a[44:47], a[172:175], a[136:139], a[44:47]\n" \
X_"a[246:247], v37 offset:9216\n" \
"v_mul_u32_u24_e32 v213, 0x480, v220\n" \
M_"a[44:47], a[236:239], a[200:203], a[44:47]\n" \
"v_add3_u32 v2, v213, v217, s7\n" \
"v_mul_u32_u24_e32 v213, 0x480, v222\n" \
M_"a[60:63], a[172:175], a[140:143], a[60:63]\n" \
"v_add3_u32 v3, v213, v221, s40\n" \
"v_mul_u32_u24_e32 v213, 0x480, v226\n" \
M_"a[60:63], a[236:239], a[204:207], a[60:63]\n" \
"v_add3_u32 v5, v213, v225, s6\n" \
"v_mul_u32_u24_e32 v213, 0x480, v228\n" \
M_"a[56:59], a[168:171], a[140:143], a[56:59]\n" \
"buffer_load_dword v209, v194, s[76:79], s34 offen offset:1184\n" \
M_"a[56:59], a[232:235], a[204:207], a[56:59]\n" \
X_"a[184:185], v38 offset:0\n" \
"v_add3_u32 v6, v213, v227, s7\n" \
M_"a[52:55], a[164:167], a[140:143], a[52:55]\n" \
"v_mul_u32_u24_e32 v213, 0x480, v230\n" \
"v_add3_u32 v7, v213, v229, s40\n" \
M_"a[52:55], a[228:231], a[204:207], a[52:55]\n" \
X_"a[186:187], v38 offset:1024\n" \
"s_lshl_b32 s7, s3, 3\n" \
M_"a[48:51], a[160:163], a[140:143], a[48:51]\n" \
"s_lshl_b32 s40, s3, 1\n" \
"s_and_b32 s7, s7, 16\n" \
M_"a[48:51], a[224:227], a[204:207], a[48:51]\n" \
"s_and_b32 s40, s40, 2\n" \
"s_and_b32 s6, s2, 7\n" \
M_"a[64:67], a[160:163], a[144:147], a[64:67]\n" \
"buffer_load_dword v211, v194, s[76:79], s34 offen offset:2336\n" \
M_"a[64:67], a[224:227], a[208:211], a[64:67]\n" \
X_"a[248:249], v38 offset:8192\n" \
"v_bfe_u32 v213, v20, 7, 1\n" \
M_"a[68:71], a[164:167], a[144:147], a[68:71]\n" \
"s_or_b32 s7, s40, s7\n" \
"v_and_b32_e32 v234, 0xf0, v234\n" \
M_"a[68:71], a[228:231], a[208:211], a[68:71]\n" \
X_"a[250:251], v38 offset:9216\n" \
"v_cmp_gt_u32_e64 vcc, 16, v231\n" \
M_"a[72:75], a[168:171], a[144:147], a[72:75]\n" \
"v_or3_b32 v213, s7, v213, v219\n" \
"v_and_b32_e64 v214, 32, v20\n" \
M_"a[72:75], a[232:235], a[208:211], a[72:75]\n" \
"s_mov_b32 s76, s20\n" \
"s_and_b32 s77, s21, 0xffff\n" \
M_"a[76:79], a[172:175], a[144:147], a[76:79]\n" \
"s_mov_b32 s78, 0x480\n" \
"s_lshl_b32 s6, s6, 5\n" \
M_"a[76:79], a[236:239], a[208:211], a[76:79]\n" \
"buffer_load_ushort v210, v195, s[76:79], 0 offen\n" \
M_"a[92:95], a[172:175], a[148:151], a[92:95]\n" \
X_"a[188:189], v39 offset:0\n" \
"v_cndmask_b32_e64 v231, v233, v234, vcc\n" \
M_"a[92:95], a[236:239], a[212:215], a[92:95]\n" \
"v_mul_u32_u24_e64 v213, 18, v213\n" \
"v_mov_b32_e32 v215, 0x100\n" \
M_"a[88:91], a[168:171], a[148:151], a[88:91]\n" \
"s_mov_b32 s76, s8\n" \
"s_and_b32 s77, s9, 0xffff\n" \
M_"a[88:91], a[232:235], a[212:215], a[88:91]\n" \
"s_mov_b32 s78, 0x240000\n" \
X_"a[190:191], v39 offset:1024\n" \
".fill (L_pb + 7904 - .) / 4, 4, 0xBF800000\n" \
"L_pp2:\n" \
"s_cbranch_execz L_pret\n" \
M_"a[84:87], a[164:167], a[148:151], a[84:87]\n" \
"v_mov_b32_e32 v216, s6\n" \
"v_cmp_eq_u32_e64 vcc, 0, v214\n" \
M_"a[84:87], a[228:231], a[212:215], a[84:87]\n" \
X_"a[252:253], v39 offset:8192\n" \
"v_and_or_b32 v213, v218, 1, v213\n" \
M_"a[80:83], a[160:163], a[148:151], a[80:83]\n" \
"v_lshlrev_b32_e32 v212, 3, v212\n" \
"v_cndmask_b32_e32 v214, v215, v216, vcc\n" \
M_"a[80:83], a[224:227], a[212:215], a[80:83]\n" \
"v_mad_u32_u24 v213, v213, s5, v214\n" \
"v_and_or_b32 v213, v20, 16, v213\n" \
M_"a[96:99], a[160:163], a[152:155], a[96:99]\n" \
X_"a[254:255], v39 offset:9216\n" \
"v_add_u32_e32 v9, 0x4800, v213\n" \
M_"a[96:99], a[224:227], a[216:219], a[96:99]\n" \
"v_mul_u32_u24_e32 v213, 0x120, v218\n" \
"v_and_or_b32 v10, v212, 24, v213\n" \
M_"a[100:103], a[164:167], a[152:155], a[100:103]\n" \
"v_lshrrev_b32_e64 v213, 2, v20\n" \
"v_and_or_b32 v213, v213, 60, s4\n" \
M_"a[100:103], a[228:231], a[216:219], a[100:103]\n" \
"v_lshl_or_b32 v212, s3, 9, v212\n" \
"v_mad_u32_u24 v4, v224, s5, v223\n" \
M_"a[104:107], a[168:171], a[152:155], a[104:107]\n" \
"v_mad_u32_u24 v8, v232, s5, v231\n" \
"v_or_b32_e32 v18, 0x27000, v213\n" \
M_"a[104:107], a[232:235], a[216:219], a[104:107]\n" \
"v_or_b32_e32 v19, 0x27800, v212\n" \
"v_mov_b32_e32 v215, 0x1000c0c\n" \
M_"a[108:111], a[172:175], a[152:155], a[108:111]\n" \
"v_mov_b32_e32 v216, 0xc0c0100\n" \
"v_bfe_u32 v214, v20, 4, 1\n" \
M_"a[108:111], a[236:239], a[216:219], a[108:111]\n" \
"v_lshrrev_b32_e32 v212, 8, v20\n" \
"v_cmp_eq_u32_e32 vcc, 0, v214\n" \
"s_waitcnt lgkmcnt(15)\n" \
M_"a[124:127], a[172:175], a[156:159], a[124:127]\n" \
"v_bfe_u32 v213, v20, 4, 4\n" \
"v_mov_b32_e32 v217, 0xc0c0302\n" \
M_"a[124:127], a[236:239], a[220:223], a[124:127]\n" \
"v_cndmask_b32_e32 v215, v215, v216, vcc\n" \
"v_mov_b32_e32 v216, 0x3020c0c\n" \
M_"a[120:123], a[168:171], a[156:159], a[120:123]\n" \
"v_lshl_or_b32 v214, v212, 2, v214\n" \
"v_cndmask_b32_e64 v216, v216, v217, vcc\n" \
M_"a[120:123], a[232:235], a[220:223], a[120:123]\n" \
"v_mov_b32_e32 v217, 0xc0c0c0c\n" \
"v_cmp_gt_u32_e64 vcc, v214, v213\n" \
M_"a[116:119], a[164:167], a[156:159], a[116:119]\n" \
"v_or_b32_e32 v214, 2, v214\n" \
"s_lshr_b32 s4, s2, 5\n" \
M_"a[116:119], a[228:231], a[220:223], a[116:119]\n" \
"v_cndmask_b32_e32 v40, v217, v215, vcc\n" \
"v_cndmask_b32_e32 v42, v215, v217, vcc\n" \
M_"a[112:115], a[160:163], a[156:159], a[112:115]\n" \
"v_cmp_gt_u32_e32 vcc, v214, v213\n" \
"v_and_b32_e32 v214, 16, v20\n" \
"s_waitcnt vmcnt(7) lgkmcnt(0)\n" \
M_"a[112:115], a[224:227], a[220:223], a[112:115]\n" \
"s_barrier\n" \
M_"v[52:55], a[176:179], a[128:131], v[52:55]\n" \
"s_add_u32 m0, s30, 0x0\n" \
"s_movk_i32 s84, 0x200\n" \
M_"v[52:55], a[240:243], a[192:195], v[52:55]\n" \
B_"v22, s[64:67], s84 offen offset:0 lds\n" \
M_"v[56:59], a[180:183], a[128:131], v[56:59]\n" \
X_"a[160:161], v32 offset:32768\n" \
"s_movk_i32 s42, 0x3f80\n" \
M_"v[56:59], a[244:247], a[192:195], v[56:59]\n" \
"s_movk_i32 s84, 0x1e00\n" \
M_"v[60:63], a[184:187], a[128:131], v[60:63]\n" \
B_"v23, s[64:67], s84 offen offset:1024 lds\n" \
M_"v[60:63], a[248:251], a[192:195], v[60:63]\n" \
X_"a[162:163], v32 offset:33792\n" \
"s_xor_b32 s5, s4, 15\n" \
M_"v[64:67], a[188:191], a[128:131], v[64:67]\n" \
X_"a[224:225], v32 offset:40960\n" \
M_"v[64:67], a[252:255], a[192:195], v[64:67]\n" \
R_"a[128:131], v28 offset:49152\n" \
M_"v[80:83], a[188:191], a[132:135], v[80:83]\n" \
R_"a[192:195], v29 offset:49152\n" \
M_"v[80:83], a[252:255], a[196:199], v[80:83]\n" \
"s_movk_i32 s84, 0x3a00\n" \
M_"v[76:79], a[184:187], a[132:135], v[76:79]\n" \
B_"v22, s[64:67], s84 offen offset:2048 lds\n" \
M_"v[76:79], a[248:251], a[196:199], v[76:79]\n" \
"s_movk_i32 s84, 0x5600\n" \
M_"v[72:75], a[180:183], a[132:135], v[72:75]\n" \
B_"v23, s[64:67], s84 offen offset:3072 lds\n" \
M_"v[72:75], a[244:247], a[196:199], v[72:75]\n" \
"s_add_u32 m0, s31, 0x0\n" \
"s_mov_b32 s84, 0x200000\n" \
M_"v[68:71], a[176:179], a[132:135], v[68:71]\n" \
B_"v26, s[68:71], s84 offen offset:0 lds\n" \
M_"v[68:71], a[240:243], a[196:199], v[68:71]\n" \
R_"a[132:135], v28 offset:51200\n" \
M_"v[84:87], a[176:179], a[136:139], v[84:87]\n" \
R_"a[196:199], v29 offset:51200\n" \
M_"v[84:87], a[240:243], a[200:203], v[84:87]\n" \
"s_addk_i32 s84, 0x7c00\n" \
M_"v[88:91], a[180:183], a[136:139], v[88:91]\n" \
B_"v26, s[68:71], s84 offen offset:1024 lds\n" \
M_"v[88:91], a[244:247], a[200:203], v[88:91]\n" \
X_"a[226:227], v32 offset:41984\n" \
M_"v[92:95], a[184:187], a[136:139], v[92:95]\n" \
"s_addk_i32 s84, 0x7c00\n" \
M_"v[92:95], a[248:251], a[200:203], v[92:95]\n" \
B_"v27, s[68:71], s84 offen offset:2048 lds\n" \
M_"v[96:99], a[188:191], a[136:139], v[96:99]\n" \
X_"a[164:165], v33 offset:32768\n" \
M_"v[96:99], a[252:255], a[200:203], v[96:99]\n" \
R_"a[136:139], v28 offset:53248\n" \
M_"v[112:115], a[188:191], a[140:143], v[112:115]\n" \
R_"a[200:203], v29 offset:53248\n" \
M_"v[112:115], a[252:255], a[204:207], v[112:115]\n" \
"s_addk_i32 s84, 0x7c00\n" \
"s_nop 0\n" \
M_"v[108:111], a[184:187], a[140:143], v[108:111]\n" \
B_"v27, s[68:71], s84 offen offset:3072 lds\n" \
M_"v[108:111], a[248:251], a[204:207], v[108:111]\n" \
"s_add_u32 m0, s30, 0x1000\n" \
"s_mov_b32 s84, 0x8200\n" \
M_"v[104:107], a[180:183], a[140:143], v[104:107]\n" \
B_"v22, s[64:67], s84 offen offset:0 lds\n" \
M_"v[104:107], a[244:247], a[204:207], v[104:107]\n" \
X_"a[166:167], v33 offset:33792\n" \
M_"v[100:103], a[176:179], a[140:143], v[100:103]\n" \
X_"a[228:229], v33 offset:40960\n" \
M_"v[100:103], a[240:243], a[204:207], v[100:103]\n" \
R_"a[140:143], v28 offset:55296\n" \
M_"v[116:119], a[176:179], a[144:147], v[116:119]\n" \
R_"a[204:207], v29 offset:55296\n" \
M_"v[116:119], a[240:243], a[208:211], v[116:119]\n" \
"s_addk_i32 s84, 0x1c00\n" \
M_"v[120:123], a[180:183], a[144:147], v[120:123]\n" \
B_"v23, s[64:67], s84 offen offset:1024 lds\n" \
M_"v[120:123], a[244:247], a[208:211], v[120:123]\n" \
"s_addk_i32 s84, 0x1c00\n" \
M_"v[124:127], a[184:187], a[144:147], v[124:127]\n" \
B_"v22, s[64:67], s84 offen offset:2048 lds\n" \
M_"v[124:127], a[248:251], a[208:211], v[124:127]\n" \
"s_addk_i32 s84, 0x1c00\n" \
"s_nop 0\n" \
M_"v[128:131], a[188:191], a[144:147], v[128:131]\n" \
B_"v23, s[64:67], s84 offen offset:3072 lds\n" \
M_"v[128:131], a[252:255], a[208:211], v[128:131]\n" \
R_"a[144:147], v28 offset:32768\n" \
M_"v[144:147], a[188:191], a[148:151], v[144:147]\n" \
R_"a[208:211], v29 offset:32768\n" \
M_"v[144:147], a[252:255], a[212:215], v[144:147]\n" \
"s_add_u32 m0, s31, 0x4000\n" \
"s_mov_b32 s84, 0x200000\n" \
M_"v[140:143], a[184:187], a[148:151], v[140:143]\n" \
B_"v26, s[72:75], s84 offen offset:0 lds\n" \
M_"v[140:143], a[248:251], a[212:215], v[140:143]\n" \
X_"a[230:231], v33 offset:41984\n" \
M_"v[136:139], a[180:183], a[148:151], v[136:139]\n" \
"s_addk_i32 s84, 0x7c00\n" \
"s_nop 0\n" \
M_"v[136:139], a[244:247], a[212:215], v[136:139]\n" \
B_"v26, s[72:75], s84 offen offset:1024 lds\n" \
M_"v[132:135], a[176:179], a[148:151], v[132:135]\n" \
X_"a[168:169], v34 offset:32768\n" \
M_"v[132:135], a[240:243], a[212:215], v[132:135]\n" \
R_"a[148:151], v28 offset:34816\n" \
M_"v[148:151], a[176:179], a[152:155], v[148:151]\n" \
R_"a[212:215], v29 offset:34816\n" \
M_"v[148:151], a[240:243], a[216:219], v[148:151]\n" \
X_"a[170:171], v34 offset:33792\n" \
M_"v[152:155], a[180:183], a[152:155], v[152:155]\n" \
X_"a[232:233], v34 offset:40960\n" \
M_"v[152:155], a[244:247], a[216:219], v[152:155]\n" \
X_"a[234:235], v34 offset:41984\n" \
M_"v[156:159], a[184:187], a[152:155], v[156:159]\n" \
X_"a[172:173], v35 offset:32768\n" \
M_"v[156:159], a[248:251], a[216:219], v[156:159]\n" \
X_"a[174:175], v35 offset:33792\n" \
M_"v[160:163], a[188:191], a[152:155], v[160:163]\n" \
X_"a[236:237], v35 offset:40960\n" \
M_"v[160:163], a[252:255], a[216:219], v[160:163]\n" \
R_"a[152:155], v28 offset:36864\n" \
M_"v[176:179], a[188:191], a[156:159], v[176:179]\n" \
R_"a[216:219], v29 offset:36864\n" \
M_"v[176:179], a[252:255], a[220:223], v[176:179]\n" \
X_"a[238:239], v35 offset:41984\n" \
M_"v[172:175], a[184:187], a[156:159], v[172:175]\n" \
"s_cmpk_lt_u32 s3, 2\n" \
"s_cselect_b32 s6, s5, s4\n" \
M_"v[172:175], a[248:251], a[220:223], v[172:175]\n" \
"s_lshl_b32 s7, s6, 7\n" \
"v_and_b32_e32 v213, 0x320, v20\n" \
M_"v[168:171], a[180:183], a[156:159], v[168:171]\n" \
"s_movk_i32 s41, 0x100\n" \
"v_lshlrev_b32_e64 v214, v214, s42\n" \
M_"v[168:171], a[244:247], a[220:223], v[168:171]\n" \
"v_mov_b32_e32 v215, 0\n" \
"s_add_i32 s40, s7, -16\n" \
M_"v[164:167], a[176:179], a[156:159], v[164:167]\n" \
"v_cndmask_b32_e32 v41, v217, v216, vcc\n" \
"v_cndmask_b32_e32 v43, v216, v217, vcc\n" \
M_"v[164:167], a[240:243], a[220:223], v[164:167]\n" \
R_"a[156:159], v28 offset:38912\n" \
R_"a[220:223], v29 offset:38912\n" \
"s_waitcnt lgkmcnt(15)\n" \
M_"a[0:3], a[160:163], a[128:131], a[0:3]\n" \
"s_addk_i32 s84, 0x7c00\n" \
M_"a[0:3], a[224:227], a[192:195], a[0:3]\n" \
B_"v27, s[72:75], s84 offen offset:2048 lds\n" \
M_"a[4:7], a[164:167], a[128:131], a[4:7]\n" \
X_"a[176:177], v36 offset:32768\n" \
"v_cmp_eq_u32_e32 vcc, s41, v213\n" \
"s_waitcnt lgkmcnt(15)\n" \
M_"a[4:7], a[228:231], a[192:195], a[4:7]\n" \
"s_addk_i32 s84, 0x7c00\n" \
"s_waitcnt lgkmcnt(11)\n" \
M_"a[8:11], a[168:171], a[128:131], a[8:11]\n" \
B_"v27, s[72:75], s84 offen offset:3072 lds\n" \
"s_waitcnt lgkmcnt(9)\n" \
M_"a[8:11], a[232:235], a[192:195], a[8:11]\n" \
X_"a[178:179], v36 offset:33792\n" \
"s_mov_b32 s41, 0\n" \
"s_waitcnt lgkmcnt(8)\n" \
M_"a[12:15], a[172:175], a[128:131], a[12:15]\n" \
X_"a[240:241], v36 offset:40960\n" \
"s_and_b32 s43, s3, 1\n" \
"s_waitcnt lgkmcnt(5)\n" \
M_"a[12:15], a[236:239], a[192:195], a[12:15]\n" \
"v_cndmask_b32_e32 v46, v215, v214, vcc\n" \
"v_and_b32_e32 v213, 0x330, v20\n" \
M_"a[28:31], a[172:175], a[132:135], a[28:31]\n" \
"s_cmp_lg_u32 s7, 0\n" \
"s_cselect_b32 s40, s40, s41\n" \
M_"a[28:31], a[236:239], a[196:199], a[28:31]\n" \
X_"a[242:243], v36 offset:41984\n" \
"s_mov_b32 s42, 0xff800000\n" \
M_"a[24:27], a[168:171], a[132:135], a[24:27]\n" \
"s_cmp_lg_u32 s7, 0\n" \
"s_cselect_b32 s47, s41, s42\n" \
M_"a[24:27], a[232:235], a[196:199], a[24:27]\n" \
"s_lshl_b32 s41, s43, 6\n" \
"v_lshrrev_b32_e32 v215, 6, v20\n" \
M_"a[20:23], a[164:167], a[132:135], a[20:23]\n" \
"v_mov_b32_e32 v214, 0x3f80\n" \
"v_cmp_eq_u32_e64 vcc, 32, v213\n" \
M_"a[20:23], a[228:231], a[196:199], a[20:23]\n" \
X_"a[180:181], v37 offset:32768\n" \
"s_or_b32 s7, s41, s7\n" \
M_"a[16:19], a[160:163], a[132:135], a[16:19]\n" \
"v_lshrrev_b32_e32 v213, 7, v20\n" \
"v_bfe_u32 v216, v20, 6, 2\n" \
M_"a[16:19], a[224:227], a[196:199], a[16:19]\n" \
"v_lshrrev_b32_e64 v219, 1, v20\n" \
"v_bfe_u32 v215, v215, 1, 1\n" \
M_"a[32:35], a[160:163], a[136:139], a[32:35]\n" \
"s_waitcnt vmcnt(16)\n" \
"v_or_b32_e32 v204, v210, v204\n" \
"v_or_b32_e64 v206, v210, v206\n" \
M_"a[32:35], a[224:227], a[200:203], a[32:35]\n" \
"v_mov_b32_e64 v210, 0\n" \
"s_add_u32 s88, s35, 0x1000\n" \
M_"a[36:39], a[164:167], a[136:139], a[36:39]\n" \
X_"a[182:183], v37 offset:33792\n" \
"v_cndmask_b32_e64 v47, 0, v214, vcc\n" \
M_"a[36:39], a[228:231], a[200:203], a[36:39]\n" \
X_"a[244:245], v37 offset:40960\n" \
"v_or_b32_e64 v214, s7, v213\n" \
M_"a[40:43], a[168:171], a[136:139], a[40:43]\n" \
"s_mov_b32 m0, s32\n" \
"v_lshl_or_b32 v217, s43, 3, v213\n" \
M_"a[40:43], a[232:235], a[200:203], a[40:43]\n" \
"s_mov_b32 exec_lo, 0xffffffff\n" \
"s_mov_b32 exec_hi, 0x0\n" \
"global_load_lds_dword v[188:189], off\n" \
"s_mov_b64 exec, -1\n" \
M_"a[44:47], a[172:175], a[136:139], a[44:47]\n" \
X_"a[246:247], v37 offset:41984\n" \
"v_lshlrev_b32_e64 v218, 10, v212\n" \
M_"a[44:47], a[236:239], a[200:203], a[44:47]\n" \
"v_lshlrev_b32_e32 v216, 7, v216\n" \
"v_and_b32_e32 v219, 24, v219\n" \
M_"a[60:63], a[172:175], a[140:143], a[60:63]\n" \
"ds_write_b128 v196, v[200:203]\n" \
M_"a[60:63], a[236:239], a[204:207], a[60:63]\n" \
"v_and_or_b32 v213, v213, 2, v215\n" \
"v_add_u32_e64 v217, s40, v217\n" \
M_"a[56:59], a[168:171], a[140:143], a[56:59]\n" \
X_"a[184:185], v38 offset:32768\n" \
"v_or3_b32 v216, v216, v218, v219\n" \
M_"a[56:59], a[232:235], a[204:207], a[56:59]\n" \
"v_lshlrev_b32_e64 v213, 5, v213\n" \
"s_mov_b32 s40, 0x14000\n" \
M_"a[52:55], a[164:167], a[140:143], a[52:55]\n" \
"s_lshl_b32 s7, s2, 2\n" \
"v_or3_b32 v48, v216, v213, s40\n" \
M_"a[52:55], a[228:231], a[204:207], a[52:55]\n" \
X_"a[186:187], v38 offset:33792\n" \
"s_lshl_b32 s40, s43, 2\n" \
M_"a[48:51], a[160:163], a[140:143], a[48:51]\n" \
"s_lshl_b32 s6, s6, 3\n" \
"v_or_b32_e32 v213, s40, v212\n" \
M_"a[48:51], a[224:227], a[204:207], a[48:51]\n" \
"s_or_b32 s6, s6, s40\n" \
"s_and_b32 s40, s7, 0x7c\n" \
M_"a[64:67], a[160:163], a[144:147], a[64:67]\n" \
"ds_write_b128 v197, v[204:207]\n" \
M_"a[64:67], a[224:227], a[208:211], a[64:67]\n" \
X_"a[248:249], v38 offset:40960\n" \
"s_or_b32 s40, s40, s3\n" \
M_"a[68:71], a[164:167], a[144:147], a[68:71]\n" \
"s_cmpk_lt_u32 s40, 64\n" \
"s_cselect_b32 s41, s4, s5\n" \
M_"a[68:71], a[228:231], a[208:211], a[68:71]\n" \
X_"a[250:251], v38 offset:41984\n" \
"s_lshl_b32 s40, s40, 1\n" \
M_"a[72:75], a[168:171], a[144:147], a[72:75]\n" \
"s_lshl_b32 s41, s41, 7\n" \
"s_and_b32 s40, s40, 0x7e\n" \
M_"a[72:75], a[232:235], a[208:211], a[72:75]\n" \
"s_or_b32 s40, s41, s40\n" \
"s_mulk_i32 s40, 0x480\n" \
M_"a[76:79], a[172:175], a[144:147], a[76:79]\n" \
"v_add_u32_e64 v49, s40, v20\n" \
"s_mov_b32 s40, 0xc000\n" \
M_"a[76:79], a[236:239], a[208:211], a[76:79]\n" \
X_"a[188:189], v39 offset:32768\n" \
"s_and_b32 s7, s7, 28\n" \
M_"a[92:95], a[172:175], a[148:151], a[92:95]\n" \
"ds_write_b128 v198, v[208:211]\n" \
M_"a[92:95], a[236:239], a[212:215], a[92:95]\n" \
"s_bfe_u32 s45, s2, 0x20003\n" \
"s_mov_b32 s41, 0x8000\n" \
M_"a[88:91], a[168:171], a[148:151], a[88:91]\n" \
X_"a[190:191], v39 offset:33792\n" \
M_"a[88:91], a[232:235], a[212:215], a[88:91]\n" \
"s_cmp_lg_u32 s4, 0\n" \
"s_cselect_b32 s51, s40, s41\n" \
M_"a[84:87], a[164:167], a[148:151], a[84:87]\n" \
"s_movk_i32 s40, 0x4000\n" \
"s_lshr_b32 s44, s3, 1\n" \
M_"a[84:87], a[228:231], a[212:215], a[84:87]\n" \
X_"a[252:253], v39 offset:40960\n" \
"s_or_b32 s46, s7, s45\n" \
M_"a[80:83], a[160:163], a[148:151], a[80:83]\n" \
"s_cmp_lg_u32 s4, 0\n" \
"s_cselect_b32 s53, s41, s40\n" \
M_"a[80:83], a[224:227], a[212:215], a[80:83]\n" \
"s_lshl_b32 s40, s44, 15\n" \
"s_lshl_b32 s41, s43, 14\n" \
M_"a[96:99], a[160:163], a[152:155], a[96:99]\n" \
X_"a[254:255], v39 offset:41984\n" \
"s_lshl_b32 s48, s46, 20\n" \
M_"a[96:99], a[224:227], a[216:219], a[96:99]\n" \
"s_or_b32 s41, s40, s41\n" \
"s_add_i32 s49, s48, 0x1000000\n" \
M_"a[100:103], a[164:167], a[152:155], a[100:103]\n" \
"s_or_b32 s42, s49, s41\n" \
"s_lshl_b32 s4, s4, 16\n" \
M_"a[100:103], a[228:231], a[216:219], a[100:103]\n" \
"s_or_b32 s41, s42, s4\n" \
"s_lshl_b32 s4, s5, 16\n" \
M_"a[104:107], a[168:171], a[152:155], a[104:107]\n" \
"s_mul_i32 s5, s6, 0x24800\n" \
"s_lshl_b32 s6, s46, 9\n" \
M_"a[104:107], a[232:235], a[216:219], a[104:107]\n" \
"s_lshl_b32 s7, s7, 6\n" \
"s_add_i32 s6, s5, s6\n" \
M_"a[108:111], a[172:175], a[152:155], a[108:111]\n" \
"s_or_b32 s5, s5, s7\n" \
"s_lshl_b32 s7, s45, 2\n" \
M_"a[108:111], a[236:239], a[216:219], a[108:111]\n" \
"s_or_b32 s5, s5, s7\n" \
"s_lshl_b32 s7, s3, 12\n" \
"s_waitcnt lgkmcnt(15)\n" \
M_"a[124:127], a[172:175], a[156:159], a[124:127]\n" \
"s_or_b32 s50, s49, s7\n" \
"s_lshl_b32 s7, s3, 11\n" \
M_"a[124:127], a[236:239], a[220:223], a[124:127]\n" \
"v_lshrrev_b32_e64 v215, 4, v20\n" \
"v_bfe_u32 v216, v20, 4, 3\n" \
M_"a[120:123], a[168:171], a[156:159], a[120:123]\n" \
"s_or_b32 s42, s42, s4\n" \
"s_lshl_b32 s4, s43, 11\n" \
M_"a[120:123], a[232:235], a[220:223], a[120:123]\n" \
"s_add_i32 s52, s48, s7\n" \
"s_add_i32 s53, s53, s7\n" \
M_"a[116:119], a[164:167], a[156:159], a[116:119]\n" \
"v_mul_u32_u24_e32 v214, 0x480, v214\n" \
"v_bitop3_b32 v218, v212, v215, 7 bitop3:0x78\n" \
M_"a[116:119], a[228:231], a[220:223], a[116:119]\n" \
"v_bitop3_b32 v212, v212, v216, 4 bitop3:0x36\n" \
"s_lshl_b32 s7, s3, 10\n" \
M_"a[112:115], a[160:163], a[156:159], a[112:115]\n" \
"s_or_b32 s4, s40, s4\n" \
"v_lshl_or_b32 v25, v212, 4, v214\n" \
"s_waitcnt vmcnt(1) lgkmcnt(0)\n" \
M_"a[112:115], a[224:227], a[220:223], a[112:115]\n" \
"s_barrier\n" \
M_"v[52:55], a[176:179], a[128:131], v[52:55]\n" \
"s_add_u32 m0, s30, 0x8000\n" \
"s_movk_i32 s84, 0x280\n" \
M_"v[52:55], a[240:243], a[192:195], v[52:55]\n" \
B_"v22, s[64:67], s84 offen offset:0 lds\n" \
M_"v[56:59], a[180:183], a[128:131], v[56:59]\n" \
"s_movk_i32 s84, 0x1e80\n" \
M_"v[56:59], a[244:247], a[192:195], v[56:59]\n" \
B_"v23, s[64:67], s84 offen offset:1024 lds\n" \
M_"v[60:63], a[184:187], a[128:131], v[60:63]\n" \
X_"a[160:161], v32 offset:0\n" \
"v_mul_u32_u24_e32 v212, 0x480, v217\n" \
M_"v[60:63], a[248:251], a[192:195], v[60:63]\n" \
"s_movk_i32 s84, 0x3a80\n" \
M_"v[64:67], a[188:191], a[128:131], v[64:67]\n" \
B_"v22, s[64:67], s84 offen offset:2048 lds\n" \
M_"v[64:67], a[252:255], a[192:195], v[64:67]\n" \
R_"a[128:131], v28 offset:16384\n" \
"v_bitop3_b32 v213, v215, v213, 7 bitop3:0x6c\n" \
M_"v[80:83], a[188:191], a[132:135], v[80:83]\n" \
R_"a[192:195], v29 offset:16384\n" \
"s_or_b32 s40, s7, 0x14000\n" \
M_"v[80:83], a[252:255], a[196:199], v[80:83]\n" \
"s_movk_i32 s84, 0x5680\n" \
M_"v[76:79], a[184:187], a[132:135], v[76:79]\n" \
B_"v23, s[64:67], s84 offen offset:3072 lds\n" \
M_"v[76:79], a[248:251], a[196:199], v[76:79]\n" \
"s_add_u32 m0, s31, 0x8000\n" \
"s_mov_b32 s84, 0x280000\n" \
M_"v[72:75], a[180:183], a[132:135], v[72:75]\n" \
B_"v26, s[68:71], s84 offen offset:0 lds\n" \
M_"v[72:75], a[244:247], a[196:199], v[72:75]\n" \
X_"a[162:163], v32 offset:1024\n" \
"s_lshl_b32 s43, s44, 13\n" \
M_"v[68:71], a[176:179], a[132:135], v[68:71]\n" \
X_"a[224:225], v32 offset:8192\n" \
"s_mulk_i32 s44, 0xe800\n" \
M_"v[68:71], a[240:243], a[196:199], v[68:71]\n" \
R_"a[132:135], v28 offset:18432\n" \
"s_lshl_b32 s7, s3, 8\n" \
M_"v[84:87], a[176:179], a[136:139], v[84:87]\n" \
R_"a[196:199], v29 offset:18432\n" \
"v_lshl_or_b32 v24, v218, 4, v214\n" \
M_"v[84:87], a[240:243], a[200:203], v[84:87]\n" \
"s_addk_i32 s84, 0x7c00\n" \
M_"v[88:91], a[180:183], a[136:139], v[88:91]\n" \
B_"v26, s[68:71], s84 offen offset:1024 lds\n" \
M_"v[88:91], a[244:247], a[200:203], v[88:91]\n" \
"s_addk_i32 s84, 0x7c00\n" \
M_"v[92:95], a[184:187], a[136:139], v[92:95]\n" \
B_"v27, s[68:71], s84 offen offset:2048 lds\n" \
M_"v[92:95], a[248:251], a[200:203], v[92:95]\n" \
"s_addk_i32 s84, 0x7c00\n" \
M_"v[96:99], a[188:191], a[136:139], v[96:99]\n" \
B_"v27, s[68:71], s84 offen offset:3072 lds\n" \
M_"v[96:99], a[252:255], a[200:203], v[96:99]\n" \
R_"a[136:139], v28 offset:20480\n" \
"v_lshl_or_b32 v21, v213, 4, v212\n" \
M_"v[112:115], a[188:191], a[140:143], v[112:115]\n" \
R_"a[200:203], v29 offset:20480\n" \
"s_add_i32 s44, s44, 0x17000\n" \
M_"v[112:115], a[252:255], a[204:207], v[112:115]\n" \
"s_add_u32 m0, s30, 0x9000\n" \
"s_mov_b32 s84, 0x8280\n" \
M_"v[108:111], a[184:187], a[140:143], v[108:111]\n" \
B_"v22, s[64:67], s84 offen offset:0 lds\n" \
M_"v[108:111], a[248:251], a[204:207], v[108:111]\n" \
"s_addk_i32 s84, 0x1c00\n" \
M_"v[104:107], a[180:183], a[140:143], v[104:107]\n" \
B_"v23, s[64:67], s84 offen offset:1024 lds\n" \
M_"v[104:107], a[244:247], a[204:207], v[104:107]\n" \
"s_addk_i32 s84, 0x1c00\n" \
M_"v[100:103], a[176:179], a[140:143], v[100:103]\n" \
B_"v22, s[64:67], s84 offen offset:2048 lds\n" \
M_"v[100:103], a[240:243], a[204:207], v[100:103]\n" \
R_"a[140:143], v28 offset:22528\n" \
"s_or_b32 s45, s7, 0x27000\n" \
M_"v[116:119], a[176:179], a[144:147], v[116:119]\n" \
R_"a[204:207], v29 offset:22528\n" \
"s_or_b32 s46, s4, 0x10000\n" \
M_"v[116:119], a[240:243], a[208:211], v[116:119]\n" \
"s_addk_i32 s84, 0x1c00\n" \
M_"v[120:123], a[180:183], a[144:147], v[120:123]\n" \
B_"v23, s[64:67], s84 offen offset:3072 lds\n" \
M_"v[120:123], a[244:247], a[208:211], v[120:123]\n" \
"s_add_u32 m0, s31, 0xc000\n" \
"s_mov_b32 s84, 0x280000\n" \
M_"v[124:127], a[184:187], a[144:147], v[124:127]\n" \
B_"v26, s[72:75], s84 offen offset:0 lds\n" \
M_"v[124:127], a[248:251], a[208:211], v[124:127]\n" \
"s_addk_i32 s84, 0x7c00\n" \
M_"v[128:131], a[188:191], a[144:147], v[128:131]\n" \
B_"v26, s[72:75], s84 offen offset:1024 lds\n" \
M_"v[128:131], a[252:255], a[208:211], v[128:131]\n" \
R_"a[144:147], v28 offset:0\n" \
"s_add_i32 s48, s6, 0x3000000\n" \
M_"v[144:147], a[188:191], a[148:151], v[144:147]\n" \
R_"a[208:211], v29 offset:0\n" \
"s_add_i32 s49, s5, 0x3004000\n" \
M_"v[144:147], a[252:255], a[212:215], v[144:147]\n" \
R_"v[200:203], v199 offset:0\n" \
"s_add_i32 s52, s52, 0x1010000\n" \
M_"v[140:143], a[184:187], a[148:151], v[140:143]\n" \
R_"v[204:207], v199 offset:4096\n" \
M_"v[140:143], a[248:251], a[212:215], v[140:143]\n" \
X_"a[226:227], v32 offset:9216\n" \
M_"v[136:139], a[180:183], a[148:151], v[136:139]\n" \
X_"a[164:165], v33 offset:0\n" \
M_"v[136:139], a[244:247], a[212:215], v[136:139]\n" \
R_"v[208:211], v199 offset:8192\n" \
M_"v[132:135], a[176:179], a[148:151], v[132:135]\n" \
X_"a[166:167], v33 offset:1024\n" \
M_"v[132:135], a[240:243], a[212:215], v[132:135]\n" \
R_"a[148:151], v28 offset:2048\n" \
M_"v[148:151], a[176:179], a[152:155], v[148:151]\n" \
R_"a[212:215], v29 offset:2048\n" \
M_"v[148:151], a[240:243], a[216:219], v[148:151]\n" \
X_"a[228:229], v33 offset:8192\n" \
M_"v[152:155], a[180:183], a[152:155], v[152:155]\n" \
X_"a[230:231], v33 offset:9216\n" \
M_"v[152:155], a[244:247], a[216:219], v[152:155]\n" \
X_"a[168:169], v34 offset:0\n" \
M_"v[156:159], a[184:187], a[152:155], v[156:159]\n" \
X_"a[170:171], v34 offset:1024\n" \
M_"v[156:159], a[248:251], a[216:219], v[156:159]\n" \
X_"a[232:233], v34 offset:8192\n" \
M_"v[160:163], a[188:191], a[152:155], v[160:163]\n" \
X_"a[234:235], v34 offset:9216\n" \
M_"v[160:163], a[252:255], a[216:219], v[160:163]\n" \
R_"a[152:155], v28 offset:4096\n" \
M_"v[176:179], a[188:191], a[156:159], v[176:179]\n" \
R_"a[216:219], v29 offset:4096\n" \
M_"v[176:179], a[252:255], a[220:223], v[176:179]\n" \
X_"a[172:173], v35 offset:0\n" \
M_"v[172:175], a[184:187], a[156:159], v[172:175]\n" \
X_"a[174:175], v35 offset:1024\n" \
M_"v[172:175], a[248:251], a[220:223], v[172:175]\n" \
X_"a[236:237], v35 offset:8192\n" \
M_"v[168:171], a[180:183], a[156:159], v[168:171]\n" \
X_"a[238:239], v35 offset:9216\n" \
M_"v[168:171], a[244:247], a[220:223], v[168:171]\n" \
M_"v[164:167], a[176:179], a[156:159], v[164:167]\n" \
M_"v[164:167], a[240:243], a[220:223], v[164:167]\n" \
R_"a[156:159], v28 offset:6144\n" \
R_"a[220:223], v29 offset:6144\n" \
"s_waitcnt lgkmcnt(15)\n" \
M_"a[0:3], a[160:163], a[128:131], a[0:3]\n" \
"s_addk_i32 s84, 0x7c00\n" \
M_"a[0:3], a[224:227], a[192:195], a[0:3]\n" \
B_"v27, s[72:75], s84 offen offset:2048 lds\n" \
M_"a[4:7], a[164:167], a[128:131], a[4:7]\n" \
X_"a[176:177], v36 offset:0\n" \
"s_waitcnt lgkmcnt(13)\n" \
M_"a[4:7], a[228:231], a[192:195], a[4:7]\n" \
"s_addk_i32 s84, 0x7c00\n" \
"s_waitcnt lgkmcnt(11)\n" \
M_"a[8:11], a[168:171], a[128:131], a[8:11]\n" \
B_"v27, s[72:75], s84 offen offset:3072 lds\n" \
"s_waitcnt lgkmcnt(9)\n" \
M_"a[8:11], a[232:235], a[192:195], a[8:11]\n" \
X_"a[178:179], v36 offset:1024\n" \
"s_waitcnt lgkmcnt(6)\n" \
M_"a[12:15], a[172:175], a[128:131], a[12:15]\n" \
X_"a[240:241], v36 offset:8192\n" \
"s_waitcnt lgkmcnt(5)\n" \
M_"a[12:15], a[236:239], a[192:195], a[12:15]\n" \
M_"a[28:31], a[172:175], a[132:135], a[28:31]\n" \
M_"a[28:31], a[236:239], a[196:199], a[28:31]\n" \
X_"a[242:243], v36 offset:9216\n" \
M_"a[24:27], a[168:171], a[132:135], a[24:27]\n" \
M_"a[24:27], a[232:235], a[196:199], a[24:27]\n" \
M_"a[20:23], a[164:167], a[132:135], a[20:23]\n" \
M_"a[20:23], a[228:231], a[196:199], a[20:23]\n" \
X_"a[180:181], v37 offset:0\n" \
M_"a[16:19], a[160:163], a[132:135], a[16:19]\n" \
M_"a[16:19], a[224:227], a[196:199], a[16:19]\n" \
M_"a[32:35], a[160:163], a[136:139], a[32:35]\n" \
X_"a[182:183], v37 offset:1024\n" \
M_"a[32:35], a[224:227], a[200:203], a[32:35]\n" \
M_"a[36:39], a[164:167], a[136:139], a[36:39]\n" \
M_"a[36:39], a[228:231], a[200:203], a[36:39]\n" \
"buffer_store_dwordx4 v[200:203], v20, s[80:83], s35 offen sc1\n" \
".fill (L_pb + 12000 - .) / 4, 4, 0xBF800000\n" \
"L_pp3:\n" \
"s_cbranch_execz L_pret\n" \
M_"a[40:43], a[168:171], a[136:139], a[40:43]\n" \
"s_mov_b32 m0, s32\n" \
X_"a[244:245], v37 offset:8192\n" \
M_"a[40:43], a[232:235], a[200:203], a[40:43]\n" \
"s_mov_b32 exec_lo, 0x0\n" \
"s_mov_b32 exec_hi, 0xffffffff\n" \
"global_load_lds_dword v[188:189], off\n" \
"s_mov_b64 exec, -1\n" \
"s_nop 0\n" \
M_"a[44:47], a[172:175], a[136:139], a[44:47]\n" \
X_"a[246:247], v37 offset:9216\n" \
M_"a[44:47], a[236:239], a[200:203], a[44:47]\n" \
M_"a[60:63], a[172:175], a[140:143], a[60:63]\n" \
M_"a[60:63], a[236:239], a[204:207], a[60:63]\n" \
M_"a[56:59], a[168:171], a[140:143], a[56:59]\n" \
X_"a[184:185], v38 offset:0\n" \
M_"a[56:59], a[232:235], a[204:207], a[56:59]\n" \
M_"a[52:55], a[164:167], a[140:143], a[52:55]\n" \
"buffer_store_dwordx4 v[204:207], v20, s[80:83], s88 offen sc1\n" \
M_"a[52:55], a[228:231], a[204:207], a[52:55]\n" \
X_"a[186:187], v38 offset:1024\n" \
M_"a[48:51], a[160:163], a[140:143], a[48:51]\n" \
M_"a[48:51], a[224:227], a[204:207], a[48:51]\n" \
M_"a[64:67], a[160:163], a[144:147], a[64:67]\n" \
X_"a[248:249], v38 offset:8192\n" \
M_"a[64:67], a[224:227], a[208:211], a[64:67]\n" \
M_"a[68:71], a[164:167], a[144:147], a[68:71]\n" \
M_"a[68:71], a[228:231], a[208:211], a[68:71]\n" \
X_"a[250:251], v38 offset:9216\n" \
M_"a[72:75], a[168:171], a[144:147], a[72:75]\n" \
M_"a[72:75], a[232:235], a[208:211], a[72:75]\n" \
M_"a[76:79], a[172:175], a[144:147], a[76:79]\n" \
"buffer_store_dwordx4 v[208:211], v20, s[80:83], s36 offen sc1\n" \
M_"a[76:79], a[236:239], a[208:211], a[76:79]\n" \
X_"a[188:189], v39 offset:0\n" \
M_"a[92:95], a[172:175], a[148:151], a[92:95]\n" \
M_"a[92:95], a[236:239], a[212:215], a[92:95]\n" \
M_"a[88:91], a[168:171], a[148:151], a[88:91]\n" \
X_"a[190:191], v39 offset:1024\n" \
M_"a[88:91], a[232:235], a[212:215], a[88:91]\n" \
M_"a[84:87], a[164:167], a[148:151], a[84:87]\n" \
M_"a[84:87], a[228:231], a[212:215], a[84:87]\n" \
X_"a[252:253], v39 offset:8192\n" \
M_"a[80:83], a[160:163], a[148:151], a[80:83]\n" \
M_"a[80:83], a[224:227], a[212:215], a[80:83]\n" \
M_"a[96:99], a[160:163], a[152:155], a[96:99]\n" \
X_"a[254:255], v39 offset:9216\n" \
M_"a[96:99], a[224:227], a[216:219], a[96:99]\n" \
M_"a[100:103], a[164:167], a[152:155], a[100:103]\n" \
M_"a[100:103], a[228:231], a[216:219], a[100:103]\n" \
M_"a[104:107], a[168:171], a[152:155], a[104:107]\n" \
M_"a[104:107], a[232:235], a[216:219], a[104:107]\n" \
M_"a[108:111], a[172:175], a[152:155], a[108:111]\n" \
M_"a[108:111], a[236:239], a[216:219], a[108:111]\n" \
"s_waitcnt lgkmcnt(15)\n" \
M_"a[124:127], a[172:175], a[156:159], a[124:127]\n" \
M_"a[124:127], a[236:239], a[220:223], a[124:127]\n" \
M_"a[120:123], a[168:171], a[156:159], a[120:123]\n" \
M_"a[120:123], a[232:235], a[220:223], a[120:123]\n" \
M_"a[116:119], a[164:167], a[156:159], a[116:119]\n" \
M_"a[116:119], a[228:231], a[220:223], a[116:119]\n" \
M_"a[112:115], a[160:163], a[156:159], a[112:115]\n" \
"s_waitcnt vmcnt(4) lgkmcnt(0)\n" \
M_"a[112:115], a[224:227], a[220:223], a[112:115]\n" \
"s_barrier\n" \
M_"v[52:55], a[176:179], a[128:131], v[52:55]\n" \
"s_add_u32 m0, s30, 0x0\n" \
"s_movk_i32 s84, 0x300\n" \
M_"v[52:55], a[240:243], a[192:195], v[52:55]\n" \
B_"v22, s[64:67], s84 offen offset:0 lds\n" \
M_"v[56:59], a[180:183], a[128:131], v[56:59]\n" \
X_"a[160:161], v32 offset:32768\n" \
M_"v[56:59], a[244:247], a[192:195], v[56:59]\n" \
"s_movk_i32 s84, 0x1f00\n" \
M_"v[60:63], a[184:187], a[128:131], v[60:63]\n" \
B_"v23, s[64:67], s84 offen offset:1024 lds\n" \
M_"v[60:63], a[248:251], a[192:195], v[60:63]\n" \
X_"a[162:163], v32 offset:33792\n" \
M_"v[64:67], a[188:191], a[128:131], v[64:67]\n" \
X_"a[224:225], v32 offset:40960\n" \
M_"v[64:67], a[252:255], a[192:195], v[64:67]\n" \
R_"a[128:131], v28 offset:49152\n" \
M_"v[80:83], a[188:191], a[132:135], v[80:83]\n" \
R_"a[192:195], v29 offset:49152\n" \
M_"v[80:83], a[252:255], a[196:199], v[80:83]\n" \
"s_movk_i32 s84, 0x3b00\n" \
M_"v[76:79], a[184:187], a[132:135], v[76:79]\n" \
B_"v22, s[64:67], s84 offen offset:2048 lds\n" \
M_"v[76:79], a[248:251], a[196:199], v[76:79]\n" \
"s_movk_i32 s84, 0x5700\n" \
M_"v[72:75], a[180:183], a[132:135], v[72:75]\n" \
B_"v23, s[64:67], s84 offen offset:3072 lds\n" \
M_"v[72:75], a[244:247], a[196:199], v[72:75]\n" \
"s_add_u32 m0, s31, 0x0\n" \
"s_mov_b32 s84, 0x300000\n" \
M_"v[68:71], a[176:179], a[132:135], v[68:71]\n" \
B_"v26, s[68:71], s84 offen offset:0 lds\n" \
M_"v[68:71], a[240:243], a[196:199], v[68:71]\n" \
R_"a[132:135], v28 offset:51200\n" \
M_"v[84:87], a[176:179], a[136:139], v[84:87]\n" \
R_"a[196:199], v29 offset:51200\n" \
M_"v[84:87], a[240:243], a[200:203], v[84:87]\n" \
"s_addk_i32 s84, 0x7c00\n" \
M_"v[88:91], a[180:183], a[136:139], v[88:91]\n" \
B_"v26, s[68:71], s84 offen offset:1024 lds\n" \
M_"v[88:91], a[244:247], a[200:203], v[88:91]\n" \
X_"a[226:227], v32 offset:41984\n" \
M_"v[92:95], a[184:187], a[136:139], v[92:95]\n" \
"s_addk_i32 s84, 0x7c00\n" \
M_"v[92:95], a[248:251], a[200:203], v[92:95]\n" \
B_"v27, s[68:71], s84 offen offset:2048 lds\n" \
M_"v[96:99], a[188:191], a[136:139], v[96:99]\n" \
X_"a[164:165], v33 offset:32768\n" \
M_"v[96:99], a[252:255], a[200:203], v[96:99]\n" \
R_"a[136:139], v28 offset:53248\n" \
M_"v[112:115], a[188:191], a[140:143], v[112:115]\n" \
R_"a[200:203], v29 offset:53248\n" \
M_"v[112:115], a[252:255], a[204:207], v[112:115]\n" \
"s_addk_i32 s84, 0x7c00\n" \
M_"v[108:111], a[184:187], a[140:143], v[108:111]\n" \
B_"v27, s[68:71], s84 offen offset:3072 lds\n" \
M_"v[108:111], a[248:251], a[204:207], v[108:111]\n" \
"s_add_u32 m0, s30, 0x1000\n" \
"s_mov_b32 s84, 0x8300\n" \
M_"v[104:107], a[180:183], a[140:143], v[104:107]\n" \
B_"v22, s[64:67], s84 offen offset:0 lds\n" \
M_"v[104:107], a[244:247], a[204:207], v[104:107]\n" \
X_"a[166:167], v33 offset:33792\n" \
M_"v[100:103], a[176:179], a[140:143], v[100:103]\n" \
X_"a[228:229], v33 offset:40960\n" \
M_"v[100:103], a[240:243], a[204:207], v[100:103]\n" \
R_"a[140:143], v28 offset:55296\n" \
M_"v[116:119], a[176:179], a[144:147], v[116:119]\n" \
R_"a[204:207], v29 offset:55296\n" \
M_"v[116:119], a[240:243], a[208:211], v[116:119]\n" \
"s_addk_i32 s84, 0x1c00\n" \
M_"v[120:123], a[180:183], a[144:147], v[120:123]\n" \
B_"v23, s[64:67], s84 offen offset:1024 lds\n" \
M_"v[120:123], a[244:247], a[208:211], v[120:123]\n" \
"s_addk_i32 s84, 0x1c00\n" \
M_"v[124:127], a[184:187], a[144:147], v[124:127]\n" \
B_"v22, s[64:67], s84 offen offset:2048 lds\n" \
M_"v[124:127], a[248:251], a[208:211], v[124:127]\n" \
"s_addk_i32 s84, 0x1c00\n" \
"s_nop 0\n" \
M_"v[128:131], a[188:191], a[144:147], v[128:131]\n" \
B_"v23, s[64:67], s84 offen offset:3072 lds\n" \
M_"v[128:131], a[252:255], a[208:211], v[128:131]\n" \
R_"a[144:147], v28 offset:32768\n" \
M_"v[144:147], a[188:191], a[148:151], v[144:147]\n" \
R_"a[208:211], v29 offset:32768\n" \
M_"v[144:147], a[252:255], a[212:215], v[144:147]\n" \
"s_add_u32 m0, s31, 0x4000\n" \
"s_mov_b32 s84, 0x300000\n" \
M_"v[140:143], a[184:187], a[148:151], v[140:143]\n" \
B_"v26, s[72:75], s84 offen offset:0 lds\n" \
M_"v[140:143], a[248:251], a[212:215], v[140:143]\n" \
X_"a[230:231], v33 offset:41984\n" \
M_"v[136:139], a[180:183], a[148:151], v[136:139]\n" \
"s_addk_i32 s84, 0x7c00\n" \
"s_nop 0\n" \
M_"v[136:139], a[244:247], a[212:215], v[136:139]\n" \
B_"v26, s[72:75], s84 offen offset:1024 lds\n" \
M_"v[132:135], a[176:179], a[148:151], v[132:135]\n" \
X_"a[168:169], v34 offset:32768\n" \
M_"v[132:135], a[240:243], a[212:215], v[132:135]\n" \
R_"a[148:151], v28 offset:34816\n" \
M_"v[148:151], a[176:179], a[152:155], v[148:151]\n" \
R_"a[212:215], v29 offset:34816\n" \
M_"v[148:151], a[240:243], a[216:219], v[148:151]\n" \
X_"a[170:171], v34 offset:33792\n" \
M_"v[152:155], a[180:183], a[152:155], v[152:155]\n" \
X_"a[232:233], v34 offset:40960\n" \
M_"v[152:155], a[244:247], a[216:219], v[152:155]\n" \
X_"a[234:235], v34 offset:41984\n" \
M_"v[156:159], a[184:187], a[152:155], v[156:159]\n" \
X_"a[172:173], v35 offset:32768\n" \
M_"v[156:159], a[248:251], a[216:219], v[156:159]\n" \
X_"a[174:175], v35 offset:33792\n" \
M_"v[160:163], a[188:191], a[152:155], v[160:163]\n" \
X_"a[236:237], v35 offset:40960\n" \
M_"v[160:163], a[252:255], a[216:219], v[160:163]\n" \
R_"a[152:155], v28 offset:36864\n" \
M_"v[176:179], a[188:191], a[156:159], v[176:179]\n" \
R_"a[216:219], v29 offset:36864\n" \
M_"v[176:179], a[252:255], a[220:223], v[176:179]\n" \
X_"a[238:239], v35 offset:41984\n" \
M_"v[172:175], a[184:187], a[156:159], v[172:175]\n" \
M_"v[172:175], a[248:251], a[220:223], v[172:175]\n" \
M_"v[168:171], a[180:183], a[156:159], v[168:171]\n" \
M_"v[168:171], a[244:247], a[220:223], v[168:171]\n" \
M_"v[164:167], a[176:179], a[156:159], v[164:167]\n" \
M_"v[164:167], a[240:243], a[220:223], v[164:167]\n" \
R_"a[156:159], v28 offset:38912\n" \
R_"a[220:223], v29 offset:38912\n" \
"s_waitcnt lgkmcnt(15)\n" \
M_"a[0:3], a[160:163], a[128:131], a[0:3]\n" \
"s_addk_i32 s84, 0x7c00\n" \
M_"a[0:3], a[224:227], a[192:195], a[0:3]\n" \
B_"v27, s[72:75], s84 offen offset:2048 lds\n" \
M_"a[4:7], a[164:167], a[128:131], a[4:7]\n" \
X_"a[176:177], v36 offset:32768\n" \
"s_waitcnt lgkmcnt(15)\n" \
M_"a[4:7], a[228:231], a[192:195], a[4:7]\n" \
"s_addk_i32 s84, 0x7c00\n" \
"s_waitcnt lgkmcnt(11)\n" \
M_"a[8:11], a[168:171], a[128:131], a[8:11]\n" \
B_"v27, s[72:75], s84 offen offset:3072 lds\n" \
"s_waitcnt lgkmcnt(9)\n" \
M_"a[8:11], a[232:235], a[192:195], a[8:11]\n" \
X_"a[178:179], v36 offset:33792\n" \
"s_waitcnt lgkmcnt(8)\n" \
M_"a[12:15], a[172:175], a[128:131], a[12:15]\n" \
X_"a[240:241], v36 offset:40960\n" \
"s_waitcnt lgkmcnt(5)\n" \
M_"a[12:15], a[236:239], a[192:195], a[12:15]\n" \
M_"a[28:31], a[172:175], a[132:135], a[28:31]\n" \
M_"a[28:31], a[236:239], a[196:199], a[28:31]\n" \
X_"a[242:243], v36 offset:41984\n" \
M_"a[24:27], a[168:171], a[132:135], a[24:27]\n" \
M_"a[24:27], a[232:235], a[196:199], a[24:27]\n" \
M_"a[20:23], a[164:167], a[132:135], a[20:23]\n" \
M_"a[20:23], a[228:231], a[196:199], a[20:23]\n" \
X_"a[180:181], v37 offset:32768\n" \
M_"a[16:19], a[160:163], a[132:135], a[16:19]\n" \
M_"a[16:19], a[224:227], a[196:199], a[16:19]\n" \
M_"a[32:35], a[160:163], a[136:139], a[32:35]\n" \
X_"a[182:183], v37 offset:33792\n" \
M_"a[32:35], a[224:227], a[200:203], a[32:35]\n" \
M_"a[36:39], a[164:167], a[136:139], a[36:39]\n" \
"v_lshlrev_b32_e32 v45, 3, v20\n" \
"v_add_u32_e32 v45, s31, v45\n" \
M_"a[36:39], a[228:231], a[200:203], a[36:39]\n" \
"s_getpc_b64 s[86:87]\n" \
"s_sub_u32 s86, s86, 0xf800\n" \
"s_subb_u32 s87, s87, 0\n" \
"s_mov_b32 m0, s32\n" \
"s_nop 0\n" \
M_"a[40:43], a[168:171], a[136:139], a[40:43]\n" \
"global_load_lds_dword v45, s[86:87]\n" \
M_"a[40:43], a[232:235], a[200:203], a[40:43]\n" \
X_"a[244:245], v37 offset:40960\n" \
M_"a[44:47], a[172:175], a[136:139], a[44:47]\n" \
X_"a[246:247], v37 offset:41984\n" \
M_"a[44:47], a[236:239], a[200:203], a[44:47]\n" \
M_"a[60:63], a[172:175], a[140:143], a[60:63]\n" \
M_"a[60:63], a[236:239], a[204:207], a[60:63]\n" \
M_"a[56:59], a[168:171], a[140:143], a[56:59]\n" \
X_"a[184:185], v38 offset:32768\n" \
M_"a[56:59], a[232:235], a[204:207], a[56:59]\n" \
M_"a[52:55], a[164:167], a[140:143], a[52:55]\n" \
M_"a[52:55], a[228:231], a[204:207], a[52:55]\n" \
X_"a[186:187], v38 offset:33792\n" \
M_"a[48:51], a[160:163], a[140:143], a[48:51]\n" \
M_"a[48:51], a[224:227], a[204:207], a[48:51]\n" \
M_"a[64:67], a[160:163], a[144:147], a[64:67]\n" \
X_"a[248:249], v38 offset:40960\n" \
M_"a[64:67], a[224:227], a[208:211], a[64:67]\n" \
M_"a[68:71], a[164:167], a[144:147], a[68:71]\n" \
M_"a[68:71], a[228:231], a[208:211], a[68:71]\n" \
X_"a[250:251], v38 offset:41984\n" \
M_"a[72:75], a[168:171], a[144:147], a[72:75]\n" \
M_"a[72:75], a[232:235], a[208:211], a[72:75]\n" \
M_"a[76:79], a[172:175], a[144:147], a[76:79]\n" \
M_"a[76:79], a[236:239], a[208:211], a[76:79]\n" \
X_"a[188:189], v39 offset:32768\n" \
M_"a[92:95], a[172:175], a[148:151], a[92:95]\n" \
M_"a[92:95], a[236:239], a[212:215], a[92:95]\n" \
M_"a[88:91], a[168:171], a[148:151], a[88:91]\n" \
X_"a[190:191], v39 offset:33792\n" \
M_"a[88:91], a[232:235], a[212:215], a[88:91]\n" \
M_"a[84:87], a[164:167], a[148:151], a[84:87]\n" \
M_"a[84:87], a[228:231], a[212:215], a[84:87]\n" \
X_"a[252:253], v39 offset:40960\n" \
M_"a[80:83], a[160:163], a[148:151], a[80:83]\n" \
M_"a[80:83], a[224:227], a[212:215], a[80:83]\n" \
M_"a[96:99], a[160:163], a[152:155], a[96:99]\n" \
X_"a[254:255], v39 offset:41984\n" \
M_"a[96:99], a[224:227], a[216:219], a[96:99]\n" \
M_"a[100:103], a[164:167], a[152:155], a[100:103]\n" \
M_"a[100:103], a[228:231], a[216:219], a[100:103]\n" \
M_"a[104:107], a[168:171], a[152:155], a[104:107]\n" \
M_"a[104:107], a[232:235], a[216:219], a[104:107]\n" \
M_"a[108:111], a[172:175], a[152:155], a[108:111]\n" \
M_"a[108:111], a[236:239], a[216:219], a[108:111]\n" \
"s_waitcnt lgkmcnt(15)\n" \
M_"a[124:127], a[172:175], a[156:159], a[124:127]\n" \
M_"a[124:127], a[236:239], a[220:223], a[124:127]\n" \
M_"a[120:123], a[168:171], a[156:159], a[120:123]\n" \
M_"a[120:123], a[232:235], a[220:223], a[120:123]\n" \
M_"a[116:119], a[164:167], a[156:159], a[116:119]\n" \
M_"a[116:119], a[228:231], a[220:223], a[116:119]\n" \
M_"a[112:115], a[160:163], a[156:159], a[112:115]\n" \
"s_waitcnt vmcnt(1) lgkmcnt(0)\n" \
M_"a[112:115], a[224:227], a[220:223], a[112:115]\n" \
"s_barrier\n" \
"v_mov_b32_e32 v45, s62\n" \
"s_mov_b64 exec, 1\n" \
"buffer_store_dword v45, off, s[80:83], s37 offset:128 sc1\n" \
"s_mov_b64 exec, -1\n" \
M_"v[52:55], a[176:179], a[128:131], v[52:55]\n" \
"s_add_u32 m0, s30, 0x8000\n" \
"s_movk_i32 s84, 0x380\n" \
M_"v[52:55], a[240:243], a[192:195], v[52:55]\n" \
B_"v22, s[64:67], s84 offen offset:0 lds\n" \
M_"v[56:59], a[180:183], a[128:131], v[56:59]\n" \
X_"a[160:161], v32 offset:0\n" \
M_"v[56:59], a[244:247], a[192:195], v[56:59]\n" \
"s_movk_i32 s84, 0x1f80\n" \
M_"v[60:63], a[184:187], a[128:131], v[60:63]\n" \
B_"v23, s[64:67], s84 offen offset:1024 lds\n" \
M_"v[60:63], a[248:251], a[192:195], v[60:63]\n" \
X_"a[162:163], v32 offset:1024\n" \
M_"v[64:67], a[188:191], a[128:131], v[64:67]\n" \
X_"a[224:225], v32 offset:8192\n" \
M_"v[64:67], a[252:255], a[192:195], v[64:67]\n" \
R_"a[128:131], v28 offset:16384\n" \
M_"v[80:83], a[188:191], a[132:135], v[80:83]\n" \
R_"a[192:195], v29 offset:16384\n" \
M_"v[80:83], a[252:255], a[196:199], v[80:83]\n" \
"s_movk_i32 s84, 0x3b80\n" \
M_"v[76:79], a[184:187], a[132:135], v[76:79]\n" \
B_"v22, s[64:67], s84 offen offset:2048 lds\n" \
M_"v[76:79], a[248:251], a[196:199], v[76:79]\n" \
"s_movk_i32 s84, 0x5780\n" \
M_"v[72:75], a[180:183], a[132:135], v[72:75]\n" \
B_"v23, s[64:67], s84 offen offset:3072 lds\n" \
M_"v[72:75], a[244:247], a[196:199], v[72:75]\n" \
"s_add_u32 m0, s31, 0x8000\n" \
"s_mov_b32 s84, 0x380000\n" \
M_"v[68:71], a[176:179], a[132:135], v[68:71]\n" \
B_"v26, s[68:71], s84 offen offset:0 lds\n" \
M_"v[68:71], a[240:243], a[196:199], v[68:71]\n" \
R_"a[132:135], v28 offset:18432\n" \
M_"v[84:87], a[176:179], a[136:139], v[84:87]\n" \
R_"a[196:199], v29 offset:18432\n" \
M_"v[84:87], a[240:243], a[200:203], v[84:87]\n" \
"s_addk_i32 s84, 0x7c00\n" \
M_"v[88:91], a[180:183], a[136:139], v[88:91]\n" \
B_"v26, s[68:71], s84 offen offset:1024 lds\n" \
M_"v[88:91], a[244:247], a[200:203], v[88:91]\n" \
X_"a[226:227], v32 offset:9216\n" \
M_"v[92:95], a[184:187], a[136:139], v[92:95]\n" \
"s_addk_i32 s84, 0x7c00\n" \
M_"v[92:95], a[248:251], a[200:203], v[92:95]\n" \
B_"v27, s[68:71], s84 offen offset:2048 lds\n" \
M_"v[96:99], a[188:191], a[136:139], v[96:99]\n" \
X_"a[164:165], v33 offset:0\n" \
M_"v[96:99], a[252:255], a[200:203], v[96:99]\n" \
R_"a[136:139], v28 offset:20480\n" \
M_"v[112:115], a[188:191], a[140:143], v[112:115]\n" \
R_"a[200:203], v29 offset:20480\n" \
M_"v[112:115], a[252:255], a[204:207], v[112:115]\n" \
"s_addk_i32 s84, 0x7c00\n" \
"s_nop 0\n" \
M_"v[108:111], a[184:187], a[140:143], v[108:111]\n" \
B_"v27, s[68:71], s84 offen offset:3072 lds\n" \
M_"v[108:111], a[248:251], a[204:207], v[108:111]\n" \
"s_add_u32 m0, s30, 0x9000\n" \
"s_mov_b32 s84, 0x8380\n" \
M_"v[104:107], a[180:183], a[140:143], v[104:107]\n" \
B_"v22, s[64:67], s84 offen offset:0 lds\n" \
M_"v[104:107], a[244:247], a[204:207], v[104:107]\n" \
X_"a[166:167], v33 offset:1024\n" \
M_"v[100:103], a[176:179], a[140:143], v[100:103]\n" \
X_"a[228:229], v33 offset:8192\n" \
M_"v[100:103], a[240:243], a[204:207], v[100:103]\n" \
R_"a[140:143], v28 offset:22528\n" \
M_"v[116:119], a[176:179], a[144:147], v[116:119]\n" \
R_"a[204:207], v29 offset:22528\n" \
M_"v[116:119], a[240:243], a[208:211], v[116:119]\n" \
"s_addk_i32 s84, 0x1c00\n" \
M_"v[120:123], a[180:183], a[144:147], v[120:123]\n" \
B_"v23, s[64:67], s84 offen offset:1024 lds\n" \
M_"v[120:123], a[244:247], a[208:211], v[120:123]\n" \
"s_addk_i32 s84, 0x1c00\n" \
M_"v[124:127], a[184:187], a[144:147], v[124:127]\n" \
B_"v22, s[64:67], s84 offen offset:2048 lds\n" \
M_"v[124:127], a[248:251], a[208:211], v[124:127]\n" \
"s_addk_i32 s84, 0x1c00\n" \
"s_nop 0\n" \
M_"v[128:131], a[188:191], a[144:147], v[128:131]\n" \
B_"v23, s[64:67], s84 offen offset:3072 lds\n" \
M_"v[128:131], a[252:255], a[208:211], v[128:131]\n" \
R_"a[144:147], v28 offset:0\n" \
M_"v[144:147], a[188:191], a[148:151], v[144:147]\n" \
R_"a[208:211], v29 offset:0\n" \
M_"v[144:147], a[252:255], a[212:215], v[144:147]\n" \
"s_add_u32 m0, s31, 0xc000\n" \
"s_mov_b32 s84, 0x380000\n" \
M_"v[140:143], a[184:187], a[148:151], v[140:143]\n" \
B_"v26, s[72:75], s84 offen offset:0 lds\n" \
M_"v[140:143], a[248:251], a[212:215], v[140:143]\n" \
X_"a[230:231], v33 offset:9216\n" \
M_"v[136:139], a[180:183], a[148:151], v[136:139]\n" \
"s_addk_i32 s84, 0x7c00\n" \
"s_nop 0\n" \
M_"v[136:139], a[244:247], a[212:215], v[136:139]\n" \
B_"v26, s[72:75], s84 offen offset:1024 lds\n" \
M_"v[132:135], a[176:179], a[148:151], v[132:135]\n" \
X_"a[168:169], v34 offset:0\n" \
M_"v[132:135], a[240:243], a[212:215], v[132:135]\n" \
R_"a[148:151], v28 offset:2048\n" \
M_"v[148:151], a[176:179], a[152:155], v[148:151]\n" \
R_"a[212:215], v29 offset:2048\n" \
M_"v[148:151], a[240:243], a[216:219], v[148:151]\n" \
X_"a[170:171], v34 offset:1024\n" \
M_"v[152:155], a[180:183], a[152:155], v[152:155]\n" \
X_"a[232:233], v34 offset:8192\n" \
M_"v[152:155], a[244:247], a[216:219], v[152:155]\n" \
X_"a[234:235], v34 offset:9216\n" \
M_"v[156:159], a[184:187], a[152:155], v[156:159]\n" \
X_"a[172:173], v35 offset:0\n" \
M_"v[156:159], a[248:251], a[216:219], v[156:159]\n" \
X_"a[174:175], v35 offset:1024\n" \
M_"v[160:163], a[188:191], a[152:155], v[160:163]\n" \
X_"a[236:237], v35 offset:8192\n" \
M_"v[160:163], a[252:255], a[216:219], v[160:163]\n" \
R_"a[152:155], v28 offset:4096\n" \
M_"v[176:179], a[188:191], a[156:159], v[176:179]\n" \
R_"a[216:219], v29 offset:4096\n" \
M_"v[176:179], a[252:255], a[220:223], v[176:179]\n" \
X_"a[238:239], v35 offset:9216\n" \
M_"v[172:175], a[184:187], a[156:159], v[172:175]\n" \
M_"v[172:175], a[248:251], a[220:223], v[172:175]\n" \
M_"v[168:171], a[180:183], a[156:159], v[168:171]\n" \
M_"v[168:171], a[244:247], a[220:223], v[168:171]\n" \
M_"v[164:167], a[176:179], a[156:159], v[164:167]\n" \
M_"v[164:167], a[240:243], a[220:223], v[164:167]\n" \
R_"a[156:159], v28 offset:6144\n" \
R_"a[220:223], v29 offset:6144\n" \
"s_waitcnt lgkmcnt(15)\n" \
M_"a[0:3], a[160:163], a[128:131], a[0:3]\n" \
"s_addk_i32 s84, 0x7c00\n" \
M_"a[0:3], a[224:227], a[192:195], a[0:3]\n" \
B_"v27, s[72:75], s84 offen offset:2048 lds\n" \
M_"a[4:7], a[164:167], a[128:131], a[4:7]\n" \
X_"a[176:177], v36 offset:0\n" \
"s_waitcnt lgkmcnt(15)\n" \
M_"a[4:7], a[228:231], a[192:195], a[4:7]\n" \
"s_addk_i32 s84, 0x7c00\n" \
"s_waitcnt lgkmcnt(11)\n" \
M_"a[8:11], a[168:171], a[128:131], a[8:11]\n" \
B_"v27, s[72:75], s84 offen offset:3072 lds\n" \
"s_waitcnt lgkmcnt(9)\n" \
M_"a[8:11], a[232:235], a[192:195], a[8:11]\n" \
X_"a[178:179], v36 offset:1024\n" \
"s_waitcnt lgkmcnt(8)\n" \
M_"a[12:15], a[172:175], a[128:131], a[12:15]\n" \
X_"a[240:241], v36 offset:8192\n" \
"s_waitcnt lgkmcnt(5)\n" \
M_"a[12:15], a[236:239], a[192:195], a[12:15]\n" \
M_"a[28:31], a[172:175], a[132:135], a[28:31]\n" \
M_"a[28:31], a[236:239], a[196:199], a[28:31]\n" \
X_"a[242:243], v36 offset:9216\n" \
M_"a[24:27], a[168:171], a[132:135], a[24:27]\n" \
M_"a[24:27], a[232:235], a[196:199], a[24:27]\n" \
M_"a[20:23], a[164:167], a[132:135], a[20:23]\n" \
M_"a[20:23], a[228:231], a[196:199], a[20:23]\n" \
X_"a[180:181], v37 offset:0\n" \
M_"a[16:19], a[160:163], a[132:135], a[16:19]\n" \
M_"a[16:19], a[224:227], a[196:199], a[16:19]\n" \
M_"a[32:35], a[160:163], a[136:139], a[32:35]\n" \
"v_bfe_u32 v44, v20, 2, 5\n" \
X_"a[182:183], v37 offset:1024\n" \
M_"a[32:35], a[224:227], a[200:203], a[32:35]\n" \
"buffer_load_dword v44, v44, s[80:83], s38 offen offset:128 sc1\n" \
M_"a[36:39], a[164:167], a[136:139], a[36:39]\n" \
M_"a[36:39], a[228:231], a[200:203], a[36:39]\n" \
X_"a[244:245], v37 offset:8192\n" \
M_"a[40:43], a[168:171], a[136:139], a[40:43]\n" \
M_"a[40:43], a[232:235], a[200:203], a[40:43]\n" \
M_"a[44:47], a[172:175], a[136:139], a[44:47]\n" \
X_"a[246:247], v37 offset:9216\n" \
M_"a[44:47], a[236:239], a[200:203], a[44:47]\n" \
M_"a[60:63], a[172:175], a[140:143], a[60:63]\n" \
"global_store_dwordx4 v49, v[0:3], s[28:29] offset:0 sc1\n" \
M_"a[60:63], a[236:239], a[204:207], a[60:63]\n" \
M_"a[56:59], a[168:171], a[140:143], a[56:59]\n" \
X_"a[184:185], v38 offset:0\n" \
M_"a[56:59], a[232:235], a[204:207], a[56:59]\n" \
M_"a[52:55], a[164:167], a[140:143], a[52:55]\n" \
M_"a[52:55], a[228:231], a[204:207], a[52:55]\n" \
X_"a[186:187], v38 offset:1024\n" \
M_"a[48:51], a[160:163], a[140:143], a[48:51]\n" \
M_"a[48:51], a[224:227], a[204:207], a[48:51]\n" \
M_"a[64:67], a[160:163], a[144:147], a[64:67]\n" \
"global_store_dwordx4 v49, v[0:3], s[28:29] offset:1024 sc1\n" \
M_"a[64:67], a[224:227], a[208:211], a[64:67]\n" \
X_"a[248:249], v38 offset:8192\n" \
M_"a[68:71], a[164:167], a[144:147], a[68:71]\n" \
M_"a[68:71], a[228:231], a[208:211], a[68:71]\n" \
X_"a[250:251], v38 offset:9216\n" \
M_"a[72:75], a[168:171], a[144:147], a[72:75]\n" \
M_"a[72:75], a[232:235], a[208:211], a[72:75]\n" \
M_"a[76:79], a[172:175], a[144:147], a[76:79]\n" \
M_"a[76:79], a[236:239], a[208:211], a[76:79]\n" \
X_"a[188:189], v39 offset:0\n" \
M_"a[92:95], a[172:175], a[148:151], a[92:95]\n" \
"s_mov_b64 exec, 0xffff\n" \
"global_store_dwordx4 v49, v[0:3], s[28:29] offset:2048 sc1\n" \
"s_mov_b64 exec, -1\n" \
"s_nop 0\n" \
M_"a[92:95], a[236:239], a[212:215], a[92:95]\n" \
M_"a[88:91], a[168:171], a[148:151], a[88:91]\n" \
X_"a[190:191], v39 offset:1024\n" \
M_"a[88:91], a[232:235], a[212:215], a[88:91]\n" \
M_"a[84:87], a[164:167], a[148:151], a[84:87]\n" \
M_"a[84:87], a[228:231], a[212:215], a[84:87]\n" \
X_"a[252:253], v39 offset:8192\n" \
M_"a[80:83], a[160:163], a[148:151], a[80:83]\n" \
M_"a[80:83], a[224:227], a[212:215], a[80:83]\n" \
M_"a[96:99], a[160:163], a[152:155], a[96:99]\n" \
X_"a[254:255], v39 offset:9216\n" \
M_"a[96:99], a[224:227], a[216:219], a[96:99]\n" \
M_"a[100:103], a[164:167], a[152:155], a[100:103]\n" \
M_"a[100:103], a[228:231], a[216:219], a[100:103]\n" \
M_"a[104:107], a[168:171], a[152:155], a[104:107]\n" \
M_"a[104:107], a[232:235], a[216:219], a[104:107]\n" \
M_"a[108:111], a[172:175], a[152:155], a[108:111]\n" \
M_"a[108:111], a[236:239], a[216:219], a[108:111]\n" \
"s_waitcnt lgkmcnt(15)\n" \
M_"a[124:127], a[172:175], a[156:159], a[124:127]\n" \
M_"a[124:127], a[236:239], a[220:223], a[124:127]\n" \
M_"a[120:123], a[168:171], a[156:159], a[120:123]\n" \
M_"a[120:123], a[232:235], a[220:223], a[120:123]\n" \
M_"a[116:119], a[164:167], a[156:159], a[116:119]\n" \
M_"a[116:119], a[228:231], a[220:223], a[116:119]\n" \
M_"a[112:115], a[160:163], a[156:159], a[112:115]\n" \
"s_waitcnt vmcnt(3) lgkmcnt(0)\n" \
M_"a[112:115], a[224:227], a[220:223], a[112:115]\n" \
"s_barrier\n" \
"v_cmp_ne_u32_e32 vcc, s62, v44\n" \
"s_nop 4\n" \
"s_cbranch_vccnz L_poll60_%=\n" \
"L_pollr60_%=:\n" \
"s_mov_b32 s68, s12\n" \
"s_and_b32 s69, s13, 0xffff\n" \
"s_mov_b32 s70, 0x480000\n" \
"s_mov_b32 s71, 0x20000\n" \
M_"v[52:55], a[176:179], a[128:131], v[52:55]\n" \
"s_add_u32 m0, s40, 0x0\n" \
"s_add_u32 s85, s39, 0x0\n" \
"s_nop 0\n" \
M_"v[52:55], a[240:243], a[192:195], v[52:55]\n" \
B_"v20, s[80:83], s85 offen sc1 lds\n" \
M_"v[56:59], a[180:183], a[128:131], v[56:59]\n" \
X_"a[160:161], v32 offset:32768\n" \
M_"v[56:59], a[244:247], a[192:195], v[56:59]\n" \
"s_add_u32 m0, s40, 0x1000\n" \
"s_add_u32 s85, s39, 0x1000\n" \
".fill (L_pb + 16096 - .) / 4, 4, 0xBF800000\n" \
"L_pp4:\n" \
"s_cbranch_execz L_pret\n" \
"s_nop 0\n" \
M_"v[60:63], a[184:187], a[128:131], v[60:63]\n" \
B_"v20, s[80:83], s85 offen sc1 lds\n" \
M_"v[60:63], a[248:251], a[192:195], v[60:63]\n" \
X_"a[162:163], v32 offset:33792\n" \
M_"v[64:67], a[188:191], a[128:131], v[64:67]\n" \
X_"a[224:225], v32 offset:40960\n" \
M_"v[64:67], a[252:255], a[192:195], v[64:67]\n" \
R_"a[128:131], v28 offset:49152\n" \
M_"v[80:83], a[188:191], a[132:135], v[80:83]\n" \
R_"a[192:195], v29 offset:49152\n" \
M_"v[80:83], a[252:255], a[196:199], v[80:83]\n" \
"s_add_u32 m0, s40, 0x3000\n" \
"s_movk_i32 s84, 0x0\n" \
M_"v[76:79], a[184:187], a[132:135], v[76:79]\n" \
B_"v21, s[76:79], s84 offen offset:0 lds\n" \
M_"v[76:79], a[248:251], a[196:199], v[76:79]\n" \
"s_add_u32 m0, s30, 0x0\n" \
"s_movk_i32 s84, 0x0\n" \
"s_nop 0\n" \
M_"v[72:75], a[180:183], a[132:135], v[72:75]\n" \
B_"v24, s[76:79], s84 offen offset:0 lds\n" \
M_"v[72:75], a[244:247], a[196:199], v[72:75]\n" \
X_"a[226:227], v32 offset:41984\n" \
M_"v[68:71], a[176:179], a[132:135], v[68:71]\n" \
X_"a[164:165], v33 offset:32768\n" \
M_"v[68:71], a[240:243], a[196:199], v[68:71]\n" \
R_"a[132:135], v28 offset:51200\n" \
M_"v[84:87], a[176:179], a[136:139], v[84:87]\n" \
R_"a[196:199], v29 offset:51200\n" \
M_"v[84:87], a[240:243], a[200:203], v[84:87]\n" \
"s_movk_i32 s84, 0x2000\n" \
M_"v[88:91], a[180:183], a[136:139], v[88:91]\n" \
B_"v25, s[76:79], s84 offen offset:1024 lds\n" \
M_"v[88:91], a[244:247], a[200:203], v[88:91]\n" \
"s_movk_i32 s84, 0x4000\n" \
M_"v[92:95], a[184:187], a[136:139], v[92:95]\n" \
B_"v24, s[76:79], s84 offen offset:2048 lds\n" \
M_"v[92:95], a[248:251], a[200:203], v[92:95]\n" \
X_"a[166:167], v33 offset:33792\n" \
M_"v[96:99], a[188:191], a[136:139], v[96:99]\n" \
X_"a[228:229], v33 offset:40960\n" \
M_"v[96:99], a[252:255], a[200:203], v[96:99]\n" \
R_"a[136:139], v28 offset:53248\n" \
M_"v[112:115], a[188:191], a[140:143], v[112:115]\n" \
R_"a[200:203], v29 offset:53248\n" \
M_"v[112:115], a[252:255], a[204:207], v[112:115]\n" \
"s_movk_i32 s84, 0x6000\n" \
M_"v[108:111], a[184:187], a[140:143], v[108:111]\n" \
B_"v25, s[76:79], s84 offen offset:3072 lds\n" \
M_"v[108:111], a[248:251], a[204:207], v[108:111]\n" \
"s_add_u32 m0, s31, 0x0\n" \
"s_movk_i32 s84, 0x0\n" \
M_"v[104:107], a[180:183], a[140:143], v[104:107]\n" \
B_"v26, s[68:71], s84 offen offset:0 lds\n" \
M_"v[104:107], a[244:247], a[204:207], v[104:107]\n" \
"s_movk_i32 s84, 0x7c00\n" \
M_"v[100:103], a[176:179], a[140:143], v[100:103]\n" \
B_"v26, s[68:71], s84 offen offset:1024 lds\n" \
M_"v[100:103], a[240:243], a[204:207], v[100:103]\n" \
R_"a[140:143], v28 offset:55296\n" \
M_"v[116:119], a[176:179], a[144:147], v[116:119]\n" \
R_"a[204:207], v29 offset:55296\n" \
M_"v[116:119], a[240:243], a[208:211], v[116:119]\n" \
"s_addk_i32 s84, 0x7c00\n" \
M_"v[120:123], a[180:183], a[144:147], v[120:123]\n" \
B_"v27, s[68:71], s84 offen offset:2048 lds\n" \
M_"v[120:123], a[244:247], a[208:211], v[120:123]\n" \
X_"a[230:231], v33 offset:41984\n" \
M_"v[124:127], a[184:187], a[144:147], v[124:127]\n" \
"s_addk_i32 s84, 0x7c00\n" \
M_"v[124:127], a[248:251], a[208:211], v[124:127]\n" \
B_"v27, s[68:71], s84 offen offset:3072 lds\n" \
M_"v[128:131], a[188:191], a[144:147], v[128:131]\n" \
X_"a[168:169], v34 offset:32768\n" \
M_"v[128:131], a[252:255], a[208:211], v[128:131]\n" \
R_"a[144:147], v28 offset:32768\n" \
M_"v[144:147], a[188:191], a[148:151], v[144:147]\n" \
R_"a[208:211], v29 offset:32768\n" \
M_"v[144:147], a[252:255], a[212:215], v[144:147]\n" \
"s_add_u32 m0, s30, 0x1000\n" \
"s_mov_b32 s84, 0x9000\n" \
M_"v[140:143], a[184:187], a[148:151], v[140:143]\n" \
B_"v24, s[76:79], s84 offen offset:0 lds\n" \
M_"v[140:143], a[248:251], a[212:215], v[140:143]\n" \
X_"a[170:171], v34 offset:33792\n" \
M_"v[136:139], a[180:183], a[148:151], v[136:139]\n" \
"s_addk_i32 s84, 0x2000\n" \
"s_nop 0\n" \
M_"v[136:139], a[244:247], a[212:215], v[136:139]\n" \
B_"v25, s[76:79], s84 offen offset:1024 lds\n" \
M_"v[132:135], a[176:179], a[148:151], v[132:135]\n" \
X_"a[232:233], v34 offset:40960\n" \
M_"v[132:135], a[240:243], a[212:215], v[132:135]\n" \
R_"a[148:151], v28 offset:34816\n" \
M_"v[148:151], a[176:179], a[152:155], v[148:151]\n" \
R_"a[212:215], v29 offset:34816\n" \
M_"v[148:151], a[240:243], a[216:219], v[148:151]\n" \
X_"a[234:235], v34 offset:41984\n" \
M_"v[152:155], a[180:183], a[152:155], v[152:155]\n" \
X_"a[172:173], v35 offset:32768\n" \
M_"v[152:155], a[244:247], a[216:219], v[152:155]\n" \
X_"a[174:175], v35 offset:33792\n" \
M_"v[156:159], a[184:187], a[152:155], v[156:159]\n" \
X_"a[236:237], v35 offset:40960\n" \
M_"v[156:159], a[248:251], a[216:219], v[156:159]\n" \
X_"a[238:239], v35 offset:41984\n" \
M_"v[160:163], a[188:191], a[152:155], v[160:163]\n" \
M_"v[160:163], a[252:255], a[216:219], v[160:163]\n" \
R_"a[152:155], v28 offset:36864\n" \
M_"v[176:179], a[188:191], a[156:159], v[176:179]\n" \
R_"a[216:219], v29 offset:36864\n" \
M_"v[176:179], a[252:255], a[220:223], v[176:179]\n" \
M_"v[172:175], a[184:187], a[156:159], v[172:175]\n" \
M_"v[172:175], a[248:251], a[220:223], v[172:175]\n" \
M_"v[168:171], a[180:183], a[156:159], v[168:171]\n" \
M_"v[168:171], a[244:247], a[220:223], v[168:171]\n" \
M_"v[164:167], a[176:179], a[156:159], v[164:167]\n" \
M_"v[164:167], a[240:243], a[220:223], v[164:167]\n" \
R_"a[156:159], v28 offset:38912\n" \
R_"a[220:223], v29 offset:38912\n" \
"s_waitcnt lgkmcnt(15)\n" \
M_"a[0:3], a[160:163], a[128:131], a[0:3]\n" \
"s_addk_i32 s84, 0x2000\n" \
M_"a[0:3], a[224:227], a[192:195], a[0:3]\n" \
B_"v24, s[76:79], s84 offen offset:2048 lds\n" \
M_"a[4:7], a[164:167], a[128:131], a[4:7]\n" \
X_"a[176:177], v36 offset:32768\n" \
M_"a[4:7], a[228:231], a[192:195], a[4:7]\n" \
"s_addk_i32 s84, 0x2000\n" \
"s_waitcnt lgkmcnt(13)\n" \
M_"a[8:11], a[168:171], a[128:131], a[8:11]\n" \
B_"v25, s[76:79], s84 offen offset:3072 lds\n" \
"s_waitcnt lgkmcnt(9)\n" \
M_"a[8:11], a[232:235], a[192:195], a[8:11]\n" \
X_"a[178:179], v36 offset:33792\n" \
"s_waitcnt lgkmcnt(8)\n" \
M_"a[12:15], a[172:175], a[128:131], a[12:15]\n" \
X_"a[240:241], v36 offset:40960\n" \
"v_accvgpr_read_b32 v184, a0\n" \
"s_waitcnt lgkmcnt(7)\n" \
M_"a[12:15], a[236:239], a[192:195], a[12:15]\n" \
"v_accvgpr_read_b32 v185, a1\n" \
"v_accvgpr_read_b32 v186, a2\n" \
M_"a[28:31], a[172:175], a[132:135], a[28:31]\n" \
"v_accvgpr_read_b32 v187, a3\n" \
"v_accvgpr_read_b32 v180, a4\n" \
M_"a[28:31], a[236:239], a[196:199], a[28:31]\n" \
"s_add_u32 m0, s30, 0x20000\n" \
"s_movk_i32 s84, 0x100\n" \
M_"a[24:27], a[168:171], a[132:135], a[24:27]\n" \
B_"v24, s[76:79], s84 offen offset:0 lds\n" \
M_"a[24:27], a[232:235], a[196:199], a[24:27]\n" \
X_"a[242:243], v36 offset:41984\n" \
"v_accvgpr_read_b32 v181, a5\n" \
M_"a[20:23], a[164:167], a[132:135], a[20:23]\n" \
"v_accvgpr_read_b32 v182, a6\n" \
"v_accvgpr_read_b32 v183, a7\n" \
M_"a[20:23], a[228:231], a[196:199], a[20:23]\n" \
X_"a[180:181], v37 offset:32768\n" \
C_"v184, v184, v185\n" \
M_"a[16:19], a[160:163], a[132:135], a[16:19]\n" \
"s_movk_i32 s84, 0x2100\n" \
"s_nop 0\n" \
M_"a[16:19], a[224:227], a[196:199], a[16:19]\n" \
B_"v25, s[76:79], s84 offen offset:1024 lds\n" \
M_"a[32:35], a[160:163], a[136:139], a[32:35]\n" \
X_"a[182:183], v37 offset:33792\n" \
C_"v185, v186, v187\n" \
M_"a[32:35], a[224:227], a[200:203], a[32:35]\n" \
C_"v186, v180, v181\n" \
C_"v187, v182, v183\n" \
M_"a[36:39], a[164:167], a[136:139], a[36:39]\n" \
"v_accvgpr_read_b32 v188, a8\n" \
"v_accvgpr_read_b32 v189, a9\n" \
M_"a[36:39], a[228:231], a[200:203], a[36:39]\n" \
"s_movk_i32 s84, 0x4100\n" \
"s_nop 0\n" \
M_"a[40:43], a[168:171], a[136:139], a[40:43]\n" \
B_"v24, s[76:79], s84 offen offset:2048 lds\n" \
M_"a[40:43], a[232:235], a[200:203], a[40:43]\n" \
X_"a[244:245], v37 offset:40960\n" \
"v_accvgpr_read_b32 v190, a10\n" \
M_"a[44:47], a[172:175], a[136:139], a[44:47]\n" \
X_"a[246:247], v37 offset:41984\n" \
"v_accvgpr_read_b32 v191, a11\n" \
M_"a[44:47], a[236:239], a[200:203], a[44:47]\n" \
"v_accvgpr_read_b32 v180, a12\n" \
"v_accvgpr_read_b32 v181, a13\n" \
M_"a[60:63], a[172:175], a[140:143], a[60:63]\n" \
"s_movk_i32 s84, 0x6100\n" \
M_"a[60:63], a[236:239], a[204:207], a[60:63]\n" \
B_"v25, s[76:79], s84 offen offset:3072 lds\n" \
M_"a[56:59], a[168:171], a[140:143], a[56:59]\n" \
X_"a[184:185], v38 offset:32768\n" \
"v_accvgpr_read_b32 v182, a14\n" \
M_"a[56:59], a[232:235], a[204:207], a[56:59]\n" \
"v_accvgpr_read_b32 v183, a15\n" \
C_"v188, v188, v189\n" \
M_"a[52:55], a[164:167], a[140:143], a[52:55]\n" \
"s_add_u32 m0, s30, 0x21000\n" \
"s_addk_i32 s84, 0x3000\n" \
M_"a[52:55], a[228:231], a[204:207], a[52:55]\n" \
B_"v24, s[76:79], s84 offen offset:0 lds\n" \
M_"a[48:51], a[160:163], a[140:143], a[48:51]\n" \
X_"a[186:187], v38 offset:33792\n" \
C_"v189, v190, v191\n" \
M_"a[48:51], a[224:227], a[204:207], a[48:51]\n" \
C_"v190, v180, v181\n" \
C_"v191, v182, v183\n" \
M_"a[64:67], a[160:163], a[144:147], a[64:67]\n" \
X_"a[248:249], v38 offset:40960\n" \
"v_accvgpr_read_b32 v192, a24\n" \
M_"a[64:67], a[224:227], a[208:211], a[64:67]\n" \
"s_addk_i32 s84, 0x2000\n" \
"s_nop 0\n" \
M_"a[68:71], a[164:167], a[144:147], a[68:71]\n" \
B_"v25, s[76:79], s84 offen offset:1024 lds\n" \
M_"a[68:71], a[228:231], a[208:211], a[68:71]\n" \
X_"a[250:251], v38 offset:41984\n" \
"v_accvgpr_read_b32 v193, a25\n" \
M_"a[72:75], a[168:171], a[144:147], a[72:75]\n" \
"v_accvgpr_read_b32 v194, a26\n" \
"v_accvgpr_read_b32 v195, a27\n" \
M_"a[72:75], a[232:235], a[208:211], a[72:75]\n" \
"v_accvgpr_read_b32 v180, a28\n" \
"v_accvgpr_read_b32 v181, a29\n" \
M_"a[76:79], a[172:175], a[144:147], a[76:79]\n" \
"s_addk_i32 s84, 0x2000\n" \
M_"a[76:79], a[236:239], a[208:211], a[76:79]\n" \
B_"v24, s[76:79], s84 offen offset:2048 lds\n" \
M_"a[92:95], a[172:175], a[148:151], a[92:95]\n" \
"s_add_u32 s89, s41, 0x0\n" \
"buffer_store_dwordx4 v[184:187], v20, s[80:83], s89 offen offset:0 sc1\n" \
M_"a[92:95], a[236:239], a[212:215], a[92:95]\n" \
X_"a[188:189], v39 offset:32768\n" \
"v_accvgpr_read_b32 v182, a30\n" \
M_"a[88:91], a[168:171], a[148:151], a[88:91]\n" \
X_"a[190:191], v39 offset:33792\n" \
"v_accvgpr_read_b32 v183, a31\n" \
M_"a[88:91], a[232:235], a[212:215], a[88:91]\n" \
"s_addk_i32 s84, 0x2000\n" \
M_"a[84:87], a[164:167], a[148:151], a[84:87]\n" \
B_"v25, s[76:79], s84 offen offset:3072 lds\n" \
M_"a[84:87], a[228:231], a[212:215], a[84:87]\n" \
X_"a[252:253], v39 offset:40960\n" \
C_"v192, v192, v193\n" \
M_"a[80:83], a[160:163], a[148:151], a[80:83]\n" \
"s_add_u32 s88, s41, 0x0\n" \
"buffer_store_dwordx4 v[188:191], v20, s[80:83], s88 offen offset:1024 sc1\n" \
M_"a[80:83], a[224:227], a[212:215], a[80:83]\n" \
C_"v193, v194, v195\n" \
C_"v194, v180, v181\n" \
"s_waitcnt lgkmcnt(15)\n" \
"s_nop 0\n" \
M_"a[96:99], a[160:163], a[152:155], a[96:99]\n" \
X_"a[254:255], v39 offset:41984\n" \
C_"v195, v182, v183\n" \
M_"a[96:99], a[224:227], a[216:219], a[96:99]\n" \
"v_accvgpr_read_b32 v196, a16\n" \
"v_accvgpr_read_b32 v197, a17\n" \
M_"a[100:103], a[164:167], a[152:155], a[100:103]\n" \
"v_accvgpr_read_b32 v198, a18\n" \
"v_accvgpr_read_b32 v199, a19\n" \
M_"a[100:103], a[228:231], a[216:219], a[100:103]\n" \
"v_accvgpr_read_b32 v180, a20\n" \
"v_accvgpr_read_b32 v181, a21\n" \
M_"a[104:107], a[168:171], a[152:155], a[104:107]\n" \
"s_add_u32 s89, s41, 0x1000\n" \
"buffer_store_dwordx4 v[192:195], v20, s[80:83], s89 offen offset:1024 sc1\n" \
M_"a[104:107], a[232:235], a[216:219], a[104:107]\n" \
"v_accvgpr_read_b32 v182, a22\n" \
"v_accvgpr_read_b32 v183, a23\n" \
M_"a[108:111], a[172:175], a[152:155], a[108:111]\n" \
C_"v196, v196, v197\n" \
C_"v197, v198, v199\n" \
M_"a[108:111], a[236:239], a[216:219], a[108:111]\n" \
C_"v198, v180, v181\n" \
C_"v199, v182, v183\n" \
M_"a[124:127], a[172:175], a[156:159], a[124:127]\n" \
"v_accvgpr_read_b32 v200, a32\n" \
"v_accvgpr_read_b32 v201, a33\n" \
M_"a[124:127], a[236:239], a[220:223], a[124:127]\n" \
"v_accvgpr_read_b32 v202, a34\n" \
"v_accvgpr_read_b32 v203, a35\n" \
M_"a[120:123], a[168:171], a[156:159], a[120:123]\n" \
"s_add_u32 s88, s41, 0x1000\n" \
"buffer_store_dwordx4 v[196:199], v20, s[80:83], s88 offen offset:0 sc1\n" \
M_"a[120:123], a[232:235], a[220:223], a[120:123]\n" \
"v_accvgpr_read_b32 v180, a36\n" \
"v_accvgpr_read_b32 v181, a37\n" \
M_"a[116:119], a[164:167], a[156:159], a[116:119]\n" \
"v_accvgpr_read_b32 v182, a38\n" \
"v_accvgpr_read_b32 v183, a39\n" \
M_"a[116:119], a[228:231], a[220:223], a[116:119]\n" \
C_"v200, v200, v201\n" \
C_"v201, v202, v203\n" \
M_"a[112:115], a[160:163], a[156:159], a[112:115]\n" \
C_"v202, v180, v181\n" \
C_"v203, v182, v183\n" \
M_"a[112:115], a[224:227], a[220:223], a[112:115]\n" \
"v_accvgpr_read_b32 v204, a40\n" \
"v_accvgpr_read_b32 v205, a41\n" \
"s_waitcnt vmcnt(12) lgkmcnt(0)\n" \
"s_barrier\n" \
"v_mov_b32_e32 v36, v32\n" \
"v_xor_b32_e32 v32, 0x0, v48\n" \
"v_mov_b32_e32 v37, v33\n" \
"v_xor_b32_e32 v33, 0x20, v48\n" \
"v_mov_b32_e32 v38, v34\n" \
"v_xor_b32_e32 v34, 0x40, v48\n" \
"v_mov_b32_e64 v39, v35\n" \
"v_xor_b32_e32 v35, 0x60, v48\n" \
"v_add_u32_e32 v22, s44, v28\n" \
"v_add_u32_e32 v28, s43, v28\n" \
"v_add_u32_e32 v23, s44, v29\n" \
"v_add_u32_e32 v29, s43, v29\n" \
M_"v[52:55], a[176:179], a[128:131], v[52:55]\n" \
"s_add_u32 m0, s40, 0x8000\n" \
"s_add_u32 s85, s39, 0x2000\n" \
M_"v[52:55], a[240:243], a[192:195], v[52:55]\n" \
B_"v20, s[80:83], s85 offen sc1 lds\n" \
M_"v[56:59], a[180:183], a[128:131], v[56:59]\n" \
X_"a[160:161], v32 offset:0\n" \
"v_accvgpr_read_b32 v206, a42\n" \
M_"v[56:59], a[244:247], a[192:195], v[56:59]\n" \
"s_add_u32 m0, s40, 0x9000\n" \
"s_add_u32 s85, s39, 0x3000\n" \
M_"v[60:63], a[184:187], a[128:131], v[60:63]\n" \
B_"v20, s[80:83], s85 offen sc1 lds\n" \
M_"v[60:63], a[248:251], a[192:195], v[60:63]\n" \
X_"a[162:163], v32 offset:512\n" \
"v_accvgpr_read_b32 v207, a43\n" \
M_"v[64:67], a[188:191], a[128:131], v[64:67]\n" \
X_"a[224:225], v32 offset:4096\n" \
"v_accvgpr_read_b32 v180, a44\n" \
M_"v[64:67], a[252:255], a[192:195], v[64:67]\n" \
R_"a[128:131], v28 offset:0\n" \
"v_accvgpr_read_b32 v181, a45\n" \
M_"v[80:83], a[188:191], a[132:135], v[80:83]\n" \
R_"a[192:195], v29 offset:0\n" \
"v_accvgpr_read_b32 v182, a46\n" \
M_"v[80:83], a[252:255], a[196:199], v[80:83]\n" \
"s_add_u32 m0, s40, 0xb000\n" \
"s_movk_i32 s84, 0x80\n" \
M_"v[76:79], a[184:187], a[132:135], v[76:79]\n" \
B_"v21, s[76:79], s84 offen offset:0 lds\n" \
M_"v[76:79], a[248:251], a[196:199], v[76:79]\n" \
"s_add_u32 m0, s30, 0x8000\n" \
"s_movk_i32 s84, 0x80\n" \
M_"v[72:75], a[180:183], a[132:135], v[72:75]\n" \
B_"v24, s[76:79], s84 offen offset:0 lds\n" \
M_"v[72:75], a[244:247], a[196:199], v[72:75]\n" \
X_"a[226:227], v32 offset:4608\n" \
"v_accvgpr_read_b32 v183, a47\n" \
M_"v[68:71], a[176:179], a[132:135], v[68:71]\n" \
X_"a[164:165], v33 offset:0\n" \
C_"v204, v204, v205\n" \
M_"v[68:71], a[240:243], a[196:199], v[68:71]\n" \
R_"a[132:135], v28 offset:2048\n" \
C_"v205, v206, v207\n" \
M_"v[84:87], a[176:179], a[136:139], v[84:87]\n" \
R_"a[196:199], v29 offset:2048\n" \
C_"v206, v180, v181\n" \
M_"v[84:87], a[240:243], a[200:203], v[84:87]\n" \
"s_movk_i32 s84, 0x2080\n" \
M_"v[88:91], a[180:183], a[136:139], v[88:91]\n" \
B_"v25, s[76:79], s84 offen offset:1024 lds\n" \
M_"v[88:91], a[244:247], a[200:203], v[88:91]\n" \
"s_movk_i32 s84, 0x4080\n" \
M_"v[92:95], a[184:187], a[136:139], v[92:95]\n" \
B_"v24, s[76:79], s84 offen offset:2048 lds\n" \
M_"v[92:95], a[248:251], a[200:203], v[92:95]\n" \
X_"a[166:167], v33 offset:512\n" \
C_"v207, v182, v183\n" \
M_"v[96:99], a[188:191], a[136:139], v[96:99]\n" \
X_"a[228:229], v33 offset:4096\n" \
"v_accvgpr_read_b32 v208, a56\n" \
M_"v[96:99], a[252:255], a[200:203], v[96:99]\n" \
R_"a[136:139], v28 offset:4096\n" \
"v_accvgpr_read_b32 v209, a57\n" \
M_"v[112:115], a[188:191], a[140:143], v[112:115]\n" \
R_"a[200:203], v29 offset:4096\n" \
"v_accvgpr_read_b32 v210, a58\n" \
M_"v[112:115], a[252:255], a[204:207], v[112:115]\n" \
"s_movk_i32 s84, 0x6080\n" \
M_"v[108:111], a[184:187], a[140:143], v[108:111]\n" \
B_"v25, s[76:79], s84 offen offset:3072 lds\n" \
M_"v[108:111], a[248:251], a[204:207], v[108:111]\n" \
"s_add_u32 m0, s31, 0x8000\n" \
"s_mov_b32 s84, 0x80000\n" \
M_"v[104:107], a[180:183], a[140:143], v[104:107]\n" \
B_"v26, s[68:71], s84 offen offset:0 lds\n" \
M_"v[104:107], a[244:247], a[204:207], v[104:107]\n" \
"s_addk_i32 s84, 0x7c00\n" \
M_"v[100:103], a[176:179], a[140:143], v[100:103]\n" \
B_"v26, s[68:71], s84 offen offset:1024 lds\n" \
M_"v[100:103], a[240:243], a[204:207], v[100:103]\n" \
R_"a[140:143], v28 offset:6144\n" \
"v_accvgpr_read_b32 v211, a59\n" \
M_"v[116:119], a[176:179], a[144:147], v[116:119]\n" \
R_"a[204:207], v29 offset:6144\n" \
"v_accvgpr_read_b32 v180, a60\n" \
M_"v[116:119], a[240:243], a[208:211], v[116:119]\n" \
"s_addk_i32 s84, 0x7c00\n" \
M_"v[120:123], a[180:183], a[144:147], v[120:123]\n" \
B_"v27, s[68:71], s84 offen offset:2048 lds\n" \
M_"v[120:123], a[244:247], a[208:211], v[120:123]\n" \
X_"a[230:231], v33 offset:4608\n" \
"v_accvgpr_read_b32 v181, a61\n" \
M_"v[124:127], a[184:187], a[144:147], v[124:127]\n" \
"s_addk_i32 s84, 0x7c00\n" \
M_"v[124:127], a[248:251], a[208:211], v[124:127]\n" \
B_"v27, s[68:71], s84 offen offset:3072 lds\n" \
M_"v[128:131], a[188:191], a[144:147], v[128:131]\n" \
X_"a[172:173], v35 offset:0\n" \
"v_accvgpr_read_b32 v182, a62\n" \
M_"v[128:131], a[252:255], a[208:211], v[128:131]\n" \
R_"a[144:147], v28 offset:8192\n" \
"v_accvgpr_read_b32 v183, a63\n" \
M_"v[144:147], a[188:191], a[148:151], v[144:147]\n" \
R_"a[208:211], v29 offset:8192\n" \
C_"v208, v208, v209\n" \
M_"v[144:147], a[252:255], a[212:215], v[144:147]\n" \
"s_add_u32 m0, s30, 0x9000\n" \
"s_mov_b32 s84, 0x9080\n" \
M_"v[140:143], a[184:187], a[148:151], v[140:143]\n" \
B_"v24, s[76:79], s84 offen offset:0 lds\n" \
M_"v[140:143], a[248:251], a[212:215], v[140:143]\n" \
X_"a[174:175], v35 offset:512\n" \
C_"v209, v210, v211\n" \
M_"v[136:139], a[180:183], a[148:151], v[136:139]\n" \
"s_addk_i32 s84, 0x2000\n" \
"s_nop 0\n" \
M_"v[136:139], a[244:247], a[212:215], v[136:139]\n" \
B_"v25, s[76:79], s84 offen offset:1024 lds\n" \
M_"v[132:135], a[176:179], a[148:151], v[132:135]\n" \
X_"a[236:237], v35 offset:4096\n" \
C_"v210, v180, v181\n" \
M_"v[132:135], a[240:243], a[212:215], v[132:135]\n" \
R_"a[148:151], v28 offset:10240\n" \
C_"v211, v182, v183\n" \
M_"v[148:151], a[176:179], a[152:155], v[148:151]\n" \
R_"a[212:215], v29 offset:10240\n" \
"v_accvgpr_read_b32 v212, a48\n" \
M_"v[148:151], a[240:243], a[216:219], v[148:151]\n" \
X_"a[238:239], v35 offset:4608\n" \
"v_accvgpr_read_b32 v213, a49\n" \
M_"v[152:155], a[180:183], a[152:155], v[152:155]\n" \
X_"a[168:169], v34 offset:0\n" \
"v_accvgpr_read_b32 v214, a50\n" \
M_"v[152:155], a[244:247], a[216:219], v[152:155]\n" \
X_"a[170:171], v34 offset:512\n" \
"v_accvgpr_read_b32 v215, a51\n" \
M_"v[156:159], a[184:187], a[152:155], v[156:159]\n" \
X_"a[232:233], v34 offset:4096\n" \
"v_accvgpr_read_b32 v180, a52\n" \
M_"v[156:159], a[248:251], a[216:219], v[156:159]\n" \
X_"a[234:235], v34 offset:4608\n" \
"v_accvgpr_read_b32 v181, a53\n" \
M_"v[160:163], a[188:191], a[152:155], v[160:163]\n" \
R_"v[248:251], v22 offset:0\n" \
"v_accvgpr_read_b32 v182, a54\n" \
M_"v[160:163], a[252:255], a[216:219], v[160:163]\n" \
R_"a[152:155], v28 offset:12288\n" \
"v_accvgpr_read_b32 v183, a55\n" \
M_"v[176:179], a[188:191], a[156:159], v[176:179]\n" \
R_"a[216:219], v29 offset:12288\n" \
C_"v212, v212, v213\n" \
M_"v[176:179], a[252:255], a[220:223], v[176:179]\n" \
R_"v[48:51], v23 offset:0\n" \
C_"v213, v214, v215\n" \
M_"v[172:175], a[184:187], a[156:159], v[172:175]\n" \
C_"v214, v180, v181\n" \
C_"v215, v182, v183\n" \
M_"v[172:175], a[248:251], a[220:223], v[172:175]\n" \
"v_accvgpr_read_b32 v216, a64\n" \
"v_accvgpr_read_b32 v217, a65\n" \
M_"v[168:171], a[180:183], a[156:159], v[168:171]\n" \
"v_accvgpr_read_b32 v218, a66\n" \
"v_accvgpr_read_b32 v219, a67\n" \
M_"v[168:171], a[244:247], a[220:223], v[168:171]\n" \
"v_accvgpr_read_b32 v180, a68\n" \
"v_accvgpr_read_b32 v181, a69\n" \
M_"v[164:167], a[176:179], a[156:159], v[164:167]\n" \
"v_accvgpr_read_b32 v182, a70\n" \
"v_accvgpr_read_b32 v183, a71\n" \
M_"v[164:167], a[240:243], a[220:223], v[164:167]\n" \
R_"a[156:159], v28 offset:14336\n" \
R_"a[220:223], v29 offset:14336\n" \
"s_waitcnt lgkmcnt(15)\n" \
M_"a[0:3], a[160:163], a[128:131], 0\n" \
"s_addk_i32 s84, 0x2000\n" \
C_"v216, v216, v217\n" \
M_"a[0:3], a[224:227], a[192:195], a[0:3]\n" \
B_"v24, s[76:79], s84 offen offset:2048 lds\n" \
C_"v217, v218, v219\n" \
M_"a[4:7], a[164:167], a[128:131], 0\n" \
X_"a[176:177], v36 offset:0\n" \
C_"v218, v180, v181\n" \
M_"a[4:7], a[228:231], a[192:195], a[4:7]\n" \
"s_addk_i32 s84, 0x2000\n" \
C_"v219, v182, v183\n" \
"s_waitcnt lgkmcnt(15)\n" \
M_"a[12:15], a[172:175], a[128:131], 0\n" \
B_"v25, s[76:79], s84 offen offset:3072 lds\n" \
"v_accvgpr_read_b32 v220, a80\n" \
"s_waitcnt lgkmcnt(11)\n" \
M_"a[12:15], a[236:239], a[192:195], a[12:15]\n" \
X_"a[178:179], v36 offset:1024\n" \
"v_accvgpr_read_b32 v221, a81\n" \
"s_waitcnt lgkmcnt(10)\n" \
M_"a[8:11], a[128:131], a[168:171], 0\n" \
X_"a[240:241], v36 offset:8192\n" \
"v_accvgpr_read_b32 v222, a82\n" \
"s_waitcnt lgkmcnt(9)\n" \
"s_nop 0\n" \
M_"a[8:11], a[192:195], a[232:235], a[8:11]\n" \
X_"a[242:243], v36 offset:9216\n" \
"v_accvgpr_read_b32 v223, a83\n" \
M_"a[24:27], a[132:135], a[168:171], 0\n" \
X_"a[180:181], v37 offset:0\n" \
"v_accvgpr_read_b32 v180, a84\n" \
M_"a[24:27], a[196:199], a[232:235], a[24:27]\n" \
X_"a[182:183], v37 offset:1024\n" \
"v_accvgpr_read_b32 v181, a85\n" \
M_"a[28:31], a[172:175], a[132:135], 0\n" \
X_"a[244:245], v37 offset:8192\n" \
"v_accvgpr_read_b32 v182, a86\n" \
M_"a[28:31], a[236:239], a[196:199], a[28:31]\n" \
"s_add_u32 s89, s41, 0x2000\n" \
"buffer_store_dwordx4 v[200:203], v20, s[80:83], s89 offen offset:0 sc1\n" \
M_"a[20:23], a[164:167], a[132:135], 0\n" \
X_"a[246:247], v37 offset:9216\n" \
"v_accvgpr_read_b32 v183, a87\n" \
M_"a[20:23], a[228:231], a[196:199], a[20:23]\n" \
X_"a[184:185], v38 offset:0\n" \
C_"v220, v220, v221\n" \
M_"a[16:19], a[160:163], a[132:135], 0\n" \
X_"a[186:187], v38 offset:1024\n" \
C_"v221, v222, v223\n" \
M_"a[16:19], a[224:227], a[196:199], a[16:19]\n" \
C_"v222, v180, v181\n" \
C_"v223, v182, v183\n" \
C_"v224, v52, v53\n" \
".fill (L_pb + 20192 - .) / 4, 4, 0xBF800000\n" \
"L_pp5:\n" \
"s_cbranch_execz L_pret\n" \
"s_nop 0\n" \
M_"a[32:35], a[160:163], a[136:139], 0\n" \
"s_add_u32 s88, s41, 0x2000\n" \
"buffer_store_dwordx4 v[204:207], v20, s[80:83], s88 offen offset:1024 sc1\n" \
M_"a[32:35], a[224:227], a[200:203], a[32:35]\n" \
X_"a[248:249], v38 offset:8192\n" \
C_"v225, v54, v55\n" \
M_"a[36:39], a[164:167], a[136:139], 0\n" \
X_"a[250:251], v38 offset:9216\n" \
C_"v226, v56, v57\n" \
M_"a[36:39], a[228:231], a[200:203], a[36:39]\n" \
X_"a[188:189], v39 offset:0\n" \
C_"v227, v58, v59\n" \
M_"a[44:47], a[172:175], a[136:139], 0\n" \
C_"v228, v60, v61\n" \
C_"v229, v62, v63\n" \
C_"v230, v64, v65\n" \
M_"a[44:47], a[236:239], a[200:203], a[44:47]\n" \
X_"a[190:191], v39 offset:1024\n" \
C_"v231, v66, v67\n" \
M_"a[40:43], a[136:139], a[168:171], 0\n" \
"s_add_u32 s89, s41, 0x3000\n" \
"buffer_store_dwordx4 v[208:211], v20, s[80:83], s89 offen offset:1024 sc1\n" \
M_"a[40:43], a[200:203], a[232:235], a[40:43]\n" \
X_"a[252:253], v39 offset:8192\n" \
C_"v232, v76, v77\n" \
M_"a[56:59], a[140:143], a[168:171], 0\n" \
X_"a[254:255], v39 offset:9216\n" \
C_"v233, v78, v79\n" \
M_"a[56:59], a[204:207], a[232:235], a[56:59]\n" \
C_"v234, v80, v81\n" \
C_"v235, v82, v83\n" \
C_"v236, v68, v69\n" \
M_"a[60:63], a[172:175], a[140:143], 0\n" \
C_"v237, v70, v71\n" \
C_"v238, v72, v73\n" \
C_"v239, v74, v75\n" \
M_"a[60:63], a[236:239], a[204:207], a[60:63]\n" \
"s_add_u32 s88, s41, 0x3000\n" \
"buffer_store_dwordx4 v[212:215], v20, s[80:83], s88 offen offset:0 sc1\n" \
M_"a[52:55], a[164:167], a[140:143], 0\n" \
C_"v240, v84, v85\n" \
C_"v241, v86, v87\n" \
C_"v242, v88, v89\n" \
M_"a[52:55], a[228:231], a[204:207], a[52:55]\n" \
C_"v243, v90, v91\n" \
C_"v244, v92, v93\n" \
C_"v245, v94, v95\n" \
M_"a[48:51], a[160:163], a[140:143], 0\n" \
C_"v246, v96, v97\n" \
C_"v247, v98, v99\n" \
C_"v184, v108, v109\n" \
M_"a[48:51], a[224:227], a[204:207], a[48:51]\n" \
C_"v185, v110, v111\n" \
C_"v186, v112, v113\n" \
C_"v187, v114, v115\n" \
"s_waitcnt lgkmcnt(15)\n" \
"s_nop 0\n" \
M_"a[64:67], a[164:167], v[248:251], 0\n" \
C_"v188, v100, v101\n" \
C_"v189, v102, v103\n" \
C_"v190, v104, v105\n" \
M_"a[64:67], a[228:231], v[48:51], a[64:67]\n" \
C_"v191, v106, v107\n" \
C_"v192, v116, v117\n" \
C_"v193, v118, v119\n" \
M_"a[80:83], a[172:175], v[248:251], 0\n" \
C_"v194, v120, v121\n" \
C_"v195, v122, v123\n" \
C_"v196, v124, v125\n" \
M_"a[80:83], a[236:239], v[48:51], a[80:83]\n" \
C_"v197, v126, v127\n" \
C_"v198, v128, v129\n" \
C_"v199, v130, v131\n" \
M_"a[68:71], v[248:251], a[168:171], 0\n" \
C_"v252, v140, v141\n" \
C_"v253, v142, v143\n" \
C_"v254, v144, v145\n" \
M_"a[68:71], v[48:51], a[232:235], a[68:71]\n" \
C_"v255, v146, v147\n" \
"v_accvgpr_write_b32 a84, v252\n" \
"v_accvgpr_write_b32 a85, v253\n" \
"s_waitcnt vmcnt(4) lgkmcnt(0)\n" \
"s_barrier\n" \
M_"v[52:55], a[176:179], a[128:131], 0\n" \
"s_add_u32 m0, s40, 0x0\n" \
"s_nop 0\n" \
"s_add_u32 s85, s39, 0x4000\n" \
"v_accvgpr_write_b32 a86, v254\n" \
M_"v[52:55], a[240:243], a[192:195], v[52:55]\n" \
B_"v20, s[80:83], s85 offen sc1 lds\n" \
"v_accvgpr_write_b32 a87, v255\n" \
M_"v[56:59], a[180:183], a[128:131], 0\n" \
X_"a[160:161], v32 offset:32768\n" \
C_"v252, v132, v133\n" \
M_"v[56:59], a[244:247], a[192:195], v[56:59]\n" \
X_"a[162:163], v32 offset:33280\n" \
C_"v253, v134, v135\n" \
M_"v[60:63], a[184:187], a[128:131], 0\n" \
"s_add_u32 m0, s40, 0x1000\n" \
"s_add_u32 s85, s39, 0x5000\n" \
C_"v254, v136, v137\n" \
M_"v[60:63], a[248:251], a[192:195], v[60:63]\n" \
B_"v20, s[80:83], s85 offen sc1 lds\n" \
C_"v255, v138, v139\n" \
M_"v[64:67], a[188:191], a[128:131], 0\n" \
X_"a[224:225], v32 offset:36864\n" \
C_"v200, v148, v149\n" \
M_"v[64:67], a[252:255], a[192:195], v[64:67]\n" \
R_"a[128:131], v28 offset:32768\n" \
C_"v201, v150, v151\n" \
M_"v[80:83], a[188:191], a[132:135], 0\n" \
R_"a[192:195], v29 offset:32768\n" \
C_"v202, v152, v153\n" \
M_"v[80:83], a[252:255], a[196:199], v[80:83]\n" \
"s_add_u32 m0, s40, 0x3000\n" \
"s_movk_i32 s84, 0x100\n" \
"s_nop 0\n" \
C_"v203, v154, v155\n" \
M_"v[76:79], a[184:187], a[132:135], 0\n" \
B_"v21, s[76:79], s84 offen offset:0 lds\n" \
C_"v204, v156, v157\n" \
M_"v[76:79], a[248:251], a[196:199], v[76:79]\n" \
X_"a[226:227], v32 offset:37376\n" \
C_"v205, v158, v159\n" \
M_"v[72:75], a[180:183], a[132:135], 0\n" \
X_"a[164:165], v33 offset:32768\n" \
C_"v206, v160, v161\n" \
M_"v[72:75], a[244:247], a[196:199], v[72:75]\n" \
C_"v207, v162, v163\n" \
C_"v208, v172, v173\n" \
C_"v209, v174, v175\n" \
M_"v[68:71], a[176:179], a[132:135], 0\n" \
X_"a[166:167], v33 offset:33280\n" \
C_"v210, v176, v177\n" \
M_"v[68:71], a[240:243], a[196:199], v[68:71]\n" \
R_"a[132:135], v28 offset:34816\n" \
C_"v211, v178, v179\n" \
M_"v[84:87], a[176:179], a[136:139], 0\n" \
R_"a[196:199], v29 offset:34816\n" \
C_"v212, v164, v165\n" \
M_"v[84:87], a[240:243], a[200:203], v[84:87]\n" \
"s_add_u32 m0, s31, 0x0\n" \
"s_mov_b32 s84, 0x100000\n" \
C_"v213, v166, v167\n" \
M_"v[88:91], a[180:183], a[136:139], 0\n" \
B_"v26, s[68:71], s84 offen offset:0 lds\n" \
C_"v214, v168, v169\n" \
M_"v[88:91], a[244:247], a[200:203], v[88:91]\n" \
"s_add_u32 s89, s41, 0x0\n" \
"buffer_store_dwordx4 v[224:227], v20, s[80:83], s89 offen offset:2048 sc1\n" \
M_"v[92:95], a[184:187], a[136:139], 0\n" \
X_"a[228:229], v33 offset:36864\n" \
C_"v215, v170, v171\n" \
M_"v[92:95], a[248:251], a[200:203], v[92:95]\n" \
X_"a[230:231], v33 offset:37376\n" \
M_"v[96:99], a[188:191], a[136:139], 0\n" \
M_"v[96:99], a[252:255], a[200:203], v[96:99]\n" \
R_"a[136:139], v28 offset:36864\n" \
M_"v[112:115], a[188:191], a[140:143], 0\n" \
R_"a[200:203], v29 offset:36864\n" \
M_"v[112:115], a[252:255], a[204:207], v[112:115]\n" \
"s_addk_i32 s84, 0x7c00\n" \
M_"v[108:111], a[184:187], a[140:143], 0\n" \
B_"v26, s[68:71], s84 offen offset:1024 lds\n" \
M_"v[108:111], a[248:251], a[204:207], v[108:111]\n" \
X_"a[172:173], v35 offset:32768\n" \
M_"v[104:107], a[180:183], a[140:143], 0\n" \
"s_addk_i32 s84, 0x7c00\n" \
M_"v[104:107], a[244:247], a[204:207], v[104:107]\n" \
B_"v27, s[68:71], s84 offen offset:2048 lds\n" \
M_"v[100:103], a[176:179], a[140:143], 0\n" \
X_"a[174:175], v35 offset:33280\n" \
M_"v[100:103], a[240:243], a[204:207], v[100:103]\n" \
R_"a[140:143], v28 offset:38912\n" \
M_"v[116:119], a[176:179], a[144:147], 0\n" \
R_"a[204:207], v29 offset:38912\n" \
M_"v[116:119], a[240:243], a[208:211], v[116:119]\n" \
"s_addk_i32 s84, 0x7c00\n" \
M_"v[120:123], a[180:183], a[144:147], 0\n" \
B_"v27, s[68:71], s84 offen offset:3072 lds\n" \
M_"v[120:123], a[244:247], a[208:211], v[120:123]\n" \
X_"a[236:237], v35 offset:36864\n" \
M_"v[124:127], a[184:187], a[144:147], 0\n" \
X_"a[238:239], v35 offset:37376\n" \
M_"v[124:127], a[248:251], a[208:211], v[124:127]\n" \
"s_add_u32 m0, s30, 0x0\n" \
"s_movk_i32 s84, 0x180\n" \
M_"v[128:131], a[188:191], a[144:147], 0\n" \
B_"v24, s[76:79], s84 offen offset:0 lds\n" \
M_"v[128:131], a[252:255], a[208:211], v[128:131]\n" \
R_"a[144:147], v28 offset:40960\n" \
M_"v[144:147], a[188:191], a[148:151], 0\n" \
R_"a[208:211], v29 offset:40960\n" \
M_"v[144:147], a[252:255], a[212:215], v[144:147]\n" \
X_"a[168:169], v34 offset:32768\n" \
M_"v[140:143], a[184:187], a[148:151], 0\n" \
"s_movk_i32 s84, 0x2180\n" \
M_"v[140:143], a[248:251], a[212:215], v[140:143]\n" \
B_"v25, s[76:79], s84 offen offset:1024 lds\n" \
M_"v[136:139], a[180:183], a[148:151], 0\n" \
X_"a[170:171], v34 offset:33280\n" \
M_"v[136:139], a[244:247], a[212:215], v[136:139]\n" \
X_"a[232:233], v34 offset:36864\n" \
M_"v[132:135], a[176:179], a[148:151], 0\n" \
X_"a[234:235], v34 offset:37376\n" \
M_"v[132:135], a[240:243], a[212:215], v[132:135]\n" \
R_"a[148:151], v28 offset:43008\n" \
M_"v[148:151], a[176:179], a[152:155], 0\n" \
R_"a[212:215], v29 offset:43008\n" \
M_"v[148:151], a[240:243], a[216:219], v[148:151]\n" \
"s_movk_i32 s84, 0x4180\n" \
M_"v[152:155], a[180:183], a[152:155], 0\n" \
B_"v24, s[76:79], s84 offen offset:2048 lds\n" \
M_"v[152:155], a[244:247], a[216:219], v[152:155]\n" \
"s_movk_i32 s84, 0x6180\n" \
M_"v[156:159], a[184:187], a[152:155], 0\n" \
B_"v25, s[76:79], s84 offen offset:3072 lds\n" \
M_"v[156:159], a[248:251], a[216:219], v[156:159]\n" \
R_"v[248:251], v22 offset:32768\n" \
M_"v[160:163], a[188:191], a[152:155], 0\n" \
R_"v[48:51], v23 offset:32768\n" \
M_"v[160:163], a[252:255], a[216:219], v[160:163]\n" \
R_"a[152:155], v28 offset:45056\n" \
M_"v[176:179], a[188:191], a[156:159], 0\n" \
R_"a[216:219], v29 offset:45056\n" \
M_"v[176:179], a[252:255], a[220:223], v[176:179]\n" \
M_"v[172:175], a[184:187], a[156:159], 0\n" \
M_"v[172:175], a[248:251], a[220:223], v[172:175]\n" \
M_"v[168:171], a[180:183], a[156:159], 0\n" \
M_"v[168:171], a[244:247], a[220:223], v[168:171]\n" \
M_"v[164:167], a[176:179], a[156:159], 0\n" \
M_"v[164:167], a[240:243], a[220:223], v[164:167]\n" \
R_"a[156:159], v28 offset:47104\n" \
R_"a[220:223], v29 offset:47104\n" \
"s_waitcnt lgkmcnt(15)\n" \
M_"a[0:3], a[160:163], a[128:131], a[0:3]\n" \
"s_add_u32 m0, s30, 0x1000\n" \
"s_addk_i32 s84, 0x3000\n" \
M_"a[0:3], a[224:227], a[192:195], a[0:3]\n" \
B_"v24, s[76:79], s84 offen offset:0 lds\n" \
M_"a[4:7], a[164:167], a[128:131], a[4:7]\n" \
X_"a[176:177], v36 offset:32768\n" \
"v_mov_b32_e64 v11, 0\n" \
M_"a[4:7], a[228:231], a[192:195], a[4:7]\n" \
X_"a[178:179], v36 offset:33792\n" \
"s_and_b32 s5, s2, 7\n" \
M_"a[12:15], a[172:175], a[128:131], a[12:15]\n" \
X_"a[240:241], v36 offset:40960\n" \
"s_lshl_b32 s6, s3, 1\n" \
"s_waitcnt lgkmcnt(15)\n" \
M_"a[12:15], a[236:239], a[192:195], a[12:15]\n" \
"s_add_u32 s88, s41, 0x0\n" \
"buffer_store_dwordx4 v[228:231], v20, s[80:83], s88 offen offset:3072 sc1\n" \
"s_waitcnt lgkmcnt(13)\n" \
M_"a[8:11], a[128:131], a[168:171], a[8:11]\n" \
X_"a[242:243], v36 offset:41984\n" \
"s_lshl_b32 s4, s5, 2\n" \
"s_waitcnt lgkmcnt(12)\n" \
M_"a[8:11], a[192:195], a[232:235], a[8:11]\n" \
X_"a[180:181], v37 offset:32768\n" \
"s_bfe_u32 s7, s2, 0x20003\n" \
M_"a[24:27], a[132:135], a[168:171], a[24:27]\n" \
X_"a[182:183], v37 offset:33792\n" \
"s_or_b32 s12, s4, s7\n" \
M_"a[24:27], a[196:199], a[232:235], a[24:27]\n" \
X_"a[244:245], v37 offset:40960\n" \
"s_and_b32 s6, s6, 4\n" \
M_"a[28:31], a[172:175], a[132:135], a[28:31]\n" \
"s_and_b32 s8, s3, 1\n" \
"s_or_b32 s8, s6, s8\n" \
M_"a[28:31], a[236:239], a[196:199], a[28:31]\n" \
"s_addk_i32 s84, 0x2000\n" \
M_"a[20:23], a[164:167], a[132:135], a[20:23]\n" \
B_"v25, s[76:79], s84 offen offset:1024 lds\n" \
M_"a[20:23], a[228:231], a[196:199], a[20:23]\n" \
X_"a[246:247], v37 offset:41984\n" \
"s_lshl_b32 s6, s12, 4\n" \
M_"a[16:19], a[160:163], a[132:135], a[16:19]\n" \
X_"a[184:185], v38 offset:32768\n" \
"s_lshl_b32 s9, s3, 2\n" \
M_"a[16:19], a[224:227], a[196:199], a[16:19]\n" \
X_"a[186:187], v38 offset:33792\n" \
"s_or_b32 s6, s6, s9\n" \
M_"a[32:35], a[160:163], a[136:139], a[32:35]\n" \
"s_addk_i32 s84, 0x2000\n" \
M_"a[32:35], a[224:227], a[200:203], a[32:35]\n" \
B_"v24, s[76:79], s84 offen offset:2048 lds\n" \
M_"a[36:39], a[164:167], a[136:139], a[36:39]\n" \
X_"a[248:249], v38 offset:40960\n" \
"s_lshr_b32 s9, s2, 5\n" \
M_"a[36:39], a[228:231], a[200:203], a[36:39]\n" \
X_"a[250:251], v38 offset:41984\n" \
"s_mul_i32 s10, s9, 0xa00\n" \
M_"a[44:47], a[172:175], a[136:139], a[44:47]\n" \
X_"a[188:189], v39 offset:32768\n" \
"s_add_i32 s10, s10, 0x5bc1000\n" \
M_"a[44:47], a[236:239], a[200:203], a[44:47]\n" \
X_"a[190:191], v39 offset:33792\n" \
"s_or_b32 s6, s6, s10\n" \
M_"a[40:43], a[136:139], a[168:171], a[40:43]\n" \
"s_add_u32 s89, s41, 0x1000\n" \
"buffer_store_dwordx4 v[232:235], v20, s[80:83], s89 offen offset:3072 sc1\n" \
M_"a[40:43], a[200:203], a[232:235], a[40:43]\n" \
X_"a[252:253], v39 offset:40960\n" \
"s_lshl_b32 s13, s9, 3\n" \
M_"a[56:59], a[140:143], a[168:171], a[56:59]\n" \
X_"a[254:255], v39 offset:41984\n" \
"v_writelane_b32 v11, s6, 0\n" \
M_"a[56:59], a[204:207], a[232:235], a[56:59]\n" \
"s_or_b32 s6, s13, s5\n" \
"v_writelane_b32 v11, s10, 1\n" \
M_"a[60:63], a[172:175], a[140:143], a[60:63]\n" \
"s_lshl_b32 s6, s6, 10\n" \
"s_lshl_b32 s10, s3, 8\n" \
M_"a[60:63], a[236:239], a[204:207], a[60:63]\n" \
"s_addk_i32 s84, 0x2000\n" \
M_"a[52:55], a[164:167], a[140:143], a[52:55]\n" \
B_"v25, s[76:79], s84 offen offset:3072 lds\n" \
M_"a[52:55], a[228:231], a[204:207], a[52:55]\n" \
"s_or_b32 s6, s6, s10\n" \
"s_or_b32 s10, s6, 0x5c00200\n" \
M_"a[48:51], a[160:163], a[140:143], a[48:51]\n" \
"v_writelane_b32 v11, s10, 2\n" \
"s_lshl_b32 s10, s7, 2\n" \
M_"a[48:51], a[224:227], a[204:207], a[48:51]\n" \
"s_or_b32 s11, s10, s6\n" \
"s_or_b32 s11, s11, 0x5c00000\n" \
"s_waitcnt lgkmcnt(15)\n" \
M_"a[64:67], a[164:167], v[248:251], a[64:67]\n" \
"v_writelane_b32 v11, s11, 3\n" \
"s_or_b32 s6, s6, 0x5c00080\n" \
M_"a[64:67], a[228:231], v[48:51], a[64:67]\n" \
"v_writelane_b32 v11, s6, 4\n" \
"s_or_b32 s6, s6, s10\n" \
M_"a[80:83], a[172:175], v[248:251], a[80:83]\n" \
"s_add_i32 s10, s3, s2\n" \
"v_writelane_b32 v11, s6, 5\n" \
M_"a[80:83], a[236:239], v[48:51], a[80:83]\n" \
"s_mov_b32 s6, 2.0\n" \
"s_mov_b32 s11, 0x5bc0000\n" \
M_"a[68:71], v[248:251], a[168:171], a[68:71]\n" \
"s_cmp_lg_u32 s10, 0\n" \
"s_cselect_b32 s10, s6, s11\n" \
"s_waitcnt vmcnt(10) lgkmcnt(0)\n" \
M_"a[68:71], v[48:51], a[232:235], a[68:71]\n" \
"s_barrier\n" \
"v_xor_b32_e32 v28, 0x20000, v28\n" \
"v_xor_b32_e32 v29, 0x20000, v29\n" \
M_"v[52:55], a[176:179], a[128:131], v[52:55]\n" \
"s_add_u32 m0, s40, 0x8000\n" \
"s_add_u32 s85, s39, 0x6000\n" \
M_"v[52:55], a[240:243], a[192:195], v[52:55]\n" \
B_"v20, s[80:83], s85 offen sc1 lds\n" \
M_"v[56:59], a[180:183], a[128:131], v[56:59]\n" \
X_"a[160:161], v32 offset:0\n" \
"s_lshr_b32 s14, s2, 3\n" \
M_"v[56:59], a[244:247], a[192:195], v[56:59]\n" \
X_"a[162:163], v32 offset:512\n" \
"v_writelane_b32 v11, s10, 57\n" \
M_"v[60:63], a[184:187], a[128:131], v[60:63]\n" \
"s_add_u32 m0, s40, 0x9000\n" \
"s_add_u32 s85, s39, 0x7000\n" \
M_"v[60:63], a[248:251], a[192:195], v[60:63]\n" \
B_"v20, s[80:83], s85 offen sc1 lds\n" \
M_"v[64:67], a[188:191], a[128:131], v[64:67]\n" \
X_"a[224:225], v32 offset:4096\n" \
"s_lshl_b32 s10, s5, 5\n" \
M_"v[64:67], a[252:255], a[192:195], v[64:67]\n" \
R_"a[128:131], v28 offset:0\n" \
"s_min_u32 s15, s3, 2\n" \
"s_nop 0\n" \
M_"v[80:83], a[188:191], a[132:135], v[80:83]\n" \
R_"a[192:195], v29 offset:0\n" \
"s_mul_i32 s11, s7, 0x120\n" \
M_"v[80:83], a[252:255], a[196:199], v[80:83]\n" \
"s_add_u32 s88, s41, 0x1000\n" \
"buffer_store_dwordx4 v[236:239], v20, s[80:83], s88 offen offset:2048 sc1\n" \
M_"v[76:79], a[184:187], a[132:135], v[76:79]\n" \
X_"a[226:227], v32 offset:4608\n" \
M_"v[76:79], a[248:251], a[196:199], v[76:79]\n" \
X_"a[164:165], v33 offset:0\n" \
M_"v[72:75], a[180:183], a[132:135], v[72:75]\n" \
"s_add_u32 m0, s40, 0xb000\n" \
"s_movk_i32 s84, 0x180\n" \
"s_nop 0\n" \
M_"v[72:75], a[244:247], a[196:199], v[72:75]\n" \
B_"v21, s[76:79], s84 offen offset:0 lds\n" \
M_"v[68:71], a[176:179], a[132:135], v[68:71]\n" \
X_"a[166:167], v33 offset:512\n" \
M_"v[68:71], a[240:243], a[196:199], v[68:71]\n" \
R_"a[132:135], v28 offset:2048\n" \
M_"v[84:87], a[176:179], a[136:139], v[84:87]\n" \
R_"a[196:199], v29 offset:2048\n" \
M_"v[84:87], a[240:243], a[200:203], v[84:87]\n" \
"s_add_u32 m0, s31, 0x8000\n" \
"s_mov_b32 s84, 0x180000\n" \
M_"v[88:91], a[180:183], a[136:139], v[88:91]\n" \
B_"v26, s[68:71], s84 offen offset:0 lds\n" \
M_"v[88:91], a[244:247], a[200:203], v[88:91]\n" \
X_"a[228:229], v33 offset:4096\n" \
M_"v[92:95], a[184:187], a[136:139], v[92:95]\n" \
X_"a[230:231], v33 offset:4608\n" \
M_"v[92:95], a[248:251], a[200:203], v[92:95]\n" \
"s_addk_i32 s84, 0x7c00\n" \
M_"v[96:99], a[188:191], a[136:139], v[96:99]\n" \
B_"v26, s[68:71], s84 offen offset:1024 lds\n" \
M_"v[96:99], a[252:255], a[200:203], v[96:99]\n" \
R_"a[136:139], v28 offset:4096\n" \
M_"v[112:115], a[188:191], a[140:143], v[112:115]\n" \
R_"a[200:203], v29 offset:4096\n" \
M_"v[112:115], a[252:255], a[204:207], v[112:115]\n" \
"s_addk_i32 s84, 0x7c00\n" \
M_"v[108:111], a[184:187], a[140:143], v[108:111]\n" \
B_"v27, s[68:71], s84 offen offset:2048 lds\n" \
M_"v[108:111], a[248:251], a[204:207], v[108:111]\n" \
X_"a[172:173], v35 offset:0\n" \
M_"v[104:107], a[180:183], a[140:143], v[104:107]\n" \
X_"a[174:175], v35 offset:512\n" \
M_"v[104:107], a[244:247], a[204:207], v[104:107]\n" \
X_"a[236:237], v35 offset:4096\n" \
M_"v[100:103], a[176:179], a[140:143], v[100:103]\n" \
"s_add_u32 s16, s22, s11\n" \
"s_addc_u32 s17, s23, 0\n" \
M_"v[100:103], a[240:243], a[204:207], v[100:103]\n" \
R_"a[140:143], v28 offset:6144\n" \
M_"v[116:119], a[176:179], a[144:147], v[116:119]\n" \
R_"a[204:207], v29 offset:6144\n" \
M_"v[116:119], a[240:243], a[208:211], v[116:119]\n" \
"s_addk_i32 s84, 0x7c00\n" \
M_"v[120:123], a[180:183], a[144:147], v[120:123]\n" \
B_"v27, s[68:71], s84 offen offset:3072 lds\n" \
M_"v[120:123], a[244:247], a[208:211], v[120:123]\n" \
"s_add_u32 m0, s30, 0x8000\n" \
"s_movk_i32 s84, 0x200\n" \
M_"v[124:127], a[184:187], a[144:147], v[124:127]\n" \
B_"v24, s[76:79], s84 offen offset:0 lds\n" \
M_"v[124:127], a[248:251], a[208:211], v[124:127]\n" \
X_"a[238:239], v35 offset:4608\n" \
M_"v[128:131], a[188:191], a[144:147], v[128:131]\n" \
"s_add_u32 s89, s41, 0x2000\n" \
"buffer_store_dwordx4 v[240:243], v20, s[80:83], s89 offen offset:2048 sc1\n" \
M_"v[128:131], a[252:255], a[208:211], v[128:131]\n" \
R_"a[144:147], v28 offset:8192\n" \
M_"v[144:147], a[188:191], a[148:151], v[144:147]\n" \
R_"a[208:211], v29 offset:8192\n" \
M_"v[144:147], a[252:255], a[212:215], v[144:147]\n" \
X_"a[168:169], v34 offset:0\n" \
M_"v[140:143], a[184:187], a[148:151], v[140:143]\n" \
X_"a[170:171], v34 offset:512\n" \
M_"v[140:143], a[248:251], a[212:215], v[140:143]\n" \
"s_movk_i32 s84, 0x2200\n" \
"s_nop 0\n" \
M_"v[136:139], a[180:183], a[148:151], v[136:139]\n" \
B_"v25, s[76:79], s84 offen offset:1024 lds\n" \
M_"v[136:139], a[244:247], a[212:215], v[136:139]\n" \
X_"a[232:233], v34 offset:4096\n" \
M_"v[132:135], a[176:179], a[148:151], v[132:135]\n" \
X_"a[234:235], v34 offset:4608\n" \
M_"v[132:135], a[240:243], a[212:215], v[132:135]\n" \
R_"a[148:151], v28 offset:10240\n" \
M_"v[148:151], a[176:179], a[152:155], v[148:151]\n" \
R_"a[212:215], v29 offset:10240\n" \
M_"v[148:151], a[240:243], a[216:219], v[148:151]\n" \
"s_movk_i32 s84, 0x4200\n" \
M_"v[152:155], a[180:183], a[152:155], v[152:155]\n" \
B_"v24, s[76:79], s84 offen offset:2048 lds\n" \
M_"v[152:155], a[244:247], a[216:219], v[152:155]\n" \
"s_movk_i32 s84, 0x6200\n" \
M_"v[156:159], a[184:187], a[152:155], v[156:159]\n" \
B_"v25, s[76:79], s84 offen offset:3072 lds\n" \
M_"v[156:159], a[248:251], a[216:219], v[156:159]\n" \
R_"v[248:251], v22 offset:0\n" \
M_"v[160:163], a[188:191], a[152:155], v[160:163]\n" \
R_"v[48:51], v23 offset:0\n" \
M_"v[160:163], a[252:255], a[216:219], v[160:163]\n" \
R_"a[152:155], v28 offset:12288\n" \
M_"v[176:179], a[188:191], a[156:159], v[176:179]\n" \
R_"a[216:219], v29 offset:12288\n" \
M_"v[176:179], a[252:255], a[220:223], v[176:179]\n" \
"s_add_u32 s18, s24, s11\n" \
"v_writelane_b32 v11, s16, 9\n" \
"s_addc_u32 s19, s25, 0\n" \
M_"v[172:175], a[184:187], a[156:159], v[172:175]\n" \
"s_and_b32 s20, s17, 0xffff\n" \
"v_writelane_b32 v11, s20, 10\n" \
M_"v[172:175], a[248:251], a[220:223], v[172:175]\n" \
"s_and_b32 s20, s19, 0xffff\n" \
"v_writelane_b32 v11, s18, 11\n" \
M_"v[168:171], a[180:183], a[156:159], v[168:171]\n" \
"s_cmpk_lt_u32 s3, 3\n" \
"s_cselect_b32 s16, s16, s18\n" \
M_"v[168:171], a[244:247], a[220:223], v[168:171]\n" \
"s_movk_i32 s18, 0xfff\n" \
"v_writelane_b32 v11, s20, 12\n" \
M_"v[164:167], a[176:179], a[156:159], v[164:167]\n" \
"s_and_b32 s20, s29, 0xffff\n" \
"v_writelane_b32 v11, s28, 13\n" \
M_"v[164:167], a[240:243], a[220:223], v[164:167]\n" \
R_"a[156:159], v28 offset:14336\n" \
R_"a[220:223], v29 offset:14336\n" \
"s_waitcnt lgkmcnt(15)\n" \
M_"a[0:3], a[160:163], a[128:131], a[0:3]\n" \
"s_add_u32 m0, s30, 0x9000\n" \
"s_addk_i32 s84, 0x3000\n" \
M_"a[0:3], a[224:227], a[192:195], a[0:3]\n" \
B_"v24, s[76:79], s84 offen offset:0 lds\n" \
M_"a[4:7], a[164:167], a[128:131], a[4:7]\n" \
X_"a[176:177], v36 offset:0\n" \
"v_writelane_b32 v11, s20, 14\n" \
M_"a[4:7], a[228:231], a[192:195], a[4:7]\n" \
X_"a[178:179], v36 offset:1024\n" \
"v_writelane_b32 v11, s16, 15\n" \
M_"a[12:15], a[172:175], a[128:131], a[12:15]\n" \
X_"a[240:241], v36 offset:8192\n" \
"s_waitcnt lgkmcnt(15)\n" \
M_"a[12:15], a[236:239], a[192:195], a[12:15]\n" \
"s_add_u32 s88, s41, 0x2000\n" \
"buffer_store_dwordx4 v[244:247], v20, s[80:83], s88 offen offset:3072 sc1\n" \
"s_waitcnt lgkmcnt(13)\n" \
M_"a[8:11], a[128:131], a[168:171], a[8:11]\n" \
X_"a[242:243], v36 offset:9216\n" \
"s_waitcnt lgkmcnt(12)\n" \
M_"a[8:11], a[192:195], a[232:235], a[8:11]\n" \
X_"a[180:181], v37 offset:0\n" \
M_"a[24:27], a[132:135], a[168:171], a[24:27]\n" \
X_"a[182:183], v37 offset:1024\n" \
M_"a[24:27], a[196:199], a[232:235], a[24:27]\n" \
X_"a[244:245], v37 offset:8192\n" \
M_"a[28:31], a[172:175], a[132:135], a[28:31]\n" \
"s_cmpk_lt_u32 s3, 3\n" \
"s_cselect_b32 s16, s17, s19\n" \
M_"a[28:31], a[236:239], a[196:199], a[28:31]\n" \
"s_addk_i32 s84, 0x2000\n" \
"s_nop 0\n" \
M_"a[20:23], a[164:167], a[132:135], a[20:23]\n" \
B_"v25, s[76:79], s84 offen offset:1024 lds\n" \
M_"a[20:23], a[228:231], a[196:199], a[20:23]\n" \
X_"a[246:247], v37 offset:9216\n" \
"s_and_b32 s16, s16, 0xffff\n" \
M_"a[16:19], a[160:163], a[132:135], a[16:19]\n" \
X_"a[184:185], v38 offset:0\n" \
"v_writelane_b32 v11, s16, 16\n" \
M_"a[16:19], a[224:227], a[196:199], a[16:19]\n" \
X_"a[186:187], v38 offset:1024\n" \
"s_mul_i32 s16, s5, 0x48\n" \
M_"a[32:35], a[160:163], a[136:139], a[32:35]\n" \
"s_addk_i32 s84, 0x2000\n" \
"s_nop 0\n" \
M_"a[32:35], a[224:227], a[200:203], a[32:35]\n" \
B_"v24, s[76:79], s84 offen offset:2048 lds\n" \
M_"a[36:39], a[164:167], a[136:139], a[36:39]\n" \
X_"a[248:249], v38 offset:8192\n" \
"s_add_i32 s17, s16, 0x47\n" \
M_"a[36:39], a[228:231], a[200:203], a[36:39]\n" \
".fill (L_pb + 24288 - .) / 4, 4, 0xBF800000\n" \
"L_pp6:\n" \
"s_cbranch_execz L_pret\n" \
"s_nop 0\n" \
X_"a[250:251], v38 offset:9216\n" \
M_"a[44:47], a[172:175], a[136:139], a[44:47]\n" \
X_"a[188:189], v39 offset:0\n" \
M_"a[44:47], a[236:239], a[200:203], a[44:47]\n" \
X_"a[190:191], v39 offset:1024\n" \
M_"a[40:43], a[136:139], a[168:171], a[40:43]\n" \
"s_add_u32 s89, s41, 0x3000\n" \
"buffer_store_dwordx4 v[184:187], v20, s[80:83], s89 offen offset:3072 sc1\n" \
M_"a[40:43], a[200:203], a[232:235], a[40:43]\n" \
X_"a[252:253], v39 offset:8192\n" \
M_"a[56:59], a[140:143], a[168:171], a[56:59]\n" \
X_"a[254:255], v39 offset:9216\n" \
M_"a[56:59], a[204:207], a[232:235], a[56:59]\n" \
"s_cmpk_lt_u32 s3, 3\n" \
"s_cselect_b32 s17, s17, s18\n" \
M_"a[60:63], a[172:175], a[140:143], a[60:63]\n" \
"s_mulk_i32 s17, 0x480\n" \
"s_addk_i32 s17, 0x120\n" \
M_"a[60:63], a[236:239], a[204:207], a[60:63]\n" \
"s_addk_i32 s84, 0x2000\n" \
M_"a[52:55], a[164:167], a[140:143], a[52:55]\n" \
B_"v25, s[76:79], s84 offen offset:3072 lds\n" \
M_"a[52:55], a[228:231], a[204:207], a[52:55]\n" \
"v_writelane_b32 v11, s17, 17\n" \
"s_lshl_b32 s17, s3, 5\n" \
M_"a[48:51], a[160:163], a[140:143], a[48:51]\n" \
"s_add_i32 s16, s16, s17\n" \
"s_mulk_i32 s16, 0x480\n" \
M_"a[48:51], a[224:227], a[204:207], a[48:51]\n" \
"s_mul_i32 s17, s12, 0x24000\n" \
"s_add_i32 s18, s17, 0x1b000\n" \
"s_waitcnt lgkmcnt(15)\n" \
M_"a[64:67], a[164:167], v[248:251], a[64:67]\n" \
"s_cmpk_lt_u32 s3, 3\n" \
"s_cselect_b32 s16, s16, s18\n" \
M_"a[64:67], a[228:231], v[48:51], a[64:67]\n" \
"s_xor_b32 s18, s3, 2\n" \
"v_writelane_b32 v11, s16, 18\n" \
M_"a[80:83], a[172:175], v[248:251], a[80:83]\n" \
"s_mul_i32 s16, s15, 0x2400\n" \
"s_mul_i32 s15, s15, 0x9000\n" \
M_"a[80:83], a[236:239], v[48:51], a[80:83]\n" \
"s_bitset1_b32 s16, 17\n" \
"v_writelane_b32 v11, s16, 22\n" \
M_"a[68:71], v[248:251], a[168:171], a[68:71]\n" \
"s_add_i32 s17, s17, s15\n" \
"s_mul_i32 s15, s3, 0x2400\n" \
"s_waitcnt vmcnt(11) lgkmcnt(0)\n" \
M_"a[68:71], v[48:51], a[232:235], a[68:71]\n" \
"s_barrier\n" \
"v_xor_b32_e32 v28, 0x20000, v28\n" \
"v_xor_b32_e32 v29, 0x20000, v29\n" \
M_"v[52:55], a[176:179], a[128:131], v[52:55]\n" \
"s_add_u32 m0, s40, 0x0\n" \
"s_nop 0\n" \
"s_add_u32 s85, s39, 0x8000\n" \
M_"v[52:55], a[240:243], a[192:195], v[52:55]\n" \
B_"v20, s[80:83], s85 offen sc1 lds\n" \
M_"v[56:59], a[180:183], a[128:131], v[56:59]\n" \
X_"a[160:161], v32 offset:32768\n" \
"v_writelane_b32 v11, s17, 29\n" \
M_"v[56:59], a[244:247], a[192:195], v[56:59]\n" \
X_"a[162:163], v32 offset:33280\n" \
"s_add_i32 s16, s15, 0xa400\n" \
M_"v[60:63], a[184:187], a[128:131], v[60:63]\n" \
"s_add_u32 m0, s40, 0x1000\n" \
"s_add_u32 s85, s39, 0x9000\n" \
M_"v[60:63], a[248:251], a[192:195], v[60:63]\n" \
B_"v20, s[80:83], s85 offen sc1 lds\n" \
M_"v[64:67], a[188:191], a[128:131], v[64:67]\n" \
X_"a[224:225], v32 offset:36864\n" \
"s_mov_b32 s17, 0x8000\n" \
M_"v[64:67], a[252:255], a[192:195], v[64:67]\n" \
R_"a[128:131], v28 offset:0\n" \
M_"v[80:83], a[188:191], a[132:135], v[80:83]\n" \
R_"a[192:195], v29 offset:0\n" \
M_"v[80:83], a[252:255], a[196:199], v[80:83]\n" \
"s_add_u32 s88, s41, 0x3000\n" \
"buffer_store_dwordx4 v[188:191], v20, s[80:83], s88 offen offset:2048 sc1\n" \
M_"v[76:79], a[184:187], a[132:135], v[76:79]\n" \
X_"a[226:227], v32 offset:37376\n" \
M_"v[76:79], a[248:251], a[196:199], v[76:79]\n" \
X_"a[164:165], v33 offset:32768\n" \
M_"v[72:75], a[180:183], a[132:135], v[72:75]\n" \
"s_add_u32 m0, s40, 0x3000\n" \
"s_movk_i32 s84, 0x200\n" \
M_"v[72:75], a[244:247], a[196:199], v[72:75]\n" \
B_"v21, s[76:79], s84 offen offset:0 lds\n" \
M_"v[68:71], a[176:179], a[132:135], v[68:71]\n" \
X_"a[166:167], v33 offset:33280\n" \
M_"v[68:71], a[240:243], a[196:199], v[68:71]\n" \
R_"a[132:135], v28 offset:2048\n" \
M_"v[84:87], a[176:179], a[136:139], v[84:87]\n" \
R_"a[196:199], v29 offset:2048\n" \
M_"v[84:87], a[240:243], a[200:203], v[84:87]\n" \
"s_add_u32 m0, s31, 0x0\n" \
"s_mov_b32 s84, 0x200000\n" \
M_"v[88:91], a[180:183], a[136:139], v[88:91]\n" \
B_"v26, s[68:71], s84 offen offset:0 lds\n" \
M_"v[88:91], a[244:247], a[200:203], v[88:91]\n" \
X_"a[228:229], v33 offset:36864\n" \
M_"v[92:95], a[184:187], a[136:139], v[92:95]\n" \
X_"a[230:231], v33 offset:37376\n" \
M_"v[92:95], a[248:251], a[200:203], v[92:95]\n" \
"s_addk_i32 s84, 0x7c00\n" \
M_"v[96:99], a[188:191], a[136:139], v[96:99]\n" \
B_"v26, s[68:71], s84 offen offset:1024 lds\n" \
M_"v[96:99], a[252:255], a[200:203], v[96:99]\n" \
R_"a[136:139], v28 offset:4096\n" \
M_"v[112:115], a[188:191], a[140:143], v[112:115]\n" \
R_"a[200:203], v29 offset:4096\n" \
M_"v[112:115], a[252:255], a[204:207], v[112:115]\n" \
"s_addk_i32 s84, 0x7c00\n" \
M_"v[108:111], a[184:187], a[140:143], v[108:111]\n" \
B_"v27, s[68:71], s84 offen offset:2048 lds\n" \
M_"v[108:111], a[248:251], a[204:207], v[108:111]\n" \
X_"a[172:173], v35 offset:32768\n" \
M_"v[104:107], a[180:183], a[140:143], v[104:107]\n" \
X_"a[174:175], v35 offset:33280\n" \
M_"v[104:107], a[244:247], a[204:207], v[104:107]\n" \
X_"a[236:237], v35 offset:36864\n" \
M_"v[100:103], a[176:179], a[140:143], v[100:103]\n" \
"s_cmpk_lt_u32 s3, 3\n" \
"s_cselect_b32 s16, s16, s17\n" \
M_"v[100:103], a[240:243], a[204:207], v[100:103]\n" \
R_"a[140:143], v28 offset:6144\n" \
"s_mov_b32 s17, 0x1b000\n" \
M_"v[116:119], a[176:179], a[144:147], v[116:119]\n" \
R_"a[204:207], v29 offset:6144\n" \
M_"v[116:119], a[240:243], a[208:211], v[116:119]\n" \
"s_addk_i32 s84, 0x7c00\n" \
M_"v[120:123], a[180:183], a[144:147], v[120:123]\n" \
B_"v27, s[68:71], s84 offen offset:3072 lds\n" \
M_"v[120:123], a[244:247], a[208:211], v[120:123]\n" \
"s_add_u32 m0, s30, 0x20000\n" \
"s_movk_i32 s84, 0x280\n" \
M_"v[124:127], a[184:187], a[144:147], v[124:127]\n" \
B_"v24, s[76:79], s84 offen offset:0 lds\n" \
M_"v[124:127], a[248:251], a[208:211], v[124:127]\n" \
X_"a[238:239], v35 offset:37376\n" \
M_"v[128:131], a[188:191], a[144:147], v[128:131]\n" \
"s_add_u32 s89, s42, 0x0\n" \
"buffer_store_dwordx4 v[216:219], v20, s[80:83], s89 offen offset:0 sc1\n" \
M_"v[128:131], a[252:255], a[208:211], v[128:131]\n" \
R_"a[144:147], v28 offset:8192\n" \
M_"v[144:147], a[188:191], a[148:151], v[144:147]\n" \
R_"a[208:211], v29 offset:8192\n" \
M_"v[144:147], a[252:255], a[212:215], v[144:147]\n" \
X_"a[168:169], v34 offset:32768\n" \
M_"v[140:143], a[184:187], a[148:151], v[140:143]\n" \
X_"a[170:171], v34 offset:33280\n" \
M_"v[140:143], a[248:251], a[212:215], v[140:143]\n" \
"s_movk_i32 s84, 0x2280\n" \
M_"v[136:139], a[180:183], a[148:151], v[136:139]\n" \
B_"v25, s[76:79], s84 offen offset:1024 lds\n" \
M_"v[136:139], a[244:247], a[212:215], v[136:139]\n" \
X_"a[232:233], v34 offset:36864\n" \
M_"v[132:135], a[176:179], a[148:151], v[132:135]\n" \
X_"a[234:235], v34 offset:37376\n" \
M_"v[132:135], a[240:243], a[212:215], v[132:135]\n" \
R_"a[148:151], v28 offset:10240\n" \
M_"v[148:151], a[176:179], a[152:155], v[148:151]\n" \
R_"a[212:215], v29 offset:10240\n" \
M_"v[148:151], a[240:243], a[216:219], v[148:151]\n" \
"s_movk_i32 s84, 0x4280\n" \
M_"v[152:155], a[180:183], a[152:155], v[152:155]\n" \
B_"v24, s[76:79], s84 offen offset:2048 lds\n" \
M_"v[152:155], a[244:247], a[216:219], v[152:155]\n" \
"s_movk_i32 s84, 0x6280\n" \
M_"v[156:159], a[184:187], a[152:155], v[156:159]\n" \
B_"v25, s[76:79], s84 offen offset:3072 lds\n" \
M_"v[156:159], a[248:251], a[216:219], v[156:159]\n" \
R_"v[248:251], v22 offset:32768\n" \
M_"v[160:163], a[188:191], a[152:155], v[160:163]\n" \
R_"v[48:51], v23 offset:32768\n" \
M_"v[160:163], a[252:255], a[216:219], v[160:163]\n" \
R_"a[152:155], v28 offset:12288\n" \
M_"v[176:179], a[188:191], a[156:159], v[176:179]\n" \
R_"a[216:219], v29 offset:12288\n" \
M_"v[176:179], a[252:255], a[220:223], v[176:179]\n" \
"s_cmp_lg_u32 s18, 0\n" \
"s_cselect_b32 s17, s16, s17\n" \
M_"v[172:175], a[184:187], a[156:159], v[172:175]\n" \
"v_writelane_b32 v11, s16, 23\n" \
"s_or_b32 s16, s15, 0x10000\n" \
M_"v[172:175], a[248:251], a[220:223], v[172:175]\n" \
"v_writelane_b32 v11, s17, 24\n" \
"s_add_i32 s17, s15, 0x19000\n" \
M_"v[168:171], a[180:183], a[156:159], v[168:171]\n" \
"v_writelane_b32 v11, s16, 25\n" \
"s_mov_b32 s16, 0\n" \
M_"v[168:171], a[244:247], a[220:223], v[168:171]\n" \
"s_cmpk_lt_u32 s3, 3\n" \
"s_cselect_b32 s17, s17, s16\n" \
M_"v[164:167], a[176:179], a[156:159], v[164:167]\n" \
"s_bitset1_b32 s15, 17\n" \
"v_writelane_b32 v11, s17, 26\n" \
M_"v[164:167], a[240:243], a[220:223], v[164:167]\n" \
R_"a[156:159], v28 offset:14336\n" \
R_"a[220:223], v29 offset:14336\n" \
"s_waitcnt lgkmcnt(15)\n" \
M_"a[0:3], a[160:163], a[128:131], a[0:3]\n" \
"s_add_u32 m0, s30, 0x21000\n" \
"s_addk_i32 s84, 0x3000\n" \
M_"a[0:3], a[224:227], a[192:195], a[0:3]\n" \
B_"v24, s[76:79], s84 offen offset:0 lds\n" \
M_"a[4:7], a[164:167], a[128:131], a[4:7]\n" \
X_"a[176:177], v36 offset:32768\n" \
"s_movk_i32 s17, 0x2400\n" \
M_"a[4:7], a[228:231], a[192:195], a[4:7]\n" \
X_"a[178:179], v36 offset:33792\n" \
M_"a[12:15], a[172:175], a[128:131], a[12:15]\n" \
X_"a[240:241], v36 offset:40960\n" \
"s_waitcnt lgkmcnt(15)\n" \
M_"a[12:15], a[236:239], a[192:195], a[12:15]\n" \
"s_add_u32 s88, s42, 0x1000\n" \
"buffer_store_dwordx4 v[220:223], v20, s[80:83], s88 offen offset:0 sc1\n" \
"s_waitcnt lgkmcnt(13)\n" \
M_"a[8:11], a[128:131], a[168:171], a[8:11]\n" \
X_"a[242:243], v36 offset:41984\n" \
"s_waitcnt lgkmcnt(12)\n" \
M_"a[8:11], a[192:195], a[232:235], a[8:11]\n" \
X_"a[180:181], v37 offset:32768\n" \
M_"a[24:27], a[132:135], a[168:171], a[24:27]\n" \
X_"a[182:183], v37 offset:33792\n" \
M_"a[24:27], a[196:199], a[232:235], a[24:27]\n" \
X_"a[244:245], v37 offset:40960\n" \
M_"a[28:31], a[172:175], a[132:135], a[28:31]\n" \
"s_cmpk_lt_u32 s3, 3\n" \
"s_cselect_b32 s15, s15, s17\n" \
M_"a[28:31], a[236:239], a[196:199], a[28:31]\n" \
"s_addk_i32 s84, 0x2000\n" \
M_"a[20:23], a[164:167], a[132:135], a[20:23]\n" \
B_"v25, s[76:79], s84 offen offset:1024 lds\n" \
M_"a[20:23], a[228:231], a[196:199], a[20:23]\n" \
X_"a[246:247], v37 offset:41984\n" \
"v_writelane_b32 v11, s15, 27\n" \
M_"a[16:19], a[160:163], a[132:135], a[16:19]\n" \
X_"a[184:185], v38 offset:32768\n" \
"s_lshl_b32 s15, s3, 10\n" \
M_"a[16:19], a[224:227], a[196:199], a[16:19]\n" \
X_"a[186:187], v38 offset:33792\n" \
"s_addk_i32 s15, 0x4800\n" \
M_"a[32:35], a[160:163], a[136:139], a[32:35]\n" \
"s_addk_i32 s84, 0x2000\n" \
M_"a[32:35], a[224:227], a[200:203], a[32:35]\n" \
B_"v24, s[76:79], s84 offen offset:2048 lds\n" \
M_"a[36:39], a[164:167], a[136:139], a[36:39]\n" \
X_"a[248:249], v38 offset:40960\n" \
"v_writelane_b32 v11, s15, 28\n" \
M_"a[36:39], a[228:231], a[200:203], a[36:39]\n" \
X_"a[250:251], v38 offset:41984\n" \
"s_add_i32 s15, s14, 1\n" \
M_"a[44:47], a[172:175], a[136:139], a[44:47]\n" \
X_"a[188:189], v39 offset:32768\n" \
"s_and_b32 s15, s15, 3\n" \
M_"a[44:47], a[236:239], a[200:203], a[44:47]\n" \
X_"a[190:191], v39 offset:33792\n" \
"s_or_b32 s15, s15, s4\n" \
M_"a[40:43], a[136:139], a[168:171], a[40:43]\n" \
"s_add_u32 s89, s42, 0x0\n" \
"buffer_store_dwordx4 v[192:195], v20, s[80:83], s89 offen offset:2048 sc1\n" \
M_"a[40:43], a[200:203], a[232:235], a[40:43]\n" \
X_"a[252:253], v39 offset:40960\n" \
"s_lshl_b32 s17, s15, 2\n" \
M_"a[56:59], a[140:143], a[168:171], a[56:59]\n" \
X_"a[254:255], v39 offset:41984\n" \
"s_or_b32 s17, s17, s3\n" \
M_"a[56:59], a[204:207], a[232:235], a[56:59]\n" \
"s_mul_i32 s17, s17, 0x9000\n" \
"v_writelane_b32 v11, s17, 30\n" \
M_"a[60:63], a[172:175], a[140:143], a[60:63]\n" \
"s_lshl_b32 s17, s12, 2\n" \
"s_or_b32 s17, s17, s3\n" \
M_"a[60:63], a[236:239], a[204:207], a[60:63]\n" \
"s_addk_i32 s84, 0x2000\n" \
M_"a[52:55], a[164:167], a[140:143], a[52:55]\n" \
B_"v25, s[76:79], s84 offen offset:3072 lds\n" \
M_"a[52:55], a[228:231], a[204:207], a[52:55]\n" \
"s_add_i32 s14, s14, -1\n" \
"s_xor_b32 s17, s17, 8\n" \
M_"a[48:51], a[160:163], a[140:143], a[48:51]\n" \
"s_and_b32 s14, s14, 3\n" \
"s_mul_i32 s17, s17, 0x9000\n" \
M_"a[48:51], a[224:227], a[204:207], a[48:51]\n" \
"s_or_b32 s14, s14, s4\n" \
"v_writelane_b32 v11, s17, 31\n" \
"s_waitcnt lgkmcnt(15)\n" \
M_"a[64:67], a[164:167], v[248:251], a[64:67]\n" \
"s_lshl_b32 s17, s14, 2\n" \
"s_or_b32 s17, s17, s3\n" \
M_"a[64:67], a[228:231], v[48:51], a[64:67]\n" \
"s_mul_i32 s17, s17, 0x9000\n" \
"v_writelane_b32 v11, s17, 32\n" \
M_"a[80:83], a[172:175], v[248:251], a[80:83]\n" \
"s_or_b32 s17, s8, s13\n" \
"s_mul_i32 s17, s17, 0x24800\n" \
M_"a[80:83], a[236:239], v[48:51], a[80:83]\n" \
"s_lshl_b32 s15, s15, 12\n" \
"s_add_i32 s18, s17, 0x3004800\n" \
M_"a[68:71], v[248:251], a[168:171], a[68:71]\n" \
"s_lshl_b32 s12, s12, 12\n" \
"s_add_i32 s19, s18, s15\n" \
"s_waitcnt vmcnt(11) lgkmcnt(0)\n" \
M_"a[68:71], v[48:51], a[232:235], a[68:71]\n" \
"s_barrier\n" \
M_"v[52:55], a[176:179], a[128:131], v[52:55]\n" \
"s_add_u32 m0, s40, 0x8000\n" \
"s_add_u32 s85, s39, 0xa000\n" \
M_"v[52:55], a[240:243], a[192:195], v[52:55]\n" \
B_"v20, s[80:83], s85 offen sc1 lds\n" \
M_"v[56:59], a[180:183], a[128:131], v[56:59]\n" \
X_"a[160:161], v32 offset:0\n" \
"v_accvgpr_read_b32 v224, a120\n" \
M_"v[56:59], a[244:247], a[192:195], v[56:59]\n" \
X_"a[162:163], v32 offset:512\n" \
"v_accvgpr_read_b32 v225, a121\n" \
M_"v[60:63], a[184:187], a[128:131], v[60:63]\n" \
"s_add_u32 m0, s40, 0x9000\n" \
"s_add_u32 s85, s39, 0xb000\n" \
M_"v[60:63], a[248:251], a[192:195], v[60:63]\n" \
B_"v20, s[80:83], s85 offen sc1 lds\n" \
M_"v[64:67], a[188:191], a[128:131], v[64:67]\n" \
X_"a[224:225], v32 offset:4096\n" \
"v_accvgpr_read_b32 v226, a122\n" \
M_"v[64:67], a[252:255], a[192:195], v[64:67]\n" \
R_"a[128:131], v28 offset:32768\n" \
"v_accvgpr_read_b32 v227, a123\n" \
M_"v[80:83], a[188:191], a[132:135], v[80:83]\n" \
R_"a[192:195], v29 offset:32768\n" \
"v_accvgpr_read_b32 v180, a124\n" \
M_"v[80:83], a[252:255], a[196:199], v[80:83]\n" \
"s_add_u32 s88, s42, 0x0\n" \
"buffer_store_dwordx4 v[196:199], v20, s[80:83], s88 offen offset:3072 sc1\n" \
M_"v[76:79], a[184:187], a[132:135], v[76:79]\n" \
X_"a[226:227], v32 offset:4608\n" \
"v_accvgpr_read_b32 v181, a125\n" \
M_"v[76:79], a[248:251], a[196:199], v[76:79]\n" \
X_"a[164:165], v33 offset:0\n" \
"v_accvgpr_read_b32 v182, a126\n" \
M_"v[72:75], a[180:183], a[132:135], v[72:75]\n" \
"s_add_u32 m0, s40, 0xb000\n" \
"s_movk_i32 s84, 0x280\n" \
M_"v[72:75], a[244:247], a[196:199], v[72:75]\n" \
B_"v21, s[76:79], s84 offen offset:0 lds\n" \
M_"v[68:71], a[176:179], a[132:135], v[68:71]\n" \
X_"a[166:167], v33 offset:512\n" \
"v_accvgpr_read_b32 v183, a127\n" \
M_"v[68:71], a[240:243], a[196:199], v[68:71]\n" \
R_"a[132:135], v28 offset:34816\n" \
C_"v224, v224, v225\n" \
M_"v[84:87], a[176:179], a[136:139], v[84:87]\n" \
R_"a[196:199], v29 offset:34816\n" \
C_"v225, v226, v227\n" \
M_"v[84:87], a[240:243], a[200:203], v[84:87]\n" \
"s_add_u32 m0, s31, 0x8000\n" \
"s_mov_b32 s84, 0x280000\n" \
M_"v[88:91], a[180:183], a[136:139], v[88:91]\n" \
B_"v26, s[68:71], s84 offen offset:0 lds\n" \
M_"v[88:91], a[244:247], a[200:203], v[88:91]\n" \
X_"a[228:229], v33 offset:4096\n" \
C_"v226, v180, v181\n" \
M_"v[92:95], a[184:187], a[136:139], v[92:95]\n" \
X_"a[230:231], v33 offset:4608\n" \
C_"v227, v182, v183\n" \
M_"v[92:95], a[248:251], a[200:203], v[92:95]\n" \
"s_addk_i32 s84, 0x7c00\n" \
M_"v[96:99], a[188:191], a[136:139], v[96:99]\n" \
B_"v26, s[68:71], s84 offen offset:1024 lds\n" \
M_"v[96:99], a[252:255], a[200:203], v[96:99]\n" \
R_"a[136:139], v28 offset:36864\n" \
"v_accvgpr_read_b32 v220, a112\n" \
M_"v[112:115], a[188:191], a[140:143], v[112:115]\n" \
R_"a[200:203], v29 offset:36864\n" \
"v_accvgpr_read_b32 v221, a113\n" \
M_"v[112:115], a[252:255], a[204:207], v[112:115]\n" \
"s_addk_i32 s84, 0x7c00\n" \
M_"v[108:111], a[184:187], a[140:143], v[108:111]\n" \
B_"v27, s[68:71], s84 offen offset:2048 lds\n" \
M_"v[108:111], a[248:251], a[204:207], v[108:111]\n" \
X_"a[172:173], v35 offset:0\n" \
"v_accvgpr_read_b32 v222, a114\n" \
M_"v[104:107], a[180:183], a[140:143], v[104:107]\n" \
X_"a[174:175], v35 offset:512\n" \
"v_accvgpr_read_b32 v223, a115\n" \
M_"v[104:107], a[244:247], a[204:207], v[104:107]\n" \
X_"a[236:237], v35 offset:4096\n" \
"v_accvgpr_read_b32 v180, a116\n" \
M_"v[100:103], a[176:179], a[140:143], v[100:103]\n" \
"v_accvgpr_read_b32 v181, a117\n" \
"v_accvgpr_read_b32 v182, a118\n" \
M_"v[100:103], a[240:243], a[204:207], v[100:103]\n" \
R_"a[140:143], v28 offset:38912\n" \
"v_accvgpr_read_b32 v183, a119\n" \
M_"v[116:119], a[176:179], a[144:147], v[116:119]\n" \
R_"a[204:207], v29 offset:38912\n" \
C_"v220, v220, v221\n" \
M_"v[116:119], a[240:243], a[208:211], v[116:119]\n" \
"s_addk_i32 s84, 0x7c00\n" \
M_"v[120:123], a[180:183], a[144:147], v[120:123]\n" \
B_"v27, s[68:71], s84 offen offset:3072 lds\n" \
M_"v[120:123], a[244:247], a[208:211], v[120:123]\n" \
"s_add_u32 m0, s30, 0x0\n" \
"s_movk_i32 s84, 0x300\n" \
"s_nop 0\n" \
M_"v[124:127], a[184:187], a[144:147], v[124:127]\n" \
B_"v24, s[76:79], s84 offen offset:0 lds\n" \
M_"v[124:127], a[248:251], a[208:211], v[124:127]\n" \
X_"a[238:239], v35 offset:4608\n" \
C_"v221, v222, v223\n" \
M_"v[128:131], a[188:191], a[144:147], v[128:131]\n" \
"s_add_u32 s89, s42, 0x1000\n" \
"buffer_store_dwordx4 a[84:87], v20, s[80:83], s89 offen offset:3072 sc1\n" \
M_"v[128:131], a[252:255], a[208:211], v[128:131]\n" \
R_"a[144:147], v28 offset:40960\n" \
C_"v222, v180, v181\n" \
M_"v[144:147], a[188:191], a[148:151], v[144:147]\n" \
R_"a[208:211], v29 offset:40960\n" \
C_"v223, v182, v183\n" \
M_"v[144:147], a[252:255], a[212:215], v[144:147]\n" \
X_"a[168:169], v34 offset:0\n" \
"v_accvgpr_read_b32 v216, a104\n" \
M_"v[140:143], a[184:187], a[148:151], v[140:143]\n" \
X_"a[170:171], v34 offset:512\n" \
"v_accvgpr_read_b32 v217, a105\n" \
M_"v[140:143], a[248:251], a[212:215], v[140:143]\n" \
"s_movk_i32 s84, 0x2300\n" \
"s_nop 0\n" \
M_"v[136:139], a[180:183], a[148:151], v[136:139]\n" \
B_"v25, s[76:79], s84 offen offset:1024 lds\n" \
M_"v[136:139], a[244:247], a[212:215], v[136:139]\n" \
X_"a[232:233], v34 offset:4096\n" \
"v_accvgpr_read_b32 v218, a106\n" \
M_"v[132:135], a[176:179], a[148:151], v[132:135]\n" \
X_"a[234:235], v34 offset:4608\n" \
"v_accvgpr_read_b32 v219, a107\n" \
M_"v[132:135], a[240:243], a[212:215], v[132:135]\n" \
R_"a[148:151], v28 offset:43008\n" \
"v_accvgpr_read_b32 v180, a108\n" \
M_"v[148:151], a[176:179], a[152:155], v[148:151]\n" \
R_"a[212:215], v29 offset:43008\n" \
"v_accvgpr_read_b32 v181, a109\n" \
M_"v[148:151], a[240:243], a[216:219], v[148:151]\n" \
"s_movk_i32 s84, 0x4300\n" \
M_"v[152:155], a[180:183], a[152:155], v[152:155]\n" \
B_"v24, s[76:79], s84 offen offset:2048 lds\n" \
M_"v[152:155], a[244:247], a[216:219], v[152:155]\n" \
"s_movk_i32 s84, 0x6300\n" \
M_"v[156:159], a[184:187], a[152:155], v[156:159]\n" \
B_"v25, s[76:79], s84 offen offset:3072 lds\n" \
M_"v[156:159], a[248:251], a[216:219], v[156:159]\n" \
R_"v[248:251], v22 offset:0\n" \
"v_accvgpr_read_b32 v182, a110\n" \
M_"v[160:163], a[188:191], a[152:155], v[160:163]\n" \
R_"v[48:51], v23 offset:0\n" \
"v_accvgpr_read_b32 v183, a111\n" \
M_"v[160:163], a[252:255], a[216:219], v[160:163]\n" \
R_"a[152:155], v28 offset:45056\n" \
C_"v216, v216, v217\n" \
M_"v[176:179], a[188:191], a[156:159], v[176:179]\n" \
R_"a[216:219], v29 offset:45056\n" \
C_"v217, v218, v219\n" \
M_"v[176:179], a[252:255], a[220:223], v[176:179]\n" \
C_"v218, v180, v181\n" \
C_"v219, v182, v183\n" \
M_"v[172:175], a[184:187], a[156:159], v[172:175]\n" \
"v_accvgpr_read_b32 v192, a96\n" \
"v_accvgpr_read_b32 v193, a97\n" \
M_"v[172:175], a[248:251], a[220:223], v[172:175]\n" \
"v_accvgpr_read_b32 v194, a98\n" \
"v_accvgpr_read_b32 v195, a99\n" \
M_"v[168:171], a[180:183], a[156:159], v[168:171]\n" \
"v_accvgpr_read_b32 v180, a100\n" \
"v_accvgpr_read_b32 v181, a101\n" \
M_"v[168:171], a[244:247], a[220:223], v[168:171]\n" \
"v_accvgpr_read_b32 v182, a102\n" \
"v_accvgpr_read_b32 v183, a103\n" \
M_"v[164:167], a[176:179], a[156:159], v[164:167]\n" \
C_"v192, v192, v193\n" \
C_"v193, v194, v195\n" \
M_"v[164:167], a[240:243], a[220:223], v[164:167]\n" \
R_"a[156:159], v28 offset:47104\n" \
R_"a[220:223], v29 offset:47104\n" \
"s_waitcnt lgkmcnt(15)\n" \
M_"a[0:3], a[160:163], a[128:131], a[0:3]\n" \
"s_add_u32 m0, s30, 0x1000\n" \
"s_addk_i32 s84, 0x3000\n" \
M_"a[0:3], a[224:227], a[192:195], a[0:3]\n" \
B_"v24, s[76:79], s84 offen offset:0 lds\n" \
M_"a[4:7], a[164:167], a[128:131], a[4:7]\n" \
X_"a[176:177], v36 offset:0\n" \
C_"v194, v180, v181\n" \
M_"a[4:7], a[228:231], a[192:195], a[4:7]\n" \
X_"a[178:179], v36 offset:1024\n" \
C_"v195, v182, v183\n" \
M_"a[12:15], a[172:175], a[128:131], a[12:15]\n" \
X_"a[240:241], v36 offset:8192\n" \
"v_accvgpr_read_b32 v188, a88\n" \
"s_waitcnt lgkmcnt(15)\n" \
M_"a[12:15], a[236:239], a[192:195], a[12:15]\n" \
"s_add_u32 s88, s42, 0x1000\n" \
"buffer_store_dwordx4 v[252:255], v20, s[80:83], s88 offen offset:2048 sc1\n" \
"s_waitcnt lgkmcnt(13)\n" \
M_"a[8:11], a[128:131], a[168:171], a[8:11]\n" \
X_"a[242:243], v36 offset:9216\n" \
"v_accvgpr_read_b32 v189, a89\n" \
"s_waitcnt lgkmcnt(12)\n" \
"s_nop 0\n" \
M_"a[8:11], a[192:195], a[232:235], a[8:11]\n" \
X_"a[180:181], v37 offset:0\n" \
"v_accvgpr_read_b32 v190, a90\n" \
M_"a[24:27], a[132:135], a[168:171], a[24:27]\n" \
X_"a[182:183], v37 offset:1024\n" \
"v_accvgpr_read_b32 v191, a91\n" \
M_"a[24:27], a[196:199], a[232:235], a[24:27]\n" \
X_"a[244:245], v37 offset:8192\n" \
"v_accvgpr_read_b32 v180, a92\n" \
M_"a[28:31], a[172:175], a[132:135], a[28:31]\n" \
"v_accvgpr_read_b32 v181, a93\n" \
"v_accvgpr_read_b32 v182, a94\n" \
M_"a[28:31], a[236:239], a[196:199], a[28:31]\n" \
"s_addk_i32 s84, 0x2000\n" \
"s_nop 0\n" \
M_"a[20:23], a[164:167], a[132:135], a[20:23]\n" \
B_"v25, s[76:79], s84 offen offset:1024 lds\n" \
M_"a[20:23], a[228:231], a[196:199], a[20:23]\n" \
X_"a[246:247], v37 offset:9216\n" \
"v_accvgpr_read_b32 v183, a95\n" \
M_"a[16:19], a[160:163], a[132:135], a[16:19]\n" \
X_"a[184:185], v38 offset:0\n" \
C_"v188, v188, v189\n" \
M_"a[16:19], a[224:227], a[196:199], a[16:19]\n" \
X_"a[186:187], v38 offset:1024\n" \
C_"v189, v190, v191\n" \
M_"a[32:35], a[160:163], a[136:139], a[32:35]\n" \
"s_addk_i32 s84, 0x2000\n" \
"s_nop 0\n" \
M_"a[32:35], a[224:227], a[200:203], a[32:35]\n" \
B_"v24, s[76:79], s84 offen offset:2048 lds\n" \
M_"a[36:39], a[164:167], a[136:139], a[36:39]\n" \
X_"a[248:249], v38 offset:8192\n" \
C_"v190, v180, v181\n" \
M_"a[36:39], a[228:231], a[200:203], a[36:39]\n" \
X_"a[250:251], v38 offset:9216\n" \
C_"v191, v182, v183\n" \
M_"a[44:47], a[172:175], a[136:139], a[44:47]\n" \
X_"a[188:189], v39 offset:0\n" \
"v_accvgpr_read_b32 v184, a72\n" \
M_"a[44:47], a[236:239], a[200:203], a[44:47]\n" \
X_"a[190:191], v39 offset:1024\n" \
"v_accvgpr_read_b32 v185, a73\n" \
M_"a[40:43], a[136:139], a[168:171], a[40:43]\n" \
"s_add_u32 s89, s42, 0x2000\n" \
"buffer_store_dwordx4 v[200:203], v20, s[80:83], s89 offen offset:2048 sc1\n" \
M_"a[40:43], a[200:203], a[232:235], a[40:43]\n" \
X_"a[252:253], v39 offset:8192\n" \
"v_accvgpr_read_b32 v186, a74\n" \
".fill (L_pb + 28384 - .) / 4, 4, 0xBF800000\n" \
"L_pp7:\n" \
"s_cbranch_execz L_pret\n" \
M_"a[56:59], a[140:143], a[168:171], a[56:59]\n" \
X_"a[254:255], v39 offset:9216\n" \
"v_accvgpr_read_b32 v187, a75\n" \
M_"a[56:59], a[204:207], a[232:235], a[56:59]\n" \
"v_accvgpr_read_b32 v180, a76\n" \
"v_accvgpr_read_b32 v181, a77\n" \
M_"a[60:63], a[172:175], a[140:143], a[60:63]\n" \
"v_accvgpr_read_b32 v182, a78\n" \
"v_accvgpr_read_b32 v183, a79\n" \
M_"a[60:63], a[236:239], a[204:207], a[60:63]\n" \
"s_addk_i32 s84, 0x2000\n" \
M_"a[52:55], a[164:167], a[140:143], a[52:55]\n" \
B_"v25, s[76:79], s84 offen offset:3072 lds\n" \
M_"a[52:55], a[228:231], a[204:207], a[52:55]\n" \
C_"v184, v184, v185\n" \
C_"v185, v186, v187\n" \
M_"a[48:51], a[160:163], a[140:143], a[48:51]\n" \
C_"v186, v180, v181\n" \
C_"v187, v182, v183\n" \
M_"a[48:51], a[224:227], a[204:207], a[48:51]\n" \
"v_writelane_b32 v11, s19, 33\n" \
"s_xor_b32 s12, s12, 0x2000\n" \
"s_waitcnt lgkmcnt(15)\n" \
M_"a[64:67], a[164:167], v[248:251], a[64:67]\n" \
"s_lshl_b32 s14, s14, 12\n" \
"s_sub_i32 s13, s8, s13\n" \
M_"a[64:67], a[228:231], v[48:51], a[64:67]\n" \
"s_add_i32 s19, s18, s12\n" \
"v_writelane_b32 v11, s19, 34\n" \
M_"a[80:83], a[172:175], v[248:251], a[80:83]\n" \
"s_add_i32 s18, s18, s14\n" \
"s_mul_i32 s13, s13, 0x24800\n" \
M_"a[80:83], a[236:239], v[48:51], a[80:83]\n" \
"v_writelane_b32 v11, s18, 35\n" \
"s_add_i32 s18, s13, 0x4120800\n" \
M_"a[68:71], v[248:251], a[168:171], a[68:71]\n" \
"s_add_i32 s15, s18, s15\n" \
"v_writelane_b32 v11, s15, 36\n" \
"s_waitcnt vmcnt(11) lgkmcnt(0)\n" \
M_"a[68:71], v[48:51], a[232:235], a[68:71]\n" \
"s_barrier\n" \
"v_mov_b32_e32 v45, s62\n" \
"s_mov_b64 exec, 1\n" \
"buffer_store_dword v45, off, s[80:83], s37 offset:0 sc1\n" \
"s_mov_b64 exec, -1\n" \
"v_xor_b32_e32 v28, 0x20000, v28\n" \
"v_xor_b32_e32 v29, 0x20000, v29\n" \
M_"v[52:55], a[176:179], a[128:131], v[52:55]\n" \
"s_add_u32 m0, s40, 0x0\n" \
"s_add_u32 s85, s39, 0xc000\n" \
M_"v[52:55], a[240:243], a[192:195], v[52:55]\n" \
B_"v20, s[80:83], s85 offen sc1 lds\n" \
M_"v[56:59], a[180:183], a[128:131], v[56:59]\n" \
X_"a[160:161], v32 offset:32768\n" \
"s_add_i32 s12, s18, s12\n" \
M_"v[56:59], a[244:247], a[192:195], v[56:59]\n" \
X_"a[162:163], v32 offset:33280\n" \
"v_writelane_b32 v11, s12, 37\n" \
M_"v[60:63], a[184:187], a[128:131], v[60:63]\n" \
"s_add_u32 m0, s40, 0x1000\n" \
"s_add_u32 s85, s39, 0xd000\n" \
M_"v[60:63], a[248:251], a[192:195], v[60:63]\n" \
B_"v20, s[80:83], s85 offen sc1 lds\n" \
M_"v[64:67], a[188:191], a[128:131], v[64:67]\n" \
X_"a[224:225], v32 offset:36864\n" \
"s_add_i32 s18, s18, s14\n" \
M_"v[64:67], a[252:255], a[192:195], v[64:67]\n" \
R_"a[128:131], v28 offset:0\n" \
"s_or_b32 s12, s17, 0x3000000\n" \
M_"v[80:83], a[188:191], a[132:135], v[80:83]\n" \
R_"a[192:195], v29 offset:0\n" \
"s_lshl_b32 s14, s5, 11\n" \
M_"v[80:83], a[252:255], a[196:199], v[80:83]\n" \
"s_add_u32 m0, s40, 0x3000\n" \
"s_movk_i32 s84, 0x300\n" \
M_"v[76:79], a[184:187], a[132:135], v[76:79]\n" \
B_"v21, s[76:79], s84 offen offset:0 lds\n" \
M_"v[76:79], a[248:251], a[196:199], v[76:79]\n" \
X_"a[226:227], v32 offset:37376\n" \
"v_writelane_b32 v11, s18, 38\n" \
M_"v[72:75], a[180:183], a[132:135], v[72:75]\n" \
X_"a[164:165], v33 offset:32768\n" \
"s_add_i32 s15, s12, s14\n" \
M_"v[72:75], a[244:247], a[196:199], v[72:75]\n" \
"v_writelane_b32 v11, s15, 39\n" \
"s_add_i32 s15, s13, 0x411c000\n" \
M_"v[68:71], a[176:179], a[132:135], v[68:71]\n" \
X_"a[166:167], v33 offset:33280\n" \
"s_add_i32 s18, s15, s14\n" \
M_"v[68:71], a[240:243], a[196:199], v[68:71]\n" \
R_"a[132:135], v28 offset:2048\n" \
"v_writelane_b32 v11, s18, 40\n" \
M_"v[84:87], a[176:179], a[136:139], v[84:87]\n" \
R_"a[196:199], v29 offset:2048\n" \
"s_add_i32 s17, s17, 0x3004000\n" \
M_"v[84:87], a[240:243], a[200:203], v[84:87]\n" \
"s_add_u32 m0, s31, 0x0\n" \
"s_nop 0\n" \
"s_mov_b32 s84, 0x300000\n" \
M_"v[88:91], a[180:183], a[136:139], v[88:91]\n" \
B_"v26, s[68:71], s84 offen offset:0 lds\n" \
M_"v[88:91], a[244:247], a[200:203], v[88:91]\n" \
"s_add_u32 s88, s42, 0x2000\n" \
"buffer_store_dwordx4 v[204:207], v20, s[80:83], s88 offen offset:3072 sc1\n" \
M_"v[92:95], a[184:187], a[136:139], v[92:95]\n" \
X_"a[228:229], v33 offset:36864\n" \
"v_writelane_b32 v11, s17, 41\n" \
M_"v[92:95], a[248:251], a[200:203], v[92:95]\n" \
X_"a[230:231], v33 offset:37376\n" \
"s_add_i32 s13, s13, 0x4120000\n" \
M_"v[96:99], a[188:191], a[136:139], v[96:99]\n" \
"v_writelane_b32 v11, s13, 42\n" \
"s_lshl_b32 s13, s9, 2\n" \
M_"v[96:99], a[252:255], a[200:203], v[96:99]\n" \
R_"a[136:139], v28 offset:4096\n" \
"s_or_b32 s7, s13, s7\n" \
M_"v[112:115], a[188:191], a[140:143], v[112:115]\n" \
R_"a[200:203], v29 offset:4096\n" \
"s_mul_i32 s7, s7, 0x90000\n" \
M_"v[112:115], a[252:255], a[204:207], v[112:115]\n" \
"s_addk_i32 s84, 0x7c00\n" \
M_"v[108:111], a[184:187], a[140:143], v[108:111]\n" \
B_"v26, s[68:71], s84 offen offset:1024 lds\n" \
M_"v[108:111], a[248:251], a[204:207], v[108:111]\n" \
X_"a[172:173], v35 offset:32768\n" \
"s_lshl_b32 s13, s3, 14\n" \
M_"v[104:107], a[180:183], a[140:143], v[104:107]\n" \
"s_addk_i32 s84, 0x7c00\n" \
M_"v[104:107], a[244:247], a[204:207], v[104:107]\n" \
B_"v27, s[68:71], s84 offen offset:2048 lds\n" \
M_"v[100:103], a[176:179], a[140:143], v[100:103]\n" \
X_"a[174:175], v35 offset:33280\n" \
"s_lshl_b32 s9, s9, 7\n" \
M_"v[100:103], a[240:243], a[204:207], v[100:103]\n" \
R_"a[140:143], v28 offset:6144\n" \
"s_lshl_b32 s8, s8, 4\n" \
M_"v[116:119], a[176:179], a[144:147], v[116:119]\n" \
R_"a[204:207], v29 offset:6144\n" \
"s_add_i32 s7, s7, s13\n" \
M_"v[116:119], a[240:243], a[208:211], v[116:119]\n" \
"s_addk_i32 s84, 0x7c00\n" \
M_"v[120:123], a[180:183], a[144:147], v[120:123]\n" \
B_"v27, s[68:71], s84 offen offset:3072 lds\n" \
M_"v[120:123], a[244:247], a[208:211], v[120:123]\n" \
X_"a[236:237], v35 offset:36864\n" \
"s_or_b32 s13, s8, s9\n" \
M_"v[124:127], a[184:187], a[144:147], v[124:127]\n" \
X_"a[238:239], v35 offset:37376\n" \
"s_sub_i32 s8, s8, s9\n" \
M_"v[124:127], a[248:251], a[208:211], v[124:127]\n" \
"s_add_u32 m0, s30, 0x8000\n" \
"s_movk_i32 s84, 0x380\n" \
M_"v[128:131], a[188:191], a[144:147], v[128:131]\n" \
B_"v24, s[76:79], s84 offen offset:0 lds\n" \
M_"v[128:131], a[252:255], a[208:211], v[128:131]\n" \
R_"a[144:147], v28 offset:8192\n" \
"s_mulk_i32 s13, 0x480\n" \
M_"v[144:147], a[188:191], a[148:151], v[144:147]\n" \
R_"a[208:211], v29 offset:8192\n" \
"s_mulk_i32 s8, 0x480\n" \
M_"v[144:147], a[252:255], a[212:215], v[144:147]\n" \
X_"a[168:169], v34 offset:32768\n" \
"s_or_b32 s13, s13, s11\n" \
M_"v[140:143], a[184:187], a[148:151], v[140:143]\n" \
"s_movk_i32 s84, 0x2380\n" \
M_"v[140:143], a[248:251], a[212:215], v[140:143]\n" \
B_"v25, s[76:79], s84 offen offset:1024 lds\n" \
M_"v[136:139], a[180:183], a[148:151], v[136:139]\n" \
X_"a[170:171], v34 offset:33280\n" \
"s_or_b32 s8, s8, s11\n" \
M_"v[136:139], a[244:247], a[212:215], v[136:139]\n" \
X_"a[232:233], v34 offset:36864\n" \
"s_and_b32 s11, s2, 1\n" \
M_"v[132:135], a[176:179], a[148:151], v[132:135]\n" \
X_"a[234:235], v34 offset:37376\n" \
M_"v[132:135], a[240:243], a[212:215], v[132:135]\n" \
R_"a[148:151], v28 offset:10240\n" \
M_"v[148:151], a[176:179], a[152:155], v[148:151]\n" \
R_"a[212:215], v29 offset:10240\n" \
M_"v[148:151], a[240:243], a[216:219], v[148:151]\n" \
"s_movk_i32 s84, 0x4380\n" \
M_"v[152:155], a[180:183], a[152:155], v[152:155]\n" \
B_"v24, s[76:79], s84 offen offset:2048 lds\n" \
M_"v[152:155], a[244:247], a[216:219], v[152:155]\n" \
"s_movk_i32 s84, 0x6380\n" \
M_"v[156:159], a[184:187], a[152:155], v[156:159]\n" \
B_"v25, s[76:79], s84 offen offset:3072 lds\n" \
M_"v[156:159], a[248:251], a[216:219], v[156:159]\n" \
R_"v[248:251], v22 offset:32768\n" \
M_"v[160:163], a[188:191], a[152:155], v[160:163]\n" \
R_"v[48:51], v23 offset:32768\n" \
M_"v[160:163], a[252:255], a[216:219], v[160:163]\n" \
R_"a[152:155], v28 offset:12288\n" \
M_"v[176:179], a[188:191], a[156:159], v[176:179]\n" \
R_"a[216:219], v29 offset:12288\n" \
M_"v[176:179], a[252:255], a[220:223], v[176:179]\n" \
"s_cmp_lg_u32 s11, 0\n" \
"s_cselect_b32 s12, s15, s12\n" \
M_"v[172:175], a[184:187], a[156:159], v[172:175]\n" \
"s_bfe_u32 s9, s2, 0x20001\n" \
"s_add_i32 s15, s7, 0x4240000\n" \
M_"v[172:175], a[248:251], a[220:223], v[172:175]\n" \
"s_addk_i32 s12, 0x4000\n" \
"s_lshl_b32 s5, s5, 16\n" \
M_"v[168:171], a[180:183], a[156:159], v[168:171]\n" \
"s_xor_b32 s9, s9, s3\n" \
M_"v[168:171], a[244:247], a[220:223], v[168:171]\n" \
"s_cmp_lg_u32 s9, 0\n" \
"s_cselect_b32 s12, s6, s12\n" \
M_"v[164:167], a[176:179], a[156:159], v[164:167]\n" \
"s_add_i32 s5, s15, s5\n" \
"v_writelane_b32 v11, s12, 43\n" \
M_"v[164:167], a[240:243], a[220:223], v[164:167]\n" \
R_"a[156:159], v28 offset:14336\n" \
R_"a[220:223], v29 offset:14336\n" \
"s_waitcnt lgkmcnt(15)\n" \
M_"a[0:3], a[160:163], a[128:131], a[0:3]\n" \
"s_add_u32 m0, s30, 0x9000\n" \
"s_addk_i32 s84, 0x3000\n" \
M_"a[0:3], a[224:227], a[192:195], a[0:3]\n" \
B_"v24, s[76:79], s84 offen offset:0 lds\n" \
M_"a[4:7], a[164:167], a[128:131], a[4:7]\n" \
X_"a[176:177], v36 offset:32768\n" \
"s_add_i32 s7, s7, 0x42c0000\n" \
M_"a[4:7], a[228:231], a[192:195], a[4:7]\n" \
X_"a[178:179], v36 offset:33792\n" \
"v_writelane_b32 v11, s5, 44\n" \
M_"a[12:15], a[172:175], a[128:131], a[12:15]\n" \
X_"a[240:241], v36 offset:40960\n" \
"s_lshl_b32 s5, s11, 10\n" \
"s_waitcnt lgkmcnt(15)\n" \
M_"a[12:15], a[236:239], a[192:195], a[12:15]\n" \
"s_add_u32 s89, s42, 0x3000\n" \
"buffer_store_dwordx4 v[208:211], v20, s[80:83], s89 offen offset:3072 sc1\n" \
"s_waitcnt lgkmcnt(13)\n" \
M_"a[8:11], a[128:131], a[168:171], a[8:11]\n" \
X_"a[242:243], v36 offset:41984\n" \
"s_or_b32 s5, s7, s5\n" \
"s_waitcnt lgkmcnt(12)\n" \
M_"a[8:11], a[192:195], a[232:235], a[8:11]\n" \
X_"a[180:181], v37 offset:32768\n" \
M_"a[24:27], a[132:135], a[168:171], a[24:27]\n" \
X_"a[182:183], v37 offset:33792\n" \
M_"a[24:27], a[196:199], a[232:235], a[24:27]\n" \
X_"a[244:245], v37 offset:40960\n" \
M_"a[28:31], a[172:175], a[132:135], a[28:31]\n" \
"s_cmp_lg_u32 s9, 0\n" \
"s_cselect_b32 s5, s6, s5\n" \
M_"a[28:31], a[236:239], a[196:199], a[28:31]\n" \
"s_addk_i32 s84, 0x2000\n" \
M_"a[20:23], a[164:167], a[132:135], a[20:23]\n" \
B_"v25, s[76:79], s84 offen offset:1024 lds\n" \
M_"a[20:23], a[228:231], a[196:199], a[20:23]\n" \
X_"a[246:247], v37 offset:41984\n" \
"s_add_i32 s8, s8, 0x21c000\n" \
M_"a[16:19], a[160:163], a[132:135], a[16:19]\n" \
X_"a[184:185], v38 offset:32768\n" \
"v_writelane_b32 v11, s5, 45\n" \
M_"a[16:19], a[224:227], a[196:199], a[16:19]\n" \
X_"a[186:187], v38 offset:33792\n" \
"s_add_i32 s5, s13, s10\n" \
M_"a[32:35], a[160:163], a[136:139], a[32:35]\n" \
"s_addk_i32 s84, 0x2000\n" \
M_"a[32:35], a[224:227], a[200:203], a[32:35]\n" \
B_"v24, s[76:79], s84 offen offset:2048 lds\n" \
M_"a[36:39], a[164:167], a[136:139], a[36:39]\n" \
X_"a[248:249], v38 offset:40960\n" \
"v_writelane_b32 v11, s5, 46\n" \
M_"a[36:39], a[228:231], a[200:203], a[36:39]\n" \
X_"a[250:251], v38 offset:41984\n" \
"s_add_i32 s5, s8, s10\n" \
"s_nop 0\n" \
M_"a[44:47], a[172:175], a[136:139], a[44:47]\n" \
X_"a[188:189], v39 offset:32768\n" \
"v_writelane_b32 v11, s5, 47\n" \
M_"a[44:47], a[236:239], a[200:203], a[44:47]\n" \
X_"a[190:191], v39 offset:33792\n" \
M_"a[40:43], a[136:139], a[168:171], a[40:43]\n" \
"s_add_u32 s88, s42, 0x3000\n" \
"buffer_store_dwordx4 v[212:215], v20, s[80:83], s88 offen offset:2048 sc1\n" \
M_"a[40:43], a[200:203], a[232:235], a[40:43]\n" \
X_"a[252:253], v39 offset:40960\n" \
M_"a[56:59], a[140:143], a[168:171], a[56:59]\n" \
X_"a[254:255], v39 offset:41984\n" \
M_"a[56:59], a[204:207], a[232:235], a[56:59]\n" \
"s_cmp_lg_u32 s11, 0\n" \
"s_cselect_b32 s5, s8, s13\n" \
M_"a[60:63], a[172:175], a[140:143], a[60:63]\n" \
"s_addk_i32 s5, 0x100\n" \
M_"a[60:63], a[236:239], a[204:207], a[60:63]\n" \
"s_addk_i32 s84, 0x2000\n" \
M_"a[52:55], a[164:167], a[140:143], a[52:55]\n" \
B_"v25, s[76:79], s84 offen offset:3072 lds\n" \
M_"a[52:55], a[228:231], a[204:207], a[52:55]\n" \
"s_cmp_lg_u32 s9, 0\n" \
"s_cselect_b32 s5, s6, s5\n" \
M_"a[48:51], a[160:163], a[140:143], a[48:51]\n" \
"v_writelane_b32 v11, s5, 48\n" \
"s_or_b32 s5, s7, s14\n" \
M_"a[48:51], a[224:227], a[204:207], a[48:51]\n" \
"v_writelane_b32 v11, s5, 56\n" \
"s_lshl_b32 s5, s3, 17\n" \
"s_waitcnt lgkmcnt(15)\n" \
M_"a[64:67], a[164:167], v[248:251], a[64:67]\n" \
"s_add_i32 s5, s15, s5\n" \
"s_or_b32 s4, s5, s4\n" \
M_"a[64:67], a[228:231], v[48:51], a[64:67]\n" \
"s_or_b32 s4, s4, 0x3840\n" \
"v_writelane_b32 v11, s4, 6\n" \
M_"a[80:83], a[172:175], v[248:251], a[80:83]\n" \
"s_mov_b32 s4, 0xff0000\n" \
M_"a[80:83], a[236:239], v[48:51], a[80:83]\n" \
"s_cmp_lg_u32 s9, 0\n" \
"s_cselect_b32 s4, s16, s4\n" \
M_"a[68:71], v[248:251], a[168:171], a[68:71]\n" \
"v_writelane_b32 v11, s4, 7\n" \
"s_lshl_b32 s4, s2, 16\n" \
"s_waitcnt vmcnt(10) lgkmcnt(0)\n" \
M_"a[68:71], v[48:51], a[232:235], a[68:71]\n" \
"s_barrier\n" \
"v_xor_b32_e32 v28, 0x20000, v28\n" \
"v_xor_b32_e32 v29, 0x20000, v29\n" \
M_"v[52:55], a[176:179], a[128:131], v[52:55]\n" \
"s_add_u32 m0, s40, 0x8000\n" \
"s_add_u32 s85, s39, 0xe000\n" \
M_"v[52:55], a[240:243], a[192:195], v[52:55]\n" \
B_"v20, s[80:83], s85 offen sc1 lds\n" \
M_"v[56:59], a[180:183], a[128:131], v[56:59]\n" \
X_"a[160:161], v32 offset:0\n" \
"s_add_i32 s5, s4, 0x10000\n" \
M_"v[56:59], a[244:247], a[192:195], v[56:59]\n" \
X_"a[162:163], v32 offset:512\n" \
"s_and_b32 s5, s5, 0x70000\n" \
M_"v[60:63], a[184:187], a[128:131], v[60:63]\n" \
"s_add_u32 m0, s40, 0x9000\n" \
"s_add_u32 s85, s39, 0xf000\n" \
M_"v[60:63], a[248:251], a[192:195], v[60:63]\n" \
B_"v20, s[80:83], s85 offen sc1 lds\n" \
M_"v[64:67], a[188:191], a[128:131], v[64:67]\n" \
X_"a[224:225], v32 offset:4096\n" \
"s_add_i32 s5, s15, s5\n" \
M_"v[64:67], a[252:255], a[192:195], v[64:67]\n" \
R_"a[128:131], v28 offset:0\n" \
"v_writelane_b32 v11, s5, 49\n" \
M_"v[80:83], a[188:191], a[132:135], v[80:83]\n" \
R_"a[192:195], v29 offset:0\n" \
"s_add_i32 s5, s4, 0x20000\n" \
M_"v[80:83], a[252:255], a[196:199], v[80:83]\n" \
"s_add_u32 m0, s40, 0xb000\n" \
"s_movk_i32 s84, 0x380\n" \
M_"v[76:79], a[184:187], a[132:135], v[76:79]\n" \
B_"v21, s[76:79], s84 offen offset:0 lds\n" \
M_"v[76:79], a[248:251], a[196:199], v[76:79]\n" \
X_"a[226:227], v32 offset:4608\n" \
"s_and_b32 s5, s5, 0x70000\n" \
M_"v[72:75], a[180:183], a[132:135], v[72:75]\n" \
X_"a[164:165], v33 offset:0\n" \
"s_add_i32 s5, s15, s5\n" \
M_"v[72:75], a[244:247], a[196:199], v[72:75]\n" \
"s_bitset1_b32 s5, 11\n" \
"v_writelane_b32 v11, s5, 50\n" \
M_"v[68:71], a[176:179], a[132:135], v[68:71]\n" \
X_"a[166:167], v33 offset:512\n" \
"s_add_i32 s5, s4, 0x30000\n" \
M_"v[68:71], a[240:243], a[196:199], v[68:71]\n" \
R_"a[132:135], v28 offset:2048\n" \
"s_and_b32 s5, s5, 0x70000\n" \
M_"v[84:87], a[176:179], a[136:139], v[84:87]\n" \
R_"a[196:199], v29 offset:2048\n" \
"s_add_i32 s5, s15, s5\n" \
M_"v[84:87], a[240:243], a[200:203], v[84:87]\n" \
"s_add_u32 m0, s31, 0x8000\n" \
"s_mov_b32 s84, 0x380000\n" \
M_"v[88:91], a[180:183], a[136:139], v[88:91]\n" \
B_"v26, s[68:71], s84 offen offset:0 lds\n" \
M_"v[88:91], a[244:247], a[200:203], v[88:91]\n" \
"s_add_u32 s89, s42, 0x0\n" \
"buffer_store_dwordx4 v[184:187], v20, s[80:83], s89 offen offset:1024 sc1\n" \
M_"v[92:95], a[184:187], a[136:139], v[92:95]\n" \
X_"a[228:229], v33 offset:4096\n" \
"s_bitset1_b32 s5, 12\n" \
M_"v[92:95], a[248:251], a[200:203], v[92:95]\n" \
X_"a[230:231], v33 offset:4608\n" \
"v_writelane_b32 v11, s5, 51\n" \
M_"v[96:99], a[188:191], a[136:139], v[96:99]\n" \
"s_and_b32 s5, s4, 0x70000\n" \
"s_xor_b32 s5, s5, 0x40000\n" \
M_"v[96:99], a[252:255], a[200:203], v[96:99]\n" \
R_"a[136:139], v28 offset:4096\n" \
"s_add_i32 s5, s15, s5\n" \
M_"v[112:115], a[188:191], a[140:143], v[112:115]\n" \
R_"a[200:203], v29 offset:4096\n" \
"s_or_b32 s5, s5, 0x1800\n" \
M_"v[112:115], a[252:255], a[204:207], v[112:115]\n" \
"s_addk_i32 s84, 0x7c00\n" \
M_"v[108:111], a[184:187], a[140:143], v[108:111]\n" \
B_"v26, s[68:71], s84 offen offset:1024 lds\n" \
M_"v[108:111], a[248:251], a[204:207], v[108:111]\n" \
X_"a[172:173], v35 offset:0\n" \
"v_writelane_b32 v11, s5, 52\n" \
M_"v[104:107], a[180:183], a[140:143], v[104:107]\n" \
"s_addk_i32 s84, 0x7c00\n" \
M_"v[104:107], a[244:247], a[204:207], v[104:107]\n" \
B_"v27, s[68:71], s84 offen offset:2048 lds\n" \
M_"v[100:103], a[176:179], a[140:143], v[100:103]\n" \
X_"a[174:175], v35 offset:512\n" \
"s_add_i32 s5, s4, 0x50000\n" \
M_"v[100:103], a[240:243], a[204:207], v[100:103]\n" \
R_"a[140:143], v28 offset:6144\n" \
"s_and_b32 s5, s5, 0x70000\n" \
M_"v[116:119], a[176:179], a[144:147], v[116:119]\n" \
R_"a[204:207], v29 offset:6144\n" \
"s_add_i32 s5, s15, s5\n" \
M_"v[116:119], a[240:243], a[208:211], v[116:119]\n" \
"s_addk_i32 s84, 0x7c00\n" \
M_"v[120:123], a[180:183], a[144:147], v[120:123]\n" \
B_"v27, s[68:71], s84 offen offset:3072 lds\n" \
M_"v[120:123], a[244:247], a[208:211], v[120:123]\n" \
X_"a[236:237], v35 offset:4096\n" \
"s_bitset1_b32 s5, 13\n" \
M_"v[124:127], a[184:187], a[144:147], v[124:127]\n" \
X_"a[238:239], v35 offset:4608\n" \
"v_writelane_b32 v11, s5, 53\n" \
M_"v[124:127], a[248:251], a[208:211], v[124:127]\n" \
"s_add_u32 m0, s30, 0x20000\n" \
"s_movk_i32 s84, 0x400\n" \
M_"v[128:131], a[188:191], a[144:147], v[128:131]\n" \
B_"v24, s[76:79], s84 offen offset:0 lds\n" \
M_"v[128:131], a[252:255], a[208:211], v[128:131]\n" \
R_"a[144:147], v28 offset:8192\n" \
"s_add_i32 s5, s4, 0x60000\n" \
M_"v[144:147], a[188:191], a[148:151], v[144:147]\n" \
R_"a[208:211], v29 offset:8192\n" \
"s_and_b32 s5, s5, 0x70000\n" \
M_"v[144:147], a[252:255], a[212:215], v[144:147]\n" \
X_"a[168:169], v34 offset:0\n" \
"s_add_i32 s4, s4, 0x70000\n" \
M_"v[140:143], a[184:187], a[148:151], v[140:143]\n" \
"s_movk_i32 s84, 0x2400\n" \
M_"v[140:143], a[248:251], a[212:215], v[140:143]\n" \
B_"v25, s[76:79], s84 offen offset:1024 lds\n" \
M_"v[136:139], a[180:183], a[148:151], v[136:139]\n" \
X_"a[170:171], v34 offset:512\n" \
"s_add_i32 s5, s15, s5\n" \
M_"v[136:139], a[244:247], a[212:215], v[136:139]\n" \
X_"a[232:233], v34 offset:4096\n" \
"s_and_b32 s4, s4, 0x70000\n" \
M_"v[132:135], a[176:179], a[148:151], v[132:135]\n" \
X_"a[234:235], v34 offset:4608\n" \
"s_or_b32 s5, s5, 0x2800\n" \
M_"v[132:135], a[240:243], a[212:215], v[132:135]\n" \
R_"a[148:151], v28 offset:10240\n" \
"v_writelane_b32 v11, s5, 54\n" \
M_"v[148:151], a[176:179], a[152:155], v[148:151]\n" \
R_"a[212:215], v29 offset:10240\n" \
"s_add_i32 s15, s15, s4\n" \
M_"v[148:151], a[240:243], a[216:219], v[148:151]\n" \
"s_movk_i32 s84, 0x4400\n" \
M_"v[152:155], a[180:183], a[152:155], v[152:155]\n" \
B_"v24, s[76:79], s84 offen offset:2048 lds\n" \
M_"v[152:155], a[244:247], a[216:219], v[152:155]\n" \
"s_movk_i32 s84, 0x6400\n" \
M_"v[156:159], a[184:187], a[152:155], v[156:159]\n" \
B_"v25, s[76:79], s84 offen offset:3072 lds\n" \
M_"v[156:159], a[248:251], a[216:219], v[156:159]\n" \
R_"v[248:251], v22 offset:0\n" \
"s_or_b32 s4, s15, 0x3000\n" \
M_"v[160:163], a[188:191], a[152:155], v[160:163]\n" \
R_"v[48:51], v23 offset:0\n" \
"v_writelane_b32 v11, s4, 55\n" \
M_"v[160:163], a[252:255], a[216:219], v[160:163]\n" \
R_"a[152:155], v28 offset:12288\n" \
"s_movk_i32 s11, 0x4000\n" \
M_"v[176:179], a[188:191], a[156:159], v[176:179]\n" \
R_"a[216:219], v29 offset:12288\n" \
"s_lshl_b32 s4, s2, 2\n" \
M_"v[176:179], a[252:255], a[220:223], v[176:179]\n" \
"s_and_b32 s4, s4, 28\n" \
"s_bfe_u32 s5, s2, 0x20003\n" \
M_"v[172:175], a[184:187], a[156:159], v[172:175]\n" \
"s_or_b32 s4, s4, s5\n" \
"s_lshr_b32 s5, s2, 5\n" \
M_"v[172:175], a[248:251], a[220:223], v[172:175]\n" \
"s_lshr_b32 s19, s3, 1\n" \
"s_lshl_b32 s18, s5, 1\n" \
M_"v[168:171], a[180:183], a[156:159], v[168:171]\n" \
"s_lshl_b32 s6, s4, 20\n" \
"s_lshl_b32 s7, s4, 8\n" \
M_"v[168:171], a[244:247], a[220:223], v[168:171]\n" \
"s_and_b32 s13, s3, 1\n" \
"s_add_i32 s14, s18, s19\n" \
M_"v[164:167], a[176:179], a[156:159], v[164:167]\n" \
"s_add_u32 s6, s26, s6\n" \
"s_addc_u32 s9, s27, 0\n" \
M_"v[164:167], a[240:243], a[220:223], v[164:167]\n" \
R_"a[156:159], v28 offset:14336\n" \
R_"a[220:223], v29 offset:14336\n" \
"s_waitcnt lgkmcnt(15)\n" \
M_"a[0:3], a[160:163], a[128:131], a[0:3]\n" \
"s_add_u32 m0, s30, 0x21000\n" \
"s_addk_i32 s84, 0x3000\n" \
M_"a[0:3], a[224:227], a[192:195], a[0:3]\n" \
B_"v24, s[76:79], s84 offen offset:0 lds\n" \
M_"a[4:7], a[164:167], a[128:131], a[4:7]\n" \
X_"a[176:177], v36 offset:0\n" \
M_"a[4:7], a[228:231], a[192:195], a[4:7]\n" \
X_"a[178:179], v36 offset:1024\n" \
M_"a[12:15], a[172:175], a[128:131], a[12:15]\n" \
X_"a[240:241], v36 offset:8192\n" \
"s_waitcnt lgkmcnt(15)\n" \
M_"a[12:15], a[236:239], a[192:195], a[12:15]\n" \
"s_add_u32 s88, s42, 0x1000\n" \
"buffer_store_dwordx4 v[188:191], v20, s[80:83], s88 offen offset:1024 sc1\n" \
"s_waitcnt lgkmcnt(13)\n" \
M_"a[8:11], a[128:131], a[168:171], a[8:11]\n" \
X_"a[242:243], v36 offset:9216\n" \
"s_waitcnt lgkmcnt(12)\n" \
M_"a[8:11], a[192:195], a[232:235], a[8:11]\n" \
X_"a[180:181], v37 offset:0\n" \
M_"a[24:27], a[132:135], a[168:171], a[24:27]\n" \
X_"a[182:183], v37 offset:1024\n" \
M_"a[24:27], a[196:199], a[232:235], a[24:27]\n" \
X_"a[244:245], v37 offset:8192\n" \
M_"a[28:31], a[172:175], a[132:135], a[28:31]\n" \
"s_add_u32 s8, s6, 0x1000000\n" \
"s_addc_u32 s6, s9, 0\n" \
M_"a[28:31], a[236:239], a[196:199], a[28:31]\n" \
"s_addk_i32 s84, 0x2000\n" \
M_"a[20:23], a[164:167], a[132:135], a[20:23]\n" \
B_"v25, s[76:79], s84 offen offset:1024 lds\n" \
M_"a[20:23], a[228:231], a[196:199], a[20:23]\n" \
X_"a[246:247], v37 offset:9216\n" \
"s_and_b32 s9, s6, 0xffff\n" \
M_"a[16:19], a[160:163], a[132:135], a[16:19]\n" \
X_"a[184:185], v38 offset:0\n" \
"s_lshl_b32 s6, s5, 16\n" \
M_"a[16:19], a[224:227], a[196:199], a[16:19]\n" \
X_"a[186:187], v38 offset:1024\n" \
"s_sub_i32 s10, 0x100000, s6\n" \
M_"a[32:35], a[160:163], a[136:139], a[32:35]\n" \
"s_addk_i32 s84, 0x2000\n" \
"s_nop 0\n" \
M_"a[32:35], a[224:227], a[200:203], a[32:35]\n" \
B_"v24, s[76:79], s84 offen offset:2048 lds\n" \
M_"a[36:39], a[164:167], a[136:139], a[36:39]\n" \
X_"a[248:249], v38 offset:8192\n" \
"s_mov_b32 s6, 0x8000\n" \
M_"a[36:39], a[228:231], a[200:203], a[36:39]\n" \
X_"a[250:251], v38 offset:9216\n" \
M_"a[44:47], a[172:175], a[136:139], a[44:47]\n" \
X_"a[188:189], v39 offset:0\n" \
M_"a[44:47], a[236:239], a[200:203], a[44:47]\n" \
X_"a[190:191], v39 offset:1024\n" \
M_"a[40:43], a[136:139], a[168:171], a[40:43]\n" \
"s_add_u32 s89, s42, 0x2000\n" \
"buffer_store_dwordx4 v[192:195], v20, s[80:83], s89 offen offset:0 sc1\n" \
M_"a[40:43], a[200:203], a[232:235], a[40:43]\n" \
X_"a[252:253], v39 offset:8192\n" \
M_"a[56:59], a[140:143], a[168:171], a[56:59]\n" \
X_"a[254:255], v39 offset:9216\n" \
M_"a[56:59], a[204:207], a[232:235], a[56:59]\n" \
"s_cmp_lg_u32 s5, 0\n" \
"s_cselect_b32 s17, s11, s6\n" \
M_"a[60:63], a[172:175], a[140:143], a[60:63]\n" \
"s_lshl_b32 s6, s3, 1\n" \
"s_lshl_b32 s5, s5, 3\n" \
M_"a[60:63], a[236:239], a[204:207], a[60:63]\n" \
"s_addk_i32 s84, 0x2000\n" \
M_"a[52:55], a[164:167], a[140:143], a[52:55]\n" \
B_"v25, s[76:79], s84 offen offset:3072 lds\n" \
M_"a[52:55], a[228:231], a[204:207], a[52:55]\n" \
"s_and_b32 s6, s6, 4\n" \
"s_or_b32 s12, s5, s6\n" \
M_"a[48:51], a[160:163], a[140:143], a[48:51]\n" \
"s_sub_i32 s5, s13, s5\n" \
"s_or_b32 s5, s5, s6\n" \
".fill (L_pb + 32480 - .) / 4, 4, 0xBF800000\n" \
"L_pp8:\n" \
"s_cbranch_execz L_pret\n" \
M_"a[48:51], a[224:227], a[204:207], a[48:51]\n" \
"s_or_b32 s6, s12, s13\n" \
"s_lshl_b32 s4, s4, 2\n" \
"s_waitcnt lgkmcnt(15)\n" \
M_"a[64:67], a[164:167], v[248:251], a[64:67]\n" \
"s_mulk_i32 s6, 0x92\n" \
"s_add_i32 s4, s4, 18\n" \
M_"a[64:67], a[228:231], v[48:51], a[64:67]\n" \
"s_mulk_i32 s5, 0x92\n" \
"s_add_i32 s6, s4, s6\n" \
M_"a[80:83], a[172:175], v[248:251], a[80:83]\n" \
"s_add_i32 s5, s5, s4\n" \
"s_lshl_b32 s4, s3, 11\n" \
M_"a[80:83], a[236:239], v[48:51], a[80:83]\n" \
"s_lshl_b32 s15, s13, 12\n" \
"s_lshl_b32 s16, s19, 14\n" \
M_"a[68:71], v[248:251], a[168:171], a[68:71]\n" \
"s_mul_i32 s19, s19, 0xe000\n" \
"s_mov_b32 s11, 0x20000\n" \
"s_waitcnt vmcnt(10) lgkmcnt(0)\n" \
M_"a[68:71], v[48:51], a[232:235], a[68:71]\n" \
"s_barrier\n" \
M_"v[52:55], a[176:179], a[128:131], v[52:55]\n" \
"s_add_u32 m0, s40, 0x0\n" \
"s_add_u32 s85, s39, 0x10000\n" \
M_"v[52:55], a[240:243], a[192:195], v[52:55]\n" \
B_"v20, s[80:83], s85 offen sc1 lds\n" \
M_"v[56:59], a[180:183], a[128:131], v[56:59]\n" \
X_"a[160:161], v32 offset:32768\n" \
"s_or_b32 s12, s4, 0xc000\n" \
M_"v[56:59], a[244:247], a[192:195], v[56:59]\n" \
X_"a[162:163], v32 offset:33280\n" \
"s_lshl_b32 s4, s3, 8\n" \
M_"v[60:63], a[184:187], a[128:131], v[60:63]\n" \
"s_or_b32 s22, s15, s16\n" \
"s_or_b32 s23, s19, s15\n" \
M_"v[60:63], a[248:251], a[192:195], v[60:63]\n" \
X_"a[224:225], v32 offset:36864\n" \
"s_lshl_b32 s60, s6, 10\n" \
M_"v[64:67], a[188:191], a[128:131], v[64:67]\n" \
"s_lshl_b32 s61, s5, 10\n" \
"s_lshl_b32 s14, s14, 15\n" \
"s_nop 0\n" \
M_"v[64:67], a[252:255], a[192:195], v[64:67]\n" \
R_"a[128:131], v28 offset:32768\n" \
"s_or_b32 s20, s4, 0x26c00\n" \
M_"v[80:83], a[188:191], a[132:135], v[80:83]\n" \
R_"a[192:195], v29 offset:32768\n" \
"s_or_b32 s13, s22, 0x8000\n" \
M_"v[80:83], a[252:255], a[196:199], v[80:83]\n" \
"s_add_u32 m0, s40, 0x1000\n" \
"s_add_u32 s85, s39, 0x11000\n" \
M_"v[76:79], a[184:187], a[132:135], v[76:79]\n" \
B_"v20, s[80:83], s85 offen sc1 lds\n" \
M_"v[76:79], a[248:251], a[196:199], v[76:79]\n" \
X_"a[226:227], v32 offset:37376\n" \
"s_add_i32 s22, s22, 0x14000\n" \
M_"v[72:75], a[180:183], a[132:135], v[72:75]\n" \
X_"a[164:165], v33 offset:32768\n" \
"s_bitset1_b32 s23, 16\n" \
M_"v[72:75], a[244:247], a[196:199], v[72:75]\n" \
"s_or_b32 s24, s7, 0x5be8020\n" \
"s_and_b32 s57, s27, 0xffff\n" \
M_"v[68:71], a[176:179], a[132:135], v[68:71]\n" \
X_"a[166:167], v33 offset:33280\n" \
"s_mov_b32 s58, 0x6000000\n" \
M_"v[68:71], a[240:243], a[196:199], v[68:71]\n" \
R_"a[132:135], v28 offset:34816\n" \
"s_mov_b32 s56, s26\n" \
M_"v[84:87], a[176:179], a[136:139], v[84:87]\n" \
R_"a[196:199], v29 offset:34816\n" \
"s_mov_b32 s59, s11\n" \
M_"v[84:87], a[240:243], a[200:203], v[84:87]\n" \
"s_add_u32 m0, s40, 0x3000\n" \
"s_movk_i32 s84, 0x400\n" \
M_"v[88:91], a[180:183], a[136:139], v[88:91]\n" \
B_"v21, s[76:79], s84 offen offset:0 lds\n" \
M_"v[88:91], a[244:247], a[200:203], v[88:91]\n" \
X_"a[228:229], v33 offset:36864\n" \
"s_add_i32 s60, s60, 0x3000000\n" \
M_"v[92:95], a[184:187], a[136:139], v[92:95]\n" \
X_"a[230:231], v33 offset:37376\n" \
"s_add_i32 s61, s61, 0x411c000\n" \
M_"v[92:95], a[248:251], a[200:203], v[92:95]\n" \
"s_add_u32 s88, s42, 0x2000\n" \
"buffer_store_dwordx4 v[216:219], v20, s[80:83], s88 offen offset:1024 sc1\n" \
M_"v[96:99], a[188:191], a[136:139], v[96:99]\n" \
"s_sub_i32 s19, 14, s18\n" \
"s_nop 0\n" \
"s_mov_b32 s21, 0x71800000\n" \
M_"v[96:99], a[252:255], a[200:203], v[96:99]\n" \
R_"a[136:139], v28 offset:36864\n" \
"s_mov_b32 s4, L_gsw-L_gtr\n" \
M_"v[112:115], a[188:191], a[140:143], v[112:115]\n" \
R_"a[200:203], v29 offset:36864\n" \
M_"v[112:115], a[252:255], a[204:207], v[112:115]\n" \
X_"a[172:173], v35 offset:32768\n" \
M_"v[108:111], a[184:187], a[140:143], v[108:111]\n" \
X_"a[174:175], v35 offset:33280\n" \
M_"v[108:111], a[248:251], a[204:207], v[108:111]\n" \
"s_cmp_eq_u32 s19, 0\n" \
"s_cselect_b32 s4, L_gs7-L_gtr, s4\n" \
M_"v[104:107], a[180:183], a[140:143], v[104:107]\n" \
"s_add_u32 m0, s31, 0x0\n" \
"s_mov_b32 s84, 0x400000\n" \
M_"v[104:107], a[244:247], a[204:207], v[104:107]\n" \
B_"v26, s[68:71], s84 offen offset:0 lds\n" \
M_"v[100:103], a[176:179], a[140:143], v[100:103]\n" \
X_"a[236:237], v35 offset:36864\n" \
M_"v[100:103], a[240:243], a[204:207], v[100:103]\n" \
R_"a[140:143], v28 offset:38912\n" \
M_"v[116:119], a[176:179], a[144:147], v[116:119]\n" \
R_"a[204:207], v29 offset:38912\n" \
M_"v[116:119], a[240:243], a[208:211], v[116:119]\n" \
X_"a[238:239], v35 offset:37376\n" \
M_"v[120:123], a[180:183], a[144:147], v[120:123]\n" \
X_"a[168:169], v34 offset:32768\n" \
M_"v[120:123], a[244:247], a[208:211], v[120:123]\n" \
"s_addk_i32 s84, 0x7c00\n" \
M_"v[124:127], a[184:187], a[144:147], v[124:127]\n" \
B_"v26, s[68:71], s84 offen offset:1024 lds\n" \
M_"v[124:127], a[248:251], a[208:211], v[124:127]\n" \
X_"a[170:171], v34 offset:33280\n" \
M_"v[128:131], a[188:191], a[144:147], v[128:131]\n" \
"s_cmp_eq_u32 s16, 0\n" \
"s_cselect_b32 s4, 0, s4\n" \
M_"v[128:131], a[252:255], a[208:211], v[128:131]\n" \
R_"a[144:147], v28 offset:40960\n" \
M_"v[144:147], a[188:191], a[148:151], v[144:147]\n" \
R_"a[208:211], v29 offset:40960\n" \
M_"v[144:147], a[252:255], a[212:215], v[144:147]\n" \
X_"a[232:233], v34 offset:36864\n" \
M_"v[140:143], a[184:187], a[148:151], v[140:143]\n" \
"s_addk_i32 s84, 0x7c00\n" \
M_"v[140:143], a[248:251], a[212:215], v[140:143]\n" \
B_"v27, s[68:71], s84 offen offset:2048 lds\n" \
M_"v[136:139], a[180:183], a[148:151], v[136:139]\n" \
X_"a[234:235], v34 offset:37376\n" \
M_"v[136:139], a[244:247], a[212:215], v[136:139]\n" \
"s_getpc_b64 s[28:29]\n" \
"L_gp0_%=:\n" \
"s_add_u32 s28, s28, L_gtr-L_gp0_%=\n" \
"s_addc_u32 s29, s29, 0\n" \
M_"v[132:135], a[176:179], a[148:151], v[132:135]\n" \
R_"v[248:251], v22 offset:32768\n" \
M_"v[132:135], a[240:243], a[212:215], v[132:135]\n" \
R_"a[148:151], v28 offset:43008\n" \
M_"v[148:151], a[176:179], a[152:155], v[148:151]\n" \
R_"a[212:215], v29 offset:43008\n" \
M_"v[148:151], a[240:243], a[216:219], v[148:151]\n" \
"s_add_u32 m0, s31, 0x0\n" \
"s_mov_b32 s84, 0x417400\n" \
M_"v[152:155], a[180:183], a[152:155], v[152:155]\n" \
B_"v27, s[68:71], s84 offen offset:3072 lds\n" \
M_"v[152:155], a[244:247], a[216:219], v[152:155]\n" \
R_"v[48:51], v23 offset:32768\n" \
M_"v[156:159], a[184:187], a[152:155], v[156:159]\n" \
"s_add_u32 s28, s28, s4\n" \
"s_addc_u32 s29, s29, 0\n" \
"s_nop 0\n" \
M_"v[156:159], a[248:251], a[216:219], v[156:159]\n" \
M_"v[160:163], a[188:191], a[152:155], v[160:163]\n" \
M_"v[160:163], a[252:255], a[216:219], v[160:163]\n" \
R_"a[152:155], v28 offset:45056\n" \
M_"v[176:179], a[188:191], a[156:159], v[176:179]\n" \
R_"a[216:219], v29 offset:45056\n" \
M_"v[176:179], a[252:255], a[220:223], v[176:179]\n" \
M_"v[172:175], a[184:187], a[156:159], v[172:175]\n" \
M_"v[172:175], a[248:251], a[220:223], v[172:175]\n" \
M_"v[168:171], a[180:183], a[156:159], v[168:171]\n" \
M_"v[168:171], a[244:247], a[220:223], v[168:171]\n" \
M_"v[164:167], a[176:179], a[156:159], v[164:167]\n" \
M_"v[164:167], a[240:243], a[220:223], v[164:167]\n" \
R_"a[156:159], v28 offset:47104\n" \
R_"a[220:223], v29 offset:47104\n" \
"s_waitcnt lgkmcnt(15)\n" \
"s_nop 0\n" \
M_"a[0:3], a[160:163], a[128:131], a[0:3]\n" \
"s_add_u32 s89, s42, 0x3000\n" \
"buffer_store_dwordx4 v[220:223], v20, s[80:83], s89 offen offset:0 sc1\n" \
M_"a[0:3], a[224:227], a[192:195], a[0:3]\n" \
X_"a[176:177], v36 offset:32768\n" \
M_"a[4:7], a[164:167], a[128:131], a[4:7]\n" \
X_"a[178:179], v36 offset:33792\n" \
M_"a[4:7], a[228:231], a[192:195], a[4:7]\n" \
X_"a[240:241], v36 offset:40960\n" \
M_"a[12:15], a[172:175], a[128:131], a[12:15]\n" \
X_"a[242:243], v36 offset:41984\n" \
"s_waitcnt lgkmcnt(15)\n" \
M_"a[12:15], a[236:239], a[192:195], a[12:15]\n" \
M_"a[8:11], a[128:131], a[168:171], a[8:11]\n" \
X_"a[180:181], v37 offset:32768\n" \
"s_waitcnt lgkmcnt(13)\n" \
M_"a[8:11], a[192:195], a[232:235], a[8:11]\n" \
M_"a[24:27], a[132:135], a[168:171], a[24:27]\n" \
X_"a[182:183], v37 offset:33792\n" \
M_"a[24:27], a[196:199], a[232:235], a[24:27]\n" \
X_"a[244:245], v37 offset:40960\n" \
M_"a[28:31], a[172:175], a[132:135], a[28:31]\n" \
M_"a[28:31], a[236:239], a[196:199], a[28:31]\n" \
X_"a[246:247], v37 offset:41984\n" \
M_"a[20:23], a[164:167], a[132:135], a[20:23]\n" \
M_"a[20:23], a[228:231], a[196:199], a[20:23]\n" \
X_"a[184:185], v38 offset:32768\n" \
M_"a[16:19], a[160:163], a[132:135], a[16:19]\n" \
X_"a[186:187], v38 offset:33792\n" \
M_"a[16:19], a[224:227], a[196:199], a[16:19]\n" \
M_"a[32:35], a[160:163], a[136:139], a[32:35]\n" \
"s_add_u32 s88, s42, 0x3000\n" \
"buffer_store_dwordx4 v[224:227], v20, s[80:83], s88 offen offset:1024 sc1\n" \
M_"a[32:35], a[224:227], a[200:203], a[32:35]\n" \
X_"a[248:249], v38 offset:40960\n" \
M_"a[36:39], a[164:167], a[136:139], a[36:39]\n" \
"v_lshlrev_b32_e32 v45, 3, v20\n" \
"v_add_u32_e32 v45, s30, v45\n" \
M_"a[36:39], a[228:231], a[200:203], a[36:39]\n" \
"s_getpc_b64 s[86:87]\n" \
"L_ct0_%=:\n" \
"s_add_u32 s86, s86, L_ap0-L_ct0_%=\n" \
"s_addc_u32 s87, s87, 0\n" \
"s_add_u32 m0, s40, 0x2000\n" \
M_"a[44:47], a[172:175], a[136:139], a[44:47]\n" \
"global_load_lds_dword v45, s[86:87]\n" \
M_"a[44:47], a[236:239], a[200:203], a[44:47]\n" \
X_"a[250:251], v38 offset:41984\n" \
M_"a[40:43], a[136:139], a[168:171], a[40:43]\n" \
X_"a[188:189], v39 offset:32768\n" \
M_"a[40:43], a[200:203], a[232:235], a[40:43]\n" \
X_"a[190:191], v39 offset:33792\n" \
M_"a[56:59], a[140:143], a[168:171], a[56:59]\n" \
X_"a[252:253], v39 offset:40960\n" \
M_"a[56:59], a[204:207], a[232:235], a[56:59]\n" \
X_"a[254:255], v39 offset:41984\n" \
M_"a[60:63], a[172:175], a[140:143], a[60:63]\n" \
M_"a[60:63], a[236:239], a[204:207], a[60:63]\n" \
M_"a[52:55], a[164:167], a[140:143], a[52:55]\n" \
M_"a[52:55], a[228:231], a[204:207], a[52:55]\n" \
M_"a[48:51], a[160:163], a[140:143], a[48:51]\n" \
M_"a[48:51], a[224:227], a[204:207], a[48:51]\n" \
"s_waitcnt lgkmcnt(15)\n" \
M_"a[64:67], a[164:167], v[248:251], a[64:67]\n" \
M_"a[64:67], a[228:231], v[48:51], a[64:67]\n" \
M_"a[80:83], a[172:175], v[248:251], a[80:83]\n" \
M_"a[80:83], a[236:239], v[48:51], a[80:83]\n" \
M_"a[68:71], v[248:251], a[168:171], a[68:71]\n" \
"s_waitcnt vmcnt(3) lgkmcnt(0)\n" \
M_"a[68:71], v[48:51], a[232:235], a[68:71]\n" \
"s_barrier\n" \
"v_xor_b32_e32 v28, 0x20000, v28\n" \
"v_xor_b32_e32 v29, 0x20000, v29\n" \
M_"v[52:55], a[176:179], a[128:131], v[52:55]\n" \
X_"a[160:161], v32 offset:0\n" \
"v_accvgpr_read_b32 v184, a0\n" \
M_"v[52:55], a[240:243], a[192:195], v[52:55]\n" \
"v_accvgpr_read_b32 v185, a1\n" \
"v_accvgpr_read_b32 v186, a2\n" \
M_"v[56:59], a[180:183], a[128:131], v[56:59]\n" \
X_"a[162:163], v32 offset:512\n" \
"v_accvgpr_read_b32 v187, a3\n" \
M_"v[56:59], a[244:247], a[192:195], v[56:59]\n" \
"v_accvgpr_read_b32 v188, a4\n" \
"v_accvgpr_read_b32 v189, a5\n" \
M_"v[60:63], a[184:187], a[128:131], v[60:63]\n" \
"v_accvgpr_read_b32 v190, a6\n" \
"v_accvgpr_read_b32 v191, a7\n" \
M_"v[60:63], a[248:251], a[192:195], v[60:63]\n" \
X_"a[224:225], v32 offset:4096\n" \
"v_accvgpr_read_b32 v196, a12\n" \
M_"v[64:67], a[188:191], a[128:131], v[64:67]\n" \
"v_accvgpr_read_b32 v197, a13\n" \
"v_accvgpr_read_b32 v198, a14\n" \
M_"v[64:67], a[252:255], a[192:195], v[64:67]\n" \
R_"a[128:131], v28 offset:0\n" \
"v_accvgpr_read_b32 v199, a15\n" \
M_"v[80:83], a[188:191], a[132:135], v[80:83]\n" \
R_"a[192:195], v29 offset:0\n" \
"v_accvgpr_read_b32 v192, a8\n" \
M_"v[80:83], a[252:255], a[196:199], v[80:83]\n" \
X_"a[226:227], v32 offset:4608\n" \
"v_accvgpr_read_b32 v193, a9\n" \
M_"v[76:79], a[184:187], a[132:135], v[76:79]\n" \
"v_accvgpr_read_b32 v194, a10\n" \
"v_accvgpr_read_b32 v195, a11\n" \
M_"v[76:79], a[248:251], a[196:199], v[76:79]\n" \
X_"a[164:165], v33 offset:0\n" \
"v_accvgpr_read_b32 v208, a24\n" \
M_"v[72:75], a[180:183], a[132:135], v[72:75]\n" \
"v_accvgpr_read_b32 v209, a25\n" \
"v_accvgpr_read_b32 v210, a26\n" \
M_"v[72:75], a[244:247], a[196:199], v[72:75]\n" \
"v_accvgpr_read_b32 v211, a27\n" \
"v_accvgpr_read_b32 v212, a28\n" \
M_"v[68:71], a[176:179], a[132:135], v[68:71]\n" \
X_"a[166:167], v33 offset:512\n" \
"v_accvgpr_read_b32 v213, a29\n" \
M_"v[68:71], a[240:243], a[196:199], v[68:71]\n" \
R_"a[132:135], v28 offset:2048\n" \
"v_accvgpr_read_b32 v214, a30\n" \
M_"v[84:87], a[176:179], a[136:139], v[84:87]\n" \
R_"a[196:199], v29 offset:2048\n" \
"v_accvgpr_read_b32 v215, a31\n" \
M_"v[84:87], a[240:243], a[200:203], v[84:87]\n" \
X_"a[228:229], v33 offset:4096\n" \
"v_accvgpr_read_b32 v204, a20\n" \
M_"v[88:91], a[180:183], a[136:139], v[88:91]\n" \
"v_accvgpr_read_b32 v205, a21\n" \
"v_accvgpr_read_b32 v206, a22\n" \
M_"v[88:91], a[244:247], a[200:203], v[88:91]\n" \
"v_accvgpr_read_b32 v207, a23\n" \
"v_accvgpr_read_b32 v200, a16\n" \
M_"v[92:95], a[184:187], a[136:139], v[92:95]\n" \
X_"a[230:231], v33 offset:4608\n" \
"v_accvgpr_read_b32 v201, a17\n" \
M_"v[92:95], a[248:251], a[200:203], v[92:95]\n" \
"v_accvgpr_read_b32 v202, a18\n" \
"v_accvgpr_read_b32 v203, a19\n" \
M_"v[96:99], a[188:191], a[136:139], v[96:99]\n" \
"v_accvgpr_read_b32 v216, a32\n" \
"v_accvgpr_read_b32 v217, a33\n" \
M_"v[96:99], a[252:255], a[200:203], v[96:99]\n" \
R_"a[136:139], v28 offset:4096\n" \
"v_accvgpr_read_b32 v218, a34\n" \
M_"v[112:115], a[188:191], a[140:143], v[112:115]\n" \
R_"a[200:203], v29 offset:4096\n" \
"v_accvgpr_read_b32 v219, a35\n" \
M_"v[112:115], a[252:255], a[204:207], v[112:115]\n" \
X_"a[172:173], v35 offset:0\n" \
"v_accvgpr_read_b32 v220, a36\n" \
M_"v[108:111], a[184:187], a[140:143], v[108:111]\n" \
X_"a[174:175], v35 offset:512\n" \
"v_accvgpr_read_b32 v221, a37\n" \
M_"v[108:111], a[248:251], a[204:207], v[108:111]\n" \
"v_accvgpr_read_b32 v222, a38\n" \
"v_accvgpr_read_b32 v223, a39\n" \
M_"v[104:107], a[180:183], a[140:143], v[104:107]\n" \
X_"a[236:237], v35 offset:4096\n" \
"v_accvgpr_read_b32 v228, a44\n" \
M_"v[104:107], a[244:247], a[204:207], v[104:107]\n" \
"v_accvgpr_read_b32 v229, a45\n" \
"v_accvgpr_read_b32 v230, a46\n" \
M_"v[100:103], a[176:179], a[140:143], v[100:103]\n" \
"v_accvgpr_read_b32 v231, a47\n" \
"v_accvgpr_read_b32 v224, a40\n" \
M_"v[100:103], a[240:243], a[204:207], v[100:103]\n" \
R_"a[140:143], v28 offset:6144\n" \
"v_accvgpr_read_b32 v225, a41\n" \
M_"v[116:119], a[176:179], a[144:147], v[116:119]\n" \
R_"a[204:207], v29 offset:6144\n" \
"v_accvgpr_read_b32 v226, a42\n" \
M_"v[116:119], a[240:243], a[208:211], v[116:119]\n" \
X_"a[238:239], v35 offset:4608\n" \
"v_accvgpr_read_b32 v227, a43\n" \
M_"v[120:123], a[180:183], a[144:147], v[120:123]\n" \
X_"a[168:169], v34 offset:0\n" \
"v_accvgpr_read_b32 v240, a56\n" \
M_"v[120:123], a[244:247], a[208:211], v[120:123]\n" \
"v_accvgpr_read_b32 v241, a57\n" \
"v_accvgpr_read_b32 v242, a58\n" \
M_"v[124:127], a[184:187], a[144:147], v[124:127]\n" \
"v_accvgpr_read_b32 v243, a59\n" \
"v_accvgpr_read_b32 v244, a60\n" \
M_"v[124:127], a[248:251], a[208:211], v[124:127]\n" \
X_"a[170:171], v34 offset:512\n" \
"v_accvgpr_read_b32 v245, a61\n" \
M_"v[128:131], a[188:191], a[144:147], v[128:131]\n" \
"v_accvgpr_read_b32 v246, a62\n" \
"v_accvgpr_read_b32 v247, a63\n" \
M_"v[128:131], a[252:255], a[208:211], v[128:131]\n" \
R_"a[144:147], v28 offset:8192\n" \
"v_accvgpr_read_b32 v236, a52\n" \
M_"v[144:147], a[188:191], a[148:151], v[144:147]\n" \
R_"a[208:211], v29 offset:8192\n" \
"v_accvgpr_read_b32 v237, a53\n" \
M_"v[144:147], a[252:255], a[212:215], v[144:147]\n" \
X_"a[232:233], v34 offset:4096\n" \
"v_accvgpr_read_b32 v238, a54\n" \
M_"v[140:143], a[184:187], a[148:151], v[140:143]\n" \
"v_accvgpr_read_b32 v239, a55\n" \
"v_accvgpr_read_b32 v232, a48\n" \
M_"v[140:143], a[248:251], a[212:215], v[140:143]\n" \
X_"a[234:235], v34 offset:4608\n" \
"v_accvgpr_read_b32 v233, a49\n" \
M_"v[136:139], a[180:183], a[148:151], v[136:139]\n" \
"v_accvgpr_read_b32 v234, a50\n" \
"v_accvgpr_read_b32 v235, a51\n" \
M_"v[136:139], a[244:247], a[212:215], v[136:139]\n" \
"v_accvgpr_read_b32 v180, a64\n" \
"v_accvgpr_read_b32 v181, a65\n" \
M_"v[132:135], a[176:179], a[148:151], v[132:135]\n" \
R_"v[248:251], v22 offset:0\n" \
"v_accvgpr_read_b32 v182, a66\n" \
M_"v[132:135], a[240:243], a[212:215], v[132:135]\n" \
R_"a[148:151], v28 offset:10240\n" \
"v_accvgpr_read_b32 v183, a67\n" \
M_"v[148:151], a[176:179], a[152:155], v[148:151]\n" \
R_"a[212:215], v29 offset:10240\n" \
"v_accvgpr_read_b32 v252, a68\n" \
M_"v[148:151], a[240:243], a[216:219], v[148:151]\n" \
R_"v[48:51], v23 offset:0\n" \
"v_accvgpr_read_b32 v253, a69\n" \
M_"v[152:155], a[180:183], a[152:155], v[152:155]\n" \
"v_accvgpr_read_b32 v254, a70\n" \
"v_accvgpr_read_b32 v255, a71\n" \
M_"v[152:155], a[244:247], a[216:219], v[152:155]\n" \
"v_lshrrev_b32_e32 v24, 9, v20\n" \
"v_and_b32_e32 v26, 0x1ff, v20\n" \
M_"v[156:159], a[184:187], a[152:155], v[156:159]\n" \
"v_lshl_add_u32 v24, v24, 3, v26\n" \
"v_lshrrev_b32_e64 v25, 8, v20\n" \
M_"v[156:159], a[248:251], a[216:219], v[156:159]\n" \
"v_mul_u32_u24_e32 v25, 0x24700, v25\n" \
"v_add_u32_e64 v25, v25, v20\n" \
M_"v[160:163], a[188:191], a[152:155], v[160:163]\n" \
"v_lshrrev_b32_e32 v26, 2, v20\n" \
"v_add_u32_e32 v26, s45, v26\n" \
M_"v[160:163], a[252:255], a[216:219], v[160:163]\n" \
R_"a[152:155], v28 offset:12288\n" \
"v_add_u32_e64 v27, s46, v20\n" \
M_"v[176:179], a[188:191], a[156:159], v[176:179]\n" \
R_"a[216:219], v29 offset:12288\n" \
"v_add_u32_e64 v21, s43, v27\n" \
M_"v[176:179], a[252:255], a[220:223], v[176:179]\n" \
M_"v[172:175], a[184:187], a[156:159], v[172:175]\n" \
M_"v[172:175], a[248:251], a[220:223], v[172:175]\n" \
M_"v[168:171], a[180:183], a[156:159], v[168:171]\n" \
M_"v[168:171], a[244:247], a[220:223], v[168:171]\n" \
M_"v[164:167], a[176:179], a[156:159], v[164:167]\n" \
M_"v[164:167], a[240:243], a[220:223], v[164:167]\n" \
R_"a[156:159], v28 offset:14336\n" \
R_"a[220:223], v29 offset:14336\n" \
"s_waitcnt lgkmcnt(15)\n" \
"s_nop 0\n" \
M_"v[184:187], a[160:163], a[128:131], v[184:187]\n" \
"v_bfe_u32 v44, v20, 2, 5\n" \
X_"a[176:177], v36 offset:0\n" \
M_"v[184:187], a[224:227], a[192:195], v[184:187]\n" \
"buffer_load_dword v44, v44, s[80:83], s38 offen offset:0 sc1\n" \
M_"v[188:191], a[164:167], a[128:131], v[188:191]\n" \
X_"a[178:179], v36 offset:1024\n" \
M_"v[188:191], a[228:231], a[192:195], v[188:191]\n" \
X_"a[240:241], v36 offset:8192\n" \
M_"v[196:199], a[172:175], a[128:131], v[196:199]\n" \
X_"a[242:243], v36 offset:9216\n" \
"s_waitcnt lgkmcnt(15)\n" \
M_"v[196:199], a[236:239], a[192:195], v[196:199]\n" \
M_"v[192:195], a[128:131], a[168:171], v[192:195]\n" \
X_"a[180:181], v37 offset:0\n" \
"s_waitcnt lgkmcnt(13)\n" \
M_"v[192:195], a[192:195], a[232:235], v[192:195]\n" \
M_"v[208:211], a[132:135], a[168:171], v[208:211]\n" \
X_"a[182:183], v37 offset:1024\n" \
M_"v[208:211], a[196:199], a[232:235], v[208:211]\n" \
X_"a[244:245], v37 offset:8192\n" \
M_"v[212:215], a[172:175], a[132:135], v[212:215]\n" \
M_"v[212:215], a[236:239], a[196:199], v[212:215]\n" \
X_"a[246:247], v37 offset:9216\n" \
C_"v188, v188, v189\n" \
M_"v[204:207], a[164:167], a[132:135], v[204:207]\n" \
C_"v189, v190, v191\n" \
C_"v190, v198, v199\n" \
M_"v[204:207], a[228:231], a[196:199], v[204:207]\n" \
X_"a[184:185], v38 offset:0\n" \
"v_mov_b32_e32 v191, 0x3f80\n" \
M_"v[200:203], a[160:163], a[132:135], v[200:203]\n" \
X_"a[186:187], v38 offset:1024\n" \
C_"v184, v184, v185\n" \
M_"v[200:203], a[224:227], a[196:199], v[200:203]\n" \
C_"v185, v186, v187\n" \
C_"v186, v196, v197\n" \
M_"v[216:219], a[160:163], a[136:139], v[216:219]\n" \
X_"a[248:249], v38 offset:8192\n" \
"v_mov_b32_e64 v187, 0\n" \
M_"v[216:219], a[224:227], a[200:203], v[216:219]\n" \
X_"a[250:251], v38 offset:9216\n" \
M_"v[220:223], a[164:167], a[136:139], v[220:223]\n" \
M_"v[220:223], a[228:231], a[200:203], v[220:223]\n" \
X_"a[188:189], v39 offset:0\n" \
C_"v204, v204, v205\n" \
M_"v[228:231], a[172:175], a[136:139], v[228:231]\n" \
C_"v205, v206, v207\n" \
C_"v206, v214, v215\n" \
M_"v[228:231], a[236:239], a[200:203], v[228:231]\n" \
X_"a[190:191], v39 offset:1024\n" \
"v_mov_b32_e32 v207, 0x3f80\n" \
M_"v[224:227], a[136:139], a[168:171], v[224:227]\n" \
X_"a[252:253], v39 offset:8192\n" \
C_"v200, v200, v201\n" \
M_"v[224:227], a[200:203], a[232:235], v[224:227]\n" \
C_"v201, v202, v203\n" \
C_"v202, v212, v213\n" \
M_"v[240:243], a[140:143], a[168:171], v[240:243]\n" \
X_"a[254:255], v39 offset:9216\n" \
"v_mov_b32_e64 v203, 0\n" \
M_"v[240:243], a[204:207], a[232:235], v[240:243]\n" \
M_"v[244:247], a[172:175], a[140:143], v[244:247]\n" \
M_"v[244:247], a[236:239], a[204:207], v[244:247]\n" \
C_"v220, v220, v221\n" \
C_"v221, v222, v223\n" \
M_"v[236:239], a[164:167], a[140:143], v[236:239]\n" \
C_"v222, v230, v231\n" \
"v_mov_b32_e32 v223, 0x3f80\n" \
M_"v[236:239], a[228:231], a[204:207], v[236:239]\n" \
C_"v216, v216, v217\n" \
C_"v217, v218, v219\n" \
M_"v[232:235], a[160:163], a[140:143], v[232:235]\n" \
C_"v218, v228, v229\n" \
"v_mov_b32_e64 v219, 0\n" \
M_"v[232:235], a[224:227], a[204:207], v[232:235]\n" \
"s_waitcnt lgkmcnt(15)\n" \
"s_nop 0\n" \
M_"v[180:183], a[164:167], v[248:251], v[180:183]\n" \
M_"v[180:183], a[228:231], v[48:51], v[180:183]\n" \
M_"a[80:83], a[172:175], v[248:251], a[80:83]\n" \
M_"a[80:83], a[236:239], v[48:51], a[80:83]\n" \
C_"v236, v236, v237\n" \
C_"v237, v238, v239\n" \
M_"v[252:255], v[248:251], a[168:171], v[252:255]\n" \
C_"v238, v246, v247\n" \
"v_mov_b32_e32 v239, 0x3f80\n" \
M_"v[252:255], v[48:51], a[232:235], v[252:255]\n" \
C_"v232, v232, v233\n" \
C_"v233, v234, v235\n" \
"s_waitcnt vmcnt(0) lgkmcnt(0)\n" \
"s_barrier\n" \
"v_mov_b32_e32 v45, s62\n" \
"s_mov_b64 exec, 1\n" \
"buffer_store_dword v45, off, s[80:83], s37 offset:32 sc1\n" \
"s_mov_b64 exec, -1\n" \
"v_cmp_ne_u32_e32 vcc, s62, v44\n" \
"s_nop 4\n" \
"s_cbranch_vccnz L_poll160_%=\n" \
"L_pollr160_%=:\n" \
M_"v[52:55], a[176:179], a[128:131], v[52:55]\n" \
"s_mov_b32 s73, s50\n" \
"s_sub_u32 m0, s31, 0x10000\n" \
M_"v[52:55], a[240:243], a[192:195], v[52:55]\n" \
B_"v20, s[80:83], s73 offen offset:0 sc1 lds\n" \
M_"v[56:59], a[180:183], a[128:131], v[56:59]\n" \
"s_add_u32 s74, s50, 0x4000\n" \
"s_add_u32 s75, s50, 0x8000\n" \
M_"v[56:59], a[244:247], a[192:195], v[56:59]\n" \
"s_mov_b32 s70, 0x3e0293ee\n" \
"s_mov_b32 s76, s52\n" \
M_"v[60:63], a[184:187], a[128:131], v[60:63]\n" \
"v_mul_f32_e32 v52, s70, v52\n" \
"v_mul_f32_e32 v53, s70, v53\n" \
M_"v[60:63], a[248:251], a[192:195], v[60:63]\n" \
B_"v20, s[80:83], s73 offen offset:1024 sc1 lds\n" \
M_"v[64:67], a[188:191], a[128:131], v[64:67]\n" \
"v_mul_f32_e32 v54, s70, v54\n" \
"v_mul_f32_e32 v55, s70, v55\n" \
".fill (L_pb + 36576 - .) / 4, 4, 0xBF800000\n" \
"L_pp9:\n" \
"s_cbranch_execz L_pret\n" \
M_"v[64:67], a[252:255], a[192:195], v[64:67]\n" \
C_"v52, v52, v53\n" \
"v_mul_f32_e32 v56, s70, v56\n" \
M_"v[80:83], a[188:191], a[132:135], v[80:83]\n" \
"v_mul_f32_e32 v60, s70, v60\n" \
"v_mul_f32_e32 v61, s70, v61\n" \
M_"v[80:83], a[252:255], a[196:199], v[80:83]\n" \
B_"v20, s[80:83], s73 offen offset:2048 sc1 lds\n" \
M_"v[76:79], a[184:187], a[132:135], v[76:79]\n" \
"v_mul_f32_e32 v62, s70, v62\n" \
"v_mul_f32_e32 v63, s70, v63\n" \
M_"v[76:79], a[248:251], a[196:199], v[76:79]\n" \
C_"v60, v60, v61\n" \
"v_mul_f32_e64 v57, s70, v57\n" \
M_"v[72:75], a[180:183], a[132:135], v[72:75]\n" \
"v_mul_f32_e32 v58, s70, v58\n" \
"v_mul_f32_e32 v59, s70, v59\n" \
M_"v[72:75], a[244:247], a[196:199], v[72:75]\n" \
B_"v20, s[80:83], s73 offen offset:3072 sc1 lds\n" \
M_"v[68:71], a[176:179], a[132:135], v[68:71]\n" \
"s_sub_u32 m0, s31, s51\n" \
C_"v53, v54, v55\n" \
M_"v[68:71], a[240:243], a[196:199], v[68:71]\n" \
"v_mul_f32_e32 v64, s70, v64\n" \
"v_mul_f32_e64 v65, s70, v65\n" \
M_"v[84:87], a[176:179], a[136:139], v[84:87]\n" \
"v_mul_f32_e32 v66, s70, v66\n" \
"v_mul_f32_e32 v67, s70, v67\n" \
M_"v[84:87], a[240:243], a[200:203], v[84:87]\n" \
B_"v20, s[80:83], s74 offen offset:0 sc1 lds\n" \
M_"v[88:91], a[180:183], a[136:139], v[88:91]\n" \
C_"v61, v62, v63\n" \
C_"v54, v56, v57\n" \
M_"v[88:91], a[244:247], a[200:203], v[88:91]\n" \
C_"v55, v58, v59\n" \
C_"v62, v64, v65\n" \
M_"v[92:95], a[184:187], a[136:139], v[92:95]\n" \
C_"v63, v66, v67\n" \
"v_mul_f32_e64 v68, s70, v68\n" \
M_"v[92:95], a[248:251], a[200:203], v[92:95]\n" \
B_"v20, s[80:83], s74 offen offset:1024 sc1 lds\n" \
M_"v[96:99], a[188:191], a[136:139], v[96:99]\n" \
"ds_write_b128 v27, v[52:55] offset:0\n" \
M_"v[96:99], a[252:255], a[200:203], v[96:99]\n" \
"v_mul_f32_e32 v69, s70, v69\n" \
"v_mul_f32_e32 v76, s70, v76\n" \
M_"v[112:115], a[188:191], a[140:143], v[112:115]\n" \
"v_mul_f32_e32 v77, s70, v77\n" \
"v_mul_f32_e32 v70, s70, v70\n" \
M_"v[112:115], a[252:255], a[204:207], v[112:115]\n" \
B_"v20, s[80:83], s74 offen offset:2048 sc1 lds\n" \
M_"v[108:111], a[184:187], a[140:143], v[108:111]\n" \
"ds_write_b128 v27, v[60:63] offset:1024\n" \
M_"v[108:111], a[248:251], a[204:207], v[108:111]\n" \
"v_mul_f32_e64 v71, s70, v71\n" \
C_"v68, v68, v69\n" \
M_"v[104:107], a[180:183], a[140:143], v[104:107]\n" \
"v_mul_f32_e32 v78, s70, v78\n" \
"v_mul_f32_e32 v79, s70, v79\n" \
M_"v[104:107], a[244:247], a[204:207], v[104:107]\n" \
B_"v20, s[80:83], s74 offen offset:3072 sc1 lds\n" \
M_"v[100:103], a[176:179], a[140:143], v[100:103]\n" \
"s_sub_u32 m0, s31, 0x4000\n" \
C_"v76, v76, v77\n" \
M_"v[100:103], a[240:243], a[204:207], v[100:103]\n" \
"v_mul_f32_e32 v72, s70, v72\n" \
"v_mul_f32_e32 v73, s70, v73\n" \
M_"v[116:119], a[176:179], a[144:147], v[116:119]\n" \
"v_mul_f32_e32 v74, s70, v74\n" \
"v_mul_f32_e32 v75, s70, v75\n" \
M_"v[116:119], a[240:243], a[208:211], v[116:119]\n" \
B_"v20, s[80:83], s75 offen offset:0 sc1 lds\n" \
M_"v[120:123], a[180:183], a[144:147], v[120:123]\n" \
C_"v69, v70, v71\n" \
"v_mul_f32_e64 v80, s70, v80\n" \
M_"v[120:123], a[244:247], a[208:211], v[120:123]\n" \
"v_mul_f32_e32 v81, s70, v81\n" \
"v_mul_f32_e32 v82, s70, v82\n" \
M_"v[124:127], a[184:187], a[144:147], v[124:127]\n" \
"v_mul_f32_e64 v83, s70, v83\n" \
C_"v77, v78, v79\n" \
M_"v[124:127], a[248:251], a[208:211], v[124:127]\n" \
B_"v20, s[80:83], s75 offen offset:1024 sc1 lds\n" \
M_"v[128:131], a[188:191], a[144:147], v[128:131]\n" \
C_"v70, v72, v73\n" \
C_"v71, v74, v75\n" \
M_"v[128:131], a[252:255], a[208:211], v[128:131]\n" \
C_"v78, v80, v81\n" \
C_"v79, v82, v83\n" \
M_"v[144:147], a[188:191], a[148:151], v[144:147]\n" \
"ds_write_b128 v27, v[68:71] offset:4096\n" \
M_"v[144:147], a[252:255], a[212:215], v[144:147]\n" \
B_"v20, s[80:83], s75 offen offset:2048 sc1 lds\n" \
M_"v[140:143], a[184:187], a[148:151], v[140:143]\n" \
C_"v180, v180, v181\n" \
"v_accvgpr_read_b32 v50, a82\n" \
M_"v[140:143], a[248:251], a[212:215], v[140:143]\n" \
"ds_write_b128 v27, v[76:79] offset:5120\n" \
M_"v[136:139], a[180:183], a[148:151], v[136:139]\n" \
"v_accvgpr_read_b32 v51, a83\n" \
C_"v181, v182, v183\n" \
M_"v[136:139], a[244:247], a[212:215], v[136:139]\n" \
B_"v20, s[80:83], s75 offen offset:3072 sc1 lds\n" \
M_"v[132:135], a[176:179], a[148:151], v[132:135]\n" \
"s_mov_b32 m0, s53\n" \
C_"v182, v50, v51\n" \
M_"v[132:135], a[240:243], a[212:215], v[132:135]\n" \
"v_mov_b32_e32 v183, 0x3f80\n" \
C_"v234, v244, v245\n" \
M_"v[148:151], a[176:179], a[152:155], v[148:151]\n" \
"v_mov_b32_e32 v235, 0\n" \
C_"v54, v192, v193\n" \
M_"v[148:151], a[240:243], a[216:219], v[148:151]\n" \
B_"v20, s[80:83], s76 offen offset:0 sc1 lds\n" \
M_"v[152:155], a[180:183], a[152:155], v[152:155]\n" \
C_"v55, v194, v195\n" \
C_"v56, v208, v209\n" \
M_"v[152:155], a[244:247], a[216:219], v[152:155]\n" \
C_"v57, v210, v211\n" \
C_"v58, v224, v225\n" \
C_"v59, v226, v227\n" \
C_"v60, v240, v241\n" \
C_"v61, v242, v243\n" \
M_"v[32:35], v[180:183], v[184:187], 0\n" \
B_"v20, s[80:83], s76 offen offset:1024 sc1 lds\n" \
M_"v[156:159], a[184:187], a[152:155], v[156:159]\n" \
M_"v[192:195], v[188:191], v[184:187], 0\n" \
M_"v[208:211], v[204:207], v[200:203], 0\n" \
M_"v[36:39], v[188:191], v[200:203], 0\n" \
M_"v[224:227], v[220:223], v[216:219], 0\n" \
M_"v[248:251], v[204:207], v[216:219], 0\n" \
M_"v[240:243], v[236:239], v[232:235], 0\n" \
M_"v[64:67], v[220:223], v[232:235], 0\n" \
"s_mov_b32 s64, 0xaaaaaaaa\n" \
"s_mov_b32 s65, 0xaaaaaaaa\n" \
M_"v[156:159], a[248:251], a[216:219], v[156:159]\n" \
"v_cndmask_b32_e64 v32, v32, v33, s[64:65]\n" \
"v_cndmask_b32_e64 v34, v34, v35, s[64:65]\n" \
M_"v[160:163], a[188:191], a[152:155], v[160:163]\n" \
"s_mov_b32 s66, 0xfff0ffff\n" \
"s_mov_b32 s67, 0xf000ff00\n" \
"s_mov_b32 s68, 0xffc0fffc\n" \
"s_mov_b32 s69, 0xc000fc00\n" \
"v_cndmask_b32_e64 v192, v192, v193, s[64:65]\n" \
"v_add_f32_e64 v32, s47, v32\n" \
"v_cndmask_b32_e64 v194, v194, v195, s[64:65]\n" \
"v_add_f32_e64 v34, s47, v34\n" \
"v_cndmask_b32_e64 v208, v208, v209, s[64:65]\n" \
"v_cndmask_b32_e64 v36, v36, v37, s[64:65]\n" \
"v_cndmask_b32_e64 v210, v210, v211, s[64:65]\n" \
"v_cndmask_b32_e64 v38, v38, v39, s[64:65]\n" \
"v_cndmask_b32_e64 v224, v224, v225, s[64:65]\n" \
"v_cndmask_b32_e64 v248, v248, v249, s[64:65]\n" \
"v_cndmask_b32_e64 v226, v226, v227, s[64:65]\n" \
"v_cndmask_b32_e64 v250, v250, v251, s[64:65]\n" \
"v_cndmask_b32_e64 v240, v240, v241, s[64:65]\n" \
"v_cndmask_b32_e64 v64, v64, v65, s[64:65]\n" \
"v_cndmask_b32_e64 v242, v242, v243, s[64:65]\n" \
"v_cndmask_b32_e64 v66, v66, v67, s[64:65]\n" \
"v_cndmask_b32_e64 v192, v32, v192, s[66:67]\n" \
"v_cndmask_b32_e64 v194, v34, v194, s[68:69]\n" \
"v_cndmask_b32_e64 v208, v36, v208, s[66:67]\n" \
"v_cndmask_b32_e64 v210, v38, v210, s[68:69]\n" \
"v_cndmask_b32_e64 v224, v248, v224, s[66:67]\n" \
"v_cndmask_b32_e64 v226, v250, v226, s[68:69]\n" \
"v_cndmask_b32_e64 v240, v64, v240, s[66:67]\n" \
"v_cndmask_b32_e64 v242, v66, v242, s[68:69]\n" \
M_"v[160:163], a[252:255], a[216:219], v[160:163]\n" \
"v_max_f32_e32 v193, v192, v194\n" \
"v_max_f32_e32 v209, v208, v210\n" \
"v_max_f32_e32 v225, v224, v226\n" \
"v_max_f32_e32 v241, v240, v242\n" \
"v_mul_f32_e32 v84, s70, v84\n" \
"v_permlane32_swap_b32_e32 v193, v225\n" \
"v_permlane32_swap_b32_e32 v209, v241\n" \
"v_mul_f32_e32 v85, s70, v85\n" \
"v_mul_f32_e32 v92, s70, v92\n" \
"v_mul_f32_e32 v93, s70, v93\n" \
"v_permlane32_swap_b32_e32 v196, v228\n" \
"v_permlane32_swap_b32_e32 v212, v244\n" \
"v_max_f32_e32 v193, v193, v225\n" \
"v_max_f32_e32 v209, v209, v241\n" \
"v_accvgpr_read_b32 v48, a80\n" \
"v_accvgpr_read_b32 v49, a81\n" \
"v_mul_f32_e32 v86, s70, v86\n" \
"v_mul_f32_e32 v87, s70, v87\n" \
C_"v84, v84, v85\n" \
"v_mul_f32_e32 v94, s70, v94\n" \
"v_mul_f32_e32 v95, s70, v95\n" \
C_"v92, v92, v93\n" \
"v_permlane16_swap_b32_e32 v228, v244\n" \
"v_permlane16_swap_b32_e32 v193, v209\n" \
M_"v[176:179], a[188:191], a[156:159], v[176:179]\n" \
"s_mov_b32 s72, 0x3eae1a93\n" \
"v_mul_f32_e32 v88, s70, v88\n" \
"v_mul_f32_e32 v89, s70, v89\n" \
"v_mul_f32_e32 v90, s70, v90\n" \
"v_mul_f32_e32 v91, s70, v91\n" \
C_"v85, v86, v87\n" \
"v_mul_f32_e32 v96, s70, v96\n" \
"v_mul_f32_e32 v97, s70, v97\n" \
"v_mul_f32_e32 v98, s70, v98\n" \
"v_mul_f32_e32 v99, s70, v99\n" \
C_"v93, v94, v95\n" \
"v_mul_f32_e32 v45, 0xbfb8aa3b, v228\n" \
"v_mfma_f32_16x16x16_bf16 v[48:51], v[182:183], v[46:47], 0\n" \
"v_mfma_f32_16x16x16_bf16 v[196:199], v[190:191], v[46:47], 0\n" \
"v_mfma_f32_16x16x16_bf16 v[212:215], v[206:207], v[46:47], 0\n" \
"v_max_f32_e64 v193, v193, v209\n" \
C_"v86, v88, v89\n" \
C_"v87, v90, v91\n" \
C_"v94, v96, v97\n" \
C_"v95, v98, v99\n" \
"v_mfma_f32_16x16x16_bf16 v[228:231], v[222:223], v[46:47], 0\n" \
"v_mul_f32_e32 v193, s72, v193\n" \
"v_mov_b32_e32 v209, v193\n" \
"ds_write_b128 v27, v[84:87] offset:8192\n" \
"v_mfma_f32_16x16x16_bf16 v[244:247], v[238:239], v[46:47], 0\n" \
"v_permlane16_swap_b32_e32 v193, v209\n" \
"s_mov_b32 s71, 0x3e0293ee\n" \
M_"v[176:179], a[252:255], a[220:223], v[176:179]\n" \
"v_mov_b32_e32 v225, v193\n" \
"v_mov_b32_e32 v241, v209\n" \
"v_mul_f32_e32 v100, s70, v100\n" \
"v_mul_f32_e64 v101, s70, v101\n" \
"ds_write_b128 v27, v[92:95] offset:9216\n" \
"v_mul_f32_e32 v108, s70, v108\n" \
"v_mul_f32_e32 v109, s70, v109\n" \
"v_pk_mul_f32 v[116:117], v[116:117], s[70:71]\n" \
"v_pk_mul_f32 v[124:125], v[124:125], s[70:71]\n" \
"v_pk_mul_f32 v[132:133], v[132:133], s[70:71]\n" \
"v_pk_mul_f32 v[140:141], v[140:141], s[70:71]\n" \
"v_pk_mul_f32 v[148:149], v[148:149], s[70:71]\n" \
"v_pk_mul_f32 v[156:157], v[156:157], s[70:71]\n" \
"v_permlane32_swap_b32_e32 v193, v225\n" \
"v_permlane32_swap_b32_e32 v209, v241\n" \
"v_mul_f32_e32 v102, s70, v102\n" \
"v_mul_f32_e32 v103, s70, v103\n" \
C_"v100, v100, v101\n" \
"v_mul_f32_e32 v110, s70, v110\n" \
"v_mul_f32_e32 v111, s70, v111\n" \
C_"v108, v108, v109\n" \
"v_pk_mul_f32 v[118:119], v[118:119], s[70:71]\n" \
C_"v116, v116, v117\n" \
"v_pk_mul_f32 v[126:127], v[126:127], s[70:71]\n" \
C_"v124, v124, v125\n" \
"v_pk_mul_f32 v[134:135], v[134:135], s[70:71]\n" \
C_"v132, v132, v133\n" \
"v_pk_mul_f32 v[142:143], v[142:143], s[70:71]\n" \
C_"v140, v140, v141\n" \
"v_pk_mul_f32 v[150:151], v[150:151], s[70:71]\n" \
C_"v148, v148, v149\n" \
"v_pk_mul_f32 v[158:159], v[158:159], s[70:71]\n" \
C_"v156, v156, v157\n" \
"v_fma_f32 v192, v192, s72, -v193\n" \
"v_fma_f32 v194, v194, s72, -v193\n" \
"v_fma_f32 v208, v208, s72, -v209\n" \
"v_fma_f32 v210, v210, s72, -v209\n" \
"v_mul_f32_e32 v104, s70, v104\n" \
"v_mul_f32_e32 v105, s70, v105\n" \
"v_mul_f32_e32 v106, s70, v106\n" \
"v_mul_f32_e32 v107, s70, v107\n" \
C_"v101, v102, v103\n" \
"v_mul_f32_e32 v112, s70, v112\n" \
"v_mul_f32_e32 v113, s70, v113\n" \
"v_mul_f32_e32 v114, s70, v114\n" \
"v_mul_f32_e32 v115, s70, v115\n" \
C_"v109, v110, v111\n" \
"v_pk_mul_f32 v[120:121], v[120:121], s[70:71]\n" \
"v_pk_mul_f32 v[122:123], v[122:123], s[70:71]\n" \
C_"v117, v118, v119\n" \
"v_pk_mul_f32 v[128:129], v[128:129], s[70:71]\n" \
"v_pk_mul_f32 v[130:131], v[130:131], s[70:71]\n" \
C_"v125, v126, v127\n" \
"v_pk_mul_f32 v[136:137], v[136:137], s[70:71]\n" \
"v_pk_mul_f32 v[138:139], v[138:139], s[70:71]\n" \
C_"v133, v134, v135\n" \
"v_pk_mul_f32 v[144:145], v[144:145], s[70:71]\n" \
"v_pk_mul_f32 v[146:147], v[146:147], s[70:71]\n" \
C_"v141, v142, v143\n" \
"v_pk_mul_f32 v[152:153], v[152:153], s[70:71]\n" \
"v_pk_mul_f32 v[154:155], v[154:155], s[70:71]\n" \
C_"v149, v150, v151\n" \
"v_pk_mul_f32 v[160:161], v[160:161], s[70:71]\n" \
"v_pk_mul_f32 v[162:163], v[162:163], s[70:71]\n" \
C_"v157, v158, v159\n" \
"v_pk_mul_f32 v[176:177], v[176:177], s[70:71]\n" \
"v_pk_mul_f32 v[178:179], v[178:179], s[70:71]\n" \
E_"v192, v192\n" \
E_"v194, v194\n" \
E_"v208, v208\n" \
E_"v210, v210\n" \
"v_fma_f32 v224, v224, s72, -v225\n" \
"v_fma_f32 v226, v226, s72, -v225\n" \
M_"v[172:175], a[184:187], a[156:159], v[172:175]\n" \
C_"v102, v104, v105\n" \
C_"v103, v106, v107\n" \
C_"v110, v112, v113\n" \
C_"v111, v114, v115\n" \
C_"v118, v120, v121\n" \
C_"v119, v122, v123\n" \
C_"v126, v128, v129\n" \
C_"v127, v130, v131\n" \
C_"v134, v136, v137\n" \
C_"v135, v138, v139\n" \
C_"v142, v144, v145\n" \
C_"v143, v146, v147\n" \
C_"v150, v152, v153\n" \
C_"v151, v154, v155\n" \
C_"v158, v160, v161\n" \
C_"v159, v162, v163\n" \
E_"v224, v224\n" \
E_"v226, v226\n" \
"v_fma_f32 v240, v240, s72, -v241\n" \
"v_fma_f32 v242, v242, s72, -v241\n" \
"ds_write_b128 v27, v[100:103] offset:12288\n" \
C_"v192, v192, v194\n" \
C_"v208, v208, v210\n" \
E_"v240, v240\n" \
E_"v242, v242\n" \
C_"v68, v48, v49\n" \
C_"v69, v50, v51\n" \
C_"v70, v196, v197\n" \
C_"v71, v198, v199\n" \
"ds_write_b128 v27, v[108:111] offset:13312\n" \
C_"v72, v212, v213\n" \
C_"v73, v214, v215\n" \
"v_perm_b32 v184, v192, v192, v40\n" \
"v_perm_b32 v185, v192, v192, v41\n" \
"v_perm_b32 v186, v192, v192, v42\n" \
"v_perm_b32 v187, v192, v192, v43\n" \
"v_perm_b32 v200, v208, v208, v40\n" \
"v_perm_b32 v201, v208, v208, v41\n" \
"v_perm_b32 v202, v208, v208, v42\n" \
"v_perm_b32 v203, v208, v208, v43\n" \
"ds_write_b128 v21, v[116:119] offset:16384\n" \
C_"v224, v224, v226\n" \
C_"v74, v228, v229\n" \
C_"v75, v230, v231\n" \
M_"v[32:35], v[68:71], v[184:187], 0\n" \
M_"v[36:39], v[70:73], v[200:203], 0\n" \
"v_perm_b32 v216, v224, v224, v40\n" \
"v_perm_b32 v217, v224, v224, v41\n" \
"ds_write_b128 v21, v[124:127] offset:17408\n" \
"v_perm_b32 v218, v224, v224, v42\n" \
"v_perm_b32 v219, v224, v224, v43\n" \
C_"v240, v240, v242\n" \
M_"v[172:175], a[248:251], a[220:223], v[172:175]\n" \
C_"v76, v244, v245\n" \
C_"v77, v246, v247\n" \
M_"v[248:251], v[72:75], v[216:219], 0\n" \
"v_perm_b32 v232, v240, v240, v40\n" \
"v_perm_b32 v233, v240, v240, v41\n" \
"ds_write_b128 v21, v[132:135] offset:20480\n" \
"v_perm_b32 v234, v240, v240, v42\n" \
"v_perm_b32 v235, v240, v240, v43\n" \
"v_pk_mul_f32 v[172:173], v[172:173], s[70:71]\n" \
"v_pk_mul_f32 v[174:175], v[174:175], s[70:71]\n" \
C_"v172, v172, v173\n" \
C_"v173, v174, v175\n" \
C_"v174, v176, v177\n" \
C_"v175, v178, v179\n" \
M_"v[64:67], v[74:77], v[232:235], 0\n" \
"ds_write_b128 v21, v[140:143] offset:21504\n" \
M_"v[168:171], a[180:183], a[156:159], v[168:171]\n" \
C_"v52, v252, v253\n" \
C_"v53, v254, v255\n" \
M_"v[168:171], a[244:247], a[220:223], v[168:171]\n" \
"v_exp_f32_e64 v45, v45\n" \
M_"v[192:195], v[52:55], v[184:187], 0\n" \
"ds_write_b128 v21, v[148:151] offset:24576\n" \
M_"v[208:211], v[54:57], v[200:203], 0\n" \
M_"v[224:227], v[56:59], v[216:219], 0\n" \
M_"v[240:243], v[58:61], v[232:235], 0\n" \
"ds_write_b128 v21, v[156:159] offset:25600\n" \
"v_mov_b32_dpp v34, v38 quad_perm:[0,1,2,3] row_mask:0x2 bank_mask:0xf\n" \
"v_pk_mul_f32 v[168:169], v[168:169], s[70:71]\n" \
"v_pk_mul_f32 v[170:171], v[170:171], s[70:71]\n" \
"v_mov_b32_dpp v34, v250 quad_perm:[0,1,2,3] row_mask:0x4 bank_mask:0xf\n" \
"v_add_f32_e64 v45, 1.0, v45\n" \
M_"v[164:167], a[176:179], a[156:159], v[164:167]\n" \
"v_mov_b32_dpp v34, v66 quad_perm:[0,1,2,3] row_mask:0x8 bank_mask:0xf\n" \
"v_mov_b32_dpp v32, v36 quad_perm:[0,1,2,3] row_mask:0x2 bank_mask:0xf\n" \
"v_rcp_f32_e64 v45, v45\n" \
"ds_write_b128 v21, v[172:175] offset:29696\n" \
"v_mov_b32_dpp v33, v37 quad_perm:[0,1,2,3] row_mask:0x2 bank_mask:0xf\n" \
"v_rcp_f32_e64 v34, v34\n" \
"v_mov_b32_dpp v32, v248 quad_perm:[0,1,2,3] row_mask:0x4 bank_mask:0xf\n" \
"v_mov_b32_dpp v33, v249 quad_perm:[0,1,2,3] row_mask:0x4 bank_mask:0xf\n" \
"v_mul_f32_e64 v34, v45, v34\n" \
"v_mov_b32_dpp v32, v64 quad_perm:[0,1,2,3] row_mask:0x8 bank_mask:0xf\n" \
"v_mov_b32_dpp v33, v65 quad_perm:[0,1,2,3] row_mask:0x8 bank_mask:0xf\n" \
"v_mul_f32_e32 v32, v34, v32\n" \
"v_mul_f32_e32 v33, v34, v33\n" \
"v_mov_b32_e64 v38, v34\n" \
M_"v[164:167], a[240:243], a[220:223], v[164:167]\n" \
"v_sub_f32_e32 v44, 1.0, v45\n" \
"v_permlane16_swap_b32_e32 v34, v38\n" \
"v_mov_b32_e32 v250, v34\n" \
"v_mov_b32_e32 v66, v38\n" \
C_"v32, v32, v33\n" \
"v_permlane32_swap_b32_e32 v34, v250\n" \
"v_permlane32_swap_b32_e32 v38, v66\n" \
"v_mul_f32_e64 v192, v34, v192\n" \
"v_pk_mul_f32 v[164:165], v[164:165], s[70:71]\n" \
"v_pk_mul_f32 v[166:167], v[166:167], s[70:71]\n" \
C_"v164, v164, v165\n" \
C_"v165, v166, v167\n" \
C_"v166, v168, v169\n" \
C_"v167, v170, v171\n" \
"ds_write_b128 v21, v[164:167] offset:28672\n" \
"v_mul_f32_e32 v193, v34, v193\n" \
"v_mul_f32_e32 v208, v38, v208\n" \
"v_mul_f32_e32 v209, v38, v209\n" \
"v_mul_f32_e32 v224, v250, v224\n" \
"v_mul_f32_e32 v225, v250, v225\n" \
"v_mul_f32_e32 v240, v66, v240\n" \
"v_mul_f32_e32 v241, v66, v241\n" \
"v_mul_f32_e32 v194, v34, v194\n" \
"v_mul_f32_e64 v195, v34, v195\n" \
C_"v192, v192, v193\n" \
"v_mul_f32_e32 v210, v38, v210\n" \
"v_mul_f32_e32 v211, v38, v211\n" \
C_"v208, v208, v209\n" \
"v_mul_f32_e32 v226, v250, v226\n" \
"v_mul_f32_e32 v227, v250, v227\n" \
C_"v224, v224, v225\n" \
"v_mul_f32_e32 v242, v66, v242\n" \
"v_mul_f32_e32 v243, v66, v243\n" \
C_"v240, v240, v241\n" \
C_"v193, v194, v195\n" \
"s_add_u32 s84, s48, 0x0\n" \
"s_nop 0\n" \
C_"v209, v210, v211\n" \
"s_add_u32 s85, s48, 0x24800\n" \
C_"v225, v226, v227\n" \
"s_add_u32 s86, s48, 0x49000\n" \
C_"v241, v242, v243\n" \
"s_add_u32 s87, s48, 0x6d800\n" \
"ds_write_b32 v26, v44\n" \
"buffer_store_dword v32, v25, s[80:83], s49 offen sc1\n" \
"buffer_store_dwordx2 v[192:193], v24, s[80:83], s84 offen sc1\n" \
"buffer_store_dwordx2 v[208:209], v24, s[80:83], s85 offen sc1\n" \
"buffer_store_dwordx2 v[224:225], v24, s[80:83], s86 offen sc1\n" \
"buffer_store_dwordx2 v[240:241], v24, s[80:83], s87 offen sc1\n" \
"s_branch L_end_%=\n" \
"L_poll60_%=:\n" \
"s_mov_b32 s84, 0\n" \
"L_pl60_%=:\n" \
"s_sleep 1\n" \
"v_bfe_u32 v44, v20, 2, 5\n" \
"buffer_load_dword v44, v44, s[80:83], s38 offen offset:128 sc1\n" \
"s_waitcnt vmcnt(0)\n" \
"v_cmp_ne_u32_e32 vcc, s62, v44\n" \
"s_add_u32 s84, s84, 1\n" \
"s_nop 3\n" \
"s_cbranch_vccz L_pollr60_%=\n" \
"s_cmp_gt_u32 s84, 0x400000\n" \
"s_cbranch_scc0 L_pl60_%=\n" \
"s_branch L_pollr60_%=\n" \
"L_poll160_%=:\n" \
"s_mov_b32 s84, 0\n" \
"L_pl160_%=:\n" \
"s_sleep 1\n" \
"v_bfe_u32 v44, v20, 2, 5\n" \
"buffer_load_dword v44, v44, s[80:83], s38 offen offset:0 sc1\n" \
"s_waitcnt vmcnt(0)\n" \
"v_cmp_ne_u32_e32 vcc, s62, v44\n" \
"s_add_u32 s84, s84, 1\n" \
"s_nop 3\n" \
"s_cbranch_vccz L_pollr160_%=\n" \
"s_cmp_gt_u32 s84, 0x400000\n" \
"s_cbranch_scc0 L_pl160_%=\n" \
"s_branch L_pollr160_%=\n" \
".fill (L_pb + 42552 - .) / 4, 4, 0xBF800000\n" \
"L_w1_%=:\n" \
"s_waitcnt vmcnt(19)\n" \
"s_barrier\n" \
X_"a[160:161], v32 offset:0\n" \
X_"a[162:163], v32 offset:1024\n" \
R_"a[128:131], v28 offset:16384\n" \
X_"a[224:225], v32 offset:8192\n" \
X_"a[226:227], v32 offset:9216\n" \
R_"a[192:195], v29 offset:16384\n" \
R_"a[132:135], v28 offset:18432\n" \
R_"a[196:199], v29 offset:18432\n" \
X_"a[164:165], v33 offset:0\n" \
X_"a[166:167], v33 offset:1024\n" \
X_"a[228:229], v33 offset:8192\n" \
X_"a[230:231], v33 offset:9216\n" \
R_"a[136:139], v28 offset:20480\n" \
R_"a[200:203], v29 offset:20480\n" \
"s_waitcnt lgkmcnt(11)\n" \
M_"a[0:3], a[128:131], a[160:163], 0\n" \
X_"a[168:169], v34 offset:0\n" \
"s_lshr_b32 s4, s2, 1\n" \
"s_waitcnt lgkmcnt(9)\n" \
M_"a[0:3], a[192:195], a[224:227], a[0:3]\n" \
X_"a[170:171], v34 offset:1024\n" \
"s_and_b32 s4, s4, 0x7c\n" \
"s_waitcnt lgkmcnt(9)\n" \
M_"a[16:19], a[132:135], a[160:163], 0\n" \
X_"a[232:233], v34 offset:8192\n" \
"v_mov_b32_e32 v213, s13\n" \
"s_waitcnt lgkmcnt(9)\n" \
M_"a[16:19], a[196:199], a[224:227], a[16:19]\n" \
X_"a[234:235], v34 offset:9216\n" \
"s_or_b32 s4, s4, s3\n" \
"s_waitcnt lgkmcnt(8)\n" \
M_"a[20:23], a[132:135], a[164:167], 0\n" \
R_"a[140:143], v28 offset:22528\n" \
"v_lshrrev_b32_e32 v212, 4, v20\n" \
"s_waitcnt lgkmcnt(7)\n" \
M_"a[20:23], a[196:199], a[228:231], a[20:23]\n" \
R_"a[204:207], v29 offset:22528\n" \
"v_mad_u64_u32 v[214:215], s[4:5], s4, 36, v[212:213]\n" \
M_"a[4:7], a[128:131], a[164:167], 0\n" \
X_"a[172:173], v35 offset:0\n" \
"v_mov_b32_e64 v217, s9\n" \
M_"a[4:7], a[192:195], a[228:231], a[4:7]\n" \
X_"a[174:175], v35 offset:1024\n" \
"s_movk_i32 s4, 0x240\n" \
"s_waitcnt lgkmcnt(9)\n" \
M_"a[32:35], a[136:139], a[160:163], 0\n" \
X_"a[236:237], v35 offset:8192\n" \
"v_cmp_gt_u32_e32 vcc, s4, v20\n" \
"s_waitcnt lgkmcnt(9)\n" \
M_"a[32:35], a[200:203], a[224:227], a[32:35]\n" \
X_"a[238:239], v35 offset:9216\n" \
"s_lshl_b32 s4, s2, 3\n" \
M_"a[36:39], a[136:139], a[164:167], 0\n" \
R_"a[144:147], v28 offset:0\n" \
"s_and_b32 s4, s4, 56\n" \
M_"a[36:39], a[200:203], a[228:231], a[36:39]\n" \
R_"a[208:211], v29 offset:0\n" \
"v_cndmask_b32_e32 v217, v217, v213, vcc\n" \
"s_waitcnt lgkmcnt(10)\n" \
M_"a[40:43], a[136:139], a[168:171], 0\n" \
R_"a[148:151], v28 offset:2048\n" \
"v_lshlrev_b32_e32 v213, 3, v214\n" \
"s_waitcnt lgkmcnt(9)\n" \
M_"a[40:43], a[200:203], a[232:235], a[40:43]\n" \
R_"a[212:215], v29 offset:2048\n" \
"v_and_b32_e32 v213, 0x1ffc0, v213\n" \
M_"a[24:27], a[132:135], a[168:171], 0\n" \
R_"a[152:155], v28 offset:4096\n" \
"v_and_b32_e64 v214, 7, v214\n" \
M_"a[24:27], a[196:199], a[232:235], a[24:27]\n" \
R_"a[216:219], v29 offset:4096\n" \
"v_or3_b32 v213, v214, s4, v213\n" \
M_"a[8:11], a[128:131], a[168:171], 0\n" \
R_"a[156:159], v28 offset:6144\n" \
"s_lshl_b32 s4, s2, 2\n" \
M_"a[8:11], a[192:195], a[232:235], a[8:11]\n" \
R_"a[220:223], v29 offset:6144\n" \
"s_mov_b32 m0, s32\n" \
"s_waitcnt lgkmcnt(13)\n" \
M_"a[48:51], a[140:143], a[160:163], 0\n" \
"s_mov_b32 exec_lo, 0xff000000\n" \
"s_mov_b32 exec_hi, 0x0\n" \
"global_load_lds_dword v[184:185], off\n" \
"s_mov_b64 exec, -1\n" \
"s_waitcnt lgkmcnt(12)\n" \
M_"a[48:51], a[204:207], a[224:227], a[48:51]\n" \
X_"a[176:177], v36 offset:0\n" \
"s_or_b32 s4, s4, s3\n" \
M_"a[52:55], a[140:143], a[164:167], 0\n" \
X_"a[178:179], v36 offset:1024\n" \
"s_mul_i32 s4, s4, 18\n" \
M_"a[52:55], a[204:207], a[228:231], a[52:55]\n" \
"s_mov_b32 m0, s32\n" \
X_"a[240:241], v36 offset:8192\n" \
M_"a[56:59], a[140:143], a[168:171], 0\n" \
"s_mov_b32 exec_lo, 0xffff\n" \
"s_mov_b32 exec_hi, 0x0\n" \
"global_load_lds_dword v[186:187], off\n" \
"s_mov_b64 exec, -1\n" \
M_"a[56:59], a[204:207], a[232:235], a[56:59]\n" \
X_"a[242:243], v36 offset:9216\n" \
"v_min_u32_e32 v212, 53, v212\n" \
"s_waitcnt lgkmcnt(14)\n" \
M_"a[60:63], a[140:143], a[172:175], 0\n" \
X_"a[180:181], v37 offset:0\n" \
"s_sub_i32 s4, s4, 36\n" \
"s_waitcnt lgkmcnt(13)\n" \
M_"a[60:63], a[204:207], a[236:239], a[60:63]\n" \
X_"a[182:183], v37 offset:1024\n" \
"v_add_u32_e32 v212, s4, v212\n" \
M_"a[44:47], a[136:139], a[172:175], 0\n" \
X_"a[244:245], v37 offset:8192\n" \
"v_mov_b32_e64 v215, s12\n" \
M_"a[44:47], a[200:203], a[236:239], a[44:47]\n" \
X_"a[246:247], v37 offset:9216\n" \
"v_mov_b32_e64 v216, s8\n" \
M_"a[28:31], a[132:135], a[172:175], 0\n" \
X_"a[184:185], v38 offset:0\n" \
"v_cndmask_b32_e64 v212, v212, v213, vcc\n" \
M_"a[28:31], a[196:199], a[236:239], a[28:31]\n" \
X_"a[186:187], v38 offset:1024\n" \
"v_cndmask_b32_e64 v216, v216, v215, vcc\n" \
M_"a[12:15], a[128:131], a[172:175], 0\n" \
"v_lshlrev_b32_e32 v212, 7, v212\n" \
"v_mov_b32_e32 v213, 0\n" \
M_"a[12:15], a[192:195], a[236:239], a[12:15]\n" \
"v_lshl_add_u64 v[188:189], v[216:217], 0, v[212:213]\n" \
"s_movk_i32 s5, 0xd80\n" \
"s_waitcnt lgkmcnt(15)\n" \
M_"a[76:79], a[144:147], a[172:175], 0\n" \
X_"a[248:249], v38 offset:8192\n" \
"s_lshr_b32 s6, s2, 5\n" \
M_"a[76:79], a[208:211], a[236:239], a[76:79]\n" \
"v_lshrrev_b32_e32 v212, 4, v20\n" \
"v_lshl_or_b32 v215, s3, 6, v212\n" \
M_"a[72:75], a[144:147], a[168:171], 0\n" \
"v_mul_u32_u24_e32 v216, 0x2aab, v215\n" \
"v_lshrrev_b32_e64 v216, 16, v216\n" \
M_"a[72:75], a[208:211], a[232:235], a[72:75]\n" \
X_"a[250:251], v38 offset:9216\n" \
"v_mad_i32_i24 v217, v216, -6, v215\n" \
M_"a[68:71], a[144:147], a[164:167], 0\n" \
"v_add_u32_e32 v218, 0xfffffe50, v215\n" \
"s_mul_i32 s35, s6, 0x48\n" \
M_"a[68:71], a[208:211], a[228:231], a[68:71]\n" \
"v_lshrrev_b32_e32 v219, 1, v217\n" \
"v_add_u32_e32 v218, s35, v216\n" \
M_"a[64:67], a[144:147], a[160:163], 0\n" \
"v_mul_u32_u24_e32 v219, 0x480, v219\n" \
"v_and_b32_e64 v214, 16, v20\n" \
M_"a[64:67], a[208:211], a[224:227], a[64:67]\n" \
X_"a[188:189], v39 offset:0\n" \
"v_mad_u32_u24 v219, v218, s5, v219\n" \
M_"a[80:83], a[148:151], a[160:163], 0\n" \
"v_or_b32_e32 v192, v219, v214\n" \
"v_mov_b32_e32 v220, 2.0\n" \
"s_waitcnt lgkmcnt(15)\n" \
M_"a[80:83], a[212:215], a[224:227], a[80:83]\n" \
"v_mov_b32_e32 v222, 7\n" \
"v_lshlrev_b32_e64 v220, 7, v218\n" \
M_"a[84:87], a[148:151], a[164:167], 0\n" \
X_"a[190:191], v39 offset:1024\n" \
"v_lshrrev_b32_e64 v218, 1, v218\n" \
M_"a[84:87], a[212:215], a[228:231], a[84:87]\n" \
"v_and_b32_e32 v218, 4, v218\n" \
"v_and_b32_e32 v216, 2, v216\n" \
M_"a[88:91], a[148:151], a[168:171], 0\n" \
"s_mul_i32 s7, s6, 0x2400\n" \
"s_sub_i32 s34, 0x22000, s7\n" \
M_"a[88:91], a[212:215], a[232:235], a[88:91]\n" \
X_"a[252:253], v39 offset:8192\n" \
"v_bitop3_b32 v216, v217, v218, v216 bitop3:0x1e\n" \
M_"a[92:95], a[148:151], a[172:175], 0\n" \
"v_or_b32_e32 v213, 0x24400, v20\n" \
"v_lshlrev_b32_e64 v216, 4, v216\n" \
M_"a[92:95], a[212:215], a[236:239], a[92:95]\n" \
"v_add3_u32 v196, v220, s34, v216\n" \
"v_mov_b32_e64 v216, v213\n" \
M_"a[108:111], a[152:155], a[172:175], 0\n" \
X_"a[254:255], v39 offset:9216\n" \
"s_movk_i32 s36, 0x1b0\n" \
M_"a[108:111], a[216:219], a[236:239], a[108:111]\n" \
"v_or_b32_e32 v216, 0x100, v215\n" \
"v_mul_u32_u24_e32 v217, 0x2aab, v216\n" \
M_"a[104:107], a[152:155], a[168:171], 0\n" \
"v_add_u32_e32 v215, 0xffffff50, v215\n" \
"v_lshrrev_b32_e32 v217, 16, v217\n" \
M_"a[104:107], a[216:219], a[232:235], a[104:107]\n" \
"v_mov_b32_e64 v220, v215\n" \
"v_mad_i32_i24 v218, v217, -6, v216\n" \
M_"a[100:103], a[152:155], a[164:167], 0\n" \
"v_cmp_gt_u32_e32 vcc, s36, v216\n" \
"v_mov_b32_e32 v221, 7\n" \
M_"a[100:103], a[216:219], a[228:231], a[100:103]\n" \
"v_lshrrev_b32_e32 v222, 1, v218\n" \
"v_cndmask_b32_e32 v217, v220, v217, vcc\n" \
M_"a[96:99], a[152:155], a[160:163], 0\n" \
"s_movk_i32 s4, 0x48\n" \
"v_add_u32_e32 v220, s35, v217\n" \
M_"a[96:99], a[216:219], a[224:227], a[96:99]\n" \
"v_mul_u32_u24_e32 v222, 0x480, v222\n" \
"v_mad_u32_u24 v222, v220, s5, v222\n" \
"s_waitcnt lgkmcnt(15)\n" \
M_"a[112:115], a[156:159], a[160:163], 0\n" \
"v_cmp_gt_u32_e64 s[4:5], s4, v215\n" \
"v_cndmask_b32_e32 v215, v221, v218, vcc\n" \
M_"a[112:115], a[220:223], a[224:227], a[112:115]\n" \
"v_lshrrev_b32_e32 v218, 1, v220\n" \
"v_or_b32_e32 v214, v222, v214\n" \
M_"a[116:119], a[156:159], a[164:167], 0\n" \
"v_mov_b32_e32 v222, 2.0\n" \
"v_and_b32_e32 v218, 4, v218\n" \
M_"a[116:119], a[220:223], a[228:231], a[116:119]\n" \
"v_and_b32_e64 v217, 2, v217\n" \
"v_bitop3_b32 v215, v215, v218, v217 bitop3:0x1e\n" \
M_"a[120:123], a[156:159], a[168:171], 0\n" \
"v_lshlrev_b32_e32 v223, 1, v220\n" \
"v_mov_b32_e32 v224, 2.0\n" \
M_"a[120:123], a[220:223], a[232:235], a[120:123]\n" \
"v_cndmask_b32_e32 v193, v222, v214, vcc\n" \
"v_lshlrev_b32_e32 v214, 7, v220\n" \
M_"a[124:127], a[156:159], a[172:175], 0\n" \
"v_lshlrev_b32_e32 v215, 4, v215\n" \
"s_mul_i32 s39, s3, 18\n" \
"s_waitcnt vmcnt(5) lgkmcnt(0)\n" \
M_"a[124:127], a[220:223], a[236:239], a[124:127]\n" \
"s_barrier\n" \
M_"v[52:55], a[128:131], a[176:179], 0\n" \
"s_add_u32 m0, s30, 0x0\n" \
"s_movk_i32 s84, 0x100\n" \
M_"v[52:55], a[192:195], a[240:243], v[52:55]\n" \
B_"v22, s[64:67], s84 offen offset:0 lds\n" \
M_"v[56:59], a[128:131], a[180:183], 0\n" \
X_"a[160:161], v32 offset:32768\n" \
"v_add3_u32 v214, v214, s34, v215\n" \
M_"v[56:59], a[192:195], a[244:247], v[56:59]\n" \
"s_movk_i32 s84, 0x1d00\n" \
M_"v[60:63], a[128:131], a[184:187], 0\n" \
B_"v23, s[64:67], s84 offen offset:1024 lds\n" \
M_"v[60:63], a[192:195], a[248:251], v[60:63]\n" \
X_"a[162:163], v32 offset:33792\n" \
"v_cndmask_b32_e64 v195, v224, v223, s[4:5]\n" \
M_"v[64:67], a[128:131], a[188:191], 0\n" \
X_"a[224:225], v32 offset:40960\n" \
"s_movk_i32 s4, 0x1f8\n" \
M_"v[64:67], a[192:195], a[252:255], v[64:67]\n" \
R_"a[128:131], v28 offset:49152\n" \
"v_mov_b32_e64 v215, v213\n" \
M_"v[80:83], a[132:135], a[188:191], 0\n" \
R_"a[192:195], v29 offset:49152\n" \
"s_add_i32 s35, s35, s39\n" \
M_"v[80:83], a[196:199], a[252:255], v[80:83]\n" \
"s_movk_i32 s84, 0x3900\n" \
M_"v[76:79], a[132:135], a[184:187], 0\n" \
B_"v22, s[64:67], s84 offen offset:2048 lds\n" \
M_"v[76:79], a[196:199], a[248:251], v[76:79]\n" \
"s_movk_i32 s84, 0x5500\n" \
M_"v[72:75], a[132:135], a[180:183], 0\n" \
B_"v23, s[64:67], s84 offen offset:3072 lds\n" \
M_"v[72:75], a[196:199], a[244:247], v[72:75]\n" \
"s_add_u32 m0, s31, 0x0\n" \
"s_mov_b32 s84, 0x100000\n" \
M_"v[68:71], a[132:135], a[176:179], 0\n" \
B_"v26, s[68:71], s84 offen offset:0 lds\n" \
M_"v[68:71], a[196:199], a[240:243], v[68:71]\n" \
R_"a[132:135], v28 offset:51200\n" \
"v_cmp_gt_u32_e64 vcc, s4, v216\n" \
M_"v[84:87], a[136:139], a[176:179], 0\n" \
R_"a[196:199], v29 offset:51200\n" \
"v_add_u32_e64 v212, s35, v212\n" \
M_"v[84:87], a[200:203], a[240:243], v[84:87]\n" \
"s_addk_i32 s84, 0x7c00\n" \
M_"v[88:91], a[136:139], a[180:183], 0\n" \
B_"v26, s[68:71], s84 offen offset:1024 lds\n" \
M_"v[88:91], a[200:203], a[244:247], v[88:91]\n" \
X_"a[226:227], v32 offset:41984\n" \
"v_mov_b32_e32 v219, 2.0\n" \
M_"v[92:95], a[136:139], a[184:187], 0\n" \
"s_addk_i32 s84, 0x7c00\n" \
M_"v[92:95], a[200:203], a[248:251], v[92:95]\n" \
B_"v27, s[68:71], s84 offen offset:2048 lds\n" \
M_"v[96:99], a[136:139], a[188:191], 0\n" \
X_"a[164:165], v33 offset:32768\n" \
"v_cndmask_b32_e32 v197, v215, v214, vcc\n" \
M_"v[96:99], a[200:203], a[252:255], v[96:99]\n" \
R_"a[136:139], v28 offset:53248\n" \
"s_movk_i32 s35, 0x120\n" \
M_"v[112:115], a[140:143], a[188:191], 0\n" \
R_"a[200:203], v29 offset:53248\n" \
"v_mul_u32_u24_e32 v214, 0xd80, v212\n" \
M_"v[112:115], a[204:207], a[252:255], v[112:115]\n" \
"s_addk_i32 s84, 0x7c00\n" \
M_"v[108:111], a[140:143], a[184:187], 0\n" \
B_"v27, s[68:71], s84 offen offset:3072 lds\n" \
M_"v[108:111], a[204:207], a[248:251], v[108:111]\n" \
"s_add_u32 m0, s30, 0x1000\n" \
"s_mov_b32 s84, 0x8100\n" \
M_"v[104:107], a[140:143], a[180:183], 0\n" \
B_"v22, s[64:67], s84 offen offset:0 lds\n" \
M_"v[104:107], a[204:207], a[244:247], v[104:107]\n" \
X_"a[166:167], v33 offset:33792\n" \
"v_cmp_gt_u32_e64 vcc, s35, v20\n" \
M_"v[100:103], a[140:143], a[176:179], 0\n" \
X_"a[228:229], v33 offset:40960\n" \
"v_lshrrev_b32_e64 v215, 1, v212\n" \
M_"v[100:103], a[204:207], a[240:243], v[100:103]\n" \
R_"a[140:143], v28 offset:55296\n" \
"s_and_b32 s4, s2, 7\n" \
M_"v[116:119], a[144:147], a[176:179], 0\n" \
R_"a[204:207], v29 offset:55296\n" \
"v_cndmask_b32_e32 v194, v219, v214, vcc\n" \
M_"v[116:119], a[208:211], a[240:243], v[116:119]\n" \
"s_addk_i32 s84, 0x1c00\n" \
M_"v[120:123], a[144:147], a[180:183], 0\n" \
B_"v23, s[64:67], s84 offen offset:1024 lds\n" \
M_"v[120:123], a[208:211], a[244:247], v[120:123]\n" \
"s_addk_i32 s84, 0x1c00\n" \
M_"v[124:127], a[144:147], a[184:187], 0\n" \
B_"v22, s[64:67], s84 offen offset:2048 lds\n" \
M_"v[124:127], a[208:211], a[248:251], v[124:127]\n" \
"s_addk_i32 s84, 0x1c00\n" \
".fill (L_pb + 44768 - .) / 4, 4, 0xBF800000\n" \
"L_pp11:\n" \
"s_cbranch_execz L_pret\n" \
M_"v[128:131], a[144:147], a[188:191], 0\n" \
B_"v23, s[64:67], s84 offen offset:3072 lds\n" \
M_"v[128:131], a[208:211], a[252:255], v[128:131]\n" \
R_"a[144:147], v28 offset:32768\n" \
"v_lshlrev_b32_e32 v214, 7, v212\n" \
M_"v[144:147], a[148:151], a[188:191], 0\n" \
R_"a[208:211], v29 offset:32768\n" \
"v_lshrrev_b32_e64 v212, 2, v212\n" \
M_"v[144:147], a[212:215], a[252:255], v[144:147]\n" \
"s_add_u32 m0, s31, 0x4000\n" \
"s_mov_b32 s84, 0x100000\n" \
M_"v[140:143], a[148:151], a[184:187], 0\n" \
B_"v26, s[72:75], s84 offen offset:0 lds\n" \
M_"v[140:143], a[212:215], a[248:251], v[140:143]\n" \
X_"a[230:231], v33 offset:41984\n" \
"v_and_b32_e64 v212, 2, v212\n" \
M_"v[136:139], a[148:151], a[180:183], 0\n" \
"s_addk_i32 s84, 0x7c00\n" \
M_"v[136:139], a[212:215], a[244:247], v[136:139]\n" \
B_"v26, s[72:75], s84 offen offset:1024 lds\n" \
M_"v[132:135], a[148:151], a[176:179], 0\n" \
X_"a[168:169], v34 offset:32768\n" \
"v_and_or_b32 v212, v215, 1, v212\n" \
M_"v[132:135], a[212:215], a[240:243], v[132:135]\n" \
R_"a[148:151], v28 offset:34816\n" \
"s_lshl_b32 s5, s4, 2\n" \
M_"v[148:151], a[152:155], a[176:179], 0\n" \
R_"a[212:215], v29 offset:34816\n" \
"s_bfe_u32 s38, s2, 0x20003\n" \
M_"v[148:151], a[216:219], a[240:243], v[148:151]\n" \
X_"a[170:171], v34 offset:33792\n" \
"v_lshlrev_b32_e64 v212, 5, v212\n" \
M_"v[152:155], a[152:155], a[180:183], 0\n" \
X_"a[232:233], v34 offset:40960\n" \
"s_movk_i32 s35, 0x60\n" \
M_"v[152:155], a[216:219], a[244:247], v[152:155]\n" \
X_"a[234:235], v34 offset:41984\n" \
"s_or_b32 s5, s5, s38\n" \
M_"v[156:159], a[152:155], a[184:187], 0\n" \
X_"a[172:173], v35 offset:32768\n" \
"v_bitop3_b32 v212, v212, s35, v214 bitop3:0x36\n" \
M_"v[156:159], a[216:219], a[248:251], v[156:159]\n" \
X_"a[174:175], v35 offset:33792\n" \
"s_lshr_b32 s36, s2, 3\n" \
M_"v[160:163], a[152:155], a[188:191], 0\n" \
X_"a[236:237], v35 offset:40960\n" \
"v_add_u32_e32 v212, s34, v212\n" \
M_"v[160:163], a[216:219], a[252:255], v[160:163]\n" \
R_"a[152:155], v28 offset:36864\n" \
"s_mul_i32 s34, s5, 0x12000\n" \
M_"v[176:179], a[156:159], a[188:191], 0\n" \
R_"a[216:219], v29 offset:36864\n" \
"s_or_b32 s40, s34, 0x6000000\n" \
M_"v[176:179], a[220:223], a[252:255], v[176:179]\n" \
X_"a[238:239], v35 offset:41984\n" \
"s_mul_i32 s34, s36, 0x90000\n" \
M_"v[172:175], a[156:159], a[184:187], 0\n" \
"s_lshl_b32 s36, s3, 14\n" \
"s_add_i32 s34, s34, s36\n" \
M_"v[172:175], a[220:223], a[248:251], v[172:175]\n" \
"s_lshl_b32 s38, s5, 8\n" \
"s_add_i32 s41, s34, 0x4240000\n" \
M_"v[168:171], a[156:159], a[180:183], 0\n" \
"s_mul_i32 s34, s5, 36\n" \
"s_add_i32 s5, s7, s40\n" \
M_"v[168:171], a[220:223], a[244:247], v[168:171]\n" \
"s_addk_i32 s5, 0x2000\n" \
"s_mov_b32 s37, 2.0\n" \
M_"v[164:167], a[156:159], a[176:179], 0\n" \
"s_or_b32 s38, s38, 0x5be8000\n" \
M_"v[164:167], a[220:223], a[240:243], v[164:167]\n" \
R_"a[156:159], v28 offset:38912\n" \
R_"a[220:223], v29 offset:38912\n" \
"s_waitcnt lgkmcnt(15)\n" \
M_"a[0:3], a[128:131], a[160:163], a[0:3]\n" \
"s_addk_i32 s84, 0x7c00\n" \
M_"a[0:3], a[192:195], a[224:227], a[0:3]\n" \
B_"v27, s[72:75], s84 offen offset:2048 lds\n" \
M_"a[4:7], a[128:131], a[164:167], a[4:7]\n" \
X_"a[176:177], v36 offset:32768\n" \
"s_waitcnt lgkmcnt(15)\n" \
M_"a[4:7], a[192:195], a[228:231], a[4:7]\n" \
"s_addk_i32 s84, 0x7c00\n" \
"s_waitcnt lgkmcnt(11)\n" \
M_"a[8:11], a[128:131], a[168:171], a[8:11]\n" \
B_"v27, s[72:75], s84 offen offset:3072 lds\n" \
"s_waitcnt lgkmcnt(9)\n" \
M_"a[8:11], a[192:195], a[232:235], a[8:11]\n" \
X_"a[178:179], v36 offset:33792\n" \
"s_waitcnt lgkmcnt(8)\n" \
M_"a[12:15], a[128:131], a[172:175], a[12:15]\n" \
X_"a[240:241], v36 offset:40960\n" \
"s_waitcnt lgkmcnt(5)\n" \
M_"a[12:15], a[192:195], a[236:239], a[12:15]\n" \
"s_cmp_lg_u32 s3, 0\n" \
"s_cselect_b32 s36, s37, s5\n" \
M_"a[28:31], a[132:135], a[172:175], a[28:31]\n" \
"s_lshl_b32 s5, s6, 2\n" \
"s_or_b32 s5, s38, s5\n" \
M_"a[28:31], a[196:199], a[236:239], a[28:31]\n" \
X_"a[242:243], v36 offset:41984\n" \
"s_lshl_b32 s35, s3, 10\n" \
M_"a[24:27], a[132:135], a[168:171], a[24:27]\n" \
"s_cmpk_lt_u32 s3, 3\n" \
"s_cselect_b32 s37, s37, s5\n" \
M_"a[24:27], a[196:199], a[232:235], a[24:27]\n" \
"s_lshl_b32 s5, s4, 16\n" \
"s_lshl_b32 s4, s4, 11\n" \
M_"a[20:23], a[132:135], a[164:167], a[20:23]\n" \
"v_cndmask_b32_e32 v198, v213, v212, vcc\n" \
"v_or_b32_e32 v212, s35, v20\n" \
M_"a[20:23], a[196:199], a[228:231], a[20:23]\n" \
X_"a[180:181], v37 offset:32768\n" \
"s_or_b32 s39, s40, s35\n" \
M_"a[16:19], a[132:135], a[160:163], a[16:19]\n" \
"s_or_b32 s55, s41, s4\n" \
"v_or_b32_e32 v199, 0x22000, v212\n" \
M_"a[16:19], a[196:199], a[224:227], a[16:19]\n" \
"s_add_i32 s35, s39, s7\n" \
"s_add_i32 s54, s41, s5\n" \
M_"a[32:35], a[136:139], a[160:163], a[32:35]\n" \
"s_mov_b32 m0, s32\n" \
X_"a[182:183], v37 offset:33792\n" \
M_"a[32:35], a[200:203], a[224:227], a[32:35]\n" \
"s_mov_b32 exec_lo, 0x0\n" \
"s_mov_b32 exec_hi, 0xff\n" \
"global_load_lds_dword v[184:185], off\n" \
"s_mov_b64 exec, -1\n" \
M_"a[36:39], a[136:139], a[164:167], a[36:39]\n" \
"s_add_i32 s55, s55, 0x80000\n" \
"v_mov_b32_e32 v215, 0xffff\n" \
M_"a[36:39], a[200:203], a[228:231], a[36:39]\n" \
X_"a[244:245], v37 offset:40960\n" \
"s_and_b32 s4, s3, 1\n" \
M_"a[40:43], a[136:139], a[168:171], a[40:43]\n" \
"s_lshl_b32 s5, s4, 4\n" \
"v_lshrrev_b32_e32 v212, 4, v20\n" \
M_"a[40:43], a[200:203], a[232:235], a[40:43]\n" \
"s_mov_b32 m0, s32\n" \
"v_lshrrev_b32_e32 v218, 6, v20\n" \
M_"a[44:47], a[136:139], a[172:175], a[44:47]\n" \
"s_mov_b32 exec_lo, 0xffff0000\n" \
"s_mov_b32 exec_hi, 0x0\n" \
"global_load_lds_dword v[186:187], off\n" \
"s_mov_b64 exec, -1\n" \
M_"a[44:47], a[200:203], a[236:239], a[44:47]\n" \
X_"a[246:247], v37 offset:41984\n" \
"v_and_b32_e64 v219, 12, v218\n" \
M_"a[60:63], a[140:143], a[172:175], a[60:63]\n" \
"v_and_or_b32 v214, v212, 15, s5\n" \
"v_cmp_gt_u32_e64 vcc, v219, v214\n" \
M_"a[60:63], a[204:207], a[236:239], a[60:63]\n" \
"v_mov_b32_e32 v220, 0xffff0000\n" \
"s_lshl_b32 s5, s2, 2\n" \
M_"a[56:59], a[140:143], a[168:171], a[56:59]\n" \
X_"a[184:185], v38 offset:32768\n" \
"v_cndmask_b32_e64 v217, v215, 0, vcc\n" \
M_"a[56:59], a[204:207], a[232:235], a[56:59]\n" \
"v_cmp_lt_u32_e32 vcc, v219, v214\n" \
"v_mov_b32_e64 v213, s23\n" \
M_"a[52:55], a[140:143], a[164:167], a[52:55]\n" \
"s_or_b32 s5, s5, s3\n" \
"v_cndmask_b32_e32 v221, 0, v220, vcc\n" \
M_"a[52:55], a[204:207], a[228:231], a[52:55]\n" \
X_"a[186:187], v38 offset:33792\n" \
"v_or_b32_e64 v12, v217, v221\n" \
M_"a[48:51], a[140:143], a[160:163], a[48:51]\n" \
"v_or_b32_e32 v217, 2, v219\n" \
"v_cmp_gt_u32_e32 vcc, v217, v214\n" \
M_"a[48:51], a[204:207], a[224:227], a[48:51]\n" \
"v_mov_b32_e32 v216, s25\n" \
"s_movk_i32 s40, 0xf400\n" \
M_"a[64:67], a[144:147], a[160:163], a[64:67]\n" \
X_"a[248:249], v38 offset:40960\n" \
"v_cndmask_b32_e64 v221, v215, 0, vcc\n" \
M_"a[64:67], a[208:211], a[224:227], a[64:67]\n" \
"v_cmp_lt_u32_e32 vcc, v217, v214\n" \
"s_lshl_b32 s4, s4, 6\n" \
M_"a[68:71], a[144:147], a[164:167], a[68:71]\n" \
"v_cndmask_b32_e32 v217, 0, v220, vcc\n" \
"v_or_b32_e32 v13, v221, v217\n" \
M_"a[68:71], a[208:211], a[228:231], a[68:71]\n" \
X_"a[250:251], v38 offset:41984\n" \
"v_or_b32_e64 v217, 16, v219\n" \
M_"a[72:75], a[144:147], a[168:171], a[72:75]\n" \
"v_cmp_gt_u32_e32 vcc, v217, v214\n" \
"s_nop 1\n" \
"v_cndmask_b32_e64 v221, v215, 0, vcc\n" \
M_"a[72:75], a[208:211], a[232:235], a[72:75]\n" \
"v_cmp_lt_u32_e32 vcc, v217, v214\n" \
"s_nop 1\n" \
"v_cndmask_b32_e64 v217, 0, v220, vcc\n" \
M_"a[76:79], a[144:147], a[172:175], a[76:79]\n" \
"v_or_b32_e32 v14, v221, v217\n" \
"v_or_b32_e32 v217, 18, v219\n" \
M_"a[76:79], a[208:211], a[236:239], a[76:79]\n" \
X_"a[188:189], v39 offset:32768\n" \
"v_cmp_gt_u32_e64 vcc, v217, v214\n" \
M_"a[92:95], a[148:151], a[172:175], a[92:95]\n" \
"s_nop 0\n" \
"v_cndmask_b32_e64 v215, v215, 0, vcc\n" \
"v_cmp_lt_u32_e32 vcc, v217, v214\n" \
M_"a[92:95], a[212:215], a[236:239], a[92:95]\n" \
"s_nop 0\n" \
"v_cndmask_b32_e32 v214, 0, v220, vcc\n" \
"v_or_b32_e64 v15, v215, v214\n" \
M_"a[88:91], a[148:151], a[168:171], a[88:91]\n" \
X_"a[190:191], v39 offset:33792\n" \
"v_mad_u64_u32 v[214:215], s[6:7], s5, 42, v[212:213]\n" \
M_"a[88:91], a[212:215], a[232:235], a[88:91]\n" \
"s_movk_i32 s5, 0x1440\n" \
"v_cmp_gt_u32_e32 vcc, s5, v214\n" \
M_"a[84:87], a[148:151], a[164:167], a[84:87]\n" \
"v_min_u32_e32 v217, 0xa43f, v214\n" \
"v_mov_b32_e64 v214, s24\n" \
M_"a[84:87], a[212:215], a[228:231], a[84:87]\n" \
X_"a[252:253], v39 offset:40960\n" \
"v_cndmask_b32_e64 v215, v216, v213, vcc\n" \
M_"a[80:83], a[148:151], a[160:163], a[80:83]\n" \
"v_mov_b32_e32 v213, s22\n" \
"s_movk_i32 s6, 0xffee\n" \
M_"a[80:83], a[212:215], a[224:227], a[80:83]\n" \
"v_cndmask_b32_e64 v214, v214, v213, vcc\n" \
"v_add_u32_e32 v213, 0xffffebc0, v217\n" \
M_"a[96:99], a[152:155], a[160:163], a[96:99]\n" \
X_"a[254:255], v39 offset:41984\n" \
"s_lshl_b32 s5, s2, 5\n" \
M_"a[96:99], a[216:219], a[224:227], a[96:99]\n" \
"v_cndmask_b32_e32 v213, v213, v217, vcc\n" \
"v_lshlrev_b32_e64 v216, 7, v213\n" \
M_"a[100:103], a[152:155], a[164:167], a[100:103]\n" \
"v_mul_u32_u24_e32 v213, 0xe39, v212\n" \
"v_mov_b32_e64 v217, 0\n" \
M_"a[100:103], a[216:219], a[228:231], a[100:103]\n" \
"v_lshrrev_b32_e64 v213, 16, v213\n" \
"v_lshl_add_u64 v[16:17], v[214:215], 0, v[216:217]\n" \
M_"a[104:107], a[152:155], a[168:171], a[104:107]\n" \
"v_mad_i32_i24 v214, v213, s6, v212\n" \
"s_add_i32 s5, s5, 32\n" \
M_"a[104:107], a[216:219], a[232:235], a[104:107]\n" \
"v_lshlrev_b32_e32 v215, 4, v214\n" \
"v_add_u32_e64 v216, s5, v215\n" \
M_"a[108:111], a[152:155], a[172:175], a[108:111]\n" \
"v_and_b32_e32 v216, 0xf0, v216\n" \
"v_cmp_gt_u32_e64 vcc, 16, v214\n" \
M_"a[108:111], a[216:219], a[236:239], a[108:111]\n" \
"s_movk_i32 s7, 0xf800\n" \
"v_cndmask_b32_e32 v214, v215, v216, vcc\n" \
"s_waitcnt lgkmcnt(15)\n" \
M_"a[124:127], a[156:159], a[172:175], a[124:127]\n" \
"v_or_b32_e32 v215, 64, v212\n" \
"v_mul_u32_u24_e32 v216, 0xe39, v215\n" \
M_"a[124:127], a[220:223], a[236:239], a[124:127]\n" \
"v_lshrrev_b32_e64 v216, 16, v216\n" \
"v_mad_i32_i24 v215, v216, s6, v215\n" \
M_"a[120:123], a[156:159], a[168:171], a[120:123]\n" \
"v_lshlrev_b32_e32 v217, 4, v215\n" \
"v_add_u32_e32 v220, s5, v217\n" \
M_"a[120:123], a[220:223], a[232:235], a[120:123]\n" \
"v_and_b32_e32 v220, 0xf0, v220\n" \
"v_cmp_gt_u32_e64 vcc, 16, v215\n" \
M_"a[116:119], a[156:159], a[164:167], a[116:119]\n" \
"s_nop 0\n" \
"v_cndmask_b32_e32 v215, v217, v220, vcc\n" \
"v_or_b32_e32 v217, 0x80, v212\n" \
M_"a[116:119], a[220:223], a[228:231], a[116:119]\n" \
"v_mul_u32_u24_e32 v220, 0xe39, v217\n" \
"v_lshrrev_b32_e64 v220, 16, v220\n" \
M_"a[112:115], a[156:159], a[160:163], a[112:115]\n" \
"v_mad_i32_i24 v217, v220, s6, v217\n" \
"v_lshlrev_b32_e64 v221, 4, v217\n" \
M_"a[112:115], a[220:223], a[224:227], a[112:115]\n" \
"s_waitcnt vmcnt(20)\n" \
"v_readfirstlane_b32 s62, v180\n" \
"s_nop 3\n" \
"s_add_u32 s62, s62, 0x9E3779B1\n" \
"s_waitcnt vmcnt(2) lgkmcnt(0)\n" \
"s_barrier\n" \
M_"v[52:55], a[128:131], a[176:179], v[52:55]\n" \
"s_add_u32 m0, s30, 0x8000\n" \
"s_movk_i32 s84, 0x180\n" \
M_"v[52:55], a[192:195], a[240:243], v[52:55]\n" \
B_"v22, s[64:67], s84 offen offset:0 lds\n" \
M_"v[56:59], a[128:131], a[180:183], v[56:59]\n" \
X_"a[160:161], v32 offset:0\n" \
"v_add_u32_e64 v222, s5, v221\n" \
M_"v[56:59], a[192:195], a[244:247], v[56:59]\n" \
"s_movk_i32 s84, 0x1d80\n" \
M_"v[60:63], a[128:131], a[184:187], v[60:63]\n" \
B_"v23, s[64:67], s84 offen offset:1024 lds\n" \
M_"v[60:63], a[192:195], a[248:251], v[60:63]\n" \
X_"a[162:163], v32 offset:1024\n" \
"v_and_b32_e32 v222, 0xf0, v222\n" \
M_"v[64:67], a[128:131], a[188:191], v[64:67]\n" \
X_"a[224:225], v32 offset:8192\n" \
"v_cmp_gt_u32_e32 vcc, 16, v217\n" \
M_"v[64:67], a[192:195], a[252:255], v[64:67]\n" \
R_"a[128:131], v28 offset:16384\n" \
"v_cndmask_b32_e64 v217, v221, v222, vcc\n" \
M_"v[80:83], a[132:135], a[188:191], v[80:83]\n" \
R_"a[192:195], v29 offset:16384\n" \
"v_or_b32_e32 v221, 0xc0, v212\n" \
M_"v[80:83], a[196:199], a[252:255], v[80:83]\n" \
"s_movk_i32 s84, 0x3980\n" \
M_"v[76:79], a[132:135], a[184:187], v[76:79]\n" \
B_"v22, s[64:67], s84 offen offset:2048 lds\n" \
M_"v[76:79], a[196:199], a[248:251], v[76:79]\n" \
"s_movk_i32 s84, 0x5580\n" \
M_"v[72:75], a[132:135], a[180:183], v[72:75]\n" \
B_"v23, s[64:67], s84 offen offset:3072 lds\n" \
M_"v[72:75], a[196:199], a[244:247], v[72:75]\n" \
"s_add_u32 m0, s31, 0x8000\n" \
"s_mov_b32 s84, 0x180000\n" \
M_"v[68:71], a[132:135], a[176:179], v[68:71]\n" \
B_"v26, s[68:71], s84 offen offset:0 lds\n" \
M_"v[68:71], a[196:199], a[240:243], v[68:71]\n" \
R_"a[132:135], v28 offset:18432\n" \
"v_mul_u32_u24_e32 v222, 0xe39, v221\n" \
M_"v[84:87], a[136:139], a[176:179], v[84:87]\n" \
R_"a[196:199], v29 offset:18432\n" \
"v_lshrrev_b32_e64 v222, 16, v222\n" \
M_"v[84:87], a[200:203], a[240:243], v[84:87]\n" \
"s_addk_i32 s84, 0x7c00\n" \
M_"v[88:91], a[136:139], a[180:183], v[88:91]\n" \
B_"v26, s[68:71], s84 offen offset:1024 lds\n" \
M_"v[88:91], a[200:203], a[244:247], v[88:91]\n" \
X_"a[226:227], v32 offset:9216\n" \
"v_mad_i32_i24 v221, v222, s6, v221\n" \
M_"v[92:95], a[136:139], a[184:187], v[92:95]\n" \
"s_addk_i32 s84, 0x7c00\n" \
M_"v[92:95], a[200:203], a[248:251], v[92:95]\n" \
B_"v27, s[68:71], s84 offen offset:2048 lds\n" \
M_"v[96:99], a[136:139], a[188:191], v[96:99]\n" \
X_"a[164:165], v33 offset:0\n" \
"v_lshlrev_b32_e64 v223, 4, v221\n" \
M_"v[96:99], a[200:203], a[252:255], v[96:99]\n" \
R_"a[136:139], v28 offset:20480\n" \
"v_add_u32_e64 v224, s5, v223\n" \
M_"v[112:115], a[140:143], a[188:191], v[112:115]\n" \
R_"a[200:203], v29 offset:20480\n" \
"v_and_b32_e32 v224, 0xf0, v224\n" \
M_"v[112:115], a[204:207], a[252:255], v[112:115]\n" \
"s_addk_i32 s84, 0x7c00\n" \
M_"v[108:111], a[140:143], a[184:187], v[108:111]\n" \
B_"v27, s[68:71], s84 offen offset:3072 lds\n" \
M_"v[108:111], a[204:207], a[248:251], v[108:111]\n" \
"s_add_u32 m0, s30, 0x9000\n" \
"s_mov_b32 s84, 0x8180\n" \
M_"v[104:107], a[140:143], a[180:183], v[104:107]\n" \
B_"v22, s[64:67], s84 offen offset:0 lds\n" \
M_"v[104:107], a[204:207], a[244:247], v[104:107]\n" \
X_"a[166:167], v33 offset:1024\n" \
"v_cmp_gt_u32_e32 vcc, 16, v221\n" \
M_"v[100:103], a[140:143], a[176:179], v[100:103]\n" \
X_"a[228:229], v33 offset:8192\n" \
"v_cndmask_b32_e64 v221, v223, v224, vcc\n" \
M_"v[100:103], a[204:207], a[240:243], v[100:103]\n" \
R_"a[140:143], v28 offset:22528\n" \
"v_or_b32_e32 v223, 0x100, v212\n" \
M_"v[116:119], a[144:147], a[176:179], v[116:119]\n" \
R_"a[204:207], v29 offset:22528\n" \
"v_mul_u32_u24_e32 v224, 0xe39, v223\n" \
M_"v[116:119], a[208:211], a[240:243], v[116:119]\n" \
"s_addk_i32 s84, 0x1c00\n" \
M_"v[120:123], a[144:147], a[180:183], v[120:123]\n" \
B_"v23, s[64:67], s84 offen offset:1024 lds\n" \
M_"v[120:123], a[208:211], a[244:247], v[120:123]\n" \
"s_addk_i32 s84, 0x1c00\n" \
M_"v[124:127], a[144:147], a[184:187], v[124:127]\n" \
B_"v22, s[64:67], s84 offen offset:2048 lds\n" \
M_"v[124:127], a[208:211], a[248:251], v[124:127]\n" \
"s_addk_i32 s84, 0x1c00\n" \
M_"v[128:131], a[144:147], a[188:191], v[128:131]\n" \
B_"v23, s[64:67], s84 offen offset:3072 lds\n" \
M_"v[128:131], a[208:211], a[252:255], v[128:131]\n" \
R_"a[144:147], v28 offset:0\n" \
"v_lshrrev_b32_e32 v224, 16, v224\n" \
M_"v[144:147], a[148:151], a[188:191], v[144:147]\n" \
R_"a[208:211], v29 offset:0\n" \
"v_mad_i32_i24 v223, v224, s6, v223\n" \
M_"v[144:147], a[212:215], a[252:255], v[144:147]\n" \
"s_add_u32 m0, s31, 0xc000\n" \
"s_mov_b32 s84, 0x180000\n" \
M_"v[140:143], a[148:151], a[184:187], v[140:143]\n" \
B_"v26, s[72:75], s84 offen offset:0 lds\n" \
M_"v[140:143], a[212:215], a[248:251], v[140:143]\n" \
X_"a[230:231], v33 offset:9216\n" \
"v_lshlrev_b32_e64 v225, 4, v223\n" \
M_"v[136:139], a[148:151], a[180:183], v[136:139]\n" \
"s_addk_i32 s84, 0x7c00\n" \
M_"v[136:139], a[212:215], a[244:247], v[136:139]\n" \
B_"v26, s[72:75], s84 offen offset:1024 lds\n" \
M_"v[132:135], a[148:151], a[176:179], v[132:135]\n" \
X_"a[168:169], v34 offset:0\n" \
"v_add_u32_e32 v226, s5, v225\n" \
M_"v[132:135], a[212:215], a[240:243], v[132:135]\n" \
R_"a[148:151], v28 offset:2048\n" \
"v_and_b32_e32 v226, 0xf0, v226\n" \
M_"v[148:151], a[152:155], a[176:179], v[148:151]\n" \
R_"a[212:215], v29 offset:2048\n" \
"v_cmp_gt_u32_e64 vcc, 16, v223\n" \
M_"v[148:151], a[216:219], a[240:243], v[148:151]\n" \
X_"a[170:171], v34 offset:1024\n" \
"v_cndmask_b32_e64 v223, v225, v226, vcc\n" \
M_"v[152:155], a[152:155], a[180:183], v[152:155]\n" \
X_"a[232:233], v34 offset:8192\n" \
"v_or_b32_e32 v225, 0x140, v212\n" \
M_"v[152:155], a[216:219], a[244:247], v[152:155]\n" \
X_"a[234:235], v34 offset:9216\n" \
"v_mul_u32_u24_e32 v226, 0xe39, v225\n" \
M_"v[156:159], a[152:155], a[184:187], v[156:159]\n" \
X_"a[172:173], v35 offset:0\n" \
"v_lshrrev_b32_e64 v226, 16, v226\n" \
M_"v[156:159], a[216:219], a[248:251], v[156:159]\n" \
X_"a[174:175], v35 offset:1024\n" \
"v_mad_i32_i24 v225, v226, s6, v225\n" \
M_"v[160:163], a[152:155], a[188:191], v[160:163]\n" \
X_"a[236:237], v35 offset:8192\n" \
"v_lshlrev_b32_e64 v227, 4, v225\n" \
M_"v[160:163], a[216:219], a[252:255], v[160:163]\n" \
R_"a[152:155], v28 offset:4096\n" \
"v_add_u32_e64 v228, s5, v227\n" \
M_"v[176:179], a[156:159], a[188:191], v[176:179]\n" \
R_"a[216:219], v29 offset:4096\n" \
"v_and_b32_e32 v228, 0xf0, v228\n" \
M_"v[176:179], a[220:223], a[252:255], v[176:179]\n" \
X_"a[238:239], v35 offset:9216\n" \
"v_cmp_gt_u32_e64 vcc, 16, v225\n" \
M_"v[172:175], a[156:159], a[184:187], v[172:175]\n" \
"s_nop 0\n" \
"v_cndmask_b32_e32 v225, v227, v228, vcc\n" \
"v_or_b32_e32 v227, 0x180, v212\n" \
M_"v[172:175], a[220:223], a[248:251], v[172:175]\n" \
"v_mul_u32_u24_e32 v228, 0xe39, v227\n" \
"v_lshrrev_b32_e64 v228, 16, v228\n" \
M_"v[168:171], a[156:159], a[180:183], v[168:171]\n" \
"v_mad_i32_i24 v227, v228, s6, v227\n" \
"v_lshlrev_b32_e64 v229, 4, v227\n" \
M_"v[168:171], a[220:223], a[244:247], v[168:171]\n" \
"v_add_u32_e64 v230, s5, v229\n" \
"v_and_b32_e32 v230, 0xf0, v230\n" \
M_"v[164:167], a[156:159], a[176:179], v[164:167]\n" \
"v_cmp_gt_u32_e32 vcc, 16, v227\n" \
"s_nop 1\n" \
"v_cndmask_b32_e64 v227, v229, v230, vcc\n" \
M_"v[164:167], a[220:223], a[240:243], v[164:167]\n" \
R_"a[156:159], v28 offset:6144\n" \
R_"a[220:223], v29 offset:6144\n" \
"s_waitcnt lgkmcnt(15)\n" \
M_"a[0:3], a[128:131], a[160:163], a[0:3]\n" \
"s_addk_i32 s84, 0x7c00\n" \
M_"a[0:3], a[192:195], a[224:227], a[0:3]\n" \
B_"v27, s[72:75], s84 offen offset:2048 lds\n" \
M_"a[4:7], a[128:131], a[164:167], a[4:7]\n" \
X_"a[176:177], v36 offset:0\n" \
"v_or_b32_e32 v229, 0x1c0, v212\n" \
"s_waitcnt lgkmcnt(15)\n" \
M_"a[4:7], a[192:195], a[228:231], a[4:7]\n" \
"s_addk_i32 s84, 0x7c00\n" \
"s_waitcnt lgkmcnt(11)\n" \
M_"a[8:11], a[128:131], a[168:171], a[8:11]\n" \
B_"v27, s[72:75], s84 offen offset:3072 lds\n" \
"s_waitcnt lgkmcnt(9)\n" \
M_"a[8:11], a[192:195], a[232:235], a[8:11]\n" \
X_"a[178:179], v36 offset:1024\n" \
"v_mul_u32_u24_e32 v230, 0xe39, v229\n" \
"s_waitcnt lgkmcnt(8)\n" \
M_"a[12:15], a[128:131], a[172:175], a[12:15]\n" \
X_"a[240:241], v36 offset:8192\n" \
"v_lshrrev_b32_e32 v230, 16, v230\n" \
"s_waitcnt lgkmcnt(5)\n" \
M_"a[12:15], a[192:195], a[236:239], a[12:15]\n" \
"v_mad_i32_i24 v229, v230, s6, v229\n" \
"v_lshlrev_b32_e32 v231, 4, v229\n" \
M_"a[28:31], a[132:135], a[172:175], a[28:31]\n" \
"v_add_u32_e64 v232, s5, v231\n" \
"v_and_b32_e32 v232, 0xf0, v232\n" \
M_"a[28:31], a[196:199], a[236:239], a[28:31]\n" \
X_"a[242:243], v36 offset:9216\n" \
"v_cmp_gt_u32_e64 vcc, 16, v229\n" \
M_"a[24:27], a[132:135], a[168:171], a[24:27]\n" \
B_"v[200:203], v192, s[76:79], s34 offen offset:0\n" \
M_"a[24:27], a[196:199], a[232:235], a[24:27]\n" \
"v_cndmask_b32_e64 v229, v231, v232, vcc\n" \
"v_or_b32_e32 v231, 0x200, v212\n" \
M_"a[20:23], a[132:135], a[164:167], a[20:23]\n" \
"v_mul_u32_u24_e32 v232, 0xe39, v231\n" \
"v_lshrrev_b32_e64 v232, 16, v232\n" \
M_"a[20:23], a[196:199], a[228:231], a[20:23]\n" \
X_"a[180:181], v37 offset:0\n" \
"v_mad_i32_i24 v231, v232, s6, v231\n" \
M_"a[16:19], a[132:135], a[160:163], a[16:19]\n" \
"v_lshlrev_b32_e32 v233, 4, v231\n" \
"v_add_u32_e32 v234, s5, v233\n" \
M_"a[16:19], a[196:199], a[224:227], a[16:19]\n" \
"s_movk_i32 s5, 0x480\n" \
"v_mad_u32_u24 v0, v213, s5, v214\n" \
M_"a[32:35], a[136:139], a[160:163], a[32:35]\n" \
B_"v[204:207], v193, s[76:79], s34 offen offset:0\n" \
M_"a[32:35], a[200:203], a[224:227], a[32:35]\n" \
"s_mov_b32 m0, s32\n" \
X_"a[182:183], v37 offset:1024\n" \
M_"a[36:39], a[136:139], a[164:167], a[36:39]\n" \
"s_mov_b32 exec_lo, 0x0\n" \
"s_mov_b32 exec_hi, 0xff00\n" \
"global_load_lds_dword v[184:185], off\n" \
"s_mov_b64 exec, -1\n" \
M_"a[36:39], a[200:203], a[228:231], a[36:39]\n" \
X_"a[244:245], v37 offset:8192\n" \
"v_mul_u32_u24_e32 v213, 0x480, v216\n" \
M_"a[40:43], a[136:139], a[168:171], a[40:43]\n" \
"s_movk_i32 s6, 0xfc00\n" \
"s_nop 0\n" \
"v_add3_u32 v1, v213, v215, s6\n" \
M_"a[40:43], a[200:203], a[232:235], a[40:43]\n" \
"buffer_load_dword v208, v194, s[76:79], s34 offen offset:32\n" \
M_"a[44:47], a[136:139], a[172:175], a[44:47]\n" \
X_"a[246:247], v37 offset:9216\n" \
"v_mul_u32_u24_e32 v213, 0x480, v220\n" \
M_"a[44:47], a[200:203], a[236:239], a[44:47]\n" \
"v_add3_u32 v2, v213, v217, s7\n" \
"v_mul_u32_u24_e32 v213, 0x480, v222\n" \
M_"a[60:63], a[140:143], a[172:175], a[60:63]\n" \
"v_add3_u32 v3, v213, v221, s40\n" \
"v_mul_u32_u24_e32 v213, 0x480, v226\n" \
M_"a[60:63], a[204:207], a[236:239], a[60:63]\n" \
"v_add3_u32 v5, v213, v225, s6\n" \
"v_mul_u32_u24_e32 v213, 0x480, v228\n" \
M_"a[56:59], a[140:143], a[168:171], a[56:59]\n" \
"buffer_load_dword v209, v194, s[76:79], s34 offen offset:1184\n" \
M_"a[56:59], a[204:207], a[232:235], a[56:59]\n" \
X_"a[184:185], v38 offset:0\n" \
"v_add3_u32 v6, v213, v227, s7\n" \
M_"a[52:55], a[140:143], a[164:167], a[52:55]\n" \
"v_mul_u32_u24_e32 v213, 0x480, v230\n" \
"v_add3_u32 v7, v213, v229, s40\n" \
M_"a[52:55], a[204:207], a[228:231], a[52:55]\n" \
X_"a[186:187], v38 offset:1024\n" \
"s_lshl_b32 s7, s3, 3\n" \
M_"a[48:51], a[140:143], a[160:163], a[48:51]\n" \
"s_lshl_b32 s40, s3, 1\n" \
"s_and_b32 s7, s7, 16\n" \
M_"a[48:51], a[204:207], a[224:227], a[48:51]\n" \
"s_and_b32 s40, s40, 2\n" \
"s_and_b32 s6, s2, 7\n" \
M_"a[64:67], a[144:147], a[160:163], a[64:67]\n" \
"buffer_load_dword v211, v194, s[76:79], s34 offen offset:2336\n" \
M_"a[64:67], a[208:211], a[224:227], a[64:67]\n" \
X_"a[248:249], v38 offset:8192\n" \
"v_bfe_u32 v213, v20, 7, 1\n" \
M_"a[68:71], a[144:147], a[164:167], a[68:71]\n" \
"s_or_b32 s7, s40, s7\n" \
"v_and_b32_e32 v234, 0xf0, v234\n" \
M_"a[68:71], a[208:211], a[228:231], a[68:71]\n" \
X_"a[250:251], v38 offset:9216\n" \
"v_cmp_gt_u32_e64 vcc, 16, v231\n" \
M_"a[72:75], a[144:147], a[168:171], a[72:75]\n" \
"v_or3_b32 v213, s7, v213, v219\n" \
"v_and_b32_e64 v214, 32, v20\n" \
M_"a[72:75], a[208:211], a[232:235], a[72:75]\n" \
"s_mov_b32 s76, s20\n" \
"s_and_b32 s77, s21, 0xffff\n" \
M_"a[76:79], a[144:147], a[172:175], a[76:79]\n" \
"s_mov_b32 s78, 0x480\n" \
"s_lshl_b32 s6, s6, 5\n" \
M_"a[76:79], a[208:211], a[236:239], a[76:79]\n" \
"buffer_load_ushort v210, v195, s[76:79], 0 offen\n" \
M_"a[92:95], a[148:151], a[172:175], a[92:95]\n" \
X_"a[188:189], v39 offset:0\n" \
"v_cndmask_b32_e64 v231, v233, v234, vcc\n" \
M_"a[92:95], a[212:215], a[236:239], a[92:95]\n" \
"v_mul_u32_u24_e64 v213, 18, v213\n" \
"v_mov_b32_e32 v215, 0x100\n" \
M_"a[88:91], a[148:151], a[168:171], a[88:91]\n" \
"s_mov_b32 s76, s8\n" \
"s_and_b32 s77, s9, 0xffff\n" \
M_"a[88:91], a[212:215], a[232:235], a[88:91]\n" \
"s_mov_b32 s78, 0x240000\n" \
X_"a[190:191], v39 offset:1024\n" \
".fill (L_pb + 48864 - .) / 4, 4, 0xBF800000\n" \
"L_pp12:\n" \
"s_cbranch_execz L_pret\n" \
M_"a[84:87], a[148:151], a[164:167], a[84:87]\n" \
"v_mov_b32_e32 v216, s6\n" \
"v_cmp_eq_u32_e64 vcc, 0, v214\n" \
M_"a[84:87], a[212:215], a[228:231], a[84:87]\n" \
X_"a[252:253], v39 offset:8192\n" \
"v_and_or_b32 v213, v218, 1, v213\n" \
M_"a[80:83], a[148:151], a[160:163], a[80:83]\n" \
"v_lshlrev_b32_e32 v212, 3, v212\n" \
"v_cndmask_b32_e32 v214, v215, v216, vcc\n" \
M_"a[80:83], a[212:215], a[224:227], a[80:83]\n" \
"v_mad_u32_u24 v213, v213, s5, v214\n" \
"v_and_or_b32 v213, v20, 16, v213\n" \
M_"a[96:99], a[152:155], a[160:163], a[96:99]\n" \
X_"a[254:255], v39 offset:9216\n" \
"v_add_u32_e32 v9, 0x4800, v213\n" \
M_"a[96:99], a[216:219], a[224:227], a[96:99]\n" \
"v_mul_u32_u24_e32 v213, 0x120, v218\n" \
"v_and_or_b32 v10, v212, 24, v213\n" \
M_"a[100:103], a[152:155], a[164:167], a[100:103]\n" \
"v_lshrrev_b32_e64 v213, 2, v20\n" \
"v_and_or_b32 v213, v213, 60, s4\n" \
M_"a[100:103], a[216:219], a[228:231], a[100:103]\n" \
"v_lshl_or_b32 v212, s3, 9, v212\n" \
"v_mad_u32_u24 v4, v224, s5, v223\n" \
M_"a[104:107], a[152:155], a[168:171], a[104:107]\n" \
"v_mad_u32_u24 v8, v232, s5, v231\n" \
"v_or_b32_e32 v18, 0x27000, v213\n" \
M_"a[104:107], a[216:219], a[232:235], a[104:107]\n" \
"v_or_b32_e32 v19, 0x27800, v212\n" \
"v_mov_b32_e32 v215, 0x1000c0c\n" \
M_"a[108:111], a[152:155], a[172:175], a[108:111]\n" \
"v_mov_b32_e32 v216, 0xc0c0100\n" \
"v_bfe_u32 v214, v20, 4, 1\n" \
M_"a[108:111], a[216:219], a[236:239], a[108:111]\n" \
"v_lshrrev_b32_e32 v212, 8, v20\n" \
"v_cmp_eq_u32_e32 vcc, 0, v214\n" \
"s_waitcnt lgkmcnt(15)\n" \
M_"a[124:127], a[156:159], a[172:175], a[124:127]\n" \
"v_bfe_u32 v213, v20, 4, 4\n" \
"v_mov_b32_e32 v217, 0xc0c0302\n" \
M_"a[124:127], a[220:223], a[236:239], a[124:127]\n" \
"v_cndmask_b32_e32 v215, v215, v216, vcc\n" \
"v_mov_b32_e32 v216, 0x3020c0c\n" \
M_"a[120:123], a[156:159], a[168:171], a[120:123]\n" \
"v_lshl_or_b32 v214, v212, 2, v214\n" \
"v_cndmask_b32_e64 v216, v216, v217, vcc\n" \
M_"a[120:123], a[220:223], a[232:235], a[120:123]\n" \
"v_mov_b32_e32 v217, 0xc0c0c0c\n" \
"v_cmp_gt_u32_e64 vcc, v214, v213\n" \
M_"a[116:119], a[156:159], a[164:167], a[116:119]\n" \
"v_or_b32_e32 v214, 2, v214\n" \
"s_lshr_b32 s4, s2, 5\n" \
M_"a[116:119], a[220:223], a[228:231], a[116:119]\n" \
"v_cndmask_b32_e32 v40, v217, v215, vcc\n" \
"v_cndmask_b32_e32 v42, v215, v217, vcc\n" \
M_"a[112:115], a[156:159], a[160:163], a[112:115]\n" \
"v_cmp_gt_u32_e32 vcc, v214, v213\n" \
"v_and_b32_e32 v214, 16, v20\n" \
"s_waitcnt vmcnt(7) lgkmcnt(0)\n" \
M_"a[112:115], a[220:223], a[224:227], a[112:115]\n" \
"s_barrier\n" \
M_"v[52:55], a[128:131], a[176:179], v[52:55]\n" \
"s_add_u32 m0, s30, 0x0\n" \
"s_movk_i32 s84, 0x200\n" \
M_"v[52:55], a[192:195], a[240:243], v[52:55]\n" \
B_"v22, s[64:67], s84 offen offset:0 lds\n" \
M_"v[56:59], a[128:131], a[180:183], v[56:59]\n" \
X_"a[160:161], v32 offset:32768\n" \
"s_movk_i32 s42, 0x3f80\n" \
M_"v[56:59], a[192:195], a[244:247], v[56:59]\n" \
"s_movk_i32 s84, 0x1e00\n" \
M_"v[60:63], a[128:131], a[184:187], v[60:63]\n" \
B_"v23, s[64:67], s84 offen offset:1024 lds\n" \
M_"v[60:63], a[192:195], a[248:251], v[60:63]\n" \
X_"a[162:163], v32 offset:33792\n" \
"s_xor_b32 s5, s4, 15\n" \
M_"v[64:67], a[128:131], a[188:191], v[64:67]\n" \
X_"a[224:225], v32 offset:40960\n" \
M_"v[64:67], a[192:195], a[252:255], v[64:67]\n" \
R_"a[128:131], v28 offset:49152\n" \
M_"v[80:83], a[132:135], a[188:191], v[80:83]\n" \
R_"a[192:195], v29 offset:49152\n" \
M_"v[80:83], a[196:199], a[252:255], v[80:83]\n" \
"s_movk_i32 s84, 0x3a00\n" \
M_"v[76:79], a[132:135], a[184:187], v[76:79]\n" \
B_"v22, s[64:67], s84 offen offset:2048 lds\n" \
M_"v[76:79], a[196:199], a[248:251], v[76:79]\n" \
"s_movk_i32 s84, 0x5600\n" \
M_"v[72:75], a[132:135], a[180:183], v[72:75]\n" \
B_"v23, s[64:67], s84 offen offset:3072 lds\n" \
M_"v[72:75], a[196:199], a[244:247], v[72:75]\n" \
"s_add_u32 m0, s31, 0x0\n" \
"s_mov_b32 s84, 0x200000\n" \
M_"v[68:71], a[132:135], a[176:179], v[68:71]\n" \
B_"v26, s[68:71], s84 offen offset:0 lds\n" \
M_"v[68:71], a[196:199], a[240:243], v[68:71]\n" \
R_"a[132:135], v28 offset:51200\n" \
M_"v[84:87], a[136:139], a[176:179], v[84:87]\n" \
R_"a[196:199], v29 offset:51200\n" \
M_"v[84:87], a[200:203], a[240:243], v[84:87]\n" \
"s_addk_i32 s84, 0x7c00\n" \
M_"v[88:91], a[136:139], a[180:183], v[88:91]\n" \
B_"v26, s[68:71], s84 offen offset:1024 lds\n" \
M_"v[88:91], a[200:203], a[244:247], v[88:91]\n" \
X_"a[226:227], v32 offset:41984\n" \
M_"v[92:95], a[136:139], a[184:187], v[92:95]\n" \
"s_addk_i32 s84, 0x7c00\n" \
M_"v[92:95], a[200:203], a[248:251], v[92:95]\n" \
B_"v27, s[68:71], s84 offen offset:2048 lds\n" \
M_"v[96:99], a[136:139], a[188:191], v[96:99]\n" \
X_"a[164:165], v33 offset:32768\n" \
M_"v[96:99], a[200:203], a[252:255], v[96:99]\n" \
R_"a[136:139], v28 offset:53248\n" \
M_"v[112:115], a[140:143], a[188:191], v[112:115]\n" \
R_"a[200:203], v29 offset:53248\n" \
M_"v[112:115], a[204:207], a[252:255], v[112:115]\n" \
"s_addk_i32 s84, 0x7c00\n" \
"s_nop 0\n" \
M_"v[108:111], a[140:143], a[184:187], v[108:111]\n" \
B_"v27, s[68:71], s84 offen offset:3072 lds\n" \
M_"v[108:111], a[204:207], a[248:251], v[108:111]\n" \
"s_add_u32 m0, s30, 0x1000\n" \
"s_mov_b32 s84, 0x8200\n" \
M_"v[104:107], a[140:143], a[180:183], v[104:107]\n" \
B_"v22, s[64:67], s84 offen offset:0 lds\n" \
M_"v[104:107], a[204:207], a[244:247], v[104:107]\n" \
X_"a[166:167], v33 offset:33792\n" \
M_"v[100:103], a[140:143], a[176:179], v[100:103]\n" \
X_"a[228:229], v33 offset:40960\n" \
M_"v[100:103], a[204:207], a[240:243], v[100:103]\n" \
R_"a[140:143], v28 offset:55296\n" \
M_"v[116:119], a[144:147], a[176:179], v[116:119]\n" \
R_"a[204:207], v29 offset:55296\n" \
M_"v[116:119], a[208:211], a[240:243], v[116:119]\n" \
"s_addk_i32 s84, 0x1c00\n" \
M_"v[120:123], a[144:147], a[180:183], v[120:123]\n" \
B_"v23, s[64:67], s84 offen offset:1024 lds\n" \
M_"v[120:123], a[208:211], a[244:247], v[120:123]\n" \
"s_addk_i32 s84, 0x1c00\n" \
M_"v[124:127], a[144:147], a[184:187], v[124:127]\n" \
B_"v22, s[64:67], s84 offen offset:2048 lds\n" \
M_"v[124:127], a[208:211], a[248:251], v[124:127]\n" \
"s_addk_i32 s84, 0x1c00\n" \
"s_nop 0\n" \
M_"v[128:131], a[144:147], a[188:191], v[128:131]\n" \
B_"v23, s[64:67], s84 offen offset:3072 lds\n" \
M_"v[128:131], a[208:211], a[252:255], v[128:131]\n" \
R_"a[144:147], v28 offset:32768\n" \
M_"v[144:147], a[148:151], a[188:191], v[144:147]\n" \
R_"a[208:211], v29 offset:32768\n" \
M_"v[144:147], a[212:215], a[252:255], v[144:147]\n" \
"s_add_u32 m0, s31, 0x4000\n" \
"s_mov_b32 s84, 0x200000\n" \
M_"v[140:143], a[148:151], a[184:187], v[140:143]\n" \
B_"v26, s[72:75], s84 offen offset:0 lds\n" \
M_"v[140:143], a[212:215], a[248:251], v[140:143]\n" \
X_"a[230:231], v33 offset:41984\n" \
M_"v[136:139], a[148:151], a[180:183], v[136:139]\n" \
"s_addk_i32 s84, 0x7c00\n" \
"s_nop 0\n" \
M_"v[136:139], a[212:215], a[244:247], v[136:139]\n" \
B_"v26, s[72:75], s84 offen offset:1024 lds\n" \
M_"v[132:135], a[148:151], a[176:179], v[132:135]\n" \
X_"a[168:169], v34 offset:32768\n" \
M_"v[132:135], a[212:215], a[240:243], v[132:135]\n" \
R_"a[148:151], v28 offset:34816\n" \
M_"v[148:151], a[152:155], a[176:179], v[148:151]\n" \
R_"a[212:215], v29 offset:34816\n" \
M_"v[148:151], a[216:219], a[240:243], v[148:151]\n" \
X_"a[170:171], v34 offset:33792\n" \
M_"v[152:155], a[152:155], a[180:183], v[152:155]\n" \
X_"a[232:233], v34 offset:40960\n" \
M_"v[152:155], a[216:219], a[244:247], v[152:155]\n" \
X_"a[234:235], v34 offset:41984\n" \
M_"v[156:159], a[152:155], a[184:187], v[156:159]\n" \
X_"a[172:173], v35 offset:32768\n" \
M_"v[156:159], a[216:219], a[248:251], v[156:159]\n" \
X_"a[174:175], v35 offset:33792\n" \
M_"v[160:163], a[152:155], a[188:191], v[160:163]\n" \
X_"a[236:237], v35 offset:40960\n" \
M_"v[160:163], a[216:219], a[252:255], v[160:163]\n" \
R_"a[152:155], v28 offset:36864\n" \
M_"v[176:179], a[156:159], a[188:191], v[176:179]\n" \
R_"a[216:219], v29 offset:36864\n" \
M_"v[176:179], a[220:223], a[252:255], v[176:179]\n" \
X_"a[238:239], v35 offset:41984\n" \
M_"v[172:175], a[156:159], a[184:187], v[172:175]\n" \
"s_cmpk_lt_u32 s3, 2\n" \
"s_cselect_b32 s6, s5, s4\n" \
M_"v[172:175], a[220:223], a[248:251], v[172:175]\n" \
"s_lshl_b32 s7, s6, 7\n" \
"v_and_b32_e32 v213, 0x320, v20\n" \
M_"v[168:171], a[156:159], a[180:183], v[168:171]\n" \
"s_movk_i32 s41, 0x100\n" \
"v_lshlrev_b32_e64 v214, v214, s42\n" \
M_"v[168:171], a[220:223], a[244:247], v[168:171]\n" \
"v_mov_b32_e32 v215, 0\n" \
"s_add_i32 s40, s7, -16\n" \
M_"v[164:167], a[156:159], a[176:179], v[164:167]\n" \
"v_cndmask_b32_e32 v41, v217, v216, vcc\n" \
"v_cndmask_b32_e32 v43, v216, v217, vcc\n" \
M_"v[164:167], a[220:223], a[240:243], v[164:167]\n" \
R_"a[156:159], v28 offset:38912\n" \
R_"a[220:223], v29 offset:38912\n" \
"s_waitcnt lgkmcnt(15)\n" \
M_"a[0:3], a[128:131], a[160:163], a[0:3]\n" \
"s_addk_i32 s84, 0x7c00\n" \
M_"a[0:3], a[192:195], a[224:227], a[0:3]\n" \
B_"v27, s[72:75], s84 offen offset:2048 lds\n" \
M_"a[4:7], a[128:131], a[164:167], a[4:7]\n" \
X_"a[176:177], v36 offset:32768\n" \
"v_cmp_eq_u32_e32 vcc, s41, v213\n" \
"s_waitcnt lgkmcnt(15)\n" \
M_"a[4:7], a[192:195], a[228:231], a[4:7]\n" \
"s_addk_i32 s84, 0x7c00\n" \
"s_waitcnt lgkmcnt(11)\n" \
M_"a[8:11], a[128:131], a[168:171], a[8:11]\n" \
B_"v27, s[72:75], s84 offen offset:3072 lds\n" \
"s_waitcnt lgkmcnt(9)\n" \
M_"a[8:11], a[192:195], a[232:235], a[8:11]\n" \
X_"a[178:179], v36 offset:33792\n" \
"s_mov_b32 s41, 0\n" \
"s_waitcnt lgkmcnt(8)\n" \
M_"a[12:15], a[128:131], a[172:175], a[12:15]\n" \
X_"a[240:241], v36 offset:40960\n" \
"s_and_b32 s43, s3, 1\n" \
"s_waitcnt lgkmcnt(5)\n" \
M_"a[12:15], a[192:195], a[236:239], a[12:15]\n" \
"v_cndmask_b32_e32 v46, v215, v214, vcc\n" \
"v_and_b32_e32 v213, 0x330, v20\n" \
M_"a[28:31], a[132:135], a[172:175], a[28:31]\n" \
"s_cmp_lg_u32 s7, 0\n" \
"s_cselect_b32 s40, s40, s41\n" \
M_"a[28:31], a[196:199], a[236:239], a[28:31]\n" \
X_"a[242:243], v36 offset:41984\n" \
"s_mov_b32 s42, 0xff800000\n" \
M_"a[24:27], a[132:135], a[168:171], a[24:27]\n" \
"s_cmp_lg_u32 s7, 0\n" \
"s_cselect_b32 s47, s41, s42\n" \
M_"a[24:27], a[196:199], a[232:235], a[24:27]\n" \
"s_lshl_b32 s41, s43, 6\n" \
"v_lshrrev_b32_e32 v215, 6, v20\n" \
M_"a[20:23], a[132:135], a[164:167], a[20:23]\n" \
"v_mov_b32_e32 v214, 0x3f80\n" \
"v_cmp_eq_u32_e64 vcc, 32, v213\n" \
M_"a[20:23], a[196:199], a[228:231], a[20:23]\n" \
X_"a[180:181], v37 offset:32768\n" \
"s_or_b32 s7, s41, s7\n" \
M_"a[16:19], a[132:135], a[160:163], a[16:19]\n" \
"v_lshrrev_b32_e32 v213, 7, v20\n" \
"v_bfe_u32 v216, v20, 6, 2\n" \
M_"a[16:19], a[196:199], a[224:227], a[16:19]\n" \
"v_lshrrev_b32_e64 v219, 1, v20\n" \
"v_bfe_u32 v215, v215, 1, 1\n" \
M_"a[32:35], a[136:139], a[160:163], a[32:35]\n" \
"s_waitcnt vmcnt(16)\n" \
"v_or_b32_e32 v204, v210, v204\n" \
"v_or_b32_e64 v206, v210, v206\n" \
M_"a[32:35], a[200:203], a[224:227], a[32:35]\n" \
"v_mov_b32_e64 v210, 0\n" \
"s_add_u32 s88, s35, 0x1000\n" \
M_"a[36:39], a[136:139], a[164:167], a[36:39]\n" \
X_"a[182:183], v37 offset:33792\n" \
"v_cndmask_b32_e64 v47, 0, v214, vcc\n" \
M_"a[36:39], a[200:203], a[228:231], a[36:39]\n" \
X_"a[244:245], v37 offset:40960\n" \
"v_or_b32_e64 v214, s7, v213\n" \
M_"a[40:43], a[136:139], a[168:171], a[40:43]\n" \
"s_mov_b32 m0, s32\n" \
"v_lshl_or_b32 v217, s43, 3, v213\n" \
M_"a[40:43], a[200:203], a[232:235], a[40:43]\n" \
"s_mov_b32 exec_lo, 0xffffffff\n" \
"s_mov_b32 exec_hi, 0x0\n" \
"global_load_lds_dword v[188:189], off\n" \
"s_mov_b64 exec, -1\n" \
M_"a[44:47], a[136:139], a[172:175], a[44:47]\n" \
X_"a[246:247], v37 offset:41984\n" \
"v_lshlrev_b32_e64 v218, 10, v212\n" \
M_"a[44:47], a[200:203], a[236:239], a[44:47]\n" \
"v_lshlrev_b32_e32 v216, 7, v216\n" \
"v_and_b32_e32 v219, 24, v219\n" \
M_"a[60:63], a[140:143], a[172:175], a[60:63]\n" \
"ds_write_b128 v196, v[200:203]\n" \
M_"a[60:63], a[204:207], a[236:239], a[60:63]\n" \
"v_and_or_b32 v213, v213, 2, v215\n" \
"v_add_u32_e64 v217, s40, v217\n" \
M_"a[56:59], a[140:143], a[168:171], a[56:59]\n" \
X_"a[184:185], v38 offset:32768\n" \
"v_or3_b32 v216, v216, v218, v219\n" \
M_"a[56:59], a[204:207], a[232:235], a[56:59]\n" \
"v_lshlrev_b32_e64 v213, 5, v213\n" \
"s_mov_b32 s40, 0x14000\n" \
M_"a[52:55], a[140:143], a[164:167], a[52:55]\n" \
"s_lshl_b32 s7, s2, 2\n" \
"v_or3_b32 v48, v216, v213, s40\n" \
M_"a[52:55], a[204:207], a[228:231], a[52:55]\n" \
X_"a[186:187], v38 offset:33792\n" \
"s_lshl_b32 s40, s43, 2\n" \
M_"a[48:51], a[140:143], a[160:163], a[48:51]\n" \
"s_lshl_b32 s6, s6, 3\n" \
"v_or_b32_e32 v213, s40, v212\n" \
M_"a[48:51], a[204:207], a[224:227], a[48:51]\n" \
"s_or_b32 s6, s6, s40\n" \
"s_and_b32 s40, s7, 0x7c\n" \
M_"a[64:67], a[144:147], a[160:163], a[64:67]\n" \
"ds_write_b128 v197, v[204:207]\n" \
M_"a[64:67], a[208:211], a[224:227], a[64:67]\n" \
X_"a[248:249], v38 offset:40960\n" \
"s_or_b32 s40, s40, s3\n" \
M_"a[68:71], a[144:147], a[164:167], a[68:71]\n" \
"s_cmpk_lt_u32 s40, 64\n" \
"s_cselect_b32 s41, s4, s5\n" \
M_"a[68:71], a[208:211], a[228:231], a[68:71]\n" \
X_"a[250:251], v38 offset:41984\n" \
"s_lshl_b32 s40, s40, 1\n" \
M_"a[72:75], a[144:147], a[168:171], a[72:75]\n" \
"s_lshl_b32 s41, s41, 7\n" \
"s_and_b32 s40, s40, 0x7e\n" \
M_"a[72:75], a[208:211], a[232:235], a[72:75]\n" \
"s_or_b32 s40, s41, s40\n" \
"s_mulk_i32 s40, 0x480\n" \
M_"a[76:79], a[144:147], a[172:175], a[76:79]\n" \
"v_add_u32_e64 v49, s40, v20\n" \
"s_mov_b32 s40, 0xc000\n" \
M_"a[76:79], a[208:211], a[236:239], a[76:79]\n" \
X_"a[188:189], v39 offset:32768\n" \
"s_and_b32 s7, s7, 28\n" \
M_"a[92:95], a[148:151], a[172:175], a[92:95]\n" \
"ds_write_b128 v198, v[208:211]\n" \
M_"a[92:95], a[212:215], a[236:239], a[92:95]\n" \
"s_bfe_u32 s45, s2, 0x20003\n" \
"s_mov_b32 s41, 0x8000\n" \
M_"a[88:91], a[148:151], a[168:171], a[88:91]\n" \
X_"a[190:191], v39 offset:33792\n" \
M_"a[88:91], a[212:215], a[232:235], a[88:91]\n" \
"s_cmp_lg_u32 s4, 0\n" \
"s_cselect_b32 s51, s40, s41\n" \
M_"a[84:87], a[148:151], a[164:167], a[84:87]\n" \
"s_movk_i32 s40, 0x4000\n" \
"s_lshr_b32 s44, s3, 1\n" \
M_"a[84:87], a[212:215], a[228:231], a[84:87]\n" \
X_"a[252:253], v39 offset:40960\n" \
"s_or_b32 s46, s7, s45\n" \
M_"a[80:83], a[148:151], a[160:163], a[80:83]\n" \
"s_cmp_lg_u32 s4, 0\n" \
"s_cselect_b32 s53, s41, s40\n" \
M_"a[80:83], a[212:215], a[224:227], a[80:83]\n" \
"s_lshl_b32 s40, s44, 15\n" \
"s_lshl_b32 s41, s43, 14\n" \
M_"a[96:99], a[152:155], a[160:163], a[96:99]\n" \
X_"a[254:255], v39 offset:41984\n" \
"s_lshl_b32 s48, s46, 20\n" \
M_"a[96:99], a[216:219], a[224:227], a[96:99]\n" \
"s_or_b32 s41, s40, s41\n" \
"s_add_i32 s49, s48, 0x1000000\n" \
M_"a[100:103], a[152:155], a[164:167], a[100:103]\n" \
"s_or_b32 s42, s49, s41\n" \
"s_lshl_b32 s4, s4, 16\n" \
M_"a[100:103], a[216:219], a[228:231], a[100:103]\n" \
"s_or_b32 s41, s42, s4\n" \
"s_lshl_b32 s4, s5, 16\n" \
M_"a[104:107], a[152:155], a[168:171], a[104:107]\n" \
"s_mul_i32 s5, s6, 0x24800\n" \
"s_lshl_b32 s6, s46, 9\n" \
M_"a[104:107], a[216:219], a[232:235], a[104:107]\n" \
"s_lshl_b32 s7, s7, 6\n" \
"s_add_i32 s6, s5, s6\n" \
M_"a[108:111], a[152:155], a[172:175], a[108:111]\n" \
"s_or_b32 s5, s5, s7\n" \
"s_lshl_b32 s7, s45, 2\n" \
M_"a[108:111], a[216:219], a[236:239], a[108:111]\n" \
"s_or_b32 s5, s5, s7\n" \
"s_lshl_b32 s7, s3, 12\n" \
"s_waitcnt lgkmcnt(15)\n" \
M_"a[124:127], a[156:159], a[172:175], a[124:127]\n" \
"s_or_b32 s50, s49, s7\n" \
"s_lshl_b32 s7, s3, 11\n" \
M_"a[124:127], a[220:223], a[236:239], a[124:127]\n" \
"v_lshrrev_b32_e64 v215, 4, v20\n" \
"v_bfe_u32 v216, v20, 4, 3\n" \
M_"a[120:123], a[156:159], a[168:171], a[120:123]\n" \
"s_or_b32 s42, s42, s4\n" \
"s_lshl_b32 s4, s43, 11\n" \
M_"a[120:123], a[220:223], a[232:235], a[120:123]\n" \
"s_add_i32 s52, s48, s7\n" \
"s_add_i32 s53, s53, s7\n" \
M_"a[116:119], a[156:159], a[164:167], a[116:119]\n" \
"v_mul_u32_u24_e32 v214, 0x480, v214\n" \
"v_bitop3_b32 v218, v212, v215, 7 bitop3:0x78\n" \
M_"a[116:119], a[220:223], a[228:231], a[116:119]\n" \
"v_bitop3_b32 v212, v212, v216, 4 bitop3:0x36\n" \
"s_lshl_b32 s7, s3, 10\n" \
M_"a[112:115], a[156:159], a[160:163], a[112:115]\n" \
"s_or_b32 s4, s40, s4\n" \
"v_lshl_or_b32 v25, v212, 4, v214\n" \
"s_waitcnt vmcnt(1) lgkmcnt(0)\n" \
M_"a[112:115], a[220:223], a[224:227], a[112:115]\n" \
"s_barrier\n" \
M_"v[52:55], a[128:131], a[176:179], v[52:55]\n" \
"s_add_u32 m0, s30, 0x8000\n" \
"s_movk_i32 s84, 0x280\n" \
M_"v[52:55], a[192:195], a[240:243], v[52:55]\n" \
B_"v22, s[64:67], s84 offen offset:0 lds\n" \
M_"v[56:59], a[128:131], a[180:183], v[56:59]\n" \
"s_movk_i32 s84, 0x1e80\n" \
M_"v[56:59], a[192:195], a[244:247], v[56:59]\n" \
B_"v23, s[64:67], s84 offen offset:1024 lds\n" \
M_"v[60:63], a[128:131], a[184:187], v[60:63]\n" \
X_"a[160:161], v32 offset:0\n" \
"v_mul_u32_u24_e32 v212, 0x480, v217\n" \
M_"v[60:63], a[192:195], a[248:251], v[60:63]\n" \
"s_movk_i32 s84, 0x3a80\n" \
M_"v[64:67], a[128:131], a[188:191], v[64:67]\n" \
B_"v22, s[64:67], s84 offen offset:2048 lds\n" \
M_"v[64:67], a[192:195], a[252:255], v[64:67]\n" \
R_"a[128:131], v28 offset:16384\n" \
"v_bitop3_b32 v213, v215, v213, 7 bitop3:0x6c\n" \
M_"v[80:83], a[132:135], a[188:191], v[80:83]\n" \
R_"a[192:195], v29 offset:16384\n" \
"s_or_b32 s40, s7, 0x14000\n" \
M_"v[80:83], a[196:199], a[252:255], v[80:83]\n" \
"s_movk_i32 s84, 0x5680\n" \
M_"v[76:79], a[132:135], a[184:187], v[76:79]\n" \
B_"v23, s[64:67], s84 offen offset:3072 lds\n" \
M_"v[76:79], a[196:199], a[248:251], v[76:79]\n" \
"s_add_u32 m0, s31, 0x8000\n" \
"s_mov_b32 s84, 0x280000\n" \
M_"v[72:75], a[132:135], a[180:183], v[72:75]\n" \
B_"v26, s[68:71], s84 offen offset:0 lds\n" \
M_"v[72:75], a[196:199], a[244:247], v[72:75]\n" \
X_"a[162:163], v32 offset:1024\n" \
"s_lshl_b32 s43, s44, 13\n" \
M_"v[68:71], a[132:135], a[176:179], v[68:71]\n" \
X_"a[224:225], v32 offset:8192\n" \
"s_mulk_i32 s44, 0xe800\n" \
M_"v[68:71], a[196:199], a[240:243], v[68:71]\n" \
R_"a[132:135], v28 offset:18432\n" \
"s_lshl_b32 s7, s3, 8\n" \
M_"v[84:87], a[136:139], a[176:179], v[84:87]\n" \
R_"a[196:199], v29 offset:18432\n" \
"v_lshl_or_b32 v24, v218, 4, v214\n" \
M_"v[84:87], a[200:203], a[240:243], v[84:87]\n" \
"s_addk_i32 s84, 0x7c00\n" \
M_"v[88:91], a[136:139], a[180:183], v[88:91]\n" \
B_"v26, s[68:71], s84 offen offset:1024 lds\n" \
M_"v[88:91], a[200:203], a[244:247], v[88:91]\n" \
"s_addk_i32 s84, 0x7c00\n" \
M_"v[92:95], a[136:139], a[184:187], v[92:95]\n" \
B_"v27, s[68:71], s84 offen offset:2048 lds\n" \
M_"v[92:95], a[200:203], a[248:251], v[92:95]\n" \
"s_addk_i32 s84, 0x7c00\n" \
M_"v[96:99], a[136:139], a[188:191], v[96:99]\n" \
B_"v27, s[68:71], s84 offen offset:3072 lds\n" \
M_"v[96:99], a[200:203], a[252:255], v[96:99]\n" \
R_"a[136:139], v28 offset:20480\n" \
"v_lshl_or_b32 v21, v213, 4, v212\n" \
M_"v[112:115], a[140:143], a[188:191], v[112:115]\n" \
R_"a[200:203], v29 offset:20480\n" \
"s_add_i32 s44, s44, 0x17000\n" \
M_"v[112:115], a[204:207], a[252:255], v[112:115]\n" \
"s_add_u32 m0, s30, 0x9000\n" \
"s_mov_b32 s84, 0x8280\n" \
M_"v[108:111], a[140:143], a[184:187], v[108:111]\n" \
B_"v22, s[64:67], s84 offen offset:0 lds\n" \
M_"v[108:111], a[204:207], a[248:251], v[108:111]\n" \
"s_addk_i32 s84, 0x1c00\n" \
M_"v[104:107], a[140:143], a[180:183], v[104:107]\n" \
B_"v23, s[64:67], s84 offen offset:1024 lds\n" \
M_"v[104:107], a[204:207], a[244:247], v[104:107]\n" \
"s_addk_i32 s84, 0x1c00\n" \
M_"v[100:103], a[140:143], a[176:179], v[100:103]\n" \
B_"v22, s[64:67], s84 offen offset:2048 lds\n" \
M_"v[100:103], a[204:207], a[240:243], v[100:103]\n" \
R_"a[140:143], v28 offset:22528\n" \
"s_or_b32 s45, s7, 0x27000\n" \
M_"v[116:119], a[144:147], a[176:179], v[116:119]\n" \
R_"a[204:207], v29 offset:22528\n" \
"s_or_b32 s46, s4, 0x10000\n" \
M_"v[116:119], a[208:211], a[240:243], v[116:119]\n" \
"s_addk_i32 s84, 0x1c00\n" \
M_"v[120:123], a[144:147], a[180:183], v[120:123]\n" \
B_"v23, s[64:67], s84 offen offset:3072 lds\n" \
M_"v[120:123], a[208:211], a[244:247], v[120:123]\n" \
"s_add_u32 m0, s31, 0xc000\n" \
"s_mov_b32 s84, 0x280000\n" \
M_"v[124:127], a[144:147], a[184:187], v[124:127]\n" \
B_"v26, s[72:75], s84 offen offset:0 lds\n" \
M_"v[124:127], a[208:211], a[248:251], v[124:127]\n" \
"s_addk_i32 s84, 0x7c00\n" \
M_"v[128:131], a[144:147], a[188:191], v[128:131]\n" \
B_"v26, s[72:75], s84 offen offset:1024 lds\n" \
M_"v[128:131], a[208:211], a[252:255], v[128:131]\n" \
R_"a[144:147], v28 offset:0\n" \
"s_add_i32 s48, s6, 0x3000000\n" \
M_"v[144:147], a[148:151], a[188:191], v[144:147]\n" \
R_"a[208:211], v29 offset:0\n" \
"s_add_i32 s49, s5, 0x3004000\n" \
M_"v[144:147], a[212:215], a[252:255], v[144:147]\n" \
R_"v[200:203], v199 offset:0\n" \
"s_add_i32 s52, s52, 0x1010000\n" \
M_"v[140:143], a[148:151], a[184:187], v[140:143]\n" \
R_"v[204:207], v199 offset:4096\n" \
M_"v[140:143], a[212:215], a[248:251], v[140:143]\n" \
X_"a[226:227], v32 offset:9216\n" \
M_"v[136:139], a[148:151], a[180:183], v[136:139]\n" \
X_"a[164:165], v33 offset:0\n" \
M_"v[136:139], a[212:215], a[244:247], v[136:139]\n" \
R_"v[208:211], v199 offset:8192\n" \
M_"v[132:135], a[148:151], a[176:179], v[132:135]\n" \
X_"a[166:167], v33 offset:1024\n" \
M_"v[132:135], a[212:215], a[240:243], v[132:135]\n" \
R_"a[148:151], v28 offset:2048\n" \
M_"v[148:151], a[152:155], a[176:179], v[148:151]\n" \
R_"a[212:215], v29 offset:2048\n" \
M_"v[148:151], a[216:219], a[240:243], v[148:151]\n" \
X_"a[228:229], v33 offset:8192\n" \
M_"v[152:155], a[152:155], a[180:183], v[152:155]\n" \
X_"a[230:231], v33 offset:9216\n" \
M_"v[152:155], a[216:219], a[244:247], v[152:155]\n" \
X_"a[168:169], v34 offset:0\n" \
M_"v[156:159], a[152:155], a[184:187], v[156:159]\n" \
X_"a[170:171], v34 offset:1024\n" \
M_"v[156:159], a[216:219], a[248:251], v[156:159]\n" \
X_"a[232:233], v34 offset:8192\n" \
M_"v[160:163], a[152:155], a[188:191], v[160:163]\n" \
X_"a[234:235], v34 offset:9216\n" \
M_"v[160:163], a[216:219], a[252:255], v[160:163]\n" \
R_"a[152:155], v28 offset:4096\n" \
M_"v[176:179], a[156:159], a[188:191], v[176:179]\n" \
R_"a[216:219], v29 offset:4096\n" \
M_"v[176:179], a[220:223], a[252:255], v[176:179]\n" \
X_"a[172:173], v35 offset:0\n" \
M_"v[172:175], a[156:159], a[184:187], v[172:175]\n" \
X_"a[174:175], v35 offset:1024\n" \
M_"v[172:175], a[220:223], a[248:251], v[172:175]\n" \
X_"a[236:237], v35 offset:8192\n" \
M_"v[168:171], a[156:159], a[180:183], v[168:171]\n" \
X_"a[238:239], v35 offset:9216\n" \
M_"v[168:171], a[220:223], a[244:247], v[168:171]\n" \
M_"v[164:167], a[156:159], a[176:179], v[164:167]\n" \
M_"v[164:167], a[220:223], a[240:243], v[164:167]\n" \
R_"a[156:159], v28 offset:6144\n" \
R_"a[220:223], v29 offset:6144\n" \
"s_waitcnt lgkmcnt(15)\n" \
M_"a[0:3], a[128:131], a[160:163], a[0:3]\n" \
"s_addk_i32 s84, 0x7c00\n" \
M_"a[0:3], a[192:195], a[224:227], a[0:3]\n" \
B_"v27, s[72:75], s84 offen offset:2048 lds\n" \
M_"a[4:7], a[128:131], a[164:167], a[4:7]\n" \
X_"a[176:177], v36 offset:0\n" \
"s_waitcnt lgkmcnt(13)\n" \
M_"a[4:7], a[192:195], a[228:231], a[4:7]\n" \
"s_addk_i32 s84, 0x7c00\n" \
"s_waitcnt lgkmcnt(11)\n" \
M_"a[8:11], a[128:131], a[168:171], a[8:11]\n" \
B_"v27, s[72:75], s84 offen offset:3072 lds\n" \
"s_waitcnt lgkmcnt(9)\n" \
M_"a[8:11], a[192:195], a[232:235], a[8:11]\n" \
X_"a[178:179], v36 offset:1024\n" \
"s_waitcnt lgkmcnt(6)\n" \
M_"a[12:15], a[128:131], a[172:175], a[12:15]\n" \
X_"a[240:241], v36 offset:8192\n" \
"s_waitcnt lgkmcnt(5)\n" \
M_"a[12:15], a[192:195], a[236:239], a[12:15]\n" \
M_"a[28:31], a[132:135], a[172:175], a[28:31]\n" \
M_"a[28:31], a[196:199], a[236:239], a[28:31]\n" \
X_"a[242:243], v36 offset:9216\n" \
M_"a[24:27], a[132:135], a[168:171], a[24:27]\n" \
M_"a[24:27], a[196:199], a[232:235], a[24:27]\n" \
M_"a[20:23], a[132:135], a[164:167], a[20:23]\n" \
M_"a[20:23], a[196:199], a[228:231], a[20:23]\n" \
X_"a[180:181], v37 offset:0\n" \
M_"a[16:19], a[132:135], a[160:163], a[16:19]\n" \
M_"a[16:19], a[196:199], a[224:227], a[16:19]\n" \
M_"a[32:35], a[136:139], a[160:163], a[32:35]\n" \
X_"a[182:183], v37 offset:1024\n" \
M_"a[32:35], a[200:203], a[224:227], a[32:35]\n" \
M_"a[36:39], a[136:139], a[164:167], a[36:39]\n" \
M_"a[36:39], a[200:203], a[228:231], a[36:39]\n" \
"buffer_store_dwordx4 v[200:203], v20, s[80:83], s35 offen sc1\n" \
".fill (L_pb + 52960 - .) / 4, 4, 0xBF800000\n" \
"L_pp13:\n" \
"s_cbranch_execz L_pret\n" \
M_"a[40:43], a[136:139], a[168:171], a[40:43]\n" \
"s_mov_b32 m0, s32\n" \
X_"a[244:245], v37 offset:8192\n" \
M_"a[40:43], a[200:203], a[232:235], a[40:43]\n" \
"s_mov_b32 exec_lo, 0x0\n" \
"s_mov_b32 exec_hi, 0xffffffff\n" \
"global_load_lds_dword v[188:189], off\n" \
"s_mov_b64 exec, -1\n" \
"s_nop 0\n" \
M_"a[44:47], a[136:139], a[172:175], a[44:47]\n" \
X_"a[246:247], v37 offset:9216\n" \
M_"a[44:47], a[200:203], a[236:239], a[44:47]\n" \
M_"a[60:63], a[140:143], a[172:175], a[60:63]\n" \
M_"a[60:63], a[204:207], a[236:239], a[60:63]\n" \
M_"a[56:59], a[140:143], a[168:171], a[56:59]\n" \
X_"a[184:185], v38 offset:0\n" \
M_"a[56:59], a[204:207], a[232:235], a[56:59]\n" \
M_"a[52:55], a[140:143], a[164:167], a[52:55]\n" \
"buffer_store_dwordx4 v[204:207], v20, s[80:83], s88 offen sc1\n" \
M_"a[52:55], a[204:207], a[228:231], a[52:55]\n" \
X_"a[186:187], v38 offset:1024\n" \
M_"a[48:51], a[140:143], a[160:163], a[48:51]\n" \
M_"a[48:51], a[204:207], a[224:227], a[48:51]\n" \
M_"a[64:67], a[144:147], a[160:163], a[64:67]\n" \
X_"a[248:249], v38 offset:8192\n" \
M_"a[64:67], a[208:211], a[224:227], a[64:67]\n" \
M_"a[68:71], a[144:147], a[164:167], a[68:71]\n" \
M_"a[68:71], a[208:211], a[228:231], a[68:71]\n" \
X_"a[250:251], v38 offset:9216\n" \
M_"a[72:75], a[144:147], a[168:171], a[72:75]\n" \
M_"a[72:75], a[208:211], a[232:235], a[72:75]\n" \
M_"a[76:79], a[144:147], a[172:175], a[76:79]\n" \
"buffer_store_dwordx4 v[208:211], v20, s[80:83], s36 offen sc1\n" \
M_"a[76:79], a[208:211], a[236:239], a[76:79]\n" \
X_"a[188:189], v39 offset:0\n" \
M_"a[92:95], a[148:151], a[172:175], a[92:95]\n" \
M_"a[92:95], a[212:215], a[236:239], a[92:95]\n" \
M_"a[88:91], a[148:151], a[168:171], a[88:91]\n" \
X_"a[190:191], v39 offset:1024\n" \
M_"a[88:91], a[212:215], a[232:235], a[88:91]\n" \
M_"a[84:87], a[148:151], a[164:167], a[84:87]\n" \
M_"a[84:87], a[212:215], a[228:231], a[84:87]\n" \
X_"a[252:253], v39 offset:8192\n" \
M_"a[80:83], a[148:151], a[160:163], a[80:83]\n" \
M_"a[80:83], a[212:215], a[224:227], a[80:83]\n" \
M_"a[96:99], a[152:155], a[160:163], a[96:99]\n" \
X_"a[254:255], v39 offset:9216\n" \
M_"a[96:99], a[216:219], a[224:227], a[96:99]\n" \
M_"a[100:103], a[152:155], a[164:167], a[100:103]\n" \
M_"a[100:103], a[216:219], a[228:231], a[100:103]\n" \
M_"a[104:107], a[152:155], a[168:171], a[104:107]\n" \
M_"a[104:107], a[216:219], a[232:235], a[104:107]\n" \
M_"a[108:111], a[152:155], a[172:175], a[108:111]\n" \
M_"a[108:111], a[216:219], a[236:239], a[108:111]\n" \
"s_waitcnt lgkmcnt(15)\n" \
M_"a[124:127], a[156:159], a[172:175], a[124:127]\n" \
M_"a[124:127], a[220:223], a[236:239], a[124:127]\n" \
M_"a[120:123], a[156:159], a[168:171], a[120:123]\n" \
M_"a[120:123], a[220:223], a[232:235], a[120:123]\n" \
M_"a[116:119], a[156:159], a[164:167], a[116:119]\n" \
M_"a[116:119], a[220:223], a[228:231], a[116:119]\n" \
M_"a[112:115], a[156:159], a[160:163], a[112:115]\n" \
"s_waitcnt vmcnt(4) lgkmcnt(0)\n" \
M_"a[112:115], a[220:223], a[224:227], a[112:115]\n" \
"s_barrier\n" \
M_"v[52:55], a[128:131], a[176:179], v[52:55]\n" \
"s_add_u32 m0, s30, 0x0\n" \
"s_movk_i32 s84, 0x300\n" \
M_"v[52:55], a[192:195], a[240:243], v[52:55]\n" \
B_"v22, s[64:67], s84 offen offset:0 lds\n" \
M_"v[56:59], a[128:131], a[180:183], v[56:59]\n" \
X_"a[160:161], v32 offset:32768\n" \
M_"v[56:59], a[192:195], a[244:247], v[56:59]\n" \
"s_movk_i32 s84, 0x1f00\n" \
M_"v[60:63], a[128:131], a[184:187], v[60:63]\n" \
B_"v23, s[64:67], s84 offen offset:1024 lds\n" \
M_"v[60:63], a[192:195], a[248:251], v[60:63]\n" \
X_"a[162:163], v32 offset:33792\n" \
M_"v[64:67], a[128:131], a[188:191], v[64:67]\n" \
X_"a[224:225], v32 offset:40960\n" \
M_"v[64:67], a[192:195], a[252:255], v[64:67]\n" \
R_"a[128:131], v28 offset:49152\n" \
M_"v[80:83], a[132:135], a[188:191], v[80:83]\n" \
R_"a[192:195], v29 offset:49152\n" \
M_"v[80:83], a[196:199], a[252:255], v[80:83]\n" \
"s_movk_i32 s84, 0x3b00\n" \
M_"v[76:79], a[132:135], a[184:187], v[76:79]\n" \
B_"v22, s[64:67], s84 offen offset:2048 lds\n" \
M_"v[76:79], a[196:199], a[248:251], v[76:79]\n" \
"s_movk_i32 s84, 0x5700\n" \
M_"v[72:75], a[132:135], a[180:183], v[72:75]\n" \
B_"v23, s[64:67], s84 offen offset:3072 lds\n" \
M_"v[72:75], a[196:199], a[244:247], v[72:75]\n" \
"s_add_u32 m0, s31, 0x0\n" \
"s_mov_b32 s84, 0x300000\n" \
M_"v[68:71], a[132:135], a[176:179], v[68:71]\n" \
B_"v26, s[68:71], s84 offen offset:0 lds\n" \
M_"v[68:71], a[196:199], a[240:243], v[68:71]\n" \
R_"a[132:135], v28 offset:51200\n" \
M_"v[84:87], a[136:139], a[176:179], v[84:87]\n" \
R_"a[196:199], v29 offset:51200\n" \
M_"v[84:87], a[200:203], a[240:243], v[84:87]\n" \
"s_addk_i32 s84, 0x7c00\n" \
M_"v[88:91], a[136:139], a[180:183], v[88:91]\n" \
B_"v26, s[68:71], s84 offen offset:1024 lds\n" \
M_"v[88:91], a[200:203], a[244:247], v[88:91]\n" \
X_"a[226:227], v32 offset:41984\n" \
M_"v[92:95], a[136:139], a[184:187], v[92:95]\n" \
"s_addk_i32 s84, 0x7c00\n" \
M_"v[92:95], a[200:203], a[248:251], v[92:95]\n" \
B_"v27, s[68:71], s84 offen offset:2048 lds\n" \
M_"v[96:99], a[136:139], a[188:191], v[96:99]\n" \
X_"a[164:165], v33 offset:32768\n" \
M_"v[96:99], a[200:203], a[252:255], v[96:99]\n" \
R_"a[136:139], v28 offset:53248\n" \
M_"v[112:115], a[140:143], a[188:191], v[112:115]\n" \
R_"a[200:203], v29 offset:53248\n" \
M_"v[112:115], a[204:207], a[252:255], v[112:115]\n" \
"s_addk_i32 s84, 0x7c00\n" \
M_"v[108:111], a[140:143], a[184:187], v[108:111]\n" \
B_"v27, s[68:71], s84 offen offset:3072 lds\n" \
M_"v[108:111], a[204:207], a[248:251], v[108:111]\n" \
"s_add_u32 m0, s30, 0x1000\n" \
"s_mov_b32 s84, 0x8300\n" \
M_"v[104:107], a[140:143], a[180:183], v[104:107]\n" \
B_"v22, s[64:67], s84 offen offset:0 lds\n" \
M_"v[104:107], a[204:207], a[244:247], v[104:107]\n" \
X_"a[166:167], v33 offset:33792\n" \
M_"v[100:103], a[140:143], a[176:179], v[100:103]\n" \
X_"a[228:229], v33 offset:40960\n" \
M_"v[100:103], a[204:207], a[240:243], v[100:103]\n" \
R_"a[140:143], v28 offset:55296\n" \
M_"v[116:119], a[144:147], a[176:179], v[116:119]\n" \
R_"a[204:207], v29 offset:55296\n" \
M_"v[116:119], a[208:211], a[240:243], v[116:119]\n" \
"s_addk_i32 s84, 0x1c00\n" \
M_"v[120:123], a[144:147], a[180:183], v[120:123]\n" \
B_"v23, s[64:67], s84 offen offset:1024 lds\n" \
M_"v[120:123], a[208:211], a[244:247], v[120:123]\n" \
"s_addk_i32 s84, 0x1c00\n" \
M_"v[124:127], a[144:147], a[184:187], v[124:127]\n" \
B_"v22, s[64:67], s84 offen offset:2048 lds\n" \
M_"v[124:127], a[208:211], a[248:251], v[124:127]\n" \
"s_addk_i32 s84, 0x1c00\n" \
"s_nop 0\n" \
M_"v[128:131], a[144:147], a[188:191], v[128:131]\n" \
B_"v23, s[64:67], s84 offen offset:3072 lds\n" \
M_"v[128:131], a[208:211], a[252:255], v[128:131]\n" \
R_"a[144:147], v28 offset:32768\n" \
M_"v[144:147], a[148:151], a[188:191], v[144:147]\n" \
R_"a[208:211], v29 offset:32768\n" \
M_"v[144:147], a[212:215], a[252:255], v[144:147]\n" \
"s_add_u32 m0, s31, 0x4000\n" \
"s_mov_b32 s84, 0x300000\n" \
M_"v[140:143], a[148:151], a[184:187], v[140:143]\n" \
B_"v26, s[72:75], s84 offen offset:0 lds\n" \
M_"v[140:143], a[212:215], a[248:251], v[140:143]\n" \
X_"a[230:231], v33 offset:41984\n" \
M_"v[136:139], a[148:151], a[180:183], v[136:139]\n" \
"s_addk_i32 s84, 0x7c00\n" \
"s_nop 0\n" \
M_"v[136:139], a[212:215], a[244:247], v[136:139]\n" \
B_"v26, s[72:75], s84 offen offset:1024 lds\n" \
M_"v[132:135], a[148:151], a[176:179], v[132:135]\n" \
X_"a[168:169], v34 offset:32768\n" \
M_"v[132:135], a[212:215], a[240:243], v[132:135]\n" \
R_"a[148:151], v28 offset:34816\n" \
M_"v[148:151], a[152:155], a[176:179], v[148:151]\n" \
R_"a[212:215], v29 offset:34816\n" \
M_"v[148:151], a[216:219], a[240:243], v[148:151]\n" \
X_"a[170:171], v34 offset:33792\n" \
M_"v[152:155], a[152:155], a[180:183], v[152:155]\n" \
X_"a[232:233], v34 offset:40960\n" \
M_"v[152:155], a[216:219], a[244:247], v[152:155]\n" \
X_"a[234:235], v34 offset:41984\n" \
M_"v[156:159], a[152:155], a[184:187], v[156:159]\n" \
X_"a[172:173], v35 offset:32768\n" \
M_"v[156:159], a[216:219], a[248:251], v[156:159]\n" \
X_"a[174:175], v35 offset:33792\n" \
M_"v[160:163], a[152:155], a[188:191], v[160:163]\n" \
X_"a[236:237], v35 offset:40960\n" \
M_"v[160:163], a[216:219], a[252:255], v[160:163]\n" \
R_"a[152:155], v28 offset:36864\n" \
M_"v[176:179], a[156:159], a[188:191], v[176:179]\n" \
R_"a[216:219], v29 offset:36864\n" \
M_"v[176:179], a[220:223], a[252:255], v[176:179]\n" \
X_"a[238:239], v35 offset:41984\n" \
M_"v[172:175], a[156:159], a[184:187], v[172:175]\n" \
M_"v[172:175], a[220:223], a[248:251], v[172:175]\n" \
M_"v[168:171], a[156:159], a[180:183], v[168:171]\n" \
M_"v[168:171], a[220:223], a[244:247], v[168:171]\n" \
M_"v[164:167], a[156:159], a[176:179], v[164:167]\n" \
M_"v[164:167], a[220:223], a[240:243], v[164:167]\n" \
R_"a[156:159], v28 offset:38912\n" \
R_"a[220:223], v29 offset:38912\n" \
"s_waitcnt lgkmcnt(15)\n" \
M_"a[0:3], a[128:131], a[160:163], a[0:3]\n" \
"s_addk_i32 s84, 0x7c00\n" \
M_"a[0:3], a[192:195], a[224:227], a[0:3]\n" \
B_"v27, s[72:75], s84 offen offset:2048 lds\n" \
M_"a[4:7], a[128:131], a[164:167], a[4:7]\n" \
X_"a[176:177], v36 offset:32768\n" \
"s_waitcnt lgkmcnt(15)\n" \
M_"a[4:7], a[192:195], a[228:231], a[4:7]\n" \
"s_addk_i32 s84, 0x7c00\n" \
"s_waitcnt lgkmcnt(11)\n" \
M_"a[8:11], a[128:131], a[168:171], a[8:11]\n" \
B_"v27, s[72:75], s84 offen offset:3072 lds\n" \
"s_waitcnt lgkmcnt(9)\n" \
M_"a[8:11], a[192:195], a[232:235], a[8:11]\n" \
X_"a[178:179], v36 offset:33792\n" \
"s_waitcnt lgkmcnt(8)\n" \
M_"a[12:15], a[128:131], a[172:175], a[12:15]\n" \
X_"a[240:241], v36 offset:40960\n" \
"s_waitcnt lgkmcnt(5)\n" \
M_"a[12:15], a[192:195], a[236:239], a[12:15]\n" \
M_"a[28:31], a[132:135], a[172:175], a[28:31]\n" \
M_"a[28:31], a[196:199], a[236:239], a[28:31]\n" \
X_"a[242:243], v36 offset:41984\n" \
M_"a[24:27], a[132:135], a[168:171], a[24:27]\n" \
M_"a[24:27], a[196:199], a[232:235], a[24:27]\n" \
M_"a[20:23], a[132:135], a[164:167], a[20:23]\n" \
M_"a[20:23], a[196:199], a[228:231], a[20:23]\n" \
X_"a[180:181], v37 offset:32768\n" \
M_"a[16:19], a[132:135], a[160:163], a[16:19]\n" \
M_"a[16:19], a[196:199], a[224:227], a[16:19]\n" \
M_"a[32:35], a[136:139], a[160:163], a[32:35]\n" \
X_"a[182:183], v37 offset:33792\n" \
M_"a[32:35], a[200:203], a[224:227], a[32:35]\n" \
M_"a[36:39], a[136:139], a[164:167], a[36:39]\n" \
"v_lshlrev_b32_e32 v45, 3, v20\n" \
"v_add_u32_e32 v45, s31, v45\n" \
M_"a[36:39], a[200:203], a[228:231], a[36:39]\n" \
"s_getpc_b64 s[86:87]\n" \
"s_sub_u32 s86, s86, 0x10800\n" \
"s_subb_u32 s87, s87, 0\n" \
"s_mov_b32 m0, s32\n" \
"s_nop 0\n" \
M_"a[40:43], a[136:139], a[168:171], a[40:43]\n" \
"global_load_lds_dword v45, s[86:87]\n" \
M_"a[40:43], a[200:203], a[232:235], a[40:43]\n" \
X_"a[244:245], v37 offset:40960\n" \
M_"a[44:47], a[136:139], a[172:175], a[44:47]\n" \
X_"a[246:247], v37 offset:41984\n" \
M_"a[44:47], a[200:203], a[236:239], a[44:47]\n" \
M_"a[60:63], a[140:143], a[172:175], a[60:63]\n" \
M_"a[60:63], a[204:207], a[236:239], a[60:63]\n" \
M_"a[56:59], a[140:143], a[168:171], a[56:59]\n" \
X_"a[184:185], v38 offset:32768\n" \
M_"a[56:59], a[204:207], a[232:235], a[56:59]\n" \
M_"a[52:55], a[140:143], a[164:167], a[52:55]\n" \
M_"a[52:55], a[204:207], a[228:231], a[52:55]\n" \
X_"a[186:187], v38 offset:33792\n" \
M_"a[48:51], a[140:143], a[160:163], a[48:51]\n" \
M_"a[48:51], a[204:207], a[224:227], a[48:51]\n" \
M_"a[64:67], a[144:147], a[160:163], a[64:67]\n" \
X_"a[248:249], v38 offset:40960\n" \
M_"a[64:67], a[208:211], a[224:227], a[64:67]\n" \
M_"a[68:71], a[144:147], a[164:167], a[68:71]\n" \
M_"a[68:71], a[208:211], a[228:231], a[68:71]\n" \
X_"a[250:251], v38 offset:41984\n" \
M_"a[72:75], a[144:147], a[168:171], a[72:75]\n" \
M_"a[72:75], a[208:211], a[232:235], a[72:75]\n" \
M_"a[76:79], a[144:147], a[172:175], a[76:79]\n" \
M_"a[76:79], a[208:211], a[236:239], a[76:79]\n" \
X_"a[188:189], v39 offset:32768\n" \
M_"a[92:95], a[148:151], a[172:175], a[92:95]\n" \
M_"a[92:95], a[212:215], a[236:239], a[92:95]\n" \
M_"a[88:91], a[148:151], a[168:171], a[88:91]\n" \
X_"a[190:191], v39 offset:33792\n" \
M_"a[88:91], a[212:215], a[232:235], a[88:91]\n" \
M_"a[84:87], a[148:151], a[164:167], a[84:87]\n" \
M_"a[84:87], a[212:215], a[228:231], a[84:87]\n" \
X_"a[252:253], v39 offset:40960\n" \
M_"a[80:83], a[148:151], a[160:163], a[80:83]\n" \
M_"a[80:83], a[212:215], a[224:227], a[80:83]\n" \
M_"a[96:99], a[152:155], a[160:163], a[96:99]\n" \
X_"a[254:255], v39 offset:41984\n" \
M_"a[96:99], a[216:219], a[224:227], a[96:99]\n" \
M_"a[100:103], a[152:155], a[164:167], a[100:103]\n" \
M_"a[100:103], a[216:219], a[228:231], a[100:103]\n" \
M_"a[104:107], a[152:155], a[168:171], a[104:107]\n" \
M_"a[104:107], a[216:219], a[232:235], a[104:107]\n" \
M_"a[108:111], a[152:155], a[172:175], a[108:111]\n" \
M_"a[108:111], a[216:219], a[236:239], a[108:111]\n" \
"s_waitcnt lgkmcnt(15)\n" \
M_"a[124:127], a[156:159], a[172:175], a[124:127]\n" \
M_"a[124:127], a[220:223], a[236:239], a[124:127]\n" \
M_"a[120:123], a[156:159], a[168:171], a[120:123]\n" \
M_"a[120:123], a[220:223], a[232:235], a[120:123]\n" \
M_"a[116:119], a[156:159], a[164:167], a[116:119]\n" \
M_"a[116:119], a[220:223], a[228:231], a[116:119]\n" \
M_"a[112:115], a[156:159], a[160:163], a[112:115]\n" \
"s_waitcnt vmcnt(1) lgkmcnt(0)\n" \
M_"a[112:115], a[220:223], a[224:227], a[112:115]\n" \
"s_barrier\n" \
"v_mov_b32_e32 v45, s62\n" \
"s_mov_b64 exec, 1\n" \
"buffer_store_dword v45, off, s[80:83], s37 offset:128 sc1\n" \
"s_mov_b64 exec, -1\n" \
M_"v[52:55], a[128:131], a[176:179], v[52:55]\n" \
"s_add_u32 m0, s30, 0x8000\n" \
"s_movk_i32 s84, 0x380\n" \
M_"v[52:55], a[192:195], a[240:243], v[52:55]\n" \
B_"v22, s[64:67], s84 offen offset:0 lds\n" \
M_"v[56:59], a[128:131], a[180:183], v[56:59]\n" \
X_"a[160:161], v32 offset:0\n" \
M_"v[56:59], a[192:195], a[244:247], v[56:59]\n" \
"s_movk_i32 s84, 0x1f80\n" \
M_"v[60:63], a[128:131], a[184:187], v[60:63]\n" \
B_"v23, s[64:67], s84 offen offset:1024 lds\n" \
M_"v[60:63], a[192:195], a[248:251], v[60:63]\n" \
X_"a[162:163], v32 offset:1024\n" \
M_"v[64:67], a[128:131], a[188:191], v[64:67]\n" \
X_"a[224:225], v32 offset:8192\n" \
M_"v[64:67], a[192:195], a[252:255], v[64:67]\n" \
R_"a[128:131], v28 offset:16384\n" \
M_"v[80:83], a[132:135], a[188:191], v[80:83]\n" \
R_"a[192:195], v29 offset:16384\n" \
M_"v[80:83], a[196:199], a[252:255], v[80:83]\n" \
"s_movk_i32 s84, 0x3b80\n" \
M_"v[76:79], a[132:135], a[184:187], v[76:79]\n" \
B_"v22, s[64:67], s84 offen offset:2048 lds\n" \
M_"v[76:79], a[196:199], a[248:251], v[76:79]\n" \
"s_movk_i32 s84, 0x5780\n" \
M_"v[72:75], a[132:135], a[180:183], v[72:75]\n" \
B_"v23, s[64:67], s84 offen offset:3072 lds\n" \
M_"v[72:75], a[196:199], a[244:247], v[72:75]\n" \
"s_add_u32 m0, s31, 0x8000\n" \
"s_mov_b32 s84, 0x380000\n" \
M_"v[68:71], a[132:135], a[176:179], v[68:71]\n" \
B_"v26, s[68:71], s84 offen offset:0 lds\n" \
M_"v[68:71], a[196:199], a[240:243], v[68:71]\n" \
R_"a[132:135], v28 offset:18432\n" \
M_"v[84:87], a[136:139], a[176:179], v[84:87]\n" \
R_"a[196:199], v29 offset:18432\n" \
M_"v[84:87], a[200:203], a[240:243], v[84:87]\n" \
"s_addk_i32 s84, 0x7c00\n" \
M_"v[88:91], a[136:139], a[180:183], v[88:91]\n" \
B_"v26, s[68:71], s84 offen offset:1024 lds\n" \
M_"v[88:91], a[200:203], a[244:247], v[88:91]\n" \
X_"a[226:227], v32 offset:9216\n" \
M_"v[92:95], a[136:139], a[184:187], v[92:95]\n" \
"s_addk_i32 s84, 0x7c00\n" \
M_"v[92:95], a[200:203], a[248:251], v[92:95]\n" \
B_"v27, s[68:71], s84 offen offset:2048 lds\n" \
M_"v[96:99], a[136:139], a[188:191], v[96:99]\n" \
X_"a[164:165], v33 offset:0\n" \
M_"v[96:99], a[200:203], a[252:255], v[96:99]\n" \
R_"a[136:139], v28 offset:20480\n" \
M_"v[112:115], a[140:143], a[188:191], v[112:115]\n" \
R_"a[200:203], v29 offset:20480\n" \
M_"v[112:115], a[204:207], a[252:255], v[112:115]\n" \
"s_addk_i32 s84, 0x7c00\n" \
"s_nop 0\n" \
M_"v[108:111], a[140:143], a[184:187], v[108:111]\n" \
B_"v27, s[68:71], s84 offen offset:3072 lds\n" \
M_"v[108:111], a[204:207], a[248:251], v[108:111]\n" \
"s_add_u32 m0, s30, 0x9000\n" \
"s_mov_b32 s84, 0x8380\n" \
M_"v[104:107], a[140:143], a[180:183], v[104:107]\n" \
B_"v22, s[64:67], s84 offen offset:0 lds\n" \
M_"v[104:107], a[204:207], a[244:247], v[104:107]\n" \
X_"a[166:167], v33 offset:1024\n" \
M_"v[100:103], a[140:143], a[176:179], v[100:103]\n" \
X_"a[228:229], v33 offset:8192\n" \
M_"v[100:103], a[204:207], a[240:243], v[100:103]\n" \
R_"a[140:143], v28 offset:22528\n" \
M_"v[116:119], a[144:147], a[176:179], v[116:119]\n" \
R_"a[204:207], v29 offset:22528\n" \
M_"v[116:119], a[208:211], a[240:243], v[116:119]\n" \
"s_addk_i32 s84, 0x1c00\n" \
M_"v[120:123], a[144:147], a[180:183], v[120:123]\n" \
B_"v23, s[64:67], s84 offen offset:1024 lds\n" \
M_"v[120:123], a[208:211], a[244:247], v[120:123]\n" \
"s_addk_i32 s84, 0x1c00\n" \
M_"v[124:127], a[144:147], a[184:187], v[124:127]\n" \
B_"v22, s[64:67], s84 offen offset:2048 lds\n" \
M_"v[124:127], a[208:211], a[248:251], v[124:127]\n" \
"s_addk_i32 s84, 0x1c00\n" \
"s_nop 0\n" \
M_"v[128:131], a[144:147], a[188:191], v[128:131]\n" \
B_"v23, s[64:67], s84 offen offset:3072 lds\n" \
M_"v[128:131], a[208:211], a[252:255], v[128:131]\n" \
R_"a[144:147], v28 offset:0\n" \
M_"v[144:147], a[148:151], a[188:191], v[144:147]\n" \
R_"a[208:211], v29 offset:0\n" \
M_"v[144:147], a[212:215], a[252:255], v[144:147]\n" \
"s_add_u32 m0, s31, 0xc000\n" \
"s_mov_b32 s84, 0x380000\n" \
M_"v[140:143], a[148:151], a[184:187], v[140:143]\n" \
B_"v26, s[72:75], s84 offen offset:0 lds\n" \
M_"v[140:143], a[212:215], a[248:251], v[140:143]\n" \
X_"a[230:231], v33 offset:9216\n" \
M_"v[136:139], a[148:151], a[180:183], v[136:139]\n" \
"s_addk_i32 s84, 0x7c00\n" \
"s_nop 0\n" \
M_"v[136:139], a[212:215], a[244:247], v[136:139]\n" \
B_"v26, s[72:75], s84 offen offset:1024 lds\n" \
M_"v[132:135], a[148:151], a[176:179], v[132:135]\n" \
X_"a[168:169], v34 offset:0\n" \
M_"v[132:135], a[212:215], a[240:243], v[132:135]\n" \
R_"a[148:151], v28 offset:2048\n" \
M_"v[148:151], a[152:155], a[176:179], v[148:151]\n" \
R_"a[212:215], v29 offset:2048\n" \
M_"v[148:151], a[216:219], a[240:243], v[148:151]\n" \
X_"a[170:171], v34 offset:1024\n" \
M_"v[152:155], a[152:155], a[180:183], v[152:155]\n" \
X_"a[232:233], v34 offset:8192\n" \
M_"v[152:155], a[216:219], a[244:247], v[152:155]\n" \
X_"a[234:235], v34 offset:9216\n" \
M_"v[156:159], a[152:155], a[184:187], v[156:159]\n" \
X_"a[172:173], v35 offset:0\n" \
M_"v[156:159], a[216:219], a[248:251], v[156:159]\n" \
X_"a[174:175], v35 offset:1024\n" \
M_"v[160:163], a[152:155], a[188:191], v[160:163]\n" \
X_"a[236:237], v35 offset:8192\n" \
M_"v[160:163], a[216:219], a[252:255], v[160:163]\n" \
R_"a[152:155], v28 offset:4096\n" \
M_"v[176:179], a[156:159], a[188:191], v[176:179]\n" \
R_"a[216:219], v29 offset:4096\n" \
M_"v[176:179], a[220:223], a[252:255], v[176:179]\n" \
X_"a[238:239], v35 offset:9216\n" \
M_"v[172:175], a[156:159], a[184:187], v[172:175]\n" \
M_"v[172:175], a[220:223], a[248:251], v[172:175]\n" \
M_"v[168:171], a[156:159], a[180:183], v[168:171]\n" \
M_"v[168:171], a[220:223], a[244:247], v[168:171]\n" \
M_"v[164:167], a[156:159], a[176:179], v[164:167]\n" \
M_"v[164:167], a[220:223], a[240:243], v[164:167]\n" \
R_"a[156:159], v28 offset:6144\n" \
R_"a[220:223], v29 offset:6144\n" \
"s_waitcnt lgkmcnt(15)\n" \
M_"a[0:3], a[128:131], a[160:163], a[0:3]\n" \
"s_addk_i32 s84, 0x7c00\n" \
M_"a[0:3], a[192:195], a[224:227], a[0:3]\n" \
B_"v27, s[72:75], s84 offen offset:2048 lds\n" \
M_"a[4:7], a[128:131], a[164:167], a[4:7]\n" \
X_"a[176:177], v36 offset:0\n" \
"s_waitcnt lgkmcnt(15)\n" \
M_"a[4:7], a[192:195], a[228:231], a[4:7]\n" \
"s_addk_i32 s84, 0x7c00\n" \
"s_waitcnt lgkmcnt(11)\n" \
M_"a[8:11], a[128:131], a[168:171], a[8:11]\n" \
B_"v27, s[72:75], s84 offen offset:3072 lds\n" \
"s_waitcnt lgkmcnt(9)\n" \
M_"a[8:11], a[192:195], a[232:235], a[8:11]\n" \
X_"a[178:179], v36 offset:1024\n" \
"s_waitcnt lgkmcnt(8)\n" \
M_"a[12:15], a[128:131], a[172:175], a[12:15]\n" \
X_"a[240:241], v36 offset:8192\n" \
"s_waitcnt lgkmcnt(5)\n" \
M_"a[12:15], a[192:195], a[236:239], a[12:15]\n" \
M_"a[28:31], a[132:135], a[172:175], a[28:31]\n" \
M_"a[28:31], a[196:199], a[236:239], a[28:31]\n" \
X_"a[242:243], v36 offset:9216\n" \
M_"a[24:27], a[132:135], a[168:171], a[24:27]\n" \
M_"a[24:27], a[196:199], a[232:235], a[24:27]\n" \
M_"a[20:23], a[132:135], a[164:167], a[20:23]\n" \
M_"a[20:23], a[196:199], a[228:231], a[20:23]\n" \
X_"a[180:181], v37 offset:0\n" \
M_"a[16:19], a[132:135], a[160:163], a[16:19]\n" \
M_"a[16:19], a[196:199], a[224:227], a[16:19]\n" \
M_"a[32:35], a[136:139], a[160:163], a[32:35]\n" \
"v_bfe_u32 v44, v20, 2, 5\n" \
X_"a[182:183], v37 offset:1024\n" \
M_"a[32:35], a[200:203], a[224:227], a[32:35]\n" \
"buffer_load_dword v44, v44, s[80:83], s38 offen offset:128 sc1\n" \
M_"a[36:39], a[136:139], a[164:167], a[36:39]\n" \
M_"a[36:39], a[200:203], a[228:231], a[36:39]\n" \
X_"a[244:245], v37 offset:8192\n" \
M_"a[40:43], a[136:139], a[168:171], a[40:43]\n" \
M_"a[40:43], a[200:203], a[232:235], a[40:43]\n" \
M_"a[44:47], a[136:139], a[172:175], a[44:47]\n" \
X_"a[246:247], v37 offset:9216\n" \
M_"a[44:47], a[200:203], a[236:239], a[44:47]\n" \
M_"a[60:63], a[140:143], a[172:175], a[60:63]\n" \
"global_store_dwordx4 v49, v[0:3], s[28:29] offset:0 sc1\n" \
M_"a[60:63], a[204:207], a[236:239], a[60:63]\n" \
M_"a[56:59], a[140:143], a[168:171], a[56:59]\n" \
X_"a[184:185], v38 offset:0\n" \
M_"a[56:59], a[204:207], a[232:235], a[56:59]\n" \
M_"a[52:55], a[140:143], a[164:167], a[52:55]\n" \
M_"a[52:55], a[204:207], a[228:231], a[52:55]\n" \
X_"a[186:187], v38 offset:1024\n" \
M_"a[48:51], a[140:143], a[160:163], a[48:51]\n" \
M_"a[48:51], a[204:207], a[224:227], a[48:51]\n" \
M_"a[64:67], a[144:147], a[160:163], a[64:67]\n" \
"global_store_dwordx4 v49, v[0:3], s[28:29] offset:1024 sc1\n" \
M_"a[64:67], a[208:211], a[224:227], a[64:67]\n" \
X_"a[248:249], v38 offset:8192\n" \
M_"a[68:71], a[144:147], a[164:167], a[68:71]\n" \
M_"a[68:71], a[208:211], a[228:231], a[68:71]\n" \
X_"a[250:251], v38 offset:9216\n" \
M_"a[72:75], a[144:147], a[168:171], a[72:75]\n" \
M_"a[72:75], a[208:211], a[232:235], a[72:75]\n" \
M_"a[76:79], a[144:147], a[172:175], a[76:79]\n" \
M_"a[76:79], a[208:211], a[236:239], a[76:79]\n" \
X_"a[188:189], v39 offset:0\n" \
M_"a[92:95], a[148:151], a[172:175], a[92:95]\n" \
"s_mov_b64 exec, 0xffff\n" \
"global_store_dwordx4 v49, v[0:3], s[28:29] offset:2048 sc1\n" \
"s_mov_b64 exec, -1\n" \
"s_nop 0\n" \
M_"a[92:95], a[212:215], a[236:239], a[92:95]\n" \
M_"a[88:91], a[148:151], a[168:171], a[88:91]\n" \
X_"a[190:191], v39 offset:1024\n" \
M_"a[88:91], a[212:215], a[232:235], a[88:91]\n" \
M_"a[84:87], a[148:151], a[164:167], a[84:87]\n" \
M_"a[84:87], a[212:215], a[228:231], a[84:87]\n" \
X_"a[252:253], v39 offset:8192\n" \
M_"a[80:83], a[148:151], a[160:163], a[80:83]\n" \
M_"a[80:83], a[212:215], a[224:227], a[80:83]\n" \
M_"a[96:99], a[152:155], a[160:163], a[96:99]\n" \
X_"a[254:255], v39 offset:9216\n" \
M_"a[96:99], a[216:219], a[224:227], a[96:99]\n" \
M_"a[100:103], a[152:155], a[164:167], a[100:103]\n" \
M_"a[100:103], a[216:219], a[228:231], a[100:103]\n" \
M_"a[104:107], a[152:155], a[168:171], a[104:107]\n" \
M_"a[104:107], a[216:219], a[232:235], a[104:107]\n" \
M_"a[108:111], a[152:155], a[172:175], a[108:111]\n" \
M_"a[108:111], a[216:219], a[236:239], a[108:111]\n" \
"s_waitcnt lgkmcnt(15)\n" \
M_"a[124:127], a[156:159], a[172:175], a[124:127]\n" \
M_"a[124:127], a[220:223], a[236:239], a[124:127]\n" \
M_"a[120:123], a[156:159], a[168:171], a[120:123]\n" \
M_"a[120:123], a[220:223], a[232:235], a[120:123]\n" \
M_"a[116:119], a[156:159], a[164:167], a[116:119]\n" \
M_"a[116:119], a[220:223], a[228:231], a[116:119]\n" \
M_"a[112:115], a[156:159], a[160:163], a[112:115]\n" \
"s_waitcnt vmcnt(3) lgkmcnt(0)\n" \
M_"a[112:115], a[220:223], a[224:227], a[112:115]\n" \
"s_barrier\n" \
"v_cmp_ne_u32_e32 vcc, s62, v44\n" \
"s_nop 4\n" \
"s_cbranch_vccnz L_poll61_%=\n" \
"L_pollr61_%=:\n" \
"s_mov_b32 s68, s12\n" \
"s_and_b32 s69, s13, 0xffff\n" \
"s_mov_b32 s70, 0x480000\n" \
"s_mov_b32 s71, 0x20000\n" \
M_"v[52:55], a[128:131], a[176:179], v[52:55]\n" \
"s_add_u32 m0, s40, 0x0\n" \
"s_add_u32 s85, s39, 0x0\n" \
"s_nop 0\n" \
M_"v[52:55], a[192:195], a[240:243], v[52:55]\n" \
B_"v20, s[80:83], s85 offen sc1 lds\n" \
M_"v[56:59], a[128:131], a[180:183], v[56:59]\n" \
X_"a[160:161], v32 offset:32768\n" \
M_"v[56:59], a[192:195], a[244:247], v[56:59]\n" \
"s_add_u32 m0, s40, 0x1000\n" \
"s_add_u32 s85, s39, 0x1000\n" \
".fill (L_pb + 57056 - .) / 4, 4, 0xBF800000\n" \
"L_pp14:\n" \
"s_cbranch_execz L_pret\n" \
"s_nop 0\n" \
M_"v[60:63], a[128:131], a[184:187], v[60:63]\n" \
B_"v20, s[80:83], s85 offen sc1 lds\n" \
M_"v[60:63], a[192:195], a[248:251], v[60:63]\n" \
X_"a[162:163], v32 offset:33792\n" \
M_"v[64:67], a[128:131], a[188:191], v[64:67]\n" \
X_"a[224:225], v32 offset:40960\n" \
M_"v[64:67], a[192:195], a[252:255], v[64:67]\n" \
R_"a[128:131], v28 offset:49152\n" \
M_"v[80:83], a[132:135], a[188:191], v[80:83]\n" \
R_"a[192:195], v29 offset:49152\n" \
M_"v[80:83], a[196:199], a[252:255], v[80:83]\n" \
"s_add_u32 m0, s40, 0x3000\n" \
"s_movk_i32 s84, 0x0\n" \
M_"v[76:79], a[132:135], a[184:187], v[76:79]\n" \
B_"v21, s[76:79], s84 offen offset:0 lds\n" \
M_"v[76:79], a[196:199], a[248:251], v[76:79]\n" \
"s_add_u32 m0, s30, 0x0\n" \
"s_movk_i32 s84, 0x0\n" \
"s_nop 0\n" \
M_"v[72:75], a[132:135], a[180:183], v[72:75]\n" \
B_"v24, s[76:79], s84 offen offset:0 lds\n" \
M_"v[72:75], a[196:199], a[244:247], v[72:75]\n" \
X_"a[226:227], v32 offset:41984\n" \
M_"v[68:71], a[132:135], a[176:179], v[68:71]\n" \
X_"a[164:165], v33 offset:32768\n" \
M_"v[68:71], a[196:199], a[240:243], v[68:71]\n" \
R_"a[132:135], v28 offset:51200\n" \
M_"v[84:87], a[136:139], a[176:179], v[84:87]\n" \
R_"a[196:199], v29 offset:51200\n" \
M_"v[84:87], a[200:203], a[240:243], v[84:87]\n" \
"s_movk_i32 s84, 0x2000\n" \
M_"v[88:91], a[136:139], a[180:183], v[88:91]\n" \
B_"v25, s[76:79], s84 offen offset:1024 lds\n" \
M_"v[88:91], a[200:203], a[244:247], v[88:91]\n" \
"s_movk_i32 s84, 0x4000\n" \
M_"v[92:95], a[136:139], a[184:187], v[92:95]\n" \
B_"v24, s[76:79], s84 offen offset:2048 lds\n" \
M_"v[92:95], a[200:203], a[248:251], v[92:95]\n" \
X_"a[166:167], v33 offset:33792\n" \
M_"v[96:99], a[136:139], a[188:191], v[96:99]\n" \
X_"a[228:229], v33 offset:40960\n" \
M_"v[96:99], a[200:203], a[252:255], v[96:99]\n" \
R_"a[136:139], v28 offset:53248\n" \
M_"v[112:115], a[140:143], a[188:191], v[112:115]\n" \
R_"a[200:203], v29 offset:53248\n" \
M_"v[112:115], a[204:207], a[252:255], v[112:115]\n" \
"s_movk_i32 s84, 0x6000\n" \
M_"v[108:111], a[140:143], a[184:187], v[108:111]\n" \
B_"v25, s[76:79], s84 offen offset:3072 lds\n" \
M_"v[108:111], a[204:207], a[248:251], v[108:111]\n" \
"s_add_u32 m0, s31, 0x0\n" \
"s_movk_i32 s84, 0x0\n" \
M_"v[104:107], a[140:143], a[180:183], v[104:107]\n" \
B_"v26, s[68:71], s84 offen offset:0 lds\n" \
M_"v[104:107], a[204:207], a[244:247], v[104:107]\n" \
"s_movk_i32 s84, 0x7c00\n" \
M_"v[100:103], a[140:143], a[176:179], v[100:103]\n" \
B_"v26, s[68:71], s84 offen offset:1024 lds\n" \
M_"v[100:103], a[204:207], a[240:243], v[100:103]\n" \
R_"a[140:143], v28 offset:55296\n" \
M_"v[116:119], a[144:147], a[176:179], v[116:119]\n" \
R_"a[204:207], v29 offset:55296\n" \
M_"v[116:119], a[208:211], a[240:243], v[116:119]\n" \
"s_addk_i32 s84, 0x7c00\n" \
M_"v[120:123], a[144:147], a[180:183], v[120:123]\n" \
B_"v27, s[68:71], s84 offen offset:2048 lds\n" \
M_"v[120:123], a[208:211], a[244:247], v[120:123]\n" \
X_"a[230:231], v33 offset:41984\n" \
M_"v[124:127], a[144:147], a[184:187], v[124:127]\n" \
"s_addk_i32 s84, 0x7c00\n" \
M_"v[124:127], a[208:211], a[248:251], v[124:127]\n" \
B_"v27, s[68:71], s84 offen offset:3072 lds\n" \
M_"v[128:131], a[144:147], a[188:191], v[128:131]\n" \
X_"a[168:169], v34 offset:32768\n" \
M_"v[128:131], a[208:211], a[252:255], v[128:131]\n" \
R_"a[144:147], v28 offset:32768\n" \
M_"v[144:147], a[148:151], a[188:191], v[144:147]\n" \
R_"a[208:211], v29 offset:32768\n" \
M_"v[144:147], a[212:215], a[252:255], v[144:147]\n" \
"s_add_u32 m0, s30, 0x1000\n" \
"s_mov_b32 s84, 0x9000\n" \
M_"v[140:143], a[148:151], a[184:187], v[140:143]\n" \
B_"v24, s[76:79], s84 offen offset:0 lds\n" \
M_"v[140:143], a[212:215], a[248:251], v[140:143]\n" \
X_"a[170:171], v34 offset:33792\n" \
M_"v[136:139], a[148:151], a[180:183], v[136:139]\n" \
"s_addk_i32 s84, 0x2000\n" \
"s_nop 0\n" \
M_"v[136:139], a[212:215], a[244:247], v[136:139]\n" \
B_"v25, s[76:79], s84 offen offset:1024 lds\n" \
M_"v[132:135], a[148:151], a[176:179], v[132:135]\n" \
X_"a[232:233], v34 offset:40960\n" \
M_"v[132:135], a[212:215], a[240:243], v[132:135]\n" \
R_"a[148:151], v28 offset:34816\n" \
M_"v[148:151], a[152:155], a[176:179], v[148:151]\n" \
R_"a[212:215], v29 offset:34816\n" \
M_"v[148:151], a[216:219], a[240:243], v[148:151]\n" \
X_"a[234:235], v34 offset:41984\n" \
M_"v[152:155], a[152:155], a[180:183], v[152:155]\n" \
X_"a[172:173], v35 offset:32768\n" \
M_"v[152:155], a[216:219], a[244:247], v[152:155]\n" \
X_"a[174:175], v35 offset:33792\n" \
M_"v[156:159], a[152:155], a[184:187], v[156:159]\n" \
X_"a[236:237], v35 offset:40960\n" \
M_"v[156:159], a[216:219], a[248:251], v[156:159]\n" \
X_"a[238:239], v35 offset:41984\n" \
M_"v[160:163], a[152:155], a[188:191], v[160:163]\n" \
M_"v[160:163], a[216:219], a[252:255], v[160:163]\n" \
R_"a[152:155], v28 offset:36864\n" \
M_"v[176:179], a[156:159], a[188:191], v[176:179]\n" \
R_"a[216:219], v29 offset:36864\n" \
M_"v[176:179], a[220:223], a[252:255], v[176:179]\n" \
M_"v[172:175], a[156:159], a[184:187], v[172:175]\n" \
M_"v[172:175], a[220:223], a[248:251], v[172:175]\n" \
M_"v[168:171], a[156:159], a[180:183], v[168:171]\n" \
M_"v[168:171], a[220:223], a[244:247], v[168:171]\n" \
M_"v[164:167], a[156:159], a[176:179], v[164:167]\n" \
M_"v[164:167], a[220:223], a[240:243], v[164:167]\n" \
R_"a[156:159], v28 offset:38912\n" \
R_"a[220:223], v29 offset:38912\n" \
"s_waitcnt lgkmcnt(15)\n" \
M_"a[0:3], a[128:131], a[160:163], a[0:3]\n" \
"s_addk_i32 s84, 0x2000\n" \
M_"a[0:3], a[192:195], a[224:227], a[0:3]\n" \
B_"v24, s[76:79], s84 offen offset:2048 lds\n" \
M_"a[4:7], a[128:131], a[164:167], a[4:7]\n" \
X_"a[176:177], v36 offset:32768\n" \
M_"a[4:7], a[192:195], a[228:231], a[4:7]\n" \
"s_addk_i32 s84, 0x2000\n" \
"s_waitcnt lgkmcnt(13)\n" \
M_"a[8:11], a[128:131], a[168:171], a[8:11]\n" \
B_"v25, s[76:79], s84 offen offset:3072 lds\n" \
"s_waitcnt lgkmcnt(9)\n" \
M_"a[8:11], a[192:195], a[232:235], a[8:11]\n" \
X_"a[178:179], v36 offset:33792\n" \
"s_waitcnt lgkmcnt(8)\n" \
M_"a[12:15], a[128:131], a[172:175], a[12:15]\n" \
X_"a[240:241], v36 offset:40960\n" \
"s_waitcnt lgkmcnt(7)\n" \
M_"a[12:15], a[192:195], a[236:239], a[12:15]\n" \
M_"a[28:31], a[132:135], a[172:175], a[28:31]\n" \
M_"a[28:31], a[196:199], a[236:239], a[28:31]\n" \
"s_add_u32 m0, s30, 0x20000\n" \
"s_movk_i32 s84, 0x100\n" \
M_"a[24:27], a[132:135], a[168:171], a[24:27]\n" \
B_"v24, s[76:79], s84 offen offset:0 lds\n" \
M_"a[24:27], a[196:199], a[232:235], a[24:27]\n" \
X_"a[242:243], v36 offset:41984\n" \
M_"a[20:23], a[132:135], a[164:167], a[20:23]\n" \
"v_accvgpr_read_b32 v184, a12\n" \
"v_accvgpr_read_b32 v185, a13\n" \
M_"a[20:23], a[196:199], a[228:231], a[20:23]\n" \
X_"a[180:181], v37 offset:32768\n" \
"v_accvgpr_read_b32 v186, a14\n" \
M_"a[16:19], a[132:135], a[160:163], a[16:19]\n" \
"s_movk_i32 s84, 0x2100\n" \
"s_nop 0\n" \
M_"a[16:19], a[196:199], a[224:227], a[16:19]\n" \
B_"v25, s[76:79], s84 offen offset:1024 lds\n" \
M_"a[32:35], a[136:139], a[160:163], a[32:35]\n" \
X_"a[182:183], v37 offset:33792\n" \
"v_accvgpr_read_b32 v187, a15\n" \
M_"a[32:35], a[200:203], a[224:227], a[32:35]\n" \
"v_accvgpr_read_b32 v180, a28\n" \
"v_accvgpr_read_b32 v181, a29\n" \
M_"a[36:39], a[136:139], a[164:167], a[36:39]\n" \
"v_accvgpr_read_b32 v182, a30\n" \
"v_accvgpr_read_b32 v183, a31\n" \
M_"a[36:39], a[200:203], a[228:231], a[36:39]\n" \
"s_movk_i32 s84, 0x4100\n" \
"s_nop 0\n" \
M_"a[40:43], a[136:139], a[168:171], a[40:43]\n" \
B_"v24, s[76:79], s84 offen offset:2048 lds\n" \
M_"a[40:43], a[200:203], a[232:235], a[40:43]\n" \
X_"a[244:245], v37 offset:40960\n" \
C_"v184, v184, v185\n" \
M_"a[44:47], a[136:139], a[172:175], a[44:47]\n" \
X_"a[246:247], v37 offset:41984\n" \
C_"v185, v186, v187\n" \
M_"a[44:47], a[200:203], a[236:239], a[44:47]\n" \
C_"v186, v180, v181\n" \
C_"v187, v182, v183\n" \
M_"a[60:63], a[140:143], a[172:175], a[60:63]\n" \
"s_movk_i32 s84, 0x6100\n" \
M_"a[60:63], a[204:207], a[236:239], a[60:63]\n" \
B_"v25, s[76:79], s84 offen offset:3072 lds\n" \
M_"a[56:59], a[140:143], a[168:171], a[56:59]\n" \
X_"a[184:185], v38 offset:32768\n" \
"v_accvgpr_read_b32 v188, a8\n" \
M_"a[56:59], a[204:207], a[232:235], a[56:59]\n" \
"v_accvgpr_read_b32 v189, a9\n" \
"v_accvgpr_read_b32 v190, a10\n" \
M_"a[52:55], a[140:143], a[164:167], a[52:55]\n" \
"s_add_u32 m0, s30, 0x21000\n" \
"s_addk_i32 s84, 0x3000\n" \
M_"a[52:55], a[204:207], a[228:231], a[52:55]\n" \
B_"v24, s[76:79], s84 offen offset:0 lds\n" \
M_"a[48:51], a[140:143], a[160:163], a[48:51]\n" \
X_"a[186:187], v38 offset:33792\n" \
"v_accvgpr_read_b32 v191, a11\n" \
M_"a[48:51], a[204:207], a[224:227], a[48:51]\n" \
"v_accvgpr_read_b32 v180, a24\n" \
"v_accvgpr_read_b32 v181, a25\n" \
M_"a[64:67], a[144:147], a[160:163], a[64:67]\n" \
X_"a[248:249], v38 offset:40960\n" \
"v_accvgpr_read_b32 v182, a26\n" \
M_"a[64:67], a[208:211], a[224:227], a[64:67]\n" \
"s_addk_i32 s84, 0x2000\n" \
"s_nop 0\n" \
M_"a[68:71], a[144:147], a[164:167], a[68:71]\n" \
B_"v25, s[76:79], s84 offen offset:1024 lds\n" \
M_"a[68:71], a[208:211], a[228:231], a[68:71]\n" \
X_"a[250:251], v38 offset:41984\n" \
"v_accvgpr_read_b32 v183, a27\n" \
M_"a[72:75], a[144:147], a[168:171], a[72:75]\n" \
C_"v188, v188, v189\n" \
C_"v189, v190, v191\n" \
M_"a[72:75], a[208:211], a[232:235], a[72:75]\n" \
C_"v190, v180, v181\n" \
C_"v191, v182, v183\n" \
M_"a[76:79], a[144:147], a[172:175], a[76:79]\n" \
"s_addk_i32 s84, 0x2000\n" \
M_"a[76:79], a[208:211], a[236:239], a[76:79]\n" \
B_"v24, s[76:79], s84 offen offset:2048 lds\n" \
M_"a[92:95], a[148:151], a[172:175], a[92:95]\n" \
"s_add_u32 s89, s41, 0x0\n" \
"buffer_store_dwordx4 v[184:187], v20, s[80:83], s89 offen offset:3072 sc1\n" \
M_"a[92:95], a[212:215], a[236:239], a[92:95]\n" \
X_"a[188:189], v39 offset:32768\n" \
"v_accvgpr_read_b32 v192, a4\n" \
M_"a[88:91], a[148:151], a[168:171], a[88:91]\n" \
X_"a[190:191], v39 offset:33792\n" \
"v_accvgpr_read_b32 v193, a5\n" \
M_"a[88:91], a[212:215], a[232:235], a[88:91]\n" \
"s_addk_i32 s84, 0x2000\n" \
M_"a[84:87], a[148:151], a[164:167], a[84:87]\n" \
B_"v25, s[76:79], s84 offen offset:3072 lds\n" \
M_"a[84:87], a[212:215], a[228:231], a[84:87]\n" \
X_"a[252:253], v39 offset:40960\n" \
"v_accvgpr_read_b32 v194, a6\n" \
M_"a[80:83], a[148:151], a[160:163], a[80:83]\n" \
"s_add_u32 s88, s41, 0x0\n" \
"buffer_store_dwordx4 v[188:191], v20, s[80:83], s88 offen offset:2048 sc1\n" \
M_"a[80:83], a[212:215], a[224:227], a[80:83]\n" \
"v_accvgpr_read_b32 v195, a7\n" \
"v_accvgpr_read_b32 v180, a20\n" \
"s_waitcnt lgkmcnt(15)\n" \
"s_nop 0\n" \
M_"a[96:99], a[152:155], a[160:163], a[96:99]\n" \
X_"a[254:255], v39 offset:41984\n" \
"v_accvgpr_read_b32 v181, a21\n" \
M_"a[96:99], a[216:219], a[224:227], a[96:99]\n" \
"v_accvgpr_read_b32 v182, a22\n" \
"v_accvgpr_read_b32 v183, a23\n" \
M_"a[100:103], a[152:155], a[164:167], a[100:103]\n" \
C_"v192, v192, v193\n" \
C_"v193, v194, v195\n" \
M_"a[100:103], a[216:219], a[228:231], a[100:103]\n" \
C_"v194, v180, v181\n" \
C_"v195, v182, v183\n" \
M_"a[104:107], a[152:155], a[168:171], a[104:107]\n" \
"s_add_u32 s89, s41, 0x0\n" \
"s_nop 0\n" \
"buffer_store_dwordx4 v[192:195], v20, s[80:83], s89 offen offset:1024 sc1\n" \
M_"a[104:107], a[216:219], a[232:235], a[104:107]\n" \
"v_accvgpr_read_b32 v196, a0\n" \
"v_accvgpr_read_b32 v197, a1\n" \
M_"a[108:111], a[152:155], a[172:175], a[108:111]\n" \
"v_accvgpr_read_b32 v198, a2\n" \
"v_accvgpr_read_b32 v199, a3\n" \
M_"a[108:111], a[216:219], a[236:239], a[108:111]\n" \
"v_accvgpr_read_b32 v180, a16\n" \
"v_accvgpr_read_b32 v181, a17\n" \
M_"a[124:127], a[156:159], a[172:175], a[124:127]\n" \
"v_accvgpr_read_b32 v182, a18\n" \
"v_accvgpr_read_b32 v183, a19\n" \
M_"a[124:127], a[220:223], a[236:239], a[124:127]\n" \
C_"v196, v196, v197\n" \
C_"v197, v198, v199\n" \
M_"a[120:123], a[156:159], a[168:171], a[120:123]\n" \
C_"v198, v180, v181\n" \
C_"v199, v182, v183\n" \
"s_add_u32 s88, s41, 0x0\n" \
"s_nop 0\n" \
"buffer_store_dwordx4 v[196:199], v20, s[80:83], s88 offen offset:0 sc1\n" \
M_"a[120:123], a[220:223], a[232:235], a[120:123]\n" \
"v_accvgpr_read_b32 v200, a44\n" \
"v_accvgpr_read_b32 v201, a45\n" \
M_"a[116:119], a[156:159], a[164:167], a[116:119]\n" \
"v_accvgpr_read_b32 v202, a46\n" \
"v_accvgpr_read_b32 v203, a47\n" \
M_"a[116:119], a[220:223], a[228:231], a[116:119]\n" \
"v_accvgpr_read_b32 v180, a60\n" \
"v_accvgpr_read_b32 v181, a61\n" \
M_"a[112:115], a[156:159], a[160:163], a[112:115]\n" \
"v_accvgpr_read_b32 v182, a62\n" \
"v_accvgpr_read_b32 v183, a63\n" \
M_"a[112:115], a[220:223], a[224:227], a[112:115]\n" \
C_"v200, v200, v201\n" \
C_"v201, v202, v203\n" \
"s_waitcnt vmcnt(12) lgkmcnt(0)\n" \
"s_barrier\n" \
"v_xor_b32_e32 v36, 0x4000, v36\n" \
"v_xor_b32_e64 v32, 0x0, v48\n" \
"v_xor_b32_e32 v37, 0x4000, v37\n" \
"v_xor_b32_e64 v33, 0x20, v48\n" \
"v_xor_b32_e32 v38, 0x4000, v38\n" \
"v_xor_b32_e64 v34, 0x40, v48\n" \
"v_xor_b32_e32 v39, 0x4000, v39\n" \
"v_xor_b32_e32 v35, 0x60, v48\n" \
"v_add_u32_e32 v28, s43, v28\n" \
"v_add_u32_e32 v29, s43, v29\n" \
M_"v[116:119], a[144:147], a[176:179], v[116:119]\n" \
"s_add_u32 m0, s40, 0x8000\n" \
"s_add_u32 s85, s39, 0x2000\n" \
M_"v[116:119], a[208:211], a[240:243], v[116:119]\n" \
B_"v20, s[80:83], s85 offen sc1 lds\n" \
M_"v[120:123], a[144:147], a[180:183], v[120:123]\n" \
X_"a[160:161], v32 offset:0\n" \
C_"v202, v180, v181\n" \
M_"v[120:123], a[208:211], a[244:247], v[120:123]\n" \
"s_add_u32 m0, s40, 0x9000\n" \
"s_add_u32 s85, s39, 0x3000\n" \
M_"v[124:127], a[144:147], a[184:187], v[124:127]\n" \
B_"v20, s[80:83], s85 offen sc1 lds\n" \
M_"v[124:127], a[208:211], a[248:251], v[124:127]\n" \
X_"a[162:163], v32 offset:512\n" \
C_"v203, v182, v183\n" \
M_"v[128:131], a[144:147], a[188:191], v[128:131]\n" \
X_"a[224:225], v32 offset:4096\n" \
"v_accvgpr_read_b32 v204, a40\n" \
M_"v[128:131], a[208:211], a[252:255], v[128:131]\n" \
R_"a[144:147], v28 offset:8192\n" \
"v_accvgpr_read_b32 v205, a41\n" \
M_"v[144:147], a[148:151], a[188:191], v[144:147]\n" \
R_"a[208:211], v29 offset:8192\n" \
"v_accvgpr_read_b32 v206, a42\n" \
M_"v[144:147], a[212:215], a[252:255], v[144:147]\n" \
"s_add_u32 m0, s40, 0xb000\n" \
"s_movk_i32 s84, 0x80\n" \
M_"v[140:143], a[148:151], a[184:187], v[140:143]\n" \
B_"v21, s[76:79], s84 offen offset:0 lds\n" \
M_"v[140:143], a[212:215], a[248:251], v[140:143]\n" \
"s_add_u32 m0, s30, 0x8000\n" \
"s_movk_i32 s84, 0x80\n" \
M_"v[136:139], a[148:151], a[180:183], v[136:139]\n" \
B_"v24, s[76:79], s84 offen offset:0 lds\n" \
M_"v[136:139], a[212:215], a[244:247], v[136:139]\n" \
X_"a[226:227], v32 offset:4608\n" \
"v_accvgpr_read_b32 v207, a43\n" \
M_"v[132:135], a[148:151], a[176:179], v[132:135]\n" \
X_"a[164:165], v33 offset:0\n" \
"v_accvgpr_read_b32 v180, a56\n" \
M_"v[132:135], a[212:215], a[240:243], v[132:135]\n" \
R_"a[148:151], v28 offset:10240\n" \
"v_accvgpr_read_b32 v181, a57\n" \
M_"v[148:151], a[152:155], a[176:179], v[148:151]\n" \
R_"a[212:215], v29 offset:10240\n" \
"v_accvgpr_read_b32 v182, a58\n" \
M_"v[148:151], a[216:219], a[240:243], v[148:151]\n" \
"s_movk_i32 s84, 0x2080\n" \
M_"v[152:155], a[152:155], a[180:183], v[152:155]\n" \
B_"v25, s[76:79], s84 offen offset:1024 lds\n" \
M_"v[152:155], a[216:219], a[244:247], v[152:155]\n" \
"s_movk_i32 s84, 0x4080\n" \
M_"v[156:159], a[152:155], a[184:187], v[156:159]\n" \
B_"v24, s[76:79], s84 offen offset:2048 lds\n" \
M_"v[156:159], a[216:219], a[248:251], v[156:159]\n" \
X_"a[166:167], v33 offset:512\n" \
"v_accvgpr_read_b32 v183, a59\n" \
M_"v[160:163], a[152:155], a[188:191], v[160:163]\n" \
X_"a[228:229], v33 offset:4096\n" \
C_"v204, v204, v205\n" \
M_"v[160:163], a[216:219], a[252:255], v[160:163]\n" \
R_"a[152:155], v28 offset:12288\n" \
C_"v205, v206, v207\n" \
M_"v[176:179], a[156:159], a[188:191], v[176:179]\n" \
R_"a[216:219], v29 offset:12288\n" \
C_"v206, v180, v181\n" \
M_"v[176:179], a[220:223], a[252:255], v[176:179]\n" \
"s_movk_i32 s84, 0x6080\n" \
M_"v[172:175], a[156:159], a[184:187], v[172:175]\n" \
B_"v25, s[76:79], s84 offen offset:3072 lds\n" \
M_"v[172:175], a[220:223], a[248:251], v[172:175]\n" \
"s_add_u32 m0, s31, 0x8000\n" \
"s_mov_b32 s84, 0x80000\n" \
M_"v[168:171], a[156:159], a[180:183], v[168:171]\n" \
B_"v26, s[68:71], s84 offen offset:0 lds\n" \
M_"v[168:171], a[220:223], a[244:247], v[168:171]\n" \
"s_addk_i32 s84, 0x7c00\n" \
M_"v[164:167], a[156:159], a[176:179], v[164:167]\n" \
B_"v26, s[68:71], s84 offen offset:1024 lds\n" \
M_"v[164:167], a[220:223], a[240:243], v[164:167]\n" \
R_"a[156:159], v28 offset:14336\n" \
C_"v207, v182, v183\n" \
M_"v[84:87], a[136:139], a[176:179], v[84:87]\n" \
R_"a[220:223], v29 offset:14336\n" \
"v_accvgpr_read_b32 v208, a36\n" \
M_"v[84:87], a[200:203], a[240:243], v[84:87]\n" \
"s_addk_i32 s84, 0x7c00\n" \
M_"v[88:91], a[136:139], a[180:183], v[88:91]\n" \
B_"v27, s[68:71], s84 offen offset:2048 lds\n" \
M_"v[88:91], a[200:203], a[244:247], v[88:91]\n" \
X_"a[230:231], v33 offset:4608\n" \
"v_accvgpr_read_b32 v209, a37\n" \
M_"v[92:95], a[136:139], a[184:187], v[92:95]\n" \
"s_addk_i32 s84, 0x7c00\n" \
M_"v[92:95], a[200:203], a[248:251], v[92:95]\n" \
B_"v27, s[68:71], s84 offen offset:3072 lds\n" \
M_"v[96:99], a[136:139], a[188:191], v[96:99]\n" \
X_"a[172:173], v35 offset:0\n" \
"v_accvgpr_read_b32 v210, a38\n" \
M_"v[96:99], a[200:203], a[252:255], v[96:99]\n" \
R_"a[136:139], v28 offset:4096\n" \
"v_accvgpr_read_b32 v211, a39\n" \
M_"v[112:115], a[140:143], a[188:191], v[112:115]\n" \
R_"a[200:203], v29 offset:4096\n" \
"v_accvgpr_read_b32 v180, a52\n" \
M_"v[112:115], a[204:207], a[252:255], v[112:115]\n" \
"s_add_u32 m0, s30, 0x9000\n" \
"s_mov_b32 s84, 0x9080\n" \
M_"v[108:111], a[140:143], a[184:187], v[108:111]\n" \
B_"v24, s[76:79], s84 offen offset:0 lds\n" \
M_"v[108:111], a[204:207], a[248:251], v[108:111]\n" \
X_"a[174:175], v35 offset:512\n" \
"v_accvgpr_read_b32 v181, a53\n" \
M_"v[104:107], a[140:143], a[180:183], v[104:107]\n" \
"s_addk_i32 s84, 0x2000\n" \
"s_nop 0\n" \
M_"v[104:107], a[204:207], a[244:247], v[104:107]\n" \
B_"v25, s[76:79], s84 offen offset:1024 lds\n" \
M_"v[100:103], a[140:143], a[176:179], v[100:103]\n" \
X_"a[236:237], v35 offset:4096\n" \
"v_accvgpr_read_b32 v182, a54\n" \
M_"v[100:103], a[204:207], a[240:243], v[100:103]\n" \
R_"a[140:143], v28 offset:6144\n" \
"v_accvgpr_read_b32 v183, a55\n" \
M_"v[52:55], a[128:131], a[176:179], v[52:55]\n" \
R_"a[204:207], v29 offset:6144\n" \
C_"v208, v208, v209\n" \
M_"v[52:55], a[192:195], a[240:243], v[52:55]\n" \
X_"a[238:239], v35 offset:4608\n" \
C_"v209, v210, v211\n" \
M_"v[56:59], a[128:131], a[180:183], v[56:59]\n" \
X_"a[168:169], v34 offset:0\n" \
C_"v210, v180, v181\n" \
M_"v[56:59], a[192:195], a[244:247], v[56:59]\n" \
X_"a[170:171], v34 offset:512\n" \
C_"v211, v182, v183\n" \
M_"v[60:63], a[128:131], a[184:187], v[60:63]\n" \
X_"a[232:233], v34 offset:4096\n" \
"v_accvgpr_read_b32 v212, a32\n" \
M_"v[60:63], a[192:195], a[248:251], v[60:63]\n" \
X_"a[234:235], v34 offset:4608\n" \
"v_accvgpr_read_b32 v213, a33\n" \
M_"v[64:67], a[128:131], a[188:191], v[64:67]\n" \
"v_accvgpr_read_b32 v214, a34\n" \
"v_accvgpr_read_b32 v215, a35\n" \
M_"v[64:67], a[192:195], a[252:255], v[64:67]\n" \
R_"a[128:131], v28 offset:0\n" \
"v_accvgpr_read_b32 v180, a48\n" \
M_"v[80:83], a[132:135], a[188:191], v[80:83]\n" \
R_"a[192:195], v29 offset:0\n" \
"v_accvgpr_read_b32 v181, a49\n" \
M_"v[80:83], a[196:199], a[252:255], v[80:83]\n" \
"v_accvgpr_read_b32 v182, a50\n" \
"v_accvgpr_read_b32 v183, a51\n" \
M_"v[76:79], a[132:135], a[184:187], v[76:79]\n" \
C_"v212, v212, v213\n" \
C_"v213, v214, v215\n" \
M_"v[76:79], a[196:199], a[248:251], v[76:79]\n" \
C_"v214, v180, v181\n" \
C_"v215, v182, v183\n" \
M_"v[72:75], a[132:135], a[180:183], v[72:75]\n" \
"v_accvgpr_read_b32 v216, a68\n" \
"v_accvgpr_read_b32 v217, a69\n" \
M_"v[72:75], a[196:199], a[244:247], v[72:75]\n" \
"v_accvgpr_read_b32 v218, a70\n" \
"v_accvgpr_read_b32 v219, a71\n" \
M_"v[68:71], a[132:135], a[176:179], v[68:71]\n" \
"v_accvgpr_read_b32 v180, a84\n" \
"v_accvgpr_read_b32 v181, a85\n" \
M_"v[68:71], a[196:199], a[240:243], v[68:71]\n" \
R_"a[132:135], v28 offset:2048\n" \
R_"a[196:199], v29 offset:2048\n" \
"s_waitcnt lgkmcnt(15)\n" \
M_"a[0:3], a[160:163], a[144:147], 0\n" \
"s_addk_i32 s84, 0x2000\n" \
"v_accvgpr_read_b32 v182, a86\n" \
M_"a[0:3], a[224:227], a[208:211], a[0:3]\n" \
B_"v24, s[76:79], s84 offen offset:2048 lds\n" \
"v_accvgpr_read_b32 v183, a87\n" \
M_"a[4:7], a[164:167], a[144:147], 0\n" \
X_"a[176:177], v36 offset:0\n" \
C_"v216, v216, v217\n" \
M_"a[4:7], a[228:231], a[208:211], a[4:7]\n" \
"s_addk_i32 s84, 0x2000\n" \
C_"v217, v218, v219\n" \
"s_waitcnt lgkmcnt(13)\n" \
M_"a[12:15], a[172:175], a[144:147], 0\n" \
B_"v25, s[76:79], s84 offen offset:3072 lds\n" \
C_"v218, v180, v181\n" \
"s_waitcnt lgkmcnt(9)\n" \
M_"a[12:15], a[236:239], a[208:211], a[12:15]\n" \
X_"a[178:179], v36 offset:1024\n" \
C_"v219, v182, v183\n" \
"s_waitcnt lgkmcnt(8)\n" \
M_"a[8:11], a[144:147], a[168:171], 0\n" \
X_"a[240:241], v36 offset:8192\n" \
"v_accvgpr_read_b32 v220, a64\n" \
"s_waitcnt lgkmcnt(7)\n" \
"s_nop 0\n" \
M_"a[8:11], a[208:211], a[232:235], a[8:11]\n" \
X_"a[242:243], v36 offset:9216\n" \
"v_accvgpr_read_b32 v221, a65\n" \
M_"a[24:27], a[148:151], a[168:171], 0\n" \
X_"a[180:181], v37 offset:0\n" \
"v_accvgpr_read_b32 v222, a66\n" \
M_"a[24:27], a[212:215], a[232:235], a[24:27]\n" \
X_"a[182:183], v37 offset:1024\n" \
"v_accvgpr_read_b32 v223, a67\n" \
M_"a[28:31], a[172:175], a[148:151], 0\n" \
X_"a[244:245], v37 offset:8192\n" \
"v_accvgpr_read_b32 v180, a80\n" \
M_"a[28:31], a[236:239], a[212:215], a[28:31]\n" \
"s_add_u32 s89, s41, 0x2000\n" \
"buffer_store_dwordx4 v[200:203], v20, s[80:83], s89 offen offset:3072 sc1\n" \
M_"a[20:23], a[164:167], a[148:151], 0\n" \
X_"a[246:247], v37 offset:9216\n" \
"v_accvgpr_read_b32 v181, a81\n" \
M_"a[20:23], a[228:231], a[212:215], a[20:23]\n" \
X_"a[184:185], v38 offset:0\n" \
"v_accvgpr_read_b32 v182, a82\n" \
M_"a[16:19], a[160:163], a[148:151], 0\n" \
X_"a[186:187], v38 offset:1024\n" \
"v_accvgpr_read_b32 v183, a83\n" \
M_"a[16:19], a[224:227], a[212:215], a[16:19]\n" \
C_"v220, v220, v221\n" \
C_"v221, v222, v223\n" \
C_"v222, v180, v181\n" \
M_"a[32:35], a[160:163], a[152:155], 0\n" \
"s_add_u32 s88, s41, 0x2000\n" \
".fill (L_pb + 61152 - .) / 4, 4, 0xBF800000\n" \
"L_pp15:\n" \
"s_cbranch_execz L_pret\n" \
"s_nop 0\n" \
"buffer_store_dwordx4 v[204:207], v20, s[80:83], s88 offen offset:2048 sc1\n" \
M_"a[32:35], a[224:227], a[216:219], a[32:35]\n" \
X_"a[248:249], v38 offset:8192\n" \
C_"v223, v182, v183\n" \
M_"a[36:39], a[164:167], a[152:155], 0\n" \
X_"a[250:251], v38 offset:9216\n" \
C_"v224, v128, v129\n" \
M_"a[36:39], a[228:231], a[216:219], a[36:39]\n" \
X_"a[188:189], v39 offset:0\n" \
C_"v225, v130, v131\n" \
M_"a[44:47], a[172:175], a[152:155], 0\n" \
C_"v226, v144, v145\n" \
C_"v227, v146, v147\n" \
C_"v228, v124, v125\n" \
M_"a[44:47], a[236:239], a[216:219], a[44:47]\n" \
X_"a[190:191], v39 offset:1024\n" \
C_"v229, v126, v127\n" \
M_"a[40:43], a[152:155], a[168:171], 0\n" \
"s_add_u32 s89, s41, 0x2000\n" \
"buffer_store_dwordx4 v[208:211], v20, s[80:83], s89 offen offset:1024 sc1\n" \
M_"a[40:43], a[216:219], a[232:235], a[40:43]\n" \
X_"a[252:253], v39 offset:8192\n" \
C_"v230, v140, v141\n" \
M_"a[56:59], a[156:159], a[168:171], 0\n" \
X_"a[254:255], v39 offset:9216\n" \
C_"v231, v142, v143\n" \
M_"a[56:59], a[220:223], a[232:235], a[56:59]\n" \
C_"v232, v120, v121\n" \
C_"v233, v122, v123\n" \
C_"v234, v136, v137\n" \
M_"a[60:63], a[172:175], a[156:159], 0\n" \
C_"v235, v138, v139\n" \
C_"v236, v116, v117\n" \
C_"v237, v118, v119\n" \
M_"a[60:63], a[236:239], a[220:223], a[60:63]\n" \
"s_add_u32 s88, s41, 0x2000\n" \
"buffer_store_dwordx4 v[212:215], v20, s[80:83], s88 offen offset:0 sc1\n" \
M_"a[52:55], a[164:167], a[156:159], 0\n" \
C_"v238, v132, v133\n" \
C_"v239, v134, v135\n" \
C_"v240, v160, v161\n" \
M_"a[52:55], a[228:231], a[220:223], a[52:55]\n" \
C_"v241, v162, v163\n" \
C_"v242, v176, v177\n" \
C_"v243, v178, v179\n" \
M_"a[48:51], a[160:163], a[156:159], 0\n" \
C_"v244, v156, v157\n" \
C_"v245, v158, v159\n" \
C_"v246, v172, v173\n" \
M_"a[48:51], a[224:227], a[220:223], a[48:51]\n" \
C_"v247, v174, v175\n" \
C_"v184, v152, v153\n" \
C_"v185, v154, v155\n" \
M_"a[64:67], a[164:167], a[140:143], 0\n" \
C_"v186, v168, v169\n" \
C_"v187, v170, v171\n" \
C_"v188, v148, v149\n" \
M_"a[64:67], a[228:231], a[204:207], a[64:67]\n" \
C_"v189, v150, v151\n" \
C_"v190, v164, v165\n" \
C_"v191, v166, v167\n" \
M_"a[80:83], a[172:175], a[140:143], 0\n" \
C_"v192, v96, v97\n" \
C_"v193, v98, v99\n" \
C_"v194, v112, v113\n" \
M_"a[80:83], a[236:239], a[204:207], a[80:83]\n" \
C_"v195, v114, v115\n" \
C_"v196, v92, v93\n" \
C_"v197, v94, v95\n" \
M_"a[68:71], a[140:143], a[168:171], 0\n" \
C_"v198, v108, v109\n" \
C_"v199, v110, v111\n" \
C_"v252, v88, v89\n" \
M_"a[68:71], a[204:207], a[232:235], a[68:71]\n" \
C_"v253, v90, v91\n" \
C_"v254, v104, v105\n" \
C_"v255, v106, v107\n" \
"s_waitcnt vmcnt(4) lgkmcnt(0)\n" \
"s_barrier\n" \
M_"v[116:119], a[176:179], a[144:147], 0\n" \
"s_add_u32 m0, s40, 0x0\n" \
"s_nop 0\n" \
"s_add_u32 s85, s39, 0x4000\n" \
"v_accvgpr_write_b32 a84, v252\n" \
M_"v[116:119], a[240:243], a[208:211], v[116:119]\n" \
B_"v20, s[80:83], s85 offen sc1 lds\n" \
"v_accvgpr_write_b32 a85, v253\n" \
M_"v[120:123], a[180:183], a[144:147], 0\n" \
X_"a[160:161], v32 offset:32768\n" \
"v_accvgpr_write_b32 a86, v254\n" \
M_"v[120:123], a[244:247], a[208:211], v[120:123]\n" \
X_"a[162:163], v32 offset:33280\n" \
"v_accvgpr_write_b32 a87, v255\n" \
M_"v[124:127], a[184:187], a[144:147], 0\n" \
"s_add_u32 m0, s40, 0x1000\n" \
"s_add_u32 s85, s39, 0x5000\n" \
C_"v252, v84, v85\n" \
M_"v[124:127], a[248:251], a[208:211], v[124:127]\n" \
B_"v20, s[80:83], s85 offen sc1 lds\n" \
C_"v253, v86, v87\n" \
M_"v[128:131], a[188:191], a[144:147], 0\n" \
X_"a[224:225], v32 offset:36864\n" \
C_"v254, v100, v101\n" \
M_"v[128:131], a[252:255], a[208:211], v[128:131]\n" \
R_"a[144:147], v28 offset:40960\n" \
C_"v255, v102, v103\n" \
M_"v[144:147], a[188:191], a[148:151], 0\n" \
R_"a[208:211], v29 offset:40960\n" \
C_"v200, v64, v65\n" \
M_"v[144:147], a[252:255], a[212:215], v[144:147]\n" \
"s_add_u32 m0, s40, 0x3000\n" \
"s_movk_i32 s84, 0x100\n" \
"s_nop 0\n" \
C_"v201, v66, v67\n" \
M_"v[140:143], a[184:187], a[148:151], 0\n" \
B_"v21, s[76:79], s84 offen offset:0 lds\n" \
C_"v202, v80, v81\n" \
M_"v[140:143], a[248:251], a[212:215], v[140:143]\n" \
X_"a[226:227], v32 offset:37376\n" \
C_"v203, v82, v83\n" \
M_"v[136:139], a[180:183], a[148:151], 0\n" \
C_"v204, v60, v61\n" \
C_"v205, v62, v63\n" \
C_"v206, v76, v77\n" \
M_"v[136:139], a[244:247], a[212:215], v[136:139]\n" \
X_"a[164:165], v33 offset:32768\n" \
C_"v207, v78, v79\n" \
M_"v[132:135], a[176:179], a[148:151], 0\n" \
C_"v208, v56, v57\n" \
C_"v209, v58, v59\n" \
C_"v210, v72, v73\n" \
M_"v[132:135], a[240:243], a[212:215], v[132:135]\n" \
R_"a[148:151], v28 offset:43008\n" \
C_"v211, v74, v75\n" \
M_"v[148:151], a[176:179], a[152:155], 0\n" \
R_"a[212:215], v29 offset:43008\n" \
C_"v212, v52, v53\n" \
M_"v[148:151], a[240:243], a[216:219], v[148:151]\n" \
"s_add_u32 m0, s31, 0x0\n" \
"s_mov_b32 s84, 0x100000\n" \
C_"v213, v54, v55\n" \
M_"v[152:155], a[180:183], a[152:155], 0\n" \
B_"v26, s[68:71], s84 offen offset:0 lds\n" \
C_"v214, v68, v69\n" \
M_"v[152:155], a[244:247], a[216:219], v[152:155]\n" \
"s_addk_i32 s84, 0x7c00\n" \
C_"v215, v70, v71\n" \
M_"v[156:159], a[184:187], a[152:155], 0\n" \
B_"v26, s[68:71], s84 offen offset:1024 lds\n" \
M_"v[156:159], a[248:251], a[216:219], v[156:159]\n" \
X_"a[166:167], v33 offset:33280\n" \
M_"v[160:163], a[188:191], a[152:155], 0\n" \
X_"a[228:229], v33 offset:36864\n" \
M_"v[160:163], a[252:255], a[216:219], v[160:163]\n" \
R_"a[152:155], v28 offset:45056\n" \
M_"v[176:179], a[188:191], a[156:159], 0\n" \
R_"a[216:219], v29 offset:45056\n" \
M_"v[176:179], a[252:255], a[220:223], v[176:179]\n" \
"s_addk_i32 s84, 0x7c00\n" \
M_"v[172:175], a[184:187], a[156:159], 0\n" \
B_"v27, s[68:71], s84 offen offset:2048 lds\n" \
M_"v[172:175], a[248:251], a[220:223], v[172:175]\n" \
X_"a[230:231], v33 offset:37376\n" \
M_"v[168:171], a[180:183], a[156:159], 0\n" \
"s_addk_i32 s84, 0x7c00\n" \
M_"v[168:171], a[244:247], a[220:223], v[168:171]\n" \
B_"v27, s[68:71], s84 offen offset:3072 lds\n" \
M_"v[164:167], a[176:179], a[156:159], 0\n" \
X_"a[172:173], v35 offset:32768\n" \
M_"v[164:167], a[240:243], a[220:223], v[164:167]\n" \
R_"a[156:159], v28 offset:47104\n" \
M_"v[84:87], a[176:179], a[136:139], 0\n" \
R_"a[220:223], v29 offset:47104\n" \
M_"v[84:87], a[240:243], a[200:203], v[84:87]\n" \
"s_add_u32 s89, s41, 0x3000\n" \
"buffer_store_dwordx4 v[192:195], v20, s[80:83], s89 offen offset:3072 sc1\n" \
M_"v[88:91], a[180:183], a[136:139], 0\n" \
X_"a[174:175], v35 offset:33280\n" \
M_"v[88:91], a[244:247], a[200:203], v[88:91]\n" \
X_"a[236:237], v35 offset:36864\n" \
M_"v[92:95], a[184:187], a[136:139], 0\n" \
X_"a[238:239], v35 offset:37376\n" \
M_"v[92:95], a[248:251], a[200:203], v[92:95]\n" \
"s_add_u32 m0, s30, 0x0\n" \
"s_movk_i32 s84, 0x180\n" \
M_"v[96:99], a[188:191], a[136:139], 0\n" \
B_"v24, s[76:79], s84 offen offset:0 lds\n" \
M_"v[96:99], a[252:255], a[200:203], v[96:99]\n" \
R_"a[136:139], v28 offset:36864\n" \
M_"v[112:115], a[188:191], a[140:143], 0\n" \
R_"a[200:203], v29 offset:36864\n" \
M_"v[112:115], a[252:255], a[204:207], v[112:115]\n" \
X_"a[168:169], v34 offset:32768\n" \
M_"v[108:111], a[184:187], a[140:143], 0\n" \
"s_movk_i32 s84, 0x2180\n" \
"s_nop 0\n" \
M_"v[108:111], a[248:251], a[204:207], v[108:111]\n" \
B_"v25, s[76:79], s84 offen offset:1024 lds\n" \
M_"v[104:107], a[180:183], a[140:143], 0\n" \
X_"a[170:171], v34 offset:33280\n" \
M_"v[104:107], a[244:247], a[204:207], v[104:107]\n" \
X_"a[232:233], v34 offset:36864\n" \
M_"v[100:103], a[176:179], a[140:143], 0\n" \
M_"v[100:103], a[240:243], a[204:207], v[100:103]\n" \
R_"a[140:143], v28 offset:38912\n" \
M_"v[52:55], a[176:179], a[128:131], 0\n" \
R_"a[204:207], v29 offset:38912\n" \
M_"v[52:55], a[240:243], a[192:195], v[52:55]\n" \
"s_movk_i32 s84, 0x4180\n" \
M_"v[56:59], a[180:183], a[128:131], 0\n" \
B_"v24, s[76:79], s84 offen offset:2048 lds\n" \
M_"v[56:59], a[244:247], a[192:195], v[56:59]\n" \
"s_movk_i32 s84, 0x6180\n" \
M_"v[60:63], a[184:187], a[128:131], 0\n" \
B_"v25, s[76:79], s84 offen offset:3072 lds\n" \
M_"v[60:63], a[248:251], a[192:195], v[60:63]\n" \
X_"a[234:235], v34 offset:37376\n" \
M_"v[64:67], a[188:191], a[128:131], 0\n" \
M_"v[64:67], a[252:255], a[192:195], v[64:67]\n" \
R_"a[128:131], v28 offset:32768\n" \
M_"v[80:83], a[188:191], a[132:135], 0\n" \
R_"a[192:195], v29 offset:32768\n" \
M_"v[80:83], a[252:255], a[196:199], v[80:83]\n" \
M_"v[76:79], a[184:187], a[132:135], 0\n" \
M_"v[76:79], a[248:251], a[196:199], v[76:79]\n" \
M_"v[72:75], a[180:183], a[132:135], 0\n" \
M_"v[72:75], a[244:247], a[196:199], v[72:75]\n" \
M_"v[68:71], a[176:179], a[132:135], 0\n" \
M_"v[68:71], a[240:243], a[196:199], v[68:71]\n" \
R_"a[132:135], v28 offset:34816\n" \
R_"a[196:199], v29 offset:34816\n" \
"s_waitcnt lgkmcnt(15)\n" \
M_"a[0:3], a[160:163], a[144:147], a[0:3]\n" \
"s_add_u32 m0, s30, 0x1000\n" \
"s_addk_i32 s84, 0x3000\n" \
M_"a[0:3], a[224:227], a[208:211], a[0:3]\n" \
B_"v24, s[76:79], s84 offen offset:0 lds\n" \
M_"a[4:7], a[164:167], a[144:147], a[4:7]\n" \
X_"a[176:177], v36 offset:32768\n" \
"v_mov_b32_e64 v11, 0\n" \
M_"a[4:7], a[228:231], a[208:211], a[4:7]\n" \
X_"a[178:179], v36 offset:33792\n" \
"s_and_b32 s5, s2, 7\n" \
"s_waitcnt lgkmcnt(15)\n" \
M_"a[12:15], a[172:175], a[144:147], a[12:15]\n" \
X_"a[240:241], v36 offset:40960\n" \
"s_lshl_b32 s6, s3, 1\n" \
"s_waitcnt lgkmcnt(15)\n" \
M_"a[12:15], a[236:239], a[208:211], a[12:15]\n" \
"s_add_u32 s88, s41, 0x3000\n" \
"buffer_store_dwordx4 v[196:199], v20, s[80:83], s88 offen offset:2048 sc1\n" \
"s_waitcnt lgkmcnt(11)\n" \
M_"a[8:11], a[144:147], a[168:171], a[8:11]\n" \
X_"a[242:243], v36 offset:41984\n" \
"s_lshl_b32 s4, s5, 2\n" \
"s_waitcnt lgkmcnt(8)\n" \
M_"a[8:11], a[208:211], a[232:235], a[8:11]\n" \
X_"a[180:181], v37 offset:32768\n" \
"s_bfe_u32 s7, s2, 0x20003\n" \
M_"a[24:27], a[148:151], a[168:171], a[24:27]\n" \
X_"a[182:183], v37 offset:33792\n" \
"s_or_b32 s12, s4, s7\n" \
M_"a[24:27], a[212:215], a[232:235], a[24:27]\n" \
X_"a[244:245], v37 offset:40960\n" \
"s_and_b32 s6, s6, 4\n" \
M_"a[28:31], a[172:175], a[148:151], a[28:31]\n" \
"s_and_b32 s8, s3, 1\n" \
"s_or_b32 s8, s6, s8\n" \
M_"a[28:31], a[236:239], a[212:215], a[28:31]\n" \
"s_addk_i32 s84, 0x2000\n" \
M_"a[20:23], a[164:167], a[148:151], a[20:23]\n" \
B_"v25, s[76:79], s84 offen offset:1024 lds\n" \
M_"a[20:23], a[228:231], a[212:215], a[20:23]\n" \
X_"a[246:247], v37 offset:41984\n" \
"s_lshl_b32 s6, s12, 4\n" \
M_"a[16:19], a[160:163], a[148:151], a[16:19]\n" \
X_"a[184:185], v38 offset:32768\n" \
"s_lshl_b32 s9, s3, 2\n" \
M_"a[16:19], a[224:227], a[212:215], a[16:19]\n" \
X_"a[186:187], v38 offset:33792\n" \
"s_or_b32 s6, s6, s9\n" \
M_"a[32:35], a[160:163], a[152:155], a[32:35]\n" \
"s_addk_i32 s84, 0x2000\n" \
M_"a[32:35], a[224:227], a[216:219], a[32:35]\n" \
B_"v24, s[76:79], s84 offen offset:2048 lds\n" \
M_"a[36:39], a[164:167], a[152:155], a[36:39]\n" \
X_"a[248:249], v38 offset:40960\n" \
"s_lshr_b32 s9, s2, 5\n" \
M_"a[36:39], a[228:231], a[216:219], a[36:39]\n" \
X_"a[250:251], v38 offset:41984\n" \
"s_mul_i32 s10, s9, 0xa00\n" \
M_"a[44:47], a[172:175], a[152:155], a[44:47]\n" \
X_"a[188:189], v39 offset:32768\n" \
"s_add_i32 s10, s10, 0x5bc1000\n" \
M_"a[44:47], a[236:239], a[216:219], a[44:47]\n" \
X_"a[190:191], v39 offset:33792\n" \
"s_or_b32 s6, s6, s10\n" \
M_"a[40:43], a[152:155], a[168:171], a[40:43]\n" \
"s_add_u32 s89, s41, 0x3000\n" \
"buffer_store_dwordx4 a[84:87], v20, s[80:83], s89 offen offset:1024 sc1\n" \
M_"a[40:43], a[216:219], a[232:235], a[40:43]\n" \
X_"a[252:253], v39 offset:40960\n" \
"s_lshl_b32 s13, s9, 3\n" \
M_"a[56:59], a[156:159], a[168:171], a[56:59]\n" \
X_"a[254:255], v39 offset:41984\n" \
"v_writelane_b32 v11, s6, 0\n" \
M_"a[56:59], a[220:223], a[232:235], a[56:59]\n" \
"s_or_b32 s6, s13, s5\n" \
"v_writelane_b32 v11, s10, 1\n" \
M_"a[60:63], a[172:175], a[156:159], a[60:63]\n" \
"s_lshl_b32 s6, s6, 10\n" \
"s_lshl_b32 s10, s3, 8\n" \
M_"a[60:63], a[236:239], a[220:223], a[60:63]\n" \
"s_addk_i32 s84, 0x2000\n" \
M_"a[52:55], a[164:167], a[156:159], a[52:55]\n" \
B_"v25, s[76:79], s84 offen offset:3072 lds\n" \
M_"a[52:55], a[228:231], a[220:223], a[52:55]\n" \
"s_or_b32 s6, s6, s10\n" \
"s_or_b32 s10, s6, 0x5c00200\n" \
M_"a[48:51], a[160:163], a[156:159], a[48:51]\n" \
"v_writelane_b32 v11, s10, 2\n" \
"s_lshl_b32 s10, s7, 2\n" \
M_"a[48:51], a[224:227], a[220:223], a[48:51]\n" \
"s_or_b32 s11, s10, s6\n" \
"s_or_b32 s11, s11, 0x5c00000\n" \
M_"a[64:67], a[164:167], a[140:143], a[64:67]\n" \
"v_writelane_b32 v11, s11, 3\n" \
"s_or_b32 s6, s6, 0x5c00080\n" \
M_"a[64:67], a[228:231], a[204:207], a[64:67]\n" \
"v_writelane_b32 v11, s6, 4\n" \
"s_or_b32 s6, s6, s10\n" \
M_"a[80:83], a[172:175], a[140:143], a[80:83]\n" \
"s_add_i32 s10, s3, s2\n" \
"v_writelane_b32 v11, s6, 5\n" \
M_"a[80:83], a[236:239], a[204:207], a[80:83]\n" \
"s_mov_b32 s6, 2.0\n" \
"s_mov_b32 s11, 0x5bc0000\n" \
M_"a[68:71], a[140:143], a[168:171], a[68:71]\n" \
"s_cmp_lg_u32 s10, 0\n" \
"s_cselect_b32 s10, s6, s11\n" \
"s_waitcnt vmcnt(11) lgkmcnt(0)\n" \
M_"a[68:71], a[204:207], a[232:235], a[68:71]\n" \
"s_barrier\n" \
"v_xor_b32_e32 v28, 0x20000, v28\n" \
"v_xor_b32_e32 v29, 0x20000, v29\n" \
M_"v[116:119], a[176:179], a[144:147], v[116:119]\n" \
"s_add_u32 m0, s40, 0x8000\n" \
"s_add_u32 s85, s39, 0x6000\n" \
M_"v[116:119], a[240:243], a[208:211], v[116:119]\n" \
B_"v20, s[80:83], s85 offen sc1 lds\n" \
M_"v[120:123], a[180:183], a[144:147], v[120:123]\n" \
X_"a[160:161], v32 offset:0\n" \
"s_lshr_b32 s14, s2, 3\n" \
M_"v[120:123], a[244:247], a[208:211], v[120:123]\n" \
X_"a[162:163], v32 offset:512\n" \
"v_writelane_b32 v11, s10, 57\n" \
M_"v[124:127], a[184:187], a[144:147], v[124:127]\n" \
"s_add_u32 m0, s40, 0x9000\n" \
"s_add_u32 s85, s39, 0x7000\n" \
M_"v[124:127], a[248:251], a[208:211], v[124:127]\n" \
B_"v20, s[80:83], s85 offen sc1 lds\n" \
M_"v[128:131], a[188:191], a[144:147], v[128:131]\n" \
X_"a[224:225], v32 offset:4096\n" \
"s_lshl_b32 s10, s5, 5\n" \
M_"v[128:131], a[252:255], a[208:211], v[128:131]\n" \
R_"a[144:147], v28 offset:8192\n" \
"s_min_u32 s15, s3, 2\n" \
M_"v[144:147], a[188:191], a[148:151], v[144:147]\n" \
R_"a[208:211], v29 offset:8192\n" \
"s_mul_i32 s11, s7, 0x120\n" \
M_"v[144:147], a[252:255], a[212:215], v[144:147]\n" \
"s_add_u32 m0, s40, 0xb000\n" \
"s_movk_i32 s84, 0x180\n" \
M_"v[140:143], a[184:187], a[148:151], v[140:143]\n" \
B_"v21, s[76:79], s84 offen offset:0 lds\n" \
M_"v[140:143], a[248:251], a[212:215], v[140:143]\n" \
X_"a[226:227], v32 offset:4608\n" \
M_"v[136:139], a[180:183], a[148:151], v[136:139]\n" \
"s_add_u32 s88, s41, 0x3000\n" \
"buffer_store_dwordx4 v[252:255], v20, s[80:83], s88 offen offset:0 sc1\n" \
M_"v[136:139], a[244:247], a[212:215], v[136:139]\n" \
X_"a[164:165], v33 offset:0\n" \
M_"v[132:135], a[176:179], a[148:151], v[132:135]\n" \
"s_add_u32 s16, s22, s11\n" \
"s_addc_u32 s17, s23, 0\n" \
"s_nop 0\n" \
M_"v[132:135], a[240:243], a[212:215], v[132:135]\n" \
R_"a[148:151], v28 offset:10240\n" \
M_"v[148:151], a[176:179], a[152:155], v[148:151]\n" \
R_"a[212:215], v29 offset:10240\n" \
M_"v[148:151], a[240:243], a[216:219], v[148:151]\n" \
"s_add_u32 m0, s31, 0x8000\n" \
"s_mov_b32 s84, 0x180000\n" \
M_"v[152:155], a[180:183], a[152:155], v[152:155]\n" \
B_"v26, s[68:71], s84 offen offset:0 lds\n" \
M_"v[152:155], a[244:247], a[216:219], v[152:155]\n" \
X_"a[166:167], v33 offset:512\n" \
M_"v[156:159], a[184:187], a[152:155], v[156:159]\n" \
X_"a[228:229], v33 offset:4096\n" \
M_"v[156:159], a[248:251], a[216:219], v[156:159]\n" \
"s_addk_i32 s84, 0x7c00\n" \
M_"v[160:163], a[188:191], a[152:155], v[160:163]\n" \
B_"v26, s[68:71], s84 offen offset:1024 lds\n" \
M_"v[160:163], a[252:255], a[216:219], v[160:163]\n" \
R_"a[152:155], v28 offset:12288\n" \
M_"v[176:179], a[188:191], a[156:159], v[176:179]\n" \
R_"a[216:219], v29 offset:12288\n" \
M_"v[176:179], a[252:255], a[220:223], v[176:179]\n" \
"s_addk_i32 s84, 0x7c00\n" \
M_"v[172:175], a[184:187], a[156:159], v[172:175]\n" \
B_"v27, s[68:71], s84 offen offset:2048 lds\n" \
M_"v[172:175], a[248:251], a[220:223], v[172:175]\n" \
X_"a[230:231], v33 offset:4608\n" \
M_"v[168:171], a[180:183], a[156:159], v[168:171]\n" \
X_"a[172:173], v35 offset:0\n" \
M_"v[168:171], a[244:247], a[220:223], v[168:171]\n" \
X_"a[174:175], v35 offset:512\n" \
M_"v[164:167], a[176:179], a[156:159], v[164:167]\n" \
"s_add_u32 s18, s24, s11\n" \
"v_writelane_b32 v11, s16, 9\n" \
"s_addc_u32 s19, s25, 0\n" \
M_"v[164:167], a[240:243], a[220:223], v[164:167]\n" \
R_"a[156:159], v28 offset:14336\n" \
"s_and_b32 s20, s17, 0xffff\n" \
M_"v[84:87], a[176:179], a[136:139], v[84:87]\n" \
R_"a[220:223], v29 offset:14336\n" \
"v_writelane_b32 v11, s20, 10\n" \
M_"v[84:87], a[240:243], a[200:203], v[84:87]\n" \
"s_addk_i32 s84, 0x7c00\n" \
M_"v[88:91], a[180:183], a[136:139], v[88:91]\n" \
B_"v27, s[68:71], s84 offen offset:3072 lds\n" \
M_"v[88:91], a[244:247], a[200:203], v[88:91]\n" \
"s_add_u32 m0, s30, 0x8000\n" \
"s_movk_i32 s84, 0x200\n" \
M_"v[92:95], a[184:187], a[136:139], v[92:95]\n" \
B_"v24, s[76:79], s84 offen offset:0 lds\n" \
M_"v[92:95], a[248:251], a[200:203], v[92:95]\n" \
X_"a[236:237], v35 offset:4096\n" \
"s_and_b32 s20, s19, 0xffff\n" \
M_"v[96:99], a[188:191], a[136:139], v[96:99]\n" \
X_"a[238:239], v35 offset:4608\n" \
"v_writelane_b32 v11, s18, 11\n" \
M_"v[96:99], a[252:255], a[200:203], v[96:99]\n" \
R_"a[136:139], v28 offset:4096\n" \
M_"v[112:115], a[188:191], a[140:143], v[112:115]\n" \
R_"a[200:203], v29 offset:4096\n" \
M_"v[112:115], a[252:255], a[204:207], v[112:115]\n" \
"s_movk_i32 s84, 0x2200\n" \
"s_nop 0\n" \
M_"v[108:111], a[184:187], a[140:143], v[108:111]\n" \
B_"v25, s[76:79], s84 offen offset:1024 lds\n" \
M_"v[108:111], a[248:251], a[204:207], v[108:111]\n" \
"s_add_u32 s89, s41, 0x1000\n" \
"buffer_store_dwordx4 v[200:203], v20, s[80:83], s89 offen offset:3072 sc1\n" \
M_"v[104:107], a[180:183], a[140:143], v[104:107]\n" \
X_"a[168:169], v34 offset:0\n" \
M_"v[104:107], a[244:247], a[204:207], v[104:107]\n" \
X_"a[170:171], v34 offset:512\n" \
M_"v[100:103], a[176:179], a[140:143], v[100:103]\n" \
X_"a[232:233], v34 offset:4096\n" \
M_"v[100:103], a[240:243], a[204:207], v[100:103]\n" \
R_"a[140:143], v28 offset:6144\n" \
M_"v[52:55], a[176:179], a[128:131], v[52:55]\n" \
R_"a[204:207], v29 offset:6144\n" \
M_"v[52:55], a[240:243], a[192:195], v[52:55]\n" \
"s_movk_i32 s84, 0x4200\n" \
M_"v[56:59], a[180:183], a[128:131], v[56:59]\n" \
B_"v24, s[76:79], s84 offen offset:2048 lds\n" \
M_"v[56:59], a[244:247], a[192:195], v[56:59]\n" \
"s_movk_i32 s84, 0x6200\n" \
M_"v[60:63], a[184:187], a[128:131], v[60:63]\n" \
B_"v25, s[76:79], s84 offen offset:3072 lds\n" \
M_"v[60:63], a[248:251], a[192:195], v[60:63]\n" \
X_"a[234:235], v34 offset:4608\n" \
M_"v[64:67], a[188:191], a[128:131], v[64:67]\n" \
"s_cmpk_lt_u32 s3, 3\n" \
"s_cselect_b32 s16, s16, s18\n" \
M_"v[64:67], a[252:255], a[192:195], v[64:67]\n" \
R_"a[128:131], v28 offset:0\n" \
"s_movk_i32 s18, 0xfff\n" \
M_"v[80:83], a[188:191], a[132:135], v[80:83]\n" \
R_"a[192:195], v29 offset:0\n" \
"v_writelane_b32 v11, s20, 12\n" \
M_"v[80:83], a[252:255], a[196:199], v[80:83]\n" \
"s_and_b32 s20, s29, 0xffff\n" \
"v_writelane_b32 v11, s28, 13\n" \
M_"v[76:79], a[184:187], a[132:135], v[76:79]\n" \
"v_writelane_b32 v11, s20, 14\n" \
"v_writelane_b32 v11, s16, 15\n" \
M_"v[76:79], a[248:251], a[196:199], v[76:79]\n" \
"s_cmpk_lt_u32 s3, 3\n" \
"s_cselect_b32 s16, s17, s19\n" \
M_"v[72:75], a[180:183], a[132:135], v[72:75]\n" \
"s_and_b32 s16, s16, 0xffff\n" \
"v_writelane_b32 v11, s16, 16\n" \
M_"v[72:75], a[244:247], a[196:199], v[72:75]\n" \
"s_mul_i32 s16, s5, 0x48\n" \
"s_add_i32 s17, s16, 0x47\n" \
M_"v[68:71], a[176:179], a[132:135], v[68:71]\n" \
"s_cmpk_lt_u32 s3, 3\n" \
"s_cselect_b32 s17, s17, s18\n" \
M_"v[68:71], a[240:243], a[196:199], v[68:71]\n" \
R_"a[132:135], v28 offset:2048\n" \
R_"a[196:199], v29 offset:2048\n" \
"s_waitcnt lgkmcnt(15)\n" \
M_"a[0:3], a[160:163], a[144:147], a[0:3]\n" \
"s_add_u32 m0, s30, 0x9000\n" \
"s_addk_i32 s84, 0x3000\n" \
M_"a[0:3], a[224:227], a[208:211], a[0:3]\n" \
B_"v24, s[76:79], s84 offen offset:0 lds\n" \
M_"a[4:7], a[164:167], a[144:147], a[4:7]\n" \
X_"a[176:177], v36 offset:0\n" \
"s_mulk_i32 s17, 0x480\n" \
M_"a[4:7], a[228:231], a[208:211], a[4:7]\n" \
X_"a[178:179], v36 offset:1024\n" \
"s_addk_i32 s17, 0x120\n" \
M_"a[12:15], a[172:175], a[144:147], a[12:15]\n" \
X_"a[240:241], v36 offset:8192\n" \
"v_writelane_b32 v11, s17, 17\n" \
"s_waitcnt lgkmcnt(15)\n" \
M_"a[12:15], a[236:239], a[208:211], a[12:15]\n" \
"s_add_u32 s88, s41, 0x1000\n" \
"buffer_store_dwordx4 v[204:207], v20, s[80:83], s88 offen offset:2048 sc1\n" \
"s_waitcnt lgkmcnt(11)\n" \
M_"a[8:11], a[144:147], a[168:171], a[8:11]\n" \
X_"a[242:243], v36 offset:9216\n" \
"s_lshl_b32 s17, s3, 5\n" \
"s_waitcnt lgkmcnt(8)\n" \
M_"a[8:11], a[208:211], a[232:235], a[8:11]\n" \
X_"a[180:181], v37 offset:0\n" \
"s_add_i32 s16, s16, s17\n" \
M_"a[24:27], a[148:151], a[168:171], a[24:27]\n" \
X_"a[182:183], v37 offset:1024\n" \
"s_mulk_i32 s16, 0x480\n" \
M_"a[24:27], a[212:215], a[232:235], a[24:27]\n" \
X_"a[244:245], v37 offset:8192\n" \
"s_mul_i32 s17, s12, 0x24000\n" \
M_"a[28:31], a[172:175], a[148:151], a[28:31]\n" \
"s_add_i32 s18, s17, 0x1b000\n" \
M_"a[28:31], a[236:239], a[212:215], a[28:31]\n" \
"s_addk_i32 s84, 0x2000\n" \
M_"a[20:23], a[164:167], a[148:151], a[20:23]\n" \
B_"v25, s[76:79], s84 offen offset:1024 lds\n" \
M_"a[20:23], a[228:231], a[212:215], a[20:23]\n" \
X_"a[246:247], v37 offset:9216\n" \
M_"a[16:19], a[160:163], a[148:151], a[16:19]\n" \
X_"a[184:185], v38 offset:0\n" \
M_"a[16:19], a[224:227], a[212:215], a[16:19]\n" \
X_"a[186:187], v38 offset:1024\n" \
M_"a[32:35], a[160:163], a[152:155], a[32:35]\n" \
"s_addk_i32 s84, 0x2000\n" \
"s_nop 0\n" \
M_"a[32:35], a[224:227], a[216:219], a[32:35]\n" \
B_"v24, s[76:79], s84 offen offset:2048 lds\n" \
M_"a[36:39], a[164:167], a[152:155], a[36:39]\n" \
".fill (L_pb + 65248 - .) / 4, 4, 0xBF800000\n" \
"L_pp16:\n" \
"s_cbranch_execz L_pret\n" \
"s_nop 0\n" \
X_"a[248:249], v38 offset:8192\n" \
M_"a[36:39], a[228:231], a[216:219], a[36:39]\n" \
X_"a[250:251], v38 offset:9216\n" \
M_"a[44:47], a[172:175], a[152:155], a[44:47]\n" \
X_"a[188:189], v39 offset:0\n" \
M_"a[44:47], a[236:239], a[216:219], a[44:47]\n" \
X_"a[190:191], v39 offset:1024\n" \
M_"a[40:43], a[152:155], a[168:171], a[40:43]\n" \
"s_add_u32 s89, s41, 0x1000\n" \
"buffer_store_dwordx4 v[208:211], v20, s[80:83], s89 offen offset:1024 sc1\n" \
M_"a[40:43], a[216:219], a[232:235], a[40:43]\n" \
X_"a[252:253], v39 offset:8192\n" \
M_"a[56:59], a[156:159], a[168:171], a[56:59]\n" \
X_"a[254:255], v39 offset:9216\n" \
M_"a[56:59], a[220:223], a[232:235], a[56:59]\n" \
"s_cmpk_lt_u32 s3, 3\n" \
"s_cselect_b32 s16, s16, s18\n" \
M_"a[60:63], a[172:175], a[156:159], a[60:63]\n" \
"s_xor_b32 s18, s3, 2\n" \
"v_writelane_b32 v11, s16, 18\n" \
M_"a[60:63], a[236:239], a[220:223], a[60:63]\n" \
"s_addk_i32 s84, 0x2000\n" \
M_"a[52:55], a[164:167], a[156:159], a[52:55]\n" \
B_"v25, s[76:79], s84 offen offset:3072 lds\n" \
M_"a[52:55], a[228:231], a[220:223], a[52:55]\n" \
"s_mul_i32 s16, s15, 0x2400\n" \
"s_mul_i32 s15, s15, 0x9000\n" \
M_"a[48:51], a[160:163], a[156:159], a[48:51]\n" \
"s_bitset1_b32 s16, 17\n" \
"v_writelane_b32 v11, s16, 22\n" \
M_"a[48:51], a[224:227], a[220:223], a[48:51]\n" \
"s_add_i32 s17, s17, s15\n" \
"s_mul_i32 s15, s3, 0x2400\n" \
M_"a[64:67], a[164:167], a[140:143], a[64:67]\n" \
"v_writelane_b32 v11, s17, 29\n" \
"s_add_i32 s16, s15, 0xa400\n" \
M_"a[64:67], a[228:231], a[204:207], a[64:67]\n" \
"s_mov_b32 s17, 0x8000\n" \
M_"a[80:83], a[172:175], a[140:143], a[80:83]\n" \
"s_cmpk_lt_u32 s3, 3\n" \
"s_cselect_b32 s16, s16, s17\n" \
M_"a[80:83], a[236:239], a[204:207], a[80:83]\n" \
"s_mov_b32 s17, 0x1b000\n" \
M_"a[68:71], a[140:143], a[168:171], a[68:71]\n" \
"s_cmp_lg_u32 s18, 0\n" \
"s_cselect_b32 s17, s16, s17\n" \
"s_waitcnt vmcnt(11) lgkmcnt(0)\n" \
M_"a[68:71], a[204:207], a[232:235], a[68:71]\n" \
"s_barrier\n" \
"v_xor_b32_e32 v28, 0x20000, v28\n" \
"v_xor_b32_e32 v29, 0x20000, v29\n" \
M_"v[116:119], a[176:179], a[144:147], v[116:119]\n" \
"s_add_u32 m0, s40, 0x0\n" \
"s_nop 0\n" \
"s_add_u32 s85, s39, 0x8000\n" \
M_"v[116:119], a[240:243], a[208:211], v[116:119]\n" \
B_"v20, s[80:83], s85 offen sc1 lds\n" \
M_"v[120:123], a[180:183], a[144:147], v[120:123]\n" \
X_"a[160:161], v32 offset:32768\n" \
"v_writelane_b32 v11, s16, 23\n" \
M_"v[120:123], a[244:247], a[208:211], v[120:123]\n" \
X_"a[162:163], v32 offset:33280\n" \
"s_or_b32 s16, s15, 0x10000\n" \
M_"v[124:127], a[184:187], a[144:147], v[124:127]\n" \
"s_add_u32 m0, s40, 0x1000\n" \
"s_add_u32 s85, s39, 0x9000\n" \
M_"v[124:127], a[248:251], a[208:211], v[124:127]\n" \
B_"v20, s[80:83], s85 offen sc1 lds\n" \
M_"v[128:131], a[188:191], a[144:147], v[128:131]\n" \
X_"a[224:225], v32 offset:36864\n" \
"v_writelane_b32 v11, s17, 24\n" \
M_"v[128:131], a[252:255], a[208:211], v[128:131]\n" \
R_"a[144:147], v28 offset:8192\n" \
"s_add_i32 s17, s15, 0x19000\n" \
M_"v[144:147], a[188:191], a[148:151], v[144:147]\n" \
R_"a[208:211], v29 offset:8192\n" \
"v_writelane_b32 v11, s16, 25\n" \
M_"v[144:147], a[252:255], a[212:215], v[144:147]\n" \
"s_add_u32 m0, s40, 0x3000\n" \
"s_movk_i32 s84, 0x200\n" \
M_"v[140:143], a[184:187], a[148:151], v[140:143]\n" \
B_"v21, s[76:79], s84 offen offset:0 lds\n" \
M_"v[140:143], a[248:251], a[212:215], v[140:143]\n" \
X_"a[226:227], v32 offset:37376\n" \
"s_mov_b32 s16, 0\n" \
M_"v[136:139], a[180:183], a[148:151], v[136:139]\n" \
"s_add_u32 s88, s41, 0x1000\n" \
"buffer_store_dwordx4 v[212:215], v20, s[80:83], s88 offen offset:0 sc1\n" \
M_"v[136:139], a[244:247], a[212:215], v[136:139]\n" \
X_"a[164:165], v33 offset:32768\n" \
M_"v[132:135], a[176:179], a[148:151], v[132:135]\n" \
"s_cmpk_lt_u32 s3, 3\n" \
"s_cselect_b32 s17, s17, s16\n" \
M_"v[132:135], a[240:243], a[212:215], v[132:135]\n" \
R_"a[148:151], v28 offset:10240\n" \
"s_bitset1_b32 s15, 17\n" \
M_"v[148:151], a[176:179], a[152:155], v[148:151]\n" \
R_"a[212:215], v29 offset:10240\n" \
"v_writelane_b32 v11, s17, 26\n" \
M_"v[148:151], a[240:243], a[216:219], v[148:151]\n" \
"s_add_u32 m0, s31, 0x0\n" \
"s_mov_b32 s84, 0x200000\n" \
M_"v[152:155], a[180:183], a[152:155], v[152:155]\n" \
B_"v26, s[68:71], s84 offen offset:0 lds\n" \
M_"v[152:155], a[244:247], a[216:219], v[152:155]\n" \
X_"a[166:167], v33 offset:33280\n" \
"s_movk_i32 s17, 0x2400\n" \
M_"v[156:159], a[184:187], a[152:155], v[156:159]\n" \
X_"a[228:229], v33 offset:36864\n" \
M_"v[156:159], a[248:251], a[216:219], v[156:159]\n" \
"s_addk_i32 s84, 0x7c00\n" \
M_"v[160:163], a[188:191], a[152:155], v[160:163]\n" \
B_"v26, s[68:71], s84 offen offset:1024 lds\n" \
M_"v[160:163], a[252:255], a[216:219], v[160:163]\n" \
R_"a[152:155], v28 offset:12288\n" \
M_"v[176:179], a[188:191], a[156:159], v[176:179]\n" \
R_"a[216:219], v29 offset:12288\n" \
M_"v[176:179], a[252:255], a[220:223], v[176:179]\n" \
"s_addk_i32 s84, 0x7c00\n" \
M_"v[172:175], a[184:187], a[156:159], v[172:175]\n" \
B_"v27, s[68:71], s84 offen offset:2048 lds\n" \
M_"v[172:175], a[248:251], a[220:223], v[172:175]\n" \
X_"a[230:231], v33 offset:37376\n" \
M_"v[168:171], a[180:183], a[156:159], v[168:171]\n" \
X_"a[172:173], v35 offset:32768\n" \
M_"v[168:171], a[244:247], a[220:223], v[168:171]\n" \
X_"a[174:175], v35 offset:33280\n" \
M_"v[164:167], a[176:179], a[156:159], v[164:167]\n" \
"s_cmpk_lt_u32 s3, 3\n" \
"s_cselect_b32 s15, s15, s17\n" \
M_"v[164:167], a[240:243], a[220:223], v[164:167]\n" \
R_"a[156:159], v28 offset:14336\n" \
"v_writelane_b32 v11, s15, 27\n" \
M_"v[84:87], a[176:179], a[136:139], v[84:87]\n" \
R_"a[220:223], v29 offset:14336\n" \
"s_lshl_b32 s15, s3, 10\n" \
M_"v[84:87], a[240:243], a[200:203], v[84:87]\n" \
"s_addk_i32 s84, 0x7c00\n" \
M_"v[88:91], a[180:183], a[136:139], v[88:91]\n" \
B_"v27, s[68:71], s84 offen offset:3072 lds\n" \
M_"v[88:91], a[244:247], a[200:203], v[88:91]\n" \
"s_add_u32 m0, s30, 0x20000\n" \
"s_movk_i32 s84, 0x280\n" \
M_"v[92:95], a[184:187], a[136:139], v[92:95]\n" \
B_"v24, s[76:79], s84 offen offset:0 lds\n" \
M_"v[92:95], a[248:251], a[200:203], v[92:95]\n" \
X_"a[236:237], v35 offset:36864\n" \
"s_addk_i32 s15, 0x4800\n" \
M_"v[96:99], a[188:191], a[136:139], v[96:99]\n" \
X_"a[238:239], v35 offset:37376\n" \
"v_writelane_b32 v11, s15, 28\n" \
M_"v[96:99], a[252:255], a[200:203], v[96:99]\n" \
R_"a[136:139], v28 offset:4096\n" \
"s_add_i32 s15, s14, 1\n" \
M_"v[112:115], a[188:191], a[140:143], v[112:115]\n" \
R_"a[200:203], v29 offset:4096\n" \
"s_and_b32 s15, s15, 3\n" \
M_"v[112:115], a[252:255], a[204:207], v[112:115]\n" \
"s_movk_i32 s84, 0x2280\n" \
M_"v[108:111], a[184:187], a[140:143], v[108:111]\n" \
B_"v25, s[76:79], s84 offen offset:1024 lds\n" \
M_"v[108:111], a[248:251], a[204:207], v[108:111]\n" \
"s_add_u32 s89, s42, 0x0\n" \
"buffer_store_dwordx4 v[216:219], v20, s[80:83], s89 offen offset:1024 sc1\n" \
M_"v[104:107], a[180:183], a[140:143], v[104:107]\n" \
X_"a[168:169], v34 offset:32768\n" \
"s_or_b32 s15, s15, s4\n" \
M_"v[104:107], a[244:247], a[204:207], v[104:107]\n" \
X_"a[170:171], v34 offset:33280\n" \
"s_lshl_b32 s17, s15, 2\n" \
M_"v[100:103], a[176:179], a[140:143], v[100:103]\n" \
X_"a[232:233], v34 offset:36864\n" \
"s_or_b32 s17, s17, s3\n" \
M_"v[100:103], a[240:243], a[204:207], v[100:103]\n" \
R_"a[140:143], v28 offset:6144\n" \
"s_mul_i32 s17, s17, 0x9000\n" \
M_"v[52:55], a[176:179], a[128:131], v[52:55]\n" \
R_"a[204:207], v29 offset:6144\n" \
"v_writelane_b32 v11, s17, 30\n" \
M_"v[52:55], a[240:243], a[192:195], v[52:55]\n" \
"s_movk_i32 s84, 0x4280\n" \
M_"v[56:59], a[180:183], a[128:131], v[56:59]\n" \
B_"v24, s[76:79], s84 offen offset:2048 lds\n" \
M_"v[56:59], a[244:247], a[192:195], v[56:59]\n" \
"s_movk_i32 s84, 0x6280\n" \
M_"v[60:63], a[184:187], a[128:131], v[60:63]\n" \
B_"v25, s[76:79], s84 offen offset:3072 lds\n" \
M_"v[60:63], a[248:251], a[192:195], v[60:63]\n" \
X_"a[234:235], v34 offset:37376\n" \
"s_lshl_b32 s17, s12, 2\n" \
M_"v[64:67], a[188:191], a[128:131], v[64:67]\n" \
"s_or_b32 s17, s17, s3\n" \
"s_add_i32 s14, s14, -1\n" \
M_"v[64:67], a[252:255], a[192:195], v[64:67]\n" \
R_"a[128:131], v28 offset:0\n" \
"s_xor_b32 s17, s17, 8\n" \
M_"v[80:83], a[188:191], a[132:135], v[80:83]\n" \
R_"a[192:195], v29 offset:0\n" \
"s_and_b32 s14, s14, 3\n" \
M_"v[80:83], a[252:255], a[196:199], v[80:83]\n" \
"s_mul_i32 s17, s17, 0x9000\n" \
"s_or_b32 s14, s14, s4\n" \
M_"v[76:79], a[184:187], a[132:135], v[76:79]\n" \
"v_writelane_b32 v11, s17, 31\n" \
"s_lshl_b32 s17, s14, 2\n" \
M_"v[76:79], a[248:251], a[196:199], v[76:79]\n" \
"s_or_b32 s17, s17, s3\n" \
"s_mul_i32 s17, s17, 0x9000\n" \
M_"v[72:75], a[180:183], a[132:135], v[72:75]\n" \
"v_writelane_b32 v11, s17, 32\n" \
"s_or_b32 s17, s8, s13\n" \
M_"v[72:75], a[244:247], a[196:199], v[72:75]\n" \
"s_mul_i32 s17, s17, 0x24800\n" \
"s_lshl_b32 s15, s15, 12\n" \
M_"v[68:71], a[176:179], a[132:135], v[68:71]\n" \
"s_add_i32 s18, s17, 0x3004800\n" \
"s_lshl_b32 s12, s12, 12\n" \
M_"v[68:71], a[240:243], a[196:199], v[68:71]\n" \
R_"a[132:135], v28 offset:2048\n" \
R_"a[196:199], v29 offset:2048\n" \
"s_waitcnt lgkmcnt(15)\n" \
M_"a[0:3], a[160:163], a[144:147], a[0:3]\n" \
"s_add_u32 m0, s30, 0x21000\n" \
"s_addk_i32 s84, 0x3000\n" \
M_"a[0:3], a[224:227], a[208:211], a[0:3]\n" \
B_"v24, s[76:79], s84 offen offset:0 lds\n" \
M_"a[4:7], a[164:167], a[144:147], a[4:7]\n" \
X_"a[176:177], v36 offset:32768\n" \
"s_add_i32 s19, s18, s15\n" \
M_"a[4:7], a[228:231], a[208:211], a[4:7]\n" \
X_"a[178:179], v36 offset:33792\n" \
"v_writelane_b32 v11, s19, 33\n" \
M_"a[12:15], a[172:175], a[144:147], a[12:15]\n" \
X_"a[240:241], v36 offset:40960\n" \
"s_xor_b32 s12, s12, 0x2000\n" \
"s_waitcnt lgkmcnt(15)\n" \
M_"a[12:15], a[236:239], a[208:211], a[12:15]\n" \
"s_add_u32 s88, s42, 0x0\n" \
"buffer_store_dwordx4 v[220:223], v20, s[80:83], s88 offen offset:0 sc1\n" \
"s_waitcnt lgkmcnt(11)\n" \
M_"a[8:11], a[144:147], a[168:171], a[8:11]\n" \
X_"a[242:243], v36 offset:41984\n" \
"s_lshl_b32 s14, s14, 12\n" \
"s_waitcnt lgkmcnt(8)\n" \
M_"a[8:11], a[208:211], a[232:235], a[8:11]\n" \
X_"a[180:181], v37 offset:32768\n" \
"s_sub_i32 s13, s8, s13\n" \
M_"a[24:27], a[148:151], a[168:171], a[24:27]\n" \
X_"a[182:183], v37 offset:33792\n" \
"s_add_i32 s19, s18, s12\n" \
M_"a[24:27], a[212:215], a[232:235], a[24:27]\n" \
X_"a[244:245], v37 offset:40960\n" \
"v_writelane_b32 v11, s19, 34\n" \
M_"a[28:31], a[172:175], a[148:151], a[28:31]\n" \
"s_add_i32 s18, s18, s14\n" \
"s_mul_i32 s13, s13, 0x24800\n" \
M_"a[28:31], a[236:239], a[212:215], a[28:31]\n" \
"s_addk_i32 s84, 0x2000\n" \
M_"a[20:23], a[164:167], a[148:151], a[20:23]\n" \
B_"v25, s[76:79], s84 offen offset:1024 lds\n" \
M_"a[20:23], a[228:231], a[212:215], a[20:23]\n" \
X_"a[246:247], v37 offset:41984\n" \
"v_writelane_b32 v11, s18, 35\n" \
M_"a[16:19], a[160:163], a[148:151], a[16:19]\n" \
X_"a[184:185], v38 offset:32768\n" \
"s_add_i32 s18, s13, 0x4120800\n" \
M_"a[16:19], a[224:227], a[212:215], a[16:19]\n" \
X_"a[186:187], v38 offset:33792\n" \
"s_add_i32 s15, s18, s15\n" \
M_"a[32:35], a[160:163], a[152:155], a[32:35]\n" \
"s_addk_i32 s84, 0x2000\n" \
M_"a[32:35], a[224:227], a[216:219], a[32:35]\n" \
B_"v24, s[76:79], s84 offen offset:2048 lds\n" \
M_"a[36:39], a[164:167], a[152:155], a[36:39]\n" \
X_"a[248:249], v38 offset:40960\n" \
"v_writelane_b32 v11, s15, 36\n" \
M_"a[36:39], a[228:231], a[216:219], a[36:39]\n" \
X_"a[250:251], v38 offset:41984\n" \
"s_add_i32 s12, s18, s12\n" \
M_"a[44:47], a[172:175], a[152:155], a[44:47]\n" \
X_"a[188:189], v39 offset:32768\n" \
"v_writelane_b32 v11, s12, 37\n" \
M_"a[44:47], a[236:239], a[216:219], a[44:47]\n" \
X_"a[190:191], v39 offset:33792\n" \
"s_add_i32 s18, s18, s14\n" \
M_"a[40:43], a[152:155], a[168:171], a[40:43]\n" \
"s_add_u32 s89, s42, 0x1000\n" \
"buffer_store_dwordx4 v[224:227], v20, s[80:83], s89 offen offset:3072 sc1\n" \
M_"a[40:43], a[216:219], a[232:235], a[40:43]\n" \
X_"a[252:253], v39 offset:40960\n" \
"s_or_b32 s12, s17, 0x3000000\n" \
M_"a[56:59], a[156:159], a[168:171], a[56:59]\n" \
X_"a[254:255], v39 offset:41984\n" \
"s_lshl_b32 s14, s5, 11\n" \
M_"a[56:59], a[220:223], a[232:235], a[56:59]\n" \
"v_writelane_b32 v11, s18, 38\n" \
"s_add_i32 s15, s12, s14\n" \
M_"a[60:63], a[172:175], a[156:159], a[60:63]\n" \
"v_writelane_b32 v11, s15, 39\n" \
"s_add_i32 s15, s13, 0x411c000\n" \
M_"a[60:63], a[236:239], a[220:223], a[60:63]\n" \
"s_addk_i32 s84, 0x2000\n" \
M_"a[52:55], a[164:167], a[156:159], a[52:55]\n" \
B_"v25, s[76:79], s84 offen offset:3072 lds\n" \
M_"a[52:55], a[228:231], a[220:223], a[52:55]\n" \
"s_add_i32 s18, s15, s14\n" \
"v_writelane_b32 v11, s18, 40\n" \
M_"a[48:51], a[160:163], a[156:159], a[48:51]\n" \
"s_add_i32 s17, s17, 0x3004000\n" \
"v_writelane_b32 v11, s17, 41\n" \
M_"a[48:51], a[224:227], a[220:223], a[48:51]\n" \
"s_add_i32 s13, s13, 0x4120000\n" \
"v_writelane_b32 v11, s13, 42\n" \
M_"a[64:67], a[164:167], a[140:143], a[64:67]\n" \
"s_lshl_b32 s13, s9, 2\n" \
"s_or_b32 s7, s13, s7\n" \
M_"a[64:67], a[228:231], a[204:207], a[64:67]\n" \
"s_mul_i32 s7, s7, 0x90000\n" \
"s_lshl_b32 s13, s3, 14\n" \
M_"a[80:83], a[172:175], a[140:143], a[80:83]\n" \
"s_lshl_b32 s9, s9, 7\n" \
"s_lshl_b32 s8, s8, 4\n" \
M_"a[80:83], a[236:239], a[204:207], a[80:83]\n" \
"s_add_i32 s7, s7, s13\n" \
"s_or_b32 s13, s8, s9\n" \
M_"a[68:71], a[140:143], a[168:171], a[68:71]\n" \
"s_sub_i32 s8, s8, s9\n" \
"s_mulk_i32 s13, 0x480\n" \
"s_waitcnt vmcnt(11) lgkmcnt(0)\n" \
M_"a[68:71], a[204:207], a[232:235], a[68:71]\n" \
"s_barrier\n" \
M_"v[116:119], a[176:179], a[144:147], v[116:119]\n" \
"s_add_u32 m0, s40, 0x8000\n" \
"s_add_u32 s85, s39, 0xa000\n" \
M_"v[116:119], a[240:243], a[208:211], v[116:119]\n" \
B_"v20, s[80:83], s85 offen sc1 lds\n" \
M_"v[120:123], a[180:183], a[144:147], v[120:123]\n" \
X_"a[160:161], v32 offset:0\n" \
"v_accvgpr_read_b32 v212, a108\n" \
M_"v[120:123], a[244:247], a[208:211], v[120:123]\n" \
X_"a[162:163], v32 offset:512\n" \
"v_accvgpr_read_b32 v213, a109\n" \
M_"v[124:127], a[184:187], a[144:147], v[124:127]\n" \
"s_add_u32 m0, s40, 0x9000\n" \
"s_add_u32 s85, s39, 0xb000\n" \
M_"v[124:127], a[248:251], a[208:211], v[124:127]\n" \
B_"v20, s[80:83], s85 offen sc1 lds\n" \
M_"v[128:131], a[188:191], a[144:147], v[128:131]\n" \
X_"a[224:225], v32 offset:4096\n" \
"v_accvgpr_read_b32 v214, a110\n" \
M_"v[128:131], a[252:255], a[208:211], v[128:131]\n" \
R_"a[144:147], v28 offset:40960\n" \
"v_accvgpr_read_b32 v215, a111\n" \
M_"v[144:147], a[188:191], a[148:151], v[144:147]\n" \
R_"a[208:211], v29 offset:40960\n" \
"v_accvgpr_read_b32 v180, a124\n" \
M_"v[144:147], a[252:255], a[212:215], v[144:147]\n" \
"s_add_u32 m0, s40, 0xb000\n" \
"s_movk_i32 s84, 0x280\n" \
"s_nop 0\n" \
M_"v[140:143], a[184:187], a[148:151], v[140:143]\n" \
B_"v21, s[76:79], s84 offen offset:0 lds\n" \
M_"v[140:143], a[248:251], a[212:215], v[140:143]\n" \
X_"a[226:227], v32 offset:4608\n" \
"v_accvgpr_read_b32 v181, a125\n" \
M_"v[136:139], a[180:183], a[148:151], v[136:139]\n" \
"s_add_u32 s88, s42, 0x1000\n" \
"buffer_store_dwordx4 v[228:231], v20, s[80:83], s88 offen offset:2048 sc1\n" \
M_"v[136:139], a[244:247], a[212:215], v[136:139]\n" \
X_"a[164:165], v33 offset:0\n" \
"v_accvgpr_read_b32 v182, a126\n" \
M_"v[132:135], a[176:179], a[148:151], v[132:135]\n" \
"v_accvgpr_read_b32 v183, a127\n" \
C_"v212, v212, v213\n" \
M_"v[132:135], a[240:243], a[212:215], v[132:135]\n" \
R_"a[148:151], v28 offset:43008\n" \
C_"v213, v214, v215\n" \
M_"v[148:151], a[176:179], a[152:155], v[148:151]\n" \
R_"a[212:215], v29 offset:43008\n" \
C_"v214, v180, v181\n" \
M_"v[148:151], a[240:243], a[216:219], v[148:151]\n" \
"s_add_u32 m0, s31, 0x8000\n" \
"s_mov_b32 s84, 0x280000\n" \
M_"v[152:155], a[180:183], a[152:155], v[152:155]\n" \
B_"v26, s[68:71], s84 offen offset:0 lds\n" \
M_"v[152:155], a[244:247], a[216:219], v[152:155]\n" \
X_"a[166:167], v33 offset:512\n" \
C_"v215, v182, v183\n" \
M_"v[156:159], a[184:187], a[152:155], v[156:159]\n" \
X_"a[228:229], v33 offset:4096\n" \
"v_accvgpr_read_b32 v208, a104\n" \
M_"v[156:159], a[248:251], a[216:219], v[156:159]\n" \
"s_addk_i32 s84, 0x7c00\n" \
M_"v[160:163], a[188:191], a[152:155], v[160:163]\n" \
B_"v26, s[68:71], s84 offen offset:1024 lds\n" \
M_"v[160:163], a[252:255], a[216:219], v[160:163]\n" \
R_"a[152:155], v28 offset:45056\n" \
"v_accvgpr_read_b32 v209, a105\n" \
M_"v[176:179], a[188:191], a[156:159], v[176:179]\n" \
R_"a[216:219], v29 offset:45056\n" \
"v_accvgpr_read_b32 v210, a106\n" \
M_"v[176:179], a[252:255], a[220:223], v[176:179]\n" \
"s_addk_i32 s84, 0x7c00\n" \
M_"v[172:175], a[184:187], a[156:159], v[172:175]\n" \
B_"v27, s[68:71], s84 offen offset:2048 lds\n" \
M_"v[172:175], a[248:251], a[220:223], v[172:175]\n" \
X_"a[230:231], v33 offset:4608\n" \
"v_accvgpr_read_b32 v211, a107\n" \
M_"v[168:171], a[180:183], a[156:159], v[168:171]\n" \
X_"a[172:173], v35 offset:0\n" \
"v_accvgpr_read_b32 v180, a120\n" \
M_"v[168:171], a[244:247], a[220:223], v[168:171]\n" \
X_"a[174:175], v35 offset:512\n" \
"v_accvgpr_read_b32 v181, a121\n" \
M_"v[164:167], a[176:179], a[156:159], v[164:167]\n" \
"v_accvgpr_read_b32 v182, a122\n" \
"v_accvgpr_read_b32 v183, a123\n" \
M_"v[164:167], a[240:243], a[220:223], v[164:167]\n" \
R_"a[156:159], v28 offset:47104\n" \
C_"v208, v208, v209\n" \
M_"v[84:87], a[176:179], a[136:139], v[84:87]\n" \
R_"a[220:223], v29 offset:47104\n" \
C_"v209, v210, v211\n" \
M_"v[84:87], a[240:243], a[200:203], v[84:87]\n" \
"s_addk_i32 s84, 0x7c00\n" \
M_"v[88:91], a[180:183], a[136:139], v[88:91]\n" \
B_"v27, s[68:71], s84 offen offset:3072 lds\n" \
M_"v[88:91], a[244:247], a[200:203], v[88:91]\n" \
"s_add_u32 m0, s30, 0x0\n" \
"s_movk_i32 s84, 0x300\n" \
"s_nop 0\n" \
M_"v[92:95], a[184:187], a[136:139], v[92:95]\n" \
B_"v24, s[76:79], s84 offen offset:0 lds\n" \
M_"v[92:95], a[248:251], a[200:203], v[92:95]\n" \
X_"a[236:237], v35 offset:4096\n" \
C_"v210, v180, v181\n" \
M_"v[96:99], a[188:191], a[136:139], v[96:99]\n" \
X_"a[238:239], v35 offset:4608\n" \
C_"v211, v182, v183\n" \
M_"v[96:99], a[252:255], a[200:203], v[96:99]\n" \
R_"a[136:139], v28 offset:36864\n" \
"v_accvgpr_read_b32 v204, a100\n" \
M_"v[112:115], a[188:191], a[140:143], v[112:115]\n" \
R_"a[200:203], v29 offset:36864\n" \
"v_accvgpr_read_b32 v205, a101\n" \
M_"v[112:115], a[252:255], a[204:207], v[112:115]\n" \
"s_movk_i32 s84, 0x2300\n" \
"s_nop 0\n" \
M_"v[108:111], a[184:187], a[140:143], v[108:111]\n" \
B_"v25, s[76:79], s84 offen offset:1024 lds\n" \
M_"v[108:111], a[248:251], a[204:207], v[108:111]\n" \
"s_add_u32 s89, s42, 0x1000\n" \
"buffer_store_dwordx4 v[232:235], v20, s[80:83], s89 offen offset:1024 sc1\n" \
M_"v[104:107], a[180:183], a[140:143], v[104:107]\n" \
X_"a[168:169], v34 offset:0\n" \
"v_accvgpr_read_b32 v206, a102\n" \
M_"v[104:107], a[244:247], a[204:207], v[104:107]\n" \
X_"a[170:171], v34 offset:512\n" \
"v_accvgpr_read_b32 v207, a103\n" \
M_"v[100:103], a[176:179], a[140:143], v[100:103]\n" \
X_"a[232:233], v34 offset:4096\n" \
"v_accvgpr_read_b32 v180, a116\n" \
M_"v[100:103], a[240:243], a[204:207], v[100:103]\n" \
R_"a[140:143], v28 offset:38912\n" \
"v_accvgpr_read_b32 v181, a117\n" \
M_"v[52:55], a[176:179], a[128:131], v[52:55]\n" \
R_"a[204:207], v29 offset:38912\n" \
"v_accvgpr_read_b32 v182, a118\n" \
M_"v[52:55], a[240:243], a[192:195], v[52:55]\n" \
"s_movk_i32 s84, 0x4300\n" \
M_"v[56:59], a[180:183], a[128:131], v[56:59]\n" \
B_"v24, s[76:79], s84 offen offset:2048 lds\n" \
M_"v[56:59], a[244:247], a[192:195], v[56:59]\n" \
"s_movk_i32 s84, 0x6300\n" \
M_"v[60:63], a[184:187], a[128:131], v[60:63]\n" \
B_"v25, s[76:79], s84 offen offset:3072 lds\n" \
M_"v[60:63], a[248:251], a[192:195], v[60:63]\n" \
X_"a[234:235], v34 offset:4608\n" \
"v_accvgpr_read_b32 v183, a119\n" \
M_"v[64:67], a[188:191], a[128:131], v[64:67]\n" \
C_"v204, v204, v205\n" \
C_"v205, v206, v207\n" \
M_"v[64:67], a[252:255], a[192:195], v[64:67]\n" \
R_"a[128:131], v28 offset:32768\n" \
C_"v206, v180, v181\n" \
M_"v[80:83], a[188:191], a[132:135], v[80:83]\n" \
R_"a[192:195], v29 offset:32768\n" \
C_"v207, v182, v183\n" \
M_"v[80:83], a[252:255], a[196:199], v[80:83]\n" \
"v_accvgpr_read_b32 v200, a96\n" \
"v_accvgpr_read_b32 v201, a97\n" \
M_"v[76:79], a[184:187], a[132:135], v[76:79]\n" \
"v_accvgpr_read_b32 v202, a98\n" \
"v_accvgpr_read_b32 v203, a99\n" \
M_"v[76:79], a[248:251], a[196:199], v[76:79]\n" \
"v_accvgpr_read_b32 v180, a112\n" \
"v_accvgpr_read_b32 v181, a113\n" \
M_"v[72:75], a[180:183], a[132:135], v[72:75]\n" \
"v_accvgpr_read_b32 v182, a114\n" \
"v_accvgpr_read_b32 v183, a115\n" \
M_"v[72:75], a[244:247], a[196:199], v[72:75]\n" \
C_"v200, v200, v201\n" \
C_"v201, v202, v203\n" \
M_"v[68:71], a[176:179], a[132:135], v[68:71]\n" \
C_"v202, v180, v181\n" \
C_"v203, v182, v183\n" \
M_"v[68:71], a[240:243], a[196:199], v[68:71]\n" \
R_"a[132:135], v28 offset:34816\n" \
R_"a[196:199], v29 offset:34816\n" \
"s_waitcnt lgkmcnt(15)\n" \
M_"a[0:3], a[160:163], a[144:147], a[0:3]\n" \
"s_add_u32 m0, s30, 0x1000\n" \
"s_addk_i32 s84, 0x3000\n" \
M_"a[0:3], a[224:227], a[208:211], a[0:3]\n" \
B_"v24, s[76:79], s84 offen offset:0 lds\n" \
M_"a[4:7], a[164:167], a[144:147], a[4:7]\n" \
X_"a[176:177], v36 offset:0\n" \
"v_accvgpr_read_b32 v196, a76\n" \
M_"a[4:7], a[228:231], a[208:211], a[4:7]\n" \
X_"a[178:179], v36 offset:1024\n" \
"v_accvgpr_read_b32 v197, a77\n" \
M_"a[12:15], a[172:175], a[144:147], a[12:15]\n" \
X_"a[240:241], v36 offset:8192\n" \
"v_accvgpr_read_b32 v198, a78\n" \
"s_waitcnt lgkmcnt(15)\n" \
M_"a[12:15], a[236:239], a[208:211], a[12:15]\n" \
"s_add_u32 s88, s42, 0x1000\n" \
"buffer_store_dwordx4 v[236:239], v20, s[80:83], s88 offen offset:0 sc1\n" \
"s_waitcnt lgkmcnt(11)\n" \
M_"a[8:11], a[144:147], a[168:171], a[8:11]\n" \
X_"a[242:243], v36 offset:9216\n" \
"v_accvgpr_read_b32 v199, a79\n" \
"s_waitcnt lgkmcnt(8)\n" \
"s_nop 0\n" \
M_"a[8:11], a[208:211], a[232:235], a[8:11]\n" \
X_"a[180:181], v37 offset:0\n" \
"v_accvgpr_read_b32 v180, a92\n" \
M_"a[24:27], a[148:151], a[168:171], a[24:27]\n" \
X_"a[182:183], v37 offset:1024\n" \
"v_accvgpr_read_b32 v181, a93\n" \
M_"a[24:27], a[212:215], a[232:235], a[24:27]\n" \
X_"a[244:245], v37 offset:8192\n" \
"v_accvgpr_read_b32 v182, a94\n" \
M_"a[28:31], a[172:175], a[148:151], a[28:31]\n" \
"v_accvgpr_read_b32 v183, a95\n" \
C_"v196, v196, v197\n" \
M_"a[28:31], a[236:239], a[212:215], a[28:31]\n" \
"s_addk_i32 s84, 0x2000\n" \
"s_nop 0\n" \
M_"a[20:23], a[164:167], a[148:151], a[20:23]\n" \
B_"v25, s[76:79], s84 offen offset:1024 lds\n" \
M_"a[20:23], a[228:231], a[212:215], a[20:23]\n" \
X_"a[246:247], v37 offset:9216\n" \
C_"v197, v198, v199\n" \
M_"a[16:19], a[160:163], a[148:151], a[16:19]\n" \
X_"a[184:185], v38 offset:0\n" \
C_"v198, v180, v181\n" \
M_"a[16:19], a[224:227], a[212:215], a[16:19]\n" \
X_"a[186:187], v38 offset:1024\n" \
C_"v199, v182, v183\n" \
M_"a[32:35], a[160:163], a[152:155], a[32:35]\n" \
".fill (L_pb + 69344 - .) / 4, 4, 0xBF800000\n" \
"L_pp17:\n" \
"s_cbranch_execz L_pret\n" \
"s_addk_i32 s84, 0x2000\n" \
M_"a[32:35], a[224:227], a[216:219], a[32:35]\n" \
B_"v24, s[76:79], s84 offen offset:2048 lds\n" \
M_"a[36:39], a[164:167], a[152:155], a[36:39]\n" \
X_"a[248:249], v38 offset:8192\n" \
"v_accvgpr_read_b32 v192, a72\n" \
M_"a[36:39], a[228:231], a[216:219], a[36:39]\n" \
X_"a[250:251], v38 offset:9216\n" \
"v_accvgpr_read_b32 v193, a73\n" \
M_"a[44:47], a[172:175], a[152:155], a[44:47]\n" \
X_"a[188:189], v39 offset:0\n" \
"v_accvgpr_read_b32 v194, a74\n" \
M_"a[44:47], a[236:239], a[216:219], a[44:47]\n" \
X_"a[190:191], v39 offset:1024\n" \
"v_accvgpr_read_b32 v195, a75\n" \
M_"a[40:43], a[152:155], a[168:171], a[40:43]\n" \
"s_add_u32 s89, s42, 0x3000\n" \
"buffer_store_dwordx4 v[240:243], v20, s[80:83], s89 offen offset:3072 sc1\n" \
M_"a[40:43], a[216:219], a[232:235], a[40:43]\n" \
X_"a[252:253], v39 offset:8192\n" \
"v_accvgpr_read_b32 v180, a88\n" \
M_"a[56:59], a[156:159], a[168:171], a[56:59]\n" \
X_"a[254:255], v39 offset:9216\n" \
"v_accvgpr_read_b32 v181, a89\n" \
M_"a[56:59], a[220:223], a[232:235], a[56:59]\n" \
"v_accvgpr_read_b32 v182, a90\n" \
"v_accvgpr_read_b32 v183, a91\n" \
M_"a[60:63], a[172:175], a[156:159], a[60:63]\n" \
C_"v192, v192, v193\n" \
C_"v193, v194, v195\n" \
M_"a[60:63], a[236:239], a[220:223], a[60:63]\n" \
"s_addk_i32 s84, 0x2000\n" \
M_"a[52:55], a[164:167], a[156:159], a[52:55]\n" \
B_"v25, s[76:79], s84 offen offset:3072 lds\n" \
M_"a[52:55], a[228:231], a[220:223], a[52:55]\n" \
C_"v194, v180, v181\n" \
C_"v195, v182, v183\n" \
M_"a[48:51], a[160:163], a[156:159], a[48:51]\n" \
"s_mulk_i32 s8, 0x480\n" \
"s_or_b32 s13, s13, s11\n" \
M_"a[48:51], a[224:227], a[220:223], a[48:51]\n" \
"s_or_b32 s8, s8, s11\n" \
"s_and_b32 s11, s2, 1\n" \
M_"a[64:67], a[164:167], a[140:143], a[64:67]\n" \
"s_cmp_lg_u32 s11, 0\n" \
"s_cselect_b32 s12, s15, s12\n" \
M_"a[64:67], a[228:231], a[204:207], a[64:67]\n" \
"s_bfe_u32 s9, s2, 0x20001\n" \
"s_add_i32 s15, s7, 0x4240000\n" \
M_"a[80:83], a[172:175], a[140:143], a[80:83]\n" \
"s_addk_i32 s12, 0x4000\n" \
"s_lshl_b32 s5, s5, 16\n" \
M_"a[80:83], a[236:239], a[204:207], a[80:83]\n" \
"s_xor_b32 s9, s9, s3\n" \
M_"a[68:71], a[140:143], a[168:171], a[68:71]\n" \
"s_cmp_lg_u32 s9, 0\n" \
"s_cselect_b32 s12, s6, s12\n" \
"s_waitcnt vmcnt(11) lgkmcnt(0)\n" \
M_"a[68:71], a[204:207], a[232:235], a[68:71]\n" \
"s_barrier\n" \
"v_mov_b32_e32 v45, s62\n" \
"s_mov_b64 exec, 1\n" \
"buffer_store_dword v45, off, s[80:83], s37 offset:0 sc1\n" \
"s_mov_b64 exec, -1\n" \
"v_xor_b32_e32 v28, 0x20000, v28\n" \
"v_xor_b32_e32 v29, 0x20000, v29\n" \
M_"v[116:119], a[176:179], a[144:147], v[116:119]\n" \
"s_add_u32 m0, s40, 0x0\n" \
"s_add_u32 s85, s39, 0xc000\n" \
M_"v[116:119], a[240:243], a[208:211], v[116:119]\n" \
B_"v20, s[80:83], s85 offen sc1 lds\n" \
M_"v[120:123], a[180:183], a[144:147], v[120:123]\n" \
X_"a[160:161], v32 offset:32768\n" \
"s_add_i32 s5, s15, s5\n" \
"s_nop 0\n" \
M_"v[120:123], a[244:247], a[208:211], v[120:123]\n" \
X_"a[162:163], v32 offset:33280\n" \
"v_writelane_b32 v11, s12, 43\n" \
M_"v[124:127], a[184:187], a[144:147], v[124:127]\n" \
"s_add_u32 m0, s40, 0x1000\n" \
"s_add_u32 s85, s39, 0xd000\n" \
M_"v[124:127], a[248:251], a[208:211], v[124:127]\n" \
B_"v20, s[80:83], s85 offen sc1 lds\n" \
M_"v[128:131], a[188:191], a[144:147], v[128:131]\n" \
X_"a[224:225], v32 offset:36864\n" \
"s_add_i32 s7, s7, 0x42c0000\n" \
M_"v[128:131], a[252:255], a[208:211], v[128:131]\n" \
R_"a[144:147], v28 offset:8192\n" \
"v_writelane_b32 v11, s5, 44\n" \
M_"v[144:147], a[188:191], a[148:151], v[144:147]\n" \
R_"a[208:211], v29 offset:8192\n" \
"s_lshl_b32 s5, s11, 10\n" \
M_"v[144:147], a[252:255], a[212:215], v[144:147]\n" \
"s_add_u32 m0, s40, 0x3000\n" \
"s_movk_i32 s84, 0x300\n" \
M_"v[140:143], a[184:187], a[148:151], v[140:143]\n" \
B_"v21, s[76:79], s84 offen offset:0 lds\n" \
M_"v[140:143], a[248:251], a[212:215], v[140:143]\n" \
X_"a[226:227], v32 offset:37376\n" \
"s_or_b32 s5, s7, s5\n" \
M_"v[136:139], a[180:183], a[148:151], v[136:139]\n" \
"s_cmp_lg_u32 s9, 0\n" \
"s_cselect_b32 s5, s6, s5\n" \
M_"v[136:139], a[244:247], a[212:215], v[136:139]\n" \
X_"a[164:165], v33 offset:32768\n" \
"s_add_i32 s8, s8, 0x21c000\n" \
M_"v[132:135], a[176:179], a[148:151], v[132:135]\n" \
"v_writelane_b32 v11, s5, 45\n" \
"s_add_i32 s5, s13, s10\n" \
M_"v[132:135], a[240:243], a[212:215], v[132:135]\n" \
R_"a[148:151], v28 offset:10240\n" \
"v_writelane_b32 v11, s5, 46\n" \
M_"v[148:151], a[176:179], a[152:155], v[148:151]\n" \
R_"a[212:215], v29 offset:10240\n" \
"s_add_i32 s5, s8, s10\n" \
M_"v[148:151], a[240:243], a[216:219], v[148:151]\n" \
"s_add_u32 m0, s31, 0x0\n" \
"s_mov_b32 s84, 0x300000\n" \
M_"v[152:155], a[180:183], a[152:155], v[152:155]\n" \
B_"v26, s[68:71], s84 offen offset:0 lds\n" \
M_"v[152:155], a[244:247], a[216:219], v[152:155]\n" \
"s_addk_i32 s84, 0x7c00\n" \
"s_nop 0\n" \
M_"v[156:159], a[184:187], a[152:155], v[156:159]\n" \
B_"v26, s[68:71], s84 offen offset:1024 lds\n" \
M_"v[156:159], a[248:251], a[216:219], v[156:159]\n" \
X_"a[166:167], v33 offset:33280\n" \
"v_writelane_b32 v11, s5, 47\n" \
M_"v[160:163], a[188:191], a[152:155], v[160:163]\n" \
X_"a[228:229], v33 offset:36864\n" \
M_"v[160:163], a[252:255], a[216:219], v[160:163]\n" \
R_"a[152:155], v28 offset:12288\n" \
M_"v[176:179], a[188:191], a[156:159], v[176:179]\n" \
R_"a[216:219], v29 offset:12288\n" \
M_"v[176:179], a[252:255], a[220:223], v[176:179]\n" \
"s_addk_i32 s84, 0x7c00\n" \
M_"v[172:175], a[184:187], a[156:159], v[172:175]\n" \
B_"v27, s[68:71], s84 offen offset:2048 lds\n" \
M_"v[172:175], a[248:251], a[220:223], v[172:175]\n" \
X_"a[230:231], v33 offset:37376\n" \
M_"v[168:171], a[180:183], a[156:159], v[168:171]\n" \
"s_addk_i32 s84, 0x7c00\n" \
M_"v[168:171], a[244:247], a[220:223], v[168:171]\n" \
B_"v27, s[68:71], s84 offen offset:3072 lds\n" \
M_"v[164:167], a[176:179], a[156:159], v[164:167]\n" \
X_"a[172:173], v35 offset:32768\n" \
M_"v[164:167], a[240:243], a[220:223], v[164:167]\n" \
R_"a[156:159], v28 offset:14336\n" \
M_"v[84:87], a[176:179], a[136:139], v[84:87]\n" \
R_"a[220:223], v29 offset:14336\n" \
M_"v[84:87], a[240:243], a[200:203], v[84:87]\n" \
"s_add_u32 s88, s42, 0x3000\n" \
"buffer_store_dwordx4 v[244:247], v20, s[80:83], s88 offen offset:2048 sc1\n" \
M_"v[88:91], a[180:183], a[136:139], v[88:91]\n" \
X_"a[174:175], v35 offset:33280\n" \
M_"v[88:91], a[244:247], a[200:203], v[88:91]\n" \
X_"a[236:237], v35 offset:36864\n" \
M_"v[92:95], a[184:187], a[136:139], v[92:95]\n" \
X_"a[238:239], v35 offset:37376\n" \
M_"v[92:95], a[248:251], a[200:203], v[92:95]\n" \
"s_add_u32 m0, s30, 0x8000\n" \
"s_movk_i32 s84, 0x380\n" \
M_"v[96:99], a[188:191], a[136:139], v[96:99]\n" \
B_"v24, s[76:79], s84 offen offset:0 lds\n" \
M_"v[96:99], a[252:255], a[200:203], v[96:99]\n" \
R_"a[136:139], v28 offset:4096\n" \
M_"v[112:115], a[188:191], a[140:143], v[112:115]\n" \
R_"a[200:203], v29 offset:4096\n" \
M_"v[112:115], a[252:255], a[204:207], v[112:115]\n" \
X_"a[168:169], v34 offset:32768\n" \
M_"v[108:111], a[184:187], a[140:143], v[108:111]\n" \
"s_movk_i32 s84, 0x2380\n" \
M_"v[108:111], a[248:251], a[204:207], v[108:111]\n" \
B_"v25, s[76:79], s84 offen offset:1024 lds\n" \
M_"v[104:107], a[180:183], a[140:143], v[104:107]\n" \
X_"a[170:171], v34 offset:33280\n" \
M_"v[104:107], a[244:247], a[204:207], v[104:107]\n" \
X_"a[232:233], v34 offset:36864\n" \
M_"v[100:103], a[176:179], a[140:143], v[100:103]\n" \
"s_cmp_lg_u32 s11, 0\n" \
"s_cselect_b32 s5, s8, s13\n" \
M_"v[100:103], a[240:243], a[204:207], v[100:103]\n" \
R_"a[140:143], v28 offset:6144\n" \
"s_addk_i32 s5, 0x100\n" \
M_"v[52:55], a[176:179], a[128:131], v[52:55]\n" \
R_"a[204:207], v29 offset:6144\n" \
M_"v[52:55], a[240:243], a[192:195], v[52:55]\n" \
"s_movk_i32 s84, 0x4380\n" \
M_"v[56:59], a[180:183], a[128:131], v[56:59]\n" \
B_"v24, s[76:79], s84 offen offset:2048 lds\n" \
M_"v[56:59], a[244:247], a[192:195], v[56:59]\n" \
"s_movk_i32 s84, 0x6380\n" \
M_"v[60:63], a[184:187], a[128:131], v[60:63]\n" \
B_"v25, s[76:79], s84 offen offset:3072 lds\n" \
M_"v[60:63], a[248:251], a[192:195], v[60:63]\n" \
X_"a[234:235], v34 offset:37376\n" \
M_"v[64:67], a[188:191], a[128:131], v[64:67]\n" \
"s_cmp_lg_u32 s9, 0\n" \
"s_cselect_b32 s5, s6, s5\n" \
M_"v[64:67], a[252:255], a[192:195], v[64:67]\n" \
R_"a[128:131], v28 offset:0\n" \
"v_writelane_b32 v11, s5, 48\n" \
M_"v[80:83], a[188:191], a[132:135], v[80:83]\n" \
R_"a[192:195], v29 offset:0\n" \
"s_or_b32 s5, s7, s14\n" \
M_"v[80:83], a[252:255], a[196:199], v[80:83]\n" \
"v_writelane_b32 v11, s5, 56\n" \
"s_lshl_b32 s5, s3, 17\n" \
M_"v[76:79], a[184:187], a[132:135], v[76:79]\n" \
"s_add_i32 s5, s15, s5\n" \
"s_or_b32 s4, s5, s4\n" \
M_"v[76:79], a[248:251], a[196:199], v[76:79]\n" \
"s_or_b32 s4, s4, 0x3840\n" \
"v_writelane_b32 v11, s4, 6\n" \
M_"v[72:75], a[180:183], a[132:135], v[72:75]\n" \
"s_mov_b32 s4, 0xff0000\n" \
M_"v[72:75], a[244:247], a[196:199], v[72:75]\n" \
"s_cmp_lg_u32 s9, 0\n" \
"s_cselect_b32 s4, s16, s4\n" \
M_"v[68:71], a[176:179], a[132:135], v[68:71]\n" \
"v_writelane_b32 v11, s4, 7\n" \
"s_lshl_b32 s4, s2, 16\n" \
M_"v[68:71], a[240:243], a[196:199], v[68:71]\n" \
R_"a[132:135], v28 offset:2048\n" \
R_"a[196:199], v29 offset:2048\n" \
"s_waitcnt lgkmcnt(15)\n" \
M_"a[0:3], a[160:163], a[144:147], a[0:3]\n" \
"s_add_u32 m0, s30, 0x9000\n" \
"s_addk_i32 s84, 0x3000\n" \
M_"a[0:3], a[224:227], a[208:211], a[0:3]\n" \
B_"v24, s[76:79], s84 offen offset:0 lds\n" \
M_"a[4:7], a[164:167], a[144:147], a[4:7]\n" \
X_"a[176:177], v36 offset:32768\n" \
"s_add_i32 s5, s4, 0x10000\n" \
M_"a[4:7], a[228:231], a[208:211], a[4:7]\n" \
X_"a[178:179], v36 offset:33792\n" \
"s_and_b32 s5, s5, 0x70000\n" \
"s_waitcnt lgkmcnt(15)\n" \
M_"a[12:15], a[172:175], a[144:147], a[12:15]\n" \
X_"a[240:241], v36 offset:40960\n" \
"s_add_i32 s5, s15, s5\n" \
"s_waitcnt lgkmcnt(15)\n" \
M_"a[12:15], a[236:239], a[208:211], a[12:15]\n" \
"s_add_u32 s89, s42, 0x3000\n" \
"buffer_store_dwordx4 v[184:187], v20, s[80:83], s89 offen offset:1024 sc1\n" \
"s_waitcnt lgkmcnt(11)\n" \
M_"a[8:11], a[144:147], a[168:171], a[8:11]\n" \
X_"a[242:243], v36 offset:41984\n" \
"v_writelane_b32 v11, s5, 49\n" \
"s_waitcnt lgkmcnt(8)\n" \
M_"a[8:11], a[208:211], a[232:235], a[8:11]\n" \
X_"a[180:181], v37 offset:32768\n" \
"s_add_i32 s5, s4, 0x20000\n" \
M_"a[24:27], a[148:151], a[168:171], a[24:27]\n" \
X_"a[182:183], v37 offset:33792\n" \
"s_and_b32 s5, s5, 0x70000\n" \
M_"a[24:27], a[212:215], a[232:235], a[24:27]\n" \
X_"a[244:245], v37 offset:40960\n" \
"s_add_i32 s5, s15, s5\n" \
M_"a[28:31], a[172:175], a[148:151], a[28:31]\n" \
"s_bitset1_b32 s5, 11\n" \
"v_writelane_b32 v11, s5, 50\n" \
M_"a[28:31], a[236:239], a[212:215], a[28:31]\n" \
"s_addk_i32 s84, 0x2000\n" \
M_"a[20:23], a[164:167], a[148:151], a[20:23]\n" \
B_"v25, s[76:79], s84 offen offset:1024 lds\n" \
M_"a[20:23], a[228:231], a[212:215], a[20:23]\n" \
X_"a[246:247], v37 offset:41984\n" \
"s_add_i32 s5, s4, 0x30000\n" \
M_"a[16:19], a[160:163], a[148:151], a[16:19]\n" \
X_"a[184:185], v38 offset:32768\n" \
"s_and_b32 s5, s5, 0x70000\n" \
M_"a[16:19], a[224:227], a[212:215], a[16:19]\n" \
X_"a[186:187], v38 offset:33792\n" \
"s_add_i32 s5, s15, s5\n" \
M_"a[32:35], a[160:163], a[152:155], a[32:35]\n" \
"s_addk_i32 s84, 0x2000\n" \
M_"a[32:35], a[224:227], a[216:219], a[32:35]\n" \
B_"v24, s[76:79], s84 offen offset:2048 lds\n" \
M_"a[36:39], a[164:167], a[152:155], a[36:39]\n" \
X_"a[248:249], v38 offset:40960\n" \
"s_bitset1_b32 s5, 12\n" \
"s_nop 0\n" \
M_"a[36:39], a[228:231], a[216:219], a[36:39]\n" \
X_"a[250:251], v38 offset:41984\n" \
"v_writelane_b32 v11, s5, 51\n" \
M_"a[44:47], a[172:175], a[152:155], a[44:47]\n" \
X_"a[188:189], v39 offset:32768\n" \
"s_and_b32 s5, s4, 0x70000\n" \
M_"a[44:47], a[236:239], a[216:219], a[44:47]\n" \
X_"a[190:191], v39 offset:33792\n" \
"s_xor_b32 s5, s5, 0x40000\n" \
M_"a[40:43], a[152:155], a[168:171], a[40:43]\n" \
"s_add_u32 s88, s42, 0x3000\n" \
"buffer_store_dwordx4 v[188:191], v20, s[80:83], s88 offen offset:0 sc1\n" \
M_"a[40:43], a[216:219], a[232:235], a[40:43]\n" \
X_"a[252:253], v39 offset:40960\n" \
"s_add_i32 s5, s15, s5\n" \
M_"a[56:59], a[156:159], a[168:171], a[56:59]\n" \
X_"a[254:255], v39 offset:41984\n" \
"s_or_b32 s5, s5, 0x1800\n" \
M_"a[56:59], a[220:223], a[232:235], a[56:59]\n" \
"v_writelane_b32 v11, s5, 52\n" \
"s_add_i32 s5, s4, 0x50000\n" \
M_"a[60:63], a[172:175], a[156:159], a[60:63]\n" \
"s_and_b32 s5, s5, 0x70000\n" \
"s_add_i32 s5, s15, s5\n" \
M_"a[60:63], a[236:239], a[220:223], a[60:63]\n" \
"s_addk_i32 s84, 0x2000\n" \
M_"a[52:55], a[164:167], a[156:159], a[52:55]\n" \
B_"v25, s[76:79], s84 offen offset:3072 lds\n" \
M_"a[52:55], a[228:231], a[220:223], a[52:55]\n" \
"s_bitset1_b32 s5, 13\n" \
"v_writelane_b32 v11, s5, 53\n" \
M_"a[48:51], a[160:163], a[156:159], a[48:51]\n" \
"s_add_i32 s5, s4, 0x60000\n" \
"s_and_b32 s5, s5, 0x70000\n" \
M_"a[48:51], a[224:227], a[220:223], a[48:51]\n" \
"s_add_i32 s4, s4, 0x70000\n" \
"s_add_i32 s5, s15, s5\n" \
M_"a[64:67], a[164:167], a[140:143], a[64:67]\n" \
"s_and_b32 s4, s4, 0x70000\n" \
"s_or_b32 s5, s5, 0x2800\n" \
M_"a[64:67], a[228:231], a[204:207], a[64:67]\n" \
"v_writelane_b32 v11, s5, 54\n" \
"s_add_i32 s15, s15, s4\n" \
M_"a[80:83], a[172:175], a[140:143], a[80:83]\n" \
"s_or_b32 s4, s15, 0x3000\n" \
"v_writelane_b32 v11, s4, 55\n" \
M_"a[80:83], a[236:239], a[204:207], a[80:83]\n" \
"s_movk_i32 s11, 0x4000\n" \
"s_lshl_b32 s4, s2, 2\n" \
M_"a[68:71], a[140:143], a[168:171], a[68:71]\n" \
"s_and_b32 s4, s4, 28\n" \
"s_bfe_u32 s5, s2, 0x20003\n" \
"s_waitcnt vmcnt(11) lgkmcnt(0)\n" \
M_"a[68:71], a[204:207], a[232:235], a[68:71]\n" \
"s_barrier\n" \
"v_xor_b32_e32 v28, 0x20000, v28\n" \
"v_xor_b32_e32 v29, 0x20000, v29\n" \
M_"v[116:119], a[176:179], a[144:147], v[116:119]\n" \
"s_add_u32 m0, s40, 0x8000\n" \
"s_add_u32 s85, s39, 0xe000\n" \
M_"v[116:119], a[240:243], a[208:211], v[116:119]\n" \
B_"v20, s[80:83], s85 offen sc1 lds\n" \
M_"v[120:123], a[180:183], a[144:147], v[120:123]\n" \
X_"a[160:161], v32 offset:0\n" \
"s_or_b32 s4, s4, s5\n" \
M_"v[120:123], a[244:247], a[208:211], v[120:123]\n" \
X_"a[162:163], v32 offset:512\n" \
"s_lshr_b32 s5, s2, 5\n" \
M_"v[124:127], a[184:187], a[144:147], v[124:127]\n" \
"s_add_u32 m0, s40, 0x9000\n" \
"s_add_u32 s85, s39, 0xf000\n" \
M_"v[124:127], a[248:251], a[208:211], v[124:127]\n" \
B_"v20, s[80:83], s85 offen sc1 lds\n" \
M_"v[128:131], a[188:191], a[144:147], v[128:131]\n" \
X_"a[224:225], v32 offset:4096\n" \
"s_lshr_b32 s19, s3, 1\n" \
M_"v[128:131], a[252:255], a[208:211], v[128:131]\n" \
R_"a[144:147], v28 offset:8192\n" \
"s_lshl_b32 s18, s5, 1\n" \
M_"v[144:147], a[188:191], a[148:151], v[144:147]\n" \
R_"a[208:211], v29 offset:8192\n" \
"s_lshl_b32 s6, s4, 20\n" \
M_"v[144:147], a[252:255], a[212:215], v[144:147]\n" \
"s_add_u32 m0, s40, 0xb000\n" \
"s_movk_i32 s84, 0x380\n" \
M_"v[140:143], a[184:187], a[148:151], v[140:143]\n" \
B_"v21, s[76:79], s84 offen offset:0 lds\n" \
M_"v[140:143], a[248:251], a[212:215], v[140:143]\n" \
X_"a[226:227], v32 offset:4608\n" \
"s_lshl_b32 s7, s4, 8\n" \
M_"v[136:139], a[180:183], a[148:151], v[136:139]\n" \
"s_and_b32 s13, s3, 1\n" \
"s_add_i32 s14, s18, s19\n" \
M_"v[136:139], a[244:247], a[212:215], v[136:139]\n" \
X_"a[164:165], v33 offset:0\n" \
M_"v[132:135], a[176:179], a[148:151], v[132:135]\n" \
"s_add_u32 s6, s26, s6\n" \
"s_addc_u32 s9, s27, 0\n" \
M_"v[132:135], a[240:243], a[212:215], v[132:135]\n" \
R_"a[148:151], v28 offset:10240\n" \
M_"v[148:151], a[176:179], a[152:155], v[148:151]\n" \
R_"a[212:215], v29 offset:10240\n" \
M_"v[148:151], a[240:243], a[216:219], v[148:151]\n" \
"s_add_u32 m0, s31, 0x8000\n" \
"s_mov_b32 s84, 0x380000\n" \
M_"v[152:155], a[180:183], a[152:155], v[152:155]\n" \
B_"v26, s[68:71], s84 offen offset:0 lds\n" \
M_"v[152:155], a[244:247], a[216:219], v[152:155]\n" \
"s_addk_i32 s84, 0x7c00\n" \
"s_nop 0\n" \
M_"v[156:159], a[184:187], a[152:155], v[156:159]\n" \
B_"v26, s[68:71], s84 offen offset:1024 lds\n" \
M_"v[156:159], a[248:251], a[216:219], v[156:159]\n" \
X_"a[166:167], v33 offset:512\n" \
M_"v[160:163], a[188:191], a[152:155], v[160:163]\n" \
X_"a[228:229], v33 offset:4096\n" \
M_"v[160:163], a[252:255], a[216:219], v[160:163]\n" \
R_"a[152:155], v28 offset:12288\n" \
M_"v[176:179], a[188:191], a[156:159], v[176:179]\n" \
R_"a[216:219], v29 offset:12288\n" \
M_"v[176:179], a[252:255], a[220:223], v[176:179]\n" \
"s_addk_i32 s84, 0x7c00\n" \
M_"v[172:175], a[184:187], a[156:159], v[172:175]\n" \
B_"v27, s[68:71], s84 offen offset:2048 lds\n" \
M_"v[172:175], a[248:251], a[220:223], v[172:175]\n" \
X_"a[230:231], v33 offset:4608\n" \
M_"v[168:171], a[180:183], a[156:159], v[168:171]\n" \
"s_addk_i32 s84, 0x7c00\n" \
M_"v[168:171], a[244:247], a[220:223], v[168:171]\n" \
B_"v27, s[68:71], s84 offen offset:3072 lds\n" \
M_"v[164:167], a[176:179], a[156:159], v[164:167]\n" \
X_"a[172:173], v35 offset:0\n" \
M_"v[164:167], a[240:243], a[220:223], v[164:167]\n" \
R_"a[156:159], v28 offset:14336\n" \
M_"v[84:87], a[176:179], a[136:139], v[84:87]\n" \
R_"a[220:223], v29 offset:14336\n" \
M_"v[84:87], a[240:243], a[200:203], v[84:87]\n" \
"s_add_u32 s89, s42, 0x0\n" \
"buffer_store_dwordx4 v[192:195], v20, s[80:83], s89 offen offset:2048 sc1\n" \
M_"v[88:91], a[180:183], a[136:139], v[88:91]\n" \
X_"a[174:175], v35 offset:512\n" \
M_"v[88:91], a[244:247], a[200:203], v[88:91]\n" \
X_"a[236:237], v35 offset:4096\n" \
M_"v[92:95], a[184:187], a[136:139], v[92:95]\n" \
X_"a[238:239], v35 offset:4608\n" \
M_"v[92:95], a[248:251], a[200:203], v[92:95]\n" \
"s_add_u32 m0, s30, 0x20000\n" \
"s_movk_i32 s84, 0x400\n" \
M_"v[96:99], a[188:191], a[136:139], v[96:99]\n" \
B_"v24, s[76:79], s84 offen offset:0 lds\n" \
M_"v[96:99], a[252:255], a[200:203], v[96:99]\n" \
R_"a[136:139], v28 offset:4096\n" \
M_"v[112:115], a[188:191], a[140:143], v[112:115]\n" \
R_"a[200:203], v29 offset:4096\n" \
M_"v[112:115], a[252:255], a[204:207], v[112:115]\n" \
X_"a[168:169], v34 offset:0\n" \
M_"v[108:111], a[184:187], a[140:143], v[108:111]\n" \
"s_movk_i32 s84, 0x2400\n" \
M_"v[108:111], a[248:251], a[204:207], v[108:111]\n" \
B_"v25, s[76:79], s84 offen offset:1024 lds\n" \
M_"v[104:107], a[180:183], a[140:143], v[104:107]\n" \
X_"a[170:171], v34 offset:512\n" \
M_"v[104:107], a[244:247], a[204:207], v[104:107]\n" \
X_"a[232:233], v34 offset:4096\n" \
M_"v[100:103], a[176:179], a[140:143], v[100:103]\n" \
"s_add_u32 s8, s6, 0x1000000\n" \
"s_addc_u32 s6, s9, 0\n" \
M_"v[100:103], a[240:243], a[204:207], v[100:103]\n" \
R_"a[140:143], v28 offset:6144\n" \
"s_and_b32 s9, s6, 0xffff\n" \
M_"v[52:55], a[176:179], a[128:131], v[52:55]\n" \
R_"a[204:207], v29 offset:6144\n" \
"s_lshl_b32 s6, s5, 16\n" \
M_"v[52:55], a[240:243], a[192:195], v[52:55]\n" \
"s_movk_i32 s84, 0x4400\n" \
M_"v[56:59], a[180:183], a[128:131], v[56:59]\n" \
B_"v24, s[76:79], s84 offen offset:2048 lds\n" \
M_"v[56:59], a[244:247], a[192:195], v[56:59]\n" \
"s_movk_i32 s84, 0x6400\n" \
"s_nop 0\n" \
M_"v[60:63], a[184:187], a[128:131], v[60:63]\n" \
B_"v25, s[76:79], s84 offen offset:3072 lds\n" \
M_"v[60:63], a[248:251], a[192:195], v[60:63]\n" \
X_"a[234:235], v34 offset:4608\n" \
"s_sub_i32 s10, 0x100000, s6\n" \
M_"v[64:67], a[188:191], a[128:131], v[64:67]\n" \
"s_mov_b32 s6, 0x8000\n" \
M_"v[64:67], a[252:255], a[192:195], v[64:67]\n" \
R_"a[128:131], v28 offset:0\n" \
M_"v[80:83], a[188:191], a[132:135], v[80:83]\n" \
R_"a[192:195], v29 offset:0\n" \
M_"v[80:83], a[252:255], a[196:199], v[80:83]\n" \
"s_cmp_lg_u32 s5, 0\n" \
"s_cselect_b32 s17, s11, s6\n" \
M_"v[76:79], a[184:187], a[132:135], v[76:79]\n" \
"s_lshl_b32 s6, s3, 1\n" \
"s_lshl_b32 s5, s5, 3\n" \
M_"v[76:79], a[248:251], a[196:199], v[76:79]\n" \
"s_and_b32 s6, s6, 4\n" \
"s_or_b32 s12, s5, s6\n" \
M_"v[72:75], a[180:183], a[132:135], v[72:75]\n" \
"s_sub_i32 s5, s13, s5\n" \
"s_or_b32 s5, s5, s6\n" \
M_"v[72:75], a[244:247], a[196:199], v[72:75]\n" \
"s_or_b32 s6, s12, s13\n" \
"s_lshl_b32 s4, s4, 2\n" \
M_"v[68:71], a[176:179], a[132:135], v[68:71]\n" \
"s_mulk_i32 s6, 0x92\n" \
"s_add_i32 s4, s4, 18\n" \
M_"v[68:71], a[240:243], a[196:199], v[68:71]\n" \
R_"a[132:135], v28 offset:2048\n" \
R_"a[196:199], v29 offset:2048\n" \
"s_waitcnt lgkmcnt(15)\n" \
M_"a[0:3], a[160:163], a[144:147], a[0:3]\n" \
"s_add_u32 m0, s30, 0x21000\n" \
"s_addk_i32 s84, 0x3000\n" \
M_"a[0:3], a[224:227], a[208:211], a[0:3]\n" \
B_"v24, s[76:79], s84 offen offset:0 lds\n" \
M_"a[4:7], a[164:167], a[144:147], a[4:7]\n" \
X_"a[176:177], v36 offset:0\n" \
"s_mulk_i32 s5, 0x92\n" \
M_"a[4:7], a[228:231], a[208:211], a[4:7]\n" \
X_"a[178:179], v36 offset:1024\n" \
"s_add_i32 s6, s4, s6\n" \
"s_waitcnt lgkmcnt(15)\n" \
M_"a[12:15], a[172:175], a[144:147], a[12:15]\n" \
X_"a[240:241], v36 offset:8192\n" \
"s_add_i32 s5, s5, s4\n" \
"s_waitcnt lgkmcnt(15)\n" \
M_"a[12:15], a[236:239], a[208:211], a[12:15]\n" \
"s_add_u32 s88, s42, 0x0\n" \
"buffer_store_dwordx4 v[196:199], v20, s[80:83], s88 offen offset:3072 sc1\n" \
"s_waitcnt lgkmcnt(11)\n" \
M_"a[8:11], a[144:147], a[168:171], a[8:11]\n" \
X_"a[242:243], v36 offset:9216\n" \
"s_lshl_b32 s4, s3, 11\n" \
"s_waitcnt lgkmcnt(8)\n" \
M_"a[8:11], a[208:211], a[232:235], a[8:11]\n" \
X_"a[180:181], v37 offset:0\n" \
"s_lshl_b32 s15, s13, 12\n" \
M_"a[24:27], a[148:151], a[168:171], a[24:27]\n" \
X_"a[182:183], v37 offset:1024\n" \
"s_lshl_b32 s16, s19, 14\n" \
M_"a[24:27], a[212:215], a[232:235], a[24:27]\n" \
X_"a[244:245], v37 offset:8192\n" \
"s_mul_i32 s19, s19, 0xe000\n" \
M_"a[28:31], a[172:175], a[148:151], a[28:31]\n" \
"s_mov_b32 s11, 0x20000\n" \
"s_or_b32 s12, s4, 0xc000\n" \
M_"a[28:31], a[236:239], a[212:215], a[28:31]\n" \
"s_addk_i32 s84, 0x2000\n" \
M_"a[20:23], a[164:167], a[148:151], a[20:23]\n" \
B_"v25, s[76:79], s84 offen offset:1024 lds\n" \
M_"a[20:23], a[228:231], a[212:215], a[20:23]\n" \
X_"a[246:247], v37 offset:9216\n" \
"s_lshl_b32 s4, s3, 8\n" \
M_"a[16:19], a[160:163], a[148:151], a[16:19]\n" \
X_"a[184:185], v38 offset:0\n" \
"s_or_b32 s22, s15, s16\n" \
M_"a[16:19], a[224:227], a[212:215], a[16:19]\n" \
X_"a[186:187], v38 offset:1024\n" \
"s_or_b32 s23, s19, s15\n" \
M_"a[32:35], a[160:163], a[152:155], a[32:35]\n" \
"s_addk_i32 s84, 0x2000\n" \
M_"a[32:35], a[224:227], a[216:219], a[32:35]\n" \
B_"v24, s[76:79], s84 offen offset:2048 lds\n" \
M_"a[36:39], a[164:167], a[152:155], a[36:39]\n" \
X_"a[248:249], v38 offset:8192\n" \
"s_lshl_b32 s60, s6, 10\n" \
M_"a[36:39], a[228:231], a[216:219], a[36:39]\n" \
X_"a[250:251], v38 offset:9216\n" \
"s_lshl_b32 s61, s5, 10\n" \
M_"a[44:47], a[172:175], a[152:155], a[44:47]\n" \
X_"a[188:189], v39 offset:0\n" \
"s_lshl_b32 s14, s14, 15\n" \
"s_nop 0\n" \
M_"a[44:47], a[236:239], a[216:219], a[44:47]\n" \
X_"a[190:191], v39 offset:1024\n" \
"s_or_b32 s20, s4, 0x26c00\n" \
M_"a[40:43], a[152:155], a[168:171], a[40:43]\n" \
"s_add_u32 s89, s42, 0x2000\n" \
"buffer_store_dwordx4 v[200:203], v20, s[80:83], s89 offen offset:0 sc1\n" \
M_"a[40:43], a[216:219], a[232:235], a[40:43]\n" \
X_"a[252:253], v39 offset:8192\n" \
"s_or_b32 s13, s22, 0x8000\n" \
M_"a[56:59], a[156:159], a[168:171], a[56:59]\n" \
X_"a[254:255], v39 offset:9216\n" \
"s_add_i32 s22, s22, 0x14000\n" \
M_"a[56:59], a[220:223], a[232:235], a[56:59]\n" \
"s_bitset1_b32 s23, 16\n" \
"s_or_b32 s24, s7, 0x5be8020\n" \
M_"a[60:63], a[172:175], a[156:159], a[60:63]\n" \
"s_and_b32 s57, s27, 0xffff\n" \
"s_mov_b32 s58, 0x6000000\n" \
".fill (L_pb + 73440 - .) / 4, 4, 0xBF800000\n" \
"L_pp18:\n" \
"s_cbranch_execz L_pret\n" \
M_"a[60:63], a[236:239], a[220:223], a[60:63]\n" \
"s_addk_i32 s84, 0x2000\n" \
M_"a[52:55], a[164:167], a[156:159], a[52:55]\n" \
B_"v25, s[76:79], s84 offen offset:3072 lds\n" \
M_"a[52:55], a[228:231], a[220:223], a[52:55]\n" \
"s_mov_b32 s56, s26\n" \
"s_mov_b32 s59, s11\n" \
M_"a[48:51], a[160:163], a[156:159], a[48:51]\n" \
"s_add_i32 s60, s60, 0x3000000\n" \
"s_add_i32 s61, s61, 0x411c000\n" \
M_"a[48:51], a[224:227], a[220:223], a[48:51]\n" \
"s_sub_i32 s19, 14, s18\n" \
"s_mov_b32 s21, 0x71800000\n" \
M_"a[64:67], a[164:167], a[140:143], a[64:67]\n" \
"s_mov_b32 s4, L_gsw-L_gtr\n" \
M_"a[64:67], a[228:231], a[204:207], a[64:67]\n" \
"s_cmp_eq_u32 s19, 0\n" \
"s_cselect_b32 s4, L_gs7-L_gtr, s4\n" \
M_"a[80:83], a[172:175], a[140:143], a[80:83]\n" \
"s_cmp_eq_u32 s16, 0\n" \
"s_cselect_b32 s4, 0, s4\n" \
M_"a[80:83], a[236:239], a[204:207], a[80:83]\n" \
"s_getpc_b64 s[28:29]\n" \
"L_gp1_%=:\n" \
"s_add_u32 s28, s28, L_gtr-L_gp1_%=\n" \
"s_addc_u32 s29, s29, 0\n" \
M_"a[68:71], a[140:143], a[168:171], a[68:71]\n" \
"s_add_u32 s28, s28, s4\n" \
"s_addc_u32 s29, s29, 0\n" \
"s_waitcnt vmcnt(11) lgkmcnt(0)\n" \
M_"a[68:71], a[204:207], a[232:235], a[68:71]\n" \
"s_barrier\n" \
M_"v[116:119], a[176:179], a[144:147], v[116:119]\n" \
"s_add_u32 m0, s40, 0x0\n" \
"s_nop 0\n" \
"s_add_u32 s85, s39, 0x10000\n" \
M_"v[116:119], a[240:243], a[208:211], v[116:119]\n" \
B_"v20, s[80:83], s85 offen sc1 lds\n" \
M_"v[120:123], a[180:183], a[144:147], v[120:123]\n" \
X_"a[160:161], v32 offset:32768\n" \
M_"v[120:123], a[244:247], a[208:211], v[120:123]\n" \
X_"a[162:163], v32 offset:33280\n" \
M_"v[124:127], a[184:187], a[144:147], v[124:127]\n" \
M_"v[124:127], a[248:251], a[208:211], v[124:127]\n" \
M_"v[128:131], a[188:191], a[144:147], v[128:131]\n" \
X_"a[224:225], v32 offset:36864\n" \
M_"v[128:131], a[252:255], a[208:211], v[128:131]\n" \
R_"a[144:147], v28 offset:40960\n" \
M_"v[144:147], a[188:191], a[148:151], v[144:147]\n" \
R_"a[208:211], v29 offset:40960\n" \
M_"v[144:147], a[252:255], a[212:215], v[144:147]\n" \
"s_add_u32 m0, s40, 0x1000\n" \
"s_add_u32 s85, s39, 0x11000\n" \
M_"v[140:143], a[184:187], a[148:151], v[140:143]\n" \
B_"v20, s[80:83], s85 offen sc1 lds\n" \
M_"v[140:143], a[248:251], a[212:215], v[140:143]\n" \
X_"a[226:227], v32 offset:37376\n" \
M_"v[136:139], a[180:183], a[148:151], v[136:139]\n" \
M_"v[136:139], a[244:247], a[212:215], v[136:139]\n" \
X_"a[164:165], v33 offset:32768\n" \
M_"v[132:135], a[176:179], a[148:151], v[132:135]\n" \
M_"v[132:135], a[240:243], a[212:215], v[132:135]\n" \
R_"a[148:151], v28 offset:43008\n" \
M_"v[148:151], a[176:179], a[152:155], v[148:151]\n" \
R_"a[212:215], v29 offset:43008\n" \
M_"v[148:151], a[240:243], a[216:219], v[148:151]\n" \
"s_add_u32 m0, s40, 0x3000\n" \
"s_movk_i32 s84, 0x400\n" \
M_"v[152:155], a[180:183], a[152:155], v[152:155]\n" \
B_"v21, s[76:79], s84 offen offset:0 lds\n" \
M_"v[152:155], a[244:247], a[216:219], v[152:155]\n" \
X_"a[166:167], v33 offset:33280\n" \
M_"v[156:159], a[184:187], a[152:155], v[156:159]\n" \
X_"a[228:229], v33 offset:36864\n" \
M_"v[156:159], a[248:251], a[216:219], v[156:159]\n" \
"s_add_u32 m0, s31, 0x0\n" \
"s_mov_b32 s84, 0x400000\n" \
M_"v[160:163], a[188:191], a[152:155], v[160:163]\n" \
B_"v26, s[68:71], s84 offen offset:0 lds\n" \
M_"v[160:163], a[252:255], a[216:219], v[160:163]\n" \
R_"a[152:155], v28 offset:45056\n" \
M_"v[176:179], a[188:191], a[156:159], v[176:179]\n" \
R_"a[216:219], v29 offset:45056\n" \
M_"v[176:179], a[252:255], a[220:223], v[176:179]\n" \
X_"a[230:231], v33 offset:37376\n" \
M_"v[172:175], a[184:187], a[156:159], v[172:175]\n" \
X_"a[172:173], v35 offset:32768\n" \
M_"v[172:175], a[248:251], a[220:223], v[172:175]\n" \
M_"v[168:171], a[180:183], a[156:159], v[168:171]\n" \
"s_add_u32 s88, s42, 0x2000\n" \
"buffer_store_dwordx4 v[204:207], v20, s[80:83], s88 offen offset:1024 sc1\n" \
M_"v[168:171], a[244:247], a[220:223], v[168:171]\n" \
X_"a[174:175], v35 offset:33280\n" \
M_"v[164:167], a[176:179], a[156:159], v[164:167]\n" \
M_"v[164:167], a[240:243], a[220:223], v[164:167]\n" \
R_"a[156:159], v28 offset:47104\n" \
M_"v[84:87], a[176:179], a[136:139], v[84:87]\n" \
R_"a[220:223], v29 offset:47104\n" \
M_"v[84:87], a[240:243], a[200:203], v[84:87]\n" \
X_"a[236:237], v35 offset:36864\n" \
M_"v[88:91], a[180:183], a[136:139], v[88:91]\n" \
M_"v[88:91], a[244:247], a[200:203], v[88:91]\n" \
"s_addk_i32 s84, 0x7c00\n" \
"s_nop 0\n" \
M_"v[92:95], a[184:187], a[136:139], v[92:95]\n" \
B_"v26, s[68:71], s84 offen offset:1024 lds\n" \
M_"v[92:95], a[248:251], a[200:203], v[92:95]\n" \
X_"a[238:239], v35 offset:37376\n" \
M_"v[96:99], a[188:191], a[136:139], v[96:99]\n" \
M_"v[96:99], a[252:255], a[200:203], v[96:99]\n" \
R_"a[136:139], v28 offset:36864\n" \
M_"v[112:115], a[188:191], a[140:143], v[112:115]\n" \
R_"a[200:203], v29 offset:36864\n" \
M_"v[112:115], a[252:255], a[204:207], v[112:115]\n" \
X_"a[168:169], v34 offset:32768\n" \
M_"v[108:111], a[184:187], a[140:143], v[108:111]\n" \
"s_addk_i32 s84, 0x7c00\n" \
"s_nop 0\n" \
M_"v[108:111], a[248:251], a[204:207], v[108:111]\n" \
B_"v27, s[68:71], s84 offen offset:2048 lds\n" \
M_"v[104:107], a[180:183], a[140:143], v[104:107]\n" \
X_"a[170:171], v34 offset:33280\n" \
M_"v[104:107], a[244:247], a[204:207], v[104:107]\n" \
X_"a[232:233], v34 offset:36864\n" \
M_"v[100:103], a[176:179], a[140:143], v[100:103]\n" \
M_"v[100:103], a[240:243], a[204:207], v[100:103]\n" \
R_"a[140:143], v28 offset:38912\n" \
M_"v[52:55], a[176:179], a[128:131], v[52:55]\n" \
R_"a[204:207], v29 offset:38912\n" \
M_"v[52:55], a[240:243], a[192:195], v[52:55]\n" \
"s_addk_i32 s84, 0x7c00\n" \
"s_nop 0\n" \
M_"v[56:59], a[180:183], a[128:131], v[56:59]\n" \
B_"v27, s[68:71], s84 offen offset:3072 lds\n" \
M_"v[56:59], a[244:247], a[192:195], v[56:59]\n" \
X_"a[234:235], v34 offset:37376\n" \
M_"v[60:63], a[184:187], a[128:131], v[60:63]\n" \
M_"v[60:63], a[248:251], a[192:195], v[60:63]\n" \
M_"v[64:67], a[188:191], a[128:131], v[64:67]\n" \
M_"v[64:67], a[252:255], a[192:195], v[64:67]\n" \
R_"a[128:131], v28 offset:32768\n" \
M_"v[80:83], a[188:191], a[132:135], v[80:83]\n" \
R_"a[192:195], v29 offset:32768\n" \
M_"v[80:83], a[252:255], a[196:199], v[80:83]\n" \
M_"v[76:79], a[184:187], a[132:135], v[76:79]\n" \
M_"v[76:79], a[248:251], a[196:199], v[76:79]\n" \
M_"v[72:75], a[180:183], a[132:135], v[72:75]\n" \
M_"v[72:75], a[244:247], a[196:199], v[72:75]\n" \
M_"v[68:71], a[176:179], a[132:135], v[68:71]\n" \
M_"v[68:71], a[240:243], a[196:199], v[68:71]\n" \
R_"a[132:135], v28 offset:34816\n" \
R_"a[196:199], v29 offset:34816\n" \
"s_waitcnt lgkmcnt(15)\n" \
"s_nop 0\n" \
M_"a[0:3], a[160:163], a[144:147], a[0:3]\n" \
"s_add_u32 s89, s42, 0x2000\n" \
"buffer_store_dwordx4 v[208:211], v20, s[80:83], s89 offen offset:2048 sc1\n" \
M_"a[0:3], a[224:227], a[208:211], a[0:3]\n" \
X_"a[176:177], v36 offset:32768\n" \
M_"a[4:7], a[164:167], a[144:147], a[4:7]\n" \
X_"a[178:179], v36 offset:33792\n" \
M_"a[4:7], a[228:231], a[208:211], a[4:7]\n" \
X_"a[240:241], v36 offset:40960\n" \
M_"a[12:15], a[172:175], a[144:147], a[12:15]\n" \
X_"a[242:243], v36 offset:41984\n" \
"s_waitcnt lgkmcnt(15)\n" \
M_"a[12:15], a[236:239], a[208:211], a[12:15]\n" \
"s_waitcnt lgkmcnt(12)\n" \
M_"a[8:11], a[144:147], a[168:171], a[8:11]\n" \
X_"a[180:181], v37 offset:32768\n" \
"s_waitcnt lgkmcnt(9)\n" \
"s_nop 0\n" \
M_"a[8:11], a[208:211], a[232:235], a[8:11]\n" \
M_"a[24:27], a[148:151], a[168:171], a[24:27]\n" \
X_"a[182:183], v37 offset:33792\n" \
M_"a[24:27], a[212:215], a[232:235], a[24:27]\n" \
X_"a[244:245], v37 offset:40960\n" \
M_"a[28:31], a[172:175], a[148:151], a[28:31]\n" \
M_"a[28:31], a[236:239], a[212:215], a[28:31]\n" \
X_"a[246:247], v37 offset:41984\n" \
M_"a[20:23], a[164:167], a[148:151], a[20:23]\n" \
M_"a[20:23], a[228:231], a[212:215], a[20:23]\n" \
X_"a[184:185], v38 offset:32768\n" \
M_"a[16:19], a[160:163], a[148:151], a[16:19]\n" \
X_"a[186:187], v38 offset:33792\n" \
M_"a[16:19], a[224:227], a[212:215], a[16:19]\n" \
M_"a[32:35], a[160:163], a[152:155], a[32:35]\n" \
"s_add_u32 s88, s42, 0x2000\n" \
"buffer_store_dwordx4 v[212:215], v20, s[80:83], s88 offen offset:3072 sc1\n" \
M_"a[32:35], a[224:227], a[216:219], a[32:35]\n" \
X_"a[248:249], v38 offset:40960\n" \
M_"a[36:39], a[164:167], a[152:155], a[36:39]\n" \
"v_lshlrev_b32_e32 v45, 3, v20\n" \
"v_add_u32_e32 v45, s30, v45\n" \
M_"a[36:39], a[228:231], a[216:219], a[36:39]\n" \
"s_getpc_b64 s[86:87]\n" \
"L_ct1_%=:\n" \
"s_add_u32 s86, s86, L_ap0-L_ct1_%=\n" \
"s_addc_u32 s87, s87, 0\n" \
"s_add_u32 m0, s40, 0x2000\n" \
M_"a[44:47], a[172:175], a[152:155], a[44:47]\n" \
"global_load_lds_dword v45, s[86:87]\n" \
M_"a[44:47], a[236:239], a[216:219], a[44:47]\n" \
X_"a[250:251], v38 offset:41984\n" \
M_"a[40:43], a[152:155], a[168:171], a[40:43]\n" \
X_"a[188:189], v39 offset:32768\n" \
M_"a[40:43], a[216:219], a[232:235], a[40:43]\n" \
X_"a[190:191], v39 offset:33792\n" \
M_"a[56:59], a[156:159], a[168:171], a[56:59]\n" \
X_"a[252:253], v39 offset:40960\n" \
M_"a[56:59], a[220:223], a[232:235], a[56:59]\n" \
X_"a[254:255], v39 offset:41984\n" \
M_"a[60:63], a[172:175], a[156:159], a[60:63]\n" \
M_"a[60:63], a[236:239], a[220:223], a[60:63]\n" \
M_"a[52:55], a[164:167], a[156:159], a[52:55]\n" \
M_"a[52:55], a[228:231], a[220:223], a[52:55]\n" \
M_"a[48:51], a[160:163], a[156:159], a[48:51]\n" \
M_"a[48:51], a[224:227], a[220:223], a[48:51]\n" \
M_"a[64:67], a[164:167], a[140:143], a[64:67]\n" \
M_"a[64:67], a[228:231], a[204:207], a[64:67]\n" \
M_"a[80:83], a[172:175], a[140:143], a[80:83]\n" \
M_"a[80:83], a[236:239], a[204:207], a[80:83]\n" \
M_"a[68:71], a[140:143], a[168:171], a[68:71]\n" \
"s_waitcnt vmcnt(3) lgkmcnt(0)\n" \
M_"a[68:71], a[204:207], a[232:235], a[68:71]\n" \
"s_barrier\n" \
"v_xor_b32_e32 v28, 0x20000, v28\n" \
"v_xor_b32_e32 v29, 0x20000, v29\n" \
M_"v[116:119], a[176:179], a[144:147], v[116:119]\n" \
X_"a[160:161], v32 offset:0\n" \
"v_accvgpr_read_b32 v184, a0\n" \
M_"v[116:119], a[240:243], a[208:211], v[116:119]\n" \
"v_accvgpr_read_b32 v185, a1\n" \
"v_accvgpr_read_b32 v186, a2\n" \
M_"v[120:123], a[180:183], a[144:147], v[120:123]\n" \
"v_accvgpr_read_b32 v187, a3\n" \
"v_accvgpr_read_b32 v188, a4\n" \
M_"v[120:123], a[244:247], a[208:211], v[120:123]\n" \
X_"a[162:163], v32 offset:512\n" \
"v_accvgpr_read_b32 v189, a5\n" \
M_"v[124:127], a[184:187], a[144:147], v[124:127]\n" \
"v_accvgpr_read_b32 v190, a6\n" \
"v_accvgpr_read_b32 v191, a7\n" \
M_"v[124:127], a[248:251], a[208:211], v[124:127]\n" \
"v_accvgpr_read_b32 v196, a12\n" \
"v_accvgpr_read_b32 v197, a13\n" \
M_"v[128:131], a[188:191], a[144:147], v[128:131]\n" \
X_"a[224:225], v32 offset:4096\n" \
"v_accvgpr_read_b32 v198, a14\n" \
M_"v[128:131], a[252:255], a[208:211], v[128:131]\n" \
R_"a[144:147], v28 offset:8192\n" \
"v_accvgpr_read_b32 v199, a15\n" \
M_"v[144:147], a[188:191], a[148:151], v[144:147]\n" \
R_"a[208:211], v29 offset:8192\n" \
"v_accvgpr_read_b32 v192, a8\n" \
M_"v[144:147], a[252:255], a[212:215], v[144:147]\n" \
X_"a[226:227], v32 offset:4608\n" \
"v_accvgpr_read_b32 v193, a9\n" \
M_"v[140:143], a[184:187], a[148:151], v[140:143]\n" \
"v_accvgpr_read_b32 v194, a10\n" \
"v_accvgpr_read_b32 v195, a11\n" \
M_"v[140:143], a[248:251], a[212:215], v[140:143]\n" \
"v_accvgpr_read_b32 v208, a24\n" \
"v_accvgpr_read_b32 v209, a25\n" \
M_"v[136:139], a[180:183], a[148:151], v[136:139]\n" \
"v_accvgpr_read_b32 v210, a26\n" \
"v_accvgpr_read_b32 v211, a27\n" \
M_"v[136:139], a[244:247], a[212:215], v[136:139]\n" \
X_"a[164:165], v33 offset:0\n" \
"v_accvgpr_read_b32 v212, a28\n" \
M_"v[132:135], a[176:179], a[148:151], v[132:135]\n" \
"v_accvgpr_read_b32 v213, a29\n" \
"v_accvgpr_read_b32 v214, a30\n" \
M_"v[132:135], a[240:243], a[212:215], v[132:135]\n" \
R_"a[148:151], v28 offset:10240\n" \
"v_accvgpr_read_b32 v215, a31\n" \
M_"v[148:151], a[176:179], a[152:155], v[148:151]\n" \
R_"a[212:215], v29 offset:10240\n" \
"v_accvgpr_read_b32 v204, a20\n" \
M_"v[148:151], a[240:243], a[216:219], v[148:151]\n" \
X_"a[166:167], v33 offset:512\n" \
"v_accvgpr_read_b32 v205, a21\n" \
M_"v[152:155], a[180:183], a[152:155], v[152:155]\n" \
"v_accvgpr_read_b32 v206, a22\n" \
"v_accvgpr_read_b32 v207, a23\n" \
M_"v[152:155], a[244:247], a[216:219], v[152:155]\n" \
X_"a[228:229], v33 offset:4096\n" \
"v_accvgpr_read_b32 v200, a16\n" \
M_"v[156:159], a[184:187], a[152:155], v[156:159]\n" \
"v_accvgpr_read_b32 v201, a17\n" \
"v_accvgpr_read_b32 v202, a18\n" \
M_"v[156:159], a[248:251], a[216:219], v[156:159]\n" \
"v_accvgpr_read_b32 v203, a19\n" \
"v_accvgpr_read_b32 v216, a32\n" \
M_"v[160:163], a[188:191], a[152:155], v[160:163]\n" \
X_"a[230:231], v33 offset:4608\n" \
"v_accvgpr_read_b32 v217, a33\n" \
M_"v[160:163], a[252:255], a[216:219], v[160:163]\n" \
R_"a[152:155], v28 offset:12288\n" \
"v_accvgpr_read_b32 v218, a34\n" \
M_"v[176:179], a[188:191], a[156:159], v[176:179]\n" \
R_"a[216:219], v29 offset:12288\n" \
"v_accvgpr_read_b32 v219, a35\n" \
M_"v[176:179], a[252:255], a[220:223], v[176:179]\n" \
"v_accvgpr_read_b32 v220, a36\n" \
"v_accvgpr_read_b32 v221, a37\n" \
M_"v[172:175], a[184:187], a[156:159], v[172:175]\n" \
X_"a[172:173], v35 offset:0\n" \
"v_accvgpr_read_b32 v222, a38\n" \
M_"v[172:175], a[248:251], a[220:223], v[172:175]\n" \
"v_accvgpr_read_b32 v223, a39\n" \
"v_accvgpr_read_b32 v228, a44\n" \
M_"v[168:171], a[180:183], a[156:159], v[168:171]\n" \
"v_accvgpr_read_b32 v229, a45\n" \
"v_accvgpr_read_b32 v230, a46\n" \
M_"v[168:171], a[244:247], a[220:223], v[168:171]\n" \
X_"a[174:175], v35 offset:512\n" \
"v_accvgpr_read_b32 v231, a47\n" \
M_"v[164:167], a[176:179], a[156:159], v[164:167]\n" \
"v_accvgpr_read_b32 v224, a40\n" \
"v_accvgpr_read_b32 v225, a41\n" \
M_"v[164:167], a[240:243], a[220:223], v[164:167]\n" \
R_"a[156:159], v28 offset:14336\n" \
"v_accvgpr_read_b32 v226, a42\n" \
M_"v[84:87], a[176:179], a[136:139], v[84:87]\n" \
R_"a[220:223], v29 offset:14336\n" \
"v_accvgpr_read_b32 v227, a43\n" \
M_"v[84:87], a[240:243], a[200:203], v[84:87]\n" \
X_"a[236:237], v35 offset:4096\n" \
"v_accvgpr_read_b32 v240, a56\n" \
M_"v[88:91], a[180:183], a[136:139], v[88:91]\n" \
"v_accvgpr_read_b32 v241, a57\n" \
"v_accvgpr_read_b32 v242, a58\n" \
M_"v[88:91], a[244:247], a[200:203], v[88:91]\n" \
X_"a[238:239], v35 offset:4608\n" \
"v_accvgpr_read_b32 v243, a59\n" \
M_"v[92:95], a[184:187], a[136:139], v[92:95]\n" \
"v_accvgpr_read_b32 v244, a60\n" \
"v_accvgpr_read_b32 v245, a61\n" \
M_"v[92:95], a[248:251], a[200:203], v[92:95]\n" \
"v_accvgpr_read_b32 v246, a62\n" \
"v_accvgpr_read_b32 v247, a63\n" \
M_"v[96:99], a[188:191], a[136:139], v[96:99]\n" \
"v_accvgpr_read_b32 v236, a52\n" \
"v_accvgpr_read_b32 v237, a53\n" \
M_"v[96:99], a[252:255], a[200:203], v[96:99]\n" \
R_"a[136:139], v28 offset:4096\n" \
"v_accvgpr_read_b32 v238, a54\n" \
M_"v[112:115], a[188:191], a[140:143], v[112:115]\n" \
R_"a[200:203], v29 offset:4096\n" \
"v_accvgpr_read_b32 v239, a55\n" \
M_"v[112:115], a[252:255], a[204:207], v[112:115]\n" \
X_"a[168:169], v34 offset:0\n" \
"v_accvgpr_read_b32 v232, a48\n" \
M_"v[108:111], a[184:187], a[140:143], v[108:111]\n" \
X_"a[170:171], v34 offset:512\n" \
"v_accvgpr_read_b32 v233, a49\n" \
M_"v[108:111], a[248:251], a[204:207], v[108:111]\n" \
"v_accvgpr_read_b32 v234, a50\n" \
"v_accvgpr_read_b32 v235, a51\n" \
M_"v[104:107], a[180:183], a[140:143], v[104:107]\n" \
"v_accvgpr_read_b32 v180, a64\n" \
"v_accvgpr_read_b32 v181, a65\n" \
M_"v[104:107], a[244:247], a[204:207], v[104:107]\n" \
X_"a[232:233], v34 offset:4096\n" \
"v_accvgpr_read_b32 v182, a66\n" \
M_"v[100:103], a[176:179], a[140:143], v[100:103]\n" \
"v_accvgpr_read_b32 v183, a67\n" \
"v_accvgpr_read_b32 v48, a80\n" \
M_"v[100:103], a[240:243], a[204:207], v[100:103]\n" \
R_"a[140:143], v28 offset:6144\n" \
"v_accvgpr_read_b32 v49, a81\n" \
M_"v[52:55], a[176:179], a[128:131], v[52:55]\n" \
R_"a[204:207], v29 offset:6144\n" \
"v_accvgpr_read_b32 v50, a82\n" \
M_"v[52:55], a[240:243], a[192:195], v[52:55]\n" \
X_"a[234:235], v34 offset:4608\n" \
"v_accvgpr_read_b32 v51, a83\n" \
M_"v[56:59], a[180:183], a[128:131], v[56:59]\n" \
"v_accvgpr_read_b32 v252, a68\n" \
"v_accvgpr_read_b32 v253, a69\n" \
M_"v[56:59], a[244:247], a[192:195], v[56:59]\n" \
"v_accvgpr_read_b32 v254, a70\n" \
"v_accvgpr_read_b32 v255, a71\n" \
M_"v[60:63], a[184:187], a[128:131], v[60:63]\n" \
"v_lshrrev_b32_e64 v24, 9, v20\n" \
"v_and_b32_e32 v26, 0x1ff, v20\n" \
M_"v[60:63], a[248:251], a[192:195], v[60:63]\n" \
"v_lshl_add_u32 v24, v24, 3, v26\n" \
"v_lshrrev_b32_e64 v25, 8, v20\n" \
M_"v[64:67], a[188:191], a[128:131], v[64:67]\n" \
"v_mul_u32_u24_e32 v25, 0x24700, v25\n" \
"v_add_u32_e64 v25, v25, v20\n" \
M_"v[64:67], a[252:255], a[192:195], v[64:67]\n" \
R_"a[128:131], v28 offset:0\n" \
"v_lshrrev_b32_e64 v26, 2, v20\n" \
M_"v[80:83], a[188:191], a[132:135], v[80:83]\n" \
R_"a[192:195], v29 offset:0\n" \
"v_add_u32_e64 v26, s45, v26\n" \
M_"v[80:83], a[252:255], a[196:199], v[80:83]\n" \
"v_add_u32_e32 v27, s46, v20\n" \
"v_add_u32_e32 v21, s43, v27\n" \
M_"v[76:79], a[184:187], a[132:135], v[76:79]\n" \
M_"v[76:79], a[248:251], a[196:199], v[76:79]\n" \
M_"v[72:75], a[180:183], a[132:135], v[72:75]\n" \
M_"v[72:75], a[244:247], a[196:199], v[72:75]\n" \
M_"v[68:71], a[176:179], a[132:135], v[68:71]\n" \
M_"v[68:71], a[240:243], a[196:199], v[68:71]\n" \
R_"a[132:135], v28 offset:2048\n" \
R_"a[196:199], v29 offset:2048\n" \
"s_waitcnt lgkmcnt(15)\n" \
"s_nop 0\n" \
M_"v[184:187], a[160:163], a[144:147], v[184:187]\n" \
"v_bfe_u32 v44, v20, 2, 5\n" \
X_"a[176:177], v36 offset:0\n" \
M_"v[184:187], a[224:227], a[208:211], v[184:187]\n" \
"buffer_load_dword v44, v44, s[80:83], s38 offen offset:0 sc1\n" \
M_"v[188:191], a[164:167], a[144:147], v[188:191]\n" \
X_"a[178:179], v36 offset:1024\n" \
M_"v[188:191], a[228:231], a[208:211], v[188:191]\n" \
X_"a[240:241], v36 offset:8192\n" \
M_"v[196:199], a[172:175], a[144:147], v[196:199]\n" \
X_"a[242:243], v36 offset:9216\n" \
"s_waitcnt lgkmcnt(15)\n" \
M_"v[196:199], a[236:239], a[208:211], v[196:199]\n" \
"s_waitcnt lgkmcnt(12)\n" \
M_"v[192:195], a[144:147], a[168:171], v[192:195]\n" \
X_"a[180:181], v37 offset:0\n" \
"s_waitcnt lgkmcnt(9)\n" \
"s_nop 0\n" \
M_"v[192:195], a[208:211], a[232:235], v[192:195]\n" \
M_"v[208:211], a[148:151], a[168:171], v[208:211]\n" \
X_"a[182:183], v37 offset:1024\n" \
M_"v[208:211], a[212:215], a[232:235], v[208:211]\n" \
X_"a[244:245], v37 offset:8192\n" \
M_"v[212:215], a[172:175], a[148:151], v[212:215]\n" \
M_"v[212:215], a[236:239], a[212:215], v[212:215]\n" \
X_"a[246:247], v37 offset:9216\n" \
C_"v188, v188, v189\n" \
M_"v[204:207], a[164:167], a[148:151], v[204:207]\n" \
C_"v189, v190, v191\n" \
C_"v190, v198, v199\n" \
M_"v[204:207], a[228:231], a[212:215], v[204:207]\n" \
X_"a[184:185], v38 offset:0\n" \
"v_mov_b32_e32 v191, 0x3f80\n" \
M_"v[200:203], a[160:163], a[148:151], v[200:203]\n" \
X_"a[186:187], v38 offset:1024\n" \
C_"v184, v184, v185\n" \
M_"v[200:203], a[224:227], a[212:215], v[200:203]\n" \
C_"v185, v186, v187\n" \
C_"v186, v196, v197\n" \
M_"v[216:219], a[160:163], a[152:155], v[216:219]\n" \
X_"a[248:249], v38 offset:8192\n" \
"v_mov_b32_e64 v187, 0\n" \
M_"v[216:219], a[224:227], a[216:219], v[216:219]\n" \
X_"a[250:251], v38 offset:9216\n" \
M_"v[220:223], a[164:167], a[152:155], v[220:223]\n" \
M_"v[220:223], a[228:231], a[216:219], v[220:223]\n" \
X_"a[188:189], v39 offset:0\n" \
C_"v204, v204, v205\n" \
M_"v[228:231], a[172:175], a[152:155], v[228:231]\n" \
C_"v205, v206, v207\n" \
C_"v206, v214, v215\n" \
M_"v[228:231], a[236:239], a[216:219], v[228:231]\n" \
X_"a[190:191], v39 offset:1024\n" \
"v_mov_b32_e32 v207, 0x3f80\n" \
M_"v[224:227], a[152:155], a[168:171], v[224:227]\n" \
X_"a[252:253], v39 offset:8192\n" \
C_"v200, v200, v201\n" \
M_"v[224:227], a[216:219], a[232:235], v[224:227]\n" \
C_"v201, v202, v203\n" \
C_"v202, v212, v213\n" \
M_"v[240:243], a[156:159], a[168:171], v[240:243]\n" \
X_"a[254:255], v39 offset:9216\n" \
"v_mov_b32_e64 v203, 0\n" \
M_"v[240:243], a[220:223], a[232:235], v[240:243]\n" \
M_"v[244:247], a[172:175], a[156:159], v[244:247]\n" \
M_"v[244:247], a[236:239], a[220:223], v[244:247]\n" \
C_"v220, v220, v221\n" \
C_"v221, v222, v223\n" \
M_"v[236:239], a[164:167], a[156:159], v[236:239]\n" \
C_"v222, v230, v231\n" \
"v_mov_b32_e32 v223, 0x3f80\n" \
M_"v[236:239], a[228:231], a[220:223], v[236:239]\n" \
C_"v216, v216, v217\n" \
C_"v217, v218, v219\n" \
M_"v[232:235], a[160:163], a[156:159], v[232:235]\n" \
C_"v218, v228, v229\n" \
"v_mov_b32_e64 v219, 0\n" \
M_"v[232:235], a[224:227], a[220:223], v[232:235]\n" \
M_"v[180:183], a[164:167], a[140:143], v[180:183]\n" \
M_"v[180:183], a[228:231], a[204:207], v[180:183]\n" \
M_"v[48:51], a[172:175], a[140:143], v[48:51]\n" \
M_"v[48:51], a[236:239], a[204:207], v[48:51]\n" \
C_"v236, v236, v237\n" \
C_"v237, v238, v239\n" \
M_"v[252:255], a[140:143], a[168:171], v[252:255]\n" \
C_"v238, v246, v247\n" \
"v_mov_b32_e32 v239, 0x3f80\n" \
M_"v[252:255], a[204:207], a[232:235], v[252:255]\n" \
C_"v232, v232, v233\n" \
C_"v233, v234, v235\n" \
"s_waitcnt vmcnt(0) lgkmcnt(0)\n" \
"s_barrier\n" \
"v_mov_b32_e32 v45, s62\n" \
"s_mov_b64 exec, 1\n" \
"buffer_store_dword v45, off, s[80:83], s37 offset:32 sc1\n" \
"s_mov_b64 exec, -1\n" \
"v_cmp_ne_u32_e32 vcc, s62, v44\n" \
"s_nop 4\n" \
"s_cbranch_vccnz L_poll161_%=\n" \
"L_pollr161_%=:\n" \
M_"v[116:119], a[176:179], a[144:147], v[116:119]\n" \
"s_mov_b32 s73, s50\n" \
"s_sub_u32 m0, s31, 0x10000\n" \
M_"v[116:119], a[240:243], a[208:211], v[116:119]\n" \
B_"v20, s[80:83], s73 offen offset:0 sc1 lds\n" \
M_"v[120:123], a[180:183], a[144:147], v[120:123]\n" \
"s_add_u32 s74, s50, 0x4000\n" \
"s_add_u32 s75, s50, 0x8000\n" \
M_"v[120:123], a[244:247], a[208:211], v[120:123]\n" \
"s_mov_b32 s70, 0x3e0293ee\n" \
"s_mov_b32 s76, s52\n" \
M_"v[124:127], a[184:187], a[144:147], v[124:127]\n" \
"v_mul_f32_e32 v116, s70, v116\n" \
"v_mul_f32_e32 v117, s70, v117\n" \
M_"v[124:127], a[248:251], a[208:211], v[124:127]\n" \
B_"v20, s[80:83], s73 offen offset:1024 sc1 lds\n" \
M_"v[128:131], a[188:191], a[144:147], v[128:131]\n" \
"v_mul_f32_e32 v118, s70, v118\n" \
"v_mul_f32_e32 v119, s70, v119\n" \
M_"v[128:131], a[252:255], a[208:211], v[128:131]\n" \
C_"v116, v116, v117\n" \
"v_mul_f32_e64 v120, s70, v120\n" \
M_"v[144:147], a[188:191], a[148:151], v[144:147]\n" \
"v_mul_f32_e32 v124, s70, v124\n" \
"v_mul_f32_e32 v125, s70, v125\n" \
M_"v[144:147], a[252:255], a[212:215], v[144:147]\n" \
B_"v20, s[80:83], s73 offen offset:2048 sc1 lds\n" \
M_"v[140:143], a[184:187], a[148:151], v[140:143]\n" \
"v_mul_f32_e32 v126, s70, v126\n" \
"v_mul_f32_e32 v127, s70, v127\n" \
M_"v[140:143], a[248:251], a[212:215], v[140:143]\n" \
C_"v124, v124, v125\n" \
"v_mul_f32_e32 v121, s70, v121\n" \
".fill (L_pb + 77536 - .) / 4, 4, 0xBF800000\n" \
"L_pp19:\n" \
"s_cbranch_execz L_pret\n" \
M_"v[136:139], a[180:183], a[148:151], v[136:139]\n" \
"v_mul_f32_e32 v122, s70, v122\n" \
"v_mul_f32_e64 v123, s70, v123\n" \
M_"v[136:139], a[244:247], a[212:215], v[136:139]\n" \
B_"v20, s[80:83], s73 offen offset:3072 sc1 lds\n" \
M_"v[132:135], a[176:179], a[148:151], v[132:135]\n" \
"s_sub_u32 m0, s31, s51\n" \
C_"v117, v118, v119\n" \
M_"v[132:135], a[240:243], a[212:215], v[132:135]\n" \
"v_mul_f32_e32 v128, s70, v128\n" \
"v_mul_f32_e64 v129, s70, v129\n" \
M_"v[148:151], a[176:179], a[152:155], v[148:151]\n" \
"v_mul_f32_e32 v130, s70, v130\n" \
"v_mul_f32_e32 v131, s70, v131\n" \
M_"v[148:151], a[240:243], a[216:219], v[148:151]\n" \
B_"v20, s[80:83], s74 offen offset:0 sc1 lds\n" \
M_"v[152:155], a[180:183], a[152:155], v[152:155]\n" \
C_"v125, v126, v127\n" \
C_"v118, v120, v121\n" \
M_"v[152:155], a[244:247], a[216:219], v[152:155]\n" \
C_"v119, v122, v123\n" \
C_"v126, v128, v129\n" \
M_"v[156:159], a[184:187], a[152:155], v[156:159]\n" \
C_"v127, v130, v131\n" \
"v_mul_f32_e64 v132, s70, v132\n" \
M_"v[156:159], a[248:251], a[216:219], v[156:159]\n" \
B_"v20, s[80:83], s74 offen offset:1024 sc1 lds\n" \
M_"v[160:163], a[188:191], a[152:155], v[160:163]\n" \
"ds_write_b128 v21, v[116:119] offset:16384\n" \
M_"v[160:163], a[252:255], a[216:219], v[160:163]\n" \
"v_mul_f32_e32 v133, s70, v133\n" \
"v_mul_f32_e32 v140, s70, v140\n" \
M_"v[176:179], a[188:191], a[156:159], v[176:179]\n" \
"v_mul_f32_e32 v141, s70, v141\n" \
"v_mul_f32_e32 v134, s70, v134\n" \
M_"v[176:179], a[252:255], a[220:223], v[176:179]\n" \
B_"v20, s[80:83], s74 offen offset:2048 sc1 lds\n" \
M_"v[172:175], a[184:187], a[156:159], v[172:175]\n" \
"ds_write_b128 v21, v[124:127] offset:17408\n" \
M_"v[172:175], a[248:251], a[220:223], v[172:175]\n" \
"v_mul_f32_e64 v135, s70, v135\n" \
C_"v132, v132, v133\n" \
M_"v[168:171], a[180:183], a[156:159], v[168:171]\n" \
"v_mul_f32_e32 v142, s70, v142\n" \
"v_mul_f32_e32 v143, s70, v143\n" \
M_"v[168:171], a[244:247], a[220:223], v[168:171]\n" \
B_"v20, s[80:83], s74 offen offset:3072 sc1 lds\n" \
M_"v[164:167], a[176:179], a[156:159], v[164:167]\n" \
"s_sub_u32 m0, s31, 0x4000\n" \
C_"v140, v140, v141\n" \
M_"v[164:167], a[240:243], a[220:223], v[164:167]\n" \
"v_mul_f32_e32 v136, s70, v136\n" \
"v_mul_f32_e32 v137, s70, v137\n" \
M_"v[84:87], a[176:179], a[136:139], v[84:87]\n" \
"v_mul_f32_e32 v138, s70, v138\n" \
"v_mul_f32_e32 v139, s70, v139\n" \
M_"v[84:87], a[240:243], a[200:203], v[84:87]\n" \
B_"v20, s[80:83], s75 offen offset:0 sc1 lds\n" \
M_"v[88:91], a[180:183], a[136:139], v[88:91]\n" \
C_"v133, v134, v135\n" \
"v_mul_f32_e64 v144, s70, v144\n" \
M_"v[88:91], a[244:247], a[200:203], v[88:91]\n" \
"v_mul_f32_e32 v145, s70, v145\n" \
"v_mul_f32_e32 v146, s70, v146\n" \
M_"v[92:95], a[184:187], a[136:139], v[92:95]\n" \
"v_mul_f32_e64 v147, s70, v147\n" \
C_"v141, v142, v143\n" \
M_"v[92:95], a[248:251], a[200:203], v[92:95]\n" \
B_"v20, s[80:83], s75 offen offset:1024 sc1 lds\n" \
M_"v[96:99], a[188:191], a[136:139], v[96:99]\n" \
C_"v134, v136, v137\n" \
C_"v135, v138, v139\n" \
M_"v[96:99], a[252:255], a[200:203], v[96:99]\n" \
C_"v142, v144, v145\n" \
C_"v143, v146, v147\n" \
M_"v[112:115], a[188:191], a[140:143], v[112:115]\n" \
"ds_write_b128 v21, v[132:135] offset:20480\n" \
M_"v[112:115], a[252:255], a[204:207], v[112:115]\n" \
B_"v20, s[80:83], s75 offen offset:2048 sc1 lds\n" \
M_"v[108:111], a[184:187], a[140:143], v[108:111]\n" \
C_"v180, v180, v181\n" \
C_"v181, v182, v183\n" \
M_"v[108:111], a[248:251], a[204:207], v[108:111]\n" \
"ds_write_b128 v21, v[140:143] offset:21504\n" \
M_"v[104:107], a[180:183], a[140:143], v[104:107]\n" \
C_"v234, v244, v245\n" \
"v_mov_b32_e64 v235, 0\n" \
M_"v[104:107], a[244:247], a[204:207], v[104:107]\n" \
B_"v20, s[80:83], s75 offen offset:3072 sc1 lds\n" \
M_"v[100:103], a[176:179], a[140:143], v[100:103]\n" \
"s_mov_b32 m0, s53\n" \
"s_nop 0\n" \
C_"v182, v50, v51\n" \
M_"v[100:103], a[240:243], a[204:207], v[100:103]\n" \
"v_mov_b32_e32 v183, 0x3f80\n" \
C_"v118, v192, v193\n" \
M_"v[52:55], a[176:179], a[128:131], v[52:55]\n" \
C_"v119, v194, v195\n" \
C_"v120, v208, v209\n" \
M_"v[52:55], a[240:243], a[192:195], v[52:55]\n" \
B_"v20, s[80:83], s76 offen offset:0 sc1 lds\n" \
M_"v[56:59], a[180:183], a[128:131], v[56:59]\n" \
C_"v121, v210, v211\n" \
C_"v122, v224, v225\n" \
M_"v[56:59], a[244:247], a[192:195], v[56:59]\n" \
C_"v123, v226, v227\n" \
C_"v124, v240, v241\n" \
C_"v125, v242, v243\n" \
M_"v[60:63], a[184:187], a[128:131], v[60:63]\n" \
M_"v[192:195], v[188:191], v[184:187], 0\n" \
B_"v20, s[80:83], s76 offen offset:1024 sc1 lds\n" \
M_"v[32:35], v[180:183], v[184:187], 0\n" \
M_"v[208:211], v[204:207], v[200:203], 0\n" \
M_"v[36:39], v[188:191], v[200:203], 0\n" \
M_"v[224:227], v[220:223], v[216:219], 0\n" \
M_"v[248:251], v[204:207], v[216:219], 0\n" \
M_"v[240:243], v[236:239], v[232:235], 0\n" \
M_"v[128:131], v[220:223], v[232:235], 0\n" \
"s_mov_b32 s64, 0xaaaaaaaa\n" \
"s_mov_b32 s65, 0xaaaaaaaa\n" \
M_"v[60:63], a[248:251], a[192:195], v[60:63]\n" \
"s_mov_b32 s66, 0xfff0ffff\n" \
"s_mov_b32 s67, 0xf000ff00\n" \
M_"v[64:67], a[188:191], a[128:131], v[64:67]\n" \
"s_mov_b32 s68, 0xffc0fffc\n" \
"s_mov_b32 s69, 0xc000fc00\n" \
"v_cndmask_b32_e64 v192, v192, v193, s[64:65]\n" \
"v_cndmask_b32_e64 v32, v32, v33, s[64:65]\n" \
"v_cndmask_b32_e64 v194, v194, v195, s[64:65]\n" \
"v_cndmask_b32_e64 v34, v34, v35, s[64:65]\n" \
"v_cndmask_b32_e64 v208, v208, v209, s[64:65]\n" \
"v_cndmask_b32_e64 v36, v36, v37, s[64:65]\n" \
"v_cndmask_b32_e64 v210, v210, v211, s[64:65]\n" \
"v_cndmask_b32_e64 v38, v38, v39, s[64:65]\n" \
"v_cndmask_b32_e64 v224, v224, v225, s[64:65]\n" \
"v_cndmask_b32_e64 v248, v248, v249, s[64:65]\n" \
"v_cndmask_b32_e64 v226, v226, v227, s[64:65]\n" \
"v_cndmask_b32_e64 v250, v250, v251, s[64:65]\n" \
"v_cndmask_b32_e64 v240, v240, v241, s[64:65]\n" \
"v_cndmask_b32_e64 v128, v128, v129, s[64:65]\n" \
"v_cndmask_b32_e64 v242, v242, v243, s[64:65]\n" \
"v_cndmask_b32_e64 v130, v130, v131, s[64:65]\n" \
"v_cndmask_b32_e64 v192, v32, v192, s[66:67]\n" \
"v_cndmask_b32_e64 v194, v34, v194, s[68:69]\n" \
"v_cndmask_b32_e64 v208, v36, v208, s[66:67]\n" \
"v_cndmask_b32_e64 v210, v38, v210, s[68:69]\n" \
"v_cndmask_b32_e64 v224, v248, v224, s[66:67]\n" \
"v_cndmask_b32_e64 v226, v250, v226, s[68:69]\n" \
"v_cndmask_b32_e64 v240, v128, v240, s[66:67]\n" \
"v_cndmask_b32_e64 v242, v130, v242, s[68:69]\n" \
M_"v[64:67], a[252:255], a[192:195], v[64:67]\n" \
"v_max_f32_e32 v193, v192, v194\n" \
"v_max_f32_e32 v209, v208, v210\n" \
"v_max_f32_e32 v225, v224, v226\n" \
"v_max_f32_e32 v241, v240, v242\n" \
"v_mul_f32_e32 v148, s70, v148\n" \
"v_permlane32_swap_b32_e32 v193, v225\n" \
"v_permlane32_swap_b32_e32 v209, v241\n" \
"v_mul_f32_e32 v149, s70, v149\n" \
"v_mul_f32_e32 v156, s70, v156\n" \
"v_mul_f32_e32 v157, s70, v157\n" \
"v_permlane32_swap_b32_e32 v196, v228\n" \
"v_permlane32_swap_b32_e32 v212, v244\n" \
"v_max_f32_e32 v193, v193, v225\n" \
"v_max_f32_e32 v209, v209, v241\n" \
"v_mul_f32_e32 v150, s70, v150\n" \
"v_mul_f32_e32 v151, s70, v151\n" \
C_"v148, v148, v149\n" \
"v_mul_f32_e32 v158, s70, v158\n" \
"v_mul_f32_e32 v159, s70, v159\n" \
C_"v156, v156, v157\n" \
"v_permlane16_swap_b32_e32 v228, v244\n" \
"v_permlane16_swap_b32_e32 v193, v209\n" \
M_"v[80:83], a[188:191], a[132:135], v[80:83]\n" \
"s_mov_b32 s72, 0x3eae1a93\n" \
"v_mul_f32_e32 v152, s70, v152\n" \
"v_mul_f32_e32 v153, s70, v153\n" \
"v_mul_f32_e32 v154, s70, v154\n" \
"v_mul_f32_e32 v155, s70, v155\n" \
C_"v149, v150, v151\n" \
"v_mul_f32_e32 v160, s70, v160\n" \
"v_mul_f32_e32 v161, s70, v161\n" \
"v_mul_f32_e32 v162, s70, v162\n" \
"v_mul_f32_e32 v163, s70, v163\n" \
C_"v157, v158, v159\n" \
"v_mul_f32_e32 v45, 0xbfb8aa3b, v228\n" \
"v_mfma_f32_16x16x16_bf16 v[48:51], v[182:183], v[46:47], 0\n" \
"v_mfma_f32_16x16x16_bf16 v[196:199], v[190:191], v[46:47], 0\n" \
"v_mfma_f32_16x16x16_bf16 v[212:215], v[206:207], v[46:47], 0\n" \
"v_max_f32_e64 v193, v193, v209\n" \
C_"v150, v152, v153\n" \
C_"v151, v154, v155\n" \
C_"v158, v160, v161\n" \
C_"v159, v162, v163\n" \
"v_mfma_f32_16x16x16_bf16 v[228:231], v[222:223], v[46:47], 0\n" \
"v_mul_f32_e32 v193, s72, v193\n" \
"v_mov_b32_e32 v209, v193\n" \
"ds_write_b128 v21, v[148:151] offset:24576\n" \
"v_mfma_f32_16x16x16_bf16 v[244:247], v[238:239], v[46:47], 0\n" \
"v_permlane16_swap_b32_e32 v193, v209\n" \
"s_mov_b32 s71, 0x3e0293ee\n" \
M_"v[80:83], a[252:255], a[196:199], v[80:83]\n" \
"v_mov_b32_e32 v225, v193\n" \
"v_mov_b32_e32 v241, v209\n" \
"v_mul_f32_e32 v164, s70, v164\n" \
"v_mul_f32_e64 v165, s70, v165\n" \
"ds_write_b128 v21, v[156:159] offset:25600\n" \
"v_mul_f32_e32 v172, s70, v172\n" \
"v_mul_f32_e32 v173, s70, v173\n" \
"v_pk_mul_f32 v[84:85], v[84:85], s[70:71]\n" \
"v_pk_mul_f32 v[92:93], v[92:93], s[70:71]\n" \
"v_pk_mul_f32 v[100:101], v[100:101], s[70:71]\n" \
"v_pk_mul_f32 v[108:109], v[108:109], s[70:71]\n" \
"v_pk_mul_f32 v[52:53], v[52:53], s[70:71]\n" \
"v_pk_mul_f32 v[60:61], v[60:61], s[70:71]\n" \
"v_permlane32_swap_b32_e32 v193, v225\n" \
"v_permlane32_swap_b32_e32 v209, v241\n" \
"v_mul_f32_e32 v166, s70, v166\n" \
"v_mul_f32_e32 v167, s70, v167\n" \
C_"v164, v164, v165\n" \
"v_mul_f32_e32 v174, s70, v174\n" \
"v_mul_f32_e32 v175, s70, v175\n" \
C_"v172, v172, v173\n" \
"v_pk_mul_f32 v[86:87], v[86:87], s[70:71]\n" \
C_"v84, v84, v85\n" \
"v_pk_mul_f32 v[94:95], v[94:95], s[70:71]\n" \
C_"v92, v92, v93\n" \
"v_pk_mul_f32 v[102:103], v[102:103], s[70:71]\n" \
C_"v100, v100, v101\n" \
"v_pk_mul_f32 v[110:111], v[110:111], s[70:71]\n" \
C_"v108, v108, v109\n" \
"v_pk_mul_f32 v[54:55], v[54:55], s[70:71]\n" \
C_"v52, v52, v53\n" \
"v_pk_mul_f32 v[62:63], v[62:63], s[70:71]\n" \
C_"v60, v60, v61\n" \
"v_fma_f32 v192, v192, s72, -v193\n" \
"v_fma_f32 v194, v194, s72, -v193\n" \
"v_fma_f32 v208, v208, s72, -v209\n" \
"v_fma_f32 v210, v210, s72, -v209\n" \
"v_mul_f32_e32 v168, s70, v168\n" \
"v_mul_f32_e32 v169, s70, v169\n" \
"v_mul_f32_e32 v170, s70, v170\n" \
"v_mul_f32_e32 v171, s70, v171\n" \
C_"v165, v166, v167\n" \
"v_mul_f32_e32 v176, s70, v176\n" \
"v_mul_f32_e32 v177, s70, v177\n" \
"v_mul_f32_e32 v178, s70, v178\n" \
"v_mul_f32_e32 v179, s70, v179\n" \
C_"v173, v174, v175\n" \
"v_pk_mul_f32 v[88:89], v[88:89], s[70:71]\n" \
"v_pk_mul_f32 v[90:91], v[90:91], s[70:71]\n" \
C_"v85, v86, v87\n" \
"v_pk_mul_f32 v[96:97], v[96:97], s[70:71]\n" \
"v_pk_mul_f32 v[98:99], v[98:99], s[70:71]\n" \
C_"v93, v94, v95\n" \
"v_pk_mul_f32 v[104:105], v[104:105], s[70:71]\n" \
"v_pk_mul_f32 v[106:107], v[106:107], s[70:71]\n" \
C_"v101, v102, v103\n" \
"v_pk_mul_f32 v[112:113], v[112:113], s[70:71]\n" \
"v_pk_mul_f32 v[114:115], v[114:115], s[70:71]\n" \
C_"v109, v110, v111\n" \
"v_pk_mul_f32 v[56:57], v[56:57], s[70:71]\n" \
"v_pk_mul_f32 v[58:59], v[58:59], s[70:71]\n" \
C_"v53, v54, v55\n" \
"v_pk_mul_f32 v[64:65], v[64:65], s[70:71]\n" \
"v_pk_mul_f32 v[66:67], v[66:67], s[70:71]\n" \
C_"v61, v62, v63\n" \
"v_pk_mul_f32 v[80:81], v[80:81], s[70:71]\n" \
"v_pk_mul_f32 v[82:83], v[82:83], s[70:71]\n" \
E_"v192, v192\n" \
E_"v194, v194\n" \
E_"v208, v208\n" \
E_"v210, v210\n" \
"v_fma_f32 v224, v224, s72, -v225\n" \
"v_fma_f32 v226, v226, s72, -v225\n" \
M_"v[76:79], a[184:187], a[132:135], v[76:79]\n" \
C_"v166, v168, v169\n" \
C_"v167, v170, v171\n" \
C_"v174, v176, v177\n" \
C_"v175, v178, v179\n" \
C_"v86, v88, v89\n" \
C_"v87, v90, v91\n" \
C_"v94, v96, v97\n" \
C_"v95, v98, v99\n" \
C_"v102, v104, v105\n" \
C_"v103, v106, v107\n" \
C_"v110, v112, v113\n" \
C_"v111, v114, v115\n" \
C_"v54, v56, v57\n" \
C_"v55, v58, v59\n" \
C_"v62, v64, v65\n" \
C_"v63, v66, v67\n" \
E_"v224, v224\n" \
E_"v226, v226\n" \
"v_fma_f32 v240, v240, s72, -v241\n" \
"v_fma_f32 v242, v242, s72, -v241\n" \
"ds_write_b128 v21, v[164:167] offset:28672\n" \
C_"v192, v192, v194\n" \
C_"v208, v208, v210\n" \
E_"v240, v240\n" \
E_"v242, v242\n" \
C_"v132, v48, v49\n" \
C_"v133, v50, v51\n" \
C_"v134, v196, v197\n" \
C_"v135, v198, v199\n" \
"ds_write_b128 v21, v[172:175] offset:29696\n" \
C_"v136, v212, v213\n" \
C_"v137, v214, v215\n" \
"v_perm_b32 v184, v192, v192, v40\n" \
"v_perm_b32 v185, v192, v192, v41\n" \
"v_perm_b32 v186, v192, v192, v42\n" \
"v_perm_b32 v187, v192, v192, v43\n" \
"v_perm_b32 v200, v208, v208, v40\n" \
"v_perm_b32 v201, v208, v208, v41\n" \
"v_perm_b32 v202, v208, v208, v42\n" \
"v_perm_b32 v203, v208, v208, v43\n" \
"ds_write_b128 v27, v[84:87] offset:8192\n" \
C_"v224, v224, v226\n" \
C_"v138, v228, v229\n" \
C_"v139, v230, v231\n" \
M_"v[32:35], v[132:135], v[184:187], 0\n" \
M_"v[36:39], v[134:137], v[200:203], 0\n" \
"v_perm_b32 v216, v224, v224, v40\n" \
"v_perm_b32 v217, v224, v224, v41\n" \
"ds_write_b128 v27, v[92:95] offset:9216\n" \
"v_perm_b32 v218, v224, v224, v42\n" \
"v_perm_b32 v219, v224, v224, v43\n" \
C_"v240, v240, v242\n" \
M_"v[76:79], a[248:251], a[196:199], v[76:79]\n" \
C_"v140, v244, v245\n" \
C_"v141, v246, v247\n" \
M_"v[248:251], v[136:139], v[216:219], 0\n" \
"v_perm_b32 v232, v240, v240, v40\n" \
"v_perm_b32 v233, v240, v240, v41\n" \
"ds_write_b128 v27, v[100:103] offset:12288\n" \
"v_perm_b32 v234, v240, v240, v42\n" \
"v_perm_b32 v235, v240, v240, v43\n" \
"v_pk_mul_f32 v[76:77], v[76:77], s[70:71]\n" \
"v_pk_mul_f32 v[78:79], v[78:79], s[70:71]\n" \
C_"v76, v76, v77\n" \
C_"v77, v78, v79\n" \
C_"v78, v80, v81\n" \
C_"v79, v82, v83\n" \
M_"v[128:131], v[138:141], v[232:235], 0\n" \
"ds_write_b128 v27, v[108:111] offset:13312\n" \
M_"v[72:75], a[180:183], a[132:135], v[72:75]\n" \
C_"v116, v252, v253\n" \
C_"v117, v254, v255\n" \
M_"v[72:75], a[244:247], a[196:199], v[72:75]\n" \
"v_exp_f32_e64 v45, v45\n" \
M_"v[192:195], v[116:119], v[184:187], 0\n" \
"ds_write_b128 v27, v[52:55] offset:0\n" \
M_"v[208:211], v[118:121], v[200:203], 0\n" \
M_"v[224:227], v[120:123], v[216:219], 0\n" \
M_"v[240:243], v[122:125], v[232:235], 0\n" \
"ds_write_b128 v27, v[60:63] offset:1024\n" \
"v_mov_b32_dpp v34, v38 quad_perm:[0,1,2,3] row_mask:0x2 bank_mask:0xf\n" \
"v_pk_mul_f32 v[72:73], v[72:73], s[70:71]\n" \
"v_pk_mul_f32 v[74:75], v[74:75], s[70:71]\n" \
"v_mov_b32_dpp v34, v250 quad_perm:[0,1,2,3] row_mask:0x4 bank_mask:0xf\n" \
"v_add_f32_e64 v45, 1.0, v45\n" \
M_"v[68:71], a[176:179], a[132:135], v[68:71]\n" \
"v_mov_b32_dpp v34, v130 quad_perm:[0,1,2,3] row_mask:0x8 bank_mask:0xf\n" \
"v_mov_b32_dpp v32, v36 quad_perm:[0,1,2,3] row_mask:0x2 bank_mask:0xf\n" \
"v_rcp_f32_e64 v45, v45\n" \
"ds_write_b128 v27, v[76:79] offset:5120\n" \
"v_mov_b32_dpp v33, v37 quad_perm:[0,1,2,3] row_mask:0x2 bank_mask:0xf\n" \
"v_rcp_f32_e64 v34, v34\n" \
"v_mov_b32_dpp v32, v248 quad_perm:[0,1,2,3] row_mask:0x4 bank_mask:0xf\n" \
"v_mov_b32_dpp v33, v249 quad_perm:[0,1,2,3] row_mask:0x4 bank_mask:0xf\n" \
"v_mul_f32_e64 v34, v45, v34\n" \
"v_mov_b32_dpp v32, v128 quad_perm:[0,1,2,3] row_mask:0x8 bank_mask:0xf\n" \
"v_mov_b32_dpp v33, v129 quad_perm:[0,1,2,3] row_mask:0x8 bank_mask:0xf\n" \
"v_mul_f32_e32 v32, v34, v32\n" \
"v_mul_f32_e32 v33, v34, v33\n" \
"v_mov_b32_e64 v38, v34\n" \
M_"v[68:71], a[240:243], a[196:199], v[68:71]\n" \
"v_sub_f32_e32 v44, 1.0, v45\n" \
"v_permlane16_swap_b32_e32 v34, v38\n" \
"v_mov_b32_e32 v250, v34\n" \
"v_mov_b32_e32 v130, v38\n" \
C_"v32, v32, v33\n" \
"v_permlane32_swap_b32_e32 v34, v250\n" \
"v_permlane32_swap_b32_e32 v38, v130\n" \
"v_mul_f32_e64 v192, v34, v192\n" \
"v_pk_mul_f32 v[68:69], v[68:69], s[70:71]\n" \
"v_pk_mul_f32 v[70:71], v[70:71], s[70:71]\n" \
C_"v68, v68, v69\n" \
C_"v69, v70, v71\n" \
C_"v70, v72, v73\n" \
C_"v71, v74, v75\n" \
"ds_write_b128 v27, v[68:71] offset:4096\n" \
"v_mul_f32_e32 v193, v34, v193\n" \
"v_mul_f32_e32 v208, v38, v208\n" \
"v_mul_f32_e32 v209, v38, v209\n" \
"v_mul_f32_e32 v224, v250, v224\n" \
"v_mul_f32_e32 v225, v250, v225\n" \
"v_mul_f32_e32 v240, v130, v240\n" \
"v_mul_f32_e32 v241, v130, v241\n" \
"v_mul_f32_e32 v194, v34, v194\n" \
"v_mul_f32_e64 v195, v34, v195\n" \
C_"v192, v192, v193\n" \
"v_mul_f32_e32 v210, v38, v210\n" \
"v_mul_f32_e32 v211, v38, v211\n" \
C_"v208, v208, v209\n" \
"v_mul_f32_e32 v226, v250, v226\n" \
"v_mul_f32_e32 v227, v250, v227\n" \
C_"v224, v224, v225\n" \
"v_mul_f32_e32 v242, v130, v242\n" \
"v_mul_f32_e32 v243, v130, v243\n" \
C_"v240, v240, v241\n" \
C_"v193, v194, v195\n" \
"s_add_u32 s84, s48, 0x0\n" \
"s_nop 0\n" \
C_"v209, v210, v211\n" \
"s_add_u32 s85, s48, 0x24800\n" \
C_"v225, v226, v227\n" \
"s_add_u32 s86, s48, 0x49000\n" \
C_"v241, v242, v243\n" \
"s_add_u32 s87, s48, 0x6d800\n" \
"ds_write_b32 v26, v44\n" \
"buffer_store_dword v32, v25, s[80:83], s49 offen sc1\n" \
"buffer_store_dwordx2 v[192:193], v24, s[80:83], s84 offen sc1\n" \
"buffer_store_dwordx2 v[208:209], v24, s[80:83], s85 offen sc1\n" \
"buffer_store_dwordx2 v[224:225], v24, s[80:83], s86 offen sc1\n" \
"buffer_store_dwordx2 v[240:241], v24, s[80:83], s87 offen sc1\n" \
"s_branch L_end_%=\n" \
"L_poll61_%=:\n" \
"s_mov_b32 s84, 0\n" \
"L_pl61_%=:\n" \
"s_sleep 1\n" \
"v_bfe_u32 v44, v20, 2, 5\n" \
"buffer_load_dword v44, v44, s[80:83], s38 offen offset:128 sc1\n" \
"s_waitcnt vmcnt(0)\n" \
"v_cmp_ne_u32_e32 vcc, s62, v44\n" \
"s_add_u32 s84, s84, 1\n" \
"s_nop 3\n" \
"s_cbranch_vccz L_pollr61_%=\n" \
"s_cmp_gt_u32 s84, 0x400000\n" \
"s_cbranch_scc0 L_pl61_%=\n" \
"s_branch L_pollr61_%=\n" \
"L_poll161_%=:\n" \
"s_mov_b32 s84, 0\n" \
"L_pl161_%=:\n" \
"s_sleep 1\n" \
"v_bfe_u32 v44, v20, 2, 5\n" \
"buffer_load_dword v44, v44, s[80:83], s38 offen offset:0 sc1\n" \
"s_waitcnt vmcnt(0)\n" \
"v_cmp_ne_u32_e32 vcc, s62, v44\n" \
"s_add_u32 s84, s84, 1\n" \
"s_nop 3\n" \
"s_cbranch_vccz L_pollr161_%=\n" \
"s_cmp_gt_u32 s84, 0x400000\n" \
"s_cbranch_scc0 L_pl161_%=\n" \
"s_branch L_pollr161_%=\n" \
"L_end_%=:\n"
#define PROJ_PAGES 20
#define PROJ_VCLOB "v21","v24","v25","v28","v29","v32","v33","v34","v35","v36","v37","v38","v39","v40","v41","v42","v43","v44","v45","v46","v47","v48","v49","v50","v51","v52","v53","v54","v55","v56","v57","v58","v59","v60","v61","v62","v63","v64","v65","v66","v67","v68","v69","v70","v71","v72","v73","v74","v75","v76","v77","v78","v79","v80","v81","v82","v83","v84","v85","v86","v87","v88","v89","v90","v91","v92","v93","v94","v95","v96","v97","v98","v99","v100","v101","v102","v103","v104","v105","v106","v107","v108","v109","v110","v111","v112","v113","v114","v115","v116","v117","v118","v119","v120","v121","v122","v123","v124","v125","v126","v127","v128","v129","v130","v131","v132","v133","v134","v135","v136","v137","v138","v139","v140","v141","v142","v143","v144","v145","v146","v147","v148","v149","v150","v151","v152","v153","v154","v155","v156","v157","v158","v159","v160","v161","v162","v163","v164","v165","v166","v167","v168","v169","v170","v171","v172","v173","v174","v175","v176","v177","v178","v179","v180","v181","v182","v183","v184","v185","v186","v187","v188","v189","v190","v192","v193","v194","v195","v196","v197","v198","v199","v200","v201","v202","v203","v204","v205","v206","v207","v208","v209","v210","v211","v212","v213","v214","v215","v216","v217","v218","v219","v220","v221","v222","v223","v224","v225","v226","v227","v228","v229","v230","v231","v232","v233","v234","v235","v236","v237","v238","v239","v240","v241","v242","v243","v244","v245","v246","v247","v248","v249","v250","v251","v252","v253","v254","v255"
#define PROJ_SCLOB "s4","s5","s6","s7","s32","s33","s34","s35","s36","s37","s38","s39","s40","s41","s42","s43","s44","s45","s46","s47","s48","s49","s50","s51","s52","s53","s54","s55","s63","s64","s65","s66","s67","s68","s69","s70","s71","s72","s73","s74","s75","s76","s77","s78","s79","s80","s81","s82","s83","s84","s85","s86","s87","s88","s89","s90","s91","s92","s93","s94","s95","s96","s97","s98","s99","s100","s101"

typedef const u16* const __attribute__((address_space(4)))* kaptr;
__device__ __forceinline__ void proj_phase(kaptr& ka, char*& ws, const unsigned wid, u32x4 (&vp)[5], unsigned& vl, u32x4 (&sa)[5], u32x2 s24, u32x2& s28, unsigned tdum, u32x4& srdA, u32x2& sd, unsigned& tag
    ) {
    const unsigned b = blockIdx.x, h = (b & 7) * 4 + ((b >> 3) & 3), mt = b >> 5;
    const unsigned rb = (wid < 2u ? 15u - mt : mt) * 128u;
    const unsigned lane = threadIdx.x & 63u, wc = wid & 1u;
    u32x2 vAc, vB;
#pragma unroll
    for (int par = 0; par < 2; ++par) {
        const unsigned lr = lane >> 3, cc = (lane & 7u) ^ (4u * par + (lr >> 1)), r0 = rb + wc * 64u + lr;
        vAc[par] = r0 * (DC * 2) + cc * 16u;
        const unsigned br = lane >> 4, p16 = lane & 15u, sc = (((p16 >> 1) ^ (br | (par << 2))) << 1) | (p16 & 1u);
        vB[par] = (16u * wid + br) * (NBIG * 2) + h * (HD * 2) + sc * 16u;
    }
    vl = lane * 16u;
    u32x2 s30 = {wid * 8192u, 65536u + wid * 4096u};
    asm volatile(PROJ_ASM
                 : "={s62}"(tag), "={s[56:59]}"(srdA), "={s[60:61]}"(sd), "+{s[8:11]}"(sa[0]), "+{s[12:15]}"(sa[1]), "+{s[16:19]}"(sa[2]), "+{s[20:23]}"(sa[3]), "+{s[24:25]}"(s24), "+{s[28:29]}"(s28), "+{s[30:31]}"(s30),
                   "={v[0:3]}"(vp[0]), "={v[4:7]}"(vp[1]), "={v[8:11]}"(vp[2]), "={v[12:15]}"(vp[3]), "={v[16:19]}"(vp[4]), "+{v[22:23]}"(vAc), "+{v[26:27]}"(vB), "+{v191}"(tdum), "+{s[26:27]}"(ws), "+{s[0:1]}"(ka)
                 : "{s2}"(b), "{s3}"(wid), "{v20}"(vl)
                 : "memory", "m0", "vcc", "scc", PROJ_VCLOB, PROJ_SCLOB, ACLOB_LO, ACLOB_HI);
    sa[4] = (u32x4){s24[0], 0u, 0u, 0u};
}

namespace att {
#define DI __device__ __forceinline__
typedef __bf16 bfx8 __attribute__((ext_vector_type(8)));
DI unsigned rfl(unsigned x) { return __builtin_amdgcn_readfirstlane(x); }
DI f32x4 mfma(u32x4 a, u32x4 b, f32x4 c) { return __builtin_amdgcn_mfma_f32_16x16x32_bf16(__builtin_bit_cast(bfx8, a), __builtin_bit_cast(bfx8, b), c, 0, 0, 0); }
typedef __bf16 bfx2 __attribute__((ext_vector_type(2)));
typedef float f32x2 __attribute__((ext_vector_type(2)));
DI unsigned cvtpk2(float lo, float hi) { f32x2 v = {lo, hi}; bfx2 b = __builtin_convertvector(v, bfx2); return __builtin_bit_cast(unsigned, b); }
DI void att_exact(const u16* __restrict__ x, const u16* __restrict__ wq, char* __restrict__ ws, unsigned h, unsigned r0, float g1) {
    const unsigned lane = lane_id(), g = lane >> 4, c = lane & 15u;
    f32x4 o[8];
#pragma unroll
    for (int n = 0; n < 8; ++n) o[n] = (f32x4){0.f, 0.f, 0.f, 0.f};
#pragma unroll 1
    for (unsigned kk = 0; kk < (unsigned)DM; kk += 32u) {
        const unsigned k = kk + 8u * g;
        const u32x4 xf = *reinterpret_cast<const u32x4*>(x + (r0 + c) * DM + k);
#pragma unroll
        for (int n = 0; n < 8; ++n) {
            const u16* w = wq + k * NBIG + h * HD + n * 16 + c; u32x4 wf;
#pragma unroll
            for (int e = 0; e < 4; ++e) wf[e] = (unsigned)w[2 * e * NBIG] | ((unsigned)w[(2 * e + 1) * NBIG] << 16);
            o[n] = mfma(wf, xf, o[n]);
        }
    }
    u32x4 q[4];
#pragma unroll
    for (int p = 0; p < 4; ++p) {
        const f32x4 a = o[2 * p] * 0.12751743082f, b = o[2 * p + 1] * 0.12751743082f;
        q[p] = (u32x4){cvtpk2(a[0], a[1]), cvtpk2(a[2], a[3]), cvtpk2(b[0], b[1]), cvtpk2(b[2], b[3])};
    }
#pragma unroll
    for (int n = 0; n < 8; ++n) o[n] = (f32x4){0.f, 0.f, 0.f, 0.f};
    const __amdgpu_buffer_rsrc_t srd = __builtin_amdgcn_make_buffer_rsrc((void*)(ws + WS_K + (size_t)h * 1048576), (short)0, 1048576, 0x00020000);
    float m = -3.0e38f, l = 0.f;
#pragma unroll 1
    for (unsigned k0 = 0; k0 <= r0 + 15u; k0 += 32u) {
        const unsigned tb = (k0 >> 6) * 32768u + (k0 & 32u) * 256u;
        f32x4 s[2]; float mx = m;
#pragma unroll
        for (int kt = 0; kt < 2; ++kt) {
            s[kt] = (f32x4){0.f, 0.f, 0.f, 0.f};
#pragma unroll
            for (int p = 0; p < 4; ++p) s[kt] = mfma(__builtin_amdgcn_raw_buffer_load_b128(srd, lane * 16u + (kt * 4 + p) * 1024u, tb, 16), q[p], s[kt]);
#pragma unroll
            for (int e = 0; e < 4; ++e) { if (k0 + 16u * kt + 4u * g + e > r0 + c) s[kt][e] = -3.0e38f; mx = fmaxf(mx, s[kt][e]); }
        }
        mx = fmaxf(mx, __shfl_xor(mx, 16)); mx = fmaxf(mx, __shfl_xor(mx, 32));
        const float al = __builtin_amdgcn_exp2f(m - mx); m = mx;
        u32x4 pf;
#pragma unroll
        for (int e = 0; e < 4; ++e) pf[e] = cvtpk2(__builtin_amdgcn_exp2f(s[e >> 1][2 * (e & 1)] - m), __builtin_amdgcn_exp2f(s[e >> 1][2 * (e & 1) + 1] - m));
        l *= al;
#pragma unroll
        for (int e = 0; e < 4; ++e) l += __uint_as_float(pf[e] << 16) + __uint_as_float(pf[e] & 0xffff0000u);
#pragma unroll
        for (int n = 0; n < 8; ++n) o[n] = mfma(__builtin_amdgcn_raw_buffer_load_b128(srd, lane * 16u + 16384u + n * 1024u, tb, 16), pf, o[n] * al);
    }
    l += __shfl_xor(l, 16); l += __shfl_xor(l, 32);
    const float f = g1 / l;
    const __amdgpu_buffer_rsrc_t srdA = __builtin_amdgcn_make_buffer_rsrc((void*)(ws + WS_AF), (short)0, 128 * KS_TOT * 1024, 0x00020000);
#pragma unroll
    for (int p = 0; p < 4; ++p) {
        const f32x4 a = o[2 * p] * f, b = o[2 * p + 1] * f;
        __builtin_amdgcn_raw_buffer_store_b128((u32x4){cvtpk2(a[0], a[1]), cvtpk2(a[2], a[3]), cvtpk2(b[0], b[1]), cvtpk2(b[2], b[3])}, srdA, lane * 16u + p * 1024u, ((r0 >> 4) * KS_TOT + 18u + 4u * h) * 1024u, 16);
    }
}
}

#define ATT_ASM \
"s_branch L_ago_%=\n" \
".p2align 12\n" \
"L_ab:\n" \
".fill (L_ab + 0 - .) / 4, 4, 0xBF800000\n" \
"L_ap0:\n" \
"s_setpc_b64 s[88:89]\n" \
"L_ago_%=:\n" \
"s_mov_b32 s101, 0\n" \
"s_waitcnt vmcnt(15) lgkmcnt(0)\n" \
"s_barrier\n" \
"v_accvgpr_write_b32 a216, 0\n" \
"v_accvgpr_write_b32 a217, 0\n" \
"v_accvgpr_write_b32 a218, 0\n" \
"v_accvgpr_write_b32 a219, 0\n" \
"v_bfe_u32 v61, v20, 2, 5\n" \
"s_mov_b32 s96, s24\n" \
"v_mfma_f32_32x32x16_bf16 v[128:143], a[216:219], a[216:219], 0\n" \
"buffer_load_dword v57, v61, s[56:59], s96 offen sc1\n" \
"v_and_b32_e32 v63, 0x300, v20\n" \
R_"v[96:99], v63 offset:0\n" \
"v_add_u32_e32 v58, s22, v20\n" \
R_"a[0:3], v58 offset:0\n" \
"v_mfma_f32_32x32x16_bf16 v[144:159], a[216:219], a[216:219], 0\n" \
R_"a[16:19], v58 offset:8192\n" \
"v_add_u32_e32 v59, s23, v20\n" \
R_"a[32:35], v59 offset:0\n" \
R_"a[48:51], v59 offset:8192\n" \
R_"v[100:103], v63 offset:1024\n" \
R_"a[4:7], v58 offset:1024\n" \
"v_mfma_f32_32x32x16_bf16 v[160:175], a[216:219], a[216:219], 0\n" \
R_"a[20:23], v58 offset:9216\n" \
R_"a[36:39], v59 offset:1024\n" \
R_"a[52:55], v59 offset:9216\n" \
"s_sub_u32 s67, s12, 0xc000\n" \
R_"v[104:107], v63 offset:2048\n" \
"s_mov_b32 s65, s12\n" \
"v_mfma_f32_32x32x16_bf16 v[176:191], a[216:219], a[216:219], 0\n" \
"v_add_u32_e32 v55, s67, v20\n" \
"v_add_u32_e32 v52, 0xc000, v55\n" \
"s_xor_b32 m0, s65, 0x10000\n" \
R_"a[8:11], v58 offset:2048\n" \
B_"v52, s[8:11], 0 offen offset:0 sc1 lds\n" \
"v_mfma_f32_32x32x16_bf16 v[192:207], a[216:219], a[216:219], 0\n" \
R_"a[24:27], v58 offset:10240\n" \
R_"a[40:43], v59 offset:2048\n" \
R_"a[56:59], v59 offset:10240\n" \
R_"v[108:111], v63 offset:3072\n" \
B_"v52, s[8:11], 0 offen offset:1024 sc1 lds\n" \
"v_mfma_f32_32x32x16_bf16 v[208:223], a[216:219], a[216:219], 0\n" \
R_"a[12:15], v58 offset:3072\n" \
R_"a[28:31], v58 offset:11264\n" \
R_"a[44:47], v59 offset:3072\n" \
R_"a[60:63], v59 offset:11264\n" \
"s_waitcnt lgkmcnt(15)\n" \
"v_xor_b32_e32 v96, 0x80008000, v96\n" \
"v_mfma_f32_32x32x16_bf16 v[224:239], a[216:219], a[216:219], 0\n" \
"v_xor_b32_e32 v97, 0x80008000, v97\n" \
"v_xor_b32_e32 v98, 0x80008000, v98\n" \
"v_xor_b32_e32 v99, 0x80008000, v99\n" \
R_"a[160:163], v20 offset:0\n" \
R_"a[164:167], v20 offset:1024\n" \
"s_waitcnt lgkmcnt(15)\n" \
"v_xor_b32_e32 v100, 0x80008000, v100\n" \
"v_mfma_f32_32x32x16_bf16 v[240:255], a[216:219], a[216:219], 0\n" \
"v_xor_b32_e32 v101, 0x80008000, v101\n" \
"v_xor_b32_e32 v102, 0x80008000, v102\n" \
"v_xor_b32_e32 v103, 0x80008000, v103\n" \
R_"a[168:171], v20 offset:2048\n" \
R_"a[172:175], v20 offset:3072\n" \
R_"a[188:191], v20 offset:7168\n" \
M_"a[192:195], a[216:219], a[216:219], 0\n" \
R_"a[184:187], v20 offset:6144\n" \
"s_waitcnt lgkmcnt(15)\n" \
"v_xor_b32_e32 v104, 0x80008000, v104\n" \
M_"a[196:199], a[216:219], a[216:219], 0\n" \
"v_xor_b32_e32 v105, 0x80008000, v105\n" \
"v_xor_b32_e32 v106, 0x80008000, v106\n" \
M_"v[32:35], v[96:99], a[0:3], 0\n" \
"v_xor_b32_e32 v107, 0x80008000, v107\n" \
R_"a[180:183], v20 offset:5120\n" \
M_"v[36:39], v[96:99], a[16:19], 0\n" \
R_"a[176:179], v20 offset:4096\n" \
"s_waitcnt lgkmcnt(12)\n" \
"v_xor_b32_e32 v108, 0x80008000, v108\n" \
M_"v[40:43], v[96:99], a[32:35], 0\n" \
"v_xor_b32_e32 v109, 0x80008000, v109\n" \
"v_xor_b32_e32 v110, 0x80008000, v110\n" \
M_"v[44:47], v[96:99], a[48:51], 0\n" \
"v_xor_b32_e32 v111, 0x80008000, v111\n" \
"v_add_u32_e32 v60, s15, v20\n" \
M_"v[32:35], v[100:103], a[4:7], v[32:35]\n" \
"v_add_u32_e32 v60, 0x10000, v60\n" \
R_"a[128:131], v20 offset:8192\n" \
M_"v[36:39], v[100:103], a[20:23], v[36:39]\n" \
R_"a[144:147], v20 offset:12288\n" \
R_"a[132:135], v20 offset:9216\n" \
M_"v[40:43], v[100:103], a[36:39], v[40:43]\n" \
R_"a[148:151], v20 offset:13312\n" \
R_"a[136:139], v20 offset:10240\n" \
M_"v[44:47], v[100:103], a[52:55], v[44:47]\n" \
R_"a[152:155], v20 offset:14336\n" \
R_"a[140:143], v20 offset:11264\n" \
M_"v[32:35], v[104:107], a[8:11], v[32:35]\n" \
R_"a[156:159], v20 offset:15360\n" \
R_"a[64:67], v60 offset:16384\n" \
M_"v[36:39], v[104:107], a[24:27], v[36:39]\n" \
R_"a[68:71], v60 offset:17408\n" \
R_"a[72:75], v60 offset:18432\n" \
M_"v[40:43], v[104:107], a[40:43], v[40:43]\n" \
R_"a[76:79], v60 offset:19456\n" \
R_"a[80:83], v60 offset:24576\n" \
M_"v[44:47], v[104:107], a[56:59], v[44:47]\n" \
R_"a[84:87], v60 offset:25600\n" \
"s_waitcnt lgkmcnt(15)\n" \
M_"v[32:35], v[108:111], a[12:15], v[32:35]\n" \
R_"a[88:91], v60 offset:26624\n" \
R_"a[92:95], v60 offset:27648\n" \
M_"v[36:39], v[108:111], a[28:31], v[36:39]\n" \
R_"a[96:99], v60 offset:0\n" \
R_"a[100:103], v60 offset:1024\n" \
M_"v[40:43], v[108:111], a[44:47], v[40:43]\n" \
R_"a[104:107], v60 offset:2048\n" \
R_"a[108:111], v60 offset:3072\n" \
M_"v[44:47], v[108:111], a[60:63], v[44:47]\n" \
R_"a[112:115], v60 offset:8192\n" \
R_"a[116:119], v60 offset:9216\n" \
M_"v[64:67], a[160:163], a[0:3], v[32:35]\n" \
R_"a[120:123], v60 offset:10240\n" \
R_"a[124:127], v60 offset:11264\n" \
M_"v[64:67], a[164:167], a[4:7], v[64:67]\n" \
"v_mov_b32_e32 v56, s62\n" \
"v_readlane_b32 s68, v11, 0\n" \
M_"v[64:67], a[168:171], a[8:11], v[64:67]\n" \
"v_bfe_u32 v26, v20, 8, 2\n" \
"s_mov_b32 m0, s20\n" \
M_"v[64:67], a[172:175], a[12:15], v[64:67]\n" \
"v_bfe_u32 v25, v20, 5, 3\n" \
"v_lshlrev_b32_e32 v26, 1, v26\n" \
M_"v[68:71], a[188:191], a[12:15], v[32:35]\n" \
"global_load_lds_dword v[16:17], off\n" \
M_"v[68:71], a[184:187], a[8:11], v[68:71]\n" \
"v_sub_u32_e32 v25, v25, v26\n" \
"v_and_b32_e32 v26, 16, v20\n" \
M_"v[68:71], a[180:183], a[4:7], v[68:71]\n" \
"v_mov_b32_e32 v27, 0x3f80\n" \
"s_waitcnt lgkmcnt(15)\n" \
M_"v[68:71], a[176:179], a[0:3], v[68:71]\n" \
"v_lshlrev_b32_e32 v26, v26, v27\n" \
"v_mov_b32_e32 v54, 0x3f803f80\n" \
M_"v[76:79], a[176:179], a[16:19], v[36:39]\n" \
"s_mov_b32 s84, 0x33333333\n" \
"s_mov_b32 s85, 0xcccccccc\n" \
M_"v[76:79], a[180:183], a[20:23], v[76:79]\n" \
"s_mov_b32 s86, 0x11111111\n" \
"s_mov_b32 s87, 0x11111111\n" \
M_"v[76:79], a[184:187], a[24:27], v[76:79]\n" \
E_"v70, v70\n" \
M_"v[76:79], a[188:191], a[28:31], v[76:79]\n" \
E_"v71, v71\n" \
M_"v[72:75], a[172:175], a[28:31], v[36:39]\n" \
E_"v68, v68\n" \
M_"v[72:75], a[168:171], a[24:27], v[72:75]\n" \
E_"v69, v69\n" \
M_"v[72:75], a[164:167], a[20:23], v[72:75]\n" \
"s_mov_b32 s88, 0x44444444\n" \
"s_mov_b32 s89, 0x44444444\n" \
M_"v[72:75], a[160:163], a[16:19], v[72:75]\n" \
"v_bfe_i32 v21, v20, 4, 1\n" \
C_"v71, v70, v71\n" \
M_"v[80:83], a[160:163], a[32:35], v[40:43]\n" \
E_"v66, v66\n" \
M_"v[80:83], a[164:167], a[36:39], v[80:83]\n" \
E_"v67, v67\n" \
M_"v[80:83], a[168:171], a[40:43], v[80:83]\n" \
E_"v64, v64\n" \
M_"v[80:83], a[172:175], a[44:47], v[80:83]\n" \
E_"v72, v72\n" \
M_"v[84:87], a[188:191], a[44:47], v[40:43]\n" \
"s_waitcnt vmcnt(3)\n" \
"v_cmp_ne_u32_e32 vcc, s62, v57\n" \
M_"v[84:87], a[184:187], a[40:43], v[84:87]\n" \
E_"v73, v73\n" \
M_"v[84:87], a[180:183], a[36:39], v[84:87]\n" \
E_"v74, v74\n" \
M_"v[84:87], a[176:179], a[32:35], v[84:87]\n" \
"s_cbranch_vccnz L_skb_%=\n" \
"L_rkb_%=:\n" \
"v_cmp_eq_u32_e32 vcc, 0, v25\n" \
"s_mov_b64 exec, 1\n" \
"buffer_store_dword v56, off, s[56:59], s68 offset:0 sc1\n" \
"s_mov_b64 exec, -1\n" \
M_"v[92:95], a[176:179], a[48:51], v[44:47]\n" \
E_"v75, v75\n" \
M_"v[92:95], a[180:183], a[52:55], v[92:95]\n" \
"v_cndmask_b32_e32 v28, 0, v26, vcc\n" \
C_"v72, v72, v73\n" \
M_"v[92:95], a[184:187], a[56:59], v[92:95]\n" \
E_"v65, v65\n" \
M_"v[92:95], a[188:191], a[60:63], v[92:95]\n" \
E_"v76, v76\n" \
M_"v[88:91], a[172:175], a[60:63], v[44:47]\n" \
E_"v77, v77\n" \
M_"v[88:91], a[168:171], a[56:59], v[88:91]\n" \
E_"v78, v78\n" \
M_"v[88:91], a[164:167], a[52:55], v[88:91]\n" \
E_"v79, v79\n" \
M_"v[88:91], a[160:163], a[48:51], v[88:91]\n" \
"v_cndmask_b32_e64 v22, 0, v54, s[84:85]\n" \
"v_cndmask_b32_e64 v23, 0, v54, s[86:87]\n" \
"s_waitcnt vmcnt(5) lgkmcnt(0)\n" \
"s_barrier\n" \
"v_cndmask_b32_e64 v24, 0, v54, s[88:89]\n" \
"v_and_b32_e32 v21, 0xaaaaaaaa, v21\n" \
"v_cmp_eq_u32_e32 vcc, 1, v25\n" \
C_"v70, v68, v69\n" \
C_"v73, v74, v75\n" \
"v_mov_b32_e32 v62, 0\n" \
"v_accvgpr_write_b32 a208, 0\n" \
"v_accvgpr_write_b32 a209, 0\n" \
"v_accvgpr_write_b32 a210, 0\n" \
"v_accvgpr_write_b32 a211, 0\n" \
"v_accvgpr_write_b32 a200, v22\n" \
"v_accvgpr_write_b32 a201, v22\n" \
"v_accvgpr_write_b32 a202, v22\n" \
"v_accvgpr_write_b32 a203, v22\n" \
"v_accvgpr_write_b32 a204, v23\n" \
"v_accvgpr_write_b32 a205, v23\n" \
"v_accvgpr_write_b32 a206, v23\n" \
"v_accvgpr_write_b32 a207, v23\n" \
"v_accvgpr_write_b32 a212, v24\n" \
"v_accvgpr_write_b32 a213, v24\n" \
"v_accvgpr_write_b32 a214, v24\n" \
"v_accvgpr_write_b32 a215, v24\n" \
"v_or_b32_e32 v21, 0x44444444, v21\n" \
"s_mov_b32 s64, 0x8000\n" \
"v_add_u32_e32 v50, 0x6000, v55\n" \
"v_add_u32_e32 v51, 0xa000, v55\n" \
"v_add_u32_e32 v53, 0x10000, v55\n" \
"v_add_u32_e32 v48, 0xc000, v20\n" \
"v_add_u32_e32 v49, s17, v20\n" \
"v_cndmask_b32_e32 v29, 0, v26, vcc\n" \
"v_accvgpr_write_b32 a220, v96\n" \
"v_accvgpr_write_b32 a221, v97\n" \
"v_accvgpr_write_b32 a222, v98\n" \
"v_accvgpr_write_b32 a223, v99\n" \
"v_accvgpr_write_b32 a224, v100\n" \
"v_accvgpr_write_b32 a225, v101\n" \
"v_accvgpr_write_b32 a226, v102\n" \
"v_accvgpr_write_b32 a227, v103\n" \
"v_accvgpr_write_b32 a228, v104\n" \
"v_accvgpr_write_b32 a229, v105\n" \
"v_accvgpr_write_b32 a230, v106\n" \
"v_accvgpr_write_b32 a231, v107\n" \
"v_accvgpr_write_b32 a232, v108\n" \
"v_accvgpr_write_b32 a233, v109\n" \
"v_accvgpr_write_b32 a234, v110\n" \
"v_accvgpr_write_b32 a235, v111\n" \
C_"v69, v66, v67\n" \
C_"v68, v64, v65\n" \
C_"v74, v76, v77\n" \
C_"v75, v78, v79\n" \
"s_sub_u32 s66, s18, 1\n" \
"s_cbranch_scc1 L_tr_%=\n" \
"s_branch L_1_%=\n" \
".fill (L_ab + 1840 - .) / 4, 4, 0xBF800000\n" \
"L_tL_1_%=:\n" \
"s_waitcnt vmcnt(4) lgkmcnt(0)\n" \
"v_smfmac_f32_16x16x64_bf16 a[196:199], a[200:203], v[116:123], v21\n" \
"s_barrier\n" \
"L_1_%=:\n" \
M_"v[96:99], a[128:131], a[0:3], v[32:35]\n" \
"s_xor_b32 m0, s65, 0x12000\n" \
R_"a[160:163], v49 offset:0\n" \
M_"v[96:99], a[132:135], a[4:7], v[96:99]\n" \
B_"v50, s[8:11], s64 offen offset:0 sc1 lds\n" \
M_"v[96:99], a[136:139], a[8:11], v[96:99]\n" \
R_"a[164:167], v49 offset:1024\n" \
R_"a[168:171], v49 offset:2048\n" \
M_"v[96:99], a[140:143], a[12:15], v[96:99]\n" \
R_"a[172:175], v49 offset:3072\n" \
R_"a[176:179], v49 offset:4096\n" \
M_"v[100:103], a[156:159], a[12:15], v[32:35]\n" \
R_"a[180:183], v49 offset:5120\n" \
R_"a[184:187], v49 offset:6144\n" \
M_"v[100:103], a[152:155], a[8:11], v[100:103]\n" \
"v_exp_f32_e64 v86, v86\n" \
M_"v[100:103], a[148:151], a[4:7], v[100:103]\n" \
"v_exp_f32_e64 v87, v87\n" \
M_"v[100:103], a[144:147], a[0:3], v[100:103]\n" \
R_"a[188:191], v49 offset:7168\n" \
C_"v87, v86, v87\n" \
M_"v[108:111], a[144:147], a[16:19], v[36:39]\n" \
"v_exp_f32_e64 v84, v84\n" \
M_"v[108:111], a[148:151], a[20:23], v[108:111]\n" \
"v_exp_f32_e64 v85, v85\n" \
M_"v[108:111], a[152:155], a[24:27], v[108:111]\n" \
"v_exp_f32_e64 v82, v82\n" \
M_"v[108:111], a[156:159], a[28:31], v[108:111]\n" \
"v_exp_f32_e64 v83, v83\n" \
M_"v[104:107], a[140:143], a[28:31], v[36:39]\n" \
"v_exp_f32_e64 v80, v80\n" \
M_"v[104:107], a[136:139], a[24:27], v[104:107]\n" \
"v_exp_f32_e64 v81, v81\n" \
M_"v[104:107], a[132:135], a[20:23], v[104:107]\n" \
"v_exp_f32_e64 v88, v88\n" \
M_"v[104:107], a[128:131], a[16:19], v[104:107]\n" \
B_"v50, s[8:11], s64 offen offset:1024 sc1 lds\n" \
M_"v[112:115], a[128:131], a[32:35], v[40:43]\n" \
"s_xor_b32 m0, s65, 0x6000\n" \
C_"v86, v84, v85\n" \
M_"v[112:115], a[132:135], a[36:39], v[112:115]\n" \
C_"v85, v82, v83\n" \
C_"v84, v80, v81\n" \
M_"v[112:115], a[136:139], a[40:43], v[112:115]\n" \
"v_exp_f32_e64 v89, v89\n" \
M_"v[112:115], a[140:143], a[44:47], v[112:115]\n" \
"v_exp_f32_e64 v90, v90\n" \
M_"v[116:119], a[156:159], a[44:47], v[40:43]\n" \
"v_exp_f32_e64 v91, v91\n" \
M_"v[116:119], a[152:155], a[40:43], v[116:119]\n" \
"v_exp_f32_e64 v92, v92\n" \
M_"v[116:119], a[148:151], a[36:39], v[116:119]\n" \
"v_exp_f32_e64 v93, v93\n" \
M_"v[116:119], a[144:147], a[32:35], v[116:119]\n" \
"v_exp_f32_e64 v94, v94\n" \
M_"v[124:127], a[144:147], a[48:51], v[44:47]\n" \
C_"v88, v88, v89\n" \
C_"v89, v90, v91\n" \
M_"v[124:127], a[148:151], a[52:55], v[124:127]\n" \
"v_exp_f32_e64 v95, v95\n" \
M_"v[124:127], a[152:155], a[56:59], v[124:127]\n" \
C_"v90, v92, v93\n" \
C_"v91, v94, v95\n" \
M_"v[124:127], a[156:159], a[60:63], v[124:127]\n" \
R_"a[144:147], v48 offset:4096\n" \
R_"a[148:151], v48 offset:5120\n" \
M_"v[120:123], a[140:143], a[60:63], v[44:47]\n" \
R_"a[152:155], v48 offset:6144\n" \
R_"a[140:143], v48 offset:3072\n" \
M_"v[120:123], a[136:139], a[56:59], v[120:123]\n" \
R_"a[136:139], v48 offset:2048\n" \
R_"a[156:159], v48 offset:7168\n" \
M_"v[120:123], a[132:135], a[52:55], v[120:123]\n" \
B_"v51, s[8:11], s64 offen offset:0 sc1 lds\n" \
M_"v[120:123], a[128:131], a[48:51], v[120:123]\n" \
R_"a[128:131], v48 offset:0\n" \
"s_waitcnt lgkmcnt(7)\n" \
M_"v[128:131], a[160:163], v[68:71], v[128:131]\n" \
E_"v102, v102\n" \
M_"v[160:163], a[160:163], v[72:75], v[160:163]\n" \
"v_exp_f32_e64 v103, v103\n" \
M_"v[132:135], a[164:167], v[68:71], v[132:135]\n" \
R_"a[132:135], v48 offset:1024\n" \
C_"v103, v102, v103\n" \
M_"v[164:167], a[164:167], v[72:75], v[164:167]\n" \
"v_exp_f32_e64 v100, v100\n" \
M_"v[136:139], a[168:171], v[68:71], v[136:139]\n" \
"v_exp_f32_e64 v101, v101\n" \
M_"v[168:171], a[168:171], v[72:75], v[168:171]\n" \
"v_exp_f32_e64 v98, v98\n" \
M_"v[140:143], a[172:175], v[68:71], v[140:143]\n" \
"v_exp_f32_e64 v99, v99\n" \
M_"v[172:175], a[172:175], v[72:75], v[172:175]\n" \
"v_exp_f32_e64 v96, v96\n" \
M_"v[144:147], a[176:179], v[68:71], v[144:147]\n" \
"v_exp_f32_e64 v97, v97\n" \
M_"v[176:179], a[176:179], v[72:75], v[176:179]\n" \
"v_exp_f32_e64 v104, v104\n" \
M_"v[148:151], a[180:183], v[68:71], v[148:151]\n" \
"v_exp_f32_e64 v105, v105\n" \
M_"v[180:183], a[180:183], v[72:75], v[180:183]\n" \
C_"v102, v100, v101\n" \
C_"v101, v98, v99\n" \
M_"v[152:155], a[184:187], v[68:71], v[152:155]\n" \
C_"v100, v96, v97\n" \
C_"v104, v104, v105\n" \
M_"v[184:187], a[184:187], v[72:75], v[184:187]\n" \
B_"v51, s[8:11], s64 offen offset:1024 sc1 lds\n" \
M_"v[156:159], a[188:191], v[68:71], v[156:159]\n" \
"v_exp_f32_e64 v106, v106\n" \
M_"v[188:191], a[188:191], v[72:75], v[188:191]\n" \
"v_exp_f32_e64 v107, v107\n" \
"v_smfmac_f32_16x16x64_bf16 a[192:195], a[200:203], v[68:75], v21\n" \
"v_exp_f32_e64 v108, v108\n" \
M_"v[220:223], a[188:191], v[84:87], v[220:223]\n" \
"v_exp_f32_e64 v109, v109\n" \
M_"v[252:255], a[188:191], v[88:91], v[252:255]\n" \
"v_exp_f32_e64 v110, v110\n" \
M_"v[216:219], a[184:187], v[84:87], v[216:219]\n" \
"v_exp_f32_e64 v111, v111\n" \
M_"v[248:251], a[184:187], v[88:91], v[248:251]\n" \
"v_exp_f32_e64 v118, v118\n" \
M_"v[212:215], a[180:183], v[84:87], v[212:215]\n" \
"v_exp_f32_e64 v119, v119\n" \
M_"v[244:247], a[180:183], v[88:91], v[244:247]\n" \
C_"v105, v106, v107\n" \
C_"v106, v108, v109\n" \
M_"v[208:211], a[176:179], v[84:87], v[208:211]\n" \
C_"v107, v110, v111\n" \
C_"v119, v118, v119\n" \
M_"v[240:243], a[176:179], v[88:91], v[240:243]\n" \
"v_exp_f32_e64 v116, v116\n" \
M_"v[204:207], a[172:175], v[84:87], v[204:207]\n" \
"v_exp_f32_e64 v117, v117\n" \
M_"v[236:239], a[172:175], v[88:91], v[236:239]\n" \
"v_exp_f32_e64 v114, v114\n" \
M_"v[200:203], a[168:171], v[84:87], v[200:203]\n" \
"v_exp_f32_e64 v115, v115\n" \
M_"v[232:235], a[168:171], v[88:91], v[232:235]\n" \
"v_exp_f32_e64 v112, v112\n" \
M_"v[196:199], a[164:167], v[84:87], v[196:199]\n" \
"v_exp_f32_e64 v113, v113\n" \
M_"v[228:231], a[164:167], v[88:91], v[228:231]\n" \
"v_exp_f32_e64 v120, v120\n" \
M_"v[192:195], a[160:163], v[84:87], v[192:195]\n" \
"v_exp_f32_e64 v121, v121\n" \
M_"v[224:227], a[160:163], v[88:91], v[224:227]\n" \
C_"v118, v116, v117\n" \
"s_waitcnt vmcnt(4) lgkmcnt(0)\n" \
"v_smfmac_f32_16x16x64_bf16 a[196:199], a[200:203], v[84:91], v21\n" \
"s_barrier\n" \
M_"v[64:67], a[128:131], a[0:3], v[32:35]\n" \
"s_xor_b32 m0, s65, 0x14000\n" \
R_"a[160:163], v49 offset:8192\n" \
M_"v[64:67], a[132:135], a[4:7], v[64:67]\n" \
B_"v52, s[8:11], s64 offen offset:0 sc1 lds\n" \
M_"v[64:67], a[136:139], a[8:11], v[64:67]\n" \
R_"a[164:167], v49 offset:9216\n" \
R_"a[168:171], v49 offset:10240\n" \
M_"v[64:67], a[140:143], a[12:15], v[64:67]\n" \
R_"a[172:175], v49 offset:11264\n" \
R_"a[176:179], v49 offset:12288\n" \
M_"v[68:71], a[156:159], a[12:15], v[32:35]\n" \
R_"a[180:183], v49 offset:13312\n" \
R_"a[184:187], v49 offset:14336\n" \
M_"v[68:71], a[152:155], a[8:11], v[68:71]\n" \
R_"a[188:191], v49 offset:15360\n" \
C_"v117, v114, v115\n" \
M_"v[68:71], a[148:151], a[4:7], v[68:71]\n" \
C_"v116, v112, v113\n" \
C_"v120, v120, v121\n" \
M_"v[68:71], a[144:147], a[0:3], v[68:71]\n" \
"v_exp_f32_e64 v122, v122\n" \
M_"v[76:79], a[144:147], a[16:19], v[36:39]\n" \
"v_exp_f32_e64 v123, v123\n" \
M_"v[76:79], a[148:151], a[20:23], v[76:79]\n" \
"v_exp_f32_e64 v124, v124\n" \
M_"v[76:79], a[152:155], a[24:27], v[76:79]\n" \
"v_exp_f32_e64 v125, v125\n" \
M_"v[76:79], a[156:159], a[28:31], v[76:79]\n" \
"v_exp_f32_e64 v126, v126\n" \
M_"v[72:75], a[140:143], a[28:31], v[36:39]\n" \
"v_exp_f32_e64 v127, v127\n" \
M_"v[72:75], a[136:139], a[24:27], v[72:75]\n" \
C_"v121, v122, v123\n" \
C_"v122, v124, v125\n" \
M_"v[72:75], a[132:135], a[20:23], v[72:75]\n" \
C_"v123, v126, v127\n" \
"v_add_u32_e32 v49, 0x10000, v48\n" \
M_"v[72:75], a[128:131], a[16:19], v[72:75]\n" \
B_"v52, s[8:11], s64 offen offset:1024 sc1 lds\n" \
M_"v[80:83], a[128:131], a[32:35], v[40:43]\n" \
"v_exp_f32_e64 v70, v70\n" \
M_"v[80:83], a[132:135], a[36:39], v[80:83]\n" \
"v_exp_f32_e64 v71, v71\n" \
M_"v[80:83], a[136:139], a[40:43], v[80:83]\n" \
"s_mov_b32 m0, s65\n" \
C_"v71, v70, v71\n" \
M_"v[80:83], a[140:143], a[44:47], v[80:83]\n" \
E_"v72, v72\n" \
M_"v[84:87], a[156:159], a[44:47], v[40:43]\n" \
"v_exp_f32_e64 v73, v73\n" \
M_"v[84:87], a[152:155], a[40:43], v[84:87]\n" \
"v_exp_f32_e64 v68, v68\n" \
M_"v[84:87], a[148:151], a[36:39], v[84:87]\n" \
"v_exp_f32_e64 v69, v69\n" \
M_"v[84:87], a[144:147], a[32:35], v[84:87]\n" \
"v_exp_f32_e64 v74, v74\n" \
M_"v[92:95], a[144:147], a[48:51], v[44:47]\n" \
R_"a[144:147], v48 offset:12288\n" \
C_"v72, v72, v73\n" \
M_"v[92:95], a[148:151], a[52:55], v[92:95]\n" \
R_"a[148:151], v48 offset:13312\n" \
C_"v70, v68, v69\n" \
M_"v[92:95], a[152:155], a[56:59], v[92:95]\n" \
R_"a[152:155], v48 offset:14336\n" \
"s_xor_b32 s65, s65, 0x4000\n" \
M_"v[92:95], a[156:159], a[60:63], v[92:95]\n" \
"v_exp_f32_e64 v75, v75\n" \
M_"v[88:91], a[140:143], a[60:63], v[44:47]\n" \
R_"a[140:143], v48 offset:11264\n" \
R_"a[156:159], v48 offset:15360\n" \
M_"v[88:91], a[136:139], a[56:59], v[88:91]\n" \
R_"a[136:139], v48 offset:10240\n" \
C_"v73, v74, v75\n" \
M_"v[88:91], a[132:135], a[52:55], v[88:91]\n" \
B_"v53, s[8:11], s64 offen offset:0 sc1 lds\n" \
M_"v[88:91], a[128:131], a[48:51], v[88:91]\n" \
R_"a[128:131], v48 offset:8192\n" \
"s_waitcnt lgkmcnt(7)\n" \
M_"v[128:131], a[160:163], v[100:103], v[128:131]\n" \
E_"v66, v66\n" \
M_"v[160:163], a[160:163], v[104:107], v[160:163]\n" \
"v_exp_f32_e64 v67, v67\n" \
M_"v[132:135], a[164:167], v[100:103], v[132:135]\n" \
R_"a[132:135], v48 offset:9216\n" \
C_"v69, v66, v67\n" \
M_"v[164:167], a[164:167], v[104:107], v[164:167]\n" \
"v_exp_f32_e64 v64, v64\n" \
M_"v[136:139], a[168:171], v[100:103], v[136:139]\n" \
"v_exp_f32_e64 v65, v65\n" \
M_"v[168:171], a[168:171], v[104:107], v[168:171]\n" \
"v_exp_f32_e64 v76, v76\n" \
M_"v[140:143], a[172:175], v[100:103], v[140:143]\n" \
"v_exp_f32_e64 v77, v77\n" \
M_"v[172:175], a[172:175], v[104:107], v[172:175]\n" \
"v_exp_f32_e64 v78, v78\n" \
M_"v[144:147], a[176:179], v[100:103], v[144:147]\n" \
"v_exp_f32_e64 v79, v79\n" \
M_"v[176:179], a[176:179], v[104:107], v[176:179]\n" \
C_"v68, v64, v65\n" \
C_"v74, v76, v77\n" \
M_"v[148:151], a[180:183], v[100:103], v[148:151]\n" \
C_"v75, v78, v79\n" \
"v_xor_b32_e32 v48, 0x4000, v48\n" \
M_"v[180:183], a[180:183], v[104:107], v[180:183]\n" \
M_"v[152:155], a[184:187], v[100:103], v[152:155]\n" \
M_"v[184:187], a[184:187], v[104:107], v[184:187]\n" \
B_"v53, s[8:11], s64 offen offset:1024 sc1 lds\n" \
M_"v[156:159], a[188:191], v[100:103], v[156:159]\n" \
"s_add_u32 s64, s64, 0x8000\n" \
"s_sub_u32 s66, s66, 1\n" \
"s_nop 0\n" \
M_"v[188:191], a[188:191], v[104:107], v[188:191]\n" \
"v_smfmac_f32_16x16x64_bf16 a[192:195], a[200:203], v[100:107], v21\n" \
M_"v[220:223], a[188:191], v[116:119], v[220:223]\n" \
M_"v[252:255], a[188:191], v[120:123], v[252:255]\n" \
M_"v[216:219], a[184:187], v[116:119], v[216:219]\n" \
M_"v[248:251], a[184:187], v[120:123], v[248:251]\n" \
M_"v[212:215], a[180:183], v[116:119], v[212:215]\n" \
M_"v[244:247], a[180:183], v[120:123], v[244:247]\n" \
M_"v[208:211], a[176:179], v[116:119], v[208:211]\n" \
M_"v[240:243], a[176:179], v[120:123], v[240:243]\n" \
M_"v[204:207], a[172:175], v[116:119], v[204:207]\n" \
M_"v[236:239], a[172:175], v[120:123], v[236:239]\n" \
M_"v[200:203], a[168:171], v[116:119], v[200:203]\n" \
M_"v[232:235], a[168:171], v[120:123], v[232:235]\n" \
M_"v[196:199], a[164:167], v[116:119], v[196:199]\n" \
M_"v[228:231], a[164:167], v[120:123], v[228:231]\n" \
M_"v[192:195], a[160:163], v[116:119], v[192:195]\n" \
M_"v[224:227], a[160:163], v[120:123], v[224:227]\n" \
"s_cbranch_scc0 L_tL_1_%=\n" \
"s_waitcnt vmcnt(4) lgkmcnt(0)\n" \
"v_smfmac_f32_16x16x64_bf16 a[196:199], a[200:203], v[116:123], v21\n" \
"s_barrier\n" \
"L_tr_%=:\n" \
"s_setpc_b64 s[28:29]\n" \
".fill (L_ab + 4096 - .) / 4, 4, 0xBF800000\n" \
".fill (L_ab + 4096 - .) / 4, 4, 0xBF800000\n" \
"L_ap1:\n" \
"s_setpc_b64 s[88:89]\n" \
"L_gtr:\n" \
M_"v[96:99], a[128:131], a[0:3], v[32:35]\n" \
"s_xor_b32 m0, s65, 0x12000\n" \
"s_mov_b32 s65, s13\n" \
M_"v[96:99], a[132:135], a[4:7], v[96:99]\n" \
B_"v50, s[8:11], s64 offen offset:0 sc1 lds\n" \
M_"v[96:99], a[136:139], a[8:11], v[96:99]\n" \
"v_add_u32_e32 v63, s15, v20\n" \
"s_add_u32 s67, s14, 40960\n" \
M_"v[96:99], a[140:143], a[12:15], v[96:99]\n" \
R_"a[160:163], v49 offset:0\n" \
R_"a[164:167], v49 offset:1024\n" \
M_"v[100:103], a[156:159], a[12:15], v[32:35]\n" \
B_"v50, s[8:11], s64 offen offset:1024 sc1 lds\n" \
M_"v[100:103], a[152:155], a[8:11], v[100:103]\n" \
"v_add_u32_e32 v50, 0x6000, v63\n" \
"s_xor_b32 m0, s65, 0x8000\n" \
M_"v[100:103], a[148:151], a[4:7], v[100:103]\n" \
R_"a[168:171], v49 offset:2048\n" \
R_"a[172:175], v49 offset:3072\n" \
M_"v[100:103], a[144:147], a[0:3], v[100:103]\n" \
R_"a[176:179], v49 offset:4096\n" \
R_"a[180:183], v49 offset:5120\n" \
M_"v[108:111], a[144:147], a[16:19], v[36:39]\n" \
B_"v50, s[8:11], s67 offen offset:0 sc1 lds\n" \
M_"v[108:111], a[148:151], a[20:23], v[108:111]\n" \
R_"a[184:187], v49 offset:6144\n" \
R_"a[188:191], v49 offset:7168\n" \
M_"v[108:111], a[152:155], a[24:27], v[108:111]\n" \
"s_mov_b32 s64, s14\n" \
"v_add_u32_e32 v51, 0x12000, v63\n" \
M_"v[108:111], a[156:159], a[28:31], v[108:111]\n" \
E_"v86, v86\n" \
M_"v[104:107], a[140:143], a[28:31], v[36:39]\n" \
B_"v50, s[8:11], s67 offen offset:1024 sc1 lds\n" \
M_"v[104:107], a[136:139], a[24:27], v[104:107]\n" \
E_"v87, v87\n" \
M_"v[104:107], a[132:135], a[20:23], v[104:107]\n" \
E_"v84, v84\n" \
M_"v[104:107], a[128:131], a[16:19], v[104:107]\n" \
E_"v85, v85\n" \
M_"v[112:115], a[128:131], a[32:35], v[40:43]\n" \
C_"v87, v86, v87\n" \
C_"v86, v84, v85\n" \
M_"v[112:115], a[132:135], a[36:39], v[112:115]\n" \
B_"v50, s[8:11], s67 offen offset:2048 sc1 lds\n" \
M_"v[112:115], a[136:139], a[40:43], v[112:115]\n" \
E_"v82, v82\n" \
M_"v[112:115], a[140:143], a[44:47], v[112:115]\n" \
E_"v83, v83\n" \
M_"v[116:119], a[156:159], a[44:47], v[40:43]\n" \
E_"v80, v80\n" \
M_"v[116:119], a[152:155], a[40:43], v[116:119]\n" \
B_"v50, s[8:11], s67 offen offset:3072 sc1 lds\n" \
M_"v[116:119], a[148:151], a[36:39], v[116:119]\n" \
"s_xor_b32 m0, s65, 0xa000\n" \
C_"v85, v82, v83\n" \
M_"v[116:119], a[144:147], a[32:35], v[116:119]\n" \
E_"v81, v81\n" \
M_"v[124:127], a[144:147], a[48:51], v[44:47]\n" \
C_"v84, v80, v81\n" \
"v_readlane_b32 s93, v11, 22\n" \
M_"v[124:127], a[148:151], a[52:55], v[124:127]\n" \
B_"v51, s[8:11], s64 offen offset:0 sc1 lds\n" \
M_"v[124:127], a[152:155], a[56:59], v[124:127]\n" \
E_"v88, v88\n" \
M_"v[124:127], a[156:159], a[60:63], v[124:127]\n" \
E_"v89, v89\n" \
M_"v[120:123], a[140:143], a[60:63], v[44:47]\n" \
E_"v90, v90\n" \
M_"v[120:123], a[136:139], a[56:59], v[120:123]\n" \
E_"v91, v91\n" \
M_"v[120:123], a[132:135], a[52:55], v[120:123]\n" \
B_"v51, s[8:11], s64 offen offset:1024 sc1 lds\n" \
M_"v[120:123], a[128:131], a[48:51], v[120:123]\n" \
C_"v88, v88, v89\n" \
"s_waitcnt lgkmcnt(0)\n" \
M_"v[128:131], a[160:163], v[68:71], v[128:131]\n" \
E_"v92, v92\n" \
M_"v[160:163], a[160:163], v[72:75], v[160:163]\n" \
E_"v93, v93\n" \
M_"v[132:135], a[164:167], v[68:71], v[132:135]\n" \
B_"v51, s[8:11], s64 offen offset:2048 sc1 lds\n" \
M_"v[164:167], a[164:167], v[72:75], v[164:167]\n" \
E_"v94, v94\n" \
M_"v[136:139], a[168:171], v[68:71], v[136:139]\n" \
E_"v95, v95\n" \
M_"v[168:171], a[168:171], v[72:75], v[168:171]\n" \
C_"v89, v90, v91\n" \
C_"v90, v92, v93\n" \
M_"v[140:143], a[172:175], v[68:71], v[140:143]\n" \
B_"v51, s[8:11], s64 offen offset:3072 sc1 lds\n" \
M_"v[172:175], a[172:175], v[72:75], v[172:175]\n" \
C_"v91, v94, v95\n" \
"v_readlane_b32 s80, v11, 11\n" \
M_"v[144:147], a[176:179], v[68:71], v[144:147]\n" \
"v_readlane_b32 s81, v11, 12\n" \
"s_mov_b32 s82, 0x47fca0\n" \
M_"v[176:179], a[176:179], v[72:75], v[176:179]\n" \
"s_mov_b32 s83, 0x20000\n" \
"v_readlane_b32 s92, v11, 29\n" \
M_"v[148:151], a[180:183], v[68:71], v[148:151]\n" \
"s_add_u32 m0, s93, 0x0\n" \
R_"a[128:131], v48 offset:0\n" \
M_"v[180:183], a[180:183], v[72:75], v[180:183]\n" \
R_"a[144:147], v48 offset:4096\n" \
R_"a[132:135], v48 offset:1024\n" \
M_"v[152:155], a[184:187], v[68:71], v[152:155]\n" \
B_"v0, s[80:83], s92 offen offset:0 lds\n" \
M_"v[184:187], a[184:187], v[72:75], v[184:187]\n" \
R_"a[148:151], v48 offset:5120\n" \
R_"a[136:139], v48 offset:2048\n" \
M_"v[156:159], a[188:191], v[68:71], v[156:159]\n" \
R_"a[152:155], v48 offset:6144\n" \
R_"a[140:143], v48 offset:3072\n" \
M_"v[188:191], a[188:191], v[72:75], v[188:191]\n" \
R_"a[156:159], v48 offset:7168\n" \
"v_add_u32_e32 v52, 0x14000, v63\n" \
"v_smfmac_f32_16x16x64_bf16 a[192:195], a[200:203], v[68:75], v21\n" \
B_"v1, s[80:83], s92 offen offset:1024 lds\n" \
M_"v[220:223], a[188:191], v[84:87], v[220:223]\n" \
E_"v102, v102\n" \
M_"v[252:255], a[188:191], v[88:91], v[252:255]\n" \
E_"v103, v103\n" \
M_"v[216:219], a[184:187], v[84:87], v[216:219]\n" \
B_"v2, s[80:83], s92 offen offset:2048 lds\n" \
M_"v[248:251], a[184:187], v[88:91], v[248:251]\n" \
"v_add_u32_e32 v53, 0x20000, v63\n" \
C_"v103, v102, v103\n" \
M_"v[212:215], a[180:183], v[84:87], v[212:215]\n" \
E_"v100, v100\n" \
M_"v[244:247], a[180:183], v[88:91], v[244:247]\n" \
E_"v101, v101\n" \
M_"v[208:211], a[176:179], v[84:87], v[208:211]\n" \
B_"v3, s[80:83], s92 offen offset:3072 lds\n" \
M_"v[240:243], a[176:179], v[88:91], v[240:243]\n" \
E_"v98, v98\n" \
M_"v[204:207], a[172:175], v[84:87], v[204:207]\n" \
E_"v99, v99\n" \
M_"v[236:239], a[172:175], v[88:91], v[236:239]\n" \
E_"v96, v96\n" \
M_"v[200:203], a[168:171], v[84:87], v[200:203]\n" \
E_"v97, v97\n" \
M_"v[232:235], a[168:171], v[88:91], v[232:235]\n" \
E_"v104, v104\n" \
M_"v[196:199], a[164:167], v[84:87], v[196:199]\n" \
E_"v105, v105\n" \
M_"v[228:231], a[164:167], v[88:91], v[228:231]\n" \
C_"v102, v100, v101\n" \
C_"v101, v98, v99\n" \
M_"v[192:195], a[160:163], v[84:87], v[192:195]\n" \
C_"v100, v96, v97\n" \
C_"v104, v104, v105\n" \
M_"v[224:227], a[160:163], v[88:91], v[224:227]\n" \
E_"v106, v106\n" \
"v_smfmac_f32_16x16x64_bf16 a[196:199], a[200:203], v[84:91], v21\n" \
E_"v107, v107\n" \
"s_waitcnt vmcnt(14) lgkmcnt(0)\n" \
"s_barrier\n" \
M_"v[64:67], a[128:131], a[0:3], v[32:35]\n" \
"s_xor_b32 m0, s65, 0x18000\n" \
R_"a[160:163], v49 offset:8192\n" \
M_"v[64:67], a[132:135], a[4:7], v[64:67]\n" \
B_"v52, s[8:11], s64 offen offset:0 sc1 lds\n" \
M_"v[64:67], a[136:139], a[8:11], v[64:67]\n" \
R_"a[164:167], v49 offset:9216\n" \
R_"a[168:171], v49 offset:10240\n" \
M_"v[64:67], a[140:143], a[12:15], v[64:67]\n" \
R_"a[172:175], v49 offset:11264\n" \
R_"a[176:179], v49 offset:12288\n" \
M_"v[68:71], a[156:159], a[12:15], v[32:35]\n" \
B_"v52, s[8:11], s64 offen offset:1024 sc1 lds\n" \
M_"v[68:71], a[152:155], a[8:11], v[68:71]\n" \
R_"a[180:183], v49 offset:13312\n" \
R_"a[184:187], v49 offset:14336\n" \
M_"v[68:71], a[148:151], a[4:7], v[68:71]\n" \
E_"v108, v108\n" \
M_"v[68:71], a[144:147], a[0:3], v[68:71]\n" \
E_"v109, v109\n" \
M_"v[76:79], a[144:147], a[16:19], v[36:39]\n" \
E_"v110, v110\n" \
M_"v[76:79], a[148:151], a[20:23], v[76:79]\n" \
B_"v52, s[8:11], s64 offen offset:2048 sc1 lds\n" \
M_"v[76:79], a[152:155], a[24:27], v[76:79]\n" \
E_"v111, v111\n" \
M_"v[76:79], a[156:159], a[28:31], v[76:79]\n" \
C_"v105, v106, v107\n" \
C_"v106, v108, v109\n" \
M_"v[72:75], a[140:143], a[28:31], v[36:39]\n" \
C_"v107, v110, v111\n" \
R_"a[188:191], v49 offset:15360\n" \
M_"v[72:75], a[136:139], a[24:27], v[72:75]\n" \
B_"v52, s[8:11], s64 offen offset:3072 sc1 lds\n" \
M_"v[72:75], a[132:135], a[20:23], v[72:75]\n" \
"s_mov_b32 m0, s65\n" \
"v_add_u32_e32 v49, 0x10000, v48\n" \
M_"v[72:75], a[128:131], a[16:19], v[72:75]\n" \
E_"v118, v118\n" \
M_"v[80:83], a[128:131], a[32:35], v[40:43]\n" \
E_"v119, v119\n" \
M_"v[80:83], a[132:135], a[36:39], v[80:83]\n" \
E_"v116, v116\n" \
M_"v[80:83], a[136:139], a[40:43], v[80:83]\n" \
B_"v53, s[8:11], s64 offen offset:0 sc1 lds\n" \
M_"v[80:83], a[140:143], a[44:47], v[80:83]\n" \
E_"v117, v117\n" \
M_"v[84:87], a[156:159], a[44:47], v[40:43]\n" \
C_"v119, v118, v119\n" \
C_"v118, v116, v117\n" \
M_"v[84:87], a[152:155], a[40:43], v[84:87]\n" \
E_"v114, v114\n" \
M_"v[84:87], a[148:151], a[36:39], v[84:87]\n" \
E_"v115, v115\n" \
M_"v[84:87], a[144:147], a[32:35], v[84:87]\n" \
B_"v53, s[8:11], s64 offen offset:1024 sc1 lds\n" \
M_"v[92:95], a[144:147], a[48:51], v[44:47]\n" \
E_"v112, v112\n" \
M_"v[92:95], a[148:151], a[52:55], v[92:95]\n" \
E_"v113, v113\n" \
M_"v[92:95], a[152:155], a[56:59], v[92:95]\n" \
C_"v117, v114, v115\n" \
C_"v116, v112, v113\n" \
M_"v[92:95], a[156:159], a[60:63], v[92:95]\n" \
B_"v53, s[8:11], s64 offen offset:2048 sc1 lds\n" \
M_"v[88:91], a[140:143], a[60:63], v[44:47]\n" \
E_"v120, v120\n" \
M_"v[88:91], a[136:139], a[56:59], v[88:91]\n" \
E_"v121, v121\n" \
M_"v[88:91], a[132:135], a[52:55], v[88:91]\n" \
E_"v122, v122\n" \
M_"v[88:91], a[128:131], a[48:51], v[88:91]\n" \
C_"v120, v120, v121\n" \
"s_waitcnt lgkmcnt(0)\n" \
M_"v[128:131], a[160:163], v[100:103], v[128:131]\n" \
B_"v53, s[8:11], s64 offen offset:3072 sc1 lds\n" \
M_"v[160:163], a[160:163], v[104:107], v[160:163]\n" \
"s_add_u32 m0, s93, 0x1000\n" \
R_"a[128:131], v48 offset:8192\n" \
M_"v[132:135], a[164:167], v[100:103], v[132:135]\n" \
E_"v123, v123\n" \
M_"v[164:167], a[164:167], v[104:107], v[164:167]\n" \
E_"v124, v124\n" \
M_"v[136:139], a[168:171], v[100:103], v[136:139]\n" \
B_"v4, s[80:83], s92 offen offset:0 lds\n" \
M_"v[168:171], a[168:171], v[104:107], v[168:171]\n" \
E_"v125, v125\n" \
M_"v[140:143], a[172:175], v[100:103], v[140:143]\n" \
E_"v126, v126\n" \
M_"v[172:175], a[172:175], v[104:107], v[172:175]\n" \
E_"v127, v127\n" \
M_"v[144:147], a[176:179], v[100:103], v[144:147]\n" \
C_"v121, v122, v123\n" \
C_"v122, v124, v125\n" \
M_"v[176:179], a[176:179], v[104:107], v[176:179]\n" \
B_"v5, s[80:83], s92 offen offset:1024 lds\n" \
M_"v[148:151], a[180:183], v[100:103], v[148:151]\n" \
C_"v123, v126, v127\n" \
R_"a[144:147], v48 offset:12288\n" \
M_"v[180:183], a[180:183], v[104:107], v[180:183]\n" \
R_"a[132:135], v48 offset:9216\n" \
R_"a[148:151], v48 offset:13312\n" \
M_"v[152:155], a[184:187], v[100:103], v[152:155]\n" \
R_"a[136:139], v48 offset:10240\n" \
R_"a[152:155], v48 offset:14336\n" \
M_"v[184:187], a[184:187], v[104:107], v[184:187]\n" \
R_"a[140:143], v48 offset:11264\n" \
R_"a[156:159], v48 offset:15360\n" \
M_"v[156:159], a[188:191], v[100:103], v[156:159]\n" \
B_"v6, s[80:83], s92 offen offset:2048 lds\n" \
M_"v[188:191], a[188:191], v[104:107], v[188:191]\n" \
E_"v70, v70\n" \
"v_smfmac_f32_16x16x64_bf16 a[192:195], a[200:203], v[100:107], v21\n" \
E_"v71, v71\n" \
M_"v[220:223], a[188:191], v[116:119], v[220:223]\n" \
E_"v72, v72\n" \
M_"v[252:255], a[188:191], v[120:123], v[252:255]\n" \
B_"v7, s[80:83], s92 offen offset:3072 lds\n" \
M_"v[216:219], a[184:187], v[116:119], v[216:219]\n" \
"s_add_u32 m0, s93, 0x2000\n" \
C_"v71, v70, v71\n" \
M_"v[248:251], a[184:187], v[120:123], v[248:251]\n" \
E_"v73, v73\n" \
M_"v[212:215], a[180:183], v[116:119], v[212:215]\n" \
E_"v68, v68\n" \
M_"v[244:247], a[180:183], v[120:123], v[244:247]\n" \
E_"v69, v69\n" \
M_"v[208:211], a[176:179], v[116:119], v[208:211]\n" \
B_"v8, s[80:83], s92 offen offset:0 lds\n" \
M_"v[240:243], a[176:179], v[120:123], v[240:243]\n" \
E_"v74, v74\n" \
M_"v[204:207], a[172:175], v[116:119], v[204:207]\n" \
E_"v75, v75\n" \
M_"v[236:239], a[172:175], v[120:123], v[236:239]\n" \
E_"v66, v66\n" \
M_"v[200:203], a[168:171], v[116:119], v[200:203]\n" \
E_"v67, v67\n" \
M_"v[232:235], a[168:171], v[120:123], v[232:235]\n" \
E_"v64, v64\n" \
M_"v[196:199], a[164:167], v[116:119], v[196:199]\n" \
E_"v65, v65\n" \
M_"v[228:231], a[164:167], v[120:123], v[228:231]\n" \
C_"v72, v72, v73\n" \
C_"v70, v68, v69\n" \
M_"v[192:195], a[160:163], v[116:119], v[192:195]\n" \
E_"v76, v76\n" \
M_"v[224:227], a[160:163], v[120:123], v[224:227]\n" \
E_"v77, v77\n" \
"v_smfmac_f32_16x16x64_bf16 a[196:199], a[200:203], v[116:123], v21\n" \
E_"v78, v78\n" \
"s_waitcnt vmcnt(13) lgkmcnt(0)\n" \
"s_barrier\n" \
E_"v79, v79\n" \
C_"v73, v74, v75\n" \
C_"v69, v66, v67\n" \
C_"v68, v64, v65\n" \
C_"v74, v76, v77\n" \
C_"v75, v78, v79\n" \
"v_add_u32_e32 v48, s16, v20\n" \
"s_xor_b32 s65, s65, 0x8000\n" \
"s_add_u32 s64, s64, 0x10000\n" \
"s_getpc_b64 s[86:87]\n" \
"L_hpt_%=:\n" \
"s_add_u32 s86, s86, L_ap8-L_hpt_%=\n" \
"s_addc_u32 s87, s87, 0\n" \
"s_add_u32 s86, s86, s15\n" \
"s_addc_u32 s87, s87, 0\n" \
"s_mov_b64 exec, 0\n" \
"s_swappc_b64 s[88:89], s[86:87]\n" \
"s_mov_b64 exec, -1\n" \
"s_sub_u32 s66, s19, 1\n" \
"s_cbranch_scc1 L_la_%=\n" \
"s_branch L_2_%=\n" \
".fill (L_ab + 6400 - .) / 4, 4, 0xBF800000\n" \
"L_tL_2_%=:\n" \
"s_waitcnt vmcnt(8) lgkmcnt(0)\n" \
"v_smfmac_f32_16x16x64_bf16 a[196:199], a[200:203], v[116:123], v21\n" \
"s_barrier\n" \
"L_2_%=:\n" \
M_"v[96:99], a[128:131], a[64:67], v[32:35]\n" \
"s_xor_b32 m0, s65, 0x12000\n" \
R_"a[160:163], v49 offset:0\n" \
M_"v[96:99], a[132:135], a[68:71], v[96:99]\n" \
B_"v50, s[8:11], s64 offen offset:0 sc1 lds\n" \
M_"v[96:99], a[136:139], a[72:75], v[96:99]\n" \
R_"a[164:167], v49 offset:1024\n" \
R_"a[168:171], v49 offset:2048\n" \
M_"v[96:99], a[140:143], a[76:79], v[96:99]\n" \
R_"a[172:175], v49 offset:3072\n" \
R_"a[176:179], v49 offset:4096\n" \
M_"v[100:103], a[156:159], a[76:79], v[32:35]\n" \
R_"a[180:183], v49 offset:5120\n" \
R_"a[184:187], v49 offset:6144\n" \
M_"v[100:103], a[152:155], a[72:75], v[100:103]\n" \
"v_exp_f32_e64 v86, v86\n" \
M_"v[100:103], a[148:151], a[68:71], v[100:103]\n" \
"v_exp_f32_e64 v87, v87\n" \
M_"v[100:103], a[144:147], a[64:67], v[100:103]\n" \
B_"v50, s[8:11], s64 offen offset:1024 sc1 lds\n" \
M_"v[108:111], a[144:147], a[80:83], v[36:39]\n" \
R_"a[188:191], v49 offset:7168\n" \
C_"v87, v86, v87\n" \
M_"v[108:111], a[148:151], a[84:87], v[108:111]\n" \
"v_exp_f32_e64 v84, v84\n" \
M_"v[108:111], a[152:155], a[88:91], v[108:111]\n" \
"v_exp_f32_e64 v85, v85\n" \
M_"v[108:111], a[156:159], a[92:95], v[108:111]\n" \
"v_exp_f32_e64 v82, v82\n" \
M_"v[104:107], a[140:143], a[92:95], v[36:39]\n" \
"v_exp_f32_e64 v83, v83\n" \
M_"v[104:107], a[136:139], a[88:91], v[104:107]\n" \
"v_exp_f32_e64 v80, v80\n" \
M_"v[104:107], a[132:135], a[84:87], v[104:107]\n" \
"v_exp_f32_e64 v81, v81\n" \
M_"v[104:107], a[128:131], a[80:83], v[104:107]\n" \
B_"v50, s[8:11], s64 offen offset:2048 sc1 lds\n" \
M_"v[112:115], a[128:131], a[96:99], v[40:43]\n" \
"v_exp_f32_e64 v88, v88\n" \
M_"v[112:115], a[132:135], a[100:103], v[112:115]\n" \
"v_exp_f32_e64 v89, v89\n" \
M_"v[112:115], a[136:139], a[104:107], v[112:115]\n" \
C_"v86, v84, v85\n" \
C_"v85, v82, v83\n" \
M_"v[112:115], a[140:143], a[108:111], v[112:115]\n" \
C_"v84, v80, v81\n" \
C_"v88, v88, v89\n" \
M_"v[116:119], a[156:159], a[108:111], v[40:43]\n" \
"v_exp_f32_e64 v90, v90\n" \
M_"v[116:119], a[152:155], a[104:107], v[116:119]\n" \
"v_exp_f32_e64 v91, v91\n" \
M_"v[116:119], a[148:151], a[100:103], v[116:119]\n" \
B_"v50, s[8:11], s64 offen offset:3072 sc1 lds\n" \
M_"v[116:119], a[144:147], a[96:99], v[116:119]\n" \
"s_xor_b32 m0, s65, 0xa000\n" \
C_"v89, v90, v91\n" \
M_"v[124:127], a[144:147], a[112:115], v[44:47]\n" \
"v_exp_f32_e64 v92, v92\n" \
M_"v[124:127], a[148:151], a[116:119], v[124:127]\n" \
"v_exp_f32_e64 v93, v93\n" \
M_"v[124:127], a[152:155], a[120:123], v[124:127]\n" \
"v_exp_f32_e64 v94, v94\n" \
M_"v[124:127], a[156:159], a[124:127], v[124:127]\n" \
"v_exp_f32_e64 v95, v95\n" \
M_"v[120:123], a[140:143], a[124:127], v[44:47]\n" \
C_"v90, v92, v93\n" \
C_"v91, v94, v95\n" \
M_"v[120:123], a[136:139], a[120:123], v[120:123]\n" \
R_"a[144:147], v48 offset:4096\n" \
R_"a[148:151], v48 offset:5120\n" \
M_"v[120:123], a[132:135], a[116:119], v[120:123]\n" \
B_"v51, s[8:11], s64 offen offset:0 sc1 lds\n" \
M_"v[120:123], a[128:131], a[112:115], v[120:123]\n" \
R_"a[128:131], v48 offset:0\n" \
"s_waitcnt lgkmcnt(3)\n" \
M_"v[128:131], a[160:163], v[68:71], v[128:131]\n" \
R_"a[132:135], v48 offset:1024\n" \
R_"a[136:139], v48 offset:2048\n" \
M_"v[160:163], a[160:163], v[72:75], v[160:163]\n" \
R_"a[152:155], v48 offset:6144\n" \
R_"a[140:143], v48 offset:3072\n" \
M_"v[132:135], a[164:167], v[68:71], v[132:135]\n" \
E_"v102, v102\n" \
M_"v[164:167], a[164:167], v[72:75], v[164:167]\n" \
"v_exp_f32_e64 v103, v103\n" \
M_"v[136:139], a[168:171], v[68:71], v[136:139]\n" \
R_"a[156:159], v48 offset:7168\n" \
C_"v103, v102, v103\n" \
M_"v[168:171], a[168:171], v[72:75], v[168:171]\n" \
B_"v51, s[8:11], s64 offen offset:1024 sc1 lds\n" \
M_"v[140:143], a[172:175], v[68:71], v[140:143]\n" \
"v_exp_f32_e64 v100, v100\n" \
M_"v[172:175], a[172:175], v[72:75], v[172:175]\n" \
"v_exp_f32_e64 v101, v101\n" \
M_"v[144:147], a[176:179], v[68:71], v[144:147]\n" \
"v_exp_f32_e64 v98, v98\n" \
M_"v[176:179], a[176:179], v[72:75], v[176:179]\n" \
"v_exp_f32_e64 v99, v99\n" \
M_"v[148:151], a[180:183], v[68:71], v[148:151]\n" \
"v_exp_f32_e64 v96, v96\n" \
M_"v[180:183], a[180:183], v[72:75], v[180:183]\n" \
"v_exp_f32_e64 v97, v97\n" \
M_"v[152:155], a[184:187], v[68:71], v[152:155]\n" \
"v_exp_f32_e64 v104, v104\n" \
M_"v[184:187], a[184:187], v[72:75], v[184:187]\n" \
B_"v51, s[8:11], s64 offen offset:2048 sc1 lds\n" \
M_"v[156:159], a[188:191], v[68:71], v[156:159]\n" \
"v_exp_f32_e64 v105, v105\n" \
M_"v[188:191], a[188:191], v[72:75], v[188:191]\n" \
C_"v102, v100, v101\n" \
C_"v101, v98, v99\n" \
"v_smfmac_f32_16x16x64_bf16 a[192:195], a[200:203], v[68:75], v21\n" \
C_"v100, v96, v97\n" \
C_"v104, v104, v105\n" \
M_"v[220:223], a[188:191], v[84:87], v[220:223]\n" \
"v_exp_f32_e64 v106, v106\n" \
M_"v[252:255], a[188:191], v[88:91], v[252:255]\n" \
"v_exp_f32_e64 v107, v107\n" \
M_"v[216:219], a[184:187], v[84:87], v[216:219]\n" \
"v_exp_f32_e64 v108, v108\n" \
M_"v[248:251], a[184:187], v[88:91], v[248:251]\n" \
B_"v51, s[8:11], s64 offen offset:3072 sc1 lds\n" \
M_"v[212:215], a[180:183], v[84:87], v[212:215]\n" \
"v_exp_f32_e64 v109, v109\n" \
M_"v[244:247], a[180:183], v[88:91], v[244:247]\n" \
"v_exp_f32_e64 v110, v110\n" \
M_"v[208:211], a[176:179], v[84:87], v[208:211]\n" \
"v_exp_f32_e64 v111, v111\n" \
M_"v[240:243], a[176:179], v[88:91], v[240:243]\n" \
"v_exp_f32_e64 v118, v118\n" \
M_"v[204:207], a[172:175], v[84:87], v[204:207]\n" \
"v_exp_f32_e64 v119, v119\n" \
M_"v[236:239], a[172:175], v[88:91], v[236:239]\n" \
C_"v105, v106, v107\n" \
C_"v106, v108, v109\n" \
M_"v[200:203], a[168:171], v[84:87], v[200:203]\n" \
C_"v107, v110, v111\n" \
C_"v119, v118, v119\n" \
M_"v[232:235], a[168:171], v[88:91], v[232:235]\n" \
"v_exp_f32_e64 v116, v116\n" \
M_"v[196:199], a[164:167], v[84:87], v[196:199]\n" \
"v_exp_f32_e64 v117, v117\n" \
M_"v[228:231], a[164:167], v[88:91], v[228:231]\n" \
"v_exp_f32_e64 v114, v114\n" \
M_"v[192:195], a[160:163], v[84:87], v[192:195]\n" \
"v_exp_f32_e64 v115, v115\n" \
M_"v[224:227], a[160:163], v[88:91], v[224:227]\n" \
C_"v118, v116, v117\n" \
"s_waitcnt vmcnt(8) lgkmcnt(0)\n" \
"v_smfmac_f32_16x16x64_bf16 a[196:199], a[200:203], v[84:91], v21\n" \
"s_barrier\n" \
M_"v[64:67], a[128:131], a[64:67], v[32:35]\n" \
"s_xor_b32 m0, s65, 0x18000\n" \
R_"a[160:163], v49 offset:8192\n" \
M_"v[64:67], a[132:135], a[68:71], v[64:67]\n" \
B_"v52, s[8:11], s64 offen offset:0 sc1 lds\n" \
M_"v[64:67], a[136:139], a[72:75], v[64:67]\n" \
R_"a[164:167], v49 offset:9216\n" \
R_"a[168:171], v49 offset:10240\n" \
M_"v[64:67], a[140:143], a[76:79], v[64:67]\n" \
R_"a[172:175], v49 offset:11264\n" \
R_"a[176:179], v49 offset:12288\n" \
M_"v[68:71], a[156:159], a[76:79], v[32:35]\n" \
R_"a[180:183], v49 offset:13312\n" \
R_"a[184:187], v49 offset:14336\n" \
M_"v[68:71], a[152:155], a[72:75], v[68:71]\n" \
R_"a[188:191], v49 offset:15360\n" \
C_"v117, v114, v115\n" \
M_"v[68:71], a[148:151], a[68:71], v[68:71]\n" \
"v_exp_f32_e64 v112, v112\n" \
M_"v[68:71], a[144:147], a[64:67], v[68:71]\n" \
B_"v52, s[8:11], s64 offen offset:1024 sc1 lds\n" \
M_"v[76:79], a[144:147], a[80:83], v[36:39]\n" \
"v_exp_f32_e64 v113, v113\n" \
M_"v[76:79], a[148:151], a[84:87], v[76:79]\n" \
C_"v116, v112, v113\n" \
"v_add_u32_e32 v49, 0x10000, v48\n" \
M_"v[76:79], a[152:155], a[88:91], v[76:79]\n" \
"v_exp_f32_e64 v120, v120\n" \
M_"v[76:79], a[156:159], a[92:95], v[76:79]\n" \
"v_exp_f32_e64 v121, v121\n" \
M_"v[72:75], a[140:143], a[92:95], v[36:39]\n" \
"v_exp_f32_e64 v122, v122\n" \
M_"v[72:75], a[136:139], a[88:91], v[72:75]\n" \
"v_exp_f32_e64 v123, v123\n" \
M_"v[72:75], a[132:135], a[84:87], v[72:75]\n" \
"v_exp_f32_e64 v124, v124\n" \
M_"v[72:75], a[128:131], a[80:83], v[72:75]\n" \
B_"v52, s[8:11], s64 offen offset:2048 sc1 lds\n" \
M_"v[80:83], a[128:131], a[96:99], v[40:43]\n" \
"v_exp_f32_e64 v125, v125\n" \
M_"v[80:83], a[132:135], a[100:103], v[80:83]\n" \
"v_exp_f32_e64 v126, v126\n" \
M_"v[80:83], a[136:139], a[104:107], v[80:83]\n" \
"v_exp_f32_e64 v127, v127\n" \
M_"v[80:83], a[140:143], a[108:111], v[80:83]\n" \
"v_exp_f32_e64 v70, v70\n" \
M_"v[84:87], a[156:159], a[108:111], v[40:43]\n" \
"v_exp_f32_e64 v71, v71\n" \
M_"v[84:87], a[152:155], a[104:107], v[84:87]\n" \
C_"v120, v120, v121\n" \
C_"v121, v122, v123\n" \
M_"v[84:87], a[148:151], a[100:103], v[84:87]\n" \
B_"v52, s[8:11], s64 offen offset:3072 sc1 lds\n" \
M_"v[84:87], a[144:147], a[96:99], v[84:87]\n" \
"s_mov_b32 m0, s65\n" \
"s_nop 0\n" \
C_"v122, v124, v125\n" \
M_"v[92:95], a[144:147], a[112:115], v[44:47]\n" \
C_"v123, v126, v127\n" \
R_"a[144:147], v48 offset:12288\n" \
M_"v[92:95], a[148:151], a[116:119], v[92:95]\n" \
R_"a[148:151], v48 offset:13312\n" \
C_"v71, v70, v71\n" \
M_"v[92:95], a[152:155], a[120:123], v[92:95]\n" \
R_"a[152:155], v48 offset:14336\n" \
"s_xor_b32 s65, s65, 0x8000\n" \
M_"v[92:95], a[156:159], a[124:127], v[92:95]\n" \
"v_exp_f32_e64 v72, v72\n" \
M_"v[88:91], a[140:143], a[124:127], v[44:47]\n" \
R_"a[140:143], v48 offset:11264\n" \
R_"a[156:159], v48 offset:15360\n" \
M_"v[88:91], a[136:139], a[120:123], v[88:91]\n" \
"v_exp_f32_e64 v73, v73\n" \
M_"v[88:91], a[132:135], a[116:119], v[88:91]\n" \
".fill (L_ab + 8192 - .) / 4, 4, 0xBF800000\n" \
"L_ap2:\n" \
"s_cbranch_execz L_ap0\n" \
B_"v53, s[8:11], s64 offen offset:0 sc1 lds\n" \
M_"v[88:91], a[128:131], a[112:115], v[88:91]\n" \
R_"a[128:131], v48 offset:8192\n" \
"s_waitcnt lgkmcnt(6)\n" \
M_"v[128:131], a[160:163], v[100:103], v[128:131]\n" \
R_"a[132:135], v48 offset:9216\n" \
R_"a[136:139], v48 offset:10240\n" \
M_"v[160:163], a[160:163], v[104:107], v[160:163]\n" \
"v_exp_f32_e64 v68, v68\n" \
M_"v[132:135], a[164:167], v[100:103], v[132:135]\n" \
"v_exp_f32_e64 v69, v69\n" \
M_"v[164:167], a[164:167], v[104:107], v[164:167]\n" \
"v_exp_f32_e64 v74, v74\n" \
M_"v[136:139], a[168:171], v[100:103], v[136:139]\n" \
"v_exp_f32_e64 v75, v75\n" \
M_"v[168:171], a[168:171], v[104:107], v[168:171]\n" \
B_"v53, s[8:11], s64 offen offset:1024 sc1 lds\n" \
M_"v[140:143], a[172:175], v[100:103], v[140:143]\n" \
"v_exp_f32_e64 v66, v66\n" \
M_"v[172:175], a[172:175], v[104:107], v[172:175]\n" \
"v_exp_f32_e64 v67, v67\n" \
M_"v[144:147], a[176:179], v[100:103], v[144:147]\n" \
"v_exp_f32_e64 v64, v64\n" \
M_"v[176:179], a[176:179], v[104:107], v[176:179]\n" \
"v_exp_f32_e64 v65, v65\n" \
M_"v[148:151], a[180:183], v[100:103], v[148:151]\n" \
C_"v72, v72, v73\n" \
C_"v70, v68, v69\n" \
M_"v[180:183], a[180:183], v[104:107], v[180:183]\n" \
"v_exp_f32_e64 v76, v76\n" \
M_"v[152:155], a[184:187], v[100:103], v[152:155]\n" \
"v_exp_f32_e64 v77, v77\n" \
M_"v[184:187], a[184:187], v[104:107], v[184:187]\n" \
B_"v53, s[8:11], s64 offen offset:2048 sc1 lds\n" \
M_"v[156:159], a[188:191], v[100:103], v[156:159]\n" \
"v_exp_f32_e64 v78, v78\n" \
M_"v[188:191], a[188:191], v[104:107], v[188:191]\n" \
"v_exp_f32_e64 v79, v79\n" \
"v_smfmac_f32_16x16x64_bf16 a[192:195], a[200:203], v[100:107], v21\n" \
C_"v73, v74, v75\n" \
C_"v69, v66, v67\n" \
M_"v[220:223], a[188:191], v[116:119], v[220:223]\n" \
C_"v68, v64, v65\n" \
C_"v74, v76, v77\n" \
M_"v[252:255], a[188:191], v[120:123], v[252:255]\n" \
C_"v75, v78, v79\n" \
"v_xor_b32_e32 v48, 0x8000, v48\n" \
M_"v[216:219], a[184:187], v[116:119], v[216:219]\n" \
M_"v[248:251], a[184:187], v[120:123], v[248:251]\n" \
B_"v53, s[8:11], s64 offen offset:3072 sc1 lds\n" \
M_"v[212:215], a[180:183], v[116:119], v[212:215]\n" \
"s_add_u32 s64, s64, 0x10000\n" \
"s_sub_u32 s66, s66, 1\n" \
"s_nop 0\n" \
M_"v[244:247], a[180:183], v[120:123], v[244:247]\n" \
M_"v[208:211], a[176:179], v[116:119], v[208:211]\n" \
M_"v[240:243], a[176:179], v[120:123], v[240:243]\n" \
M_"v[204:207], a[172:175], v[116:119], v[204:207]\n" \
M_"v[236:239], a[172:175], v[120:123], v[236:239]\n" \
M_"v[200:203], a[168:171], v[116:119], v[200:203]\n" \
M_"v[232:235], a[168:171], v[120:123], v[232:235]\n" \
M_"v[196:199], a[164:167], v[116:119], v[196:199]\n" \
M_"v[228:231], a[164:167], v[120:123], v[228:231]\n" \
M_"v[192:195], a[160:163], v[116:119], v[192:195]\n" \
M_"v[224:227], a[160:163], v[120:123], v[224:227]\n" \
"s_cbranch_scc0 L_tL_2_%=\n" \
"s_waitcnt vmcnt(8) lgkmcnt(0)\n" \
"v_smfmac_f32_16x16x64_bf16 a[196:199], a[200:203], v[116:123], v21\n" \
"s_barrier\n" \
"L_la_%=:\n" \
M_"v[96:99], a[128:131], a[64:67], v[32:35]\n" \
"v_readlane_b32 s96, v11, 1\n" \
"v_lshrrev_b32_e32 v16, 1, v20\n" \
M_"v[96:99], a[132:135], a[68:71], v[96:99]\n" \
"s_xor_b32 m0, s65, 0x12000\n" \
R_"a[160:163], v49 offset:0\n" \
M_"v[96:99], a[136:139], a[72:75], v[96:99]\n" \
"buffer_load_dwordx2 v[26:27], v16, s[56:59], s96 offen sc1\n" \
M_"v[96:99], a[140:143], a[76:79], v[96:99]\n" \
B_"v50, s[8:11], s64 offen offset:0 sc1 lds\n" \
M_"v[100:103], a[156:159], a[76:79], v[32:35]\n" \
R_"a[164:167], v49 offset:1024\n" \
R_"a[168:171], v49 offset:2048\n" \
M_"v[100:103], a[152:155], a[72:75], v[100:103]\n" \
R_"a[172:175], v49 offset:3072\n" \
R_"a[176:179], v49 offset:4096\n" \
M_"v[100:103], a[148:151], a[68:71], v[100:103]\n" \
R_"a[180:183], v49 offset:5120\n" \
R_"a[184:187], v49 offset:6144\n" \
M_"v[100:103], a[144:147], a[64:67], v[100:103]\n" \
R_"a[188:191], v49 offset:7168\n" \
"s_mov_b32 s63, s62\n" \
M_"v[108:111], a[144:147], a[80:83], v[36:39]\n" \
E_"v86, v86\n" \
M_"v[108:111], a[148:151], a[84:87], v[108:111]\n" \
"v_exp_f32_e64 v87, v87\n" \
M_"v[108:111], a[152:155], a[88:91], v[108:111]\n" \
B_"v50, s[8:11], s64 offen offset:1024 sc1 lds\n" \
M_"v[108:111], a[156:159], a[92:95], v[108:111]\n" \
"v_exp_f32_e64 v84, v84\n" \
M_"v[104:107], a[140:143], a[92:95], v[36:39]\n" \
"v_exp_f32_e64 v85, v85\n" \
M_"v[104:107], a[136:139], a[88:91], v[104:107]\n" \
C_"v87, v86, v87\n" \
C_"v86, v84, v85\n" \
M_"v[104:107], a[132:135], a[84:87], v[104:107]\n" \
"v_exp_f32_e64 v82, v82\n" \
M_"v[104:107], a[128:131], a[80:83], v[104:107]\n" \
"v_exp_f32_e64 v83, v83\n" \
M_"v[112:115], a[128:131], a[96:99], v[40:43]\n" \
"v_exp_f32_e64 v80, v80\n" \
M_"v[112:115], a[132:135], a[100:103], v[112:115]\n" \
"v_exp_f32_e64 v81, v81\n" \
M_"v[112:115], a[136:139], a[104:107], v[112:115]\n" \
B_"v50, s[8:11], s64 offen offset:2048 sc1 lds\n" \
M_"v[112:115], a[140:143], a[108:111], v[112:115]\n" \
C_"v85, v82, v83\n" \
C_"v84, v80, v81\n" \
M_"v[116:119], a[156:159], a[108:111], v[40:43]\n" \
"v_exp_f32_e64 v88, v88\n" \
M_"v[116:119], a[152:155], a[104:107], v[116:119]\n" \
"v_exp_f32_e64 v89, v89\n" \
M_"v[116:119], a[148:151], a[100:103], v[116:119]\n" \
"v_exp_f32_e64 v90, v90\n" \
M_"v[116:119], a[144:147], a[96:99], v[116:119]\n" \
"v_exp_f32_e64 v91, v91\n" \
M_"v[124:127], a[144:147], a[112:115], v[44:47]\n" \
C_"v88, v88, v89\n" \
C_"v89, v90, v91\n" \
M_"v[124:127], a[148:151], a[116:119], v[124:127]\n" \
B_"v50, s[8:11], s64 offen offset:3072 sc1 lds\n" \
M_"v[124:127], a[152:155], a[120:123], v[124:127]\n" \
"v_exp_f32_e64 v92, v92\n" \
M_"v[124:127], a[156:159], a[124:127], v[124:127]\n" \
"v_exp_f32_e64 v93, v93\n" \
M_"v[120:123], a[140:143], a[124:127], v[44:47]\n" \
"v_exp_f32_e64 v94, v94\n" \
M_"v[120:123], a[136:139], a[120:123], v[120:123]\n" \
"v_exp_f32_e64 v95, v95\n" \
M_"v[120:123], a[132:135], a[116:119], v[120:123]\n" \
C_"v90, v92, v93\n" \
C_"v91, v94, v95\n" \
M_"v[120:123], a[128:131], a[112:115], v[120:123]\n" \
"v_readlane_b32 s97, v11, 39\n" \
"s_waitcnt lgkmcnt(0)\n" \
"s_nop 0\n" \
M_"v[128:131], a[160:163], v[68:71], v[128:131]\n" \
R_"a[128:131], v48 offset:0\n" \
R_"a[144:147], v48 offset:4096\n" \
M_"v[160:163], a[160:163], v[72:75], v[160:163]\n" \
R_"a[132:135], v48 offset:1024\n" \
R_"a[148:151], v48 offset:5120\n" \
M_"v[132:135], a[164:167], v[68:71], v[132:135]\n" \
R_"a[136:139], v48 offset:2048\n" \
R_"a[152:155], v48 offset:6144\n" \
M_"v[164:167], a[164:167], v[72:75], v[164:167]\n" \
R_"a[140:143], v48 offset:3072\n" \
R_"a[156:159], v48 offset:7168\n" \
M_"v[136:139], a[168:171], v[68:71], v[136:139]\n" \
"v_add_u32_e32 v17, 0x49000, v20\n" \
"v_readlane_b32 s98, v11, 40\n" \
M_"v[168:171], a[168:171], v[72:75], v[168:171]\n" \
"v_readlane_b32 s93, v11, 23\n" \
"v_readlane_b32 s84, v11, 15\n" \
M_"v[140:143], a[172:175], v[68:71], v[140:143]\n" \
"v_readlane_b32 s85, v11, 16\n" \
"v_readlane_b32 s86, v11, 17\n" \
M_"v[172:175], a[172:175], v[72:75], v[172:175]\n" \
"s_mov_b32 s87, 0x20000\n" \
"v_readlane_b32 s92, v11, 18\n" \
M_"v[144:147], a[176:179], v[68:71], v[144:147]\n" \
"v_exp_f32_e64 v102, v102\n" \
M_"v[176:179], a[176:179], v[72:75], v[176:179]\n" \
"v_exp_f32_e64 v103, v103\n" \
M_"v[148:151], a[180:183], v[68:71], v[148:151]\n" \
"v_exp_f32_e64 v100, v100\n" \
M_"v[180:183], a[180:183], v[72:75], v[180:183]\n" \
"v_exp_f32_e64 v101, v101\n" \
M_"v[152:155], a[184:187], v[68:71], v[152:155]\n" \
C_"v103, v102, v103\n" \
C_"v102, v100, v101\n" \
M_"v[184:187], a[184:187], v[72:75], v[184:187]\n" \
"v_exp_f32_e64 v98, v98\n" \
M_"v[156:159], a[188:191], v[68:71], v[156:159]\n" \
"v_exp_f32_e64 v99, v99\n" \
M_"v[188:191], a[188:191], v[72:75], v[188:191]\n" \
"s_waitcnt vmcnt(4)\n" \
"v_cmp_ne_u64_e32 vcc, s[62:63], v[26:27]\n" \
"v_smfmac_f32_16x16x64_bf16 a[192:195], a[200:203], v[68:75], v21\n" \
"v_exp_f32_e64 v96, v96\n" \
M_"v[220:223], a[188:191], v[84:87], v[220:223]\n" \
"v_exp_f32_e64 v97, v97\n" \
M_"v[252:255], a[188:191], v[88:91], v[252:255]\n" \
"s_cbranch_vccnz L_sol_%=\n" \
"L_rol_%=:\n" \
C_"v101, v98, v99\n" \
B_"a[220:223], v20, s[56:59], s97 offen offset:0 sc1\n" \
M_"v[216:219], a[184:187], v[84:87], v[216:219]\n" \
C_"v100, v96, v97\n" \
"v_readlane_b32 s91, v11, 24\n" \
M_"v[248:251], a[184:187], v[88:91], v[248:251]\n" \
E_"v104, v104\n" \
M_"v[212:215], a[180:183], v[84:87], v[212:215]\n" \
"v_exp_f32_e64 v105, v105\n" \
M_"v[244:247], a[180:183], v[88:91], v[244:247]\n" \
"v_exp_f32_e64 v106, v106\n" \
M_"v[208:211], a[176:179], v[84:87], v[208:211]\n" \
"v_exp_f32_e64 v107, v107\n" \
M_"v[240:243], a[176:179], v[88:91], v[240:243]\n" \
B_"a[228:231], v17, s[56:59], s97 offen offset:0 sc1\n" \
M_"v[204:207], a[172:175], v[84:87], v[204:207]\n" \
C_"v104, v104, v105\n" \
C_"v105, v106, v107\n" \
M_"v[236:239], a[172:175], v[88:91], v[236:239]\n" \
"v_exp_f32_e64 v108, v108\n" \
M_"v[200:203], a[168:171], v[84:87], v[200:203]\n" \
"v_exp_f32_e64 v109, v109\n" \
M_"v[232:235], a[168:171], v[88:91], v[232:235]\n" \
"v_exp_f32_e64 v110, v110\n" \
M_"v[196:199], a[164:167], v[84:87], v[196:199]\n" \
"v_exp_f32_e64 v111, v111\n" \
M_"v[228:231], a[164:167], v[88:91], v[228:231]\n" \
C_"v106, v108, v109\n" \
C_"v107, v110, v111\n" \
M_"v[192:195], a[160:163], v[84:87], v[192:195]\n" \
"v_exp_f32_e64 v118, v118\n" \
M_"v[224:227], a[160:163], v[88:91], v[224:227]\n" \
"s_xor_b32 s65, s65, 0x8000\n" \
"s_waitcnt vmcnt(7) lgkmcnt(0)\n" \
"v_smfmac_f32_16x16x64_bf16 a[196:199], a[200:203], v[84:91], v21\n" \
"s_barrier\n" \
M_"v[64:67], a[128:131], a[64:67], v[32:35]\n" \
B_"a[236:239], v20, s[56:59], s98 offen offset:0 sc1\n" \
M_"v[64:67], a[132:135], a[68:71], v[64:67]\n" \
"s_add_u32 m0, s93, 0x0\n" \
"s_nop 0\n" \
R_"a[160:163], v49 offset:8192\n" \
M_"v[64:67], a[136:139], a[72:75], v[64:67]\n" \
B_"v0, s[84:87], s92 offen offset:0 lds\n" \
M_"v[64:67], a[140:143], a[76:79], v[64:67]\n" \
R_"a[164:167], v49 offset:9216\n" \
R_"a[168:171], v49 offset:10240\n" \
M_"v[68:71], a[156:159], a[76:79], v[32:35]\n" \
R_"a[172:175], v49 offset:11264\n" \
R_"a[176:179], v49 offset:12288\n" \
M_"v[68:71], a[152:155], a[72:75], v[68:71]\n" \
R_"a[180:183], v49 offset:13312\n" \
R_"a[184:187], v49 offset:14336\n" \
M_"v[68:71], a[148:151], a[68:71], v[68:71]\n" \
B_"v1, s[84:87], s92 offen offset:1024 lds\n" \
M_"v[68:71], a[144:147], a[64:67], v[68:71]\n" \
B_"a[244:247], v17, s[56:59], s98 offen offset:0 sc1\n" \
M_"v[76:79], a[144:147], a[80:83], v[36:39]\n" \
R_"a[188:191], v49 offset:15360\n" \
"v_add_u32_e32 v49, 0x10000, v48\n" \
M_"v[76:79], a[148:151], a[84:87], v[76:79]\n" \
"v_exp_f32_e64 v119, v119\n" \
M_"v[76:79], a[152:155], a[88:91], v[76:79]\n" \
"v_exp_f32_e64 v116, v116\n" \
M_"v[76:79], a[156:159], a[92:95], v[76:79]\n" \
"v_exp_f32_e64 v117, v117\n" \
M_"v[72:75], a[140:143], a[92:95], v[36:39]\n" \
C_"v119, v118, v119\n" \
C_"v118, v116, v117\n" \
M_"v[72:75], a[136:139], a[88:91], v[72:75]\n" \
B_"v2, s[84:87], s92 offen offset:2048 lds\n" \
M_"v[72:75], a[132:135], a[84:87], v[72:75]\n" \
"v_exp_f32_e64 v114, v114\n" \
M_"v[72:75], a[128:131], a[80:83], v[72:75]\n" \
"v_exp_f32_e64 v115, v115\n" \
M_"v[80:83], a[128:131], a[96:99], v[40:43]\n" \
B_"a[224:227], v20, s[56:59], s97 offen offset:1024 sc1\n" \
M_"v[80:83], a[132:135], a[100:103], v[80:83]\n" \
"v_exp_f32_e64 v112, v112\n" \
M_"v[80:83], a[136:139], a[104:107], v[80:83]\n" \
"v_exp_f32_e64 v113, v113\n" \
M_"v[80:83], a[140:143], a[108:111], v[80:83]\n" \
C_"v117, v114, v115\n" \
C_"v116, v112, v113\n" \
M_"v[84:87], a[156:159], a[108:111], v[40:43]\n" \
B_"v3, s[84:87], s92 offen offset:3072 lds\n" \
M_"v[84:87], a[152:155], a[104:107], v[84:87]\n" \
"s_add_u32 m0, s91, 0x1000\n" \
"s_add_u32 s64, s64, 0x10000\n" \
M_"v[84:87], a[148:151], a[100:103], v[84:87]\n" \
"v_exp_f32_e64 v120, v120\n" \
M_"v[84:87], a[144:147], a[96:99], v[84:87]\n" \
"v_exp_f32_e64 v121, v121\n" \
M_"v[92:95], a[144:147], a[112:115], v[44:47]\n" \
"v_exp_f32_e64 v122, v122\n" \
M_"v[92:95], a[148:151], a[116:119], v[92:95]\n" \
B_"a[232:235], v17, s[56:59], s97 offen offset:1024 sc1\n" \
M_"v[92:95], a[152:155], a[120:123], v[92:95]\n" \
B_"v4, s[84:87], s92 offen offset:0 lds\n" \
M_"v[92:95], a[156:159], a[124:127], v[92:95]\n" \
"v_exp_f32_e64 v123, v123\n" \
M_"v[88:91], a[140:143], a[124:127], v[44:47]\n" \
C_"v120, v120, v121\n" \
C_"v121, v122, v123\n" \
M_"v[88:91], a[136:139], a[120:123], v[88:91]\n" \
"v_exp_f32_e64 v124, v124\n" \
M_"v[88:91], a[132:135], a[116:119], v[88:91]\n" \
"v_exp_f32_e64 v125, v125\n" \
M_"v[88:91], a[128:131], a[112:115], v[88:91]\n" \
C_"v122, v124, v125\n" \
"s_waitcnt lgkmcnt(0)\n" \
M_"v[128:131], a[160:163], v[100:103], v[128:131]\n" \
E_"v126, v126\n" \
M_"v[160:163], a[160:163], v[104:107], v[160:163]\n" \
B_"v5, s[84:87], s92 offen offset:1024 lds\n" \
M_"v[132:135], a[164:167], v[100:103], v[132:135]\n" \
B_"a[240:243], v20, s[56:59], s98 offen offset:1024 sc1\n" \
M_"v[164:167], a[164:167], v[104:107], v[164:167]\n" \
"v_exp_f32_e64 v127, v127\n" \
M_"v[136:139], a[168:171], v[100:103], v[136:139]\n" \
C_"v123, v126, v127\n" \
R_"a[128:131], v48 offset:8192\n" \
M_"v[168:171], a[168:171], v[104:107], v[168:171]\n" \
R_"a[144:147], v48 offset:12288\n" \
R_"a[132:135], v48 offset:9216\n" \
M_"v[140:143], a[172:175], v[100:103], v[140:143]\n" \
R_"a[148:151], v48 offset:13312\n" \
R_"a[136:139], v48 offset:10240\n" \
M_"v[172:175], a[172:175], v[104:107], v[172:175]\n" \
R_"a[152:155], v48 offset:14336\n" \
R_"a[140:143], v48 offset:11264\n" \
M_"v[144:147], a[176:179], v[100:103], v[144:147]\n" \
B_"v6, s[84:87], s92 offen offset:2048 lds\n" \
M_"v[176:179], a[176:179], v[104:107], v[176:179]\n" \
"v_exp_f32_e64 v70, v70\n" \
M_"v[148:151], a[180:183], v[100:103], v[148:151]\n" \
"v_exp_f32_e64 v71, v71\n" \
M_"v[180:183], a[180:183], v[104:107], v[180:183]\n" \
B_"a[248:251], v17, s[56:59], s98 offen offset:1024 sc1\n" \
M_"v[152:155], a[184:187], v[100:103], v[152:155]\n" \
R_"a[156:159], v48 offset:15360\n" \
C_"v71, v70, v71\n" \
M_"v[184:187], a[184:187], v[104:107], v[184:187]\n" \
"v_exp_f32_e64 v72, v72\n" \
M_"v[156:159], a[188:191], v[100:103], v[156:159]\n" \
B_"v7, s[84:87], s92 offen offset:3072 lds\n" \
M_"v[188:191], a[188:191], v[104:107], v[188:191]\n" \
"s_add_u32 m0, s91, 0x2000\n" \
"v_xor_b32_e32 v48, 0x8000, v48\n" \
"v_smfmac_f32_16x16x64_bf16 a[192:195], a[200:203], v[100:107], v21\n" \
"v_exp_f32_e64 v73, v73\n" \
M_"v[220:223], a[188:191], v[116:119], v[220:223]\n" \
"v_exp_f32_e64 v68, v68\n" \
M_"v[252:255], a[188:191], v[120:123], v[252:255]\n" \
"v_exp_f32_e64 v69, v69\n" \
M_"v[216:219], a[184:187], v[116:119], v[216:219]\n" \
"v_exp_f32_e64 v74, v74\n" \
M_"v[248:251], a[184:187], v[120:123], v[248:251]\n" \
"v_exp_f32_e64 v75, v75\n" \
M_"v[212:215], a[180:183], v[116:119], v[212:215]\n" \
B_"v8, s[84:87], s92 offen offset:0 lds\n" \
M_"v[244:247], a[180:183], v[120:123], v[244:247]\n" \
"v_exp_f32_e64 v66, v66\n" \
M_"v[208:211], a[176:179], v[116:119], v[208:211]\n" \
"v_exp_f32_e64 v67, v67\n" \
M_"v[240:243], a[176:179], v[120:123], v[240:243]\n" \
"v_exp_f32_e64 v64, v64\n" \
M_"v[204:207], a[172:175], v[116:119], v[204:207]\n" \
"v_exp_f32_e64 v65, v65\n" \
M_"v[236:239], a[172:175], v[120:123], v[236:239]\n" \
C_"v72, v72, v73\n" \
C_"v70, v68, v69\n" \
M_"v[200:203], a[168:171], v[116:119], v[200:203]\n" \
"v_exp_f32_e64 v76, v76\n" \
M_"v[232:235], a[168:171], v[120:123], v[232:235]\n" \
"v_exp_f32_e64 v77, v77\n" \
M_"v[196:199], a[164:167], v[116:119], v[196:199]\n" \
"v_exp_f32_e64 v78, v78\n" \
M_"v[228:231], a[164:167], v[120:123], v[228:231]\n" \
"v_exp_f32_e64 v79, v79\n" \
M_"v[192:195], a[160:163], v[116:119], v[192:195]\n" \
C_"v73, v74, v75\n" \
C_"v69, v66, v67\n" \
M_"v[224:227], a[160:163], v[120:123], v[224:227]\n" \
C_"v68, v64, v65\n" \
"s_waitcnt vmcnt(15) lgkmcnt(0)\n" \
"v_smfmac_f32_16x16x64_bf16 a[196:199], a[200:203], v[116:123], v21\n" \
"s_barrier\n" \
C_"v74, v76, v77\n" \
C_"v75, v78, v79\n" \
"s_cmp_lg_u32 s16, 0\n" \
"s_cbranch_scc1 L_f1_%=\n" \
R_"a[160:163], v49 offset:0\n" \
R_"a[164:167], v49 offset:1024\n" \
R_"a[168:171], v49 offset:2048\n" \
R_"a[172:175], v49 offset:3072\n" \
R_"a[176:179], v49 offset:4096\n" \
R_"a[180:183], v49 offset:5120\n" \
R_"a[184:187], v49 offset:6144\n" \
R_"a[188:191], v49 offset:7168\n" \
"v_add_u32_e32 v54, 0x49000, v20\n" \
B_"a[96:99], v20, s[56:59], s60 offen offset:0 sc1\n" \
"v_add_u32_e32 v50, s15, v20\n" \
"v_add_u32_e32 v51, 0x10000, v50\n" \
"ds_read_b32 v60, v18 offset:0\n" \
"ds_read_b32 v61, v18 offset:128\n" \
R_"a[0:3], v49 offset:8192\n" \
R_"a[4:7], v49 offset:9216\n" \
R_"a[8:11], v49 offset:10240\n" \
R_"a[12:15], v49 offset:11264\n" \
R_"a[16:19], v49 offset:12288\n" \
R_"a[20:23], v49 offset:13312\n" \
R_"a[24:27], v49 offset:14336\n" \
R_"a[28:31], v49 offset:15360\n" \
E_"v86, v86\n" \
E_"v87, v87\n" \
E_"v84, v84\n" \
E_"v85, v85\n" \
C_"v87, v86, v87\n" \
B_"a[100:103], v20, s[56:59], s60 offen offset:1024 sc1\n" \
E_"v82, v82\n" \
E_"v83, v83\n" \
E_"v80, v80\n" \
E_"v81, v81\n" \
"s_waitcnt lgkmcnt(15)\n" \
M_"v[128:131], a[160:163], v[68:71], v[128:131]\n" \
C_"v86, v84, v85\n" \
C_"v85, v82, v83\n" \
M_"v[160:163], a[160:163], v[72:75], v[160:163]\n" \
C_"v84, v80, v81\n" \
"v_and_b32_e32 v84, v12, v84\n" \
M_"v[132:135], a[164:167], v[68:71], v[132:135]\n" \
"v_and_b32_e32 v85, v13, v85\n" \
"v_and_b32_e32 v86, v14, v86\n" \
M_"v[164:167], a[164:167], v[72:75], v[164:167]\n" \
B_"a[104:107], v20, s[56:59], s60 offen offset:2048 sc1\n" \
M_"v[136:139], a[168:171], v[68:71], v[136:139]\n" \
C_"v128, v128, v129\n" \
C_"v160, v160, v161\n" \
M_"v[168:171], a[168:171], v[72:75], v[168:171]\n" \
C_"v129, v130, v131\n" \
"s_waitcnt lgkmcnt(10)\n" \
M_"v[140:143], a[172:175], v[68:71], v[140:143]\n" \
C_"v161, v162, v163\n" \
C_"v130, v132, v133\n" \
M_"v[172:175], a[172:175], v[72:75], v[172:175]\n" \
C_"v131, v134, v135\n" \
C_"v162, v164, v165\n" \
M_"v[144:147], a[176:179], v[68:71], v[144:147]\n" \
C_"v163, v166, v167\n" \
C_"v132, v136, v137\n" \
M_"v[176:179], a[176:179], v[72:75], v[176:179]\n" \
B_"a[108:111], v20, s[56:59], s60 offen offset:3072 sc1\n" \
M_"v[148:151], a[180:183], v[68:71], v[148:151]\n" \
C_"v133, v138, v139\n" \
C_"v134, v140, v141\n" \
M_"v[180:183], a[180:183], v[72:75], v[180:183]\n" \
C_"v135, v142, v143\n" \
C_"v164, v168, v169\n" \
M_"v[152:155], a[184:187], v[68:71], v[152:155]\n" \
C_"v165, v170, v171\n" \
C_"v166, v172, v173\n" \
M_"v[184:187], a[184:187], v[72:75], v[184:187]\n" \
C_"v167, v174, v175\n" \
C_"v136, v144, v145\n" \
M_"v[156:159], a[188:191], v[68:71], v[156:159]\n" \
C_"v137, v146, v147\n" \
C_"v138, v148, v149\n" \
M_"v[188:191], a[188:191], v[72:75], v[188:191]\n" \
C_"v139, v150, v151\n" \
C_"v140, v152, v153\n" \
"v_smfmac_f32_16x16x64_bf16 a[192:195], a[200:203], v[68:75], v21\n" \
C_"v141, v154, v155\n" \
C_"v168, v176, v177\n" \
M_"v[120:123], a[128:131], a[112:115], v[44:47]\n" \
C_"v142, v156, v157\n" \
C_"v143, v158, v159\n" \
M_"v[120:123], a[132:135], a[116:119], v[120:123]\n" \
C_"v169, v178, v179\n" \
C_"v170, v180, v181\n" \
C_"v171, v182, v183\n" \
C_"v172, v184, v185\n" \
C_"v173, v186, v187\n" \
C_"v174, v188, v189\n" \
C_"v175, v190, v191\n" \
"ds_write_b128 v50, v[128:131] offset:0\n" \
M_"v[120:123], a[136:139], a[120:123], v[120:123]\n" \
"v_and_b32_e32 v87, v15, v87\n" \
"v_accvgpr_read_b32 v40, a192\n" \
M_"v[120:123], a[140:143], a[124:127], v[120:123]\n" \
"ds_write_b128 v50, v[132:135] offset:1024\n" \
M_"v[124:127], a[156:159], a[124:127], v[44:47]\n" \
B_"a[124:127], v54, s[56:59], s60 offen offset:3072 sc1\n" \
M_"v[124:127], a[152:155], a[120:123], v[124:127]\n" \
"ds_write_b128 v50, v[136:139] offset:2048\n" \
M_"v[124:127], a[148:151], a[116:119], v[124:127]\n" \
"v_accvgpr_read_b32 v22, a193\n" \
"v_accvgpr_read_b32 v41, a194\n" \
M_"v[124:127], a[144:147], a[112:115], v[124:127]\n" \
"ds_write_b128 v50, v[140:143] offset:3072\n" \
M_"a[196:199], a[204:207], v[84:87], a[196:199]\n" \
"v_accvgpr_read_b32 v23, a195\n" \
"v_add_f32_e32 v40, v40, v22\n" \
M_"v[192:195], a[160:163], v[84:87], v[192:195]\n" \
"ds_write_b128 v51, v[160:163] offset:32768\n" \
M_"v[196:199], a[164:167], v[84:87], v[196:199]\n" \
B_"a[112:115], v54, s[56:59], s60 offen offset:0 sc1\n" \
M_"v[200:203], a[168:171], v[84:87], v[200:203]\n" \
"ds_write_b128 v51, v[164:167] offset:33792\n" \
"v_add_f32_e32 v41, v41, v23\n" \
"ds_write_b64 v19, v[40:41]\n" \
M_"v[204:207], a[172:175], v[84:87], v[204:207]\n" \
"v_lshl_add_u32 v52, s15, 2, v20\n" \
"v_add_u32_e32 v52, 0x1a000, v52\n" \
"ds_write_b128 v51, v[168:171] offset:34816\n" \
M_"v[208:211], a[176:179], v[84:87], v[208:211]\n" \
E_"v88, v88\n" \
M_"v[212:215], a[180:183], v[84:87], v[212:215]\n" \
"ds_write_b128 v51, v[172:175] offset:35840\n" \
M_"v[216:219], a[184:187], v[84:87], v[216:219]\n" \
B_"a[116:119], v54, s[56:59], s60 offen offset:1024 sc1\n" \
M_"v[220:223], a[188:191], v[84:87], v[220:223]\n" \
E_"v89, v89\n" \
E_"v90, v90\n" \
E_"v91, v91\n" \
C_"v88, v88, v89\n" \
E_"v92, v92\n" \
E_"v93, v93\n" \
E_"v94, v94\n" \
E_"v95, v95\n" \
C_"v89, v90, v91\n" \
C_"v90, v92, v93\n" \
C_"v91, v94, v95\n" \
"v_xor_b32_e32 v63, 0x400, v19\n" \
B_"a[120:123], v54, s[56:59], s60 offen offset:2048 sc1\n" \
E_"v120, v120\n" \
E_"v121, v121\n" \
E_"v122, v122\n" \
E_"v123, v123\n" \
C_"v120, v120, v121\n" \
E_"v124, v124\n" \
E_"v125, v125\n" \
"s_waitcnt lgkmcnt(0)\n" \
"s_barrier\n" \
R_"v[144:147], v52 offset:0\n" \
R_"v[148:151], v52 offset:1024\n" \
R_"v[152:155], v52 offset:2048\n" \
".fill (L_ab + 12288 - .) / 4, 4, 0xBF800000\n" \
"L_ap3:\n" \
"s_cbranch_execz L_ap0\n" \
R_"v[156:159], v52 offset:3072\n" \
R_"v[176:179], v52 offset:4096\n" \
R_"v[180:183], v52 offset:5120\n" \
R_"v[184:187], v52 offset:6144\n" \
R_"v[188:191], v52 offset:7168\n" \
M_"a[196:199], a[212:215], v[88:91], a[196:199]\n" \
"ds_read_b64 v[42:43], v63\n" \
C_"v121, v122, v123\n" \
M_"v[224:227], a[160:163], v[88:91], v[224:227]\n" \
E_"v126, v126\n" \
M_"v[228:231], a[164:167], v[88:91], v[228:231]\n" \
E_"v127, v127\n" \
M_"v[232:235], a[168:171], v[88:91], v[232:235]\n" \
C_"v122, v124, v125\n" \
C_"v123, v126, v127\n" \
M_"v[236:239], a[172:175], v[88:91], v[236:239]\n" \
"v_and_b32_e32 v120, v12, v120\n" \
"v_and_b32_e32 v121, v13, v121\n" \
M_"v[240:243], a[176:179], v[88:91], v[240:243]\n" \
"v_and_b32_e32 v122, v14, v122\n" \
"v_and_b32_e32 v123, v15, v123\n" \
M_"v[244:247], a[180:183], v[88:91], v[244:247]\n" \
"v_readlane_b32 s93, v11, 25\n" \
"v_readlane_b32 s92, v11, 30\n" \
M_"v[248:251], a[184:187], v[88:91], v[248:251]\n" \
"s_add_u32 m0, s93, 0x0\n" \
"v_mov_b32_e32 v130, v28\n" \
M_"v[252:255], a[188:191], v[88:91], v[252:255]\n" \
"v_mov_b32_e32 v131, v29\n" \
"v_mov_b32_e32 v132, 0\n" \
"v_mov_b32_e32 v133, 0\n" \
"v_mov_b32_e32 v128, 0\n" \
"v_mov_b32_e32 v129, 0\n" \
"s_waitcnt vmcnt(8) lgkmcnt(1)\n" \
"s_barrier\n" \
M_"a[196:199], a[212:215], v[120:123], a[196:199]\n" \
B_"v0, s[80:83], s92 offen offset:0 lds\n" \
M_"v[224:227], a[0:3], v[120:123], v[224:227]\n" \
M_"v[228:231], a[4:7], v[120:123], v[228:231]\n" \
M_"v[232:235], a[8:11], v[120:123], v[232:235]\n" \
M_"v[236:239], a[12:15], v[120:123], v[236:239]\n" \
M_"v[240:243], a[16:19], v[120:123], v[240:243]\n" \
M_"v[244:247], a[20:23], v[120:123], v[244:247]\n" \
B_"v1, s[80:83], s92 offen offset:1024 lds\n" \
M_"v[248:251], a[24:27], v[120:123], v[248:251]\n" \
"v_accvgpr_read_b32 v56, a196\n" \
"v_accvgpr_read_b32 v24, a197\n" \
M_"v[252:255], a[28:31], v[120:123], v[252:255]\n" \
"v_add_f32_e32 v56, v56, v24\n" \
"s_waitcnt lgkmcnt(0)\n" \
"v_add_f32_e32 v56, v56, v42\n" \
M_"v[192:195], v[130:133], v[144:147], v[192:195]\n" \
"v_max_f32_e32 v62, v62, v56\n" \
"v_accvgpr_read_b32 v57, a198\n" \
M_"v[196:199], v[128:131], v[144:147], v[196:199]\n" \
"v_rcp_f32_e32 v56, v56\n" \
M_"v[200:203], v[130:133], v[148:151], v[200:203]\n" \
"v_mul_f32_e32 v56, v56, v60\n" \
"v_accvgpr_read_b32 v25, a199\n" \
B_"v2, s[80:83], s92 offen offset:2048 lds\n" \
M_"v[204:207], v[128:131], v[148:151], v[204:207]\n" \
"v_mul_f32_e32 v192, v56, v192\n" \
"v_mul_f32_e32 v193, v56, v193\n" \
M_"v[208:211], v[130:133], v[152:155], v[208:211]\n" \
"v_mul_f32_e32 v194, v56, v194\n" \
"v_mul_f32_e32 v195, v56, v195\n" \
M_"v[212:215], v[128:131], v[152:155], v[212:215]\n" \
"v_mul_f32_e32 v196, v56, v196\n" \
"v_mul_f32_e32 v197, v56, v197\n" \
M_"v[216:219], v[130:133], v[156:159], v[216:219]\n" \
"v_mul_f32_e32 v198, v56, v198\n" \
"v_mul_f32_e32 v199, v56, v199\n" \
M_"v[220:223], v[128:131], v[156:159], v[220:223]\n" \
C_"v64, v192, v193\n" \
C_"v65, v194, v195\n" \
M_"v[224:227], v[130:133], v[176:179], v[224:227]\n" \
B_"v3, s[80:83], s92 offen offset:3072 lds\n" \
"s_add_u32 m0, s93, 0x1000\n" \
M_"v[228:231], v[128:131], v[176:179], v[228:231]\n" \
C_"v66, v196, v197\n" \
C_"v67, v198, v199\n" \
M_"v[232:235], v[130:133], v[180:183], v[232:235]\n" \
"v_mul_f32_e32 v200, v56, v200\n" \
"v_mul_f32_e32 v201, v56, v201\n" \
M_"v[236:239], v[128:131], v[180:183], v[236:239]\n" \
M_"a[208:211], a[216:219], v[64:67], a[208:211]\n" \
"v_mul_f32_e32 v202, v56, v202\n" \
"v_mul_f32_e32 v203, v56, v203\n" \
M_"v[240:243], v[130:133], v[184:187], v[240:243]\n" \
"v_mul_f32_e32 v204, v56, v204\n" \
"v_mul_f32_e32 v205, v56, v205\n" \
B_"v4, s[80:83], s92 offen offset:0 lds\n" \
"v_mul_f32_e32 v206, v56, v206\n" \
"v_mul_f32_e32 v207, v56, v207\n" \
"v_add_f32_e32 v57, v57, v25\n" \
C_"v68, v200, v201\n" \
C_"v69, v202, v203\n" \
C_"v70, v204, v205\n" \
C_"v71, v206, v207\n" \
"v_add_f32_e32 v57, v57, v43\n" \
"v_mul_f32_e32 v208, v56, v208\n" \
M_"a[208:211], a[216:219], v[68:71], a[208:211]\n" \
"v_mul_f32_e32 v209, v56, v209\n" \
"v_mul_f32_e32 v210, v56, v210\n" \
"v_mul_f32_e32 v211, v56, v211\n" \
"v_mul_f32_e32 v212, v56, v212\n" \
"v_mul_f32_e32 v213, v56, v213\n" \
"v_mul_f32_e32 v214, v56, v214\n" \
"v_mul_f32_e32 v215, v56, v215\n" \
"v_max_f32_e32 v62, v62, v57\n" \
C_"v72, v208, v209\n" \
C_"v73, v210, v211\n" \
B_"v5, s[80:83], s92 offen offset:1024 lds\n" \
C_"v74, v212, v213\n" \
C_"v75, v214, v215\n" \
"v_rcp_f32_e32 v57, v57\n" \
M_"v[244:247], v[128:131], v[184:187], v[244:247]\n" \
M_"a[208:211], a[216:219], v[72:75], a[208:211]\n" \
"v_mul_f32_e32 v216, v56, v216\n" \
"v_mul_f32_e32 v217, v56, v217\n" \
"v_mul_f32_e32 v218, v56, v218\n" \
"v_mul_f32_e32 v219, v56, v219\n" \
"v_mul_f32_e32 v220, v56, v220\n" \
"v_mul_f32_e32 v221, v56, v221\n" \
"v_mul_f32_e32 v222, v56, v222\n" \
"v_mul_f32_e32 v223, v56, v223\n" \
C_"v76, v216, v217\n" \
C_"v77, v218, v219\n" \
C_"v78, v220, v221\n" \
B_"v6, s[80:83], s92 offen offset:2048 lds\n" \
C_"v79, v222, v223\n" \
"v_mul_f32_e32 v57, v57, v61\n" \
"v_mul_f32_e32 v224, v57, v224\n" \
M_"a[208:211], a[216:219], v[76:79], a[208:211]\n" \
"v_mul_f32_e32 v225, v57, v225\n" \
"v_mul_f32_e32 v226, v57, v226\n" \
"v_mul_f32_e32 v227, v57, v227\n" \
"v_mul_f32_e32 v228, v57, v228\n" \
"v_mul_f32_e32 v229, v57, v229\n" \
"v_mul_f32_e32 v230, v57, v230\n" \
"v_mul_f32_e32 v231, v57, v231\n" \
C_"v80, v224, v225\n" \
C_"v81, v226, v227\n" \
C_"v82, v228, v229\n" \
C_"v83, v230, v231\n" \
M_"v[248:251], v[130:133], v[188:191], v[248:251]\n" \
"v_mul_f32_e32 v232, v57, v232\n" \
"v_mul_f32_e32 v233, v57, v233\n" \
M_"a[208:211], a[216:219], v[80:83], a[208:211]\n" \
B_"v7, s[80:83], s92 offen offset:3072 lds\n" \
"s_add_u32 m0, s93, 0x2000\n" \
"v_mul_f32_e32 v234, v57, v234\n" \
"v_mul_f32_e32 v235, v57, v235\n" \
"v_mul_f32_e32 v236, v57, v236\n" \
"v_mul_f32_e32 v237, v57, v237\n" \
"v_mul_f32_e32 v238, v57, v238\n" \
"v_mul_f32_e32 v239, v57, v239\n" \
C_"v84, v232, v233\n" \
C_"v85, v234, v235\n" \
C_"v86, v236, v237\n" \
C_"v87, v238, v239\n" \
"v_mul_f32_e32 v240, v57, v240\n" \
"v_mul_f32_e32 v241, v57, v241\n" \
M_"a[208:211], a[216:219], v[84:87], a[208:211]\n" \
"v_mul_f32_e32 v242, v57, v242\n" \
"v_mul_f32_e32 v243, v57, v243\n" \
"v_mul_f32_e32 v244, v57, v244\n" \
"v_mul_f32_e32 v245, v57, v245\n" \
"v_mul_f32_e32 v246, v57, v246\n" \
"v_mul_f32_e32 v247, v57, v247\n" \
B_"v8, s[80:83], s92 offen offset:0 lds\n" \
C_"v96, v240, v241\n" \
C_"v97, v242, v243\n" \
C_"v98, v244, v245\n" \
C_"v99, v246, v247\n" \
M_"v[252:255], v[128:131], v[188:191], v[252:255]\n" \
"v_mul_f32_e32 v248, v57, v248\n" \
M_"a[208:211], a[216:219], v[96:99], a[208:211]\n" \
"v_mul_f32_e32 v249, v57, v249\n" \
"v_mul_f32_e32 v250, v57, v250\n" \
"v_mul_f32_e32 v251, v57, v251\n" \
C_"v100, v248, v249\n" \
C_"v101, v250, v251\n" \
"v_cmp_lt_f32_e64 s[72:73], s21, v62\n" \
"v_mul_f32_e32 v252, v57, v252\n" \
"v_mul_f32_e32 v253, v57, v253\n" \
"v_mul_f32_e32 v254, v57, v254\n" \
"v_mul_f32_e32 v255, v57, v255\n" \
C_"v102, v252, v253\n" \
C_"v103, v254, v255\n" \
"s_nop 1\n" \
M_"a[208:211], a[216:219], v[100:103], a[208:211]\n" \
"s_nop 7\n" \
"v_accvgpr_read_b32 v63, a208\n" \
"v_cmp_u_f32_e32 vcc, v63, v63\n" \
"s_nop 0\n" \
"s_or_b64 vcc, s[72:73], vcc\n" \
"s_cbranch_scc1 L_end_%=\n" \
"s_branch L_out_%=\n" \
"L_skb_%=:\n" \
"s_sleep 1\n" \
"buffer_load_dword v57, v61, s[56:59], s96 offen sc1\n" \
"s_add_u32 s101, s101, 1\n" \
"s_waitcnt vmcnt(0)\n" \
"v_cmp_ne_u32_e32 vcc, s62, v57\n" \
"s_cmp_gt_u32 s101, 0x100000\n" \
"s_cbranch_scc1 L_rkb_%=\n" \
"s_nop 2\n" \
"s_cbranch_vccnz L_skb_%=\n" \
"s_branch L_rkb_%=\n" \
"L_sol_%=:\n" \
"s_sleep 1\n" \
"buffer_load_dwordx2 v[26:27], v16, s[56:59], s96 offen sc1\n" \
"s_add_u32 s101, s101, 1\n" \
"s_waitcnt vmcnt(0)\n" \
"v_cmp_ne_u64_e32 vcc, s[62:63], v[26:27]\n" \
"s_cmp_gt_u32 s101, 0x100000\n" \
"s_cbranch_scc1 L_rol_%=\n" \
"s_nop 2\n" \
"s_cbranch_vccnz L_sol_%=\n" \
"s_branch L_rol_%=\n" \
"L_sos_%=:\n" \
"s_sleep 1\n" \
"buffer_load_dwordx2 v[26:27], v16, s[56:59], s96 offen sc1\n" \
"s_add_u32 s101, s101, 1\n" \
"s_waitcnt vmcnt(0)\n" \
"v_cmp_ne_u64_e32 vcc, s[62:63], v[26:27]\n" \
"s_cmp_gt_u32 s101, 0x100000\n" \
"s_cbranch_scc1 L_ros_%=\n" \
"s_nop 2\n" \
"s_cbranch_vccnz L_sos_%=\n" \
"s_branch L_ros_%=\n" \
"L_sa_%=:\n" \
"s_sleep 1\n" \
"buffer_load_dword v26, v38, s[56:59], s64 offen sc1\n" \
"s_add_u32 s101, s101, 1\n" \
"s_waitcnt vmcnt(0)\n" \
"v_cmp_ne_u32_e32 vcc, s62, v26\n" \
"s_cmp_gt_u32 s101, 0x100000\n" \
"s_cbranch_scc1 L_ra_%=\n" \
"s_nop 2\n" \
"s_cbranch_vccnz L_sa_%=\n" \
"s_branch L_ra_%=\n" \
"L_sb_%=:\n" \
"s_sleep 1\n" \
"buffer_load_dword v47, v38, s[56:59], s71 offen sc1\n" \
"s_add_u32 s101, s101, 1\n" \
"s_waitcnt vmcnt(0)\n" \
"v_cmp_ne_u32_e32 vcc, s62, v47\n" \
"s_cmp_gt_u32 s101, 0x100000\n" \
"s_cbranch_scc1 L_rb_%=\n" \
"s_nop 2\n" \
"s_cbranch_vccnz L_sb_%=\n" \
"s_branch L_rb_%=\n" \
"L_sna_%=:\n" \
"buffer_load_dword v37, v40, s[56:59], s74 offen sc1\n" \
"s_add_u32 s101, s101, 1\n" \
"s_waitcnt vmcnt(0)\n" \
"v_cmp_eq_u32_e64 s[98:99], s62, v37\n" \
"s_cmp_gt_u32 s101, 0x100000\n" \
"s_cbranch_scc1 L_rna_%=\n" \
"s_andn2_b32 s75, s20, s98\n" \
"s_cbranch_scc0 L_gna_%=\n" \
"s_sleep 1\n" \
"s_branch L_sna_%=\n" \
"L_gna_%=:\n" \
"s_bitcmp1_b32 s20, 0\n" \
"s_cbranch_scc0 L_kna0_%=\n" \
B_"v[116:119], v20, s[56:59], s12 offen offset:0 sc1\n" \
"L_kna0_%=:\n" \
"s_bitcmp1_b32 s20, 1\n" \
"s_cbranch_scc0 L_kna1_%=\n" \
B_"v[124:127], v20, s[56:59], s12 offen offset:2048 sc1\n" \
"L_kna1_%=:\n" \
"s_bitcmp1_b32 s20, 2\n" \
"s_cbranch_scc0 L_kna2_%=\n" \
B_"v[132:135], v20, s[56:59], s13 offen offset:0 sc1\n" \
"L_kna2_%=:\n" \
"s_bitcmp1_b32 s20, 3\n" \
"s_cbranch_scc0 L_kna3_%=\n" \
B_"v[140:143], v20, s[56:59], s13 offen offset:2048 sc1\n" \
"L_kna3_%=:\n" \
"s_bitcmp1_b32 s20, 4\n" \
"s_cbranch_scc0 L_kna4_%=\n" \
B_"v[148:151], v20, s[56:59], s14 offen offset:0 sc1\n" \
"L_kna4_%=:\n" \
"s_bitcmp1_b32 s20, 5\n" \
"s_cbranch_scc0 L_kna5_%=\n" \
B_"v[156:159], v20, s[56:59], s14 offen offset:2048 sc1\n" \
"L_kna5_%=:\n" \
"s_bitcmp1_b32 s20, 6\n" \
"s_cbranch_scc0 L_kna6_%=\n" \
B_"v[164:167], v20, s[56:59], s15 offen offset:0 sc1\n" \
"L_kna6_%=:\n" \
"s_waitcnt vmcnt(0)\n" \
"s_branch L_rna_%=\n" \
"L_snx_%=:\n" \
"buffer_load_dword v37, v40, s[56:59], s74 offen sc1\n" \
"s_add_u32 s101, s101, 1\n" \
"s_waitcnt vmcnt(0)\n" \
"v_cmp_eq_u32_e64 s[98:99], s62, v37\n" \
"s_cmp_gt_u32 s101, 0x100000\n" \
"s_cbranch_scc1 L_rnx_%=\n" \
"s_andn2_b32 s75, s21, s98\n" \
"s_cbranch_scc0 L_gnx_%=\n" \
"s_sleep 1\n" \
"s_branch L_snx_%=\n" \
"L_gnx_%=:\n" \
"s_bitcmp1_b32 s21, 16\n" \
"s_cbranch_scc0 L_knx16_%=\n" \
B_"v[112:115], v20, s[56:59], s16 offen offset:0 sc1\n" \
"L_knx16_%=:\n" \
"s_bitcmp1_b32 s21, 17\n" \
"s_cbranch_scc0 L_knx17_%=\n" \
B_"v[184:187], v20, s[56:59], s16 offen offset:2048 sc1\n" \
"L_knx17_%=:\n" \
"s_bitcmp1_b32 s21, 18\n" \
"s_cbranch_scc0 L_knx18_%=\n" \
B_"v[120:123], v20, s[56:59], s17 offen offset:0 sc1\n" \
"L_knx18_%=:\n" \
"s_bitcmp1_b32 s21, 19\n" \
"s_cbranch_scc0 L_knx19_%=\n" \
B_"v[192:195], v20, s[56:59], s17 offen offset:2048 sc1\n" \
"L_knx19_%=:\n" \
"s_bitcmp1_b32 s21, 20\n" \
"s_cbranch_scc0 L_knx20_%=\n" \
B_"v[128:131], v20, s[56:59], s18 offen offset:0 sc1\n" \
"L_knx20_%=:\n" \
"s_bitcmp1_b32 s21, 21\n" \
"s_cbranch_scc0 L_knx21_%=\n" \
B_"v[200:203], v20, s[56:59], s18 offen offset:2048 sc1\n" \
"L_knx21_%=:\n" \
"s_bitcmp1_b32 s21, 22\n" \
"s_cbranch_scc0 L_knx22_%=\n" \
B_"v[176:179], v20, s[56:59], s19 offen offset:0 sc1\n" \
"L_knx22_%=:\n" \
"s_bitcmp1_b32 s21, 23\n" \
"s_cbranch_scc0 L_knx23_%=\n" \
B_"v[248:251], v20, s[56:59], s19 offen offset:2048 sc1\n" \
"L_knx23_%=:\n" \
"s_bitcmp1_b32 s21, 8\n" \
"s_cbranch_scc0 L_knx8_%=\n" \
B_"v[188:191], v20, s[56:59], s12 offen offset:1024 sc1\n" \
"L_knx8_%=:\n" \
"s_bitcmp1_b32 s21, 9\n" \
"s_cbranch_scc0 L_knx9_%=\n" \
B_"v[196:199], v20, s[56:59], s12 offen offset:3072 sc1\n" \
"L_knx9_%=:\n" \
"s_waitcnt vmcnt(0)\n" \
"s_branch L_rnx_%=\n" \
"L_sx0_%=:\n" \
"s_sleep 1\n" \
"buffer_load_dword v37, v40, s[56:59], s74 offen sc1\n" \
"s_add_u32 s101, s101, 1\n" \
"s_waitcnt vmcnt(0)\n" \
"v_cmp_eq_u32_e64 s[98:99], s62, v37\n" \
"s_cmp_gt_u32 s101, 0x100000\n" \
"s_cbranch_scc1 L_rx0_%=\n" \
"s_andn2_b32 s75, 0x400, s98\n" \
"s_cbranch_scc1 L_sx0_%=\n" \
"s_branch L_rx0_%=\n" \
"L_sx1_%=:\n" \
"s_sleep 1\n" \
"buffer_load_dword v37, v40, s[56:59], s74 offen sc1\n" \
"s_add_u32 s101, s101, 1\n" \
"s_waitcnt vmcnt(0)\n" \
"v_cmp_eq_u32_e64 s[98:99], s62, v37\n" \
"s_cmp_gt_u32 s101, 0x100000\n" \
"s_cbranch_scc1 L_rx1_%=\n" \
"s_andn2_b32 s75, 0x800, s98\n" \
"s_cbranch_scc1 L_sx1_%=\n" \
"s_branch L_rx1_%=\n" \
"L_sx2_%=:\n" \
"s_sleep 1\n" \
"buffer_load_dword v37, v40, s[56:59], s74 offen sc1\n" \
"s_add_u32 s101, s101, 1\n" \
"s_waitcnt vmcnt(0)\n" \
"v_cmp_eq_u32_e64 s[98:99], s62, v37\n" \
"s_cmp_gt_u32 s101, 0x100000\n" \
"s_cbranch_scc1 L_rx2_%=\n" \
"s_andn2_b32 s75, 0x1000, s98\n" \
"s_cbranch_scc1 L_sx2_%=\n" \
"s_branch L_rx2_%=\n" \
"L_sx3_%=:\n" \
"s_sleep 1\n" \
"buffer_load_dword v37, v40, s[56:59], s74 offen sc1\n" \
"s_add_u32 s101, s101, 1\n" \
"s_waitcnt vmcnt(0)\n" \
"v_cmp_eq_u32_e64 s[98:99], s62, v37\n" \
"s_cmp_gt_u32 s101, 0x100000\n" \
"s_cbranch_scc1 L_rx3_%=\n" \
"s_andn2_b32 s75, 0x2000, s98\n" \
"s_cbranch_scc1 L_sx3_%=\n" \
"s_branch L_rx3_%=\n" \
"L_sx4_%=:\n" \
"s_sleep 1\n" \
"buffer_load_dword v37, v40, s[56:59], s74 offen sc1\n" \
"s_add_u32 s101, s101, 1\n" \
"s_waitcnt vmcnt(0)\n" \
"v_cmp_eq_u32_e64 s[98:99], s62, v37\n" \
"s_cmp_gt_u32 s101, 0x100000\n" \
"s_cbranch_scc1 L_rx4_%=\n" \
"s_andn2_b32 s75, 0x4000, s98\n" \
"s_cbranch_scc1 L_sx4_%=\n" \
"s_branch L_rx4_%=\n" \
"L_att_stub:\n" \
"s_mov_b32 s101, 0\n" \
"v_add_u32_e32 v32, 0x49000, v20\n" \
"v_readlane_b32 s96, v11, 39\n" \
"v_readlane_b32 s97, v11, 40\n" \
B_"a[96:99], v20, s[56:59], s60 offen offset:0 sc1\n" \
B_"a[112:115], v32, s[56:59], s60 offen offset:0 sc1\n" \
B_"a[100:103], v20, s[56:59], s60 offen offset:1024 sc1\n" \
B_"a[116:119], v32, s[56:59], s60 offen offset:1024 sc1\n" \
B_"a[104:107], v20, s[56:59], s60 offen offset:2048 sc1\n" \
B_"a[120:123], v32, s[56:59], s60 offen offset:2048 sc1\n" \
B_"a[108:111], v20, s[56:59], s60 offen offset:3072 sc1\n" \
B_"a[124:127], v32, s[56:59], s60 offen offset:3072 sc1\n" \
B_"a[220:223], v20, s[56:59], s96 offen offset:0 sc1\n" \
B_"a[228:231], v32, s[56:59], s96 offen offset:0 sc1\n" \
B_"a[236:239], v20, s[56:59], s97 offen offset:0 sc1\n" \
B_"a[244:247], v32, s[56:59], s97 offen offset:0 sc1\n" \
B_"a[224:227], v20, s[56:59], s96 offen offset:1024 sc1\n" \
B_"a[232:235], v32, s[56:59], s96 offen offset:1024 sc1\n" \
B_"a[240:243], v20, s[56:59], s97 offen offset:1024 sc1\n" \
B_"a[248:251], v32, s[56:59], s97 offen offset:1024 sc1\n" \
B_"v[64:67], v20, s[56:59], s61 offen offset:0 sc1\n" \
B_"v[68:71], v20, s[56:59], s61 offen offset:1024 sc1\n" \
B_"v[72:75], v20, s[56:59], s61 offen offset:2048 sc1\n" \
B_"v[76:79], v20, s[56:59], s61 offen offset:3072 sc1\n" \
B_"v[80:83], v32, s[56:59], s61 offen offset:0 sc1\n" \
B_"v[84:87], v32, s[56:59], s61 offen offset:1024 sc1\n" \
B_"v[96:99], v32, s[56:59], s61 offen offset:2048 sc1\n" \
B_"v[100:103], v32, s[56:59], s61 offen offset:3072 sc1\n" \
"v_not_b32_e32 v26, s62\n" \
"s_waitcnt vmcnt(0)\n" \
"s_branch L_out_%=\n" \
".fill (L_ab + 16384 - .) / 4, 4, 0xBF800000\n" \
".fill (L_ab + 16384 - .) / 4, 4, 0xBF800000\n" \
"L_ap4:\n" \
"s_setpc_b64 s[88:89]\n" \
"L_gsw:\n" \
M_"v[104:107], a[128:131], a[16:19], v[36:39]\n" \
"s_add_u32 s68, s60, 0xfff6e000\n" \
"s_add_u32 s69, s60, 0xfffb7000\n" \
"s_add_u32 s70, s60, 0x0\n" \
"s_add_u32 s71, s60, 0x49000\n" \
M_"v[104:107], a[132:135], a[20:23], v[104:107]\n" \
"s_xor_b32 m0, s65, 0x12000\n" \
R_"a[160:163], v49 offset:0\n" \
M_"v[104:107], a[136:139], a[24:27], v[104:107]\n" \
B_"v50, s[8:11], s64 offen offset:0 sc1 lds\n" \
M_"v[104:107], a[140:143], a[28:31], v[104:107]\n" \
R_"a[164:167], v49 offset:1024\n" \
R_"a[168:171], v49 offset:2048\n" \
M_"v[108:111], a[156:159], a[28:31], v[36:39]\n" \
R_"a[172:175], v49 offset:3072\n" \
R_"a[176:179], v49 offset:4096\n" \
M_"v[108:111], a[152:155], a[24:27], v[108:111]\n" \
B_"v50, s[8:11], s64 offen offset:1024 sc1 lds\n" \
M_"v[108:111], a[148:151], a[20:23], v[108:111]\n" \
"s_mov_b32 s65, s13\n" \
"v_add_u32_e32 v63, s15, v20\n" \
M_"v[108:111], a[144:147], a[16:19], v[108:111]\n" \
"s_add_u32 s67, s14, 40960\n" \
"v_add_u32_e32 v50, 0x6000, v63\n" \
M_"v[116:119], a[144:147], a[32:35], v[40:43]\n" \
"s_xor_b32 m0, s65, 0x8000\n" \
R_"a[180:183], v49 offset:5120\n" \
M_"v[116:119], a[148:151], a[36:39], v[116:119]\n" \
R_"a[184:187], v49 offset:6144\n" \
"v_and_b32_e32 v68, v12, v68\n" \
M_"v[116:119], a[152:155], a[40:43], v[116:119]\n" \
"v_and_b32_e32 v69, v13, v69\n" \
"v_and_b32_e32 v70, v14, v70\n" \
M_"v[116:119], a[156:159], a[44:47], v[116:119]\n" \
B_"v50, s[8:11], s67 offen offset:0 sc1 lds\n" \
M_"v[112:115], a[140:143], a[44:47], v[40:43]\n" \
"v_and_b32_e32 v71, v15, v71\n" \
R_"a[188:191], v49 offset:7168\n" \
M_"v[112:115], a[136:139], a[40:43], v[112:115]\n" \
E_"v86, v86\n" \
M_"v[112:115], a[132:135], a[36:39], v[112:115]\n" \
E_"v87, v87\n" \
M_"v[112:115], a[128:131], a[32:35], v[112:115]\n" \
E_"v84, v84\n" \
M_"v[120:123], a[128:131], a[48:51], v[44:47]\n" \
B_"v50, s[8:11], s67 offen offset:1024 sc1 lds\n" \
M_"v[120:123], a[132:135], a[52:55], v[120:123]\n" \
E_"v85, v85\n" \
M_"v[120:123], a[136:139], a[56:59], v[120:123]\n" \
C_"v87, v86, v87\n" \
C_"v86, v84, v85\n" \
M_"v[120:123], a[140:143], a[60:63], v[120:123]\n" \
E_"v82, v82\n" \
M_"v[124:127], a[156:159], a[60:63], v[44:47]\n" \
E_"v83, v83\n" \
M_"v[124:127], a[152:155], a[56:59], v[124:127]\n" \
E_"v80, v80\n" \
M_"v[124:127], a[148:151], a[52:55], v[124:127]\n" \
B_"v50, s[8:11], s67 offen offset:2048 sc1 lds\n" \
M_"v[124:127], a[144:147], a[48:51], v[124:127]\n" \
C_"v85, v82, v83\n" \
"s_waitcnt lgkmcnt(1)\n" \
M_"v[128:131], a[160:163], v[68:71], v[128:131]\n" \
E_"v81, v81\n" \
M_"v[160:163], a[160:163], v[72:75], v[160:163]\n" \
C_"v84, v80, v81\n" \
"s_mov_b32 s64, s14\n" \
M_"v[132:135], a[164:167], v[68:71], v[132:135]\n" \
E_"v88, v88\n" \
M_"v[164:167], a[164:167], v[72:75], v[164:167]\n" \
E_"v89, v89\n" \
M_"v[136:139], a[168:171], v[68:71], v[136:139]\n" \
B_"v50, s[8:11], s67 offen offset:3072 sc1 lds\n" \
M_"v[168:171], a[168:171], v[72:75], v[168:171]\n" \
E_"v90, v90\n" \
M_"v[140:143], a[172:175], v[68:71], v[140:143]\n" \
E_"v91, v91\n" \
M_"v[172:175], a[172:175], v[72:75], v[172:175]\n" \
C_"v88, v88, v89\n" \
C_"v89, v90, v91\n" \
M_"v[144:147], a[176:179], v[68:71], v[144:147]\n" \
E_"v92, v92\n" \
M_"v[176:179], a[176:179], v[72:75], v[176:179]\n" \
E_"v93, v93\n" \
M_"v[148:151], a[180:183], v[68:71], v[148:151]\n" \
E_"v94, v94\n" \
M_"v[180:183], a[180:183], v[72:75], v[180:183]\n" \
E_"v95, v95\n" \
M_"v[152:155], a[184:187], v[68:71], v[152:155]\n" \
C_"v90, v92, v93\n" \
C_"v91, v94, v95\n" \
M_"v[184:187], a[184:187], v[72:75], v[184:187]\n" \
"v_add_u32_e32 v51, 0x12000, v63\n" \
"s_waitcnt lgkmcnt(0)\n" \
M_"v[156:159], a[188:191], v[68:71], v[156:159]\n" \
"s_xor_b32 m0, s65, 0xa000\n" \
"ds_read_b32 v58, v18 offset:512\n" \
M_"v[188:191], a[188:191], v[72:75], v[188:191]\n" \
B_"v51, s[8:11], s64 offen offset:0 sc1 lds\n" \
"v_smfmac_f32_16x16x64_bf16 a[192:195], a[200:203], v[68:75], v21\n" \
B_"v51, s[8:11], s64 offen offset:1024 sc1 lds\n" \
M_"v[220:223], a[188:191], v[84:87], v[220:223]\n" \
"ds_read_b32 v59, v18 offset:640\n" \
"ds_read_b32 v60, v18 offset:768\n" \
M_"v[252:255], a[188:191], v[88:91], v[252:255]\n" \
"ds_read_b32 v61, v18 offset:896\n" \
R_"a[128:131], v48 offset:0\n" \
M_"v[216:219], a[184:187], v[84:87], v[216:219]\n" \
R_"a[144:147], v48 offset:4096\n" \
R_"a[132:135], v48 offset:1024\n" \
M_"v[248:251], a[184:187], v[88:91], v[248:251]\n" \
R_"a[148:151], v48 offset:5120\n" \
R_"a[136:139], v48 offset:2048\n" \
M_"v[212:215], a[180:183], v[84:87], v[212:215]\n" \
R_"a[152:155], v48 offset:6144\n" \
R_"a[140:143], v48 offset:3072\n" \
M_"v[244:247], a[180:183], v[88:91], v[244:247]\n" \
R_"a[156:159], v48 offset:7168\n" \
"v_add_u32_e32 v52, 0x14000, v63\n" \
M_"v[208:211], a[176:179], v[84:87], v[208:211]\n" \
B_"v51, s[8:11], s64 offen offset:2048 sc1 lds\n" \
M_"v[240:243], a[176:179], v[88:91], v[240:243]\n" \
E_"v104, v104\n" \
M_"v[204:207], a[172:175], v[84:87], v[204:207]\n" \
E_"v105, v105\n" \
M_"v[236:239], a[172:175], v[88:91], v[236:239]\n" \
B_"v51, s[8:11], s64 offen offset:3072 sc1 lds\n" \
M_"v[200:203], a[168:171], v[84:87], v[200:203]\n" \
E_"v106, v106\n" \
M_"v[232:235], a[168:171], v[88:91], v[232:235]\n" \
E_"v107, v107\n" \
M_"v[196:199], a[164:167], v[84:87], v[196:199]\n" \
C_"v104, v104, v105\n" \
C_"v105, v106, v107\n" \
M_"v[228:231], a[164:167], v[88:91], v[228:231]\n" \
E_"v108, v108\n" \
M_"v[192:195], a[160:163], v[84:87], v[192:195]\n" \
E_"v109, v109\n" \
M_"v[224:227], a[160:163], v[88:91], v[224:227]\n" \
E_"v110, v110\n" \
"v_smfmac_f32_16x16x64_bf16 a[196:199], a[200:203], v[84:91], v21\n" \
E_"v111, v111\n" \
C_"v106, v108, v109\n" \
C_"v107, v110, v111\n" \
"v_and_b32_e32 v104, v12, v104\n" \
"v_and_b32_e32 v105, v13, v105\n" \
"v_and_b32_e32 v106, v14, v106\n" \
"v_and_b32_e32 v107, v15, v107\n" \
"v_accvgpr_read_b32 v54, a192\n" \
"v_accvgpr_read_b32 v22, a193\n" \
"v_add_u32_e32 v53, 0x20000, v63\n" \
E_"v118, v118\n" \
E_"v119, v119\n" \
E_"v116, v116\n" \
E_"v117, v117\n" \
C_"v119, v118, v119\n" \
E_"v114, v114\n" \
E_"v115, v115\n" \
E_"v112, v112\n" \
E_"v113, v113\n" \
C_"v118, v116, v117\n" \
C_"v117, v114, v115\n" \
C_"v116, v112, v113\n" \
"v_readlane_b32 s93, v11, 22\n" \
"s_waitcnt vmcnt(10) lgkmcnt(0)\n" \
"s_barrier\n" \
M_"v[80:83], a[128:131], a[32:35], v[40:43]\n" \
R_"a[160:163], v49 offset:8192\n" \
R_"a[164:167], v49 offset:9216\n" \
M_"v[80:83], a[132:135], a[36:39], v[80:83]\n" \
R_"a[168:171], v49 offset:10240\n" \
"s_xor_b32 m0, s65, 0x18000\n" \
M_"v[80:83], a[136:139], a[40:43], v[80:83]\n" \
B_"v52, s[8:11], s64 offen offset:0 sc1 lds\n" \
M_"v[80:83], a[140:143], a[44:47], v[80:83]\n" \
R_"a[172:175], v49 offset:11264\n" \
R_"a[176:179], v49 offset:12288\n" \
M_"v[84:87], a[156:159], a[44:47], v[40:43]\n" \
R_"a[180:183], v49 offset:13312\n" \
R_"a[184:187], v49 offset:14336\n" \
M_"v[84:87], a[152:155], a[40:43], v[84:87]\n" \
B_"v52, s[8:11], s64 offen offset:1024 sc1 lds\n" \
M_"v[84:87], a[148:151], a[36:39], v[84:87]\n" \
R_"a[188:191], v49 offset:15360\n" \
"v_readlane_b32 s80, v11, 11\n" \
M_"v[84:87], a[144:147], a[32:35], v[84:87]\n" \
B_"v52, s[8:11], s64 offen offset:2048 sc1 lds\n" \
M_"v[92:95], a[144:147], a[48:51], v[44:47]\n" \
E_"v120, v120\n" \
M_"v[92:95], a[148:151], a[52:55], v[92:95]\n" \
E_"v121, v121\n" \
M_"v[92:95], a[152:155], a[56:59], v[92:95]\n" \
B_"v52, s[8:11], s64 offen offset:3072 sc1 lds\n" \
M_"v[92:95], a[156:159], a[60:63], v[92:95]\n" \
"s_mov_b32 m0, s65\n" \
C_"v120, v120, v121\n" \
M_"v[88:91], a[140:143], a[60:63], v[44:47]\n" \
E_"v122, v122\n" \
M_"v[88:91], a[136:139], a[56:59], v[88:91]\n" \
B_"v53, s[8:11], s64 offen offset:0 sc1 lds\n" \
M_"v[88:91], a[132:135], a[52:55], v[88:91]\n" \
E_"v123, v123\n" \
M_"v[88:91], a[128:131], a[48:51], v[88:91]\n" \
E_"v124, v124\n" \
M_"a[192:195], a[212:215], v[104:107], a[192:195]\n" \
C_"v121, v122, v123\n" \
"s_waitcnt lgkmcnt(5)\n" \
M_"v[160:163], a[160:163], v[104:107], v[160:163]\n" \
B_"v53, s[8:11], s64 offen offset:1024 sc1 lds\n" \
M_"v[164:167], a[164:167], v[104:107], v[164:167]\n" \
E_"v125, v125\n" \
M_"v[168:171], a[168:171], v[104:107], v[168:171]\n" \
C_"v122, v124, v125\n" \
"s_waitcnt lgkmcnt(1)\n" \
M_"v[172:175], a[172:175], v[104:107], v[172:175]\n" \
E_"v126, v126\n" \
M_"v[176:179], a[176:179], v[104:107], v[176:179]\n" \
E_"v127, v127\n" \
M_"v[180:183], a[180:183], v[104:107], v[180:183]\n" \
C_"v123, v126, v127\n" \
"v_readlane_b32 s81, v11, 12\n" \
M_"v[184:187], a[184:187], v[104:107], v[184:187]\n" \
"s_mov_b32 s82, 0x47fca0\n" \
"s_waitcnt lgkmcnt(0)\n" \
M_"v[188:191], a[188:191], v[104:107], v[188:191]\n" \
B_"v53, s[8:11], s64 offen offset:2048 sc1 lds\n" \
M_"v[220:223], a[188:191], v[116:119], v[220:223]\n" \
B_"v53, s[8:11], s64 offen offset:3072 sc1 lds\n" \
M_"v[252:255], a[188:191], v[120:123], v[252:255]\n" \
"s_mov_b32 s83, 0x20000\n" \
"v_readlane_b32 s92, v11, 29\n" \
M_"v[216:219], a[184:187], v[116:119], v[216:219]\n" \
"s_add_u32 m0, s93, 0x0\n" \
R_"a[128:131], v48 offset:8192\n" \
M_"v[248:251], a[184:187], v[120:123], v[248:251]\n" \
R_"a[144:147], v48 offset:12288\n" \
R_"a[132:135], v48 offset:9216\n" \
M_"v[212:215], a[180:183], v[116:119], v[212:215]\n" \
B_"v0, s[80:83], s92 offen offset:0 lds\n" \
M_"v[244:247], a[180:183], v[120:123], v[244:247]\n" \
B_"v1, s[80:83], s92 offen offset:1024 lds\n" \
M_"v[208:211], a[176:179], v[116:119], v[208:211]\n" \
R_"a[148:151], v48 offset:13312\n" \
R_"a[136:139], v48 offset:10240\n" \
M_"v[240:243], a[176:179], v[120:123], v[240:243]\n" \
R_"a[152:155], v48 offset:14336\n" \
R_"a[140:143], v48 offset:11264\n" \
M_"v[204:207], a[172:175], v[116:119], v[204:207]\n" \
B_"v2, s[80:83], s92 offen offset:2048 lds\n" \
M_"v[236:239], a[172:175], v[120:123], v[236:239]\n" \
R_"a[156:159], v48 offset:15360\n" \
"s_xor_b32 s65, s65, 0x8000\n" \
M_"v[200:203], a[168:171], v[116:119], v[200:203]\n" \
"s_add_u32 s64, s64, 0x10000\n" \
"v_add_u32_e32 v49, 0x10000, v48\n" \
M_"v[232:235], a[168:171], v[120:123], v[232:235]\n" \
B_"v3, s[80:83], s92 offen offset:3072 lds\n" \
M_"v[196:199], a[164:167], v[116:119], v[196:199]\n" \
E_"v86, v86\n" \
M_"v[228:231], a[164:167], v[120:123], v[228:231]\n" \
E_"v87, v87\n" \
M_"v[192:195], a[160:163], v[116:119], v[192:195]\n" \
E_"v84, v84\n" \
M_"v[224:227], a[160:163], v[120:123], v[224:227]\n" \
E_"v85, v85\n" \
"v_smfmac_f32_16x16x64_bf16 a[196:199], a[200:203], v[116:123], v21\n" \
E_"v88, v88\n" \
E_"v89, v89\n" \
C_"v87, v86, v87\n" \
E_"v82, v82\n" \
E_"v83, v83\n" \
E_"v80, v80\n" \
E_"v81, v81\n" \
E_"v90, v90\n" \
E_"v91, v91\n" \
C_"v86, v84, v85\n" \
C_"v88, v88, v89\n" \
E_"v92, v92\n" \
E_"v93, v93\n" \
E_"v94, v94\n" \
E_"v95, v95\n" \
C_"v85, v82, v83\n" \
C_"v84, v80, v81\n" \
C_"v89, v90, v91\n" \
"v_and_b32_e32 v84, v12, v84\n" \
"v_and_b32_e32 v85, v13, v85\n" \
"v_and_b32_e32 v86, v14, v86\n" \
"v_and_b32_e32 v87, v15, v87\n" \
C_"v90, v92, v93\n" \
C_"v91, v94, v95\n" \
"v_add_u32_e32 v48, s16, v20\n" \
"v_add_f32_e32 v54, v54, v22\n" \
"v_accvgpr_read_b32 v55, a194\n" \
"v_accvgpr_read_b32 v23, a195\n" \
M_"a[192:195], a[216:219], a[216:219], 0\n" \
"v_max_f32_e32 v62, v62, v54\n" \
"v_add_f32_e32 v55, v55, v23\n" \
"v_max_f32_e32 v62, v62, v55\n" \
"v_rcp_f32_e32 v54, v54\n" \
"v_rcp_f32_e32 v55, v55\n" \
"v_mul_f32_e32 v54, v54, v58\n" \
"v_mul_f32_e32 v55, v55, v59\n" \
"v_mul_f32_e32 v128, v54, v128\n" \
"v_mul_f32_e32 v129, v54, v129\n" \
"v_mul_f32_e32 v136, v54, v136\n" \
"v_mul_f32_e32 v137, v54, v137\n" \
"v_mul_f32_e32 v144, v54, v144\n" \
"v_mul_f32_e32 v145, v54, v145\n" \
"v_mul_f32_e32 v152, v54, v152\n" \
"v_mul_f32_e32 v153, v54, v153\n" \
"v_mul_f32_e32 v160, v55, v160\n" \
"v_mul_f32_e32 v161, v55, v161\n" \
"v_mul_f32_e32 v168, v55, v168\n" \
"v_mul_f32_e32 v169, v55, v169\n" \
"v_mul_f32_e32 v176, v55, v176\n" \
"v_mul_f32_e32 v177, v55, v177\n" \
"v_mul_f32_e32 v184, v55, v184\n" \
"v_mul_f32_e32 v185, v55, v185\n" \
C_"v128, v128, v129\n" \
"v_mul_f32_e32 v130, v54, v130\n" \
"v_mul_f32_e32 v131, v54, v131\n" \
C_"v136, v136, v137\n" \
"v_mul_f32_e32 v138, v54, v138\n" \
"v_mul_f32_e32 v139, v54, v139\n" \
C_"v144, v144, v145\n" \
"v_mul_f32_e32 v146, v54, v146\n" \
"v_mul_f32_e32 v147, v54, v147\n" \
C_"v152, v152, v153\n" \
"v_mul_f32_e32 v154, v54, v154\n" \
"v_mul_f32_e32 v155, v54, v155\n" \
C_"v160, v160, v161\n" \
"v_mul_f32_e32 v162, v55, v162\n" \
"v_mul_f32_e32 v163, v55, v163\n" \
C_"v168, v168, v169\n" \
"v_mul_f32_e32 v170, v55, v170\n" \
"v_mul_f32_e32 v171, v55, v171\n" \
C_"v176, v176, v177\n" \
"v_mul_f32_e32 v178, v55, v178\n" \
"v_mul_f32_e32 v179, v55, v179\n" \
C_"v184, v184, v185\n" \
"v_mul_f32_e32 v186, v55, v186\n" \
"v_mul_f32_e32 v187, v55, v187\n" \
C_"v129, v130, v131\n" \
"v_mul_f32_e32 v132, v54, v132\n" \
"v_mul_f32_e32 v133, v54, v133\n" \
"v_mul_f32_e32 v134, v54, v134\n" \
"v_mul_f32_e32 v135, v54, v135\n" \
C_"v137, v138, v139\n" \
"v_mul_f32_e32 v140, v54, v140\n" \
"s_waitcnt vmcnt(12) lgkmcnt(0)\n" \
"s_barrier\n" \
M_"v[120:123], a[128:131], a[48:51], v[44:47]\n" \
"s_xor_b32 m0, s65, 0x12000\n" \
R_"a[188:191], v49 offset:7168\n" \
M_"v[120:123], a[132:135], a[52:55], v[120:123]\n" \
B_"v50, s[8:11], s64 offen offset:0 sc1 lds\n" \
M_"v[120:123], a[136:139], a[56:59], v[120:123]\n" \
R_"a[184:187], v49 offset:6144\n" \
R_"a[180:183], v49 offset:5120\n" \
M_"v[120:123], a[140:143], a[60:63], v[120:123]\n" \
B_"v50, s[8:11], s64 offen offset:1024 sc1 lds\n" \
M_"v[124:127], a[156:159], a[60:63], v[44:47]\n" \
R_"a[176:179], v49 offset:4096\n" \
R_"a[172:175], v49 offset:3072\n" \
M_"v[124:127], a[152:155], a[56:59], v[124:127]\n" \
R_"a[168:171], v49 offset:2048\n" \
R_"a[164:167], v49 offset:1024\n" \
M_"v[124:127], a[148:151], a[52:55], v[124:127]\n" \
R_"a[160:163], v49 offset:0\n" \
R_"a[0:3], v49 offset:8192\n" \
M_"v[124:127], a[144:147], a[48:51], v[124:127]\n" \
B_"v50, s[8:11], s64 offen offset:2048 sc1 lds\n" \
M_"v[32:35], a[220:223], a[64:67], 0\n" \
R_"a[4:7], v49 offset:9216\n" \
R_"a[8:11], v49 offset:10240\n" \
M_"v[36:39], a[220:223], a[80:83], 0\n" \
R_"a[12:15], v49 offset:11264\n" \
R_"a[16:19], v49 offset:12288\n" \
M_"v[40:43], a[220:223], a[96:99], 0\n" \
R_"a[20:23], v49 offset:13312\n" \
R_"a[24:27], v49 offset:14336\n" \
M_"v[44:47], a[220:223], a[112:115], 0\n" \
B_"v50, s[8:11], s64 offen offset:3072 sc1 lds\n" \
M_"v[32:35], a[224:227], a[68:71], v[32:35]\n" \
"s_xor_b32 m0, s65, 0xa000\n" \
R_"a[28:31], v49 offset:15360\n" \
M_"v[36:39], a[224:227], a[84:87], v[36:39]\n" \
E_"v120, v120\n" \
M_"v[40:43], a[224:227], a[100:103], v[40:43]\n" \
B_"v51, s[8:11], s64 offen offset:0 sc1 lds\n" \
M_"v[44:47], a[224:227], a[116:119], v[44:47]\n" \
E_"v121, v121\n" \
M_"v[32:35], a[228:231], a[72:75], v[32:35]\n" \
E_"v122, v122\n" \
M_"v[36:39], a[228:231], a[88:91], v[36:39]\n" \
E_"v123, v123\n" \
M_"v[40:43], a[228:231], a[104:107], v[40:43]\n" \
B_"v51, s[8:11], s64 offen offset:1024 sc1 lds\n" \
M_"v[44:47], a[228:231], a[120:123], v[44:47]\n" \
C_"v120, v120, v121\n" \
C_"v121, v122, v123\n" \
M_"v[32:35], a[232:235], a[76:79], v[32:35]\n" \
E_"v124, v124\n" \
M_"v[36:39], a[232:235], a[92:95], v[36:39]\n" \
E_"v125, v125\n" \
M_"v[40:43], a[232:235], a[108:111], v[40:43]\n" \
B_"v51, s[8:11], s64 offen offset:2048 sc1 lds\n" \
M_"v[44:47], a[232:235], a[124:127], v[44:47]\n" \
E_"v126, v126\n" \
"v_smfmac_f32_16x16x64_bf16 a[196:199], a[200:203], v[84:91], v21\n" \
C_"v122, v124, v125\n" \
"s_waitcnt lgkmcnt(1)\n" \
M_"v[220:223], a[188:191], v[84:87], v[220:223]\n" \
B_"v51, s[8:11], s64 offen offset:3072 sc1 lds\n" \
M_"v[252:255], a[188:191], v[88:91], v[252:255]\n" \
"s_add_u32 m0, s93, 0x1000\n" \
"v_and_b32_e32 v120, v12, v120\n" \
M_"v[216:219], a[184:187], v[84:87], v[216:219]\n" \
E_"v127, v127\n" \
M_"v[248:251], a[184:187], v[88:91], v[248:251]\n" \
C_"v123, v126, v127\n" \
"v_and_b32_e32 v121, v13, v121\n" \
M_"v[212:215], a[180:183], v[84:87], v[212:215]\n" \
B_"v4, s[80:83], s92 offen offset:0 lds\n" \
M_"v[244:247], a[180:183], v[88:91], v[244:247]\n" \
"v_and_b32_e32 v122, v14, v122\n" \
"v_and_b32_e32 v123, v15, v123\n" \
M_"v[208:211], a[176:179], v[84:87], v[208:211]\n" \
"v_accvgpr_read_b32 v56, a196\n" \
"v_accvgpr_read_b32 v24, a197\n" \
M_"v[240:243], a[176:179], v[88:91], v[240:243]\n" \
R_"a[128:131], v48 offset:0\n" \
R_"a[144:147], v48 offset:4096\n" \
M_"v[204:207], a[172:175], v[84:87], v[204:207]\n" \
B_"v5, s[80:83], s92 offen offset:1024 lds\n" \
M_"v[236:239], a[172:175], v[88:91], v[236:239]\n" \
R_"a[132:135], v48 offset:1024\n" \
R_"a[148:151], v48 offset:5120\n" \
M_"v[200:203], a[168:171], v[84:87], v[200:203]\n" \
R_"a[136:139], v48 offset:2048\n" \
R_"a[152:155], v48 offset:6144\n" \
M_"v[232:235], a[168:171], v[88:91], v[232:235]\n" \
B_"v6, s[80:83], s92 offen offset:2048 lds\n" \
M_"v[196:199], a[164:167], v[84:87], v[196:199]\n" \
R_"a[140:143], v48 offset:3072\n" \
R_"a[156:159], v48 offset:7168\n" \
M_"v[228:231], a[164:167], v[88:91], v[228:231]\n" \
"v_mul_f32_e32 v141, v54, v141\n" \
"v_mul_f32_e32 v142, v54, v142\n" \
M_"v[192:195], a[160:163], v[84:87], v[192:195]\n" \
"v_mul_f32_e32 v143, v54, v143\n" \
C_"v145, v146, v147\n" \
M_"v[224:227], a[160:163], v[88:91], v[224:227]\n" \
B_"v7, s[80:83], s92 offen offset:3072 lds\n" \
M_"a[196:199], a[212:215], v[120:123], a[196:199]\n" \
"v_mul_f32_e32 v148, v54, v148\n" \
"v_mul_f32_e32 v149, v54, v149\n" \
M_"v[224:227], a[0:3], v[120:123], v[224:227]\n" \
"v_mul_f32_e32 v150, v54, v150\n" \
"v_mul_f32_e32 v151, v54, v151\n" \
M_"v[228:231], a[4:7], v[120:123], v[228:231]\n" \
C_"v153, v154, v155\n" \
"v_mul_f32_e32 v156, v54, v156\n" \
M_"v[232:235], a[8:11], v[120:123], v[232:235]\n" \
"v_mul_f32_e32 v157, v54, v157\n" \
"v_mul_f32_e32 v158, v54, v158\n" \
M_"v[236:239], a[12:15], v[120:123], v[236:239]\n" \
"v_mul_f32_e32 v159, v54, v159\n" \
C_"v161, v162, v163\n" \
M_"v[240:243], a[16:19], v[120:123], v[240:243]\n" \
"v_mul_f32_e32 v164, v55, v164\n" \
"v_mul_f32_e32 v165, v55, v165\n" \
M_"v[244:247], a[20:23], v[120:123], v[244:247]\n" \
"v_mul_f32_e32 v166, v55, v166\n" \
"v_mul_f32_e32 v167, v55, v167\n" \
M_"v[248:251], a[24:27], v[120:123], v[248:251]\n" \
C_"v169, v170, v171\n" \
"s_waitcnt lgkmcnt(4)\n" \
M_"v[252:255], a[28:31], v[120:123], v[252:255]\n" \
"v_mul_f32_e32 v172, v55, v172\n" \
"v_mul_f32_e32 v173, v55, v173\n" \
"v_mul_f32_e32 v174, v55, v174\n" \
"v_mul_f32_e32 v175, v55, v175\n" \
C_"v177, v178, v179\n" \
"v_mul_f32_e32 v180, v55, v180\n" \
"v_mul_f32_e32 v181, v55, v181\n" \
"v_mul_f32_e32 v182, v55, v182\n" \
"v_mul_f32_e32 v183, v55, v183\n" \
C_"v185, v186, v187\n" \
"v_mul_f32_e32 v188, v55, v188\n" \
"v_mul_f32_e32 v189, v55, v189\n" \
"v_mul_f32_e32 v190, v55, v190\n" \
"v_mul_f32_e32 v191, v55, v191\n" \
C_"v130, v132, v133\n" \
C_"v131, v134, v135\n" \
"buffer_store_dwordx4 v[128:131], v20, s[56:59], s68 offen offset:0 sc1\n" \
C_"v138, v140, v141\n" \
C_"v139, v142, v143\n" \
"buffer_store_dwordx4 v[136:139], v20, s[56:59], s68 offen offset:1024 sc1\n" \
C_"v146, v148, v149\n" \
C_"v147, v150, v151\n" \
"buffer_store_dwordx4 v[144:147], v20, s[56:59], s68 offen offset:2048 sc1\n" \
C_"v154, v156, v157\n" \
C_"v155, v158, v159\n" \
"buffer_store_dwordx4 v[152:155], v20, s[56:59], s68 offen offset:3072 sc1\n" \
C_"v162, v164, v165\n" \
C_"v163, v166, v167\n" \
"buffer_store_dwordx4 v[160:163], v20, s[56:59], s69 offen offset:0 sc1\n" \
C_"v170, v172, v173\n" \
C_"v171, v174, v175\n" \
"buffer_store_dwordx4 v[168:171], v20, s[56:59], s69 offen offset:1024 sc1\n" \
C_"v178, v180, v181\n" \
C_"v179, v182, v183\n" \
"buffer_store_dwordx4 v[176:179], v20, s[56:59], s69 offen offset:2048 sc1\n" \
C_"v186, v188, v189\n" \
C_"v187, v190, v191\n" \
"buffer_store_dwordx4 v[184:187], v20, s[56:59], s69 offen offset:3072 sc1\n" \
"s_add_u32 m0, s93, 0x2000\n" \
M_"a[208:211], a[216:219], v[128:131], a[208:211]\n" \
B_"v8, s[80:83], s92 offen offset:0 lds\n" \
M_"a[208:211], a[216:219], v[136:139], a[208:211]\n" \
"v_mfma_f32_32x32x16_bf16 v[128:143], a[216:219], a[216:219], 0\n" \
M_"a[208:211], a[216:219], v[144:147], a[208:211]\n" \
M_"a[208:211], a[216:219], v[152:155], a[208:211]\n" \
"v_mfma_f32_32x32x16_bf16 v[144:159], a[216:219], a[216:219], 0\n" \
"v_add_f32_e32 v56, v56, v24\n" \
M_"a[208:211], a[216:219], v[160:163], a[208:211]\n" \
"v_max_f32_e32 v62, v62, v56\n" \
"v_accvgpr_read_b32 v57, a198\n" \
"s_waitcnt vmcnt(21) lgkmcnt(0)\n" \
"s_barrier\n" \
M_"v[64:67], a[128:131], a[64:67], v[32:35]\n" \
"s_xor_b32 m0, s65, 0x18000\n" \
"v_accvgpr_read_b32 v25, a199\n" \
M_"a[196:199], a[216:219], a[216:219], 0\n" \
B_"v52, s[8:11], s64 offen offset:0 sc1 lds\n" \
M_"v[64:67], a[132:135], a[68:71], v[64:67]\n" \
"v_rcp_f32_e32 v56, v56\n" \
M_"v[64:67], a[136:139], a[72:75], v[64:67]\n" \
M_"a[208:211], a[216:219], v[168:171], a[208:211]\n" \
"v_mfma_f32_32x32x16_bf16 v[160:175], a[216:219], a[216:219], 0\n" \
"v_mul_f32_e32 v56, v56, v60\n" \
M_"a[208:211], a[216:219], v[176:179], a[208:211]\n" \
"v_mul_f32_e32 v192, v56, v192\n" \
"v_mul_f32_e32 v193, v56, v193\n" \
M_"v[64:67], a[140:143], a[76:79], v[64:67]\n" \
B_"v52, s[8:11], s64 offen offset:1024 sc1 lds\n" \
M_"v[68:71], a[156:159], a[76:79], v[32:35]\n" \
C_"v192, v192, v193\n" \
"v_mul_f32_e32 v194, v56, v194\n" \
M_"v[68:71], a[152:155], a[72:75], v[68:71]\n" \
"v_mul_f32_e32 v195, v56, v195\n" \
C_"v193, v194, v195\n" \
M_"v[68:71], a[148:151], a[68:71], v[68:71]\n" \
M_"a[208:211], a[216:219], v[184:187], a[208:211]\n" \
"v_mfma_f32_32x32x16_bf16 v[176:191], a[216:219], a[216:219], 0\n" \
"v_mul_f32_e32 v196, v56, v196\n" \
"v_mul_f32_e32 v197, v56, v197\n" \
"v_mul_f32_e32 v198, v56, v198\n" \
"v_mul_f32_e32 v199, v56, v199\n" \
"v_mul_f32_e32 v200, v56, v200\n" \
"v_mul_f32_e32 v201, v56, v201\n" \
M_"v[68:71], a[144:147], a[64:67], v[68:71]\n" \
B_"v52, s[8:11], s64 offen offset:2048 sc1 lds\n" \
M_"v[76:79], a[144:147], a[80:83], v[36:39]\n" \
C_"v194, v196, v197\n" \
C_"v195, v198, v199\n" \
"buffer_store_dwordx4 v[192:195], v20, s[56:59], s70 offen offset:0 sc1\n" \
M_"v[76:79], a[148:151], a[84:87], v[76:79]\n" \
C_"v200, v200, v201\n" \
"v_mul_f32_e32 v202, v56, v202\n" \
M_"v[76:79], a[152:155], a[88:91], v[76:79]\n" \
"v_mul_f32_e32 v203, v56, v203\n" \
"v_add_f32_e32 v57, v57, v25\n" \
M_"v[76:79], a[156:159], a[92:95], v[76:79]\n" \
B_"v52, s[8:11], s64 offen offset:3072 sc1 lds\n" \
M_"v[72:75], a[140:143], a[92:95], v[36:39]\n" \
"s_mov_b32 m0, s65\n" \
C_"v201, v202, v203\n" \
M_"v[72:75], a[136:139], a[88:91], v[72:75]\n" \
M_"a[208:211], a[216:219], v[192:195], a[208:211]\n" \
"v_mul_f32_e32 v204, v56, v204\n" \
"v_mul_f32_e32 v205, v56, v205\n" \
M_"v[72:75], a[132:135], a[84:87], v[72:75]\n" \
"v_mul_f32_e32 v206, v56, v206\n" \
"v_mul_f32_e32 v207, v56, v207\n" \
M_"v[72:75], a[128:131], a[80:83], v[72:75]\n" \
B_"v53, s[8:11], s64 offen offset:0 sc1 lds\n" \
M_"v[80:83], a[128:131], a[96:99], v[40:43]\n" \
"v_mul_f32_e32 v208, v56, v208\n" \
"v_mul_f32_e32 v209, v56, v209\n" \
M_"v[80:83], a[132:135], a[100:103], v[80:83]\n" \
"v_max_f32_e32 v62, v62, v57\n" \
C_"v202, v204, v205\n" \
M_"v[80:83], a[136:139], a[104:107], v[80:83]\n" \
B_"v53, s[8:11], s64 offen offset:1024 sc1 lds\n" \
M_"v[80:83], a[140:143], a[108:111], v[80:83]\n" \
C_"v203, v206, v207\n" \
C_"v208, v208, v209\n" \
"buffer_store_dwordx4 v[200:203], v20, s[56:59], s70 offen offset:1024 sc1\n" \
M_"v[84:87], a[156:159], a[108:111], v[40:43]\n" \
"v_mul_f32_e32 v210, v56, v210\n" \
"v_mul_f32_e32 v211, v56, v211\n" \
M_"v[84:87], a[152:155], a[104:107], v[84:87]\n" \
"v_rcp_f32_e32 v57, v57\n" \
M_"v[84:87], a[148:151], a[100:103], v[84:87]\n" \
B_"v53, s[8:11], s64 offen offset:2048 sc1 lds\n" \
M_"v[84:87], a[144:147], a[96:99], v[84:87]\n" \
M_"a[208:211], a[216:219], v[200:203], a[208:211]\n" \
"v_mfma_f32_32x32x16_bf16 v[192:207], a[216:219], a[216:219], 0\n" \
C_"v209, v210, v211\n" \
"v_mul_f32_e32 v212, v56, v212\n" \
".fill (L_ab + 20480 - .) / 4, 4, 0xBF800000\n" \
"L_ap5:\n" \
"s_cbranch_execz L_ap0\n" \
"v_mul_f32_e32 v213, v56, v213\n" \
"v_mul_f32_e32 v214, v56, v214\n" \
"v_mul_f32_e32 v215, v56, v215\n" \
"v_mul_f32_e32 v216, v56, v216\n" \
M_"v[92:95], a[144:147], a[112:115], v[44:47]\n" \
R_"a[144:147], v48 offset:12288\n" \
"v_mul_f32_e32 v217, v56, v217\n" \
M_"v[92:95], a[148:151], a[116:119], v[92:95]\n" \
R_"a[148:151], v48 offset:13312\n" \
C_"v210, v212, v213\n" \
M_"v[92:95], a[152:155], a[120:123], v[92:95]\n" \
R_"a[152:155], v48 offset:14336\n" \
C_"v211, v214, v215\n" \
"buffer_store_dwordx4 v[208:211], v20, s[56:59], s70 offen offset:2048 sc1\n" \
M_"v[92:95], a[156:159], a[124:127], v[92:95]\n" \
R_"a[156:159], v48 offset:15360\n" \
C_"v216, v216, v217\n" \
M_"v[88:91], a[140:143], a[124:127], v[44:47]\n" \
R_"a[140:143], v48 offset:11264\n" \
"v_mul_f32_e32 v218, v56, v218\n" \
M_"v[88:91], a[136:139], a[120:123], v[88:91]\n" \
R_"a[136:139], v48 offset:10240\n" \
"v_mul_f32_e32 v219, v56, v219\n" \
M_"v[88:91], a[132:135], a[116:119], v[88:91]\n" \
R_"a[132:135], v48 offset:9216\n" \
"v_mul_f32_e32 v57, v57, v61\n" \
M_"v[88:91], a[128:131], a[112:115], v[88:91]\n" \
R_"a[128:131], v48 offset:8192\n" \
M_"a[208:211], a[216:219], v[208:211], a[208:211]\n" \
C_"v217, v218, v219\n" \
"v_mul_f32_e32 v220, v56, v220\n" \
"v_mul_f32_e32 v221, v56, v221\n" \
"v_mul_f32_e32 v222, v56, v222\n" \
"v_mul_f32_e32 v223, v56, v223\n" \
"v_mul_f32_e32 v224, v57, v224\n" \
"v_mul_f32_e32 v225, v57, v225\n" \
C_"v218, v220, v221\n" \
C_"v219, v222, v223\n" \
"buffer_store_dwordx4 v[216:219], v20, s[56:59], s70 offen offset:3072 sc1\n" \
C_"v224, v224, v225\n" \
"v_mul_f32_e32 v226, v57, v226\n" \
"v_mul_f32_e32 v227, v57, v227\n" \
M_"a[208:211], a[216:219], v[216:219], a[208:211]\n" \
"v_mfma_f32_32x32x16_bf16 v[208:223], a[216:219], a[216:219], 0\n" \
C_"v225, v226, v227\n" \
"v_mul_f32_e32 v228, v57, v228\n" \
"v_mul_f32_e32 v229, v57, v229\n" \
"v_mul_f32_e32 v230, v57, v230\n" \
"v_mul_f32_e32 v231, v57, v231\n" \
"v_mul_f32_e32 v232, v57, v232\n" \
"v_mul_f32_e32 v233, v57, v233\n" \
C_"v226, v228, v229\n" \
C_"v227, v230, v231\n" \
"buffer_store_dwordx4 v[224:227], v20, s[56:59], s71 offen offset:0 sc1\n" \
C_"v232, v232, v233\n" \
"v_mul_f32_e32 v234, v57, v234\n" \
"v_mul_f32_e32 v235, v57, v235\n" \
M_"a[208:211], a[216:219], v[224:227], a[208:211]\n" \
C_"v233, v234, v235\n" \
"v_mul_f32_e32 v236, v57, v236\n" \
"v_mul_f32_e32 v237, v57, v237\n" \
"v_mul_f32_e32 v238, v57, v238\n" \
"v_mul_f32_e32 v239, v57, v239\n" \
"v_mul_f32_e32 v240, v57, v240\n" \
"v_mul_f32_e32 v241, v57, v241\n" \
C_"v234, v236, v237\n" \
C_"v235, v238, v239\n" \
"buffer_store_dwordx4 v[232:235], v20, s[56:59], s71 offen offset:1024 sc1\n" \
C_"v240, v240, v241\n" \
"v_mul_f32_e32 v242, v57, v242\n" \
"v_mul_f32_e32 v243, v57, v243\n" \
M_"a[208:211], a[216:219], v[232:235], a[208:211]\n" \
"v_mfma_f32_32x32x16_bf16 v[224:239], a[216:219], a[216:219], 0\n" \
C_"v241, v242, v243\n" \
"v_mul_f32_e32 v244, v57, v244\n" \
"v_mul_f32_e32 v245, v57, v245\n" \
"v_mul_f32_e32 v246, v57, v246\n" \
"v_mul_f32_e32 v247, v57, v247\n" \
"v_mul_f32_e32 v248, v57, v248\n" \
"v_mul_f32_e32 v249, v57, v249\n" \
C_"v242, v244, v245\n" \
C_"v243, v246, v247\n" \
"buffer_store_dwordx4 v[240:243], v20, s[56:59], s71 offen offset:2048 sc1\n" \
C_"v248, v248, v249\n" \
"v_mul_f32_e32 v250, v57, v250\n" \
"v_mul_f32_e32 v251, v57, v251\n" \
M_"a[208:211], a[216:219], v[240:243], a[208:211]\n" \
C_"v249, v250, v251\n" \
"v_mul_f32_e32 v252, v57, v252\n" \
"v_mul_f32_e32 v253, v57, v253\n" \
"v_mul_f32_e32 v254, v57, v254\n" \
"v_mul_f32_e32 v255, v57, v255\n" \
C_"v250, v252, v253\n" \
C_"v251, v254, v255\n" \
"buffer_store_dwordx4 v[248:251], v20, s[56:59], s71 offen offset:3072 sc1\n" \
B_"v53, s[8:11], s64 offen offset:3072 sc1 lds\n" \
"s_waitcnt vmcnt(16) lgkmcnt(0)\n" \
"s_barrier\n" \
M_"a[208:211], a[216:219], v[248:251], a[208:211]\n" \
"v_mfma_f32_32x32x16_bf16 v[240:255], a[216:219], a[216:219], 0\n" \
E_"v70, v70\n" \
E_"v71, v71\n" \
E_"v72, v72\n" \
E_"v73, v73\n" \
"s_add_u32 s64, s64, 0x10000\n" \
"v_cmp_lt_f32_e64 s[72:73], s21, v62\n" \
"v_readlane_b32 s95, v11, 3\n" \
"v_accvgpr_read_b32 v63, a208\n" \
"v_cmp_u_f32_e32 vcc, v63, v63\n" \
E_"v68, v68\n" \
E_"v69, v69\n" \
E_"v74, v74\n" \
E_"v75, v75\n" \
"s_or_b64 vcc, s[72:73], vcc\n" \
"s_cselect_b32 s95, 0x40000000, s95\n" \
"s_cselect_b32 s94, 0x7f800000, 0\n" \
C_"v71, v70, v71\n" \
E_"v66, v66\n" \
E_"v67, v67\n" \
E_"v64, v64\n" \
E_"v65, v65\n" \
C_"v72, v72, v73\n" \
E_"v76, v76\n" \
E_"v77, v77\n" \
E_"v78, v78\n" \
E_"v79, v79\n" \
C_"v70, v68, v69\n" \
C_"v73, v74, v75\n" \
"v_add_u32_e32 v49, 0x10000, v48\n" \
"s_xor_b32 s65, s65, 0x8000\n" \
C_"v69, v66, v67\n" \
C_"v68, v64, v65\n" \
C_"v74, v76, v77\n" \
C_"v75, v78, v79\n" \
"v_xor_b32_e32 v48, 0x8000, v48\n" \
"s_sub_u32 s66, s19, 2\n" \
"s_branch L_2_%=\n" \
"L_f1_%=:\n" \
R_"a[160:163], v49 offset:0\n" \
R_"a[164:167], v49 offset:1024\n" \
R_"a[168:171], v49 offset:2048\n" \
M_"v[104:107], a[128:131], a[80:83], v[36:39]\n" \
C_"v192, v192, v193\n" \
C_"v224, v224, v225\n" \
C_"v193, v194, v195\n" \
C_"v225, v226, v227\n" \
R_"a[172:175], v49 offset:3072\n" \
C_"v194, v196, v197\n" \
C_"v195, v198, v199\n" \
C_"v226, v228, v229\n" \
C_"v227, v230, v231\n" \
M_"v[104:107], a[132:135], a[84:87], v[104:107]\n" \
"v_lshl_add_u32 v50, s15, 2, v20\n" \
C_"v196, v200, v201\n" \
C_"v197, v202, v203\n" \
C_"v198, v204, v205\n" \
C_"v199, v206, v207\n" \
C_"v228, v232, v233\n" \
C_"v229, v234, v235\n" \
C_"v230, v236, v237\n" \
C_"v231, v238, v239\n" \
"v_add_u32_e32 v50, 0x1a000, v50\n" \
"v_mov_b32_e32 v61, s62\n" \
C_"v200, v208, v209\n" \
C_"v201, v210, v211\n" \
C_"v202, v212, v213\n" \
C_"v203, v214, v215\n" \
C_"v204, v216, v217\n" \
C_"v205, v218, v219\n" \
C_"v206, v220, v221\n" \
C_"v207, v222, v223\n" \
C_"v232, v240, v241\n" \
C_"v233, v242, v243\n" \
C_"v234, v244, v245\n" \
C_"v235, v246, v247\n" \
C_"v236, v248, v249\n" \
C_"v237, v250, v251\n" \
C_"v238, v252, v253\n" \
C_"v239, v254, v255\n" \
"ds_write_b128 v50, v[192:195] offset:0\n" \
M_"v[104:107], a[136:139], a[88:91], v[104:107]\n" \
R_"a[176:179], v49 offset:4096\n" \
"v_add_u32_e32 v56, 0x49000, v20\n" \
M_"v[104:107], a[140:143], a[92:95], v[104:107]\n" \
"ds_write_b128 v50, v[196:199] offset:1024\n" \
M_"v[108:111], a[156:159], a[92:95], v[36:39]\n" \
R_"a[180:183], v49 offset:5120\n" \
R_"a[184:187], v49 offset:6144\n" \
M_"v[108:111], a[152:155], a[88:91], v[108:111]\n" \
"ds_write_b128 v50, v[200:203] offset:2048\n" \
M_"v[108:111], a[148:151], a[84:87], v[108:111]\n" \
"v_and_b32_e32 v68, v12, v68\n" \
"v_and_b32_e32 v69, v13, v69\n" \
M_"v[108:111], a[144:147], a[80:83], v[108:111]\n" \
"ds_write_b128 v50, v[204:207] offset:3072\n" \
"v_and_b32_e32 v70, v14, v70\n" \
"v_and_b32_e32 v71, v15, v71\n" \
R_"a[188:191], v49 offset:7168\n" \
R_"a[0:3], v20 offset:8192\n" \
"s_waitcnt lgkmcnt(9)\n" \
M_"v[128:131], a[160:163], v[68:71], v[128:131]\n" \
"ds_write_b128 v50, v[224:227] offset:4096\n" \
M_"v[160:163], a[160:163], v[72:75], v[160:163]\n" \
R_"a[4:7], v20 offset:9216\n" \
R_"a[8:11], v20 offset:10240\n" \
M_"v[132:135], a[164:167], v[68:71], v[132:135]\n" \
"ds_write_b128 v50, v[228:231] offset:5120\n" \
M_"v[164:167], a[164:167], v[72:75], v[164:167]\n" \
R_"a[12:15], v20 offset:11264\n" \
R_"a[28:31], v20 offset:15360\n" \
M_"v[136:139], a[168:171], v[68:71], v[136:139]\n" \
"ds_write_b128 v50, v[232:235] offset:6144\n" \
M_"v[168:171], a[168:171], v[72:75], v[168:171]\n" \
R_"a[24:27], v20 offset:14336\n" \
R_"a[20:23], v20 offset:13312\n" \
M_"v[140:143], a[172:175], v[68:71], v[140:143]\n" \
"ds_write_b128 v50, v[236:239] offset:7168\n" \
M_"v[172:175], a[172:175], v[72:75], v[172:175]\n" \
R_"a[16:19], v20 offset:12288\n" \
"s_waitcnt lgkmcnt(15)\n" \
M_"v[144:147], a[176:179], v[68:71], v[144:147]\n" \
"v_xor_b32_e32 v48, 0x4000, v49\n" \
"ds_read_b32 v58, v18 offset:256\n" \
M_"v[176:179], a[176:179], v[72:75], v[176:179]\n" \
"ds_read_b32 v59, v18 offset:384\n" \
R_"a[32:35], v48 offset:8192\n" \
M_"v[148:151], a[180:183], v[68:71], v[148:151]\n" \
R_"a[36:39], v48 offset:9216\n" \
R_"a[40:43], v48 offset:10240\n" \
M_"v[180:183], a[180:183], v[72:75], v[180:183]\n" \
R_"a[44:47], v48 offset:11264\n" \
R_"a[48:51], v48 offset:12288\n" \
M_"v[152:155], a[184:187], v[68:71], v[152:155]\n" \
R_"a[52:55], v48 offset:13312\n" \
R_"a[56:59], v48 offset:14336\n" \
M_"v[184:187], a[184:187], v[72:75], v[184:187]\n" \
R_"a[60:63], v48 offset:15360\n" \
"s_waitcnt lgkmcnt(15)\n" \
M_"v[156:159], a[188:191], v[68:71], v[156:159]\n" \
R_"a[128:131], v49 offset:8192\n" \
R_"a[132:135], v49 offset:9216\n" \
M_"v[188:191], a[188:191], v[72:75], v[188:191]\n" \
R_"a[136:139], v49 offset:10240\n" \
R_"a[140:143], v49 offset:11264\n" \
"v_smfmac_f32_16x16x64_bf16 a[192:195], a[200:203], v[68:75], v21\n" \
R_"a[144:147], v49 offset:12288\n" \
R_"a[148:151], v49 offset:13312\n" \
M_"v[112:115], a[0:3], a[64:67], v[32:35]\n" \
R_"a[152:155], v49 offset:14336\n" \
R_"a[156:159], v49 offset:15360\n" \
M_"v[112:115], a[4:7], a[68:71], v[112:115]\n" \
"v_accvgpr_read_b32 v40, a196\n" \
"v_accvgpr_read_b32 v24, a197\n" \
M_"v[112:115], a[8:11], a[72:75], v[112:115]\n" \
"v_accvgpr_read_b32 v41, a198\n" \
"v_accvgpr_read_b32 v25, a199\n" \
M_"v[112:115], a[12:15], a[76:79], v[112:115]\n" \
"v_add_f32_e32 v40, v40, v24\n" \
"v_add_f32_e32 v41, v41, v25\n" \
M_"v[116:119], a[28:31], a[76:79], v[32:35]\n" \
"v_add_f32_e32 v40, s94, v40\n" \
"s_waitcnt lgkmcnt(15)\n" \
M_"v[116:119], a[24:27], a[72:75], v[116:119]\n" \
"v_add_f32_e32 v41, s94, v41\n" \
"ds_write_b64 v19, v[40:41]\n" \
M_"v[116:119], a[20:23], a[68:71], v[116:119]\n" \
"v_add_u32_e32 v52, s15, v20\n" \
"v_add_u32_e32 v51, 0x10000, v52\n" \
M_"v[116:119], a[16:19], a[64:67], v[116:119]\n" \
E_"v104, v104\n" \
M_"v[124:127], a[16:19], a[80:83], v[36:39]\n" \
E_"v105, v105\n" \
M_"v[124:127], a[20:23], a[84:87], v[124:127]\n" \
E_"v106, v106\n" \
M_"v[124:127], a[24:27], a[88:91], v[124:127]\n" \
E_"v107, v107\n" \
M_"v[124:127], a[28:31], a[92:95], v[124:127]\n" \
C_"v104, v104, v105\n" \
C_"v105, v106, v107\n" \
M_"v[120:123], a[12:15], a[92:95], v[36:39]\n" \
E_"v108, v108\n" \
M_"v[120:123], a[8:11], a[88:91], v[120:123]\n" \
E_"v109, v109\n" \
M_"v[120:123], a[4:7], a[84:87], v[120:123]\n" \
E_"v110, v110\n" \
M_"v[120:123], a[0:3], a[80:83], v[120:123]\n" \
E_"v111, v111\n" \
"s_waitcnt lgkmcnt(0)\n" \
"s_barrier\n" \
R_"v[208:211], v52 offset:0\n" \
R_"v[212:215], v52 offset:1024\n" \
R_"v[216:219], v52 offset:2048\n" \
R_"v[220:223], v52 offset:3072\n" \
R_"v[240:243], v51 offset:32768\n" \
R_"v[244:247], v51 offset:33792\n" \
R_"v[248:251], v51 offset:34816\n" \
R_"v[252:255], v51 offset:35840\n" \
C_"v106, v108, v109\n" \
C_"v107, v110, v111\n" \
"v_and_b32_e32 v104, v12, v104\n" \
"v_and_b32_e32 v105, v13, v105\n" \
"v_and_b32_e32 v106, v14, v106\n" \
"s_waitcnt vmcnt(0)\n" \
"s_mov_b64 exec, 1\n" \
"buffer_store_dword v61, off, s[56:59], s95 offset:0 sc1\n" \
"s_mov_b64 exec, -1\n" \
"v_and_b32_e32 v107, v15, v107\n" \
"v_xor_b32_e32 v63, 0x400, v19\n" \
"ds_read_b64 v[42:43], v63\n" \
M_"a[192:195], a[212:215], v[104:107], a[192:195]\n" \
"v_mov_b32_e32 v194, v28\n" \
"v_mov_b32_e32 v195, v29\n" \
M_"v[160:163], a[128:131], v[104:107], v[160:163]\n" \
"v_mov_b32_e32 v196, 0\n" \
"v_mov_b32_e32 v197, 0\n" \
M_"v[164:167], a[132:135], v[104:107], v[164:167]\n" \
"v_mov_b32_e32 v192, 0\n" \
"v_mov_b32_e32 v193, 0\n" \
M_"v[168:171], a[136:139], v[104:107], v[168:171]\n" \
"v_readlane_b32 s93, v11, 25\n" \
"v_readlane_b32 s92, v11, 30\n" \
M_"v[172:175], a[140:143], v[104:107], v[172:175]\n" \
B_"a[96:99], v20, s[56:59], s60 offen offset:0 sc1\n" \
M_"v[176:179], a[144:147], v[104:107], v[176:179]\n" \
E_"v118, v118\n" \
M_"v[180:183], a[148:151], v[104:107], v[180:183]\n" \
E_"v119, v119\n" \
M_"v[184:187], a[152:155], v[104:107], v[184:187]\n" \
"s_add_u32 m0, s93, 0x0\n" \
C_"v119, v118, v119\n" \
M_"v[188:191], a[156:159], v[104:107], v[188:191]\n" \
E_"v120, v120\n" \
"s_waitcnt lgkmcnt(1)\n" \
"s_barrier\n" \
B_"a[112:115], v56, s[56:59], s60 offen offset:0 sc1\n" \
M_"v[128:131], v[194:197], v[208:211], v[128:131]\n" \
E_"v121, v121\n" \
M_"v[132:135], v[192:195], v[208:211], v[132:135]\n" \
E_"v116, v116\n" \
M_"v[136:139], v[194:197], v[212:215], v[136:139]\n" \
E_"v117, v117\n" \
M_"v[140:143], v[192:195], v[212:215], v[140:143]\n" \
E_"v122, v122\n" \
M_"v[144:147], v[194:197], v[216:219], v[144:147]\n" \
E_"v123, v123\n" \
M_"v[148:151], v[192:195], v[216:219], v[148:151]\n" \
B_"a[100:103], v20, s[56:59], s60 offen offset:1024 sc1\n" \
M_"v[152:155], v[194:197], v[220:223], v[152:155]\n" \
E_"v114, v114\n" \
M_"v[156:159], v[192:195], v[220:223], v[156:159]\n" \
E_"v115, v115\n" \
M_"v[160:163], v[194:197], v[240:243], v[160:163]\n" \
E_"v112, v112\n" \
M_"v[164:167], v[192:195], v[240:243], v[164:167]\n" \
E_"v113, v113\n" \
M_"v[168:171], v[194:197], v[244:247], v[168:171]\n" \
C_"v120, v120, v121\n" \
C_"v118, v116, v117\n" \
M_"v[172:175], v[192:195], v[244:247], v[172:175]\n" \
B_"a[116:119], v56, s[56:59], s60 offen offset:1024 sc1\n" \
M_"v[176:179], v[194:197], v[248:251], v[176:179]\n" \
E_"v124, v124\n" \
M_"v[180:183], v[192:195], v[248:251], v[180:183]\n" \
E_"v125, v125\n" \
M_"v[184:187], v[194:197], v[252:255], v[184:187]\n" \
E_"v126, v126\n" \
M_"v[188:191], v[192:195], v[252:255], v[188:191]\n" \
E_"v127, v127\n" \
C_"v121, v122, v123\n" \
C_"v117, v114, v115\n" \
C_"v116, v112, v113\n" \
C_"v122, v124, v125\n" \
C_"v123, v126, v127\n" \
B_"a[104:107], v20, s[56:59], s60 offen offset:2048 sc1\n" \
"s_nop 0\n" \
"v_smfmac_f32_16x16x64_bf16 a[192:195], a[200:203], v[116:123], v21\n" \
M_"v[128:131], a[32:35], v[116:119], v[128:131]\n" \
M_"v[160:163], a[32:35], v[120:123], v[160:163]\n" \
M_"v[132:135], a[36:39], v[116:119], v[132:135]\n" \
M_"v[164:167], a[36:39], v[120:123], v[164:167]\n" \
M_"v[136:139], a[40:43], v[116:119], v[136:139]\n" \
B_"a[120:123], v56, s[56:59], s60 offen offset:2048 sc1\n" \
M_"v[168:171], a[40:43], v[120:123], v[168:171]\n" \
M_"v[140:143], a[44:47], v[116:119], v[140:143]\n" \
"v_accvgpr_read_b32 v54, a192\n" \
"v_accvgpr_read_b32 v22, a193\n" \
M_"v[172:175], a[44:47], v[120:123], v[172:175]\n" \
"v_add_f32_e32 v54, v54, v22\n" \
"s_waitcnt lgkmcnt(0)\n" \
"v_add_f32_e32 v54, v54, v42\n" \
M_"v[144:147], a[48:51], v[116:119], v[144:147]\n" \
"v_max_f32_e32 v62, v62, v54\n" \
"v_accvgpr_read_b32 v55, a194\n" \
M_"v[176:179], a[48:51], v[120:123], v[176:179]\n" \
"v_rcp_f32_e32 v54, v54\n" \
B_"a[108:111], v20, s[56:59], s60 offen offset:3072 sc1\n" \
M_"v[148:151], a[52:55], v[116:119], v[148:151]\n" \
"v_mul_f32_e32 v54, v54, v58\n" \
"v_mul_f32_e32 v128, v54, v128\n" \
M_"v[180:183], a[52:55], v[120:123], v[180:183]\n" \
"v_mul_f32_e32 v129, v54, v129\n" \
"v_mul_f32_e32 v130, v54, v130\n" \
"v_mul_f32_e32 v131, v54, v131\n" \
"v_mul_f32_e32 v132, v54, v132\n" \
"v_mul_f32_e32 v133, v54, v133\n" \
"v_mul_f32_e32 v134, v54, v134\n" \
"v_mul_f32_e32 v135, v54, v135\n" \
C_"v64, v128, v129\n" \
C_"v65, v130, v131\n" \
C_"v66, v132, v133\n" \
C_"v67, v134, v135\n" \
"v_accvgpr_read_b32 v23, a195\n" \
M_"v[152:155], a[56:59], v[116:119], v[152:155]\n" \
M_"a[208:211], a[216:219], v[64:67], a[208:211]\n" \
B_"a[124:127], v56, s[56:59], s60 offen offset:3072 sc1\n" \
"v_mul_f32_e32 v136, v54, v136\n" \
"v_mul_f32_e32 v137, v54, v137\n" \
"v_mul_f32_e32 v138, v54, v138\n" \
"v_mul_f32_e32 v139, v54, v139\n" \
"v_mul_f32_e32 v140, v54, v140\n" \
"v_mul_f32_e32 v141, v54, v141\n" \
"v_mul_f32_e32 v142, v54, v142\n" \
"v_mul_f32_e32 v143, v54, v143\n" \
"v_add_f32_e32 v55, v55, v23\n" \
C_"v68, v136, v137\n" \
C_"v69, v138, v139\n" \
C_"v70, v140, v141\n" \
C_"v71, v142, v143\n" \
"v_add_f32_e32 v55, v55, v43\n" \
"v_mul_f32_e32 v144, v54, v144\n" \
M_"a[208:211], a[216:219], v[68:71], a[208:211]\n" \
"v_mul_f32_e32 v145, v54, v145\n" \
"v_mul_f32_e32 v146, v54, v146\n" \
"v_mul_f32_e32 v147, v54, v147\n" \
"v_mul_f32_e32 v148, v54, v148\n" \
B_"v0, s[80:83], s92 offen offset:0 lds\n" \
"v_mul_f32_e32 v149, v54, v149\n" \
"v_mul_f32_e32 v150, v54, v150\n" \
"v_mul_f32_e32 v151, v54, v151\n" \
"v_max_f32_e32 v62, v62, v55\n" \
C_"v72, v144, v145\n" \
C_"v73, v146, v147\n" \
C_"v74, v148, v149\n" \
C_"v75, v150, v151\n" \
"v_rcp_f32_e32 v55, v55\n" \
M_"v[184:187], a[56:59], v[120:123], v[184:187]\n" \
M_"a[208:211], a[216:219], v[72:75], a[208:211]\n" \
"v_mul_f32_e32 v152, v54, v152\n" \
"v_mul_f32_e32 v153, v54, v153\n" \
"v_mul_f32_e32 v154, v54, v154\n" \
"v_mul_f32_e32 v155, v54, v155\n" \
C_"v76, v152, v153\n" \
B_"v1, s[80:83], s92 offen offset:1024 lds\n" \
C_"v77, v154, v155\n" \
"v_mul_f32_e32 v55, v55, v59\n" \
"v_mul_f32_e32 v160, v55, v160\n" \
"v_mul_f32_e32 v161, v55, v161\n" \
"v_mul_f32_e32 v162, v55, v162\n" \
"v_mul_f32_e32 v163, v55, v163\n" \
"v_mul_f32_e32 v164, v55, v164\n" \
"v_mul_f32_e32 v165, v55, v165\n" \
"v_mul_f32_e32 v166, v55, v166\n" \
"v_mul_f32_e32 v167, v55, v167\n" \
C_"v80, v160, v161\n" \
C_"v81, v162, v163\n" \
C_"v82, v164, v165\n" \
C_"v83, v166, v167\n" \
M_"v[156:159], a[60:63], v[116:119], v[156:159]\n" \
"v_mul_f32_e32 v168, v55, v168\n" \
"v_mul_f32_e32 v169, v55, v169\n" \
"v_mul_f32_e32 v170, v55, v170\n" \
"v_mul_f32_e32 v171, v55, v171\n" \
"v_mul_f32_e32 v172, v55, v172\n" \
B_"v2, s[80:83], s92 offen offset:2048 lds\n" \
"v_mul_f32_e32 v173, v55, v173\n" \
"v_mul_f32_e32 v174, v55, v174\n" \
"v_mul_f32_e32 v156, v54, v156\n" \
"v_mul_f32_e32 v157, v54, v157\n" \
"v_mul_f32_e32 v158, v54, v158\n" \
"v_mul_f32_e32 v159, v54, v159\n" \
C_"v78, v156, v157\n" \
C_"v79, v158, v159\n" \
"v_mul_f32_e32 v175, v55, v175\n" \
C_"v84, v168, v169\n" \
M_"a[208:211], a[216:219], v[76:79], a[208:211]\n" \
M_"a[208:211], a[216:219], v[80:83], a[208:211]\n" \
C_"v85, v170, v171\n" \
C_"v86, v172, v173\n" \
C_"v87, v174, v175\n" \
"v_mul_f32_e32 v176, v55, v176\n" \
"v_mul_f32_e32 v177, v55, v177\n" \
B_"v3, s[80:83], s92 offen offset:3072 lds\n" \
"s_add_u32 m0, s93, 0x1000\n" \
M_"a[208:211], a[216:219], v[84:87], a[208:211]\n" \
"v_mul_f32_e32 v178, v55, v178\n" \
"v_mul_f32_e32 v179, v55, v179\n" \
"v_mul_f32_e32 v180, v55, v180\n" \
"v_mul_f32_e32 v181, v55, v181\n" \
"v_mul_f32_e32 v182, v55, v182\n" \
"v_mul_f32_e32 v183, v55, v183\n" \
C_"v96, v176, v177\n" \
C_"v97, v178, v179\n" \
C_"v98, v180, v181\n" \
C_"v99, v182, v183\n" \
M_"v[188:191], a[60:63], v[120:123], v[188:191]\n" \
"v_mul_f32_e32 v184, v55, v184\n" \
M_"a[208:211], a[216:219], v[96:99], a[208:211]\n" \
"v_mul_f32_e32 v185, v55, v185\n" \
"v_mul_f32_e32 v186, v55, v186\n" \
B_"v4, s[80:83], s92 offen offset:0 lds\n" \
"v_mul_f32_e32 v187, v55, v187\n" \
C_"v100, v184, v185\n" \
C_"v101, v186, v187\n" \
"v_mul_f32_e32 v188, v55, v188\n" \
"v_mul_f32_e32 v189, v55, v189\n" \
"v_mul_f32_e32 v190, v55, v190\n" \
"v_mul_f32_e32 v191, v55, v191\n" \
C_"v102, v188, v189\n" \
C_"v103, v190, v191\n" \
"v_cmp_lt_f32_e64 s[72:73], s21, v62\n" \
B_"v5, s[80:83], s92 offen offset:1024 lds\n" \
M_"a[208:211], a[216:219], v[100:103], a[208:211]\n" \
B_"v6, s[80:83], s92 offen offset:2048 lds\n" \
B_"v7, s[80:83], s92 offen offset:3072 lds\n" \
"s_add_u32 m0, s93, 0x2000\n" \
"s_nop 0\n" \
B_"v8, s[80:83], s92 offen offset:0 lds\n" \
"s_nop 2\n" \
"v_accvgpr_read_b32 v63, a208\n" \
"v_cmp_u_f32_e32 vcc, v63, v63\n" \
"s_nop 0\n" \
"s_or_b64 vcc, s[72:73], vcc\n" \
"s_cbranch_scc1 L_end_%=\n" \
"s_branch L_out_%=\n" \
".fill (L_ab + 24576 - .) / 4, 4, 0xBF800000\n" \
".fill (L_ab + 24576 - .) / 4, 4, 0xBF800000\n" \
"L_ap6:\n" \
"s_setpc_b64 s[88:89]\n" \
"L_gs7:\n" \
M_"v[104:107], a[128:131], a[16:19], v[36:39]\n" \
"s_add_u32 s68, s60, 0xfff6e000\n" \
"s_add_u32 s69, s60, 0xfffb7000\n" \
"s_add_u32 s70, s60, 0x0\n" \
"s_add_u32 s71, s60, 0x49000\n" \
M_"v[104:107], a[132:135], a[20:23], v[104:107]\n" \
"s_xor_b32 m0, s65, 0x12000\n" \
R_"a[160:163], v49 offset:0\n" \
M_"v[104:107], a[136:139], a[24:27], v[104:107]\n" \
B_"v50, s[8:11], s64 offen offset:0 sc1 lds\n" \
M_"v[104:107], a[140:143], a[28:31], v[104:107]\n" \
R_"a[164:167], v49 offset:1024\n" \
R_"a[168:171], v49 offset:2048\n" \
M_"v[108:111], a[156:159], a[28:31], v[36:39]\n" \
R_"a[172:175], v49 offset:3072\n" \
R_"a[176:179], v49 offset:4096\n" \
M_"v[108:111], a[152:155], a[24:27], v[108:111]\n" \
B_"v50, s[8:11], s64 offen offset:1024 sc1 lds\n" \
M_"v[108:111], a[148:151], a[20:23], v[108:111]\n" \
"s_mov_b32 s65, s13\n" \
"v_add_u32_e32 v63, s15, v20\n" \
M_"v[108:111], a[144:147], a[16:19], v[108:111]\n" \
"s_add_u32 s67, s14, 40960\n" \
"v_add_u32_e32 v50, 0x6000, v63\n" \
M_"v[116:119], a[144:147], a[32:35], v[40:43]\n" \
"s_xor_b32 m0, s65, 0x8000\n" \
R_"a[180:183], v49 offset:5120\n" \
M_"v[116:119], a[148:151], a[36:39], v[116:119]\n" \
R_"a[184:187], v49 offset:6144\n" \
"v_and_b32_e32 v68, v12, v68\n" \
M_"v[116:119], a[152:155], a[40:43], v[116:119]\n" \
"v_and_b32_e32 v69, v13, v69\n" \
"v_and_b32_e32 v70, v14, v70\n" \
M_"v[116:119], a[156:159], a[44:47], v[116:119]\n" \
B_"v50, s[8:11], s67 offen offset:0 sc1 lds\n" \
M_"v[112:115], a[140:143], a[44:47], v[40:43]\n" \
"v_and_b32_e32 v71, v15, v71\n" \
R_"a[188:191], v49 offset:7168\n" \
M_"v[112:115], a[136:139], a[40:43], v[112:115]\n" \
E_"v86, v86\n" \
M_"v[112:115], a[132:135], a[36:39], v[112:115]\n" \
E_"v87, v87\n" \
M_"v[112:115], a[128:131], a[32:35], v[112:115]\n" \
E_"v84, v84\n" \
M_"v[120:123], a[128:131], a[48:51], v[44:47]\n" \
B_"v50, s[8:11], s67 offen offset:1024 sc1 lds\n" \
M_"v[120:123], a[132:135], a[52:55], v[120:123]\n" \
E_"v85, v85\n" \
M_"v[120:123], a[136:139], a[56:59], v[120:123]\n" \
C_"v87, v86, v87\n" \
C_"v86, v84, v85\n" \
M_"v[120:123], a[140:143], a[60:63], v[120:123]\n" \
E_"v82, v82\n" \
M_"v[124:127], a[156:159], a[60:63], v[44:47]\n" \
E_"v83, v83\n" \
M_"v[124:127], a[152:155], a[56:59], v[124:127]\n" \
E_"v80, v80\n" \
M_"v[124:127], a[148:151], a[52:55], v[124:127]\n" \
B_"v50, s[8:11], s67 offen offset:2048 sc1 lds\n" \
M_"v[124:127], a[144:147], a[48:51], v[124:127]\n" \
C_"v85, v82, v83\n" \
"s_waitcnt lgkmcnt(1)\n" \
M_"v[128:131], a[160:163], v[68:71], v[128:131]\n" \
E_"v81, v81\n" \
M_"v[160:163], a[160:163], v[72:75], v[160:163]\n" \
C_"v84, v80, v81\n" \
"s_mov_b32 s64, s14\n" \
M_"v[132:135], a[164:167], v[68:71], v[132:135]\n" \
E_"v88, v88\n" \
M_"v[164:167], a[164:167], v[72:75], v[164:167]\n" \
E_"v89, v89\n" \
M_"v[136:139], a[168:171], v[68:71], v[136:139]\n" \
B_"v50, s[8:11], s67 offen offset:3072 sc1 lds\n" \
M_"v[168:171], a[168:171], v[72:75], v[168:171]\n" \
E_"v90, v90\n" \
M_"v[140:143], a[172:175], v[68:71], v[140:143]\n" \
E_"v91, v91\n" \
M_"v[172:175], a[172:175], v[72:75], v[172:175]\n" \
C_"v88, v88, v89\n" \
C_"v89, v90, v91\n" \
M_"v[144:147], a[176:179], v[68:71], v[144:147]\n" \
E_"v92, v92\n" \
M_"v[176:179], a[176:179], v[72:75], v[176:179]\n" \
E_"v93, v93\n" \
M_"v[148:151], a[180:183], v[68:71], v[148:151]\n" \
E_"v94, v94\n" \
M_"v[180:183], a[180:183], v[72:75], v[180:183]\n" \
E_"v95, v95\n" \
M_"v[152:155], a[184:187], v[68:71], v[152:155]\n" \
C_"v90, v92, v93\n" \
C_"v91, v94, v95\n" \
M_"v[184:187], a[184:187], v[72:75], v[184:187]\n" \
"v_add_u32_e32 v51, 0x12000, v63\n" \
"s_waitcnt lgkmcnt(0)\n" \
M_"v[156:159], a[188:191], v[68:71], v[156:159]\n" \
"s_xor_b32 m0, s65, 0xa000\n" \
"ds_read_b32 v58, v18 offset:512\n" \
M_"v[188:191], a[188:191], v[72:75], v[188:191]\n" \
B_"v51, s[8:11], s64 offen offset:0 sc1 lds\n" \
"v_smfmac_f32_16x16x64_bf16 a[192:195], a[200:203], v[68:75], v21\n" \
"ds_read_b32 v59, v18 offset:640\n" \
"ds_read_b32 v60, v18 offset:768\n" \
M_"v[220:223], a[188:191], v[84:87], v[220:223]\n" \
B_"v51, s[8:11], s64 offen offset:1024 sc1 lds\n" \
M_"v[252:255], a[188:191], v[88:91], v[252:255]\n" \
"ds_read_b32 v61, v18 offset:896\n" \
R_"a[128:131], v48 offset:0\n" \
M_"v[216:219], a[184:187], v[84:87], v[216:219]\n" \
R_"a[144:147], v48 offset:4096\n" \
R_"a[132:135], v48 offset:1024\n" \
M_"v[248:251], a[184:187], v[88:91], v[248:251]\n" \
R_"a[148:151], v48 offset:5120\n" \
R_"a[136:139], v48 offset:2048\n" \
M_"v[212:215], a[180:183], v[84:87], v[212:215]\n" \
R_"a[152:155], v48 offset:6144\n" \
R_"a[140:143], v48 offset:3072\n" \
M_"v[244:247], a[180:183], v[88:91], v[244:247]\n" \
R_"a[156:159], v48 offset:7168\n" \
"v_add_u32_e32 v52, 0x14000, v63\n" \
M_"v[208:211], a[176:179], v[84:87], v[208:211]\n" \
B_"v51, s[8:11], s64 offen offset:2048 sc1 lds\n" \
M_"v[240:243], a[176:179], v[88:91], v[240:243]\n" \
E_"v104, v104\n" \
M_"v[204:207], a[172:175], v[84:87], v[204:207]\n" \
E_"v105, v105\n" \
M_"v[236:239], a[172:175], v[88:91], v[236:239]\n" \
B_"v51, s[8:11], s64 offen offset:3072 sc1 lds\n" \
M_"v[200:203], a[168:171], v[84:87], v[200:203]\n" \
E_"v106, v106\n" \
M_"v[232:235], a[168:171], v[88:91], v[232:235]\n" \
E_"v107, v107\n" \
M_"v[196:199], a[164:167], v[84:87], v[196:199]\n" \
C_"v104, v104, v105\n" \
C_"v105, v106, v107\n" \
M_"v[228:231], a[164:167], v[88:91], v[228:231]\n" \
E_"v108, v108\n" \
M_"v[192:195], a[160:163], v[84:87], v[192:195]\n" \
E_"v109, v109\n" \
M_"v[224:227], a[160:163], v[88:91], v[224:227]\n" \
E_"v110, v110\n" \
"v_smfmac_f32_16x16x64_bf16 a[196:199], a[200:203], v[84:91], v21\n" \
E_"v111, v111\n" \
C_"v106, v108, v109\n" \
C_"v107, v110, v111\n" \
"v_and_b32_e32 v104, v12, v104\n" \
"v_and_b32_e32 v105, v13, v105\n" \
"v_and_b32_e32 v106, v14, v106\n" \
"v_and_b32_e32 v107, v15, v107\n" \
"v_accvgpr_read_b32 v54, a192\n" \
"v_accvgpr_read_b32 v22, a193\n" \
"v_readlane_b32 s93, v11, 22\n" \
"v_readlane_b32 s80, v11, 11\n" \
"v_readlane_b32 s81, v11, 12\n" \
"s_mov_b32 s82, 0x47fca0\n" \
"s_mov_b32 s83, 0x20000\n" \
"v_readlane_b32 s92, v11, 29\n" \
E_"v118, v118\n" \
E_"v119, v119\n" \
E_"v116, v116\n" \
E_"v117, v117\n" \
C_"v119, v118, v119\n" \
E_"v114, v114\n" \
E_"v115, v115\n" \
E_"v112, v112\n" \
C_"v118, v116, v117\n" \
"s_waitcnt vmcnt(10) lgkmcnt(0)\n" \
"s_barrier\n" \
M_"v[80:83], a[128:131], a[32:35], v[40:43]\n" \
R_"a[160:163], v49 offset:8192\n" \
R_"a[164:167], v49 offset:9216\n" \
M_"v[80:83], a[132:135], a[36:39], v[80:83]\n" \
R_"a[168:171], v49 offset:10240\n" \
"s_xor_b32 m0, s65, 0x18000\n" \
M_"v[80:83], a[136:139], a[40:43], v[80:83]\n" \
B_"v52, s[8:11], s64 offen offset:0 sc1 lds\n" \
M_"v[80:83], a[140:143], a[44:47], v[80:83]\n" \
R_"a[172:175], v49 offset:11264\n" \
R_"a[176:179], v49 offset:12288\n" \
M_"v[84:87], a[156:159], a[44:47], v[40:43]\n" \
R_"a[180:183], v49 offset:13312\n" \
R_"a[184:187], v49 offset:14336\n" \
M_"v[84:87], a[152:155], a[40:43], v[84:87]\n" \
B_"v52, s[8:11], s64 offen offset:1024 sc1 lds\n" \
M_"v[84:87], a[148:151], a[36:39], v[84:87]\n" \
R_"a[188:191], v49 offset:15360\n" \
C_"v117, v114, v115\n" \
M_"v[84:87], a[144:147], a[32:35], v[84:87]\n" \
B_"v52, s[8:11], s64 offen offset:2048 sc1 lds\n" \
M_"v[92:95], a[144:147], a[48:51], v[44:47]\n" \
E_"v113, v113\n" \
M_"v[92:95], a[148:151], a[52:55], v[92:95]\n" \
B_"v52, s[8:11], s64 offen offset:3072 sc1 lds\n" \
M_"v[92:95], a[152:155], a[56:59], v[92:95]\n" \
"s_add_u32 m0, s93, 0x0\n" \
C_"v116, v112, v113\n" \
M_"v[92:95], a[156:159], a[60:63], v[92:95]\n" \
E_"v120, v120\n" \
M_"v[88:91], a[140:143], a[60:63], v[44:47]\n" \
B_"v0, s[80:83], s92 offen offset:0 lds\n" \
M_"v[88:91], a[136:139], a[56:59], v[88:91]\n" \
E_"v121, v121\n" \
M_"v[88:91], a[132:135], a[52:55], v[88:91]\n" \
E_"v122, v122\n" \
M_"v[88:91], a[128:131], a[48:51], v[88:91]\n" \
B_"v1, s[80:83], s92 offen offset:1024 lds\n" \
M_"a[192:195], a[212:215], v[104:107], a[192:195]\n" \
C_"v120, v120, v121\n" \
"s_waitcnt lgkmcnt(5)\n" \
M_"v[160:163], a[160:163], v[104:107], v[160:163]\n" \
E_"v123, v123\n" \
M_"v[164:167], a[164:167], v[104:107], v[164:167]\n" \
B_"v2, s[80:83], s92 offen offset:2048 lds\n" \
M_"v[168:171], a[168:171], v[104:107], v[168:171]\n" \
C_"v121, v122, v123\n" \
"s_waitcnt lgkmcnt(1)\n" \
M_"v[172:175], a[172:175], v[104:107], v[172:175]\n" \
E_"v124, v124\n" \
M_"v[176:179], a[176:179], v[104:107], v[176:179]\n" \
E_"v125, v125\n" \
M_"v[180:183], a[180:183], v[104:107], v[180:183]\n" \
E_"v126, v126\n" \
M_"v[184:187], a[184:187], v[104:107], v[184:187]\n" \
C_"v122, v124, v125\n" \
"s_waitcnt lgkmcnt(0)\n" \
M_"v[188:191], a[188:191], v[104:107], v[188:191]\n" \
E_"v127, v127\n" \
M_"v[220:223], a[188:191], v[116:119], v[220:223]\n" \
C_"v123, v126, v127\n" \
B_"v3, s[80:83], s92 offen offset:3072 lds\n" \
"s_add_u32 m0, s93, 0x1000\n" \
M_"v[252:255], a[188:191], v[120:123], v[252:255]\n" \
R_"a[128:131], v48 offset:8192\n" \
R_"a[144:147], v48 offset:12288\n" \
M_"v[216:219], a[184:187], v[116:119], v[216:219]\n" \
B_"v4, s[80:83], s92 offen offset:0 lds\n" \
M_"v[248:251], a[184:187], v[120:123], v[248:251]\n" \
R_"a[132:135], v48 offset:9216\n" \
R_"a[148:151], v48 offset:13312\n" \
M_"v[212:215], a[180:183], v[116:119], v[212:215]\n" \
B_"v5, s[80:83], s92 offen offset:1024 lds\n" \
M_"v[244:247], a[180:183], v[120:123], v[244:247]\n" \
R_"a[136:139], v48 offset:10240\n" \
R_"a[152:155], v48 offset:14336\n" \
M_"v[208:211], a[176:179], v[116:119], v[208:211]\n" \
B_"v6, s[80:83], s92 offen offset:2048 lds\n" \
M_"v[240:243], a[176:179], v[120:123], v[240:243]\n" \
R_"a[140:143], v48 offset:11264\n" \
R_"a[156:159], v48 offset:15360\n" \
M_"v[204:207], a[172:175], v[116:119], v[204:207]\n" \
B_"v7, s[80:83], s92 offen offset:3072 lds\n" \
M_"v[236:239], a[172:175], v[120:123], v[236:239]\n" \
"v_readlane_b32 s96, v11, 1\n" \
"v_lshrrev_b32_e32 v16, 1, v20\n" \
M_"v[200:203], a[168:171], v[116:119], v[200:203]\n" \
"v_add_f32_e32 v54, v54, v22\n" \
"v_max_f32_e32 v62, v62, v54\n" \
M_"v[232:235], a[168:171], v[120:123], v[232:235]\n" \
"v_rcp_f32_e32 v54, v54\n" \
M_"v[196:199], a[164:167], v[116:119], v[196:199]\n" \
"v_mul_f32_e32 v54, v54, v58\n" \
"v_mul_f32_e32 v128, v54, v128\n" \
M_"v[228:231], a[164:167], v[120:123], v[228:231]\n" \
"v_mul_f32_e32 v129, v54, v129\n" \
"v_mul_f32_e32 v136, v54, v136\n" \
M_"v[192:195], a[160:163], v[116:119], v[192:195]\n" \
"v_mul_f32_e32 v137, v54, v137\n" \
"v_mul_f32_e32 v144, v54, v144\n" \
M_"v[224:227], a[160:163], v[120:123], v[224:227]\n" \
"v_mul_f32_e32 v145, v54, v145\n" \
"v_mul_f32_e32 v152, v54, v152\n" \
"v_smfmac_f32_16x16x64_bf16 a[196:199], a[200:203], v[116:123], v21\n" \
"v_mul_f32_e32 v153, v54, v153\n" \
C_"v128, v128, v129\n" \
"v_mul_f32_e32 v130, v54, v130\n" \
"v_mul_f32_e32 v131, v54, v131\n" \
C_"v136, v136, v137\n" \
"v_mul_f32_e32 v138, v54, v138\n" \
"v_mul_f32_e32 v139, v54, v139\n" \
C_"v144, v144, v145\n" \
"v_mul_f32_e32 v146, v54, v146\n" \
"v_mul_f32_e32 v147, v54, v147\n" \
C_"v152, v152, v153\n" \
"v_mul_f32_e32 v154, v54, v154\n" \
"v_mul_f32_e32 v155, v54, v155\n" \
C_"v129, v130, v131\n" \
"v_mul_f32_e32 v132, v54, v132\n" \
"v_mul_f32_e32 v133, v54, v133\n" \
"v_mul_f32_e32 v134, v54, v134\n" \
"v_mul_f32_e32 v135, v54, v135\n" \
C_"v137, v138, v139\n" \
"v_mul_f32_e32 v140, v54, v140\n" \
"v_mul_f32_e32 v141, v54, v141\n" \
"v_mul_f32_e32 v142, v54, v142\n" \
"v_mul_f32_e32 v143, v54, v143\n" \
C_"v145, v146, v147\n" \
"v_mul_f32_e32 v148, v54, v148\n" \
"v_mul_f32_e32 v149, v54, v149\n" \
"v_mul_f32_e32 v150, v54, v150\n" \
"v_mul_f32_e32 v151, v54, v151\n" \
C_"v153, v154, v155\n" \
"v_mul_f32_e32 v156, v54, v156\n" \
"v_mul_f32_e32 v157, v54, v157\n" \
"v_mul_f32_e32 v158, v54, v158\n" \
"v_mul_f32_e32 v159, v54, v159\n" \
C_"v130, v132, v133\n" \
C_"v131, v134, v135\n" \
"buffer_store_dwordx4 v[128:131], v20, s[56:59], s68 offen offset:0 sc1\n" \
C_"v138, v140, v141\n" \
C_"v139, v142, v143\n" \
C_"v146, v148, v149\n" \
C_"v147, v150, v151\n" \
C_"v154, v156, v157\n" \
C_"v155, v158, v159\n" \
"buffer_store_dwordx4 v[136:139], v20, s[56:59], s68 offen offset:1024 sc1\n" \
"s_add_u32 m0, s93, 0x2000\n" \
"v_add_u32_e32 v49, 0x10000, v48\n" \
"s_xor_b32 s65, s65, 0x8000\n" \
"s_add_u32 s64, s64, 0x10000\n" \
E_"v86, v86\n" \
"buffer_store_dwordx4 v[144:147], v20, s[56:59], s68 offen offset:2048 sc1\n" \
E_"v87, v87\n" \
E_"v84, v84\n" \
E_"v85, v85\n" \
"buffer_store_dwordx4 v[152:155], v20, s[56:59], s68 offen offset:3072 sc1\n" \
E_"v88, v88\n" \
E_"v89, v89\n" \
C_"v87, v86, v87\n" \
E_"v82, v82\n" \
B_"v8, s[80:83], s92 offen offset:0 lds\n" \
E_"v83, v83\n" \
E_"v80, v80\n" \
E_"v81, v81\n" \
E_"v90, v90\n" \
E_"v91, v91\n" \
C_"v86, v84, v85\n" \
C_"v88, v88, v89\n" \
E_"v92, v92\n" \
E_"v93, v93\n" \
E_"v94, v94\n" \
E_"v95, v95\n" \
C_"v85, v82, v83\n" \
C_"v84, v80, v81\n" \
C_"v89, v90, v91\n" \
"v_and_b32_e32 v84, v12, v84\n" \
"v_and_b32_e32 v85, v13, v85\n" \
"v_and_b32_e32 v86, v14, v86\n" \
"s_waitcnt vmcnt(17) lgkmcnt(0)\n" \
"s_barrier\n" \
M_"v[120:123], a[128:131], a[48:51], v[44:47]\n" \
"buffer_load_dwordx2 v[26:27], v16, s[56:59], s96 offen sc1\n" \
M_"v[120:123], a[132:135], a[52:55], v[120:123]\n" \
R_"a[188:191], v49 offset:7168\n" \
R_"a[184:187], v49 offset:6144\n" \
M_"v[120:123], a[136:139], a[56:59], v[120:123]\n" \
"s_xor_b32 m0, s65, 0x12000\n" \
R_"a[180:183], v49 offset:5120\n" \
M_"v[120:123], a[140:143], a[60:63], v[120:123]\n" \
B_"v50, s[8:11], s64 offen offset:0 sc1 lds\n" \
M_"v[124:127], a[156:159], a[60:63], v[44:47]\n" \
R_"a[176:179], v49 offset:4096\n" \
R_"a[172:175], v49 offset:3072\n" \
M_"v[124:127], a[152:155], a[56:59], v[124:127]\n" \
B_"v50, s[8:11], s64 offen offset:1024 sc1 lds\n" \
M_"v[124:127], a[148:151], a[52:55], v[124:127]\n" \
R_"a[168:171], v49 offset:2048\n" \
"v_and_b32_e32 v87, v15, v87\n" \
M_"v[124:127], a[144:147], a[48:51], v[124:127]\n" \
C_"v90, v92, v93\n" \
C_"v91, v94, v95\n" \
M_"v[32:35], a[220:223], a[64:67], 0\n" \
B_"v50, s[8:11], s64 offen offset:2048 sc1 lds\n" \
M_"v[36:39], a[220:223], a[80:83], 0\n" \
R_"a[164:167], v49 offset:1024\n" \
R_"a[160:163], v49 offset:0\n" \
M_"v[32:35], a[224:227], a[68:71], v[32:35]\n" \
R_"a[0:3], v49 offset:8192\n" \
R_"a[4:7], v49 offset:9216\n" \
M_"v[36:39], a[224:227], a[84:87], v[36:39]\n" \
R_"a[8:11], v49 offset:10240\n" \
R_"a[12:15], v49 offset:11264\n" \
M_"v[32:35], a[228:231], a[72:75], v[32:35]\n" \
B_"v50, s[8:11], s64 offen offset:3072 sc1 lds\n" \
M_"v[36:39], a[228:231], a[88:91], v[36:39]\n" \
R_"a[16:19], v49 offset:12288\n" \
R_"a[20:23], v49 offset:13312\n" \
M_"v[32:35], a[232:235], a[76:79], v[32:35]\n" \
R_"a[24:27], v49 offset:14336\n" \
R_"a[28:31], v49 offset:15360\n" \
M_"v[36:39], a[232:235], a[92:95], v[36:39]\n" \
E_"v120, v120\n" \
"v_smfmac_f32_16x16x64_bf16 a[196:199], a[200:203], v[84:91], v21\n" \
"s_mov_b32 s63, s62\n" \
"s_waitcnt lgkmcnt(13)\n" \
M_"v[220:223], a[188:191], v[84:87], v[220:223]\n" \
E_"v121, v121\n" \
M_"v[252:255], a[188:191], v[88:91], v[252:255]\n" \
E_"v122, v122\n" \
M_"v[216:219], a[184:187], v[84:87], v[216:219]\n" \
E_"v123, v123\n" \
M_"v[248:251], a[184:187], v[88:91], v[248:251]\n" \
C_"v120, v120, v121\n" \
C_"v121, v122, v123\n" \
M_"v[212:215], a[180:183], v[84:87], v[212:215]\n" \
E_"v124, v124\n" \
M_"v[244:247], a[180:183], v[88:91], v[244:247]\n" \
"v_and_b32_e32 v120, v12, v120\n" \
"s_waitcnt lgkmcnt(10)\n" \
M_"v[208:211], a[176:179], v[84:87], v[208:211]\n" \
E_"v125, v125\n" \
M_"v[240:243], a[176:179], v[88:91], v[240:243]\n" \
E_"v126, v126\n" \
M_"v[204:207], a[172:175], v[84:87], v[204:207]\n" \
E_"v127, v127\n" \
M_"v[236:239], a[172:175], v[88:91], v[236:239]\n" \
C_"v122, v124, v125\n" \
C_"v123, v126, v127\n" \
M_"v[200:203], a[168:171], v[84:87], v[200:203]\n" \
"v_and_b32_e32 v121, v13, v121\n" \
"v_and_b32_e32 v122, v14, v122\n" \
M_"v[232:235], a[168:171], v[88:91], v[232:235]\n" \
"v_and_b32_e32 v123, v15, v123\n" \
"s_waitcnt lgkmcnt(0)\n" \
M_"v[196:199], a[164:167], v[84:87], v[196:199]\n" \
"v_accvgpr_read_b32 v56, a196\n" \
"v_accvgpr_read_b32 v24, a197\n" \
M_"v[228:231], a[164:167], v[88:91], v[228:231]\n" \
"v_readlane_b32 s97, v11, 39\n" \
"v_add_u32_e32 v17, 0x49000, v20\n" \
M_"v[192:195], a[160:163], v[84:87], v[192:195]\n" \
"v_readlane_b32 s98, v11, 40\n" \
"v_add_u32_e32 v48, s16, v20\n" \
M_"v[224:227], a[160:163], v[88:91], v[224:227]\n" \
R_"a[128:131], v48 offset:0\n" \
R_"a[144:147], v48 offset:4096\n" \
M_"a[196:199], a[212:215], v[120:123], a[196:199]\n" \
R_"a[132:135], v48 offset:1024\n" \
R_"a[148:151], v48 offset:5120\n" \
M_"v[224:227], a[0:3], v[120:123], v[224:227]\n" \
R_"a[136:139], v48 offset:2048\n" \
R_"a[152:155], v48 offset:6144\n" \
M_"v[228:231], a[4:7], v[120:123], v[228:231]\n" \
R_"a[140:143], v48 offset:3072\n" \
R_"a[156:159], v48 offset:7168\n" \
M_"v[232:235], a[8:11], v[120:123], v[232:235]\n" \
"v_accvgpr_read_b32 v55, a194\n" \
"v_accvgpr_read_b32 v23, a195\n" \
M_"a[192:195], a[216:219], a[216:219], 0\n" \
"v_add_f32_e32 v55, v55, v23\n" \
"v_max_f32_e32 v62, v62, v55\n" \
M_"v[236:239], a[12:15], v[120:123], v[236:239]\n" \
"v_rcp_f32_e32 v55, v55\n" \
M_"v[240:243], a[16:19], v[120:123], v[240:243]\n" \
"v_mul_f32_e32 v55, v55, v59\n" \
"v_mul_f32_e32 v160, v55, v160\n" \
M_"v[244:247], a[20:23], v[120:123], v[244:247]\n" \
"v_mul_f32_e32 v161, v55, v161\n" \
"v_mul_f32_e32 v168, v55, v168\n" \
M_"v[248:251], a[24:27], v[120:123], v[248:251]\n" \
"v_mul_f32_e32 v169, v55, v169\n" \
"v_mul_f32_e32 v176, v55, v176\n" \
M_"v[252:255], a[28:31], v[120:123], v[252:255]\n" \
"v_mul_f32_e32 v177, v55, v177\n" \
"v_mul_f32_e32 v184, v55, v184\n" \
"v_mul_f32_e32 v185, v55, v185\n" \
C_"v160, v160, v161\n" \
"v_mul_f32_e32 v162, v55, v162\n" \
"v_mul_f32_e32 v163, v55, v163\n" \
C_"v168, v168, v169\n" \
"v_mul_f32_e32 v170, v55, v170\n" \
"v_mul_f32_e32 v171, v55, v171\n" \
"s_waitcnt vmcnt(4)\n" \
"v_cmp_ne_u64_e32 vcc, s[62:63], v[26:27]\n" \
C_"v176, v176, v177\n" \
"v_mul_f32_e32 v178, v55, v178\n" \
"v_mul_f32_e32 v179, v55, v179\n" \
C_"v184, v184, v185\n" \
"v_mul_f32_e32 v186, v55, v186\n" \
"s_cbranch_vccnz L_sos_%=\n" \
"L_ros_%=:\n" \
B_"a[220:223], v20, s[56:59], s97 offen offset:0 sc1\n" \
"v_mul_f32_e32 v187, v55, v187\n" \
C_"v161, v162, v163\n" \
"v_mul_f32_e32 v164, v55, v164\n" \
"v_mul_f32_e32 v165, v55, v165\n" \
"v_mul_f32_e32 v166, v55, v166\n" \
"v_mul_f32_e32 v167, v55, v167\n" \
B_"a[228:231], v17, s[56:59], s97 offen offset:0 sc1\n" \
C_"v169, v170, v171\n" \
"v_mul_f32_e32 v172, v55, v172\n" \
"v_mul_f32_e32 v173, v55, v173\n" \
"v_mul_f32_e32 v174, v55, v174\n" \
"v_mul_f32_e32 v175, v55, v175\n" \
C_"v177, v178, v179\n" \
B_"a[236:239], v20, s[56:59], s98 offen offset:0 sc1\n" \
"v_mul_f32_e32 v180, v55, v180\n" \
"v_mul_f32_e32 v181, v55, v181\n" \
"v_mul_f32_e32 v182, v55, v182\n" \
"v_mul_f32_e32 v183, v55, v183\n" \
C_"v185, v186, v187\n" \
"v_mul_f32_e32 v188, v55, v188\n" \
B_"a[244:247], v17, s[56:59], s98 offen offset:0 sc1\n" \
"v_mul_f32_e32 v189, v55, v189\n" \
"v_mul_f32_e32 v190, v55, v190\n" \
"v_mul_f32_e32 v191, v55, v191\n" \
C_"v162, v164, v165\n" \
C_"v163, v166, v167\n" \
C_"v170, v172, v173\n" \
"buffer_store_dwordx4 v[160:163], v20, s[56:59], s69 offen offset:0 sc1\n" \
C_"v171, v174, v175\n" \
C_"v178, v180, v181\n" \
C_"v179, v182, v183\n" \
C_"v186, v188, v189\n" \
C_"v187, v190, v191\n" \
"v_readlane_b32 s93, v11, 23\n" \
"buffer_store_dwordx4 v[168:171], v20, s[56:59], s69 offen offset:1024 sc1\n" \
"v_readlane_b32 s84, v11, 15\n" \
"v_readlane_b32 s85, v11, 16\n" \
"v_readlane_b32 s86, v11, 17\n" \
"s_mov_b32 s87, 0x20000\n" \
"v_readlane_b32 s92, v11, 18\n" \
"v_readlane_b32 s91, v11, 24\n" \
"buffer_store_dwordx4 v[176:179], v20, s[56:59], s69 offen offset:2048 sc1\n" \
M_"a[208:211], a[216:219], v[128:131], a[208:211]\n" \
M_"a[208:211], a[216:219], v[136:139], a[208:211]\n" \
"buffer_store_dwordx4 v[184:187], v20, s[56:59], s69 offen offset:3072 sc1\n" \
"v_mfma_f32_32x32x16_bf16 v[128:143], a[216:219], a[216:219], 0\n" \
M_"a[208:211], a[216:219], v[144:147], a[208:211]\n" \
B_"a[224:227], v20, s[56:59], s97 offen offset:1024 sc1\n" \
M_"a[208:211], a[216:219], v[152:155], a[208:211]\n" \
"v_mfma_f32_32x32x16_bf16 v[144:159], a[216:219], a[216:219], 0\n" \
B_"a[232:235], v17, s[56:59], s97 offen offset:1024 sc1\n" \
"v_add_f32_e32 v56, v56, v24\n" \
M_"a[208:211], a[216:219], v[160:163], a[208:211]\n" \
B_"a[240:243], v20, s[56:59], s98 offen offset:1024 sc1\n" \
"v_max_f32_e32 v62, v62, v56\n" \
"v_rcp_f32_e32 v56, v56\n" \
M_"a[208:211], a[216:219], v[168:171], a[208:211]\n" \
"v_mfma_f32_32x32x16_bf16 v[160:175], a[216:219], a[216:219], 0\n" \
B_"a[248:251], v17, s[56:59], s98 offen offset:1024 sc1\n" \
"v_mul_f32_e32 v56, v56, v60\n" \
M_"a[208:211], a[216:219], v[176:179], a[208:211]\n" \
"v_mul_f32_e32 v192, v56, v192\n" \
"v_mul_f32_e32 v193, v56, v193\n" \
"s_waitcnt vmcnt(17) lgkmcnt(0)\n" \
"s_barrier\n" \
M_"v[64:67], a[128:131], a[64:67], v[32:35]\n" \
"s_add_u32 m0, s93, 0x0\n" \
C_"v192, v192, v193\n" \
M_"v[64:67], a[132:135], a[68:71], v[64:67]\n" \
B_"v0, s[84:87], s92 offen offset:0 lds\n" \
M_"v[64:67], a[136:139], a[72:75], v[64:67]\n" \
"v_mul_f32_e32 v194, v56, v194\n" \
"v_mul_f32_e32 v195, v56, v195\n" \
M_"v[64:67], a[140:143], a[76:79], v[64:67]\n" \
B_"v1, s[84:87], s92 offen offset:1024 lds\n" \
M_"v[68:71], a[156:159], a[76:79], v[32:35]\n" \
M_"a[208:211], a[216:219], v[184:187], a[208:211]\n" \
"v_mfma_f32_32x32x16_bf16 v[176:191], a[216:219], a[216:219], 0\n" \
B_"v2, s[84:87], s92 offen offset:2048 lds\n" \
C_"v193, v194, v195\n" \
"v_mul_f32_e32 v196, v56, v196\n" \
"v_mul_f32_e32 v197, v56, v197\n" \
"v_mul_f32_e32 v198, v56, v198\n" \
M_"v[68:71], a[152:155], a[72:75], v[68:71]\n" \
B_"v3, s[84:87], s92 offen offset:3072 lds\n" \
M_"v[68:71], a[148:151], a[68:71], v[68:71]\n" \
"s_add_u32 m0, s91, 0x1000\n" \
"v_mul_f32_e32 v199, v56, v199\n" \
M_"v[68:71], a[144:147], a[64:67], v[68:71]\n" \
B_"v4, s[84:87], s92 offen offset:0 lds\n" \
M_"v[76:79], a[144:147], a[80:83], v[36:39]\n" \
R_"a[144:147], v48 offset:12288\n" \
"v_mul_f32_e32 v200, v56, v200\n" \
M_"v[76:79], a[148:151], a[84:87], v[76:79]\n" \
B_"v5, s[84:87], s92 offen offset:1024 lds\n" \
M_"v[76:79], a[152:155], a[88:91], v[76:79]\n" \
R_"a[148:151], v48 offset:13312\n" \
R_"a[152:155], v48 offset:14336\n" \
M_"v[76:79], a[156:159], a[92:95], v[76:79]\n" \
B_"v6, s[84:87], s92 offen offset:2048 lds\n" \
M_"v[72:75], a[140:143], a[92:95], v[36:39]\n" \
R_"a[140:143], v48 offset:11264\n" \
R_"a[156:159], v48 offset:15360\n" \
M_"v[72:75], a[136:139], a[88:91], v[72:75]\n" \
B_"v7, s[84:87], s92 offen offset:3072 lds\n" \
M_"v[72:75], a[132:135], a[84:87], v[72:75]\n" \
R_"a[132:135], v48 offset:9216\n" \
R_"a[136:139], v48 offset:10240\n" \
M_"v[72:75], a[128:131], a[80:83], v[72:75]\n" \
R_"a[128:131], v48 offset:8192\n" \
"v_mul_f32_e32 v201, v56, v201\n" \
"v_accvgpr_read_b32 v57, a198\n" \
"v_accvgpr_read_b32 v25, a199\n" \
M_"a[196:199], a[216:219], a[216:219], 0\n" \
C_"v194, v196, v197\n" \
C_"v195, v198, v199\n" \
"buffer_store_dwordx4 v[192:195], v20, s[56:59], s70 offen offset:0 sc1\n" \
C_"v200, v200, v201\n" \
"v_mul_f32_e32 v202, v56, v202\n" \
"v_mul_f32_e32 v203, v56, v203\n" \
"v_add_f32_e32 v57, v57, v25\n" \
M_"a[208:211], a[216:219], v[192:195], a[208:211]\n" \
C_"v201, v202, v203\n" \
"v_mul_f32_e32 v204, v56, v204\n" \
"v_mul_f32_e32 v205, v56, v205\n" \
"v_mul_f32_e32 v206, v56, v206\n" \
"v_mul_f32_e32 v207, v56, v207\n" \
"v_mul_f32_e32 v208, v56, v208\n" \
"v_mul_f32_e32 v209, v56, v209\n" \
"v_max_f32_e32 v62, v62, v57\n" \
C_"v202, v204, v205\n" \
C_"v203, v206, v207\n" \
"buffer_store_dwordx4 v[200:203], v20, s[56:59], s70 offen offset:1024 sc1\n" \
".fill (L_ab + 28672 - .) / 4, 4, 0xBF800000\n" \
"L_ap7:\n" \
"s_cbranch_execz L_ap0\n" \
C_"v208, v208, v209\n" \
"v_mul_f32_e32 v210, v56, v210\n" \
"v_mul_f32_e32 v211, v56, v211\n" \
"v_rcp_f32_e32 v57, v57\n" \
M_"a[208:211], a[216:219], v[200:203], a[208:211]\n" \
"v_mfma_f32_32x32x16_bf16 v[192:207], a[216:219], a[216:219], 0\n" \
C_"v209, v210, v211\n" \
"v_mul_f32_e32 v212, v56, v212\n" \
"v_mul_f32_e32 v213, v56, v213\n" \
"v_mul_f32_e32 v214, v56, v214\n" \
"v_mul_f32_e32 v215, v56, v215\n" \
"v_mul_f32_e32 v216, v56, v216\n" \
"v_mul_f32_e32 v217, v56, v217\n" \
C_"v210, v212, v213\n" \
C_"v211, v214, v215\n" \
"buffer_store_dwordx4 v[208:211], v20, s[56:59], s70 offen offset:2048 sc1\n" \
C_"v216, v216, v217\n" \
"v_mul_f32_e32 v218, v56, v218\n" \
"v_mul_f32_e32 v219, v56, v219\n" \
"v_mul_f32_e32 v57, v57, v61\n" \
M_"a[208:211], a[216:219], v[208:211], a[208:211]\n" \
C_"v217, v218, v219\n" \
"v_mul_f32_e32 v220, v56, v220\n" \
"v_mul_f32_e32 v221, v56, v221\n" \
"v_mul_f32_e32 v222, v56, v222\n" \
"v_mul_f32_e32 v223, v56, v223\n" \
"v_mul_f32_e32 v224, v57, v224\n" \
"v_mul_f32_e32 v225, v57, v225\n" \
C_"v218, v220, v221\n" \
C_"v219, v222, v223\n" \
"buffer_store_dwordx4 v[216:219], v20, s[56:59], s70 offen offset:3072 sc1\n" \
C_"v224, v224, v225\n" \
"v_mul_f32_e32 v226, v57, v226\n" \
"v_mul_f32_e32 v227, v57, v227\n" \
M_"a[208:211], a[216:219], v[216:219], a[208:211]\n" \
"v_mfma_f32_32x32x16_bf16 v[208:223], a[216:219], a[216:219], 0\n" \
C_"v225, v226, v227\n" \
"v_mul_f32_e32 v228, v57, v228\n" \
"v_mul_f32_e32 v229, v57, v229\n" \
"v_mul_f32_e32 v230, v57, v230\n" \
"v_mul_f32_e32 v231, v57, v231\n" \
"v_mul_f32_e32 v232, v57, v232\n" \
"v_mul_f32_e32 v233, v57, v233\n" \
C_"v226, v228, v229\n" \
C_"v227, v230, v231\n" \
"buffer_store_dwordx4 v[224:227], v20, s[56:59], s71 offen offset:0 sc1\n" \
C_"v232, v232, v233\n" \
"v_mul_f32_e32 v234, v57, v234\n" \
"v_mul_f32_e32 v235, v57, v235\n" \
M_"a[208:211], a[216:219], v[224:227], a[208:211]\n" \
C_"v233, v234, v235\n" \
"v_mul_f32_e32 v236, v57, v236\n" \
"v_mul_f32_e32 v237, v57, v237\n" \
"v_mul_f32_e32 v238, v57, v238\n" \
"v_mul_f32_e32 v239, v57, v239\n" \
"v_mul_f32_e32 v240, v57, v240\n" \
"v_mul_f32_e32 v241, v57, v241\n" \
C_"v234, v236, v237\n" \
C_"v235, v238, v239\n" \
"buffer_store_dwordx4 v[232:235], v20, s[56:59], s71 offen offset:1024 sc1\n" \
C_"v240, v240, v241\n" \
"v_mul_f32_e32 v242, v57, v242\n" \
"v_mul_f32_e32 v243, v57, v243\n" \
M_"a[208:211], a[216:219], v[232:235], a[208:211]\n" \
"v_mfma_f32_32x32x16_bf16 v[224:239], a[216:219], a[216:219], 0\n" \
C_"v241, v242, v243\n" \
"v_mul_f32_e32 v244, v57, v244\n" \
"v_mul_f32_e32 v245, v57, v245\n" \
"v_mul_f32_e32 v246, v57, v246\n" \
"v_mul_f32_e32 v247, v57, v247\n" \
"v_mul_f32_e32 v248, v57, v248\n" \
"v_mul_f32_e32 v249, v57, v249\n" \
C_"v242, v244, v245\n" \
C_"v243, v246, v247\n" \
"buffer_store_dwordx4 v[240:243], v20, s[56:59], s71 offen offset:2048 sc1\n" \
C_"v248, v248, v249\n" \
"v_mul_f32_e32 v250, v57, v250\n" \
"v_mul_f32_e32 v251, v57, v251\n" \
M_"a[208:211], a[216:219], v[240:243], a[208:211]\n" \
C_"v249, v250, v251\n" \
"v_mul_f32_e32 v252, v57, v252\n" \
"v_mul_f32_e32 v253, v57, v253\n" \
"v_mul_f32_e32 v254, v57, v254\n" \
"v_mul_f32_e32 v255, v57, v255\n" \
C_"v250, v252, v253\n" \
C_"v251, v254, v255\n" \
"buffer_store_dwordx4 v[248:251], v20, s[56:59], s71 offen offset:3072 sc1\n" \
"s_add_u32 m0, s91, 0x2000\n" \
M_"a[208:211], a[216:219], v[248:251], a[208:211]\n" \
"v_mfma_f32_32x32x16_bf16 v[240:255], a[216:219], a[216:219], 0\n" \
"v_add_u32_e32 v53, 0x20000, v63\n" \
B_"v8, s[84:87], s92 offen offset:0 lds\n" \
E_"v70, v70\n" \
E_"v71, v71\n" \
E_"v72, v72\n" \
E_"v73, v73\n" \
"s_add_u32 s64, s64, 0x10000\n" \
"v_accvgpr_read_b32 v63, a208\n" \
"v_cmp_u_f32_e32 vcc, v63, v63\n" \
"v_cmp_lt_f32_e64 s[72:73], s21, v62\n" \
"v_readlane_b32 s95, v11, 3\n" \
E_"v68, v68\n" \
E_"v69, v69\n" \
E_"v74, v74\n" \
E_"v75, v75\n" \
"s_or_b64 vcc, s[72:73], vcc\n" \
"s_cselect_b32 s95, 0x40000000, s95\n" \
"s_cselect_b32 s94, 0x7f800000, 0\n" \
C_"v71, v70, v71\n" \
E_"v66, v66\n" \
E_"v67, v67\n" \
E_"v64, v64\n" \
E_"v65, v65\n" \
C_"v72, v72, v73\n" \
E_"v76, v76\n" \
E_"v77, v77\n" \
E_"v78, v78\n" \
E_"v79, v79\n" \
C_"v70, v68, v69\n" \
"s_waitcnt vmcnt(17) lgkmcnt(0)\n" \
"s_barrier\n" \
C_"v73, v74, v75\n" \
"v_add_u32_e32 v49, 0x10000, v48\n" \
"s_xor_b32 s65, s65, 0x8000\n" \
C_"v69, v66, v67\n" \
C_"v68, v64, v65\n" \
C_"v74, v76, v77\n" \
C_"v75, v78, v79\n" \
"v_xor_b32_e32 v48, 0x8000, v48\n" \
"s_sub_u32 s66, s19, 2\n" \
"s_branch L_f1_%=\n" \
".fill (L_ab + 32768 - .) / 4, 4, 0xBF800000\n" \
".fill (L_ab + 32768 - .) / 4, 4, 0xBF800000\n" \
"L_ap8:\n" \
"s_setpc_b64 s[88:89]\n" \
"L_out_%=:\n" \
"v_bfe_u32 v38, v20, 2, 4\n" \
"v_readlane_b32 s64, v11, 2\n" \
"v_add_u32_e32 v32, 0x49000, v20\n" \
"v_add_u32_e32 v34, 0x20000, v10\n" \
X_"a[0:1], v34 offset:0\n" \
X_"a[2:3], v34 offset:4608\n" \
X_"a[4:5], v34 offset:9216\n" \
"buffer_load_dword v26, v38, s[56:59], s64 offen sc1\n" \
X_"a[6:7], v34 offset:13824\n" \
X_"a[8:9], v34 offset:32\n" \
X_"a[10:11], v34 offset:4640\n" \
X_"a[12:13], v34 offset:9248\n" \
"buffer_store_dwordx4 v[64:67], v20, s[56:59], s61 offen offset:0 sc1\n" \
X_"a[14:15], v34 offset:13856\n" \
"v_mov_b32_e32 v39, s62\n" \
"v_readlane_b32 s70, v11, 5\n" \
"v_readlane_b32 s71, v11, 4\n" \
"buffer_store_dwordx4 v[68:71], v20, s[56:59], s61 offen offset:1024 sc1\n" \
X_"a[16:17], v34 offset:64\n" \
X_"a[18:19], v34 offset:4672\n" \
X_"a[20:21], v34 offset:9280\n" \
X_"a[22:23], v34 offset:13888\n" \
"buffer_store_dwordx4 v[72:75], v20, s[56:59], s61 offen offset:2048 sc1\n" \
X_"a[24:25], v34 offset:96\n" \
X_"a[26:27], v34 offset:4704\n" \
X_"a[28:29], v34 offset:9312\n" \
X_"a[30:31], v34 offset:13920\n" \
"buffer_store_dwordx4 v[76:79], v20, s[56:59], s61 offen offset:3072 sc1\n" \
X_"a[192:193], v34 offset:128\n" \
X_"a[194:195], v34 offset:4736\n" \
X_"a[196:197], v34 offset:9344\n" \
X_"a[198:199], v34 offset:13952\n" \
"buffer_store_dwordx4 v[80:83], v32, s[56:59], s61 offen offset:0 sc1\n" \
X_"a[200:201], v34 offset:160\n" \
X_"a[202:203], v34 offset:4768\n" \
X_"a[204:205], v34 offset:9376\n" \
X_"a[206:207], v34 offset:13984\n" \
"buffer_store_dwordx4 v[84:87], v32, s[56:59], s61 offen offset:1024 sc1\n" \
X_"a[208:209], v34 offset:192\n" \
"s_waitcnt vmcnt(16) lgkmcnt(15)\n" \
M_"v[112:115], a[0:3], a[96:99], 0\n" \
"buffer_store_dwordx4 v[96:99], v32, s[56:59], s61 offen offset:2048 sc1\n" \
M_"v[112:115], a[4:7], a[100:103], v[112:115]\n" \
X_"a[210:211], v34 offset:4800\n" \
X_"a[212:213], v34 offset:9408\n" \
"buffer_store_dwordx4 v[100:103], v32, s[56:59], s61 offen offset:3072 sc1\n" \
M_"v[116:119], a[0:3], a[112:115], 0\n" \
X_"a[214:215], v34 offset:14016\n" \
X_"a[216:217], v34 offset:224\n" \
M_"v[116:119], a[4:7], a[116:119], v[116:119]\n" \
X_"a[218:219], v34 offset:4832\n" \
"v_lshrrev_b32_e32 v44, 9, v20\n" \
M_"v[184:187], a[0:3], v[64:67], 0\n" \
"v_mul_u32_u24_e32 v44, 0xb40, v44\n" \
"v_add_u32_e32 v35, v44, v10\n" \
M_"v[184:187], a[4:7], v[68:71], v[184:187]\n" \
"v_readlane_b32 s65, v11, 33\n" \
"v_readlane_b32 s67, v11, 26\n" \
M_"v[188:191], a[0:3], v[80:83], 0\n" \
X_"a[0:1], v34 offset:9440\n" \
X_"a[2:3], v34 offset:14048\n" \
M_"v[188:191], a[4:7], v[84:87], v[188:191]\n" \
X_"a[4:5], v34 offset:256\n" \
X_"a[6:7], v34 offset:4864\n" \
M_"v[196:199], a[8:11], v[80:83], 0\n" \
"v_readlane_b32 s80, v11, 11\n" \
"v_readlane_b32 s81, v11, 12\n" \
M_"v[196:199], a[12:15], v[84:87], v[196:199]\n" \
"s_mov_b32 s82, 0x47fca0\n" \
"s_mov_b32 s83, 0x20000\n" \
M_"v[192:195], a[8:11], v[64:67], 0\n" \
"v_readlane_b32 s66, v11, 31\n" \
"s_add_u32 m0, s67, 0x0\n" \
M_"v[192:195], a[12:15], v[68:71], v[192:195]\n" \
"v_readlane_b32 s92, v11, 27\n" \
"v_readlane_b32 s73, v11, 32\n" \
M_"v[124:127], a[8:11], a[112:115], 0\n" \
B_"v0, s[80:83], s66 offen offset:0 lds\n" \
M_"v[124:127], a[12:15], a[116:119], v[124:127]\n" \
"v_readlane_b32 s94, v11, 28\n" \
"v_readlane_b32 s84, v11, 9\n" \
M_"v[120:123], a[8:11], a[96:99], 0\n" \
X_"a[8:9], v34 offset:9472\n" \
X_"a[10:11], v34 offset:14080\n" \
M_"v[120:123], a[12:15], a[100:103], v[120:123]\n" \
X_"a[12:13], v34 offset:18432\n" \
X_"a[14:15], v34 offset:23040\n" \
M_"v[128:131], a[16:19], a[96:99], 0\n" \
"v_readlane_b32 s85, v11, 10\n" \
"s_waitcnt lgkmcnt(9)\n" \
M_"v[128:131], a[20:23], a[100:103], v[128:131]\n" \
B_"v1, s[80:83], s66 offen offset:1024 lds\n" \
M_"v[132:135], a[16:19], a[112:115], 0\n" \
"s_mov_b32 s86, 0xa1ca0\n" \
"s_mov_b32 s87, 0x20000\n" \
M_"v[132:135], a[20:23], a[116:119], v[132:135]\n" \
"v_add_u32_e32 v33, 0x10000, v10\n" \
"v_readlane_b32 s68, v11, 34\n" \
M_"v[200:203], a[16:19], v[64:67], 0\n" \
"v_readlane_b32 s69, v11, 35\n" \
"v_readlane_b32 s72, v11, 37\n" \
M_"v[200:203], a[20:23], v[68:71], v[200:203]\n" \
"v_readlane_b32 s93, v11, 38\n" \
"v_readlane_b32 s63, v11, 44\n" \
M_"v[204:207], a[16:19], v[80:83], 0\n" \
X_"a[16:17], v34 offset:18464\n" \
X_"a[18:19], v34 offset:23072\n" \
M_"v[204:207], a[20:23], v[84:87], v[204:207]\n" \
X_"a[20:21], v34 offset:18496\n" \
X_"a[22:23], v34 offset:23104\n" \
M_"v[212:215], a[24:27], v[80:83], 0\n" \
B_"v2, s[80:83], s66 offen offset:2048 lds\n" \
M_"v[212:215], a[28:31], v[84:87], v[212:215]\n" \
"v_bfe_u32 v40, v20, 2, 7\n" \
"s_add_u32 s74, s63, 0x3800\n" \
M_"v[208:211], a[24:27], v[64:67], 0\n" \
"v_lshlrev_b32_e32 v41, 12, v20\n" \
"s_add_u32 s12, s63, 0x0\n" \
M_"v[208:211], a[28:31], v[68:71], v[208:211]\n" \
"v_readlane_b32 s95, v11, 41\n" \
"v_readlane_b32 s96, v11, 42\n" \
M_"v[140:143], a[24:27], a[112:115], 0\n" \
"s_add_u32 s13, s63, 0x1000\n" \
"v_readlane_b32 s97, v11, 43\n" \
M_"v[140:143], a[28:31], a[116:119], v[140:143]\n" \
B_"v3, s[80:83], s66 offen offset:3072 lds\n" \
M_"v[136:139], a[24:27], a[96:99], 0\n" \
X_"a[24:25], v34 offset:18528\n" \
X_"a[26:27], v34 offset:23136\n" \
M_"v[136:139], a[28:31], a[100:103], v[136:139]\n" \
X_"a[28:29], v34 offset:18560\n" \
X_"a[30:31], v34 offset:23168\n" \
M_"v[144:147], a[192:195], a[96:99], 0\n" \
"s_add_u32 m0, s67, 0x1000\n" \
"s_add_u32 s14, s63, 0x2000\n" \
M_"v[144:147], a[196:199], a[100:103], v[144:147]\n" \
"s_add_u32 s15, s63, 0x3000\n" \
"v_readlane_b32 s100, v11, 7\n" \
M_"v[148:151], a[192:195], a[112:115], 0\n" \
B_"v4, s[80:83], s66 offen offset:0 lds\n" \
M_"v[148:151], a[196:199], a[116:119], v[148:151]\n" \
"v_readlane_b32 s16, v11, 45\n" \
"v_bfe_u32 v44, v20, 4, 4\n" \
M_"v[216:219], a[192:195], v[64:67], 0\n" \
"s_add_u32 s17, s16, 0x1000\n" \
"s_add_u32 s18, s16, 0x2000\n" \
M_"v[216:219], a[196:199], v[68:71], v[216:219]\n" \
"s_add_u32 s19, s16, 0x3000\n" \
"v_bfe_u32 v45, v20, 8, 2\n" \
M_"v[220:223], a[192:195], v[80:83], 0\n" \
X_"a[192:193], v34 offset:18592\n" \
X_"a[194:195], v34 offset:23200\n" \
M_"v[220:223], a[196:199], v[84:87], v[220:223]\n" \
X_"a[196:197], v34 offset:18624\n" \
X_"a[198:199], v34 offset:23232\n" \
M_"v[228:231], a[200:203], v[80:83], 0\n" \
B_"v5, s[80:83], s66 offen offset:1024 lds\n" \
M_"v[228:231], a[204:207], v[84:87], v[228:231]\n" \
"v_mul_u32_u24_e32 v42, 0x480, v44\n" \
"v_lshl_add_u32 v42, v45, 3, v42\n" \
M_"v[224:227], a[200:203], v[64:67], 0\n" \
"s_waitcnt vmcnt(14)\n" \
"v_cmp_ne_u32_e32 vcc, s62, v26\n" \
M_"v[224:227], a[204:207], v[68:71], v[224:227]\n" \
"v_lshrrev_b32_e32 v44, 1, v44\n" \
"v_lshlrev_b32_e32 v45, 1, v45\n" \
M_"v[156:159], a[200:203], a[112:115], 0\n" \
"v_sub_u32_e32 v44, v44, v45\n" \
"s_cbranch_vccnz L_sa_%=\n" \
"L_ra_%=:\n" \
M_"v[156:159], a[204:207], a[116:119], v[156:159]\n" \
B_"a[64:67], v20, s[56:59], s65 offen offset:0 sc1\n" \
M_"v[152:155], a[200:203], a[96:99], 0\n" \
X_"a[200:201], v34 offset:18656\n" \
X_"a[202:203], v34 offset:23264\n" \
M_"v[152:155], a[204:207], a[100:103], v[152:155]\n" \
X_"a[204:205], v34 offset:18688\n" \
X_"a[206:207], v34 offset:23296\n" \
M_"v[160:163], a[208:211], a[96:99], 0\n" \
"v_readlane_b32 s64, v11, 36\n" \
"v_and_b32_e32 v45, 16, v20\n" \
M_"v[160:163], a[212:215], a[100:103], v[160:163]\n" \
"v_mov_b32_e32 v46, 0x3f80\n" \
"v_lshlrev_b32_e32 v45, v45, v46\n" \
M_"v[164:167], a[208:211], a[112:115], 0\n" \
B_"a[68:71], v20, s[56:59], s65 offen offset:1024 sc1\n" \
M_"v[164:167], a[212:215], a[116:119], v[164:167]\n" \
"v_mov_b32_e32 v52, 0\n" \
"v_mov_b32_e32 v53, 0\n" \
M_"v[232:235], a[208:211], v[64:67], 0\n" \
"v_mov_b32_e32 v48, 0\n" \
"v_mov_b32_e32 v49, 0\n" \
M_"v[232:235], a[212:215], v[68:71], v[232:235]\n" \
"v_add_u32_e32 v43, 0x9000, v42\n" \
"v_readlane_b32 s88, v11, 13\n" \
M_"v[236:239], a[208:211], v[80:83], 0\n" \
X_"a[208:209], v10 offset:32768\n" \
X_"a[210:211], v10 offset:37376\n" \
M_"v[236:239], a[212:215], v[84:87], v[236:239]\n" \
X_"a[212:213], v10 offset:32800\n" \
"s_waitcnt lgkmcnt(7)\n" \
M_"v[244:247], a[216:219], v[80:83], 0\n" \
X_"a[214:215], v10 offset:37408\n" \
"v_readlane_b32 s89, v11, 14\n" \
M_"v[244:247], a[0:3], v[84:87], v[244:247]\n" \
B_"a[80:83], v32, s[56:59], s65 offen offset:0 sc1\n" \
M_"v[240:243], a[216:219], v[64:67], 0\n" \
"s_mov_b32 s90, 0x240000\n" \
"s_mov_b32 s91, 0x20000\n" \
M_"v[240:243], a[0:3], v[68:71], v[240:243]\n" \
"v_mov_b32_e32 v46, s62\n" \
M_"v[172:175], a[216:219], a[112:115], 0\n" \
M_"v[172:175], a[0:3], a[116:119], v[172:175]\n" \
M_"v[168:171], a[216:219], a[96:99], 0\n" \
X_"a[216:217], v10 offset:32832\n" \
X_"a[218:219], v10 offset:37440\n" \
M_"v[168:171], a[0:3], a[100:103], v[168:171]\n" \
X_"a[0:1], v10 offset:32864\n" \
X_"a[2:3], v10 offset:37472\n" \
M_"v[176:179], a[4:7], a[96:99], 0\n" \
B_"a[84:87], v32, s[56:59], s65 offen offset:1024 sc1\n" \
M_"v[176:179], a[8:11], a[100:103], v[176:179]\n" \
M_"v[180:183], a[4:7], a[112:115], 0\n" \
M_"v[180:183], a[8:11], a[116:119], v[180:183]\n" \
M_"v[248:251], a[4:7], v[64:67], 0\n" \
M_"v[248:251], a[8:11], v[68:71], v[248:251]\n" \
B_"a[72:75], v20, s[56:59], s65 offen offset:2048 sc1\n" \
M_"v[252:255], a[4:7], v[80:83], 0\n" \
X_"a[4:5], v10 offset:32896\n" \
X_"a[6:7], v10 offset:37504\n" \
M_"v[252:255], a[8:11], v[84:87], v[252:255]\n" \
X_"a[8:9], v10 offset:32928\n" \
X_"a[10:11], v10 offset:37536\n" \
M_"v[112:115], a[12:15], a[104:107], v[112:115]\n" \
M_"v[116:119], a[12:15], a[120:123], v[116:119]\n" \
M_"v[184:187], a[12:15], v[72:75], v[184:187]\n" \
B_"a[76:79], v20, s[56:59], s65 offen offset:3072 sc1\n" \
M_"v[188:191], a[12:15], v[96:99], v[188:191]\n" \
X_"a[12:13], v10 offset:32960\n" \
X_"a[14:15], v10 offset:37568\n" \
M_"v[196:199], a[16:19], v[96:99], v[196:199]\n" \
M_"v[192:195], a[16:19], v[72:75], v[192:195]\n" \
M_"v[124:127], a[16:19], a[120:123], v[124:127]\n" \
M_"v[120:123], a[16:19], a[104:107], v[120:123]\n" \
X_"a[16:17], v10 offset:32992\n" \
X_"a[18:19], v10 offset:37600\n" \
M_"v[128:131], a[20:23], a[104:107], v[128:131]\n" \
B_"a[88:91], v32, s[56:59], s65 offen offset:2048 sc1\n" \
M_"v[132:135], a[20:23], a[120:123], v[132:135]\n" \
M_"v[200:203], a[20:23], v[72:75], v[200:203]\n" \
M_"v[204:207], a[20:23], v[96:99], v[204:207]\n" \
X_"a[20:21], v10 offset:33024\n" \
X_"a[22:23], v10 offset:37632\n" \
"s_waitcnt lgkmcnt(10)\n" \
"s_barrier\n" \
M_"v[212:215], a[24:27], v[96:99], v[212:215]\n" \
B_"a[92:95], v32, s[56:59], s65 offen offset:3072 sc1\n" \
M_"v[208:211], a[24:27], v[72:75], v[208:211]\n" \
"v_readlane_b32 s65, v11, 49\n" \
M_"v[140:143], a[24:27], a[120:123], v[140:143]\n" \
M_"v[136:139], a[24:27], a[104:107], v[136:139]\n" \
X_"a[24:25], v35 offset:41984\n" \
X_"a[26:27], v35 offset:44288\n" \
M_"v[144:147], a[28:31], a[104:107], v[144:147]\n" \
M_"v[148:151], a[28:31], a[120:123], v[148:151]\n" \
B_"v6, s[80:83], s66 offen offset:2048 lds\n" \
M_"v[216:219], a[28:31], v[72:75], v[216:219]\n" \
M_"v[220:223], a[28:31], v[96:99], v[220:223]\n" \
X_"a[28:29], v35 offset:52352\n" \
X_"a[30:31], v35 offset:54656\n" \
M_"v[228:231], a[192:195], v[96:99], v[228:231]\n" \
M_"v[224:227], a[192:195], v[72:75], v[224:227]\n" \
M_"v[156:159], a[192:195], a[120:123], v[156:159]\n" \
B_"v7, s[80:83], s66 offen offset:3072 lds\n" \
M_"v[152:155], a[192:195], a[104:107], v[152:155]\n" \
X_"a[192:193], v35 offset:42016\n" \
X_"a[194:195], v35 offset:44320\n" \
M_"v[160:163], a[196:199], a[104:107], v[160:163]\n" \
"s_add_u32 m0, s67, 0x2000\n" \
"v_readlane_b32 s67, v11, 51\n" \
M_"v[164:167], a[196:199], a[120:123], v[164:167]\n" \
M_"v[232:235], a[196:199], v[72:75], v[232:235]\n" \
M_"v[236:239], a[196:199], v[96:99], v[236:239]\n" \
X_"a[196:197], v35 offset:52384\n" \
X_"a[198:199], v35 offset:54688\n" \
M_"v[244:247], a[200:203], v[96:99], v[244:247]\n" \
B_"v8, s[80:83], s66 offen offset:0 lds\n" \
M_"v[240:243], a[200:203], v[72:75], v[240:243]\n" \
"s_add_u32 m0, s92, 0x0\n" \
"v_readlane_b32 s66, v11, 50\n" \
M_"v[172:175], a[200:203], a[120:123], v[172:175]\n" \
M_"v[168:171], a[200:203], a[104:107], v[168:171]\n" \
X_"a[200:201], v35 offset:42048\n" \
X_"a[202:203], v35 offset:44352\n" \
M_"v[176:179], a[204:207], a[104:107], v[176:179]\n" \
M_"v[180:183], a[204:207], a[120:123], v[180:183]\n" \
B_"v0, s[80:83], s73 offen offset:0 lds\n" \
M_"v[248:251], a[204:207], v[72:75], v[248:251]\n" \
M_"v[252:255], a[204:207], v[96:99], v[252:255]\n" \
X_"a[204:205], v35 offset:52416\n" \
X_"a[206:207], v35 offset:54720\n" \
M_"v[112:115], a[208:211], a[108:111], v[112:115]\n" \
M_"v[116:119], a[208:211], a[124:127], v[116:119]\n" \
M_"v[184:187], a[208:211], v[76:79], v[184:187]\n" \
B_"v1, s[80:83], s73 offen offset:1024 lds\n" \
M_"v[188:191], a[208:211], v[100:103], v[188:191]\n" \
X_"a[208:209], v35 offset:42080\n" \
X_"a[210:211], v35 offset:44384\n" \
M_"v[196:199], a[212:215], v[100:103], v[196:199]\n" \
M_"v[192:195], a[212:215], v[76:79], v[192:195]\n" \
"s_waitcnt vmcnt(18)\n" \
"s_mov_b64 exec, 1\n" \
"buffer_store_dword v39, off, s[56:59], s70 offset:0 sc1\n" \
"s_mov_b64 exec, -1\n" \
M_"v[124:127], a[212:215], a[124:127], v[124:127]\n" \
"v_readlane_b32 s70, v11, 54\n" \
M_"v[120:123], a[212:215], a[108:111], v[120:123]\n" \
X_"a[212:213], v35 offset:52448\n" \
X_"a[214:215], v35 offset:54752\n" \
M_"v[128:131], a[216:219], a[108:111], v[128:131]\n" \
M_"v[132:135], a[216:219], a[124:127], v[132:135]\n" \
B_"v2, s[80:83], s73 offen offset:2048 lds\n" \
M_"v[200:203], a[216:219], v[76:79], v[200:203]\n" \
M_"v[204:207], a[216:219], v[100:103], v[204:207]\n" \
X_"a[216:217], v35 offset:42112\n" \
X_"a[218:219], v35 offset:44416\n" \
M_"v[212:215], a[0:3], v[100:103], v[212:215]\n" \
M_"v[208:211], a[0:3], v[76:79], v[208:211]\n" \
M_"v[140:143], a[0:3], a[124:127], v[140:143]\n" \
B_"v3, s[80:83], s73 offen offset:3072 lds\n" \
M_"v[136:139], a[0:3], a[108:111], v[136:139]\n" \
X_"a[0:1], v35 offset:52480\n" \
"s_waitcnt lgkmcnt(7)\n" \
M_"v[144:147], a[4:7], a[108:111], v[144:147]\n" \
X_"a[2:3], v35 offset:54784\n" \
"s_add_u32 m0, s92, 0x1000\n" \
M_"v[148:151], a[4:7], a[124:127], v[148:151]\n" \
M_"v[216:219], a[4:7], v[76:79], v[216:219]\n" \
M_"v[220:223], a[4:7], v[100:103], v[220:223]\n" \
X_"a[4:5], v35 offset:42144\n" \
X_"a[6:7], v35 offset:44448\n" \
M_"v[228:231], a[8:11], v[100:103], v[228:231]\n" \
B_"v4, s[80:83], s73 offen offset:0 lds\n" \
M_"v[224:227], a[8:11], v[76:79], v[224:227]\n" \
M_"v[156:159], a[8:11], a[124:127], v[156:159]\n" \
M_"v[152:155], a[8:11], a[108:111], v[152:155]\n" \
X_"a[8:9], v35 offset:52512\n" \
X_"a[10:11], v35 offset:54816\n" \
M_"v[160:163], a[12:15], a[108:111], v[160:163]\n" \
M_"v[164:167], a[12:15], a[124:127], v[164:167]\n" \
B_"v5, s[80:83], s73 offen offset:1024 lds\n" \
M_"v[232:235], a[12:15], v[76:79], v[232:235]\n" \
M_"v[236:239], a[12:15], v[100:103], v[236:239]\n" \
X_"a[12:13], v35 offset:42176\n" \
X_"a[14:15], v35 offset:44480\n" \
M_"v[244:247], a[16:19], v[100:103], v[244:247]\n" \
M_"v[240:243], a[16:19], v[76:79], v[240:243]\n" \
M_"v[172:175], a[16:19], a[124:127], v[172:175]\n" \
B_"v6, s[80:83], s73 offen offset:2048 lds\n" \
M_"v[168:171], a[16:19], a[108:111], v[168:171]\n" \
X_"a[16:17], v35 offset:52544\n" \
X_"a[18:19], v35 offset:54848\n" \
M_"v[176:179], a[20:23], a[108:111], v[176:179]\n" \
M_"v[180:183], a[20:23], a[124:127], v[180:183]\n" \
M_"v[248:251], a[20:23], v[76:79], v[248:251]\n" \
M_"v[252:255], a[20:23], v[100:103], v[252:255]\n" \
X_"a[20:21], v35 offset:42208\n" \
X_"a[22:23], v35 offset:44512\n" \
M_"v[112:115], a[24:27], a[220:223], v[112:115]\n" \
B_"v7, s[80:83], s73 offen offset:3072 lds\n" \
M_"v[112:115], a[28:31], a[224:227], v[112:115]\n" \
"s_add_u32 m0, s92, 0x2000\n" \
"v_readlane_b32 s92, v11, 49\n" \
M_"v[116:119], a[24:27], a[228:231], v[116:119]\n" \
M_"v[116:119], a[28:31], a[232:235], v[116:119]\n" \
M_"v[184:187], a[24:27], a[236:239], v[184:187]\n" \
M_"v[184:187], a[28:31], a[240:243], v[184:187]\n" \
B_"v8, s[80:83], s73 offen offset:0 lds\n" \
M_"v[188:191], a[24:27], a[244:247], v[188:191]\n" \
X_"a[24:25], v35 offset:52576\n" \
X_"a[26:27], v35 offset:54880\n" \
M_"v[188:191], a[28:31], a[248:251], v[188:191]\n" \
"s_mov_b32 m0, s94\n" \
X_"a[28:29], v35 offset:42240\n" \
M_"v[196:199], a[192:195], a[244:247], v[196:199]\n" \
X_"a[30:31], v35 offset:44544\n" \
"v_readlane_b32 s73, v11, 6\n" \
M_"v[196:199], a[196:199], a[248:251], v[196:199]\n" \
M_"v[192:195], a[192:195], a[236:239], v[192:195]\n" \
B_"v9, s[84:87], 0 offen lds\n" \
M_"v[192:195], a[196:199], a[240:243], v[192:195]\n" \
M_"v[124:127], a[192:195], a[228:231], v[124:127]\n" \
M_"v[124:127], a[196:199], a[232:235], v[124:127]\n" \
M_"v[120:123], a[192:195], a[220:223], v[120:123]\n" \
X_"a[192:193], v35 offset:52608\n" \
X_"a[194:195], v35 offset:54912\n" \
M_"v[120:123], a[196:199], a[224:227], v[120:123]\n" \
X_"a[196:197], v33 offset:0\n" \
X_"a[198:199], v33 offset:4608\n" \
M_"v[128:131], a[200:203], a[220:223], v[128:131]\n" \
B_"a[128:131], v20, s[56:59], s68 offen offset:0 sc1\n" \
M_"v[128:131], a[204:207], a[224:227], v[128:131]\n" \
M_"v[132:135], a[200:203], a[228:231], v[132:135]\n" \
M_"v[132:135], a[204:207], a[232:235], v[132:135]\n" \
M_"v[200:203], a[200:203], a[236:239], v[200:203]\n" \
M_"v[200:203], a[204:207], a[240:243], v[200:203]\n" \
B_"a[132:135], v20, s[56:59], s68 offen offset:1024 sc1\n" \
M_"v[204:207], a[200:203], a[244:247], v[204:207]\n" \
X_"a[200:201], v33 offset:9216\n" \
X_"a[202:203], v33 offset:13824\n" \
M_"v[204:207], a[204:207], a[248:251], v[204:207]\n" \
X_"a[204:205], v33 offset:32\n" \
"s_waitcnt lgkmcnt(7)\n" \
M_"v[212:215], a[208:211], a[244:247], v[212:215]\n" \
X_"a[206:207], v33 offset:4640\n" \
M_"v[212:215], a[212:215], a[248:251], v[212:215]\n" \
M_"v[208:211], a[208:211], a[236:239], v[208:211]\n" \
"buffer_load_dword v47, v38, s[56:59], s71 offen sc1\n" \
M_"v[208:211], a[212:215], a[240:243], v[208:211]\n" \
M_"v[140:143], a[208:211], a[228:231], v[140:143]\n" \
M_"v[140:143], a[212:215], a[232:235], v[140:143]\n" \
M_"v[136:139], a[208:211], a[220:223], v[136:139]\n" \
X_"a[208:209], v33 offset:9248\n" \
X_"a[210:211], v33 offset:13856\n" \
M_"v[136:139], a[212:215], a[224:227], v[136:139]\n" \
X_"a[212:213], v33 offset:64\n" \
X_"a[214:215], v33 offset:4672\n" \
M_"v[144:147], a[216:219], a[220:223], v[144:147]\n" \
B_"a[144:147], v32, s[56:59], s68 offen offset:0 sc1\n" \
M_"v[144:147], a[0:3], a[224:227], v[144:147]\n" \
M_"v[148:151], a[216:219], a[228:231], v[148:151]\n" \
M_"v[148:151], a[0:3], a[232:235], v[148:151]\n" \
M_"v[216:219], a[216:219], a[236:239], v[216:219]\n" \
M_"v[216:219], a[0:3], a[240:243], v[216:219]\n" \
B_"a[148:151], v32, s[56:59], s68 offen offset:1024 sc1\n" \
M_"v[220:223], a[216:219], a[244:247], v[220:223]\n" \
X_"a[216:217], v33 offset:9280\n" \
X_"a[218:219], v33 offset:13888\n" \
M_"v[220:223], a[0:3], a[248:251], v[220:223]\n" \
X_"a[0:1], v33 offset:96\n" \
X_"a[2:3], v33 offset:4704\n" \
M_"v[228:231], a[4:7], a[244:247], v[228:231]\n" \
M_"v[228:231], a[8:11], a[248:251], v[228:231]\n" \
M_"v[224:227], a[4:7], a[236:239], v[224:227]\n" \
B_"a[152:155], v32, s[56:59], s68 offen offset:2048 sc1\n" \
M_"v[224:227], a[8:11], a[240:243], v[224:227]\n" \
M_"v[156:159], a[4:7], a[228:231], v[156:159]\n" \
M_"v[156:159], a[8:11], a[232:235], v[156:159]\n" \
M_"v[152:155], a[4:7], a[220:223], v[152:155]\n" \
X_"a[4:5], v33 offset:9312\n" \
X_"a[6:7], v33 offset:13920\n" \
M_"v[152:155], a[8:11], a[224:227], v[152:155]\n" \
X_"a[8:9], v33 offset:128\n" \
X_"a[10:11], v33 offset:4736\n" \
M_"v[160:163], a[12:15], a[220:223], v[160:163]\n" \
B_"a[156:159], v32, s[56:59], s68 offen offset:3072 sc1\n" \
M_"v[160:163], a[16:19], a[224:227], v[160:163]\n" \
M_"v[164:167], a[12:15], a[228:231], v[164:167]\n" \
M_"v[164:167], a[16:19], a[232:235], v[164:167]\n" \
M_"v[232:235], a[12:15], a[236:239], v[232:235]\n" \
M_"v[232:235], a[16:19], a[240:243], v[232:235]\n" \
B_"a[136:139], v20, s[56:59], s68 offen offset:2048 sc1\n" \
M_"v[236:239], a[12:15], a[244:247], v[236:239]\n" \
X_"a[12:13], v33 offset:9344\n" \
X_"a[14:15], v33 offset:13952\n" \
M_"v[236:239], a[16:19], a[248:251], v[236:239]\n" \
X_"a[16:17], v33 offset:160\n" \
X_"a[18:19], v33 offset:4768\n" \
M_"v[244:247], a[20:23], a[244:247], v[244:247]\n" \
M_"v[244:247], a[24:27], a[248:251], v[244:247]\n" \
M_"v[240:243], a[20:23], a[236:239], v[240:243]\n" \
B_"a[140:143], v20, s[56:59], s68 offen offset:3072 sc1\n" \
M_"v[240:243], a[24:27], a[240:243], v[240:243]\n" \
"v_readlane_b32 s68, v11, 52\n" \
M_"v[172:175], a[20:23], a[228:231], v[172:175]\n" \
M_"v[172:175], a[24:27], a[232:235], v[172:175]\n" \
M_"v[168:171], a[20:23], a[220:223], v[168:171]\n" \
X_"a[20:21], v33 offset:9376\n" \
X_"a[22:23], v33 offset:13984\n" \
M_"v[168:171], a[24:27], a[224:227], v[168:171]\n" \
X_"a[24:25], v33 offset:192\n" \
X_"a[26:27], v33 offset:4800\n" \
M_"v[176:179], a[28:31], a[220:223], v[176:179]\n" \
"s_waitcnt lgkmcnt(8)\n" \
M_"v[176:179], a[192:195], a[224:227], v[176:179]\n" \
B_"a[32:35], v20, s[56:59], s69 offen offset:0 sc1\n" \
M_"v[180:183], a[28:31], a[228:231], v[180:183]\n" \
M_"v[180:183], a[192:195], a[232:235], v[180:183]\n" \
M_"v[248:251], a[28:31], a[236:239], v[248:251]\n" \
M_"v[248:251], a[192:195], a[240:243], v[248:251]\n" \
M_"v[252:255], a[28:31], a[244:247], v[252:255]\n" \
X_"a[28:29], v33 offset:9408\n" \
X_"a[30:31], v33 offset:14016\n" \
M_"v[252:255], a[192:195], a[248:251], v[252:255]\n" \
X_"a[192:193], v33 offset:224\n" \
"s_waitcnt vmcnt(18)\n" \
M_"v[112:115], a[196:199], a[64:67], v[112:115]\n" \
B_"a[36:39], v20, s[56:59], s69 offen offset:1024 sc1\n" \
M_"v[112:115], a[200:203], a[68:71], v[112:115]\n" \
X_"a[194:195], v33 offset:4832\n" \
M_"v[116:119], a[196:199], a[80:83], v[116:119]\n" \
X_"a[196:197], v33 offset:9440\n" \
X_"a[198:199], v33 offset:14048\n" \
M_"v[116:119], a[200:203], a[84:87], v[116:119]\n" \
X_"a[200:201], v33 offset:256\n" \
X_"a[202:203], v33 offset:4864\n" \
M_"v[124:127], a[204:207], a[80:83], v[124:127]\n" \
M_"v[124:127], a[208:211], a[84:87], v[124:127]\n" \
B_"a[48:51], v32, s[56:59], s69 offen offset:0 sc1\n" \
M_"v[120:123], a[204:207], a[64:67], v[120:123]\n" \
X_"a[204:205], v33 offset:9472\n" \
X_"a[206:207], v33 offset:14080\n" \
M_"v[120:123], a[208:211], a[68:71], v[120:123]\n" \
X_"a[208:209], v33 offset:18432\n" \
X_"a[210:211], v33 offset:23040\n" \
M_"v[128:131], a[212:215], a[64:67], v[128:131]\n" \
M_"v[128:131], a[216:219], a[68:71], v[128:131]\n" \
M_"v[132:135], a[212:215], a[80:83], v[132:135]\n" \
B_"a[52:55], v32, s[56:59], s69 offen offset:1024 sc1\n" \
M_"v[132:135], a[216:219], a[84:87], v[132:135]\n" \
X_"a[212:213], v33 offset:27648\n" \
X_"a[214:215], v33 offset:32256\n" \
M_"v[140:143], a[0:3], a[80:83], v[140:143]\n" \
X_"a[216:217], v33 offset:18464\n" \
X_"a[218:219], v33 offset:23072\n" \
M_"v[140:143], a[4:7], a[84:87], v[140:143]\n" \
"s_waitcnt vmcnt(10)\n" \
"v_cmp_ne_u32_e32 vcc, s62, v47\n" \
M_"v[136:139], a[0:3], a[64:67], v[136:139]\n" \
X_"a[0:1], v33 offset:27680\n" \
".fill (L_ab + 36864 - .) / 4, 4, 0xBF800000\n" \
"L_ap9:\n" \
"s_cbranch_execz L_ap0\n" \
X_"a[2:3], v33 offset:32288\n" \
M_"v[136:139], a[4:7], a[68:71], v[136:139]\n" \
B_"a[56:59], v32, s[56:59], s69 offen offset:2048 sc1\n" \
M_"v[144:147], a[8:11], a[64:67], v[144:147]\n" \
X_"a[4:5], v33 offset:18496\n" \
"s_waitcnt lgkmcnt(13)\n" \
M_"v[144:147], a[12:15], a[68:71], v[144:147]\n" \
X_"a[6:7], v33 offset:23104\n" \
"s_cbranch_vccnz L_sb_%=\n" \
"L_rb_%=:\n" \
M_"v[148:151], a[8:11], a[80:83], v[148:151]\n" \
X_"a[8:9], v33 offset:27712\n" \
X_"a[10:11], v33 offset:32320\n" \
M_"v[148:151], a[12:15], a[84:87], v[148:151]\n" \
X_"a[12:13], v33 offset:18528\n" \
X_"a[14:15], v33 offset:23136\n" \
B_"a[60:63], v32, s[56:59], s69 offen offset:3072 sc1\n" \
M_"v[156:159], a[16:19], a[80:83], v[156:159]\n" \
"v_readlane_b32 s71, v11, 55\n" \
"v_cmp_eq_u32_e32 vcc, 0, v44\n" \
M_"v[156:159], a[20:23], a[84:87], v[156:159]\n" \
M_"v[152:155], a[16:19], a[64:67], v[152:155]\n" \
X_"a[16:17], v33 offset:27744\n" \
X_"a[18:19], v33 offset:32352\n" \
M_"v[152:155], a[20:23], a[68:71], v[152:155]\n" \
X_"a[20:21], v33 offset:18560\n" \
X_"a[22:23], v33 offset:23168\n" \
B_"a[40:43], v20, s[56:59], s69 offen offset:2048 sc1\n" \
M_"v[160:163], a[24:27], a[64:67], v[160:163]\n" \
"v_cndmask_b32_e32 v50, 0, v45, vcc\n" \
"v_cmp_eq_u32_e32 vcc, 1, v44\n" \
M_"v[160:163], a[28:31], a[68:71], v[160:163]\n" \
"v_and_b32_e32 v44, 0x3c0, v20\n" \
"v_cndmask_b32_e32 v51, 0, v45, vcc\n" \
M_"v[164:167], a[24:27], a[80:83], v[164:167]\n" \
X_"a[24:25], v33 offset:27776\n" \
X_"a[26:27], v33 offset:32384\n" \
M_"v[164:167], a[28:31], a[84:87], v[164:167]\n" \
X_"a[28:29], v33 offset:18592\n" \
X_"a[30:31], v33 offset:23200\n" \
B_"a[44:47], v20, s[56:59], s69 offen offset:3072 sc1\n" \
M_"v[172:175], a[192:195], a[80:83], v[172:175]\n" \
"v_readlane_b32 s69, v11, 53\n" \
"v_bfe_u32 v45, v20, 1, 5\n" \
M_"v[172:175], a[196:199], a[84:87], v[172:175]\n" \
"v_and_b32_e32 v45, 24, v45\n" \
"v_add_u32_e32 v45, 0x4800, v45\n" \
M_"v[168:171], a[192:195], a[64:67], v[168:171]\n" \
X_"a[192:193], v33 offset:27808\n" \
X_"a[194:195], v33 offset:32416\n" \
M_"v[168:171], a[196:199], a[68:71], v[168:171]\n" \
X_"a[196:197], v33 offset:18624\n" \
"s_waitcnt lgkmcnt(13)\n" \
M_"v[176:179], a[200:203], a[64:67], v[176:179]\n" \
X_"a[198:199], v33 offset:23232\n" \
"v_add_u32_e32 v36, v44, v45\n" \
M_"v[176:179], a[204:207], a[68:71], v[176:179]\n" \
B_"a[220:223], v20, s[56:59], s64 offen offset:0 sc1\n" \
M_"v[180:183], a[200:203], a[80:83], v[180:183]\n" \
X_"a[200:201], v33 offset:27840\n" \
X_"a[202:203], v33 offset:32448\n" \
M_"v[180:183], a[204:207], a[84:87], v[180:183]\n" \
X_"a[204:205], v33 offset:18656\n" \
X_"a[206:207], v33 offset:23264\n" \
M_"v[112:115], a[208:211], a[72:75], v[112:115]\n" \
M_"v[112:115], a[212:215], a[76:79], v[112:115]\n" \
M_"v[116:119], a[208:211], a[88:91], v[116:119]\n" \
X_"a[208:209], v33 offset:27872\n" \
X_"a[210:211], v33 offset:32480\n" \
M_"v[116:119], a[212:215], a[92:95], v[116:119]\n" \
X_"a[212:213], v33 offset:18688\n" \
X_"a[214:215], v33 offset:23296\n" \
M_"v[124:127], a[216:219], a[88:91], v[124:127]\n" \
B_"a[224:227], v20, s[56:59], s64 offen offset:1024 sc1\n" \
M_"v[124:127], a[0:3], a[92:95], v[124:127]\n" \
M_"v[120:123], a[216:219], a[72:75], v[120:123]\n" \
X_"a[216:217], v33 offset:27904\n" \
X_"a[218:219], v33 offset:32512\n" \
M_"v[120:123], a[0:3], a[76:79], v[120:123]\n" \
"s_barrier\n" \
M_"v[128:131], a[4:7], a[72:75], v[128:131]\n" \
X_"a[0:1], v33 offset:36864\n" \
X_"a[2:3], v33 offset:41472\n" \
M_"v[128:131], a[8:11], a[76:79], v[128:131]\n" \
B_"a[236:239], v32, s[56:59], s64 offen offset:0 sc1\n" \
M_"v[132:135], a[4:7], a[88:91], v[132:135]\n" \
X_"a[4:5], v33 offset:46080\n" \
X_"a[6:7], v33 offset:50688\n" \
M_"v[132:135], a[8:11], a[92:95], v[132:135]\n" \
X_"a[8:9], v33 offset:55296\n" \
"s_waitcnt lgkmcnt(15)\n" \
M_"v[140:143], a[12:15], a[88:91], v[140:143]\n" \
X_"a[10:11], v33 offset:59904\n" \
M_"v[140:143], a[16:19], a[92:95], v[140:143]\n" \
M_"v[136:139], a[12:15], a[72:75], v[136:139]\n" \
X_"a[12:13], v10 offset:0\n" \
X_"a[14:15], v10 offset:4608\n" \
M_"v[136:139], a[16:19], a[76:79], v[136:139]\n" \
X_"a[16:17], v34 offset:0\n" \
X_"a[18:19], v34 offset:4608\n" \
M_"v[144:147], a[20:23], a[72:75], v[144:147]\n" \
B_"a[240:243], v32, s[56:59], s64 offen offset:1024 sc1\n" \
M_"v[144:147], a[24:27], a[76:79], v[144:147]\n" \
M_"v[148:151], a[20:23], a[88:91], v[148:151]\n" \
X_"a[20:21], v34 offset:9216\n" \
X_"a[22:23], v34 offset:13824\n" \
M_"v[148:151], a[24:27], a[92:95], v[148:151]\n" \
X_"a[24:25], v34 offset:18432\n" \
X_"a[26:27], v34 offset:23040\n" \
M_"v[156:159], a[28:31], a[88:91], v[156:159]\n" \
M_"v[156:159], a[192:195], a[92:95], v[156:159]\n" \
B_"a[244:247], v32, s[56:59], s64 offen offset:2048 sc1\n" \
M_"v[152:155], a[28:31], a[72:75], v[152:155]\n" \
X_"a[28:29], v10 offset:9216\n" \
X_"a[30:31], v10 offset:13824\n" \
M_"v[152:155], a[192:195], a[76:79], v[152:155]\n" \
X_"a[192:193], v33 offset:36896\n" \
X_"a[194:195], v33 offset:41504\n" \
M_"v[160:163], a[196:199], a[72:75], v[160:163]\n" \
"s_waitcnt lgkmcnt(15)\n" \
M_"v[160:163], a[200:203], a[76:79], v[160:163]\n" \
M_"v[164:167], a[196:199], a[88:91], v[164:167]\n" \
X_"a[196:197], v33 offset:46112\n" \
X_"a[198:199], v33 offset:50720\n" \
M_"v[164:167], a[200:203], a[92:95], v[164:167]\n" \
X_"a[200:201], v33 offset:55328\n" \
X_"a[202:203], v33 offset:59936\n" \
M_"v[172:175], a[204:207], a[88:91], v[172:175]\n" \
B_"a[248:251], v32, s[56:59], s64 offen offset:3072 sc1\n" \
M_"v[172:175], a[208:211], a[92:95], v[172:175]\n" \
M_"v[168:171], a[204:207], a[72:75], v[168:171]\n" \
X_"a[204:205], v10 offset:32\n" \
X_"a[206:207], v10 offset:4640\n" \
M_"v[168:171], a[208:211], a[76:79], v[168:171]\n" \
X_"a[208:209], v34 offset:32\n" \
X_"a[210:211], v34 offset:4640\n" \
M_"v[176:179], a[212:215], a[72:75], v[176:179]\n" \
M_"v[176:179], a[216:219], a[76:79], v[176:179]\n" \
B_"a[228:231], v20, s[56:59], s64 offen offset:2048 sc1\n" \
M_"v[180:183], a[212:215], a[88:91], v[180:183]\n" \
X_"a[212:213], v34 offset:9248\n" \
X_"a[214:215], v34 offset:13856\n" \
M_"v[180:183], a[216:219], a[92:95], v[180:183]\n" \
X_"a[216:217], v34 offset:18464\n" \
X_"a[218:219], v34 offset:23072\n" \
M_"v[112:115], a[0:3], a[128:131], v[112:115]\n" \
"s_waitcnt lgkmcnt(15)\n" \
M_"v[112:115], a[4:7], a[132:135], v[112:115]\n" \
"s_waitcnt vmcnt(18)\n" \
M_"v[116:119], a[0:3], a[144:147], v[116:119]\n" \
X_"a[0:1], v10 offset:9248\n" \
X_"a[2:3], v10 offset:13856\n" \
M_"v[116:119], a[4:7], a[148:151], v[116:119]\n" \
X_"a[4:5], v33 offset:36928\n" \
X_"a[6:7], v33 offset:41536\n" \
M_"v[116:119], a[8:11], a[152:155], v[116:119]\n" \
"s_waitcnt vmcnt(17)\n" \
M_"v[116:119], a[12:15], a[156:159], v[116:119]\n" \
"s_waitcnt vmcnt(16)\n" \
M_"v[112:115], a[8:11], a[136:139], v[112:115]\n" \
X_"a[8:9], v33 offset:46144\n" \
"s_waitcnt vmcnt(15)\n" \
M_"v[112:115], a[12:15], a[140:143], v[112:115]\n" \
X_"a[10:11], v33 offset:50752\n" \
"s_waitcnt vmcnt(14)\n" \
M_"v[112:115], a[16:19], a[32:35], v[112:115]\n" \
X_"a[12:13], v33 offset:55360\n" \
"s_waitcnt vmcnt(13)\n" \
M_"v[112:115], a[20:23], a[36:39], v[112:115]\n" \
X_"a[14:15], v33 offset:59968\n" \
"s_waitcnt vmcnt(12)\n" \
M_"v[116:119], a[16:19], a[48:51], v[116:119]\n" \
X_"a[16:17], v10 offset:64\n" \
"s_waitcnt vmcnt(11)\n" \
M_"v[116:119], a[20:23], a[52:55], v[116:119]\n" \
X_"a[18:19], v10 offset:4672\n" \
"s_waitcnt vmcnt(10)\n" \
M_"v[116:119], a[24:27], a[56:59], v[116:119]\n" \
X_"a[20:21], v34 offset:64\n" \
X_"a[22:23], v34 offset:4672\n" \
B_"a[232:235], v20, s[56:59], s64 offen offset:3072 sc1\n" \
"s_waitcnt vmcnt(10) lgkmcnt(12)\n" \
M_"v[116:119], a[28:31], a[60:63], v[116:119]\n" \
B_"a[160:163], v20, s[56:59], s72 offen offset:0 sc1\n" \
"s_waitcnt vmcnt(10)\n" \
M_"v[112:115], a[24:27], a[40:43], v[112:115]\n" \
X_"a[24:25], v34 offset:9280\n" \
X_"a[26:27], v34 offset:13888\n" \
B_"a[164:167], v20, s[56:59], s72 offen offset:1024 sc1\n" \
"s_waitcnt vmcnt(10)\n" \
M_"v[112:115], a[28:31], a[44:47], v[112:115]\n" \
X_"a[28:29], v34 offset:18496\n" \
X_"a[30:31], v34 offset:23104\n" \
M_"v[120:123], a[192:195], a[128:131], v[120:123]\n" \
B_"a[176:179], v32, s[56:59], s72 offen offset:0 sc1\n" \
M_"v[120:123], a[196:199], a[132:135], v[120:123]\n" \
M_"v[124:127], a[192:195], a[144:147], v[124:127]\n" \
X_"a[192:193], v10 offset:9280\n" \
X_"a[194:195], v10 offset:13888\n" \
C_"v112, v112, v113\n" \
C_"v113, v114, v115\n" \
C_"v114, v116, v117\n" \
C_"v115, v118, v119\n" \
"buffer_store_dwordx4 v[112:115], v20, s[56:59], s65 offen offset:0 sc1\n" \
"s_add_u32 s65, s65, 0x3800\n" \
M_"v[124:127], a[196:199], a[148:151], v[124:127]\n" \
X_"a[196:197], v33 offset:36960\n" \
X_"a[198:199], v33 offset:41568\n" \
M_"v[124:127], a[200:203], a[152:155], v[124:127]\n" \
M_"v[124:127], a[204:207], a[156:159], v[124:127]\n" \
M_"v[120:123], a[200:203], a[136:139], v[120:123]\n" \
X_"a[200:201], v33 offset:46176\n" \
X_"a[202:203], v33 offset:50784\n" \
M_"v[120:123], a[204:207], a[140:143], v[120:123]\n" \
X_"a[204:205], v33 offset:55392\n" \
X_"a[206:207], v33 offset:60000\n" \
M_"v[120:123], a[208:211], a[32:35], v[120:123]\n" \
B_"a[180:183], v32, s[56:59], s72 offen offset:1024 sc1\n" \
M_"v[120:123], a[212:215], a[36:39], v[120:123]\n" \
M_"v[124:127], a[208:211], a[48:51], v[124:127]\n" \
X_"a[208:209], v10 offset:96\n" \
X_"a[210:211], v10 offset:4704\n" \
M_"v[124:127], a[212:215], a[52:55], v[124:127]\n" \
X_"a[212:213], v34 offset:96\n" \
X_"a[214:215], v34 offset:4704\n" \
M_"v[124:127], a[216:219], a[56:59], v[124:127]\n" \
"s_waitcnt lgkmcnt(12)\n" \
M_"v[124:127], a[0:3], a[60:63], v[124:127]\n" \
B_"a[168:171], v20, s[56:59], s72 offen offset:2048 sc1\n" \
M_"v[120:123], a[216:219], a[40:43], v[120:123]\n" \
X_"a[216:217], v34 offset:9312\n" \
X_"a[218:219], v34 offset:13920\n" \
M_"v[120:123], a[0:3], a[44:47], v[120:123]\n" \
X_"a[0:1], v34 offset:18528\n" \
X_"a[2:3], v34 offset:23136\n" \
M_"v[128:131], a[4:7], a[128:131], v[128:131]\n" \
M_"v[128:131], a[8:11], a[132:135], v[128:131]\n" \
M_"v[132:135], a[4:7], a[144:147], v[132:135]\n" \
X_"a[4:5], v10 offset:9312\n" \
X_"a[6:7], v10 offset:13920\n" \
M_"v[132:135], a[8:11], a[148:151], v[132:135]\n" \
C_"v120, v120, v121\n" \
C_"v121, v122, v123\n" \
C_"v122, v124, v125\n" \
C_"v123, v126, v127\n" \
"buffer_store_dwordx4 v[120:123], v20, s[56:59], s66 offen offset:0 sc1\n" \
"s_add_u32 s66, s66, 0x3000\n" \
M_"v[132:135], a[12:15], a[152:155], v[132:135]\n" \
X_"a[8:9], v33 offset:36992\n" \
X_"a[10:11], v33 offset:41600\n" \
M_"v[132:135], a[16:19], a[156:159], v[132:135]\n" \
M_"v[128:131], a[12:15], a[136:139], v[128:131]\n" \
X_"a[12:13], v33 offset:46208\n" \
X_"a[14:15], v33 offset:50816\n" \
M_"v[128:131], a[16:19], a[140:143], v[128:131]\n" \
X_"a[16:17], v33 offset:55424\n" \
X_"a[18:19], v33 offset:60032\n" \
M_"v[128:131], a[20:23], a[32:35], v[128:131]\n" \
B_"a[184:187], v32, s[56:59], s72 offen offset:2048 sc1\n" \
M_"v[128:131], a[24:27], a[36:39], v[128:131]\n" \
M_"v[132:135], a[20:23], a[48:51], v[132:135]\n" \
X_"a[20:21], v10 offset:128\n" \
X_"a[22:23], v10 offset:4736\n" \
M_"v[132:135], a[24:27], a[52:55], v[132:135]\n" \
X_"a[24:25], v34 offset:128\n" \
X_"a[26:27], v34 offset:4736\n" \
M_"v[132:135], a[28:31], a[56:59], v[132:135]\n" \
"s_waitcnt lgkmcnt(12)\n" \
M_"v[132:135], a[192:195], a[60:63], v[132:135]\n" \
B_"a[172:175], v20, s[56:59], s72 offen offset:3072 sc1\n" \
M_"v[128:131], a[28:31], a[40:43], v[128:131]\n" \
X_"a[28:29], v34 offset:9344\n" \
X_"a[30:31], v34 offset:13952\n" \
M_"v[128:131], a[192:195], a[44:47], v[128:131]\n" \
X_"a[192:193], v34 offset:18560\n" \
X_"a[194:195], v34 offset:23168\n" \
M_"v[136:139], a[196:199], a[128:131], v[136:139]\n" \
M_"v[136:139], a[200:203], a[132:135], v[136:139]\n" \
M_"v[140:143], a[196:199], a[144:147], v[140:143]\n" \
B_"a[188:191], v32, s[56:59], s72 offen offset:3072 sc1\n" \
M_"v[140:143], a[200:203], a[148:151], v[140:143]\n" \
"v_readlane_b32 s72, v11, 56\n" \
C_"v128, v128, v129\n" \
C_"v129, v130, v131\n" \
C_"v130, v132, v133\n" \
C_"v131, v134, v135\n" \
M_"v[140:143], a[204:207], a[152:155], v[140:143]\n" \
"buffer_store_dwordx4 v[128:131], v20, s[56:59], s67 offen offset:0 sc1\n" \
"s_add_u32 s67, s67, 0x2800\n" \
M_"v[140:143], a[208:211], a[156:159], v[140:143]\n" \
X_"a[196:197], v10 offset:9344\n" \
X_"a[198:199], v10 offset:13952\n" \
M_"v[136:139], a[204:207], a[136:139], v[136:139]\n" \
X_"a[200:201], v33 offset:37024\n" \
X_"a[202:203], v33 offset:41632\n" \
M_"v[136:139], a[208:211], a[140:143], v[136:139]\n" \
X_"a[204:205], v33 offset:46240\n" \
X_"a[206:207], v33 offset:50848\n" \
M_"v[136:139], a[212:215], a[32:35], v[136:139]\n" \
X_"a[208:209], v33 offset:55456\n" \
X_"a[210:211], v33 offset:60064\n" \
M_"v[136:139], a[216:219], a[36:39], v[136:139]\n" \
B_"a[96:99], v20, s[56:59], s93 offen offset:0 sc1\n" \
M_"v[140:143], a[212:215], a[48:51], v[140:143]\n" \
X_"a[212:213], v10 offset:160\n" \
X_"a[214:215], v10 offset:4768\n" \
M_"v[140:143], a[216:219], a[52:55], v[140:143]\n" \
X_"a[216:217], v34 offset:160\n" \
X_"a[218:219], v34 offset:4768\n" \
M_"v[140:143], a[0:3], a[56:59], v[140:143]\n" \
"s_waitcnt lgkmcnt(13)\n" \
M_"v[140:143], a[4:7], a[60:63], v[140:143]\n" \
M_"v[136:139], a[0:3], a[40:43], v[136:139]\n" \
B_"a[100:103], v20, s[56:59], s93 offen offset:1024 sc1\n" \
M_"v[136:139], a[4:7], a[44:47], v[136:139]\n" \
X_"a[0:1], v34 offset:9376\n" \
X_"a[2:3], v34 offset:13984\n" \
M_"v[144:147], a[8:11], a[128:131], v[144:147]\n" \
X_"a[4:5], v34 offset:18592\n" \
X_"a[6:7], v34 offset:23200\n" \
M_"v[144:147], a[12:15], a[132:135], v[144:147]\n" \
M_"v[148:151], a[8:11], a[144:147], v[148:151]\n" \
X_"a[8:9], v10 offset:9376\n" \
C_"v136, v136, v137\n" \
C_"v137, v138, v139\n" \
C_"v138, v140, v141\n" \
C_"v139, v142, v143\n" \
"buffer_store_dwordx4 v[136:139], v20, s[56:59], s68 offen offset:0 sc1\n" \
"s_add_u32 s68, s68, 0x2000\n" \
M_"v[148:151], a[12:15], a[148:151], v[148:151]\n" \
X_"a[10:11], v10 offset:13984\n" \
X_"a[12:13], v33 offset:37056\n" \
M_"v[148:151], a[16:19], a[152:155], v[148:151]\n" \
X_"a[14:15], v33 offset:41664\n" \
M_"v[148:151], a[20:23], a[156:159], v[148:151]\n" \
M_"v[144:147], a[16:19], a[136:139], v[144:147]\n" \
B_"a[112:115], v32, s[56:59], s93 offen offset:0 sc1\n" \
M_"v[144:147], a[20:23], a[140:143], v[144:147]\n" \
X_"a[16:17], v33 offset:46272\n" \
X_"a[18:19], v33 offset:50880\n" \
M_"v[144:147], a[24:27], a[32:35], v[144:147]\n" \
X_"a[20:21], v33 offset:55488\n" \
X_"a[22:23], v33 offset:60096\n" \
M_"v[144:147], a[28:31], a[36:39], v[144:147]\n" \
M_"v[148:151], a[24:27], a[48:51], v[148:151]\n" \
X_"a[24:25], v10 offset:192\n" \
X_"a[26:27], v10 offset:4800\n" \
B_"a[116:119], v32, s[56:59], s93 offen offset:1024 sc1\n" \
M_"v[148:151], a[28:31], a[52:55], v[148:151]\n" \
X_"a[28:29], v34 offset:192\n" \
"s_waitcnt lgkmcnt(13)\n" \
M_"v[148:151], a[192:195], a[56:59], v[148:151]\n" \
X_"a[30:31], v34 offset:4800\n" \
M_"v[148:151], a[196:199], a[60:63], v[148:151]\n" \
M_"v[144:147], a[192:195], a[40:43], v[144:147]\n" \
X_"a[192:193], v34 offset:9408\n" \
X_"a[194:195], v34 offset:14016\n" \
B_"a[120:123], v32, s[56:59], s93 offen offset:2048 sc1\n" \
M_"v[144:147], a[196:199], a[44:47], v[144:147]\n" \
X_"a[196:197], v34 offset:18624\n" \
X_"a[198:199], v34 offset:23232\n" \
M_"v[152:155], a[200:203], a[128:131], v[152:155]\n" \
M_"v[152:155], a[204:207], a[132:135], v[152:155]\n" \
M_"v[156:159], a[200:203], a[144:147], v[156:159]\n" \
X_"a[200:201], v10 offset:9408\n" \
X_"a[202:203], v10 offset:14016\n" \
B_"a[124:127], v32, s[56:59], s93 offen offset:3072 sc1\n" \
C_"v144, v144, v145\n" \
C_"v145, v146, v147\n" \
C_"v146, v148, v149\n" \
C_"v147, v150, v151\n" \
M_"v[156:159], a[204:207], a[148:151], v[156:159]\n" \
X_"a[204:205], v33 offset:256\n" \
X_"a[206:207], v33 offset:4864\n" \
"buffer_store_dwordx4 v[144:147], v20, s[56:59], s69 offen offset:0 sc1\n" \
"s_add_u32 s69, s69, 0x1800\n" \
M_"v[156:159], a[208:211], a[152:155], v[156:159]\n" \
M_"v[156:159], a[212:215], a[156:159], v[156:159]\n" \
M_"v[152:155], a[208:211], a[136:139], v[152:155]\n" \
X_"a[208:209], v33 offset:9472\n" \
X_"a[210:211], v33 offset:14080\n" \
M_"v[152:155], a[212:215], a[140:143], v[152:155]\n" \
B_"a[104:107], v20, s[56:59], s93 offen offset:2048 sc1\n" \
M_"v[152:155], a[216:219], a[32:35], v[152:155]\n" \
X_"a[212:213], v33 offset:18688\n" \
X_"a[214:215], v33 offset:23296\n" \
M_"v[152:155], a[0:3], a[36:39], v[152:155]\n" \
M_"v[156:159], a[216:219], a[48:51], v[156:159]\n" \
X_"a[216:217], v33 offset:27904\n" \
X_"a[218:219], v33 offset:32512\n" \
M_"v[156:159], a[0:3], a[52:55], v[156:159]\n" \
X_"a[0:1], v33 offset:37120\n" \
B_"a[108:111], v20, s[56:59], s93 offen offset:3072 sc1\n" \
"s_waitcnt lgkmcnt(11)\n" \
M_"v[156:159], a[4:7], a[56:59], v[156:159]\n" \
X_"a[2:3], v33 offset:41728\n" \
M_"v[156:159], a[8:11], a[60:63], v[156:159]\n" \
M_"v[152:155], a[4:7], a[40:43], v[152:155]\n" \
X_"a[4:5], v33 offset:46336\n" \
X_"a[6:7], v33 offset:50944\n" \
M_"v[152:155], a[8:11], a[44:47], v[152:155]\n" \
X_"a[8:9], v33 offset:55552\n" \
X_"a[10:11], v33 offset:60160\n" \
M_"v[160:163], a[12:15], a[128:131], v[160:163]\n" \
B_"v[64:67], v20, s[56:59], s95 offen offset:0 sc1\n" \
M_"v[160:163], a[16:19], a[132:135], v[160:163]\n" \
M_"v[164:167], a[12:15], a[144:147], v[164:167]\n" \
X_"a[12:13], v10 offset:256\n" \
X_"a[14:15], v10 offset:4864\n" \
C_"v152, v152, v153\n" \
C_"v153, v154, v155\n" \
C_"v154, v156, v157\n" \
C_"v155, v158, v159\n" \
"buffer_store_dwordx4 v[152:155], v20, s[56:59], s70 offen offset:0 sc1\n" \
"s_add_u32 s70, s70, 0x1000\n" \
M_"v[164:167], a[16:19], a[148:151], v[164:167]\n" \
X_"a[16:17], v34 offset:256\n" \
X_"a[18:19], v34 offset:4864\n" \
M_"v[164:167], a[20:23], a[152:155], v[164:167]\n" \
M_"v[164:167], a[24:27], a[156:159], v[164:167]\n" \
M_"v[160:163], a[20:23], a[136:139], v[160:163]\n" \
X_"a[20:21], v34 offset:9472\n" \
X_"a[22:23], v34 offset:14080\n" \
M_"v[160:163], a[24:27], a[140:143], v[160:163]\n" \
X_"a[24:25], v34 offset:18688\n" \
X_"a[26:27], v34 offset:23296\n" \
"s_waitcnt vmcnt(19)\n" \
"s_mov_b64 exec, 1\n" \
"buffer_store_dword v39, off, s[56:59], s65 offset:0 sc1\n" \
"s_mov_b64 exec, -1\n" \
M_"v[160:163], a[28:31], a[32:35], v[160:163]\n" \
M_"v[160:163], a[192:195], a[36:39], v[160:163]\n" \
M_"v[164:167], a[28:31], a[48:51], v[164:167]\n" \
X_"a[28:29], v10 offset:9472\n" \
X_"a[30:31], v10 offset:14080\n" \
M_"v[164:167], a[192:195], a[52:55], v[164:167]\n" \
X_"a[192:193], v33 offset:0\n" \
X_"a[194:195], v33 offset:4608\n" \
M_"v[164:167], a[196:199], a[56:59], v[164:167]\n" \
"s_waitcnt lgkmcnt(12)\n" \
M_"v[164:167], a[200:203], a[60:63], v[164:167]\n" \
B_"v[68:71], v20, s[56:59], s95 offen offset:1024 sc1\n" \
M_"v[160:163], a[196:199], a[40:43], v[160:163]\n" \
X_"a[196:197], v33 offset:9216\n" \
X_"a[198:199], v33 offset:13824\n" \
M_"v[160:163], a[200:203], a[44:47], v[160:163]\n" \
X_"a[200:201], v33 offset:18432\n" \
X_"a[202:203], v33 offset:23040\n" \
M_"v[248:251], a[204:207], a[220:223], v[248:251]\n" \
M_"v[248:251], a[208:211], a[224:227], v[248:251]\n" \
M_"v[252:255], a[204:207], a[236:239], v[252:255]\n" \
X_"a[204:205], v33 offset:27648\n" \
X_"a[206:207], v33 offset:32256\n" \
M_"v[252:255], a[208:211], a[240:243], v[252:255]\n" \
C_"v160, v160, v161\n" \
C_"v161, v162, v163\n" \
C_"v162, v164, v165\n" \
C_"v163, v166, v167\n" \
"buffer_store_dwordx4 v[160:163], v20, s[56:59], s71 offen offset:0 sc1\n" \
"s_add_u32 s71, s71, 0x800\n" \
M_"v[252:255], a[212:215], a[244:247], v[252:255]\n" \
X_"a[208:209], v33 offset:36864\n" \
X_"a[210:211], v33 offset:41472\n" \
M_"v[252:255], a[216:219], a[248:251], v[252:255]\n" \
M_"v[248:251], a[212:215], a[228:231], v[248:251]\n" \
X_"a[212:213], v33 offset:46080\n" \
X_"a[214:215], v33 offset:50688\n" \
"s_waitcnt vmcnt(19)\n" \
"s_mov_b64 exec, 1\n" \
"buffer_store_dword v39, off, s[56:59], s66 offset:4 sc1\n" \
"s_mov_b64 exec, -1\n" \
"v_readlane_b32 s66, v11, 50\n" \
M_"v[248:251], a[216:219], a[232:235], v[248:251]\n" \
X_"a[216:217], v33 offset:55296\n" \
X_"a[218:219], v33 offset:59904\n" \
M_"v[176:179], a[0:3], a[128:131], v[176:179]\n" \
M_"v[176:179], a[4:7], a[132:135], v[176:179]\n" \
M_"v[180:183], a[0:3], a[144:147], v[180:183]\n" \
B_"v[72:75], v32, s[56:59], s95 offen offset:0 sc1\n" \
M_"v[180:183], a[4:7], a[148:151], v[180:183]\n" \
M_"v[248:251], a[0:3], a[160:163], v[248:251]\n" \
M_"v[248:251], a[4:7], a[164:167], v[248:251]\n" \
M_"v[252:255], a[0:3], a[176:179], v[252:255]\n" \
X_"a[0:1], v10 offset:0\n" \
X_"a[2:3], v10 offset:4608\n" \
M_"v[252:255], a[4:7], a[180:183], v[252:255]\n" \
X_"a[4:5], v34 offset:0\n" \
"s_waitcnt vmcnt(20)\n" \
M_"v[252:255], a[8:11], a[184:187], v[252:255]\n" \
X_"a[6:7], v34 offset:4608\n" \
"s_waitcnt vmcnt(18) lgkmcnt(10)\n" \
M_"v[252:255], a[12:15], a[188:191], v[252:255]\n" \
B_"v[76:79], v32, s[56:59], s95 offen offset:1024 sc1\n" \
M_"v[248:251], a[8:11], a[168:171], v[248:251]\n" \
M_"v[248:251], a[12:15], a[172:175], v[248:251]\n" \
M_"v[180:183], a[8:11], a[152:155], v[180:183]\n" \
M_"v[180:183], a[12:15], a[156:159], v[180:183]\n" \
M_"v[176:179], a[8:11], a[136:139], v[176:179]\n" \
X_"a[8:9], v34 offset:9216\n" \
X_"a[10:11], v34 offset:13824\n" \
M_"v[176:179], a[12:15], a[140:143], v[176:179]\n" \
X_"a[12:13], v34 offset:18432\n" \
X_"a[14:15], v34 offset:23040\n" \
"s_waitcnt vmcnt(18)\n" \
"s_mov_b64 exec, 1\n" \
"buffer_store_dword v39, off, s[56:59], s67 offset:8 sc1\n" \
"s_mov_b64 exec, -1\n" \
"v_readlane_b32 s67, v11, 51\n" \
M_"v[176:179], a[16:19], a[32:35], v[176:179]\n" \
M_"v[176:179], a[20:23], a[36:39], v[176:179]\n" \
M_"v[180:183], a[16:19], a[48:51], v[180:183]\n" \
M_"v[180:183], a[20:23], a[52:55], v[180:183]\n" \
"s_waitcnt vmcnt(18)\n" \
M_"v[248:251], a[16:19], a[96:99], v[248:251]\n" \
"s_waitcnt vmcnt(17)\n" \
M_"v[248:251], a[20:23], a[100:103], v[248:251]\n" \
"s_waitcnt vmcnt(15)\n" \
M_"v[252:255], a[16:19], a[112:115], v[252:255]\n" \
X_"a[16:17], v10 offset:9216\n" \
"s_mov_b64 exec, 1\n" \
"buffer_store_dword v39, off, s[56:59], s68 offset:12 sc1\n" \
"s_mov_b64 exec, -1\n" \
"s_waitcnt vmcnt(15)\n" \
M_"v[252:255], a[20:23], a[116:119], v[252:255]\n" \
"v_readlane_b32 s68, v11, 52\n" \
"s_waitcnt vmcnt(14)\n" \
M_"v[252:255], a[24:27], a[120:123], v[252:255]\n" \
X_"a[18:19], v10 offset:13824\n" \
"s_waitcnt vmcnt(13)\n" \
M_"v[252:255], a[28:31], a[124:127], v[252:255]\n" \
X_"a[20:21], v33 offset:32\n" \
X_"a[22:23], v33 offset:4640\n" \
B_"v[80:83], v20, s[56:59], s96 offen offset:0 sc1\n" \
"s_waitcnt vmcnt(12)\n" \
M_"v[248:251], a[24:27], a[104:107], v[248:251]\n" \
"s_mov_b64 exec, 1\n" \
"buffer_store_dword v39, off, s[56:59], s69 offset:16 sc1\n" \
"s_mov_b64 exec, -1\n" \
"v_readlane_b32 s69, v11, 53\n" \
".fill (L_ab + 40960 - .) / 4, 4, 0xBF800000\n" \
"L_ap10:\n" \
"s_cbranch_execz L_ap0\n" \
"s_waitcnt vmcnt(12)\n" \
M_"v[248:251], a[28:31], a[108:111], v[248:251]\n" \
M_"v[180:183], a[24:27], a[56:59], v[180:183]\n" \
B_"v[84:87], v20, s[56:59], s96 offen offset:1024 sc1\n" \
M_"v[180:183], a[28:31], a[60:63], v[180:183]\n" \
M_"v[176:179], a[24:27], a[40:43], v[176:179]\n" \
X_"a[24:25], v33 offset:9248\n" \
X_"a[26:27], v33 offset:13856\n" \
M_"v[176:179], a[28:31], a[44:47], v[176:179]\n" \
X_"a[28:29], v33 offset:18464\n" \
C_"v248, v248, v249\n" \
C_"v249, v250, v251\n" \
C_"v250, v252, v253\n" \
C_"v251, v254, v255\n" \
"buffer_store_dwordx4 v[248:251], v20, s[56:59], s72 offen offset:1024 sc1\n" \
M_"v[184:187], a[192:195], a[220:223], v[184:187]\n" \
X_"a[30:31], v33 offset:23072\n" \
C_"v176, v176, v177\n" \
C_"v177, v178, v179\n" \
C_"v178, v180, v181\n" \
C_"v179, v182, v183\n" \
M_"v[184:187], a[196:199], a[224:227], v[184:187]\n" \
"buffer_store_dwordx4 v[176:179], v20, s[56:59], s72 offen offset:0 sc1\n" \
M_"v[188:191], a[192:195], a[236:239], v[188:191]\n" \
X_"a[192:193], v33 offset:27680\n" \
X_"a[194:195], v33 offset:32288\n" \
M_"v[188:191], a[196:199], a[240:243], v[188:191]\n" \
X_"a[196:197], v33 offset:36896\n" \
X_"a[198:199], v33 offset:41504\n" \
M_"v[188:191], a[200:203], a[244:247], v[188:191]\n" \
M_"v[188:191], a[204:207], a[248:251], v[188:191]\n" \
M_"v[184:187], a[200:203], a[228:231], v[184:187]\n" \
X_"a[200:201], v33 offset:46112\n" \
X_"a[202:203], v33 offset:50720\n" \
M_"v[184:187], a[204:207], a[232:235], v[184:187]\n" \
X_"a[204:205], v33 offset:55328\n" \
"s_waitcnt lgkmcnt(11)\n" \
M_"v[184:187], a[208:211], a[160:163], v[184:187]\n" \
B_"v[88:91], v32, s[56:59], s96 offen offset:0 sc1\n" \
M_"v[184:187], a[212:215], a[164:167], v[184:187]\n" \
X_"a[206:207], v33 offset:59936\n" \
M_"v[188:191], a[208:211], a[176:179], v[188:191]\n" \
X_"a[208:209], v10 offset:32\n" \
X_"a[210:211], v10 offset:4640\n" \
M_"v[188:191], a[212:215], a[180:183], v[188:191]\n" \
X_"a[212:213], v34 offset:32\n" \
X_"a[214:215], v34 offset:4640\n" \
M_"v[188:191], a[216:219], a[184:187], v[188:191]\n" \
M_"v[188:191], a[0:3], a[188:191], v[188:191]\n" \
B_"v[92:95], v32, s[56:59], s96 offen offset:1024 sc1\n" \
M_"v[184:187], a[216:219], a[168:171], v[184:187]\n" \
X_"a[216:217], v34 offset:9248\n" \
X_"a[218:219], v34 offset:13856\n" \
M_"v[184:187], a[0:3], a[172:175], v[184:187]\n" \
X_"a[0:1], v34 offset:18464\n" \
X_"a[2:3], v34 offset:23072\n" \
M_"v[184:187], a[4:7], a[96:99], v[184:187]\n" \
M_"v[184:187], a[8:11], a[100:103], v[184:187]\n" \
M_"v[188:191], a[4:7], a[112:115], v[188:191]\n" \
X_"a[4:5], v10 offset:9248\n" \
X_"a[6:7], v10 offset:13856\n" \
M_"v[188:191], a[8:11], a[116:119], v[188:191]\n" \
X_"a[8:9], v33 offset:64\n" \
X_"a[10:11], v33 offset:4672\n" \
M_"v[188:191], a[12:15], a[120:123], v[188:191]\n" \
B_"v[96:99], v20, s[56:59], s97 offen offset:0 sc1\n" \
M_"v[188:191], a[16:19], a[124:127], v[188:191]\n" \
M_"v[184:187], a[12:15], a[104:107], v[184:187]\n" \
X_"a[12:13], v33 offset:9280\n" \
X_"a[14:15], v33 offset:13888\n" \
M_"v[184:187], a[16:19], a[108:111], v[184:187]\n" \
"buffer_load_dword v37, v40, s[56:59], s74 offen sc1\n" \
M_"v[192:195], a[20:23], a[220:223], v[192:195]\n" \
X_"a[16:17], v33 offset:18496\n" \
"s_waitcnt lgkmcnt(13)\n" \
M_"v[192:195], a[24:27], a[224:227], v[192:195]\n" \
X_"a[18:19], v33 offset:23104\n" \
M_"v[196:199], a[20:23], a[236:239], v[196:199]\n" \
X_"a[20:21], v33 offset:27712\n" \
C_"v184, v184, v185\n" \
C_"v185, v186, v187\n" \
C_"v186, v188, v189\n" \
C_"v187, v190, v191\n" \
"buffer_store_dwordx4 v[184:187], v20, s[56:59], s92 offen offset:1024 sc1\n" \
"s_waitcnt vmcnt(18)\n" \
"s_mov_b64 exec, 1\n" \
"buffer_store_dword v39, off, s[56:59], s70 offset:20 sc1\n" \
"s_mov_b64 exec, -1\n" \
"s_add_u32 s92, s92, 0x3800\n" \
M_"v[196:199], a[24:27], a[240:243], v[196:199]\n" \
X_"a[22:23], v33 offset:32320\n" \
X_"a[24:25], v33 offset:36928\n" \
M_"v[196:199], a[28:31], a[244:247], v[196:199]\n" \
X_"a[26:27], v33 offset:41536\n" \
M_"v[196:199], a[192:195], a[248:251], v[196:199]\n" \
M_"v[192:195], a[28:31], a[228:231], v[192:195]\n" \
X_"a[28:29], v33 offset:46144\n" \
X_"a[30:31], v33 offset:50752\n" \
M_"v[192:195], a[192:195], a[232:235], v[192:195]\n" \
B_"v[100:103], v20, s[56:59], s97 offen offset:1024 sc1\n" \
M_"v[192:195], a[196:199], a[160:163], v[192:195]\n" \
X_"a[192:193], v33 offset:55360\n" \
X_"a[194:195], v33 offset:59968\n" \
M_"v[192:195], a[200:203], a[164:167], v[192:195]\n" \
M_"v[196:199], a[196:199], a[176:179], v[196:199]\n" \
X_"a[196:197], v10 offset:64\n" \
X_"a[198:199], v10 offset:4672\n" \
M_"v[196:199], a[200:203], a[180:183], v[196:199]\n" \
X_"a[200:201], v34 offset:64\n" \
X_"a[202:203], v34 offset:4672\n" \
M_"v[196:199], a[204:207], a[184:187], v[196:199]\n" \
B_"v[104:107], v32, s[56:59], s97 offen offset:0 sc1\n" \
M_"v[196:199], a[208:211], a[188:191], v[196:199]\n" \
M_"v[192:195], a[204:207], a[168:171], v[192:195]\n" \
X_"a[204:205], v34 offset:9280\n" \
X_"a[206:207], v34 offset:13888\n" \
M_"v[192:195], a[208:211], a[172:175], v[192:195]\n" \
X_"a[208:209], v34 offset:18496\n" \
"s_waitcnt lgkmcnt(14)\n" \
M_"v[192:195], a[212:215], a[96:99], v[192:195]\n" \
X_"a[210:211], v34 offset:23104\n" \
M_"v[192:195], a[216:219], a[100:103], v[192:195]\n" \
B_"v[108:111], v32, s[56:59], s97 offen offset:1024 sc1\n" \
M_"v[196:199], a[212:215], a[112:115], v[196:199]\n" \
X_"a[212:213], v10 offset:9280\n" \
X_"a[214:215], v10 offset:13888\n" \
M_"v[196:199], a[216:219], a[116:119], v[196:199]\n" \
X_"a[216:217], v33 offset:96\n" \
X_"a[218:219], v33 offset:4704\n" \
"s_waitcnt vmcnt(19)\n" \
"s_mov_b64 exec, 1\n" \
"buffer_store_dword v39, off, s[56:59], s71 offset:24 sc1\n" \
"s_mov_b64 exec, -1\n" \
M_"v[196:199], a[0:3], a[120:123], v[196:199]\n" \
M_"v[196:199], a[4:7], a[124:127], v[196:199]\n" \
M_"v[192:195], a[0:3], a[104:107], v[192:195]\n" \
X_"a[0:1], v33 offset:9312\n" \
X_"a[2:3], v33 offset:13920\n" \
M_"v[192:195], a[4:7], a[108:111], v[192:195]\n" \
X_"a[4:5], v33 offset:18528\n" \
X_"a[6:7], v33 offset:23136\n" \
M_"v[200:203], a[8:11], a[220:223], v[200:203]\n" \
M_"v[200:203], a[12:15], a[224:227], v[200:203]\n" \
M_"v[204:207], a[8:11], a[236:239], v[204:207]\n" \
X_"a[8:9], v33 offset:27744\n" \
X_"a[10:11], v33 offset:32352\n" \
M_"v[204:207], a[12:15], a[240:243], v[204:207]\n" \
C_"v192, v192, v193\n" \
C_"v193, v194, v195\n" \
C_"v194, v196, v197\n" \
C_"v195, v198, v199\n" \
"buffer_store_dwordx4 v[192:195], v20, s[56:59], s66 offen offset:1024 sc1\n" \
M_"v[204:207], a[16:19], a[244:247], v[204:207]\n" \
X_"a[12:13], v33 offset:36960\n" \
"s_waitcnt lgkmcnt(11)\n" \
M_"v[204:207], a[20:23], a[248:251], v[204:207]\n" \
X_"a[14:15], v33 offset:41568\n" \
M_"v[200:203], a[16:19], a[228:231], v[200:203]\n" \
X_"a[16:17], v33 offset:46176\n" \
X_"a[18:19], v33 offset:50784\n" \
M_"v[200:203], a[20:23], a[232:235], v[200:203]\n" \
X_"a[20:21], v33 offset:55392\n" \
X_"a[22:23], v33 offset:60000\n" \
M_"v[200:203], a[24:27], a[160:163], v[200:203]\n" \
M_"v[200:203], a[28:31], a[164:167], v[200:203]\n" \
M_"v[204:207], a[24:27], a[176:179], v[204:207]\n" \
X_"a[24:25], v10 offset:96\n" \
X_"a[26:27], v10 offset:4704\n" \
M_"v[204:207], a[28:31], a[180:183], v[204:207]\n" \
X_"a[28:29], v34 offset:96\n" \
X_"a[30:31], v34 offset:4704\n" \
M_"v[204:207], a[192:195], a[184:187], v[204:207]\n" \
M_"v[204:207], a[196:199], a[188:191], v[204:207]\n" \
M_"v[200:203], a[192:195], a[168:171], v[200:203]\n" \
X_"a[192:193], v34 offset:9312\n" \
X_"a[194:195], v34 offset:13920\n" \
M_"v[200:203], a[196:199], a[172:175], v[200:203]\n" \
X_"a[196:197], v34 offset:18528\n" \
X_"a[198:199], v34 offset:23136\n" \
M_"v[200:203], a[200:203], a[96:99], v[200:203]\n" \
M_"v[200:203], a[204:207], a[100:103], v[200:203]\n" \
M_"v[204:207], a[200:203], a[112:115], v[204:207]\n" \
X_"a[200:201], v10 offset:9312\n" \
X_"a[202:203], v10 offset:13920\n" \
M_"v[204:207], a[204:207], a[116:119], v[204:207]\n" \
X_"a[204:205], v33 offset:128\n" \
X_"a[206:207], v33 offset:4736\n" \
M_"v[204:207], a[208:211], a[120:123], v[204:207]\n" \
"s_waitcnt lgkmcnt(15)\n" \
M_"v[204:207], a[212:215], a[124:127], v[204:207]\n" \
M_"v[200:203], a[208:211], a[104:107], v[200:203]\n" \
X_"a[208:209], v33 offset:9344\n" \
X_"a[210:211], v33 offset:13952\n" \
M_"v[200:203], a[212:215], a[108:111], v[200:203]\n" \
X_"a[212:213], v33 offset:18560\n" \
X_"a[214:215], v33 offset:23168\n" \
M_"v[208:211], a[216:219], a[220:223], v[208:211]\n" \
M_"v[208:211], a[0:3], a[224:227], v[208:211]\n" \
M_"v[212:215], a[216:219], a[236:239], v[212:215]\n" \
X_"a[216:217], v33 offset:27776\n" \
X_"a[218:219], v33 offset:32384\n" \
M_"v[212:215], a[0:3], a[240:243], v[212:215]\n" \
C_"v200, v200, v201\n" \
C_"v201, v202, v203\n" \
C_"v202, v204, v205\n" \
C_"v203, v206, v207\n" \
"buffer_store_dwordx4 v[200:203], v20, s[56:59], s67 offen offset:1024 sc1\n" \
M_"v[212:215], a[4:7], a[244:247], v[212:215]\n" \
X_"a[0:1], v33 offset:36992\n" \
X_"a[2:3], v33 offset:41600\n" \
M_"v[212:215], a[8:11], a[248:251], v[212:215]\n" \
M_"v[208:211], a[4:7], a[228:231], v[208:211]\n" \
X_"a[4:5], v33 offset:46208\n" \
X_"a[6:7], v33 offset:50816\n" \
M_"v[208:211], a[8:11], a[232:235], v[208:211]\n" \
X_"a[8:9], v33 offset:55424\n" \
X_"a[10:11], v33 offset:60032\n" \
M_"v[208:211], a[12:15], a[160:163], v[208:211]\n" \
"s_waitcnt lgkmcnt(13)\n" \
M_"v[208:211], a[16:19], a[164:167], v[208:211]\n" \
M_"v[212:215], a[12:15], a[176:179], v[212:215]\n" \
"s_waitcnt vmcnt(8)\n" \
"v_cmp_eq_u32_e64 s[98:99], s62, v37\n" \
"s_mov_b64 exec, 3\n" \
"buffer_store_dword v39, v41, s[56:59], s73 offen sc1\n" \
"s_mov_b64 exec, -1\n" \
"s_andn2_b32 s75, 0x7f, s98\n" \
"s_mov_b32 s20, s75\n" \
"s_bitcmp1_b32 s20, 0\n" \
"s_cselect_b32 s93, 0x40000000, s12\n" \
"s_bitcmp1_b32 s20, 1\n" \
"s_cselect_b32 s94, 0x40000000, s12\n" \
"s_bitcmp1_b32 s20, 2\n" \
"s_cselect_b32 s95, 0x40000000, s13\n" \
"s_bitcmp1_b32 s20, 3\n" \
"s_cselect_b32 s96, 0x40000000, s13\n" \
"s_bitcmp1_b32 s20, 4\n" \
"s_cselect_b32 s97, 0x40000000, s14\n" \
"s_bitcmp1_b32 s20, 5\n" \
"s_cselect_b32 s64, 0x40000000, s14\n" \
"s_bitcmp1_b32 s20, 6\n" \
"s_cselect_b32 s65, 0x40000000, s15\n" \
"s_add_u32 s66, s66, 0x3000\n" \
"s_add_u32 s67, s67, 0x2800\n" \
M_"v[212:215], a[16:19], a[180:183], v[212:215]\n" \
B_"v[116:119], v20, s[56:59], s93 offen offset:0 sc1\n" \
M_"v[212:215], a[20:23], a[184:187], v[212:215]\n" \
X_"a[12:13], v10 offset:128\n" \
X_"a[14:15], v10 offset:4736\n" \
M_"v[212:215], a[24:27], a[188:191], v[212:215]\n" \
X_"a[16:17], v34 offset:128\n" \
X_"a[18:19], v34 offset:4736\n" \
M_"v[208:211], a[20:23], a[168:171], v[208:211]\n" \
X_"a[20:21], v34 offset:9344\n" \
X_"a[22:23], v34 offset:13952\n" \
M_"v[208:211], a[24:27], a[172:175], v[208:211]\n" \
X_"a[24:25], v34 offset:18560\n" \
X_"a[26:27], v34 offset:23168\n" \
M_"v[208:211], a[28:31], a[96:99], v[208:211]\n" \
M_"v[208:211], a[192:195], a[100:103], v[208:211]\n" \
M_"v[212:215], a[28:31], a[112:115], v[212:215]\n" \
X_"a[28:29], v10 offset:9344\n" \
X_"a[30:31], v10 offset:13952\n" \
M_"v[212:215], a[192:195], a[116:119], v[212:215]\n" \
X_"a[192:193], v33 offset:160\n" \
X_"a[194:195], v33 offset:4768\n" \
M_"v[212:215], a[196:199], a[120:123], v[212:215]\n" \
M_"v[212:215], a[200:203], a[124:127], v[212:215]\n" \
M_"v[208:211], a[196:199], a[104:107], v[208:211]\n" \
X_"a[196:197], v33 offset:9376\n" \
X_"a[198:199], v33 offset:13984\n" \
M_"v[208:211], a[200:203], a[108:111], v[208:211]\n" \
X_"a[200:201], v33 offset:18592\n" \
"s_waitcnt lgkmcnt(15)\n" \
M_"v[216:219], a[204:207], a[220:223], v[216:219]\n" \
X_"a[202:203], v33 offset:23200\n" \
M_"v[216:219], a[208:211], a[224:227], v[216:219]\n" \
B_"v[124:127], v20, s[56:59], s94 offen offset:2048 sc1\n" \
M_"v[220:223], a[204:207], a[236:239], v[220:223]\n" \
X_"a[204:205], v33 offset:27808\n" \
C_"v208, v208, v209\n" \
C_"v209, v210, v211\n" \
C_"v210, v212, v213\n" \
C_"v211, v214, v215\n" \
M_"v[220:223], a[208:211], a[240:243], v[220:223]\n" \
"buffer_store_dwordx4 v[208:211], v20, s[56:59], s68 offen offset:1024 sc1\n" \
"s_add_u32 s68, s68, 0x2000\n" \
M_"v[220:223], a[212:215], a[244:247], v[220:223]\n" \
X_"a[206:207], v33 offset:32416\n" \
X_"a[208:209], v33 offset:37024\n" \
M_"v[220:223], a[216:219], a[248:251], v[220:223]\n" \
X_"a[210:211], v33 offset:41632\n" \
M_"v[216:219], a[212:215], a[228:231], v[216:219]\n" \
X_"a[212:213], v33 offset:46240\n" \
X_"a[214:215], v33 offset:50848\n" \
M_"v[216:219], a[216:219], a[232:235], v[216:219]\n" \
X_"a[216:217], v33 offset:55456\n" \
X_"a[218:219], v33 offset:60064\n" \
M_"v[216:219], a[0:3], a[160:163], v[216:219]\n" \
M_"v[216:219], a[4:7], a[164:167], v[216:219]\n" \
M_"v[220:223], a[0:3], a[176:179], v[220:223]\n" \
X_"a[0:1], v10 offset:160\n" \
X_"a[2:3], v10 offset:4768\n" \
M_"v[220:223], a[4:7], a[180:183], v[220:223]\n" \
X_"a[4:5], v34 offset:160\n" \
X_"a[6:7], v34 offset:4768\n" \
M_"v[220:223], a[8:11], a[184:187], v[220:223]\n" \
"s_waitcnt lgkmcnt(13)\n" \
M_"v[220:223], a[12:15], a[188:191], v[220:223]\n" \
"s_waitcnt vmcnt(11)\n" \
"s_mov_b64 exec, 1\n" \
"buffer_store_dword v39, off, s[56:59], s92 offset:32 sc1\n" \
"s_mov_b64 exec, -1\n" \
M_"v[216:219], a[8:11], a[168:171], v[216:219]\n" \
X_"a[8:9], v34 offset:9376\n" \
X_"a[10:11], v34 offset:13984\n" \
M_"v[216:219], a[12:15], a[172:175], v[216:219]\n" \
X_"a[12:13], v34 offset:18592\n" \
X_"a[14:15], v34 offset:23200\n" \
M_"v[216:219], a[16:19], a[96:99], v[216:219]\n" \
M_"v[216:219], a[20:23], a[100:103], v[216:219]\n" \
B_"v[132:135], v20, s[56:59], s95 offen offset:0 sc1\n" \
M_"v[220:223], a[16:19], a[112:115], v[220:223]\n" \
X_"a[16:17], v10 offset:9376\n" \
X_"a[18:19], v10 offset:13984\n" \
M_"v[220:223], a[20:23], a[116:119], v[220:223]\n" \
X_"a[20:21], v33 offset:192\n" \
X_"a[22:23], v33 offset:4800\n" \
M_"v[220:223], a[24:27], a[120:123], v[220:223]\n" \
M_"v[220:223], a[28:31], a[124:127], v[220:223]\n" \
M_"v[216:219], a[24:27], a[104:107], v[216:219]\n" \
X_"a[24:25], v33 offset:9408\n" \
X_"a[26:27], v33 offset:14016\n" \
M_"v[216:219], a[28:31], a[108:111], v[216:219]\n" \
"buffer_load_dword v37, v40, s[56:59], s74 offen sc1\n" \
M_"v[224:227], a[192:195], a[220:223], v[224:227]\n" \
X_"a[28:29], v33 offset:18624\n" \
X_"a[30:31], v33 offset:23232\n" \
M_"v[224:227], a[196:199], a[224:227], v[224:227]\n" \
M_"v[228:231], a[192:195], a[236:239], v[228:231]\n" \
X_"a[192:193], v33 offset:27840\n" \
X_"a[194:195], v33 offset:32448\n" \
C_"v216, v216, v217\n" \
C_"v217, v218, v219\n" \
C_"v218, v220, v221\n" \
C_"v219, v222, v223\n" \
"buffer_store_dwordx4 v[216:219], v20, s[56:59], s69 offen offset:1024 sc1\n" \
"s_add_u32 s69, s69, 0x1800\n" \
M_"v[228:231], a[196:199], a[240:243], v[228:231]\n" \
X_"a[196:197], v33 offset:37056\n" \
"s_waitcnt lgkmcnt(12)\n" \
M_"v[228:231], a[200:203], a[244:247], v[228:231]\n" \
X_"a[198:199], v33 offset:41664\n" \
M_"v[228:231], a[204:207], a[248:251], v[228:231]\n" \
M_"v[224:227], a[200:203], a[228:231], v[224:227]\n" \
B_"v[140:143], v20, s[56:59], s96 offen offset:2048 sc1\n" \
M_"v[224:227], a[204:207], a[232:235], v[224:227]\n" \
X_"a[200:201], v33 offset:46272\n" \
X_"a[202:203], v33 offset:50880\n" \
M_"v[224:227], a[208:211], a[160:163], v[224:227]\n" \
X_"a[204:205], v33 offset:55488\n" \
X_"a[206:207], v33 offset:60096\n" \
M_"v[224:227], a[212:215], a[164:167], v[224:227]\n" \
M_"v[228:231], a[208:211], a[176:179], v[228:231]\n" \
X_"a[208:209], v10 offset:192\n" \
X_"a[210:211], v10 offset:4800\n" \
M_"v[228:231], a[212:215], a[180:183], v[228:231]\n" \
X_"a[212:213], v34 offset:192\n" \
X_"a[214:215], v34 offset:4800\n" \
M_"v[228:231], a[216:219], a[184:187], v[228:231]\n" \
M_"v[228:231], a[0:3], a[188:191], v[228:231]\n" \
M_"v[224:227], a[216:219], a[168:171], v[224:227]\n" \
X_"a[216:217], v34 offset:9408\n" \
X_"a[218:219], v34 offset:14016\n" \
M_"v[224:227], a[0:3], a[172:175], v[224:227]\n" \
X_"a[0:1], v34 offset:18624\n" \
X_"a[2:3], v34 offset:23232\n" \
"s_waitcnt vmcnt(10)\n" \
"s_mov_b64 exec, 1\n" \
"buffer_store_dword v39, off, s[56:59], s66 offset:36 sc1\n" \
"s_mov_b64 exec, -1\n" \
M_"v[224:227], a[4:7], a[96:99], v[224:227]\n" \
M_"v[224:227], a[8:11], a[100:103], v[224:227]\n" \
M_"v[228:231], a[4:7], a[112:115], v[228:231]\n" \
X_"a[4:5], v10 offset:9408\n" \
X_"a[6:7], v10 offset:14016\n" \
M_"v[228:231], a[8:11], a[116:119], v[228:231]\n" \
X_"a[8:9], v33 offset:224\n" \
"s_waitcnt lgkmcnt(15)\n" \
M_"v[228:231], a[12:15], a[120:123], v[228:231]\n" \
B_"v[148:151], v20, s[56:59], s97 offen offset:0 sc1\n" \
M_"v[228:231], a[16:19], a[124:127], v[228:231]\n" \
X_"a[10:11], v33 offset:4832\n" \
M_"v[224:227], a[12:15], a[104:107], v[224:227]\n" \
X_"a[12:13], v33 offset:9440\n" \
X_"a[14:15], v33 offset:14048\n" \
M_"v[224:227], a[16:19], a[108:111], v[224:227]\n" \
X_"a[16:17], v33 offset:18656\n" \
X_"a[18:19], v33 offset:23264\n" \
M_"v[232:235], a[20:23], a[220:223], v[232:235]\n" \
M_"v[232:235], a[24:27], a[224:227], v[232:235]\n" \
M_"v[236:239], a[20:23], a[236:239], v[236:239]\n" \
X_"a[20:21], v33 offset:27872\n" \
X_"a[22:23], v33 offset:32480\n" \
M_"v[236:239], a[24:27], a[240:243], v[236:239]\n" \
C_"v224, v224, v225\n" \
C_"v225, v226, v227\n" \
C_"v226, v228, v229\n" \
C_"v227, v230, v231\n" \
M_"v[236:239], a[28:31], a[244:247], v[236:239]\n" \
X_"a[24:25], v33 offset:37088\n" \
X_"a[26:27], v33 offset:41696\n" \
M_"v[236:239], a[192:195], a[248:251], v[236:239]\n" \
M_"v[232:235], a[28:31], a[228:231], v[232:235]\n" \
X_"a[28:29], v33 offset:46304\n" \
X_"a[30:31], v33 offset:50912\n" \
M_"v[232:235], a[192:195], a[232:235], v[232:235]\n" \
X_"a[192:193], v33 offset:55520\n" \
X_"a[194:195], v33 offset:60128\n" \
M_"v[232:235], a[196:199], a[160:163], v[232:235]\n" \
"s_waitcnt lgkmcnt(14)\n" \
M_"v[232:235], a[200:203], a[164:167], v[232:235]\n" \
B_"v[156:159], v20, s[56:59], s64 offen offset:2048 sc1\n" \
"v_readlane_b32 s64, v11, 54\n" \
M_"v[236:239], a[196:199], a[176:179], v[236:239]\n" \
X_"a[196:197], v10 offset:224\n" \
X_"a[198:199], v10 offset:4832\n" \
M_"v[236:239], a[200:203], a[180:183], v[236:239]\n" \
X_"a[200:201], v34 offset:224\n" \
X_"a[202:203], v34 offset:4832\n" \
"buffer_store_dwordx4 v[224:227], v20, s[56:59], s64 offen offset:1024 sc1\n" \
M_"v[236:239], a[204:207], a[184:187], v[236:239]\n" \
M_"v[236:239], a[208:211], a[188:191], v[236:239]\n" \
M_"v[232:235], a[204:207], a[168:171], v[232:235]\n" \
X_"a[204:205], v34 offset:9440\n" \
X_"a[206:207], v34 offset:14048\n" \
"s_waitcnt vmcnt(13)\n" \
"s_mov_b64 exec, 1\n" \
"buffer_store_dword v39, off, s[56:59], s67 offset:40 sc1\n" \
"s_mov_b64 exec, -1\n" \
M_"v[232:235], a[208:211], a[172:175], v[232:235]\n" \
X_"a[208:209], v34 offset:18656\n" \
X_"a[210:211], v34 offset:23264\n" \
M_"v[232:235], a[212:215], a[96:99], v[232:235]\n" \
M_"v[232:235], a[216:219], a[100:103], v[232:235]\n" \
M_"v[236:239], a[212:215], a[112:115], v[236:239]\n" \
X_"a[212:213], v10 offset:9440\n" \
X_"a[214:215], v10 offset:14048\n" \
M_"v[236:239], a[216:219], a[116:119], v[236:239]\n" \
X_"a[216:217], v36 offset:0\n" \
X_"a[218:219], v36 offset:1024\n" \
M_"v[236:239], a[0:3], a[120:123], v[236:239]\n" \
M_"v[236:239], a[4:7], a[124:127], v[236:239]\n" \
M_"v[232:235], a[0:3], a[104:107], v[232:235]\n" \
X_"a[0:1], v36 offset:2048\n" \
X_"a[2:3], v36 offset:3072\n" \
M_"v[232:235], a[4:7], a[108:111], v[232:235]\n" \
X_"a[4:5], v36 offset:32\n" \
B_"v[164:167], v20, s[56:59], s65 offen offset:0 sc1\n" \
"v_readlane_b32 s65, v11, 55\n" \
"s_waitcnt lgkmcnt(11)\n" \
M_"v[240:243], a[8:11], a[220:223], v[240:243]\n" \
X_"a[6:7], v36 offset:1056\n" \
M_"v[240:243], a[12:15], a[224:227], v[240:243]\n" \
M_"v[244:247], a[8:11], a[236:239], v[244:247]\n" \
C_"v232, v232, v233\n" \
C_"v233, v234, v235\n" \
C_"v234, v236, v237\n" \
C_"v235, v238, v239\n" \
"buffer_store_dwordx4 v[232:235], v20, s[56:59], s65 offen offset:1024 sc1\n" \
M_"v[244:247], a[12:15], a[240:243], v[244:247]\n" \
X_"a[8:9], v36 offset:2080\n" \
X_"a[10:11], v36 offset:3104\n" \
M_"v[244:247], a[16:19], a[244:247], v[244:247]\n" \
M_"v[244:247], a[20:23], a[248:251], v[244:247]\n" \
M_"v[240:243], a[16:19], a[228:231], v[240:243]\n" \
M_"v[240:243], a[20:23], a[232:235], v[240:243]\n" \
M_"v[168:171], a[24:27], a[128:131], v[168:171]\n" \
M_"v[168:171], a[28:31], a[132:135], v[168:171]\n" \
M_"v[172:175], a[24:27], a[144:147], v[172:175]\n" \
"s_waitcnt vmcnt(9)\n" \
"v_cmp_eq_u32_e64 s[98:99], s62, v37\n" \
"buffer_load_dword v37, v40, s[56:59], s74 offen sc1\n" \
"s_mov_b64 exec, 1\n" \
"buffer_store_dword v39, off, s[56:59], s68 offset:44 sc1\n" \
"s_mov_b64 exec, -1\n" \
"s_or_b32 s75, 0x300, s100\n" \
"s_andn2_b32 s75, s75, s98\n" \
"s_mov_b32 s21, s75\n" \
"s_bitcmp1_b32 s21, 16\n" \
"s_cselect_b32 s70, 0x40000000, s16\n" \
"s_bitcmp1_b32 s21, 17\n" \
"s_cselect_b32 s71, 0x40000000, s16\n" \
"s_bitcmp1_b32 s21, 18\n" \
"s_cselect_b32 s72, 0x40000000, s17\n" \
"s_bitcmp1_b32 s21, 19\n" \
"s_cselect_b32 s73, 0x40000000, s17\n" \
"s_bitcmp1_b32 s21, 20\n" \
"s_cselect_b32 s92, 0x40000000, s18\n" \
"s_bitcmp1_b32 s21, 21\n" \
"s_cselect_b32 s93, 0x40000000, s18\n" \
"s_bitcmp1_b32 s21, 22\n" \
"s_cselect_b32 s94, 0x40000000, s19\n" \
"s_bitcmp1_b32 s21, 23\n" \
"s_cselect_b32 s95, 0x40000000, s19\n" \
"s_bitcmp1_b32 s21, 8\n" \
"s_cselect_b32 s96, 0x40000000, s12\n" \
"s_bitcmp1_b32 s21, 9\n" \
"s_cselect_b32 s97, 0x40000000, s12\n" \
"s_add_u32 s64, s64, 0x1000\n" \
"s_add_u32 s65, s65, 0x800\n" \
M_"v[172:175], a[28:31], a[148:151], v[172:175]\n" \
B_"v[112:115], v20, s[56:59], s70 offen offset:0 sc1\n" \
M_"v[240:243], a[24:27], a[160:163], v[240:243]\n" \
M_"v[240:243], a[28:31], a[164:167], v[240:243]\n" \
M_"v[244:247], a[24:27], a[176:179], v[244:247]\n" \
M_"v[244:247], a[28:31], a[180:183], v[244:247]\n" \
B_"v[184:187], v20, s[56:59], s71 offen offset:2048 sc1\n" \
M_"v[244:247], a[192:195], a[184:187], v[244:247]\n" \
"v_readlane_b32 s71, v11, 57\n" \
M_"v[244:247], a[196:199], a[188:191], v[244:247]\n" \
M_"v[240:243], a[192:195], a[168:171], v[240:243]\n" \
M_"v[240:243], a[196:199], a[172:175], v[240:243]\n" \
B_"v[120:123], v20, s[56:59], s72 offen offset:0 sc1\n" \
M_"v[172:175], a[192:195], a[152:155], v[172:175]\n" \
"v_readlane_b32 s72, v11, 48\n" \
M_"v[172:175], a[196:199], a[156:159], v[172:175]\n" \
M_"v[168:171], a[192:195], a[136:139], v[168:171]\n" \
M_"v[168:171], a[196:199], a[140:143], v[168:171]\n" \
B_"v[192:195], v20, s[56:59], s73 offen offset:2048 sc1\n" \
M_"v[168:171], a[200:203], a[32:35], v[168:171]\n" \
"v_readlane_b32 s73, v11, 46\n" \
"s_waitcnt lgkmcnt(0)\n" \
M_"v[168:171], a[204:207], a[36:39], v[168:171]\n" \
M_"v[172:175], a[200:203], a[48:51], v[172:175]\n" \
M_"v[172:175], a[204:207], a[52:55], v[172:175]\n" \
B_"v[128:131], v20, s[56:59], s92 offen offset:0 sc1\n" \
M_"v[240:243], a[200:203], a[96:99], v[240:243]\n" \
"v_readlane_b32 s92, v11, 47\n" \
M_"v[240:243], a[204:207], a[100:103], v[240:243]\n" \
M_"v[244:247], a[200:203], a[112:115], v[244:247]\n" \
M_"v[244:247], a[204:207], a[116:119], v[244:247]\n" \
B_"v[200:203], v20, s[56:59], s93 offen offset:2048 sc1\n" \
M_"v[244:247], a[208:211], a[120:123], v[244:247]\n" \
M_"v[244:247], a[212:215], a[124:127], v[244:247]\n" \
M_"v[240:243], a[208:211], a[104:107], v[240:243]\n" \
M_"v[240:243], a[212:215], a[108:111], v[240:243]\n" \
".fill (L_ab + 45056 - .) / 4, 4, 0xBF800000\n" \
"L_ap11:\n" \
"s_cbranch_execz L_ap0\n" \
B_"v[176:179], v20, s[56:59], s94 offen offset:0 sc1\n" \
"s_waitcnt vmcnt(17)\n" \
"s_mov_b64 exec, 1\n" \
"buffer_store_dword v39, off, s[56:59], s69 offset:48 sc1\n" \
"s_mov_b64 exec, -1\n" \
M_"v[172:175], a[208:211], a[56:59], v[172:175]\n" \
M_"v[172:175], a[212:215], a[60:63], v[172:175]\n" \
M_"v[168:171], a[208:211], a[40:43], v[168:171]\n" \
M_"v[168:171], a[212:215], a[44:47], v[168:171]\n" \
B_"v[248:251], v20, s[56:59], s95 offen offset:2048 sc1\n" \
M_"v[168:171], a[216:219], v[64:67], v[168:171]\n" \
M_"v[172:175], a[216:219], v[72:75], v[172:175]\n" \
M_"v[240:243], a[216:219], v[80:83], v[240:243]\n" \
M_"v[244:247], a[216:219], v[88:91], v[244:247]\n" \
B_"v[188:191], v20, s[56:59], s96 offen offset:1024 sc1\n" \
M_"v[168:171], a[0:3], v[68:71], v[168:171]\n" \
M_"v[172:175], a[0:3], v[76:79], v[172:175]\n" \
M_"v[240:243], a[0:3], v[84:87], v[240:243]\n" \
M_"v[244:247], a[0:3], v[92:95], v[244:247]\n" \
B_"v[196:199], v20, s[56:59], s97 offen offset:3072 sc1\n" \
M_"v[56:59], a[4:7], v[96:99], 0\n" \
M_"v[60:63], a[4:7], v[104:107], 0\n" \
M_"v[56:59], a[8:11], v[100:103], v[56:59]\n" \
M_"v[60:63], a[8:11], v[108:111], v[60:63]\n" \
"s_waitcnt vmcnt(16)\n" \
"s_mov_b64 exec, 1\n" \
"buffer_store_dword v39, off, s[56:59], s64 offset:52 sc1\n" \
"s_mov_b64 exec, -1\n" \
"s_waitcnt vmcnt(14)\n" \
"s_mov_b64 exec, 1\n" \
"buffer_store_dword v39, off, s[56:59], s65 offset:56 sc1\n" \
"s_mov_b64 exec, -1\n" \
"s_cmp_lg_u32 s20, 0\n" \
"s_cbranch_scc1 L_sna_%=\n" \
"L_rna_%=:\n" \
"s_cmp_lg_u32 s21, 0\n" \
"s_cbranch_scc1 L_snx_%=\n" \
"L_rnx_%=:\n" \
"s_waitcnt vmcnt(14)\n" \
"v_cmp_eq_u32_e64 s[98:99], s62, v37\n" \
M_"v[168:171], v[50:53], v[116:119], v[168:171]\n" \
"s_andn2_b32 s75, 0x400, s98\n" \
"s_cbranch_scc1 L_sx0_%=\n" \
"L_rx0_%=:\n" \
"s_add_u32 s66, s63, 0x1000\n" \
M_"v[172:175], v[48:51], v[116:119], v[172:175]\n" \
B_"v[204:207], v20, s[56:59], s66 offen offset:1024 sc1\n" \
"s_andn2_b32 s75, 0x800, s98\n" \
"s_cbranch_scc1 L_sx1_%=\n" \
"L_rx1_%=:\n" \
"s_add_u32 s67, s63, 0x1800\n" \
M_"v[168:171], v[50:53], v[124:127], v[168:171]\n" \
M_"v[172:175], v[48:51], v[124:127], v[172:175]\n" \
M_"v[168:171], v[50:53], v[132:135], v[168:171]\n" \
B_"v[212:215], v20, s[56:59], s67 offen offset:1024 sc1\n" \
"s_andn2_b32 s75, 0x1000, s98\n" \
"s_cbranch_scc1 L_sx2_%=\n" \
"L_rx2_%=:\n" \
"s_add_u32 s68, s63, 0x2000\n" \
M_"v[172:175], v[48:51], v[132:135], v[172:175]\n" \
M_"v[168:171], v[50:53], v[140:143], v[168:171]\n" \
M_"v[172:175], v[48:51], v[140:143], v[172:175]\n" \
B_"v[220:223], v20, s[56:59], s68 offen offset:1024 sc1\n" \
"s_andn2_b32 s75, 0x2000, s98\n" \
"s_cbranch_scc1 L_sx3_%=\n" \
"L_rx3_%=:\n" \
"s_add_u32 s69, s63, 0x2800\n" \
M_"v[168:171], v[50:53], v[148:151], v[168:171]\n" \
M_"v[172:175], v[48:51], v[148:151], v[172:175]\n" \
M_"v[168:171], v[50:53], v[156:159], v[168:171]\n" \
B_"v[228:231], v20, s[56:59], s69 offen offset:1024 sc1\n" \
"s_andn2_b32 s75, 0x4000, s98\n" \
"s_cbranch_scc1 L_sx4_%=\n" \
"L_rx4_%=:\n" \
"s_add_u32 s70, s63, 0x3000\n" \
M_"v[172:175], v[48:51], v[156:159], v[172:175]\n" \
M_"v[168:171], v[50:53], v[164:167], v[168:171]\n" \
M_"v[172:175], v[48:51], v[164:167], v[172:175]\n" \
B_"v[236:239], v20, s[56:59], s70 offen offset:1024 sc1\n" \
"s_waitcnt vmcnt(17)\n" \
M_"v[56:59], v[50:53], v[112:115], v[56:59]\n" \
M_"v[60:63], v[48:51], v[112:115], v[60:63]\n" \
"s_waitcnt vmcnt(16)\n" \
M_"v[56:59], v[50:53], v[184:187], v[56:59]\n" \
M_"v[60:63], v[48:51], v[184:187], v[60:63]\n" \
C_"v168, v168, v169\n" \
"s_waitcnt vmcnt(15)\n" \
M_"v[56:59], v[50:53], v[120:123], v[56:59]\n" \
C_"v172, v172, v173\n" \
C_"v169, v170, v171\n" \
M_"v[60:63], v[48:51], v[120:123], v[60:63]\n" \
C_"v173, v174, v175\n" \
"s_waitcnt vmcnt(14)\n" \
M_"v[56:59], v[50:53], v[192:195], v[56:59]\n" \
"buffer_store_dwordx2 v[168:169], v42, s[88:91], s73 offen sc1\n" \
M_"v[60:63], v[48:51], v[192:195], v[60:63]\n" \
"s_waitcnt vmcnt(14)\n" \
M_"v[56:59], v[50:53], v[128:131], v[56:59]\n" \
"buffer_store_dwordx2 v[172:173], v43, s[88:91], s73 offen sc1\n" \
M_"v[60:63], v[48:51], v[128:131], v[60:63]\n" \
"s_waitcnt vmcnt(14)\n" \
M_"v[56:59], v[50:53], v[200:203], v[56:59]\n" \
M_"v[60:63], v[48:51], v[200:203], v[60:63]\n" \
"s_waitcnt vmcnt(13)\n" \
M_"v[56:59], v[50:53], v[176:179], v[56:59]\n" \
M_"v[60:63], v[48:51], v[176:179], v[60:63]\n" \
"s_waitcnt vmcnt(11)\n" \
M_"v[56:59], v[50:53], v[248:251], v[56:59]\n" \
M_"v[60:63], v[48:51], v[248:251], v[60:63]\n" \
"s_waitcnt vmcnt(10)\n" \
M_"v[240:243], v[50:53], v[188:191], v[240:243]\n" \
M_"v[244:247], v[48:51], v[188:191], v[244:247]\n" \
"s_waitcnt vmcnt(9)\n" \
M_"v[240:243], v[50:53], v[196:199], v[240:243]\n" \
M_"v[244:247], v[48:51], v[196:199], v[244:247]\n" \
"s_mov_b64 exec, 1\n" \
"buffer_store_dword v46, off, s[56:59], s71 offset:0 sc1\n" \
"s_mov_b64 exec, -1\n" \
C_"v56, v56, v57\n" \
C_"v60, v60, v61\n" \
C_"v57, v58, v59\n" \
C_"v61, v62, v63\n" \
"buffer_store_dwordx2 v[56:57], v42, s[88:91], s72 offen sc1\n" \
"buffer_store_dwordx2 v[60:61], v43, s[88:91], s72 offen sc1\n" \
"s_waitcnt vmcnt(9)\n" \
M_"v[240:243], v[50:53], v[204:207], v[240:243]\n" \
M_"v[244:247], v[48:51], v[204:207], v[244:247]\n" \
"s_waitcnt vmcnt(8)\n" \
M_"v[240:243], v[50:53], v[212:215], v[240:243]\n" \
M_"v[244:247], v[48:51], v[212:215], v[244:247]\n" \
"s_waitcnt vmcnt(7)\n" \
M_"v[240:243], v[50:53], v[220:223], v[240:243]\n" \
M_"v[244:247], v[48:51], v[220:223], v[244:247]\n" \
"s_waitcnt vmcnt(6)\n" \
M_"v[240:243], v[50:53], v[228:231], v[240:243]\n" \
M_"v[244:247], v[48:51], v[228:231], v[244:247]\n" \
"s_waitcnt vmcnt(5)\n" \
M_"v[240:243], v[50:53], v[236:239], v[240:243]\n" \
M_"v[244:247], v[48:51], v[236:239], v[244:247]\n" \
"s_nop 6\n" \
C_"v240, v240, v241\n" \
C_"v244, v244, v245\n" \
C_"v241, v242, v243\n" \
C_"v245, v246, v247\n" \
"buffer_store_dwordx2 v[240:241], v42, s[88:91], s92 offen sc1\n" \
"buffer_store_dwordx2 v[244:245], v43, s[88:91], s92 offen sc1\n" \
"L_fin_%=:\n"
#define ATT_VCLOB "v21","v22","v23","v24","v25","v26","v27","v28","v29","v32","v33","v34","v35","v36","v37","v38","v39","v40","v41","v42","v43","v44","v45","v46","v47","v48","v49","v50","v51","v52","v53","v54","v55","v56","v57","v58","v59","v60","v61","v62","v63","v64","v65","v66","v67","v68","v69","v70","v71","v72","v73","v74","v75","v76","v77","v78","v79","v80","v81","v82","v83","v84","v85","v86","v87","v88","v89","v90","v91","v92","v93","v94","v95","v96","v97","v98","v99","v100","v101","v102","v103","v104","v105","v106","v107","v108","v109","v110","v111","v112","v113","v114","v115","v116","v117","v118","v119","v120","v121","v122","v123","v124","v125","v126","v127","v128","v129","v130","v131","v132","v133","v134","v135","v136","v137","v138","v139","v140","v141","v142","v143","v144","v145","v146","v147","v148","v149","v150","v151","v152","v153","v154","v155","v156","v157","v158","v159","v160","v161","v162","v163","v164","v165","v166","v167","v168","v169","v170","v171","v172","v173","v174","v175","v176","v177","v178","v179","v180","v181","v182","v183","v184","v185","v186","v187","v188","v189","v190","v191","v192","v193","v194","v195","v196","v197","v198","v199","v200","v201","v202","v203","v204","v205","v206","v207","v208","v209","v210","v211","v212","v213","v214","v215","v216","v217","v218","v219","v220","v221","v222","v223","v224","v225","v226","v227","v228","v229","v230","v231","v232","v233","v234","v235","v236","v237","v238","v239","v240","v241","v242","v243","v244","v245","v246","v247","v248","v249","v250","v251","v252","v253","v254","v255"
#define ATT_SCLOB "s63","s64","s65","s66","s67","s68","s69","s70","s71","s72","s73","s74","s75","s76","s77","s78","s79","s80","s81","s82","s83","s84","s85","s86","s87","s88","s89","s90","s91","s92","s93","s95","s96","s97","s98","s99","s100","s101"
#define ATT_WTCH 0
#define ATT_PEEL 1
#define ATT_PEEL_FOW 1
#define ATT_PEEL_PRE 0
#define ATT_PEEL_ODD(b) (0u)
#define ATT_PAGES 11
#define ATT_TAILB 968
enum { T_fo, T_fj, T_fa, T_fas, T_fb, T_fbs, T_f8, T_mk8, T_wt, T_wol, T_woh, T_wbl, T_wbh, T_outl, T_outh, T_r1l, T_r1h, T_r1n, T_r1s, T_r2n, T_r2s, T_m2b, T_m0, T_m1, T_m1b, T_mw, T_m3, T_m4, T_ml, T_w0, T_w1, T_w2, T_w3, T_A1, T_A2, T_A3, T_B1, T_B2, T_B3, T_Ao, T_Bo, T_Al, T_Bl, T_l8, T_xr, T_x8r, T_oA, T_oB, T_o8, T_x0, T_x1, T_x2, T_x3, T_x4, T_x5, T_x6, T_x8, T_ep };

__device__ __forceinline__ void out_consts(u32x4 (&vp)[3], const u16* wo, const u16* wob, const u16* out, const unsigned b, const unsigned w, const unsigned lane) {
    const unsigned sp = b & 7u, nt = (b >> 3) & 3u, j = b >> 5, head = 4u * sp + nt, OOR = 0x40000000u;
    unsigned vw[9];
#pragma unroll
    for (unsigned i = 0; i < 9u; ++i) {
        const unsigned q = 64u * i + lane, row = __umul24(q, 3641u) >> 16, c = q - __umul24(row, 18u);
        vw[i] = __umul24(row, DM * 2) + (c < 16u ? (c * 16u + sp * 32u + 32u) & 255u : c * 16u) - (i & 3u) * 1024u;
    }
    const unsigned row = 16u * w + (lane >> 2), r9 = __umul24(16u * (row >> 5) + (row & 12u) + 2u * ((row >> 4) & 1u) + ((row >> 1) & 1u), 18u) + 16u + (row & 1u);
    unsigned vwl = __umul24(r9, DM * 2) + ((lane & 2u) ? 256u : sp * 32u) + (lane & 1u) * 16u, vrd = __umul24(lane >> 2, 288u) + (lane & 3u) * 8u;
    unsigned tab = 0;
#define TB(n, v) asm("v_writelane_b32 %0, %1, %2" : "+v"(tab) : "s"((unsigned)(v)), "n"((int)T_##n))
    const unsigned FJ = (unsigned)WS_F + (F_J + 640u * j) * 4u, mt = 2u * (w & 2u) + (w & 1u), w2 = w < 2u ? w : 2u, hw = head * 147456u;
    const unsigned FR = (unsigned)WS_F + F_R * 4u + (8u * j + sp) * 1024u;
    TB(fo, FJ + (ATT_PEEL_FOW ? 16u * head + 4u * w : 4u * head)); TB(fj, FJ); TB(fa, FR + (w | 2u) * 256u); TB(fas, FR + w * 256u + nt * 4u);
    TB(fb, FR + 128u + w * 256u); TB(fbs, FR + 128u + w * 256u + nt * 4u);
    TB(ep, ssel(b + w, OOR, (unsigned)WS_F + F_EPOCH * 4u));
    const u16* pwo = wo + nt * 144u; const u16* pwb = wob + nt * 144u;
    TB(wol, PLO(pwo)); TB(woh, PHI(pwo) & 0xffffu); TB(wbl, PLO(pwb)); TB(wbh, PHI(pwb) & 0xffffu); TB(outl, PLO(out)); TB(outh, PHI(out) & 0xffffu);
    const unsigned odd = ATT_PEEL_ODD(b), wo1 = w + 3u * odd;
#define OSEL(a, b_) (ATT_PEEL_ODD(1u) ? ssel(odd, a, b_) : (b_))
#define WU(u) (((4u * sp + ((nt + 1u + ((u) >> 2)) & 3u)) * 4u + ((u) & 3u)) * 36864u)
    TB(r1l, sslt<3>(wo1, PLO(pwo), PLO(pwb))); TB(r1h, sslt<3>(wo1, PHI(pwo), PHI(pwb)) & 0xffffu);
    TB(r1n, sslt<3>(wo1, 72u * sp + 71u, 4095u) * 1152u + 288u); TB(r1s, sslt<3>(wo1, (72u * sp + 32u * w) * 1152u, ATT_PEEL_ODD(1u) ? sslt<2>(w, WU(w), sslt<3>(w, OOR, hw + 110592u)) : hw + 110592u));
    if (ATT_PEEL_ODD(1u)) { TB(r2n, (72u * sp + 143u) * 1152u + 288u); TB(r2s, sslt<3>(w, (72u * sp + 72u + 32u * w) * 1152u, OOR)); TB(m2b, sslt<2>(w, 41984u + 9216u * w, 32768u)); }
    TB(m0, 131072u + 9216u * w2); TB(w0, hw + 36864u * w2); TB(m1, sslt<3>(w, 41984u + 9216u * w, 32768u)); TB(m1b, ssel(w ^ 2u, sslt<3>(w, 41984u + 9216u * w, 32768u), 114688u - 4096u));
    TB(mw, 65536u + 9216u * w); TB(m3, sslt<3>(w, 102400u + 9216u * w, 0u)); TB(m4, OSEL(9216u + 13312u * (w & 1u), sslt<3>(w, 131072u + 9216u * w, 9216u))); TB(ml, 18432u + 1024u * w);
    const unsigned tA = (unsigned)WS_AF + (8u * j + mt) * (KS_TOT * 1024u), tB = (unsigned)WS_AF + (120u - 8u * j + mt) * (KS_TOT * 1024u);
#define HK(k) (4u * sp + ((nt + k) & 3u))
    TB(w1, WU(w + 2u * odd)); TB(w2, WU(4u + w + 2u * odd)); TB(w3, WU(8u + (w | (2u * odd))));
    TB(A1, tA + 18432u + HK(1u) * 4096u); TB(A2, tA + 18432u + HK(2u) * 4096u); TB(A3, tA + 18432u + HK(3u) * 4096u);
    TB(B1, tB + 18432u + HK(1u) * 4096u); TB(B2, tB + 18432u + HK(2u) * 4096u); TB(B3, tB + 18432u + HK(3u) * 4096u);
    const unsigned pre = ATT_PEEL_PRE ? sslt<7>(j, tA + 18432u + head * 4096u, OOR) : OOR;
    TB(Ao, OSEL(pre, tA + sp * 2048u)); TB(Bo, OSEL(pre + 2048u, tB + sp * 2048u)); TB(Al, tA + 16384u); TB(Bl, tB + 16384u);
    const unsigned nred = w ^ (sp >> 1), blk = sp & 1u;
    const unsigned xg = (unsigned)WS_X + (4u * j + nt) * 589824u + w * 16384u, oA = (128u * j + 16u * mt) * 1152u + nt * 288u, oB = (1920u - 128u * j + 16u * mt) * 1152u + nt * 288u;
    TB(l8, ssel(nred, OOR, ssel(blk, tB, tA) + 16384u)); TB(xr, xg + sp * 65536u); TB(x8r, ssel(nred, OOR, xg + 524288u + (sp & 1u) * 1024u));
    TB(oA, oA + sp * 32u); TB(oB, oB + sp * 32u); TB(o8, ssel(nred, OOR, ssel(blk, oB, oA) + 256u)); TB(x8, xg + 524288u + sp * 2048u);
    TB(f8, xg + w * 131072u + 14400u + sp * 4u); TB(mk8, ssel(nred, 0u, 0xff0000u));
#define XI(i) TB(x##i, xg + ((sp + 1u + i) & 7u) * 65536u + i * 2048u)
    XI(0); XI(1); XI(2); XI(3); XI(4); XI(5); XI(6);
#undef XI
#undef HK
#undef WU
#undef TB
    vp[0] = (u32x4){vw[0], vw[1], vw[2], vw[3]}; vp[1] = (u32x4){vw[4], vw[5], vw[6], vw[7]}; vp[2] = (u32x4){vw[8], vwl, vrd, tab};
}
__global__ __launch_bounds__(256, 1) __attribute__((aligned(4096))) void fused_kernel(const u16* __restrict__ x, const u16* __restrict__ ckv, const u16* __restrict__ wq, const u16* __restrict__ wuk,
                                                       const u16* __restrict__ wuv, const u16* __restrict__ wqkv, const u16* __restrict__ gw,
                                                       const u16* __restrict__ wo, const u16* __restrict__ wob, char* __restrict__ ws_, u16* __restrict__ out) {
    using namespace att;
    extern __shared__ __attribute__((aligned(16))) char lds[];
    u32x4 sa[5]; u32x2 s24, s28; char* ws; unsigned tdum; kaptr ka = (kaptr)__builtin_amdgcn_kernarg_segment_ptr();
    asm volatile("s_load_dwordx8 s[8:15], %[ka], 0x0\n\ts_load_dwordx8 s[16:23], %[ka], 0x20\n\ts_getpc_b64 s[4:5]\n\tv_lshlrev_b32_e32 v191, 7, %[t]\n\ts_bfe_u32 s6, %[b], %[cb]\n\t"
                 "v_lshl_add_u32 v191, s6, %[cs], v191\n\tv_min_u32_e32 v191, %[e], v191\n\tglobal_load_dword v191, v191, s[4:5]\n\ts_load_dwordx4 s[24:27], %[ka], 0x40\n\ts_load_dwordx2 s[28:29], %[ka], 0x50"
                 : "={s[8:11]}"(sa[0]), "={s[12:15]}"(sa[1]), "={s[16:19]}"(sa[2]), "={s[20:23]}"(sa[3]), "={s[24:25]}"(s24), "={s[26:27]}"(ws), "={s[28:29]}"(s28), "={v191}"(tdum), [ka] "+{s[0:1]}"(ka)
                 : [t] "v"(threadIdx.x), [b] "s"(blockIdx.x), [e] "i"(CT_END), [cb] "i"(CT_END < 0x20000 ? 0x20003 : 0x30003), [cs] "i"(CT_END < 0x20000 ? 15 : 14) : "s4", "s5", "s6", "scc");
    SBAR();
    const unsigned wid = rfl(threadIdx.x >> 6);
    unsigned tag, vl;
    const int b = blockIdx.x;
    u32x4 vp[5], srdA; u32x2 sd;
    proj_phase(ka, ws, wid, vp, vl, sa, s24, s28, tdum, srdA, sd, tag
    );
    unsigned* const flags = (unsigned*)(ws + WS_F);
    RTS(2);
#define AST(L) do {} while (0)
#define ATT_TAIL "s_endpgm\n"
    unsigned abad;
    asm volatile(ATT_ASM ATT_TAIL "L_end_%=:\n s_nop 1"
                 : "={s94}"(abad)
                 : "{v[0:3]}"(vp[0]), "{v[4:7]}"(vp[1]), "{v[8:11]}"(vp[2]), "{v[12:15]}"(vp[3]), "{v[16:19]}"(vp[4]), "{v20}"(vl), "{s[56:59]}"(srdA), "{s[60:61]}"(sd), "{s62}"(tag),
                   "{s[8:11]}"(sa[0]), "{s[12:15]}"(sa[1]), "{s[16:19]}"(sa[2]), "{s[20:23]}"(sa[3]), "{s24}"(sa[4][0]), "{s3}"(wid), "{s2}"(b), "{s[28:29]}"(s28)
                 : "memory", "m0", "vcc", "scc", ATT_VCLOB, ATT_SCLOB, ACLOB_LO, ACLOB_HI);
    {
        unsigned b2 = b, w2 = wid;
        asm volatile("s_waitcnt vmcnt(0)" : "+s"(ka), "+s"(b2), "+s"(w2), "+s"(ws) :: "memory");
        const unsigned j2 = b2 >> 5, p2 = w2 & 1u, h2 = (b2 & 7u) * 4u + ((b2 >> 3) & 3u);
#pragma unroll 1
        for (unsigned i = w2 & 2u; i < ((w2 & 2u) ? (abad ? 8u : 4u) : 2u); ++i)
            att_exact(ka[0], ka[2], ws, h2, (i < 4u ? 1920u - 128u * j2 : 128u * j2 - 128u) + 32u * i + 16u * p2, *reinterpret_cast<const float*>(lds + LDS_G + (32u * i + 16u * p2 + (lane_id() & 15u)) * 4u));
        asm volatile("s_waitcnt vmcnt(0)" ::: "memory");
        unsigned* fa = (unsigned*)(ws + WS_F) + F_R + (8u * j2 + (b2 & 7u)) * 256u + (2u + p2) * 64u + ((b2 >> 3) & 3u);
        if (w2 & 2u) st_flag(fa, tag);
        wait_flags(fa, ld_flag(fa), tag);
        const unsigned l2 = lane_id(); u32x4 c[3]; out_consts(c, ka[7], ka[8], ka[10], b2, w2, l2);
        asm volatile("s_waitcnt vmcnt(0) lgkmcnt(0)\n\ts_nop 7\n\ts_branch L_att_stub" :: "{v[0:3]}"(c[0]), "{v[4:7]}"(c[1]), "{v[8:11]}"(c[2]), "{v20}"(l2 * 16u), "{s[56:59]}"(SRDA), "{s60}"(SD(8u * j2, h2, w2)),
                     "{s61}"(SD(120u - 8u * j2, h2, w2)), "{s62}"(tag), "{s3}"(w2), "{s2}"(b2) : "memory");
    }
    RTS(5);
}

extern "C" void kernel_launch(void* const* d_in, const int* in_sizes, int n_in, void* d_out, int out_size, void* d_ws, size_t ws_size,
                              hipStream_t stream) {
    (void)hipFuncSetAttribute((const void*)fused_kernel, hipFuncAttributeMaxDynamicSharedMemorySize, att::LDS_BYTES);
    fused_kernel<<<dim3(256), 256, att::LDS_BYTES, stream>>>((const u16*)d_in[0], (const u16*)d_in[4], (const u16*)d_in[5], (const u16*)d_in[6], (const u16*)d_in[7], (const u16*)d_in[1],
                                                             (const u16*)d_in[9], (const u16*)d_in[2], (const u16*)d_in[8], (char*)d_ws, (u16*)d_out);
}
```

```cpp
#include <hip/hip_runtime.h>
#include <hip/hip_bf16.h>
#include <stdint.h>
#include <utility>

typedef short bf16x8 __attribute__((ext_vector_type(8)));
typedef short s16x4 __attribute__((ext_vector_type(4)));
typedef float f32x16 __attribute__((ext_vector_type(16)));
typedef float f32x4 __attribute__((ext_vector_type(4)));
typedef unsigned u32x4 __attribute__((ext_vector_type(4)));
typedef unsigned u32x2 __attribute__((ext_vector_type(2)));
typedef unsigned short u16;

constexpr int S = 2048, DM = 576, NH = 32, HD = 128, DC = 512, NQKV = 1728, DHA = 18;
constexpr int NBIG = NH * HD;
constexpr int KS_TOT = 146;

#define SBAR() __builtin_amdgcn_sched_barrier(0)
constexpr size_t WS_Q = 0, WS_K = WS_Q + (size_t)S * NBIG * 2, WS_V = WS_K + (size_t)S * NBIG * 2, WS_AF = WS_V + (size_t)S * NBIG * 2, WS_X = WS_AF + (size_t)128 * KS_TOT * 1024,
                 WS_P = WS_X + (size_t)64 * 16 * 9216, WS_F = WS_P + (size_t)128 * (4 * 32 * 256 + 1024) * 4;
constexpr size_t WS_L = (size_t)96 << 20;
static_assert(WS_F == 96206848, "workspace map is baked into the generated asm");
constexpr int F_EPOCH = 0, F_J = 1024, F_ST = 8192, F_KV = 40960, F_R = 65536;
constexpr unsigned TAG_STEP = 0x9E3779B1u;
__device__ __forceinline__ unsigned ld_flag(const unsigned* p) { return __hip_atomic_load(p, __ATOMIC_RELAXED, __HIP_MEMORY_SCOPE_AGENT); }
__device__ __forceinline__ void st_flag(unsigned* p, unsigned v) { __hip_atomic_store(p, v, __ATOMIC_RELAXED, __HIP_MEMORY_SCOPE_AGENT); }
__device__ __forceinline__ void wait_flags(const unsigned* p, unsigned v, unsigned tag) {
    if (__builtin_expect(__builtin_amdgcn_ballot_w64(v != tag) != 0ull, 0)) {
        unsigned spins = 0;
        do { __builtin_amdgcn_s_sleep(1); } while (__builtin_amdgcn_ballot_w64(ld_flag(p) != tag) != 0ull && ++spins < (1u << 22));
    }
    asm volatile("" ::: "memory");
}

__device__ __forceinline__ unsigned lane_id() { unsigned l; asm volatile("v_mbcnt_lo_u32_b32 %0, -1, 0\n\tv_mbcnt_hi_u32_b32 %0, -1, %0" : "=v"(l)); return l; }
#define RTS(slot) do {} while (0)
#define CT_END ((PROJ_PAGES + ATT_PAGES) * 4096 + (ATT_TAILB & ~127) - 128)
#define ACLOB_LO "a0","a1","a2","a3","a4","a5","a6","a7","a8","a9","a10","a11","a12","a13","a14","a15","a16","a17","a18","a19","a20","a21","a22","a23","a24","a25","a26","a27","a28","a29","a30","a31","a32","a33","a34","a35","a36","a37","a38","a39","a40","a41","a42","a43","a44","a45","a46","a47","a48","a49","a50","a51","a52","a53","a54","a55","a56","a57","a58","a59","a60","a61","a62","a63","a64","a65","a66","a67","a68","a69","a70","a71","a72","a73","a74","a75","a76","a77","a78","a79","a80","a81","a82","a83","a84","a85","a86","a87","a88","a89","a90","a91","a92","a93","a94","a95","a96","a97","a98","a99","a100","a101","a102","a103","a104","a105","a106","a107","a108","a109","a110","a111","a112","a113","a114","a115","a116","a117","a118","a119","a120","a121","a122","a123","a124","a125","a126","a127"
#define ACLOB_HI "a128","a129","a130","a131","a132","a133","a134","a135","a136","a137","a138","a139","a140","a141","a142","a143","a144","a145","a146","a147","a148","a149","a150","a151","a152","a153","a154","a155","a156","a157","a158","a159","a160","a161","a162","a163","a164","a165","a166","a167","a168","a169","a170","a171","a172","a173","a174","a175","a176","a177","a178","a179","a180","a181","a182","a183","a184","a185","a186","a187","a188","a189","a190","a191","a192","a193","a194","a195","a196","a197","a198","a199","a200","a201","a202","a203","a204","a205","a206","a207","a208","a209","a210","a211","a212","a213","a214","a215","a216","a217","a218","a219","a220","a221","a222","a223","a224","a225","a226","a227","a228","a229","a230","a231","a232","a233","a234","a235","a236","a237","a238","a239","a240","a241","a242","a243","a244","a245","a246","a247","a248","a249","a250","a251","a252","a253","a254","a255"
#define M_ "v_mfma_f32_16x16x32_bf16 "
#define X_ "ds_read_b64_tr_b16 "
#define B_ "buffer_load_dwordx4 "
#define C_ "v_cvt_pk_bf16_f32 "
#define R_ "ds_read_b128 "
#define E_ "v_exp_f32_e32 "
#define PLO(p) ((unsigned)(size_t)(p))
#define PHI(p) ((unsigned)((size_t)(p) >> 32))
__device__ __forceinline__ unsigned vsel(bool c, unsigned a, unsigned b) { asm("" : "+v"(a), "+v"(b)); return c ? a : b; }
__device__ __forceinline__ unsigned ssel(unsigned c, unsigned a, unsigned b) { unsigned r; asm("s_cmp_lg_u32 %1, 0\n\ts_cselect_b32 %0, %2, %3" : "=s"(r) : "s"(c), "s"(a), "s"(b) : "scc"); return r; }
template <int N> __device__ __forceinline__ unsigned sslt(unsigned c, unsigned a, unsigned b) { unsigned r; asm("s_cmpk_lt_u32 %1, %4\n\ts_cselect_b32 %0, %2, %3" : "=s"(r) : "s"(c), "s"(a), "s"(b), "n"(N) : "scc"); return r; }
namespace att {
constexpr int LDS_BYTES = 163840;
constexpr unsigned LDS_G = 159744u, LDS_L = 161792u;
}
#define SD(J8, H, W) ((unsigned)WS_AF + (((J8) + 2u * ((W) & 2u) + ((W) & 1u)) * KS_TOT + 18u + 4u * (H)) * 1024u)
#define SRDA (u32x4){PLO(ws), PHI(ws) & 0xffffu, (unsigned)WS_L, 0x00020000u}
#define PROJ_ASM \
".p2align 3\n" \
"L_pb:\n" \
"s_waitcnt lgkmcnt(0)\n" \
"s_mov_b32 s64, s10\n" \
"s_and_b32 s65, s11, 0xffff\n" \
"s_mov_b32 s66, 0x200000\n" \
"s_mov_b32 s67, 0x20000\n" \
"s_mov_b32 s68, s14\n" \
"s_and_b32 s69, s15, 0xffff\n" \
"s_mov_b32 s70, 0x400000\n" \
"s_mov_b32 s71, 0x20000\n" \
"s_mov_b32 s72, s16\n" \
"s_and_b32 s73, s17, 0xffff\n" \
"s_mov_b32 s74, 0x400000\n" \
"s_mov_b32 s75, 0x20000\n" \
"s_add_u32 m0, s30, 0x0\n" \
"s_movk_i32 s84, 0x0\n" \
B_"v22, s[64:67], s84 offen offset:0 lds\n" \
"s_movk_i32 s84, 0x1c00\n" \
B_"v23, s[64:67], s84 offen offset:1024 lds\n" \
"s_movk_i32 s84, 0x3800\n" \
B_"v22, s[64:67], s84 offen offset:2048 lds\n" \
"s_movk_i32 s84, 0x5400\n" \
B_"v23, s[64:67], s84 offen offset:3072 lds\n" \
"s_add_u32 m0, s31, 0x0\n" \
"s_movk_i32 s84, 0x0\n" \
B_"v26, s[68:71], s84 offen offset:0 lds\n" \
"s_movk_i32 s84, 0x7c00\n" \
B_"v26, s[68:71], s84 offen offset:1024 lds\n" \
"s_addk_i32 s84, 0x7c00\n" \
B_"v27, s[68:71], s84 offen offset:2048 lds\n" \
"s_addk_i32 s84, 0x7c00\n" \
B_"v27, s[68:71], s84 offen offset:3072 lds\n" \
"s_movk_i32 s7, 0x300\n" \
"s_lshr_b32 s4, s2, 1\n" \
"s_and_b32 s4, s4, 60\n" \
"s_or_b32 s4, s4, s3\n" \
"v_lshrrev_b32_e32 v212, 4, v20\n" \
"v_min_u32_e32 v212, 59, v212\n" \
"s_cmpk_lt_u32 s2, 0x80\n" \
"s_cselect_b32 s6, s14, s16\n" \
"v_subrev_u32_e32 v214, 48, v212\n" \
"v_mov_b32_e32 v212, s10\n" \
"v_mov_b32_e32 v215, s6\n" \
"s_cmpk_lt_u32 s2, 0x80\n" \
"s_cselect_b32 s5, s15, s17\n" \
"v_cmp_gt_u32_e32 vcc, s7, v20\n" \
"v_mov_b32_e64 v213, s11\n" \
"v_bfe_u32 v216, v20, 4, 3\n" \
"v_cndmask_b32_e32 v212, v212, v215, vcc\n" \
"v_mov_b32_e32 v215, s5\n" \
"s_lshl_b32 s5, s3, 4\n" \
"v_cndmask_b32_e32 v213, v213, v215, vcc\n" \
"v_lshlrev_b32_e64 v215, 5, v20\n" \
"v_and_b32_e32 v215, 0x7000, v215\n" \
"v_lshl_add_u32 v215, s4, 6, v215\n" \
"s_lshl_b32 s4, s2, 3\n" \
"s_and_b32 s4, s4, 56\n" \
"v_or3_b32 v215, v215, s4, v216\n" \
"s_lshl_b32 s4, s2, 6\n" \
"s_or_b32 s6, s5, s4\n" \
"v_cmp_gt_u32_e64 s[4:5], 6, v214\n" \
"v_add_u32_e32 v215, 0x2000, v215\n" \
"s_nop 0\n" \
"v_cndmask_b32_e64 v216, 4, 2, s[4:5]\n" \
"v_add3_u32 v214, s6, v214, v216\n" \
"s_lshl_b32 s4, s3, 9\n" \
"v_cndmask_b32_e32 v214, v214, v215, vcc\n" \
"v_lshlrev_b32_e32 v214, 7, v214\n" \
"v_mov_b32_e64 v215, 0\n" \
"v_lshl_add_u64 v[184:185], v[212:213], 0, v[214:215]\n" \
"s_or_b32 s32, s4, 0x25000\n" \
"s_add_u32 m0, s30, 0x1000\n" \
"s_mov_b32 s84, 0x8000\n" \
B_"v22, s[64:67], s84 offen offset:0 lds\n" \
"s_and_b32 s33, s3, 1\n" \
"v_lshrrev_b32_e32 v212, 4, v20\n" \
"v_lshrrev_b32_e64 v214, 8, v20\n" \
"v_bfe_u32 v212, v212, 1, 3\n" \
"v_lshlrev_b32_e32 v215, 2, v20\n" \
"s_addk_i32 s84, 0x1c00\n" \
B_"v23, s[64:67], s84 offen offset:1024 lds\n" \
"v_lshrrev_b32_e64 v216, 1, v20\n" \
"v_bfe_u32 v213, v20, 4, 4\n" \
"s_lshl_b32 s4, s3, 5\n" \
"v_xor_b32_e32 v212, v212, v214\n" \
"v_lshlrev_b32_e32 v214, 11, v214\n" \
"s_addk_i32 s84, 0x1c00\n" \
B_"v22, s[64:67], s84 offen offset:2048 lds\n" \
"v_and_b32_e32 v215, 0x300, v215\n" \
"v_and_b32_e64 v217, 24, v216\n" \
"v_and_or_b32 v213, s4, 64, v213\n" \
"v_or3_b32 v214, v215, v214, v217\n" \
"s_lshl_b32 s4, s33, 14\n" \
"s_addk_i32 s84, 0x1c00\n" \
B_"v23, s[64:67], s84 offen offset:3072 lds\n" \
"v_and_b32_e32 v215, 0xe0, v216\n" \
"v_lshlrev_b32_e32 v213, 7, v213\n" \
"v_lshlrev_b32_e32 v212, 4, v212\n" \
"v_or3_b32 v214, v214, s4, v215\n" \
"v_or_b32_e64 v28, v212, v213\n" \
"s_add_u32 m0, s31, 0x4000\n" \
"s_movk_i32 s84, 0x0\n" \
B_"v26, s[72:75], s84 offen offset:0 lds\n" \
"v_add_u32_e32 v32, 0x10000, v214\n" \
"v_bitop3_b32 v29, v212, 64, v213 bitop3:0x36\n" \
"v_xor_b32_e32 v33, 0x20, v32\n" \
"v_xor_b32_e64 v34, 0x40, v32\n" \
"v_xor_b32_e32 v35, 0x60, v32\n" \
"s_movk_i32 s84, 0x7c00\n" \
B_"v26, s[72:75], s84 offen offset:1024 lds\n" \
"v_xor_b32_e32 v36, 0x80, v32\n" \
"v_xor_b32_e32 v37, 0xa0, v32\n" \
"v_xor_b32_e32 v38, 0xc0, v32\n" \
"v_xor_b32_e32 v39, 0xe0, v32\n" \
"s_mov_b32 s80, s26\n" \
"s_addk_i32 s84, 0x7c00\n" \
B_"v27, s[72:75], s84 offen offset:2048 lds\n" \
"s_and_b32 s81, s27, 0xffff\n" \
"s_mov_b32 s82, 0x10000000\n" \
"s_mov_b32 s83, 0x20000\n" \
"s_mov_b32 s76, s18\n" \
"s_and_b32 s77, s19, 0xffff\n" \
"s_addk_i32 s84, 0x7c00\n" \
B_"v27, s[72:75], s84 offen offset:3072 lds\n" \
"s_mov_b32 s78, 0x1e6000\n" \
"s_mov_b32 s79, 0x20000\n" \
"s_branch L_pgo_%=\n" \
"L_pret:\n" \
"s_setpc_b64 s[88:89]\n" \
"L_pgo_%=:\n" \
"s_mul_i32 s88, s33, 0xa000\n" \
"s_lshl_b32 s89, s3, 11\n" \
"s_and_b32 s89, s89, 0x1000\n" \
"s_add_u32 s88, s88, s89\n" \
"s_getpc_b64 s[86:87]\n" \
"L_hpa_%=:\n" \
"s_add_u32 s86, s86, L_pp1-L_hpa_%=\n" \
"s_addc_u32 s87, s87, 0\n" \
"s_add_u32 s86, s86, s88\n" \
"s_addc_u32 s87, s87, 0\n" \
"s_add_u32 m0, s30, 0x8000\n" \
"s_movk_i32 s84, 0x80\n" \
B_"v22, s[64:67], s84 offen offset:0 lds\n" \
"s_lshr_b32 s4, s2, 1\n" \
"s_and_b32 s4, s4, 0x7c\n" \
"v_bfe_u32 v212, v20, 4, 5\n" \
"s_movk_i32 s84, 0x1c80\n" \
B_"v23, s[64:67], s84 offen offset:1024 lds\n" \
"s_or_b32 s4, s4, s3\n" \
"v_min_u32_e32 v212, 26, v212\n" \
"v_mad_u64_u32 v[212:213], s[4:5], s4, 27, v[212:213]\n" \
"s_movk_i32 s84, 0x3880\n" \
B_"v22, s[64:67], s84 offen offset:2048 lds\n" \
"v_mul_u32_u24_e32 v213, 0xaaab, v212\n" \
"s_lshr_b32 s7, s2, 5\n" \
"v_lshrrev_b32_e32 v213, 18, v213\n" \
"s_movk_i32 s84, 0x5480\n" \
B_"v23, s[64:67], s84 offen offset:3072 lds\n" \
"s_mov_b32 s4, 0x1fffffa\n" \
"s_and_b32 s6, s2, 7\n" \
"v_mad_u64_u32 v[214:215], s[4:5], v213, s4, v[212:213]\n" \
"s_mov_b64 exec, 0\n" \
"s_swappc_b64 s[88:89], s[86:87]\n" \
"s_mov_b64 exec, -1\n" \
"s_add_u32 s86, s86, 0x2000\n" \
"s_addc_u32 s87, s87, 0\n" \
"s_add_u32 m0, s31, 0x8000\n" \
"s_mov_b32 s84, 0x80000\n" \
B_"v26, s[68:71], s84 offen offset:0 lds\n" \
"s_mulk_i32 s7, 0x90\n" \
"v_mul_u32_u24_e32 v213, 27, v213\n" \
"v_lshrrev_b32_e32 v214, 1, v214\n" \
"s_addk_i32 s84, 0x7c00\n" \
B_"v26, s[68:71], s84 offen offset:1024 lds\n" \
"v_and_b32_e32 v212, 1, v212\n" \
"s_add_u32 s4, s7, s20\n" \
"v_mul_u32_u24_e32 v214, 9, v214\n" \
"v_add3_u32 v212, v212, s6, v213\n" \
"s_addc_u32 s5, 0, s21\n" \
"s_mov_b64 exec, 0\n" \
"s_swappc_b64 s[88:89], s[86:87]\n" \
"s_mov_b64 exec, -1\n" \
"s_add_u32 s86, s86, 0x2000\n" \
"s_addc_u32 s87, s87, 0\n" \
"s_addk_i32 s84, 0x7c00\n" \
B_"v27, s[68:71], s84 offen offset:2048 lds\n" \
"v_add_lshl_u32 v212, v212, v214, 7\n" \
"v_mov_b32_e64 v213, 0\n" \
"v_lshl_add_u64 v[186:187], s[18:19], 0, v[212:213]\n" \
"s_addk_i32 s84, 0x7c00\n" \
B_"v27, s[68:71], s84 offen offset:3072 lds\n" \
"s_bitcmp1_b32 s3, 0\n" \
"s_cselect_b32 s6, 0x8c, 0\n" \
"s_add_u32 s4, s4, s6\n" \
"v_mov_b32_e32 v212, v187\n" \
"s_addc_u32 s5, s5, 0\n" \
"s_mov_b64 exec, 0\n" \
"s_swappc_b64 s[88:89], s[86:87]\n" \
"s_mov_b64 exec, -1\n" \
"s_add_u32 s86, s86, 0x2000\n" \
"s_addc_u32 s87, s87, 0\n" \
"s_add_u32 m0, s30, 0x9000\n" \
"s_mov_b32 s84, 0x8080\n" \
B_"v22, s[64:67], s84 offen offset:0 lds\n" \
"v_writelane_b32 v186, s4, 31\n" \
"v_writelane_b32 v212, s5, 31\n" \
"v_mov_b32_e32 v187, v212\n" \
"s_addk_i32 s84, 0x1c00\n" \
B_"v23, s[64:67], s84 offen offset:1024 lds\n" \
"s_lshl_b32 s35, s2, 10\n" \
"s_lshr_b32 s37, s2, 1\n" \
"s_mul_i32 s37, s37, 0x24800\n" \
"s_addk_i32 s84, 0x1c00\n" \
B_"v22, s[64:67], s84 offen offset:2048 lds\n" \
"s_and_b32 s35, s35, 0x400\n" \
"s_lshl_b32 s36, s3, 8\n" \
"s_or_b32 s35, s37, s35\n" \
"s_mov_b64 exec, 0\n" \
"s_swappc_b64 s[88:89], s[86:87]\n" \
"s_mov_b64 exec, -1\n" \
"s_andn2_b32 s85, 2, s3\n" \
"s_lshl_b32 s85, s85, 12\n" \
"s_add_u32 s86, s86, s85\n" \
"s_addc_u32 s87, s87, 0\n" \
"s_addk_i32 s84, 0x1c00\n" \
B_"v23, s[64:67], s84 offen offset:3072 lds\n" \
"s_and_b32 s4, s2, 7\n" \
"s_lshr_b32 s6, s2, 5\n" \
"s_lshl_b32 s34, s2, 5\n" \
"s_add_u32 m0, s31, 0xc000\n" \
"s_mov_b32 s84, 0x80000\n" \
B_"v26, s[72:75], s84 offen offset:0 lds\n" \
"s_add_i32 s35, s35, s36\n" \
"s_lshl_b32 s7, s4, 10\n" \
"s_and_b32 s34, s34, 0x300\n" \
"s_mov_b64 exec, 0\n" \
"s_swappc_b64 s[88:89], s[86:87]\n" \
"s_mov_b64 exec, -1\n" \
"s_addk_i32 s84, 0x7c00\n" \
B_"v26, s[72:75], s84 offen offset:1024 lds\n" \
"s_add_i32 s35, s35, 0x3004000\n" \
"s_lshl_b32 s37, s6, 13\n" \
"s_or_b32 s34, s7, s34\n" \
"s_addk_i32 s84, 0x7c00\n" \
B_"v27, s[72:75], s84 offen offset:2048 lds\n" \
"s_or_b32 s7, s37, s7\n" \
"v_mov_b32_e32 v213, s35\n" \
"s_or_b32 s7, s7, s36\n" \
"s_addk_i32 s84, 0x7c00\n" \
B_"v27, s[72:75], s84 offen offset:3072 lds\n" \
"s_lshr_b32 s5, s2, 3\n" \
"s_or_b32 s7, s7, 0x5c00000\n" \
"v_writelane_b32 v213, s7, 2\n" \
"s_mul_i32 s5, s5, 0x90000\n" \
"v_writelane_b32 v213, s7, 3\n" \
"s_lshl_b32 s7, s3, 14\n" \
"s_lshl_b32 s4, s4, 16\n" \
"s_add_i32 s5, s5, s7\n" \
"s_or_b32 s34, s34, 0x5be8000\n" \
"v_writelane_b32 v213, s34, 4\n" \
"s_add_i32 s4, s5, s4\n" \
"v_writelane_b32 v213, s34, 5\n" \
"s_add_i32 s4, s4, 0x4243800\n" \
"v_writelane_b32 v213, s4, 6\n" \
"s_mul_i32 s4, s6, 0xa00\n" \
"v_lshlrev_b32_e32 v212, 3, v20\n" \
"s_lshl_b32 s5, s3, 7\n" \
"v_and_b32_e32 v212, 0x80, v212\n" \
"s_add_i32 s4, s4, s5\n" \
"s_add_i32 s4, s4, 0x5bc0f80\n" \
"v_writelane_b32 v213, s4, 7\n" \
"v_add_u32_e32 v49, v213, v212\n" \
"s_mov_b32 m0, s32\n" \
"s_mov_b32 exec_lo, 0xffffff\n" \
"s_mov_b32 exec_hi, 0xfff0000\n" \
"global_load_lds_dword v[184:185], off\n" \
"s_mov_b64 exec, -1\n" \
"s_mov_b32 m0, s32\n" \
"s_mov_b64 exec, 0xff\n" \
"buffer_load_dword v49, s[80:83], 0 offen lds\n" \
"s_mov_b64 exec, -1\n" \
"s_mov_b32 s84, 0x5bc0000\n" \
"buffer_load_dword v180, off, s[80:83], s84 sc1\n" \
"s_cmp_lg_u32 s33, 0\n" \
"s_cbranch_scc1 L_w1_%=\n" \
"L_w0_%=:\n" \
"s_waitcnt vmcnt(19)\n" \
"s_barrier\n" \
X_"a[160:161], v32 offset:0\n" \
X_"a[162:163], v32 offset:1024\n" \
R_"a[128:131], v28 offset:16384\n" \
X_"a[224:225], v32 offset:8192\n" \
X_"a[226:227], v32 offset:9216\n" \
R_"a[192:195], v29 offset:16384\n" \
R_"a[132:135], v28 offset:18432\n" \
R_"a[196:199], v29 offset:18432\n" \
X_"a[164:165], v33 offset:0\n" \
X_"a[166:167], v33 offset:1024\n" \
X_"a[228:229], v33 offset:8192\n" \
X_"a[230:231], v33 offset:9216\n" \
R_"a[136:139], v28 offset:20480\n" \
R_"a[200:203], v29 offset:20480\n" \
"s_waitcnt lgkmcnt(11)\n" \
M_"a[0:3], a[160:163], a[128:131], 0\n" \
X_"a[168:169], v34 offset:0\n" \
"s_lshr_b32 s4, s2, 1\n" \
"s_waitcnt lgkmcnt(9)\n" \
M_"a[0:3], a[224:227], a[192:195], a[0:3]\n" \
X_"a[170:171], v34 offset:1024\n" \
"s_nop 5\n" \
"s_waitcnt lgkmcnt(9)\n" \
M_"a[16:19], a[160:163], a[132:135], 0\n" \
X_"a[232:233], v34 offset:8192\n" \
"s_and_b32 s4, s4, 0x7c\n" \
"s_waitcnt lgkmcnt(9)\n" \
M_"a[16:19], a[224:227], a[196:199], a[16:19]\n" \
X_"a[234:235], v34 offset:9216\n" \
"s_nop 5\n" \
"s_waitcnt lgkmcnt(8)\n" \
M_"a[20:23], a[164:167], a[132:135], 0\n" \
R_"a[140:143], v28 offset:22528\n" \
"v_mov_b32_e32 v213, s13\n" \
"s_waitcnt lgkmcnt(7)\n" \
M_"a[20:23], a[228:231], a[196:199], a[20:23]\n" \
R_"a[204:207], v29 offset:22528\n" \
"s_nop 5\n" \
M_"a[4:7], a[164:167], a[128:131], 0\n" \
X_"a[172:173], v35 offset:0\n" \
"s_or_b32 s4, s4, s3\n" \
M_"a[4:7], a[228:231], a[192:195], a[4:7]\n" \
X_"a[174:175], v35 offset:1024\n" \
"s_nop 5\n" \
"s_waitcnt lgkmcnt(9)\n" \
M_"a[32:35], a[160:163], a[136:139], 0\n" \
X_"a[236:237], v35 offset:8192\n" \
"v_lshrrev_b32_e32 v212, 4, v20\n" \
"s_waitcnt lgkmcnt(9)\n" \
M_"a[32:35], a[224:227], a[200:203], a[32:35]\n" \
X_"a[238:239], v35 offset:9216\n" \
"s_nop 5\n" \
M_"a[36:39], a[164:167], a[136:139], 0\n" \
R_"a[144:147], v28 offset:0\n" \
"v_mad_u64_u32 v[214:215], s[4:5], s4, 36, v[212:213]\n" \
M_"a[36:39], a[228:231], a[200:203], a[36:39]\n" \
R_"a[208:211], v29 offset:0\n" \
"s_nop 5\n" \
"s_waitcnt lgkmcnt(10)\n" \
M_"a[40:43], a[168:171], a[136:139], 0\n" \
R_"a[148:151], v28 offset:2048\n" \
"v_mov_b32_e32 v217, s9\n" \
"s_waitcnt lgkmcnt(9)\n" \
M_"a[40:43], a[232:235], a[200:203], a[40:43]\n" \
R_"a[212:215], v29 offset:2048\n" \
"s_nop 5\n" \
M_"a[24:27], a[168:171], a[132:135], 0\n" \
R_"a[152:155], v28 offset:4096\n" \
"s_movk_i32 s4, 0x240\n" \
M_"a[24:27], a[232:235], a[196:199], a[24:27]\n" \
R_"a[216:219], v29 offset:4096\n" \
"s_nop 5\n" \
M_"a[8:11], a[168:171], a[128:131], 0\n" \
R_"a[156:159], v28 offset:6144\n" \
"v_cmp_gt_u32_e32 vcc, s4, v20\n" \
M_"a[8:11], a[232:235], a[192:195], a[8:11]\n" \
R_"a[220:223], v29 offset:6144\n" \
"s_mov_b32 m0, s32\n" \
"s_nop 5\n" \
"s_waitcnt lgkmcnt(13)\n" \
M_"a[48:51], a[160:163], a[140:143], 0\n" \
"s_mov_b32 exec_lo, 0xff000000\n" \
"s_mov_b32 exec_hi, 0x0\n" \
"global_load_lds_dword v[184:185], off\n" \
"s_mov_b64 exec, -1\n" \
"s_waitcnt lgkmcnt(12)\n" \
M_"a[48:51], a[224:227], a[204:207], a[48:51]\n" \
"s_nop 5\n" \
X_"a[176:177], v36 offset:0\n" \
M_"a[52:55], a[164:167], a[140:143], 0\n" \
X_"a[178:179], v36 offset:1024\n" \
"s_lshl_b32 s4, s2, 3\n" \
M_"a[52:55], a[228:231], a[204:207], a[52:55]\n" \
"s_mov_b32 m0, s32\n" \
"s_nop 5\n" \
M_"a[56:59], a[168:171], a[140:143], 0\n" \
"s_mov_b32 exec_lo, 0xffff\n" \
"s_mov_b32 exec_hi, 0x0\n" \
"global_load_lds_dword v[186:187], off\n" \
"s_mov_b64 exec, -1\n" \
M_"a[56:59], a[232:235], a[204:207], a[56:59]\n" \
"s_nop 5\n" \
X_"a[240:241], v36 offset:8192\n" \
"s_waitcnt lgkmcnt(13)\n" \
M_"a[60:63], a[172:175], a[140:143], 0\n" \
X_"a[242:243], v36 offset:9216\n" \
"s_and_b32 s4, s4, 56\n" \
"s_waitcnt lgkmcnt(12)\n" \
M_"a[60:63], a[236:239], a[204:207], a[60:63]\n" \
"s_nop 5\n" \
X_"a[180:181], v37 offset:0\n" \
M_"a[44:47], a[172:175], a[136:139], 0\n" \
X_"a[182:183], v37 offset:1024\n" \
"v_cndmask_b32_e32 v217, v217, v213, vcc\n" \
M_"a[44:47], a[236:239], a[200:203], a[44:47]\n" \
"s_nop 5\n" \
X_"a[244:245], v37 offset:8192\n" \
M_"a[28:31], a[172:175], a[132:135], 0\n" \
X_"a[246:247], v37 offset:9216\n" \
"v_lshlrev_b32_e32 v213, 3, v214\n" \
M_"a[28:31], a[236:239], a[196:199], a[28:31]\n" \
"s_nop 5\n" \
X_"a[184:185], v38 offset:0\n" \
M_"a[12:15], a[172:175], a[128:131], 0\n" \
X_"a[186:187], v38 offset:1024\n" \
"v_and_b32_e32 v213, 0x1ffc0, v213\n" \
M_"a[12:15], a[236:239], a[192:195], a[12:15]\n" \
"s_nop 5\n" \
"v_and_b32_e32 v214, 7, v214\n" \
"s_waitcnt lgkmcnt(15)\n" \
M_"a[76:79], a[172:175], a[144:147], 0\n" \
X_"a[248:249], v38 offset:8192\n" \
"v_or3_b32 v213, v214, s4, v213\n" \
M_"a[76:79], a[236:239], a[208:211], a[76:79]\n" \
"s_nop 5\n" \
"s_lshl_b32 s4, s2, 2\n" \
M_"a[72:75], a[168:171], a[144:147], 0\n" \
"s_or_b32 s4, s4, s3\n" \
"s_mul_i32 s4, s4, 18\n" \
M_"a[72:75], a[232:235], a[208:211], a[72:75]\n" \
"s_nop 5\n" \
X_"a[250:251], v38 offset:9216\n" \
M_"a[68:71], a[164:167], a[144:147], 0\n" \
"v_min_u32_e32 v212, 53, v212\n" \
"s_sub_i32 s4, s4, 36\n" \
M_"a[68:71], a[228:231], a[208:211], a[68:71]\n" \
"s_nop 5\n" \
"v_add_u32_e64 v212, s4, v212\n" \
M_"a[64:67], a[160:163], a[144:147], 0\n" \
"v_mov_b32_e32 v215, s12\n" \
"v_mov_b32_e32 v216, s8\n" \
M_"a[64:67], a[224:227], a[208:211], a[64:67]\n" \
"s_nop 5\n" \
X_"a[188:189], v39 offset:0\n" \
M_"a[80:83], a[160:163], a[148:151], 0\n" \
"v_cndmask_b32_e32 v212, v212, v213, vcc\n" \
"v_cndmask_b32_e32 v216, v216, v215, vcc\n" \
"s_waitcnt lgkmcnt(15)\n" \
M_"a[80:83], a[224:227], a[212:215], a[80:83]\n" \
"s_nop 5\n" \
"v_lshlrev_b32_e32 v212, 7, v212\n" \
M_"a[84:87], a[164:167], a[148:151], 0\n" \
X_"a[190:191], v39 offset:1024\n" \
"v_mov_b32_e64 v213, 0\n" \
M_"a[84:87], a[228:231], a[212:215], a[84:87]\n" \
"s_nop 5\n" \
"v_lshl_add_u64 v[188:189], v[216:217], 0, v[212:213]\n" \
M_"a[88:91], a[168:171], a[148:151], 0\n" \
"s_movk_i32 s5, 0xd80\n" \
"s_lshr_b32 s6, s2, 5\n" \
M_"a[88:91], a[232:235], a[212:215], a[88:91]\n" \
"s_nop 5\n" \
X_"a[252:253], v39 offset:8192\n" \
M_"a[92:95], a[172:175], a[148:151], 0\n" \
"v_lshrrev_b32_e64 v212, 4, v20\n" \
"v_lshl_or_b32 v215, s3, 6, v212\n" \
M_"a[92:95], a[236:239], a[212:215], a[92:95]\n" \
"s_nop 5\n" \
"v_mul_u32_u24_e32 v216, 0x2aab, v215\n" \
M_"a[108:111], a[172:175], a[152:155], 0\n" \
X_"a[254:255], v39 offset:9216\n" \
"v_lshrrev_b32_e32 v216, 16, v216\n" \
M_"a[108:111], a[236:239], a[216:219], a[108:111]\n" \
"s_nop 5\n" \
"v_mad_i32_i24 v217, v216, -6, v215\n" \
M_"a[104:107], a[168:171], a[152:155], 0\n" \
"v_add_u32_e32 v218, 0xfffffe50, v215\n" \
"s_mul_i32 s35, s6, 0x48\n" \
M_"a[104:107], a[232:235], a[216:219], a[104:107]\n" \
"s_nop 5\n" \
"v_lshrrev_b32_e64 v219, 1, v217\n" \
M_"a[100:103], a[164:167], a[152:155], 0\n" \
"v_add_u32_e64 v218, s35, v216\n" \
"v_mul_u32_u24_e32 v219, 0x480, v219\n" \
M_"a[100:103], a[228:231], a[216:219], a[100:103]\n" \
"s_nop 5\n" \
"v_and_b32_e32 v214, 16, v20\n" \
M_"a[96:99], a[160:163], a[152:155], 0\n" \
"v_mad_u32_u24 v219, v218, s5, v219\n" \
"v_or_b32_e64 v192, v219, v214\n" \
M_"a[96:99], a[224:227], a[216:219], a[96:99]\n" \
"s_nop 5\n" \
"v_mov_b32_e32 v220, 2.0\n" \
"s_waitcnt lgkmcnt(15)\n" \
M_"a[112:115], a[160:163], a[156:159], 0\n" \
"v_mov_b32_e32 v222, 7\n" \
"v_lshlrev_b32_e64 v220, 7, v218\n" \
M_"a[112:115], a[224:227], a[220:223], a[112:115]\n" \
"s_nop 5\n" \
"v_lshrrev_b32_e32 v218, 1, v218\n" \
M_"a[116:119], a[164:167], a[156:159], 0\n" \
"v_and_b32_e32 v218, 4, v218\n" \
"v_and_b32_e32 v216, 2, v216\n" \
M_"a[116:119], a[228:231], a[220:223], a[116:119]\n" \
"s_nop 5\n" \
"s_mul_i32 s7, s6, 0x2400\n" \
M_"a[120:123], a[168:171], a[156:159], 0\n" \
"s_sub_i32 s34, 0x22000, s7\n" \
"v_bitop3_b32 v216, v217, v218, v216 bitop3:0x1e\n" \
M_"a[120:123], a[232:235], a[220:223], a[120:123]\n" \
"s_nop 5\n" \
"v_or_b32_e32 v213, 0x24400, v20\n" \
M_"a[124:127], a[172:175], a[156:159], 0\n" \
"v_lshlrev_b32_e64 v216, 4, v216\n" \
"v_add3_u32 v196, v220, s34, v216\n" \
"s_waitcnt vmcnt(5) lgkmcnt(0)\n" \
M_"a[124:127], a[236:239], a[220:223], a[124:127]\n" \
"s_barrier\n" \
M_"v[52:55], a[176:179], a[128:131], 0\n" \
"s_add_u32 m0, s30, 0x0\n" \
"s_movk_i32 s84, 0x100\n" \
M_"v[52:55], a[240:243], a[192:195], v[52:55]\n" \
B_"v22, s[64:67], s84 offen offset:0 lds\n" \
"s_nop 5\n" \
M_"v[56:59], a[180:183], a[128:131], 0\n" \
X_"a[160:161], v32 offset:32768\n" \
"v_mov_b32_e32 v216, v213\n" \
M_"v[56:59], a[244:247], a[192:195], v[56:59]\n" \
"s_movk_i32 s84, 0x1d00\n" \
"s_nop 5\n" \
M_"v[60:63], a[184:187], a[128:131], 0\n" \
B_"v23, s[64:67], s84 offen offset:1024 lds\n" \
M_"v[60:63], a[248:251], a[192:195], v[60:63]\n" \
"s_nop 5\n" \
X_"a[162:163], v32 offset:33792\n" \
M_"v[64:67], a[188:191], a[128:131], 0\n" \
X_"a[224:225], v32 offset:40960\n" \
"s_movk_i32 s36, 0x1b0\n" \
M_"v[64:67], a[252:255], a[192:195], v[64:67]\n" \
R_"a[128:131], v28 offset:49152\n" \
"s_nop 5\n" \
M_"v[80:83], a[188:191], a[132:135], 0\n" \
R_"a[192:195], v29 offset:49152\n" \
"v_or_b32_e32 v216, 0x100, v215\n" \
M_"v[80:83], a[252:255], a[196:199], v[80:83]\n" \
"s_movk_i32 s84, 0x3900\n" \
"s_nop 5\n" \
M_"v[76:79], a[184:187], a[132:135], 0\n" \
B_"v22, s[64:67], s84 offen offset:2048 lds\n" \
M_"v[76:79], a[248:251], a[196:199], v[76:79]\n" \
"s_movk_i32 s84, 0x5500\n" \
"s_nop 5\n" \
M_"v[72:75], a[180:183], a[132:135], 0\n" \
B_"v23, s[64:67], s84 offen offset:3072 lds\n" \
M_"v[72:75], a[244:247], a[196:199], v[72:75]\n" \
"s_add_u32 m0, s31, 0x0\n" \
"s_mov_b32 s84, 0x100000\n" \
"s_nop 5\n" \
M_"v[68:71], a[176:179], a[132:135], 0\n" \
B_"v26, s[68:71], s84 offen offset:0 lds\n" \
M_"v[68:71], a[240:243], a[196:199], v[68:71]\n" \
R_"a[132:135], v28 offset:51200\n" \
"s_nop 5\n" \
M_"v[84:87], a[176:179], a[136:139], 0\n" \
R_"a[196:199], v29 offset:51200\n" \
"v_mul_u32_u24_e32 v217, 0x2aab, v216\n" \
M_"v[84:87], a[240:243], a[200:203], v[84:87]\n" \
"s_addk_i32 s84, 0x7c00\n" \
"s_nop 5\n" \
M_"v[88:91], a[180:183], a[136:139], 0\n" \
B_"v26, s[68:71], s84 offen offset:1024 lds\n" \
M_"v[88:91], a[244:247], a[200:203], v[88:91]\n" \
"s_nop 5\n" \
X_"a[226:227], v32 offset:41984\n" \
M_"v[92:95], a[184:187], a[136:139], 0\n" \
"s_addk_i32 s84, 0x7c00\n" \
M_"v[92:95], a[248:251], a[200:203], v[92:95]\n" \
B_"v27, s[68:71], s84 offen offset:2048 lds\n" \
"s_nop 5\n" \
M_"v[96:99], a[188:191], a[136:139], 0\n" \
X_"a[164:165], v33 offset:32768\n" \
"v_add_u32_e32 v215, 0xffffff50, v215\n" \
M_"v[96:99], a[252:255], a[200:203], v[96:99]\n" \
R_"a[136:139], v28 offset:53248\n" \
"s_nop 5\n" \
M_"v[112:115], a[188:191], a[140:143], 0\n" \
R_"a[200:203], v29 offset:53248\n" \
"v_lshrrev_b32_e64 v217, 16, v217\n" \
M_"v[112:115], a[252:255], a[204:207], v[112:115]\n" \
"s_addk_i32 s84, 0x7c00\n" \
"s_nop 5\n" \
M_"v[108:111], a[184:187], a[140:143], 0\n" \
B_"v27, s[68:71], s84 offen offset:3072 lds\n" \
M_"v[108:111], a[248:251], a[204:207], v[108:111]\n" \
"s_add_u32 m0, s30, 0x1000\n" \
"s_mov_b32 s84, 0x8100\n" \
"s_nop 5\n" \
M_"v[104:107], a[180:183], a[140:143], 0\n" \
B_"v22, s[64:67], s84 offen offset:0 lds\n" \
M_"v[104:107], a[244:247], a[204:207], v[104:107]\n" \
"s_nop 5\n" \
X_"a[166:167], v33 offset:33792\n" \
M_"v[100:103], a[176:179], a[140:143], 0\n" \
X_"a[228:229], v33 offset:40960\n" \
"v_mov_b32_e64 v220, v215\n" \
M_"v[100:103], a[240:243], a[204:207], v[100:103]\n" \
R_"a[140:143], v28 offset:55296\n" \
"s_nop 5\n" \
M_"v[116:119], a[176:179], a[144:147], 0\n" \
R_"a[204:207], v29 offset:55296\n" \
"v_mad_i32_i24 v218, v217, -6, v216\n" \
M_"v[116:119], a[240:243], a[208:211], v[116:119]\n" \
"s_addk_i32 s84, 0x1c00\n" \
"s_nop 5\n" \
M_"v[120:123], a[180:183], a[144:147], 0\n" \
B_"v23, s[64:67], s84 offen offset:1024 lds\n" \
".fill (L_pb + 3808 - .) / 4, 4, 0xBF800000\n" \
"L_pp1:\n" \
"s_cbranch_execz L_pret\n" \
M_"v[120:123], a[244:247], a[208:211], v[120:123]\n" \
"s_addk_i32 s84, 0x1c00\n" \
"s_nop 5\n" \
M_"v[124:127], a[184:187], a[144:147], 0\n" \
B_"v22, s[64:67], s84 offen offset:2048 lds\n" \
M_"v[124:127], a[248:251], a[208:211], v[124:127]\n" \
"s_addk_i32 s84, 0x1c00\n" \
"s_nop 5\n" \
M_"v[128:131], a[188:191], a[144:147], 0\n" \
B_"v23, s[64:67], s84 offen offset:3072 lds\n" \
M_"v[128:131], a[252:255], a[208:211], v[128:131]\n" \
R_"a[144:147], v28 offset:32768\n" \
"s_nop 5\n" \
M_"v[144:147], a[188:191], a[148:151], 0\n" \
R_"a[208:211], v29 offset:32768\n" \
"v_cmp_gt_u32_e64 vcc, s36, v216\n" \
M_"v[144:147], a[252:255], a[212:215], v[144:147]\n" \
"s_add_u32 m0, s31, 0x4000\n" \
"s_mov_b32 s84, 0x100000\n" \
"s_nop 5\n" \
M_"v[140:143], a[184:187], a[148:151], 0\n" \
B_"v26, s[72:75], s84 offen offset:0 lds\n" \
M_"v[140:143], a[248:251], a[212:215], v[140:143]\n" \
"s_nop 5\n" \
X_"a[230:231], v33 offset:41984\n" \
M_"v[136:139], a[180:183], a[148:151], 0\n" \
"s_addk_i32 s84, 0x7c00\n" \
M_"v[136:139], a[244:247], a[212:215], v[136:139]\n" \
B_"v26, s[72:75], s84 offen offset:1024 lds\n" \
"s_nop 5\n" \
M_"v[132:135], a[176:179], a[148:151], 0\n" \
X_"a[168:169], v34 offset:32768\n" \
"v_mov_b32_e64 v221, 7\n" \
M_"v[132:135], a[240:243], a[212:215], v[132:135]\n" \
R_"a[148:151], v28 offset:34816\n" \
"s_nop 5\n" \
M_"v[148:151], a[176:179], a[152:155], 0\n" \
R_"a[212:215], v29 offset:34816\n" \
"v_lshrrev_b32_e32 v222, 1, v218\n" \
M_"v[148:151], a[240:243], a[216:219], v[148:151]\n" \
"s_nop 5\n" \
X_"a[170:171], v34 offset:33792\n" \
M_"v[152:155], a[180:183], a[152:155], 0\n" \
X_"a[232:233], v34 offset:40960\n" \
"v_cndmask_b32_e32 v217, v220, v217, vcc\n" \
M_"v[152:155], a[244:247], a[216:219], v[152:155]\n" \
"s_nop 5\n" \
X_"a[234:235], v34 offset:41984\n" \
M_"v[156:159], a[184:187], a[152:155], 0\n" \
X_"a[172:173], v35 offset:32768\n" \
"s_movk_i32 s4, 0x48\n" \
M_"v[156:159], a[248:251], a[216:219], v[156:159]\n" \
"s_nop 5\n" \
X_"a[174:175], v35 offset:33792\n" \
M_"v[160:163], a[188:191], a[152:155], 0\n" \
X_"a[236:237], v35 offset:40960\n" \
"v_add_u32_e32 v220, s35, v217\n" \
M_"v[160:163], a[252:255], a[216:219], v[160:163]\n" \
R_"a[152:155], v28 offset:36864\n" \
"s_nop 5\n" \
M_"v[176:179], a[188:191], a[156:159], 0\n" \
R_"a[216:219], v29 offset:36864\n" \
"v_mul_u32_u24_e32 v222, 0x480, v222\n" \
M_"v[176:179], a[252:255], a[220:223], v[176:179]\n" \
"s_nop 5\n" \
X_"a[238:239], v35 offset:41984\n" \
M_"v[172:175], a[184:187], a[156:159], 0\n" \
"v_mad_u32_u24 v222, v220, s5, v222\n" \
"v_cmp_gt_u32_e64 s[4:5], s4, v215\n" \
M_"v[172:175], a[248:251], a[220:223], v[172:175]\n" \
"s_nop 5\n" \
"v_cndmask_b32_e32 v215, v221, v218, vcc\n" \
M_"v[168:171], a[180:183], a[156:159], 0\n" \
"v_lshrrev_b32_e32 v218, 1, v220\n" \
"v_or_b32_e32 v214, v222, v214\n" \
M_"v[168:171], a[244:247], a[220:223], v[168:171]\n" \
"s_nop 5\n" \
"v_mov_b32_e32 v222, 2.0\n" \
M_"v[164:167], a[176:179], a[156:159], 0\n" \
"v_and_b32_e32 v218, 4, v218\n" \
"v_and_b32_e32 v217, 2, v217\n" \
M_"v[164:167], a[240:243], a[220:223], v[164:167]\n" \
R_"a[156:159], v28 offset:38912\n" \
R_"a[220:223], v29 offset:38912\n" \
"s_waitcnt lgkmcnt(15)\n" \
M_"a[0:3], a[160:163], a[128:131], a[0:3]\n" \
"s_addk_i32 s84, 0x7c00\n" \
M_"a[0:3], a[224:227], a[192:195], a[0:3]\n" \
B_"v27, s[72:75], s84 offen offset:2048 lds\n" \
M_"a[4:7], a[164:167], a[128:131], a[4:7]\n" \
X_"a[176:177], v36 offset:32768\n" \
"v_bitop3_b32 v215, v215, v218, v217 bitop3:0x1e\n" \
"s_waitcnt lgkmcnt(15)\n" \
M_"a[4:7], a[228:231], a[192:195], a[4:7]\n" \
"s_addk_i32 s84, 0x7c00\n" \
"s_waitcnt lgkmcnt(11)\n" \
M_"a[8:11], a[168:171], a[128:131], a[8:11]\n" \
B_"v27, s[72:75], s84 offen offset:3072 lds\n" \
"s_waitcnt lgkmcnt(9)\n" \
M_"a[8:11], a[232:235], a[192:195], a[8:11]\n" \
X_"a[178:179], v36 offset:33792\n" \
"v_lshlrev_b32_e32 v223, 1, v220\n" \
"s_waitcnt lgkmcnt(8)\n" \
M_"a[12:15], a[172:175], a[128:131], a[12:15]\n" \
X_"a[240:241], v36 offset:40960\n" \
"v_mov_b32_e32 v224, 2.0\n" \
"s_waitcnt lgkmcnt(5)\n" \
M_"a[12:15], a[236:239], a[192:195], a[12:15]\n" \
"v_cndmask_b32_e32 v193, v222, v214, vcc\n" \
"v_lshlrev_b32_e32 v214, 7, v220\n" \
M_"a[28:31], a[172:175], a[132:135], a[28:31]\n" \
"v_lshlrev_b32_e32 v215, 4, v215\n" \
"s_mul_i32 s39, s3, 18\n" \
M_"a[28:31], a[236:239], a[196:199], a[28:31]\n" \
X_"a[242:243], v36 offset:41984\n" \
"v_add3_u32 v214, v214, s34, v215\n" \
M_"a[24:27], a[168:171], a[132:135], a[24:27]\n" \
"v_cndmask_b32_e64 v195, v224, v223, s[4:5]\n" \
"s_movk_i32 s4, 0x1f8\n" \
M_"a[24:27], a[232:235], a[196:199], a[24:27]\n" \
"v_mov_b32_e32 v215, v213\n" \
"s_add_i32 s35, s35, s39\n" \
M_"a[20:23], a[164:167], a[132:135], a[20:23]\n" \
"v_cmp_gt_u32_e32 vcc, s4, v216\n" \
"v_add_u32_e64 v212, s35, v212\n" \
M_"a[20:23], a[228:231], a[196:199], a[20:23]\n" \
X_"a[180:181], v37 offset:32768\n" \
"v_mov_b32_e64 v219, 2.0\n" \
M_"a[16:19], a[160:163], a[132:135], a[16:19]\n" \
"v_cndmask_b32_e32 v197, v215, v214, vcc\n" \
"s_movk_i32 s35, 0x120\n" \
M_"a[16:19], a[224:227], a[196:199], a[16:19]\n" \
"v_mul_u32_u24_e32 v214, 0xd80, v212\n" \
"v_cmp_gt_u32_e64 vcc, s35, v20\n" \
M_"a[32:35], a[160:163], a[136:139], a[32:35]\n" \
"s_mov_b32 m0, s32\n" \
X_"a[182:183], v37 offset:33792\n" \
M_"a[32:35], a[224:227], a[200:203], a[32:35]\n" \
"s_mov_b32 exec_lo, 0x0\n" \
"s_mov_b32 exec_hi, 0xff\n" \
"global_load_lds_dword v[184:185], off\n" \
"s_mov_b64 exec, -1\n" \
M_"a[36:39], a[164:167], a[136:139], a[36:39]\n" \
"v_lshrrev_b32_e32 v215, 1, v212\n" \
"s_and_b32 s4, s2, 7\n" \
M_"a[36:39], a[228:231], a[200:203], a[36:39]\n" \
X_"a[244:245], v37 offset:40960\n" \
"v_cndmask_b32_e32 v194, v219, v214, vcc\n" \
M_"a[40:43], a[168:171], a[136:139], a[40:43]\n" \
"v_lshlrev_b32_e32 v214, 7, v212\n" \
"v_lshrrev_b32_e32 v212, 2, v212\n" \
M_"a[40:43], a[232:235], a[200:203], a[40:43]\n" \
"s_mov_b32 m0, s32\n" \
"v_and_b32_e32 v212, 2, v212\n" \
M_"a[44:47], a[172:175], a[136:139], a[44:47]\n" \
"s_mov_b32 exec_lo, 0xffff0000\n" \
"s_mov_b32 exec_hi, 0x0\n" \
"global_load_lds_dword v[186:187], off\n" \
"s_mov_b64 exec, -1\n" \
M_"a[44:47], a[236:239], a[200:203], a[44:47]\n" \
X_"a[246:247], v37 offset:41984\n" \
"v_and_or_b32 v212, v215, 1, v212\n" \
M_"a[60:63], a[172:175], a[140:143], a[60:63]\n" \
"s_lshl_b32 s5, s4, 2\n" \
"s_bfe_u32 s38, s2, 0x20003\n" \
M_"a[60:63], a[236:239], a[204:207], a[60:63]\n" \
"v_lshlrev_b32_e32 v212, 5, v212\n" \
"s_movk_i32 s35, 0x60\n" \
M_"a[56:59], a[168:171], a[140:143], a[56:59]\n" \
X_"a[184:185], v38 offset:32768\n" \
"s_or_b32 s5, s5, s38\n" \
M_"a[56:59], a[232:235], a[204:207], a[56:59]\n" \
"v_bitop3_b32 v212, v212, s35, v214 bitop3:0x36\n" \
"s_lshr_b32 s36, s2, 3\n" \
M_"a[52:55], a[164:167], a[140:143], a[52:55]\n" \
"v_add_u32_e32 v212, s34, v212\n" \
"s_mul_i32 s34, s5, 0x12000\n" \
M_"a[52:55], a[228:231], a[204:207], a[52:55]\n" \
X_"a[186:187], v38 offset:33792\n" \
"s_or_b32 s40, s34, 0x6000000\n" \
M_"a[48:51], a[160:163], a[140:143], a[48:51]\n" \
"s_mul_i32 s34, s36, 0x90000\n" \
"s_lshl_b32 s36, s3, 14\n" \
M_"a[48:51], a[224:227], a[204:207], a[48:51]\n" \
"s_add_i32 s34, s34, s36\n" \
"s_lshl_b32 s38, s5, 8\n" \
M_"a[64:67], a[160:163], a[144:147], a[64:67]\n" \
X_"a[248:249], v38 offset:40960\n" \
"s_add_i32 s41, s34, 0x4240000\n" \
M_"a[64:67], a[224:227], a[208:211], a[64:67]\n" \
"s_mul_i32 s34, s5, 36\n" \
"s_add_i32 s5, s7, s40\n" \
M_"a[68:71], a[164:167], a[144:147], a[68:71]\n" \
"s_addk_i32 s5, 0x2000\n" \
"s_mov_b32 s37, 2.0\n" \
M_"a[68:71], a[228:231], a[208:211], a[68:71]\n" \
X_"a[250:251], v38 offset:41984\n" \
"s_or_b32 s38, s38, 0x5be8000\n" \
M_"a[72:75], a[168:171], a[144:147], a[72:75]\n" \
"s_cmp_lg_u32 s3, 0\n" \
"s_cselect_b32 s36, s37, s5\n" \
M_"a[72:75], a[232:235], a[208:211], a[72:75]\n" \
"s_lshl_b32 s5, s6, 2\n" \
"s_or_b32 s5, s38, s5\n" \
M_"a[76:79], a[172:175], a[144:147], a[76:79]\n" \
"s_lshl_b32 s35, s3, 10\n" \
M_"a[76:79], a[236:239], a[208:211], a[76:79]\n" \
X_"a[188:189], v39 offset:32768\n" \
M_"a[92:95], a[172:175], a[148:151], a[92:95]\n" \
"s_cmpk_lt_u32 s3, 3\n" \
"s_cselect_b32 s37, s37, s5\n" \
M_"a[92:95], a[236:239], a[212:215], a[92:95]\n" \
"s_lshl_b32 s5, s4, 16\n" \
"s_lshl_b32 s4, s4, 11\n" \
M_"a[88:91], a[168:171], a[148:151], a[88:91]\n" \
X_"a[190:191], v39 offset:33792\n" \
"v_cndmask_b32_e64 v198, v213, v212, vcc\n" \
M_"a[88:91], a[232:235], a[212:215], a[88:91]\n" \
"v_or_b32_e32 v212, s35, v20\n" \
"s_or_b32 s39, s40, s35\n" \
M_"a[84:87], a[164:167], a[148:151], a[84:87]\n" \
"s_or_b32 s55, s41, s4\n" \
"v_or_b32_e32 v199, 0x22000, v212\n" \
M_"a[84:87], a[228:231], a[212:215], a[84:87]\n" \
X_"a[252:253], v39 offset:40960\n" \
"s_add_i32 s35, s39, s7\n" \
M_"a[80:83], a[160:163], a[148:151], a[80:83]\n" \
"s_add_i32 s54, s41, s5\n" \
"s_add_i32 s55, s55, 0x80000\n" \
M_"a[80:83], a[224:227], a[212:215], a[80:83]\n" \
"v_mov_b32_e32 v215, 0xffff\n" \
"s_and_b32 s4, s3, 1\n" \
M_"a[96:99], a[160:163], a[152:155], a[96:99]\n" \
X_"a[254:255], v39 offset:41984\n" \
"s_lshl_b32 s5, s4, 4\n" \
M_"a[96:99], a[224:227], a[216:219], a[96:99]\n" \
"v_lshrrev_b32_e32 v212, 4, v20\n" \
"v_lshrrev_b32_e64 v218, 6, v20\n" \
M_"a[100:103], a[164:167], a[152:155], a[100:103]\n" \
"v_and_b32_e64 v219, 12, v218\n" \
"v_and_or_b32 v214, v212, 15, s5\n" \
M_"a[100:103], a[228:231], a[216:219], a[100:103]\n" \
"v_cmp_gt_u32_e64 vcc, v219, v214\n" \
"v_mov_b32_e32 v220, 0xffff0000\n" \
M_"a[104:107], a[168:171], a[152:155], a[104:107]\n" \
"s_lshl_b32 s5, s2, 2\n" \
"v_cndmask_b32_e64 v217, v215, 0, vcc\n" \
M_"a[104:107], a[232:235], a[216:219], a[104:107]\n" \
"v_cmp_lt_u32_e32 vcc, v219, v214\n" \
"v_mov_b32_e64 v213, s23\n" \
M_"a[108:111], a[172:175], a[152:155], a[108:111]\n" \
"s_or_b32 s5, s5, s3\n" \
"v_cndmask_b32_e32 v221, 0, v220, vcc\n" \
M_"a[108:111], a[236:239], a[216:219], a[108:111]\n" \
"v_or_b32_e32 v12, v217, v221\n" \
"v_or_b32_e32 v217, 2, v219\n" \
"s_waitcnt lgkmcnt(15)\n" \
M_"a[124:127], a[172:175], a[156:159], a[124:127]\n" \
"v_cmp_gt_u32_e32 vcc, v217, v214\n" \
"v_mov_b32_e64 v216, s25\n" \
M_"a[124:127], a[236:239], a[220:223], a[124:127]\n" \
"s_movk_i32 s40, 0xf400\n" \
"v_cndmask_b32_e64 v221, v215, 0, vcc\n" \
M_"a[120:123], a[168:171], a[156:159], a[120:123]\n" \
"v_cmp_lt_u32_e32 vcc, v217, v214\n" \
"s_lshl_b32 s4, s4, 6\n" \
M_"a[120:123], a[232:235], a[220:223], a[120:123]\n" \
"v_cndmask_b32_e32 v217, 0, v220, vcc\n" \
"v_or_b32_e64 v13, v221, v217\n" \
M_"a[116:119], a[164:167], a[156:159], a[116:119]\n" \
"v_or_b32_e32 v217, 16, v219\n" \
"v_cmp_gt_u32_e32 vcc, v217, v214\n" \
M_"a[116:119], a[228:231], a[220:223], a[116:119]\n" \
"s_nop 0\n" \
"v_cndmask_b32_e64 v221, v215, 0, vcc\n" \
"v_cmp_lt_u32_e32 vcc, v217, v214\n" \
M_"a[112:115], a[160:163], a[156:159], a[112:115]\n" \
"s_nop 0\n" \
"v_cndmask_b32_e32 v217, 0, v220, vcc\n" \
"v_or_b32_e64 v14, v221, v217\n" \
M_"a[112:115], a[224:227], a[220:223], a[112:115]\n" \
"s_waitcnt vmcnt(20)\n" \
"v_readfirstlane_b32 s62, v180\n" \
"s_nop 3\n" \
"s_add_u32 s62, s62, 0x9E3779B1\n" \
"s_waitcnt vmcnt(2) lgkmcnt(0)\n" \
"s_barrier\n" \
M_"v[52:55], a[176:179], a[128:131], v[52:55]\n" \
"s_add_u32 m0, s30, 0x8000\n" \
"s_movk_i32 s84, 0x180\n" \
M_"v[52:55], a[240:243], a[192:195], v[52:55]\n" \
B_"v22, s[64:67], s84 offen offset:0 lds\n" \
M_"v[56:59], a[180:183], a[128:131], v[56:59]\n" \
X_"a[160:161], v32 offset:0\n" \
"v_or_b32_e64 v217, 18, v219\n" \
M_"v[56:59], a[244:247], a[192:195], v[56:59]\n" \
"s_movk_i32 s84, 0x1d80\n" \
M_"v[60:63], a[184:187], a[128:131], v[60:63]\n" \
B_"v23, s[64:67], s84 offen offset:1024 lds\n" \
M_"v[60:63], a[248:251], a[192:195], v[60:63]\n" \
X_"a[162:163], v32 offset:1024\n" \
"v_cmp_gt_u32_e32 vcc, v217, v214\n" \
M_"v[64:67], a[188:191], a[128:131], v[64:67]\n" \
X_"a[224:225], v32 offset:8192\n" \
"v_cndmask_b32_e64 v215, v215, 0, vcc\n" \
M_"v[64:67], a[252:255], a[192:195], v[64:67]\n" \
R_"a[128:131], v28 offset:16384\n" \
"v_cmp_lt_u32_e64 vcc, v217, v214\n" \
M_"v[80:83], a[188:191], a[132:135], v[80:83]\n" \
R_"a[192:195], v29 offset:16384\n" \
"v_cndmask_b32_e64 v214, 0, v220, vcc\n" \
M_"v[80:83], a[252:255], a[196:199], v[80:83]\n" \
"s_movk_i32 s84, 0x3980\n" \
M_"v[76:79], a[184:187], a[132:135], v[76:79]\n" \
B_"v22, s[64:67], s84 offen offset:2048 lds\n" \
M_"v[76:79], a[248:251], a[196:199], v[76:79]\n" \
"s_movk_i32 s84, 0x5580\n" \
M_"v[72:75], a[180:183], a[132:135], v[72:75]\n" \
B_"v23, s[64:67], s84 offen offset:3072 lds\n" \
M_"v[72:75], a[244:247], a[196:199], v[72:75]\n" \
"s_add_u32 m0, s31, 0x8000\n" \
"s_mov_b32 s84, 0x180000\n" \
M_"v[68:71], a[176:179], a[132:135], v[68:71]\n" \
B_"v26, s[68:71], s84 offen offset:0 lds\n" \
M_"v[68:71], a[240:243], a[196:199], v[68:71]\n" \
R_"a[132:135], v28 offset:18432\n" \
"v_or_b32_e64 v15, v215, v214\n" \
M_"v[84:87], a[176:179], a[136:139], v[84:87]\n" \
R_"a[196:199], v29 offset:18432\n" \
"v_mad_u64_u32 v[214:215], s[6:7], s5, 42, v[212:213]\n" \
M_"v[84:87], a[240:243], a[200:203], v[84:87]\n" \
"s_addk_i32 s84, 0x7c00\n" \
M_"v[88:91], a[180:183], a[136:139], v[88:91]\n" \
B_"v26, s[68:71], s84 offen offset:1024 lds\n" \
M_"v[88:91], a[244:247], a[200:203], v[88:91]\n" \
X_"a[226:227], v32 offset:9216\n" \
"s_movk_i32 s5, 0x1440\n" \
M_"v[92:95], a[184:187], a[136:139], v[92:95]\n" \
"s_addk_i32 s84, 0x7c00\n" \
M_"v[92:95], a[248:251], a[200:203], v[92:95]\n" \
B_"v27, s[68:71], s84 offen offset:2048 lds\n" \
M_"v[96:99], a[188:191], a[136:139], v[96:99]\n" \
X_"a[164:165], v33 offset:0\n" \
"v_cmp_gt_u32_e32 vcc, s5, v214\n" \
M_"v[96:99], a[252:255], a[200:203], v[96:99]\n" \
R_"a[136:139], v28 offset:20480\n" \
"v_min_u32_e32 v217, 0xa43f, v214\n" \
M_"v[112:115], a[188:191], a[140:143], v[112:115]\n" \
R_"a[200:203], v29 offset:20480\n" \
"v_mov_b32_e64 v214, s24\n" \
M_"v[112:115], a[252:255], a[204:207], v[112:115]\n" \
"s_addk_i32 s84, 0x7c00\n" \
M_"v[108:111], a[184:187], a[140:143], v[108:111]\n" \
B_"v27, s[68:71], s84 offen offset:3072 lds\n" \
M_"v[108:111], a[248:251], a[204:207], v[108:111]\n" \
"s_add_u32 m0, s30, 0x9000\n" \
"s_mov_b32 s84, 0x8180\n" \
M_"v[104:107], a[180:183], a[140:143], v[104:107]\n" \
B_"v22, s[64:67], s84 offen offset:0 lds\n" \
M_"v[104:107], a[244:247], a[204:207], v[104:107]\n" \
X_"a[166:167], v33 offset:1024\n" \
"v_cndmask_b32_e32 v215, v216, v213, vcc\n" \
M_"v[100:103], a[176:179], a[140:143], v[100:103]\n" \
X_"a[228:229], v33 offset:8192\n" \
"v_mov_b32_e64 v213, s22\n" \
M_"v[100:103], a[240:243], a[204:207], v[100:103]\n" \
R_"a[140:143], v28 offset:22528\n" \
"s_movk_i32 s6, 0xffee\n" \
M_"v[116:119], a[176:179], a[144:147], v[116:119]\n" \
R_"a[204:207], v29 offset:22528\n" \
"v_cndmask_b32_e32 v214, v214, v213, vcc\n" \
M_"v[116:119], a[240:243], a[208:211], v[116:119]\n" \
"s_addk_i32 s84, 0x1c00\n" \
M_"v[120:123], a[180:183], a[144:147], v[120:123]\n" \
B_"v23, s[64:67], s84 offen offset:1024 lds\n" \
M_"v[120:123], a[244:247], a[208:211], v[120:123]\n" \
"s_addk_i32 s84, 0x1c00\n" \
M_"v[124:127], a[184:187], a[144:147], v[124:127]\n" \
B_"v22, s[64:67], s84 offen offset:2048 lds\n" \
M_"v[124:127], a[248:251], a[208:211], v[124:127]\n" \
"s_addk_i32 s84, 0x1c00\n" \
M_"v[128:131], a[188:191], a[144:147], v[128:131]\n" \
B_"v23, s[64:67], s84 offen offset:3072 lds\n" \
M_"v[128:131], a[252:255], a[208:211], v[128:131]\n" \
R_"a[144:147], v28 offset:0\n" \
"v_add_u32_e32 v213, 0xffffebc0, v217\n" \
M_"v[144:147], a[188:191], a[148:151], v[144:147]\n" \
R_"a[208:211], v29 offset:0\n" \
"s_lshl_b32 s5, s2, 5\n" \
M_"v[144:147], a[252:255], a[212:215], v[144:147]\n" \
"s_add_u32 m0, s31, 0xc000\n" \
"s_mov_b32 s84, 0x180000\n" \
M_"v[140:143], a[184:187], a[148:151], v[140:143]\n" \
B_"v26, s[72:75], s84 offen offset:0 lds\n" \
M_"v[140:143], a[248:251], a[212:215], v[140:143]\n" \
X_"a[230:231], v33 offset:9216\n" \
"v_cndmask_b32_e64 v213, v213, v217, vcc\n" \
M_"v[136:139], a[180:183], a[148:151], v[136:139]\n" \
"s_addk_i32 s84, 0x7c00\n" \
M_"v[136:139], a[244:247], a[212:215], v[136:139]\n" \
B_"v26, s[72:75], s84 offen offset:1024 lds\n" \
M_"v[132:135], a[176:179], a[148:151], v[132:135]\n" \
X_"a[168:169], v34 offset:0\n" \
"v_lshlrev_b32_e32 v216, 7, v213\n" \
M_"v[132:135], a[240:243], a[212:215], v[132:135]\n" \
R_"a[148:151], v28 offset:2048\n" \
"v_mul_u32_u24_e32 v213, 0xe39, v212\n" \
M_"v[148:151], a[176:179], a[152:155], v[148:151]\n" \
R_"a[212:215], v29 offset:2048\n" \
"v_mov_b32_e64 v217, 0\n" \
M_"v[148:151], a[240:243], a[216:219], v[148:151]\n" \
X_"a[170:171], v34 offset:1024\n" \
"v_lshrrev_b32_e64 v213, 16, v213\n" \
M_"v[152:155], a[180:183], a[152:155], v[152:155]\n" \
X_"a[232:233], v34 offset:8192\n" \
"v_lshl_add_u64 v[16:17], v[214:215], 0, v[216:217]\n" \
M_"v[152:155], a[244:247], a[216:219], v[152:155]\n" \
X_"a[234:235], v34 offset:9216\n" \
"v_mad_i32_i24 v214, v213, s6, v212\n" \
M_"v[156:159], a[184:187], a[152:155], v[156:159]\n" \
X_"a[172:173], v35 offset:0\n" \
"s_add_i32 s5, s5, 32\n" \
M_"v[156:159], a[248:251], a[216:219], v[156:159]\n" \
X_"a[174:175], v35 offset:1024\n" \
"v_lshlrev_b32_e32 v215, 4, v214\n" \
M_"v[160:163], a[188:191], a[152:155], v[160:163]\n" \
X_"a[236:237], v35 offset:8192\n" \
"v_add_u32_e64 v216, s5, v215\n" \
M_"v[160:163], a[252:255], a[216:219], v[160:163]\n" \
R_"a[152:155], v28 offset:4096\n" \
"v_and_b32_e32 v216, 0xf0, v216\n" \
M_"v[176:179], a[188:191], a[156:159], v[176:179]\n" \
R_"a[216:219], v29 offset:4096\n" \
"v_cmp_gt_u32_e64 vcc, 16, v214\n" \
M_"v[176:179], a[252:255], a[220:223], v[176:179]\n" \
X_"a[238:239], v35 offset:9216\n" \
"s_movk_i32 s7, 0xf800\n" \
M_"v[172:175], a[184:187], a[156:159], v[172:175]\n" \
"v_cndmask_b32_e32 v214, v215, v216, vcc\n" \
"v_or_b32_e64 v215, 64, v212\n" \
M_"v[172:175], a[248:251], a[220:223], v[172:175]\n" \
"v_mul_u32_u24_e32 v216, 0xe39, v215\n" \
"v_lshrrev_b32_e64 v216, 16, v216\n" \
M_"v[168:171], a[180:183], a[156:159], v[168:171]\n" \
"v_mad_i32_i24 v215, v216, s6, v215\n" \
"v_lshlrev_b32_e64 v217, 4, v215\n" \
M_"v[168:171], a[244:247], a[220:223], v[168:171]\n" \
"v_add_u32_e64 v220, s5, v217\n" \
"v_and_b32_e32 v220, 0xf0, v220\n" \
M_"v[164:167], a[176:179], a[156:159], v[164:167]\n" \
"v_cmp_gt_u32_e32 vcc, 16, v215\n" \
"s_nop 1\n" \
"v_cndmask_b32_e64 v215, v217, v220, vcc\n" \
M_"v[164:167], a[240:243], a[220:223], v[164:167]\n" \
R_"a[156:159], v28 offset:6144\n" \
R_"a[220:223], v29 offset:6144\n" \
"s_waitcnt lgkmcnt(15)\n" \
M_"a[0:3], a[160:163], a[128:131], a[0:3]\n" \
"s_addk_i32 s84, 0x7c00\n" \
M_"a[0:3], a[224:227], a[192:195], a[0:3]\n" \
B_"v27, s[72:75], s84 offen offset:2048 lds\n" \
M_"a[4:7], a[164:167], a[128:131], a[4:7]\n" \
X_"a[176:177], v36 offset:0\n" \
"v_or_b32_e32 v217, 0x80, v212\n" \
"s_waitcnt lgkmcnt(15)\n" \
M_"a[4:7], a[228:231], a[192:195], a[4:7]\n" \
"s_addk_i32 s84, 0x7c00\n" \
"s_waitcnt lgkmcnt(11)\n" \
M_"a[8:11], a[168:171], a[128:131], a[8:11]\n" \
B_"v27, s[72:75], s84 offen offset:3072 lds\n" \
"s_waitcnt lgkmcnt(9)\n" \
M_"a[8:11], a[232:235], a[192:195], a[8:11]\n" \
X_"a[178:179], v36 offset:1024\n" \
"v_mul_u32_u24_e32 v220, 0xe39, v217\n" \
"s_waitcnt lgkmcnt(8)\n" \
M_"a[12:15], a[172:175], a[128:131], a[12:15]\n" \
X_"a[240:241], v36 offset:8192\n" \
"v_lshrrev_b32_e32 v220, 16, v220\n" \
"s_waitcnt lgkmcnt(5)\n" \
M_"a[12:15], a[236:239], a[192:195], a[12:15]\n" \
"v_mad_i32_i24 v217, v220, s6, v217\n" \
"v_lshlrev_b32_e32 v221, 4, v217\n" \
M_"a[28:31], a[172:175], a[132:135], a[28:31]\n" \
"v_add_u32_e64 v222, s5, v221\n" \
"v_and_b32_e32 v222, 0xf0, v222\n" \
M_"a[28:31], a[236:239], a[196:199], a[28:31]\n" \
X_"a[242:243], v36 offset:9216\n" \
"v_cmp_gt_u32_e64 vcc, 16, v217\n" \
M_"a[24:27], a[168:171], a[132:135], a[24:27]\n" \
B_"v[200:203], v192, s[76:79], s34 offen offset:0\n" \
M_"a[24:27], a[232:235], a[196:199], a[24:27]\n" \
"v_cndmask_b32_e64 v217, v221, v222, vcc\n" \
"v_or_b32_e32 v221, 0xc0, v212\n" \
M_"a[20:23], a[164:167], a[132:135], a[20:23]\n" \
"v_mul_u32_u24_e32 v222, 0xe39, v221\n" \
"v_lshrrev_b32_e64 v222, 16, v222\n" \
M_"a[20:23], a[228:231], a[196:199], a[20:23]\n" \
X_"a[180:181], v37 offset:0\n" \
"v_mad_i32_i24 v221, v222, s6, v221\n" \
M_"a[16:19], a[160:163], a[132:135], a[16:19]\n" \
"v_lshlrev_b32_e32 v223, 4, v221\n" \
"v_add_u32_e32 v224, s5, v223\n" \
M_"a[16:19], a[224:227], a[196:199], a[16:19]\n" \
"v_and_b32_e32 v224, 0xf0, v224\n" \
"v_cmp_gt_u32_e64 vcc, 16, v221\n" \
M_"a[32:35], a[160:163], a[136:139], a[32:35]\n" \
B_"v[204:207], v193, s[76:79], s34 offen offset:0\n" \
M_"a[32:35], a[224:227], a[200:203], a[32:35]\n" \
"s_mov_b32 m0, s32\n" \
X_"a[182:183], v37 offset:1024\n" \
M_"a[36:39], a[164:167], a[136:139], a[36:39]\n" \
"s_mov_b32 exec_lo, 0x0\n" \
"s_mov_b32 exec_hi, 0xff00\n" \
"global_load_lds_dword v[184:185], off\n" \
"s_mov_b64 exec, -1\n" \
M_"a[36:39], a[228:231], a[200:203], a[36:39]\n" \
X_"a[244:245], v37 offset:8192\n" \
"v_cndmask_b32_e32 v221, v223, v224, vcc\n" \
M_"a[40:43], a[168:171], a[136:139], a[40:43]\n" \
"v_or_b32_e32 v223, 0x100, v212\n" \
"v_mul_u32_u24_e32 v224, 0xe39, v223\n" \
M_"a[40:43], a[232:235], a[200:203], a[40:43]\n" \
"buffer_load_dword v208, v194, s[76:79], s34 offen offset:32\n" \
M_"a[44:47], a[172:175], a[136:139], a[44:47]\n" \
X_"a[246:247], v37 offset:9216\n" \
"v_lshrrev_b32_e64 v224, 16, v224\n" \
M_"a[44:47], a[236:239], a[200:203], a[44:47]\n" \
"v_mad_i32_i24 v223, v224, s6, v223\n" \
"v_lshlrev_b32_e64 v225, 4, v223\n" \
M_"a[60:63], a[172:175], a[140:143], a[60:63]\n" \
"v_add_u32_e64 v226, s5, v225\n" \
"v_and_b32_e32 v226, 0xf0, v226\n" \
M_"a[60:63], a[236:239], a[204:207], a[60:63]\n" \
"v_cmp_gt_u32_e32 vcc, 16, v223\n" \
"s_nop 1\n" \
"v_cndmask_b32_e64 v223, v225, v226, vcc\n" \
M_"a[56:59], a[168:171], a[140:143], a[56:59]\n" \
"buffer_load_dword v209, v194, s[76:79], s34 offen offset:1184\n" \
M_"a[56:59], a[232:235], a[204:207], a[56:59]\n" \
X_"a[184:185], v38 offset:0\n" \
"v_or_b32_e32 v225, 0x140, v212\n" \
M_"a[52:55], a[164:167], a[140:143], a[52:55]\n" \
"v_mul_u32_u24_e32 v226, 0xe39, v225\n" \
"v_lshrrev_b32_e64 v226, 16, v226\n" \
M_"a[52:55], a[228:231], a[204:207], a[52:55]\n" \
X_"a[186:187], v38 offset:1024\n" \
"v_mad_i32_i24 v225, v226, s6, v225\n" \
M_"a[48:51], a[160:163], a[140:143], a[48:51]\n" \
"v_lshlrev_b32_e32 v227, 4, v225\n" \
"v_add_u32_e32 v228, s5, v227\n" \
M_"a[48:51], a[224:227], a[204:207], a[48:51]\n" \
"v_and_b32_e32 v228, 0xf0, v228\n" \
"v_cmp_gt_u32_e64 vcc, 16, v225\n" \
M_"a[64:67], a[160:163], a[144:147], a[64:67]\n" \
"buffer_load_dword v211, v194, s[76:79], s34 offen offset:2336\n" \
M_"a[64:67], a[224:227], a[208:211], a[64:67]\n" \
X_"a[248:249], v38 offset:8192\n" \
"v_cndmask_b32_e64 v225, v227, v228, vcc\n" \
M_"a[68:71], a[164:167], a[144:147], a[68:71]\n" \
"v_or_b32_e32 v227, 0x180, v212\n" \
"v_mul_u32_u24_e32 v228, 0xe39, v227\n" \
M_"a[68:71], a[228:231], a[208:211], a[68:71]\n" \
X_"a[250:251], v38 offset:9216\n" \
"v_lshrrev_b32_e64 v228, 16, v228\n" \
M_"a[72:75], a[168:171], a[144:147], a[72:75]\n" \
"v_mad_i32_i24 v227, v228, s6, v227\n" \
"v_lshlrev_b32_e64 v229, 4, v227\n" \
M_"a[72:75], a[232:235], a[208:211], a[72:75]\n" \
"s_mov_b32 s76, s20\n" \
"s_and_b32 s77, s21, 0xffff\n" \
M_"a[76:79], a[172:175], a[144:147], a[76:79]\n" \
"s_mov_b32 s78, 0x480\n" \
"v_add_u32_e32 v230, s5, v229\n" \
M_"a[76:79], a[236:239], a[208:211], a[76:79]\n" \
"buffer_load_ushort v210, v195, s[76:79], 0 offen\n" \
M_"a[92:95], a[172:175], a[148:151], a[92:95]\n" \
X_"a[188:189], v39 offset:0\n" \
"v_and_b32_e32 v230, 0xf0, v230\n" \
M_"a[92:95], a[236:239], a[212:215], a[92:95]\n" \
"v_cmp_gt_u32_e32 vcc, 16, v227\n" \
"s_nop 1\n" \
"v_cndmask_b32_e64 v227, v229, v230, vcc\n" \
M_"a[88:91], a[168:171], a[148:151], a[88:91]\n" \
"s_mov_b32 s76, s8\n" \
"s_and_b32 s77, s9, 0xffff\n" \
M_"a[88:91], a[232:235], a[212:215], a[88:91]\n" \
"s_mov_b32 s78, 0x240000\n" \
X_"a[190:191], v39 offset:1024\n" \
M_"a[84:87], a[164:167], a[148:151], a[84:87]\n" \
"v_or_b32_e32 v229, 0x1c0, v212\n" \
"v_mul_u32_u24_e32 v230, 0xe39, v229\n" \
M_"a[84:87], a[228:231], a[212:215], a[84:87]\n" \
".fill (L_pb + 7904 - .) / 4, 4, 0xBF800000\n" \
"L_pp2:\n" \
"s_cbranch_execz L_pret\n" \
X_"a[252:253], v39 offset:8192\n" \
"v_lshrrev_b32_e32 v230, 16, v230\n" \
M_"a[80:83], a[160:163], a[148:151], a[80:83]\n" \
"v_mad_i32_i24 v229, v230, s6, v229\n" \
"v_lshlrev_b32_e64 v231, 4, v229\n" \
M_"a[80:83], a[224:227], a[212:215], a[80:83]\n" \
"v_add_u32_e64 v232, s5, v231\n" \
"v_and_b32_e32 v232, 0xf0, v232\n" \
M_"a[96:99], a[160:163], a[152:155], a[96:99]\n" \
X_"a[254:255], v39 offset:9216\n" \
"v_cmp_gt_u32_e64 vcc, 16, v229\n" \
M_"a[96:99], a[224:227], a[216:219], a[96:99]\n" \
"s_nop 0\n" \
"v_cndmask_b32_e32 v229, v231, v232, vcc\n" \
"v_or_b32_e32 v231, 0x200, v212\n" \
M_"a[100:103], a[164:167], a[152:155], a[100:103]\n" \
"v_mul_u32_u24_e32 v232, 0xe39, v231\n" \
"v_lshrrev_b32_e64 v232, 16, v232\n" \
M_"a[100:103], a[228:231], a[216:219], a[100:103]\n" \
"v_mad_i32_i24 v231, v232, s6, v231\n" \
"v_lshlrev_b32_e64 v233, 4, v231\n" \
M_"a[104:107], a[168:171], a[152:155], a[104:107]\n" \
"v_add_u32_e32 v234, s5, v233\n" \
"s_movk_i32 s5, 0x480\n" \
M_"a[104:107], a[232:235], a[216:219], a[104:107]\n" \
"v_mad_u32_u24 v0, v213, s5, v214\n" \
"v_mul_u32_u24_e32 v213, 0x480, v216\n" \
M_"a[108:111], a[172:175], a[152:155], a[108:111]\n" \
"s_movk_i32 s6, 0xfc00\n" \
"v_add3_u32 v1, v213, v215, s6\n" \
M_"a[108:111], a[236:239], a[216:219], a[108:111]\n" \
"v_mul_u32_u24_e32 v213, 0x480, v220\n" \
"v_add3_u32 v2, v213, v217, s7\n" \
"s_waitcnt lgkmcnt(15)\n" \
M_"a[124:127], a[172:175], a[156:159], a[124:127]\n" \
"v_mul_u32_u24_e32 v213, 0x480, v222\n" \
"v_add3_u32 v3, v213, v221, s40\n" \
M_"a[124:127], a[236:239], a[220:223], a[124:127]\n" \
"v_mul_u32_u24_e32 v213, 0x480, v226\n" \
"v_add3_u32 v5, v213, v225, s6\n" \
M_"a[120:123], a[168:171], a[156:159], a[120:123]\n" \
"v_mul_u32_u24_e32 v213, 0x480, v228\n" \
"v_add3_u32 v6, v213, v227, s7\n" \
M_"a[120:123], a[232:235], a[220:223], a[120:123]\n" \
"v_mul_u32_u24_e32 v213, 0x480, v230\n" \
"v_add3_u32 v7, v213, v229, s40\n" \
M_"a[116:119], a[164:167], a[156:159], a[116:119]\n" \
"s_lshl_b32 s7, s3, 3\n" \
"s_lshl_b32 s40, s3, 1\n" \
M_"a[116:119], a[228:231], a[220:223], a[116:119]\n" \
"s_and_b32 s7, s7, 16\n" \
"s_and_b32 s40, s40, 2\n" \
M_"a[112:115], a[160:163], a[156:159], a[112:115]\n" \
"s_and_b32 s6, s2, 7\n" \
"v_bfe_u32 v213, v20, 7, 1\n" \
"s_waitcnt vmcnt(7) lgkmcnt(0)\n" \
M_"a[112:115], a[224:227], a[220:223], a[112:115]\n" \
"s_barrier\n" \
M_"v[52:55], a[176:179], a[128:131], v[52:55]\n" \
"s_add_u32 m0, s30, 0x0\n" \
"s_movk_i32 s84, 0x200\n" \
M_"v[52:55], a[240:243], a[192:195], v[52:55]\n" \
B_"v22, s[64:67], s84 offen offset:0 lds\n" \
M_"v[56:59], a[180:183], a[128:131], v[56:59]\n" \
X_"a[160:161], v32 offset:32768\n" \
"s_or_b32 s7, s40, s7\n" \
M_"v[56:59], a[244:247], a[192:195], v[56:59]\n" \
"s_movk_i32 s84, 0x1e00\n" \
M_"v[60:63], a[184:187], a[128:131], v[60:63]\n" \
B_"v23, s[64:67], s84 offen offset:1024 lds\n" \
M_"v[60:63], a[248:251], a[192:195], v[60:63]\n" \
X_"a[162:163], v32 offset:33792\n" \
"v_and_b32_e32 v234, 0xf0, v234\n" \
M_"v[64:67], a[188:191], a[128:131], v[64:67]\n" \
X_"a[224:225], v32 offset:40960\n" \
"v_cmp_gt_u32_e32 vcc, 16, v231\n" \
M_"v[64:67], a[252:255], a[192:195], v[64:67]\n" \
R_"a[128:131], v28 offset:49152\n" \
"v_or3_b32 v213, s7, v213, v219\n" \
M_"v[80:83], a[188:191], a[132:135], v[80:83]\n" \
R_"a[192:195], v29 offset:49152\n" \
"v_and_b32_e64 v214, 32, v20\n" \
M_"v[80:83], a[252:255], a[196:199], v[80:83]\n" \
"s_movk_i32 s84, 0x3a00\n" \
M_"v[76:79], a[184:187], a[132:135], v[76:79]\n" \
B_"v22, s[64:67], s84 offen offset:2048 lds\n" \
M_"v[76:79], a[248:251], a[196:199], v[76:79]\n" \
"s_movk_i32 s84, 0x5600\n" \
M_"v[72:75], a[180:183], a[132:135], v[72:75]\n" \
B_"v23, s[64:67], s84 offen offset:3072 lds\n" \
M_"v[72:75], a[244:247], a[196:199], v[72:75]\n" \
"s_add_u32 m0, s31, 0x0\n" \
"s_mov_b32 s84, 0x200000\n" \
M_"v[68:71], a[176:179], a[132:135], v[68:71]\n" \
B_"v26, s[68:71], s84 offen offset:0 lds\n" \
M_"v[68:71], a[240:243], a[196:199], v[68:71]\n" \
R_"a[132:135], v28 offset:51200\n" \
"s_lshl_b32 s6, s6, 5\n" \
M_"v[84:87], a[176:179], a[136:139], v[84:87]\n" \
R_"a[196:199], v29 offset:51200\n" \
"v_cndmask_b32_e64 v231, v233, v234, vcc\n" \
M_"v[84:87], a[240:243], a[200:203], v[84:87]\n" \
"s_addk_i32 s84, 0x7c00\n" \
M_"v[88:91], a[180:183], a[136:139], v[88:91]\n" \
B_"v26, s[68:71], s84 offen offset:1024 lds\n" \
M_"v[88:91], a[244:247], a[200:203], v[88:91]\n" \
X_"a[226:227], v32 offset:41984\n" \
"v_mul_u32_u24_e32 v213, 18, v213\n" \
M_"v[92:95], a[184:187], a[136:139], v[92:95]\n" \
"s_addk_i32 s84, 0x7c00\n" \
M_"v[92:95], a[248:251], a[200:203], v[92:95]\n" \
B_"v27, s[68:71], s84 offen offset:2048 lds\n" \
M_"v[96:99], a[188:191], a[136:139], v[96:99]\n" \
X_"a[164:165], v33 offset:32768\n" \
"v_mov_b32_e32 v215, 0x100\n" \
M_"v[96:99], a[252:255], a[200:203], v[96:99]\n" \
R_"a[136:139], v28 offset:53248\n" \
"v_mov_b32_e32 v216, s6\n" \
M_"v[112:115], a[188:191], a[140:143], v[112:115]\n" \
R_"a[200:203], v29 offset:53248\n" \
"v_cmp_eq_u32_e64 vcc, 0, v214\n" \
M_"v[112:115], a[252:255], a[204:207], v[112:115]\n" \
"s_addk_i32 s84, 0x7c00\n" \
"s_nop 0\n" \
M_"v[108:111], a[184:187], a[140:143], v[108:111]\n" \
B_"v27, s[68:71], s84 offen offset:3072 lds\n" \
M_"v[108:111], a[248:251], a[204:207], v[108:111]\n" \
"s_add_u32 m0, s30, 0x1000\n" \
"s_mov_b32 s84, 0x8200\n" \
M_"v[104:107], a[180:183], a[140:143], v[104:107]\n" \
B_"v22, s[64:67], s84 offen offset:0 lds\n" \
M_"v[104:107], a[244:247], a[204:207], v[104:107]\n" \
X_"a[166:167], v33 offset:33792\n" \
"v_and_or_b32 v213, v218, 1, v213\n" \
M_"v[100:103], a[176:179], a[140:143], v[100:103]\n" \
X_"a[228:229], v33 offset:40960\n" \
"v_lshlrev_b32_e64 v212, 3, v212\n" \
M_"v[100:103], a[240:243], a[204:207], v[100:103]\n" \
R_"a[140:143], v28 offset:55296\n" \
"v_cndmask_b32_e64 v214, v215, v216, vcc\n" \
M_"v[116:119], a[176:179], a[144:147], v[116:119]\n" \
R_"a[204:207], v29 offset:55296\n" \
"v_mad_u32_u24 v213, v213, s5, v214\n" \
M_"v[116:119], a[240:243], a[208:211], v[116:119]\n" \
"s_addk_i32 s84, 0x1c00\n" \
M_"v[120:123], a[180:183], a[144:147], v[120:123]\n" \
B_"v23, s[64:67], s84 offen offset:1024 lds\n" \
M_"v[120:123], a[244:247], a[208:211], v[120:123]\n" \
"s_addk_i32 s84, 0x1c00\n" \
M_"v[124:127], a[184:187], a[144:147], v[124:127]\n" \
B_"v22, s[64:67], s84 offen offset:2048 lds\n" \
M_"v[124:127], a[248:251], a[208:211], v[124:127]\n" \
"s_addk_i32 s84, 0x1c00\n" \
"s_nop 0\n" \
M_"v[128:131], a[188:191], a[144:147], v[128:131]\n" \
B_"v23, s[64:67], s84 offen offset:3072 lds\n" \
M_"v[128:131], a[252:255], a[208:211], v[128:131]\n" \
R_"a[144:147], v28 offset:32768\n" \
"v_and_or_b32 v213, v20, 16, v213\n" \
M_"v[144:147], a[188:191], a[148:151], v[144:147]\n" \
R_"a[208:211], v29 offset:32768\n" \
"v_add_u32_e32 v9, 0x4800, v213\n" \
M_"v[144:147], a[252:255], a[212:215], v[144:147]\n" \
"s_add_u32 m0, s31, 0x4000\n" \
"s_mov_b32 s84, 0x200000\n" \
M_"v[140:143], a[184:187], a[148:151], v[140:143]\n" \
B_"v26, s[72:75], s84 offen offset:0 lds\n" \
M_"v[140:143], a[248:251], a[212:215], v[140:143]\n" \
X_"a[230:231], v33 offset:41984\n" \
"v_mul_u32_u24_e32 v213, 0x120, v218\n" \
M_"v[136:139], a[180:183], a[148:151], v[136:139]\n" \
"s_addk_i32 s84, 0x7c00\n" \
M_"v[136:139], a[244:247], a[212:215], v[136:139]\n" \
B_"v26, s[72:75], s84 offen offset:1024 lds\n" \
M_"v[132:135], a[176:179], a[148:151], v[132:135]\n" \
X_"a[168:169], v34 offset:32768\n" \
"v_and_or_b32 v10, v212, 24, v213\n" \
M_"v[132:135], a[240:243], a[212:215], v[132:135]\n" \
R_"a[148:151], v28 offset:34816\n" \
"v_lshrrev_b32_e32 v213, 2, v20\n" \
M_"v[148:151], a[176:179], a[152:155], v[148:151]\n" \
R_"a[212:215], v29 offset:34816\n" \
"v_and_or_b32 v213, v213, 60, s4\n" \
M_"v[148:151], a[240:243], a[216:219], v[148:151]\n" \
X_"a[170:171], v34 offset:33792\n" \
"v_lshl_or_b32 v212, s3, 9, v212\n" \
M_"v[152:155], a[180:183], a[152:155], v[152:155]\n" \
X_"a[232:233], v34 offset:40960\n" \
"v_mad_u32_u24 v4, v224, s5, v223\n" \
M_"v[152:155], a[244:247], a[216:219], v[152:155]\n" \
X_"a[234:235], v34 offset:41984\n" \
"v_mad_u32_u24 v8, v232, s5, v231\n" \
M_"v[156:159], a[184:187], a[152:155], v[156:159]\n" \
X_"a[172:173], v35 offset:32768\n" \
"v_or_b32_e32 v18, 0x27000, v213\n" \
M_"v[156:159], a[248:251], a[216:219], v[156:159]\n" \
X_"a[174:175], v35 offset:33792\n" \
"v_or_b32_e32 v19, 0x27800, v212\n" \
M_"v[160:163], a[188:191], a[152:155], v[160:163]\n" \
X_"a[236:237], v35 offset:40960\n" \
"v_mov_b32_e32 v215, 0x1000c0c\n" \
M_"v[160:163], a[252:255], a[216:219], v[160:163]\n" \
R_"a[152:155], v28 offset:36864\n" \
"v_mov_b32_e32 v216, 0xc0c0100\n" \
M_"v[176:179], a[188:191], a[156:159], v[176:179]\n" \
R_"a[216:219], v29 offset:36864\n" \
"v_bfe_u32 v214, v20, 4, 1\n" \
M_"v[176:179], a[252:255], a[220:223], v[176:179]\n" \
X_"a[238:239], v35 offset:41984\n" \
"v_lshrrev_b32_e64 v212, 8, v20\n" \
M_"v[172:175], a[184:187], a[156:159], v[172:175]\n" \
"v_cmp_eq_u32_e64 vcc, 0, v214\n" \
"v_bfe_u32 v213, v20, 4, 4\n" \
M_"v[172:175], a[248:251], a[220:223], v[172:175]\n" \
"v_mov_b32_e32 v217, 0xc0c0302\n" \
"v_cndmask_b32_e64 v215, v215, v216, vcc\n" \
M_"v[168:171], a[180:183], a[156:159], v[168:171]\n" \
"v_mov_b32_e32 v216, 0x3020c0c\n" \
"v_lshl_or_b32 v214, v212, 2, v214\n" \
M_"v[168:171], a[244:247], a[220:223], v[168:171]\n" \
"v_cndmask_b32_e64 v216, v216, v217, vcc\n" \
"v_mov_b32_e32 v217, 0xc0c0c0c\n" \
M_"v[164:167], a[176:179], a[156:159], v[164:167]\n" \
"v_cmp_gt_u32_e32 vcc, v214, v213\n" \
"v_or_b32_e32 v214, 2, v214\n" \
M_"v[164:167], a[240:243], a[220:223], v[164:167]\n" \
R_"a[156:159], v28 offset:38912\n" \
R_"a[220:223], v29 offset:38912\n" \
"s_waitcnt lgkmcnt(15)\n" \
M_"a[0:3], a[160:163], a[128:131], a[0:3]\n" \
"s_addk_i32 s84, 0x7c00\n" \
M_"a[0:3], a[224:227], a[192:195], a[0:3]\n" \
B_"v27, s[72:75], s84 offen offset:2048 lds\n" \
M_"a[4:7], a[164:167], a[128:131], a[4:7]\n" \
X_"a[176:177], v36 offset:32768\n" \
"s_lshr_b32 s4, s2, 5\n" \
"s_waitcnt lgkmcnt(15)\n" \
M_"a[4:7], a[228:231], a[192:195], a[4:7]\n" \
"s_addk_i32 s84, 0x7c00\n" \
"s_waitcnt lgkmcnt(11)\n" \
M_"a[8:11], a[168:171], a[128:131], a[8:11]\n" \
B_"v27, s[72:75], s84 offen offset:3072 lds\n" \
"s_waitcnt lgkmcnt(9)\n" \
M_"a[8:11], a[232:235], a[192:195], a[8:11]\n" \
X_"a[178:179], v36 offset:33792\n" \
"v_cndmask_b32_e32 v40, v217, v215, vcc\n" \
"s_waitcnt lgkmcnt(8)\n" \
M_"a[12:15], a[172:175], a[128:131], a[12:15]\n" \
X_"a[240:241], v36 offset:40960\n" \
"v_cndmask_b32_e32 v42, v215, v217, vcc\n" \
"s_waitcnt lgkmcnt(5)\n" \
M_"a[12:15], a[236:239], a[192:195], a[12:15]\n" \
"v_cmp_gt_u32_e32 vcc, v214, v213\n" \
"v_and_b32_e64 v214, 16, v20\n" \
M_"a[28:31], a[172:175], a[132:135], a[28:31]\n" \
"s_movk_i32 s42, 0x3f80\n" \
"s_xor_b32 s5, s4, 15\n" \
M_"a[28:31], a[236:239], a[196:199], a[28:31]\n" \
X_"a[242:243], v36 offset:41984\n" \
M_"a[24:27], a[168:171], a[132:135], a[24:27]\n" \
"s_cmpk_lt_u32 s3, 2\n" \
"s_cselect_b32 s6, s5, s4\n" \
M_"a[24:27], a[232:235], a[196:199], a[24:27]\n" \
"s_lshl_b32 s7, s6, 7\n" \
"v_and_b32_e32 v213, 0x320, v20\n" \
M_"a[20:23], a[164:167], a[132:135], a[20:23]\n" \
"s_movk_i32 s41, 0x100\n" \
"v_lshlrev_b32_e64 v214, v214, s42\n" \
M_"a[20:23], a[228:231], a[196:199], a[20:23]\n" \
X_"a[180:181], v37 offset:32768\n" \
"v_mov_b32_e64 v215, 0\n" \
M_"a[16:19], a[160:163], a[132:135], a[16:19]\n" \
"s_add_i32 s40, s7, -16\n" \
"v_cndmask_b32_e32 v41, v217, v216, vcc\n" \
M_"a[16:19], a[224:227], a[196:199], a[16:19]\n" \
"v_cndmask_b32_e32 v43, v216, v217, vcc\n" \
"v_cmp_eq_u32_e32 vcc, s41, v213\n" \
M_"a[32:35], a[160:163], a[136:139], a[32:35]\n" \
X_"a[182:183], v37 offset:33792\n" \
"s_mov_b32 s41, 0\n" \
M_"a[32:35], a[224:227], a[200:203], a[32:35]\n" \
"s_and_b32 s43, s3, 1\n" \
"v_cndmask_b32_e64 v46, v215, v214, vcc\n" \
M_"a[36:39], a[164:167], a[136:139], a[36:39]\n" \
"v_and_b32_e32 v213, 0x330, v20\n" \
M_"a[36:39], a[228:231], a[200:203], a[36:39]\n" \
X_"a[244:245], v37 offset:40960\n" \
M_"a[40:43], a[168:171], a[136:139], a[40:43]\n" \
"s_mov_b32 m0, s32\n" \
M_"a[40:43], a[232:235], a[200:203], a[40:43]\n" \
"s_mov_b32 exec_lo, 0xffffffff\n" \
"s_mov_b32 exec_hi, 0x0\n" \
"global_load_lds_dword v[188:189], off\n" \
"s_mov_b64 exec, -1\n" \
M_"a[44:47], a[172:175], a[136:139], a[44:47]\n" \
X_"a[246:247], v37 offset:41984\n" \
M_"a[44:47], a[236:239], a[200:203], a[44:47]\n" \
"s_cmp_lg_u32 s7, 0\n" \
"s_cselect_b32 s40, s40, s41\n" \
M_"a[60:63], a[172:175], a[140:143], a[60:63]\n" \
"s_mov_b32 s42, 0xff800000\n" \
M_"a[60:63], a[236:239], a[204:207], a[60:63]\n" \
"s_cmp_lg_u32 s7, 0\n" \
"s_cselect_b32 s47, s41, s42\n" \
M_"a[56:59], a[168:171], a[140:143], a[56:59]\n" \
X_"a[184:185], v38 offset:32768\n" \
"s_lshl_b32 s41, s43, 6\n" \
M_"a[56:59], a[232:235], a[204:207], a[56:59]\n" \
"v_lshrrev_b32_e32 v215, 6, v20\n" \
"v_mov_b32_e32 v214, 0x3f80\n" \
M_"a[52:55], a[164:167], a[140:143], a[52:55]\n" \
"v_cmp_eq_u32_e32 vcc, 32, v213\n" \
"s_or_b32 s7, s41, s7\n" \
M_"a[52:55], a[228:231], a[204:207], a[52:55]\n" \
X_"a[186:187], v38 offset:33792\n" \
"v_lshrrev_b32_e64 v213, 7, v20\n" \
M_"a[48:51], a[160:163], a[140:143], a[48:51]\n" \
"v_bfe_u32 v216, v20, 6, 2\n" \
"v_lshrrev_b32_e64 v219, 1, v20\n" \
M_"a[48:51], a[224:227], a[204:207], a[48:51]\n" \
"v_bfe_u32 v215, v215, 1, 1\n" \
"v_cndmask_b32_e64 v47, 0, v214, vcc\n" \
M_"a[64:67], a[160:163], a[144:147], a[64:67]\n" \
X_"a[248:249], v38 offset:40960\n" \
"v_or_b32_e64 v214, s7, v213\n" \
M_"a[64:67], a[224:227], a[208:211], a[64:67]\n" \
"v_lshl_or_b32 v217, s43, 3, v213\n" \
"v_lshlrev_b32_e64 v218, 10, v212\n" \
M_"a[68:71], a[164:167], a[144:147], a[68:71]\n" \
"v_lshlrev_b32_e32 v216, 7, v216\n" \
"v_and_b32_e32 v219, 24, v219\n" \
M_"a[68:71], a[228:231], a[208:211], a[68:71]\n" \
X_"a[250:251], v38 offset:41984\n" \
"v_and_or_b32 v213, v213, 2, v215\n" \
M_"a[72:75], a[168:171], a[144:147], a[72:75]\n" \
"v_add_u32_e64 v217, s40, v217\n" \
"v_or3_b32 v216, v216, v218, v219\n" \
M_"a[72:75], a[232:235], a[208:211], a[72:75]\n" \
"v_lshlrev_b32_e64 v213, 5, v213\n" \
"s_mov_b32 s40, 0x14000\n" \
M_"a[76:79], a[172:175], a[144:147], a[76:79]\n" \
"s_lshl_b32 s7, s2, 2\n" \
"v_or3_b32 v48, v216, v213, s40\n" \
M_"a[76:79], a[236:239], a[208:211], a[76:79]\n" \
X_"a[188:189], v39 offset:32768\n" \
"s_lshl_b32 s40, s43, 2\n" \
M_"a[92:95], a[172:175], a[148:151], a[92:95]\n" \
"s_lshl_b32 s6, s6, 3\n" \
"v_or_b32_e32 v213, s40, v212\n" \
M_"a[92:95], a[236:239], a[212:215], a[92:95]\n" \
"s_or_b32 s6, s6, s40\n" \
"s_and_b32 s40, s7, 0x7c\n" \
M_"a[88:91], a[168:171], a[148:151], a[88:91]\n" \
X_"a[190:191], v39 offset:33792\n" \
"s_or_b32 s40, s40, s3\n" \
M_"a[88:91], a[232:235], a[212:215], a[88:91]\n" \
"s_cmpk_lt_u32 s40, 64\n" \
"s_cselect_b32 s41, s4, s5\n" \
M_"a[84:87], a[164:167], a[148:151], a[84:87]\n" \
"s_waitcnt vmcnt(17)\n" \
"v_or_b32_e32 v204, v210, v204\n" \
"v_or_b32_e64 v206, v210, v206\n" \
M_"a[84:87], a[228:231], a[212:215], a[84:87]\n" \
"v_mov_b32_e64 v210, 0\n" \
"s_add_u32 s88, s35, 0x1000\n" \
M_"a[80:83], a[160:163], a[148:151], a[80:83]\n" \
X_"a[252:253], v39 offset:40960\n" \
"s_lshl_b32 s40, s40, 1\n" \
M_"a[80:83], a[224:227], a[212:215], a[80:83]\n" \
"ds_write_b128 v196, v[200:203]\n" \
M_"a[96:99], a[160:163], a[152:155], a[96:99]\n" \
X_"a[254:255], v39 offset:41984\n" \
"s_lshl_b32 s41, s41, 7\n" \
M_"a[96:99], a[224:227], a[216:219], a[96:99]\n" \
"s_and_b32 s40, s40, 0x7e\n" \
"s_or_b32 s40, s41, s40\n" \
M_"a[100:103], a[164:167], a[152:155], a[100:103]\n" \
"ds_write_b128 v197, v[204:207]\n" \
M_"a[100:103], a[228:231], a[216:219], a[100:103]\n" \
"s_mulk_i32 s40, 0x480\n" \
"v_add_u32_e64 v49, s40, v20\n" \
M_"a[104:107], a[168:171], a[152:155], a[104:107]\n" \
"s_mov_b32 s40, 0xc000\n" \
"s_and_b32 s7, s7, 28\n" \
M_"a[104:107], a[232:235], a[216:219], a[104:107]\n" \
"ds_write_b128 v198, v[208:211]\n" \
M_"a[108:111], a[172:175], a[152:155], a[108:111]\n" \
"s_bfe_u32 s45, s2, 0x20003\n" \
"s_mov_b32 s41, 0x8000\n" \
M_"a[108:111], a[236:239], a[216:219], a[108:111]\n" \
"s_cmp_lg_u32 s4, 0\n" \
"s_cselect_b32 s51, s40, s41\n" \
"s_waitcnt lgkmcnt(15)\n" \
M_"a[124:127], a[172:175], a[156:159], a[124:127]\n" \
"s_movk_i32 s40, 0x4000\n" \
"s_lshr_b32 s44, s3, 1\n" \
M_"a[124:127], a[236:239], a[220:223], a[124:127]\n" \
"s_or_b32 s46, s7, s45\n" \
M_"a[120:123], a[168:171], a[156:159], a[120:123]\n" \
"s_cmp_lg_u32 s4, 0\n" \
"s_cselect_b32 s53, s41, s40\n" \
M_"a[120:123], a[232:235], a[220:223], a[120:123]\n" \
"s_lshl_b32 s40, s44, 15\n" \
"s_lshl_b32 s41, s43, 14\n" \
M_"a[116:119], a[164:167], a[156:159], a[116:119]\n" \
"s_lshl_b32 s48, s46, 20\n" \
"s_or_b32 s41, s40, s41\n" \
M_"a[116:119], a[228:231], a[220:223], a[116:119]\n" \
"s_add_i32 s49, s48, 0x1000000\n" \
"s_or_b32 s42, s49, s41\n" \
M_"a[112:115], a[160:163], a[156:159], a[112:115]\n" \
"s_lshl_b32 s4, s4, 16\n" \
"s_or_b32 s41, s42, s4\n" \
"s_waitcnt vmcnt(1) lgkmcnt(0)\n" \
M_"a[112:115], a[224:227], a[220:223], a[112:115]\n" \
"s_barrier\n" \
M_"v[52:55], a[176:179], a[128:131], v[52:55]\n" \
"s_add_u32 m0, s30, 0x8000\n" \
"s_movk_i32 s84, 0x280\n" \
M_"v[52:55], a[240:243], a[192:195], v[52:55]\n" \
B_"v22, s[64:67], s84 offen offset:0 lds\n" \
M_"v[56:59], a[180:183], a[128:131], v[56:59]\n" \
"s_movk_i32 s84, 0x1e80\n" \
M_"v[56:59], a[244:247], a[192:195], v[56:59]\n" \
B_"v23, s[64:67], s84 offen offset:1024 lds\n" \
M_"v[60:63], a[184:187], a[128:131], v[60:63]\n" \
X_"a[160:161], v32 offset:0\n" \
"s_lshl_b32 s4, s5, 16\n" \
M_"v[60:63], a[248:251], a[192:195], v[60:63]\n" \
"s_movk_i32 s84, 0x3a80\n" \
M_"v[64:67], a[188:191], a[128:131], v[64:67]\n" \
B_"v22, s[64:67], s84 offen offset:2048 lds\n" \
M_"v[64:67], a[252:255], a[192:195], v[64:67]\n" \
R_"a[128:131], v28 offset:16384\n" \
"s_mul_i32 s5, s6, 0x24800\n" \
M_"v[80:83], a[188:191], a[132:135], v[80:83]\n" \
R_"a[192:195], v29 offset:16384\n" \
"s_lshl_b32 s6, s46, 9\n" \
M_"v[80:83], a[252:255], a[196:199], v[80:83]\n" \
"s_movk_i32 s84, 0x5680\n" \
M_"v[76:79], a[184:187], a[132:135], v[76:79]\n" \
B_"v23, s[64:67], s84 offen offset:3072 lds\n" \
M_"v[76:79], a[248:251], a[196:199], v[76:79]\n" \
"s_add_u32 m0, s31, 0x8000\n" \
"s_mov_b32 s84, 0x280000\n" \
M_"v[72:75], a[180:183], a[132:135], v[72:75]\n" \
B_"v26, s[68:71], s84 offen offset:0 lds\n" \
M_"v[72:75], a[244:247], a[196:199], v[72:75]\n" \
X_"a[162:163], v32 offset:1024\n" \
"s_lshl_b32 s7, s7, 6\n" \
M_"v[68:71], a[176:179], a[132:135], v[68:71]\n" \
X_"a[224:225], v32 offset:8192\n" \
"s_add_i32 s6, s5, s6\n" \
M_"v[68:71], a[240:243], a[196:199], v[68:71]\n" \
R_"a[132:135], v28 offset:18432\n" \
"s_or_b32 s5, s5, s7\n" \
M_"v[84:87], a[176:179], a[136:139], v[84:87]\n" \
R_"a[196:199], v29 offset:18432\n" \
"s_lshl_b32 s7, s45, 2\n" \
M_"v[84:87], a[240:243], a[200:203], v[84:87]\n" \
"s_addk_i32 s84, 0x7c00\n" \
M_"v[88:91], a[180:183], a[136:139], v[88:91]\n" \
B_"v26, s[68:71], s84 offen offset:1024 lds\n" \
M_"v[88:91], a[244:247], a[200:203], v[88:91]\n" \
"s_addk_i32 s84, 0x7c00\n" \
M_"v[92:95], a[184:187], a[136:139], v[92:95]\n" \
B_"v27, s[68:71], s84 offen offset:2048 lds\n" \
M_"v[92:95], a[248:251], a[200:203], v[92:95]\n" \
"s_addk_i32 s84, 0x7c00\n" \
M_"v[96:99], a[188:191], a[136:139], v[96:99]\n" \
B_"v27, s[68:71], s84 offen offset:3072 lds\n" \
M_"v[96:99], a[252:255], a[200:203], v[96:99]\n" \
R_"a[136:139], v28 offset:20480\n" \
"s_or_b32 s5, s5, s7\n" \
M_"v[112:115], a[188:191], a[140:143], v[112:115]\n" \
R_"a[200:203], v29 offset:20480\n" \
"s_lshl_b32 s7, s3, 12\n" \
M_"v[112:115], a[252:255], a[204:207], v[112:115]\n" \
"s_add_u32 m0, s30, 0x9000\n" \
"s_mov_b32 s84, 0x8280\n" \
M_"v[108:111], a[184:187], a[140:143], v[108:111]\n" \
B_"v22, s[64:67], s84 offen offset:0 lds\n" \
M_"v[108:111], a[248:251], a[204:207], v[108:111]\n" \
"s_addk_i32 s84, 0x1c00\n" \
M_"v[104:107], a[180:183], a[140:143], v[104:107]\n" \
B_"v23, s[64:67], s84 offen offset:1024 lds\n" \
M_"v[104:107], a[244:247], a[204:207], v[104:107]\n" \
"s_addk_i32 s84, 0x1c00\n" \
M_"v[100:103], a[176:179], a[140:143], v[100:103]\n" \
B_"v22, s[64:67], s84 offen offset:2048 lds\n" \
M_"v[100:103], a[240:243], a[204:207], v[100:103]\n" \
R_"a[140:143], v28 offset:22528\n" \
"s_or_b32 s50, s49, s7\n" \
M_"v[116:119], a[176:179], a[144:147], v[116:119]\n" \
R_"a[204:207], v29 offset:22528\n" \
"s_lshl_b32 s7, s3, 11\n" \
M_"v[116:119], a[240:243], a[208:211], v[116:119]\n" \
"s_addk_i32 s84, 0x1c00\n" \
M_"v[120:123], a[180:183], a[144:147], v[120:123]\n" \
B_"v23, s[64:67], s84 offen offset:3072 lds\n" \
M_"v[120:123], a[244:247], a[208:211], v[120:123]\n" \
"s_add_u32 m0, s31, 0xc000\n" \
"s_mov_b32 s84, 0x280000\n" \
M_"v[124:127], a[184:187], a[144:147], v[124:127]\n" \
B_"v26, s[72:75], s84 offen offset:0 lds\n" \
M_"v[124:127], a[248:251], a[208:211], v[124:127]\n" \
"s_addk_i32 s84, 0x7c00\n" \
M_"v[128:131], a[188:191], a[144:147], v[128:131]\n" \
B_"v26, s[72:75], s84 offen offset:1024 lds\n" \
M_"v[128:131], a[252:255], a[208:211], v[128:131]\n" \
R_"a[144:147], v28 offset:0\n" \
"v_lshrrev_b32_e32 v215, 4, v20\n" \
M_"v[144:147], a[188:191], a[148:151], v[144:147]\n" \
R_"a[208:211], v29 offset:0\n" \
"v_bfe_u32 v216, v20, 4, 3\n" \
M_"v[144:147], a[252:255], a[212:215], v[144:147]\n" \
R_"v[200:203], v199 offset:0\n" \
"s_or_b32 s42, s42, s4\n" \
M_"v[140:143], a[184:187], a[148:151], v[140:143]\n" \
R_"v[204:207], v199 offset:4096\n" \
"s_lshl_b32 s4, s43, 11\n" \
M_"v[140:143], a[248:251], a[212:215], v[140:143]\n" \
X_"a[226:227], v32 offset:9216\n" \
"s_add_i32 s52, s48, s7\n" \
M_"v[136:139], a[180:183], a[148:151], v[136:139]\n" \
X_"a[164:165], v33 offset:0\n" \
"s_add_i32 s53, s53, s7\n" \
M_"v[136:139], a[244:247], a[212:215], v[136:139]\n" \
R_"v[208:211], v199 offset:8192\n" \
"v_mul_u32_u24_e32 v214, 0x480, v214\n" \
M_"v[132:135], a[176:179], a[148:151], v[132:135]\n" \
X_"a[166:167], v33 offset:1024\n" \
"v_bitop3_b32 v218, v212, v215, 7 bitop3:0x78\n" \
M_"v[132:135], a[240:243], a[212:215], v[132:135]\n" \
R_"a[148:151], v28 offset:2048\n" \
"v_bitop3_b32 v212, v212, v216, 4 bitop3:0x36\n" \
M_"v[148:151], a[176:179], a[152:155], v[148:151]\n" \
R_"a[212:215], v29 offset:2048\n" \
"s_lshl_b32 s7, s3, 10\n" \
M_"v[148:151], a[240:243], a[216:219], v[148:151]\n" \
X_"a[228:229], v33 offset:8192\n" \
"s_or_b32 s4, s40, s4\n" \
M_"v[152:155], a[180:183], a[152:155], v[152:155]\n" \
X_"a[230:231], v33 offset:9216\n" \
"v_lshl_or_b32 v25, v212, 4, v214\n" \
M_"v[152:155], a[244:247], a[216:219], v[152:155]\n" \
X_"a[168:169], v34 offset:0\n" \
"v_mul_u32_u24_e32 v212, 0x480, v217\n" \
M_"v[156:159], a[184:187], a[152:155], v[156:159]\n" \
X_"a[170:171], v34 offset:1024\n" \
"v_bitop3_b32 v213, v215, v213, 7 bitop3:0x6c\n" \
M_"v[156:159], a[248:251], a[216:219], v[156:159]\n" \
X_"a[232:233], v34 offset:8192\n" \
"s_or_b32 s40, s7, 0x14000\n" \
M_"v[160:163], a[188:191], a[152:155], v[160:163]\n" \
X_"a[234:235], v34 offset:9216\n" \
"s_lshl_b32 s43, s44, 13\n" \
M_"v[160:163], a[252:255], a[216:219], v[160:163]\n" \
R_"a[152:155], v28 offset:4096\n" \
"s_mulk_i32 s44, 0xe800\n" \
M_"v[176:179], a[188:191], a[156:159], v[176:179]\n" \
R_"a[216:219], v29 offset:4096\n" \
"s_lshl_b32 s7, s3, 8\n" \
"s_nop 0\n" \
M_"v[176:179], a[252:255], a[220:223], v[176:179]\n" \
X_"a[172:173], v35 offset:0\n" \
"v_lshl_or_b32 v24, v218, 4, v214\n" \
M_"v[172:175], a[184:187], a[156:159], v[172:175]\n" \
X_"a[174:175], v35 offset:1024\n" \
"v_lshl_or_b32 v21, v213, 4, v212\n" \
M_"v[172:175], a[248:251], a[220:223], v[172:175]\n" \
X_"a[236:237], v35 offset:8192\n" \
"s_add_i32 s44, s44, 0x17000\n" \
M_"v[168:171], a[180:183], a[156:159], v[168:171]\n" \
X_"a[238:239], v35 offset:9216\n" \
"s_or_b32 s45, s7, 0x27000\n" \
M_"v[168:171], a[244:247], a[220:223], v[168:171]\n" \
"s_or_b32 s46, s4, 0x10000\n" \
"s_add_i32 s48, s6, 0x3000000\n" \
M_"v[164:167], a[176:179], a[156:159], v[164:167]\n" \
"s_add_i32 s49, s5, 0x3004000\n" \
".fill (L_pb + 12000 - .) / 4, 4, 0xBF800000\n" \
"L_pp3:\n" \
"s_cbranch_execz L_pret\n" \
"s_add_i32 s52, s52, 0x1010000\n" \
M_"v[164:167], a[240:243], a[220:223], v[164:167]\n" \
R_"a[156:159], v28 offset:6144\n" \
R_"a[220:223], v29 offset:6144\n" \
"s_waitcnt lgkmcnt(15)\n" \
M_"a[0:3], a[160:163], a[128:131], a[0:3]\n" \
"s_addk_i32 s84, 0x7c00\n" \
M_"a[0:3], a[224:227], a[192:195], a[0:3]\n" \
B_"v27, s[72:75], s84 offen offset:2048 lds\n" \
M_"a[4:7], a[164:167], a[128:131], a[4:7]\n" \
X_"a[176:177], v36 offset:0\n" \
"s_waitcnt lgkmcnt(13)\n" \
M_"a[4:7], a[228:231], a[192:195], a[4:7]\n" \
"s_addk_i32 s84, 0x7c00\n" \
"s_waitcnt lgkmcnt(11)\n" \
M_"a[8:11], a[168:171], a[128:131], a[8:11]\n" \
B_"v27, s[72:75], s84 offen offset:3072 lds\n" \
"s_waitcnt lgkmcnt(9)\n" \
M_"a[8:11], a[232:235], a[192:195], a[8:11]\n" \
X_"a[178:179], v36 offset:1024\n" \
"s_waitcnt lgkmcnt(6)\n" \
M_"a[12:15], a[172:175], a[128:131], a[12:15]\n" \
X_"a[240:241], v36 offset:8192\n" \
"s_waitcnt lgkmcnt(5)\n" \
"s_nop 0\n" \
M_"a[12:15], a[236:239], a[192:195], a[12:15]\n" \
M_"a[28:31], a[172:175], a[132:135], a[28:31]\n" \
M_"a[28:31], a[236:239], a[196:199], a[28:31]\n" \
X_"a[242:243], v36 offset:9216\n" \
M_"a[24:27], a[168:171], a[132:135], a[24:27]\n" \
M_"a[24:27], a[232:235], a[196:199], a[24:27]\n" \
M_"a[20:23], a[164:167], a[132:135], a[20:23]\n" \
M_"a[20:23], a[228:231], a[196:199], a[20:23]\n" \
X_"a[180:181], v37 offset:0\n" \
M_"a[16:19], a[160:163], a[132:135], a[16:19]\n" \
M_"a[16:19], a[224:227], a[196:199], a[16:19]\n" \
M_"a[32:35], a[160:163], a[136:139], a[32:35]\n" \
X_"a[182:183], v37 offset:1024\n" \
M_"a[32:35], a[224:227], a[200:203], a[32:35]\n" \
M_"a[36:39], a[164:167], a[136:139], a[36:39]\n" \
M_"a[36:39], a[228:231], a[200:203], a[36:39]\n" \
"buffer_store_dwordx4 v[200:203], v20, s[80:83], s35 offen sc1\n" \
M_"a[40:43], a[168:171], a[136:139], a[40:43]\n" \
"s_mov_b32 m0, s32\n" \
X_"a[244:245], v37 offset:8192\n" \
M_"a[40:43], a[232:235], a[200:203], a[40:43]\n" \
"s_mov_b32 exec_lo, 0x0\n" \
"s_mov_b32 exec_hi, 0xffffffff\n" \
"global_load_lds_dword v[188:189], off\n" \
"s_mov_b64 exec, -1\n" \
M_"a[44:47], a[172:175], a[136:139], a[44:47]\n" \
X_"a[246:247], v37 offset:9216\n" \
M_"a[44:47], a[236:239], a[200:203], a[44:47]\n" \
M_"a[60:63], a[172:175], a[140:143], a[60:63]\n" \
M_"a[60:63], a[236:239], a[204:207], a[60:63]\n" \
M_"a[56:59], a[168:171], a[140:143], a[56:59]\n" \
X_"a[184:185], v38 offset:0\n" \
M_"a[56:59], a[232:235], a[204:207], a[56:59]\n" \
M_"a[52:55], a[164:167], a[140:143], a[52:55]\n" \
"buffer_store_dwordx4 v[204:207], v20, s[80:83], s88 offen sc1\n" \
M_"a[52:55], a[228:231], a[204:207], a[52:55]\n" \
X_"a[186:187], v38 offset:1024\n" \
M_"a[48:51], a[160:163], a[140:143], a[48:51]\n" \
M_"a[48:51], a[224:227], a[204:207], a[48:51]\n" \
M_"a[64:67], a[160:163], a[144:147], a[64:67]\n" \
X_"a[248:249], v38 offset:8192\n" \
M_"a[64:67], a[224:227], a[208:211], a[64:67]\n" \
M_"a[68:71], a[164:167], a[144:147], a[68:71]\n" \
M_"a[68:71], a[228:231], a[208:211], a[68:71]\n" \
X_"a[250:251], v38 offset:9216\n" \
M_"a[72:75], a[168:171], a[144:147], a[72:75]\n" \
M_"a[72:75], a[232:235], a[208:211], a[72:75]\n" \
M_"a[76:79], a[172:175], a[144:147], a[76:79]\n" \
"buffer_store_dwordx4 v[208:211], v20, s[80:83], s36 offen sc1\n" \
M_"a[76:79], a[236:239], a[208:211], a[76:79]\n" \
X_"a[188:189], v39 offset:0\n" \
M_"a[92:95], a[172:175], a[148:151], a[92:95]\n" \
M_"a[92:95], a[236:239], a[212:215], a[92:95]\n" \
M_"a[88:91], a[168:171], a[148:151], a[88:91]\n" \
X_"a[190:191], v39 offset:1024\n" \
M_"a[88:91], a[232:235], a[212:215], a[88:91]\n" \
M_"a[84:87], a[164:167], a[148:151], a[84:87]\n" \
M_"a[84:87], a[228:231], a[212:215], a[84:87]\n" \
X_"a[252:253], v39 offset:8192\n" \
M_"a[80:83], a[160:163], a[148:151], a[80:83]\n" \
M_"a[80:83], a[224:227], a[212:215], a[80:83]\n" \
M_"a[96:99], a[160:163], a[152:155], a[96:99]\n" \
X_"a[254:255], v39 offset:9216\n" \
M_"a[96:99], a[224:227], a[216:219], a[96:99]\n" \
M_"a[100:103], a[164:167], a[152:155], a[100:103]\n" \
M_"a[100:103], a[228:231], a[216:219], a[100:103]\n" \
M_"a[104:107], a[168:171], a[152:155], a[104:107]\n" \
M_"a[104:107], a[232:235], a[216:219], a[104:107]\n" \
M_"a[108:111], a[172:175], a[152:155], a[108:111]\n" \
M_"a[108:111], a[236:239], a[216:219], a[108:111]\n" \
"s_waitcnt lgkmcnt(15)\n" \
M_"a[124:127], a[172:175], a[156:159], a[124:127]\n" \
M_"a[124:127], a[236:239], a[220:223], a[124:127]\n" \
M_"a[120:123], a[168:171], a[156:159], a[120:123]\n" \
M_"a[120:123], a[232:235], a[220:223], a[120:123]\n" \
M_"a[116:119], a[164:167], a[156:159], a[116:119]\n" \
M_"a[116:119], a[228:231], a[220:223], a[116:119]\n" \
M_"a[112:115], a[160:163], a[156:159], a[112:115]\n" \
"s_waitcnt vmcnt(4) lgkmcnt(0)\n" \
M_"a[112:115], a[224:227], a[220:223], a[112:115]\n" \
"s_barrier\n" \
M_"v[52:55], a[176:179], a[128:131], v[52:55]\n" \
"s_add_u32 m0, s30, 0x0\n" \
"s_movk_i32 s84, 0x300\n" \
M_"v[52:55], a[240:243], a[192:195], v[52:55]\n" \
B_"v22, s[64:67], s84 offen offset:0 lds\n" \
M_"v[56:59], a[180:183], a[128:131], v[56:59]\n" \
X_"a[160:161], v32 offset:32768\n" \
M_"v[56:59], a[244:247], a[192:195], v[56:59]\n" \
"s_movk_i32 s84, 0x1f00\n" \
M_"v[60:63], a[184:187], a[128:131], v[60:63]\n" \
B_"v23, s[64:67], s84 offen offset:1024 lds\n" \
M_"v[60:63], a[248:251], a[192:195], v[60:63]\n" \
X_"a[162:163], v32 offset:33792\n" \
M_"v[64:67], a[188:191], a[128:131], v[64:67]\n" \
X_"a[224:225], v32 offset:40960\n" \
M_"v[64:67], a[252:255], a[192:195], v[64:67]\n" \
R_"a[128:131], v28 offset:49152\n" \
M_"v[80:83], a[188:191], a[132:135], v[80:83]\n" \
R_"a[192:195], v29 offset:49152\n" \
M_"v[80:83], a[252:255], a[196:199], v[80:83]\n" \
"s_movk_i32 s84, 0x3b00\n" \
M_"v[76:79], a[184:187], a[132:135], v[76:79]\n" \
B_"v22, s[64:67], s84 offen offset:2048 lds\n" \
M_"v[76:79], a[248:251], a[196:199], v[76:79]\n" \
"s_movk_i32 s84, 0x5700\n" \
M_"v[72:75], a[180:183], a[132:135], v[72:75]\n" \
B_"v23, s[64:67], s84 offen offset:3072 lds\n" \
M_"v[72:75], a[244:247], a[196:199], v[72:75]\n" \
"s_add_u32 m0, s31, 0x0\n" \
"s_mov_b32 s84, 0x300000\n" \
M_"v[68:71], a[176:179], a[132:135], v[68:71]\n" \
B_"v26, s[68:71], s84 offen offset:0 lds\n" \
M_"v[68:71], a[240:243], a[196:199], v[68:71]\n" \
R_"a[132:135], v28 offset:51200\n" \
M_"v[84:87], a[176:179], a[136:139], v[84:87]\n" \
R_"a[196:199], v29 offset:51200\n" \
M_"v[84:87], a[240:243], a[200:203], v[84:87]\n" \
"s_addk_i32 s84, 0x7c00\n" \
M_"v[88:91], a[180:183], a[136:139], v[88:91]\n" \
B_"v26, s[68:71], s84 offen offset:1024 lds\n" \
M_"v[88:91], a[244:247], a[200:203], v[88:91]\n" \
X_"a[226:227], v32 offset:41984\n" \
M_"v[92:95], a[184:187], a[136:139], v[92:95]\n" \
"s_addk_i32 s84, 0x7c00\n" \
M_"v[92:95], a[248:251], a[200:203], v[92:95]\n" \
B_"v27, s[68:71], s84 offen offset:2048 lds\n" \
M_"v[96:99], a[188:191], a[136:139], v[96:99]\n" \
X_"a[164:165], v33 offset:32768\n" \
M_"v[96:99], a[252:255], a[200:203], v[96:99]\n" \
R_"a[136:139], v28 offset:53248\n" \
M_"v[112:115], a[188:191], a[140:143], v[112:115]\n" \
R_"a[200:203], v29 offset:53248\n" \
M_"v[112:115], a[252:255], a[204:207], v[112:115]\n" \
"s_addk_i32 s84, 0x7c00\n" \
M_"v[108:111], a[184:187], a[140:143], v[108:111]\n" \
B_"v27, s[68:71], s84 offen offset:3072 lds\n" \
M_"v[108:111], a[248:251], a[204:207], v[108:111]\n" \
"s_add_u32 m0, s30, 0x1000\n" \
"s_mov_b32 s84, 0x8300\n" \
M_"v[104:107], a[180:183], a[140:143], v[104:107]\n" \
B_"v22, s[64:67], s84 offen offset:0 lds\n" \
M_"v[104:107], a[244:247], a[204:207], v[104:107]\n" \
X_"a[166:167], v33 offset:33792\n" \
M_"v[100:103], a[176:179], a[140:143], v[100:103]\n" \
X_"a[228:229], v33 offset:40960\n" \
M_"v[100:103], a[240:243], a[204:207], v[100:103]\n" \
R_"a[140:143], v28 offset:55296\n" \
M_"v[116:119], a[176:179], a[144:147], v[116:119]\n" \
R_"a[204:207], v29 offset:55296\n" \
M_"v[116:119], a[240:243], a[208:211], v[116:119]\n" \
"s_addk_i32 s84, 0x1c00\n" \
M_"v[120:123], a[180:183], a[144:147], v[120:123]\n" \
B_"v23, s[64:67], s84 offen offset:1024 lds\n" \
M_"v[120:123], a[244:247], a[208:211], v[120:123]\n" \
"s_addk_i32 s84, 0x1c00\n" \
M_"v[124:127], a[184:187], a[144:147], v[124:127]\n" \
B_"v22, s[64:67], s84 offen offset:2048 lds\n" \
M_"v[124:127], a[248:251], a[208:211], v[124:127]\n" \
"s_addk_i32 s84, 0x1c00\n" \
"s_nop 0\n" \
M_"v[128:131], a[188:191], a[144:147], v[128:131]\n" \
B_"v23, s[64:67], s84 offen offset:3072 lds\n" \
M_"v[128:131], a[252:255], a[208:211], v[128:131]\n" \
R_"a[144:147], v28 offset:32768\n" \
M_"v[144:147], a[188:191], a[148:151], v[144:147]\n" \
R_"a[208:211], v29 offset:32768\n" \
M_"v[144:147], a[252:255], a[212:215], v[144:147]\n" \
"s_add_u32 m0, s31, 0x4000\n" \
"s_mov_b32 s84, 0x300000\n" \
M_"v[140:143], a[184:187], a[148:151], v[140:143]\n" \
B_"v26, s[72:75], s84 offen offset:0 lds\n" \
M_"v[140:143], a[248:251], a[212:215], v[140:143]\n" \
X_"a[230:231], v33 offset:41984\n" \
M_"v[136:139], a[180:183], a[148:151], v[136:139]\n" \
"s_addk_i32 s84, 0x7c00\n" \
"s_nop 0\n" \
M_"v[136:139], a[244:247], a[212:215], v[136:139]\n" \
B_"v26, s[72:75], s84 offen offset:1024 lds\n" \
M_"v[132:135], a[176:179], a[148:151], v[132:135]\n" \
X_"a[168:169], v34 offset:32768\n" \
M_"v[132:135], a[240:243], a[212:215], v[132:135]\n" \
R_"a[148:151], v28 offset:34816\n" \
M_"v[148:151], a[176:179], a[152:155], v[148:151]\n" \
R_"a[212:215], v29 offset:34816\n" \
M_"v[148:151], a[240:243], a[216:219], v[148:151]\n" \
X_"a[170:171], v34 offset:33792\n" \
M_"v[152:155], a[180:183], a[152:155], v[152:155]\n" \
X_"a[232:233], v34 offset:40960\n" \
M_"v[152:155], a[244:247], a[216:219], v[152:155]\n" \
X_"a[234:235], v34 offset:41984\n" \
M_"v[156:159], a[184:187], a[152:155], v[156:159]\n" \
X_"a[172:173], v35 offset:32768\n" \
M_"v[156:159], a[248:251], a[216:219], v[156:159]\n" \
X_"a[174:175], v35 offset:33792\n" \
M_"v[160:163], a[188:191], a[152:155], v[160:163]\n" \
X_"a[236:237], v35 offset:40960\n" \
M_"v[160:163], a[252:255], a[216:219], v[160:163]\n" \
R_"a[152:155], v28 offset:36864\n" \
M_"v[176:179], a[188:191], a[156:159], v[176:179]\n" \
R_"a[216:219], v29 offset:36864\n" \
M_"v[176:179], a[252:255], a[220:223], v[176:179]\n" \
X_"a[238:239], v35 offset:41984\n" \
M_"v[172:175], a[184:187], a[156:159], v[172:175]\n" \
M_"v[172:175], a[248:251], a[220:223], v[172:175]\n" \
M_"v[168:171], a[180:183], a[156:159], v[168:171]\n" \
M_"v[168:171], a[244:247], a[220:223], v[168:171]\n" \
M_"v[164:167], a[176:179], a[156:159], v[164:167]\n" \
M_"v[164:167], a[240:243], a[220:223], v[164:167]\n" \
R_"a[156:159], v28 offset:38912\n" \
R_"a[220:223], v29 offset:38912\n" \
"s_waitcnt lgkmcnt(15)\n" \
M_"a[0:3], a[160:163], a[128:131], a[0:3]\n" \
"s_addk_i32 s84, 0x7c00\n" \
M_"a[0:3], a[224:227], a[192:195], a[0:3]\n" \
B_"v27, s[72:75], s84 offen offset:2048 lds\n" \
M_"a[4:7], a[164:167], a[128:131], a[4:7]\n" \
X_"a[176:177], v36 offset:32768\n" \
"s_waitcnt lgkmcnt(15)\n" \
M_"a[4:7], a[228:231], a[192:195], a[4:7]\n" \
"s_addk_i32 s84, 0x7c00\n" \
"s_waitcnt lgkmcnt(11)\n" \
M_"a[8:11], a[168:171], a[128:131], a[8:11]\n" \
B_"v27, s[72:75], s84 offen offset:3072 lds\n" \
"s_waitcnt lgkmcnt(9)\n" \
M_"a[8:11], a[232:235], a[192:195], a[8:11]\n" \
X_"a[178:179], v36 offset:33792\n" \
"s_waitcnt lgkmcnt(8)\n" \
M_"a[12:15], a[172:175], a[128:131], a[12:15]\n" \
X_"a[240:241], v36 offset:40960\n" \
"s_waitcnt lgkmcnt(5)\n" \
M_"a[12:15], a[236:239], a[192:195], a[12:15]\n" \
M_"a[28:31], a[172:175], a[132:135], a[28:31]\n" \
M_"a[28:31], a[236:239], a[196:199], a[28:31]\n" \
X_"a[242:243], v36 offset:41984\n" \
M_"a[24:27], a[168:171], a[132:135], a[24:27]\n" \
M_"a[24:27], a[232:235], a[196:199], a[24:27]\n" \
M_"a[20:23], a[164:167], a[132:135], a[20:23]\n" \
M_"a[20:23], a[228:231], a[196:199], a[20:23]\n" \
X_"a[180:181], v37 offset:32768\n" \
M_"a[16:19], a[160:163], a[132:135], a[16:19]\n" \
M_"a[16:19], a[224:227], a[196:199], a[16:19]\n" \
M_"a[32:35], a[160:163], a[136:139], a[32:35]\n" \
X_"a[182:183], v37 offset:33792\n" \
M_"a[32:35], a[224:227], a[200:203], a[32:35]\n" \
M_"a[36:39], a[164:167], a[136:139], a[36:39]\n" \
"v_lshlrev_b32_e32 v45, 3, v20\n" \
"v_add_u32_e32 v45, s31, v45\n" \
M_"a[36:39], a[228:231], a[200:203], a[36:39]\n" \
"s_getpc_b64 s[86:87]\n" \
"s_sub_u32 s86, s86, 0xf800\n" \
"s_subb_u32 s87, s87, 0\n" \
"s_mov_b32 m0, s32\n" \
"s_nop 0\n" \
M_"a[40:43], a[168:171], a[136:139], a[40:43]\n" \
"global_load_lds_dword v45, s[86:87]\n" \
M_"a[40:43], a[232:235], a[200:203], a[40:43]\n" \
X_"a[244:245], v37 offset:40960\n" \
M_"a[44:47], a[172:175], a[136:139], a[44:47]\n" \
X_"a[246:247], v37 offset:41984\n" \
M_"a[44:47], a[236:239], a[200:203], a[44:47]\n" \
M_"a[60:63], a[172:175], a[140:143], a[60:63]\n" \
M_"a[60:63], a[236:239], a[204:207], a[60:63]\n" \
M_"a[56:59], a[168:171], a[140:143], a[56:59]\n" \
X_"a[184:185], v38 offset:32768\n" \
M_"a[56:59], a[232:235], a[204:207], a[56:59]\n" \
M_"a[52:55], a[164:167], a[140:143], a[52:55]\n" \
M_"a[52:55], a[228:231], a[204:207], a[52:55]\n" \
X_"a[186:187], v38 offset:33792\n" \
M_"a[48:51], a[160:163], a[140:143], a[48:51]\n" \
M_"a[48:51], a[224:227], a[204:207], a[48:51]\n" \
M_"a[64:67], a[160:163], a[144:147], a[64:67]\n" \
X_"a[248:249], v38 offset:40960\n" \
M_"a[64:67], a[224:227], a[208:211], a[64:67]\n" \
M_"a[68:71], a[164:167], a[144:147], a[68:71]\n" \
M_"a[68:71], a[228:231], a[208:211], a[68:71]\n" \
X_"a[250:251], v38 offset:41984\n" \
M_"a[72:75], a[168:171], a[144:147], a[72:75]\n" \
M_"a[72:75], a[232:235], a[208:211], a[72:75]\n" \
M_"a[76:79], a[172:175], a[144:147], a[76:79]\n" \
M_"a[76:79], a[236:239], a[208:211], a[76:79]\n" \
X_"a[188:189], v39 offset:32768\n" \
M_"a[92:95], a[172:175], a[148:151], a[92:95]\n" \
M_"a[92:95], a[236:239], a[212:215], a[92:95]\n" \
M_"a[88:91], a[168:171], a[148:151], a[88:91]\n" \
X_"a[190:191], v39 offset:33792\n" \
M_"a[88:91], a[232:235], a[212:215], a[88:91]\n" \
M_"a[84:87], a[164:167], a[148:151], a[84:87]\n" \
M_"a[84:87], a[228:231], a[212:215], a[84:87]\n" \
X_"a[252:253], v39 offset:40960\n" \
M_"a[80:83], a[160:163], a[148:151], a[80:83]\n" \
M_"a[80:83], a[224:227], a[212:215], a[80:83]\n" \
M_"a[96:99], a[160:163], a[152:155], a[96:99]\n" \
X_"a[254:255], v39 offset:41984\n" \
M_"a[96:99], a[224:227], a[216:219], a[96:99]\n" \
M_"a[100:103], a[164:167], a[152:155], a[100:103]\n" \
M_"a[100:103], a[228:231], a[216:219], a[100:103]\n" \
M_"a[104:107], a[168:171], a[152:155], a[104:107]\n" \
M_"a[104:107], a[232:235], a[216:219], a[104:107]\n" \
M_"a[108:111], a[172:175], a[152:155], a[108:111]\n" \
M_"a[108:111], a[236:239], a[216:219], a[108:111]\n" \
"s_waitcnt lgkmcnt(15)\n" \
M_"a[124:127], a[172:175], a[156:159], a[124:127]\n" \
M_"a[124:127], a[236:239], a[220:223], a[124:127]\n" \
M_"a[120:123], a[168:171], a[156:159], a[120:123]\n" \
M_"a[120:123], a[232:235], a[220:223], a[120:123]\n" \
M_"a[116:119], a[164:167], a[156:159], a[116:119]\n" \
M_"a[116:119], a[228:231], a[220:223], a[116:119]\n" \
M_"a[112:115], a[160:163], a[156:159], a[112:115]\n" \
"s_waitcnt vmcnt(1) lgkmcnt(0)\n" \
M_"a[112:115], a[224:227], a[220:223], a[112:115]\n" \
"s_barrier\n" \
"v_mov_b32_e32 v45, s62\n" \
"s_mov_b64 exec, 1\n" \
"buffer_store_dword v45, off, s[80:83], s37 offset:128 sc1\n" \
"s_mov_b64 exec, -1\n" \
M_"v[52:55], a[176:179], a[128:131], v[52:55]\n" \
"s_add_u32 m0, s30, 0x8000\n" \
"s_movk_i32 s84, 0x380\n" \
M_"v[52:55], a[240:243], a[192:195], v[52:55]\n" \
B_"v22, s[64:67], s84 offen offset:0 lds\n" \
M_"v[56:59], a[180:183], a[128:131], v[56:59]\n" \
X_"a[160:161], v32 offset:0\n" \
M_"v[56:59], a[244:247], a[192:195], v[56:59]\n" \
"s_movk_i32 s84, 0x1f80\n" \
M_"v[60:63], a[184:187], a[128:131], v[60:63]\n" \
B_"v23, s[64:67], s84 offen offset:1024 lds\n" \
M_"v[60:63], a[248:251], a[192:195], v[60:63]\n" \
X_"a[162:163], v32 offset:1024\n" \
M_"v[64:67], a[188:191], a[128:131], v[64:67]\n" \
X_"a[224:225], v32 offset:8192\n" \
M_"v[64:67], a[252:255], a[192:195], v[64:67]\n" \
R_"a[128:131], v28 offset:16384\n" \
M_"v[80:83], a[188:191], a[132:135], v[80:83]\n" \
R_"a[192:195], v29 offset:16384\n" \
M_"v[80:83], a[252:255], a[196:199], v[80:83]\n" \
"s_movk_i32 s84, 0x3b80\n" \
M_"v[76:79], a[184:187], a[132:135], v[76:79]\n" \
B_"v22, s[64:67], s84 offen offset:2048 lds\n" \
M_"v[76:79], a[248:251], a[196:199], v[76:79]\n" \
"s_movk_i32 s84, 0x5780\n" \
M_"v[72:75], a[180:183], a[132:135], v[72:75]\n" \
B_"v23, s[64:67], s84 offen offset:3072 lds\n" \
M_"v[72:75], a[244:247], a[196:199], v[72:75]\n" \
"s_add_u32 m0, s31, 0x8000\n" \
"s_mov_b32 s84, 0x380000\n" \
M_"v[68:71], a[176:179], a[132:135], v[68:71]\n" \
B_"v26, s[68:71], s84 offen offset:0 lds\n" \
M_"v[68:71], a[240:243], a[196:199], v[68:71]\n" \
R_"a[132:135], v28 offset:18432\n" \
M_"v[84:87], a[176:179], a[136:139], v[84:87]\n" \
R_"a[196:199], v29 offset:18432\n" \
M_"v[84:87], a[240:243], a[200:203], v[84:87]\n" \
"s_addk_i32 s84, 0x7c00\n" \
M_"v[88:91], a[180:183], a[136:139], v[88:91]\n" \
B_"v26, s[68:71], s84 offen offset:1024 lds\n" \
M_"v[88:91], a[244:247], a[200:203], v[88:91]\n" \
X_"a[226:227], v32 offset:9216\n" \
M_"v[92:95], a[184:187], a[136:139], v[92:95]\n" \
"s_addk_i32 s84, 0x7c00\n" \
M_"v[92:95], a[248:251], a[200:203], v[92:95]\n" \
B_"v27, s[68:71], s84 offen offset:2048 lds\n" \
M_"v[96:99], a[188:191], a[136:139], v[96:99]\n" \
X_"a[164:165], v33 offset:0\n" \
M_"v[96:99], a[252:255], a[200:203], v[96:99]\n" \
R_"a[136:139], v28 offset:20480\n" \
M_"v[112:115], a[188:191], a[140:143], v[112:115]\n" \
R_"a[200:203], v29 offset:20480\n" \
M_"v[112:115], a[252:255], a[204:207], v[112:115]\n" \
"s_addk_i32 s84, 0x7c00\n" \
"s_nop 0\n" \
M_"v[108:111], a[184:187], a[140:143], v[108:111]\n" \
B_"v27, s[68:71], s84 offen offset:3072 lds\n" \
M_"v[108:111], a[248:251], a[204:207], v[108:111]\n" \
"s_add_u32 m0, s30, 0x9000\n" \
"s_mov_b32 s84, 0x8380\n" \
M_"v[104:107], a[180:183], a[140:143], v[104:107]\n" \
B_"v22, s[64:67], s84 offen offset:0 lds\n" \
M_"v[104:107], a[244:247], a[204:207], v[104:107]\n" \
X_"a[166:167], v33 offset:1024\n" \
M_"v[100:103], a[176:179], a[140:143], v[100:103]\n" \
X_"a[228:229], v33 offset:8192\n" \
M_"v[100:103], a[240:243], a[204:207], v[100:103]\n" \
R_"a[140:143], v28 offset:22528\n" \
M_"v[116:119], a[176:179], a[144:147], v[116:119]\n" \
R_"a[204:207], v29 offset:22528\n" \
M_"v[116:119], a[240:243], a[208:211], v[116:119]\n" \
"s_addk_i32 s84, 0x1c00\n" \
M_"v[120:123], a[180:183], a[144:147], v[120:123]\n" \
B_"v23, s[64:67], s84 offen offset:1024 lds\n" \
M_"v[120:123], a[244:247], a[208:211], v[120:123]\n" \
"s_addk_i32 s84, 0x1c00\n" \
M_"v[124:127], a[184:187], a[144:147], v[124:127]\n" \
B_"v22, s[64:67], s84 offen offset:2048 lds\n" \
M_"v[124:127], a[248:251], a[208:211], v[124:127]\n" \
"s_addk_i32 s84, 0x1c00\n" \
"s_nop 0\n" \
M_"v[128:131], a[188:191], a[144:147], v[128:131]\n" \
B_"v23, s[64:67], s84 offen offset:3072 lds\n" \
M_"v[128:131], a[252:255], a[208:211], v[128:131]\n" \
R_"a[144:147], v28 offset:0\n" \
M_"v[144:147], a[188:191], a[148:151], v[144:147]\n" \
R_"a[208:211], v29 offset:0\n" \
M_"v[144:147], a[252:255], a[212:215], v[144:147]\n" \
"s_add_u32 m0, s31, 0xc000\n" \
"s_mov_b32 s84, 0x380000\n" \
M_"v[140:143], a[184:187], a[148:151], v[140:143]\n" \
B_"v26, s[72:75], s84 offen offset:0 lds\n" \
M_"v[140:143], a[248:251], a[212:215], v[140:143]\n" \
X_"a[230:231], v33 offset:9216\n" \
M_"v[136:139], a[180:183], a[148:151], v[136:139]\n" \
"s_addk_i32 s84, 0x7c00\n" \
"s_nop 0\n" \
M_"v[136:139], a[244:247], a[212:215], v[136:139]\n" \
B_"v26, s[72:75], s84 offen offset:1024 lds\n" \
M_"v[132:135], a[176:179], a[148:151], v[132:135]\n" \
X_"a[168:169], v34 offset:0\n" \
M_"v[132:135], a[240:243], a[212:215], v[132:135]\n" \
R_"a[148:151], v28 offset:2048\n" \
M_"v[148:151], a[176:179], a[152:155], v[148:151]\n" \
R_"a[212:215], v29 offset:2048\n" \
M_"v[148:151], a[240:243], a[216:219], v[148:151]\n" \
X_"a[170:171], v34 offset:1024\n" \
M_"v[152:155], a[180:183], a[152:155], v[152:155]\n" \
X_"a[232:233], v34 offset:8192\n" \
M_"v[152:155], a[244:247], a[216:219], v[152:155]\n" \
X_"a[234:235], v34 offset:9216\n" \
M_"v[156:159], a[184:187], a[152:155], v[156:159]\n" \
X_"a[172:173], v35 offset:0\n" \
M_"v[156:159], a[248:251], a[216:219], v[156:159]\n" \
X_"a[174:175], v35 offset:1024\n" \
M_"v[160:163], a[188:191], a[152:155], v[160:163]\n" \
X_"a[236:237], v35 offset:8192\n" \
M_"v[160:163], a[252:255], a[216:219], v[160:163]\n" \
R_"a[152:155], v28 offset:4096\n" \
M_"v[176:179], a[188:191], a[156:159], v[176:179]\n" \
R_"a[216:219], v29 offset:4096\n" \
M_"v[176:179], a[252:255], a[220:223], v[176:179]\n" \
X_"a[238:239], v35 offset:9216\n" \
M_"v[172:175], a[184:187], a[156:159], v[172:175]\n" \
M_"v[172:175], a[248:251], a[220:223], v[172:175]\n" \
M_"v[168:171], a[180:183], a[156:159], v[168:171]\n" \
M_"v[168:171], a[244:247], a[220:223], v[168:171]\n" \
M_"v[164:167], a[176:179], a[156:159], v[164:167]\n" \
M_"v[164:167], a[240:243], a[220:223], v[164:167]\n" \
R_"a[156:159], v28 offset:6144\n" \
R_"a[220:223], v29 offset:6144\n" \
"s_waitcnt lgkmcnt(15)\n" \
M_"a[0:3], a[160:163], a[128:131], a[0:3]\n" \
"s_addk_i32 s84, 0x7c00\n" \
M_"a[0:3], a[224:227], a[192:195], a[0:3]\n" \
B_"v27, s[72:75], s84 offen offset:2048 lds\n" \
M_"a[4:7], a[164:167], a[128:131], a[4:7]\n" \
X_"a[176:177], v36 offset:0\n" \
"s_waitcnt lgkmcnt(15)\n" \
M_"a[4:7], a[228:231], a[192:195], a[4:7]\n" \
"s_addk_i32 s84, 0x7c00\n" \
"s_waitcnt lgkmcnt(11)\n" \
M_"a[8:11], a[168:171], a[128:131], a[8:11]\n" \
B_"v27, s[72:75], s84 offen offset:3072 lds\n" \
"s_waitcnt lgkmcnt(9)\n" \
M_"a[8:11], a[232:235], a[192:195], a[8:11]\n" \
X_"a[178:179], v36 offset:1024\n" \
"s_waitcnt lgkmcnt(8)\n" \
M_"a[12:15], a[172:175], a[128:131], a[12:15]\n" \
X_"a[240:241], v36 offset:8192\n" \
"s_waitcnt lgkmcnt(5)\n" \
M_"a[12:15], a[236:239], a[192:195], a[12:15]\n" \
M_"a[28:31], a[172:175], a[132:135], a[28:31]\n" \
M_"a[28:31], a[236:239], a[196:199], a[28:31]\n" \
X_"a[242:243], v36 offset:9216\n" \
M_"a[24:27], a[168:171], a[132:135], a[24:27]\n" \
M_"a[24:27], a[232:235], a[196:199], a[24:27]\n" \
M_"a[20:23], a[164:167], a[132:135], a[20:23]\n" \
M_"a[20:23], a[228:231], a[196:199], a[20:23]\n" \
X_"a[180:181], v37 offset:0\n" \
M_"a[16:19], a[160:163], a[132:135], a[16:19]\n" \
M_"a[16:19], a[224:227], a[196:199], a[16:19]\n" \
M_"a[32:35], a[160:163], a[136:139], a[32:35]\n" \
"v_bfe_u32 v44, v20, 2, 5\n" \
X_"a[182:183], v37 offset:1024\n" \
M_"a[32:35], a[224:227], a[200:203], a[32:35]\n" \
"buffer_load_dword v44, v44, s[80:83], s38 offen offset:128 sc1\n" \
M_"a[36:39], a[164:167], a[136:139], a[36:39]\n" \
M_"a[36:39], a[228:231], a[200:203], a[36:39]\n" \
X_"a[244:245], v37 offset:8192\n" \
M_"a[40:43], a[168:171], a[136:139], a[40:43]\n" \
M_"a[40:43], a[232:235], a[200:203], a[40:43]\n" \
M_"a[44:47], a[172:175], a[136:139], a[44:47]\n" \
X_"a[246:247], v37 offset:9216\n" \
M_"a[44:47], a[236:239], a[200:203], a[44:47]\n" \
M_"a[60:63], a[172:175], a[140:143], a[60:63]\n" \
"global_store_dwordx4 v49, v[0:3], s[28:29] offset:0 sc1\n" \
M_"a[60:63], a[236:239], a[204:207], a[60:63]\n" \
M_"a[56:59], a[168:171], a[140:143], a[56:59]\n" \
X_"a[184:185], v38 offset:0\n" \
M_"a[56:59], a[232:235], a[204:207], a[56:59]\n" \
M_"a[52:55], a[164:167], a[140:143], a[52:55]\n" \
M_"a[52:55], a[228:231], a[204:207], a[52:55]\n" \
X_"a[186:187], v38 offset:1024\n" \
M_"a[48:51], a[160:163], a[140:143], a[48:51]\n" \
M_"a[48:51], a[224:227], a[204:207], a[48:51]\n" \
M_"a[64:67], a[160:163], a[144:147], a[64:67]\n" \
"global_store_dwordx4 v49, v[0:3], s[28:29] offset:1024 sc1\n" \
M_"a[64:67], a[224:227], a[208:211], a[64:67]\n" \
X_"a[248:249], v38 offset:8192\n" \
M_"a[68:71], a[164:167], a[144:147], a[68:71]\n" \
M_"a[68:71], a[228:231], a[208:211], a[68:71]\n" \
X_"a[250:251], v38 offset:9216\n" \
M_"a[72:75], a[168:171], a[144:147], a[72:75]\n" \
M_"a[72:75], a[232:235], a[208:211], a[72:75]\n" \
M_"a[76:79], a[172:175], a[144:147], a[76:79]\n" \
M_"a[76:79], a[236:239], a[208:211], a[76:79]\n" \
X_"a[188:189], v39 offset:0\n" \
M_"a[92:95], a[172:175], a[148:151], a[92:95]\n" \
"s_mov_b64 exec, 0xffff\n" \
"global_store_dwordx4 v49, v[0:3], s[28:29] offset:2048 sc1\n" \
"s_mov_b64 exec, -1\n" \
"s_nop 0\n" \
M_"a[92:95], a[236:239], a[212:215], a[92:95]\n" \
M_"a[88:91], a[168:171], a[148:151], a[88:91]\n" \
X_"a[190:191], v39 offset:1024\n" \
M_"a[88:91], a[232:235], a[212:215], a[88:91]\n" \
M_"a[84:87], a[164:167], a[148:151], a[84:87]\n" \
".fill (L_pb + 16096 - .) / 4, 4, 0xBF800000\n" \
"L_pp4:\n" \
"s_cbranch_execz L_pret\n" \
"s_nop 0\n" \
M_"a[84:87], a[228:231], a[212:215], a[84:87]\n" \
X_"a[252:253], v39 offset:8192\n" \
M_"a[80:83], a[160:163], a[148:151], a[80:83]\n" \
M_"a[80:83], a[224:227], a[212:215], a[80:83]\n" \
M_"a[96:99], a[160:163], a[152:155], a[96:99]\n" \
X_"a[254:255], v39 offset:9216\n" \
M_"a[96:99], a[224:227], a[216:219], a[96:99]\n" \
M_"a[100:103], a[164:167], a[152:155], a[100:103]\n" \
M_"a[100:103], a[228:231], a[216:219], a[100:103]\n" \
M_"a[104:107], a[168:171], a[152:155], a[104:107]\n" \
M_"a[104:107], a[232:235], a[216:219], a[104:107]\n" \
M_"a[108:111], a[172:175], a[152:155], a[108:111]\n" \
M_"a[108:111], a[236:239], a[216:219], a[108:111]\n" \
"s_waitcnt lgkmcnt(15)\n" \
M_"a[124:127], a[172:175], a[156:159], a[124:127]\n" \
M_"a[124:127], a[236:239], a[220:223], a[124:127]\n" \
M_"a[120:123], a[168:171], a[156:159], a[120:123]\n" \
M_"a[120:123], a[232:235], a[220:223], a[120:123]\n" \
M_"a[116:119], a[164:167], a[156:159], a[116:119]\n" \
M_"a[116:119], a[228:231], a[220:223], a[116:119]\n" \
M_"a[112:115], a[160:163], a[156:159], a[112:115]\n" \
"s_waitcnt vmcnt(3) lgkmcnt(0)\n" \
M_"a[112:115], a[224:227], a[220:223], a[112:115]\n" \
"s_barrier\n" \
"v_cmp_ne_u32_e32 vcc, s62, v44\n" \
"s_nop 4\n" \
"s_cbranch_vccnz L_poll60_%=\n" \
"L_pollr60_%=:\n" \
"s_mov_b32 s68, s12\n" \
"s_and_b32 s69, s13, 0xffff\n" \
"s_mov_b32 s70, 0x480000\n" \
"s_mov_b32 s71, 0x20000\n" \
M_"v[52:55], a[176:179], a[128:131], v[52:55]\n" \
"s_add_u32 m0, s40, 0x0\n" \
"s_add_u32 s85, s39, 0x0\n" \
"s_nop 0\n" \
M_"v[52:55], a[240:243], a[192:195], v[52:55]\n" \
B_"v20, s[80:83], s85 offen sc1 lds\n" \
M_"v[56:59], a[180:183], a[128:131], v[56:59]\n" \
X_"a[160:161], v32 offset:32768\n" \
M_"v[56:59], a[244:247], a[192:195], v[56:59]\n" \
"s_add_u32 m0, s40, 0x1000\n" \
"s_add_u32 s85, s39, 0x1000\n" \
M_"v[60:63], a[184:187], a[128:131], v[60:63]\n" \
B_"v20, s[80:83], s85 offen sc1 lds\n" \
M_"v[60:63], a[248:251], a[192:195], v[60:63]\n" \
X_"a[162:163], v32 offset:33792\n" \
M_"v[64:67], a[188:191], a[128:131], v[64:67]\n" \
X_"a[224:225], v32 offset:40960\n" \
M_"v[64:67], a[252:255], a[192:195], v[64:67]\n" \
R_"a[128:131], v28 offset:49152\n" \
M_"v[80:83], a[188:191], a[132:135], v[80:83]\n" \
R_"a[192:195], v29 offset:49152\n" \
M_"v[80:83], a[252:255], a[196:199], v[80:83]\n" \
"s_add_u32 m0, s40, 0x3000\n" \
"s_movk_i32 s84, 0x0\n" \
M_"v[76:79], a[184:187], a[132:135], v[76:79]\n" \
B_"v21, s[76:79], s84 offen offset:0 lds\n" \
M_"v[76:79], a[248:251], a[196:199], v[76:79]\n" \
"s_add_u32 m0, s30, 0x0\n" \
"s_movk_i32 s84, 0x0\n" \
"s_nop 0\n" \
M_"v[72:75], a[180:183], a[132:135], v[72:75]\n" \
B_"v24, s[76:79], s84 offen offset:0 lds\n" \
M_"v[72:75], a[244:247], a[196:199], v[72:75]\n" \
X_"a[226:227], v32 offset:41984\n" \
M_"v[68:71], a[176:179], a[132:135], v[68:71]\n" \
X_"a[164:165], v33 offset:32768\n" \
M_"v[68:71], a[240:243], a[196:199], v[68:71]\n" \
R_"a[132:135], v28 offset:51200\n" \
M_"v[84:87], a[176:179], a[136:139], v[84:87]\n" \
R_"a[196:199], v29 offset:51200\n" \
M_"v[84:87], a[240:243], a[200:203], v[84:87]\n" \
"s_movk_i32 s84, 0x2000\n" \
M_"v[88:91], a[180:183], a[136:139], v[88:91]\n" \
B_"v25, s[76:79], s84 offen offset:1024 lds\n" \
M_"v[88:91], a[244:247], a[200:203], v[88:91]\n" \
"s_movk_i32 s84, 0x4000\n" \
M_"v[92:95], a[184:187], a[136:139], v[92:95]\n" \
B_"v24, s[76:79], s84 offen offset:2048 lds\n" \
M_"v[92:95], a[248:251], a[200:203], v[92:95]\n" \
X_"a[166:167], v33 offset:33792\n" \
M_"v[96:99], a[188:191], a[136:139], v[96:99]\n" \
X_"a[228:229], v33 offset:40960\n" \
M_"v[96:99], a[252:255], a[200:203], v[96:99]\n" \
R_"a[136:139], v28 offset:53248\n" \
M_"v[112:115], a[188:191], a[140:143], v[112:115]\n" \
R_"a[200:203], v29 offset:53248\n" \
M_"v[112:115], a[252:255], a[204:207], v[112:115]\n" \
"s_movk_i32 s84, 0x6000\n" \
M_"v[108:111], a[184:187], a[140:143], v[108:111]\n" \
B_"v25, s[76:79], s84 offen offset:3072 lds\n" \
M_"v[108:111], a[248:251], a[204:207], v[108:111]\n" \
"s_add_u32 m0, s31, 0x0\n" \
"s_movk_i32 s84, 0x0\n" \
M_"v[104:107], a[180:183], a[140:143], v[104:107]\n" \
B_"v26, s[68:71], s84 offen offset:0 lds\n" \
M_"v[104:107], a[244:247], a[204:207], v[104:107]\n" \
"s_movk_i32 s84, 0x7c00\n" \
M_"v[100:103], a[176:179], a[140:143], v[100:103]\n" \
B_"v26, s[68:71], s84 offen offset:1024 lds\n" \
M_"v[100:103], a[240:243], a[204:207], v[100:103]\n" \
R_"a[140:143], v28 offset:55296\n" \
M_"v[116:119], a[176:179], a[144:147], v[116:119]\n" \
R_"a[204:207], v29 offset:55296\n" \
M_"v[116:119], a[240:243], a[208:211], v[116:119]\n" \
"s_addk_i32 s84, 0x7c00\n" \
M_"v[120:123], a[180:183], a[144:147], v[120:123]\n" \
B_"v27, s[68:71], s84 offen offset:2048 lds\n" \
M_"v[120:123], a[244:247], a[208:211], v[120:123]\n" \
X_"a[230:231], v33 offset:41984\n" \
M_"v[124:127], a[184:187], a[144:147], v[124:127]\n" \
"s_addk_i32 s84, 0x7c00\n" \
M_"v[124:127], a[248:251], a[208:211], v[124:127]\n" \
B_"v27, s[68:71], s84 offen offset:3072 lds\n" \
M_"v[128:131], a[188:191], a[144:147], v[128:131]\n" \
X_"a[168:169], v34 offset:32768\n" \
M_"v[128:131], a[252:255], a[208:211], v[128:131]\n" \
R_"a[144:147], v28 offset:32768\n" \
M_"v[144:147], a[188:191], a[148:151], v[144:147]\n" \
R_"a[208:211], v29 offset:32768\n" \
M_"v[144:147], a[252:255], a[212:215], v[144:147]\n" \
"s_add_u32 m0, s30, 0x1000\n" \
"s_mov_b32 s84, 0x9000\n" \
M_"v[140:143], a[184:187], a[148:151], v[140:143]\n" \
B_"v24, s[76:79], s84 offen offset:0 lds\n" \
M_"v[140:143], a[248:251], a[212:215], v[140:143]\n" \
X_"a[170:171], v34 offset:33792\n" \
M_"v[136:139], a[180:183], a[148:151], v[136:139]\n" \
"s_addk_i32 s84, 0x2000\n" \
"s_nop 0\n" \
M_"v[136:139], a[244:247], a[212:215], v[136:139]\n" \
B_"v25, s[76:79], s84 offen offset:1024 lds\n" \
M_"v[132:135], a[176:179], a[148:151], v[132:135]\n" \
X_"a[232:233], v34 offset:40960\n" \
M_"v[132:135], a[240:243], a[212:215], v[132:135]\n" \
R_"a[148:151], v28 offset:34816\n" \
M_"v[148:151], a[176:179], a[152:155], v[148:151]\n" \
R_"a[212:215], v29 offset:34816\n" \
M_"v[148:151], a[240:243], a[216:219], v[148:151]\n" \
X_"a[234:235], v34 offset:41984\n" \
M_"v[152:155], a[180:183], a[152:155], v[152:155]\n" \
X_"a[172:173], v35 offset:32768\n" \
M_"v[152:155], a[244:247], a[216:219], v[152:155]\n" \
X_"a[174:175], v35 offset:33792\n" \
M_"v[156:159], a[184:187], a[152:155], v[156:159]\n" \
X_"a[236:237], v35 offset:40960\n" \
M_"v[156:159], a[248:251], a[216:219], v[156:159]\n" \
X_"a[238:239], v35 offset:41984\n" \
M_"v[160:163], a[188:191], a[152:155], v[160:163]\n" \
M_"v[160:163], a[252:255], a[216:219], v[160:163]\n" \
R_"a[152:155], v28 offset:36864\n" \
M_"v[176:179], a[188:191], a[156:159], v[176:179]\n" \
R_"a[216:219], v29 offset:36864\n" \
M_"v[176:179], a[252:255], a[220:223], v[176:179]\n" \
M_"v[172:175], a[184:187], a[156:159], v[172:175]\n" \
M_"v[172:175], a[248:251], a[220:223], v[172:175]\n" \
M_"v[168:171], a[180:183], a[156:159], v[168:171]\n" \
M_"v[168:171], a[244:247], a[220:223], v[168:171]\n" \
M_"v[164:167], a[176:179], a[156:159], v[164:167]\n" \
M_"v[164:167], a[240:243], a[220:223], v[164:167]\n" \
R_"a[156:159], v28 offset:38912\n" \
R_"a[220:223], v29 offset:38912\n" \
"s_waitcnt lgkmcnt(15)\n" \
M_"a[0:3], a[160:163], a[128:131], a[0:3]\n" \
"s_addk_i32 s84, 0x2000\n" \
M_"a[0:3], a[224:227], a[192:195], a[0:3]\n" \
B_"v24, s[76:79], s84 offen offset:2048 lds\n" \
M_"a[4:7], a[164:167], a[128:131], a[4:7]\n" \
X_"a[176:177], v36 offset:32768\n" \
M_"a[4:7], a[228:231], a[192:195], a[4:7]\n" \
"s_addk_i32 s84, 0x2000\n" \
"s_waitcnt lgkmcnt(13)\n" \
M_"a[8:11], a[168:171], a[128:131], a[8:11]\n" \
B_"v25, s[76:79], s84 offen offset:3072 lds\n" \
"s_waitcnt lgkmcnt(9)\n" \
M_"a[8:11], a[232:235], a[192:195], a[8:11]\n" \
X_"a[178:179], v36 offset:33792\n" \
"s_waitcnt lgkmcnt(8)\n" \
M_"a[12:15], a[172:175], a[128:131], a[12:15]\n" \
X_"a[240:241], v36 offset:40960\n" \
"v_accvgpr_read_b32 v184, a0\n" \
"s_waitcnt lgkmcnt(7)\n" \
M_"a[12:15], a[236:239], a[192:195], a[12:15]\n" \
"v_accvgpr_read_b32 v185, a1\n" \
"v_accvgpr_read_b32 v186, a2\n" \
M_"a[28:31], a[172:175], a[132:135], a[28:31]\n" \
"v_accvgpr_read_b32 v187, a3\n" \
"v_accvgpr_read_b32 v180, a4\n" \
M_"a[28:31], a[236:239], a[196:199], a[28:31]\n" \
"s_add_u32 m0, s30, 0x20000\n" \
"s_movk_i32 s84, 0x100\n" \
M_"a[24:27], a[168:171], a[132:135], a[24:27]\n" \
B_"v24, s[76:79], s84 offen offset:0 lds\n" \
M_"a[24:27], a[232:235], a[196:199], a[24:27]\n" \
X_"a[242:243], v36 offset:41984\n" \
"v_accvgpr_read_b32 v181, a5\n" \
M_"a[20:23], a[164:167], a[132:135], a[20:23]\n" \
"v_accvgpr_read_b32 v182, a6\n" \
"v_accvgpr_read_b32 v183, a7\n" \
M_"a[20:23], a[228:231], a[196:199], a[20:23]\n" \
X_"a[180:181], v37 offset:32768\n" \
C_"v184, v184, v185\n" \
M_"a[16:19], a[160:163], a[132:135], a[16:19]\n" \
"s_movk_i32 s84, 0x2100\n" \
"s_nop 0\n" \
M_"a[16:19], a[224:227], a[196:199], a[16:19]\n" \
B_"v25, s[76:79], s84 offen offset:1024 lds\n" \
M_"a[32:35], a[160:163], a[136:139], a[32:35]\n" \
X_"a[182:183], v37 offset:33792\n" \
C_"v185, v186, v187\n" \
M_"a[32:35], a[224:227], a[200:203], a[32:35]\n" \
C_"v186, v180, v181\n" \
C_"v187, v182, v183\n" \
M_"a[36:39], a[164:167], a[136:139], a[36:39]\n" \
"v_accvgpr_read_b32 v188, a8\n" \
"v_accvgpr_read_b32 v189, a9\n" \
M_"a[36:39], a[228:231], a[200:203], a[36:39]\n" \
"s_movk_i32 s84, 0x4100\n" \
"s_nop 0\n" \
M_"a[40:43], a[168:171], a[136:139], a[40:43]\n" \
B_"v24, s[76:79], s84 offen offset:2048 lds\n" \
M_"a[40:43], a[232:235], a[200:203], a[40:43]\n" \
X_"a[244:245], v37 offset:40960\n" \
"v_accvgpr_read_b32 v190, a10\n" \
M_"a[44:47], a[172:175], a[136:139], a[44:47]\n" \
X_"a[246:247], v37 offset:41984\n" \
"v_accvgpr_read_b32 v191, a11\n" \
M_"a[44:47], a[236:239], a[200:203], a[44:47]\n" \
"v_accvgpr_read_b32 v180, a12\n" \
"v_accvgpr_read_b32 v181, a13\n" \
M_"a[60:63], a[172:175], a[140:143], a[60:63]\n" \
"s_movk_i32 s84, 0x6100\n" \
M_"a[60:63], a[236:239], a[204:207], a[60:63]\n" \
B_"v25, s[76:79], s84 offen offset:3072 lds\n" \
M_"a[56:59], a[168:171], a[140:143], a[56:59]\n" \
X_"a[184:185], v38 offset:32768\n" \
"v_accvgpr_read_b32 v182, a14\n" \
M_"a[56:59], a[232:235], a[204:207], a[56:59]\n" \
"v_accvgpr_read_b32 v183, a15\n" \
C_"v188, v188, v189\n" \
M_"a[52:55], a[164:167], a[140:143], a[52:55]\n" \
"s_add_u32 m0, s30, 0x21000\n" \
"s_addk_i32 s84, 0x3000\n" \
M_"a[52:55], a[228:231], a[204:207], a[52:55]\n" \
B_"v24, s[76:79], s84 offen offset:0 lds\n" \
M_"a[48:51], a[160:163], a[140:143], a[48:51]\n" \
X_"a[186:187], v38 offset:33792\n" \
C_"v189, v190, v191\n" \
M_"a[48:51], a[224:227], a[204:207], a[48:51]\n" \
C_"v190, v180, v181\n" \
C_"v191, v182, v183\n" \
M_"a[64:67], a[160:163], a[144:147], a[64:67]\n" \
X_"a[248:249], v38 offset:40960\n" \
"v_accvgpr_read_b32 v192, a24\n" \
M_"a[64:67], a[224:227], a[208:211], a[64:67]\n" \
"s_addk_i32 s84, 0x2000\n" \
"s_nop 0\n" \
M_"a[68:71], a[164:167], a[144:147], a[68:71]\n" \
B_"v25, s[76:79], s84 offen offset:1024 lds\n" \
M_"a[68:71], a[228:231], a[208:211], a[68:71]\n" \
X_"a[250:251], v38 offset:41984\n" \
"v_accvgpr_read_b32 v193, a25\n" \
M_"a[72:75], a[168:171], a[144:147], a[72:75]\n" \
"v_accvgpr_read_b32 v194, a26\n" \
"v_accvgpr_read_b32 v195, a27\n" \
M_"a[72:75], a[232:235], a[208:211], a[72:75]\n" \
"v_accvgpr_read_b32 v180, a28\n" \
"v_accvgpr_read_b32 v181, a29\n" \
M_"a[76:79], a[172:175], a[144:147], a[76:79]\n" \
"s_addk_i32 s84, 0x2000\n" \
M_"a[76:79], a[236:239], a[208:211], a[76:79]\n" \
B_"v24, s[76:79], s84 offen offset:2048 lds\n" \
M_"a[92:95], a[172:175], a[148:151], a[92:95]\n" \
"s_add_u32 s89, s41, 0x0\n" \
"buffer_store_dwordx4 v[184:187], v20, s[80:83], s89 offen offset:0 sc1\n" \
M_"a[92:95], a[236:239], a[212:215], a[92:95]\n" \
X_"a[188:189], v39 offset:32768\n" \
"v_accvgpr_read_b32 v182, a30\n" \
M_"a[88:91], a[168:171], a[148:151], a[88:91]\n" \
X_"a[190:191], v39 offset:33792\n" \
"v_accvgpr_read_b32 v183, a31\n" \
M_"a[88:91], a[232:235], a[212:215], a[88:91]\n" \
"s_addk_i32 s84, 0x2000\n" \
M_"a[84:87], a[164:167], a[148:151], a[84:87]\n" \
B_"v25, s[76:79], s84 offen offset:3072 lds\n" \
M_"a[84:87], a[228:231], a[212:215], a[84:87]\n" \
X_"a[252:253], v39 offset:40960\n" \
C_"v192, v192, v193\n" \
M_"a[80:83], a[160:163], a[148:151], a[80:83]\n" \
"s_add_u32 s88, s41, 0x0\n" \
"buffer_store_dwordx4 v[188:191], v20, s[80:83], s88 offen offset:1024 sc1\n" \
M_"a[80:83], a[224:227], a[212:215], a[80:83]\n" \
C_"v193, v194, v195\n" \
C_"v194, v180, v181\n" \
"s_waitcnt lgkmcnt(15)\n" \
"s_nop 0\n" \
M_"a[96:99], a[160:163], a[152:155], a[96:99]\n" \
X_"a[254:255], v39 offset:41984\n" \
C_"v195, v182, v183\n" \
M_"a[96:99], a[224:227], a[216:219], a[96:99]\n" \
"v_accvgpr_read_b32 v196, a16\n" \
"v_accvgpr_read_b32 v197, a17\n" \
M_"a[100:103], a[164:167], a[152:155], a[100:103]\n" \
"v_accvgpr_read_b32 v198, a18\n" \
"v_accvgpr_read_b32 v199, a19\n" \
M_"a[100:103], a[228:231], a[216:219], a[100:103]\n" \
"v_accvgpr_read_b32 v180, a20\n" \
"v_accvgpr_read_b32 v181, a21\n" \
M_"a[104:107], a[168:171], a[152:155], a[104:107]\n" \
"s_add_u32 s89, s41, 0x1000\n" \
"buffer_store_dwordx4 v[192:195], v20, s[80:83], s89 offen offset:1024 sc1\n" \
M_"a[104:107], a[232:235], a[216:219], a[104:107]\n" \
"v_accvgpr_read_b32 v182, a22\n" \
"v_accvgpr_read_b32 v183, a23\n" \
M_"a[108:111], a[172:175], a[152:155], a[108:111]\n" \
C_"v196, v196, v197\n" \
C_"v197, v198, v199\n" \
M_"a[108:111], a[236:239], a[216:219], a[108:111]\n" \
C_"v198, v180, v181\n" \
C_"v199, v182, v183\n" \
M_"a[124:127], a[172:175], a[156:159], a[124:127]\n" \
"v_accvgpr_read_b32 v200, a32\n" \
"v_accvgpr_read_b32 v201, a33\n" \
M_"a[124:127], a[236:239], a[220:223], a[124:127]\n" \
"v_accvgpr_read_b32 v202, a34\n" \
"v_accvgpr_read_b32 v203, a35\n" \
M_"a[120:123], a[168:171], a[156:159], a[120:123]\n" \
"s_add_u32 s88, s41, 0x1000\n" \
"buffer_store_dwordx4 v[196:199], v20, s[80:83], s88 offen offset:0 sc1\n" \
M_"a[120:123], a[232:235], a[220:223], a[120:123]\n" \
"v_accvgpr_read_b32 v180, a36\n" \
"v_accvgpr_read_b32 v181, a37\n" \
M_"a[116:119], a[164:167], a[156:159], a[116:119]\n" \
"v_accvgpr_read_b32 v182, a38\n" \
"v_accvgpr_read_b32 v183, a39\n" \
M_"a[116:119], a[228:231], a[220:223], a[116:119]\n" \
C_"v200, v200, v201\n" \
C_"v201, v202, v203\n" \
M_"a[112:115], a[160:163], a[156:159], a[112:115]\n" \
C_"v202, v180, v181\n" \
C_"v203, v182, v183\n" \
M_"a[112:115], a[224:227], a[220:223], a[112:115]\n" \
"v_accvgpr_read_b32 v204, a40\n" \
"v_accvgpr_read_b32 v205, a41\n" \
"s_waitcnt vmcnt(12) lgkmcnt(0)\n" \
"s_barrier\n" \
"v_mov_b32_e32 v36, v32\n" \
"v_xor_b32_e32 v32, 0x0, v48\n" \
"v_mov_b32_e32 v37, v33\n" \
"v_xor_b32_e32 v33, 0x20, v48\n" \
"v_mov_b32_e32 v38, v34\n" \
"v_xor_b32_e32 v34, 0x40, v48\n" \
"v_mov_b32_e64 v39, v35\n" \
"v_xor_b32_e32 v35, 0x60, v48\n" \
"v_add_u32_e32 v22, s44, v28\n" \
"v_add_u32_e32 v28, s43, v28\n" \
"v_add_u32_e32 v23, s44, v29\n" \
"v_add_u32_e32 v29, s43, v29\n" \
M_"v[52:55], a[176:179], a[128:131], v[52:55]\n" \
"s_add_u32 m0, s40, 0x8000\n" \
"s_add_u32 s85, s39, 0x2000\n" \
M_"v[52:55], a[240:243], a[192:195], v[52:55]\n" \
B_"v20, s[80:83], s85 offen sc1 lds\n" \
M_"v[56:59], a[180:183], a[128:131], v[56:59]\n" \
X_"a[160:161], v32 offset:0\n" \
"v_accvgpr_read_b32 v206, a42\n" \
M_"v[56:59], a[244:247], a[192:195], v[56:59]\n" \
"s_add_u32 m0, s40, 0x9000\n" \
"s_add_u32 s85, s39, 0x3000\n" \
M_"v[60:63], a[184:187], a[128:131], v[60:63]\n" \
B_"v20, s[80:83], s85 offen sc1 lds\n" \
M_"v[60:63], a[248:251], a[192:195], v[60:63]\n" \
X_"a[162:163], v32 offset:512\n" \
"v_accvgpr_read_b32 v207, a43\n" \
M_"v[64:67], a[188:191], a[128:131], v[64:67]\n" \
X_"a[224:225], v32 offset:4096\n" \
"v_accvgpr_read_b32 v180, a44\n" \
M_"v[64:67], a[252:255], a[192:195], v[64:67]\n" \
R_"a[128:131], v28 offset:0\n" \
"v_accvgpr_read_b32 v181, a45\n" \
M_"v[80:83], a[188:191], a[132:135], v[80:83]\n" \
R_"a[192:195], v29 offset:0\n" \
"v_accvgpr_read_b32 v182, a46\n" \
M_"v[80:83], a[252:255], a[196:199], v[80:83]\n" \
"s_add_u32 m0, s40, 0xb000\n" \
"s_movk_i32 s84, 0x80\n" \
M_"v[76:79], a[184:187], a[132:135], v[76:79]\n" \
B_"v21, s[76:79], s84 offen offset:0 lds\n" \
M_"v[76:79], a[248:251], a[196:199], v[76:79]\n" \
"s_add_u32 m0, s30, 0x8000\n" \
"s_movk_i32 s84, 0x80\n" \
M_"v[72:75], a[180:183], a[132:135], v[72:75]\n" \
B_"v24, s[76:79], s84 offen offset:0 lds\n" \
M_"v[72:75], a[244:247], a[196:199], v[72:75]\n" \
X_"a[226:227], v32 offset:4608\n" \
"v_accvgpr_read_b32 v183, a47\n" \
M_"v[68:71], a[176:179], a[132:135], v[68:71]\n" \
X_"a[164:165], v33 offset:0\n" \
C_"v204, v204, v205\n" \
M_"v[68:71], a[240:243], a[196:199], v[68:71]\n" \
R_"a[132:135], v28 offset:2048\n" \
C_"v205, v206, v207\n" \
M_"v[84:87], a[176:179], a[136:139], v[84:87]\n" \
R_"a[196:199], v29 offset:2048\n" \
C_"v206, v180, v181\n" \
M_"v[84:87], a[240:243], a[200:203], v[84:87]\n" \
"s_movk_i32 s84, 0x2080\n" \
M_"v[88:91], a[180:183], a[136:139], v[88:91]\n" \
B_"v25, s[76:79], s84 offen offset:1024 lds\n" \
M_"v[88:91], a[244:247], a[200:203], v[88:91]\n" \
"s_movk_i32 s84, 0x4080\n" \
M_"v[92:95], a[184:187], a[136:139], v[92:95]\n" \
B_"v24, s[76:79], s84 offen offset:2048 lds\n" \
M_"v[92:95], a[248:251], a[200:203], v[92:95]\n" \
X_"a[166:167], v33 offset:512\n" \
C_"v207, v182, v183\n" \
M_"v[96:99], a[188:191], a[136:139], v[96:99]\n" \
X_"a[228:229], v33 offset:4096\n" \
"v_accvgpr_read_b32 v208, a56\n" \
M_"v[96:99], a[252:255], a[200:203], v[96:99]\n" \
R_"a[136:139], v28 offset:4096\n" \
"v_accvgpr_read_b32 v209, a57\n" \
M_"v[112:115], a[188:191], a[140:143], v[112:115]\n" \
R_"a[200:203], v29 offset:4096\n" \
"v_accvgpr_read_b32 v210, a58\n" \
M_"v[112:115], a[252:255], a[204:207], v[112:115]\n" \
"s_movk_i32 s84, 0x6080\n" \
M_"v[108:111], a[184:187], a[140:143], v[108:111]\n" \
B_"v25, s[76:79], s84 offen offset:3072 lds\n" \
M_"v[108:111], a[248:251], a[204:207], v[108:111]\n" \
"s_add_u32 m0, s31, 0x8000\n" \
"s_mov_b32 s84, 0x80000\n" \
M_"v[104:107], a[180:183], a[140:143], v[104:107]\n" \
B_"v26, s[68:71], s84 offen offset:0 lds\n" \
M_"v[104:107], a[244:247], a[204:207], v[104:107]\n" \
"s_addk_i32 s84, 0x7c00\n" \
M_"v[100:103], a[176:179], a[140:143], v[100:103]\n" \
B_"v26, s[68:71], s84 offen offset:1024 lds\n" \
M_"v[100:103], a[240:243], a[204:207], v[100:103]\n" \
R_"a[140:143], v28 offset:6144\n" \
"v_accvgpr_read_b32 v211, a59\n" \
M_"v[116:119], a[176:179], a[144:147], v[116:119]\n" \
R_"a[204:207], v29 offset:6144\n" \
"v_accvgpr_read_b32 v180, a60\n" \
M_"v[116:119], a[240:243], a[208:211], v[116:119]\n" \
"s_addk_i32 s84, 0x7c00\n" \
M_"v[120:123], a[180:183], a[144:147], v[120:123]\n" \
B_"v27, s[68:71], s84 offen offset:2048 lds\n" \
M_"v[120:123], a[244:247], a[208:211], v[120:123]\n" \
X_"a[230:231], v33 offset:4608\n" \
"v_accvgpr_read_b32 v181, a61\n" \
M_"v[124:127], a[184:187], a[144:147], v[124:127]\n" \
"s_addk_i32 s84, 0x7c00\n" \
M_"v[124:127], a[248:251], a[208:211], v[124:127]\n" \
B_"v27, s[68:71], s84 offen offset:3072 lds\n" \
M_"v[128:131], a[188:191], a[144:147], v[128:131]\n" \
X_"a[172:173], v35 offset:0\n" \
"v_accvgpr_read_b32 v182, a62\n" \
M_"v[128:131], a[252:255], a[208:211], v[128:131]\n" \
R_"a[144:147], v28 offset:8192\n" \
"v_accvgpr_read_b32 v183, a63\n" \
M_"v[144:147], a[188:191], a[148:151], v[144:147]\n" \
R_"a[208:211], v29 offset:8192\n" \
C_"v208, v208, v209\n" \
M_"v[144:147], a[252:255], a[212:215], v[144:147]\n" \
"s_add_u32 m0, s30, 0x9000\n" \
"s_mov_b32 s84, 0x9080\n" \
M_"v[140:143], a[184:187], a[148:151], v[140:143]\n" \
B_"v24, s[76:79], s84 offen offset:0 lds\n" \
M_"v[140:143], a[248:251], a[212:215], v[140:143]\n" \
X_"a[174:175], v35 offset:512\n" \
C_"v209, v210, v211\n" \
M_"v[136:139], a[180:183], a[148:151], v[136:139]\n" \
"s_addk_i32 s84, 0x2000\n" \
"s_nop 0\n" \
M_"v[136:139], a[244:247], a[212:215], v[136:139]\n" \
B_"v25, s[76:79], s84 offen offset:1024 lds\n" \
M_"v[132:135], a[176:179], a[148:151], v[132:135]\n" \
X_"a[236:237], v35 offset:4096\n" \
C_"v210, v180, v181\n" \
M_"v[132:135], a[240:243], a[212:215], v[132:135]\n" \
R_"a[148:151], v28 offset:10240\n" \
C_"v211, v182, v183\n" \
M_"v[148:151], a[176:179], a[152:155], v[148:151]\n" \
R_"a[212:215], v29 offset:10240\n" \
"v_accvgpr_read_b32 v212, a48\n" \
M_"v[148:151], a[240:243], a[216:219], v[148:151]\n" \
X_"a[238:239], v35 offset:4608\n" \
"v_accvgpr_read_b32 v213, a49\n" \
M_"v[152:155], a[180:183], a[152:155], v[152:155]\n" \
X_"a[168:169], v34 offset:0\n" \
"v_accvgpr_read_b32 v214, a50\n" \
M_"v[152:155], a[244:247], a[216:219], v[152:155]\n" \
X_"a[170:171], v34 offset:512\n" \
"v_accvgpr_read_b32 v215, a51\n" \
M_"v[156:159], a[184:187], a[152:155], v[156:159]\n" \
X_"a[232:233], v34 offset:4096\n" \
"v_accvgpr_read_b32 v180, a52\n" \
M_"v[156:159], a[248:251], a[216:219], v[156:159]\n" \
X_"a[234:235], v34 offset:4608\n" \
"v_accvgpr_read_b32 v181, a53\n" \
M_"v[160:163], a[188:191], a[152:155], v[160:163]\n" \
R_"v[248:251], v22 offset:0\n" \
"v_accvgpr_read_b32 v182, a54\n" \
M_"v[160:163], a[252:255], a[216:219], v[160:163]\n" \
R_"a[152:155], v28 offset:12288\n" \
"v_accvgpr_read_b32 v183, a55\n" \
M_"v[176:179], a[188:191], a[156:159], v[176:179]\n" \
R_"a[216:219], v29 offset:12288\n" \
C_"v212, v212, v213\n" \
M_"v[176:179], a[252:255], a[220:223], v[176:179]\n" \
R_"v[48:51], v23 offset:0\n" \
C_"v213, v214, v215\n" \
M_"v[172:175], a[184:187], a[156:159], v[172:175]\n" \
C_"v214, v180, v181\n" \
C_"v215, v182, v183\n" \
M_"v[172:175], a[248:251], a[220:223], v[172:175]\n" \
"v_accvgpr_read_b32 v216, a64\n" \
"v_accvgpr_read_b32 v217, a65\n" \
M_"v[168:171], a[180:183], a[156:159], v[168:171]\n" \
"v_accvgpr_read_b32 v218, a66\n" \
"v_accvgpr_read_b32 v219, a67\n" \
M_"v[168:171], a[244:247], a[220:223], v[168:171]\n" \
"v_accvgpr_read_b32 v180, a68\n" \
"v_accvgpr_read_b32 v181, a69\n" \
M_"v[164:167], a[176:179], a[156:159], v[164:167]\n" \
"v_accvgpr_read_b32 v182, a70\n" \
"v_accvgpr_read_b32 v183, a71\n" \
M_"v[164:167], a[240:243], a[220:223], v[164:167]\n" \
R_"a[156:159], v28 offset:14336\n" \
R_"a[220:223], v29 offset:14336\n" \
"s_waitcnt lgkmcnt(15)\n" \
M_"a[0:3], a[160:163], a[128:131], 0\n" \
"s_addk_i32 s84, 0x2000\n" \
C_"v216, v216, v217\n" \
M_"a[0:3], a[224:227], a[192:195], a[0:3]\n" \
B_"v24, s[76:79], s84 offen offset:2048 lds\n" \
C_"v217, v218, v219\n" \
M_"a[4:7], a[164:167], a[128:131], 0\n" \
X_"a[176:177], v36 offset:0\n" \
C_"v218, v180, v181\n" \
M_"a[4:7], a[228:231], a[192:195], a[4:7]\n" \
"s_addk_i32 s84, 0x2000\n" \
C_"v219, v182, v183\n" \
"s_waitcnt lgkmcnt(15)\n" \
M_"a[12:15], a[172:175], a[128:131], 0\n" \
B_"v25, s[76:79], s84 offen offset:3072 lds\n" \
".fill (L_pb + 20192 - .) / 4, 4, 0xBF800000\n" \
"L_pp5:\n" \
"s_cbranch_execz L_pret\n" \
"v_accvgpr_read_b32 v220, a80\n" \
"s_waitcnt lgkmcnt(11)\n" \
M_"a[12:15], a[236:239], a[192:195], a[12:15]\n" \
X_"a[178:179], v36 offset:1024\n" \
"v_accvgpr_read_b32 v221, a81\n" \
"s_waitcnt lgkmcnt(10)\n" \
M_"a[8:11], a[128:131], a[168:171], 0\n" \
X_"a[240:241], v36 offset:8192\n" \
"v_accvgpr_read_b32 v222, a82\n" \
"s_waitcnt lgkmcnt(9)\n" \
M_"a[8:11], a[192:195], a[232:235], a[8:11]\n" \
X_"a[242:243], v36 offset:9216\n" \
"v_accvgpr_read_b32 v223, a83\n" \
M_"a[24:27], a[132:135], a[168:171], 0\n" \
X_"a[180:181], v37 offset:0\n" \
"v_accvgpr_read_b32 v180, a84\n" \
M_"a[24:27], a[196:199], a[232:235], a[24:27]\n" \
X_"a[182:183], v37 offset:1024\n" \
"v_accvgpr_read_b32 v181, a85\n" \
M_"a[28:31], a[172:175], a[132:135], 0\n" \
X_"a[244:245], v37 offset:8192\n" \
"v_accvgpr_read_b32 v182, a86\n" \
M_"a[28:31], a[236:239], a[196:199], a[28:31]\n" \
"s_add_u32 s89, s41, 0x2000\n" \
"buffer_store_dwordx4 v[200:203], v20, s[80:83], s89 offen offset:0 sc1\n" \
M_"a[20:23], a[164:167], a[132:135], 0\n" \
X_"a[246:247], v37 offset:9216\n" \
"v_accvgpr_read_b32 v183, a87\n" \
M_"a[20:23], a[228:231], a[196:199], a[20:23]\n" \
X_"a[184:185], v38 offset:0\n" \
C_"v220, v220, v221\n" \
M_"a[16:19], a[160:163], a[132:135], 0\n" \
X_"a[186:187], v38 offset:1024\n" \
C_"v221, v222, v223\n" \
M_"a[16:19], a[224:227], a[196:199], a[16:19]\n" \
C_"v222, v180, v181\n" \
C_"v223, v182, v183\n" \
C_"v224, v52, v53\n" \
M_"a[32:35], a[160:163], a[136:139], 0\n" \
"s_add_u32 s88, s41, 0x2000\n" \
"buffer_store_dwordx4 v[204:207], v20, s[80:83], s88 offen offset:1024 sc1\n" \
M_"a[32:35], a[224:227], a[200:203], a[32:35]\n" \
X_"a[248:249], v38 offset:8192\n" \
C_"v225, v54, v55\n" \
M_"a[36:39], a[164:167], a[136:139], 0\n" \
X_"a[250:251], v38 offset:9216\n" \
C_"v226, v56, v57\n" \
M_"a[36:39], a[228:231], a[200:203], a[36:39]\n" \
X_"a[188:189], v39 offset:0\n" \
C_"v227, v58, v59\n" \
M_"a[44:47], a[172:175], a[136:139], 0\n" \
C_"v228, v60, v61\n" \
C_"v229, v62, v63\n" \
C_"v230, v64, v65\n" \
M_"a[44:47], a[236:239], a[200:203], a[44:47]\n" \
X_"a[190:191], v39 offset:1024\n" \
C_"v231, v66, v67\n" \
M_"a[40:43], a[136:139], a[168:171], 0\n" \
"s_add_u32 s89, s41, 0x3000\n" \
"buffer_store_dwordx4 v[208:211], v20, s[80:83], s89 offen offset:1024 sc1\n" \
M_"a[40:43], a[200:203], a[232:235], a[40:43]\n" \
X_"a[252:253], v39 offset:8192\n" \
C_"v232, v76, v77\n" \
M_"a[56:59], a[140:143], a[168:171], 0\n" \
X_"a[254:255], v39 offset:9216\n" \
C_"v233, v78, v79\n" \
M_"a[56:59], a[204:207], a[232:235], a[56:59]\n" \
C_"v234, v80, v81\n" \
C_"v235, v82, v83\n" \
C_"v236, v68, v69\n" \
M_"a[60:63], a[172:175], a[140:143], 0\n" \
C_"v237, v70, v71\n" \
C_"v238, v72, v73\n" \
C_"v239, v74, v75\n" \
M_"a[60:63], a[236:239], a[204:207], a[60:63]\n" \
"s_add_u32 s88, s41, 0x3000\n" \
"buffer_store_dwordx4 v[212:215], v20, s[80:83], s88 offen offset:0 sc1\n" \
M_"a[52:55], a[164:167], a[140:143], 0\n" \
C_"v240, v84, v85\n" \
C_"v241, v86, v87\n" \
C_"v242, v88, v89\n" \
M_"a[52:55], a[228:231], a[204:207], a[52:55]\n" \
C_"v243, v90, v91\n" \
C_"v244, v92, v93\n" \
C_"v245, v94, v95\n" \
M_"a[48:51], a[160:163], a[140:143], 0\n" \
C_"v246, v96, v97\n" \
C_"v247, v98, v99\n" \
C_"v184, v108, v109\n" \
M_"a[48:51], a[224:227], a[204:207], a[48:51]\n" \
C_"v185, v110, v111\n" \
C_"v186, v112, v113\n" \
C_"v187, v114, v115\n" \
"s_waitcnt lgkmcnt(15)\n" \
"s_nop 0\n" \
M_"a[64:67], a[164:167], v[248:251], 0\n" \
C_"v188, v100, v101\n" \
C_"v189, v102, v103\n" \
C_"v190, v104, v105\n" \
M_"a[64:67], a[228:231], v[48:51], a[64:67]\n" \
C_"v191, v106, v107\n" \
C_"v192, v116, v117\n" \
C_"v193, v118, v119\n" \
M_"a[80:83], a[172:175], v[248:251], 0\n" \
C_"v194, v120, v121\n" \
C_"v195, v122, v123\n" \
C_"v196, v124, v125\n" \
M_"a[80:83], a[236:239], v[48:51], a[80:83]\n" \
C_"v197, v126, v127\n" \
C_"v198, v128, v129\n" \
C_"v199, v130, v131\n" \
M_"a[68:71], v[248:251], a[168:171], 0\n" \
C_"v252, v140, v141\n" \
C_"v253, v142, v143\n" \
C_"v254, v144, v145\n" \
M_"a[68:71], v[48:51], a[232:235], a[68:71]\n" \
C_"v255, v146, v147\n" \
"v_accvgpr_write_b32 a84, v252\n" \
"v_accvgpr_write_b32 a85, v253\n" \
"s_waitcnt vmcnt(4) lgkmcnt(0)\n" \
"s_barrier\n" \
M_"v[52:55], a[176:179], a[128:131], 0\n" \
"s_add_u32 m0, s40, 0x0\n" \
"s_nop 0\n" \
"s_add_u32 s85, s39, 0x4000\n" \
"v_accvgpr_write_b32 a86, v254\n" \
M_"v[52:55], a[240:243], a[192:195], v[52:55]\n" \
B_"v20, s[80:83], s85 offen sc1 lds\n" \
"v_accvgpr_write_b32 a87, v255\n" \
M_"v[56:59], a[180:183], a[128:131], 0\n" \
X_"a[160:161], v32 offset:32768\n" \
C_"v252, v132, v133\n" \
M_"v[56:59], a[244:247], a[192:195], v[56:59]\n" \
X_"a[162:163], v32 offset:33280\n" \
C_"v253, v134, v135\n" \
M_"v[60:63], a[184:187], a[128:131], 0\n" \
"s_add_u32 m0, s40, 0x1000\n" \
"s_add_u32 s85, s39, 0x5000\n" \
C_"v254, v136, v137\n" \
M_"v[60:63], a[248:251], a[192:195], v[60:63]\n" \
B_"v20, s[80:83], s85 offen sc1 lds\n" \
C_"v255, v138, v139\n" \
M_"v[64:67], a[188:191], a[128:131], 0\n" \
X_"a[224:225], v32 offset:36864\n" \
C_"v200, v148, v149\n" \
M_"v[64:67], a[252:255], a[192:195], v[64:67]\n" \
R_"a[128:131], v28 offset:32768\n" \
C_"v201, v150, v151\n" \
M_"v[80:83], a[188:191], a[132:135], 0\n" \
R_"a[192:195], v29 offset:32768\n" \
C_"v202, v152, v153\n" \
M_"v[80:83], a[252:255], a[196:199], v[80:83]\n" \
"s_add_u32 m0, s40, 0x3000\n" \
"s_movk_i32 s84, 0x100\n" \
"s_nop 0\n" \
C_"v203, v154, v155\n" \
M_"v[76:79], a[184:187], a[132:135], 0\n" \
B_"v21, s[76:79], s84 offen offset:0 lds\n" \
C_"v204, v156, v157\n" \
M_"v[76:79], a[248:251], a[196:199], v[76:79]\n" \
X_"a[226:227], v32 offset:37376\n" \
C_"v205, v158, v159\n" \
M_"v[72:75], a[180:183], a[132:135], 0\n" \
X_"a[164:165], v33 offset:32768\n" \
C_"v206, v160, v161\n" \
M_"v[72:75], a[244:247], a[196:199], v[72:75]\n" \
C_"v207, v162, v163\n" \
C_"v208, v172, v173\n" \
C_"v209, v174, v175\n" \
M_"v[68:71], a[176:179], a[132:135], 0\n" \
X_"a[166:167], v33 offset:33280\n" \
C_"v210, v176, v177\n" \
M_"v[68:71], a[240:243], a[196:199], v[68:71]\n" \
R_"a[132:135], v28 offset:34816\n" \
C_"v211, v178, v179\n" \
M_"v[84:87], a[176:179], a[136:139], 0\n" \
R_"a[196:199], v29 offset:34816\n" \
C_"v212, v164, v165\n" \
M_"v[84:87], a[240:243], a[200:203], v[84:87]\n" \
"s_add_u32 m0, s31, 0x0\n" \
"s_mov_b32 s84, 0x100000\n" \
C_"v213, v166, v167\n" \
M_"v[88:91], a[180:183], a[136:139], 0\n" \
B_"v26, s[68:71], s84 offen offset:0 lds\n" \
C_"v214, v168, v169\n" \
M_"v[88:91], a[244:247], a[200:203], v[88:91]\n" \
"s_add_u32 s89, s41, 0x0\n" \
"buffer_store_dwordx4 v[224:227], v20, s[80:83], s89 offen offset:2048 sc1\n" \
M_"v[92:95], a[184:187], a[136:139], 0\n" \
X_"a[228:229], v33 offset:36864\n" \
C_"v215, v170, v171\n" \
M_"v[92:95], a[248:251], a[200:203], v[92:95]\n" \
X_"a[230:231], v33 offset:37376\n" \
M_"v[96:99], a[188:191], a[136:139], 0\n" \
M_"v[96:99], a[252:255], a[200:203], v[96:99]\n" \
R_"a[136:139], v28 offset:36864\n" \
M_"v[112:115], a[188:191], a[140:143], 0\n" \
R_"a[200:203], v29 offset:36864\n" \
M_"v[112:115], a[252:255], a[204:207], v[112:115]\n" \
"s_addk_i32 s84, 0x7c00\n" \
M_"v[108:111], a[184:187], a[140:143], 0\n" \
B_"v26, s[68:71], s84 offen offset:1024 lds\n" \
M_"v[108:111], a[248:251], a[204:207], v[108:111]\n" \
X_"a[172:173], v35 offset:32768\n" \
M_"v[104:107], a[180:183], a[140:143], 0\n" \
"s_addk_i32 s84, 0x7c00\n" \
M_"v[104:107], a[244:247], a[204:207], v[104:107]\n" \
B_"v27, s[68:71], s84 offen offset:2048 lds\n" \
M_"v[100:103], a[176:179], a[140:143], 0\n" \
X_"a[174:175], v35 offset:33280\n" \
M_"v[100:103], a[240:243], a[204:207], v[100:103]\n" \
R_"a[140:143], v28 offset:38912\n" \
M_"v[116:119], a[176:179], a[144:147], 0\n" \
R_"a[204:207], v29 offset:38912\n" \
M_"v[116:119], a[240:243], a[208:211], v[116:119]\n" \
"s_addk_i32 s84, 0x7c00\n" \
M_"v[120:123], a[180:183], a[144:147], 0\n" \
B_"v27, s[68:71], s84 offen offset:3072 lds\n" \
M_"v[120:123], a[244:247], a[208:211], v[120:123]\n" \
X_"a[236:237], v35 offset:36864\n" \
M_"v[124:127], a[184:187], a[144:147], 0\n" \
X_"a[238:239], v35 offset:37376\n" \
M_"v[124:127], a[248:251], a[208:211], v[124:127]\n" \
"s_add_u32 m0, s30, 0x0\n" \
"s_movk_i32 s84, 0x180\n" \
M_"v[128:131], a[188:191], a[144:147], 0\n" \
B_"v24, s[76:79], s84 offen offset:0 lds\n" \
M_"v[128:131], a[252:255], a[208:211], v[128:131]\n" \
R_"a[144:147], v28 offset:40960\n" \
M_"v[144:147], a[188:191], a[148:151], 0\n" \
R_"a[208:211], v29 offset:40960\n" \
M_"v[144:147], a[252:255], a[212:215], v[144:147]\n" \
X_"a[168:169], v34 offset:32768\n" \
M_"v[140:143], a[184:187], a[148:151], 0\n" \
"s_movk_i32 s84, 0x2180\n" \
M_"v[140:143], a[248:251], a[212:215], v[140:143]\n" \
B_"v25, s[76:79], s84 offen offset:1024 lds\n" \
M_"v[136:139], a[180:183], a[148:151], 0\n" \
X_"a[170:171], v34 offset:33280\n" \
M_"v[136:139], a[244:247], a[212:215], v[136:139]\n" \
X_"a[232:233], v34 offset:36864\n" \
M_"v[132:135], a[176:179], a[148:151], 0\n" \
X_"a[234:235], v34 offset:37376\n" \
M_"v[132:135], a[240:243], a[212:215], v[132:135]\n" \
R_"a[148:151], v28 offset:43008\n" \
M_"v[148:151], a[176:179], a[152:155], 0\n" \
R_"a[212:215], v29 offset:43008\n" \
M_"v[148:151], a[240:243], a[216:219], v[148:151]\n" \
"s_movk_i32 s84, 0x4180\n" \
M_"v[152:155], a[180:183], a[152:155], 0\n" \
B_"v24, s[76:79], s84 offen offset:2048 lds\n" \
M_"v[152:155], a[244:247], a[216:219], v[152:155]\n" \
"s_movk_i32 s84, 0x6180\n" \
M_"v[156:159], a[184:187], a[152:155], 0\n" \
B_"v25, s[76:79], s84 offen offset:3072 lds\n" \
M_"v[156:159], a[248:251], a[216:219], v[156:159]\n" \
R_"v[248:251], v22 offset:32768\n" \
M_"v[160:163], a[188:191], a[152:155], 0\n" \
R_"v[48:51], v23 offset:32768\n" \
M_"v[160:163], a[252:255], a[216:219], v[160:163]\n" \
R_"a[152:155], v28 offset:45056\n" \
M_"v[176:179], a[188:191], a[156:159], 0\n" \
R_"a[216:219], v29 offset:45056\n" \
M_"v[176:179], a[252:255], a[220:223], v[176:179]\n" \
M_"v[172:175], a[184:187], a[156:159], 0\n" \
M_"v[172:175], a[248:251], a[220:223], v[172:175]\n" \
M_"v[168:171], a[180:183], a[156:159], 0\n" \
M_"v[168:171], a[244:247], a[220:223], v[168:171]\n" \
M_"v[164:167], a[176:179], a[156:159], 0\n" \
M_"v[164:167], a[240:243], a[220:223], v[164:167]\n" \
R_"a[156:159], v28 offset:47104\n" \
R_"a[220:223], v29 offset:47104\n" \
"s_waitcnt lgkmcnt(15)\n" \
M_"a[0:3], a[160:163], a[128:131], a[0:3]\n" \
"s_add_u32 m0, s30, 0x1000\n" \
"s_addk_i32 s84, 0x3000\n" \
M_"a[0:3], a[224:227], a[192:195], a[0:3]\n" \
B_"v24, s[76:79], s84 offen offset:0 lds\n" \
M_"a[4:7], a[164:167], a[128:131], a[4:7]\n" \
X_"a[176:177], v36 offset:32768\n" \
"v_mov_b32_e64 v11, 0\n" \
M_"a[4:7], a[228:231], a[192:195], a[4:7]\n" \
X_"a[178:179], v36 offset:33792\n" \
"s_and_b32 s5, s2, 7\n" \
M_"a[12:15], a[172:175], a[128:131], a[12:15]\n" \
X_"a[240:241], v36 offset:40960\n" \
"s_lshl_b32 s6, s3, 1\n" \
"s_waitcnt lgkmcnt(15)\n" \
M_"a[12:15], a[236:239], a[192:195], a[12:15]\n" \
"s_add_u32 s88, s41, 0x0\n" \
"buffer_store_dwordx4 v[228:231], v20, s[80:83], s88 offen offset:3072 sc1\n" \
"s_waitcnt lgkmcnt(13)\n" \
M_"a[8:11], a[128:131], a[168:171], a[8:11]\n" \
X_"a[242:243], v36 offset:41984\n" \
"s_lshl_b32 s4, s5, 2\n" \
"s_waitcnt lgkmcnt(12)\n" \
M_"a[8:11], a[192:195], a[232:235], a[8:11]\n" \
X_"a[180:181], v37 offset:32768\n" \
"s_bfe_u32 s7, s2, 0x20003\n" \
M_"a[24:27], a[132:135], a[168:171], a[24:27]\n" \
X_"a[182:183], v37 offset:33792\n" \
"s_or_b32 s12, s4, s7\n" \
M_"a[24:27], a[196:199], a[232:235], a[24:27]\n" \
X_"a[244:245], v37 offset:40960\n" \
"s_and_b32 s6, s6, 4\n" \
M_"a[28:31], a[172:175], a[132:135], a[28:31]\n" \
"s_and_b32 s8, s3, 1\n" \
"s_or_b32 s8, s6, s8\n" \
M_"a[28:31], a[236:239], a[196:199], a[28:31]\n" \
"s_addk_i32 s84, 0x2000\n" \
M_"a[20:23], a[164:167], a[132:135], a[20:23]\n" \
B_"v25, s[76:79], s84 offen offset:1024 lds\n" \
M_"a[20:23], a[228:231], a[196:199], a[20:23]\n" \
X_"a[246:247], v37 offset:41984\n" \
"s_lshl_b32 s6, s12, 4\n" \
M_"a[16:19], a[160:163], a[132:135], a[16:19]\n" \
X_"a[184:185], v38 offset:32768\n" \
"s_lshl_b32 s9, s3, 2\n" \
M_"a[16:19], a[224:227], a[196:199], a[16:19]\n" \
X_"a[186:187], v38 offset:33792\n" \
"s_or_b32 s6, s6, s9\n" \
M_"a[32:35], a[160:163], a[136:139], a[32:35]\n" \
"s_addk_i32 s84, 0x2000\n" \
M_"a[32:35], a[224:227], a[200:203], a[32:35]\n" \
B_"v24, s[76:79], s84 offen offset:2048 lds\n" \
M_"a[36:39], a[164:167], a[136:139], a[36:39]\n" \
X_"a[248:249], v38 offset:40960\n" \
"s_lshr_b32 s9, s2, 5\n" \
M_"a[36:39], a[228:231], a[200:203], a[36:39]\n" \
X_"a[250:251], v38 offset:41984\n" \
"s_mul_i32 s10, s9, 0xa00\n" \
M_"a[44:47], a[172:175], a[136:139], a[44:47]\n" \
X_"a[188:189], v39 offset:32768\n" \
"s_add_i32 s10, s10, 0x5bc1000\n" \
M_"a[44:47], a[236:239], a[200:203], a[44:47]\n" \
X_"a[190:191], v39 offset:33792\n" \
"s_or_b32 s6, s6, s10\n" \
M_"a[40:43], a[136:139], a[168:171], a[40:43]\n" \
"s_add_u32 s89, s41, 0x1000\n" \
"buffer_store_dwordx4 v[232:235], v20, s[80:83], s89 offen offset:3072 sc1\n" \
M_"a[40:43], a[200:203], a[232:235], a[40:43]\n" \
X_"a[252:253], v39 offset:40960\n" \
"s_lshl_b32 s13, s9, 3\n" \
M_"a[56:59], a[140:143], a[168:171], a[56:59]\n" \
X_"a[254:255], v39 offset:41984\n" \
"v_writelane_b32 v11, s6, 0\n" \
M_"a[56:59], a[204:207], a[232:235], a[56:59]\n" \
"s_or_b32 s6, s13, s5\n" \
"v_writelane_b32 v11, s10, 1\n" \
M_"a[60:63], a[172:175], a[140:143], a[60:63]\n" \
"s_lshl_b32 s6, s6, 10\n" \
"s_lshl_b32 s10, s3, 8\n" \
M_"a[60:63], a[236:239], a[204:207], a[60:63]\n" \
"s_addk_i32 s84, 0x2000\n" \
M_"a[52:55], a[164:167], a[140:143], a[52:55]\n" \
B_"v25, s[76:79], s84 offen offset:3072 lds\n" \
M_"a[52:55], a[228:231], a[204:207], a[52:55]\n" \
"s_or_b32 s6, s6, s10\n" \
"s_or_b32 s10, s6, 0x5c00200\n" \
M_"a[48:51], a[160:163], a[140:143], a[48:51]\n" \
"v_writelane_b32 v11, s10, 2\n" \
"s_lshl_b32 s10, s7, 2\n" \
M_"a[48:51], a[224:227], a[204:207], a[48:51]\n" \
"s_or_b32 s11, s10, s6\n" \
"s_or_b32 s11, s11, 0x5c00000\n" \
"s_waitcnt lgkmcnt(15)\n" \
M_"a[64:67], a[164:167], v[248:251], a[64:67]\n" \
"v_writelane_b32 v11, s11, 3\n" \
"s_or_b32 s6, s6, 0x5c00080\n" \
M_"a[64:67], a[228:231], v[48:51], a[64:67]\n" \
"v_writelane_b32 v11, s6, 4\n" \
"s_or_b32 s6, s6, s10\n" \
M_"a[80:83], a[172:175], v[248:251], a[80:83]\n" \
"s_add_i32 s10, s3, s2\n" \
"v_writelane_b32 v11, s6, 5\n" \
M_"a[80:83], a[236:239], v[48:51], a[80:83]\n" \
"s_mov_b32 s6, 2.0\n" \
"s_mov_b32 s11, 0x5bc0000\n" \
M_"a[68:71], v[248:251], a[168:171], a[68:71]\n" \
"s_cmp_lg_u32 s10, 0\n" \
"s_cselect_b32 s10, s6, s11\n" \
"s_waitcnt vmcnt(10) lgkmcnt(0)\n" \
M_"a[68:71], v[48:51], a[232:235], a[68:71]\n" \
"s_barrier\n" \
"v_xor_b32_e32 v28, 0x20000, v28\n" \
"v_xor_b32_e32 v29, 0x20000, v29\n" \
M_"v[52:55], a[176:179], a[128:131], v[52:55]\n" \
"s_add_u32 m0, s40, 0x8000\n" \
"s_add_u32 s85, s39, 0x6000\n" \
M_"v[52:55], a[240:243], a[192:195], v[52:55]\n" \
B_"v20, s[80:83], s85 offen sc1 lds\n" \
M_"v[56:59], a[180:183], a[128:131], v[56:59]\n" \
X_"a[160:161], v32 offset:0\n" \
"s_lshr_b32 s14, s2, 3\n" \
M_"v[56:59], a[244:247], a[192:195], v[56:59]\n" \
X_"a[162:163], v32 offset:512\n" \
"v_writelane_b32 v11, s10, 57\n" \
M_"v[60:63], a[184:187], a[128:131], v[60:63]\n" \
"s_add_u32 m0, s40, 0x9000\n" \
"s_add_u32 s85, s39, 0x7000\n" \
M_"v[60:63], a[248:251], a[192:195], v[60:63]\n" \
B_"v20, s[80:83], s85 offen sc1 lds\n" \
M_"v[64:67], a[188:191], a[128:131], v[64:67]\n" \
X_"a[224:225], v32 offset:4096\n" \
"s_lshl_b32 s10, s5, 5\n" \
M_"v[64:67], a[252:255], a[192:195], v[64:67]\n" \
R_"a[128:131], v28 offset:0\n" \
"s_min_u32 s15, s3, 2\n" \
"s_nop 0\n" \
M_"v[80:83], a[188:191], a[132:135], v[80:83]\n" \
R_"a[192:195], v29 offset:0\n" \
"s_mul_i32 s11, s7, 0x120\n" \
M_"v[80:83], a[252:255], a[196:199], v[80:83]\n" \
"s_add_u32 s88, s41, 0x1000\n" \
"buffer_store_dwordx4 v[236:239], v20, s[80:83], s88 offen offset:2048 sc1\n" \
M_"v[76:79], a[184:187], a[132:135], v[76:79]\n" \
X_"a[226:227], v32 offset:4608\n" \
M_"v[76:79], a[248:251], a[196:199], v[76:79]\n" \
X_"a[164:165], v33 offset:0\n" \
M_"v[72:75], a[180:183], a[132:135], v[72:75]\n" \
"s_add_u32 m0, s40, 0xb000\n" \
"s_movk_i32 s84, 0x180\n" \
"s_nop 0\n" \
M_"v[72:75], a[244:247], a[196:199], v[72:75]\n" \
B_"v21, s[76:79], s84 offen offset:0 lds\n" \
M_"v[68:71], a[176:179], a[132:135], v[68:71]\n" \
X_"a[166:167], v33 offset:512\n" \
M_"v[68:71], a[240:243], a[196:199], v[68:71]\n" \
R_"a[132:135], v28 offset:2048\n" \
M_"v[84:87], a[176:179], a[136:139], v[84:87]\n" \
R_"a[196:199], v29 offset:2048\n" \
M_"v[84:87], a[240:243], a[200:203], v[84:87]\n" \
"s_add_u32 m0, s31, 0x8000\n" \
"s_mov_b32 s84, 0x180000\n" \
M_"v[88:91], a[180:183], a[136:139], v[88:91]\n" \
B_"v26, s[68:71], s84 offen offset:0 lds\n" \
M_"v[88:91], a[244:247], a[200:203], v[88:91]\n" \
X_"a[228:229], v33 offset:4096\n" \
M_"v[92:95], a[184:187], a[136:139], v[92:95]\n" \
X_"a[230:231], v33 offset:4608\n" \
M_"v[92:95], a[248:251], a[200:203], v[92:95]\n" \
"s_addk_i32 s84, 0x7c00\n" \
M_"v[96:99], a[188:191], a[136:139], v[96:99]\n" \
B_"v26, s[68:71], s84 offen offset:1024 lds\n" \
M_"v[96:99], a[252:255], a[200:203], v[96:99]\n" \
R_"a[136:139], v28 offset:4096\n" \
M_"v[112:115], a[188:191], a[140:143], v[112:115]\n" \
R_"a[200:203], v29 offset:4096\n" \
M_"v[112:115], a[252:255], a[204:207], v[112:115]\n" \
"s_addk_i32 s84, 0x7c00\n" \
M_"v[108:111], a[184:187], a[140:143], v[108:111]\n" \
B_"v27, s[68:71], s84 offen offset:2048 lds\n" \
M_"v[108:111], a[248:251], a[204:207], v[108:111]\n" \
X_"a[172:173], v35 offset:0\n" \
M_"v[104:107], a[180:183], a[140:143], v[104:107]\n" \
X_"a[174:175], v35 offset:512\n" \
M_"v[104:107], a[244:247], a[204:207], v[104:107]\n" \
X_"a[236:237], v35 offset:4096\n" \
M_"v[100:103], a[176:179], a[140:143], v[100:103]\n" \
"s_add_u32 s16, s22, s11\n" \
"s_addc_u32 s17, s23, 0\n" \
M_"v[100:103], a[240:243], a[204:207], v[100:103]\n" \
R_"a[140:143], v28 offset:6144\n" \
M_"v[116:119], a[176:179], a[144:147], v[116:119]\n" \
R_"a[204:207], v29 offset:6144\n" \
M_"v[116:119], a[240:243], a[208:211], v[116:119]\n" \
"s_addk_i32 s84, 0x7c00\n" \
M_"v[120:123], a[180:183], a[144:147], v[120:123]\n" \
B_"v27, s[68:71], s84 offen offset:3072 lds\n" \
M_"v[120:123], a[244:247], a[208:211], v[120:123]\n" \
"s_add_u32 m0, s30, 0x8000\n" \
"s_movk_i32 s84, 0x200\n" \
M_"v[124:127], a[184:187], a[144:147], v[124:127]\n" \
B_"v24, s[76:79], s84 offen offset:0 lds\n" \
M_"v[124:127], a[248:251], a[208:211], v[124:127]\n" \
X_"a[238:239], v35 offset:4608\n" \
M_"v[128:131], a[188:191], a[144:147], v[128:131]\n" \
"s_add_u32 s89, s41, 0x2000\n" \
"buffer_store_dwordx4 v[240:243], v20, s[80:83], s89 offen offset:2048 sc1\n" \
M_"v[128:131], a[252:255], a[208:211], v[128:131]\n" \
R_"a[144:147], v28 offset:8192\n" \
M_"v[144:147], a[188:191], a[148:151], v[144:147]\n" \
R_"a[208:211], v29 offset:8192\n" \
M_"v[144:147], a[252:255], a[212:215], v[144:147]\n" \
X_"a[168:169], v34 offset:0\n" \
M_"v[140:143], a[184:187], a[148:151], v[140:143]\n" \
X_"a[170:171], v34 offset:512\n" \
M_"v[140:143], a[248:251], a[212:215], v[140:143]\n" \
"s_movk_i32 s84, 0x2200\n" \
"s_nop 0\n" \
M_"v[136:139], a[180:183], a[148:151], v[136:139]\n" \
B_"v25, s[76:79], s84 offen offset:1024 lds\n" \
M_"v[136:139], a[244:247], a[212:215], v[136:139]\n" \
X_"a[232:233], v34 offset:4096\n" \
M_"v[132:135], a[176:179], a[148:151], v[132:135]\n" \
X_"a[234:235], v34 offset:4608\n" \
M_"v[132:135], a[240:243], a[212:215], v[132:135]\n" \
R_"a[148:151], v28 offset:10240\n" \
M_"v[148:151], a[176:179], a[152:155], v[148:151]\n" \
R_"a[212:215], v29 offset:10240\n" \
M_"v[148:151], a[240:243], a[216:219], v[148:151]\n" \
"s_movk_i32 s84, 0x4200\n" \
M_"v[152:155], a[180:183], a[152:155], v[152:155]\n" \
B_"v24, s[76:79], s84 offen offset:2048 lds\n" \
M_"v[152:155], a[244:247], a[216:219], v[152:155]\n" \
"s_movk_i32 s84, 0x6200\n" \
M_"v[156:159], a[184:187], a[152:155], v[156:159]\n" \
B_"v25, s[76:79], s84 offen offset:3072 lds\n" \
M_"v[156:159], a[248:251], a[216:219], v[156:159]\n" \
R_"v[248:251], v22 offset:0\n" \
M_"v[160:163], a[188:191], a[152:155], v[160:163]\n" \
R_"v[48:51], v23 offset:0\n" \
M_"v[160:163], a[252:255], a[216:219], v[160:163]\n" \
R_"a[152:155], v28 offset:12288\n" \
M_"v[176:179], a[188:191], a[156:159], v[176:179]\n" \
R_"a[216:219], v29 offset:12288\n" \
M_"v[176:179], a[252:255], a[220:223], v[176:179]\n" \
"s_add_u32 s18, s24, s11\n" \
"v_writelane_b32 v11, s16, 9\n" \
"s_addc_u32 s19, s25, 0\n" \
M_"v[172:175], a[184:187], a[156:159], v[172:175]\n" \
"s_and_b32 s20, s17, 0xffff\n" \
"v_writelane_b32 v11, s20, 10\n" \
M_"v[172:175], a[248:251], a[220:223], v[172:175]\n" \
"s_and_b32 s20, s19, 0xffff\n" \
"v_writelane_b32 v11, s18, 11\n" \
M_"v[168:171], a[180:183], a[156:159], v[168:171]\n" \
"s_cmpk_lt_u32 s3, 3\n" \
"s_cselect_b32 s16, s16, s18\n" \
M_"v[168:171], a[244:247], a[220:223], v[168:171]\n" \
"s_movk_i32 s18, 0xfff\n" \
"v_writelane_b32 v11, s20, 12\n" \
M_"v[164:167], a[176:179], a[156:159], v[164:167]\n" \
"s_and_b32 s20, s29, 0xffff\n" \
"v_writelane_b32 v11, s28, 13\n" \
M_"v[164:167], a[240:243], a[220:223], v[164:167]\n" \
R_"a[156:159], v28 offset:14336\n" \
R_"a[220:223], v29 offset:14336\n" \
"s_waitcnt lgkmcnt(15)\n" \
M_"a[0:3], a[160:163], a[128:131], a[0:3]\n" \
"s_add_u32 m0, s30, 0x9000\n" \
"s_addk_i32 s84, 0x3000\n" \
M_"a[0:3], a[224:227], a[192:195], a[0:3]\n" \
B_"v24, s[76:79], s84 offen offset:0 lds\n" \
M_"a[4:7], a[164:167], a[128:131], a[4:7]\n" \
X_"a[176:177], v36 offset:0\n" \
"v_writelane_b32 v11, s20, 14\n" \
M_"a[4:7], a[228:231], a[192:195], a[4:7]\n" \
X_"a[178:179], v36 offset:1024\n" \
"v_writelane_b32 v11, s16, 15\n" \
M_"a[12:15], a[172:175], a[128:131], a[12:15]\n" \
".fill (L_pb + 24288 - .) / 4, 4, 0xBF800000\n" \
"L_pp6:\n" \
"s_cbranch_execz L_pret\n" \
X_"a[240:241], v36 offset:8192\n" \
"s_waitcnt lgkmcnt(15)\n" \
M_"a[12:15], a[236:239], a[192:195], a[12:15]\n" \
"s_add_u32 s88, s41, 0x2000\n" \
"buffer_store_dwordx4 v[244:247], v20, s[80:83], s88 offen offset:3072 sc1\n" \
"s_waitcnt lgkmcnt(13)\n" \
M_"a[8:11], a[128:131], a[168:171], a[8:11]\n" \
X_"a[242:243], v36 offset:9216\n" \
"s_waitcnt lgkmcnt(12)\n" \
M_"a[8:11], a[192:195], a[232:235], a[8:11]\n" \
X_"a[180:181], v37 offset:0\n" \
M_"a[24:27], a[132:135], a[168:171], a[24:27]\n" \
X_"a[182:183], v37 offset:1024\n" \
M_"a[24:27], a[196:199], a[232:235], a[24:27]\n" \
X_"a[244:245], v37 offset:8192\n" \
M_"a[28:31], a[172:175], a[132:135], a[28:31]\n" \
"s_cmpk_lt_u32 s3, 3\n" \
"s_cselect_b32 s16, s17, s19\n" \
M_"a[28:31], a[236:239], a[196:199], a[28:31]\n" \
"s_addk_i32 s84, 0x2000\n" \
"s_nop 0\n" \
M_"a[20:23], a[164:167], a[132:135], a[20:23]\n" \
B_"v25, s[76:79], s84 offen offset:1024 lds\n" \
M_"a[20:23], a[228:231], a[196:199], a[20:23]\n" \
X_"a[246:247], v37 offset:9216\n" \
"s_and_b32 s16, s16, 0xffff\n" \
M_"a[16:19], a[160:163], a[132:135], a[16:19]\n" \
X_"a[184:185], v38 offset:0\n" \
"v_writelane_b32 v11, s16, 16\n" \
M_"a[16:19], a[224:227], a[196:199], a[16:19]\n" \
X_"a[186:187], v38 offset:1024\n" \
"s_mul_i32 s16, s5, 0x48\n" \
M_"a[32:35], a[160:163], a[136:139], a[32:35]\n" \
"s_addk_i32 s84, 0x2000\n" \
"s_nop 0\n" \
M_"a[32:35], a[224:227], a[200:203], a[32:35]\n" \
B_"v24, s[76:79], s84 offen offset:2048 lds\n" \
M_"a[36:39], a[164:167], a[136:139], a[36:39]\n" \
X_"a[248:249], v38 offset:8192\n" \
"s_add_i32 s17, s16, 0x47\n" \
M_"a[36:39], a[228:231], a[200:203], a[36:39]\n" \
X_"a[250:251], v38 offset:9216\n" \
M_"a[44:47], a[172:175], a[136:139], a[44:47]\n" \
X_"a[188:189], v39 offset:0\n" \
M_"a[44:47], a[236:239], a[200:203], a[44:47]\n" \
X_"a[190:191], v39 offset:1024\n" \
M_"a[40:43], a[136:139], a[168:171], a[40:43]\n" \
"s_add_u32 s89, s41, 0x3000\n" \
"buffer_store_dwordx4 v[184:187], v20, s[80:83], s89 offen offset:3072 sc1\n" \
M_"a[40:43], a[200:203], a[232:235], a[40:43]\n" \
X_"a[252:253], v39 offset:8192\n" \
M_"a[56:59], a[140:143], a[168:171], a[56:59]\n" \
X_"a[254:255], v39 offset:9216\n" \
M_"a[56:59], a[204:207], a[232:235], a[56:59]\n" \
"s_cmpk_lt_u32 s3, 3\n" \
"s_cselect_b32 s17, s17, s18\n" \
M_"a[60:63], a[172:175], a[140:143], a[60:63]\n" \
"s_mulk_i32 s17, 0x480\n" \
"s_addk_i32 s17, 0x120\n" \
M_"a[60:63], a[236:239], a[204:207], a[60:63]\n" \
"s_addk_i32 s84, 0x2000\n" \
M_"a[52:55], a[164:167], a[140:143], a[52:55]\n" \
B_"v25, s[76:79], s84 offen offset:3072 lds\n" \
M_"a[52:55], a[228:231], a[204:207], a[52:55]\n" \
"v_writelane_b32 v11, s17, 17\n" \
"s_lshl_b32 s17, s3, 5\n" \
M_"a[48:51], a[160:163], a[140:143], a[48:51]\n" \
"s_add_i32 s16, s16, s17\n" \
"s_mulk_i32 s16, 0x480\n" \
M_"a[48:51], a[224:227], a[204:207], a[48:51]\n" \
"s_mul_i32 s17, s12, 0x24000\n" \
"s_add_i32 s18, s17, 0x1b000\n" \
"s_waitcnt lgkmcnt(15)\n" \
M_"a[64:67], a[164:167], v[248:251], a[64:67]\n" \
"s_cmpk_lt_u32 s3, 3\n" \
"s_cselect_b32 s16, s16, s18\n" \
M_"a[64:67], a[228:231], v[48:51], a[64:67]\n" \
"s_xor_b32 s18, s3, 2\n" \
"v_writelane_b32 v11, s16, 18\n" \
M_"a[80:83], a[172:175], v[248:251], a[80:83]\n" \
"s_mul_i32 s16, s15, 0x2400\n" \
"s_mul_i32 s15, s15, 0x9000\n" \
M_"a[80:83], a[236:239], v[48:51], a[80:83]\n" \
"s_bitset1_b32 s16, 17\n" \
"v_writelane_b32 v11, s16, 22\n" \
M_"a[68:71], v[248:251], a[168:171], a[68:71]\n" \
"s_add_i32 s17, s17, s15\n" \
"s_mul_i32 s15, s3, 0x2400\n" \
"s_waitcnt vmcnt(11) lgkmcnt(0)\n" \
M_"a[68:71], v[48:51], a[232:235], a[68:71]\n" \
"s_barrier\n" \
"v_xor_b32_e32 v28, 0x20000, v28\n" \
"v_xor_b32_e32 v29, 0x20000, v29\n" \
M_"v[52:55], a[176:179], a[128:131], v[52:55]\n" \
"s_add_u32 m0, s40, 0x0\n" \
"s_nop 0\n" \
"s_add_u32 s85, s39, 0x8000\n" \
M_"v[52:55], a[240:243], a[192:195], v[52:55]\n" \
B_"v20, s[80:83], s85 offen sc1 lds\n" \
M_"v[56:59], a[180:183], a[128:131], v[56:59]\n" \
X_"a[160:161], v32 offset:32768\n" \
"v_writelane_b32 v11, s17, 29\n" \
M_"v[56:59], a[244:247], a[192:195], v[56:59]\n" \
X_"a[162:163], v32 offset:33280\n" \
"s_add_i32 s16, s15, 0xa400\n" \
M_"v[60:63], a[184:187], a[128:131], v[60:63]\n" \
"s_add_u32 m0, s40, 0x1000\n" \
"s_add_u32 s85, s39, 0x9000\n" \
M_"v[60:63], a[248:251], a[192:195], v[60:63]\n" \
B_"v20, s[80:83], s85 offen sc1 lds\n" \
M_"v[64:67], a[188:191], a[128:131], v[64:67]\n" \
X_"a[224:225], v32 offset:36864\n" \
"s_mov_b32 s17, 0x8000\n" \
M_"v[64:67], a[252:255], a[192:195], v[64:67]\n" \
R_"a[128:131], v28 offset:0\n" \
M_"v[80:83], a[188:191], a[132:135], v[80:83]\n" \
R_"a[192:195], v29 offset:0\n" \
M_"v[80:83], a[252:255], a[196:199], v[80:83]\n" \
"s_add_u32 s88, s41, 0x3000\n" \
"buffer_store_dwordx4 v[188:191], v20, s[80:83], s88 offen offset:2048 sc1\n" \
M_"v[76:79], a[184:187], a[132:135], v[76:79]\n" \
X_"a[226:227], v32 offset:37376\n" \
M_"v[76:79], a[248:251], a[196:199], v[76:79]\n" \
X_"a[164:165], v33 offset:32768\n" \
M_"v[72:75], a[180:183], a[132:135], v[72:75]\n" \
"s_add_u32 m0, s40, 0x3000\n" \
"s_movk_i32 s84, 0x200\n" \
M_"v[72:75], a[244:247], a[196:199], v[72:75]\n" \
B_"v21, s[76:79], s84 offen offset:0 lds\n" \
M_"v[68:71], a[176:179], a[132:135], v[68:71]\n" \
X_"a[166:167], v33 offset:33280\n" \
M_"v[68:71], a[240:243], a[196:199], v[68:71]\n" \
R_"a[132:135], v28 offset:2048\n" \
M_"v[84:87], a[176:179], a[136:139], v[84:87]\n" \
R_"a[196:199], v29 offset:2048\n" \
M_"v[84:87], a[240:243], a[200:203], v[84:87]\n" \
"s_add_u32 m0, s31, 0x0\n" \
"s_mov_b32 s84, 0x200000\n" \
M_"v[88:91], a[180:183], a[136:139], v[88:91]\n" \
B_"v26, s[68:71], s84 offen offset:0 lds\n" \
M_"v[88:91], a[244:247], a[200:203], v[88:91]\n" \
X_"a[228:229], v33 offset:36864\n" \
M_"v[92:95], a[184:187], a[136:139], v[92:95]\n" \
X_"a[230:231], v33 offset:37376\n" \
M_"v[92:95], a[248:251], a[200:203], v[92:95]\n" \
"s_addk_i32 s84, 0x7c00\n" \
M_"v[96:99], a[188:191], a[136:139], v[96:99]\n" \
B_"v26, s[68:71], s84 offen offset:1024 lds\n" \
M_"v[96:99], a[252:255], a[200:203], v[96:99]\n" \
R_"a[136:139], v28 offset:4096\n" \
M_"v[112:115], a[188:191], a[140:143], v[112:115]\n" \
R_"a[200:203], v29 offset:4096\n" \
M_"v[112:115], a[252:255], a[204:207], v[112:115]\n" \
"s_addk_i32 s84, 0x7c00\n" \
M_"v[108:111], a[184:187], a[140:143], v[108:111]\n" \
B_"v27, s[68:71], s84 offen offset:2048 lds\n" \
M_"v[108:111], a[248:251], a[204:207], v[108:111]\n" \
X_"a[172:173], v35 offset:32768\n" \
M_"v[104:107], a[180:183], a[140:143], v[104:107]\n" \
X_"a[174:175], v35 offset:33280\n" \
M_"v[104:107], a[244:247], a[204:207], v[104:107]\n" \
X_"a[236:237], v35 offset:36864\n" \
M_"v[100:103], a[176:179], a[140:143], v[100:103]\n" \
"s_cmpk_lt_u32 s3, 3\n" \
"s_cselect_b32 s16, s16, s17\n" \
M_"v[100:103], a[240:243], a[204:207], v[100:103]\n" \
R_"a[140:143], v28 offset:6144\n" \
"s_mov_b32 s17, 0x1b000\n" \
M_"v[116:119], a[176:179], a[144:147], v[116:119]\n" \
R_"a[204:207], v29 offset:6144\n" \
M_"v[116:119], a[240:243], a[208:211], v[116:119]\n" \
"s_addk_i32 s84, 0x7c00\n" \
M_"v[120:123], a[180:183], a[144:147], v[120:123]\n" \
B_"v27, s[68:71], s84 offen offset:3072 lds\n" \
M_"v[120:123], a[244:247], a[208:211], v[120:123]\n" \
"s_add_u32 m0, s30, 0x20000\n" \
"s_movk_i32 s84, 0x280\n" \
M_"v[124:127], a[184:187], a[144:147], v[124:127]\n" \
B_"v24, s[76:79], s84 offen offset:0 lds\n" \
M_"v[124:127], a[248:251], a[208:211], v[124:127]\n" \
X_"a[238:239], v35 offset:37376\n" \
M_"v[128:131], a[188:191], a[144:147], v[128:131]\n" \
"s_add_u32 s89, s42, 0x0\n" \
"buffer_store_dwordx4 v[216:219], v20, s[80:83], s89 offen offset:0 sc1\n" \
M_"v[128:131], a[252:255], a[208:211], v[128:131]\n" \
R_"a[144:147], v28 offset:8192\n" \
M_"v[144:147], a[188:191], a[148:151], v[144:147]\n" \
R_"a[208:211], v29 offset:8192\n" \
M_"v[144:147], a[252:255], a[212:215], v[144:147]\n" \
X_"a[168:169], v34 offset:32768\n" \
M_"v[140:143], a[184:187], a[148:151], v[140:143]\n" \
X_"a[170:171], v34 offset:33280\n" \
M_"v[140:143], a[248:251], a[212:215], v[140:143]\n" \
"s_movk_i32 s84, 0x2280\n" \
M_"v[136:139], a[180:183], a[148:151], v[136:139]\n" \
B_"v25, s[76:79], s84 offen offset:1024 lds\n" \
M_"v[136:139], a[244:247], a[212:215], v[136:139]\n" \
X_"a[232:233], v34 offset:36864\n" \
M_"v[132:135], a[176:179], a[148:151], v[132:135]\n" \
X_"a[234:235], v34 offset:37376\n" \
M_"v[132:135], a[240:243], a[212:215], v[132:135]\n" \
R_"a[148:151], v28 offset:10240\n" \
M_"v[148:151], a[176:179], a[152:155], v[148:151]\n" \
R_"a[212:215], v29 offset:10240\n" \
M_"v[148:151], a[240:243], a[216:219], v[148:151]\n" \
"s_movk_i32 s84, 0x4280\n" \
M_"v[152:155], a[180:183], a[152:155], v[152:155]\n" \
B_"v24, s[76:79], s84 offen offset:2048 lds\n" \
M_"v[152:155], a[244:247], a[216:219], v[152:155]\n" \
"s_movk_i32 s84, 0x6280\n" \
M_"v[156:159], a[184:187], a[152:155], v[156:159]\n" \
B_"v25, s[76:79], s84 offen offset:3072 lds\n" \
M_"v[156:159], a[248:251], a[216:219], v[156:159]\n" \
R_"v[248:251], v22 offset:32768\n" \
M_"v[160:163], a[188:191], a[152:155], v[160:163]\n" \
R_"v[48:51], v23 offset:32768\n" \
M_"v[160:163], a[252:255], a[216:219], v[160:163]\n" \
R_"a[152:155], v28 offset:12288\n" \
M_"v[176:179], a[188:191], a[156:159], v[176:179]\n" \
R_"a[216:219], v29 offset:12288\n" \
M_"v[176:179], a[252:255], a[220:223], v[176:179]\n" \
"s_cmp_lg_u32 s18, 0\n" \
"s_cselect_b32 s17, s16, s17\n" \
M_"v[172:175], a[184:187], a[156:159], v[172:175]\n" \
"v_writelane_b32 v11, s16, 23\n" \
"s_or_b32 s16, s15, 0x10000\n" \
M_"v[172:175], a[248:251], a[220:223], v[172:175]\n" \
"v_writelane_b32 v11, s17, 24\n" \
"s_add_i32 s17, s15, 0x19000\n" \
M_"v[168:171], a[180:183], a[156:159], v[168:171]\n" \
"v_writelane_b32 v11, s16, 25\n" \
"s_mov_b32 s16, 0\n" \
M_"v[168:171], a[244:247], a[220:223], v[168:171]\n" \
"s_cmpk_lt_u32 s3, 3\n" \
"s_cselect_b32 s17, s17, s16\n" \
M_"v[164:167], a[176:179], a[156:159], v[164:167]\n" \
"s_bitset1_b32 s15, 17\n" \
"v_writelane_b32 v11, s17, 26\n" \
M_"v[164:167], a[240:243], a[220:223], v[164:167]\n" \
R_"a[156:159], v28 offset:14336\n" \
R_"a[220:223], v29 offset:14336\n" \
"s_waitcnt lgkmcnt(15)\n" \
M_"a[0:3], a[160:163], a[128:131], a[0:3]\n" \
"s_add_u32 m0, s30, 0x21000\n" \
"s_addk_i32 s84, 0x3000\n" \
M_"a[0:3], a[224:227], a[192:195], a[0:3]\n" \
B_"v24, s[76:79], s84 offen offset:0 lds\n" \
M_"a[4:7], a[164:167], a[128:131], a[4:7]\n" \
X_"a[176:177], v36 offset:32768\n" \
"s_movk_i32 s17, 0x2400\n" \
M_"a[4:7], a[228:231], a[192:195], a[4:7]\n" \
X_"a[178:179], v36 offset:33792\n" \
M_"a[12:15], a[172:175], a[128:131], a[12:15]\n" \
X_"a[240:241], v36 offset:40960\n" \
"s_waitcnt lgkmcnt(15)\n" \
M_"a[12:15], a[236:239], a[192:195], a[12:15]\n" \
"s_add_u32 s88, s42, 0x1000\n" \
"buffer_store_dwordx4 v[220:223], v20, s[80:83], s88 offen offset:0 sc1\n" \
"s_waitcnt lgkmcnt(13)\n" \
M_"a[8:11], a[128:131], a[168:171], a[8:11]\n" \
X_"a[242:243], v36 offset:41984\n" \
"s_waitcnt lgkmcnt(12)\n" \
M_"a[8:11], a[192:195], a[232:235], a[8:11]\n" \
X_"a[180:181], v37 offset:32768\n" \
M_"a[24:27], a[132:135], a[168:171], a[24:27]\n" \
X_"a[182:183], v37 offset:33792\n" \
M_"a[24:27], a[196:199], a[232:235], a[24:27]\n" \
X_"a[244:245], v37 offset:40960\n" \
M_"a[28:31], a[172:175], a[132:135], a[28:31]\n" \
"s_cmpk_lt_u32 s3, 3\n" \
"s_cselect_b32 s15, s15, s17\n" \
M_"a[28:31], a[236:239], a[196:199], a[28:31]\n" \
"s_addk_i32 s84, 0x2000\n" \
M_"a[20:23], a[164:167], a[132:135], a[20:23]\n" \
B_"v25, s[76:79], s84 offen offset:1024 lds\n" \
M_"a[20:23], a[228:231], a[196:199], a[20:23]\n" \
X_"a[246:247], v37 offset:41984\n" \
"v_writelane_b32 v11, s15, 27\n" \
M_"a[16:19], a[160:163], a[132:135], a[16:19]\n" \
X_"a[184:185], v38 offset:32768\n" \
"s_lshl_b32 s15, s3, 10\n" \
M_"a[16:19], a[224:227], a[196:199], a[16:19]\n" \
X_"a[186:187], v38 offset:33792\n" \
"s_addk_i32 s15, 0x4800\n" \
M_"a[32:35], a[160:163], a[136:139], a[32:35]\n" \
"s_addk_i32 s84, 0x2000\n" \
M_"a[32:35], a[224:227], a[200:203], a[32:35]\n" \
B_"v24, s[76:79], s84 offen offset:2048 lds\n" \
M_"a[36:39], a[164:167], a[136:139], a[36:39]\n" \
X_"a[248:249], v38 offset:40960\n" \
"v_writelane_b32 v11, s15, 28\n" \
M_"a[36:39], a[228:231], a[200:203], a[36:39]\n" \
X_"a[250:251], v38 offset:41984\n" \
"s_add_i32 s15, s14, 1\n" \
M_"a[44:47], a[172:175], a[136:139], a[44:47]\n" \
X_"a[188:189], v39 offset:32768\n" \
"s_and_b32 s15, s15, 3\n" \
M_"a[44:47], a[236:239], a[200:203], a[44:47]\n" \
X_"a[190:191], v39 offset:33792\n" \
"s_or_b32 s15, s15, s4\n" \
M_"a[40:43], a[136:139], a[168:171], a[40:43]\n" \
"s_add_u32 s89, s42, 0x0\n" \
"buffer_store_dwordx4 v[192:195], v20, s[80:83], s89 offen offset:2048 sc1\n" \
M_"a[40:43], a[200:203], a[232:235], a[40:43]\n" \
X_"a[252:253], v39 offset:40960\n" \
"s_lshl_b32 s17, s15, 2\n" \
M_"a[56:59], a[140:143], a[168:171], a[56:59]\n" \
X_"a[254:255], v39 offset:41984\n" \
"s_or_b32 s17, s17, s3\n" \
M_"a[56:59], a[204:207], a[232:235], a[56:59]\n" \
"s_mul_i32 s17, s17, 0x9000\n" \
"v_writelane_b32 v11, s17, 30\n" \
M_"a[60:63], a[172:175], a[140:143], a[60:63]\n" \
"s_lshl_b32 s17, s12, 2\n" \
"s_or_b32 s17, s17, s3\n" \
M_"a[60:63], a[236:239], a[204:207], a[60:63]\n" \
"s_addk_i32 s84, 0x2000\n" \
M_"a[52:55], a[164:167], a[140:143], a[52:55]\n" \
B_"v25, s[76:79], s84 offen offset:3072 lds\n" \
M_"a[52:55], a[228:231], a[204:207], a[52:55]\n" \
"s_add_i32 s14, s14, -1\n" \
"s_xor_b32 s17, s17, 8\n" \
M_"a[48:51], a[160:163], a[140:143], a[48:51]\n" \
"s_and_b32 s14, s14, 3\n" \
"s_mul_i32 s17, s17, 0x9000\n" \
M_"a[48:51], a[224:227], a[204:207], a[48:51]\n" \
"s_or_b32 s14, s14, s4\n" \
"v_writelane_b32 v11, s17, 31\n" \
"s_waitcnt lgkmcnt(15)\n" \
M_"a[64:67], a[164:167], v[248:251], a[64:67]\n" \
"s_lshl_b32 s17, s14, 2\n" \
"s_or_b32 s17, s17, s3\n" \
M_"a[64:67], a[228:231], v[48:51], a[64:67]\n" \
"s_mul_i32 s17, s17, 0x9000\n" \
"v_writelane_b32 v11, s17, 32\n" \
M_"a[80:83], a[172:175], v[248:251], a[80:83]\n" \
"s_or_b32 s17, s8, s13\n" \
"s_mul_i32 s17, s17, 0x24800\n" \
M_"a[80:83], a[236:239], v[48:51], a[80:83]\n" \
"s_lshl_b32 s15, s15, 12\n" \
"s_add_i32 s18, s17, 0x3004800\n" \
M_"a[68:71], v[248:251], a[168:171], a[68:71]\n" \
"s_lshl_b32 s12, s12, 12\n" \
"s_add_i32 s19, s18, s15\n" \
"s_waitcnt vmcnt(11) lgkmcnt(0)\n" \
M_"a[68:71], v[48:51], a[232:235], a[68:71]\n" \
"s_barrier\n" \
M_"v[52:55], a[176:179], a[128:131], v[52:55]\n" \
"s_add_u32 m0, s40, 0x8000\n" \
"s_add_u32 s85, s39, 0xa000\n" \
M_"v[52:55], a[240:243], a[192:195], v[52:55]\n" \
B_"v20, s[80:83], s85 offen sc1 lds\n" \
M_"v[56:59], a[180:183], a[128:131], v[56:59]\n" \
X_"a[160:161], v32 offset:0\n" \
"v_accvgpr_read_b32 v224, a120\n" \
M_"v[56:59], a[244:247], a[192:195], v[56:59]\n" \
X_"a[162:163], v32 offset:512\n" \
"v_accvgpr_read_b32 v225, a121\n" \
M_"v[60:63], a[184:187], a[128:131], v[60:63]\n" \
"s_add_u32 m0, s40, 0x9000\n" \
"s_add_u32 s85, s39, 0xb000\n" \
M_"v[60:63], a[248:251], a[192:195], v[60:63]\n" \
B_"v20, s[80:83], s85 offen sc1 lds\n" \
M_"v[64:67], a[188:191], a[128:131], v[64:67]\n" \
X_"a[224:225], v32 offset:4096\n" \
"v_accvgpr_read_b32 v226, a122\n" \
M_"v[64:67], a[252:255], a[192:195], v[64:67]\n" \
R_"a[128:131], v28 offset:32768\n" \
"v_accvgpr_read_b32 v227, a123\n" \
M_"v[80:83], a[188:191], a[132:135], v[80:83]\n" \
R_"a[192:195], v29 offset:32768\n" \
"v_accvgpr_read_b32 v180, a124\n" \
M_"v[80:83], a[252:255], a[196:199], v[80:83]\n" \
"s_add_u32 s88, s42, 0x0\n" \
"buffer_store_dwordx4 v[196:199], v20, s[80:83], s88 offen offset:3072 sc1\n" \
M_"v[76:79], a[184:187], a[132:135], v[76:79]\n" \
X_"a[226:227], v32 offset:4608\n" \
"v_accvgpr_read_b32 v181, a125\n" \
M_"v[76:79], a[248:251], a[196:199], v[76:79]\n" \
X_"a[164:165], v33 offset:0\n" \
"v_accvgpr_read_b32 v182, a126\n" \
M_"v[72:75], a[180:183], a[132:135], v[72:75]\n" \
"s_add_u32 m0, s40, 0xb000\n" \
"s_movk_i32 s84, 0x280\n" \
M_"v[72:75], a[244:247], a[196:199], v[72:75]\n" \
B_"v21, s[76:79], s84 offen offset:0 lds\n" \
M_"v[68:71], a[176:179], a[132:135], v[68:71]\n" \
X_"a[166:167], v33 offset:512\n" \
"v_accvgpr_read_b32 v183, a127\n" \
M_"v[68:71], a[240:243], a[196:199], v[68:71]\n" \
R_"a[132:135], v28 offset:34816\n" \
C_"v224, v224, v225\n" \
M_"v[84:87], a[176:179], a[136:139], v[84:87]\n" \
R_"a[196:199], v29 offset:34816\n" \
C_"v225, v226, v227\n" \
M_"v[84:87], a[240:243], a[200:203], v[84:87]\n" \
"s_add_u32 m0, s31, 0x8000\n" \
"s_mov_b32 s84, 0x280000\n" \
M_"v[88:91], a[180:183], a[136:139], v[88:91]\n" \
B_"v26, s[68:71], s84 offen offset:0 lds\n" \
M_"v[88:91], a[244:247], a[200:203], v[88:91]\n" \
X_"a[228:229], v33 offset:4096\n" \
C_"v226, v180, v181\n" \
M_"v[92:95], a[184:187], a[136:139], v[92:95]\n" \
X_"a[230:231], v33 offset:4608\n" \
C_"v227, v182, v183\n" \
M_"v[92:95], a[248:251], a[200:203], v[92:95]\n" \
"s_addk_i32 s84, 0x7c00\n" \
M_"v[96:99], a[188:191], a[136:139], v[96:99]\n" \
B_"v26, s[68:71], s84 offen offset:1024 lds\n" \
M_"v[96:99], a[252:255], a[200:203], v[96:99]\n" \
R_"a[136:139], v28 offset:36864\n" \
"v_accvgpr_read_b32 v220, a112\n" \
M_"v[112:115], a[188:191], a[140:143], v[112:115]\n" \
R_"a[200:203], v29 offset:36864\n" \
"v_accvgpr_read_b32 v221, a113\n" \
M_"v[112:115], a[252:255], a[204:207], v[112:115]\n" \
"s_addk_i32 s84, 0x7c00\n" \
M_"v[108:111], a[184:187], a[140:143], v[108:111]\n" \
B_"v27, s[68:71], s84 offen offset:2048 lds\n" \
M_"v[108:111], a[248:251], a[204:207], v[108:111]\n" \
X_"a[172:173], v35 offset:0\n" \
"v_accvgpr_read_b32 v222, a114\n" \
M_"v[104:107], a[180:183], a[140:143], v[104:107]\n" \
X_"a[174:175], v35 offset:512\n" \
"v_accvgpr_read_b32 v223, a115\n" \
M_"v[104:107], a[244:247], a[204:207], v[104:107]\n" \
X_"a[236:237], v35 offset:4096\n" \
"v_accvgpr_read_b32 v180, a116\n" \
M_"v[100:103], a[176:179], a[140:143], v[100:103]\n" \
"v_accvgpr_read_b32 v181, a117\n" \
"v_accvgpr_read_b32 v182, a118\n" \
M_"v[100:103], a[240:243], a[204:207], v[100:103]\n" \
R_"a[140:143], v28 offset:38912\n" \
"v_accvgpr_read_b32 v183, a119\n" \
M_"v[116:119], a[176:179], a[144:147], v[116:119]\n" \
R_"a[204:207], v29 offset:38912\n" \
C_"v220, v220, v221\n" \
M_"v[116:119], a[240:243], a[208:211], v[116:119]\n" \
"s_addk_i32 s84, 0x7c00\n" \
M_"v[120:123], a[180:183], a[144:147], v[120:123]\n" \
B_"v27, s[68:71], s84 offen offset:3072 lds\n" \
M_"v[120:123], a[244:247], a[208:211], v[120:123]\n" \
"s_add_u32 m0, s30, 0x0\n" \
"s_movk_i32 s84, 0x300\n" \
"s_nop 0\n" \
M_"v[124:127], a[184:187], a[144:147], v[124:127]\n" \
B_"v24, s[76:79], s84 offen offset:0 lds\n" \
M_"v[124:127], a[248:251], a[208:211], v[124:127]\n" \
X_"a[238:239], v35 offset:4608\n" \
C_"v221, v222, v223\n" \
M_"v[128:131], a[188:191], a[144:147], v[128:131]\n" \
"s_add_u32 s89, s42, 0x1000\n" \
"buffer_store_dwordx4 a[84:87], v20, s[80:83], s89 offen offset:3072 sc1\n" \
M_"v[128:131], a[252:255], a[208:211], v[128:131]\n" \
R_"a[144:147], v28 offset:40960\n" \
C_"v222, v180, v181\n" \
M_"v[144:147], a[188:191], a[148:151], v[144:147]\n" \
R_"a[208:211], v29 offset:40960\n" \
C_"v223, v182, v183\n" \
M_"v[144:147], a[252:255], a[212:215], v[144:147]\n" \
X_"a[168:169], v34 offset:0\n" \
"v_accvgpr_read_b32 v216, a104\n" \
M_"v[140:143], a[184:187], a[148:151], v[140:143]\n" \
X_"a[170:171], v34 offset:512\n" \
"v_accvgpr_read_b32 v217, a105\n" \
M_"v[140:143], a[248:251], a[212:215], v[140:143]\n" \
"s_movk_i32 s84, 0x2300\n" \
"s_nop 0\n" \
M_"v[136:139], a[180:183], a[148:151], v[136:139]\n" \
B_"v25, s[76:79], s84 offen offset:1024 lds\n" \
M_"v[136:139], a[244:247], a[212:215], v[136:139]\n" \
X_"a[232:233], v34 offset:4096\n" \
"v_accvgpr_read_b32 v218, a106\n" \
M_"v[132:135], a[176:179], a[148:151], v[132:135]\n" \
X_"a[234:235], v34 offset:4608\n" \
"v_accvgpr_read_b32 v219, a107\n" \
M_"v[132:135], a[240:243], a[212:215], v[132:135]\n" \
R_"a[148:151], v28 offset:43008\n" \
"v_accvgpr_read_b32 v180, a108\n" \
M_"v[148:151], a[176:179], a[152:155], v[148:151]\n" \
R_"a[212:215], v29 offset:43008\n" \
"v_accvgpr_read_b32 v181, a109\n" \
M_"v[148:151], a[240:243], a[216:219], v[148:151]\n" \
"s_movk_i32 s84, 0x4300\n" \
M_"v[152:155], a[180:183], a[152:155], v[152:155]\n" \
B_"v24, s[76:79], s84 offen offset:2048 lds\n" \
M_"v[152:155], a[244:247], a[216:219], v[152:155]\n" \
"s_movk_i32 s84, 0x6300\n" \
M_"v[156:159], a[184:187], a[152:155], v[156:159]\n" \
B_"v25, s[76:79], s84 offen offset:3072 lds\n" \
M_"v[156:159], a[248:251], a[216:219], v[156:159]\n" \
R_"v[248:251], v22 offset:0\n" \
"v_accvgpr_read_b32 v182, a110\n" \
M_"v[160:163], a[188:191], a[152:155], v[160:163]\n" \
R_"v[48:51], v23 offset:0\n" \
"v_accvgpr_read_b32 v183, a111\n" \
M_"v[160:163], a[252:255], a[216:219], v[160:163]\n" \
R_"a[152:155], v28 offset:45056\n" \
C_"v216, v216, v217\n" \
M_"v[176:179], a[188:191], a[156:159], v[176:179]\n" \
R_"a[216:219], v29 offset:45056\n" \
C_"v217, v218, v219\n" \
M_"v[176:179], a[252:255], a[220:223], v[176:179]\n" \
C_"v218, v180, v181\n" \
C_"v219, v182, v183\n" \
M_"v[172:175], a[184:187], a[156:159], v[172:175]\n" \
"v_accvgpr_read_b32 v192, a96\n" \
"v_accvgpr_read_b32 v193, a97\n" \
M_"v[172:175], a[248:251], a[220:223], v[172:175]\n" \
"v_accvgpr_read_b32 v194, a98\n" \
"v_accvgpr_read_b32 v195, a99\n" \
M_"v[168:171], a[180:183], a[156:159], v[168:171]\n" \
"v_accvgpr_read_b32 v180, a100\n" \
"v_accvgpr_read_b32 v181, a101\n" \
M_"v[168:171], a[244:247], a[220:223], v[168:171]\n" \
"v_accvgpr_read_b32 v182, a102\n" \
"v_accvgpr_read_b32 v183, a103\n" \
M_"v[164:167], a[176:179], a[156:159], v[164:167]\n" \
C_"v192, v192, v193\n" \
C_"v193, v194, v195\n" \
M_"v[164:167], a[240:243], a[220:223], v[164:167]\n" \
R_"a[156:159], v28 offset:47104\n" \
R_"a[220:223], v29 offset:47104\n" \
"s_waitcnt lgkmcnt(15)\n" \
M_"a[0:3], a[160:163], a[128:131], a[0:3]\n" \
"s_add_u32 m0, s30, 0x1000\n" \
"s_addk_i32 s84, 0x3000\n" \
M_"a[0:3], a[224:227], a[192:195], a[0:3]\n" \
B_"v24, s[76:79], s84 offen offset:0 lds\n" \
M_"a[4:7], a[164:167], a[128:131], a[4:7]\n" \
X_"a[176:177], v36 offset:0\n" \
C_"v194, v180, v181\n" \
M_"a[4:7], a[228:231], a[192:195], a[4:7]\n" \
X_"a[178:179], v36 offset:1024\n" \
C_"v195, v182, v183\n" \
M_"a[12:15], a[172:175], a[128:131], a[12:15]\n" \
X_"a[240:241], v36 offset:8192\n" \
"v_accvgpr_read_b32 v188, a88\n" \
"s_waitcnt lgkmcnt(15)\n" \
M_"a[12:15], a[236:239], a[192:195], a[12:15]\n" \
"s_add_u32 s88, s42, 0x1000\n" \
"buffer_store_dwordx4 v[252:255], v20, s[80:83], s88 offen offset:2048 sc1\n" \
"s_waitcnt lgkmcnt(13)\n" \
M_"a[8:11], a[128:131], a[168:171], a[8:11]\n" \
X_"a[242:243], v36 offset:9216\n" \
"v_accvgpr_read_b32 v189, a89\n" \
"s_waitcnt lgkmcnt(12)\n" \
"s_nop 0\n" \
M_"a[8:11], a[192:195], a[232:235], a[8:11]\n" \
X_"a[180:181], v37 offset:0\n" \
"v_accvgpr_read_b32 v190, a90\n" \
M_"a[24:27], a[132:135], a[168:171], a[24:27]\n" \
X_"a[182:183], v37 offset:1024\n" \
"v_accvgpr_read_b32 v191, a91\n" \
M_"a[24:27], a[196:199], a[232:235], a[24:27]\n" \
X_"a[244:245], v37 offset:8192\n" \
"v_accvgpr_read_b32 v180, a92\n" \
M_"a[28:31], a[172:175], a[132:135], a[28:31]\n" \
"v_accvgpr_read_b32 v181, a93\n" \
".fill (L_pb + 28384 - .) / 4, 4, 0xBF800000\n" \
"L_pp7:\n" \
"s_cbranch_execz L_pret\n" \
"v_accvgpr_read_b32 v182, a94\n" \
M_"a[28:31], a[236:239], a[196:199], a[28:31]\n" \
"s_addk_i32 s84, 0x2000\n" \
M_"a[20:23], a[164:167], a[132:135], a[20:23]\n" \
B_"v25, s[76:79], s84 offen offset:1024 lds\n" \
M_"a[20:23], a[228:231], a[196:199], a[20:23]\n" \
X_"a[246:247], v37 offset:9216\n" \
"v_accvgpr_read_b32 v183, a95\n" \
M_"a[16:19], a[160:163], a[132:135], a[16:19]\n" \
X_"a[184:185], v38 offset:0\n" \
C_"v188, v188, v189\n" \
M_"a[16:19], a[224:227], a[196:199], a[16:19]\n" \
X_"a[186:187], v38 offset:1024\n" \
C_"v189, v190, v191\n" \
M_"a[32:35], a[160:163], a[136:139], a[32:35]\n" \
"s_addk_i32 s84, 0x2000\n" \
"s_nop 0\n" \
M_"a[32:35], a[224:227], a[200:203], a[32:35]\n" \
B_"v24, s[76:79], s84 offen offset:2048 lds\n" \
M_"a[36:39], a[164:167], a[136:139], a[36:39]\n" \
X_"a[248:249], v38 offset:8192\n" \
C_"v190, v180, v181\n" \
M_"a[36:39], a[228:231], a[200:203], a[36:39]\n" \
X_"a[250:251], v38 offset:9216\n" \
C_"v191, v182, v183\n" \
M_"a[44:47], a[172:175], a[136:139], a[44:47]\n" \
X_"a[188:189], v39 offset:0\n" \
"v_accvgpr_read_b32 v184, a72\n" \
M_"a[44:47], a[236:239], a[200:203], a[44:47]\n" \
X_"a[190:191], v39 offset:1024\n" \
"v_accvgpr_read_b32 v185, a73\n" \
M_"a[40:43], a[136:139], a[168:171], a[40:43]\n" \
"s_add_u32 s89, s42, 0x2000\n" \
"buffer_store_dwordx4 v[200:203], v20, s[80:83], s89 offen offset:2048 sc1\n" \
M_"a[40:43], a[200:203], a[232:235], a[40:43]\n" \
X_"a[252:253], v39 offset:8192\n" \
"v_accvgpr_read_b32 v186, a74\n" \
M_"a[56:59], a[140:143], a[168:171], a[56:59]\n" \
X_"a[254:255], v39 offset:9216\n" \
"v_accvgpr_read_b32 v187, a75\n" \
M_"a[56:59], a[204:207], a[232:235], a[56:59]\n" \
"v_accvgpr_read_b32 v180, a76\n" \
"v_accvgpr_read_b32 v181, a77\n" \
M_"a[60:63], a[172:175], a[140:143], a[60:63]\n" \
"v_accvgpr_read_b32 v182, a78\n" \
"v_accvgpr_read_b32 v183, a79\n" \
M_"a[60:63], a[236:239], a[204:207], a[60:63]\n" \
"s_addk_i32 s84, 0x2000\n" \
"s_nop 0\n" \
M_"a[52:55], a[164:167], a[140:143], a[52:55]\n" \
B_"v25, s[76:79], s84 offen offset:3072 lds\n" \
M_"a[52:55], a[228:231], a[204:207], a[52:55]\n" \
C_"v184, v184, v185\n" \
C_"v185, v186, v187\n" \
M_"a[48:51], a[160:163], a[140:143], a[48:51]\n" \
C_"v186, v180, v181\n" \
C_"v187, v182, v183\n" \
M_"a[48:51], a[224:227], a[204:207], a[48:51]\n" \
"v_writelane_b32 v11, s19, 33\n" \
"s_xor_b32 s12, s12, 0x2000\n" \
"s_waitcnt lgkmcnt(15)\n" \
M_"a[64:67], a[164:167], v[248:251], a[64:67]\n" \
"s_lshl_b32 s14, s14, 12\n" \
"s_sub_i32 s13, s8, s13\n" \
M_"a[64:67], a[228:231], v[48:51], a[64:67]\n" \
"s_add_i32 s19, s18, s12\n" \
"v_writelane_b32 v11, s19, 34\n" \
M_"a[80:83], a[172:175], v[248:251], a[80:83]\n" \
"s_add_i32 s18, s18, s14\n" \
"s_mul_i32 s13, s13, 0x24800\n" \
M_"a[80:83], a[236:239], v[48:51], a[80:83]\n" \
"v_writelane_b32 v11, s18, 35\n" \
"s_add_i32 s18, s13, 0x4120800\n" \
M_"a[68:71], v[248:251], a[168:171], a[68:71]\n" \
"s_add_i32 s15, s18, s15\n" \
"v_writelane_b32 v11, s15, 36\n" \
"s_waitcnt vmcnt(11) lgkmcnt(0)\n" \
M_"a[68:71], v[48:51], a[232:235], a[68:71]\n" \
"s_barrier\n" \
"v_mov_b32_e32 v45, s62\n" \
"s_mov_b64 exec, 1\n" \
"buffer_store_dword v45, off, s[80:83], s37 offset:0 sc1\n" \
"s_mov_b64 exec, -1\n" \
"v_xor_b32_e32 v28, 0x20000, v28\n" \
"v_xor_b32_e32 v29, 0x20000, v29\n" \
M_"v[52:55], a[176:179], a[128:131], v[52:55]\n" \
"s_add_u32 m0, s40, 0x0\n" \
"s_add_u32 s85, s39, 0xc000\n" \
M_"v[52:55], a[240:243], a[192:195], v[52:55]\n" \
B_"v20, s[80:83], s85 offen sc1 lds\n" \
M_"v[56:59], a[180:183], a[128:131], v[56:59]\n" \
X_"a[160:161], v32 offset:32768\n" \
"s_add_i32 s12, s18, s12\n" \
M_"v[56:59], a[244:247], a[192:195], v[56:59]\n" \
X_"a[162:163], v32 offset:33280\n" \
"v_writelane_b32 v11, s12, 37\n" \
M_"v[60:63], a[184:187], a[128:131], v[60:63]\n" \
"s_add_u32 m0, s40, 0x1000\n" \
"s_add_u32 s85, s39, 0xd000\n" \
M_"v[60:63], a[248:251], a[192:195], v[60:63]\n" \
B_"v20, s[80:83], s85 offen sc1 lds\n" \
M_"v[64:67], a[188:191], a[128:131], v[64:67]\n" \
X_"a[224:225], v32 offset:36864\n" \
"s_add_i32 s18, s18, s14\n" \
M_"v[64:67], a[252:255], a[192:195], v[64:67]\n" \
R_"a[128:131], v28 offset:0\n" \
"s_or_b32 s12, s17, 0x3000000\n" \
M_"v[80:83], a[188:191], a[132:135], v[80:83]\n" \
R_"a[192:195], v29 offset:0\n" \
"s_lshl_b32 s14, s5, 11\n" \
M_"v[80:83], a[252:255], a[196:199], v[80:83]\n" \
"s_add_u32 m0, s40, 0x3000\n" \
"s_movk_i32 s84, 0x300\n" \
M_"v[76:79], a[184:187], a[132:135], v[76:79]\n" \
B_"v21, s[76:79], s84 offen offset:0 lds\n" \
M_"v[76:79], a[248:251], a[196:199], v[76:79]\n" \
X_"a[226:227], v32 offset:37376\n" \
"v_writelane_b32 v11, s18, 38\n" \
M_"v[72:75], a[180:183], a[132:135], v[72:75]\n" \
X_"a[164:165], v33 offset:32768\n" \
"s_add_i32 s15, s12, s14\n" \
M_"v[72:75], a[244:247], a[196:199], v[72:75]\n" \
"v_writelane_b32 v11, s15, 39\n" \
"s_add_i32 s15, s13, 0x411c000\n" \
M_"v[68:71], a[176:179], a[132:135], v[68:71]\n" \
X_"a[166:167], v33 offset:33280\n" \
"s_add_i32 s18, s15, s14\n" \
M_"v[68:71], a[240:243], a[196:199], v[68:71]\n" \
R_"a[132:135], v28 offset:2048\n" \
"v_writelane_b32 v11, s18, 40\n" \
M_"v[84:87], a[176:179], a[136:139], v[84:87]\n" \
R_"a[196:199], v29 offset:2048\n" \
"s_add_i32 s17, s17, 0x3004000\n" \
M_"v[84:87], a[240:243], a[200:203], v[84:87]\n" \
"s_add_u32 m0, s31, 0x0\n" \
"s_nop 0\n" \
"s_mov_b32 s84, 0x300000\n" \
M_"v[88:91], a[180:183], a[136:139], v[88:91]\n" \
B_"v26, s[68:71], s84 offen offset:0 lds\n" \
M_"v[88:91], a[244:247], a[200:203], v[88:91]\n" \
"s_add_u32 s88, s42, 0x2000\n" \
"buffer_store_dwordx4 v[204:207], v20, s[80:83], s88 offen offset:3072 sc1\n" \
M_"v[92:95], a[184:187], a[136:139], v[92:95]\n" \
X_"a[228:229], v33 offset:36864\n" \
"v_writelane_b32 v11, s17, 41\n" \
M_"v[92:95], a[248:251], a[200:203], v[92:95]\n" \
X_"a[230:231], v33 offset:37376\n" \
"s_add_i32 s13, s13, 0x4120000\n" \
M_"v[96:99], a[188:191], a[136:139], v[96:99]\n" \
"v_writelane_b32 v11, s13, 42\n" \
"s_lshl_b32 s13, s9, 2\n" \
M_"v[96:99], a[252:255], a[200:203], v[96:99]\n" \
R_"a[136:139], v28 offset:4096\n" \
"s_or_b32 s7, s13, s7\n" \
M_"v[112:115], a[188:191], a[140:143], v[112:115]\n" \
R_"a[200:203], v29 offset:4096\n" \
"s_mul_i32 s7, s7, 0x90000\n" \
M_"v[112:115], a[252:255], a[204:207], v[112:115]\n" \
"s_addk_i32 s84, 0x7c00\n" \
M_"v[108:111], a[184:187], a[140:143], v[108:111]\n" \
B_"v26, s[68:71], s84 offen offset:1024 lds\n" \
M_"v[108:111], a[248:251], a[204:207], v[108:111]\n" \
X_"a[172:173], v35 offset:32768\n" \
"s_lshl_b32 s13, s3, 14\n" \
M_"v[104:107], a[180:183], a[140:143], v[104:107]\n" \
"s_addk_i32 s84, 0x7c00\n" \
M_"v[104:107], a[244:247], a[204:207], v[104:107]\n" \
B_"v27, s[68:71], s84 offen offset:2048 lds\n" \
M_"v[100:103], a[176:179], a[140:143], v[100:103]\n" \
X_"a[174:175], v35 offset:33280\n" \
"s_lshl_b32 s9, s9, 7\n" \
M_"v[100:103], a[240:243], a[204:207], v[100:103]\n" \
R_"a[140:143], v28 offset:6144\n" \
"s_lshl_b32 s8, s8, 4\n" \
M_"v[116:119], a[176:179], a[144:147], v[116:119]\n" \
R_"a[204:207], v29 offset:6144\n" \
"s_add_i32 s7, s7, s13\n" \
M_"v[116:119], a[240:243], a[208:211], v[116:119]\n" \
"s_addk_i32 s84, 0x7c00\n" \
M_"v[120:123], a[180:183], a[144:147], v[120:123]\n" \
B_"v27, s[68:71], s84 offen offset:3072 lds\n" \
M_"v[120:123], a[244:247], a[208:211], v[120:123]\n" \
X_"a[236:237], v35 offset:36864\n" \
"s_or_b32 s13, s8, s9\n" \
M_"v[124:127], a[184:187], a[144:147], v[124:127]\n" \
X_"a[238:239], v35 offset:37376\n" \
"s_sub_i32 s8, s8, s9\n" \
M_"v[124:127], a[248:251], a[208:211], v[124:127]\n" \
"s_add_u32 m0, s30, 0x8000\n" \
"s_movk_i32 s84, 0x380\n" \
M_"v[128:131], a[188:191], a[144:147], v[128:131]\n" \
B_"v24, s[76:79], s84 offen offset:0 lds\n" \
M_"v[128:131], a[252:255], a[208:211], v[128:131]\n" \
R_"a[144:147], v28 offset:8192\n" \
"s_mulk_i32 s13, 0x480\n" \
M_"v[144:147], a[188:191], a[148:151], v[144:147]\n" \
R_"a[208:211], v29 offset:8192\n" \
"s_mulk_i32 s8, 0x480\n" \
M_"v[144:147], a[252:255], a[212:215], v[144:147]\n" \
X_"a[168:169], v34 offset:32768\n" \
"s_or_b32 s13, s13, s11\n" \
M_"v[140:143], a[184:187], a[148:151], v[140:143]\n" \
"s_movk_i32 s84, 0x2380\n" \
M_"v[140:143], a[248:251], a[212:215], v[140:143]\n" \
B_"v25, s[76:79], s84 offen offset:1024 lds\n" \
M_"v[136:139], a[180:183], a[148:151], v[136:139]\n" \
X_"a[170:171], v34 offset:33280\n" \
"s_or_b32 s8, s8, s11\n" \
M_"v[136:139], a[244:247], a[212:215], v[136:139]\n" \
X_"a[232:233], v34 offset:36864\n" \
"s_and_b32 s11, s2, 1\n" \
M_"v[132:135], a[176:179], a[148:151], v[132:135]\n" \
X_"a[234:235], v34 offset:37376\n" \
M_"v[132:135], a[240:243], a[212:215], v[132:135]\n" \
R_"a[148:151], v28 offset:10240\n" \
M_"v[148:151], a[176:179], a[152:155], v[148:151]\n" \
R_"a[212:215], v29 offset:10240\n" \
M_"v[148:151], a[240:243], a[216:219], v[148:151]\n" \
"s_movk_i32 s84, 0x4380\n" \
M_"v[152:155], a[180:183], a[152:155], v[152:155]\n" \
B_"v24, s[76:79], s84 offen offset:2048 lds\n" \
M_"v[152:155], a[244:247], a[216:219], v[152:155]\n" \
"s_movk_i32 s84, 0x6380\n" \
M_"v[156:159], a[184:187], a[152:155], v[156:159]\n" \
B_"v25, s[76:79], s84 offen offset:3072 lds\n" \
M_"v[156:159], a[248:251], a[216:219], v[156:159]\n" \
R_"v[248:251], v22 offset:32768\n" \
M_"v[160:163], a[188:191], a[152:155], v[160:163]\n" \
R_"v[48:51], v23 offset:32768\n" \
M_"v[160:163], a[252:255], a[216:219], v[160:163]\n" \
R_"a[152:155], v28 offset:12288\n" \
M_"v[176:179], a[188:191], a[156:159], v[176:179]\n" \
R_"a[216:219], v29 offset:12288\n" \
M_"v[176:179], a[252:255], a[220:223], v[176:179]\n" \
"s_cmp_lg_u32 s11, 0\n" \
"s_cselect_b32 s12, s15, s12\n" \
M_"v[172:175], a[184:187], a[156:159], v[172:175]\n" \
"s_bfe_u32 s9, s2, 0x20001\n" \
"s_add_i32 s15, s7, 0x4240000\n" \
M_"v[172:175], a[248:251], a[220:223], v[172:175]\n" \
"s_addk_i32 s12, 0x4000\n" \
"s_lshl_b32 s5, s5, 16\n" \
M_"v[168:171], a[180:183], a[156:159], v[168:171]\n" \
"s_xor_b32 s9, s9, s3\n" \
M_"v[168:171], a[244:247], a[220:223], v[168:171]\n" \
"s_cmp_lg_u32 s9, 0\n" \
"s_cselect_b32 s12, s6, s12\n" \
M_"v[164:167], a[176:179], a[156:159], v[164:167]\n" \
"s_add_i32 s5, s15, s5\n" \
"v_writelane_b32 v11, s12, 43\n" \
M_"v[164:167], a[240:243], a[220:223], v[164:167]\n" \
R_"a[156:159], v28 offset:14336\n" \
R_"a[220:223], v29 offset:14336\n" \
"s_waitcnt lgkmcnt(15)\n" \
M_"a[0:3], a[160:163], a[128:131], a[0:3]\n" \
"s_add_u32 m0, s30, 0x9000\n" \
"s_addk_i32 s84, 0x3000\n" \
M_"a[0:3], a[224:227], a[192:195], a[0:3]\n" \
B_"v24, s[76:79], s84 offen offset:0 lds\n" \
M_"a[4:7], a[164:167], a[128:131], a[4:7]\n" \
X_"a[176:177], v36 offset:32768\n" \
"s_add_i32 s7, s7, 0x42c0000\n" \
M_"a[4:7], a[228:231], a[192:195], a[4:7]\n" \
X_"a[178:179], v36 offset:33792\n" \
"v_writelane_b32 v11, s5, 44\n" \
M_"a[12:15], a[172:175], a[128:131], a[12:15]\n" \
X_"a[240:241], v36 offset:40960\n" \
"s_lshl_b32 s5, s11, 10\n" \
"s_waitcnt lgkmcnt(15)\n" \
M_"a[12:15], a[236:239], a[192:195], a[12:15]\n" \
"s_add_u32 s89, s42, 0x3000\n" \
"buffer_store_dwordx4 v[208:211], v20, s[80:83], s89 offen offset:3072 sc1\n" \
"s_waitcnt lgkmcnt(13)\n" \
M_"a[8:11], a[128:131], a[168:171], a[8:11]\n" \
X_"a[242:243], v36 offset:41984\n" \
"s_or_b32 s5, s7, s5\n" \
"s_waitcnt lgkmcnt(12)\n" \
M_"a[8:11], a[192:195], a[232:235], a[8:11]\n" \
X_"a[180:181], v37 offset:32768\n" \
M_"a[24:27], a[132:135], a[168:171], a[24:27]\n" \
X_"a[182:183], v37 offset:33792\n" \
M_"a[24:27], a[196:199], a[232:235], a[24:27]\n" \
X_"a[244:245], v37 offset:40960\n" \
M_"a[28:31], a[172:175], a[132:135], a[28:31]\n" \
"s_cmp_lg_u32 s9, 0\n" \
"s_cselect_b32 s5, s6, s5\n" \
M_"a[28:31], a[236:239], a[196:199], a[28:31]\n" \
"s_addk_i32 s84, 0x2000\n" \
M_"a[20:23], a[164:167], a[132:135], a[20:23]\n" \
B_"v25, s[76:79], s84 offen offset:1024 lds\n" \
M_"a[20:23], a[228:231], a[196:199], a[20:23]\n" \
X_"a[246:247], v37 offset:41984\n" \
"s_add_i32 s8, s8, 0x21c000\n" \
M_"a[16:19], a[160:163], a[132:135], a[16:19]\n" \
X_"a[184:185], v38 offset:32768\n" \
"v_writelane_b32 v11, s5, 45\n" \
M_"a[16:19], a[224:227], a[196:199], a[16:19]\n" \
X_"a[186:187], v38 offset:33792\n" \
"s_add_i32 s5, s13, s10\n" \
M_"a[32:35], a[160:163], a[136:139], a[32:35]\n" \
"s_addk_i32 s84, 0x2000\n" \
M_"a[32:35], a[224:227], a[200:203], a[32:35]\n" \
B_"v24, s[76:79], s84 offen offset:2048 lds\n" \
M_"a[36:39], a[164:167], a[136:139], a[36:39]\n" \
X_"a[248:249], v38 offset:40960\n" \
"v_writelane_b32 v11, s5, 46\n" \
M_"a[36:39], a[228:231], a[200:203], a[36:39]\n" \
X_"a[250:251], v38 offset:41984\n" \
"s_add_i32 s5, s8, s10\n" \
"s_nop 0\n" \
M_"a[44:47], a[172:175], a[136:139], a[44:47]\n" \
X_"a[188:189], v39 offset:32768\n" \
"v_writelane_b32 v11, s5, 47\n" \
M_"a[44:47], a[236:239], a[200:203], a[44:47]\n" \
X_"a[190:191], v39 offset:33792\n" \
M_"a[40:43], a[136:139], a[168:171], a[40:43]\n" \
"s_add_u32 s88, s42, 0x3000\n" \
"buffer_store_dwordx4 v[212:215], v20, s[80:83], s88 offen offset:2048 sc1\n" \
M_"a[40:43], a[200:203], a[232:235], a[40:43]\n" \
X_"a[252:253], v39 offset:40960\n" \
M_"a[56:59], a[140:143], a[168:171], a[56:59]\n" \
X_"a[254:255], v39 offset:41984\n" \
M_"a[56:59], a[204:207], a[232:235], a[56:59]\n" \
"s_cmp_lg_u32 s11, 0\n" \
"s_cselect_b32 s5, s8, s13\n" \
M_"a[60:63], a[172:175], a[140:143], a[60:63]\n" \
"s_addk_i32 s5, 0x100\n" \
M_"a[60:63], a[236:239], a[204:207], a[60:63]\n" \
"s_addk_i32 s84, 0x2000\n" \
M_"a[52:55], a[164:167], a[140:143], a[52:55]\n" \
B_"v25, s[76:79], s84 offen offset:3072 lds\n" \
M_"a[52:55], a[228:231], a[204:207], a[52:55]\n" \
"s_cmp_lg_u32 s9, 0\n" \
"s_cselect_b32 s5, s6, s5\n" \
M_"a[48:51], a[160:163], a[140:143], a[48:51]\n" \
"v_writelane_b32 v11, s5, 48\n" \
"s_or_b32 s5, s7, s14\n" \
M_"a[48:51], a[224:227], a[204:207], a[48:51]\n" \
"v_writelane_b32 v11, s5, 56\n" \
"s_lshl_b32 s5, s3, 17\n" \
"s_waitcnt lgkmcnt(15)\n" \
M_"a[64:67], a[164:167], v[248:251], a[64:67]\n" \
"s_add_i32 s5, s15, s5\n" \
"s_or_b32 s4, s5, s4\n" \
M_"a[64:67], a[228:231], v[48:51], a[64:67]\n" \
"s_or_b32 s4, s4, 0x3840\n" \
"v_writelane_b32 v11, s4, 6\n" \
M_"a[80:83], a[172:175], v[248:251], a[80:83]\n" \
"s_mov_b32 s4, 0xff0000\n" \
M_"a[80:83], a[236:239], v[48:51], a[80:83]\n" \
"s_cmp_lg_u32 s9, 0\n" \
"s_cselect_b32 s4, s16, s4\n" \
M_"a[68:71], v[248:251], a[168:171], a[68:71]\n" \
"v_writelane_b32 v11, s4, 7\n" \
"s_lshl_b32 s4, s2, 16\n" \
"s_waitcnt vmcnt(10) lgkmcnt(0)\n" \
M_"a[68:71], v[48:51], a[232:235], a[68:71]\n" \
"s_barrier\n" \
"v_xor_b32_e32 v28, 0x20000, v28\n" \
"v_xor_b32_e32 v29, 0x20000, v29\n" \
M_"v[52:55], a[176:179], a[128:131], v[52:55]\n" \
"s_add_u32 m0, s40, 0x8000\n" \
"s_add_u32 s85, s39, 0xe000\n" \
M_"v[52:55], a[240:243], a[192:195], v[52:55]\n" \
B_"v20, s[80:83], s85 offen sc1 lds\n" \
M_"v[56:59], a[180:183], a[128:131], v[56:59]\n" \
X_"a[160:161], v32 offset:0\n" \
"s_add_i32 s5, s4, 0x10000\n" \
M_"v[56:59], a[244:247], a[192:195], v[56:59]\n" \
X_"a[162:163], v32 offset:512\n" \
"s_and_b32 s5, s5, 0x70000\n" \
M_"v[60:63], a[184:187], a[128:131], v[60:63]\n" \
"s_add_u32 m0, s40, 0x9000\n" \
"s_add_u32 s85, s39, 0xf000\n" \
M_"v[60:63], a[248:251], a[192:195], v[60:63]\n" \
B_"v20, s[80:83], s85 offen sc1 lds\n" \
M_"v[64:67], a[188:191], a[128:131], v[64:67]\n" \
X_"a[224:225], v32 offset:4096\n" \
"s_add_i32 s5, s15, s5\n" \
M_"v[64:67], a[252:255], a[192:195], v[64:67]\n" \
R_"a[128:131], v28 offset:0\n" \
"v_writelane_b32 v11, s5, 49\n" \
M_"v[80:83], a[188:191], a[132:135], v[80:83]\n" \
R_"a[192:195], v29 offset:0\n" \
"s_add_i32 s5, s4, 0x20000\n" \
M_"v[80:83], a[252:255], a[196:199], v[80:83]\n" \
"s_add_u32 m0, s40, 0xb000\n" \
"s_movk_i32 s84, 0x380\n" \
M_"v[76:79], a[184:187], a[132:135], v[76:79]\n" \
B_"v21, s[76:79], s84 offen offset:0 lds\n" \
M_"v[76:79], a[248:251], a[196:199], v[76:79]\n" \
X_"a[226:227], v32 offset:4608\n" \
"s_and_b32 s5, s5, 0x70000\n" \
M_"v[72:75], a[180:183], a[132:135], v[72:75]\n" \
X_"a[164:165], v33 offset:0\n" \
"s_add_i32 s5, s15, s5\n" \
M_"v[72:75], a[244:247], a[196:199], v[72:75]\n" \
"s_bitset1_b32 s5, 11\n" \
"v_writelane_b32 v11, s5, 50\n" \
M_"v[68:71], a[176:179], a[132:135], v[68:71]\n" \
X_"a[166:167], v33 offset:512\n" \
"s_add_i32 s5, s4, 0x30000\n" \
M_"v[68:71], a[240:243], a[196:199], v[68:71]\n" \
R_"a[132:135], v28 offset:2048\n" \
"s_and_b32 s5, s5, 0x70000\n" \
M_"v[84:87], a[176:179], a[136:139], v[84:87]\n" \
R_"a[196:199], v29 offset:2048\n" \
"s_add_i32 s5, s15, s5\n" \
M_"v[84:87], a[240:243], a[200:203], v[84:87]\n" \
"s_add_u32 m0, s31, 0x8000\n" \
"s_mov_b32 s84, 0x380000\n" \
M_"v[88:91], a[180:183], a[136:139], v[88:91]\n" \
B_"v26, s[68:71], s84 offen offset:0 lds\n" \
M_"v[88:91], a[244:247], a[200:203], v[88:91]\n" \
"s_add_u32 s89, s42, 0x0\n" \
"buffer_store_dwordx4 v[184:187], v20, s[80:83], s89 offen offset:1024 sc1\n" \
M_"v[92:95], a[184:187], a[136:139], v[92:95]\n" \
X_"a[228:229], v33 offset:4096\n" \
"s_bitset1_b32 s5, 12\n" \
M_"v[92:95], a[248:251], a[200:203], v[92:95]\n" \
X_"a[230:231], v33 offset:4608\n" \
"v_writelane_b32 v11, s5, 51\n" \
M_"v[96:99], a[188:191], a[136:139], v[96:99]\n" \
"s_and_b32 s5, s4, 0x70000\n" \
"s_xor_b32 s5, s5, 0x40000\n" \
M_"v[96:99], a[252:255], a[200:203], v[96:99]\n" \
R_"a[136:139], v28 offset:4096\n" \
"s_add_i32 s5, s15, s5\n" \
M_"v[112:115], a[188:191], a[140:143], v[112:115]\n" \
R_"a[200:203], v29 offset:4096\n" \
"s_or_b32 s5, s5, 0x1800\n" \
M_"v[112:115], a[252:255], a[204:207], v[112:115]\n" \
"s_addk_i32 s84, 0x7c00\n" \
M_"v[108:111], a[184:187], a[140:143], v[108:111]\n" \
B_"v26, s[68:71], s84 offen offset:1024 lds\n" \
M_"v[108:111], a[248:251], a[204:207], v[108:111]\n" \
X_"a[172:173], v35 offset:0\n" \
"v_writelane_b32 v11, s5, 52\n" \
M_"v[104:107], a[180:183], a[140:143], v[104:107]\n" \
"s_addk_i32 s84, 0x7c00\n" \
M_"v[104:107], a[244:247], a[204:207], v[104:107]\n" \
B_"v27, s[68:71], s84 offen offset:2048 lds\n" \
M_"v[100:103], a[176:179], a[140:143], v[100:103]\n" \
X_"a[174:175], v35 offset:512\n" \
"s_add_i32 s5, s4, 0x50000\n" \
M_"v[100:103], a[240:243], a[204:207], v[100:103]\n" \
R_"a[140:143], v28 offset:6144\n" \
"s_and_b32 s5, s5, 0x70000\n" \
M_"v[116:119], a[176:179], a[144:147], v[116:119]\n" \
R_"a[204:207], v29 offset:6144\n" \
"s_add_i32 s5, s15, s5\n" \
M_"v[116:119], a[240:243], a[208:211], v[116:119]\n" \
"s_addk_i32 s84, 0x7c00\n" \
M_"v[120:123], a[180:183], a[144:147], v[120:123]\n" \
B_"v27, s[68:71], s84 offen offset:3072 lds\n" \
M_"v[120:123], a[244:247], a[208:211], v[120:123]\n" \
X_"a[236:237], v35 offset:4096\n" \
"s_bitset1_b32 s5, 13\n" \
M_"v[124:127], a[184:187], a[144:147], v[124:127]\n" \
X_"a[238:239], v35 offset:4608\n" \
"v_writelane_b32 v11, s5, 53\n" \
M_"v[124:127], a[248:251], a[208:211], v[124:127]\n" \
"s_add_u32 m0, s30, 0x20000\n" \
"s_movk_i32 s84, 0x400\n" \
M_"v[128:131], a[188:191], a[144:147], v[128:131]\n" \
B_"v24, s[76:79], s84 offen offset:0 lds\n" \
M_"v[128:131], a[252:255], a[208:211], v[128:131]\n" \
R_"a[144:147], v28 offset:8192\n" \
"s_add_i32 s5, s4, 0x60000\n" \
M_"v[144:147], a[188:191], a[148:151], v[144:147]\n" \
R_"a[208:211], v29 offset:8192\n" \
"s_and_b32 s5, s5, 0x70000\n" \
M_"v[144:147], a[252:255], a[212:215], v[144:147]\n" \
X_"a[168:169], v34 offset:0\n" \
"s_add_i32 s4, s4, 0x70000\n" \
M_"v[140:143], a[184:187], a[148:151], v[140:143]\n" \
"s_movk_i32 s84, 0x2400\n" \
M_"v[140:143], a[248:251], a[212:215], v[140:143]\n" \
B_"v25, s[76:79], s84 offen offset:1024 lds\n" \
M_"v[136:139], a[180:183], a[148:151], v[136:139]\n" \
X_"a[170:171], v34 offset:512\n" \
"s_add_i32 s5, s15, s5\n" \
M_"v[136:139], a[244:247], a[212:215], v[136:139]\n" \
X_"a[232:233], v34 offset:4096\n" \
"s_and_b32 s4, s4, 0x70000\n" \
M_"v[132:135], a[176:179], a[148:151], v[132:135]\n" \
X_"a[234:235], v34 offset:4608\n" \
"s_or_b32 s5, s5, 0x2800\n" \
M_"v[132:135], a[240:243], a[212:215], v[132:135]\n" \
R_"a[148:151], v28 offset:10240\n" \
"v_writelane_b32 v11, s5, 54\n" \
M_"v[148:151], a[176:179], a[152:155], v[148:151]\n" \
R_"a[212:215], v29 offset:10240\n" \
"s_add_i32 s15, s15, s4\n" \
M_"v[148:151], a[240:243], a[216:219], v[148:151]\n" \
"s_movk_i32 s84, 0x4400\n" \
M_"v[152:155], a[180:183], a[152:155], v[152:155]\n" \
B_"v24, s[76:79], s84 offen offset:2048 lds\n" \
M_"v[152:155], a[244:247], a[216:219], v[152:155]\n" \
"s_movk_i32 s84, 0x6400\n" \
M_"v[156:159], a[184:187], a[152:155], v[156:159]\n" \
B_"v25, s[76:79], s84 offen offset:3072 lds\n" \
M_"v[156:159], a[248:251], a[216:219], v[156:159]\n" \
R_"v[248:251], v22 offset:0\n" \
"s_or_b32 s4, s15, 0x3000\n" \
M_"v[160:163], a[188:191], a[152:155], v[160:163]\n" \
R_"v[48:51], v23 offset:0\n" \
"v_writelane_b32 v11, s4, 55\n" \
M_"v[160:163], a[252:255], a[216:219], v[160:163]\n" \
R_"a[152:155], v28 offset:12288\n" \
"s_movk_i32 s11, 0x4000\n" \
M_"v[176:179], a[188:191], a[156:159], v[176:179]\n" \
R_"a[216:219], v29 offset:12288\n" \
"s_lshl_b32 s4, s2, 2\n" \
M_"v[176:179], a[252:255], a[220:223], v[176:179]\n" \
"s_and_b32 s4, s4, 28\n" \
"s_bfe_u32 s5, s2, 0x20003\n" \
M_"v[172:175], a[184:187], a[156:159], v[172:175]\n" \
"s_or_b32 s4, s4, s5\n" \
"s_lshr_b32 s5, s2, 5\n" \
M_"v[172:175], a[248:251], a[220:223], v[172:175]\n" \
"s_lshr_b32 s19, s3, 1\n" \
"s_lshl_b32 s18, s5, 1\n" \
M_"v[168:171], a[180:183], a[156:159], v[168:171]\n" \
"s_lshl_b32 s6, s4, 20\n" \
"s_lshl_b32 s7, s4, 8\n" \
M_"v[168:171], a[244:247], a[220:223], v[168:171]\n" \
"s_and_b32 s13, s3, 1\n" \
"s_add_i32 s14, s18, s19\n" \
M_"v[164:167], a[176:179], a[156:159], v[164:167]\n" \
"s_add_u32 s6, s26, s6\n" \
"s_addc_u32 s9, s27, 0\n" \
M_"v[164:167], a[240:243], a[220:223], v[164:167]\n" \
R_"a[156:159], v28 offset:14336\n" \
R_"a[220:223], v29 offset:14336\n" \
"s_waitcnt lgkmcnt(15)\n" \
M_"a[0:3], a[160:163], a[128:131], a[0:3]\n" \
"s_add_u32 m0, s30, 0x21000\n" \
"s_addk_i32 s84, 0x3000\n" \
M_"a[0:3], a[224:227], a[192:195], a[0:3]\n" \
B_"v24, s[76:79], s84 offen offset:0 lds\n" \
M_"a[4:7], a[164:167], a[128:131], a[4:7]\n" \
X_"a[176:177], v36 offset:0\n" \
M_"a[4:7], a[228:231], a[192:195], a[4:7]\n" \
X_"a[178:179], v36 offset:1024\n" \
M_"a[12:15], a[172:175], a[128:131], a[12:15]\n" \
X_"a[240:241], v36 offset:8192\n" \
"s_waitcnt lgkmcnt(15)\n" \
M_"a[12:15], a[236:239], a[192:195], a[12:15]\n" \
"s_add_u32 s88, s42, 0x1000\n" \
"buffer_store_dwordx4 v[188:191], v20, s[80:83], s88 offen offset:1024 sc1\n" \
"s_waitcnt lgkmcnt(13)\n" \
M_"a[8:11], a[128:131], a[168:171], a[8:11]\n" \
X_"a[242:243], v36 offset:9216\n" \
"s_waitcnt lgkmcnt(12)\n" \
M_"a[8:11], a[192:195], a[232:235], a[8:11]\n" \
X_"a[180:181], v37 offset:0\n" \
M_"a[24:27], a[132:135], a[168:171], a[24:27]\n" \
X_"a[182:183], v37 offset:1024\n" \
M_"a[24:27], a[196:199], a[232:235], a[24:27]\n" \
X_"a[244:245], v37 offset:8192\n" \
M_"a[28:31], a[172:175], a[132:135], a[28:31]\n" \
"s_add_u32 s8, s6, 0x1000000\n" \
"s_addc_u32 s6, s9, 0\n" \
M_"a[28:31], a[236:239], a[196:199], a[28:31]\n" \
"s_addk_i32 s84, 0x2000\n" \
M_"a[20:23], a[164:167], a[132:135], a[20:23]\n" \
B_"v25, s[76:79], s84 offen offset:1024 lds\n" \
M_"a[20:23], a[228:231], a[196:199], a[20:23]\n" \
X_"a[246:247], v37 offset:9216\n" \
"s_and_b32 s9, s6, 0xffff\n" \
M_"a[16:19], a[160:163], a[132:135], a[16:19]\n" \
".fill (L_pb + 32480 - .) / 4, 4, 0xBF800000\n" \
"L_pp8:\n" \
"s_cbranch_execz L_pret\n" \
X_"a[184:185], v38 offset:0\n" \
"s_lshl_b32 s6, s5, 16\n" \
M_"a[16:19], a[224:227], a[196:199], a[16:19]\n" \
X_"a[186:187], v38 offset:1024\n" \
"s_sub_i32 s10, 0x100000, s6\n" \
M_"a[32:35], a[160:163], a[136:139], a[32:35]\n" \
"s_addk_i32 s84, 0x2000\n" \
"s_nop 0\n" \
M_"a[32:35], a[224:227], a[200:203], a[32:35]\n" \
B_"v24, s[76:79], s84 offen offset:2048 lds\n" \
M_"a[36:39], a[164:167], a[136:139], a[36:39]\n" \
X_"a[248:249], v38 offset:8192\n" \
"s_mov_b32 s6, 0x8000\n" \
M_"a[36:39], a[228:231], a[200:203], a[36:39]\n" \
X_"a[250:251], v38 offset:9216\n" \
M_"a[44:47], a[172:175], a[136:139], a[44:47]\n" \
X_"a[188:189], v39 offset:0\n" \
M_"a[44:47], a[236:239], a[200:203], a[44:47]\n" \
X_"a[190:191], v39 offset:1024\n" \
M_"a[40:43], a[136:139], a[168:171], a[40:43]\n" \
"s_add_u32 s89, s42, 0x2000\n" \
"buffer_store_dwordx4 v[192:195], v20, s[80:83], s89 offen offset:0 sc1\n" \
M_"a[40:43], a[200:203], a[232:235], a[40:43]\n" \
X_"a[252:253], v39 offset:8192\n" \
M_"a[56:59], a[140:143], a[168:171], a[56:59]\n" \
X_"a[254:255], v39 offset:9216\n" \
M_"a[56:59], a[204:207], a[232:235], a[56:59]\n" \
"s_cmp_lg_u32 s5, 0\n" \
"s_cselect_b32 s17, s11, s6\n" \
M_"a[60:63], a[172:175], a[140:143], a[60:63]\n" \
"s_lshl_b32 s6, s3, 1\n" \
"s_lshl_b32 s5, s5, 3\n" \
M_"a[60:63], a[236:239], a[204:207], a[60:63]\n" \
"s_addk_i32 s84, 0x2000\n" \
M_"a[52:55], a[164:167], a[140:143], a[52:55]\n" \
B_"v25, s[76:79], s84 offen offset:3072 lds\n" \
M_"a[52:55], a[228:231], a[204:207], a[52:55]\n" \
"s_and_b32 s6, s6, 4\n" \
"s_or_b32 s12, s5, s6\n" \
M_"a[48:51], a[160:163], a[140:143], a[48:51]\n" \
"s_sub_i32 s5, s13, s5\n" \
"s_or_b32 s5, s5, s6\n" \
M_"a[48:51], a[224:227], a[204:207], a[48:51]\n" \
"s_or_b32 s6, s12, s13\n" \
"s_lshl_b32 s4, s4, 2\n" \
"s_waitcnt lgkmcnt(15)\n" \
M_"a[64:67], a[164:167], v[248:251], a[64:67]\n" \
"s_mulk_i32 s6, 0x92\n" \
"s_add_i32 s4, s4, 18\n" \
M_"a[64:67], a[228:231], v[48:51], a[64:67]\n" \
"s_mulk_i32 s5, 0x92\n" \
"s_add_i32 s6, s4, s6\n" \
M_"a[80:83], a[172:175], v[248:251], a[80:83]\n" \
"s_add_i32 s5, s5, s4\n" \
"s_lshl_b32 s4, s3, 11\n" \
M_"a[80:83], a[236:239], v[48:51], a[80:83]\n" \
"s_lshl_b32 s15, s13, 12\n" \
"s_lshl_b32 s16, s19, 14\n" \
M_"a[68:71], v[248:251], a[168:171], a[68:71]\n" \
"s_mul_i32 s19, s19, 0xe000\n" \
"s_mov_b32 s11, 0x20000\n" \
"s_waitcnt vmcnt(10) lgkmcnt(0)\n" \
M_"a[68:71], v[48:51], a[232:235], a[68:71]\n" \
"s_barrier\n" \
M_"v[52:55], a[176:179], a[128:131], v[52:55]\n" \
"s_add_u32 m0, s40, 0x0\n" \
"s_add_u32 s85, s39, 0x10000\n" \
M_"v[52:55], a[240:243], a[192:195], v[52:55]\n" \
B_"v20, s[80:83], s85 offen sc1 lds\n" \
M_"v[56:59], a[180:183], a[128:131], v[56:59]\n" \
X_"a[160:161], v32 offset:32768\n" \
"s_or_b32 s12, s4, 0xc000\n" \
M_"v[56:59], a[244:247], a[192:195], v[56:59]\n" \
X_"a[162:163], v32 offset:33280\n" \
"s_lshl_b32 s4, s3, 8\n" \
M_"v[60:63], a[184:187], a[128:131], v[60:63]\n" \
"s_or_b32 s22, s15, s16\n" \
"s_or_b32 s23, s19, s15\n" \
M_"v[60:63], a[248:251], a[192:195], v[60:63]\n" \
X_"a[224:225], v32 offset:36864\n" \
"s_lshl_b32 s60, s6, 10\n" \
M_"v[64:67], a[188:191], a[128:131], v[64:67]\n" \
"s_lshl_b32 s61, s5, 10\n" \
"s_lshl_b32 s14, s14, 15\n" \
"s_nop 0\n" \
M_"v[64:67], a[252:255], a[192:195], v[64:67]\n" \
R_"a[128:131], v28 offset:32768\n" \
"s_or_b32 s20, s4, 0x26c00\n" \
M_"v[80:83], a[188:191], a[132:135], v[80:83]\n" \
R_"a[192:195], v29 offset:32768\n" \
"s_or_b32 s13, s22, 0x8000\n" \
M_"v[80:83], a[252:255], a[196:199], v[80:83]\n" \
"s_add_u32 m0, s40, 0x1000\n" \
"s_add_u32 s85, s39, 0x11000\n" \
M_"v[76:79], a[184:187], a[132:135], v[76:79]\n" \
B_"v20, s[80:83], s85 offen sc1 lds\n" \
M_"v[76:79], a[248:251], a[196:199], v[76:79]\n" \
X_"a[226:227], v32 offset:37376\n" \
"s_add_i32 s22, s22, 0x14000\n" \
M_"v[72:75], a[180:183], a[132:135], v[72:75]\n" \
X_"a[164:165], v33 offset:32768\n" \
"s_bitset1_b32 s23, 16\n" \
M_"v[72:75], a[244:247], a[196:199], v[72:75]\n" \
"s_or_b32 s24, s7, 0x5be8020\n" \
"s_and_b32 s57, s27, 0xffff\n" \
M_"v[68:71], a[176:179], a[132:135], v[68:71]\n" \
X_"a[166:167], v33 offset:33280\n" \
"s_mov_b32 s58, 0x6000000\n" \
M_"v[68:71], a[240:243], a[196:199], v[68:71]\n" \
R_"a[132:135], v28 offset:34816\n" \
"s_mov_b32 s56, s26\n" \
M_"v[84:87], a[176:179], a[136:139], v[84:87]\n" \
R_"a[196:199], v29 offset:34816\n" \
"s_mov_b32 s59, s11\n" \
M_"v[84:87], a[240:243], a[200:203], v[84:87]\n" \
"s_add_u32 m0, s40, 0x3000\n" \
"s_movk_i32 s84, 0x400\n" \
M_"v[88:91], a[180:183], a[136:139], v[88:91]\n" \
B_"v21, s[76:79], s84 offen offset:0 lds\n" \
M_"v[88:91], a[244:247], a[200:203], v[88:91]\n" \
X_"a[228:229], v33 offset:36864\n" \
"s_add_i32 s60, s60, 0x3000000\n" \
M_"v[92:95], a[184:187], a[136:139], v[92:95]\n" \
X_"a[230:231], v33 offset:37376\n" \
"s_add_i32 s61, s61, 0x411c000\n" \
M_"v[92:95], a[248:251], a[200:203], v[92:95]\n" \
"s_add_u32 s88, s42, 0x2000\n" \
"buffer_store_dwordx4 v[216:219], v20, s[80:83], s88 offen offset:1024 sc1\n" \
M_"v[96:99], a[188:191], a[136:139], v[96:99]\n" \
"s_sub_i32 s19, 14, s18\n" \
"s_nop 0\n" \
"s_mov_b32 s21, 0x71800000\n" \
M_"v[96:99], a[252:255], a[200:203], v[96:99]\n" \
R_"a[136:139], v28 offset:36864\n" \
"s_mov_b32 s4, L_gsw-L_gtr\n" \
M_"v[112:115], a[188:191], a[140:143], v[112:115]\n" \
R_"a[200:203], v29 offset:36864\n" \
M_"v[112:115], a[252:255], a[204:207], v[112:115]\n" \
X_"a[172:173], v35 offset:32768\n" \
M_"v[108:111], a[184:187], a[140:143], v[108:111]\n" \
X_"a[174:175], v35 offset:33280\n" \
M_"v[108:111], a[248:251], a[204:207], v[108:111]\n" \
"s_cmp_eq_u32 s19, 0\n" \
"s_cselect_b32 s4, L_gs7-L_gtr, s4\n" \
M_"v[104:107], a[180:183], a[140:143], v[104:107]\n" \
"s_add_u32 m0, s31, 0x0\n" \
"s_mov_b32 s84, 0x400000\n" \
M_"v[104:107], a[244:247], a[204:207], v[104:107]\n" \
B_"v26, s[68:71], s84 offen offset:0 lds\n" \
M_"v[100:103], a[176:179], a[140:143], v[100:103]\n" \
X_"a[236:237], v35 offset:36864\n" \
M_"v[100:103], a[240:243], a[204:207], v[100:103]\n" \
R_"a[140:143], v28 offset:38912\n" \
M_"v[116:119], a[176:179], a[144:147], v[116:119]\n" \
R_"a[204:207], v29 offset:38912\n" \
M_"v[116:119], a[240:243], a[208:211], v[116:119]\n" \
X_"a[238:239], v35 offset:37376\n" \
M_"v[120:123], a[180:183], a[144:147], v[120:123]\n" \
X_"a[168:169], v34 offset:32768\n" \
M_"v[120:123], a[244:247], a[208:211], v[120:123]\n" \
"s_addk_i32 s84, 0x7c00\n" \
M_"v[124:127], a[184:187], a[144:147], v[124:127]\n" \
B_"v26, s[68:71], s84 offen offset:1024 lds\n" \
M_"v[124:127], a[248:251], a[208:211], v[124:127]\n" \
X_"a[170:171], v34 offset:33280\n" \
M_"v[128:131], a[188:191], a[144:147], v[128:131]\n" \
"s_cmp_eq_u32 s16, 0\n" \
"s_cselect_b32 s4, 0, s4\n" \
M_"v[128:131], a[252:255], a[208:211], v[128:131]\n" \
R_"a[144:147], v28 offset:40960\n" \
M_"v[144:147], a[188:191], a[148:151], v[144:147]\n" \
R_"a[208:211], v29 offset:40960\n" \
M_"v[144:147], a[252:255], a[212:215], v[144:147]\n" \
X_"a[232:233], v34 offset:36864\n" \
M_"v[140:143], a[184:187], a[148:151], v[140:143]\n" \
"s_addk_i32 s84, 0x7c00\n" \
M_"v[140:143], a[248:251], a[212:215], v[140:143]\n" \
B_"v27, s[68:71], s84 offen offset:2048 lds\n" \
M_"v[136:139], a[180:183], a[148:151], v[136:139]\n" \
X_"a[234:235], v34 offset:37376\n" \
M_"v[136:139], a[244:247], a[212:215], v[136:139]\n" \
"s_getpc_b64 s[28:29]\n" \
"L_gp0_%=:\n" \
"s_add_u32 s28, s28, L_gtr-L_gp0_%=\n" \
"s_addc_u32 s29, s29, 0\n" \
M_"v[132:135], a[176:179], a[148:151], v[132:135]\n" \
R_"v[248:251], v22 offset:32768\n" \
M_"v[132:135], a[240:243], a[212:215], v[132:135]\n" \
R_"a[148:151], v28 offset:43008\n" \
M_"v[148:151], a[176:179], a[152:155], v[148:151]\n" \
R_"a[212:215], v29 offset:43008\n" \
M_"v[148:151], a[240:243], a[216:219], v[148:151]\n" \
"s_add_u32 m0, s31, 0x0\n" \
"s_mov_b32 s84, 0x417400\n" \
M_"v[152:155], a[180:183], a[152:155], v[152:155]\n" \
B_"v27, s[68:71], s84 offen offset:3072 lds\n" \
M_"v[152:155], a[244:247], a[216:219], v[152:155]\n" \
R_"v[48:51], v23 offset:32768\n" \
M_"v[156:159], a[184:187], a[152:155], v[156:159]\n" \
"s_add_u32 s28, s28, s4\n" \
"s_addc_u32 s29, s29, 0\n" \
"s_nop 0\n" \
M_"v[156:159], a[248:251], a[216:219], v[156:159]\n" \
"v_accvgpr_write_b32 a96, 0\n" \
"v_accvgpr_write_b32 a97, 0\n" \
M_"v[160:163], a[188:191], a[152:155], v[160:163]\n" \
"v_accvgpr_write_b32 a98, 0\n" \
"v_accvgpr_write_b32 a99, 0\n" \
M_"v[160:163], a[252:255], a[216:219], v[160:163]\n" \
R_"a[152:155], v28 offset:45056\n" \
"v_accvgpr_write_b32 a100, 0\n" \
M_"v[176:179], a[188:191], a[156:159], v[176:179]\n" \
R_"a[216:219], v29 offset:45056\n" \
"v_accvgpr_write_b32 a101, 0\n" \
M_"v[176:179], a[252:255], a[220:223], v[176:179]\n" \
"v_accvgpr_write_b32 a102, 0\n" \
"v_accvgpr_write_b32 a103, 0\n" \
M_"v[172:175], a[184:187], a[156:159], v[172:175]\n" \
"v_accvgpr_write_b32 a112, 0\n" \
"v_accvgpr_write_b32 a113, 0\n" \
M_"v[172:175], a[248:251], a[220:223], v[172:175]\n" \
"v_accvgpr_write_b32 a114, 0\n" \
"v_accvgpr_write_b32 a115, 0\n" \
M_"v[168:171], a[180:183], a[156:159], v[168:171]\n" \
"v_accvgpr_write_b32 a120, 0\n" \
"v_accvgpr_write_b32 a121, 0\n" \
M_"v[168:171], a[244:247], a[220:223], v[168:171]\n" \
"v_accvgpr_write_b32 a122, 0\n" \
"v_accvgpr_write_b32 a123, 0\n" \
M_"v[164:167], a[176:179], a[156:159], v[164:167]\n" \
"v_mov_b32_e32 v180, 0x3f803f80\n" \
"s_mov_b32 s6, 0x33333333\n" \
M_"v[164:167], a[240:243], a[220:223], v[164:167]\n" \
R_"a[156:159], v28 offset:47104\n" \
R_"a[220:223], v29 offset:47104\n" \
"s_waitcnt lgkmcnt(15)\n" \
"s_nop 0\n" \
M_"a[0:3], a[160:163], a[128:131], a[0:3]\n" \
"s_add_u32 s89, s42, 0x3000\n" \
"buffer_store_dwordx4 v[220:223], v20, s[80:83], s89 offen offset:0 sc1\n" \
M_"a[0:3], a[224:227], a[192:195], a[0:3]\n" \
X_"a[176:177], v36 offset:32768\n" \
"s_mov_b32 s7, 0xcccccccc\n" \
M_"a[4:7], a[164:167], a[128:131], a[4:7]\n" \
X_"a[178:179], v36 offset:33792\n" \
"v_cndmask_b32_e64 v181, 0, v180, s[6:7]\n" \
M_"a[4:7], a[228:231], a[192:195], a[4:7]\n" \
X_"a[240:241], v36 offset:40960\n" \
"v_accvgpr_write_b32 a104, v181\n" \
M_"a[12:15], a[172:175], a[128:131], a[12:15]\n" \
X_"a[242:243], v36 offset:41984\n" \
"v_accvgpr_write_b32 a105, v181\n" \
"s_waitcnt lgkmcnt(15)\n" \
M_"a[12:15], a[236:239], a[192:195], a[12:15]\n" \
"v_accvgpr_write_b32 a106, v181\n" \
"v_accvgpr_write_b32 a107, v181\n" \
M_"a[8:11], a[128:131], a[168:171], a[8:11]\n" \
X_"a[180:181], v37 offset:32768\n" \
"s_mov_b32 s6, 0x11111111\n" \
"s_waitcnt lgkmcnt(13)\n" \
M_"a[8:11], a[192:195], a[232:235], a[8:11]\n" \
"s_mov_b32 s7, 0x11111111\n" \
"v_cndmask_b32_e64 v181, 0, v180, s[6:7]\n" \
M_"a[24:27], a[132:135], a[168:171], a[24:27]\n" \
X_"a[182:183], v37 offset:33792\n" \
"v_accvgpr_write_b32 a108, v181\n" \
M_"a[24:27], a[196:199], a[232:235], a[24:27]\n" \
X_"a[244:245], v37 offset:40960\n" \
"v_accvgpr_write_b32 a109, v181\n" \
M_"a[28:31], a[172:175], a[132:135], a[28:31]\n" \
"v_accvgpr_write_b32 a110, v181\n" \
"v_accvgpr_write_b32 a111, v181\n" \
M_"a[28:31], a[236:239], a[196:199], a[28:31]\n" \
X_"a[246:247], v37 offset:41984\n" \
"s_mov_b32 s6, 0x44444444\n" \
M_"a[20:23], a[164:167], a[132:135], a[20:23]\n" \
"s_mov_b32 s7, 0x44444444\n" \
"v_cndmask_b32_e64 v181, 0, v180, s[6:7]\n" \
M_"a[20:23], a[228:231], a[196:199], a[20:23]\n" \
X_"a[184:185], v38 offset:32768\n" \
"v_accvgpr_write_b32 a116, v181\n" \
M_"a[16:19], a[160:163], a[132:135], a[16:19]\n" \
X_"a[186:187], v38 offset:33792\n" \
"v_accvgpr_write_b32 a117, v181\n" \
M_"a[16:19], a[224:227], a[196:199], a[16:19]\n" \
"v_accvgpr_write_b32 a118, v181\n" \
"v_accvgpr_write_b32 a119, v181\n" \
M_"a[32:35], a[160:163], a[136:139], a[32:35]\n" \
"s_add_u32 s88, s42, 0x3000\n" \
"buffer_store_dwordx4 v[224:227], v20, s[80:83], s88 offen offset:1024 sc1\n" \
M_"a[32:35], a[224:227], a[200:203], a[32:35]\n" \
X_"a[248:249], v38 offset:40960\n" \
"v_bfe_i32 v181, v20, 4, 1\n" \
M_"a[36:39], a[164:167], a[136:139], a[36:39]\n" \
"v_lshlrev_b32_e32 v45, 3, v20\n" \
"v_add_u32_e32 v45, s30, v45\n" \
M_"a[36:39], a[228:231], a[200:203], a[36:39]\n" \
"s_getpc_b64 s[86:87]\n" \
"L_ct0_%=:\n" \
"s_add_u32 s86, s86, L_ap0-L_ct0_%=\n" \
"s_addc_u32 s87, s87, 0\n" \
"s_add_u32 m0, s40, 0x2000\n" \
M_"a[44:47], a[172:175], a[136:139], a[44:47]\n" \
"global_load_lds_dword v45, s[86:87]\n" \
M_"a[44:47], a[236:239], a[200:203], a[44:47]\n" \
X_"a[250:251], v38 offset:41984\n" \
"v_and_b32_e32 v181, 0xaaaaaaaa, v181\n" \
M_"a[40:43], a[136:139], a[168:171], a[40:43]\n" \
X_"a[188:189], v39 offset:32768\n" \
"v_or_b32_e32 v181, 0x44444444, v181\n" \
M_"a[40:43], a[200:203], a[232:235], a[40:43]\n" \
X_"a[190:191], v39 offset:33792\n" \
"v_accvgpr_write_b32 a126, v181\n" \
M_"a[56:59], a[140:143], a[168:171], a[56:59]\n" \
X_"a[252:253], v39 offset:40960\n" \
"v_bfe_u32 v180, v20, 5, 3\n" \
M_"a[56:59], a[204:207], a[232:235], a[56:59]\n" \
X_"a[254:255], v39 offset:41984\n" \
"v_bfe_u32 v181, v20, 8, 2\n" \
M_"a[60:63], a[172:175], a[140:143], a[60:63]\n" \
"v_lshlrev_b32_e32 v181, 1, v181\n" \
"v_sub_u32_e32 v180, v180, v181\n" \
M_"a[60:63], a[236:239], a[204:207], a[60:63]\n" \
"v_and_b32_e64 v181, 16, v20\n" \
"v_mov_b32_e32 v182, 0x3f80\n" \
M_"a[52:55], a[164:167], a[140:143], a[52:55]\n" \
"v_lshlrev_b32_e32 v181, v181, v182\n" \
"v_cmp_eq_u32_e32 vcc, 0, v180\n" \
M_"a[52:55], a[228:231], a[204:207], a[52:55]\n" \
"v_cmp_eq_u32_e64 s[6:7], 1, v180\n" \
"v_cndmask_b32_e64 v182, 0, v181, vcc\n" \
M_"a[48:51], a[160:163], a[140:143], a[48:51]\n" \
"v_cndmask_b32_e64 v183, 0, v181, s[6:7]\n" \
"v_accvgpr_write_b32 a124, v182\n" \
M_"a[48:51], a[224:227], a[204:207], a[48:51]\n" \
"v_accvgpr_write_b32 a125, v183\n" \
"s_waitcnt lgkmcnt(15)\n" \
M_"a[64:67], a[164:167], v[248:251], a[64:67]\n" \
M_"a[64:67], a[228:231], v[48:51], a[64:67]\n" \
M_"a[80:83], a[172:175], v[248:251], a[80:83]\n" \
M_"a[80:83], a[236:239], v[48:51], a[80:83]\n" \
M_"a[68:71], v[248:251], a[168:171], a[68:71]\n" \
"s_waitcnt vmcnt(3) lgkmcnt(0)\n" \
M_"a[68:71], v[48:51], a[232:235], a[68:71]\n" \
"s_barrier\n" \
"v_xor_b32_e32 v28, 0x20000, v28\n" \
"v_xor_b32_e32 v29, 0x20000, v29\n" \
M_"v[52:55], a[176:179], a[128:131], v[52:55]\n" \
X_"a[160:161], v32 offset:0\n" \
"v_accvgpr_read_b32 v184, a0\n" \
M_"v[52:55], a[240:243], a[192:195], v[52:55]\n" \
"v_accvgpr_read_b32 v185, a1\n" \
"v_accvgpr_read_b32 v186, a2\n" \
M_"v[56:59], a[180:183], a[128:131], v[56:59]\n" \
X_"a[162:163], v32 offset:512\n" \
"v_accvgpr_read_b32 v187, a3\n" \
M_"v[56:59], a[244:247], a[192:195], v[56:59]\n" \
"v_accvgpr_read_b32 v188, a4\n" \
"v_accvgpr_read_b32 v189, a5\n" \
M_"v[60:63], a[184:187], a[128:131], v[60:63]\n" \
"v_accvgpr_read_b32 v190, a6\n" \
"v_accvgpr_read_b32 v191, a7\n" \
M_"v[60:63], a[248:251], a[192:195], v[60:63]\n" \
X_"a[224:225], v32 offset:4096\n" \
"v_accvgpr_read_b32 v196, a12\n" \
M_"v[64:67], a[188:191], a[128:131], v[64:67]\n" \
"v_accvgpr_read_b32 v197, a13\n" \
"v_accvgpr_read_b32 v198, a14\n" \
M_"v[64:67], a[252:255], a[192:195], v[64:67]\n" \
R_"a[128:131], v28 offset:0\n" \
"v_accvgpr_read_b32 v199, a15\n" \
M_"v[80:83], a[188:191], a[132:135], v[80:83]\n" \
R_"a[192:195], v29 offset:0\n" \
"v_accvgpr_read_b32 v192, a8\n" \
M_"v[80:83], a[252:255], a[196:199], v[80:83]\n" \
X_"a[226:227], v32 offset:4608\n" \
"v_accvgpr_read_b32 v193, a9\n" \
M_"v[76:79], a[184:187], a[132:135], v[76:79]\n" \
"v_accvgpr_read_b32 v194, a10\n" \
"v_accvgpr_read_b32 v195, a11\n" \
M_"v[76:79], a[248:251], a[196:199], v[76:79]\n" \
X_"a[164:165], v33 offset:0\n" \
"v_accvgpr_read_b32 v208, a24\n" \
M_"v[72:75], a[180:183], a[132:135], v[72:75]\n" \
"v_accvgpr_read_b32 v209, a25\n" \
"v_accvgpr_read_b32 v210, a26\n" \
M_"v[72:75], a[244:247], a[196:199], v[72:75]\n" \
"v_accvgpr_read_b32 v211, a27\n" \
"v_accvgpr_read_b32 v212, a28\n" \
M_"v[68:71], a[176:179], a[132:135], v[68:71]\n" \
X_"a[166:167], v33 offset:512\n" \
"v_accvgpr_read_b32 v213, a29\n" \
M_"v[68:71], a[240:243], a[196:199], v[68:71]\n" \
R_"a[132:135], v28 offset:2048\n" \
"v_accvgpr_read_b32 v214, a30\n" \
M_"v[84:87], a[176:179], a[136:139], v[84:87]\n" \
R_"a[196:199], v29 offset:2048\n" \
"v_accvgpr_read_b32 v215, a31\n" \
M_"v[84:87], a[240:243], a[200:203], v[84:87]\n" \
X_"a[228:229], v33 offset:4096\n" \
"v_accvgpr_read_b32 v204, a20\n" \
M_"v[88:91], a[180:183], a[136:139], v[88:91]\n" \
"v_accvgpr_read_b32 v205, a21\n" \
"v_accvgpr_read_b32 v206, a22\n" \
M_"v[88:91], a[244:247], a[200:203], v[88:91]\n" \
"v_accvgpr_read_b32 v207, a23\n" \
"v_accvgpr_read_b32 v200, a16\n" \
M_"v[92:95], a[184:187], a[136:139], v[92:95]\n" \
X_"a[230:231], v33 offset:4608\n" \
"v_accvgpr_read_b32 v201, a17\n" \
M_"v[92:95], a[248:251], a[200:203], v[92:95]\n" \
"v_accvgpr_read_b32 v202, a18\n" \
"v_accvgpr_read_b32 v203, a19\n" \
M_"v[96:99], a[188:191], a[136:139], v[96:99]\n" \
"v_accvgpr_read_b32 v216, a32\n" \
"v_accvgpr_read_b32 v217, a33\n" \
M_"v[96:99], a[252:255], a[200:203], v[96:99]\n" \
R_"a[136:139], v28 offset:4096\n" \
"v_accvgpr_read_b32 v218, a34\n" \
M_"v[112:115], a[188:191], a[140:143], v[112:115]\n" \
R_"a[200:203], v29 offset:4096\n" \
"v_accvgpr_read_b32 v219, a35\n" \
M_"v[112:115], a[252:255], a[204:207], v[112:115]\n" \
X_"a[172:173], v35 offset:0\n" \
"v_accvgpr_read_b32 v220, a36\n" \
M_"v[108:111], a[184:187], a[140:143], v[108:111]\n" \
X_"a[174:175], v35 offset:512\n" \
"v_accvgpr_read_b32 v221, a37\n" \
M_"v[108:111], a[248:251], a[204:207], v[108:111]\n" \
"v_accvgpr_read_b32 v222, a38\n" \
"v_accvgpr_read_b32 v223, a39\n" \
M_"v[104:107], a[180:183], a[140:143], v[104:107]\n" \
X_"a[236:237], v35 offset:4096\n" \
"v_accvgpr_read_b32 v228, a44\n" \
M_"v[104:107], a[244:247], a[204:207], v[104:107]\n" \
"v_accvgpr_read_b32 v229, a45\n" \
"v_accvgpr_read_b32 v230, a46\n" \
M_"v[100:103], a[176:179], a[140:143], v[100:103]\n" \
"v_accvgpr_read_b32 v231, a47\n" \
"v_accvgpr_read_b32 v224, a40\n" \
M_"v[100:103], a[240:243], a[204:207], v[100:103]\n" \
R_"a[140:143], v28 offset:6144\n" \
"v_accvgpr_read_b32 v225, a41\n" \
M_"v[116:119], a[176:179], a[144:147], v[116:119]\n" \
R_"a[204:207], v29 offset:6144\n" \
"v_accvgpr_read_b32 v226, a42\n" \
M_"v[116:119], a[240:243], a[208:211], v[116:119]\n" \
X_"a[238:239], v35 offset:4608\n" \
"v_accvgpr_read_b32 v227, a43\n" \
M_"v[120:123], a[180:183], a[144:147], v[120:123]\n" \
X_"a[168:169], v34 offset:0\n" \
"v_accvgpr_read_b32 v240, a56\n" \
M_"v[120:123], a[244:247], a[208:211], v[120:123]\n" \
"v_accvgpr_read_b32 v241, a57\n" \
"v_accvgpr_read_b32 v242, a58\n" \
M_"v[124:127], a[184:187], a[144:147], v[124:127]\n" \
"v_accvgpr_read_b32 v243, a59\n" \
"v_accvgpr_read_b32 v244, a60\n" \
M_"v[124:127], a[248:251], a[208:211], v[124:127]\n" \
X_"a[170:171], v34 offset:512\n" \
"v_accvgpr_read_b32 v245, a61\n" \
M_"v[128:131], a[188:191], a[144:147], v[128:131]\n" \
"v_accvgpr_read_b32 v246, a62\n" \
"v_accvgpr_read_b32 v247, a63\n" \
M_"v[128:131], a[252:255], a[208:211], v[128:131]\n" \
R_"a[144:147], v28 offset:8192\n" \
"v_accvgpr_read_b32 v236, a52\n" \
M_"v[144:147], a[188:191], a[148:151], v[144:147]\n" \
R_"a[208:211], v29 offset:8192\n" \
"v_accvgpr_read_b32 v237, a53\n" \
M_"v[144:147], a[252:255], a[212:215], v[144:147]\n" \
X_"a[232:233], v34 offset:4096\n" \
"v_accvgpr_read_b32 v238, a54\n" \
M_"v[140:143], a[184:187], a[148:151], v[140:143]\n" \
"v_accvgpr_read_b32 v239, a55\n" \
"v_accvgpr_read_b32 v232, a48\n" \
M_"v[140:143], a[248:251], a[212:215], v[140:143]\n" \
X_"a[234:235], v34 offset:4608\n" \
"v_accvgpr_read_b32 v233, a49\n" \
M_"v[136:139], a[180:183], a[148:151], v[136:139]\n" \
"v_accvgpr_read_b32 v234, a50\n" \
"v_accvgpr_read_b32 v235, a51\n" \
M_"v[136:139], a[244:247], a[212:215], v[136:139]\n" \
"v_accvgpr_read_b32 v180, a64\n" \
"v_accvgpr_read_b32 v181, a65\n" \
M_"v[132:135], a[176:179], a[148:151], v[132:135]\n" \
R_"v[248:251], v22 offset:0\n" \
"v_accvgpr_read_b32 v182, a66\n" \
M_"v[132:135], a[240:243], a[212:215], v[132:135]\n" \
R_"a[148:151], v28 offset:10240\n" \
"v_accvgpr_read_b32 v183, a67\n" \
M_"v[148:151], a[176:179], a[152:155], v[148:151]\n" \
R_"a[212:215], v29 offset:10240\n" \
"v_accvgpr_read_b32 v252, a68\n" \
M_"v[148:151], a[240:243], a[216:219], v[148:151]\n" \
R_"v[48:51], v23 offset:0\n" \
"v_accvgpr_read_b32 v253, a69\n" \
M_"v[152:155], a[180:183], a[152:155], v[152:155]\n" \
"v_accvgpr_read_b32 v254, a70\n" \
"v_accvgpr_read_b32 v255, a71\n" \
M_"v[152:155], a[244:247], a[216:219], v[152:155]\n" \
"v_lshrrev_b32_e32 v24, 9, v20\n" \
"v_and_b32_e32 v26, 0x1ff, v20\n" \
M_"v[156:159], a[184:187], a[152:155], v[156:159]\n" \
"v_lshl_add_u32 v24, v24, 3, v26\n" \
"v_lshrrev_b32_e64 v25, 8, v20\n" \
M_"v[156:159], a[248:251], a[216:219], v[156:159]\n" \
"v_mul_u32_u24_e32 v25, 0x24700, v25\n" \
"v_add_u32_e64 v25, v25, v20\n" \
M_"v[160:163], a[188:191], a[152:155], v[160:163]\n" \
"v_lshrrev_b32_e32 v26, 2, v20\n" \
"v_add_u32_e32 v26, s45, v26\n" \
M_"v[160:163], a[252:255], a[216:219], v[160:163]\n" \
R_"a[152:155], v28 offset:12288\n" \
"v_add_u32_e64 v27, s46, v20\n" \
M_"v[176:179], a[188:191], a[156:159], v[176:179]\n" \
R_"a[216:219], v29 offset:12288\n" \
"v_add_u32_e64 v21, s43, v27\n" \
M_"v[176:179], a[252:255], a[220:223], v[176:179]\n" \
M_"v[172:175], a[184:187], a[156:159], v[172:175]\n" \
M_"v[172:175], a[248:251], a[220:223], v[172:175]\n" \
M_"v[168:171], a[180:183], a[156:159], v[168:171]\n" \
M_"v[168:171], a[244:247], a[220:223], v[168:171]\n" \
M_"v[164:167], a[176:179], a[156:159], v[164:167]\n" \
M_"v[164:167], a[240:243], a[220:223], v[164:167]\n" \
R_"a[156:159], v28 offset:14336\n" \
R_"a[220:223], v29 offset:14336\n" \
"s_waitcnt lgkmcnt(15)\n" \
"s_nop 0\n" \
M_"v[184:187], a[160:163], a[128:131], v[184:187]\n" \
"v_bfe_u32 v44, v20, 2, 5\n" \
X_"a[176:177], v36 offset:0\n" \
M_"v[184:187], a[224:227], a[192:195], v[184:187]\n" \
"buffer_load_dword v44, v44, s[80:83], s38 offen offset:0 sc1\n" \
M_"v[188:191], a[164:167], a[128:131], v[188:191]\n" \
X_"a[178:179], v36 offset:1024\n" \
M_"v[188:191], a[228:231], a[192:195], v[188:191]\n" \
X_"a[240:241], v36 offset:8192\n" \
M_"v[196:199], a[172:175], a[128:131], v[196:199]\n" \
X_"a[242:243], v36 offset:9216\n" \
"s_waitcnt lgkmcnt(15)\n" \
M_"v[196:199], a[236:239], a[192:195], v[196:199]\n" \
M_"v[192:195], a[128:131], a[168:171], v[192:195]\n" \
X_"a[180:181], v37 offset:0\n" \
"s_waitcnt lgkmcnt(13)\n" \
M_"v[192:195], a[192:195], a[232:235], v[192:195]\n" \
M_"v[208:211], a[132:135], a[168:171], v[208:211]\n" \
X_"a[182:183], v37 offset:1024\n" \
M_"v[208:211], a[196:199], a[232:235], v[208:211]\n" \
X_"a[244:245], v37 offset:8192\n" \
".fill (L_pb + 36576 - .) / 4, 4, 0xBF800000\n" \
"L_pp9:\n" \
"s_cbranch_execz L_pret\n" \
"s_nop 0\n" \
M_"v[212:215], a[172:175], a[132:135], v[212:215]\n" \
M_"v[212:215], a[236:239], a[196:199], v[212:215]\n" \
X_"a[246:247], v37 offset:9216\n" \
C_"v188, v188, v189\n" \
M_"v[204:207], a[164:167], a[132:135], v[204:207]\n" \
C_"v189, v190, v191\n" \
C_"v190, v198, v199\n" \
M_"v[204:207], a[228:231], a[196:199], v[204:207]\n" \
X_"a[184:185], v38 offset:0\n" \
"v_mov_b32_e32 v191, 0x3f80\n" \
M_"v[200:203], a[160:163], a[132:135], v[200:203]\n" \
X_"a[186:187], v38 offset:1024\n" \
C_"v184, v184, v185\n" \
M_"v[200:203], a[224:227], a[196:199], v[200:203]\n" \
C_"v185, v186, v187\n" \
C_"v186, v196, v197\n" \
M_"v[216:219], a[160:163], a[136:139], v[216:219]\n" \
X_"a[248:249], v38 offset:8192\n" \
"v_mov_b32_e64 v187, 0\n" \
M_"v[216:219], a[224:227], a[200:203], v[216:219]\n" \
X_"a[250:251], v38 offset:9216\n" \
M_"v[220:223], a[164:167], a[136:139], v[220:223]\n" \
M_"v[220:223], a[228:231], a[200:203], v[220:223]\n" \
X_"a[188:189], v39 offset:0\n" \
C_"v204, v204, v205\n" \
M_"v[228:231], a[172:175], a[136:139], v[228:231]\n" \
C_"v205, v206, v207\n" \
C_"v206, v214, v215\n" \
M_"v[228:231], a[236:239], a[200:203], v[228:231]\n" \
X_"a[190:191], v39 offset:1024\n" \
"v_mov_b32_e32 v207, 0x3f80\n" \
M_"v[224:227], a[136:139], a[168:171], v[224:227]\n" \
X_"a[252:253], v39 offset:8192\n" \
C_"v200, v200, v201\n" \
M_"v[224:227], a[200:203], a[232:235], v[224:227]\n" \
C_"v201, v202, v203\n" \
C_"v202, v212, v213\n" \
M_"v[240:243], a[140:143], a[168:171], v[240:243]\n" \
X_"a[254:255], v39 offset:9216\n" \
"v_mov_b32_e64 v203, 0\n" \
M_"v[240:243], a[204:207], a[232:235], v[240:243]\n" \
M_"v[244:247], a[172:175], a[140:143], v[244:247]\n" \
M_"v[244:247], a[236:239], a[204:207], v[244:247]\n" \
C_"v220, v220, v221\n" \
C_"v221, v222, v223\n" \
M_"v[236:239], a[164:167], a[140:143], v[236:239]\n" \
C_"v222, v230, v231\n" \
"v_mov_b32_e32 v223, 0x3f80\n" \
M_"v[236:239], a[228:231], a[204:207], v[236:239]\n" \
C_"v216, v216, v217\n" \
C_"v217, v218, v219\n" \
M_"v[232:235], a[160:163], a[140:143], v[232:235]\n" \
C_"v218, v228, v229\n" \
"v_mov_b32_e64 v219, 0\n" \
M_"v[232:235], a[224:227], a[204:207], v[232:235]\n" \
"s_waitcnt lgkmcnt(15)\n" \
"s_nop 0\n" \
M_"v[180:183], a[164:167], v[248:251], v[180:183]\n" \
M_"v[180:183], a[228:231], v[48:51], v[180:183]\n" \
M_"a[80:83], a[172:175], v[248:251], a[80:83]\n" \
M_"a[80:83], a[236:239], v[48:51], a[80:83]\n" \
C_"v236, v236, v237\n" \
C_"v237, v238, v239\n" \
M_"v[252:255], v[248:251], a[168:171], v[252:255]\n" \
C_"v238, v246, v247\n" \
"v_mov_b32_e32 v239, 0x3f80\n" \
M_"v[252:255], v[48:51], a[232:235], v[252:255]\n" \
C_"v232, v232, v233\n" \
C_"v233, v234, v235\n" \
"s_waitcnt vmcnt(0) lgkmcnt(0)\n" \
"s_barrier\n" \
"v_mov_b32_e32 v45, s62\n" \
"s_mov_b64 exec, 1\n" \
"buffer_store_dword v45, off, s[80:83], s37 offset:32 sc1\n" \
"s_mov_b64 exec, -1\n" \
"v_cmp_ne_u32_e32 vcc, s62, v44\n" \
"s_nop 4\n" \
"s_cbranch_vccnz L_poll160_%=\n" \
"L_pollr160_%=:\n" \
M_"v[52:55], a[176:179], a[128:131], v[52:55]\n" \
"s_mov_b32 s73, s50\n" \
"s_sub_u32 m0, s31, 0x10000\n" \
M_"v[52:55], a[240:243], a[192:195], v[52:55]\n" \
B_"v20, s[80:83], s73 offen offset:0 sc1 lds\n" \
M_"v[56:59], a[180:183], a[128:131], v[56:59]\n" \
"s_add_u32 s74, s50, 0x4000\n" \
"s_add_u32 s75, s50, 0x8000\n" \
M_"v[56:59], a[244:247], a[192:195], v[56:59]\n" \
"s_mov_b32 s70, 0x3e0293ee\n" \
"s_mov_b32 s76, s52\n" \
M_"v[60:63], a[184:187], a[128:131], v[60:63]\n" \
"v_mul_f32_e32 v52, s70, v52\n" \
"v_mul_f32_e32 v53, s70, v53\n" \
M_"v[60:63], a[248:251], a[192:195], v[60:63]\n" \
B_"v20, s[80:83], s73 offen offset:1024 sc1 lds\n" \
M_"v[64:67], a[188:191], a[128:131], v[64:67]\n" \
"v_mul_f32_e32 v54, s70, v54\n" \
"v_mul_f32_e32 v55, s70, v55\n" \
M_"v[64:67], a[252:255], a[192:195], v[64:67]\n" \
C_"v52, v52, v53\n" \
"v_mul_f32_e64 v56, s70, v56\n" \
M_"v[80:83], a[188:191], a[132:135], v[80:83]\n" \
"v_mul_f32_e32 v60, s70, v60\n" \
"v_mul_f32_e32 v61, s70, v61\n" \
M_"v[80:83], a[252:255], a[196:199], v[80:83]\n" \
B_"v20, s[80:83], s73 offen offset:2048 sc1 lds\n" \
M_"v[76:79], a[184:187], a[132:135], v[76:79]\n" \
"v_mul_f32_e32 v62, s70, v62\n" \
"v_mul_f32_e32 v63, s70, v63\n" \
M_"v[76:79], a[248:251], a[196:199], v[76:79]\n" \
C_"v60, v60, v61\n" \
"v_mul_f32_e64 v57, s70, v57\n" \
M_"v[72:75], a[180:183], a[132:135], v[72:75]\n" \
"v_mul_f32_e32 v58, s70, v58\n" \
"v_mul_f32_e32 v59, s70, v59\n" \
M_"v[72:75], a[244:247], a[196:199], v[72:75]\n" \
B_"v20, s[80:83], s73 offen offset:3072 sc1 lds\n" \
M_"v[68:71], a[176:179], a[132:135], v[68:71]\n" \
"s_sub_u32 m0, s31, s51\n" \
C_"v53, v54, v55\n" \
M_"v[68:71], a[240:243], a[196:199], v[68:71]\n" \
"v_mul_f32_e32 v64, s70, v64\n" \
"v_mul_f32_e64 v65, s70, v65\n" \
M_"v[84:87], a[176:179], a[136:139], v[84:87]\n" \
"v_mul_f32_e32 v66, s70, v66\n" \
"v_mul_f32_e32 v67, s70, v67\n" \
M_"v[84:87], a[240:243], a[200:203], v[84:87]\n" \
B_"v20, s[80:83], s74 offen offset:0 sc1 lds\n" \
M_"v[88:91], a[180:183], a[136:139], v[88:91]\n" \
C_"v61, v62, v63\n" \
C_"v54, v56, v57\n" \
M_"v[88:91], a[244:247], a[200:203], v[88:91]\n" \
C_"v55, v58, v59\n" \
C_"v62, v64, v65\n" \
M_"v[92:95], a[184:187], a[136:139], v[92:95]\n" \
C_"v63, v66, v67\n" \
"v_mul_f32_e64 v68, s70, v68\n" \
M_"v[92:95], a[248:251], a[200:203], v[92:95]\n" \
B_"v20, s[80:83], s74 offen offset:1024 sc1 lds\n" \
M_"v[96:99], a[188:191], a[136:139], v[96:99]\n" \
"ds_write_b128 v27, v[52:55] offset:0\n" \
M_"v[96:99], a[252:255], a[200:203], v[96:99]\n" \
"v_mul_f32_e32 v69, s70, v69\n" \
"v_mul_f32_e32 v76, s70, v76\n" \
M_"v[112:115], a[188:191], a[140:143], v[112:115]\n" \
"v_mul_f32_e32 v77, s70, v77\n" \
"v_mul_f32_e32 v70, s70, v70\n" \
M_"v[112:115], a[252:255], a[204:207], v[112:115]\n" \
B_"v20, s[80:83], s74 offen offset:2048 sc1 lds\n" \
M_"v[108:111], a[184:187], a[140:143], v[108:111]\n" \
"ds_write_b128 v27, v[60:63] offset:1024\n" \
M_"v[108:111], a[248:251], a[204:207], v[108:111]\n" \
"v_mul_f32_e64 v71, s70, v71\n" \
C_"v68, v68, v69\n" \
M_"v[104:107], a[180:183], a[140:143], v[104:107]\n" \
"v_mul_f32_e32 v78, s70, v78\n" \
"v_mul_f32_e32 v79, s70, v79\n" \
M_"v[104:107], a[244:247], a[204:207], v[104:107]\n" \
B_"v20, s[80:83], s74 offen offset:3072 sc1 lds\n" \
M_"v[100:103], a[176:179], a[140:143], v[100:103]\n" \
"s_sub_u32 m0, s31, 0x4000\n" \
C_"v76, v76, v77\n" \
M_"v[100:103], a[240:243], a[204:207], v[100:103]\n" \
"v_mul_f32_e32 v72, s70, v72\n" \
"v_mul_f32_e32 v73, s70, v73\n" \
M_"v[116:119], a[176:179], a[144:147], v[116:119]\n" \
"v_mul_f32_e32 v74, s70, v74\n" \
"v_mul_f32_e32 v75, s70, v75\n" \
M_"v[116:119], a[240:243], a[208:211], v[116:119]\n" \
B_"v20, s[80:83], s75 offen offset:0 sc1 lds\n" \
M_"v[120:123], a[180:183], a[144:147], v[120:123]\n" \
C_"v69, v70, v71\n" \
"v_mul_f32_e64 v80, s70, v80\n" \
M_"v[120:123], a[244:247], a[208:211], v[120:123]\n" \
"v_mul_f32_e32 v81, s70, v81\n" \
"v_mul_f32_e32 v82, s70, v82\n" \
M_"v[124:127], a[184:187], a[144:147], v[124:127]\n" \
"v_mul_f32_e64 v83, s70, v83\n" \
C_"v77, v78, v79\n" \
M_"v[124:127], a[248:251], a[208:211], v[124:127]\n" \
B_"v20, s[80:83], s75 offen offset:1024 sc1 lds\n" \
M_"v[128:131], a[188:191], a[144:147], v[128:131]\n" \
C_"v70, v72, v73\n" \
C_"v71, v74, v75\n" \
M_"v[128:131], a[252:255], a[208:211], v[128:131]\n" \
C_"v78, v80, v81\n" \
C_"v79, v82, v83\n" \
M_"v[144:147], a[188:191], a[148:151], v[144:147]\n" \
"ds_write_b128 v27, v[68:71] offset:4096\n" \
M_"v[144:147], a[252:255], a[212:215], v[144:147]\n" \
B_"v20, s[80:83], s75 offen offset:2048 sc1 lds\n" \
M_"v[140:143], a[184:187], a[148:151], v[140:143]\n" \
C_"v180, v180, v181\n" \
"v_accvgpr_read_b32 v50, a82\n" \
M_"v[140:143], a[248:251], a[212:215], v[140:143]\n" \
"ds_write_b128 v27, v[76:79] offset:5120\n" \
M_"v[136:139], a[180:183], a[148:151], v[136:139]\n" \
"v_accvgpr_read_b32 v51, a83\n" \
C_"v181, v182, v183\n" \
M_"v[136:139], a[244:247], a[212:215], v[136:139]\n" \
B_"v20, s[80:83], s75 offen offset:3072 sc1 lds\n" \
M_"v[132:135], a[176:179], a[148:151], v[132:135]\n" \
"s_mov_b32 m0, s53\n" \
C_"v182, v50, v51\n" \
M_"v[132:135], a[240:243], a[212:215], v[132:135]\n" \
"v_mov_b32_e32 v183, 0x3f80\n" \
C_"v234, v244, v245\n" \
M_"v[148:151], a[176:179], a[152:155], v[148:151]\n" \
"v_mov_b32_e32 v235, 0\n" \
C_"v54, v192, v193\n" \
M_"v[148:151], a[240:243], a[216:219], v[148:151]\n" \
B_"v20, s[80:83], s76 offen offset:0 sc1 lds\n" \
M_"v[152:155], a[180:183], a[152:155], v[152:155]\n" \
C_"v55, v194, v195\n" \
C_"v56, v208, v209\n" \
M_"v[152:155], a[244:247], a[216:219], v[152:155]\n" \
C_"v57, v210, v211\n" \
C_"v58, v224, v225\n" \
C_"v59, v226, v227\n" \
C_"v60, v240, v241\n" \
C_"v61, v242, v243\n" \
M_"v[32:35], v[180:183], v[184:187], 0\n" \
B_"v20, s[80:83], s76 offen offset:1024 sc1 lds\n" \
M_"v[156:159], a[184:187], a[152:155], v[156:159]\n" \
M_"v[192:195], v[188:191], v[184:187], 0\n" \
M_"v[208:211], v[204:207], v[200:203], 0\n" \
M_"v[36:39], v[188:191], v[200:203], 0\n" \
M_"v[224:227], v[220:223], v[216:219], 0\n" \
M_"v[248:251], v[204:207], v[216:219], 0\n" \
M_"v[240:243], v[236:239], v[232:235], 0\n" \
M_"v[64:67], v[220:223], v[232:235], 0\n" \
"s_mov_b32 s64, 0xaaaaaaaa\n" \
"s_mov_b32 s65, 0xaaaaaaaa\n" \
M_"v[156:159], a[248:251], a[216:219], v[156:159]\n" \
"v_cndmask_b32_e64 v32, v32, v33, s[64:65]\n" \
"v_cndmask_b32_e64 v34, v34, v35, s[64:65]\n" \
M_"v[160:163], a[188:191], a[152:155], v[160:163]\n" \
"s_mov_b32 s66, 0xfff0ffff\n" \
"s_mov_b32 s67, 0xf000ff00\n" \
"s_mov_b32 s68, 0xffc0fffc\n" \
"s_mov_b32 s69, 0xc000fc00\n" \
"v_cndmask_b32_e64 v192, v192, v193, s[64:65]\n" \
"v_add_f32_e64 v32, s47, v32\n" \
"v_cndmask_b32_e64 v194, v194, v195, s[64:65]\n" \
"v_add_f32_e64 v34, s47, v34\n" \
"v_cndmask_b32_e64 v208, v208, v209, s[64:65]\n" \
"v_cndmask_b32_e64 v36, v36, v37, s[64:65]\n" \
"v_cndmask_b32_e64 v210, v210, v211, s[64:65]\n" \
"v_cndmask_b32_e64 v38, v38, v39, s[64:65]\n" \
"v_cndmask_b32_e64 v224, v224, v225, s[64:65]\n" \
"v_cndmask_b32_e64 v248, v248, v249, s[64:65]\n" \
"v_cndmask_b32_e64 v226, v226, v227, s[64:65]\n" \
"v_cndmask_b32_e64 v250, v250, v251, s[64:65]\n" \
"v_cndmask_b32_e64 v240, v240, v241, s[64:65]\n" \
"v_cndmask_b32_e64 v64, v64, v65, s[64:65]\n" \
"v_cndmask_b32_e64 v242, v242, v243, s[64:65]\n" \
"v_cndmask_b32_e64 v66, v66, v67, s[64:65]\n" \
"v_cndmask_b32_e64 v192, v32, v192, s[66:67]\n" \
"v_cndmask_b32_e64 v194, v34, v194, s[68:69]\n" \
"v_cndmask_b32_e64 v208, v36, v208, s[66:67]\n" \
"v_cndmask_b32_e64 v210, v38, v210, s[68:69]\n" \
"v_cndmask_b32_e64 v224, v248, v224, s[66:67]\n" \
"v_cndmask_b32_e64 v226, v250, v226, s[68:69]\n" \
"v_cndmask_b32_e64 v240, v64, v240, s[66:67]\n" \
"v_cndmask_b32_e64 v242, v66, v242, s[68:69]\n" \
M_"v[160:163], a[252:255], a[216:219], v[160:163]\n" \
"v_max_f32_e32 v193, v192, v194\n" \
"v_max_f32_e32 v209, v208, v210\n" \
"v_max_f32_e32 v225, v224, v226\n" \
"v_max_f32_e32 v241, v240, v242\n" \
"v_mul_f32_e32 v84, s70, v84\n" \
"v_permlane32_swap_b32_e32 v193, v225\n" \
"v_permlane32_swap_b32_e32 v209, v241\n" \
"v_mul_f32_e32 v85, s70, v85\n" \
"v_mul_f32_e32 v92, s70, v92\n" \
"v_mul_f32_e32 v93, s70, v93\n" \
"v_permlane32_swap_b32_e32 v196, v228\n" \
"v_permlane32_swap_b32_e32 v212, v244\n" \
"v_max_f32_e32 v193, v193, v225\n" \
"v_max_f32_e32 v209, v209, v241\n" \
"v_accvgpr_read_b32 v48, a80\n" \
"v_accvgpr_read_b32 v49, a81\n" \
"v_mul_f32_e32 v86, s70, v86\n" \
"v_mul_f32_e32 v87, s70, v87\n" \
C_"v84, v84, v85\n" \
"v_mul_f32_e32 v94, s70, v94\n" \
"v_mul_f32_e32 v95, s70, v95\n" \
C_"v92, v92, v93\n" \
"v_permlane16_swap_b32_e32 v228, v244\n" \
"v_permlane16_swap_b32_e32 v193, v209\n" \
M_"v[176:179], a[188:191], a[156:159], v[176:179]\n" \
"s_mov_b32 s72, 0x3eae1a93\n" \
"v_mul_f32_e32 v88, s70, v88\n" \
"v_mul_f32_e32 v89, s70, v89\n" \
"v_mul_f32_e32 v90, s70, v90\n" \
"v_mul_f32_e32 v91, s70, v91\n" \
C_"v85, v86, v87\n" \
"v_mul_f32_e32 v96, s70, v96\n" \
"v_mul_f32_e32 v97, s70, v97\n" \
"v_mul_f32_e32 v98, s70, v98\n" \
"v_mul_f32_e32 v99, s70, v99\n" \
C_"v93, v94, v95\n" \
"v_mul_f32_e32 v45, 0xbfb8aa3b, v228\n" \
"v_mfma_f32_16x16x16_bf16 v[48:51], v[182:183], v[46:47], 0\n" \
"v_mfma_f32_16x16x16_bf16 v[196:199], v[190:191], v[46:47], 0\n" \
"v_mfma_f32_16x16x16_bf16 v[212:215], v[206:207], v[46:47], 0\n" \
"v_max_f32_e64 v193, v193, v209\n" \
C_"v86, v88, v89\n" \
C_"v87, v90, v91\n" \
C_"v94, v96, v97\n" \
C_"v95, v98, v99\n" \
"v_mfma_f32_16x16x16_bf16 v[228:231], v[222:223], v[46:47], 0\n" \
"v_mul_f32_e32 v193, s72, v193\n" \
"v_mov_b32_e32 v209, v193\n" \
"ds_write_b128 v27, v[84:87] offset:8192\n" \
"v_mfma_f32_16x16x16_bf16 v[244:247], v[238:239], v[46:47], 0\n" \
"v_permlane16_swap_b32_e32 v193, v209\n" \
"s_mov_b32 s71, 0x3e0293ee\n" \
M_"v[176:179], a[252:255], a[220:223], v[176:179]\n" \
"v_mov_b32_e32 v225, v193\n" \
"v_mov_b32_e32 v241, v209\n" \
"v_mul_f32_e32 v100, s70, v100\n" \
"v_mul_f32_e64 v101, s70, v101\n" \
"ds_write_b128 v27, v[92:95] offset:9216\n" \
"v_mul_f32_e32 v108, s70, v108\n" \
"v_mul_f32_e32 v109, s70, v109\n" \
"v_pk_mul_f32 v[116:117], v[116:117], s[70:71]\n" \
"v_pk_mul_f32 v[124:125], v[124:125], s[70:71]\n" \
"v_pk_mul_f32 v[132:133], v[132:133], s[70:71]\n" \
"v_pk_mul_f32 v[140:141], v[140:141], s[70:71]\n" \
"v_pk_mul_f32 v[148:149], v[148:149], s[70:71]\n" \
"v_pk_mul_f32 v[156:157], v[156:157], s[70:71]\n" \
"v_permlane32_swap_b32_e32 v193, v225\n" \
"v_permlane32_swap_b32_e32 v209, v241\n" \
"v_mul_f32_e32 v102, s70, v102\n" \
"v_mul_f32_e32 v103, s70, v103\n" \
C_"v100, v100, v101\n" \
"v_mul_f32_e32 v110, s70, v110\n" \
"v_mul_f32_e32 v111, s70, v111\n" \
C_"v108, v108, v109\n" \
"v_pk_mul_f32 v[118:119], v[118:119], s[70:71]\n" \
C_"v116, v116, v117\n" \
"v_pk_mul_f32 v[126:127], v[126:127], s[70:71]\n" \
C_"v124, v124, v125\n" \
"v_pk_mul_f32 v[134:135], v[134:135], s[70:71]\n" \
C_"v132, v132, v133\n" \
"v_pk_mul_f32 v[142:143], v[142:143], s[70:71]\n" \
C_"v140, v140, v141\n" \
"v_pk_mul_f32 v[150:151], v[150:151], s[70:71]\n" \
C_"v148, v148, v149\n" \
"v_pk_mul_f32 v[158:159], v[158:159], s[70:71]\n" \
C_"v156, v156, v157\n" \
"v_fma_f32 v192, v192, s72, -v193\n" \
"v_fma_f32 v194, v194, s72, -v193\n" \
"v_fma_f32 v208, v208, s72, -v209\n" \
"v_fma_f32 v210, v210, s72, -v209\n" \
"v_mul_f32_e32 v104, s70, v104\n" \
"v_mul_f32_e32 v105, s70, v105\n" \
"v_mul_f32_e32 v106, s70, v106\n" \
"v_mul_f32_e32 v107, s70, v107\n" \
C_"v101, v102, v103\n" \
"v_mul_f32_e32 v112, s70, v112\n" \
"v_mul_f32_e32 v113, s70, v113\n" \
"v_mul_f32_e32 v114, s70, v114\n" \
"v_mul_f32_e32 v115, s70, v115\n" \
C_"v109, v110, v111\n" \
"v_pk_mul_f32 v[120:121], v[120:121], s[70:71]\n" \
"v_pk_mul_f32 v[122:123], v[122:123], s[70:71]\n" \
C_"v117, v118, v119\n" \
"v_pk_mul_f32 v[128:129], v[128:129], s[70:71]\n" \
"v_pk_mul_f32 v[130:131], v[130:131], s[70:71]\n" \
C_"v125, v126, v127\n" \
"v_pk_mul_f32 v[136:137], v[136:137], s[70:71]\n" \
"v_pk_mul_f32 v[138:139], v[138:139], s[70:71]\n" \
C_"v133, v134, v135\n" \
"v_pk_mul_f32 v[144:145], v[144:145], s[70:71]\n" \
"v_pk_mul_f32 v[146:147], v[146:147], s[70:71]\n" \
C_"v141, v142, v143\n" \
"v_pk_mul_f32 v[152:153], v[152:153], s[70:71]\n" \
"v_pk_mul_f32 v[154:155], v[154:155], s[70:71]\n" \
C_"v149, v150, v151\n" \
"v_pk_mul_f32 v[160:161], v[160:161], s[70:71]\n" \
"v_pk_mul_f32 v[162:163], v[162:163], s[70:71]\n" \
C_"v157, v158, v159\n" \
"v_pk_mul_f32 v[176:177], v[176:177], s[70:71]\n" \
"v_pk_mul_f32 v[178:179], v[178:179], s[70:71]\n" \
E_"v192, v192\n" \
E_"v194, v194\n" \
E_"v208, v208\n" \
E_"v210, v210\n" \
"v_fma_f32 v224, v224, s72, -v225\n" \
"v_fma_f32 v226, v226, s72, -v225\n" \
M_"v[172:175], a[184:187], a[156:159], v[172:175]\n" \
C_"v102, v104, v105\n" \
C_"v103, v106, v107\n" \
C_"v110, v112, v113\n" \
C_"v111, v114, v115\n" \
C_"v118, v120, v121\n" \
C_"v119, v122, v123\n" \
C_"v126, v128, v129\n" \
C_"v127, v130, v131\n" \
C_"v134, v136, v137\n" \
C_"v135, v138, v139\n" \
C_"v142, v144, v145\n" \
C_"v143, v146, v147\n" \
C_"v150, v152, v153\n" \
C_"v151, v154, v155\n" \
C_"v158, v160, v161\n" \
C_"v159, v162, v163\n" \
E_"v224, v224\n" \
E_"v226, v226\n" \
"v_fma_f32 v240, v240, s72, -v241\n" \
"v_fma_f32 v242, v242, s72, -v241\n" \
"ds_write_b128 v27, v[100:103] offset:12288\n" \
C_"v192, v192, v194\n" \
C_"v208, v208, v210\n" \
E_"v240, v240\n" \
E_"v242, v242\n" \
C_"v68, v48, v49\n" \
C_"v69, v50, v51\n" \
C_"v70, v196, v197\n" \
C_"v71, v198, v199\n" \
"ds_write_b128 v27, v[108:111] offset:13312\n" \
C_"v72, v212, v213\n" \
C_"v73, v214, v215\n" \
"v_perm_b32 v184, v192, v192, v40\n" \
"v_perm_b32 v185, v192, v192, v41\n" \
"v_perm_b32 v186, v192, v192, v42\n" \
"v_perm_b32 v187, v192, v192, v43\n" \
"v_perm_b32 v200, v208, v208, v40\n" \
"v_perm_b32 v201, v208, v208, v41\n" \
"v_perm_b32 v202, v208, v208, v42\n" \
"v_perm_b32 v203, v208, v208, v43\n" \
"ds_write_b128 v21, v[116:119] offset:16384\n" \
C_"v224, v224, v226\n" \
C_"v74, v228, v229\n" \
C_"v75, v230, v231\n" \
M_"v[32:35], v[68:71], v[184:187], 0\n" \
M_"v[36:39], v[70:73], v[200:203], 0\n" \
"v_perm_b32 v216, v224, v224, v40\n" \
"v_perm_b32 v217, v224, v224, v41\n" \
"ds_write_b128 v21, v[124:127] offset:17408\n" \
"v_perm_b32 v218, v224, v224, v42\n" \
"v_perm_b32 v219, v224, v224, v43\n" \
C_"v240, v240, v242\n" \
M_"v[172:175], a[248:251], a[220:223], v[172:175]\n" \
C_"v76, v244, v245\n" \
C_"v77, v246, v247\n" \
M_"v[248:251], v[72:75], v[216:219], 0\n" \
"v_perm_b32 v232, v240, v240, v40\n" \
"v_perm_b32 v233, v240, v240, v41\n" \
"ds_write_b128 v21, v[132:135] offset:20480\n" \
"v_perm_b32 v234, v240, v240, v42\n" \
"v_perm_b32 v235, v240, v240, v43\n" \
"v_pk_mul_f32 v[172:173], v[172:173], s[70:71]\n" \
"v_pk_mul_f32 v[174:175], v[174:175], s[70:71]\n" \
C_"v172, v172, v173\n" \
C_"v173, v174, v175\n" \
C_"v174, v176, v177\n" \
C_"v175, v178, v179\n" \
M_"v[64:67], v[74:77], v[232:235], 0\n" \
"ds_write_b128 v21, v[140:143] offset:21504\n" \
M_"v[168:171], a[180:183], a[156:159], v[168:171]\n" \
C_"v52, v252, v253\n" \
C_"v53, v254, v255\n" \
M_"v[168:171], a[244:247], a[220:223], v[168:171]\n" \
"v_exp_f32_e64 v45, v45\n" \
M_"v[192:195], v[52:55], v[184:187], 0\n" \
"ds_write_b128 v21, v[148:151] offset:24576\n" \
M_"v[208:211], v[54:57], v[200:203], 0\n" \
M_"v[224:227], v[56:59], v[216:219], 0\n" \
M_"v[240:243], v[58:61], v[232:235], 0\n" \
"ds_write_b128 v21, v[156:159] offset:25600\n" \
"v_mov_b32_dpp v34, v38 quad_perm:[0,1,2,3] row_mask:0x2 bank_mask:0xf\n" \
"v_pk_mul_f32 v[168:169], v[168:169], s[70:71]\n" \
"v_pk_mul_f32 v[170:171], v[170:171], s[70:71]\n" \
"v_mov_b32_dpp v34, v250 quad_perm:[0,1,2,3] row_mask:0x4 bank_mask:0xf\n" \
"v_add_f32_e64 v45, 1.0, v45\n" \
M_"v[164:167], a[176:179], a[156:159], v[164:167]\n" \
"v_mov_b32_dpp v34, v66 quad_perm:[0,1,2,3] row_mask:0x8 bank_mask:0xf\n" \
"v_mov_b32_dpp v32, v36 quad_perm:[0,1,2,3] row_mask:0x2 bank_mask:0xf\n" \
"v_rcp_f32_e64 v45, v45\n" \
"ds_write_b128 v21, v[172:175] offset:29696\n" \
"v_mov_b32_dpp v33, v37 quad_perm:[0,1,2,3] row_mask:0x2 bank_mask:0xf\n" \
"v_rcp_f32_e64 v34, v34\n" \
"v_mov_b32_dpp v32, v248 quad_perm:[0,1,2,3] row_mask:0x4 bank_mask:0xf\n" \
"v_mov_b32_dpp v33, v249 quad_perm:[0,1,2,3] row_mask:0x4 bank_mask:0xf\n" \
"v_mul_f32_e64 v34, v45, v34\n" \
"v_mov_b32_dpp v32, v64 quad_perm:[0,1,2,3] row_mask:0x8 bank_mask:0xf\n" \
"v_mov_b32_dpp v33, v65 quad_perm:[0,1,2,3] row_mask:0x8 bank_mask:0xf\n" \
"v_mul_f32_e32 v32, v34, v32\n" \
"v_mul_f32_e32 v33, v34, v33\n" \
"v_mov_b32_e64 v38, v34\n" \
M_"v[164:167], a[240:243], a[220:223], v[164:167]\n" \
"v_sub_f32_e32 v44, 1.0, v45\n" \
"v_permlane16_swap_b32_e32 v34, v38\n" \
"v_mov_b32_e32 v250, v34\n" \
"v_mov_b32_e32 v66, v38\n" \
C_"v32, v32, v33\n" \
"v_permlane32_swap_b32_e32 v34, v250\n" \
"v_permlane32_swap_b32_e32 v38, v66\n" \
"v_mul_f32_e64 v192, v34, v192\n" \
"v_pk_mul_f32 v[164:165], v[164:165], s[70:71]\n" \
"v_pk_mul_f32 v[166:167], v[166:167], s[70:71]\n" \
C_"v164, v164, v165\n" \
C_"v165, v166, v167\n" \
C_"v166, v168, v169\n" \
C_"v167, v170, v171\n" \
"ds_write_b128 v21, v[164:167] offset:28672\n" \
"v_mul_f32_e32 v193, v34, v193\n" \
"v_mul_f32_e32 v208, v38, v208\n" \
"v_mul_f32_e32 v209, v38, v209\n" \
"v_mul_f32_e32 v224, v250, v224\n" \
"v_mul_f32_e32 v225, v250, v225\n" \
"v_mul_f32_e32 v240, v66, v240\n" \
"v_mul_f32_e32 v241, v66, v241\n" \
"v_mul_f32_e32 v194, v34, v194\n" \
"v_mul_f32_e64 v195, v34, v195\n" \
C_"v192, v192, v193\n" \
"v_mul_f32_e32 v210, v38, v210\n" \
"v_mul_f32_e32 v211, v38, v211\n" \
C_"v208, v208, v209\n" \
"v_mul_f32_e32 v226, v250, v226\n" \
"v_mul_f32_e32 v227, v250, v227\n" \
C_"v224, v224, v225\n" \
"v_mul_f32_e32 v242, v66, v242\n" \
"v_mul_f32_e32 v243, v66, v243\n" \
C_"v240, v240, v241\n" \
C_"v193, v194, v195\n" \
"s_add_u32 s84, s48, 0x0\n" \
"s_nop 0\n" \
C_"v209, v210, v211\n" \
"s_add_u32 s85, s48, 0x24800\n" \
C_"v225, v226, v227\n" \
"s_add_u32 s86, s48, 0x49000\n" \
C_"v241, v242, v243\n" \
"s_add_u32 s87, s48, 0x6d800\n" \
"ds_write_b32 v26, v44\n" \
"buffer_store_dword v32, v25, s[80:83], s49 offen sc1\n" \
"buffer_store_dwordx2 v[192:193], v24, s[80:83], s84 offen sc1\n" \
"buffer_store_dwordx2 v[208:209], v24, s[80:83], s85 offen sc1\n" \
"buffer_store_dwordx2 v[224:225], v24, s[80:83], s86 offen sc1\n" \
"buffer_store_dwordx2 v[240:241], v24, s[80:83], s87 offen sc1\n" \
"s_branch L_end_%=\n" \
"L_poll60_%=:\n" \
"s_mov_b32 s84, 0\n" \
"L_pl60_%=:\n" \
"s_sleep 1\n" \
"v_bfe_u32 v44, v20, 2, 5\n" \
"buffer_load_dword v44, v44, s[80:83], s38 offen offset:128 sc1\n" \
"s_waitcnt vmcnt(0)\n" \
"v_cmp_ne_u32_e32 vcc, s62, v44\n" \
"s_add_u32 s84, s84, 1\n" \
"s_nop 3\n" \
"s_cbranch_vccz L_pollr60_%=\n" \
"s_cmp_gt_u32 s84, 0x400000\n" \
"s_cbranch_scc0 L_pl60_%=\n" \
"s_branch L_pollr60_%=\n" \
"L_poll160_%=:\n" \
"s_mov_b32 s84, 0\n" \
"L_pl160_%=:\n" \
"s_sleep 1\n" \
"v_bfe_u32 v44, v20, 2, 5\n" \
"buffer_load_dword v44, v44, s[80:83], s38 offen offset:0 sc1\n" \
"s_waitcnt vmcnt(0)\n" \
"v_cmp_ne_u32_e32 vcc, s62, v44\n" \
"s_add_u32 s84, s84, 1\n" \
"s_nop 3\n" \
"s_cbranch_vccz L_pollr160_%=\n" \
"s_cmp_gt_u32 s84, 0x400000\n" \
"s_cbranch_scc0 L_pl160_%=\n" \
"s_branch L_pollr160_%=\n" \
".fill (L_pb + 42612 - .) / 4, 4, 0xBF800000\n" \
"L_w1_%=:\n" \
"s_waitcnt vmcnt(19)\n" \
"s_barrier\n" \
X_"a[160:161], v32 offset:0\n" \
X_"a[162:163], v32 offset:1024\n" \
R_"a[128:131], v28 offset:16384\n" \
X_"a[224:225], v32 offset:8192\n" \
X_"a[226:227], v32 offset:9216\n" \
R_"a[192:195], v29 offset:16384\n" \
R_"a[132:135], v28 offset:18432\n" \
R_"a[196:199], v29 offset:18432\n" \
X_"a[164:165], v33 offset:0\n" \
X_"a[166:167], v33 offset:1024\n" \
X_"a[228:229], v33 offset:8192\n" \
X_"a[230:231], v33 offset:9216\n" \
R_"a[136:139], v28 offset:20480\n" \
R_"a[200:203], v29 offset:20480\n" \
"s_waitcnt lgkmcnt(11)\n" \
M_"a[0:3], a[128:131], a[160:163], 0\n" \
X_"a[168:169], v34 offset:0\n" \
"s_lshr_b32 s4, s2, 1\n" \
"s_waitcnt lgkmcnt(9)\n" \
M_"a[0:3], a[192:195], a[224:227], a[0:3]\n" \
X_"a[170:171], v34 offset:1024\n" \
"s_nop 5\n" \
"s_waitcnt lgkmcnt(9)\n" \
M_"a[16:19], a[132:135], a[160:163], 0\n" \
X_"a[232:233], v34 offset:8192\n" \
"s_and_b32 s4, s4, 0x7c\n" \
"s_waitcnt lgkmcnt(9)\n" \
M_"a[16:19], a[196:199], a[224:227], a[16:19]\n" \
X_"a[234:235], v34 offset:9216\n" \
"s_nop 5\n" \
"s_waitcnt lgkmcnt(8)\n" \
M_"a[20:23], a[132:135], a[164:167], 0\n" \
R_"a[140:143], v28 offset:22528\n" \
"v_mov_b32_e32 v213, s13\n" \
"s_waitcnt lgkmcnt(7)\n" \
M_"a[20:23], a[196:199], a[228:231], a[20:23]\n" \
R_"a[204:207], v29 offset:22528\n" \
"s_nop 5\n" \
M_"a[4:7], a[128:131], a[164:167], 0\n" \
X_"a[172:173], v35 offset:0\n" \
"s_or_b32 s4, s4, s3\n" \
M_"a[4:7], a[192:195], a[228:231], a[4:7]\n" \
X_"a[174:175], v35 offset:1024\n" \
"s_nop 5\n" \
"s_waitcnt lgkmcnt(9)\n" \
M_"a[32:35], a[136:139], a[160:163], 0\n" \
X_"a[236:237], v35 offset:8192\n" \
"v_lshrrev_b32_e32 v212, 4, v20\n" \
"s_waitcnt lgkmcnt(9)\n" \
M_"a[32:35], a[200:203], a[224:227], a[32:35]\n" \
X_"a[238:239], v35 offset:9216\n" \
"s_nop 5\n" \
M_"a[36:39], a[136:139], a[164:167], 0\n" \
R_"a[144:147], v28 offset:0\n" \
"v_mad_u64_u32 v[214:215], s[4:5], s4, 36, v[212:213]\n" \
M_"a[36:39], a[200:203], a[228:231], a[36:39]\n" \
R_"a[208:211], v29 offset:0\n" \
"s_nop 5\n" \
"s_waitcnt lgkmcnt(10)\n" \
M_"a[40:43], a[136:139], a[168:171], 0\n" \
R_"a[148:151], v28 offset:2048\n" \
"v_mov_b32_e32 v217, s9\n" \
"s_waitcnt lgkmcnt(9)\n" \
M_"a[40:43], a[200:203], a[232:235], a[40:43]\n" \
R_"a[212:215], v29 offset:2048\n" \
"s_nop 5\n" \
M_"a[24:27], a[132:135], a[168:171], 0\n" \
R_"a[152:155], v28 offset:4096\n" \
"s_movk_i32 s4, 0x240\n" \
M_"a[24:27], a[196:199], a[232:235], a[24:27]\n" \
R_"a[216:219], v29 offset:4096\n" \
"s_nop 5\n" \
M_"a[8:11], a[128:131], a[168:171], 0\n" \
R_"a[156:159], v28 offset:6144\n" \
"v_cmp_gt_u32_e32 vcc, s4, v20\n" \
M_"a[8:11], a[192:195], a[232:235], a[8:11]\n" \
R_"a[220:223], v29 offset:6144\n" \
"s_mov_b32 m0, s32\n" \
"s_nop 5\n" \
"s_waitcnt lgkmcnt(13)\n" \
M_"a[48:51], a[140:143], a[160:163], 0\n" \
"s_mov_b32 exec_lo, 0xff000000\n" \
"s_mov_b32 exec_hi, 0x0\n" \
"global_load_lds_dword v[184:185], off\n" \
"s_mov_b64 exec, -1\n" \
"s_waitcnt lgkmcnt(12)\n" \
M_"a[48:51], a[204:207], a[224:227], a[48:51]\n" \
"s_nop 5\n" \
X_"a[176:177], v36 offset:0\n" \
M_"a[52:55], a[140:143], a[164:167], 0\n" \
X_"a[178:179], v36 offset:1024\n" \
"s_lshl_b32 s4, s2, 3\n" \
M_"a[52:55], a[204:207], a[228:231], a[52:55]\n" \
"s_mov_b32 m0, s32\n" \
"s_nop 5\n" \
M_"a[56:59], a[140:143], a[168:171], 0\n" \
"s_mov_b32 exec_lo, 0xffff\n" \
"s_mov_b32 exec_hi, 0x0\n" \
"global_load_lds_dword v[186:187], off\n" \
"s_mov_b64 exec, -1\n" \
M_"a[56:59], a[204:207], a[232:235], a[56:59]\n" \
"s_nop 5\n" \
X_"a[240:241], v36 offset:8192\n" \
"s_waitcnt lgkmcnt(13)\n" \
M_"a[60:63], a[140:143], a[172:175], 0\n" \
X_"a[242:243], v36 offset:9216\n" \
"s_and_b32 s4, s4, 56\n" \
"s_waitcnt lgkmcnt(12)\n" \
M_"a[60:63], a[204:207], a[236:239], a[60:63]\n" \
"s_nop 5\n" \
X_"a[180:181], v37 offset:0\n" \
M_"a[44:47], a[136:139], a[172:175], 0\n" \
X_"a[182:183], v37 offset:1024\n" \
"v_cndmask_b32_e32 v217, v217, v213, vcc\n" \
M_"a[44:47], a[200:203], a[236:239], a[44:47]\n" \
"s_nop 5\n" \
X_"a[244:245], v37 offset:8192\n" \
M_"a[28:31], a[132:135], a[172:175], 0\n" \
X_"a[246:247], v37 offset:9216\n" \
"v_lshlrev_b32_e32 v213, 3, v214\n" \
M_"a[28:31], a[196:199], a[236:239], a[28:31]\n" \
"s_nop 5\n" \
X_"a[184:185], v38 offset:0\n" \
M_"a[12:15], a[128:131], a[172:175], 0\n" \
X_"a[186:187], v38 offset:1024\n" \
"v_and_b32_e32 v213, 0x1ffc0, v213\n" \
M_"a[12:15], a[192:195], a[236:239], a[12:15]\n" \
"s_nop 5\n" \
"v_and_b32_e32 v214, 7, v214\n" \
"s_waitcnt lgkmcnt(15)\n" \
M_"a[76:79], a[144:147], a[172:175], 0\n" \
X_"a[248:249], v38 offset:8192\n" \
"v_or3_b32 v213, v214, s4, v213\n" \
M_"a[76:79], a[208:211], a[236:239], a[76:79]\n" \
"s_nop 5\n" \
"s_lshl_b32 s4, s2, 2\n" \
M_"a[72:75], a[144:147], a[168:171], 0\n" \
"s_or_b32 s4, s4, s3\n" \
"s_mul_i32 s4, s4, 18\n" \
M_"a[72:75], a[208:211], a[232:235], a[72:75]\n" \
"s_nop 5\n" \
X_"a[250:251], v38 offset:9216\n" \
M_"a[68:71], a[144:147], a[164:167], 0\n" \
"v_min_u32_e32 v212, 53, v212\n" \
"s_sub_i32 s4, s4, 36\n" \
M_"a[68:71], a[208:211], a[228:231], a[68:71]\n" \
"s_nop 5\n" \
"v_add_u32_e64 v212, s4, v212\n" \
M_"a[64:67], a[144:147], a[160:163], 0\n" \
"v_mov_b32_e32 v215, s12\n" \
"v_mov_b32_e32 v216, s8\n" \
M_"a[64:67], a[208:211], a[224:227], a[64:67]\n" \
"s_nop 5\n" \
X_"a[188:189], v39 offset:0\n" \
M_"a[80:83], a[148:151], a[160:163], 0\n" \
"v_cndmask_b32_e32 v212, v212, v213, vcc\n" \
"v_cndmask_b32_e32 v216, v216, v215, vcc\n" \
"s_waitcnt lgkmcnt(15)\n" \
M_"a[80:83], a[212:215], a[224:227], a[80:83]\n" \
"s_nop 5\n" \
"v_lshlrev_b32_e32 v212, 7, v212\n" \
M_"a[84:87], a[148:151], a[164:167], 0\n" \
X_"a[190:191], v39 offset:1024\n" \
"v_mov_b32_e64 v213, 0\n" \
M_"a[84:87], a[212:215], a[228:231], a[84:87]\n" \
"s_nop 5\n" \
"v_lshl_add_u64 v[188:189], v[216:217], 0, v[212:213]\n" \
M_"a[88:91], a[148:151], a[168:171], 0\n" \
"s_movk_i32 s5, 0xd80\n" \
"s_lshr_b32 s6, s2, 5\n" \
M_"a[88:91], a[212:215], a[232:235], a[88:91]\n" \
"s_nop 5\n" \
X_"a[252:253], v39 offset:8192\n" \
M_"a[92:95], a[148:151], a[172:175], 0\n" \
"v_lshrrev_b32_e64 v212, 4, v20\n" \
"v_lshl_or_b32 v215, s3, 6, v212\n" \
M_"a[92:95], a[212:215], a[236:239], a[92:95]\n" \
"s_nop 5\n" \
"v_mul_u32_u24_e32 v216, 0x2aab, v215\n" \
M_"a[108:111], a[152:155], a[172:175], 0\n" \
X_"a[254:255], v39 offset:9216\n" \
"v_lshrrev_b32_e32 v216, 16, v216\n" \
M_"a[108:111], a[216:219], a[236:239], a[108:111]\n" \
"s_nop 5\n" \
"v_mad_i32_i24 v217, v216, -6, v215\n" \
M_"a[104:107], a[152:155], a[168:171], 0\n" \
"v_add_u32_e32 v218, 0xfffffe50, v215\n" \
"s_mul_i32 s35, s6, 0x48\n" \
M_"a[104:107], a[216:219], a[232:235], a[104:107]\n" \
"s_nop 5\n" \
"v_lshrrev_b32_e64 v219, 1, v217\n" \
M_"a[100:103], a[152:155], a[164:167], 0\n" \
"v_add_u32_e64 v218, s35, v216\n" \
"v_mul_u32_u24_e32 v219, 0x480, v219\n" \
M_"a[100:103], a[216:219], a[228:231], a[100:103]\n" \
"s_nop 5\n" \
"v_and_b32_e32 v214, 16, v20\n" \
M_"a[96:99], a[152:155], a[160:163], 0\n" \
"v_mad_u32_u24 v219, v218, s5, v219\n" \
"v_or_b32_e64 v192, v219, v214\n" \
M_"a[96:99], a[216:219], a[224:227], a[96:99]\n" \
"s_nop 5\n" \
"v_mov_b32_e32 v220, 2.0\n" \
"s_waitcnt lgkmcnt(15)\n" \
M_"a[112:115], a[156:159], a[160:163], 0\n" \
"v_mov_b32_e32 v222, 7\n" \
"v_lshlrev_b32_e64 v220, 7, v218\n" \
M_"a[112:115], a[220:223], a[224:227], a[112:115]\n" \
"s_nop 5\n" \
"v_lshrrev_b32_e32 v218, 1, v218\n" \
M_"a[116:119], a[156:159], a[164:167], 0\n" \
"v_and_b32_e32 v218, 4, v218\n" \
"v_and_b32_e32 v216, 2, v216\n" \
M_"a[116:119], a[220:223], a[228:231], a[116:119]\n" \
"s_nop 5\n" \
"s_mul_i32 s7, s6, 0x2400\n" \
M_"a[120:123], a[156:159], a[168:171], 0\n" \
"s_sub_i32 s34, 0x22000, s7\n" \
"v_bitop3_b32 v216, v217, v218, v216 bitop3:0x1e\n" \
M_"a[120:123], a[220:223], a[232:235], a[120:123]\n" \
"s_nop 5\n" \
"v_or_b32_e32 v213, 0x24400, v20\n" \
M_"a[124:127], a[156:159], a[172:175], 0\n" \
"v_lshlrev_b32_e64 v216, 4, v216\n" \
"v_add3_u32 v196, v220, s34, v216\n" \
"s_waitcnt vmcnt(5) lgkmcnt(0)\n" \
M_"a[124:127], a[220:223], a[236:239], a[124:127]\n" \
"s_barrier\n" \
M_"v[52:55], a[128:131], a[176:179], 0\n" \
"s_add_u32 m0, s30, 0x0\n" \
"s_movk_i32 s84, 0x100\n" \
M_"v[52:55], a[192:195], a[240:243], v[52:55]\n" \
B_"v22, s[64:67], s84 offen offset:0 lds\n" \
"s_nop 5\n" \
M_"v[56:59], a[128:131], a[180:183], 0\n" \
X_"a[160:161], v32 offset:32768\n" \
"v_mov_b32_e32 v216, v213\n" \
M_"v[56:59], a[192:195], a[244:247], v[56:59]\n" \
"s_movk_i32 s84, 0x1d00\n" \
"s_nop 5\n" \
M_"v[60:63], a[128:131], a[184:187], 0\n" \
B_"v23, s[64:67], s84 offen offset:1024 lds\n" \
M_"v[60:63], a[192:195], a[248:251], v[60:63]\n" \
"s_nop 5\n" \
X_"a[162:163], v32 offset:33792\n" \
M_"v[64:67], a[128:131], a[188:191], 0\n" \
X_"a[224:225], v32 offset:40960\n" \
"s_movk_i32 s36, 0x1b0\n" \
M_"v[64:67], a[192:195], a[252:255], v[64:67]\n" \
R_"a[128:131], v28 offset:49152\n" \
"s_nop 5\n" \
M_"v[80:83], a[132:135], a[188:191], 0\n" \
R_"a[192:195], v29 offset:49152\n" \
"v_or_b32_e32 v216, 0x100, v215\n" \
M_"v[80:83], a[196:199], a[252:255], v[80:83]\n" \
"s_movk_i32 s84, 0x3900\n" \
"s_nop 5\n" \
M_"v[76:79], a[132:135], a[184:187], 0\n" \
B_"v22, s[64:67], s84 offen offset:2048 lds\n" \
M_"v[76:79], a[196:199], a[248:251], v[76:79]\n" \
"s_movk_i32 s84, 0x5500\n" \
"s_nop 5\n" \
M_"v[72:75], a[132:135], a[180:183], 0\n" \
B_"v23, s[64:67], s84 offen offset:3072 lds\n" \
M_"v[72:75], a[196:199], a[244:247], v[72:75]\n" \
"s_add_u32 m0, s31, 0x0\n" \
"s_mov_b32 s84, 0x100000\n" \
"s_nop 5\n" \
M_"v[68:71], a[132:135], a[176:179], 0\n" \
B_"v26, s[68:71], s84 offen offset:0 lds\n" \
M_"v[68:71], a[196:199], a[240:243], v[68:71]\n" \
R_"a[132:135], v28 offset:51200\n" \
"s_nop 5\n" \
M_"v[84:87], a[136:139], a[176:179], 0\n" \
R_"a[196:199], v29 offset:51200\n" \
"v_mul_u32_u24_e32 v217, 0x2aab, v216\n" \
M_"v[84:87], a[200:203], a[240:243], v[84:87]\n" \
"s_addk_i32 s84, 0x7c00\n" \
"s_nop 5\n" \
M_"v[88:91], a[136:139], a[180:183], 0\n" \
B_"v26, s[68:71], s84 offen offset:1024 lds\n" \
M_"v[88:91], a[200:203], a[244:247], v[88:91]\n" \
"s_nop 5\n" \
X_"a[226:227], v32 offset:41984\n" \
M_"v[92:95], a[136:139], a[184:187], 0\n" \
"s_addk_i32 s84, 0x7c00\n" \
M_"v[92:95], a[200:203], a[248:251], v[92:95]\n" \
B_"v27, s[68:71], s84 offen offset:2048 lds\n" \
"s_nop 5\n" \
M_"v[96:99], a[136:139], a[188:191], 0\n" \
X_"a[164:165], v33 offset:32768\n" \
"v_add_u32_e32 v215, 0xffffff50, v215\n" \
M_"v[96:99], a[200:203], a[252:255], v[96:99]\n" \
R_"a[136:139], v28 offset:53248\n" \
"s_nop 5\n" \
M_"v[112:115], a[140:143], a[188:191], 0\n" \
R_"a[200:203], v29 offset:53248\n" \
"v_lshrrev_b32_e64 v217, 16, v217\n" \
M_"v[112:115], a[204:207], a[252:255], v[112:115]\n" \
"s_addk_i32 s84, 0x7c00\n" \
"s_nop 5\n" \
M_"v[108:111], a[140:143], a[184:187], 0\n" \
B_"v27, s[68:71], s84 offen offset:3072 lds\n" \
M_"v[108:111], a[204:207], a[248:251], v[108:111]\n" \
"s_add_u32 m0, s30, 0x1000\n" \
"s_mov_b32 s84, 0x8100\n" \
"s_nop 5\n" \
M_"v[104:107], a[140:143], a[180:183], 0\n" \
B_"v22, s[64:67], s84 offen offset:0 lds\n" \
M_"v[104:107], a[204:207], a[244:247], v[104:107]\n" \
"s_nop 5\n" \
X_"a[166:167], v33 offset:33792\n" \
M_"v[100:103], a[140:143], a[176:179], 0\n" \
X_"a[228:229], v33 offset:40960\n" \
"v_mov_b32_e64 v220, v215\n" \
M_"v[100:103], a[204:207], a[240:243], v[100:103]\n" \
R_"a[140:143], v28 offset:55296\n" \
"s_nop 5\n" \
M_"v[116:119], a[144:147], a[176:179], 0\n" \
R_"a[204:207], v29 offset:55296\n" \
"v_mad_i32_i24 v218, v217, -6, v216\n" \
M_"v[116:119], a[208:211], a[240:243], v[116:119]\n" \
"s_addk_i32 s84, 0x1c00\n" \
"s_nop 5\n" \
M_"v[120:123], a[144:147], a[180:183], 0\n" \
B_"v23, s[64:67], s84 offen offset:1024 lds\n" \
".fill (L_pb + 44768 - .) / 4, 4, 0xBF800000\n" \
"L_pp11:\n" \
"s_cbranch_execz L_pret\n" \
M_"v[120:123], a[208:211], a[244:247], v[120:123]\n" \
"s_addk_i32 s84, 0x1c00\n" \
"s_nop 5\n" \
M_"v[124:127], a[144:147], a[184:187], 0\n" \
B_"v22, s[64:67], s84 offen offset:2048 lds\n" \
M_"v[124:127], a[208:211], a[248:251], v[124:127]\n" \
"s_addk_i32 s84, 0x1c00\n" \
"s_nop 5\n" \
M_"v[128:131], a[144:147], a[188:191], 0\n" \
B_"v23, s[64:67], s84 offen offset:3072 lds\n" \
M_"v[128:131], a[208:211], a[252:255], v[128:131]\n" \
R_"a[144:147], v28 offset:32768\n" \
"s_nop 5\n" \
M_"v[144:147], a[148:151], a[188:191], 0\n" \
R_"a[208:211], v29 offset:32768\n" \
"v_cmp_gt_u32_e64 vcc, s36, v216\n" \
M_"v[144:147], a[212:215], a[252:255], v[144:147]\n" \
"s_add_u32 m0, s31, 0x4000\n" \
"s_mov_b32 s84, 0x100000\n" \
"s_nop 5\n" \
M_"v[140:143], a[148:151], a[184:187], 0\n" \
B_"v26, s[72:75], s84 offen offset:0 lds\n" \
M_"v[140:143], a[212:215], a[248:251], v[140:143]\n" \
"s_nop 5\n" \
X_"a[230:231], v33 offset:41984\n" \
M_"v[136:139], a[148:151], a[180:183], 0\n" \
"s_addk_i32 s84, 0x7c00\n" \
M_"v[136:139], a[212:215], a[244:247], v[136:139]\n" \
B_"v26, s[72:75], s84 offen offset:1024 lds\n" \
"s_nop 5\n" \
M_"v[132:135], a[148:151], a[176:179], 0\n" \
X_"a[168:169], v34 offset:32768\n" \
"v_mov_b32_e64 v221, 7\n" \
M_"v[132:135], a[212:215], a[240:243], v[132:135]\n" \
R_"a[148:151], v28 offset:34816\n" \
"s_nop 5\n" \
M_"v[148:151], a[152:155], a[176:179], 0\n" \
R_"a[212:215], v29 offset:34816\n" \
"v_lshrrev_b32_e32 v222, 1, v218\n" \
M_"v[148:151], a[216:219], a[240:243], v[148:151]\n" \
"s_nop 5\n" \
X_"a[170:171], v34 offset:33792\n" \
M_"v[152:155], a[152:155], a[180:183], 0\n" \
X_"a[232:233], v34 offset:40960\n" \
"v_cndmask_b32_e32 v217, v220, v217, vcc\n" \
M_"v[152:155], a[216:219], a[244:247], v[152:155]\n" \
"s_nop 5\n" \
X_"a[234:235], v34 offset:41984\n" \
M_"v[156:159], a[152:155], a[184:187], 0\n" \
X_"a[172:173], v35 offset:32768\n" \
"s_movk_i32 s4, 0x48\n" \
M_"v[156:159], a[216:219], a[248:251], v[156:159]\n" \
"s_nop 5\n" \
X_"a[174:175], v35 offset:33792\n" \
M_"v[160:163], a[152:155], a[188:191], 0\n" \
X_"a[236:237], v35 offset:40960\n" \
"v_add_u32_e32 v220, s35, v217\n" \
M_"v[160:163], a[216:219], a[252:255], v[160:163]\n" \
R_"a[152:155], v28 offset:36864\n" \
"s_nop 5\n" \
M_"v[176:179], a[156:159], a[188:191], 0\n" \
R_"a[216:219], v29 offset:36864\n" \
"v_mul_u32_u24_e32 v222, 0x480, v222\n" \
M_"v[176:179], a[220:223], a[252:255], v[176:179]\n" \
"s_nop 5\n" \
X_"a[238:239], v35 offset:41984\n" \
M_"v[172:175], a[156:159], a[184:187], 0\n" \
"v_mad_u32_u24 v222, v220, s5, v222\n" \
"v_cmp_gt_u32_e64 s[4:5], s4, v215\n" \
M_"v[172:175], a[220:223], a[248:251], v[172:175]\n" \
"s_nop 5\n" \
"v_cndmask_b32_e32 v215, v221, v218, vcc\n" \
M_"v[168:171], a[156:159], a[180:183], 0\n" \
"v_lshrrev_b32_e32 v218, 1, v220\n" \
"v_or_b32_e32 v214, v222, v214\n" \
M_"v[168:171], a[220:223], a[244:247], v[168:171]\n" \
"s_nop 5\n" \
"v_mov_b32_e32 v222, 2.0\n" \
M_"v[164:167], a[156:159], a[176:179], 0\n" \
"v_and_b32_e32 v218, 4, v218\n" \
"v_and_b32_e32 v217, 2, v217\n" \
M_"v[164:167], a[220:223], a[240:243], v[164:167]\n" \
R_"a[156:159], v28 offset:38912\n" \
R_"a[220:223], v29 offset:38912\n" \
"s_waitcnt lgkmcnt(15)\n" \
M_"a[0:3], a[128:131], a[160:163], a[0:3]\n" \
"s_addk_i32 s84, 0x7c00\n" \
M_"a[0:3], a[192:195], a[224:227], a[0:3]\n" \
B_"v27, s[72:75], s84 offen offset:2048 lds\n" \
M_"a[4:7], a[128:131], a[164:167], a[4:7]\n" \
X_"a[176:177], v36 offset:32768\n" \
"v_bitop3_b32 v215, v215, v218, v217 bitop3:0x1e\n" \
"s_waitcnt lgkmcnt(15)\n" \
M_"a[4:7], a[192:195], a[228:231], a[4:7]\n" \
"s_addk_i32 s84, 0x7c00\n" \
"s_waitcnt lgkmcnt(11)\n" \
M_"a[8:11], a[128:131], a[168:171], a[8:11]\n" \
B_"v27, s[72:75], s84 offen offset:3072 lds\n" \
"s_waitcnt lgkmcnt(9)\n" \
M_"a[8:11], a[192:195], a[232:235], a[8:11]\n" \
X_"a[178:179], v36 offset:33792\n" \
"v_lshlrev_b32_e32 v223, 1, v220\n" \
"s_waitcnt lgkmcnt(8)\n" \
M_"a[12:15], a[128:131], a[172:175], a[12:15]\n" \
X_"a[240:241], v36 offset:40960\n" \
"v_mov_b32_e32 v224, 2.0\n" \
"s_waitcnt lgkmcnt(5)\n" \
M_"a[12:15], a[192:195], a[236:239], a[12:15]\n" \
"v_cndmask_b32_e32 v193, v222, v214, vcc\n" \
"v_lshlrev_b32_e32 v214, 7, v220\n" \
M_"a[28:31], a[132:135], a[172:175], a[28:31]\n" \
"v_lshlrev_b32_e32 v215, 4, v215\n" \
"s_mul_i32 s39, s3, 18\n" \
M_"a[28:31], a[196:199], a[236:239], a[28:31]\n" \
X_"a[242:243], v36 offset:41984\n" \
"v_add3_u32 v214, v214, s34, v215\n" \
M_"a[24:27], a[132:135], a[168:171], a[24:27]\n" \
"v_cndmask_b32_e64 v195, v224, v223, s[4:5]\n" \
"s_movk_i32 s4, 0x1f8\n" \
M_"a[24:27], a[196:199], a[232:235], a[24:27]\n" \
"v_mov_b32_e32 v215, v213\n" \
"s_add_i32 s35, s35, s39\n" \
M_"a[20:23], a[132:135], a[164:167], a[20:23]\n" \
"v_cmp_gt_u32_e32 vcc, s4, v216\n" \
"v_add_u32_e64 v212, s35, v212\n" \
M_"a[20:23], a[196:199], a[228:231], a[20:23]\n" \
X_"a[180:181], v37 offset:32768\n" \
"v_mov_b32_e64 v219, 2.0\n" \
M_"a[16:19], a[132:135], a[160:163], a[16:19]\n" \
"v_cndmask_b32_e32 v197, v215, v214, vcc\n" \
"s_movk_i32 s35, 0x120\n" \
M_"a[16:19], a[196:199], a[224:227], a[16:19]\n" \
"v_mul_u32_u24_e32 v214, 0xd80, v212\n" \
"v_cmp_gt_u32_e64 vcc, s35, v20\n" \
M_"a[32:35], a[136:139], a[160:163], a[32:35]\n" \
"s_mov_b32 m0, s32\n" \
X_"a[182:183], v37 offset:33792\n" \
M_"a[32:35], a[200:203], a[224:227], a[32:35]\n" \
"s_mov_b32 exec_lo, 0x0\n" \
"s_mov_b32 exec_hi, 0xff\n" \
"global_load_lds_dword v[184:185], off\n" \
"s_mov_b64 exec, -1\n" \
M_"a[36:39], a[136:139], a[164:167], a[36:39]\n" \
"v_lshrrev_b32_e32 v215, 1, v212\n" \
"s_and_b32 s4, s2, 7\n" \
M_"a[36:39], a[200:203], a[228:231], a[36:39]\n" \
X_"a[244:245], v37 offset:40960\n" \
"v_cndmask_b32_e32 v194, v219, v214, vcc\n" \
M_"a[40:43], a[136:139], a[168:171], a[40:43]\n" \
"v_lshlrev_b32_e32 v214, 7, v212\n" \
"v_lshrrev_b32_e32 v212, 2, v212\n" \
M_"a[40:43], a[200:203], a[232:235], a[40:43]\n" \
"s_mov_b32 m0, s32\n" \
"v_and_b32_e32 v212, 2, v212\n" \
M_"a[44:47], a[136:139], a[172:175], a[44:47]\n" \
"s_mov_b32 exec_lo, 0xffff0000\n" \
"s_mov_b32 exec_hi, 0x0\n" \
"global_load_lds_dword v[186:187], off\n" \
"s_mov_b64 exec, -1\n" \
M_"a[44:47], a[200:203], a[236:239], a[44:47]\n" \
X_"a[246:247], v37 offset:41984\n" \
"v_and_or_b32 v212, v215, 1, v212\n" \
M_"a[60:63], a[140:143], a[172:175], a[60:63]\n" \
"s_lshl_b32 s5, s4, 2\n" \
"s_bfe_u32 s38, s2, 0x20003\n" \
M_"a[60:63], a[204:207], a[236:239], a[60:63]\n" \
"v_lshlrev_b32_e32 v212, 5, v212\n" \
"s_movk_i32 s35, 0x60\n" \
M_"a[56:59], a[140:143], a[168:171], a[56:59]\n" \
X_"a[184:185], v38 offset:32768\n" \
"s_or_b32 s5, s5, s38\n" \
M_"a[56:59], a[204:207], a[232:235], a[56:59]\n" \
"v_bitop3_b32 v212, v212, s35, v214 bitop3:0x36\n" \
"s_lshr_b32 s36, s2, 3\n" \
M_"a[52:55], a[140:143], a[164:167], a[52:55]\n" \
"v_add_u32_e32 v212, s34, v212\n" \
"s_mul_i32 s34, s5, 0x12000\n" \
M_"a[52:55], a[204:207], a[228:231], a[52:55]\n" \
X_"a[186:187], v38 offset:33792\n" \
"s_or_b32 s40, s34, 0x6000000\n" \
M_"a[48:51], a[140:143], a[160:163], a[48:51]\n" \
"s_mul_i32 s34, s36, 0x90000\n" \
"s_lshl_b32 s36, s3, 14\n" \
M_"a[48:51], a[204:207], a[224:227], a[48:51]\n" \
"s_add_i32 s34, s34, s36\n" \
"s_lshl_b32 s38, s5, 8\n" \
M_"a[64:67], a[144:147], a[160:163], a[64:67]\n" \
X_"a[248:249], v38 offset:40960\n" \
"s_add_i32 s41, s34, 0x4240000\n" \
M_"a[64:67], a[208:211], a[224:227], a[64:67]\n" \
"s_mul_i32 s34, s5, 36\n" \
"s_add_i32 s5, s7, s40\n" \
M_"a[68:71], a[144:147], a[164:167], a[68:71]\n" \
"s_addk_i32 s5, 0x2000\n" \
"s_mov_b32 s37, 2.0\n" \
M_"a[68:71], a[208:211], a[228:231], a[68:71]\n" \
X_"a[250:251], v38 offset:41984\n" \
"s_or_b32 s38, s38, 0x5be8000\n" \
M_"a[72:75], a[144:147], a[168:171], a[72:75]\n" \
"s_cmp_lg_u32 s3, 0\n" \
"s_cselect_b32 s36, s37, s5\n" \
M_"a[72:75], a[208:211], a[232:235], a[72:75]\n" \
"s_lshl_b32 s5, s6, 2\n" \
"s_or_b32 s5, s38, s5\n" \
M_"a[76:79], a[144:147], a[172:175], a[76:79]\n" \
"s_lshl_b32 s35, s3, 10\n" \
M_"a[76:79], a[208:211], a[236:239], a[76:79]\n" \
X_"a[188:189], v39 offset:32768\n" \
M_"a[92:95], a[148:151], a[172:175], a[92:95]\n" \
"s_cmpk_lt_u32 s3, 3\n" \
"s_cselect_b32 s37, s37, s5\n" \
M_"a[92:95], a[212:215], a[236:239], a[92:95]\n" \
"s_lshl_b32 s5, s4, 16\n" \
"s_lshl_b32 s4, s4, 11\n" \
M_"a[88:91], a[148:151], a[168:171], a[88:91]\n" \
X_"a[190:191], v39 offset:33792\n" \
"v_cndmask_b32_e64 v198, v213, v212, vcc\n" \
M_"a[88:91], a[212:215], a[232:235], a[88:91]\n" \
"v_or_b32_e32 v212, s35, v20\n" \
"s_or_b32 s39, s40, s35\n" \
M_"a[84:87], a[148:151], a[164:167], a[84:87]\n" \
"s_or_b32 s55, s41, s4\n" \
"v_or_b32_e32 v199, 0x22000, v212\n" \
M_"a[84:87], a[212:215], a[228:231], a[84:87]\n" \
X_"a[252:253], v39 offset:40960\n" \
"s_add_i32 s35, s39, s7\n" \
M_"a[80:83], a[148:151], a[160:163], a[80:83]\n" \
"s_add_i32 s54, s41, s5\n" \
"s_add_i32 s55, s55, 0x80000\n" \
M_"a[80:83], a[212:215], a[224:227], a[80:83]\n" \
"v_mov_b32_e32 v215, 0xffff\n" \
"s_and_b32 s4, s3, 1\n" \
M_"a[96:99], a[152:155], a[160:163], a[96:99]\n" \
X_"a[254:255], v39 offset:41984\n" \
"s_lshl_b32 s5, s4, 4\n" \
M_"a[96:99], a[216:219], a[224:227], a[96:99]\n" \
"v_lshrrev_b32_e32 v212, 4, v20\n" \
"v_lshrrev_b32_e64 v218, 6, v20\n" \
M_"a[100:103], a[152:155], a[164:167], a[100:103]\n" \
"v_and_b32_e64 v219, 12, v218\n" \
"v_and_or_b32 v214, v212, 15, s5\n" \
M_"a[100:103], a[216:219], a[228:231], a[100:103]\n" \
"v_cmp_gt_u32_e64 vcc, v219, v214\n" \
"v_mov_b32_e32 v220, 0xffff0000\n" \
M_"a[104:107], a[152:155], a[168:171], a[104:107]\n" \
"s_lshl_b32 s5, s2, 2\n" \
"v_cndmask_b32_e64 v217, v215, 0, vcc\n" \
M_"a[104:107], a[216:219], a[232:235], a[104:107]\n" \
"v_cmp_lt_u32_e32 vcc, v219, v214\n" \
"v_mov_b32_e64 v213, s23\n" \
M_"a[108:111], a[152:155], a[172:175], a[108:111]\n" \
"s_or_b32 s5, s5, s3\n" \
"v_cndmask_b32_e32 v221, 0, v220, vcc\n" \
M_"a[108:111], a[216:219], a[236:239], a[108:111]\n" \
"v_or_b32_e32 v12, v217, v221\n" \
"v_or_b32_e32 v217, 2, v219\n" \
"s_waitcnt lgkmcnt(15)\n" \
M_"a[124:127], a[156:159], a[172:175], a[124:127]\n" \
"v_cmp_gt_u32_e32 vcc, v217, v214\n" \
"v_mov_b32_e64 v216, s25\n" \
M_"a[124:127], a[220:223], a[236:239], a[124:127]\n" \
"s_movk_i32 s40, 0xf400\n" \
"v_cndmask_b32_e64 v221, v215, 0, vcc\n" \
M_"a[120:123], a[156:159], a[168:171], a[120:123]\n" \
"v_cmp_lt_u32_e32 vcc, v217, v214\n" \
"s_lshl_b32 s4, s4, 6\n" \
M_"a[120:123], a[220:223], a[232:235], a[120:123]\n" \
"v_cndmask_b32_e32 v217, 0, v220, vcc\n" \
"v_or_b32_e64 v13, v221, v217\n" \
M_"a[116:119], a[156:159], a[164:167], a[116:119]\n" \
"v_or_b32_e32 v217, 16, v219\n" \
"v_cmp_gt_u32_e32 vcc, v217, v214\n" \
M_"a[116:119], a[220:223], a[228:231], a[116:119]\n" \
"s_nop 0\n" \
"v_cndmask_b32_e64 v221, v215, 0, vcc\n" \
"v_cmp_lt_u32_e32 vcc, v217, v214\n" \
M_"a[112:115], a[156:159], a[160:163], a[112:115]\n" \
"s_nop 0\n" \
"v_cndmask_b32_e32 v217, 0, v220, vcc\n" \
"v_or_b32_e64 v14, v221, v217\n" \
M_"a[112:115], a[220:223], a[224:227], a[112:115]\n" \
"s_waitcnt vmcnt(20)\n" \
"v_readfirstlane_b32 s62, v180\n" \
"s_nop 3\n" \
"s_add_u32 s62, s62, 0x9E3779B1\n" \
"s_waitcnt vmcnt(2) lgkmcnt(0)\n" \
"s_barrier\n" \
M_"v[52:55], a[128:131], a[176:179], v[52:55]\n" \
"s_add_u32 m0, s30, 0x8000\n" \
"s_movk_i32 s84, 0x180\n" \
M_"v[52:55], a[192:195], a[240:243], v[52:55]\n" \
B_"v22, s[64:67], s84 offen offset:0 lds\n" \
M_"v[56:59], a[128:131], a[180:183], v[56:59]\n" \
X_"a[160:161], v32 offset:0\n" \
"v_or_b32_e64 v217, 18, v219\n" \
M_"v[56:59], a[192:195], a[244:247], v[56:59]\n" \
"s_movk_i32 s84, 0x1d80\n" \
M_"v[60:63], a[128:131], a[184:187], v[60:63]\n" \
B_"v23, s[64:67], s84 offen offset:1024 lds\n" \
M_"v[60:63], a[192:195], a[248:251], v[60:63]\n" \
X_"a[162:163], v32 offset:1024\n" \
"v_cmp_gt_u32_e32 vcc, v217, v214\n" \
M_"v[64:67], a[128:131], a[188:191], v[64:67]\n" \
X_"a[224:225], v32 offset:8192\n" \
"v_cndmask_b32_e64 v215, v215, 0, vcc\n" \
M_"v[64:67], a[192:195], a[252:255], v[64:67]\n" \
R_"a[128:131], v28 offset:16384\n" \
"v_cmp_lt_u32_e64 vcc, v217, v214\n" \
M_"v[80:83], a[132:135], a[188:191], v[80:83]\n" \
R_"a[192:195], v29 offset:16384\n" \
"v_cndmask_b32_e64 v214, 0, v220, vcc\n" \
M_"v[80:83], a[196:199], a[252:255], v[80:83]\n" \
"s_movk_i32 s84, 0x3980\n" \
M_"v[76:79], a[132:135], a[184:187], v[76:79]\n" \
B_"v22, s[64:67], s84 offen offset:2048 lds\n" \
M_"v[76:79], a[196:199], a[248:251], v[76:79]\n" \
"s_movk_i32 s84, 0x5580\n" \
M_"v[72:75], a[132:135], a[180:183], v[72:75]\n" \
B_"v23, s[64:67], s84 offen offset:3072 lds\n" \
M_"v[72:75], a[196:199], a[244:247], v[72:75]\n" \
"s_add_u32 m0, s31, 0x8000\n" \
"s_mov_b32 s84, 0x180000\n" \
M_"v[68:71], a[132:135], a[176:179], v[68:71]\n" \
B_"v26, s[68:71], s84 offen offset:0 lds\n" \
M_"v[68:71], a[196:199], a[240:243], v[68:71]\n" \
R_"a[132:135], v28 offset:18432\n" \
"v_or_b32_e64 v15, v215, v214\n" \
M_"v[84:87], a[136:139], a[176:179], v[84:87]\n" \
R_"a[196:199], v29 offset:18432\n" \
"v_mad_u64_u32 v[214:215], s[6:7], s5, 42, v[212:213]\n" \
M_"v[84:87], a[200:203], a[240:243], v[84:87]\n" \
"s_addk_i32 s84, 0x7c00\n" \
M_"v[88:91], a[136:139], a[180:183], v[88:91]\n" \
B_"v26, s[68:71], s84 offen offset:1024 lds\n" \
M_"v[88:91], a[200:203], a[244:247], v[88:91]\n" \
X_"a[226:227], v32 offset:9216\n" \
"s_movk_i32 s5, 0x1440\n" \
M_"v[92:95], a[136:139], a[184:187], v[92:95]\n" \
"s_addk_i32 s84, 0x7c00\n" \
M_"v[92:95], a[200:203], a[248:251], v[92:95]\n" \
B_"v27, s[68:71], s84 offen offset:2048 lds\n" \
M_"v[96:99], a[136:139], a[188:191], v[96:99]\n" \
X_"a[164:165], v33 offset:0\n" \
"v_cmp_gt_u32_e32 vcc, s5, v214\n" \
M_"v[96:99], a[200:203], a[252:255], v[96:99]\n" \
R_"a[136:139], v28 offset:20480\n" \
"v_min_u32_e32 v217, 0xa43f, v214\n" \
M_"v[112:115], a[140:143], a[188:191], v[112:115]\n" \
R_"a[200:203], v29 offset:20480\n" \
"v_mov_b32_e64 v214, s24\n" \
M_"v[112:115], a[204:207], a[252:255], v[112:115]\n" \
"s_addk_i32 s84, 0x7c00\n" \
M_"v[108:111], a[140:143], a[184:187], v[108:111]\n" \
B_"v27, s[68:71], s84 offen offset:3072 lds\n" \
M_"v[108:111], a[204:207], a[248:251], v[108:111]\n" \
"s_add_u32 m0, s30, 0x9000\n" \
"s_mov_b32 s84, 0x8180\n" \
M_"v[104:107], a[140:143], a[180:183], v[104:107]\n" \
B_"v22, s[64:67], s84 offen offset:0 lds\n" \
M_"v[104:107], a[204:207], a[244:247], v[104:107]\n" \
X_"a[166:167], v33 offset:1024\n" \
"v_cndmask_b32_e32 v215, v216, v213, vcc\n" \
M_"v[100:103], a[140:143], a[176:179], v[100:103]\n" \
X_"a[228:229], v33 offset:8192\n" \
"v_mov_b32_e64 v213, s22\n" \
M_"v[100:103], a[204:207], a[240:243], v[100:103]\n" \
R_"a[140:143], v28 offset:22528\n" \
"s_movk_i32 s6, 0xffee\n" \
M_"v[116:119], a[144:147], a[176:179], v[116:119]\n" \
R_"a[204:207], v29 offset:22528\n" \
"v_cndmask_b32_e32 v214, v214, v213, vcc\n" \
M_"v[116:119], a[208:211], a[240:243], v[116:119]\n" \
"s_addk_i32 s84, 0x1c00\n" \
M_"v[120:123], a[144:147], a[180:183], v[120:123]\n" \
B_"v23, s[64:67], s84 offen offset:1024 lds\n" \
M_"v[120:123], a[208:211], a[244:247], v[120:123]\n" \
"s_addk_i32 s84, 0x1c00\n" \
M_"v[124:127], a[144:147], a[184:187], v[124:127]\n" \
B_"v22, s[64:67], s84 offen offset:2048 lds\n" \
M_"v[124:127], a[208:211], a[248:251], v[124:127]\n" \
"s_addk_i32 s84, 0x1c00\n" \
M_"v[128:131], a[144:147], a[188:191], v[128:131]\n" \
B_"v23, s[64:67], s84 offen offset:3072 lds\n" \
M_"v[128:131], a[208:211], a[252:255], v[128:131]\n" \
R_"a[144:147], v28 offset:0\n" \
"v_add_u32_e32 v213, 0xffffebc0, v217\n" \
M_"v[144:147], a[148:151], a[188:191], v[144:147]\n" \
R_"a[208:211], v29 offset:0\n" \
"s_lshl_b32 s5, s2, 5\n" \
M_"v[144:147], a[212:215], a[252:255], v[144:147]\n" \
"s_add_u32 m0, s31, 0xc000\n" \
"s_mov_b32 s84, 0x180000\n" \
M_"v[140:143], a[148:151], a[184:187], v[140:143]\n" \
B_"v26, s[72:75], s84 offen offset:0 lds\n" \
M_"v[140:143], a[212:215], a[248:251], v[140:143]\n" \
X_"a[230:231], v33 offset:9216\n" \
"v_cndmask_b32_e64 v213, v213, v217, vcc\n" \
M_"v[136:139], a[148:151], a[180:183], v[136:139]\n" \
"s_addk_i32 s84, 0x7c00\n" \
M_"v[136:139], a[212:215], a[244:247], v[136:139]\n" \
B_"v26, s[72:75], s84 offen offset:1024 lds\n" \
M_"v[132:135], a[148:151], a[176:179], v[132:135]\n" \
X_"a[168:169], v34 offset:0\n" \
"v_lshlrev_b32_e32 v216, 7, v213\n" \
M_"v[132:135], a[212:215], a[240:243], v[132:135]\n" \
R_"a[148:151], v28 offset:2048\n" \
"v_mul_u32_u24_e32 v213, 0xe39, v212\n" \
M_"v[148:151], a[152:155], a[176:179], v[148:151]\n" \
R_"a[212:215], v29 offset:2048\n" \
"v_mov_b32_e64 v217, 0\n" \
M_"v[148:151], a[216:219], a[240:243], v[148:151]\n" \
X_"a[170:171], v34 offset:1024\n" \
"v_lshrrev_b32_e64 v213, 16, v213\n" \
M_"v[152:155], a[152:155], a[180:183], v[152:155]\n" \
X_"a[232:233], v34 offset:8192\n" \
"v_lshl_add_u64 v[16:17], v[214:215], 0, v[216:217]\n" \
M_"v[152:155], a[216:219], a[244:247], v[152:155]\n" \
X_"a[234:235], v34 offset:9216\n" \
"v_mad_i32_i24 v214, v213, s6, v212\n" \
M_"v[156:159], a[152:155], a[184:187], v[156:159]\n" \
X_"a[172:173], v35 offset:0\n" \
"s_add_i32 s5, s5, 32\n" \
M_"v[156:159], a[216:219], a[248:251], v[156:159]\n" \
X_"a[174:175], v35 offset:1024\n" \
"v_lshlrev_b32_e32 v215, 4, v214\n" \
M_"v[160:163], a[152:155], a[188:191], v[160:163]\n" \
X_"a[236:237], v35 offset:8192\n" \
"v_add_u32_e64 v216, s5, v215\n" \
M_"v[160:163], a[216:219], a[252:255], v[160:163]\n" \
R_"a[152:155], v28 offset:4096\n" \
"v_and_b32_e32 v216, 0xf0, v216\n" \
M_"v[176:179], a[156:159], a[188:191], v[176:179]\n" \
R_"a[216:219], v29 offset:4096\n" \
"v_cmp_gt_u32_e64 vcc, 16, v214\n" \
M_"v[176:179], a[220:223], a[252:255], v[176:179]\n" \
X_"a[238:239], v35 offset:9216\n" \
"s_movk_i32 s7, 0xf800\n" \
M_"v[172:175], a[156:159], a[184:187], v[172:175]\n" \
"v_cndmask_b32_e32 v214, v215, v216, vcc\n" \
"v_or_b32_e64 v215, 64, v212\n" \
M_"v[172:175], a[220:223], a[248:251], v[172:175]\n" \
"v_mul_u32_u24_e32 v216, 0xe39, v215\n" \
"v_lshrrev_b32_e64 v216, 16, v216\n" \
M_"v[168:171], a[156:159], a[180:183], v[168:171]\n" \
"v_mad_i32_i24 v215, v216, s6, v215\n" \
"v_lshlrev_b32_e64 v217, 4, v215\n" \
M_"v[168:171], a[220:223], a[244:247], v[168:171]\n" \
"v_add_u32_e64 v220, s5, v217\n" \
"v_and_b32_e32 v220, 0xf0, v220\n" \
M_"v[164:167], a[156:159], a[176:179], v[164:167]\n" \
"v_cmp_gt_u32_e32 vcc, 16, v215\n" \
"s_nop 1\n" \
"v_cndmask_b32_e64 v215, v217, v220, vcc\n" \
M_"v[164:167], a[220:223], a[240:243], v[164:167]\n" \
R_"a[156:159], v28 offset:6144\n" \
R_"a[220:223], v29 offset:6144\n" \
"s_waitcnt lgkmcnt(15)\n" \
M_"a[0:3], a[128:131], a[160:163], a[0:3]\n" \
"s_addk_i32 s84, 0x7c00\n" \
M_"a[0:3], a[192:195], a[224:227], a[0:3]\n" \
B_"v27, s[72:75], s84 offen offset:2048 lds\n" \
M_"a[4:7], a[128:131], a[164:167], a[4:7]\n" \
X_"a[176:177], v36 offset:0\n" \
"v_or_b32_e32 v217, 0x80, v212\n" \
"s_waitcnt lgkmcnt(15)\n" \
M_"a[4:7], a[192:195], a[228:231], a[4:7]\n" \
"s_addk_i32 s84, 0x7c00\n" \
"s_waitcnt lgkmcnt(11)\n" \
M_"a[8:11], a[128:131], a[168:171], a[8:11]\n" \
B_"v27, s[72:75], s84 offen offset:3072 lds\n" \
"s_waitcnt lgkmcnt(9)\n" \
M_"a[8:11], a[192:195], a[232:235], a[8:11]\n" \
X_"a[178:179], v36 offset:1024\n" \
"v_mul_u32_u24_e32 v220, 0xe39, v217\n" \
"s_waitcnt lgkmcnt(8)\n" \
M_"a[12:15], a[128:131], a[172:175], a[12:15]\n" \
X_"a[240:241], v36 offset:8192\n" \
"v_lshrrev_b32_e32 v220, 16, v220\n" \
"s_waitcnt lgkmcnt(5)\n" \
M_"a[12:15], a[192:195], a[236:239], a[12:15]\n" \
"v_mad_i32_i24 v217, v220, s6, v217\n" \
"v_lshlrev_b32_e32 v221, 4, v217\n" \
M_"a[28:31], a[132:135], a[172:175], a[28:31]\n" \
"v_add_u32_e64 v222, s5, v221\n" \
"v_and_b32_e32 v222, 0xf0, v222\n" \
M_"a[28:31], a[196:199], a[236:239], a[28:31]\n" \
X_"a[242:243], v36 offset:9216\n" \
"v_cmp_gt_u32_e64 vcc, 16, v217\n" \
M_"a[24:27], a[132:135], a[168:171], a[24:27]\n" \
B_"v[200:203], v192, s[76:79], s34 offen offset:0\n" \
M_"a[24:27], a[196:199], a[232:235], a[24:27]\n" \
"v_cndmask_b32_e64 v217, v221, v222, vcc\n" \
"v_or_b32_e32 v221, 0xc0, v212\n" \
M_"a[20:23], a[132:135], a[164:167], a[20:23]\n" \
"v_mul_u32_u24_e32 v222, 0xe39, v221\n" \
"v_lshrrev_b32_e64 v222, 16, v222\n" \
M_"a[20:23], a[196:199], a[228:231], a[20:23]\n" \
X_"a[180:181], v37 offset:0\n" \
"v_mad_i32_i24 v221, v222, s6, v221\n" \
M_"a[16:19], a[132:135], a[160:163], a[16:19]\n" \
"v_lshlrev_b32_e32 v223, 4, v221\n" \
"v_add_u32_e32 v224, s5, v223\n" \
M_"a[16:19], a[196:199], a[224:227], a[16:19]\n" \
"v_and_b32_e32 v224, 0xf0, v224\n" \
"v_cmp_gt_u32_e64 vcc, 16, v221\n" \
M_"a[32:35], a[136:139], a[160:163], a[32:35]\n" \
B_"v[204:207], v193, s[76:79], s34 offen offset:0\n" \
M_"a[32:35], a[200:203], a[224:227], a[32:35]\n" \
"s_mov_b32 m0, s32\n" \
X_"a[182:183], v37 offset:1024\n" \
M_"a[36:39], a[136:139], a[164:167], a[36:39]\n" \
"s_mov_b32 exec_lo, 0x0\n" \
"s_mov_b32 exec_hi, 0xff00\n" \
"global_load_lds_dword v[184:185], off\n" \
"s_mov_b64 exec, -1\n" \
M_"a[36:39], a[200:203], a[228:231], a[36:39]\n" \
X_"a[244:245], v37 offset:8192\n" \
"v_cndmask_b32_e32 v221, v223, v224, vcc\n" \
M_"a[40:43], a[136:139], a[168:171], a[40:43]\n" \
"v_or_b32_e32 v223, 0x100, v212\n" \
"v_mul_u32_u24_e32 v224, 0xe39, v223\n" \
M_"a[40:43], a[200:203], a[232:235], a[40:43]\n" \
"buffer_load_dword v208, v194, s[76:79], s34 offen offset:32\n" \
M_"a[44:47], a[136:139], a[172:175], a[44:47]\n" \
X_"a[246:247], v37 offset:9216\n" \
"v_lshrrev_b32_e64 v224, 16, v224\n" \
M_"a[44:47], a[200:203], a[236:239], a[44:47]\n" \
"v_mad_i32_i24 v223, v224, s6, v223\n" \
"v_lshlrev_b32_e64 v225, 4, v223\n" \
M_"a[60:63], a[140:143], a[172:175], a[60:63]\n" \
"v_add_u32_e64 v226, s5, v225\n" \
"v_and_b32_e32 v226, 0xf0, v226\n" \
M_"a[60:63], a[204:207], a[236:239], a[60:63]\n" \
"v_cmp_gt_u32_e32 vcc, 16, v223\n" \
"s_nop 1\n" \
"v_cndmask_b32_e64 v223, v225, v226, vcc\n" \
M_"a[56:59], a[140:143], a[168:171], a[56:59]\n" \
"buffer_load_dword v209, v194, s[76:79], s34 offen offset:1184\n" \
M_"a[56:59], a[204:207], a[232:235], a[56:59]\n" \
X_"a[184:185], v38 offset:0\n" \
"v_or_b32_e32 v225, 0x140, v212\n" \
M_"a[52:55], a[140:143], a[164:167], a[52:55]\n" \
"v_mul_u32_u24_e32 v226, 0xe39, v225\n" \
"v_lshrrev_b32_e64 v226, 16, v226\n" \
M_"a[52:55], a[204:207], a[228:231], a[52:55]\n" \
X_"a[186:187], v38 offset:1024\n" \
"v_mad_i32_i24 v225, v226, s6, v225\n" \
M_"a[48:51], a[140:143], a[160:163], a[48:51]\n" \
"v_lshlrev_b32_e32 v227, 4, v225\n" \
"v_add_u32_e32 v228, s5, v227\n" \
M_"a[48:51], a[204:207], a[224:227], a[48:51]\n" \
"v_and_b32_e32 v228, 0xf0, v228\n" \
"v_cmp_gt_u32_e64 vcc, 16, v225\n" \
M_"a[64:67], a[144:147], a[160:163], a[64:67]\n" \
"buffer_load_dword v211, v194, s[76:79], s34 offen offset:2336\n" \
M_"a[64:67], a[208:211], a[224:227], a[64:67]\n" \
X_"a[248:249], v38 offset:8192\n" \
"v_cndmask_b32_e64 v225, v227, v228, vcc\n" \
M_"a[68:71], a[144:147], a[164:167], a[68:71]\n" \
"v_or_b32_e32 v227, 0x180, v212\n" \
"v_mul_u32_u24_e32 v228, 0xe39, v227\n" \
M_"a[68:71], a[208:211], a[228:231], a[68:71]\n" \
X_"a[250:251], v38 offset:9216\n" \
"v_lshrrev_b32_e64 v228, 16, v228\n" \
M_"a[72:75], a[144:147], a[168:171], a[72:75]\n" \
"v_mad_i32_i24 v227, v228, s6, v227\n" \
"v_lshlrev_b32_e64 v229, 4, v227\n" \
M_"a[72:75], a[208:211], a[232:235], a[72:75]\n" \
"s_mov_b32 s76, s20\n" \
"s_and_b32 s77, s21, 0xffff\n" \
M_"a[76:79], a[144:147], a[172:175], a[76:79]\n" \
"s_mov_b32 s78, 0x480\n" \
"v_add_u32_e32 v230, s5, v229\n" \
M_"a[76:79], a[208:211], a[236:239], a[76:79]\n" \
"buffer_load_ushort v210, v195, s[76:79], 0 offen\n" \
M_"a[92:95], a[148:151], a[172:175], a[92:95]\n" \
X_"a[188:189], v39 offset:0\n" \
"v_and_b32_e32 v230, 0xf0, v230\n" \
M_"a[92:95], a[212:215], a[236:239], a[92:95]\n" \
"v_cmp_gt_u32_e32 vcc, 16, v227\n" \
"s_nop 1\n" \
"v_cndmask_b32_e64 v227, v229, v230, vcc\n" \
M_"a[88:91], a[148:151], a[168:171], a[88:91]\n" \
"s_mov_b32 s76, s8\n" \
"s_and_b32 s77, s9, 0xffff\n" \
M_"a[88:91], a[212:215], a[232:235], a[88:91]\n" \
"s_mov_b32 s78, 0x240000\n" \
X_"a[190:191], v39 offset:1024\n" \
M_"a[84:87], a[148:151], a[164:167], a[84:87]\n" \
"v_or_b32_e32 v229, 0x1c0, v212\n" \
"v_mul_u32_u24_e32 v230, 0xe39, v229\n" \
M_"a[84:87], a[212:215], a[228:231], a[84:87]\n" \
".fill (L_pb + 48864 - .) / 4, 4, 0xBF800000\n" \
"L_pp12:\n" \
"s_cbranch_execz L_pret\n" \
X_"a[252:253], v39 offset:8192\n" \
"v_lshrrev_b32_e32 v230, 16, v230\n" \
M_"a[80:83], a[148:151], a[160:163], a[80:83]\n" \
"v_mad_i32_i24 v229, v230, s6, v229\n" \
"v_lshlrev_b32_e64 v231, 4, v229\n" \
M_"a[80:83], a[212:215], a[224:227], a[80:83]\n" \
"v_add_u32_e64 v232, s5, v231\n" \
"v_and_b32_e32 v232, 0xf0, v232\n" \
M_"a[96:99], a[152:155], a[160:163], a[96:99]\n" \
X_"a[254:255], v39 offset:9216\n" \
"v_cmp_gt_u32_e64 vcc, 16, v229\n" \
M_"a[96:99], a[216:219], a[224:227], a[96:99]\n" \
"s_nop 0\n" \
"v_cndmask_b32_e32 v229, v231, v232, vcc\n" \
"v_or_b32_e32 v231, 0x200, v212\n" \
M_"a[100:103], a[152:155], a[164:167], a[100:103]\n" \
"v_mul_u32_u24_e32 v232, 0xe39, v231\n" \
"v_lshrrev_b32_e64 v232, 16, v232\n" \
M_"a[100:103], a[216:219], a[228:231], a[100:103]\n" \
"v_mad_i32_i24 v231, v232, s6, v231\n" \
"v_lshlrev_b32_e64 v233, 4, v231\n" \
M_"a[104:107], a[152:155], a[168:171], a[104:107]\n" \
"v_add_u32_e32 v234, s5, v233\n" \
"s_movk_i32 s5, 0x480\n" \
M_"a[104:107], a[216:219], a[232:235], a[104:107]\n" \
"v_mad_u32_u24 v0, v213, s5, v214\n" \
"v_mul_u32_u24_e32 v213, 0x480, v216\n" \
M_"a[108:111], a[152:155], a[172:175], a[108:111]\n" \
"s_movk_i32 s6, 0xfc00\n" \
"v_add3_u32 v1, v213, v215, s6\n" \
M_"a[108:111], a[216:219], a[236:239], a[108:111]\n" \
"v_mul_u32_u24_e32 v213, 0x480, v220\n" \
"v_add3_u32 v2, v213, v217, s7\n" \
"s_waitcnt lgkmcnt(15)\n" \
M_"a[124:127], a[156:159], a[172:175], a[124:127]\n" \
"v_mul_u32_u24_e32 v213, 0x480, v222\n" \
"v_add3_u32 v3, v213, v221, s40\n" \
M_"a[124:127], a[220:223], a[236:239], a[124:127]\n" \
"v_mul_u32_u24_e32 v213, 0x480, v226\n" \
"v_add3_u32 v5, v213, v225, s6\n" \
M_"a[120:123], a[156:159], a[168:171], a[120:123]\n" \
"v_mul_u32_u24_e32 v213, 0x480, v228\n" \
"v_add3_u32 v6, v213, v227, s7\n" \
M_"a[120:123], a[220:223], a[232:235], a[120:123]\n" \
"v_mul_u32_u24_e32 v213, 0x480, v230\n" \
"v_add3_u32 v7, v213, v229, s40\n" \
M_"a[116:119], a[156:159], a[164:167], a[116:119]\n" \
"s_lshl_b32 s7, s3, 3\n" \
"s_lshl_b32 s40, s3, 1\n" \
M_"a[116:119], a[220:223], a[228:231], a[116:119]\n" \
"s_and_b32 s7, s7, 16\n" \
"s_and_b32 s40, s40, 2\n" \
M_"a[112:115], a[156:159], a[160:163], a[112:115]\n" \
"s_and_b32 s6, s2, 7\n" \
"v_bfe_u32 v213, v20, 7, 1\n" \
"s_waitcnt vmcnt(7) lgkmcnt(0)\n" \
M_"a[112:115], a[220:223], a[224:227], a[112:115]\n" \
"s_barrier\n" \
M_"v[52:55], a[128:131], a[176:179], v[52:55]\n" \
"s_add_u32 m0, s30, 0x0\n" \
"s_movk_i32 s84, 0x200\n" \
M_"v[52:55], a[192:195], a[240:243], v[52:55]\n" \
B_"v22, s[64:67], s84 offen offset:0 lds\n" \
M_"v[56:59], a[128:131], a[180:183], v[56:59]\n" \
X_"a[160:161], v32 offset:32768\n" \
"s_or_b32 s7, s40, s7\n" \
M_"v[56:59], a[192:195], a[244:247], v[56:59]\n" \
"s_movk_i32 s84, 0x1e00\n" \
M_"v[60:63], a[128:131], a[184:187], v[60:63]\n" \
B_"v23, s[64:67], s84 offen offset:1024 lds\n" \
M_"v[60:63], a[192:195], a[248:251], v[60:63]\n" \
X_"a[162:163], v32 offset:33792\n" \
"v_and_b32_e32 v234, 0xf0, v234\n" \
M_"v[64:67], a[128:131], a[188:191], v[64:67]\n" \
X_"a[224:225], v32 offset:40960\n" \
"v_cmp_gt_u32_e32 vcc, 16, v231\n" \
M_"v[64:67], a[192:195], a[252:255], v[64:67]\n" \
R_"a[128:131], v28 offset:49152\n" \
"v_or3_b32 v213, s7, v213, v219\n" \
M_"v[80:83], a[132:135], a[188:191], v[80:83]\n" \
R_"a[192:195], v29 offset:49152\n" \
"v_and_b32_e64 v214, 32, v20\n" \
M_"v[80:83], a[196:199], a[252:255], v[80:83]\n" \
"s_movk_i32 s84, 0x3a00\n" \
M_"v[76:79], a[132:135], a[184:187], v[76:79]\n" \
B_"v22, s[64:67], s84 offen offset:2048 lds\n" \
M_"v[76:79], a[196:199], a[248:251], v[76:79]\n" \
"s_movk_i32 s84, 0x5600\n" \
M_"v[72:75], a[132:135], a[180:183], v[72:75]\n" \
B_"v23, s[64:67], s84 offen offset:3072 lds\n" \
M_"v[72:75], a[196:199], a[244:247], v[72:75]\n" \
"s_add_u32 m0, s31, 0x0\n" \
"s_mov_b32 s84, 0x200000\n" \
M_"v[68:71], a[132:135], a[176:179], v[68:71]\n" \
B_"v26, s[68:71], s84 offen offset:0 lds\n" \
M_"v[68:71], a[196:199], a[240:243], v[68:71]\n" \
R_"a[132:135], v28 offset:51200\n" \
"s_lshl_b32 s6, s6, 5\n" \
M_"v[84:87], a[136:139], a[176:179], v[84:87]\n" \
R_"a[196:199], v29 offset:51200\n" \
"v_cndmask_b32_e64 v231, v233, v234, vcc\n" \
M_"v[84:87], a[200:203], a[240:243], v[84:87]\n" \
"s_addk_i32 s84, 0x7c00\n" \
M_"v[88:91], a[136:139], a[180:183], v[88:91]\n" \
B_"v26, s[68:71], s84 offen offset:1024 lds\n" \
M_"v[88:91], a[200:203], a[244:247], v[88:91]\n" \
X_"a[226:227], v32 offset:41984\n" \
"v_mul_u32_u24_e32 v213, 18, v213\n" \
M_"v[92:95], a[136:139], a[184:187], v[92:95]\n" \
"s_addk_i32 s84, 0x7c00\n" \
M_"v[92:95], a[200:203], a[248:251], v[92:95]\n" \
B_"v27, s[68:71], s84 offen offset:2048 lds\n" \
M_"v[96:99], a[136:139], a[188:191], v[96:99]\n" \
X_"a[164:165], v33 offset:32768\n" \
"v_mov_b32_e32 v215, 0x100\n" \
M_"v[96:99], a[200:203], a[252:255], v[96:99]\n" \
R_"a[136:139], v28 offset:53248\n" \
"v_mov_b32_e32 v216, s6\n" \
M_"v[112:115], a[140:143], a[188:191], v[112:115]\n" \
R_"a[200:203], v29 offset:53248\n" \
"v_cmp_eq_u32_e64 vcc, 0, v214\n" \
M_"v[112:115], a[204:207], a[252:255], v[112:115]\n" \
"s_addk_i32 s84, 0x7c00\n" \
"s_nop 0\n" \
M_"v[108:111], a[140:143], a[184:187], v[108:111]\n" \
B_"v27, s[68:71], s84 offen offset:3072 lds\n" \
M_"v[108:111], a[204:207], a[248:251], v[108:111]\n" \
"s_add_u32 m0, s30, 0x1000\n" \
"s_mov_b32 s84, 0x8200\n" \
M_"v[104:107], a[140:143], a[180:183], v[104:107]\n" \
B_"v22, s[64:67], s84 offen offset:0 lds\n" \
M_"v[104:107], a[204:207], a[244:247], v[104:107]\n" \
X_"a[166:167], v33 offset:33792\n" \
"v_and_or_b32 v213, v218, 1, v213\n" \
M_"v[100:103], a[140:143], a[176:179], v[100:103]\n" \
X_"a[228:229], v33 offset:40960\n" \
"v_lshlrev_b32_e64 v212, 3, v212\n" \
M_"v[100:103], a[204:207], a[240:243], v[100:103]\n" \
R_"a[140:143], v28 offset:55296\n" \
"v_cndmask_b32_e64 v214, v215, v216, vcc\n" \
M_"v[116:119], a[144:147], a[176:179], v[116:119]\n" \
R_"a[204:207], v29 offset:55296\n" \
"v_mad_u32_u24 v213, v213, s5, v214\n" \
M_"v[116:119], a[208:211], a[240:243], v[116:119]\n" \
"s_addk_i32 s84, 0x1c00\n" \
M_"v[120:123], a[144:147], a[180:183], v[120:123]\n" \
B_"v23, s[64:67], s84 offen offset:1024 lds\n" \
M_"v[120:123], a[208:211], a[244:247], v[120:123]\n" \
"s_addk_i32 s84, 0x1c00\n" \
M_"v[124:127], a[144:147], a[184:187], v[124:127]\n" \
B_"v22, s[64:67], s84 offen offset:2048 lds\n" \
M_"v[124:127], a[208:211], a[248:251], v[124:127]\n" \
"s_addk_i32 s84, 0x1c00\n" \
"s_nop 0\n" \
M_"v[128:131], a[144:147], a[188:191], v[128:131]\n" \
B_"v23, s[64:67], s84 offen offset:3072 lds\n" \
M_"v[128:131], a[208:211], a[252:255], v[128:131]\n" \
R_"a[144:147], v28 offset:32768\n" \
"v_and_or_b32 v213, v20, 16, v213\n" \
M_"v[144:147], a[148:151], a[188:191], v[144:147]\n" \
R_"a[208:211], v29 offset:32768\n" \
"v_add_u32_e32 v9, 0x4800, v213\n" \
M_"v[144:147], a[212:215], a[252:255], v[144:147]\n" \
"s_add_u32 m0, s31, 0x4000\n" \
"s_mov_b32 s84, 0x200000\n" \
M_"v[140:143], a[148:151], a[184:187], v[140:143]\n" \
B_"v26, s[72:75], s84 offen offset:0 lds\n" \
M_"v[140:143], a[212:215], a[248:251], v[140:143]\n" \
X_"a[230:231], v33 offset:41984\n" \
"v_mul_u32_u24_e32 v213, 0x120, v218\n" \
M_"v[136:139], a[148:151], a[180:183], v[136:139]\n" \
"s_addk_i32 s84, 0x7c00\n" \
M_"v[136:139], a[212:215], a[244:247], v[136:139]\n" \
B_"v26, s[72:75], s84 offen offset:1024 lds\n" \
M_"v[132:135], a[148:151], a[176:179], v[132:135]\n" \
X_"a[168:169], v34 offset:32768\n" \
"v_and_or_b32 v10, v212, 24, v213\n" \
M_"v[132:135], a[212:215], a[240:243], v[132:135]\n" \
R_"a[148:151], v28 offset:34816\n" \
"v_lshrrev_b32_e32 v213, 2, v20\n" \
M_"v[148:151], a[152:155], a[176:179], v[148:151]\n" \
R_"a[212:215], v29 offset:34816\n" \
"v_and_or_b32 v213, v213, 60, s4\n" \
M_"v[148:151], a[216:219], a[240:243], v[148:151]\n" \
X_"a[170:171], v34 offset:33792\n" \
"v_lshl_or_b32 v212, s3, 9, v212\n" \
M_"v[152:155], a[152:155], a[180:183], v[152:155]\n" \
X_"a[232:233], v34 offset:40960\n" \
"v_mad_u32_u24 v4, v224, s5, v223\n" \
M_"v[152:155], a[216:219], a[244:247], v[152:155]\n" \
X_"a[234:235], v34 offset:41984\n" \
"v_mad_u32_u24 v8, v232, s5, v231\n" \
M_"v[156:159], a[152:155], a[184:187], v[156:159]\n" \
X_"a[172:173], v35 offset:32768\n" \
"v_or_b32_e32 v18, 0x27000, v213\n" \
M_"v[156:159], a[216:219], a[248:251], v[156:159]\n" \
X_"a[174:175], v35 offset:33792\n" \
"v_or_b32_e32 v19, 0x27800, v212\n" \
M_"v[160:163], a[152:155], a[188:191], v[160:163]\n" \
X_"a[236:237], v35 offset:40960\n" \
"v_mov_b32_e32 v215, 0x1000c0c\n" \
M_"v[160:163], a[216:219], a[252:255], v[160:163]\n" \
R_"a[152:155], v28 offset:36864\n" \
"v_mov_b32_e32 v216, 0xc0c0100\n" \
M_"v[176:179], a[156:159], a[188:191], v[176:179]\n" \
R_"a[216:219], v29 offset:36864\n" \
"v_bfe_u32 v214, v20, 4, 1\n" \
M_"v[176:179], a[220:223], a[252:255], v[176:179]\n" \
X_"a[238:239], v35 offset:41984\n" \
"v_lshrrev_b32_e64 v212, 8, v20\n" \
M_"v[172:175], a[156:159], a[184:187], v[172:175]\n" \
"v_cmp_eq_u32_e64 vcc, 0, v214\n" \
"v_bfe_u32 v213, v20, 4, 4\n" \
M_"v[172:175], a[220:223], a[248:251], v[172:175]\n" \
"v_mov_b32_e32 v217, 0xc0c0302\n" \
"v_cndmask_b32_e64 v215, v215, v216, vcc\n" \
M_"v[168:171], a[156:159], a[180:183], v[168:171]\n" \
"v_mov_b32_e32 v216, 0x3020c0c\n" \
"v_lshl_or_b32 v214, v212, 2, v214\n" \
M_"v[168:171], a[220:223], a[244:247], v[168:171]\n" \
"v_cndmask_b32_e64 v216, v216, v217, vcc\n" \
"v_mov_b32_e32 v217, 0xc0c0c0c\n" \
M_"v[164:167], a[156:159], a[176:179], v[164:167]\n" \
"v_cmp_gt_u32_e32 vcc, v214, v213\n" \
"v_or_b32_e32 v214, 2, v214\n" \
M_"v[164:167], a[220:223], a[240:243], v[164:167]\n" \
R_"a[156:159], v28 offset:38912\n" \
R_"a[220:223], v29 offset:38912\n" \
"s_waitcnt lgkmcnt(15)\n" \
M_"a[0:3], a[128:131], a[160:163], a[0:3]\n" \
"s_addk_i32 s84, 0x7c00\n" \
M_"a[0:3], a[192:195], a[224:227], a[0:3]\n" \
B_"v27, s[72:75], s84 offen offset:2048 lds\n" \
M_"a[4:7], a[128:131], a[164:167], a[4:7]\n" \
X_"a[176:177], v36 offset:32768\n" \
"s_lshr_b32 s4, s2, 5\n" \
"s_waitcnt lgkmcnt(15)\n" \
M_"a[4:7], a[192:195], a[228:231], a[4:7]\n" \
"s_addk_i32 s84, 0x7c00\n" \
"s_waitcnt lgkmcnt(11)\n" \
M_"a[8:11], a[128:131], a[168:171], a[8:11]\n" \
B_"v27, s[72:75], s84 offen offset:3072 lds\n" \
"s_waitcnt lgkmcnt(9)\n" \
M_"a[8:11], a[192:195], a[232:235], a[8:11]\n" \
X_"a[178:179], v36 offset:33792\n" \
"v_cndmask_b32_e32 v40, v217, v215, vcc\n" \
"s_waitcnt lgkmcnt(8)\n" \
M_"a[12:15], a[128:131], a[172:175], a[12:15]\n" \
X_"a[240:241], v36 offset:40960\n" \
"v_cndmask_b32_e32 v42, v215, v217, vcc\n" \
"s_waitcnt lgkmcnt(5)\n" \
M_"a[12:15], a[192:195], a[236:239], a[12:15]\n" \
"v_cmp_gt_u32_e32 vcc, v214, v213\n" \
"v_and_b32_e64 v214, 16, v20\n" \
M_"a[28:31], a[132:135], a[172:175], a[28:31]\n" \
"s_movk_i32 s42, 0x3f80\n" \
"s_xor_b32 s5, s4, 15\n" \
M_"a[28:31], a[196:199], a[236:239], a[28:31]\n" \
X_"a[242:243], v36 offset:41984\n" \
M_"a[24:27], a[132:135], a[168:171], a[24:27]\n" \
"s_cmpk_lt_u32 s3, 2\n" \
"s_cselect_b32 s6, s5, s4\n" \
M_"a[24:27], a[196:199], a[232:235], a[24:27]\n" \
"s_lshl_b32 s7, s6, 7\n" \
"v_and_b32_e32 v213, 0x320, v20\n" \
M_"a[20:23], a[132:135], a[164:167], a[20:23]\n" \
"s_movk_i32 s41, 0x100\n" \
"v_lshlrev_b32_e64 v214, v214, s42\n" \
M_"a[20:23], a[196:199], a[228:231], a[20:23]\n" \
X_"a[180:181], v37 offset:32768\n" \
"v_mov_b32_e64 v215, 0\n" \
M_"a[16:19], a[132:135], a[160:163], a[16:19]\n" \
"s_add_i32 s40, s7, -16\n" \
"v_cndmask_b32_e32 v41, v217, v216, vcc\n" \
M_"a[16:19], a[196:199], a[224:227], a[16:19]\n" \
"v_cndmask_b32_e32 v43, v216, v217, vcc\n" \
"v_cmp_eq_u32_e32 vcc, s41, v213\n" \
M_"a[32:35], a[136:139], a[160:163], a[32:35]\n" \
X_"a[182:183], v37 offset:33792\n" \
"s_mov_b32 s41, 0\n" \
M_"a[32:35], a[200:203], a[224:227], a[32:35]\n" \
"s_and_b32 s43, s3, 1\n" \
"v_cndmask_b32_e64 v46, v215, v214, vcc\n" \
M_"a[36:39], a[136:139], a[164:167], a[36:39]\n" \
"v_and_b32_e32 v213, 0x330, v20\n" \
M_"a[36:39], a[200:203], a[228:231], a[36:39]\n" \
X_"a[244:245], v37 offset:40960\n" \
M_"a[40:43], a[136:139], a[168:171], a[40:43]\n" \
"s_mov_b32 m0, s32\n" \
M_"a[40:43], a[200:203], a[232:235], a[40:43]\n" \
"s_mov_b32 exec_lo, 0xffffffff\n" \
"s_mov_b32 exec_hi, 0x0\n" \
"global_load_lds_dword v[188:189], off\n" \
"s_mov_b64 exec, -1\n" \
M_"a[44:47], a[136:139], a[172:175], a[44:47]\n" \
X_"a[246:247], v37 offset:41984\n" \
M_"a[44:47], a[200:203], a[236:239], a[44:47]\n" \
"s_cmp_lg_u32 s7, 0\n" \
"s_cselect_b32 s40, s40, s41\n" \
M_"a[60:63], a[140:143], a[172:175], a[60:63]\n" \
"s_mov_b32 s42, 0xff800000\n" \
M_"a[60:63], a[204:207], a[236:239], a[60:63]\n" \
"s_cmp_lg_u32 s7, 0\n" \
"s_cselect_b32 s47, s41, s42\n" \
M_"a[56:59], a[140:143], a[168:171], a[56:59]\n" \
X_"a[184:185], v38 offset:32768\n" \
"s_lshl_b32 s41, s43, 6\n" \
M_"a[56:59], a[204:207], a[232:235], a[56:59]\n" \
"v_lshrrev_b32_e32 v215, 6, v20\n" \
"v_mov_b32_e32 v214, 0x3f80\n" \
M_"a[52:55], a[140:143], a[164:167], a[52:55]\n" \
"v_cmp_eq_u32_e32 vcc, 32, v213\n" \
"s_or_b32 s7, s41, s7\n" \
M_"a[52:55], a[204:207], a[228:231], a[52:55]\n" \
X_"a[186:187], v38 offset:33792\n" \
"v_lshrrev_b32_e64 v213, 7, v20\n" \
M_"a[48:51], a[140:143], a[160:163], a[48:51]\n" \
"v_bfe_u32 v216, v20, 6, 2\n" \
"v_lshrrev_b32_e64 v219, 1, v20\n" \
M_"a[48:51], a[204:207], a[224:227], a[48:51]\n" \
"v_bfe_u32 v215, v215, 1, 1\n" \
"v_cndmask_b32_e64 v47, 0, v214, vcc\n" \
M_"a[64:67], a[144:147], a[160:163], a[64:67]\n" \
X_"a[248:249], v38 offset:40960\n" \
"v_or_b32_e64 v214, s7, v213\n" \
M_"a[64:67], a[208:211], a[224:227], a[64:67]\n" \
"v_lshl_or_b32 v217, s43, 3, v213\n" \
"v_lshlrev_b32_e64 v218, 10, v212\n" \
M_"a[68:71], a[144:147], a[164:167], a[68:71]\n" \
"v_lshlrev_b32_e32 v216, 7, v216\n" \
"v_and_b32_e32 v219, 24, v219\n" \
M_"a[68:71], a[208:211], a[228:231], a[68:71]\n" \
X_"a[250:251], v38 offset:41984\n" \
"v_and_or_b32 v213, v213, 2, v215\n" \
M_"a[72:75], a[144:147], a[168:171], a[72:75]\n" \
"v_add_u32_e64 v217, s40, v217\n" \
"v_or3_b32 v216, v216, v218, v219\n" \
M_"a[72:75], a[208:211], a[232:235], a[72:75]\n" \
"v_lshlrev_b32_e64 v213, 5, v213\n" \
"s_mov_b32 s40, 0x14000\n" \
M_"a[76:79], a[144:147], a[172:175], a[76:79]\n" \
"s_lshl_b32 s7, s2, 2\n" \
"v_or3_b32 v48, v216, v213, s40\n" \
M_"a[76:79], a[208:211], a[236:239], a[76:79]\n" \
X_"a[188:189], v39 offset:32768\n" \
"s_lshl_b32 s40, s43, 2\n" \
M_"a[92:95], a[148:151], a[172:175], a[92:95]\n" \
"s_lshl_b32 s6, s6, 3\n" \
"v_or_b32_e32 v213, s40, v212\n" \
M_"a[92:95], a[212:215], a[236:239], a[92:95]\n" \
"s_or_b32 s6, s6, s40\n" \
"s_and_b32 s40, s7, 0x7c\n" \
M_"a[88:91], a[148:151], a[168:171], a[88:91]\n" \
X_"a[190:191], v39 offset:33792\n" \
"s_or_b32 s40, s40, s3\n" \
M_"a[88:91], a[212:215], a[232:235], a[88:91]\n" \
"s_cmpk_lt_u32 s40, 64\n" \
"s_cselect_b32 s41, s4, s5\n" \
M_"a[84:87], a[148:151], a[164:167], a[84:87]\n" \
"s_waitcnt vmcnt(17)\n" \
"v_or_b32_e32 v204, v210, v204\n" \
"v_or_b32_e64 v206, v210, v206\n" \
M_"a[84:87], a[212:215], a[228:231], a[84:87]\n" \
"v_mov_b32_e64 v210, 0\n" \
"s_add_u32 s88, s35, 0x1000\n" \
M_"a[80:83], a[148:151], a[160:163], a[80:83]\n" \
X_"a[252:253], v39 offset:40960\n" \
"s_lshl_b32 s40, s40, 1\n" \
M_"a[80:83], a[212:215], a[224:227], a[80:83]\n" \
"ds_write_b128 v196, v[200:203]\n" \
M_"a[96:99], a[152:155], a[160:163], a[96:99]\n" \
X_"a[254:255], v39 offset:41984\n" \
"s_lshl_b32 s41, s41, 7\n" \
M_"a[96:99], a[216:219], a[224:227], a[96:99]\n" \
"s_and_b32 s40, s40, 0x7e\n" \
"s_or_b32 s40, s41, s40\n" \
M_"a[100:103], a[152:155], a[164:167], a[100:103]\n" \
"ds_write_b128 v197, v[204:207]\n" \
M_"a[100:103], a[216:219], a[228:231], a[100:103]\n" \
"s_mulk_i32 s40, 0x480\n" \
"v_add_u32_e64 v49, s40, v20\n" \
M_"a[104:107], a[152:155], a[168:171], a[104:107]\n" \
"s_mov_b32 s40, 0xc000\n" \
"s_and_b32 s7, s7, 28\n" \
M_"a[104:107], a[216:219], a[232:235], a[104:107]\n" \
"ds_write_b128 v198, v[208:211]\n" \
M_"a[108:111], a[152:155], a[172:175], a[108:111]\n" \
"s_bfe_u32 s45, s2, 0x20003\n" \
"s_mov_b32 s41, 0x8000\n" \
M_"a[108:111], a[216:219], a[236:239], a[108:111]\n" \
"s_cmp_lg_u32 s4, 0\n" \
"s_cselect_b32 s51, s40, s41\n" \
"s_waitcnt lgkmcnt(15)\n" \
M_"a[124:127], a[156:159], a[172:175], a[124:127]\n" \
"s_movk_i32 s40, 0x4000\n" \
"s_lshr_b32 s44, s3, 1\n" \
M_"a[124:127], a[220:223], a[236:239], a[124:127]\n" \
"s_or_b32 s46, s7, s45\n" \
M_"a[120:123], a[156:159], a[168:171], a[120:123]\n" \
"s_cmp_lg_u32 s4, 0\n" \
"s_cselect_b32 s53, s41, s40\n" \
M_"a[120:123], a[220:223], a[232:235], a[120:123]\n" \
"s_lshl_b32 s40, s44, 15\n" \
"s_lshl_b32 s41, s43, 14\n" \
M_"a[116:119], a[156:159], a[164:167], a[116:119]\n" \
"s_lshl_b32 s48, s46, 20\n" \
"s_or_b32 s41, s40, s41\n" \
M_"a[116:119], a[220:223], a[228:231], a[116:119]\n" \
"s_add_i32 s49, s48, 0x1000000\n" \
"s_or_b32 s42, s49, s41\n" \
M_"a[112:115], a[156:159], a[160:163], a[112:115]\n" \
"s_lshl_b32 s4, s4, 16\n" \
"s_or_b32 s41, s42, s4\n" \
"s_waitcnt vmcnt(1) lgkmcnt(0)\n" \
M_"a[112:115], a[220:223], a[224:227], a[112:115]\n" \
"s_barrier\n" \
M_"v[52:55], a[128:131], a[176:179], v[52:55]\n" \
"s_add_u32 m0, s30, 0x8000\n" \
"s_movk_i32 s84, 0x280\n" \
M_"v[52:55], a[192:195], a[240:243], v[52:55]\n" \
B_"v22, s[64:67], s84 offen offset:0 lds\n" \
M_"v[56:59], a[128:131], a[180:183], v[56:59]\n" \
"s_movk_i32 s84, 0x1e80\n" \
M_"v[56:59], a[192:195], a[244:247], v[56:59]\n" \
B_"v23, s[64:67], s84 offen offset:1024 lds\n" \
M_"v[60:63], a[128:131], a[184:187], v[60:63]\n" \
X_"a[160:161], v32 offset:0\n" \
"s_lshl_b32 s4, s5, 16\n" \
M_"v[60:63], a[192:195], a[248:251], v[60:63]\n" \
"s_movk_i32 s84, 0x3a80\n" \
M_"v[64:67], a[128:131], a[188:191], v[64:67]\n" \
B_"v22, s[64:67], s84 offen offset:2048 lds\n" \
M_"v[64:67], a[192:195], a[252:255], v[64:67]\n" \
R_"a[128:131], v28 offset:16384\n" \
"s_mul_i32 s5, s6, 0x24800\n" \
M_"v[80:83], a[132:135], a[188:191], v[80:83]\n" \
R_"a[192:195], v29 offset:16384\n" \
"s_lshl_b32 s6, s46, 9\n" \
M_"v[80:83], a[196:199], a[252:255], v[80:83]\n" \
"s_movk_i32 s84, 0x5680\n" \
M_"v[76:79], a[132:135], a[184:187], v[76:79]\n" \
B_"v23, s[64:67], s84 offen offset:3072 lds\n" \
M_"v[76:79], a[196:199], a[248:251], v[76:79]\n" \
"s_add_u32 m0, s31, 0x8000\n" \
"s_mov_b32 s84, 0x280000\n" \
M_"v[72:75], a[132:135], a[180:183], v[72:75]\n" \
B_"v26, s[68:71], s84 offen offset:0 lds\n" \
M_"v[72:75], a[196:199], a[244:247], v[72:75]\n" \
X_"a[162:163], v32 offset:1024\n" \
"s_lshl_b32 s7, s7, 6\n" \
M_"v[68:71], a[132:135], a[176:179], v[68:71]\n" \
X_"a[224:225], v32 offset:8192\n" \
"s_add_i32 s6, s5, s6\n" \
M_"v[68:71], a[196:199], a[240:243], v[68:71]\n" \
R_"a[132:135], v28 offset:18432\n" \
"s_or_b32 s5, s5, s7\n" \
M_"v[84:87], a[136:139], a[176:179], v[84:87]\n" \
R_"a[196:199], v29 offset:18432\n" \
"s_lshl_b32 s7, s45, 2\n" \
M_"v[84:87], a[200:203], a[240:243], v[84:87]\n" \
"s_addk_i32 s84, 0x7c00\n" \
M_"v[88:91], a[136:139], a[180:183], v[88:91]\n" \
B_"v26, s[68:71], s84 offen offset:1024 lds\n" \
M_"v[88:91], a[200:203], a[244:247], v[88:91]\n" \
"s_addk_i32 s84, 0x7c00\n" \
M_"v[92:95], a[136:139], a[184:187], v[92:95]\n" \
B_"v27, s[68:71], s84 offen offset:2048 lds\n" \
M_"v[92:95], a[200:203], a[248:251], v[92:95]\n" \
"s_addk_i32 s84, 0x7c00\n" \
M_"v[96:99], a[136:139], a[188:191], v[96:99]\n" \
B_"v27, s[68:71], s84 offen offset:3072 lds\n" \
M_"v[96:99], a[200:203], a[252:255], v[96:99]\n" \
R_"a[136:139], v28 offset:20480\n" \
"s_or_b32 s5, s5, s7\n" \
M_"v[112:115], a[140:143], a[188:191], v[112:115]\n" \
R_"a[200:203], v29 offset:20480\n" \
"s_lshl_b32 s7, s3, 12\n" \
M_"v[112:115], a[204:207], a[252:255], v[112:115]\n" \
"s_add_u32 m0, s30, 0x9000\n" \
"s_mov_b32 s84, 0x8280\n" \
M_"v[108:111], a[140:143], a[184:187], v[108:111]\n" \
B_"v22, s[64:67], s84 offen offset:0 lds\n" \
M_"v[108:111], a[204:207], a[248:251], v[108:111]\n" \
"s_addk_i32 s84, 0x1c00\n" \
M_"v[104:107], a[140:143], a[180:183], v[104:107]\n" \
B_"v23, s[64:67], s84 offen offset:1024 lds\n" \
M_"v[104:107], a[204:207], a[244:247], v[104:107]\n" \
"s_addk_i32 s84, 0x1c00\n" \
M_"v[100:103], a[140:143], a[176:179], v[100:103]\n" \
B_"v22, s[64:67], s84 offen offset:2048 lds\n" \
M_"v[100:103], a[204:207], a[240:243], v[100:103]\n" \
R_"a[140:143], v28 offset:22528\n" \
"s_or_b32 s50, s49, s7\n" \
M_"v[116:119], a[144:147], a[176:179], v[116:119]\n" \
R_"a[204:207], v29 offset:22528\n" \
"s_lshl_b32 s7, s3, 11\n" \
M_"v[116:119], a[208:211], a[240:243], v[116:119]\n" \
"s_addk_i32 s84, 0x1c00\n" \
M_"v[120:123], a[144:147], a[180:183], v[120:123]\n" \
B_"v23, s[64:67], s84 offen offset:3072 lds\n" \
M_"v[120:123], a[208:211], a[244:247], v[120:123]\n" \
"s_add_u32 m0, s31, 0xc000\n" \
"s_mov_b32 s84, 0x280000\n" \
M_"v[124:127], a[144:147], a[184:187], v[124:127]\n" \
B_"v26, s[72:75], s84 offen offset:0 lds\n" \
M_"v[124:127], a[208:211], a[248:251], v[124:127]\n" \
"s_addk_i32 s84, 0x7c00\n" \
M_"v[128:131], a[144:147], a[188:191], v[128:131]\n" \
B_"v26, s[72:75], s84 offen offset:1024 lds\n" \
M_"v[128:131], a[208:211], a[252:255], v[128:131]\n" \
R_"a[144:147], v28 offset:0\n" \
"v_lshrrev_b32_e32 v215, 4, v20\n" \
M_"v[144:147], a[148:151], a[188:191], v[144:147]\n" \
R_"a[208:211], v29 offset:0\n" \
"v_bfe_u32 v216, v20, 4, 3\n" \
M_"v[144:147], a[212:215], a[252:255], v[144:147]\n" \
R_"v[200:203], v199 offset:0\n" \
"s_or_b32 s42, s42, s4\n" \
M_"v[140:143], a[148:151], a[184:187], v[140:143]\n" \
R_"v[204:207], v199 offset:4096\n" \
"s_lshl_b32 s4, s43, 11\n" \
M_"v[140:143], a[212:215], a[248:251], v[140:143]\n" \
X_"a[226:227], v32 offset:9216\n" \
"s_add_i32 s52, s48, s7\n" \
M_"v[136:139], a[148:151], a[180:183], v[136:139]\n" \
X_"a[164:165], v33 offset:0\n" \
"s_add_i32 s53, s53, s7\n" \
M_"v[136:139], a[212:215], a[244:247], v[136:139]\n" \
R_"v[208:211], v199 offset:8192\n" \
"v_mul_u32_u24_e32 v214, 0x480, v214\n" \
M_"v[132:135], a[148:151], a[176:179], v[132:135]\n" \
X_"a[166:167], v33 offset:1024\n" \
"v_bitop3_b32 v218, v212, v215, 7 bitop3:0x78\n" \
M_"v[132:135], a[212:215], a[240:243], v[132:135]\n" \
R_"a[148:151], v28 offset:2048\n" \
"v_bitop3_b32 v212, v212, v216, 4 bitop3:0x36\n" \
M_"v[148:151], a[152:155], a[176:179], v[148:151]\n" \
R_"a[212:215], v29 offset:2048\n" \
"s_lshl_b32 s7, s3, 10\n" \
M_"v[148:151], a[216:219], a[240:243], v[148:151]\n" \
X_"a[228:229], v33 offset:8192\n" \
"s_or_b32 s4, s40, s4\n" \
M_"v[152:155], a[152:155], a[180:183], v[152:155]\n" \
X_"a[230:231], v33 offset:9216\n" \
"v_lshl_or_b32 v25, v212, 4, v214\n" \
M_"v[152:155], a[216:219], a[244:247], v[152:155]\n" \
X_"a[168:169], v34 offset:0\n" \
"v_mul_u32_u24_e32 v212, 0x480, v217\n" \
M_"v[156:159], a[152:155], a[184:187], v[156:159]\n" \
X_"a[170:171], v34 offset:1024\n" \
"v_bitop3_b32 v213, v215, v213, 7 bitop3:0x6c\n" \
M_"v[156:159], a[216:219], a[248:251], v[156:159]\n" \
X_"a[232:233], v34 offset:8192\n" \
"s_or_b32 s40, s7, 0x14000\n" \
M_"v[160:163], a[152:155], a[188:191], v[160:163]\n" \
X_"a[234:235], v34 offset:9216\n" \
"s_lshl_b32 s43, s44, 13\n" \
M_"v[160:163], a[216:219], a[252:255], v[160:163]\n" \
R_"a[152:155], v28 offset:4096\n" \
"s_mulk_i32 s44, 0xe800\n" \
M_"v[176:179], a[156:159], a[188:191], v[176:179]\n" \
R_"a[216:219], v29 offset:4096\n" \
"s_lshl_b32 s7, s3, 8\n" \
"s_nop 0\n" \
M_"v[176:179], a[220:223], a[252:255], v[176:179]\n" \
X_"a[172:173], v35 offset:0\n" \
"v_lshl_or_b32 v24, v218, 4, v214\n" \
M_"v[172:175], a[156:159], a[184:187], v[172:175]\n" \
X_"a[174:175], v35 offset:1024\n" \
"v_lshl_or_b32 v21, v213, 4, v212\n" \
M_"v[172:175], a[220:223], a[248:251], v[172:175]\n" \
X_"a[236:237], v35 offset:8192\n" \
"s_add_i32 s44, s44, 0x17000\n" \
M_"v[168:171], a[156:159], a[180:183], v[168:171]\n" \
X_"a[238:239], v35 offset:9216\n" \
"s_or_b32 s45, s7, 0x27000\n" \
M_"v[168:171], a[220:223], a[244:247], v[168:171]\n" \
"s_or_b32 s46, s4, 0x10000\n" \
"s_add_i32 s48, s6, 0x3000000\n" \
M_"v[164:167], a[156:159], a[176:179], v[164:167]\n" \
"s_add_i32 s49, s5, 0x3004000\n" \
".fill (L_pb + 52960 - .) / 4, 4, 0xBF800000\n" \
"L_pp13:\n" \
"s_cbranch_execz L_pret\n" \
"s_add_i32 s52, s52, 0x1010000\n" \
M_"v[164:167], a[220:223], a[240:243], v[164:167]\n" \
R_"a[156:159], v28 offset:6144\n" \
R_"a[220:223], v29 offset:6144\n" \
"s_waitcnt lgkmcnt(15)\n" \
M_"a[0:3], a[128:131], a[160:163], a[0:3]\n" \
"s_addk_i32 s84, 0x7c00\n" \
M_"a[0:3], a[192:195], a[224:227], a[0:3]\n" \
B_"v27, s[72:75], s84 offen offset:2048 lds\n" \
M_"a[4:7], a[128:131], a[164:167], a[4:7]\n" \
X_"a[176:177], v36 offset:0\n" \
"s_waitcnt lgkmcnt(13)\n" \
M_"a[4:7], a[192:195], a[228:231], a[4:7]\n" \
"s_addk_i32 s84, 0x7c00\n" \
"s_waitcnt lgkmcnt(11)\n" \
M_"a[8:11], a[128:131], a[168:171], a[8:11]\n" \
B_"v27, s[72:75], s84 offen offset:3072 lds\n" \
"s_waitcnt lgkmcnt(9)\n" \
M_"a[8:11], a[192:195], a[232:235], a[8:11]\n" \
X_"a[178:179], v36 offset:1024\n" \
"s_waitcnt lgkmcnt(6)\n" \
M_"a[12:15], a[128:131], a[172:175], a[12:15]\n" \
X_"a[240:241], v36 offset:8192\n" \
"s_waitcnt lgkmcnt(5)\n" \
"s_nop 0\n" \
M_"a[12:15], a[192:195], a[236:239], a[12:15]\n" \
M_"a[28:31], a[132:135], a[172:175], a[28:31]\n" \
M_"a[28:31], a[196:199], a[236:239], a[28:31]\n" \
X_"a[242:243], v36 offset:9216\n" \
M_"a[24:27], a[132:135], a[168:171], a[24:27]\n" \
M_"a[24:27], a[196:199], a[232:235], a[24:27]\n" \
M_"a[20:23], a[132:135], a[164:167], a[20:23]\n" \
M_"a[20:23], a[196:199], a[228:231], a[20:23]\n" \
X_"a[180:181], v37 offset:0\n" \
M_"a[16:19], a[132:135], a[160:163], a[16:19]\n" \
M_"a[16:19], a[196:199], a[224:227], a[16:19]\n" \
M_"a[32:35], a[136:139], a[160:163], a[32:35]\n" \
X_"a[182:183], v37 offset:1024\n" \
M_"a[32:35], a[200:203], a[224:227], a[32:35]\n" \
M_"a[36:39], a[136:139], a[164:167], a[36:39]\n" \
M_"a[36:39], a[200:203], a[228:231], a[36:39]\n" \
"buffer_store_dwordx4 v[200:203], v20, s[80:83], s35 offen sc1\n" \
M_"a[40:43], a[136:139], a[168:171], a[40:43]\n" \
"s_mov_b32 m0, s32\n" \
X_"a[244:245], v37 offset:8192\n" \
M_"a[40:43], a[200:203], a[232:235], a[40:43]\n" \
"s_mov_b32 exec_lo, 0x0\n" \
"s_mov_b32 exec_hi, 0xffffffff\n" \
"global_load_lds_dword v[188:189], off\n" \
"s_mov_b64 exec, -1\n" \
M_"a[44:47], a[136:139], a[172:175], a[44:47]\n" \
X_"a[246:247], v37 offset:9216\n" \
M_"a[44:47], a[200:203], a[236:239], a[44:47]\n" \
M_"a[60:63], a[140:143], a[172:175], a[60:63]\n" \
M_"a[60:63], a[204:207], a[236:239], a[60:63]\n" \
M_"a[56:59], a[140:143], a[168:171], a[56:59]\n" \
X_"a[184:185], v38 offset:0\n" \
M_"a[56:59], a[204:207], a[232:235], a[56:59]\n" \
M_"a[52:55], a[140:143], a[164:167], a[52:55]\n" \
"buffer_store_dwordx4 v[204:207], v20, s[80:83], s88 offen sc1\n" \
M_"a[52:55], a[204:207], a[228:231], a[52:55]\n" \
X_"a[186:187], v38 offset:1024\n" \
M_"a[48:51], a[140:143], a[160:163], a[48:51]\n" \
M_"a[48:51], a[204:207], a[224:227], a[48:51]\n" \
M_"a[64:67], a[144:147], a[160:163], a[64:67]\n" \
X_"a[248:249], v38 offset:8192\n" \
M_"a[64:67], a[208:211], a[224:227], a[64:67]\n" \
M_"a[68:71], a[144:147], a[164:167], a[68:71]\n" \
M_"a[68:71], a[208:211], a[228:231], a[68:71]\n" \
X_"a[250:251], v38 offset:9216\n" \
M_"a[72:75], a[144:147], a[168:171], a[72:75]\n" \
M_"a[72:75], a[208:211], a[232:235], a[72:75]\n" \
M_"a[76:79], a[144:147], a[172:175], a[76:79]\n" \
"buffer_store_dwordx4 v[208:211], v20, s[80:83], s36 offen sc1\n" \
M_"a[76:79], a[208:211], a[236:239], a[76:79]\n" \
X_"a[188:189], v39 offset:0\n" \
M_"a[92:95], a[148:151], a[172:175], a[92:95]\n" \
M_"a[92:95], a[212:215], a[236:239], a[92:95]\n" \
M_"a[88:91], a[148:151], a[168:171], a[88:91]\n" \
X_"a[190:191], v39 offset:1024\n" \
M_"a[88:91], a[212:215], a[232:235], a[88:91]\n" \
M_"a[84:87], a[148:151], a[164:167], a[84:87]\n" \
M_"a[84:87], a[212:215], a[228:231], a[84:87]\n" \
X_"a[252:253], v39 offset:8192\n" \
M_"a[80:83], a[148:151], a[160:163], a[80:83]\n" \
M_"a[80:83], a[212:215], a[224:227], a[80:83]\n" \
M_"a[96:99], a[152:155], a[160:163], a[96:99]\n" \
X_"a[254:255], v39 offset:9216\n" \
M_"a[96:99], a[216:219], a[224:227], a[96:99]\n" \
M_"a[100:103], a[152:155], a[164:167], a[100:103]\n" \
M_"a[100:103], a[216:219], a[228:231], a[100:103]\n" \
M_"a[104:107], a[152:155], a[168:171], a[104:107]\n" \
M_"a[104:107], a[216:219], a[232:235], a[104:107]\n" \
M_"a[108:111], a[152:155], a[172:175], a[108:111]\n" \
M_"a[108:111], a[216:219], a[236:239], a[108:111]\n" \
"s_waitcnt lgkmcnt(15)\n" \
M_"a[124:127], a[156:159], a[172:175], a[124:127]\n" \
M_"a[124:127], a[220:223], a[236:239], a[124:127]\n" \
M_"a[120:123], a[156:159], a[168:171], a[120:123]\n" \
M_"a[120:123], a[220:223], a[232:235], a[120:123]\n" \
M_"a[116:119], a[156:159], a[164:167], a[116:119]\n" \
M_"a[116:119], a[220:223], a[228:231], a[116:119]\n" \
M_"a[112:115], a[156:159], a[160:163], a[112:115]\n" \
"s_waitcnt vmcnt(4) lgkmcnt(0)\n" \
M_"a[112:115], a[220:223], a[224:227], a[112:115]\n" \
"s_barrier\n" \
M_"v[52:55], a[128:131], a[176:179], v[52:55]\n" \
"s_add_u32 m0, s30, 0x0\n" \
"s_movk_i32 s84, 0x300\n" \
M_"v[52:55], a[192:195], a[240:243], v[52:55]\n" \
B_"v22, s[64:67], s84 offen offset:0 lds\n" \
M_"v[56:59], a[128:131], a[180:183], v[56:59]\n" \
X_"a[160:161], v32 offset:32768\n" \
M_"v[56:59], a[192:195], a[244:247], v[56:59]\n" \
"s_movk_i32 s84, 0x1f00\n" \
M_"v[60:63], a[128:131], a[184:187], v[60:63]\n" \
B_"v23, s[64:67], s84 offen offset:1024 lds\n" \
M_"v[60:63], a[192:195], a[248:251], v[60:63]\n" \
X_"a[162:163], v32 offset:33792\n" \
M_"v[64:67], a[128:131], a[188:191], v[64:67]\n" \
X_"a[224:225], v32 offset:40960\n" \
M_"v[64:67], a[192:195], a[252:255], v[64:67]\n" \
R_"a[128:131], v28 offset:49152\n" \
M_"v[80:83], a[132:135], a[188:191], v[80:83]\n" \
R_"a[192:195], v29 offset:49152\n" \
M_"v[80:83], a[196:199], a[252:255], v[80:83]\n" \
"s_movk_i32 s84, 0x3b00\n" \
M_"v[76:79], a[132:135], a[184:187], v[76:79]\n" \
B_"v22, s[64:67], s84 offen offset:2048 lds\n" \
M_"v[76:79], a[196:199], a[248:251], v[76:79]\n" \
"s_movk_i32 s84, 0x5700\n" \
M_"v[72:75], a[132:135], a[180:183], v[72:75]\n" \
B_"v23, s[64:67], s84 offen offset:3072 lds\n" \
M_"v[72:75], a[196:199], a[244:247], v[72:75]\n" \
"s_add_u32 m0, s31, 0x0\n" \
"s_mov_b32 s84, 0x300000\n" \
M_"v[68:71], a[132:135], a[176:179], v[68:71]\n" \
B_"v26, s[68:71], s84 offen offset:0 lds\n" \
M_"v[68:71], a[196:199], a[240:243], v[68:71]\n" \
R_"a[132:135], v28 offset:51200\n" \
M_"v[84:87], a[136:139], a[176:179], v[84:87]\n" \
R_"a[196:199], v29 offset:51200\n" \
M_"v[84:87], a[200:203], a[240:243], v[84:87]\n" \
"s_addk_i32 s84, 0x7c00\n" \
M_"v[88:91], a[136:139], a[180:183], v[88:91]\n" \
B_"v26, s[68:71], s84 offen offset:1024 lds\n" \
M_"v[88:91], a[200:203], a[244:247], v[88:91]\n" \
X_"a[226:227], v32 offset:41984\n" \
M_"v[92:95], a[136:139], a[184:187], v[92:95]\n" \
"s_addk_i32 s84, 0x7c00\n" \
M_"v[92:95], a[200:203], a[248:251], v[92:95]\n" \
B_"v27, s[68:71], s84 offen offset:2048 lds\n" \
M_"v[96:99], a[136:139], a[188:191], v[96:99]\n" \
X_"a[164:165], v33 offset:32768\n" \
M_"v[96:99], a[200:203], a[252:255], v[96:99]\n" \
R_"a[136:139], v28 offset:53248\n" \
M_"v[112:115], a[140:143], a[188:191], v[112:115]\n" \
R_"a[200:203], v29 offset:53248\n" \
M_"v[112:115], a[204:207], a[252:255], v[112:115]\n" \
"s_addk_i32 s84, 0x7c00\n" \
M_"v[108:111], a[140:143], a[184:187], v[108:111]\n" \
B_"v27, s[68:71], s84 offen offset:3072 lds\n" \
M_"v[108:111], a[204:207], a[248:251], v[108:111]\n" \
"s_add_u32 m0, s30, 0x1000\n" \
"s_mov_b32 s84, 0x8300\n" \
M_"v[104:107], a[140:143], a[180:183], v[104:107]\n" \
B_"v22, s[64:67], s84 offen offset:0 lds\n" \
M_"v[104:107], a[204:207], a[244:247], v[104:107]\n" \
X_"a[166:167], v33 offset:33792\n" \
M_"v[100:103], a[140:143], a[176:179], v[100:103]\n" \
X_"a[228:229], v33 offset:40960\n" \
M_"v[100:103], a[204:207], a[240:243], v[100:103]\n" \
R_"a[140:143], v28 offset:55296\n" \
M_"v[116:119], a[144:147], a[176:179], v[116:119]\n" \
R_"a[204:207], v29 offset:55296\n" \
M_"v[116:119], a[208:211], a[240:243], v[116:119]\n" \
"s_addk_i32 s84, 0x1c00\n" \
M_"v[120:123], a[144:147], a[180:183], v[120:123]\n" \
B_"v23, s[64:67], s84 offen offset:1024 lds\n" \
M_"v[120:123], a[208:211], a[244:247], v[120:123]\n" \
"s_addk_i32 s84, 0x1c00\n" \
M_"v[124:127], a[144:147], a[184:187], v[124:127]\n" \
B_"v22, s[64:67], s84 offen offset:2048 lds\n" \
M_"v[124:127], a[208:211], a[248:251], v[124:127]\n" \
"s_addk_i32 s84, 0x1c00\n" \
"s_nop 0\n" \
M_"v[128:131], a[144:147], a[188:191], v[128:131]\n" \
B_"v23, s[64:67], s84 offen offset:3072 lds\n" \
M_"v[128:131], a[208:211], a[252:255], v[128:131]\n" \
R_"a[144:147], v28 offset:32768\n" \
M_"v[144:147], a[148:151], a[188:191], v[144:147]\n" \
R_"a[208:211], v29 offset:32768\n" \
M_"v[144:147], a[212:215], a[252:255], v[144:147]\n" \
"s_add_u32 m0, s31, 0x4000\n" \
"s_mov_b32 s84, 0x300000\n" \
M_"v[140:143], a[148:151], a[184:187], v[140:143]\n" \
B_"v26, s[72:75], s84 offen offset:0 lds\n" \
M_"v[140:143], a[212:215], a[248:251], v[140:143]\n" \
X_"a[230:231], v33 offset:41984\n" \
M_"v[136:139], a[148:151], a[180:183], v[136:139]\n" \
"s_addk_i32 s84, 0x7c00\n" \
"s_nop 0\n" \
M_"v[136:139], a[212:215], a[244:247], v[136:139]\n" \
B_"v26, s[72:75], s84 offen offset:1024 lds\n" \
M_"v[132:135], a[148:151], a[176:179], v[132:135]\n" \
X_"a[168:169], v34 offset:32768\n" \
M_"v[132:135], a[212:215], a[240:243], v[132:135]\n" \
R_"a[148:151], v28 offset:34816\n" \
M_"v[148:151], a[152:155], a[176:179], v[148:151]\n" \
R_"a[212:215], v29 offset:34816\n" \
M_"v[148:151], a[216:219], a[240:243], v[148:151]\n" \
X_"a[170:171], v34 offset:33792\n" \
M_"v[152:155], a[152:155], a[180:183], v[152:155]\n" \
X_"a[232:233], v34 offset:40960\n" \
M_"v[152:155], a[216:219], a[244:247], v[152:155]\n" \
X_"a[234:235], v34 offset:41984\n" \
M_"v[156:159], a[152:155], a[184:187], v[156:159]\n" \
X_"a[172:173], v35 offset:32768\n" \
M_"v[156:159], a[216:219], a[248:251], v[156:159]\n" \
X_"a[174:175], v35 offset:33792\n" \
M_"v[160:163], a[152:155], a[188:191], v[160:163]\n" \
X_"a[236:237], v35 offset:40960\n" \
M_"v[160:163], a[216:219], a[252:255], v[160:163]\n" \
R_"a[152:155], v28 offset:36864\n" \
M_"v[176:179], a[156:159], a[188:191], v[176:179]\n" \
R_"a[216:219], v29 offset:36864\n" \
M_"v[176:179], a[220:223], a[252:255], v[176:179]\n" \
X_"a[238:239], v35 offset:41984\n" \
M_"v[172:175], a[156:159], a[184:187], v[172:175]\n" \
M_"v[172:175], a[220:223], a[248:251], v[172:175]\n" \
M_"v[168:171], a[156:159], a[180:183], v[168:171]\n" \
M_"v[168:171], a[220:223], a[244:247], v[168:171]\n" \
M_"v[164:167], a[156:159], a[176:179], v[164:167]\n" \
M_"v[164:167], a[220:223], a[240:243], v[164:167]\n" \
R_"a[156:159], v28 offset:38912\n" \
R_"a[220:223], v29 offset:38912\n" \
"s_waitcnt lgkmcnt(15)\n" \
M_"a[0:3], a[128:131], a[160:163], a[0:3]\n" \
"s_addk_i32 s84, 0x7c00\n" \
M_"a[0:3], a[192:195], a[224:227], a[0:3]\n" \
B_"v27, s[72:75], s84 offen offset:2048 lds\n" \
M_"a[4:7], a[128:131], a[164:167], a[4:7]\n" \
X_"a[176:177], v36 offset:32768\n" \
"s_waitcnt lgkmcnt(15)\n" \
M_"a[4:7], a[192:195], a[228:231], a[4:7]\n" \
"s_addk_i32 s84, 0x7c00\n" \
"s_waitcnt lgkmcnt(11)\n" \
M_"a[8:11], a[128:131], a[168:171], a[8:11]\n" \
B_"v27, s[72:75], s84 offen offset:3072 lds\n" \
"s_waitcnt lgkmcnt(9)\n" \
M_"a[8:11], a[192:195], a[232:235], a[8:11]\n" \
X_"a[178:179], v36 offset:33792\n" \
"s_waitcnt lgkmcnt(8)\n" \
M_"a[12:15], a[128:131], a[172:175], a[12:15]\n" \
X_"a[240:241], v36 offset:40960\n" \
"s_waitcnt lgkmcnt(5)\n" \
M_"a[12:15], a[192:195], a[236:239], a[12:15]\n" \
M_"a[28:31], a[132:135], a[172:175], a[28:31]\n" \
M_"a[28:31], a[196:199], a[236:239], a[28:31]\n" \
X_"a[242:243], v36 offset:41984\n" \
M_"a[24:27], a[132:135], a[168:171], a[24:27]\n" \
M_"a[24:27], a[196:199], a[232:235], a[24:27]\n" \
M_"a[20:23], a[132:135], a[164:167], a[20:23]\n" \
M_"a[20:23], a[196:199], a[228:231], a[20:23]\n" \
X_"a[180:181], v37 offset:32768\n" \
M_"a[16:19], a[132:135], a[160:163], a[16:19]\n" \
M_"a[16:19], a[196:199], a[224:227], a[16:19]\n" \
M_"a[32:35], a[136:139], a[160:163], a[32:35]\n" \
X_"a[182:183], v37 offset:33792\n" \
M_"a[32:35], a[200:203], a[224:227], a[32:35]\n" \
M_"a[36:39], a[136:139], a[164:167], a[36:39]\n" \
"v_lshlrev_b32_e32 v45, 3, v20\n" \
"v_add_u32_e32 v45, s31, v45\n" \
M_"a[36:39], a[200:203], a[228:231], a[36:39]\n" \
"s_getpc_b64 s[86:87]\n" \
"s_sub_u32 s86, s86, 0x10800\n" \
"s_subb_u32 s87, s87, 0\n" \
"s_mov_b32 m0, s32\n" \
"s_nop 0\n" \
M_"a[40:43], a[136:139], a[168:171], a[40:43]\n" \
"global_load_lds_dword v45, s[86:87]\n" \
M_"a[40:43], a[200:203], a[232:235], a[40:43]\n" \
X_"a[244:245], v37 offset:40960\n" \
M_"a[44:47], a[136:139], a[172:175], a[44:47]\n" \
X_"a[246:247], v37 offset:41984\n" \
M_"a[44:47], a[200:203], a[236:239], a[44:47]\n" \
M_"a[60:63], a[140:143], a[172:175], a[60:63]\n" \
M_"a[60:63], a[204:207], a[236:239], a[60:63]\n" \
M_"a[56:59], a[140:143], a[168:171], a[56:59]\n" \
X_"a[184:185], v38 offset:32768\n" \
M_"a[56:59], a[204:207], a[232:235], a[56:59]\n" \
M_"a[52:55], a[140:143], a[164:167], a[52:55]\n" \
M_"a[52:55], a[204:207], a[228:231], a[52:55]\n" \
X_"a[186:187], v38 offset:33792\n" \
M_"a[48:51], a[140:143], a[160:163], a[48:51]\n" \
M_"a[48:51], a[204:207], a[224:227], a[48:51]\n" \
M_"a[64:67], a[144:147], a[160:163], a[64:67]\n" \
X_"a[248:249], v38 offset:40960\n" \
M_"a[64:67], a[208:211], a[224:227], a[64:67]\n" \
M_"a[68:71], a[144:147], a[164:167], a[68:71]\n" \
M_"a[68:71], a[208:211], a[228:231], a[68:71]\n" \
X_"a[250:251], v38 offset:41984\n" \
M_"a[72:75], a[144:147], a[168:171], a[72:75]\n" \
M_"a[72:75], a[208:211], a[232:235], a[72:75]\n" \
M_"a[76:79], a[144:147], a[172:175], a[76:79]\n" \
M_"a[76:79], a[208:211], a[236:239], a[76:79]\n" \
X_"a[188:189], v39 offset:32768\n" \
M_"a[92:95], a[148:151], a[172:175], a[92:95]\n" \
M_"a[92:95], a[212:215], a[236:239], a[92:95]\n" \
M_"a[88:91], a[148:151], a[168:171], a[88:91]\n" \
X_"a[190:191], v39 offset:33792\n" \
M_"a[88:91], a[212:215], a[232:235], a[88:91]\n" \
M_"a[84:87], a[148:151], a[164:167], a[84:87]\n" \
M_"a[84:87], a[212:215], a[228:231], a[84:87]\n" \
X_"a[252:253], v39 offset:40960\n" \
M_"a[80:83], a[148:151], a[160:163], a[80:83]\n" \
M_"a[80:83], a[212:215], a[224:227], a[80:83]\n" \
M_"a[96:99], a[152:155], a[160:163], a[96:99]\n" \
X_"a[254:255], v39 offset:41984\n" \
M_"a[96:99], a[216:219], a[224:227], a[96:99]\n" \
M_"a[100:103], a[152:155], a[164:167], a[100:103]\n" \
M_"a[100:103], a[216:219], a[228:231], a[100:103]\n" \
M_"a[104:107], a[152:155], a[168:171], a[104:107]\n" \
M_"a[104:107], a[216:219], a[232:235], a[104:107]\n" \
M_"a[108:111], a[152:155], a[172:175], a[108:111]\n" \
M_"a[108:111], a[216:219], a[236:239], a[108:111]\n" \
"s_waitcnt lgkmcnt(15)\n" \
M_"a[124:127], a[156:159], a[172:175], a[124:127]\n" \
M_"a[124:127], a[220:223], a[236:239], a[124:127]\n" \
M_"a[120:123], a[156:159], a[168:171], a[120:123]\n" \
M_"a[120:123], a[220:223], a[232:235], a[120:123]\n" \
M_"a[116:119], a[156:159], a[164:167], a[116:119]\n" \
M_"a[116:119], a[220:223], a[228:231], a[116:119]\n" \
M_"a[112:115], a[156:159], a[160:163], a[112:115]\n" \
"s_waitcnt vmcnt(1) lgkmcnt(0)\n" \
M_"a[112:115], a[220:223], a[224:227], a[112:115]\n" \
"s_barrier\n" \
"v_mov_b32_e32 v45, s62\n" \
"s_mov_b64 exec, 1\n" \
"buffer_store_dword v45, off, s[80:83], s37 offset:128 sc1\n" \
"s_mov_b64 exec, -1\n" \
M_"v[52:55], a[128:131], a[176:179], v[52:55]\n" \
"s_add_u32 m0, s30, 0x8000\n" \
"s_movk_i32 s84, 0x380\n" \
M_"v[52:55], a[192:195], a[240:243], v[52:55]\n" \
B_"v22, s[64:67], s84 offen offset:0 lds\n" \
M_"v[56:59], a[128:131], a[180:183], v[56:59]\n" \
X_"a[160:161], v32 offset:0\n" \
M_"v[56:59], a[192:195], a[244:247], v[56:59]\n" \
"s_movk_i32 s84, 0x1f80\n" \
M_"v[60:63], a[128:131], a[184:187], v[60:63]\n" \
B_"v23, s[64:67], s84 offen offset:1024 lds\n" \
M_"v[60:63], a[192:195], a[248:251], v[60:63]\n" \
X_"a[162:163], v32 offset:1024\n" \
M_"v[64:67], a[128:131], a[188:191], v[64:67]\n" \
X_"a[224:225], v32 offset:8192\n" \
M_"v[64:67], a[192:195], a[252:255], v[64:67]\n" \
R_"a[128:131], v28 offset:16384\n" \
M_"v[80:83], a[132:135], a[188:191], v[80:83]\n" \
R_"a[192:195], v29 offset:16384\n" \
M_"v[80:83], a[196:199], a[252:255], v[80:83]\n" \
"s_movk_i32 s84, 0x3b80\n" \
M_"v[76:79], a[132:135], a[184:187], v[76:79]\n" \
B_"v22, s[64:67], s84 offen offset:2048 lds\n" \
M_"v[76:79], a[196:199], a[248:251], v[76:79]\n" \
"s_movk_i32 s84, 0x5780\n" \
M_"v[72:75], a[132:135], a[180:183], v[72:75]\n" \
B_"v23, s[64:67], s84 offen offset:3072 lds\n" \
M_"v[72:75], a[196:199], a[244:247], v[72:75]\n" \
"s_add_u32 m0, s31, 0x8000\n" \
"s_mov_b32 s84, 0x380000\n" \
M_"v[68:71], a[132:135], a[176:179], v[68:71]\n" \
B_"v26, s[68:71], s84 offen offset:0 lds\n" \
M_"v[68:71], a[196:199], a[240:243], v[68:71]\n" \
R_"a[132:135], v28 offset:18432\n" \
M_"v[84:87], a[136:139], a[176:179], v[84:87]\n" \
R_"a[196:199], v29 offset:18432\n" \
M_"v[84:87], a[200:203], a[240:243], v[84:87]\n" \
"s_addk_i32 s84, 0x7c00\n" \
M_"v[88:91], a[136:139], a[180:183], v[88:91]\n" \
B_"v26, s[68:71], s84 offen offset:1024 lds\n" \
M_"v[88:91], a[200:203], a[244:247], v[88:91]\n" \
X_"a[226:227], v32 offset:9216\n" \
M_"v[92:95], a[136:139], a[184:187], v[92:95]\n" \
"s_addk_i32 s84, 0x7c00\n" \
M_"v[92:95], a[200:203], a[248:251], v[92:95]\n" \
B_"v27, s[68:71], s84 offen offset:2048 lds\n" \
M_"v[96:99], a[136:139], a[188:191], v[96:99]\n" \
X_"a[164:165], v33 offset:0\n" \
M_"v[96:99], a[200:203], a[252:255], v[96:99]\n" \
R_"a[136:139], v28 offset:20480\n" \
M_"v[112:115], a[140:143], a[188:191], v[112:115]\n" \
R_"a[200:203], v29 offset:20480\n" \
M_"v[112:115], a[204:207], a[252:255], v[112:115]\n" \
"s_addk_i32 s84, 0x7c00\n" \
"s_nop 0\n" \
M_"v[108:111], a[140:143], a[184:187], v[108:111]\n" \
B_"v27, s[68:71], s84 offen offset:3072 lds\n" \
M_"v[108:111], a[204:207], a[248:251], v[108:111]\n" \
"s_add_u32 m0, s30, 0x9000\n" \
"s_mov_b32 s84, 0x8380\n" \
M_"v[104:107], a[140:143], a[180:183], v[104:107]\n" \
B_"v22, s[64:67], s84 offen offset:0 lds\n" \
M_"v[104:107], a[204:207], a[244:247], v[104:107]\n" \
X_"a[166:167], v33 offset:1024\n" \
M_"v[100:103], a[140:143], a[176:179], v[100:103]\n" \
X_"a[228:229], v33 offset:8192\n" \
M_"v[100:103], a[204:207], a[240:243], v[100:103]\n" \
R_"a[140:143], v28 offset:22528\n" \
M_"v[116:119], a[144:147], a[176:179], v[116:119]\n" \
R_"a[204:207], v29 offset:22528\n" \
M_"v[116:119], a[208:211], a[240:243], v[116:119]\n" \
"s_addk_i32 s84, 0x1c00\n" \
M_"v[120:123], a[144:147], a[180:183], v[120:123]\n" \
B_"v23, s[64:67], s84 offen offset:1024 lds\n" \
M_"v[120:123], a[208:211], a[244:247], v[120:123]\n" \
"s_addk_i32 s84, 0x1c00\n" \
M_"v[124:127], a[144:147], a[184:187], v[124:127]\n" \
B_"v22, s[64:67], s84 offen offset:2048 lds\n" \
M_"v[124:127], a[208:211], a[248:251], v[124:127]\n" \
"s_addk_i32 s84, 0x1c00\n" \
"s_nop 0\n" \
M_"v[128:131], a[144:147], a[188:191], v[128:131]\n" \
B_"v23, s[64:67], s84 offen offset:3072 lds\n" \
M_"v[128:131], a[208:211], a[252:255], v[128:131]\n" \
R_"a[144:147], v28 offset:0\n" \
M_"v[144:147], a[148:151], a[188:191], v[144:147]\n" \
R_"a[208:211], v29 offset:0\n" \
M_"v[144:147], a[212:215], a[252:255], v[144:147]\n" \
"s_add_u32 m0, s31, 0xc000\n" \
"s_mov_b32 s84, 0x380000\n" \
M_"v[140:143], a[148:151], a[184:187], v[140:143]\n" \
B_"v26, s[72:75], s84 offen offset:0 lds\n" \
M_"v[140:143], a[212:215], a[248:251], v[140:143]\n" \
X_"a[230:231], v33 offset:9216\n" \
M_"v[136:139], a[148:151], a[180:183], v[136:139]\n" \
"s_addk_i32 s84, 0x7c00\n" \
"s_nop 0\n" \
M_"v[136:139], a[212:215], a[244:247], v[136:139]\n" \
B_"v26, s[72:75], s84 offen offset:1024 lds\n" \
M_"v[132:135], a[148:151], a[176:179], v[132:135]\n" \
X_"a[168:169], v34 offset:0\n" \
M_"v[132:135], a[212:215], a[240:243], v[132:135]\n" \
R_"a[148:151], v28 offset:2048\n" \
M_"v[148:151], a[152:155], a[176:179], v[148:151]\n" \
R_"a[212:215], v29 offset:2048\n" \
M_"v[148:151], a[216:219], a[240:243], v[148:151]\n" \
X_"a[170:171], v34 offset:1024\n" \
M_"v[152:155], a[152:155], a[180:183], v[152:155]\n" \
X_"a[232:233], v34 offset:8192\n" \
M_"v[152:155], a[216:219], a[244:247], v[152:155]\n" \
X_"a[234:235], v34 offset:9216\n" \
M_"v[156:159], a[152:155], a[184:187], v[156:159]\n" \
X_"a[172:173], v35 offset:0\n" \
M_"v[156:159], a[216:219], a[248:251], v[156:159]\n" \
X_"a[174:175], v35 offset:1024\n" \
M_"v[160:163], a[152:155], a[188:191], v[160:163]\n" \
X_"a[236:237], v35 offset:8192\n" \
M_"v[160:163], a[216:219], a[252:255], v[160:163]\n" \
R_"a[152:155], v28 offset:4096\n" \
M_"v[176:179], a[156:159], a[188:191], v[176:179]\n" \
R_"a[216:219], v29 offset:4096\n" \
M_"v[176:179], a[220:223], a[252:255], v[176:179]\n" \
X_"a[238:239], v35 offset:9216\n" \
M_"v[172:175], a[156:159], a[184:187], v[172:175]\n" \
M_"v[172:175], a[220:223], a[248:251], v[172:175]\n" \
M_"v[168:171], a[156:159], a[180:183], v[168:171]\n" \
M_"v[168:171], a[220:223], a[244:247], v[168:171]\n" \
M_"v[164:167], a[156:159], a[176:179], v[164:167]\n" \
M_"v[164:167], a[220:223], a[240:243], v[164:167]\n" \
R_"a[156:159], v28 offset:6144\n" \
R_"a[220:223], v29 offset:6144\n" \
"s_waitcnt lgkmcnt(15)\n" \
M_"a[0:3], a[128:131], a[160:163], a[0:3]\n" \
"s_addk_i32 s84, 0x7c00\n" \
M_"a[0:3], a[192:195], a[224:227], a[0:3]\n" \
B_"v27, s[72:75], s84 offen offset:2048 lds\n" \
M_"a[4:7], a[128:131], a[164:167], a[4:7]\n" \
X_"a[176:177], v36 offset:0\n" \
"s_waitcnt lgkmcnt(15)\n" \
M_"a[4:7], a[192:195], a[228:231], a[4:7]\n" \
"s_addk_i32 s84, 0x7c00\n" \
"s_waitcnt lgkmcnt(11)\n" \
M_"a[8:11], a[128:131], a[168:171], a[8:11]\n" \
B_"v27, s[72:75], s84 offen offset:3072 lds\n" \
"s_waitcnt lgkmcnt(9)\n" \
M_"a[8:11], a[192:195], a[232:235], a[8:11]\n" \
X_"a[178:179], v36 offset:1024\n" \
"s_waitcnt lgkmcnt(8)\n" \
M_"a[12:15], a[128:131], a[172:175], a[12:15]\n" \
X_"a[240:241], v36 offset:8192\n" \
"s_waitcnt lgkmcnt(5)\n" \
M_"a[12:15], a[192:195], a[236:239], a[12:15]\n" \
M_"a[28:31], a[132:135], a[172:175], a[28:31]\n" \
M_"a[28:31], a[196:199], a[236:239], a[28:31]\n" \
X_"a[242:243], v36 offset:9216\n" \
M_"a[24:27], a[132:135], a[168:171], a[24:27]\n" \
M_"a[24:27], a[196:199], a[232:235], a[24:27]\n" \
M_"a[20:23], a[132:135], a[164:167], a[20:23]\n" \
M_"a[20:23], a[196:199], a[228:231], a[20:23]\n" \
X_"a[180:181], v37 offset:0\n" \
M_"a[16:19], a[132:135], a[160:163], a[16:19]\n" \
M_"a[16:19], a[196:199], a[224:227], a[16:19]\n" \
M_"a[32:35], a[136:139], a[160:163], a[32:35]\n" \
"v_bfe_u32 v44, v20, 2, 5\n" \
X_"a[182:183], v37 offset:1024\n" \
M_"a[32:35], a[200:203], a[224:227], a[32:35]\n" \
"buffer_load_dword v44, v44, s[80:83], s38 offen offset:128 sc1\n" \
M_"a[36:39], a[136:139], a[164:167], a[36:39]\n" \
M_"a[36:39], a[200:203], a[228:231], a[36:39]\n" \
X_"a[244:245], v37 offset:8192\n" \
M_"a[40:43], a[136:139], a[168:171], a[40:43]\n" \
M_"a[40:43], a[200:203], a[232:235], a[40:43]\n" \
M_"a[44:47], a[136:139], a[172:175], a[44:47]\n" \
X_"a[246:247], v37 offset:9216\n" \
M_"a[44:47], a[200:203], a[236:239], a[44:47]\n" \
M_"a[60:63], a[140:143], a[172:175], a[60:63]\n" \
"global_store_dwordx4 v49, v[0:3], s[28:29] offset:0 sc1\n" \
M_"a[60:63], a[204:207], a[236:239], a[60:63]\n" \
M_"a[56:59], a[140:143], a[168:171], a[56:59]\n" \
X_"a[184:185], v38 offset:0\n" \
M_"a[56:59], a[204:207], a[232:235], a[56:59]\n" \
M_"a[52:55], a[140:143], a[164:167], a[52:55]\n" \
M_"a[52:55], a[204:207], a[228:231], a[52:55]\n" \
X_"a[186:187], v38 offset:1024\n" \
M_"a[48:51], a[140:143], a[160:163], a[48:51]\n" \
M_"a[48:51], a[204:207], a[224:227], a[48:51]\n" \
M_"a[64:67], a[144:147], a[160:163], a[64:67]\n" \
"global_store_dwordx4 v49, v[0:3], s[28:29] offset:1024 sc1\n" \
M_"a[64:67], a[208:211], a[224:227], a[64:67]\n" \
X_"a[248:249], v38 offset:8192\n" \
M_"a[68:71], a[144:147], a[164:167], a[68:71]\n" \
M_"a[68:71], a[208:211], a[228:231], a[68:71]\n" \
X_"a[250:251], v38 offset:9216\n" \
M_"a[72:75], a[144:147], a[168:171], a[72:75]\n" \
M_"a[72:75], a[208:211], a[232:235], a[72:75]\n" \
M_"a[76:79], a[144:147], a[172:175], a[76:79]\n" \
M_"a[76:79], a[208:211], a[236:239], a[76:79]\n" \
X_"a[188:189], v39 offset:0\n" \
M_"a[92:95], a[148:151], a[172:175], a[92:95]\n" \
"s_mov_b64 exec, 0xffff\n" \
"global_store_dwordx4 v49, v[0:3], s[28:29] offset:2048 sc1\n" \
"s_mov_b64 exec, -1\n" \
"s_nop 0\n" \
M_"a[92:95], a[212:215], a[236:239], a[92:95]\n" \
M_"a[88:91], a[148:151], a[168:171], a[88:91]\n" \
X_"a[190:191], v39 offset:1024\n" \
M_"a[88:91], a[212:215], a[232:235], a[88:91]\n" \
M_"a[84:87], a[148:151], a[164:167], a[84:87]\n" \
".fill (L_pb + 57056 - .) / 4, 4, 0xBF800000\n" \
"L_pp14:\n" \
"s_cbranch_execz L_pret\n" \
"s_nop 0\n" \
M_"a[84:87], a[212:215], a[228:231], a[84:87]\n" \
X_"a[252:253], v39 offset:8192\n" \
M_"a[80:83], a[148:151], a[160:163], a[80:83]\n" \
M_"a[80:83], a[212:215], a[224:227], a[80:83]\n" \
M_"a[96:99], a[152:155], a[160:163], a[96:99]\n" \
X_"a[254:255], v39 offset:9216\n" \
M_"a[96:99], a[216:219], a[224:227], a[96:99]\n" \
M_"a[100:103], a[152:155], a[164:167], a[100:103]\n" \
M_"a[100:103], a[216:219], a[228:231], a[100:103]\n" \
M_"a[104:107], a[152:155], a[168:171], a[104:107]\n" \
M_"a[104:107], a[216:219], a[232:235], a[104:107]\n" \
M_"a[108:111], a[152:155], a[172:175], a[108:111]\n" \
M_"a[108:111], a[216:219], a[236:239], a[108:111]\n" \
"s_waitcnt lgkmcnt(15)\n" \
M_"a[124:127], a[156:159], a[172:175], a[124:127]\n" \
M_"a[124:127], a[220:223], a[236:239], a[124:127]\n" \
M_"a[120:123], a[156:159], a[168:171], a[120:123]\n" \
M_"a[120:123], a[220:223], a[232:235], a[120:123]\n" \
M_"a[116:119], a[156:159], a[164:167], a[116:119]\n" \
M_"a[116:119], a[220:223], a[228:231], a[116:119]\n" \
M_"a[112:115], a[156:159], a[160:163], a[112:115]\n" \
"s_waitcnt vmcnt(3) lgkmcnt(0)\n" \
M_"a[112:115], a[220:223], a[224:227], a[112:115]\n" \
"s_barrier\n" \
"v_cmp_ne_u32_e32 vcc, s62, v44\n" \
"s_nop 4\n" \
"s_cbranch_vccnz L_poll61_%=\n" \
"L_pollr61_%=:\n" \
"s_mov_b32 s68, s12\n" \
"s_and_b32 s69, s13, 0xffff\n" \
"s_mov_b32 s70, 0x480000\n" \
"s_mov_b32 s71, 0x20000\n" \
M_"v[52:55], a[128:131], a[176:179], v[52:55]\n" \
"s_add_u32 m0, s40, 0x0\n" \
"s_add_u32 s85, s39, 0x0\n" \
"s_nop 0\n" \
M_"v[52:55], a[192:195], a[240:243], v[52:55]\n" \
B_"v20, s[80:83], s85 offen sc1 lds\n" \
M_"v[56:59], a[128:131], a[180:183], v[56:59]\n" \
X_"a[160:161], v32 offset:32768\n" \
M_"v[56:59], a[192:195], a[244:247], v[56:59]\n" \
"s_add_u32 m0, s40, 0x1000\n" \
"s_add_u32 s85, s39, 0x1000\n" \
M_"v[60:63], a[128:131], a[184:187], v[60:63]\n" \
B_"v20, s[80:83], s85 offen sc1 lds\n" \
M_"v[60:63], a[192:195], a[248:251], v[60:63]\n" \
X_"a[162:163], v32 offset:33792\n" \
M_"v[64:67], a[128:131], a[188:191], v[64:67]\n" \
X_"a[224:225], v32 offset:40960\n" \
M_"v[64:67], a[192:195], a[252:255], v[64:67]\n" \
R_"a[128:131], v28 offset:49152\n" \
M_"v[80:83], a[132:135], a[188:191], v[80:83]\n" \
R_"a[192:195], v29 offset:49152\n" \
M_"v[80:83], a[196:199], a[252:255], v[80:83]\n" \
"s_add_u32 m0, s40, 0x3000\n" \
"s_movk_i32 s84, 0x0\n" \
M_"v[76:79], a[132:135], a[184:187], v[76:79]\n" \
B_"v21, s[76:79], s84 offen offset:0 lds\n" \
M_"v[76:79], a[196:199], a[248:251], v[76:79]\n" \
"s_add_u32 m0, s30, 0x0\n" \
"s_movk_i32 s84, 0x0\n" \
"s_nop 0\n" \
M_"v[72:75], a[132:135], a[180:183], v[72:75]\n" \
B_"v24, s[76:79], s84 offen offset:0 lds\n" \
M_"v[72:75], a[196:199], a[244:247], v[72:75]\n" \
X_"a[226:227], v32 offset:41984\n" \
M_"v[68:71], a[132:135], a[176:179], v[68:71]\n" \
X_"a[164:165], v33 offset:32768\n" \
M_"v[68:71], a[196:199], a[240:243], v[68:71]\n" \
R_"a[132:135], v28 offset:51200\n" \
M_"v[84:87], a[136:139], a[176:179], v[84:87]\n" \
R_"a[196:199], v29 offset:51200\n" \
M_"v[84:87], a[200:203], a[240:243], v[84:87]\n" \
"s_movk_i32 s84, 0x2000\n" \
M_"v[88:91], a[136:139], a[180:183], v[88:91]\n" \
B_"v25, s[76:79], s84 offen offset:1024 lds\n" \
M_"v[88:91], a[200:203], a[244:247], v[88:91]\n" \
"s_movk_i32 s84, 0x4000\n" \
M_"v[92:95], a[136:139], a[184:187], v[92:95]\n" \
B_"v24, s[76:79], s84 offen offset:2048 lds\n" \
M_"v[92:95], a[200:203], a[248:251], v[92:95]\n" \
X_"a[166:167], v33 offset:33792\n" \
M_"v[96:99], a[136:139], a[188:191], v[96:99]\n" \
X_"a[228:229], v33 offset:40960\n" \
M_"v[96:99], a[200:203], a[252:255], v[96:99]\n" \
R_"a[136:139], v28 offset:53248\n" \
M_"v[112:115], a[140:143], a[188:191], v[112:115]\n" \
R_"a[200:203], v29 offset:53248\n" \
M_"v[112:115], a[204:207], a[252:255], v[112:115]\n" \
"s_movk_i32 s84, 0x6000\n" \
M_"v[108:111], a[140:143], a[184:187], v[108:111]\n" \
B_"v25, s[76:79], s84 offen offset:3072 lds\n" \
M_"v[108:111], a[204:207], a[248:251], v[108:111]\n" \
"s_add_u32 m0, s31, 0x0\n" \
"s_movk_i32 s84, 0x0\n" \
M_"v[104:107], a[140:143], a[180:183], v[104:107]\n" \
B_"v26, s[68:71], s84 offen offset:0 lds\n" \
M_"v[104:107], a[204:207], a[244:247], v[104:107]\n" \
"s_movk_i32 s84, 0x7c00\n" \
M_"v[100:103], a[140:143], a[176:179], v[100:103]\n" \
B_"v26, s[68:71], s84 offen offset:1024 lds\n" \
M_"v[100:103], a[204:207], a[240:243], v[100:103]\n" \
R_"a[140:143], v28 offset:55296\n" \
M_"v[116:119], a[144:147], a[176:179], v[116:119]\n" \
R_"a[204:207], v29 offset:55296\n" \
M_"v[116:119], a[208:211], a[240:243], v[116:119]\n" \
"s_addk_i32 s84, 0x7c00\n" \
M_"v[120:123], a[144:147], a[180:183], v[120:123]\n" \
B_"v27, s[68:71], s84 offen offset:2048 lds\n" \
M_"v[120:123], a[208:211], a[244:247], v[120:123]\n" \
X_"a[230:231], v33 offset:41984\n" \
M_"v[124:127], a[144:147], a[184:187], v[124:127]\n" \
"s_addk_i32 s84, 0x7c00\n" \
M_"v[124:127], a[208:211], a[248:251], v[124:127]\n" \
B_"v27, s[68:71], s84 offen offset:3072 lds\n" \
M_"v[128:131], a[144:147], a[188:191], v[128:131]\n" \
X_"a[168:169], v34 offset:32768\n" \
M_"v[128:131], a[208:211], a[252:255], v[128:131]\n" \
R_"a[144:147], v28 offset:32768\n" \
M_"v[144:147], a[148:151], a[188:191], v[144:147]\n" \
R_"a[208:211], v29 offset:32768\n" \
M_"v[144:147], a[212:215], a[252:255], v[144:147]\n" \
"s_add_u32 m0, s30, 0x1000\n" \
"s_mov_b32 s84, 0x9000\n" \
M_"v[140:143], a[148:151], a[184:187], v[140:143]\n" \
B_"v24, s[76:79], s84 offen offset:0 lds\n" \
M_"v[140:143], a[212:215], a[248:251], v[140:143]\n" \
X_"a[170:171], v34 offset:33792\n" \
M_"v[136:139], a[148:151], a[180:183], v[136:139]\n" \
"s_addk_i32 s84, 0x2000\n" \
"s_nop 0\n" \
M_"v[136:139], a[212:215], a[244:247], v[136:139]\n" \
B_"v25, s[76:79], s84 offen offset:1024 lds\n" \
M_"v[132:135], a[148:151], a[176:179], v[132:135]\n" \
X_"a[232:233], v34 offset:40960\n" \
M_"v[132:135], a[212:215], a[240:243], v[132:135]\n" \
R_"a[148:151], v28 offset:34816\n" \
M_"v[148:151], a[152:155], a[176:179], v[148:151]\n" \
R_"a[212:215], v29 offset:34816\n" \
M_"v[148:151], a[216:219], a[240:243], v[148:151]\n" \
X_"a[234:235], v34 offset:41984\n" \
M_"v[152:155], a[152:155], a[180:183], v[152:155]\n" \
X_"a[172:173], v35 offset:32768\n" \
M_"v[152:155], a[216:219], a[244:247], v[152:155]\n" \
X_"a[174:175], v35 offset:33792\n" \
M_"v[156:159], a[152:155], a[184:187], v[156:159]\n" \
X_"a[236:237], v35 offset:40960\n" \
M_"v[156:159], a[216:219], a[248:251], v[156:159]\n" \
X_"a[238:239], v35 offset:41984\n" \
M_"v[160:163], a[152:155], a[188:191], v[160:163]\n" \
M_"v[160:163], a[216:219], a[252:255], v[160:163]\n" \
R_"a[152:155], v28 offset:36864\n" \
M_"v[176:179], a[156:159], a[188:191], v[176:179]\n" \
R_"a[216:219], v29 offset:36864\n" \
M_"v[176:179], a[220:223], a[252:255], v[176:179]\n" \
M_"v[172:175], a[156:159], a[184:187], v[172:175]\n" \
M_"v[172:175], a[220:223], a[248:251], v[172:175]\n" \
M_"v[168:171], a[156:159], a[180:183], v[168:171]\n" \
M_"v[168:171], a[220:223], a[244:247], v[168:171]\n" \
M_"v[164:167], a[156:159], a[176:179], v[164:167]\n" \
M_"v[164:167], a[220:223], a[240:243], v[164:167]\n" \
R_"a[156:159], v28 offset:38912\n" \
R_"a[220:223], v29 offset:38912\n" \
"s_waitcnt lgkmcnt(15)\n" \
M_"a[0:3], a[128:131], a[160:163], a[0:3]\n" \
"s_addk_i32 s84, 0x2000\n" \
M_"a[0:3], a[192:195], a[224:227], a[0:3]\n" \
B_"v24, s[76:79], s84 offen offset:2048 lds\n" \
M_"a[4:7], a[128:131], a[164:167], a[4:7]\n" \
X_"a[176:177], v36 offset:32768\n" \
M_"a[4:7], a[192:195], a[228:231], a[4:7]\n" \
"s_addk_i32 s84, 0x2000\n" \
"s_waitcnt lgkmcnt(13)\n" \
M_"a[8:11], a[128:131], a[168:171], a[8:11]\n" \
B_"v25, s[76:79], s84 offen offset:3072 lds\n" \
"s_waitcnt lgkmcnt(9)\n" \
M_"a[8:11], a[192:195], a[232:235], a[8:11]\n" \
X_"a[178:179], v36 offset:33792\n" \
"s_waitcnt lgkmcnt(8)\n" \
M_"a[12:15], a[128:131], a[172:175], a[12:15]\n" \
X_"a[240:241], v36 offset:40960\n" \
"s_waitcnt lgkmcnt(7)\n" \
M_"a[12:15], a[192:195], a[236:239], a[12:15]\n" \
M_"a[28:31], a[132:135], a[172:175], a[28:31]\n" \
M_"a[28:31], a[196:199], a[236:239], a[28:31]\n" \
"s_add_u32 m0, s30, 0x20000\n" \
"s_movk_i32 s84, 0x100\n" \
M_"a[24:27], a[132:135], a[168:171], a[24:27]\n" \
B_"v24, s[76:79], s84 offen offset:0 lds\n" \
M_"a[24:27], a[196:199], a[232:235], a[24:27]\n" \
X_"a[242:243], v36 offset:41984\n" \
M_"a[20:23], a[132:135], a[164:167], a[20:23]\n" \
"v_accvgpr_read_b32 v184, a12\n" \
"v_accvgpr_read_b32 v185, a13\n" \
M_"a[20:23], a[196:199], a[228:231], a[20:23]\n" \
X_"a[180:181], v37 offset:32768\n" \
"v_accvgpr_read_b32 v186, a14\n" \
M_"a[16:19], a[132:135], a[160:163], a[16:19]\n" \
"s_movk_i32 s84, 0x2100\n" \
"s_nop 0\n" \
M_"a[16:19], a[196:199], a[224:227], a[16:19]\n" \
B_"v25, s[76:79], s84 offen offset:1024 lds\n" \
M_"a[32:35], a[136:139], a[160:163], a[32:35]\n" \
X_"a[182:183], v37 offset:33792\n" \
"v_accvgpr_read_b32 v187, a15\n" \
M_"a[32:35], a[200:203], a[224:227], a[32:35]\n" \
"v_accvgpr_read_b32 v180, a28\n" \
"v_accvgpr_read_b32 v181, a29\n" \
M_"a[36:39], a[136:139], a[164:167], a[36:39]\n" \
"v_accvgpr_read_b32 v182, a30\n" \
"v_accvgpr_read_b32 v183, a31\n" \
M_"a[36:39], a[200:203], a[228:231], a[36:39]\n" \
"s_movk_i32 s84, 0x4100\n" \
"s_nop 0\n" \
M_"a[40:43], a[136:139], a[168:171], a[40:43]\n" \
B_"v24, s[76:79], s84 offen offset:2048 lds\n" \
M_"a[40:43], a[200:203], a[232:235], a[40:43]\n" \
X_"a[244:245], v37 offset:40960\n" \
C_"v184, v184, v185\n" \
M_"a[44:47], a[136:139], a[172:175], a[44:47]\n" \
X_"a[246:247], v37 offset:41984\n" \
C_"v185, v186, v187\n" \
M_"a[44:47], a[200:203], a[236:239], a[44:47]\n" \
C_"v186, v180, v181\n" \
C_"v187, v182, v183\n" \
M_"a[60:63], a[140:143], a[172:175], a[60:63]\n" \
"s_movk_i32 s84, 0x6100\n" \
M_"a[60:63], a[204:207], a[236:239], a[60:63]\n" \
B_"v25, s[76:79], s84 offen offset:3072 lds\n" \
M_"a[56:59], a[140:143], a[168:171], a[56:59]\n" \
X_"a[184:185], v38 offset:32768\n" \
"v_accvgpr_read_b32 v188, a8\n" \
M_"a[56:59], a[204:207], a[232:235], a[56:59]\n" \
"v_accvgpr_read_b32 v189, a9\n" \
"v_accvgpr_read_b32 v190, a10\n" \
M_"a[52:55], a[140:143], a[164:167], a[52:55]\n" \
"s_add_u32 m0, s30, 0x21000\n" \
"s_addk_i32 s84, 0x3000\n" \
M_"a[52:55], a[204:207], a[228:231], a[52:55]\n" \
B_"v24, s[76:79], s84 offen offset:0 lds\n" \
M_"a[48:51], a[140:143], a[160:163], a[48:51]\n" \
X_"a[186:187], v38 offset:33792\n" \
"v_accvgpr_read_b32 v191, a11\n" \
M_"a[48:51], a[204:207], a[224:227], a[48:51]\n" \
"v_accvgpr_read_b32 v180, a24\n" \
"v_accvgpr_read_b32 v181, a25\n" \
M_"a[64:67], a[144:147], a[160:163], a[64:67]\n" \
X_"a[248:249], v38 offset:40960\n" \
"v_accvgpr_read_b32 v182, a26\n" \
M_"a[64:67], a[208:211], a[224:227], a[64:67]\n" \
"s_addk_i32 s84, 0x2000\n" \
"s_nop 0\n" \
M_"a[68:71], a[144:147], a[164:167], a[68:71]\n" \
B_"v25, s[76:79], s84 offen offset:1024 lds\n" \
M_"a[68:71], a[208:211], a[228:231], a[68:71]\n" \
X_"a[250:251], v38 offset:41984\n" \
"v_accvgpr_read_b32 v183, a27\n" \
M_"a[72:75], a[144:147], a[168:171], a[72:75]\n" \
C_"v188, v188, v189\n" \
C_"v189, v190, v191\n" \
M_"a[72:75], a[208:211], a[232:235], a[72:75]\n" \
C_"v190, v180, v181\n" \
C_"v191, v182, v183\n" \
M_"a[76:79], a[144:147], a[172:175], a[76:79]\n" \
"s_addk_i32 s84, 0x2000\n" \
M_"a[76:79], a[208:211], a[236:239], a[76:79]\n" \
B_"v24, s[76:79], s84 offen offset:2048 lds\n" \
M_"a[92:95], a[148:151], a[172:175], a[92:95]\n" \
"s_add_u32 s89, s41, 0x0\n" \
"buffer_store_dwordx4 v[184:187], v20, s[80:83], s89 offen offset:3072 sc1\n" \
M_"a[92:95], a[212:215], a[236:239], a[92:95]\n" \
X_"a[188:189], v39 offset:32768\n" \
"v_accvgpr_read_b32 v192, a4\n" \
M_"a[88:91], a[148:151], a[168:171], a[88:91]\n" \
X_"a[190:191], v39 offset:33792\n" \
"v_accvgpr_read_b32 v193, a5\n" \
M_"a[88:91], a[212:215], a[232:235], a[88:91]\n" \
"s_addk_i32 s84, 0x2000\n" \
M_"a[84:87], a[148:151], a[164:167], a[84:87]\n" \
B_"v25, s[76:79], s84 offen offset:3072 lds\n" \
M_"a[84:87], a[212:215], a[228:231], a[84:87]\n" \
X_"a[252:253], v39 offset:40960\n" \
"v_accvgpr_read_b32 v194, a6\n" \
M_"a[80:83], a[148:151], a[160:163], a[80:83]\n" \
"s_add_u32 s88, s41, 0x0\n" \
"buffer_store_dwordx4 v[188:191], v20, s[80:83], s88 offen offset:2048 sc1\n" \
M_"a[80:83], a[212:215], a[224:227], a[80:83]\n" \
"v_accvgpr_read_b32 v195, a7\n" \
"v_accvgpr_read_b32 v180, a20\n" \
"s_waitcnt lgkmcnt(15)\n" \
"s_nop 0\n" \
M_"a[96:99], a[152:155], a[160:163], a[96:99]\n" \
X_"a[254:255], v39 offset:41984\n" \
"v_accvgpr_read_b32 v181, a21\n" \
M_"a[96:99], a[216:219], a[224:227], a[96:99]\n" \
"v_accvgpr_read_b32 v182, a22\n" \
"v_accvgpr_read_b32 v183, a23\n" \
M_"a[100:103], a[152:155], a[164:167], a[100:103]\n" \
C_"v192, v192, v193\n" \
C_"v193, v194, v195\n" \
M_"a[100:103], a[216:219], a[228:231], a[100:103]\n" \
C_"v194, v180, v181\n" \
C_"v195, v182, v183\n" \
M_"a[104:107], a[152:155], a[168:171], a[104:107]\n" \
"s_add_u32 s89, s41, 0x0\n" \
"s_nop 0\n" \
"buffer_store_dwordx4 v[192:195], v20, s[80:83], s89 offen offset:1024 sc1\n" \
M_"a[104:107], a[216:219], a[232:235], a[104:107]\n" \
"v_accvgpr_read_b32 v196, a0\n" \
"v_accvgpr_read_b32 v197, a1\n" \
M_"a[108:111], a[152:155], a[172:175], a[108:111]\n" \
"v_accvgpr_read_b32 v198, a2\n" \
"v_accvgpr_read_b32 v199, a3\n" \
M_"a[108:111], a[216:219], a[236:239], a[108:111]\n" \
"v_accvgpr_read_b32 v180, a16\n" \
"v_accvgpr_read_b32 v181, a17\n" \
M_"a[124:127], a[156:159], a[172:175], a[124:127]\n" \
"v_accvgpr_read_b32 v182, a18\n" \
"v_accvgpr_read_b32 v183, a19\n" \
M_"a[124:127], a[220:223], a[236:239], a[124:127]\n" \
C_"v196, v196, v197\n" \
C_"v197, v198, v199\n" \
M_"a[120:123], a[156:159], a[168:171], a[120:123]\n" \
C_"v198, v180, v181\n" \
C_"v199, v182, v183\n" \
"s_add_u32 s88, s41, 0x0\n" \
"s_nop 0\n" \
"buffer_store_dwordx4 v[196:199], v20, s[80:83], s88 offen offset:0 sc1\n" \
M_"a[120:123], a[220:223], a[232:235], a[120:123]\n" \
"v_accvgpr_read_b32 v200, a44\n" \
"v_accvgpr_read_b32 v201, a45\n" \
M_"a[116:119], a[156:159], a[164:167], a[116:119]\n" \
"v_accvgpr_read_b32 v202, a46\n" \
"v_accvgpr_read_b32 v203, a47\n" \
M_"a[116:119], a[220:223], a[228:231], a[116:119]\n" \
"v_accvgpr_read_b32 v180, a60\n" \
"v_accvgpr_read_b32 v181, a61\n" \
M_"a[112:115], a[156:159], a[160:163], a[112:115]\n" \
"v_accvgpr_read_b32 v182, a62\n" \
"v_accvgpr_read_b32 v183, a63\n" \
M_"a[112:115], a[220:223], a[224:227], a[112:115]\n" \
C_"v200, v200, v201\n" \
C_"v201, v202, v203\n" \
"s_waitcnt vmcnt(12) lgkmcnt(0)\n" \
"s_barrier\n" \
"v_xor_b32_e32 v36, 0x4000, v36\n" \
"v_xor_b32_e64 v32, 0x0, v48\n" \
"v_xor_b32_e32 v37, 0x4000, v37\n" \
"v_xor_b32_e64 v33, 0x20, v48\n" \
"v_xor_b32_e32 v38, 0x4000, v38\n" \
"v_xor_b32_e64 v34, 0x40, v48\n" \
"v_xor_b32_e32 v39, 0x4000, v39\n" \
"v_xor_b32_e32 v35, 0x60, v48\n" \
"v_add_u32_e32 v28, s43, v28\n" \
"v_add_u32_e32 v29, s43, v29\n" \
M_"v[116:119], a[144:147], a[176:179], v[116:119]\n" \
"s_add_u32 m0, s40, 0x8000\n" \
"s_add_u32 s85, s39, 0x2000\n" \
M_"v[116:119], a[208:211], a[240:243], v[116:119]\n" \
B_"v20, s[80:83], s85 offen sc1 lds\n" \
M_"v[120:123], a[144:147], a[180:183], v[120:123]\n" \
X_"a[160:161], v32 offset:0\n" \
C_"v202, v180, v181\n" \
M_"v[120:123], a[208:211], a[244:247], v[120:123]\n" \
"s_add_u32 m0, s40, 0x9000\n" \
"s_add_u32 s85, s39, 0x3000\n" \
M_"v[124:127], a[144:147], a[184:187], v[124:127]\n" \
B_"v20, s[80:83], s85 offen sc1 lds\n" \
M_"v[124:127], a[208:211], a[248:251], v[124:127]\n" \
X_"a[162:163], v32 offset:512\n" \
C_"v203, v182, v183\n" \
M_"v[128:131], a[144:147], a[188:191], v[128:131]\n" \
X_"a[224:225], v32 offset:4096\n" \
"v_accvgpr_read_b32 v204, a40\n" \
M_"v[128:131], a[208:211], a[252:255], v[128:131]\n" \
R_"a[144:147], v28 offset:8192\n" \
"v_accvgpr_read_b32 v205, a41\n" \
M_"v[144:147], a[148:151], a[188:191], v[144:147]\n" \
R_"a[208:211], v29 offset:8192\n" \
"v_accvgpr_read_b32 v206, a42\n" \
M_"v[144:147], a[212:215], a[252:255], v[144:147]\n" \
"s_add_u32 m0, s40, 0xb000\n" \
"s_movk_i32 s84, 0x80\n" \
M_"v[140:143], a[148:151], a[184:187], v[140:143]\n" \
B_"v21, s[76:79], s84 offen offset:0 lds\n" \
M_"v[140:143], a[212:215], a[248:251], v[140:143]\n" \
"s_add_u32 m0, s30, 0x8000\n" \
"s_movk_i32 s84, 0x80\n" \
M_"v[136:139], a[148:151], a[180:183], v[136:139]\n" \
B_"v24, s[76:79], s84 offen offset:0 lds\n" \
M_"v[136:139], a[212:215], a[244:247], v[136:139]\n" \
X_"a[226:227], v32 offset:4608\n" \
"v_accvgpr_read_b32 v207, a43\n" \
M_"v[132:135], a[148:151], a[176:179], v[132:135]\n" \
X_"a[164:165], v33 offset:0\n" \
"v_accvgpr_read_b32 v180, a56\n" \
M_"v[132:135], a[212:215], a[240:243], v[132:135]\n" \
R_"a[148:151], v28 offset:10240\n" \
"v_accvgpr_read_b32 v181, a57\n" \
M_"v[148:151], a[152:155], a[176:179], v[148:151]\n" \
R_"a[212:215], v29 offset:10240\n" \
"v_accvgpr_read_b32 v182, a58\n" \
M_"v[148:151], a[216:219], a[240:243], v[148:151]\n" \
"s_movk_i32 s84, 0x2080\n" \
M_"v[152:155], a[152:155], a[180:183], v[152:155]\n" \
B_"v25, s[76:79], s84 offen offset:1024 lds\n" \
M_"v[152:155], a[216:219], a[244:247], v[152:155]\n" \
"s_movk_i32 s84, 0x4080\n" \
M_"v[156:159], a[152:155], a[184:187], v[156:159]\n" \
B_"v24, s[76:79], s84 offen offset:2048 lds\n" \
M_"v[156:159], a[216:219], a[248:251], v[156:159]\n" \
X_"a[166:167], v33 offset:512\n" \
"v_accvgpr_read_b32 v183, a59\n" \
M_"v[160:163], a[152:155], a[188:191], v[160:163]\n" \
X_"a[228:229], v33 offset:4096\n" \
C_"v204, v204, v205\n" \
M_"v[160:163], a[216:219], a[252:255], v[160:163]\n" \
R_"a[152:155], v28 offset:12288\n" \
C_"v205, v206, v207\n" \
M_"v[176:179], a[156:159], a[188:191], v[176:179]\n" \
R_"a[216:219], v29 offset:12288\n" \
C_"v206, v180, v181\n" \
M_"v[176:179], a[220:223], a[252:255], v[176:179]\n" \
"s_movk_i32 s84, 0x6080\n" \
M_"v[172:175], a[156:159], a[184:187], v[172:175]\n" \
B_"v25, s[76:79], s84 offen offset:3072 lds\n" \
M_"v[172:175], a[220:223], a[248:251], v[172:175]\n" \
"s_add_u32 m0, s31, 0x8000\n" \
"s_mov_b32 s84, 0x80000\n" \
M_"v[168:171], a[156:159], a[180:183], v[168:171]\n" \
B_"v26, s[68:71], s84 offen offset:0 lds\n" \
M_"v[168:171], a[220:223], a[244:247], v[168:171]\n" \
"s_addk_i32 s84, 0x7c00\n" \
M_"v[164:167], a[156:159], a[176:179], v[164:167]\n" \
B_"v26, s[68:71], s84 offen offset:1024 lds\n" \
M_"v[164:167], a[220:223], a[240:243], v[164:167]\n" \
R_"a[156:159], v28 offset:14336\n" \
C_"v207, v182, v183\n" \
M_"v[84:87], a[136:139], a[176:179], v[84:87]\n" \
R_"a[220:223], v29 offset:14336\n" \
"v_accvgpr_read_b32 v208, a36\n" \
M_"v[84:87], a[200:203], a[240:243], v[84:87]\n" \
"s_addk_i32 s84, 0x7c00\n" \
M_"v[88:91], a[136:139], a[180:183], v[88:91]\n" \
B_"v27, s[68:71], s84 offen offset:2048 lds\n" \
M_"v[88:91], a[200:203], a[244:247], v[88:91]\n" \
X_"a[230:231], v33 offset:4608\n" \
"v_accvgpr_read_b32 v209, a37\n" \
M_"v[92:95], a[136:139], a[184:187], v[92:95]\n" \
"s_addk_i32 s84, 0x7c00\n" \
M_"v[92:95], a[200:203], a[248:251], v[92:95]\n" \
B_"v27, s[68:71], s84 offen offset:3072 lds\n" \
M_"v[96:99], a[136:139], a[188:191], v[96:99]\n" \
X_"a[172:173], v35 offset:0\n" \
"v_accvgpr_read_b32 v210, a38\n" \
M_"v[96:99], a[200:203], a[252:255], v[96:99]\n" \
R_"a[136:139], v28 offset:4096\n" \
"v_accvgpr_read_b32 v211, a39\n" \
M_"v[112:115], a[140:143], a[188:191], v[112:115]\n" \
R_"a[200:203], v29 offset:4096\n" \
"v_accvgpr_read_b32 v180, a52\n" \
M_"v[112:115], a[204:207], a[252:255], v[112:115]\n" \
"s_add_u32 m0, s30, 0x9000\n" \
"s_mov_b32 s84, 0x9080\n" \
M_"v[108:111], a[140:143], a[184:187], v[108:111]\n" \
B_"v24, s[76:79], s84 offen offset:0 lds\n" \
M_"v[108:111], a[204:207], a[248:251], v[108:111]\n" \
X_"a[174:175], v35 offset:512\n" \
"v_accvgpr_read_b32 v181, a53\n" \
M_"v[104:107], a[140:143], a[180:183], v[104:107]\n" \
"s_addk_i32 s84, 0x2000\n" \
"s_nop 0\n" \
M_"v[104:107], a[204:207], a[244:247], v[104:107]\n" \
B_"v25, s[76:79], s84 offen offset:1024 lds\n" \
M_"v[100:103], a[140:143], a[176:179], v[100:103]\n" \
X_"a[236:237], v35 offset:4096\n" \
"v_accvgpr_read_b32 v182, a54\n" \
M_"v[100:103], a[204:207], a[240:243], v[100:103]\n" \
R_"a[140:143], v28 offset:6144\n" \
"v_accvgpr_read_b32 v183, a55\n" \
M_"v[52:55], a[128:131], a[176:179], v[52:55]\n" \
R_"a[204:207], v29 offset:6144\n" \
C_"v208, v208, v209\n" \
M_"v[52:55], a[192:195], a[240:243], v[52:55]\n" \
X_"a[238:239], v35 offset:4608\n" \
C_"v209, v210, v211\n" \
M_"v[56:59], a[128:131], a[180:183], v[56:59]\n" \
X_"a[168:169], v34 offset:0\n" \
C_"v210, v180, v181\n" \
M_"v[56:59], a[192:195], a[244:247], v[56:59]\n" \
X_"a[170:171], v34 offset:512\n" \
C_"v211, v182, v183\n" \
M_"v[60:63], a[128:131], a[184:187], v[60:63]\n" \
X_"a[232:233], v34 offset:4096\n" \
"v_accvgpr_read_b32 v212, a32\n" \
M_"v[60:63], a[192:195], a[248:251], v[60:63]\n" \
X_"a[234:235], v34 offset:4608\n" \
"v_accvgpr_read_b32 v213, a33\n" \
M_"v[64:67], a[128:131], a[188:191], v[64:67]\n" \
"v_accvgpr_read_b32 v214, a34\n" \
"v_accvgpr_read_b32 v215, a35\n" \
M_"v[64:67], a[192:195], a[252:255], v[64:67]\n" \
R_"a[128:131], v28 offset:0\n" \
"v_accvgpr_read_b32 v180, a48\n" \
M_"v[80:83], a[132:135], a[188:191], v[80:83]\n" \
R_"a[192:195], v29 offset:0\n" \
"v_accvgpr_read_b32 v181, a49\n" \
M_"v[80:83], a[196:199], a[252:255], v[80:83]\n" \
"v_accvgpr_read_b32 v182, a50\n" \
"v_accvgpr_read_b32 v183, a51\n" \
M_"v[76:79], a[132:135], a[184:187], v[76:79]\n" \
C_"v212, v212, v213\n" \
C_"v213, v214, v215\n" \
M_"v[76:79], a[196:199], a[248:251], v[76:79]\n" \
C_"v214, v180, v181\n" \
C_"v215, v182, v183\n" \
M_"v[72:75], a[132:135], a[180:183], v[72:75]\n" \
"v_accvgpr_read_b32 v216, a68\n" \
"v_accvgpr_read_b32 v217, a69\n" \
M_"v[72:75], a[196:199], a[244:247], v[72:75]\n" \
"v_accvgpr_read_b32 v218, a70\n" \
"v_accvgpr_read_b32 v219, a71\n" \
M_"v[68:71], a[132:135], a[176:179], v[68:71]\n" \
"v_accvgpr_read_b32 v180, a84\n" \
"v_accvgpr_read_b32 v181, a85\n" \
M_"v[68:71], a[196:199], a[240:243], v[68:71]\n" \
R_"a[132:135], v28 offset:2048\n" \
R_"a[196:199], v29 offset:2048\n" \
"s_waitcnt lgkmcnt(15)\n" \
M_"a[0:3], a[160:163], a[144:147], 0\n" \
"s_addk_i32 s84, 0x2000\n" \
"v_accvgpr_read_b32 v182, a86\n" \
M_"a[0:3], a[224:227], a[208:211], a[0:3]\n" \
B_"v24, s[76:79], s84 offen offset:2048 lds\n" \
"v_accvgpr_read_b32 v183, a87\n" \
M_"a[4:7], a[164:167], a[144:147], 0\n" \
X_"a[176:177], v36 offset:0\n" \
C_"v216, v216, v217\n" \
M_"a[4:7], a[228:231], a[208:211], a[4:7]\n" \
"s_addk_i32 s84, 0x2000\n" \
C_"v217, v218, v219\n" \
"s_waitcnt lgkmcnt(13)\n" \
M_"a[12:15], a[172:175], a[144:147], 0\n" \
B_"v25, s[76:79], s84 offen offset:3072 lds\n" \
C_"v218, v180, v181\n" \
"s_waitcnt lgkmcnt(9)\n" \
".fill (L_pb + 61152 - .) / 4, 4, 0xBF800000\n" \
"L_pp15:\n" \
"s_cbranch_execz L_pret\n" \
M_"a[12:15], a[236:239], a[208:211], a[12:15]\n" \
X_"a[178:179], v36 offset:1024\n" \
C_"v219, v182, v183\n" \
"s_waitcnt lgkmcnt(8)\n" \
M_"a[8:11], a[144:147], a[168:171], 0\n" \
X_"a[240:241], v36 offset:8192\n" \
"v_accvgpr_read_b32 v220, a64\n" \
"s_waitcnt lgkmcnt(7)\n" \
"s_nop 0\n" \
M_"a[8:11], a[208:211], a[232:235], a[8:11]\n" \
X_"a[242:243], v36 offset:9216\n" \
"v_accvgpr_read_b32 v221, a65\n" \
M_"a[24:27], a[148:151], a[168:171], 0\n" \
X_"a[180:181], v37 offset:0\n" \
"v_accvgpr_read_b32 v222, a66\n" \
M_"a[24:27], a[212:215], a[232:235], a[24:27]\n" \
X_"a[182:183], v37 offset:1024\n" \
"v_accvgpr_read_b32 v223, a67\n" \
M_"a[28:31], a[172:175], a[148:151], 0\n" \
X_"a[244:245], v37 offset:8192\n" \
"v_accvgpr_read_b32 v180, a80\n" \
M_"a[28:31], a[236:239], a[212:215], a[28:31]\n" \
"s_add_u32 s89, s41, 0x2000\n" \
"buffer_store_dwordx4 v[200:203], v20, s[80:83], s89 offen offset:3072 sc1\n" \
M_"a[20:23], a[164:167], a[148:151], 0\n" \
X_"a[246:247], v37 offset:9216\n" \
"v_accvgpr_read_b32 v181, a81\n" \
M_"a[20:23], a[228:231], a[212:215], a[20:23]\n" \
X_"a[184:185], v38 offset:0\n" \
"v_accvgpr_read_b32 v182, a82\n" \
M_"a[16:19], a[160:163], a[148:151], 0\n" \
X_"a[186:187], v38 offset:1024\n" \
"v_accvgpr_read_b32 v183, a83\n" \
M_"a[16:19], a[224:227], a[212:215], a[16:19]\n" \
C_"v220, v220, v221\n" \
C_"v221, v222, v223\n" \
C_"v222, v180, v181\n" \
M_"a[32:35], a[160:163], a[152:155], 0\n" \
"s_add_u32 s88, s41, 0x2000\n" \
"buffer_store_dwordx4 v[204:207], v20, s[80:83], s88 offen offset:2048 sc1\n" \
M_"a[32:35], a[224:227], a[216:219], a[32:35]\n" \
X_"a[248:249], v38 offset:8192\n" \
C_"v223, v182, v183\n" \
M_"a[36:39], a[164:167], a[152:155], 0\n" \
X_"a[250:251], v38 offset:9216\n" \
C_"v224, v128, v129\n" \
M_"a[36:39], a[228:231], a[216:219], a[36:39]\n" \
X_"a[188:189], v39 offset:0\n" \
C_"v225, v130, v131\n" \
M_"a[44:47], a[172:175], a[152:155], 0\n" \
C_"v226, v144, v145\n" \
C_"v227, v146, v147\n" \
C_"v228, v124, v125\n" \
M_"a[44:47], a[236:239], a[216:219], a[44:47]\n" \
X_"a[190:191], v39 offset:1024\n" \
C_"v229, v126, v127\n" \
M_"a[40:43], a[152:155], a[168:171], 0\n" \
"s_add_u32 s89, s41, 0x2000\n" \
"buffer_store_dwordx4 v[208:211], v20, s[80:83], s89 offen offset:1024 sc1\n" \
M_"a[40:43], a[216:219], a[232:235], a[40:43]\n" \
X_"a[252:253], v39 offset:8192\n" \
C_"v230, v140, v141\n" \
M_"a[56:59], a[156:159], a[168:171], 0\n" \
X_"a[254:255], v39 offset:9216\n" \
C_"v231, v142, v143\n" \
M_"a[56:59], a[220:223], a[232:235], a[56:59]\n" \
C_"v232, v120, v121\n" \
C_"v233, v122, v123\n" \
C_"v234, v136, v137\n" \
M_"a[60:63], a[172:175], a[156:159], 0\n" \
C_"v235, v138, v139\n" \
C_"v236, v116, v117\n" \
C_"v237, v118, v119\n" \
M_"a[60:63], a[236:239], a[220:223], a[60:63]\n" \
"s_add_u32 s88, s41, 0x2000\n" \
"buffer_store_dwordx4 v[212:215], v20, s[80:83], s88 offen offset:0 sc1\n" \
M_"a[52:55], a[164:167], a[156:159], 0\n" \
C_"v238, v132, v133\n" \
C_"v239, v134, v135\n" \
C_"v240, v160, v161\n" \
M_"a[52:55], a[228:231], a[220:223], a[52:55]\n" \
C_"v241, v162, v163\n" \
C_"v242, v176, v177\n" \
C_"v243, v178, v179\n" \
M_"a[48:51], a[160:163], a[156:159], 0\n" \
C_"v244, v156, v157\n" \
C_"v245, v158, v159\n" \
C_"v246, v172, v173\n" \
M_"a[48:51], a[224:227], a[220:223], a[48:51]\n" \
C_"v247, v174, v175\n" \
C_"v184, v152, v153\n" \
C_"v185, v154, v155\n" \
M_"a[64:67], a[164:167], a[140:143], 0\n" \
C_"v186, v168, v169\n" \
C_"v187, v170, v171\n" \
C_"v188, v148, v149\n" \
M_"a[64:67], a[228:231], a[204:207], a[64:67]\n" \
C_"v189, v150, v151\n" \
C_"v190, v164, v165\n" \
C_"v191, v166, v167\n" \
M_"a[80:83], a[172:175], a[140:143], 0\n" \
C_"v192, v96, v97\n" \
C_"v193, v98, v99\n" \
C_"v194, v112, v113\n" \
M_"a[80:83], a[236:239], a[204:207], a[80:83]\n" \
C_"v195, v114, v115\n" \
C_"v196, v92, v93\n" \
C_"v197, v94, v95\n" \
M_"a[68:71], a[140:143], a[168:171], 0\n" \
C_"v198, v108, v109\n" \
C_"v199, v110, v111\n" \
C_"v252, v88, v89\n" \
M_"a[68:71], a[204:207], a[232:235], a[68:71]\n" \
C_"v253, v90, v91\n" \
C_"v254, v104, v105\n" \
C_"v255, v106, v107\n" \
"s_waitcnt vmcnt(4) lgkmcnt(0)\n" \
"s_barrier\n" \
M_"v[116:119], a[176:179], a[144:147], 0\n" \
"s_add_u32 m0, s40, 0x0\n" \
"s_nop 0\n" \
"s_add_u32 s85, s39, 0x4000\n" \
"v_accvgpr_write_b32 a84, v252\n" \
M_"v[116:119], a[240:243], a[208:211], v[116:119]\n" \
B_"v20, s[80:83], s85 offen sc1 lds\n" \
"v_accvgpr_write_b32 a85, v253\n" \
M_"v[120:123], a[180:183], a[144:147], 0\n" \
X_"a[160:161], v32 offset:32768\n" \
"v_accvgpr_write_b32 a86, v254\n" \
M_"v[120:123], a[244:247], a[208:211], v[120:123]\n" \
X_"a[162:163], v32 offset:33280\n" \
"v_accvgpr_write_b32 a87, v255\n" \
M_"v[124:127], a[184:187], a[144:147], 0\n" \
"s_add_u32 m0, s40, 0x1000\n" \
"s_add_u32 s85, s39, 0x5000\n" \
C_"v252, v84, v85\n" \
M_"v[124:127], a[248:251], a[208:211], v[124:127]\n" \
B_"v20, s[80:83], s85 offen sc1 lds\n" \
C_"v253, v86, v87\n" \
M_"v[128:131], a[188:191], a[144:147], 0\n" \
X_"a[224:225], v32 offset:36864\n" \
C_"v254, v100, v101\n" \
M_"v[128:131], a[252:255], a[208:211], v[128:131]\n" \
R_"a[144:147], v28 offset:40960\n" \
C_"v255, v102, v103\n" \
M_"v[144:147], a[188:191], a[148:151], 0\n" \
R_"a[208:211], v29 offset:40960\n" \
C_"v200, v64, v65\n" \
M_"v[144:147], a[252:255], a[212:215], v[144:147]\n" \
"s_add_u32 m0, s40, 0x3000\n" \
"s_movk_i32 s84, 0x100\n" \
"s_nop 0\n" \
C_"v201, v66, v67\n" \
M_"v[140:143], a[184:187], a[148:151], 0\n" \
B_"v21, s[76:79], s84 offen offset:0 lds\n" \
C_"v202, v80, v81\n" \
M_"v[140:143], a[248:251], a[212:215], v[140:143]\n" \
X_"a[226:227], v32 offset:37376\n" \
C_"v203, v82, v83\n" \
M_"v[136:139], a[180:183], a[148:151], 0\n" \
C_"v204, v60, v61\n" \
C_"v205, v62, v63\n" \
C_"v206, v76, v77\n" \
M_"v[136:139], a[244:247], a[212:215], v[136:139]\n" \
X_"a[164:165], v33 offset:32768\n" \
C_"v207, v78, v79\n" \
M_"v[132:135], a[176:179], a[148:151], 0\n" \
C_"v208, v56, v57\n" \
C_"v209, v58, v59\n" \
C_"v210, v72, v73\n" \
M_"v[132:135], a[240:243], a[212:215], v[132:135]\n" \
R_"a[148:151], v28 offset:43008\n" \
C_"v211, v74, v75\n" \
M_"v[148:151], a[176:179], a[152:155], 0\n" \
R_"a[212:215], v29 offset:43008\n" \
C_"v212, v52, v53\n" \
M_"v[148:151], a[240:243], a[216:219], v[148:151]\n" \
"s_add_u32 m0, s31, 0x0\n" \
"s_mov_b32 s84, 0x100000\n" \
C_"v213, v54, v55\n" \
M_"v[152:155], a[180:183], a[152:155], 0\n" \
B_"v26, s[68:71], s84 offen offset:0 lds\n" \
C_"v214, v68, v69\n" \
M_"v[152:155], a[244:247], a[216:219], v[152:155]\n" \
"s_addk_i32 s84, 0x7c00\n" \
C_"v215, v70, v71\n" \
M_"v[156:159], a[184:187], a[152:155], 0\n" \
B_"v26, s[68:71], s84 offen offset:1024 lds\n" \
M_"v[156:159], a[248:251], a[216:219], v[156:159]\n" \
X_"a[166:167], v33 offset:33280\n" \
M_"v[160:163], a[188:191], a[152:155], 0\n" \
X_"a[228:229], v33 offset:36864\n" \
M_"v[160:163], a[252:255], a[216:219], v[160:163]\n" \
R_"a[152:155], v28 offset:45056\n" \
M_"v[176:179], a[188:191], a[156:159], 0\n" \
R_"a[216:219], v29 offset:45056\n" \
M_"v[176:179], a[252:255], a[220:223], v[176:179]\n" \
"s_addk_i32 s84, 0x7c00\n" \
M_"v[172:175], a[184:187], a[156:159], 0\n" \
B_"v27, s[68:71], s84 offen offset:2048 lds\n" \
M_"v[172:175], a[248:251], a[220:223], v[172:175]\n" \
X_"a[230:231], v33 offset:37376\n" \
M_"v[168:171], a[180:183], a[156:159], 0\n" \
"s_addk_i32 s84, 0x7c00\n" \
M_"v[168:171], a[244:247], a[220:223], v[168:171]\n" \
B_"v27, s[68:71], s84 offen offset:3072 lds\n" \
M_"v[164:167], a[176:179], a[156:159], 0\n" \
X_"a[172:173], v35 offset:32768\n" \
M_"v[164:167], a[240:243], a[220:223], v[164:167]\n" \
R_"a[156:159], v28 offset:47104\n" \
M_"v[84:87], a[176:179], a[136:139], 0\n" \
R_"a[220:223], v29 offset:47104\n" \
M_"v[84:87], a[240:243], a[200:203], v[84:87]\n" \
"s_add_u32 s89, s41, 0x3000\n" \
"buffer_store_dwordx4 v[192:195], v20, s[80:83], s89 offen offset:3072 sc1\n" \
M_"v[88:91], a[180:183], a[136:139], 0\n" \
X_"a[174:175], v35 offset:33280\n" \
M_"v[88:91], a[244:247], a[200:203], v[88:91]\n" \
X_"a[236:237], v35 offset:36864\n" \
M_"v[92:95], a[184:187], a[136:139], 0\n" \
X_"a[238:239], v35 offset:37376\n" \
M_"v[92:95], a[248:251], a[200:203], v[92:95]\n" \
"s_add_u32 m0, s30, 0x0\n" \
"s_movk_i32 s84, 0x180\n" \
M_"v[96:99], a[188:191], a[136:139], 0\n" \
B_"v24, s[76:79], s84 offen offset:0 lds\n" \
M_"v[96:99], a[252:255], a[200:203], v[96:99]\n" \
R_"a[136:139], v28 offset:36864\n" \
M_"v[112:115], a[188:191], a[140:143], 0\n" \
R_"a[200:203], v29 offset:36864\n" \
M_"v[112:115], a[252:255], a[204:207], v[112:115]\n" \
X_"a[168:169], v34 offset:32768\n" \
M_"v[108:111], a[184:187], a[140:143], 0\n" \
"s_movk_i32 s84, 0x2180\n" \
"s_nop 0\n" \
M_"v[108:111], a[248:251], a[204:207], v[108:111]\n" \
B_"v25, s[76:79], s84 offen offset:1024 lds\n" \
M_"v[104:107], a[180:183], a[140:143], 0\n" \
X_"a[170:171], v34 offset:33280\n" \
M_"v[104:107], a[244:247], a[204:207], v[104:107]\n" \
X_"a[232:233], v34 offset:36864\n" \
M_"v[100:103], a[176:179], a[140:143], 0\n" \
M_"v[100:103], a[240:243], a[204:207], v[100:103]\n" \
R_"a[140:143], v28 offset:38912\n" \
M_"v[52:55], a[176:179], a[128:131], 0\n" \
R_"a[204:207], v29 offset:38912\n" \
M_"v[52:55], a[240:243], a[192:195], v[52:55]\n" \
"s_movk_i32 s84, 0x4180\n" \
M_"v[56:59], a[180:183], a[128:131], 0\n" \
B_"v24, s[76:79], s84 offen offset:2048 lds\n" \
M_"v[56:59], a[244:247], a[192:195], v[56:59]\n" \
"s_movk_i32 s84, 0x6180\n" \
M_"v[60:63], a[184:187], a[128:131], 0\n" \
B_"v25, s[76:79], s84 offen offset:3072 lds\n" \
M_"v[60:63], a[248:251], a[192:195], v[60:63]\n" \
X_"a[234:235], v34 offset:37376\n" \
M_"v[64:67], a[188:191], a[128:131], 0\n" \
M_"v[64:67], a[252:255], a[192:195], v[64:67]\n" \
R_"a[128:131], v28 offset:32768\n" \
M_"v[80:83], a[188:191], a[132:135], 0\n" \
R_"a[192:195], v29 offset:32768\n" \
M_"v[80:83], a[252:255], a[196:199], v[80:83]\n" \
M_"v[76:79], a[184:187], a[132:135], 0\n" \
M_"v[76:79], a[248:251], a[196:199], v[76:79]\n" \
M_"v[72:75], a[180:183], a[132:135], 0\n" \
M_"v[72:75], a[244:247], a[196:199], v[72:75]\n" \
M_"v[68:71], a[176:179], a[132:135], 0\n" \
M_"v[68:71], a[240:243], a[196:199], v[68:71]\n" \
R_"a[132:135], v28 offset:34816\n" \
R_"a[196:199], v29 offset:34816\n" \
"s_waitcnt lgkmcnt(15)\n" \
M_"a[0:3], a[160:163], a[144:147], a[0:3]\n" \
"s_add_u32 m0, s30, 0x1000\n" \
"s_addk_i32 s84, 0x3000\n" \
M_"a[0:3], a[224:227], a[208:211], a[0:3]\n" \
B_"v24, s[76:79], s84 offen offset:0 lds\n" \
M_"a[4:7], a[164:167], a[144:147], a[4:7]\n" \
X_"a[176:177], v36 offset:32768\n" \
"v_mov_b32_e64 v11, 0\n" \
M_"a[4:7], a[228:231], a[208:211], a[4:7]\n" \
X_"a[178:179], v36 offset:33792\n" \
"s_and_b32 s5, s2, 7\n" \
"s_waitcnt lgkmcnt(15)\n" \
M_"a[12:15], a[172:175], a[144:147], a[12:15]\n" \
X_"a[240:241], v36 offset:40960\n" \
"s_lshl_b32 s6, s3, 1\n" \
"s_waitcnt lgkmcnt(15)\n" \
M_"a[12:15], a[236:239], a[208:211], a[12:15]\n" \
"s_add_u32 s88, s41, 0x3000\n" \
"buffer_store_dwordx4 v[196:199], v20, s[80:83], s88 offen offset:2048 sc1\n" \
"s_waitcnt lgkmcnt(11)\n" \
M_"a[8:11], a[144:147], a[168:171], a[8:11]\n" \
X_"a[242:243], v36 offset:41984\n" \
"s_lshl_b32 s4, s5, 2\n" \
"s_waitcnt lgkmcnt(8)\n" \
M_"a[8:11], a[208:211], a[232:235], a[8:11]\n" \
X_"a[180:181], v37 offset:32768\n" \
"s_bfe_u32 s7, s2, 0x20003\n" \
M_"a[24:27], a[148:151], a[168:171], a[24:27]\n" \
X_"a[182:183], v37 offset:33792\n" \
"s_or_b32 s12, s4, s7\n" \
M_"a[24:27], a[212:215], a[232:235], a[24:27]\n" \
X_"a[244:245], v37 offset:40960\n" \
"s_and_b32 s6, s6, 4\n" \
M_"a[28:31], a[172:175], a[148:151], a[28:31]\n" \
"s_and_b32 s8, s3, 1\n" \
"s_or_b32 s8, s6, s8\n" \
M_"a[28:31], a[236:239], a[212:215], a[28:31]\n" \
"s_addk_i32 s84, 0x2000\n" \
M_"a[20:23], a[164:167], a[148:151], a[20:23]\n" \
B_"v25, s[76:79], s84 offen offset:1024 lds\n" \
M_"a[20:23], a[228:231], a[212:215], a[20:23]\n" \
X_"a[246:247], v37 offset:41984\n" \
"s_lshl_b32 s6, s12, 4\n" \
M_"a[16:19], a[160:163], a[148:151], a[16:19]\n" \
X_"a[184:185], v38 offset:32768\n" \
"s_lshl_b32 s9, s3, 2\n" \
M_"a[16:19], a[224:227], a[212:215], a[16:19]\n" \
X_"a[186:187], v38 offset:33792\n" \
"s_or_b32 s6, s6, s9\n" \
M_"a[32:35], a[160:163], a[152:155], a[32:35]\n" \
"s_addk_i32 s84, 0x2000\n" \
M_"a[32:35], a[224:227], a[216:219], a[32:35]\n" \
B_"v24, s[76:79], s84 offen offset:2048 lds\n" \
M_"a[36:39], a[164:167], a[152:155], a[36:39]\n" \
X_"a[248:249], v38 offset:40960\n" \
"s_lshr_b32 s9, s2, 5\n" \
M_"a[36:39], a[228:231], a[216:219], a[36:39]\n" \
X_"a[250:251], v38 offset:41984\n" \
"s_mul_i32 s10, s9, 0xa00\n" \
M_"a[44:47], a[172:175], a[152:155], a[44:47]\n" \
X_"a[188:189], v39 offset:32768\n" \
"s_add_i32 s10, s10, 0x5bc1000\n" \
M_"a[44:47], a[236:239], a[216:219], a[44:47]\n" \
X_"a[190:191], v39 offset:33792\n" \
"s_or_b32 s6, s6, s10\n" \
M_"a[40:43], a[152:155], a[168:171], a[40:43]\n" \
"s_add_u32 s89, s41, 0x3000\n" \
"buffer_store_dwordx4 a[84:87], v20, s[80:83], s89 offen offset:1024 sc1\n" \
M_"a[40:43], a[216:219], a[232:235], a[40:43]\n" \
X_"a[252:253], v39 offset:40960\n" \
"s_lshl_b32 s13, s9, 3\n" \
M_"a[56:59], a[156:159], a[168:171], a[56:59]\n" \
X_"a[254:255], v39 offset:41984\n" \
"v_writelane_b32 v11, s6, 0\n" \
M_"a[56:59], a[220:223], a[232:235], a[56:59]\n" \
"s_or_b32 s6, s13, s5\n" \
"v_writelane_b32 v11, s10, 1\n" \
M_"a[60:63], a[172:175], a[156:159], a[60:63]\n" \
"s_lshl_b32 s6, s6, 10\n" \
"s_lshl_b32 s10, s3, 8\n" \
M_"a[60:63], a[236:239], a[220:223], a[60:63]\n" \
"s_addk_i32 s84, 0x2000\n" \
M_"a[52:55], a[164:167], a[156:159], a[52:55]\n" \
B_"v25, s[76:79], s84 offen offset:3072 lds\n" \
M_"a[52:55], a[228:231], a[220:223], a[52:55]\n" \
"s_or_b32 s6, s6, s10\n" \
"s_or_b32 s10, s6, 0x5c00200\n" \
M_"a[48:51], a[160:163], a[156:159], a[48:51]\n" \
"v_writelane_b32 v11, s10, 2\n" \
"s_lshl_b32 s10, s7, 2\n" \
M_"a[48:51], a[224:227], a[220:223], a[48:51]\n" \
"s_or_b32 s11, s10, s6\n" \
"s_or_b32 s11, s11, 0x5c00000\n" \
M_"a[64:67], a[164:167], a[140:143], a[64:67]\n" \
"v_writelane_b32 v11, s11, 3\n" \
"s_or_b32 s6, s6, 0x5c00080\n" \
M_"a[64:67], a[228:231], a[204:207], a[64:67]\n" \
"v_writelane_b32 v11, s6, 4\n" \
"s_or_b32 s6, s6, s10\n" \
M_"a[80:83], a[172:175], a[140:143], a[80:83]\n" \
"s_add_i32 s10, s3, s2\n" \
"v_writelane_b32 v11, s6, 5\n" \
M_"a[80:83], a[236:239], a[204:207], a[80:83]\n" \
"s_mov_b32 s6, 2.0\n" \
"s_mov_b32 s11, 0x5bc0000\n" \
M_"a[68:71], a[140:143], a[168:171], a[68:71]\n" \
"s_cmp_lg_u32 s10, 0\n" \
"s_cselect_b32 s10, s6, s11\n" \
"s_waitcnt vmcnt(11) lgkmcnt(0)\n" \
M_"a[68:71], a[204:207], a[232:235], a[68:71]\n" \
"s_barrier\n" \
"v_xor_b32_e32 v28, 0x20000, v28\n" \
"v_xor_b32_e32 v29, 0x20000, v29\n" \
M_"v[116:119], a[176:179], a[144:147], v[116:119]\n" \
"s_add_u32 m0, s40, 0x8000\n" \
"s_add_u32 s85, s39, 0x6000\n" \
M_"v[116:119], a[240:243], a[208:211], v[116:119]\n" \
B_"v20, s[80:83], s85 offen sc1 lds\n" \
M_"v[120:123], a[180:183], a[144:147], v[120:123]\n" \
X_"a[160:161], v32 offset:0\n" \
"s_lshr_b32 s14, s2, 3\n" \
M_"v[120:123], a[244:247], a[208:211], v[120:123]\n" \
X_"a[162:163], v32 offset:512\n" \
"v_writelane_b32 v11, s10, 57\n" \
M_"v[124:127], a[184:187], a[144:147], v[124:127]\n" \
"s_add_u32 m0, s40, 0x9000\n" \
"s_add_u32 s85, s39, 0x7000\n" \
M_"v[124:127], a[248:251], a[208:211], v[124:127]\n" \
B_"v20, s[80:83], s85 offen sc1 lds\n" \
M_"v[128:131], a[188:191], a[144:147], v[128:131]\n" \
X_"a[224:225], v32 offset:4096\n" \
"s_lshl_b32 s10, s5, 5\n" \
M_"v[128:131], a[252:255], a[208:211], v[128:131]\n" \
R_"a[144:147], v28 offset:8192\n" \
"s_min_u32 s15, s3, 2\n" \
M_"v[144:147], a[188:191], a[148:151], v[144:147]\n" \
R_"a[208:211], v29 offset:8192\n" \
"s_mul_i32 s11, s7, 0x120\n" \
M_"v[144:147], a[252:255], a[212:215], v[144:147]\n" \
"s_add_u32 m0, s40, 0xb000\n" \
"s_movk_i32 s84, 0x180\n" \
M_"v[140:143], a[184:187], a[148:151], v[140:143]\n" \
B_"v21, s[76:79], s84 offen offset:0 lds\n" \
M_"v[140:143], a[248:251], a[212:215], v[140:143]\n" \
X_"a[226:227], v32 offset:4608\n" \
M_"v[136:139], a[180:183], a[148:151], v[136:139]\n" \
"s_add_u32 s88, s41, 0x3000\n" \
"buffer_store_dwordx4 v[252:255], v20, s[80:83], s88 offen offset:0 sc1\n" \
M_"v[136:139], a[244:247], a[212:215], v[136:139]\n" \
X_"a[164:165], v33 offset:0\n" \
M_"v[132:135], a[176:179], a[148:151], v[132:135]\n" \
"s_add_u32 s16, s22, s11\n" \
"s_addc_u32 s17, s23, 0\n" \
"s_nop 0\n" \
M_"v[132:135], a[240:243], a[212:215], v[132:135]\n" \
R_"a[148:151], v28 offset:10240\n" \
M_"v[148:151], a[176:179], a[152:155], v[148:151]\n" \
R_"a[212:215], v29 offset:10240\n" \
M_"v[148:151], a[240:243], a[216:219], v[148:151]\n" \
"s_add_u32 m0, s31, 0x8000\n" \
"s_mov_b32 s84, 0x180000\n" \
M_"v[152:155], a[180:183], a[152:155], v[152:155]\n" \
B_"v26, s[68:71], s84 offen offset:0 lds\n" \
M_"v[152:155], a[244:247], a[216:219], v[152:155]\n" \
X_"a[166:167], v33 offset:512\n" \
M_"v[156:159], a[184:187], a[152:155], v[156:159]\n" \
X_"a[228:229], v33 offset:4096\n" \
M_"v[156:159], a[248:251], a[216:219], v[156:159]\n" \
"s_addk_i32 s84, 0x7c00\n" \
M_"v[160:163], a[188:191], a[152:155], v[160:163]\n" \
B_"v26, s[68:71], s84 offen offset:1024 lds\n" \
M_"v[160:163], a[252:255], a[216:219], v[160:163]\n" \
R_"a[152:155], v28 offset:12288\n" \
M_"v[176:179], a[188:191], a[156:159], v[176:179]\n" \
R_"a[216:219], v29 offset:12288\n" \
M_"v[176:179], a[252:255], a[220:223], v[176:179]\n" \
"s_addk_i32 s84, 0x7c00\n" \
M_"v[172:175], a[184:187], a[156:159], v[172:175]\n" \
B_"v27, s[68:71], s84 offen offset:2048 lds\n" \
M_"v[172:175], a[248:251], a[220:223], v[172:175]\n" \
X_"a[230:231], v33 offset:4608\n" \
M_"v[168:171], a[180:183], a[156:159], v[168:171]\n" \
X_"a[172:173], v35 offset:0\n" \
M_"v[168:171], a[244:247], a[220:223], v[168:171]\n" \
X_"a[174:175], v35 offset:512\n" \
M_"v[164:167], a[176:179], a[156:159], v[164:167]\n" \
"s_add_u32 s18, s24, s11\n" \
"v_writelane_b32 v11, s16, 9\n" \
"s_addc_u32 s19, s25, 0\n" \
M_"v[164:167], a[240:243], a[220:223], v[164:167]\n" \
R_"a[156:159], v28 offset:14336\n" \
"s_and_b32 s20, s17, 0xffff\n" \
M_"v[84:87], a[176:179], a[136:139], v[84:87]\n" \
R_"a[220:223], v29 offset:14336\n" \
"v_writelane_b32 v11, s20, 10\n" \
M_"v[84:87], a[240:243], a[200:203], v[84:87]\n" \
"s_addk_i32 s84, 0x7c00\n" \
M_"v[88:91], a[180:183], a[136:139], v[88:91]\n" \
B_"v27, s[68:71], s84 offen offset:3072 lds\n" \
M_"v[88:91], a[244:247], a[200:203], v[88:91]\n" \
"s_add_u32 m0, s30, 0x8000\n" \
"s_movk_i32 s84, 0x200\n" \
M_"v[92:95], a[184:187], a[136:139], v[92:95]\n" \
B_"v24, s[76:79], s84 offen offset:0 lds\n" \
M_"v[92:95], a[248:251], a[200:203], v[92:95]\n" \
X_"a[236:237], v35 offset:4096\n" \
"s_and_b32 s20, s19, 0xffff\n" \
M_"v[96:99], a[188:191], a[136:139], v[96:99]\n" \
X_"a[238:239], v35 offset:4608\n" \
"v_writelane_b32 v11, s18, 11\n" \
M_"v[96:99], a[252:255], a[200:203], v[96:99]\n" \
R_"a[136:139], v28 offset:4096\n" \
M_"v[112:115], a[188:191], a[140:143], v[112:115]\n" \
R_"a[200:203], v29 offset:4096\n" \
M_"v[112:115], a[252:255], a[204:207], v[112:115]\n" \
"s_movk_i32 s84, 0x2200\n" \
"s_nop 0\n" \
M_"v[108:111], a[184:187], a[140:143], v[108:111]\n" \
B_"v25, s[76:79], s84 offen offset:1024 lds\n" \
M_"v[108:111], a[248:251], a[204:207], v[108:111]\n" \
"s_add_u32 s89, s41, 0x1000\n" \
"buffer_store_dwordx4 v[200:203], v20, s[80:83], s89 offen offset:3072 sc1\n" \
M_"v[104:107], a[180:183], a[140:143], v[104:107]\n" \
X_"a[168:169], v34 offset:0\n" \
M_"v[104:107], a[244:247], a[204:207], v[104:107]\n" \
X_"a[170:171], v34 offset:512\n" \
M_"v[100:103], a[176:179], a[140:143], v[100:103]\n" \
X_"a[232:233], v34 offset:4096\n" \
M_"v[100:103], a[240:243], a[204:207], v[100:103]\n" \
R_"a[140:143], v28 offset:6144\n" \
M_"v[52:55], a[176:179], a[128:131], v[52:55]\n" \
R_"a[204:207], v29 offset:6144\n" \
M_"v[52:55], a[240:243], a[192:195], v[52:55]\n" \
"s_movk_i32 s84, 0x4200\n" \
M_"v[56:59], a[180:183], a[128:131], v[56:59]\n" \
B_"v24, s[76:79], s84 offen offset:2048 lds\n" \
M_"v[56:59], a[244:247], a[192:195], v[56:59]\n" \
"s_movk_i32 s84, 0x6200\n" \
M_"v[60:63], a[184:187], a[128:131], v[60:63]\n" \
B_"v25, s[76:79], s84 offen offset:3072 lds\n" \
M_"v[60:63], a[248:251], a[192:195], v[60:63]\n" \
X_"a[234:235], v34 offset:4608\n" \
M_"v[64:67], a[188:191], a[128:131], v[64:67]\n" \
"s_cmpk_lt_u32 s3, 3\n" \
"s_cselect_b32 s16, s16, s18\n" \
M_"v[64:67], a[252:255], a[192:195], v[64:67]\n" \
R_"a[128:131], v28 offset:0\n" \
"s_movk_i32 s18, 0xfff\n" \
M_"v[80:83], a[188:191], a[132:135], v[80:83]\n" \
R_"a[192:195], v29 offset:0\n" \
"v_writelane_b32 v11, s20, 12\n" \
M_"v[80:83], a[252:255], a[196:199], v[80:83]\n" \
"s_and_b32 s20, s29, 0xffff\n" \
"v_writelane_b32 v11, s28, 13\n" \
M_"v[76:79], a[184:187], a[132:135], v[76:79]\n" \
"v_writelane_b32 v11, s20, 14\n" \
"v_writelane_b32 v11, s16, 15\n" \
M_"v[76:79], a[248:251], a[196:199], v[76:79]\n" \
"s_cmpk_lt_u32 s3, 3\n" \
"s_cselect_b32 s16, s17, s19\n" \
M_"v[72:75], a[180:183], a[132:135], v[72:75]\n" \
"s_and_b32 s16, s16, 0xffff\n" \
"v_writelane_b32 v11, s16, 16\n" \
M_"v[72:75], a[244:247], a[196:199], v[72:75]\n" \
"s_mul_i32 s16, s5, 0x48\n" \
"s_add_i32 s17, s16, 0x47\n" \
M_"v[68:71], a[176:179], a[132:135], v[68:71]\n" \
"s_cmpk_lt_u32 s3, 3\n" \
"s_cselect_b32 s17, s17, s18\n" \
M_"v[68:71], a[240:243], a[196:199], v[68:71]\n" \
R_"a[132:135], v28 offset:2048\n" \
R_"a[196:199], v29 offset:2048\n" \
"s_waitcnt lgkmcnt(15)\n" \
M_"a[0:3], a[160:163], a[144:147], a[0:3]\n" \
"s_add_u32 m0, s30, 0x9000\n" \
"s_addk_i32 s84, 0x3000\n" \
M_"a[0:3], a[224:227], a[208:211], a[0:3]\n" \
B_"v24, s[76:79], s84 offen offset:0 lds\n" \
M_"a[4:7], a[164:167], a[144:147], a[4:7]\n" \
X_"a[176:177], v36 offset:0\n" \
"s_mulk_i32 s17, 0x480\n" \
".fill (L_pb + 65248 - .) / 4, 4, 0xBF800000\n" \
"L_pp16:\n" \
"s_cbranch_execz L_pret\n" \
M_"a[4:7], a[228:231], a[208:211], a[4:7]\n" \
X_"a[178:179], v36 offset:1024\n" \
"s_addk_i32 s17, 0x120\n" \
M_"a[12:15], a[172:175], a[144:147], a[12:15]\n" \
X_"a[240:241], v36 offset:8192\n" \
"v_writelane_b32 v11, s17, 17\n" \
"s_waitcnt lgkmcnt(15)\n" \
M_"a[12:15], a[236:239], a[208:211], a[12:15]\n" \
"s_add_u32 s88, s41, 0x1000\n" \
"buffer_store_dwordx4 v[204:207], v20, s[80:83], s88 offen offset:2048 sc1\n" \
"s_waitcnt lgkmcnt(11)\n" \
M_"a[8:11], a[144:147], a[168:171], a[8:11]\n" \
X_"a[242:243], v36 offset:9216\n" \
"s_lshl_b32 s17, s3, 5\n" \
"s_waitcnt lgkmcnt(8)\n" \
M_"a[8:11], a[208:211], a[232:235], a[8:11]\n" \
X_"a[180:181], v37 offset:0\n" \
"s_add_i32 s16, s16, s17\n" \
M_"a[24:27], a[148:151], a[168:171], a[24:27]\n" \
X_"a[182:183], v37 offset:1024\n" \
"s_mulk_i32 s16, 0x480\n" \
M_"a[24:27], a[212:215], a[232:235], a[24:27]\n" \
X_"a[244:245], v37 offset:8192\n" \
"s_mul_i32 s17, s12, 0x24000\n" \
M_"a[28:31], a[172:175], a[148:151], a[28:31]\n" \
"s_add_i32 s18, s17, 0x1b000\n" \
M_"a[28:31], a[236:239], a[212:215], a[28:31]\n" \
"s_addk_i32 s84, 0x2000\n" \
M_"a[20:23], a[164:167], a[148:151], a[20:23]\n" \
B_"v25, s[76:79], s84 offen offset:1024 lds\n" \
M_"a[20:23], a[228:231], a[212:215], a[20:23]\n" \
X_"a[246:247], v37 offset:9216\n" \
M_"a[16:19], a[160:163], a[148:151], a[16:19]\n" \
X_"a[184:185], v38 offset:0\n" \
M_"a[16:19], a[224:227], a[212:215], a[16:19]\n" \
X_"a[186:187], v38 offset:1024\n" \
M_"a[32:35], a[160:163], a[152:155], a[32:35]\n" \
"s_addk_i32 s84, 0x2000\n" \
M_"a[32:35], a[224:227], a[216:219], a[32:35]\n" \
B_"v24, s[76:79], s84 offen offset:2048 lds\n" \
M_"a[36:39], a[164:167], a[152:155], a[36:39]\n" \
X_"a[248:249], v38 offset:8192\n" \
M_"a[36:39], a[228:231], a[216:219], a[36:39]\n" \
X_"a[250:251], v38 offset:9216\n" \
M_"a[44:47], a[172:175], a[152:155], a[44:47]\n" \
X_"a[188:189], v39 offset:0\n" \
M_"a[44:47], a[236:239], a[216:219], a[44:47]\n" \
X_"a[190:191], v39 offset:1024\n" \
M_"a[40:43], a[152:155], a[168:171], a[40:43]\n" \
"s_add_u32 s89, s41, 0x1000\n" \
"buffer_store_dwordx4 v[208:211], v20, s[80:83], s89 offen offset:1024 sc1\n" \
M_"a[40:43], a[216:219], a[232:235], a[40:43]\n" \
X_"a[252:253], v39 offset:8192\n" \
M_"a[56:59], a[156:159], a[168:171], a[56:59]\n" \
X_"a[254:255], v39 offset:9216\n" \
M_"a[56:59], a[220:223], a[232:235], a[56:59]\n" \
"s_cmpk_lt_u32 s3, 3\n" \
"s_cselect_b32 s16, s16, s18\n" \
M_"a[60:63], a[172:175], a[156:159], a[60:63]\n" \
"s_xor_b32 s18, s3, 2\n" \
"v_writelane_b32 v11, s16, 18\n" \
M_"a[60:63], a[236:239], a[220:223], a[60:63]\n" \
"s_addk_i32 s84, 0x2000\n" \
M_"a[52:55], a[164:167], a[156:159], a[52:55]\n" \
B_"v25, s[76:79], s84 offen offset:3072 lds\n" \
M_"a[52:55], a[228:231], a[220:223], a[52:55]\n" \
"s_mul_i32 s16, s15, 0x2400\n" \
"s_mul_i32 s15, s15, 0x9000\n" \
M_"a[48:51], a[160:163], a[156:159], a[48:51]\n" \
"s_bitset1_b32 s16, 17\n" \
"v_writelane_b32 v11, s16, 22\n" \
M_"a[48:51], a[224:227], a[220:223], a[48:51]\n" \
"s_add_i32 s17, s17, s15\n" \
"s_mul_i32 s15, s3, 0x2400\n" \
M_"a[64:67], a[164:167], a[140:143], a[64:67]\n" \
"v_writelane_b32 v11, s17, 29\n" \
"s_add_i32 s16, s15, 0xa400\n" \
M_"a[64:67], a[228:231], a[204:207], a[64:67]\n" \
"s_mov_b32 s17, 0x8000\n" \
M_"a[80:83], a[172:175], a[140:143], a[80:83]\n" \
"s_cmpk_lt_u32 s3, 3\n" \
"s_cselect_b32 s16, s16, s17\n" \
M_"a[80:83], a[236:239], a[204:207], a[80:83]\n" \
"s_mov_b32 s17, 0x1b000\n" \
M_"a[68:71], a[140:143], a[168:171], a[68:71]\n" \
"s_cmp_lg_u32 s18, 0\n" \
"s_cselect_b32 s17, s16, s17\n" \
"s_waitcnt vmcnt(11) lgkmcnt(0)\n" \
M_"a[68:71], a[204:207], a[232:235], a[68:71]\n" \
"s_barrier\n" \
"v_xor_b32_e32 v28, 0x20000, v28\n" \
"v_xor_b32_e32 v29, 0x20000, v29\n" \
M_"v[116:119], a[176:179], a[144:147], v[116:119]\n" \
"s_add_u32 m0, s40, 0x0\n" \
"s_nop 0\n" \
"s_add_u32 s85, s39, 0x8000\n" \
M_"v[116:119], a[240:243], a[208:211], v[116:119]\n" \
B_"v20, s[80:83], s85 offen sc1 lds\n" \
M_"v[120:123], a[180:183], a[144:147], v[120:123]\n" \
X_"a[160:161], v32 offset:32768\n" \
"v_writelane_b32 v11, s16, 23\n" \
M_"v[120:123], a[244:247], a[208:211], v[120:123]\n" \
X_"a[162:163], v32 offset:33280\n" \
"s_or_b32 s16, s15, 0x10000\n" \
M_"v[124:127], a[184:187], a[144:147], v[124:127]\n" \
"s_add_u32 m0, s40, 0x1000\n" \
"s_add_u32 s85, s39, 0x9000\n" \
M_"v[124:127], a[248:251], a[208:211], v[124:127]\n" \
B_"v20, s[80:83], s85 offen sc1 lds\n" \
M_"v[128:131], a[188:191], a[144:147], v[128:131]\n" \
X_"a[224:225], v32 offset:36864\n" \
"v_writelane_b32 v11, s17, 24\n" \
M_"v[128:131], a[252:255], a[208:211], v[128:131]\n" \
R_"a[144:147], v28 offset:8192\n" \
"s_add_i32 s17, s15, 0x19000\n" \
M_"v[144:147], a[188:191], a[148:151], v[144:147]\n" \
R_"a[208:211], v29 offset:8192\n" \
"v_writelane_b32 v11, s16, 25\n" \
M_"v[144:147], a[252:255], a[212:215], v[144:147]\n" \
"s_add_u32 m0, s40, 0x3000\n" \
"s_movk_i32 s84, 0x200\n" \
M_"v[140:143], a[184:187], a[148:151], v[140:143]\n" \
B_"v21, s[76:79], s84 offen offset:0 lds\n" \
M_"v[140:143], a[248:251], a[212:215], v[140:143]\n" \
X_"a[226:227], v32 offset:37376\n" \
"s_mov_b32 s16, 0\n" \
M_"v[136:139], a[180:183], a[148:151], v[136:139]\n" \
"s_add_u32 s88, s41, 0x1000\n" \
"buffer_store_dwordx4 v[212:215], v20, s[80:83], s88 offen offset:0 sc1\n" \
M_"v[136:139], a[244:247], a[212:215], v[136:139]\n" \
X_"a[164:165], v33 offset:32768\n" \
M_"v[132:135], a[176:179], a[148:151], v[132:135]\n" \
"s_cmpk_lt_u32 s3, 3\n" \
"s_cselect_b32 s17, s17, s16\n" \
M_"v[132:135], a[240:243], a[212:215], v[132:135]\n" \
R_"a[148:151], v28 offset:10240\n" \
"s_bitset1_b32 s15, 17\n" \
M_"v[148:151], a[176:179], a[152:155], v[148:151]\n" \
R_"a[212:215], v29 offset:10240\n" \
"v_writelane_b32 v11, s17, 26\n" \
M_"v[148:151], a[240:243], a[216:219], v[148:151]\n" \
"s_add_u32 m0, s31, 0x0\n" \
"s_mov_b32 s84, 0x200000\n" \
M_"v[152:155], a[180:183], a[152:155], v[152:155]\n" \
B_"v26, s[68:71], s84 offen offset:0 lds\n" \
M_"v[152:155], a[244:247], a[216:219], v[152:155]\n" \
X_"a[166:167], v33 offset:33280\n" \
"s_movk_i32 s17, 0x2400\n" \
M_"v[156:159], a[184:187], a[152:155], v[156:159]\n" \
X_"a[228:229], v33 offset:36864\n" \
M_"v[156:159], a[248:251], a[216:219], v[156:159]\n" \
"s_addk_i32 s84, 0x7c00\n" \
M_"v[160:163], a[188:191], a[152:155], v[160:163]\n" \
B_"v26, s[68:71], s84 offen offset:1024 lds\n" \
M_"v[160:163], a[252:255], a[216:219], v[160:163]\n" \
R_"a[152:155], v28 offset:12288\n" \
M_"v[176:179], a[188:191], a[156:159], v[176:179]\n" \
R_"a[216:219], v29 offset:12288\n" \
M_"v[176:179], a[252:255], a[220:223], v[176:179]\n" \
"s_addk_i32 s84, 0x7c00\n" \
M_"v[172:175], a[184:187], a[156:159], v[172:175]\n" \
B_"v27, s[68:71], s84 offen offset:2048 lds\n" \
M_"v[172:175], a[248:251], a[220:223], v[172:175]\n" \
X_"a[230:231], v33 offset:37376\n" \
M_"v[168:171], a[180:183], a[156:159], v[168:171]\n" \
X_"a[172:173], v35 offset:32768\n" \
M_"v[168:171], a[244:247], a[220:223], v[168:171]\n" \
X_"a[174:175], v35 offset:33280\n" \
M_"v[164:167], a[176:179], a[156:159], v[164:167]\n" \
"s_cmpk_lt_u32 s3, 3\n" \
"s_cselect_b32 s15, s15, s17\n" \
M_"v[164:167], a[240:243], a[220:223], v[164:167]\n" \
R_"a[156:159], v28 offset:14336\n" \
"v_writelane_b32 v11, s15, 27\n" \
M_"v[84:87], a[176:179], a[136:139], v[84:87]\n" \
R_"a[220:223], v29 offset:14336\n" \
"s_lshl_b32 s15, s3, 10\n" \
M_"v[84:87], a[240:243], a[200:203], v[84:87]\n" \
"s_addk_i32 s84, 0x7c00\n" \
M_"v[88:91], a[180:183], a[136:139], v[88:91]\n" \
B_"v27, s[68:71], s84 offen offset:3072 lds\n" \
M_"v[88:91], a[244:247], a[200:203], v[88:91]\n" \
"s_add_u32 m0, s30, 0x20000\n" \
"s_movk_i32 s84, 0x280\n" \
M_"v[92:95], a[184:187], a[136:139], v[92:95]\n" \
B_"v24, s[76:79], s84 offen offset:0 lds\n" \
M_"v[92:95], a[248:251], a[200:203], v[92:95]\n" \
X_"a[236:237], v35 offset:36864\n" \
"s_addk_i32 s15, 0x4800\n" \
M_"v[96:99], a[188:191], a[136:139], v[96:99]\n" \
X_"a[238:239], v35 offset:37376\n" \
"v_writelane_b32 v11, s15, 28\n" \
M_"v[96:99], a[252:255], a[200:203], v[96:99]\n" \
R_"a[136:139], v28 offset:4096\n" \
"s_add_i32 s15, s14, 1\n" \
M_"v[112:115], a[188:191], a[140:143], v[112:115]\n" \
R_"a[200:203], v29 offset:4096\n" \
"s_and_b32 s15, s15, 3\n" \
M_"v[112:115], a[252:255], a[204:207], v[112:115]\n" \
"s_movk_i32 s84, 0x2280\n" \
M_"v[108:111], a[184:187], a[140:143], v[108:111]\n" \
B_"v25, s[76:79], s84 offen offset:1024 lds\n" \
M_"v[108:111], a[248:251], a[204:207], v[108:111]\n" \
"s_add_u32 s89, s42, 0x0\n" \
"buffer_store_dwordx4 v[216:219], v20, s[80:83], s89 offen offset:1024 sc1\n" \
M_"v[104:107], a[180:183], a[140:143], v[104:107]\n" \
X_"a[168:169], v34 offset:32768\n" \
"s_or_b32 s15, s15, s4\n" \
M_"v[104:107], a[244:247], a[204:207], v[104:107]\n" \
X_"a[170:171], v34 offset:33280\n" \
"s_lshl_b32 s17, s15, 2\n" \
M_"v[100:103], a[176:179], a[140:143], v[100:103]\n" \
X_"a[232:233], v34 offset:36864\n" \
"s_or_b32 s17, s17, s3\n" \
M_"v[100:103], a[240:243], a[204:207], v[100:103]\n" \
R_"a[140:143], v28 offset:6144\n" \
"s_mul_i32 s17, s17, 0x9000\n" \
M_"v[52:55], a[176:179], a[128:131], v[52:55]\n" \
R_"a[204:207], v29 offset:6144\n" \
"v_writelane_b32 v11, s17, 30\n" \
M_"v[52:55], a[240:243], a[192:195], v[52:55]\n" \
"s_movk_i32 s84, 0x4280\n" \
M_"v[56:59], a[180:183], a[128:131], v[56:59]\n" \
B_"v24, s[76:79], s84 offen offset:2048 lds\n" \
M_"v[56:59], a[244:247], a[192:195], v[56:59]\n" \
"s_movk_i32 s84, 0x6280\n" \
M_"v[60:63], a[184:187], a[128:131], v[60:63]\n" \
B_"v25, s[76:79], s84 offen offset:3072 lds\n" \
M_"v[60:63], a[248:251], a[192:195], v[60:63]\n" \
X_"a[234:235], v34 offset:37376\n" \
"s_lshl_b32 s17, s12, 2\n" \
M_"v[64:67], a[188:191], a[128:131], v[64:67]\n" \
"s_or_b32 s17, s17, s3\n" \
"s_add_i32 s14, s14, -1\n" \
M_"v[64:67], a[252:255], a[192:195], v[64:67]\n" \
R_"a[128:131], v28 offset:0\n" \
"s_xor_b32 s17, s17, 8\n" \
M_"v[80:83], a[188:191], a[132:135], v[80:83]\n" \
R_"a[192:195], v29 offset:0\n" \
"s_and_b32 s14, s14, 3\n" \
M_"v[80:83], a[252:255], a[196:199], v[80:83]\n" \
"s_mul_i32 s17, s17, 0x9000\n" \
"s_or_b32 s14, s14, s4\n" \
M_"v[76:79], a[184:187], a[132:135], v[76:79]\n" \
"v_writelane_b32 v11, s17, 31\n" \
"s_lshl_b32 s17, s14, 2\n" \
M_"v[76:79], a[248:251], a[196:199], v[76:79]\n" \
"s_or_b32 s17, s17, s3\n" \
"s_mul_i32 s17, s17, 0x9000\n" \
M_"v[72:75], a[180:183], a[132:135], v[72:75]\n" \
"v_writelane_b32 v11, s17, 32\n" \
"s_or_b32 s17, s8, s13\n" \
M_"v[72:75], a[244:247], a[196:199], v[72:75]\n" \
"s_mul_i32 s17, s17, 0x24800\n" \
"s_lshl_b32 s15, s15, 12\n" \
M_"v[68:71], a[176:179], a[132:135], v[68:71]\n" \
"s_add_i32 s18, s17, 0x3004800\n" \
"s_lshl_b32 s12, s12, 12\n" \
M_"v[68:71], a[240:243], a[196:199], v[68:71]\n" \
R_"a[132:135], v28 offset:2048\n" \
R_"a[196:199], v29 offset:2048\n" \
"s_waitcnt lgkmcnt(15)\n" \
M_"a[0:3], a[160:163], a[144:147], a[0:3]\n" \
"s_add_u32 m0, s30, 0x21000\n" \
"s_addk_i32 s84, 0x3000\n" \
M_"a[0:3], a[224:227], a[208:211], a[0:3]\n" \
B_"v24, s[76:79], s84 offen offset:0 lds\n" \
M_"a[4:7], a[164:167], a[144:147], a[4:7]\n" \
X_"a[176:177], v36 offset:32768\n" \
"s_add_i32 s19, s18, s15\n" \
M_"a[4:7], a[228:231], a[208:211], a[4:7]\n" \
X_"a[178:179], v36 offset:33792\n" \
"v_writelane_b32 v11, s19, 33\n" \
M_"a[12:15], a[172:175], a[144:147], a[12:15]\n" \
X_"a[240:241], v36 offset:40960\n" \
"s_xor_b32 s12, s12, 0x2000\n" \
"s_waitcnt lgkmcnt(15)\n" \
M_"a[12:15], a[236:239], a[208:211], a[12:15]\n" \
"s_add_u32 s88, s42, 0x0\n" \
"buffer_store_dwordx4 v[220:223], v20, s[80:83], s88 offen offset:0 sc1\n" \
"s_waitcnt lgkmcnt(11)\n" \
M_"a[8:11], a[144:147], a[168:171], a[8:11]\n" \
X_"a[242:243], v36 offset:41984\n" \
"s_lshl_b32 s14, s14, 12\n" \
"s_waitcnt lgkmcnt(8)\n" \
M_"a[8:11], a[208:211], a[232:235], a[8:11]\n" \
X_"a[180:181], v37 offset:32768\n" \
"s_sub_i32 s13, s8, s13\n" \
M_"a[24:27], a[148:151], a[168:171], a[24:27]\n" \
X_"a[182:183], v37 offset:33792\n" \
"s_add_i32 s19, s18, s12\n" \
M_"a[24:27], a[212:215], a[232:235], a[24:27]\n" \
X_"a[244:245], v37 offset:40960\n" \
"v_writelane_b32 v11, s19, 34\n" \
M_"a[28:31], a[172:175], a[148:151], a[28:31]\n" \
"s_add_i32 s18, s18, s14\n" \
"s_mul_i32 s13, s13, 0x24800\n" \
M_"a[28:31], a[236:239], a[212:215], a[28:31]\n" \
"s_addk_i32 s84, 0x2000\n" \
M_"a[20:23], a[164:167], a[148:151], a[20:23]\n" \
B_"v25, s[76:79], s84 offen offset:1024 lds\n" \
M_"a[20:23], a[228:231], a[212:215], a[20:23]\n" \
X_"a[246:247], v37 offset:41984\n" \
"v_writelane_b32 v11, s18, 35\n" \
M_"a[16:19], a[160:163], a[148:151], a[16:19]\n" \
X_"a[184:185], v38 offset:32768\n" \
"s_add_i32 s18, s13, 0x4120800\n" \
M_"a[16:19], a[224:227], a[212:215], a[16:19]\n" \
X_"a[186:187], v38 offset:33792\n" \
"s_add_i32 s15, s18, s15\n" \
M_"a[32:35], a[160:163], a[152:155], a[32:35]\n" \
"s_addk_i32 s84, 0x2000\n" \
M_"a[32:35], a[224:227], a[216:219], a[32:35]\n" \
B_"v24, s[76:79], s84 offen offset:2048 lds\n" \
M_"a[36:39], a[164:167], a[152:155], a[36:39]\n" \
X_"a[248:249], v38 offset:40960\n" \
"v_writelane_b32 v11, s15, 36\n" \
M_"a[36:39], a[228:231], a[216:219], a[36:39]\n" \
X_"a[250:251], v38 offset:41984\n" \
"s_add_i32 s12, s18, s12\n" \
M_"a[44:47], a[172:175], a[152:155], a[44:47]\n" \
X_"a[188:189], v39 offset:32768\n" \
"v_writelane_b32 v11, s12, 37\n" \
M_"a[44:47], a[236:239], a[216:219], a[44:47]\n" \
X_"a[190:191], v39 offset:33792\n" \
"s_add_i32 s18, s18, s14\n" \
M_"a[40:43], a[152:155], a[168:171], a[40:43]\n" \
"s_add_u32 s89, s42, 0x1000\n" \
"buffer_store_dwordx4 v[224:227], v20, s[80:83], s89 offen offset:3072 sc1\n" \
M_"a[40:43], a[216:219], a[232:235], a[40:43]\n" \
X_"a[252:253], v39 offset:40960\n" \
"s_or_b32 s12, s17, 0x3000000\n" \
M_"a[56:59], a[156:159], a[168:171], a[56:59]\n" \
X_"a[254:255], v39 offset:41984\n" \
"s_lshl_b32 s14, s5, 11\n" \
M_"a[56:59], a[220:223], a[232:235], a[56:59]\n" \
"v_writelane_b32 v11, s18, 38\n" \
"s_add_i32 s15, s12, s14\n" \
M_"a[60:63], a[172:175], a[156:159], a[60:63]\n" \
"v_writelane_b32 v11, s15, 39\n" \
"s_add_i32 s15, s13, 0x411c000\n" \
M_"a[60:63], a[236:239], a[220:223], a[60:63]\n" \
"s_addk_i32 s84, 0x2000\n" \
M_"a[52:55], a[164:167], a[156:159], a[52:55]\n" \
B_"v25, s[76:79], s84 offen offset:3072 lds\n" \
M_"a[52:55], a[228:231], a[220:223], a[52:55]\n" \
"s_add_i32 s18, s15, s14\n" \
"v_writelane_b32 v11, s18, 40\n" \
M_"a[48:51], a[160:163], a[156:159], a[48:51]\n" \
"s_add_i32 s17, s17, 0x3004000\n" \
"v_writelane_b32 v11, s17, 41\n" \
M_"a[48:51], a[224:227], a[220:223], a[48:51]\n" \
"s_add_i32 s13, s13, 0x4120000\n" \
"v_writelane_b32 v11, s13, 42\n" \
M_"a[64:67], a[164:167], a[140:143], a[64:67]\n" \
"s_lshl_b32 s13, s9, 2\n" \
"s_or_b32 s7, s13, s7\n" \
M_"a[64:67], a[228:231], a[204:207], a[64:67]\n" \
"s_mul_i32 s7, s7, 0x90000\n" \
"s_lshl_b32 s13, s3, 14\n" \
M_"a[80:83], a[172:175], a[140:143], a[80:83]\n" \
"s_lshl_b32 s9, s9, 7\n" \
"s_lshl_b32 s8, s8, 4\n" \
M_"a[80:83], a[236:239], a[204:207], a[80:83]\n" \
"s_add_i32 s7, s7, s13\n" \
"s_or_b32 s13, s8, s9\n" \
M_"a[68:71], a[140:143], a[168:171], a[68:71]\n" \
"s_sub_i32 s8, s8, s9\n" \
"s_mulk_i32 s13, 0x480\n" \
"s_waitcnt vmcnt(11) lgkmcnt(0)\n" \
M_"a[68:71], a[204:207], a[232:235], a[68:71]\n" \
"s_barrier\n" \
M_"v[116:119], a[176:179], a[144:147], v[116:119]\n" \
"s_add_u32 m0, s40, 0x8000\n" \
"s_add_u32 s85, s39, 0xa000\n" \
M_"v[116:119], a[240:243], a[208:211], v[116:119]\n" \
B_"v20, s[80:83], s85 offen sc1 lds\n" \
M_"v[120:123], a[180:183], a[144:147], v[120:123]\n" \
X_"a[160:161], v32 offset:0\n" \
"v_accvgpr_read_b32 v212, a108\n" \
M_"v[120:123], a[244:247], a[208:211], v[120:123]\n" \
X_"a[162:163], v32 offset:512\n" \
"v_accvgpr_read_b32 v213, a109\n" \
M_"v[124:127], a[184:187], a[144:147], v[124:127]\n" \
"s_add_u32 m0, s40, 0x9000\n" \
"s_add_u32 s85, s39, 0xb000\n" \
M_"v[124:127], a[248:251], a[208:211], v[124:127]\n" \
B_"v20, s[80:83], s85 offen sc1 lds\n" \
M_"v[128:131], a[188:191], a[144:147], v[128:131]\n" \
X_"a[224:225], v32 offset:4096\n" \
"v_accvgpr_read_b32 v214, a110\n" \
M_"v[128:131], a[252:255], a[208:211], v[128:131]\n" \
R_"a[144:147], v28 offset:40960\n" \
"v_accvgpr_read_b32 v215, a111\n" \
M_"v[144:147], a[188:191], a[148:151], v[144:147]\n" \
R_"a[208:211], v29 offset:40960\n" \
"v_accvgpr_read_b32 v180, a124\n" \
M_"v[144:147], a[252:255], a[212:215], v[144:147]\n" \
"s_add_u32 m0, s40, 0xb000\n" \
"s_movk_i32 s84, 0x280\n" \
"s_nop 0\n" \
M_"v[140:143], a[184:187], a[148:151], v[140:143]\n" \
B_"v21, s[76:79], s84 offen offset:0 lds\n" \
M_"v[140:143], a[248:251], a[212:215], v[140:143]\n" \
X_"a[226:227], v32 offset:4608\n" \
"v_accvgpr_read_b32 v181, a125\n" \
M_"v[136:139], a[180:183], a[148:151], v[136:139]\n" \
"s_add_u32 s88, s42, 0x1000\n" \
"buffer_store_dwordx4 v[228:231], v20, s[80:83], s88 offen offset:2048 sc1\n" \
M_"v[136:139], a[244:247], a[212:215], v[136:139]\n" \
X_"a[164:165], v33 offset:0\n" \
"v_accvgpr_read_b32 v182, a126\n" \
M_"v[132:135], a[176:179], a[148:151], v[132:135]\n" \
"v_accvgpr_read_b32 v183, a127\n" \
C_"v212, v212, v213\n" \
M_"v[132:135], a[240:243], a[212:215], v[132:135]\n" \
R_"a[148:151], v28 offset:43008\n" \
C_"v213, v214, v215\n" \
M_"v[148:151], a[176:179], a[152:155], v[148:151]\n" \
R_"a[212:215], v29 offset:43008\n" \
C_"v214, v180, v181\n" \
M_"v[148:151], a[240:243], a[216:219], v[148:151]\n" \
"s_add_u32 m0, s31, 0x8000\n" \
"s_mov_b32 s84, 0x280000\n" \
M_"v[152:155], a[180:183], a[152:155], v[152:155]\n" \
B_"v26, s[68:71], s84 offen offset:0 lds\n" \
M_"v[152:155], a[244:247], a[216:219], v[152:155]\n" \
X_"a[166:167], v33 offset:512\n" \
C_"v215, v182, v183\n" \
M_"v[156:159], a[184:187], a[152:155], v[156:159]\n" \
X_"a[228:229], v33 offset:4096\n" \
"v_accvgpr_read_b32 v208, a104\n" \
M_"v[156:159], a[248:251], a[216:219], v[156:159]\n" \
"s_addk_i32 s84, 0x7c00\n" \
M_"v[160:163], a[188:191], a[152:155], v[160:163]\n" \
B_"v26, s[68:71], s84 offen offset:1024 lds\n" \
M_"v[160:163], a[252:255], a[216:219], v[160:163]\n" \
R_"a[152:155], v28 offset:45056\n" \
"v_accvgpr_read_b32 v209, a105\n" \
M_"v[176:179], a[188:191], a[156:159], v[176:179]\n" \
R_"a[216:219], v29 offset:45056\n" \
"v_accvgpr_read_b32 v210, a106\n" \
M_"v[176:179], a[252:255], a[220:223], v[176:179]\n" \
"s_addk_i32 s84, 0x7c00\n" \
M_"v[172:175], a[184:187], a[156:159], v[172:175]\n" \
B_"v27, s[68:71], s84 offen offset:2048 lds\n" \
M_"v[172:175], a[248:251], a[220:223], v[172:175]\n" \
X_"a[230:231], v33 offset:4608\n" \
"v_accvgpr_read_b32 v211, a107\n" \
M_"v[168:171], a[180:183], a[156:159], v[168:171]\n" \
X_"a[172:173], v35 offset:0\n" \
"v_accvgpr_read_b32 v180, a120\n" \
M_"v[168:171], a[244:247], a[220:223], v[168:171]\n" \
X_"a[174:175], v35 offset:512\n" \
"v_accvgpr_read_b32 v181, a121\n" \
M_"v[164:167], a[176:179], a[156:159], v[164:167]\n" \
"v_accvgpr_read_b32 v182, a122\n" \
"v_accvgpr_read_b32 v183, a123\n" \
M_"v[164:167], a[240:243], a[220:223], v[164:167]\n" \
R_"a[156:159], v28 offset:47104\n" \
C_"v208, v208, v209\n" \
M_"v[84:87], a[176:179], a[136:139], v[84:87]\n" \
R_"a[220:223], v29 offset:47104\n" \
C_"v209, v210, v211\n" \
M_"v[84:87], a[240:243], a[200:203], v[84:87]\n" \
"s_addk_i32 s84, 0x7c00\n" \
M_"v[88:91], a[180:183], a[136:139], v[88:91]\n" \
B_"v27, s[68:71], s84 offen offset:3072 lds\n" \
M_"v[88:91], a[244:247], a[200:203], v[88:91]\n" \
"s_add_u32 m0, s30, 0x0\n" \
"s_movk_i32 s84, 0x300\n" \
"s_nop 0\n" \
M_"v[92:95], a[184:187], a[136:139], v[92:95]\n" \
B_"v24, s[76:79], s84 offen offset:0 lds\n" \
M_"v[92:95], a[248:251], a[200:203], v[92:95]\n" \
X_"a[236:237], v35 offset:4096\n" \
C_"v210, v180, v181\n" \
M_"v[96:99], a[188:191], a[136:139], v[96:99]\n" \
X_"a[238:239], v35 offset:4608\n" \
C_"v211, v182, v183\n" \
M_"v[96:99], a[252:255], a[200:203], v[96:99]\n" \
R_"a[136:139], v28 offset:36864\n" \
"v_accvgpr_read_b32 v204, a100\n" \
M_"v[112:115], a[188:191], a[140:143], v[112:115]\n" \
R_"a[200:203], v29 offset:36864\n" \
"v_accvgpr_read_b32 v205, a101\n" \
M_"v[112:115], a[252:255], a[204:207], v[112:115]\n" \
"s_movk_i32 s84, 0x2300\n" \
"s_nop 0\n" \
M_"v[108:111], a[184:187], a[140:143], v[108:111]\n" \
B_"v25, s[76:79], s84 offen offset:1024 lds\n" \
M_"v[108:111], a[248:251], a[204:207], v[108:111]\n" \
"s_add_u32 s89, s42, 0x1000\n" \
"buffer_store_dwordx4 v[232:235], v20, s[80:83], s89 offen offset:1024 sc1\n" \
M_"v[104:107], a[180:183], a[140:143], v[104:107]\n" \
X_"a[168:169], v34 offset:0\n" \
"v_accvgpr_read_b32 v206, a102\n" \
M_"v[104:107], a[244:247], a[204:207], v[104:107]\n" \
X_"a[170:171], v34 offset:512\n" \
"v_accvgpr_read_b32 v207, a103\n" \
M_"v[100:103], a[176:179], a[140:143], v[100:103]\n" \
X_"a[232:233], v34 offset:4096\n" \
"v_accvgpr_read_b32 v180, a116\n" \
M_"v[100:103], a[240:243], a[204:207], v[100:103]\n" \
R_"a[140:143], v28 offset:38912\n" \
"v_accvgpr_read_b32 v181, a117\n" \
M_"v[52:55], a[176:179], a[128:131], v[52:55]\n" \
R_"a[204:207], v29 offset:38912\n" \
"v_accvgpr_read_b32 v182, a118\n" \
M_"v[52:55], a[240:243], a[192:195], v[52:55]\n" \
"s_movk_i32 s84, 0x4300\n" \
M_"v[56:59], a[180:183], a[128:131], v[56:59]\n" \
B_"v24, s[76:79], s84 offen offset:2048 lds\n" \
M_"v[56:59], a[244:247], a[192:195], v[56:59]\n" \
"s_movk_i32 s84, 0x6300\n" \
M_"v[60:63], a[184:187], a[128:131], v[60:63]\n" \
B_"v25, s[76:79], s84 offen offset:3072 lds\n" \
M_"v[60:63], a[248:251], a[192:195], v[60:63]\n" \
X_"a[234:235], v34 offset:4608\n" \
"v_accvgpr_read_b32 v183, a119\n" \
M_"v[64:67], a[188:191], a[128:131], v[64:67]\n" \
C_"v204, v204, v205\n" \
C_"v205, v206, v207\n" \
M_"v[64:67], a[252:255], a[192:195], v[64:67]\n" \
R_"a[128:131], v28 offset:32768\n" \
C_"v206, v180, v181\n" \
M_"v[80:83], a[188:191], a[132:135], v[80:83]\n" \
R_"a[192:195], v29 offset:32768\n" \
C_"v207, v182, v183\n" \
M_"v[80:83], a[252:255], a[196:199], v[80:83]\n" \
"v_accvgpr_read_b32 v200, a96\n" \
"v_accvgpr_read_b32 v201, a97\n" \
M_"v[76:79], a[184:187], a[132:135], v[76:79]\n" \
"v_accvgpr_read_b32 v202, a98\n" \
"v_accvgpr_read_b32 v203, a99\n" \
M_"v[76:79], a[248:251], a[196:199], v[76:79]\n" \
"v_accvgpr_read_b32 v180, a112\n" \
"v_accvgpr_read_b32 v181, a113\n" \
M_"v[72:75], a[180:183], a[132:135], v[72:75]\n" \
"v_accvgpr_read_b32 v182, a114\n" \
"v_accvgpr_read_b32 v183, a115\n" \
M_"v[72:75], a[244:247], a[196:199], v[72:75]\n" \
C_"v200, v200, v201\n" \
C_"v201, v202, v203\n" \
M_"v[68:71], a[176:179], a[132:135], v[68:71]\n" \
C_"v202, v180, v181\n" \
C_"v203, v182, v183\n" \
M_"v[68:71], a[240:243], a[196:199], v[68:71]\n" \
R_"a[132:135], v28 offset:34816\n" \
R_"a[196:199], v29 offset:34816\n" \
"s_waitcnt lgkmcnt(15)\n" \
M_"a[0:3], a[160:163], a[144:147], a[0:3]\n" \
"s_add_u32 m0, s30, 0x1000\n" \
"s_addk_i32 s84, 0x3000\n" \
M_"a[0:3], a[224:227], a[208:211], a[0:3]\n" \
B_"v24, s[76:79], s84 offen offset:0 lds\n" \
M_"a[4:7], a[164:167], a[144:147], a[4:7]\n" \
X_"a[176:177], v36 offset:0\n" \
"v_accvgpr_read_b32 v196, a76\n" \
M_"a[4:7], a[228:231], a[208:211], a[4:7]\n" \
X_"a[178:179], v36 offset:1024\n" \
"v_accvgpr_read_b32 v197, a77\n" \
M_"a[12:15], a[172:175], a[144:147], a[12:15]\n" \
".fill (L_pb + 69344 - .) / 4, 4, 0xBF800000\n" \
"L_pp17:\n" \
"s_cbranch_execz L_pret\n" \
X_"a[240:241], v36 offset:8192\n" \
"v_accvgpr_read_b32 v198, a78\n" \
"s_waitcnt lgkmcnt(15)\n" \
M_"a[12:15], a[236:239], a[208:211], a[12:15]\n" \
"s_add_u32 s88, s42, 0x1000\n" \
"buffer_store_dwordx4 v[236:239], v20, s[80:83], s88 offen offset:0 sc1\n" \
"s_waitcnt lgkmcnt(11)\n" \
M_"a[8:11], a[144:147], a[168:171], a[8:11]\n" \
X_"a[242:243], v36 offset:9216\n" \
"v_accvgpr_read_b32 v199, a79\n" \
"s_waitcnt lgkmcnt(8)\n" \
M_"a[8:11], a[208:211], a[232:235], a[8:11]\n" \
X_"a[180:181], v37 offset:0\n" \
"v_accvgpr_read_b32 v180, a92\n" \
M_"a[24:27], a[148:151], a[168:171], a[24:27]\n" \
X_"a[182:183], v37 offset:1024\n" \
"v_accvgpr_read_b32 v181, a93\n" \
M_"a[24:27], a[212:215], a[232:235], a[24:27]\n" \
X_"a[244:245], v37 offset:8192\n" \
"v_accvgpr_read_b32 v182, a94\n" \
M_"a[28:31], a[172:175], a[148:151], a[28:31]\n" \
"v_accvgpr_read_b32 v183, a95\n" \
C_"v196, v196, v197\n" \
M_"a[28:31], a[236:239], a[212:215], a[28:31]\n" \
"s_addk_i32 s84, 0x2000\n" \
"s_nop 0\n" \
M_"a[20:23], a[164:167], a[148:151], a[20:23]\n" \
B_"v25, s[76:79], s84 offen offset:1024 lds\n" \
M_"a[20:23], a[228:231], a[212:215], a[20:23]\n" \
X_"a[246:247], v37 offset:9216\n" \
C_"v197, v198, v199\n" \
M_"a[16:19], a[160:163], a[148:151], a[16:19]\n" \
X_"a[184:185], v38 offset:0\n" \
C_"v198, v180, v181\n" \
M_"a[16:19], a[224:227], a[212:215], a[16:19]\n" \
X_"a[186:187], v38 offset:1024\n" \
C_"v199, v182, v183\n" \
M_"a[32:35], a[160:163], a[152:155], a[32:35]\n" \
"s_addk_i32 s84, 0x2000\n" \
"s_nop 0\n" \
M_"a[32:35], a[224:227], a[216:219], a[32:35]\n" \
B_"v24, s[76:79], s84 offen offset:2048 lds\n" \
M_"a[36:39], a[164:167], a[152:155], a[36:39]\n" \
X_"a[248:249], v38 offset:8192\n" \
"v_accvgpr_read_b32 v192, a72\n" \
M_"a[36:39], a[228:231], a[216:219], a[36:39]\n" \
X_"a[250:251], v38 offset:9216\n" \
"v_accvgpr_read_b32 v193, a73\n" \
M_"a[44:47], a[172:175], a[152:155], a[44:47]\n" \
X_"a[188:189], v39 offset:0\n" \
"v_accvgpr_read_b32 v194, a74\n" \
M_"a[44:47], a[236:239], a[216:219], a[44:47]\n" \
X_"a[190:191], v39 offset:1024\n" \
"v_accvgpr_read_b32 v195, a75\n" \
M_"a[40:43], a[152:155], a[168:171], a[40:43]\n" \
"s_add_u32 s89, s42, 0x3000\n" \
"buffer_store_dwordx4 v[240:243], v20, s[80:83], s89 offen offset:3072 sc1\n" \
M_"a[40:43], a[216:219], a[232:235], a[40:43]\n" \
X_"a[252:253], v39 offset:8192\n" \
"v_accvgpr_read_b32 v180, a88\n" \
M_"a[56:59], a[156:159], a[168:171], a[56:59]\n" \
X_"a[254:255], v39 offset:9216\n" \
"v_accvgpr_read_b32 v181, a89\n" \
M_"a[56:59], a[220:223], a[232:235], a[56:59]\n" \
"v_accvgpr_read_b32 v182, a90\n" \
"v_accvgpr_read_b32 v183, a91\n" \
M_"a[60:63], a[172:175], a[156:159], a[60:63]\n" \
C_"v192, v192, v193\n" \
C_"v193, v194, v195\n" \
M_"a[60:63], a[236:239], a[220:223], a[60:63]\n" \
"s_addk_i32 s84, 0x2000\n" \
M_"a[52:55], a[164:167], a[156:159], a[52:55]\n" \
B_"v25, s[76:79], s84 offen offset:3072 lds\n" \
M_"a[52:55], a[228:231], a[220:223], a[52:55]\n" \
C_"v194, v180, v181\n" \
C_"v195, v182, v183\n" \
M_"a[48:51], a[160:163], a[156:159], a[48:51]\n" \
"s_mulk_i32 s8, 0x480\n" \
"s_or_b32 s13, s13, s11\n" \
M_"a[48:51], a[224:227], a[220:223], a[48:51]\n" \
"s_or_b32 s8, s8, s11\n" \
"s_and_b32 s11, s2, 1\n" \
M_"a[64:67], a[164:167], a[140:143], a[64:67]\n" \
"s_cmp_lg_u32 s11, 0\n" \
"s_cselect_b32 s12, s15, s12\n" \
M_"a[64:67], a[228:231], a[204:207], a[64:67]\n" \
"s_bfe_u32 s9, s2, 0x20001\n" \
"s_add_i32 s15, s7, 0x4240000\n" \
M_"a[80:83], a[172:175], a[140:143], a[80:83]\n" \
"s_addk_i32 s12, 0x4000\n" \
"s_lshl_b32 s5, s5, 16\n" \
M_"a[80:83], a[236:239], a[204:207], a[80:83]\n" \
"s_xor_b32 s9, s9, s3\n" \
M_"a[68:71], a[140:143], a[168:171], a[68:71]\n" \
"s_cmp_lg_u32 s9, 0\n" \
"s_cselect_b32 s12, s6, s12\n" \
"s_waitcnt vmcnt(11) lgkmcnt(0)\n" \
M_"a[68:71], a[204:207], a[232:235], a[68:71]\n" \
"s_barrier\n" \
"v_mov_b32_e32 v45, s62\n" \
"s_mov_b64 exec, 1\n" \
"buffer_store_dword v45, off, s[80:83], s37 offset:0 sc1\n" \
"s_mov_b64 exec, -1\n" \
"v_xor_b32_e32 v28, 0x20000, v28\n" \
"v_xor_b32_e32 v29, 0x20000, v29\n" \
M_"v[116:119], a[176:179], a[144:147], v[116:119]\n" \
"s_add_u32 m0, s40, 0x0\n" \
"s_add_u32 s85, s39, 0xc000\n" \
M_"v[116:119], a[240:243], a[208:211], v[116:119]\n" \
B_"v20, s[80:83], s85 offen sc1 lds\n" \
M_"v[120:123], a[180:183], a[144:147], v[120:123]\n" \
X_"a[160:161], v32 offset:32768\n" \
"s_add_i32 s5, s15, s5\n" \
"s_nop 0\n" \
M_"v[120:123], a[244:247], a[208:211], v[120:123]\n" \
X_"a[162:163], v32 offset:33280\n" \
"v_writelane_b32 v11, s12, 43\n" \
M_"v[124:127], a[184:187], a[144:147], v[124:127]\n" \
"s_add_u32 m0, s40, 0x1000\n" \
"s_add_u32 s85, s39, 0xd000\n" \
M_"v[124:127], a[248:251], a[208:211], v[124:127]\n" \
B_"v20, s[80:83], s85 offen sc1 lds\n" \
M_"v[128:131], a[188:191], a[144:147], v[128:131]\n" \
X_"a[224:225], v32 offset:36864\n" \
"s_add_i32 s7, s7, 0x42c0000\n" \
M_"v[128:131], a[252:255], a[208:211], v[128:131]\n" \
R_"a[144:147], v28 offset:8192\n" \
"v_writelane_b32 v11, s5, 44\n" \
M_"v[144:147], a[188:191], a[148:151], v[144:147]\n" \
R_"a[208:211], v29 offset:8192\n" \
"s_lshl_b32 s5, s11, 10\n" \
M_"v[144:147], a[252:255], a[212:215], v[144:147]\n" \
"s_add_u32 m0, s40, 0x3000\n" \
"s_movk_i32 s84, 0x300\n" \
M_"v[140:143], a[184:187], a[148:151], v[140:143]\n" \
B_"v21, s[76:79], s84 offen offset:0 lds\n" \
M_"v[140:143], a[248:251], a[212:215], v[140:143]\n" \
X_"a[226:227], v32 offset:37376\n" \
"s_or_b32 s5, s7, s5\n" \
M_"v[136:139], a[180:183], a[148:151], v[136:139]\n" \
"s_cmp_lg_u32 s9, 0\n" \
"s_cselect_b32 s5, s6, s5\n" \
M_"v[136:139], a[244:247], a[212:215], v[136:139]\n" \
X_"a[164:165], v33 offset:32768\n" \
"s_add_i32 s8, s8, 0x21c000\n" \
M_"v[132:135], a[176:179], a[148:151], v[132:135]\n" \
"v_writelane_b32 v11, s5, 45\n" \
"s_add_i32 s5, s13, s10\n" \
M_"v[132:135], a[240:243], a[212:215], v[132:135]\n" \
R_"a[148:151], v28 offset:10240\n" \
"v_writelane_b32 v11, s5, 46\n" \
M_"v[148:151], a[176:179], a[152:155], v[148:151]\n" \
R_"a[212:215], v29 offset:10240\n" \
"s_add_i32 s5, s8, s10\n" \
M_"v[148:151], a[240:243], a[216:219], v[148:151]\n" \
"s_add_u32 m0, s31, 0x0\n" \
"s_mov_b32 s84, 0x300000\n" \
M_"v[152:155], a[180:183], a[152:155], v[152:155]\n" \
B_"v26, s[68:71], s84 offen offset:0 lds\n" \
M_"v[152:155], a[244:247], a[216:219], v[152:155]\n" \
"s_addk_i32 s84, 0x7c00\n" \
"s_nop 0\n" \
M_"v[156:159], a[184:187], a[152:155], v[156:159]\n" \
B_"v26, s[68:71], s84 offen offset:1024 lds\n" \
M_"v[156:159], a[248:251], a[216:219], v[156:159]\n" \
X_"a[166:167], v33 offset:33280\n" \
"v_writelane_b32 v11, s5, 47\n" \
M_"v[160:163], a[188:191], a[152:155], v[160:163]\n" \
X_"a[228:229], v33 offset:36864\n" \
M_"v[160:163], a[252:255], a[216:219], v[160:163]\n" \
R_"a[152:155], v28 offset:12288\n" \
M_"v[176:179], a[188:191], a[156:159], v[176:179]\n" \
R_"a[216:219], v29 offset:12288\n" \
M_"v[176:179], a[252:255], a[220:223], v[176:179]\n" \
"s_addk_i32 s84, 0x7c00\n" \
M_"v[172:175], a[184:187], a[156:159], v[172:175]\n" \
B_"v27, s[68:71], s84 offen offset:2048 lds\n" \
M_"v[172:175], a[248:251], a[220:223], v[172:175]\n" \
X_"a[230:231], v33 offset:37376\n" \
M_"v[168:171], a[180:183], a[156:159], v[168:171]\n" \
"s_addk_i32 s84, 0x7c00\n" \
M_"v[168:171], a[244:247], a[220:223], v[168:171]\n" \
B_"v27, s[68:71], s84 offen offset:3072 lds\n" \
M_"v[164:167], a[176:179], a[156:159], v[164:167]\n" \
X_"a[172:173], v35 offset:32768\n" \
M_"v[164:167], a[240:243], a[220:223], v[164:167]\n" \
R_"a[156:159], v28 offset:14336\n" \
M_"v[84:87], a[176:179], a[136:139], v[84:87]\n" \
R_"a[220:223], v29 offset:14336\n" \
M_"v[84:87], a[240:243], a[200:203], v[84:87]\n" \
"s_add_u32 s88, s42, 0x3000\n" \
"buffer_store_dwordx4 v[244:247], v20, s[80:83], s88 offen offset:2048 sc1\n" \
M_"v[88:91], a[180:183], a[136:139], v[88:91]\n" \
X_"a[174:175], v35 offset:33280\n" \
M_"v[88:91], a[244:247], a[200:203], v[88:91]\n" \
X_"a[236:237], v35 offset:36864\n" \
M_"v[92:95], a[184:187], a[136:139], v[92:95]\n" \
X_"a[238:239], v35 offset:37376\n" \
M_"v[92:95], a[248:251], a[200:203], v[92:95]\n" \
"s_add_u32 m0, s30, 0x8000\n" \
"s_movk_i32 s84, 0x380\n" \
M_"v[96:99], a[188:191], a[136:139], v[96:99]\n" \
B_"v24, s[76:79], s84 offen offset:0 lds\n" \
M_"v[96:99], a[252:255], a[200:203], v[96:99]\n" \
R_"a[136:139], v28 offset:4096\n" \
M_"v[112:115], a[188:191], a[140:143], v[112:115]\n" \
R_"a[200:203], v29 offset:4096\n" \
M_"v[112:115], a[252:255], a[204:207], v[112:115]\n" \
X_"a[168:169], v34 offset:32768\n" \
M_"v[108:111], a[184:187], a[140:143], v[108:111]\n" \
"s_movk_i32 s84, 0x2380\n" \
M_"v[108:111], a[248:251], a[204:207], v[108:111]\n" \
B_"v25, s[76:79], s84 offen offset:1024 lds\n" \
M_"v[104:107], a[180:183], a[140:143], v[104:107]\n" \
X_"a[170:171], v34 offset:33280\n" \
M_"v[104:107], a[244:247], a[204:207], v[104:107]\n" \
X_"a[232:233], v34 offset:36864\n" \
M_"v[100:103], a[176:179], a[140:143], v[100:103]\n" \
"s_cmp_lg_u32 s11, 0\n" \
"s_cselect_b32 s5, s8, s13\n" \
M_"v[100:103], a[240:243], a[204:207], v[100:103]\n" \
R_"a[140:143], v28 offset:6144\n" \
"s_addk_i32 s5, 0x100\n" \
M_"v[52:55], a[176:179], a[128:131], v[52:55]\n" \
R_"a[204:207], v29 offset:6144\n" \
M_"v[52:55], a[240:243], a[192:195], v[52:55]\n" \
"s_movk_i32 s84, 0x4380\n" \
M_"v[56:59], a[180:183], a[128:131], v[56:59]\n" \
B_"v24, s[76:79], s84 offen offset:2048 lds\n" \
M_"v[56:59], a[244:247], a[192:195], v[56:59]\n" \
"s_movk_i32 s84, 0x6380\n" \
M_"v[60:63], a[184:187], a[128:131], v[60:63]\n" \
B_"v25, s[76:79], s84 offen offset:3072 lds\n" \
M_"v[60:63], a[248:251], a[192:195], v[60:63]\n" \
X_"a[234:235], v34 offset:37376\n" \
M_"v[64:67], a[188:191], a[128:131], v[64:67]\n" \
"s_cmp_lg_u32 s9, 0\n" \
"s_cselect_b32 s5, s6, s5\n" \
M_"v[64:67], a[252:255], a[192:195], v[64:67]\n" \
R_"a[128:131], v28 offset:0\n" \
"v_writelane_b32 v11, s5, 48\n" \
M_"v[80:83], a[188:191], a[132:135], v[80:83]\n" \
R_"a[192:195], v29 offset:0\n" \
"s_or_b32 s5, s7, s14\n" \
M_"v[80:83], a[252:255], a[196:199], v[80:83]\n" \
"v_writelane_b32 v11, s5, 56\n" \
"s_lshl_b32 s5, s3, 17\n" \
M_"v[76:79], a[184:187], a[132:135], v[76:79]\n" \
"s_add_i32 s5, s15, s5\n" \
"s_or_b32 s4, s5, s4\n" \
M_"v[76:79], a[248:251], a[196:199], v[76:79]\n" \
"s_or_b32 s4, s4, 0x3840\n" \
"v_writelane_b32 v11, s4, 6\n" \
M_"v[72:75], a[180:183], a[132:135], v[72:75]\n" \
"s_mov_b32 s4, 0xff0000\n" \
M_"v[72:75], a[244:247], a[196:199], v[72:75]\n" \
"s_cmp_lg_u32 s9, 0\n" \
"s_cselect_b32 s4, s16, s4\n" \
M_"v[68:71], a[176:179], a[132:135], v[68:71]\n" \
"v_writelane_b32 v11, s4, 7\n" \
"s_lshl_b32 s4, s2, 16\n" \
M_"v[68:71], a[240:243], a[196:199], v[68:71]\n" \
R_"a[132:135], v28 offset:2048\n" \
R_"a[196:199], v29 offset:2048\n" \
"s_waitcnt lgkmcnt(15)\n" \
M_"a[0:3], a[160:163], a[144:147], a[0:3]\n" \
"s_add_u32 m0, s30, 0x9000\n" \
"s_addk_i32 s84, 0x3000\n" \
M_"a[0:3], a[224:227], a[208:211], a[0:3]\n" \
B_"v24, s[76:79], s84 offen offset:0 lds\n" \
M_"a[4:7], a[164:167], a[144:147], a[4:7]\n" \
X_"a[176:177], v36 offset:32768\n" \
"s_add_i32 s5, s4, 0x10000\n" \
M_"a[4:7], a[228:231], a[208:211], a[4:7]\n" \
X_"a[178:179], v36 offset:33792\n" \
"s_and_b32 s5, s5, 0x70000\n" \
"s_waitcnt lgkmcnt(15)\n" \
M_"a[12:15], a[172:175], a[144:147], a[12:15]\n" \
X_"a[240:241], v36 offset:40960\n" \
"s_add_i32 s5, s15, s5\n" \
"s_waitcnt lgkmcnt(15)\n" \
M_"a[12:15], a[236:239], a[208:211], a[12:15]\n" \
"s_add_u32 s89, s42, 0x3000\n" \
"buffer_store_dwordx4 v[184:187], v20, s[80:83], s89 offen offset:1024 sc1\n" \
"s_waitcnt lgkmcnt(11)\n" \
M_"a[8:11], a[144:147], a[168:171], a[8:11]\n" \
X_"a[242:243], v36 offset:41984\n" \
"v_writelane_b32 v11, s5, 49\n" \
"s_waitcnt lgkmcnt(8)\n" \
M_"a[8:11], a[208:211], a[232:235], a[8:11]\n" \
X_"a[180:181], v37 offset:32768\n" \
"s_add_i32 s5, s4, 0x20000\n" \
M_"a[24:27], a[148:151], a[168:171], a[24:27]\n" \
X_"a[182:183], v37 offset:33792\n" \
"s_and_b32 s5, s5, 0x70000\n" \
M_"a[24:27], a[212:215], a[232:235], a[24:27]\n" \
X_"a[244:245], v37 offset:40960\n" \
"s_add_i32 s5, s15, s5\n" \
M_"a[28:31], a[172:175], a[148:151], a[28:31]\n" \
"s_bitset1_b32 s5, 11\n" \
"v_writelane_b32 v11, s5, 50\n" \
M_"a[28:31], a[236:239], a[212:215], a[28:31]\n" \
"s_addk_i32 s84, 0x2000\n" \
M_"a[20:23], a[164:167], a[148:151], a[20:23]\n" \
B_"v25, s[76:79], s84 offen offset:1024 lds\n" \
M_"a[20:23], a[228:231], a[212:215], a[20:23]\n" \
X_"a[246:247], v37 offset:41984\n" \
"s_add_i32 s5, s4, 0x30000\n" \
M_"a[16:19], a[160:163], a[148:151], a[16:19]\n" \
X_"a[184:185], v38 offset:32768\n" \
"s_and_b32 s5, s5, 0x70000\n" \
M_"a[16:19], a[224:227], a[212:215], a[16:19]\n" \
X_"a[186:187], v38 offset:33792\n" \
"s_add_i32 s5, s15, s5\n" \
M_"a[32:35], a[160:163], a[152:155], a[32:35]\n" \
"s_addk_i32 s84, 0x2000\n" \
M_"a[32:35], a[224:227], a[216:219], a[32:35]\n" \
B_"v24, s[76:79], s84 offen offset:2048 lds\n" \
M_"a[36:39], a[164:167], a[152:155], a[36:39]\n" \
X_"a[248:249], v38 offset:40960\n" \
"s_bitset1_b32 s5, 12\n" \
"s_nop 0\n" \
M_"a[36:39], a[228:231], a[216:219], a[36:39]\n" \
X_"a[250:251], v38 offset:41984\n" \
"v_writelane_b32 v11, s5, 51\n" \
M_"a[44:47], a[172:175], a[152:155], a[44:47]\n" \
X_"a[188:189], v39 offset:32768\n" \
"s_and_b32 s5, s4, 0x70000\n" \
M_"a[44:47], a[236:239], a[216:219], a[44:47]\n" \
X_"a[190:191], v39 offset:33792\n" \
"s_xor_b32 s5, s5, 0x40000\n" \
M_"a[40:43], a[152:155], a[168:171], a[40:43]\n" \
"s_add_u32 s88, s42, 0x3000\n" \
"buffer_store_dwordx4 v[188:191], v20, s[80:83], s88 offen offset:0 sc1\n" \
M_"a[40:43], a[216:219], a[232:235], a[40:43]\n" \
X_"a[252:253], v39 offset:40960\n" \
"s_add_i32 s5, s15, s5\n" \
M_"a[56:59], a[156:159], a[168:171], a[56:59]\n" \
X_"a[254:255], v39 offset:41984\n" \
"s_or_b32 s5, s5, 0x1800\n" \
M_"a[56:59], a[220:223], a[232:235], a[56:59]\n" \
"v_writelane_b32 v11, s5, 52\n" \
"s_add_i32 s5, s4, 0x50000\n" \
M_"a[60:63], a[172:175], a[156:159], a[60:63]\n" \
"s_and_b32 s5, s5, 0x70000\n" \
"s_add_i32 s5, s15, s5\n" \
M_"a[60:63], a[236:239], a[220:223], a[60:63]\n" \
"s_addk_i32 s84, 0x2000\n" \
M_"a[52:55], a[164:167], a[156:159], a[52:55]\n" \
B_"v25, s[76:79], s84 offen offset:3072 lds\n" \
M_"a[52:55], a[228:231], a[220:223], a[52:55]\n" \
"s_bitset1_b32 s5, 13\n" \
"v_writelane_b32 v11, s5, 53\n" \
M_"a[48:51], a[160:163], a[156:159], a[48:51]\n" \
"s_add_i32 s5, s4, 0x60000\n" \
"s_and_b32 s5, s5, 0x70000\n" \
M_"a[48:51], a[224:227], a[220:223], a[48:51]\n" \
"s_add_i32 s4, s4, 0x70000\n" \
"s_add_i32 s5, s15, s5\n" \
M_"a[64:67], a[164:167], a[140:143], a[64:67]\n" \
"s_and_b32 s4, s4, 0x70000\n" \
"s_or_b32 s5, s5, 0x2800\n" \
M_"a[64:67], a[228:231], a[204:207], a[64:67]\n" \
"v_writelane_b32 v11, s5, 54\n" \
"s_add_i32 s15, s15, s4\n" \
M_"a[80:83], a[172:175], a[140:143], a[80:83]\n" \
"s_or_b32 s4, s15, 0x3000\n" \
"v_writelane_b32 v11, s4, 55\n" \
M_"a[80:83], a[236:239], a[204:207], a[80:83]\n" \
"s_movk_i32 s11, 0x4000\n" \
"s_lshl_b32 s4, s2, 2\n" \
M_"a[68:71], a[140:143], a[168:171], a[68:71]\n" \
"s_and_b32 s4, s4, 28\n" \
"s_bfe_u32 s5, s2, 0x20003\n" \
"s_waitcnt vmcnt(11) lgkmcnt(0)\n" \
M_"a[68:71], a[204:207], a[232:235], a[68:71]\n" \
"s_barrier\n" \
"v_xor_b32_e32 v28, 0x20000, v28\n" \
"v_xor_b32_e32 v29, 0x20000, v29\n" \
M_"v[116:119], a[176:179], a[144:147], v[116:119]\n" \
"s_add_u32 m0, s40, 0x8000\n" \
"s_add_u32 s85, s39, 0xe000\n" \
M_"v[116:119], a[240:243], a[208:211], v[116:119]\n" \
B_"v20, s[80:83], s85 offen sc1 lds\n" \
M_"v[120:123], a[180:183], a[144:147], v[120:123]\n" \
X_"a[160:161], v32 offset:0\n" \
"s_or_b32 s4, s4, s5\n" \
M_"v[120:123], a[244:247], a[208:211], v[120:123]\n" \
X_"a[162:163], v32 offset:512\n" \
"s_lshr_b32 s5, s2, 5\n" \
M_"v[124:127], a[184:187], a[144:147], v[124:127]\n" \
"s_add_u32 m0, s40, 0x9000\n" \
"s_add_u32 s85, s39, 0xf000\n" \
M_"v[124:127], a[248:251], a[208:211], v[124:127]\n" \
B_"v20, s[80:83], s85 offen sc1 lds\n" \
M_"v[128:131], a[188:191], a[144:147], v[128:131]\n" \
X_"a[224:225], v32 offset:4096\n" \
"s_lshr_b32 s19, s3, 1\n" \
M_"v[128:131], a[252:255], a[208:211], v[128:131]\n" \
R_"a[144:147], v28 offset:8192\n" \
"s_lshl_b32 s18, s5, 1\n" \
M_"v[144:147], a[188:191], a[148:151], v[144:147]\n" \
R_"a[208:211], v29 offset:8192\n" \
"s_lshl_b32 s6, s4, 20\n" \
M_"v[144:147], a[252:255], a[212:215], v[144:147]\n" \
"s_add_u32 m0, s40, 0xb000\n" \
"s_movk_i32 s84, 0x380\n" \
M_"v[140:143], a[184:187], a[148:151], v[140:143]\n" \
B_"v21, s[76:79], s84 offen offset:0 lds\n" \
M_"v[140:143], a[248:251], a[212:215], v[140:143]\n" \
X_"a[226:227], v32 offset:4608\n" \
"s_lshl_b32 s7, s4, 8\n" \
M_"v[136:139], a[180:183], a[148:151], v[136:139]\n" \
"s_and_b32 s13, s3, 1\n" \
"s_add_i32 s14, s18, s19\n" \
M_"v[136:139], a[244:247], a[212:215], v[136:139]\n" \
X_"a[164:165], v33 offset:0\n" \
M_"v[132:135], a[176:179], a[148:151], v[132:135]\n" \
"s_add_u32 s6, s26, s6\n" \
"s_addc_u32 s9, s27, 0\n" \
M_"v[132:135], a[240:243], a[212:215], v[132:135]\n" \
R_"a[148:151], v28 offset:10240\n" \
M_"v[148:151], a[176:179], a[152:155], v[148:151]\n" \
R_"a[212:215], v29 offset:10240\n" \
M_"v[148:151], a[240:243], a[216:219], v[148:151]\n" \
"s_add_u32 m0, s31, 0x8000\n" \
"s_mov_b32 s84, 0x380000\n" \
M_"v[152:155], a[180:183], a[152:155], v[152:155]\n" \
B_"v26, s[68:71], s84 offen offset:0 lds\n" \
M_"v[152:155], a[244:247], a[216:219], v[152:155]\n" \
"s_addk_i32 s84, 0x7c00\n" \
"s_nop 0\n" \
M_"v[156:159], a[184:187], a[152:155], v[156:159]\n" \
B_"v26, s[68:71], s84 offen offset:1024 lds\n" \
M_"v[156:159], a[248:251], a[216:219], v[156:159]\n" \
X_"a[166:167], v33 offset:512\n" \
M_"v[160:163], a[188:191], a[152:155], v[160:163]\n" \
X_"a[228:229], v33 offset:4096\n" \
M_"v[160:163], a[252:255], a[216:219], v[160:163]\n" \
R_"a[152:155], v28 offset:12288\n" \
M_"v[176:179], a[188:191], a[156:159], v[176:179]\n" \
R_"a[216:219], v29 offset:12288\n" \
M_"v[176:179], a[252:255], a[220:223], v[176:179]\n" \
"s_addk_i32 s84, 0x7c00\n" \
M_"v[172:175], a[184:187], a[156:159], v[172:175]\n" \
B_"v27, s[68:71], s84 offen offset:2048 lds\n" \
M_"v[172:175], a[248:251], a[220:223], v[172:175]\n" \
X_"a[230:231], v33 offset:4608\n" \
M_"v[168:171], a[180:183], a[156:159], v[168:171]\n" \
"s_addk_i32 s84, 0x7c00\n" \
M_"v[168:171], a[244:247], a[220:223], v[168:171]\n" \
B_"v27, s[68:71], s84 offen offset:3072 lds\n" \
M_"v[164:167], a[176:179], a[156:159], v[164:167]\n" \
X_"a[172:173], v35 offset:0\n" \
M_"v[164:167], a[240:243], a[220:223], v[164:167]\n" \
R_"a[156:159], v28 offset:14336\n" \
M_"v[84:87], a[176:179], a[136:139], v[84:87]\n" \
R_"a[220:223], v29 offset:14336\n" \
M_"v[84:87], a[240:243], a[200:203], v[84:87]\n" \
"s_add_u32 s89, s42, 0x0\n" \
"buffer_store_dwordx4 v[192:195], v20, s[80:83], s89 offen offset:2048 sc1\n" \
M_"v[88:91], a[180:183], a[136:139], v[88:91]\n" \
X_"a[174:175], v35 offset:512\n" \
M_"v[88:91], a[244:247], a[200:203], v[88:91]\n" \
X_"a[236:237], v35 offset:4096\n" \
M_"v[92:95], a[184:187], a[136:139], v[92:95]\n" \
X_"a[238:239], v35 offset:4608\n" \
M_"v[92:95], a[248:251], a[200:203], v[92:95]\n" \
"s_add_u32 m0, s30, 0x20000\n" \
"s_movk_i32 s84, 0x400\n" \
M_"v[96:99], a[188:191], a[136:139], v[96:99]\n" \
B_"v24, s[76:79], s84 offen offset:0 lds\n" \
M_"v[96:99], a[252:255], a[200:203], v[96:99]\n" \
R_"a[136:139], v28 offset:4096\n" \
M_"v[112:115], a[188:191], a[140:143], v[112:115]\n" \
R_"a[200:203], v29 offset:4096\n" \
M_"v[112:115], a[252:255], a[204:207], v[112:115]\n" \
X_"a[168:169], v34 offset:0\n" \
M_"v[108:111], a[184:187], a[140:143], v[108:111]\n" \
"s_movk_i32 s84, 0x2400\n" \
M_"v[108:111], a[248:251], a[204:207], v[108:111]\n" \
B_"v25, s[76:79], s84 offen offset:1024 lds\n" \
M_"v[104:107], a[180:183], a[140:143], v[104:107]\n" \
X_"a[170:171], v34 offset:512\n" \
M_"v[104:107], a[244:247], a[204:207], v[104:107]\n" \
X_"a[232:233], v34 offset:4096\n" \
M_"v[100:103], a[176:179], a[140:143], v[100:103]\n" \
"s_add_u32 s8, s6, 0x1000000\n" \
"s_addc_u32 s6, s9, 0\n" \
M_"v[100:103], a[240:243], a[204:207], v[100:103]\n" \
R_"a[140:143], v28 offset:6144\n" \
"s_and_b32 s9, s6, 0xffff\n" \
M_"v[52:55], a[176:179], a[128:131], v[52:55]\n" \
R_"a[204:207], v29 offset:6144\n" \
"s_lshl_b32 s6, s5, 16\n" \
M_"v[52:55], a[240:243], a[192:195], v[52:55]\n" \
"s_movk_i32 s84, 0x4400\n" \
M_"v[56:59], a[180:183], a[128:131], v[56:59]\n" \
B_"v24, s[76:79], s84 offen offset:2048 lds\n" \
M_"v[56:59], a[244:247], a[192:195], v[56:59]\n" \
"s_movk_i32 s84, 0x6400\n" \
"s_nop 0\n" \
M_"v[60:63], a[184:187], a[128:131], v[60:63]\n" \
B_"v25, s[76:79], s84 offen offset:3072 lds\n" \
M_"v[60:63], a[248:251], a[192:195], v[60:63]\n" \
X_"a[234:235], v34 offset:4608\n" \
"s_sub_i32 s10, 0x100000, s6\n" \
M_"v[64:67], a[188:191], a[128:131], v[64:67]\n" \
"s_mov_b32 s6, 0x8000\n" \
M_"v[64:67], a[252:255], a[192:195], v[64:67]\n" \
R_"a[128:131], v28 offset:0\n" \
M_"v[80:83], a[188:191], a[132:135], v[80:83]\n" \
R_"a[192:195], v29 offset:0\n" \
M_"v[80:83], a[252:255], a[196:199], v[80:83]\n" \
"s_cmp_lg_u32 s5, 0\n" \
"s_cselect_b32 s17, s11, s6\n" \
M_"v[76:79], a[184:187], a[132:135], v[76:79]\n" \
"s_lshl_b32 s6, s3, 1\n" \
"s_lshl_b32 s5, s5, 3\n" \
M_"v[76:79], a[248:251], a[196:199], v[76:79]\n" \
"s_and_b32 s6, s6, 4\n" \
"s_or_b32 s12, s5, s6\n" \
M_"v[72:75], a[180:183], a[132:135], v[72:75]\n" \
"s_sub_i32 s5, s13, s5\n" \
"s_or_b32 s5, s5, s6\n" \
M_"v[72:75], a[244:247], a[196:199], v[72:75]\n" \
"s_or_b32 s6, s12, s13\n" \
"s_lshl_b32 s4, s4, 2\n" \
M_"v[68:71], a[176:179], a[132:135], v[68:71]\n" \
"s_mulk_i32 s6, 0x92\n" \
"s_add_i32 s4, s4, 18\n" \
M_"v[68:71], a[240:243], a[196:199], v[68:71]\n" \
R_"a[132:135], v28 offset:2048\n" \
R_"a[196:199], v29 offset:2048\n" \
"s_waitcnt lgkmcnt(15)\n" \
M_"a[0:3], a[160:163], a[144:147], a[0:3]\n" \
"s_add_u32 m0, s30, 0x21000\n" \
"s_addk_i32 s84, 0x3000\n" \
M_"a[0:3], a[224:227], a[208:211], a[0:3]\n" \
B_"v24, s[76:79], s84 offen offset:0 lds\n" \
M_"a[4:7], a[164:167], a[144:147], a[4:7]\n" \
X_"a[176:177], v36 offset:0\n" \
"s_mulk_i32 s5, 0x92\n" \
M_"a[4:7], a[228:231], a[208:211], a[4:7]\n" \
X_"a[178:179], v36 offset:1024\n" \
"s_add_i32 s6, s4, s6\n" \
"s_waitcnt lgkmcnt(15)\n" \
M_"a[12:15], a[172:175], a[144:147], a[12:15]\n" \
X_"a[240:241], v36 offset:8192\n" \
"s_add_i32 s5, s5, s4\n" \
"s_waitcnt lgkmcnt(15)\n" \
M_"a[12:15], a[236:239], a[208:211], a[12:15]\n" \
"s_add_u32 s88, s42, 0x0\n" \
"buffer_store_dwordx4 v[196:199], v20, s[80:83], s88 offen offset:3072 sc1\n" \
"s_waitcnt lgkmcnt(11)\n" \
M_"a[8:11], a[144:147], a[168:171], a[8:11]\n" \
X_"a[242:243], v36 offset:9216\n" \
"s_lshl_b32 s4, s3, 11\n" \
"s_waitcnt lgkmcnt(8)\n" \
M_"a[8:11], a[208:211], a[232:235], a[8:11]\n" \
X_"a[180:181], v37 offset:0\n" \
"s_lshl_b32 s15, s13, 12\n" \
M_"a[24:27], a[148:151], a[168:171], a[24:27]\n" \
X_"a[182:183], v37 offset:1024\n" \
"s_lshl_b32 s16, s19, 14\n" \
M_"a[24:27], a[212:215], a[232:235], a[24:27]\n" \
X_"a[244:245], v37 offset:8192\n" \
"s_mul_i32 s19, s19, 0xe000\n" \
M_"a[28:31], a[172:175], a[148:151], a[28:31]\n" \
"s_mov_b32 s11, 0x20000\n" \
"s_or_b32 s12, s4, 0xc000\n" \
M_"a[28:31], a[236:239], a[212:215], a[28:31]\n" \
"s_addk_i32 s84, 0x2000\n" \
M_"a[20:23], a[164:167], a[148:151], a[20:23]\n" \
B_"v25, s[76:79], s84 offen offset:1024 lds\n" \
M_"a[20:23], a[228:231], a[212:215], a[20:23]\n" \
".fill (L_pb + 73440 - .) / 4, 4, 0xBF800000\n" \
"L_pp18:\n" \
"s_cbranch_execz L_pret\n" \
X_"a[246:247], v37 offset:9216\n" \
"s_lshl_b32 s4, s3, 8\n" \
M_"a[16:19], a[160:163], a[148:151], a[16:19]\n" \
X_"a[184:185], v38 offset:0\n" \
"s_or_b32 s22, s15, s16\n" \
M_"a[16:19], a[224:227], a[212:215], a[16:19]\n" \
X_"a[186:187], v38 offset:1024\n" \
"s_or_b32 s23, s19, s15\n" \
M_"a[32:35], a[160:163], a[152:155], a[32:35]\n" \
"s_addk_i32 s84, 0x2000\n" \
M_"a[32:35], a[224:227], a[216:219], a[32:35]\n" \
B_"v24, s[76:79], s84 offen offset:2048 lds\n" \
M_"a[36:39], a[164:167], a[152:155], a[36:39]\n" \
X_"a[248:249], v38 offset:8192\n" \
"s_lshl_b32 s60, s6, 10\n" \
M_"a[36:39], a[228:231], a[216:219], a[36:39]\n" \
X_"a[250:251], v38 offset:9216\n" \
"s_lshl_b32 s61, s5, 10\n" \
M_"a[44:47], a[172:175], a[152:155], a[44:47]\n" \
X_"a[188:189], v39 offset:0\n" \
"s_lshl_b32 s14, s14, 15\n" \
M_"a[44:47], a[236:239], a[216:219], a[44:47]\n" \
X_"a[190:191], v39 offset:1024\n" \
"s_or_b32 s20, s4, 0x26c00\n" \
M_"a[40:43], a[152:155], a[168:171], a[40:43]\n" \
"s_add_u32 s89, s42, 0x2000\n" \
"buffer_store_dwordx4 v[200:203], v20, s[80:83], s89 offen offset:0 sc1\n" \
M_"a[40:43], a[216:219], a[232:235], a[40:43]\n" \
X_"a[252:253], v39 offset:8192\n" \
"s_or_b32 s13, s22, 0x8000\n" \
M_"a[56:59], a[156:159], a[168:171], a[56:59]\n" \
X_"a[254:255], v39 offset:9216\n" \
"s_add_i32 s22, s22, 0x14000\n" \
M_"a[56:59], a[220:223], a[232:235], a[56:59]\n" \
"s_bitset1_b32 s23, 16\n" \
"s_or_b32 s24, s7, 0x5be8020\n" \
M_"a[60:63], a[172:175], a[156:159], a[60:63]\n" \
"s_and_b32 s57, s27, 0xffff\n" \
"s_mov_b32 s58, 0x6000000\n" \
M_"a[60:63], a[236:239], a[220:223], a[60:63]\n" \
"s_addk_i32 s84, 0x2000\n" \
M_"a[52:55], a[164:167], a[156:159], a[52:55]\n" \
B_"v25, s[76:79], s84 offen offset:3072 lds\n" \
M_"a[52:55], a[228:231], a[220:223], a[52:55]\n" \
"s_mov_b32 s56, s26\n" \
"s_mov_b32 s59, s11\n" \
M_"a[48:51], a[160:163], a[156:159], a[48:51]\n" \
"s_add_i32 s60, s60, 0x3000000\n" \
"s_add_i32 s61, s61, 0x411c000\n" \
M_"a[48:51], a[224:227], a[220:223], a[48:51]\n" \
"s_sub_i32 s19, 14, s18\n" \
"s_mov_b32 s21, 0x71800000\n" \
M_"a[64:67], a[164:167], a[140:143], a[64:67]\n" \
"s_mov_b32 s4, L_gsw-L_gtr\n" \
M_"a[64:67], a[228:231], a[204:207], a[64:67]\n" \
"s_cmp_eq_u32 s19, 0\n" \
"s_cselect_b32 s4, L_gs7-L_gtr, s4\n" \
M_"a[80:83], a[172:175], a[140:143], a[80:83]\n" \
"s_cmp_eq_u32 s16, 0\n" \
"s_cselect_b32 s4, 0, s4\n" \
M_"a[80:83], a[236:239], a[204:207], a[80:83]\n" \
"s_getpc_b64 s[28:29]\n" \
"L_gp1_%=:\n" \
"s_add_u32 s28, s28, L_gtr-L_gp1_%=\n" \
"s_addc_u32 s29, s29, 0\n" \
M_"a[68:71], a[140:143], a[168:171], a[68:71]\n" \
"s_add_u32 s28, s28, s4\n" \
"s_addc_u32 s29, s29, 0\n" \
"s_waitcnt vmcnt(11) lgkmcnt(0)\n" \
M_"a[68:71], a[204:207], a[232:235], a[68:71]\n" \
"s_barrier\n" \
M_"v[116:119], a[176:179], a[144:147], v[116:119]\n" \
"s_add_u32 m0, s40, 0x0\n" \
"s_nop 0\n" \
"s_add_u32 s85, s39, 0x10000\n" \
M_"v[116:119], a[240:243], a[208:211], v[116:119]\n" \
B_"v20, s[80:83], s85 offen sc1 lds\n" \
M_"v[120:123], a[180:183], a[144:147], v[120:123]\n" \
X_"a[160:161], v32 offset:32768\n" \
"v_accvgpr_write_b32 a96, 0\n" \
M_"v[120:123], a[244:247], a[208:211], v[120:123]\n" \
X_"a[162:163], v32 offset:33280\n" \
"v_accvgpr_write_b32 a97, 0\n" \
M_"v[124:127], a[184:187], a[144:147], v[124:127]\n" \
"v_accvgpr_write_b32 a98, 0\n" \
"v_accvgpr_write_b32 a99, 0\n" \
M_"v[124:127], a[248:251], a[208:211], v[124:127]\n" \
"v_accvgpr_write_b32 a100, 0\n" \
"v_accvgpr_write_b32 a101, 0\n" \
M_"v[128:131], a[188:191], a[144:147], v[128:131]\n" \
X_"a[224:225], v32 offset:36864\n" \
"v_accvgpr_write_b32 a102, 0\n" \
M_"v[128:131], a[252:255], a[208:211], v[128:131]\n" \
R_"a[144:147], v28 offset:40960\n" \
"v_accvgpr_write_b32 a103, 0\n" \
M_"v[144:147], a[188:191], a[148:151], v[144:147]\n" \
R_"a[208:211], v29 offset:40960\n" \
"v_accvgpr_write_b32 a112, 0\n" \
M_"v[144:147], a[252:255], a[212:215], v[144:147]\n" \
"s_add_u32 m0, s40, 0x1000\n" \
"s_add_u32 s85, s39, 0x11000\n" \
M_"v[140:143], a[184:187], a[148:151], v[140:143]\n" \
B_"v20, s[80:83], s85 offen sc1 lds\n" \
M_"v[140:143], a[248:251], a[212:215], v[140:143]\n" \
X_"a[226:227], v32 offset:37376\n" \
"v_accvgpr_write_b32 a113, 0\n" \
M_"v[136:139], a[180:183], a[148:151], v[136:139]\n" \
"v_accvgpr_write_b32 a114, 0\n" \
"v_accvgpr_write_b32 a115, 0\n" \
M_"v[136:139], a[244:247], a[212:215], v[136:139]\n" \
X_"a[164:165], v33 offset:32768\n" \
"v_accvgpr_write_b32 a120, 0\n" \
M_"v[132:135], a[176:179], a[148:151], v[132:135]\n" \
"v_accvgpr_write_b32 a121, 0\n" \
"v_accvgpr_write_b32 a122, 0\n" \
M_"v[132:135], a[240:243], a[212:215], v[132:135]\n" \
R_"a[148:151], v28 offset:43008\n" \
"v_accvgpr_write_b32 a123, 0\n" \
M_"v[148:151], a[176:179], a[152:155], v[148:151]\n" \
R_"a[212:215], v29 offset:43008\n" \
"v_mov_b32_e32 v180, 0x3f803f80\n" \
M_"v[148:151], a[240:243], a[216:219], v[148:151]\n" \
"s_add_u32 m0, s40, 0x3000\n" \
"s_movk_i32 s84, 0x400\n" \
M_"v[152:155], a[180:183], a[152:155], v[152:155]\n" \
B_"v21, s[76:79], s84 offen offset:0 lds\n" \
M_"v[152:155], a[244:247], a[216:219], v[152:155]\n" \
X_"a[166:167], v33 offset:33280\n" \
"s_mov_b32 s6, 0x33333333\n" \
M_"v[156:159], a[184:187], a[152:155], v[156:159]\n" \
X_"a[228:229], v33 offset:36864\n" \
"s_mov_b32 s7, 0xcccccccc\n" \
M_"v[156:159], a[248:251], a[216:219], v[156:159]\n" \
"s_add_u32 m0, s31, 0x0\n" \
"s_mov_b32 s84, 0x400000\n" \
M_"v[160:163], a[188:191], a[152:155], v[160:163]\n" \
B_"v26, s[68:71], s84 offen offset:0 lds\n" \
M_"v[160:163], a[252:255], a[216:219], v[160:163]\n" \
R_"a[152:155], v28 offset:45056\n" \
"v_cndmask_b32_e64 v181, 0, v180, s[6:7]\n" \
M_"v[176:179], a[188:191], a[156:159], v[176:179]\n" \
R_"a[216:219], v29 offset:45056\n" \
"v_accvgpr_write_b32 a104, v181\n" \
M_"v[176:179], a[252:255], a[220:223], v[176:179]\n" \
X_"a[230:231], v33 offset:37376\n" \
"v_accvgpr_write_b32 a105, v181\n" \
M_"v[172:175], a[184:187], a[156:159], v[172:175]\n" \
X_"a[172:173], v35 offset:32768\n" \
"v_accvgpr_write_b32 a106, v181\n" \
M_"v[172:175], a[248:251], a[220:223], v[172:175]\n" \
"v_accvgpr_write_b32 a107, v181\n" \
"s_mov_b32 s6, 0x11111111\n" \
M_"v[168:171], a[180:183], a[156:159], v[168:171]\n" \
"s_add_u32 s88, s42, 0x2000\n" \
"buffer_store_dwordx4 v[204:207], v20, s[80:83], s88 offen offset:1024 sc1\n" \
M_"v[168:171], a[244:247], a[220:223], v[168:171]\n" \
X_"a[174:175], v35 offset:33280\n" \
"s_mov_b32 s7, 0x11111111\n" \
M_"v[164:167], a[176:179], a[156:159], v[164:167]\n" \
"v_cndmask_b32_e64 v181, 0, v180, s[6:7]\n" \
"v_accvgpr_write_b32 a108, v181\n" \
M_"v[164:167], a[240:243], a[220:223], v[164:167]\n" \
R_"a[156:159], v28 offset:47104\n" \
"v_accvgpr_write_b32 a109, v181\n" \
M_"v[84:87], a[176:179], a[136:139], v[84:87]\n" \
R_"a[220:223], v29 offset:47104\n" \
"v_accvgpr_write_b32 a110, v181\n" \
M_"v[84:87], a[240:243], a[200:203], v[84:87]\n" \
X_"a[236:237], v35 offset:36864\n" \
"v_accvgpr_write_b32 a111, v181\n" \
M_"v[88:91], a[180:183], a[136:139], v[88:91]\n" \
"s_mov_b32 s6, 0x44444444\n" \
"s_mov_b32 s7, 0x44444444\n" \
M_"v[88:91], a[244:247], a[200:203], v[88:91]\n" \
"s_addk_i32 s84, 0x7c00\n" \
"s_nop 0\n" \
M_"v[92:95], a[184:187], a[136:139], v[92:95]\n" \
B_"v26, s[68:71], s84 offen offset:1024 lds\n" \
M_"v[92:95], a[248:251], a[200:203], v[92:95]\n" \
X_"a[238:239], v35 offset:37376\n" \
"v_cndmask_b32_e64 v181, 0, v180, s[6:7]\n" \
M_"v[96:99], a[188:191], a[136:139], v[96:99]\n" \
"v_accvgpr_write_b32 a116, v181\n" \
"v_accvgpr_write_b32 a117, v181\n" \
M_"v[96:99], a[252:255], a[200:203], v[96:99]\n" \
R_"a[136:139], v28 offset:36864\n" \
"v_accvgpr_write_b32 a118, v181\n" \
M_"v[112:115], a[188:191], a[140:143], v[112:115]\n" \
R_"a[200:203], v29 offset:36864\n" \
"v_accvgpr_write_b32 a119, v181\n" \
M_"v[112:115], a[252:255], a[204:207], v[112:115]\n" \
X_"a[168:169], v34 offset:32768\n" \
"v_bfe_i32 v181, v20, 4, 1\n" \
M_"v[108:111], a[184:187], a[140:143], v[108:111]\n" \
"s_addk_i32 s84, 0x7c00\n" \
"s_nop 0\n" \
M_"v[108:111], a[248:251], a[204:207], v[108:111]\n" \
B_"v27, s[68:71], s84 offen offset:2048 lds\n" \
M_"v[104:107], a[180:183], a[140:143], v[104:107]\n" \
X_"a[170:171], v34 offset:33280\n" \
"v_and_b32_e32 v181, 0xaaaaaaaa, v181\n" \
M_"v[104:107], a[244:247], a[204:207], v[104:107]\n" \
X_"a[232:233], v34 offset:36864\n" \
"v_or_b32_e32 v181, 0x44444444, v181\n" \
M_"v[100:103], a[176:179], a[140:143], v[100:103]\n" \
"v_accvgpr_write_b32 a126, v181\n" \
"v_bfe_u32 v180, v20, 5, 3\n" \
M_"v[100:103], a[240:243], a[204:207], v[100:103]\n" \
R_"a[140:143], v28 offset:38912\n" \
"v_bfe_u32 v181, v20, 8, 2\n" \
M_"v[52:55], a[176:179], a[128:131], v[52:55]\n" \
R_"a[204:207], v29 offset:38912\n" \
"v_lshlrev_b32_e64 v181, 1, v181\n" \
M_"v[52:55], a[240:243], a[192:195], v[52:55]\n" \
"s_addk_i32 s84, 0x7c00\n" \
M_"v[56:59], a[180:183], a[128:131], v[56:59]\n" \
B_"v27, s[68:71], s84 offen offset:3072 lds\n" \
M_"v[56:59], a[244:247], a[192:195], v[56:59]\n" \
X_"a[234:235], v34 offset:37376\n" \
"v_sub_u32_e32 v180, v180, v181\n" \
M_"v[60:63], a[184:187], a[128:131], v[60:63]\n" \
"v_and_b32_e64 v181, 16, v20\n" \
"v_mov_b32_e32 v182, 0x3f80\n" \
M_"v[60:63], a[248:251], a[192:195], v[60:63]\n" \
"v_lshlrev_b32_e32 v181, v181, v182\n" \
"v_cmp_eq_u32_e32 vcc, 0, v180\n" \
M_"v[64:67], a[188:191], a[128:131], v[64:67]\n" \
"v_cmp_eq_u32_e64 s[6:7], 1, v180\n" \
"v_cndmask_b32_e64 v182, 0, v181, vcc\n" \
M_"v[64:67], a[252:255], a[192:195], v[64:67]\n" \
R_"a[128:131], v28 offset:32768\n" \
"v_cndmask_b32_e64 v183, 0, v181, s[6:7]\n" \
M_"v[80:83], a[188:191], a[132:135], v[80:83]\n" \
R_"a[192:195], v29 offset:32768\n" \
"v_accvgpr_write_b32 a124, v182\n" \
M_"v[80:83], a[252:255], a[196:199], v[80:83]\n" \
"v_accvgpr_write_b32 a125, v183\n" \
M_"v[76:79], a[184:187], a[132:135], v[76:79]\n" \
M_"v[76:79], a[248:251], a[196:199], v[76:79]\n" \
M_"v[72:75], a[180:183], a[132:135], v[72:75]\n" \
M_"v[72:75], a[244:247], a[196:199], v[72:75]\n" \
M_"v[68:71], a[176:179], a[132:135], v[68:71]\n" \
M_"v[68:71], a[240:243], a[196:199], v[68:71]\n" \
R_"a[132:135], v28 offset:34816\n" \
R_"a[196:199], v29 offset:34816\n" \
"s_waitcnt lgkmcnt(15)\n" \
"s_nop 0\n" \
M_"a[0:3], a[160:163], a[144:147], a[0:3]\n" \
"s_add_u32 s89, s42, 0x2000\n" \
"buffer_store_dwordx4 v[208:211], v20, s[80:83], s89 offen offset:2048 sc1\n" \
M_"a[0:3], a[224:227], a[208:211], a[0:3]\n" \
X_"a[176:177], v36 offset:32768\n" \
M_"a[4:7], a[164:167], a[144:147], a[4:7]\n" \
X_"a[178:179], v36 offset:33792\n" \
M_"a[4:7], a[228:231], a[208:211], a[4:7]\n" \
X_"a[240:241], v36 offset:40960\n" \
M_"a[12:15], a[172:175], a[144:147], a[12:15]\n" \
X_"a[242:243], v36 offset:41984\n" \
"s_waitcnt lgkmcnt(15)\n" \
M_"a[12:15], a[236:239], a[208:211], a[12:15]\n" \
"s_waitcnt lgkmcnt(12)\n" \
M_"a[8:11], a[144:147], a[168:171], a[8:11]\n" \
X_"a[180:181], v37 offset:32768\n" \
"s_waitcnt lgkmcnt(9)\n" \
"s_nop 0\n" \
M_"a[8:11], a[208:211], a[232:235], a[8:11]\n" \
M_"a[24:27], a[148:151], a[168:171], a[24:27]\n" \
X_"a[182:183], v37 offset:33792\n" \
M_"a[24:27], a[212:215], a[232:235], a[24:27]\n" \
X_"a[244:245], v37 offset:40960\n" \
M_"a[28:31], a[172:175], a[148:151], a[28:31]\n" \
M_"a[28:31], a[236:239], a[212:215], a[28:31]\n" \
X_"a[246:247], v37 offset:41984\n" \
M_"a[20:23], a[164:167], a[148:151], a[20:23]\n" \
M_"a[20:23], a[228:231], a[212:215], a[20:23]\n" \
X_"a[184:185], v38 offset:32768\n" \
M_"a[16:19], a[160:163], a[148:151], a[16:19]\n" \
X_"a[186:187], v38 offset:33792\n" \
M_"a[16:19], a[224:227], a[212:215], a[16:19]\n" \
M_"a[32:35], a[160:163], a[152:155], a[32:35]\n" \
"s_add_u32 s88, s42, 0x2000\n" \
"buffer_store_dwordx4 v[212:215], v20, s[80:83], s88 offen offset:3072 sc1\n" \
M_"a[32:35], a[224:227], a[216:219], a[32:35]\n" \
X_"a[248:249], v38 offset:40960\n" \
M_"a[36:39], a[164:167], a[152:155], a[36:39]\n" \
"v_lshlrev_b32_e32 v45, 3, v20\n" \
"v_add_u32_e32 v45, s30, v45\n" \
M_"a[36:39], a[228:231], a[216:219], a[36:39]\n" \
"s_getpc_b64 s[86:87]\n" \
"L_ct1_%=:\n" \
"s_add_u32 s86, s86, L_ap0-L_ct1_%=\n" \
"s_addc_u32 s87, s87, 0\n" \
"s_add_u32 m0, s40, 0x2000\n" \
M_"a[44:47], a[172:175], a[152:155], a[44:47]\n" \
"global_load_lds_dword v45, s[86:87]\n" \
M_"a[44:47], a[236:239], a[216:219], a[44:47]\n" \
X_"a[250:251], v38 offset:41984\n" \
M_"a[40:43], a[152:155], a[168:171], a[40:43]\n" \
X_"a[188:189], v39 offset:32768\n" \
M_"a[40:43], a[216:219], a[232:235], a[40:43]\n" \
X_"a[190:191], v39 offset:33792\n" \
M_"a[56:59], a[156:159], a[168:171], a[56:59]\n" \
X_"a[252:253], v39 offset:40960\n" \
M_"a[56:59], a[220:223], a[232:235], a[56:59]\n" \
X_"a[254:255], v39 offset:41984\n" \
M_"a[60:63], a[172:175], a[156:159], a[60:63]\n" \
M_"a[60:63], a[236:239], a[220:223], a[60:63]\n" \
M_"a[52:55], a[164:167], a[156:159], a[52:55]\n" \
M_"a[52:55], a[228:231], a[220:223], a[52:55]\n" \
M_"a[48:51], a[160:163], a[156:159], a[48:51]\n" \
M_"a[48:51], a[224:227], a[220:223], a[48:51]\n" \
M_"a[64:67], a[164:167], a[140:143], a[64:67]\n" \
M_"a[64:67], a[228:231], a[204:207], a[64:67]\n" \
M_"a[80:83], a[172:175], a[140:143], a[80:83]\n" \
M_"a[80:83], a[236:239], a[204:207], a[80:83]\n" \
M_"a[68:71], a[140:143], a[168:171], a[68:71]\n" \
"s_waitcnt vmcnt(3) lgkmcnt(0)\n" \
M_"a[68:71], a[204:207], a[232:235], a[68:71]\n" \
"s_barrier\n" \
"v_xor_b32_e32 v28, 0x20000, v28\n" \
"v_xor_b32_e32 v29, 0x20000, v29\n" \
M_"v[116:119], a[176:179], a[144:147], v[116:119]\n" \
X_"a[160:161], v32 offset:0\n" \
"v_accvgpr_read_b32 v184, a0\n" \
M_"v[116:119], a[240:243], a[208:211], v[116:119]\n" \
"v_accvgpr_read_b32 v185, a1\n" \
"v_accvgpr_read_b32 v186, a2\n" \
M_"v[120:123], a[180:183], a[144:147], v[120:123]\n" \
"v_accvgpr_read_b32 v187, a3\n" \
"v_accvgpr_read_b32 v188, a4\n" \
M_"v[120:123], a[244:247], a[208:211], v[120:123]\n" \
X_"a[162:163], v32 offset:512\n" \
"v_accvgpr_read_b32 v189, a5\n" \
M_"v[124:127], a[184:187], a[144:147], v[124:127]\n" \
"v_accvgpr_read_b32 v190, a6\n" \
"v_accvgpr_read_b32 v191, a7\n" \
M_"v[124:127], a[248:251], a[208:211], v[124:127]\n" \
"v_accvgpr_read_b32 v196, a12\n" \
"v_accvgpr_read_b32 v197, a13\n" \
M_"v[128:131], a[188:191], a[144:147], v[128:131]\n" \
X_"a[224:225], v32 offset:4096\n" \
"v_accvgpr_read_b32 v198, a14\n" \
M_"v[128:131], a[252:255], a[208:211], v[128:131]\n" \
R_"a[144:147], v28 offset:8192\n" \
"v_accvgpr_read_b32 v199, a15\n" \
M_"v[144:147], a[188:191], a[148:151], v[144:147]\n" \
R_"a[208:211], v29 offset:8192\n" \
"v_accvgpr_read_b32 v192, a8\n" \
M_"v[144:147], a[252:255], a[212:215], v[144:147]\n" \
X_"a[226:227], v32 offset:4608\n" \
"v_accvgpr_read_b32 v193, a9\n" \
M_"v[140:143], a[184:187], a[148:151], v[140:143]\n" \
"v_accvgpr_read_b32 v194, a10\n" \
"v_accvgpr_read_b32 v195, a11\n" \
M_"v[140:143], a[248:251], a[212:215], v[140:143]\n" \
"v_accvgpr_read_b32 v208, a24\n" \
"v_accvgpr_read_b32 v209, a25\n" \
M_"v[136:139], a[180:183], a[148:151], v[136:139]\n" \
"v_accvgpr_read_b32 v210, a26\n" \
"v_accvgpr_read_b32 v211, a27\n" \
M_"v[136:139], a[244:247], a[212:215], v[136:139]\n" \
X_"a[164:165], v33 offset:0\n" \
"v_accvgpr_read_b32 v212, a28\n" \
M_"v[132:135], a[176:179], a[148:151], v[132:135]\n" \
"v_accvgpr_read_b32 v213, a29\n" \
"v_accvgpr_read_b32 v214, a30\n" \
M_"v[132:135], a[240:243], a[212:215], v[132:135]\n" \
R_"a[148:151], v28 offset:10240\n" \
"v_accvgpr_read_b32 v215, a31\n" \
M_"v[148:151], a[176:179], a[152:155], v[148:151]\n" \
R_"a[212:215], v29 offset:10240\n" \
"v_accvgpr_read_b32 v204, a20\n" \
M_"v[148:151], a[240:243], a[216:219], v[148:151]\n" \
X_"a[166:167], v33 offset:512\n" \
"v_accvgpr_read_b32 v205, a21\n" \
M_"v[152:155], a[180:183], a[152:155], v[152:155]\n" \
"v_accvgpr_read_b32 v206, a22\n" \
"v_accvgpr_read_b32 v207, a23\n" \
M_"v[152:155], a[244:247], a[216:219], v[152:155]\n" \
X_"a[228:229], v33 offset:4096\n" \
"v_accvgpr_read_b32 v200, a16\n" \
M_"v[156:159], a[184:187], a[152:155], v[156:159]\n" \
"v_accvgpr_read_b32 v201, a17\n" \
"v_accvgpr_read_b32 v202, a18\n" \
M_"v[156:159], a[248:251], a[216:219], v[156:159]\n" \
"v_accvgpr_read_b32 v203, a19\n" \
"v_accvgpr_read_b32 v216, a32\n" \
M_"v[160:163], a[188:191], a[152:155], v[160:163]\n" \
X_"a[230:231], v33 offset:4608\n" \
"v_accvgpr_read_b32 v217, a33\n" \
M_"v[160:163], a[252:255], a[216:219], v[160:163]\n" \
R_"a[152:155], v28 offset:12288\n" \
"v_accvgpr_read_b32 v218, a34\n" \
M_"v[176:179], a[188:191], a[156:159], v[176:179]\n" \
R_"a[216:219], v29 offset:12288\n" \
"v_accvgpr_read_b32 v219, a35\n" \
M_"v[176:179], a[252:255], a[220:223], v[176:179]\n" \
"v_accvgpr_read_b32 v220, a36\n" \
"v_accvgpr_read_b32 v221, a37\n" \
M_"v[172:175], a[184:187], a[156:159], v[172:175]\n" \
X_"a[172:173], v35 offset:0\n" \
"v_accvgpr_read_b32 v222, a38\n" \
M_"v[172:175], a[248:251], a[220:223], v[172:175]\n" \
"v_accvgpr_read_b32 v223, a39\n" \
"v_accvgpr_read_b32 v228, a44\n" \
M_"v[168:171], a[180:183], a[156:159], v[168:171]\n" \
"v_accvgpr_read_b32 v229, a45\n" \
"v_accvgpr_read_b32 v230, a46\n" \
M_"v[168:171], a[244:247], a[220:223], v[168:171]\n" \
X_"a[174:175], v35 offset:512\n" \
"v_accvgpr_read_b32 v231, a47\n" \
M_"v[164:167], a[176:179], a[156:159], v[164:167]\n" \
"v_accvgpr_read_b32 v224, a40\n" \
"v_accvgpr_read_b32 v225, a41\n" \
M_"v[164:167], a[240:243], a[220:223], v[164:167]\n" \
R_"a[156:159], v28 offset:14336\n" \
"v_accvgpr_read_b32 v226, a42\n" \
M_"v[84:87], a[176:179], a[136:139], v[84:87]\n" \
R_"a[220:223], v29 offset:14336\n" \
"v_accvgpr_read_b32 v227, a43\n" \
M_"v[84:87], a[240:243], a[200:203], v[84:87]\n" \
X_"a[236:237], v35 offset:4096\n" \
"v_accvgpr_read_b32 v240, a56\n" \
M_"v[88:91], a[180:183], a[136:139], v[88:91]\n" \
"v_accvgpr_read_b32 v241, a57\n" \
"v_accvgpr_read_b32 v242, a58\n" \
M_"v[88:91], a[244:247], a[200:203], v[88:91]\n" \
X_"a[238:239], v35 offset:4608\n" \
"v_accvgpr_read_b32 v243, a59\n" \
M_"v[92:95], a[184:187], a[136:139], v[92:95]\n" \
"v_accvgpr_read_b32 v244, a60\n" \
"v_accvgpr_read_b32 v245, a61\n" \
M_"v[92:95], a[248:251], a[200:203], v[92:95]\n" \
"v_accvgpr_read_b32 v246, a62\n" \
"v_accvgpr_read_b32 v247, a63\n" \
M_"v[96:99], a[188:191], a[136:139], v[96:99]\n" \
"v_accvgpr_read_b32 v236, a52\n" \
"v_accvgpr_read_b32 v237, a53\n" \
M_"v[96:99], a[252:255], a[200:203], v[96:99]\n" \
R_"a[136:139], v28 offset:4096\n" \
"v_accvgpr_read_b32 v238, a54\n" \
M_"v[112:115], a[188:191], a[140:143], v[112:115]\n" \
R_"a[200:203], v29 offset:4096\n" \
"v_accvgpr_read_b32 v239, a55\n" \
M_"v[112:115], a[252:255], a[204:207], v[112:115]\n" \
X_"a[168:169], v34 offset:0\n" \
"v_accvgpr_read_b32 v232, a48\n" \
M_"v[108:111], a[184:187], a[140:143], v[108:111]\n" \
X_"a[170:171], v34 offset:512\n" \
"v_accvgpr_read_b32 v233, a49\n" \
M_"v[108:111], a[248:251], a[204:207], v[108:111]\n" \
"v_accvgpr_read_b32 v234, a50\n" \
"v_accvgpr_read_b32 v235, a51\n" \
M_"v[104:107], a[180:183], a[140:143], v[104:107]\n" \
"v_accvgpr_read_b32 v180, a64\n" \
"v_accvgpr_read_b32 v181, a65\n" \
M_"v[104:107], a[244:247], a[204:207], v[104:107]\n" \
X_"a[232:233], v34 offset:4096\n" \
"v_accvgpr_read_b32 v182, a66\n" \
M_"v[100:103], a[176:179], a[140:143], v[100:103]\n" \
"v_accvgpr_read_b32 v183, a67\n" \
"v_accvgpr_read_b32 v48, a80\n" \
M_"v[100:103], a[240:243], a[204:207], v[100:103]\n" \
R_"a[140:143], v28 offset:6144\n" \
"v_accvgpr_read_b32 v49, a81\n" \
M_"v[52:55], a[176:179], a[128:131], v[52:55]\n" \
R_"a[204:207], v29 offset:6144\n" \
"v_accvgpr_read_b32 v50, a82\n" \
M_"v[52:55], a[240:243], a[192:195], v[52:55]\n" \
X_"a[234:235], v34 offset:4608\n" \
"v_accvgpr_read_b32 v51, a83\n" \
M_"v[56:59], a[180:183], a[128:131], v[56:59]\n" \
"v_accvgpr_read_b32 v252, a68\n" \
"v_accvgpr_read_b32 v253, a69\n" \
M_"v[56:59], a[244:247], a[192:195], v[56:59]\n" \
"v_accvgpr_read_b32 v254, a70\n" \
"v_accvgpr_read_b32 v255, a71\n" \
M_"v[60:63], a[184:187], a[128:131], v[60:63]\n" \
"v_lshrrev_b32_e64 v24, 9, v20\n" \
"v_and_b32_e32 v26, 0x1ff, v20\n" \
M_"v[60:63], a[248:251], a[192:195], v[60:63]\n" \
"v_lshl_add_u32 v24, v24, 3, v26\n" \
"v_lshrrev_b32_e64 v25, 8, v20\n" \
M_"v[64:67], a[188:191], a[128:131], v[64:67]\n" \
"v_mul_u32_u24_e32 v25, 0x24700, v25\n" \
"v_add_u32_e64 v25, v25, v20\n" \
M_"v[64:67], a[252:255], a[192:195], v[64:67]\n" \
R_"a[128:131], v28 offset:0\n" \
"v_lshrrev_b32_e64 v26, 2, v20\n" \
M_"v[80:83], a[188:191], a[132:135], v[80:83]\n" \
R_"a[192:195], v29 offset:0\n" \
"v_add_u32_e64 v26, s45, v26\n" \
M_"v[80:83], a[252:255], a[196:199], v[80:83]\n" \
"v_add_u32_e32 v27, s46, v20\n" \
"v_add_u32_e32 v21, s43, v27\n" \
M_"v[76:79], a[184:187], a[132:135], v[76:79]\n" \
M_"v[76:79], a[248:251], a[196:199], v[76:79]\n" \
M_"v[72:75], a[180:183], a[132:135], v[72:75]\n" \
M_"v[72:75], a[244:247], a[196:199], v[72:75]\n" \
M_"v[68:71], a[176:179], a[132:135], v[68:71]\n" \
M_"v[68:71], a[240:243], a[196:199], v[68:71]\n" \
R_"a[132:135], v28 offset:2048\n" \
R_"a[196:199], v29 offset:2048\n" \
"s_waitcnt lgkmcnt(15)\n" \
"s_nop 0\n" \
M_"v[184:187], a[160:163], a[144:147], v[184:187]\n" \
"v_bfe_u32 v44, v20, 2, 5\n" \
X_"a[176:177], v36 offset:0\n" \
M_"v[184:187], a[224:227], a[208:211], v[184:187]\n" \
"buffer_load_dword v44, v44, s[80:83], s38 offen offset:0 sc1\n" \
M_"v[188:191], a[164:167], a[144:147], v[188:191]\n" \
X_"a[178:179], v36 offset:1024\n" \
M_"v[188:191], a[228:231], a[208:211], v[188:191]\n" \
X_"a[240:241], v36 offset:8192\n" \
M_"v[196:199], a[172:175], a[144:147], v[196:199]\n" \
X_"a[242:243], v36 offset:9216\n" \
"s_waitcnt lgkmcnt(15)\n" \
M_"v[196:199], a[236:239], a[208:211], v[196:199]\n" \
"s_waitcnt lgkmcnt(12)\n" \
M_"v[192:195], a[144:147], a[168:171], v[192:195]\n" \
X_"a[180:181], v37 offset:0\n" \
"s_waitcnt lgkmcnt(9)\n" \
"s_nop 0\n" \
M_"v[192:195], a[208:211], a[232:235], v[192:195]\n" \
M_"v[208:211], a[148:151], a[168:171], v[208:211]\n" \
X_"a[182:183], v37 offset:1024\n" \
M_"v[208:211], a[212:215], a[232:235], v[208:211]\n" \
X_"a[244:245], v37 offset:8192\n" \
M_"v[212:215], a[172:175], a[148:151], v[212:215]\n" \
M_"v[212:215], a[236:239], a[212:215], v[212:215]\n" \
X_"a[246:247], v37 offset:9216\n" \
C_"v188, v188, v189\n" \
M_"v[204:207], a[164:167], a[148:151], v[204:207]\n" \
C_"v189, v190, v191\n" \
C_"v190, v198, v199\n" \
M_"v[204:207], a[228:231], a[212:215], v[204:207]\n" \
X_"a[184:185], v38 offset:0\n" \
"v_mov_b32_e32 v191, 0x3f80\n" \
M_"v[200:203], a[160:163], a[148:151], v[200:203]\n" \
X_"a[186:187], v38 offset:1024\n" \
".fill (L_pb + 77536 - .) / 4, 4, 0xBF800000\n" \
"L_pp19:\n" \
"s_cbranch_execz L_pret\n" \
C_"v184, v184, v185\n" \
M_"v[200:203], a[224:227], a[212:215], v[200:203]\n" \
C_"v185, v186, v187\n" \
C_"v186, v196, v197\n" \
M_"v[216:219], a[160:163], a[152:155], v[216:219]\n" \
X_"a[248:249], v38 offset:8192\n" \
"v_mov_b32_e32 v187, 0\n" \
M_"v[216:219], a[224:227], a[216:219], v[216:219]\n" \
X_"a[250:251], v38 offset:9216\n" \
M_"v[220:223], a[164:167], a[152:155], v[220:223]\n" \
M_"v[220:223], a[228:231], a[216:219], v[220:223]\n" \
X_"a[188:189], v39 offset:0\n" \
C_"v204, v204, v205\n" \
M_"v[228:231], a[172:175], a[152:155], v[228:231]\n" \
C_"v205, v206, v207\n" \
C_"v206, v214, v215\n" \
M_"v[228:231], a[236:239], a[216:219], v[228:231]\n" \
X_"a[190:191], v39 offset:1024\n" \
"v_mov_b32_e32 v207, 0x3f80\n" \
M_"v[224:227], a[152:155], a[168:171], v[224:227]\n" \
X_"a[252:253], v39 offset:8192\n" \
C_"v200, v200, v201\n" \
M_"v[224:227], a[216:219], a[232:235], v[224:227]\n" \
C_"v201, v202, v203\n" \
C_"v202, v212, v213\n" \
M_"v[240:243], a[156:159], a[168:171], v[240:243]\n" \
X_"a[254:255], v39 offset:9216\n" \
"v_mov_b32_e64 v203, 0\n" \
M_"v[240:243], a[220:223], a[232:235], v[240:243]\n" \
M_"v[244:247], a[172:175], a[156:159], v[244:247]\n" \
M_"v[244:247], a[236:239], a[220:223], v[244:247]\n" \
C_"v220, v220, v221\n" \
C_"v221, v222, v223\n" \
M_"v[236:239], a[164:167], a[156:159], v[236:239]\n" \
C_"v222, v230, v231\n" \
"v_mov_b32_e32 v223, 0x3f80\n" \
M_"v[236:239], a[228:231], a[220:223], v[236:239]\n" \
C_"v216, v216, v217\n" \
C_"v217, v218, v219\n" \
M_"v[232:235], a[160:163], a[156:159], v[232:235]\n" \
C_"v218, v228, v229\n" \
"v_mov_b32_e64 v219, 0\n" \
M_"v[232:235], a[224:227], a[220:223], v[232:235]\n" \
M_"v[180:183], a[164:167], a[140:143], v[180:183]\n" \
M_"v[180:183], a[228:231], a[204:207], v[180:183]\n" \
M_"v[48:51], a[172:175], a[140:143], v[48:51]\n" \
M_"v[48:51], a[236:239], a[204:207], v[48:51]\n" \
C_"v236, v236, v237\n" \
C_"v237, v238, v239\n" \
M_"v[252:255], a[140:143], a[168:171], v[252:255]\n" \
C_"v238, v246, v247\n" \
"v_mov_b32_e32 v239, 0x3f80\n" \
M_"v[252:255], a[204:207], a[232:235], v[252:255]\n" \
C_"v232, v232, v233\n" \
C_"v233, v234, v235\n" \
"s_waitcnt vmcnt(0) lgkmcnt(0)\n" \
"s_barrier\n" \
"v_mov_b32_e32 v45, s62\n" \
"s_mov_b64 exec, 1\n" \
"buffer_store_dword v45, off, s[80:83], s37 offset:32 sc1\n" \
"s_mov_b64 exec, -1\n" \
"v_cmp_ne_u32_e32 vcc, s62, v44\n" \
"s_nop 4\n" \
"s_cbranch_vccnz L_poll161_%=\n" \
"L_pollr161_%=:\n" \
M_"v[116:119], a[176:179], a[144:147], v[116:119]\n" \
"s_mov_b32 s73, s50\n" \
"s_sub_u32 m0, s31, 0x10000\n" \
M_"v[116:119], a[240:243], a[208:211], v[116:119]\n" \
B_"v20, s[80:83], s73 offen offset:0 sc1 lds\n" \
M_"v[120:123], a[180:183], a[144:147], v[120:123]\n" \
"s_add_u32 s74, s50, 0x4000\n" \
"s_add_u32 s75, s50, 0x8000\n" \
M_"v[120:123], a[244:247], a[208:211], v[120:123]\n" \
"s_mov_b32 s70, 0x3e0293ee\n" \
"s_mov_b32 s76, s52\n" \
M_"v[124:127], a[184:187], a[144:147], v[124:127]\n" \
"v_mul_f32_e32 v116, s70, v116\n" \
"v_mul_f32_e32 v117, s70, v117\n" \
M_"v[124:127], a[248:251], a[208:211], v[124:127]\n" \
B_"v20, s[80:83], s73 offen offset:1024 sc1 lds\n" \
M_"v[128:131], a[188:191], a[144:147], v[128:131]\n" \
"v_mul_f32_e32 v118, s70, v118\n" \
"v_mul_f32_e32 v119, s70, v119\n" \
M_"v[128:131], a[252:255], a[208:211], v[128:131]\n" \
C_"v116, v116, v117\n" \
"v_mul_f32_e64 v120, s70, v120\n" \
M_"v[144:147], a[188:191], a[148:151], v[144:147]\n" \
"v_mul_f32_e32 v124, s70, v124\n" \
"v_mul_f32_e32 v125, s70, v125\n" \
M_"v[144:147], a[252:255], a[212:215], v[144:147]\n" \
B_"v20, s[80:83], s73 offen offset:2048 sc1 lds\n" \
M_"v[140:143], a[184:187], a[148:151], v[140:143]\n" \
"v_mul_f32_e32 v126, s70, v126\n" \
"v_mul_f32_e32 v127, s70, v127\n" \
M_"v[140:143], a[248:251], a[212:215], v[140:143]\n" \
C_"v124, v124, v125\n" \
"v_mul_f32_e64 v121, s70, v121\n" \
M_"v[136:139], a[180:183], a[148:151], v[136:139]\n" \
"v_mul_f32_e32 v122, s70, v122\n" \
"v_mul_f32_e32 v123, s70, v123\n" \
M_"v[136:139], a[244:247], a[212:215], v[136:139]\n" \
B_"v20, s[80:83], s73 offen offset:3072 sc1 lds\n" \
M_"v[132:135], a[176:179], a[148:151], v[132:135]\n" \
"s_sub_u32 m0, s31, s51\n" \
C_"v117, v118, v119\n" \
M_"v[132:135], a[240:243], a[212:215], v[132:135]\n" \
"v_mul_f32_e32 v128, s70, v128\n" \
"v_mul_f32_e64 v129, s70, v129\n" \
M_"v[148:151], a[176:179], a[152:155], v[148:151]\n" \
"v_mul_f32_e32 v130, s70, v130\n" \
"v_mul_f32_e32 v131, s70, v131\n" \
M_"v[148:151], a[240:243], a[216:219], v[148:151]\n" \
B_"v20, s[80:83], s74 offen offset:0 sc1 lds\n" \
M_"v[152:155], a[180:183], a[152:155], v[152:155]\n" \
C_"v125, v126, v127\n" \
C_"v118, v120, v121\n" \
M_"v[152:155], a[244:247], a[216:219], v[152:155]\n" \
C_"v119, v122, v123\n" \
C_"v126, v128, v129\n" \
M_"v[156:159], a[184:187], a[152:155], v[156:159]\n" \
C_"v127, v130, v131\n" \
"v_mul_f32_e64 v132, s70, v132\n" \
M_"v[156:159], a[248:251], a[216:219], v[156:159]\n" \
B_"v20, s[80:83], s74 offen offset:1024 sc1 lds\n" \
M_"v[160:163], a[188:191], a[152:155], v[160:163]\n" \
"ds_write_b128 v21, v[116:119] offset:16384\n" \
M_"v[160:163], a[252:255], a[216:219], v[160:163]\n" \
"v_mul_f32_e32 v133, s70, v133\n" \
"v_mul_f32_e32 v140, s70, v140\n" \
M_"v[176:179], a[188:191], a[156:159], v[176:179]\n" \
"v_mul_f32_e32 v141, s70, v141\n" \
"v_mul_f32_e32 v134, s70, v134\n" \
M_"v[176:179], a[252:255], a[220:223], v[176:179]\n" \
B_"v20, s[80:83], s74 offen offset:2048 sc1 lds\n" \
M_"v[172:175], a[184:187], a[156:159], v[172:175]\n" \
"ds_write_b128 v21, v[124:127] offset:17408\n" \
M_"v[172:175], a[248:251], a[220:223], v[172:175]\n" \
"v_mul_f32_e64 v135, s70, v135\n" \
C_"v132, v132, v133\n" \
M_"v[168:171], a[180:183], a[156:159], v[168:171]\n" \
"v_mul_f32_e32 v142, s70, v142\n" \
"v_mul_f32_e32 v143, s70, v143\n" \
M_"v[168:171], a[244:247], a[220:223], v[168:171]\n" \
B_"v20, s[80:83], s74 offen offset:3072 sc1 lds\n" \
M_"v[164:167], a[176:179], a[156:159], v[164:167]\n" \
"s_sub_u32 m0, s31, 0x4000\n" \
C_"v140, v140, v141\n" \
M_"v[164:167], a[240:243], a[220:223], v[164:167]\n" \
"v_mul_f32_e32 v136, s70, v136\n" \
"v_mul_f32_e32 v137, s70, v137\n" \
M_"v[84:87], a[176:179], a[136:139], v[84:87]\n" \
"v_mul_f32_e32 v138, s70, v138\n" \
"v_mul_f32_e32 v139, s70, v139\n" \
M_"v[84:87], a[240:243], a[200:203], v[84:87]\n" \
B_"v20, s[80:83], s75 offen offset:0 sc1 lds\n" \
M_"v[88:91], a[180:183], a[136:139], v[88:91]\n" \
C_"v133, v134, v135\n" \
"v_mul_f32_e64 v144, s70, v144\n" \
M_"v[88:91], a[244:247], a[200:203], v[88:91]\n" \
"v_mul_f32_e32 v145, s70, v145\n" \
"v_mul_f32_e32 v146, s70, v146\n" \
M_"v[92:95], a[184:187], a[136:139], v[92:95]\n" \
"v_mul_f32_e64 v147, s70, v147\n" \
C_"v141, v142, v143\n" \
M_"v[92:95], a[248:251], a[200:203], v[92:95]\n" \
B_"v20, s[80:83], s75 offen offset:1024 sc1 lds\n" \
M_"v[96:99], a[188:191], a[136:139], v[96:99]\n" \
C_"v134, v136, v137\n" \
C_"v135, v138, v139\n" \
M_"v[96:99], a[252:255], a[200:203], v[96:99]\n" \
C_"v142, v144, v145\n" \
C_"v143, v146, v147\n" \
M_"v[112:115], a[188:191], a[140:143], v[112:115]\n" \
"ds_write_b128 v21, v[132:135] offset:20480\n" \
M_"v[112:115], a[252:255], a[204:207], v[112:115]\n" \
B_"v20, s[80:83], s75 offen offset:2048 sc1 lds\n" \
M_"v[108:111], a[184:187], a[140:143], v[108:111]\n" \
C_"v180, v180, v181\n" \
C_"v181, v182, v183\n" \
M_"v[108:111], a[248:251], a[204:207], v[108:111]\n" \
"ds_write_b128 v21, v[140:143] offset:21504\n" \
M_"v[104:107], a[180:183], a[140:143], v[104:107]\n" \
C_"v234, v244, v245\n" \
"v_mov_b32_e64 v235, 0\n" \
M_"v[104:107], a[244:247], a[204:207], v[104:107]\n" \
B_"v20, s[80:83], s75 offen offset:3072 sc1 lds\n" \
M_"v[100:103], a[176:179], a[140:143], v[100:103]\n" \
"s_mov_b32 m0, s53\n" \
"s_nop 0\n" \
C_"v182, v50, v51\n" \
M_"v[100:103], a[240:243], a[204:207], v[100:103]\n" \
"v_mov_b32_e32 v183, 0x3f80\n" \
C_"v118, v192, v193\n" \
M_"v[52:55], a[176:179], a[128:131], v[52:55]\n" \
C_"v119, v194, v195\n" \
C_"v120, v208, v209\n" \
M_"v[52:55], a[240:243], a[192:195], v[52:55]\n" \
B_"v20, s[80:83], s76 offen offset:0 sc1 lds\n" \
M_"v[56:59], a[180:183], a[128:131], v[56:59]\n" \
C_"v121, v210, v211\n" \
C_"v122, v224, v225\n" \
M_"v[56:59], a[244:247], a[192:195], v[56:59]\n" \
C_"v123, v226, v227\n" \
C_"v124, v240, v241\n" \
C_"v125, v242, v243\n" \
M_"v[60:63], a[184:187], a[128:131], v[60:63]\n" \
M_"v[192:195], v[188:191], v[184:187], 0\n" \
B_"v20, s[80:83], s76 offen offset:1024 sc1 lds\n" \
M_"v[32:35], v[180:183], v[184:187], 0\n" \
M_"v[208:211], v[204:207], v[200:203], 0\n" \
M_"v[36:39], v[188:191], v[200:203], 0\n" \
M_"v[224:227], v[220:223], v[216:219], 0\n" \
M_"v[248:251], v[204:207], v[216:219], 0\n" \
M_"v[240:243], v[236:239], v[232:235], 0\n" \
M_"v[128:131], v[220:223], v[232:235], 0\n" \
"s_mov_b32 s64, 0xaaaaaaaa\n" \
"s_mov_b32 s65, 0xaaaaaaaa\n" \
M_"v[60:63], a[248:251], a[192:195], v[60:63]\n" \
"s_mov_b32 s66, 0xfff0ffff\n" \
"s_mov_b32 s67, 0xf000ff00\n" \
M_"v[64:67], a[188:191], a[128:131], v[64:67]\n" \
"s_mov_b32 s68, 0xffc0fffc\n" \
"s_mov_b32 s69, 0xc000fc00\n" \
"v_cndmask_b32_e64 v192, v192, v193, s[64:65]\n" \
"v_cndmask_b32_e64 v32, v32, v33, s[64:65]\n" \
"v_cndmask_b32_e64 v194, v194, v195, s[64:65]\n" \
"v_cndmask_b32_e64 v34, v34, v35, s[64:65]\n" \
"v_cndmask_b32_e64 v208, v208, v209, s[64:65]\n" \
"v_cndmask_b32_e64 v36, v36, v37, s[64:65]\n" \
"v_cndmask_b32_e64 v210, v210, v211, s[64:65]\n" \
"v_cndmask_b32_e64 v38, v38, v39, s[64:65]\n" \
"v_cndmask_b32_e64 v224, v224, v225, s[64:65]\n" \
"v_cndmask_b32_e64 v248, v248, v249, s[64:65]\n" \
"v_cndmask_b32_e64 v226, v226, v227, s[64:65]\n" \
"v_cndmask_b32_e64 v250, v250, v251, s[64:65]\n" \
"v_cndmask_b32_e64 v240, v240, v241, s[64:65]\n" \
"v_cndmask_b32_e64 v128, v128, v129, s[64:65]\n" \
"v_cndmask_b32_e64 v242, v242, v243, s[64:65]\n" \
"v_cndmask_b32_e64 v130, v130, v131, s[64:65]\n" \
"v_cndmask_b32_e64 v192, v32, v192, s[66:67]\n" \
"v_cndmask_b32_e64 v194, v34, v194, s[68:69]\n" \
"v_cndmask_b32_e64 v208, v36, v208, s[66:67]\n" \
"v_cndmask_b32_e64 v210, v38, v210, s[68:69]\n" \
"v_cndmask_b32_e64 v224, v248, v224, s[66:67]\n" \
"v_cndmask_b32_e64 v226, v250, v226, s[68:69]\n" \
"v_cndmask_b32_e64 v240, v128, v240, s[66:67]\n" \
"v_cndmask_b32_e64 v242, v130, v242, s[68:69]\n" \
M_"v[64:67], a[252:255], a[192:195], v[64:67]\n" \
"v_max_f32_e32 v193, v192, v194\n" \
"v_max_f32_e32 v209, v208, v210\n" \
"v_max_f32_e32 v225, v224, v226\n" \
"v_max_f32_e32 v241, v240, v242\n" \
"v_mul_f32_e32 v148, s70, v148\n" \
"v_permlane32_swap_b32_e32 v193, v225\n" \
"v_permlane32_swap_b32_e32 v209, v241\n" \
"v_mul_f32_e32 v149, s70, v149\n" \
"v_mul_f32_e32 v156, s70, v156\n" \
"v_mul_f32_e32 v157, s70, v157\n" \
"v_permlane32_swap_b32_e32 v196, v228\n" \
"v_permlane32_swap_b32_e32 v212, v244\n" \
"v_max_f32_e32 v193, v193, v225\n" \
"v_max_f32_e32 v209, v209, v241\n" \
"v_mul_f32_e32 v150, s70, v150\n" \
"v_mul_f32_e32 v151, s70, v151\n" \
C_"v148, v148, v149\n" \
"v_mul_f32_e32 v158, s70, v158\n" \
"v_mul_f32_e32 v159, s70, v159\n" \
C_"v156, v156, v157\n" \
"v_permlane16_swap_b32_e32 v228, v244\n" \
"v_permlane16_swap_b32_e32 v193, v209\n" \
M_"v[80:83], a[188:191], a[132:135], v[80:83]\n" \
"s_mov_b32 s72, 0x3eae1a93\n" \
"v_mul_f32_e32 v152, s70, v152\n" \
"v_mul_f32_e32 v153, s70, v153\n" \
"v_mul_f32_e32 v154, s70, v154\n" \
"v_mul_f32_e32 v155, s70, v155\n" \
C_"v149, v150, v151\n" \
"v_mul_f32_e32 v160, s70, v160\n" \
"v_mul_f32_e32 v161, s70, v161\n" \
"v_mul_f32_e32 v162, s70, v162\n" \
"v_mul_f32_e32 v163, s70, v163\n" \
C_"v157, v158, v159\n" \
"v_mul_f32_e32 v45, 0xbfb8aa3b, v228\n" \
"v_mfma_f32_16x16x16_bf16 v[48:51], v[182:183], v[46:47], 0\n" \
"v_mfma_f32_16x16x16_bf16 v[196:199], v[190:191], v[46:47], 0\n" \
"v_mfma_f32_16x16x16_bf16 v[212:215], v[206:207], v[46:47], 0\n" \
"v_max_f32_e64 v193, v193, v209\n" \
C_"v150, v152, v153\n" \
C_"v151, v154, v155\n" \
C_"v158, v160, v161\n" \
C_"v159, v162, v163\n" \
"v_mfma_f32_16x16x16_bf16 v[228:231], v[222:223], v[46:47], 0\n" \
"v_mul_f32_e32 v193, s72, v193\n" \
"v_mov_b32_e32 v209, v193\n" \
"ds_write_b128 v21, v[148:151] offset:24576\n" \
"v_mfma_f32_16x16x16_bf16 v[244:247], v[238:239], v[46:47], 0\n" \
"v_permlane16_swap_b32_e32 v193, v209\n" \
"s_mov_b32 s71, 0x3e0293ee\n" \
M_"v[80:83], a[252:255], a[196:199], v[80:83]\n" \
"v_mov_b32_e32 v225, v193\n" \
"v_mov_b32_e32 v241, v209\n" \
"v_mul_f32_e32 v164, s70, v164\n" \
"v_mul_f32_e64 v165, s70, v165\n" \
"ds_write_b128 v21, v[156:159] offset:25600\n" \
"v_mul_f32_e32 v172, s70, v172\n" \
"v_mul_f32_e32 v173, s70, v173\n" \
"v_pk_mul_f32 v[84:85], v[84:85], s[70:71]\n" \
"v_pk_mul_f32 v[92:93], v[92:93], s[70:71]\n" \
"v_pk_mul_f32 v[100:101], v[100:101], s[70:71]\n" \
"v_pk_mul_f32 v[108:109], v[108:109], s[70:71]\n" \
"v_pk_mul_f32 v[52:53], v[52:53], s[70:71]\n" \
"v_pk_mul_f32 v[60:61], v[60:61], s[70:71]\n" \
"v_permlane32_swap_b32_e32 v193, v225\n" \
"v_permlane32_swap_b32_e32 v209, v241\n" \
"v_mul_f32_e32 v166, s70, v166\n" \
"v_mul_f32_e32 v167, s70, v167\n" \
C_"v164, v164, v165\n" \
"v_mul_f32_e32 v174, s70, v174\n" \
"v_mul_f32_e32 v175, s70, v175\n" \
C_"v172, v172, v173\n" \
"v_pk_mul_f32 v[86:87], v[86:87], s[70:71]\n" \
C_"v84, v84, v85\n" \
"v_pk_mul_f32 v[94:95], v[94:95], s[70:71]\n" \
C_"v92, v92, v93\n" \
"v_pk_mul_f32 v[102:103], v[102:103], s[70:71]\n" \
C_"v100, v100, v101\n" \
"v_pk_mul_f32 v[110:111], v[110:111], s[70:71]\n" \
C_"v108, v108, v109\n" \
"v_pk_mul_f32 v[54:55], v[54:55], s[70:71]\n" \
C_"v52, v52, v53\n" \
"v_pk_mul_f32 v[62:63], v[62:63], s[70:71]\n" \
C_"v60, v60, v61\n" \
"v_fma_f32 v192, v192, s72, -v193\n" \
"v_fma_f32 v194, v194, s72, -v193\n" \
"v_fma_f32 v208, v208, s72, -v209\n" \
"v_fma_f32 v210, v210, s72, -v209\n" \
"v_mul_f32_e32 v168, s70, v168\n" \
"v_mul_f32_e32 v169, s70, v169\n" \
"v_mul_f32_e32 v170, s70, v170\n" \
"v_mul_f32_e32 v171, s70, v171\n" \
C_"v165, v166, v167\n" \
"v_mul_f32_e32 v176, s70, v176\n" \
"v_mul_f32_e32 v177, s70, v177\n" \
"v_mul_f32_e32 v178, s70, v178\n" \
"v_mul_f32_e32 v179, s70, v179\n" \
C_"v173, v174, v175\n" \
"v_pk_mul_f32 v[88:89], v[88:89], s[70:71]\n" \
"v_pk_mul_f32 v[90:91], v[90:91], s[70:71]\n" \
C_"v85, v86, v87\n" \
"v_pk_mul_f32 v[96:97], v[96:97], s[70:71]\n" \
"v_pk_mul_f32 v[98:99], v[98:99], s[70:71]\n" \
C_"v93, v94, v95\n" \
"v_pk_mul_f32 v[104:105], v[104:105], s[70:71]\n" \
"v_pk_mul_f32 v[106:107], v[106:107], s[70:71]\n" \
C_"v101, v102, v103\n" \
"v_pk_mul_f32 v[112:113], v[112:113], s[70:71]\n" \
"v_pk_mul_f32 v[114:115], v[114:115], s[70:71]\n" \
C_"v109, v110, v111\n" \
"v_pk_mul_f32 v[56:57], v[56:57], s[70:71]\n" \
"v_pk_mul_f32 v[58:59], v[58:59], s[70:71]\n" \
C_"v53, v54, v55\n" \
"v_pk_mul_f32 v[64:65], v[64:65], s[70:71]\n" \
"v_pk_mul_f32 v[66:67], v[66:67], s[70:71]\n" \
C_"v61, v62, v63\n" \
"v_pk_mul_f32 v[80:81], v[80:81], s[70:71]\n" \
"v_pk_mul_f32 v[82:83], v[82:83], s[70:71]\n" \
E_"v192, v192\n" \
E_"v194, v194\n" \
E_"v208, v208\n" \
E_"v210, v210\n" \
"v_fma_f32 v224, v224, s72, -v225\n" \
"v_fma_f32 v226, v226, s72, -v225\n" \
M_"v[76:79], a[184:187], a[132:135], v[76:79]\n" \
C_"v166, v168, v169\n" \
C_"v167, v170, v171\n" \
C_"v174, v176, v177\n" \
C_"v175, v178, v179\n" \
C_"v86, v88, v89\n" \
C_"v87, v90, v91\n" \
C_"v94, v96, v97\n" \
C_"v95, v98, v99\n" \
C_"v102, v104, v105\n" \
C_"v103, v106, v107\n" \
C_"v110, v112, v113\n" \
C_"v111, v114, v115\n" \
C_"v54, v56, v57\n" \
C_"v55, v58, v59\n" \
C_"v62, v64, v65\n" \
C_"v63, v66, v67\n" \
E_"v224, v224\n" \
E_"v226, v226\n" \
"v_fma_f32 v240, v240, s72, -v241\n" \
"v_fma_f32 v242, v242, s72, -v241\n" \
"ds_write_b128 v21, v[164:167] offset:28672\n" \
C_"v192, v192, v194\n" \
C_"v208, v208, v210\n" \
E_"v240, v240\n" \
E_"v242, v242\n" \
C_"v132, v48, v49\n" \
C_"v133, v50, v51\n" \
C_"v134, v196, v197\n" \
C_"v135, v198, v199\n" \
"ds_write_b128 v21, v[172:175] offset:29696\n" \
C_"v136, v212, v213\n" \
C_"v137, v214, v215\n" \
"v_perm_b32 v184, v192, v192, v40\n" \
"v_perm_b32 v185, v192, v192, v41\n" \
"v_perm_b32 v186, v192, v192, v42\n" \
"v_perm_b32 v187, v192, v192, v43\n" \
"v_perm_b32 v200, v208, v208, v40\n" \
"v_perm_b32 v201, v208, v208, v41\n" \
"v_perm_b32 v202, v208, v208, v42\n" \
"v_perm_b32 v203, v208, v208, v43\n" \
"ds_write_b128 v27, v[84:87] offset:8192\n" \
C_"v224, v224, v226\n" \
C_"v138, v228, v229\n" \
C_"v139, v230, v231\n" \
M_"v[32:35], v[132:135], v[184:187], 0\n" \
M_"v[36:39], v[134:137], v[200:203], 0\n" \
"v_perm_b32 v216, v224, v224, v40\n" \
"v_perm_b32 v217, v224, v224, v41\n" \
"ds_write_b128 v27, v[92:95] offset:9216\n" \
"v_perm_b32 v218, v224, v224, v42\n" \
"v_perm_b32 v219, v224, v224, v43\n" \
C_"v240, v240, v242\n" \
M_"v[76:79], a[248:251], a[196:199], v[76:79]\n" \
C_"v140, v244, v245\n" \
C_"v141, v246, v247\n" \
M_"v[248:251], v[136:139], v[216:219], 0\n" \
"v_perm_b32 v232, v240, v240, v40\n" \
"v_perm_b32 v233, v240, v240, v41\n" \
"ds_write_b128 v27, v[100:103] offset:12288\n" \
"v_perm_b32 v234, v240, v240, v42\n" \
"v_perm_b32 v235, v240, v240, v43\n" \
"v_pk_mul_f32 v[76:77], v[76:77], s[70:71]\n" \
"v_pk_mul_f32 v[78:79], v[78:79], s[70:71]\n" \
C_"v76, v76, v77\n" \
C_"v77, v78, v79\n" \
C_"v78, v80, v81\n" \
C_"v79, v82, v83\n" \
M_"v[128:131], v[138:141], v[232:235], 0\n" \
"ds_write_b128 v27, v[108:111] offset:13312\n" \
M_"v[72:75], a[180:183], a[132:135], v[72:75]\n" \
C_"v116, v252, v253\n" \
C_"v117, v254, v255\n" \
M_"v[72:75], a[244:247], a[196:199], v[72:75]\n" \
"v_exp_f32_e64 v45, v45\n" \
M_"v[192:195], v[116:119], v[184:187], 0\n" \
"ds_write_b128 v27, v[52:55] offset:0\n" \
M_"v[208:211], v[118:121], v[200:203], 0\n" \
M_"v[224:227], v[120:123], v[216:219], 0\n" \
M_"v[240:243], v[122:125], v[232:235], 0\n" \
"ds_write_b128 v27, v[60:63] offset:1024\n" \
"v_mov_b32_dpp v34, v38 quad_perm:[0,1,2,3] row_mask:0x2 bank_mask:0xf\n" \
"v_pk_mul_f32 v[72:73], v[72:73], s[70:71]\n" \
"v_pk_mul_f32 v[74:75], v[74:75], s[70:71]\n" \
"v_mov_b32_dpp v34, v250 quad_perm:[0,1,2,3] row_mask:0x4 bank_mask:0xf\n" \
"v_add_f32_e64 v45, 1.0, v45\n" \
M_"v[68:71], a[176:179], a[132:135], v[68:71]\n" \
"v_mov_b32_dpp v34, v130 quad_perm:[0,1,2,3] row_mask:0x8 bank_mask:0xf\n" \
"v_mov_b32_dpp v32, v36 quad_perm:[0,1,2,3] row_mask:0x2 bank_mask:0xf\n" \
"v_rcp_f32_e64 v45, v45\n" \
"ds_write_b128 v27, v[76:79] offset:5120\n" \
"v_mov_b32_dpp v33, v37 quad_perm:[0,1,2,3] row_mask:0x2 bank_mask:0xf\n" \
"v_rcp_f32_e64 v34, v34\n" \
"v_mov_b32_dpp v32, v248 quad_perm:[0,1,2,3] row_mask:0x4 bank_mask:0xf\n" \
"v_mov_b32_dpp v33, v249 quad_perm:[0,1,2,3] row_mask:0x4 bank_mask:0xf\n" \
"v_mul_f32_e64 v34, v45, v34\n" \
"v_mov_b32_dpp v32, v128 quad_perm:[0,1,2,3] row_mask:0x8 bank_mask:0xf\n" \
"v_mov_b32_dpp v33, v129 quad_perm:[0,1,2,3] row_mask:0x8 bank_mask:0xf\n" \
"v_mul_f32_e32 v32, v34, v32\n" \
"v_mul_f32_e32 v33, v34, v33\n" \
"v_mov_b32_e64 v38, v34\n" \
M_"v[68:71], a[240:243], a[196:199], v[68:71]\n" \
"v_sub_f32_e32 v44, 1.0, v45\n" \
"v_permlane16_swap_b32_e32 v34, v38\n" \
"v_mov_b32_e32 v250, v34\n" \
"v_mov_b32_e32 v130, v38\n" \
C_"v32, v32, v33\n" \
"v_permlane32_swap_b32_e32 v34, v250\n" \
"v_permlane32_swap_b32_e32 v38, v130\n" \
"v_mul_f32_e64 v192, v34, v192\n" \
"v_pk_mul_f32 v[68:69], v[68:69], s[70:71]\n" \
"v_pk_mul_f32 v[70:71], v[70:71], s[70:71]\n" \
C_"v68, v68, v69\n" \
C_"v69, v70, v71\n" \
C_"v70, v72, v73\n" \
C_"v71, v74, v75\n" \
"ds_write_b128 v27, v[68:71] offset:4096\n" \
"v_mul_f32_e32 v193, v34, v193\n" \
"v_mul_f32_e32 v208, v38, v208\n" \
"v_mul_f32_e32 v209, v38, v209\n" \
"v_mul_f32_e32 v224, v250, v224\n" \
"v_mul_f32_e32 v225, v250, v225\n" \
"v_mul_f32_e32 v240, v130, v240\n" \
"v_mul_f32_e32 v241, v130, v241\n" \
"v_mul_f32_e32 v194, v34, v194\n" \
"v_mul_f32_e64 v195, v34, v195\n" \
C_"v192, v192, v193\n" \
"v_mul_f32_e32 v210, v38, v210\n" \
"v_mul_f32_e32 v211, v38, v211\n" \
C_"v208, v208, v209\n" \
"v_mul_f32_e32 v226, v250, v226\n" \
"v_mul_f32_e32 v227, v250, v227\n" \
C_"v224, v224, v225\n" \
"v_mul_f32_e32 v242, v130, v242\n" \
"v_mul_f32_e32 v243, v130, v243\n" \
C_"v240, v240, v241\n" \
C_"v193, v194, v195\n" \
"s_add_u32 s84, s48, 0x0\n" \
"s_nop 0\n" \
C_"v209, v210, v211\n" \
"s_add_u32 s85, s48, 0x24800\n" \
C_"v225, v226, v227\n" \
"s_add_u32 s86, s48, 0x49000\n" \
C_"v241, v242, v243\n" \
"s_add_u32 s87, s48, 0x6d800\n" \
"ds_write_b32 v26, v44\n" \
"buffer_store_dword v32, v25, s[80:83], s49 offen sc1\n" \
"buffer_store_dwordx2 v[192:193], v24, s[80:83], s84 offen sc1\n" \
"buffer_store_dwordx2 v[208:209], v24, s[80:83], s85 offen sc1\n" \
"buffer_store_dwordx2 v[224:225], v24, s[80:83], s86 offen sc1\n" \
"buffer_store_dwordx2 v[240:241], v24, s[80:83], s87 offen sc1\n" \
"s_branch L_end_%=\n" \
"L_poll61_%=:\n" \
"s_mov_b32 s84, 0\n" \
"L_pl61_%=:\n" \
"s_sleep 1\n" \
"v_bfe_u32 v44, v20, 2, 5\n" \
"buffer_load_dword v44, v44, s[80:83], s38 offen offset:128 sc1\n" \
"s_waitcnt vmcnt(0)\n" \
"v_cmp_ne_u32_e32 vcc, s62, v44\n" \
"s_add_u32 s84, s84, 1\n" \
"s_nop 3\n" \
"s_cbranch_vccz L_pollr61_%=\n" \
"s_cmp_gt_u32 s84, 0x400000\n" \
"s_cbranch_scc0 L_pl61_%=\n" \
"s_branch L_pollr61_%=\n" \
"L_poll161_%=:\n" \
"s_mov_b32 s84, 0\n" \
"L_pl161_%=:\n" \
"s_sleep 1\n" \
"v_bfe_u32 v44, v20, 2, 5\n" \
"buffer_load_dword v44, v44, s[80:83], s38 offen offset:0 sc1\n" \
"s_waitcnt vmcnt(0)\n" \
"v_cmp_ne_u32_e32 vcc, s62, v44\n" \
"s_add_u32 s84, s84, 1\n" \
"s_nop 3\n" \
"s_cbranch_vccz L_pollr161_%=\n" \
"s_cmp_gt_u32 s84, 0x400000\n" \
"s_cbranch_scc0 L_pl161_%=\n" \
"s_branch L_pollr161_%=\n" \
"L_end_%=:\n"
#define PROJ_PAGES 20
#define PROJ_VCLOB "v21","v24","v25","v28","v29","v32","v33","v34","v35","v36","v37","v38","v39","v40","v41","v42","v43","v44","v45","v46","v47","v48","v49","v50","v51","v52","v53","v54","v55","v56","v57","v58","v59","v60","v61","v62","v63","v64","v65","v66","v67","v68","v69","v70","v71","v72","v73","v74","v75","v76","v77","v78","v79","v80","v81","v82","v83","v84","v85","v86","v87","v88","v89","v90","v91","v92","v93","v94","v95","v96","v97","v98","v99","v100","v101","v102","v103","v104","v105","v106","v107","v108","v109","v110","v111","v112","v113","v114","v115","v116","v117","v118","v119","v120","v121","v122","v123","v124","v125","v126","v127","v128","v129","v130","v131","v132","v133","v134","v135","v136","v137","v138","v139","v140","v141","v142","v143","v144","v145","v146","v147","v148","v149","v150","v151","v152","v153","v154","v155","v156","v157","v158","v159","v160","v161","v162","v163","v164","v165","v166","v167","v168","v169","v170","v171","v172","v173","v174","v175","v176","v177","v178","v179","v180","v181","v182","v183","v184","v185","v186","v187","v188","v189","v190","v192","v193","v194","v195","v196","v197","v198","v199","v200","v201","v202","v203","v204","v205","v206","v207","v208","v209","v210","v211","v212","v213","v214","v215","v216","v217","v218","v219","v220","v221","v222","v223","v224","v225","v226","v227","v228","v229","v230","v231","v232","v233","v234","v235","v236","v237","v238","v239","v240","v241","v242","v243","v244","v245","v246","v247","v248","v249","v250","v251","v252","v253","v254","v255"
#define PROJ_SCLOB "s4","s5","s6","s7","s32","s33","s34","s35","s36","s37","s38","s39","s40","s41","s42","s43","s44","s45","s46","s47","s48","s49","s50","s51","s52","s53","s54","s55","s63","s64","s65","s66","s67","s68","s69","s70","s71","s72","s73","s74","s75","s76","s77","s78","s79","s80","s81","s82","s83","s84","s85","s86","s87","s88","s89","s90","s91","s92","s93","s94","s95","s96","s97","s98","s99","s100","s101"

typedef const u16* const __attribute__((address_space(4)))* kaptr;
__device__ __forceinline__ void proj_phase(kaptr& ka, char*& ws, const unsigned wid, u32x4 (&vp)[5], unsigned& vl, u32x4 (&sa)[5], u32x2 s24, u32x2& s28, unsigned tdum, u32x4& srdA, u32x2& sd, unsigned& tag
    ) {
    const unsigned b = blockIdx.x, h = (b & 7) * 4 + ((b >> 3) & 3), mt = b >> 5;
    const unsigned rb = (wid < 2u ? 15u - mt : mt) * 128u;
    const unsigned lane = threadIdx.x & 63u, wc = wid & 1u;
    u32x2 vAc, vB;
#pragma unroll
    for (int par = 0; par < 2; ++par) {
        const unsigned lr = lane >> 3, cc = (lane & 7u) ^ (4u * par + (lr >> 1)), r0 = rb + wc * 64u + lr;
        vAc[par] = r0 * (DC * 2) + cc * 16u;
        const unsigned br = lane >> 4, p16 = lane & 15u, sc = (((p16 >> 1) ^ (br | (par << 2))) << 1) | (p16 & 1u);
        vB[par] = (16u * wid + br) * (NBIG * 2) + h * (HD * 2) + sc * 16u;
    }
    vl = lane * 16u;
    u32x2 s30 = {wid * 8192u, 65536u + wid * 4096u};
    asm volatile(PROJ_ASM
                 : "={s62}"(tag), "={s[56:59]}"(srdA), "={s[60:61]}"(sd), "+{s[8:11]}"(sa[0]), "+{s[12:15]}"(sa[1]), "+{s[16:19]}"(sa[2]), "+{s[20:23]}"(sa[3]), "+{s[24:25]}"(s24), "+{s[28:29]}"(s28), "+{s[30:31]}"(s30),
                   "={v[0:3]}"(vp[0]), "={v[4:7]}"(vp[1]), "={v[8:11]}"(vp[2]), "={v[12:15]}"(vp[3]), "={v[16:19]}"(vp[4]), "+{v[22:23]}"(vAc), "+{v[26:27]}"(vB), "+{v191}"(tdum), "+{s[26:27]}"(ws), "+{s[0:1]}"(ka)
                 : "{s2}"(b), "{s3}"(wid), "{v20}"(vl)
                 : "memory", "m0", "vcc", "scc", PROJ_VCLOB, PROJ_SCLOB, ACLOB_LO, ACLOB_HI);
    sa[4] = (u32x4){s24[0], 0u, 0u, 0u};
}

namespace att {
#define DI __device__ __forceinline__
typedef __bf16 bfx8 __attribute__((ext_vector_type(8)));
DI unsigned rfl(unsigned x) { return __builtin_amdgcn_readfirstlane(x); }
DI f32x4 mfma(u32x4 a, u32x4 b, f32x4 c) { return __builtin_amdgcn_mfma_f32_16x16x32_bf16(__builtin_bit_cast(bfx8, a), __builtin_bit_cast(bfx8, b), c, 0, 0, 0); }
typedef __bf16 bfx2 __attribute__((ext_vector_type(2)));
typedef float f32x2 __attribute__((ext_vector_type(2)));
DI unsigned cvtpk2(float lo, float hi) { f32x2 v = {lo, hi}; bfx2 b = __builtin_convertvector(v, bfx2); return __builtin_bit_cast(unsigned, b); }
DI void att_exact(const u16* __restrict__ x, const u16* __restrict__ wq, char* __restrict__ ws, unsigned h, unsigned r0, float g1) {
    const unsigned lane = lane_id(), g = lane >> 4, c = lane & 15u;
    f32x4 o[8];
#pragma unroll
    for (int n = 0; n < 8; ++n) o[n] = (f32x4){0.f, 0.f, 0.f, 0.f};
#pragma unroll 1
    for (unsigned kk = 0; kk < (unsigned)DM; kk += 32u) {
        const unsigned k = kk + 8u * g;
        const u32x4 xf = *reinterpret_cast<const u32x4*>(x + (r0 + c) * DM + k);
#pragma unroll
        for (int n = 0; n < 8; ++n) {
            const u16* w = wq + k * NBIG + h * HD + n * 16 + c; u32x4 wf;
#pragma unroll
            for (int e = 0; e < 4; ++e) wf[e] = (unsigned)w[2 * e * NBIG] | ((unsigned)w[(2 * e + 1) * NBIG] << 16);
            o[n] = mfma(wf, xf, o[n]);
        }
    }
    u32x4 q[4];
#pragma unroll
    for (int p = 0; p < 4; ++p) {
        const f32x4 a = o[2 * p] * 0.12751743082f, b = o[2 * p + 1] * 0.12751743082f;
        q[p] = (u32x4){cvtpk2(a[0], a[1]), cvtpk2(a[2], a[3]), cvtpk2(b[0], b[1]), cvtpk2(b[2], b[3])};
    }
#pragma unroll
    for (int n = 0; n < 8; ++n) o[n] = (f32x4){0.f, 0.f, 0.f, 0.f};
    const __amdgpu_buffer_rsrc_t srd = __builtin_amdgcn_make_buffer_rsrc((void*)(ws + WS_K + (size_t)h * 1048576), (short)0, 1048576, 0x00020000);
    float m = -3.0e38f, l = 0.f;
#pragma unroll 1
    for (unsigned k0 = 0; k0 <= r0 + 15u; k0 += 32u) {
        const unsigned tb = (k0 >> 6) * 32768u + (k0 & 32u) * 256u;
        f32x4 s[2]; float mx = m;
#pragma unroll
        for (int kt = 0; kt < 2; ++kt) {
            s[kt] = (f32x4){0.f, 0.f, 0.f, 0.f};
#pragma unroll
            for (int p = 0; p < 4; ++p) s[kt] = mfma(__builtin_amdgcn_raw_buffer_load_b128(srd, lane * 16u + (kt * 4 + p) * 1024u, tb, 16), q[p], s[kt]);
#pragma unroll
            for (int e = 0; e < 4; ++e) { if (k0 + 16u * kt + 4u * g + e > r0 + c) s[kt][e] = -3.0e38f; mx = fmaxf(mx, s[kt][e]); }
        }
        mx = fmaxf(mx, __shfl_xor(mx, 16)); mx = fmaxf(mx, __shfl_xor(mx, 32));
        const float al = __builtin_amdgcn_exp2f(m - mx); m = mx;
        u32x4 pf;
#pragma unroll
        for (int e = 0; e < 4; ++e) pf[e] = cvtpk2(__builtin_amdgcn_exp2f(s[e >> 1][2 * (e & 1)] - m), __builtin_amdgcn_exp2f(s[e >> 1][2 * (e & 1) + 1] - m));
        l *= al;
#pragma unroll
        for (int e = 0; e < 4; ++e) l += __uint_as_float(pf[e] << 16) + __uint_as_float(pf[e] & 0xffff0000u);
#pragma unroll
        for (int n = 0; n < 8; ++n) o[n] = mfma(__builtin_amdgcn_raw_buffer_load_b128(srd, lane * 16u + 16384u + n * 1024u, tb, 16), pf, o[n] * al);
    }
    l += __shfl_xor(l, 16); l += __shfl_xor(l, 32);
    const float f = g1 / l;
    const __amdgpu_buffer_rsrc_t srdA = __builtin_amdgcn_make_buffer_rsrc((void*)(ws + WS_AF), (short)0, 128 * KS_TOT * 1024, 0x00020000);
#pragma unroll
    for (int p = 0; p < 4; ++p) {
        const f32x4 a = o[2 * p] * f, b = o[2 * p + 1] * f;
        __builtin_amdgcn_raw_buffer_store_b128((u32x4){cvtpk2(a[0], a[1]), cvtpk2(a[2], a[3]), cvtpk2(b[0], b[1]), cvtpk2(b[2], b[3])}, srdA, lane * 16u + p * 1024u, ((r0 >> 4) * KS_TOT + 18u + 4u * h) * 1024u, 16);
    }
}
}

#define ATT_ASM \
"s_branch L_ago_%=\n" \
".p2align 12\n" \
"L_ab:\n" \
".fill (L_ab + 0 - .) / 4, 4, 0xBF800000\n" \
"L_ap0:\n" \
"s_setpc_b64 s[88:89]\n" \
"L_ago_%=:\n" \
"s_mov_b32 s101, 0\n" \
"s_waitcnt vmcnt(15) lgkmcnt(0)\n" \
"s_barrier\n" \
"v_mfma_f32_32x32x16_bf16 v[128:143], a[120:123], a[120:123], 0\n" \
"v_and_b32_e32 v63, 0x300, v20\n" \
R_"v[96:99], v63 offset:0\n" \
"v_bfe_u32 v61, v20, 2, 5\n" \
"s_mov_b32 s96, s24\n" \
"v_add_u32_e32 v58, s22, v20\n" \
R_"a[0:3], v58 offset:0\n" \
"v_mfma_f32_32x32x16_bf16 v[144:159], a[120:123], a[120:123], 0\n" \
"buffer_load_dword v57, v61, s[56:59], s96 offen sc1\n" \
R_"a[16:19], v58 offset:8192\n" \
"v_add_u32_e32 v59, s23, v20\n" \
R_"a[32:35], v59 offset:0\n" \
R_"a[48:51], v59 offset:8192\n" \
"v_mfma_f32_32x32x16_bf16 v[160:175], a[120:123], a[120:123], 0\n" \
R_"v[100:103], v63 offset:1024\n" \
R_"a[4:7], v58 offset:1024\n" \
R_"a[20:23], v58 offset:9216\n" \
"s_sub_u32 s67, s12, 0xc000\n" \
"s_mov_b32 s65, s12\n" \
"v_add_u32_e32 v55, s67, v20\n" \
"v_mfma_f32_32x32x16_bf16 v[176:191], a[120:123], a[120:123], 0\n" \
"v_add_u32_e32 v52, 0xc000, v55\n" \
"s_xor_b32 m0, s65, 0x10000\n" \
R_"a[36:39], v59 offset:1024\n" \
B_"v52, s[8:11], 0 offen offset:0 sc1 lds\n" \
R_"a[52:55], v59 offset:9216\n" \
"v_mfma_f32_32x32x16_bf16 v[192:207], a[120:123], a[120:123], 0\n" \
R_"v[104:107], v63 offset:2048\n" \
R_"a[8:11], v58 offset:2048\n" \
R_"a[24:27], v58 offset:10240\n" \
B_"v52, s[8:11], 0 offen offset:1024 sc1 lds\n" \
R_"a[40:43], v59 offset:2048\n" \
"v_mfma_f32_32x32x16_bf16 v[208:223], a[120:123], a[120:123], 0\n" \
R_"a[56:59], v59 offset:10240\n" \
R_"v[108:111], v63 offset:3072\n" \
R_"a[12:15], v58 offset:3072\n" \
R_"a[28:31], v58 offset:11264\n" \
"s_waitcnt lgkmcnt(15)\n" \
"v_xor_b32_e32 v96, 0x80008000, v96\n" \
"v_mfma_f32_32x32x16_bf16 v[224:239], a[120:123], a[120:123], 0\n" \
"v_xor_b32_e32 v97, 0x80008000, v97\n" \
"v_xor_b32_e32 v98, 0x80008000, v98\n" \
"v_xor_b32_e32 v99, 0x80008000, v99\n" \
R_"a[44:47], v59 offset:3072\n" \
R_"a[60:63], v59 offset:11264\n" \
R_"a[160:163], v20 offset:0\n" \
"v_mfma_f32_32x32x16_bf16 v[240:255], a[120:123], a[120:123], 0\n" \
R_"a[164:167], v20 offset:1024\n" \
"s_waitcnt lgkmcnt(15)\n" \
"v_xor_b32_e32 v100, 0x80008000, v100\n" \
"v_xor_b32_e32 v101, 0x80008000, v101\n" \
"v_xor_b32_e32 v102, 0x80008000, v102\n" \
"v_xor_b32_e32 v103, 0x80008000, v103\n" \
M_"v[32:35], v[96:99], a[0:3], 0\n" \
R_"a[168:171], v20 offset:2048\n" \
R_"a[172:175], v20 offset:3072\n" \
M_"v[36:39], v[96:99], a[16:19], 0\n" \
R_"a[188:191], v20 offset:7168\n" \
R_"a[184:187], v20 offset:6144\n" \
M_"v[40:43], v[96:99], a[32:35], 0\n" \
"s_waitcnt lgkmcnt(15)\n" \
"v_xor_b32_e32 v104, 0x80008000, v104\n" \
M_"v[44:47], v[96:99], a[48:51], 0\n" \
"v_xor_b32_e32 v105, 0x80008000, v105\n" \
"v_xor_b32_e32 v106, 0x80008000, v106\n" \
M_"v[32:35], v[100:103], a[4:7], v[32:35]\n" \
"v_xor_b32_e32 v107, 0x80008000, v107\n" \
R_"a[180:183], v20 offset:5120\n" \
M_"v[36:39], v[100:103], a[20:23], v[36:39]\n" \
R_"a[176:179], v20 offset:4096\n" \
"s_waitcnt lgkmcnt(12)\n" \
"v_xor_b32_e32 v108, 0x80008000, v108\n" \
M_"v[40:43], v[100:103], a[36:39], v[40:43]\n" \
"v_xor_b32_e32 v109, 0x80008000, v109\n" \
"v_xor_b32_e32 v110, 0x80008000, v110\n" \
M_"v[44:47], v[100:103], a[52:55], v[44:47]\n" \
"v_xor_b32_e32 v111, 0x80008000, v111\n" \
"v_add_u32_e32 v60, s15, v20\n" \
M_"v[32:35], v[104:107], a[8:11], v[32:35]\n" \
"v_add_u32_e32 v60, 0x10000, v60\n" \
R_"a[128:131], v20 offset:8192\n" \
M_"v[36:39], v[104:107], a[24:27], v[36:39]\n" \
R_"a[144:147], v20 offset:12288\n" \
R_"a[132:135], v20 offset:9216\n" \
M_"v[40:43], v[104:107], a[40:43], v[40:43]\n" \
R_"a[148:151], v20 offset:13312\n" \
R_"a[136:139], v20 offset:10240\n" \
M_"v[44:47], v[104:107], a[56:59], v[44:47]\n" \
R_"a[152:155], v20 offset:14336\n" \
"s_waitcnt lgkmcnt(13)\n" \
M_"v[32:35], v[108:111], a[12:15], v[32:35]\n" \
R_"a[140:143], v20 offset:11264\n" \
R_"a[156:159], v20 offset:15360\n" \
M_"v[36:39], v[108:111], a[28:31], v[36:39]\n" \
R_"a[64:67], v60 offset:16384\n" \
R_"a[68:71], v60 offset:17408\n" \
M_"v[40:43], v[108:111], a[44:47], v[40:43]\n" \
R_"a[72:75], v60 offset:18432\n" \
R_"a[76:79], v60 offset:19456\n" \
M_"v[44:47], v[108:111], a[60:63], v[44:47]\n" \
R_"a[80:83], v60 offset:24576\n" \
R_"a[84:87], v60 offset:25600\n" \
M_"v[64:67], a[160:163], a[0:3], v[32:35]\n" \
R_"a[88:91], v60 offset:26624\n" \
"s_waitcnt lgkmcnt(15)\n" \
M_"v[64:67], a[164:167], a[4:7], v[64:67]\n" \
R_"a[92:95], v60 offset:27648\n" \
R_"a[192:195], v60 offset:0\n" \
M_"v[64:67], a[168:171], a[8:11], v[64:67]\n" \
R_"a[196:199], v60 offset:1024\n" \
R_"a[200:203], v60 offset:2048\n" \
M_"v[64:67], a[172:175], a[12:15], v[64:67]\n" \
R_"a[204:207], v60 offset:3072\n" \
R_"a[208:211], v60 offset:8192\n" \
M_"v[68:71], a[188:191], a[12:15], v[32:35]\n" \
R_"a[212:215], v60 offset:9216\n" \
R_"a[216:219], v60 offset:10240\n" \
M_"v[68:71], a[184:187], a[8:11], v[68:71]\n" \
R_"a[220:223], v60 offset:11264\n" \
"v_mov_b32_e32 v56, s62\n" \
M_"v[68:71], a[180:183], a[4:7], v[68:71]\n" \
"v_readlane_b32 s68, v11, 0\n" \
"s_mov_b32 m0, s20\n" \
M_"v[68:71], a[176:179], a[0:3], v[68:71]\n" \
"global_load_lds_dword v[16:17], off\n" \
M_"v[76:79], a[176:179], a[16:19], v[36:39]\n" \
E_"v66, v66\n" \
M_"v[76:79], a[180:183], a[20:23], v[76:79]\n" \
E_"v67, v67\n" \
M_"v[76:79], a[184:187], a[24:27], v[76:79]\n" \
E_"v64, v64\n" \
M_"v[76:79], a[188:191], a[28:31], v[76:79]\n" \
E_"v70, v70\n" \
M_"v[72:75], a[172:175], a[28:31], v[36:39]\n" \
E_"v71, v71\n" \
M_"v[72:75], a[168:171], a[24:27], v[72:75]\n" \
E_"v68, v68\n" \
M_"v[72:75], a[164:167], a[20:23], v[72:75]\n" \
E_"v69, v69\n" \
M_"v[72:75], a[160:163], a[16:19], v[72:75]\n" \
C_"v71, v70, v71\n" \
"v_accvgpr_read_b32 v28, a124\n" \
M_"v[80:83], a[160:163], a[32:35], v[40:43]\n" \
E_"v65, v65\n" \
M_"v[80:83], a[164:167], a[36:39], v[80:83]\n" \
E_"v76, v76\n" \
M_"v[80:83], a[168:171], a[40:43], v[80:83]\n" \
E_"v77, v77\n" \
M_"v[80:83], a[172:175], a[44:47], v[80:83]\n" \
E_"v72, v72\n" \
M_"v[84:87], a[188:191], a[44:47], v[40:43]\n" \
E_"v73, v73\n" \
M_"v[84:87], a[184:187], a[40:43], v[84:87]\n" \
E_"v74, v74\n" \
M_"v[84:87], a[180:183], a[36:39], v[84:87]\n" \
E_"v75, v75\n" \
M_"v[84:87], a[176:179], a[32:35], v[84:87]\n" \
C_"v72, v72, v73\n" \
"v_accvgpr_read_b32 v29, a125\n" \
M_"v[92:95], a[176:179], a[48:51], v[44:47]\n" \
E_"v78, v78\n" \
M_"v[92:95], a[180:183], a[52:55], v[92:95]\n" \
"s_waitcnt vmcnt(3)\n" \
"v_cmp_ne_u32_e32 vcc, s62, v57\n" \
M_"v[92:95], a[184:187], a[56:59], v[92:95]\n" \
E_"v79, v79\n" \
M_"v[92:95], a[188:191], a[60:63], v[92:95]\n" \
"v_accvgpr_read_b32 v21, a126\n" \
C_"v70, v68, v69\n" \
M_"v[88:91], a[172:175], a[60:63], v[44:47]\n" \
"s_cbranch_vccnz L_skb_%=\n" \
"L_rkb_%=:\n" \
C_"v73, v74, v75\n" \
"s_mov_b64 exec, 1\n" \
"buffer_store_dword v56, off, s[56:59], s68 offset:0 sc1\n" \
"s_mov_b64 exec, -1\n" \
M_"v[88:91], a[168:171], a[56:59], v[88:91]\n" \
"v_mov_b32_e32 v62, 0\n" \
"s_mov_b32 s64, 0x8000\n" \
M_"v[88:91], a[164:167], a[52:55], v[88:91]\n" \
"v_add_u32_e32 v50, 0x6000, v55\n" \
"v_add_u32_e32 v51, 0xa000, v55\n" \
M_"v[88:91], a[160:163], a[48:51], v[88:91]\n" \
"v_add_u32_e32 v53, 0x10000, v55\n" \
"v_add_u32_e32 v48, 0xc000, v20\n" \
"s_waitcnt vmcnt(5) lgkmcnt(0)\n" \
"s_barrier\n" \
"v_add_u32_e32 v49, s17, v20\n" \
"v_accvgpr_write_b32 a124, v96\n" \
"v_accvgpr_write_b32 a125, v97\n" \
"v_accvgpr_write_b32 a126, v98\n" \
"v_accvgpr_write_b32 a127, v99\n" \
"v_accvgpr_write_b32 a224, v100\n" \
"v_accvgpr_write_b32 a225, v101\n" \
"v_accvgpr_write_b32 a226, v102\n" \
"v_accvgpr_write_b32 a227, v103\n" \
"v_accvgpr_write_b32 a228, v104\n" \
"v_accvgpr_write_b32 a229, v105\n" \
"v_accvgpr_write_b32 a230, v106\n" \
"v_accvgpr_write_b32 a231, v107\n" \
"v_accvgpr_write_b32 a232, v108\n" \
"v_accvgpr_write_b32 a233, v109\n" \
"v_accvgpr_write_b32 a234, v110\n" \
"v_accvgpr_write_b32 a235, v111\n" \
C_"v69, v66, v67\n" \
C_"v68, v64, v65\n" \
C_"v74, v76, v77\n" \
C_"v75, v78, v79\n" \
"s_sub_u32 s66, s18, 1\n" \
"s_cbranch_scc1 L_tr_%=\n" \
"s_branch L_1_%=\n" \
".fill (L_ab + 1528 - .) / 4, 4, 0xBF800000\n" \
"L_tL_1_%=:\n" \
"s_waitcnt vmcnt(4) lgkmcnt(0)\n" \
"v_smfmac_f32_16x16x64_bf16 a[100:103], a[104:107], v[116:123], v21\n" \
"s_barrier\n" \
"L_1_%=:\n" \
M_"v[96:99], a[128:131], a[0:3], v[32:35]\n" \
"s_xor_b32 m0, s65, 0x12000\n" \
R_"a[160:163], v49 offset:0\n" \
M_"v[96:99], a[132:135], a[4:7], v[96:99]\n" \
B_"v50, s[8:11], s64 offen offset:0 sc1 lds\n" \
M_"v[96:99], a[136:139], a[8:11], v[96:99]\n" \
R_"a[164:167], v49 offset:1024\n" \
R_"a[168:171], v49 offset:2048\n" \
M_"v[96:99], a[140:143], a[12:15], v[96:99]\n" \
R_"a[172:175], v49 offset:3072\n" \
R_"a[176:179], v49 offset:4096\n" \
M_"v[100:103], a[156:159], a[12:15], v[32:35]\n" \
R_"a[180:183], v49 offset:5120\n" \
R_"a[184:187], v49 offset:6144\n" \
M_"v[100:103], a[152:155], a[8:11], v[100:103]\n" \
"v_exp_f32_e64 v86, v86\n" \
M_"v[100:103], a[148:151], a[4:7], v[100:103]\n" \
"v_exp_f32_e64 v87, v87\n" \
M_"v[100:103], a[144:147], a[0:3], v[100:103]\n" \
R_"a[188:191], v49 offset:7168\n" \
C_"v87, v86, v87\n" \
M_"v[108:111], a[144:147], a[16:19], v[36:39]\n" \
"v_exp_f32_e64 v84, v84\n" \
M_"v[108:111], a[148:151], a[20:23], v[108:111]\n" \
"v_exp_f32_e64 v85, v85\n" \
M_"v[108:111], a[152:155], a[24:27], v[108:111]\n" \
"v_exp_f32_e64 v82, v82\n" \
M_"v[108:111], a[156:159], a[28:31], v[108:111]\n" \
"v_exp_f32_e64 v83, v83\n" \
M_"v[104:107], a[140:143], a[28:31], v[36:39]\n" \
"v_exp_f32_e64 v80, v80\n" \
M_"v[104:107], a[136:139], a[24:27], v[104:107]\n" \
"v_exp_f32_e64 v81, v81\n" \
M_"v[104:107], a[132:135], a[20:23], v[104:107]\n" \
"v_exp_f32_e64 v88, v88\n" \
M_"v[104:107], a[128:131], a[16:19], v[104:107]\n" \
B_"v50, s[8:11], s64 offen offset:1024 sc1 lds\n" \
M_"v[112:115], a[128:131], a[32:35], v[40:43]\n" \
"s_xor_b32 m0, s65, 0x6000\n" \
C_"v86, v84, v85\n" \
M_"v[112:115], a[132:135], a[36:39], v[112:115]\n" \
C_"v85, v82, v83\n" \
C_"v84, v80, v81\n" \
M_"v[112:115], a[136:139], a[40:43], v[112:115]\n" \
"v_exp_f32_e64 v89, v89\n" \
M_"v[112:115], a[140:143], a[44:47], v[112:115]\n" \
"v_exp_f32_e64 v90, v90\n" \
M_"v[116:119], a[156:159], a[44:47], v[40:43]\n" \
"v_exp_f32_e64 v91, v91\n" \
M_"v[116:119], a[152:155], a[40:43], v[116:119]\n" \
"v_exp_f32_e64 v92, v92\n" \
M_"v[116:119], a[148:151], a[36:39], v[116:119]\n" \
"v_exp_f32_e64 v93, v93\n" \
M_"v[116:119], a[144:147], a[32:35], v[116:119]\n" \
"v_exp_f32_e64 v94, v94\n" \
M_"v[124:127], a[144:147], a[48:51], v[44:47]\n" \
C_"v88, v88, v89\n" \
C_"v89, v90, v91\n" \
M_"v[124:127], a[148:151], a[52:55], v[124:127]\n" \
"v_exp_f32_e64 v95, v95\n" \
M_"v[124:127], a[152:155], a[56:59], v[124:127]\n" \
C_"v90, v92, v93\n" \
C_"v91, v94, v95\n" \
M_"v[124:127], a[156:159], a[60:63], v[124:127]\n" \
R_"a[144:147], v48 offset:4096\n" \
R_"a[148:151], v48 offset:5120\n" \
M_"v[120:123], a[140:143], a[60:63], v[44:47]\n" \
R_"a[152:155], v48 offset:6144\n" \
R_"a[140:143], v48 offset:3072\n" \
M_"v[120:123], a[136:139], a[56:59], v[120:123]\n" \
R_"a[136:139], v48 offset:2048\n" \
R_"a[156:159], v48 offset:7168\n" \
M_"v[120:123], a[132:135], a[52:55], v[120:123]\n" \
B_"v51, s[8:11], s64 offen offset:0 sc1 lds\n" \
M_"v[120:123], a[128:131], a[48:51], v[120:123]\n" \
R_"a[128:131], v48 offset:0\n" \
"s_waitcnt lgkmcnt(7)\n" \
M_"v[128:131], a[160:163], v[68:71], v[128:131]\n" \
E_"v102, v102\n" \
M_"v[160:163], a[160:163], v[72:75], v[160:163]\n" \
"v_exp_f32_e64 v103, v103\n" \
M_"v[132:135], a[164:167], v[68:71], v[132:135]\n" \
R_"a[132:135], v48 offset:1024\n" \
C_"v103, v102, v103\n" \
M_"v[164:167], a[164:167], v[72:75], v[164:167]\n" \
"v_exp_f32_e64 v100, v100\n" \
M_"v[136:139], a[168:171], v[68:71], v[136:139]\n" \
"v_exp_f32_e64 v101, v101\n" \
M_"v[168:171], a[168:171], v[72:75], v[168:171]\n" \
"v_exp_f32_e64 v98, v98\n" \
M_"v[140:143], a[172:175], v[68:71], v[140:143]\n" \
"v_exp_f32_e64 v99, v99\n" \
M_"v[172:175], a[172:175], v[72:75], v[172:175]\n" \
"v_exp_f32_e64 v96, v96\n" \
M_"v[144:147], a[176:179], v[68:71], v[144:147]\n" \
"v_exp_f32_e64 v97, v97\n" \
M_"v[176:179], a[176:179], v[72:75], v[176:179]\n" \
"v_exp_f32_e64 v104, v104\n" \
M_"v[148:151], a[180:183], v[68:71], v[148:151]\n" \
"v_exp_f32_e64 v105, v105\n" \
M_"v[180:183], a[180:183], v[72:75], v[180:183]\n" \
C_"v102, v100, v101\n" \
C_"v101, v98, v99\n" \
M_"v[152:155], a[184:187], v[68:71], v[152:155]\n" \
C_"v100, v96, v97\n" \
C_"v104, v104, v105\n" \
M_"v[184:187], a[184:187], v[72:75], v[184:187]\n" \
B_"v51, s[8:11], s64 offen offset:1024 sc1 lds\n" \
M_"v[156:159], a[188:191], v[68:71], v[156:159]\n" \
"v_exp_f32_e64 v106, v106\n" \
M_"v[188:191], a[188:191], v[72:75], v[188:191]\n" \
"v_exp_f32_e64 v107, v107\n" \
"v_smfmac_f32_16x16x64_bf16 a[96:99], a[104:107], v[68:75], v21\n" \
"v_exp_f32_e64 v108, v108\n" \
M_"v[220:223], a[188:191], v[84:87], v[220:223]\n" \
"v_exp_f32_e64 v109, v109\n" \
M_"v[252:255], a[188:191], v[88:91], v[252:255]\n" \
"v_exp_f32_e64 v110, v110\n" \
M_"v[216:219], a[184:187], v[84:87], v[216:219]\n" \
"v_exp_f32_e64 v111, v111\n" \
M_"v[248:251], a[184:187], v[88:91], v[248:251]\n" \
"v_exp_f32_e64 v118, v118\n" \
M_"v[212:215], a[180:183], v[84:87], v[212:215]\n" \
"v_exp_f32_e64 v119, v119\n" \
M_"v[244:247], a[180:183], v[88:91], v[244:247]\n" \
C_"v105, v106, v107\n" \
C_"v106, v108, v109\n" \
M_"v[208:211], a[176:179], v[84:87], v[208:211]\n" \
C_"v107, v110, v111\n" \
C_"v119, v118, v119\n" \
M_"v[240:243], a[176:179], v[88:91], v[240:243]\n" \
"v_exp_f32_e64 v116, v116\n" \
M_"v[204:207], a[172:175], v[84:87], v[204:207]\n" \
"v_exp_f32_e64 v117, v117\n" \
M_"v[236:239], a[172:175], v[88:91], v[236:239]\n" \
"v_exp_f32_e64 v114, v114\n" \
M_"v[200:203], a[168:171], v[84:87], v[200:203]\n" \
"v_exp_f32_e64 v115, v115\n" \
M_"v[232:235], a[168:171], v[88:91], v[232:235]\n" \
"v_exp_f32_e64 v112, v112\n" \
M_"v[196:199], a[164:167], v[84:87], v[196:199]\n" \
"v_exp_f32_e64 v113, v113\n" \
M_"v[228:231], a[164:167], v[88:91], v[228:231]\n" \
"v_exp_f32_e64 v120, v120\n" \
M_"v[192:195], a[160:163], v[84:87], v[192:195]\n" \
"v_exp_f32_e64 v121, v121\n" \
M_"v[224:227], a[160:163], v[88:91], v[224:227]\n" \
C_"v118, v116, v117\n" \
"s_waitcnt vmcnt(4) lgkmcnt(0)\n" \
"v_smfmac_f32_16x16x64_bf16 a[100:103], a[104:107], v[84:91], v21\n" \
"s_barrier\n" \
M_"v[64:67], a[128:131], a[0:3], v[32:35]\n" \
"s_xor_b32 m0, s65, 0x14000\n" \
R_"a[160:163], v49 offset:8192\n" \
M_"v[64:67], a[132:135], a[4:7], v[64:67]\n" \
B_"v52, s[8:11], s64 offen offset:0 sc1 lds\n" \
M_"v[64:67], a[136:139], a[8:11], v[64:67]\n" \
R_"a[164:167], v49 offset:9216\n" \
R_"a[168:171], v49 offset:10240\n" \
M_"v[64:67], a[140:143], a[12:15], v[64:67]\n" \
R_"a[172:175], v49 offset:11264\n" \
R_"a[176:179], v49 offset:12288\n" \
M_"v[68:71], a[156:159], a[12:15], v[32:35]\n" \
R_"a[180:183], v49 offset:13312\n" \
R_"a[184:187], v49 offset:14336\n" \
M_"v[68:71], a[152:155], a[8:11], v[68:71]\n" \
R_"a[188:191], v49 offset:15360\n" \
C_"v117, v114, v115\n" \
M_"v[68:71], a[148:151], a[4:7], v[68:71]\n" \
C_"v116, v112, v113\n" \
C_"v120, v120, v121\n" \
M_"v[68:71], a[144:147], a[0:3], v[68:71]\n" \
"v_exp_f32_e64 v122, v122\n" \
M_"v[76:79], a[144:147], a[16:19], v[36:39]\n" \
"v_exp_f32_e64 v123, v123\n" \
M_"v[76:79], a[148:151], a[20:23], v[76:79]\n" \
"v_exp_f32_e64 v124, v124\n" \
M_"v[76:79], a[152:155], a[24:27], v[76:79]\n" \
"v_exp_f32_e64 v125, v125\n" \
M_"v[76:79], a[156:159], a[28:31], v[76:79]\n" \
"v_exp_f32_e64 v126, v126\n" \
M_"v[72:75], a[140:143], a[28:31], v[36:39]\n" \
"v_exp_f32_e64 v127, v127\n" \
M_"v[72:75], a[136:139], a[24:27], v[72:75]\n" \
C_"v121, v122, v123\n" \
C_"v122, v124, v125\n" \
M_"v[72:75], a[132:135], a[20:23], v[72:75]\n" \
C_"v123, v126, v127\n" \
"v_add_u32_e32 v49, 0x10000, v48\n" \
M_"v[72:75], a[128:131], a[16:19], v[72:75]\n" \
B_"v52, s[8:11], s64 offen offset:1024 sc1 lds\n" \
M_"v[80:83], a[128:131], a[32:35], v[40:43]\n" \
"v_exp_f32_e64 v70, v70\n" \
M_"v[80:83], a[132:135], a[36:39], v[80:83]\n" \
"v_exp_f32_e64 v71, v71\n" \
M_"v[80:83], a[136:139], a[40:43], v[80:83]\n" \
"s_mov_b32 m0, s65\n" \
C_"v71, v70, v71\n" \
M_"v[80:83], a[140:143], a[44:47], v[80:83]\n" \
E_"v72, v72\n" \
M_"v[84:87], a[156:159], a[44:47], v[40:43]\n" \
"v_exp_f32_e64 v73, v73\n" \
M_"v[84:87], a[152:155], a[40:43], v[84:87]\n" \
"v_exp_f32_e64 v68, v68\n" \
M_"v[84:87], a[148:151], a[36:39], v[84:87]\n" \
"v_exp_f32_e64 v69, v69\n" \
M_"v[84:87], a[144:147], a[32:35], v[84:87]\n" \
"v_exp_f32_e64 v74, v74\n" \
M_"v[92:95], a[144:147], a[48:51], v[44:47]\n" \
R_"a[144:147], v48 offset:12288\n" \
C_"v72, v72, v73\n" \
M_"v[92:95], a[148:151], a[52:55], v[92:95]\n" \
R_"a[148:151], v48 offset:13312\n" \
C_"v70, v68, v69\n" \
M_"v[92:95], a[152:155], a[56:59], v[92:95]\n" \
R_"a[152:155], v48 offset:14336\n" \
"s_xor_b32 s65, s65, 0x4000\n" \
M_"v[92:95], a[156:159], a[60:63], v[92:95]\n" \
"v_exp_f32_e64 v75, v75\n" \
M_"v[88:91], a[140:143], a[60:63], v[44:47]\n" \
R_"a[140:143], v48 offset:11264\n" \
R_"a[156:159], v48 offset:15360\n" \
M_"v[88:91], a[136:139], a[56:59], v[88:91]\n" \
R_"a[136:139], v48 offset:10240\n" \
C_"v73, v74, v75\n" \
M_"v[88:91], a[132:135], a[52:55], v[88:91]\n" \
B_"v53, s[8:11], s64 offen offset:0 sc1 lds\n" \
M_"v[88:91], a[128:131], a[48:51], v[88:91]\n" \
R_"a[128:131], v48 offset:8192\n" \
"s_waitcnt lgkmcnt(7)\n" \
M_"v[128:131], a[160:163], v[100:103], v[128:131]\n" \
E_"v66, v66\n" \
M_"v[160:163], a[160:163], v[104:107], v[160:163]\n" \
"v_exp_f32_e64 v67, v67\n" \
M_"v[132:135], a[164:167], v[100:103], v[132:135]\n" \
R_"a[132:135], v48 offset:9216\n" \
C_"v69, v66, v67\n" \
M_"v[164:167], a[164:167], v[104:107], v[164:167]\n" \
"v_exp_f32_e64 v64, v64\n" \
M_"v[136:139], a[168:171], v[100:103], v[136:139]\n" \
"v_exp_f32_e64 v65, v65\n" \
M_"v[168:171], a[168:171], v[104:107], v[168:171]\n" \
"v_exp_f32_e64 v76, v76\n" \
M_"v[140:143], a[172:175], v[100:103], v[140:143]\n" \
"v_exp_f32_e64 v77, v77\n" \
M_"v[172:175], a[172:175], v[104:107], v[172:175]\n" \
"v_exp_f32_e64 v78, v78\n" \
M_"v[144:147], a[176:179], v[100:103], v[144:147]\n" \
"v_exp_f32_e64 v79, v79\n" \
M_"v[176:179], a[176:179], v[104:107], v[176:179]\n" \
C_"v68, v64, v65\n" \
C_"v74, v76, v77\n" \
M_"v[148:151], a[180:183], v[100:103], v[148:151]\n" \
C_"v75, v78, v79\n" \
"v_xor_b32_e32 v48, 0x4000, v48\n" \
M_"v[180:183], a[180:183], v[104:107], v[180:183]\n" \
M_"v[152:155], a[184:187], v[100:103], v[152:155]\n" \
M_"v[184:187], a[184:187], v[104:107], v[184:187]\n" \
B_"v53, s[8:11], s64 offen offset:1024 sc1 lds\n" \
M_"v[156:159], a[188:191], v[100:103], v[156:159]\n" \
"s_add_u32 s64, s64, 0x8000\n" \
"s_sub_u32 s66, s66, 1\n" \
"s_nop 0\n" \
M_"v[188:191], a[188:191], v[104:107], v[188:191]\n" \
"v_smfmac_f32_16x16x64_bf16 a[96:99], a[104:107], v[100:107], v21\n" \
M_"v[220:223], a[188:191], v[116:119], v[220:223]\n" \
M_"v[252:255], a[188:191], v[120:123], v[252:255]\n" \
M_"v[216:219], a[184:187], v[116:119], v[216:219]\n" \
M_"v[248:251], a[184:187], v[120:123], v[248:251]\n" \
M_"v[212:215], a[180:183], v[116:119], v[212:215]\n" \
M_"v[244:247], a[180:183], v[120:123], v[244:247]\n" \
M_"v[208:211], a[176:179], v[116:119], v[208:211]\n" \
M_"v[240:243], a[176:179], v[120:123], v[240:243]\n" \
M_"v[204:207], a[172:175], v[116:119], v[204:207]\n" \
M_"v[236:239], a[172:175], v[120:123], v[236:239]\n" \
M_"v[200:203], a[168:171], v[116:119], v[200:203]\n" \
M_"v[232:235], a[168:171], v[120:123], v[232:235]\n" \
M_"v[196:199], a[164:167], v[116:119], v[196:199]\n" \
M_"v[228:231], a[164:167], v[120:123], v[228:231]\n" \
M_"v[192:195], a[160:163], v[116:119], v[192:195]\n" \
M_"v[224:227], a[160:163], v[120:123], v[224:227]\n" \
"s_cbranch_scc0 L_tL_1_%=\n" \
"s_waitcnt vmcnt(4) lgkmcnt(0)\n" \
"v_smfmac_f32_16x16x64_bf16 a[100:103], a[104:107], v[116:123], v21\n" \
"s_barrier\n" \
"L_tr_%=:\n" \
"s_setpc_b64 s[28:29]\n" \
".fill (L_ab + 4096 - .) / 4, 4, 0xBF800000\n" \
".fill (L_ab + 4096 - .) / 4, 4, 0xBF800000\n" \
"L_ap1:\n" \
"s_setpc_b64 s[88:89]\n" \
"L_gtr:\n" \
M_"v[96:99], a[128:131], a[0:3], v[32:35]\n" \
"s_xor_b32 m0, s65, 0x12000\n" \
"s_mov_b32 s65, s13\n" \
M_"v[96:99], a[132:135], a[4:7], v[96:99]\n" \
B_"v50, s[8:11], s64 offen offset:0 sc1 lds\n" \
M_"v[96:99], a[136:139], a[8:11], v[96:99]\n" \
"v_add_u32_e32 v63, s15, v20\n" \
"s_add_u32 s67, s14, 40960\n" \
M_"v[96:99], a[140:143], a[12:15], v[96:99]\n" \
R_"a[160:163], v49 offset:0\n" \
R_"a[164:167], v49 offset:1024\n" \
M_"v[100:103], a[156:159], a[12:15], v[32:35]\n" \
B_"v50, s[8:11], s64 offen offset:1024 sc1 lds\n" \
M_"v[100:103], a[152:155], a[8:11], v[100:103]\n" \
"v_add_u32_e32 v50, 0x6000, v63\n" \
"s_xor_b32 m0, s65, 0x8000\n" \
M_"v[100:103], a[148:151], a[4:7], v[100:103]\n" \
R_"a[168:171], v49 offset:2048\n" \
R_"a[172:175], v49 offset:3072\n" \
M_"v[100:103], a[144:147], a[0:3], v[100:103]\n" \
R_"a[176:179], v49 offset:4096\n" \
R_"a[180:183], v49 offset:5120\n" \
M_"v[108:111], a[144:147], a[16:19], v[36:39]\n" \
B_"v50, s[8:11], s67 offen offset:0 sc1 lds\n" \
M_"v[108:111], a[148:151], a[20:23], v[108:111]\n" \
R_"a[184:187], v49 offset:6144\n" \
R_"a[188:191], v49 offset:7168\n" \
M_"v[108:111], a[152:155], a[24:27], v[108:111]\n" \
"s_mov_b32 s64, s14\n" \
"v_add_u32_e32 v51, 0x12000, v63\n" \
M_"v[108:111], a[156:159], a[28:31], v[108:111]\n" \
E_"v86, v86\n" \
M_"v[104:107], a[140:143], a[28:31], v[36:39]\n" \
B_"v50, s[8:11], s67 offen offset:1024 sc1 lds\n" \
M_"v[104:107], a[136:139], a[24:27], v[104:107]\n" \
E_"v87, v87\n" \
M_"v[104:107], a[132:135], a[20:23], v[104:107]\n" \
E_"v84, v84\n" \
M_"v[104:107], a[128:131], a[16:19], v[104:107]\n" \
E_"v85, v85\n" \
M_"v[112:115], a[128:131], a[32:35], v[40:43]\n" \
C_"v87, v86, v87\n" \
C_"v86, v84, v85\n" \
M_"v[112:115], a[132:135], a[36:39], v[112:115]\n" \
B_"v50, s[8:11], s67 offen offset:2048 sc1 lds\n" \
M_"v[112:115], a[136:139], a[40:43], v[112:115]\n" \
E_"v82, v82\n" \
M_"v[112:115], a[140:143], a[44:47], v[112:115]\n" \
E_"v83, v83\n" \
M_"v[116:119], a[156:159], a[44:47], v[40:43]\n" \
E_"v80, v80\n" \
M_"v[116:119], a[152:155], a[40:43], v[116:119]\n" \
B_"v50, s[8:11], s67 offen offset:3072 sc1 lds\n" \
M_"v[116:119], a[148:151], a[36:39], v[116:119]\n" \
"s_xor_b32 m0, s65, 0xa000\n" \
C_"v85, v82, v83\n" \
M_"v[116:119], a[144:147], a[32:35], v[116:119]\n" \
E_"v81, v81\n" \
M_"v[124:127], a[144:147], a[48:51], v[44:47]\n" \
C_"v84, v80, v81\n" \
"v_readlane_b32 s93, v11, 22\n" \
M_"v[124:127], a[148:151], a[52:55], v[124:127]\n" \
B_"v51, s[8:11], s64 offen offset:0 sc1 lds\n" \
M_"v[124:127], a[152:155], a[56:59], v[124:127]\n" \
E_"v88, v88\n" \
M_"v[124:127], a[156:159], a[60:63], v[124:127]\n" \
E_"v89, v89\n" \
M_"v[120:123], a[140:143], a[60:63], v[44:47]\n" \
E_"v90, v90\n" \
M_"v[120:123], a[136:139], a[56:59], v[120:123]\n" \
E_"v91, v91\n" \
M_"v[120:123], a[132:135], a[52:55], v[120:123]\n" \
B_"v51, s[8:11], s64 offen offset:1024 sc1 lds\n" \
M_"v[120:123], a[128:131], a[48:51], v[120:123]\n" \
C_"v88, v88, v89\n" \
"s_waitcnt lgkmcnt(0)\n" \
M_"v[128:131], a[160:163], v[68:71], v[128:131]\n" \
E_"v92, v92\n" \
M_"v[160:163], a[160:163], v[72:75], v[160:163]\n" \
E_"v93, v93\n" \
M_"v[132:135], a[164:167], v[68:71], v[132:135]\n" \
B_"v51, s[8:11], s64 offen offset:2048 sc1 lds\n" \
M_"v[164:167], a[164:167], v[72:75], v[164:167]\n" \
E_"v94, v94\n" \
M_"v[136:139], a[168:171], v[68:71], v[136:139]\n" \
E_"v95, v95\n" \
M_"v[168:171], a[168:171], v[72:75], v[168:171]\n" \
C_"v89, v90, v91\n" \
C_"v90, v92, v93\n" \
M_"v[140:143], a[172:175], v[68:71], v[140:143]\n" \
B_"v51, s[8:11], s64 offen offset:3072 sc1 lds\n" \
M_"v[172:175], a[172:175], v[72:75], v[172:175]\n" \
C_"v91, v94, v95\n" \
"v_readlane_b32 s80, v11, 11\n" \
M_"v[144:147], a[176:179], v[68:71], v[144:147]\n" \
"v_readlane_b32 s81, v11, 12\n" \
"s_mov_b32 s82, 0x47fca0\n" \
M_"v[176:179], a[176:179], v[72:75], v[176:179]\n" \
"s_mov_b32 s83, 0x20000\n" \
"v_readlane_b32 s92, v11, 29\n" \
M_"v[148:151], a[180:183], v[68:71], v[148:151]\n" \
"s_add_u32 m0, s93, 0x0\n" \
R_"a[128:131], v48 offset:0\n" \
M_"v[180:183], a[180:183], v[72:75], v[180:183]\n" \
R_"a[144:147], v48 offset:4096\n" \
R_"a[132:135], v48 offset:1024\n" \
M_"v[152:155], a[184:187], v[68:71], v[152:155]\n" \
B_"v0, s[80:83], s92 offen offset:0 lds\n" \
M_"v[184:187], a[184:187], v[72:75], v[184:187]\n" \
R_"a[148:151], v48 offset:5120\n" \
R_"a[136:139], v48 offset:2048\n" \
M_"v[156:159], a[188:191], v[68:71], v[156:159]\n" \
R_"a[152:155], v48 offset:6144\n" \
R_"a[140:143], v48 offset:3072\n" \
M_"v[188:191], a[188:191], v[72:75], v[188:191]\n" \
R_"a[156:159], v48 offset:7168\n" \
"v_add_u32_e32 v52, 0x14000, v63\n" \
"v_smfmac_f32_16x16x64_bf16 a[96:99], a[104:107], v[68:75], v21\n" \
B_"v1, s[80:83], s92 offen offset:1024 lds\n" \
M_"v[220:223], a[188:191], v[84:87], v[220:223]\n" \
E_"v102, v102\n" \
M_"v[252:255], a[188:191], v[88:91], v[252:255]\n" \
E_"v103, v103\n" \
M_"v[216:219], a[184:187], v[84:87], v[216:219]\n" \
B_"v2, s[80:83], s92 offen offset:2048 lds\n" \
M_"v[248:251], a[184:187], v[88:91], v[248:251]\n" \
"v_add_u32_e32 v53, 0x20000, v63\n" \
C_"v103, v102, v103\n" \
M_"v[212:215], a[180:183], v[84:87], v[212:215]\n" \
E_"v100, v100\n" \
M_"v[244:247], a[180:183], v[88:91], v[244:247]\n" \
E_"v101, v101\n" \
M_"v[208:211], a[176:179], v[84:87], v[208:211]\n" \
B_"v3, s[80:83], s92 offen offset:3072 lds\n" \
M_"v[240:243], a[176:179], v[88:91], v[240:243]\n" \
E_"v98, v98\n" \
M_"v[204:207], a[172:175], v[84:87], v[204:207]\n" \
E_"v99, v99\n" \
M_"v[236:239], a[172:175], v[88:91], v[236:239]\n" \
E_"v96, v96\n" \
M_"v[200:203], a[168:171], v[84:87], v[200:203]\n" \
E_"v97, v97\n" \
M_"v[232:235], a[168:171], v[88:91], v[232:235]\n" \
E_"v104, v104\n" \
M_"v[196:199], a[164:167], v[84:87], v[196:199]\n" \
E_"v105, v105\n" \
M_"v[228:231], a[164:167], v[88:91], v[228:231]\n" \
C_"v102, v100, v101\n" \
C_"v101, v98, v99\n" \
M_"v[192:195], a[160:163], v[84:87], v[192:195]\n" \
C_"v100, v96, v97\n" \
C_"v104, v104, v105\n" \
M_"v[224:227], a[160:163], v[88:91], v[224:227]\n" \
E_"v106, v106\n" \
"v_smfmac_f32_16x16x64_bf16 a[100:103], a[104:107], v[84:91], v21\n" \
E_"v107, v107\n" \
"s_waitcnt vmcnt(14) lgkmcnt(0)\n" \
"s_barrier\n" \
M_"v[64:67], a[128:131], a[0:3], v[32:35]\n" \
"s_xor_b32 m0, s65, 0x18000\n" \
R_"a[160:163], v49 offset:8192\n" \
M_"v[64:67], a[132:135], a[4:7], v[64:67]\n" \
B_"v52, s[8:11], s64 offen offset:0 sc1 lds\n" \
M_"v[64:67], a[136:139], a[8:11], v[64:67]\n" \
R_"a[164:167], v49 offset:9216\n" \
R_"a[168:171], v49 offset:10240\n" \
M_"v[64:67], a[140:143], a[12:15], v[64:67]\n" \
R_"a[172:175], v49 offset:11264\n" \
R_"a[176:179], v49 offset:12288\n" \
M_"v[68:71], a[156:159], a[12:15], v[32:35]\n" \
B_"v52, s[8:11], s64 offen offset:1024 sc1 lds\n" \
M_"v[68:71], a[152:155], a[8:11], v[68:71]\n" \
R_"a[180:183], v49 offset:13312\n" \
R_"a[184:187], v49 offset:14336\n" \
M_"v[68:71], a[148:151], a[4:7], v[68:71]\n" \
E_"v108, v108\n" \
M_"v[68:71], a[144:147], a[0:3], v[68:71]\n" \
E_"v109, v109\n" \
M_"v[76:79], a[144:147], a[16:19], v[36:39]\n" \
E_"v110, v110\n" \
M_"v[76:79], a[148:151], a[20:23], v[76:79]\n" \
B_"v52, s[8:11], s64 offen offset:2048 sc1 lds\n" \
M_"v[76:79], a[152:155], a[24:27], v[76:79]\n" \
E_"v111, v111\n" \
M_"v[76:79], a[156:159], a[28:31], v[76:79]\n" \
C_"v105, v106, v107\n" \
C_"v106, v108, v109\n" \
M_"v[72:75], a[140:143], a[28:31], v[36:39]\n" \
C_"v107, v110, v111\n" \
R_"a[188:191], v49 offset:15360\n" \
M_"v[72:75], a[136:139], a[24:27], v[72:75]\n" \
B_"v52, s[8:11], s64 offen offset:3072 sc1 lds\n" \
M_"v[72:75], a[132:135], a[20:23], v[72:75]\n" \
"s_mov_b32 m0, s65\n" \
"v_add_u32_e32 v49, 0x10000, v48\n" \
M_"v[72:75], a[128:131], a[16:19], v[72:75]\n" \
E_"v118, v118\n" \
M_"v[80:83], a[128:131], a[32:35], v[40:43]\n" \
E_"v119, v119\n" \
M_"v[80:83], a[132:135], a[36:39], v[80:83]\n" \
E_"v116, v116\n" \
M_"v[80:83], a[136:139], a[40:43], v[80:83]\n" \
B_"v53, s[8:11], s64 offen offset:0 sc1 lds\n" \
M_"v[80:83], a[140:143], a[44:47], v[80:83]\n" \
E_"v117, v117\n" \
M_"v[84:87], a[156:159], a[44:47], v[40:43]\n" \
C_"v119, v118, v119\n" \
C_"v118, v116, v117\n" \
M_"v[84:87], a[152:155], a[40:43], v[84:87]\n" \
E_"v114, v114\n" \
M_"v[84:87], a[148:151], a[36:39], v[84:87]\n" \
E_"v115, v115\n" \
M_"v[84:87], a[144:147], a[32:35], v[84:87]\n" \
B_"v53, s[8:11], s64 offen offset:1024 sc1 lds\n" \
M_"v[92:95], a[144:147], a[48:51], v[44:47]\n" \
E_"v112, v112\n" \
M_"v[92:95], a[148:151], a[52:55], v[92:95]\n" \
E_"v113, v113\n" \
M_"v[92:95], a[152:155], a[56:59], v[92:95]\n" \
C_"v117, v114, v115\n" \
C_"v116, v112, v113\n" \
M_"v[92:95], a[156:159], a[60:63], v[92:95]\n" \
B_"v53, s[8:11], s64 offen offset:2048 sc1 lds\n" \
M_"v[88:91], a[140:143], a[60:63], v[44:47]\n" \
E_"v120, v120\n" \
M_"v[88:91], a[136:139], a[56:59], v[88:91]\n" \
E_"v121, v121\n" \
M_"v[88:91], a[132:135], a[52:55], v[88:91]\n" \
E_"v122, v122\n" \
M_"v[88:91], a[128:131], a[48:51], v[88:91]\n" \
C_"v120, v120, v121\n" \
"s_waitcnt lgkmcnt(0)\n" \
M_"v[128:131], a[160:163], v[100:103], v[128:131]\n" \
B_"v53, s[8:11], s64 offen offset:3072 sc1 lds\n" \
M_"v[160:163], a[160:163], v[104:107], v[160:163]\n" \
"s_add_u32 m0, s93, 0x1000\n" \
R_"a[128:131], v48 offset:8192\n" \
M_"v[132:135], a[164:167], v[100:103], v[132:135]\n" \
E_"v123, v123\n" \
M_"v[164:167], a[164:167], v[104:107], v[164:167]\n" \
E_"v124, v124\n" \
M_"v[136:139], a[168:171], v[100:103], v[136:139]\n" \
B_"v4, s[80:83], s92 offen offset:0 lds\n" \
M_"v[168:171], a[168:171], v[104:107], v[168:171]\n" \
E_"v125, v125\n" \
M_"v[140:143], a[172:175], v[100:103], v[140:143]\n" \
E_"v126, v126\n" \
M_"v[172:175], a[172:175], v[104:107], v[172:175]\n" \
E_"v127, v127\n" \
M_"v[144:147], a[176:179], v[100:103], v[144:147]\n" \
C_"v121, v122, v123\n" \
C_"v122, v124, v125\n" \
M_"v[176:179], a[176:179], v[104:107], v[176:179]\n" \
B_"v5, s[80:83], s92 offen offset:1024 lds\n" \
M_"v[148:151], a[180:183], v[100:103], v[148:151]\n" \
C_"v123, v126, v127\n" \
R_"a[144:147], v48 offset:12288\n" \
M_"v[180:183], a[180:183], v[104:107], v[180:183]\n" \
R_"a[132:135], v48 offset:9216\n" \
R_"a[148:151], v48 offset:13312\n" \
M_"v[152:155], a[184:187], v[100:103], v[152:155]\n" \
R_"a[136:139], v48 offset:10240\n" \
R_"a[152:155], v48 offset:14336\n" \
M_"v[184:187], a[184:187], v[104:107], v[184:187]\n" \
R_"a[140:143], v48 offset:11264\n" \
R_"a[156:159], v48 offset:15360\n" \
M_"v[156:159], a[188:191], v[100:103], v[156:159]\n" \
B_"v6, s[80:83], s92 offen offset:2048 lds\n" \
M_"v[188:191], a[188:191], v[104:107], v[188:191]\n" \
E_"v70, v70\n" \
"v_smfmac_f32_16x16x64_bf16 a[96:99], a[104:107], v[100:107], v21\n" \
E_"v71, v71\n" \
M_"v[220:223], a[188:191], v[116:119], v[220:223]\n" \
E_"v72, v72\n" \
M_"v[252:255], a[188:191], v[120:123], v[252:255]\n" \
B_"v7, s[80:83], s92 offen offset:3072 lds\n" \
M_"v[216:219], a[184:187], v[116:119], v[216:219]\n" \
"s_add_u32 m0, s93, 0x2000\n" \
C_"v71, v70, v71\n" \
M_"v[248:251], a[184:187], v[120:123], v[248:251]\n" \
E_"v73, v73\n" \
M_"v[212:215], a[180:183], v[116:119], v[212:215]\n" \
E_"v68, v68\n" \
M_"v[244:247], a[180:183], v[120:123], v[244:247]\n" \
E_"v69, v69\n" \
M_"v[208:211], a[176:179], v[116:119], v[208:211]\n" \
B_"v8, s[80:83], s92 offen offset:0 lds\n" \
M_"v[240:243], a[176:179], v[120:123], v[240:243]\n" \
E_"v74, v74\n" \
M_"v[204:207], a[172:175], v[116:119], v[204:207]\n" \
E_"v75, v75\n" \
M_"v[236:239], a[172:175], v[120:123], v[236:239]\n" \
E_"v66, v66\n" \
M_"v[200:203], a[168:171], v[116:119], v[200:203]\n" \
E_"v67, v67\n" \
M_"v[232:235], a[168:171], v[120:123], v[232:235]\n" \
E_"v64, v64\n" \
M_"v[196:199], a[164:167], v[116:119], v[196:199]\n" \
E_"v65, v65\n" \
M_"v[228:231], a[164:167], v[120:123], v[228:231]\n" \
C_"v72, v72, v73\n" \
C_"v70, v68, v69\n" \
M_"v[192:195], a[160:163], v[116:119], v[192:195]\n" \
E_"v76, v76\n" \
M_"v[224:227], a[160:163], v[120:123], v[224:227]\n" \
E_"v77, v77\n" \
"v_smfmac_f32_16x16x64_bf16 a[100:103], a[104:107], v[116:123], v21\n" \
E_"v78, v78\n" \
"s_waitcnt vmcnt(13) lgkmcnt(0)\n" \
"s_barrier\n" \
E_"v79, v79\n" \
C_"v73, v74, v75\n" \
C_"v69, v66, v67\n" \
C_"v68, v64, v65\n" \
C_"v74, v76, v77\n" \
C_"v75, v78, v79\n" \
"v_add_u32_e32 v48, s16, v20\n" \
"s_xor_b32 s65, s65, 0x8000\n" \
"s_add_u32 s64, s64, 0x10000\n" \
"s_getpc_b64 s[86:87]\n" \
"L_hpt_%=:\n" \
"s_add_u32 s86, s86, L_ap8-L_hpt_%=\n" \
"s_addc_u32 s87, s87, 0\n" \
"s_add_u32 s86, s86, s15\n" \
"s_addc_u32 s87, s87, 0\n" \
"s_mov_b64 exec, 0\n" \
"s_swappc_b64 s[88:89], s[86:87]\n" \
"s_mov_b64 exec, -1\n" \
"s_sub_u32 s66, s19, 1\n" \
"s_cbranch_scc1 L_la_%=\n" \
"s_branch L_2_%=\n" \
".fill (L_ab + 6400 - .) / 4, 4, 0xBF800000\n" \
"L_tL_2_%=:\n" \
"s_waitcnt vmcnt(8) lgkmcnt(0)\n" \
"v_smfmac_f32_16x16x64_bf16 a[100:103], a[104:107], v[116:123], v21\n" \
"s_barrier\n" \
"L_2_%=:\n" \
M_"v[96:99], a[128:131], a[64:67], v[32:35]\n" \
"s_xor_b32 m0, s65, 0x12000\n" \
R_"a[160:163], v49 offset:0\n" \
M_"v[96:99], a[132:135], a[68:71], v[96:99]\n" \
B_"v50, s[8:11], s64 offen offset:0 sc1 lds\n" \
M_"v[96:99], a[136:139], a[72:75], v[96:99]\n" \
R_"a[164:167], v49 offset:1024\n" \
R_"a[168:171], v49 offset:2048\n" \
M_"v[96:99], a[140:143], a[76:79], v[96:99]\n" \
R_"a[172:175], v49 offset:3072\n" \
R_"a[176:179], v49 offset:4096\n" \
M_"v[100:103], a[156:159], a[76:79], v[32:35]\n" \
R_"a[180:183], v49 offset:5120\n" \
R_"a[184:187], v49 offset:6144\n" \
M_"v[100:103], a[152:155], a[72:75], v[100:103]\n" \
"v_exp_f32_e64 v86, v86\n" \
M_"v[100:103], a[148:151], a[68:71], v[100:103]\n" \
"v_exp_f32_e64 v87, v87\n" \
M_"v[100:103], a[144:147], a[64:67], v[100:103]\n" \
B_"v50, s[8:11], s64 offen offset:1024 sc1 lds\n" \
M_"v[108:111], a[144:147], a[80:83], v[36:39]\n" \
R_"a[188:191], v49 offset:7168\n" \
C_"v87, v86, v87\n" \
M_"v[108:111], a[148:151], a[84:87], v[108:111]\n" \
"v_exp_f32_e64 v84, v84\n" \
M_"v[108:111], a[152:155], a[88:91], v[108:111]\n" \
"v_exp_f32_e64 v85, v85\n" \
M_"v[108:111], a[156:159], a[92:95], v[108:111]\n" \
"v_exp_f32_e64 v82, v82\n" \
M_"v[104:107], a[140:143], a[92:95], v[36:39]\n" \
"v_exp_f32_e64 v83, v83\n" \
M_"v[104:107], a[136:139], a[88:91], v[104:107]\n" \
"v_exp_f32_e64 v80, v80\n" \
M_"v[104:107], a[132:135], a[84:87], v[104:107]\n" \
"v_exp_f32_e64 v81, v81\n" \
M_"v[104:107], a[128:131], a[80:83], v[104:107]\n" \
B_"v50, s[8:11], s64 offen offset:2048 sc1 lds\n" \
M_"v[112:115], a[128:131], a[192:195], v[40:43]\n" \
"v_exp_f32_e64 v88, v88\n" \
M_"v[112:115], a[132:135], a[196:199], v[112:115]\n" \
"v_exp_f32_e64 v89, v89\n" \
M_"v[112:115], a[136:139], a[200:203], v[112:115]\n" \
C_"v86, v84, v85\n" \
C_"v85, v82, v83\n" \
M_"v[112:115], a[140:143], a[204:207], v[112:115]\n" \
C_"v84, v80, v81\n" \
C_"v88, v88, v89\n" \
M_"v[116:119], a[156:159], a[204:207], v[40:43]\n" \
"v_exp_f32_e64 v90, v90\n" \
M_"v[116:119], a[152:155], a[200:203], v[116:119]\n" \
"v_exp_f32_e64 v91, v91\n" \
M_"v[116:119], a[148:151], a[196:199], v[116:119]\n" \
B_"v50, s[8:11], s64 offen offset:3072 sc1 lds\n" \
M_"v[116:119], a[144:147], a[192:195], v[116:119]\n" \
"s_xor_b32 m0, s65, 0xa000\n" \
C_"v89, v90, v91\n" \
M_"v[124:127], a[144:147], a[208:211], v[44:47]\n" \
"v_exp_f32_e64 v92, v92\n" \
M_"v[124:127], a[148:151], a[212:215], v[124:127]\n" \
"v_exp_f32_e64 v93, v93\n" \
M_"v[124:127], a[152:155], a[216:219], v[124:127]\n" \
"v_exp_f32_e64 v94, v94\n" \
M_"v[124:127], a[156:159], a[220:223], v[124:127]\n" \
"v_exp_f32_e64 v95, v95\n" \
M_"v[120:123], a[140:143], a[220:223], v[44:47]\n" \
C_"v90, v92, v93\n" \
C_"v91, v94, v95\n" \
M_"v[120:123], a[136:139], a[216:219], v[120:123]\n" \
R_"a[144:147], v48 offset:4096\n" \
R_"a[148:151], v48 offset:5120\n" \
M_"v[120:123], a[132:135], a[212:215], v[120:123]\n" \
B_"v51, s[8:11], s64 offen offset:0 sc1 lds\n" \
M_"v[120:123], a[128:131], a[208:211], v[120:123]\n" \
R_"a[128:131], v48 offset:0\n" \
"s_waitcnt lgkmcnt(3)\n" \
M_"v[128:131], a[160:163], v[68:71], v[128:131]\n" \
R_"a[132:135], v48 offset:1024\n" \
R_"a[136:139], v48 offset:2048\n" \
M_"v[160:163], a[160:163], v[72:75], v[160:163]\n" \
R_"a[152:155], v48 offset:6144\n" \
R_"a[140:143], v48 offset:3072\n" \
M_"v[132:135], a[164:167], v[68:71], v[132:135]\n" \
E_"v102, v102\n" \
M_"v[164:167], a[164:167], v[72:75], v[164:167]\n" \
"v_exp_f32_e64 v103, v103\n" \
M_"v[136:139], a[168:171], v[68:71], v[136:139]\n" \
R_"a[156:159], v48 offset:7168\n" \
C_"v103, v102, v103\n" \
M_"v[168:171], a[168:171], v[72:75], v[168:171]\n" \
B_"v51, s[8:11], s64 offen offset:1024 sc1 lds\n" \
M_"v[140:143], a[172:175], v[68:71], v[140:143]\n" \
"v_exp_f32_e64 v100, v100\n" \
M_"v[172:175], a[172:175], v[72:75], v[172:175]\n" \
"v_exp_f32_e64 v101, v101\n" \
M_"v[144:147], a[176:179], v[68:71], v[144:147]\n" \
"v_exp_f32_e64 v98, v98\n" \
M_"v[176:179], a[176:179], v[72:75], v[176:179]\n" \
"v_exp_f32_e64 v99, v99\n" \
M_"v[148:151], a[180:183], v[68:71], v[148:151]\n" \
"v_exp_f32_e64 v96, v96\n" \
M_"v[180:183], a[180:183], v[72:75], v[180:183]\n" \
"v_exp_f32_e64 v97, v97\n" \
M_"v[152:155], a[184:187], v[68:71], v[152:155]\n" \
"v_exp_f32_e64 v104, v104\n" \
M_"v[184:187], a[184:187], v[72:75], v[184:187]\n" \
B_"v51, s[8:11], s64 offen offset:2048 sc1 lds\n" \
M_"v[156:159], a[188:191], v[68:71], v[156:159]\n" \
"v_exp_f32_e64 v105, v105\n" \
M_"v[188:191], a[188:191], v[72:75], v[188:191]\n" \
C_"v102, v100, v101\n" \
C_"v101, v98, v99\n" \
"v_smfmac_f32_16x16x64_bf16 a[96:99], a[104:107], v[68:75], v21\n" \
C_"v100, v96, v97\n" \
C_"v104, v104, v105\n" \
M_"v[220:223], a[188:191], v[84:87], v[220:223]\n" \
"v_exp_f32_e64 v106, v106\n" \
M_"v[252:255], a[188:191], v[88:91], v[252:255]\n" \
"v_exp_f32_e64 v107, v107\n" \
M_"v[216:219], a[184:187], v[84:87], v[216:219]\n" \
"v_exp_f32_e64 v108, v108\n" \
M_"v[248:251], a[184:187], v[88:91], v[248:251]\n" \
B_"v51, s[8:11], s64 offen offset:3072 sc1 lds\n" \
M_"v[212:215], a[180:183], v[84:87], v[212:215]\n" \
"v_exp_f32_e64 v109, v109\n" \
M_"v[244:247], a[180:183], v[88:91], v[244:247]\n" \
"v_exp_f32_e64 v110, v110\n" \
M_"v[208:211], a[176:179], v[84:87], v[208:211]\n" \
"v_exp_f32_e64 v111, v111\n" \
M_"v[240:243], a[176:179], v[88:91], v[240:243]\n" \
"v_exp_f32_e64 v118, v118\n" \
M_"v[204:207], a[172:175], v[84:87], v[204:207]\n" \
"v_exp_f32_e64 v119, v119\n" \
M_"v[236:239], a[172:175], v[88:91], v[236:239]\n" \
C_"v105, v106, v107\n" \
C_"v106, v108, v109\n" \
M_"v[200:203], a[168:171], v[84:87], v[200:203]\n" \
C_"v107, v110, v111\n" \
C_"v119, v118, v119\n" \
M_"v[232:235], a[168:171], v[88:91], v[232:235]\n" \
"v_exp_f32_e64 v116, v116\n" \
M_"v[196:199], a[164:167], v[84:87], v[196:199]\n" \
"v_exp_f32_e64 v117, v117\n" \
M_"v[228:231], a[164:167], v[88:91], v[228:231]\n" \
"v_exp_f32_e64 v114, v114\n" \
M_"v[192:195], a[160:163], v[84:87], v[192:195]\n" \
"v_exp_f32_e64 v115, v115\n" \
M_"v[224:227], a[160:163], v[88:91], v[224:227]\n" \
C_"v118, v116, v117\n" \
"s_waitcnt vmcnt(8) lgkmcnt(0)\n" \
"v_smfmac_f32_16x16x64_bf16 a[100:103], a[104:107], v[84:91], v21\n" \
"s_barrier\n" \
M_"v[64:67], a[128:131], a[64:67], v[32:35]\n" \
"s_xor_b32 m0, s65, 0x18000\n" \
R_"a[160:163], v49 offset:8192\n" \
M_"v[64:67], a[132:135], a[68:71], v[64:67]\n" \
B_"v52, s[8:11], s64 offen offset:0 sc1 lds\n" \
M_"v[64:67], a[136:139], a[72:75], v[64:67]\n" \
R_"a[164:167], v49 offset:9216\n" \
R_"a[168:171], v49 offset:10240\n" \
M_"v[64:67], a[140:143], a[76:79], v[64:67]\n" \
R_"a[172:175], v49 offset:11264\n" \
R_"a[176:179], v49 offset:12288\n" \
M_"v[68:71], a[156:159], a[76:79], v[32:35]\n" \
R_"a[180:183], v49 offset:13312\n" \
R_"a[184:187], v49 offset:14336\n" \
M_"v[68:71], a[152:155], a[72:75], v[68:71]\n" \
R_"a[188:191], v49 offset:15360\n" \
C_"v117, v114, v115\n" \
M_"v[68:71], a[148:151], a[68:71], v[68:71]\n" \
"v_exp_f32_e64 v112, v112\n" \
M_"v[68:71], a[144:147], a[64:67], v[68:71]\n" \
B_"v52, s[8:11], s64 offen offset:1024 sc1 lds\n" \
M_"v[76:79], a[144:147], a[80:83], v[36:39]\n" \
"v_exp_f32_e64 v113, v113\n" \
M_"v[76:79], a[148:151], a[84:87], v[76:79]\n" \
C_"v116, v112, v113\n" \
"v_add_u32_e32 v49, 0x10000, v48\n" \
M_"v[76:79], a[152:155], a[88:91], v[76:79]\n" \
"v_exp_f32_e64 v120, v120\n" \
M_"v[76:79], a[156:159], a[92:95], v[76:79]\n" \
"v_exp_f32_e64 v121, v121\n" \
M_"v[72:75], a[140:143], a[92:95], v[36:39]\n" \
"v_exp_f32_e64 v122, v122\n" \
M_"v[72:75], a[136:139], a[88:91], v[72:75]\n" \
"v_exp_f32_e64 v123, v123\n" \
M_"v[72:75], a[132:135], a[84:87], v[72:75]\n" \
"v_exp_f32_e64 v124, v124\n" \
M_"v[72:75], a[128:131], a[80:83], v[72:75]\n" \
B_"v52, s[8:11], s64 offen offset:2048 sc1 lds\n" \
M_"v[80:83], a[128:131], a[192:195], v[40:43]\n" \
"v_exp_f32_e64 v125, v125\n" \
M_"v[80:83], a[132:135], a[196:199], v[80:83]\n" \
"v_exp_f32_e64 v126, v126\n" \
M_"v[80:83], a[136:139], a[200:203], v[80:83]\n" \
"v_exp_f32_e64 v127, v127\n" \
M_"v[80:83], a[140:143], a[204:207], v[80:83]\n" \
"v_exp_f32_e64 v70, v70\n" \
M_"v[84:87], a[156:159], a[204:207], v[40:43]\n" \
"v_exp_f32_e64 v71, v71\n" \
M_"v[84:87], a[152:155], a[200:203], v[84:87]\n" \
C_"v120, v120, v121\n" \
C_"v121, v122, v123\n" \
M_"v[84:87], a[148:151], a[196:199], v[84:87]\n" \
B_"v52, s[8:11], s64 offen offset:3072 sc1 lds\n" \
M_"v[84:87], a[144:147], a[192:195], v[84:87]\n" \
"s_mov_b32 m0, s65\n" \
"s_nop 0\n" \
C_"v122, v124, v125\n" \
M_"v[92:95], a[144:147], a[208:211], v[44:47]\n" \
C_"v123, v126, v127\n" \
R_"a[144:147], v48 offset:12288\n" \
M_"v[92:95], a[148:151], a[212:215], v[92:95]\n" \
R_"a[148:151], v48 offset:13312\n" \
C_"v71, v70, v71\n" \
M_"v[92:95], a[152:155], a[216:219], v[92:95]\n" \
R_"a[152:155], v48 offset:14336\n" \
"s_xor_b32 s65, s65, 0x8000\n" \
M_"v[92:95], a[156:159], a[220:223], v[92:95]\n" \
"v_exp_f32_e64 v72, v72\n" \
M_"v[88:91], a[140:143], a[220:223], v[44:47]\n" \
R_"a[140:143], v48 offset:11264\n" \
R_"a[156:159], v48 offset:15360\n" \
M_"v[88:91], a[136:139], a[216:219], v[88:91]\n" \
"v_exp_f32_e64 v73, v73\n" \
M_"v[88:91], a[132:135], a[212:215], v[88:91]\n" \
".fill (L_ab + 8192 - .) / 4, 4, 0xBF800000\n" \
"L_ap2:\n" \
"s_cbranch_execz L_ap0\n" \
B_"v53, s[8:11], s64 offen offset:0 sc1 lds\n" \
M_"v[88:91], a[128:131], a[208:211], v[88:91]\n" \
R_"a[128:131], v48 offset:8192\n" \
"s_waitcnt lgkmcnt(6)\n" \
M_"v[128:131], a[160:163], v[100:103], v[128:131]\n" \
R_"a[132:135], v48 offset:9216\n" \
R_"a[136:139], v48 offset:10240\n" \
M_"v[160:163], a[160:163], v[104:107], v[160:163]\n" \
"v_exp_f32_e64 v68, v68\n" \
M_"v[132:135], a[164:167], v[100:103], v[132:135]\n" \
"v_exp_f32_e64 v69, v69\n" \
M_"v[164:167], a[164:167], v[104:107], v[164:167]\n" \
"v_exp_f32_e64 v74, v74\n" \
M_"v[136:139], a[168:171], v[100:103], v[136:139]\n" \
"v_exp_f32_e64 v75, v75\n" \
M_"v[168:171], a[168:171], v[104:107], v[168:171]\n" \
B_"v53, s[8:11], s64 offen offset:1024 sc1 lds\n" \
M_"v[140:143], a[172:175], v[100:103], v[140:143]\n" \
"v_exp_f32_e64 v66, v66\n" \
M_"v[172:175], a[172:175], v[104:107], v[172:175]\n" \
"v_exp_f32_e64 v67, v67\n" \
M_"v[144:147], a[176:179], v[100:103], v[144:147]\n" \
"v_exp_f32_e64 v64, v64\n" \
M_"v[176:179], a[176:179], v[104:107], v[176:179]\n" \
"v_exp_f32_e64 v65, v65\n" \
M_"v[148:151], a[180:183], v[100:103], v[148:151]\n" \
C_"v72, v72, v73\n" \
C_"v70, v68, v69\n" \
M_"v[180:183], a[180:183], v[104:107], v[180:183]\n" \
"v_exp_f32_e64 v76, v76\n" \
M_"v[152:155], a[184:187], v[100:103], v[152:155]\n" \
"v_exp_f32_e64 v77, v77\n" \
M_"v[184:187], a[184:187], v[104:107], v[184:187]\n" \
B_"v53, s[8:11], s64 offen offset:2048 sc1 lds\n" \
M_"v[156:159], a[188:191], v[100:103], v[156:159]\n" \
"v_exp_f32_e64 v78, v78\n" \
M_"v[188:191], a[188:191], v[104:107], v[188:191]\n" \
"v_exp_f32_e64 v79, v79\n" \
"v_smfmac_f32_16x16x64_bf16 a[96:99], a[104:107], v[100:107], v21\n" \
C_"v73, v74, v75\n" \
C_"v69, v66, v67\n" \
M_"v[220:223], a[188:191], v[116:119], v[220:223]\n" \
C_"v68, v64, v65\n" \
C_"v74, v76, v77\n" \
M_"v[252:255], a[188:191], v[120:123], v[252:255]\n" \
C_"v75, v78, v79\n" \
"v_xor_b32_e32 v48, 0x8000, v48\n" \
M_"v[216:219], a[184:187], v[116:119], v[216:219]\n" \
M_"v[248:251], a[184:187], v[120:123], v[248:251]\n" \
B_"v53, s[8:11], s64 offen offset:3072 sc1 lds\n" \
M_"v[212:215], a[180:183], v[116:119], v[212:215]\n" \
"s_add_u32 s64, s64, 0x10000\n" \
"s_sub_u32 s66, s66, 1\n" \
"s_nop 0\n" \
M_"v[244:247], a[180:183], v[120:123], v[244:247]\n" \
M_"v[208:211], a[176:179], v[116:119], v[208:211]\n" \
M_"v[240:243], a[176:179], v[120:123], v[240:243]\n" \
M_"v[204:207], a[172:175], v[116:119], v[204:207]\n" \
M_"v[236:239], a[172:175], v[120:123], v[236:239]\n" \
M_"v[200:203], a[168:171], v[116:119], v[200:203]\n" \
M_"v[232:235], a[168:171], v[120:123], v[232:235]\n" \
M_"v[196:199], a[164:167], v[116:119], v[196:199]\n" \
M_"v[228:231], a[164:167], v[120:123], v[228:231]\n" \
M_"v[192:195], a[160:163], v[116:119], v[192:195]\n" \
M_"v[224:227], a[160:163], v[120:123], v[224:227]\n" \
"s_cbranch_scc0 L_tL_2_%=\n" \
"s_waitcnt vmcnt(8) lgkmcnt(0)\n" \
"v_smfmac_f32_16x16x64_bf16 a[100:103], a[104:107], v[116:123], v21\n" \
"s_barrier\n" \
"L_la_%=:\n" \
M_"v[96:99], a[128:131], a[64:67], v[32:35]\n" \
"v_readlane_b32 s96, v11, 1\n" \
"v_lshrrev_b32_e32 v16, 1, v20\n" \
M_"v[96:99], a[132:135], a[68:71], v[96:99]\n" \
"s_xor_b32 m0, s65, 0x12000\n" \
R_"a[160:163], v49 offset:0\n" \
M_"v[96:99], a[136:139], a[72:75], v[96:99]\n" \
"buffer_load_dwordx2 v[26:27], v16, s[56:59], s96 offen sc1\n" \
M_"v[96:99], a[140:143], a[76:79], v[96:99]\n" \
B_"v50, s[8:11], s64 offen offset:0 sc1 lds\n" \
M_"v[100:103], a[156:159], a[76:79], v[32:35]\n" \
R_"a[164:167], v49 offset:1024\n" \
R_"a[168:171], v49 offset:2048\n" \
M_"v[100:103], a[152:155], a[72:75], v[100:103]\n" \
R_"a[172:175], v49 offset:3072\n" \
R_"a[176:179], v49 offset:4096\n" \
M_"v[100:103], a[148:151], a[68:71], v[100:103]\n" \
R_"a[180:183], v49 offset:5120\n" \
R_"a[184:187], v49 offset:6144\n" \
M_"v[100:103], a[144:147], a[64:67], v[100:103]\n" \
R_"a[188:191], v49 offset:7168\n" \
"s_mov_b32 s63, s62\n" \
M_"v[108:111], a[144:147], a[80:83], v[36:39]\n" \
E_"v86, v86\n" \
M_"v[108:111], a[148:151], a[84:87], v[108:111]\n" \
"v_exp_f32_e64 v87, v87\n" \
M_"v[108:111], a[152:155], a[88:91], v[108:111]\n" \
B_"v50, s[8:11], s64 offen offset:1024 sc1 lds\n" \
M_"v[108:111], a[156:159], a[92:95], v[108:111]\n" \
"v_exp_f32_e64 v84, v84\n" \
M_"v[104:107], a[140:143], a[92:95], v[36:39]\n" \
"v_exp_f32_e64 v85, v85\n" \
M_"v[104:107], a[136:139], a[88:91], v[104:107]\n" \
C_"v87, v86, v87\n" \
C_"v86, v84, v85\n" \
M_"v[104:107], a[132:135], a[84:87], v[104:107]\n" \
"v_exp_f32_e64 v82, v82\n" \
M_"v[104:107], a[128:131], a[80:83], v[104:107]\n" \
"v_exp_f32_e64 v83, v83\n" \
M_"v[112:115], a[128:131], a[192:195], v[40:43]\n" \
"v_exp_f32_e64 v80, v80\n" \
M_"v[112:115], a[132:135], a[196:199], v[112:115]\n" \
"v_exp_f32_e64 v81, v81\n" \
M_"v[112:115], a[136:139], a[200:203], v[112:115]\n" \
B_"v50, s[8:11], s64 offen offset:2048 sc1 lds\n" \
M_"v[112:115], a[140:143], a[204:207], v[112:115]\n" \
C_"v85, v82, v83\n" \
C_"v84, v80, v81\n" \
M_"v[116:119], a[156:159], a[204:207], v[40:43]\n" \
"v_exp_f32_e64 v88, v88\n" \
M_"v[116:119], a[152:155], a[200:203], v[116:119]\n" \
"v_exp_f32_e64 v89, v89\n" \
M_"v[116:119], a[148:151], a[196:199], v[116:119]\n" \
"v_exp_f32_e64 v90, v90\n" \
M_"v[116:119], a[144:147], a[192:195], v[116:119]\n" \
"v_exp_f32_e64 v91, v91\n" \
M_"v[124:127], a[144:147], a[208:211], v[44:47]\n" \
C_"v88, v88, v89\n" \
C_"v89, v90, v91\n" \
M_"v[124:127], a[148:151], a[212:215], v[124:127]\n" \
B_"v50, s[8:11], s64 offen offset:3072 sc1 lds\n" \
M_"v[124:127], a[152:155], a[216:219], v[124:127]\n" \
"v_exp_f32_e64 v92, v92\n" \
M_"v[124:127], a[156:159], a[220:223], v[124:127]\n" \
"v_exp_f32_e64 v93, v93\n" \
M_"v[120:123], a[140:143], a[220:223], v[44:47]\n" \
"v_exp_f32_e64 v94, v94\n" \
M_"v[120:123], a[136:139], a[216:219], v[120:123]\n" \
"v_exp_f32_e64 v95, v95\n" \
M_"v[120:123], a[132:135], a[212:215], v[120:123]\n" \
C_"v90, v92, v93\n" \
C_"v91, v94, v95\n" \
M_"v[120:123], a[128:131], a[208:211], v[120:123]\n" \
"v_readlane_b32 s97, v11, 39\n" \
"s_waitcnt lgkmcnt(0)\n" \
"s_nop 0\n" \
M_"v[128:131], a[160:163], v[68:71], v[128:131]\n" \
R_"a[128:131], v48 offset:0\n" \
R_"a[144:147], v48 offset:4096\n" \
M_"v[160:163], a[160:163], v[72:75], v[160:163]\n" \
R_"a[132:135], v48 offset:1024\n" \
R_"a[148:151], v48 offset:5120\n" \
M_"v[132:135], a[164:167], v[68:71], v[132:135]\n" \
R_"a[136:139], v48 offset:2048\n" \
R_"a[152:155], v48 offset:6144\n" \
M_"v[164:167], a[164:167], v[72:75], v[164:167]\n" \
R_"a[140:143], v48 offset:3072\n" \
R_"a[156:159], v48 offset:7168\n" \
M_"v[136:139], a[168:171], v[68:71], v[136:139]\n" \
"v_add_u32_e32 v17, 0x49000, v20\n" \
"v_readlane_b32 s98, v11, 40\n" \
M_"v[168:171], a[168:171], v[72:75], v[168:171]\n" \
"v_readlane_b32 s93, v11, 23\n" \
"v_readlane_b32 s84, v11, 15\n" \
M_"v[140:143], a[172:175], v[68:71], v[140:143]\n" \
"v_readlane_b32 s85, v11, 16\n" \
"v_readlane_b32 s86, v11, 17\n" \
M_"v[172:175], a[172:175], v[72:75], v[172:175]\n" \
"s_mov_b32 s87, 0x20000\n" \
"v_readlane_b32 s92, v11, 18\n" \
M_"v[144:147], a[176:179], v[68:71], v[144:147]\n" \
"v_exp_f32_e64 v102, v102\n" \
M_"v[176:179], a[176:179], v[72:75], v[176:179]\n" \
"v_exp_f32_e64 v103, v103\n" \
M_"v[148:151], a[180:183], v[68:71], v[148:151]\n" \
"v_exp_f32_e64 v100, v100\n" \
M_"v[180:183], a[180:183], v[72:75], v[180:183]\n" \
"v_exp_f32_e64 v101, v101\n" \
M_"v[152:155], a[184:187], v[68:71], v[152:155]\n" \
C_"v103, v102, v103\n" \
C_"v102, v100, v101\n" \
M_"v[184:187], a[184:187], v[72:75], v[184:187]\n" \
"v_exp_f32_e64 v98, v98\n" \
M_"v[156:159], a[188:191], v[68:71], v[156:159]\n" \
"v_exp_f32_e64 v99, v99\n" \
M_"v[188:191], a[188:191], v[72:75], v[188:191]\n" \
"s_waitcnt vmcnt(4)\n" \
"v_cmp_ne_u64_e32 vcc, s[62:63], v[26:27]\n" \
"v_smfmac_f32_16x16x64_bf16 a[96:99], a[104:107], v[68:75], v21\n" \
"v_exp_f32_e64 v96, v96\n" \
M_"v[220:223], a[188:191], v[84:87], v[220:223]\n" \
"v_exp_f32_e64 v97, v97\n" \
M_"v[252:255], a[188:191], v[88:91], v[252:255]\n" \
"s_cbranch_vccnz L_sol_%=\n" \
"L_rol_%=:\n" \
C_"v101, v98, v99\n" \
B_"a[124:127], v20, s[56:59], s97 offen offset:0 sc1\n" \
M_"v[216:219], a[184:187], v[84:87], v[216:219]\n" \
C_"v100, v96, v97\n" \
"v_readlane_b32 s91, v11, 24\n" \
M_"v[248:251], a[184:187], v[88:91], v[248:251]\n" \
E_"v104, v104\n" \
M_"v[212:215], a[180:183], v[84:87], v[212:215]\n" \
"v_exp_f32_e64 v105, v105\n" \
M_"v[244:247], a[180:183], v[88:91], v[244:247]\n" \
"v_exp_f32_e64 v106, v106\n" \
M_"v[208:211], a[176:179], v[84:87], v[208:211]\n" \
"v_exp_f32_e64 v107, v107\n" \
M_"v[240:243], a[176:179], v[88:91], v[240:243]\n" \
B_"a[228:231], v17, s[56:59], s97 offen offset:0 sc1\n" \
M_"v[204:207], a[172:175], v[84:87], v[204:207]\n" \
C_"v104, v104, v105\n" \
C_"v105, v106, v107\n" \
M_"v[236:239], a[172:175], v[88:91], v[236:239]\n" \
"v_exp_f32_e64 v108, v108\n" \
M_"v[200:203], a[168:171], v[84:87], v[200:203]\n" \
"v_exp_f32_e64 v109, v109\n" \
M_"v[232:235], a[168:171], v[88:91], v[232:235]\n" \
"v_exp_f32_e64 v110, v110\n" \
M_"v[196:199], a[164:167], v[84:87], v[196:199]\n" \
"v_exp_f32_e64 v111, v111\n" \
M_"v[228:231], a[164:167], v[88:91], v[228:231]\n" \
C_"v106, v108, v109\n" \
C_"v107, v110, v111\n" \
M_"v[192:195], a[160:163], v[84:87], v[192:195]\n" \
"v_exp_f32_e64 v118, v118\n" \
M_"v[224:227], a[160:163], v[88:91], v[224:227]\n" \
"s_xor_b32 s65, s65, 0x8000\n" \
"s_waitcnt vmcnt(7) lgkmcnt(0)\n" \
"v_smfmac_f32_16x16x64_bf16 a[100:103], a[104:107], v[84:91], v21\n" \
"s_barrier\n" \
M_"v[64:67], a[128:131], a[64:67], v[32:35]\n" \
B_"a[236:239], v20, s[56:59], s98 offen offset:0 sc1\n" \
M_"v[64:67], a[132:135], a[68:71], v[64:67]\n" \
"s_add_u32 m0, s93, 0x0\n" \
"s_nop 0\n" \
R_"a[160:163], v49 offset:8192\n" \
M_"v[64:67], a[136:139], a[72:75], v[64:67]\n" \
B_"v0, s[84:87], s92 offen offset:0 lds\n" \
M_"v[64:67], a[140:143], a[76:79], v[64:67]\n" \
R_"a[164:167], v49 offset:9216\n" \
R_"a[168:171], v49 offset:10240\n" \
M_"v[68:71], a[156:159], a[76:79], v[32:35]\n" \
R_"a[172:175], v49 offset:11264\n" \
R_"a[176:179], v49 offset:12288\n" \
M_"v[68:71], a[152:155], a[72:75], v[68:71]\n" \
R_"a[180:183], v49 offset:13312\n" \
R_"a[184:187], v49 offset:14336\n" \
M_"v[68:71], a[148:151], a[68:71], v[68:71]\n" \
B_"v1, s[84:87], s92 offen offset:1024 lds\n" \
M_"v[68:71], a[144:147], a[64:67], v[68:71]\n" \
B_"a[244:247], v17, s[56:59], s98 offen offset:0 sc1\n" \
M_"v[76:79], a[144:147], a[80:83], v[36:39]\n" \
R_"a[188:191], v49 offset:15360\n" \
"v_add_u32_e32 v49, 0x10000, v48\n" \
M_"v[76:79], a[148:151], a[84:87], v[76:79]\n" \
"v_exp_f32_e64 v119, v119\n" \
M_"v[76:79], a[152:155], a[88:91], v[76:79]\n" \
"v_exp_f32_e64 v116, v116\n" \
M_"v[76:79], a[156:159], a[92:95], v[76:79]\n" \
"v_exp_f32_e64 v117, v117\n" \
M_"v[72:75], a[140:143], a[92:95], v[36:39]\n" \
C_"v119, v118, v119\n" \
C_"v118, v116, v117\n" \
M_"v[72:75], a[136:139], a[88:91], v[72:75]\n" \
B_"v2, s[84:87], s92 offen offset:2048 lds\n" \
M_"v[72:75], a[132:135], a[84:87], v[72:75]\n" \
"v_exp_f32_e64 v114, v114\n" \
M_"v[72:75], a[128:131], a[80:83], v[72:75]\n" \
"v_exp_f32_e64 v115, v115\n" \
M_"v[80:83], a[128:131], a[192:195], v[40:43]\n" \
B_"a[224:227], v20, s[56:59], s97 offen offset:1024 sc1\n" \
M_"v[80:83], a[132:135], a[196:199], v[80:83]\n" \
"v_exp_f32_e64 v112, v112\n" \
M_"v[80:83], a[136:139], a[200:203], v[80:83]\n" \
"v_exp_f32_e64 v113, v113\n" \
M_"v[80:83], a[140:143], a[204:207], v[80:83]\n" \
C_"v117, v114, v115\n" \
C_"v116, v112, v113\n" \
M_"v[84:87], a[156:159], a[204:207], v[40:43]\n" \
B_"v3, s[84:87], s92 offen offset:3072 lds\n" \
M_"v[84:87], a[152:155], a[200:203], v[84:87]\n" \
"s_add_u32 m0, s91, 0x1000\n" \
"s_add_u32 s64, s64, 0x10000\n" \
M_"v[84:87], a[148:151], a[196:199], v[84:87]\n" \
"v_exp_f32_e64 v120, v120\n" \
M_"v[84:87], a[144:147], a[192:195], v[84:87]\n" \
"v_exp_f32_e64 v121, v121\n" \
M_"v[92:95], a[144:147], a[208:211], v[44:47]\n" \
"v_exp_f32_e64 v122, v122\n" \
M_"v[92:95], a[148:151], a[212:215], v[92:95]\n" \
B_"a[232:235], v17, s[56:59], s97 offen offset:1024 sc1\n" \
M_"v[92:95], a[152:155], a[216:219], v[92:95]\n" \
B_"v4, s[84:87], s92 offen offset:0 lds\n" \
M_"v[92:95], a[156:159], a[220:223], v[92:95]\n" \
"v_exp_f32_e64 v123, v123\n" \
M_"v[88:91], a[140:143], a[220:223], v[44:47]\n" \
C_"v120, v120, v121\n" \
C_"v121, v122, v123\n" \
M_"v[88:91], a[136:139], a[216:219], v[88:91]\n" \
"v_exp_f32_e64 v124, v124\n" \
M_"v[88:91], a[132:135], a[212:215], v[88:91]\n" \
"v_exp_f32_e64 v125, v125\n" \
M_"v[88:91], a[128:131], a[208:211], v[88:91]\n" \
C_"v122, v124, v125\n" \
"s_waitcnt lgkmcnt(0)\n" \
M_"v[128:131], a[160:163], v[100:103], v[128:131]\n" \
E_"v126, v126\n" \
M_"v[160:163], a[160:163], v[104:107], v[160:163]\n" \
B_"v5, s[84:87], s92 offen offset:1024 lds\n" \
M_"v[132:135], a[164:167], v[100:103], v[132:135]\n" \
B_"a[240:243], v20, s[56:59], s98 offen offset:1024 sc1\n" \
M_"v[164:167], a[164:167], v[104:107], v[164:167]\n" \
"v_exp_f32_e64 v127, v127\n" \
M_"v[136:139], a[168:171], v[100:103], v[136:139]\n" \
C_"v123, v126, v127\n" \
R_"a[128:131], v48 offset:8192\n" \
M_"v[168:171], a[168:171], v[104:107], v[168:171]\n" \
R_"a[144:147], v48 offset:12288\n" \
R_"a[132:135], v48 offset:9216\n" \
M_"v[140:143], a[172:175], v[100:103], v[140:143]\n" \
R_"a[148:151], v48 offset:13312\n" \
R_"a[136:139], v48 offset:10240\n" \
M_"v[172:175], a[172:175], v[104:107], v[172:175]\n" \
R_"a[152:155], v48 offset:14336\n" \
R_"a[140:143], v48 offset:11264\n" \
M_"v[144:147], a[176:179], v[100:103], v[144:147]\n" \
B_"v6, s[84:87], s92 offen offset:2048 lds\n" \
M_"v[176:179], a[176:179], v[104:107], v[176:179]\n" \
"v_exp_f32_e64 v70, v70\n" \
M_"v[148:151], a[180:183], v[100:103], v[148:151]\n" \
"v_exp_f32_e64 v71, v71\n" \
M_"v[180:183], a[180:183], v[104:107], v[180:183]\n" \
B_"a[248:251], v17, s[56:59], s98 offen offset:1024 sc1\n" \
M_"v[152:155], a[184:187], v[100:103], v[152:155]\n" \
R_"a[156:159], v48 offset:15360\n" \
C_"v71, v70, v71\n" \
M_"v[184:187], a[184:187], v[104:107], v[184:187]\n" \
"v_exp_f32_e64 v72, v72\n" \
M_"v[156:159], a[188:191], v[100:103], v[156:159]\n" \
B_"v7, s[84:87], s92 offen offset:3072 lds\n" \
M_"v[188:191], a[188:191], v[104:107], v[188:191]\n" \
"s_add_u32 m0, s91, 0x2000\n" \
"v_xor_b32_e32 v48, 0x8000, v48\n" \
"v_smfmac_f32_16x16x64_bf16 a[96:99], a[104:107], v[100:107], v21\n" \
"v_exp_f32_e64 v73, v73\n" \
M_"v[220:223], a[188:191], v[116:119], v[220:223]\n" \
"v_exp_f32_e64 v68, v68\n" \
M_"v[252:255], a[188:191], v[120:123], v[252:255]\n" \
"v_exp_f32_e64 v69, v69\n" \
M_"v[216:219], a[184:187], v[116:119], v[216:219]\n" \
"v_exp_f32_e64 v74, v74\n" \
M_"v[248:251], a[184:187], v[120:123], v[248:251]\n" \
"v_exp_f32_e64 v75, v75\n" \
M_"v[212:215], a[180:183], v[116:119], v[212:215]\n" \
B_"v8, s[84:87], s92 offen offset:0 lds\n" \
M_"v[244:247], a[180:183], v[120:123], v[244:247]\n" \
"v_exp_f32_e64 v66, v66\n" \
M_"v[208:211], a[176:179], v[116:119], v[208:211]\n" \
"v_exp_f32_e64 v67, v67\n" \
M_"v[240:243], a[176:179], v[120:123], v[240:243]\n" \
"v_exp_f32_e64 v64, v64\n" \
M_"v[204:207], a[172:175], v[116:119], v[204:207]\n" \
"v_exp_f32_e64 v65, v65\n" \
M_"v[236:239], a[172:175], v[120:123], v[236:239]\n" \
C_"v72, v72, v73\n" \
C_"v70, v68, v69\n" \
M_"v[200:203], a[168:171], v[116:119], v[200:203]\n" \
"v_exp_f32_e64 v76, v76\n" \
M_"v[232:235], a[168:171], v[120:123], v[232:235]\n" \
"v_exp_f32_e64 v77, v77\n" \
M_"v[196:199], a[164:167], v[116:119], v[196:199]\n" \
"v_exp_f32_e64 v78, v78\n" \
M_"v[228:231], a[164:167], v[120:123], v[228:231]\n" \
"v_exp_f32_e64 v79, v79\n" \
M_"v[192:195], a[160:163], v[116:119], v[192:195]\n" \
C_"v73, v74, v75\n" \
C_"v69, v66, v67\n" \
M_"v[224:227], a[160:163], v[120:123], v[224:227]\n" \
C_"v68, v64, v65\n" \
"s_waitcnt vmcnt(15) lgkmcnt(0)\n" \
"v_smfmac_f32_16x16x64_bf16 a[100:103], a[104:107], v[116:123], v21\n" \
"s_barrier\n" \
C_"v74, v76, v77\n" \
C_"v75, v78, v79\n" \
"s_cmp_lg_u32 s16, 0\n" \
"s_cbranch_scc1 L_f1_%=\n" \
R_"a[160:163], v49 offset:0\n" \
R_"a[164:167], v49 offset:1024\n" \
R_"a[168:171], v49 offset:2048\n" \
R_"a[172:175], v49 offset:3072\n" \
R_"a[176:179], v49 offset:4096\n" \
R_"a[180:183], v49 offset:5120\n" \
R_"a[184:187], v49 offset:6144\n" \
R_"a[188:191], v49 offset:7168\n" \
"v_add_u32_e32 v54, 0x49000, v20\n" \
B_"a[192:195], v20, s[56:59], s60 offen offset:0 sc1\n" \
"v_add_u32_e32 v50, s15, v20\n" \
"v_add_u32_e32 v51, 0x10000, v50\n" \
"ds_read_b32 v60, v18 offset:0\n" \
"ds_read_b32 v61, v18 offset:128\n" \
R_"a[0:3], v49 offset:8192\n" \
R_"a[4:7], v49 offset:9216\n" \
R_"a[8:11], v49 offset:10240\n" \
R_"a[12:15], v49 offset:11264\n" \
R_"a[16:19], v49 offset:12288\n" \
R_"a[20:23], v49 offset:13312\n" \
R_"a[24:27], v49 offset:14336\n" \
R_"a[28:31], v49 offset:15360\n" \
E_"v86, v86\n" \
E_"v87, v87\n" \
E_"v84, v84\n" \
E_"v85, v85\n" \
C_"v87, v86, v87\n" \
B_"a[196:199], v20, s[56:59], s60 offen offset:1024 sc1\n" \
E_"v82, v82\n" \
E_"v83, v83\n" \
E_"v80, v80\n" \
E_"v81, v81\n" \
"s_waitcnt lgkmcnt(15)\n" \
M_"v[128:131], a[160:163], v[68:71], v[128:131]\n" \
C_"v86, v84, v85\n" \
C_"v85, v82, v83\n" \
M_"v[160:163], a[160:163], v[72:75], v[160:163]\n" \
C_"v84, v80, v81\n" \
"v_and_b32_e32 v84, v12, v84\n" \
M_"v[132:135], a[164:167], v[68:71], v[132:135]\n" \
"v_and_b32_e32 v85, v13, v85\n" \
"v_and_b32_e32 v86, v14, v86\n" \
M_"v[164:167], a[164:167], v[72:75], v[164:167]\n" \
B_"a[200:203], v20, s[56:59], s60 offen offset:2048 sc1\n" \
M_"v[136:139], a[168:171], v[68:71], v[136:139]\n" \
C_"v128, v128, v129\n" \
C_"v160, v160, v161\n" \
M_"v[168:171], a[168:171], v[72:75], v[168:171]\n" \
C_"v129, v130, v131\n" \
"s_waitcnt lgkmcnt(10)\n" \
M_"v[140:143], a[172:175], v[68:71], v[140:143]\n" \
C_"v161, v162, v163\n" \
C_"v130, v132, v133\n" \
M_"v[172:175], a[172:175], v[72:75], v[172:175]\n" \
C_"v131, v134, v135\n" \
C_"v162, v164, v165\n" \
M_"v[144:147], a[176:179], v[68:71], v[144:147]\n" \
C_"v163, v166, v167\n" \
C_"v132, v136, v137\n" \
M_"v[176:179], a[176:179], v[72:75], v[176:179]\n" \
B_"a[204:207], v20, s[56:59], s60 offen offset:3072 sc1\n" \
M_"v[148:151], a[180:183], v[68:71], v[148:151]\n" \
C_"v133, v138, v139\n" \
C_"v134, v140, v141\n" \
M_"v[180:183], a[180:183], v[72:75], v[180:183]\n" \
C_"v135, v142, v143\n" \
C_"v164, v168, v169\n" \
M_"v[152:155], a[184:187], v[68:71], v[152:155]\n" \
C_"v165, v170, v171\n" \
C_"v166, v172, v173\n" \
M_"v[184:187], a[184:187], v[72:75], v[184:187]\n" \
C_"v167, v174, v175\n" \
C_"v136, v144, v145\n" \
M_"v[156:159], a[188:191], v[68:71], v[156:159]\n" \
C_"v137, v146, v147\n" \
C_"v138, v148, v149\n" \
M_"v[188:191], a[188:191], v[72:75], v[188:191]\n" \
C_"v139, v150, v151\n" \
C_"v140, v152, v153\n" \
"v_smfmac_f32_16x16x64_bf16 a[96:99], a[104:107], v[68:75], v21\n" \
C_"v141, v154, v155\n" \
C_"v168, v176, v177\n" \
M_"v[120:123], a[128:131], a[208:211], v[44:47]\n" \
C_"v142, v156, v157\n" \
C_"v143, v158, v159\n" \
M_"v[120:123], a[132:135], a[212:215], v[120:123]\n" \
C_"v169, v178, v179\n" \
C_"v170, v180, v181\n" \
C_"v171, v182, v183\n" \
C_"v172, v184, v185\n" \
C_"v173, v186, v187\n" \
C_"v174, v188, v189\n" \
C_"v175, v190, v191\n" \
"ds_write_b128 v50, v[128:131] offset:0\n" \
M_"v[120:123], a[136:139], a[216:219], v[120:123]\n" \
"v_and_b32_e32 v87, v15, v87\n" \
"v_accvgpr_read_b32 v40, a96\n" \
M_"v[120:123], a[140:143], a[220:223], v[120:123]\n" \
"ds_write_b128 v50, v[132:135] offset:1024\n" \
M_"v[124:127], a[156:159], a[220:223], v[44:47]\n" \
B_"a[220:223], v54, s[56:59], s60 offen offset:3072 sc1\n" \
M_"v[124:127], a[152:155], a[216:219], v[124:127]\n" \
"ds_write_b128 v50, v[136:139] offset:2048\n" \
M_"v[124:127], a[148:151], a[212:215], v[124:127]\n" \
"v_accvgpr_read_b32 v22, a97\n" \
"v_accvgpr_read_b32 v41, a98\n" \
M_"v[124:127], a[144:147], a[208:211], v[124:127]\n" \
"ds_write_b128 v50, v[140:143] offset:3072\n" \
M_"a[100:103], a[108:111], v[84:87], a[100:103]\n" \
"v_accvgpr_read_b32 v23, a99\n" \
"v_add_f32_e32 v40, v40, v22\n" \
M_"v[192:195], a[160:163], v[84:87], v[192:195]\n" \
"ds_write_b128 v51, v[160:163] offset:32768\n" \
M_"v[196:199], a[164:167], v[84:87], v[196:199]\n" \
B_"a[208:211], v54, s[56:59], s60 offen offset:0 sc1\n" \
M_"v[200:203], a[168:171], v[84:87], v[200:203]\n" \
"ds_write_b128 v51, v[164:167] offset:33792\n" \
"v_add_f32_e32 v41, v41, v23\n" \
"ds_write_b64 v19, v[40:41]\n" \
M_"v[204:207], a[172:175], v[84:87], v[204:207]\n" \
"v_lshl_add_u32 v52, s15, 2, v20\n" \
"v_add_u32_e32 v52, 0x1a000, v52\n" \
"ds_write_b128 v51, v[168:171] offset:34816\n" \
M_"v[208:211], a[176:179], v[84:87], v[208:211]\n" \
E_"v88, v88\n" \
M_"v[212:215], a[180:183], v[84:87], v[212:215]\n" \
"ds_write_b128 v51, v[172:175] offset:35840\n" \
M_"v[216:219], a[184:187], v[84:87], v[216:219]\n" \
B_"a[212:215], v54, s[56:59], s60 offen offset:1024 sc1\n" \
M_"v[220:223], a[188:191], v[84:87], v[220:223]\n" \
E_"v89, v89\n" \
E_"v90, v90\n" \
E_"v91, v91\n" \
C_"v88, v88, v89\n" \
E_"v92, v92\n" \
E_"v93, v93\n" \
E_"v94, v94\n" \
E_"v95, v95\n" \
C_"v89, v90, v91\n" \
C_"v90, v92, v93\n" \
C_"v91, v94, v95\n" \
"v_xor_b32_e32 v63, 0x400, v19\n" \
B_"a[216:219], v54, s[56:59], s60 offen offset:2048 sc1\n" \
E_"v120, v120\n" \
E_"v121, v121\n" \
E_"v122, v122\n" \
E_"v123, v123\n" \
C_"v120, v120, v121\n" \
E_"v124, v124\n" \
E_"v125, v125\n" \
"s_waitcnt lgkmcnt(0)\n" \
"s_barrier\n" \
R_"v[144:147], v52 offset:0\n" \
R_"v[148:151], v52 offset:1024\n" \
R_"v[152:155], v52 offset:2048\n" \
".fill (L_ab + 12288 - .) / 4, 4, 0xBF800000\n" \
"L_ap3:\n" \
"s_cbranch_execz L_ap0\n" \
R_"v[156:159], v52 offset:3072\n" \
R_"v[176:179], v52 offset:4096\n" \
R_"v[180:183], v52 offset:5120\n" \
R_"v[184:187], v52 offset:6144\n" \
R_"v[188:191], v52 offset:7168\n" \
M_"a[100:103], a[116:119], v[88:91], a[100:103]\n" \
"ds_read_b64 v[42:43], v63\n" \
C_"v121, v122, v123\n" \
M_"v[224:227], a[160:163], v[88:91], v[224:227]\n" \
E_"v126, v126\n" \
M_"v[228:231], a[164:167], v[88:91], v[228:231]\n" \
E_"v127, v127\n" \
M_"v[232:235], a[168:171], v[88:91], v[232:235]\n" \
C_"v122, v124, v125\n" \
C_"v123, v126, v127\n" \
M_"v[236:239], a[172:175], v[88:91], v[236:239]\n" \
"v_and_b32_e32 v120, v12, v120\n" \
"v_and_b32_e32 v121, v13, v121\n" \
M_"v[240:243], a[176:179], v[88:91], v[240:243]\n" \
"v_and_b32_e32 v122, v14, v122\n" \
"v_and_b32_e32 v123, v15, v123\n" \
M_"v[244:247], a[180:183], v[88:91], v[244:247]\n" \
"v_readlane_b32 s93, v11, 25\n" \
"v_readlane_b32 s92, v11, 30\n" \
M_"v[248:251], a[184:187], v[88:91], v[248:251]\n" \
"s_add_u32 m0, s93, 0x0\n" \
"v_mov_b32_e32 v130, v28\n" \
M_"v[252:255], a[188:191], v[88:91], v[252:255]\n" \
"v_mov_b32_e32 v131, v29\n" \
"v_mov_b32_e32 v132, 0\n" \
"v_mov_b32_e32 v133, 0\n" \
"v_mov_b32_e32 v128, 0\n" \
"v_mov_b32_e32 v129, 0\n" \
"s_waitcnt vmcnt(8) lgkmcnt(1)\n" \
"s_barrier\n" \
M_"a[100:103], a[116:119], v[120:123], a[100:103]\n" \
B_"v0, s[80:83], s92 offen offset:0 lds\n" \
M_"v[224:227], a[0:3], v[120:123], v[224:227]\n" \
M_"v[228:231], a[4:7], v[120:123], v[228:231]\n" \
M_"v[232:235], a[8:11], v[120:123], v[232:235]\n" \
M_"v[236:239], a[12:15], v[120:123], v[236:239]\n" \
M_"v[240:243], a[16:19], v[120:123], v[240:243]\n" \
M_"v[244:247], a[20:23], v[120:123], v[244:247]\n" \
B_"v1, s[80:83], s92 offen offset:1024 lds\n" \
M_"v[248:251], a[24:27], v[120:123], v[248:251]\n" \
"v_accvgpr_read_b32 v56, a100\n" \
"v_accvgpr_read_b32 v24, a101\n" \
M_"v[252:255], a[28:31], v[120:123], v[252:255]\n" \
"v_add_f32_e32 v56, v56, v24\n" \
"s_waitcnt lgkmcnt(0)\n" \
"v_add_f32_e32 v56, v56, v42\n" \
M_"v[192:195], v[130:133], v[144:147], v[192:195]\n" \
"v_max_f32_e32 v62, v62, v56\n" \
"v_accvgpr_read_b32 v57, a102\n" \
M_"v[196:199], v[128:131], v[144:147], v[196:199]\n" \
"v_rcp_f32_e32 v56, v56\n" \
M_"v[200:203], v[130:133], v[148:151], v[200:203]\n" \
"v_mul_f32_e32 v56, v56, v60\n" \
"v_accvgpr_read_b32 v25, a103\n" \
B_"v2, s[80:83], s92 offen offset:2048 lds\n" \
M_"v[204:207], v[128:131], v[148:151], v[204:207]\n" \
"v_mul_f32_e32 v192, v56, v192\n" \
"v_mul_f32_e32 v193, v56, v193\n" \
M_"v[208:211], v[130:133], v[152:155], v[208:211]\n" \
"v_mul_f32_e32 v194, v56, v194\n" \
"v_mul_f32_e32 v195, v56, v195\n" \
M_"v[212:215], v[128:131], v[152:155], v[212:215]\n" \
"v_mul_f32_e32 v196, v56, v196\n" \
"v_mul_f32_e32 v197, v56, v197\n" \
M_"v[216:219], v[130:133], v[156:159], v[216:219]\n" \
"v_mul_f32_e32 v198, v56, v198\n" \
"v_mul_f32_e32 v199, v56, v199\n" \
M_"v[220:223], v[128:131], v[156:159], v[220:223]\n" \
C_"v64, v192, v193\n" \
C_"v65, v194, v195\n" \
M_"v[224:227], v[130:133], v[176:179], v[224:227]\n" \
B_"v3, s[80:83], s92 offen offset:3072 lds\n" \
"s_add_u32 m0, s93, 0x1000\n" \
M_"v[228:231], v[128:131], v[176:179], v[228:231]\n" \
C_"v66, v196, v197\n" \
C_"v67, v198, v199\n" \
M_"v[232:235], v[130:133], v[180:183], v[232:235]\n" \
"v_mul_f32_e32 v200, v56, v200\n" \
"v_mul_f32_e32 v201, v56, v201\n" \
M_"v[236:239], v[128:131], v[180:183], v[236:239]\n" \
M_"a[112:115], a[120:123], v[64:67], a[112:115]\n" \
"v_mul_f32_e32 v202, v56, v202\n" \
"v_mul_f32_e32 v203, v56, v203\n" \
M_"v[240:243], v[130:133], v[184:187], v[240:243]\n" \
"v_mul_f32_e32 v204, v56, v204\n" \
"v_mul_f32_e32 v205, v56, v205\n" \
B_"v4, s[80:83], s92 offen offset:0 lds\n" \
"v_mul_f32_e32 v206, v56, v206\n" \
"v_mul_f32_e32 v207, v56, v207\n" \
"v_add_f32_e32 v57, v57, v25\n" \
C_"v68, v200, v201\n" \
C_"v69, v202, v203\n" \
C_"v70, v204, v205\n" \
C_"v71, v206, v207\n" \
"v_add_f32_e32 v57, v57, v43\n" \
"v_mul_f32_e32 v208, v56, v208\n" \
M_"a[112:115], a[120:123], v[68:71], a[112:115]\n" \
"v_mul_f32_e32 v209, v56, v209\n" \
"v_mul_f32_e32 v210, v56, v210\n" \
"v_mul_f32_e32 v211, v56, v211\n" \
"v_mul_f32_e32 v212, v56, v212\n" \
"v_mul_f32_e32 v213, v56, v213\n" \
"v_mul_f32_e32 v214, v56, v214\n" \
"v_mul_f32_e32 v215, v56, v215\n" \
"v_max_f32_e32 v62, v62, v57\n" \
C_"v72, v208, v209\n" \
C_"v73, v210, v211\n" \
B_"v5, s[80:83], s92 offen offset:1024 lds\n" \
C_"v74, v212, v213\n" \
C_"v75, v214, v215\n" \
"v_rcp_f32_e32 v57, v57\n" \
M_"v[244:247], v[128:131], v[184:187], v[244:247]\n" \
M_"a[112:115], a[120:123], v[72:75], a[112:115]\n" \
"v_mul_f32_e32 v216, v56, v216\n" \
"v_mul_f32_e32 v217, v56, v217\n" \
"v_mul_f32_e32 v218, v56, v218\n" \
"v_mul_f32_e32 v219, v56, v219\n" \
"v_mul_f32_e32 v220, v56, v220\n" \
"v_mul_f32_e32 v221, v56, v221\n" \
"v_mul_f32_e32 v222, v56, v222\n" \
"v_mul_f32_e32 v223, v56, v223\n" \
C_"v76, v216, v217\n" \
C_"v77, v218, v219\n" \
C_"v78, v220, v221\n" \
B_"v6, s[80:83], s92 offen offset:2048 lds\n" \
C_"v79, v222, v223\n" \
"v_mul_f32_e32 v57, v57, v61\n" \
"v_mul_f32_e32 v224, v57, v224\n" \
M_"a[112:115], a[120:123], v[76:79], a[112:115]\n" \
"v_mul_f32_e32 v225, v57, v225\n" \
"v_mul_f32_e32 v226, v57, v226\n" \
"v_mul_f32_e32 v227, v57, v227\n" \
"v_mul_f32_e32 v228, v57, v228\n" \
"v_mul_f32_e32 v229, v57, v229\n" \
"v_mul_f32_e32 v230, v57, v230\n" \
"v_mul_f32_e32 v231, v57, v231\n" \
C_"v80, v224, v225\n" \
C_"v81, v226, v227\n" \
C_"v82, v228, v229\n" \
C_"v83, v230, v231\n" \
M_"v[248:251], v[130:133], v[188:191], v[248:251]\n" \
"v_mul_f32_e32 v232, v57, v232\n" \
"v_mul_f32_e32 v233, v57, v233\n" \
M_"a[112:115], a[120:123], v[80:83], a[112:115]\n" \
B_"v7, s[80:83], s92 offen offset:3072 lds\n" \
"s_add_u32 m0, s93, 0x2000\n" \
"v_mul_f32_e32 v234, v57, v234\n" \
"v_mul_f32_e32 v235, v57, v235\n" \
"v_mul_f32_e32 v236, v57, v236\n" \
"v_mul_f32_e32 v237, v57, v237\n" \
"v_mul_f32_e32 v238, v57, v238\n" \
"v_mul_f32_e32 v239, v57, v239\n" \
C_"v84, v232, v233\n" \
C_"v85, v234, v235\n" \
C_"v86, v236, v237\n" \
C_"v87, v238, v239\n" \
"v_mul_f32_e32 v240, v57, v240\n" \
"v_mul_f32_e32 v241, v57, v241\n" \
M_"a[112:115], a[120:123], v[84:87], a[112:115]\n" \
"v_mul_f32_e32 v242, v57, v242\n" \
"v_mul_f32_e32 v243, v57, v243\n" \
"v_mul_f32_e32 v244, v57, v244\n" \
"v_mul_f32_e32 v245, v57, v245\n" \
"v_mul_f32_e32 v246, v57, v246\n" \
"v_mul_f32_e32 v247, v57, v247\n" \
B_"v8, s[80:83], s92 offen offset:0 lds\n" \
C_"v96, v240, v241\n" \
C_"v97, v242, v243\n" \
C_"v98, v244, v245\n" \
C_"v99, v246, v247\n" \
M_"v[252:255], v[128:131], v[188:191], v[252:255]\n" \
"v_mul_f32_e32 v248, v57, v248\n" \
M_"a[112:115], a[120:123], v[96:99], a[112:115]\n" \
"v_mul_f32_e32 v249, v57, v249\n" \
"v_mul_f32_e32 v250, v57, v250\n" \
"v_mul_f32_e32 v251, v57, v251\n" \
C_"v100, v248, v249\n" \
C_"v101, v250, v251\n" \
"v_cmp_lt_f32_e64 s[72:73], s21, v62\n" \
"v_mul_f32_e32 v252, v57, v252\n" \
"v_mul_f32_e32 v253, v57, v253\n" \
"v_mul_f32_e32 v254, v57, v254\n" \
"v_mul_f32_e32 v255, v57, v255\n" \
C_"v102, v252, v253\n" \
C_"v103, v254, v255\n" \
"s_nop 1\n" \
M_"a[112:115], a[120:123], v[100:103], a[112:115]\n" \
"s_nop 7\n" \
"v_accvgpr_read_b32 v63, a112\n" \
"v_cmp_u_f32_e32 vcc, v63, v63\n" \
"s_nop 0\n" \
"s_or_b64 vcc, s[72:73], vcc\n" \
"s_cbranch_scc1 L_end_%=\n" \
"s_branch L_out_%=\n" \
"L_skb_%=:\n" \
"s_sleep 1\n" \
"buffer_load_dword v57, v61, s[56:59], s96 offen sc1\n" \
"s_add_u32 s101, s101, 1\n" \
"s_waitcnt vmcnt(0)\n" \
"v_cmp_ne_u32_e32 vcc, s62, v57\n" \
"s_cmp_gt_u32 s101, 0x100000\n" \
"s_cbranch_scc1 L_rkb_%=\n" \
"s_nop 2\n" \
"s_cbranch_vccnz L_skb_%=\n" \
"s_branch L_rkb_%=\n" \
"L_sol_%=:\n" \
"s_sleep 1\n" \
"buffer_load_dwordx2 v[26:27], v16, s[56:59], s96 offen sc1\n" \
"s_add_u32 s101, s101, 1\n" \
"s_waitcnt vmcnt(0)\n" \
"v_cmp_ne_u64_e32 vcc, s[62:63], v[26:27]\n" \
"s_cmp_gt_u32 s101, 0x100000\n" \
"s_cbranch_scc1 L_rol_%=\n" \
"s_nop 2\n" \
"s_cbranch_vccnz L_sol_%=\n" \
"s_branch L_rol_%=\n" \
"L_sos_%=:\n" \
"s_sleep 1\n" \
"buffer_load_dwordx2 v[26:27], v16, s[56:59], s96 offen sc1\n" \
"s_add_u32 s101, s101, 1\n" \
"s_waitcnt vmcnt(0)\n" \
"v_cmp_ne_u64_e32 vcc, s[62:63], v[26:27]\n" \
"s_cmp_gt_u32 s101, 0x100000\n" \
"s_cbranch_scc1 L_ros_%=\n" \
"s_nop 2\n" \
"s_cbranch_vccnz L_sos_%=\n" \
"s_branch L_ros_%=\n" \
"L_sa_%=:\n" \
"s_sleep 1\n" \
"buffer_load_dword v26, v38, s[56:59], s64 offen sc1\n" \
"s_add_u32 s101, s101, 1\n" \
"s_waitcnt vmcnt(0)\n" \
"v_cmp_ne_u32_e32 vcc, s62, v26\n" \
"s_cmp_gt_u32 s101, 0x100000\n" \
"s_cbranch_scc1 L_ra_%=\n" \
"s_nop 2\n" \
"s_cbranch_vccnz L_sa_%=\n" \
"s_branch L_ra_%=\n" \
"L_sb_%=:\n" \
"s_sleep 1\n" \
"buffer_load_dword v47, v38, s[56:59], s71 offen sc1\n" \
"s_add_u32 s101, s101, 1\n" \
"s_waitcnt vmcnt(0)\n" \
"v_cmp_ne_u32_e32 vcc, s62, v47\n" \
"s_cmp_gt_u32 s101, 0x100000\n" \
"s_cbranch_scc1 L_rb_%=\n" \
"s_nop 2\n" \
"s_cbranch_vccnz L_sb_%=\n" \
"s_branch L_rb_%=\n" \
"L_sna_%=:\n" \
"buffer_load_dword v37, v40, s[56:59], s74 offen sc1\n" \
"s_add_u32 s101, s101, 1\n" \
"s_waitcnt vmcnt(0)\n" \
"v_cmp_eq_u32_e64 s[98:99], s62, v37\n" \
"s_cmp_gt_u32 s101, 0x100000\n" \
"s_cbranch_scc1 L_rna_%=\n" \
"s_andn2_b32 s75, s20, s98\n" \
"s_cbranch_scc0 L_gna_%=\n" \
"s_sleep 1\n" \
"s_branch L_sna_%=\n" \
"L_gna_%=:\n" \
"s_bitcmp1_b32 s20, 0\n" \
"s_cbranch_scc0 L_kna0_%=\n" \
B_"v[116:119], v20, s[56:59], s12 offen offset:0 sc1\n" \
"L_kna0_%=:\n" \
"s_bitcmp1_b32 s20, 1\n" \
"s_cbranch_scc0 L_kna1_%=\n" \
B_"v[124:127], v20, s[56:59], s12 offen offset:2048 sc1\n" \
"L_kna1_%=:\n" \
"s_bitcmp1_b32 s20, 2\n" \
"s_cbranch_scc0 L_kna2_%=\n" \
B_"v[132:135], v20, s[56:59], s13 offen offset:0 sc1\n" \
"L_kna2_%=:\n" \
"s_bitcmp1_b32 s20, 3\n" \
"s_cbranch_scc0 L_kna3_%=\n" \
B_"v[140:143], v20, s[56:59], s13 offen offset:2048 sc1\n" \
"L_kna3_%=:\n" \
"s_bitcmp1_b32 s20, 4\n" \
"s_cbranch_scc0 L_kna4_%=\n" \
B_"v[148:151], v20, s[56:59], s14 offen offset:0 sc1\n" \
"L_kna4_%=:\n" \
"s_bitcmp1_b32 s20, 5\n" \
"s_cbranch_scc0 L_kna5_%=\n" \
B_"v[156:159], v20, s[56:59], s14 offen offset:2048 sc1\n" \
"L_kna5_%=:\n" \
"s_bitcmp1_b32 s20, 6\n" \
"s_cbranch_scc0 L_kna6_%=\n" \
B_"v[164:167], v20, s[56:59], s15 offen offset:0 sc1\n" \
"L_kna6_%=:\n" \
"s_waitcnt vmcnt(0)\n" \
"s_branch L_rna_%=\n" \
"L_snx_%=:\n" \
"buffer_load_dword v37, v40, s[56:59], s74 offen sc1\n" \
"s_add_u32 s101, s101, 1\n" \
"s_waitcnt vmcnt(0)\n" \
"v_cmp_eq_u32_e64 s[98:99], s62, v37\n" \
"s_cmp_gt_u32 s101, 0x100000\n" \
"s_cbranch_scc1 L_rnx_%=\n" \
"s_andn2_b32 s75, s21, s98\n" \
"s_cbranch_scc0 L_gnx_%=\n" \
"s_sleep 1\n" \
"s_branch L_snx_%=\n" \
"L_gnx_%=:\n" \
"s_bitcmp1_b32 s21, 16\n" \
"s_cbranch_scc0 L_knx16_%=\n" \
B_"v[112:115], v20, s[56:59], s16 offen offset:0 sc1\n" \
"L_knx16_%=:\n" \
"s_bitcmp1_b32 s21, 17\n" \
"s_cbranch_scc0 L_knx17_%=\n" \
B_"v[184:187], v20, s[56:59], s16 offen offset:2048 sc1\n" \
"L_knx17_%=:\n" \
"s_bitcmp1_b32 s21, 18\n" \
"s_cbranch_scc0 L_knx18_%=\n" \
B_"v[120:123], v20, s[56:59], s17 offen offset:0 sc1\n" \
"L_knx18_%=:\n" \
"s_bitcmp1_b32 s21, 19\n" \
"s_cbranch_scc0 L_knx19_%=\n" \
B_"v[192:195], v20, s[56:59], s17 offen offset:2048 sc1\n" \
"L_knx19_%=:\n" \
"s_bitcmp1_b32 s21, 20\n" \
"s_cbranch_scc0 L_knx20_%=\n" \
B_"v[128:131], v20, s[56:59], s18 offen offset:0 sc1\n" \
"L_knx20_%=:\n" \
"s_bitcmp1_b32 s21, 21\n" \
"s_cbranch_scc0 L_knx21_%=\n" \
B_"v[200:203], v20, s[56:59], s18 offen offset:2048 sc1\n" \
"L_knx21_%=:\n" \
"s_bitcmp1_b32 s21, 22\n" \
"s_cbranch_scc0 L_knx22_%=\n" \
B_"v[176:179], v20, s[56:59], s19 offen offset:0 sc1\n" \
"L_knx22_%=:\n" \
"s_bitcmp1_b32 s21, 23\n" \
"s_cbranch_scc0 L_knx23_%=\n" \
B_"v[248:251], v20, s[56:59], s19 offen offset:2048 sc1\n" \
"L_knx23_%=:\n" \
"s_bitcmp1_b32 s21, 8\n" \
"s_cbranch_scc0 L_knx8_%=\n" \
B_"v[188:191], v20, s[56:59], s12 offen offset:1024 sc1\n" \
"L_knx8_%=:\n" \
"s_bitcmp1_b32 s21, 9\n" \
"s_cbranch_scc0 L_knx9_%=\n" \
B_"v[196:199], v20, s[56:59], s12 offen offset:3072 sc1\n" \
"L_knx9_%=:\n" \
"s_waitcnt vmcnt(0)\n" \
"s_branch L_rnx_%=\n" \
"L_sx0_%=:\n" \
"s_sleep 1\n" \
"buffer_load_dword v37, v40, s[56:59], s74 offen sc1\n" \
"s_add_u32 s101, s101, 1\n" \
"s_waitcnt vmcnt(0)\n" \
"v_cmp_eq_u32_e64 s[98:99], s62, v37\n" \
"s_cmp_gt_u32 s101, 0x100000\n" \
"s_cbranch_scc1 L_rx0_%=\n" \
"s_andn2_b32 s75, 0x400, s98\n" \
"s_cbranch_scc1 L_sx0_%=\n" \
"s_branch L_rx0_%=\n" \
"L_sx1_%=:\n" \
"s_sleep 1\n" \
"buffer_load_dword v37, v40, s[56:59], s74 offen sc1\n" \
"s_add_u32 s101, s101, 1\n" \
"s_waitcnt vmcnt(0)\n" \
"v_cmp_eq_u32_e64 s[98:99], s62, v37\n" \
"s_cmp_gt_u32 s101, 0x100000\n" \
"s_cbranch_scc1 L_rx1_%=\n" \
"s_andn2_b32 s75, 0x800, s98\n" \
"s_cbranch_scc1 L_sx1_%=\n" \
"s_branch L_rx1_%=\n" \
"L_sx2_%=:\n" \
"s_sleep 1\n" \
"buffer_load_dword v37, v40, s[56:59], s74 offen sc1\n" \
"s_add_u32 s101, s101, 1\n" \
"s_waitcnt vmcnt(0)\n" \
"v_cmp_eq_u32_e64 s[98:99], s62, v37\n" \
"s_cmp_gt_u32 s101, 0x100000\n" \
"s_cbranch_scc1 L_rx2_%=\n" \
"s_andn2_b32 s75, 0x1000, s98\n" \
"s_cbranch_scc1 L_sx2_%=\n" \
"s_branch L_rx2_%=\n" \
"L_sx3_%=:\n" \
"s_sleep 1\n" \
"buffer_load_dword v37, v40, s[56:59], s74 offen sc1\n" \
"s_add_u32 s101, s101, 1\n" \
"s_waitcnt vmcnt(0)\n" \
"v_cmp_eq_u32_e64 s[98:99], s62, v37\n" \
"s_cmp_gt_u32 s101, 0x100000\n" \
"s_cbranch_scc1 L_rx3_%=\n" \
"s_andn2_b32 s75, 0x2000, s98\n" \
"s_cbranch_scc1 L_sx3_%=\n" \
"s_branch L_rx3_%=\n" \
"L_sx4_%=:\n" \
"s_sleep 1\n" \
"buffer_load_dword v37, v40, s[56:59], s74 offen sc1\n" \
"s_add_u32 s101, s101, 1\n" \
"s_waitcnt vmcnt(0)\n" \
"v_cmp_eq_u32_e64 s[98:99], s62, v37\n" \
"s_cmp_gt_u32 s101, 0x100000\n" \
"s_cbranch_scc1 L_rx4_%=\n" \
"s_andn2_b32 s75, 0x4000, s98\n" \
"s_cbranch_scc1 L_sx4_%=\n" \
"s_branch L_rx4_%=\n" \
"L_att_stub:\n" \
"s_mov_b32 s101, 0\n" \
"v_add_u32_e32 v32, 0x49000, v20\n" \
"v_readlane_b32 s96, v11, 39\n" \
"v_readlane_b32 s97, v11, 40\n" \
B_"a[192:195], v20, s[56:59], s60 offen offset:0 sc1\n" \
B_"a[208:211], v32, s[56:59], s60 offen offset:0 sc1\n" \
B_"a[196:199], v20, s[56:59], s60 offen offset:1024 sc1\n" \
B_"a[212:215], v32, s[56:59], s60 offen offset:1024 sc1\n" \
B_"a[200:203], v20, s[56:59], s60 offen offset:2048 sc1\n" \
B_"a[216:219], v32, s[56:59], s60 offen offset:2048 sc1\n" \
B_"a[204:207], v20, s[56:59], s60 offen offset:3072 sc1\n" \
B_"a[220:223], v32, s[56:59], s60 offen offset:3072 sc1\n" \
B_"a[124:127], v20, s[56:59], s96 offen offset:0 sc1\n" \
B_"a[228:231], v32, s[56:59], s96 offen offset:0 sc1\n" \
B_"a[236:239], v20, s[56:59], s97 offen offset:0 sc1\n" \
B_"a[244:247], v32, s[56:59], s97 offen offset:0 sc1\n" \
B_"a[224:227], v20, s[56:59], s96 offen offset:1024 sc1\n" \
B_"a[232:235], v32, s[56:59], s96 offen offset:1024 sc1\n" \
B_"a[240:243], v20, s[56:59], s97 offen offset:1024 sc1\n" \
B_"a[248:251], v32, s[56:59], s97 offen offset:1024 sc1\n" \
B_"v[64:67], v20, s[56:59], s61 offen offset:0 sc1\n" \
B_"v[68:71], v20, s[56:59], s61 offen offset:1024 sc1\n" \
B_"v[72:75], v20, s[56:59], s61 offen offset:2048 sc1\n" \
B_"v[76:79], v20, s[56:59], s61 offen offset:3072 sc1\n" \
B_"v[80:83], v32, s[56:59], s61 offen offset:0 sc1\n" \
B_"v[84:87], v32, s[56:59], s61 offen offset:1024 sc1\n" \
B_"v[96:99], v32, s[56:59], s61 offen offset:2048 sc1\n" \
B_"v[100:103], v32, s[56:59], s61 offen offset:3072 sc1\n" \
"v_not_b32_e32 v26, s62\n" \
"s_waitcnt vmcnt(0)\n" \
"s_branch L_out_%=\n" \
".fill (L_ab + 16384 - .) / 4, 4, 0xBF800000\n" \
".fill (L_ab + 16384 - .) / 4, 4, 0xBF800000\n" \
"L_ap4:\n" \
"s_setpc_b64 s[88:89]\n" \
"L_gsw:\n" \
M_"v[104:107], a[128:131], a[16:19], v[36:39]\n" \
"s_add_u32 s68, s60, 0xfff6e000\n" \
"s_add_u32 s69, s60, 0xfffb7000\n" \
"s_add_u32 s70, s60, 0x0\n" \
"s_add_u32 s71, s60, 0x49000\n" \
M_"v[104:107], a[132:135], a[20:23], v[104:107]\n" \
"s_xor_b32 m0, s65, 0x12000\n" \
R_"a[160:163], v49 offset:0\n" \
M_"v[104:107], a[136:139], a[24:27], v[104:107]\n" \
B_"v50, s[8:11], s64 offen offset:0 sc1 lds\n" \
M_"v[104:107], a[140:143], a[28:31], v[104:107]\n" \
R_"a[164:167], v49 offset:1024\n" \
R_"a[168:171], v49 offset:2048\n" \
M_"v[108:111], a[156:159], a[28:31], v[36:39]\n" \
R_"a[172:175], v49 offset:3072\n" \
R_"a[176:179], v49 offset:4096\n" \
M_"v[108:111], a[152:155], a[24:27], v[108:111]\n" \
B_"v50, s[8:11], s64 offen offset:1024 sc1 lds\n" \
M_"v[108:111], a[148:151], a[20:23], v[108:111]\n" \
"s_mov_b32 s65, s13\n" \
"v_add_u32_e32 v63, s15, v20\n" \
M_"v[108:111], a[144:147], a[16:19], v[108:111]\n" \
"s_add_u32 s67, s14, 40960\n" \
"v_add_u32_e32 v50, 0x6000, v63\n" \
M_"v[116:119], a[144:147], a[32:35], v[40:43]\n" \
"s_xor_b32 m0, s65, 0x8000\n" \
R_"a[180:183], v49 offset:5120\n" \
M_"v[116:119], a[148:151], a[36:39], v[116:119]\n" \
R_"a[184:187], v49 offset:6144\n" \
"v_and_b32_e32 v68, v12, v68\n" \
M_"v[116:119], a[152:155], a[40:43], v[116:119]\n" \
"v_and_b32_e32 v69, v13, v69\n" \
"v_and_b32_e32 v70, v14, v70\n" \
M_"v[116:119], a[156:159], a[44:47], v[116:119]\n" \
B_"v50, s[8:11], s67 offen offset:0 sc1 lds\n" \
M_"v[112:115], a[140:143], a[44:47], v[40:43]\n" \
"v_and_b32_e32 v71, v15, v71\n" \
R_"a[188:191], v49 offset:7168\n" \
M_"v[112:115], a[136:139], a[40:43], v[112:115]\n" \
E_"v86, v86\n" \
M_"v[112:115], a[132:135], a[36:39], v[112:115]\n" \
E_"v87, v87\n" \
M_"v[112:115], a[128:131], a[32:35], v[112:115]\n" \
E_"v84, v84\n" \
M_"v[120:123], a[128:131], a[48:51], v[44:47]\n" \
B_"v50, s[8:11], s67 offen offset:1024 sc1 lds\n" \
M_"v[120:123], a[132:135], a[52:55], v[120:123]\n" \
E_"v85, v85\n" \
M_"v[120:123], a[136:139], a[56:59], v[120:123]\n" \
C_"v87, v86, v87\n" \
C_"v86, v84, v85\n" \
M_"v[120:123], a[140:143], a[60:63], v[120:123]\n" \
E_"v82, v82\n" \
M_"v[124:127], a[156:159], a[60:63], v[44:47]\n" \
E_"v83, v83\n" \
M_"v[124:127], a[152:155], a[56:59], v[124:127]\n" \
E_"v80, v80\n" \
M_"v[124:127], a[148:151], a[52:55], v[124:127]\n" \
B_"v50, s[8:11], s67 offen offset:2048 sc1 lds\n" \
M_"v[124:127], a[144:147], a[48:51], v[124:127]\n" \
C_"v85, v82, v83\n" \
"s_waitcnt lgkmcnt(1)\n" \
M_"v[128:131], a[160:163], v[68:71], v[128:131]\n" \
E_"v81, v81\n" \
M_"v[160:163], a[160:163], v[72:75], v[160:163]\n" \
C_"v84, v80, v81\n" \
"s_mov_b32 s64, s14\n" \
M_"v[132:135], a[164:167], v[68:71], v[132:135]\n" \
E_"v88, v88\n" \
M_"v[164:167], a[164:167], v[72:75], v[164:167]\n" \
E_"v89, v89\n" \
M_"v[136:139], a[168:171], v[68:71], v[136:139]\n" \
B_"v50, s[8:11], s67 offen offset:3072 sc1 lds\n" \
M_"v[168:171], a[168:171], v[72:75], v[168:171]\n" \
E_"v90, v90\n" \
M_"v[140:143], a[172:175], v[68:71], v[140:143]\n" \
E_"v91, v91\n" \
M_"v[172:175], a[172:175], v[72:75], v[172:175]\n" \
C_"v88, v88, v89\n" \
C_"v89, v90, v91\n" \
M_"v[144:147], a[176:179], v[68:71], v[144:147]\n" \
E_"v92, v92\n" \
M_"v[176:179], a[176:179], v[72:75], v[176:179]\n" \
E_"v93, v93\n" \
M_"v[148:151], a[180:183], v[68:71], v[148:151]\n" \
E_"v94, v94\n" \
M_"v[180:183], a[180:183], v[72:75], v[180:183]\n" \
E_"v95, v95\n" \
M_"v[152:155], a[184:187], v[68:71], v[152:155]\n" \
C_"v90, v92, v93\n" \
C_"v91, v94, v95\n" \
M_"v[184:187], a[184:187], v[72:75], v[184:187]\n" \
"v_add_u32_e32 v51, 0x12000, v63\n" \
"s_waitcnt lgkmcnt(0)\n" \
M_"v[156:159], a[188:191], v[68:71], v[156:159]\n" \
"s_xor_b32 m0, s65, 0xa000\n" \
"ds_read_b32 v58, v18 offset:512\n" \
M_"v[188:191], a[188:191], v[72:75], v[188:191]\n" \
B_"v51, s[8:11], s64 offen offset:0 sc1 lds\n" \
"v_smfmac_f32_16x16x64_bf16 a[96:99], a[104:107], v[68:75], v21\n" \
B_"v51, s[8:11], s64 offen offset:1024 sc1 lds\n" \
M_"v[220:223], a[188:191], v[84:87], v[220:223]\n" \
"ds_read_b32 v59, v18 offset:640\n" \
"ds_read_b32 v60, v18 offset:768\n" \
M_"v[252:255], a[188:191], v[88:91], v[252:255]\n" \
"ds_read_b32 v61, v18 offset:896\n" \
R_"a[128:131], v48 offset:0\n" \
M_"v[216:219], a[184:187], v[84:87], v[216:219]\n" \
R_"a[144:147], v48 offset:4096\n" \
R_"a[132:135], v48 offset:1024\n" \
M_"v[248:251], a[184:187], v[88:91], v[248:251]\n" \
R_"a[148:151], v48 offset:5120\n" \
R_"a[136:139], v48 offset:2048\n" \
M_"v[212:215], a[180:183], v[84:87], v[212:215]\n" \
R_"a[152:155], v48 offset:6144\n" \
R_"a[140:143], v48 offset:3072\n" \
M_"v[244:247], a[180:183], v[88:91], v[244:247]\n" \
R_"a[156:159], v48 offset:7168\n" \
"v_add_u32_e32 v52, 0x14000, v63\n" \
M_"v[208:211], a[176:179], v[84:87], v[208:211]\n" \
B_"v51, s[8:11], s64 offen offset:2048 sc1 lds\n" \
M_"v[240:243], a[176:179], v[88:91], v[240:243]\n" \
E_"v104, v104\n" \
M_"v[204:207], a[172:175], v[84:87], v[204:207]\n" \
E_"v105, v105\n" \
M_"v[236:239], a[172:175], v[88:91], v[236:239]\n" \
B_"v51, s[8:11], s64 offen offset:3072 sc1 lds\n" \
M_"v[200:203], a[168:171], v[84:87], v[200:203]\n" \
E_"v106, v106\n" \
M_"v[232:235], a[168:171], v[88:91], v[232:235]\n" \
E_"v107, v107\n" \
M_"v[196:199], a[164:167], v[84:87], v[196:199]\n" \
C_"v104, v104, v105\n" \
C_"v105, v106, v107\n" \
M_"v[228:231], a[164:167], v[88:91], v[228:231]\n" \
E_"v108, v108\n" \
M_"v[192:195], a[160:163], v[84:87], v[192:195]\n" \
E_"v109, v109\n" \
M_"v[224:227], a[160:163], v[88:91], v[224:227]\n" \
E_"v110, v110\n" \
"v_smfmac_f32_16x16x64_bf16 a[100:103], a[104:107], v[84:91], v21\n" \
E_"v111, v111\n" \
C_"v106, v108, v109\n" \
C_"v107, v110, v111\n" \
"v_and_b32_e32 v104, v12, v104\n" \
"v_and_b32_e32 v105, v13, v105\n" \
"v_and_b32_e32 v106, v14, v106\n" \
"v_and_b32_e32 v107, v15, v107\n" \
"v_accvgpr_read_b32 v54, a96\n" \
"v_accvgpr_read_b32 v22, a97\n" \
"v_add_u32_e32 v53, 0x20000, v63\n" \
E_"v118, v118\n" \
E_"v119, v119\n" \
E_"v116, v116\n" \
E_"v117, v117\n" \
C_"v119, v118, v119\n" \
E_"v114, v114\n" \
E_"v115, v115\n" \
E_"v112, v112\n" \
E_"v113, v113\n" \
C_"v118, v116, v117\n" \
C_"v117, v114, v115\n" \
C_"v116, v112, v113\n" \
"v_readlane_b32 s93, v11, 22\n" \
"s_waitcnt vmcnt(10) lgkmcnt(0)\n" \
"s_barrier\n" \
M_"v[80:83], a[128:131], a[32:35], v[40:43]\n" \
R_"a[160:163], v49 offset:8192\n" \
R_"a[164:167], v49 offset:9216\n" \
M_"v[80:83], a[132:135], a[36:39], v[80:83]\n" \
R_"a[168:171], v49 offset:10240\n" \
"s_xor_b32 m0, s65, 0x18000\n" \
M_"v[80:83], a[136:139], a[40:43], v[80:83]\n" \
B_"v52, s[8:11], s64 offen offset:0 sc1 lds\n" \
M_"v[80:83], a[140:143], a[44:47], v[80:83]\n" \
R_"a[172:175], v49 offset:11264\n" \
R_"a[176:179], v49 offset:12288\n" \
M_"v[84:87], a[156:159], a[44:47], v[40:43]\n" \
R_"a[180:183], v49 offset:13312\n" \
R_"a[184:187], v49 offset:14336\n" \
M_"v[84:87], a[152:155], a[40:43], v[84:87]\n" \
B_"v52, s[8:11], s64 offen offset:1024 sc1 lds\n" \
M_"v[84:87], a[148:151], a[36:39], v[84:87]\n" \
R_"a[188:191], v49 offset:15360\n" \
"v_readlane_b32 s80, v11, 11\n" \
M_"v[84:87], a[144:147], a[32:35], v[84:87]\n" \
B_"v52, s[8:11], s64 offen offset:2048 sc1 lds\n" \
M_"v[92:95], a[144:147], a[48:51], v[44:47]\n" \
E_"v120, v120\n" \
M_"v[92:95], a[148:151], a[52:55], v[92:95]\n" \
E_"v121, v121\n" \
M_"v[92:95], a[152:155], a[56:59], v[92:95]\n" \
B_"v52, s[8:11], s64 offen offset:3072 sc1 lds\n" \
M_"v[92:95], a[156:159], a[60:63], v[92:95]\n" \
"s_mov_b32 m0, s65\n" \
C_"v120, v120, v121\n" \
M_"v[88:91], a[140:143], a[60:63], v[44:47]\n" \
E_"v122, v122\n" \
M_"v[88:91], a[136:139], a[56:59], v[88:91]\n" \
B_"v53, s[8:11], s64 offen offset:0 sc1 lds\n" \
M_"v[88:91], a[132:135], a[52:55], v[88:91]\n" \
E_"v123, v123\n" \
M_"v[88:91], a[128:131], a[48:51], v[88:91]\n" \
E_"v124, v124\n" \
M_"a[96:99], a[116:119], v[104:107], a[96:99]\n" \
C_"v121, v122, v123\n" \
"s_waitcnt lgkmcnt(5)\n" \
M_"v[160:163], a[160:163], v[104:107], v[160:163]\n" \
B_"v53, s[8:11], s64 offen offset:1024 sc1 lds\n" \
M_"v[164:167], a[164:167], v[104:107], v[164:167]\n" \
E_"v125, v125\n" \
M_"v[168:171], a[168:171], v[104:107], v[168:171]\n" \
C_"v122, v124, v125\n" \
"s_waitcnt lgkmcnt(1)\n" \
M_"v[172:175], a[172:175], v[104:107], v[172:175]\n" \
E_"v126, v126\n" \
M_"v[176:179], a[176:179], v[104:107], v[176:179]\n" \
E_"v127, v127\n" \
M_"v[180:183], a[180:183], v[104:107], v[180:183]\n" \
C_"v123, v126, v127\n" \
"v_readlane_b32 s81, v11, 12\n" \
M_"v[184:187], a[184:187], v[104:107], v[184:187]\n" \
"s_mov_b32 s82, 0x47fca0\n" \
"s_waitcnt lgkmcnt(0)\n" \
M_"v[188:191], a[188:191], v[104:107], v[188:191]\n" \
B_"v53, s[8:11], s64 offen offset:2048 sc1 lds\n" \
M_"v[220:223], a[188:191], v[116:119], v[220:223]\n" \
B_"v53, s[8:11], s64 offen offset:3072 sc1 lds\n" \
M_"v[252:255], a[188:191], v[120:123], v[252:255]\n" \
"s_mov_b32 s83, 0x20000\n" \
"v_readlane_b32 s92, v11, 29\n" \
M_"v[216:219], a[184:187], v[116:119], v[216:219]\n" \
"s_add_u32 m0, s93, 0x0\n" \
R_"a[128:131], v48 offset:8192\n" \
M_"v[248:251], a[184:187], v[120:123], v[248:251]\n" \
R_"a[144:147], v48 offset:12288\n" \
R_"a[132:135], v48 offset:9216\n" \
M_"v[212:215], a[180:183], v[116:119], v[212:215]\n" \
B_"v0, s[80:83], s92 offen offset:0 lds\n" \
M_"v[244:247], a[180:183], v[120:123], v[244:247]\n" \
B_"v1, s[80:83], s92 offen offset:1024 lds\n" \
M_"v[208:211], a[176:179], v[116:119], v[208:211]\n" \
R_"a[148:151], v48 offset:13312\n" \
R_"a[136:139], v48 offset:10240\n" \
M_"v[240:243], a[176:179], v[120:123], v[240:243]\n" \
R_"a[152:155], v48 offset:14336\n" \
R_"a[140:143], v48 offset:11264\n" \
M_"v[204:207], a[172:175], v[116:119], v[204:207]\n" \
B_"v2, s[80:83], s92 offen offset:2048 lds\n" \
M_"v[236:239], a[172:175], v[120:123], v[236:239]\n" \
R_"a[156:159], v48 offset:15360\n" \
"s_xor_b32 s65, s65, 0x8000\n" \
M_"v[200:203], a[168:171], v[116:119], v[200:203]\n" \
"s_add_u32 s64, s64, 0x10000\n" \
"v_add_u32_e32 v49, 0x10000, v48\n" \
M_"v[232:235], a[168:171], v[120:123], v[232:235]\n" \
B_"v3, s[80:83], s92 offen offset:3072 lds\n" \
M_"v[196:199], a[164:167], v[116:119], v[196:199]\n" \
E_"v86, v86\n" \
M_"v[228:231], a[164:167], v[120:123], v[228:231]\n" \
E_"v87, v87\n" \
M_"v[192:195], a[160:163], v[116:119], v[192:195]\n" \
E_"v84, v84\n" \
M_"v[224:227], a[160:163], v[120:123], v[224:227]\n" \
E_"v85, v85\n" \
"v_smfmac_f32_16x16x64_bf16 a[100:103], a[104:107], v[116:123], v21\n" \
E_"v88, v88\n" \
E_"v89, v89\n" \
C_"v87, v86, v87\n" \
E_"v82, v82\n" \
E_"v83, v83\n" \
E_"v80, v80\n" \
E_"v81, v81\n" \
E_"v90, v90\n" \
E_"v91, v91\n" \
C_"v86, v84, v85\n" \
C_"v88, v88, v89\n" \
E_"v92, v92\n" \
E_"v93, v93\n" \
E_"v94, v94\n" \
E_"v95, v95\n" \
C_"v85, v82, v83\n" \
C_"v84, v80, v81\n" \
C_"v89, v90, v91\n" \
"v_and_b32_e32 v84, v12, v84\n" \
"v_and_b32_e32 v85, v13, v85\n" \
"v_and_b32_e32 v86, v14, v86\n" \
"v_and_b32_e32 v87, v15, v87\n" \
C_"v90, v92, v93\n" \
C_"v91, v94, v95\n" \
"v_add_u32_e32 v48, s16, v20\n" \
"v_add_f32_e32 v54, v54, v22\n" \
"v_accvgpr_read_b32 v55, a98\n" \
"v_accvgpr_read_b32 v23, a99\n" \
M_"a[96:99], a[120:123], a[120:123], 0\n" \
"v_max_f32_e32 v62, v62, v54\n" \
"v_add_f32_e32 v55, v55, v23\n" \
"v_max_f32_e32 v62, v62, v55\n" \
"v_rcp_f32_e32 v54, v54\n" \
"v_rcp_f32_e32 v55, v55\n" \
"v_mul_f32_e32 v54, v54, v58\n" \
"v_mul_f32_e32 v55, v55, v59\n" \
"v_mul_f32_e32 v128, v54, v128\n" \
"v_mul_f32_e32 v129, v54, v129\n" \
"v_mul_f32_e32 v136, v54, v136\n" \
"v_mul_f32_e32 v137, v54, v137\n" \
"v_mul_f32_e32 v144, v54, v144\n" \
"v_mul_f32_e32 v145, v54, v145\n" \
"v_mul_f32_e32 v152, v54, v152\n" \
"v_mul_f32_e32 v153, v54, v153\n" \
"v_mul_f32_e32 v160, v55, v160\n" \
"v_mul_f32_e32 v161, v55, v161\n" \
"v_mul_f32_e32 v168, v55, v168\n" \
"v_mul_f32_e32 v169, v55, v169\n" \
"v_mul_f32_e32 v176, v55, v176\n" \
"v_mul_f32_e32 v177, v55, v177\n" \
"v_mul_f32_e32 v184, v55, v184\n" \
"v_mul_f32_e32 v185, v55, v185\n" \
C_"v128, v128, v129\n" \
"v_mul_f32_e32 v130, v54, v130\n" \
"v_mul_f32_e32 v131, v54, v131\n" \
C_"v136, v136, v137\n" \
"v_mul_f32_e32 v138, v54, v138\n" \
"v_mul_f32_e32 v139, v54, v139\n" \
C_"v144, v144, v145\n" \
"v_mul_f32_e32 v146, v54, v146\n" \
"v_mul_f32_e32 v147, v54, v147\n" \
C_"v152, v152, v153\n" \
"v_mul_f32_e32 v154, v54, v154\n" \
"v_mul_f32_e32 v155, v54, v155\n" \
C_"v160, v160, v161\n" \
"v_mul_f32_e32 v162, v55, v162\n" \
"v_mul_f32_e32 v163, v55, v163\n" \
C_"v168, v168, v169\n" \
"v_mul_f32_e32 v170, v55, v170\n" \
"v_mul_f32_e32 v171, v55, v171\n" \
C_"v176, v176, v177\n" \
"v_mul_f32_e32 v178, v55, v178\n" \
"v_mul_f32_e32 v179, v55, v179\n" \
C_"v184, v184, v185\n" \
"v_mul_f32_e32 v186, v55, v186\n" \
"v_mul_f32_e32 v187, v55, v187\n" \
C_"v129, v130, v131\n" \
"v_mul_f32_e32 v132, v54, v132\n" \
"v_mul_f32_e32 v133, v54, v133\n" \
"v_mul_f32_e32 v134, v54, v134\n" \
"v_mul_f32_e32 v135, v54, v135\n" \
C_"v137, v138, v139\n" \
"v_mul_f32_e32 v140, v54, v140\n" \
"s_waitcnt vmcnt(12) lgkmcnt(0)\n" \
"s_barrier\n" \
M_"v[120:123], a[128:131], a[48:51], v[44:47]\n" \
"s_xor_b32 m0, s65, 0x12000\n" \
R_"a[188:191], v49 offset:7168\n" \
M_"v[120:123], a[132:135], a[52:55], v[120:123]\n" \
B_"v50, s[8:11], s64 offen offset:0 sc1 lds\n" \
M_"v[120:123], a[136:139], a[56:59], v[120:123]\n" \
R_"a[184:187], v49 offset:6144\n" \
R_"a[180:183], v49 offset:5120\n" \
M_"v[120:123], a[140:143], a[60:63], v[120:123]\n" \
B_"v50, s[8:11], s64 offen offset:1024 sc1 lds\n" \
M_"v[124:127], a[156:159], a[60:63], v[44:47]\n" \
R_"a[176:179], v49 offset:4096\n" \
R_"a[172:175], v49 offset:3072\n" \
M_"v[124:127], a[152:155], a[56:59], v[124:127]\n" \
R_"a[168:171], v49 offset:2048\n" \
R_"a[164:167], v49 offset:1024\n" \
M_"v[124:127], a[148:151], a[52:55], v[124:127]\n" \
R_"a[160:163], v49 offset:0\n" \
R_"a[0:3], v49 offset:8192\n" \
M_"v[124:127], a[144:147], a[48:51], v[124:127]\n" \
B_"v50, s[8:11], s64 offen offset:2048 sc1 lds\n" \
M_"v[32:35], a[124:127], a[64:67], 0\n" \
R_"a[4:7], v49 offset:9216\n" \
R_"a[8:11], v49 offset:10240\n" \
M_"v[36:39], a[124:127], a[80:83], 0\n" \
R_"a[12:15], v49 offset:11264\n" \
R_"a[16:19], v49 offset:12288\n" \
M_"v[40:43], a[124:127], a[192:195], 0\n" \
R_"a[20:23], v49 offset:13312\n" \
R_"a[24:27], v49 offset:14336\n" \
M_"v[44:47], a[124:127], a[208:211], 0\n" \
B_"v50, s[8:11], s64 offen offset:3072 sc1 lds\n" \
M_"v[32:35], a[224:227], a[68:71], v[32:35]\n" \
"s_xor_b32 m0, s65, 0xa000\n" \
R_"a[28:31], v49 offset:15360\n" \
M_"v[36:39], a[224:227], a[84:87], v[36:39]\n" \
E_"v120, v120\n" \
M_"v[40:43], a[224:227], a[196:199], v[40:43]\n" \
B_"v51, s[8:11], s64 offen offset:0 sc1 lds\n" \
M_"v[44:47], a[224:227], a[212:215], v[44:47]\n" \
E_"v121, v121\n" \
M_"v[32:35], a[228:231], a[72:75], v[32:35]\n" \
E_"v122, v122\n" \
M_"v[36:39], a[228:231], a[88:91], v[36:39]\n" \
E_"v123, v123\n" \
M_"v[40:43], a[228:231], a[200:203], v[40:43]\n" \
B_"v51, s[8:11], s64 offen offset:1024 sc1 lds\n" \
M_"v[44:47], a[228:231], a[216:219], v[44:47]\n" \
C_"v120, v120, v121\n" \
C_"v121, v122, v123\n" \
M_"v[32:35], a[232:235], a[76:79], v[32:35]\n" \
E_"v124, v124\n" \
M_"v[36:39], a[232:235], a[92:95], v[36:39]\n" \
E_"v125, v125\n" \
M_"v[40:43], a[232:235], a[204:207], v[40:43]\n" \
B_"v51, s[8:11], s64 offen offset:2048 sc1 lds\n" \
M_"v[44:47], a[232:235], a[220:223], v[44:47]\n" \
E_"v126, v126\n" \
"v_smfmac_f32_16x16x64_bf16 a[100:103], a[104:107], v[84:91], v21\n" \
C_"v122, v124, v125\n" \
"s_waitcnt lgkmcnt(1)\n" \
M_"v[220:223], a[188:191], v[84:87], v[220:223]\n" \
B_"v51, s[8:11], s64 offen offset:3072 sc1 lds\n" \
M_"v[252:255], a[188:191], v[88:91], v[252:255]\n" \
"s_add_u32 m0, s93, 0x1000\n" \
"v_and_b32_e32 v120, v12, v120\n" \
M_"v[216:219], a[184:187], v[84:87], v[216:219]\n" \
E_"v127, v127\n" \
M_"v[248:251], a[184:187], v[88:91], v[248:251]\n" \
C_"v123, v126, v127\n" \
"v_and_b32_e32 v121, v13, v121\n" \
M_"v[212:215], a[180:183], v[84:87], v[212:215]\n" \
B_"v4, s[80:83], s92 offen offset:0 lds\n" \
M_"v[244:247], a[180:183], v[88:91], v[244:247]\n" \
"v_and_b32_e32 v122, v14, v122\n" \
"v_and_b32_e32 v123, v15, v123\n" \
M_"v[208:211], a[176:179], v[84:87], v[208:211]\n" \
"v_accvgpr_read_b32 v56, a100\n" \
"v_accvgpr_read_b32 v24, a101\n" \
M_"v[240:243], a[176:179], v[88:91], v[240:243]\n" \
R_"a[128:131], v48 offset:0\n" \
R_"a[144:147], v48 offset:4096\n" \
M_"v[204:207], a[172:175], v[84:87], v[204:207]\n" \
B_"v5, s[80:83], s92 offen offset:1024 lds\n" \
M_"v[236:239], a[172:175], v[88:91], v[236:239]\n" \
R_"a[132:135], v48 offset:1024\n" \
R_"a[148:151], v48 offset:5120\n" \
M_"v[200:203], a[168:171], v[84:87], v[200:203]\n" \
R_"a[136:139], v48 offset:2048\n" \
R_"a[152:155], v48 offset:6144\n" \
M_"v[232:235], a[168:171], v[88:91], v[232:235]\n" \
B_"v6, s[80:83], s92 offen offset:2048 lds\n" \
M_"v[196:199], a[164:167], v[84:87], v[196:199]\n" \
R_"a[140:143], v48 offset:3072\n" \
R_"a[156:159], v48 offset:7168\n" \
M_"v[228:231], a[164:167], v[88:91], v[228:231]\n" \
"v_mul_f32_e32 v141, v54, v141\n" \
"v_mul_f32_e32 v142, v54, v142\n" \
M_"v[192:195], a[160:163], v[84:87], v[192:195]\n" \
"v_mul_f32_e32 v143, v54, v143\n" \
C_"v145, v146, v147\n" \
M_"v[224:227], a[160:163], v[88:91], v[224:227]\n" \
B_"v7, s[80:83], s92 offen offset:3072 lds\n" \
M_"a[100:103], a[116:119], v[120:123], a[100:103]\n" \
"v_mul_f32_e32 v148, v54, v148\n" \
"v_mul_f32_e32 v149, v54, v149\n" \
M_"v[224:227], a[0:3], v[120:123], v[224:227]\n" \
"v_mul_f32_e32 v150, v54, v150\n" \
"v_mul_f32_e32 v151, v54, v151\n" \
M_"v[228:231], a[4:7], v[120:123], v[228:231]\n" \
C_"v153, v154, v155\n" \
"v_mul_f32_e32 v156, v54, v156\n" \
M_"v[232:235], a[8:11], v[120:123], v[232:235]\n" \
"v_mul_f32_e32 v157, v54, v157\n" \
"v_mul_f32_e32 v158, v54, v158\n" \
M_"v[236:239], a[12:15], v[120:123], v[236:239]\n" \
"v_mul_f32_e32 v159, v54, v159\n" \
C_"v161, v162, v163\n" \
M_"v[240:243], a[16:19], v[120:123], v[240:243]\n" \
"v_mul_f32_e32 v164, v55, v164\n" \
"v_mul_f32_e32 v165, v55, v165\n" \
M_"v[244:247], a[20:23], v[120:123], v[244:247]\n" \
"v_mul_f32_e32 v166, v55, v166\n" \
"v_mul_f32_e32 v167, v55, v167\n" \
M_"v[248:251], a[24:27], v[120:123], v[248:251]\n" \
C_"v169, v170, v171\n" \
"s_waitcnt lgkmcnt(4)\n" \
M_"v[252:255], a[28:31], v[120:123], v[252:255]\n" \
"v_mul_f32_e32 v172, v55, v172\n" \
"v_mul_f32_e32 v173, v55, v173\n" \
"v_mul_f32_e32 v174, v55, v174\n" \
"v_mul_f32_e32 v175, v55, v175\n" \
C_"v177, v178, v179\n" \
"v_mul_f32_e32 v180, v55, v180\n" \
"v_mul_f32_e32 v181, v55, v181\n" \
"v_mul_f32_e32 v182, v55, v182\n" \
"v_mul_f32_e32 v183, v55, v183\n" \
C_"v185, v186, v187\n" \
"v_mul_f32_e32 v188, v55, v188\n" \
"v_mul_f32_e32 v189, v55, v189\n" \
"v_mul_f32_e32 v190, v55, v190\n" \
"v_mul_f32_e32 v191, v55, v191\n" \
C_"v130, v132, v133\n" \
C_"v131, v134, v135\n" \
"buffer_store_dwordx4 v[128:131], v20, s[56:59], s68 offen offset:0 sc1\n" \
C_"v138, v140, v141\n" \
C_"v139, v142, v143\n" \
"buffer_store_dwordx4 v[136:139], v20, s[56:59], s68 offen offset:1024 sc1\n" \
C_"v146, v148, v149\n" \
C_"v147, v150, v151\n" \
"buffer_store_dwordx4 v[144:147], v20, s[56:59], s68 offen offset:2048 sc1\n" \
C_"v154, v156, v157\n" \
C_"v155, v158, v159\n" \
"buffer_store_dwordx4 v[152:155], v20, s[56:59], s68 offen offset:3072 sc1\n" \
C_"v162, v164, v165\n" \
C_"v163, v166, v167\n" \
"buffer_store_dwordx4 v[160:163], v20, s[56:59], s69 offen offset:0 sc1\n" \
C_"v170, v172, v173\n" \
C_"v171, v174, v175\n" \
"buffer_store_dwordx4 v[168:171], v20, s[56:59], s69 offen offset:1024 sc1\n" \
C_"v178, v180, v181\n" \
C_"v179, v182, v183\n" \
"buffer_store_dwordx4 v[176:179], v20, s[56:59], s69 offen offset:2048 sc1\n" \
C_"v186, v188, v189\n" \
C_"v187, v190, v191\n" \
"buffer_store_dwordx4 v[184:187], v20, s[56:59], s69 offen offset:3072 sc1\n" \
"s_add_u32 m0, s93, 0x2000\n" \
M_"a[112:115], a[120:123], v[128:131], a[112:115]\n" \
B_"v8, s[80:83], s92 offen offset:0 lds\n" \
M_"a[112:115], a[120:123], v[136:139], a[112:115]\n" \
"v_mfma_f32_32x32x16_bf16 v[128:143], a[120:123], a[120:123], 0\n" \
M_"a[112:115], a[120:123], v[144:147], a[112:115]\n" \
M_"a[112:115], a[120:123], v[152:155], a[112:115]\n" \
"v_mfma_f32_32x32x16_bf16 v[144:159], a[120:123], a[120:123], 0\n" \
"v_add_f32_e32 v56, v56, v24\n" \
M_"a[112:115], a[120:123], v[160:163], a[112:115]\n" \
"v_max_f32_e32 v62, v62, v56\n" \
"v_accvgpr_read_b32 v57, a102\n" \
"s_waitcnt vmcnt(21) lgkmcnt(0)\n" \
"s_barrier\n" \
M_"v[64:67], a[128:131], a[64:67], v[32:35]\n" \
"s_xor_b32 m0, s65, 0x18000\n" \
"v_accvgpr_read_b32 v25, a103\n" \
M_"a[100:103], a[120:123], a[120:123], 0\n" \
B_"v52, s[8:11], s64 offen offset:0 sc1 lds\n" \
M_"v[64:67], a[132:135], a[68:71], v[64:67]\n" \
"v_rcp_f32_e32 v56, v56\n" \
M_"v[64:67], a[136:139], a[72:75], v[64:67]\n" \
M_"a[112:115], a[120:123], v[168:171], a[112:115]\n" \
"v_mfma_f32_32x32x16_bf16 v[160:175], a[120:123], a[120:123], 0\n" \
"v_mul_f32_e32 v56, v56, v60\n" \
M_"a[112:115], a[120:123], v[176:179], a[112:115]\n" \
"v_mul_f32_e32 v192, v56, v192\n" \
"v_mul_f32_e32 v193, v56, v193\n" \
M_"v[64:67], a[140:143], a[76:79], v[64:67]\n" \
B_"v52, s[8:11], s64 offen offset:1024 sc1 lds\n" \
M_"v[68:71], a[156:159], a[76:79], v[32:35]\n" \
C_"v192, v192, v193\n" \
"v_mul_f32_e32 v194, v56, v194\n" \
M_"v[68:71], a[152:155], a[72:75], v[68:71]\n" \
"v_mul_f32_e32 v195, v56, v195\n" \
C_"v193, v194, v195\n" \
M_"v[68:71], a[148:151], a[68:71], v[68:71]\n" \
M_"a[112:115], a[120:123], v[184:187], a[112:115]\n" \
"v_mfma_f32_32x32x16_bf16 v[176:191], a[120:123], a[120:123], 0\n" \
"v_mul_f32_e32 v196, v56, v196\n" \
"v_mul_f32_e32 v197, v56, v197\n" \
"v_mul_f32_e32 v198, v56, v198\n" \
"v_mul_f32_e32 v199, v56, v199\n" \
"v_mul_f32_e32 v200, v56, v200\n" \
"v_mul_f32_e32 v201, v56, v201\n" \
M_"v[68:71], a[144:147], a[64:67], v[68:71]\n" \
B_"v52, s[8:11], s64 offen offset:2048 sc1 lds\n" \
M_"v[76:79], a[144:147], a[80:83], v[36:39]\n" \
C_"v194, v196, v197\n" \
C_"v195, v198, v199\n" \
"buffer_store_dwordx4 v[192:195], v20, s[56:59], s70 offen offset:0 sc1\n" \
M_"v[76:79], a[148:151], a[84:87], v[76:79]\n" \
C_"v200, v200, v201\n" \
"v_mul_f32_e32 v202, v56, v202\n" \
M_"v[76:79], a[152:155], a[88:91], v[76:79]\n" \
"v_mul_f32_e32 v203, v56, v203\n" \
"v_add_f32_e32 v57, v57, v25\n" \
M_"v[76:79], a[156:159], a[92:95], v[76:79]\n" \
B_"v52, s[8:11], s64 offen offset:3072 sc1 lds\n" \
M_"v[72:75], a[140:143], a[92:95], v[36:39]\n" \
"s_mov_b32 m0, s65\n" \
C_"v201, v202, v203\n" \
M_"v[72:75], a[136:139], a[88:91], v[72:75]\n" \
M_"a[112:115], a[120:123], v[192:195], a[112:115]\n" \
"v_mul_f32_e32 v204, v56, v204\n" \
"v_mul_f32_e32 v205, v56, v205\n" \
M_"v[72:75], a[132:135], a[84:87], v[72:75]\n" \
"v_mul_f32_e32 v206, v56, v206\n" \
"v_mul_f32_e32 v207, v56, v207\n" \
M_"v[72:75], a[128:131], a[80:83], v[72:75]\n" \
B_"v53, s[8:11], s64 offen offset:0 sc1 lds\n" \
M_"v[80:83], a[128:131], a[192:195], v[40:43]\n" \
"v_mul_f32_e32 v208, v56, v208\n" \
"v_mul_f32_e32 v209, v56, v209\n" \
M_"v[80:83], a[132:135], a[196:199], v[80:83]\n" \
"v_max_f32_e32 v62, v62, v57\n" \
C_"v202, v204, v205\n" \
M_"v[80:83], a[136:139], a[200:203], v[80:83]\n" \
B_"v53, s[8:11], s64 offen offset:1024 sc1 lds\n" \
M_"v[80:83], a[140:143], a[204:207], v[80:83]\n" \
C_"v203, v206, v207\n" \
C_"v208, v208, v209\n" \
"buffer_store_dwordx4 v[200:203], v20, s[56:59], s70 offen offset:1024 sc1\n" \
M_"v[84:87], a[156:159], a[204:207], v[40:43]\n" \
"v_mul_f32_e32 v210, v56, v210\n" \
"v_mul_f32_e32 v211, v56, v211\n" \
M_"v[84:87], a[152:155], a[200:203], v[84:87]\n" \
"v_rcp_f32_e32 v57, v57\n" \
M_"v[84:87], a[148:151], a[196:199], v[84:87]\n" \
B_"v53, s[8:11], s64 offen offset:2048 sc1 lds\n" \
M_"v[84:87], a[144:147], a[192:195], v[84:87]\n" \
M_"a[112:115], a[120:123], v[200:203], a[112:115]\n" \
"v_mfma_f32_32x32x16_bf16 v[192:207], a[120:123], a[120:123], 0\n" \
C_"v209, v210, v211\n" \
"v_mul_f32_e32 v212, v56, v212\n" \
".fill (L_ab + 20480 - .) / 4, 4, 0xBF800000\n" \
"L_ap5:\n" \
"s_cbranch_execz L_ap0\n" \
"v_mul_f32_e32 v213, v56, v213\n" \
"v_mul_f32_e32 v214, v56, v214\n" \
"v_mul_f32_e32 v215, v56, v215\n" \
"v_mul_f32_e32 v216, v56, v216\n" \
M_"v[92:95], a[144:147], a[208:211], v[44:47]\n" \
R_"a[144:147], v48 offset:12288\n" \
"v_mul_f32_e32 v217, v56, v217\n" \
M_"v[92:95], a[148:151], a[212:215], v[92:95]\n" \
R_"a[148:151], v48 offset:13312\n" \
C_"v210, v212, v213\n" \
M_"v[92:95], a[152:155], a[216:219], v[92:95]\n" \
R_"a[152:155], v48 offset:14336\n" \
C_"v211, v214, v215\n" \
"buffer_store_dwordx4 v[208:211], v20, s[56:59], s70 offen offset:2048 sc1\n" \
M_"v[92:95], a[156:159], a[220:223], v[92:95]\n" \
R_"a[156:159], v48 offset:15360\n" \
C_"v216, v216, v217\n" \
M_"v[88:91], a[140:143], a[220:223], v[44:47]\n" \
R_"a[140:143], v48 offset:11264\n" \
"v_mul_f32_e32 v218, v56, v218\n" \
M_"v[88:91], a[136:139], a[216:219], v[88:91]\n" \
R_"a[136:139], v48 offset:10240\n" \
"v_mul_f32_e32 v219, v56, v219\n" \
M_"v[88:91], a[132:135], a[212:215], v[88:91]\n" \
R_"a[132:135], v48 offset:9216\n" \
"v_mul_f32_e32 v57, v57, v61\n" \
M_"v[88:91], a[128:131], a[208:211], v[88:91]\n" \
R_"a[128:131], v48 offset:8192\n" \
M_"a[112:115], a[120:123], v[208:211], a[112:115]\n" \
C_"v217, v218, v219\n" \
"v_mul_f32_e32 v220, v56, v220\n" \
"v_mul_f32_e32 v221, v56, v221\n" \
"v_mul_f32_e32 v222, v56, v222\n" \
"v_mul_f32_e32 v223, v56, v223\n" \
"v_mul_f32_e32 v224, v57, v224\n" \
"v_mul_f32_e32 v225, v57, v225\n" \
C_"v218, v220, v221\n" \
C_"v219, v222, v223\n" \
"buffer_store_dwordx4 v[216:219], v20, s[56:59], s70 offen offset:3072 sc1\n" \
C_"v224, v224, v225\n" \
"v_mul_f32_e32 v226, v57, v226\n" \
"v_mul_f32_e32 v227, v57, v227\n" \
M_"a[112:115], a[120:123], v[216:219], a[112:115]\n" \
"v_mfma_f32_32x32x16_bf16 v[208:223], a[120:123], a[120:123], 0\n" \
C_"v225, v226, v227\n" \
"v_mul_f32_e32 v228, v57, v228\n" \
"v_mul_f32_e32 v229, v57, v229\n" \
"v_mul_f32_e32 v230, v57, v230\n" \
"v_mul_f32_e32 v231, v57, v231\n" \
"v_mul_f32_e32 v232, v57, v232\n" \
"v_mul_f32_e32 v233, v57, v233\n" \
C_"v226, v228, v229\n" \
C_"v227, v230, v231\n" \
"buffer_store_dwordx4 v[224:227], v20, s[56:59], s71 offen offset:0 sc1\n" \
C_"v232, v232, v233\n" \
"v_mul_f32_e32 v234, v57, v234\n" \
"v_mul_f32_e32 v235, v57, v235\n" \
M_"a[112:115], a[120:123], v[224:227], a[112:115]\n" \
C_"v233, v234, v235\n" \
"v_mul_f32_e32 v236, v57, v236\n" \
"v_mul_f32_e32 v237, v57, v237\n" \
"v_mul_f32_e32 v238, v57, v238\n" \
"v_mul_f32_e32 v239, v57, v239\n" \
"v_mul_f32_e32 v240, v57, v240\n" \
"v_mul_f32_e32 v241, v57, v241\n" \
C_"v234, v236, v237\n" \
C_"v235, v238, v239\n" \
"buffer_store_dwordx4 v[232:235], v20, s[56:59], s71 offen offset:1024 sc1\n" \
C_"v240, v240, v241\n" \
"v_mul_f32_e32 v242, v57, v242\n" \
"v_mul_f32_e32 v243, v57, v243\n" \
M_"a[112:115], a[120:123], v[232:235], a[112:115]\n" \
"v_mfma_f32_32x32x16_bf16 v[224:239], a[120:123], a[120:123], 0\n" \
C_"v241, v242, v243\n" \
"v_mul_f32_e32 v244, v57, v244\n" \
"v_mul_f32_e32 v245, v57, v245\n" \
"v_mul_f32_e32 v246, v57, v246\n" \
"v_mul_f32_e32 v247, v57, v247\n" \
"v_mul_f32_e32 v248, v57, v248\n" \
"v_mul_f32_e32 v249, v57, v249\n" \
C_"v242, v244, v245\n" \
C_"v243, v246, v247\n" \
"buffer_store_dwordx4 v[240:243], v20, s[56:59], s71 offen offset:2048 sc1\n" \
C_"v248, v248, v249\n" \
"v_mul_f32_e32 v250, v57, v250\n" \
"v_mul_f32_e32 v251, v57, v251\n" \
M_"a[112:115], a[120:123], v[240:243], a[112:115]\n" \
C_"v249, v250, v251\n" \
"v_mul_f32_e32 v252, v57, v252\n" \
"v_mul_f32_e32 v253, v57, v253\n" \
"v_mul_f32_e32 v254, v57, v254\n" \
"v_mul_f32_e32 v255, v57, v255\n" \
C_"v250, v252, v253\n" \
C_"v251, v254, v255\n" \
"buffer_store_dwordx4 v[248:251], v20, s[56:59], s71 offen offset:3072 sc1\n" \
B_"v53, s[8:11], s64 offen offset:3072 sc1 lds\n" \
"s_waitcnt vmcnt(16) lgkmcnt(0)\n" \
"s_barrier\n" \
M_"a[112:115], a[120:123], v[248:251], a[112:115]\n" \
"v_mfma_f32_32x32x16_bf16 v[240:255], a[120:123], a[120:123], 0\n" \
E_"v70, v70\n" \
E_"v71, v71\n" \
E_"v72, v72\n" \
E_"v73, v73\n" \
"s_add_u32 s64, s64, 0x10000\n" \
"v_cmp_lt_f32_e64 s[72:73], s21, v62\n" \
"v_readlane_b32 s95, v11, 3\n" \
"v_accvgpr_read_b32 v63, a112\n" \
"v_cmp_u_f32_e32 vcc, v63, v63\n" \
E_"v68, v68\n" \
E_"v69, v69\n" \
E_"v74, v74\n" \
E_"v75, v75\n" \
"s_or_b64 vcc, s[72:73], vcc\n" \
"s_cselect_b32 s95, 0x40000000, s95\n" \
"s_cselect_b32 s94, 0x7f800000, 0\n" \
C_"v71, v70, v71\n" \
E_"v66, v66\n" \
E_"v67, v67\n" \
E_"v64, v64\n" \
E_"v65, v65\n" \
C_"v72, v72, v73\n" \
E_"v76, v76\n" \
E_"v77, v77\n" \
E_"v78, v78\n" \
E_"v79, v79\n" \
C_"v70, v68, v69\n" \
C_"v73, v74, v75\n" \
"v_add_u32_e32 v49, 0x10000, v48\n" \
"s_xor_b32 s65, s65, 0x8000\n" \
C_"v69, v66, v67\n" \
C_"v68, v64, v65\n" \
C_"v74, v76, v77\n" \
C_"v75, v78, v79\n" \
"v_xor_b32_e32 v48, 0x8000, v48\n" \
"s_sub_u32 s66, s19, 2\n" \
"s_branch L_2_%=\n" \
"L_f1_%=:\n" \
R_"a[160:163], v49 offset:0\n" \
R_"a[164:167], v49 offset:1024\n" \
R_"a[168:171], v49 offset:2048\n" \
M_"v[104:107], a[128:131], a[80:83], v[36:39]\n" \
C_"v192, v192, v193\n" \
C_"v224, v224, v225\n" \
C_"v193, v194, v195\n" \
C_"v225, v226, v227\n" \
R_"a[172:175], v49 offset:3072\n" \
C_"v194, v196, v197\n" \
C_"v195, v198, v199\n" \
C_"v226, v228, v229\n" \
C_"v227, v230, v231\n" \
M_"v[104:107], a[132:135], a[84:87], v[104:107]\n" \
"v_lshl_add_u32 v50, s15, 2, v20\n" \
C_"v196, v200, v201\n" \
C_"v197, v202, v203\n" \
C_"v198, v204, v205\n" \
C_"v199, v206, v207\n" \
C_"v228, v232, v233\n" \
C_"v229, v234, v235\n" \
C_"v230, v236, v237\n" \
C_"v231, v238, v239\n" \
"v_add_u32_e32 v50, 0x1a000, v50\n" \
"v_mov_b32_e32 v61, s62\n" \
C_"v200, v208, v209\n" \
C_"v201, v210, v211\n" \
C_"v202, v212, v213\n" \
C_"v203, v214, v215\n" \
C_"v204, v216, v217\n" \
C_"v205, v218, v219\n" \
C_"v206, v220, v221\n" \
C_"v207, v222, v223\n" \
C_"v232, v240, v241\n" \
C_"v233, v242, v243\n" \
C_"v234, v244, v245\n" \
C_"v235, v246, v247\n" \
C_"v236, v248, v249\n" \
C_"v237, v250, v251\n" \
C_"v238, v252, v253\n" \
C_"v239, v254, v255\n" \
"ds_write_b128 v50, v[192:195] offset:0\n" \
M_"v[104:107], a[136:139], a[88:91], v[104:107]\n" \
R_"a[176:179], v49 offset:4096\n" \
"v_add_u32_e32 v56, 0x49000, v20\n" \
M_"v[104:107], a[140:143], a[92:95], v[104:107]\n" \
"ds_write_b128 v50, v[196:199] offset:1024\n" \
M_"v[108:111], a[156:159], a[92:95], v[36:39]\n" \
R_"a[180:183], v49 offset:5120\n" \
R_"a[184:187], v49 offset:6144\n" \
M_"v[108:111], a[152:155], a[88:91], v[108:111]\n" \
"ds_write_b128 v50, v[200:203] offset:2048\n" \
M_"v[108:111], a[148:151], a[84:87], v[108:111]\n" \
"v_and_b32_e32 v68, v12, v68\n" \
"v_and_b32_e32 v69, v13, v69\n" \
M_"v[108:111], a[144:147], a[80:83], v[108:111]\n" \
"ds_write_b128 v50, v[204:207] offset:3072\n" \
"v_and_b32_e32 v70, v14, v70\n" \
"v_and_b32_e32 v71, v15, v71\n" \
R_"a[188:191], v49 offset:7168\n" \
R_"a[0:3], v20 offset:8192\n" \
"s_waitcnt lgkmcnt(9)\n" \
M_"v[128:131], a[160:163], v[68:71], v[128:131]\n" \
"ds_write_b128 v50, v[224:227] offset:4096\n" \
M_"v[160:163], a[160:163], v[72:75], v[160:163]\n" \
R_"a[4:7], v20 offset:9216\n" \
R_"a[8:11], v20 offset:10240\n" \
M_"v[132:135], a[164:167], v[68:71], v[132:135]\n" \
"ds_write_b128 v50, v[228:231] offset:5120\n" \
M_"v[164:167], a[164:167], v[72:75], v[164:167]\n" \
R_"a[12:15], v20 offset:11264\n" \
R_"a[28:31], v20 offset:15360\n" \
M_"v[136:139], a[168:171], v[68:71], v[136:139]\n" \
"ds_write_b128 v50, v[232:235] offset:6144\n" \
M_"v[168:171], a[168:171], v[72:75], v[168:171]\n" \
R_"a[24:27], v20 offset:14336\n" \
R_"a[20:23], v20 offset:13312\n" \
M_"v[140:143], a[172:175], v[68:71], v[140:143]\n" \
"ds_write_b128 v50, v[236:239] offset:7168\n" \
M_"v[172:175], a[172:175], v[72:75], v[172:175]\n" \
R_"a[16:19], v20 offset:12288\n" \
"s_waitcnt lgkmcnt(15)\n" \
M_"v[144:147], a[176:179], v[68:71], v[144:147]\n" \
"v_xor_b32_e32 v48, 0x4000, v49\n" \
"ds_read_b32 v58, v18 offset:256\n" \
M_"v[176:179], a[176:179], v[72:75], v[176:179]\n" \
"ds_read_b32 v59, v18 offset:384\n" \
R_"a[32:35], v48 offset:8192\n" \
M_"v[148:151], a[180:183], v[68:71], v[148:151]\n" \
R_"a[36:39], v48 offset:9216\n" \
R_"a[40:43], v48 offset:10240\n" \
M_"v[180:183], a[180:183], v[72:75], v[180:183]\n" \
R_"a[44:47], v48 offset:11264\n" \
R_"a[48:51], v48 offset:12288\n" \
M_"v[152:155], a[184:187], v[68:71], v[152:155]\n" \
R_"a[52:55], v48 offset:13312\n" \
R_"a[56:59], v48 offset:14336\n" \
M_"v[184:187], a[184:187], v[72:75], v[184:187]\n" \
R_"a[60:63], v48 offset:15360\n" \
"s_waitcnt lgkmcnt(15)\n" \
M_"v[156:159], a[188:191], v[68:71], v[156:159]\n" \
R_"a[128:131], v49 offset:8192\n" \
R_"a[132:135], v49 offset:9216\n" \
M_"v[188:191], a[188:191], v[72:75], v[188:191]\n" \
R_"a[136:139], v49 offset:10240\n" \
R_"a[140:143], v49 offset:11264\n" \
"v_smfmac_f32_16x16x64_bf16 a[96:99], a[104:107], v[68:75], v21\n" \
R_"a[144:147], v49 offset:12288\n" \
R_"a[148:151], v49 offset:13312\n" \
M_"v[112:115], a[0:3], a[64:67], v[32:35]\n" \
R_"a[152:155], v49 offset:14336\n" \
R_"a[156:159], v49 offset:15360\n" \
M_"v[112:115], a[4:7], a[68:71], v[112:115]\n" \
"v_accvgpr_read_b32 v40, a100\n" \
"v_accvgpr_read_b32 v24, a101\n" \
M_"v[112:115], a[8:11], a[72:75], v[112:115]\n" \
"v_accvgpr_read_b32 v41, a102\n" \
"v_accvgpr_read_b32 v25, a103\n" \
M_"v[112:115], a[12:15], a[76:79], v[112:115]\n" \
"v_add_f32_e32 v40, v40, v24\n" \
"v_add_f32_e32 v41, v41, v25\n" \
M_"v[116:119], a[28:31], a[76:79], v[32:35]\n" \
"v_add_f32_e32 v40, s94, v40\n" \
"s_waitcnt lgkmcnt(15)\n" \
M_"v[116:119], a[24:27], a[72:75], v[116:119]\n" \
"v_add_f32_e32 v41, s94, v41\n" \
"ds_write_b64 v19, v[40:41]\n" \
M_"v[116:119], a[20:23], a[68:71], v[116:119]\n" \
"v_add_u32_e32 v52, s15, v20\n" \
"v_add_u32_e32 v51, 0x10000, v52\n" \
M_"v[116:119], a[16:19], a[64:67], v[116:119]\n" \
E_"v104, v104\n" \
M_"v[124:127], a[16:19], a[80:83], v[36:39]\n" \
E_"v105, v105\n" \
M_"v[124:127], a[20:23], a[84:87], v[124:127]\n" \
E_"v106, v106\n" \
M_"v[124:127], a[24:27], a[88:91], v[124:127]\n" \
E_"v107, v107\n" \
M_"v[124:127], a[28:31], a[92:95], v[124:127]\n" \
C_"v104, v104, v105\n" \
C_"v105, v106, v107\n" \
M_"v[120:123], a[12:15], a[92:95], v[36:39]\n" \
E_"v108, v108\n" \
M_"v[120:123], a[8:11], a[88:91], v[120:123]\n" \
E_"v109, v109\n" \
M_"v[120:123], a[4:7], a[84:87], v[120:123]\n" \
E_"v110, v110\n" \
M_"v[120:123], a[0:3], a[80:83], v[120:123]\n" \
E_"v111, v111\n" \
"s_waitcnt lgkmcnt(0)\n" \
"s_barrier\n" \
R_"v[208:211], v52 offset:0\n" \
R_"v[212:215], v52 offset:1024\n" \
R_"v[216:219], v52 offset:2048\n" \
R_"v[220:223], v52 offset:3072\n" \
R_"v[240:243], v51 offset:32768\n" \
R_"v[244:247], v51 offset:33792\n" \
R_"v[248:251], v51 offset:34816\n" \
R_"v[252:255], v51 offset:35840\n" \
C_"v106, v108, v109\n" \
C_"v107, v110, v111\n" \
"v_and_b32_e32 v104, v12, v104\n" \
"v_and_b32_e32 v105, v13, v105\n" \
"v_and_b32_e32 v106, v14, v106\n" \
"s_waitcnt vmcnt(0)\n" \
"s_mov_b64 exec, 1\n" \
"buffer_store_dword v61, off, s[56:59], s95 offset:0 sc1\n" \
"s_mov_b64 exec, -1\n" \
"v_and_b32_e32 v107, v15, v107\n" \
"v_xor_b32_e32 v63, 0x400, v19\n" \
"ds_read_b64 v[42:43], v63\n" \
M_"a[96:99], a[116:119], v[104:107], a[96:99]\n" \
"v_mov_b32_e32 v194, v28\n" \
"v_mov_b32_e32 v195, v29\n" \
M_"v[160:163], a[128:131], v[104:107], v[160:163]\n" \
"v_mov_b32_e32 v196, 0\n" \
"v_mov_b32_e32 v197, 0\n" \
M_"v[164:167], a[132:135], v[104:107], v[164:167]\n" \
"v_mov_b32_e32 v192, 0\n" \
"v_mov_b32_e32 v193, 0\n" \
M_"v[168:171], a[136:139], v[104:107], v[168:171]\n" \
"v_readlane_b32 s93, v11, 25\n" \
"v_readlane_b32 s92, v11, 30\n" \
M_"v[172:175], a[140:143], v[104:107], v[172:175]\n" \
B_"a[192:195], v20, s[56:59], s60 offen offset:0 sc1\n" \
M_"v[176:179], a[144:147], v[104:107], v[176:179]\n" \
E_"v118, v118\n" \
M_"v[180:183], a[148:151], v[104:107], v[180:183]\n" \
E_"v119, v119\n" \
M_"v[184:187], a[152:155], v[104:107], v[184:187]\n" \
"s_add_u32 m0, s93, 0x0\n" \
C_"v119, v118, v119\n" \
M_"v[188:191], a[156:159], v[104:107], v[188:191]\n" \
E_"v120, v120\n" \
"s_waitcnt lgkmcnt(1)\n" \
"s_barrier\n" \
B_"a[208:211], v56, s[56:59], s60 offen offset:0 sc1\n" \
M_"v[128:131], v[194:197], v[208:211], v[128:131]\n" \
E_"v121, v121\n" \
M_"v[132:135], v[192:195], v[208:211], v[132:135]\n" \
E_"v116, v116\n" \
M_"v[136:139], v[194:197], v[212:215], v[136:139]\n" \
E_"v117, v117\n" \
M_"v[140:143], v[192:195], v[212:215], v[140:143]\n" \
E_"v122, v122\n" \
M_"v[144:147], v[194:197], v[216:219], v[144:147]\n" \
E_"v123, v123\n" \
M_"v[148:151], v[192:195], v[216:219], v[148:151]\n" \
B_"a[196:199], v20, s[56:59], s60 offen offset:1024 sc1\n" \
M_"v[152:155], v[194:197], v[220:223], v[152:155]\n" \
E_"v114, v114\n" \
M_"v[156:159], v[192:195], v[220:223], v[156:159]\n" \
E_"v115, v115\n" \
M_"v[160:163], v[194:197], v[240:243], v[160:163]\n" \
E_"v112, v112\n" \
M_"v[164:167], v[192:195], v[240:243], v[164:167]\n" \
E_"v113, v113\n" \
M_"v[168:171], v[194:197], v[244:247], v[168:171]\n" \
C_"v120, v120, v121\n" \
C_"v118, v116, v117\n" \
M_"v[172:175], v[192:195], v[244:247], v[172:175]\n" \
B_"a[212:215], v56, s[56:59], s60 offen offset:1024 sc1\n" \
M_"v[176:179], v[194:197], v[248:251], v[176:179]\n" \
E_"v124, v124\n" \
M_"v[180:183], v[192:195], v[248:251], v[180:183]\n" \
E_"v125, v125\n" \
M_"v[184:187], v[194:197], v[252:255], v[184:187]\n" \
E_"v126, v126\n" \
M_"v[188:191], v[192:195], v[252:255], v[188:191]\n" \
E_"v127, v127\n" \
C_"v121, v122, v123\n" \
C_"v117, v114, v115\n" \
C_"v116, v112, v113\n" \
C_"v122, v124, v125\n" \
C_"v123, v126, v127\n" \
B_"a[200:203], v20, s[56:59], s60 offen offset:2048 sc1\n" \
"s_nop 0\n" \
"v_smfmac_f32_16x16x64_bf16 a[96:99], a[104:107], v[116:123], v21\n" \
M_"v[128:131], a[32:35], v[116:119], v[128:131]\n" \
M_"v[160:163], a[32:35], v[120:123], v[160:163]\n" \
M_"v[132:135], a[36:39], v[116:119], v[132:135]\n" \
M_"v[164:167], a[36:39], v[120:123], v[164:167]\n" \
M_"v[136:139], a[40:43], v[116:119], v[136:139]\n" \
B_"a[216:219], v56, s[56:59], s60 offen offset:2048 sc1\n" \
M_"v[168:171], a[40:43], v[120:123], v[168:171]\n" \
M_"v[140:143], a[44:47], v[116:119], v[140:143]\n" \
"v_accvgpr_read_b32 v54, a96\n" \
"v_accvgpr_read_b32 v22, a97\n" \
M_"v[172:175], a[44:47], v[120:123], v[172:175]\n" \
"v_add_f32_e32 v54, v54, v22\n" \
"s_waitcnt lgkmcnt(0)\n" \
"v_add_f32_e32 v54, v54, v42\n" \
M_"v[144:147], a[48:51], v[116:119], v[144:147]\n" \
"v_max_f32_e32 v62, v62, v54\n" \
"v_accvgpr_read_b32 v55, a98\n" \
M_"v[176:179], a[48:51], v[120:123], v[176:179]\n" \
"v_rcp_f32_e32 v54, v54\n" \
B_"a[204:207], v20, s[56:59], s60 offen offset:3072 sc1\n" \
M_"v[148:151], a[52:55], v[116:119], v[148:151]\n" \
"v_mul_f32_e32 v54, v54, v58\n" \
"v_mul_f32_e32 v128, v54, v128\n" \
M_"v[180:183], a[52:55], v[120:123], v[180:183]\n" \
"v_mul_f32_e32 v129, v54, v129\n" \
"v_mul_f32_e32 v130, v54, v130\n" \
"v_mul_f32_e32 v131, v54, v131\n" \
"v_mul_f32_e32 v132, v54, v132\n" \
"v_mul_f32_e32 v133, v54, v133\n" \
"v_mul_f32_e32 v134, v54, v134\n" \
"v_mul_f32_e32 v135, v54, v135\n" \
C_"v64, v128, v129\n" \
C_"v65, v130, v131\n" \
C_"v66, v132, v133\n" \
C_"v67, v134, v135\n" \
"v_accvgpr_read_b32 v23, a99\n" \
M_"v[152:155], a[56:59], v[116:119], v[152:155]\n" \
M_"a[112:115], a[120:123], v[64:67], a[112:115]\n" \
B_"a[220:223], v56, s[56:59], s60 offen offset:3072 sc1\n" \
"v_mul_f32_e32 v136, v54, v136\n" \
"v_mul_f32_e32 v137, v54, v137\n" \
"v_mul_f32_e32 v138, v54, v138\n" \
"v_mul_f32_e32 v139, v54, v139\n" \
"v_mul_f32_e32 v140, v54, v140\n" \
"v_mul_f32_e32 v141, v54, v141\n" \
"v_mul_f32_e32 v142, v54, v142\n" \
"v_mul_f32_e32 v143, v54, v143\n" \
"v_add_f32_e32 v55, v55, v23\n" \
C_"v68, v136, v137\n" \
C_"v69, v138, v139\n" \
C_"v70, v140, v141\n" \
C_"v71, v142, v143\n" \
"v_add_f32_e32 v55, v55, v43\n" \
"v_mul_f32_e32 v144, v54, v144\n" \
M_"a[112:115], a[120:123], v[68:71], a[112:115]\n" \
"v_mul_f32_e32 v145, v54, v145\n" \
"v_mul_f32_e32 v146, v54, v146\n" \
"v_mul_f32_e32 v147, v54, v147\n" \
"v_mul_f32_e32 v148, v54, v148\n" \
B_"v0, s[80:83], s92 offen offset:0 lds\n" \
"v_mul_f32_e32 v149, v54, v149\n" \
"v_mul_f32_e32 v150, v54, v150\n" \
"v_mul_f32_e32 v151, v54, v151\n" \
"v_max_f32_e32 v62, v62, v55\n" \
C_"v72, v144, v145\n" \
C_"v73, v146, v147\n" \
C_"v74, v148, v149\n" \
C_"v75, v150, v151\n" \
"v_rcp_f32_e32 v55, v55\n" \
M_"v[184:187], a[56:59], v[120:123], v[184:187]\n" \
M_"a[112:115], a[120:123], v[72:75], a[112:115]\n" \
"v_mul_f32_e32 v152, v54, v152\n" \
"v_mul_f32_e32 v153, v54, v153\n" \
"v_mul_f32_e32 v154, v54, v154\n" \
"v_mul_f32_e32 v155, v54, v155\n" \
C_"v76, v152, v153\n" \
B_"v1, s[80:83], s92 offen offset:1024 lds\n" \
C_"v77, v154, v155\n" \
"v_mul_f32_e32 v55, v55, v59\n" \
"v_mul_f32_e32 v160, v55, v160\n" \
"v_mul_f32_e32 v161, v55, v161\n" \
"v_mul_f32_e32 v162, v55, v162\n" \
"v_mul_f32_e32 v163, v55, v163\n" \
"v_mul_f32_e32 v164, v55, v164\n" \
"v_mul_f32_e32 v165, v55, v165\n" \
"v_mul_f32_e32 v166, v55, v166\n" \
"v_mul_f32_e32 v167, v55, v167\n" \
C_"v80, v160, v161\n" \
C_"v81, v162, v163\n" \
C_"v82, v164, v165\n" \
C_"v83, v166, v167\n" \
M_"v[156:159], a[60:63], v[116:119], v[156:159]\n" \
"v_mul_f32_e32 v168, v55, v168\n" \
"v_mul_f32_e32 v169, v55, v169\n" \
"v_mul_f32_e32 v170, v55, v170\n" \
"v_mul_f32_e32 v171, v55, v171\n" \
"v_mul_f32_e32 v172, v55, v172\n" \
B_"v2, s[80:83], s92 offen offset:2048 lds\n" \
"v_mul_f32_e32 v173, v55, v173\n" \
"v_mul_f32_e32 v174, v55, v174\n" \
"v_mul_f32_e32 v156, v54, v156\n" \
"v_mul_f32_e32 v157, v54, v157\n" \
"v_mul_f32_e32 v158, v54, v158\n" \
"v_mul_f32_e32 v159, v54, v159\n" \
C_"v78, v156, v157\n" \
C_"v79, v158, v159\n" \
"v_mul_f32_e32 v175, v55, v175\n" \
C_"v84, v168, v169\n" \
M_"a[112:115], a[120:123], v[76:79], a[112:115]\n" \
M_"a[112:115], a[120:123], v[80:83], a[112:115]\n" \
C_"v85, v170, v171\n" \
C_"v86, v172, v173\n" \
C_"v87, v174, v175\n" \
"v_mul_f32_e32 v176, v55, v176\n" \
"v_mul_f32_e32 v177, v55, v177\n" \
B_"v3, s[80:83], s92 offen offset:3072 lds\n" \
"s_add_u32 m0, s93, 0x1000\n" \
M_"a[112:115], a[120:123], v[84:87], a[112:115]\n" \
"v_mul_f32_e32 v178, v55, v178\n" \
"v_mul_f32_e32 v179, v55, v179\n" \
"v_mul_f32_e32 v180, v55, v180\n" \
"v_mul_f32_e32 v181, v55, v181\n" \
"v_mul_f32_e32 v182, v55, v182\n" \
"v_mul_f32_e32 v183, v55, v183\n" \
C_"v96, v176, v177\n" \
C_"v97, v178, v179\n" \
C_"v98, v180, v181\n" \
C_"v99, v182, v183\n" \
M_"v[188:191], a[60:63], v[120:123], v[188:191]\n" \
"v_mul_f32_e32 v184, v55, v184\n" \
M_"a[112:115], a[120:123], v[96:99], a[112:115]\n" \
"v_mul_f32_e32 v185, v55, v185\n" \
"v_mul_f32_e32 v186, v55, v186\n" \
B_"v4, s[80:83], s92 offen offset:0 lds\n" \
"v_mul_f32_e32 v187, v55, v187\n" \
C_"v100, v184, v185\n" \
C_"v101, v186, v187\n" \
"v_mul_f32_e32 v188, v55, v188\n" \
"v_mul_f32_e32 v189, v55, v189\n" \
"v_mul_f32_e32 v190, v55, v190\n" \
"v_mul_f32_e32 v191, v55, v191\n" \
C_"v102, v188, v189\n" \
C_"v103, v190, v191\n" \
"v_cmp_lt_f32_e64 s[72:73], s21, v62\n" \
B_"v5, s[80:83], s92 offen offset:1024 lds\n" \
M_"a[112:115], a[120:123], v[100:103], a[112:115]\n" \
B_"v6, s[80:83], s92 offen offset:2048 lds\n" \
B_"v7, s[80:83], s92 offen offset:3072 lds\n" \
"s_add_u32 m0, s93, 0x2000\n" \
"s_nop 0\n" \
B_"v8, s[80:83], s92 offen offset:0 lds\n" \
"s_nop 2\n" \
"v_accvgpr_read_b32 v63, a112\n" \
"v_cmp_u_f32_e32 vcc, v63, v63\n" \
"s_nop 0\n" \
"s_or_b64 vcc, s[72:73], vcc\n" \
"s_cbranch_scc1 L_end_%=\n" \
"s_branch L_out_%=\n" \
".fill (L_ab + 24576 - .) / 4, 4, 0xBF800000\n" \
".fill (L_ab + 24576 - .) / 4, 4, 0xBF800000\n" \
"L_ap6:\n" \
"s_setpc_b64 s[88:89]\n" \
"L_gs7:\n" \
M_"v[104:107], a[128:131], a[16:19], v[36:39]\n" \
"s_add_u32 s68, s60, 0xfff6e000\n" \
"s_add_u32 s69, s60, 0xfffb7000\n" \
"s_add_u32 s70, s60, 0x0\n" \
"s_add_u32 s71, s60, 0x49000\n" \
M_"v[104:107], a[132:135], a[20:23], v[104:107]\n" \
"s_xor_b32 m0, s65, 0x12000\n" \
R_"a[160:163], v49 offset:0\n" \
M_"v[104:107], a[136:139], a[24:27], v[104:107]\n" \
B_"v50, s[8:11], s64 offen offset:0 sc1 lds\n" \
M_"v[104:107], a[140:143], a[28:31], v[104:107]\n" \
R_"a[164:167], v49 offset:1024\n" \
R_"a[168:171], v49 offset:2048\n" \
M_"v[108:111], a[156:159], a[28:31], v[36:39]\n" \
R_"a[172:175], v49 offset:3072\n" \
R_"a[176:179], v49 offset:4096\n" \
M_"v[108:111], a[152:155], a[24:27], v[108:111]\n" \
B_"v50, s[8:11], s64 offen offset:1024 sc1 lds\n" \
M_"v[108:111], a[148:151], a[20:23], v[108:111]\n" \
"s_mov_b32 s65, s13\n" \
"v_add_u32_e32 v63, s15, v20\n" \
M_"v[108:111], a[144:147], a[16:19], v[108:111]\n" \
"s_add_u32 s67, s14, 40960\n" \
"v_add_u32_e32 v50, 0x6000, v63\n" \
M_"v[116:119], a[144:147], a[32:35], v[40:43]\n" \
"s_xor_b32 m0, s65, 0x8000\n" \
R_"a[180:183], v49 offset:5120\n" \
M_"v[116:119], a[148:151], a[36:39], v[116:119]\n" \
R_"a[184:187], v49 offset:6144\n" \
"v_and_b32_e32 v68, v12, v68\n" \
M_"v[116:119], a[152:155], a[40:43], v[116:119]\n" \
"v_and_b32_e32 v69, v13, v69\n" \
"v_and_b32_e32 v70, v14, v70\n" \
M_"v[116:119], a[156:159], a[44:47], v[116:119]\n" \
B_"v50, s[8:11], s67 offen offset:0 sc1 lds\n" \
M_"v[112:115], a[140:143], a[44:47], v[40:43]\n" \
"v_and_b32_e32 v71, v15, v71\n" \
R_"a[188:191], v49 offset:7168\n" \
M_"v[112:115], a[136:139], a[40:43], v[112:115]\n" \
E_"v86, v86\n" \
M_"v[112:115], a[132:135], a[36:39], v[112:115]\n" \
E_"v87, v87\n" \
M_"v[112:115], a[128:131], a[32:35], v[112:115]\n" \
E_"v84, v84\n" \
M_"v[120:123], a[128:131], a[48:51], v[44:47]\n" \
B_"v50, s[8:11], s67 offen offset:1024 sc1 lds\n" \
M_"v[120:123], a[132:135], a[52:55], v[120:123]\n" \
E_"v85, v85\n" \
M_"v[120:123], a[136:139], a[56:59], v[120:123]\n" \
C_"v87, v86, v87\n" \
C_"v86, v84, v85\n" \
M_"v[120:123], a[140:143], a[60:63], v[120:123]\n" \
E_"v82, v82\n" \
M_"v[124:127], a[156:159], a[60:63], v[44:47]\n" \
E_"v83, v83\n" \
M_"v[124:127], a[152:155], a[56:59], v[124:127]\n" \
E_"v80, v80\n" \
M_"v[124:127], a[148:151], a[52:55], v[124:127]\n" \
B_"v50, s[8:11], s67 offen offset:2048 sc1 lds\n" \
M_"v[124:127], a[144:147], a[48:51], v[124:127]\n" \
C_"v85, v82, v83\n" \
"s_waitcnt lgkmcnt(1)\n" \
M_"v[128:131], a[160:163], v[68:71], v[128:131]\n" \
E_"v81, v81\n" \
M_"v[160:163], a[160:163], v[72:75], v[160:163]\n" \
C_"v84, v80, v81\n" \
"s_mov_b32 s64, s14\n" \
M_"v[132:135], a[164:167], v[68:71], v[132:135]\n" \
E_"v88, v88\n" \
M_"v[164:167], a[164:167], v[72:75], v[164:167]\n" \
E_"v89, v89\n" \
M_"v[136:139], a[168:171], v[68:71], v[136:139]\n" \
B_"v50, s[8:11], s67 offen offset:3072 sc1 lds\n" \
M_"v[168:171], a[168:171], v[72:75], v[168:171]\n" \
E_"v90, v90\n" \
M_"v[140:143], a[172:175], v[68:71], v[140:143]\n" \
E_"v91, v91\n" \
M_"v[172:175], a[172:175], v[72:75], v[172:175]\n" \
C_"v88, v88, v89\n" \
C_"v89, v90, v91\n" \
M_"v[144:147], a[176:179], v[68:71], v[144:147]\n" \
E_"v92, v92\n" \
M_"v[176:179], a[176:179], v[72:75], v[176:179]\n" \
E_"v93, v93\n" \
M_"v[148:151], a[180:183], v[68:71], v[148:151]\n" \
E_"v94, v94\n" \
M_"v[180:183], a[180:183], v[72:75], v[180:183]\n" \
E_"v95, v95\n" \
M_"v[152:155], a[184:187], v[68:71], v[152:155]\n" \
C_"v90, v92, v93\n" \
C_"v91, v94, v95\n" \
M_"v[184:187], a[184:187], v[72:75], v[184:187]\n" \
"v_add_u32_e32 v51, 0x12000, v63\n" \
"s_waitcnt lgkmcnt(0)\n" \
M_"v[156:159], a[188:191], v[68:71], v[156:159]\n" \
"s_xor_b32 m0, s65, 0xa000\n" \
"ds_read_b32 v58, v18 offset:512\n" \
M_"v[188:191], a[188:191], v[72:75], v[188:191]\n" \
B_"v51, s[8:11], s64 offen offset:0 sc1 lds\n" \
"v_smfmac_f32_16x16x64_bf16 a[96:99], a[104:107], v[68:75], v21\n" \
"ds_read_b32 v59, v18 offset:640\n" \
"ds_read_b32 v60, v18 offset:768\n" \
M_"v[220:223], a[188:191], v[84:87], v[220:223]\n" \
B_"v51, s[8:11], s64 offen offset:1024 sc1 lds\n" \
M_"v[252:255], a[188:191], v[88:91], v[252:255]\n" \
"ds_read_b32 v61, v18 offset:896\n" \
R_"a[128:131], v48 offset:0\n" \
M_"v[216:219], a[184:187], v[84:87], v[216:219]\n" \
R_"a[144:147], v48 offset:4096\n" \
R_"a[132:135], v48 offset:1024\n" \
M_"v[248:251], a[184:187], v[88:91], v[248:251]\n" \
R_"a[148:151], v48 offset:5120\n" \
R_"a[136:139], v48 offset:2048\n" \
M_"v[212:215], a[180:183], v[84:87], v[212:215]\n" \
R_"a[152:155], v48 offset:6144\n" \
R_"a[140:143], v48 offset:3072\n" \
M_"v[244:247], a[180:183], v[88:91], v[244:247]\n" \
R_"a[156:159], v48 offset:7168\n" \
"v_add_u32_e32 v52, 0x14000, v63\n" \
M_"v[208:211], a[176:179], v[84:87], v[208:211]\n" \
B_"v51, s[8:11], s64 offen offset:2048 sc1 lds\n" \
M_"v[240:243], a[176:179], v[88:91], v[240:243]\n" \
E_"v104, v104\n" \
M_"v[204:207], a[172:175], v[84:87], v[204:207]\n" \
E_"v105, v105\n" \
M_"v[236:239], a[172:175], v[88:91], v[236:239]\n" \
B_"v51, s[8:11], s64 offen offset:3072 sc1 lds\n" \
M_"v[200:203], a[168:171], v[84:87], v[200:203]\n" \
E_"v106, v106\n" \
M_"v[232:235], a[168:171], v[88:91], v[232:235]\n" \
E_"v107, v107\n" \
M_"v[196:199], a[164:167], v[84:87], v[196:199]\n" \
C_"v104, v104, v105\n" \
C_"v105, v106, v107\n" \
M_"v[228:231], a[164:167], v[88:91], v[228:231]\n" \
E_"v108, v108\n" \
M_"v[192:195], a[160:163], v[84:87], v[192:195]\n" \
E_"v109, v109\n" \
M_"v[224:227], a[160:163], v[88:91], v[224:227]\n" \
E_"v110, v110\n" \
"v_smfmac_f32_16x16x64_bf16 a[100:103], a[104:107], v[84:91], v21\n" \
E_"v111, v111\n" \
C_"v106, v108, v109\n" \
C_"v107, v110, v111\n" \
"v_and_b32_e32 v104, v12, v104\n" \
"v_and_b32_e32 v105, v13, v105\n" \
"v_and_b32_e32 v106, v14, v106\n" \
"v_and_b32_e32 v107, v15, v107\n" \
"v_accvgpr_read_b32 v54, a96\n" \
"v_accvgpr_read_b32 v22, a97\n" \
"v_readlane_b32 s93, v11, 22\n" \
"v_readlane_b32 s80, v11, 11\n" \
"v_readlane_b32 s81, v11, 12\n" \
"s_mov_b32 s82, 0x47fca0\n" \
"s_mov_b32 s83, 0x20000\n" \
"v_readlane_b32 s92, v11, 29\n" \
E_"v118, v118\n" \
E_"v119, v119\n" \
E_"v116, v116\n" \
E_"v117, v117\n" \
C_"v119, v118, v119\n" \
E_"v114, v114\n" \
E_"v115, v115\n" \
E_"v112, v112\n" \
C_"v118, v116, v117\n" \
"s_waitcnt vmcnt(10) lgkmcnt(0)\n" \
"s_barrier\n" \
M_"v[80:83], a[128:131], a[32:35], v[40:43]\n" \
R_"a[160:163], v49 offset:8192\n" \
R_"a[164:167], v49 offset:9216\n" \
M_"v[80:83], a[132:135], a[36:39], v[80:83]\n" \
R_"a[168:171], v49 offset:10240\n" \
"s_xor_b32 m0, s65, 0x18000\n" \
M_"v[80:83], a[136:139], a[40:43], v[80:83]\n" \
B_"v52, s[8:11], s64 offen offset:0 sc1 lds\n" \
M_"v[80:83], a[140:143], a[44:47], v[80:83]\n" \
R_"a[172:175], v49 offset:11264\n" \
R_"a[176:179], v49 offset:12288\n" \
M_"v[84:87], a[156:159], a[44:47], v[40:43]\n" \
R_"a[180:183], v49 offset:13312\n" \
R_"a[184:187], v49 offset:14336\n" \
M_"v[84:87], a[152:155], a[40:43], v[84:87]\n" \
B_"v52, s[8:11], s64 offen offset:1024 sc1 lds\n" \
M_"v[84:87], a[148:151], a[36:39], v[84:87]\n" \
R_"a[188:191], v49 offset:15360\n" \
C_"v117, v114, v115\n" \
M_"v[84:87], a[144:147], a[32:35], v[84:87]\n" \
B_"v52, s[8:11], s64 offen offset:2048 sc1 lds\n" \
M_"v[92:95], a[144:147], a[48:51], v[44:47]\n" \
E_"v113, v113\n" \
M_"v[92:95], a[148:151], a[52:55], v[92:95]\n" \
B_"v52, s[8:11], s64 offen offset:3072 sc1 lds\n" \
M_"v[92:95], a[152:155], a[56:59], v[92:95]\n" \
"s_add_u32 m0, s93, 0x0\n" \
C_"v116, v112, v113\n" \
M_"v[92:95], a[156:159], a[60:63], v[92:95]\n" \
E_"v120, v120\n" \
M_"v[88:91], a[140:143], a[60:63], v[44:47]\n" \
B_"v0, s[80:83], s92 offen offset:0 lds\n" \
M_"v[88:91], a[136:139], a[56:59], v[88:91]\n" \
E_"v121, v121\n" \
M_"v[88:91], a[132:135], a[52:55], v[88:91]\n" \
E_"v122, v122\n" \
M_"v[88:91], a[128:131], a[48:51], v[88:91]\n" \
B_"v1, s[80:83], s92 offen offset:1024 lds\n" \
M_"a[96:99], a[116:119], v[104:107], a[96:99]\n" \
C_"v120, v120, v121\n" \
"s_waitcnt lgkmcnt(5)\n" \
M_"v[160:163], a[160:163], v[104:107], v[160:163]\n" \
E_"v123, v123\n" \
M_"v[164:167], a[164:167], v[104:107], v[164:167]\n" \
B_"v2, s[80:83], s92 offen offset:2048 lds\n" \
M_"v[168:171], a[168:171], v[104:107], v[168:171]\n" \
C_"v121, v122, v123\n" \
"s_waitcnt lgkmcnt(1)\n" \
M_"v[172:175], a[172:175], v[104:107], v[172:175]\n" \
E_"v124, v124\n" \
M_"v[176:179], a[176:179], v[104:107], v[176:179]\n" \
E_"v125, v125\n" \
M_"v[180:183], a[180:183], v[104:107], v[180:183]\n" \
E_"v126, v126\n" \
M_"v[184:187], a[184:187], v[104:107], v[184:187]\n" \
C_"v122, v124, v125\n" \
"s_waitcnt lgkmcnt(0)\n" \
M_"v[188:191], a[188:191], v[104:107], v[188:191]\n" \
E_"v127, v127\n" \
M_"v[220:223], a[188:191], v[116:119], v[220:223]\n" \
C_"v123, v126, v127\n" \
B_"v3, s[80:83], s92 offen offset:3072 lds\n" \
"s_add_u32 m0, s93, 0x1000\n" \
M_"v[252:255], a[188:191], v[120:123], v[252:255]\n" \
R_"a[128:131], v48 offset:8192\n" \
R_"a[144:147], v48 offset:12288\n" \
M_"v[216:219], a[184:187], v[116:119], v[216:219]\n" \
B_"v4, s[80:83], s92 offen offset:0 lds\n" \
M_"v[248:251], a[184:187], v[120:123], v[248:251]\n" \
R_"a[132:135], v48 offset:9216\n" \
R_"a[148:151], v48 offset:13312\n" \
M_"v[212:215], a[180:183], v[116:119], v[212:215]\n" \
B_"v5, s[80:83], s92 offen offset:1024 lds\n" \
M_"v[244:247], a[180:183], v[120:123], v[244:247]\n" \
R_"a[136:139], v48 offset:10240\n" \
R_"a[152:155], v48 offset:14336\n" \
M_"v[208:211], a[176:179], v[116:119], v[208:211]\n" \
B_"v6, s[80:83], s92 offen offset:2048 lds\n" \
M_"v[240:243], a[176:179], v[120:123], v[240:243]\n" \
R_"a[140:143], v48 offset:11264\n" \
R_"a[156:159], v48 offset:15360\n" \
M_"v[204:207], a[172:175], v[116:119], v[204:207]\n" \
B_"v7, s[80:83], s92 offen offset:3072 lds\n" \
M_"v[236:239], a[172:175], v[120:123], v[236:239]\n" \
"v_readlane_b32 s96, v11, 1\n" \
"v_lshrrev_b32_e32 v16, 1, v20\n" \
M_"v[200:203], a[168:171], v[116:119], v[200:203]\n" \
"v_add_f32_e32 v54, v54, v22\n" \
"v_max_f32_e32 v62, v62, v54\n" \
M_"v[232:235], a[168:171], v[120:123], v[232:235]\n" \
"v_rcp_f32_e32 v54, v54\n" \
M_"v[196:199], a[164:167], v[116:119], v[196:199]\n" \
"v_mul_f32_e32 v54, v54, v58\n" \
"v_mul_f32_e32 v128, v54, v128\n" \
M_"v[228:231], a[164:167], v[120:123], v[228:231]\n" \
"v_mul_f32_e32 v129, v54, v129\n" \
"v_mul_f32_e32 v136, v54, v136\n" \
M_"v[192:195], a[160:163], v[116:119], v[192:195]\n" \
"v_mul_f32_e32 v137, v54, v137\n" \
"v_mul_f32_e32 v144, v54, v144\n" \
M_"v[224:227], a[160:163], v[120:123], v[224:227]\n" \
"v_mul_f32_e32 v145, v54, v145\n" \
"v_mul_f32_e32 v152, v54, v152\n" \
"v_smfmac_f32_16x16x64_bf16 a[100:103], a[104:107], v[116:123], v21\n" \
"v_mul_f32_e32 v153, v54, v153\n" \
C_"v128, v128, v129\n" \
"v_mul_f32_e32 v130, v54, v130\n" \
"v_mul_f32_e32 v131, v54, v131\n" \
C_"v136, v136, v137\n" \
"v_mul_f32_e32 v138, v54, v138\n" \
"v_mul_f32_e32 v139, v54, v139\n" \
C_"v144, v144, v145\n" \
"v_mul_f32_e32 v146, v54, v146\n" \
"v_mul_f32_e32 v147, v54, v147\n" \
C_"v152, v152, v153\n" \
"v_mul_f32_e32 v154, v54, v154\n" \
"v_mul_f32_e32 v155, v54, v155\n" \
C_"v129, v130, v131\n" \
"v_mul_f32_e32 v132, v54, v132\n" \
"v_mul_f32_e32 v133, v54, v133\n" \
"v_mul_f32_e32 v134, v54, v134\n" \
"v_mul_f32_e32 v135, v54, v135\n" \
C_"v137, v138, v139\n" \
"v_mul_f32_e32 v140, v54, v140\n" \
"v_mul_f32_e32 v141, v54, v141\n" \
"v_mul_f32_e32 v142, v54, v142\n" \
"v_mul_f32_e32 v143, v54, v143\n" \
C_"v145, v146, v147\n" \
"v_mul_f32_e32 v148, v54, v148\n" \
"v_mul_f32_e32 v149, v54, v149\n" \
"v_mul_f32_e32 v150, v54, v150\n" \
"v_mul_f32_e32 v151, v54, v151\n" \
C_"v153, v154, v155\n" \
"v_mul_f32_e32 v156, v54, v156\n" \
"v_mul_f32_e32 v157, v54, v157\n" \
"v_mul_f32_e32 v158, v54, v158\n" \
"v_mul_f32_e32 v159, v54, v159\n" \
C_"v130, v132, v133\n" \
C_"v131, v134, v135\n" \
"buffer_store_dwordx4 v[128:131], v20, s[56:59], s68 offen offset:0 sc1\n" \
C_"v138, v140, v141\n" \
C_"v139, v142, v143\n" \
C_"v146, v148, v149\n" \
C_"v147, v150, v151\n" \
C_"v154, v156, v157\n" \
C_"v155, v158, v159\n" \
"buffer_store_dwordx4 v[136:139], v20, s[56:59], s68 offen offset:1024 sc1\n" \
"s_add_u32 m0, s93, 0x2000\n" \
"v_add_u32_e32 v49, 0x10000, v48\n" \
"s_xor_b32 s65, s65, 0x8000\n" \
"s_add_u32 s64, s64, 0x10000\n" \
E_"v86, v86\n" \
"buffer_store_dwordx4 v[144:147], v20, s[56:59], s68 offen offset:2048 sc1\n" \
E_"v87, v87\n" \
E_"v84, v84\n" \
E_"v85, v85\n" \
"buffer_store_dwordx4 v[152:155], v20, s[56:59], s68 offen offset:3072 sc1\n" \
E_"v88, v88\n" \
E_"v89, v89\n" \
C_"v87, v86, v87\n" \
E_"v82, v82\n" \
B_"v8, s[80:83], s92 offen offset:0 lds\n" \
E_"v83, v83\n" \
E_"v80, v80\n" \
E_"v81, v81\n" \
E_"v90, v90\n" \
E_"v91, v91\n" \
C_"v86, v84, v85\n" \
C_"v88, v88, v89\n" \
E_"v92, v92\n" \
E_"v93, v93\n" \
E_"v94, v94\n" \
E_"v95, v95\n" \
C_"v85, v82, v83\n" \
C_"v84, v80, v81\n" \
C_"v89, v90, v91\n" \
"v_and_b32_e32 v84, v12, v84\n" \
"v_and_b32_e32 v85, v13, v85\n" \
"v_and_b32_e32 v86, v14, v86\n" \
"s_waitcnt vmcnt(17) lgkmcnt(0)\n" \
"s_barrier\n" \
M_"v[120:123], a[128:131], a[48:51], v[44:47]\n" \
"buffer_load_dwordx2 v[26:27], v16, s[56:59], s96 offen sc1\n" \
M_"v[120:123], a[132:135], a[52:55], v[120:123]\n" \
R_"a[188:191], v49 offset:7168\n" \
R_"a[184:187], v49 offset:6144\n" \
M_"v[120:123], a[136:139], a[56:59], v[120:123]\n" \
"s_xor_b32 m0, s65, 0x12000\n" \
R_"a[180:183], v49 offset:5120\n" \
M_"v[120:123], a[140:143], a[60:63], v[120:123]\n" \
B_"v50, s[8:11], s64 offen offset:0 sc1 lds\n" \
M_"v[124:127], a[156:159], a[60:63], v[44:47]\n" \
R_"a[176:179], v49 offset:4096\n" \
R_"a[172:175], v49 offset:3072\n" \
M_"v[124:127], a[152:155], a[56:59], v[124:127]\n" \
B_"v50, s[8:11], s64 offen offset:1024 sc1 lds\n" \
M_"v[124:127], a[148:151], a[52:55], v[124:127]\n" \
R_"a[168:171], v49 offset:2048\n" \
"v_and_b32_e32 v87, v15, v87\n" \
M_"v[124:127], a[144:147], a[48:51], v[124:127]\n" \
C_"v90, v92, v93\n" \
C_"v91, v94, v95\n" \
M_"v[32:35], a[124:127], a[64:67], 0\n" \
B_"v50, s[8:11], s64 offen offset:2048 sc1 lds\n" \
M_"v[36:39], a[124:127], a[80:83], 0\n" \
R_"a[164:167], v49 offset:1024\n" \
R_"a[160:163], v49 offset:0\n" \
M_"v[32:35], a[224:227], a[68:71], v[32:35]\n" \
R_"a[0:3], v49 offset:8192\n" \
R_"a[4:7], v49 offset:9216\n" \
M_"v[36:39], a[224:227], a[84:87], v[36:39]\n" \
R_"a[8:11], v49 offset:10240\n" \
R_"a[12:15], v49 offset:11264\n" \
M_"v[32:35], a[228:231], a[72:75], v[32:35]\n" \
B_"v50, s[8:11], s64 offen offset:3072 sc1 lds\n" \
M_"v[36:39], a[228:231], a[88:91], v[36:39]\n" \
R_"a[16:19], v49 offset:12288\n" \
R_"a[20:23], v49 offset:13312\n" \
M_"v[32:35], a[232:235], a[76:79], v[32:35]\n" \
R_"a[24:27], v49 offset:14336\n" \
R_"a[28:31], v49 offset:15360\n" \
M_"v[36:39], a[232:235], a[92:95], v[36:39]\n" \
E_"v120, v120\n" \
"v_smfmac_f32_16x16x64_bf16 a[100:103], a[104:107], v[84:91], v21\n" \
"s_mov_b32 s63, s62\n" \
"s_waitcnt lgkmcnt(13)\n" \
M_"v[220:223], a[188:191], v[84:87], v[220:223]\n" \
E_"v121, v121\n" \
M_"v[252:255], a[188:191], v[88:91], v[252:255]\n" \
E_"v122, v122\n" \
M_"v[216:219], a[184:187], v[84:87], v[216:219]\n" \
E_"v123, v123\n" \
M_"v[248:251], a[184:187], v[88:91], v[248:251]\n" \
C_"v120, v120, v121\n" \
C_"v121, v122, v123\n" \
M_"v[212:215], a[180:183], v[84:87], v[212:215]\n" \
E_"v124, v124\n" \
M_"v[244:247], a[180:183], v[88:91], v[244:247]\n" \
"v_and_b32_e32 v120, v12, v120\n" \
"s_waitcnt lgkmcnt(10)\n" \
M_"v[208:211], a[176:179], v[84:87], v[208:211]\n" \
E_"v125, v125\n" \
M_"v[240:243], a[176:179], v[88:91], v[240:243]\n" \
E_"v126, v126\n" \
M_"v[204:207], a[172:175], v[84:87], v[204:207]\n" \
E_"v127, v127\n" \
M_"v[236:239], a[172:175], v[88:91], v[236:239]\n" \
C_"v122, v124, v125\n" \
C_"v123, v126, v127\n" \
M_"v[200:203], a[168:171], v[84:87], v[200:203]\n" \
"v_and_b32_e32 v121, v13, v121\n" \
"v_and_b32_e32 v122, v14, v122\n" \
M_"v[232:235], a[168:171], v[88:91], v[232:235]\n" \
"v_and_b32_e32 v123, v15, v123\n" \
"s_waitcnt lgkmcnt(0)\n" \
M_"v[196:199], a[164:167], v[84:87], v[196:199]\n" \
"v_accvgpr_read_b32 v56, a100\n" \
"v_accvgpr_read_b32 v24, a101\n" \
M_"v[228:231], a[164:167], v[88:91], v[228:231]\n" \
"v_readlane_b32 s97, v11, 39\n" \
"v_add_u32_e32 v17, 0x49000, v20\n" \
M_"v[192:195], a[160:163], v[84:87], v[192:195]\n" \
"v_readlane_b32 s98, v11, 40\n" \
"v_add_u32_e32 v48, s16, v20\n" \
M_"v[224:227], a[160:163], v[88:91], v[224:227]\n" \
R_"a[128:131], v48 offset:0\n" \
R_"a[144:147], v48 offset:4096\n" \
M_"a[100:103], a[116:119], v[120:123], a[100:103]\n" \
R_"a[132:135], v48 offset:1024\n" \
R_"a[148:151], v48 offset:5120\n" \
M_"v[224:227], a[0:3], v[120:123], v[224:227]\n" \
R_"a[136:139], v48 offset:2048\n" \
R_"a[152:155], v48 offset:6144\n" \
M_"v[228:231], a[4:7], v[120:123], v[228:231]\n" \
R_"a[140:143], v48 offset:3072\n" \
R_"a[156:159], v48 offset:7168\n" \
M_"v[232:235], a[8:11], v[120:123], v[232:235]\n" \
"v_accvgpr_read_b32 v55, a98\n" \
"v_accvgpr_read_b32 v23, a99\n" \
M_"a[96:99], a[120:123], a[120:123], 0\n" \
"v_add_f32_e32 v55, v55, v23\n" \
"v_max_f32_e32 v62, v62, v55\n" \
M_"v[236:239], a[12:15], v[120:123], v[236:239]\n" \
"v_rcp_f32_e32 v55, v55\n" \
M_"v[240:243], a[16:19], v[120:123], v[240:243]\n" \
"v_mul_f32_e32 v55, v55, v59\n" \
"v_mul_f32_e32 v160, v55, v160\n" \
M_"v[244:247], a[20:23], v[120:123], v[244:247]\n" \
"v_mul_f32_e32 v161, v55, v161\n" \
"v_mul_f32_e32 v168, v55, v168\n" \
M_"v[248:251], a[24:27], v[120:123], v[248:251]\n" \
"v_mul_f32_e32 v169, v55, v169\n" \
"v_mul_f32_e32 v176, v55, v176\n" \
M_"v[252:255], a[28:31], v[120:123], v[252:255]\n" \
"v_mul_f32_e32 v177, v55, v177\n" \
"v_mul_f32_e32 v184, v55, v184\n" \
"v_mul_f32_e32 v185, v55, v185\n" \
C_"v160, v160, v161\n" \
"v_mul_f32_e32 v162, v55, v162\n" \
"v_mul_f32_e32 v163, v55, v163\n" \
C_"v168, v168, v169\n" \
"v_mul_f32_e32 v170, v55, v170\n" \
"v_mul_f32_e32 v171, v55, v171\n" \
"s_waitcnt vmcnt(4)\n" \
"v_cmp_ne_u64_e32 vcc, s[62:63], v[26:27]\n" \
C_"v176, v176, v177\n" \
"v_mul_f32_e32 v178, v55, v178\n" \
"v_mul_f32_e32 v179, v55, v179\n" \
C_"v184, v184, v185\n" \
"v_mul_f32_e32 v186, v55, v186\n" \
"s_cbranch_vccnz L_sos_%=\n" \
"L_ros_%=:\n" \
B_"a[124:127], v20, s[56:59], s97 offen offset:0 sc1\n" \
"v_mul_f32_e32 v187, v55, v187\n" \
C_"v161, v162, v163\n" \
"v_mul_f32_e32 v164, v55, v164\n" \
"v_mul_f32_e32 v165, v55, v165\n" \
"v_mul_f32_e32 v166, v55, v166\n" \
"v_mul_f32_e32 v167, v55, v167\n" \
B_"a[228:231], v17, s[56:59], s97 offen offset:0 sc1\n" \
C_"v169, v170, v171\n" \
"v_mul_f32_e32 v172, v55, v172\n" \
"v_mul_f32_e32 v173, v55, v173\n" \
"v_mul_f32_e32 v174, v55, v174\n" \
"v_mul_f32_e32 v175, v55, v175\n" \
C_"v177, v178, v179\n" \
B_"a[236:239], v20, s[56:59], s98 offen offset:0 sc1\n" \
"v_mul_f32_e32 v180, v55, v180\n" \
"v_mul_f32_e32 v181, v55, v181\n" \
"v_mul_f32_e32 v182, v55, v182\n" \
"v_mul_f32_e32 v183, v55, v183\n" \
C_"v185, v186, v187\n" \
"v_mul_f32_e32 v188, v55, v188\n" \
B_"a[244:247], v17, s[56:59], s98 offen offset:0 sc1\n" \
"v_mul_f32_e32 v189, v55, v189\n" \
"v_mul_f32_e32 v190, v55, v190\n" \
"v_mul_f32_e32 v191, v55, v191\n" \
C_"v162, v164, v165\n" \
C_"v163, v166, v167\n" \
C_"v170, v172, v173\n" \
"buffer_store_dwordx4 v[160:163], v20, s[56:59], s69 offen offset:0 sc1\n" \
C_"v171, v174, v175\n" \
C_"v178, v180, v181\n" \
C_"v179, v182, v183\n" \
C_"v186, v188, v189\n" \
C_"v187, v190, v191\n" \
"v_readlane_b32 s93, v11, 23\n" \
"buffer_store_dwordx4 v[168:171], v20, s[56:59], s69 offen offset:1024 sc1\n" \
"v_readlane_b32 s84, v11, 15\n" \
"v_readlane_b32 s85, v11, 16\n" \
"v_readlane_b32 s86, v11, 17\n" \
"s_mov_b32 s87, 0x20000\n" \
"v_readlane_b32 s92, v11, 18\n" \
"v_readlane_b32 s91, v11, 24\n" \
"buffer_store_dwordx4 v[176:179], v20, s[56:59], s69 offen offset:2048 sc1\n" \
M_"a[112:115], a[120:123], v[128:131], a[112:115]\n" \
M_"a[112:115], a[120:123], v[136:139], a[112:115]\n" \
"buffer_store_dwordx4 v[184:187], v20, s[56:59], s69 offen offset:3072 sc1\n" \
"v_mfma_f32_32x32x16_bf16 v[128:143], a[120:123], a[120:123], 0\n" \
M_"a[112:115], a[120:123], v[144:147], a[112:115]\n" \
B_"a[224:227], v20, s[56:59], s97 offen offset:1024 sc1\n" \
M_"a[112:115], a[120:123], v[152:155], a[112:115]\n" \
"v_mfma_f32_32x32x16_bf16 v[144:159], a[120:123], a[120:123], 0\n" \
B_"a[232:235], v17, s[56:59], s97 offen offset:1024 sc1\n" \
"v_add_f32_e32 v56, v56, v24\n" \
M_"a[112:115], a[120:123], v[160:163], a[112:115]\n" \
B_"a[240:243], v20, s[56:59], s98 offen offset:1024 sc1\n" \
"v_max_f32_e32 v62, v62, v56\n" \
"v_rcp_f32_e32 v56, v56\n" \
M_"a[112:115], a[120:123], v[168:171], a[112:115]\n" \
"v_mfma_f32_32x32x16_bf16 v[160:175], a[120:123], a[120:123], 0\n" \
B_"a[248:251], v17, s[56:59], s98 offen offset:1024 sc1\n" \
"v_mul_f32_e32 v56, v56, v60\n" \
M_"a[112:115], a[120:123], v[176:179], a[112:115]\n" \
"v_mul_f32_e32 v192, v56, v192\n" \
"v_mul_f32_e32 v193, v56, v193\n" \
"s_waitcnt vmcnt(17) lgkmcnt(0)\n" \
"s_barrier\n" \
M_"v[64:67], a[128:131], a[64:67], v[32:35]\n" \
"s_add_u32 m0, s93, 0x0\n" \
C_"v192, v192, v193\n" \
M_"v[64:67], a[132:135], a[68:71], v[64:67]\n" \
B_"v0, s[84:87], s92 offen offset:0 lds\n" \
M_"v[64:67], a[136:139], a[72:75], v[64:67]\n" \
"v_mul_f32_e32 v194, v56, v194\n" \
"v_mul_f32_e32 v195, v56, v195\n" \
M_"v[64:67], a[140:143], a[76:79], v[64:67]\n" \
B_"v1, s[84:87], s92 offen offset:1024 lds\n" \
M_"v[68:71], a[156:159], a[76:79], v[32:35]\n" \
M_"a[112:115], a[120:123], v[184:187], a[112:115]\n" \
"v_mfma_f32_32x32x16_bf16 v[176:191], a[120:123], a[120:123], 0\n" \
B_"v2, s[84:87], s92 offen offset:2048 lds\n" \
C_"v193, v194, v195\n" \
"v_mul_f32_e32 v196, v56, v196\n" \
"v_mul_f32_e32 v197, v56, v197\n" \
"v_mul_f32_e32 v198, v56, v198\n" \
M_"v[68:71], a[152:155], a[72:75], v[68:71]\n" \
B_"v3, s[84:87], s92 offen offset:3072 lds\n" \
M_"v[68:71], a[148:151], a[68:71], v[68:71]\n" \
"s_add_u32 m0, s91, 0x1000\n" \
"v_mul_f32_e32 v199, v56, v199\n" \
M_"v[68:71], a[144:147], a[64:67], v[68:71]\n" \
B_"v4, s[84:87], s92 offen offset:0 lds\n" \
M_"v[76:79], a[144:147], a[80:83], v[36:39]\n" \
R_"a[144:147], v48 offset:12288\n" \
"v_mul_f32_e32 v200, v56, v200\n" \
M_"v[76:79], a[148:151], a[84:87], v[76:79]\n" \
B_"v5, s[84:87], s92 offen offset:1024 lds\n" \
M_"v[76:79], a[152:155], a[88:91], v[76:79]\n" \
R_"a[148:151], v48 offset:13312\n" \
R_"a[152:155], v48 offset:14336\n" \
M_"v[76:79], a[156:159], a[92:95], v[76:79]\n" \
B_"v6, s[84:87], s92 offen offset:2048 lds\n" \
M_"v[72:75], a[140:143], a[92:95], v[36:39]\n" \
R_"a[140:143], v48 offset:11264\n" \
R_"a[156:159], v48 offset:15360\n" \
M_"v[72:75], a[136:139], a[88:91], v[72:75]\n" \
B_"v7, s[84:87], s92 offen offset:3072 lds\n" \
M_"v[72:75], a[132:135], a[84:87], v[72:75]\n" \
R_"a[132:135], v48 offset:9216\n" \
R_"a[136:139], v48 offset:10240\n" \
M_"v[72:75], a[128:131], a[80:83], v[72:75]\n" \
R_"a[128:131], v48 offset:8192\n" \
"v_mul_f32_e32 v201, v56, v201\n" \
"v_accvgpr_read_b32 v57, a102\n" \
"v_accvgpr_read_b32 v25, a103\n" \
M_"a[100:103], a[120:123], a[120:123], 0\n" \
C_"v194, v196, v197\n" \
C_"v195, v198, v199\n" \
"buffer_store_dwordx4 v[192:195], v20, s[56:59], s70 offen offset:0 sc1\n" \
C_"v200, v200, v201\n" \
"v_mul_f32_e32 v202, v56, v202\n" \
"v_mul_f32_e32 v203, v56, v203\n" \
"v_add_f32_e32 v57, v57, v25\n" \
M_"a[112:115], a[120:123], v[192:195], a[112:115]\n" \
C_"v201, v202, v203\n" \
"v_mul_f32_e32 v204, v56, v204\n" \
"v_mul_f32_e32 v205, v56, v205\n" \
"v_mul_f32_e32 v206, v56, v206\n" \
"v_mul_f32_e32 v207, v56, v207\n" \
"v_mul_f32_e32 v208, v56, v208\n" \
"v_mul_f32_e32 v209, v56, v209\n" \
"v_max_f32_e32 v62, v62, v57\n" \
C_"v202, v204, v205\n" \
C_"v203, v206, v207\n" \
"buffer_store_dwordx4 v[200:203], v20, s[56:59], s70 offen offset:1024 sc1\n" \
".fill (L_ab + 28672 - .) / 4, 4, 0xBF800000\n" \
"L_ap7:\n" \
"s_cbranch_execz L_ap0\n" \
C_"v208, v208, v209\n" \
"v_mul_f32_e32 v210, v56, v210\n" \
"v_mul_f32_e32 v211, v56, v211\n" \
"v_rcp_f32_e32 v57, v57\n" \
M_"a[112:115], a[120:123], v[200:203], a[112:115]\n" \
"v_mfma_f32_32x32x16_bf16 v[192:207], a[120:123], a[120:123], 0\n" \
C_"v209, v210, v211\n" \
"v_mul_f32_e32 v212, v56, v212\n" \
"v_mul_f32_e32 v213, v56, v213\n" \
"v_mul_f32_e32 v214, v56, v214\n" \
"v_mul_f32_e32 v215, v56, v215\n" \
"v_mul_f32_e32 v216, v56, v216\n" \
"v_mul_f32_e32 v217, v56, v217\n" \
C_"v210, v212, v213\n" \
C_"v211, v214, v215\n" \
"buffer_store_dwordx4 v[208:211], v20, s[56:59], s70 offen offset:2048 sc1\n" \
C_"v216, v216, v217\n" \
"v_mul_f32_e32 v218, v56, v218\n" \
"v_mul_f32_e32 v219, v56, v219\n" \
"v_mul_f32_e32 v57, v57, v61\n" \
M_"a[112:115], a[120:123], v[208:211], a[112:115]\n" \
C_"v217, v218, v219\n" \
"v_mul_f32_e32 v220, v56, v220\n" \
"v_mul_f32_e32 v221, v56, v221\n" \
"v_mul_f32_e32 v222, v56, v222\n" \
"v_mul_f32_e32 v223, v56, v223\n" \
"v_mul_f32_e32 v224, v57, v224\n" \
"v_mul_f32_e32 v225, v57, v225\n" \
C_"v218, v220, v221\n" \
C_"v219, v222, v223\n" \
"buffer_store_dwordx4 v[216:219], v20, s[56:59], s70 offen offset:3072 sc1\n" \
C_"v224, v224, v225\n" \
"v_mul_f32_e32 v226, v57, v226\n" \
"v_mul_f32_e32 v227, v57, v227\n" \
M_"a[112:115], a[120:123], v[216:219], a[112:115]\n" \
"v_mfma_f32_32x32x16_bf16 v[208:223], a[120:123], a[120:123], 0\n" \
C_"v225, v226, v227\n" \
"v_mul_f32_e32 v228, v57, v228\n" \
"v_mul_f32_e32 v229, v57, v229\n" \
"v_mul_f32_e32 v230, v57, v230\n" \
"v_mul_f32_e32 v231, v57, v231\n" \
"v_mul_f32_e32 v232, v57, v232\n" \
"v_mul_f32_e32 v233, v57, v233\n" \
C_"v226, v228, v229\n" \
C_"v227, v230, v231\n" \
"buffer_store_dwordx4 v[224:227], v20, s[56:59], s71 offen offset:0 sc1\n" \
C_"v232, v232, v233\n" \
"v_mul_f32_e32 v234, v57, v234\n" \
"v_mul_f32_e32 v235, v57, v235\n" \
M_"a[112:115], a[120:123], v[224:227], a[112:115]\n" \
C_"v233, v234, v235\n" \
"v_mul_f32_e32 v236, v57, v236\n" \
"v_mul_f32_e32 v237, v57, v237\n" \
"v_mul_f32_e32 v238, v57, v238\n" \
"v_mul_f32_e32 v239, v57, v239\n" \
"v_mul_f32_e32 v240, v57, v240\n" \
"v_mul_f32_e32 v241, v57, v241\n" \
C_"v234, v236, v237\n" \
C_"v235, v238, v239\n" \
"buffer_store_dwordx4 v[232:235], v20, s[56:59], s71 offen offset:1024 sc1\n" \
C_"v240, v240, v241\n" \
"v_mul_f32_e32 v242, v57, v242\n" \
"v_mul_f32_e32 v243, v57, v243\n" \
M_"a[112:115], a[120:123], v[232:235], a[112:115]\n" \
"v_mfma_f32_32x32x16_bf16 v[224:239], a[120:123], a[120:123], 0\n" \
C_"v241, v242, v243\n" \
"v_mul_f32_e32 v244, v57, v244\n" \
"v_mul_f32_e32 v245, v57, v245\n" \
"v_mul_f32_e32 v246, v57, v246\n" \
"v_mul_f32_e32 v247, v57, v247\n" \
"v_mul_f32_e32 v248, v57, v248\n" \
"v_mul_f32_e32 v249, v57, v249\n" \
C_"v242, v244, v245\n" \
C_"v243, v246, v247\n" \
"buffer_store_dwordx4 v[240:243], v20, s[56:59], s71 offen offset:2048 sc1\n" \
C_"v248, v248, v249\n" \
"v_mul_f32_e32 v250, v57, v250\n" \
"v_mul_f32_e32 v251, v57, v251\n" \
M_"a[112:115], a[120:123], v[240:243], a[112:115]\n" \
C_"v249, v250, v251\n" \
"v_mul_f32_e32 v252, v57, v252\n" \
"v_mul_f32_e32 v253, v57, v253\n" \
"v_mul_f32_e32 v254, v57, v254\n" \
"v_mul_f32_e32 v255, v57, v255\n" \
C_"v250, v252, v253\n" \
C_"v251, v254, v255\n" \
"buffer_store_dwordx4 v[248:251], v20, s[56:59], s71 offen offset:3072 sc1\n" \
"s_add_u32 m0, s91, 0x2000\n" \
M_"a[112:115], a[120:123], v[248:251], a[112:115]\n" \
"v_mfma_f32_32x32x16_bf16 v[240:255], a[120:123], a[120:123], 0\n" \
"v_add_u32_e32 v53, 0x20000, v63\n" \
B_"v8, s[84:87], s92 offen offset:0 lds\n" \
E_"v70, v70\n" \
E_"v71, v71\n" \
E_"v72, v72\n" \
E_"v73, v73\n" \
"s_add_u32 s64, s64, 0x10000\n" \
"v_accvgpr_read_b32 v63, a112\n" \
"v_cmp_u_f32_e32 vcc, v63, v63\n" \
"v_cmp_lt_f32_e64 s[72:73], s21, v62\n" \
"v_readlane_b32 s95, v11, 3\n" \
E_"v68, v68\n" \
E_"v69, v69\n" \
E_"v74, v74\n" \
E_"v75, v75\n" \
"s_or_b64 vcc, s[72:73], vcc\n" \
"s_cselect_b32 s95, 0x40000000, s95\n" \
"s_cselect_b32 s94, 0x7f800000, 0\n" \
C_"v71, v70, v71\n" \
E_"v66, v66\n" \
E_"v67, v67\n" \
E_"v64, v64\n" \
E_"v65, v65\n" \
C_"v72, v72, v73\n" \
E_"v76, v76\n" \
E_"v77, v77\n" \
E_"v78, v78\n" \
E_"v79, v79\n" \
C_"v70, v68, v69\n" \
"s_waitcnt vmcnt(17) lgkmcnt(0)\n" \
"s_barrier\n" \
C_"v73, v74, v75\n" \
"v_add_u32_e32 v49, 0x10000, v48\n" \
"s_xor_b32 s65, s65, 0x8000\n" \
C_"v69, v66, v67\n" \
C_"v68, v64, v65\n" \
C_"v74, v76, v77\n" \
C_"v75, v78, v79\n" \
"v_xor_b32_e32 v48, 0x8000, v48\n" \
"s_sub_u32 s66, s19, 2\n" \
"s_branch L_f1_%=\n" \
".fill (L_ab + 32768 - .) / 4, 4, 0xBF800000\n" \
".fill (L_ab + 32768 - .) / 4, 4, 0xBF800000\n" \
"L_ap8:\n" \
"s_setpc_b64 s[88:89]\n" \
"L_out_%=:\n" \
"v_bfe_u32 v38, v20, 2, 4\n" \
"v_readlane_b32 s64, v11, 2\n" \
"v_add_u32_e32 v32, 0x49000, v20\n" \
"v_add_u32_e32 v34, 0x20000, v10\n" \
X_"a[0:1], v34 offset:0\n" \
X_"a[2:3], v34 offset:4608\n" \
X_"a[4:5], v34 offset:9216\n" \
"buffer_load_dword v26, v38, s[56:59], s64 offen sc1\n" \
X_"a[6:7], v34 offset:13824\n" \
X_"a[8:9], v34 offset:32\n" \
X_"a[10:11], v34 offset:4640\n" \
X_"a[12:13], v34 offset:9248\n" \
"buffer_store_dwordx4 v[64:67], v20, s[56:59], s61 offen offset:0 sc1\n" \
X_"a[14:15], v34 offset:13856\n" \
"v_mov_b32_e32 v39, s62\n" \
"v_readlane_b32 s70, v11, 5\n" \
"v_readlane_b32 s71, v11, 4\n" \
"buffer_store_dwordx4 v[68:71], v20, s[56:59], s61 offen offset:1024 sc1\n" \
X_"a[16:17], v34 offset:64\n" \
X_"a[18:19], v34 offset:4672\n" \
X_"a[20:21], v34 offset:9280\n" \
X_"a[22:23], v34 offset:13888\n" \
"buffer_store_dwordx4 v[72:75], v20, s[56:59], s61 offen offset:2048 sc1\n" \
X_"a[24:25], v34 offset:96\n" \
X_"a[26:27], v34 offset:4704\n" \
X_"a[28:29], v34 offset:9312\n" \
X_"a[30:31], v34 offset:13920\n" \
"buffer_store_dwordx4 v[76:79], v20, s[56:59], s61 offen offset:3072 sc1\n" \
X_"a[96:97], v34 offset:128\n" \
X_"a[98:99], v34 offset:4736\n" \
X_"a[100:101], v34 offset:9344\n" \
X_"a[102:103], v34 offset:13952\n" \
"buffer_store_dwordx4 v[80:83], v32, s[56:59], s61 offen offset:0 sc1\n" \
X_"a[104:105], v34 offset:160\n" \
X_"a[106:107], v34 offset:4768\n" \
X_"a[108:109], v34 offset:9376\n" \
X_"a[110:111], v34 offset:13984\n" \
"buffer_store_dwordx4 v[84:87], v32, s[56:59], s61 offen offset:1024 sc1\n" \
X_"a[112:113], v34 offset:192\n" \
"s_waitcnt vmcnt(16) lgkmcnt(15)\n" \
M_"v[112:115], a[0:3], a[192:195], 0\n" \
"buffer_store_dwordx4 v[96:99], v32, s[56:59], s61 offen offset:2048 sc1\n" \
M_"v[112:115], a[4:7], a[196:199], v[112:115]\n" \
X_"a[114:115], v34 offset:4800\n" \
X_"a[116:117], v34 offset:9408\n" \
"buffer_store_dwordx4 v[100:103], v32, s[56:59], s61 offen offset:3072 sc1\n" \
M_"v[116:119], a[0:3], a[208:211], 0\n" \
X_"a[118:119], v34 offset:14016\n" \
X_"a[120:121], v34 offset:224\n" \
M_"v[116:119], a[4:7], a[212:215], v[116:119]\n" \
X_"a[122:123], v34 offset:4832\n" \
"v_lshrrev_b32_e32 v44, 9, v20\n" \
M_"v[184:187], a[0:3], v[64:67], 0\n" \
"v_mul_u32_u24_e32 v44, 0xb40, v44\n" \
"v_add_u32_e32 v35, v44, v10\n" \
M_"v[184:187], a[4:7], v[68:71], v[184:187]\n" \
"v_readlane_b32 s65, v11, 33\n" \
"v_readlane_b32 s67, v11, 26\n" \
M_"v[188:191], a[0:3], v[80:83], 0\n" \
X_"a[0:1], v34 offset:9440\n" \
X_"a[2:3], v34 offset:14048\n" \
M_"v[188:191], a[4:7], v[84:87], v[188:191]\n" \
X_"a[4:5], v34 offset:256\n" \
X_"a[6:7], v34 offset:4864\n" \
M_"v[196:199], a[8:11], v[80:83], 0\n" \
"v_readlane_b32 s80, v11, 11\n" \
"v_readlane_b32 s81, v11, 12\n" \
M_"v[196:199], a[12:15], v[84:87], v[196:199]\n" \
"s_mov_b32 s82, 0x47fca0\n" \
"s_mov_b32 s83, 0x20000\n" \
M_"v[192:195], a[8:11], v[64:67], 0\n" \
"v_readlane_b32 s66, v11, 31\n" \
"s_add_u32 m0, s67, 0x0\n" \
M_"v[192:195], a[12:15], v[68:71], v[192:195]\n" \
"v_readlane_b32 s92, v11, 27\n" \
"v_readlane_b32 s73, v11, 32\n" \
M_"v[124:127], a[8:11], a[208:211], 0\n" \
B_"v0, s[80:83], s66 offen offset:0 lds\n" \
M_"v[124:127], a[12:15], a[212:215], v[124:127]\n" \
"v_readlane_b32 s94, v11, 28\n" \
"v_readlane_b32 s84, v11, 9\n" \
M_"v[120:123], a[8:11], a[192:195], 0\n" \
X_"a[8:9], v34 offset:9472\n" \
X_"a[10:11], v34 offset:14080\n" \
M_"v[120:123], a[12:15], a[196:199], v[120:123]\n" \
X_"a[12:13], v34 offset:18432\n" \
X_"a[14:15], v34 offset:23040\n" \
M_"v[128:131], a[16:19], a[192:195], 0\n" \
"v_readlane_b32 s85, v11, 10\n" \
"s_waitcnt lgkmcnt(9)\n" \
M_"v[128:131], a[20:23], a[196:199], v[128:131]\n" \
B_"v1, s[80:83], s66 offen offset:1024 lds\n" \
M_"v[132:135], a[16:19], a[208:211], 0\n" \
"s_mov_b32 s86, 0xa1ca0\n" \
"s_mov_b32 s87, 0x20000\n" \
M_"v[132:135], a[20:23], a[212:215], v[132:135]\n" \
"v_add_u32_e32 v33, 0x10000, v10\n" \
"v_readlane_b32 s68, v11, 34\n" \
M_"v[200:203], a[16:19], v[64:67], 0\n" \
"v_readlane_b32 s69, v11, 35\n" \
"v_readlane_b32 s72, v11, 37\n" \
M_"v[200:203], a[20:23], v[68:71], v[200:203]\n" \
"v_readlane_b32 s93, v11, 38\n" \
"v_readlane_b32 s63, v11, 44\n" \
M_"v[204:207], a[16:19], v[80:83], 0\n" \
X_"a[16:17], v34 offset:18464\n" \
X_"a[18:19], v34 offset:23072\n" \
M_"v[204:207], a[20:23], v[84:87], v[204:207]\n" \
X_"a[20:21], v34 offset:18496\n" \
X_"a[22:23], v34 offset:23104\n" \
M_"v[212:215], a[24:27], v[80:83], 0\n" \
B_"v2, s[80:83], s66 offen offset:2048 lds\n" \
M_"v[212:215], a[28:31], v[84:87], v[212:215]\n" \
"v_bfe_u32 v40, v20, 2, 7\n" \
"s_add_u32 s74, s63, 0x3800\n" \
M_"v[208:211], a[24:27], v[64:67], 0\n" \
"v_lshlrev_b32_e32 v41, 12, v20\n" \
"s_add_u32 s12, s63, 0x0\n" \
M_"v[208:211], a[28:31], v[68:71], v[208:211]\n" \
"v_readlane_b32 s95, v11, 41\n" \
"v_readlane_b32 s96, v11, 42\n" \
M_"v[140:143], a[24:27], a[208:211], 0\n" \
"s_add_u32 s13, s63, 0x1000\n" \
"v_readlane_b32 s97, v11, 43\n" \
M_"v[140:143], a[28:31], a[212:215], v[140:143]\n" \
B_"v3, s[80:83], s66 offen offset:3072 lds\n" \
M_"v[136:139], a[24:27], a[192:195], 0\n" \
X_"a[24:25], v34 offset:18528\n" \
X_"a[26:27], v34 offset:23136\n" \
M_"v[136:139], a[28:31], a[196:199], v[136:139]\n" \
X_"a[28:29], v34 offset:18560\n" \
X_"a[30:31], v34 offset:23168\n" \
M_"v[144:147], a[96:99], a[192:195], 0\n" \
"s_add_u32 m0, s67, 0x1000\n" \
"s_add_u32 s14, s63, 0x2000\n" \
M_"v[144:147], a[100:103], a[196:199], v[144:147]\n" \
"s_add_u32 s15, s63, 0x3000\n" \
"v_readlane_b32 s100, v11, 7\n" \
M_"v[148:151], a[96:99], a[208:211], 0\n" \
B_"v4, s[80:83], s66 offen offset:0 lds\n" \
M_"v[148:151], a[100:103], a[212:215], v[148:151]\n" \
"v_readlane_b32 s16, v11, 45\n" \
"v_bfe_u32 v44, v20, 4, 4\n" \
M_"v[216:219], a[96:99], v[64:67], 0\n" \
"s_add_u32 s17, s16, 0x1000\n" \
"s_add_u32 s18, s16, 0x2000\n" \
M_"v[216:219], a[100:103], v[68:71], v[216:219]\n" \
"s_add_u32 s19, s16, 0x3000\n" \
"v_bfe_u32 v45, v20, 8, 2\n" \
M_"v[220:223], a[96:99], v[80:83], 0\n" \
X_"a[96:97], v34 offset:18592\n" \
X_"a[98:99], v34 offset:23200\n" \
M_"v[220:223], a[100:103], v[84:87], v[220:223]\n" \
X_"a[100:101], v34 offset:18624\n" \
X_"a[102:103], v34 offset:23232\n" \
M_"v[228:231], a[104:107], v[80:83], 0\n" \
B_"v5, s[80:83], s66 offen offset:1024 lds\n" \
M_"v[228:231], a[108:111], v[84:87], v[228:231]\n" \
"v_mul_u32_u24_e32 v42, 0x480, v44\n" \
"v_lshl_add_u32 v42, v45, 3, v42\n" \
M_"v[224:227], a[104:107], v[64:67], 0\n" \
"s_waitcnt vmcnt(14)\n" \
"v_cmp_ne_u32_e32 vcc, s62, v26\n" \
M_"v[224:227], a[108:111], v[68:71], v[224:227]\n" \
"v_lshrrev_b32_e32 v44, 1, v44\n" \
"v_lshlrev_b32_e32 v45, 1, v45\n" \
M_"v[156:159], a[104:107], a[208:211], 0\n" \
"v_sub_u32_e32 v44, v44, v45\n" \
"s_cbranch_vccnz L_sa_%=\n" \
"L_ra_%=:\n" \
M_"v[156:159], a[108:111], a[212:215], v[156:159]\n" \
B_"a[64:67], v20, s[56:59], s65 offen offset:0 sc1\n" \
M_"v[152:155], a[104:107], a[192:195], 0\n" \
X_"a[104:105], v34 offset:18656\n" \
X_"a[106:107], v34 offset:23264\n" \
M_"v[152:155], a[108:111], a[196:199], v[152:155]\n" \
X_"a[108:109], v34 offset:18688\n" \
X_"a[110:111], v34 offset:23296\n" \
M_"v[160:163], a[112:115], a[192:195], 0\n" \
"v_readlane_b32 s64, v11, 36\n" \
"v_and_b32_e32 v45, 16, v20\n" \
M_"v[160:163], a[116:119], a[196:199], v[160:163]\n" \
"v_mov_b32_e32 v46, 0x3f80\n" \
"v_lshlrev_b32_e32 v45, v45, v46\n" \
M_"v[164:167], a[112:115], a[208:211], 0\n" \
B_"a[68:71], v20, s[56:59], s65 offen offset:1024 sc1\n" \
M_"v[164:167], a[116:119], a[212:215], v[164:167]\n" \
"v_mov_b32_e32 v52, 0\n" \
"v_mov_b32_e32 v53, 0\n" \
M_"v[232:235], a[112:115], v[64:67], 0\n" \
"v_mov_b32_e32 v48, 0\n" \
"v_mov_b32_e32 v49, 0\n" \
M_"v[232:235], a[116:119], v[68:71], v[232:235]\n" \
"v_add_u32_e32 v43, 0x9000, v42\n" \
"v_readlane_b32 s88, v11, 13\n" \
M_"v[236:239], a[112:115], v[80:83], 0\n" \
X_"a[112:113], v10 offset:32768\n" \
X_"a[114:115], v10 offset:37376\n" \
M_"v[236:239], a[116:119], v[84:87], v[236:239]\n" \
X_"a[116:117], v10 offset:32800\n" \
"s_waitcnt lgkmcnt(7)\n" \
M_"v[244:247], a[120:123], v[80:83], 0\n" \
X_"a[118:119], v10 offset:37408\n" \
"v_readlane_b32 s89, v11, 14\n" \
M_"v[244:247], a[0:3], v[84:87], v[244:247]\n" \
B_"a[80:83], v32, s[56:59], s65 offen offset:0 sc1\n" \
M_"v[240:243], a[120:123], v[64:67], 0\n" \
"s_mov_b32 s90, 0x240000\n" \
"s_mov_b32 s91, 0x20000\n" \
M_"v[240:243], a[0:3], v[68:71], v[240:243]\n" \
"v_mov_b32_e32 v46, s62\n" \
M_"v[172:175], a[120:123], a[208:211], 0\n" \
M_"v[172:175], a[0:3], a[212:215], v[172:175]\n" \
M_"v[168:171], a[120:123], a[192:195], 0\n" \
X_"a[120:121], v10 offset:32832\n" \
X_"a[122:123], v10 offset:37440\n" \
M_"v[168:171], a[0:3], a[196:199], v[168:171]\n" \
X_"a[0:1], v10 offset:32864\n" \
X_"a[2:3], v10 offset:37472\n" \
M_"v[176:179], a[4:7], a[192:195], 0\n" \
B_"a[84:87], v32, s[56:59], s65 offen offset:1024 sc1\n" \
M_"v[176:179], a[8:11], a[196:199], v[176:179]\n" \
M_"v[180:183], a[4:7], a[208:211], 0\n" \
M_"v[180:183], a[8:11], a[212:215], v[180:183]\n" \
M_"v[248:251], a[4:7], v[64:67], 0\n" \
M_"v[248:251], a[8:11], v[68:71], v[248:251]\n" \
B_"a[72:75], v20, s[56:59], s65 offen offset:2048 sc1\n" \
M_"v[252:255], a[4:7], v[80:83], 0\n" \
X_"a[4:5], v10 offset:32896\n" \
X_"a[6:7], v10 offset:37504\n" \
M_"v[252:255], a[8:11], v[84:87], v[252:255]\n" \
X_"a[8:9], v10 offset:32928\n" \
X_"a[10:11], v10 offset:37536\n" \
M_"v[112:115], a[12:15], a[200:203], v[112:115]\n" \
M_"v[116:119], a[12:15], a[216:219], v[116:119]\n" \
M_"v[184:187], a[12:15], v[72:75], v[184:187]\n" \
B_"a[76:79], v20, s[56:59], s65 offen offset:3072 sc1\n" \
M_"v[188:191], a[12:15], v[96:99], v[188:191]\n" \
X_"a[12:13], v10 offset:32960\n" \
X_"a[14:15], v10 offset:37568\n" \
M_"v[196:199], a[16:19], v[96:99], v[196:199]\n" \
M_"v[192:195], a[16:19], v[72:75], v[192:195]\n" \
M_"v[124:127], a[16:19], a[216:219], v[124:127]\n" \
M_"v[120:123], a[16:19], a[200:203], v[120:123]\n" \
X_"a[16:17], v10 offset:32992\n" \
X_"a[18:19], v10 offset:37600\n" \
M_"v[128:131], a[20:23], a[200:203], v[128:131]\n" \
B_"a[88:91], v32, s[56:59], s65 offen offset:2048 sc1\n" \
M_"v[132:135], a[20:23], a[216:219], v[132:135]\n" \
M_"v[200:203], a[20:23], v[72:75], v[200:203]\n" \
M_"v[204:207], a[20:23], v[96:99], v[204:207]\n" \
X_"a[20:21], v10 offset:33024\n" \
X_"a[22:23], v10 offset:37632\n" \
"s_waitcnt lgkmcnt(10)\n" \
"s_barrier\n" \
M_"v[212:215], a[24:27], v[96:99], v[212:215]\n" \
B_"a[92:95], v32, s[56:59], s65 offen offset:3072 sc1\n" \
M_"v[208:211], a[24:27], v[72:75], v[208:211]\n" \
"v_readlane_b32 s65, v11, 49\n" \
M_"v[140:143], a[24:27], a[216:219], v[140:143]\n" \
M_"v[136:139], a[24:27], a[200:203], v[136:139]\n" \
X_"a[24:25], v35 offset:41984\n" \
X_"a[26:27], v35 offset:44288\n" \
M_"v[144:147], a[28:31], a[200:203], v[144:147]\n" \
M_"v[148:151], a[28:31], a[216:219], v[148:151]\n" \
B_"v6, s[80:83], s66 offen offset:2048 lds\n" \
M_"v[216:219], a[28:31], v[72:75], v[216:219]\n" \
M_"v[220:223], a[28:31], v[96:99], v[220:223]\n" \
X_"a[28:29], v35 offset:52352\n" \
X_"a[30:31], v35 offset:54656\n" \
M_"v[228:231], a[96:99], v[96:99], v[228:231]\n" \
M_"v[224:227], a[96:99], v[72:75], v[224:227]\n" \
M_"v[156:159], a[96:99], a[216:219], v[156:159]\n" \
B_"v7, s[80:83], s66 offen offset:3072 lds\n" \
M_"v[152:155], a[96:99], a[200:203], v[152:155]\n" \
X_"a[96:97], v35 offset:42016\n" \
X_"a[98:99], v35 offset:44320\n" \
M_"v[160:163], a[100:103], a[200:203], v[160:163]\n" \
"s_add_u32 m0, s67, 0x2000\n" \
"v_readlane_b32 s67, v11, 51\n" \
M_"v[164:167], a[100:103], a[216:219], v[164:167]\n" \
M_"v[232:235], a[100:103], v[72:75], v[232:235]\n" \
M_"v[236:239], a[100:103], v[96:99], v[236:239]\n" \
X_"a[100:101], v35 offset:52384\n" \
X_"a[102:103], v35 offset:54688\n" \
M_"v[244:247], a[104:107], v[96:99], v[244:247]\n" \
B_"v8, s[80:83], s66 offen offset:0 lds\n" \
M_"v[240:243], a[104:107], v[72:75], v[240:243]\n" \
"s_add_u32 m0, s92, 0x0\n" \
"v_readlane_b32 s66, v11, 50\n" \
M_"v[172:175], a[104:107], a[216:219], v[172:175]\n" \
M_"v[168:171], a[104:107], a[200:203], v[168:171]\n" \
X_"a[104:105], v35 offset:42048\n" \
X_"a[106:107], v35 offset:44352\n" \
M_"v[176:179], a[108:111], a[200:203], v[176:179]\n" \
M_"v[180:183], a[108:111], a[216:219], v[180:183]\n" \
B_"v0, s[80:83], s73 offen offset:0 lds\n" \
M_"v[248:251], a[108:111], v[72:75], v[248:251]\n" \
M_"v[252:255], a[108:111], v[96:99], v[252:255]\n" \
X_"a[108:109], v35 offset:52416\n" \
X_"a[110:111], v35 offset:54720\n" \
M_"v[112:115], a[112:115], a[204:207], v[112:115]\n" \
M_"v[116:119], a[112:115], a[220:223], v[116:119]\n" \
M_"v[184:187], a[112:115], v[76:79], v[184:187]\n" \
B_"v1, s[80:83], s73 offen offset:1024 lds\n" \
M_"v[188:191], a[112:115], v[100:103], v[188:191]\n" \
X_"a[112:113], v35 offset:42080\n" \
X_"a[114:115], v35 offset:44384\n" \
M_"v[196:199], a[116:119], v[100:103], v[196:199]\n" \
M_"v[192:195], a[116:119], v[76:79], v[192:195]\n" \
"s_waitcnt vmcnt(18)\n" \
"s_mov_b64 exec, 1\n" \
"buffer_store_dword v39, off, s[56:59], s70 offset:0 sc1\n" \
"s_mov_b64 exec, -1\n" \
M_"v[124:127], a[116:119], a[220:223], v[124:127]\n" \
"v_readlane_b32 s70, v11, 54\n" \
M_"v[120:123], a[116:119], a[204:207], v[120:123]\n" \
X_"a[116:117], v35 offset:52448\n" \
X_"a[118:119], v35 offset:54752\n" \
M_"v[128:131], a[120:123], a[204:207], v[128:131]\n" \
M_"v[132:135], a[120:123], a[220:223], v[132:135]\n" \
B_"v2, s[80:83], s73 offen offset:2048 lds\n" \
M_"v[200:203], a[120:123], v[76:79], v[200:203]\n" \
M_"v[204:207], a[120:123], v[100:103], v[204:207]\n" \
X_"a[120:121], v35 offset:42112\n" \
X_"a[122:123], v35 offset:44416\n" \
M_"v[212:215], a[0:3], v[100:103], v[212:215]\n" \
M_"v[208:211], a[0:3], v[76:79], v[208:211]\n" \
M_"v[140:143], a[0:3], a[220:223], v[140:143]\n" \
B_"v3, s[80:83], s73 offen offset:3072 lds\n" \
M_"v[136:139], a[0:3], a[204:207], v[136:139]\n" \
X_"a[0:1], v35 offset:52480\n" \
"s_waitcnt lgkmcnt(7)\n" \
M_"v[144:147], a[4:7], a[204:207], v[144:147]\n" \
X_"a[2:3], v35 offset:54784\n" \
"s_add_u32 m0, s92, 0x1000\n" \
M_"v[148:151], a[4:7], a[220:223], v[148:151]\n" \
M_"v[216:219], a[4:7], v[76:79], v[216:219]\n" \
M_"v[220:223], a[4:7], v[100:103], v[220:223]\n" \
X_"a[4:5], v35 offset:42144\n" \
X_"a[6:7], v35 offset:44448\n" \
M_"v[228:231], a[8:11], v[100:103], v[228:231]\n" \
B_"v4, s[80:83], s73 offen offset:0 lds\n" \
M_"v[224:227], a[8:11], v[76:79], v[224:227]\n" \
M_"v[156:159], a[8:11], a[220:223], v[156:159]\n" \
M_"v[152:155], a[8:11], a[204:207], v[152:155]\n" \
X_"a[8:9], v35 offset:52512\n" \
X_"a[10:11], v35 offset:54816\n" \
M_"v[160:163], a[12:15], a[204:207], v[160:163]\n" \
M_"v[164:167], a[12:15], a[220:223], v[164:167]\n" \
B_"v5, s[80:83], s73 offen offset:1024 lds\n" \
M_"v[232:235], a[12:15], v[76:79], v[232:235]\n" \
M_"v[236:239], a[12:15], v[100:103], v[236:239]\n" \
X_"a[12:13], v35 offset:42176\n" \
X_"a[14:15], v35 offset:44480\n" \
M_"v[244:247], a[16:19], v[100:103], v[244:247]\n" \
M_"v[240:243], a[16:19], v[76:79], v[240:243]\n" \
M_"v[172:175], a[16:19], a[220:223], v[172:175]\n" \
B_"v6, s[80:83], s73 offen offset:2048 lds\n" \
M_"v[168:171], a[16:19], a[204:207], v[168:171]\n" \
X_"a[16:17], v35 offset:52544\n" \
X_"a[18:19], v35 offset:54848\n" \
M_"v[176:179], a[20:23], a[204:207], v[176:179]\n" \
M_"v[180:183], a[20:23], a[220:223], v[180:183]\n" \
M_"v[248:251], a[20:23], v[76:79], v[248:251]\n" \
M_"v[252:255], a[20:23], v[100:103], v[252:255]\n" \
X_"a[20:21], v35 offset:42208\n" \
X_"a[22:23], v35 offset:44512\n" \
M_"v[112:115], a[24:27], a[124:127], v[112:115]\n" \
B_"v7, s[80:83], s73 offen offset:3072 lds\n" \
M_"v[112:115], a[28:31], a[224:227], v[112:115]\n" \
"s_add_u32 m0, s92, 0x2000\n" \
"v_readlane_b32 s92, v11, 49\n" \
M_"v[116:119], a[24:27], a[228:231], v[116:119]\n" \
M_"v[116:119], a[28:31], a[232:235], v[116:119]\n" \
M_"v[184:187], a[24:27], a[236:239], v[184:187]\n" \
M_"v[184:187], a[28:31], a[240:243], v[184:187]\n" \
B_"v8, s[80:83], s73 offen offset:0 lds\n" \
M_"v[188:191], a[24:27], a[244:247], v[188:191]\n" \
X_"a[24:25], v35 offset:52576\n" \
X_"a[26:27], v35 offset:54880\n" \
M_"v[188:191], a[28:31], a[248:251], v[188:191]\n" \
"s_mov_b32 m0, s94\n" \
X_"a[28:29], v35 offset:42240\n" \
M_"v[196:199], a[96:99], a[244:247], v[196:199]\n" \
X_"a[30:31], v35 offset:44544\n" \
"v_readlane_b32 s73, v11, 6\n" \
M_"v[196:199], a[100:103], a[248:251], v[196:199]\n" \
M_"v[192:195], a[96:99], a[236:239], v[192:195]\n" \
B_"v9, s[84:87], 0 offen lds\n" \
M_"v[192:195], a[100:103], a[240:243], v[192:195]\n" \
M_"v[124:127], a[96:99], a[228:231], v[124:127]\n" \
M_"v[124:127], a[100:103], a[232:235], v[124:127]\n" \
M_"v[120:123], a[96:99], a[124:127], v[120:123]\n" \
X_"a[96:97], v35 offset:52608\n" \
X_"a[98:99], v35 offset:54912\n" \
M_"v[120:123], a[100:103], a[224:227], v[120:123]\n" \
X_"a[100:101], v33 offset:0\n" \
X_"a[102:103], v33 offset:4608\n" \
M_"v[128:131], a[104:107], a[124:127], v[128:131]\n" \
B_"a[128:131], v20, s[56:59], s68 offen offset:0 sc1\n" \
M_"v[128:131], a[108:111], a[224:227], v[128:131]\n" \
M_"v[132:135], a[104:107], a[228:231], v[132:135]\n" \
M_"v[132:135], a[108:111], a[232:235], v[132:135]\n" \
M_"v[200:203], a[104:107], a[236:239], v[200:203]\n" \
M_"v[200:203], a[108:111], a[240:243], v[200:203]\n" \
B_"a[132:135], v20, s[56:59], s68 offen offset:1024 sc1\n" \
M_"v[204:207], a[104:107], a[244:247], v[204:207]\n" \
X_"a[104:105], v33 offset:9216\n" \
X_"a[106:107], v33 offset:13824\n" \
M_"v[204:207], a[108:111], a[248:251], v[204:207]\n" \
X_"a[108:109], v33 offset:32\n" \
"s_waitcnt lgkmcnt(7)\n" \
M_"v[212:215], a[112:115], a[244:247], v[212:215]\n" \
X_"a[110:111], v33 offset:4640\n" \
M_"v[212:215], a[116:119], a[248:251], v[212:215]\n" \
M_"v[208:211], a[112:115], a[236:239], v[208:211]\n" \
"buffer_load_dword v47, v38, s[56:59], s71 offen sc1\n" \
M_"v[208:211], a[116:119], a[240:243], v[208:211]\n" \
M_"v[140:143], a[112:115], a[228:231], v[140:143]\n" \
M_"v[140:143], a[116:119], a[232:235], v[140:143]\n" \
M_"v[136:139], a[112:115], a[124:127], v[136:139]\n" \
X_"a[112:113], v33 offset:9248\n" \
X_"a[114:115], v33 offset:13856\n" \
M_"v[136:139], a[116:119], a[224:227], v[136:139]\n" \
X_"a[116:117], v33 offset:64\n" \
X_"a[118:119], v33 offset:4672\n" \
M_"v[144:147], a[120:123], a[124:127], v[144:147]\n" \
B_"a[144:147], v32, s[56:59], s68 offen offset:0 sc1\n" \
M_"v[144:147], a[0:3], a[224:227], v[144:147]\n" \
M_"v[148:151], a[120:123], a[228:231], v[148:151]\n" \
M_"v[148:151], a[0:3], a[232:235], v[148:151]\n" \
M_"v[216:219], a[120:123], a[236:239], v[216:219]\n" \
M_"v[216:219], a[0:3], a[240:243], v[216:219]\n" \
B_"a[148:151], v32, s[56:59], s68 offen offset:1024 sc1\n" \
M_"v[220:223], a[120:123], a[244:247], v[220:223]\n" \
X_"a[120:121], v33 offset:9280\n" \
X_"a[122:123], v33 offset:13888\n" \
M_"v[220:223], a[0:3], a[248:251], v[220:223]\n" \
X_"a[0:1], v33 offset:96\n" \
X_"a[2:3], v33 offset:4704\n" \
M_"v[228:231], a[4:7], a[244:247], v[228:231]\n" \
M_"v[228:231], a[8:11], a[248:251], v[228:231]\n" \
M_"v[224:227], a[4:7], a[236:239], v[224:227]\n" \
B_"a[152:155], v32, s[56:59], s68 offen offset:2048 sc1\n" \
M_"v[224:227], a[8:11], a[240:243], v[224:227]\n" \
M_"v[156:159], a[4:7], a[228:231], v[156:159]\n" \
M_"v[156:159], a[8:11], a[232:235], v[156:159]\n" \
M_"v[152:155], a[4:7], a[124:127], v[152:155]\n" \
X_"a[4:5], v33 offset:9312\n" \
X_"a[6:7], v33 offset:13920\n" \
M_"v[152:155], a[8:11], a[224:227], v[152:155]\n" \
X_"a[8:9], v33 offset:128\n" \
X_"a[10:11], v33 offset:4736\n" \
M_"v[160:163], a[12:15], a[124:127], v[160:163]\n" \
B_"a[156:159], v32, s[56:59], s68 offen offset:3072 sc1\n" \
M_"v[160:163], a[16:19], a[224:227], v[160:163]\n" \
M_"v[164:167], a[12:15], a[228:231], v[164:167]\n" \
M_"v[164:167], a[16:19], a[232:235], v[164:167]\n" \
M_"v[232:235], a[12:15], a[236:239], v[232:235]\n" \
M_"v[232:235], a[16:19], a[240:243], v[232:235]\n" \
B_"a[136:139], v20, s[56:59], s68 offen offset:2048 sc1\n" \
M_"v[236:239], a[12:15], a[244:247], v[236:239]\n" \
X_"a[12:13], v33 offset:9344\n" \
X_"a[14:15], v33 offset:13952\n" \
M_"v[236:239], a[16:19], a[248:251], v[236:239]\n" \
X_"a[16:17], v33 offset:160\n" \
X_"a[18:19], v33 offset:4768\n" \
M_"v[244:247], a[20:23], a[244:247], v[244:247]\n" \
M_"v[244:247], a[24:27], a[248:251], v[244:247]\n" \
M_"v[240:243], a[20:23], a[236:239], v[240:243]\n" \
B_"a[140:143], v20, s[56:59], s68 offen offset:3072 sc1\n" \
M_"v[240:243], a[24:27], a[240:243], v[240:243]\n" \
"v_readlane_b32 s68, v11, 52\n" \
M_"v[172:175], a[20:23], a[228:231], v[172:175]\n" \
M_"v[172:175], a[24:27], a[232:235], v[172:175]\n" \
M_"v[168:171], a[20:23], a[124:127], v[168:171]\n" \
X_"a[20:21], v33 offset:9376\n" \
X_"a[22:23], v33 offset:13984\n" \
M_"v[168:171], a[24:27], a[224:227], v[168:171]\n" \
X_"a[24:25], v33 offset:192\n" \
X_"a[26:27], v33 offset:4800\n" \
M_"v[176:179], a[28:31], a[124:127], v[176:179]\n" \
"s_waitcnt lgkmcnt(8)\n" \
M_"v[176:179], a[96:99], a[224:227], v[176:179]\n" \
B_"a[32:35], v20, s[56:59], s69 offen offset:0 sc1\n" \
M_"v[180:183], a[28:31], a[228:231], v[180:183]\n" \
M_"v[180:183], a[96:99], a[232:235], v[180:183]\n" \
M_"v[248:251], a[28:31], a[236:239], v[248:251]\n" \
M_"v[248:251], a[96:99], a[240:243], v[248:251]\n" \
M_"v[252:255], a[28:31], a[244:247], v[252:255]\n" \
X_"a[28:29], v33 offset:9408\n" \
X_"a[30:31], v33 offset:14016\n" \
M_"v[252:255], a[96:99], a[248:251], v[252:255]\n" \
X_"a[96:97], v33 offset:224\n" \
"s_waitcnt vmcnt(18)\n" \
M_"v[112:115], a[100:103], a[64:67], v[112:115]\n" \
B_"a[36:39], v20, s[56:59], s69 offen offset:1024 sc1\n" \
M_"v[112:115], a[104:107], a[68:71], v[112:115]\n" \
X_"a[98:99], v33 offset:4832\n" \
M_"v[116:119], a[100:103], a[80:83], v[116:119]\n" \
X_"a[100:101], v33 offset:9440\n" \
X_"a[102:103], v33 offset:14048\n" \
M_"v[116:119], a[104:107], a[84:87], v[116:119]\n" \
X_"a[104:105], v33 offset:256\n" \
X_"a[106:107], v33 offset:4864\n" \
M_"v[124:127], a[108:111], a[80:83], v[124:127]\n" \
M_"v[124:127], a[112:115], a[84:87], v[124:127]\n" \
B_"a[48:51], v32, s[56:59], s69 offen offset:0 sc1\n" \
M_"v[120:123], a[108:111], a[64:67], v[120:123]\n" \
X_"a[108:109], v33 offset:9472\n" \
X_"a[110:111], v33 offset:14080\n" \
M_"v[120:123], a[112:115], a[68:71], v[120:123]\n" \
X_"a[112:113], v33 offset:18432\n" \
X_"a[114:115], v33 offset:23040\n" \
M_"v[128:131], a[116:119], a[64:67], v[128:131]\n" \
M_"v[128:131], a[120:123], a[68:71], v[128:131]\n" \
M_"v[132:135], a[116:119], a[80:83], v[132:135]\n" \
B_"a[52:55], v32, s[56:59], s69 offen offset:1024 sc1\n" \
M_"v[132:135], a[120:123], a[84:87], v[132:135]\n" \
X_"a[116:117], v33 offset:27648\n" \
X_"a[118:119], v33 offset:32256\n" \
M_"v[140:143], a[0:3], a[80:83], v[140:143]\n" \
X_"a[120:121], v33 offset:18464\n" \
X_"a[122:123], v33 offset:23072\n" \
M_"v[140:143], a[4:7], a[84:87], v[140:143]\n" \
"s_waitcnt vmcnt(10)\n" \
"v_cmp_ne_u32_e32 vcc, s62, v47\n" \
M_"v[136:139], a[0:3], a[64:67], v[136:139]\n" \
X_"a[0:1], v33 offset:27680\n" \
".fill (L_ab + 36864 - .) / 4, 4, 0xBF800000\n" \
"L_ap9:\n" \
"s_cbranch_execz L_ap0\n" \
X_"a[2:3], v33 offset:32288\n" \
M_"v[136:139], a[4:7], a[68:71], v[136:139]\n" \
B_"a[56:59], v32, s[56:59], s69 offen offset:2048 sc1\n" \
M_"v[144:147], a[8:11], a[64:67], v[144:147]\n" \
X_"a[4:5], v33 offset:18496\n" \
"s_waitcnt lgkmcnt(13)\n" \
M_"v[144:147], a[12:15], a[68:71], v[144:147]\n" \
X_"a[6:7], v33 offset:23104\n" \
"s_cbranch_vccnz L_sb_%=\n" \
"L_rb_%=:\n" \
M_"v[148:151], a[8:11], a[80:83], v[148:151]\n" \
X_"a[8:9], v33 offset:27712\n" \
X_"a[10:11], v33 offset:32320\n" \
M_"v[148:151], a[12:15], a[84:87], v[148:151]\n" \
X_"a[12:13], v33 offset:18528\n" \
X_"a[14:15], v33 offset:23136\n" \
B_"a[60:63], v32, s[56:59], s69 offen offset:3072 sc1\n" \
M_"v[156:159], a[16:19], a[80:83], v[156:159]\n" \
"v_readlane_b32 s71, v11, 55\n" \
"v_cmp_eq_u32_e32 vcc, 0, v44\n" \
M_"v[156:159], a[20:23], a[84:87], v[156:159]\n" \
M_"v[152:155], a[16:19], a[64:67], v[152:155]\n" \
X_"a[16:17], v33 offset:27744\n" \
X_"a[18:19], v33 offset:32352\n" \
M_"v[152:155], a[20:23], a[68:71], v[152:155]\n" \
X_"a[20:21], v33 offset:18560\n" \
X_"a[22:23], v33 offset:23168\n" \
B_"a[40:43], v20, s[56:59], s69 offen offset:2048 sc1\n" \
M_"v[160:163], a[24:27], a[64:67], v[160:163]\n" \
"v_cndmask_b32_e32 v50, 0, v45, vcc\n" \
"v_cmp_eq_u32_e32 vcc, 1, v44\n" \
M_"v[160:163], a[28:31], a[68:71], v[160:163]\n" \
"v_and_b32_e32 v44, 0x3c0, v20\n" \
"v_cndmask_b32_e32 v51, 0, v45, vcc\n" \
M_"v[164:167], a[24:27], a[80:83], v[164:167]\n" \
X_"a[24:25], v33 offset:27776\n" \
X_"a[26:27], v33 offset:32384\n" \
M_"v[164:167], a[28:31], a[84:87], v[164:167]\n" \
X_"a[28:29], v33 offset:18592\n" \
X_"a[30:31], v33 offset:23200\n" \
B_"a[44:47], v20, s[56:59], s69 offen offset:3072 sc1\n" \
M_"v[172:175], a[96:99], a[80:83], v[172:175]\n" \
"v_readlane_b32 s69, v11, 53\n" \
"v_bfe_u32 v45, v20, 1, 5\n" \
M_"v[172:175], a[100:103], a[84:87], v[172:175]\n" \
"v_and_b32_e32 v45, 24, v45\n" \
"v_add_u32_e32 v45, 0x4800, v45\n" \
M_"v[168:171], a[96:99], a[64:67], v[168:171]\n" \
X_"a[96:97], v33 offset:27808\n" \
X_"a[98:99], v33 offset:32416\n" \
M_"v[168:171], a[100:103], a[68:71], v[168:171]\n" \
X_"a[100:101], v33 offset:18624\n" \
"s_waitcnt lgkmcnt(13)\n" \
M_"v[176:179], a[104:107], a[64:67], v[176:179]\n" \
X_"a[102:103], v33 offset:23232\n" \
"v_add_u32_e32 v36, v44, v45\n" \
M_"v[176:179], a[108:111], a[68:71], v[176:179]\n" \
B_"a[124:127], v20, s[56:59], s64 offen offset:0 sc1\n" \
M_"v[180:183], a[104:107], a[80:83], v[180:183]\n" \
X_"a[104:105], v33 offset:27840\n" \
X_"a[106:107], v33 offset:32448\n" \
M_"v[180:183], a[108:111], a[84:87], v[180:183]\n" \
X_"a[108:109], v33 offset:18656\n" \
X_"a[110:111], v33 offset:23264\n" \
M_"v[112:115], a[112:115], a[72:75], v[112:115]\n" \
M_"v[112:115], a[116:119], a[76:79], v[112:115]\n" \
M_"v[116:119], a[112:115], a[88:91], v[116:119]\n" \
X_"a[112:113], v33 offset:27872\n" \
X_"a[114:115], v33 offset:32480\n" \
M_"v[116:119], a[116:119], a[92:95], v[116:119]\n" \
X_"a[116:117], v33 offset:18688\n" \
X_"a[118:119], v33 offset:23296\n" \
M_"v[124:127], a[120:123], a[88:91], v[124:127]\n" \
B_"a[224:227], v20, s[56:59], s64 offen offset:1024 sc1\n" \
M_"v[124:127], a[0:3], a[92:95], v[124:127]\n" \
M_"v[120:123], a[120:123], a[72:75], v[120:123]\n" \
X_"a[120:121], v33 offset:27904\n" \
X_"a[122:123], v33 offset:32512\n" \
M_"v[120:123], a[0:3], a[76:79], v[120:123]\n" \
"s_barrier\n" \
M_"v[128:131], a[4:7], a[72:75], v[128:131]\n" \
X_"a[0:1], v33 offset:36864\n" \
X_"a[2:3], v33 offset:41472\n" \
M_"v[128:131], a[8:11], a[76:79], v[128:131]\n" \
B_"a[236:239], v32, s[56:59], s64 offen offset:0 sc1\n" \
M_"v[132:135], a[4:7], a[88:91], v[132:135]\n" \
X_"a[4:5], v33 offset:46080\n" \
X_"a[6:7], v33 offset:50688\n" \
M_"v[132:135], a[8:11], a[92:95], v[132:135]\n" \
X_"a[8:9], v33 offset:55296\n" \
"s_waitcnt lgkmcnt(15)\n" \
M_"v[140:143], a[12:15], a[88:91], v[140:143]\n" \
X_"a[10:11], v33 offset:59904\n" \
M_"v[140:143], a[16:19], a[92:95], v[140:143]\n" \
M_"v[136:139], a[12:15], a[72:75], v[136:139]\n" \
X_"a[12:13], v10 offset:0\n" \
X_"a[14:15], v10 offset:4608\n" \
M_"v[136:139], a[16:19], a[76:79], v[136:139]\n" \
X_"a[16:17], v34 offset:0\n" \
X_"a[18:19], v34 offset:4608\n" \
M_"v[144:147], a[20:23], a[72:75], v[144:147]\n" \
B_"a[240:243], v32, s[56:59], s64 offen offset:1024 sc1\n" \
M_"v[144:147], a[24:27], a[76:79], v[144:147]\n" \
M_"v[148:151], a[20:23], a[88:91], v[148:151]\n" \
X_"a[20:21], v34 offset:9216\n" \
X_"a[22:23], v34 offset:13824\n" \
M_"v[148:151], a[24:27], a[92:95], v[148:151]\n" \
X_"a[24:25], v34 offset:18432\n" \
X_"a[26:27], v34 offset:23040\n" \
M_"v[156:159], a[28:31], a[88:91], v[156:159]\n" \
M_"v[156:159], a[96:99], a[92:95], v[156:159]\n" \
B_"a[244:247], v32, s[56:59], s64 offen offset:2048 sc1\n" \
M_"v[152:155], a[28:31], a[72:75], v[152:155]\n" \
X_"a[28:29], v10 offset:9216\n" \
X_"a[30:31], v10 offset:13824\n" \
M_"v[152:155], a[96:99], a[76:79], v[152:155]\n" \
X_"a[96:97], v33 offset:36896\n" \
X_"a[98:99], v33 offset:41504\n" \
M_"v[160:163], a[100:103], a[72:75], v[160:163]\n" \
"s_waitcnt lgkmcnt(15)\n" \
M_"v[160:163], a[104:107], a[76:79], v[160:163]\n" \
M_"v[164:167], a[100:103], a[88:91], v[164:167]\n" \
X_"a[100:101], v33 offset:46112\n" \
X_"a[102:103], v33 offset:50720\n" \
M_"v[164:167], a[104:107], a[92:95], v[164:167]\n" \
X_"a[104:105], v33 offset:55328\n" \
X_"a[106:107], v33 offset:59936\n" \
M_"v[172:175], a[108:111], a[88:91], v[172:175]\n" \
B_"a[248:251], v32, s[56:59], s64 offen offset:3072 sc1\n" \
M_"v[172:175], a[112:115], a[92:95], v[172:175]\n" \
M_"v[168:171], a[108:111], a[72:75], v[168:171]\n" \
X_"a[108:109], v10 offset:32\n" \
X_"a[110:111], v10 offset:4640\n" \
M_"v[168:171], a[112:115], a[76:79], v[168:171]\n" \
X_"a[112:113], v34 offset:32\n" \
X_"a[114:115], v34 offset:4640\n" \
M_"v[176:179], a[116:119], a[72:75], v[176:179]\n" \
M_"v[176:179], a[120:123], a[76:79], v[176:179]\n" \
B_"a[228:231], v20, s[56:59], s64 offen offset:2048 sc1\n" \
M_"v[180:183], a[116:119], a[88:91], v[180:183]\n" \
X_"a[116:117], v34 offset:9248\n" \
X_"a[118:119], v34 offset:13856\n" \
M_"v[180:183], a[120:123], a[92:95], v[180:183]\n" \
X_"a[120:121], v34 offset:18464\n" \
X_"a[122:123], v34 offset:23072\n" \
M_"v[112:115], a[0:3], a[128:131], v[112:115]\n" \
"s_waitcnt lgkmcnt(15)\n" \
M_"v[112:115], a[4:7], a[132:135], v[112:115]\n" \
"s_waitcnt vmcnt(18)\n" \
M_"v[116:119], a[0:3], a[144:147], v[116:119]\n" \
X_"a[0:1], v10 offset:9248\n" \
X_"a[2:3], v10 offset:13856\n" \
M_"v[116:119], a[4:7], a[148:151], v[116:119]\n" \
X_"a[4:5], v33 offset:36928\n" \
X_"a[6:7], v33 offset:41536\n" \
M_"v[116:119], a[8:11], a[152:155], v[116:119]\n" \
"s_waitcnt vmcnt(17)\n" \
M_"v[116:119], a[12:15], a[156:159], v[116:119]\n" \
"s_waitcnt vmcnt(16)\n" \
M_"v[112:115], a[8:11], a[136:139], v[112:115]\n" \
X_"a[8:9], v33 offset:46144\n" \
"s_waitcnt vmcnt(15)\n" \
M_"v[112:115], a[12:15], a[140:143], v[112:115]\n" \
X_"a[10:11], v33 offset:50752\n" \
"s_waitcnt vmcnt(14)\n" \
M_"v[112:115], a[16:19], a[32:35], v[112:115]\n" \
X_"a[12:13], v33 offset:55360\n" \
"s_waitcnt vmcnt(13)\n" \
M_"v[112:115], a[20:23], a[36:39], v[112:115]\n" \
X_"a[14:15], v33 offset:59968\n" \
"s_waitcnt vmcnt(12)\n" \
M_"v[116:119], a[16:19], a[48:51], v[116:119]\n" \
X_"a[16:17], v10 offset:64\n" \
"s_waitcnt vmcnt(11)\n" \
M_"v[116:119], a[20:23], a[52:55], v[116:119]\n" \
X_"a[18:19], v10 offset:4672\n" \
"s_waitcnt vmcnt(10)\n" \
M_"v[116:119], a[24:27], a[56:59], v[116:119]\n" \
X_"a[20:21], v34 offset:64\n" \
X_"a[22:23], v34 offset:4672\n" \
B_"a[232:235], v20, s[56:59], s64 offen offset:3072 sc1\n" \
"s_waitcnt vmcnt(10) lgkmcnt(12)\n" \
M_"v[116:119], a[28:31], a[60:63], v[116:119]\n" \
B_"a[160:163], v20, s[56:59], s72 offen offset:0 sc1\n" \
"s_waitcnt vmcnt(10)\n" \
M_"v[112:115], a[24:27], a[40:43], v[112:115]\n" \
X_"a[24:25], v34 offset:9280\n" \
X_"a[26:27], v34 offset:13888\n" \
B_"a[164:167], v20, s[56:59], s72 offen offset:1024 sc1\n" \
"s_waitcnt vmcnt(10)\n" \
M_"v[112:115], a[28:31], a[44:47], v[112:115]\n" \
X_"a[28:29], v34 offset:18496\n" \
X_"a[30:31], v34 offset:23104\n" \
M_"v[120:123], a[96:99], a[128:131], v[120:123]\n" \
B_"a[176:179], v32, s[56:59], s72 offen offset:0 sc1\n" \
M_"v[120:123], a[100:103], a[132:135], v[120:123]\n" \
M_"v[124:127], a[96:99], a[144:147], v[124:127]\n" \
X_"a[96:97], v10 offset:9280\n" \
X_"a[98:99], v10 offset:13888\n" \
C_"v112, v112, v113\n" \
C_"v113, v114, v115\n" \
C_"v114, v116, v117\n" \
C_"v115, v118, v119\n" \
"buffer_store_dwordx4 v[112:115], v20, s[56:59], s65 offen offset:0 sc1\n" \
"s_add_u32 s65, s65, 0x3800\n" \
M_"v[124:127], a[100:103], a[148:151], v[124:127]\n" \
X_"a[100:101], v33 offset:36960\n" \
X_"a[102:103], v33 offset:41568\n" \
M_"v[124:127], a[104:107], a[152:155], v[124:127]\n" \
M_"v[124:127], a[108:111], a[156:159], v[124:127]\n" \
M_"v[120:123], a[104:107], a[136:139], v[120:123]\n" \
X_"a[104:105], v33 offset:46176\n" \
X_"a[106:107], v33 offset:50784\n" \
M_"v[120:123], a[108:111], a[140:143], v[120:123]\n" \
X_"a[108:109], v33 offset:55392\n" \
X_"a[110:111], v33 offset:60000\n" \
M_"v[120:123], a[112:115], a[32:35], v[120:123]\n" \
B_"a[180:183], v32, s[56:59], s72 offen offset:1024 sc1\n" \
M_"v[120:123], a[116:119], a[36:39], v[120:123]\n" \
M_"v[124:127], a[112:115], a[48:51], v[124:127]\n" \
X_"a[112:113], v10 offset:96\n" \
X_"a[114:115], v10 offset:4704\n" \
M_"v[124:127], a[116:119], a[52:55], v[124:127]\n" \
X_"a[116:117], v34 offset:96\n" \
X_"a[118:119], v34 offset:4704\n" \
M_"v[124:127], a[120:123], a[56:59], v[124:127]\n" \
"s_waitcnt lgkmcnt(12)\n" \
M_"v[124:127], a[0:3], a[60:63], v[124:127]\n" \
B_"a[168:171], v20, s[56:59], s72 offen offset:2048 sc1\n" \
M_"v[120:123], a[120:123], a[40:43], v[120:123]\n" \
X_"a[120:121], v34 offset:9312\n" \
X_"a[122:123], v34 offset:13920\n" \
M_"v[120:123], a[0:3], a[44:47], v[120:123]\n" \
X_"a[0:1], v34 offset:18528\n" \
X_"a[2:3], v34 offset:23136\n" \
M_"v[128:131], a[4:7], a[128:131], v[128:131]\n" \
M_"v[128:131], a[8:11], a[132:135], v[128:131]\n" \
M_"v[132:135], a[4:7], a[144:147], v[132:135]\n" \
X_"a[4:5], v10 offset:9312\n" \
X_"a[6:7], v10 offset:13920\n" \
M_"v[132:135], a[8:11], a[148:151], v[132:135]\n" \
C_"v120, v120, v121\n" \
C_"v121, v122, v123\n" \
C_"v122, v124, v125\n" \
C_"v123, v126, v127\n" \
"buffer_store_dwordx4 v[120:123], v20, s[56:59], s66 offen offset:0 sc1\n" \
"s_add_u32 s66, s66, 0x3000\n" \
M_"v[132:135], a[12:15], a[152:155], v[132:135]\n" \
X_"a[8:9], v33 offset:36992\n" \
X_"a[10:11], v33 offset:41600\n" \
M_"v[132:135], a[16:19], a[156:159], v[132:135]\n" \
M_"v[128:131], a[12:15], a[136:139], v[128:131]\n" \
X_"a[12:13], v33 offset:46208\n" \
X_"a[14:15], v33 offset:50816\n" \
M_"v[128:131], a[16:19], a[140:143], v[128:131]\n" \
X_"a[16:17], v33 offset:55424\n" \
X_"a[18:19], v33 offset:60032\n" \
M_"v[128:131], a[20:23], a[32:35], v[128:131]\n" \
B_"a[184:187], v32, s[56:59], s72 offen offset:2048 sc1\n" \
M_"v[128:131], a[24:27], a[36:39], v[128:131]\n" \
M_"v[132:135], a[20:23], a[48:51], v[132:135]\n" \
X_"a[20:21], v10 offset:128\n" \
X_"a[22:23], v10 offset:4736\n" \
M_"v[132:135], a[24:27], a[52:55], v[132:135]\n" \
X_"a[24:25], v34 offset:128\n" \
X_"a[26:27], v34 offset:4736\n" \
M_"v[132:135], a[28:31], a[56:59], v[132:135]\n" \
"s_waitcnt lgkmcnt(12)\n" \
M_"v[132:135], a[96:99], a[60:63], v[132:135]\n" \
B_"a[172:175], v20, s[56:59], s72 offen offset:3072 sc1\n" \
M_"v[128:131], a[28:31], a[40:43], v[128:131]\n" \
X_"a[28:29], v34 offset:9344\n" \
X_"a[30:31], v34 offset:13952\n" \
M_"v[128:131], a[96:99], a[44:47], v[128:131]\n" \
X_"a[96:97], v34 offset:18560\n" \
X_"a[98:99], v34 offset:23168\n" \
M_"v[136:139], a[100:103], a[128:131], v[136:139]\n" \
M_"v[136:139], a[104:107], a[132:135], v[136:139]\n" \
M_"v[140:143], a[100:103], a[144:147], v[140:143]\n" \
B_"a[188:191], v32, s[56:59], s72 offen offset:3072 sc1\n" \
M_"v[140:143], a[104:107], a[148:151], v[140:143]\n" \
"v_readlane_b32 s72, v11, 56\n" \
C_"v128, v128, v129\n" \
C_"v129, v130, v131\n" \
C_"v130, v132, v133\n" \
C_"v131, v134, v135\n" \
M_"v[140:143], a[108:111], a[152:155], v[140:143]\n" \
"buffer_store_dwordx4 v[128:131], v20, s[56:59], s67 offen offset:0 sc1\n" \
"s_add_u32 s67, s67, 0x2800\n" \
M_"v[140:143], a[112:115], a[156:159], v[140:143]\n" \
X_"a[100:101], v10 offset:9344\n" \
X_"a[102:103], v10 offset:13952\n" \
M_"v[136:139], a[108:111], a[136:139], v[136:139]\n" \
X_"a[104:105], v33 offset:37024\n" \
X_"a[106:107], v33 offset:41632\n" \
M_"v[136:139], a[112:115], a[140:143], v[136:139]\n" \
X_"a[108:109], v33 offset:46240\n" \
X_"a[110:111], v33 offset:50848\n" \
M_"v[136:139], a[116:119], a[32:35], v[136:139]\n" \
X_"a[112:113], v33 offset:55456\n" \
X_"a[114:115], v33 offset:60064\n" \
M_"v[136:139], a[120:123], a[36:39], v[136:139]\n" \
B_"a[192:195], v20, s[56:59], s93 offen offset:0 sc1\n" \
M_"v[140:143], a[116:119], a[48:51], v[140:143]\n" \
X_"a[116:117], v10 offset:160\n" \
X_"a[118:119], v10 offset:4768\n" \
M_"v[140:143], a[120:123], a[52:55], v[140:143]\n" \
X_"a[120:121], v34 offset:160\n" \
X_"a[122:123], v34 offset:4768\n" \
M_"v[140:143], a[0:3], a[56:59], v[140:143]\n" \
"s_waitcnt lgkmcnt(13)\n" \
M_"v[140:143], a[4:7], a[60:63], v[140:143]\n" \
M_"v[136:139], a[0:3], a[40:43], v[136:139]\n" \
B_"a[196:199], v20, s[56:59], s93 offen offset:1024 sc1\n" \
M_"v[136:139], a[4:7], a[44:47], v[136:139]\n" \
X_"a[0:1], v34 offset:9376\n" \
X_"a[2:3], v34 offset:13984\n" \
M_"v[144:147], a[8:11], a[128:131], v[144:147]\n" \
X_"a[4:5], v34 offset:18592\n" \
X_"a[6:7], v34 offset:23200\n" \
M_"v[144:147], a[12:15], a[132:135], v[144:147]\n" \
M_"v[148:151], a[8:11], a[144:147], v[148:151]\n" \
X_"a[8:9], v10 offset:9376\n" \
C_"v136, v136, v137\n" \
C_"v137, v138, v139\n" \
C_"v138, v140, v141\n" \
C_"v139, v142, v143\n" \
"buffer_store_dwordx4 v[136:139], v20, s[56:59], s68 offen offset:0 sc1\n" \
"s_add_u32 s68, s68, 0x2000\n" \
M_"v[148:151], a[12:15], a[148:151], v[148:151]\n" \
X_"a[10:11], v10 offset:13984\n" \
X_"a[12:13], v33 offset:37056\n" \
M_"v[148:151], a[16:19], a[152:155], v[148:151]\n" \
X_"a[14:15], v33 offset:41664\n" \
M_"v[148:151], a[20:23], a[156:159], v[148:151]\n" \
M_"v[144:147], a[16:19], a[136:139], v[144:147]\n" \
B_"a[208:211], v32, s[56:59], s93 offen offset:0 sc1\n" \
M_"v[144:147], a[20:23], a[140:143], v[144:147]\n" \
X_"a[16:17], v33 offset:46272\n" \
X_"a[18:19], v33 offset:50880\n" \
M_"v[144:147], a[24:27], a[32:35], v[144:147]\n" \
X_"a[20:21], v33 offset:55488\n" \
X_"a[22:23], v33 offset:60096\n" \
M_"v[144:147], a[28:31], a[36:39], v[144:147]\n" \
M_"v[148:151], a[24:27], a[48:51], v[148:151]\n" \
X_"a[24:25], v10 offset:192\n" \
X_"a[26:27], v10 offset:4800\n" \
B_"a[212:215], v32, s[56:59], s93 offen offset:1024 sc1\n" \
M_"v[148:151], a[28:31], a[52:55], v[148:151]\n" \
X_"a[28:29], v34 offset:192\n" \
"s_waitcnt lgkmcnt(13)\n" \
M_"v[148:151], a[96:99], a[56:59], v[148:151]\n" \
X_"a[30:31], v34 offset:4800\n" \
M_"v[148:151], a[100:103], a[60:63], v[148:151]\n" \
M_"v[144:147], a[96:99], a[40:43], v[144:147]\n" \
X_"a[96:97], v34 offset:9408\n" \
X_"a[98:99], v34 offset:14016\n" \
B_"a[216:219], v32, s[56:59], s93 offen offset:2048 sc1\n" \
M_"v[144:147], a[100:103], a[44:47], v[144:147]\n" \
X_"a[100:101], v34 offset:18624\n" \
X_"a[102:103], v34 offset:23232\n" \
M_"v[152:155], a[104:107], a[128:131], v[152:155]\n" \
M_"v[152:155], a[108:111], a[132:135], v[152:155]\n" \
M_"v[156:159], a[104:107], a[144:147], v[156:159]\n" \
X_"a[104:105], v10 offset:9408\n" \
X_"a[106:107], v10 offset:14016\n" \
B_"a[220:223], v32, s[56:59], s93 offen offset:3072 sc1\n" \
C_"v144, v144, v145\n" \
C_"v145, v146, v147\n" \
C_"v146, v148, v149\n" \
C_"v147, v150, v151\n" \
M_"v[156:159], a[108:111], a[148:151], v[156:159]\n" \
X_"a[108:109], v33 offset:256\n" \
X_"a[110:111], v33 offset:4864\n" \
"buffer_store_dwordx4 v[144:147], v20, s[56:59], s69 offen offset:0 sc1\n" \
"s_add_u32 s69, s69, 0x1800\n" \
M_"v[156:159], a[112:115], a[152:155], v[156:159]\n" \
M_"v[156:159], a[116:119], a[156:159], v[156:159]\n" \
M_"v[152:155], a[112:115], a[136:139], v[152:155]\n" \
X_"a[112:113], v33 offset:9472\n" \
X_"a[114:115], v33 offset:14080\n" \
M_"v[152:155], a[116:119], a[140:143], v[152:155]\n" \
B_"a[200:203], v20, s[56:59], s93 offen offset:2048 sc1\n" \
M_"v[152:155], a[120:123], a[32:35], v[152:155]\n" \
X_"a[116:117], v33 offset:18688\n" \
X_"a[118:119], v33 offset:23296\n" \
M_"v[152:155], a[0:3], a[36:39], v[152:155]\n" \
M_"v[156:159], a[120:123], a[48:51], v[156:159]\n" \
X_"a[120:121], v33 offset:27904\n" \
X_"a[122:123], v33 offset:32512\n" \
M_"v[156:159], a[0:3], a[52:55], v[156:159]\n" \
X_"a[0:1], v33 offset:37120\n" \
B_"a[204:207], v20, s[56:59], s93 offen offset:3072 sc1\n" \
"s_waitcnt lgkmcnt(11)\n" \
M_"v[156:159], a[4:7], a[56:59], v[156:159]\n" \
X_"a[2:3], v33 offset:41728\n" \
M_"v[156:159], a[8:11], a[60:63], v[156:159]\n" \
M_"v[152:155], a[4:7], a[40:43], v[152:155]\n" \
X_"a[4:5], v33 offset:46336\n" \
X_"a[6:7], v33 offset:50944\n" \
M_"v[152:155], a[8:11], a[44:47], v[152:155]\n" \
X_"a[8:9], v33 offset:55552\n" \
X_"a[10:11], v33 offset:60160\n" \
M_"v[160:163], a[12:15], a[128:131], v[160:163]\n" \
B_"v[64:67], v20, s[56:59], s95 offen offset:0 sc1\n" \
M_"v[160:163], a[16:19], a[132:135], v[160:163]\n" \
M_"v[164:167], a[12:15], a[144:147], v[164:167]\n" \
X_"a[12:13], v10 offset:256\n" \
X_"a[14:15], v10 offset:4864\n" \
C_"v152, v152, v153\n" \
C_"v153, v154, v155\n" \
C_"v154, v156, v157\n" \
C_"v155, v158, v159\n" \
"buffer_store_dwordx4 v[152:155], v20, s[56:59], s70 offen offset:0 sc1\n" \
"s_add_u32 s70, s70, 0x1000\n" \
M_"v[164:167], a[16:19], a[148:151], v[164:167]\n" \
X_"a[16:17], v34 offset:256\n" \
X_"a[18:19], v34 offset:4864\n" \
M_"v[164:167], a[20:23], a[152:155], v[164:167]\n" \
M_"v[164:167], a[24:27], a[156:159], v[164:167]\n" \
M_"v[160:163], a[20:23], a[136:139], v[160:163]\n" \
X_"a[20:21], v34 offset:9472\n" \
X_"a[22:23], v34 offset:14080\n" \
M_"v[160:163], a[24:27], a[140:143], v[160:163]\n" \
X_"a[24:25], v34 offset:18688\n" \
X_"a[26:27], v34 offset:23296\n" \
"s_waitcnt vmcnt(19)\n" \
"s_mov_b64 exec, 1\n" \
"buffer_store_dword v39, off, s[56:59], s65 offset:0 sc1\n" \
"s_mov_b64 exec, -1\n" \
M_"v[160:163], a[28:31], a[32:35], v[160:163]\n" \
M_"v[160:163], a[96:99], a[36:39], v[160:163]\n" \
M_"v[164:167], a[28:31], a[48:51], v[164:167]\n" \
X_"a[28:29], v10 offset:9472\n" \
X_"a[30:31], v10 offset:14080\n" \
M_"v[164:167], a[96:99], a[52:55], v[164:167]\n" \
X_"a[96:97], v33 offset:0\n" \
X_"a[98:99], v33 offset:4608\n" \
M_"v[164:167], a[100:103], a[56:59], v[164:167]\n" \
"s_waitcnt lgkmcnt(12)\n" \
M_"v[164:167], a[104:107], a[60:63], v[164:167]\n" \
B_"v[68:71], v20, s[56:59], s95 offen offset:1024 sc1\n" \
M_"v[160:163], a[100:103], a[40:43], v[160:163]\n" \
X_"a[100:101], v33 offset:9216\n" \
X_"a[102:103], v33 offset:13824\n" \
M_"v[160:163], a[104:107], a[44:47], v[160:163]\n" \
X_"a[104:105], v33 offset:18432\n" \
X_"a[106:107], v33 offset:23040\n" \
M_"v[248:251], a[108:111], a[124:127], v[248:251]\n" \
M_"v[248:251], a[112:115], a[224:227], v[248:251]\n" \
M_"v[252:255], a[108:111], a[236:239], v[252:255]\n" \
X_"a[108:109], v33 offset:27648\n" \
X_"a[110:111], v33 offset:32256\n" \
M_"v[252:255], a[112:115], a[240:243], v[252:255]\n" \
C_"v160, v160, v161\n" \
C_"v161, v162, v163\n" \
C_"v162, v164, v165\n" \
C_"v163, v166, v167\n" \
"buffer_store_dwordx4 v[160:163], v20, s[56:59], s71 offen offset:0 sc1\n" \
"s_add_u32 s71, s71, 0x800\n" \
M_"v[252:255], a[116:119], a[244:247], v[252:255]\n" \
X_"a[112:113], v33 offset:36864\n" \
X_"a[114:115], v33 offset:41472\n" \
M_"v[252:255], a[120:123], a[248:251], v[252:255]\n" \
M_"v[248:251], a[116:119], a[228:231], v[248:251]\n" \
X_"a[116:117], v33 offset:46080\n" \
X_"a[118:119], v33 offset:50688\n" \
"s_waitcnt vmcnt(19)\n" \
"s_mov_b64 exec, 1\n" \
"buffer_store_dword v39, off, s[56:59], s66 offset:4 sc1\n" \
"s_mov_b64 exec, -1\n" \
"v_readlane_b32 s66, v11, 50\n" \
M_"v[248:251], a[120:123], a[232:235], v[248:251]\n" \
X_"a[120:121], v33 offset:55296\n" \
X_"a[122:123], v33 offset:59904\n" \
M_"v[176:179], a[0:3], a[128:131], v[176:179]\n" \
M_"v[176:179], a[4:7], a[132:135], v[176:179]\n" \
M_"v[180:183], a[0:3], a[144:147], v[180:183]\n" \
B_"v[72:75], v32, s[56:59], s95 offen offset:0 sc1\n" \
M_"v[180:183], a[4:7], a[148:151], v[180:183]\n" \
M_"v[248:251], a[0:3], a[160:163], v[248:251]\n" \
M_"v[248:251], a[4:7], a[164:167], v[248:251]\n" \
M_"v[252:255], a[0:3], a[176:179], v[252:255]\n" \
X_"a[0:1], v10 offset:0\n" \
X_"a[2:3], v10 offset:4608\n" \
M_"v[252:255], a[4:7], a[180:183], v[252:255]\n" \
X_"a[4:5], v34 offset:0\n" \
"s_waitcnt vmcnt(20)\n" \
M_"v[252:255], a[8:11], a[184:187], v[252:255]\n" \
X_"a[6:7], v34 offset:4608\n" \
"s_waitcnt vmcnt(18) lgkmcnt(10)\n" \
M_"v[252:255], a[12:15], a[188:191], v[252:255]\n" \
B_"v[76:79], v32, s[56:59], s95 offen offset:1024 sc1\n" \
M_"v[248:251], a[8:11], a[168:171], v[248:251]\n" \
M_"v[248:251], a[12:15], a[172:175], v[248:251]\n" \
M_"v[180:183], a[8:11], a[152:155], v[180:183]\n" \
M_"v[180:183], a[12:15], a[156:159], v[180:183]\n" \
M_"v[176:179], a[8:11], a[136:139], v[176:179]\n" \
X_"a[8:9], v34 offset:9216\n" \
X_"a[10:11], v34 offset:13824\n" \
M_"v[176:179], a[12:15], a[140:143], v[176:179]\n" \
X_"a[12:13], v34 offset:18432\n" \
X_"a[14:15], v34 offset:23040\n" \
"s_waitcnt vmcnt(18)\n" \
"s_mov_b64 exec, 1\n" \
"buffer_store_dword v39, off, s[56:59], s67 offset:8 sc1\n" \
"s_mov_b64 exec, -1\n" \
"v_readlane_b32 s67, v11, 51\n" \
M_"v[176:179], a[16:19], a[32:35], v[176:179]\n" \
M_"v[176:179], a[20:23], a[36:39], v[176:179]\n" \
M_"v[180:183], a[16:19], a[48:51], v[180:183]\n" \
M_"v[180:183], a[20:23], a[52:55], v[180:183]\n" \
"s_waitcnt vmcnt(18)\n" \
M_"v[248:251], a[16:19], a[192:195], v[248:251]\n" \
"s_waitcnt vmcnt(17)\n" \
M_"v[248:251], a[20:23], a[196:199], v[248:251]\n" \
"s_waitcnt vmcnt(15)\n" \
M_"v[252:255], a[16:19], a[208:211], v[252:255]\n" \
X_"a[16:17], v10 offset:9216\n" \
"s_mov_b64 exec, 1\n" \
"buffer_store_dword v39, off, s[56:59], s68 offset:12 sc1\n" \
"s_mov_b64 exec, -1\n" \
"s_waitcnt vmcnt(15)\n" \
M_"v[252:255], a[20:23], a[212:215], v[252:255]\n" \
"v_readlane_b32 s68, v11, 52\n" \
"s_waitcnt vmcnt(14)\n" \
M_"v[252:255], a[24:27], a[216:219], v[252:255]\n" \
X_"a[18:19], v10 offset:13824\n" \
"s_waitcnt vmcnt(13)\n" \
M_"v[252:255], a[28:31], a[220:223], v[252:255]\n" \
X_"a[20:21], v33 offset:32\n" \
X_"a[22:23], v33 offset:4640\n" \
B_"v[80:83], v20, s[56:59], s96 offen offset:0 sc1\n" \
"s_waitcnt vmcnt(12)\n" \
M_"v[248:251], a[24:27], a[200:203], v[248:251]\n" \
"s_mov_b64 exec, 1\n" \
"buffer_store_dword v39, off, s[56:59], s69 offset:16 sc1\n" \
"s_mov_b64 exec, -1\n" \
"v_readlane_b32 s69, v11, 53\n" \
".fill (L_ab + 40960 - .) / 4, 4, 0xBF800000\n" \
"L_ap10:\n" \
"s_cbranch_execz L_ap0\n" \
"s_waitcnt vmcnt(12)\n" \
M_"v[248:251], a[28:31], a[204:207], v[248:251]\n" \
M_"v[180:183], a[24:27], a[56:59], v[180:183]\n" \
B_"v[84:87], v20, s[56:59], s96 offen offset:1024 sc1\n" \
M_"v[180:183], a[28:31], a[60:63], v[180:183]\n" \
M_"v[176:179], a[24:27], a[40:43], v[176:179]\n" \
X_"a[24:25], v33 offset:9248\n" \
X_"a[26:27], v33 offset:13856\n" \
M_"v[176:179], a[28:31], a[44:47], v[176:179]\n" \
X_"a[28:29], v33 offset:18464\n" \
C_"v248, v248, v249\n" \
C_"v249, v250, v251\n" \
C_"v250, v252, v253\n" \
C_"v251, v254, v255\n" \
"buffer_store_dwordx4 v[248:251], v20, s[56:59], s72 offen offset:1024 sc1\n" \
M_"v[184:187], a[96:99], a[124:127], v[184:187]\n" \
X_"a[30:31], v33 offset:23072\n" \
C_"v176, v176, v177\n" \
C_"v177, v178, v179\n" \
C_"v178, v180, v181\n" \
C_"v179, v182, v183\n" \
M_"v[184:187], a[100:103], a[224:227], v[184:187]\n" \
"buffer_store_dwordx4 v[176:179], v20, s[56:59], s72 offen offset:0 sc1\n" \
M_"v[188:191], a[96:99], a[236:239], v[188:191]\n" \
X_"a[96:97], v33 offset:27680\n" \
X_"a[98:99], v33 offset:32288\n" \
M_"v[188:191], a[100:103], a[240:243], v[188:191]\n" \
X_"a[100:101], v33 offset:36896\n" \
X_"a[102:103], v33 offset:41504\n" \
M_"v[188:191], a[104:107], a[244:247], v[188:191]\n" \
M_"v[188:191], a[108:111], a[248:251], v[188:191]\n" \
M_"v[184:187], a[104:107], a[228:231], v[184:187]\n" \
X_"a[104:105], v33 offset:46112\n" \
X_"a[106:107], v33 offset:50720\n" \
M_"v[184:187], a[108:111], a[232:235], v[184:187]\n" \
X_"a[108:109], v33 offset:55328\n" \
"s_waitcnt lgkmcnt(11)\n" \
M_"v[184:187], a[112:115], a[160:163], v[184:187]\n" \
B_"v[88:91], v32, s[56:59], s96 offen offset:0 sc1\n" \
M_"v[184:187], a[116:119], a[164:167], v[184:187]\n" \
X_"a[110:111], v33 offset:59936\n" \
M_"v[188:191], a[112:115], a[176:179], v[188:191]\n" \
X_"a[112:113], v10 offset:32\n" \
X_"a[114:115], v10 offset:4640\n" \
M_"v[188:191], a[116:119], a[180:183], v[188:191]\n" \
X_"a[116:117], v34 offset:32\n" \
X_"a[118:119], v34 offset:4640\n" \
M_"v[188:191], a[120:123], a[184:187], v[188:191]\n" \
M_"v[188:191], a[0:3], a[188:191], v[188:191]\n" \
B_"v[92:95], v32, s[56:59], s96 offen offset:1024 sc1\n" \
M_"v[184:187], a[120:123], a[168:171], v[184:187]\n" \
X_"a[120:121], v34 offset:9248\n" \
X_"a[122:123], v34 offset:13856\n" \
M_"v[184:187], a[0:3], a[172:175], v[184:187]\n" \
X_"a[0:1], v34 offset:18464\n" \
X_"a[2:3], v34 offset:23072\n" \
M_"v[184:187], a[4:7], a[192:195], v[184:187]\n" \
M_"v[184:187], a[8:11], a[196:199], v[184:187]\n" \
M_"v[188:191], a[4:7], a[208:211], v[188:191]\n" \
X_"a[4:5], v10 offset:9248\n" \
X_"a[6:7], v10 offset:13856\n" \
M_"v[188:191], a[8:11], a[212:215], v[188:191]\n" \
X_"a[8:9], v33 offset:64\n" \
X_"a[10:11], v33 offset:4672\n" \
M_"v[188:191], a[12:15], a[216:219], v[188:191]\n" \
B_"v[96:99], v20, s[56:59], s97 offen offset:0 sc1\n" \
M_"v[188:191], a[16:19], a[220:223], v[188:191]\n" \
M_"v[184:187], a[12:15], a[200:203], v[184:187]\n" \
X_"a[12:13], v33 offset:9280\n" \
X_"a[14:15], v33 offset:13888\n" \
M_"v[184:187], a[16:19], a[204:207], v[184:187]\n" \
"buffer_load_dword v37, v40, s[56:59], s74 offen sc1\n" \
M_"v[192:195], a[20:23], a[124:127], v[192:195]\n" \
X_"a[16:17], v33 offset:18496\n" \
"s_waitcnt lgkmcnt(13)\n" \
M_"v[192:195], a[24:27], a[224:227], v[192:195]\n" \
X_"a[18:19], v33 offset:23104\n" \
M_"v[196:199], a[20:23], a[236:239], v[196:199]\n" \
X_"a[20:21], v33 offset:27712\n" \
C_"v184, v184, v185\n" \
C_"v185, v186, v187\n" \
C_"v186, v188, v189\n" \
C_"v187, v190, v191\n" \
"buffer_store_dwordx4 v[184:187], v20, s[56:59], s92 offen offset:1024 sc1\n" \
"s_waitcnt vmcnt(18)\n" \
"s_mov_b64 exec, 1\n" \
"buffer_store_dword v39, off, s[56:59], s70 offset:20 sc1\n" \
"s_mov_b64 exec, -1\n" \
"s_add_u32 s92, s92, 0x3800\n" \
M_"v[196:199], a[24:27], a[240:243], v[196:199]\n" \
X_"a[22:23], v33 offset:32320\n" \
X_"a[24:25], v33 offset:36928\n" \
M_"v[196:199], a[28:31], a[244:247], v[196:199]\n" \
X_"a[26:27], v33 offset:41536\n" \
M_"v[196:199], a[96:99], a[248:251], v[196:199]\n" \
M_"v[192:195], a[28:31], a[228:231], v[192:195]\n" \
X_"a[28:29], v33 offset:46144\n" \
X_"a[30:31], v33 offset:50752\n" \
M_"v[192:195], a[96:99], a[232:235], v[192:195]\n" \
B_"v[100:103], v20, s[56:59], s97 offen offset:1024 sc1\n" \
M_"v[192:195], a[100:103], a[160:163], v[192:195]\n" \
X_"a[96:97], v33 offset:55360\n" \
X_"a[98:99], v33 offset:59968\n" \
M_"v[192:195], a[104:107], a[164:167], v[192:195]\n" \
M_"v[196:199], a[100:103], a[176:179], v[196:199]\n" \
X_"a[100:101], v10 offset:64\n" \
X_"a[102:103], v10 offset:4672\n" \
M_"v[196:199], a[104:107], a[180:183], v[196:199]\n" \
X_"a[104:105], v34 offset:64\n" \
X_"a[106:107], v34 offset:4672\n" \
M_"v[196:199], a[108:111], a[184:187], v[196:199]\n" \
B_"v[104:107], v32, s[56:59], s97 offen offset:0 sc1\n" \
M_"v[196:199], a[112:115], a[188:191], v[196:199]\n" \
M_"v[192:195], a[108:111], a[168:171], v[192:195]\n" \
X_"a[108:109], v34 offset:9280\n" \
X_"a[110:111], v34 offset:13888\n" \
M_"v[192:195], a[112:115], a[172:175], v[192:195]\n" \
X_"a[112:113], v34 offset:18496\n" \
"s_waitcnt lgkmcnt(14)\n" \
M_"v[192:195], a[116:119], a[192:195], v[192:195]\n" \
X_"a[114:115], v34 offset:23104\n" \
M_"v[192:195], a[120:123], a[196:199], v[192:195]\n" \
B_"v[108:111], v32, s[56:59], s97 offen offset:1024 sc1\n" \
M_"v[196:199], a[116:119], a[208:211], v[196:199]\n" \
X_"a[116:117], v10 offset:9280\n" \
X_"a[118:119], v10 offset:13888\n" \
M_"v[196:199], a[120:123], a[212:215], v[196:199]\n" \
X_"a[120:121], v33 offset:96\n" \
X_"a[122:123], v33 offset:4704\n" \
"s_waitcnt vmcnt(19)\n" \
"s_mov_b64 exec, 1\n" \
"buffer_store_dword v39, off, s[56:59], s71 offset:24 sc1\n" \
"s_mov_b64 exec, -1\n" \
M_"v[196:199], a[0:3], a[216:219], v[196:199]\n" \
M_"v[196:199], a[4:7], a[220:223], v[196:199]\n" \
M_"v[192:195], a[0:3], a[200:203], v[192:195]\n" \
X_"a[0:1], v33 offset:9312\n" \
X_"a[2:3], v33 offset:13920\n" \
M_"v[192:195], a[4:7], a[204:207], v[192:195]\n" \
X_"a[4:5], v33 offset:18528\n" \
X_"a[6:7], v33 offset:23136\n" \
M_"v[200:203], a[8:11], a[124:127], v[200:203]\n" \
M_"v[200:203], a[12:15], a[224:227], v[200:203]\n" \
M_"v[204:207], a[8:11], a[236:239], v[204:207]\n" \
X_"a[8:9], v33 offset:27744\n" \
X_"a[10:11], v33 offset:32352\n" \
M_"v[204:207], a[12:15], a[240:243], v[204:207]\n" \
C_"v192, v192, v193\n" \
C_"v193, v194, v195\n" \
C_"v194, v196, v197\n" \
C_"v195, v198, v199\n" \
"buffer_store_dwordx4 v[192:195], v20, s[56:59], s66 offen offset:1024 sc1\n" \
M_"v[204:207], a[16:19], a[244:247], v[204:207]\n" \
X_"a[12:13], v33 offset:36960\n" \
"s_waitcnt lgkmcnt(11)\n" \
M_"v[204:207], a[20:23], a[248:251], v[204:207]\n" \
X_"a[14:15], v33 offset:41568\n" \
M_"v[200:203], a[16:19], a[228:231], v[200:203]\n" \
X_"a[16:17], v33 offset:46176\n" \
X_"a[18:19], v33 offset:50784\n" \
M_"v[200:203], a[20:23], a[232:235], v[200:203]\n" \
X_"a[20:21], v33 offset:55392\n" \
X_"a[22:23], v33 offset:60000\n" \
M_"v[200:203], a[24:27], a[160:163], v[200:203]\n" \
M_"v[200:203], a[28:31], a[164:167], v[200:203]\n" \
M_"v[204:207], a[24:27], a[176:179], v[204:207]\n" \
X_"a[24:25], v10 offset:96\n" \
X_"a[26:27], v10 offset:4704\n" \
M_"v[204:207], a[28:31], a[180:183], v[204:207]\n" \
X_"a[28:29], v34 offset:96\n" \
X_"a[30:31], v34 offset:4704\n" \
M_"v[204:207], a[96:99], a[184:187], v[204:207]\n" \
M_"v[204:207], a[100:103], a[188:191], v[204:207]\n" \
M_"v[200:203], a[96:99], a[168:171], v[200:203]\n" \
X_"a[96:97], v34 offset:9312\n" \
X_"a[98:99], v34 offset:13920\n" \
M_"v[200:203], a[100:103], a[172:175], v[200:203]\n" \
X_"a[100:101], v34 offset:18528\n" \
X_"a[102:103], v34 offset:23136\n" \
M_"v[200:203], a[104:107], a[192:195], v[200:203]\n" \
M_"v[200:203], a[108:111], a[196:199], v[200:203]\n" \
M_"v[204:207], a[104:107], a[208:211], v[204:207]\n" \
X_"a[104:105], v10 offset:9312\n" \
X_"a[106:107], v10 offset:13920\n" \
M_"v[204:207], a[108:111], a[212:215], v[204:207]\n" \
X_"a[108:109], v33 offset:128\n" \
X_"a[110:111], v33 offset:4736\n" \
M_"v[204:207], a[112:115], a[216:219], v[204:207]\n" \
"s_waitcnt lgkmcnt(15)\n" \
M_"v[204:207], a[116:119], a[220:223], v[204:207]\n" \
M_"v[200:203], a[112:115], a[200:203], v[200:203]\n" \
X_"a[112:113], v33 offset:9344\n" \
X_"a[114:115], v33 offset:13952\n" \
M_"v[200:203], a[116:119], a[204:207], v[200:203]\n" \
X_"a[116:117], v33 offset:18560\n" \
X_"a[118:119], v33 offset:23168\n" \
M_"v[208:211], a[120:123], a[124:127], v[208:211]\n" \
M_"v[208:211], a[0:3], a[224:227], v[208:211]\n" \
M_"v[212:215], a[120:123], a[236:239], v[212:215]\n" \
X_"a[120:121], v33 offset:27776\n" \
X_"a[122:123], v33 offset:32384\n" \
M_"v[212:215], a[0:3], a[240:243], v[212:215]\n" \
C_"v200, v200, v201\n" \
C_"v201, v202, v203\n" \
C_"v202, v204, v205\n" \
C_"v203, v206, v207\n" \
"buffer_store_dwordx4 v[200:203], v20, s[56:59], s67 offen offset:1024 sc1\n" \
M_"v[212:215], a[4:7], a[244:247], v[212:215]\n" \
X_"a[0:1], v33 offset:36992\n" \
X_"a[2:3], v33 offset:41600\n" \
M_"v[212:215], a[8:11], a[248:251], v[212:215]\n" \
M_"v[208:211], a[4:7], a[228:231], v[208:211]\n" \
X_"a[4:5], v33 offset:46208\n" \
X_"a[6:7], v33 offset:50816\n" \
M_"v[208:211], a[8:11], a[232:235], v[208:211]\n" \
X_"a[8:9], v33 offset:55424\n" \
X_"a[10:11], v33 offset:60032\n" \
M_"v[208:211], a[12:15], a[160:163], v[208:211]\n" \
"s_waitcnt lgkmcnt(13)\n" \
M_"v[208:211], a[16:19], a[164:167], v[208:211]\n" \
M_"v[212:215], a[12:15], a[176:179], v[212:215]\n" \
"s_waitcnt vmcnt(8)\n" \
"v_cmp_eq_u32_e64 s[98:99], s62, v37\n" \
"s_mov_b64 exec, 3\n" \
"buffer_store_dword v39, v41, s[56:59], s73 offen sc1\n" \
"s_mov_b64 exec, -1\n" \
"s_andn2_b32 s75, 0x7f, s98\n" \
"s_mov_b32 s20, s75\n" \
"s_bitcmp1_b32 s20, 0\n" \
"s_cselect_b32 s93, 0x40000000, s12\n" \
"s_bitcmp1_b32 s20, 1\n" \
"s_cselect_b32 s94, 0x40000000, s12\n" \
"s_bitcmp1_b32 s20, 2\n" \
"s_cselect_b32 s95, 0x40000000, s13\n" \
"s_bitcmp1_b32 s20, 3\n" \
"s_cselect_b32 s96, 0x40000000, s13\n" \
"s_bitcmp1_b32 s20, 4\n" \
"s_cselect_b32 s97, 0x40000000, s14\n" \
"s_bitcmp1_b32 s20, 5\n" \
"s_cselect_b32 s64, 0x40000000, s14\n" \
"s_bitcmp1_b32 s20, 6\n" \
"s_cselect_b32 s65, 0x40000000, s15\n" \
"s_add_u32 s66, s66, 0x3000\n" \
"s_add_u32 s67, s67, 0x2800\n" \
M_"v[212:215], a[16:19], a[180:183], v[212:215]\n" \
B_"v[116:119], v20, s[56:59], s93 offen offset:0 sc1\n" \
M_"v[212:215], a[20:23], a[184:187], v[212:215]\n" \
X_"a[12:13], v10 offset:128\n" \
X_"a[14:15], v10 offset:4736\n" \
M_"v[212:215], a[24:27], a[188:191], v[212:215]\n" \
X_"a[16:17], v34 offset:128\n" \
X_"a[18:19], v34 offset:4736\n" \
M_"v[208:211], a[20:23], a[168:171], v[208:211]\n" \
X_"a[20:21], v34 offset:9344\n" \
X_"a[22:23], v34 offset:13952\n" \
M_"v[208:211], a[24:27], a[172:175], v[208:211]\n" \
X_"a[24:25], v34 offset:18560\n" \
X_"a[26:27], v34 offset:23168\n" \
M_"v[208:211], a[28:31], a[192:195], v[208:211]\n" \
M_"v[208:211], a[96:99], a[196:199], v[208:211]\n" \
M_"v[212:215], a[28:31], a[208:211], v[212:215]\n" \
X_"a[28:29], v10 offset:9344\n" \
X_"a[30:31], v10 offset:13952\n" \
M_"v[212:215], a[96:99], a[212:215], v[212:215]\n" \
X_"a[96:97], v33 offset:160\n" \
X_"a[98:99], v33 offset:4768\n" \
M_"v[212:215], a[100:103], a[216:219], v[212:215]\n" \
M_"v[212:215], a[104:107], a[220:223], v[212:215]\n" \
M_"v[208:211], a[100:103], a[200:203], v[208:211]\n" \
X_"a[100:101], v33 offset:9376\n" \
X_"a[102:103], v33 offset:13984\n" \
M_"v[208:211], a[104:107], a[204:207], v[208:211]\n" \
X_"a[104:105], v33 offset:18592\n" \
"s_waitcnt lgkmcnt(15)\n" \
M_"v[216:219], a[108:111], a[124:127], v[216:219]\n" \
X_"a[106:107], v33 offset:23200\n" \
M_"v[216:219], a[112:115], a[224:227], v[216:219]\n" \
B_"v[124:127], v20, s[56:59], s94 offen offset:2048 sc1\n" \
M_"v[220:223], a[108:111], a[236:239], v[220:223]\n" \
X_"a[108:109], v33 offset:27808\n" \
C_"v208, v208, v209\n" \
C_"v209, v210, v211\n" \
C_"v210, v212, v213\n" \
C_"v211, v214, v215\n" \
M_"v[220:223], a[112:115], a[240:243], v[220:223]\n" \
"buffer_store_dwordx4 v[208:211], v20, s[56:59], s68 offen offset:1024 sc1\n" \
"s_add_u32 s68, s68, 0x2000\n" \
M_"v[220:223], a[116:119], a[244:247], v[220:223]\n" \
X_"a[110:111], v33 offset:32416\n" \
X_"a[112:113], v33 offset:37024\n" \
M_"v[220:223], a[120:123], a[248:251], v[220:223]\n" \
X_"a[114:115], v33 offset:41632\n" \
M_"v[216:219], a[116:119], a[228:231], v[216:219]\n" \
X_"a[116:117], v33 offset:46240\n" \
X_"a[118:119], v33 offset:50848\n" \
M_"v[216:219], a[120:123], a[232:235], v[216:219]\n" \
X_"a[120:121], v33 offset:55456\n" \
X_"a[122:123], v33 offset:60064\n" \
M_"v[216:219], a[0:3], a[160:163], v[216:219]\n" \
M_"v[216:219], a[4:7], a[164:167], v[216:219]\n" \
M_"v[220:223], a[0:3], a[176:179], v[220:223]\n" \
X_"a[0:1], v10 offset:160\n" \
X_"a[2:3], v10 offset:4768\n" \
M_"v[220:223], a[4:7], a[180:183], v[220:223]\n" \
X_"a[4:5], v34 offset:160\n" \
X_"a[6:7], v34 offset:4768\n" \
M_"v[220:223], a[8:11], a[184:187], v[220:223]\n" \
"s_waitcnt lgkmcnt(13)\n" \
M_"v[220:223], a[12:15], a[188:191], v[220:223]\n" \
"s_waitcnt vmcnt(11)\n" \
"s_mov_b64 exec, 1\n" \
"buffer_store_dword v39, off, s[56:59], s92 offset:32 sc1\n" \
"s_mov_b64 exec, -1\n" \
M_"v[216:219], a[8:11], a[168:171], v[216:219]\n" \
X_"a[8:9], v34 offset:9376\n" \
X_"a[10:11], v34 offset:13984\n" \
M_"v[216:219], a[12:15], a[172:175], v[216:219]\n" \
X_"a[12:13], v34 offset:18592\n" \
X_"a[14:15], v34 offset:23200\n" \
M_"v[216:219], a[16:19], a[192:195], v[216:219]\n" \
M_"v[216:219], a[20:23], a[196:199], v[216:219]\n" \
B_"v[132:135], v20, s[56:59], s95 offen offset:0 sc1\n" \
M_"v[220:223], a[16:19], a[208:211], v[220:223]\n" \
X_"a[16:17], v10 offset:9376\n" \
X_"a[18:19], v10 offset:13984\n" \
M_"v[220:223], a[20:23], a[212:215], v[220:223]\n" \
X_"a[20:21], v33 offset:192\n" \
X_"a[22:23], v33 offset:4800\n" \
M_"v[220:223], a[24:27], a[216:219], v[220:223]\n" \
M_"v[220:223], a[28:31], a[220:223], v[220:223]\n" \
M_"v[216:219], a[24:27], a[200:203], v[216:219]\n" \
X_"a[24:25], v33 offset:9408\n" \
X_"a[26:27], v33 offset:14016\n" \
M_"v[216:219], a[28:31], a[204:207], v[216:219]\n" \
"buffer_load_dword v37, v40, s[56:59], s74 offen sc1\n" \
M_"v[224:227], a[96:99], a[124:127], v[224:227]\n" \
X_"a[28:29], v33 offset:18624\n" \
X_"a[30:31], v33 offset:23232\n" \
M_"v[224:227], a[100:103], a[224:227], v[224:227]\n" \
M_"v[228:231], a[96:99], a[236:239], v[228:231]\n" \
X_"a[96:97], v33 offset:27840\n" \
X_"a[98:99], v33 offset:32448\n" \
C_"v216, v216, v217\n" \
C_"v217, v218, v219\n" \
C_"v218, v220, v221\n" \
C_"v219, v222, v223\n" \
"buffer_store_dwordx4 v[216:219], v20, s[56:59], s69 offen offset:1024 sc1\n" \
"s_add_u32 s69, s69, 0x1800\n" \
M_"v[228:231], a[100:103], a[240:243], v[228:231]\n" \
X_"a[100:101], v33 offset:37056\n" \
"s_waitcnt lgkmcnt(12)\n" \
M_"v[228:231], a[104:107], a[244:247], v[228:231]\n" \
X_"a[102:103], v33 offset:41664\n" \
M_"v[228:231], a[108:111], a[248:251], v[228:231]\n" \
M_"v[224:227], a[104:107], a[228:231], v[224:227]\n" \
B_"v[140:143], v20, s[56:59], s96 offen offset:2048 sc1\n" \
M_"v[224:227], a[108:111], a[232:235], v[224:227]\n" \
X_"a[104:105], v33 offset:46272\n" \
X_"a[106:107], v33 offset:50880\n" \
M_"v[224:227], a[112:115], a[160:163], v[224:227]\n" \
X_"a[108:109], v33 offset:55488\n" \
X_"a[110:111], v33 offset:60096\n" \
M_"v[224:227], a[116:119], a[164:167], v[224:227]\n" \
M_"v[228:231], a[112:115], a[176:179], v[228:231]\n" \
X_"a[112:113], v10 offset:192\n" \
X_"a[114:115], v10 offset:4800\n" \
M_"v[228:231], a[116:119], a[180:183], v[228:231]\n" \
X_"a[116:117], v34 offset:192\n" \
X_"a[118:119], v34 offset:4800\n" \
M_"v[228:231], a[120:123], a[184:187], v[228:231]\n" \
M_"v[228:231], a[0:3], a[188:191], v[228:231]\n" \
M_"v[224:227], a[120:123], a[168:171], v[224:227]\n" \
X_"a[120:121], v34 offset:9408\n" \
X_"a[122:123], v34 offset:14016\n" \
M_"v[224:227], a[0:3], a[172:175], v[224:227]\n" \
X_"a[0:1], v34 offset:18624\n" \
X_"a[2:3], v34 offset:23232\n" \
"s_waitcnt vmcnt(10)\n" \
"s_mov_b64 exec, 1\n" \
"buffer_store_dword v39, off, s[56:59], s66 offset:36 sc1\n" \
"s_mov_b64 exec, -1\n" \
M_"v[224:227], a[4:7], a[192:195], v[224:227]\n" \
M_"v[224:227], a[8:11], a[196:199], v[224:227]\n" \
M_"v[228:231], a[4:7], a[208:211], v[228:231]\n" \
X_"a[4:5], v10 offset:9408\n" \
X_"a[6:7], v10 offset:14016\n" \
M_"v[228:231], a[8:11], a[212:215], v[228:231]\n" \
X_"a[8:9], v33 offset:224\n" \
"s_waitcnt lgkmcnt(15)\n" \
M_"v[228:231], a[12:15], a[216:219], v[228:231]\n" \
B_"v[148:151], v20, s[56:59], s97 offen offset:0 sc1\n" \
M_"v[228:231], a[16:19], a[220:223], v[228:231]\n" \
X_"a[10:11], v33 offset:4832\n" \
M_"v[224:227], a[12:15], a[200:203], v[224:227]\n" \
X_"a[12:13], v33 offset:9440\n" \
X_"a[14:15], v33 offset:14048\n" \
M_"v[224:227], a[16:19], a[204:207], v[224:227]\n" \
X_"a[16:17], v33 offset:18656\n" \
X_"a[18:19], v33 offset:23264\n" \
M_"v[232:235], a[20:23], a[124:127], v[232:235]\n" \
M_"v[232:235], a[24:27], a[224:227], v[232:235]\n" \
M_"v[236:239], a[20:23], a[236:239], v[236:239]\n" \
X_"a[20:21], v33 offset:27872\n" \
X_"a[22:23], v33 offset:32480\n" \
M_"v[236:239], a[24:27], a[240:243], v[236:239]\n" \
C_"v224, v224, v225\n" \
C_"v225, v226, v227\n" \
C_"v226, v228, v229\n" \
C_"v227, v230, v231\n" \
M_"v[236:239], a[28:31], a[244:247], v[236:239]\n" \
X_"a[24:25], v33 offset:37088\n" \
X_"a[26:27], v33 offset:41696\n" \
M_"v[236:239], a[96:99], a[248:251], v[236:239]\n" \
M_"v[232:235], a[28:31], a[228:231], v[232:235]\n" \
X_"a[28:29], v33 offset:46304\n" \
X_"a[30:31], v33 offset:50912\n" \
M_"v[232:235], a[96:99], a[232:235], v[232:235]\n" \
X_"a[96:97], v33 offset:55520\n" \
X_"a[98:99], v33 offset:60128\n" \
M_"v[232:235], a[100:103], a[160:163], v[232:235]\n" \
"s_waitcnt lgkmcnt(14)\n" \
M_"v[232:235], a[104:107], a[164:167], v[232:235]\n" \
B_"v[156:159], v20, s[56:59], s64 offen offset:2048 sc1\n" \
"v_readlane_b32 s64, v11, 54\n" \
M_"v[236:239], a[100:103], a[176:179], v[236:239]\n" \
X_"a[100:101], v10 offset:224\n" \
X_"a[102:103], v10 offset:4832\n" \
M_"v[236:239], a[104:107], a[180:183], v[236:239]\n" \
X_"a[104:105], v34 offset:224\n" \
X_"a[106:107], v34 offset:4832\n" \
"buffer_store_dwordx4 v[224:227], v20, s[56:59], s64 offen offset:1024 sc1\n" \
M_"v[236:239], a[108:111], a[184:187], v[236:239]\n" \
M_"v[236:239], a[112:115], a[188:191], v[236:239]\n" \
M_"v[232:235], a[108:111], a[168:171], v[232:235]\n" \
X_"a[108:109], v34 offset:9440\n" \
X_"a[110:111], v34 offset:14048\n" \
"s_waitcnt vmcnt(13)\n" \
"s_mov_b64 exec, 1\n" \
"buffer_store_dword v39, off, s[56:59], s67 offset:40 sc1\n" \
"s_mov_b64 exec, -1\n" \
M_"v[232:235], a[112:115], a[172:175], v[232:235]\n" \
X_"a[112:113], v34 offset:18656\n" \
X_"a[114:115], v34 offset:23264\n" \
M_"v[232:235], a[116:119], a[192:195], v[232:235]\n" \
M_"v[232:235], a[120:123], a[196:199], v[232:235]\n" \
M_"v[236:239], a[116:119], a[208:211], v[236:239]\n" \
X_"a[116:117], v10 offset:9440\n" \
X_"a[118:119], v10 offset:14048\n" \
M_"v[236:239], a[120:123], a[212:215], v[236:239]\n" \
X_"a[120:121], v36 offset:0\n" \
X_"a[122:123], v36 offset:1024\n" \
M_"v[236:239], a[0:3], a[216:219], v[236:239]\n" \
M_"v[236:239], a[4:7], a[220:223], v[236:239]\n" \
M_"v[232:235], a[0:3], a[200:203], v[232:235]\n" \
X_"a[0:1], v36 offset:2048\n" \
X_"a[2:3], v36 offset:3072\n" \
M_"v[232:235], a[4:7], a[204:207], v[232:235]\n" \
X_"a[4:5], v36 offset:32\n" \
B_"v[164:167], v20, s[56:59], s65 offen offset:0 sc1\n" \
"v_readlane_b32 s65, v11, 55\n" \
"s_waitcnt lgkmcnt(11)\n" \
M_"v[240:243], a[8:11], a[124:127], v[240:243]\n" \
X_"a[6:7], v36 offset:1056\n" \
M_"v[240:243], a[12:15], a[224:227], v[240:243]\n" \
M_"v[244:247], a[8:11], a[236:239], v[244:247]\n" \
C_"v232, v232, v233\n" \
C_"v233, v234, v235\n" \
C_"v234, v236, v237\n" \
C_"v235, v238, v239\n" \
"buffer_store_dwordx4 v[232:235], v20, s[56:59], s65 offen offset:1024 sc1\n" \
M_"v[244:247], a[12:15], a[240:243], v[244:247]\n" \
X_"a[8:9], v36 offset:2080\n" \
X_"a[10:11], v36 offset:3104\n" \
M_"v[244:247], a[16:19], a[244:247], v[244:247]\n" \
M_"v[244:247], a[20:23], a[248:251], v[244:247]\n" \
M_"v[240:243], a[16:19], a[228:231], v[240:243]\n" \
M_"v[240:243], a[20:23], a[232:235], v[240:243]\n" \
M_"v[168:171], a[24:27], a[128:131], v[168:171]\n" \
M_"v[168:171], a[28:31], a[132:135], v[168:171]\n" \
M_"v[172:175], a[24:27], a[144:147], v[172:175]\n" \
"s_waitcnt vmcnt(9)\n" \
"v_cmp_eq_u32_e64 s[98:99], s62, v37\n" \
"buffer_load_dword v37, v40, s[56:59], s74 offen sc1\n" \
"s_mov_b64 exec, 1\n" \
"buffer_store_dword v39, off, s[56:59], s68 offset:44 sc1\n" \
"s_mov_b64 exec, -1\n" \
"s_or_b32 s75, 0x300, s100\n" \
"s_andn2_b32 s75, s75, s98\n" \
"s_mov_b32 s21, s75\n" \
"s_bitcmp1_b32 s21, 16\n" \
"s_cselect_b32 s70, 0x40000000, s16\n" \
"s_bitcmp1_b32 s21, 17\n" \
"s_cselect_b32 s71, 0x40000000, s16\n" \
"s_bitcmp1_b32 s21, 18\n" \
"s_cselect_b32 s72, 0x40000000, s17\n" \
"s_bitcmp1_b32 s21, 19\n" \
"s_cselect_b32 s73, 0x40000000, s17\n" \
"s_bitcmp1_b32 s21, 20\n" \
"s_cselect_b32 s92, 0x40000000, s18\n" \
"s_bitcmp1_b32 s21, 21\n" \
"s_cselect_b32 s93, 0x40000000, s18\n" \
"s_bitcmp1_b32 s21, 22\n" \
"s_cselect_b32 s94, 0x40000000, s19\n" \
"s_bitcmp1_b32 s21, 23\n" \
"s_cselect_b32 s95, 0x40000000, s19\n" \
"s_bitcmp1_b32 s21, 8\n" \
"s_cselect_b32 s96, 0x40000000, s12\n" \
"s_bitcmp1_b32 s21, 9\n" \
"s_cselect_b32 s97, 0x40000000, s12\n" \
"s_add_u32 s64, s64, 0x1000\n" \
"s_add_u32 s65, s65, 0x800\n" \
M_"v[172:175], a[28:31], a[148:151], v[172:175]\n" \
B_"v[112:115], v20, s[56:59], s70 offen offset:0 sc1\n" \
M_"v[240:243], a[24:27], a[160:163], v[240:243]\n" \
M_"v[240:243], a[28:31], a[164:167], v[240:243]\n" \
M_"v[244:247], a[24:27], a[176:179], v[244:247]\n" \
M_"v[244:247], a[28:31], a[180:183], v[244:247]\n" \
B_"v[184:187], v20, s[56:59], s71 offen offset:2048 sc1\n" \
M_"v[244:247], a[96:99], a[184:187], v[244:247]\n" \
"v_readlane_b32 s71, v11, 57\n" \
M_"v[244:247], a[100:103], a[188:191], v[244:247]\n" \
M_"v[240:243], a[96:99], a[168:171], v[240:243]\n" \
M_"v[240:243], a[100:103], a[172:175], v[240:243]\n" \
B_"v[120:123], v20, s[56:59], s72 offen offset:0 sc1\n" \
M_"v[172:175], a[96:99], a[152:155], v[172:175]\n" \
"v_readlane_b32 s72, v11, 48\n" \
M_"v[172:175], a[100:103], a[156:159], v[172:175]\n" \
M_"v[168:171], a[96:99], a[136:139], v[168:171]\n" \
M_"v[168:171], a[100:103], a[140:143], v[168:171]\n" \
B_"v[192:195], v20, s[56:59], s73 offen offset:2048 sc1\n" \
M_"v[168:171], a[104:107], a[32:35], v[168:171]\n" \
"v_readlane_b32 s73, v11, 46\n" \
"s_waitcnt lgkmcnt(0)\n" \
M_"v[168:171], a[108:111], a[36:39], v[168:171]\n" \
M_"v[172:175], a[104:107], a[48:51], v[172:175]\n" \
M_"v[172:175], a[108:111], a[52:55], v[172:175]\n" \
B_"v[128:131], v20, s[56:59], s92 offen offset:0 sc1\n" \
M_"v[240:243], a[104:107], a[192:195], v[240:243]\n" \
"v_readlane_b32 s92, v11, 47\n" \
M_"v[240:243], a[108:111], a[196:199], v[240:243]\n" \
M_"v[244:247], a[104:107], a[208:211], v[244:247]\n" \
M_"v[244:247], a[108:111], a[212:215], v[244:247]\n" \
B_"v[200:203], v20, s[56:59], s93 offen offset:2048 sc1\n" \
M_"v[244:247], a[112:115], a[216:219], v[244:247]\n" \
M_"v[244:247], a[116:119], a[220:223], v[244:247]\n" \
M_"v[240:243], a[112:115], a[200:203], v[240:243]\n" \
M_"v[240:243], a[116:119], a[204:207], v[240:243]\n" \
".fill (L_ab + 45056 - .) / 4, 4, 0xBF800000\n" \
"L_ap11:\n" \
"s_cbranch_execz L_ap0\n" \
B_"v[176:179], v20, s[56:59], s94 offen offset:0 sc1\n" \
"s_waitcnt vmcnt(17)\n" \
"s_mov_b64 exec, 1\n" \
"buffer_store_dword v39, off, s[56:59], s69 offset:48 sc1\n" \
"s_mov_b64 exec, -1\n" \
M_"v[172:175], a[112:115], a[56:59], v[172:175]\n" \
M_"v[172:175], a[116:119], a[60:63], v[172:175]\n" \
M_"v[168:171], a[112:115], a[40:43], v[168:171]\n" \
M_"v[168:171], a[116:119], a[44:47], v[168:171]\n" \
B_"v[248:251], v20, s[56:59], s95 offen offset:2048 sc1\n" \
M_"v[168:171], a[120:123], v[64:67], v[168:171]\n" \
M_"v[172:175], a[120:123], v[72:75], v[172:175]\n" \
M_"v[240:243], a[120:123], v[80:83], v[240:243]\n" \
M_"v[244:247], a[120:123], v[88:91], v[244:247]\n" \
B_"v[188:191], v20, s[56:59], s96 offen offset:1024 sc1\n" \
M_"v[168:171], a[0:3], v[68:71], v[168:171]\n" \
M_"v[172:175], a[0:3], v[76:79], v[172:175]\n" \
M_"v[240:243], a[0:3], v[84:87], v[240:243]\n" \
M_"v[244:247], a[0:3], v[92:95], v[244:247]\n" \
B_"v[196:199], v20, s[56:59], s97 offen offset:3072 sc1\n" \
M_"v[56:59], a[4:7], v[96:99], 0\n" \
M_"v[60:63], a[4:7], v[104:107], 0\n" \
M_"v[56:59], a[8:11], v[100:103], v[56:59]\n" \
M_"v[60:63], a[8:11], v[108:111], v[60:63]\n" \
"s_waitcnt vmcnt(16)\n" \
"s_mov_b64 exec, 1\n" \
"buffer_store_dword v39, off, s[56:59], s64 offset:52 sc1\n" \
"s_mov_b64 exec, -1\n" \
"s_waitcnt vmcnt(14)\n" \
"s_mov_b64 exec, 1\n" \
"buffer_store_dword v39, off, s[56:59], s65 offset:56 sc1\n" \
"s_mov_b64 exec, -1\n" \
"s_cmp_lg_u32 s20, 0\n" \
"s_cbranch_scc1 L_sna_%=\n" \
"L_rna_%=:\n" \
"s_cmp_lg_u32 s21, 0\n" \
"s_cbranch_scc1 L_snx_%=\n" \
"L_rnx_%=:\n" \
"s_waitcnt vmcnt(14)\n" \
"v_cmp_eq_u32_e64 s[98:99], s62, v37\n" \
M_"v[168:171], v[50:53], v[116:119], v[168:171]\n" \
"s_andn2_b32 s75, 0x400, s98\n" \
"s_cbranch_scc1 L_sx0_%=\n" \
"L_rx0_%=:\n" \
"s_add_u32 s66, s63, 0x1000\n" \
M_"v[172:175], v[48:51], v[116:119], v[172:175]\n" \
B_"v[204:207], v20, s[56:59], s66 offen offset:1024 sc1\n" \
"s_andn2_b32 s75, 0x800, s98\n" \
"s_cbranch_scc1 L_sx1_%=\n" \
"L_rx1_%=:\n" \
"s_add_u32 s67, s63, 0x1800\n" \
M_"v[168:171], v[50:53], v[124:127], v[168:171]\n" \
M_"v[172:175], v[48:51], v[124:127], v[172:175]\n" \
M_"v[168:171], v[50:53], v[132:135], v[168:171]\n" \
B_"v[212:215], v20, s[56:59], s67 offen offset:1024 sc1\n" \
"s_andn2_b32 s75, 0x1000, s98\n" \
"s_cbranch_scc1 L_sx2_%=\n" \
"L_rx2_%=:\n" \
"s_add_u32 s68, s63, 0x2000\n" \
M_"v[172:175], v[48:51], v[132:135], v[172:175]\n" \
M_"v[168:171], v[50:53], v[140:143], v[168:171]\n" \
M_"v[172:175], v[48:51], v[140:143], v[172:175]\n" \
B_"v[220:223], v20, s[56:59], s68 offen offset:1024 sc1\n" \
"s_andn2_b32 s75, 0x2000, s98\n" \
"s_cbranch_scc1 L_sx3_%=\n" \
"L_rx3_%=:\n" \
"s_add_u32 s69, s63, 0x2800\n" \
M_"v[168:171], v[50:53], v[148:151], v[168:171]\n" \
M_"v[172:175], v[48:51], v[148:151], v[172:175]\n" \
M_"v[168:171], v[50:53], v[156:159], v[168:171]\n" \
B_"v[228:231], v20, s[56:59], s69 offen offset:1024 sc1\n" \
"s_andn2_b32 s75, 0x4000, s98\n" \
"s_cbranch_scc1 L_sx4_%=\n" \
"L_rx4_%=:\n" \
"s_add_u32 s70, s63, 0x3000\n" \
M_"v[172:175], v[48:51], v[156:159], v[172:175]\n" \
M_"v[168:171], v[50:53], v[164:167], v[168:171]\n" \
M_"v[172:175], v[48:51], v[164:167], v[172:175]\n" \
B_"v[236:239], v20, s[56:59], s70 offen offset:1024 sc1\n" \
"s_waitcnt vmcnt(17)\n" \
M_"v[56:59], v[50:53], v[112:115], v[56:59]\n" \
M_"v[60:63], v[48:51], v[112:115], v[60:63]\n" \
"s_waitcnt vmcnt(16)\n" \
M_"v[56:59], v[50:53], v[184:187], v[56:59]\n" \
M_"v[60:63], v[48:51], v[184:187], v[60:63]\n" \
C_"v168, v168, v169\n" \
"s_waitcnt vmcnt(15)\n" \
M_"v[56:59], v[50:53], v[120:123], v[56:59]\n" \
C_"v172, v172, v173\n" \
C_"v169, v170, v171\n" \
M_"v[60:63], v[48:51], v[120:123], v[60:63]\n" \
C_"v173, v174, v175\n" \
"s_waitcnt vmcnt(14)\n" \
M_"v[56:59], v[50:53], v[192:195], v[56:59]\n" \
"buffer_store_dwordx2 v[168:169], v42, s[88:91], s73 offen sc1\n" \
M_"v[60:63], v[48:51], v[192:195], v[60:63]\n" \
"s_waitcnt vmcnt(14)\n" \
M_"v[56:59], v[50:53], v[128:131], v[56:59]\n" \
"buffer_store_dwordx2 v[172:173], v43, s[88:91], s73 offen sc1\n" \
M_"v[60:63], v[48:51], v[128:131], v[60:63]\n" \
"s_waitcnt vmcnt(14)\n" \
M_"v[56:59], v[50:53], v[200:203], v[56:59]\n" \
M_"v[60:63], v[48:51], v[200:203], v[60:63]\n" \
"s_waitcnt vmcnt(13)\n" \
M_"v[56:59], v[50:53], v[176:179], v[56:59]\n" \
M_"v[60:63], v[48:51], v[176:179], v[60:63]\n" \
"s_waitcnt vmcnt(11)\n" \
M_"v[56:59], v[50:53], v[248:251], v[56:59]\n" \
M_"v[60:63], v[48:51], v[248:251], v[60:63]\n" \
"s_waitcnt vmcnt(10)\n" \
M_"v[240:243], v[50:53], v[188:191], v[240:243]\n" \
M_"v[244:247], v[48:51], v[188:191], v[244:247]\n" \
"s_waitcnt vmcnt(9)\n" \
M_"v[240:243], v[50:53], v[196:199], v[240:243]\n" \
M_"v[244:247], v[48:51], v[196:199], v[244:247]\n" \
"s_mov_b64 exec, 1\n" \
"buffer_store_dword v46, off, s[56:59], s71 offset:0 sc1\n" \
"s_mov_b64 exec, -1\n" \
C_"v56, v56, v57\n" \
C_"v60, v60, v61\n" \
C_"v57, v58, v59\n" \
C_"v61, v62, v63\n" \
"buffer_store_dwordx2 v[56:57], v42, s[88:91], s72 offen sc1\n" \
"buffer_store_dwordx2 v[60:61], v43, s[88:91], s72 offen sc1\n" \
"s_waitcnt vmcnt(9)\n" \
M_"v[240:243], v[50:53], v[204:207], v[240:243]\n" \
M_"v[244:247], v[48:51], v[204:207], v[244:247]\n" \
"s_waitcnt vmcnt(8)\n" \
M_"v[240:243], v[50:53], v[212:215], v[240:243]\n" \
M_"v[244:247], v[48:51], v[212:215], v[244:247]\n" \
"s_waitcnt vmcnt(7)\n" \
M_"v[240:243], v[50:53], v[220:223], v[240:243]\n" \
M_"v[244:247], v[48:51], v[220:223], v[244:247]\n" \
"s_waitcnt vmcnt(6)\n" \
M_"v[240:243], v[50:53], v[228:231], v[240:243]\n" \
M_"v[244:247], v[48:51], v[228:231], v[244:247]\n" \
"s_waitcnt vmcnt(5)\n" \
M_"v[240:243], v[50:53], v[236:239], v[240:243]\n" \
M_"v[244:247], v[48:51], v[236:239], v[244:247]\n" \
"s_nop 6\n" \
C_"v240, v240, v241\n" \
C_"v244, v244, v245\n" \
C_"v241, v242, v243\n" \
C_"v245, v246, v247\n" \
"buffer_store_dwordx2 v[240:241], v42, s[88:91], s92 offen sc1\n" \
"buffer_store_dwordx2 v[244:245], v43, s[88:91], s92 offen sc1\n" \
"L_fin_%=:\n"
#define ATT_VCLOB "v21","v22","v23","v24","v25","v26","v27","v28","v29","v32","v33","v34","v35","v36","v37","v38","v39","v40","v41","v42","v43","v44","v45","v46","v47","v48","v49","v50","v51","v52","v53","v54","v55","v56","v57","v58","v59","v60","v61","v62","v63","v64","v65","v66","v67","v68","v69","v70","v71","v72","v73","v74","v75","v76","v77","v78","v79","v80","v81","v82","v83","v84","v85","v86","v87","v88","v89","v90","v91","v92","v93","v94","v95","v96","v97","v98","v99","v100","v101","v102","v103","v104","v105","v106","v107","v108","v109","v110","v111","v112","v113","v114","v115","v116","v117","v118","v119","v120","v121","v122","v123","v124","v125","v126","v127","v128","v129","v130","v131","v132","v133","v134","v135","v136","v137","v138","v139","v140","v141","v142","v143","v144","v145","v146","v147","v148","v149","v150","v151","v152","v153","v154","v155","v156","v157","v158","v159","v160","v161","v162","v163","v164","v165","v166","v167","v168","v169","v170","v171","v172","v173","v174","v175","v176","v177","v178","v179","v180","v181","v182","v183","v184","v185","v186","v187","v188","v189","v190","v191","v192","v193","v194","v195","v196","v197","v198","v199","v200","v201","v202","v203","v204","v205","v206","v207","v208","v209","v210","v211","v212","v213","v214","v215","v216","v217","v218","v219","v220","v221","v222","v223","v224","v225","v226","v227","v228","v229","v230","v231","v232","v233","v234","v235","v236","v237","v238","v239","v240","v241","v242","v243","v244","v245","v246","v247","v248","v249","v250","v251","v252","v253","v254","v255"
#define ATT_SCLOB "s63","s64","s65","s66","s67","s68","s69","s70","s71","s72","s73","s74","s75","s76","s77","s78","s79","s80","s81","s82","s83","s84","s85","s86","s87","s88","s89","s90","s91","s92","s93","s95","s96","s97","s98","s99","s100","s101"
#define ATT_WTCH 0
#define ATT_PEEL 1
#define ATT_PEEL_FOW 1
#define ATT_PEEL_PRE 0
#define ATT_PEEL_ODD(b) (0u)
#define ATT_PAGES 11
#define ATT_TAILB 968
enum { T_fo, T_fj, T_fa, T_fas, T_fb, T_fbs, T_f8, T_mk8, T_wt, T_wol, T_woh, T_wbl, T_wbh, T_outl, T_outh, T_r1l, T_r1h, T_r1n, T_r1s, T_r2n, T_r2s, T_m2b, T_m0, T_m1, T_m1b, T_mw, T_m3, T_m4, T_ml, T_w0, T_w1, T_w2, T_w3, T_A1, T_A2, T_A3, T_B1, T_B2, T_B3, T_Ao, T_Bo, T_Al, T_Bl, T_l8, T_xr, T_x8r, T_oA, T_oB, T_o8, T_x0, T_x1, T_x2, T_x3, T_x4, T_x5, T_x6, T_x8, T_ep };

__device__ __forceinline__ void out_consts(u32x4 (&vp)[3], const u16* wo, const u16* wob, const u16* out, const unsigned b, const unsigned w, const unsigned lane) {
    const unsigned sp = b & 7u, nt = (b >> 3) & 3u, j = b >> 5, head = 4u * sp + nt, OOR = 0x40000000u;
    unsigned vw[9];
#pragma unroll
    for (unsigned i = 0; i < 9u; ++i) {
        const unsigned q = 64u * i + lane, row = __umul24(q, 3641u) >> 16, c = q - __umul24(row, 18u);
        vw[i] = __umul24(row, DM * 2) + (c < 16u ? (c * 16u + sp * 32u + 32u) & 255u : c * 16u) - (i & 3u) * 1024u;
    }
    const unsigned row = 16u * w + (lane >> 2), r9 = __umul24(16u * (row >> 5) + (row & 12u) + 2u * ((row >> 4) & 1u) + ((row >> 1) & 1u), 18u) + 16u + (row & 1u);
    unsigned vwl = __umul24(r9, DM * 2) + ((lane & 2u) ? 256u : sp * 32u) + (lane & 1u) * 16u, vrd = __umul24(lane >> 2, 288u) + (lane & 3u) * 8u;
    unsigned tab = 0;
#define TB(n, v) asm("v_writelane_b32 %0, %1, %2" : "+v"(tab) : "s"((unsigned)(v)), "n"((int)T_##n))
    const unsigned FJ = (unsigned)WS_F + (F_J + 640u * j) * 4u, mt = 2u * (w & 2u) + (w & 1u), w2 = w < 2u ? w : 2u, hw = head * 147456u;
    const unsigned FR = (unsigned)WS_F + F_R * 4u + (8u * j + sp) * 1024u;
    TB(fo, FJ + (ATT_PEEL_FOW ? 16u * head + 4u * w : 4u * head)); TB(fj, FJ); TB(fa, FR + (w | 2u) * 256u); TB(fas, FR + w * 256u + nt * 4u);
    TB(fb, FR + 128u + w * 256u); TB(fbs, FR + 128u + w * 256u + nt * 4u);
    TB(ep, ssel(b + w, OOR, (unsigned)WS_F + F_EPOCH * 4u));
    const u16* pwo = wo + nt * 144u; const u16* pwb = wob + nt * 144u;
    TB(wol, PLO(pwo)); TB(woh, PHI(pwo) & 0xffffu); TB(wbl, PLO(pwb)); TB(wbh, PHI(pwb) & 0xffffu); TB(outl, PLO(out)); TB(outh, PHI(out) & 0xffffu);
    const unsigned odd = ATT_PEEL_ODD(b), wo1 = w + 3u * odd;
#define OSEL(a, b_) (ATT_PEEL_ODD(1u) ? ssel(odd, a, b_) : (b_))
#define WU(u) (((4u * sp + ((nt + 1u + ((u) >> 2)) & 3u)) * 4u + ((u) & 3u)) * 36864u)
    TB(r1l, sslt<3>(wo1, PLO(pwo), PLO(pwb))); TB(r1h, sslt<3>(wo1, PHI(pwo), PHI(pwb)) & 0xffffu);
    TB(r1n, sslt<3>(wo1, 72u * sp + 71u, 4095u) * 1152u + 288u); TB(r1s, sslt<3>(wo1, (72u * sp + 32u * w) * 1152u, ATT_PEEL_ODD(1u) ? sslt<2>(w, WU(w), sslt<3>(w, OOR, hw + 110592u)) : hw + 110592u));
    if (ATT_PEEL_ODD(1u)) { TB(r2n, (72u * sp + 143u) * 1152u + 288u); TB(r2s, sslt<3>(w, (72u * sp + 72u + 32u * w) * 1152u, OOR)); TB(m2b, sslt<2>(w, 41984u + 9216u * w, 32768u)); }
    TB(m0, 131072u + 9216u * w2); TB(w0, hw + 36864u * w2); TB(m1, sslt<3>(w, 41984u + 9216u * w, 32768u)); TB(m1b, ssel(w ^ 2u, sslt<3>(w, 41984u + 9216u * w, 32768u), 114688u - 4096u));
    TB(mw, 65536u + 9216u * w); TB(m3, sslt<3>(w, 102400u + 9216u * w, 0u)); TB(m4, OSEL(9216u + 13312u * (w & 1u), sslt<3>(w, 131072u + 9216u * w, 9216u))); TB(ml, 18432u + 1024u * w);
    const unsigned tA = (unsigned)WS_AF + (8u * j + mt) * (KS_TOT * 1024u), tB = (unsigned)WS_AF + (120u - 8u * j + mt) * (KS_TOT * 1024u);
#define HK(k) (4u * sp + ((nt + k) & 3u))
    TB(w1, WU(w + 2u * odd)); TB(w2, WU(4u + w + 2u * odd)); TB(w3, WU(8u + (w | (2u * odd))));
    TB(A1, tA + 18432u + HK(1u) * 4096u); TB(A2, tA + 18432u + HK(2u) * 4096u); TB(A3, tA + 18432u + HK(3u) * 4096u);
    TB(B1, tB + 18432u + HK(1u) * 4096u); TB(B2, tB + 18432u + HK(2u) * 4096u); TB(B3, tB + 18432u + HK(3u) * 4096u);
    const unsigned pre = ATT_PEEL_PRE ? sslt<7>(j, tA + 18432u + head * 4096u, OOR) : OOR;
    TB(Ao, OSEL(pre, tA + sp * 2048u)); TB(Bo, OSEL(pre + 2048u, tB + sp * 2048u)); TB(Al, tA + 16384u); TB(Bl, tB + 16384u);
    const unsigned nred = w ^ (sp >> 1), blk = sp & 1u;
    const unsigned xg = (unsigned)WS_X + (4u * j + nt) * 589824u + w * 16384u, oA = (128u * j + 16u * mt) * 1152u + nt * 288u, oB = (1920u - 128u * j + 16u * mt) * 1152u + nt * 288u;
    TB(l8, ssel(nred, OOR, ssel(blk, tB, tA) + 16384u)); TB(xr, xg + sp * 65536u); TB(x8r, ssel(nred, OOR, xg + 524288u + (sp & 1u) * 1024u));
    TB(oA, oA + sp * 32u); TB(oB, oB + sp * 32u); TB(o8, ssel(nred, OOR, ssel(blk, oB, oA) + 256u)); TB(x8, xg + 524288u + sp * 2048u);
    TB(f8, xg + w * 131072u + 14400u + sp * 4u); TB(mk8, ssel(nred, 0u, 0xff0000u));
#define XI(i) TB(x##i, xg + ((sp + 1u + i) & 7u) * 65536u + i * 2048u)
    XI(0); XI(1); XI(2); XI(3); XI(4); XI(5); XI(6);
#undef XI
#undef HK
#undef WU
#undef TB
    vp[0] = (u32x4){vw[0], vw[1], vw[2], vw[3]}; vp[1] = (u32x4){vw[4], vw[5], vw[6], vw[7]}; vp[2] = (u32x4){vw[8], vwl, vrd, tab};
}
__global__ __launch_bounds__(256, 1) __attribute__((aligned(4096))) void fused_kernel(const u16* __restrict__ x, const u16* __restrict__ ckv, const u16* __restrict__ wq, const u16* __restrict__ wuk,
                                                       const u16* __restrict__ wuv, const u16* __restrict__ wqkv, const u16* __restrict__ gw,
                                                       const u16* __restrict__ wo, const u16* __restrict__ wob, char* __restrict__ ws_, u16* __restrict__ out) {
    using namespace att;
    extern __shared__ __attribute__((aligned(16))) char lds[];
    u32x4 sa[5]; u32x2 s24, s28; char* ws; unsigned tdum; kaptr ka = (kaptr)__builtin_amdgcn_kernarg_segment_ptr();
    asm volatile("s_load_dwordx8 s[8:15], %[ka], 0x0\n\ts_load_dwordx8 s[16:23], %[ka], 0x20\n\ts_getpc_b64 s[4:5]\n\tv_lshlrev_b32_e32 v191, 7, %[t]\n\ts_bfe_u32 s6, %[b], %[cb]\n\t"
                 "v_lshl_add_u32 v191, s6, %[cs], v191\n\tv_min_u32_e32 v191, %[e], v191\n\tglobal_load_dword v191, v191, s[4:5]\n\ts_load_dwordx4 s[24:27], %[ka], 0x40\n\ts_load_dwordx2 s[28:29], %[ka], 0x50"
                 : "={s[8:11]}"(sa[0]), "={s[12:15]}"(sa[1]), "={s[16:19]}"(sa[2]), "={s[20:23]}"(sa[3]), "={s[24:25]}"(s24), "={s[26:27]}"(ws), "={s[28:29]}"(s28), "={v191}"(tdum), [ka] "+{s[0:1]}"(ka)
                 : [t] "v"(threadIdx.x), [b] "s"(blockIdx.x), [e] "i"(CT_END), [cb] "i"(CT_END < 0x20000 ? 0x20003 : 0x30003), [cs] "i"(CT_END < 0x20000 ? 15 : 14) : "s4", "s5", "s6", "scc");
    SBAR();
    const unsigned wid = rfl(threadIdx.x >> 6);
    unsigned tag, vl;
    const int b = blockIdx.x;
    u32x4 vp[5], srdA; u32x2 sd;
    proj_phase(ka, ws, wid, vp, vl, sa, s24, s28, tdum, srdA, sd, tag
    );
    unsigned* const flags = (unsigned*)(ws + WS_F);
    RTS(2);
#define AST(L) do {} while (0)
#define ATT_TAIL "s_endpgm\n"
    unsigned abad;
    asm volatile(ATT_ASM ATT_TAIL "L_end_%=:\n s_nop 1"
                 : "={s94}"(abad)
                 : "{v[0:3]}"(vp[0]), "{v[4:7]}"(vp[1]), "{v[8:11]}"(vp[2]), "{v[12:15]}"(vp[3]), "{v[16:19]}"(vp[4]), "{v20}"(vl), "{s[56:59]}"(srdA), "{s[60:61]}"(sd), "{s62}"(tag),
                   "{s[8:11]}"(sa[0]), "{s[12:15]}"(sa[1]), "{s[16:19]}"(sa[2]), "{s[20:23]}"(sa[3]), "{s24}"(sa[4][0]), "{s3}"(wid), "{s2}"(b), "{s[28:29]}"(s28)
                 : "memory", "m0", "vcc", "scc", ATT_VCLOB, ATT_SCLOB, ACLOB_LO, ACLOB_HI);
    {
        unsigned b2 = b, w2 = wid;
        asm volatile("s_waitcnt vmcnt(0)" : "+s"(ka), "+s"(b2), "+s"(w2), "+s"(ws) :: "memory");
        const unsigned j2 = b2 >> 5, p2 = w2 & 1u, h2 = (b2 & 7u) * 4u + ((b2 >> 3) & 3u);
#pragma unroll 1
        for (unsigned i = w2 & 2u; i < ((w2 & 2u) ? (abad ? 8u : 4u) : 2u); ++i)
            att_exact(ka[0], ka[2], ws, h2, (i < 4u ? 1920u - 128u * j2 : 128u * j2 - 128u) + 32u * i + 16u * p2, *reinterpret_cast<const float*>(lds + LDS_G + (32u * i + 16u * p2 + (lane_id() & 15u)) * 4u));
        asm volatile("s_waitcnt vmcnt(0)" ::: "memory");
        unsigned* fa = (unsigned*)(ws + WS_F) + F_R + (8u * j2 + (b2 & 7u)) * 256u + (2u + p2) * 64u + ((b2 >> 3) & 3u);
        if (w2 & 2u) st_flag(fa, tag);
        wait_flags(fa, ld_flag(fa), tag);
        const unsigned l2 = lane_id(); u32x4 c[3]; out_consts(c, ka[7], ka[8], ka[10], b2, w2, l2);
        asm volatile("s_waitcnt vmcnt(0) lgkmcnt(0)\n\ts_nop 7\n\ts_branch L_att_stub" :: "{v[0:3]}"(c[0]), "{v[4:7]}"(c[1]), "{v[8:11]}"(c[2]), "{v20}"(l2 * 16u), "{s[56:59]}"(SRDA), "{s60}"(SD(8u * j2, h2, w2)),
                     "{s61}"(SD(120u - 8u * j2, h2, w2)), "{s62}"(tag), "{s3}"(w2), "{s2}"(b2) : "memory");
    }
    RTS(5);
}

extern "C" void kernel_launch(void* const* d_in, const int* in_sizes, int n_in, void* d_out, int out_size, void* d_ws, size_t ws_size,
                              hipStream_t stream) {
    (void)hipFuncSetAttribute((const void*)fused_kernel, hipFuncAttributeMaxDynamicSharedMemorySize, att::LDS_BYTES);
    fused_kernel<<<dim3(256), 256, att::LDS_BYTES, stream>>>((const u16*)d_in[0], (const u16*)d_in[4], (const u16*)d_in[5], (const u16*)d_in[6], (const u16*)d_in[7], (const u16*)d_in[1],
                                                             (const u16*)d_in[9], (const u16*)d_in[2], (const u16*)d_in[8], (char*)d_ws, (u16*)d_out);
}
```

```cpp
#include <hip/hip_runtime.h>
#include <hip/hip_bf16.h>
#include <stdint.h>
#include <utility>

typedef short bf16x8 __attribute__((ext_vector_type(8)));
typedef short s16x4 __attribute__((ext_vector_type(4)));
typedef float f32x16 __attribute__((ext_vector_type(16)));
typedef float f32x4 __attribute__((ext_vector_type(4)));
typedef unsigned u32x4 __attribute__((ext_vector_type(4)));
typedef unsigned u32x2 __attribute__((ext_vector_type(2)));
typedef unsigned short u16;

constexpr int S = 2048, DM = 576, NH = 32, HD = 128, DC = 512, NQKV = 1728, DHA = 18;
constexpr int NBIG = NH * HD;
constexpr int KS_TOT = 146;

#define SBAR() __builtin_amdgcn_sched_barrier(0)
constexpr size_t WS_Q = 0, WS_K = WS_Q + (size_t)S * NBIG * 2, WS_V = WS_K + (size_t)S * NBIG * 2, WS_AF = WS_V + (size_t)S * NBIG * 2, WS_X = WS_AF + (size_t)128 * KS_TOT * 1024,
                 WS_P = WS_X + (size_t)64 * 16 * 9216, WS_F = WS_P + (size_t)128 * (4 * 32 * 256 + 1024) * 4;
constexpr size_t WS_L = (size_t)96 << 20;
static_assert(WS_F == 96206848, "workspace map is baked into the generated asm");
constexpr int F_EPOCH = 0, F_J = 1024, F_ST = 8192, F_KV = 40960, F_R = 65536;
constexpr unsigned TAG_STEP = 0x9E3779B1u;
__device__ __forceinline__ unsigned ld_flag(const unsigned* p) { return __hip_atomic_load(p, __ATOMIC_RELAXED, __HIP_MEMORY_SCOPE_AGENT); }
__device__ __forceinline__ void st_flag(unsigned* p, unsigned v) { __hip_atomic_store(p, v, __ATOMIC_RELAXED, __HIP_MEMORY_SCOPE_AGENT); }
__device__ __forceinline__ void wait_flags(const unsigned* p, unsigned v, unsigned tag) {
    if (__builtin_expect(__builtin_amdgcn_ballot_w64(v != tag) != 0ull, 0)) {
        unsigned spins = 0;
        do { __builtin_amdgcn_s_sleep(1); } while (__builtin_amdgcn_ballot_w64(ld_flag(p) != tag) != 0ull && ++spins < (1u << 22));
    }
    asm volatile("" ::: "memory");
}

__device__ __forceinline__ unsigned lane_id() { unsigned l; asm volatile("v_mbcnt_lo_u32_b32 %0, -1, 0\n\tv_mbcnt_hi_u32_b32 %0, -1, %0" : "=v"(l)); return l; }
#define RTS(slot) do {} while (0)
#define CT_END ((PROJ_PAGES + ATT_PAGES) * 4096 + (ATT_TAILB & ~127) - 128)
#define ACLOB_LO "a0","a1","a2","a3","a4","a5","a6","a7","a8","a9","a10","a11","a12","a13","a14","a15","a16","a17","a18","a19","a20","a21","a22","a23","a24","a25","a26","a27","a28","a29","a30","a31","a32","a33","a34","a35","a36","a37","a38","a39","a40","a41","a42","a43","a44","a45","a46","a47","a48","a49","a50","a51","a52","a53","a54","a55","a56","a57","a58","a59","a60","a61","a62","a63","a64","a65","a66","a67","a68","a69","a70","a71","a72","a73","a74","a75","a76","a77","a78","a79","a80","a81","a82","a83","a84","a85","a86","a87","a88","a89","a90","a91","a92","a93","a94","a95","a96","a97","a98","a99","a100","a101","a102","a103","a104","a105","a106","a107","a108","a109","a110","a111","a112","a113","a114","a115","a116","a117","a118","a119","a120","a121","a122","a123","a124","a125","a126","a127"
#define ACLOB_HI "a128","a129","a130","a131","a132","a133","a134","a135","a136","a137","a138","a139","a140","a141","a142","a143","a144","a145","a146","a147","a148","a149","a150","a151","a152","a153","a154","a155","a156","a157","a158","a159","a160","a161","a162","a163","a164","a165","a166","a167","a168","a169","a170","a171","a172","a173","a174","a175","a176","a177","a178","a179","a180","a181","a182","a183","a184","a185","a186","a187","a188","a189","a190","a191","a192","a193","a194","a195","a196","a197","a198","a199","a200","a201","a202","a203","a204","a205","a206","a207","a208","a209","a210","a211","a212","a213","a214","a215","a216","a217","a218","a219","a220","a221","a222","a223","a224","a225","a226","a227","a228","a229","a230","a231","a232","a233","a234","a235","a236","a237","a238","a239","a240","a241","a242","a243","a244","a245","a246","a247","a248","a249","a250","a251","a252","a253","a254","a255"
#define M_ "v_mfma_f32_16x16x32_bf16 "
#define X_ "ds_read_b64_tr_b16 "
#define B_ "buffer_load_dwordx4 "
#define C_ "v_cvt_pk_bf16_f32 "
#define R_ "ds_read_b128 "
#define E_ "v_exp_f32_e32 "
#define PLO(p) ((unsigned)(size_t)(p))
#define PHI(p) ((unsigned)((size_t)(p) >> 32))
__device__ __forceinline__ unsigned vsel(bool c, unsigned a, unsigned b) { asm("" : "+v"(a), "+v"(b)); return c ? a : b; }
__device__ __forceinline__ unsigned ssel(unsigned c, unsigned a, unsigned b) { unsigned r; asm("s_cmp_lg_u32 %1, 0\n\ts_cselect_b32 %0, %2, %3" : "=s"(r) : "s"(c), "s"(a), "s"(b) : "scc"); return r; }
template <int N> __device__ __forceinline__ unsigned sslt(unsigned c, unsigned a, unsigned b) { unsigned r; asm("s_cmpk_lt_u32 %1, %4\n\ts_cselect_b32 %0, %2, %3" : "=s"(r) : "s"(c), "s"(a), "s"(b), "n"(N) : "scc"); return r; }
namespace att {
constexpr int LDS_BYTES = 163840;
constexpr unsigned LDS_G = 159744u, LDS_L = 161792u;
}
#define SD(J8, H, W) ((unsigned)WS_AF + (((J8) + 2u * ((W) & 2u) + ((W) & 1u)) * KS_TOT + 18u + 4u * (H)) * 1024u)
#define SRDA (u32x4){PLO(ws), PHI(ws) & 0xffffu, (unsigned)WS_L, 0x00020000u}
#define PROJ_ASM \
".p2align 3\n" \
"L_pb:\n" \
"s_waitcnt lgkmcnt(0)\n" \
"s_mov_b32 s64, s10\n" \
"s_and_b32 s65, s11, 0xffff\n" \
"s_mov_b32 s66, 0x200000\n" \
"s_mov_b32 s67, 0x20000\n" \
"s_mov_b32 s68, s14\n" \
"s_and_b32 s69, s15, 0xffff\n" \
"s_mov_b32 s70, 0x400000\n" \
"s_mov_b32 s71, 0x20000\n" \
"s_mov_b32 s72, s16\n" \
"s_and_b32 s73, s17, 0xffff\n" \
"s_mov_b32 s74, 0x400000\n" \
"s_mov_b32 s75, 0x20000\n" \
"s_add_u32 m0, s30, 0x0\n" \
"s_movk_i32 s84, 0x0\n" \
B_"v22, s[64:67], s84 offen offset:0 lds\n" \
"s_movk_i32 s84, 0x1c00\n" \
B_"v23, s[64:67], s84 offen offset:1024 lds\n" \
"s_movk_i32 s84, 0x3800\n" \
B_"v22, s[64:67], s84 offen offset:2048 lds\n" \
"s_movk_i32 s84, 0x5400\n" \
B_"v23, s[64:67], s84 offen offset:3072 lds\n" \
"s_add_u32 m0, s31, 0x0\n" \
"s_movk_i32 s84, 0x0\n" \
B_"v26, s[68:71], s84 offen offset:0 lds\n" \
"s_movk_i32 s84, 0x7c00\n" \
B_"v26, s[68:71], s84 offen offset:1024 lds\n" \
"s_addk_i32 s84, 0x7c00\n" \
B_"v27, s[68:71], s84 offen offset:2048 lds\n" \
"s_addk_i32 s84, 0x7c00\n" \
B_"v27, s[68:71], s84 offen offset:3072 lds\n" \
"s_movk_i32 s7, 0x300\n" \
"s_lshr_b32 s4, s2, 1\n" \
"s_and_b32 s4, s4, 60\n" \
"s_or_b32 s4, s4, s3\n" \
"v_lshrrev_b32_e32 v212, 4, v20\n" \
"v_min_u32_e32 v212, 59, v212\n" \
"s_cmpk_lt_u32 s2, 0x80\n" \
"s_cselect_b32 s6, s14, s16\n" \
"v_subrev_u32_e32 v214, 48, v212\n" \
"v_mov_b32_e32 v212, s10\n" \
"v_mov_b32_e32 v215, s6\n" \
"s_cmpk_lt_u32 s2, 0x80\n" \
"s_cselect_b32 s5, s15, s17\n" \
"v_cmp_gt_u32_e32 vcc, s7, v20\n" \
"v_mov_b32_e64 v213, s11\n" \
"v_bfe_u32 v216, v20, 4, 3\n" \
"v_cndmask_b32_e32 v212, v212, v215, vcc\n" \
"v_mov_b32_e32 v215, s5\n" \
"s_lshl_b32 s5, s3, 4\n" \
"v_cndmask_b32_e32 v213, v213, v215, vcc\n" \
"v_lshlrev_b32_e64 v215, 5, v20\n" \
"v_and_b32_e32 v215, 0x7000, v215\n" \
"v_lshl_add_u32 v215, s4, 6, v215\n" \
"s_lshl_b32 s4, s2, 3\n" \
"s_and_b32 s4, s4, 56\n" \
"v_or3_b32 v215, v215, s4, v216\n" \
"s_lshl_b32 s4, s2, 6\n" \
"s_or_b32 s6, s5, s4\n" \
"v_cmp_gt_u32_e64 s[4:5], 6, v214\n" \
"v_add_u32_e32 v215, 0x2000, v215\n" \
"s_nop 0\n" \
"v_cndmask_b32_e64 v216, 4, 2, s[4:5]\n" \
"v_add3_u32 v214, s6, v214, v216\n" \
"s_lshl_b32 s4, s3, 9\n" \
"v_cndmask_b32_e32 v214, v214, v215, vcc\n" \
"v_lshlrev_b32_e32 v214, 7, v214\n" \
"v_mov_b32_e64 v215, 0\n" \
"v_lshl_add_u64 v[184:185], v[212:213], 0, v[214:215]\n" \
"s_or_b32 s32, s4, 0x25000\n" \
"s_add_u32 m0, s30, 0x1000\n" \
"s_mov_b32 s84, 0x8000\n" \
B_"v22, s[64:67], s84 offen offset:0 lds\n" \
"s_and_b32 s33, s3, 1\n" \
"v_lshrrev_b32_e32 v212, 4, v20\n" \
"v_lshrrev_b32_e64 v214, 8, v20\n" \
"v_bfe_u32 v212, v212, 1, 3\n" \
"v_lshlrev_b32_e32 v215, 2, v20\n" \
"s_addk_i32 s84, 0x1c00\n" \
B_"v23, s[64:67], s84 offen offset:1024 lds\n" \
"v_lshrrev_b32_e64 v216, 1, v20\n" \
"v_bfe_u32 v213, v20, 4, 4\n" \
"s_lshl_b32 s4, s3, 5\n" \
"v_xor_b32_e32 v212, v212, v214\n" \
"v_lshlrev_b32_e32 v214, 11, v214\n" \
"s_addk_i32 s84, 0x1c00\n" \
B_"v22, s[64:67], s84 offen offset:2048 lds\n" \
"v_and_b32_e32 v215, 0x300, v215\n" \
"v_and_b32_e64 v217, 24, v216\n" \
"v_and_or_b32 v213, s4, 64, v213\n" \
"v_or3_b32 v214, v215, v214, v217\n" \
"s_lshl_b32 s4, s33, 14\n" \
"s_addk_i32 s84, 0x1c00\n" \
B_"v23, s[64:67], s84 offen offset:3072 lds\n" \
"v_and_b32_e32 v215, 0xe0, v216\n" \
"v_lshlrev_b32_e32 v213, 7, v213\n" \
"v_lshlrev_b32_e32 v212, 4, v212\n" \
"v_or3_b32 v214, v214, s4, v215\n" \
"v_or_b32_e64 v28, v212, v213\n" \
"s_add_u32 m0, s31, 0x4000\n" \
"s_movk_i32 s84, 0x0\n" \
B_"v26, s[72:75], s84 offen offset:0 lds\n" \
"v_add_u32_e32 v32, 0x10000, v214\n" \
"v_bitop3_b32 v29, v212, 64, v213 bitop3:0x36\n" \
"v_xor_b32_e32 v33, 0x20, v32\n" \
"v_xor_b32_e64 v34, 0x40, v32\n" \
"v_xor_b32_e32 v35, 0x60, v32\n" \
"s_movk_i32 s84, 0x7c00\n" \
B_"v26, s[72:75], s84 offen offset:1024 lds\n" \
"v_xor_b32_e32 v36, 0x80, v32\n" \
"v_xor_b32_e32 v37, 0xa0, v32\n" \
"v_xor_b32_e32 v38, 0xc0, v32\n" \
"v_xor_b32_e32 v39, 0xe0, v32\n" \
"s_mov_b32 s80, s26\n" \
"s_addk_i32 s84, 0x7c00\n" \
B_"v27, s[72:75], s84 offen offset:2048 lds\n" \
"s_and_b32 s81, s27, 0xffff\n" \
"s_mov_b32 s82, 0x10000000\n" \
"s_mov_b32 s83, 0x20000\n" \
"s_mov_b32 s76, s18\n" \
"s_and_b32 s77, s19, 0xffff\n" \
"s_addk_i32 s84, 0x7c00\n" \
B_"v27, s[72:75], s84 offen offset:3072 lds\n" \
"s_mov_b32 s78, 0x1e6000\n" \
"s_mov_b32 s79, 0x20000\n" \
"s_branch L_pgo_%=\n" \
"L_pret:\n" \
"s_setpc_b64 s[88:89]\n" \
"L_pgo_%=:\n" \
"s_mul_i32 s88, s33, 0xa000\n" \
"s_lshl_b32 s89, s3, 11\n" \
"s_and_b32 s89, s89, 0x1000\n" \
"s_add_u32 s88, s88, s89\n" \
"s_getpc_b64 s[86:87]\n" \
"L_hpa_%=:\n" \
"s_add_u32 s86, s86, L_pp1-L_hpa_%=\n" \
"s_addc_u32 s87, s87, 0\n" \
"s_add_u32 s86, s86, s88\n" \
"s_addc_u32 s87, s87, 0\n" \
"s_add_u32 m0, s30, 0x8000\n" \
"s_movk_i32 s84, 0x80\n" \
B_"v22, s[64:67], s84 offen offset:0 lds\n" \
"s_lshr_b32 s4, s2, 1\n" \
"s_and_b32 s4, s4, 0x7c\n" \
"v_bfe_u32 v212, v20, 4, 5\n" \
"s_movk_i32 s84, 0x1c80\n" \
B_"v23, s[64:67], s84 offen offset:1024 lds\n" \
"s_or_b32 s4, s4, s3\n" \
"v_min_u32_e32 v212, 26, v212\n" \
"v_mad_u64_u32 v[212:213], s[4:5], s4, 27, v[212:213]\n" \
"s_movk_i32 s84, 0x3880\n" \
B_"v22, s[64:67], s84 offen offset:2048 lds\n" \
"v_mul_u32_u24_e32 v213, 0xaaab, v212\n" \
"s_lshr_b32 s7, s2, 5\n" \
"v_lshrrev_b32_e32 v213, 18, v213\n" \
"s_movk_i32 s84, 0x5480\n" \
B_"v23, s[64:67], s84 offen offset:3072 lds\n" \
"s_mov_b32 s4, 0x1fffffa\n" \
"s_and_b32 s6, s2, 7\n" \
"v_mad_u64_u32 v[214:215], s[4:5], v213, s4, v[212:213]\n" \
"s_mov_b64 exec, 0\n" \
"s_swappc_b64 s[88:89], s[86:87]\n" \
"s_mov_b64 exec, -1\n" \
"s_add_u32 s86, s86, 0x2000\n" \
"s_addc_u32 s87, s87, 0\n" \
"s_add_u32 m0, s31, 0x8000\n" \
"s_mov_b32 s84, 0x80000\n" \
B_"v26, s[68:71], s84 offen offset:0 lds\n" \
"s_mulk_i32 s7, 0x90\n" \
"v_mul_u32_u24_e32 v213, 27, v213\n" \
"v_lshrrev_b32_e32 v214, 1, v214\n" \
"s_addk_i32 s84, 0x7c00\n" \
B_"v26, s[68:71], s84 offen offset:1024 lds\n" \
"v_and_b32_e32 v212, 1, v212\n" \
"s_add_u32 s4, s7, s20\n" \
"v_mul_u32_u24_e32 v214, 9, v214\n" \
"v_add3_u32 v212, v212, s6, v213\n" \
"s_addc_u32 s5, 0, s21\n" \
"s_addk_i32 s84, 0x7c00\n" \
B_"v27, s[68:71], s84 offen offset:2048 lds\n" \
"v_add_lshl_u32 v212, v212, v214, 7\n" \
"v_mov_b32_e64 v213, 0\n" \
"v_lshl_add_u64 v[186:187], s[18:19], 0, v[212:213]\n" \
"s_addk_i32 s84, 0x7c00\n" \
B_"v27, s[68:71], s84 offen offset:3072 lds\n" \
"s_bitcmp1_b32 s3, 0\n" \
"s_cselect_b32 s6, 0x8c, 0\n" \
"s_add_u32 s4, s4, s6\n" \
"v_mov_b32_e32 v212, v187\n" \
"s_addc_u32 s5, s5, 0\n" \
"s_add_u32 m0, s30, 0x9000\n" \
"s_mov_b32 s84, 0x8080\n" \
B_"v22, s[64:67], s84 offen offset:0 lds\n" \
"v_writelane_b32 v186, s4, 31\n" \
"v_writelane_b32 v212, s5, 31\n" \
"v_mov_b32_e32 v187, v212\n" \
"s_addk_i32 s84, 0x1c00\n" \
B_"v23, s[64:67], s84 offen offset:1024 lds\n" \
"s_lshl_b32 s35, s2, 10\n" \
"s_lshr_b32 s37, s2, 1\n" \
"s_mul_i32 s37, s37, 0x24800\n" \
"s_mov_b64 exec, 0\n" \
"s_swappc_b64 s[88:89], s[86:87]\n" \
"s_mov_b64 exec, -1\n" \
"s_add_u32 s86, s86, 0x2000\n" \
"s_addc_u32 s87, s87, 0\n" \
"s_addk_i32 s84, 0x1c00\n" \
B_"v22, s[64:67], s84 offen offset:2048 lds\n" \
"s_and_b32 s35, s35, 0x400\n" \
"s_lshl_b32 s36, s3, 8\n" \
"s_or_b32 s35, s37, s35\n" \
"s_addk_i32 s84, 0x1c00\n" \
B_"v23, s[64:67], s84 offen offset:3072 lds\n" \
"s_and_b32 s4, s2, 7\n" \
"s_lshr_b32 s6, s2, 5\n" \
"s_lshl_b32 s34, s2, 5\n" \
"s_add_u32 m0, s31, 0xc000\n" \
"s_mov_b32 s84, 0x80000\n" \
B_"v26, s[72:75], s84 offen offset:0 lds\n" \
"s_add_i32 s35, s35, s36\n" \
"s_lshl_b32 s7, s4, 10\n" \
"s_and_b32 s34, s34, 0x300\n" \
"s_addk_i32 s84, 0x7c00\n" \
B_"v26, s[72:75], s84 offen offset:1024 lds\n" \
"s_add_i32 s35, s35, 0x3004000\n" \
"s_lshl_b32 s37, s6, 13\n" \
"s_or_b32 s34, s7, s34\n" \
"s_addk_i32 s84, 0x7c00\n" \
B_"v27, s[72:75], s84 offen offset:2048 lds\n" \
"s_or_b32 s7, s37, s7\n" \
"v_mov_b32_e32 v213, s35\n" \
"s_or_b32 s7, s7, s36\n" \
"s_addk_i32 s84, 0x7c00\n" \
B_"v27, s[72:75], s84 offen offset:3072 lds\n" \
"s_lshr_b32 s5, s2, 3\n" \
"s_or_b32 s7, s7, 0x5c00000\n" \
"v_writelane_b32 v213, s7, 2\n" \
"s_mul_i32 s5, s5, 0x90000\n" \
"v_writelane_b32 v213, s7, 3\n" \
"s_lshl_b32 s7, s3, 14\n" \
"s_lshl_b32 s4, s4, 16\n" \
"s_add_i32 s5, s5, s7\n" \
"s_or_b32 s34, s34, 0x5be8000\n" \
"v_writelane_b32 v213, s34, 4\n" \
"s_add_i32 s4, s5, s4\n" \
"v_writelane_b32 v213, s34, 5\n" \
"s_add_i32 s4, s4, 0x4243800\n" \
"v_writelane_b32 v213, s4, 6\n" \
"s_mul_i32 s4, s6, 0xa00\n" \
"v_lshlrev_b32_e32 v212, 3, v20\n" \
"s_lshl_b32 s5, s3, 7\n" \
"v_and_b32_e32 v212, 0x80, v212\n" \
"s_add_i32 s4, s4, s5\n" \
"s_add_i32 s4, s4, 0x5bc0f80\n" \
"v_writelane_b32 v213, s4, 7\n" \
"v_add_u32_e32 v49, v213, v212\n" \
"s_mov_b32 m0, s32\n" \
"s_mov_b32 exec_lo, 0xffffff\n" \
"s_mov_b32 exec_hi, 0xfff0000\n" \
"global_load_lds_dword v[184:185], off\n" \
"s_mov_b64 exec, -1\n" \
"s_mov_b32 m0, s32\n" \
"s_mov_b64 exec, 0xff\n" \
"buffer_load_dword v49, s[80:83], 0 offen lds\n" \
"s_mov_b64 exec, -1\n" \
"s_mov_b32 s84, 0x5bc0000\n" \
"buffer_load_dword v180, off, s[80:83], s84 sc1\n" \
"s_cmp_lg_u32 s33, 0\n" \
"s_cbranch_scc1 L_w1_%=\n" \
"L_w0_%=:\n" \
"s_waitcnt vmcnt(19)\n" \
"s_barrier\n" \
X_"a[160:161], v32 offset:0\n" \
X_"a[162:163], v32 offset:1024\n" \
R_"a[128:131], v28 offset:16384\n" \
X_"a[224:225], v32 offset:8192\n" \
X_"a[226:227], v32 offset:9216\n" \
R_"a[192:195], v29 offset:16384\n" \
R_"a[132:135], v28 offset:18432\n" \
R_"a[196:199], v29 offset:18432\n" \
X_"a[164:165], v33 offset:0\n" \
X_"a[166:167], v33 offset:1024\n" \
X_"a[228:229], v33 offset:8192\n" \
X_"a[230:231], v33 offset:9216\n" \
R_"a[136:139], v28 offset:20480\n" \
R_"a[200:203], v29 offset:20480\n" \
"s_waitcnt lgkmcnt(11)\n" \
M_"a[0:3], a[160:163], a[128:131], 0\n" \
X_"a[168:169], v34 offset:0\n" \
"s_lshr_b32 s4, s2, 1\n" \
"s_waitcnt lgkmcnt(9)\n" \
M_"a[0:3], a[224:227], a[192:195], a[0:3]\n" \
X_"a[170:171], v34 offset:1024\n" \
"s_and_b32 s4, s4, 0x7c\n" \
"s_waitcnt lgkmcnt(9)\n" \
M_"a[16:19], a[160:163], a[132:135], 0\n" \
X_"a[232:233], v34 offset:8192\n" \
"s_nop 5\n" \
"s_waitcnt lgkmcnt(9)\n" \
M_"a[16:19], a[224:227], a[196:199], a[16:19]\n" \
X_"a[234:235], v34 offset:9216\n" \
"v_mov_b32_e32 v213, s13\n" \
"s_waitcnt lgkmcnt(8)\n" \
M_"a[20:23], a[164:167], a[132:135], 0\n" \
R_"a[140:143], v28 offset:22528\n" \
"s_or_b32 s4, s4, s3\n" \
"s_waitcnt lgkmcnt(7)\n" \
M_"a[20:23], a[228:231], a[196:199], a[20:23]\n" \
R_"a[204:207], v29 offset:22528\n" \
"s_nop 5\n" \
M_"a[4:7], a[164:167], a[128:131], 0\n" \
X_"a[172:173], v35 offset:0\n" \
"v_lshrrev_b32_e64 v212, 4, v20\n" \
M_"a[4:7], a[228:231], a[192:195], a[4:7]\n" \
X_"a[174:175], v35 offset:1024\n" \
"v_mad_u64_u32 v[214:215], s[4:5], s4, 36, v[212:213]\n" \
"s_waitcnt lgkmcnt(9)\n" \
M_"a[32:35], a[160:163], a[136:139], 0\n" \
X_"a[236:237], v35 offset:8192\n" \
"s_nop 5\n" \
"s_waitcnt lgkmcnt(9)\n" \
M_"a[32:35], a[224:227], a[200:203], a[32:35]\n" \
X_"a[238:239], v35 offset:9216\n" \
"v_mov_b32_e32 v217, s9\n" \
M_"a[36:39], a[164:167], a[136:139], 0\n" \
R_"a[144:147], v28 offset:0\n" \
"s_movk_i32 s4, 0x240\n" \
M_"a[36:39], a[228:231], a[200:203], a[36:39]\n" \
R_"a[208:211], v29 offset:0\n" \
"s_nop 5\n" \
"s_waitcnt lgkmcnt(10)\n" \
M_"a[40:43], a[168:171], a[136:139], 0\n" \
R_"a[148:151], v28 offset:2048\n" \
"v_cmp_gt_u32_e32 vcc, s4, v20\n" \
"s_waitcnt lgkmcnt(9)\n" \
M_"a[40:43], a[232:235], a[200:203], a[40:43]\n" \
R_"a[212:215], v29 offset:2048\n" \
"s_lshl_b32 s4, s2, 3\n" \
M_"a[24:27], a[168:171], a[132:135], 0\n" \
R_"a[152:155], v28 offset:4096\n" \
"s_nop 5\n" \
M_"a[24:27], a[232:235], a[196:199], a[24:27]\n" \
R_"a[216:219], v29 offset:4096\n" \
"s_and_b32 s4, s4, 56\n" \
M_"a[8:11], a[168:171], a[128:131], 0\n" \
R_"a[156:159], v28 offset:6144\n" \
"v_cndmask_b32_e64 v217, v217, v213, vcc\n" \
M_"a[8:11], a[232:235], a[192:195], a[8:11]\n" \
R_"a[220:223], v29 offset:6144\n" \
"s_mov_b32 m0, s32\n" \
"s_nop 5\n" \
"s_waitcnt lgkmcnt(13)\n" \
M_"a[48:51], a[160:163], a[140:143], 0\n" \
"s_mov_b32 exec_lo, 0xff000000\n" \
"s_mov_b32 exec_hi, 0x0\n" \
"global_load_lds_dword v[184:185], off\n" \
"s_mov_b64 exec, -1\n" \
"s_waitcnt lgkmcnt(12)\n" \
M_"a[48:51], a[224:227], a[204:207], a[48:51]\n" \
X_"a[176:177], v36 offset:0\n" \
"v_lshlrev_b32_e64 v213, 3, v214\n" \
M_"a[52:55], a[164:167], a[140:143], 0\n" \
"s_nop 5\n" \
X_"a[178:179], v36 offset:1024\n" \
M_"a[52:55], a[228:231], a[204:207], a[52:55]\n" \
"s_mov_b32 m0, s32\n" \
X_"a[240:241], v36 offset:8192\n" \
M_"a[56:59], a[168:171], a[140:143], 0\n" \
"s_mov_b32 exec_lo, 0xffff\n" \
"s_mov_b32 exec_hi, 0x0\n" \
"global_load_lds_dword v[186:187], off\n" \
"s_mov_b64 exec, -1\n" \
M_"a[56:59], a[232:235], a[204:207], a[56:59]\n" \
"s_nop 5\n" \
X_"a[242:243], v36 offset:9216\n" \
"s_waitcnt lgkmcnt(14)\n" \
M_"a[60:63], a[172:175], a[140:143], 0\n" \
X_"a[180:181], v37 offset:0\n" \
"v_and_b32_e32 v213, 0x1ffc0, v213\n" \
"s_waitcnt lgkmcnt(13)\n" \
M_"a[60:63], a[236:239], a[204:207], a[60:63]\n" \
X_"a[182:183], v37 offset:1024\n" \
"v_and_b32_e32 v214, 7, v214\n" \
M_"a[44:47], a[172:175], a[136:139], 0\n" \
"s_nop 5\n" \
X_"a[244:245], v37 offset:8192\n" \
M_"a[44:47], a[236:239], a[200:203], a[44:47]\n" \
X_"a[246:247], v37 offset:9216\n" \
"v_or3_b32 v213, v214, s4, v213\n" \
M_"a[28:31], a[172:175], a[132:135], 0\n" \
X_"a[184:185], v38 offset:0\n" \
"s_lshl_b32 s4, s2, 2\n" \
M_"a[28:31], a[236:239], a[196:199], a[28:31]\n" \
"s_nop 5\n" \
X_"a[186:187], v38 offset:1024\n" \
M_"a[12:15], a[172:175], a[128:131], 0\n" \
"s_mov_b64 exec, 0\n" \
"s_swappc_b64 s[88:89], s[86:87]\n" \
"s_mov_b64 exec, -1\n" \
"s_add_u32 s86, s86, 0x2000\n" \
"s_addc_u32 s87, s87, 0\n" \
M_"a[12:15], a[236:239], a[192:195], a[12:15]\n" \
"s_or_b32 s4, s4, s3\n" \
"s_mul_i32 s4, s4, 18\n" \
"s_waitcnt lgkmcnt(15)\n" \
M_"a[76:79], a[172:175], a[144:147], 0\n" \
"s_nop 5\n" \
X_"a[248:249], v38 offset:8192\n" \
M_"a[76:79], a[236:239], a[208:211], a[76:79]\n" \
"v_min_u32_e32 v212, 53, v212\n" \
"s_sub_i32 s4, s4, 36\n" \
M_"a[72:75], a[168:171], a[144:147], 0\n" \
"v_add_u32_e32 v212, s4, v212\n" \
"v_mov_b32_e64 v215, s12\n" \
M_"a[72:75], a[232:235], a[208:211], a[72:75]\n" \
"s_nop 5\n" \
X_"a[250:251], v38 offset:9216\n" \
M_"a[68:71], a[164:167], a[144:147], 0\n" \
"v_mov_b32_e32 v216, s8\n" \
"v_cndmask_b32_e64 v212, v212, v213, vcc\n" \
M_"a[68:71], a[228:231], a[208:211], a[68:71]\n" \
"v_cndmask_b32_e32 v216, v216, v215, vcc\n" \
"v_lshlrev_b32_e32 v212, 7, v212\n" \
M_"a[64:67], a[160:163], a[144:147], 0\n" \
"s_nop 5\n" \
"v_mov_b32_e32 v213, 0\n" \
M_"a[64:67], a[224:227], a[208:211], a[64:67]\n" \
X_"a[188:189], v39 offset:0\n" \
"v_lshl_add_u64 v[188:189], v[216:217], 0, v[212:213]\n" \
M_"a[80:83], a[160:163], a[148:151], 0\n" \
"s_movk_i32 s5, 0xd80\n" \
"s_lshr_b32 s6, s2, 5\n" \
"s_waitcnt lgkmcnt(15)\n" \
M_"a[80:83], a[224:227], a[212:215], a[80:83]\n" \
"s_nop 5\n" \
"v_lshrrev_b32_e64 v212, 4, v20\n" \
M_"a[84:87], a[164:167], a[148:151], 0\n" \
X_"a[190:191], v39 offset:1024\n" \
"v_lshl_or_b32 v215, s3, 6, v212\n" \
M_"a[84:87], a[228:231], a[212:215], a[84:87]\n" \
"v_mul_u32_u24_e32 v216, 0x2aab, v215\n" \
"v_lshrrev_b32_e64 v216, 16, v216\n" \
M_"a[88:91], a[168:171], a[148:151], 0\n" \
"s_nop 5\n" \
"v_mad_i32_i24 v217, v216, -6, v215\n" \
M_"a[88:91], a[232:235], a[212:215], a[88:91]\n" \
X_"a[252:253], v39 offset:8192\n" \
"v_add_u32_e32 v218, 0xfffffe50, v215\n" \
M_"a[92:95], a[172:175], a[148:151], 0\n" \
"s_mul_i32 s35, s6, 0x48\n" \
"v_lshrrev_b32_e32 v219, 1, v217\n" \
M_"a[92:95], a[236:239], a[212:215], a[92:95]\n" \
"s_nop 5\n" \
"v_add_u32_e32 v218, s35, v216\n" \
M_"a[108:111], a[172:175], a[152:155], 0\n" \
X_"a[254:255], v39 offset:9216\n" \
"v_mul_u32_u24_e32 v219, 0x480, v219\n" \
M_"a[108:111], a[236:239], a[216:219], a[108:111]\n" \
"v_and_b32_e64 v214, 16, v20\n" \
"v_mad_u32_u24 v219, v218, s5, v219\n" \
M_"a[104:107], a[168:171], a[152:155], 0\n" \
"s_mov_b64 exec, 0\n" \
"s_swappc_b64 s[88:89], s[86:87]\n" \
"s_mov_b64 exec, -1\n" \
"s_andn2_b32 s85, 2, s3\n" \
"s_lshl_b32 s85, s85, 12\n" \
"s_add_u32 s86, s86, s85\n" \
"s_addc_u32 s87, s87, 0\n" \
"s_nop 5\n" \
M_"a[104:107], a[232:235], a[216:219], a[104:107]\n" \
"v_or_b32_e32 v192, v219, v214\n" \
"v_mov_b32_e32 v220, 2.0\n" \
M_"a[100:103], a[164:167], a[152:155], 0\n" \
"v_mov_b32_e32 v222, 7\n" \
"v_lshlrev_b32_e32 v220, 7, v218\n" \
M_"a[100:103], a[228:231], a[216:219], a[100:103]\n" \
"s_nop 5\n" \
"v_lshrrev_b32_e32 v218, 1, v218\n" \
M_"a[96:99], a[160:163], a[152:155], 0\n" \
"v_and_b32_e32 v218, 4, v218\n" \
"v_and_b32_e32 v216, 2, v216\n" \
M_"a[96:99], a[224:227], a[216:219], a[96:99]\n" \
"s_mul_i32 s7, s6, 0x2400\n" \
"s_sub_i32 s34, 0x22000, s7\n" \
"s_waitcnt lgkmcnt(15)\n" \
M_"a[112:115], a[160:163], a[156:159], 0\n" \
"s_nop 5\n" \
"v_bitop3_b32 v216, v217, v218, v216 bitop3:0x1e\n" \
M_"a[112:115], a[224:227], a[220:223], a[112:115]\n" \
"v_or_b32_e32 v213, 0x24400, v20\n" \
"v_lshlrev_b32_e64 v216, 4, v216\n" \
M_"a[116:119], a[164:167], a[156:159], 0\n" \
"v_add3_u32 v196, v220, s34, v216\n" \
"v_mov_b32_e64 v216, v213\n" \
M_"a[116:119], a[228:231], a[220:223], a[116:119]\n" \
"s_nop 5\n" \
"s_movk_i32 s36, 0x1b0\n" \
M_"a[120:123], a[168:171], a[156:159], 0\n" \
"v_or_b32_e32 v216, 0x100, v215\n" \
"v_mul_u32_u24_e32 v217, 0x2aab, v216\n" \
M_"a[120:123], a[232:235], a[220:223], a[120:123]\n" \
"v_add_u32_e32 v215, 0xffffff50, v215\n" \
"v_lshrrev_b32_e64 v217, 16, v217\n" \
M_"a[124:127], a[172:175], a[156:159], 0\n" \
"s_nop 5\n" \
"v_mov_b32_e32 v220, v215\n" \
"s_waitcnt vmcnt(5) lgkmcnt(0)\n" \
M_"a[124:127], a[236:239], a[220:223], a[124:127]\n" \
"s_barrier\n" \
M_"v[52:55], a[176:179], a[128:131], 0\n" \
"s_add_u32 m0, s30, 0x0\n" \
"s_movk_i32 s84, 0x100\n" \
M_"v[52:55], a[240:243], a[192:195], v[52:55]\n" \
B_"v22, s[64:67], s84 offen offset:0 lds\n" \
M_"v[56:59], a[180:183], a[128:131], 0\n" \
"s_nop 5\n" \
X_"a[160:161], v32 offset:32768\n" \
M_"v[56:59], a[244:247], a[192:195], v[56:59]\n" \
"s_movk_i32 s84, 0x1d00\n" \
M_"v[60:63], a[184:187], a[128:131], 0\n" \
B_"v23, s[64:67], s84 offen offset:1024 lds\n" \
M_"v[60:63], a[248:251], a[192:195], v[60:63]\n" \
"s_nop 5\n" \
X_"a[162:163], v32 offset:33792\n" \
M_"v[64:67], a[188:191], a[128:131], 0\n" \
X_"a[224:225], v32 offset:40960\n" \
"v_mad_i32_i24 v218, v217, -6, v216\n" \
M_"v[64:67], a[252:255], a[192:195], v[64:67]\n" \
R_"a[128:131], v28 offset:49152\n" \
"v_cmp_gt_u32_e32 vcc, s36, v216\n" \
M_"v[80:83], a[188:191], a[132:135], 0\n" \
R_"a[192:195], v29 offset:49152\n" \
"s_nop 5\n" \
M_"v[80:83], a[252:255], a[196:199], v[80:83]\n" \
"s_movk_i32 s84, 0x3900\n" \
M_"v[76:79], a[184:187], a[132:135], 0\n" \
B_"v22, s[64:67], s84 offen offset:2048 lds\n" \
M_"v[76:79], a[248:251], a[196:199], v[76:79]\n" \
"s_movk_i32 s84, 0x5500\n" \
"s_nop 5\n" \
M_"v[72:75], a[180:183], a[132:135], 0\n" \
B_"v23, s[64:67], s84 offen offset:3072 lds\n" \
M_"v[72:75], a[244:247], a[196:199], v[72:75]\n" \
"s_add_u32 m0, s31, 0x0\n" \
"s_mov_b32 s84, 0x100000\n" \
M_"v[68:71], a[176:179], a[132:135], 0\n" \
B_"v26, s[68:71], s84 offen offset:0 lds\n" \
"s_nop 5\n" \
M_"v[68:71], a[240:243], a[196:199], v[68:71]\n" \
R_"a[132:135], v28 offset:51200\n" \
"v_mov_b32_e64 v221, 7\n" \
M_"v[84:87], a[176:179], a[136:139], 0\n" \
R_"a[196:199], v29 offset:51200\n" \
"v_lshrrev_b32_e64 v222, 1, v218\n" \
M_"v[84:87], a[240:243], a[200:203], v[84:87]\n" \
"s_addk_i32 s84, 0x7c00\n" \
"s_nop 5\n" \
M_"v[88:91], a[180:183], a[136:139], 0\n" \
B_"v26, s[68:71], s84 offen offset:1024 lds\n" \
M_"v[88:91], a[244:247], a[200:203], v[88:91]\n" \
X_"a[226:227], v32 offset:41984\n" \
"v_cndmask_b32_e64 v217, v220, v217, vcc\n" \
M_"v[92:95], a[184:187], a[136:139], 0\n" \
"s_addk_i32 s84, 0x7c00\n" \
"s_nop 5\n" \
M_"v[92:95], a[248:251], a[200:203], v[92:95]\n" \
B_"v27, s[68:71], s84 offen offset:2048 lds\n" \
M_"v[96:99], a[188:191], a[136:139], 0\n" \
X_"a[164:165], v33 offset:32768\n" \
"s_movk_i32 s4, 0x48\n" \
M_"v[96:99], a[252:255], a[200:203], v[96:99]\n" \
R_"a[136:139], v28 offset:53248\n" \
"s_nop 5\n" \
M_"v[112:115], a[188:191], a[140:143], 0\n" \
R_"a[200:203], v29 offset:53248\n" \
"v_add_u32_e64 v220, s35, v217\n" \
M_"v[112:115], a[252:255], a[204:207], v[112:115]\n" \
"s_addk_i32 s84, 0x7c00\n" \
M_"v[108:111], a[184:187], a[140:143], 0\n" \
B_"v27, s[68:71], s84 offen offset:3072 lds\n" \
"s_mov_b64 exec, 0\n" \
"s_swappc_b64 s[88:89], s[86:87]\n" \
"s_mov_b64 exec, -1\n" \
"s_nop 5\n" \
M_"v[108:111], a[248:251], a[204:207], v[108:111]\n" \
"s_add_u32 m0, s30, 0x1000\n" \
"s_mov_b32 s84, 0x8100\n" \
M_"v[104:107], a[180:183], a[140:143], 0\n" \
B_"v22, s[64:67], s84 offen offset:0 lds\n" \
M_"v[104:107], a[244:247], a[204:207], v[104:107]\n" \
"s_nop 5\n" \
X_"a[166:167], v33 offset:33792\n" \
M_"v[100:103], a[176:179], a[140:143], 0\n" \
X_"a[228:229], v33 offset:40960\n" \
"v_mul_u32_u24_e32 v222, 0x480, v222\n" \
M_"v[100:103], a[240:243], a[204:207], v[100:103]\n" \
R_"a[140:143], v28 offset:55296\n" \
"v_mad_u32_u24 v222, v220, s5, v222\n" \
M_"v[116:119], a[176:179], a[144:147], 0\n" \
R_"a[204:207], v29 offset:55296\n" \
"s_nop 5\n" \
M_"v[116:119], a[240:243], a[208:211], v[116:119]\n" \
"s_addk_i32 s84, 0x1c00\n" \
M_"v[120:123], a[180:183], a[144:147], 0\n" \
B_"v23, s[64:67], s84 offen offset:1024 lds\n" \
M_"v[120:123], a[244:247], a[208:211], v[120:123]\n" \
".fill (L_pb + 3808 - .) / 4, 4, 0xBF800000\n" \
"L_pp1:\n" \
"s_cbranch_execz L_pret\n" \
"s_addk_i32 s84, 0x1c00\n" \
"s_nop 5\n" \
M_"v[124:127], a[184:187], a[144:147], 0\n" \
B_"v22, s[64:67], s84 offen offset:2048 lds\n" \
M_"v[124:127], a[248:251], a[208:211], v[124:127]\n" \
"s_addk_i32 s84, 0x1c00\n" \
M_"v[128:131], a[188:191], a[144:147], 0\n" \
B_"v23, s[64:67], s84 offen offset:3072 lds\n" \
"s_nop 5\n" \
M_"v[128:131], a[252:255], a[208:211], v[128:131]\n" \
R_"a[144:147], v28 offset:32768\n" \
"v_cmp_gt_u32_e64 s[4:5], s4, v215\n" \
M_"v[144:147], a[188:191], a[148:151], 0\n" \
R_"a[208:211], v29 offset:32768\n" \
"v_cndmask_b32_e32 v215, v221, v218, vcc\n" \
M_"v[144:147], a[252:255], a[212:215], v[144:147]\n" \
"s_add_u32 m0, s31, 0x4000\n" \
"s_mov_b32 s84, 0x100000\n" \
"s_nop 5\n" \
M_"v[140:143], a[184:187], a[148:151], 0\n" \
B_"v26, s[72:75], s84 offen offset:0 lds\n" \
M_"v[140:143], a[248:251], a[212:215], v[140:143]\n" \
X_"a[230:231], v33 offset:41984\n" \
"v_lshrrev_b32_e32 v218, 1, v220\n" \
M_"v[136:139], a[180:183], a[148:151], 0\n" \
"s_addk_i32 s84, 0x7c00\n" \
"s_nop 5\n" \
M_"v[136:139], a[244:247], a[212:215], v[136:139]\n" \
B_"v26, s[72:75], s84 offen offset:1024 lds\n" \
M_"v[132:135], a[176:179], a[148:151], 0\n" \
X_"a[168:169], v34 offset:32768\n" \
"v_or_b32_e64 v214, v222, v214\n" \
M_"v[132:135], a[240:243], a[212:215], v[132:135]\n" \
R_"a[148:151], v28 offset:34816\n" \
"s_nop 5\n" \
M_"v[148:151], a[176:179], a[152:155], 0\n" \
R_"a[212:215], v29 offset:34816\n" \
"v_mov_b32_e32 v222, 2.0\n" \
M_"v[148:151], a[240:243], a[216:219], v[148:151]\n" \
X_"a[170:171], v34 offset:33792\n" \
"v_and_b32_e64 v218, 4, v218\n" \
M_"v[152:155], a[180:183], a[152:155], 0\n" \
"s_nop 5\n" \
X_"a[232:233], v34 offset:40960\n" \
M_"v[152:155], a[244:247], a[216:219], v[152:155]\n" \
X_"a[234:235], v34 offset:41984\n" \
"v_and_b32_e32 v217, 2, v217\n" \
M_"v[156:159], a[184:187], a[152:155], 0\n" \
X_"a[172:173], v35 offset:32768\n" \
"v_bitop3_b32 v215, v215, v218, v217 bitop3:0x1e\n" \
M_"v[156:159], a[248:251], a[216:219], v[156:159]\n" \
"s_nop 5\n" \
X_"a[174:175], v35 offset:33792\n" \
M_"v[160:163], a[188:191], a[152:155], 0\n" \
X_"a[236:237], v35 offset:40960\n" \
"v_lshlrev_b32_e32 v223, 1, v220\n" \
M_"v[160:163], a[252:255], a[216:219], v[160:163]\n" \
R_"a[152:155], v28 offset:36864\n" \
"v_mov_b32_e64 v224, 2.0\n" \
M_"v[176:179], a[188:191], a[156:159], 0\n" \
R_"a[216:219], v29 offset:36864\n" \
"s_nop 5\n" \
M_"v[176:179], a[252:255], a[220:223], v[176:179]\n" \
X_"a[238:239], v35 offset:41984\n" \
"v_cndmask_b32_e32 v193, v222, v214, vcc\n" \
M_"v[172:175], a[184:187], a[156:159], 0\n" \
"v_lshlrev_b32_e32 v214, 7, v220\n" \
"v_lshlrev_b32_e32 v215, 4, v215\n" \
M_"v[172:175], a[248:251], a[220:223], v[172:175]\n" \
"s_nop 5\n" \
"s_mul_i32 s39, s3, 18\n" \
M_"v[168:171], a[180:183], a[156:159], 0\n" \
"v_add3_u32 v214, v214, s34, v215\n" \
"v_cndmask_b32_e64 v195, v224, v223, s[4:5]\n" \
M_"v[168:171], a[244:247], a[220:223], v[168:171]\n" \
"s_movk_i32 s4, 0x1f8\n" \
"v_mov_b32_e32 v215, v213\n" \
M_"v[164:167], a[176:179], a[156:159], 0\n" \
"s_nop 5\n" \
"s_add_i32 s35, s35, s39\n" \
M_"v[164:167], a[240:243], a[220:223], v[164:167]\n" \
R_"a[156:159], v28 offset:38912\n" \
R_"a[220:223], v29 offset:38912\n" \
"s_waitcnt lgkmcnt(15)\n" \
M_"a[0:3], a[160:163], a[128:131], a[0:3]\n" \
"s_addk_i32 s84, 0x7c00\n" \
M_"a[0:3], a[224:227], a[192:195], a[0:3]\n" \
B_"v27, s[72:75], s84 offen offset:2048 lds\n" \
M_"a[4:7], a[164:167], a[128:131], a[4:7]\n" \
X_"a[176:177], v36 offset:32768\n" \
"v_cmp_gt_u32_e32 vcc, s4, v216\n" \
"s_waitcnt lgkmcnt(15)\n" \
M_"a[4:7], a[228:231], a[192:195], a[4:7]\n" \
"s_addk_i32 s84, 0x7c00\n" \
"s_waitcnt lgkmcnt(11)\n" \
M_"a[8:11], a[168:171], a[128:131], a[8:11]\n" \
B_"v27, s[72:75], s84 offen offset:3072 lds\n" \
"s_waitcnt lgkmcnt(9)\n" \
M_"a[8:11], a[232:235], a[192:195], a[8:11]\n" \
X_"a[178:179], v36 offset:33792\n" \
"v_add_u32_e32 v212, s35, v212\n" \
"s_waitcnt lgkmcnt(8)\n" \
M_"a[12:15], a[172:175], a[128:131], a[12:15]\n" \
X_"a[240:241], v36 offset:40960\n" \
"v_mov_b32_e32 v219, 2.0\n" \
"s_waitcnt lgkmcnt(5)\n" \
M_"a[12:15], a[236:239], a[192:195], a[12:15]\n" \
"v_cndmask_b32_e32 v197, v215, v214, vcc\n" \
"s_movk_i32 s35, 0x120\n" \
M_"a[28:31], a[172:175], a[132:135], a[28:31]\n" \
"v_mul_u32_u24_e32 v214, 0xd80, v212\n" \
"v_cmp_gt_u32_e32 vcc, s35, v20\n" \
M_"a[28:31], a[236:239], a[196:199], a[28:31]\n" \
X_"a[242:243], v36 offset:41984\n" \
"v_lshrrev_b32_e64 v215, 1, v212\n" \
M_"a[24:27], a[168:171], a[132:135], a[24:27]\n" \
"s_and_b32 s4, s2, 7\n" \
"v_cndmask_b32_e32 v194, v219, v214, vcc\n" \
M_"a[24:27], a[232:235], a[196:199], a[24:27]\n" \
"v_lshlrev_b32_e32 v214, 7, v212\n" \
"v_lshrrev_b32_e32 v212, 2, v212\n" \
M_"a[20:23], a[164:167], a[132:135], a[20:23]\n" \
"v_and_b32_e64 v212, 2, v212\n" \
"v_and_or_b32 v212, v215, 1, v212\n" \
M_"a[20:23], a[228:231], a[196:199], a[20:23]\n" \
X_"a[180:181], v37 offset:32768\n" \
"s_lshl_b32 s5, s4, 2\n" \
M_"a[16:19], a[160:163], a[132:135], a[16:19]\n" \
"s_bfe_u32 s38, s2, 0x20003\n" \
"v_lshlrev_b32_e32 v212, 5, v212\n" \
M_"a[16:19], a[224:227], a[196:199], a[16:19]\n" \
"s_movk_i32 s35, 0x60\n" \
"s_or_b32 s5, s5, s38\n" \
M_"a[32:35], a[160:163], a[136:139], a[32:35]\n" \
"s_mov_b32 m0, s32\n" \
X_"a[182:183], v37 offset:33792\n" \
M_"a[32:35], a[224:227], a[200:203], a[32:35]\n" \
"s_mov_b32 exec_lo, 0x0\n" \
"s_mov_b32 exec_hi, 0xff\n" \
"global_load_lds_dword v[184:185], off\n" \
"s_mov_b64 exec, -1\n" \
M_"a[36:39], a[164:167], a[136:139], a[36:39]\n" \
"v_bitop3_b32 v212, v212, s35, v214 bitop3:0x36\n" \
"s_lshr_b32 s36, s2, 3\n" \
M_"a[36:39], a[228:231], a[200:203], a[36:39]\n" \
X_"a[244:245], v37 offset:40960\n" \
"v_add_u32_e64 v212, s34, v212\n" \
M_"a[40:43], a[168:171], a[136:139], a[40:43]\n" \
"s_mul_i32 s34, s5, 0x12000\n" \
"s_or_b32 s40, s34, 0x6000000\n" \
M_"a[40:43], a[232:235], a[200:203], a[40:43]\n" \
"s_mov_b32 m0, s32\n" \
"s_mul_i32 s34, s36, 0x90000\n" \
M_"a[44:47], a[172:175], a[136:139], a[44:47]\n" \
"s_mov_b32 exec_lo, 0xffff0000\n" \
"s_mov_b32 exec_hi, 0x0\n" \
"global_load_lds_dword v[186:187], off\n" \
"s_mov_b64 exec, -1\n" \
M_"a[44:47], a[236:239], a[200:203], a[44:47]\n" \
X_"a[246:247], v37 offset:41984\n" \
"s_lshl_b32 s36, s3, 14\n" \
M_"a[60:63], a[172:175], a[140:143], a[60:63]\n" \
"s_add_i32 s34, s34, s36\n" \
"s_lshl_b32 s38, s5, 8\n" \
M_"a[60:63], a[236:239], a[204:207], a[60:63]\n" \
"s_add_i32 s41, s34, 0x4240000\n" \
"s_mul_i32 s34, s5, 36\n" \
M_"a[56:59], a[168:171], a[140:143], a[56:59]\n" \
X_"a[184:185], v38 offset:32768\n" \
"s_add_i32 s5, s7, s40\n" \
M_"a[56:59], a[232:235], a[204:207], a[56:59]\n" \
"s_addk_i32 s5, 0x2000\n" \
"s_mov_b32 s37, 2.0\n" \
M_"a[52:55], a[164:167], a[140:143], a[52:55]\n" \
"s_or_b32 s38, s38, 0x5be8000\n" \
M_"a[52:55], a[228:231], a[204:207], a[52:55]\n" \
X_"a[186:187], v38 offset:33792\n" \
M_"a[48:51], a[160:163], a[140:143], a[48:51]\n" \
"s_cmp_lg_u32 s3, 0\n" \
"s_cselect_b32 s36, s37, s5\n" \
M_"a[48:51], a[224:227], a[204:207], a[48:51]\n" \
"s_lshl_b32 s5, s6, 2\n" \
"s_or_b32 s5, s38, s5\n" \
M_"a[64:67], a[160:163], a[144:147], a[64:67]\n" \
X_"a[248:249], v38 offset:40960\n" \
"s_lshl_b32 s35, s3, 10\n" \
M_"a[64:67], a[224:227], a[208:211], a[64:67]\n" \
"s_cmpk_lt_u32 s3, 3\n" \
"s_cselect_b32 s37, s37, s5\n" \
M_"a[68:71], a[164:167], a[144:147], a[68:71]\n" \
"s_lshl_b32 s5, s4, 16\n" \
"s_lshl_b32 s4, s4, 11\n" \
M_"a[68:71], a[228:231], a[208:211], a[68:71]\n" \
X_"a[250:251], v38 offset:41984\n" \
"v_cndmask_b32_e32 v198, v213, v212, vcc\n" \
M_"a[72:75], a[168:171], a[144:147], a[72:75]\n" \
"v_or_b32_e32 v212, s35, v20\n" \
"s_or_b32 s39, s40, s35\n" \
M_"a[72:75], a[232:235], a[208:211], a[72:75]\n" \
"s_or_b32 s55, s41, s4\n" \
"v_or_b32_e32 v199, 0x22000, v212\n" \
M_"a[76:79], a[172:175], a[144:147], a[76:79]\n" \
"s_add_i32 s35, s39, s7\n" \
"s_add_i32 s54, s41, s5\n" \
M_"a[76:79], a[236:239], a[208:211], a[76:79]\n" \
X_"a[188:189], v39 offset:32768\n" \
"s_add_i32 s55, s55, 0x80000\n" \
M_"a[92:95], a[172:175], a[148:151], a[92:95]\n" \
"v_mov_b32_e32 v215, 0xffff\n" \
"s_and_b32 s4, s3, 1\n" \
M_"a[92:95], a[236:239], a[212:215], a[92:95]\n" \
"s_lshl_b32 s5, s4, 4\n" \
"v_lshrrev_b32_e32 v212, 4, v20\n" \
M_"a[88:91], a[168:171], a[148:151], a[88:91]\n" \
X_"a[190:191], v39 offset:33792\n" \
"v_lshrrev_b32_e64 v218, 6, v20\n" \
M_"a[88:91], a[232:235], a[212:215], a[88:91]\n" \
"v_and_b32_e64 v219, 12, v218\n" \
"v_and_or_b32 v214, v212, 15, s5\n" \
M_"a[84:87], a[164:167], a[148:151], a[84:87]\n" \
"v_cmp_gt_u32_e64 vcc, v219, v214\n" \
"v_mov_b32_e32 v220, 0xffff0000\n" \
M_"a[84:87], a[228:231], a[212:215], a[84:87]\n" \
X_"a[252:253], v39 offset:40960\n" \
"s_lshl_b32 s5, s2, 2\n" \
M_"a[80:83], a[160:163], a[148:151], a[80:83]\n" \
"v_cndmask_b32_e64 v217, v215, 0, vcc\n" \
"v_cmp_lt_u32_e32 vcc, v219, v214\n" \
M_"a[80:83], a[224:227], a[212:215], a[80:83]\n" \
"v_mov_b32_e32 v213, s23\n" \
"s_or_b32 s5, s5, s3\n" \
M_"a[96:99], a[160:163], a[152:155], a[96:99]\n" \
X_"a[254:255], v39 offset:41984\n" \
"v_cndmask_b32_e64 v221, 0, v220, vcc\n" \
M_"a[96:99], a[224:227], a[216:219], a[96:99]\n" \
"v_or_b32_e32 v12, v217, v221\n" \
"v_or_b32_e32 v217, 2, v219\n" \
M_"a[100:103], a[164:167], a[152:155], a[100:103]\n" \
"v_cmp_gt_u32_e32 vcc, v217, v214\n" \
"v_mov_b32_e32 v216, s25\n" \
M_"a[100:103], a[228:231], a[216:219], a[100:103]\n" \
"s_movk_i32 s40, 0xf400\n" \
"v_cndmask_b32_e64 v221, v215, 0, vcc\n" \
M_"a[104:107], a[168:171], a[152:155], a[104:107]\n" \
"v_cmp_lt_u32_e32 vcc, v217, v214\n" \
"s_lshl_b32 s4, s4, 6\n" \
M_"a[104:107], a[232:235], a[216:219], a[104:107]\n" \
"v_cndmask_b32_e32 v217, 0, v220, vcc\n" \
"v_or_b32_e64 v13, v221, v217\n" \
M_"a[108:111], a[172:175], a[152:155], a[108:111]\n" \
"v_or_b32_e32 v217, 16, v219\n" \
"v_cmp_gt_u32_e32 vcc, v217, v214\n" \
M_"a[108:111], a[236:239], a[216:219], a[108:111]\n" \
"s_nop 0\n" \
"v_cndmask_b32_e64 v221, v215, 0, vcc\n" \
"v_cmp_lt_u32_e32 vcc, v217, v214\n" \
"s_waitcnt lgkmcnt(15)\n" \
M_"a[124:127], a[172:175], a[156:159], a[124:127]\n" \
"v_cndmask_b32_e32 v217, 0, v220, vcc\n" \
"v_or_b32_e64 v14, v221, v217\n" \
M_"a[124:127], a[236:239], a[220:223], a[124:127]\n" \
"v_or_b32_e32 v217, 18, v219\n" \
"v_cmp_gt_u32_e32 vcc, v217, v214\n" \
M_"a[120:123], a[168:171], a[156:159], a[120:123]\n" \
"s_nop 0\n" \
"v_cndmask_b32_e64 v215, v215, 0, vcc\n" \
"v_cmp_lt_u32_e32 vcc, v217, v214\n" \
M_"a[120:123], a[232:235], a[220:223], a[120:123]\n" \
"s_nop 0\n" \
"v_cndmask_b32_e32 v214, 0, v220, vcc\n" \
"v_or_b32_e64 v15, v215, v214\n" \
M_"a[116:119], a[164:167], a[156:159], a[116:119]\n" \
"v_mad_u64_u32 v[214:215], s[6:7], s5, 42, v[212:213]\n" \
"s_movk_i32 s5, 0x1440\n" \
M_"a[116:119], a[228:231], a[220:223], a[116:119]\n" \
"v_cmp_gt_u32_e32 vcc, s5, v214\n" \
"v_min_u32_e32 v217, 0xa43f, v214\n" \
M_"a[112:115], a[160:163], a[156:159], a[112:115]\n" \
"v_mov_b32_e32 v214, s24\n" \
"v_cndmask_b32_e32 v215, v216, v213, vcc\n" \
M_"a[112:115], a[224:227], a[220:223], a[112:115]\n" \
"s_waitcnt vmcnt(20)\n" \
"v_readfirstlane_b32 s62, v180\n" \
"s_nop 3\n" \
"s_add_u32 s62, s62, 0x9E3779B1\n" \
"s_waitcnt vmcnt(2) lgkmcnt(0)\n" \
"s_barrier\n" \
M_"v[52:55], a[176:179], a[128:131], v[52:55]\n" \
"s_add_u32 m0, s30, 0x8000\n" \
"s_movk_i32 s84, 0x180\n" \
M_"v[52:55], a[240:243], a[192:195], v[52:55]\n" \
B_"v22, s[64:67], s84 offen offset:0 lds\n" \
M_"v[56:59], a[180:183], a[128:131], v[56:59]\n" \
X_"a[160:161], v32 offset:0\n" \
"v_mov_b32_e64 v213, s22\n" \
M_"v[56:59], a[244:247], a[192:195], v[56:59]\n" \
"s_movk_i32 s84, 0x1d80\n" \
M_"v[60:63], a[184:187], a[128:131], v[60:63]\n" \
B_"v23, s[64:67], s84 offen offset:1024 lds\n" \
M_"v[60:63], a[248:251], a[192:195], v[60:63]\n" \
X_"a[162:163], v32 offset:1024\n" \
"s_movk_i32 s6, 0xffee\n" \
M_"v[64:67], a[188:191], a[128:131], v[64:67]\n" \
X_"a[224:225], v32 offset:8192\n" \
"v_cndmask_b32_e64 v214, v214, v213, vcc\n" \
M_"v[64:67], a[252:255], a[192:195], v[64:67]\n" \
R_"a[128:131], v28 offset:16384\n" \
"v_add_u32_e32 v213, 0xffffebc0, v217\n" \
M_"v[80:83], a[188:191], a[132:135], v[80:83]\n" \
R_"a[192:195], v29 offset:16384\n" \
"s_lshl_b32 s5, s2, 5\n" \
M_"v[80:83], a[252:255], a[196:199], v[80:83]\n" \
"s_movk_i32 s84, 0x3980\n" \
M_"v[76:79], a[184:187], a[132:135], v[76:79]\n" \
B_"v22, s[64:67], s84 offen offset:2048 lds\n" \
M_"v[76:79], a[248:251], a[196:199], v[76:79]\n" \
"s_movk_i32 s84, 0x5580\n" \
M_"v[72:75], a[180:183], a[132:135], v[72:75]\n" \
B_"v23, s[64:67], s84 offen offset:3072 lds\n" \
M_"v[72:75], a[244:247], a[196:199], v[72:75]\n" \
"s_add_u32 m0, s31, 0x8000\n" \
"s_mov_b32 s84, 0x180000\n" \
M_"v[68:71], a[176:179], a[132:135], v[68:71]\n" \
B_"v26, s[68:71], s84 offen offset:0 lds\n" \
M_"v[68:71], a[240:243], a[196:199], v[68:71]\n" \
R_"a[132:135], v28 offset:18432\n" \
"v_cndmask_b32_e32 v213, v213, v217, vcc\n" \
M_"v[84:87], a[176:179], a[136:139], v[84:87]\n" \
R_"a[196:199], v29 offset:18432\n" \
"v_lshlrev_b32_e64 v216, 7, v213\n" \
M_"v[84:87], a[240:243], a[200:203], v[84:87]\n" \
"s_addk_i32 s84, 0x7c00\n" \
M_"v[88:91], a[180:183], a[136:139], v[88:91]\n" \
B_"v26, s[68:71], s84 offen offset:1024 lds\n" \
M_"v[88:91], a[244:247], a[200:203], v[88:91]\n" \
X_"a[226:227], v32 offset:9216\n" \
"v_mul_u32_u24_e32 v213, 0xe39, v212\n" \
M_"v[92:95], a[184:187], a[136:139], v[92:95]\n" \
"s_addk_i32 s84, 0x7c00\n" \
M_"v[92:95], a[248:251], a[200:203], v[92:95]\n" \
B_"v27, s[68:71], s84 offen offset:2048 lds\n" \
M_"v[96:99], a[188:191], a[136:139], v[96:99]\n" \
X_"a[164:165], v33 offset:0\n" \
"v_mov_b32_e64 v217, 0\n" \
M_"v[96:99], a[252:255], a[200:203], v[96:99]\n" \
R_"a[136:139], v28 offset:20480\n" \
"v_lshrrev_b32_e64 v213, 16, v213\n" \
M_"v[112:115], a[188:191], a[140:143], v[112:115]\n" \
R_"a[200:203], v29 offset:20480\n" \
"v_lshl_add_u64 v[16:17], v[214:215], 0, v[216:217]\n" \
M_"v[112:115], a[252:255], a[204:207], v[112:115]\n" \
"s_addk_i32 s84, 0x7c00\n" \
"s_nop 0\n" \
M_"v[108:111], a[184:187], a[140:143], v[108:111]\n" \
B_"v27, s[68:71], s84 offen offset:3072 lds\n" \
M_"v[108:111], a[248:251], a[204:207], v[108:111]\n" \
"s_add_u32 m0, s30, 0x9000\n" \
"s_mov_b32 s84, 0x8180\n" \
M_"v[104:107], a[180:183], a[140:143], v[104:107]\n" \
B_"v22, s[64:67], s84 offen offset:0 lds\n" \
M_"v[104:107], a[244:247], a[204:207], v[104:107]\n" \
X_"a[166:167], v33 offset:1024\n" \
"v_mad_i32_i24 v214, v213, s6, v212\n" \
M_"v[100:103], a[176:179], a[140:143], v[100:103]\n" \
X_"a[228:229], v33 offset:8192\n" \
"s_add_i32 s5, s5, 32\n" \
M_"v[100:103], a[240:243], a[204:207], v[100:103]\n" \
R_"a[140:143], v28 offset:22528\n" \
"v_lshlrev_b32_e32 v215, 4, v214\n" \
M_"v[116:119], a[176:179], a[144:147], v[116:119]\n" \
R_"a[204:207], v29 offset:22528\n" \
"v_add_u32_e64 v216, s5, v215\n" \
M_"v[116:119], a[240:243], a[208:211], v[116:119]\n" \
"s_addk_i32 s84, 0x1c00\n" \
M_"v[120:123], a[180:183], a[144:147], v[120:123]\n" \
B_"v23, s[64:67], s84 offen offset:1024 lds\n" \
M_"v[120:123], a[244:247], a[208:211], v[120:123]\n" \
"s_addk_i32 s84, 0x1c00\n" \
M_"v[124:127], a[184:187], a[144:147], v[124:127]\n" \
B_"v22, s[64:67], s84 offen offset:2048 lds\n" \
M_"v[124:127], a[248:251], a[208:211], v[124:127]\n" \
"s_addk_i32 s84, 0x1c00\n" \
M_"v[128:131], a[188:191], a[144:147], v[128:131]\n" \
B_"v23, s[64:67], s84 offen offset:3072 lds\n" \
M_"v[128:131], a[252:255], a[208:211], v[128:131]\n" \
R_"a[144:147], v28 offset:0\n" \
"v_and_b32_e32 v216, 0xf0, v216\n" \
M_"v[144:147], a[188:191], a[148:151], v[144:147]\n" \
R_"a[208:211], v29 offset:0\n" \
"v_cmp_gt_u32_e32 vcc, 16, v214\n" \
M_"v[144:147], a[252:255], a[212:215], v[144:147]\n" \
"s_add_u32 m0, s31, 0xc000\n" \
"s_mov_b32 s84, 0x180000\n" \
M_"v[140:143], a[184:187], a[148:151], v[140:143]\n" \
B_"v26, s[72:75], s84 offen offset:0 lds\n" \
M_"v[140:143], a[248:251], a[212:215], v[140:143]\n" \
X_"a[230:231], v33 offset:9216\n" \
"s_movk_i32 s7, 0xf800\n" \
M_"v[136:139], a[180:183], a[148:151], v[136:139]\n" \
"s_addk_i32 s84, 0x7c00\n" \
M_"v[136:139], a[244:247], a[212:215], v[136:139]\n" \
B_"v26, s[72:75], s84 offen offset:1024 lds\n" \
M_"v[132:135], a[176:179], a[148:151], v[132:135]\n" \
X_"a[168:169], v34 offset:0\n" \
"v_cndmask_b32_e64 v214, v215, v216, vcc\n" \
M_"v[132:135], a[240:243], a[212:215], v[132:135]\n" \
R_"a[148:151], v28 offset:2048\n" \
"v_or_b32_e64 v215, 64, v212\n" \
M_"v[148:151], a[176:179], a[152:155], v[148:151]\n" \
R_"a[212:215], v29 offset:2048\n" \
"v_mul_u32_u24_e32 v216, 0xe39, v215\n" \
M_"v[148:151], a[240:243], a[216:219], v[148:151]\n" \
X_"a[170:171], v34 offset:1024\n" \
"v_lshrrev_b32_e64 v216, 16, v216\n" \
M_"v[152:155], a[180:183], a[152:155], v[152:155]\n" \
X_"a[232:233], v34 offset:8192\n" \
"v_mad_i32_i24 v215, v216, s6, v215\n" \
M_"v[152:155], a[244:247], a[216:219], v[152:155]\n" \
X_"a[234:235], v34 offset:9216\n" \
"v_lshlrev_b32_e64 v217, 4, v215\n" \
M_"v[156:159], a[184:187], a[152:155], v[156:159]\n" \
X_"a[172:173], v35 offset:0\n" \
"v_add_u32_e64 v220, s5, v217\n" \
M_"v[156:159], a[248:251], a[216:219], v[156:159]\n" \
X_"a[174:175], v35 offset:1024\n" \
"v_and_b32_e32 v220, 0xf0, v220\n" \
M_"v[160:163], a[188:191], a[152:155], v[160:163]\n" \
X_"a[236:237], v35 offset:8192\n" \
"v_cmp_gt_u32_e64 vcc, 16, v215\n" \
M_"v[160:163], a[252:255], a[216:219], v[160:163]\n" \
R_"a[152:155], v28 offset:4096\n" \
"v_cndmask_b32_e64 v215, v217, v220, vcc\n" \
M_"v[176:179], a[188:191], a[156:159], v[176:179]\n" \
R_"a[216:219], v29 offset:4096\n" \
"v_or_b32_e32 v217, 0x80, v212\n" \
M_"v[176:179], a[252:255], a[220:223], v[176:179]\n" \
X_"a[238:239], v35 offset:9216\n" \
"v_mul_u32_u24_e32 v220, 0xe39, v217\n" \
M_"v[172:175], a[184:187], a[156:159], v[172:175]\n" \
"v_lshrrev_b32_e64 v220, 16, v220\n" \
"v_mad_i32_i24 v217, v220, s6, v217\n" \
M_"v[172:175], a[248:251], a[220:223], v[172:175]\n" \
"v_lshlrev_b32_e32 v221, 4, v217\n" \
"v_add_u32_e32 v222, s5, v221\n" \
M_"v[168:171], a[180:183], a[156:159], v[168:171]\n" \
"v_and_b32_e32 v222, 0xf0, v222\n" \
"v_cmp_gt_u32_e64 vcc, 16, v217\n" \
M_"v[168:171], a[244:247], a[220:223], v[168:171]\n" \
"s_nop 0\n" \
"v_cndmask_b32_e32 v217, v221, v222, vcc\n" \
"v_or_b32_e32 v221, 0xc0, v212\n" \
M_"v[164:167], a[176:179], a[156:159], v[164:167]\n" \
"v_mul_u32_u24_e32 v222, 0xe39, v221\n" \
"v_lshrrev_b32_e64 v222, 16, v222\n" \
M_"v[164:167], a[240:243], a[220:223], v[164:167]\n" \
R_"a[156:159], v28 offset:6144\n" \
R_"a[220:223], v29 offset:6144\n" \
"s_waitcnt lgkmcnt(15)\n" \
M_"a[0:3], a[160:163], a[128:131], a[0:3]\n" \
"s_addk_i32 s84, 0x7c00\n" \
M_"a[0:3], a[224:227], a[192:195], a[0:3]\n" \
B_"v27, s[72:75], s84 offen offset:2048 lds\n" \
M_"a[4:7], a[164:167], a[128:131], a[4:7]\n" \
X_"a[176:177], v36 offset:0\n" \
"v_mad_i32_i24 v221, v222, s6, v221\n" \
"s_waitcnt lgkmcnt(15)\n" \
M_"a[4:7], a[228:231], a[192:195], a[4:7]\n" \
"s_addk_i32 s84, 0x7c00\n" \
"s_waitcnt lgkmcnt(11)\n" \
M_"a[8:11], a[168:171], a[128:131], a[8:11]\n" \
B_"v27, s[72:75], s84 offen offset:3072 lds\n" \
"s_waitcnt lgkmcnt(9)\n" \
M_"a[8:11], a[232:235], a[192:195], a[8:11]\n" \
X_"a[178:179], v36 offset:1024\n" \
"v_lshlrev_b32_e32 v223, 4, v221\n" \
"s_waitcnt lgkmcnt(8)\n" \
M_"a[12:15], a[172:175], a[128:131], a[12:15]\n" \
X_"a[240:241], v36 offset:8192\n" \
"v_add_u32_e32 v224, s5, v223\n" \
"s_waitcnt lgkmcnt(5)\n" \
M_"a[12:15], a[236:239], a[192:195], a[12:15]\n" \
"v_and_b32_e32 v224, 0xf0, v224\n" \
"v_cmp_gt_u32_e64 vcc, 16, v221\n" \
M_"a[28:31], a[172:175], a[132:135], a[28:31]\n" \
"s_nop 0\n" \
"v_cndmask_b32_e32 v221, v223, v224, vcc\n" \
"v_or_b32_e32 v223, 0x100, v212\n" \
M_"a[28:31], a[236:239], a[196:199], a[28:31]\n" \
X_"a[242:243], v36 offset:9216\n" \
"v_mul_u32_u24_e32 v224, 0xe39, v223\n" \
M_"a[24:27], a[168:171], a[132:135], a[24:27]\n" \
B_"v[200:203], v192, s[76:79], s34 offen offset:0\n" \
M_"a[24:27], a[232:235], a[196:199], a[24:27]\n" \
"v_lshrrev_b32_e64 v224, 16, v224\n" \
"v_mad_i32_i24 v223, v224, s6, v223\n" \
M_"a[20:23], a[164:167], a[132:135], a[20:23]\n" \
"v_lshlrev_b32_e32 v225, 4, v223\n" \
"v_add_u32_e32 v226, s5, v225\n" \
M_"a[20:23], a[228:231], a[196:199], a[20:23]\n" \
X_"a[180:181], v37 offset:0\n" \
"v_and_b32_e32 v226, 0xf0, v226\n" \
M_"a[16:19], a[160:163], a[132:135], a[16:19]\n" \
"v_cmp_gt_u32_e32 vcc, 16, v223\n" \
"s_nop 1\n" \
"v_cndmask_b32_e64 v223, v225, v226, vcc\n" \
M_"a[16:19], a[224:227], a[196:199], a[16:19]\n" \
"v_or_b32_e32 v225, 0x140, v212\n" \
"v_mul_u32_u24_e32 v226, 0xe39, v225\n" \
M_"a[32:35], a[160:163], a[136:139], a[32:35]\n" \
B_"v[204:207], v193, s[76:79], s34 offen offset:0\n" \
M_"a[32:35], a[224:227], a[200:203], a[32:35]\n" \
"s_mov_b32 m0, s32\n" \
X_"a[182:183], v37 offset:1024\n" \
M_"a[36:39], a[164:167], a[136:139], a[36:39]\n" \
"s_mov_b32 exec_lo, 0x0\n" \
"s_mov_b32 exec_hi, 0xff00\n" \
"global_load_lds_dword v[184:185], off\n" \
"s_mov_b64 exec, -1\n" \
M_"a[36:39], a[228:231], a[200:203], a[36:39]\n" \
X_"a[244:245], v37 offset:8192\n" \
"v_lshrrev_b32_e32 v226, 16, v226\n" \
M_"a[40:43], a[168:171], a[136:139], a[40:43]\n" \
"v_mad_i32_i24 v225, v226, s6, v225\n" \
"v_lshlrev_b32_e64 v227, 4, v225\n" \
M_"a[40:43], a[232:235], a[200:203], a[40:43]\n" \
"buffer_load_dword v208, v194, s[76:79], s34 offen offset:32\n" \
M_"a[44:47], a[172:175], a[136:139], a[44:47]\n" \
X_"a[246:247], v37 offset:9216\n" \
"v_add_u32_e64 v228, s5, v227\n" \
M_"a[44:47], a[236:239], a[200:203], a[44:47]\n" \
"v_and_b32_e32 v228, 0xf0, v228\n" \
"v_cmp_gt_u32_e64 vcc, 16, v225\n" \
M_"a[60:63], a[172:175], a[140:143], a[60:63]\n" \
"s_nop 0\n" \
"v_cndmask_b32_e32 v225, v227, v228, vcc\n" \
"v_or_b32_e32 v227, 0x180, v212\n" \
M_"a[60:63], a[236:239], a[204:207], a[60:63]\n" \
"v_mul_u32_u24_e32 v228, 0xe39, v227\n" \
"v_lshrrev_b32_e64 v228, 16, v228\n" \
M_"a[56:59], a[168:171], a[140:143], a[56:59]\n" \
"buffer_load_dword v209, v194, s[76:79], s34 offen offset:1184\n" \
M_"a[56:59], a[232:235], a[204:207], a[56:59]\n" \
X_"a[184:185], v38 offset:0\n" \
"v_mad_i32_i24 v227, v228, s6, v227\n" \
M_"a[52:55], a[164:167], a[140:143], a[52:55]\n" \
"v_lshlrev_b32_e32 v229, 4, v227\n" \
"v_add_u32_e32 v230, s5, v229\n" \
M_"a[52:55], a[228:231], a[204:207], a[52:55]\n" \
X_"a[186:187], v38 offset:1024\n" \
"v_and_b32_e32 v230, 0xf0, v230\n" \
M_"a[48:51], a[160:163], a[140:143], a[48:51]\n" \
"v_cmp_gt_u32_e32 vcc, 16, v227\n" \
"s_nop 1\n" \
"v_cndmask_b32_e64 v227, v229, v230, vcc\n" \
M_"a[48:51], a[224:227], a[204:207], a[48:51]\n" \
"v_or_b32_e32 v229, 0x1c0, v212\n" \
"v_mul_u32_u24_e32 v230, 0xe39, v229\n" \
M_"a[64:67], a[160:163], a[144:147], a[64:67]\n" \
"buffer_load_dword v211, v194, s[76:79], s34 offen offset:2336\n" \
M_"a[64:67], a[224:227], a[208:211], a[64:67]\n" \
X_"a[248:249], v38 offset:8192\n" \
"v_lshrrev_b32_e64 v230, 16, v230\n" \
M_"a[68:71], a[164:167], a[144:147], a[68:71]\n" \
"v_mad_i32_i24 v229, v230, s6, v229\n" \
"v_lshlrev_b32_e64 v231, 4, v229\n" \
M_"a[68:71], a[228:231], a[208:211], a[68:71]\n" \
X_"a[250:251], v38 offset:9216\n" \
"v_add_u32_e64 v232, s5, v231\n" \
M_"a[72:75], a[168:171], a[144:147], a[72:75]\n" \
"v_and_b32_e32 v232, 0xf0, v232\n" \
"v_cmp_gt_u32_e64 vcc, 16, v229\n" \
M_"a[72:75], a[232:235], a[208:211], a[72:75]\n" \
"s_mov_b32 s76, s20\n" \
"s_and_b32 s77, s21, 0xffff\n" \
M_"a[76:79], a[172:175], a[144:147], a[76:79]\n" \
"s_mov_b32 s78, 0x480\n" \
"v_cndmask_b32_e32 v229, v231, v232, vcc\n" \
M_"a[76:79], a[236:239], a[208:211], a[76:79]\n" \
"buffer_load_ushort v210, v195, s[76:79], 0 offen\n" \
M_"a[92:95], a[172:175], a[148:151], a[92:95]\n" \
X_"a[188:189], v39 offset:0\n" \
"v_or_b32_e32 v231, 0x200, v212\n" \
M_"a[92:95], a[236:239], a[212:215], a[92:95]\n" \
"v_mul_u32_u24_e32 v232, 0xe39, v231\n" \
"v_lshrrev_b32_e64 v232, 16, v232\n" \
M_"a[88:91], a[168:171], a[148:151], a[88:91]\n" \
"s_mov_b32 s76, s8\n" \
"s_and_b32 s77, s9, 0xffff\n" \
M_"a[88:91], a[232:235], a[212:215], a[88:91]\n" \
"s_mov_b32 s78, 0x240000\n" \
X_"a[190:191], v39 offset:1024\n" \
M_"a[84:87], a[164:167], a[148:151], a[84:87]\n" \
"v_mad_i32_i24 v231, v232, s6, v231\n" \
"v_lshlrev_b32_e32 v233, 4, v231\n" \
".fill (L_pb + 7904 - .) / 4, 4, 0xBF800000\n" \
"L_pp2:\n" \
"s_cbranch_execz L_pret\n" \
M_"a[84:87], a[228:231], a[212:215], a[84:87]\n" \
X_"a[252:253], v39 offset:8192\n" \
"v_add_u32_e32 v234, s5, v233\n" \
M_"a[80:83], a[160:163], a[148:151], a[80:83]\n" \
"s_movk_i32 s5, 0x480\n" \
"v_mad_u32_u24 v0, v213, s5, v214\n" \
M_"a[80:83], a[224:227], a[212:215], a[80:83]\n" \
"v_mul_u32_u24_e32 v213, 0x480, v216\n" \
"s_movk_i32 s6, 0xfc00\n" \
M_"a[96:99], a[160:163], a[152:155], a[96:99]\n" \
X_"a[254:255], v39 offset:9216\n" \
"v_add3_u32 v1, v213, v215, s6\n" \
M_"a[96:99], a[224:227], a[216:219], a[96:99]\n" \
"v_mul_u32_u24_e32 v213, 0x480, v220\n" \
"v_add3_u32 v2, v213, v217, s7\n" \
M_"a[100:103], a[164:167], a[152:155], a[100:103]\n" \
"v_mul_u32_u24_e32 v213, 0x480, v222\n" \
"v_add3_u32 v3, v213, v221, s40\n" \
M_"a[100:103], a[228:231], a[216:219], a[100:103]\n" \
"v_mul_u32_u24_e32 v213, 0x480, v226\n" \
"v_add3_u32 v5, v213, v225, s6\n" \
M_"a[104:107], a[168:171], a[152:155], a[104:107]\n" \
"v_mul_u32_u24_e32 v213, 0x480, v228\n" \
"v_add3_u32 v6, v213, v227, s7\n" \
M_"a[104:107], a[232:235], a[216:219], a[104:107]\n" \
"v_mul_u32_u24_e32 v213, 0x480, v230\n" \
"v_add3_u32 v7, v213, v229, s40\n" \
M_"a[108:111], a[172:175], a[152:155], a[108:111]\n" \
"s_lshl_b32 s7, s3, 3\n" \
"s_lshl_b32 s40, s3, 1\n" \
M_"a[108:111], a[236:239], a[216:219], a[108:111]\n" \
"s_and_b32 s7, s7, 16\n" \
"s_and_b32 s40, s40, 2\n" \
"s_waitcnt lgkmcnt(15)\n" \
M_"a[124:127], a[172:175], a[156:159], a[124:127]\n" \
"s_and_b32 s6, s2, 7\n" \
"v_bfe_u32 v213, v20, 7, 1\n" \
M_"a[124:127], a[236:239], a[220:223], a[124:127]\n" \
"s_or_b32 s7, s40, s7\n" \
"v_and_b32_e32 v234, 0xf0, v234\n" \
M_"a[120:123], a[168:171], a[156:159], a[120:123]\n" \
"v_cmp_gt_u32_e32 vcc, 16, v231\n" \
"v_or3_b32 v213, s7, v213, v219\n" \
M_"a[120:123], a[232:235], a[220:223], a[120:123]\n" \
"v_and_b32_e32 v214, 32, v20\n" \
"s_lshl_b32 s6, s6, 5\n" \
M_"a[116:119], a[164:167], a[156:159], a[116:119]\n" \
"v_cndmask_b32_e32 v231, v233, v234, vcc\n" \
"v_mul_u32_u24_e32 v213, 18, v213\n" \
M_"a[116:119], a[228:231], a[220:223], a[116:119]\n" \
"v_mov_b32_e32 v215, 0x100\n" \
"v_mov_b32_e64 v216, s6\n" \
M_"a[112:115], a[160:163], a[156:159], a[112:115]\n" \
"v_cmp_eq_u32_e64 vcc, 0, v214\n" \
"v_and_or_b32 v213, v218, 1, v213\n" \
"s_waitcnt vmcnt(7) lgkmcnt(0)\n" \
M_"a[112:115], a[224:227], a[220:223], a[112:115]\n" \
"s_barrier\n" \
M_"v[52:55], a[176:179], a[128:131], v[52:55]\n" \
"s_add_u32 m0, s30, 0x0\n" \
"s_movk_i32 s84, 0x200\n" \
M_"v[52:55], a[240:243], a[192:195], v[52:55]\n" \
B_"v22, s[64:67], s84 offen offset:0 lds\n" \
M_"v[56:59], a[180:183], a[128:131], v[56:59]\n" \
X_"a[160:161], v32 offset:32768\n" \
"v_lshlrev_b32_e64 v212, 3, v212\n" \
M_"v[56:59], a[244:247], a[192:195], v[56:59]\n" \
"s_movk_i32 s84, 0x1e00\n" \
M_"v[60:63], a[184:187], a[128:131], v[60:63]\n" \
B_"v23, s[64:67], s84 offen offset:1024 lds\n" \
M_"v[60:63], a[248:251], a[192:195], v[60:63]\n" \
X_"a[162:163], v32 offset:33792\n" \
"v_cndmask_b32_e32 v214, v215, v216, vcc\n" \
M_"v[64:67], a[188:191], a[128:131], v[64:67]\n" \
X_"a[224:225], v32 offset:40960\n" \
"v_mad_u32_u24 v213, v213, s5, v214\n" \
M_"v[64:67], a[252:255], a[192:195], v[64:67]\n" \
R_"a[128:131], v28 offset:49152\n" \
"v_and_or_b32 v213, v20, 16, v213\n" \
M_"v[80:83], a[188:191], a[132:135], v[80:83]\n" \
R_"a[192:195], v29 offset:49152\n" \
"v_add_u32_e32 v9, 0x4800, v213\n" \
M_"v[80:83], a[252:255], a[196:199], v[80:83]\n" \
"s_movk_i32 s84, 0x3a00\n" \
M_"v[76:79], a[184:187], a[132:135], v[76:79]\n" \
B_"v22, s[64:67], s84 offen offset:2048 lds\n" \
M_"v[76:79], a[248:251], a[196:199], v[76:79]\n" \
"s_movk_i32 s84, 0x5600\n" \
M_"v[72:75], a[180:183], a[132:135], v[72:75]\n" \
B_"v23, s[64:67], s84 offen offset:3072 lds\n" \
M_"v[72:75], a[244:247], a[196:199], v[72:75]\n" \
"s_add_u32 m0, s31, 0x0\n" \
"s_mov_b32 s84, 0x200000\n" \
M_"v[68:71], a[176:179], a[132:135], v[68:71]\n" \
B_"v26, s[68:71], s84 offen offset:0 lds\n" \
M_"v[68:71], a[240:243], a[196:199], v[68:71]\n" \
R_"a[132:135], v28 offset:51200\n" \
"v_mul_u32_u24_e32 v213, 0x120, v218\n" \
M_"v[84:87], a[176:179], a[136:139], v[84:87]\n" \
R_"a[196:199], v29 offset:51200\n" \
"v_and_or_b32 v10, v212, 24, v213\n" \
M_"v[84:87], a[240:243], a[200:203], v[84:87]\n" \
"s_addk_i32 s84, 0x7c00\n" \
M_"v[88:91], a[180:183], a[136:139], v[88:91]\n" \
B_"v26, s[68:71], s84 offen offset:1024 lds\n" \
M_"v[88:91], a[244:247], a[200:203], v[88:91]\n" \
X_"a[226:227], v32 offset:41984\n" \
"v_lshrrev_b32_e64 v213, 2, v20\n" \
M_"v[92:95], a[184:187], a[136:139], v[92:95]\n" \
"s_addk_i32 s84, 0x7c00\n" \
"s_nop 0\n" \
M_"v[92:95], a[248:251], a[200:203], v[92:95]\n" \
B_"v27, s[68:71], s84 offen offset:2048 lds\n" \
M_"v[96:99], a[188:191], a[136:139], v[96:99]\n" \
X_"a[164:165], v33 offset:32768\n" \
"v_and_or_b32 v213, v213, 60, s4\n" \
M_"v[96:99], a[252:255], a[200:203], v[96:99]\n" \
R_"a[136:139], v28 offset:53248\n" \
"v_lshl_or_b32 v212, s3, 9, v212\n" \
M_"v[112:115], a[188:191], a[140:143], v[112:115]\n" \
R_"a[200:203], v29 offset:53248\n" \
"v_mad_u32_u24 v4, v224, s5, v223\n" \
M_"v[112:115], a[252:255], a[204:207], v[112:115]\n" \
"s_addk_i32 s84, 0x7c00\n" \
"s_nop 0\n" \
M_"v[108:111], a[184:187], a[140:143], v[108:111]\n" \
B_"v27, s[68:71], s84 offen offset:3072 lds\n" \
M_"v[108:111], a[248:251], a[204:207], v[108:111]\n" \
"s_add_u32 m0, s30, 0x1000\n" \
"s_mov_b32 s84, 0x8200\n" \
M_"v[104:107], a[180:183], a[140:143], v[104:107]\n" \
B_"v22, s[64:67], s84 offen offset:0 lds\n" \
M_"v[104:107], a[244:247], a[204:207], v[104:107]\n" \
X_"a[166:167], v33 offset:33792\n" \
"v_mad_u32_u24 v8, v232, s5, v231\n" \
M_"v[100:103], a[176:179], a[140:143], v[100:103]\n" \
X_"a[228:229], v33 offset:40960\n" \
"v_or_b32_e32 v18, 0x27000, v213\n" \
M_"v[100:103], a[240:243], a[204:207], v[100:103]\n" \
R_"a[140:143], v28 offset:55296\n" \
"v_or_b32_e32 v19, 0x27800, v212\n" \
M_"v[116:119], a[176:179], a[144:147], v[116:119]\n" \
R_"a[204:207], v29 offset:55296\n" \
"v_mov_b32_e32 v215, 0x1000c0c\n" \
M_"v[116:119], a[240:243], a[208:211], v[116:119]\n" \
"s_addk_i32 s84, 0x1c00\n" \
M_"v[120:123], a[180:183], a[144:147], v[120:123]\n" \
B_"v23, s[64:67], s84 offen offset:1024 lds\n" \
M_"v[120:123], a[244:247], a[208:211], v[120:123]\n" \
"s_addk_i32 s84, 0x1c00\n" \
M_"v[124:127], a[184:187], a[144:147], v[124:127]\n" \
B_"v22, s[64:67], s84 offen offset:2048 lds\n" \
M_"v[124:127], a[248:251], a[208:211], v[124:127]\n" \
"s_addk_i32 s84, 0x1c00\n" \
"s_nop 0\n" \
M_"v[128:131], a[188:191], a[144:147], v[128:131]\n" \
B_"v23, s[64:67], s84 offen offset:3072 lds\n" \
M_"v[128:131], a[252:255], a[208:211], v[128:131]\n" \
R_"a[144:147], v28 offset:32768\n" \
"v_mov_b32_e32 v216, 0xc0c0100\n" \
M_"v[144:147], a[188:191], a[148:151], v[144:147]\n" \
R_"a[208:211], v29 offset:32768\n" \
"v_bfe_u32 v214, v20, 4, 1\n" \
M_"v[144:147], a[252:255], a[212:215], v[144:147]\n" \
"s_add_u32 m0, s31, 0x4000\n" \
"s_mov_b32 s84, 0x200000\n" \
M_"v[140:143], a[184:187], a[148:151], v[140:143]\n" \
B_"v26, s[72:75], s84 offen offset:0 lds\n" \
M_"v[140:143], a[248:251], a[212:215], v[140:143]\n" \
X_"a[230:231], v33 offset:41984\n" \
"v_lshrrev_b32_e64 v212, 8, v20\n" \
M_"v[136:139], a[180:183], a[148:151], v[136:139]\n" \
"s_addk_i32 s84, 0x7c00\n" \
M_"v[136:139], a[244:247], a[212:215], v[136:139]\n" \
B_"v26, s[72:75], s84 offen offset:1024 lds\n" \
M_"v[132:135], a[176:179], a[148:151], v[132:135]\n" \
X_"a[168:169], v34 offset:32768\n" \
"v_cmp_eq_u32_e32 vcc, 0, v214\n" \
M_"v[132:135], a[240:243], a[212:215], v[132:135]\n" \
R_"a[148:151], v28 offset:34816\n" \
"v_bfe_u32 v213, v20, 4, 4\n" \
M_"v[148:151], a[176:179], a[152:155], v[148:151]\n" \
R_"a[212:215], v29 offset:34816\n" \
"v_mov_b32_e32 v217, 0xc0c0302\n" \
M_"v[148:151], a[240:243], a[216:219], v[148:151]\n" \
X_"a[170:171], v34 offset:33792\n" \
"v_cndmask_b32_e64 v215, v215, v216, vcc\n" \
M_"v[152:155], a[180:183], a[152:155], v[152:155]\n" \
X_"a[232:233], v34 offset:40960\n" \
"v_mov_b32_e32 v216, 0x3020c0c\n" \
M_"v[152:155], a[244:247], a[216:219], v[152:155]\n" \
X_"a[234:235], v34 offset:41984\n" \
"v_lshl_or_b32 v214, v212, 2, v214\n" \
M_"v[156:159], a[184:187], a[152:155], v[156:159]\n" \
X_"a[172:173], v35 offset:32768\n" \
"v_cndmask_b32_e64 v216, v216, v217, vcc\n" \
M_"v[156:159], a[248:251], a[216:219], v[156:159]\n" \
X_"a[174:175], v35 offset:33792\n" \
"v_mov_b32_e32 v217, 0xc0c0c0c\n" \
M_"v[160:163], a[188:191], a[152:155], v[160:163]\n" \
X_"a[236:237], v35 offset:40960\n" \
"v_cmp_gt_u32_e64 vcc, v214, v213\n" \
M_"v[160:163], a[252:255], a[216:219], v[160:163]\n" \
R_"a[152:155], v28 offset:36864\n" \
"v_or_b32_e64 v214, 2, v214\n" \
M_"v[176:179], a[188:191], a[156:159], v[176:179]\n" \
R_"a[216:219], v29 offset:36864\n" \
"s_lshr_b32 s4, s2, 5\n" \
M_"v[176:179], a[252:255], a[220:223], v[176:179]\n" \
X_"a[238:239], v35 offset:41984\n" \
"v_cndmask_b32_e32 v40, v217, v215, vcc\n" \
M_"v[172:175], a[184:187], a[156:159], v[172:175]\n" \
"v_cndmask_b32_e32 v42, v215, v217, vcc\n" \
"v_cmp_gt_u32_e32 vcc, v214, v213\n" \
M_"v[172:175], a[248:251], a[220:223], v[172:175]\n" \
"v_and_b32_e32 v214, 16, v20\n" \
"s_movk_i32 s42, 0x3f80\n" \
M_"v[168:171], a[180:183], a[156:159], v[168:171]\n" \
"s_xor_b32 s5, s4, 15\n" \
M_"v[168:171], a[244:247], a[220:223], v[168:171]\n" \
"s_cmpk_lt_u32 s3, 2\n" \
"s_cselect_b32 s6, s5, s4\n" \
M_"v[164:167], a[176:179], a[156:159], v[164:167]\n" \
"s_lshl_b32 s7, s6, 7\n" \
"v_and_b32_e32 v213, 0x320, v20\n" \
M_"v[164:167], a[240:243], a[220:223], v[164:167]\n" \
R_"a[156:159], v28 offset:38912\n" \
R_"a[220:223], v29 offset:38912\n" \
"s_waitcnt lgkmcnt(15)\n" \
M_"a[0:3], a[160:163], a[128:131], a[0:3]\n" \
"s_addk_i32 s84, 0x7c00\n" \
M_"a[0:3], a[224:227], a[192:195], a[0:3]\n" \
B_"v27, s[72:75], s84 offen offset:2048 lds\n" \
M_"a[4:7], a[164:167], a[128:131], a[4:7]\n" \
X_"a[176:177], v36 offset:32768\n" \
"s_movk_i32 s41, 0x100\n" \
"s_waitcnt lgkmcnt(15)\n" \
M_"a[4:7], a[228:231], a[192:195], a[4:7]\n" \
"s_addk_i32 s84, 0x7c00\n" \
"s_waitcnt lgkmcnt(11)\n" \
M_"a[8:11], a[168:171], a[128:131], a[8:11]\n" \
B_"v27, s[72:75], s84 offen offset:3072 lds\n" \
"s_waitcnt lgkmcnt(9)\n" \
M_"a[8:11], a[232:235], a[192:195], a[8:11]\n" \
X_"a[178:179], v36 offset:33792\n" \
"v_lshlrev_b32_e64 v214, v214, s42\n" \
"s_waitcnt lgkmcnt(8)\n" \
M_"a[12:15], a[172:175], a[128:131], a[12:15]\n" \
X_"a[240:241], v36 offset:40960\n" \
"v_mov_b32_e32 v215, 0\n" \
"s_waitcnt lgkmcnt(5)\n" \
M_"a[12:15], a[236:239], a[192:195], a[12:15]\n" \
"s_add_i32 s40, s7, -16\n" \
"v_cndmask_b32_e32 v41, v217, v216, vcc\n" \
M_"a[28:31], a[172:175], a[132:135], a[28:31]\n" \
"v_cndmask_b32_e32 v43, v216, v217, vcc\n" \
"v_cmp_eq_u32_e32 vcc, s41, v213\n" \
M_"a[28:31], a[236:239], a[196:199], a[28:31]\n" \
X_"a[242:243], v36 offset:41984\n" \
"s_mov_b32 s41, 0\n" \
M_"a[24:27], a[168:171], a[132:135], a[24:27]\n" \
"s_and_b32 s43, s3, 1\n" \
"v_cndmask_b32_e64 v46, v215, v214, vcc\n" \
M_"a[24:27], a[232:235], a[196:199], a[24:27]\n" \
"v_and_b32_e32 v213, 0x330, v20\n" \
M_"a[20:23], a[164:167], a[132:135], a[20:23]\n" \
"s_cmp_lg_u32 s7, 0\n" \
"s_cselect_b32 s40, s40, s41\n" \
M_"a[20:23], a[228:231], a[196:199], a[20:23]\n" \
X_"a[180:181], v37 offset:32768\n" \
"s_mov_b32 s42, 0xff800000\n" \
M_"a[16:19], a[160:163], a[132:135], a[16:19]\n" \
"s_cmp_lg_u32 s7, 0\n" \
"s_cselect_b32 s47, s41, s42\n" \
M_"a[16:19], a[224:227], a[196:199], a[16:19]\n" \
"s_lshl_b32 s41, s43, 6\n" \
"v_lshrrev_b32_e32 v215, 6, v20\n" \
M_"a[32:35], a[160:163], a[136:139], a[32:35]\n" \
X_"a[182:183], v37 offset:33792\n" \
"v_mov_b32_e32 v214, 0x3f80\n" \
M_"a[32:35], a[224:227], a[200:203], a[32:35]\n" \
"v_cmp_eq_u32_e32 vcc, 32, v213\n" \
"s_or_b32 s7, s41, s7\n" \
M_"a[36:39], a[164:167], a[136:139], a[36:39]\n" \
"v_lshrrev_b32_e64 v213, 7, v20\n" \
"v_bfe_u32 v216, v20, 6, 2\n" \
M_"a[36:39], a[228:231], a[200:203], a[36:39]\n" \
X_"a[244:245], v37 offset:40960\n" \
"v_lshrrev_b32_e64 v219, 1, v20\n" \
M_"a[40:43], a[168:171], a[136:139], a[40:43]\n" \
"s_mov_b32 m0, s32\n" \
"v_bfe_u32 v215, v215, 1, 1\n" \
M_"a[40:43], a[232:235], a[200:203], a[40:43]\n" \
"s_mov_b32 exec_lo, 0xffffffff\n" \
"s_mov_b32 exec_hi, 0x0\n" \
"global_load_lds_dword v[188:189], off\n" \
"s_mov_b64 exec, -1\n" \
M_"a[44:47], a[172:175], a[136:139], a[44:47]\n" \
X_"a[246:247], v37 offset:41984\n" \
"v_cndmask_b32_e64 v47, 0, v214, vcc\n" \
M_"a[44:47], a[236:239], a[200:203], a[44:47]\n" \
"v_or_b32_e64 v214, s7, v213\n" \
"v_lshl_or_b32 v217, s43, 3, v213\n" \
M_"a[60:63], a[172:175], a[140:143], a[60:63]\n" \
"v_lshlrev_b32_e32 v218, 10, v212\n" \
"v_lshlrev_b32_e32 v216, 7, v216\n" \
M_"a[60:63], a[236:239], a[204:207], a[60:63]\n" \
"v_and_b32_e64 v219, 24, v219\n" \
"v_and_or_b32 v213, v213, 2, v215\n" \
M_"a[56:59], a[168:171], a[140:143], a[56:59]\n" \
X_"a[184:185], v38 offset:32768\n" \
"v_add_u32_e64 v217, s40, v217\n" \
M_"a[56:59], a[232:235], a[204:207], a[56:59]\n" \
"v_or3_b32 v216, v216, v218, v219\n" \
"v_lshlrev_b32_e64 v213, 5, v213\n" \
M_"a[52:55], a[164:167], a[140:143], a[52:55]\n" \
"s_mov_b32 s40, 0x14000\n" \
"s_lshl_b32 s7, s2, 2\n" \
M_"a[52:55], a[228:231], a[204:207], a[52:55]\n" \
X_"a[186:187], v38 offset:33792\n" \
"v_or3_b32 v48, v216, v213, s40\n" \
M_"a[48:51], a[160:163], a[140:143], a[48:51]\n" \
"s_lshl_b32 s40, s43, 2\n" \
"s_lshl_b32 s6, s6, 3\n" \
M_"a[48:51], a[224:227], a[204:207], a[48:51]\n" \
"v_or_b32_e32 v213, s40, v212\n" \
"s_or_b32 s6, s6, s40\n" \
M_"a[64:67], a[160:163], a[144:147], a[64:67]\n" \
X_"a[248:249], v38 offset:40960\n" \
"s_and_b32 s40, s7, 0x7c\n" \
M_"a[64:67], a[224:227], a[208:211], a[64:67]\n" \
"s_or_b32 s40, s40, s3\n" \
M_"a[68:71], a[164:167], a[144:147], a[68:71]\n" \
"s_cmpk_lt_u32 s40, 64\n" \
"s_cselect_b32 s41, s4, s5\n" \
M_"a[68:71], a[228:231], a[208:211], a[68:71]\n" \
X_"a[250:251], v38 offset:41984\n" \
"s_lshl_b32 s40, s40, 1\n" \
M_"a[72:75], a[168:171], a[144:147], a[72:75]\n" \
"s_lshl_b32 s41, s41, 7\n" \
"s_and_b32 s40, s40, 0x7e\n" \
M_"a[72:75], a[232:235], a[208:211], a[72:75]\n" \
"s_or_b32 s40, s41, s40\n" \
"s_mulk_i32 s40, 0x480\n" \
M_"a[76:79], a[172:175], a[144:147], a[76:79]\n" \
"v_add_u32_e64 v49, s40, v20\n" \
"s_mov_b32 s40, 0xc000\n" \
M_"a[76:79], a[236:239], a[208:211], a[76:79]\n" \
X_"a[188:189], v39 offset:32768\n" \
"s_and_b32 s7, s7, 28\n" \
M_"a[92:95], a[172:175], a[148:151], a[92:95]\n" \
"s_bfe_u32 s45, s2, 0x20003\n" \
"s_mov_b32 s41, 0x8000\n" \
M_"a[92:95], a[236:239], a[212:215], a[92:95]\n" \
"s_cmp_lg_u32 s4, 0\n" \
"s_cselect_b32 s51, s40, s41\n" \
M_"a[88:91], a[168:171], a[148:151], a[88:91]\n" \
X_"a[190:191], v39 offset:33792\n" \
"s_movk_i32 s40, 0x4000\n" \
M_"a[88:91], a[232:235], a[212:215], a[88:91]\n" \
"s_lshr_b32 s44, s3, 1\n" \
"s_or_b32 s46, s7, s45\n" \
M_"a[84:87], a[164:167], a[148:151], a[84:87]\n" \
"s_waitcnt vmcnt(17)\n" \
"v_or_b32_e32 v204, v210, v204\n" \
"v_or_b32_e64 v206, v210, v206\n" \
M_"a[84:87], a[228:231], a[212:215], a[84:87]\n" \
"v_mov_b32_e64 v210, 0\n" \
"s_add_u32 s88, s35, 0x1000\n" \
M_"a[80:83], a[160:163], a[148:151], a[80:83]\n" \
X_"a[252:253], v39 offset:40960\n" \
M_"a[80:83], a[224:227], a[212:215], a[80:83]\n" \
"ds_write_b128 v196, v[200:203]\n" \
M_"a[96:99], a[160:163], a[152:155], a[96:99]\n" \
X_"a[254:255], v39 offset:41984\n" \
M_"a[96:99], a[224:227], a[216:219], a[96:99]\n" \
"s_cmp_lg_u32 s4, 0\n" \
"s_cselect_b32 s53, s41, s40\n" \
M_"a[100:103], a[164:167], a[152:155], a[100:103]\n" \
"ds_write_b128 v197, v[204:207]\n" \
M_"a[100:103], a[228:231], a[216:219], a[100:103]\n" \
"s_lshl_b32 s40, s44, 15\n" \
"s_lshl_b32 s41, s43, 14\n" \
M_"a[104:107], a[168:171], a[152:155], a[104:107]\n" \
"s_lshl_b32 s48, s46, 20\n" \
"s_or_b32 s41, s40, s41\n" \
M_"a[104:107], a[232:235], a[216:219], a[104:107]\n" \
"ds_write_b128 v198, v[208:211]\n" \
M_"a[108:111], a[172:175], a[152:155], a[108:111]\n" \
"s_add_i32 s49, s48, 0x1000000\n" \
"s_or_b32 s42, s49, s41\n" \
M_"a[108:111], a[236:239], a[216:219], a[108:111]\n" \
"s_lshl_b32 s4, s4, 16\n" \
"s_or_b32 s41, s42, s4\n" \
"s_waitcnt lgkmcnt(15)\n" \
M_"a[124:127], a[172:175], a[156:159], a[124:127]\n" \
"s_lshl_b32 s4, s5, 16\n" \
"s_mul_i32 s5, s6, 0x24800\n" \
M_"a[124:127], a[236:239], a[220:223], a[124:127]\n" \
"s_lshl_b32 s6, s46, 9\n" \
"s_lshl_b32 s7, s7, 6\n" \
M_"a[120:123], a[168:171], a[156:159], a[120:123]\n" \
"s_add_i32 s6, s5, s6\n" \
"s_or_b32 s5, s5, s7\n" \
M_"a[120:123], a[232:235], a[220:223], a[120:123]\n" \
"s_lshl_b32 s7, s45, 2\n" \
"s_or_b32 s5, s5, s7\n" \
M_"a[116:119], a[164:167], a[156:159], a[116:119]\n" \
"s_lshl_b32 s7, s3, 12\n" \
"s_or_b32 s50, s49, s7\n" \
M_"a[116:119], a[228:231], a[220:223], a[116:119]\n" \
"s_lshl_b32 s7, s3, 11\n" \
"v_lshrrev_b32_e64 v215, 4, v20\n" \
M_"a[112:115], a[160:163], a[156:159], a[112:115]\n" \
"v_bfe_u32 v216, v20, 4, 3\n" \
"s_or_b32 s42, s42, s4\n" \
"s_waitcnt vmcnt(1) lgkmcnt(0)\n" \
M_"a[112:115], a[224:227], a[220:223], a[112:115]\n" \
"s_barrier\n" \
M_"v[52:55], a[176:179], a[128:131], v[52:55]\n" \
"s_add_u32 m0, s30, 0x8000\n" \
"s_movk_i32 s84, 0x280\n" \
M_"v[52:55], a[240:243], a[192:195], v[52:55]\n" \
B_"v22, s[64:67], s84 offen offset:0 lds\n" \
M_"v[56:59], a[180:183], a[128:131], v[56:59]\n" \
"s_movk_i32 s84, 0x1e80\n" \
M_"v[56:59], a[244:247], a[192:195], v[56:59]\n" \
B_"v23, s[64:67], s84 offen offset:1024 lds\n" \
M_"v[60:63], a[184:187], a[128:131], v[60:63]\n" \
X_"a[160:161], v32 offset:0\n" \
"s_lshl_b32 s4, s43, 11\n" \
M_"v[60:63], a[248:251], a[192:195], v[60:63]\n" \
"s_movk_i32 s84, 0x3a80\n" \
M_"v[64:67], a[188:191], a[128:131], v[64:67]\n" \
B_"v22, s[64:67], s84 offen offset:2048 lds\n" \
M_"v[64:67], a[252:255], a[192:195], v[64:67]\n" \
R_"a[128:131], v28 offset:16384\n" \
"s_add_i32 s52, s48, s7\n" \
M_"v[80:83], a[188:191], a[132:135], v[80:83]\n" \
R_"a[192:195], v29 offset:16384\n" \
"s_add_i32 s53, s53, s7\n" \
M_"v[80:83], a[252:255], a[196:199], v[80:83]\n" \
"s_movk_i32 s84, 0x5680\n" \
M_"v[76:79], a[184:187], a[132:135], v[76:79]\n" \
B_"v23, s[64:67], s84 offen offset:3072 lds\n" \
M_"v[76:79], a[248:251], a[196:199], v[76:79]\n" \
"s_add_u32 m0, s31, 0x8000\n" \
"s_mov_b32 s84, 0x280000\n" \
M_"v[72:75], a[180:183], a[132:135], v[72:75]\n" \
B_"v26, s[68:71], s84 offen offset:0 lds\n" \
M_"v[72:75], a[244:247], a[196:199], v[72:75]\n" \
X_"a[162:163], v32 offset:1024\n" \
"v_mul_u32_u24_e32 v214, 0x480, v214\n" \
M_"v[68:71], a[176:179], a[132:135], v[68:71]\n" \
X_"a[224:225], v32 offset:8192\n" \
"v_bitop3_b32 v218, v212, v215, 7 bitop3:0x78\n" \
M_"v[68:71], a[240:243], a[196:199], v[68:71]\n" \
R_"a[132:135], v28 offset:18432\n" \
"v_bitop3_b32 v212, v212, v216, 4 bitop3:0x36\n" \
M_"v[84:87], a[176:179], a[136:139], v[84:87]\n" \
R_"a[196:199], v29 offset:18432\n" \
"s_lshl_b32 s7, s3, 10\n" \
M_"v[84:87], a[240:243], a[200:203], v[84:87]\n" \
"s_addk_i32 s84, 0x7c00\n" \
M_"v[88:91], a[180:183], a[136:139], v[88:91]\n" \
B_"v26, s[68:71], s84 offen offset:1024 lds\n" \
M_"v[88:91], a[244:247], a[200:203], v[88:91]\n" \
"s_addk_i32 s84, 0x7c00\n" \
M_"v[92:95], a[184:187], a[136:139], v[92:95]\n" \
B_"v27, s[68:71], s84 offen offset:2048 lds\n" \
M_"v[92:95], a[248:251], a[200:203], v[92:95]\n" \
"s_addk_i32 s84, 0x7c00\n" \
M_"v[96:99], a[188:191], a[136:139], v[96:99]\n" \
B_"v27, s[68:71], s84 offen offset:3072 lds\n" \
M_"v[96:99], a[252:255], a[200:203], v[96:99]\n" \
R_"a[136:139], v28 offset:20480\n" \
"s_or_b32 s4, s40, s4\n" \
M_"v[112:115], a[188:191], a[140:143], v[112:115]\n" \
R_"a[200:203], v29 offset:20480\n" \
"v_lshl_or_b32 v25, v212, 4, v214\n" \
M_"v[112:115], a[252:255], a[204:207], v[112:115]\n" \
"s_add_u32 m0, s30, 0x9000\n" \
"s_mov_b32 s84, 0x8280\n" \
M_"v[108:111], a[184:187], a[140:143], v[108:111]\n" \
B_"v22, s[64:67], s84 offen offset:0 lds\n" \
M_"v[108:111], a[248:251], a[204:207], v[108:111]\n" \
"s_addk_i32 s84, 0x1c00\n" \
M_"v[104:107], a[180:183], a[140:143], v[104:107]\n" \
B_"v23, s[64:67], s84 offen offset:1024 lds\n" \
M_"v[104:107], a[244:247], a[204:207], v[104:107]\n" \
"s_addk_i32 s84, 0x1c00\n" \
M_"v[100:103], a[176:179], a[140:143], v[100:103]\n" \
B_"v22, s[64:67], s84 offen offset:2048 lds\n" \
M_"v[100:103], a[240:243], a[204:207], v[100:103]\n" \
R_"a[140:143], v28 offset:22528\n" \
"v_mul_u32_u24_e32 v212, 0x480, v217\n" \
M_"v[116:119], a[176:179], a[144:147], v[116:119]\n" \
R_"a[204:207], v29 offset:22528\n" \
"v_bitop3_b32 v213, v215, v213, 7 bitop3:0x6c\n" \
M_"v[116:119], a[240:243], a[208:211], v[116:119]\n" \
"s_addk_i32 s84, 0x1c00\n" \
M_"v[120:123], a[180:183], a[144:147], v[120:123]\n" \
B_"v23, s[64:67], s84 offen offset:3072 lds\n" \
M_"v[120:123], a[244:247], a[208:211], v[120:123]\n" \
"s_add_u32 m0, s31, 0xc000\n" \
"s_mov_b32 s84, 0x280000\n" \
M_"v[124:127], a[184:187], a[144:147], v[124:127]\n" \
B_"v26, s[72:75], s84 offen offset:0 lds\n" \
M_"v[124:127], a[248:251], a[208:211], v[124:127]\n" \
"s_addk_i32 s84, 0x7c00\n" \
M_"v[128:131], a[188:191], a[144:147], v[128:131]\n" \
B_"v26, s[72:75], s84 offen offset:1024 lds\n" \
M_"v[128:131], a[252:255], a[208:211], v[128:131]\n" \
R_"a[144:147], v28 offset:0\n" \
"s_or_b32 s40, s7, 0x14000\n" \
M_"v[144:147], a[188:191], a[148:151], v[144:147]\n" \
R_"a[208:211], v29 offset:0\n" \
"s_lshl_b32 s43, s44, 13\n" \
M_"v[144:147], a[252:255], a[212:215], v[144:147]\n" \
R_"v[200:203], v199 offset:0\n" \
"s_mulk_i32 s44, 0xe800\n" \
M_"v[140:143], a[184:187], a[148:151], v[140:143]\n" \
R_"v[204:207], v199 offset:4096\n" \
"s_lshl_b32 s7, s3, 8\n" \
M_"v[140:143], a[248:251], a[212:215], v[140:143]\n" \
X_"a[226:227], v32 offset:9216\n" \
"v_lshl_or_b32 v24, v218, 4, v214\n" \
M_"v[136:139], a[180:183], a[148:151], v[136:139]\n" \
X_"a[164:165], v33 offset:0\n" \
"v_lshl_or_b32 v21, v213, 4, v212\n" \
M_"v[136:139], a[244:247], a[212:215], v[136:139]\n" \
R_"v[208:211], v199 offset:8192\n" \
"s_add_i32 s44, s44, 0x17000\n" \
M_"v[132:135], a[176:179], a[148:151], v[132:135]\n" \
X_"a[166:167], v33 offset:1024\n" \
"s_or_b32 s45, s7, 0x27000\n" \
M_"v[132:135], a[240:243], a[212:215], v[132:135]\n" \
R_"a[148:151], v28 offset:2048\n" \
"s_or_b32 s46, s4, 0x10000\n" \
M_"v[148:151], a[176:179], a[152:155], v[148:151]\n" \
R_"a[212:215], v29 offset:2048\n" \
"s_add_i32 s48, s6, 0x3000000\n" \
M_"v[148:151], a[240:243], a[216:219], v[148:151]\n" \
X_"a[228:229], v33 offset:8192\n" \
"s_add_i32 s49, s5, 0x3004000\n" \
M_"v[152:155], a[180:183], a[152:155], v[152:155]\n" \
X_"a[230:231], v33 offset:9216\n" \
"s_add_i32 s52, s52, 0x1010000\n" \
M_"v[152:155], a[244:247], a[216:219], v[152:155]\n" \
X_"a[168:169], v34 offset:0\n" \
M_"v[156:159], a[184:187], a[152:155], v[156:159]\n" \
X_"a[170:171], v34 offset:1024\n" \
M_"v[156:159], a[248:251], a[216:219], v[156:159]\n" \
X_"a[232:233], v34 offset:8192\n" \
M_"v[160:163], a[188:191], a[152:155], v[160:163]\n" \
X_"a[234:235], v34 offset:9216\n" \
M_"v[160:163], a[252:255], a[216:219], v[160:163]\n" \
R_"a[152:155], v28 offset:4096\n" \
M_"v[176:179], a[188:191], a[156:159], v[176:179]\n" \
R_"a[216:219], v29 offset:4096\n" \
M_"v[176:179], a[252:255], a[220:223], v[176:179]\n" \
X_"a[172:173], v35 offset:0\n" \
M_"v[172:175], a[184:187], a[156:159], v[172:175]\n" \
X_"a[174:175], v35 offset:1024\n" \
M_"v[172:175], a[248:251], a[220:223], v[172:175]\n" \
X_"a[236:237], v35 offset:8192\n" \
M_"v[168:171], a[180:183], a[156:159], v[168:171]\n" \
X_"a[238:239], v35 offset:9216\n" \
M_"v[168:171], a[244:247], a[220:223], v[168:171]\n" \
M_"v[164:167], a[176:179], a[156:159], v[164:167]\n" \
M_"v[164:167], a[240:243], a[220:223], v[164:167]\n" \
R_"a[156:159], v28 offset:6144\n" \
R_"a[220:223], v29 offset:6144\n" \
"s_waitcnt lgkmcnt(15)\n" \
M_"a[0:3], a[160:163], a[128:131], a[0:3]\n" \
"s_addk_i32 s84, 0x7c00\n" \
M_"a[0:3], a[224:227], a[192:195], a[0:3]\n" \
B_"v27, s[72:75], s84 offen offset:2048 lds\n" \
M_"a[4:7], a[164:167], a[128:131], a[4:7]\n" \
X_"a[176:177], v36 offset:0\n" \
"s_waitcnt lgkmcnt(13)\n" \
".fill (L_pb + 12000 - .) / 4, 4, 0xBF800000\n" \
"L_pp3:\n" \
"s_cbranch_execz L_pret\n" \
M_"a[4:7], a[228:231], a[192:195], a[4:7]\n" \
"s_addk_i32 s84, 0x7c00\n" \
"s_waitcnt lgkmcnt(11)\n" \
M_"a[8:11], a[168:171], a[128:131], a[8:11]\n" \
B_"v27, s[72:75], s84 offen offset:3072 lds\n" \
"s_waitcnt lgkmcnt(9)\n" \
M_"a[8:11], a[232:235], a[192:195], a[8:11]\n" \
X_"a[178:179], v36 offset:1024\n" \
"s_waitcnt lgkmcnt(6)\n" \
M_"a[12:15], a[172:175], a[128:131], a[12:15]\n" \
X_"a[240:241], v36 offset:8192\n" \
"s_waitcnt lgkmcnt(5)\n" \
M_"a[12:15], a[236:239], a[192:195], a[12:15]\n" \
M_"a[28:31], a[172:175], a[132:135], a[28:31]\n" \
M_"a[28:31], a[236:239], a[196:199], a[28:31]\n" \
X_"a[242:243], v36 offset:9216\n" \
M_"a[24:27], a[168:171], a[132:135], a[24:27]\n" \
M_"a[24:27], a[232:235], a[196:199], a[24:27]\n" \
M_"a[20:23], a[164:167], a[132:135], a[20:23]\n" \
M_"a[20:23], a[228:231], a[196:199], a[20:23]\n" \
X_"a[180:181], v37 offset:0\n" \
M_"a[16:19], a[160:163], a[132:135], a[16:19]\n" \
M_"a[16:19], a[224:227], a[196:199], a[16:19]\n" \
M_"a[32:35], a[160:163], a[136:139], a[32:35]\n" \
X_"a[182:183], v37 offset:1024\n" \
M_"a[32:35], a[224:227], a[200:203], a[32:35]\n" \
M_"a[36:39], a[164:167], a[136:139], a[36:39]\n" \
M_"a[36:39], a[228:231], a[200:203], a[36:39]\n" \
"buffer_store_dwordx4 v[200:203], v20, s[80:83], s35 offen sc1\n" \
M_"a[40:43], a[168:171], a[136:139], a[40:43]\n" \
"s_mov_b32 m0, s32\n" \
X_"a[244:245], v37 offset:8192\n" \
M_"a[40:43], a[232:235], a[200:203], a[40:43]\n" \
"s_mov_b32 exec_lo, 0x0\n" \
"s_mov_b32 exec_hi, 0xffffffff\n" \
"global_load_lds_dword v[188:189], off\n" \
"s_mov_b64 exec, -1\n" \
M_"a[44:47], a[172:175], a[136:139], a[44:47]\n" \
X_"a[246:247], v37 offset:9216\n" \
M_"a[44:47], a[236:239], a[200:203], a[44:47]\n" \
M_"a[60:63], a[172:175], a[140:143], a[60:63]\n" \
M_"a[60:63], a[236:239], a[204:207], a[60:63]\n" \
M_"a[56:59], a[168:171], a[140:143], a[56:59]\n" \
X_"a[184:185], v38 offset:0\n" \
M_"a[56:59], a[232:235], a[204:207], a[56:59]\n" \
M_"a[52:55], a[164:167], a[140:143], a[52:55]\n" \
"buffer_store_dwordx4 v[204:207], v20, s[80:83], s88 offen sc1\n" \
M_"a[52:55], a[228:231], a[204:207], a[52:55]\n" \
X_"a[186:187], v38 offset:1024\n" \
M_"a[48:51], a[160:163], a[140:143], a[48:51]\n" \
M_"a[48:51], a[224:227], a[204:207], a[48:51]\n" \
M_"a[64:67], a[160:163], a[144:147], a[64:67]\n" \
X_"a[248:249], v38 offset:8192\n" \
M_"a[64:67], a[224:227], a[208:211], a[64:67]\n" \
M_"a[68:71], a[164:167], a[144:147], a[68:71]\n" \
M_"a[68:71], a[228:231], a[208:211], a[68:71]\n" \
X_"a[250:251], v38 offset:9216\n" \
M_"a[72:75], a[168:171], a[144:147], a[72:75]\n" \
M_"a[72:75], a[232:235], a[208:211], a[72:75]\n" \
M_"a[76:79], a[172:175], a[144:147], a[76:79]\n" \
"buffer_store_dwordx4 v[208:211], v20, s[80:83], s36 offen sc1\n" \
M_"a[76:79], a[236:239], a[208:211], a[76:79]\n" \
X_"a[188:189], v39 offset:0\n" \
M_"a[92:95], a[172:175], a[148:151], a[92:95]\n" \
M_"a[92:95], a[236:239], a[212:215], a[92:95]\n" \
M_"a[88:91], a[168:171], a[148:151], a[88:91]\n" \
X_"a[190:191], v39 offset:1024\n" \
M_"a[88:91], a[232:235], a[212:215], a[88:91]\n" \
M_"a[84:87], a[164:167], a[148:151], a[84:87]\n" \
M_"a[84:87], a[228:231], a[212:215], a[84:87]\n" \
X_"a[252:253], v39 offset:8192\n" \
M_"a[80:83], a[160:163], a[148:151], a[80:83]\n" \
M_"a[80:83], a[224:227], a[212:215], a[80:83]\n" \
M_"a[96:99], a[160:163], a[152:155], a[96:99]\n" \
X_"a[254:255], v39 offset:9216\n" \
M_"a[96:99], a[224:227], a[216:219], a[96:99]\n" \
M_"a[100:103], a[164:167], a[152:155], a[100:103]\n" \
M_"a[100:103], a[228:231], a[216:219], a[100:103]\n" \
M_"a[104:107], a[168:171], a[152:155], a[104:107]\n" \
M_"a[104:107], a[232:235], a[216:219], a[104:107]\n" \
M_"a[108:111], a[172:175], a[152:155], a[108:111]\n" \
M_"a[108:111], a[236:239], a[216:219], a[108:111]\n" \
"s_waitcnt lgkmcnt(15)\n" \
M_"a[124:127], a[172:175], a[156:159], a[124:127]\n" \
M_"a[124:127], a[236:239], a[220:223], a[124:127]\n" \
M_"a[120:123], a[168:171], a[156:159], a[120:123]\n" \
M_"a[120:123], a[232:235], a[220:223], a[120:123]\n" \
M_"a[116:119], a[164:167], a[156:159], a[116:119]\n" \
M_"a[116:119], a[228:231], a[220:223], a[116:119]\n" \
M_"a[112:115], a[160:163], a[156:159], a[112:115]\n" \
"s_waitcnt vmcnt(4) lgkmcnt(0)\n" \
M_"a[112:115], a[224:227], a[220:223], a[112:115]\n" \
"s_barrier\n" \
M_"v[52:55], a[176:179], a[128:131], v[52:55]\n" \
"s_add_u32 m0, s30, 0x0\n" \
"s_movk_i32 s84, 0x300\n" \
M_"v[52:55], a[240:243], a[192:195], v[52:55]\n" \
B_"v22, s[64:67], s84 offen offset:0 lds\n" \
M_"v[56:59], a[180:183], a[128:131], v[56:59]\n" \
X_"a[160:161], v32 offset:32768\n" \
M_"v[56:59], a[244:247], a[192:195], v[56:59]\n" \
"s_movk_i32 s84, 0x1f00\n" \
M_"v[60:63], a[184:187], a[128:131], v[60:63]\n" \
B_"v23, s[64:67], s84 offen offset:1024 lds\n" \
M_"v[60:63], a[248:251], a[192:195], v[60:63]\n" \
X_"a[162:163], v32 offset:33792\n" \
M_"v[64:67], a[188:191], a[128:131], v[64:67]\n" \
X_"a[224:225], v32 offset:40960\n" \
M_"v[64:67], a[252:255], a[192:195], v[64:67]\n" \
R_"a[128:131], v28 offset:49152\n" \
M_"v[80:83], a[188:191], a[132:135], v[80:83]\n" \
R_"a[192:195], v29 offset:49152\n" \
M_"v[80:83], a[252:255], a[196:199], v[80:83]\n" \
"s_movk_i32 s84, 0x3b00\n" \
M_"v[76:79], a[184:187], a[132:135], v[76:79]\n" \
B_"v22, s[64:67], s84 offen offset:2048 lds\n" \
M_"v[76:79], a[248:251], a[196:199], v[76:79]\n" \
"s_movk_i32 s84, 0x5700\n" \
M_"v[72:75], a[180:183], a[132:135], v[72:75]\n" \
B_"v23, s[64:67], s84 offen offset:3072 lds\n" \
M_"v[72:75], a[244:247], a[196:199], v[72:75]\n" \
"s_add_u32 m0, s31, 0x0\n" \
"s_mov_b32 s84, 0x300000\n" \
M_"v[68:71], a[176:179], a[132:135], v[68:71]\n" \
B_"v26, s[68:71], s84 offen offset:0 lds\n" \
M_"v[68:71], a[240:243], a[196:199], v[68:71]\n" \
R_"a[132:135], v28 offset:51200\n" \
M_"v[84:87], a[176:179], a[136:139], v[84:87]\n" \
R_"a[196:199], v29 offset:51200\n" \
M_"v[84:87], a[240:243], a[200:203], v[84:87]\n" \
"s_addk_i32 s84, 0x7c00\n" \
M_"v[88:91], a[180:183], a[136:139], v[88:91]\n" \
B_"v26, s[68:71], s84 offen offset:1024 lds\n" \
M_"v[88:91], a[244:247], a[200:203], v[88:91]\n" \
X_"a[226:227], v32 offset:41984\n" \
M_"v[92:95], a[184:187], a[136:139], v[92:95]\n" \
"s_addk_i32 s84, 0x7c00\n" \
M_"v[92:95], a[248:251], a[200:203], v[92:95]\n" \
B_"v27, s[68:71], s84 offen offset:2048 lds\n" \
M_"v[96:99], a[188:191], a[136:139], v[96:99]\n" \
X_"a[164:165], v33 offset:32768\n" \
M_"v[96:99], a[252:255], a[200:203], v[96:99]\n" \
R_"a[136:139], v28 offset:53248\n" \
M_"v[112:115], a[188:191], a[140:143], v[112:115]\n" \
R_"a[200:203], v29 offset:53248\n" \
M_"v[112:115], a[252:255], a[204:207], v[112:115]\n" \
"s_addk_i32 s84, 0x7c00\n" \
M_"v[108:111], a[184:187], a[140:143], v[108:111]\n" \
B_"v27, s[68:71], s84 offen offset:3072 lds\n" \
M_"v[108:111], a[248:251], a[204:207], v[108:111]\n" \
"s_add_u32 m0, s30, 0x1000\n" \
"s_mov_b32 s84, 0x8300\n" \
M_"v[104:107], a[180:183], a[140:143], v[104:107]\n" \
B_"v22, s[64:67], s84 offen offset:0 lds\n" \
M_"v[104:107], a[244:247], a[204:207], v[104:107]\n" \
X_"a[166:167], v33 offset:33792\n" \
M_"v[100:103], a[176:179], a[140:143], v[100:103]\n" \
X_"a[228:229], v33 offset:40960\n" \
M_"v[100:103], a[240:243], a[204:207], v[100:103]\n" \
R_"a[140:143], v28 offset:55296\n" \
M_"v[116:119], a[176:179], a[144:147], v[116:119]\n" \
R_"a[204:207], v29 offset:55296\n" \
M_"v[116:119], a[240:243], a[208:211], v[116:119]\n" \
"s_addk_i32 s84, 0x1c00\n" \
M_"v[120:123], a[180:183], a[144:147], v[120:123]\n" \
B_"v23, s[64:67], s84 offen offset:1024 lds\n" \
M_"v[120:123], a[244:247], a[208:211], v[120:123]\n" \
"s_addk_i32 s84, 0x1c00\n" \
M_"v[124:127], a[184:187], a[144:147], v[124:127]\n" \
B_"v22, s[64:67], s84 offen offset:2048 lds\n" \
M_"v[124:127], a[248:251], a[208:211], v[124:127]\n" \
"s_addk_i32 s84, 0x1c00\n" \
"s_nop 0\n" \
M_"v[128:131], a[188:191], a[144:147], v[128:131]\n" \
B_"v23, s[64:67], s84 offen offset:3072 lds\n" \
M_"v[128:131], a[252:255], a[208:211], v[128:131]\n" \
R_"a[144:147], v28 offset:32768\n" \
M_"v[144:147], a[188:191], a[148:151], v[144:147]\n" \
R_"a[208:211], v29 offset:32768\n" \
M_"v[144:147], a[252:255], a[212:215], v[144:147]\n" \
"s_add_u32 m0, s31, 0x4000\n" \
"s_mov_b32 s84, 0x300000\n" \
M_"v[140:143], a[184:187], a[148:151], v[140:143]\n" \
B_"v26, s[72:75], s84 offen offset:0 lds\n" \
M_"v[140:143], a[248:251], a[212:215], v[140:143]\n" \
X_"a[230:231], v33 offset:41984\n" \
M_"v[136:139], a[180:183], a[148:151], v[136:139]\n" \
"s_addk_i32 s84, 0x7c00\n" \
"s_nop 0\n" \
M_"v[136:139], a[244:247], a[212:215], v[136:139]\n" \
B_"v26, s[72:75], s84 offen offset:1024 lds\n" \
M_"v[132:135], a[176:179], a[148:151], v[132:135]\n" \
X_"a[168:169], v34 offset:32768\n" \
M_"v[132:135], a[240:243], a[212:215], v[132:135]\n" \
R_"a[148:151], v28 offset:34816\n" \
M_"v[148:151], a[176:179], a[152:155], v[148:151]\n" \
R_"a[212:215], v29 offset:34816\n" \
M_"v[148:151], a[240:243], a[216:219], v[148:151]\n" \
X_"a[170:171], v34 offset:33792\n" \
M_"v[152:155], a[180:183], a[152:155], v[152:155]\n" \
X_"a[232:233], v34 offset:40960\n" \
M_"v[152:155], a[244:247], a[216:219], v[152:155]\n" \
X_"a[234:235], v34 offset:41984\n" \
M_"v[156:159], a[184:187], a[152:155], v[156:159]\n" \
X_"a[172:173], v35 offset:32768\n" \
M_"v[156:159], a[248:251], a[216:219], v[156:159]\n" \
X_"a[174:175], v35 offset:33792\n" \
M_"v[160:163], a[188:191], a[152:155], v[160:163]\n" \
X_"a[236:237], v35 offset:40960\n" \
M_"v[160:163], a[252:255], a[216:219], v[160:163]\n" \
R_"a[152:155], v28 offset:36864\n" \
M_"v[176:179], a[188:191], a[156:159], v[176:179]\n" \
R_"a[216:219], v29 offset:36864\n" \
M_"v[176:179], a[252:255], a[220:223], v[176:179]\n" \
X_"a[238:239], v35 offset:41984\n" \
M_"v[172:175], a[184:187], a[156:159], v[172:175]\n" \
M_"v[172:175], a[248:251], a[220:223], v[172:175]\n" \
M_"v[168:171], a[180:183], a[156:159], v[168:171]\n" \
M_"v[168:171], a[244:247], a[220:223], v[168:171]\n" \
M_"v[164:167], a[176:179], a[156:159], v[164:167]\n" \
M_"v[164:167], a[240:243], a[220:223], v[164:167]\n" \
R_"a[156:159], v28 offset:38912\n" \
R_"a[220:223], v29 offset:38912\n" \
"s_waitcnt lgkmcnt(15)\n" \
M_"a[0:3], a[160:163], a[128:131], a[0:3]\n" \
"s_addk_i32 s84, 0x7c00\n" \
M_"a[0:3], a[224:227], a[192:195], a[0:3]\n" \
B_"v27, s[72:75], s84 offen offset:2048 lds\n" \
M_"a[4:7], a[164:167], a[128:131], a[4:7]\n" \
X_"a[176:177], v36 offset:32768\n" \
"s_waitcnt lgkmcnt(15)\n" \
M_"a[4:7], a[228:231], a[192:195], a[4:7]\n" \
"s_addk_i32 s84, 0x7c00\n" \
"s_waitcnt lgkmcnt(11)\n" \
M_"a[8:11], a[168:171], a[128:131], a[8:11]\n" \
B_"v27, s[72:75], s84 offen offset:3072 lds\n" \
"s_waitcnt lgkmcnt(9)\n" \
M_"a[8:11], a[232:235], a[192:195], a[8:11]\n" \
X_"a[178:179], v36 offset:33792\n" \
"s_waitcnt lgkmcnt(8)\n" \
M_"a[12:15], a[172:175], a[128:131], a[12:15]\n" \
X_"a[240:241], v36 offset:40960\n" \
"s_waitcnt lgkmcnt(5)\n" \
M_"a[12:15], a[236:239], a[192:195], a[12:15]\n" \
M_"a[28:31], a[172:175], a[132:135], a[28:31]\n" \
M_"a[28:31], a[236:239], a[196:199], a[28:31]\n" \
X_"a[242:243], v36 offset:41984\n" \
M_"a[24:27], a[168:171], a[132:135], a[24:27]\n" \
M_"a[24:27], a[232:235], a[196:199], a[24:27]\n" \
M_"a[20:23], a[164:167], a[132:135], a[20:23]\n" \
M_"a[20:23], a[228:231], a[196:199], a[20:23]\n" \
X_"a[180:181], v37 offset:32768\n" \
M_"a[16:19], a[160:163], a[132:135], a[16:19]\n" \
M_"a[16:19], a[224:227], a[196:199], a[16:19]\n" \
M_"a[32:35], a[160:163], a[136:139], a[32:35]\n" \
X_"a[182:183], v37 offset:33792\n" \
M_"a[32:35], a[224:227], a[200:203], a[32:35]\n" \
M_"a[36:39], a[164:167], a[136:139], a[36:39]\n" \
"v_lshlrev_b32_e32 v45, 3, v20\n" \
"v_add_u32_e32 v45, s31, v45\n" \
M_"a[36:39], a[228:231], a[200:203], a[36:39]\n" \
"s_getpc_b64 s[86:87]\n" \
"s_sub_u32 s86, s86, 0xf800\n" \
"s_subb_u32 s87, s87, 0\n" \
"s_mov_b32 m0, s32\n" \
"s_nop 0\n" \
M_"a[40:43], a[168:171], a[136:139], a[40:43]\n" \
"global_load_lds_dword v45, s[86:87]\n" \
M_"a[40:43], a[232:235], a[200:203], a[40:43]\n" \
X_"a[244:245], v37 offset:40960\n" \
M_"a[44:47], a[172:175], a[136:139], a[44:47]\n" \
X_"a[246:247], v37 offset:41984\n" \
M_"a[44:47], a[236:239], a[200:203], a[44:47]\n" \
M_"a[60:63], a[172:175], a[140:143], a[60:63]\n" \
M_"a[60:63], a[236:239], a[204:207], a[60:63]\n" \
M_"a[56:59], a[168:171], a[140:143], a[56:59]\n" \
X_"a[184:185], v38 offset:32768\n" \
M_"a[56:59], a[232:235], a[204:207], a[56:59]\n" \
M_"a[52:55], a[164:167], a[140:143], a[52:55]\n" \
M_"a[52:55], a[228:231], a[204:207], a[52:55]\n" \
X_"a[186:187], v38 offset:33792\n" \
M_"a[48:51], a[160:163], a[140:143], a[48:51]\n" \
M_"a[48:51], a[224:227], a[204:207], a[48:51]\n" \
M_"a[64:67], a[160:163], a[144:147], a[64:67]\n" \
X_"a[248:249], v38 offset:40960\n" \
M_"a[64:67], a[224:227], a[208:211], a[64:67]\n" \
M_"a[68:71], a[164:167], a[144:147], a[68:71]\n" \
M_"a[68:71], a[228:231], a[208:211], a[68:71]\n" \
X_"a[250:251], v38 offset:41984\n" \
M_"a[72:75], a[168:171], a[144:147], a[72:75]\n" \
M_"a[72:75], a[232:235], a[208:211], a[72:75]\n" \
M_"a[76:79], a[172:175], a[144:147], a[76:79]\n" \
M_"a[76:79], a[236:239], a[208:211], a[76:79]\n" \
X_"a[188:189], v39 offset:32768\n" \
M_"a[92:95], a[172:175], a[148:151], a[92:95]\n" \
M_"a[92:95], a[236:239], a[212:215], a[92:95]\n" \
M_"a[88:91], a[168:171], a[148:151], a[88:91]\n" \
X_"a[190:191], v39 offset:33792\n" \
M_"a[88:91], a[232:235], a[212:215], a[88:91]\n" \
M_"a[84:87], a[164:167], a[148:151], a[84:87]\n" \
M_"a[84:87], a[228:231], a[212:215], a[84:87]\n" \
X_"a[252:253], v39 offset:40960\n" \
M_"a[80:83], a[160:163], a[148:151], a[80:83]\n" \
M_"a[80:83], a[224:227], a[212:215], a[80:83]\n" \
M_"a[96:99], a[160:163], a[152:155], a[96:99]\n" \
X_"a[254:255], v39 offset:41984\n" \
M_"a[96:99], a[224:227], a[216:219], a[96:99]\n" \
M_"a[100:103], a[164:167], a[152:155], a[100:103]\n" \
M_"a[100:103], a[228:231], a[216:219], a[100:103]\n" \
M_"a[104:107], a[168:171], a[152:155], a[104:107]\n" \
M_"a[104:107], a[232:235], a[216:219], a[104:107]\n" \
M_"a[108:111], a[172:175], a[152:155], a[108:111]\n" \
M_"a[108:111], a[236:239], a[216:219], a[108:111]\n" \
"s_waitcnt lgkmcnt(15)\n" \
M_"a[124:127], a[172:175], a[156:159], a[124:127]\n" \
M_"a[124:127], a[236:239], a[220:223], a[124:127]\n" \
M_"a[120:123], a[168:171], a[156:159], a[120:123]\n" \
M_"a[120:123], a[232:235], a[220:223], a[120:123]\n" \
M_"a[116:119], a[164:167], a[156:159], a[116:119]\n" \
M_"a[116:119], a[228:231], a[220:223], a[116:119]\n" \
M_"a[112:115], a[160:163], a[156:159], a[112:115]\n" \
"s_waitcnt vmcnt(1) lgkmcnt(0)\n" \
M_"a[112:115], a[224:227], a[220:223], a[112:115]\n" \
"s_barrier\n" \
"v_mov_b32_e32 v45, s62\n" \
"s_mov_b64 exec, 1\n" \
"buffer_store_dword v45, off, s[80:83], s37 offset:128 sc1\n" \
"s_mov_b64 exec, -1\n" \
M_"v[52:55], a[176:179], a[128:131], v[52:55]\n" \
"s_add_u32 m0, s30, 0x8000\n" \
"s_movk_i32 s84, 0x380\n" \
M_"v[52:55], a[240:243], a[192:195], v[52:55]\n" \
B_"v22, s[64:67], s84 offen offset:0 lds\n" \
M_"v[56:59], a[180:183], a[128:131], v[56:59]\n" \
X_"a[160:161], v32 offset:0\n" \
M_"v[56:59], a[244:247], a[192:195], v[56:59]\n" \
"s_movk_i32 s84, 0x1f80\n" \
M_"v[60:63], a[184:187], a[128:131], v[60:63]\n" \
B_"v23, s[64:67], s84 offen offset:1024 lds\n" \
M_"v[60:63], a[248:251], a[192:195], v[60:63]\n" \
X_"a[162:163], v32 offset:1024\n" \
M_"v[64:67], a[188:191], a[128:131], v[64:67]\n" \
X_"a[224:225], v32 offset:8192\n" \
M_"v[64:67], a[252:255], a[192:195], v[64:67]\n" \
R_"a[128:131], v28 offset:16384\n" \
M_"v[80:83], a[188:191], a[132:135], v[80:83]\n" \
R_"a[192:195], v29 offset:16384\n" \
M_"v[80:83], a[252:255], a[196:199], v[80:83]\n" \
"s_movk_i32 s84, 0x3b80\n" \
M_"v[76:79], a[184:187], a[132:135], v[76:79]\n" \
B_"v22, s[64:67], s84 offen offset:2048 lds\n" \
M_"v[76:79], a[248:251], a[196:199], v[76:79]\n" \
"s_movk_i32 s84, 0x5780\n" \
M_"v[72:75], a[180:183], a[132:135], v[72:75]\n" \
B_"v23, s[64:67], s84 offen offset:3072 lds\n" \
M_"v[72:75], a[244:247], a[196:199], v[72:75]\n" \
"s_add_u32 m0, s31, 0x8000\n" \
"s_mov_b32 s84, 0x380000\n" \
M_"v[68:71], a[176:179], a[132:135], v[68:71]\n" \
B_"v26, s[68:71], s84 offen offset:0 lds\n" \
M_"v[68:71], a[240:243], a[196:199], v[68:71]\n" \
R_"a[132:135], v28 offset:18432\n" \
M_"v[84:87], a[176:179], a[136:139], v[84:87]\n" \
R_"a[196:199], v29 offset:18432\n" \
M_"v[84:87], a[240:243], a[200:203], v[84:87]\n" \
"s_addk_i32 s84, 0x7c00\n" \
M_"v[88:91], a[180:183], a[136:139], v[88:91]\n" \
B_"v26, s[68:71], s84 offen offset:1024 lds\n" \
M_"v[88:91], a[244:247], a[200:203], v[88:91]\n" \
X_"a[226:227], v32 offset:9216\n" \
M_"v[92:95], a[184:187], a[136:139], v[92:95]\n" \
"s_addk_i32 s84, 0x7c00\n" \
M_"v[92:95], a[248:251], a[200:203], v[92:95]\n" \
B_"v27, s[68:71], s84 offen offset:2048 lds\n" \
M_"v[96:99], a[188:191], a[136:139], v[96:99]\n" \
X_"a[164:165], v33 offset:0\n" \
M_"v[96:99], a[252:255], a[200:203], v[96:99]\n" \
R_"a[136:139], v28 offset:20480\n" \
M_"v[112:115], a[188:191], a[140:143], v[112:115]\n" \
R_"a[200:203], v29 offset:20480\n" \
M_"v[112:115], a[252:255], a[204:207], v[112:115]\n" \
"s_addk_i32 s84, 0x7c00\n" \
"s_nop 0\n" \
M_"v[108:111], a[184:187], a[140:143], v[108:111]\n" \
B_"v27, s[68:71], s84 offen offset:3072 lds\n" \
M_"v[108:111], a[248:251], a[204:207], v[108:111]\n" \
"s_add_u32 m0, s30, 0x9000\n" \
"s_mov_b32 s84, 0x8380\n" \
M_"v[104:107], a[180:183], a[140:143], v[104:107]\n" \
B_"v22, s[64:67], s84 offen offset:0 lds\n" \
M_"v[104:107], a[244:247], a[204:207], v[104:107]\n" \
X_"a[166:167], v33 offset:1024\n" \
M_"v[100:103], a[176:179], a[140:143], v[100:103]\n" \
X_"a[228:229], v33 offset:8192\n" \
M_"v[100:103], a[240:243], a[204:207], v[100:103]\n" \
R_"a[140:143], v28 offset:22528\n" \
M_"v[116:119], a[176:179], a[144:147], v[116:119]\n" \
R_"a[204:207], v29 offset:22528\n" \
M_"v[116:119], a[240:243], a[208:211], v[116:119]\n" \
"s_addk_i32 s84, 0x1c00\n" \
M_"v[120:123], a[180:183], a[144:147], v[120:123]\n" \
B_"v23, s[64:67], s84 offen offset:1024 lds\n" \
M_"v[120:123], a[244:247], a[208:211], v[120:123]\n" \
"s_addk_i32 s84, 0x1c00\n" \
M_"v[124:127], a[184:187], a[144:147], v[124:127]\n" \
B_"v22, s[64:67], s84 offen offset:2048 lds\n" \
M_"v[124:127], a[248:251], a[208:211], v[124:127]\n" \
"s_addk_i32 s84, 0x1c00\n" \
"s_nop 0\n" \
M_"v[128:131], a[188:191], a[144:147], v[128:131]\n" \
B_"v23, s[64:67], s84 offen offset:3072 lds\n" \
M_"v[128:131], a[252:255], a[208:211], v[128:131]\n" \
R_"a[144:147], v28 offset:0\n" \
M_"v[144:147], a[188:191], a[148:151], v[144:147]\n" \
R_"a[208:211], v29 offset:0\n" \
M_"v[144:147], a[252:255], a[212:215], v[144:147]\n" \
"s_add_u32 m0, s31, 0xc000\n" \
"s_mov_b32 s84, 0x380000\n" \
M_"v[140:143], a[184:187], a[148:151], v[140:143]\n" \
B_"v26, s[72:75], s84 offen offset:0 lds\n" \
M_"v[140:143], a[248:251], a[212:215], v[140:143]\n" \
X_"a[230:231], v33 offset:9216\n" \
M_"v[136:139], a[180:183], a[148:151], v[136:139]\n" \
"s_addk_i32 s84, 0x7c00\n" \
"s_nop 0\n" \
M_"v[136:139], a[244:247], a[212:215], v[136:139]\n" \
B_"v26, s[72:75], s84 offen offset:1024 lds\n" \
M_"v[132:135], a[176:179], a[148:151], v[132:135]\n" \
X_"a[168:169], v34 offset:0\n" \
M_"v[132:135], a[240:243], a[212:215], v[132:135]\n" \
R_"a[148:151], v28 offset:2048\n" \
M_"v[148:151], a[176:179], a[152:155], v[148:151]\n" \
R_"a[212:215], v29 offset:2048\n" \
M_"v[148:151], a[240:243], a[216:219], v[148:151]\n" \
X_"a[170:171], v34 offset:1024\n" \
M_"v[152:155], a[180:183], a[152:155], v[152:155]\n" \
X_"a[232:233], v34 offset:8192\n" \
M_"v[152:155], a[244:247], a[216:219], v[152:155]\n" \
X_"a[234:235], v34 offset:9216\n" \
M_"v[156:159], a[184:187], a[152:155], v[156:159]\n" \
X_"a[172:173], v35 offset:0\n" \
M_"v[156:159], a[248:251], a[216:219], v[156:159]\n" \
X_"a[174:175], v35 offset:1024\n" \
M_"v[160:163], a[188:191], a[152:155], v[160:163]\n" \
X_"a[236:237], v35 offset:8192\n" \
M_"v[160:163], a[252:255], a[216:219], v[160:163]\n" \
R_"a[152:155], v28 offset:4096\n" \
M_"v[176:179], a[188:191], a[156:159], v[176:179]\n" \
R_"a[216:219], v29 offset:4096\n" \
M_"v[176:179], a[252:255], a[220:223], v[176:179]\n" \
X_"a[238:239], v35 offset:9216\n" \
M_"v[172:175], a[184:187], a[156:159], v[172:175]\n" \
M_"v[172:175], a[248:251], a[220:223], v[172:175]\n" \
M_"v[168:171], a[180:183], a[156:159], v[168:171]\n" \
M_"v[168:171], a[244:247], a[220:223], v[168:171]\n" \
M_"v[164:167], a[176:179], a[156:159], v[164:167]\n" \
M_"v[164:167], a[240:243], a[220:223], v[164:167]\n" \
R_"a[156:159], v28 offset:6144\n" \
R_"a[220:223], v29 offset:6144\n" \
"s_waitcnt lgkmcnt(15)\n" \
M_"a[0:3], a[160:163], a[128:131], a[0:3]\n" \
"s_addk_i32 s84, 0x7c00\n" \
M_"a[0:3], a[224:227], a[192:195], a[0:3]\n" \
B_"v27, s[72:75], s84 offen offset:2048 lds\n" \
M_"a[4:7], a[164:167], a[128:131], a[4:7]\n" \
X_"a[176:177], v36 offset:0\n" \
"s_waitcnt lgkmcnt(15)\n" \
M_"a[4:7], a[228:231], a[192:195], a[4:7]\n" \
"s_addk_i32 s84, 0x7c00\n" \
"s_waitcnt lgkmcnt(11)\n" \
M_"a[8:11], a[168:171], a[128:131], a[8:11]\n" \
B_"v27, s[72:75], s84 offen offset:3072 lds\n" \
"s_waitcnt lgkmcnt(9)\n" \
M_"a[8:11], a[232:235], a[192:195], a[8:11]\n" \
X_"a[178:179], v36 offset:1024\n" \
"s_waitcnt lgkmcnt(8)\n" \
M_"a[12:15], a[172:175], a[128:131], a[12:15]\n" \
X_"a[240:241], v36 offset:8192\n" \
"s_waitcnt lgkmcnt(5)\n" \
M_"a[12:15], a[236:239], a[192:195], a[12:15]\n" \
M_"a[28:31], a[172:175], a[132:135], a[28:31]\n" \
M_"a[28:31], a[236:239], a[196:199], a[28:31]\n" \
X_"a[242:243], v36 offset:9216\n" \
M_"a[24:27], a[168:171], a[132:135], a[24:27]\n" \
M_"a[24:27], a[232:235], a[196:199], a[24:27]\n" \
M_"a[20:23], a[164:167], a[132:135], a[20:23]\n" \
M_"a[20:23], a[228:231], a[196:199], a[20:23]\n" \
X_"a[180:181], v37 offset:0\n" \
M_"a[16:19], a[160:163], a[132:135], a[16:19]\n" \
M_"a[16:19], a[224:227], a[196:199], a[16:19]\n" \
M_"a[32:35], a[160:163], a[136:139], a[32:35]\n" \
"v_bfe_u32 v44, v20, 2, 5\n" \
X_"a[182:183], v37 offset:1024\n" \
M_"a[32:35], a[224:227], a[200:203], a[32:35]\n" \
"buffer_load_dword v44, v44, s[80:83], s38 offen offset:128 sc1\n" \
M_"a[36:39], a[164:167], a[136:139], a[36:39]\n" \
M_"a[36:39], a[228:231], a[200:203], a[36:39]\n" \
X_"a[244:245], v37 offset:8192\n" \
M_"a[40:43], a[168:171], a[136:139], a[40:43]\n" \
M_"a[40:43], a[232:235], a[200:203], a[40:43]\n" \
M_"a[44:47], a[172:175], a[136:139], a[44:47]\n" \
X_"a[246:247], v37 offset:9216\n" \
M_"a[44:47], a[236:239], a[200:203], a[44:47]\n" \
M_"a[60:63], a[172:175], a[140:143], a[60:63]\n" \
"global_store_dwordx4 v49, v[0:3], s[28:29] offset:0 sc1\n" \
M_"a[60:63], a[236:239], a[204:207], a[60:63]\n" \
M_"a[56:59], a[168:171], a[140:143], a[56:59]\n" \
X_"a[184:185], v38 offset:0\n" \
M_"a[56:59], a[232:235], a[204:207], a[56:59]\n" \
M_"a[52:55], a[164:167], a[140:143], a[52:55]\n" \
M_"a[52:55], a[228:231], a[204:207], a[52:55]\n" \
X_"a[186:187], v38 offset:1024\n" \
M_"a[48:51], a[160:163], a[140:143], a[48:51]\n" \
M_"a[48:51], a[224:227], a[204:207], a[48:51]\n" \
M_"a[64:67], a[160:163], a[144:147], a[64:67]\n" \
"global_store_dwordx4 v49, v[0:3], s[28:29] offset:1024 sc1\n" \
M_"a[64:67], a[224:227], a[208:211], a[64:67]\n" \
X_"a[248:249], v38 offset:8192\n" \
M_"a[68:71], a[164:167], a[144:147], a[68:71]\n" \
M_"a[68:71], a[228:231], a[208:211], a[68:71]\n" \
X_"a[250:251], v38 offset:9216\n" \
M_"a[72:75], a[168:171], a[144:147], a[72:75]\n" \
M_"a[72:75], a[232:235], a[208:211], a[72:75]\n" \
M_"a[76:79], a[172:175], a[144:147], a[76:79]\n" \
M_"a[76:79], a[236:239], a[208:211], a[76:79]\n" \
X_"a[188:189], v39 offset:0\n" \
M_"a[92:95], a[172:175], a[148:151], a[92:95]\n" \
"s_mov_b64 exec, 0xffff\n" \
"global_store_dwordx4 v49, v[0:3], s[28:29] offset:2048 sc1\n" \
"s_mov_b64 exec, -1\n" \
"s_nop 0\n" \
M_"a[92:95], a[236:239], a[212:215], a[92:95]\n" \
M_"a[88:91], a[168:171], a[148:151], a[88:91]\n" \
X_"a[190:191], v39 offset:1024\n" \
M_"a[88:91], a[232:235], a[212:215], a[88:91]\n" \
M_"a[84:87], a[164:167], a[148:151], a[84:87]\n" \
M_"a[84:87], a[228:231], a[212:215], a[84:87]\n" \
X_"a[252:253], v39 offset:8192\n" \
M_"a[80:83], a[160:163], a[148:151], a[80:83]\n" \
M_"a[80:83], a[224:227], a[212:215], a[80:83]\n" \
M_"a[96:99], a[160:163], a[152:155], a[96:99]\n" \
X_"a[254:255], v39 offset:9216\n" \
M_"a[96:99], a[224:227], a[216:219], a[96:99]\n" \
M_"a[100:103], a[164:167], a[152:155], a[100:103]\n" \
M_"a[100:103], a[228:231], a[216:219], a[100:103]\n" \
M_"a[104:107], a[168:171], a[152:155], a[104:107]\n" \
M_"a[104:107], a[232:235], a[216:219], a[104:107]\n" \
".fill (L_pb + 16096 - .) / 4, 4, 0xBF800000\n" \
"L_pp4:\n" \
"s_cbranch_execz L_pret\n" \
M_"a[108:111], a[172:175], a[152:155], a[108:111]\n" \
M_"a[108:111], a[236:239], a[216:219], a[108:111]\n" \
"s_waitcnt lgkmcnt(15)\n" \
M_"a[124:127], a[172:175], a[156:159], a[124:127]\n" \
M_"a[124:127], a[236:239], a[220:223], a[124:127]\n" \
M_"a[120:123], a[168:171], a[156:159], a[120:123]\n" \
M_"a[120:123], a[232:235], a[220:223], a[120:123]\n" \
M_"a[116:119], a[164:167], a[156:159], a[116:119]\n" \
M_"a[116:119], a[228:231], a[220:223], a[116:119]\n" \
M_"a[112:115], a[160:163], a[156:159], a[112:115]\n" \
"s_waitcnt vmcnt(3) lgkmcnt(0)\n" \
M_"a[112:115], a[224:227], a[220:223], a[112:115]\n" \
"s_barrier\n" \
"v_cmp_ne_u32_e32 vcc, s62, v44\n" \
"s_nop 4\n" \
"s_cbranch_vccnz L_poll60_%=\n" \
"L_pollr60_%=:\n" \
"s_mov_b32 s68, s12\n" \
"s_and_b32 s69, s13, 0xffff\n" \
"s_mov_b32 s70, 0x480000\n" \
"s_mov_b32 s71, 0x20000\n" \
M_"v[52:55], a[176:179], a[128:131], v[52:55]\n" \
"s_add_u32 m0, s40, 0x0\n" \
"s_add_u32 s85, s39, 0x0\n" \
M_"v[52:55], a[240:243], a[192:195], v[52:55]\n" \
B_"v20, s[80:83], s85 offen sc1 lds\n" \
M_"v[56:59], a[180:183], a[128:131], v[56:59]\n" \
X_"a[160:161], v32 offset:32768\n" \
M_"v[56:59], a[244:247], a[192:195], v[56:59]\n" \
"s_add_u32 m0, s40, 0x1000\n" \
"s_add_u32 s85, s39, 0x1000\n" \
M_"v[60:63], a[184:187], a[128:131], v[60:63]\n" \
B_"v20, s[80:83], s85 offen sc1 lds\n" \
M_"v[60:63], a[248:251], a[192:195], v[60:63]\n" \
X_"a[162:163], v32 offset:33792\n" \
M_"v[64:67], a[188:191], a[128:131], v[64:67]\n" \
X_"a[224:225], v32 offset:40960\n" \
M_"v[64:67], a[252:255], a[192:195], v[64:67]\n" \
R_"a[128:131], v28 offset:49152\n" \
M_"v[80:83], a[188:191], a[132:135], v[80:83]\n" \
R_"a[192:195], v29 offset:49152\n" \
M_"v[80:83], a[252:255], a[196:199], v[80:83]\n" \
"s_add_u32 m0, s40, 0x3000\n" \
"s_movk_i32 s84, 0x0\n" \
M_"v[76:79], a[184:187], a[132:135], v[76:79]\n" \
B_"v21, s[76:79], s84 offen offset:0 lds\n" \
M_"v[76:79], a[248:251], a[196:199], v[76:79]\n" \
"s_add_u32 m0, s30, 0x0\n" \
"s_movk_i32 s84, 0x0\n" \
"s_nop 0\n" \
M_"v[72:75], a[180:183], a[132:135], v[72:75]\n" \
B_"v24, s[76:79], s84 offen offset:0 lds\n" \
M_"v[72:75], a[244:247], a[196:199], v[72:75]\n" \
X_"a[226:227], v32 offset:41984\n" \
M_"v[68:71], a[176:179], a[132:135], v[68:71]\n" \
X_"a[164:165], v33 offset:32768\n" \
M_"v[68:71], a[240:243], a[196:199], v[68:71]\n" \
R_"a[132:135], v28 offset:51200\n" \
M_"v[84:87], a[176:179], a[136:139], v[84:87]\n" \
R_"a[196:199], v29 offset:51200\n" \
M_"v[84:87], a[240:243], a[200:203], v[84:87]\n" \
"s_movk_i32 s84, 0x2000\n" \
M_"v[88:91], a[180:183], a[136:139], v[88:91]\n" \
B_"v25, s[76:79], s84 offen offset:1024 lds\n" \
M_"v[88:91], a[244:247], a[200:203], v[88:91]\n" \
"s_movk_i32 s84, 0x4000\n" \
M_"v[92:95], a[184:187], a[136:139], v[92:95]\n" \
B_"v24, s[76:79], s84 offen offset:2048 lds\n" \
M_"v[92:95], a[248:251], a[200:203], v[92:95]\n" \
X_"a[166:167], v33 offset:33792\n" \
M_"v[96:99], a[188:191], a[136:139], v[96:99]\n" \
X_"a[228:229], v33 offset:40960\n" \
M_"v[96:99], a[252:255], a[200:203], v[96:99]\n" \
R_"a[136:139], v28 offset:53248\n" \
M_"v[112:115], a[188:191], a[140:143], v[112:115]\n" \
R_"a[200:203], v29 offset:53248\n" \
M_"v[112:115], a[252:255], a[204:207], v[112:115]\n" \
"s_movk_i32 s84, 0x6000\n" \
M_"v[108:111], a[184:187], a[140:143], v[108:111]\n" \
B_"v25, s[76:79], s84 offen offset:3072 lds\n" \
M_"v[108:111], a[248:251], a[204:207], v[108:111]\n" \
"s_add_u32 m0, s31, 0x0\n" \
"s_movk_i32 s84, 0x0\n" \
M_"v[104:107], a[180:183], a[140:143], v[104:107]\n" \
B_"v26, s[68:71], s84 offen offset:0 lds\n" \
M_"v[104:107], a[244:247], a[204:207], v[104:107]\n" \
"s_movk_i32 s84, 0x7c00\n" \
M_"v[100:103], a[176:179], a[140:143], v[100:103]\n" \
B_"v26, s[68:71], s84 offen offset:1024 lds\n" \
M_"v[100:103], a[240:243], a[204:207], v[100:103]\n" \
R_"a[140:143], v28 offset:55296\n" \
M_"v[116:119], a[176:179], a[144:147], v[116:119]\n" \
R_"a[204:207], v29 offset:55296\n" \
M_"v[116:119], a[240:243], a[208:211], v[116:119]\n" \
"s_addk_i32 s84, 0x7c00\n" \
M_"v[120:123], a[180:183], a[144:147], v[120:123]\n" \
B_"v27, s[68:71], s84 offen offset:2048 lds\n" \
M_"v[120:123], a[244:247], a[208:211], v[120:123]\n" \
X_"a[230:231], v33 offset:41984\n" \
M_"v[124:127], a[184:187], a[144:147], v[124:127]\n" \
"s_addk_i32 s84, 0x7c00\n" \
M_"v[124:127], a[248:251], a[208:211], v[124:127]\n" \
B_"v27, s[68:71], s84 offen offset:3072 lds\n" \
M_"v[128:131], a[188:191], a[144:147], v[128:131]\n" \
X_"a[168:169], v34 offset:32768\n" \
M_"v[128:131], a[252:255], a[208:211], v[128:131]\n" \
R_"a[144:147], v28 offset:32768\n" \
M_"v[144:147], a[188:191], a[148:151], v[144:147]\n" \
R_"a[208:211], v29 offset:32768\n" \
M_"v[144:147], a[252:255], a[212:215], v[144:147]\n" \
"s_add_u32 m0, s30, 0x1000\n" \
"s_mov_b32 s84, 0x9000\n" \
M_"v[140:143], a[184:187], a[148:151], v[140:143]\n" \
B_"v24, s[76:79], s84 offen offset:0 lds\n" \
M_"v[140:143], a[248:251], a[212:215], v[140:143]\n" \
X_"a[170:171], v34 offset:33792\n" \
M_"v[136:139], a[180:183], a[148:151], v[136:139]\n" \
"s_addk_i32 s84, 0x2000\n" \
"s_nop 0\n" \
M_"v[136:139], a[244:247], a[212:215], v[136:139]\n" \
B_"v25, s[76:79], s84 offen offset:1024 lds\n" \
M_"v[132:135], a[176:179], a[148:151], v[132:135]\n" \
X_"a[232:233], v34 offset:40960\n" \
M_"v[132:135], a[240:243], a[212:215], v[132:135]\n" \
R_"a[148:151], v28 offset:34816\n" \
M_"v[148:151], a[176:179], a[152:155], v[148:151]\n" \
R_"a[212:215], v29 offset:34816\n" \
M_"v[148:151], a[240:243], a[216:219], v[148:151]\n" \
X_"a[234:235], v34 offset:41984\n" \
M_"v[152:155], a[180:183], a[152:155], v[152:155]\n" \
X_"a[172:173], v35 offset:32768\n" \
M_"v[152:155], a[244:247], a[216:219], v[152:155]\n" \
X_"a[174:175], v35 offset:33792\n" \
M_"v[156:159], a[184:187], a[152:155], v[156:159]\n" \
X_"a[236:237], v35 offset:40960\n" \
M_"v[156:159], a[248:251], a[216:219], v[156:159]\n" \
X_"a[238:239], v35 offset:41984\n" \
M_"v[160:163], a[188:191], a[152:155], v[160:163]\n" \
M_"v[160:163], a[252:255], a[216:219], v[160:163]\n" \
R_"a[152:155], v28 offset:36864\n" \
M_"v[176:179], a[188:191], a[156:159], v[176:179]\n" \
R_"a[216:219], v29 offset:36864\n" \
M_"v[176:179], a[252:255], a[220:223], v[176:179]\n" \
M_"v[172:175], a[184:187], a[156:159], v[172:175]\n" \
M_"v[172:175], a[248:251], a[220:223], v[172:175]\n" \
M_"v[168:171], a[180:183], a[156:159], v[168:171]\n" \
M_"v[168:171], a[244:247], a[220:223], v[168:171]\n" \
M_"v[164:167], a[176:179], a[156:159], v[164:167]\n" \
M_"v[164:167], a[240:243], a[220:223], v[164:167]\n" \
R_"a[156:159], v28 offset:38912\n" \
R_"a[220:223], v29 offset:38912\n" \
"s_waitcnt lgkmcnt(15)\n" \
M_"a[0:3], a[160:163], a[128:131], a[0:3]\n" \
"s_addk_i32 s84, 0x2000\n" \
M_"a[0:3], a[224:227], a[192:195], a[0:3]\n" \
B_"v24, s[76:79], s84 offen offset:2048 lds\n" \
M_"a[4:7], a[164:167], a[128:131], a[4:7]\n" \
X_"a[176:177], v36 offset:32768\n" \
M_"a[4:7], a[228:231], a[192:195], a[4:7]\n" \
"s_addk_i32 s84, 0x2000\n" \
"s_waitcnt lgkmcnt(13)\n" \
M_"a[8:11], a[168:171], a[128:131], a[8:11]\n" \
B_"v25, s[76:79], s84 offen offset:3072 lds\n" \
"s_waitcnt lgkmcnt(9)\n" \
M_"a[8:11], a[232:235], a[192:195], a[8:11]\n" \
X_"a[178:179], v36 offset:33792\n" \
"s_waitcnt lgkmcnt(8)\n" \
M_"a[12:15], a[172:175], a[128:131], a[12:15]\n" \
X_"a[240:241], v36 offset:40960\n" \
"v_accvgpr_read_b32 v184, a0\n" \
"s_waitcnt lgkmcnt(7)\n" \
M_"a[12:15], a[236:239], a[192:195], a[12:15]\n" \
"v_accvgpr_read_b32 v185, a1\n" \
"v_accvgpr_read_b32 v186, a2\n" \
M_"a[28:31], a[172:175], a[132:135], a[28:31]\n" \
"v_accvgpr_read_b32 v187, a3\n" \
"v_accvgpr_read_b32 v180, a4\n" \
M_"a[28:31], a[236:239], a[196:199], a[28:31]\n" \
"s_add_u32 m0, s30, 0x20000\n" \
"s_movk_i32 s84, 0x100\n" \
M_"a[24:27], a[168:171], a[132:135], a[24:27]\n" \
B_"v24, s[76:79], s84 offen offset:0 lds\n" \
M_"a[24:27], a[232:235], a[196:199], a[24:27]\n" \
X_"a[242:243], v36 offset:41984\n" \
"v_accvgpr_read_b32 v181, a5\n" \
M_"a[20:23], a[164:167], a[132:135], a[20:23]\n" \
"v_accvgpr_read_b32 v182, a6\n" \
"v_accvgpr_read_b32 v183, a7\n" \
M_"a[20:23], a[228:231], a[196:199], a[20:23]\n" \
X_"a[180:181], v37 offset:32768\n" \
C_"v184, v184, v185\n" \
M_"a[16:19], a[160:163], a[132:135], a[16:19]\n" \
"s_movk_i32 s84, 0x2100\n" \
"s_nop 0\n" \
M_"a[16:19], a[224:227], a[196:199], a[16:19]\n" \
B_"v25, s[76:79], s84 offen offset:1024 lds\n" \
M_"a[32:35], a[160:163], a[136:139], a[32:35]\n" \
X_"a[182:183], v37 offset:33792\n" \
C_"v185, v186, v187\n" \
M_"a[32:35], a[224:227], a[200:203], a[32:35]\n" \
C_"v186, v180, v181\n" \
C_"v187, v182, v183\n" \
M_"a[36:39], a[164:167], a[136:139], a[36:39]\n" \
"v_accvgpr_read_b32 v188, a8\n" \
"v_accvgpr_read_b32 v189, a9\n" \
M_"a[36:39], a[228:231], a[200:203], a[36:39]\n" \
"s_movk_i32 s84, 0x4100\n" \
"s_nop 0\n" \
M_"a[40:43], a[168:171], a[136:139], a[40:43]\n" \
B_"v24, s[76:79], s84 offen offset:2048 lds\n" \
M_"a[40:43], a[232:235], a[200:203], a[40:43]\n" \
X_"a[244:245], v37 offset:40960\n" \
"v_accvgpr_read_b32 v190, a10\n" \
M_"a[44:47], a[172:175], a[136:139], a[44:47]\n" \
X_"a[246:247], v37 offset:41984\n" \
"v_accvgpr_read_b32 v191, a11\n" \
M_"a[44:47], a[236:239], a[200:203], a[44:47]\n" \
"v_accvgpr_read_b32 v180, a12\n" \
"v_accvgpr_read_b32 v181, a13\n" \
M_"a[60:63], a[172:175], a[140:143], a[60:63]\n" \
"s_movk_i32 s84, 0x6100\n" \
M_"a[60:63], a[236:239], a[204:207], a[60:63]\n" \
B_"v25, s[76:79], s84 offen offset:3072 lds\n" \
M_"a[56:59], a[168:171], a[140:143], a[56:59]\n" \
X_"a[184:185], v38 offset:32768\n" \
"v_accvgpr_read_b32 v182, a14\n" \
M_"a[56:59], a[232:235], a[204:207], a[56:59]\n" \
"v_accvgpr_read_b32 v183, a15\n" \
C_"v188, v188, v189\n" \
M_"a[52:55], a[164:167], a[140:143], a[52:55]\n" \
"s_add_u32 m0, s30, 0x21000\n" \
"s_addk_i32 s84, 0x3000\n" \
M_"a[52:55], a[228:231], a[204:207], a[52:55]\n" \
B_"v24, s[76:79], s84 offen offset:0 lds\n" \
M_"a[48:51], a[160:163], a[140:143], a[48:51]\n" \
X_"a[186:187], v38 offset:33792\n" \
C_"v189, v190, v191\n" \
M_"a[48:51], a[224:227], a[204:207], a[48:51]\n" \
C_"v190, v180, v181\n" \
C_"v191, v182, v183\n" \
M_"a[64:67], a[160:163], a[144:147], a[64:67]\n" \
X_"a[248:249], v38 offset:40960\n" \
"v_accvgpr_read_b32 v192, a24\n" \
M_"a[64:67], a[224:227], a[208:211], a[64:67]\n" \
"s_addk_i32 s84, 0x2000\n" \
"s_nop 0\n" \
M_"a[68:71], a[164:167], a[144:147], a[68:71]\n" \
B_"v25, s[76:79], s84 offen offset:1024 lds\n" \
M_"a[68:71], a[228:231], a[208:211], a[68:71]\n" \
X_"a[250:251], v38 offset:41984\n" \
"v_accvgpr_read_b32 v193, a25\n" \
M_"a[72:75], a[168:171], a[144:147], a[72:75]\n" \
"v_accvgpr_read_b32 v194, a26\n" \
"v_accvgpr_read_b32 v195, a27\n" \
M_"a[72:75], a[232:235], a[208:211], a[72:75]\n" \
"v_accvgpr_read_b32 v180, a28\n" \
"v_accvgpr_read_b32 v181, a29\n" \
M_"a[76:79], a[172:175], a[144:147], a[76:79]\n" \
"s_addk_i32 s84, 0x2000\n" \
M_"a[76:79], a[236:239], a[208:211], a[76:79]\n" \
B_"v24, s[76:79], s84 offen offset:2048 lds\n" \
M_"a[92:95], a[172:175], a[148:151], a[92:95]\n" \
"s_add_u32 s89, s41, 0x0\n" \
"buffer_store_dwordx4 v[184:187], v20, s[80:83], s89 offen offset:0 sc1\n" \
M_"a[92:95], a[236:239], a[212:215], a[92:95]\n" \
X_"a[188:189], v39 offset:32768\n" \
"v_accvgpr_read_b32 v182, a30\n" \
M_"a[88:91], a[168:171], a[148:151], a[88:91]\n" \
X_"a[190:191], v39 offset:33792\n" \
"v_accvgpr_read_b32 v183, a31\n" \
M_"a[88:91], a[232:235], a[212:215], a[88:91]\n" \
"s_addk_i32 s84, 0x2000\n" \
M_"a[84:87], a[164:167], a[148:151], a[84:87]\n" \
B_"v25, s[76:79], s84 offen offset:3072 lds\n" \
M_"a[84:87], a[228:231], a[212:215], a[84:87]\n" \
X_"a[252:253], v39 offset:40960\n" \
C_"v192, v192, v193\n" \
M_"a[80:83], a[160:163], a[148:151], a[80:83]\n" \
"s_add_u32 s88, s41, 0x0\n" \
"buffer_store_dwordx4 v[188:191], v20, s[80:83], s88 offen offset:1024 sc1\n" \
M_"a[80:83], a[224:227], a[212:215], a[80:83]\n" \
C_"v193, v194, v195\n" \
C_"v194, v180, v181\n" \
"s_waitcnt lgkmcnt(15)\n" \
"s_nop 0\n" \
M_"a[96:99], a[160:163], a[152:155], a[96:99]\n" \
X_"a[254:255], v39 offset:41984\n" \
C_"v195, v182, v183\n" \
M_"a[96:99], a[224:227], a[216:219], a[96:99]\n" \
"v_accvgpr_read_b32 v196, a16\n" \
"v_accvgpr_read_b32 v197, a17\n" \
M_"a[100:103], a[164:167], a[152:155], a[100:103]\n" \
"v_accvgpr_read_b32 v198, a18\n" \
"v_accvgpr_read_b32 v199, a19\n" \
M_"a[100:103], a[228:231], a[216:219], a[100:103]\n" \
"v_accvgpr_read_b32 v180, a20\n" \
"v_accvgpr_read_b32 v181, a21\n" \
M_"a[104:107], a[168:171], a[152:155], a[104:107]\n" \
"s_add_u32 s89, s41, 0x1000\n" \
"buffer_store_dwordx4 v[192:195], v20, s[80:83], s89 offen offset:1024 sc1\n" \
M_"a[104:107], a[232:235], a[216:219], a[104:107]\n" \
"v_accvgpr_read_b32 v182, a22\n" \
"v_accvgpr_read_b32 v183, a23\n" \
M_"a[108:111], a[172:175], a[152:155], a[108:111]\n" \
C_"v196, v196, v197\n" \
C_"v197, v198, v199\n" \
M_"a[108:111], a[236:239], a[216:219], a[108:111]\n" \
C_"v198, v180, v181\n" \
C_"v199, v182, v183\n" \
M_"a[124:127], a[172:175], a[156:159], a[124:127]\n" \
"v_accvgpr_read_b32 v200, a32\n" \
"v_accvgpr_read_b32 v201, a33\n" \
M_"a[124:127], a[236:239], a[220:223], a[124:127]\n" \
"v_accvgpr_read_b32 v202, a34\n" \
"v_accvgpr_read_b32 v203, a35\n" \
M_"a[120:123], a[168:171], a[156:159], a[120:123]\n" \
"s_add_u32 s88, s41, 0x1000\n" \
"buffer_store_dwordx4 v[196:199], v20, s[80:83], s88 offen offset:0 sc1\n" \
M_"a[120:123], a[232:235], a[220:223], a[120:123]\n" \
"v_accvgpr_read_b32 v180, a36\n" \
"v_accvgpr_read_b32 v181, a37\n" \
M_"a[116:119], a[164:167], a[156:159], a[116:119]\n" \
"v_accvgpr_read_b32 v182, a38\n" \
"v_accvgpr_read_b32 v183, a39\n" \
M_"a[116:119], a[228:231], a[220:223], a[116:119]\n" \
C_"v200, v200, v201\n" \
C_"v201, v202, v203\n" \
M_"a[112:115], a[160:163], a[156:159], a[112:115]\n" \
C_"v202, v180, v181\n" \
C_"v203, v182, v183\n" \
M_"a[112:115], a[224:227], a[220:223], a[112:115]\n" \
"v_accvgpr_read_b32 v204, a40\n" \
"v_accvgpr_read_b32 v205, a41\n" \
"s_waitcnt vmcnt(12) lgkmcnt(0)\n" \
"s_barrier\n" \
"v_mov_b32_e32 v36, v32\n" \
"v_xor_b32_e32 v32, 0x0, v48\n" \
"v_mov_b32_e32 v37, v33\n" \
"v_xor_b32_e32 v33, 0x20, v48\n" \
"v_mov_b32_e32 v38, v34\n" \
"v_xor_b32_e32 v34, 0x40, v48\n" \
"v_mov_b32_e64 v39, v35\n" \
"v_xor_b32_e32 v35, 0x60, v48\n" \
"v_add_u32_e32 v22, s44, v28\n" \
"v_add_u32_e32 v28, s43, v28\n" \
"v_add_u32_e32 v23, s44, v29\n" \
"v_add_u32_e32 v29, s43, v29\n" \
M_"v[52:55], a[176:179], a[128:131], v[52:55]\n" \
"s_add_u32 m0, s40, 0x8000\n" \
"s_add_u32 s85, s39, 0x2000\n" \
M_"v[52:55], a[240:243], a[192:195], v[52:55]\n" \
B_"v20, s[80:83], s85 offen sc1 lds\n" \
M_"v[56:59], a[180:183], a[128:131], v[56:59]\n" \
X_"a[160:161], v32 offset:0\n" \
"v_accvgpr_read_b32 v206, a42\n" \
M_"v[56:59], a[244:247], a[192:195], v[56:59]\n" \
"s_add_u32 m0, s40, 0x9000\n" \
"s_add_u32 s85, s39, 0x3000\n" \
M_"v[60:63], a[184:187], a[128:131], v[60:63]\n" \
B_"v20, s[80:83], s85 offen sc1 lds\n" \
M_"v[60:63], a[248:251], a[192:195], v[60:63]\n" \
X_"a[162:163], v32 offset:512\n" \
"v_accvgpr_read_b32 v207, a43\n" \
M_"v[64:67], a[188:191], a[128:131], v[64:67]\n" \
X_"a[224:225], v32 offset:4096\n" \
"v_accvgpr_read_b32 v180, a44\n" \
M_"v[64:67], a[252:255], a[192:195], v[64:67]\n" \
R_"a[128:131], v28 offset:0\n" \
"v_accvgpr_read_b32 v181, a45\n" \
M_"v[80:83], a[188:191], a[132:135], v[80:83]\n" \
R_"a[192:195], v29 offset:0\n" \
"v_accvgpr_read_b32 v182, a46\n" \
M_"v[80:83], a[252:255], a[196:199], v[80:83]\n" \
"s_add_u32 m0, s40, 0xb000\n" \
"s_movk_i32 s84, 0x80\n" \
M_"v[76:79], a[184:187], a[132:135], v[76:79]\n" \
B_"v21, s[76:79], s84 offen offset:0 lds\n" \
M_"v[76:79], a[248:251], a[196:199], v[76:79]\n" \
"s_add_u32 m0, s30, 0x8000\n" \
"s_movk_i32 s84, 0x80\n" \
M_"v[72:75], a[180:183], a[132:135], v[72:75]\n" \
B_"v24, s[76:79], s84 offen offset:0 lds\n" \
M_"v[72:75], a[244:247], a[196:199], v[72:75]\n" \
X_"a[226:227], v32 offset:4608\n" \
"v_accvgpr_read_b32 v183, a47\n" \
M_"v[68:71], a[176:179], a[132:135], v[68:71]\n" \
X_"a[164:165], v33 offset:0\n" \
C_"v204, v204, v205\n" \
M_"v[68:71], a[240:243], a[196:199], v[68:71]\n" \
R_"a[132:135], v28 offset:2048\n" \
C_"v205, v206, v207\n" \
M_"v[84:87], a[176:179], a[136:139], v[84:87]\n" \
R_"a[196:199], v29 offset:2048\n" \
C_"v206, v180, v181\n" \
M_"v[84:87], a[240:243], a[200:203], v[84:87]\n" \
"s_movk_i32 s84, 0x2080\n" \
M_"v[88:91], a[180:183], a[136:139], v[88:91]\n" \
B_"v25, s[76:79], s84 offen offset:1024 lds\n" \
M_"v[88:91], a[244:247], a[200:203], v[88:91]\n" \
"s_movk_i32 s84, 0x4080\n" \
M_"v[92:95], a[184:187], a[136:139], v[92:95]\n" \
B_"v24, s[76:79], s84 offen offset:2048 lds\n" \
M_"v[92:95], a[248:251], a[200:203], v[92:95]\n" \
X_"a[166:167], v33 offset:512\n" \
C_"v207, v182, v183\n" \
M_"v[96:99], a[188:191], a[136:139], v[96:99]\n" \
X_"a[228:229], v33 offset:4096\n" \
"v_accvgpr_read_b32 v208, a56\n" \
M_"v[96:99], a[252:255], a[200:203], v[96:99]\n" \
R_"a[136:139], v28 offset:4096\n" \
"v_accvgpr_read_b32 v209, a57\n" \
M_"v[112:115], a[188:191], a[140:143], v[112:115]\n" \
R_"a[200:203], v29 offset:4096\n" \
"v_accvgpr_read_b32 v210, a58\n" \
M_"v[112:115], a[252:255], a[204:207], v[112:115]\n" \
"s_movk_i32 s84, 0x6080\n" \
M_"v[108:111], a[184:187], a[140:143], v[108:111]\n" \
B_"v25, s[76:79], s84 offen offset:3072 lds\n" \
M_"v[108:111], a[248:251], a[204:207], v[108:111]\n" \
"s_add_u32 m0, s31, 0x8000\n" \
"s_mov_b32 s84, 0x80000\n" \
M_"v[104:107], a[180:183], a[140:143], v[104:107]\n" \
B_"v26, s[68:71], s84 offen offset:0 lds\n" \
M_"v[104:107], a[244:247], a[204:207], v[104:107]\n" \
"s_addk_i32 s84, 0x7c00\n" \
M_"v[100:103], a[176:179], a[140:143], v[100:103]\n" \
B_"v26, s[68:71], s84 offen offset:1024 lds\n" \
M_"v[100:103], a[240:243], a[204:207], v[100:103]\n" \
R_"a[140:143], v28 offset:6144\n" \
"v_accvgpr_read_b32 v211, a59\n" \
M_"v[116:119], a[176:179], a[144:147], v[116:119]\n" \
R_"a[204:207], v29 offset:6144\n" \
"v_accvgpr_read_b32 v180, a60\n" \
M_"v[116:119], a[240:243], a[208:211], v[116:119]\n" \
"s_addk_i32 s84, 0x7c00\n" \
M_"v[120:123], a[180:183], a[144:147], v[120:123]\n" \
B_"v27, s[68:71], s84 offen offset:2048 lds\n" \
M_"v[120:123], a[244:247], a[208:211], v[120:123]\n" \
X_"a[230:231], v33 offset:4608\n" \
"v_accvgpr_read_b32 v181, a61\n" \
M_"v[124:127], a[184:187], a[144:147], v[124:127]\n" \
"s_addk_i32 s84, 0x7c00\n" \
M_"v[124:127], a[248:251], a[208:211], v[124:127]\n" \
B_"v27, s[68:71], s84 offen offset:3072 lds\n" \
M_"v[128:131], a[188:191], a[144:147], v[128:131]\n" \
X_"a[172:173], v35 offset:0\n" \
"v_accvgpr_read_b32 v182, a62\n" \
M_"v[128:131], a[252:255], a[208:211], v[128:131]\n" \
R_"a[144:147], v28 offset:8192\n" \
"v_accvgpr_read_b32 v183, a63\n" \
M_"v[144:147], a[188:191], a[148:151], v[144:147]\n" \
R_"a[208:211], v29 offset:8192\n" \
C_"v208, v208, v209\n" \
M_"v[144:147], a[252:255], a[212:215], v[144:147]\n" \
"s_add_u32 m0, s30, 0x9000\n" \
"s_mov_b32 s84, 0x9080\n" \
M_"v[140:143], a[184:187], a[148:151], v[140:143]\n" \
B_"v24, s[76:79], s84 offen offset:0 lds\n" \
M_"v[140:143], a[248:251], a[212:215], v[140:143]\n" \
X_"a[174:175], v35 offset:512\n" \
C_"v209, v210, v211\n" \
M_"v[136:139], a[180:183], a[148:151], v[136:139]\n" \
"s_addk_i32 s84, 0x2000\n" \
"s_nop 0\n" \
M_"v[136:139], a[244:247], a[212:215], v[136:139]\n" \
B_"v25, s[76:79], s84 offen offset:1024 lds\n" \
M_"v[132:135], a[176:179], a[148:151], v[132:135]\n" \
X_"a[236:237], v35 offset:4096\n" \
C_"v210, v180, v181\n" \
M_"v[132:135], a[240:243], a[212:215], v[132:135]\n" \
R_"a[148:151], v28 offset:10240\n" \
C_"v211, v182, v183\n" \
M_"v[148:151], a[176:179], a[152:155], v[148:151]\n" \
R_"a[212:215], v29 offset:10240\n" \
"v_accvgpr_read_b32 v212, a48\n" \
M_"v[148:151], a[240:243], a[216:219], v[148:151]\n" \
X_"a[238:239], v35 offset:4608\n" \
"v_accvgpr_read_b32 v213, a49\n" \
M_"v[152:155], a[180:183], a[152:155], v[152:155]\n" \
X_"a[168:169], v34 offset:0\n" \
"v_accvgpr_read_b32 v214, a50\n" \
M_"v[152:155], a[244:247], a[216:219], v[152:155]\n" \
X_"a[170:171], v34 offset:512\n" \
"v_accvgpr_read_b32 v215, a51\n" \
M_"v[156:159], a[184:187], a[152:155], v[156:159]\n" \
X_"a[232:233], v34 offset:4096\n" \
"v_accvgpr_read_b32 v180, a52\n" \
M_"v[156:159], a[248:251], a[216:219], v[156:159]\n" \
X_"a[234:235], v34 offset:4608\n" \
"v_accvgpr_read_b32 v181, a53\n" \
M_"v[160:163], a[188:191], a[152:155], v[160:163]\n" \
R_"v[248:251], v22 offset:0\n" \
"v_accvgpr_read_b32 v182, a54\n" \
M_"v[160:163], a[252:255], a[216:219], v[160:163]\n" \
R_"a[152:155], v28 offset:12288\n" \
"v_accvgpr_read_b32 v183, a55\n" \
M_"v[176:179], a[188:191], a[156:159], v[176:179]\n" \
R_"a[216:219], v29 offset:12288\n" \
C_"v212, v212, v213\n" \
M_"v[176:179], a[252:255], a[220:223], v[176:179]\n" \
R_"v[48:51], v23 offset:0\n" \
C_"v213, v214, v215\n" \
M_"v[172:175], a[184:187], a[156:159], v[172:175]\n" \
C_"v214, v180, v181\n" \
C_"v215, v182, v183\n" \
M_"v[172:175], a[248:251], a[220:223], v[172:175]\n" \
"v_accvgpr_read_b32 v216, a64\n" \
"v_accvgpr_read_b32 v217, a65\n" \
M_"v[168:171], a[180:183], a[156:159], v[168:171]\n" \
"v_accvgpr_read_b32 v218, a66\n" \
"v_accvgpr_read_b32 v219, a67\n" \
M_"v[168:171], a[244:247], a[220:223], v[168:171]\n" \
"v_accvgpr_read_b32 v180, a68\n" \
"v_accvgpr_read_b32 v181, a69\n" \
M_"v[164:167], a[176:179], a[156:159], v[164:167]\n" \
"v_accvgpr_read_b32 v182, a70\n" \
"v_accvgpr_read_b32 v183, a71\n" \
M_"v[164:167], a[240:243], a[220:223], v[164:167]\n" \
R_"a[156:159], v28 offset:14336\n" \
R_"a[220:223], v29 offset:14336\n" \
"s_waitcnt lgkmcnt(15)\n" \
M_"a[0:3], a[160:163], a[128:131], 0\n" \
"s_addk_i32 s84, 0x2000\n" \
C_"v216, v216, v217\n" \
M_"a[0:3], a[224:227], a[192:195], a[0:3]\n" \
B_"v24, s[76:79], s84 offen offset:2048 lds\n" \
C_"v217, v218, v219\n" \
M_"a[4:7], a[164:167], a[128:131], 0\n" \
X_"a[176:177], v36 offset:0\n" \
C_"v218, v180, v181\n" \
M_"a[4:7], a[228:231], a[192:195], a[4:7]\n" \
"s_addk_i32 s84, 0x2000\n" \
C_"v219, v182, v183\n" \
"s_waitcnt lgkmcnt(15)\n" \
M_"a[12:15], a[172:175], a[128:131], 0\n" \
B_"v25, s[76:79], s84 offen offset:3072 lds\n" \
"v_accvgpr_read_b32 v220, a80\n" \
"s_waitcnt lgkmcnt(11)\n" \
M_"a[12:15], a[236:239], a[192:195], a[12:15]\n" \
X_"a[178:179], v36 offset:1024\n" \
"v_accvgpr_read_b32 v221, a81\n" \
"s_waitcnt lgkmcnt(10)\n" \
M_"a[8:11], a[128:131], a[168:171], 0\n" \
X_"a[240:241], v36 offset:8192\n" \
"v_accvgpr_read_b32 v222, a82\n" \
"s_waitcnt lgkmcnt(9)\n" \
"s_nop 0\n" \
M_"a[8:11], a[192:195], a[232:235], a[8:11]\n" \
X_"a[242:243], v36 offset:9216\n" \
"v_accvgpr_read_b32 v223, a83\n" \
".fill (L_pb + 20192 - .) / 4, 4, 0xBF800000\n" \
"L_pp5:\n" \
"s_cbranch_execz L_pret\n" \
"s_nop 0\n" \
M_"a[24:27], a[132:135], a[168:171], 0\n" \
X_"a[180:181], v37 offset:0\n" \
"v_accvgpr_read_b32 v180, a84\n" \
M_"a[24:27], a[196:199], a[232:235], a[24:27]\n" \
X_"a[182:183], v37 offset:1024\n" \
"v_accvgpr_read_b32 v181, a85\n" \
M_"a[28:31], a[172:175], a[132:135], 0\n" \
X_"a[244:245], v37 offset:8192\n" \
"v_accvgpr_read_b32 v182, a86\n" \
M_"a[28:31], a[236:239], a[196:199], a[28:31]\n" \
"s_add_u32 s89, s41, 0x2000\n" \
"buffer_store_dwordx4 v[200:203], v20, s[80:83], s89 offen offset:0 sc1\n" \
M_"a[20:23], a[164:167], a[132:135], 0\n" \
X_"a[246:247], v37 offset:9216\n" \
"v_accvgpr_read_b32 v183, a87\n" \
M_"a[20:23], a[228:231], a[196:199], a[20:23]\n" \
X_"a[184:185], v38 offset:0\n" \
C_"v220, v220, v221\n" \
M_"a[16:19], a[160:163], a[132:135], 0\n" \
X_"a[186:187], v38 offset:1024\n" \
C_"v221, v222, v223\n" \
M_"a[16:19], a[224:227], a[196:199], a[16:19]\n" \
C_"v222, v180, v181\n" \
C_"v223, v182, v183\n" \
C_"v224, v52, v53\n" \
M_"a[32:35], a[160:163], a[136:139], 0\n" \
"s_add_u32 s88, s41, 0x2000\n" \
"buffer_store_dwordx4 v[204:207], v20, s[80:83], s88 offen offset:1024 sc1\n" \
M_"a[32:35], a[224:227], a[200:203], a[32:35]\n" \
X_"a[248:249], v38 offset:8192\n" \
C_"v225, v54, v55\n" \
M_"a[36:39], a[164:167], a[136:139], 0\n" \
X_"a[250:251], v38 offset:9216\n" \
C_"v226, v56, v57\n" \
M_"a[36:39], a[228:231], a[200:203], a[36:39]\n" \
X_"a[188:189], v39 offset:0\n" \
C_"v227, v58, v59\n" \
M_"a[44:47], a[172:175], a[136:139], 0\n" \
C_"v228, v60, v61\n" \
C_"v229, v62, v63\n" \
C_"v230, v64, v65\n" \
M_"a[44:47], a[236:239], a[200:203], a[44:47]\n" \
X_"a[190:191], v39 offset:1024\n" \
C_"v231, v66, v67\n" \
M_"a[40:43], a[136:139], a[168:171], 0\n" \
"s_add_u32 s89, s41, 0x3000\n" \
"buffer_store_dwordx4 v[208:211], v20, s[80:83], s89 offen offset:1024 sc1\n" \
M_"a[40:43], a[200:203], a[232:235], a[40:43]\n" \
X_"a[252:253], v39 offset:8192\n" \
C_"v232, v76, v77\n" \
M_"a[56:59], a[140:143], a[168:171], 0\n" \
X_"a[254:255], v39 offset:9216\n" \
C_"v233, v78, v79\n" \
M_"a[56:59], a[204:207], a[232:235], a[56:59]\n" \
C_"v234, v80, v81\n" \
C_"v235, v82, v83\n" \
C_"v236, v68, v69\n" \
M_"a[60:63], a[172:175], a[140:143], 0\n" \
C_"v237, v70, v71\n" \
C_"v238, v72, v73\n" \
C_"v239, v74, v75\n" \
M_"a[60:63], a[236:239], a[204:207], a[60:63]\n" \
"s_add_u32 s88, s41, 0x3000\n" \
"buffer_store_dwordx4 v[212:215], v20, s[80:83], s88 offen offset:0 sc1\n" \
M_"a[52:55], a[164:167], a[140:143], 0\n" \
C_"v240, v84, v85\n" \
C_"v241, v86, v87\n" \
C_"v242, v88, v89\n" \
M_"a[52:55], a[228:231], a[204:207], a[52:55]\n" \
C_"v243, v90, v91\n" \
C_"v244, v92, v93\n" \
C_"v245, v94, v95\n" \
M_"a[48:51], a[160:163], a[140:143], 0\n" \
C_"v246, v96, v97\n" \
C_"v247, v98, v99\n" \
C_"v184, v108, v109\n" \
M_"a[48:51], a[224:227], a[204:207], a[48:51]\n" \
C_"v185, v110, v111\n" \
C_"v186, v112, v113\n" \
C_"v187, v114, v115\n" \
"s_waitcnt lgkmcnt(15)\n" \
"s_nop 0\n" \
M_"a[64:67], a[164:167], v[248:251], 0\n" \
C_"v188, v100, v101\n" \
C_"v189, v102, v103\n" \
C_"v190, v104, v105\n" \
M_"a[64:67], a[228:231], v[48:51], a[64:67]\n" \
C_"v191, v106, v107\n" \
C_"v192, v116, v117\n" \
C_"v193, v118, v119\n" \
M_"a[80:83], a[172:175], v[248:251], 0\n" \
C_"v194, v120, v121\n" \
C_"v195, v122, v123\n" \
C_"v196, v124, v125\n" \
M_"a[80:83], a[236:239], v[48:51], a[80:83]\n" \
C_"v197, v126, v127\n" \
C_"v198, v128, v129\n" \
C_"v199, v130, v131\n" \
M_"a[68:71], v[248:251], a[168:171], 0\n" \
C_"v252, v140, v141\n" \
C_"v253, v142, v143\n" \
C_"v254, v144, v145\n" \
M_"a[68:71], v[48:51], a[232:235], a[68:71]\n" \
C_"v255, v146, v147\n" \
"v_accvgpr_write_b32 a84, v252\n" \
"v_accvgpr_write_b32 a85, v253\n" \
"s_waitcnt vmcnt(4) lgkmcnt(0)\n" \
"s_barrier\n" \
M_"v[52:55], a[176:179], a[128:131], 0\n" \
"s_add_u32 m0, s40, 0x0\n" \
"s_nop 0\n" \
"s_add_u32 s85, s39, 0x4000\n" \
"v_accvgpr_write_b32 a86, v254\n" \
M_"v[52:55], a[240:243], a[192:195], v[52:55]\n" \
B_"v20, s[80:83], s85 offen sc1 lds\n" \
"v_accvgpr_write_b32 a87, v255\n" \
M_"v[56:59], a[180:183], a[128:131], 0\n" \
X_"a[160:161], v32 offset:32768\n" \
C_"v252, v132, v133\n" \
M_"v[56:59], a[244:247], a[192:195], v[56:59]\n" \
X_"a[162:163], v32 offset:33280\n" \
C_"v253, v134, v135\n" \
M_"v[60:63], a[184:187], a[128:131], 0\n" \
"s_add_u32 m0, s40, 0x1000\n" \
"s_add_u32 s85, s39, 0x5000\n" \
C_"v254, v136, v137\n" \
M_"v[60:63], a[248:251], a[192:195], v[60:63]\n" \
B_"v20, s[80:83], s85 offen sc1 lds\n" \
C_"v255, v138, v139\n" \
M_"v[64:67], a[188:191], a[128:131], 0\n" \
X_"a[224:225], v32 offset:36864\n" \
C_"v200, v148, v149\n" \
M_"v[64:67], a[252:255], a[192:195], v[64:67]\n" \
R_"a[128:131], v28 offset:32768\n" \
C_"v201, v150, v151\n" \
M_"v[80:83], a[188:191], a[132:135], 0\n" \
R_"a[192:195], v29 offset:32768\n" \
C_"v202, v152, v153\n" \
M_"v[80:83], a[252:255], a[196:199], v[80:83]\n" \
"s_add_u32 m0, s40, 0x3000\n" \
"s_movk_i32 s84, 0x100\n" \
"s_nop 0\n" \
C_"v203, v154, v155\n" \
M_"v[76:79], a[184:187], a[132:135], 0\n" \
B_"v21, s[76:79], s84 offen offset:0 lds\n" \
C_"v204, v156, v157\n" \
M_"v[76:79], a[248:251], a[196:199], v[76:79]\n" \
X_"a[226:227], v32 offset:37376\n" \
C_"v205, v158, v159\n" \
M_"v[72:75], a[180:183], a[132:135], 0\n" \
X_"a[164:165], v33 offset:32768\n" \
C_"v206, v160, v161\n" \
M_"v[72:75], a[244:247], a[196:199], v[72:75]\n" \
C_"v207, v162, v163\n" \
C_"v208, v172, v173\n" \
C_"v209, v174, v175\n" \
M_"v[68:71], a[176:179], a[132:135], 0\n" \
X_"a[166:167], v33 offset:33280\n" \
C_"v210, v176, v177\n" \
M_"v[68:71], a[240:243], a[196:199], v[68:71]\n" \
R_"a[132:135], v28 offset:34816\n" \
C_"v211, v178, v179\n" \
M_"v[84:87], a[176:179], a[136:139], 0\n" \
R_"a[196:199], v29 offset:34816\n" \
C_"v212, v164, v165\n" \
M_"v[84:87], a[240:243], a[200:203], v[84:87]\n" \
"s_add_u32 m0, s31, 0x0\n" \
"s_mov_b32 s84, 0x100000\n" \
C_"v213, v166, v167\n" \
M_"v[88:91], a[180:183], a[136:139], 0\n" \
B_"v26, s[68:71], s84 offen offset:0 lds\n" \
C_"v214, v168, v169\n" \
M_"v[88:91], a[244:247], a[200:203], v[88:91]\n" \
"s_add_u32 s89, s41, 0x0\n" \
"buffer_store_dwordx4 v[224:227], v20, s[80:83], s89 offen offset:2048 sc1\n" \
M_"v[92:95], a[184:187], a[136:139], 0\n" \
X_"a[228:229], v33 offset:36864\n" \
C_"v215, v170, v171\n" \
M_"v[92:95], a[248:251], a[200:203], v[92:95]\n" \
X_"a[230:231], v33 offset:37376\n" \
M_"v[96:99], a[188:191], a[136:139], 0\n" \
M_"v[96:99], a[252:255], a[200:203], v[96:99]\n" \
R_"a[136:139], v28 offset:36864\n" \
M_"v[112:115], a[188:191], a[140:143], 0\n" \
R_"a[200:203], v29 offset:36864\n" \
M_"v[112:115], a[252:255], a[204:207], v[112:115]\n" \
"s_addk_i32 s84, 0x7c00\n" \
M_"v[108:111], a[184:187], a[140:143], 0\n" \
B_"v26, s[68:71], s84 offen offset:1024 lds\n" \
M_"v[108:111], a[248:251], a[204:207], v[108:111]\n" \
X_"a[172:173], v35 offset:32768\n" \
M_"v[104:107], a[180:183], a[140:143], 0\n" \
"s_addk_i32 s84, 0x7c00\n" \
M_"v[104:107], a[244:247], a[204:207], v[104:107]\n" \
B_"v27, s[68:71], s84 offen offset:2048 lds\n" \
M_"v[100:103], a[176:179], a[140:143], 0\n" \
X_"a[174:175], v35 offset:33280\n" \
M_"v[100:103], a[240:243], a[204:207], v[100:103]\n" \
R_"a[140:143], v28 offset:38912\n" \
M_"v[116:119], a[176:179], a[144:147], 0\n" \
R_"a[204:207], v29 offset:38912\n" \
M_"v[116:119], a[240:243], a[208:211], v[116:119]\n" \
"s_addk_i32 s84, 0x7c00\n" \
M_"v[120:123], a[180:183], a[144:147], 0\n" \
B_"v27, s[68:71], s84 offen offset:3072 lds\n" \
M_"v[120:123], a[244:247], a[208:211], v[120:123]\n" \
X_"a[236:237], v35 offset:36864\n" \
M_"v[124:127], a[184:187], a[144:147], 0\n" \
X_"a[238:239], v35 offset:37376\n" \
M_"v[124:127], a[248:251], a[208:211], v[124:127]\n" \
"s_add_u32 m0, s30, 0x0\n" \
"s_movk_i32 s84, 0x180\n" \
M_"v[128:131], a[188:191], a[144:147], 0\n" \
B_"v24, s[76:79], s84 offen offset:0 lds\n" \
M_"v[128:131], a[252:255], a[208:211], v[128:131]\n" \
R_"a[144:147], v28 offset:40960\n" \
M_"v[144:147], a[188:191], a[148:151], 0\n" \
R_"a[208:211], v29 offset:40960\n" \
M_"v[144:147], a[252:255], a[212:215], v[144:147]\n" \
X_"a[168:169], v34 offset:32768\n" \
M_"v[140:143], a[184:187], a[148:151], 0\n" \
"s_movk_i32 s84, 0x2180\n" \
M_"v[140:143], a[248:251], a[212:215], v[140:143]\n" \
B_"v25, s[76:79], s84 offen offset:1024 lds\n" \
M_"v[136:139], a[180:183], a[148:151], 0\n" \
X_"a[170:171], v34 offset:33280\n" \
M_"v[136:139], a[244:247], a[212:215], v[136:139]\n" \
X_"a[232:233], v34 offset:36864\n" \
M_"v[132:135], a[176:179], a[148:151], 0\n" \
X_"a[234:235], v34 offset:37376\n" \
M_"v[132:135], a[240:243], a[212:215], v[132:135]\n" \
R_"a[148:151], v28 offset:43008\n" \
M_"v[148:151], a[176:179], a[152:155], 0\n" \
R_"a[212:215], v29 offset:43008\n" \
M_"v[148:151], a[240:243], a[216:219], v[148:151]\n" \
"s_movk_i32 s84, 0x4180\n" \
M_"v[152:155], a[180:183], a[152:155], 0\n" \
B_"v24, s[76:79], s84 offen offset:2048 lds\n" \
M_"v[152:155], a[244:247], a[216:219], v[152:155]\n" \
"s_movk_i32 s84, 0x6180\n" \
M_"v[156:159], a[184:187], a[152:155], 0\n" \
B_"v25, s[76:79], s84 offen offset:3072 lds\n" \
M_"v[156:159], a[248:251], a[216:219], v[156:159]\n" \
R_"v[248:251], v22 offset:32768\n" \
M_"v[160:163], a[188:191], a[152:155], 0\n" \
R_"v[48:51], v23 offset:32768\n" \
M_"v[160:163], a[252:255], a[216:219], v[160:163]\n" \
R_"a[152:155], v28 offset:45056\n" \
M_"v[176:179], a[188:191], a[156:159], 0\n" \
R_"a[216:219], v29 offset:45056\n" \
M_"v[176:179], a[252:255], a[220:223], v[176:179]\n" \
M_"v[172:175], a[184:187], a[156:159], 0\n" \
M_"v[172:175], a[248:251], a[220:223], v[172:175]\n" \
M_"v[168:171], a[180:183], a[156:159], 0\n" \
M_"v[168:171], a[244:247], a[220:223], v[168:171]\n" \
M_"v[164:167], a[176:179], a[156:159], 0\n" \
M_"v[164:167], a[240:243], a[220:223], v[164:167]\n" \
R_"a[156:159], v28 offset:47104\n" \
R_"a[220:223], v29 offset:47104\n" \
"s_waitcnt lgkmcnt(15)\n" \
M_"a[0:3], a[160:163], a[128:131], a[0:3]\n" \
"s_add_u32 m0, s30, 0x1000\n" \
"s_addk_i32 s84, 0x3000\n" \
M_"a[0:3], a[224:227], a[192:195], a[0:3]\n" \
B_"v24, s[76:79], s84 offen offset:0 lds\n" \
M_"a[4:7], a[164:167], a[128:131], a[4:7]\n" \
X_"a[176:177], v36 offset:32768\n" \
"v_mov_b32_e64 v11, 0\n" \
M_"a[4:7], a[228:231], a[192:195], a[4:7]\n" \
X_"a[178:179], v36 offset:33792\n" \
"s_and_b32 s5, s2, 7\n" \
M_"a[12:15], a[172:175], a[128:131], a[12:15]\n" \
X_"a[240:241], v36 offset:40960\n" \
"s_lshl_b32 s6, s3, 1\n" \
"s_waitcnt lgkmcnt(15)\n" \
M_"a[12:15], a[236:239], a[192:195], a[12:15]\n" \
"s_add_u32 s88, s41, 0x0\n" \
"buffer_store_dwordx4 v[228:231], v20, s[80:83], s88 offen offset:3072 sc1\n" \
"s_waitcnt lgkmcnt(13)\n" \
M_"a[8:11], a[128:131], a[168:171], a[8:11]\n" \
X_"a[242:243], v36 offset:41984\n" \
"s_lshl_b32 s4, s5, 2\n" \
"s_waitcnt lgkmcnt(12)\n" \
M_"a[8:11], a[192:195], a[232:235], a[8:11]\n" \
X_"a[180:181], v37 offset:32768\n" \
"s_bfe_u32 s7, s2, 0x20003\n" \
M_"a[24:27], a[132:135], a[168:171], a[24:27]\n" \
X_"a[182:183], v37 offset:33792\n" \
"s_or_b32 s12, s4, s7\n" \
M_"a[24:27], a[196:199], a[232:235], a[24:27]\n" \
X_"a[244:245], v37 offset:40960\n" \
"s_and_b32 s6, s6, 4\n" \
M_"a[28:31], a[172:175], a[132:135], a[28:31]\n" \
"s_and_b32 s8, s3, 1\n" \
"s_or_b32 s8, s6, s8\n" \
M_"a[28:31], a[236:239], a[196:199], a[28:31]\n" \
"s_addk_i32 s84, 0x2000\n" \
M_"a[20:23], a[164:167], a[132:135], a[20:23]\n" \
B_"v25, s[76:79], s84 offen offset:1024 lds\n" \
M_"a[20:23], a[228:231], a[196:199], a[20:23]\n" \
X_"a[246:247], v37 offset:41984\n" \
"s_lshl_b32 s6, s12, 4\n" \
M_"a[16:19], a[160:163], a[132:135], a[16:19]\n" \
X_"a[184:185], v38 offset:32768\n" \
"s_lshl_b32 s9, s3, 2\n" \
M_"a[16:19], a[224:227], a[196:199], a[16:19]\n" \
X_"a[186:187], v38 offset:33792\n" \
"s_or_b32 s6, s6, s9\n" \
M_"a[32:35], a[160:163], a[136:139], a[32:35]\n" \
"s_addk_i32 s84, 0x2000\n" \
M_"a[32:35], a[224:227], a[200:203], a[32:35]\n" \
B_"v24, s[76:79], s84 offen offset:2048 lds\n" \
M_"a[36:39], a[164:167], a[136:139], a[36:39]\n" \
X_"a[248:249], v38 offset:40960\n" \
"s_lshr_b32 s9, s2, 5\n" \
M_"a[36:39], a[228:231], a[200:203], a[36:39]\n" \
X_"a[250:251], v38 offset:41984\n" \
"s_mul_i32 s10, s9, 0xa00\n" \
M_"a[44:47], a[172:175], a[136:139], a[44:47]\n" \
X_"a[188:189], v39 offset:32768\n" \
"s_add_i32 s10, s10, 0x5bc1000\n" \
M_"a[44:47], a[236:239], a[200:203], a[44:47]\n" \
X_"a[190:191], v39 offset:33792\n" \
"s_or_b32 s6, s6, s10\n" \
M_"a[40:43], a[136:139], a[168:171], a[40:43]\n" \
"s_add_u32 s89, s41, 0x1000\n" \
"buffer_store_dwordx4 v[232:235], v20, s[80:83], s89 offen offset:3072 sc1\n" \
M_"a[40:43], a[200:203], a[232:235], a[40:43]\n" \
X_"a[252:253], v39 offset:40960\n" \
"s_lshl_b32 s13, s9, 3\n" \
M_"a[56:59], a[140:143], a[168:171], a[56:59]\n" \
X_"a[254:255], v39 offset:41984\n" \
"v_writelane_b32 v11, s6, 0\n" \
M_"a[56:59], a[204:207], a[232:235], a[56:59]\n" \
"s_or_b32 s6, s13, s5\n" \
"v_writelane_b32 v11, s10, 1\n" \
M_"a[60:63], a[172:175], a[140:143], a[60:63]\n" \
"s_lshl_b32 s6, s6, 10\n" \
"s_lshl_b32 s10, s3, 8\n" \
M_"a[60:63], a[236:239], a[204:207], a[60:63]\n" \
"s_addk_i32 s84, 0x2000\n" \
M_"a[52:55], a[164:167], a[140:143], a[52:55]\n" \
B_"v25, s[76:79], s84 offen offset:3072 lds\n" \
M_"a[52:55], a[228:231], a[204:207], a[52:55]\n" \
"s_or_b32 s6, s6, s10\n" \
"s_or_b32 s10, s6, 0x5c00200\n" \
M_"a[48:51], a[160:163], a[140:143], a[48:51]\n" \
"v_writelane_b32 v11, s10, 2\n" \
"s_lshl_b32 s10, s7, 2\n" \
M_"a[48:51], a[224:227], a[204:207], a[48:51]\n" \
"s_or_b32 s11, s10, s6\n" \
"s_or_b32 s11, s11, 0x5c00000\n" \
"s_waitcnt lgkmcnt(15)\n" \
M_"a[64:67], a[164:167], v[248:251], a[64:67]\n" \
"v_writelane_b32 v11, s11, 3\n" \
"s_or_b32 s6, s6, 0x5c00080\n" \
M_"a[64:67], a[228:231], v[48:51], a[64:67]\n" \
"v_writelane_b32 v11, s6, 4\n" \
"s_or_b32 s6, s6, s10\n" \
M_"a[80:83], a[172:175], v[248:251], a[80:83]\n" \
"s_add_i32 s10, s3, s2\n" \
"v_writelane_b32 v11, s6, 5\n" \
M_"a[80:83], a[236:239], v[48:51], a[80:83]\n" \
"s_mov_b32 s6, 2.0\n" \
"s_mov_b32 s11, 0x5bc0000\n" \
M_"a[68:71], v[248:251], a[168:171], a[68:71]\n" \
"s_cmp_lg_u32 s10, 0\n" \
"s_cselect_b32 s10, s6, s11\n" \
"s_waitcnt vmcnt(10) lgkmcnt(0)\n" \
M_"a[68:71], v[48:51], a[232:235], a[68:71]\n" \
"s_barrier\n" \
"v_xor_b32_e32 v28, 0x20000, v28\n" \
"v_xor_b32_e32 v29, 0x20000, v29\n" \
M_"v[52:55], a[176:179], a[128:131], v[52:55]\n" \
"s_add_u32 m0, s40, 0x8000\n" \
"s_add_u32 s85, s39, 0x6000\n" \
M_"v[52:55], a[240:243], a[192:195], v[52:55]\n" \
B_"v20, s[80:83], s85 offen sc1 lds\n" \
M_"v[56:59], a[180:183], a[128:131], v[56:59]\n" \
X_"a[160:161], v32 offset:0\n" \
"s_lshr_b32 s14, s2, 3\n" \
M_"v[56:59], a[244:247], a[192:195], v[56:59]\n" \
X_"a[162:163], v32 offset:512\n" \
"v_writelane_b32 v11, s10, 57\n" \
M_"v[60:63], a[184:187], a[128:131], v[60:63]\n" \
"s_add_u32 m0, s40, 0x9000\n" \
"s_add_u32 s85, s39, 0x7000\n" \
M_"v[60:63], a[248:251], a[192:195], v[60:63]\n" \
B_"v20, s[80:83], s85 offen sc1 lds\n" \
M_"v[64:67], a[188:191], a[128:131], v[64:67]\n" \
X_"a[224:225], v32 offset:4096\n" \
"s_lshl_b32 s10, s5, 5\n" \
M_"v[64:67], a[252:255], a[192:195], v[64:67]\n" \
R_"a[128:131], v28 offset:0\n" \
"s_min_u32 s15, s3, 2\n" \
"s_nop 0\n" \
M_"v[80:83], a[188:191], a[132:135], v[80:83]\n" \
R_"a[192:195], v29 offset:0\n" \
"s_mul_i32 s11, s7, 0x120\n" \
M_"v[80:83], a[252:255], a[196:199], v[80:83]\n" \
"s_add_u32 s88, s41, 0x1000\n" \
"buffer_store_dwordx4 v[236:239], v20, s[80:83], s88 offen offset:2048 sc1\n" \
M_"v[76:79], a[184:187], a[132:135], v[76:79]\n" \
X_"a[226:227], v32 offset:4608\n" \
M_"v[76:79], a[248:251], a[196:199], v[76:79]\n" \
X_"a[164:165], v33 offset:0\n" \
M_"v[72:75], a[180:183], a[132:135], v[72:75]\n" \
"s_add_u32 m0, s40, 0xb000\n" \
"s_movk_i32 s84, 0x180\n" \
"s_nop 0\n" \
M_"v[72:75], a[244:247], a[196:199], v[72:75]\n" \
B_"v21, s[76:79], s84 offen offset:0 lds\n" \
M_"v[68:71], a[176:179], a[132:135], v[68:71]\n" \
X_"a[166:167], v33 offset:512\n" \
M_"v[68:71], a[240:243], a[196:199], v[68:71]\n" \
R_"a[132:135], v28 offset:2048\n" \
M_"v[84:87], a[176:179], a[136:139], v[84:87]\n" \
R_"a[196:199], v29 offset:2048\n" \
M_"v[84:87], a[240:243], a[200:203], v[84:87]\n" \
"s_add_u32 m0, s31, 0x8000\n" \
"s_mov_b32 s84, 0x180000\n" \
M_"v[88:91], a[180:183], a[136:139], v[88:91]\n" \
B_"v26, s[68:71], s84 offen offset:0 lds\n" \
M_"v[88:91], a[244:247], a[200:203], v[88:91]\n" \
X_"a[228:229], v33 offset:4096\n" \
M_"v[92:95], a[184:187], a[136:139], v[92:95]\n" \
X_"a[230:231], v33 offset:4608\n" \
M_"v[92:95], a[248:251], a[200:203], v[92:95]\n" \
"s_addk_i32 s84, 0x7c00\n" \
M_"v[96:99], a[188:191], a[136:139], v[96:99]\n" \
B_"v26, s[68:71], s84 offen offset:1024 lds\n" \
M_"v[96:99], a[252:255], a[200:203], v[96:99]\n" \
R_"a[136:139], v28 offset:4096\n" \
M_"v[112:115], a[188:191], a[140:143], v[112:115]\n" \
R_"a[200:203], v29 offset:4096\n" \
M_"v[112:115], a[252:255], a[204:207], v[112:115]\n" \
"s_addk_i32 s84, 0x7c00\n" \
M_"v[108:111], a[184:187], a[140:143], v[108:111]\n" \
B_"v27, s[68:71], s84 offen offset:2048 lds\n" \
M_"v[108:111], a[248:251], a[204:207], v[108:111]\n" \
X_"a[172:173], v35 offset:0\n" \
M_"v[104:107], a[180:183], a[140:143], v[104:107]\n" \
X_"a[174:175], v35 offset:512\n" \
M_"v[104:107], a[244:247], a[204:207], v[104:107]\n" \
X_"a[236:237], v35 offset:4096\n" \
M_"v[100:103], a[176:179], a[140:143], v[100:103]\n" \
"s_add_u32 s16, s22, s11\n" \
"s_addc_u32 s17, s23, 0\n" \
M_"v[100:103], a[240:243], a[204:207], v[100:103]\n" \
R_"a[140:143], v28 offset:6144\n" \
M_"v[116:119], a[176:179], a[144:147], v[116:119]\n" \
R_"a[204:207], v29 offset:6144\n" \
M_"v[116:119], a[240:243], a[208:211], v[116:119]\n" \
"s_addk_i32 s84, 0x7c00\n" \
M_"v[120:123], a[180:183], a[144:147], v[120:123]\n" \
B_"v27, s[68:71], s84 offen offset:3072 lds\n" \
M_"v[120:123], a[244:247], a[208:211], v[120:123]\n" \
"s_add_u32 m0, s30, 0x8000\n" \
"s_movk_i32 s84, 0x200\n" \
M_"v[124:127], a[184:187], a[144:147], v[124:127]\n" \
B_"v24, s[76:79], s84 offen offset:0 lds\n" \
M_"v[124:127], a[248:251], a[208:211], v[124:127]\n" \
X_"a[238:239], v35 offset:4608\n" \
M_"v[128:131], a[188:191], a[144:147], v[128:131]\n" \
"s_add_u32 s89, s41, 0x2000\n" \
"buffer_store_dwordx4 v[240:243], v20, s[80:83], s89 offen offset:2048 sc1\n" \
M_"v[128:131], a[252:255], a[208:211], v[128:131]\n" \
R_"a[144:147], v28 offset:8192\n" \
M_"v[144:147], a[188:191], a[148:151], v[144:147]\n" \
R_"a[208:211], v29 offset:8192\n" \
M_"v[144:147], a[252:255], a[212:215], v[144:147]\n" \
X_"a[168:169], v34 offset:0\n" \
M_"v[140:143], a[184:187], a[148:151], v[140:143]\n" \
X_"a[170:171], v34 offset:512\n" \
M_"v[140:143], a[248:251], a[212:215], v[140:143]\n" \
"s_movk_i32 s84, 0x2200\n" \
"s_nop 0\n" \
M_"v[136:139], a[180:183], a[148:151], v[136:139]\n" \
B_"v25, s[76:79], s84 offen offset:1024 lds\n" \
M_"v[136:139], a[244:247], a[212:215], v[136:139]\n" \
X_"a[232:233], v34 offset:4096\n" \
M_"v[132:135], a[176:179], a[148:151], v[132:135]\n" \
X_"a[234:235], v34 offset:4608\n" \
M_"v[132:135], a[240:243], a[212:215], v[132:135]\n" \
R_"a[148:151], v28 offset:10240\n" \
M_"v[148:151], a[176:179], a[152:155], v[148:151]\n" \
R_"a[212:215], v29 offset:10240\n" \
M_"v[148:151], a[240:243], a[216:219], v[148:151]\n" \
"s_movk_i32 s84, 0x4200\n" \
M_"v[152:155], a[180:183], a[152:155], v[152:155]\n" \
B_"v24, s[76:79], s84 offen offset:2048 lds\n" \
M_"v[152:155], a[244:247], a[216:219], v[152:155]\n" \
"s_movk_i32 s84, 0x6200\n" \
M_"v[156:159], a[184:187], a[152:155], v[156:159]\n" \
B_"v25, s[76:79], s84 offen offset:3072 lds\n" \
M_"v[156:159], a[248:251], a[216:219], v[156:159]\n" \
R_"v[248:251], v22 offset:0\n" \
M_"v[160:163], a[188:191], a[152:155], v[160:163]\n" \
R_"v[48:51], v23 offset:0\n" \
M_"v[160:163], a[252:255], a[216:219], v[160:163]\n" \
R_"a[152:155], v28 offset:12288\n" \
M_"v[176:179], a[188:191], a[156:159], v[176:179]\n" \
R_"a[216:219], v29 offset:12288\n" \
M_"v[176:179], a[252:255], a[220:223], v[176:179]\n" \
"s_add_u32 s18, s24, s11\n" \
"v_writelane_b32 v11, s16, 9\n" \
"s_addc_u32 s19, s25, 0\n" \
M_"v[172:175], a[184:187], a[156:159], v[172:175]\n" \
"s_and_b32 s20, s17, 0xffff\n" \
"v_writelane_b32 v11, s20, 10\n" \
M_"v[172:175], a[248:251], a[220:223], v[172:175]\n" \
"s_and_b32 s20, s19, 0xffff\n" \
"v_writelane_b32 v11, s18, 11\n" \
M_"v[168:171], a[180:183], a[156:159], v[168:171]\n" \
"s_cmpk_lt_u32 s3, 3\n" \
"s_cselect_b32 s16, s16, s18\n" \
M_"v[168:171], a[244:247], a[220:223], v[168:171]\n" \
"s_movk_i32 s18, 0xfff\n" \
"v_writelane_b32 v11, s20, 12\n" \
M_"v[164:167], a[176:179], a[156:159], v[164:167]\n" \
"s_and_b32 s20, s29, 0xffff\n" \
"v_writelane_b32 v11, s28, 13\n" \
M_"v[164:167], a[240:243], a[220:223], v[164:167]\n" \
R_"a[156:159], v28 offset:14336\n" \
R_"a[220:223], v29 offset:14336\n" \
"s_waitcnt lgkmcnt(15)\n" \
M_"a[0:3], a[160:163], a[128:131], a[0:3]\n" \
"s_add_u32 m0, s30, 0x9000\n" \
"s_addk_i32 s84, 0x3000\n" \
M_"a[0:3], a[224:227], a[192:195], a[0:3]\n" \
B_"v24, s[76:79], s84 offen offset:0 lds\n" \
M_"a[4:7], a[164:167], a[128:131], a[4:7]\n" \
X_"a[176:177], v36 offset:0\n" \
"v_writelane_b32 v11, s20, 14\n" \
M_"a[4:7], a[228:231], a[192:195], a[4:7]\n" \
X_"a[178:179], v36 offset:1024\n" \
"v_writelane_b32 v11, s16, 15\n" \
M_"a[12:15], a[172:175], a[128:131], a[12:15]\n" \
X_"a[240:241], v36 offset:8192\n" \
"s_waitcnt lgkmcnt(15)\n" \
M_"a[12:15], a[236:239], a[192:195], a[12:15]\n" \
"s_add_u32 s88, s41, 0x2000\n" \
"buffer_store_dwordx4 v[244:247], v20, s[80:83], s88 offen offset:3072 sc1\n" \
"s_waitcnt lgkmcnt(13)\n" \
M_"a[8:11], a[128:131], a[168:171], a[8:11]\n" \
X_"a[242:243], v36 offset:9216\n" \
"s_waitcnt lgkmcnt(12)\n" \
M_"a[8:11], a[192:195], a[232:235], a[8:11]\n" \
X_"a[180:181], v37 offset:0\n" \
M_"a[24:27], a[132:135], a[168:171], a[24:27]\n" \
X_"a[182:183], v37 offset:1024\n" \
".fill (L_pb + 24288 - .) / 4, 4, 0xBF800000\n" \
"L_pp6:\n" \
"s_cbranch_execz L_pret\n" \
M_"a[24:27], a[196:199], a[232:235], a[24:27]\n" \
X_"a[244:245], v37 offset:8192\n" \
M_"a[28:31], a[172:175], a[132:135], a[28:31]\n" \
"s_cmpk_lt_u32 s3, 3\n" \
"s_cselect_b32 s16, s17, s19\n" \
M_"a[28:31], a[236:239], a[196:199], a[28:31]\n" \
"s_addk_i32 s84, 0x2000\n" \
M_"a[20:23], a[164:167], a[132:135], a[20:23]\n" \
B_"v25, s[76:79], s84 offen offset:1024 lds\n" \
M_"a[20:23], a[228:231], a[196:199], a[20:23]\n" \
X_"a[246:247], v37 offset:9216\n" \
"s_and_b32 s16, s16, 0xffff\n" \
M_"a[16:19], a[160:163], a[132:135], a[16:19]\n" \
X_"a[184:185], v38 offset:0\n" \
"v_writelane_b32 v11, s16, 16\n" \
M_"a[16:19], a[224:227], a[196:199], a[16:19]\n" \
X_"a[186:187], v38 offset:1024\n" \
"s_mul_i32 s16, s5, 0x48\n" \
M_"a[32:35], a[160:163], a[136:139], a[32:35]\n" \
"s_addk_i32 s84, 0x2000\n" \
"s_nop 0\n" \
M_"a[32:35], a[224:227], a[200:203], a[32:35]\n" \
B_"v24, s[76:79], s84 offen offset:2048 lds\n" \
M_"a[36:39], a[164:167], a[136:139], a[36:39]\n" \
X_"a[248:249], v38 offset:8192\n" \
"s_add_i32 s17, s16, 0x47\n" \
M_"a[36:39], a[228:231], a[200:203], a[36:39]\n" \
X_"a[250:251], v38 offset:9216\n" \
M_"a[44:47], a[172:175], a[136:139], a[44:47]\n" \
X_"a[188:189], v39 offset:0\n" \
M_"a[44:47], a[236:239], a[200:203], a[44:47]\n" \
X_"a[190:191], v39 offset:1024\n" \
M_"a[40:43], a[136:139], a[168:171], a[40:43]\n" \
"s_add_u32 s89, s41, 0x3000\n" \
"buffer_store_dwordx4 v[184:187], v20, s[80:83], s89 offen offset:3072 sc1\n" \
M_"a[40:43], a[200:203], a[232:235], a[40:43]\n" \
X_"a[252:253], v39 offset:8192\n" \
M_"a[56:59], a[140:143], a[168:171], a[56:59]\n" \
X_"a[254:255], v39 offset:9216\n" \
M_"a[56:59], a[204:207], a[232:235], a[56:59]\n" \
"s_cmpk_lt_u32 s3, 3\n" \
"s_cselect_b32 s17, s17, s18\n" \
M_"a[60:63], a[172:175], a[140:143], a[60:63]\n" \
"s_mulk_i32 s17, 0x480\n" \
"s_addk_i32 s17, 0x120\n" \
M_"a[60:63], a[236:239], a[204:207], a[60:63]\n" \
"s_addk_i32 s84, 0x2000\n" \
M_"a[52:55], a[164:167], a[140:143], a[52:55]\n" \
B_"v25, s[76:79], s84 offen offset:3072 lds\n" \
M_"a[52:55], a[228:231], a[204:207], a[52:55]\n" \
"v_writelane_b32 v11, s17, 17\n" \
"s_lshl_b32 s17, s3, 5\n" \
M_"a[48:51], a[160:163], a[140:143], a[48:51]\n" \
"s_add_i32 s16, s16, s17\n" \
"s_mulk_i32 s16, 0x480\n" \
M_"a[48:51], a[224:227], a[204:207], a[48:51]\n" \
"s_mul_i32 s17, s12, 0x24000\n" \
"s_add_i32 s18, s17, 0x1b000\n" \
"s_waitcnt lgkmcnt(15)\n" \
M_"a[64:67], a[164:167], v[248:251], a[64:67]\n" \
"s_cmpk_lt_u32 s3, 3\n" \
"s_cselect_b32 s16, s16, s18\n" \
M_"a[64:67], a[228:231], v[48:51], a[64:67]\n" \
"s_xor_b32 s18, s3, 2\n" \
"v_writelane_b32 v11, s16, 18\n" \
M_"a[80:83], a[172:175], v[248:251], a[80:83]\n" \
"s_mul_i32 s16, s15, 0x2400\n" \
"s_mul_i32 s15, s15, 0x9000\n" \
M_"a[80:83], a[236:239], v[48:51], a[80:83]\n" \
"s_bitset1_b32 s16, 17\n" \
"v_writelane_b32 v11, s16, 22\n" \
M_"a[68:71], v[248:251], a[168:171], a[68:71]\n" \
"s_add_i32 s17, s17, s15\n" \
"s_mul_i32 s15, s3, 0x2400\n" \
"s_waitcnt vmcnt(11) lgkmcnt(0)\n" \
M_"a[68:71], v[48:51], a[232:235], a[68:71]\n" \
"s_barrier\n" \
"v_xor_b32_e32 v28, 0x20000, v28\n" \
"v_xor_b32_e32 v29, 0x20000, v29\n" \
M_"v[52:55], a[176:179], a[128:131], v[52:55]\n" \
"s_add_u32 m0, s40, 0x0\n" \
"s_nop 0\n" \
"s_add_u32 s85, s39, 0x8000\n" \
M_"v[52:55], a[240:243], a[192:195], v[52:55]\n" \
B_"v20, s[80:83], s85 offen sc1 lds\n" \
M_"v[56:59], a[180:183], a[128:131], v[56:59]\n" \
X_"a[160:161], v32 offset:32768\n" \
"v_writelane_b32 v11, s17, 29\n" \
M_"v[56:59], a[244:247], a[192:195], v[56:59]\n" \
X_"a[162:163], v32 offset:33280\n" \
"s_add_i32 s16, s15, 0xa400\n" \
M_"v[60:63], a[184:187], a[128:131], v[60:63]\n" \
"s_add_u32 m0, s40, 0x1000\n" \
"s_add_u32 s85, s39, 0x9000\n" \
M_"v[60:63], a[248:251], a[192:195], v[60:63]\n" \
B_"v20, s[80:83], s85 offen sc1 lds\n" \
M_"v[64:67], a[188:191], a[128:131], v[64:67]\n" \
X_"a[224:225], v32 offset:36864\n" \
"s_mov_b32 s17, 0x8000\n" \
M_"v[64:67], a[252:255], a[192:195], v[64:67]\n" \
R_"a[128:131], v28 offset:0\n" \
M_"v[80:83], a[188:191], a[132:135], v[80:83]\n" \
R_"a[192:195], v29 offset:0\n" \
M_"v[80:83], a[252:255], a[196:199], v[80:83]\n" \
"s_add_u32 s88, s41, 0x3000\n" \
"buffer_store_dwordx4 v[188:191], v20, s[80:83], s88 offen offset:2048 sc1\n" \
M_"v[76:79], a[184:187], a[132:135], v[76:79]\n" \
X_"a[226:227], v32 offset:37376\n" \
M_"v[76:79], a[248:251], a[196:199], v[76:79]\n" \
X_"a[164:165], v33 offset:32768\n" \
M_"v[72:75], a[180:183], a[132:135], v[72:75]\n" \
"s_add_u32 m0, s40, 0x3000\n" \
"s_movk_i32 s84, 0x200\n" \
M_"v[72:75], a[244:247], a[196:199], v[72:75]\n" \
B_"v21, s[76:79], s84 offen offset:0 lds\n" \
M_"v[68:71], a[176:179], a[132:135], v[68:71]\n" \
X_"a[166:167], v33 offset:33280\n" \
M_"v[68:71], a[240:243], a[196:199], v[68:71]\n" \
R_"a[132:135], v28 offset:2048\n" \
M_"v[84:87], a[176:179], a[136:139], v[84:87]\n" \
R_"a[196:199], v29 offset:2048\n" \
M_"v[84:87], a[240:243], a[200:203], v[84:87]\n" \
"s_add_u32 m0, s31, 0x0\n" \
"s_mov_b32 s84, 0x200000\n" \
M_"v[88:91], a[180:183], a[136:139], v[88:91]\n" \
B_"v26, s[68:71], s84 offen offset:0 lds\n" \
M_"v[88:91], a[244:247], a[200:203], v[88:91]\n" \
X_"a[228:229], v33 offset:36864\n" \
M_"v[92:95], a[184:187], a[136:139], v[92:95]\n" \
X_"a[230:231], v33 offset:37376\n" \
M_"v[92:95], a[248:251], a[200:203], v[92:95]\n" \
"s_addk_i32 s84, 0x7c00\n" \
M_"v[96:99], a[188:191], a[136:139], v[96:99]\n" \
B_"v26, s[68:71], s84 offen offset:1024 lds\n" \
M_"v[96:99], a[252:255], a[200:203], v[96:99]\n" \
R_"a[136:139], v28 offset:4096\n" \
M_"v[112:115], a[188:191], a[140:143], v[112:115]\n" \
R_"a[200:203], v29 offset:4096\n" \
M_"v[112:115], a[252:255], a[204:207], v[112:115]\n" \
"s_addk_i32 s84, 0x7c00\n" \
M_"v[108:111], a[184:187], a[140:143], v[108:111]\n" \
B_"v27, s[68:71], s84 offen offset:2048 lds\n" \
M_"v[108:111], a[248:251], a[204:207], v[108:111]\n" \
X_"a[172:173], v35 offset:32768\n" \
M_"v[104:107], a[180:183], a[140:143], v[104:107]\n" \
X_"a[174:175], v35 offset:33280\n" \
M_"v[104:107], a[244:247], a[204:207], v[104:107]\n" \
X_"a[236:237], v35 offset:36864\n" \
M_"v[100:103], a[176:179], a[140:143], v[100:103]\n" \
"s_cmpk_lt_u32 s3, 3\n" \
"s_cselect_b32 s16, s16, s17\n" \
M_"v[100:103], a[240:243], a[204:207], v[100:103]\n" \
R_"a[140:143], v28 offset:6144\n" \
"s_mov_b32 s17, 0x1b000\n" \
M_"v[116:119], a[176:179], a[144:147], v[116:119]\n" \
R_"a[204:207], v29 offset:6144\n" \
M_"v[116:119], a[240:243], a[208:211], v[116:119]\n" \
"s_addk_i32 s84, 0x7c00\n" \
M_"v[120:123], a[180:183], a[144:147], v[120:123]\n" \
B_"v27, s[68:71], s84 offen offset:3072 lds\n" \
M_"v[120:123], a[244:247], a[208:211], v[120:123]\n" \
"s_add_u32 m0, s30, 0x20000\n" \
"s_movk_i32 s84, 0x280\n" \
M_"v[124:127], a[184:187], a[144:147], v[124:127]\n" \
B_"v24, s[76:79], s84 offen offset:0 lds\n" \
M_"v[124:127], a[248:251], a[208:211], v[124:127]\n" \
X_"a[238:239], v35 offset:37376\n" \
M_"v[128:131], a[188:191], a[144:147], v[128:131]\n" \
"s_add_u32 s89, s42, 0x0\n" \
"buffer_store_dwordx4 v[216:219], v20, s[80:83], s89 offen offset:0 sc1\n" \
M_"v[128:131], a[252:255], a[208:211], v[128:131]\n" \
R_"a[144:147], v28 offset:8192\n" \
M_"v[144:147], a[188:191], a[148:151], v[144:147]\n" \
R_"a[208:211], v29 offset:8192\n" \
M_"v[144:147], a[252:255], a[212:215], v[144:147]\n" \
X_"a[168:169], v34 offset:32768\n" \
M_"v[140:143], a[184:187], a[148:151], v[140:143]\n" \
X_"a[170:171], v34 offset:33280\n" \
M_"v[140:143], a[248:251], a[212:215], v[140:143]\n" \
"s_movk_i32 s84, 0x2280\n" \
M_"v[136:139], a[180:183], a[148:151], v[136:139]\n" \
B_"v25, s[76:79], s84 offen offset:1024 lds\n" \
M_"v[136:139], a[244:247], a[212:215], v[136:139]\n" \
X_"a[232:233], v34 offset:36864\n" \
M_"v[132:135], a[176:179], a[148:151], v[132:135]\n" \
X_"a[234:235], v34 offset:37376\n" \
M_"v[132:135], a[240:243], a[212:215], v[132:135]\n" \
R_"a[148:151], v28 offset:10240\n" \
M_"v[148:151], a[176:179], a[152:155], v[148:151]\n" \
R_"a[212:215], v29 offset:10240\n" \
M_"v[148:151], a[240:243], a[216:219], v[148:151]\n" \
"s_movk_i32 s84, 0x4280\n" \
M_"v[152:155], a[180:183], a[152:155], v[152:155]\n" \
B_"v24, s[76:79], s84 offen offset:2048 lds\n" \
M_"v[152:155], a[244:247], a[216:219], v[152:155]\n" \
"s_movk_i32 s84, 0x6280\n" \
M_"v[156:159], a[184:187], a[152:155], v[156:159]\n" \
B_"v25, s[76:79], s84 offen offset:3072 lds\n" \
M_"v[156:159], a[248:251], a[216:219], v[156:159]\n" \
R_"v[248:251], v22 offset:32768\n" \
M_"v[160:163], a[188:191], a[152:155], v[160:163]\n" \
R_"v[48:51], v23 offset:32768\n" \
M_"v[160:163], a[252:255], a[216:219], v[160:163]\n" \
R_"a[152:155], v28 offset:12288\n" \
M_"v[176:179], a[188:191], a[156:159], v[176:179]\n" \
R_"a[216:219], v29 offset:12288\n" \
M_"v[176:179], a[252:255], a[220:223], v[176:179]\n" \
"s_cmp_lg_u32 s18, 0\n" \
"s_cselect_b32 s17, s16, s17\n" \
M_"v[172:175], a[184:187], a[156:159], v[172:175]\n" \
"v_writelane_b32 v11, s16, 23\n" \
"s_or_b32 s16, s15, 0x10000\n" \
M_"v[172:175], a[248:251], a[220:223], v[172:175]\n" \
"v_writelane_b32 v11, s17, 24\n" \
"s_add_i32 s17, s15, 0x19000\n" \
M_"v[168:171], a[180:183], a[156:159], v[168:171]\n" \
"v_writelane_b32 v11, s16, 25\n" \
"s_mov_b32 s16, 0\n" \
M_"v[168:171], a[244:247], a[220:223], v[168:171]\n" \
"s_cmpk_lt_u32 s3, 3\n" \
"s_cselect_b32 s17, s17, s16\n" \
M_"v[164:167], a[176:179], a[156:159], v[164:167]\n" \
"s_bitset1_b32 s15, 17\n" \
"v_writelane_b32 v11, s17, 26\n" \
M_"v[164:167], a[240:243], a[220:223], v[164:167]\n" \
R_"a[156:159], v28 offset:14336\n" \
R_"a[220:223], v29 offset:14336\n" \
"s_waitcnt lgkmcnt(15)\n" \
M_"a[0:3], a[160:163], a[128:131], a[0:3]\n" \
"s_add_u32 m0, s30, 0x21000\n" \
"s_addk_i32 s84, 0x3000\n" \
M_"a[0:3], a[224:227], a[192:195], a[0:3]\n" \
B_"v24, s[76:79], s84 offen offset:0 lds\n" \
M_"a[4:7], a[164:167], a[128:131], a[4:7]\n" \
X_"a[176:177], v36 offset:32768\n" \
"s_movk_i32 s17, 0x2400\n" \
M_"a[4:7], a[228:231], a[192:195], a[4:7]\n" \
X_"a[178:179], v36 offset:33792\n" \
M_"a[12:15], a[172:175], a[128:131], a[12:15]\n" \
X_"a[240:241], v36 offset:40960\n" \
"s_waitcnt lgkmcnt(15)\n" \
M_"a[12:15], a[236:239], a[192:195], a[12:15]\n" \
"s_add_u32 s88, s42, 0x1000\n" \
"buffer_store_dwordx4 v[220:223], v20, s[80:83], s88 offen offset:0 sc1\n" \
"s_waitcnt lgkmcnt(13)\n" \
M_"a[8:11], a[128:131], a[168:171], a[8:11]\n" \
X_"a[242:243], v36 offset:41984\n" \
"s_waitcnt lgkmcnt(12)\n" \
M_"a[8:11], a[192:195], a[232:235], a[8:11]\n" \
X_"a[180:181], v37 offset:32768\n" \
M_"a[24:27], a[132:135], a[168:171], a[24:27]\n" \
X_"a[182:183], v37 offset:33792\n" \
M_"a[24:27], a[196:199], a[232:235], a[24:27]\n" \
X_"a[244:245], v37 offset:40960\n" \
M_"a[28:31], a[172:175], a[132:135], a[28:31]\n" \
"s_cmpk_lt_u32 s3, 3\n" \
"s_cselect_b32 s15, s15, s17\n" \
M_"a[28:31], a[236:239], a[196:199], a[28:31]\n" \
"s_addk_i32 s84, 0x2000\n" \
M_"a[20:23], a[164:167], a[132:135], a[20:23]\n" \
B_"v25, s[76:79], s84 offen offset:1024 lds\n" \
M_"a[20:23], a[228:231], a[196:199], a[20:23]\n" \
X_"a[246:247], v37 offset:41984\n" \
"v_writelane_b32 v11, s15, 27\n" \
M_"a[16:19], a[160:163], a[132:135], a[16:19]\n" \
X_"a[184:185], v38 offset:32768\n" \
"s_lshl_b32 s15, s3, 10\n" \
M_"a[16:19], a[224:227], a[196:199], a[16:19]\n" \
X_"a[186:187], v38 offset:33792\n" \
"s_addk_i32 s15, 0x4800\n" \
M_"a[32:35], a[160:163], a[136:139], a[32:35]\n" \
"s_addk_i32 s84, 0x2000\n" \
M_"a[32:35], a[224:227], a[200:203], a[32:35]\n" \
B_"v24, s[76:79], s84 offen offset:2048 lds\n" \
M_"a[36:39], a[164:167], a[136:139], a[36:39]\n" \
X_"a[248:249], v38 offset:40960\n" \
"v_writelane_b32 v11, s15, 28\n" \
M_"a[36:39], a[228:231], a[200:203], a[36:39]\n" \
X_"a[250:251], v38 offset:41984\n" \
"s_add_i32 s15, s14, 1\n" \
M_"a[44:47], a[172:175], a[136:139], a[44:47]\n" \
X_"a[188:189], v39 offset:32768\n" \
"s_and_b32 s15, s15, 3\n" \
M_"a[44:47], a[236:239], a[200:203], a[44:47]\n" \
X_"a[190:191], v39 offset:33792\n" \
"s_or_b32 s15, s15, s4\n" \
M_"a[40:43], a[136:139], a[168:171], a[40:43]\n" \
"s_add_u32 s89, s42, 0x0\n" \
"buffer_store_dwordx4 v[192:195], v20, s[80:83], s89 offen offset:2048 sc1\n" \
M_"a[40:43], a[200:203], a[232:235], a[40:43]\n" \
X_"a[252:253], v39 offset:40960\n" \
"s_lshl_b32 s17, s15, 2\n" \
M_"a[56:59], a[140:143], a[168:171], a[56:59]\n" \
X_"a[254:255], v39 offset:41984\n" \
"s_or_b32 s17, s17, s3\n" \
M_"a[56:59], a[204:207], a[232:235], a[56:59]\n" \
"s_mul_i32 s17, s17, 0x9000\n" \
"v_writelane_b32 v11, s17, 30\n" \
M_"a[60:63], a[172:175], a[140:143], a[60:63]\n" \
"s_lshl_b32 s17, s12, 2\n" \
"s_or_b32 s17, s17, s3\n" \
M_"a[60:63], a[236:239], a[204:207], a[60:63]\n" \
"s_addk_i32 s84, 0x2000\n" \
M_"a[52:55], a[164:167], a[140:143], a[52:55]\n" \
B_"v25, s[76:79], s84 offen offset:3072 lds\n" \
M_"a[52:55], a[228:231], a[204:207], a[52:55]\n" \
"s_add_i32 s14, s14, -1\n" \
"s_xor_b32 s17, s17, 8\n" \
M_"a[48:51], a[160:163], a[140:143], a[48:51]\n" \
"s_and_b32 s14, s14, 3\n" \
"s_mul_i32 s17, s17, 0x9000\n" \
M_"a[48:51], a[224:227], a[204:207], a[48:51]\n" \
"s_or_b32 s14, s14, s4\n" \
"v_writelane_b32 v11, s17, 31\n" \
"s_waitcnt lgkmcnt(15)\n" \
M_"a[64:67], a[164:167], v[248:251], a[64:67]\n" \
"s_lshl_b32 s17, s14, 2\n" \
"s_or_b32 s17, s17, s3\n" \
M_"a[64:67], a[228:231], v[48:51], a[64:67]\n" \
"s_mul_i32 s17, s17, 0x9000\n" \
"v_writelane_b32 v11, s17, 32\n" \
M_"a[80:83], a[172:175], v[248:251], a[80:83]\n" \
"s_or_b32 s17, s8, s13\n" \
"s_mul_i32 s17, s17, 0x24800\n" \
M_"a[80:83], a[236:239], v[48:51], a[80:83]\n" \
"s_lshl_b32 s15, s15, 12\n" \
"s_add_i32 s18, s17, 0x3004800\n" \
M_"a[68:71], v[248:251], a[168:171], a[68:71]\n" \
"s_lshl_b32 s12, s12, 12\n" \
"s_add_i32 s19, s18, s15\n" \
"s_waitcnt vmcnt(11) lgkmcnt(0)\n" \
M_"a[68:71], v[48:51], a[232:235], a[68:71]\n" \
"s_barrier\n" \
M_"v[52:55], a[176:179], a[128:131], v[52:55]\n" \
"s_add_u32 m0, s40, 0x8000\n" \
"s_add_u32 s85, s39, 0xa000\n" \
M_"v[52:55], a[240:243], a[192:195], v[52:55]\n" \
B_"v20, s[80:83], s85 offen sc1 lds\n" \
M_"v[56:59], a[180:183], a[128:131], v[56:59]\n" \
X_"a[160:161], v32 offset:0\n" \
"v_accvgpr_read_b32 v224, a120\n" \
M_"v[56:59], a[244:247], a[192:195], v[56:59]\n" \
X_"a[162:163], v32 offset:512\n" \
"v_accvgpr_read_b32 v225, a121\n" \
M_"v[60:63], a[184:187], a[128:131], v[60:63]\n" \
"s_add_u32 m0, s40, 0x9000\n" \
"s_add_u32 s85, s39, 0xb000\n" \
M_"v[60:63], a[248:251], a[192:195], v[60:63]\n" \
B_"v20, s[80:83], s85 offen sc1 lds\n" \
M_"v[64:67], a[188:191], a[128:131], v[64:67]\n" \
X_"a[224:225], v32 offset:4096\n" \
"v_accvgpr_read_b32 v226, a122\n" \
M_"v[64:67], a[252:255], a[192:195], v[64:67]\n" \
R_"a[128:131], v28 offset:32768\n" \
"v_accvgpr_read_b32 v227, a123\n" \
M_"v[80:83], a[188:191], a[132:135], v[80:83]\n" \
R_"a[192:195], v29 offset:32768\n" \
"v_accvgpr_read_b32 v180, a124\n" \
M_"v[80:83], a[252:255], a[196:199], v[80:83]\n" \
"s_add_u32 s88, s42, 0x0\n" \
"buffer_store_dwordx4 v[196:199], v20, s[80:83], s88 offen offset:3072 sc1\n" \
M_"v[76:79], a[184:187], a[132:135], v[76:79]\n" \
X_"a[226:227], v32 offset:4608\n" \
"v_accvgpr_read_b32 v181, a125\n" \
M_"v[76:79], a[248:251], a[196:199], v[76:79]\n" \
X_"a[164:165], v33 offset:0\n" \
"v_accvgpr_read_b32 v182, a126\n" \
M_"v[72:75], a[180:183], a[132:135], v[72:75]\n" \
"s_add_u32 m0, s40, 0xb000\n" \
"s_movk_i32 s84, 0x280\n" \
M_"v[72:75], a[244:247], a[196:199], v[72:75]\n" \
B_"v21, s[76:79], s84 offen offset:0 lds\n" \
M_"v[68:71], a[176:179], a[132:135], v[68:71]\n" \
X_"a[166:167], v33 offset:512\n" \
"v_accvgpr_read_b32 v183, a127\n" \
M_"v[68:71], a[240:243], a[196:199], v[68:71]\n" \
R_"a[132:135], v28 offset:34816\n" \
C_"v224, v224, v225\n" \
M_"v[84:87], a[176:179], a[136:139], v[84:87]\n" \
R_"a[196:199], v29 offset:34816\n" \
C_"v225, v226, v227\n" \
M_"v[84:87], a[240:243], a[200:203], v[84:87]\n" \
"s_add_u32 m0, s31, 0x8000\n" \
"s_mov_b32 s84, 0x280000\n" \
M_"v[88:91], a[180:183], a[136:139], v[88:91]\n" \
B_"v26, s[68:71], s84 offen offset:0 lds\n" \
M_"v[88:91], a[244:247], a[200:203], v[88:91]\n" \
X_"a[228:229], v33 offset:4096\n" \
C_"v226, v180, v181\n" \
M_"v[92:95], a[184:187], a[136:139], v[92:95]\n" \
X_"a[230:231], v33 offset:4608\n" \
C_"v227, v182, v183\n" \
M_"v[92:95], a[248:251], a[200:203], v[92:95]\n" \
"s_addk_i32 s84, 0x7c00\n" \
M_"v[96:99], a[188:191], a[136:139], v[96:99]\n" \
B_"v26, s[68:71], s84 offen offset:1024 lds\n" \
M_"v[96:99], a[252:255], a[200:203], v[96:99]\n" \
R_"a[136:139], v28 offset:36864\n" \
"v_accvgpr_read_b32 v220, a112\n" \
M_"v[112:115], a[188:191], a[140:143], v[112:115]\n" \
R_"a[200:203], v29 offset:36864\n" \
"v_accvgpr_read_b32 v221, a113\n" \
M_"v[112:115], a[252:255], a[204:207], v[112:115]\n" \
"s_addk_i32 s84, 0x7c00\n" \
M_"v[108:111], a[184:187], a[140:143], v[108:111]\n" \
B_"v27, s[68:71], s84 offen offset:2048 lds\n" \
M_"v[108:111], a[248:251], a[204:207], v[108:111]\n" \
X_"a[172:173], v35 offset:0\n" \
"v_accvgpr_read_b32 v222, a114\n" \
M_"v[104:107], a[180:183], a[140:143], v[104:107]\n" \
X_"a[174:175], v35 offset:512\n" \
"v_accvgpr_read_b32 v223, a115\n" \
M_"v[104:107], a[244:247], a[204:207], v[104:107]\n" \
X_"a[236:237], v35 offset:4096\n" \
"v_accvgpr_read_b32 v180, a116\n" \
M_"v[100:103], a[176:179], a[140:143], v[100:103]\n" \
"v_accvgpr_read_b32 v181, a117\n" \
"v_accvgpr_read_b32 v182, a118\n" \
M_"v[100:103], a[240:243], a[204:207], v[100:103]\n" \
R_"a[140:143], v28 offset:38912\n" \
"v_accvgpr_read_b32 v183, a119\n" \
M_"v[116:119], a[176:179], a[144:147], v[116:119]\n" \
R_"a[204:207], v29 offset:38912\n" \
C_"v220, v220, v221\n" \
M_"v[116:119], a[240:243], a[208:211], v[116:119]\n" \
"s_addk_i32 s84, 0x7c00\n" \
M_"v[120:123], a[180:183], a[144:147], v[120:123]\n" \
B_"v27, s[68:71], s84 offen offset:3072 lds\n" \
M_"v[120:123], a[244:247], a[208:211], v[120:123]\n" \
"s_add_u32 m0, s30, 0x0\n" \
"s_movk_i32 s84, 0x300\n" \
"s_nop 0\n" \
M_"v[124:127], a[184:187], a[144:147], v[124:127]\n" \
B_"v24, s[76:79], s84 offen offset:0 lds\n" \
M_"v[124:127], a[248:251], a[208:211], v[124:127]\n" \
X_"a[238:239], v35 offset:4608\n" \
C_"v221, v222, v223\n" \
M_"v[128:131], a[188:191], a[144:147], v[128:131]\n" \
"s_add_u32 s89, s42, 0x1000\n" \
"buffer_store_dwordx4 a[84:87], v20, s[80:83], s89 offen offset:3072 sc1\n" \
M_"v[128:131], a[252:255], a[208:211], v[128:131]\n" \
R_"a[144:147], v28 offset:40960\n" \
C_"v222, v180, v181\n" \
M_"v[144:147], a[188:191], a[148:151], v[144:147]\n" \
R_"a[208:211], v29 offset:40960\n" \
C_"v223, v182, v183\n" \
M_"v[144:147], a[252:255], a[212:215], v[144:147]\n" \
X_"a[168:169], v34 offset:0\n" \
"v_accvgpr_read_b32 v216, a104\n" \
M_"v[140:143], a[184:187], a[148:151], v[140:143]\n" \
X_"a[170:171], v34 offset:512\n" \
"v_accvgpr_read_b32 v217, a105\n" \
M_"v[140:143], a[248:251], a[212:215], v[140:143]\n" \
"s_movk_i32 s84, 0x2300\n" \
"s_nop 0\n" \
M_"v[136:139], a[180:183], a[148:151], v[136:139]\n" \
B_"v25, s[76:79], s84 offen offset:1024 lds\n" \
M_"v[136:139], a[244:247], a[212:215], v[136:139]\n" \
X_"a[232:233], v34 offset:4096\n" \
"v_accvgpr_read_b32 v218, a106\n" \
M_"v[132:135], a[176:179], a[148:151], v[132:135]\n" \
X_"a[234:235], v34 offset:4608\n" \
"v_accvgpr_read_b32 v219, a107\n" \
M_"v[132:135], a[240:243], a[212:215], v[132:135]\n" \
R_"a[148:151], v28 offset:43008\n" \
"v_accvgpr_read_b32 v180, a108\n" \
M_"v[148:151], a[176:179], a[152:155], v[148:151]\n" \
R_"a[212:215], v29 offset:43008\n" \
"v_accvgpr_read_b32 v181, a109\n" \
M_"v[148:151], a[240:243], a[216:219], v[148:151]\n" \
"s_movk_i32 s84, 0x4300\n" \
M_"v[152:155], a[180:183], a[152:155], v[152:155]\n" \
B_"v24, s[76:79], s84 offen offset:2048 lds\n" \
M_"v[152:155], a[244:247], a[216:219], v[152:155]\n" \
"s_movk_i32 s84, 0x6300\n" \
M_"v[156:159], a[184:187], a[152:155], v[156:159]\n" \
B_"v25, s[76:79], s84 offen offset:3072 lds\n" \
M_"v[156:159], a[248:251], a[216:219], v[156:159]\n" \
R_"v[248:251], v22 offset:0\n" \
"v_accvgpr_read_b32 v182, a110\n" \
M_"v[160:163], a[188:191], a[152:155], v[160:163]\n" \
R_"v[48:51], v23 offset:0\n" \
"v_accvgpr_read_b32 v183, a111\n" \
M_"v[160:163], a[252:255], a[216:219], v[160:163]\n" \
R_"a[152:155], v28 offset:45056\n" \
C_"v216, v216, v217\n" \
M_"v[176:179], a[188:191], a[156:159], v[176:179]\n" \
R_"a[216:219], v29 offset:45056\n" \
C_"v217, v218, v219\n" \
M_"v[176:179], a[252:255], a[220:223], v[176:179]\n" \
C_"v218, v180, v181\n" \
C_"v219, v182, v183\n" \
M_"v[172:175], a[184:187], a[156:159], v[172:175]\n" \
"v_accvgpr_read_b32 v192, a96\n" \
"v_accvgpr_read_b32 v193, a97\n" \
M_"v[172:175], a[248:251], a[220:223], v[172:175]\n" \
"v_accvgpr_read_b32 v194, a98\n" \
"v_accvgpr_read_b32 v195, a99\n" \
M_"v[168:171], a[180:183], a[156:159], v[168:171]\n" \
"v_accvgpr_read_b32 v180, a100\n" \
"v_accvgpr_read_b32 v181, a101\n" \
M_"v[168:171], a[244:247], a[220:223], v[168:171]\n" \
"v_accvgpr_read_b32 v182, a102\n" \
"v_accvgpr_read_b32 v183, a103\n" \
M_"v[164:167], a[176:179], a[156:159], v[164:167]\n" \
C_"v192, v192, v193\n" \
C_"v193, v194, v195\n" \
M_"v[164:167], a[240:243], a[220:223], v[164:167]\n" \
R_"a[156:159], v28 offset:47104\n" \
R_"a[220:223], v29 offset:47104\n" \
"s_waitcnt lgkmcnt(15)\n" \
M_"a[0:3], a[160:163], a[128:131], a[0:3]\n" \
"s_add_u32 m0, s30, 0x1000\n" \
"s_addk_i32 s84, 0x3000\n" \
M_"a[0:3], a[224:227], a[192:195], a[0:3]\n" \
B_"v24, s[76:79], s84 offen offset:0 lds\n" \
M_"a[4:7], a[164:167], a[128:131], a[4:7]\n" \
X_"a[176:177], v36 offset:0\n" \
C_"v194, v180, v181\n" \
M_"a[4:7], a[228:231], a[192:195], a[4:7]\n" \
X_"a[178:179], v36 offset:1024\n" \
C_"v195, v182, v183\n" \
M_"a[12:15], a[172:175], a[128:131], a[12:15]\n" \
X_"a[240:241], v36 offset:8192\n" \
"v_accvgpr_read_b32 v188, a88\n" \
"s_waitcnt lgkmcnt(15)\n" \
M_"a[12:15], a[236:239], a[192:195], a[12:15]\n" \
"s_add_u32 s88, s42, 0x1000\n" \
"buffer_store_dwordx4 v[252:255], v20, s[80:83], s88 offen offset:2048 sc1\n" \
"s_waitcnt lgkmcnt(13)\n" \
M_"a[8:11], a[128:131], a[168:171], a[8:11]\n" \
X_"a[242:243], v36 offset:9216\n" \
"v_accvgpr_read_b32 v189, a89\n" \
"s_waitcnt lgkmcnt(12)\n" \
"s_nop 0\n" \
M_"a[8:11], a[192:195], a[232:235], a[8:11]\n" \
X_"a[180:181], v37 offset:0\n" \
"v_accvgpr_read_b32 v190, a90\n" \
M_"a[24:27], a[132:135], a[168:171], a[24:27]\n" \
X_"a[182:183], v37 offset:1024\n" \
"v_accvgpr_read_b32 v191, a91\n" \
M_"a[24:27], a[196:199], a[232:235], a[24:27]\n" \
X_"a[244:245], v37 offset:8192\n" \
"v_accvgpr_read_b32 v180, a92\n" \
M_"a[28:31], a[172:175], a[132:135], a[28:31]\n" \
"v_accvgpr_read_b32 v181, a93\n" \
"v_accvgpr_read_b32 v182, a94\n" \
M_"a[28:31], a[236:239], a[196:199], a[28:31]\n" \
"s_addk_i32 s84, 0x2000\n" \
"s_nop 0\n" \
M_"a[20:23], a[164:167], a[132:135], a[20:23]\n" \
B_"v25, s[76:79], s84 offen offset:1024 lds\n" \
M_"a[20:23], a[228:231], a[196:199], a[20:23]\n" \
X_"a[246:247], v37 offset:9216\n" \
"v_accvgpr_read_b32 v183, a95\n" \
M_"a[16:19], a[160:163], a[132:135], a[16:19]\n" \
X_"a[184:185], v38 offset:0\n" \
C_"v188, v188, v189\n" \
M_"a[16:19], a[224:227], a[196:199], a[16:19]\n" \
".fill (L_pb + 28384 - .) / 4, 4, 0xBF800000\n" \
"L_pp7:\n" \
"s_cbranch_execz L_pret\n" \
X_"a[186:187], v38 offset:1024\n" \
C_"v189, v190, v191\n" \
M_"a[32:35], a[160:163], a[136:139], a[32:35]\n" \
"s_addk_i32 s84, 0x2000\n" \
M_"a[32:35], a[224:227], a[200:203], a[32:35]\n" \
B_"v24, s[76:79], s84 offen offset:2048 lds\n" \
M_"a[36:39], a[164:167], a[136:139], a[36:39]\n" \
X_"a[248:249], v38 offset:8192\n" \
C_"v190, v180, v181\n" \
M_"a[36:39], a[228:231], a[200:203], a[36:39]\n" \
X_"a[250:251], v38 offset:9216\n" \
C_"v191, v182, v183\n" \
M_"a[44:47], a[172:175], a[136:139], a[44:47]\n" \
X_"a[188:189], v39 offset:0\n" \
"v_accvgpr_read_b32 v184, a72\n" \
M_"a[44:47], a[236:239], a[200:203], a[44:47]\n" \
X_"a[190:191], v39 offset:1024\n" \
"v_accvgpr_read_b32 v185, a73\n" \
M_"a[40:43], a[136:139], a[168:171], a[40:43]\n" \
"s_add_u32 s89, s42, 0x2000\n" \
"buffer_store_dwordx4 v[200:203], v20, s[80:83], s89 offen offset:2048 sc1\n" \
M_"a[40:43], a[200:203], a[232:235], a[40:43]\n" \
X_"a[252:253], v39 offset:8192\n" \
"v_accvgpr_read_b32 v186, a74\n" \
M_"a[56:59], a[140:143], a[168:171], a[56:59]\n" \
X_"a[254:255], v39 offset:9216\n" \
"v_accvgpr_read_b32 v187, a75\n" \
M_"a[56:59], a[204:207], a[232:235], a[56:59]\n" \
"v_accvgpr_read_b32 v180, a76\n" \
"v_accvgpr_read_b32 v181, a77\n" \
M_"a[60:63], a[172:175], a[140:143], a[60:63]\n" \
"v_accvgpr_read_b32 v182, a78\n" \
"v_accvgpr_read_b32 v183, a79\n" \
M_"a[60:63], a[236:239], a[204:207], a[60:63]\n" \
"s_addk_i32 s84, 0x2000\n" \
"s_nop 0\n" \
M_"a[52:55], a[164:167], a[140:143], a[52:55]\n" \
B_"v25, s[76:79], s84 offen offset:3072 lds\n" \
M_"a[52:55], a[228:231], a[204:207], a[52:55]\n" \
C_"v184, v184, v185\n" \
C_"v185, v186, v187\n" \
M_"a[48:51], a[160:163], a[140:143], a[48:51]\n" \
C_"v186, v180, v181\n" \
C_"v187, v182, v183\n" \
M_"a[48:51], a[224:227], a[204:207], a[48:51]\n" \
"v_writelane_b32 v11, s19, 33\n" \
"s_xor_b32 s12, s12, 0x2000\n" \
"s_waitcnt lgkmcnt(15)\n" \
M_"a[64:67], a[164:167], v[248:251], a[64:67]\n" \
"s_lshl_b32 s14, s14, 12\n" \
"s_sub_i32 s13, s8, s13\n" \
M_"a[64:67], a[228:231], v[48:51], a[64:67]\n" \
"s_add_i32 s19, s18, s12\n" \
"v_writelane_b32 v11, s19, 34\n" \
M_"a[80:83], a[172:175], v[248:251], a[80:83]\n" \
"s_add_i32 s18, s18, s14\n" \
"s_mul_i32 s13, s13, 0x24800\n" \
M_"a[80:83], a[236:239], v[48:51], a[80:83]\n" \
"v_writelane_b32 v11, s18, 35\n" \
"s_add_i32 s18, s13, 0x4120800\n" \
M_"a[68:71], v[248:251], a[168:171], a[68:71]\n" \
"s_add_i32 s15, s18, s15\n" \
"v_writelane_b32 v11, s15, 36\n" \
"s_waitcnt vmcnt(11) lgkmcnt(0)\n" \
M_"a[68:71], v[48:51], a[232:235], a[68:71]\n" \
"s_barrier\n" \
"v_mov_b32_e32 v45, s62\n" \
"s_mov_b64 exec, 1\n" \
"buffer_store_dword v45, off, s[80:83], s37 offset:0 sc1\n" \
"s_mov_b64 exec, -1\n" \
"v_xor_b32_e32 v28, 0x20000, v28\n" \
"v_xor_b32_e32 v29, 0x20000, v29\n" \
M_"v[52:55], a[176:179], a[128:131], v[52:55]\n" \
"s_add_u32 m0, s40, 0x0\n" \
"s_add_u32 s85, s39, 0xc000\n" \
M_"v[52:55], a[240:243], a[192:195], v[52:55]\n" \
B_"v20, s[80:83], s85 offen sc1 lds\n" \
M_"v[56:59], a[180:183], a[128:131], v[56:59]\n" \
X_"a[160:161], v32 offset:32768\n" \
"s_add_i32 s12, s18, s12\n" \
M_"v[56:59], a[244:247], a[192:195], v[56:59]\n" \
X_"a[162:163], v32 offset:33280\n" \
"v_writelane_b32 v11, s12, 37\n" \
M_"v[60:63], a[184:187], a[128:131], v[60:63]\n" \
"s_add_u32 m0, s40, 0x1000\n" \
"s_add_u32 s85, s39, 0xd000\n" \
M_"v[60:63], a[248:251], a[192:195], v[60:63]\n" \
B_"v20, s[80:83], s85 offen sc1 lds\n" \
M_"v[64:67], a[188:191], a[128:131], v[64:67]\n" \
X_"a[224:225], v32 offset:36864\n" \
"s_add_i32 s18, s18, s14\n" \
M_"v[64:67], a[252:255], a[192:195], v[64:67]\n" \
R_"a[128:131], v28 offset:0\n" \
"s_or_b32 s12, s17, 0x3000000\n" \
M_"v[80:83], a[188:191], a[132:135], v[80:83]\n" \
R_"a[192:195], v29 offset:0\n" \
"s_lshl_b32 s14, s5, 11\n" \
M_"v[80:83], a[252:255], a[196:199], v[80:83]\n" \
"s_add_u32 m0, s40, 0x3000\n" \
"s_movk_i32 s84, 0x300\n" \
M_"v[76:79], a[184:187], a[132:135], v[76:79]\n" \
B_"v21, s[76:79], s84 offen offset:0 lds\n" \
M_"v[76:79], a[248:251], a[196:199], v[76:79]\n" \
X_"a[226:227], v32 offset:37376\n" \
"v_writelane_b32 v11, s18, 38\n" \
M_"v[72:75], a[180:183], a[132:135], v[72:75]\n" \
X_"a[164:165], v33 offset:32768\n" \
"s_add_i32 s15, s12, s14\n" \
M_"v[72:75], a[244:247], a[196:199], v[72:75]\n" \
"v_writelane_b32 v11, s15, 39\n" \
"s_add_i32 s15, s13, 0x411c000\n" \
M_"v[68:71], a[176:179], a[132:135], v[68:71]\n" \
X_"a[166:167], v33 offset:33280\n" \
"s_add_i32 s18, s15, s14\n" \
M_"v[68:71], a[240:243], a[196:199], v[68:71]\n" \
R_"a[132:135], v28 offset:2048\n" \
"v_writelane_b32 v11, s18, 40\n" \
M_"v[84:87], a[176:179], a[136:139], v[84:87]\n" \
R_"a[196:199], v29 offset:2048\n" \
"s_add_i32 s17, s17, 0x3004000\n" \
M_"v[84:87], a[240:243], a[200:203], v[84:87]\n" \
"s_add_u32 m0, s31, 0x0\n" \
"s_nop 0\n" \
"s_mov_b32 s84, 0x300000\n" \
M_"v[88:91], a[180:183], a[136:139], v[88:91]\n" \
B_"v26, s[68:71], s84 offen offset:0 lds\n" \
M_"v[88:91], a[244:247], a[200:203], v[88:91]\n" \
"s_add_u32 s88, s42, 0x2000\n" \
"buffer_store_dwordx4 v[204:207], v20, s[80:83], s88 offen offset:3072 sc1\n" \
M_"v[92:95], a[184:187], a[136:139], v[92:95]\n" \
X_"a[228:229], v33 offset:36864\n" \
"v_writelane_b32 v11, s17, 41\n" \
M_"v[92:95], a[248:251], a[200:203], v[92:95]\n" \
X_"a[230:231], v33 offset:37376\n" \
"s_add_i32 s13, s13, 0x4120000\n" \
M_"v[96:99], a[188:191], a[136:139], v[96:99]\n" \
"v_writelane_b32 v11, s13, 42\n" \
"s_lshl_b32 s13, s9, 2\n" \
M_"v[96:99], a[252:255], a[200:203], v[96:99]\n" \
R_"a[136:139], v28 offset:4096\n" \
"s_or_b32 s7, s13, s7\n" \
M_"v[112:115], a[188:191], a[140:143], v[112:115]\n" \
R_"a[200:203], v29 offset:4096\n" \
"s_mul_i32 s7, s7, 0x90000\n" \
M_"v[112:115], a[252:255], a[204:207], v[112:115]\n" \
"s_addk_i32 s84, 0x7c00\n" \
M_"v[108:111], a[184:187], a[140:143], v[108:111]\n" \
B_"v26, s[68:71], s84 offen offset:1024 lds\n" \
M_"v[108:111], a[248:251], a[204:207], v[108:111]\n" \
X_"a[172:173], v35 offset:32768\n" \
"s_lshl_b32 s13, s3, 14\n" \
M_"v[104:107], a[180:183], a[140:143], v[104:107]\n" \
"s_addk_i32 s84, 0x7c00\n" \
M_"v[104:107], a[244:247], a[204:207], v[104:107]\n" \
B_"v27, s[68:71], s84 offen offset:2048 lds\n" \
M_"v[100:103], a[176:179], a[140:143], v[100:103]\n" \
X_"a[174:175], v35 offset:33280\n" \
"s_lshl_b32 s9, s9, 7\n" \
M_"v[100:103], a[240:243], a[204:207], v[100:103]\n" \
R_"a[140:143], v28 offset:6144\n" \
"s_lshl_b32 s8, s8, 4\n" \
M_"v[116:119], a[176:179], a[144:147], v[116:119]\n" \
R_"a[204:207], v29 offset:6144\n" \
"s_add_i32 s7, s7, s13\n" \
M_"v[116:119], a[240:243], a[208:211], v[116:119]\n" \
"s_addk_i32 s84, 0x7c00\n" \
M_"v[120:123], a[180:183], a[144:147], v[120:123]\n" \
B_"v27, s[68:71], s84 offen offset:3072 lds\n" \
M_"v[120:123], a[244:247], a[208:211], v[120:123]\n" \
X_"a[236:237], v35 offset:36864\n" \
"s_or_b32 s13, s8, s9\n" \
M_"v[124:127], a[184:187], a[144:147], v[124:127]\n" \
X_"a[238:239], v35 offset:37376\n" \
"s_sub_i32 s8, s8, s9\n" \
M_"v[124:127], a[248:251], a[208:211], v[124:127]\n" \
"s_add_u32 m0, s30, 0x8000\n" \
"s_movk_i32 s84, 0x380\n" \
M_"v[128:131], a[188:191], a[144:147], v[128:131]\n" \
B_"v24, s[76:79], s84 offen offset:0 lds\n" \
M_"v[128:131], a[252:255], a[208:211], v[128:131]\n" \
R_"a[144:147], v28 offset:8192\n" \
"s_mulk_i32 s13, 0x480\n" \
M_"v[144:147], a[188:191], a[148:151], v[144:147]\n" \
R_"a[208:211], v29 offset:8192\n" \
"s_mulk_i32 s8, 0x480\n" \
M_"v[144:147], a[252:255], a[212:215], v[144:147]\n" \
X_"a[168:169], v34 offset:32768\n" \
"s_or_b32 s13, s13, s11\n" \
M_"v[140:143], a[184:187], a[148:151], v[140:143]\n" \
"s_movk_i32 s84, 0x2380\n" \
M_"v[140:143], a[248:251], a[212:215], v[140:143]\n" \
B_"v25, s[76:79], s84 offen offset:1024 lds\n" \
M_"v[136:139], a[180:183], a[148:151], v[136:139]\n" \
X_"a[170:171], v34 offset:33280\n" \
"s_or_b32 s8, s8, s11\n" \
M_"v[136:139], a[244:247], a[212:215], v[136:139]\n" \
X_"a[232:233], v34 offset:36864\n" \
"s_and_b32 s11, s2, 1\n" \
M_"v[132:135], a[176:179], a[148:151], v[132:135]\n" \
X_"a[234:235], v34 offset:37376\n" \
M_"v[132:135], a[240:243], a[212:215], v[132:135]\n" \
R_"a[148:151], v28 offset:10240\n" \
M_"v[148:151], a[176:179], a[152:155], v[148:151]\n" \
R_"a[212:215], v29 offset:10240\n" \
M_"v[148:151], a[240:243], a[216:219], v[148:151]\n" \
"s_movk_i32 s84, 0x4380\n" \
M_"v[152:155], a[180:183], a[152:155], v[152:155]\n" \
B_"v24, s[76:79], s84 offen offset:2048 lds\n" \
M_"v[152:155], a[244:247], a[216:219], v[152:155]\n" \
"s_movk_i32 s84, 0x6380\n" \
M_"v[156:159], a[184:187], a[152:155], v[156:159]\n" \
B_"v25, s[76:79], s84 offen offset:3072 lds\n" \
M_"v[156:159], a[248:251], a[216:219], v[156:159]\n" \
R_"v[248:251], v22 offset:32768\n" \
M_"v[160:163], a[188:191], a[152:155], v[160:163]\n" \
R_"v[48:51], v23 offset:32768\n" \
M_"v[160:163], a[252:255], a[216:219], v[160:163]\n" \
R_"a[152:155], v28 offset:12288\n" \
M_"v[176:179], a[188:191], a[156:159], v[176:179]\n" \
R_"a[216:219], v29 offset:12288\n" \
M_"v[176:179], a[252:255], a[220:223], v[176:179]\n" \
"s_cmp_lg_u32 s11, 0\n" \
"s_cselect_b32 s12, s15, s12\n" \
M_"v[172:175], a[184:187], a[156:159], v[172:175]\n" \
"s_bfe_u32 s9, s2, 0x20001\n" \
"s_add_i32 s15, s7, 0x4240000\n" \
M_"v[172:175], a[248:251], a[220:223], v[172:175]\n" \
"s_addk_i32 s12, 0x4000\n" \
"s_lshl_b32 s5, s5, 16\n" \
M_"v[168:171], a[180:183], a[156:159], v[168:171]\n" \
"s_xor_b32 s9, s9, s3\n" \
M_"v[168:171], a[244:247], a[220:223], v[168:171]\n" \
"s_cmp_lg_u32 s9, 0\n" \
"s_cselect_b32 s12, s6, s12\n" \
M_"v[164:167], a[176:179], a[156:159], v[164:167]\n" \
"s_add_i32 s5, s15, s5\n" \
"v_writelane_b32 v11, s12, 43\n" \
M_"v[164:167], a[240:243], a[220:223], v[164:167]\n" \
R_"a[156:159], v28 offset:14336\n" \
R_"a[220:223], v29 offset:14336\n" \
"s_waitcnt lgkmcnt(15)\n" \
M_"a[0:3], a[160:163], a[128:131], a[0:3]\n" \
"s_add_u32 m0, s30, 0x9000\n" \
"s_addk_i32 s84, 0x3000\n" \
M_"a[0:3], a[224:227], a[192:195], a[0:3]\n" \
B_"v24, s[76:79], s84 offen offset:0 lds\n" \
M_"a[4:7], a[164:167], a[128:131], a[4:7]\n" \
X_"a[176:177], v36 offset:32768\n" \
"s_add_i32 s7, s7, 0x42c0000\n" \
M_"a[4:7], a[228:231], a[192:195], a[4:7]\n" \
X_"a[178:179], v36 offset:33792\n" \
"v_writelane_b32 v11, s5, 44\n" \
M_"a[12:15], a[172:175], a[128:131], a[12:15]\n" \
X_"a[240:241], v36 offset:40960\n" \
"s_lshl_b32 s5, s11, 10\n" \
"s_waitcnt lgkmcnt(15)\n" \
M_"a[12:15], a[236:239], a[192:195], a[12:15]\n" \
"s_add_u32 s89, s42, 0x3000\n" \
"buffer_store_dwordx4 v[208:211], v20, s[80:83], s89 offen offset:3072 sc1\n" \
"s_waitcnt lgkmcnt(13)\n" \
M_"a[8:11], a[128:131], a[168:171], a[8:11]\n" \
X_"a[242:243], v36 offset:41984\n" \
"s_or_b32 s5, s7, s5\n" \
"s_waitcnt lgkmcnt(12)\n" \
M_"a[8:11], a[192:195], a[232:235], a[8:11]\n" \
X_"a[180:181], v37 offset:32768\n" \
M_"a[24:27], a[132:135], a[168:171], a[24:27]\n" \
X_"a[182:183], v37 offset:33792\n" \
M_"a[24:27], a[196:199], a[232:235], a[24:27]\n" \
X_"a[244:245], v37 offset:40960\n" \
M_"a[28:31], a[172:175], a[132:135], a[28:31]\n" \
"s_cmp_lg_u32 s9, 0\n" \
"s_cselect_b32 s5, s6, s5\n" \
M_"a[28:31], a[236:239], a[196:199], a[28:31]\n" \
"s_addk_i32 s84, 0x2000\n" \
M_"a[20:23], a[164:167], a[132:135], a[20:23]\n" \
B_"v25, s[76:79], s84 offen offset:1024 lds\n" \
M_"a[20:23], a[228:231], a[196:199], a[20:23]\n" \
X_"a[246:247], v37 offset:41984\n" \
"s_add_i32 s8, s8, 0x21c000\n" \
M_"a[16:19], a[160:163], a[132:135], a[16:19]\n" \
X_"a[184:185], v38 offset:32768\n" \
"v_writelane_b32 v11, s5, 45\n" \
M_"a[16:19], a[224:227], a[196:199], a[16:19]\n" \
X_"a[186:187], v38 offset:33792\n" \
"s_add_i32 s5, s13, s10\n" \
M_"a[32:35], a[160:163], a[136:139], a[32:35]\n" \
"s_addk_i32 s84, 0x2000\n" \
M_"a[32:35], a[224:227], a[200:203], a[32:35]\n" \
B_"v24, s[76:79], s84 offen offset:2048 lds\n" \
M_"a[36:39], a[164:167], a[136:139], a[36:39]\n" \
X_"a[248:249], v38 offset:40960\n" \
"v_writelane_b32 v11, s5, 46\n" \
M_"a[36:39], a[228:231], a[200:203], a[36:39]\n" \
X_"a[250:251], v38 offset:41984\n" \
"s_add_i32 s5, s8, s10\n" \
"s_nop 0\n" \
M_"a[44:47], a[172:175], a[136:139], a[44:47]\n" \
X_"a[188:189], v39 offset:32768\n" \
"v_writelane_b32 v11, s5, 47\n" \
M_"a[44:47], a[236:239], a[200:203], a[44:47]\n" \
X_"a[190:191], v39 offset:33792\n" \
M_"a[40:43], a[136:139], a[168:171], a[40:43]\n" \
"s_add_u32 s88, s42, 0x3000\n" \
"buffer_store_dwordx4 v[212:215], v20, s[80:83], s88 offen offset:2048 sc1\n" \
M_"a[40:43], a[200:203], a[232:235], a[40:43]\n" \
X_"a[252:253], v39 offset:40960\n" \
M_"a[56:59], a[140:143], a[168:171], a[56:59]\n" \
X_"a[254:255], v39 offset:41984\n" \
M_"a[56:59], a[204:207], a[232:235], a[56:59]\n" \
"s_cmp_lg_u32 s11, 0\n" \
"s_cselect_b32 s5, s8, s13\n" \
M_"a[60:63], a[172:175], a[140:143], a[60:63]\n" \
"s_addk_i32 s5, 0x100\n" \
M_"a[60:63], a[236:239], a[204:207], a[60:63]\n" \
"s_addk_i32 s84, 0x2000\n" \
M_"a[52:55], a[164:167], a[140:143], a[52:55]\n" \
B_"v25, s[76:79], s84 offen offset:3072 lds\n" \
M_"a[52:55], a[228:231], a[204:207], a[52:55]\n" \
"s_cmp_lg_u32 s9, 0\n" \
"s_cselect_b32 s5, s6, s5\n" \
M_"a[48:51], a[160:163], a[140:143], a[48:51]\n" \
"v_writelane_b32 v11, s5, 48\n" \
"s_or_b32 s5, s7, s14\n" \
M_"a[48:51], a[224:227], a[204:207], a[48:51]\n" \
"v_writelane_b32 v11, s5, 56\n" \
"s_lshl_b32 s5, s3, 17\n" \
"s_waitcnt lgkmcnt(15)\n" \
M_"a[64:67], a[164:167], v[248:251], a[64:67]\n" \
"s_add_i32 s5, s15, s5\n" \
"s_or_b32 s4, s5, s4\n" \
M_"a[64:67], a[228:231], v[48:51], a[64:67]\n" \
"s_or_b32 s4, s4, 0x3840\n" \
"v_writelane_b32 v11, s4, 6\n" \
M_"a[80:83], a[172:175], v[248:251], a[80:83]\n" \
"s_mov_b32 s4, 0xff0000\n" \
M_"a[80:83], a[236:239], v[48:51], a[80:83]\n" \
"s_cmp_lg_u32 s9, 0\n" \
"s_cselect_b32 s4, s16, s4\n" \
M_"a[68:71], v[248:251], a[168:171], a[68:71]\n" \
"v_writelane_b32 v11, s4, 7\n" \
"s_lshl_b32 s4, s2, 16\n" \
"s_waitcnt vmcnt(10) lgkmcnt(0)\n" \
M_"a[68:71], v[48:51], a[232:235], a[68:71]\n" \
"s_barrier\n" \
"v_xor_b32_e32 v28, 0x20000, v28\n" \
"v_xor_b32_e32 v29, 0x20000, v29\n" \
M_"v[52:55], a[176:179], a[128:131], v[52:55]\n" \
"s_add_u32 m0, s40, 0x8000\n" \
"s_add_u32 s85, s39, 0xe000\n" \
M_"v[52:55], a[240:243], a[192:195], v[52:55]\n" \
B_"v20, s[80:83], s85 offen sc1 lds\n" \
M_"v[56:59], a[180:183], a[128:131], v[56:59]\n" \
X_"a[160:161], v32 offset:0\n" \
"s_add_i32 s5, s4, 0x10000\n" \
M_"v[56:59], a[244:247], a[192:195], v[56:59]\n" \
X_"a[162:163], v32 offset:512\n" \
"s_and_b32 s5, s5, 0x70000\n" \
M_"v[60:63], a[184:187], a[128:131], v[60:63]\n" \
"s_add_u32 m0, s40, 0x9000\n" \
"s_add_u32 s85, s39, 0xf000\n" \
M_"v[60:63], a[248:251], a[192:195], v[60:63]\n" \
B_"v20, s[80:83], s85 offen sc1 lds\n" \
M_"v[64:67], a[188:191], a[128:131], v[64:67]\n" \
X_"a[224:225], v32 offset:4096\n" \
"s_add_i32 s5, s15, s5\n" \
M_"v[64:67], a[252:255], a[192:195], v[64:67]\n" \
R_"a[128:131], v28 offset:0\n" \
"v_writelane_b32 v11, s5, 49\n" \
M_"v[80:83], a[188:191], a[132:135], v[80:83]\n" \
R_"a[192:195], v29 offset:0\n" \
"s_add_i32 s5, s4, 0x20000\n" \
M_"v[80:83], a[252:255], a[196:199], v[80:83]\n" \
"s_add_u32 m0, s40, 0xb000\n" \
"s_movk_i32 s84, 0x380\n" \
M_"v[76:79], a[184:187], a[132:135], v[76:79]\n" \
B_"v21, s[76:79], s84 offen offset:0 lds\n" \
M_"v[76:79], a[248:251], a[196:199], v[76:79]\n" \
X_"a[226:227], v32 offset:4608\n" \
"s_and_b32 s5, s5, 0x70000\n" \
M_"v[72:75], a[180:183], a[132:135], v[72:75]\n" \
X_"a[164:165], v33 offset:0\n" \
"s_add_i32 s5, s15, s5\n" \
M_"v[72:75], a[244:247], a[196:199], v[72:75]\n" \
"s_bitset1_b32 s5, 11\n" \
"v_writelane_b32 v11, s5, 50\n" \
M_"v[68:71], a[176:179], a[132:135], v[68:71]\n" \
X_"a[166:167], v33 offset:512\n" \
"s_add_i32 s5, s4, 0x30000\n" \
M_"v[68:71], a[240:243], a[196:199], v[68:71]\n" \
R_"a[132:135], v28 offset:2048\n" \
"s_and_b32 s5, s5, 0x70000\n" \
M_"v[84:87], a[176:179], a[136:139], v[84:87]\n" \
R_"a[196:199], v29 offset:2048\n" \
"s_add_i32 s5, s15, s5\n" \
M_"v[84:87], a[240:243], a[200:203], v[84:87]\n" \
"s_add_u32 m0, s31, 0x8000\n" \
"s_mov_b32 s84, 0x380000\n" \
M_"v[88:91], a[180:183], a[136:139], v[88:91]\n" \
B_"v26, s[68:71], s84 offen offset:0 lds\n" \
M_"v[88:91], a[244:247], a[200:203], v[88:91]\n" \
"s_add_u32 s89, s42, 0x0\n" \
"buffer_store_dwordx4 v[184:187], v20, s[80:83], s89 offen offset:1024 sc1\n" \
M_"v[92:95], a[184:187], a[136:139], v[92:95]\n" \
X_"a[228:229], v33 offset:4096\n" \
"s_bitset1_b32 s5, 12\n" \
M_"v[92:95], a[248:251], a[200:203], v[92:95]\n" \
X_"a[230:231], v33 offset:4608\n" \
"v_writelane_b32 v11, s5, 51\n" \
M_"v[96:99], a[188:191], a[136:139], v[96:99]\n" \
"s_and_b32 s5, s4, 0x70000\n" \
"s_xor_b32 s5, s5, 0x40000\n" \
M_"v[96:99], a[252:255], a[200:203], v[96:99]\n" \
R_"a[136:139], v28 offset:4096\n" \
"s_add_i32 s5, s15, s5\n" \
M_"v[112:115], a[188:191], a[140:143], v[112:115]\n" \
R_"a[200:203], v29 offset:4096\n" \
"s_or_b32 s5, s5, 0x1800\n" \
M_"v[112:115], a[252:255], a[204:207], v[112:115]\n" \
"s_addk_i32 s84, 0x7c00\n" \
M_"v[108:111], a[184:187], a[140:143], v[108:111]\n" \
B_"v26, s[68:71], s84 offen offset:1024 lds\n" \
M_"v[108:111], a[248:251], a[204:207], v[108:111]\n" \
X_"a[172:173], v35 offset:0\n" \
"v_writelane_b32 v11, s5, 52\n" \
M_"v[104:107], a[180:183], a[140:143], v[104:107]\n" \
"s_addk_i32 s84, 0x7c00\n" \
M_"v[104:107], a[244:247], a[204:207], v[104:107]\n" \
B_"v27, s[68:71], s84 offen offset:2048 lds\n" \
M_"v[100:103], a[176:179], a[140:143], v[100:103]\n" \
X_"a[174:175], v35 offset:512\n" \
"s_add_i32 s5, s4, 0x50000\n" \
M_"v[100:103], a[240:243], a[204:207], v[100:103]\n" \
R_"a[140:143], v28 offset:6144\n" \
"s_and_b32 s5, s5, 0x70000\n" \
M_"v[116:119], a[176:179], a[144:147], v[116:119]\n" \
R_"a[204:207], v29 offset:6144\n" \
"s_add_i32 s5, s15, s5\n" \
M_"v[116:119], a[240:243], a[208:211], v[116:119]\n" \
"s_addk_i32 s84, 0x7c00\n" \
M_"v[120:123], a[180:183], a[144:147], v[120:123]\n" \
B_"v27, s[68:71], s84 offen offset:3072 lds\n" \
M_"v[120:123], a[244:247], a[208:211], v[120:123]\n" \
X_"a[236:237], v35 offset:4096\n" \
"s_bitset1_b32 s5, 13\n" \
M_"v[124:127], a[184:187], a[144:147], v[124:127]\n" \
X_"a[238:239], v35 offset:4608\n" \
"v_writelane_b32 v11, s5, 53\n" \
M_"v[124:127], a[248:251], a[208:211], v[124:127]\n" \
"s_add_u32 m0, s30, 0x20000\n" \
"s_movk_i32 s84, 0x400\n" \
M_"v[128:131], a[188:191], a[144:147], v[128:131]\n" \
B_"v24, s[76:79], s84 offen offset:0 lds\n" \
M_"v[128:131], a[252:255], a[208:211], v[128:131]\n" \
R_"a[144:147], v28 offset:8192\n" \
"s_add_i32 s5, s4, 0x60000\n" \
M_"v[144:147], a[188:191], a[148:151], v[144:147]\n" \
R_"a[208:211], v29 offset:8192\n" \
"s_and_b32 s5, s5, 0x70000\n" \
M_"v[144:147], a[252:255], a[212:215], v[144:147]\n" \
X_"a[168:169], v34 offset:0\n" \
"s_add_i32 s4, s4, 0x70000\n" \
M_"v[140:143], a[184:187], a[148:151], v[140:143]\n" \
"s_movk_i32 s84, 0x2400\n" \
M_"v[140:143], a[248:251], a[212:215], v[140:143]\n" \
B_"v25, s[76:79], s84 offen offset:1024 lds\n" \
M_"v[136:139], a[180:183], a[148:151], v[136:139]\n" \
X_"a[170:171], v34 offset:512\n" \
"s_add_i32 s5, s15, s5\n" \
M_"v[136:139], a[244:247], a[212:215], v[136:139]\n" \
X_"a[232:233], v34 offset:4096\n" \
"s_and_b32 s4, s4, 0x70000\n" \
M_"v[132:135], a[176:179], a[148:151], v[132:135]\n" \
X_"a[234:235], v34 offset:4608\n" \
"s_or_b32 s5, s5, 0x2800\n" \
M_"v[132:135], a[240:243], a[212:215], v[132:135]\n" \
R_"a[148:151], v28 offset:10240\n" \
"v_writelane_b32 v11, s5, 54\n" \
M_"v[148:151], a[176:179], a[152:155], v[148:151]\n" \
R_"a[212:215], v29 offset:10240\n" \
"s_add_i32 s15, s15, s4\n" \
M_"v[148:151], a[240:243], a[216:219], v[148:151]\n" \
"s_movk_i32 s84, 0x4400\n" \
M_"v[152:155], a[180:183], a[152:155], v[152:155]\n" \
B_"v24, s[76:79], s84 offen offset:2048 lds\n" \
M_"v[152:155], a[244:247], a[216:219], v[152:155]\n" \
"s_movk_i32 s84, 0x6400\n" \
M_"v[156:159], a[184:187], a[152:155], v[156:159]\n" \
B_"v25, s[76:79], s84 offen offset:3072 lds\n" \
M_"v[156:159], a[248:251], a[216:219], v[156:159]\n" \
R_"v[248:251], v22 offset:0\n" \
"s_or_b32 s4, s15, 0x3000\n" \
M_"v[160:163], a[188:191], a[152:155], v[160:163]\n" \
R_"v[48:51], v23 offset:0\n" \
"v_writelane_b32 v11, s4, 55\n" \
M_"v[160:163], a[252:255], a[216:219], v[160:163]\n" \
R_"a[152:155], v28 offset:12288\n" \
"s_movk_i32 s11, 0x4000\n" \
M_"v[176:179], a[188:191], a[156:159], v[176:179]\n" \
R_"a[216:219], v29 offset:12288\n" \
"s_lshl_b32 s4, s2, 2\n" \
M_"v[176:179], a[252:255], a[220:223], v[176:179]\n" \
"s_and_b32 s4, s4, 28\n" \
"s_bfe_u32 s5, s2, 0x20003\n" \
M_"v[172:175], a[184:187], a[156:159], v[172:175]\n" \
"s_or_b32 s4, s4, s5\n" \
"s_lshr_b32 s5, s2, 5\n" \
M_"v[172:175], a[248:251], a[220:223], v[172:175]\n" \
"s_lshr_b32 s19, s3, 1\n" \
"s_lshl_b32 s18, s5, 1\n" \
M_"v[168:171], a[180:183], a[156:159], v[168:171]\n" \
"s_lshl_b32 s6, s4, 20\n" \
"s_lshl_b32 s7, s4, 8\n" \
M_"v[168:171], a[244:247], a[220:223], v[168:171]\n" \
"s_and_b32 s13, s3, 1\n" \
"s_add_i32 s14, s18, s19\n" \
M_"v[164:167], a[176:179], a[156:159], v[164:167]\n" \
"s_add_u32 s6, s26, s6\n" \
"s_addc_u32 s9, s27, 0\n" \
M_"v[164:167], a[240:243], a[220:223], v[164:167]\n" \
R_"a[156:159], v28 offset:14336\n" \
R_"a[220:223], v29 offset:14336\n" \
"s_waitcnt lgkmcnt(15)\n" \
M_"a[0:3], a[160:163], a[128:131], a[0:3]\n" \
"s_add_u32 m0, s30, 0x21000\n" \
"s_addk_i32 s84, 0x3000\n" \
M_"a[0:3], a[224:227], a[192:195], a[0:3]\n" \
B_"v24, s[76:79], s84 offen offset:0 lds\n" \
M_"a[4:7], a[164:167], a[128:131], a[4:7]\n" \
X_"a[176:177], v36 offset:0\n" \
M_"a[4:7], a[228:231], a[192:195], a[4:7]\n" \
X_"a[178:179], v36 offset:1024\n" \
M_"a[12:15], a[172:175], a[128:131], a[12:15]\n" \
X_"a[240:241], v36 offset:8192\n" \
"s_waitcnt lgkmcnt(15)\n" \
M_"a[12:15], a[236:239], a[192:195], a[12:15]\n" \
"s_add_u32 s88, s42, 0x1000\n" \
"buffer_store_dwordx4 v[188:191], v20, s[80:83], s88 offen offset:1024 sc1\n" \
"s_waitcnt lgkmcnt(13)\n" \
M_"a[8:11], a[128:131], a[168:171], a[8:11]\n" \
X_"a[242:243], v36 offset:9216\n" \
"s_waitcnt lgkmcnt(12)\n" \
M_"a[8:11], a[192:195], a[232:235], a[8:11]\n" \
X_"a[180:181], v37 offset:0\n" \
M_"a[24:27], a[132:135], a[168:171], a[24:27]\n" \
X_"a[182:183], v37 offset:1024\n" \
M_"a[24:27], a[196:199], a[232:235], a[24:27]\n" \
X_"a[244:245], v37 offset:8192\n" \
M_"a[28:31], a[172:175], a[132:135], a[28:31]\n" \
"s_add_u32 s8, s6, 0x1000000\n" \
"s_addc_u32 s6, s9, 0\n" \
M_"a[28:31], a[236:239], a[196:199], a[28:31]\n" \
"s_addk_i32 s84, 0x2000\n" \
M_"a[20:23], a[164:167], a[132:135], a[20:23]\n" \
B_"v25, s[76:79], s84 offen offset:1024 lds\n" \
M_"a[20:23], a[228:231], a[196:199], a[20:23]\n" \
X_"a[246:247], v37 offset:9216\n" \
"s_and_b32 s9, s6, 0xffff\n" \
M_"a[16:19], a[160:163], a[132:135], a[16:19]\n" \
X_"a[184:185], v38 offset:0\n" \
"s_lshl_b32 s6, s5, 16\n" \
M_"a[16:19], a[224:227], a[196:199], a[16:19]\n" \
X_"a[186:187], v38 offset:1024\n" \
"s_sub_i32 s10, 0x100000, s6\n" \
M_"a[32:35], a[160:163], a[136:139], a[32:35]\n" \
"s_addk_i32 s84, 0x2000\n" \
"s_nop 0\n" \
M_"a[32:35], a[224:227], a[200:203], a[32:35]\n" \
B_"v24, s[76:79], s84 offen offset:2048 lds\n" \
M_"a[36:39], a[164:167], a[136:139], a[36:39]\n" \
X_"a[248:249], v38 offset:8192\n" \
"s_mov_b32 s6, 0x8000\n" \
M_"a[36:39], a[228:231], a[200:203], a[36:39]\n" \
".fill (L_pb + 32480 - .) / 4, 4, 0xBF800000\n" \
"L_pp8:\n" \
"s_cbranch_execz L_pret\n" \
"s_nop 0\n" \
X_"a[250:251], v38 offset:9216\n" \
M_"a[44:47], a[172:175], a[136:139], a[44:47]\n" \
X_"a[188:189], v39 offset:0\n" \
M_"a[44:47], a[236:239], a[200:203], a[44:47]\n" \
X_"a[190:191], v39 offset:1024\n" \
M_"a[40:43], a[136:139], a[168:171], a[40:43]\n" \
"s_add_u32 s89, s42, 0x2000\n" \
"buffer_store_dwordx4 v[192:195], v20, s[80:83], s89 offen offset:0 sc1\n" \
M_"a[40:43], a[200:203], a[232:235], a[40:43]\n" \
X_"a[252:253], v39 offset:8192\n" \
M_"a[56:59], a[140:143], a[168:171], a[56:59]\n" \
X_"a[254:255], v39 offset:9216\n" \
M_"a[56:59], a[204:207], a[232:235], a[56:59]\n" \
"s_cmp_lg_u32 s5, 0\n" \
"s_cselect_b32 s17, s11, s6\n" \
M_"a[60:63], a[172:175], a[140:143], a[60:63]\n" \
"s_lshl_b32 s6, s3, 1\n" \
"s_lshl_b32 s5, s5, 3\n" \
M_"a[60:63], a[236:239], a[204:207], a[60:63]\n" \
"s_addk_i32 s84, 0x2000\n" \
M_"a[52:55], a[164:167], a[140:143], a[52:55]\n" \
B_"v25, s[76:79], s84 offen offset:3072 lds\n" \
M_"a[52:55], a[228:231], a[204:207], a[52:55]\n" \
"s_and_b32 s6, s6, 4\n" \
"s_or_b32 s12, s5, s6\n" \
M_"a[48:51], a[160:163], a[140:143], a[48:51]\n" \
"s_sub_i32 s5, s13, s5\n" \
"s_or_b32 s5, s5, s6\n" \
M_"a[48:51], a[224:227], a[204:207], a[48:51]\n" \
"s_or_b32 s6, s12, s13\n" \
"s_lshl_b32 s4, s4, 2\n" \
"s_waitcnt lgkmcnt(15)\n" \
M_"a[64:67], a[164:167], v[248:251], a[64:67]\n" \
"s_mulk_i32 s6, 0x92\n" \
"s_add_i32 s4, s4, 18\n" \
M_"a[64:67], a[228:231], v[48:51], a[64:67]\n" \
"s_mulk_i32 s5, 0x92\n" \
"s_add_i32 s6, s4, s6\n" \
M_"a[80:83], a[172:175], v[248:251], a[80:83]\n" \
"s_add_i32 s5, s5, s4\n" \
"s_lshl_b32 s4, s3, 11\n" \
M_"a[80:83], a[236:239], v[48:51], a[80:83]\n" \
"s_lshl_b32 s15, s13, 12\n" \
"s_lshl_b32 s16, s19, 14\n" \
M_"a[68:71], v[248:251], a[168:171], a[68:71]\n" \
"s_mul_i32 s19, s19, 0xe000\n" \
"s_mov_b32 s11, 0x20000\n" \
"s_waitcnt vmcnt(10) lgkmcnt(0)\n" \
M_"a[68:71], v[48:51], a[232:235], a[68:71]\n" \
"s_barrier\n" \
M_"v[52:55], a[176:179], a[128:131], v[52:55]\n" \
"s_add_u32 m0, s40, 0x0\n" \
"s_add_u32 s85, s39, 0x10000\n" \
M_"v[52:55], a[240:243], a[192:195], v[52:55]\n" \
B_"v20, s[80:83], s85 offen sc1 lds\n" \
M_"v[56:59], a[180:183], a[128:131], v[56:59]\n" \
X_"a[160:161], v32 offset:32768\n" \
"s_or_b32 s12, s4, 0xc000\n" \
M_"v[56:59], a[244:247], a[192:195], v[56:59]\n" \
X_"a[162:163], v32 offset:33280\n" \
"s_lshl_b32 s4, s3, 8\n" \
M_"v[60:63], a[184:187], a[128:131], v[60:63]\n" \
"s_or_b32 s22, s15, s16\n" \
"s_or_b32 s23, s19, s15\n" \
M_"v[60:63], a[248:251], a[192:195], v[60:63]\n" \
X_"a[224:225], v32 offset:36864\n" \
"s_lshl_b32 s60, s6, 10\n" \
M_"v[64:67], a[188:191], a[128:131], v[64:67]\n" \
"s_lshl_b32 s61, s5, 10\n" \
"s_lshl_b32 s14, s14, 15\n" \
"s_nop 0\n" \
M_"v[64:67], a[252:255], a[192:195], v[64:67]\n" \
R_"a[128:131], v28 offset:32768\n" \
"s_or_b32 s20, s4, 0x26c00\n" \
M_"v[80:83], a[188:191], a[132:135], v[80:83]\n" \
R_"a[192:195], v29 offset:32768\n" \
"s_or_b32 s13, s22, 0x8000\n" \
M_"v[80:83], a[252:255], a[196:199], v[80:83]\n" \
"s_add_u32 m0, s40, 0x1000\n" \
"s_add_u32 s85, s39, 0x11000\n" \
M_"v[76:79], a[184:187], a[132:135], v[76:79]\n" \
B_"v20, s[80:83], s85 offen sc1 lds\n" \
M_"v[76:79], a[248:251], a[196:199], v[76:79]\n" \
X_"a[226:227], v32 offset:37376\n" \
"s_add_i32 s22, s22, 0x14000\n" \
M_"v[72:75], a[180:183], a[132:135], v[72:75]\n" \
X_"a[164:165], v33 offset:32768\n" \
"s_bitset1_b32 s23, 16\n" \
M_"v[72:75], a[244:247], a[196:199], v[72:75]\n" \
"s_or_b32 s24, s7, 0x5be8020\n" \
"s_and_b32 s57, s27, 0xffff\n" \
M_"v[68:71], a[176:179], a[132:135], v[68:71]\n" \
X_"a[166:167], v33 offset:33280\n" \
"s_mov_b32 s58, 0x6000000\n" \
M_"v[68:71], a[240:243], a[196:199], v[68:71]\n" \
R_"a[132:135], v28 offset:34816\n" \
"s_mov_b32 s56, s26\n" \
M_"v[84:87], a[176:179], a[136:139], v[84:87]\n" \
R_"a[196:199], v29 offset:34816\n" \
"s_mov_b32 s59, s11\n" \
M_"v[84:87], a[240:243], a[200:203], v[84:87]\n" \
"s_add_u32 m0, s40, 0x3000\n" \
"s_movk_i32 s84, 0x400\n" \
M_"v[88:91], a[180:183], a[136:139], v[88:91]\n" \
B_"v21, s[76:79], s84 offen offset:0 lds\n" \
M_"v[88:91], a[244:247], a[200:203], v[88:91]\n" \
X_"a[228:229], v33 offset:36864\n" \
"s_add_i32 s60, s60, 0x3000000\n" \
M_"v[92:95], a[184:187], a[136:139], v[92:95]\n" \
X_"a[230:231], v33 offset:37376\n" \
"s_add_i32 s61, s61, 0x411c000\n" \
M_"v[92:95], a[248:251], a[200:203], v[92:95]\n" \
"s_add_u32 s88, s42, 0x2000\n" \
"buffer_store_dwordx4 v[216:219], v20, s[80:83], s88 offen offset:1024 sc1\n" \
M_"v[96:99], a[188:191], a[136:139], v[96:99]\n" \
"s_sub_i32 s19, 14, s18\n" \
"s_nop 0\n" \
"s_mov_b32 s21, 0x71800000\n" \
M_"v[96:99], a[252:255], a[200:203], v[96:99]\n" \
R_"a[136:139], v28 offset:36864\n" \
"s_mov_b32 s4, L_gsw-L_gtr\n" \
M_"v[112:115], a[188:191], a[140:143], v[112:115]\n" \
R_"a[200:203], v29 offset:36864\n" \
M_"v[112:115], a[252:255], a[204:207], v[112:115]\n" \
X_"a[172:173], v35 offset:32768\n" \
M_"v[108:111], a[184:187], a[140:143], v[108:111]\n" \
X_"a[174:175], v35 offset:33280\n" \
M_"v[108:111], a[248:251], a[204:207], v[108:111]\n" \
"s_cmp_eq_u32 s19, 0\n" \
"s_cselect_b32 s4, L_gs7-L_gtr, s4\n" \
M_"v[104:107], a[180:183], a[140:143], v[104:107]\n" \
"s_add_u32 m0, s31, 0x0\n" \
"s_mov_b32 s84, 0x400000\n" \
M_"v[104:107], a[244:247], a[204:207], v[104:107]\n" \
B_"v26, s[68:71], s84 offen offset:0 lds\n" \
M_"v[100:103], a[176:179], a[140:143], v[100:103]\n" \
X_"a[236:237], v35 offset:36864\n" \
M_"v[100:103], a[240:243], a[204:207], v[100:103]\n" \
R_"a[140:143], v28 offset:38912\n" \
M_"v[116:119], a[176:179], a[144:147], v[116:119]\n" \
R_"a[204:207], v29 offset:38912\n" \
M_"v[116:119], a[240:243], a[208:211], v[116:119]\n" \
X_"a[238:239], v35 offset:37376\n" \
M_"v[120:123], a[180:183], a[144:147], v[120:123]\n" \
X_"a[168:169], v34 offset:32768\n" \
M_"v[120:123], a[244:247], a[208:211], v[120:123]\n" \
"s_addk_i32 s84, 0x7c00\n" \
M_"v[124:127], a[184:187], a[144:147], v[124:127]\n" \
B_"v26, s[68:71], s84 offen offset:1024 lds\n" \
M_"v[124:127], a[248:251], a[208:211], v[124:127]\n" \
X_"a[170:171], v34 offset:33280\n" \
M_"v[128:131], a[188:191], a[144:147], v[128:131]\n" \
"s_cmp_eq_u32 s16, 0\n" \
"s_cselect_b32 s4, 0, s4\n" \
M_"v[128:131], a[252:255], a[208:211], v[128:131]\n" \
R_"a[144:147], v28 offset:40960\n" \
M_"v[144:147], a[188:191], a[148:151], v[144:147]\n" \
R_"a[208:211], v29 offset:40960\n" \
M_"v[144:147], a[252:255], a[212:215], v[144:147]\n" \
X_"a[232:233], v34 offset:36864\n" \
M_"v[140:143], a[184:187], a[148:151], v[140:143]\n" \
"s_addk_i32 s84, 0x7c00\n" \
M_"v[140:143], a[248:251], a[212:215], v[140:143]\n" \
B_"v27, s[68:71], s84 offen offset:2048 lds\n" \
M_"v[136:139], a[180:183], a[148:151], v[136:139]\n" \
X_"a[234:235], v34 offset:37376\n" \
M_"v[136:139], a[244:247], a[212:215], v[136:139]\n" \
"s_getpc_b64 s[28:29]\n" \
"L_gp0_%=:\n" \
"s_add_u32 s28, s28, L_gtr-L_gp0_%=\n" \
"s_addc_u32 s29, s29, 0\n" \
M_"v[132:135], a[176:179], a[148:151], v[132:135]\n" \
R_"v[248:251], v22 offset:32768\n" \
M_"v[132:135], a[240:243], a[212:215], v[132:135]\n" \
R_"a[148:151], v28 offset:43008\n" \
M_"v[148:151], a[176:179], a[152:155], v[148:151]\n" \
R_"a[212:215], v29 offset:43008\n" \
M_"v[148:151], a[240:243], a[216:219], v[148:151]\n" \
"s_add_u32 m0, s31, 0x0\n" \
"s_mov_b32 s84, 0x417400\n" \
M_"v[152:155], a[180:183], a[152:155], v[152:155]\n" \
B_"v27, s[68:71], s84 offen offset:3072 lds\n" \
M_"v[152:155], a[244:247], a[216:219], v[152:155]\n" \
R_"v[48:51], v23 offset:32768\n" \
M_"v[156:159], a[184:187], a[152:155], v[156:159]\n" \
"s_add_u32 s28, s28, s4\n" \
"s_addc_u32 s29, s29, 0\n" \
"s_nop 0\n" \
M_"v[156:159], a[248:251], a[216:219], v[156:159]\n" \
"v_accvgpr_write_b32 a96, 0\n" \
"v_accvgpr_write_b32 a97, 0\n" \
M_"v[160:163], a[188:191], a[152:155], v[160:163]\n" \
"v_accvgpr_write_b32 a98, 0\n" \
"v_accvgpr_write_b32 a99, 0\n" \
M_"v[160:163], a[252:255], a[216:219], v[160:163]\n" \
R_"a[152:155], v28 offset:45056\n" \
"v_accvgpr_write_b32 a100, 0\n" \
M_"v[176:179], a[188:191], a[156:159], v[176:179]\n" \
R_"a[216:219], v29 offset:45056\n" \
"v_accvgpr_write_b32 a101, 0\n" \
M_"v[176:179], a[252:255], a[220:223], v[176:179]\n" \
"v_accvgpr_write_b32 a102, 0\n" \
"v_accvgpr_write_b32 a103, 0\n" \
M_"v[172:175], a[184:187], a[156:159], v[172:175]\n" \
"v_accvgpr_write_b32 a112, 0\n" \
"v_accvgpr_write_b32 a113, 0\n" \
M_"v[172:175], a[248:251], a[220:223], v[172:175]\n" \
"v_accvgpr_write_b32 a114, 0\n" \
"v_accvgpr_write_b32 a115, 0\n" \
M_"v[168:171], a[180:183], a[156:159], v[168:171]\n" \
"v_accvgpr_write_b32 a120, 0\n" \
"v_accvgpr_write_b32 a121, 0\n" \
M_"v[168:171], a[244:247], a[220:223], v[168:171]\n" \
"v_accvgpr_write_b32 a122, 0\n" \
"v_accvgpr_write_b32 a123, 0\n" \
M_"v[164:167], a[176:179], a[156:159], v[164:167]\n" \
"v_mov_b32_e32 v180, 0x3f803f80\n" \
"s_mov_b32 s6, 0x33333333\n" \
M_"v[164:167], a[240:243], a[220:223], v[164:167]\n" \
R_"a[156:159], v28 offset:47104\n" \
R_"a[220:223], v29 offset:47104\n" \
"s_waitcnt lgkmcnt(15)\n" \
"s_nop 0\n" \
M_"a[0:3], a[160:163], a[128:131], a[0:3]\n" \
"s_add_u32 s89, s42, 0x3000\n" \
"buffer_store_dwordx4 v[220:223], v20, s[80:83], s89 offen offset:0 sc1\n" \
M_"a[0:3], a[224:227], a[192:195], a[0:3]\n" \
X_"a[176:177], v36 offset:32768\n" \
"s_mov_b32 s7, 0xcccccccc\n" \
M_"a[4:7], a[164:167], a[128:131], a[4:7]\n" \
X_"a[178:179], v36 offset:33792\n" \
"v_cndmask_b32_e64 v181, 0, v180, s[6:7]\n" \
M_"a[4:7], a[228:231], a[192:195], a[4:7]\n" \
X_"a[240:241], v36 offset:40960\n" \
"v_accvgpr_write_b32 a104, v181\n" \
M_"a[12:15], a[172:175], a[128:131], a[12:15]\n" \
X_"a[242:243], v36 offset:41984\n" \
"v_accvgpr_write_b32 a105, v181\n" \
"s_waitcnt lgkmcnt(15)\n" \
M_"a[12:15], a[236:239], a[192:195], a[12:15]\n" \
"v_accvgpr_write_b32 a106, v181\n" \
"v_accvgpr_write_b32 a107, v181\n" \
M_"a[8:11], a[128:131], a[168:171], a[8:11]\n" \
X_"a[180:181], v37 offset:32768\n" \
"s_mov_b32 s6, 0x11111111\n" \
"s_waitcnt lgkmcnt(13)\n" \
M_"a[8:11], a[192:195], a[232:235], a[8:11]\n" \
"s_mov_b32 s7, 0x11111111\n" \
"v_cndmask_b32_e64 v181, 0, v180, s[6:7]\n" \
M_"a[24:27], a[132:135], a[168:171], a[24:27]\n" \
X_"a[182:183], v37 offset:33792\n" \
"v_accvgpr_write_b32 a108, v181\n" \
M_"a[24:27], a[196:199], a[232:235], a[24:27]\n" \
X_"a[244:245], v37 offset:40960\n" \
"v_accvgpr_write_b32 a109, v181\n" \
M_"a[28:31], a[172:175], a[132:135], a[28:31]\n" \
"v_accvgpr_write_b32 a110, v181\n" \
"v_accvgpr_write_b32 a111, v181\n" \
M_"a[28:31], a[236:239], a[196:199], a[28:31]\n" \
X_"a[246:247], v37 offset:41984\n" \
"s_mov_b32 s6, 0x44444444\n" \
M_"a[20:23], a[164:167], a[132:135], a[20:23]\n" \
"s_mov_b32 s7, 0x44444444\n" \
"v_cndmask_b32_e64 v181, 0, v180, s[6:7]\n" \
M_"a[20:23], a[228:231], a[196:199], a[20:23]\n" \
X_"a[184:185], v38 offset:32768\n" \
"v_accvgpr_write_b32 a116, v181\n" \
M_"a[16:19], a[160:163], a[132:135], a[16:19]\n" \
X_"a[186:187], v38 offset:33792\n" \
"v_accvgpr_write_b32 a117, v181\n" \
M_"a[16:19], a[224:227], a[196:199], a[16:19]\n" \
"v_accvgpr_write_b32 a118, v181\n" \
"v_accvgpr_write_b32 a119, v181\n" \
M_"a[32:35], a[160:163], a[136:139], a[32:35]\n" \
"s_add_u32 s88, s42, 0x3000\n" \
"buffer_store_dwordx4 v[224:227], v20, s[80:83], s88 offen offset:1024 sc1\n" \
M_"a[32:35], a[224:227], a[200:203], a[32:35]\n" \
X_"a[248:249], v38 offset:40960\n" \
"v_bfe_i32 v181, v20, 4, 1\n" \
M_"a[36:39], a[164:167], a[136:139], a[36:39]\n" \
"v_lshlrev_b32_e32 v45, 3, v20\n" \
"v_add_u32_e32 v45, s30, v45\n" \
M_"a[36:39], a[228:231], a[200:203], a[36:39]\n" \
"s_getpc_b64 s[86:87]\n" \
"L_ct0_%=:\n" \
"s_add_u32 s86, s86, L_ap0-L_ct0_%=\n" \
"s_addc_u32 s87, s87, 0\n" \
"s_add_u32 m0, s40, 0x2000\n" \
M_"a[44:47], a[172:175], a[136:139], a[44:47]\n" \
"global_load_lds_dword v45, s[86:87]\n" \
M_"a[44:47], a[236:239], a[200:203], a[44:47]\n" \
X_"a[250:251], v38 offset:41984\n" \
"v_and_b32_e32 v181, 0xaaaaaaaa, v181\n" \
M_"a[40:43], a[136:139], a[168:171], a[40:43]\n" \
X_"a[188:189], v39 offset:32768\n" \
"v_or_b32_e32 v181, 0x44444444, v181\n" \
M_"a[40:43], a[200:203], a[232:235], a[40:43]\n" \
X_"a[190:191], v39 offset:33792\n" \
"v_accvgpr_write_b32 a126, v181\n" \
M_"a[56:59], a[140:143], a[168:171], a[56:59]\n" \
X_"a[252:253], v39 offset:40960\n" \
"v_bfe_u32 v180, v20, 5, 3\n" \
M_"a[56:59], a[204:207], a[232:235], a[56:59]\n" \
X_"a[254:255], v39 offset:41984\n" \
"v_bfe_u32 v181, v20, 8, 2\n" \
M_"a[60:63], a[172:175], a[140:143], a[60:63]\n" \
"v_lshlrev_b32_e32 v181, 1, v181\n" \
"v_sub_u32_e32 v180, v180, v181\n" \
M_"a[60:63], a[236:239], a[204:207], a[60:63]\n" \
"v_and_b32_e64 v181, 16, v20\n" \
"v_mov_b32_e32 v182, 0x3f80\n" \
M_"a[52:55], a[164:167], a[140:143], a[52:55]\n" \
"v_lshlrev_b32_e32 v181, v181, v182\n" \
"v_cmp_eq_u32_e32 vcc, 0, v180\n" \
M_"a[52:55], a[228:231], a[204:207], a[52:55]\n" \
"v_cmp_eq_u32_e64 s[6:7], 1, v180\n" \
"v_cndmask_b32_e64 v182, 0, v181, vcc\n" \
M_"a[48:51], a[160:163], a[140:143], a[48:51]\n" \
"v_cndmask_b32_e64 v183, 0, v181, s[6:7]\n" \
"v_accvgpr_write_b32 a124, v182\n" \
M_"a[48:51], a[224:227], a[204:207], a[48:51]\n" \
"v_accvgpr_write_b32 a125, v183\n" \
"s_waitcnt lgkmcnt(15)\n" \
M_"a[64:67], a[164:167], v[248:251], a[64:67]\n" \
M_"a[64:67], a[228:231], v[48:51], a[64:67]\n" \
M_"a[80:83], a[172:175], v[248:251], a[80:83]\n" \
M_"a[80:83], a[236:239], v[48:51], a[80:83]\n" \
M_"a[68:71], v[248:251], a[168:171], a[68:71]\n" \
"s_waitcnt vmcnt(3) lgkmcnt(0)\n" \
M_"a[68:71], v[48:51], a[232:235], a[68:71]\n" \
"s_barrier\n" \
"v_xor_b32_e32 v28, 0x20000, v28\n" \
"v_xor_b32_e32 v29, 0x20000, v29\n" \
M_"v[52:55], a[176:179], a[128:131], v[52:55]\n" \
X_"a[160:161], v32 offset:0\n" \
"v_accvgpr_read_b32 v184, a0\n" \
M_"v[52:55], a[240:243], a[192:195], v[52:55]\n" \
"v_accvgpr_read_b32 v185, a1\n" \
"v_accvgpr_read_b32 v186, a2\n" \
M_"v[56:59], a[180:183], a[128:131], v[56:59]\n" \
X_"a[162:163], v32 offset:512\n" \
"v_accvgpr_read_b32 v187, a3\n" \
M_"v[56:59], a[244:247], a[192:195], v[56:59]\n" \
"v_accvgpr_read_b32 v188, a4\n" \
"v_accvgpr_read_b32 v189, a5\n" \
M_"v[60:63], a[184:187], a[128:131], v[60:63]\n" \
"v_accvgpr_read_b32 v190, a6\n" \
"v_accvgpr_read_b32 v191, a7\n" \
M_"v[60:63], a[248:251], a[192:195], v[60:63]\n" \
X_"a[224:225], v32 offset:4096\n" \
"v_accvgpr_read_b32 v196, a12\n" \
M_"v[64:67], a[188:191], a[128:131], v[64:67]\n" \
"v_accvgpr_read_b32 v197, a13\n" \
"v_accvgpr_read_b32 v198, a14\n" \
M_"v[64:67], a[252:255], a[192:195], v[64:67]\n" \
R_"a[128:131], v28 offset:0\n" \
"v_accvgpr_read_b32 v199, a15\n" \
M_"v[80:83], a[188:191], a[132:135], v[80:83]\n" \
R_"a[192:195], v29 offset:0\n" \
"v_accvgpr_read_b32 v192, a8\n" \
M_"v[80:83], a[252:255], a[196:199], v[80:83]\n" \
X_"a[226:227], v32 offset:4608\n" \
"v_accvgpr_read_b32 v193, a9\n" \
M_"v[76:79], a[184:187], a[132:135], v[76:79]\n" \
"v_accvgpr_read_b32 v194, a10\n" \
"v_accvgpr_read_b32 v195, a11\n" \
M_"v[76:79], a[248:251], a[196:199], v[76:79]\n" \
X_"a[164:165], v33 offset:0\n" \
"v_accvgpr_read_b32 v208, a24\n" \
M_"v[72:75], a[180:183], a[132:135], v[72:75]\n" \
"v_accvgpr_read_b32 v209, a25\n" \
"v_accvgpr_read_b32 v210, a26\n" \
M_"v[72:75], a[244:247], a[196:199], v[72:75]\n" \
"v_accvgpr_read_b32 v211, a27\n" \
"v_accvgpr_read_b32 v212, a28\n" \
M_"v[68:71], a[176:179], a[132:135], v[68:71]\n" \
X_"a[166:167], v33 offset:512\n" \
"v_accvgpr_read_b32 v213, a29\n" \
M_"v[68:71], a[240:243], a[196:199], v[68:71]\n" \
R_"a[132:135], v28 offset:2048\n" \
"v_accvgpr_read_b32 v214, a30\n" \
M_"v[84:87], a[176:179], a[136:139], v[84:87]\n" \
R_"a[196:199], v29 offset:2048\n" \
"v_accvgpr_read_b32 v215, a31\n" \
M_"v[84:87], a[240:243], a[200:203], v[84:87]\n" \
X_"a[228:229], v33 offset:4096\n" \
"v_accvgpr_read_b32 v204, a20\n" \
M_"v[88:91], a[180:183], a[136:139], v[88:91]\n" \
"v_accvgpr_read_b32 v205, a21\n" \
"v_accvgpr_read_b32 v206, a22\n" \
M_"v[88:91], a[244:247], a[200:203], v[88:91]\n" \
"v_accvgpr_read_b32 v207, a23\n" \
"v_accvgpr_read_b32 v200, a16\n" \
M_"v[92:95], a[184:187], a[136:139], v[92:95]\n" \
X_"a[230:231], v33 offset:4608\n" \
"v_accvgpr_read_b32 v201, a17\n" \
M_"v[92:95], a[248:251], a[200:203], v[92:95]\n" \
"v_accvgpr_read_b32 v202, a18\n" \
"v_accvgpr_read_b32 v203, a19\n" \
M_"v[96:99], a[188:191], a[136:139], v[96:99]\n" \
"v_accvgpr_read_b32 v216, a32\n" \
"v_accvgpr_read_b32 v217, a33\n" \
M_"v[96:99], a[252:255], a[200:203], v[96:99]\n" \
R_"a[136:139], v28 offset:4096\n" \
"v_accvgpr_read_b32 v218, a34\n" \
M_"v[112:115], a[188:191], a[140:143], v[112:115]\n" \
R_"a[200:203], v29 offset:4096\n" \
"v_accvgpr_read_b32 v219, a35\n" \
M_"v[112:115], a[252:255], a[204:207], v[112:115]\n" \
X_"a[172:173], v35 offset:0\n" \
"v_accvgpr_read_b32 v220, a36\n" \
M_"v[108:111], a[184:187], a[140:143], v[108:111]\n" \
X_"a[174:175], v35 offset:512\n" \
"v_accvgpr_read_b32 v221, a37\n" \
M_"v[108:111], a[248:251], a[204:207], v[108:111]\n" \
"v_accvgpr_read_b32 v222, a38\n" \
"v_accvgpr_read_b32 v223, a39\n" \
M_"v[104:107], a[180:183], a[140:143], v[104:107]\n" \
X_"a[236:237], v35 offset:4096\n" \
"v_accvgpr_read_b32 v228, a44\n" \
M_"v[104:107], a[244:247], a[204:207], v[104:107]\n" \
"v_accvgpr_read_b32 v229, a45\n" \
"v_accvgpr_read_b32 v230, a46\n" \
M_"v[100:103], a[176:179], a[140:143], v[100:103]\n" \
"v_accvgpr_read_b32 v231, a47\n" \
"v_accvgpr_read_b32 v224, a40\n" \
M_"v[100:103], a[240:243], a[204:207], v[100:103]\n" \
R_"a[140:143], v28 offset:6144\n" \
"v_accvgpr_read_b32 v225, a41\n" \
M_"v[116:119], a[176:179], a[144:147], v[116:119]\n" \
R_"a[204:207], v29 offset:6144\n" \
"v_accvgpr_read_b32 v226, a42\n" \
M_"v[116:119], a[240:243], a[208:211], v[116:119]\n" \
X_"a[238:239], v35 offset:4608\n" \
"v_accvgpr_read_b32 v227, a43\n" \
M_"v[120:123], a[180:183], a[144:147], v[120:123]\n" \
X_"a[168:169], v34 offset:0\n" \
"v_accvgpr_read_b32 v240, a56\n" \
M_"v[120:123], a[244:247], a[208:211], v[120:123]\n" \
"v_accvgpr_read_b32 v241, a57\n" \
"v_accvgpr_read_b32 v242, a58\n" \
M_"v[124:127], a[184:187], a[144:147], v[124:127]\n" \
"v_accvgpr_read_b32 v243, a59\n" \
"v_accvgpr_read_b32 v244, a60\n" \
M_"v[124:127], a[248:251], a[208:211], v[124:127]\n" \
X_"a[170:171], v34 offset:512\n" \
"v_accvgpr_read_b32 v245, a61\n" \
M_"v[128:131], a[188:191], a[144:147], v[128:131]\n" \
"v_accvgpr_read_b32 v246, a62\n" \
"v_accvgpr_read_b32 v247, a63\n" \
M_"v[128:131], a[252:255], a[208:211], v[128:131]\n" \
R_"a[144:147], v28 offset:8192\n" \
"v_accvgpr_read_b32 v236, a52\n" \
M_"v[144:147], a[188:191], a[148:151], v[144:147]\n" \
R_"a[208:211], v29 offset:8192\n" \
"v_accvgpr_read_b32 v237, a53\n" \
M_"v[144:147], a[252:255], a[212:215], v[144:147]\n" \
X_"a[232:233], v34 offset:4096\n" \
"v_accvgpr_read_b32 v238, a54\n" \
M_"v[140:143], a[184:187], a[148:151], v[140:143]\n" \
"v_accvgpr_read_b32 v239, a55\n" \
"v_accvgpr_read_b32 v232, a48\n" \
M_"v[140:143], a[248:251], a[212:215], v[140:143]\n" \
X_"a[234:235], v34 offset:4608\n" \
"v_accvgpr_read_b32 v233, a49\n" \
M_"v[136:139], a[180:183], a[148:151], v[136:139]\n" \
"v_accvgpr_read_b32 v234, a50\n" \
"v_accvgpr_read_b32 v235, a51\n" \
M_"v[136:139], a[244:247], a[212:215], v[136:139]\n" \
"v_accvgpr_read_b32 v180, a64\n" \
"v_accvgpr_read_b32 v181, a65\n" \
M_"v[132:135], a[176:179], a[148:151], v[132:135]\n" \
R_"v[248:251], v22 offset:0\n" \
"v_accvgpr_read_b32 v182, a66\n" \
M_"v[132:135], a[240:243], a[212:215], v[132:135]\n" \
R_"a[148:151], v28 offset:10240\n" \
"v_accvgpr_read_b32 v183, a67\n" \
M_"v[148:151], a[176:179], a[152:155], v[148:151]\n" \
R_"a[212:215], v29 offset:10240\n" \
"v_accvgpr_read_b32 v252, a68\n" \
M_"v[148:151], a[240:243], a[216:219], v[148:151]\n" \
R_"v[48:51], v23 offset:0\n" \
"v_accvgpr_read_b32 v253, a69\n" \
M_"v[152:155], a[180:183], a[152:155], v[152:155]\n" \
"v_accvgpr_read_b32 v254, a70\n" \
"v_accvgpr_read_b32 v255, a71\n" \
M_"v[152:155], a[244:247], a[216:219], v[152:155]\n" \
"v_lshrrev_b32_e32 v24, 9, v20\n" \
"v_and_b32_e32 v26, 0x1ff, v20\n" \
M_"v[156:159], a[184:187], a[152:155], v[156:159]\n" \
"v_lshl_add_u32 v24, v24, 3, v26\n" \
"v_lshrrev_b32_e64 v25, 8, v20\n" \
M_"v[156:159], a[248:251], a[216:219], v[156:159]\n" \
"v_mul_u32_u24_e32 v25, 0x24700, v25\n" \
"v_add_u32_e64 v25, v25, v20\n" \
M_"v[160:163], a[188:191], a[152:155], v[160:163]\n" \
"v_lshrrev_b32_e32 v26, 2, v20\n" \
"v_add_u32_e32 v26, s45, v26\n" \
M_"v[160:163], a[252:255], a[216:219], v[160:163]\n" \
R_"a[152:155], v28 offset:12288\n" \
"v_add_u32_e64 v27, s46, v20\n" \
M_"v[176:179], a[188:191], a[156:159], v[176:179]\n" \
R_"a[216:219], v29 offset:12288\n" \
"v_add_u32_e64 v21, s43, v27\n" \
M_"v[176:179], a[252:255], a[220:223], v[176:179]\n" \
M_"v[172:175], a[184:187], a[156:159], v[172:175]\n" \
M_"v[172:175], a[248:251], a[220:223], v[172:175]\n" \
M_"v[168:171], a[180:183], a[156:159], v[168:171]\n" \
M_"v[168:171], a[244:247], a[220:223], v[168:171]\n" \
M_"v[164:167], a[176:179], a[156:159], v[164:167]\n" \
M_"v[164:167], a[240:243], a[220:223], v[164:167]\n" \
R_"a[156:159], v28 offset:14336\n" \
R_"a[220:223], v29 offset:14336\n" \
"s_waitcnt lgkmcnt(15)\n" \
"s_nop 0\n" \
M_"v[184:187], a[160:163], a[128:131], v[184:187]\n" \
"v_bfe_u32 v44, v20, 2, 5\n" \
X_"a[176:177], v36 offset:0\n" \
M_"v[184:187], a[224:227], a[192:195], v[184:187]\n" \
"buffer_load_dword v44, v44, s[80:83], s38 offen offset:0 sc1\n" \
M_"v[188:191], a[164:167], a[128:131], v[188:191]\n" \
X_"a[178:179], v36 offset:1024\n" \
M_"v[188:191], a[228:231], a[192:195], v[188:191]\n" \
X_"a[240:241], v36 offset:8192\n" \
M_"v[196:199], a[172:175], a[128:131], v[196:199]\n" \
X_"a[242:243], v36 offset:9216\n" \
"s_waitcnt lgkmcnt(15)\n" \
M_"v[196:199], a[236:239], a[192:195], v[196:199]\n" \
M_"v[192:195], a[128:131], a[168:171], v[192:195]\n" \
X_"a[180:181], v37 offset:0\n" \
"s_waitcnt lgkmcnt(13)\n" \
M_"v[192:195], a[192:195], a[232:235], v[192:195]\n" \
M_"v[208:211], a[132:135], a[168:171], v[208:211]\n" \
X_"a[182:183], v37 offset:1024\n" \
M_"v[208:211], a[196:199], a[232:235], v[208:211]\n" \
X_"a[244:245], v37 offset:8192\n" \
M_"v[212:215], a[172:175], a[132:135], v[212:215]\n" \
M_"v[212:215], a[236:239], a[196:199], v[212:215]\n" \
X_"a[246:247], v37 offset:9216\n" \
C_"v188, v188, v189\n" \
M_"v[204:207], a[164:167], a[132:135], v[204:207]\n" \
C_"v189, v190, v191\n" \
C_"v190, v198, v199\n" \
M_"v[204:207], a[228:231], a[196:199], v[204:207]\n" \
X_"a[184:185], v38 offset:0\n" \
"v_mov_b32_e32 v191, 0x3f80\n" \
M_"v[200:203], a[160:163], a[132:135], v[200:203]\n" \
X_"a[186:187], v38 offset:1024\n" \
".fill (L_pb + 36576 - .) / 4, 4, 0xBF800000\n" \
"L_pp9:\n" \
"s_cbranch_execz L_pret\n" \
C_"v184, v184, v185\n" \
M_"v[200:203], a[224:227], a[196:199], v[200:203]\n" \
C_"v185, v186, v187\n" \
C_"v186, v196, v197\n" \
M_"v[216:219], a[160:163], a[136:139], v[216:219]\n" \
X_"a[248:249], v38 offset:8192\n" \
"v_mov_b32_e32 v187, 0\n" \
M_"v[216:219], a[224:227], a[200:203], v[216:219]\n" \
X_"a[250:251], v38 offset:9216\n" \
M_"v[220:223], a[164:167], a[136:139], v[220:223]\n" \
M_"v[220:223], a[228:231], a[200:203], v[220:223]\n" \
X_"a[188:189], v39 offset:0\n" \
C_"v204, v204, v205\n" \
M_"v[228:231], a[172:175], a[136:139], v[228:231]\n" \
C_"v205, v206, v207\n" \
C_"v206, v214, v215\n" \
M_"v[228:231], a[236:239], a[200:203], v[228:231]\n" \
X_"a[190:191], v39 offset:1024\n" \
"v_mov_b32_e32 v207, 0x3f80\n" \
M_"v[224:227], a[136:139], a[168:171], v[224:227]\n" \
X_"a[252:253], v39 offset:8192\n" \
C_"v200, v200, v201\n" \
M_"v[224:227], a[200:203], a[232:235], v[224:227]\n" \
C_"v201, v202, v203\n" \
C_"v202, v212, v213\n" \
M_"v[240:243], a[140:143], a[168:171], v[240:243]\n" \
X_"a[254:255], v39 offset:9216\n" \
"v_mov_b32_e64 v203, 0\n" \
M_"v[240:243], a[204:207], a[232:235], v[240:243]\n" \
M_"v[244:247], a[172:175], a[140:143], v[244:247]\n" \
M_"v[244:247], a[236:239], a[204:207], v[244:247]\n" \
C_"v220, v220, v221\n" \
C_"v221, v222, v223\n" \
M_"v[236:239], a[164:167], a[140:143], v[236:239]\n" \
C_"v222, v230, v231\n" \
"v_mov_b32_e32 v223, 0x3f80\n" \
M_"v[236:239], a[228:231], a[204:207], v[236:239]\n" \
C_"v216, v216, v217\n" \
C_"v217, v218, v219\n" \
M_"v[232:235], a[160:163], a[140:143], v[232:235]\n" \
C_"v218, v228, v229\n" \
"v_mov_b32_e64 v219, 0\n" \
M_"v[232:235], a[224:227], a[204:207], v[232:235]\n" \
"s_waitcnt lgkmcnt(15)\n" \
"s_nop 0\n" \
M_"v[180:183], a[164:167], v[248:251], v[180:183]\n" \
M_"v[180:183], a[228:231], v[48:51], v[180:183]\n" \
M_"a[80:83], a[172:175], v[248:251], a[80:83]\n" \
M_"a[80:83], a[236:239], v[48:51], a[80:83]\n" \
C_"v236, v236, v237\n" \
C_"v237, v238, v239\n" \
M_"v[252:255], v[248:251], a[168:171], v[252:255]\n" \
C_"v238, v246, v247\n" \
"v_mov_b32_e32 v239, 0x3f80\n" \
M_"v[252:255], v[48:51], a[232:235], v[252:255]\n" \
C_"v232, v232, v233\n" \
C_"v233, v234, v235\n" \
"s_waitcnt vmcnt(0) lgkmcnt(0)\n" \
"s_barrier\n" \
"v_mov_b32_e32 v45, s62\n" \
"s_mov_b64 exec, 1\n" \
"buffer_store_dword v45, off, s[80:83], s37 offset:32 sc1\n" \
"s_mov_b64 exec, -1\n" \
"v_cmp_ne_u32_e32 vcc, s62, v44\n" \
"s_nop 4\n" \
"s_cbranch_vccnz L_poll160_%=\n" \
"L_pollr160_%=:\n" \
M_"v[52:55], a[176:179], a[128:131], v[52:55]\n" \
"s_mov_b32 s73, s50\n" \
"s_sub_u32 m0, s31, 0x10000\n" \
M_"v[52:55], a[240:243], a[192:195], v[52:55]\n" \
B_"v20, s[80:83], s73 offen offset:0 sc1 lds\n" \
M_"v[56:59], a[180:183], a[128:131], v[56:59]\n" \
"s_add_u32 s74, s50, 0x4000\n" \
"s_add_u32 s75, s50, 0x8000\n" \
M_"v[56:59], a[244:247], a[192:195], v[56:59]\n" \
"s_mov_b32 s70, 0x3e0293ee\n" \
"s_mov_b32 s76, s52\n" \
M_"v[60:63], a[184:187], a[128:131], v[60:63]\n" \
"v_mul_f32_e32 v52, s70, v52\n" \
"v_mul_f32_e32 v53, s70, v53\n" \
M_"v[60:63], a[248:251], a[192:195], v[60:63]\n" \
B_"v20, s[80:83], s73 offen offset:1024 sc1 lds\n" \
M_"v[64:67], a[188:191], a[128:131], v[64:67]\n" \
"v_mul_f32_e32 v54, s70, v54\n" \
"v_mul_f32_e32 v55, s70, v55\n" \
M_"v[64:67], a[252:255], a[192:195], v[64:67]\n" \
C_"v52, v52, v53\n" \
"v_mul_f32_e64 v56, s70, v56\n" \
M_"v[80:83], a[188:191], a[132:135], v[80:83]\n" \
"v_mul_f32_e32 v60, s70, v60\n" \
"v_mul_f32_e32 v61, s70, v61\n" \
M_"v[80:83], a[252:255], a[196:199], v[80:83]\n" \
B_"v20, s[80:83], s73 offen offset:2048 sc1 lds\n" \
M_"v[76:79], a[184:187], a[132:135], v[76:79]\n" \
"v_mul_f32_e32 v62, s70, v62\n" \
"v_mul_f32_e32 v63, s70, v63\n" \
M_"v[76:79], a[248:251], a[196:199], v[76:79]\n" \
C_"v60, v60, v61\n" \
"v_mul_f32_e64 v57, s70, v57\n" \
M_"v[72:75], a[180:183], a[132:135], v[72:75]\n" \
"v_mul_f32_e32 v58, s70, v58\n" \
"v_mul_f32_e32 v59, s70, v59\n" \
M_"v[72:75], a[244:247], a[196:199], v[72:75]\n" \
B_"v20, s[80:83], s73 offen offset:3072 sc1 lds\n" \
M_"v[68:71], a[176:179], a[132:135], v[68:71]\n" \
"s_sub_u32 m0, s31, s51\n" \
C_"v53, v54, v55\n" \
M_"v[68:71], a[240:243], a[196:199], v[68:71]\n" \
"v_mul_f32_e32 v64, s70, v64\n" \
"v_mul_f32_e64 v65, s70, v65\n" \
M_"v[84:87], a[176:179], a[136:139], v[84:87]\n" \
"v_mul_f32_e32 v66, s70, v66\n" \
"v_mul_f32_e32 v67, s70, v67\n" \
M_"v[84:87], a[240:243], a[200:203], v[84:87]\n" \
B_"v20, s[80:83], s74 offen offset:0 sc1 lds\n" \
M_"v[88:91], a[180:183], a[136:139], v[88:91]\n" \
C_"v61, v62, v63\n" \
C_"v54, v56, v57\n" \
M_"v[88:91], a[244:247], a[200:203], v[88:91]\n" \
C_"v55, v58, v59\n" \
C_"v62, v64, v65\n" \
M_"v[92:95], a[184:187], a[136:139], v[92:95]\n" \
C_"v63, v66, v67\n" \
"v_mul_f32_e64 v68, s70, v68\n" \
M_"v[92:95], a[248:251], a[200:203], v[92:95]\n" \
B_"v20, s[80:83], s74 offen offset:1024 sc1 lds\n" \
M_"v[96:99], a[188:191], a[136:139], v[96:99]\n" \
"ds_write_b128 v27, v[52:55] offset:0\n" \
M_"v[96:99], a[252:255], a[200:203], v[96:99]\n" \
"v_mul_f32_e32 v69, s70, v69\n" \
"v_mul_f32_e32 v76, s70, v76\n" \
M_"v[112:115], a[188:191], a[140:143], v[112:115]\n" \
"v_mul_f32_e32 v77, s70, v77\n" \
"v_mul_f32_e32 v70, s70, v70\n" \
M_"v[112:115], a[252:255], a[204:207], v[112:115]\n" \
B_"v20, s[80:83], s74 offen offset:2048 sc1 lds\n" \
M_"v[108:111], a[184:187], a[140:143], v[108:111]\n" \
"ds_write_b128 v27, v[60:63] offset:1024\n" \
M_"v[108:111], a[248:251], a[204:207], v[108:111]\n" \
"v_mul_f32_e64 v71, s70, v71\n" \
C_"v68, v68, v69\n" \
M_"v[104:107], a[180:183], a[140:143], v[104:107]\n" \
"v_mul_f32_e32 v78, s70, v78\n" \
"v_mul_f32_e32 v79, s70, v79\n" \
M_"v[104:107], a[244:247], a[204:207], v[104:107]\n" \
B_"v20, s[80:83], s74 offen offset:3072 sc1 lds\n" \
M_"v[100:103], a[176:179], a[140:143], v[100:103]\n" \
"s_sub_u32 m0, s31, 0x4000\n" \
C_"v76, v76, v77\n" \
M_"v[100:103], a[240:243], a[204:207], v[100:103]\n" \
"v_mul_f32_e32 v72, s70, v72\n" \
"v_mul_f32_e32 v73, s70, v73\n" \
M_"v[116:119], a[176:179], a[144:147], v[116:119]\n" \
"v_mul_f32_e32 v74, s70, v74\n" \
"v_mul_f32_e32 v75, s70, v75\n" \
M_"v[116:119], a[240:243], a[208:211], v[116:119]\n" \
B_"v20, s[80:83], s75 offen offset:0 sc1 lds\n" \
M_"v[120:123], a[180:183], a[144:147], v[120:123]\n" \
C_"v69, v70, v71\n" \
"v_mul_f32_e64 v80, s70, v80\n" \
M_"v[120:123], a[244:247], a[208:211], v[120:123]\n" \
"v_mul_f32_e32 v81, s70, v81\n" \
"v_mul_f32_e32 v82, s70, v82\n" \
M_"v[124:127], a[184:187], a[144:147], v[124:127]\n" \
"v_mul_f32_e64 v83, s70, v83\n" \
C_"v77, v78, v79\n" \
M_"v[124:127], a[248:251], a[208:211], v[124:127]\n" \
B_"v20, s[80:83], s75 offen offset:1024 sc1 lds\n" \
M_"v[128:131], a[188:191], a[144:147], v[128:131]\n" \
C_"v70, v72, v73\n" \
C_"v71, v74, v75\n" \
M_"v[128:131], a[252:255], a[208:211], v[128:131]\n" \
C_"v78, v80, v81\n" \
C_"v79, v82, v83\n" \
M_"v[144:147], a[188:191], a[148:151], v[144:147]\n" \
"ds_write_b128 v27, v[68:71] offset:4096\n" \
M_"v[144:147], a[252:255], a[212:215], v[144:147]\n" \
B_"v20, s[80:83], s75 offen offset:2048 sc1 lds\n" \
M_"v[140:143], a[184:187], a[148:151], v[140:143]\n" \
C_"v180, v180, v181\n" \
"v_accvgpr_read_b32 v50, a82\n" \
M_"v[140:143], a[248:251], a[212:215], v[140:143]\n" \
"ds_write_b128 v27, v[76:79] offset:5120\n" \
M_"v[136:139], a[180:183], a[148:151], v[136:139]\n" \
"v_accvgpr_read_b32 v51, a83\n" \
C_"v181, v182, v183\n" \
M_"v[136:139], a[244:247], a[212:215], v[136:139]\n" \
B_"v20, s[80:83], s75 offen offset:3072 sc1 lds\n" \
M_"v[132:135], a[176:179], a[148:151], v[132:135]\n" \
"s_mov_b32 m0, s53\n" \
C_"v182, v50, v51\n" \
M_"v[132:135], a[240:243], a[212:215], v[132:135]\n" \
"v_mov_b32_e32 v183, 0x3f80\n" \
C_"v234, v244, v245\n" \
M_"v[148:151], a[176:179], a[152:155], v[148:151]\n" \
"v_mov_b32_e32 v235, 0\n" \
C_"v54, v192, v193\n" \
M_"v[148:151], a[240:243], a[216:219], v[148:151]\n" \
B_"v20, s[80:83], s76 offen offset:0 sc1 lds\n" \
M_"v[152:155], a[180:183], a[152:155], v[152:155]\n" \
C_"v55, v194, v195\n" \
C_"v56, v208, v209\n" \
M_"v[152:155], a[244:247], a[216:219], v[152:155]\n" \
C_"v57, v210, v211\n" \
C_"v58, v224, v225\n" \
C_"v59, v226, v227\n" \
C_"v60, v240, v241\n" \
C_"v61, v242, v243\n" \
M_"v[32:35], v[180:183], v[184:187], 0\n" \
B_"v20, s[80:83], s76 offen offset:1024 sc1 lds\n" \
M_"v[156:159], a[184:187], a[152:155], v[156:159]\n" \
M_"v[192:195], v[188:191], v[184:187], 0\n" \
M_"v[208:211], v[204:207], v[200:203], 0\n" \
M_"v[36:39], v[188:191], v[200:203], 0\n" \
M_"v[224:227], v[220:223], v[216:219], 0\n" \
M_"v[248:251], v[204:207], v[216:219], 0\n" \
M_"v[240:243], v[236:239], v[232:235], 0\n" \
M_"v[64:67], v[220:223], v[232:235], 0\n" \
"s_mov_b32 s64, 0xaaaaaaaa\n" \
"s_mov_b32 s65, 0xaaaaaaaa\n" \
M_"v[156:159], a[248:251], a[216:219], v[156:159]\n" \
"v_cndmask_b32_e64 v32, v32, v33, s[64:65]\n" \
"v_cndmask_b32_e64 v34, v34, v35, s[64:65]\n" \
M_"v[160:163], a[188:191], a[152:155], v[160:163]\n" \
"s_mov_b32 s66, 0xfff0ffff\n" \
"s_mov_b32 s67, 0xf000ff00\n" \
"s_mov_b32 s68, 0xffc0fffc\n" \
"s_mov_b32 s69, 0xc000fc00\n" \
"v_cndmask_b32_e64 v192, v192, v193, s[64:65]\n" \
"v_add_f32_e64 v32, s47, v32\n" \
"v_cndmask_b32_e64 v194, v194, v195, s[64:65]\n" \
"v_add_f32_e64 v34, s47, v34\n" \
"v_cndmask_b32_e64 v208, v208, v209, s[64:65]\n" \
"v_cndmask_b32_e64 v36, v36, v37, s[64:65]\n" \
"v_cndmask_b32_e64 v210, v210, v211, s[64:65]\n" \
"v_cndmask_b32_e64 v38, v38, v39, s[64:65]\n" \
"v_cndmask_b32_e64 v224, v224, v225, s[64:65]\n" \
"v_cndmask_b32_e64 v248, v248, v249, s[64:65]\n" \
"v_cndmask_b32_e64 v226, v226, v227, s[64:65]\n" \
"v_cndmask_b32_e64 v250, v250, v251, s[64:65]\n" \
"v_cndmask_b32_e64 v240, v240, v241, s[64:65]\n" \
"v_cndmask_b32_e64 v64, v64, v65, s[64:65]\n" \
"v_cndmask_b32_e64 v242, v242, v243, s[64:65]\n" \
"v_cndmask_b32_e64 v66, v66, v67, s[64:65]\n" \
"v_cndmask_b32_e64 v192, v32, v192, s[66:67]\n" \
"v_cndmask_b32_e64 v194, v34, v194, s[68:69]\n" \
"v_cndmask_b32_e64 v208, v36, v208, s[66:67]\n" \
"v_cndmask_b32_e64 v210, v38, v210, s[68:69]\n" \
"v_cndmask_b32_e64 v224, v248, v224, s[66:67]\n" \
"v_cndmask_b32_e64 v226, v250, v226, s[68:69]\n" \
"v_cndmask_b32_e64 v240, v64, v240, s[66:67]\n" \
"v_cndmask_b32_e64 v242, v66, v242, s[68:69]\n" \
M_"v[160:163], a[252:255], a[216:219], v[160:163]\n" \
"v_max_f32_e32 v193, v192, v194\n" \
"v_max_f32_e32 v209, v208, v210\n" \
"v_max_f32_e32 v225, v224, v226\n" \
"v_max_f32_e32 v241, v240, v242\n" \
"v_mul_f32_e32 v84, s70, v84\n" \
"v_permlane32_swap_b32_e32 v193, v225\n" \
"v_permlane32_swap_b32_e32 v209, v241\n" \
"v_mul_f32_e32 v85, s70, v85\n" \
"v_mul_f32_e32 v92, s70, v92\n" \
"v_mul_f32_e32 v93, s70, v93\n" \
"v_permlane32_swap_b32_e32 v196, v228\n" \
"v_permlane32_swap_b32_e32 v212, v244\n" \
"v_max_f32_e32 v193, v193, v225\n" \
"v_max_f32_e32 v209, v209, v241\n" \
"v_accvgpr_read_b32 v48, a80\n" \
"v_accvgpr_read_b32 v49, a81\n" \
"v_mul_f32_e32 v86, s70, v86\n" \
"v_mul_f32_e32 v87, s70, v87\n" \
C_"v84, v84, v85\n" \
"v_mul_f32_e32 v94, s70, v94\n" \
"v_mul_f32_e32 v95, s70, v95\n" \
C_"v92, v92, v93\n" \
"v_permlane16_swap_b32_e32 v228, v244\n" \
"v_permlane16_swap_b32_e32 v193, v209\n" \
M_"v[176:179], a[188:191], a[156:159], v[176:179]\n" \
"s_mov_b32 s72, 0x3eae1a93\n" \
"v_mul_f32_e32 v88, s70, v88\n" \
"v_mul_f32_e32 v89, s70, v89\n" \
"v_mul_f32_e32 v90, s70, v90\n" \
"v_mul_f32_e32 v91, s70, v91\n" \
C_"v85, v86, v87\n" \
"v_mul_f32_e32 v96, s70, v96\n" \
"v_mul_f32_e32 v97, s70, v97\n" \
"v_mul_f32_e32 v98, s70, v98\n" \
"v_mul_f32_e32 v99, s70, v99\n" \
C_"v93, v94, v95\n" \
"v_mul_f32_e32 v45, 0xbfb8aa3b, v228\n" \
"v_mfma_f32_16x16x16_bf16 v[48:51], v[182:183], v[46:47], 0\n" \
"v_mfma_f32_16x16x16_bf16 v[196:199], v[190:191], v[46:47], 0\n" \
"v_mfma_f32_16x16x16_bf16 v[212:215], v[206:207], v[46:47], 0\n" \
"v_max_f32_e64 v193, v193, v209\n" \
C_"v86, v88, v89\n" \
C_"v87, v90, v91\n" \
C_"v94, v96, v97\n" \
C_"v95, v98, v99\n" \
"v_mfma_f32_16x16x16_bf16 v[228:231], v[222:223], v[46:47], 0\n" \
"v_mul_f32_e32 v193, s72, v193\n" \
"v_mov_b32_e32 v209, v193\n" \
"ds_write_b128 v27, v[84:87] offset:8192\n" \
"v_mfma_f32_16x16x16_bf16 v[244:247], v[238:239], v[46:47], 0\n" \
"v_permlane16_swap_b32_e32 v193, v209\n" \
"s_mov_b32 s71, 0x3e0293ee\n" \
M_"v[176:179], a[252:255], a[220:223], v[176:179]\n" \
"v_mov_b32_e32 v225, v193\n" \
"v_mov_b32_e32 v241, v209\n" \
"v_mul_f32_e32 v100, s70, v100\n" \
"v_mul_f32_e64 v101, s70, v101\n" \
"ds_write_b128 v27, v[92:95] offset:9216\n" \
"v_mul_f32_e32 v108, s70, v108\n" \
"v_mul_f32_e32 v109, s70, v109\n" \
"v_pk_mul_f32 v[116:117], v[116:117], s[70:71]\n" \
"v_pk_mul_f32 v[124:125], v[124:125], s[70:71]\n" \
"v_pk_mul_f32 v[132:133], v[132:133], s[70:71]\n" \
"v_pk_mul_f32 v[140:141], v[140:141], s[70:71]\n" \
"v_pk_mul_f32 v[148:149], v[148:149], s[70:71]\n" \
"v_pk_mul_f32 v[156:157], v[156:157], s[70:71]\n" \
"v_permlane32_swap_b32_e32 v193, v225\n" \
"v_permlane32_swap_b32_e32 v209, v241\n" \
"v_mul_f32_e32 v102, s70, v102\n" \
"v_mul_f32_e32 v103, s70, v103\n" \
C_"v100, v100, v101\n" \
"v_mul_f32_e32 v110, s70, v110\n" \
"v_mul_f32_e32 v111, s70, v111\n" \
C_"v108, v108, v109\n" \
"v_pk_mul_f32 v[118:119], v[118:119], s[70:71]\n" \
C_"v116, v116, v117\n" \
"v_pk_mul_f32 v[126:127], v[126:127], s[70:71]\n" \
C_"v124, v124, v125\n" \
"v_pk_mul_f32 v[134:135], v[134:135], s[70:71]\n" \
C_"v132, v132, v133\n" \
"v_pk_mul_f32 v[142:143], v[142:143], s[70:71]\n" \
C_"v140, v140, v141\n" \
"v_pk_mul_f32 v[150:151], v[150:151], s[70:71]\n" \
C_"v148, v148, v149\n" \
"v_pk_mul_f32 v[158:159], v[158:159], s[70:71]\n" \
C_"v156, v156, v157\n" \
"v_fma_f32 v192, v192, s72, -v193\n" \
"v_fma_f32 v194, v194, s72, -v193\n" \
"v_fma_f32 v208, v208, s72, -v209\n" \
"v_fma_f32 v210, v210, s72, -v209\n" \
"v_mul_f32_e32 v104, s70, v104\n" \
"v_mul_f32_e32 v105, s70, v105\n" \
"v_mul_f32_e32 v106, s70, v106\n" \
"v_mul_f32_e32 v107, s70, v107\n" \
C_"v101, v102, v103\n" \
"v_mul_f32_e32 v112, s70, v112\n" \
"v_mul_f32_e32 v113, s70, v113\n" \
"v_mul_f32_e32 v114, s70, v114\n" \
"v_mul_f32_e32 v115, s70, v115\n" \
C_"v109, v110, v111\n" \
"v_pk_mul_f32 v[120:121], v[120:121], s[70:71]\n" \
"v_pk_mul_f32 v[122:123], v[122:123], s[70:71]\n" \
C_"v117, v118, v119\n" \
"v_pk_mul_f32 v[128:129], v[128:129], s[70:71]\n" \
"v_pk_mul_f32 v[130:131], v[130:131], s[70:71]\n" \
C_"v125, v126, v127\n" \
"v_pk_mul_f32 v[136:137], v[136:137], s[70:71]\n" \
"v_pk_mul_f32 v[138:139], v[138:139], s[70:71]\n" \
C_"v133, v134, v135\n" \
"v_pk_mul_f32 v[144:145], v[144:145], s[70:71]\n" \
"v_pk_mul_f32 v[146:147], v[146:147], s[70:71]\n" \
C_"v141, v142, v143\n" \
"v_pk_mul_f32 v[152:153], v[152:153], s[70:71]\n" \
"v_pk_mul_f32 v[154:155], v[154:155], s[70:71]\n" \
C_"v149, v150, v151\n" \
"v_pk_mul_f32 v[160:161], v[160:161], s[70:71]\n" \
"v_pk_mul_f32 v[162:163], v[162:163], s[70:71]\n" \
C_"v157, v158, v159\n" \
"v_pk_mul_f32 v[176:177], v[176:177], s[70:71]\n" \
"v_pk_mul_f32 v[178:179], v[178:179], s[70:71]\n" \
E_"v192, v192\n" \
E_"v194, v194\n" \
E_"v208, v208\n" \
E_"v210, v210\n" \
"v_fma_f32 v224, v224, s72, -v225\n" \
"v_fma_f32 v226, v226, s72, -v225\n" \
M_"v[172:175], a[184:187], a[156:159], v[172:175]\n" \
C_"v102, v104, v105\n" \
C_"v103, v106, v107\n" \
C_"v110, v112, v113\n" \
C_"v111, v114, v115\n" \
C_"v118, v120, v121\n" \
C_"v119, v122, v123\n" \
C_"v126, v128, v129\n" \
C_"v127, v130, v131\n" \
C_"v134, v136, v137\n" \
C_"v135, v138, v139\n" \
C_"v142, v144, v145\n" \
C_"v143, v146, v147\n" \
C_"v150, v152, v153\n" \
C_"v151, v154, v155\n" \
C_"v158, v160, v161\n" \
C_"v159, v162, v163\n" \
E_"v224, v224\n" \
E_"v226, v226\n" \
"v_fma_f32 v240, v240, s72, -v241\n" \
"v_fma_f32 v242, v242, s72, -v241\n" \
"ds_write_b128 v27, v[100:103] offset:12288\n" \
C_"v192, v192, v194\n" \
C_"v208, v208, v210\n" \
E_"v240, v240\n" \
E_"v242, v242\n" \
C_"v68, v48, v49\n" \
C_"v69, v50, v51\n" \
C_"v70, v196, v197\n" \
C_"v71, v198, v199\n" \
"ds_write_b128 v27, v[108:111] offset:13312\n" \
C_"v72, v212, v213\n" \
C_"v73, v214, v215\n" \
"v_perm_b32 v184, v192, v192, v40\n" \
"v_perm_b32 v185, v192, v192, v41\n" \
"v_perm_b32 v186, v192, v192, v42\n" \
"v_perm_b32 v187, v192, v192, v43\n" \
"v_perm_b32 v200, v208, v208, v40\n" \
"v_perm_b32 v201, v208, v208, v41\n" \
"v_perm_b32 v202, v208, v208, v42\n" \
"v_perm_b32 v203, v208, v208, v43\n" \
"ds_write_b128 v21, v[116:119] offset:16384\n" \
C_"v224, v224, v226\n" \
C_"v74, v228, v229\n" \
C_"v75, v230, v231\n" \
M_"v[32:35], v[68:71], v[184:187], 0\n" \
M_"v[36:39], v[70:73], v[200:203], 0\n" \
"v_perm_b32 v216, v224, v224, v40\n" \
"v_perm_b32 v217, v224, v224, v41\n" \
"ds_write_b128 v21, v[124:127] offset:17408\n" \
"v_perm_b32 v218, v224, v224, v42\n" \
"v_perm_b32 v219, v224, v224, v43\n" \
C_"v240, v240, v242\n" \
M_"v[172:175], a[248:251], a[220:223], v[172:175]\n" \
C_"v76, v244, v245\n" \
C_"v77, v246, v247\n" \
M_"v[248:251], v[72:75], v[216:219], 0\n" \
"v_perm_b32 v232, v240, v240, v40\n" \
"v_perm_b32 v233, v240, v240, v41\n" \
"ds_write_b128 v21, v[132:135] offset:20480\n" \
"v_perm_b32 v234, v240, v240, v42\n" \
"v_perm_b32 v235, v240, v240, v43\n" \
"v_pk_mul_f32 v[172:173], v[172:173], s[70:71]\n" \
"v_pk_mul_f32 v[174:175], v[174:175], s[70:71]\n" \
C_"v172, v172, v173\n" \
C_"v173, v174, v175\n" \
C_"v174, v176, v177\n" \
C_"v175, v178, v179\n" \
M_"v[64:67], v[74:77], v[232:235], 0\n" \
"ds_write_b128 v21, v[140:143] offset:21504\n" \
M_"v[168:171], a[180:183], a[156:159], v[168:171]\n" \
C_"v52, v252, v253\n" \
C_"v53, v254, v255\n" \
M_"v[168:171], a[244:247], a[220:223], v[168:171]\n" \
"v_exp_f32_e64 v45, v45\n" \
M_"v[192:195], v[52:55], v[184:187], 0\n" \
"ds_write_b128 v21, v[148:151] offset:24576\n" \
M_"v[208:211], v[54:57], v[200:203], 0\n" \
M_"v[224:227], v[56:59], v[216:219], 0\n" \
M_"v[240:243], v[58:61], v[232:235], 0\n" \
"ds_write_b128 v21, v[156:159] offset:25600\n" \
"v_mov_b32_dpp v34, v38 quad_perm:[0,1,2,3] row_mask:0x2 bank_mask:0xf\n" \
"v_pk_mul_f32 v[168:169], v[168:169], s[70:71]\n" \
"v_pk_mul_f32 v[170:171], v[170:171], s[70:71]\n" \
"v_mov_b32_dpp v34, v250 quad_perm:[0,1,2,3] row_mask:0x4 bank_mask:0xf\n" \
"v_add_f32_e64 v45, 1.0, v45\n" \
M_"v[164:167], a[176:179], a[156:159], v[164:167]\n" \
"v_mov_b32_dpp v34, v66 quad_perm:[0,1,2,3] row_mask:0x8 bank_mask:0xf\n" \
"v_mov_b32_dpp v32, v36 quad_perm:[0,1,2,3] row_mask:0x2 bank_mask:0xf\n" \
"v_rcp_f32_e64 v45, v45\n" \
"ds_write_b128 v21, v[172:175] offset:29696\n" \
"v_mov_b32_dpp v33, v37 quad_perm:[0,1,2,3] row_mask:0x2 bank_mask:0xf\n" \
"v_rcp_f32_e64 v34, v34\n" \
"v_mov_b32_dpp v32, v248 quad_perm:[0,1,2,3] row_mask:0x4 bank_mask:0xf\n" \
"v_mov_b32_dpp v33, v249 quad_perm:[0,1,2,3] row_mask:0x4 bank_mask:0xf\n" \
"v_mul_f32_e64 v34, v45, v34\n" \
"v_mov_b32_dpp v32, v64 quad_perm:[0,1,2,3] row_mask:0x8 bank_mask:0xf\n" \
"v_mov_b32_dpp v33, v65 quad_perm:[0,1,2,3] row_mask:0x8 bank_mask:0xf\n" \
"v_mul_f32_e32 v32, v34, v32\n" \
"v_mul_f32_e32 v33, v34, v33\n" \
"v_mov_b32_e64 v38, v34\n" \
M_"v[164:167], a[240:243], a[220:223], v[164:167]\n" \
"v_sub_f32_e32 v44, 1.0, v45\n" \
"v_permlane16_swap_b32_e32 v34, v38\n" \
"v_mov_b32_e32 v250, v34\n" \
"v_mov_b32_e32 v66, v38\n" \
C_"v32, v32, v33\n" \
"v_permlane32_swap_b32_e32 v34, v250\n" \
"v_permlane32_swap_b32_e32 v38, v66\n" \
"v_mul_f32_e64 v192, v34, v192\n" \
"v_pk_mul_f32 v[164:165], v[164:165], s[70:71]\n" \
"v_pk_mul_f32 v[166:167], v[166:167], s[70:71]\n" \
C_"v164, v164, v165\n" \
C_"v165, v166, v167\n" \
C_"v166, v168, v169\n" \
C_"v167, v170, v171\n" \
"ds_write_b128 v21, v[164:167] offset:28672\n" \
"v_mul_f32_e32 v193, v34, v193\n" \
"v_mul_f32_e32 v208, v38, v208\n" \
"v_mul_f32_e32 v209, v38, v209\n" \
"v_mul_f32_e32 v224, v250, v224\n" \
"v_mul_f32_e32 v225, v250, v225\n" \
"v_mul_f32_e32 v240, v66, v240\n" \
"v_mul_f32_e32 v241, v66, v241\n" \
"v_mul_f32_e32 v194, v34, v194\n" \
"v_mul_f32_e64 v195, v34, v195\n" \
C_"v192, v192, v193\n" \
"v_mul_f32_e32 v210, v38, v210\n" \
"v_mul_f32_e32 v211, v38, v211\n" \
C_"v208, v208, v209\n" \
"v_mul_f32_e32 v226, v250, v226\n" \
"v_mul_f32_e32 v227, v250, v227\n" \
C_"v224, v224, v225\n" \
"v_mul_f32_e32 v242, v66, v242\n" \
"v_mul_f32_e32 v243, v66, v243\n" \
C_"v240, v240, v241\n" \
C_"v193, v194, v195\n" \
"s_add_u32 s84, s48, 0x0\n" \
"s_nop 0\n" \
C_"v209, v210, v211\n" \
"s_add_u32 s85, s48, 0x24800\n" \
C_"v225, v226, v227\n" \
"s_add_u32 s86, s48, 0x49000\n" \
C_"v241, v242, v243\n" \
"s_add_u32 s87, s48, 0x6d800\n" \
"ds_write_b32 v26, v44\n" \
"buffer_store_dword v32, v25, s[80:83], s49 offen sc1\n" \
"buffer_store_dwordx2 v[192:193], v24, s[80:83], s84 offen sc1\n" \
"buffer_store_dwordx2 v[208:209], v24, s[80:83], s85 offen sc1\n" \
"buffer_store_dwordx2 v[224:225], v24, s[80:83], s86 offen sc1\n" \
"buffer_store_dwordx2 v[240:241], v24, s[80:83], s87 offen sc1\n" \
"s_branch L_end_%=\n" \
"L_poll60_%=:\n" \
"s_mov_b32 s84, 0\n" \
"L_pl60_%=:\n" \
"s_sleep 1\n" \
"v_bfe_u32 v44, v20, 2, 5\n" \
"buffer_load_dword v44, v44, s[80:83], s38 offen offset:128 sc1\n" \
"s_waitcnt vmcnt(0)\n" \
"v_cmp_ne_u32_e32 vcc, s62, v44\n" \
"s_add_u32 s84, s84, 1\n" \
"s_nop 3\n" \
"s_cbranch_vccz L_pollr60_%=\n" \
"s_cmp_gt_u32 s84, 0x400000\n" \
"s_cbranch_scc0 L_pl60_%=\n" \
"s_branch L_pollr60_%=\n" \
"L_poll160_%=:\n" \
"s_mov_b32 s84, 0\n" \
"L_pl160_%=:\n" \
"s_sleep 1\n" \
"v_bfe_u32 v44, v20, 2, 5\n" \
"buffer_load_dword v44, v44, s[80:83], s38 offen offset:0 sc1\n" \
"s_waitcnt vmcnt(0)\n" \
"v_cmp_ne_u32_e32 vcc, s62, v44\n" \
"s_add_u32 s84, s84, 1\n" \
"s_nop 3\n" \
"s_cbranch_vccz L_pollr160_%=\n" \
"s_cmp_gt_u32 s84, 0x400000\n" \
"s_cbranch_scc0 L_pl160_%=\n" \
"s_branch L_pollr160_%=\n" \
".fill (L_pb + 42548 - .) / 4, 4, 0xBF800000\n" \
"L_w1_%=:\n" \
"s_waitcnt vmcnt(19)\n" \
"s_barrier\n" \
X_"a[160:161], v32 offset:0\n" \
X_"a[162:163], v32 offset:1024\n" \
R_"a[128:131], v28 offset:16384\n" \
X_"a[224:225], v32 offset:8192\n" \
X_"a[226:227], v32 offset:9216\n" \
R_"a[192:195], v29 offset:16384\n" \
R_"a[132:135], v28 offset:18432\n" \
R_"a[196:199], v29 offset:18432\n" \
X_"a[164:165], v33 offset:0\n" \
X_"a[166:167], v33 offset:1024\n" \
X_"a[228:229], v33 offset:8192\n" \
X_"a[230:231], v33 offset:9216\n" \
R_"a[136:139], v28 offset:20480\n" \
R_"a[200:203], v29 offset:20480\n" \
"s_waitcnt lgkmcnt(11)\n" \
M_"a[0:3], a[128:131], a[160:163], 0\n" \
X_"a[168:169], v34 offset:0\n" \
"s_lshr_b32 s4, s2, 1\n" \
"s_waitcnt lgkmcnt(9)\n" \
M_"a[0:3], a[192:195], a[224:227], a[0:3]\n" \
X_"a[170:171], v34 offset:1024\n" \
"s_and_b32 s4, s4, 0x7c\n" \
"s_waitcnt lgkmcnt(9)\n" \
M_"a[16:19], a[132:135], a[160:163], 0\n" \
X_"a[232:233], v34 offset:8192\n" \
"s_nop 5\n" \
"s_waitcnt lgkmcnt(9)\n" \
M_"a[16:19], a[196:199], a[224:227], a[16:19]\n" \
X_"a[234:235], v34 offset:9216\n" \
"v_mov_b32_e32 v213, s13\n" \
"s_waitcnt lgkmcnt(8)\n" \
M_"a[20:23], a[132:135], a[164:167], 0\n" \
R_"a[140:143], v28 offset:22528\n" \
"s_or_b32 s4, s4, s3\n" \
"s_waitcnt lgkmcnt(7)\n" \
M_"a[20:23], a[196:199], a[228:231], a[20:23]\n" \
R_"a[204:207], v29 offset:22528\n" \
"s_nop 5\n" \
M_"a[4:7], a[128:131], a[164:167], 0\n" \
X_"a[172:173], v35 offset:0\n" \
"v_lshrrev_b32_e64 v212, 4, v20\n" \
M_"a[4:7], a[192:195], a[228:231], a[4:7]\n" \
X_"a[174:175], v35 offset:1024\n" \
"v_mad_u64_u32 v[214:215], s[4:5], s4, 36, v[212:213]\n" \
"s_waitcnt lgkmcnt(9)\n" \
M_"a[32:35], a[136:139], a[160:163], 0\n" \
X_"a[236:237], v35 offset:8192\n" \
"s_nop 5\n" \
"s_waitcnt lgkmcnt(9)\n" \
M_"a[32:35], a[200:203], a[224:227], a[32:35]\n" \
X_"a[238:239], v35 offset:9216\n" \
"v_mov_b32_e32 v217, s9\n" \
M_"a[36:39], a[136:139], a[164:167], 0\n" \
R_"a[144:147], v28 offset:0\n" \
"s_movk_i32 s4, 0x240\n" \
M_"a[36:39], a[200:203], a[228:231], a[36:39]\n" \
R_"a[208:211], v29 offset:0\n" \
"s_nop 5\n" \
"s_waitcnt lgkmcnt(10)\n" \
M_"a[40:43], a[136:139], a[168:171], 0\n" \
R_"a[148:151], v28 offset:2048\n" \
"v_cmp_gt_u32_e32 vcc, s4, v20\n" \
"s_waitcnt lgkmcnt(9)\n" \
M_"a[40:43], a[200:203], a[232:235], a[40:43]\n" \
R_"a[212:215], v29 offset:2048\n" \
"s_lshl_b32 s4, s2, 3\n" \
M_"a[24:27], a[132:135], a[168:171], 0\n" \
R_"a[152:155], v28 offset:4096\n" \
"s_nop 5\n" \
M_"a[24:27], a[196:199], a[232:235], a[24:27]\n" \
R_"a[216:219], v29 offset:4096\n" \
"s_and_b32 s4, s4, 56\n" \
M_"a[8:11], a[128:131], a[168:171], 0\n" \
R_"a[156:159], v28 offset:6144\n" \
"v_cndmask_b32_e64 v217, v217, v213, vcc\n" \
M_"a[8:11], a[192:195], a[232:235], a[8:11]\n" \
R_"a[220:223], v29 offset:6144\n" \
"s_mov_b32 m0, s32\n" \
"s_nop 5\n" \
"s_waitcnt lgkmcnt(13)\n" \
M_"a[48:51], a[140:143], a[160:163], 0\n" \
"s_mov_b32 exec_lo, 0xff000000\n" \
"s_mov_b32 exec_hi, 0x0\n" \
"global_load_lds_dword v[184:185], off\n" \
"s_mov_b64 exec, -1\n" \
"s_waitcnt lgkmcnt(12)\n" \
M_"a[48:51], a[204:207], a[224:227], a[48:51]\n" \
X_"a[176:177], v36 offset:0\n" \
"v_lshlrev_b32_e64 v213, 3, v214\n" \
M_"a[52:55], a[140:143], a[164:167], 0\n" \
"s_nop 5\n" \
X_"a[178:179], v36 offset:1024\n" \
M_"a[52:55], a[204:207], a[228:231], a[52:55]\n" \
"s_mov_b32 m0, s32\n" \
X_"a[240:241], v36 offset:8192\n" \
M_"a[56:59], a[140:143], a[168:171], 0\n" \
"s_mov_b32 exec_lo, 0xffff\n" \
"s_mov_b32 exec_hi, 0x0\n" \
"global_load_lds_dword v[186:187], off\n" \
"s_mov_b64 exec, -1\n" \
M_"a[56:59], a[204:207], a[232:235], a[56:59]\n" \
"s_nop 5\n" \
X_"a[242:243], v36 offset:9216\n" \
"s_waitcnt lgkmcnt(14)\n" \
M_"a[60:63], a[140:143], a[172:175], 0\n" \
X_"a[180:181], v37 offset:0\n" \
"v_and_b32_e32 v213, 0x1ffc0, v213\n" \
"s_waitcnt lgkmcnt(13)\n" \
M_"a[60:63], a[204:207], a[236:239], a[60:63]\n" \
X_"a[182:183], v37 offset:1024\n" \
"v_and_b32_e32 v214, 7, v214\n" \
M_"a[44:47], a[136:139], a[172:175], 0\n" \
"s_nop 5\n" \
X_"a[244:245], v37 offset:8192\n" \
M_"a[44:47], a[200:203], a[236:239], a[44:47]\n" \
X_"a[246:247], v37 offset:9216\n" \
"v_or3_b32 v213, v214, s4, v213\n" \
M_"a[28:31], a[132:135], a[172:175], 0\n" \
X_"a[184:185], v38 offset:0\n" \
"s_lshl_b32 s4, s2, 2\n" \
M_"a[28:31], a[196:199], a[236:239], a[28:31]\n" \
"s_nop 5\n" \
X_"a[186:187], v38 offset:1024\n" \
M_"a[12:15], a[128:131], a[172:175], 0\n" \
"s_mov_b64 exec, 0\n" \
"s_swappc_b64 s[88:89], s[86:87]\n" \
"s_mov_b64 exec, -1\n" \
"s_add_u32 s86, s86, 0x2000\n" \
"s_addc_u32 s87, s87, 0\n" \
M_"a[12:15], a[192:195], a[236:239], a[12:15]\n" \
"s_or_b32 s4, s4, s3\n" \
"s_mul_i32 s4, s4, 18\n" \
"s_waitcnt lgkmcnt(15)\n" \
M_"a[76:79], a[144:147], a[172:175], 0\n" \
"s_nop 5\n" \
X_"a[248:249], v38 offset:8192\n" \
M_"a[76:79], a[208:211], a[236:239], a[76:79]\n" \
"v_min_u32_e32 v212, 53, v212\n" \
"s_sub_i32 s4, s4, 36\n" \
M_"a[72:75], a[144:147], a[168:171], 0\n" \
"v_add_u32_e32 v212, s4, v212\n" \
"v_mov_b32_e64 v215, s12\n" \
M_"a[72:75], a[208:211], a[232:235], a[72:75]\n" \
"s_nop 5\n" \
X_"a[250:251], v38 offset:9216\n" \
M_"a[68:71], a[144:147], a[164:167], 0\n" \
"v_mov_b32_e32 v216, s8\n" \
"v_cndmask_b32_e64 v212, v212, v213, vcc\n" \
M_"a[68:71], a[208:211], a[228:231], a[68:71]\n" \
"v_cndmask_b32_e32 v216, v216, v215, vcc\n" \
"v_lshlrev_b32_e32 v212, 7, v212\n" \
M_"a[64:67], a[144:147], a[160:163], 0\n" \
"s_nop 5\n" \
"v_mov_b32_e32 v213, 0\n" \
M_"a[64:67], a[208:211], a[224:227], a[64:67]\n" \
X_"a[188:189], v39 offset:0\n" \
"v_lshl_add_u64 v[188:189], v[216:217], 0, v[212:213]\n" \
M_"a[80:83], a[148:151], a[160:163], 0\n" \
"s_movk_i32 s5, 0xd80\n" \
"s_lshr_b32 s6, s2, 5\n" \
"s_waitcnt lgkmcnt(15)\n" \
M_"a[80:83], a[212:215], a[224:227], a[80:83]\n" \
"s_nop 5\n" \
"v_lshrrev_b32_e64 v212, 4, v20\n" \
M_"a[84:87], a[148:151], a[164:167], 0\n" \
X_"a[190:191], v39 offset:1024\n" \
"v_lshl_or_b32 v215, s3, 6, v212\n" \
M_"a[84:87], a[212:215], a[228:231], a[84:87]\n" \
"v_mul_u32_u24_e32 v216, 0x2aab, v215\n" \
"v_lshrrev_b32_e64 v216, 16, v216\n" \
M_"a[88:91], a[148:151], a[168:171], 0\n" \
"s_nop 5\n" \
"v_mad_i32_i24 v217, v216, -6, v215\n" \
M_"a[88:91], a[212:215], a[232:235], a[88:91]\n" \
X_"a[252:253], v39 offset:8192\n" \
"v_add_u32_e32 v218, 0xfffffe50, v215\n" \
M_"a[92:95], a[148:151], a[172:175], 0\n" \
"s_mul_i32 s35, s6, 0x48\n" \
"v_lshrrev_b32_e32 v219, 1, v217\n" \
M_"a[92:95], a[212:215], a[236:239], a[92:95]\n" \
"s_nop 5\n" \
"v_add_u32_e32 v218, s35, v216\n" \
M_"a[108:111], a[152:155], a[172:175], 0\n" \
X_"a[254:255], v39 offset:9216\n" \
"v_mul_u32_u24_e32 v219, 0x480, v219\n" \
M_"a[108:111], a[216:219], a[236:239], a[108:111]\n" \
"v_and_b32_e64 v214, 16, v20\n" \
"v_mad_u32_u24 v219, v218, s5, v219\n" \
M_"a[104:107], a[152:155], a[168:171], 0\n" \
"s_mov_b64 exec, 0\n" \
"s_swappc_b64 s[88:89], s[86:87]\n" \
"s_mov_b64 exec, -1\n" \
"s_andn2_b32 s85, 2, s3\n" \
"s_lshl_b32 s85, s85, 12\n" \
"s_add_u32 s86, s86, s85\n" \
"s_addc_u32 s87, s87, 0\n" \
"s_nop 5\n" \
M_"a[104:107], a[216:219], a[232:235], a[104:107]\n" \
"v_or_b32_e32 v192, v219, v214\n" \
"v_mov_b32_e32 v220, 2.0\n" \
M_"a[100:103], a[152:155], a[164:167], 0\n" \
"v_mov_b32_e32 v222, 7\n" \
"v_lshlrev_b32_e32 v220, 7, v218\n" \
M_"a[100:103], a[216:219], a[228:231], a[100:103]\n" \
"s_nop 5\n" \
"v_lshrrev_b32_e32 v218, 1, v218\n" \
M_"a[96:99], a[152:155], a[160:163], 0\n" \
"v_and_b32_e32 v218, 4, v218\n" \
"v_and_b32_e32 v216, 2, v216\n" \
M_"a[96:99], a[216:219], a[224:227], a[96:99]\n" \
"s_mul_i32 s7, s6, 0x2400\n" \
"s_sub_i32 s34, 0x22000, s7\n" \
"s_waitcnt lgkmcnt(15)\n" \
M_"a[112:115], a[156:159], a[160:163], 0\n" \
"s_nop 5\n" \
"v_bitop3_b32 v216, v217, v218, v216 bitop3:0x1e\n" \
M_"a[112:115], a[220:223], a[224:227], a[112:115]\n" \
"v_or_b32_e32 v213, 0x24400, v20\n" \
"v_lshlrev_b32_e64 v216, 4, v216\n" \
M_"a[116:119], a[156:159], a[164:167], 0\n" \
"v_add3_u32 v196, v220, s34, v216\n" \
"v_mov_b32_e64 v216, v213\n" \
M_"a[116:119], a[220:223], a[228:231], a[116:119]\n" \
"s_nop 5\n" \
"s_movk_i32 s36, 0x1b0\n" \
M_"a[120:123], a[156:159], a[168:171], 0\n" \
"v_or_b32_e32 v216, 0x100, v215\n" \
"v_mul_u32_u24_e32 v217, 0x2aab, v216\n" \
M_"a[120:123], a[220:223], a[232:235], a[120:123]\n" \
"v_add_u32_e32 v215, 0xffffff50, v215\n" \
"v_lshrrev_b32_e64 v217, 16, v217\n" \
M_"a[124:127], a[156:159], a[172:175], 0\n" \
"s_nop 5\n" \
"v_mov_b32_e32 v220, v215\n" \
"s_waitcnt vmcnt(5) lgkmcnt(0)\n" \
M_"a[124:127], a[220:223], a[236:239], a[124:127]\n" \
"s_barrier\n" \
M_"v[52:55], a[128:131], a[176:179], 0\n" \
"s_add_u32 m0, s30, 0x0\n" \
"s_movk_i32 s84, 0x100\n" \
M_"v[52:55], a[192:195], a[240:243], v[52:55]\n" \
B_"v22, s[64:67], s84 offen offset:0 lds\n" \
M_"v[56:59], a[128:131], a[180:183], 0\n" \
"s_nop 5\n" \
X_"a[160:161], v32 offset:32768\n" \
M_"v[56:59], a[192:195], a[244:247], v[56:59]\n" \
"s_movk_i32 s84, 0x1d00\n" \
M_"v[60:63], a[128:131], a[184:187], 0\n" \
B_"v23, s[64:67], s84 offen offset:1024 lds\n" \
M_"v[60:63], a[192:195], a[248:251], v[60:63]\n" \
"s_nop 5\n" \
X_"a[162:163], v32 offset:33792\n" \
M_"v[64:67], a[128:131], a[188:191], 0\n" \
X_"a[224:225], v32 offset:40960\n" \
"v_mad_i32_i24 v218, v217, -6, v216\n" \
M_"v[64:67], a[192:195], a[252:255], v[64:67]\n" \
R_"a[128:131], v28 offset:49152\n" \
"v_cmp_gt_u32_e32 vcc, s36, v216\n" \
M_"v[80:83], a[132:135], a[188:191], 0\n" \
R_"a[192:195], v29 offset:49152\n" \
"s_nop 5\n" \
M_"v[80:83], a[196:199], a[252:255], v[80:83]\n" \
"s_movk_i32 s84, 0x3900\n" \
M_"v[76:79], a[132:135], a[184:187], 0\n" \
B_"v22, s[64:67], s84 offen offset:2048 lds\n" \
M_"v[76:79], a[196:199], a[248:251], v[76:79]\n" \
"s_movk_i32 s84, 0x5500\n" \
"s_nop 5\n" \
M_"v[72:75], a[132:135], a[180:183], 0\n" \
B_"v23, s[64:67], s84 offen offset:3072 lds\n" \
M_"v[72:75], a[196:199], a[244:247], v[72:75]\n" \
"s_add_u32 m0, s31, 0x0\n" \
"s_mov_b32 s84, 0x100000\n" \
M_"v[68:71], a[132:135], a[176:179], 0\n" \
B_"v26, s[68:71], s84 offen offset:0 lds\n" \
"s_nop 5\n" \
M_"v[68:71], a[196:199], a[240:243], v[68:71]\n" \
R_"a[132:135], v28 offset:51200\n" \
"v_mov_b32_e64 v221, 7\n" \
M_"v[84:87], a[136:139], a[176:179], 0\n" \
R_"a[196:199], v29 offset:51200\n" \
"v_lshrrev_b32_e64 v222, 1, v218\n" \
M_"v[84:87], a[200:203], a[240:243], v[84:87]\n" \
"s_addk_i32 s84, 0x7c00\n" \
"s_nop 5\n" \
M_"v[88:91], a[136:139], a[180:183], 0\n" \
B_"v26, s[68:71], s84 offen offset:1024 lds\n" \
M_"v[88:91], a[200:203], a[244:247], v[88:91]\n" \
X_"a[226:227], v32 offset:41984\n" \
"v_cndmask_b32_e64 v217, v220, v217, vcc\n" \
M_"v[92:95], a[136:139], a[184:187], 0\n" \
"s_addk_i32 s84, 0x7c00\n" \
"s_nop 5\n" \
M_"v[92:95], a[200:203], a[248:251], v[92:95]\n" \
B_"v27, s[68:71], s84 offen offset:2048 lds\n" \
M_"v[96:99], a[136:139], a[188:191], 0\n" \
X_"a[164:165], v33 offset:32768\n" \
"s_movk_i32 s4, 0x48\n" \
M_"v[96:99], a[200:203], a[252:255], v[96:99]\n" \
R_"a[136:139], v28 offset:53248\n" \
"s_nop 5\n" \
M_"v[112:115], a[140:143], a[188:191], 0\n" \
R_"a[200:203], v29 offset:53248\n" \
"v_add_u32_e64 v220, s35, v217\n" \
M_"v[112:115], a[204:207], a[252:255], v[112:115]\n" \
"s_addk_i32 s84, 0x7c00\n" \
M_"v[108:111], a[140:143], a[184:187], 0\n" \
B_"v27, s[68:71], s84 offen offset:3072 lds\n" \
"s_mov_b64 exec, 0\n" \
"s_swappc_b64 s[88:89], s[86:87]\n" \
"s_mov_b64 exec, -1\n" \
"s_nop 5\n" \
M_"v[108:111], a[204:207], a[248:251], v[108:111]\n" \
"s_add_u32 m0, s30, 0x1000\n" \
"s_mov_b32 s84, 0x8100\n" \
M_"v[104:107], a[140:143], a[180:183], 0\n" \
B_"v22, s[64:67], s84 offen offset:0 lds\n" \
M_"v[104:107], a[204:207], a[244:247], v[104:107]\n" \
"s_nop 5\n" \
X_"a[166:167], v33 offset:33792\n" \
M_"v[100:103], a[140:143], a[176:179], 0\n" \
X_"a[228:229], v33 offset:40960\n" \
"v_mul_u32_u24_e32 v222, 0x480, v222\n" \
M_"v[100:103], a[204:207], a[240:243], v[100:103]\n" \
R_"a[140:143], v28 offset:55296\n" \
"v_mad_u32_u24 v222, v220, s5, v222\n" \
M_"v[116:119], a[144:147], a[176:179], 0\n" \
R_"a[204:207], v29 offset:55296\n" \
"s_nop 5\n" \
M_"v[116:119], a[208:211], a[240:243], v[116:119]\n" \
"s_addk_i32 s84, 0x1c00\n" \
M_"v[120:123], a[144:147], a[180:183], 0\n" \
B_"v23, s[64:67], s84 offen offset:1024 lds\n" \
M_"v[120:123], a[208:211], a[244:247], v[120:123]\n" \
".fill (L_pb + 44768 - .) / 4, 4, 0xBF800000\n" \
"L_pp11:\n" \
"s_cbranch_execz L_pret\n" \
"s_addk_i32 s84, 0x1c00\n" \
"s_nop 5\n" \
M_"v[124:127], a[144:147], a[184:187], 0\n" \
B_"v22, s[64:67], s84 offen offset:2048 lds\n" \
M_"v[124:127], a[208:211], a[248:251], v[124:127]\n" \
"s_addk_i32 s84, 0x1c00\n" \
M_"v[128:131], a[144:147], a[188:191], 0\n" \
B_"v23, s[64:67], s84 offen offset:3072 lds\n" \
"s_nop 5\n" \
M_"v[128:131], a[208:211], a[252:255], v[128:131]\n" \
R_"a[144:147], v28 offset:32768\n" \
"v_cmp_gt_u32_e64 s[4:5], s4, v215\n" \
M_"v[144:147], a[148:151], a[188:191], 0\n" \
R_"a[208:211], v29 offset:32768\n" \
"v_cndmask_b32_e32 v215, v221, v218, vcc\n" \
M_"v[144:147], a[212:215], a[252:255], v[144:147]\n" \
"s_add_u32 m0, s31, 0x4000\n" \
"s_mov_b32 s84, 0x100000\n" \
"s_nop 5\n" \
M_"v[140:143], a[148:151], a[184:187], 0\n" \
B_"v26, s[72:75], s84 offen offset:0 lds\n" \
M_"v[140:143], a[212:215], a[248:251], v[140:143]\n" \
X_"a[230:231], v33 offset:41984\n" \
"v_lshrrev_b32_e32 v218, 1, v220\n" \
M_"v[136:139], a[148:151], a[180:183], 0\n" \
"s_addk_i32 s84, 0x7c00\n" \
"s_nop 5\n" \
M_"v[136:139], a[212:215], a[244:247], v[136:139]\n" \
B_"v26, s[72:75], s84 offen offset:1024 lds\n" \
M_"v[132:135], a[148:151], a[176:179], 0\n" \
X_"a[168:169], v34 offset:32768\n" \
"v_or_b32_e64 v214, v222, v214\n" \
M_"v[132:135], a[212:215], a[240:243], v[132:135]\n" \
R_"a[148:151], v28 offset:34816\n" \
"s_nop 5\n" \
M_"v[148:151], a[152:155], a[176:179], 0\n" \
R_"a[212:215], v29 offset:34816\n" \
"v_mov_b32_e32 v222, 2.0\n" \
M_"v[148:151], a[216:219], a[240:243], v[148:151]\n" \
X_"a[170:171], v34 offset:33792\n" \
"v_and_b32_e64 v218, 4, v218\n" \
M_"v[152:155], a[152:155], a[180:183], 0\n" \
"s_nop 5\n" \
X_"a[232:233], v34 offset:40960\n" \
M_"v[152:155], a[216:219], a[244:247], v[152:155]\n" \
X_"a[234:235], v34 offset:41984\n" \
"v_and_b32_e32 v217, 2, v217\n" \
M_"v[156:159], a[152:155], a[184:187], 0\n" \
X_"a[172:173], v35 offset:32768\n" \
"v_bitop3_b32 v215, v215, v218, v217 bitop3:0x1e\n" \
M_"v[156:159], a[216:219], a[248:251], v[156:159]\n" \
"s_nop 5\n" \
X_"a[174:175], v35 offset:33792\n" \
M_"v[160:163], a[152:155], a[188:191], 0\n" \
X_"a[236:237], v35 offset:40960\n" \
"v_lshlrev_b32_e32 v223, 1, v220\n" \
M_"v[160:163], a[216:219], a[252:255], v[160:163]\n" \
R_"a[152:155], v28 offset:36864\n" \
"v_mov_b32_e64 v224, 2.0\n" \
M_"v[176:179], a[156:159], a[188:191], 0\n" \
R_"a[216:219], v29 offset:36864\n" \
"s_nop 5\n" \
M_"v[176:179], a[220:223], a[252:255], v[176:179]\n" \
X_"a[238:239], v35 offset:41984\n" \
"v_cndmask_b32_e32 v193, v222, v214, vcc\n" \
M_"v[172:175], a[156:159], a[184:187], 0\n" \
"v_lshlrev_b32_e32 v214, 7, v220\n" \
"v_lshlrev_b32_e32 v215, 4, v215\n" \
M_"v[172:175], a[220:223], a[248:251], v[172:175]\n" \
"s_nop 5\n" \
"s_mul_i32 s39, s3, 18\n" \
M_"v[168:171], a[156:159], a[180:183], 0\n" \
"v_add3_u32 v214, v214, s34, v215\n" \
"v_cndmask_b32_e64 v195, v224, v223, s[4:5]\n" \
M_"v[168:171], a[220:223], a[244:247], v[168:171]\n" \
"s_movk_i32 s4, 0x1f8\n" \
"v_mov_b32_e32 v215, v213\n" \
M_"v[164:167], a[156:159], a[176:179], 0\n" \
"s_nop 5\n" \
"s_add_i32 s35, s35, s39\n" \
M_"v[164:167], a[220:223], a[240:243], v[164:167]\n" \
R_"a[156:159], v28 offset:38912\n" \
R_"a[220:223], v29 offset:38912\n" \
"s_waitcnt lgkmcnt(15)\n" \
M_"a[0:3], a[128:131], a[160:163], a[0:3]\n" \
"s_addk_i32 s84, 0x7c00\n" \
M_"a[0:3], a[192:195], a[224:227], a[0:3]\n" \
B_"v27, s[72:75], s84 offen offset:2048 lds\n" \
M_"a[4:7], a[128:131], a[164:167], a[4:7]\n" \
X_"a[176:177], v36 offset:32768\n" \
"v_cmp_gt_u32_e32 vcc, s4, v216\n" \
"s_waitcnt lgkmcnt(15)\n" \
M_"a[4:7], a[192:195], a[228:231], a[4:7]\n" \
"s_addk_i32 s84, 0x7c00\n" \
"s_waitcnt lgkmcnt(11)\n" \
M_"a[8:11], a[128:131], a[168:171], a[8:11]\n" \
B_"v27, s[72:75], s84 offen offset:3072 lds\n" \
"s_waitcnt lgkmcnt(9)\n" \
M_"a[8:11], a[192:195], a[232:235], a[8:11]\n" \
X_"a[178:179], v36 offset:33792\n" \
"v_add_u32_e32 v212, s35, v212\n" \
"s_waitcnt lgkmcnt(8)\n" \
M_"a[12:15], a[128:131], a[172:175], a[12:15]\n" \
X_"a[240:241], v36 offset:40960\n" \
"v_mov_b32_e32 v219, 2.0\n" \
"s_waitcnt lgkmcnt(5)\n" \
M_"a[12:15], a[192:195], a[236:239], a[12:15]\n" \
"v_cndmask_b32_e32 v197, v215, v214, vcc\n" \
"s_movk_i32 s35, 0x120\n" \
M_"a[28:31], a[132:135], a[172:175], a[28:31]\n" \
"v_mul_u32_u24_e32 v214, 0xd80, v212\n" \
"v_cmp_gt_u32_e32 vcc, s35, v20\n" \
M_"a[28:31], a[196:199], a[236:239], a[28:31]\n" \
X_"a[242:243], v36 offset:41984\n" \
"v_lshrrev_b32_e64 v215, 1, v212\n" \
M_"a[24:27], a[132:135], a[168:171], a[24:27]\n" \
"s_and_b32 s4, s2, 7\n" \
"v_cndmask_b32_e32 v194, v219, v214, vcc\n" \
M_"a[24:27], a[196:199], a[232:235], a[24:27]\n" \
"v_lshlrev_b32_e32 v214, 7, v212\n" \
"v_lshrrev_b32_e32 v212, 2, v212\n" \
M_"a[20:23], a[132:135], a[164:167], a[20:23]\n" \
"v_and_b32_e64 v212, 2, v212\n" \
"v_and_or_b32 v212, v215, 1, v212\n" \
M_"a[20:23], a[196:199], a[228:231], a[20:23]\n" \
X_"a[180:181], v37 offset:32768\n" \
"s_lshl_b32 s5, s4, 2\n" \
M_"a[16:19], a[132:135], a[160:163], a[16:19]\n" \
"s_bfe_u32 s38, s2, 0x20003\n" \
"v_lshlrev_b32_e32 v212, 5, v212\n" \
M_"a[16:19], a[196:199], a[224:227], a[16:19]\n" \
"s_movk_i32 s35, 0x60\n" \
"s_or_b32 s5, s5, s38\n" \
M_"a[32:35], a[136:139], a[160:163], a[32:35]\n" \
"s_mov_b32 m0, s32\n" \
X_"a[182:183], v37 offset:33792\n" \
M_"a[32:35], a[200:203], a[224:227], a[32:35]\n" \
"s_mov_b32 exec_lo, 0x0\n" \
"s_mov_b32 exec_hi, 0xff\n" \
"global_load_lds_dword v[184:185], off\n" \
"s_mov_b64 exec, -1\n" \
M_"a[36:39], a[136:139], a[164:167], a[36:39]\n" \
"v_bitop3_b32 v212, v212, s35, v214 bitop3:0x36\n" \
"s_lshr_b32 s36, s2, 3\n" \
M_"a[36:39], a[200:203], a[228:231], a[36:39]\n" \
X_"a[244:245], v37 offset:40960\n" \
"v_add_u32_e64 v212, s34, v212\n" \
M_"a[40:43], a[136:139], a[168:171], a[40:43]\n" \
"s_mul_i32 s34, s5, 0x12000\n" \
"s_or_b32 s40, s34, 0x6000000\n" \
M_"a[40:43], a[200:203], a[232:235], a[40:43]\n" \
"s_mov_b32 m0, s32\n" \
"s_mul_i32 s34, s36, 0x90000\n" \
M_"a[44:47], a[136:139], a[172:175], a[44:47]\n" \
"s_mov_b32 exec_lo, 0xffff0000\n" \
"s_mov_b32 exec_hi, 0x0\n" \
"global_load_lds_dword v[186:187], off\n" \
"s_mov_b64 exec, -1\n" \
M_"a[44:47], a[200:203], a[236:239], a[44:47]\n" \
X_"a[246:247], v37 offset:41984\n" \
"s_lshl_b32 s36, s3, 14\n" \
M_"a[60:63], a[140:143], a[172:175], a[60:63]\n" \
"s_add_i32 s34, s34, s36\n" \
"s_lshl_b32 s38, s5, 8\n" \
M_"a[60:63], a[204:207], a[236:239], a[60:63]\n" \
"s_add_i32 s41, s34, 0x4240000\n" \
"s_mul_i32 s34, s5, 36\n" \
M_"a[56:59], a[140:143], a[168:171], a[56:59]\n" \
X_"a[184:185], v38 offset:32768\n" \
"s_add_i32 s5, s7, s40\n" \
M_"a[56:59], a[204:207], a[232:235], a[56:59]\n" \
"s_addk_i32 s5, 0x2000\n" \
"s_mov_b32 s37, 2.0\n" \
M_"a[52:55], a[140:143], a[164:167], a[52:55]\n" \
"s_or_b32 s38, s38, 0x5be8000\n" \
M_"a[52:55], a[204:207], a[228:231], a[52:55]\n" \
X_"a[186:187], v38 offset:33792\n" \
M_"a[48:51], a[140:143], a[160:163], a[48:51]\n" \
"s_cmp_lg_u32 s3, 0\n" \
"s_cselect_b32 s36, s37, s5\n" \
M_"a[48:51], a[204:207], a[224:227], a[48:51]\n" \
"s_lshl_b32 s5, s6, 2\n" \
"s_or_b32 s5, s38, s5\n" \
M_"a[64:67], a[144:147], a[160:163], a[64:67]\n" \
X_"a[248:249], v38 offset:40960\n" \
"s_lshl_b32 s35, s3, 10\n" \
M_"a[64:67], a[208:211], a[224:227], a[64:67]\n" \
"s_cmpk_lt_u32 s3, 3\n" \
"s_cselect_b32 s37, s37, s5\n" \
M_"a[68:71], a[144:147], a[164:167], a[68:71]\n" \
"s_lshl_b32 s5, s4, 16\n" \
"s_lshl_b32 s4, s4, 11\n" \
M_"a[68:71], a[208:211], a[228:231], a[68:71]\n" \
X_"a[250:251], v38 offset:41984\n" \
"v_cndmask_b32_e32 v198, v213, v212, vcc\n" \
M_"a[72:75], a[144:147], a[168:171], a[72:75]\n" \
"v_or_b32_e32 v212, s35, v20\n" \
"s_or_b32 s39, s40, s35\n" \
M_"a[72:75], a[208:211], a[232:235], a[72:75]\n" \
"s_or_b32 s55, s41, s4\n" \
"v_or_b32_e32 v199, 0x22000, v212\n" \
M_"a[76:79], a[144:147], a[172:175], a[76:79]\n" \
"s_add_i32 s35, s39, s7\n" \
"s_add_i32 s54, s41, s5\n" \
M_"a[76:79], a[208:211], a[236:239], a[76:79]\n" \
X_"a[188:189], v39 offset:32768\n" \
"s_add_i32 s55, s55, 0x80000\n" \
M_"a[92:95], a[148:151], a[172:175], a[92:95]\n" \
"v_mov_b32_e32 v215, 0xffff\n" \
"s_and_b32 s4, s3, 1\n" \
M_"a[92:95], a[212:215], a[236:239], a[92:95]\n" \
"s_lshl_b32 s5, s4, 4\n" \
"v_lshrrev_b32_e32 v212, 4, v20\n" \
M_"a[88:91], a[148:151], a[168:171], a[88:91]\n" \
X_"a[190:191], v39 offset:33792\n" \
"v_lshrrev_b32_e64 v218, 6, v20\n" \
M_"a[88:91], a[212:215], a[232:235], a[88:91]\n" \
"v_and_b32_e64 v219, 12, v218\n" \
"v_and_or_b32 v214, v212, 15, s5\n" \
M_"a[84:87], a[148:151], a[164:167], a[84:87]\n" \
"v_cmp_gt_u32_e64 vcc, v219, v214\n" \
"v_mov_b32_e32 v220, 0xffff0000\n" \
M_"a[84:87], a[212:215], a[228:231], a[84:87]\n" \
X_"a[252:253], v39 offset:40960\n" \
"s_lshl_b32 s5, s2, 2\n" \
M_"a[80:83], a[148:151], a[160:163], a[80:83]\n" \
"v_cndmask_b32_e64 v217, v215, 0, vcc\n" \
"v_cmp_lt_u32_e32 vcc, v219, v214\n" \
M_"a[80:83], a[212:215], a[224:227], a[80:83]\n" \
"v_mov_b32_e32 v213, s23\n" \
"s_or_b32 s5, s5, s3\n" \
M_"a[96:99], a[152:155], a[160:163], a[96:99]\n" \
X_"a[254:255], v39 offset:41984\n" \
"v_cndmask_b32_e64 v221, 0, v220, vcc\n" \
M_"a[96:99], a[216:219], a[224:227], a[96:99]\n" \
"v_or_b32_e32 v12, v217, v221\n" \
"v_or_b32_e32 v217, 2, v219\n" \
M_"a[100:103], a[152:155], a[164:167], a[100:103]\n" \
"v_cmp_gt_u32_e32 vcc, v217, v214\n" \
"v_mov_b32_e32 v216, s25\n" \
M_"a[100:103], a[216:219], a[228:231], a[100:103]\n" \
"s_movk_i32 s40, 0xf400\n" \
"v_cndmask_b32_e64 v221, v215, 0, vcc\n" \
M_"a[104:107], a[152:155], a[168:171], a[104:107]\n" \
"v_cmp_lt_u32_e32 vcc, v217, v214\n" \
"s_lshl_b32 s4, s4, 6\n" \
M_"a[104:107], a[216:219], a[232:235], a[104:107]\n" \
"v_cndmask_b32_e32 v217, 0, v220, vcc\n" \
"v_or_b32_e64 v13, v221, v217\n" \
M_"a[108:111], a[152:155], a[172:175], a[108:111]\n" \
"v_or_b32_e32 v217, 16, v219\n" \
"v_cmp_gt_u32_e32 vcc, v217, v214\n" \
M_"a[108:111], a[216:219], a[236:239], a[108:111]\n" \
"s_nop 0\n" \
"v_cndmask_b32_e64 v221, v215, 0, vcc\n" \
"v_cmp_lt_u32_e32 vcc, v217, v214\n" \
"s_waitcnt lgkmcnt(15)\n" \
M_"a[124:127], a[156:159], a[172:175], a[124:127]\n" \
"v_cndmask_b32_e32 v217, 0, v220, vcc\n" \
"v_or_b32_e64 v14, v221, v217\n" \
M_"a[124:127], a[220:223], a[236:239], a[124:127]\n" \
"v_or_b32_e32 v217, 18, v219\n" \
"v_cmp_gt_u32_e32 vcc, v217, v214\n" \
M_"a[120:123], a[156:159], a[168:171], a[120:123]\n" \
"s_nop 0\n" \
"v_cndmask_b32_e64 v215, v215, 0, vcc\n" \
"v_cmp_lt_u32_e32 vcc, v217, v214\n" \
M_"a[120:123], a[220:223], a[232:235], a[120:123]\n" \
"s_nop 0\n" \
"v_cndmask_b32_e32 v214, 0, v220, vcc\n" \
"v_or_b32_e64 v15, v215, v214\n" \
M_"a[116:119], a[156:159], a[164:167], a[116:119]\n" \
"v_mad_u64_u32 v[214:215], s[6:7], s5, 42, v[212:213]\n" \
"s_movk_i32 s5, 0x1440\n" \
M_"a[116:119], a[220:223], a[228:231], a[116:119]\n" \
"v_cmp_gt_u32_e32 vcc, s5, v214\n" \
"v_min_u32_e32 v217, 0xa43f, v214\n" \
M_"a[112:115], a[156:159], a[160:163], a[112:115]\n" \
"v_mov_b32_e32 v214, s24\n" \
"v_cndmask_b32_e32 v215, v216, v213, vcc\n" \
M_"a[112:115], a[220:223], a[224:227], a[112:115]\n" \
"s_waitcnt vmcnt(20)\n" \
"v_readfirstlane_b32 s62, v180\n" \
"s_nop 3\n" \
"s_add_u32 s62, s62, 0x9E3779B1\n" \
"s_waitcnt vmcnt(2) lgkmcnt(0)\n" \
"s_barrier\n" \
M_"v[52:55], a[128:131], a[176:179], v[52:55]\n" \
"s_add_u32 m0, s30, 0x8000\n" \
"s_movk_i32 s84, 0x180\n" \
M_"v[52:55], a[192:195], a[240:243], v[52:55]\n" \
B_"v22, s[64:67], s84 offen offset:0 lds\n" \
M_"v[56:59], a[128:131], a[180:183], v[56:59]\n" \
X_"a[160:161], v32 offset:0\n" \
"v_mov_b32_e64 v213, s22\n" \
M_"v[56:59], a[192:195], a[244:247], v[56:59]\n" \
"s_movk_i32 s84, 0x1d80\n" \
M_"v[60:63], a[128:131], a[184:187], v[60:63]\n" \
B_"v23, s[64:67], s84 offen offset:1024 lds\n" \
M_"v[60:63], a[192:195], a[248:251], v[60:63]\n" \
X_"a[162:163], v32 offset:1024\n" \
"s_movk_i32 s6, 0xffee\n" \
M_"v[64:67], a[128:131], a[188:191], v[64:67]\n" \
X_"a[224:225], v32 offset:8192\n" \
"v_cndmask_b32_e64 v214, v214, v213, vcc\n" \
M_"v[64:67], a[192:195], a[252:255], v[64:67]\n" \
R_"a[128:131], v28 offset:16384\n" \
"v_add_u32_e32 v213, 0xffffebc0, v217\n" \
M_"v[80:83], a[132:135], a[188:191], v[80:83]\n" \
R_"a[192:195], v29 offset:16384\n" \
"s_lshl_b32 s5, s2, 5\n" \
M_"v[80:83], a[196:199], a[252:255], v[80:83]\n" \
"s_movk_i32 s84, 0x3980\n" \
M_"v[76:79], a[132:135], a[184:187], v[76:79]\n" \
B_"v22, s[64:67], s84 offen offset:2048 lds\n" \
M_"v[76:79], a[196:199], a[248:251], v[76:79]\n" \
"s_movk_i32 s84, 0x5580\n" \
M_"v[72:75], a[132:135], a[180:183], v[72:75]\n" \
B_"v23, s[64:67], s84 offen offset:3072 lds\n" \
M_"v[72:75], a[196:199], a[244:247], v[72:75]\n" \
"s_add_u32 m0, s31, 0x8000\n" \
"s_mov_b32 s84, 0x180000\n" \
M_"v[68:71], a[132:135], a[176:179], v[68:71]\n" \
B_"v26, s[68:71], s84 offen offset:0 lds\n" \
M_"v[68:71], a[196:199], a[240:243], v[68:71]\n" \
R_"a[132:135], v28 offset:18432\n" \
"v_cndmask_b32_e32 v213, v213, v217, vcc\n" \
M_"v[84:87], a[136:139], a[176:179], v[84:87]\n" \
R_"a[196:199], v29 offset:18432\n" \
"v_lshlrev_b32_e64 v216, 7, v213\n" \
M_"v[84:87], a[200:203], a[240:243], v[84:87]\n" \
"s_addk_i32 s84, 0x7c00\n" \
M_"v[88:91], a[136:139], a[180:183], v[88:91]\n" \
B_"v26, s[68:71], s84 offen offset:1024 lds\n" \
M_"v[88:91], a[200:203], a[244:247], v[88:91]\n" \
X_"a[226:227], v32 offset:9216\n" \
"v_mul_u32_u24_e32 v213, 0xe39, v212\n" \
M_"v[92:95], a[136:139], a[184:187], v[92:95]\n" \
"s_addk_i32 s84, 0x7c00\n" \
M_"v[92:95], a[200:203], a[248:251], v[92:95]\n" \
B_"v27, s[68:71], s84 offen offset:2048 lds\n" \
M_"v[96:99], a[136:139], a[188:191], v[96:99]\n" \
X_"a[164:165], v33 offset:0\n" \
"v_mov_b32_e64 v217, 0\n" \
M_"v[96:99], a[200:203], a[252:255], v[96:99]\n" \
R_"a[136:139], v28 offset:20480\n" \
"v_lshrrev_b32_e64 v213, 16, v213\n" \
M_"v[112:115], a[140:143], a[188:191], v[112:115]\n" \
R_"a[200:203], v29 offset:20480\n" \
"v_lshl_add_u64 v[16:17], v[214:215], 0, v[216:217]\n" \
M_"v[112:115], a[204:207], a[252:255], v[112:115]\n" \
"s_addk_i32 s84, 0x7c00\n" \
"s_nop 0\n" \
M_"v[108:111], a[140:143], a[184:187], v[108:111]\n" \
B_"v27, s[68:71], s84 offen offset:3072 lds\n" \
M_"v[108:111], a[204:207], a[248:251], v[108:111]\n" \
"s_add_u32 m0, s30, 0x9000\n" \
"s_mov_b32 s84, 0x8180\n" \
M_"v[104:107], a[140:143], a[180:183], v[104:107]\n" \
B_"v22, s[64:67], s84 offen offset:0 lds\n" \
M_"v[104:107], a[204:207], a[244:247], v[104:107]\n" \
X_"a[166:167], v33 offset:1024\n" \
"v_mad_i32_i24 v214, v213, s6, v212\n" \
M_"v[100:103], a[140:143], a[176:179], v[100:103]\n" \
X_"a[228:229], v33 offset:8192\n" \
"s_add_i32 s5, s5, 32\n" \
M_"v[100:103], a[204:207], a[240:243], v[100:103]\n" \
R_"a[140:143], v28 offset:22528\n" \
"v_lshlrev_b32_e32 v215, 4, v214\n" \
M_"v[116:119], a[144:147], a[176:179], v[116:119]\n" \
R_"a[204:207], v29 offset:22528\n" \
"v_add_u32_e64 v216, s5, v215\n" \
M_"v[116:119], a[208:211], a[240:243], v[116:119]\n" \
"s_addk_i32 s84, 0x1c00\n" \
M_"v[120:123], a[144:147], a[180:183], v[120:123]\n" \
B_"v23, s[64:67], s84 offen offset:1024 lds\n" \
M_"v[120:123], a[208:211], a[244:247], v[120:123]\n" \
"s_addk_i32 s84, 0x1c00\n" \
M_"v[124:127], a[144:147], a[184:187], v[124:127]\n" \
B_"v22, s[64:67], s84 offen offset:2048 lds\n" \
M_"v[124:127], a[208:211], a[248:251], v[124:127]\n" \
"s_addk_i32 s84, 0x1c00\n" \
M_"v[128:131], a[144:147], a[188:191], v[128:131]\n" \
B_"v23, s[64:67], s84 offen offset:3072 lds\n" \
M_"v[128:131], a[208:211], a[252:255], v[128:131]\n" \
R_"a[144:147], v28 offset:0\n" \
"v_and_b32_e32 v216, 0xf0, v216\n" \
M_"v[144:147], a[148:151], a[188:191], v[144:147]\n" \
R_"a[208:211], v29 offset:0\n" \
"v_cmp_gt_u32_e32 vcc, 16, v214\n" \
M_"v[144:147], a[212:215], a[252:255], v[144:147]\n" \
"s_add_u32 m0, s31, 0xc000\n" \
"s_mov_b32 s84, 0x180000\n" \
M_"v[140:143], a[148:151], a[184:187], v[140:143]\n" \
B_"v26, s[72:75], s84 offen offset:0 lds\n" \
M_"v[140:143], a[212:215], a[248:251], v[140:143]\n" \
X_"a[230:231], v33 offset:9216\n" \
"s_movk_i32 s7, 0xf800\n" \
M_"v[136:139], a[148:151], a[180:183], v[136:139]\n" \
"s_addk_i32 s84, 0x7c00\n" \
M_"v[136:139], a[212:215], a[244:247], v[136:139]\n" \
B_"v26, s[72:75], s84 offen offset:1024 lds\n" \
M_"v[132:135], a[148:151], a[176:179], v[132:135]\n" \
X_"a[168:169], v34 offset:0\n" \
"v_cndmask_b32_e64 v214, v215, v216, vcc\n" \
M_"v[132:135], a[212:215], a[240:243], v[132:135]\n" \
R_"a[148:151], v28 offset:2048\n" \
"v_or_b32_e64 v215, 64, v212\n" \
M_"v[148:151], a[152:155], a[176:179], v[148:151]\n" \
R_"a[212:215], v29 offset:2048\n" \
"v_mul_u32_u24_e32 v216, 0xe39, v215\n" \
M_"v[148:151], a[216:219], a[240:243], v[148:151]\n" \
X_"a[170:171], v34 offset:1024\n" \
"v_lshrrev_b32_e64 v216, 16, v216\n" \
M_"v[152:155], a[152:155], a[180:183], v[152:155]\n" \
X_"a[232:233], v34 offset:8192\n" \
"v_mad_i32_i24 v215, v216, s6, v215\n" \
M_"v[152:155], a[216:219], a[244:247], v[152:155]\n" \
X_"a[234:235], v34 offset:9216\n" \
"v_lshlrev_b32_e64 v217, 4, v215\n" \
M_"v[156:159], a[152:155], a[184:187], v[156:159]\n" \
X_"a[172:173], v35 offset:0\n" \
"v_add_u32_e64 v220, s5, v217\n" \
M_"v[156:159], a[216:219], a[248:251], v[156:159]\n" \
X_"a[174:175], v35 offset:1024\n" \
"v_and_b32_e32 v220, 0xf0, v220\n" \
M_"v[160:163], a[152:155], a[188:191], v[160:163]\n" \
X_"a[236:237], v35 offset:8192\n" \
"v_cmp_gt_u32_e64 vcc, 16, v215\n" \
M_"v[160:163], a[216:219], a[252:255], v[160:163]\n" \
R_"a[152:155], v28 offset:4096\n" \
"v_cndmask_b32_e64 v215, v217, v220, vcc\n" \
M_"v[176:179], a[156:159], a[188:191], v[176:179]\n" \
R_"a[216:219], v29 offset:4096\n" \
"v_or_b32_e32 v217, 0x80, v212\n" \
M_"v[176:179], a[220:223], a[252:255], v[176:179]\n" \
X_"a[238:239], v35 offset:9216\n" \
"v_mul_u32_u24_e32 v220, 0xe39, v217\n" \
M_"v[172:175], a[156:159], a[184:187], v[172:175]\n" \
"v_lshrrev_b32_e64 v220, 16, v220\n" \
"v_mad_i32_i24 v217, v220, s6, v217\n" \
M_"v[172:175], a[220:223], a[248:251], v[172:175]\n" \
"v_lshlrev_b32_e32 v221, 4, v217\n" \
"v_add_u32_e32 v222, s5, v221\n" \
M_"v[168:171], a[156:159], a[180:183], v[168:171]\n" \
"v_and_b32_e32 v222, 0xf0, v222\n" \
"v_cmp_gt_u32_e64 vcc, 16, v217\n" \
M_"v[168:171], a[220:223], a[244:247], v[168:171]\n" \
"s_nop 0\n" \
"v_cndmask_b32_e32 v217, v221, v222, vcc\n" \
"v_or_b32_e32 v221, 0xc0, v212\n" \
M_"v[164:167], a[156:159], a[176:179], v[164:167]\n" \
"v_mul_u32_u24_e32 v222, 0xe39, v221\n" \
"v_lshrrev_b32_e64 v222, 16, v222\n" \
M_"v[164:167], a[220:223], a[240:243], v[164:167]\n" \
R_"a[156:159], v28 offset:6144\n" \
R_"a[220:223], v29 offset:6144\n" \
"s_waitcnt lgkmcnt(15)\n" \
M_"a[0:3], a[128:131], a[160:163], a[0:3]\n" \
"s_addk_i32 s84, 0x7c00\n" \
M_"a[0:3], a[192:195], a[224:227], a[0:3]\n" \
B_"v27, s[72:75], s84 offen offset:2048 lds\n" \
M_"a[4:7], a[128:131], a[164:167], a[4:7]\n" \
X_"a[176:177], v36 offset:0\n" \
"v_mad_i32_i24 v221, v222, s6, v221\n" \
"s_waitcnt lgkmcnt(15)\n" \
M_"a[4:7], a[192:195], a[228:231], a[4:7]\n" \
"s_addk_i32 s84, 0x7c00\n" \
"s_waitcnt lgkmcnt(11)\n" \
M_"a[8:11], a[128:131], a[168:171], a[8:11]\n" \
B_"v27, s[72:75], s84 offen offset:3072 lds\n" \
"s_waitcnt lgkmcnt(9)\n" \
M_"a[8:11], a[192:195], a[232:235], a[8:11]\n" \
X_"a[178:179], v36 offset:1024\n" \
"v_lshlrev_b32_e32 v223, 4, v221\n" \
"s_waitcnt lgkmcnt(8)\n" \
M_"a[12:15], a[128:131], a[172:175], a[12:15]\n" \
X_"a[240:241], v36 offset:8192\n" \
"v_add_u32_e32 v224, s5, v223\n" \
"s_waitcnt lgkmcnt(5)\n" \
M_"a[12:15], a[192:195], a[236:239], a[12:15]\n" \
"v_and_b32_e32 v224, 0xf0, v224\n" \
"v_cmp_gt_u32_e64 vcc, 16, v221\n" \
M_"a[28:31], a[132:135], a[172:175], a[28:31]\n" \
"s_nop 0\n" \
"v_cndmask_b32_e32 v221, v223, v224, vcc\n" \
"v_or_b32_e32 v223, 0x100, v212\n" \
M_"a[28:31], a[196:199], a[236:239], a[28:31]\n" \
X_"a[242:243], v36 offset:9216\n" \
"v_mul_u32_u24_e32 v224, 0xe39, v223\n" \
M_"a[24:27], a[132:135], a[168:171], a[24:27]\n" \
B_"v[200:203], v192, s[76:79], s34 offen offset:0\n" \
M_"a[24:27], a[196:199], a[232:235], a[24:27]\n" \
"v_lshrrev_b32_e64 v224, 16, v224\n" \
"v_mad_i32_i24 v223, v224, s6, v223\n" \
M_"a[20:23], a[132:135], a[164:167], a[20:23]\n" \
"v_lshlrev_b32_e32 v225, 4, v223\n" \
"v_add_u32_e32 v226, s5, v225\n" \
M_"a[20:23], a[196:199], a[228:231], a[20:23]\n" \
X_"a[180:181], v37 offset:0\n" \
"v_and_b32_e32 v226, 0xf0, v226\n" \
M_"a[16:19], a[132:135], a[160:163], a[16:19]\n" \
"v_cmp_gt_u32_e32 vcc, 16, v223\n" \
"s_nop 1\n" \
"v_cndmask_b32_e64 v223, v225, v226, vcc\n" \
M_"a[16:19], a[196:199], a[224:227], a[16:19]\n" \
"v_or_b32_e32 v225, 0x140, v212\n" \
"v_mul_u32_u24_e32 v226, 0xe39, v225\n" \
M_"a[32:35], a[136:139], a[160:163], a[32:35]\n" \
B_"v[204:207], v193, s[76:79], s34 offen offset:0\n" \
M_"a[32:35], a[200:203], a[224:227], a[32:35]\n" \
"s_mov_b32 m0, s32\n" \
X_"a[182:183], v37 offset:1024\n" \
M_"a[36:39], a[136:139], a[164:167], a[36:39]\n" \
"s_mov_b32 exec_lo, 0x0\n" \
"s_mov_b32 exec_hi, 0xff00\n" \
"global_load_lds_dword v[184:185], off\n" \
"s_mov_b64 exec, -1\n" \
M_"a[36:39], a[200:203], a[228:231], a[36:39]\n" \
X_"a[244:245], v37 offset:8192\n" \
"v_lshrrev_b32_e32 v226, 16, v226\n" \
M_"a[40:43], a[136:139], a[168:171], a[40:43]\n" \
"v_mad_i32_i24 v225, v226, s6, v225\n" \
"v_lshlrev_b32_e64 v227, 4, v225\n" \
M_"a[40:43], a[200:203], a[232:235], a[40:43]\n" \
"buffer_load_dword v208, v194, s[76:79], s34 offen offset:32\n" \
M_"a[44:47], a[136:139], a[172:175], a[44:47]\n" \
X_"a[246:247], v37 offset:9216\n" \
"v_add_u32_e64 v228, s5, v227\n" \
M_"a[44:47], a[200:203], a[236:239], a[44:47]\n" \
"v_and_b32_e32 v228, 0xf0, v228\n" \
"v_cmp_gt_u32_e64 vcc, 16, v225\n" \
M_"a[60:63], a[140:143], a[172:175], a[60:63]\n" \
"s_nop 0\n" \
"v_cndmask_b32_e32 v225, v227, v228, vcc\n" \
"v_or_b32_e32 v227, 0x180, v212\n" \
M_"a[60:63], a[204:207], a[236:239], a[60:63]\n" \
"v_mul_u32_u24_e32 v228, 0xe39, v227\n" \
"v_lshrrev_b32_e64 v228, 16, v228\n" \
M_"a[56:59], a[140:143], a[168:171], a[56:59]\n" \
"buffer_load_dword v209, v194, s[76:79], s34 offen offset:1184\n" \
M_"a[56:59], a[204:207], a[232:235], a[56:59]\n" \
X_"a[184:185], v38 offset:0\n" \
"v_mad_i32_i24 v227, v228, s6, v227\n" \
M_"a[52:55], a[140:143], a[164:167], a[52:55]\n" \
"v_lshlrev_b32_e32 v229, 4, v227\n" \
"v_add_u32_e32 v230, s5, v229\n" \
M_"a[52:55], a[204:207], a[228:231], a[52:55]\n" \
X_"a[186:187], v38 offset:1024\n" \
"v_and_b32_e32 v230, 0xf0, v230\n" \
M_"a[48:51], a[140:143], a[160:163], a[48:51]\n" \
"v_cmp_gt_u32_e32 vcc, 16, v227\n" \
"s_nop 1\n" \
"v_cndmask_b32_e64 v227, v229, v230, vcc\n" \
M_"a[48:51], a[204:207], a[224:227], a[48:51]\n" \
"v_or_b32_e32 v229, 0x1c0, v212\n" \
"v_mul_u32_u24_e32 v230, 0xe39, v229\n" \
M_"a[64:67], a[144:147], a[160:163], a[64:67]\n" \
"buffer_load_dword v211, v194, s[76:79], s34 offen offset:2336\n" \
M_"a[64:67], a[208:211], a[224:227], a[64:67]\n" \
X_"a[248:249], v38 offset:8192\n" \
"v_lshrrev_b32_e64 v230, 16, v230\n" \
M_"a[68:71], a[144:147], a[164:167], a[68:71]\n" \
"v_mad_i32_i24 v229, v230, s6, v229\n" \
"v_lshlrev_b32_e64 v231, 4, v229\n" \
M_"a[68:71], a[208:211], a[228:231], a[68:71]\n" \
X_"a[250:251], v38 offset:9216\n" \
"v_add_u32_e64 v232, s5, v231\n" \
M_"a[72:75], a[144:147], a[168:171], a[72:75]\n" \
"v_and_b32_e32 v232, 0xf0, v232\n" \
"v_cmp_gt_u32_e64 vcc, 16, v229\n" \
M_"a[72:75], a[208:211], a[232:235], a[72:75]\n" \
"s_mov_b32 s76, s20\n" \
"s_and_b32 s77, s21, 0xffff\n" \
M_"a[76:79], a[144:147], a[172:175], a[76:79]\n" \
"s_mov_b32 s78, 0x480\n" \
"v_cndmask_b32_e32 v229, v231, v232, vcc\n" \
M_"a[76:79], a[208:211], a[236:239], a[76:79]\n" \
"buffer_load_ushort v210, v195, s[76:79], 0 offen\n" \
M_"a[92:95], a[148:151], a[172:175], a[92:95]\n" \
X_"a[188:189], v39 offset:0\n" \
"v_or_b32_e32 v231, 0x200, v212\n" \
M_"a[92:95], a[212:215], a[236:239], a[92:95]\n" \
"v_mul_u32_u24_e32 v232, 0xe39, v231\n" \
"v_lshrrev_b32_e64 v232, 16, v232\n" \
M_"a[88:91], a[148:151], a[168:171], a[88:91]\n" \
"s_mov_b32 s76, s8\n" \
"s_and_b32 s77, s9, 0xffff\n" \
M_"a[88:91], a[212:215], a[232:235], a[88:91]\n" \
"s_mov_b32 s78, 0x240000\n" \
X_"a[190:191], v39 offset:1024\n" \
M_"a[84:87], a[148:151], a[164:167], a[84:87]\n" \
"v_mad_i32_i24 v231, v232, s6, v231\n" \
"v_lshlrev_b32_e32 v233, 4, v231\n" \
".fill (L_pb + 48864 - .) / 4, 4, 0xBF800000\n" \
"L_pp12:\n" \
"s_cbranch_execz L_pret\n" \
M_"a[84:87], a[212:215], a[228:231], a[84:87]\n" \
X_"a[252:253], v39 offset:8192\n" \
"v_add_u32_e32 v234, s5, v233\n" \
M_"a[80:83], a[148:151], a[160:163], a[80:83]\n" \
"s_movk_i32 s5, 0x480\n" \
"v_mad_u32_u24 v0, v213, s5, v214\n" \
M_"a[80:83], a[212:215], a[224:227], a[80:83]\n" \
"v_mul_u32_u24_e32 v213, 0x480, v216\n" \
"s_movk_i32 s6, 0xfc00\n" \
M_"a[96:99], a[152:155], a[160:163], a[96:99]\n" \
X_"a[254:255], v39 offset:9216\n" \
"v_add3_u32 v1, v213, v215, s6\n" \
M_"a[96:99], a[216:219], a[224:227], a[96:99]\n" \
"v_mul_u32_u24_e32 v213, 0x480, v220\n" \
"v_add3_u32 v2, v213, v217, s7\n" \
M_"a[100:103], a[152:155], a[164:167], a[100:103]\n" \
"v_mul_u32_u24_e32 v213, 0x480, v222\n" \
"v_add3_u32 v3, v213, v221, s40\n" \
M_"a[100:103], a[216:219], a[228:231], a[100:103]\n" \
"v_mul_u32_u24_e32 v213, 0x480, v226\n" \
"v_add3_u32 v5, v213, v225, s6\n" \
M_"a[104:107], a[152:155], a[168:171], a[104:107]\n" \
"v_mul_u32_u24_e32 v213, 0x480, v228\n" \
"v_add3_u32 v6, v213, v227, s7\n" \
M_"a[104:107], a[216:219], a[232:235], a[104:107]\n" \
"v_mul_u32_u24_e32 v213, 0x480, v230\n" \
"v_add3_u32 v7, v213, v229, s40\n" \
M_"a[108:111], a[152:155], a[172:175], a[108:111]\n" \
"s_lshl_b32 s7, s3, 3\n" \
"s_lshl_b32 s40, s3, 1\n" \
M_"a[108:111], a[216:219], a[236:239], a[108:111]\n" \
"s_and_b32 s7, s7, 16\n" \
"s_and_b32 s40, s40, 2\n" \
"s_waitcnt lgkmcnt(15)\n" \
M_"a[124:127], a[156:159], a[172:175], a[124:127]\n" \
"s_and_b32 s6, s2, 7\n" \
"v_bfe_u32 v213, v20, 7, 1\n" \
M_"a[124:127], a[220:223], a[236:239], a[124:127]\n" \
"s_or_b32 s7, s40, s7\n" \
"v_and_b32_e32 v234, 0xf0, v234\n" \
M_"a[120:123], a[156:159], a[168:171], a[120:123]\n" \
"v_cmp_gt_u32_e32 vcc, 16, v231\n" \
"v_or3_b32 v213, s7, v213, v219\n" \
M_"a[120:123], a[220:223], a[232:235], a[120:123]\n" \
"v_and_b32_e32 v214, 32, v20\n" \
"s_lshl_b32 s6, s6, 5\n" \
M_"a[116:119], a[156:159], a[164:167], a[116:119]\n" \
"v_cndmask_b32_e32 v231, v233, v234, vcc\n" \
"v_mul_u32_u24_e32 v213, 18, v213\n" \
M_"a[116:119], a[220:223], a[228:231], a[116:119]\n" \
"v_mov_b32_e32 v215, 0x100\n" \
"v_mov_b32_e64 v216, s6\n" \
M_"a[112:115], a[156:159], a[160:163], a[112:115]\n" \
"v_cmp_eq_u32_e64 vcc, 0, v214\n" \
"v_and_or_b32 v213, v218, 1, v213\n" \
"s_waitcnt vmcnt(7) lgkmcnt(0)\n" \
M_"a[112:115], a[220:223], a[224:227], a[112:115]\n" \
"s_barrier\n" \
M_"v[52:55], a[128:131], a[176:179], v[52:55]\n" \
"s_add_u32 m0, s30, 0x0\n" \
"s_movk_i32 s84, 0x200\n" \
M_"v[52:55], a[192:195], a[240:243], v[52:55]\n" \
B_"v22, s[64:67], s84 offen offset:0 lds\n" \
M_"v[56:59], a[128:131], a[180:183], v[56:59]\n" \
X_"a[160:161], v32 offset:32768\n" \
"v_lshlrev_b32_e64 v212, 3, v212\n" \
M_"v[56:59], a[192:195], a[244:247], v[56:59]\n" \
"s_movk_i32 s84, 0x1e00\n" \
M_"v[60:63], a[128:131], a[184:187], v[60:63]\n" \
B_"v23, s[64:67], s84 offen offset:1024 lds\n" \
M_"v[60:63], a[192:195], a[248:251], v[60:63]\n" \
X_"a[162:163], v32 offset:33792\n" \
"v_cndmask_b32_e32 v214, v215, v216, vcc\n" \
M_"v[64:67], a[128:131], a[188:191], v[64:67]\n" \
X_"a[224:225], v32 offset:40960\n" \
"v_mad_u32_u24 v213, v213, s5, v214\n" \
M_"v[64:67], a[192:195], a[252:255], v[64:67]\n" \
R_"a[128:131], v28 offset:49152\n" \
"v_and_or_b32 v213, v20, 16, v213\n" \
M_"v[80:83], a[132:135], a[188:191], v[80:83]\n" \
R_"a[192:195], v29 offset:49152\n" \
"v_add_u32_e32 v9, 0x4800, v213\n" \
M_"v[80:83], a[196:199], a[252:255], v[80:83]\n" \
"s_movk_i32 s84, 0x3a00\n" \
M_"v[76:79], a[132:135], a[184:187], v[76:79]\n" \
B_"v22, s[64:67], s84 offen offset:2048 lds\n" \
M_"v[76:79], a[196:199], a[248:251], v[76:79]\n" \
"s_movk_i32 s84, 0x5600\n" \
M_"v[72:75], a[132:135], a[180:183], v[72:75]\n" \
B_"v23, s[64:67], s84 offen offset:3072 lds\n" \
M_"v[72:75], a[196:199], a[244:247], v[72:75]\n" \
"s_add_u32 m0, s31, 0x0\n" \
"s_mov_b32 s84, 0x200000\n" \
M_"v[68:71], a[132:135], a[176:179], v[68:71]\n" \
B_"v26, s[68:71], s84 offen offset:0 lds\n" \
M_"v[68:71], a[196:199], a[240:243], v[68:71]\n" \
R_"a[132:135], v28 offset:51200\n" \
"v_mul_u32_u24_e32 v213, 0x120, v218\n" \
M_"v[84:87], a[136:139], a[176:179], v[84:87]\n" \
R_"a[196:199], v29 offset:51200\n" \
"v_and_or_b32 v10, v212, 24, v213\n" \
M_"v[84:87], a[200:203], a[240:243], v[84:87]\n" \
"s_addk_i32 s84, 0x7c00\n" \
M_"v[88:91], a[136:139], a[180:183], v[88:91]\n" \
B_"v26, s[68:71], s84 offen offset:1024 lds\n" \
M_"v[88:91], a[200:203], a[244:247], v[88:91]\n" \
X_"a[226:227], v32 offset:41984\n" \
"v_lshrrev_b32_e64 v213, 2, v20\n" \
M_"v[92:95], a[136:139], a[184:187], v[92:95]\n" \
"s_addk_i32 s84, 0x7c00\n" \
"s_nop 0\n" \
M_"v[92:95], a[200:203], a[248:251], v[92:95]\n" \
B_"v27, s[68:71], s84 offen offset:2048 lds\n" \
M_"v[96:99], a[136:139], a[188:191], v[96:99]\n" \
X_"a[164:165], v33 offset:32768\n" \
"v_and_or_b32 v213, v213, 60, s4\n" \
M_"v[96:99], a[200:203], a[252:255], v[96:99]\n" \
R_"a[136:139], v28 offset:53248\n" \
"v_lshl_or_b32 v212, s3, 9, v212\n" \
M_"v[112:115], a[140:143], a[188:191], v[112:115]\n" \
R_"a[200:203], v29 offset:53248\n" \
"v_mad_u32_u24 v4, v224, s5, v223\n" \
M_"v[112:115], a[204:207], a[252:255], v[112:115]\n" \
"s_addk_i32 s84, 0x7c00\n" \
"s_nop 0\n" \
M_"v[108:111], a[140:143], a[184:187], v[108:111]\n" \
B_"v27, s[68:71], s84 offen offset:3072 lds\n" \
M_"v[108:111], a[204:207], a[248:251], v[108:111]\n" \
"s_add_u32 m0, s30, 0x1000\n" \
"s_mov_b32 s84, 0x8200\n" \
M_"v[104:107], a[140:143], a[180:183], v[104:107]\n" \
B_"v22, s[64:67], s84 offen offset:0 lds\n" \
M_"v[104:107], a[204:207], a[244:247], v[104:107]\n" \
X_"a[166:167], v33 offset:33792\n" \
"v_mad_u32_u24 v8, v232, s5, v231\n" \
M_"v[100:103], a[140:143], a[176:179], v[100:103]\n" \
X_"a[228:229], v33 offset:40960\n" \
"v_or_b32_e32 v18, 0x27000, v213\n" \
M_"v[100:103], a[204:207], a[240:243], v[100:103]\n" \
R_"a[140:143], v28 offset:55296\n" \
"v_or_b32_e32 v19, 0x27800, v212\n" \
M_"v[116:119], a[144:147], a[176:179], v[116:119]\n" \
R_"a[204:207], v29 offset:55296\n" \
"v_mov_b32_e32 v215, 0x1000c0c\n" \
M_"v[116:119], a[208:211], a[240:243], v[116:119]\n" \
"s_addk_i32 s84, 0x1c00\n" \
M_"v[120:123], a[144:147], a[180:183], v[120:123]\n" \
B_"v23, s[64:67], s84 offen offset:1024 lds\n" \
M_"v[120:123], a[208:211], a[244:247], v[120:123]\n" \
"s_addk_i32 s84, 0x1c00\n" \
M_"v[124:127], a[144:147], a[184:187], v[124:127]\n" \
B_"v22, s[64:67], s84 offen offset:2048 lds\n" \
M_"v[124:127], a[208:211], a[248:251], v[124:127]\n" \
"s_addk_i32 s84, 0x1c00\n" \
"s_nop 0\n" \
M_"v[128:131], a[144:147], a[188:191], v[128:131]\n" \
B_"v23, s[64:67], s84 offen offset:3072 lds\n" \
M_"v[128:131], a[208:211], a[252:255], v[128:131]\n" \
R_"a[144:147], v28 offset:32768\n" \
"v_mov_b32_e32 v216, 0xc0c0100\n" \
M_"v[144:147], a[148:151], a[188:191], v[144:147]\n" \
R_"a[208:211], v29 offset:32768\n" \
"v_bfe_u32 v214, v20, 4, 1\n" \
M_"v[144:147], a[212:215], a[252:255], v[144:147]\n" \
"s_add_u32 m0, s31, 0x4000\n" \
"s_mov_b32 s84, 0x200000\n" \
M_"v[140:143], a[148:151], a[184:187], v[140:143]\n" \
B_"v26, s[72:75], s84 offen offset:0 lds\n" \
M_"v[140:143], a[212:215], a[248:251], v[140:143]\n" \
X_"a[230:231], v33 offset:41984\n" \
"v_lshrrev_b32_e64 v212, 8, v20\n" \
M_"v[136:139], a[148:151], a[180:183], v[136:139]\n" \
"s_addk_i32 s84, 0x7c00\n" \
M_"v[136:139], a[212:215], a[244:247], v[136:139]\n" \
B_"v26, s[72:75], s84 offen offset:1024 lds\n" \
M_"v[132:135], a[148:151], a[176:179], v[132:135]\n" \
X_"a[168:169], v34 offset:32768\n" \
"v_cmp_eq_u32_e32 vcc, 0, v214\n" \
M_"v[132:135], a[212:215], a[240:243], v[132:135]\n" \
R_"a[148:151], v28 offset:34816\n" \
"v_bfe_u32 v213, v20, 4, 4\n" \
M_"v[148:151], a[152:155], a[176:179], v[148:151]\n" \
R_"a[212:215], v29 offset:34816\n" \
"v_mov_b32_e32 v217, 0xc0c0302\n" \
M_"v[148:151], a[216:219], a[240:243], v[148:151]\n" \
X_"a[170:171], v34 offset:33792\n" \
"v_cndmask_b32_e64 v215, v215, v216, vcc\n" \
M_"v[152:155], a[152:155], a[180:183], v[152:155]\n" \
X_"a[232:233], v34 offset:40960\n" \
"v_mov_b32_e32 v216, 0x3020c0c\n" \
M_"v[152:155], a[216:219], a[244:247], v[152:155]\n" \
X_"a[234:235], v34 offset:41984\n" \
"v_lshl_or_b32 v214, v212, 2, v214\n" \
M_"v[156:159], a[152:155], a[184:187], v[156:159]\n" \
X_"a[172:173], v35 offset:32768\n" \
"v_cndmask_b32_e64 v216, v216, v217, vcc\n" \
M_"v[156:159], a[216:219], a[248:251], v[156:159]\n" \
X_"a[174:175], v35 offset:33792\n" \
"v_mov_b32_e32 v217, 0xc0c0c0c\n" \
M_"v[160:163], a[152:155], a[188:191], v[160:163]\n" \
X_"a[236:237], v35 offset:40960\n" \
"v_cmp_gt_u32_e64 vcc, v214, v213\n" \
M_"v[160:163], a[216:219], a[252:255], v[160:163]\n" \
R_"a[152:155], v28 offset:36864\n" \
"v_or_b32_e64 v214, 2, v214\n" \
M_"v[176:179], a[156:159], a[188:191], v[176:179]\n" \
R_"a[216:219], v29 offset:36864\n" \
"s_lshr_b32 s4, s2, 5\n" \
M_"v[176:179], a[220:223], a[252:255], v[176:179]\n" \
X_"a[238:239], v35 offset:41984\n" \
"v_cndmask_b32_e32 v40, v217, v215, vcc\n" \
M_"v[172:175], a[156:159], a[184:187], v[172:175]\n" \
"v_cndmask_b32_e32 v42, v215, v217, vcc\n" \
"v_cmp_gt_u32_e32 vcc, v214, v213\n" \
M_"v[172:175], a[220:223], a[248:251], v[172:175]\n" \
"v_and_b32_e32 v214, 16, v20\n" \
"s_movk_i32 s42, 0x3f80\n" \
M_"v[168:171], a[156:159], a[180:183], v[168:171]\n" \
"s_xor_b32 s5, s4, 15\n" \
M_"v[168:171], a[220:223], a[244:247], v[168:171]\n" \
"s_cmpk_lt_u32 s3, 2\n" \
"s_cselect_b32 s6, s5, s4\n" \
M_"v[164:167], a[156:159], a[176:179], v[164:167]\n" \
"s_lshl_b32 s7, s6, 7\n" \
"v_and_b32_e32 v213, 0x320, v20\n" \
M_"v[164:167], a[220:223], a[240:243], v[164:167]\n" \
R_"a[156:159], v28 offset:38912\n" \
R_"a[220:223], v29 offset:38912\n" \
"s_waitcnt lgkmcnt(15)\n" \
M_"a[0:3], a[128:131], a[160:163], a[0:3]\n" \
"s_addk_i32 s84, 0x7c00\n" \
M_"a[0:3], a[192:195], a[224:227], a[0:3]\n" \
B_"v27, s[72:75], s84 offen offset:2048 lds\n" \
M_"a[4:7], a[128:131], a[164:167], a[4:7]\n" \
X_"a[176:177], v36 offset:32768\n" \
"s_movk_i32 s41, 0x100\n" \
"s_waitcnt lgkmcnt(15)\n" \
M_"a[4:7], a[192:195], a[228:231], a[4:7]\n" \
"s_addk_i32 s84, 0x7c00\n" \
"s_waitcnt lgkmcnt(11)\n" \
M_"a[8:11], a[128:131], a[168:171], a[8:11]\n" \
B_"v27, s[72:75], s84 offen offset:3072 lds\n" \
"s_waitcnt lgkmcnt(9)\n" \
M_"a[8:11], a[192:195], a[232:235], a[8:11]\n" \
X_"a[178:179], v36 offset:33792\n" \
"v_lshlrev_b32_e64 v214, v214, s42\n" \
"s_waitcnt lgkmcnt(8)\n" \
M_"a[12:15], a[128:131], a[172:175], a[12:15]\n" \
X_"a[240:241], v36 offset:40960\n" \
"v_mov_b32_e32 v215, 0\n" \
"s_waitcnt lgkmcnt(5)\n" \
M_"a[12:15], a[192:195], a[236:239], a[12:15]\n" \
"s_add_i32 s40, s7, -16\n" \
"v_cndmask_b32_e32 v41, v217, v216, vcc\n" \
M_"a[28:31], a[132:135], a[172:175], a[28:31]\n" \
"v_cndmask_b32_e32 v43, v216, v217, vcc\n" \
"v_cmp_eq_u32_e32 vcc, s41, v213\n" \
M_"a[28:31], a[196:199], a[236:239], a[28:31]\n" \
X_"a[242:243], v36 offset:41984\n" \
"s_mov_b32 s41, 0\n" \
M_"a[24:27], a[132:135], a[168:171], a[24:27]\n" \
"s_and_b32 s43, s3, 1\n" \
"v_cndmask_b32_e64 v46, v215, v214, vcc\n" \
M_"a[24:27], a[196:199], a[232:235], a[24:27]\n" \
"v_and_b32_e32 v213, 0x330, v20\n" \
M_"a[20:23], a[132:135], a[164:167], a[20:23]\n" \
"s_cmp_lg_u32 s7, 0\n" \
"s_cselect_b32 s40, s40, s41\n" \
M_"a[20:23], a[196:199], a[228:231], a[20:23]\n" \
X_"a[180:181], v37 offset:32768\n" \
"s_mov_b32 s42, 0xff800000\n" \
M_"a[16:19], a[132:135], a[160:163], a[16:19]\n" \
"s_cmp_lg_u32 s7, 0\n" \
"s_cselect_b32 s47, s41, s42\n" \
M_"a[16:19], a[196:199], a[224:227], a[16:19]\n" \
"s_lshl_b32 s41, s43, 6\n" \
"v_lshrrev_b32_e32 v215, 6, v20\n" \
M_"a[32:35], a[136:139], a[160:163], a[32:35]\n" \
X_"a[182:183], v37 offset:33792\n" \
"v_mov_b32_e32 v214, 0x3f80\n" \
M_"a[32:35], a[200:203], a[224:227], a[32:35]\n" \
"v_cmp_eq_u32_e32 vcc, 32, v213\n" \
"s_or_b32 s7, s41, s7\n" \
M_"a[36:39], a[136:139], a[164:167], a[36:39]\n" \
"v_lshrrev_b32_e64 v213, 7, v20\n" \
"v_bfe_u32 v216, v20, 6, 2\n" \
M_"a[36:39], a[200:203], a[228:231], a[36:39]\n" \
X_"a[244:245], v37 offset:40960\n" \
"v_lshrrev_b32_e64 v219, 1, v20\n" \
M_"a[40:43], a[136:139], a[168:171], a[40:43]\n" \
"s_mov_b32 m0, s32\n" \
"v_bfe_u32 v215, v215, 1, 1\n" \
M_"a[40:43], a[200:203], a[232:235], a[40:43]\n" \
"s_mov_b32 exec_lo, 0xffffffff\n" \
"s_mov_b32 exec_hi, 0x0\n" \
"global_load_lds_dword v[188:189], off\n" \
"s_mov_b64 exec, -1\n" \
M_"a[44:47], a[136:139], a[172:175], a[44:47]\n" \
X_"a[246:247], v37 offset:41984\n" \
"v_cndmask_b32_e64 v47, 0, v214, vcc\n" \
M_"a[44:47], a[200:203], a[236:239], a[44:47]\n" \
"v_or_b32_e64 v214, s7, v213\n" \
"v_lshl_or_b32 v217, s43, 3, v213\n" \
M_"a[60:63], a[140:143], a[172:175], a[60:63]\n" \
"v_lshlrev_b32_e32 v218, 10, v212\n" \
"v_lshlrev_b32_e32 v216, 7, v216\n" \
M_"a[60:63], a[204:207], a[236:239], a[60:63]\n" \
"v_and_b32_e64 v219, 24, v219\n" \
"v_and_or_b32 v213, v213, 2, v215\n" \
M_"a[56:59], a[140:143], a[168:171], a[56:59]\n" \
X_"a[184:185], v38 offset:32768\n" \
"v_add_u32_e64 v217, s40, v217\n" \
M_"a[56:59], a[204:207], a[232:235], a[56:59]\n" \
"v_or3_b32 v216, v216, v218, v219\n" \
"v_lshlrev_b32_e64 v213, 5, v213\n" \
M_"a[52:55], a[140:143], a[164:167], a[52:55]\n" \
"s_mov_b32 s40, 0x14000\n" \
"s_lshl_b32 s7, s2, 2\n" \
M_"a[52:55], a[204:207], a[228:231], a[52:55]\n" \
X_"a[186:187], v38 offset:33792\n" \
"v_or3_b32 v48, v216, v213, s40\n" \
M_"a[48:51], a[140:143], a[160:163], a[48:51]\n" \
"s_lshl_b32 s40, s43, 2\n" \
"s_lshl_b32 s6, s6, 3\n" \
M_"a[48:51], a[204:207], a[224:227], a[48:51]\n" \
"v_or_b32_e32 v213, s40, v212\n" \
"s_or_b32 s6, s6, s40\n" \
M_"a[64:67], a[144:147], a[160:163], a[64:67]\n" \
X_"a[248:249], v38 offset:40960\n" \
"s_and_b32 s40, s7, 0x7c\n" \
M_"a[64:67], a[208:211], a[224:227], a[64:67]\n" \
"s_or_b32 s40, s40, s3\n" \
M_"a[68:71], a[144:147], a[164:167], a[68:71]\n" \
"s_cmpk_lt_u32 s40, 64\n" \
"s_cselect_b32 s41, s4, s5\n" \
M_"a[68:71], a[208:211], a[228:231], a[68:71]\n" \
X_"a[250:251], v38 offset:41984\n" \
"s_lshl_b32 s40, s40, 1\n" \
M_"a[72:75], a[144:147], a[168:171], a[72:75]\n" \
"s_lshl_b32 s41, s41, 7\n" \
"s_and_b32 s40, s40, 0x7e\n" \
M_"a[72:75], a[208:211], a[232:235], a[72:75]\n" \
"s_or_b32 s40, s41, s40\n" \
"s_mulk_i32 s40, 0x480\n" \
M_"a[76:79], a[144:147], a[172:175], a[76:79]\n" \
"v_add_u32_e64 v49, s40, v20\n" \
"s_mov_b32 s40, 0xc000\n" \
M_"a[76:79], a[208:211], a[236:239], a[76:79]\n" \
X_"a[188:189], v39 offset:32768\n" \
"s_and_b32 s7, s7, 28\n" \
M_"a[92:95], a[148:151], a[172:175], a[92:95]\n" \
"s_bfe_u32 s45, s2, 0x20003\n" \
"s_mov_b32 s41, 0x8000\n" \
M_"a[92:95], a[212:215], a[236:239], a[92:95]\n" \
"s_cmp_lg_u32 s4, 0\n" \
"s_cselect_b32 s51, s40, s41\n" \
M_"a[88:91], a[148:151], a[168:171], a[88:91]\n" \
X_"a[190:191], v39 offset:33792\n" \
"s_movk_i32 s40, 0x4000\n" \
M_"a[88:91], a[212:215], a[232:235], a[88:91]\n" \
"s_lshr_b32 s44, s3, 1\n" \
"s_or_b32 s46, s7, s45\n" \
M_"a[84:87], a[148:151], a[164:167], a[84:87]\n" \
"s_waitcnt vmcnt(17)\n" \
"v_or_b32_e32 v204, v210, v204\n" \
"v_or_b32_e64 v206, v210, v206\n" \
M_"a[84:87], a[212:215], a[228:231], a[84:87]\n" \
"v_mov_b32_e64 v210, 0\n" \
"s_add_u32 s88, s35, 0x1000\n" \
M_"a[80:83], a[148:151], a[160:163], a[80:83]\n" \
X_"a[252:253], v39 offset:40960\n" \
M_"a[80:83], a[212:215], a[224:227], a[80:83]\n" \
"ds_write_b128 v196, v[200:203]\n" \
M_"a[96:99], a[152:155], a[160:163], a[96:99]\n" \
X_"a[254:255], v39 offset:41984\n" \
M_"a[96:99], a[216:219], a[224:227], a[96:99]\n" \
"s_cmp_lg_u32 s4, 0\n" \
"s_cselect_b32 s53, s41, s40\n" \
M_"a[100:103], a[152:155], a[164:167], a[100:103]\n" \
"ds_write_b128 v197, v[204:207]\n" \
M_"a[100:103], a[216:219], a[228:231], a[100:103]\n" \
"s_lshl_b32 s40, s44, 15\n" \
"s_lshl_b32 s41, s43, 14\n" \
M_"a[104:107], a[152:155], a[168:171], a[104:107]\n" \
"s_lshl_b32 s48, s46, 20\n" \
"s_or_b32 s41, s40, s41\n" \
M_"a[104:107], a[216:219], a[232:235], a[104:107]\n" \
"ds_write_b128 v198, v[208:211]\n" \
M_"a[108:111], a[152:155], a[172:175], a[108:111]\n" \
"s_add_i32 s49, s48, 0x1000000\n" \
"s_or_b32 s42, s49, s41\n" \
M_"a[108:111], a[216:219], a[236:239], a[108:111]\n" \
"s_lshl_b32 s4, s4, 16\n" \
"s_or_b32 s41, s42, s4\n" \
"s_waitcnt lgkmcnt(15)\n" \
M_"a[124:127], a[156:159], a[172:175], a[124:127]\n" \
"s_lshl_b32 s4, s5, 16\n" \
"s_mul_i32 s5, s6, 0x24800\n" \
M_"a[124:127], a[220:223], a[236:239], a[124:127]\n" \
"s_lshl_b32 s6, s46, 9\n" \
"s_lshl_b32 s7, s7, 6\n" \
M_"a[120:123], a[156:159], a[168:171], a[120:123]\n" \
"s_add_i32 s6, s5, s6\n" \
"s_or_b32 s5, s5, s7\n" \
M_"a[120:123], a[220:223], a[232:235], a[120:123]\n" \
"s_lshl_b32 s7, s45, 2\n" \
"s_or_b32 s5, s5, s7\n" \
M_"a[116:119], a[156:159], a[164:167], a[116:119]\n" \
"s_lshl_b32 s7, s3, 12\n" \
"s_or_b32 s50, s49, s7\n" \
M_"a[116:119], a[220:223], a[228:231], a[116:119]\n" \
"s_lshl_b32 s7, s3, 11\n" \
"v_lshrrev_b32_e64 v215, 4, v20\n" \
M_"a[112:115], a[156:159], a[160:163], a[112:115]\n" \
"v_bfe_u32 v216, v20, 4, 3\n" \
"s_or_b32 s42, s42, s4\n" \
"s_waitcnt vmcnt(1) lgkmcnt(0)\n" \
M_"a[112:115], a[220:223], a[224:227], a[112:115]\n" \
"s_barrier\n" \
M_"v[52:55], a[128:131], a[176:179], v[52:55]\n" \
"s_add_u32 m0, s30, 0x8000\n" \
"s_movk_i32 s84, 0x280\n" \
M_"v[52:55], a[192:195], a[240:243], v[52:55]\n" \
B_"v22, s[64:67], s84 offen offset:0 lds\n" \
M_"v[56:59], a[128:131], a[180:183], v[56:59]\n" \
"s_movk_i32 s84, 0x1e80\n" \
M_"v[56:59], a[192:195], a[244:247], v[56:59]\n" \
B_"v23, s[64:67], s84 offen offset:1024 lds\n" \
M_"v[60:63], a[128:131], a[184:187], v[60:63]\n" \
X_"a[160:161], v32 offset:0\n" \
"s_lshl_b32 s4, s43, 11\n" \
M_"v[60:63], a[192:195], a[248:251], v[60:63]\n" \
"s_movk_i32 s84, 0x3a80\n" \
M_"v[64:67], a[128:131], a[188:191], v[64:67]\n" \
B_"v22, s[64:67], s84 offen offset:2048 lds\n" \
M_"v[64:67], a[192:195], a[252:255], v[64:67]\n" \
R_"a[128:131], v28 offset:16384\n" \
"s_add_i32 s52, s48, s7\n" \
M_"v[80:83], a[132:135], a[188:191], v[80:83]\n" \
R_"a[192:195], v29 offset:16384\n" \
"s_add_i32 s53, s53, s7\n" \
M_"v[80:83], a[196:199], a[252:255], v[80:83]\n" \
"s_movk_i32 s84, 0x5680\n" \
M_"v[76:79], a[132:135], a[184:187], v[76:79]\n" \
B_"v23, s[64:67], s84 offen offset:3072 lds\n" \
M_"v[76:79], a[196:199], a[248:251], v[76:79]\n" \
"s_add_u32 m0, s31, 0x8000\n" \
"s_mov_b32 s84, 0x280000\n" \
M_"v[72:75], a[132:135], a[180:183], v[72:75]\n" \
B_"v26, s[68:71], s84 offen offset:0 lds\n" \
M_"v[72:75], a[196:199], a[244:247], v[72:75]\n" \
X_"a[162:163], v32 offset:1024\n" \
"v_mul_u32_u24_e32 v214, 0x480, v214\n" \
M_"v[68:71], a[132:135], a[176:179], v[68:71]\n" \
X_"a[224:225], v32 offset:8192\n" \
"v_bitop3_b32 v218, v212, v215, 7 bitop3:0x78\n" \
M_"v[68:71], a[196:199], a[240:243], v[68:71]\n" \
R_"a[132:135], v28 offset:18432\n" \
"v_bitop3_b32 v212, v212, v216, 4 bitop3:0x36\n" \
M_"v[84:87], a[136:139], a[176:179], v[84:87]\n" \
R_"a[196:199], v29 offset:18432\n" \
"s_lshl_b32 s7, s3, 10\n" \
M_"v[84:87], a[200:203], a[240:243], v[84:87]\n" \
"s_addk_i32 s84, 0x7c00\n" \
M_"v[88:91], a[136:139], a[180:183], v[88:91]\n" \
B_"v26, s[68:71], s84 offen offset:1024 lds\n" \
M_"v[88:91], a[200:203], a[244:247], v[88:91]\n" \
"s_addk_i32 s84, 0x7c00\n" \
M_"v[92:95], a[136:139], a[184:187], v[92:95]\n" \
B_"v27, s[68:71], s84 offen offset:2048 lds\n" \
M_"v[92:95], a[200:203], a[248:251], v[92:95]\n" \
"s_addk_i32 s84, 0x7c00\n" \
M_"v[96:99], a[136:139], a[188:191], v[96:99]\n" \
B_"v27, s[68:71], s84 offen offset:3072 lds\n" \
M_"v[96:99], a[200:203], a[252:255], v[96:99]\n" \
R_"a[136:139], v28 offset:20480\n" \
"s_or_b32 s4, s40, s4\n" \
M_"v[112:115], a[140:143], a[188:191], v[112:115]\n" \
R_"a[200:203], v29 offset:20480\n" \
"v_lshl_or_b32 v25, v212, 4, v214\n" \
M_"v[112:115], a[204:207], a[252:255], v[112:115]\n" \
"s_add_u32 m0, s30, 0x9000\n" \
"s_mov_b32 s84, 0x8280\n" \
M_"v[108:111], a[140:143], a[184:187], v[108:111]\n" \
B_"v22, s[64:67], s84 offen offset:0 lds\n" \
M_"v[108:111], a[204:207], a[248:251], v[108:111]\n" \
"s_addk_i32 s84, 0x1c00\n" \
M_"v[104:107], a[140:143], a[180:183], v[104:107]\n" \
B_"v23, s[64:67], s84 offen offset:1024 lds\n" \
M_"v[104:107], a[204:207], a[244:247], v[104:107]\n" \
"s_addk_i32 s84, 0x1c00\n" \
M_"v[100:103], a[140:143], a[176:179], v[100:103]\n" \
B_"v22, s[64:67], s84 offen offset:2048 lds\n" \
M_"v[100:103], a[204:207], a[240:243], v[100:103]\n" \
R_"a[140:143], v28 offset:22528\n" \
"v_mul_u32_u24_e32 v212, 0x480, v217\n" \
M_"v[116:119], a[144:147], a[176:179], v[116:119]\n" \
R_"a[204:207], v29 offset:22528\n" \
"v_bitop3_b32 v213, v215, v213, 7 bitop3:0x6c\n" \
M_"v[116:119], a[208:211], a[240:243], v[116:119]\n" \
"s_addk_i32 s84, 0x1c00\n" \
M_"v[120:123], a[144:147], a[180:183], v[120:123]\n" \
B_"v23, s[64:67], s84 offen offset:3072 lds\n" \
M_"v[120:123], a[208:211], a[244:247], v[120:123]\n" \
"s_add_u32 m0, s31, 0xc000\n" \
"s_mov_b32 s84, 0x280000\n" \
M_"v[124:127], a[144:147], a[184:187], v[124:127]\n" \
B_"v26, s[72:75], s84 offen offset:0 lds\n" \
M_"v[124:127], a[208:211], a[248:251], v[124:127]\n" \
"s_addk_i32 s84, 0x7c00\n" \
M_"v[128:131], a[144:147], a[188:191], v[128:131]\n" \
B_"v26, s[72:75], s84 offen offset:1024 lds\n" \
M_"v[128:131], a[208:211], a[252:255], v[128:131]\n" \
R_"a[144:147], v28 offset:0\n" \
"s_or_b32 s40, s7, 0x14000\n" \
M_"v[144:147], a[148:151], a[188:191], v[144:147]\n" \
R_"a[208:211], v29 offset:0\n" \
"s_lshl_b32 s43, s44, 13\n" \
M_"v[144:147], a[212:215], a[252:255], v[144:147]\n" \
R_"v[200:203], v199 offset:0\n" \
"s_mulk_i32 s44, 0xe800\n" \
M_"v[140:143], a[148:151], a[184:187], v[140:143]\n" \
R_"v[204:207], v199 offset:4096\n" \
"s_lshl_b32 s7, s3, 8\n" \
M_"v[140:143], a[212:215], a[248:251], v[140:143]\n" \
X_"a[226:227], v32 offset:9216\n" \
"v_lshl_or_b32 v24, v218, 4, v214\n" \
M_"v[136:139], a[148:151], a[180:183], v[136:139]\n" \
X_"a[164:165], v33 offset:0\n" \
"v_lshl_or_b32 v21, v213, 4, v212\n" \
M_"v[136:139], a[212:215], a[244:247], v[136:139]\n" \
R_"v[208:211], v199 offset:8192\n" \
"s_add_i32 s44, s44, 0x17000\n" \
M_"v[132:135], a[148:151], a[176:179], v[132:135]\n" \
X_"a[166:167], v33 offset:1024\n" \
"s_or_b32 s45, s7, 0x27000\n" \
M_"v[132:135], a[212:215], a[240:243], v[132:135]\n" \
R_"a[148:151], v28 offset:2048\n" \
"s_or_b32 s46, s4, 0x10000\n" \
M_"v[148:151], a[152:155], a[176:179], v[148:151]\n" \
R_"a[212:215], v29 offset:2048\n" \
"s_add_i32 s48, s6, 0x3000000\n" \
M_"v[148:151], a[216:219], a[240:243], v[148:151]\n" \
X_"a[228:229], v33 offset:8192\n" \
"s_add_i32 s49, s5, 0x3004000\n" \
M_"v[152:155], a[152:155], a[180:183], v[152:155]\n" \
X_"a[230:231], v33 offset:9216\n" \
"s_add_i32 s52, s52, 0x1010000\n" \
M_"v[152:155], a[216:219], a[244:247], v[152:155]\n" \
X_"a[168:169], v34 offset:0\n" \
M_"v[156:159], a[152:155], a[184:187], v[156:159]\n" \
X_"a[170:171], v34 offset:1024\n" \
M_"v[156:159], a[216:219], a[248:251], v[156:159]\n" \
X_"a[232:233], v34 offset:8192\n" \
M_"v[160:163], a[152:155], a[188:191], v[160:163]\n" \
X_"a[234:235], v34 offset:9216\n" \
M_"v[160:163], a[216:219], a[252:255], v[160:163]\n" \
R_"a[152:155], v28 offset:4096\n" \
M_"v[176:179], a[156:159], a[188:191], v[176:179]\n" \
R_"a[216:219], v29 offset:4096\n" \
M_"v[176:179], a[220:223], a[252:255], v[176:179]\n" \
X_"a[172:173], v35 offset:0\n" \
M_"v[172:175], a[156:159], a[184:187], v[172:175]\n" \
X_"a[174:175], v35 offset:1024\n" \
M_"v[172:175], a[220:223], a[248:251], v[172:175]\n" \
X_"a[236:237], v35 offset:8192\n" \
M_"v[168:171], a[156:159], a[180:183], v[168:171]\n" \
X_"a[238:239], v35 offset:9216\n" \
M_"v[168:171], a[220:223], a[244:247], v[168:171]\n" \
M_"v[164:167], a[156:159], a[176:179], v[164:167]\n" \
M_"v[164:167], a[220:223], a[240:243], v[164:167]\n" \
R_"a[156:159], v28 offset:6144\n" \
R_"a[220:223], v29 offset:6144\n" \
"s_waitcnt lgkmcnt(15)\n" \
M_"a[0:3], a[128:131], a[160:163], a[0:3]\n" \
"s_addk_i32 s84, 0x7c00\n" \
M_"a[0:3], a[192:195], a[224:227], a[0:3]\n" \
B_"v27, s[72:75], s84 offen offset:2048 lds\n" \
M_"a[4:7], a[128:131], a[164:167], a[4:7]\n" \
X_"a[176:177], v36 offset:0\n" \
"s_waitcnt lgkmcnt(13)\n" \
".fill (L_pb + 52960 - .) / 4, 4, 0xBF800000\n" \
"L_pp13:\n" \
"s_cbranch_execz L_pret\n" \
M_"a[4:7], a[192:195], a[228:231], a[4:7]\n" \
"s_addk_i32 s84, 0x7c00\n" \
"s_waitcnt lgkmcnt(11)\n" \
M_"a[8:11], a[128:131], a[168:171], a[8:11]\n" \
B_"v27, s[72:75], s84 offen offset:3072 lds\n" \
"s_waitcnt lgkmcnt(9)\n" \
M_"a[8:11], a[192:195], a[232:235], a[8:11]\n" \
X_"a[178:179], v36 offset:1024\n" \
"s_waitcnt lgkmcnt(6)\n" \
M_"a[12:15], a[128:131], a[172:175], a[12:15]\n" \
X_"a[240:241], v36 offset:8192\n" \
"s_waitcnt lgkmcnt(5)\n" \
M_"a[12:15], a[192:195], a[236:239], a[12:15]\n" \
M_"a[28:31], a[132:135], a[172:175], a[28:31]\n" \
M_"a[28:31], a[196:199], a[236:239], a[28:31]\n" \
X_"a[242:243], v36 offset:9216\n" \
M_"a[24:27], a[132:135], a[168:171], a[24:27]\n" \
M_"a[24:27], a[196:199], a[232:235], a[24:27]\n" \
M_"a[20:23], a[132:135], a[164:167], a[20:23]\n" \
M_"a[20:23], a[196:199], a[228:231], a[20:23]\n" \
X_"a[180:181], v37 offset:0\n" \
M_"a[16:19], a[132:135], a[160:163], a[16:19]\n" \
M_"a[16:19], a[196:199], a[224:227], a[16:19]\n" \
M_"a[32:35], a[136:139], a[160:163], a[32:35]\n" \
X_"a[182:183], v37 offset:1024\n" \
M_"a[32:35], a[200:203], a[224:227], a[32:35]\n" \
M_"a[36:39], a[136:139], a[164:167], a[36:39]\n" \
M_"a[36:39], a[200:203], a[228:231], a[36:39]\n" \
"buffer_store_dwordx4 v[200:203], v20, s[80:83], s35 offen sc1\n" \
M_"a[40:43], a[136:139], a[168:171], a[40:43]\n" \
"s_mov_b32 m0, s32\n" \
X_"a[244:245], v37 offset:8192\n" \
M_"a[40:43], a[200:203], a[232:235], a[40:43]\n" \
"s_mov_b32 exec_lo, 0x0\n" \
"s_mov_b32 exec_hi, 0xffffffff\n" \
"global_load_lds_dword v[188:189], off\n" \
"s_mov_b64 exec, -1\n" \
M_"a[44:47], a[136:139], a[172:175], a[44:47]\n" \
X_"a[246:247], v37 offset:9216\n" \
M_"a[44:47], a[200:203], a[236:239], a[44:47]\n" \
M_"a[60:63], a[140:143], a[172:175], a[60:63]\n" \
M_"a[60:63], a[204:207], a[236:239], a[60:63]\n" \
M_"a[56:59], a[140:143], a[168:171], a[56:59]\n" \
X_"a[184:185], v38 offset:0\n" \
M_"a[56:59], a[204:207], a[232:235], a[56:59]\n" \
M_"a[52:55], a[140:143], a[164:167], a[52:55]\n" \
"buffer_store_dwordx4 v[204:207], v20, s[80:83], s88 offen sc1\n" \
M_"a[52:55], a[204:207], a[228:231], a[52:55]\n" \
X_"a[186:187], v38 offset:1024\n" \
M_"a[48:51], a[140:143], a[160:163], a[48:51]\n" \
M_"a[48:51], a[204:207], a[224:227], a[48:51]\n" \
M_"a[64:67], a[144:147], a[160:163], a[64:67]\n" \
X_"a[248:249], v38 offset:8192\n" \
M_"a[64:67], a[208:211], a[224:227], a[64:67]\n" \
M_"a[68:71], a[144:147], a[164:167], a[68:71]\n" \
M_"a[68:71], a[208:211], a[228:231], a[68:71]\n" \
X_"a[250:251], v38 offset:9216\n" \
M_"a[72:75], a[144:147], a[168:171], a[72:75]\n" \
M_"a[72:75], a[208:211], a[232:235], a[72:75]\n" \
M_"a[76:79], a[144:147], a[172:175], a[76:79]\n" \
"buffer_store_dwordx4 v[208:211], v20, s[80:83], s36 offen sc1\n" \
M_"a[76:79], a[208:211], a[236:239], a[76:79]\n" \
X_"a[188:189], v39 offset:0\n" \
M_"a[92:95], a[148:151], a[172:175], a[92:95]\n" \
M_"a[92:95], a[212:215], a[236:239], a[92:95]\n" \
M_"a[88:91], a[148:151], a[168:171], a[88:91]\n" \
X_"a[190:191], v39 offset:1024\n" \
M_"a[88:91], a[212:215], a[232:235], a[88:91]\n" \
M_"a[84:87], a[148:151], a[164:167], a[84:87]\n" \
M_"a[84:87], a[212:215], a[228:231], a[84:87]\n" \
X_"a[252:253], v39 offset:8192\n" \
M_"a[80:83], a[148:151], a[160:163], a[80:83]\n" \
M_"a[80:83], a[212:215], a[224:227], a[80:83]\n" \
M_"a[96:99], a[152:155], a[160:163], a[96:99]\n" \
X_"a[254:255], v39 offset:9216\n" \
M_"a[96:99], a[216:219], a[224:227], a[96:99]\n" \
M_"a[100:103], a[152:155], a[164:167], a[100:103]\n" \
M_"a[100:103], a[216:219], a[228:231], a[100:103]\n" \
M_"a[104:107], a[152:155], a[168:171], a[104:107]\n" \
M_"a[104:107], a[216:219], a[232:235], a[104:107]\n" \
M_"a[108:111], a[152:155], a[172:175], a[108:111]\n" \
M_"a[108:111], a[216:219], a[236:239], a[108:111]\n" \
"s_waitcnt lgkmcnt(15)\n" \
M_"a[124:127], a[156:159], a[172:175], a[124:127]\n" \
M_"a[124:127], a[220:223], a[236:239], a[124:127]\n" \
M_"a[120:123], a[156:159], a[168:171], a[120:123]\n" \
M_"a[120:123], a[220:223], a[232:235], a[120:123]\n" \
M_"a[116:119], a[156:159], a[164:167], a[116:119]\n" \
M_"a[116:119], a[220:223], a[228:231], a[116:119]\n" \
M_"a[112:115], a[156:159], a[160:163], a[112:115]\n" \
"s_waitcnt vmcnt(4) lgkmcnt(0)\n" \
M_"a[112:115], a[220:223], a[224:227], a[112:115]\n" \
"s_barrier\n" \
M_"v[52:55], a[128:131], a[176:179], v[52:55]\n" \
"s_add_u32 m0, s30, 0x0\n" \
"s_movk_i32 s84, 0x300\n" \
M_"v[52:55], a[192:195], a[240:243], v[52:55]\n" \
B_"v22, s[64:67], s84 offen offset:0 lds\n" \
M_"v[56:59], a[128:131], a[180:183], v[56:59]\n" \
X_"a[160:161], v32 offset:32768\n" \
M_"v[56:59], a[192:195], a[244:247], v[56:59]\n" \
"s_movk_i32 s84, 0x1f00\n" \
M_"v[60:63], a[128:131], a[184:187], v[60:63]\n" \
B_"v23, s[64:67], s84 offen offset:1024 lds\n" \
M_"v[60:63], a[192:195], a[248:251], v[60:63]\n" \
X_"a[162:163], v32 offset:33792\n" \
M_"v[64:67], a[128:131], a[188:191], v[64:67]\n" \
X_"a[224:225], v32 offset:40960\n" \
M_"v[64:67], a[192:195], a[252:255], v[64:67]\n" \
R_"a[128:131], v28 offset:49152\n" \
M_"v[80:83], a[132:135], a[188:191], v[80:83]\n" \
R_"a[192:195], v29 offset:49152\n" \
M_"v[80:83], a[196:199], a[252:255], v[80:83]\n" \
"s_movk_i32 s84, 0x3b00\n" \
M_"v[76:79], a[132:135], a[184:187], v[76:79]\n" \
B_"v22, s[64:67], s84 offen offset:2048 lds\n" \
M_"v[76:79], a[196:199], a[248:251], v[76:79]\n" \
"s_movk_i32 s84, 0x5700\n" \
M_"v[72:75], a[132:135], a[180:183], v[72:75]\n" \
B_"v23, s[64:67], s84 offen offset:3072 lds\n" \
M_"v[72:75], a[196:199], a[244:247], v[72:75]\n" \
"s_add_u32 m0, s31, 0x0\n" \
"s_mov_b32 s84, 0x300000\n" \
M_"v[68:71], a[132:135], a[176:179], v[68:71]\n" \
B_"v26, s[68:71], s84 offen offset:0 lds\n" \
M_"v[68:71], a[196:199], a[240:243], v[68:71]\n" \
R_"a[132:135], v28 offset:51200\n" \
M_"v[84:87], a[136:139], a[176:179], v[84:87]\n" \
R_"a[196:199], v29 offset:51200\n" \
M_"v[84:87], a[200:203], a[240:243], v[84:87]\n" \
"s_addk_i32 s84, 0x7c00\n" \
M_"v[88:91], a[136:139], a[180:183], v[88:91]\n" \
B_"v26, s[68:71], s84 offen offset:1024 lds\n" \
M_"v[88:91], a[200:203], a[244:247], v[88:91]\n" \
X_"a[226:227], v32 offset:41984\n" \
M_"v[92:95], a[136:139], a[184:187], v[92:95]\n" \
"s_addk_i32 s84, 0x7c00\n" \
M_"v[92:95], a[200:203], a[248:251], v[92:95]\n" \
B_"v27, s[68:71], s84 offen offset:2048 lds\n" \
M_"v[96:99], a[136:139], a[188:191], v[96:99]\n" \
X_"a[164:165], v33 offset:32768\n" \
M_"v[96:99], a[200:203], a[252:255], v[96:99]\n" \
R_"a[136:139], v28 offset:53248\n" \
M_"v[112:115], a[140:143], a[188:191], v[112:115]\n" \
R_"a[200:203], v29 offset:53248\n" \
M_"v[112:115], a[204:207], a[252:255], v[112:115]\n" \
"s_addk_i32 s84, 0x7c00\n" \
M_"v[108:111], a[140:143], a[184:187], v[108:111]\n" \
B_"v27, s[68:71], s84 offen offset:3072 lds\n" \
M_"v[108:111], a[204:207], a[248:251], v[108:111]\n" \
"s_add_u32 m0, s30, 0x1000\n" \
"s_mov_b32 s84, 0x8300\n" \
M_"v[104:107], a[140:143], a[180:183], v[104:107]\n" \
B_"v22, s[64:67], s84 offen offset:0 lds\n" \
M_"v[104:107], a[204:207], a[244:247], v[104:107]\n" \
X_"a[166:167], v33 offset:33792\n" \
M_"v[100:103], a[140:143], a[176:179], v[100:103]\n" \
X_"a[228:229], v33 offset:40960\n" \
M_"v[100:103], a[204:207], a[240:243], v[100:103]\n" \
R_"a[140:143], v28 offset:55296\n" \
M_"v[116:119], a[144:147], a[176:179], v[116:119]\n" \
R_"a[204:207], v29 offset:55296\n" \
M_"v[116:119], a[208:211], a[240:243], v[116:119]\n" \
"s_addk_i32 s84, 0x1c00\n" \
M_"v[120:123], a[144:147], a[180:183], v[120:123]\n" \
B_"v23, s[64:67], s84 offen offset:1024 lds\n" \
M_"v[120:123], a[208:211], a[244:247], v[120:123]\n" \
"s_addk_i32 s84, 0x1c00\n" \
M_"v[124:127], a[144:147], a[184:187], v[124:127]\n" \
B_"v22, s[64:67], s84 offen offset:2048 lds\n" \
M_"v[124:127], a[208:211], a[248:251], v[124:127]\n" \
"s_addk_i32 s84, 0x1c00\n" \
"s_nop 0\n" \
M_"v[128:131], a[144:147], a[188:191], v[128:131]\n" \
B_"v23, s[64:67], s84 offen offset:3072 lds\n" \
M_"v[128:131], a[208:211], a[252:255], v[128:131]\n" \
R_"a[144:147], v28 offset:32768\n" \
M_"v[144:147], a[148:151], a[188:191], v[144:147]\n" \
R_"a[208:211], v29 offset:32768\n" \
M_"v[144:147], a[212:215], a[252:255], v[144:147]\n" \
"s_add_u32 m0, s31, 0x4000\n" \
"s_mov_b32 s84, 0x300000\n" \
M_"v[140:143], a[148:151], a[184:187], v[140:143]\n" \
B_"v26, s[72:75], s84 offen offset:0 lds\n" \
M_"v[140:143], a[212:215], a[248:251], v[140:143]\n" \
X_"a[230:231], v33 offset:41984\n" \
M_"v[136:139], a[148:151], a[180:183], v[136:139]\n" \
"s_addk_i32 s84, 0x7c00\n" \
"s_nop 0\n" \
M_"v[136:139], a[212:215], a[244:247], v[136:139]\n" \
B_"v26, s[72:75], s84 offen offset:1024 lds\n" \
M_"v[132:135], a[148:151], a[176:179], v[132:135]\n" \
X_"a[168:169], v34 offset:32768\n" \
M_"v[132:135], a[212:215], a[240:243], v[132:135]\n" \
R_"a[148:151], v28 offset:34816\n" \
M_"v[148:151], a[152:155], a[176:179], v[148:151]\n" \
R_"a[212:215], v29 offset:34816\n" \
M_"v[148:151], a[216:219], a[240:243], v[148:151]\n" \
X_"a[170:171], v34 offset:33792\n" \
M_"v[152:155], a[152:155], a[180:183], v[152:155]\n" \
X_"a[232:233], v34 offset:40960\n" \
M_"v[152:155], a[216:219], a[244:247], v[152:155]\n" \
X_"a[234:235], v34 offset:41984\n" \
M_"v[156:159], a[152:155], a[184:187], v[156:159]\n" \
X_"a[172:173], v35 offset:32768\n" \
M_"v[156:159], a[216:219], a[248:251], v[156:159]\n" \
X_"a[174:175], v35 offset:33792\n" \
M_"v[160:163], a[152:155], a[188:191], v[160:163]\n" \
X_"a[236:237], v35 offset:40960\n" \
M_"v[160:163], a[216:219], a[252:255], v[160:163]\n" \
R_"a[152:155], v28 offset:36864\n" \
M_"v[176:179], a[156:159], a[188:191], v[176:179]\n" \
R_"a[216:219], v29 offset:36864\n" \
M_"v[176:179], a[220:223], a[252:255], v[176:179]\n" \
X_"a[238:239], v35 offset:41984\n" \
M_"v[172:175], a[156:159], a[184:187], v[172:175]\n" \
M_"v[172:175], a[220:223], a[248:251], v[172:175]\n" \
M_"v[168:171], a[156:159], a[180:183], v[168:171]\n" \
M_"v[168:171], a[220:223], a[244:247], v[168:171]\n" \
M_"v[164:167], a[156:159], a[176:179], v[164:167]\n" \
M_"v[164:167], a[220:223], a[240:243], v[164:167]\n" \
R_"a[156:159], v28 offset:38912\n" \
R_"a[220:223], v29 offset:38912\n" \
"s_waitcnt lgkmcnt(15)\n" \
M_"a[0:3], a[128:131], a[160:163], a[0:3]\n" \
"s_addk_i32 s84, 0x7c00\n" \
M_"a[0:3], a[192:195], a[224:227], a[0:3]\n" \
B_"v27, s[72:75], s84 offen offset:2048 lds\n" \
M_"a[4:7], a[128:131], a[164:167], a[4:7]\n" \
X_"a[176:177], v36 offset:32768\n" \
"s_waitcnt lgkmcnt(15)\n" \
M_"a[4:7], a[192:195], a[228:231], a[4:7]\n" \
"s_addk_i32 s84, 0x7c00\n" \
"s_waitcnt lgkmcnt(11)\n" \
M_"a[8:11], a[128:131], a[168:171], a[8:11]\n" \
B_"v27, s[72:75], s84 offen offset:3072 lds\n" \
"s_waitcnt lgkmcnt(9)\n" \
M_"a[8:11], a[192:195], a[232:235], a[8:11]\n" \
X_"a[178:179], v36 offset:33792\n" \
"s_waitcnt lgkmcnt(8)\n" \
M_"a[12:15], a[128:131], a[172:175], a[12:15]\n" \
X_"a[240:241], v36 offset:40960\n" \
"s_waitcnt lgkmcnt(5)\n" \
M_"a[12:15], a[192:195], a[236:239], a[12:15]\n" \
M_"a[28:31], a[132:135], a[172:175], a[28:31]\n" \
M_"a[28:31], a[196:199], a[236:239], a[28:31]\n" \
X_"a[242:243], v36 offset:41984\n" \
M_"a[24:27], a[132:135], a[168:171], a[24:27]\n" \
M_"a[24:27], a[196:199], a[232:235], a[24:27]\n" \
M_"a[20:23], a[132:135], a[164:167], a[20:23]\n" \
M_"a[20:23], a[196:199], a[228:231], a[20:23]\n" \
X_"a[180:181], v37 offset:32768\n" \
M_"a[16:19], a[132:135], a[160:163], a[16:19]\n" \
M_"a[16:19], a[196:199], a[224:227], a[16:19]\n" \
M_"a[32:35], a[136:139], a[160:163], a[32:35]\n" \
X_"a[182:183], v37 offset:33792\n" \
M_"a[32:35], a[200:203], a[224:227], a[32:35]\n" \
M_"a[36:39], a[136:139], a[164:167], a[36:39]\n" \
"v_lshlrev_b32_e32 v45, 3, v20\n" \
"v_add_u32_e32 v45, s31, v45\n" \
M_"a[36:39], a[200:203], a[228:231], a[36:39]\n" \
"s_getpc_b64 s[86:87]\n" \
"s_sub_u32 s86, s86, 0x10800\n" \
"s_subb_u32 s87, s87, 0\n" \
"s_mov_b32 m0, s32\n" \
"s_nop 0\n" \
M_"a[40:43], a[136:139], a[168:171], a[40:43]\n" \
"global_load_lds_dword v45, s[86:87]\n" \
M_"a[40:43], a[200:203], a[232:235], a[40:43]\n" \
X_"a[244:245], v37 offset:40960\n" \
M_"a[44:47], a[136:139], a[172:175], a[44:47]\n" \
X_"a[246:247], v37 offset:41984\n" \
M_"a[44:47], a[200:203], a[236:239], a[44:47]\n" \
M_"a[60:63], a[140:143], a[172:175], a[60:63]\n" \
M_"a[60:63], a[204:207], a[236:239], a[60:63]\n" \
M_"a[56:59], a[140:143], a[168:171], a[56:59]\n" \
X_"a[184:185], v38 offset:32768\n" \
M_"a[56:59], a[204:207], a[232:235], a[56:59]\n" \
M_"a[52:55], a[140:143], a[164:167], a[52:55]\n" \
M_"a[52:55], a[204:207], a[228:231], a[52:55]\n" \
X_"a[186:187], v38 offset:33792\n" \
M_"a[48:51], a[140:143], a[160:163], a[48:51]\n" \
M_"a[48:51], a[204:207], a[224:227], a[48:51]\n" \
M_"a[64:67], a[144:147], a[160:163], a[64:67]\n" \
X_"a[248:249], v38 offset:40960\n" \
M_"a[64:67], a[208:211], a[224:227], a[64:67]\n" \
M_"a[68:71], a[144:147], a[164:167], a[68:71]\n" \
M_"a[68:71], a[208:211], a[228:231], a[68:71]\n" \
X_"a[250:251], v38 offset:41984\n" \
M_"a[72:75], a[144:147], a[168:171], a[72:75]\n" \
M_"a[72:75], a[208:211], a[232:235], a[72:75]\n" \
M_"a[76:79], a[144:147], a[172:175], a[76:79]\n" \
M_"a[76:79], a[208:211], a[236:239], a[76:79]\n" \
X_"a[188:189], v39 offset:32768\n" \
M_"a[92:95], a[148:151], a[172:175], a[92:95]\n" \
M_"a[92:95], a[212:215], a[236:239], a[92:95]\n" \
M_"a[88:91], a[148:151], a[168:171], a[88:91]\n" \
X_"a[190:191], v39 offset:33792\n" \
M_"a[88:91], a[212:215], a[232:235], a[88:91]\n" \
M_"a[84:87], a[148:151], a[164:167], a[84:87]\n" \
M_"a[84:87], a[212:215], a[228:231], a[84:87]\n" \
X_"a[252:253], v39 offset:40960\n" \
M_"a[80:83], a[148:151], a[160:163], a[80:83]\n" \
M_"a[80:83], a[212:215], a[224:227], a[80:83]\n" \
M_"a[96:99], a[152:155], a[160:163], a[96:99]\n" \
X_"a[254:255], v39 offset:41984\n" \
M_"a[96:99], a[216:219], a[224:227], a[96:99]\n" \
M_"a[100:103], a[152:155], a[164:167], a[100:103]\n" \
M_"a[100:103], a[216:219], a[228:231], a[100:103]\n" \
M_"a[104:107], a[152:155], a[168:171], a[104:107]\n" \
M_"a[104:107], a[216:219], a[232:235], a[104:107]\n" \
M_"a[108:111], a[152:155], a[172:175], a[108:111]\n" \
M_"a[108:111], a[216:219], a[236:239], a[108:111]\n" \
"s_waitcnt lgkmcnt(15)\n" \
M_"a[124:127], a[156:159], a[172:175], a[124:127]\n" \
M_"a[124:127], a[220:223], a[236:239], a[124:127]\n" \
M_"a[120:123], a[156:159], a[168:171], a[120:123]\n" \
M_"a[120:123], a[220:223], a[232:235], a[120:123]\n" \
M_"a[116:119], a[156:159], a[164:167], a[116:119]\n" \
M_"a[116:119], a[220:223], a[228:231], a[116:119]\n" \
M_"a[112:115], a[156:159], a[160:163], a[112:115]\n" \
"s_waitcnt vmcnt(1) lgkmcnt(0)\n" \
M_"a[112:115], a[220:223], a[224:227], a[112:115]\n" \
"s_barrier\n" \
"v_mov_b32_e32 v45, s62\n" \
"s_mov_b64 exec, 1\n" \
"buffer_store_dword v45, off, s[80:83], s37 offset:128 sc1\n" \
"s_mov_b64 exec, -1\n" \
M_"v[52:55], a[128:131], a[176:179], v[52:55]\n" \
"s_add_u32 m0, s30, 0x8000\n" \
"s_movk_i32 s84, 0x380\n" \
M_"v[52:55], a[192:195], a[240:243], v[52:55]\n" \
B_"v22, s[64:67], s84 offen offset:0 lds\n" \
M_"v[56:59], a[128:131], a[180:183], v[56:59]\n" \
X_"a[160:161], v32 offset:0\n" \
M_"v[56:59], a[192:195], a[244:247], v[56:59]\n" \
"s_movk_i32 s84, 0x1f80\n" \
M_"v[60:63], a[128:131], a[184:187], v[60:63]\n" \
B_"v23, s[64:67], s84 offen offset:1024 lds\n" \
M_"v[60:63], a[192:195], a[248:251], v[60:63]\n" \
X_"a[162:163], v32 offset:1024\n" \
M_"v[64:67], a[128:131], a[188:191], v[64:67]\n" \
X_"a[224:225], v32 offset:8192\n" \
M_"v[64:67], a[192:195], a[252:255], v[64:67]\n" \
R_"a[128:131], v28 offset:16384\n" \
M_"v[80:83], a[132:135], a[188:191], v[80:83]\n" \
R_"a[192:195], v29 offset:16384\n" \
M_"v[80:83], a[196:199], a[252:255], v[80:83]\n" \
"s_movk_i32 s84, 0x3b80\n" \
M_"v[76:79], a[132:135], a[184:187], v[76:79]\n" \
B_"v22, s[64:67], s84 offen offset:2048 lds\n" \
M_"v[76:79], a[196:199], a[248:251], v[76:79]\n" \
"s_movk_i32 s84, 0x5780\n" \
M_"v[72:75], a[132:135], a[180:183], v[72:75]\n" \
B_"v23, s[64:67], s84 offen offset:3072 lds\n" \
M_"v[72:75], a[196:199], a[244:247], v[72:75]\n" \
"s_add_u32 m0, s31, 0x8000\n" \
"s_mov_b32 s84, 0x380000\n" \
M_"v[68:71], a[132:135], a[176:179], v[68:71]\n" \
B_"v26, s[68:71], s84 offen offset:0 lds\n" \
M_"v[68:71], a[196:199], a[240:243], v[68:71]\n" \
R_"a[132:135], v28 offset:18432\n" \
M_"v[84:87], a[136:139], a[176:179], v[84:87]\n" \
R_"a[196:199], v29 offset:18432\n" \
M_"v[84:87], a[200:203], a[240:243], v[84:87]\n" \
"s_addk_i32 s84, 0x7c00\n" \
M_"v[88:91], a[136:139], a[180:183], v[88:91]\n" \
B_"v26, s[68:71], s84 offen offset:1024 lds\n" \
M_"v[88:91], a[200:203], a[244:247], v[88:91]\n" \
X_"a[226:227], v32 offset:9216\n" \
M_"v[92:95], a[136:139], a[184:187], v[92:95]\n" \
"s_addk_i32 s84, 0x7c00\n" \
M_"v[92:95], a[200:203], a[248:251], v[92:95]\n" \
B_"v27, s[68:71], s84 offen offset:2048 lds\n" \
M_"v[96:99], a[136:139], a[188:191], v[96:99]\n" \
X_"a[164:165], v33 offset:0\n" \
M_"v[96:99], a[200:203], a[252:255], v[96:99]\n" \
R_"a[136:139], v28 offset:20480\n" \
M_"v[112:115], a[140:143], a[188:191], v[112:115]\n" \
R_"a[200:203], v29 offset:20480\n" \
M_"v[112:115], a[204:207], a[252:255], v[112:115]\n" \
"s_addk_i32 s84, 0x7c00\n" \
"s_nop 0\n" \
M_"v[108:111], a[140:143], a[184:187], v[108:111]\n" \
B_"v27, s[68:71], s84 offen offset:3072 lds\n" \
M_"v[108:111], a[204:207], a[248:251], v[108:111]\n" \
"s_add_u32 m0, s30, 0x9000\n" \
"s_mov_b32 s84, 0x8380\n" \
M_"v[104:107], a[140:143], a[180:183], v[104:107]\n" \
B_"v22, s[64:67], s84 offen offset:0 lds\n" \
M_"v[104:107], a[204:207], a[244:247], v[104:107]\n" \
X_"a[166:167], v33 offset:1024\n" \
M_"v[100:103], a[140:143], a[176:179], v[100:103]\n" \
X_"a[228:229], v33 offset:8192\n" \
M_"v[100:103], a[204:207], a[240:243], v[100:103]\n" \
R_"a[140:143], v28 offset:22528\n" \
M_"v[116:119], a[144:147], a[176:179], v[116:119]\n" \
R_"a[204:207], v29 offset:22528\n" \
M_"v[116:119], a[208:211], a[240:243], v[116:119]\n" \
"s_addk_i32 s84, 0x1c00\n" \
M_"v[120:123], a[144:147], a[180:183], v[120:123]\n" \
B_"v23, s[64:67], s84 offen offset:1024 lds\n" \
M_"v[120:123], a[208:211], a[244:247], v[120:123]\n" \
"s_addk_i32 s84, 0x1c00\n" \
M_"v[124:127], a[144:147], a[184:187], v[124:127]\n" \
B_"v22, s[64:67], s84 offen offset:2048 lds\n" \
M_"v[124:127], a[208:211], a[248:251], v[124:127]\n" \
"s_addk_i32 s84, 0x1c00\n" \
"s_nop 0\n" \
M_"v[128:131], a[144:147], a[188:191], v[128:131]\n" \
B_"v23, s[64:67], s84 offen offset:3072 lds\n" \
M_"v[128:131], a[208:211], a[252:255], v[128:131]\n" \
R_"a[144:147], v28 offset:0\n" \
M_"v[144:147], a[148:151], a[188:191], v[144:147]\n" \
R_"a[208:211], v29 offset:0\n" \
M_"v[144:147], a[212:215], a[252:255], v[144:147]\n" \
"s_add_u32 m0, s31, 0xc000\n" \
"s_mov_b32 s84, 0x380000\n" \
M_"v[140:143], a[148:151], a[184:187], v[140:143]\n" \
B_"v26, s[72:75], s84 offen offset:0 lds\n" \
M_"v[140:143], a[212:215], a[248:251], v[140:143]\n" \
X_"a[230:231], v33 offset:9216\n" \
M_"v[136:139], a[148:151], a[180:183], v[136:139]\n" \
"s_addk_i32 s84, 0x7c00\n" \
"s_nop 0\n" \
M_"v[136:139], a[212:215], a[244:247], v[136:139]\n" \
B_"v26, s[72:75], s84 offen offset:1024 lds\n" \
M_"v[132:135], a[148:151], a[176:179], v[132:135]\n" \
X_"a[168:169], v34 offset:0\n" \
M_"v[132:135], a[212:215], a[240:243], v[132:135]\n" \
R_"a[148:151], v28 offset:2048\n" \
M_"v[148:151], a[152:155], a[176:179], v[148:151]\n" \
R_"a[212:215], v29 offset:2048\n" \
M_"v[148:151], a[216:219], a[240:243], v[148:151]\n" \
X_"a[170:171], v34 offset:1024\n" \
M_"v[152:155], a[152:155], a[180:183], v[152:155]\n" \
X_"a[232:233], v34 offset:8192\n" \
M_"v[152:155], a[216:219], a[244:247], v[152:155]\n" \
X_"a[234:235], v34 offset:9216\n" \
M_"v[156:159], a[152:155], a[184:187], v[156:159]\n" \
X_"a[172:173], v35 offset:0\n" \
M_"v[156:159], a[216:219], a[248:251], v[156:159]\n" \
X_"a[174:175], v35 offset:1024\n" \
M_"v[160:163], a[152:155], a[188:191], v[160:163]\n" \
X_"a[236:237], v35 offset:8192\n" \
M_"v[160:163], a[216:219], a[252:255], v[160:163]\n" \
R_"a[152:155], v28 offset:4096\n" \
M_"v[176:179], a[156:159], a[188:191], v[176:179]\n" \
R_"a[216:219], v29 offset:4096\n" \
M_"v[176:179], a[220:223], a[252:255], v[176:179]\n" \
X_"a[238:239], v35 offset:9216\n" \
M_"v[172:175], a[156:159], a[184:187], v[172:175]\n" \
M_"v[172:175], a[220:223], a[248:251], v[172:175]\n" \
M_"v[168:171], a[156:159], a[180:183], v[168:171]\n" \
M_"v[168:171], a[220:223], a[244:247], v[168:171]\n" \
M_"v[164:167], a[156:159], a[176:179], v[164:167]\n" \
M_"v[164:167], a[220:223], a[240:243], v[164:167]\n" \
R_"a[156:159], v28 offset:6144\n" \
R_"a[220:223], v29 offset:6144\n" \
"s_waitcnt lgkmcnt(15)\n" \
M_"a[0:3], a[128:131], a[160:163], a[0:3]\n" \
"s_addk_i32 s84, 0x7c00\n" \
M_"a[0:3], a[192:195], a[224:227], a[0:3]\n" \
B_"v27, s[72:75], s84 offen offset:2048 lds\n" \
M_"a[4:7], a[128:131], a[164:167], a[4:7]\n" \
X_"a[176:177], v36 offset:0\n" \
"s_waitcnt lgkmcnt(15)\n" \
M_"a[4:7], a[192:195], a[228:231], a[4:7]\n" \
"s_addk_i32 s84, 0x7c00\n" \
"s_waitcnt lgkmcnt(11)\n" \
M_"a[8:11], a[128:131], a[168:171], a[8:11]\n" \
B_"v27, s[72:75], s84 offen offset:3072 lds\n" \
"s_waitcnt lgkmcnt(9)\n" \
M_"a[8:11], a[192:195], a[232:235], a[8:11]\n" \
X_"a[178:179], v36 offset:1024\n" \
"s_waitcnt lgkmcnt(8)\n" \
M_"a[12:15], a[128:131], a[172:175], a[12:15]\n" \
X_"a[240:241], v36 offset:8192\n" \
"s_waitcnt lgkmcnt(5)\n" \
M_"a[12:15], a[192:195], a[236:239], a[12:15]\n" \
M_"a[28:31], a[132:135], a[172:175], a[28:31]\n" \
M_"a[28:31], a[196:199], a[236:239], a[28:31]\n" \
X_"a[242:243], v36 offset:9216\n" \
M_"a[24:27], a[132:135], a[168:171], a[24:27]\n" \
M_"a[24:27], a[196:199], a[232:235], a[24:27]\n" \
M_"a[20:23], a[132:135], a[164:167], a[20:23]\n" \
M_"a[20:23], a[196:199], a[228:231], a[20:23]\n" \
X_"a[180:181], v37 offset:0\n" \
M_"a[16:19], a[132:135], a[160:163], a[16:19]\n" \
M_"a[16:19], a[196:199], a[224:227], a[16:19]\n" \
M_"a[32:35], a[136:139], a[160:163], a[32:35]\n" \
"v_bfe_u32 v44, v20, 2, 5\n" \
X_"a[182:183], v37 offset:1024\n" \
M_"a[32:35], a[200:203], a[224:227], a[32:35]\n" \
"buffer_load_dword v44, v44, s[80:83], s38 offen offset:128 sc1\n" \
M_"a[36:39], a[136:139], a[164:167], a[36:39]\n" \
M_"a[36:39], a[200:203], a[228:231], a[36:39]\n" \
X_"a[244:245], v37 offset:8192\n" \
M_"a[40:43], a[136:139], a[168:171], a[40:43]\n" \
M_"a[40:43], a[200:203], a[232:235], a[40:43]\n" \
M_"a[44:47], a[136:139], a[172:175], a[44:47]\n" \
X_"a[246:247], v37 offset:9216\n" \
M_"a[44:47], a[200:203], a[236:239], a[44:47]\n" \
M_"a[60:63], a[140:143], a[172:175], a[60:63]\n" \
"global_store_dwordx4 v49, v[0:3], s[28:29] offset:0 sc1\n" \
M_"a[60:63], a[204:207], a[236:239], a[60:63]\n" \
M_"a[56:59], a[140:143], a[168:171], a[56:59]\n" \
X_"a[184:185], v38 offset:0\n" \
M_"a[56:59], a[204:207], a[232:235], a[56:59]\n" \
M_"a[52:55], a[140:143], a[164:167], a[52:55]\n" \
M_"a[52:55], a[204:207], a[228:231], a[52:55]\n" \
X_"a[186:187], v38 offset:1024\n" \
M_"a[48:51], a[140:143], a[160:163], a[48:51]\n" \
M_"a[48:51], a[204:207], a[224:227], a[48:51]\n" \
M_"a[64:67], a[144:147], a[160:163], a[64:67]\n" \
"global_store_dwordx4 v49, v[0:3], s[28:29] offset:1024 sc1\n" \
M_"a[64:67], a[208:211], a[224:227], a[64:67]\n" \
X_"a[248:249], v38 offset:8192\n" \
M_"a[68:71], a[144:147], a[164:167], a[68:71]\n" \
M_"a[68:71], a[208:211], a[228:231], a[68:71]\n" \
X_"a[250:251], v38 offset:9216\n" \
M_"a[72:75], a[144:147], a[168:171], a[72:75]\n" \
M_"a[72:75], a[208:211], a[232:235], a[72:75]\n" \
M_"a[76:79], a[144:147], a[172:175], a[76:79]\n" \
M_"a[76:79], a[208:211], a[236:239], a[76:79]\n" \
X_"a[188:189], v39 offset:0\n" \
M_"a[92:95], a[148:151], a[172:175], a[92:95]\n" \
"s_mov_b64 exec, 0xffff\n" \
"global_store_dwordx4 v49, v[0:3], s[28:29] offset:2048 sc1\n" \
"s_mov_b64 exec, -1\n" \
"s_nop 0\n" \
M_"a[92:95], a[212:215], a[236:239], a[92:95]\n" \
M_"a[88:91], a[148:151], a[168:171], a[88:91]\n" \
X_"a[190:191], v39 offset:1024\n" \
M_"a[88:91], a[212:215], a[232:235], a[88:91]\n" \
M_"a[84:87], a[148:151], a[164:167], a[84:87]\n" \
M_"a[84:87], a[212:215], a[228:231], a[84:87]\n" \
X_"a[252:253], v39 offset:8192\n" \
M_"a[80:83], a[148:151], a[160:163], a[80:83]\n" \
M_"a[80:83], a[212:215], a[224:227], a[80:83]\n" \
M_"a[96:99], a[152:155], a[160:163], a[96:99]\n" \
X_"a[254:255], v39 offset:9216\n" \
M_"a[96:99], a[216:219], a[224:227], a[96:99]\n" \
M_"a[100:103], a[152:155], a[164:167], a[100:103]\n" \
M_"a[100:103], a[216:219], a[228:231], a[100:103]\n" \
M_"a[104:107], a[152:155], a[168:171], a[104:107]\n" \
M_"a[104:107], a[216:219], a[232:235], a[104:107]\n" \
".fill (L_pb + 57056 - .) / 4, 4, 0xBF800000\n" \
"L_pp14:\n" \
"s_cbranch_execz L_pret\n" \
M_"a[108:111], a[152:155], a[172:175], a[108:111]\n" \
M_"a[108:111], a[216:219], a[236:239], a[108:111]\n" \
"s_waitcnt lgkmcnt(15)\n" \
M_"a[124:127], a[156:159], a[172:175], a[124:127]\n" \
M_"a[124:127], a[220:223], a[236:239], a[124:127]\n" \
M_"a[120:123], a[156:159], a[168:171], a[120:123]\n" \
M_"a[120:123], a[220:223], a[232:235], a[120:123]\n" \
M_"a[116:119], a[156:159], a[164:167], a[116:119]\n" \
M_"a[116:119], a[220:223], a[228:231], a[116:119]\n" \
M_"a[112:115], a[156:159], a[160:163], a[112:115]\n" \
"s_waitcnt vmcnt(3) lgkmcnt(0)\n" \
M_"a[112:115], a[220:223], a[224:227], a[112:115]\n" \
"s_barrier\n" \
"v_cmp_ne_u32_e32 vcc, s62, v44\n" \
"s_nop 4\n" \
"s_cbranch_vccnz L_poll61_%=\n" \
"L_pollr61_%=:\n" \
"s_mov_b32 s68, s12\n" \
"s_and_b32 s69, s13, 0xffff\n" \
"s_mov_b32 s70, 0x480000\n" \
"s_mov_b32 s71, 0x20000\n" \
M_"v[52:55], a[128:131], a[176:179], v[52:55]\n" \
"s_add_u32 m0, s40, 0x0\n" \
"s_add_u32 s85, s39, 0x0\n" \
M_"v[52:55], a[192:195], a[240:243], v[52:55]\n" \
B_"v20, s[80:83], s85 offen sc1 lds\n" \
M_"v[56:59], a[128:131], a[180:183], v[56:59]\n" \
X_"a[160:161], v32 offset:32768\n" \
M_"v[56:59], a[192:195], a[244:247], v[56:59]\n" \
"s_add_u32 m0, s40, 0x1000\n" \
"s_add_u32 s85, s39, 0x1000\n" \
M_"v[60:63], a[128:131], a[184:187], v[60:63]\n" \
B_"v20, s[80:83], s85 offen sc1 lds\n" \
M_"v[60:63], a[192:195], a[248:251], v[60:63]\n" \
X_"a[162:163], v32 offset:33792\n" \
M_"v[64:67], a[128:131], a[188:191], v[64:67]\n" \
X_"a[224:225], v32 offset:40960\n" \
M_"v[64:67], a[192:195], a[252:255], v[64:67]\n" \
R_"a[128:131], v28 offset:49152\n" \
M_"v[80:83], a[132:135], a[188:191], v[80:83]\n" \
R_"a[192:195], v29 offset:49152\n" \
M_"v[80:83], a[196:199], a[252:255], v[80:83]\n" \
"s_add_u32 m0, s40, 0x3000\n" \
"s_movk_i32 s84, 0x0\n" \
M_"v[76:79], a[132:135], a[184:187], v[76:79]\n" \
B_"v21, s[76:79], s84 offen offset:0 lds\n" \
M_"v[76:79], a[196:199], a[248:251], v[76:79]\n" \
"s_add_u32 m0, s30, 0x0\n" \
"s_movk_i32 s84, 0x0\n" \
"s_nop 0\n" \
M_"v[72:75], a[132:135], a[180:183], v[72:75]\n" \
B_"v24, s[76:79], s84 offen offset:0 lds\n" \
M_"v[72:75], a[196:199], a[244:247], v[72:75]\n" \
X_"a[226:227], v32 offset:41984\n" \
M_"v[68:71], a[132:135], a[176:179], v[68:71]\n" \
X_"a[164:165], v33 offset:32768\n" \
M_"v[68:71], a[196:199], a[240:243], v[68:71]\n" \
R_"a[132:135], v28 offset:51200\n" \
M_"v[84:87], a[136:139], a[176:179], v[84:87]\n" \
R_"a[196:199], v29 offset:51200\n" \
M_"v[84:87], a[200:203], a[240:243], v[84:87]\n" \
"s_movk_i32 s84, 0x2000\n" \
M_"v[88:91], a[136:139], a[180:183], v[88:91]\n" \
B_"v25, s[76:79], s84 offen offset:1024 lds\n" \
M_"v[88:91], a[200:203], a[244:247], v[88:91]\n" \
"s_movk_i32 s84, 0x4000\n" \
M_"v[92:95], a[136:139], a[184:187], v[92:95]\n" \
B_"v24, s[76:79], s84 offen offset:2048 lds\n" \
M_"v[92:95], a[200:203], a[248:251], v[92:95]\n" \
X_"a[166:167], v33 offset:33792\n" \
M_"v[96:99], a[136:139], a[188:191], v[96:99]\n" \
X_"a[228:229], v33 offset:40960\n" \
M_"v[96:99], a[200:203], a[252:255], v[96:99]\n" \
R_"a[136:139], v28 offset:53248\n" \
M_"v[112:115], a[140:143], a[188:191], v[112:115]\n" \
R_"a[200:203], v29 offset:53248\n" \
M_"v[112:115], a[204:207], a[252:255], v[112:115]\n" \
"s_movk_i32 s84, 0x6000\n" \
M_"v[108:111], a[140:143], a[184:187], v[108:111]\n" \
B_"v25, s[76:79], s84 offen offset:3072 lds\n" \
M_"v[108:111], a[204:207], a[248:251], v[108:111]\n" \
"s_add_u32 m0, s31, 0x0\n" \
"s_movk_i32 s84, 0x0\n" \
M_"v[104:107], a[140:143], a[180:183], v[104:107]\n" \
B_"v26, s[68:71], s84 offen offset:0 lds\n" \
M_"v[104:107], a[204:207], a[244:247], v[104:107]\n" \
"s_movk_i32 s84, 0x7c00\n" \
M_"v[100:103], a[140:143], a[176:179], v[100:103]\n" \
B_"v26, s[68:71], s84 offen offset:1024 lds\n" \
M_"v[100:103], a[204:207], a[240:243], v[100:103]\n" \
R_"a[140:143], v28 offset:55296\n" \
M_"v[116:119], a[144:147], a[176:179], v[116:119]\n" \
R_"a[204:207], v29 offset:55296\n" \
M_"v[116:119], a[208:211], a[240:243], v[116:119]\n" \
"s_addk_i32 s84, 0x7c00\n" \
M_"v[120:123], a[144:147], a[180:183], v[120:123]\n" \
B_"v27, s[68:71], s84 offen offset:2048 lds\n" \
M_"v[120:123], a[208:211], a[244:247], v[120:123]\n" \
X_"a[230:231], v33 offset:41984\n" \
M_"v[124:127], a[144:147], a[184:187], v[124:127]\n" \
"s_addk_i32 s84, 0x7c00\n" \
M_"v[124:127], a[208:211], a[248:251], v[124:127]\n" \
B_"v27, s[68:71], s84 offen offset:3072 lds\n" \
M_"v[128:131], a[144:147], a[188:191], v[128:131]\n" \
X_"a[168:169], v34 offset:32768\n" \
M_"v[128:131], a[208:211], a[252:255], v[128:131]\n" \
R_"a[144:147], v28 offset:32768\n" \
M_"v[144:147], a[148:151], a[188:191], v[144:147]\n" \
R_"a[208:211], v29 offset:32768\n" \
M_"v[144:147], a[212:215], a[252:255], v[144:147]\n" \
"s_add_u32 m0, s30, 0x1000\n" \
"s_mov_b32 s84, 0x9000\n" \
M_"v[140:143], a[148:151], a[184:187], v[140:143]\n" \
B_"v24, s[76:79], s84 offen offset:0 lds\n" \
M_"v[140:143], a[212:215], a[248:251], v[140:143]\n" \
X_"a[170:171], v34 offset:33792\n" \
M_"v[136:139], a[148:151], a[180:183], v[136:139]\n" \
"s_addk_i32 s84, 0x2000\n" \
"s_nop 0\n" \
M_"v[136:139], a[212:215], a[244:247], v[136:139]\n" \
B_"v25, s[76:79], s84 offen offset:1024 lds\n" \
M_"v[132:135], a[148:151], a[176:179], v[132:135]\n" \
X_"a[232:233], v34 offset:40960\n" \
M_"v[132:135], a[212:215], a[240:243], v[132:135]\n" \
R_"a[148:151], v28 offset:34816\n" \
M_"v[148:151], a[152:155], a[176:179], v[148:151]\n" \
R_"a[212:215], v29 offset:34816\n" \
M_"v[148:151], a[216:219], a[240:243], v[148:151]\n" \
X_"a[234:235], v34 offset:41984\n" \
M_"v[152:155], a[152:155], a[180:183], v[152:155]\n" \
X_"a[172:173], v35 offset:32768\n" \
M_"v[152:155], a[216:219], a[244:247], v[152:155]\n" \
X_"a[174:175], v35 offset:33792\n" \
M_"v[156:159], a[152:155], a[184:187], v[156:159]\n" \
X_"a[236:237], v35 offset:40960\n" \
M_"v[156:159], a[216:219], a[248:251], v[156:159]\n" \
X_"a[238:239], v35 offset:41984\n" \
M_"v[160:163], a[152:155], a[188:191], v[160:163]\n" \
M_"v[160:163], a[216:219], a[252:255], v[160:163]\n" \
R_"a[152:155], v28 offset:36864\n" \
M_"v[176:179], a[156:159], a[188:191], v[176:179]\n" \
R_"a[216:219], v29 offset:36864\n" \
M_"v[176:179], a[220:223], a[252:255], v[176:179]\n" \
M_"v[172:175], a[156:159], a[184:187], v[172:175]\n" \
M_"v[172:175], a[220:223], a[248:251], v[172:175]\n" \
M_"v[168:171], a[156:159], a[180:183], v[168:171]\n" \
M_"v[168:171], a[220:223], a[244:247], v[168:171]\n" \
M_"v[164:167], a[156:159], a[176:179], v[164:167]\n" \
M_"v[164:167], a[220:223], a[240:243], v[164:167]\n" \
R_"a[156:159], v28 offset:38912\n" \
R_"a[220:223], v29 offset:38912\n" \
"s_waitcnt lgkmcnt(15)\n" \
M_"a[0:3], a[128:131], a[160:163], a[0:3]\n" \
"s_addk_i32 s84, 0x2000\n" \
M_"a[0:3], a[192:195], a[224:227], a[0:3]\n" \
B_"v24, s[76:79], s84 offen offset:2048 lds\n" \
M_"a[4:7], a[128:131], a[164:167], a[4:7]\n" \
X_"a[176:177], v36 offset:32768\n" \
M_"a[4:7], a[192:195], a[228:231], a[4:7]\n" \
"s_addk_i32 s84, 0x2000\n" \
"s_waitcnt lgkmcnt(13)\n" \
M_"a[8:11], a[128:131], a[168:171], a[8:11]\n" \
B_"v25, s[76:79], s84 offen offset:3072 lds\n" \
"s_waitcnt lgkmcnt(9)\n" \
M_"a[8:11], a[192:195], a[232:235], a[8:11]\n" \
X_"a[178:179], v36 offset:33792\n" \
"s_waitcnt lgkmcnt(8)\n" \
M_"a[12:15], a[128:131], a[172:175], a[12:15]\n" \
X_"a[240:241], v36 offset:40960\n" \
"s_waitcnt lgkmcnt(7)\n" \
M_"a[12:15], a[192:195], a[236:239], a[12:15]\n" \
M_"a[28:31], a[132:135], a[172:175], a[28:31]\n" \
M_"a[28:31], a[196:199], a[236:239], a[28:31]\n" \
"s_add_u32 m0, s30, 0x20000\n" \
"s_movk_i32 s84, 0x100\n" \
M_"a[24:27], a[132:135], a[168:171], a[24:27]\n" \
B_"v24, s[76:79], s84 offen offset:0 lds\n" \
M_"a[24:27], a[196:199], a[232:235], a[24:27]\n" \
X_"a[242:243], v36 offset:41984\n" \
M_"a[20:23], a[132:135], a[164:167], a[20:23]\n" \
"v_accvgpr_read_b32 v184, a12\n" \
"v_accvgpr_read_b32 v185, a13\n" \
M_"a[20:23], a[196:199], a[228:231], a[20:23]\n" \
X_"a[180:181], v37 offset:32768\n" \
"v_accvgpr_read_b32 v186, a14\n" \
M_"a[16:19], a[132:135], a[160:163], a[16:19]\n" \
"s_movk_i32 s84, 0x2100\n" \
"s_nop 0\n" \
M_"a[16:19], a[196:199], a[224:227], a[16:19]\n" \
B_"v25, s[76:79], s84 offen offset:1024 lds\n" \
M_"a[32:35], a[136:139], a[160:163], a[32:35]\n" \
X_"a[182:183], v37 offset:33792\n" \
"v_accvgpr_read_b32 v187, a15\n" \
M_"a[32:35], a[200:203], a[224:227], a[32:35]\n" \
"v_accvgpr_read_b32 v180, a28\n" \
"v_accvgpr_read_b32 v181, a29\n" \
M_"a[36:39], a[136:139], a[164:167], a[36:39]\n" \
"v_accvgpr_read_b32 v182, a30\n" \
"v_accvgpr_read_b32 v183, a31\n" \
M_"a[36:39], a[200:203], a[228:231], a[36:39]\n" \
"s_movk_i32 s84, 0x4100\n" \
"s_nop 0\n" \
M_"a[40:43], a[136:139], a[168:171], a[40:43]\n" \
B_"v24, s[76:79], s84 offen offset:2048 lds\n" \
M_"a[40:43], a[200:203], a[232:235], a[40:43]\n" \
X_"a[244:245], v37 offset:40960\n" \
C_"v184, v184, v185\n" \
M_"a[44:47], a[136:139], a[172:175], a[44:47]\n" \
X_"a[246:247], v37 offset:41984\n" \
C_"v185, v186, v187\n" \
M_"a[44:47], a[200:203], a[236:239], a[44:47]\n" \
C_"v186, v180, v181\n" \
C_"v187, v182, v183\n" \
M_"a[60:63], a[140:143], a[172:175], a[60:63]\n" \
"s_movk_i32 s84, 0x6100\n" \
M_"a[60:63], a[204:207], a[236:239], a[60:63]\n" \
B_"v25, s[76:79], s84 offen offset:3072 lds\n" \
M_"a[56:59], a[140:143], a[168:171], a[56:59]\n" \
X_"a[184:185], v38 offset:32768\n" \
"v_accvgpr_read_b32 v188, a8\n" \
M_"a[56:59], a[204:207], a[232:235], a[56:59]\n" \
"v_accvgpr_read_b32 v189, a9\n" \
"v_accvgpr_read_b32 v190, a10\n" \
M_"a[52:55], a[140:143], a[164:167], a[52:55]\n" \
"s_add_u32 m0, s30, 0x21000\n" \
"s_addk_i32 s84, 0x3000\n" \
M_"a[52:55], a[204:207], a[228:231], a[52:55]\n" \
B_"v24, s[76:79], s84 offen offset:0 lds\n" \
M_"a[48:51], a[140:143], a[160:163], a[48:51]\n" \
X_"a[186:187], v38 offset:33792\n" \
"v_accvgpr_read_b32 v191, a11\n" \
M_"a[48:51], a[204:207], a[224:227], a[48:51]\n" \
"v_accvgpr_read_b32 v180, a24\n" \
"v_accvgpr_read_b32 v181, a25\n" \
M_"a[64:67], a[144:147], a[160:163], a[64:67]\n" \
X_"a[248:249], v38 offset:40960\n" \
"v_accvgpr_read_b32 v182, a26\n" \
M_"a[64:67], a[208:211], a[224:227], a[64:67]\n" \
"s_addk_i32 s84, 0x2000\n" \
"s_nop 0\n" \
M_"a[68:71], a[144:147], a[164:167], a[68:71]\n" \
B_"v25, s[76:79], s84 offen offset:1024 lds\n" \
M_"a[68:71], a[208:211], a[228:231], a[68:71]\n" \
X_"a[250:251], v38 offset:41984\n" \
"v_accvgpr_read_b32 v183, a27\n" \
M_"a[72:75], a[144:147], a[168:171], a[72:75]\n" \
C_"v188, v188, v189\n" \
C_"v189, v190, v191\n" \
M_"a[72:75], a[208:211], a[232:235], a[72:75]\n" \
C_"v190, v180, v181\n" \
C_"v191, v182, v183\n" \
M_"a[76:79], a[144:147], a[172:175], a[76:79]\n" \
"s_addk_i32 s84, 0x2000\n" \
M_"a[76:79], a[208:211], a[236:239], a[76:79]\n" \
B_"v24, s[76:79], s84 offen offset:2048 lds\n" \
M_"a[92:95], a[148:151], a[172:175], a[92:95]\n" \
"s_add_u32 s89, s41, 0x0\n" \
"buffer_store_dwordx4 v[184:187], v20, s[80:83], s89 offen offset:3072 sc1\n" \
M_"a[92:95], a[212:215], a[236:239], a[92:95]\n" \
X_"a[188:189], v39 offset:32768\n" \
"v_accvgpr_read_b32 v192, a4\n" \
M_"a[88:91], a[148:151], a[168:171], a[88:91]\n" \
X_"a[190:191], v39 offset:33792\n" \
"v_accvgpr_read_b32 v193, a5\n" \
M_"a[88:91], a[212:215], a[232:235], a[88:91]\n" \
"s_addk_i32 s84, 0x2000\n" \
M_"a[84:87], a[148:151], a[164:167], a[84:87]\n" \
B_"v25, s[76:79], s84 offen offset:3072 lds\n" \
M_"a[84:87], a[212:215], a[228:231], a[84:87]\n" \
X_"a[252:253], v39 offset:40960\n" \
"v_accvgpr_read_b32 v194, a6\n" \
M_"a[80:83], a[148:151], a[160:163], a[80:83]\n" \
"s_add_u32 s88, s41, 0x0\n" \
"buffer_store_dwordx4 v[188:191], v20, s[80:83], s88 offen offset:2048 sc1\n" \
M_"a[80:83], a[212:215], a[224:227], a[80:83]\n" \
"v_accvgpr_read_b32 v195, a7\n" \
"v_accvgpr_read_b32 v180, a20\n" \
"s_waitcnt lgkmcnt(15)\n" \
"s_nop 0\n" \
M_"a[96:99], a[152:155], a[160:163], a[96:99]\n" \
X_"a[254:255], v39 offset:41984\n" \
"v_accvgpr_read_b32 v181, a21\n" \
M_"a[96:99], a[216:219], a[224:227], a[96:99]\n" \
"v_accvgpr_read_b32 v182, a22\n" \
"v_accvgpr_read_b32 v183, a23\n" \
M_"a[100:103], a[152:155], a[164:167], a[100:103]\n" \
C_"v192, v192, v193\n" \
C_"v193, v194, v195\n" \
M_"a[100:103], a[216:219], a[228:231], a[100:103]\n" \
C_"v194, v180, v181\n" \
C_"v195, v182, v183\n" \
M_"a[104:107], a[152:155], a[168:171], a[104:107]\n" \
"s_add_u32 s89, s41, 0x0\n" \
"s_nop 0\n" \
"buffer_store_dwordx4 v[192:195], v20, s[80:83], s89 offen offset:1024 sc1\n" \
M_"a[104:107], a[216:219], a[232:235], a[104:107]\n" \
"v_accvgpr_read_b32 v196, a0\n" \
"v_accvgpr_read_b32 v197, a1\n" \
M_"a[108:111], a[152:155], a[172:175], a[108:111]\n" \
"v_accvgpr_read_b32 v198, a2\n" \
"v_accvgpr_read_b32 v199, a3\n" \
M_"a[108:111], a[216:219], a[236:239], a[108:111]\n" \
"v_accvgpr_read_b32 v180, a16\n" \
"v_accvgpr_read_b32 v181, a17\n" \
M_"a[124:127], a[156:159], a[172:175], a[124:127]\n" \
"v_accvgpr_read_b32 v182, a18\n" \
"v_accvgpr_read_b32 v183, a19\n" \
M_"a[124:127], a[220:223], a[236:239], a[124:127]\n" \
C_"v196, v196, v197\n" \
C_"v197, v198, v199\n" \
M_"a[120:123], a[156:159], a[168:171], a[120:123]\n" \
C_"v198, v180, v181\n" \
C_"v199, v182, v183\n" \
"s_add_u32 s88, s41, 0x0\n" \
"s_nop 0\n" \
"buffer_store_dwordx4 v[196:199], v20, s[80:83], s88 offen offset:0 sc1\n" \
M_"a[120:123], a[220:223], a[232:235], a[120:123]\n" \
"v_accvgpr_read_b32 v200, a44\n" \
"v_accvgpr_read_b32 v201, a45\n" \
M_"a[116:119], a[156:159], a[164:167], a[116:119]\n" \
"v_accvgpr_read_b32 v202, a46\n" \
"v_accvgpr_read_b32 v203, a47\n" \
M_"a[116:119], a[220:223], a[228:231], a[116:119]\n" \
"v_accvgpr_read_b32 v180, a60\n" \
"v_accvgpr_read_b32 v181, a61\n" \
M_"a[112:115], a[156:159], a[160:163], a[112:115]\n" \
"v_accvgpr_read_b32 v182, a62\n" \
"v_accvgpr_read_b32 v183, a63\n" \
M_"a[112:115], a[220:223], a[224:227], a[112:115]\n" \
C_"v200, v200, v201\n" \
C_"v201, v202, v203\n" \
"s_waitcnt vmcnt(12) lgkmcnt(0)\n" \
"s_barrier\n" \
"v_xor_b32_e32 v36, 0x4000, v36\n" \
"v_xor_b32_e64 v32, 0x0, v48\n" \
"v_xor_b32_e32 v37, 0x4000, v37\n" \
"v_xor_b32_e64 v33, 0x20, v48\n" \
"v_xor_b32_e32 v38, 0x4000, v38\n" \
"v_xor_b32_e64 v34, 0x40, v48\n" \
"v_xor_b32_e32 v39, 0x4000, v39\n" \
"v_xor_b32_e32 v35, 0x60, v48\n" \
"v_add_u32_e32 v28, s43, v28\n" \
"v_add_u32_e32 v29, s43, v29\n" \
M_"v[116:119], a[144:147], a[176:179], v[116:119]\n" \
"s_add_u32 m0, s40, 0x8000\n" \
"s_add_u32 s85, s39, 0x2000\n" \
M_"v[116:119], a[208:211], a[240:243], v[116:119]\n" \
B_"v20, s[80:83], s85 offen sc1 lds\n" \
M_"v[120:123], a[144:147], a[180:183], v[120:123]\n" \
X_"a[160:161], v32 offset:0\n" \
C_"v202, v180, v181\n" \
M_"v[120:123], a[208:211], a[244:247], v[120:123]\n" \
"s_add_u32 m0, s40, 0x9000\n" \
"s_add_u32 s85, s39, 0x3000\n" \
M_"v[124:127], a[144:147], a[184:187], v[124:127]\n" \
B_"v20, s[80:83], s85 offen sc1 lds\n" \
M_"v[124:127], a[208:211], a[248:251], v[124:127]\n" \
X_"a[162:163], v32 offset:512\n" \
C_"v203, v182, v183\n" \
M_"v[128:131], a[144:147], a[188:191], v[128:131]\n" \
X_"a[224:225], v32 offset:4096\n" \
"v_accvgpr_read_b32 v204, a40\n" \
M_"v[128:131], a[208:211], a[252:255], v[128:131]\n" \
R_"a[144:147], v28 offset:8192\n" \
"v_accvgpr_read_b32 v205, a41\n" \
M_"v[144:147], a[148:151], a[188:191], v[144:147]\n" \
R_"a[208:211], v29 offset:8192\n" \
"v_accvgpr_read_b32 v206, a42\n" \
M_"v[144:147], a[212:215], a[252:255], v[144:147]\n" \
"s_add_u32 m0, s40, 0xb000\n" \
"s_movk_i32 s84, 0x80\n" \
M_"v[140:143], a[148:151], a[184:187], v[140:143]\n" \
B_"v21, s[76:79], s84 offen offset:0 lds\n" \
M_"v[140:143], a[212:215], a[248:251], v[140:143]\n" \
"s_add_u32 m0, s30, 0x8000\n" \
"s_movk_i32 s84, 0x80\n" \
M_"v[136:139], a[148:151], a[180:183], v[136:139]\n" \
B_"v24, s[76:79], s84 offen offset:0 lds\n" \
M_"v[136:139], a[212:215], a[244:247], v[136:139]\n" \
X_"a[226:227], v32 offset:4608\n" \
"v_accvgpr_read_b32 v207, a43\n" \
M_"v[132:135], a[148:151], a[176:179], v[132:135]\n" \
X_"a[164:165], v33 offset:0\n" \
"v_accvgpr_read_b32 v180, a56\n" \
M_"v[132:135], a[212:215], a[240:243], v[132:135]\n" \
R_"a[148:151], v28 offset:10240\n" \
"v_accvgpr_read_b32 v181, a57\n" \
M_"v[148:151], a[152:155], a[176:179], v[148:151]\n" \
R_"a[212:215], v29 offset:10240\n" \
"v_accvgpr_read_b32 v182, a58\n" \
M_"v[148:151], a[216:219], a[240:243], v[148:151]\n" \
"s_movk_i32 s84, 0x2080\n" \
M_"v[152:155], a[152:155], a[180:183], v[152:155]\n" \
B_"v25, s[76:79], s84 offen offset:1024 lds\n" \
M_"v[152:155], a[216:219], a[244:247], v[152:155]\n" \
"s_movk_i32 s84, 0x4080\n" \
M_"v[156:159], a[152:155], a[184:187], v[156:159]\n" \
B_"v24, s[76:79], s84 offen offset:2048 lds\n" \
M_"v[156:159], a[216:219], a[248:251], v[156:159]\n" \
X_"a[166:167], v33 offset:512\n" \
"v_accvgpr_read_b32 v183, a59\n" \
M_"v[160:163], a[152:155], a[188:191], v[160:163]\n" \
X_"a[228:229], v33 offset:4096\n" \
C_"v204, v204, v205\n" \
M_"v[160:163], a[216:219], a[252:255], v[160:163]\n" \
R_"a[152:155], v28 offset:12288\n" \
C_"v205, v206, v207\n" \
M_"v[176:179], a[156:159], a[188:191], v[176:179]\n" \
R_"a[216:219], v29 offset:12288\n" \
C_"v206, v180, v181\n" \
M_"v[176:179], a[220:223], a[252:255], v[176:179]\n" \
"s_movk_i32 s84, 0x6080\n" \
M_"v[172:175], a[156:159], a[184:187], v[172:175]\n" \
B_"v25, s[76:79], s84 offen offset:3072 lds\n" \
M_"v[172:175], a[220:223], a[248:251], v[172:175]\n" \
"s_add_u32 m0, s31, 0x8000\n" \
"s_mov_b32 s84, 0x80000\n" \
M_"v[168:171], a[156:159], a[180:183], v[168:171]\n" \
B_"v26, s[68:71], s84 offen offset:0 lds\n" \
M_"v[168:171], a[220:223], a[244:247], v[168:171]\n" \
"s_addk_i32 s84, 0x7c00\n" \
M_"v[164:167], a[156:159], a[176:179], v[164:167]\n" \
B_"v26, s[68:71], s84 offen offset:1024 lds\n" \
M_"v[164:167], a[220:223], a[240:243], v[164:167]\n" \
R_"a[156:159], v28 offset:14336\n" \
C_"v207, v182, v183\n" \
M_"v[84:87], a[136:139], a[176:179], v[84:87]\n" \
R_"a[220:223], v29 offset:14336\n" \
"v_accvgpr_read_b32 v208, a36\n" \
M_"v[84:87], a[200:203], a[240:243], v[84:87]\n" \
"s_addk_i32 s84, 0x7c00\n" \
M_"v[88:91], a[136:139], a[180:183], v[88:91]\n" \
B_"v27, s[68:71], s84 offen offset:2048 lds\n" \
M_"v[88:91], a[200:203], a[244:247], v[88:91]\n" \
X_"a[230:231], v33 offset:4608\n" \
"v_accvgpr_read_b32 v209, a37\n" \
M_"v[92:95], a[136:139], a[184:187], v[92:95]\n" \
"s_addk_i32 s84, 0x7c00\n" \
M_"v[92:95], a[200:203], a[248:251], v[92:95]\n" \
B_"v27, s[68:71], s84 offen offset:3072 lds\n" \
M_"v[96:99], a[136:139], a[188:191], v[96:99]\n" \
X_"a[172:173], v35 offset:0\n" \
"v_accvgpr_read_b32 v210, a38\n" \
M_"v[96:99], a[200:203], a[252:255], v[96:99]\n" \
R_"a[136:139], v28 offset:4096\n" \
"v_accvgpr_read_b32 v211, a39\n" \
M_"v[112:115], a[140:143], a[188:191], v[112:115]\n" \
R_"a[200:203], v29 offset:4096\n" \
"v_accvgpr_read_b32 v180, a52\n" \
M_"v[112:115], a[204:207], a[252:255], v[112:115]\n" \
"s_add_u32 m0, s30, 0x9000\n" \
"s_mov_b32 s84, 0x9080\n" \
M_"v[108:111], a[140:143], a[184:187], v[108:111]\n" \
B_"v24, s[76:79], s84 offen offset:0 lds\n" \
M_"v[108:111], a[204:207], a[248:251], v[108:111]\n" \
X_"a[174:175], v35 offset:512\n" \
"v_accvgpr_read_b32 v181, a53\n" \
M_"v[104:107], a[140:143], a[180:183], v[104:107]\n" \
"s_addk_i32 s84, 0x2000\n" \
"s_nop 0\n" \
M_"v[104:107], a[204:207], a[244:247], v[104:107]\n" \
B_"v25, s[76:79], s84 offen offset:1024 lds\n" \
M_"v[100:103], a[140:143], a[176:179], v[100:103]\n" \
X_"a[236:237], v35 offset:4096\n" \
"v_accvgpr_read_b32 v182, a54\n" \
M_"v[100:103], a[204:207], a[240:243], v[100:103]\n" \
R_"a[140:143], v28 offset:6144\n" \
"v_accvgpr_read_b32 v183, a55\n" \
M_"v[52:55], a[128:131], a[176:179], v[52:55]\n" \
R_"a[204:207], v29 offset:6144\n" \
C_"v208, v208, v209\n" \
M_"v[52:55], a[192:195], a[240:243], v[52:55]\n" \
X_"a[238:239], v35 offset:4608\n" \
C_"v209, v210, v211\n" \
M_"v[56:59], a[128:131], a[180:183], v[56:59]\n" \
X_"a[168:169], v34 offset:0\n" \
C_"v210, v180, v181\n" \
M_"v[56:59], a[192:195], a[244:247], v[56:59]\n" \
X_"a[170:171], v34 offset:512\n" \
C_"v211, v182, v183\n" \
M_"v[60:63], a[128:131], a[184:187], v[60:63]\n" \
X_"a[232:233], v34 offset:4096\n" \
"v_accvgpr_read_b32 v212, a32\n" \
M_"v[60:63], a[192:195], a[248:251], v[60:63]\n" \
X_"a[234:235], v34 offset:4608\n" \
"v_accvgpr_read_b32 v213, a33\n" \
M_"v[64:67], a[128:131], a[188:191], v[64:67]\n" \
"v_accvgpr_read_b32 v214, a34\n" \
"v_accvgpr_read_b32 v215, a35\n" \
M_"v[64:67], a[192:195], a[252:255], v[64:67]\n" \
R_"a[128:131], v28 offset:0\n" \
"v_accvgpr_read_b32 v180, a48\n" \
M_"v[80:83], a[132:135], a[188:191], v[80:83]\n" \
R_"a[192:195], v29 offset:0\n" \
"v_accvgpr_read_b32 v181, a49\n" \
M_"v[80:83], a[196:199], a[252:255], v[80:83]\n" \
"v_accvgpr_read_b32 v182, a50\n" \
"v_accvgpr_read_b32 v183, a51\n" \
M_"v[76:79], a[132:135], a[184:187], v[76:79]\n" \
C_"v212, v212, v213\n" \
C_"v213, v214, v215\n" \
M_"v[76:79], a[196:199], a[248:251], v[76:79]\n" \
C_"v214, v180, v181\n" \
C_"v215, v182, v183\n" \
M_"v[72:75], a[132:135], a[180:183], v[72:75]\n" \
"v_accvgpr_read_b32 v216, a68\n" \
"v_accvgpr_read_b32 v217, a69\n" \
M_"v[72:75], a[196:199], a[244:247], v[72:75]\n" \
"v_accvgpr_read_b32 v218, a70\n" \
"v_accvgpr_read_b32 v219, a71\n" \
M_"v[68:71], a[132:135], a[176:179], v[68:71]\n" \
"v_accvgpr_read_b32 v180, a84\n" \
"v_accvgpr_read_b32 v181, a85\n" \
M_"v[68:71], a[196:199], a[240:243], v[68:71]\n" \
R_"a[132:135], v28 offset:2048\n" \
R_"a[196:199], v29 offset:2048\n" \
"s_waitcnt lgkmcnt(15)\n" \
M_"a[0:3], a[160:163], a[144:147], 0\n" \
"s_addk_i32 s84, 0x2000\n" \
"v_accvgpr_read_b32 v182, a86\n" \
M_"a[0:3], a[224:227], a[208:211], a[0:3]\n" \
B_"v24, s[76:79], s84 offen offset:2048 lds\n" \
"v_accvgpr_read_b32 v183, a87\n" \
M_"a[4:7], a[164:167], a[144:147], 0\n" \
X_"a[176:177], v36 offset:0\n" \
C_"v216, v216, v217\n" \
M_"a[4:7], a[228:231], a[208:211], a[4:7]\n" \
"s_addk_i32 s84, 0x2000\n" \
C_"v217, v218, v219\n" \
"s_waitcnt lgkmcnt(13)\n" \
M_"a[12:15], a[172:175], a[144:147], 0\n" \
B_"v25, s[76:79], s84 offen offset:3072 lds\n" \
C_"v218, v180, v181\n" \
"s_waitcnt lgkmcnt(9)\n" \
M_"a[12:15], a[236:239], a[208:211], a[12:15]\n" \
X_"a[178:179], v36 offset:1024\n" \
C_"v219, v182, v183\n" \
"s_waitcnt lgkmcnt(8)\n" \
M_"a[8:11], a[144:147], a[168:171], 0\n" \
X_"a[240:241], v36 offset:8192\n" \
"v_accvgpr_read_b32 v220, a64\n" \
"s_waitcnt lgkmcnt(7)\n" \
"s_nop 0\n" \
M_"a[8:11], a[208:211], a[232:235], a[8:11]\n" \
X_"a[242:243], v36 offset:9216\n" \
"v_accvgpr_read_b32 v221, a65\n" \
M_"a[24:27], a[148:151], a[168:171], 0\n" \
X_"a[180:181], v37 offset:0\n" \
".fill (L_pb + 61152 - .) / 4, 4, 0xBF800000\n" \
"L_pp15:\n" \
"s_cbranch_execz L_pret\n" \
"s_nop 0\n" \
"v_accvgpr_read_b32 v222, a66\n" \
M_"a[24:27], a[212:215], a[232:235], a[24:27]\n" \
X_"a[182:183], v37 offset:1024\n" \
"v_accvgpr_read_b32 v223, a67\n" \
M_"a[28:31], a[172:175], a[148:151], 0\n" \
X_"a[244:245], v37 offset:8192\n" \
"v_accvgpr_read_b32 v180, a80\n" \
M_"a[28:31], a[236:239], a[212:215], a[28:31]\n" \
"s_add_u32 s89, s41, 0x2000\n" \
"buffer_store_dwordx4 v[200:203], v20, s[80:83], s89 offen offset:3072 sc1\n" \
M_"a[20:23], a[164:167], a[148:151], 0\n" \
X_"a[246:247], v37 offset:9216\n" \
"v_accvgpr_read_b32 v181, a81\n" \
M_"a[20:23], a[228:231], a[212:215], a[20:23]\n" \
X_"a[184:185], v38 offset:0\n" \
"v_accvgpr_read_b32 v182, a82\n" \
M_"a[16:19], a[160:163], a[148:151], 0\n" \
X_"a[186:187], v38 offset:1024\n" \
"v_accvgpr_read_b32 v183, a83\n" \
M_"a[16:19], a[224:227], a[212:215], a[16:19]\n" \
C_"v220, v220, v221\n" \
C_"v221, v222, v223\n" \
C_"v222, v180, v181\n" \
M_"a[32:35], a[160:163], a[152:155], 0\n" \
"s_add_u32 s88, s41, 0x2000\n" \
"buffer_store_dwordx4 v[204:207], v20, s[80:83], s88 offen offset:2048 sc1\n" \
M_"a[32:35], a[224:227], a[216:219], a[32:35]\n" \
X_"a[248:249], v38 offset:8192\n" \
C_"v223, v182, v183\n" \
M_"a[36:39], a[164:167], a[152:155], 0\n" \
X_"a[250:251], v38 offset:9216\n" \
C_"v224, v128, v129\n" \
M_"a[36:39], a[228:231], a[216:219], a[36:39]\n" \
X_"a[188:189], v39 offset:0\n" \
C_"v225, v130, v131\n" \
M_"a[44:47], a[172:175], a[152:155], 0\n" \
C_"v226, v144, v145\n" \
C_"v227, v146, v147\n" \
C_"v228, v124, v125\n" \
M_"a[44:47], a[236:239], a[216:219], a[44:47]\n" \
X_"a[190:191], v39 offset:1024\n" \
C_"v229, v126, v127\n" \
M_"a[40:43], a[152:155], a[168:171], 0\n" \
"s_add_u32 s89, s41, 0x2000\n" \
"buffer_store_dwordx4 v[208:211], v20, s[80:83], s89 offen offset:1024 sc1\n" \
M_"a[40:43], a[216:219], a[232:235], a[40:43]\n" \
X_"a[252:253], v39 offset:8192\n" \
C_"v230, v140, v141\n" \
M_"a[56:59], a[156:159], a[168:171], 0\n" \
X_"a[254:255], v39 offset:9216\n" \
C_"v231, v142, v143\n" \
M_"a[56:59], a[220:223], a[232:235], a[56:59]\n" \
C_"v232, v120, v121\n" \
C_"v233, v122, v123\n" \
C_"v234, v136, v137\n" \
M_"a[60:63], a[172:175], a[156:159], 0\n" \
C_"v235, v138, v139\n" \
C_"v236, v116, v117\n" \
C_"v237, v118, v119\n" \
M_"a[60:63], a[236:239], a[220:223], a[60:63]\n" \
"s_add_u32 s88, s41, 0x2000\n" \
"buffer_store_dwordx4 v[212:215], v20, s[80:83], s88 offen offset:0 sc1\n" \
M_"a[52:55], a[164:167], a[156:159], 0\n" \
C_"v238, v132, v133\n" \
C_"v239, v134, v135\n" \
C_"v240, v160, v161\n" \
M_"a[52:55], a[228:231], a[220:223], a[52:55]\n" \
C_"v241, v162, v163\n" \
C_"v242, v176, v177\n" \
C_"v243, v178, v179\n" \
M_"a[48:51], a[160:163], a[156:159], 0\n" \
C_"v244, v156, v157\n" \
C_"v245, v158, v159\n" \
C_"v246, v172, v173\n" \
M_"a[48:51], a[224:227], a[220:223], a[48:51]\n" \
C_"v247, v174, v175\n" \
C_"v184, v152, v153\n" \
C_"v185, v154, v155\n" \
M_"a[64:67], a[164:167], a[140:143], 0\n" \
C_"v186, v168, v169\n" \
C_"v187, v170, v171\n" \
C_"v188, v148, v149\n" \
M_"a[64:67], a[228:231], a[204:207], a[64:67]\n" \
C_"v189, v150, v151\n" \
C_"v190, v164, v165\n" \
C_"v191, v166, v167\n" \
M_"a[80:83], a[172:175], a[140:143], 0\n" \
C_"v192, v96, v97\n" \
C_"v193, v98, v99\n" \
C_"v194, v112, v113\n" \
M_"a[80:83], a[236:239], a[204:207], a[80:83]\n" \
C_"v195, v114, v115\n" \
C_"v196, v92, v93\n" \
C_"v197, v94, v95\n" \
M_"a[68:71], a[140:143], a[168:171], 0\n" \
C_"v198, v108, v109\n" \
C_"v199, v110, v111\n" \
C_"v252, v88, v89\n" \
M_"a[68:71], a[204:207], a[232:235], a[68:71]\n" \
C_"v253, v90, v91\n" \
C_"v254, v104, v105\n" \
C_"v255, v106, v107\n" \
"s_waitcnt vmcnt(4) lgkmcnt(0)\n" \
"s_barrier\n" \
M_"v[116:119], a[176:179], a[144:147], 0\n" \
"s_add_u32 m0, s40, 0x0\n" \
"s_nop 0\n" \
"s_add_u32 s85, s39, 0x4000\n" \
"v_accvgpr_write_b32 a84, v252\n" \
M_"v[116:119], a[240:243], a[208:211], v[116:119]\n" \
B_"v20, s[80:83], s85 offen sc1 lds\n" \
"v_accvgpr_write_b32 a85, v253\n" \
M_"v[120:123], a[180:183], a[144:147], 0\n" \
X_"a[160:161], v32 offset:32768\n" \
"v_accvgpr_write_b32 a86, v254\n" \
M_"v[120:123], a[244:247], a[208:211], v[120:123]\n" \
X_"a[162:163], v32 offset:33280\n" \
"v_accvgpr_write_b32 a87, v255\n" \
M_"v[124:127], a[184:187], a[144:147], 0\n" \
"s_add_u32 m0, s40, 0x1000\n" \
"s_add_u32 s85, s39, 0x5000\n" \
C_"v252, v84, v85\n" \
M_"v[124:127], a[248:251], a[208:211], v[124:127]\n" \
B_"v20, s[80:83], s85 offen sc1 lds\n" \
C_"v253, v86, v87\n" \
M_"v[128:131], a[188:191], a[144:147], 0\n" \
X_"a[224:225], v32 offset:36864\n" \
C_"v254, v100, v101\n" \
M_"v[128:131], a[252:255], a[208:211], v[128:131]\n" \
R_"a[144:147], v28 offset:40960\n" \
C_"v255, v102, v103\n" \
M_"v[144:147], a[188:191], a[148:151], 0\n" \
R_"a[208:211], v29 offset:40960\n" \
C_"v200, v64, v65\n" \
M_"v[144:147], a[252:255], a[212:215], v[144:147]\n" \
"s_add_u32 m0, s40, 0x3000\n" \
"s_movk_i32 s84, 0x100\n" \
"s_nop 0\n" \
C_"v201, v66, v67\n" \
M_"v[140:143], a[184:187], a[148:151], 0\n" \
B_"v21, s[76:79], s84 offen offset:0 lds\n" \
C_"v202, v80, v81\n" \
M_"v[140:143], a[248:251], a[212:215], v[140:143]\n" \
X_"a[226:227], v32 offset:37376\n" \
C_"v203, v82, v83\n" \
M_"v[136:139], a[180:183], a[148:151], 0\n" \
C_"v204, v60, v61\n" \
C_"v205, v62, v63\n" \
C_"v206, v76, v77\n" \
M_"v[136:139], a[244:247], a[212:215], v[136:139]\n" \
X_"a[164:165], v33 offset:32768\n" \
C_"v207, v78, v79\n" \
M_"v[132:135], a[176:179], a[148:151], 0\n" \
C_"v208, v56, v57\n" \
C_"v209, v58, v59\n" \
C_"v210, v72, v73\n" \
M_"v[132:135], a[240:243], a[212:215], v[132:135]\n" \
R_"a[148:151], v28 offset:43008\n" \
C_"v211, v74, v75\n" \
M_"v[148:151], a[176:179], a[152:155], 0\n" \
R_"a[212:215], v29 offset:43008\n" \
C_"v212, v52, v53\n" \
M_"v[148:151], a[240:243], a[216:219], v[148:151]\n" \
"s_add_u32 m0, s31, 0x0\n" \
"s_mov_b32 s84, 0x100000\n" \
C_"v213, v54, v55\n" \
M_"v[152:155], a[180:183], a[152:155], 0\n" \
B_"v26, s[68:71], s84 offen offset:0 lds\n" \
C_"v214, v68, v69\n" \
M_"v[152:155], a[244:247], a[216:219], v[152:155]\n" \
"s_addk_i32 s84, 0x7c00\n" \
C_"v215, v70, v71\n" \
M_"v[156:159], a[184:187], a[152:155], 0\n" \
B_"v26, s[68:71], s84 offen offset:1024 lds\n" \
M_"v[156:159], a[248:251], a[216:219], v[156:159]\n" \
X_"a[166:167], v33 offset:33280\n" \
M_"v[160:163], a[188:191], a[152:155], 0\n" \
X_"a[228:229], v33 offset:36864\n" \
M_"v[160:163], a[252:255], a[216:219], v[160:163]\n" \
R_"a[152:155], v28 offset:45056\n" \
M_"v[176:179], a[188:191], a[156:159], 0\n" \
R_"a[216:219], v29 offset:45056\n" \
M_"v[176:179], a[252:255], a[220:223], v[176:179]\n" \
"s_addk_i32 s84, 0x7c00\n" \
M_"v[172:175], a[184:187], a[156:159], 0\n" \
B_"v27, s[68:71], s84 offen offset:2048 lds\n" \
M_"v[172:175], a[248:251], a[220:223], v[172:175]\n" \
X_"a[230:231], v33 offset:37376\n" \
M_"v[168:171], a[180:183], a[156:159], 0\n" \
"s_addk_i32 s84, 0x7c00\n" \
M_"v[168:171], a[244:247], a[220:223], v[168:171]\n" \
B_"v27, s[68:71], s84 offen offset:3072 lds\n" \
M_"v[164:167], a[176:179], a[156:159], 0\n" \
X_"a[172:173], v35 offset:32768\n" \
M_"v[164:167], a[240:243], a[220:223], v[164:167]\n" \
R_"a[156:159], v28 offset:47104\n" \
M_"v[84:87], a[176:179], a[136:139], 0\n" \
R_"a[220:223], v29 offset:47104\n" \
M_"v[84:87], a[240:243], a[200:203], v[84:87]\n" \
"s_add_u32 s89, s41, 0x3000\n" \
"buffer_store_dwordx4 v[192:195], v20, s[80:83], s89 offen offset:3072 sc1\n" \
M_"v[88:91], a[180:183], a[136:139], 0\n" \
X_"a[174:175], v35 offset:33280\n" \
M_"v[88:91], a[244:247], a[200:203], v[88:91]\n" \
X_"a[236:237], v35 offset:36864\n" \
M_"v[92:95], a[184:187], a[136:139], 0\n" \
X_"a[238:239], v35 offset:37376\n" \
M_"v[92:95], a[248:251], a[200:203], v[92:95]\n" \
"s_add_u32 m0, s30, 0x0\n" \
"s_movk_i32 s84, 0x180\n" \
M_"v[96:99], a[188:191], a[136:139], 0\n" \
B_"v24, s[76:79], s84 offen offset:0 lds\n" \
M_"v[96:99], a[252:255], a[200:203], v[96:99]\n" \
R_"a[136:139], v28 offset:36864\n" \
M_"v[112:115], a[188:191], a[140:143], 0\n" \
R_"a[200:203], v29 offset:36864\n" \
M_"v[112:115], a[252:255], a[204:207], v[112:115]\n" \
X_"a[168:169], v34 offset:32768\n" \
M_"v[108:111], a[184:187], a[140:143], 0\n" \
"s_movk_i32 s84, 0x2180\n" \
"s_nop 0\n" \
M_"v[108:111], a[248:251], a[204:207], v[108:111]\n" \
B_"v25, s[76:79], s84 offen offset:1024 lds\n" \
M_"v[104:107], a[180:183], a[140:143], 0\n" \
X_"a[170:171], v34 offset:33280\n" \
M_"v[104:107], a[244:247], a[204:207], v[104:107]\n" \
X_"a[232:233], v34 offset:36864\n" \
M_"v[100:103], a[176:179], a[140:143], 0\n" \
M_"v[100:103], a[240:243], a[204:207], v[100:103]\n" \
R_"a[140:143], v28 offset:38912\n" \
M_"v[52:55], a[176:179], a[128:131], 0\n" \
R_"a[204:207], v29 offset:38912\n" \
M_"v[52:55], a[240:243], a[192:195], v[52:55]\n" \
"s_movk_i32 s84, 0x4180\n" \
M_"v[56:59], a[180:183], a[128:131], 0\n" \
B_"v24, s[76:79], s84 offen offset:2048 lds\n" \
M_"v[56:59], a[244:247], a[192:195], v[56:59]\n" \
"s_movk_i32 s84, 0x6180\n" \
M_"v[60:63], a[184:187], a[128:131], 0\n" \
B_"v25, s[76:79], s84 offen offset:3072 lds\n" \
M_"v[60:63], a[248:251], a[192:195], v[60:63]\n" \
X_"a[234:235], v34 offset:37376\n" \
M_"v[64:67], a[188:191], a[128:131], 0\n" \
M_"v[64:67], a[252:255], a[192:195], v[64:67]\n" \
R_"a[128:131], v28 offset:32768\n" \
M_"v[80:83], a[188:191], a[132:135], 0\n" \
R_"a[192:195], v29 offset:32768\n" \
M_"v[80:83], a[252:255], a[196:199], v[80:83]\n" \
M_"v[76:79], a[184:187], a[132:135], 0\n" \
M_"v[76:79], a[248:251], a[196:199], v[76:79]\n" \
M_"v[72:75], a[180:183], a[132:135], 0\n" \
M_"v[72:75], a[244:247], a[196:199], v[72:75]\n" \
M_"v[68:71], a[176:179], a[132:135], 0\n" \
M_"v[68:71], a[240:243], a[196:199], v[68:71]\n" \
R_"a[132:135], v28 offset:34816\n" \
R_"a[196:199], v29 offset:34816\n" \
"s_waitcnt lgkmcnt(15)\n" \
M_"a[0:3], a[160:163], a[144:147], a[0:3]\n" \
"s_add_u32 m0, s30, 0x1000\n" \
"s_addk_i32 s84, 0x3000\n" \
M_"a[0:3], a[224:227], a[208:211], a[0:3]\n" \
B_"v24, s[76:79], s84 offen offset:0 lds\n" \
M_"a[4:7], a[164:167], a[144:147], a[4:7]\n" \
X_"a[176:177], v36 offset:32768\n" \
"v_mov_b32_e64 v11, 0\n" \
M_"a[4:7], a[228:231], a[208:211], a[4:7]\n" \
X_"a[178:179], v36 offset:33792\n" \
"s_and_b32 s5, s2, 7\n" \
"s_waitcnt lgkmcnt(15)\n" \
M_"a[12:15], a[172:175], a[144:147], a[12:15]\n" \
X_"a[240:241], v36 offset:40960\n" \
"s_lshl_b32 s6, s3, 1\n" \
"s_waitcnt lgkmcnt(15)\n" \
M_"a[12:15], a[236:239], a[208:211], a[12:15]\n" \
"s_add_u32 s88, s41, 0x3000\n" \
"buffer_store_dwordx4 v[196:199], v20, s[80:83], s88 offen offset:2048 sc1\n" \
"s_waitcnt lgkmcnt(11)\n" \
M_"a[8:11], a[144:147], a[168:171], a[8:11]\n" \
X_"a[242:243], v36 offset:41984\n" \
"s_lshl_b32 s4, s5, 2\n" \
"s_waitcnt lgkmcnt(8)\n" \
M_"a[8:11], a[208:211], a[232:235], a[8:11]\n" \
X_"a[180:181], v37 offset:32768\n" \
"s_bfe_u32 s7, s2, 0x20003\n" \
M_"a[24:27], a[148:151], a[168:171], a[24:27]\n" \
X_"a[182:183], v37 offset:33792\n" \
"s_or_b32 s12, s4, s7\n" \
M_"a[24:27], a[212:215], a[232:235], a[24:27]\n" \
X_"a[244:245], v37 offset:40960\n" \
"s_and_b32 s6, s6, 4\n" \
M_"a[28:31], a[172:175], a[148:151], a[28:31]\n" \
"s_and_b32 s8, s3, 1\n" \
"s_or_b32 s8, s6, s8\n" \
M_"a[28:31], a[236:239], a[212:215], a[28:31]\n" \
"s_addk_i32 s84, 0x2000\n" \
M_"a[20:23], a[164:167], a[148:151], a[20:23]\n" \
B_"v25, s[76:79], s84 offen offset:1024 lds\n" \
M_"a[20:23], a[228:231], a[212:215], a[20:23]\n" \
X_"a[246:247], v37 offset:41984\n" \
"s_lshl_b32 s6, s12, 4\n" \
M_"a[16:19], a[160:163], a[148:151], a[16:19]\n" \
X_"a[184:185], v38 offset:32768\n" \
"s_lshl_b32 s9, s3, 2\n" \
M_"a[16:19], a[224:227], a[212:215], a[16:19]\n" \
X_"a[186:187], v38 offset:33792\n" \
"s_or_b32 s6, s6, s9\n" \
M_"a[32:35], a[160:163], a[152:155], a[32:35]\n" \
"s_addk_i32 s84, 0x2000\n" \
M_"a[32:35], a[224:227], a[216:219], a[32:35]\n" \
B_"v24, s[76:79], s84 offen offset:2048 lds\n" \
M_"a[36:39], a[164:167], a[152:155], a[36:39]\n" \
X_"a[248:249], v38 offset:40960\n" \
"s_lshr_b32 s9, s2, 5\n" \
M_"a[36:39], a[228:231], a[216:219], a[36:39]\n" \
X_"a[250:251], v38 offset:41984\n" \
"s_mul_i32 s10, s9, 0xa00\n" \
M_"a[44:47], a[172:175], a[152:155], a[44:47]\n" \
X_"a[188:189], v39 offset:32768\n" \
"s_add_i32 s10, s10, 0x5bc1000\n" \
M_"a[44:47], a[236:239], a[216:219], a[44:47]\n" \
X_"a[190:191], v39 offset:33792\n" \
"s_or_b32 s6, s6, s10\n" \
M_"a[40:43], a[152:155], a[168:171], a[40:43]\n" \
"s_add_u32 s89, s41, 0x3000\n" \
"buffer_store_dwordx4 a[84:87], v20, s[80:83], s89 offen offset:1024 sc1\n" \
M_"a[40:43], a[216:219], a[232:235], a[40:43]\n" \
X_"a[252:253], v39 offset:40960\n" \
"s_lshl_b32 s13, s9, 3\n" \
M_"a[56:59], a[156:159], a[168:171], a[56:59]\n" \
X_"a[254:255], v39 offset:41984\n" \
"v_writelane_b32 v11, s6, 0\n" \
M_"a[56:59], a[220:223], a[232:235], a[56:59]\n" \
"s_or_b32 s6, s13, s5\n" \
"v_writelane_b32 v11, s10, 1\n" \
M_"a[60:63], a[172:175], a[156:159], a[60:63]\n" \
"s_lshl_b32 s6, s6, 10\n" \
"s_lshl_b32 s10, s3, 8\n" \
M_"a[60:63], a[236:239], a[220:223], a[60:63]\n" \
"s_addk_i32 s84, 0x2000\n" \
M_"a[52:55], a[164:167], a[156:159], a[52:55]\n" \
B_"v25, s[76:79], s84 offen offset:3072 lds\n" \
M_"a[52:55], a[228:231], a[220:223], a[52:55]\n" \
"s_or_b32 s6, s6, s10\n" \
"s_or_b32 s10, s6, 0x5c00200\n" \
M_"a[48:51], a[160:163], a[156:159], a[48:51]\n" \
"v_writelane_b32 v11, s10, 2\n" \
"s_lshl_b32 s10, s7, 2\n" \
M_"a[48:51], a[224:227], a[220:223], a[48:51]\n" \
"s_or_b32 s11, s10, s6\n" \
"s_or_b32 s11, s11, 0x5c00000\n" \
M_"a[64:67], a[164:167], a[140:143], a[64:67]\n" \
"v_writelane_b32 v11, s11, 3\n" \
"s_or_b32 s6, s6, 0x5c00080\n" \
M_"a[64:67], a[228:231], a[204:207], a[64:67]\n" \
"v_writelane_b32 v11, s6, 4\n" \
"s_or_b32 s6, s6, s10\n" \
M_"a[80:83], a[172:175], a[140:143], a[80:83]\n" \
"s_add_i32 s10, s3, s2\n" \
"v_writelane_b32 v11, s6, 5\n" \
M_"a[80:83], a[236:239], a[204:207], a[80:83]\n" \
"s_mov_b32 s6, 2.0\n" \
"s_mov_b32 s11, 0x5bc0000\n" \
M_"a[68:71], a[140:143], a[168:171], a[68:71]\n" \
"s_cmp_lg_u32 s10, 0\n" \
"s_cselect_b32 s10, s6, s11\n" \
"s_waitcnt vmcnt(11) lgkmcnt(0)\n" \
M_"a[68:71], a[204:207], a[232:235], a[68:71]\n" \
"s_barrier\n" \
"v_xor_b32_e32 v28, 0x20000, v28\n" \
"v_xor_b32_e32 v29, 0x20000, v29\n" \
M_"v[116:119], a[176:179], a[144:147], v[116:119]\n" \
"s_add_u32 m0, s40, 0x8000\n" \
"s_add_u32 s85, s39, 0x6000\n" \
M_"v[116:119], a[240:243], a[208:211], v[116:119]\n" \
B_"v20, s[80:83], s85 offen sc1 lds\n" \
M_"v[120:123], a[180:183], a[144:147], v[120:123]\n" \
X_"a[160:161], v32 offset:0\n" \
"s_lshr_b32 s14, s2, 3\n" \
M_"v[120:123], a[244:247], a[208:211], v[120:123]\n" \
X_"a[162:163], v32 offset:512\n" \
"v_writelane_b32 v11, s10, 57\n" \
M_"v[124:127], a[184:187], a[144:147], v[124:127]\n" \
"s_add_u32 m0, s40, 0x9000\n" \
"s_add_u32 s85, s39, 0x7000\n" \
M_"v[124:127], a[248:251], a[208:211], v[124:127]\n" \
B_"v20, s[80:83], s85 offen sc1 lds\n" \
M_"v[128:131], a[188:191], a[144:147], v[128:131]\n" \
X_"a[224:225], v32 offset:4096\n" \
"s_lshl_b32 s10, s5, 5\n" \
M_"v[128:131], a[252:255], a[208:211], v[128:131]\n" \
R_"a[144:147], v28 offset:8192\n" \
"s_min_u32 s15, s3, 2\n" \
M_"v[144:147], a[188:191], a[148:151], v[144:147]\n" \
R_"a[208:211], v29 offset:8192\n" \
"s_mul_i32 s11, s7, 0x120\n" \
M_"v[144:147], a[252:255], a[212:215], v[144:147]\n" \
"s_add_u32 m0, s40, 0xb000\n" \
"s_movk_i32 s84, 0x180\n" \
M_"v[140:143], a[184:187], a[148:151], v[140:143]\n" \
B_"v21, s[76:79], s84 offen offset:0 lds\n" \
M_"v[140:143], a[248:251], a[212:215], v[140:143]\n" \
X_"a[226:227], v32 offset:4608\n" \
M_"v[136:139], a[180:183], a[148:151], v[136:139]\n" \
"s_add_u32 s88, s41, 0x3000\n" \
"buffer_store_dwordx4 v[252:255], v20, s[80:83], s88 offen offset:0 sc1\n" \
M_"v[136:139], a[244:247], a[212:215], v[136:139]\n" \
X_"a[164:165], v33 offset:0\n" \
M_"v[132:135], a[176:179], a[148:151], v[132:135]\n" \
"s_add_u32 s16, s22, s11\n" \
"s_addc_u32 s17, s23, 0\n" \
"s_nop 0\n" \
M_"v[132:135], a[240:243], a[212:215], v[132:135]\n" \
R_"a[148:151], v28 offset:10240\n" \
M_"v[148:151], a[176:179], a[152:155], v[148:151]\n" \
R_"a[212:215], v29 offset:10240\n" \
M_"v[148:151], a[240:243], a[216:219], v[148:151]\n" \
"s_add_u32 m0, s31, 0x8000\n" \
"s_mov_b32 s84, 0x180000\n" \
M_"v[152:155], a[180:183], a[152:155], v[152:155]\n" \
B_"v26, s[68:71], s84 offen offset:0 lds\n" \
M_"v[152:155], a[244:247], a[216:219], v[152:155]\n" \
X_"a[166:167], v33 offset:512\n" \
M_"v[156:159], a[184:187], a[152:155], v[156:159]\n" \
X_"a[228:229], v33 offset:4096\n" \
M_"v[156:159], a[248:251], a[216:219], v[156:159]\n" \
"s_addk_i32 s84, 0x7c00\n" \
M_"v[160:163], a[188:191], a[152:155], v[160:163]\n" \
B_"v26, s[68:71], s84 offen offset:1024 lds\n" \
M_"v[160:163], a[252:255], a[216:219], v[160:163]\n" \
R_"a[152:155], v28 offset:12288\n" \
M_"v[176:179], a[188:191], a[156:159], v[176:179]\n" \
R_"a[216:219], v29 offset:12288\n" \
M_"v[176:179], a[252:255], a[220:223], v[176:179]\n" \
"s_addk_i32 s84, 0x7c00\n" \
M_"v[172:175], a[184:187], a[156:159], v[172:175]\n" \
B_"v27, s[68:71], s84 offen offset:2048 lds\n" \
M_"v[172:175], a[248:251], a[220:223], v[172:175]\n" \
X_"a[230:231], v33 offset:4608\n" \
M_"v[168:171], a[180:183], a[156:159], v[168:171]\n" \
X_"a[172:173], v35 offset:0\n" \
M_"v[168:171], a[244:247], a[220:223], v[168:171]\n" \
X_"a[174:175], v35 offset:512\n" \
M_"v[164:167], a[176:179], a[156:159], v[164:167]\n" \
"s_add_u32 s18, s24, s11\n" \
"v_writelane_b32 v11, s16, 9\n" \
"s_addc_u32 s19, s25, 0\n" \
M_"v[164:167], a[240:243], a[220:223], v[164:167]\n" \
R_"a[156:159], v28 offset:14336\n" \
"s_and_b32 s20, s17, 0xffff\n" \
M_"v[84:87], a[176:179], a[136:139], v[84:87]\n" \
R_"a[220:223], v29 offset:14336\n" \
"v_writelane_b32 v11, s20, 10\n" \
M_"v[84:87], a[240:243], a[200:203], v[84:87]\n" \
"s_addk_i32 s84, 0x7c00\n" \
M_"v[88:91], a[180:183], a[136:139], v[88:91]\n" \
B_"v27, s[68:71], s84 offen offset:3072 lds\n" \
M_"v[88:91], a[244:247], a[200:203], v[88:91]\n" \
"s_add_u32 m0, s30, 0x8000\n" \
"s_movk_i32 s84, 0x200\n" \
M_"v[92:95], a[184:187], a[136:139], v[92:95]\n" \
B_"v24, s[76:79], s84 offen offset:0 lds\n" \
M_"v[92:95], a[248:251], a[200:203], v[92:95]\n" \
X_"a[236:237], v35 offset:4096\n" \
"s_and_b32 s20, s19, 0xffff\n" \
M_"v[96:99], a[188:191], a[136:139], v[96:99]\n" \
X_"a[238:239], v35 offset:4608\n" \
"v_writelane_b32 v11, s18, 11\n" \
M_"v[96:99], a[252:255], a[200:203], v[96:99]\n" \
R_"a[136:139], v28 offset:4096\n" \
M_"v[112:115], a[188:191], a[140:143], v[112:115]\n" \
R_"a[200:203], v29 offset:4096\n" \
M_"v[112:115], a[252:255], a[204:207], v[112:115]\n" \
"s_movk_i32 s84, 0x2200\n" \
"s_nop 0\n" \
M_"v[108:111], a[184:187], a[140:143], v[108:111]\n" \
B_"v25, s[76:79], s84 offen offset:1024 lds\n" \
M_"v[108:111], a[248:251], a[204:207], v[108:111]\n" \
"s_add_u32 s89, s41, 0x1000\n" \
"buffer_store_dwordx4 v[200:203], v20, s[80:83], s89 offen offset:3072 sc1\n" \
M_"v[104:107], a[180:183], a[140:143], v[104:107]\n" \
X_"a[168:169], v34 offset:0\n" \
M_"v[104:107], a[244:247], a[204:207], v[104:107]\n" \
X_"a[170:171], v34 offset:512\n" \
M_"v[100:103], a[176:179], a[140:143], v[100:103]\n" \
X_"a[232:233], v34 offset:4096\n" \
M_"v[100:103], a[240:243], a[204:207], v[100:103]\n" \
R_"a[140:143], v28 offset:6144\n" \
M_"v[52:55], a[176:179], a[128:131], v[52:55]\n" \
R_"a[204:207], v29 offset:6144\n" \
M_"v[52:55], a[240:243], a[192:195], v[52:55]\n" \
"s_movk_i32 s84, 0x4200\n" \
M_"v[56:59], a[180:183], a[128:131], v[56:59]\n" \
B_"v24, s[76:79], s84 offen offset:2048 lds\n" \
M_"v[56:59], a[244:247], a[192:195], v[56:59]\n" \
"s_movk_i32 s84, 0x6200\n" \
M_"v[60:63], a[184:187], a[128:131], v[60:63]\n" \
B_"v25, s[76:79], s84 offen offset:3072 lds\n" \
M_"v[60:63], a[248:251], a[192:195], v[60:63]\n" \
X_"a[234:235], v34 offset:4608\n" \
M_"v[64:67], a[188:191], a[128:131], v[64:67]\n" \
"s_cmpk_lt_u32 s3, 3\n" \
"s_cselect_b32 s16, s16, s18\n" \
M_"v[64:67], a[252:255], a[192:195], v[64:67]\n" \
R_"a[128:131], v28 offset:0\n" \
"s_movk_i32 s18, 0xfff\n" \
M_"v[80:83], a[188:191], a[132:135], v[80:83]\n" \
R_"a[192:195], v29 offset:0\n" \
"v_writelane_b32 v11, s20, 12\n" \
M_"v[80:83], a[252:255], a[196:199], v[80:83]\n" \
"s_and_b32 s20, s29, 0xffff\n" \
"v_writelane_b32 v11, s28, 13\n" \
M_"v[76:79], a[184:187], a[132:135], v[76:79]\n" \
"v_writelane_b32 v11, s20, 14\n" \
"v_writelane_b32 v11, s16, 15\n" \
M_"v[76:79], a[248:251], a[196:199], v[76:79]\n" \
"s_cmpk_lt_u32 s3, 3\n" \
"s_cselect_b32 s16, s17, s19\n" \
M_"v[72:75], a[180:183], a[132:135], v[72:75]\n" \
"s_and_b32 s16, s16, 0xffff\n" \
"v_writelane_b32 v11, s16, 16\n" \
M_"v[72:75], a[244:247], a[196:199], v[72:75]\n" \
"s_mul_i32 s16, s5, 0x48\n" \
"s_add_i32 s17, s16, 0x47\n" \
M_"v[68:71], a[176:179], a[132:135], v[68:71]\n" \
"s_cmpk_lt_u32 s3, 3\n" \
"s_cselect_b32 s17, s17, s18\n" \
M_"v[68:71], a[240:243], a[196:199], v[68:71]\n" \
R_"a[132:135], v28 offset:2048\n" \
R_"a[196:199], v29 offset:2048\n" \
"s_waitcnt lgkmcnt(15)\n" \
M_"a[0:3], a[160:163], a[144:147], a[0:3]\n" \
"s_add_u32 m0, s30, 0x9000\n" \
"s_addk_i32 s84, 0x3000\n" \
M_"a[0:3], a[224:227], a[208:211], a[0:3]\n" \
B_"v24, s[76:79], s84 offen offset:0 lds\n" \
M_"a[4:7], a[164:167], a[144:147], a[4:7]\n" \
X_"a[176:177], v36 offset:0\n" \
"s_mulk_i32 s17, 0x480\n" \
M_"a[4:7], a[228:231], a[208:211], a[4:7]\n" \
X_"a[178:179], v36 offset:1024\n" \
"s_addk_i32 s17, 0x120\n" \
M_"a[12:15], a[172:175], a[144:147], a[12:15]\n" \
X_"a[240:241], v36 offset:8192\n" \
"v_writelane_b32 v11, s17, 17\n" \
"s_waitcnt lgkmcnt(15)\n" \
M_"a[12:15], a[236:239], a[208:211], a[12:15]\n" \
"s_add_u32 s88, s41, 0x1000\n" \
"buffer_store_dwordx4 v[204:207], v20, s[80:83], s88 offen offset:2048 sc1\n" \
"s_waitcnt lgkmcnt(11)\n" \
M_"a[8:11], a[144:147], a[168:171], a[8:11]\n" \
X_"a[242:243], v36 offset:9216\n" \
"s_lshl_b32 s17, s3, 5\n" \
".fill (L_pb + 65248 - .) / 4, 4, 0xBF800000\n" \
"L_pp16:\n" \
"s_cbranch_execz L_pret\n" \
"s_waitcnt lgkmcnt(8)\n" \
M_"a[8:11], a[208:211], a[232:235], a[8:11]\n" \
X_"a[180:181], v37 offset:0\n" \
"s_add_i32 s16, s16, s17\n" \
M_"a[24:27], a[148:151], a[168:171], a[24:27]\n" \
X_"a[182:183], v37 offset:1024\n" \
"s_mulk_i32 s16, 0x480\n" \
M_"a[24:27], a[212:215], a[232:235], a[24:27]\n" \
X_"a[244:245], v37 offset:8192\n" \
"s_mul_i32 s17, s12, 0x24000\n" \
M_"a[28:31], a[172:175], a[148:151], a[28:31]\n" \
"s_add_i32 s18, s17, 0x1b000\n" \
M_"a[28:31], a[236:239], a[212:215], a[28:31]\n" \
"s_addk_i32 s84, 0x2000\n" \
M_"a[20:23], a[164:167], a[148:151], a[20:23]\n" \
B_"v25, s[76:79], s84 offen offset:1024 lds\n" \
M_"a[20:23], a[228:231], a[212:215], a[20:23]\n" \
X_"a[246:247], v37 offset:9216\n" \
M_"a[16:19], a[160:163], a[148:151], a[16:19]\n" \
X_"a[184:185], v38 offset:0\n" \
M_"a[16:19], a[224:227], a[212:215], a[16:19]\n" \
X_"a[186:187], v38 offset:1024\n" \
M_"a[32:35], a[160:163], a[152:155], a[32:35]\n" \
"s_addk_i32 s84, 0x2000\n" \
M_"a[32:35], a[224:227], a[216:219], a[32:35]\n" \
B_"v24, s[76:79], s84 offen offset:2048 lds\n" \
M_"a[36:39], a[164:167], a[152:155], a[36:39]\n" \
X_"a[248:249], v38 offset:8192\n" \
M_"a[36:39], a[228:231], a[216:219], a[36:39]\n" \
X_"a[250:251], v38 offset:9216\n" \
M_"a[44:47], a[172:175], a[152:155], a[44:47]\n" \
X_"a[188:189], v39 offset:0\n" \
M_"a[44:47], a[236:239], a[216:219], a[44:47]\n" \
X_"a[190:191], v39 offset:1024\n" \
M_"a[40:43], a[152:155], a[168:171], a[40:43]\n" \
"s_add_u32 s89, s41, 0x1000\n" \
"buffer_store_dwordx4 v[208:211], v20, s[80:83], s89 offen offset:1024 sc1\n" \
M_"a[40:43], a[216:219], a[232:235], a[40:43]\n" \
X_"a[252:253], v39 offset:8192\n" \
M_"a[56:59], a[156:159], a[168:171], a[56:59]\n" \
X_"a[254:255], v39 offset:9216\n" \
M_"a[56:59], a[220:223], a[232:235], a[56:59]\n" \
"s_cmpk_lt_u32 s3, 3\n" \
"s_cselect_b32 s16, s16, s18\n" \
M_"a[60:63], a[172:175], a[156:159], a[60:63]\n" \
"s_xor_b32 s18, s3, 2\n" \
"v_writelane_b32 v11, s16, 18\n" \
M_"a[60:63], a[236:239], a[220:223], a[60:63]\n" \
"s_addk_i32 s84, 0x2000\n" \
M_"a[52:55], a[164:167], a[156:159], a[52:55]\n" \
B_"v25, s[76:79], s84 offen offset:3072 lds\n" \
M_"a[52:55], a[228:231], a[220:223], a[52:55]\n" \
"s_mul_i32 s16, s15, 0x2400\n" \
"s_mul_i32 s15, s15, 0x9000\n" \
M_"a[48:51], a[160:163], a[156:159], a[48:51]\n" \
"s_bitset1_b32 s16, 17\n" \
"v_writelane_b32 v11, s16, 22\n" \
M_"a[48:51], a[224:227], a[220:223], a[48:51]\n" \
"s_add_i32 s17, s17, s15\n" \
"s_mul_i32 s15, s3, 0x2400\n" \
M_"a[64:67], a[164:167], a[140:143], a[64:67]\n" \
"v_writelane_b32 v11, s17, 29\n" \
"s_add_i32 s16, s15, 0xa400\n" \
M_"a[64:67], a[228:231], a[204:207], a[64:67]\n" \
"s_mov_b32 s17, 0x8000\n" \
M_"a[80:83], a[172:175], a[140:143], a[80:83]\n" \
"s_cmpk_lt_u32 s3, 3\n" \
"s_cselect_b32 s16, s16, s17\n" \
M_"a[80:83], a[236:239], a[204:207], a[80:83]\n" \
"s_mov_b32 s17, 0x1b000\n" \
M_"a[68:71], a[140:143], a[168:171], a[68:71]\n" \
"s_cmp_lg_u32 s18, 0\n" \
"s_cselect_b32 s17, s16, s17\n" \
"s_waitcnt vmcnt(11) lgkmcnt(0)\n" \
M_"a[68:71], a[204:207], a[232:235], a[68:71]\n" \
"s_barrier\n" \
"v_xor_b32_e32 v28, 0x20000, v28\n" \
"v_xor_b32_e32 v29, 0x20000, v29\n" \
M_"v[116:119], a[176:179], a[144:147], v[116:119]\n" \
"s_add_u32 m0, s40, 0x0\n" \
"s_nop 0\n" \
"s_add_u32 s85, s39, 0x8000\n" \
M_"v[116:119], a[240:243], a[208:211], v[116:119]\n" \
B_"v20, s[80:83], s85 offen sc1 lds\n" \
M_"v[120:123], a[180:183], a[144:147], v[120:123]\n" \
X_"a[160:161], v32 offset:32768\n" \
"v_writelane_b32 v11, s16, 23\n" \
M_"v[120:123], a[244:247], a[208:211], v[120:123]\n" \
X_"a[162:163], v32 offset:33280\n" \
"s_or_b32 s16, s15, 0x10000\n" \
M_"v[124:127], a[184:187], a[144:147], v[124:127]\n" \
"s_add_u32 m0, s40, 0x1000\n" \
"s_add_u32 s85, s39, 0x9000\n" \
M_"v[124:127], a[248:251], a[208:211], v[124:127]\n" \
B_"v20, s[80:83], s85 offen sc1 lds\n" \
M_"v[128:131], a[188:191], a[144:147], v[128:131]\n" \
X_"a[224:225], v32 offset:36864\n" \
"v_writelane_b32 v11, s17, 24\n" \
M_"v[128:131], a[252:255], a[208:211], v[128:131]\n" \
R_"a[144:147], v28 offset:8192\n" \
"s_add_i32 s17, s15, 0x19000\n" \
M_"v[144:147], a[188:191], a[148:151], v[144:147]\n" \
R_"a[208:211], v29 offset:8192\n" \
"v_writelane_b32 v11, s16, 25\n" \
M_"v[144:147], a[252:255], a[212:215], v[144:147]\n" \
"s_add_u32 m0, s40, 0x3000\n" \
"s_movk_i32 s84, 0x200\n" \
M_"v[140:143], a[184:187], a[148:151], v[140:143]\n" \
B_"v21, s[76:79], s84 offen offset:0 lds\n" \
M_"v[140:143], a[248:251], a[212:215], v[140:143]\n" \
X_"a[226:227], v32 offset:37376\n" \
"s_mov_b32 s16, 0\n" \
M_"v[136:139], a[180:183], a[148:151], v[136:139]\n" \
"s_add_u32 s88, s41, 0x1000\n" \
"buffer_store_dwordx4 v[212:215], v20, s[80:83], s88 offen offset:0 sc1\n" \
M_"v[136:139], a[244:247], a[212:215], v[136:139]\n" \
X_"a[164:165], v33 offset:32768\n" \
M_"v[132:135], a[176:179], a[148:151], v[132:135]\n" \
"s_cmpk_lt_u32 s3, 3\n" \
"s_cselect_b32 s17, s17, s16\n" \
M_"v[132:135], a[240:243], a[212:215], v[132:135]\n" \
R_"a[148:151], v28 offset:10240\n" \
"s_bitset1_b32 s15, 17\n" \
M_"v[148:151], a[176:179], a[152:155], v[148:151]\n" \
R_"a[212:215], v29 offset:10240\n" \
"v_writelane_b32 v11, s17, 26\n" \
M_"v[148:151], a[240:243], a[216:219], v[148:151]\n" \
"s_add_u32 m0, s31, 0x0\n" \
"s_mov_b32 s84, 0x200000\n" \
M_"v[152:155], a[180:183], a[152:155], v[152:155]\n" \
B_"v26, s[68:71], s84 offen offset:0 lds\n" \
M_"v[152:155], a[244:247], a[216:219], v[152:155]\n" \
X_"a[166:167], v33 offset:33280\n" \
"s_movk_i32 s17, 0x2400\n" \
M_"v[156:159], a[184:187], a[152:155], v[156:159]\n" \
X_"a[228:229], v33 offset:36864\n" \
M_"v[156:159], a[248:251], a[216:219], v[156:159]\n" \
"s_addk_i32 s84, 0x7c00\n" \
M_"v[160:163], a[188:191], a[152:155], v[160:163]\n" \
B_"v26, s[68:71], s84 offen offset:1024 lds\n" \
M_"v[160:163], a[252:255], a[216:219], v[160:163]\n" \
R_"a[152:155], v28 offset:12288\n" \
M_"v[176:179], a[188:191], a[156:159], v[176:179]\n" \
R_"a[216:219], v29 offset:12288\n" \
M_"v[176:179], a[252:255], a[220:223], v[176:179]\n" \
"s_addk_i32 s84, 0x7c00\n" \
M_"v[172:175], a[184:187], a[156:159], v[172:175]\n" \
B_"v27, s[68:71], s84 offen offset:2048 lds\n" \
M_"v[172:175], a[248:251], a[220:223], v[172:175]\n" \
X_"a[230:231], v33 offset:37376\n" \
M_"v[168:171], a[180:183], a[156:159], v[168:171]\n" \
X_"a[172:173], v35 offset:32768\n" \
M_"v[168:171], a[244:247], a[220:223], v[168:171]\n" \
X_"a[174:175], v35 offset:33280\n" \
M_"v[164:167], a[176:179], a[156:159], v[164:167]\n" \
"s_cmpk_lt_u32 s3, 3\n" \
"s_cselect_b32 s15, s15, s17\n" \
M_"v[164:167], a[240:243], a[220:223], v[164:167]\n" \
R_"a[156:159], v28 offset:14336\n" \
"v_writelane_b32 v11, s15, 27\n" \
M_"v[84:87], a[176:179], a[136:139], v[84:87]\n" \
R_"a[220:223], v29 offset:14336\n" \
"s_lshl_b32 s15, s3, 10\n" \
M_"v[84:87], a[240:243], a[200:203], v[84:87]\n" \
"s_addk_i32 s84, 0x7c00\n" \
M_"v[88:91], a[180:183], a[136:139], v[88:91]\n" \
B_"v27, s[68:71], s84 offen offset:3072 lds\n" \
M_"v[88:91], a[244:247], a[200:203], v[88:91]\n" \
"s_add_u32 m0, s30, 0x20000\n" \
"s_movk_i32 s84, 0x280\n" \
M_"v[92:95], a[184:187], a[136:139], v[92:95]\n" \
B_"v24, s[76:79], s84 offen offset:0 lds\n" \
M_"v[92:95], a[248:251], a[200:203], v[92:95]\n" \
X_"a[236:237], v35 offset:36864\n" \
"s_addk_i32 s15, 0x4800\n" \
M_"v[96:99], a[188:191], a[136:139], v[96:99]\n" \
X_"a[238:239], v35 offset:37376\n" \
"v_writelane_b32 v11, s15, 28\n" \
M_"v[96:99], a[252:255], a[200:203], v[96:99]\n" \
R_"a[136:139], v28 offset:4096\n" \
"s_add_i32 s15, s14, 1\n" \
M_"v[112:115], a[188:191], a[140:143], v[112:115]\n" \
R_"a[200:203], v29 offset:4096\n" \
"s_and_b32 s15, s15, 3\n" \
M_"v[112:115], a[252:255], a[204:207], v[112:115]\n" \
"s_movk_i32 s84, 0x2280\n" \
M_"v[108:111], a[184:187], a[140:143], v[108:111]\n" \
B_"v25, s[76:79], s84 offen offset:1024 lds\n" \
M_"v[108:111], a[248:251], a[204:207], v[108:111]\n" \
"s_add_u32 s89, s42, 0x0\n" \
"buffer_store_dwordx4 v[216:219], v20, s[80:83], s89 offen offset:1024 sc1\n" \
M_"v[104:107], a[180:183], a[140:143], v[104:107]\n" \
X_"a[168:169], v34 offset:32768\n" \
"s_or_b32 s15, s15, s4\n" \
M_"v[104:107], a[244:247], a[204:207], v[104:107]\n" \
X_"a[170:171], v34 offset:33280\n" \
"s_lshl_b32 s17, s15, 2\n" \
M_"v[100:103], a[176:179], a[140:143], v[100:103]\n" \
X_"a[232:233], v34 offset:36864\n" \
"s_or_b32 s17, s17, s3\n" \
M_"v[100:103], a[240:243], a[204:207], v[100:103]\n" \
R_"a[140:143], v28 offset:6144\n" \
"s_mul_i32 s17, s17, 0x9000\n" \
M_"v[52:55], a[176:179], a[128:131], v[52:55]\n" \
R_"a[204:207], v29 offset:6144\n" \
"v_writelane_b32 v11, s17, 30\n" \
M_"v[52:55], a[240:243], a[192:195], v[52:55]\n" \
"s_movk_i32 s84, 0x4280\n" \
M_"v[56:59], a[180:183], a[128:131], v[56:59]\n" \
B_"v24, s[76:79], s84 offen offset:2048 lds\n" \
M_"v[56:59], a[244:247], a[192:195], v[56:59]\n" \
"s_movk_i32 s84, 0x6280\n" \
M_"v[60:63], a[184:187], a[128:131], v[60:63]\n" \
B_"v25, s[76:79], s84 offen offset:3072 lds\n" \
M_"v[60:63], a[248:251], a[192:195], v[60:63]\n" \
X_"a[234:235], v34 offset:37376\n" \
"s_lshl_b32 s17, s12, 2\n" \
M_"v[64:67], a[188:191], a[128:131], v[64:67]\n" \
"s_or_b32 s17, s17, s3\n" \
"s_add_i32 s14, s14, -1\n" \
M_"v[64:67], a[252:255], a[192:195], v[64:67]\n" \
R_"a[128:131], v28 offset:0\n" \
"s_xor_b32 s17, s17, 8\n" \
M_"v[80:83], a[188:191], a[132:135], v[80:83]\n" \
R_"a[192:195], v29 offset:0\n" \
"s_and_b32 s14, s14, 3\n" \
M_"v[80:83], a[252:255], a[196:199], v[80:83]\n" \
"s_mul_i32 s17, s17, 0x9000\n" \
"s_or_b32 s14, s14, s4\n" \
M_"v[76:79], a[184:187], a[132:135], v[76:79]\n" \
"v_writelane_b32 v11, s17, 31\n" \
"s_lshl_b32 s17, s14, 2\n" \
M_"v[76:79], a[248:251], a[196:199], v[76:79]\n" \
"s_or_b32 s17, s17, s3\n" \
"s_mul_i32 s17, s17, 0x9000\n" \
M_"v[72:75], a[180:183], a[132:135], v[72:75]\n" \
"v_writelane_b32 v11, s17, 32\n" \
"s_or_b32 s17, s8, s13\n" \
M_"v[72:75], a[244:247], a[196:199], v[72:75]\n" \
"s_mul_i32 s17, s17, 0x24800\n" \
"s_lshl_b32 s15, s15, 12\n" \
M_"v[68:71], a[176:179], a[132:135], v[68:71]\n" \
"s_add_i32 s18, s17, 0x3004800\n" \
"s_lshl_b32 s12, s12, 12\n" \
M_"v[68:71], a[240:243], a[196:199], v[68:71]\n" \
R_"a[132:135], v28 offset:2048\n" \
R_"a[196:199], v29 offset:2048\n" \
"s_waitcnt lgkmcnt(15)\n" \
M_"a[0:3], a[160:163], a[144:147], a[0:3]\n" \
"s_add_u32 m0, s30, 0x21000\n" \
"s_addk_i32 s84, 0x3000\n" \
M_"a[0:3], a[224:227], a[208:211], a[0:3]\n" \
B_"v24, s[76:79], s84 offen offset:0 lds\n" \
M_"a[4:7], a[164:167], a[144:147], a[4:7]\n" \
X_"a[176:177], v36 offset:32768\n" \
"s_add_i32 s19, s18, s15\n" \
M_"a[4:7], a[228:231], a[208:211], a[4:7]\n" \
X_"a[178:179], v36 offset:33792\n" \
"v_writelane_b32 v11, s19, 33\n" \
M_"a[12:15], a[172:175], a[144:147], a[12:15]\n" \
X_"a[240:241], v36 offset:40960\n" \
"s_xor_b32 s12, s12, 0x2000\n" \
"s_waitcnt lgkmcnt(15)\n" \
M_"a[12:15], a[236:239], a[208:211], a[12:15]\n" \
"s_add_u32 s88, s42, 0x0\n" \
"buffer_store_dwordx4 v[220:223], v20, s[80:83], s88 offen offset:0 sc1\n" \
"s_waitcnt lgkmcnt(11)\n" \
M_"a[8:11], a[144:147], a[168:171], a[8:11]\n" \
X_"a[242:243], v36 offset:41984\n" \
"s_lshl_b32 s14, s14, 12\n" \
"s_waitcnt lgkmcnt(8)\n" \
M_"a[8:11], a[208:211], a[232:235], a[8:11]\n" \
X_"a[180:181], v37 offset:32768\n" \
"s_sub_i32 s13, s8, s13\n" \
M_"a[24:27], a[148:151], a[168:171], a[24:27]\n" \
X_"a[182:183], v37 offset:33792\n" \
"s_add_i32 s19, s18, s12\n" \
M_"a[24:27], a[212:215], a[232:235], a[24:27]\n" \
X_"a[244:245], v37 offset:40960\n" \
"v_writelane_b32 v11, s19, 34\n" \
M_"a[28:31], a[172:175], a[148:151], a[28:31]\n" \
"s_add_i32 s18, s18, s14\n" \
"s_mul_i32 s13, s13, 0x24800\n" \
M_"a[28:31], a[236:239], a[212:215], a[28:31]\n" \
"s_addk_i32 s84, 0x2000\n" \
M_"a[20:23], a[164:167], a[148:151], a[20:23]\n" \
B_"v25, s[76:79], s84 offen offset:1024 lds\n" \
M_"a[20:23], a[228:231], a[212:215], a[20:23]\n" \
X_"a[246:247], v37 offset:41984\n" \
"v_writelane_b32 v11, s18, 35\n" \
M_"a[16:19], a[160:163], a[148:151], a[16:19]\n" \
X_"a[184:185], v38 offset:32768\n" \
"s_add_i32 s18, s13, 0x4120800\n" \
M_"a[16:19], a[224:227], a[212:215], a[16:19]\n" \
X_"a[186:187], v38 offset:33792\n" \
"s_add_i32 s15, s18, s15\n" \
M_"a[32:35], a[160:163], a[152:155], a[32:35]\n" \
"s_addk_i32 s84, 0x2000\n" \
M_"a[32:35], a[224:227], a[216:219], a[32:35]\n" \
B_"v24, s[76:79], s84 offen offset:2048 lds\n" \
M_"a[36:39], a[164:167], a[152:155], a[36:39]\n" \
X_"a[248:249], v38 offset:40960\n" \
"v_writelane_b32 v11, s15, 36\n" \
M_"a[36:39], a[228:231], a[216:219], a[36:39]\n" \
X_"a[250:251], v38 offset:41984\n" \
"s_add_i32 s12, s18, s12\n" \
M_"a[44:47], a[172:175], a[152:155], a[44:47]\n" \
X_"a[188:189], v39 offset:32768\n" \
"v_writelane_b32 v11, s12, 37\n" \
M_"a[44:47], a[236:239], a[216:219], a[44:47]\n" \
X_"a[190:191], v39 offset:33792\n" \
"s_add_i32 s18, s18, s14\n" \
M_"a[40:43], a[152:155], a[168:171], a[40:43]\n" \
"s_add_u32 s89, s42, 0x1000\n" \
"buffer_store_dwordx4 v[224:227], v20, s[80:83], s89 offen offset:3072 sc1\n" \
M_"a[40:43], a[216:219], a[232:235], a[40:43]\n" \
X_"a[252:253], v39 offset:40960\n" \
"s_or_b32 s12, s17, 0x3000000\n" \
M_"a[56:59], a[156:159], a[168:171], a[56:59]\n" \
X_"a[254:255], v39 offset:41984\n" \
"s_lshl_b32 s14, s5, 11\n" \
M_"a[56:59], a[220:223], a[232:235], a[56:59]\n" \
"v_writelane_b32 v11, s18, 38\n" \
"s_add_i32 s15, s12, s14\n" \
M_"a[60:63], a[172:175], a[156:159], a[60:63]\n" \
"v_writelane_b32 v11, s15, 39\n" \
"s_add_i32 s15, s13, 0x411c000\n" \
M_"a[60:63], a[236:239], a[220:223], a[60:63]\n" \
"s_addk_i32 s84, 0x2000\n" \
M_"a[52:55], a[164:167], a[156:159], a[52:55]\n" \
B_"v25, s[76:79], s84 offen offset:3072 lds\n" \
M_"a[52:55], a[228:231], a[220:223], a[52:55]\n" \
"s_add_i32 s18, s15, s14\n" \
"v_writelane_b32 v11, s18, 40\n" \
M_"a[48:51], a[160:163], a[156:159], a[48:51]\n" \
"s_add_i32 s17, s17, 0x3004000\n" \
"v_writelane_b32 v11, s17, 41\n" \
M_"a[48:51], a[224:227], a[220:223], a[48:51]\n" \
"s_add_i32 s13, s13, 0x4120000\n" \
"v_writelane_b32 v11, s13, 42\n" \
M_"a[64:67], a[164:167], a[140:143], a[64:67]\n" \
"s_lshl_b32 s13, s9, 2\n" \
"s_or_b32 s7, s13, s7\n" \
M_"a[64:67], a[228:231], a[204:207], a[64:67]\n" \
"s_mul_i32 s7, s7, 0x90000\n" \
"s_lshl_b32 s13, s3, 14\n" \
M_"a[80:83], a[172:175], a[140:143], a[80:83]\n" \
"s_lshl_b32 s9, s9, 7\n" \
"s_lshl_b32 s8, s8, 4\n" \
M_"a[80:83], a[236:239], a[204:207], a[80:83]\n" \
"s_add_i32 s7, s7, s13\n" \
"s_or_b32 s13, s8, s9\n" \
M_"a[68:71], a[140:143], a[168:171], a[68:71]\n" \
"s_sub_i32 s8, s8, s9\n" \
"s_mulk_i32 s13, 0x480\n" \
"s_waitcnt vmcnt(11) lgkmcnt(0)\n" \
M_"a[68:71], a[204:207], a[232:235], a[68:71]\n" \
"s_barrier\n" \
M_"v[116:119], a[176:179], a[144:147], v[116:119]\n" \
"s_add_u32 m0, s40, 0x8000\n" \
"s_add_u32 s85, s39, 0xa000\n" \
M_"v[116:119], a[240:243], a[208:211], v[116:119]\n" \
B_"v20, s[80:83], s85 offen sc1 lds\n" \
M_"v[120:123], a[180:183], a[144:147], v[120:123]\n" \
X_"a[160:161], v32 offset:0\n" \
"v_accvgpr_read_b32 v212, a108\n" \
M_"v[120:123], a[244:247], a[208:211], v[120:123]\n" \
X_"a[162:163], v32 offset:512\n" \
"v_accvgpr_read_b32 v213, a109\n" \
M_"v[124:127], a[184:187], a[144:147], v[124:127]\n" \
"s_add_u32 m0, s40, 0x9000\n" \
"s_add_u32 s85, s39, 0xb000\n" \
M_"v[124:127], a[248:251], a[208:211], v[124:127]\n" \
B_"v20, s[80:83], s85 offen sc1 lds\n" \
M_"v[128:131], a[188:191], a[144:147], v[128:131]\n" \
X_"a[224:225], v32 offset:4096\n" \
"v_accvgpr_read_b32 v214, a110\n" \
M_"v[128:131], a[252:255], a[208:211], v[128:131]\n" \
R_"a[144:147], v28 offset:40960\n" \
"v_accvgpr_read_b32 v215, a111\n" \
M_"v[144:147], a[188:191], a[148:151], v[144:147]\n" \
R_"a[208:211], v29 offset:40960\n" \
"v_accvgpr_read_b32 v180, a124\n" \
M_"v[144:147], a[252:255], a[212:215], v[144:147]\n" \
"s_add_u32 m0, s40, 0xb000\n" \
"s_movk_i32 s84, 0x280\n" \
"s_nop 0\n" \
M_"v[140:143], a[184:187], a[148:151], v[140:143]\n" \
B_"v21, s[76:79], s84 offen offset:0 lds\n" \
M_"v[140:143], a[248:251], a[212:215], v[140:143]\n" \
X_"a[226:227], v32 offset:4608\n" \
"v_accvgpr_read_b32 v181, a125\n" \
M_"v[136:139], a[180:183], a[148:151], v[136:139]\n" \
"s_add_u32 s88, s42, 0x1000\n" \
"buffer_store_dwordx4 v[228:231], v20, s[80:83], s88 offen offset:2048 sc1\n" \
M_"v[136:139], a[244:247], a[212:215], v[136:139]\n" \
X_"a[164:165], v33 offset:0\n" \
"v_accvgpr_read_b32 v182, a126\n" \
M_"v[132:135], a[176:179], a[148:151], v[132:135]\n" \
"v_accvgpr_read_b32 v183, a127\n" \
C_"v212, v212, v213\n" \
M_"v[132:135], a[240:243], a[212:215], v[132:135]\n" \
R_"a[148:151], v28 offset:43008\n" \
C_"v213, v214, v215\n" \
M_"v[148:151], a[176:179], a[152:155], v[148:151]\n" \
R_"a[212:215], v29 offset:43008\n" \
C_"v214, v180, v181\n" \
M_"v[148:151], a[240:243], a[216:219], v[148:151]\n" \
"s_add_u32 m0, s31, 0x8000\n" \
"s_mov_b32 s84, 0x280000\n" \
M_"v[152:155], a[180:183], a[152:155], v[152:155]\n" \
B_"v26, s[68:71], s84 offen offset:0 lds\n" \
M_"v[152:155], a[244:247], a[216:219], v[152:155]\n" \
X_"a[166:167], v33 offset:512\n" \
C_"v215, v182, v183\n" \
M_"v[156:159], a[184:187], a[152:155], v[156:159]\n" \
X_"a[228:229], v33 offset:4096\n" \
"v_accvgpr_read_b32 v208, a104\n" \
M_"v[156:159], a[248:251], a[216:219], v[156:159]\n" \
"s_addk_i32 s84, 0x7c00\n" \
M_"v[160:163], a[188:191], a[152:155], v[160:163]\n" \
B_"v26, s[68:71], s84 offen offset:1024 lds\n" \
M_"v[160:163], a[252:255], a[216:219], v[160:163]\n" \
R_"a[152:155], v28 offset:45056\n" \
"v_accvgpr_read_b32 v209, a105\n" \
M_"v[176:179], a[188:191], a[156:159], v[176:179]\n" \
R_"a[216:219], v29 offset:45056\n" \
"v_accvgpr_read_b32 v210, a106\n" \
M_"v[176:179], a[252:255], a[220:223], v[176:179]\n" \
"s_addk_i32 s84, 0x7c00\n" \
M_"v[172:175], a[184:187], a[156:159], v[172:175]\n" \
B_"v27, s[68:71], s84 offen offset:2048 lds\n" \
M_"v[172:175], a[248:251], a[220:223], v[172:175]\n" \
X_"a[230:231], v33 offset:4608\n" \
"v_accvgpr_read_b32 v211, a107\n" \
M_"v[168:171], a[180:183], a[156:159], v[168:171]\n" \
X_"a[172:173], v35 offset:0\n" \
"v_accvgpr_read_b32 v180, a120\n" \
M_"v[168:171], a[244:247], a[220:223], v[168:171]\n" \
X_"a[174:175], v35 offset:512\n" \
"v_accvgpr_read_b32 v181, a121\n" \
M_"v[164:167], a[176:179], a[156:159], v[164:167]\n" \
"v_accvgpr_read_b32 v182, a122\n" \
"v_accvgpr_read_b32 v183, a123\n" \
M_"v[164:167], a[240:243], a[220:223], v[164:167]\n" \
R_"a[156:159], v28 offset:47104\n" \
C_"v208, v208, v209\n" \
M_"v[84:87], a[176:179], a[136:139], v[84:87]\n" \
R_"a[220:223], v29 offset:47104\n" \
C_"v209, v210, v211\n" \
M_"v[84:87], a[240:243], a[200:203], v[84:87]\n" \
"s_addk_i32 s84, 0x7c00\n" \
M_"v[88:91], a[180:183], a[136:139], v[88:91]\n" \
B_"v27, s[68:71], s84 offen offset:3072 lds\n" \
M_"v[88:91], a[244:247], a[200:203], v[88:91]\n" \
"s_add_u32 m0, s30, 0x0\n" \
"s_movk_i32 s84, 0x300\n" \
"s_nop 0\n" \
M_"v[92:95], a[184:187], a[136:139], v[92:95]\n" \
B_"v24, s[76:79], s84 offen offset:0 lds\n" \
M_"v[92:95], a[248:251], a[200:203], v[92:95]\n" \
X_"a[236:237], v35 offset:4096\n" \
C_"v210, v180, v181\n" \
M_"v[96:99], a[188:191], a[136:139], v[96:99]\n" \
X_"a[238:239], v35 offset:4608\n" \
C_"v211, v182, v183\n" \
M_"v[96:99], a[252:255], a[200:203], v[96:99]\n" \
R_"a[136:139], v28 offset:36864\n" \
"v_accvgpr_read_b32 v204, a100\n" \
M_"v[112:115], a[188:191], a[140:143], v[112:115]\n" \
R_"a[200:203], v29 offset:36864\n" \
"v_accvgpr_read_b32 v205, a101\n" \
M_"v[112:115], a[252:255], a[204:207], v[112:115]\n" \
"s_movk_i32 s84, 0x2300\n" \
"s_nop 0\n" \
M_"v[108:111], a[184:187], a[140:143], v[108:111]\n" \
B_"v25, s[76:79], s84 offen offset:1024 lds\n" \
M_"v[108:111], a[248:251], a[204:207], v[108:111]\n" \
"s_add_u32 s89, s42, 0x1000\n" \
"buffer_store_dwordx4 v[232:235], v20, s[80:83], s89 offen offset:1024 sc1\n" \
M_"v[104:107], a[180:183], a[140:143], v[104:107]\n" \
X_"a[168:169], v34 offset:0\n" \
"v_accvgpr_read_b32 v206, a102\n" \
M_"v[104:107], a[244:247], a[204:207], v[104:107]\n" \
X_"a[170:171], v34 offset:512\n" \
"v_accvgpr_read_b32 v207, a103\n" \
M_"v[100:103], a[176:179], a[140:143], v[100:103]\n" \
X_"a[232:233], v34 offset:4096\n" \
"v_accvgpr_read_b32 v180, a116\n" \
M_"v[100:103], a[240:243], a[204:207], v[100:103]\n" \
R_"a[140:143], v28 offset:38912\n" \
"v_accvgpr_read_b32 v181, a117\n" \
M_"v[52:55], a[176:179], a[128:131], v[52:55]\n" \
R_"a[204:207], v29 offset:38912\n" \
"v_accvgpr_read_b32 v182, a118\n" \
M_"v[52:55], a[240:243], a[192:195], v[52:55]\n" \
"s_movk_i32 s84, 0x4300\n" \
M_"v[56:59], a[180:183], a[128:131], v[56:59]\n" \
B_"v24, s[76:79], s84 offen offset:2048 lds\n" \
M_"v[56:59], a[244:247], a[192:195], v[56:59]\n" \
"s_movk_i32 s84, 0x6300\n" \
M_"v[60:63], a[184:187], a[128:131], v[60:63]\n" \
B_"v25, s[76:79], s84 offen offset:3072 lds\n" \
M_"v[60:63], a[248:251], a[192:195], v[60:63]\n" \
X_"a[234:235], v34 offset:4608\n" \
"v_accvgpr_read_b32 v183, a119\n" \
M_"v[64:67], a[188:191], a[128:131], v[64:67]\n" \
C_"v204, v204, v205\n" \
C_"v205, v206, v207\n" \
M_"v[64:67], a[252:255], a[192:195], v[64:67]\n" \
R_"a[128:131], v28 offset:32768\n" \
C_"v206, v180, v181\n" \
M_"v[80:83], a[188:191], a[132:135], v[80:83]\n" \
R_"a[192:195], v29 offset:32768\n" \
C_"v207, v182, v183\n" \
M_"v[80:83], a[252:255], a[196:199], v[80:83]\n" \
"v_accvgpr_read_b32 v200, a96\n" \
"v_accvgpr_read_b32 v201, a97\n" \
M_"v[76:79], a[184:187], a[132:135], v[76:79]\n" \
"v_accvgpr_read_b32 v202, a98\n" \
"v_accvgpr_read_b32 v203, a99\n" \
M_"v[76:79], a[248:251], a[196:199], v[76:79]\n" \
"v_accvgpr_read_b32 v180, a112\n" \
"v_accvgpr_read_b32 v181, a113\n" \
M_"v[72:75], a[180:183], a[132:135], v[72:75]\n" \
"v_accvgpr_read_b32 v182, a114\n" \
"v_accvgpr_read_b32 v183, a115\n" \
M_"v[72:75], a[244:247], a[196:199], v[72:75]\n" \
C_"v200, v200, v201\n" \
C_"v201, v202, v203\n" \
M_"v[68:71], a[176:179], a[132:135], v[68:71]\n" \
C_"v202, v180, v181\n" \
C_"v203, v182, v183\n" \
M_"v[68:71], a[240:243], a[196:199], v[68:71]\n" \
R_"a[132:135], v28 offset:34816\n" \
R_"a[196:199], v29 offset:34816\n" \
"s_waitcnt lgkmcnt(15)\n" \
M_"a[0:3], a[160:163], a[144:147], a[0:3]\n" \
"s_add_u32 m0, s30, 0x1000\n" \
"s_addk_i32 s84, 0x3000\n" \
M_"a[0:3], a[224:227], a[208:211], a[0:3]\n" \
B_"v24, s[76:79], s84 offen offset:0 lds\n" \
M_"a[4:7], a[164:167], a[144:147], a[4:7]\n" \
X_"a[176:177], v36 offset:0\n" \
"v_accvgpr_read_b32 v196, a76\n" \
M_"a[4:7], a[228:231], a[208:211], a[4:7]\n" \
X_"a[178:179], v36 offset:1024\n" \
"v_accvgpr_read_b32 v197, a77\n" \
M_"a[12:15], a[172:175], a[144:147], a[12:15]\n" \
X_"a[240:241], v36 offset:8192\n" \
"v_accvgpr_read_b32 v198, a78\n" \
"s_waitcnt lgkmcnt(15)\n" \
M_"a[12:15], a[236:239], a[208:211], a[12:15]\n" \
"s_add_u32 s88, s42, 0x1000\n" \
"buffer_store_dwordx4 v[236:239], v20, s[80:83], s88 offen offset:0 sc1\n" \
"s_waitcnt lgkmcnt(11)\n" \
M_"a[8:11], a[144:147], a[168:171], a[8:11]\n" \
X_"a[242:243], v36 offset:9216\n" \
"v_accvgpr_read_b32 v199, a79\n" \
"s_waitcnt lgkmcnt(8)\n" \
"s_nop 0\n" \
M_"a[8:11], a[208:211], a[232:235], a[8:11]\n" \
X_"a[180:181], v37 offset:0\n" \
".fill (L_pb + 69344 - .) / 4, 4, 0xBF800000\n" \
"L_pp17:\n" \
"s_cbranch_execz L_pret\n" \
"s_nop 0\n" \
"v_accvgpr_read_b32 v180, a92\n" \
M_"a[24:27], a[148:151], a[168:171], a[24:27]\n" \
X_"a[182:183], v37 offset:1024\n" \
"v_accvgpr_read_b32 v181, a93\n" \
M_"a[24:27], a[212:215], a[232:235], a[24:27]\n" \
X_"a[244:245], v37 offset:8192\n" \
"v_accvgpr_read_b32 v182, a94\n" \
M_"a[28:31], a[172:175], a[148:151], a[28:31]\n" \
"v_accvgpr_read_b32 v183, a95\n" \
C_"v196, v196, v197\n" \
M_"a[28:31], a[236:239], a[212:215], a[28:31]\n" \
"s_addk_i32 s84, 0x2000\n" \
"s_nop 0\n" \
M_"a[20:23], a[164:167], a[148:151], a[20:23]\n" \
B_"v25, s[76:79], s84 offen offset:1024 lds\n" \
M_"a[20:23], a[228:231], a[212:215], a[20:23]\n" \
X_"a[246:247], v37 offset:9216\n" \
C_"v197, v198, v199\n" \
M_"a[16:19], a[160:163], a[148:151], a[16:19]\n" \
X_"a[184:185], v38 offset:0\n" \
C_"v198, v180, v181\n" \
M_"a[16:19], a[224:227], a[212:215], a[16:19]\n" \
X_"a[186:187], v38 offset:1024\n" \
C_"v199, v182, v183\n" \
M_"a[32:35], a[160:163], a[152:155], a[32:35]\n" \
"s_addk_i32 s84, 0x2000\n" \
"s_nop 0\n" \
M_"a[32:35], a[224:227], a[216:219], a[32:35]\n" \
B_"v24, s[76:79], s84 offen offset:2048 lds\n" \
M_"a[36:39], a[164:167], a[152:155], a[36:39]\n" \
X_"a[248:249], v38 offset:8192\n" \
"v_accvgpr_read_b32 v192, a72\n" \
M_"a[36:39], a[228:231], a[216:219], a[36:39]\n" \
X_"a[250:251], v38 offset:9216\n" \
"v_accvgpr_read_b32 v193, a73\n" \
M_"a[44:47], a[172:175], a[152:155], a[44:47]\n" \
X_"a[188:189], v39 offset:0\n" \
"v_accvgpr_read_b32 v194, a74\n" \
M_"a[44:47], a[236:239], a[216:219], a[44:47]\n" \
X_"a[190:191], v39 offset:1024\n" \
"v_accvgpr_read_b32 v195, a75\n" \
M_"a[40:43], a[152:155], a[168:171], a[40:43]\n" \
"s_add_u32 s89, s42, 0x3000\n" \
"buffer_store_dwordx4 v[240:243], v20, s[80:83], s89 offen offset:3072 sc1\n" \
M_"a[40:43], a[216:219], a[232:235], a[40:43]\n" \
X_"a[252:253], v39 offset:8192\n" \
"v_accvgpr_read_b32 v180, a88\n" \
M_"a[56:59], a[156:159], a[168:171], a[56:59]\n" \
X_"a[254:255], v39 offset:9216\n" \
"v_accvgpr_read_b32 v181, a89\n" \
M_"a[56:59], a[220:223], a[232:235], a[56:59]\n" \
"v_accvgpr_read_b32 v182, a90\n" \
"v_accvgpr_read_b32 v183, a91\n" \
M_"a[60:63], a[172:175], a[156:159], a[60:63]\n" \
C_"v192, v192, v193\n" \
C_"v193, v194, v195\n" \
M_"a[60:63], a[236:239], a[220:223], a[60:63]\n" \
"s_addk_i32 s84, 0x2000\n" \
M_"a[52:55], a[164:167], a[156:159], a[52:55]\n" \
B_"v25, s[76:79], s84 offen offset:3072 lds\n" \
M_"a[52:55], a[228:231], a[220:223], a[52:55]\n" \
C_"v194, v180, v181\n" \
C_"v195, v182, v183\n" \
M_"a[48:51], a[160:163], a[156:159], a[48:51]\n" \
"s_mulk_i32 s8, 0x480\n" \
"s_or_b32 s13, s13, s11\n" \
M_"a[48:51], a[224:227], a[220:223], a[48:51]\n" \
"s_or_b32 s8, s8, s11\n" \
"s_and_b32 s11, s2, 1\n" \
M_"a[64:67], a[164:167], a[140:143], a[64:67]\n" \
"s_cmp_lg_u32 s11, 0\n" \
"s_cselect_b32 s12, s15, s12\n" \
M_"a[64:67], a[228:231], a[204:207], a[64:67]\n" \
"s_bfe_u32 s9, s2, 0x20001\n" \
"s_add_i32 s15, s7, 0x4240000\n" \
M_"a[80:83], a[172:175], a[140:143], a[80:83]\n" \
"s_addk_i32 s12, 0x4000\n" \
"s_lshl_b32 s5, s5, 16\n" \
M_"a[80:83], a[236:239], a[204:207], a[80:83]\n" \
"s_xor_b32 s9, s9, s3\n" \
M_"a[68:71], a[140:143], a[168:171], a[68:71]\n" \
"s_cmp_lg_u32 s9, 0\n" \
"s_cselect_b32 s12, s6, s12\n" \
"s_waitcnt vmcnt(11) lgkmcnt(0)\n" \
M_"a[68:71], a[204:207], a[232:235], a[68:71]\n" \
"s_barrier\n" \
"v_mov_b32_e32 v45, s62\n" \
"s_mov_b64 exec, 1\n" \
"buffer_store_dword v45, off, s[80:83], s37 offset:0 sc1\n" \
"s_mov_b64 exec, -1\n" \
"v_xor_b32_e32 v28, 0x20000, v28\n" \
"v_xor_b32_e32 v29, 0x20000, v29\n" \
M_"v[116:119], a[176:179], a[144:147], v[116:119]\n" \
"s_add_u32 m0, s40, 0x0\n" \
"s_add_u32 s85, s39, 0xc000\n" \
M_"v[116:119], a[240:243], a[208:211], v[116:119]\n" \
B_"v20, s[80:83], s85 offen sc1 lds\n" \
M_"v[120:123], a[180:183], a[144:147], v[120:123]\n" \
X_"a[160:161], v32 offset:32768\n" \
"s_add_i32 s5, s15, s5\n" \
"s_nop 0\n" \
M_"v[120:123], a[244:247], a[208:211], v[120:123]\n" \
X_"a[162:163], v32 offset:33280\n" \
"v_writelane_b32 v11, s12, 43\n" \
M_"v[124:127], a[184:187], a[144:147], v[124:127]\n" \
"s_add_u32 m0, s40, 0x1000\n" \
"s_add_u32 s85, s39, 0xd000\n" \
M_"v[124:127], a[248:251], a[208:211], v[124:127]\n" \
B_"v20, s[80:83], s85 offen sc1 lds\n" \
M_"v[128:131], a[188:191], a[144:147], v[128:131]\n" \
X_"a[224:225], v32 offset:36864\n" \
"s_add_i32 s7, s7, 0x42c0000\n" \
M_"v[128:131], a[252:255], a[208:211], v[128:131]\n" \
R_"a[144:147], v28 offset:8192\n" \
"v_writelane_b32 v11, s5, 44\n" \
M_"v[144:147], a[188:191], a[148:151], v[144:147]\n" \
R_"a[208:211], v29 offset:8192\n" \
"s_lshl_b32 s5, s11, 10\n" \
M_"v[144:147], a[252:255], a[212:215], v[144:147]\n" \
"s_add_u32 m0, s40, 0x3000\n" \
"s_movk_i32 s84, 0x300\n" \
M_"v[140:143], a[184:187], a[148:151], v[140:143]\n" \
B_"v21, s[76:79], s84 offen offset:0 lds\n" \
M_"v[140:143], a[248:251], a[212:215], v[140:143]\n" \
X_"a[226:227], v32 offset:37376\n" \
"s_or_b32 s5, s7, s5\n" \
M_"v[136:139], a[180:183], a[148:151], v[136:139]\n" \
"s_cmp_lg_u32 s9, 0\n" \
"s_cselect_b32 s5, s6, s5\n" \
M_"v[136:139], a[244:247], a[212:215], v[136:139]\n" \
X_"a[164:165], v33 offset:32768\n" \
"s_add_i32 s8, s8, 0x21c000\n" \
M_"v[132:135], a[176:179], a[148:151], v[132:135]\n" \
"v_writelane_b32 v11, s5, 45\n" \
"s_add_i32 s5, s13, s10\n" \
M_"v[132:135], a[240:243], a[212:215], v[132:135]\n" \
R_"a[148:151], v28 offset:10240\n" \
"v_writelane_b32 v11, s5, 46\n" \
M_"v[148:151], a[176:179], a[152:155], v[148:151]\n" \
R_"a[212:215], v29 offset:10240\n" \
"s_add_i32 s5, s8, s10\n" \
M_"v[148:151], a[240:243], a[216:219], v[148:151]\n" \
"s_add_u32 m0, s31, 0x0\n" \
"s_mov_b32 s84, 0x300000\n" \
M_"v[152:155], a[180:183], a[152:155], v[152:155]\n" \
B_"v26, s[68:71], s84 offen offset:0 lds\n" \
M_"v[152:155], a[244:247], a[216:219], v[152:155]\n" \
"s_addk_i32 s84, 0x7c00\n" \
"s_nop 0\n" \
M_"v[156:159], a[184:187], a[152:155], v[156:159]\n" \
B_"v26, s[68:71], s84 offen offset:1024 lds\n" \
M_"v[156:159], a[248:251], a[216:219], v[156:159]\n" \
X_"a[166:167], v33 offset:33280\n" \
"v_writelane_b32 v11, s5, 47\n" \
M_"v[160:163], a[188:191], a[152:155], v[160:163]\n" \
X_"a[228:229], v33 offset:36864\n" \
M_"v[160:163], a[252:255], a[216:219], v[160:163]\n" \
R_"a[152:155], v28 offset:12288\n" \
M_"v[176:179], a[188:191], a[156:159], v[176:179]\n" \
R_"a[216:219], v29 offset:12288\n" \
M_"v[176:179], a[252:255], a[220:223], v[176:179]\n" \
"s_addk_i32 s84, 0x7c00\n" \
M_"v[172:175], a[184:187], a[156:159], v[172:175]\n" \
B_"v27, s[68:71], s84 offen offset:2048 lds\n" \
M_"v[172:175], a[248:251], a[220:223], v[172:175]\n" \
X_"a[230:231], v33 offset:37376\n" \
M_"v[168:171], a[180:183], a[156:159], v[168:171]\n" \
"s_addk_i32 s84, 0x7c00\n" \
M_"v[168:171], a[244:247], a[220:223], v[168:171]\n" \
B_"v27, s[68:71], s84 offen offset:3072 lds\n" \
M_"v[164:167], a[176:179], a[156:159], v[164:167]\n" \
X_"a[172:173], v35 offset:32768\n" \
M_"v[164:167], a[240:243], a[220:223], v[164:167]\n" \
R_"a[156:159], v28 offset:14336\n" \
M_"v[84:87], a[176:179], a[136:139], v[84:87]\n" \
R_"a[220:223], v29 offset:14336\n" \
M_"v[84:87], a[240:243], a[200:203], v[84:87]\n" \
"s_add_u32 s88, s42, 0x3000\n" \
"buffer_store_dwordx4 v[244:247], v20, s[80:83], s88 offen offset:2048 sc1\n" \
M_"v[88:91], a[180:183], a[136:139], v[88:91]\n" \
X_"a[174:175], v35 offset:33280\n" \
M_"v[88:91], a[244:247], a[200:203], v[88:91]\n" \
X_"a[236:237], v35 offset:36864\n" \
M_"v[92:95], a[184:187], a[136:139], v[92:95]\n" \
X_"a[238:239], v35 offset:37376\n" \
M_"v[92:95], a[248:251], a[200:203], v[92:95]\n" \
"s_add_u32 m0, s30, 0x8000\n" \
"s_movk_i32 s84, 0x380\n" \
M_"v[96:99], a[188:191], a[136:139], v[96:99]\n" \
B_"v24, s[76:79], s84 offen offset:0 lds\n" \
M_"v[96:99], a[252:255], a[200:203], v[96:99]\n" \
R_"a[136:139], v28 offset:4096\n" \
M_"v[112:115], a[188:191], a[140:143], v[112:115]\n" \
R_"a[200:203], v29 offset:4096\n" \
M_"v[112:115], a[252:255], a[204:207], v[112:115]\n" \
X_"a[168:169], v34 offset:32768\n" \
M_"v[108:111], a[184:187], a[140:143], v[108:111]\n" \
"s_movk_i32 s84, 0x2380\n" \
M_"v[108:111], a[248:251], a[204:207], v[108:111]\n" \
B_"v25, s[76:79], s84 offen offset:1024 lds\n" \
M_"v[104:107], a[180:183], a[140:143], v[104:107]\n" \
X_"a[170:171], v34 offset:33280\n" \
M_"v[104:107], a[244:247], a[204:207], v[104:107]\n" \
X_"a[232:233], v34 offset:36864\n" \
M_"v[100:103], a[176:179], a[140:143], v[100:103]\n" \
"s_cmp_lg_u32 s11, 0\n" \
"s_cselect_b32 s5, s8, s13\n" \
M_"v[100:103], a[240:243], a[204:207], v[100:103]\n" \
R_"a[140:143], v28 offset:6144\n" \
"s_addk_i32 s5, 0x100\n" \
M_"v[52:55], a[176:179], a[128:131], v[52:55]\n" \
R_"a[204:207], v29 offset:6144\n" \
M_"v[52:55], a[240:243], a[192:195], v[52:55]\n" \
"s_movk_i32 s84, 0x4380\n" \
M_"v[56:59], a[180:183], a[128:131], v[56:59]\n" \
B_"v24, s[76:79], s84 offen offset:2048 lds\n" \
M_"v[56:59], a[244:247], a[192:195], v[56:59]\n" \
"s_movk_i32 s84, 0x6380\n" \
M_"v[60:63], a[184:187], a[128:131], v[60:63]\n" \
B_"v25, s[76:79], s84 offen offset:3072 lds\n" \
M_"v[60:63], a[248:251], a[192:195], v[60:63]\n" \
X_"a[234:235], v34 offset:37376\n" \
M_"v[64:67], a[188:191], a[128:131], v[64:67]\n" \
"s_cmp_lg_u32 s9, 0\n" \
"s_cselect_b32 s5, s6, s5\n" \
M_"v[64:67], a[252:255], a[192:195], v[64:67]\n" \
R_"a[128:131], v28 offset:0\n" \
"v_writelane_b32 v11, s5, 48\n" \
M_"v[80:83], a[188:191], a[132:135], v[80:83]\n" \
R_"a[192:195], v29 offset:0\n" \
"s_or_b32 s5, s7, s14\n" \
M_"v[80:83], a[252:255], a[196:199], v[80:83]\n" \
"v_writelane_b32 v11, s5, 56\n" \
"s_lshl_b32 s5, s3, 17\n" \
M_"v[76:79], a[184:187], a[132:135], v[76:79]\n" \
"s_add_i32 s5, s15, s5\n" \
"s_or_b32 s4, s5, s4\n" \
M_"v[76:79], a[248:251], a[196:199], v[76:79]\n" \
"s_or_b32 s4, s4, 0x3840\n" \
"v_writelane_b32 v11, s4, 6\n" \
M_"v[72:75], a[180:183], a[132:135], v[72:75]\n" \
"s_mov_b32 s4, 0xff0000\n" \
M_"v[72:75], a[244:247], a[196:199], v[72:75]\n" \
"s_cmp_lg_u32 s9, 0\n" \
"s_cselect_b32 s4, s16, s4\n" \
M_"v[68:71], a[176:179], a[132:135], v[68:71]\n" \
"v_writelane_b32 v11, s4, 7\n" \
"s_lshl_b32 s4, s2, 16\n" \
M_"v[68:71], a[240:243], a[196:199], v[68:71]\n" \
R_"a[132:135], v28 offset:2048\n" \
R_"a[196:199], v29 offset:2048\n" \
"s_waitcnt lgkmcnt(15)\n" \
M_"a[0:3], a[160:163], a[144:147], a[0:3]\n" \
"s_add_u32 m0, s30, 0x9000\n" \
"s_addk_i32 s84, 0x3000\n" \
M_"a[0:3], a[224:227], a[208:211], a[0:3]\n" \
B_"v24, s[76:79], s84 offen offset:0 lds\n" \
M_"a[4:7], a[164:167], a[144:147], a[4:7]\n" \
X_"a[176:177], v36 offset:32768\n" \
"s_add_i32 s5, s4, 0x10000\n" \
M_"a[4:7], a[228:231], a[208:211], a[4:7]\n" \
X_"a[178:179], v36 offset:33792\n" \
"s_and_b32 s5, s5, 0x70000\n" \
"s_waitcnt lgkmcnt(15)\n" \
M_"a[12:15], a[172:175], a[144:147], a[12:15]\n" \
X_"a[240:241], v36 offset:40960\n" \
"s_add_i32 s5, s15, s5\n" \
"s_waitcnt lgkmcnt(15)\n" \
M_"a[12:15], a[236:239], a[208:211], a[12:15]\n" \
"s_add_u32 s89, s42, 0x3000\n" \
"buffer_store_dwordx4 v[184:187], v20, s[80:83], s89 offen offset:1024 sc1\n" \
"s_waitcnt lgkmcnt(11)\n" \
M_"a[8:11], a[144:147], a[168:171], a[8:11]\n" \
X_"a[242:243], v36 offset:41984\n" \
"v_writelane_b32 v11, s5, 49\n" \
"s_waitcnt lgkmcnt(8)\n" \
M_"a[8:11], a[208:211], a[232:235], a[8:11]\n" \
X_"a[180:181], v37 offset:32768\n" \
"s_add_i32 s5, s4, 0x20000\n" \
M_"a[24:27], a[148:151], a[168:171], a[24:27]\n" \
X_"a[182:183], v37 offset:33792\n" \
"s_and_b32 s5, s5, 0x70000\n" \
M_"a[24:27], a[212:215], a[232:235], a[24:27]\n" \
X_"a[244:245], v37 offset:40960\n" \
"s_add_i32 s5, s15, s5\n" \
M_"a[28:31], a[172:175], a[148:151], a[28:31]\n" \
"s_bitset1_b32 s5, 11\n" \
"v_writelane_b32 v11, s5, 50\n" \
M_"a[28:31], a[236:239], a[212:215], a[28:31]\n" \
"s_addk_i32 s84, 0x2000\n" \
M_"a[20:23], a[164:167], a[148:151], a[20:23]\n" \
B_"v25, s[76:79], s84 offen offset:1024 lds\n" \
M_"a[20:23], a[228:231], a[212:215], a[20:23]\n" \
X_"a[246:247], v37 offset:41984\n" \
"s_add_i32 s5, s4, 0x30000\n" \
M_"a[16:19], a[160:163], a[148:151], a[16:19]\n" \
X_"a[184:185], v38 offset:32768\n" \
"s_and_b32 s5, s5, 0x70000\n" \
M_"a[16:19], a[224:227], a[212:215], a[16:19]\n" \
X_"a[186:187], v38 offset:33792\n" \
"s_add_i32 s5, s15, s5\n" \
M_"a[32:35], a[160:163], a[152:155], a[32:35]\n" \
"s_addk_i32 s84, 0x2000\n" \
M_"a[32:35], a[224:227], a[216:219], a[32:35]\n" \
B_"v24, s[76:79], s84 offen offset:2048 lds\n" \
M_"a[36:39], a[164:167], a[152:155], a[36:39]\n" \
X_"a[248:249], v38 offset:40960\n" \
"s_bitset1_b32 s5, 12\n" \
"s_nop 0\n" \
M_"a[36:39], a[228:231], a[216:219], a[36:39]\n" \
X_"a[250:251], v38 offset:41984\n" \
"v_writelane_b32 v11, s5, 51\n" \
M_"a[44:47], a[172:175], a[152:155], a[44:47]\n" \
X_"a[188:189], v39 offset:32768\n" \
"s_and_b32 s5, s4, 0x70000\n" \
M_"a[44:47], a[236:239], a[216:219], a[44:47]\n" \
X_"a[190:191], v39 offset:33792\n" \
"s_xor_b32 s5, s5, 0x40000\n" \
M_"a[40:43], a[152:155], a[168:171], a[40:43]\n" \
"s_add_u32 s88, s42, 0x3000\n" \
"buffer_store_dwordx4 v[188:191], v20, s[80:83], s88 offen offset:0 sc1\n" \
M_"a[40:43], a[216:219], a[232:235], a[40:43]\n" \
X_"a[252:253], v39 offset:40960\n" \
"s_add_i32 s5, s15, s5\n" \
M_"a[56:59], a[156:159], a[168:171], a[56:59]\n" \
X_"a[254:255], v39 offset:41984\n" \
"s_or_b32 s5, s5, 0x1800\n" \
M_"a[56:59], a[220:223], a[232:235], a[56:59]\n" \
"v_writelane_b32 v11, s5, 52\n" \
"s_add_i32 s5, s4, 0x50000\n" \
M_"a[60:63], a[172:175], a[156:159], a[60:63]\n" \
"s_and_b32 s5, s5, 0x70000\n" \
"s_add_i32 s5, s15, s5\n" \
M_"a[60:63], a[236:239], a[220:223], a[60:63]\n" \
"s_addk_i32 s84, 0x2000\n" \
M_"a[52:55], a[164:167], a[156:159], a[52:55]\n" \
B_"v25, s[76:79], s84 offen offset:3072 lds\n" \
M_"a[52:55], a[228:231], a[220:223], a[52:55]\n" \
"s_bitset1_b32 s5, 13\n" \
"v_writelane_b32 v11, s5, 53\n" \
M_"a[48:51], a[160:163], a[156:159], a[48:51]\n" \
"s_add_i32 s5, s4, 0x60000\n" \
"s_and_b32 s5, s5, 0x70000\n" \
M_"a[48:51], a[224:227], a[220:223], a[48:51]\n" \
"s_add_i32 s4, s4, 0x70000\n" \
"s_add_i32 s5, s15, s5\n" \
M_"a[64:67], a[164:167], a[140:143], a[64:67]\n" \
"s_and_b32 s4, s4, 0x70000\n" \
"s_or_b32 s5, s5, 0x2800\n" \
M_"a[64:67], a[228:231], a[204:207], a[64:67]\n" \
"v_writelane_b32 v11, s5, 54\n" \
"s_add_i32 s15, s15, s4\n" \
M_"a[80:83], a[172:175], a[140:143], a[80:83]\n" \
"s_or_b32 s4, s15, 0x3000\n" \
"v_writelane_b32 v11, s4, 55\n" \
M_"a[80:83], a[236:239], a[204:207], a[80:83]\n" \
"s_movk_i32 s11, 0x4000\n" \
"s_lshl_b32 s4, s2, 2\n" \
M_"a[68:71], a[140:143], a[168:171], a[68:71]\n" \
"s_and_b32 s4, s4, 28\n" \
"s_bfe_u32 s5, s2, 0x20003\n" \
"s_waitcnt vmcnt(11) lgkmcnt(0)\n" \
M_"a[68:71], a[204:207], a[232:235], a[68:71]\n" \
"s_barrier\n" \
"v_xor_b32_e32 v28, 0x20000, v28\n" \
"v_xor_b32_e32 v29, 0x20000, v29\n" \
M_"v[116:119], a[176:179], a[144:147], v[116:119]\n" \
"s_add_u32 m0, s40, 0x8000\n" \
"s_add_u32 s85, s39, 0xe000\n" \
M_"v[116:119], a[240:243], a[208:211], v[116:119]\n" \
B_"v20, s[80:83], s85 offen sc1 lds\n" \
M_"v[120:123], a[180:183], a[144:147], v[120:123]\n" \
X_"a[160:161], v32 offset:0\n" \
"s_or_b32 s4, s4, s5\n" \
M_"v[120:123], a[244:247], a[208:211], v[120:123]\n" \
X_"a[162:163], v32 offset:512\n" \
"s_lshr_b32 s5, s2, 5\n" \
M_"v[124:127], a[184:187], a[144:147], v[124:127]\n" \
"s_add_u32 m0, s40, 0x9000\n" \
"s_add_u32 s85, s39, 0xf000\n" \
M_"v[124:127], a[248:251], a[208:211], v[124:127]\n" \
B_"v20, s[80:83], s85 offen sc1 lds\n" \
M_"v[128:131], a[188:191], a[144:147], v[128:131]\n" \
X_"a[224:225], v32 offset:4096\n" \
"s_lshr_b32 s19, s3, 1\n" \
M_"v[128:131], a[252:255], a[208:211], v[128:131]\n" \
R_"a[144:147], v28 offset:8192\n" \
"s_lshl_b32 s18, s5, 1\n" \
M_"v[144:147], a[188:191], a[148:151], v[144:147]\n" \
R_"a[208:211], v29 offset:8192\n" \
"s_lshl_b32 s6, s4, 20\n" \
M_"v[144:147], a[252:255], a[212:215], v[144:147]\n" \
"s_add_u32 m0, s40, 0xb000\n" \
"s_movk_i32 s84, 0x380\n" \
M_"v[140:143], a[184:187], a[148:151], v[140:143]\n" \
B_"v21, s[76:79], s84 offen offset:0 lds\n" \
M_"v[140:143], a[248:251], a[212:215], v[140:143]\n" \
X_"a[226:227], v32 offset:4608\n" \
"s_lshl_b32 s7, s4, 8\n" \
M_"v[136:139], a[180:183], a[148:151], v[136:139]\n" \
"s_and_b32 s13, s3, 1\n" \
"s_add_i32 s14, s18, s19\n" \
M_"v[136:139], a[244:247], a[212:215], v[136:139]\n" \
X_"a[164:165], v33 offset:0\n" \
M_"v[132:135], a[176:179], a[148:151], v[132:135]\n" \
"s_add_u32 s6, s26, s6\n" \
"s_addc_u32 s9, s27, 0\n" \
M_"v[132:135], a[240:243], a[212:215], v[132:135]\n" \
R_"a[148:151], v28 offset:10240\n" \
M_"v[148:151], a[176:179], a[152:155], v[148:151]\n" \
R_"a[212:215], v29 offset:10240\n" \
M_"v[148:151], a[240:243], a[216:219], v[148:151]\n" \
"s_add_u32 m0, s31, 0x8000\n" \
"s_mov_b32 s84, 0x380000\n" \
M_"v[152:155], a[180:183], a[152:155], v[152:155]\n" \
B_"v26, s[68:71], s84 offen offset:0 lds\n" \
M_"v[152:155], a[244:247], a[216:219], v[152:155]\n" \
"s_addk_i32 s84, 0x7c00\n" \
"s_nop 0\n" \
M_"v[156:159], a[184:187], a[152:155], v[156:159]\n" \
B_"v26, s[68:71], s84 offen offset:1024 lds\n" \
M_"v[156:159], a[248:251], a[216:219], v[156:159]\n" \
X_"a[166:167], v33 offset:512\n" \
M_"v[160:163], a[188:191], a[152:155], v[160:163]\n" \
X_"a[228:229], v33 offset:4096\n" \
M_"v[160:163], a[252:255], a[216:219], v[160:163]\n" \
R_"a[152:155], v28 offset:12288\n" \
M_"v[176:179], a[188:191], a[156:159], v[176:179]\n" \
R_"a[216:219], v29 offset:12288\n" \
M_"v[176:179], a[252:255], a[220:223], v[176:179]\n" \
"s_addk_i32 s84, 0x7c00\n" \
M_"v[172:175], a[184:187], a[156:159], v[172:175]\n" \
B_"v27, s[68:71], s84 offen offset:2048 lds\n" \
M_"v[172:175], a[248:251], a[220:223], v[172:175]\n" \
X_"a[230:231], v33 offset:4608\n" \
M_"v[168:171], a[180:183], a[156:159], v[168:171]\n" \
"s_addk_i32 s84, 0x7c00\n" \
M_"v[168:171], a[244:247], a[220:223], v[168:171]\n" \
B_"v27, s[68:71], s84 offen offset:3072 lds\n" \
M_"v[164:167], a[176:179], a[156:159], v[164:167]\n" \
X_"a[172:173], v35 offset:0\n" \
M_"v[164:167], a[240:243], a[220:223], v[164:167]\n" \
R_"a[156:159], v28 offset:14336\n" \
M_"v[84:87], a[176:179], a[136:139], v[84:87]\n" \
R_"a[220:223], v29 offset:14336\n" \
M_"v[84:87], a[240:243], a[200:203], v[84:87]\n" \
"s_add_u32 s89, s42, 0x0\n" \
"buffer_store_dwordx4 v[192:195], v20, s[80:83], s89 offen offset:2048 sc1\n" \
M_"v[88:91], a[180:183], a[136:139], v[88:91]\n" \
X_"a[174:175], v35 offset:512\n" \
M_"v[88:91], a[244:247], a[200:203], v[88:91]\n" \
X_"a[236:237], v35 offset:4096\n" \
M_"v[92:95], a[184:187], a[136:139], v[92:95]\n" \
X_"a[238:239], v35 offset:4608\n" \
M_"v[92:95], a[248:251], a[200:203], v[92:95]\n" \
"s_add_u32 m0, s30, 0x20000\n" \
"s_movk_i32 s84, 0x400\n" \
M_"v[96:99], a[188:191], a[136:139], v[96:99]\n" \
B_"v24, s[76:79], s84 offen offset:0 lds\n" \
M_"v[96:99], a[252:255], a[200:203], v[96:99]\n" \
R_"a[136:139], v28 offset:4096\n" \
M_"v[112:115], a[188:191], a[140:143], v[112:115]\n" \
R_"a[200:203], v29 offset:4096\n" \
M_"v[112:115], a[252:255], a[204:207], v[112:115]\n" \
X_"a[168:169], v34 offset:0\n" \
M_"v[108:111], a[184:187], a[140:143], v[108:111]\n" \
"s_movk_i32 s84, 0x2400\n" \
M_"v[108:111], a[248:251], a[204:207], v[108:111]\n" \
B_"v25, s[76:79], s84 offen offset:1024 lds\n" \
M_"v[104:107], a[180:183], a[140:143], v[104:107]\n" \
X_"a[170:171], v34 offset:512\n" \
M_"v[104:107], a[244:247], a[204:207], v[104:107]\n" \
X_"a[232:233], v34 offset:4096\n" \
M_"v[100:103], a[176:179], a[140:143], v[100:103]\n" \
"s_add_u32 s8, s6, 0x1000000\n" \
"s_addc_u32 s6, s9, 0\n" \
M_"v[100:103], a[240:243], a[204:207], v[100:103]\n" \
R_"a[140:143], v28 offset:6144\n" \
"s_and_b32 s9, s6, 0xffff\n" \
M_"v[52:55], a[176:179], a[128:131], v[52:55]\n" \
R_"a[204:207], v29 offset:6144\n" \
"s_lshl_b32 s6, s5, 16\n" \
M_"v[52:55], a[240:243], a[192:195], v[52:55]\n" \
"s_movk_i32 s84, 0x4400\n" \
M_"v[56:59], a[180:183], a[128:131], v[56:59]\n" \
B_"v24, s[76:79], s84 offen offset:2048 lds\n" \
M_"v[56:59], a[244:247], a[192:195], v[56:59]\n" \
"s_movk_i32 s84, 0x6400\n" \
"s_nop 0\n" \
M_"v[60:63], a[184:187], a[128:131], v[60:63]\n" \
B_"v25, s[76:79], s84 offen offset:3072 lds\n" \
M_"v[60:63], a[248:251], a[192:195], v[60:63]\n" \
X_"a[234:235], v34 offset:4608\n" \
"s_sub_i32 s10, 0x100000, s6\n" \
M_"v[64:67], a[188:191], a[128:131], v[64:67]\n" \
"s_mov_b32 s6, 0x8000\n" \
M_"v[64:67], a[252:255], a[192:195], v[64:67]\n" \
R_"a[128:131], v28 offset:0\n" \
M_"v[80:83], a[188:191], a[132:135], v[80:83]\n" \
R_"a[192:195], v29 offset:0\n" \
M_"v[80:83], a[252:255], a[196:199], v[80:83]\n" \
"s_cmp_lg_u32 s5, 0\n" \
"s_cselect_b32 s17, s11, s6\n" \
M_"v[76:79], a[184:187], a[132:135], v[76:79]\n" \
"s_lshl_b32 s6, s3, 1\n" \
"s_lshl_b32 s5, s5, 3\n" \
M_"v[76:79], a[248:251], a[196:199], v[76:79]\n" \
"s_and_b32 s6, s6, 4\n" \
"s_or_b32 s12, s5, s6\n" \
M_"v[72:75], a[180:183], a[132:135], v[72:75]\n" \
"s_sub_i32 s5, s13, s5\n" \
"s_or_b32 s5, s5, s6\n" \
M_"v[72:75], a[244:247], a[196:199], v[72:75]\n" \
"s_or_b32 s6, s12, s13\n" \
"s_lshl_b32 s4, s4, 2\n" \
M_"v[68:71], a[176:179], a[132:135], v[68:71]\n" \
"s_mulk_i32 s6, 0x92\n" \
"s_add_i32 s4, s4, 18\n" \
M_"v[68:71], a[240:243], a[196:199], v[68:71]\n" \
R_"a[132:135], v28 offset:2048\n" \
R_"a[196:199], v29 offset:2048\n" \
"s_waitcnt lgkmcnt(15)\n" \
M_"a[0:3], a[160:163], a[144:147], a[0:3]\n" \
"s_add_u32 m0, s30, 0x21000\n" \
"s_addk_i32 s84, 0x3000\n" \
M_"a[0:3], a[224:227], a[208:211], a[0:3]\n" \
B_"v24, s[76:79], s84 offen offset:0 lds\n" \
M_"a[4:7], a[164:167], a[144:147], a[4:7]\n" \
X_"a[176:177], v36 offset:0\n" \
"s_mulk_i32 s5, 0x92\n" \
M_"a[4:7], a[228:231], a[208:211], a[4:7]\n" \
X_"a[178:179], v36 offset:1024\n" \
"s_add_i32 s6, s4, s6\n" \
"s_waitcnt lgkmcnt(15)\n" \
M_"a[12:15], a[172:175], a[144:147], a[12:15]\n" \
X_"a[240:241], v36 offset:8192\n" \
"s_add_i32 s5, s5, s4\n" \
"s_waitcnt lgkmcnt(15)\n" \
M_"a[12:15], a[236:239], a[208:211], a[12:15]\n" \
"s_add_u32 s88, s42, 0x0\n" \
"buffer_store_dwordx4 v[196:199], v20, s[80:83], s88 offen offset:3072 sc1\n" \
"s_waitcnt lgkmcnt(11)\n" \
M_"a[8:11], a[144:147], a[168:171], a[8:11]\n" \
X_"a[242:243], v36 offset:9216\n" \
"s_lshl_b32 s4, s3, 11\n" \
"s_waitcnt lgkmcnt(8)\n" \
M_"a[8:11], a[208:211], a[232:235], a[8:11]\n" \
X_"a[180:181], v37 offset:0\n" \
"s_lshl_b32 s15, s13, 12\n" \
M_"a[24:27], a[148:151], a[168:171], a[24:27]\n" \
X_"a[182:183], v37 offset:1024\n" \
"s_lshl_b32 s16, s19, 14\n" \
M_"a[24:27], a[212:215], a[232:235], a[24:27]\n" \
X_"a[244:245], v37 offset:8192\n" \
"s_mul_i32 s19, s19, 0xe000\n" \
M_"a[28:31], a[172:175], a[148:151], a[28:31]\n" \
"s_mov_b32 s11, 0x20000\n" \
"s_or_b32 s12, s4, 0xc000\n" \
M_"a[28:31], a[236:239], a[212:215], a[28:31]\n" \
"s_addk_i32 s84, 0x2000\n" \
M_"a[20:23], a[164:167], a[148:151], a[20:23]\n" \
B_"v25, s[76:79], s84 offen offset:1024 lds\n" \
M_"a[20:23], a[228:231], a[212:215], a[20:23]\n" \
X_"a[246:247], v37 offset:9216\n" \
"s_lshl_b32 s4, s3, 8\n" \
M_"a[16:19], a[160:163], a[148:151], a[16:19]\n" \
X_"a[184:185], v38 offset:0\n" \
"s_or_b32 s22, s15, s16\n" \
M_"a[16:19], a[224:227], a[212:215], a[16:19]\n" \
X_"a[186:187], v38 offset:1024\n" \
"s_or_b32 s23, s19, s15\n" \
M_"a[32:35], a[160:163], a[152:155], a[32:35]\n" \
"s_addk_i32 s84, 0x2000\n" \
M_"a[32:35], a[224:227], a[216:219], a[32:35]\n" \
B_"v24, s[76:79], s84 offen offset:2048 lds\n" \
M_"a[36:39], a[164:167], a[152:155], a[36:39]\n" \
".fill (L_pb + 73440 - .) / 4, 4, 0xBF800000\n" \
"L_pp18:\n" \
"s_cbranch_execz L_pret\n" \
X_"a[248:249], v38 offset:8192\n" \
"s_lshl_b32 s60, s6, 10\n" \
M_"a[36:39], a[228:231], a[216:219], a[36:39]\n" \
X_"a[250:251], v38 offset:9216\n" \
"s_lshl_b32 s61, s5, 10\n" \
M_"a[44:47], a[172:175], a[152:155], a[44:47]\n" \
X_"a[188:189], v39 offset:0\n" \
"s_lshl_b32 s14, s14, 15\n" \
M_"a[44:47], a[236:239], a[216:219], a[44:47]\n" \
X_"a[190:191], v39 offset:1024\n" \
"s_or_b32 s20, s4, 0x26c00\n" \
M_"a[40:43], a[152:155], a[168:171], a[40:43]\n" \
"s_add_u32 s89, s42, 0x2000\n" \
"buffer_store_dwordx4 v[200:203], v20, s[80:83], s89 offen offset:0 sc1\n" \
M_"a[40:43], a[216:219], a[232:235], a[40:43]\n" \
X_"a[252:253], v39 offset:8192\n" \
"s_or_b32 s13, s22, 0x8000\n" \
M_"a[56:59], a[156:159], a[168:171], a[56:59]\n" \
X_"a[254:255], v39 offset:9216\n" \
"s_add_i32 s22, s22, 0x14000\n" \
M_"a[56:59], a[220:223], a[232:235], a[56:59]\n" \
"s_bitset1_b32 s23, 16\n" \
"s_or_b32 s24, s7, 0x5be8020\n" \
M_"a[60:63], a[172:175], a[156:159], a[60:63]\n" \
"s_and_b32 s57, s27, 0xffff\n" \
"s_mov_b32 s58, 0x6000000\n" \
M_"a[60:63], a[236:239], a[220:223], a[60:63]\n" \
"s_addk_i32 s84, 0x2000\n" \
M_"a[52:55], a[164:167], a[156:159], a[52:55]\n" \
B_"v25, s[76:79], s84 offen offset:3072 lds\n" \
M_"a[52:55], a[228:231], a[220:223], a[52:55]\n" \
"s_mov_b32 s56, s26\n" \
"s_mov_b32 s59, s11\n" \
M_"a[48:51], a[160:163], a[156:159], a[48:51]\n" \
"s_add_i32 s60, s60, 0x3000000\n" \
"s_add_i32 s61, s61, 0x411c000\n" \
M_"a[48:51], a[224:227], a[220:223], a[48:51]\n" \
"s_sub_i32 s19, 14, s18\n" \
"s_mov_b32 s21, 0x71800000\n" \
M_"a[64:67], a[164:167], a[140:143], a[64:67]\n" \
"s_mov_b32 s4, L_gsw-L_gtr\n" \
M_"a[64:67], a[228:231], a[204:207], a[64:67]\n" \
"s_cmp_eq_u32 s19, 0\n" \
"s_cselect_b32 s4, L_gs7-L_gtr, s4\n" \
M_"a[80:83], a[172:175], a[140:143], a[80:83]\n" \
"s_cmp_eq_u32 s16, 0\n" \
"s_cselect_b32 s4, 0, s4\n" \
M_"a[80:83], a[236:239], a[204:207], a[80:83]\n" \
"s_getpc_b64 s[28:29]\n" \
"L_gp1_%=:\n" \
"s_add_u32 s28, s28, L_gtr-L_gp1_%=\n" \
"s_addc_u32 s29, s29, 0\n" \
M_"a[68:71], a[140:143], a[168:171], a[68:71]\n" \
"s_add_u32 s28, s28, s4\n" \
"s_addc_u32 s29, s29, 0\n" \
"s_waitcnt vmcnt(11) lgkmcnt(0)\n" \
M_"a[68:71], a[204:207], a[232:235], a[68:71]\n" \
"s_barrier\n" \
M_"v[116:119], a[176:179], a[144:147], v[116:119]\n" \
"s_add_u32 m0, s40, 0x0\n" \
"s_nop 0\n" \
"s_add_u32 s85, s39, 0x10000\n" \
M_"v[116:119], a[240:243], a[208:211], v[116:119]\n" \
B_"v20, s[80:83], s85 offen sc1 lds\n" \
M_"v[120:123], a[180:183], a[144:147], v[120:123]\n" \
X_"a[160:161], v32 offset:32768\n" \
"v_accvgpr_write_b32 a96, 0\n" \
M_"v[120:123], a[244:247], a[208:211], v[120:123]\n" \
X_"a[162:163], v32 offset:33280\n" \
"v_accvgpr_write_b32 a97, 0\n" \
M_"v[124:127], a[184:187], a[144:147], v[124:127]\n" \
"v_accvgpr_write_b32 a98, 0\n" \
"v_accvgpr_write_b32 a99, 0\n" \
M_"v[124:127], a[248:251], a[208:211], v[124:127]\n" \
"v_accvgpr_write_b32 a100, 0\n" \
"v_accvgpr_write_b32 a101, 0\n" \
M_"v[128:131], a[188:191], a[144:147], v[128:131]\n" \
X_"a[224:225], v32 offset:36864\n" \
"v_accvgpr_write_b32 a102, 0\n" \
M_"v[128:131], a[252:255], a[208:211], v[128:131]\n" \
R_"a[144:147], v28 offset:40960\n" \
"v_accvgpr_write_b32 a103, 0\n" \
M_"v[144:147], a[188:191], a[148:151], v[144:147]\n" \
R_"a[208:211], v29 offset:40960\n" \
"v_accvgpr_write_b32 a112, 0\n" \
M_"v[144:147], a[252:255], a[212:215], v[144:147]\n" \
"s_add_u32 m0, s40, 0x1000\n" \
"s_add_u32 s85, s39, 0x11000\n" \
M_"v[140:143], a[184:187], a[148:151], v[140:143]\n" \
B_"v20, s[80:83], s85 offen sc1 lds\n" \
M_"v[140:143], a[248:251], a[212:215], v[140:143]\n" \
X_"a[226:227], v32 offset:37376\n" \
"v_accvgpr_write_b32 a113, 0\n" \
M_"v[136:139], a[180:183], a[148:151], v[136:139]\n" \
"v_accvgpr_write_b32 a114, 0\n" \
"v_accvgpr_write_b32 a115, 0\n" \
M_"v[136:139], a[244:247], a[212:215], v[136:139]\n" \
X_"a[164:165], v33 offset:32768\n" \
"v_accvgpr_write_b32 a120, 0\n" \
M_"v[132:135], a[176:179], a[148:151], v[132:135]\n" \
"v_accvgpr_write_b32 a121, 0\n" \
"v_accvgpr_write_b32 a122, 0\n" \
M_"v[132:135], a[240:243], a[212:215], v[132:135]\n" \
R_"a[148:151], v28 offset:43008\n" \
"v_accvgpr_write_b32 a123, 0\n" \
M_"v[148:151], a[176:179], a[152:155], v[148:151]\n" \
R_"a[212:215], v29 offset:43008\n" \
"v_mov_b32_e32 v180, 0x3f803f80\n" \
M_"v[148:151], a[240:243], a[216:219], v[148:151]\n" \
"s_add_u32 m0, s40, 0x3000\n" \
"s_movk_i32 s84, 0x400\n" \
M_"v[152:155], a[180:183], a[152:155], v[152:155]\n" \
B_"v21, s[76:79], s84 offen offset:0 lds\n" \
M_"v[152:155], a[244:247], a[216:219], v[152:155]\n" \
X_"a[166:167], v33 offset:33280\n" \
"s_mov_b32 s6, 0x33333333\n" \
M_"v[156:159], a[184:187], a[152:155], v[156:159]\n" \
X_"a[228:229], v33 offset:36864\n" \
"s_mov_b32 s7, 0xcccccccc\n" \
M_"v[156:159], a[248:251], a[216:219], v[156:159]\n" \
"s_add_u32 m0, s31, 0x0\n" \
"s_mov_b32 s84, 0x400000\n" \
M_"v[160:163], a[188:191], a[152:155], v[160:163]\n" \
B_"v26, s[68:71], s84 offen offset:0 lds\n" \
M_"v[160:163], a[252:255], a[216:219], v[160:163]\n" \
R_"a[152:155], v28 offset:45056\n" \
"v_cndmask_b32_e64 v181, 0, v180, s[6:7]\n" \
M_"v[176:179], a[188:191], a[156:159], v[176:179]\n" \
R_"a[216:219], v29 offset:45056\n" \
"v_accvgpr_write_b32 a104, v181\n" \
M_"v[176:179], a[252:255], a[220:223], v[176:179]\n" \
X_"a[230:231], v33 offset:37376\n" \
"v_accvgpr_write_b32 a105, v181\n" \
M_"v[172:175], a[184:187], a[156:159], v[172:175]\n" \
X_"a[172:173], v35 offset:32768\n" \
"v_accvgpr_write_b32 a106, v181\n" \
M_"v[172:175], a[248:251], a[220:223], v[172:175]\n" \
"v_accvgpr_write_b32 a107, v181\n" \
"s_mov_b32 s6, 0x11111111\n" \
M_"v[168:171], a[180:183], a[156:159], v[168:171]\n" \
"s_add_u32 s88, s42, 0x2000\n" \
"buffer_store_dwordx4 v[204:207], v20, s[80:83], s88 offen offset:1024 sc1\n" \
M_"v[168:171], a[244:247], a[220:223], v[168:171]\n" \
X_"a[174:175], v35 offset:33280\n" \
"s_mov_b32 s7, 0x11111111\n" \
M_"v[164:167], a[176:179], a[156:159], v[164:167]\n" \
"v_cndmask_b32_e64 v181, 0, v180, s[6:7]\n" \
"v_accvgpr_write_b32 a108, v181\n" \
M_"v[164:167], a[240:243], a[220:223], v[164:167]\n" \
R_"a[156:159], v28 offset:47104\n" \
"v_accvgpr_write_b32 a109, v181\n" \
M_"v[84:87], a[176:179], a[136:139], v[84:87]\n" \
R_"a[220:223], v29 offset:47104\n" \
"v_accvgpr_write_b32 a110, v181\n" \
M_"v[84:87], a[240:243], a[200:203], v[84:87]\n" \
X_"a[236:237], v35 offset:36864\n" \
"v_accvgpr_write_b32 a111, v181\n" \
M_"v[88:91], a[180:183], a[136:139], v[88:91]\n" \
"s_mov_b32 s6, 0x44444444\n" \
"s_mov_b32 s7, 0x44444444\n" \
M_"v[88:91], a[244:247], a[200:203], v[88:91]\n" \
"s_addk_i32 s84, 0x7c00\n" \
"s_nop 0\n" \
M_"v[92:95], a[184:187], a[136:139], v[92:95]\n" \
B_"v26, s[68:71], s84 offen offset:1024 lds\n" \
M_"v[92:95], a[248:251], a[200:203], v[92:95]\n" \
X_"a[238:239], v35 offset:37376\n" \
"v_cndmask_b32_e64 v181, 0, v180, s[6:7]\n" \
M_"v[96:99], a[188:191], a[136:139], v[96:99]\n" \
"v_accvgpr_write_b32 a116, v181\n" \
"v_accvgpr_write_b32 a117, v181\n" \
M_"v[96:99], a[252:255], a[200:203], v[96:99]\n" \
R_"a[136:139], v28 offset:36864\n" \
"v_accvgpr_write_b32 a118, v181\n" \
M_"v[112:115], a[188:191], a[140:143], v[112:115]\n" \
R_"a[200:203], v29 offset:36864\n" \
"v_accvgpr_write_b32 a119, v181\n" \
M_"v[112:115], a[252:255], a[204:207], v[112:115]\n" \
X_"a[168:169], v34 offset:32768\n" \
"v_bfe_i32 v181, v20, 4, 1\n" \
M_"v[108:111], a[184:187], a[140:143], v[108:111]\n" \
"s_addk_i32 s84, 0x7c00\n" \
"s_nop 0\n" \
M_"v[108:111], a[248:251], a[204:207], v[108:111]\n" \
B_"v27, s[68:71], s84 offen offset:2048 lds\n" \
M_"v[104:107], a[180:183], a[140:143], v[104:107]\n" \
X_"a[170:171], v34 offset:33280\n" \
"v_and_b32_e32 v181, 0xaaaaaaaa, v181\n" \
M_"v[104:107], a[244:247], a[204:207], v[104:107]\n" \
X_"a[232:233], v34 offset:36864\n" \
"v_or_b32_e32 v181, 0x44444444, v181\n" \
M_"v[100:103], a[176:179], a[140:143], v[100:103]\n" \
"v_accvgpr_write_b32 a126, v181\n" \
"v_bfe_u32 v180, v20, 5, 3\n" \
M_"v[100:103], a[240:243], a[204:207], v[100:103]\n" \
R_"a[140:143], v28 offset:38912\n" \
"v_bfe_u32 v181, v20, 8, 2\n" \
M_"v[52:55], a[176:179], a[128:131], v[52:55]\n" \
R_"a[204:207], v29 offset:38912\n" \
"v_lshlrev_b32_e64 v181, 1, v181\n" \
M_"v[52:55], a[240:243], a[192:195], v[52:55]\n" \
"s_addk_i32 s84, 0x7c00\n" \
M_"v[56:59], a[180:183], a[128:131], v[56:59]\n" \
B_"v27, s[68:71], s84 offen offset:3072 lds\n" \
M_"v[56:59], a[244:247], a[192:195], v[56:59]\n" \
X_"a[234:235], v34 offset:37376\n" \
"v_sub_u32_e32 v180, v180, v181\n" \
M_"v[60:63], a[184:187], a[128:131], v[60:63]\n" \
"v_and_b32_e64 v181, 16, v20\n" \
"v_mov_b32_e32 v182, 0x3f80\n" \
M_"v[60:63], a[248:251], a[192:195], v[60:63]\n" \
"v_lshlrev_b32_e32 v181, v181, v182\n" \
"v_cmp_eq_u32_e32 vcc, 0, v180\n" \
M_"v[64:67], a[188:191], a[128:131], v[64:67]\n" \
"v_cmp_eq_u32_e64 s[6:7], 1, v180\n" \
"v_cndmask_b32_e64 v182, 0, v181, vcc\n" \
M_"v[64:67], a[252:255], a[192:195], v[64:67]\n" \
R_"a[128:131], v28 offset:32768\n" \
"v_cndmask_b32_e64 v183, 0, v181, s[6:7]\n" \
M_"v[80:83], a[188:191], a[132:135], v[80:83]\n" \
R_"a[192:195], v29 offset:32768\n" \
"v_accvgpr_write_b32 a124, v182\n" \
M_"v[80:83], a[252:255], a[196:199], v[80:83]\n" \
"v_accvgpr_write_b32 a125, v183\n" \
M_"v[76:79], a[184:187], a[132:135], v[76:79]\n" \
M_"v[76:79], a[248:251], a[196:199], v[76:79]\n" \
M_"v[72:75], a[180:183], a[132:135], v[72:75]\n" \
M_"v[72:75], a[244:247], a[196:199], v[72:75]\n" \
M_"v[68:71], a[176:179], a[132:135], v[68:71]\n" \
M_"v[68:71], a[240:243], a[196:199], v[68:71]\n" \
R_"a[132:135], v28 offset:34816\n" \
R_"a[196:199], v29 offset:34816\n" \
"s_waitcnt lgkmcnt(15)\n" \
"s_nop 0\n" \
M_"a[0:3], a[160:163], a[144:147], a[0:3]\n" \
"s_add_u32 s89, s42, 0x2000\n" \
"buffer_store_dwordx4 v[208:211], v20, s[80:83], s89 offen offset:2048 sc1\n" \
M_"a[0:3], a[224:227], a[208:211], a[0:3]\n" \
X_"a[176:177], v36 offset:32768\n" \
M_"a[4:7], a[164:167], a[144:147], a[4:7]\n" \
X_"a[178:179], v36 offset:33792\n" \
M_"a[4:7], a[228:231], a[208:211], a[4:7]\n" \
X_"a[240:241], v36 offset:40960\n" \
M_"a[12:15], a[172:175], a[144:147], a[12:15]\n" \
X_"a[242:243], v36 offset:41984\n" \
"s_waitcnt lgkmcnt(15)\n" \
M_"a[12:15], a[236:239], a[208:211], a[12:15]\n" \
"s_waitcnt lgkmcnt(12)\n" \
M_"a[8:11], a[144:147], a[168:171], a[8:11]\n" \
X_"a[180:181], v37 offset:32768\n" \
"s_waitcnt lgkmcnt(9)\n" \
"s_nop 0\n" \
M_"a[8:11], a[208:211], a[232:235], a[8:11]\n" \
M_"a[24:27], a[148:151], a[168:171], a[24:27]\n" \
X_"a[182:183], v37 offset:33792\n" \
M_"a[24:27], a[212:215], a[232:235], a[24:27]\n" \
X_"a[244:245], v37 offset:40960\n" \
M_"a[28:31], a[172:175], a[148:151], a[28:31]\n" \
M_"a[28:31], a[236:239], a[212:215], a[28:31]\n" \
X_"a[246:247], v37 offset:41984\n" \
M_"a[20:23], a[164:167], a[148:151], a[20:23]\n" \
M_"a[20:23], a[228:231], a[212:215], a[20:23]\n" \
X_"a[184:185], v38 offset:32768\n" \
M_"a[16:19], a[160:163], a[148:151], a[16:19]\n" \
X_"a[186:187], v38 offset:33792\n" \
M_"a[16:19], a[224:227], a[212:215], a[16:19]\n" \
M_"a[32:35], a[160:163], a[152:155], a[32:35]\n" \
"s_add_u32 s88, s42, 0x2000\n" \
"buffer_store_dwordx4 v[212:215], v20, s[80:83], s88 offen offset:3072 sc1\n" \
M_"a[32:35], a[224:227], a[216:219], a[32:35]\n" \
X_"a[248:249], v38 offset:40960\n" \
M_"a[36:39], a[164:167], a[152:155], a[36:39]\n" \
"v_lshlrev_b32_e32 v45, 3, v20\n" \
"v_add_u32_e32 v45, s30, v45\n" \
M_"a[36:39], a[228:231], a[216:219], a[36:39]\n" \
"s_getpc_b64 s[86:87]\n" \
"L_ct1_%=:\n" \
"s_add_u32 s86, s86, L_ap0-L_ct1_%=\n" \
"s_addc_u32 s87, s87, 0\n" \
"s_add_u32 m0, s40, 0x2000\n" \
M_"a[44:47], a[172:175], a[152:155], a[44:47]\n" \
"global_load_lds_dword v45, s[86:87]\n" \
M_"a[44:47], a[236:239], a[216:219], a[44:47]\n" \
X_"a[250:251], v38 offset:41984\n" \
M_"a[40:43], a[152:155], a[168:171], a[40:43]\n" \
X_"a[188:189], v39 offset:32768\n" \
M_"a[40:43], a[216:219], a[232:235], a[40:43]\n" \
X_"a[190:191], v39 offset:33792\n" \
M_"a[56:59], a[156:159], a[168:171], a[56:59]\n" \
X_"a[252:253], v39 offset:40960\n" \
M_"a[56:59], a[220:223], a[232:235], a[56:59]\n" \
X_"a[254:255], v39 offset:41984\n" \
M_"a[60:63], a[172:175], a[156:159], a[60:63]\n" \
M_"a[60:63], a[236:239], a[220:223], a[60:63]\n" \
M_"a[52:55], a[164:167], a[156:159], a[52:55]\n" \
M_"a[52:55], a[228:231], a[220:223], a[52:55]\n" \
M_"a[48:51], a[160:163], a[156:159], a[48:51]\n" \
M_"a[48:51], a[224:227], a[220:223], a[48:51]\n" \
M_"a[64:67], a[164:167], a[140:143], a[64:67]\n" \
M_"a[64:67], a[228:231], a[204:207], a[64:67]\n" \
M_"a[80:83], a[172:175], a[140:143], a[80:83]\n" \
M_"a[80:83], a[236:239], a[204:207], a[80:83]\n" \
M_"a[68:71], a[140:143], a[168:171], a[68:71]\n" \
"s_waitcnt vmcnt(3) lgkmcnt(0)\n" \
M_"a[68:71], a[204:207], a[232:235], a[68:71]\n" \
"s_barrier\n" \
"v_xor_b32_e32 v28, 0x20000, v28\n" \
"v_xor_b32_e32 v29, 0x20000, v29\n" \
M_"v[116:119], a[176:179], a[144:147], v[116:119]\n" \
X_"a[160:161], v32 offset:0\n" \
"v_accvgpr_read_b32 v184, a0\n" \
M_"v[116:119], a[240:243], a[208:211], v[116:119]\n" \
"v_accvgpr_read_b32 v185, a1\n" \
"v_accvgpr_read_b32 v186, a2\n" \
M_"v[120:123], a[180:183], a[144:147], v[120:123]\n" \
"v_accvgpr_read_b32 v187, a3\n" \
"v_accvgpr_read_b32 v188, a4\n" \
M_"v[120:123], a[244:247], a[208:211], v[120:123]\n" \
X_"a[162:163], v32 offset:512\n" \
"v_accvgpr_read_b32 v189, a5\n" \
M_"v[124:127], a[184:187], a[144:147], v[124:127]\n" \
"v_accvgpr_read_b32 v190, a6\n" \
"v_accvgpr_read_b32 v191, a7\n" \
M_"v[124:127], a[248:251], a[208:211], v[124:127]\n" \
"v_accvgpr_read_b32 v196, a12\n" \
"v_accvgpr_read_b32 v197, a13\n" \
M_"v[128:131], a[188:191], a[144:147], v[128:131]\n" \
X_"a[224:225], v32 offset:4096\n" \
"v_accvgpr_read_b32 v198, a14\n" \
M_"v[128:131], a[252:255], a[208:211], v[128:131]\n" \
R_"a[144:147], v28 offset:8192\n" \
"v_accvgpr_read_b32 v199, a15\n" \
M_"v[144:147], a[188:191], a[148:151], v[144:147]\n" \
R_"a[208:211], v29 offset:8192\n" \
"v_accvgpr_read_b32 v192, a8\n" \
M_"v[144:147], a[252:255], a[212:215], v[144:147]\n" \
X_"a[226:227], v32 offset:4608\n" \
"v_accvgpr_read_b32 v193, a9\n" \
M_"v[140:143], a[184:187], a[148:151], v[140:143]\n" \
"v_accvgpr_read_b32 v194, a10\n" \
"v_accvgpr_read_b32 v195, a11\n" \
M_"v[140:143], a[248:251], a[212:215], v[140:143]\n" \
"v_accvgpr_read_b32 v208, a24\n" \
"v_accvgpr_read_b32 v209, a25\n" \
M_"v[136:139], a[180:183], a[148:151], v[136:139]\n" \
"v_accvgpr_read_b32 v210, a26\n" \
"v_accvgpr_read_b32 v211, a27\n" \
M_"v[136:139], a[244:247], a[212:215], v[136:139]\n" \
X_"a[164:165], v33 offset:0\n" \
"v_accvgpr_read_b32 v212, a28\n" \
M_"v[132:135], a[176:179], a[148:151], v[132:135]\n" \
"v_accvgpr_read_b32 v213, a29\n" \
"v_accvgpr_read_b32 v214, a30\n" \
M_"v[132:135], a[240:243], a[212:215], v[132:135]\n" \
R_"a[148:151], v28 offset:10240\n" \
"v_accvgpr_read_b32 v215, a31\n" \
M_"v[148:151], a[176:179], a[152:155], v[148:151]\n" \
R_"a[212:215], v29 offset:10240\n" \
"v_accvgpr_read_b32 v204, a20\n" \
M_"v[148:151], a[240:243], a[216:219], v[148:151]\n" \
X_"a[166:167], v33 offset:512\n" \
"v_accvgpr_read_b32 v205, a21\n" \
M_"v[152:155], a[180:183], a[152:155], v[152:155]\n" \
"v_accvgpr_read_b32 v206, a22\n" \
"v_accvgpr_read_b32 v207, a23\n" \
M_"v[152:155], a[244:247], a[216:219], v[152:155]\n" \
X_"a[228:229], v33 offset:4096\n" \
"v_accvgpr_read_b32 v200, a16\n" \
M_"v[156:159], a[184:187], a[152:155], v[156:159]\n" \
"v_accvgpr_read_b32 v201, a17\n" \
"v_accvgpr_read_b32 v202, a18\n" \
M_"v[156:159], a[248:251], a[216:219], v[156:159]\n" \
"v_accvgpr_read_b32 v203, a19\n" \
"v_accvgpr_read_b32 v216, a32\n" \
M_"v[160:163], a[188:191], a[152:155], v[160:163]\n" \
X_"a[230:231], v33 offset:4608\n" \
"v_accvgpr_read_b32 v217, a33\n" \
M_"v[160:163], a[252:255], a[216:219], v[160:163]\n" \
R_"a[152:155], v28 offset:12288\n" \
"v_accvgpr_read_b32 v218, a34\n" \
M_"v[176:179], a[188:191], a[156:159], v[176:179]\n" \
R_"a[216:219], v29 offset:12288\n" \
"v_accvgpr_read_b32 v219, a35\n" \
M_"v[176:179], a[252:255], a[220:223], v[176:179]\n" \
"v_accvgpr_read_b32 v220, a36\n" \
"v_accvgpr_read_b32 v221, a37\n" \
M_"v[172:175], a[184:187], a[156:159], v[172:175]\n" \
X_"a[172:173], v35 offset:0\n" \
"v_accvgpr_read_b32 v222, a38\n" \
M_"v[172:175], a[248:251], a[220:223], v[172:175]\n" \
"v_accvgpr_read_b32 v223, a39\n" \
"v_accvgpr_read_b32 v228, a44\n" \
M_"v[168:171], a[180:183], a[156:159], v[168:171]\n" \
"v_accvgpr_read_b32 v229, a45\n" \
"v_accvgpr_read_b32 v230, a46\n" \
M_"v[168:171], a[244:247], a[220:223], v[168:171]\n" \
X_"a[174:175], v35 offset:512\n" \
"v_accvgpr_read_b32 v231, a47\n" \
M_"v[164:167], a[176:179], a[156:159], v[164:167]\n" \
"v_accvgpr_read_b32 v224, a40\n" \
"v_accvgpr_read_b32 v225, a41\n" \
M_"v[164:167], a[240:243], a[220:223], v[164:167]\n" \
R_"a[156:159], v28 offset:14336\n" \
"v_accvgpr_read_b32 v226, a42\n" \
M_"v[84:87], a[176:179], a[136:139], v[84:87]\n" \
R_"a[220:223], v29 offset:14336\n" \
"v_accvgpr_read_b32 v227, a43\n" \
M_"v[84:87], a[240:243], a[200:203], v[84:87]\n" \
X_"a[236:237], v35 offset:4096\n" \
"v_accvgpr_read_b32 v240, a56\n" \
M_"v[88:91], a[180:183], a[136:139], v[88:91]\n" \
"v_accvgpr_read_b32 v241, a57\n" \
"v_accvgpr_read_b32 v242, a58\n" \
M_"v[88:91], a[244:247], a[200:203], v[88:91]\n" \
X_"a[238:239], v35 offset:4608\n" \
"v_accvgpr_read_b32 v243, a59\n" \
M_"v[92:95], a[184:187], a[136:139], v[92:95]\n" \
"v_accvgpr_read_b32 v244, a60\n" \
"v_accvgpr_read_b32 v245, a61\n" \
M_"v[92:95], a[248:251], a[200:203], v[92:95]\n" \
"v_accvgpr_read_b32 v246, a62\n" \
"v_accvgpr_read_b32 v247, a63\n" \
M_"v[96:99], a[188:191], a[136:139], v[96:99]\n" \
"v_accvgpr_read_b32 v236, a52\n" \
"v_accvgpr_read_b32 v237, a53\n" \
M_"v[96:99], a[252:255], a[200:203], v[96:99]\n" \
R_"a[136:139], v28 offset:4096\n" \
"v_accvgpr_read_b32 v238, a54\n" \
M_"v[112:115], a[188:191], a[140:143], v[112:115]\n" \
R_"a[200:203], v29 offset:4096\n" \
"v_accvgpr_read_b32 v239, a55\n" \
M_"v[112:115], a[252:255], a[204:207], v[112:115]\n" \
X_"a[168:169], v34 offset:0\n" \
"v_accvgpr_read_b32 v232, a48\n" \
M_"v[108:111], a[184:187], a[140:143], v[108:111]\n" \
X_"a[170:171], v34 offset:512\n" \
"v_accvgpr_read_b32 v233, a49\n" \
M_"v[108:111], a[248:251], a[204:207], v[108:111]\n" \
"v_accvgpr_read_b32 v234, a50\n" \
"v_accvgpr_read_b32 v235, a51\n" \
M_"v[104:107], a[180:183], a[140:143], v[104:107]\n" \
"v_accvgpr_read_b32 v180, a64\n" \
"v_accvgpr_read_b32 v181, a65\n" \
M_"v[104:107], a[244:247], a[204:207], v[104:107]\n" \
X_"a[232:233], v34 offset:4096\n" \
"v_accvgpr_read_b32 v182, a66\n" \
M_"v[100:103], a[176:179], a[140:143], v[100:103]\n" \
"v_accvgpr_read_b32 v183, a67\n" \
"v_accvgpr_read_b32 v48, a80\n" \
M_"v[100:103], a[240:243], a[204:207], v[100:103]\n" \
R_"a[140:143], v28 offset:6144\n" \
"v_accvgpr_read_b32 v49, a81\n" \
M_"v[52:55], a[176:179], a[128:131], v[52:55]\n" \
R_"a[204:207], v29 offset:6144\n" \
"v_accvgpr_read_b32 v50, a82\n" \
M_"v[52:55], a[240:243], a[192:195], v[52:55]\n" \
X_"a[234:235], v34 offset:4608\n" \
"v_accvgpr_read_b32 v51, a83\n" \
M_"v[56:59], a[180:183], a[128:131], v[56:59]\n" \
"v_accvgpr_read_b32 v252, a68\n" \
"v_accvgpr_read_b32 v253, a69\n" \
M_"v[56:59], a[244:247], a[192:195], v[56:59]\n" \
"v_accvgpr_read_b32 v254, a70\n" \
"v_accvgpr_read_b32 v255, a71\n" \
M_"v[60:63], a[184:187], a[128:131], v[60:63]\n" \
"v_lshrrev_b32_e64 v24, 9, v20\n" \
"v_and_b32_e32 v26, 0x1ff, v20\n" \
M_"v[60:63], a[248:251], a[192:195], v[60:63]\n" \
"v_lshl_add_u32 v24, v24, 3, v26\n" \
"v_lshrrev_b32_e64 v25, 8, v20\n" \
M_"v[64:67], a[188:191], a[128:131], v[64:67]\n" \
"v_mul_u32_u24_e32 v25, 0x24700, v25\n" \
"v_add_u32_e64 v25, v25, v20\n" \
M_"v[64:67], a[252:255], a[192:195], v[64:67]\n" \
R_"a[128:131], v28 offset:0\n" \
"v_lshrrev_b32_e64 v26, 2, v20\n" \
M_"v[80:83], a[188:191], a[132:135], v[80:83]\n" \
R_"a[192:195], v29 offset:0\n" \
"v_add_u32_e64 v26, s45, v26\n" \
M_"v[80:83], a[252:255], a[196:199], v[80:83]\n" \
"v_add_u32_e32 v27, s46, v20\n" \
"v_add_u32_e32 v21, s43, v27\n" \
M_"v[76:79], a[184:187], a[132:135], v[76:79]\n" \
M_"v[76:79], a[248:251], a[196:199], v[76:79]\n" \
M_"v[72:75], a[180:183], a[132:135], v[72:75]\n" \
M_"v[72:75], a[244:247], a[196:199], v[72:75]\n" \
M_"v[68:71], a[176:179], a[132:135], v[68:71]\n" \
M_"v[68:71], a[240:243], a[196:199], v[68:71]\n" \
R_"a[132:135], v28 offset:2048\n" \
R_"a[196:199], v29 offset:2048\n" \
"s_waitcnt lgkmcnt(15)\n" \
"s_nop 0\n" \
M_"v[184:187], a[160:163], a[144:147], v[184:187]\n" \
"v_bfe_u32 v44, v20, 2, 5\n" \
X_"a[176:177], v36 offset:0\n" \
M_"v[184:187], a[224:227], a[208:211], v[184:187]\n" \
"buffer_load_dword v44, v44, s[80:83], s38 offen offset:0 sc1\n" \
M_"v[188:191], a[164:167], a[144:147], v[188:191]\n" \
X_"a[178:179], v36 offset:1024\n" \
M_"v[188:191], a[228:231], a[208:211], v[188:191]\n" \
X_"a[240:241], v36 offset:8192\n" \
M_"v[196:199], a[172:175], a[144:147], v[196:199]\n" \
X_"a[242:243], v36 offset:9216\n" \
"s_waitcnt lgkmcnt(15)\n" \
M_"v[196:199], a[236:239], a[208:211], v[196:199]\n" \
"s_waitcnt lgkmcnt(12)\n" \
M_"v[192:195], a[144:147], a[168:171], v[192:195]\n" \
X_"a[180:181], v37 offset:0\n" \
"s_waitcnt lgkmcnt(9)\n" \
"s_nop 0\n" \
M_"v[192:195], a[208:211], a[232:235], v[192:195]\n" \
M_"v[208:211], a[148:151], a[168:171], v[208:211]\n" \
X_"a[182:183], v37 offset:1024\n" \
M_"v[208:211], a[212:215], a[232:235], v[208:211]\n" \
X_"a[244:245], v37 offset:8192\n" \
M_"v[212:215], a[172:175], a[148:151], v[212:215]\n" \
M_"v[212:215], a[236:239], a[212:215], v[212:215]\n" \
X_"a[246:247], v37 offset:9216\n" \
C_"v188, v188, v189\n" \
M_"v[204:207], a[164:167], a[148:151], v[204:207]\n" \
C_"v189, v190, v191\n" \
C_"v190, v198, v199\n" \
M_"v[204:207], a[228:231], a[212:215], v[204:207]\n" \
X_"a[184:185], v38 offset:0\n" \
"v_mov_b32_e32 v191, 0x3f80\n" \
M_"v[200:203], a[160:163], a[148:151], v[200:203]\n" \
X_"a[186:187], v38 offset:1024\n" \
C_"v184, v184, v185\n" \
M_"v[200:203], a[224:227], a[212:215], v[200:203]\n" \
C_"v185, v186, v187\n" \
C_"v186, v196, v197\n" \
M_"v[216:219], a[160:163], a[152:155], v[216:219]\n" \
X_"a[248:249], v38 offset:8192\n" \
"v_mov_b32_e64 v187, 0\n" \
M_"v[216:219], a[224:227], a[216:219], v[216:219]\n" \
X_"a[250:251], v38 offset:9216\n" \
M_"v[220:223], a[164:167], a[152:155], v[220:223]\n" \
M_"v[220:223], a[228:231], a[216:219], v[220:223]\n" \
".fill (L_pb + 77536 - .) / 4, 4, 0xBF800000\n" \
"L_pp19:\n" \
"s_cbranch_execz L_pret\n" \
"s_nop 0\n" \
X_"a[188:189], v39 offset:0\n" \
C_"v204, v204, v205\n" \
M_"v[228:231], a[172:175], a[152:155], v[228:231]\n" \
C_"v205, v206, v207\n" \
C_"v206, v214, v215\n" \
M_"v[228:231], a[236:239], a[216:219], v[228:231]\n" \
X_"a[190:191], v39 offset:1024\n" \
"v_mov_b32_e32 v207, 0x3f80\n" \
M_"v[224:227], a[152:155], a[168:171], v[224:227]\n" \
X_"a[252:253], v39 offset:8192\n" \
C_"v200, v200, v201\n" \
M_"v[224:227], a[216:219], a[232:235], v[224:227]\n" \
C_"v201, v202, v203\n" \
C_"v202, v212, v213\n" \
M_"v[240:243], a[156:159], a[168:171], v[240:243]\n" \
X_"a[254:255], v39 offset:9216\n" \
"v_mov_b32_e64 v203, 0\n" \
M_"v[240:243], a[220:223], a[232:235], v[240:243]\n" \
M_"v[244:247], a[172:175], a[156:159], v[244:247]\n" \
M_"v[244:247], a[236:239], a[220:223], v[244:247]\n" \
C_"v220, v220, v221\n" \
C_"v221, v222, v223\n" \
M_"v[236:239], a[164:167], a[156:159], v[236:239]\n" \
C_"v222, v230, v231\n" \
"v_mov_b32_e32 v223, 0x3f80\n" \
M_"v[236:239], a[228:231], a[220:223], v[236:239]\n" \
C_"v216, v216, v217\n" \
C_"v217, v218, v219\n" \
M_"v[232:235], a[160:163], a[156:159], v[232:235]\n" \
C_"v218, v228, v229\n" \
"v_mov_b32_e64 v219, 0\n" \
M_"v[232:235], a[224:227], a[220:223], v[232:235]\n" \
M_"v[180:183], a[164:167], a[140:143], v[180:183]\n" \
M_"v[180:183], a[228:231], a[204:207], v[180:183]\n" \
M_"v[48:51], a[172:175], a[140:143], v[48:51]\n" \
M_"v[48:51], a[236:239], a[204:207], v[48:51]\n" \
C_"v236, v236, v237\n" \
C_"v237, v238, v239\n" \
M_"v[252:255], a[140:143], a[168:171], v[252:255]\n" \
C_"v238, v246, v247\n" \
"v_mov_b32_e32 v239, 0x3f80\n" \
M_"v[252:255], a[204:207], a[232:235], v[252:255]\n" \
C_"v232, v232, v233\n" \
C_"v233, v234, v235\n" \
"s_waitcnt vmcnt(0) lgkmcnt(0)\n" \
"s_barrier\n" \
"v_mov_b32_e32 v45, s62\n" \
"s_mov_b64 exec, 1\n" \
"buffer_store_dword v45, off, s[80:83], s37 offset:32 sc1\n" \
"s_mov_b64 exec, -1\n" \
"v_cmp_ne_u32_e32 vcc, s62, v44\n" \
"s_nop 4\n" \
"s_cbranch_vccnz L_poll161_%=\n" \
"L_pollr161_%=:\n" \
M_"v[116:119], a[176:179], a[144:147], v[116:119]\n" \
"s_mov_b32 s73, s50\n" \
"s_sub_u32 m0, s31, 0x10000\n" \
M_"v[116:119], a[240:243], a[208:211], v[116:119]\n" \
B_"v20, s[80:83], s73 offen offset:0 sc1 lds\n" \
M_"v[120:123], a[180:183], a[144:147], v[120:123]\n" \
"s_add_u32 s74, s50, 0x4000\n" \
"s_add_u32 s75, s50, 0x8000\n" \
M_"v[120:123], a[244:247], a[208:211], v[120:123]\n" \
"s_mov_b32 s70, 0x3e0293ee\n" \
"s_mov_b32 s76, s52\n" \
M_"v[124:127], a[184:187], a[144:147], v[124:127]\n" \
"v_mul_f32_e32 v116, s70, v116\n" \
"v_mul_f32_e32 v117, s70, v117\n" \
M_"v[124:127], a[248:251], a[208:211], v[124:127]\n" \
B_"v20, s[80:83], s73 offen offset:1024 sc1 lds\n" \
M_"v[128:131], a[188:191], a[144:147], v[128:131]\n" \
"v_mul_f32_e32 v118, s70, v118\n" \
"v_mul_f32_e32 v119, s70, v119\n" \
M_"v[128:131], a[252:255], a[208:211], v[128:131]\n" \
C_"v116, v116, v117\n" \
"v_mul_f32_e64 v120, s70, v120\n" \
M_"v[144:147], a[188:191], a[148:151], v[144:147]\n" \
"v_mul_f32_e32 v124, s70, v124\n" \
"v_mul_f32_e32 v125, s70, v125\n" \
M_"v[144:147], a[252:255], a[212:215], v[144:147]\n" \
B_"v20, s[80:83], s73 offen offset:2048 sc1 lds\n" \
M_"v[140:143], a[184:187], a[148:151], v[140:143]\n" \
"v_mul_f32_e32 v126, s70, v126\n" \
"v_mul_f32_e32 v127, s70, v127\n" \
M_"v[140:143], a[248:251], a[212:215], v[140:143]\n" \
C_"v124, v124, v125\n" \
"v_mul_f32_e64 v121, s70, v121\n" \
M_"v[136:139], a[180:183], a[148:151], v[136:139]\n" \
"v_mul_f32_e32 v122, s70, v122\n" \
"v_mul_f32_e32 v123, s70, v123\n" \
M_"v[136:139], a[244:247], a[212:215], v[136:139]\n" \
B_"v20, s[80:83], s73 offen offset:3072 sc1 lds\n" \
M_"v[132:135], a[176:179], a[148:151], v[132:135]\n" \
"s_sub_u32 m0, s31, s51\n" \
C_"v117, v118, v119\n" \
M_"v[132:135], a[240:243], a[212:215], v[132:135]\n" \
"v_mul_f32_e32 v128, s70, v128\n" \
"v_mul_f32_e64 v129, s70, v129\n" \
M_"v[148:151], a[176:179], a[152:155], v[148:151]\n" \
"v_mul_f32_e32 v130, s70, v130\n" \
"v_mul_f32_e32 v131, s70, v131\n" \
M_"v[148:151], a[240:243], a[216:219], v[148:151]\n" \
B_"v20, s[80:83], s74 offen offset:0 sc1 lds\n" \
M_"v[152:155], a[180:183], a[152:155], v[152:155]\n" \
C_"v125, v126, v127\n" \
C_"v118, v120, v121\n" \
M_"v[152:155], a[244:247], a[216:219], v[152:155]\n" \
C_"v119, v122, v123\n" \
C_"v126, v128, v129\n" \
M_"v[156:159], a[184:187], a[152:155], v[156:159]\n" \
C_"v127, v130, v131\n" \
"v_mul_f32_e64 v132, s70, v132\n" \
M_"v[156:159], a[248:251], a[216:219], v[156:159]\n" \
B_"v20, s[80:83], s74 offen offset:1024 sc1 lds\n" \
M_"v[160:163], a[188:191], a[152:155], v[160:163]\n" \
"ds_write_b128 v21, v[116:119] offset:16384\n" \
M_"v[160:163], a[252:255], a[216:219], v[160:163]\n" \
"v_mul_f32_e32 v133, s70, v133\n" \
"v_mul_f32_e32 v140, s70, v140\n" \
M_"v[176:179], a[188:191], a[156:159], v[176:179]\n" \
"v_mul_f32_e32 v141, s70, v141\n" \
"v_mul_f32_e32 v134, s70, v134\n" \
M_"v[176:179], a[252:255], a[220:223], v[176:179]\n" \
B_"v20, s[80:83], s74 offen offset:2048 sc1 lds\n" \
M_"v[172:175], a[184:187], a[156:159], v[172:175]\n" \
"ds_write_b128 v21, v[124:127] offset:17408\n" \
M_"v[172:175], a[248:251], a[220:223], v[172:175]\n" \
"v_mul_f32_e64 v135, s70, v135\n" \
C_"v132, v132, v133\n" \
M_"v[168:171], a[180:183], a[156:159], v[168:171]\n" \
"v_mul_f32_e32 v142, s70, v142\n" \
"v_mul_f32_e32 v143, s70, v143\n" \
M_"v[168:171], a[244:247], a[220:223], v[168:171]\n" \
B_"v20, s[80:83], s74 offen offset:3072 sc1 lds\n" \
M_"v[164:167], a[176:179], a[156:159], v[164:167]\n" \
"s_sub_u32 m0, s31, 0x4000\n" \
C_"v140, v140, v141\n" \
M_"v[164:167], a[240:243], a[220:223], v[164:167]\n" \
"v_mul_f32_e32 v136, s70, v136\n" \
"v_mul_f32_e32 v137, s70, v137\n" \
M_"v[84:87], a[176:179], a[136:139], v[84:87]\n" \
"v_mul_f32_e32 v138, s70, v138\n" \
"v_mul_f32_e32 v139, s70, v139\n" \
M_"v[84:87], a[240:243], a[200:203], v[84:87]\n" \
B_"v20, s[80:83], s75 offen offset:0 sc1 lds\n" \
M_"v[88:91], a[180:183], a[136:139], v[88:91]\n" \
C_"v133, v134, v135\n" \
"v_mul_f32_e64 v144, s70, v144\n" \
M_"v[88:91], a[244:247], a[200:203], v[88:91]\n" \
"v_mul_f32_e32 v145, s70, v145\n" \
"v_mul_f32_e32 v146, s70, v146\n" \
M_"v[92:95], a[184:187], a[136:139], v[92:95]\n" \
"v_mul_f32_e64 v147, s70, v147\n" \
C_"v141, v142, v143\n" \
M_"v[92:95], a[248:251], a[200:203], v[92:95]\n" \
B_"v20, s[80:83], s75 offen offset:1024 sc1 lds\n" \
M_"v[96:99], a[188:191], a[136:139], v[96:99]\n" \
C_"v134, v136, v137\n" \
C_"v135, v138, v139\n" \
M_"v[96:99], a[252:255], a[200:203], v[96:99]\n" \
C_"v142, v144, v145\n" \
C_"v143, v146, v147\n" \
M_"v[112:115], a[188:191], a[140:143], v[112:115]\n" \
"ds_write_b128 v21, v[132:135] offset:20480\n" \
M_"v[112:115], a[252:255], a[204:207], v[112:115]\n" \
B_"v20, s[80:83], s75 offen offset:2048 sc1 lds\n" \
M_"v[108:111], a[184:187], a[140:143], v[108:111]\n" \
C_"v180, v180, v181\n" \
C_"v181, v182, v183\n" \
M_"v[108:111], a[248:251], a[204:207], v[108:111]\n" \
"ds_write_b128 v21, v[140:143] offset:21504\n" \
M_"v[104:107], a[180:183], a[140:143], v[104:107]\n" \
C_"v234, v244, v245\n" \
"v_mov_b32_e64 v235, 0\n" \
M_"v[104:107], a[244:247], a[204:207], v[104:107]\n" \
B_"v20, s[80:83], s75 offen offset:3072 sc1 lds\n" \
M_"v[100:103], a[176:179], a[140:143], v[100:103]\n" \
"s_mov_b32 m0, s53\n" \
"s_nop 0\n" \
C_"v182, v50, v51\n" \
M_"v[100:103], a[240:243], a[204:207], v[100:103]\n" \
"v_mov_b32_e32 v183, 0x3f80\n" \
C_"v118, v192, v193\n" \
M_"v[52:55], a[176:179], a[128:131], v[52:55]\n" \
C_"v119, v194, v195\n" \
C_"v120, v208, v209\n" \
M_"v[52:55], a[240:243], a[192:195], v[52:55]\n" \
B_"v20, s[80:83], s76 offen offset:0 sc1 lds\n" \
M_"v[56:59], a[180:183], a[128:131], v[56:59]\n" \
C_"v121, v210, v211\n" \
C_"v122, v224, v225\n" \
M_"v[56:59], a[244:247], a[192:195], v[56:59]\n" \
C_"v123, v226, v227\n" \
C_"v124, v240, v241\n" \
C_"v125, v242, v243\n" \
M_"v[60:63], a[184:187], a[128:131], v[60:63]\n" \
M_"v[192:195], v[188:191], v[184:187], 0\n" \
B_"v20, s[80:83], s76 offen offset:1024 sc1 lds\n" \
M_"v[32:35], v[180:183], v[184:187], 0\n" \
M_"v[208:211], v[204:207], v[200:203], 0\n" \
M_"v[36:39], v[188:191], v[200:203], 0\n" \
M_"v[224:227], v[220:223], v[216:219], 0\n" \
M_"v[248:251], v[204:207], v[216:219], 0\n" \
M_"v[240:243], v[236:239], v[232:235], 0\n" \
M_"v[128:131], v[220:223], v[232:235], 0\n" \
"s_mov_b32 s64, 0xaaaaaaaa\n" \
"s_mov_b32 s65, 0xaaaaaaaa\n" \
M_"v[60:63], a[248:251], a[192:195], v[60:63]\n" \
"s_mov_b32 s66, 0xfff0ffff\n" \
"s_mov_b32 s67, 0xf000ff00\n" \
M_"v[64:67], a[188:191], a[128:131], v[64:67]\n" \
"s_mov_b32 s68, 0xffc0fffc\n" \
"s_mov_b32 s69, 0xc000fc00\n" \
"v_cndmask_b32_e64 v192, v192, v193, s[64:65]\n" \
"v_cndmask_b32_e64 v32, v32, v33, s[64:65]\n" \
"v_cndmask_b32_e64 v194, v194, v195, s[64:65]\n" \
"v_cndmask_b32_e64 v34, v34, v35, s[64:65]\n" \
"v_cndmask_b32_e64 v208, v208, v209, s[64:65]\n" \
"v_cndmask_b32_e64 v36, v36, v37, s[64:65]\n" \
"v_cndmask_b32_e64 v210, v210, v211, s[64:65]\n" \
"v_cndmask_b32_e64 v38, v38, v39, s[64:65]\n" \
"v_cndmask_b32_e64 v224, v224, v225, s[64:65]\n" \
"v_cndmask_b32_e64 v248, v248, v249, s[64:65]\n" \
"v_cndmask_b32_e64 v226, v226, v227, s[64:65]\n" \
"v_cndmask_b32_e64 v250, v250, v251, s[64:65]\n" \
"v_cndmask_b32_e64 v240, v240, v241, s[64:65]\n" \
"v_cndmask_b32_e64 v128, v128, v129, s[64:65]\n" \
"v_cndmask_b32_e64 v242, v242, v243, s[64:65]\n" \
"v_cndmask_b32_e64 v130, v130, v131, s[64:65]\n" \
"v_cndmask_b32_e64 v192, v32, v192, s[66:67]\n" \
"v_cndmask_b32_e64 v194, v34, v194, s[68:69]\n" \
"v_cndmask_b32_e64 v208, v36, v208, s[66:67]\n" \
"v_cndmask_b32_e64 v210, v38, v210, s[68:69]\n" \
"v_cndmask_b32_e64 v224, v248, v224, s[66:67]\n" \
"v_cndmask_b32_e64 v226, v250, v226, s[68:69]\n" \
"v_cndmask_b32_e64 v240, v128, v240, s[66:67]\n" \
"v_cndmask_b32_e64 v242, v130, v242, s[68:69]\n" \
M_"v[64:67], a[252:255], a[192:195], v[64:67]\n" \
"v_max_f32_e32 v193, v192, v194\n" \
"v_max_f32_e32 v209, v208, v210\n" \
"v_max_f32_e32 v225, v224, v226\n" \
"v_max_f32_e32 v241, v240, v242\n" \
"v_mul_f32_e32 v148, s70, v148\n" \
"v_permlane32_swap_b32_e32 v193, v225\n" \
"v_permlane32_swap_b32_e32 v209, v241\n" \
"v_mul_f32_e32 v149, s70, v149\n" \
"v_mul_f32_e32 v156, s70, v156\n" \
"v_mul_f32_e32 v157, s70, v157\n" \
"v_permlane32_swap_b32_e32 v196, v228\n" \
"v_permlane32_swap_b32_e32 v212, v244\n" \
"v_max_f32_e32 v193, v193, v225\n" \
"v_max_f32_e32 v209, v209, v241\n" \
"v_mul_f32_e32 v150, s70, v150\n" \
"v_mul_f32_e32 v151, s70, v151\n" \
C_"v148, v148, v149\n" \
"v_mul_f32_e32 v158, s70, v158\n" \
"v_mul_f32_e32 v159, s70, v159\n" \
C_"v156, v156, v157\n" \
"v_permlane16_swap_b32_e32 v228, v244\n" \
"v_permlane16_swap_b32_e32 v193, v209\n" \
M_"v[80:83], a[188:191], a[132:135], v[80:83]\n" \
"s_mov_b32 s72, 0x3eae1a93\n" \
"v_mul_f32_e32 v152, s70, v152\n" \
"v_mul_f32_e32 v153, s70, v153\n" \
"v_mul_f32_e32 v154, s70, v154\n" \
"v_mul_f32_e32 v155, s70, v155\n" \
C_"v149, v150, v151\n" \
"v_mul_f32_e32 v160, s70, v160\n" \
"v_mul_f32_e32 v161, s70, v161\n" \
"v_mul_f32_e32 v162, s70, v162\n" \
"v_mul_f32_e32 v163, s70, v163\n" \
C_"v157, v158, v159\n" \
"v_mul_f32_e32 v45, 0xbfb8aa3b, v228\n" \
"v_mfma_f32_16x16x16_bf16 v[48:51], v[182:183], v[46:47], 0\n" \
"v_mfma_f32_16x16x16_bf16 v[196:199], v[190:191], v[46:47], 0\n" \
"v_mfma_f32_16x16x16_bf16 v[212:215], v[206:207], v[46:47], 0\n" \
"v_max_f32_e64 v193, v193, v209\n" \
C_"v150, v152, v153\n" \
C_"v151, v154, v155\n" \
C_"v158, v160, v161\n" \
C_"v159, v162, v163\n" \
"v_mfma_f32_16x16x16_bf16 v[228:231], v[222:223], v[46:47], 0\n" \
"v_mul_f32_e32 v193, s72, v193\n" \
"v_mov_b32_e32 v209, v193\n" \
"ds_write_b128 v21, v[148:151] offset:24576\n" \
"v_mfma_f32_16x16x16_bf16 v[244:247], v[238:239], v[46:47], 0\n" \
"v_permlane16_swap_b32_e32 v193, v209\n" \
"s_mov_b32 s71, 0x3e0293ee\n" \
M_"v[80:83], a[252:255], a[196:199], v[80:83]\n" \
"v_mov_b32_e32 v225, v193\n" \
"v_mov_b32_e32 v241, v209\n" \
"v_mul_f32_e32 v164, s70, v164\n" \
"v_mul_f32_e64 v165, s70, v165\n" \
"ds_write_b128 v21, v[156:159] offset:25600\n" \
"v_mul_f32_e32 v172, s70, v172\n" \
"v_mul_f32_e32 v173, s70, v173\n" \
"v_pk_mul_f32 v[84:85], v[84:85], s[70:71]\n" \
"v_pk_mul_f32 v[92:93], v[92:93], s[70:71]\n" \
"v_pk_mul_f32 v[100:101], v[100:101], s[70:71]\n" \
"v_pk_mul_f32 v[108:109], v[108:109], s[70:71]\n" \
"v_pk_mul_f32 v[52:53], v[52:53], s[70:71]\n" \
"v_pk_mul_f32 v[60:61], v[60:61], s[70:71]\n" \
"v_permlane32_swap_b32_e32 v193, v225\n" \
"v_permlane32_swap_b32_e32 v209, v241\n" \
"v_mul_f32_e32 v166, s70, v166\n" \
"v_mul_f32_e32 v167, s70, v167\n" \
C_"v164, v164, v165\n" \
"v_mul_f32_e32 v174, s70, v174\n" \
"v_mul_f32_e32 v175, s70, v175\n" \
C_"v172, v172, v173\n" \
"v_pk_mul_f32 v[86:87], v[86:87], s[70:71]\n" \
C_"v84, v84, v85\n" \
"v_pk_mul_f32 v[94:95], v[94:95], s[70:71]\n" \
C_"v92, v92, v93\n" \
"v_pk_mul_f32 v[102:103], v[102:103], s[70:71]\n" \
C_"v100, v100, v101\n" \
"v_pk_mul_f32 v[110:111], v[110:111], s[70:71]\n" \
C_"v108, v108, v109\n" \
"v_pk_mul_f32 v[54:55], v[54:55], s[70:71]\n" \
C_"v52, v52, v53\n" \
"v_pk_mul_f32 v[62:63], v[62:63], s[70:71]\n" \
C_"v60, v60, v61\n" \
"v_fma_f32 v192, v192, s72, -v193\n" \
"v_fma_f32 v194, v194, s72, -v193\n" \
"v_fma_f32 v208, v208, s72, -v209\n" \
"v_fma_f32 v210, v210, s72, -v209\n" \
"v_mul_f32_e32 v168, s70, v168\n" \
"v_mul_f32_e32 v169, s70, v169\n" \
"v_mul_f32_e32 v170, s70, v170\n" \
"v_mul_f32_e32 v171, s70, v171\n" \
C_"v165, v166, v167\n" \
"v_mul_f32_e32 v176, s70, v176\n" \
"v_mul_f32_e32 v177, s70, v177\n" \
"v_mul_f32_e32 v178, s70, v178\n" \
"v_mul_f32_e32 v179, s70, v179\n" \
C_"v173, v174, v175\n" \
"v_pk_mul_f32 v[88:89], v[88:89], s[70:71]\n" \
"v_pk_mul_f32 v[90:91], v[90:91], s[70:71]\n" \
C_"v85, v86, v87\n" \
"v_pk_mul_f32 v[96:97], v[96:97], s[70:71]\n" \
"v_pk_mul_f32 v[98:99], v[98:99], s[70:71]\n" \
C_"v93, v94, v95\n" \
"v_pk_mul_f32 v[104:105], v[104:105], s[70:71]\n" \
"v_pk_mul_f32 v[106:107], v[106:107], s[70:71]\n" \
C_"v101, v102, v103\n" \
"v_pk_mul_f32 v[112:113], v[112:113], s[70:71]\n" \
"v_pk_mul_f32 v[114:115], v[114:115], s[70:71]\n" \
C_"v109, v110, v111\n" \
"v_pk_mul_f32 v[56:57], v[56:57], s[70:71]\n" \
"v_pk_mul_f32 v[58:59], v[58:59], s[70:71]\n" \
C_"v53, v54, v55\n" \
"v_pk_mul_f32 v[64:65], v[64:65], s[70:71]\n" \
"v_pk_mul_f32 v[66:67], v[66:67], s[70:71]\n" \
C_"v61, v62, v63\n" \
"v_pk_mul_f32 v[80:81], v[80:81], s[70:71]\n" \
"v_pk_mul_f32 v[82:83], v[82:83], s[70:71]\n" \
E_"v192, v192\n" \
E_"v194, v194\n" \
E_"v208, v208\n" \
E_"v210, v210\n" \
"v_fma_f32 v224, v224, s72, -v225\n" \
"v_fma_f32 v226, v226, s72, -v225\n" \
M_"v[76:79], a[184:187], a[132:135], v[76:79]\n" \
C_"v166, v168, v169\n" \
C_"v167, v170, v171\n" \
C_"v174, v176, v177\n" \
C_"v175, v178, v179\n" \
C_"v86, v88, v89\n" \
C_"v87, v90, v91\n" \
C_"v94, v96, v97\n" \
C_"v95, v98, v99\n" \
C_"v102, v104, v105\n" \
C_"v103, v106, v107\n" \
C_"v110, v112, v113\n" \
C_"v111, v114, v115\n" \
C_"v54, v56, v57\n" \
C_"v55, v58, v59\n" \
C_"v62, v64, v65\n" \
C_"v63, v66, v67\n" \
E_"v224, v224\n" \
E_"v226, v226\n" \
"v_fma_f32 v240, v240, s72, -v241\n" \
"v_fma_f32 v242, v242, s72, -v241\n" \
"ds_write_b128 v21, v[164:167] offset:28672\n" \
C_"v192, v192, v194\n" \
C_"v208, v208, v210\n" \
E_"v240, v240\n" \
E_"v242, v242\n" \
C_"v132, v48, v49\n" \
C_"v133, v50, v51\n" \
C_"v134, v196, v197\n" \
C_"v135, v198, v199\n" \
"ds_write_b128 v21, v[172:175] offset:29696\n" \
C_"v136, v212, v213\n" \
C_"v137, v214, v215\n" \
"v_perm_b32 v184, v192, v192, v40\n" \
"v_perm_b32 v185, v192, v192, v41\n" \
"v_perm_b32 v186, v192, v192, v42\n" \
"v_perm_b32 v187, v192, v192, v43\n" \
"v_perm_b32 v200, v208, v208, v40\n" \
"v_perm_b32 v201, v208, v208, v41\n" \
"v_perm_b32 v202, v208, v208, v42\n" \
"v_perm_b32 v203, v208, v208, v43\n" \
"ds_write_b128 v27, v[84:87] offset:8192\n" \
C_"v224, v224, v226\n" \
C_"v138, v228, v229\n" \
C_"v139, v230, v231\n" \
M_"v[32:35], v[132:135], v[184:187], 0\n" \
M_"v[36:39], v[134:137], v[200:203], 0\n" \
"v_perm_b32 v216, v224, v224, v40\n" \
"v_perm_b32 v217, v224, v224, v41\n" \
"ds_write_b128 v27, v[92:95] offset:9216\n" \
"v_perm_b32 v218, v224, v224, v42\n" \
"v_perm_b32 v219, v224, v224, v43\n" \
C_"v240, v240, v242\n" \
M_"v[76:79], a[248:251], a[196:199], v[76:79]\n" \
C_"v140, v244, v245\n" \
C_"v141, v246, v247\n" \
M_"v[248:251], v[136:139], v[216:219], 0\n" \
"v_perm_b32 v232, v240, v240, v40\n" \
"v_perm_b32 v233, v240, v240, v41\n" \
"ds_write_b128 v27, v[100:103] offset:12288\n" \
"v_perm_b32 v234, v240, v240, v42\n" \
"v_perm_b32 v235, v240, v240, v43\n" \
"v_pk_mul_f32 v[76:77], v[76:77], s[70:71]\n" \
"v_pk_mul_f32 v[78:79], v[78:79], s[70:71]\n" \
C_"v76, v76, v77\n" \
C_"v77, v78, v79\n" \
C_"v78, v80, v81\n" \
C_"v79, v82, v83\n" \
M_"v[128:131], v[138:141], v[232:235], 0\n" \
"ds_write_b128 v27, v[108:111] offset:13312\n" \
M_"v[72:75], a[180:183], a[132:135], v[72:75]\n" \
C_"v116, v252, v253\n" \
C_"v117, v254, v255\n" \
M_"v[72:75], a[244:247], a[196:199], v[72:75]\n" \
"v_exp_f32_e64 v45, v45\n" \
M_"v[192:195], v[116:119], v[184:187], 0\n" \
"ds_write_b128 v27, v[52:55] offset:0\n" \
M_"v[208:211], v[118:121], v[200:203], 0\n" \
M_"v[224:227], v[120:123], v[216:219], 0\n" \
M_"v[240:243], v[122:125], v[232:235], 0\n" \
"ds_write_b128 v27, v[60:63] offset:1024\n" \
"v_mov_b32_dpp v34, v38 quad_perm:[0,1,2,3] row_mask:0x2 bank_mask:0xf\n" \
"v_pk_mul_f32 v[72:73], v[72:73], s[70:71]\n" \
"v_pk_mul_f32 v[74:75], v[74:75], s[70:71]\n" \
"v_mov_b32_dpp v34, v250 quad_perm:[0,1,2,3] row_mask:0x4 bank_mask:0xf\n" \
"v_add_f32_e64 v45, 1.0, v45\n" \
M_"v[68:71], a[176:179], a[132:135], v[68:71]\n" \
"v_mov_b32_dpp v34, v130 quad_perm:[0,1,2,3] row_mask:0x8 bank_mask:0xf\n" \
"v_mov_b32_dpp v32, v36 quad_perm:[0,1,2,3] row_mask:0x2 bank_mask:0xf\n" \
"v_rcp_f32_e64 v45, v45\n" \
"ds_write_b128 v27, v[76:79] offset:5120\n" \
"v_mov_b32_dpp v33, v37 quad_perm:[0,1,2,3] row_mask:0x2 bank_mask:0xf\n" \
"v_rcp_f32_e64 v34, v34\n" \
"v_mov_b32_dpp v32, v248 quad_perm:[0,1,2,3] row_mask:0x4 bank_mask:0xf\n" \
"v_mov_b32_dpp v33, v249 quad_perm:[0,1,2,3] row_mask:0x4 bank_mask:0xf\n" \
"v_mul_f32_e64 v34, v45, v34\n" \
"v_mov_b32_dpp v32, v128 quad_perm:[0,1,2,3] row_mask:0x8 bank_mask:0xf\n" \
"v_mov_b32_dpp v33, v129 quad_perm:[0,1,2,3] row_mask:0x8 bank_mask:0xf\n" \
"v_mul_f32_e32 v32, v34, v32\n" \
"v_mul_f32_e32 v33, v34, v33\n" \
"v_mov_b32_e64 v38, v34\n" \
M_"v[68:71], a[240:243], a[196:199], v[68:71]\n" \
"v_sub_f32_e32 v44, 1.0, v45\n" \
"v_permlane16_swap_b32_e32 v34, v38\n" \
"v_mov_b32_e32 v250, v34\n" \
"v_mov_b32_e32 v130, v38\n" \
C_"v32, v32, v33\n" \
"v_permlane32_swap_b32_e32 v34, v250\n" \
"v_permlane32_swap_b32_e32 v38, v130\n" \
"v_mul_f32_e64 v192, v34, v192\n" \
"v_pk_mul_f32 v[68:69], v[68:69], s[70:71]\n" \
"v_pk_mul_f32 v[70:71], v[70:71], s[70:71]\n" \
C_"v68, v68, v69\n" \
C_"v69, v70, v71\n" \
C_"v70, v72, v73\n" \
C_"v71, v74, v75\n" \
"ds_write_b128 v27, v[68:71] offset:4096\n" \
"v_mul_f32_e32 v193, v34, v193\n" \
"v_mul_f32_e32 v208, v38, v208\n" \
"v_mul_f32_e32 v209, v38, v209\n" \
"v_mul_f32_e32 v224, v250, v224\n" \
"v_mul_f32_e32 v225, v250, v225\n" \
"v_mul_f32_e32 v240, v130, v240\n" \
"v_mul_f32_e32 v241, v130, v241\n" \
"v_mul_f32_e32 v194, v34, v194\n" \
"v_mul_f32_e64 v195, v34, v195\n" \
C_"v192, v192, v193\n" \
"v_mul_f32_e32 v210, v38, v210\n" \
"v_mul_f32_e32 v211, v38, v211\n" \
C_"v208, v208, v209\n" \
"v_mul_f32_e32 v226, v250, v226\n" \
"v_mul_f32_e32 v227, v250, v227\n" \
C_"v224, v224, v225\n" \
"v_mul_f32_e32 v242, v130, v242\n" \
"v_mul_f32_e32 v243, v130, v243\n" \
C_"v240, v240, v241\n" \
C_"v193, v194, v195\n" \
"s_add_u32 s84, s48, 0x0\n" \
"s_nop 0\n" \
C_"v209, v210, v211\n" \
"s_add_u32 s85, s48, 0x24800\n" \
C_"v225, v226, v227\n" \
"s_add_u32 s86, s48, 0x49000\n" \
C_"v241, v242, v243\n" \
"s_add_u32 s87, s48, 0x6d800\n" \
"ds_write_b32 v26, v44\n" \
"buffer_store_dword v32, v25, s[80:83], s49 offen sc1\n" \
"buffer_store_dwordx2 v[192:193], v24, s[80:83], s84 offen sc1\n" \
"buffer_store_dwordx2 v[208:209], v24, s[80:83], s85 offen sc1\n" \
"buffer_store_dwordx2 v[224:225], v24, s[80:83], s86 offen sc1\n" \
"buffer_store_dwordx2 v[240:241], v24, s[80:83], s87 offen sc1\n" \
"s_branch L_end_%=\n" \
"L_poll61_%=:\n" \
"s_mov_b32 s84, 0\n" \
"L_pl61_%=:\n" \
"s_sleep 1\n" \
"v_bfe_u32 v44, v20, 2, 5\n" \
"buffer_load_dword v44, v44, s[80:83], s38 offen offset:128 sc1\n" \
"s_waitcnt vmcnt(0)\n" \
"v_cmp_ne_u32_e32 vcc, s62, v44\n" \
"s_add_u32 s84, s84, 1\n" \
"s_nop 3\n" \
"s_cbranch_vccz L_pollr61_%=\n" \
"s_cmp_gt_u32 s84, 0x400000\n" \
"s_cbranch_scc0 L_pl61_%=\n" \
"s_branch L_pollr61_%=\n" \
"L_poll161_%=:\n" \
"s_mov_b32 s84, 0\n" \
"L_pl161_%=:\n" \
"s_sleep 1\n" \
"v_bfe_u32 v44, v20, 2, 5\n" \
"buffer_load_dword v44, v44, s[80:83], s38 offen offset:0 sc1\n" \
"s_waitcnt vmcnt(0)\n" \
"v_cmp_ne_u32_e32 vcc, s62, v44\n" \
"s_add_u32 s84, s84, 1\n" \
"s_nop 3\n" \
"s_cbranch_vccz L_pollr161_%=\n" \
"s_cmp_gt_u32 s84, 0x400000\n" \
"s_cbranch_scc0 L_pl161_%=\n" \
"s_branch L_pollr161_%=\n" \
"L_end_%=:\n"
#define PROJ_PAGES 20
#define PROJ_VCLOB "v21","v24","v25","v28","v29","v32","v33","v34","v35","v36","v37","v38","v39","v40","v41","v42","v43","v44","v45","v46","v47","v48","v49","v50","v51","v52","v53","v54","v55","v56","v57","v58","v59","v60","v61","v62","v63","v64","v65","v66","v67","v68","v69","v70","v71","v72","v73","v74","v75","v76","v77","v78","v79","v80","v81","v82","v83","v84","v85","v86","v87","v88","v89","v90","v91","v92","v93","v94","v95","v96","v97","v98","v99","v100","v101","v102","v103","v104","v105","v106","v107","v108","v109","v110","v111","v112","v113","v114","v115","v116","v117","v118","v119","v120","v121","v122","v123","v124","v125","v126","v127","v128","v129","v130","v131","v132","v133","v134","v135","v136","v137","v138","v139","v140","v141","v142","v143","v144","v145","v146","v147","v148","v149","v150","v151","v152","v153","v154","v155","v156","v157","v158","v159","v160","v161","v162","v163","v164","v165","v166","v167","v168","v169","v170","v171","v172","v173","v174","v175","v176","v177","v178","v179","v180","v181","v182","v183","v184","v185","v186","v187","v188","v189","v190","v192","v193","v194","v195","v196","v197","v198","v199","v200","v201","v202","v203","v204","v205","v206","v207","v208","v209","v210","v211","v212","v213","v214","v215","v216","v217","v218","v219","v220","v221","v222","v223","v224","v225","v226","v227","v228","v229","v230","v231","v232","v233","v234","v235","v236","v237","v238","v239","v240","v241","v242","v243","v244","v245","v246","v247","v248","v249","v250","v251","v252","v253","v254","v255"
#define PROJ_SCLOB "s4","s5","s6","s7","s32","s33","s34","s35","s36","s37","s38","s39","s40","s41","s42","s43","s44","s45","s46","s47","s48","s49","s50","s51","s52","s53","s54","s55","s63","s64","s65","s66","s67","s68","s69","s70","s71","s72","s73","s74","s75","s76","s77","s78","s79","s80","s81","s82","s83","s84","s85","s86","s87","s88","s89","s90","s91","s92","s93","s94","s95","s96","s97","s98","s99","s100","s101"

typedef const u16* const __attribute__((address_space(4)))* kaptr;
__device__ __forceinline__ void proj_phase(kaptr& ka, char*& ws, const unsigned wid, u32x4 (&vp)[5], unsigned& vl, u32x4 (&sa)[5], u32x2 s24, u32x2& s28, unsigned tdum, u32x4& srdA, u32x2& sd, unsigned& tag
    ) {
    const unsigned b = blockIdx.x, h = (b & 7) * 4 + ((b >> 3) & 3), mt = b >> 5;
    const unsigned rb = (wid < 2u ? 15u - mt : mt) * 128u;
    const unsigned lane = threadIdx.x & 63u, wc = wid & 1u;
    u32x2 vAc, vB;
#pragma unroll
    for (int par = 0; par < 2; ++par) {
        const unsigned lr = lane >> 3, cc = (lane & 7u) ^ (4u * par + (lr >> 1)), r0 = rb + wc * 64u + lr;
        vAc[par] = r0 * (DC * 2) + cc * 16u;
        const unsigned br = lane >> 4, p16 = lane & 15u, sc = (((p16 >> 1) ^ (br | (par << 2))) << 1) | (p16 & 1u);
        vB[par] = (16u * wid + br) * (NBIG * 2) + h * (HD * 2) + sc * 16u;
    }
    vl = lane * 16u;
    u32x2 s30 = {wid * 8192u, 65536u + wid * 4096u};
    asm volatile(PROJ_ASM
                 : "={s62}"(tag), "={s[56:59]}"(srdA), "={s[60:61]}"(sd), "+{s[8:11]}"(sa[0]), "+{s[12:15]}"(sa[1]), "+{s[16:19]}"(sa[2]), "+{s[20:23]}"(sa[3]), "+{s[24:25]}"(s24), "+{s[28:29]}"(s28), "+{s[30:31]}"(s30),
                   "={v[0:3]}"(vp[0]), "={v[4:7]}"(vp[1]), "={v[8:11]}"(vp[2]), "={v[12:15]}"(vp[3]), "={v[16:19]}"(vp[4]), "+{v[22:23]}"(vAc), "+{v[26:27]}"(vB), "+{v191}"(tdum), "+{s[26:27]}"(ws), "+{s[0:1]}"(ka)
                 : "{s2}"(b), "{s3}"(wid), "{v20}"(vl)
                 : "memory", "m0", "vcc", "scc", PROJ_VCLOB, PROJ_SCLOB, ACLOB_LO, ACLOB_HI);
    sa[4] = (u32x4){s24[0], 0u, 0u, 0u};
}

namespace att {
#define DI __device__ __forceinline__
typedef __bf16 bfx8 __attribute__((ext_vector_type(8)));
DI unsigned rfl(unsigned x) { return __builtin_amdgcn_readfirstlane(x); }
DI f32x4 mfma(u32x4 a, u32x4 b, f32x4 c) { return __builtin_amdgcn_mfma_f32_16x16x32_bf16(__builtin_bit_cast(bfx8, a), __builtin_bit_cast(bfx8, b), c, 0, 0, 0); }
typedef __bf16 bfx2 __attribute__((ext_vector_type(2)));
typedef float f32x2 __attribute__((ext_vector_type(2)));
DI unsigned cvtpk2(float lo, float hi) { f32x2 v = {lo, hi}; bfx2 b = __builtin_convertvector(v, bfx2); return __builtin_bit_cast(unsigned, b); }
DI void att_exact(const u16* __restrict__ x, const u16* __restrict__ wq, char* __restrict__ ws, unsigned h, unsigned r0, float g1) {
    const unsigned lane = lane_id(), g = lane >> 4, c = lane & 15u;
    f32x4 o[8];
#pragma unroll
    for (int n = 0; n < 8; ++n) o[n] = (f32x4){0.f, 0.f, 0.f, 0.f};
#pragma unroll 1
    for (unsigned kk = 0; kk < (unsigned)DM; kk += 32u) {
        const unsigned k = kk + 8u * g;
        const u32x4 xf = *reinterpret_cast<const u32x4*>(x + (r0 + c) * DM + k);
#pragma unroll
        for (int n = 0; n < 8; ++n) {
            const u16* w = wq + k * NBIG + h * HD + n * 16 + c; u32x4 wf;
#pragma unroll
            for (int e = 0; e < 4; ++e) wf[e] = (unsigned)w[2 * e * NBIG] | ((unsigned)w[(2 * e + 1) * NBIG] << 16);
            o[n] = mfma(wf, xf, o[n]);
        }
    }
    u32x4 q[4];
#pragma unroll
    for (int p = 0; p < 4; ++p) {
        const f32x4 a = o[2 * p] * 0.12751743082f, b = o[2 * p + 1] * 0.12751743082f;
        q[p] = (u32x4){cvtpk2(a[0], a[1]), cvtpk2(a[2], a[3]), cvtpk2(b[0], b[1]), cvtpk2(b[2], b[3])};
    }
#pragma unroll
    for (int n = 0; n < 8; ++n) o[n] = (f32x4){0.f, 0.f, 0.f, 0.f};
    const __amdgpu_buffer_rsrc_t srd = __builtin_amdgcn_make_buffer_rsrc((void*)(ws + WS_K + (size_t)h * 1048576), (short)0, 1048576, 0x00020000);
    float m = -3.0e38f, l = 0.f;
#pragma unroll 1
    for (unsigned k0 = 0; k0 <= r0 + 15u; k0 += 32u) {
        const unsigned tb = (k0 >> 6) * 32768u + (k0 & 32u) * 256u;
        f32x4 s[2]; float mx = m;
#pragma unroll
        for (int kt = 0; kt < 2; ++kt) {
            s[kt] = (f32x4){0.f, 0.f, 0.f, 0.f};
#pragma unroll
            for (int p = 0; p < 4; ++p) s[kt] = mfma(__builtin_amdgcn_raw_buffer_load_b128(srd, lane * 16u + (kt * 4 + p) * 1024u, tb, 16), q[p], s[kt]);
#pragma unroll
            for (int e = 0; e < 4; ++e) { if (k0 + 16u * kt + 4u * g + e > r0 + c) s[kt][e] = -3.0e38f; mx = fmaxf(mx, s[kt][e]); }
        }
        mx = fmaxf(mx, __shfl_xor(mx, 16)); mx = fmaxf(mx, __shfl_xor(mx, 32));
        const float al = __builtin_amdgcn_exp2f(m - mx); m = mx;
        u32x4 pf;
#pragma unroll
        for (int e = 0; e < 4; ++e) pf[e] = cvtpk2(__builtin_amdgcn_exp2f(s[e >> 1][2 * (e & 1)] - m), __builtin_amdgcn_exp2f(s[e >> 1][2 * (e & 1) + 1] - m));
        l *= al;
#pragma unroll
        for (int e = 0; e < 4; ++e) l += __uint_as_float(pf[e] << 16) + __uint_as_float(pf[e] & 0xffff0000u);
#pragma unroll
        for (int n = 0; n < 8; ++n) o[n] = mfma(__builtin_amdgcn_raw_buffer_load_b128(srd, lane * 16u + 16384u + n * 1024u, tb, 16), pf, o[n] * al);
    }
    l += __shfl_xor(l, 16); l += __shfl_xor(l, 32);
    const float f = g1 / l;
    const __amdgpu_buffer_rsrc_t srdA = __builtin_amdgcn_make_buffer_rsrc((void*)(ws + WS_AF), (short)0, 128 * KS_TOT * 1024, 0x00020000);
#pragma unroll
    for (int p = 0; p < 4; ++p) {
        const f32x4 a = o[2 * p] * f, b = o[2 * p + 1] * f;
        __builtin_amdgcn_raw_buffer_store_b128((u32x4){cvtpk2(a[0], a[1]), cvtpk2(a[2], a[3]), cvtpk2(b[0], b[1]), cvtpk2(b[2], b[3])}, srdA, lane * 16u + p * 1024u, ((r0 >> 4) * KS_TOT + 18u + 4u * h) * 1024u, 16);
    }
}
}

#define ATT_ASM \
"s_branch L_ago_%=\n" \
".p2align 12\n" \
"L_ab:\n" \
".fill (L_ab + 0 - .) / 4, 4, 0xBF800000\n" \
"L_ap0:\n" \
"s_setpc_b64 s[88:89]\n" \
"L_ago_%=:\n" \
"s_mov_b32 s101, 0\n" \
"s_waitcnt vmcnt(15) lgkmcnt(0)\n" \
"s_barrier\n" \
"v_mfma_f32_32x32x16_bf16 v[128:143], a[120:123], a[120:123], 0\n" \
"v_and_b32_e32 v63, 0x300, v20\n" \
R_"v[96:99], v63 offset:0\n" \
"v_bfe_u32 v61, v20, 2, 5\n" \
"s_mov_b32 s96, s24\n" \
"v_add_u32_e32 v58, s22, v20\n" \
R_"a[0:3], v58 offset:0\n" \
"v_mfma_f32_32x32x16_bf16 v[144:159], a[120:123], a[120:123], 0\n" \
"buffer_load_dword v57, v61, s[56:59], s96 offen sc1\n" \
R_"a[16:19], v58 offset:8192\n" \
"v_add_u32_e32 v59, s23, v20\n" \
R_"a[32:35], v59 offset:0\n" \
R_"a[48:51], v59 offset:8192\n" \
"v_mfma_f32_32x32x16_bf16 v[160:175], a[120:123], a[120:123], 0\n" \
R_"v[100:103], v63 offset:1024\n" \
R_"a[4:7], v58 offset:1024\n" \
R_"a[20:23], v58 offset:9216\n" \
"s_sub_u32 s67, s12, 0xc000\n" \
"s_mov_b32 s65, s12\n" \
"v_add_u32_e32 v55, s67, v20\n" \
"v_mfma_f32_32x32x16_bf16 v[176:191], a[120:123], a[120:123], 0\n" \
"v_add_u32_e32 v52, 0xc000, v55\n" \
"s_xor_b32 m0, s65, 0x10000\n" \
R_"a[36:39], v59 offset:1024\n" \
B_"v52, s[8:11], 0 offen offset:0 sc1 lds\n" \
R_"a[52:55], v59 offset:9216\n" \
"v_mfma_f32_32x32x16_bf16 v[192:207], a[120:123], a[120:123], 0\n" \
R_"v[104:107], v63 offset:2048\n" \
R_"a[8:11], v58 offset:2048\n" \
R_"a[24:27], v58 offset:10240\n" \
B_"v52, s[8:11], 0 offen offset:1024 sc1 lds\n" \
R_"a[40:43], v59 offset:2048\n" \
"v_mfma_f32_32x32x16_bf16 v[208:223], a[120:123], a[120:123], 0\n" \
R_"a[56:59], v59 offset:10240\n" \
R_"v[108:111], v63 offset:3072\n" \
R_"a[12:15], v58 offset:3072\n" \
R_"a[28:31], v58 offset:11264\n" \
"s_waitcnt lgkmcnt(15)\n" \
"v_xor_b32_e32 v96, 0x80008000, v96\n" \
"v_mfma_f32_32x32x16_bf16 v[224:239], a[120:123], a[120:123], 0\n" \
"v_xor_b32_e32 v97, 0x80008000, v97\n" \
"v_xor_b32_e32 v98, 0x80008000, v98\n" \
"v_xor_b32_e32 v99, 0x80008000, v99\n" \
R_"a[44:47], v59 offset:3072\n" \
R_"a[60:63], v59 offset:11264\n" \
R_"a[160:163], v20 offset:0\n" \
"v_mfma_f32_32x32x16_bf16 v[240:255], a[120:123], a[120:123], 0\n" \
R_"a[164:167], v20 offset:1024\n" \
"s_waitcnt lgkmcnt(15)\n" \
"v_xor_b32_e32 v100, 0x80008000, v100\n" \
"v_xor_b32_e32 v101, 0x80008000, v101\n" \
"v_xor_b32_e32 v102, 0x80008000, v102\n" \
"v_xor_b32_e32 v103, 0x80008000, v103\n" \
M_"v[32:35], v[96:99], a[0:3], 0\n" \
R_"a[168:171], v20 offset:2048\n" \
R_"a[172:175], v20 offset:3072\n" \
M_"v[36:39], v[96:99], a[16:19], 0\n" \
R_"a[188:191], v20 offset:7168\n" \
R_"a[184:187], v20 offset:6144\n" \
M_"v[40:43], v[96:99], a[32:35], 0\n" \
"s_waitcnt lgkmcnt(15)\n" \
"v_xor_b32_e32 v104, 0x80008000, v104\n" \
M_"v[44:47], v[96:99], a[48:51], 0\n" \
"v_xor_b32_e32 v105, 0x80008000, v105\n" \
"v_xor_b32_e32 v106, 0x80008000, v106\n" \
M_"v[32:35], v[100:103], a[4:7], v[32:35]\n" \
"v_xor_b32_e32 v107, 0x80008000, v107\n" \
R_"a[180:183], v20 offset:5120\n" \
M_"v[36:39], v[100:103], a[20:23], v[36:39]\n" \
R_"a[176:179], v20 offset:4096\n" \
"s_waitcnt lgkmcnt(12)\n" \
"v_xor_b32_e32 v108, 0x80008000, v108\n" \
M_"v[40:43], v[100:103], a[36:39], v[40:43]\n" \
"v_xor_b32_e32 v109, 0x80008000, v109\n" \
"v_xor_b32_e32 v110, 0x80008000, v110\n" \
M_"v[44:47], v[100:103], a[52:55], v[44:47]\n" \
"v_xor_b32_e32 v111, 0x80008000, v111\n" \
"v_add_u32_e32 v60, s15, v20\n" \
M_"v[32:35], v[104:107], a[8:11], v[32:35]\n" \
"v_add_u32_e32 v60, 0x10000, v60\n" \
R_"a[128:131], v20 offset:8192\n" \
M_"v[36:39], v[104:107], a[24:27], v[36:39]\n" \
R_"a[144:147], v20 offset:12288\n" \
R_"a[132:135], v20 offset:9216\n" \
M_"v[40:43], v[104:107], a[40:43], v[40:43]\n" \
R_"a[148:151], v20 offset:13312\n" \
R_"a[136:139], v20 offset:10240\n" \
M_"v[44:47], v[104:107], a[56:59], v[44:47]\n" \
R_"a[152:155], v20 offset:14336\n" \
"s_waitcnt lgkmcnt(13)\n" \
M_"v[32:35], v[108:111], a[12:15], v[32:35]\n" \
R_"a[140:143], v20 offset:11264\n" \
R_"a[156:159], v20 offset:15360\n" \
M_"v[36:39], v[108:111], a[28:31], v[36:39]\n" \
R_"a[64:67], v60 offset:16384\n" \
R_"a[68:71], v60 offset:17408\n" \
M_"v[40:43], v[108:111], a[44:47], v[40:43]\n" \
R_"a[72:75], v60 offset:18432\n" \
R_"a[76:79], v60 offset:19456\n" \
M_"v[44:47], v[108:111], a[60:63], v[44:47]\n" \
R_"a[80:83], v60 offset:24576\n" \
R_"a[84:87], v60 offset:25600\n" \
M_"v[64:67], a[160:163], a[0:3], v[32:35]\n" \
R_"a[88:91], v60 offset:26624\n" \
"s_waitcnt lgkmcnt(15)\n" \
M_"v[64:67], a[164:167], a[4:7], v[64:67]\n" \
R_"a[92:95], v60 offset:27648\n" \
R_"a[192:195], v60 offset:0\n" \
M_"v[64:67], a[168:171], a[8:11], v[64:67]\n" \
R_"a[196:199], v60 offset:1024\n" \
R_"a[200:203], v60 offset:2048\n" \
M_"v[64:67], a[172:175], a[12:15], v[64:67]\n" \
R_"a[204:207], v60 offset:3072\n" \
R_"a[208:211], v60 offset:8192\n" \
M_"v[68:71], a[188:191], a[12:15], v[32:35]\n" \
R_"a[212:215], v60 offset:9216\n" \
R_"a[216:219], v60 offset:10240\n" \
M_"v[68:71], a[184:187], a[8:11], v[68:71]\n" \
R_"a[220:223], v60 offset:11264\n" \
"v_mov_b32_e32 v56, s62\n" \
M_"v[68:71], a[180:183], a[4:7], v[68:71]\n" \
"v_readlane_b32 s68, v11, 0\n" \
"s_mov_b32 m0, s20\n" \
M_"v[68:71], a[176:179], a[0:3], v[68:71]\n" \
"global_load_lds_dword v[16:17], off\n" \
M_"v[76:79], a[176:179], a[16:19], v[36:39]\n" \
E_"v66, v66\n" \
M_"v[76:79], a[180:183], a[20:23], v[76:79]\n" \
E_"v67, v67\n" \
M_"v[76:79], a[184:187], a[24:27], v[76:79]\n" \
E_"v64, v64\n" \
M_"v[76:79], a[188:191], a[28:31], v[76:79]\n" \
E_"v70, v70\n" \
M_"v[72:75], a[172:175], a[28:31], v[36:39]\n" \
E_"v71, v71\n" \
M_"v[72:75], a[168:171], a[24:27], v[72:75]\n" \
E_"v68, v68\n" \
M_"v[72:75], a[164:167], a[20:23], v[72:75]\n" \
E_"v69, v69\n" \
M_"v[72:75], a[160:163], a[16:19], v[72:75]\n" \
C_"v71, v70, v71\n" \
"v_accvgpr_read_b32 v28, a124\n" \
M_"v[80:83], a[160:163], a[32:35], v[40:43]\n" \
E_"v65, v65\n" \
M_"v[80:83], a[164:167], a[36:39], v[80:83]\n" \
E_"v76, v76\n" \
M_"v[80:83], a[168:171], a[40:43], v[80:83]\n" \
E_"v77, v77\n" \
M_"v[80:83], a[172:175], a[44:47], v[80:83]\n" \
E_"v72, v72\n" \
M_"v[84:87], a[188:191], a[44:47], v[40:43]\n" \
E_"v73, v73\n" \
M_"v[84:87], a[184:187], a[40:43], v[84:87]\n" \
E_"v74, v74\n" \
M_"v[84:87], a[180:183], a[36:39], v[84:87]\n" \
E_"v75, v75\n" \
M_"v[84:87], a[176:179], a[32:35], v[84:87]\n" \
C_"v72, v72, v73\n" \
"v_accvgpr_read_b32 v29, a125\n" \
M_"v[92:95], a[176:179], a[48:51], v[44:47]\n" \
E_"v78, v78\n" \
M_"v[92:95], a[180:183], a[52:55], v[92:95]\n" \
"s_waitcnt vmcnt(3)\n" \
"v_cmp_ne_u32_e32 vcc, s62, v57\n" \
M_"v[92:95], a[184:187], a[56:59], v[92:95]\n" \
E_"v79, v79\n" \
M_"v[92:95], a[188:191], a[60:63], v[92:95]\n" \
"v_accvgpr_read_b32 v21, a126\n" \
C_"v70, v68, v69\n" \
M_"v[88:91], a[172:175], a[60:63], v[44:47]\n" \
"s_cbranch_vccnz L_skb_%=\n" \
"L_rkb_%=:\n" \
C_"v73, v74, v75\n" \
"s_mov_b64 exec, 1\n" \
"buffer_store_dword v56, off, s[56:59], s68 offset:0 sc1\n" \
"s_mov_b64 exec, -1\n" \
M_"v[88:91], a[168:171], a[56:59], v[88:91]\n" \
"v_mov_b32_e32 v62, 0\n" \
"s_mov_b32 s64, 0x8000\n" \
M_"v[88:91], a[164:167], a[52:55], v[88:91]\n" \
"v_add_u32_e32 v50, 0x6000, v55\n" \
"v_add_u32_e32 v51, 0xa000, v55\n" \
M_"v[88:91], a[160:163], a[48:51], v[88:91]\n" \
"v_add_u32_e32 v53, 0x10000, v55\n" \
"v_add_u32_e32 v48, 0xc000, v20\n" \
"s_waitcnt vmcnt(5) lgkmcnt(0)\n" \
"s_barrier\n" \
"v_add_u32_e32 v49, s17, v20\n" \
"v_accvgpr_write_b32 a124, v96\n" \
"v_accvgpr_write_b32 a125, v97\n" \
"v_accvgpr_write_b32 a126, v98\n" \
"v_accvgpr_write_b32 a127, v99\n" \
"v_accvgpr_write_b32 a224, v100\n" \
"v_accvgpr_write_b32 a225, v101\n" \
"v_accvgpr_write_b32 a226, v102\n" \
"v_accvgpr_write_b32 a227, v103\n" \
"v_accvgpr_write_b32 a228, v104\n" \
"v_accvgpr_write_b32 a229, v105\n" \
"v_accvgpr_write_b32 a230, v106\n" \
"v_accvgpr_write_b32 a231, v107\n" \
"v_accvgpr_write_b32 a232, v108\n" \
"v_accvgpr_write_b32 a233, v109\n" \
"v_accvgpr_write_b32 a234, v110\n" \
"v_accvgpr_write_b32 a235, v111\n" \
C_"v69, v66, v67\n" \
C_"v68, v64, v65\n" \
C_"v74, v76, v77\n" \
C_"v75, v78, v79\n" \
"s_sub_u32 s66, s18, 1\n" \
"s_cbranch_scc1 L_tr_%=\n" \
"s_branch L_1_%=\n" \
".fill (L_ab + 1528 - .) / 4, 4, 0xBF800000\n" \
"L_tL_1_%=:\n" \
"s_waitcnt vmcnt(4) lgkmcnt(0)\n" \
"v_smfmac_f32_16x16x64_bf16 a[100:103], a[104:107], v[116:123], v21\n" \
"s_barrier\n" \
"L_1_%=:\n" \
M_"v[96:99], a[128:131], a[0:3], v[32:35]\n" \
"s_xor_b32 m0, s65, 0x12000\n" \
R_"a[160:163], v49 offset:0\n" \
M_"v[96:99], a[132:135], a[4:7], v[96:99]\n" \
B_"v50, s[8:11], s64 offen offset:0 sc1 lds\n" \
M_"v[96:99], a[136:139], a[8:11], v[96:99]\n" \
R_"a[164:167], v49 offset:1024\n" \
R_"a[168:171], v49 offset:2048\n" \
M_"v[96:99], a[140:143], a[12:15], v[96:99]\n" \
R_"a[172:175], v49 offset:3072\n" \
R_"a[176:179], v49 offset:4096\n" \
M_"v[100:103], a[156:159], a[12:15], v[32:35]\n" \
R_"a[180:183], v49 offset:5120\n" \
R_"a[184:187], v49 offset:6144\n" \
M_"v[100:103], a[152:155], a[8:11], v[100:103]\n" \
"v_exp_f32_e64 v86, v86\n" \
M_"v[100:103], a[148:151], a[4:7], v[100:103]\n" \
"v_exp_f32_e64 v87, v87\n" \
M_"v[100:103], a[144:147], a[0:3], v[100:103]\n" \
R_"a[188:191], v49 offset:7168\n" \
C_"v87, v86, v87\n" \
M_"v[108:111], a[144:147], a[16:19], v[36:39]\n" \
"v_exp_f32_e64 v84, v84\n" \
M_"v[108:111], a[148:151], a[20:23], v[108:111]\n" \
"v_exp_f32_e64 v85, v85\n" \
M_"v[108:111], a[152:155], a[24:27], v[108:111]\n" \
"v_exp_f32_e64 v82, v82\n" \
M_"v[108:111], a[156:159], a[28:31], v[108:111]\n" \
"v_exp_f32_e64 v83, v83\n" \
M_"v[104:107], a[140:143], a[28:31], v[36:39]\n" \
"v_exp_f32_e64 v80, v80\n" \
M_"v[104:107], a[136:139], a[24:27], v[104:107]\n" \
"v_exp_f32_e64 v81, v81\n" \
M_"v[104:107], a[132:135], a[20:23], v[104:107]\n" \
"v_exp_f32_e64 v88, v88\n" \
M_"v[104:107], a[128:131], a[16:19], v[104:107]\n" \
B_"v50, s[8:11], s64 offen offset:1024 sc1 lds\n" \
M_"v[112:115], a[128:131], a[32:35], v[40:43]\n" \
"s_xor_b32 m0, s65, 0x6000\n" \
C_"v86, v84, v85\n" \
M_"v[112:115], a[132:135], a[36:39], v[112:115]\n" \
C_"v85, v82, v83\n" \
C_"v84, v80, v81\n" \
M_"v[112:115], a[136:139], a[40:43], v[112:115]\n" \
"v_exp_f32_e64 v89, v89\n" \
M_"v[112:115], a[140:143], a[44:47], v[112:115]\n" \
"v_exp_f32_e64 v90, v90\n" \
M_"v[116:119], a[156:159], a[44:47], v[40:43]\n" \
"v_exp_f32_e64 v91, v91\n" \
M_"v[116:119], a[152:155], a[40:43], v[116:119]\n" \
"v_exp_f32_e64 v92, v92\n" \
M_"v[116:119], a[148:151], a[36:39], v[116:119]\n" \
"v_exp_f32_e64 v93, v93\n" \
M_"v[116:119], a[144:147], a[32:35], v[116:119]\n" \
"v_exp_f32_e64 v94, v94\n" \
M_"v[124:127], a[144:147], a[48:51], v[44:47]\n" \
C_"v88, v88, v89\n" \
C_"v89, v90, v91\n" \
M_"v[124:127], a[148:151], a[52:55], v[124:127]\n" \
"v_exp_f32_e64 v95, v95\n" \
M_"v[124:127], a[152:155], a[56:59], v[124:127]\n" \
C_"v90, v92, v93\n" \
C_"v91, v94, v95\n" \
M_"v[124:127], a[156:159], a[60:63], v[124:127]\n" \
R_"a[144:147], v48 offset:4096\n" \
R_"a[148:151], v48 offset:5120\n" \
M_"v[120:123], a[140:143], a[60:63], v[44:47]\n" \
R_"a[152:155], v48 offset:6144\n" \
R_"a[140:143], v48 offset:3072\n" \
M_"v[120:123], a[136:139], a[56:59], v[120:123]\n" \
R_"a[136:139], v48 offset:2048\n" \
R_"a[156:159], v48 offset:7168\n" \
M_"v[120:123], a[132:135], a[52:55], v[120:123]\n" \
B_"v51, s[8:11], s64 offen offset:0 sc1 lds\n" \
M_"v[120:123], a[128:131], a[48:51], v[120:123]\n" \
R_"a[128:131], v48 offset:0\n" \
"s_waitcnt lgkmcnt(7)\n" \
M_"v[128:131], a[160:163], v[68:71], v[128:131]\n" \
E_"v102, v102\n" \
M_"v[160:163], a[160:163], v[72:75], v[160:163]\n" \
"v_exp_f32_e64 v103, v103\n" \
M_"v[132:135], a[164:167], v[68:71], v[132:135]\n" \
R_"a[132:135], v48 offset:1024\n" \
C_"v103, v102, v103\n" \
M_"v[164:167], a[164:167], v[72:75], v[164:167]\n" \
"v_exp_f32_e64 v100, v100\n" \
M_"v[136:139], a[168:171], v[68:71], v[136:139]\n" \
"v_exp_f32_e64 v101, v101\n" \
M_"v[168:171], a[168:171], v[72:75], v[168:171]\n" \
"v_exp_f32_e64 v98, v98\n" \
M_"v[140:143], a[172:175], v[68:71], v[140:143]\n" \
"v_exp_f32_e64 v99, v99\n" \
M_"v[172:175], a[172:175], v[72:75], v[172:175]\n" \
"v_exp_f32_e64 v96, v96\n" \
M_"v[144:147], a[176:179], v[68:71], v[144:147]\n" \
"v_exp_f32_e64 v97, v97\n" \
M_"v[176:179], a[176:179], v[72:75], v[176:179]\n" \
"v_exp_f32_e64 v104, v104\n" \
M_"v[148:151], a[180:183], v[68:71], v[148:151]\n" \
"v_exp_f32_e64 v105, v105\n" \
M_"v[180:183], a[180:183], v[72:75], v[180:183]\n" \
C_"v102, v100, v101\n" \
C_"v101, v98, v99\n" \
M_"v[152:155], a[184:187], v[68:71], v[152:155]\n" \
C_"v100, v96, v97\n" \
C_"v104, v104, v105\n" \
M_"v[184:187], a[184:187], v[72:75], v[184:187]\n" \
B_"v51, s[8:11], s64 offen offset:1024 sc1 lds\n" \
M_"v[156:159], a[188:191], v[68:71], v[156:159]\n" \
"v_exp_f32_e64 v106, v106\n" \
M_"v[188:191], a[188:191], v[72:75], v[188:191]\n" \
"v_exp_f32_e64 v107, v107\n" \
"v_smfmac_f32_16x16x64_bf16 a[96:99], a[104:107], v[68:75], v21\n" \
"v_exp_f32_e64 v108, v108\n" \
M_"v[220:223], a[188:191], v[84:87], v[220:223]\n" \
"v_exp_f32_e64 v109, v109\n" \
M_"v[252:255], a[188:191], v[88:91], v[252:255]\n" \
"v_exp_f32_e64 v110, v110\n" \
M_"v[216:219], a[184:187], v[84:87], v[216:219]\n" \
"v_exp_f32_e64 v111, v111\n" \
M_"v[248:251], a[184:187], v[88:91], v[248:251]\n" \
"v_exp_f32_e64 v118, v118\n" \
M_"v[212:215], a[180:183], v[84:87], v[212:215]\n" \
"v_exp_f32_e64 v119, v119\n" \
M_"v[244:247], a[180:183], v[88:91], v[244:247]\n" \
C_"v105, v106, v107\n" \
C_"v106, v108, v109\n" \
M_"v[208:211], a[176:179], v[84:87], v[208:211]\n" \
C_"v107, v110, v111\n" \
C_"v119, v118, v119\n" \
M_"v[240:243], a[176:179], v[88:91], v[240:243]\n" \
"v_exp_f32_e64 v116, v116\n" \
M_"v[204:207], a[172:175], v[84:87], v[204:207]\n" \
"v_exp_f32_e64 v117, v117\n" \
M_"v[236:239], a[172:175], v[88:91], v[236:239]\n" \
"v_exp_f32_e64 v114, v114\n" \
M_"v[200:203], a[168:171], v[84:87], v[200:203]\n" \
"v_exp_f32_e64 v115, v115\n" \
M_"v[232:235], a[168:171], v[88:91], v[232:235]\n" \
"v_exp_f32_e64 v112, v112\n" \
M_"v[196:199], a[164:167], v[84:87], v[196:199]\n" \
"v_exp_f32_e64 v113, v113\n" \
M_"v[228:231], a[164:167], v[88:91], v[228:231]\n" \
"v_exp_f32_e64 v120, v120\n" \
M_"v[192:195], a[160:163], v[84:87], v[192:195]\n" \
"v_exp_f32_e64 v121, v121\n" \
M_"v[224:227], a[160:163], v[88:91], v[224:227]\n" \
C_"v118, v116, v117\n" \
"s_waitcnt vmcnt(4) lgkmcnt(0)\n" \
"v_smfmac_f32_16x16x64_bf16 a[100:103], a[104:107], v[84:91], v21\n" \
"s_barrier\n" \
M_"v[64:67], a[128:131], a[0:3], v[32:35]\n" \
"s_xor_b32 m0, s65, 0x14000\n" \
R_"a[160:163], v49 offset:8192\n" \
M_"v[64:67], a[132:135], a[4:7], v[64:67]\n" \
B_"v52, s[8:11], s64 offen offset:0 sc1 lds\n" \
M_"v[64:67], a[136:139], a[8:11], v[64:67]\n" \
R_"a[164:167], v49 offset:9216\n" \
R_"a[168:171], v49 offset:10240\n" \
M_"v[64:67], a[140:143], a[12:15], v[64:67]\n" \
R_"a[172:175], v49 offset:11264\n" \
R_"a[176:179], v49 offset:12288\n" \
M_"v[68:71], a[156:159], a[12:15], v[32:35]\n" \
R_"a[180:183], v49 offset:13312\n" \
R_"a[184:187], v49 offset:14336\n" \
M_"v[68:71], a[152:155], a[8:11], v[68:71]\n" \
R_"a[188:191], v49 offset:15360\n" \
C_"v117, v114, v115\n" \
M_"v[68:71], a[148:151], a[4:7], v[68:71]\n" \
C_"v116, v112, v113\n" \
C_"v120, v120, v121\n" \
M_"v[68:71], a[144:147], a[0:3], v[68:71]\n" \
"v_exp_f32_e64 v122, v122\n" \
M_"v[76:79], a[144:147], a[16:19], v[36:39]\n" \
"v_exp_f32_e64 v123, v123\n" \
M_"v[76:79], a[148:151], a[20:23], v[76:79]\n" \
"v_exp_f32_e64 v124, v124\n" \
M_"v[76:79], a[152:155], a[24:27], v[76:79]\n" \
"v_exp_f32_e64 v125, v125\n" \
M_"v[76:79], a[156:159], a[28:31], v[76:79]\n" \
"v_exp_f32_e64 v126, v126\n" \
M_"v[72:75], a[140:143], a[28:31], v[36:39]\n" \
"v_exp_f32_e64 v127, v127\n" \
M_"v[72:75], a[136:139], a[24:27], v[72:75]\n" \
C_"v121, v122, v123\n" \
C_"v122, v124, v125\n" \
M_"v[72:75], a[132:135], a[20:23], v[72:75]\n" \
C_"v123, v126, v127\n" \
"v_add_u32_e32 v49, 0x10000, v48\n" \
M_"v[72:75], a[128:131], a[16:19], v[72:75]\n" \
B_"v52, s[8:11], s64 offen offset:1024 sc1 lds\n" \
M_"v[80:83], a[128:131], a[32:35], v[40:43]\n" \
"v_exp_f32_e64 v70, v70\n" \
M_"v[80:83], a[132:135], a[36:39], v[80:83]\n" \
"v_exp_f32_e64 v71, v71\n" \
M_"v[80:83], a[136:139], a[40:43], v[80:83]\n" \
"s_mov_b32 m0, s65\n" \
C_"v71, v70, v71\n" \
M_"v[80:83], a[140:143], a[44:47], v[80:83]\n" \
E_"v72, v72\n" \
M_"v[84:87], a[156:159], a[44:47], v[40:43]\n" \
"v_exp_f32_e64 v73, v73\n" \
M_"v[84:87], a[152:155], a[40:43], v[84:87]\n" \
"v_exp_f32_e64 v68, v68\n" \
M_"v[84:87], a[148:151], a[36:39], v[84:87]\n" \
"v_exp_f32_e64 v69, v69\n" \
M_"v[84:87], a[144:147], a[32:35], v[84:87]\n" \
"v_exp_f32_e64 v74, v74\n" \
M_"v[92:95], a[144:147], a[48:51], v[44:47]\n" \
R_"a[144:147], v48 offset:12288\n" \
C_"v72, v72, v73\n" \
M_"v[92:95], a[148:151], a[52:55], v[92:95]\n" \
R_"a[148:151], v48 offset:13312\n" \
C_"v70, v68, v69\n" \
M_"v[92:95], a[152:155], a[56:59], v[92:95]\n" \
R_"a[152:155], v48 offset:14336\n" \
"s_xor_b32 s65, s65, 0x4000\n" \
M_"v[92:95], a[156:159], a[60:63], v[92:95]\n" \
"v_exp_f32_e64 v75, v75\n" \
M_"v[88:91], a[140:143], a[60:63], v[44:47]\n" \
R_"a[140:143], v48 offset:11264\n" \
R_"a[156:159], v48 offset:15360\n" \
M_"v[88:91], a[136:139], a[56:59], v[88:91]\n" \
R_"a[136:139], v48 offset:10240\n" \
C_"v73, v74, v75\n" \
M_"v[88:91], a[132:135], a[52:55], v[88:91]\n" \
B_"v53, s[8:11], s64 offen offset:0 sc1 lds\n" \
M_"v[88:91], a[128:131], a[48:51], v[88:91]\n" \
R_"a[128:131], v48 offset:8192\n" \
"s_waitcnt lgkmcnt(7)\n" \
M_"v[128:131], a[160:163], v[100:103], v[128:131]\n" \
E_"v66, v66\n" \
M_"v[160:163], a[160:163], v[104:107], v[160:163]\n" \
"v_exp_f32_e64 v67, v67\n" \
M_"v[132:135], a[164:167], v[100:103], v[132:135]\n" \
R_"a[132:135], v48 offset:9216\n" \
C_"v69, v66, v67\n" \
M_"v[164:167], a[164:167], v[104:107], v[164:167]\n" \
"v_exp_f32_e64 v64, v64\n" \
M_"v[136:139], a[168:171], v[100:103], v[136:139]\n" \
"v_exp_f32_e64 v65, v65\n" \
M_"v[168:171], a[168:171], v[104:107], v[168:171]\n" \
"v_exp_f32_e64 v76, v76\n" \
M_"v[140:143], a[172:175], v[100:103], v[140:143]\n" \
"v_exp_f32_e64 v77, v77\n" \
M_"v[172:175], a[172:175], v[104:107], v[172:175]\n" \
"v_exp_f32_e64 v78, v78\n" \
M_"v[144:147], a[176:179], v[100:103], v[144:147]\n" \
"v_exp_f32_e64 v79, v79\n" \
M_"v[176:179], a[176:179], v[104:107], v[176:179]\n" \
C_"v68, v64, v65\n" \
C_"v74, v76, v77\n" \
M_"v[148:151], a[180:183], v[100:103], v[148:151]\n" \
C_"v75, v78, v79\n" \
"v_xor_b32_e32 v48, 0x4000, v48\n" \
M_"v[180:183], a[180:183], v[104:107], v[180:183]\n" \
M_"v[152:155], a[184:187], v[100:103], v[152:155]\n" \
M_"v[184:187], a[184:187], v[104:107], v[184:187]\n" \
B_"v53, s[8:11], s64 offen offset:1024 sc1 lds\n" \
M_"v[156:159], a[188:191], v[100:103], v[156:159]\n" \
"s_add_u32 s64, s64, 0x8000\n" \
"s_sub_u32 s66, s66, 1\n" \
"s_nop 0\n" \
M_"v[188:191], a[188:191], v[104:107], v[188:191]\n" \
"v_smfmac_f32_16x16x64_bf16 a[96:99], a[104:107], v[100:107], v21\n" \
M_"v[220:223], a[188:191], v[116:119], v[220:223]\n" \
M_"v[252:255], a[188:191], v[120:123], v[252:255]\n" \
M_"v[216:219], a[184:187], v[116:119], v[216:219]\n" \
M_"v[248:251], a[184:187], v[120:123], v[248:251]\n" \
M_"v[212:215], a[180:183], v[116:119], v[212:215]\n" \
M_"v[244:247], a[180:183], v[120:123], v[244:247]\n" \
M_"v[208:211], a[176:179], v[116:119], v[208:211]\n" \
M_"v[240:243], a[176:179], v[120:123], v[240:243]\n" \
M_"v[204:207], a[172:175], v[116:119], v[204:207]\n" \
M_"v[236:239], a[172:175], v[120:123], v[236:239]\n" \
M_"v[200:203], a[168:171], v[116:119], v[200:203]\n" \
M_"v[232:235], a[168:171], v[120:123], v[232:235]\n" \
M_"v[196:199], a[164:167], v[116:119], v[196:199]\n" \
M_"v[228:231], a[164:167], v[120:123], v[228:231]\n" \
M_"v[192:195], a[160:163], v[116:119], v[192:195]\n" \
M_"v[224:227], a[160:163], v[120:123], v[224:227]\n" \
"s_cbranch_scc0 L_tL_1_%=\n" \
"s_waitcnt vmcnt(4) lgkmcnt(0)\n" \
"v_smfmac_f32_16x16x64_bf16 a[100:103], a[104:107], v[116:123], v21\n" \
"s_barrier\n" \
"L_tr_%=:\n" \
"s_setpc_b64 s[28:29]\n" \
".fill (L_ab + 4096 - .) / 4, 4, 0xBF800000\n" \
".fill (L_ab + 4096 - .) / 4, 4, 0xBF800000\n" \
"L_ap1:\n" \
"s_setpc_b64 s[88:89]\n" \
"L_gtr:\n" \
M_"v[96:99], a[128:131], a[0:3], v[32:35]\n" \
"s_xor_b32 m0, s65, 0x12000\n" \
"s_mov_b32 s65, s13\n" \
M_"v[96:99], a[132:135], a[4:7], v[96:99]\n" \
B_"v50, s[8:11], s64 offen offset:0 sc1 lds\n" \
M_"v[96:99], a[136:139], a[8:11], v[96:99]\n" \
"v_add_u32_e32 v63, s15, v20\n" \
"s_add_u32 s67, s14, 40960\n" \
M_"v[96:99], a[140:143], a[12:15], v[96:99]\n" \
R_"a[160:163], v49 offset:0\n" \
R_"a[164:167], v49 offset:1024\n" \
M_"v[100:103], a[156:159], a[12:15], v[32:35]\n" \
B_"v50, s[8:11], s64 offen offset:1024 sc1 lds\n" \
M_"v[100:103], a[152:155], a[8:11], v[100:103]\n" \
"v_add_u32_e32 v50, 0x6000, v63\n" \
"s_xor_b32 m0, s65, 0x8000\n" \
M_"v[100:103], a[148:151], a[4:7], v[100:103]\n" \
R_"a[168:171], v49 offset:2048\n" \
R_"a[172:175], v49 offset:3072\n" \
M_"v[100:103], a[144:147], a[0:3], v[100:103]\n" \
R_"a[176:179], v49 offset:4096\n" \
R_"a[180:183], v49 offset:5120\n" \
M_"v[108:111], a[144:147], a[16:19], v[36:39]\n" \
B_"v50, s[8:11], s67 offen offset:0 sc1 lds\n" \
M_"v[108:111], a[148:151], a[20:23], v[108:111]\n" \
R_"a[184:187], v49 offset:6144\n" \
R_"a[188:191], v49 offset:7168\n" \
M_"v[108:111], a[152:155], a[24:27], v[108:111]\n" \
"s_mov_b32 s64, s14\n" \
"v_add_u32_e32 v51, 0x12000, v63\n" \
M_"v[108:111], a[156:159], a[28:31], v[108:111]\n" \
E_"v86, v86\n" \
M_"v[104:107], a[140:143], a[28:31], v[36:39]\n" \
B_"v50, s[8:11], s67 offen offset:1024 sc1 lds\n" \
M_"v[104:107], a[136:139], a[24:27], v[104:107]\n" \
E_"v87, v87\n" \
M_"v[104:107], a[132:135], a[20:23], v[104:107]\n" \
E_"v84, v84\n" \
M_"v[104:107], a[128:131], a[16:19], v[104:107]\n" \
E_"v85, v85\n" \
M_"v[112:115], a[128:131], a[32:35], v[40:43]\n" \
C_"v87, v86, v87\n" \
C_"v86, v84, v85\n" \
M_"v[112:115], a[132:135], a[36:39], v[112:115]\n" \
B_"v50, s[8:11], s67 offen offset:2048 sc1 lds\n" \
M_"v[112:115], a[136:139], a[40:43], v[112:115]\n" \
E_"v82, v82\n" \
M_"v[112:115], a[140:143], a[44:47], v[112:115]\n" \
E_"v83, v83\n" \
M_"v[116:119], a[156:159], a[44:47], v[40:43]\n" \
E_"v80, v80\n" \
M_"v[116:119], a[152:155], a[40:43], v[116:119]\n" \
B_"v50, s[8:11], s67 offen offset:3072 sc1 lds\n" \
M_"v[116:119], a[148:151], a[36:39], v[116:119]\n" \
"s_xor_b32 m0, s65, 0xa000\n" \
C_"v85, v82, v83\n" \
M_"v[116:119], a[144:147], a[32:35], v[116:119]\n" \
E_"v81, v81\n" \
M_"v[124:127], a[144:147], a[48:51], v[44:47]\n" \
C_"v84, v80, v81\n" \
"v_readlane_b32 s93, v11, 22\n" \
M_"v[124:127], a[148:151], a[52:55], v[124:127]\n" \
B_"v51, s[8:11], s64 offen offset:0 sc1 lds\n" \
M_"v[124:127], a[152:155], a[56:59], v[124:127]\n" \
E_"v88, v88\n" \
M_"v[124:127], a[156:159], a[60:63], v[124:127]\n" \
E_"v89, v89\n" \
M_"v[120:123], a[140:143], a[60:63], v[44:47]\n" \
E_"v90, v90\n" \
M_"v[120:123], a[136:139], a[56:59], v[120:123]\n" \
E_"v91, v91\n" \
M_"v[120:123], a[132:135], a[52:55], v[120:123]\n" \
B_"v51, s[8:11], s64 offen offset:1024 sc1 lds\n" \
M_"v[120:123], a[128:131], a[48:51], v[120:123]\n" \
C_"v88, v88, v89\n" \
"s_waitcnt lgkmcnt(0)\n" \
M_"v[128:131], a[160:163], v[68:71], v[128:131]\n" \
E_"v92, v92\n" \
M_"v[160:163], a[160:163], v[72:75], v[160:163]\n" \
E_"v93, v93\n" \
M_"v[132:135], a[164:167], v[68:71], v[132:135]\n" \
B_"v51, s[8:11], s64 offen offset:2048 sc1 lds\n" \
M_"v[164:167], a[164:167], v[72:75], v[164:167]\n" \
E_"v94, v94\n" \
M_"v[136:139], a[168:171], v[68:71], v[136:139]\n" \
E_"v95, v95\n" \
M_"v[168:171], a[168:171], v[72:75], v[168:171]\n" \
C_"v89, v90, v91\n" \
C_"v90, v92, v93\n" \
M_"v[140:143], a[172:175], v[68:71], v[140:143]\n" \
B_"v51, s[8:11], s64 offen offset:3072 sc1 lds\n" \
M_"v[172:175], a[172:175], v[72:75], v[172:175]\n" \
C_"v91, v94, v95\n" \
"v_readlane_b32 s80, v11, 11\n" \
M_"v[144:147], a[176:179], v[68:71], v[144:147]\n" \
"v_readlane_b32 s81, v11, 12\n" \
"s_mov_b32 s82, 0x47fca0\n" \
M_"v[176:179], a[176:179], v[72:75], v[176:179]\n" \
"s_mov_b32 s83, 0x20000\n" \
"v_readlane_b32 s92, v11, 29\n" \
M_"v[148:151], a[180:183], v[68:71], v[148:151]\n" \
"s_add_u32 m0, s93, 0x0\n" \
R_"a[128:131], v48 offset:0\n" \
M_"v[180:183], a[180:183], v[72:75], v[180:183]\n" \
R_"a[144:147], v48 offset:4096\n" \
R_"a[132:135], v48 offset:1024\n" \
M_"v[152:155], a[184:187], v[68:71], v[152:155]\n" \
B_"v0, s[80:83], s92 offen offset:0 lds\n" \
M_"v[184:187], a[184:187], v[72:75], v[184:187]\n" \
R_"a[148:151], v48 offset:5120\n" \
R_"a[136:139], v48 offset:2048\n" \
M_"v[156:159], a[188:191], v[68:71], v[156:159]\n" \
R_"a[152:155], v48 offset:6144\n" \
R_"a[140:143], v48 offset:3072\n" \
M_"v[188:191], a[188:191], v[72:75], v[188:191]\n" \
R_"a[156:159], v48 offset:7168\n" \
"v_add_u32_e32 v52, 0x14000, v63\n" \
"v_smfmac_f32_16x16x64_bf16 a[96:99], a[104:107], v[68:75], v21\n" \
B_"v1, s[80:83], s92 offen offset:1024 lds\n" \
M_"v[220:223], a[188:191], v[84:87], v[220:223]\n" \
E_"v102, v102\n" \
M_"v[252:255], a[188:191], v[88:91], v[252:255]\n" \
E_"v103, v103\n" \
M_"v[216:219], a[184:187], v[84:87], v[216:219]\n" \
B_"v2, s[80:83], s92 offen offset:2048 lds\n" \
M_"v[248:251], a[184:187], v[88:91], v[248:251]\n" \
"v_add_u32_e32 v53, 0x20000, v63\n" \
C_"v103, v102, v103\n" \
M_"v[212:215], a[180:183], v[84:87], v[212:215]\n" \
E_"v100, v100\n" \
M_"v[244:247], a[180:183], v[88:91], v[244:247]\n" \
E_"v101, v101\n" \
M_"v[208:211], a[176:179], v[84:87], v[208:211]\n" \
B_"v3, s[80:83], s92 offen offset:3072 lds\n" \
M_"v[240:243], a[176:179], v[88:91], v[240:243]\n" \
E_"v98, v98\n" \
M_"v[204:207], a[172:175], v[84:87], v[204:207]\n" \
E_"v99, v99\n" \
M_"v[236:239], a[172:175], v[88:91], v[236:239]\n" \
E_"v96, v96\n" \
M_"v[200:203], a[168:171], v[84:87], v[200:203]\n" \
E_"v97, v97\n" \
M_"v[232:235], a[168:171], v[88:91], v[232:235]\n" \
E_"v104, v104\n" \
M_"v[196:199], a[164:167], v[84:87], v[196:199]\n" \
E_"v105, v105\n" \
M_"v[228:231], a[164:167], v[88:91], v[228:231]\n" \
C_"v102, v100, v101\n" \
C_"v101, v98, v99\n" \
M_"v[192:195], a[160:163], v[84:87], v[192:195]\n" \
C_"v100, v96, v97\n" \
C_"v104, v104, v105\n" \
M_"v[224:227], a[160:163], v[88:91], v[224:227]\n" \
E_"v106, v106\n" \
"v_smfmac_f32_16x16x64_bf16 a[100:103], a[104:107], v[84:91], v21\n" \
E_"v107, v107\n" \
"s_waitcnt vmcnt(14) lgkmcnt(0)\n" \
"s_barrier\n" \
M_"v[64:67], a[128:131], a[0:3], v[32:35]\n" \
"s_xor_b32 m0, s65, 0x18000\n" \
R_"a[160:163], v49 offset:8192\n" \
M_"v[64:67], a[132:135], a[4:7], v[64:67]\n" \
B_"v52, s[8:11], s64 offen offset:0 sc1 lds\n" \
M_"v[64:67], a[136:139], a[8:11], v[64:67]\n" \
R_"a[164:167], v49 offset:9216\n" \
R_"a[168:171], v49 offset:10240\n" \
M_"v[64:67], a[140:143], a[12:15], v[64:67]\n" \
R_"a[172:175], v49 offset:11264\n" \
R_"a[176:179], v49 offset:12288\n" \
M_"v[68:71], a[156:159], a[12:15], v[32:35]\n" \
B_"v52, s[8:11], s64 offen offset:1024 sc1 lds\n" \
M_"v[68:71], a[152:155], a[8:11], v[68:71]\n" \
R_"a[180:183], v49 offset:13312\n" \
R_"a[184:187], v49 offset:14336\n" \
M_"v[68:71], a[148:151], a[4:7], v[68:71]\n" \
E_"v108, v108\n" \
M_"v[68:71], a[144:147], a[0:3], v[68:71]\n" \
E_"v109, v109\n" \
M_"v[76:79], a[144:147], a[16:19], v[36:39]\n" \
E_"v110, v110\n" \
M_"v[76:79], a[148:151], a[20:23], v[76:79]\n" \
B_"v52, s[8:11], s64 offen offset:2048 sc1 lds\n" \
M_"v[76:79], a[152:155], a[24:27], v[76:79]\n" \
E_"v111, v111\n" \
M_"v[76:79], a[156:159], a[28:31], v[76:79]\n" \
C_"v105, v106, v107\n" \
C_"v106, v108, v109\n" \
M_"v[72:75], a[140:143], a[28:31], v[36:39]\n" \
C_"v107, v110, v111\n" \
R_"a[188:191], v49 offset:15360\n" \
M_"v[72:75], a[136:139], a[24:27], v[72:75]\n" \
B_"v52, s[8:11], s64 offen offset:3072 sc1 lds\n" \
M_"v[72:75], a[132:135], a[20:23], v[72:75]\n" \
"s_mov_b32 m0, s65\n" \
"v_add_u32_e32 v49, 0x10000, v48\n" \
M_"v[72:75], a[128:131], a[16:19], v[72:75]\n" \
E_"v118, v118\n" \
M_"v[80:83], a[128:131], a[32:35], v[40:43]\n" \
E_"v119, v119\n" \
M_"v[80:83], a[132:135], a[36:39], v[80:83]\n" \
E_"v116, v116\n" \
M_"v[80:83], a[136:139], a[40:43], v[80:83]\n" \
B_"v53, s[8:11], s64 offen offset:0 sc1 lds\n" \
M_"v[80:83], a[140:143], a[44:47], v[80:83]\n" \
E_"v117, v117\n" \
M_"v[84:87], a[156:159], a[44:47], v[40:43]\n" \
C_"v119, v118, v119\n" \
C_"v118, v116, v117\n" \
M_"v[84:87], a[152:155], a[40:43], v[84:87]\n" \
E_"v114, v114\n" \
M_"v[84:87], a[148:151], a[36:39], v[84:87]\n" \
E_"v115, v115\n" \
M_"v[84:87], a[144:147], a[32:35], v[84:87]\n" \
B_"v53, s[8:11], s64 offen offset:1024 sc1 lds\n" \
M_"v[92:95], a[144:147], a[48:51], v[44:47]\n" \
E_"v112, v112\n" \
M_"v[92:95], a[148:151], a[52:55], v[92:95]\n" \
E_"v113, v113\n" \
M_"v[92:95], a[152:155], a[56:59], v[92:95]\n" \
C_"v117, v114, v115\n" \
C_"v116, v112, v113\n" \
M_"v[92:95], a[156:159], a[60:63], v[92:95]\n" \
B_"v53, s[8:11], s64 offen offset:2048 sc1 lds\n" \
M_"v[88:91], a[140:143], a[60:63], v[44:47]\n" \
E_"v120, v120\n" \
M_"v[88:91], a[136:139], a[56:59], v[88:91]\n" \
E_"v121, v121\n" \
M_"v[88:91], a[132:135], a[52:55], v[88:91]\n" \
E_"v122, v122\n" \
M_"v[88:91], a[128:131], a[48:51], v[88:91]\n" \
C_"v120, v120, v121\n" \
"s_waitcnt lgkmcnt(0)\n" \
M_"v[128:131], a[160:163], v[100:103], v[128:131]\n" \
B_"v53, s[8:11], s64 offen offset:3072 sc1 lds\n" \
M_"v[160:163], a[160:163], v[104:107], v[160:163]\n" \
"s_add_u32 m0, s93, 0x1000\n" \
R_"a[128:131], v48 offset:8192\n" \
M_"v[132:135], a[164:167], v[100:103], v[132:135]\n" \
E_"v123, v123\n" \
M_"v[164:167], a[164:167], v[104:107], v[164:167]\n" \
E_"v124, v124\n" \
M_"v[136:139], a[168:171], v[100:103], v[136:139]\n" \
B_"v4, s[80:83], s92 offen offset:0 lds\n" \
M_"v[168:171], a[168:171], v[104:107], v[168:171]\n" \
E_"v125, v125\n" \
M_"v[140:143], a[172:175], v[100:103], v[140:143]\n" \
E_"v126, v126\n" \
M_"v[172:175], a[172:175], v[104:107], v[172:175]\n" \
E_"v127, v127\n" \
M_"v[144:147], a[176:179], v[100:103], v[144:147]\n" \
C_"v121, v122, v123\n" \
C_"v122, v124, v125\n" \
M_"v[176:179], a[176:179], v[104:107], v[176:179]\n" \
B_"v5, s[80:83], s92 offen offset:1024 lds\n" \
M_"v[148:151], a[180:183], v[100:103], v[148:151]\n" \
C_"v123, v126, v127\n" \
R_"a[144:147], v48 offset:12288\n" \
M_"v[180:183], a[180:183], v[104:107], v[180:183]\n" \
R_"a[132:135], v48 offset:9216\n" \
R_"a[148:151], v48 offset:13312\n" \
M_"v[152:155], a[184:187], v[100:103], v[152:155]\n" \
R_"a[136:139], v48 offset:10240\n" \
R_"a[152:155], v48 offset:14336\n" \
M_"v[184:187], a[184:187], v[104:107], v[184:187]\n" \
R_"a[140:143], v48 offset:11264\n" \
R_"a[156:159], v48 offset:15360\n" \
M_"v[156:159], a[188:191], v[100:103], v[156:159]\n" \
B_"v6, s[80:83], s92 offen offset:2048 lds\n" \
M_"v[188:191], a[188:191], v[104:107], v[188:191]\n" \
E_"v70, v70\n" \
"v_smfmac_f32_16x16x64_bf16 a[96:99], a[104:107], v[100:107], v21\n" \
E_"v71, v71\n" \
M_"v[220:223], a[188:191], v[116:119], v[220:223]\n" \
E_"v72, v72\n" \
M_"v[252:255], a[188:191], v[120:123], v[252:255]\n" \
B_"v7, s[80:83], s92 offen offset:3072 lds\n" \
M_"v[216:219], a[184:187], v[116:119], v[216:219]\n" \
"s_add_u32 m0, s93, 0x2000\n" \
C_"v71, v70, v71\n" \
M_"v[248:251], a[184:187], v[120:123], v[248:251]\n" \
E_"v73, v73\n" \
M_"v[212:215], a[180:183], v[116:119], v[212:215]\n" \
E_"v68, v68\n" \
M_"v[244:247], a[180:183], v[120:123], v[244:247]\n" \
E_"v69, v69\n" \
M_"v[208:211], a[176:179], v[116:119], v[208:211]\n" \
B_"v8, s[80:83], s92 offen offset:0 lds\n" \
M_"v[240:243], a[176:179], v[120:123], v[240:243]\n" \
E_"v74, v74\n" \
M_"v[204:207], a[172:175], v[116:119], v[204:207]\n" \
E_"v75, v75\n" \
M_"v[236:239], a[172:175], v[120:123], v[236:239]\n" \
E_"v66, v66\n" \
M_"v[200:203], a[168:171], v[116:119], v[200:203]\n" \
E_"v67, v67\n" \
M_"v[232:235], a[168:171], v[120:123], v[232:235]\n" \
E_"v64, v64\n" \
M_"v[196:199], a[164:167], v[116:119], v[196:199]\n" \
E_"v65, v65\n" \
M_"v[228:231], a[164:167], v[120:123], v[228:231]\n" \
C_"v72, v72, v73\n" \
C_"v70, v68, v69\n" \
M_"v[192:195], a[160:163], v[116:119], v[192:195]\n" \
E_"v76, v76\n" \
M_"v[224:227], a[160:163], v[120:123], v[224:227]\n" \
E_"v77, v77\n" \
"v_smfmac_f32_16x16x64_bf16 a[100:103], a[104:107], v[116:123], v21\n" \
E_"v78, v78\n" \
"s_waitcnt vmcnt(13) lgkmcnt(0)\n" \
"s_barrier\n" \
E_"v79, v79\n" \
C_"v73, v74, v75\n" \
C_"v69, v66, v67\n" \
C_"v68, v64, v65\n" \
C_"v74, v76, v77\n" \
C_"v75, v78, v79\n" \
"v_add_u32_e32 v48, s16, v20\n" \
"s_xor_b32 s65, s65, 0x8000\n" \
"s_add_u32 s64, s64, 0x10000\n" \
"s_getpc_b64 s[86:87]\n" \
"L_hpt_%=:\n" \
"s_add_u32 s86, s86, L_ap8-L_hpt_%=\n" \
"s_addc_u32 s87, s87, 0\n" \
"s_add_u32 s86, s86, s15\n" \
"s_addc_u32 s87, s87, 0\n" \
"s_mov_b64 exec, 0\n" \
"s_swappc_b64 s[88:89], s[86:87]\n" \
"s_mov_b64 exec, -1\n" \
"s_sub_u32 s66, s19, 1\n" \
"s_cbranch_scc1 L_la_%=\n" \
"s_branch L_2_%=\n" \
".fill (L_ab + 6400 - .) / 4, 4, 0xBF800000\n" \
"L_tL_2_%=:\n" \
"s_waitcnt vmcnt(8) lgkmcnt(0)\n" \
"v_smfmac_f32_16x16x64_bf16 a[100:103], a[104:107], v[116:123], v21\n" \
"s_barrier\n" \
"L_2_%=:\n" \
M_"v[96:99], a[128:131], a[64:67], v[32:35]\n" \
"s_xor_b32 m0, s65, 0x12000\n" \
R_"a[160:163], v49 offset:0\n" \
M_"v[96:99], a[132:135], a[68:71], v[96:99]\n" \
B_"v50, s[8:11], s64 offen offset:0 sc1 lds\n" \
M_"v[96:99], a[136:139], a[72:75], v[96:99]\n" \
R_"a[164:167], v49 offset:1024\n" \
R_"a[168:171], v49 offset:2048\n" \
M_"v[96:99], a[140:143], a[76:79], v[96:99]\n" \
R_"a[172:175], v49 offset:3072\n" \
R_"a[176:179], v49 offset:4096\n" \
M_"v[100:103], a[156:159], a[76:79], v[32:35]\n" \
R_"a[180:183], v49 offset:5120\n" \
R_"a[184:187], v49 offset:6144\n" \
M_"v[100:103], a[152:155], a[72:75], v[100:103]\n" \
"v_exp_f32_e64 v86, v86\n" \
M_"v[100:103], a[148:151], a[68:71], v[100:103]\n" \
"v_exp_f32_e64 v87, v87\n" \
M_"v[100:103], a[144:147], a[64:67], v[100:103]\n" \
B_"v50, s[8:11], s64 offen offset:1024 sc1 lds\n" \
M_"v[108:111], a[144:147], a[80:83], v[36:39]\n" \
R_"a[188:191], v49 offset:7168\n" \
C_"v87, v86, v87\n" \
M_"v[108:111], a[148:151], a[84:87], v[108:111]\n" \
"v_exp_f32_e64 v84, v84\n" \
M_"v[108:111], a[152:155], a[88:91], v[108:111]\n" \
"v_exp_f32_e64 v85, v85\n" \
M_"v[108:111], a[156:159], a[92:95], v[108:111]\n" \
"v_exp_f32_e64 v82, v82\n" \
M_"v[104:107], a[140:143], a[92:95], v[36:39]\n" \
"v_exp_f32_e64 v83, v83\n" \
M_"v[104:107], a[136:139], a[88:91], v[104:107]\n" \
"v_exp_f32_e64 v80, v80\n" \
M_"v[104:107], a[132:135], a[84:87], v[104:107]\n" \
"v_exp_f32_e64 v81, v81\n" \
M_"v[104:107], a[128:131], a[80:83], v[104:107]\n" \
B_"v50, s[8:11], s64 offen offset:2048 sc1 lds\n" \
M_"v[112:115], a[128:131], a[192:195], v[40:43]\n" \
"v_exp_f32_e64 v88, v88\n" \
M_"v[112:115], a[132:135], a[196:199], v[112:115]\n" \
"v_exp_f32_e64 v89, v89\n" \
M_"v[112:115], a[136:139], a[200:203], v[112:115]\n" \
C_"v86, v84, v85\n" \
C_"v85, v82, v83\n" \
M_"v[112:115], a[140:143], a[204:207], v[112:115]\n" \
C_"v84, v80, v81\n" \
C_"v88, v88, v89\n" \
M_"v[116:119], a[156:159], a[204:207], v[40:43]\n" \
"v_exp_f32_e64 v90, v90\n" \
M_"v[116:119], a[152:155], a[200:203], v[116:119]\n" \
"v_exp_f32_e64 v91, v91\n" \
M_"v[116:119], a[148:151], a[196:199], v[116:119]\n" \
B_"v50, s[8:11], s64 offen offset:3072 sc1 lds\n" \
M_"v[116:119], a[144:147], a[192:195], v[116:119]\n" \
"s_xor_b32 m0, s65, 0xa000\n" \
C_"v89, v90, v91\n" \
M_"v[124:127], a[144:147], a[208:211], v[44:47]\n" \
"v_exp_f32_e64 v92, v92\n" \
M_"v[124:127], a[148:151], a[212:215], v[124:127]\n" \
"v_exp_f32_e64 v93, v93\n" \
M_"v[124:127], a[152:155], a[216:219], v[124:127]\n" \
"v_exp_f32_e64 v94, v94\n" \
M_"v[124:127], a[156:159], a[220:223], v[124:127]\n" \
"v_exp_f32_e64 v95, v95\n" \
M_"v[120:123], a[140:143], a[220:223], v[44:47]\n" \
C_"v90, v92, v93\n" \
C_"v91, v94, v95\n" \
M_"v[120:123], a[136:139], a[216:219], v[120:123]\n" \
R_"a[144:147], v48 offset:4096\n" \
R_"a[148:151], v48 offset:5120\n" \
M_"v[120:123], a[132:135], a[212:215], v[120:123]\n" \
B_"v51, s[8:11], s64 offen offset:0 sc1 lds\n" \
M_"v[120:123], a[128:131], a[208:211], v[120:123]\n" \
R_"a[128:131], v48 offset:0\n" \
"s_waitcnt lgkmcnt(3)\n" \
M_"v[128:131], a[160:163], v[68:71], v[128:131]\n" \
R_"a[132:135], v48 offset:1024\n" \
R_"a[136:139], v48 offset:2048\n" \
M_"v[160:163], a[160:163], v[72:75], v[160:163]\n" \
R_"a[152:155], v48 offset:6144\n" \
R_"a[140:143], v48 offset:3072\n" \
M_"v[132:135], a[164:167], v[68:71], v[132:135]\n" \
E_"v102, v102\n" \
M_"v[164:167], a[164:167], v[72:75], v[164:167]\n" \
"v_exp_f32_e64 v103, v103\n" \
M_"v[136:139], a[168:171], v[68:71], v[136:139]\n" \
R_"a[156:159], v48 offset:7168\n" \
C_"v103, v102, v103\n" \
M_"v[168:171], a[168:171], v[72:75], v[168:171]\n" \
B_"v51, s[8:11], s64 offen offset:1024 sc1 lds\n" \
M_"v[140:143], a[172:175], v[68:71], v[140:143]\n" \
"v_exp_f32_e64 v100, v100\n" \
M_"v[172:175], a[172:175], v[72:75], v[172:175]\n" \
"v_exp_f32_e64 v101, v101\n" \
M_"v[144:147], a[176:179], v[68:71], v[144:147]\n" \
"v_exp_f32_e64 v98, v98\n" \
M_"v[176:179], a[176:179], v[72:75], v[176:179]\n" \
"v_exp_f32_e64 v99, v99\n" \
M_"v[148:151], a[180:183], v[68:71], v[148:151]\n" \
"v_exp_f32_e64 v96, v96\n" \
M_"v[180:183], a[180:183], v[72:75], v[180:183]\n" \
"v_exp_f32_e64 v97, v97\n" \
M_"v[152:155], a[184:187], v[68:71], v[152:155]\n" \
"v_exp_f32_e64 v104, v104\n" \
M_"v[184:187], a[184:187], v[72:75], v[184:187]\n" \
B_"v51, s[8:11], s64 offen offset:2048 sc1 lds\n" \
M_"v[156:159], a[188:191], v[68:71], v[156:159]\n" \
"v_exp_f32_e64 v105, v105\n" \
M_"v[188:191], a[188:191], v[72:75], v[188:191]\n" \
C_"v102, v100, v101\n" \
C_"v101, v98, v99\n" \
"v_smfmac_f32_16x16x64_bf16 a[96:99], a[104:107], v[68:75], v21\n" \
C_"v100, v96, v97\n" \
C_"v104, v104, v105\n" \
M_"v[220:223], a[188:191], v[84:87], v[220:223]\n" \
"v_exp_f32_e64 v106, v106\n" \
M_"v[252:255], a[188:191], v[88:91], v[252:255]\n" \
"v_exp_f32_e64 v107, v107\n" \
M_"v[216:219], a[184:187], v[84:87], v[216:219]\n" \
"v_exp_f32_e64 v108, v108\n" \
M_"v[248:251], a[184:187], v[88:91], v[248:251]\n" \
B_"v51, s[8:11], s64 offen offset:3072 sc1 lds\n" \
M_"v[212:215], a[180:183], v[84:87], v[212:215]\n" \
"v_exp_f32_e64 v109, v109\n" \
M_"v[244:247], a[180:183], v[88:91], v[244:247]\n" \
"v_exp_f32_e64 v110, v110\n" \
M_"v[208:211], a[176:179], v[84:87], v[208:211]\n" \
"v_exp_f32_e64 v111, v111\n" \
M_"v[240:243], a[176:179], v[88:91], v[240:243]\n" \
"v_exp_f32_e64 v118, v118\n" \
M_"v[204:207], a[172:175], v[84:87], v[204:207]\n" \
"v_exp_f32_e64 v119, v119\n" \
M_"v[236:239], a[172:175], v[88:91], v[236:239]\n" \
C_"v105, v106, v107\n" \
C_"v106, v108, v109\n" \
M_"v[200:203], a[168:171], v[84:87], v[200:203]\n" \
C_"v107, v110, v111\n" \
C_"v119, v118, v119\n" \
M_"v[232:235], a[168:171], v[88:91], v[232:235]\n" \
"v_exp_f32_e64 v116, v116\n" \
M_"v[196:199], a[164:167], v[84:87], v[196:199]\n" \
"v_exp_f32_e64 v117, v117\n" \
M_"v[228:231], a[164:167], v[88:91], v[228:231]\n" \
"v_exp_f32_e64 v114, v114\n" \
M_"v[192:195], a[160:163], v[84:87], v[192:195]\n" \
"v_exp_f32_e64 v115, v115\n" \
M_"v[224:227], a[160:163], v[88:91], v[224:227]\n" \
C_"v118, v116, v117\n" \
"s_waitcnt vmcnt(8) lgkmcnt(0)\n" \
"v_smfmac_f32_16x16x64_bf16 a[100:103], a[104:107], v[84:91], v21\n" \
"s_barrier\n" \
M_"v[64:67], a[128:131], a[64:67], v[32:35]\n" \
"s_xor_b32 m0, s65, 0x18000\n" \
R_"a[160:163], v49 offset:8192\n" \
M_"v[64:67], a[132:135], a[68:71], v[64:67]\n" \
B_"v52, s[8:11], s64 offen offset:0 sc1 lds\n" \
M_"v[64:67], a[136:139], a[72:75], v[64:67]\n" \
R_"a[164:167], v49 offset:9216\n" \
R_"a[168:171], v49 offset:10240\n" \
M_"v[64:67], a[140:143], a[76:79], v[64:67]\n" \
R_"a[172:175], v49 offset:11264\n" \
R_"a[176:179], v49 offset:12288\n" \
M_"v[68:71], a[156:159], a[76:79], v[32:35]\n" \
R_"a[180:183], v49 offset:13312\n" \
R_"a[184:187], v49 offset:14336\n" \
M_"v[68:71], a[152:155], a[72:75], v[68:71]\n" \
R_"a[188:191], v49 offset:15360\n" \
C_"v117, v114, v115\n" \
M_"v[68:71], a[148:151], a[68:71], v[68:71]\n" \
"v_exp_f32_e64 v112, v112\n" \
M_"v[68:71], a[144:147], a[64:67], v[68:71]\n" \
B_"v52, s[8:11], s64 offen offset:1024 sc1 lds\n" \
M_"v[76:79], a[144:147], a[80:83], v[36:39]\n" \
"v_exp_f32_e64 v113, v113\n" \
M_"v[76:79], a[148:151], a[84:87], v[76:79]\n" \
C_"v116, v112, v113\n" \
"v_add_u32_e32 v49, 0x10000, v48\n" \
M_"v[76:79], a[152:155], a[88:91], v[76:79]\n" \
"v_exp_f32_e64 v120, v120\n" \
M_"v[76:79], a[156:159], a[92:95], v[76:79]\n" \
"v_exp_f32_e64 v121, v121\n" \
M_"v[72:75], a[140:143], a[92:95], v[36:39]\n" \
"v_exp_f32_e64 v122, v122\n" \
M_"v[72:75], a[136:139], a[88:91], v[72:75]\n" \
"v_exp_f32_e64 v123, v123\n" \
M_"v[72:75], a[132:135], a[84:87], v[72:75]\n" \
"v_exp_f32_e64 v124, v124\n" \
M_"v[72:75], a[128:131], a[80:83], v[72:75]\n" \
B_"v52, s[8:11], s64 offen offset:2048 sc1 lds\n" \
M_"v[80:83], a[128:131], a[192:195], v[40:43]\n" \
"v_exp_f32_e64 v125, v125\n" \
M_"v[80:83], a[132:135], a[196:199], v[80:83]\n" \
"v_exp_f32_e64 v126, v126\n" \
M_"v[80:83], a[136:139], a[200:203], v[80:83]\n" \
"v_exp_f32_e64 v127, v127\n" \
M_"v[80:83], a[140:143], a[204:207], v[80:83]\n" \
"v_exp_f32_e64 v70, v70\n" \
M_"v[84:87], a[156:159], a[204:207], v[40:43]\n" \
"v_exp_f32_e64 v71, v71\n" \
M_"v[84:87], a[152:155], a[200:203], v[84:87]\n" \
C_"v120, v120, v121\n" \
C_"v121, v122, v123\n" \
M_"v[84:87], a[148:151], a[196:199], v[84:87]\n" \
B_"v52, s[8:11], s64 offen offset:3072 sc1 lds\n" \
M_"v[84:87], a[144:147], a[192:195], v[84:87]\n" \
"s_mov_b32 m0, s65\n" \
"s_nop 0\n" \
C_"v122, v124, v125\n" \
M_"v[92:95], a[144:147], a[208:211], v[44:47]\n" \
C_"v123, v126, v127\n" \
R_"a[144:147], v48 offset:12288\n" \
M_"v[92:95], a[148:151], a[212:215], v[92:95]\n" \
R_"a[148:151], v48 offset:13312\n" \
C_"v71, v70, v71\n" \
M_"v[92:95], a[152:155], a[216:219], v[92:95]\n" \
R_"a[152:155], v48 offset:14336\n" \
"s_xor_b32 s65, s65, 0x8000\n" \
M_"v[92:95], a[156:159], a[220:223], v[92:95]\n" \
"v_exp_f32_e64 v72, v72\n" \
M_"v[88:91], a[140:143], a[220:223], v[44:47]\n" \
R_"a[140:143], v48 offset:11264\n" \
R_"a[156:159], v48 offset:15360\n" \
M_"v[88:91], a[136:139], a[216:219], v[88:91]\n" \
"v_exp_f32_e64 v73, v73\n" \
M_"v[88:91], a[132:135], a[212:215], v[88:91]\n" \
".fill (L_ab + 8192 - .) / 4, 4, 0xBF800000\n" \
"L_ap2:\n" \
"s_cbranch_execz L_ap0\n" \
B_"v53, s[8:11], s64 offen offset:0 sc1 lds\n" \
M_"v[88:91], a[128:131], a[208:211], v[88:91]\n" \
R_"a[128:131], v48 offset:8192\n" \
"s_waitcnt lgkmcnt(6)\n" \
M_"v[128:131], a[160:163], v[100:103], v[128:131]\n" \
R_"a[132:135], v48 offset:9216\n" \
R_"a[136:139], v48 offset:10240\n" \
M_"v[160:163], a[160:163], v[104:107], v[160:163]\n" \
"v_exp_f32_e64 v68, v68\n" \
M_"v[132:135], a[164:167], v[100:103], v[132:135]\n" \
"v_exp_f32_e64 v69, v69\n" \
M_"v[164:167], a[164:167], v[104:107], v[164:167]\n" \
"v_exp_f32_e64 v74, v74\n" \
M_"v[136:139], a[168:171], v[100:103], v[136:139]\n" \
"v_exp_f32_e64 v75, v75\n" \
M_"v[168:171], a[168:171], v[104:107], v[168:171]\n" \
B_"v53, s[8:11], s64 offen offset:1024 sc1 lds\n" \
M_"v[140:143], a[172:175], v[100:103], v[140:143]\n" \
"v_exp_f32_e64 v66, v66\n" \
M_"v[172:175], a[172:175], v[104:107], v[172:175]\n" \
"v_exp_f32_e64 v67, v67\n" \
M_"v[144:147], a[176:179], v[100:103], v[144:147]\n" \
"v_exp_f32_e64 v64, v64\n" \
M_"v[176:179], a[176:179], v[104:107], v[176:179]\n" \
"v_exp_f32_e64 v65, v65\n" \
M_"v[148:151], a[180:183], v[100:103], v[148:151]\n" \
C_"v72, v72, v73\n" \
C_"v70, v68, v69\n" \
M_"v[180:183], a[180:183], v[104:107], v[180:183]\n" \
"v_exp_f32_e64 v76, v76\n" \
M_"v[152:155], a[184:187], v[100:103], v[152:155]\n" \
"v_exp_f32_e64 v77, v77\n" \
M_"v[184:187], a[184:187], v[104:107], v[184:187]\n" \
B_"v53, s[8:11], s64 offen offset:2048 sc1 lds\n" \
M_"v[156:159], a[188:191], v[100:103], v[156:159]\n" \
"v_exp_f32_e64 v78, v78\n" \
M_"v[188:191], a[188:191], v[104:107], v[188:191]\n" \
"v_exp_f32_e64 v79, v79\n" \
"v_smfmac_f32_16x16x64_bf16 a[96:99], a[104:107], v[100:107], v21\n" \
C_"v73, v74, v75\n" \
C_"v69, v66, v67\n" \
M_"v[220:223], a[188:191], v[116:119], v[220:223]\n" \
C_"v68, v64, v65\n" \
C_"v74, v76, v77\n" \
M_"v[252:255], a[188:191], v[120:123], v[252:255]\n" \
C_"v75, v78, v79\n" \
"v_xor_b32_e32 v48, 0x8000, v48\n" \
M_"v[216:219], a[184:187], v[116:119], v[216:219]\n" \
M_"v[248:251], a[184:187], v[120:123], v[248:251]\n" \
B_"v53, s[8:11], s64 offen offset:3072 sc1 lds\n" \
M_"v[212:215], a[180:183], v[116:119], v[212:215]\n" \
"s_add_u32 s64, s64, 0x10000\n" \
"s_sub_u32 s66, s66, 1\n" \
"s_nop 0\n" \
M_"v[244:247], a[180:183], v[120:123], v[244:247]\n" \
M_"v[208:211], a[176:179], v[116:119], v[208:211]\n" \
M_"v[240:243], a[176:179], v[120:123], v[240:243]\n" \
M_"v[204:207], a[172:175], v[116:119], v[204:207]\n" \
M_"v[236:239], a[172:175], v[120:123], v[236:239]\n" \
M_"v[200:203], a[168:171], v[116:119], v[200:203]\n" \
M_"v[232:235], a[168:171], v[120:123], v[232:235]\n" \
M_"v[196:199], a[164:167], v[116:119], v[196:199]\n" \
M_"v[228:231], a[164:167], v[120:123], v[228:231]\n" \
M_"v[192:195], a[160:163], v[116:119], v[192:195]\n" \
M_"v[224:227], a[160:163], v[120:123], v[224:227]\n" \
"s_cbranch_scc0 L_tL_2_%=\n" \
"s_waitcnt vmcnt(8) lgkmcnt(0)\n" \
"v_smfmac_f32_16x16x64_bf16 a[100:103], a[104:107], v[116:123], v21\n" \
"s_barrier\n" \
"L_la_%=:\n" \
M_"v[96:99], a[128:131], a[64:67], v[32:35]\n" \
"v_readlane_b32 s96, v11, 1\n" \
"v_lshrrev_b32_e32 v16, 1, v20\n" \
M_"v[96:99], a[132:135], a[68:71], v[96:99]\n" \
"s_xor_b32 m0, s65, 0x12000\n" \
R_"a[160:163], v49 offset:0\n" \
M_"v[96:99], a[136:139], a[72:75], v[96:99]\n" \
"buffer_load_dwordx2 v[26:27], v16, s[56:59], s96 offen sc1\n" \
M_"v[96:99], a[140:143], a[76:79], v[96:99]\n" \
B_"v50, s[8:11], s64 offen offset:0 sc1 lds\n" \
M_"v[100:103], a[156:159], a[76:79], v[32:35]\n" \
R_"a[164:167], v49 offset:1024\n" \
R_"a[168:171], v49 offset:2048\n" \
M_"v[100:103], a[152:155], a[72:75], v[100:103]\n" \
R_"a[172:175], v49 offset:3072\n" \
R_"a[176:179], v49 offset:4096\n" \
M_"v[100:103], a[148:151], a[68:71], v[100:103]\n" \
R_"a[180:183], v49 offset:5120\n" \
R_"a[184:187], v49 offset:6144\n" \
M_"v[100:103], a[144:147], a[64:67], v[100:103]\n" \
R_"a[188:191], v49 offset:7168\n" \
"s_mov_b32 s63, s62\n" \
M_"v[108:111], a[144:147], a[80:83], v[36:39]\n" \
E_"v86, v86\n" \
M_"v[108:111], a[148:151], a[84:87], v[108:111]\n" \
"v_exp_f32_e64 v87, v87\n" \
M_"v[108:111], a[152:155], a[88:91], v[108:111]\n" \
B_"v50, s[8:11], s64 offen offset:1024 sc1 lds\n" \
M_"v[108:111], a[156:159], a[92:95], v[108:111]\n" \
"v_exp_f32_e64 v84, v84\n" \
M_"v[104:107], a[140:143], a[92:95], v[36:39]\n" \
"v_exp_f32_e64 v85, v85\n" \
M_"v[104:107], a[136:139], a[88:91], v[104:107]\n" \
C_"v87, v86, v87\n" \
C_"v86, v84, v85\n" \
M_"v[104:107], a[132:135], a[84:87], v[104:107]\n" \
"v_exp_f32_e64 v82, v82\n" \
M_"v[104:107], a[128:131], a[80:83], v[104:107]\n" \
"v_exp_f32_e64 v83, v83\n" \
M_"v[112:115], a[128:131], a[192:195], v[40:43]\n" \
"v_exp_f32_e64 v80, v80\n" \
M_"v[112:115], a[132:135], a[196:199], v[112:115]\n" \
"v_exp_f32_e64 v81, v81\n" \
M_"v[112:115], a[136:139], a[200:203], v[112:115]\n" \
B_"v50, s[8:11], s64 offen offset:2048 sc1 lds\n" \
M_"v[112:115], a[140:143], a[204:207], v[112:115]\n" \
C_"v85, v82, v83\n" \
C_"v84, v80, v81\n" \
M_"v[116:119], a[156:159], a[204:207], v[40:43]\n" \
"v_exp_f32_e64 v88, v88\n" \
M_"v[116:119], a[152:155], a[200:203], v[116:119]\n" \
"v_exp_f32_e64 v89, v89\n" \
M_"v[116:119], a[148:151], a[196:199], v[116:119]\n" \
"v_exp_f32_e64 v90, v90\n" \
M_"v[116:119], a[144:147], a[192:195], v[116:119]\n" \
"v_exp_f32_e64 v91, v91\n" \
M_"v[124:127], a[144:147], a[208:211], v[44:47]\n" \
C_"v88, v88, v89\n" \
C_"v89, v90, v91\n" \
M_"v[124:127], a[148:151], a[212:215], v[124:127]\n" \
B_"v50, s[8:11], s64 offen offset:3072 sc1 lds\n" \
M_"v[124:127], a[152:155], a[216:219], v[124:127]\n" \
"v_exp_f32_e64 v92, v92\n" \
M_"v[124:127], a[156:159], a[220:223], v[124:127]\n" \
"v_exp_f32_e64 v93, v93\n" \
M_"v[120:123], a[140:143], a[220:223], v[44:47]\n" \
"v_exp_f32_e64 v94, v94\n" \
M_"v[120:123], a[136:139], a[216:219], v[120:123]\n" \
"v_exp_f32_e64 v95, v95\n" \
M_"v[120:123], a[132:135], a[212:215], v[120:123]\n" \
C_"v90, v92, v93\n" \
C_"v91, v94, v95\n" \
M_"v[120:123], a[128:131], a[208:211], v[120:123]\n" \
"v_readlane_b32 s97, v11, 39\n" \
"s_waitcnt lgkmcnt(0)\n" \
"s_nop 0\n" \
M_"v[128:131], a[160:163], v[68:71], v[128:131]\n" \
R_"a[128:131], v48 offset:0\n" \
R_"a[144:147], v48 offset:4096\n" \
M_"v[160:163], a[160:163], v[72:75], v[160:163]\n" \
R_"a[132:135], v48 offset:1024\n" \
R_"a[148:151], v48 offset:5120\n" \
M_"v[132:135], a[164:167], v[68:71], v[132:135]\n" \
R_"a[136:139], v48 offset:2048\n" \
R_"a[152:155], v48 offset:6144\n" \
M_"v[164:167], a[164:167], v[72:75], v[164:167]\n" \
R_"a[140:143], v48 offset:3072\n" \
R_"a[156:159], v48 offset:7168\n" \
M_"v[136:139], a[168:171], v[68:71], v[136:139]\n" \
"v_add_u32_e32 v17, 0x49000, v20\n" \
"v_readlane_b32 s98, v11, 40\n" \
M_"v[168:171], a[168:171], v[72:75], v[168:171]\n" \
"v_readlane_b32 s93, v11, 23\n" \
"v_readlane_b32 s84, v11, 15\n" \
M_"v[140:143], a[172:175], v[68:71], v[140:143]\n" \
"v_readlane_b32 s85, v11, 16\n" \
"v_readlane_b32 s86, v11, 17\n" \
M_"v[172:175], a[172:175], v[72:75], v[172:175]\n" \
"s_mov_b32 s87, 0x20000\n" \
"v_readlane_b32 s92, v11, 18\n" \
M_"v[144:147], a[176:179], v[68:71], v[144:147]\n" \
"v_exp_f32_e64 v102, v102\n" \
M_"v[176:179], a[176:179], v[72:75], v[176:179]\n" \
"v_exp_f32_e64 v103, v103\n" \
M_"v[148:151], a[180:183], v[68:71], v[148:151]\n" \
"v_exp_f32_e64 v100, v100\n" \
M_"v[180:183], a[180:183], v[72:75], v[180:183]\n" \
"v_exp_f32_e64 v101, v101\n" \
M_"v[152:155], a[184:187], v[68:71], v[152:155]\n" \
C_"v103, v102, v103\n" \
C_"v102, v100, v101\n" \
M_"v[184:187], a[184:187], v[72:75], v[184:187]\n" \
"v_exp_f32_e64 v98, v98\n" \
M_"v[156:159], a[188:191], v[68:71], v[156:159]\n" \
"v_exp_f32_e64 v99, v99\n" \
M_"v[188:191], a[188:191], v[72:75], v[188:191]\n" \
"s_waitcnt vmcnt(4)\n" \
"v_cmp_ne_u64_e32 vcc, s[62:63], v[26:27]\n" \
"v_smfmac_f32_16x16x64_bf16 a[96:99], a[104:107], v[68:75], v21\n" \
"v_exp_f32_e64 v96, v96\n" \
M_"v[220:223], a[188:191], v[84:87], v[220:223]\n" \
"v_exp_f32_e64 v97, v97\n" \
M_"v[252:255], a[188:191], v[88:91], v[252:255]\n" \
"s_cbranch_vccnz L_sol_%=\n" \
"L_rol_%=:\n" \
C_"v101, v98, v99\n" \
B_"a[124:127], v20, s[56:59], s97 offen offset:0 sc1\n" \
M_"v[216:219], a[184:187], v[84:87], v[216:219]\n" \
C_"v100, v96, v97\n" \
"v_readlane_b32 s91, v11, 24\n" \
M_"v[248:251], a[184:187], v[88:91], v[248:251]\n" \
E_"v104, v104\n" \
M_"v[212:215], a[180:183], v[84:87], v[212:215]\n" \
"v_exp_f32_e64 v105, v105\n" \
M_"v[244:247], a[180:183], v[88:91], v[244:247]\n" \
"v_exp_f32_e64 v106, v106\n" \
M_"v[208:211], a[176:179], v[84:87], v[208:211]\n" \
"v_exp_f32_e64 v107, v107\n" \
M_"v[240:243], a[176:179], v[88:91], v[240:243]\n" \
B_"a[228:231], v17, s[56:59], s97 offen offset:0 sc1\n" \
M_"v[204:207], a[172:175], v[84:87], v[204:207]\n" \
C_"v104, v104, v105\n" \
C_"v105, v106, v107\n" \
M_"v[236:239], a[172:175], v[88:91], v[236:239]\n" \
"v_exp_f32_e64 v108, v108\n" \
M_"v[200:203], a[168:171], v[84:87], v[200:203]\n" \
"v_exp_f32_e64 v109, v109\n" \
M_"v[232:235], a[168:171], v[88:91], v[232:235]\n" \
"v_exp_f32_e64 v110, v110\n" \
M_"v[196:199], a[164:167], v[84:87], v[196:199]\n" \
"v_exp_f32_e64 v111, v111\n" \
M_"v[228:231], a[164:167], v[88:91], v[228:231]\n" \
C_"v106, v108, v109\n" \
C_"v107, v110, v111\n" \
M_"v[192:195], a[160:163], v[84:87], v[192:195]\n" \
"v_exp_f32_e64 v118, v118\n" \
M_"v[224:227], a[160:163], v[88:91], v[224:227]\n" \
"s_xor_b32 s65, s65, 0x8000\n" \
"s_waitcnt vmcnt(7) lgkmcnt(0)\n" \
"v_smfmac_f32_16x16x64_bf16 a[100:103], a[104:107], v[84:91], v21\n" \
"s_barrier\n" \
M_"v[64:67], a[128:131], a[64:67], v[32:35]\n" \
B_"a[236:239], v20, s[56:59], s98 offen offset:0 sc1\n" \
M_"v[64:67], a[132:135], a[68:71], v[64:67]\n" \
"s_add_u32 m0, s93, 0x0\n" \
"s_nop 0\n" \
R_"a[160:163], v49 offset:8192\n" \
M_"v[64:67], a[136:139], a[72:75], v[64:67]\n" \
B_"v0, s[84:87], s92 offen offset:0 lds\n" \
M_"v[64:67], a[140:143], a[76:79], v[64:67]\n" \
R_"a[164:167], v49 offset:9216\n" \
R_"a[168:171], v49 offset:10240\n" \
M_"v[68:71], a[156:159], a[76:79], v[32:35]\n" \
R_"a[172:175], v49 offset:11264\n" \
R_"a[176:179], v49 offset:12288\n" \
M_"v[68:71], a[152:155], a[72:75], v[68:71]\n" \
R_"a[180:183], v49 offset:13312\n" \
R_"a[184:187], v49 offset:14336\n" \
M_"v[68:71], a[148:151], a[68:71], v[68:71]\n" \
B_"v1, s[84:87], s92 offen offset:1024 lds\n" \
M_"v[68:71], a[144:147], a[64:67], v[68:71]\n" \
B_"a[244:247], v17, s[56:59], s98 offen offset:0 sc1\n" \
M_"v[76:79], a[144:147], a[80:83], v[36:39]\n" \
R_"a[188:191], v49 offset:15360\n" \
"v_add_u32_e32 v49, 0x10000, v48\n" \
M_"v[76:79], a[148:151], a[84:87], v[76:79]\n" \
"v_exp_f32_e64 v119, v119\n" \
M_"v[76:79], a[152:155], a[88:91], v[76:79]\n" \
"v_exp_f32_e64 v116, v116\n" \
M_"v[76:79], a[156:159], a[92:95], v[76:79]\n" \
"v_exp_f32_e64 v117, v117\n" \
M_"v[72:75], a[140:143], a[92:95], v[36:39]\n" \
C_"v119, v118, v119\n" \
C_"v118, v116, v117\n" \
M_"v[72:75], a[136:139], a[88:91], v[72:75]\n" \
B_"v2, s[84:87], s92 offen offset:2048 lds\n" \
M_"v[72:75], a[132:135], a[84:87], v[72:75]\n" \
"v_exp_f32_e64 v114, v114\n" \
M_"v[72:75], a[128:131], a[80:83], v[72:75]\n" \
"v_exp_f32_e64 v115, v115\n" \
M_"v[80:83], a[128:131], a[192:195], v[40:43]\n" \
B_"a[224:227], v20, s[56:59], s97 offen offset:1024 sc1\n" \
M_"v[80:83], a[132:135], a[196:199], v[80:83]\n" \
"v_exp_f32_e64 v112, v112\n" \
M_"v[80:83], a[136:139], a[200:203], v[80:83]\n" \
"v_exp_f32_e64 v113, v113\n" \
M_"v[80:83], a[140:143], a[204:207], v[80:83]\n" \
C_"v117, v114, v115\n" \
C_"v116, v112, v113\n" \
M_"v[84:87], a[156:159], a[204:207], v[40:43]\n" \
B_"v3, s[84:87], s92 offen offset:3072 lds\n" \
M_"v[84:87], a[152:155], a[200:203], v[84:87]\n" \
"s_add_u32 m0, s91, 0x1000\n" \
"s_add_u32 s64, s64, 0x10000\n" \
M_"v[84:87], a[148:151], a[196:199], v[84:87]\n" \
"v_exp_f32_e64 v120, v120\n" \
M_"v[84:87], a[144:147], a[192:195], v[84:87]\n" \
"v_exp_f32_e64 v121, v121\n" \
M_"v[92:95], a[144:147], a[208:211], v[44:47]\n" \
"v_exp_f32_e64 v122, v122\n" \
M_"v[92:95], a[148:151], a[212:215], v[92:95]\n" \
B_"a[232:235], v17, s[56:59], s97 offen offset:1024 sc1\n" \
M_"v[92:95], a[152:155], a[216:219], v[92:95]\n" \
B_"v4, s[84:87], s92 offen offset:0 lds\n" \
M_"v[92:95], a[156:159], a[220:223], v[92:95]\n" \
"v_exp_f32_e64 v123, v123\n" \
M_"v[88:91], a[140:143], a[220:223], v[44:47]\n" \
C_"v120, v120, v121\n" \
C_"v121, v122, v123\n" \
M_"v[88:91], a[136:139], a[216:219], v[88:91]\n" \
"v_exp_f32_e64 v124, v124\n" \
M_"v[88:91], a[132:135], a[212:215], v[88:91]\n" \
"v_exp_f32_e64 v125, v125\n" \
M_"v[88:91], a[128:131], a[208:211], v[88:91]\n" \
C_"v122, v124, v125\n" \
"s_waitcnt lgkmcnt(0)\n" \
M_"v[128:131], a[160:163], v[100:103], v[128:131]\n" \
E_"v126, v126\n" \
M_"v[160:163], a[160:163], v[104:107], v[160:163]\n" \
B_"v5, s[84:87], s92 offen offset:1024 lds\n" \
M_"v[132:135], a[164:167], v[100:103], v[132:135]\n" \
B_"a[240:243], v20, s[56:59], s98 offen offset:1024 sc1\n" \
M_"v[164:167], a[164:167], v[104:107], v[164:167]\n" \
"v_exp_f32_e64 v127, v127\n" \
M_"v[136:139], a[168:171], v[100:103], v[136:139]\n" \
C_"v123, v126, v127\n" \
R_"a[128:131], v48 offset:8192\n" \
M_"v[168:171], a[168:171], v[104:107], v[168:171]\n" \
R_"a[144:147], v48 offset:12288\n" \
R_"a[132:135], v48 offset:9216\n" \
M_"v[140:143], a[172:175], v[100:103], v[140:143]\n" \
R_"a[148:151], v48 offset:13312\n" \
R_"a[136:139], v48 offset:10240\n" \
M_"v[172:175], a[172:175], v[104:107], v[172:175]\n" \
R_"a[152:155], v48 offset:14336\n" \
R_"a[140:143], v48 offset:11264\n" \
M_"v[144:147], a[176:179], v[100:103], v[144:147]\n" \
B_"v6, s[84:87], s92 offen offset:2048 lds\n" \
M_"v[176:179], a[176:179], v[104:107], v[176:179]\n" \
"v_exp_f32_e64 v70, v70\n" \
M_"v[148:151], a[180:183], v[100:103], v[148:151]\n" \
"v_exp_f32_e64 v71, v71\n" \
M_"v[180:183], a[180:183], v[104:107], v[180:183]\n" \
B_"a[248:251], v17, s[56:59], s98 offen offset:1024 sc1\n" \
M_"v[152:155], a[184:187], v[100:103], v[152:155]\n" \
R_"a[156:159], v48 offset:15360\n" \
C_"v71, v70, v71\n" \
M_"v[184:187], a[184:187], v[104:107], v[184:187]\n" \
"v_exp_f32_e64 v72, v72\n" \
M_"v[156:159], a[188:191], v[100:103], v[156:159]\n" \
B_"v7, s[84:87], s92 offen offset:3072 lds\n" \
M_"v[188:191], a[188:191], v[104:107], v[188:191]\n" \
"s_add_u32 m0, s91, 0x2000\n" \
"v_xor_b32_e32 v48, 0x8000, v48\n" \
"v_smfmac_f32_16x16x64_bf16 a[96:99], a[104:107], v[100:107], v21\n" \
"v_exp_f32_e64 v73, v73\n" \
M_"v[220:223], a[188:191], v[116:119], v[220:223]\n" \
"v_exp_f32_e64 v68, v68\n" \
M_"v[252:255], a[188:191], v[120:123], v[252:255]\n" \
"v_exp_f32_e64 v69, v69\n" \
M_"v[216:219], a[184:187], v[116:119], v[216:219]\n" \
"v_exp_f32_e64 v74, v74\n" \
M_"v[248:251], a[184:187], v[120:123], v[248:251]\n" \
"v_exp_f32_e64 v75, v75\n" \
M_"v[212:215], a[180:183], v[116:119], v[212:215]\n" \
B_"v8, s[84:87], s92 offen offset:0 lds\n" \
M_"v[244:247], a[180:183], v[120:123], v[244:247]\n" \
"v_exp_f32_e64 v66, v66\n" \
M_"v[208:211], a[176:179], v[116:119], v[208:211]\n" \
"v_exp_f32_e64 v67, v67\n" \
M_"v[240:243], a[176:179], v[120:123], v[240:243]\n" \
"v_exp_f32_e64 v64, v64\n" \
M_"v[204:207], a[172:175], v[116:119], v[204:207]\n" \
"v_exp_f32_e64 v65, v65\n" \
M_"v[236:239], a[172:175], v[120:123], v[236:239]\n" \
C_"v72, v72, v73\n" \
C_"v70, v68, v69\n" \
M_"v[200:203], a[168:171], v[116:119], v[200:203]\n" \
"v_exp_f32_e64 v76, v76\n" \
M_"v[232:235], a[168:171], v[120:123], v[232:235]\n" \
"v_exp_f32_e64 v77, v77\n" \
M_"v[196:199], a[164:167], v[116:119], v[196:199]\n" \
"v_exp_f32_e64 v78, v78\n" \
M_"v[228:231], a[164:167], v[120:123], v[228:231]\n" \
"v_exp_f32_e64 v79, v79\n" \
M_"v[192:195], a[160:163], v[116:119], v[192:195]\n" \
C_"v73, v74, v75\n" \
C_"v69, v66, v67\n" \
M_"v[224:227], a[160:163], v[120:123], v[224:227]\n" \
C_"v68, v64, v65\n" \
"s_waitcnt vmcnt(15) lgkmcnt(0)\n" \
"v_smfmac_f32_16x16x64_bf16 a[100:103], a[104:107], v[116:123], v21\n" \
"s_barrier\n" \
C_"v74, v76, v77\n" \
C_"v75, v78, v79\n" \
"s_cmp_lg_u32 s16, 0\n" \
"s_cbranch_scc1 L_f1_%=\n" \
R_"a[160:163], v49 offset:0\n" \
R_"a[164:167], v49 offset:1024\n" \
R_"a[168:171], v49 offset:2048\n" \
R_"a[172:175], v49 offset:3072\n" \
R_"a[176:179], v49 offset:4096\n" \
R_"a[180:183], v49 offset:5120\n" \
R_"a[184:187], v49 offset:6144\n" \
R_"a[188:191], v49 offset:7168\n" \
"v_add_u32_e32 v54, 0x49000, v20\n" \
B_"a[192:195], v20, s[56:59], s60 offen offset:0 sc1\n" \
"v_add_u32_e32 v50, s15, v20\n" \
"v_add_u32_e32 v51, 0x10000, v50\n" \
"ds_read_b32 v60, v18 offset:0\n" \
"ds_read_b32 v61, v18 offset:128\n" \
R_"a[0:3], v49 offset:8192\n" \
R_"a[4:7], v49 offset:9216\n" \
R_"a[8:11], v49 offset:10240\n" \
R_"a[12:15], v49 offset:11264\n" \
R_"a[16:19], v49 offset:12288\n" \
R_"a[20:23], v49 offset:13312\n" \
R_"a[24:27], v49 offset:14336\n" \
R_"a[28:31], v49 offset:15360\n" \
E_"v86, v86\n" \
E_"v87, v87\n" \
E_"v84, v84\n" \
E_"v85, v85\n" \
C_"v87, v86, v87\n" \
B_"a[196:199], v20, s[56:59], s60 offen offset:1024 sc1\n" \
E_"v82, v82\n" \
E_"v83, v83\n" \
E_"v80, v80\n" \
E_"v81, v81\n" \
"s_waitcnt lgkmcnt(15)\n" \
M_"v[128:131], a[160:163], v[68:71], v[128:131]\n" \
C_"v86, v84, v85\n" \
C_"v85, v82, v83\n" \
M_"v[160:163], a[160:163], v[72:75], v[160:163]\n" \
C_"v84, v80, v81\n" \
"v_and_b32_e32 v84, v12, v84\n" \
M_"v[132:135], a[164:167], v[68:71], v[132:135]\n" \
"v_and_b32_e32 v85, v13, v85\n" \
"v_and_b32_e32 v86, v14, v86\n" \
M_"v[164:167], a[164:167], v[72:75], v[164:167]\n" \
B_"a[200:203], v20, s[56:59], s60 offen offset:2048 sc1\n" \
M_"v[136:139], a[168:171], v[68:71], v[136:139]\n" \
C_"v128, v128, v129\n" \
C_"v160, v160, v161\n" \
M_"v[168:171], a[168:171], v[72:75], v[168:171]\n" \
C_"v129, v130, v131\n" \
"s_waitcnt lgkmcnt(10)\n" \
M_"v[140:143], a[172:175], v[68:71], v[140:143]\n" \
C_"v161, v162, v163\n" \
C_"v130, v132, v133\n" \
M_"v[172:175], a[172:175], v[72:75], v[172:175]\n" \
C_"v131, v134, v135\n" \
C_"v162, v164, v165\n" \
M_"v[144:147], a[176:179], v[68:71], v[144:147]\n" \
C_"v163, v166, v167\n" \
C_"v132, v136, v137\n" \
M_"v[176:179], a[176:179], v[72:75], v[176:179]\n" \
B_"a[204:207], v20, s[56:59], s60 offen offset:3072 sc1\n" \
M_"v[148:151], a[180:183], v[68:71], v[148:151]\n" \
C_"v133, v138, v139\n" \
C_"v134, v140, v141\n" \
M_"v[180:183], a[180:183], v[72:75], v[180:183]\n" \
C_"v135, v142, v143\n" \
C_"v164, v168, v169\n" \
M_"v[152:155], a[184:187], v[68:71], v[152:155]\n" \
C_"v165, v170, v171\n" \
C_"v166, v172, v173\n" \
M_"v[184:187], a[184:187], v[72:75], v[184:187]\n" \
C_"v167, v174, v175\n" \
C_"v136, v144, v145\n" \
M_"v[156:159], a[188:191], v[68:71], v[156:159]\n" \
C_"v137, v146, v147\n" \
C_"v138, v148, v149\n" \
M_"v[188:191], a[188:191], v[72:75], v[188:191]\n" \
C_"v139, v150, v151\n" \
C_"v140, v152, v153\n" \
"v_smfmac_f32_16x16x64_bf16 a[96:99], a[104:107], v[68:75], v21\n" \
C_"v141, v154, v155\n" \
C_"v168, v176, v177\n" \
M_"v[120:123], a[128:131], a[208:211], v[44:47]\n" \
C_"v142, v156, v157\n" \
C_"v143, v158, v159\n" \
M_"v[120:123], a[132:135], a[212:215], v[120:123]\n" \
C_"v169, v178, v179\n" \
C_"v170, v180, v181\n" \
C_"v171, v182, v183\n" \
C_"v172, v184, v185\n" \
C_"v173, v186, v187\n" \
C_"v174, v188, v189\n" \
C_"v175, v190, v191\n" \
"ds_write_b128 v50, v[128:131] offset:0\n" \
M_"v[120:123], a[136:139], a[216:219], v[120:123]\n" \
"v_and_b32_e32 v87, v15, v87\n" \
"v_accvgpr_read_b32 v40, a96\n" \
M_"v[120:123], a[140:143], a[220:223], v[120:123]\n" \
"ds_write_b128 v50, v[132:135] offset:1024\n" \
M_"v[124:127], a[156:159], a[220:223], v[44:47]\n" \
B_"a[220:223], v54, s[56:59], s60 offen offset:3072 sc1\n" \
M_"v[124:127], a[152:155], a[216:219], v[124:127]\n" \
"ds_write_b128 v50, v[136:139] offset:2048\n" \
M_"v[124:127], a[148:151], a[212:215], v[124:127]\n" \
"v_accvgpr_read_b32 v22, a97\n" \
"v_accvgpr_read_b32 v41, a98\n" \
M_"v[124:127], a[144:147], a[208:211], v[124:127]\n" \
"ds_write_b128 v50, v[140:143] offset:3072\n" \
M_"a[100:103], a[108:111], v[84:87], a[100:103]\n" \
"v_accvgpr_read_b32 v23, a99\n" \
"v_add_f32_e32 v40, v40, v22\n" \
M_"v[192:195], a[160:163], v[84:87], v[192:195]\n" \
"ds_write_b128 v51, v[160:163] offset:32768\n" \
M_"v[196:199], a[164:167], v[84:87], v[196:199]\n" \
B_"a[208:211], v54, s[56:59], s60 offen offset:0 sc1\n" \
M_"v[200:203], a[168:171], v[84:87], v[200:203]\n" \
"ds_write_b128 v51, v[164:167] offset:33792\n" \
"v_add_f32_e32 v41, v41, v23\n" \
"ds_write_b64 v19, v[40:41]\n" \
M_"v[204:207], a[172:175], v[84:87], v[204:207]\n" \
"v_lshl_add_u32 v52, s15, 2, v20\n" \
"v_add_u32_e32 v52, 0x1a000, v52\n" \
"ds_write_b128 v51, v[168:171] offset:34816\n" \
M_"v[208:211], a[176:179], v[84:87], v[208:211]\n" \
E_"v88, v88\n" \
M_"v[212:215], a[180:183], v[84:87], v[212:215]\n" \
"ds_write_b128 v51, v[172:175] offset:35840\n" \
M_"v[216:219], a[184:187], v[84:87], v[216:219]\n" \
B_"a[212:215], v54, s[56:59], s60 offen offset:1024 sc1\n" \
M_"v[220:223], a[188:191], v[84:87], v[220:223]\n" \
E_"v89, v89\n" \
E_"v90, v90\n" \
E_"v91, v91\n" \
C_"v88, v88, v89\n" \
E_"v92, v92\n" \
E_"v93, v93\n" \
E_"v94, v94\n" \
E_"v95, v95\n" \
C_"v89, v90, v91\n" \
C_"v90, v92, v93\n" \
C_"v91, v94, v95\n" \
"v_xor_b32_e32 v63, 0x400, v19\n" \
B_"a[216:219], v54, s[56:59], s60 offen offset:2048 sc1\n" \
E_"v120, v120\n" \
E_"v121, v121\n" \
E_"v122, v122\n" \
E_"v123, v123\n" \
C_"v120, v120, v121\n" \
E_"v124, v124\n" \
E_"v125, v125\n" \
"s_waitcnt lgkmcnt(0)\n" \
"s_barrier\n" \
R_"v[144:147], v52 offset:0\n" \
R_"v[148:151], v52 offset:1024\n" \
R_"v[152:155], v52 offset:2048\n" \
".fill (L_ab + 12288 - .) / 4, 4, 0xBF800000\n" \
"L_ap3:\n" \
"s_cbranch_execz L_ap0\n" \
R_"v[156:159], v52 offset:3072\n" \
R_"v[176:179], v52 offset:4096\n" \
R_"v[180:183], v52 offset:5120\n" \
R_"v[184:187], v52 offset:6144\n" \
R_"v[188:191], v52 offset:7168\n" \
M_"a[100:103], a[116:119], v[88:91], a[100:103]\n" \
"ds_read_b64 v[42:43], v63\n" \
C_"v121, v122, v123\n" \
M_"v[224:227], a[160:163], v[88:91], v[224:227]\n" \
E_"v126, v126\n" \
M_"v[228:231], a[164:167], v[88:91], v[228:231]\n" \
E_"v127, v127\n" \
M_"v[232:235], a[168:171], v[88:91], v[232:235]\n" \
C_"v122, v124, v125\n" \
C_"v123, v126, v127\n" \
M_"v[236:239], a[172:175], v[88:91], v[236:239]\n" \
"v_and_b32_e32 v120, v12, v120\n" \
"v_and_b32_e32 v121, v13, v121\n" \
M_"v[240:243], a[176:179], v[88:91], v[240:243]\n" \
"v_and_b32_e32 v122, v14, v122\n" \
"v_and_b32_e32 v123, v15, v123\n" \
M_"v[244:247], a[180:183], v[88:91], v[244:247]\n" \
"v_readlane_b32 s93, v11, 25\n" \
"v_readlane_b32 s92, v11, 30\n" \
M_"v[248:251], a[184:187], v[88:91], v[248:251]\n" \
"s_add_u32 m0, s93, 0x0\n" \
"v_mov_b32_e32 v130, v28\n" \
M_"v[252:255], a[188:191], v[88:91], v[252:255]\n" \
"v_mov_b32_e32 v131, v29\n" \
"v_mov_b32_e32 v132, 0\n" \
"v_mov_b32_e32 v133, 0\n" \
"v_mov_b32_e32 v128, 0\n" \
"v_mov_b32_e32 v129, 0\n" \
"s_waitcnt vmcnt(8) lgkmcnt(1)\n" \
"s_barrier\n" \
M_"a[100:103], a[116:119], v[120:123], a[100:103]\n" \
B_"v0, s[80:83], s92 offen offset:0 lds\n" \
M_"v[224:227], a[0:3], v[120:123], v[224:227]\n" \
M_"v[228:231], a[4:7], v[120:123], v[228:231]\n" \
M_"v[232:235], a[8:11], v[120:123], v[232:235]\n" \
M_"v[236:239], a[12:15], v[120:123], v[236:239]\n" \
M_"v[240:243], a[16:19], v[120:123], v[240:243]\n" \
M_"v[244:247], a[20:23], v[120:123], v[244:247]\n" \
B_"v1, s[80:83], s92 offen offset:1024 lds\n" \
M_"v[248:251], a[24:27], v[120:123], v[248:251]\n" \
"v_accvgpr_read_b32 v56, a100\n" \
"v_accvgpr_read_b32 v24, a101\n" \
M_"v[252:255], a[28:31], v[120:123], v[252:255]\n" \
"v_add_f32_e32 v56, v56, v24\n" \
"s_waitcnt lgkmcnt(0)\n" \
"v_add_f32_e32 v56, v56, v42\n" \
M_"v[192:195], v[130:133], v[144:147], v[192:195]\n" \
"v_max_f32_e32 v62, v62, v56\n" \
"v_accvgpr_read_b32 v57, a102\n" \
M_"v[196:199], v[128:131], v[144:147], v[196:199]\n" \
"v_rcp_f32_e32 v56, v56\n" \
M_"v[200:203], v[130:133], v[148:151], v[200:203]\n" \
"v_mul_f32_e32 v56, v56, v60\n" \
"v_accvgpr_read_b32 v25, a103\n" \
B_"v2, s[80:83], s92 offen offset:2048 lds\n" \
M_"v[204:207], v[128:131], v[148:151], v[204:207]\n" \
"v_mul_f32_e32 v192, v56, v192\n" \
"v_mul_f32_e32 v193, v56, v193\n" \
M_"v[208:211], v[130:133], v[152:155], v[208:211]\n" \
"v_mul_f32_e32 v194, v56, v194\n" \
"v_mul_f32_e32 v195, v56, v195\n" \
M_"v[212:215], v[128:131], v[152:155], v[212:215]\n" \
"v_mul_f32_e32 v196, v56, v196\n" \
"v_mul_f32_e32 v197, v56, v197\n" \
M_"v[216:219], v[130:133], v[156:159], v[216:219]\n" \
"v_mul_f32_e32 v198, v56, v198\n" \
"v_mul_f32_e32 v199, v56, v199\n" \
M_"v[220:223], v[128:131], v[156:159], v[220:223]\n" \
C_"v64, v192, v193\n" \
C_"v65, v194, v195\n" \
M_"v[224:227], v[130:133], v[176:179], v[224:227]\n" \
B_"v3, s[80:83], s92 offen offset:3072 lds\n" \
"s_add_u32 m0, s93, 0x1000\n" \
M_"v[228:231], v[128:131], v[176:179], v[228:231]\n" \
C_"v66, v196, v197\n" \
C_"v67, v198, v199\n" \
M_"v[232:235], v[130:133], v[180:183], v[232:235]\n" \
"v_mul_f32_e32 v200, v56, v200\n" \
"v_mul_f32_e32 v201, v56, v201\n" \
M_"v[236:239], v[128:131], v[180:183], v[236:239]\n" \
M_"a[112:115], a[120:123], v[64:67], a[112:115]\n" \
"v_mul_f32_e32 v202, v56, v202\n" \
"v_mul_f32_e32 v203, v56, v203\n" \
M_"v[240:243], v[130:133], v[184:187], v[240:243]\n" \
"v_mul_f32_e32 v204, v56, v204\n" \
"v_mul_f32_e32 v205, v56, v205\n" \
B_"v4, s[80:83], s92 offen offset:0 lds\n" \
"v_mul_f32_e32 v206, v56, v206\n" \
"v_mul_f32_e32 v207, v56, v207\n" \
"v_add_f32_e32 v57, v57, v25\n" \
C_"v68, v200, v201\n" \
C_"v69, v202, v203\n" \
C_"v70, v204, v205\n" \
C_"v71, v206, v207\n" \
"v_add_f32_e32 v57, v57, v43\n" \
"v_mul_f32_e32 v208, v56, v208\n" \
M_"a[112:115], a[120:123], v[68:71], a[112:115]\n" \
"v_mul_f32_e32 v209, v56, v209\n" \
"v_mul_f32_e32 v210, v56, v210\n" \
"v_mul_f32_e32 v211, v56, v211\n" \
"v_mul_f32_e32 v212, v56, v212\n" \
"v_mul_f32_e32 v213, v56, v213\n" \
"v_mul_f32_e32 v214, v56, v214\n" \
"v_mul_f32_e32 v215, v56, v215\n" \
"v_max_f32_e32 v62, v62, v57\n" \
C_"v72, v208, v209\n" \
C_"v73, v210, v211\n" \
B_"v5, s[80:83], s92 offen offset:1024 lds\n" \
C_"v74, v212, v213\n" \
C_"v75, v214, v215\n" \
"v_rcp_f32_e32 v57, v57\n" \
M_"v[244:247], v[128:131], v[184:187], v[244:247]\n" \
M_"a[112:115], a[120:123], v[72:75], a[112:115]\n" \
"v_mul_f32_e32 v216, v56, v216\n" \
"v_mul_f32_e32 v217, v56, v217\n" \
"v_mul_f32_e32 v218, v56, v218\n" \
"v_mul_f32_e32 v219, v56, v219\n" \
"v_mul_f32_e32 v220, v56, v220\n" \
"v_mul_f32_e32 v221, v56, v221\n" \
"v_mul_f32_e32 v222, v56, v222\n" \
"v_mul_f32_e32 v223, v56, v223\n" \
C_"v76, v216, v217\n" \
C_"v77, v218, v219\n" \
C_"v78, v220, v221\n" \
B_"v6, s[80:83], s92 offen offset:2048 lds\n" \
C_"v79, v222, v223\n" \
"v_mul_f32_e32 v57, v57, v61\n" \
"v_mul_f32_e32 v224, v57, v224\n" \
M_"a[112:115], a[120:123], v[76:79], a[112:115]\n" \
"v_mul_f32_e32 v225, v57, v225\n" \
"v_mul_f32_e32 v226, v57, v226\n" \
"v_mul_f32_e32 v227, v57, v227\n" \
"v_mul_f32_e32 v228, v57, v228\n" \
"v_mul_f32_e32 v229, v57, v229\n" \
"v_mul_f32_e32 v230, v57, v230\n" \
"v_mul_f32_e32 v231, v57, v231\n" \
C_"v80, v224, v225\n" \
C_"v81, v226, v227\n" \
C_"v82, v228, v229\n" \
C_"v83, v230, v231\n" \
M_"v[248:251], v[130:133], v[188:191], v[248:251]\n" \
"v_mul_f32_e32 v232, v57, v232\n" \
"v_mul_f32_e32 v233, v57, v233\n" \
M_"a[112:115], a[120:123], v[80:83], a[112:115]\n" \
B_"v7, s[80:83], s92 offen offset:3072 lds\n" \
"s_add_u32 m0, s93, 0x2000\n" \
"v_mul_f32_e32 v234, v57, v234\n" \
"v_mul_f32_e32 v235, v57, v235\n" \
"v_mul_f32_e32 v236, v57, v236\n" \
"v_mul_f32_e32 v237, v57, v237\n" \
"v_mul_f32_e32 v238, v57, v238\n" \
"v_mul_f32_e32 v239, v57, v239\n" \
C_"v84, v232, v233\n" \
C_"v85, v234, v235\n" \
C_"v86, v236, v237\n" \
C_"v87, v238, v239\n" \
"v_mul_f32_e32 v240, v57, v240\n" \
"v_mul_f32_e32 v241, v57, v241\n" \
M_"a[112:115], a[120:123], v[84:87], a[112:115]\n" \
"v_mul_f32_e32 v242, v57, v242\n" \
"v_mul_f32_e32 v243, v57, v243\n" \
"v_mul_f32_e32 v244, v57, v244\n" \
"v_mul_f32_e32 v245, v57, v245\n" \
"v_mul_f32_e32 v246, v57, v246\n" \
"v_mul_f32_e32 v247, v57, v247\n" \
B_"v8, s[80:83], s92 offen offset:0 lds\n" \
C_"v96, v240, v241\n" \
C_"v97, v242, v243\n" \
C_"v98, v244, v245\n" \
C_"v99, v246, v247\n" \
M_"v[252:255], v[128:131], v[188:191], v[252:255]\n" \
"v_mul_f32_e32 v248, v57, v248\n" \
M_"a[112:115], a[120:123], v[96:99], a[112:115]\n" \
"v_mul_f32_e32 v249, v57, v249\n" \
"v_mul_f32_e32 v250, v57, v250\n" \
"v_mul_f32_e32 v251, v57, v251\n" \
C_"v100, v248, v249\n" \
C_"v101, v250, v251\n" \
"v_cmp_lt_f32_e64 s[72:73], s21, v62\n" \
"v_mul_f32_e32 v252, v57, v252\n" \
"v_mul_f32_e32 v253, v57, v253\n" \
"v_mul_f32_e32 v254, v57, v254\n" \
"v_mul_f32_e32 v255, v57, v255\n" \
C_"v102, v252, v253\n" \
C_"v103, v254, v255\n" \
"s_nop 1\n" \
M_"a[112:115], a[120:123], v[100:103], a[112:115]\n" \
"s_nop 7\n" \
"v_accvgpr_read_b32 v63, a112\n" \
"v_cmp_u_f32_e32 vcc, v63, v63\n" \
"s_nop 0\n" \
"s_or_b64 vcc, s[72:73], vcc\n" \
"s_cbranch_scc1 L_end_%=\n" \
"s_branch L_out_%=\n" \
"L_skb_%=:\n" \
"s_sleep 1\n" \
"buffer_load_dword v57, v61, s[56:59], s96 offen sc1\n" \
"s_add_u32 s101, s101, 1\n" \
"s_waitcnt vmcnt(0)\n" \
"v_cmp_ne_u32_e32 vcc, s62, v57\n" \
"s_cmp_gt_u32 s101, 0x100000\n" \
"s_cbranch_scc1 L_rkb_%=\n" \
"s_nop 2\n" \
"s_cbranch_vccnz L_skb_%=\n" \
"s_branch L_rkb_%=\n" \
"L_sol_%=:\n" \
"s_sleep 1\n" \
"buffer_load_dwordx2 v[26:27], v16, s[56:59], s96 offen sc1\n" \
"s_add_u32 s101, s101, 1\n" \
"s_waitcnt vmcnt(0)\n" \
"v_cmp_ne_u64_e32 vcc, s[62:63], v[26:27]\n" \
"s_cmp_gt_u32 s101, 0x100000\n" \
"s_cbranch_scc1 L_rol_%=\n" \
"s_nop 2\n" \
"s_cbranch_vccnz L_sol_%=\n" \
"s_branch L_rol_%=\n" \
"L_sos_%=:\n" \
"s_sleep 1\n" \
"buffer_load_dwordx2 v[26:27], v16, s[56:59], s96 offen sc1\n" \
"s_add_u32 s101, s101, 1\n" \
"s_waitcnt vmcnt(0)\n" \
"v_cmp_ne_u64_e32 vcc, s[62:63], v[26:27]\n" \
"s_cmp_gt_u32 s101, 0x100000\n" \
"s_cbranch_scc1 L_ros_%=\n" \
"s_nop 2\n" \
"s_cbranch_vccnz L_sos_%=\n" \
"s_branch L_ros_%=\n" \
"L_sa_%=:\n" \
"s_sleep 1\n" \
"buffer_load_dword v26, v38, s[56:59], s64 offen sc1\n" \
"s_add_u32 s101, s101, 1\n" \
"s_waitcnt vmcnt(0)\n" \
"v_cmp_ne_u32_e32 vcc, s62, v26\n" \
"s_cmp_gt_u32 s101, 0x100000\n" \
"s_cbranch_scc1 L_ra_%=\n" \
"s_nop 2\n" \
"s_cbranch_vccnz L_sa_%=\n" \
"s_branch L_ra_%=\n" \
"L_sb_%=:\n" \
"s_sleep 1\n" \
"buffer_load_dword v47, v38, s[56:59], s71 offen sc1\n" \
"s_add_u32 s101, s101, 1\n" \
"s_waitcnt vmcnt(0)\n" \
"v_cmp_ne_u32_e32 vcc, s62, v47\n" \
"s_cmp_gt_u32 s101, 0x100000\n" \
"s_cbranch_scc1 L_rb_%=\n" \
"s_nop 2\n" \
"s_cbranch_vccnz L_sb_%=\n" \
"s_branch L_rb_%=\n" \
"L_sna_%=:\n" \
"buffer_load_dword v37, v40, s[56:59], s74 offen sc1\n" \
"s_add_u32 s101, s101, 1\n" \
"s_waitcnt vmcnt(0)\n" \
"v_cmp_eq_u32_e64 s[98:99], s62, v37\n" \
"s_cmp_gt_u32 s101, 0x100000\n" \
"s_cbranch_scc1 L_rna_%=\n" \
"s_andn2_b32 s75, s20, s98\n" \
"s_cbranch_scc0 L_gna_%=\n" \
"s_sleep 1\n" \
"s_branch L_sna_%=\n" \
"L_gna_%=:\n" \
"s_bitcmp1_b32 s20, 0\n" \
"s_cbranch_scc0 L_kna0_%=\n" \
B_"v[116:119], v20, s[56:59], s12 offen offset:0 sc1\n" \
"L_kna0_%=:\n" \
"s_bitcmp1_b32 s20, 1\n" \
"s_cbranch_scc0 L_kna1_%=\n" \
B_"v[124:127], v20, s[56:59], s12 offen offset:2048 sc1\n" \
"L_kna1_%=:\n" \
"s_bitcmp1_b32 s20, 2\n" \
"s_cbranch_scc0 L_kna2_%=\n" \
B_"v[132:135], v20, s[56:59], s13 offen offset:0 sc1\n" \
"L_kna2_%=:\n" \
"s_bitcmp1_b32 s20, 3\n" \
"s_cbranch_scc0 L_kna3_%=\n" \
B_"v[140:143], v20, s[56:59], s13 offen offset:2048 sc1\n" \
"L_kna3_%=:\n" \
"s_bitcmp1_b32 s20, 4\n" \
"s_cbranch_scc0 L_kna4_%=\n" \
B_"v[148:151], v20, s[56:59], s14 offen offset:0 sc1\n" \
"L_kna4_%=:\n" \
"s_bitcmp1_b32 s20, 5\n" \
"s_cbranch_scc0 L_kna5_%=\n" \
B_"v[156:159], v20, s[56:59], s14 offen offset:2048 sc1\n" \
"L_kna5_%=:\n" \
"s_bitcmp1_b32 s20, 6\n" \
"s_cbranch_scc0 L_kna6_%=\n" \
B_"v[164:167], v20, s[56:59], s15 offen offset:0 sc1\n" \
"L_kna6_%=:\n" \
"s_waitcnt vmcnt(0)\n" \
"s_branch L_rna_%=\n" \
"L_snx_%=:\n" \
"buffer_load_dword v37, v40, s[56:59], s74 offen sc1\n" \
"s_add_u32 s101, s101, 1\n" \
"s_waitcnt vmcnt(0)\n" \
"v_cmp_eq_u32_e64 s[98:99], s62, v37\n" \
"s_cmp_gt_u32 s101, 0x100000\n" \
"s_cbranch_scc1 L_rnx_%=\n" \
"s_andn2_b32 s75, s21, s98\n" \
"s_cbranch_scc0 L_gnx_%=\n" \
"s_sleep 1\n" \
"s_branch L_snx_%=\n" \
"L_gnx_%=:\n" \
"s_bitcmp1_b32 s21, 16\n" \
"s_cbranch_scc0 L_knx16_%=\n" \
B_"v[112:115], v20, s[56:59], s16 offen offset:0 sc1\n" \
"L_knx16_%=:\n" \
"s_bitcmp1_b32 s21, 17\n" \
"s_cbranch_scc0 L_knx17_%=\n" \
B_"v[184:187], v20, s[56:59], s16 offen offset:2048 sc1\n" \
"L_knx17_%=:\n" \
"s_bitcmp1_b32 s21, 18\n" \
"s_cbranch_scc0 L_knx18_%=\n" \
B_"v[120:123], v20, s[56:59], s17 offen offset:0 sc1\n" \
"L_knx18_%=:\n" \
"s_bitcmp1_b32 s21, 19\n" \
"s_cbranch_scc0 L_knx19_%=\n" \
B_"v[192:195], v20, s[56:59], s17 offen offset:2048 sc1\n" \
"L_knx19_%=:\n" \
"s_bitcmp1_b32 s21, 20\n" \
"s_cbranch_scc0 L_knx20_%=\n" \
B_"v[128:131], v20, s[56:59], s18 offen offset:0 sc1\n" \
"L_knx20_%=:\n" \
"s_bitcmp1_b32 s21, 21\n" \
"s_cbranch_scc0 L_knx21_%=\n" \
B_"v[200:203], v20, s[56:59], s18 offen offset:2048 sc1\n" \
"L_knx21_%=:\n" \
"s_bitcmp1_b32 s21, 22\n" \
"s_cbranch_scc0 L_knx22_%=\n" \
B_"v[176:179], v20, s[56:59], s19 offen offset:0 sc1\n" \
"L_knx22_%=:\n" \
"s_bitcmp1_b32 s21, 23\n" \
"s_cbranch_scc0 L_knx23_%=\n" \
B_"v[248:251], v20, s[56:59], s19 offen offset:2048 sc1\n" \
"L_knx23_%=:\n" \
"s_bitcmp1_b32 s21, 8\n" \
"s_cbranch_scc0 L_knx8_%=\n" \
B_"v[188:191], v20, s[56:59], s12 offen offset:1024 sc1\n" \
"L_knx8_%=:\n" \
"s_bitcmp1_b32 s21, 9\n" \
"s_cbranch_scc0 L_knx9_%=\n" \
B_"v[196:199], v20, s[56:59], s12 offen offset:3072 sc1\n" \
"L_knx9_%=:\n" \
"s_waitcnt vmcnt(0)\n" \
"s_branch L_rnx_%=\n" \
"L_sx0_%=:\n" \
"s_sleep 1\n" \
"buffer_load_dword v37, v40, s[56:59], s74 offen sc1\n" \
"s_add_u32 s101, s101, 1\n" \
"s_waitcnt vmcnt(0)\n" \
"v_cmp_eq_u32_e64 s[98:99], s62, v37\n" \
"s_cmp_gt_u32 s101, 0x100000\n" \
"s_cbranch_scc1 L_rx0_%=\n" \
"s_andn2_b32 s75, 0x400, s98\n" \
"s_cbranch_scc1 L_sx0_%=\n" \
"s_branch L_rx0_%=\n" \
"L_sx1_%=:\n" \
"s_sleep 1\n" \
"buffer_load_dword v37, v40, s[56:59], s74 offen sc1\n" \
"s_add_u32 s101, s101, 1\n" \
"s_waitcnt vmcnt(0)\n" \
"v_cmp_eq_u32_e64 s[98:99], s62, v37\n" \
"s_cmp_gt_u32 s101, 0x100000\n" \
"s_cbranch_scc1 L_rx1_%=\n" \
"s_andn2_b32 s75, 0x800, s98\n" \
"s_cbranch_scc1 L_sx1_%=\n" \
"s_branch L_rx1_%=\n" \
"L_sx2_%=:\n" \
"s_sleep 1\n" \
"buffer_load_dword v37, v40, s[56:59], s74 offen sc1\n" \
"s_add_u32 s101, s101, 1\n" \
"s_waitcnt vmcnt(0)\n" \
"v_cmp_eq_u32_e64 s[98:99], s62, v37\n" \
"s_cmp_gt_u32 s101, 0x100000\n" \
"s_cbranch_scc1 L_rx2_%=\n" \
"s_andn2_b32 s75, 0x1000, s98\n" \
"s_cbranch_scc1 L_sx2_%=\n" \
"s_branch L_rx2_%=\n" \
"L_sx3_%=:\n" \
"s_sleep 1\n" \
"buffer_load_dword v37, v40, s[56:59], s74 offen sc1\n" \
"s_add_u32 s101, s101, 1\n" \
"s_waitcnt vmcnt(0)\n" \
"v_cmp_eq_u32_e64 s[98:99], s62, v37\n" \
"s_cmp_gt_u32 s101, 0x100000\n" \
"s_cbranch_scc1 L_rx3_%=\n" \
"s_andn2_b32 s75, 0x2000, s98\n" \
"s_cbranch_scc1 L_sx3_%=\n" \
"s_branch L_rx3_%=\n" \
"L_sx4_%=:\n" \
"s_sleep 1\n" \
"buffer_load_dword v37, v40, s[56:59], s74 offen sc1\n" \
"s_add_u32 s101, s101, 1\n" \
"s_waitcnt vmcnt(0)\n" \
"v_cmp_eq_u32_e64 s[98:99], s62, v37\n" \
"s_cmp_gt_u32 s101, 0x100000\n" \
"s_cbranch_scc1 L_rx4_%=\n" \
"s_andn2_b32 s75, 0x4000, s98\n" \
"s_cbranch_scc1 L_sx4_%=\n" \
"s_branch L_rx4_%=\n" \
"L_att_stub:\n" \
"s_mov_b32 s101, 0\n" \
"v_add_u32_e32 v32, 0x49000, v20\n" \
"v_readlane_b32 s96, v11, 39\n" \
"v_readlane_b32 s97, v11, 40\n" \
B_"a[192:195], v20, s[56:59], s60 offen offset:0 sc1\n" \
B_"a[208:211], v32, s[56:59], s60 offen offset:0 sc1\n" \
B_"a[196:199], v20, s[56:59], s60 offen offset:1024 sc1\n" \
B_"a[212:215], v32, s[56:59], s60 offen offset:1024 sc1\n" \
B_"a[200:203], v20, s[56:59], s60 offen offset:2048 sc1\n" \
B_"a[216:219], v32, s[56:59], s60 offen offset:2048 sc1\n" \
B_"a[204:207], v20, s[56:59], s60 offen offset:3072 sc1\n" \
B_"a[220:223], v32, s[56:59], s60 offen offset:3072 sc1\n" \
B_"a[124:127], v20, s[56:59], s96 offen offset:0 sc1\n" \
B_"a[228:231], v32, s[56:59], s96 offen offset:0 sc1\n" \
B_"a[236:239], v20, s[56:59], s97 offen offset:0 sc1\n" \
B_"a[244:247], v32, s[56:59], s97 offen offset:0 sc1\n" \
B_"a[224:227], v20, s[56:59], s96 offen offset:1024 sc1\n" \
B_"a[232:235], v32, s[56:59], s96 offen offset:1024 sc1\n" \
B_"a[240:243], v20, s[56:59], s97 offen offset:1024 sc1\n" \
B_"a[248:251], v32, s[56:59], s97 offen offset:1024 sc1\n" \
B_"v[64:67], v20, s[56:59], s61 offen offset:0 sc1\n" \
B_"v[68:71], v20, s[56:59], s61 offen offset:1024 sc1\n" \
B_"v[72:75], v20, s[56:59], s61 offen offset:2048 sc1\n" \
B_"v[76:79], v20, s[56:59], s61 offen offset:3072 sc1\n" \
B_"v[80:83], v32, s[56:59], s61 offen offset:0 sc1\n" \
B_"v[84:87], v32, s[56:59], s61 offen offset:1024 sc1\n" \
B_"v[96:99], v32, s[56:59], s61 offen offset:2048 sc1\n" \
B_"v[100:103], v32, s[56:59], s61 offen offset:3072 sc1\n" \
"v_not_b32_e32 v26, s62\n" \
"s_waitcnt vmcnt(0)\n" \
"s_branch L_out_%=\n" \
".fill (L_ab + 16384 - .) / 4, 4, 0xBF800000\n" \
".fill (L_ab + 16384 - .) / 4, 4, 0xBF800000\n" \
"L_ap4:\n" \
"s_setpc_b64 s[88:89]\n" \
"L_gsw:\n" \
M_"v[104:107], a[128:131], a[16:19], v[36:39]\n" \
"s_add_u32 s68, s60, 0xfff6e000\n" \
"s_add_u32 s69, s60, 0xfffb7000\n" \
"s_add_u32 s70, s60, 0x0\n" \
"s_add_u32 s71, s60, 0x49000\n" \
M_"v[104:107], a[132:135], a[20:23], v[104:107]\n" \
"s_xor_b32 m0, s65, 0x12000\n" \
R_"a[160:163], v49 offset:0\n" \
M_"v[104:107], a[136:139], a[24:27], v[104:107]\n" \
B_"v50, s[8:11], s64 offen offset:0 sc1 lds\n" \
M_"v[104:107], a[140:143], a[28:31], v[104:107]\n" \
R_"a[164:167], v49 offset:1024\n" \
R_"a[168:171], v49 offset:2048\n" \
M_"v[108:111], a[156:159], a[28:31], v[36:39]\n" \
R_"a[172:175], v49 offset:3072\n" \
R_"a[176:179], v49 offset:4096\n" \
M_"v[108:111], a[152:155], a[24:27], v[108:111]\n" \
B_"v50, s[8:11], s64 offen offset:1024 sc1 lds\n" \
M_"v[108:111], a[148:151], a[20:23], v[108:111]\n" \
"s_mov_b32 s65, s13\n" \
"v_add_u32_e32 v63, s15, v20\n" \
M_"v[108:111], a[144:147], a[16:19], v[108:111]\n" \
"s_add_u32 s67, s14, 40960\n" \
"v_add_u32_e32 v50, 0x6000, v63\n" \
M_"v[116:119], a[144:147], a[32:35], v[40:43]\n" \
"s_xor_b32 m0, s65, 0x8000\n" \
R_"a[180:183], v49 offset:5120\n" \
M_"v[116:119], a[148:151], a[36:39], v[116:119]\n" \
R_"a[184:187], v49 offset:6144\n" \
"v_and_b32_e32 v68, v12, v68\n" \
M_"v[116:119], a[152:155], a[40:43], v[116:119]\n" \
"v_and_b32_e32 v69, v13, v69\n" \
"v_and_b32_e32 v70, v14, v70\n" \
M_"v[116:119], a[156:159], a[44:47], v[116:119]\n" \
B_"v50, s[8:11], s67 offen offset:0 sc1 lds\n" \
M_"v[112:115], a[140:143], a[44:47], v[40:43]\n" \
"v_and_b32_e32 v71, v15, v71\n" \
R_"a[188:191], v49 offset:7168\n" \
M_"v[112:115], a[136:139], a[40:43], v[112:115]\n" \
E_"v86, v86\n" \
M_"v[112:115], a[132:135], a[36:39], v[112:115]\n" \
E_"v87, v87\n" \
M_"v[112:115], a[128:131], a[32:35], v[112:115]\n" \
E_"v84, v84\n" \
M_"v[120:123], a[128:131], a[48:51], v[44:47]\n" \
B_"v50, s[8:11], s67 offen offset:1024 sc1 lds\n" \
M_"v[120:123], a[132:135], a[52:55], v[120:123]\n" \
E_"v85, v85\n" \
M_"v[120:123], a[136:139], a[56:59], v[120:123]\n" \
C_"v87, v86, v87\n" \
C_"v86, v84, v85\n" \
M_"v[120:123], a[140:143], a[60:63], v[120:123]\n" \
E_"v82, v82\n" \
M_"v[124:127], a[156:159], a[60:63], v[44:47]\n" \
E_"v83, v83\n" \
M_"v[124:127], a[152:155], a[56:59], v[124:127]\n" \
E_"v80, v80\n" \
M_"v[124:127], a[148:151], a[52:55], v[124:127]\n" \
B_"v50, s[8:11], s67 offen offset:2048 sc1 lds\n" \
M_"v[124:127], a[144:147], a[48:51], v[124:127]\n" \
C_"v85, v82, v83\n" \
"s_waitcnt lgkmcnt(1)\n" \
M_"v[128:131], a[160:163], v[68:71], v[128:131]\n" \
E_"v81, v81\n" \
M_"v[160:163], a[160:163], v[72:75], v[160:163]\n" \
C_"v84, v80, v81\n" \
"s_mov_b32 s64, s14\n" \
M_"v[132:135], a[164:167], v[68:71], v[132:135]\n" \
E_"v88, v88\n" \
M_"v[164:167], a[164:167], v[72:75], v[164:167]\n" \
E_"v89, v89\n" \
M_"v[136:139], a[168:171], v[68:71], v[136:139]\n" \
B_"v50, s[8:11], s67 offen offset:3072 sc1 lds\n" \
M_"v[168:171], a[168:171], v[72:75], v[168:171]\n" \
E_"v90, v90\n" \
M_"v[140:143], a[172:175], v[68:71], v[140:143]\n" \
E_"v91, v91\n" \
M_"v[172:175], a[172:175], v[72:75], v[172:175]\n" \
C_"v88, v88, v89\n" \
C_"v89, v90, v91\n" \
M_"v[144:147], a[176:179], v[68:71], v[144:147]\n" \
E_"v92, v92\n" \
M_"v[176:179], a[176:179], v[72:75], v[176:179]\n" \
E_"v93, v93\n" \
M_"v[148:151], a[180:183], v[68:71], v[148:151]\n" \
E_"v94, v94\n" \
M_"v[180:183], a[180:183], v[72:75], v[180:183]\n" \
E_"v95, v95\n" \
M_"v[152:155], a[184:187], v[68:71], v[152:155]\n" \
C_"v90, v92, v93\n" \
C_"v91, v94, v95\n" \
M_"v[184:187], a[184:187], v[72:75], v[184:187]\n" \
"v_add_u32_e32 v51, 0x12000, v63\n" \
"s_waitcnt lgkmcnt(0)\n" \
M_"v[156:159], a[188:191], v[68:71], v[156:159]\n" \
"s_xor_b32 m0, s65, 0xa000\n" \
"ds_read_b32 v58, v18 offset:512\n" \
M_"v[188:191], a[188:191], v[72:75], v[188:191]\n" \
B_"v51, s[8:11], s64 offen offset:0 sc1 lds\n" \
"v_smfmac_f32_16x16x64_bf16 a[96:99], a[104:107], v[68:75], v21\n" \
B_"v51, s[8:11], s64 offen offset:1024 sc1 lds\n" \
M_"v[220:223], a[188:191], v[84:87], v[220:223]\n" \
"ds_read_b32 v59, v18 offset:640\n" \
"ds_read_b32 v60, v18 offset:768\n" \
M_"v[252:255], a[188:191], v[88:91], v[252:255]\n" \
"ds_read_b32 v61, v18 offset:896\n" \
R_"a[128:131], v48 offset:0\n" \
M_"v[216:219], a[184:187], v[84:87], v[216:219]\n" \
R_"a[144:147], v48 offset:4096\n" \
R_"a[132:135], v48 offset:1024\n" \
M_"v[248:251], a[184:187], v[88:91], v[248:251]\n" \
R_"a[148:151], v48 offset:5120\n" \
R_"a[136:139], v48 offset:2048\n" \
M_"v[212:215], a[180:183], v[84:87], v[212:215]\n" \
R_"a[152:155], v48 offset:6144\n" \
R_"a[140:143], v48 offset:3072\n" \
M_"v[244:247], a[180:183], v[88:91], v[244:247]\n" \
R_"a[156:159], v48 offset:7168\n" \
"v_add_u32_e32 v52, 0x14000, v63\n" \
M_"v[208:211], a[176:179], v[84:87], v[208:211]\n" \
B_"v51, s[8:11], s64 offen offset:2048 sc1 lds\n" \
M_"v[240:243], a[176:179], v[88:91], v[240:243]\n" \
E_"v104, v104\n" \
M_"v[204:207], a[172:175], v[84:87], v[204:207]\n" \
E_"v105, v105\n" \
M_"v[236:239], a[172:175], v[88:91], v[236:239]\n" \
B_"v51, s[8:11], s64 offen offset:3072 sc1 lds\n" \
M_"v[200:203], a[168:171], v[84:87], v[200:203]\n" \
E_"v106, v106\n" \
M_"v[232:235], a[168:171], v[88:91], v[232:235]\n" \
E_"v107, v107\n" \
M_"v[196:199], a[164:167], v[84:87], v[196:199]\n" \
C_"v104, v104, v105\n" \
C_"v105, v106, v107\n" \
M_"v[228:231], a[164:167], v[88:91], v[228:231]\n" \
E_"v108, v108\n" \
M_"v[192:195], a[160:163], v[84:87], v[192:195]\n" \
E_"v109, v109\n" \
M_"v[224:227], a[160:163], v[88:91], v[224:227]\n" \
E_"v110, v110\n" \
"v_smfmac_f32_16x16x64_bf16 a[100:103], a[104:107], v[84:91], v21\n" \
E_"v111, v111\n" \
C_"v106, v108, v109\n" \
C_"v107, v110, v111\n" \
"v_and_b32_e32 v104, v12, v104\n" \
"v_and_b32_e32 v105, v13, v105\n" \
"v_and_b32_e32 v106, v14, v106\n" \
"v_and_b32_e32 v107, v15, v107\n" \
"v_accvgpr_read_b32 v54, a96\n" \
"v_accvgpr_read_b32 v22, a97\n" \
"v_add_u32_e32 v53, 0x20000, v63\n" \
E_"v118, v118\n" \
E_"v119, v119\n" \
E_"v116, v116\n" \
E_"v117, v117\n" \
C_"v119, v118, v119\n" \
E_"v114, v114\n" \
E_"v115, v115\n" \
E_"v112, v112\n" \
E_"v113, v113\n" \
C_"v118, v116, v117\n" \
C_"v117, v114, v115\n" \
C_"v116, v112, v113\n" \
"v_readlane_b32 s93, v11, 22\n" \
"s_waitcnt vmcnt(10) lgkmcnt(0)\n" \
"s_barrier\n" \
M_"v[80:83], a[128:131], a[32:35], v[40:43]\n" \
R_"a[160:163], v49 offset:8192\n" \
R_"a[164:167], v49 offset:9216\n" \
M_"v[80:83], a[132:135], a[36:39], v[80:83]\n" \
R_"a[168:171], v49 offset:10240\n" \
"s_xor_b32 m0, s65, 0x18000\n" \
M_"v[80:83], a[136:139], a[40:43], v[80:83]\n" \
B_"v52, s[8:11], s64 offen offset:0 sc1 lds\n" \
M_"v[80:83], a[140:143], a[44:47], v[80:83]\n" \
R_"a[172:175], v49 offset:11264\n" \
R_"a[176:179], v49 offset:12288\n" \
M_"v[84:87], a[156:159], a[44:47], v[40:43]\n" \
R_"a[180:183], v49 offset:13312\n" \
R_"a[184:187], v49 offset:14336\n" \
M_"v[84:87], a[152:155], a[40:43], v[84:87]\n" \
B_"v52, s[8:11], s64 offen offset:1024 sc1 lds\n" \
M_"v[84:87], a[148:151], a[36:39], v[84:87]\n" \
R_"a[188:191], v49 offset:15360\n" \
"v_readlane_b32 s80, v11, 11\n" \
M_"v[84:87], a[144:147], a[32:35], v[84:87]\n" \
B_"v52, s[8:11], s64 offen offset:2048 sc1 lds\n" \
M_"v[92:95], a[144:147], a[48:51], v[44:47]\n" \
E_"v120, v120\n" \
M_"v[92:95], a[148:151], a[52:55], v[92:95]\n" \
E_"v121, v121\n" \
M_"v[92:95], a[152:155], a[56:59], v[92:95]\n" \
B_"v52, s[8:11], s64 offen offset:3072 sc1 lds\n" \
M_"v[92:95], a[156:159], a[60:63], v[92:95]\n" \
"s_mov_b32 m0, s65\n" \
C_"v120, v120, v121\n" \
M_"v[88:91], a[140:143], a[60:63], v[44:47]\n" \
E_"v122, v122\n" \
M_"v[88:91], a[136:139], a[56:59], v[88:91]\n" \
B_"v53, s[8:11], s64 offen offset:0 sc1 lds\n" \
M_"v[88:91], a[132:135], a[52:55], v[88:91]\n" \
E_"v123, v123\n" \
M_"v[88:91], a[128:131], a[48:51], v[88:91]\n" \
E_"v124, v124\n" \
M_"a[96:99], a[116:119], v[104:107], a[96:99]\n" \
C_"v121, v122, v123\n" \
"s_waitcnt lgkmcnt(5)\n" \
M_"v[160:163], a[160:163], v[104:107], v[160:163]\n" \
B_"v53, s[8:11], s64 offen offset:1024 sc1 lds\n" \
M_"v[164:167], a[164:167], v[104:107], v[164:167]\n" \
E_"v125, v125\n" \
M_"v[168:171], a[168:171], v[104:107], v[168:171]\n" \
C_"v122, v124, v125\n" \
"s_waitcnt lgkmcnt(1)\n" \
M_"v[172:175], a[172:175], v[104:107], v[172:175]\n" \
E_"v126, v126\n" \
M_"v[176:179], a[176:179], v[104:107], v[176:179]\n" \
E_"v127, v127\n" \
M_"v[180:183], a[180:183], v[104:107], v[180:183]\n" \
C_"v123, v126, v127\n" \
"v_readlane_b32 s81, v11, 12\n" \
M_"v[184:187], a[184:187], v[104:107], v[184:187]\n" \
"s_mov_b32 s82, 0x47fca0\n" \
"s_waitcnt lgkmcnt(0)\n" \
M_"v[188:191], a[188:191], v[104:107], v[188:191]\n" \
B_"v53, s[8:11], s64 offen offset:2048 sc1 lds\n" \
M_"v[220:223], a[188:191], v[116:119], v[220:223]\n" \
B_"v53, s[8:11], s64 offen offset:3072 sc1 lds\n" \
M_"v[252:255], a[188:191], v[120:123], v[252:255]\n" \
"s_mov_b32 s83, 0x20000\n" \
"v_readlane_b32 s92, v11, 29\n" \
M_"v[216:219], a[184:187], v[116:119], v[216:219]\n" \
"s_add_u32 m0, s93, 0x0\n" \
R_"a[128:131], v48 offset:8192\n" \
M_"v[248:251], a[184:187], v[120:123], v[248:251]\n" \
R_"a[144:147], v48 offset:12288\n" \
R_"a[132:135], v48 offset:9216\n" \
M_"v[212:215], a[180:183], v[116:119], v[212:215]\n" \
B_"v0, s[80:83], s92 offen offset:0 lds\n" \
M_"v[244:247], a[180:183], v[120:123], v[244:247]\n" \
B_"v1, s[80:83], s92 offen offset:1024 lds\n" \
M_"v[208:211], a[176:179], v[116:119], v[208:211]\n" \
R_"a[148:151], v48 offset:13312\n" \
R_"a[136:139], v48 offset:10240\n" \
M_"v[240:243], a[176:179], v[120:123], v[240:243]\n" \
R_"a[152:155], v48 offset:14336\n" \
R_"a[140:143], v48 offset:11264\n" \
M_"v[204:207], a[172:175], v[116:119], v[204:207]\n" \
B_"v2, s[80:83], s92 offen offset:2048 lds\n" \
M_"v[236:239], a[172:175], v[120:123], v[236:239]\n" \
R_"a[156:159], v48 offset:15360\n" \
"s_xor_b32 s65, s65, 0x8000\n" \
M_"v[200:203], a[168:171], v[116:119], v[200:203]\n" \
"s_add_u32 s64, s64, 0x10000\n" \
"v_add_u32_e32 v49, 0x10000, v48\n" \
M_"v[232:235], a[168:171], v[120:123], v[232:235]\n" \
B_"v3, s[80:83], s92 offen offset:3072 lds\n" \
M_"v[196:199], a[164:167], v[116:119], v[196:199]\n" \
E_"v86, v86\n" \
M_"v[228:231], a[164:167], v[120:123], v[228:231]\n" \
E_"v87, v87\n" \
M_"v[192:195], a[160:163], v[116:119], v[192:195]\n" \
E_"v84, v84\n" \
M_"v[224:227], a[160:163], v[120:123], v[224:227]\n" \
E_"v85, v85\n" \
"v_smfmac_f32_16x16x64_bf16 a[100:103], a[104:107], v[116:123], v21\n" \
E_"v88, v88\n" \
E_"v89, v89\n" \
C_"v87, v86, v87\n" \
E_"v82, v82\n" \
E_"v83, v83\n" \
E_"v80, v80\n" \
E_"v81, v81\n" \
E_"v90, v90\n" \
E_"v91, v91\n" \
C_"v86, v84, v85\n" \
C_"v88, v88, v89\n" \
E_"v92, v92\n" \
E_"v93, v93\n" \
E_"v94, v94\n" \
E_"v95, v95\n" \
C_"v85, v82, v83\n" \
C_"v84, v80, v81\n" \
C_"v89, v90, v91\n" \
"v_and_b32_e32 v84, v12, v84\n" \
"v_and_b32_e32 v85, v13, v85\n" \
"v_and_b32_e32 v86, v14, v86\n" \
"v_and_b32_e32 v87, v15, v87\n" \
C_"v90, v92, v93\n" \
C_"v91, v94, v95\n" \
"v_add_u32_e32 v48, s16, v20\n" \
"v_add_f32_e32 v54, v54, v22\n" \
"v_accvgpr_read_b32 v55, a98\n" \
"v_accvgpr_read_b32 v23, a99\n" \
M_"a[96:99], a[120:123], a[120:123], 0\n" \
"v_max_f32_e32 v62, v62, v54\n" \
"v_add_f32_e32 v55, v55, v23\n" \
"v_max_f32_e32 v62, v62, v55\n" \
"v_rcp_f32_e32 v54, v54\n" \
"v_rcp_f32_e32 v55, v55\n" \
"v_mul_f32_e32 v54, v54, v58\n" \
"v_mul_f32_e32 v55, v55, v59\n" \
"v_mul_f32_e32 v128, v54, v128\n" \
"v_mul_f32_e32 v129, v54, v129\n" \
"v_mul_f32_e32 v136, v54, v136\n" \
"v_mul_f32_e32 v137, v54, v137\n" \
"v_mul_f32_e32 v144, v54, v144\n" \
"v_mul_f32_e32 v145, v54, v145\n" \
"v_mul_f32_e32 v152, v54, v152\n" \
"v_mul_f32_e32 v153, v54, v153\n" \
"v_mul_f32_e32 v160, v55, v160\n" \
"v_mul_f32_e32 v161, v55, v161\n" \
"v_mul_f32_e32 v168, v55, v168\n" \
"v_mul_f32_e32 v169, v55, v169\n" \
"v_mul_f32_e32 v176, v55, v176\n" \
"v_mul_f32_e32 v177, v55, v177\n" \
"v_mul_f32_e32 v184, v55, v184\n" \
"v_mul_f32_e32 v185, v55, v185\n" \
C_"v128, v128, v129\n" \
"v_mul_f32_e32 v130, v54, v130\n" \
"v_mul_f32_e32 v131, v54, v131\n" \
C_"v136, v136, v137\n" \
"v_mul_f32_e32 v138, v54, v138\n" \
"v_mul_f32_e32 v139, v54, v139\n" \
C_"v144, v144, v145\n" \
"v_mul_f32_e32 v146, v54, v146\n" \
"v_mul_f32_e32 v147, v54, v147\n" \
C_"v152, v152, v153\n" \
"v_mul_f32_e32 v154, v54, v154\n" \
"v_mul_f32_e32 v155, v54, v155\n" \
C_"v160, v160, v161\n" \
"v_mul_f32_e32 v162, v55, v162\n" \
"v_mul_f32_e32 v163, v55, v163\n" \
C_"v168, v168, v169\n" \
"v_mul_f32_e32 v170, v55, v170\n" \
"v_mul_f32_e32 v171, v55, v171\n" \
C_"v176, v176, v177\n" \
"v_mul_f32_e32 v178, v55, v178\n" \
"v_mul_f32_e32 v179, v55, v179\n" \
C_"v184, v184, v185\n" \
"v_mul_f32_e32 v186, v55, v186\n" \
"v_mul_f32_e32 v187, v55, v187\n" \
C_"v129, v130, v131\n" \
"v_mul_f32_e32 v132, v54, v132\n" \
"v_mul_f32_e32 v133, v54, v133\n" \
"v_mul_f32_e32 v134, v54, v134\n" \
"v_mul_f32_e32 v135, v54, v135\n" \
C_"v137, v138, v139\n" \
"v_mul_f32_e32 v140, v54, v140\n" \
"s_waitcnt vmcnt(12) lgkmcnt(0)\n" \
"s_barrier\n" \
M_"v[120:123], a[128:131], a[48:51], v[44:47]\n" \
"s_xor_b32 m0, s65, 0x12000\n" \
R_"a[188:191], v49 offset:7168\n" \
M_"v[120:123], a[132:135], a[52:55], v[120:123]\n" \
B_"v50, s[8:11], s64 offen offset:0 sc1 lds\n" \
M_"v[120:123], a[136:139], a[56:59], v[120:123]\n" \
R_"a[184:187], v49 offset:6144\n" \
R_"a[180:183], v49 offset:5120\n" \
M_"v[120:123], a[140:143], a[60:63], v[120:123]\n" \
B_"v50, s[8:11], s64 offen offset:1024 sc1 lds\n" \
M_"v[124:127], a[156:159], a[60:63], v[44:47]\n" \
R_"a[176:179], v49 offset:4096\n" \
R_"a[172:175], v49 offset:3072\n" \
M_"v[124:127], a[152:155], a[56:59], v[124:127]\n" \
R_"a[168:171], v49 offset:2048\n" \
R_"a[164:167], v49 offset:1024\n" \
M_"v[124:127], a[148:151], a[52:55], v[124:127]\n" \
R_"a[160:163], v49 offset:0\n" \
R_"a[0:3], v49 offset:8192\n" \
M_"v[124:127], a[144:147], a[48:51], v[124:127]\n" \
B_"v50, s[8:11], s64 offen offset:2048 sc1 lds\n" \
M_"v[32:35], a[124:127], a[64:67], 0\n" \
R_"a[4:7], v49 offset:9216\n" \
R_"a[8:11], v49 offset:10240\n" \
M_"v[36:39], a[124:127], a[80:83], 0\n" \
R_"a[12:15], v49 offset:11264\n" \
R_"a[16:19], v49 offset:12288\n" \
M_"v[40:43], a[124:127], a[192:195], 0\n" \
R_"a[20:23], v49 offset:13312\n" \
R_"a[24:27], v49 offset:14336\n" \
M_"v[44:47], a[124:127], a[208:211], 0\n" \
B_"v50, s[8:11], s64 offen offset:3072 sc1 lds\n" \
M_"v[32:35], a[224:227], a[68:71], v[32:35]\n" \
"s_xor_b32 m0, s65, 0xa000\n" \
R_"a[28:31], v49 offset:15360\n" \
M_"v[36:39], a[224:227], a[84:87], v[36:39]\n" \
E_"v120, v120\n" \
M_"v[40:43], a[224:227], a[196:199], v[40:43]\n" \
B_"v51, s[8:11], s64 offen offset:0 sc1 lds\n" \
M_"v[44:47], a[224:227], a[212:215], v[44:47]\n" \
E_"v121, v121\n" \
M_"v[32:35], a[228:231], a[72:75], v[32:35]\n" \
E_"v122, v122\n" \
M_"v[36:39], a[228:231], a[88:91], v[36:39]\n" \
E_"v123, v123\n" \
M_"v[40:43], a[228:231], a[200:203], v[40:43]\n" \
B_"v51, s[8:11], s64 offen offset:1024 sc1 lds\n" \
M_"v[44:47], a[228:231], a[216:219], v[44:47]\n" \
C_"v120, v120, v121\n" \
C_"v121, v122, v123\n" \
M_"v[32:35], a[232:235], a[76:79], v[32:35]\n" \
E_"v124, v124\n" \
M_"v[36:39], a[232:235], a[92:95], v[36:39]\n" \
E_"v125, v125\n" \
M_"v[40:43], a[232:235], a[204:207], v[40:43]\n" \
B_"v51, s[8:11], s64 offen offset:2048 sc1 lds\n" \
M_"v[44:47], a[232:235], a[220:223], v[44:47]\n" \
E_"v126, v126\n" \
"v_smfmac_f32_16x16x64_bf16 a[100:103], a[104:107], v[84:91], v21\n" \
C_"v122, v124, v125\n" \
"s_waitcnt lgkmcnt(1)\n" \
M_"v[220:223], a[188:191], v[84:87], v[220:223]\n" \
B_"v51, s[8:11], s64 offen offset:3072 sc1 lds\n" \
M_"v[252:255], a[188:191], v[88:91], v[252:255]\n" \
"s_add_u32 m0, s93, 0x1000\n" \
"v_and_b32_e32 v120, v12, v120\n" \
M_"v[216:219], a[184:187], v[84:87], v[216:219]\n" \
E_"v127, v127\n" \
M_"v[248:251], a[184:187], v[88:91], v[248:251]\n" \
C_"v123, v126, v127\n" \
"v_and_b32_e32 v121, v13, v121\n" \
M_"v[212:215], a[180:183], v[84:87], v[212:215]\n" \
B_"v4, s[80:83], s92 offen offset:0 lds\n" \
M_"v[244:247], a[180:183], v[88:91], v[244:247]\n" \
"v_and_b32_e32 v122, v14, v122\n" \
"v_and_b32_e32 v123, v15, v123\n" \
M_"v[208:211], a[176:179], v[84:87], v[208:211]\n" \
"v_accvgpr_read_b32 v56, a100\n" \
"v_accvgpr_read_b32 v24, a101\n" \
M_"v[240:243], a[176:179], v[88:91], v[240:243]\n" \
R_"a[128:131], v48 offset:0\n" \
R_"a[144:147], v48 offset:4096\n" \
M_"v[204:207], a[172:175], v[84:87], v[204:207]\n" \
B_"v5, s[80:83], s92 offen offset:1024 lds\n" \
M_"v[236:239], a[172:175], v[88:91], v[236:239]\n" \
R_"a[132:135], v48 offset:1024\n" \
R_"a[148:151], v48 offset:5120\n" \
M_"v[200:203], a[168:171], v[84:87], v[200:203]\n" \
R_"a[136:139], v48 offset:2048\n" \
R_"a[152:155], v48 offset:6144\n" \
M_"v[232:235], a[168:171], v[88:91], v[232:235]\n" \
B_"v6, s[80:83], s92 offen offset:2048 lds\n" \
M_"v[196:199], a[164:167], v[84:87], v[196:199]\n" \
R_"a[140:143], v48 offset:3072\n" \
R_"a[156:159], v48 offset:7168\n" \
M_"v[228:231], a[164:167], v[88:91], v[228:231]\n" \
"v_mul_f32_e32 v141, v54, v141\n" \
"v_mul_f32_e32 v142, v54, v142\n" \
M_"v[192:195], a[160:163], v[84:87], v[192:195]\n" \
"v_mul_f32_e32 v143, v54, v143\n" \
C_"v145, v146, v147\n" \
M_"v[224:227], a[160:163], v[88:91], v[224:227]\n" \
B_"v7, s[80:83], s92 offen offset:3072 lds\n" \
M_"a[100:103], a[116:119], v[120:123], a[100:103]\n" \
"v_mul_f32_e32 v148, v54, v148\n" \
"v_mul_f32_e32 v149, v54, v149\n" \
M_"v[224:227], a[0:3], v[120:123], v[224:227]\n" \
"v_mul_f32_e32 v150, v54, v150\n" \
"v_mul_f32_e32 v151, v54, v151\n" \
M_"v[228:231], a[4:7], v[120:123], v[228:231]\n" \
C_"v153, v154, v155\n" \
"v_mul_f32_e32 v156, v54, v156\n" \
M_"v[232:235], a[8:11], v[120:123], v[232:235]\n" \
"v_mul_f32_e32 v157, v54, v157\n" \
"v_mul_f32_e32 v158, v54, v158\n" \
M_"v[236:239], a[12:15], v[120:123], v[236:239]\n" \
"v_mul_f32_e32 v159, v54, v159\n" \
C_"v161, v162, v163\n" \
M_"v[240:243], a[16:19], v[120:123], v[240:243]\n" \
"v_mul_f32_e32 v164, v55, v164\n" \
"v_mul_f32_e32 v165, v55, v165\n" \
M_"v[244:247], a[20:23], v[120:123], v[244:247]\n" \
"v_mul_f32_e32 v166, v55, v166\n" \
"v_mul_f32_e32 v167, v55, v167\n" \
M_"v[248:251], a[24:27], v[120:123], v[248:251]\n" \
C_"v169, v170, v171\n" \
"s_waitcnt lgkmcnt(4)\n" \
M_"v[252:255], a[28:31], v[120:123], v[252:255]\n" \
"v_mul_f32_e32 v172, v55, v172\n" \
"v_mul_f32_e32 v173, v55, v173\n" \
"v_mul_f32_e32 v174, v55, v174\n" \
"v_mul_f32_e32 v175, v55, v175\n" \
C_"v177, v178, v179\n" \
"v_mul_f32_e32 v180, v55, v180\n" \
"v_mul_f32_e32 v181, v55, v181\n" \
"v_mul_f32_e32 v182, v55, v182\n" \
"v_mul_f32_e32 v183, v55, v183\n" \
C_"v185, v186, v187\n" \
"v_mul_f32_e32 v188, v55, v188\n" \
"v_mul_f32_e32 v189, v55, v189\n" \
"v_mul_f32_e32 v190, v55, v190\n" \
"v_mul_f32_e32 v191, v55, v191\n" \
C_"v130, v132, v133\n" \
C_"v131, v134, v135\n" \
"buffer_store_dwordx4 v[128:131], v20, s[56:59], s68 offen offset:0 sc1\n" \
C_"v138, v140, v141\n" \
C_"v139, v142, v143\n" \
"buffer_store_dwordx4 v[136:139], v20, s[56:59], s68 offen offset:1024 sc1\n" \
C_"v146, v148, v149\n" \
C_"v147, v150, v151\n" \
"buffer_store_dwordx4 v[144:147], v20, s[56:59], s68 offen offset:2048 sc1\n" \
C_"v154, v156, v157\n" \
C_"v155, v158, v159\n" \
"buffer_store_dwordx4 v[152:155], v20, s[56:59], s68 offen offset:3072 sc1\n" \
C_"v162, v164, v165\n" \
C_"v163, v166, v167\n" \
"buffer_store_dwordx4 v[160:163], v20, s[56:59], s69 offen offset:0 sc1\n" \
C_"v170, v172, v173\n" \
C_"v171, v174, v175\n" \
"buffer_store_dwordx4 v[168:171], v20, s[56:59], s69 offen offset:1024 sc1\n" \
C_"v178, v180, v181\n" \
C_"v179, v182, v183\n" \
"buffer_store_dwordx4 v[176:179], v20, s[56:59], s69 offen offset:2048 sc1\n" \
C_"v186, v188, v189\n" \
C_"v187, v190, v191\n" \
"buffer_store_dwordx4 v[184:187], v20, s[56:59], s69 offen offset:3072 sc1\n" \
"s_add_u32 m0, s93, 0x2000\n" \
M_"a[112:115], a[120:123], v[128:131], a[112:115]\n" \
B_"v8, s[80:83], s92 offen offset:0 lds\n" \
M_"a[112:115], a[120:123], v[136:139], a[112:115]\n" \
"v_mfma_f32_32x32x16_bf16 v[128:143], a[120:123], a[120:123], 0\n" \
M_"a[112:115], a[120:123], v[144:147], a[112:115]\n" \
M_"a[112:115], a[120:123], v[152:155], a[112:115]\n" \
"v_mfma_f32_32x32x16_bf16 v[144:159], a[120:123], a[120:123], 0\n" \
"v_add_f32_e32 v56, v56, v24\n" \
M_"a[112:115], a[120:123], v[160:163], a[112:115]\n" \
"v_max_f32_e32 v62, v62, v56\n" \
"v_accvgpr_read_b32 v57, a102\n" \
"s_waitcnt vmcnt(21) lgkmcnt(0)\n" \
"s_barrier\n" \
M_"v[64:67], a[128:131], a[64:67], v[32:35]\n" \
"s_xor_b32 m0, s65, 0x18000\n" \
"v_accvgpr_read_b32 v25, a103\n" \
M_"a[100:103], a[120:123], a[120:123], 0\n" \
B_"v52, s[8:11], s64 offen offset:0 sc1 lds\n" \
M_"v[64:67], a[132:135], a[68:71], v[64:67]\n" \
"v_rcp_f32_e32 v56, v56\n" \
M_"v[64:67], a[136:139], a[72:75], v[64:67]\n" \
M_"a[112:115], a[120:123], v[168:171], a[112:115]\n" \
"v_mfma_f32_32x32x16_bf16 v[160:175], a[120:123], a[120:123], 0\n" \
"v_mul_f32_e32 v56, v56, v60\n" \
M_"a[112:115], a[120:123], v[176:179], a[112:115]\n" \
"v_mul_f32_e32 v192, v56, v192\n" \
"v_mul_f32_e32 v193, v56, v193\n" \
M_"v[64:67], a[140:143], a[76:79], v[64:67]\n" \
B_"v52, s[8:11], s64 offen offset:1024 sc1 lds\n" \
M_"v[68:71], a[156:159], a[76:79], v[32:35]\n" \
C_"v192, v192, v193\n" \
"v_mul_f32_e32 v194, v56, v194\n" \
M_"v[68:71], a[152:155], a[72:75], v[68:71]\n" \
"v_mul_f32_e32 v195, v56, v195\n" \
C_"v193, v194, v195\n" \
M_"v[68:71], a[148:151], a[68:71], v[68:71]\n" \
M_"a[112:115], a[120:123], v[184:187], a[112:115]\n" \
"v_mfma_f32_32x32x16_bf16 v[176:191], a[120:123], a[120:123], 0\n" \
"v_mul_f32_e32 v196, v56, v196\n" \
"v_mul_f32_e32 v197, v56, v197\n" \
"v_mul_f32_e32 v198, v56, v198\n" \
"v_mul_f32_e32 v199, v56, v199\n" \
"v_mul_f32_e32 v200, v56, v200\n" \
"v_mul_f32_e32 v201, v56, v201\n" \
M_"v[68:71], a[144:147], a[64:67], v[68:71]\n" \
B_"v52, s[8:11], s64 offen offset:2048 sc1 lds\n" \
M_"v[76:79], a[144:147], a[80:83], v[36:39]\n" \
C_"v194, v196, v197\n" \
C_"v195, v198, v199\n" \
"buffer_store_dwordx4 v[192:195], v20, s[56:59], s70 offen offset:0 sc1\n" \
M_"v[76:79], a[148:151], a[84:87], v[76:79]\n" \
C_"v200, v200, v201\n" \
"v_mul_f32_e32 v202, v56, v202\n" \
M_"v[76:79], a[152:155], a[88:91], v[76:79]\n" \
"v_mul_f32_e32 v203, v56, v203\n" \
"v_add_f32_e32 v57, v57, v25\n" \
M_"v[76:79], a[156:159], a[92:95], v[76:79]\n" \
B_"v52, s[8:11], s64 offen offset:3072 sc1 lds\n" \
M_"v[72:75], a[140:143], a[92:95], v[36:39]\n" \
"s_mov_b32 m0, s65\n" \
C_"v201, v202, v203\n" \
M_"v[72:75], a[136:139], a[88:91], v[72:75]\n" \
M_"a[112:115], a[120:123], v[192:195], a[112:115]\n" \
"v_mul_f32_e32 v204, v56, v204\n" \
"v_mul_f32_e32 v205, v56, v205\n" \
M_"v[72:75], a[132:135], a[84:87], v[72:75]\n" \
"v_mul_f32_e32 v206, v56, v206\n" \
"v_mul_f32_e32 v207, v56, v207\n" \
M_"v[72:75], a[128:131], a[80:83], v[72:75]\n" \
B_"v53, s[8:11], s64 offen offset:0 sc1 lds\n" \
M_"v[80:83], a[128:131], a[192:195], v[40:43]\n" \
"v_mul_f32_e32 v208, v56, v208\n" \
"v_mul_f32_e32 v209, v56, v209\n" \
M_"v[80:83], a[132:135], a[196:199], v[80:83]\n" \
"v_max_f32_e32 v62, v62, v57\n" \
C_"v202, v204, v205\n" \
M_"v[80:83], a[136:139], a[200:203], v[80:83]\n" \
B_"v53, s[8:11], s64 offen offset:1024 sc1 lds\n" \
M_"v[80:83], a[140:143], a[204:207], v[80:83]\n" \
C_"v203, v206, v207\n" \
C_"v208, v208, v209\n" \
"buffer_store_dwordx4 v[200:203], v20, s[56:59], s70 offen offset:1024 sc1\n" \
M_"v[84:87], a[156:159], a[204:207], v[40:43]\n" \
"v_mul_f32_e32 v210, v56, v210\n" \
"v_mul_f32_e32 v211, v56, v211\n" \
M_"v[84:87], a[152:155], a[200:203], v[84:87]\n" \
"v_rcp_f32_e32 v57, v57\n" \
M_"v[84:87], a[148:151], a[196:199], v[84:87]\n" \
B_"v53, s[8:11], s64 offen offset:2048 sc1 lds\n" \
M_"v[84:87], a[144:147], a[192:195], v[84:87]\n" \
M_"a[112:115], a[120:123], v[200:203], a[112:115]\n" \
"v_mfma_f32_32x32x16_bf16 v[192:207], a[120:123], a[120:123], 0\n" \
C_"v209, v210, v211\n" \
"v_mul_f32_e32 v212, v56, v212\n" \
".fill (L_ab + 20480 - .) / 4, 4, 0xBF800000\n" \
"L_ap5:\n" \
"s_cbranch_execz L_ap0\n" \
"v_mul_f32_e32 v213, v56, v213\n" \
"v_mul_f32_e32 v214, v56, v214\n" \
"v_mul_f32_e32 v215, v56, v215\n" \
"v_mul_f32_e32 v216, v56, v216\n" \
M_"v[92:95], a[144:147], a[208:211], v[44:47]\n" \
R_"a[144:147], v48 offset:12288\n" \
"v_mul_f32_e32 v217, v56, v217\n" \
M_"v[92:95], a[148:151], a[212:215], v[92:95]\n" \
R_"a[148:151], v48 offset:13312\n" \
C_"v210, v212, v213\n" \
M_"v[92:95], a[152:155], a[216:219], v[92:95]\n" \
R_"a[152:155], v48 offset:14336\n" \
C_"v211, v214, v215\n" \
"buffer_store_dwordx4 v[208:211], v20, s[56:59], s70 offen offset:2048 sc1\n" \
M_"v[92:95], a[156:159], a[220:223], v[92:95]\n" \
R_"a[156:159], v48 offset:15360\n" \
C_"v216, v216, v217\n" \
M_"v[88:91], a[140:143], a[220:223], v[44:47]\n" \
R_"a[140:143], v48 offset:11264\n" \
"v_mul_f32_e32 v218, v56, v218\n" \
M_"v[88:91], a[136:139], a[216:219], v[88:91]\n" \
R_"a[136:139], v48 offset:10240\n" \
"v_mul_f32_e32 v219, v56, v219\n" \
M_"v[88:91], a[132:135], a[212:215], v[88:91]\n" \
R_"a[132:135], v48 offset:9216\n" \
"v_mul_f32_e32 v57, v57, v61\n" \
M_"v[88:91], a[128:131], a[208:211], v[88:91]\n" \
R_"a[128:131], v48 offset:8192\n" \
M_"a[112:115], a[120:123], v[208:211], a[112:115]\n" \
C_"v217, v218, v219\n" \
"v_mul_f32_e32 v220, v56, v220\n" \
"v_mul_f32_e32 v221, v56, v221\n" \
"v_mul_f32_e32 v222, v56, v222\n" \
"v_mul_f32_e32 v223, v56, v223\n" \
"v_mul_f32_e32 v224, v57, v224\n" \
"v_mul_f32_e32 v225, v57, v225\n" \
C_"v218, v220, v221\n" \
C_"v219, v222, v223\n" \
"buffer_store_dwordx4 v[216:219], v20, s[56:59], s70 offen offset:3072 sc1\n" \
C_"v224, v224, v225\n" \
"v_mul_f32_e32 v226, v57, v226\n" \
"v_mul_f32_e32 v227, v57, v227\n" \
M_"a[112:115], a[120:123], v[216:219], a[112:115]\n" \
"v_mfma_f32_32x32x16_bf16 v[208:223], a[120:123], a[120:123], 0\n" \
C_"v225, v226, v227\n" \
"v_mul_f32_e32 v228, v57, v228\n" \
"v_mul_f32_e32 v229, v57, v229\n" \
"v_mul_f32_e32 v230, v57, v230\n" \
"v_mul_f32_e32 v231, v57, v231\n" \
"v_mul_f32_e32 v232, v57, v232\n" \
"v_mul_f32_e32 v233, v57, v233\n" \
C_"v226, v228, v229\n" \
C_"v227, v230, v231\n" \
"buffer_store_dwordx4 v[224:227], v20, s[56:59], s71 offen offset:0 sc1\n" \
C_"v232, v232, v233\n" \
"v_mul_f32_e32 v234, v57, v234\n" \
"v_mul_f32_e32 v235, v57, v235\n" \
M_"a[112:115], a[120:123], v[224:227], a[112:115]\n" \
C_"v233, v234, v235\n" \
"v_mul_f32_e32 v236, v57, v236\n" \
"v_mul_f32_e32 v237, v57, v237\n" \
"v_mul_f32_e32 v238, v57, v238\n" \
"v_mul_f32_e32 v239, v57, v239\n" \
"v_mul_f32_e32 v240, v57, v240\n" \
"v_mul_f32_e32 v241, v57, v241\n" \
C_"v234, v236, v237\n" \
C_"v235, v238, v239\n" \
"buffer_store_dwordx4 v[232:235], v20, s[56:59], s71 offen offset:1024 sc1\n" \
C_"v240, v240, v241\n" \
"v_mul_f32_e32 v242, v57, v242\n" \
"v_mul_f32_e32 v243, v57, v243\n" \
M_"a[112:115], a[120:123], v[232:235], a[112:115]\n" \
"v_mfma_f32_32x32x16_bf16 v[224:239], a[120:123], a[120:123], 0\n" \
C_"v241, v242, v243\n" \
"v_mul_f32_e32 v244, v57, v244\n" \
"v_mul_f32_e32 v245, v57, v245\n" \
"v_mul_f32_e32 v246, v57, v246\n" \
"v_mul_f32_e32 v247, v57, v247\n" \
"v_mul_f32_e32 v248, v57, v248\n" \
"v_mul_f32_e32 v249, v57, v249\n" \
C_"v242, v244, v245\n" \
C_"v243, v246, v247\n" \
"buffer_store_dwordx4 v[240:243], v20, s[56:59], s71 offen offset:2048 sc1\n" \
C_"v248, v248, v249\n" \
"v_mul_f32_e32 v250, v57, v250\n" \
"v_mul_f32_e32 v251, v57, v251\n" \
M_"a[112:115], a[120:123], v[240:243], a[112:115]\n" \
C_"v249, v250, v251\n" \
"v_mul_f32_e32 v252, v57, v252\n" \
"v_mul_f32_e32 v253, v57, v253\n" \
"v_mul_f32_e32 v254, v57, v254\n" \
"v_mul_f32_e32 v255, v57, v255\n" \
C_"v250, v252, v253\n" \
C_"v251, v254, v255\n" \
"buffer_store_dwordx4 v[248:251], v20, s[56:59], s71 offen offset:3072 sc1\n" \
B_"v53, s[8:11], s64 offen offset:3072 sc1 lds\n" \
"s_waitcnt vmcnt(16) lgkmcnt(0)\n" \
"s_barrier\n" \
M_"a[112:115], a[120:123], v[248:251], a[112:115]\n" \
"v_mfma_f32_32x32x16_bf16 v[240:255], a[120:123], a[120:123], 0\n" \
E_"v70, v70\n" \
E_"v71, v71\n" \
E_"v72, v72\n" \
E_"v73, v73\n" \
"s_add_u32 s64, s64, 0x10000\n" \
"v_cmp_lt_f32_e64 s[72:73], s21, v62\n" \
"v_readlane_b32 s95, v11, 3\n" \
"v_accvgpr_read_b32 v63, a112\n" \
"v_cmp_u_f32_e32 vcc, v63, v63\n" \
E_"v68, v68\n" \
E_"v69, v69\n" \
E_"v74, v74\n" \
E_"v75, v75\n" \
"s_or_b64 vcc, s[72:73], vcc\n" \
"s_cselect_b32 s95, 0x40000000, s95\n" \
"s_cselect_b32 s94, 0x7f800000, 0\n" \
C_"v71, v70, v71\n" \
E_"v66, v66\n" \
E_"v67, v67\n" \
E_"v64, v64\n" \
E_"v65, v65\n" \
C_"v72, v72, v73\n" \
E_"v76, v76\n" \
E_"v77, v77\n" \
E_"v78, v78\n" \
E_"v79, v79\n" \
C_"v70, v68, v69\n" \
C_"v73, v74, v75\n" \
"v_add_u32_e32 v49, 0x10000, v48\n" \
"s_xor_b32 s65, s65, 0x8000\n" \
C_"v69, v66, v67\n" \
C_"v68, v64, v65\n" \
C_"v74, v76, v77\n" \
C_"v75, v78, v79\n" \
"v_xor_b32_e32 v48, 0x8000, v48\n" \
"s_sub_u32 s66, s19, 2\n" \
"s_branch L_2_%=\n" \
"L_f1_%=:\n" \
R_"a[160:163], v49 offset:0\n" \
R_"a[164:167], v49 offset:1024\n" \
R_"a[168:171], v49 offset:2048\n" \
M_"v[104:107], a[128:131], a[80:83], v[36:39]\n" \
C_"v192, v192, v193\n" \
C_"v224, v224, v225\n" \
C_"v193, v194, v195\n" \
C_"v225, v226, v227\n" \
R_"a[172:175], v49 offset:3072\n" \
C_"v194, v196, v197\n" \
C_"v195, v198, v199\n" \
C_"v226, v228, v229\n" \
C_"v227, v230, v231\n" \
M_"v[104:107], a[132:135], a[84:87], v[104:107]\n" \
"v_lshl_add_u32 v50, s15, 2, v20\n" \
C_"v196, v200, v201\n" \
C_"v197, v202, v203\n" \
C_"v198, v204, v205\n" \
C_"v199, v206, v207\n" \
C_"v228, v232, v233\n" \
C_"v229, v234, v235\n" \
C_"v230, v236, v237\n" \
C_"v231, v238, v239\n" \
"v_add_u32_e32 v50, 0x1a000, v50\n" \
"v_mov_b32_e32 v61, s62\n" \
C_"v200, v208, v209\n" \
C_"v201, v210, v211\n" \
C_"v202, v212, v213\n" \
C_"v203, v214, v215\n" \
C_"v204, v216, v217\n" \
C_"v205, v218, v219\n" \
C_"v206, v220, v221\n" \
C_"v207, v222, v223\n" \
C_"v232, v240, v241\n" \
C_"v233, v242, v243\n" \
C_"v234, v244, v245\n" \
C_"v235, v246, v247\n" \
C_"v236, v248, v249\n" \
C_"v237, v250, v251\n" \
C_"v238, v252, v253\n" \
C_"v239, v254, v255\n" \
"ds_write_b128 v50, v[192:195] offset:0\n" \
M_"v[104:107], a[136:139], a[88:91], v[104:107]\n" \
R_"a[176:179], v49 offset:4096\n" \
"v_add_u32_e32 v56, 0x49000, v20\n" \
M_"v[104:107], a[140:143], a[92:95], v[104:107]\n" \
"ds_write_b128 v50, v[196:199] offset:1024\n" \
M_"v[108:111], a[156:159], a[92:95], v[36:39]\n" \
R_"a[180:183], v49 offset:5120\n" \
R_"a[184:187], v49 offset:6144\n" \
M_"v[108:111], a[152:155], a[88:91], v[108:111]\n" \
"ds_write_b128 v50, v[200:203] offset:2048\n" \
M_"v[108:111], a[148:151], a[84:87], v[108:111]\n" \
"v_and_b32_e32 v68, v12, v68\n" \
"v_and_b32_e32 v69, v13, v69\n" \
M_"v[108:111], a[144:147], a[80:83], v[108:111]\n" \
"ds_write_b128 v50, v[204:207] offset:3072\n" \
"v_and_b32_e32 v70, v14, v70\n" \
"v_and_b32_e32 v71, v15, v71\n" \
R_"a[188:191], v49 offset:7168\n" \
R_"a[0:3], v20 offset:8192\n" \
"s_waitcnt lgkmcnt(9)\n" \
M_"v[128:131], a[160:163], v[68:71], v[128:131]\n" \
"ds_write_b128 v50, v[224:227] offset:4096\n" \
M_"v[160:163], a[160:163], v[72:75], v[160:163]\n" \
R_"a[4:7], v20 offset:9216\n" \
R_"a[8:11], v20 offset:10240\n" \
M_"v[132:135], a[164:167], v[68:71], v[132:135]\n" \
"ds_write_b128 v50, v[228:231] offset:5120\n" \
M_"v[164:167], a[164:167], v[72:75], v[164:167]\n" \
R_"a[12:15], v20 offset:11264\n" \
R_"a[28:31], v20 offset:15360\n" \
M_"v[136:139], a[168:171], v[68:71], v[136:139]\n" \
"ds_write_b128 v50, v[232:235] offset:6144\n" \
M_"v[168:171], a[168:171], v[72:75], v[168:171]\n" \
R_"a[24:27], v20 offset:14336\n" \
R_"a[20:23], v20 offset:13312\n" \
M_"v[140:143], a[172:175], v[68:71], v[140:143]\n" \
"ds_write_b128 v50, v[236:239] offset:7168\n" \
M_"v[172:175], a[172:175], v[72:75], v[172:175]\n" \
R_"a[16:19], v20 offset:12288\n" \
"s_waitcnt lgkmcnt(15)\n" \
M_"v[144:147], a[176:179], v[68:71], v[144:147]\n" \
"v_xor_b32_e32 v48, 0x4000, v49\n" \
"ds_read_b32 v58, v18 offset:256\n" \
M_"v[176:179], a[176:179], v[72:75], v[176:179]\n" \
"ds_read_b32 v59, v18 offset:384\n" \
R_"a[32:35], v48 offset:8192\n" \
M_"v[148:151], a[180:183], v[68:71], v[148:151]\n" \
R_"a[36:39], v48 offset:9216\n" \
R_"a[40:43], v48 offset:10240\n" \
M_"v[180:183], a[180:183], v[72:75], v[180:183]\n" \
R_"a[44:47], v48 offset:11264\n" \
R_"a[48:51], v48 offset:12288\n" \
M_"v[152:155], a[184:187], v[68:71], v[152:155]\n" \
R_"a[52:55], v48 offset:13312\n" \
R_"a[56:59], v48 offset:14336\n" \
M_"v[184:187], a[184:187], v[72:75], v[184:187]\n" \
R_"a[60:63], v48 offset:15360\n" \
"s_waitcnt lgkmcnt(15)\n" \
M_"v[156:159], a[188:191], v[68:71], v[156:159]\n" \
R_"a[128:131], v49 offset:8192\n" \
R_"a[132:135], v49 offset:9216\n" \
M_"v[188:191], a[188:191], v[72:75], v[188:191]\n" \
R_"a[136:139], v49 offset:10240\n" \
R_"a[140:143], v49 offset:11264\n" \
"v_smfmac_f32_16x16x64_bf16 a[96:99], a[104:107], v[68:75], v21\n" \
R_"a[144:147], v49 offset:12288\n" \
R_"a[148:151], v49 offset:13312\n" \
M_"v[112:115], a[0:3], a[64:67], v[32:35]\n" \
R_"a[152:155], v49 offset:14336\n" \
R_"a[156:159], v49 offset:15360\n" \
M_"v[112:115], a[4:7], a[68:71], v[112:115]\n" \
"v_accvgpr_read_b32 v40, a100\n" \
"v_accvgpr_read_b32 v24, a101\n" \
M_"v[112:115], a[8:11], a[72:75], v[112:115]\n" \
"v_accvgpr_read_b32 v41, a102\n" \
"v_accvgpr_read_b32 v25, a103\n" \
M_"v[112:115], a[12:15], a[76:79], v[112:115]\n" \
"v_add_f32_e32 v40, v40, v24\n" \
"v_add_f32_e32 v41, v41, v25\n" \
M_"v[116:119], a[28:31], a[76:79], v[32:35]\n" \
"v_add_f32_e32 v40, s94, v40\n" \
"s_waitcnt lgkmcnt(15)\n" \
M_"v[116:119], a[24:27], a[72:75], v[116:119]\n" \
"v_add_f32_e32 v41, s94, v41\n" \
"ds_write_b64 v19, v[40:41]\n" \
M_"v[116:119], a[20:23], a[68:71], v[116:119]\n" \
"v_add_u32_e32 v52, s15, v20\n" \
"v_add_u32_e32 v51, 0x10000, v52\n" \
M_"v[116:119], a[16:19], a[64:67], v[116:119]\n" \
E_"v104, v104\n" \
M_"v[124:127], a[16:19], a[80:83], v[36:39]\n" \
E_"v105, v105\n" \
M_"v[124:127], a[20:23], a[84:87], v[124:127]\n" \
E_"v106, v106\n" \
M_"v[124:127], a[24:27], a[88:91], v[124:127]\n" \
E_"v107, v107\n" \
M_"v[124:127], a[28:31], a[92:95], v[124:127]\n" \
C_"v104, v104, v105\n" \
C_"v105, v106, v107\n" \
M_"v[120:123], a[12:15], a[92:95], v[36:39]\n" \
E_"v108, v108\n" \
M_"v[120:123], a[8:11], a[88:91], v[120:123]\n" \
E_"v109, v109\n" \
M_"v[120:123], a[4:7], a[84:87], v[120:123]\n" \
E_"v110, v110\n" \
M_"v[120:123], a[0:3], a[80:83], v[120:123]\n" \
E_"v111, v111\n" \
"s_waitcnt lgkmcnt(0)\n" \
"s_barrier\n" \
R_"v[208:211], v52 offset:0\n" \
R_"v[212:215], v52 offset:1024\n" \
R_"v[216:219], v52 offset:2048\n" \
R_"v[220:223], v52 offset:3072\n" \
R_"v[240:243], v51 offset:32768\n" \
R_"v[244:247], v51 offset:33792\n" \
R_"v[248:251], v51 offset:34816\n" \
R_"v[252:255], v51 offset:35840\n" \
C_"v106, v108, v109\n" \
C_"v107, v110, v111\n" \
"v_and_b32_e32 v104, v12, v104\n" \
"v_and_b32_e32 v105, v13, v105\n" \
"v_and_b32_e32 v106, v14, v106\n" \
"s_waitcnt vmcnt(0)\n" \
"s_mov_b64 exec, 1\n" \
"buffer_store_dword v61, off, s[56:59], s95 offset:0 sc1\n" \
"s_mov_b64 exec, -1\n" \
"v_and_b32_e32 v107, v15, v107\n" \
"v_xor_b32_e32 v63, 0x400, v19\n" \
"ds_read_b64 v[42:43], v63\n" \
M_"a[96:99], a[116:119], v[104:107], a[96:99]\n" \
"v_mov_b32_e32 v194, v28\n" \
"v_mov_b32_e32 v195, v29\n" \
M_"v[160:163], a[128:131], v[104:107], v[160:163]\n" \
"v_mov_b32_e32 v196, 0\n" \
"v_mov_b32_e32 v197, 0\n" \
M_"v[164:167], a[132:135], v[104:107], v[164:167]\n" \
"v_mov_b32_e32 v192, 0\n" \
"v_mov_b32_e32 v193, 0\n" \
M_"v[168:171], a[136:139], v[104:107], v[168:171]\n" \
"v_readlane_b32 s93, v11, 25\n" \
"v_readlane_b32 s92, v11, 30\n" \
M_"v[172:175], a[140:143], v[104:107], v[172:175]\n" \
B_"a[192:195], v20, s[56:59], s60 offen offset:0 sc1\n" \
M_"v[176:179], a[144:147], v[104:107], v[176:179]\n" \
E_"v118, v118\n" \
M_"v[180:183], a[148:151], v[104:107], v[180:183]\n" \
E_"v119, v119\n" \
M_"v[184:187], a[152:155], v[104:107], v[184:187]\n" \
"s_add_u32 m0, s93, 0x0\n" \
C_"v119, v118, v119\n" \
M_"v[188:191], a[156:159], v[104:107], v[188:191]\n" \
E_"v120, v120\n" \
"s_waitcnt lgkmcnt(1)\n" \
"s_barrier\n" \
B_"a[208:211], v56, s[56:59], s60 offen offset:0 sc1\n" \
M_"v[128:131], v[194:197], v[208:211], v[128:131]\n" \
E_"v121, v121\n" \
M_"v[132:135], v[192:195], v[208:211], v[132:135]\n" \
E_"v116, v116\n" \
M_"v[136:139], v[194:197], v[212:215], v[136:139]\n" \
E_"v117, v117\n" \
M_"v[140:143], v[192:195], v[212:215], v[140:143]\n" \
E_"v122, v122\n" \
M_"v[144:147], v[194:197], v[216:219], v[144:147]\n" \
E_"v123, v123\n" \
M_"v[148:151], v[192:195], v[216:219], v[148:151]\n" \
B_"a[196:199], v20, s[56:59], s60 offen offset:1024 sc1\n" \
M_"v[152:155], v[194:197], v[220:223], v[152:155]\n" \
E_"v114, v114\n" \
M_"v[156:159], v[192:195], v[220:223], v[156:159]\n" \
E_"v115, v115\n" \
M_"v[160:163], v[194:197], v[240:243], v[160:163]\n" \
E_"v112, v112\n" \
M_"v[164:167], v[192:195], v[240:243], v[164:167]\n" \
E_"v113, v113\n" \
M_"v[168:171], v[194:197], v[244:247], v[168:171]\n" \
C_"v120, v120, v121\n" \
C_"v118, v116, v117\n" \
M_"v[172:175], v[192:195], v[244:247], v[172:175]\n" \
B_"a[212:215], v56, s[56:59], s60 offen offset:1024 sc1\n" \
M_"v[176:179], v[194:197], v[248:251], v[176:179]\n" \
E_"v124, v124\n" \
M_"v[180:183], v[192:195], v[248:251], v[180:183]\n" \
E_"v125, v125\n" \
M_"v[184:187], v[194:197], v[252:255], v[184:187]\n" \
E_"v126, v126\n" \
M_"v[188:191], v[192:195], v[252:255], v[188:191]\n" \
E_"v127, v127\n" \
C_"v121, v122, v123\n" \
C_"v117, v114, v115\n" \
C_"v116, v112, v113\n" \
C_"v122, v124, v125\n" \
C_"v123, v126, v127\n" \
B_"a[200:203], v20, s[56:59], s60 offen offset:2048 sc1\n" \
"s_nop 0\n" \
"v_smfmac_f32_16x16x64_bf16 a[96:99], a[104:107], v[116:123], v21\n" \
M_"v[128:131], a[32:35], v[116:119], v[128:131]\n" \
M_"v[160:163], a[32:35], v[120:123], v[160:163]\n" \
M_"v[132:135], a[36:39], v[116:119], v[132:135]\n" \
M_"v[164:167], a[36:39], v[120:123], v[164:167]\n" \
M_"v[136:139], a[40:43], v[116:119], v[136:139]\n" \
B_"a[216:219], v56, s[56:59], s60 offen offset:2048 sc1\n" \
M_"v[168:171], a[40:43], v[120:123], v[168:171]\n" \
M_"v[140:143], a[44:47], v[116:119], v[140:143]\n" \
"v_accvgpr_read_b32 v54, a96\n" \
"v_accvgpr_read_b32 v22, a97\n" \
M_"v[172:175], a[44:47], v[120:123], v[172:175]\n" \
"v_add_f32_e32 v54, v54, v22\n" \
"s_waitcnt lgkmcnt(0)\n" \
"v_add_f32_e32 v54, v54, v42\n" \
M_"v[144:147], a[48:51], v[116:119], v[144:147]\n" \
"v_max_f32_e32 v62, v62, v54\n" \
"v_accvgpr_read_b32 v55, a98\n" \
M_"v[176:179], a[48:51], v[120:123], v[176:179]\n" \
"v_rcp_f32_e32 v54, v54\n" \
B_"a[204:207], v20, s[56:59], s60 offen offset:3072 sc1\n" \
M_"v[148:151], a[52:55], v[116:119], v[148:151]\n" \
"v_mul_f32_e32 v54, v54, v58\n" \
"v_mul_f32_e32 v128, v54, v128\n" \
M_"v[180:183], a[52:55], v[120:123], v[180:183]\n" \
"v_mul_f32_e32 v129, v54, v129\n" \
"v_mul_f32_e32 v130, v54, v130\n" \
"v_mul_f32_e32 v131, v54, v131\n" \
"v_mul_f32_e32 v132, v54, v132\n" \
"v_mul_f32_e32 v133, v54, v133\n" \
"v_mul_f32_e32 v134, v54, v134\n" \
"v_mul_f32_e32 v135, v54, v135\n" \
C_"v64, v128, v129\n" \
C_"v65, v130, v131\n" \
C_"v66, v132, v133\n" \
C_"v67, v134, v135\n" \
"v_accvgpr_read_b32 v23, a99\n" \
M_"v[152:155], a[56:59], v[116:119], v[152:155]\n" \
M_"a[112:115], a[120:123], v[64:67], a[112:115]\n" \
B_"a[220:223], v56, s[56:59], s60 offen offset:3072 sc1\n" \
"v_mul_f32_e32 v136, v54, v136\n" \
"v_mul_f32_e32 v137, v54, v137\n" \
"v_mul_f32_e32 v138, v54, v138\n" \
"v_mul_f32_e32 v139, v54, v139\n" \
"v_mul_f32_e32 v140, v54, v140\n" \
"v_mul_f32_e32 v141, v54, v141\n" \
"v_mul_f32_e32 v142, v54, v142\n" \
"v_mul_f32_e32 v143, v54, v143\n" \
"v_add_f32_e32 v55, v55, v23\n" \
C_"v68, v136, v137\n" \
C_"v69, v138, v139\n" \
C_"v70, v140, v141\n" \
C_"v71, v142, v143\n" \
"v_add_f32_e32 v55, v55, v43\n" \
"v_mul_f32_e32 v144, v54, v144\n" \
M_"a[112:115], a[120:123], v[68:71], a[112:115]\n" \
"v_mul_f32_e32 v145, v54, v145\n" \
"v_mul_f32_e32 v146, v54, v146\n" \
"v_mul_f32_e32 v147, v54, v147\n" \
"v_mul_f32_e32 v148, v54, v148\n" \
B_"v0, s[80:83], s92 offen offset:0 lds\n" \
"v_mul_f32_e32 v149, v54, v149\n" \
"v_mul_f32_e32 v150, v54, v150\n" \
"v_mul_f32_e32 v151, v54, v151\n" \
"v_max_f32_e32 v62, v62, v55\n" \
C_"v72, v144, v145\n" \
C_"v73, v146, v147\n" \
C_"v74, v148, v149\n" \
C_"v75, v150, v151\n" \
"v_rcp_f32_e32 v55, v55\n" \
M_"v[184:187], a[56:59], v[120:123], v[184:187]\n" \
M_"a[112:115], a[120:123], v[72:75], a[112:115]\n" \
"v_mul_f32_e32 v152, v54, v152\n" \
"v_mul_f32_e32 v153, v54, v153\n" \
"v_mul_f32_e32 v154, v54, v154\n" \
"v_mul_f32_e32 v155, v54, v155\n" \
C_"v76, v152, v153\n" \
B_"v1, s[80:83], s92 offen offset:1024 lds\n" \
C_"v77, v154, v155\n" \
"v_mul_f32_e32 v55, v55, v59\n" \
"v_mul_f32_e32 v160, v55, v160\n" \
"v_mul_f32_e32 v161, v55, v161\n" \
"v_mul_f32_e32 v162, v55, v162\n" \
"v_mul_f32_e32 v163, v55, v163\n" \
"v_mul_f32_e32 v164, v55, v164\n" \
"v_mul_f32_e32 v165, v55, v165\n" \
"v_mul_f32_e32 v166, v55, v166\n" \
"v_mul_f32_e32 v167, v55, v167\n" \
C_"v80, v160, v161\n" \
C_"v81, v162, v163\n" \
C_"v82, v164, v165\n" \
C_"v83, v166, v167\n" \
M_"v[156:159], a[60:63], v[116:119], v[156:159]\n" \
"v_mul_f32_e32 v168, v55, v168\n" \
"v_mul_f32_e32 v169, v55, v169\n" \
"v_mul_f32_e32 v170, v55, v170\n" \
"v_mul_f32_e32 v171, v55, v171\n" \
"v_mul_f32_e32 v172, v55, v172\n" \
B_"v2, s[80:83], s92 offen offset:2048 lds\n" \
"v_mul_f32_e32 v173, v55, v173\n" \
"v_mul_f32_e32 v174, v55, v174\n" \
"v_mul_f32_e32 v156, v54, v156\n" \
"v_mul_f32_e32 v157, v54, v157\n" \
"v_mul_f32_e32 v158, v54, v158\n" \
"v_mul_f32_e32 v159, v54, v159\n" \
C_"v78, v156, v157\n" \
C_"v79, v158, v159\n" \
"v_mul_f32_e32 v175, v55, v175\n" \
C_"v84, v168, v169\n" \
M_"a[112:115], a[120:123], v[76:79], a[112:115]\n" \
M_"a[112:115], a[120:123], v[80:83], a[112:115]\n" \
C_"v85, v170, v171\n" \
C_"v86, v172, v173\n" \
C_"v87, v174, v175\n" \
"v_mul_f32_e32 v176, v55, v176\n" \
"v_mul_f32_e32 v177, v55, v177\n" \
B_"v3, s[80:83], s92 offen offset:3072 lds\n" \
"s_add_u32 m0, s93, 0x1000\n" \
M_"a[112:115], a[120:123], v[84:87], a[112:115]\n" \
"v_mul_f32_e32 v178, v55, v178\n" \
"v_mul_f32_e32 v179, v55, v179\n" \
"v_mul_f32_e32 v180, v55, v180\n" \
"v_mul_f32_e32 v181, v55, v181\n" \
"v_mul_f32_e32 v182, v55, v182\n" \
"v_mul_f32_e32 v183, v55, v183\n" \
C_"v96, v176, v177\n" \
C_"v97, v178, v179\n" \
C_"v98, v180, v181\n" \
C_"v99, v182, v183\n" \
M_"v[188:191], a[60:63], v[120:123], v[188:191]\n" \
"v_mul_f32_e32 v184, v55, v184\n" \
M_"a[112:115], a[120:123], v[96:99], a[112:115]\n" \
"v_mul_f32_e32 v185, v55, v185\n" \
"v_mul_f32_e32 v186, v55, v186\n" \
B_"v4, s[80:83], s92 offen offset:0 lds\n" \
"v_mul_f32_e32 v187, v55, v187\n" \
C_"v100, v184, v185\n" \
C_"v101, v186, v187\n" \
"v_mul_f32_e32 v188, v55, v188\n" \
"v_mul_f32_e32 v189, v55, v189\n" \
"v_mul_f32_e32 v190, v55, v190\n" \
"v_mul_f32_e32 v191, v55, v191\n" \
C_"v102, v188, v189\n" \
C_"v103, v190, v191\n" \
"v_cmp_lt_f32_e64 s[72:73], s21, v62\n" \
B_"v5, s[80:83], s92 offen offset:1024 lds\n" \
M_"a[112:115], a[120:123], v[100:103], a[112:115]\n" \
B_"v6, s[80:83], s92 offen offset:2048 lds\n" \
B_"v7, s[80:83], s92 offen offset:3072 lds\n" \
"s_add_u32 m0, s93, 0x2000\n" \
"s_nop 0\n" \
B_"v8, s[80:83], s92 offen offset:0 lds\n" \
"s_nop 2\n" \
"v_accvgpr_read_b32 v63, a112\n" \
"v_cmp_u_f32_e32 vcc, v63, v63\n" \
"s_nop 0\n" \
"s_or_b64 vcc, s[72:73], vcc\n" \
"s_cbranch_scc1 L_end_%=\n" \
"s_branch L_out_%=\n" \
".fill (L_ab + 24576 - .) / 4, 4, 0xBF800000\n" \
".fill (L_ab + 24576 - .) / 4, 4, 0xBF800000\n" \
"L_ap6:\n" \
"s_setpc_b64 s[88:89]\n" \
"L_gs7:\n" \
M_"v[104:107], a[128:131], a[16:19], v[36:39]\n" \
"s_add_u32 s68, s60, 0xfff6e000\n" \
"s_add_u32 s69, s60, 0xfffb7000\n" \
"s_add_u32 s70, s60, 0x0\n" \
"s_add_u32 s71, s60, 0x49000\n" \
M_"v[104:107], a[132:135], a[20:23], v[104:107]\n" \
"s_xor_b32 m0, s65, 0x12000\n" \
R_"a[160:163], v49 offset:0\n" \
M_"v[104:107], a[136:139], a[24:27], v[104:107]\n" \
B_"v50, s[8:11], s64 offen offset:0 sc1 lds\n" \
M_"v[104:107], a[140:143], a[28:31], v[104:107]\n" \
R_"a[164:167], v49 offset:1024\n" \
R_"a[168:171], v49 offset:2048\n" \
M_"v[108:111], a[156:159], a[28:31], v[36:39]\n" \
R_"a[172:175], v49 offset:3072\n" \
R_"a[176:179], v49 offset:4096\n" \
M_"v[108:111], a[152:155], a[24:27], v[108:111]\n" \
B_"v50, s[8:11], s64 offen offset:1024 sc1 lds\n" \
M_"v[108:111], a[148:151], a[20:23], v[108:111]\n" \
"s_mov_b32 s65, s13\n" \
"v_add_u32_e32 v63, s15, v20\n" \
M_"v[108:111], a[144:147], a[16:19], v[108:111]\n" \
"s_add_u32 s67, s14, 40960\n" \
"v_add_u32_e32 v50, 0x6000, v63\n" \
M_"v[116:119], a[144:147], a[32:35], v[40:43]\n" \
"s_xor_b32 m0, s65, 0x8000\n" \
R_"a[180:183], v49 offset:5120\n" \
M_"v[116:119], a[148:151], a[36:39], v[116:119]\n" \
R_"a[184:187], v49 offset:6144\n" \
"v_and_b32_e32 v68, v12, v68\n" \
M_"v[116:119], a[152:155], a[40:43], v[116:119]\n" \
"v_and_b32_e32 v69, v13, v69\n" \
"v_and_b32_e32 v70, v14, v70\n" \
M_"v[116:119], a[156:159], a[44:47], v[116:119]\n" \
B_"v50, s[8:11], s67 offen offset:0 sc1 lds\n" \
M_"v[112:115], a[140:143], a[44:47], v[40:43]\n" \
"v_and_b32_e32 v71, v15, v71\n" \
R_"a[188:191], v49 offset:7168\n" \
M_"v[112:115], a[136:139], a[40:43], v[112:115]\n" \
E_"v86, v86\n" \
M_"v[112:115], a[132:135], a[36:39], v[112:115]\n" \
E_"v87, v87\n" \
M_"v[112:115], a[128:131], a[32:35], v[112:115]\n" \
E_"v84, v84\n" \
M_"v[120:123], a[128:131], a[48:51], v[44:47]\n" \
B_"v50, s[8:11], s67 offen offset:1024 sc1 lds\n" \
M_"v[120:123], a[132:135], a[52:55], v[120:123]\n" \
E_"v85, v85\n" \
M_"v[120:123], a[136:139], a[56:59], v[120:123]\n" \
C_"v87, v86, v87\n" \
C_"v86, v84, v85\n" \
M_"v[120:123], a[140:143], a[60:63], v[120:123]\n" \
E_"v82, v82\n" \
M_"v[124:127], a[156:159], a[60:63], v[44:47]\n" \
E_"v83, v83\n" \
M_"v[124:127], a[152:155], a[56:59], v[124:127]\n" \
E_"v80, v80\n" \
M_"v[124:127], a[148:151], a[52:55], v[124:127]\n" \
B_"v50, s[8:11], s67 offen offset:2048 sc1 lds\n" \
M_"v[124:127], a[144:147], a[48:51], v[124:127]\n" \
C_"v85, v82, v83\n" \
"s_waitcnt lgkmcnt(1)\n" \
M_"v[128:131], a[160:163], v[68:71], v[128:131]\n" \
E_"v81, v81\n" \
M_"v[160:163], a[160:163], v[72:75], v[160:163]\n" \
C_"v84, v80, v81\n" \
"s_mov_b32 s64, s14\n" \
M_"v[132:135], a[164:167], v[68:71], v[132:135]\n" \
E_"v88, v88\n" \
M_"v[164:167], a[164:167], v[72:75], v[164:167]\n" \
E_"v89, v89\n" \
M_"v[136:139], a[168:171], v[68:71], v[136:139]\n" \
B_"v50, s[8:11], s67 offen offset:3072 sc1 lds\n" \
M_"v[168:171], a[168:171], v[72:75], v[168:171]\n" \
E_"v90, v90\n" \
M_"v[140:143], a[172:175], v[68:71], v[140:143]\n" \
E_"v91, v91\n" \
M_"v[172:175], a[172:175], v[72:75], v[172:175]\n" \
C_"v88, v88, v89\n" \
C_"v89, v90, v91\n" \
M_"v[144:147], a[176:179], v[68:71], v[144:147]\n" \
E_"v92, v92\n" \
M_"v[176:179], a[176:179], v[72:75], v[176:179]\n" \
E_"v93, v93\n" \
M_"v[148:151], a[180:183], v[68:71], v[148:151]\n" \
E_"v94, v94\n" \
M_"v[180:183], a[180:183], v[72:75], v[180:183]\n" \
E_"v95, v95\n" \
M_"v[152:155], a[184:187], v[68:71], v[152:155]\n" \
C_"v90, v92, v93\n" \
C_"v91, v94, v95\n" \
M_"v[184:187], a[184:187], v[72:75], v[184:187]\n" \
"v_add_u32_e32 v51, 0x12000, v63\n" \
"s_waitcnt lgkmcnt(0)\n" \
M_"v[156:159], a[188:191], v[68:71], v[156:159]\n" \
"s_xor_b32 m0, s65, 0xa000\n" \
"ds_read_b32 v58, v18 offset:512\n" \
M_"v[188:191], a[188:191], v[72:75], v[188:191]\n" \
B_"v51, s[8:11], s64 offen offset:0 sc1 lds\n" \
"v_smfmac_f32_16x16x64_bf16 a[96:99], a[104:107], v[68:75], v21\n" \
"ds_read_b32 v59, v18 offset:640\n" \
"ds_read_b32 v60, v18 offset:768\n" \
M_"v[220:223], a[188:191], v[84:87], v[220:223]\n" \
B_"v51, s[8:11], s64 offen offset:1024 sc1 lds\n" \
M_"v[252:255], a[188:191], v[88:91], v[252:255]\n" \
"ds_read_b32 v61, v18 offset:896\n" \
R_"a[128:131], v48 offset:0\n" \
M_"v[216:219], a[184:187], v[84:87], v[216:219]\n" \
R_"a[144:147], v48 offset:4096\n" \
R_"a[132:135], v48 offset:1024\n" \
M_"v[248:251], a[184:187], v[88:91], v[248:251]\n" \
R_"a[148:151], v48 offset:5120\n" \
R_"a[136:139], v48 offset:2048\n" \
M_"v[212:215], a[180:183], v[84:87], v[212:215]\n" \
R_"a[152:155], v48 offset:6144\n" \
R_"a[140:143], v48 offset:3072\n" \
M_"v[244:247], a[180:183], v[88:91], v[244:247]\n" \
R_"a[156:159], v48 offset:7168\n" \
"v_add_u32_e32 v52, 0x14000, v63\n" \
M_"v[208:211], a[176:179], v[84:87], v[208:211]\n" \
B_"v51, s[8:11], s64 offen offset:2048 sc1 lds\n" \
M_"v[240:243], a[176:179], v[88:91], v[240:243]\n" \
E_"v104, v104\n" \
M_"v[204:207], a[172:175], v[84:87], v[204:207]\n" \
E_"v105, v105\n" \
M_"v[236:239], a[172:175], v[88:91], v[236:239]\n" \
B_"v51, s[8:11], s64 offen offset:3072 sc1 lds\n" \
M_"v[200:203], a[168:171], v[84:87], v[200:203]\n" \
E_"v106, v106\n" \
M_"v[232:235], a[168:171], v[88:91], v[232:235]\n" \
E_"v107, v107\n" \
M_"v[196:199], a[164:167], v[84:87], v[196:199]\n" \
C_"v104, v104, v105\n" \
C_"v105, v106, v107\n" \
M_"v[228:231], a[164:167], v[88:91], v[228:231]\n" \
E_"v108, v108\n" \
M_"v[192:195], a[160:163], v[84:87], v[192:195]\n" \
E_"v109, v109\n" \
M_"v[224:227], a[160:163], v[88:91], v[224:227]\n" \
E_"v110, v110\n" \
"v_smfmac_f32_16x16x64_bf16 a[100:103], a[104:107], v[84:91], v21\n" \
E_"v111, v111\n" \
C_"v106, v108, v109\n" \
C_"v107, v110, v111\n" \
"v_and_b32_e32 v104, v12, v104\n" \
"v_and_b32_e32 v105, v13, v105\n" \
"v_and_b32_e32 v106, v14, v106\n" \
"v_and_b32_e32 v107, v15, v107\n" \
"v_accvgpr_read_b32 v54, a96\n" \
"v_accvgpr_read_b32 v22, a97\n" \
"v_readlane_b32 s93, v11, 22\n" \
"v_readlane_b32 s80, v11, 11\n" \
"v_readlane_b32 s81, v11, 12\n" \
"s_mov_b32 s82, 0x47fca0\n" \
"s_mov_b32 s83, 0x20000\n" \
"v_readlane_b32 s92, v11, 29\n" \
E_"v118, v118\n" \
E_"v119, v119\n" \
E_"v116, v116\n" \
E_"v117, v117\n" \
C_"v119, v118, v119\n" \
E_"v114, v114\n" \
E_"v115, v115\n" \
E_"v112, v112\n" \
C_"v118, v116, v117\n" \
"s_waitcnt vmcnt(10) lgkmcnt(0)\n" \
"s_barrier\n" \
M_"v[80:83], a[128:131], a[32:35], v[40:43]\n" \
R_"a[160:163], v49 offset:8192\n" \
R_"a[164:167], v49 offset:9216\n" \
M_"v[80:83], a[132:135], a[36:39], v[80:83]\n" \
R_"a[168:171], v49 offset:10240\n" \
"s_xor_b32 m0, s65, 0x18000\n" \
M_"v[80:83], a[136:139], a[40:43], v[80:83]\n" \
B_"v52, s[8:11], s64 offen offset:0 sc1 lds\n" \
M_"v[80:83], a[140:143], a[44:47], v[80:83]\n" \
R_"a[172:175], v49 offset:11264\n" \
R_"a[176:179], v49 offset:12288\n" \
M_"v[84:87], a[156:159], a[44:47], v[40:43]\n" \
R_"a[180:183], v49 offset:13312\n" \
R_"a[184:187], v49 offset:14336\n" \
M_"v[84:87], a[152:155], a[40:43], v[84:87]\n" \
B_"v52, s[8:11], s64 offen offset:1024 sc1 lds\n" \
M_"v[84:87], a[148:151], a[36:39], v[84:87]\n" \
R_"a[188:191], v49 offset:15360\n" \
C_"v117, v114, v115\n" \
M_"v[84:87], a[144:147], a[32:35], v[84:87]\n" \
B_"v52, s[8:11], s64 offen offset:2048 sc1 lds\n" \
M_"v[92:95], a[144:147], a[48:51], v[44:47]\n" \
E_"v113, v113\n" \
M_"v[92:95], a[148:151], a[52:55], v[92:95]\n" \
B_"v52, s[8:11], s64 offen offset:3072 sc1 lds\n" \
M_"v[92:95], a[152:155], a[56:59], v[92:95]\n" \
"s_add_u32 m0, s93, 0x0\n" \
C_"v116, v112, v113\n" \
M_"v[92:95], a[156:159], a[60:63], v[92:95]\n" \
E_"v120, v120\n" \
M_"v[88:91], a[140:143], a[60:63], v[44:47]\n" \
B_"v0, s[80:83], s92 offen offset:0 lds\n" \
M_"v[88:91], a[136:139], a[56:59], v[88:91]\n" \
E_"v121, v121\n" \
M_"v[88:91], a[132:135], a[52:55], v[88:91]\n" \
E_"v122, v122\n" \
M_"v[88:91], a[128:131], a[48:51], v[88:91]\n" \
B_"v1, s[80:83], s92 offen offset:1024 lds\n" \
M_"a[96:99], a[116:119], v[104:107], a[96:99]\n" \
C_"v120, v120, v121\n" \
"s_waitcnt lgkmcnt(5)\n" \
M_"v[160:163], a[160:163], v[104:107], v[160:163]\n" \
E_"v123, v123\n" \
M_"v[164:167], a[164:167], v[104:107], v[164:167]\n" \
B_"v2, s[80:83], s92 offen offset:2048 lds\n" \
M_"v[168:171], a[168:171], v[104:107], v[168:171]\n" \
C_"v121, v122, v123\n" \
"s_waitcnt lgkmcnt(1)\n" \
M_"v[172:175], a[172:175], v[104:107], v[172:175]\n" \
E_"v124, v124\n" \
M_"v[176:179], a[176:179], v[104:107], v[176:179]\n" \
E_"v125, v125\n" \
M_"v[180:183], a[180:183], v[104:107], v[180:183]\n" \
E_"v126, v126\n" \
M_"v[184:187], a[184:187], v[104:107], v[184:187]\n" \
C_"v122, v124, v125\n" \
"s_waitcnt lgkmcnt(0)\n" \
M_"v[188:191], a[188:191], v[104:107], v[188:191]\n" \
E_"v127, v127\n" \
M_"v[220:223], a[188:191], v[116:119], v[220:223]\n" \
C_"v123, v126, v127\n" \
B_"v3, s[80:83], s92 offen offset:3072 lds\n" \
"s_add_u32 m0, s93, 0x1000\n" \
M_"v[252:255], a[188:191], v[120:123], v[252:255]\n" \
R_"a[128:131], v48 offset:8192\n" \
R_"a[144:147], v48 offset:12288\n" \
M_"v[216:219], a[184:187], v[116:119], v[216:219]\n" \
B_"v4, s[80:83], s92 offen offset:0 lds\n" \
M_"v[248:251], a[184:187], v[120:123], v[248:251]\n" \
R_"a[132:135], v48 offset:9216\n" \
R_"a[148:151], v48 offset:13312\n" \
M_"v[212:215], a[180:183], v[116:119], v[212:215]\n" \
B_"v5, s[80:83], s92 offen offset:1024 lds\n" \
M_"v[244:247], a[180:183], v[120:123], v[244:247]\n" \
R_"a[136:139], v48 offset:10240\n" \
R_"a[152:155], v48 offset:14336\n" \
M_"v[208:211], a[176:179], v[116:119], v[208:211]\n" \
B_"v6, s[80:83], s92 offen offset:2048 lds\n" \
M_"v[240:243], a[176:179], v[120:123], v[240:243]\n" \
R_"a[140:143], v48 offset:11264\n" \
R_"a[156:159], v48 offset:15360\n" \
M_"v[204:207], a[172:175], v[116:119], v[204:207]\n" \
B_"v7, s[80:83], s92 offen offset:3072 lds\n" \
M_"v[236:239], a[172:175], v[120:123], v[236:239]\n" \
"v_readlane_b32 s96, v11, 1\n" \
"v_lshrrev_b32_e32 v16, 1, v20\n" \
M_"v[200:203], a[168:171], v[116:119], v[200:203]\n" \
"v_add_f32_e32 v54, v54, v22\n" \
"v_max_f32_e32 v62, v62, v54\n" \
M_"v[232:235], a[168:171], v[120:123], v[232:235]\n" \
"v_rcp_f32_e32 v54, v54\n" \
M_"v[196:199], a[164:167], v[116:119], v[196:199]\n" \
"v_mul_f32_e32 v54, v54, v58\n" \
"v_mul_f32_e32 v128, v54, v128\n" \
M_"v[228:231], a[164:167], v[120:123], v[228:231]\n" \
"v_mul_f32_e32 v129, v54, v129\n" \
"v_mul_f32_e32 v136, v54, v136\n" \
M_"v[192:195], a[160:163], v[116:119], v[192:195]\n" \
"v_mul_f32_e32 v137, v54, v137\n" \
"v_mul_f32_e32 v144, v54, v144\n" \
M_"v[224:227], a[160:163], v[120:123], v[224:227]\n" \
"v_mul_f32_e32 v145, v54, v145\n" \
"v_mul_f32_e32 v152, v54, v152\n" \
"v_smfmac_f32_16x16x64_bf16 a[100:103], a[104:107], v[116:123], v21\n" \
"v_mul_f32_e32 v153, v54, v153\n" \
C_"v128, v128, v129\n" \
"v_mul_f32_e32 v130, v54, v130\n" \
"v_mul_f32_e32 v131, v54, v131\n" \
C_"v136, v136, v137\n" \
"v_mul_f32_e32 v138, v54, v138\n" \
"v_mul_f32_e32 v139, v54, v139\n" \
C_"v144, v144, v145\n" \
"v_mul_f32_e32 v146, v54, v146\n" \
"v_mul_f32_e32 v147, v54, v147\n" \
C_"v152, v152, v153\n" \
"v_mul_f32_e32 v154, v54, v154\n" \
"v_mul_f32_e32 v155, v54, v155\n" \
C_"v129, v130, v131\n" \
"v_mul_f32_e32 v132, v54, v132\n" \
"v_mul_f32_e32 v133, v54, v133\n" \
"v_mul_f32_e32 v134, v54, v134\n" \
"v_mul_f32_e32 v135, v54, v135\n" \
C_"v137, v138, v139\n" \
"v_mul_f32_e32 v140, v54, v140\n" \
"v_mul_f32_e32 v141, v54, v141\n" \
"v_mul_f32_e32 v142, v54, v142\n" \
"v_mul_f32_e32 v143, v54, v143\n" \
C_"v145, v146, v147\n" \
"v_mul_f32_e32 v148, v54, v148\n" \
"v_mul_f32_e32 v149, v54, v149\n" \
"v_mul_f32_e32 v150, v54, v150\n" \
"v_mul_f32_e32 v151, v54, v151\n" \
C_"v153, v154, v155\n" \
"v_mul_f32_e32 v156, v54, v156\n" \
"v_mul_f32_e32 v157, v54, v157\n" \
"v_mul_f32_e32 v158, v54, v158\n" \
"v_mul_f32_e32 v159, v54, v159\n" \
C_"v130, v132, v133\n" \
C_"v131, v134, v135\n" \
"buffer_store_dwordx4 v[128:131], v20, s[56:59], s68 offen offset:0 sc1\n" \
C_"v138, v140, v141\n" \
C_"v139, v142, v143\n" \
C_"v146, v148, v149\n" \
C_"v147, v150, v151\n" \
C_"v154, v156, v157\n" \
C_"v155, v158, v159\n" \
"buffer_store_dwordx4 v[136:139], v20, s[56:59], s68 offen offset:1024 sc1\n" \
"s_add_u32 m0, s93, 0x2000\n" \
"v_add_u32_e32 v49, 0x10000, v48\n" \
"s_xor_b32 s65, s65, 0x8000\n" \
"s_add_u32 s64, s64, 0x10000\n" \
E_"v86, v86\n" \
"buffer_store_dwordx4 v[144:147], v20, s[56:59], s68 offen offset:2048 sc1\n" \
E_"v87, v87\n" \
E_"v84, v84\n" \
E_"v85, v85\n" \
"buffer_store_dwordx4 v[152:155], v20, s[56:59], s68 offen offset:3072 sc1\n" \
E_"v88, v88\n" \
E_"v89, v89\n" \
C_"v87, v86, v87\n" \
E_"v82, v82\n" \
B_"v8, s[80:83], s92 offen offset:0 lds\n" \
E_"v83, v83\n" \
E_"v80, v80\n" \
E_"v81, v81\n" \
E_"v90, v90\n" \
E_"v91, v91\n" \
C_"v86, v84, v85\n" \
C_"v88, v88, v89\n" \
E_"v92, v92\n" \
E_"v93, v93\n" \
E_"v94, v94\n" \
E_"v95, v95\n" \
C_"v85, v82, v83\n" \
C_"v84, v80, v81\n" \
C_"v89, v90, v91\n" \
"v_and_b32_e32 v84, v12, v84\n" \
"v_and_b32_e32 v85, v13, v85\n" \
"v_and_b32_e32 v86, v14, v86\n" \
"s_waitcnt vmcnt(17) lgkmcnt(0)\n" \
"s_barrier\n" \
M_"v[120:123], a[128:131], a[48:51], v[44:47]\n" \
"buffer_load_dwordx2 v[26:27], v16, s[56:59], s96 offen sc1\n" \
M_"v[120:123], a[132:135], a[52:55], v[120:123]\n" \
R_"a[188:191], v49 offset:7168\n" \
R_"a[184:187], v49 offset:6144\n" \
M_"v[120:123], a[136:139], a[56:59], v[120:123]\n" \
"s_xor_b32 m0, s65, 0x12000\n" \
R_"a[180:183], v49 offset:5120\n" \
M_"v[120:123], a[140:143], a[60:63], v[120:123]\n" \
B_"v50, s[8:11], s64 offen offset:0 sc1 lds\n" \
M_"v[124:127], a[156:159], a[60:63], v[44:47]\n" \
R_"a[176:179], v49 offset:4096\n" \
R_"a[172:175], v49 offset:3072\n" \
M_"v[124:127], a[152:155], a[56:59], v[124:127]\n" \
B_"v50, s[8:11], s64 offen offset:1024 sc1 lds\n" \
M_"v[124:127], a[148:151], a[52:55], v[124:127]\n" \
R_"a[168:171], v49 offset:2048\n" \
"v_and_b32_e32 v87, v15, v87\n" \
M_"v[124:127], a[144:147], a[48:51], v[124:127]\n" \
C_"v90, v92, v93\n" \
C_"v91, v94, v95\n" \
M_"v[32:35], a[124:127], a[64:67], 0\n" \
B_"v50, s[8:11], s64 offen offset:2048 sc1 lds\n" \
M_"v[36:39], a[124:127], a[80:83], 0\n" \
R_"a[164:167], v49 offset:1024\n" \
R_"a[160:163], v49 offset:0\n" \
M_"v[32:35], a[224:227], a[68:71], v[32:35]\n" \
R_"a[0:3], v49 offset:8192\n" \
R_"a[4:7], v49 offset:9216\n" \
M_"v[36:39], a[224:227], a[84:87], v[36:39]\n" \
R_"a[8:11], v49 offset:10240\n" \
R_"a[12:15], v49 offset:11264\n" \
M_"v[32:35], a[228:231], a[72:75], v[32:35]\n" \
B_"v50, s[8:11], s64 offen offset:3072 sc1 lds\n" \
M_"v[36:39], a[228:231], a[88:91], v[36:39]\n" \
R_"a[16:19], v49 offset:12288\n" \
R_"a[20:23], v49 offset:13312\n" \
M_"v[32:35], a[232:235], a[76:79], v[32:35]\n" \
R_"a[24:27], v49 offset:14336\n" \
R_"a[28:31], v49 offset:15360\n" \
M_"v[36:39], a[232:235], a[92:95], v[36:39]\n" \
E_"v120, v120\n" \
"v_smfmac_f32_16x16x64_bf16 a[100:103], a[104:107], v[84:91], v21\n" \
"s_mov_b32 s63, s62\n" \
"s_waitcnt lgkmcnt(13)\n" \
M_"v[220:223], a[188:191], v[84:87], v[220:223]\n" \
E_"v121, v121\n" \
M_"v[252:255], a[188:191], v[88:91], v[252:255]\n" \
E_"v122, v122\n" \
M_"v[216:219], a[184:187], v[84:87], v[216:219]\n" \
E_"v123, v123\n" \
M_"v[248:251], a[184:187], v[88:91], v[248:251]\n" \
C_"v120, v120, v121\n" \
C_"v121, v122, v123\n" \
M_"v[212:215], a[180:183], v[84:87], v[212:215]\n" \
E_"v124, v124\n" \
M_"v[244:247], a[180:183], v[88:91], v[244:247]\n" \
"v_and_b32_e32 v120, v12, v120\n" \
"s_waitcnt lgkmcnt(10)\n" \
M_"v[208:211], a[176:179], v[84:87], v[208:211]\n" \
E_"v125, v125\n" \
M_"v[240:243], a[176:179], v[88:91], v[240:243]\n" \
E_"v126, v126\n" \
M_"v[204:207], a[172:175], v[84:87], v[204:207]\n" \
E_"v127, v127\n" \
M_"v[236:239], a[172:175], v[88:91], v[236:239]\n" \
C_"v122, v124, v125\n" \
C_"v123, v126, v127\n" \
M_"v[200:203], a[168:171], v[84:87], v[200:203]\n" \
"v_and_b32_e32 v121, v13, v121\n" \
"v_and_b32_e32 v122, v14, v122\n" \
M_"v[232:235], a[168:171], v[88:91], v[232:235]\n" \
"v_and_b32_e32 v123, v15, v123\n" \
"s_waitcnt lgkmcnt(0)\n" \
M_"v[196:199], a[164:167], v[84:87], v[196:199]\n" \
"v_accvgpr_read_b32 v56, a100\n" \
"v_accvgpr_read_b32 v24, a101\n" \
M_"v[228:231], a[164:167], v[88:91], v[228:231]\n" \
"v_readlane_b32 s97, v11, 39\n" \
"v_add_u32_e32 v17, 0x49000, v20\n" \
M_"v[192:195], a[160:163], v[84:87], v[192:195]\n" \
"v_readlane_b32 s98, v11, 40\n" \
"v_add_u32_e32 v48, s16, v20\n" \
M_"v[224:227], a[160:163], v[88:91], v[224:227]\n" \
R_"a[128:131], v48 offset:0\n" \
R_"a[144:147], v48 offset:4096\n" \
M_"a[100:103], a[116:119], v[120:123], a[100:103]\n" \
R_"a[132:135], v48 offset:1024\n" \
R_"a[148:151], v48 offset:5120\n" \
M_"v[224:227], a[0:3], v[120:123], v[224:227]\n" \
R_"a[136:139], v48 offset:2048\n" \
R_"a[152:155], v48 offset:6144\n" \
M_"v[228:231], a[4:7], v[120:123], v[228:231]\n" \
R_"a[140:143], v48 offset:3072\n" \
R_"a[156:159], v48 offset:7168\n" \
M_"v[232:235], a[8:11], v[120:123], v[232:235]\n" \
"v_accvgpr_read_b32 v55, a98\n" \
"v_accvgpr_read_b32 v23, a99\n" \
M_"a[96:99], a[120:123], a[120:123], 0\n" \
"v_add_f32_e32 v55, v55, v23\n" \
"v_max_f32_e32 v62, v62, v55\n" \
M_"v[236:239], a[12:15], v[120:123], v[236:239]\n" \
"v_rcp_f32_e32 v55, v55\n" \
M_"v[240:243], a[16:19], v[120:123], v[240:243]\n" \
"v_mul_f32_e32 v55, v55, v59\n" \
"v_mul_f32_e32 v160, v55, v160\n" \
M_"v[244:247], a[20:23], v[120:123], v[244:247]\n" \
"v_mul_f32_e32 v161, v55, v161\n" \
"v_mul_f32_e32 v168, v55, v168\n" \
M_"v[248:251], a[24:27], v[120:123], v[248:251]\n" \
"v_mul_f32_e32 v169, v55, v169\n" \
"v_mul_f32_e32 v176, v55, v176\n" \
M_"v[252:255], a[28:31], v[120:123], v[252:255]\n" \
"v_mul_f32_e32 v177, v55, v177\n" \
"v_mul_f32_e32 v184, v55, v184\n" \
"v_mul_f32_e32 v185, v55, v185\n" \
C_"v160, v160, v161\n" \
"v_mul_f32_e32 v162, v55, v162\n" \
"v_mul_f32_e32 v163, v55, v163\n" \
C_"v168, v168, v169\n" \
"v_mul_f32_e32 v170, v55, v170\n" \
"v_mul_f32_e32 v171, v55, v171\n" \
"s_waitcnt vmcnt(4)\n" \
"v_cmp_ne_u64_e32 vcc, s[62:63], v[26:27]\n" \
C_"v176, v176, v177\n" \
"v_mul_f32_e32 v178, v55, v178\n" \
"v_mul_f32_e32 v179, v55, v179\n" \
C_"v184, v184, v185\n" \
"v_mul_f32_e32 v186, v55, v186\n" \
"s_cbranch_vccnz L_sos_%=\n" \
"L_ros_%=:\n" \
B_"a[124:127], v20, s[56:59], s97 offen offset:0 sc1\n" \
"v_mul_f32_e32 v187, v55, v187\n" \
C_"v161, v162, v163\n" \
"v_mul_f32_e32 v164, v55, v164\n" \
"v_mul_f32_e32 v165, v55, v165\n" \
"v_mul_f32_e32 v166, v55, v166\n" \
"v_mul_f32_e32 v167, v55, v167\n" \
B_"a[228:231], v17, s[56:59], s97 offen offset:0 sc1\n" \
C_"v169, v170, v171\n" \
"v_mul_f32_e32 v172, v55, v172\n" \
"v_mul_f32_e32 v173, v55, v173\n" \
"v_mul_f32_e32 v174, v55, v174\n" \
"v_mul_f32_e32 v175, v55, v175\n" \
C_"v177, v178, v179\n" \
B_"a[236:239], v20, s[56:59], s98 offen offset:0 sc1\n" \
"v_mul_f32_e32 v180, v55, v180\n" \
"v_mul_f32_e32 v181, v55, v181\n" \
"v_mul_f32_e32 v182, v55, v182\n" \
"v_mul_f32_e32 v183, v55, v183\n" \
C_"v185, v186, v187\n" \
"v_mul_f32_e32 v188, v55, v188\n" \
B_"a[244:247], v17, s[56:59], s98 offen offset:0 sc1\n" \
"v_mul_f32_e32 v189, v55, v189\n" \
"v_mul_f32_e32 v190, v55, v190\n" \
"v_mul_f32_e32 v191, v55, v191\n" \
C_"v162, v164, v165\n" \
C_"v163, v166, v167\n" \
C_"v170, v172, v173\n" \
"buffer_store_dwordx4 v[160:163], v20, s[56:59], s69 offen offset:0 sc1\n" \
C_"v171, v174, v175\n" \
C_"v178, v180, v181\n" \
C_"v179, v182, v183\n" \
C_"v186, v188, v189\n" \
C_"v187, v190, v191\n" \
"v_readlane_b32 s93, v11, 23\n" \
"buffer_store_dwordx4 v[168:171], v20, s[56:59], s69 offen offset:1024 sc1\n" \
"v_readlane_b32 s84, v11, 15\n" \
"v_readlane_b32 s85, v11, 16\n" \
"v_readlane_b32 s86, v11, 17\n" \
"s_mov_b32 s87, 0x20000\n" \
"v_readlane_b32 s92, v11, 18\n" \
"v_readlane_b32 s91, v11, 24\n" \
"buffer_store_dwordx4 v[176:179], v20, s[56:59], s69 offen offset:2048 sc1\n" \
M_"a[112:115], a[120:123], v[128:131], a[112:115]\n" \
M_"a[112:115], a[120:123], v[136:139], a[112:115]\n" \
"buffer_store_dwordx4 v[184:187], v20, s[56:59], s69 offen offset:3072 sc1\n" \
"v_mfma_f32_32x32x16_bf16 v[128:143], a[120:123], a[120:123], 0\n" \
M_"a[112:115], a[120:123], v[144:147], a[112:115]\n" \
B_"a[224:227], v20, s[56:59], s97 offen offset:1024 sc1\n" \
M_"a[112:115], a[120:123], v[152:155], a[112:115]\n" \
"v_mfma_f32_32x32x16_bf16 v[144:159], a[120:123], a[120:123], 0\n" \
B_"a[232:235], v17, s[56:59], s97 offen offset:1024 sc1\n" \
"v_add_f32_e32 v56, v56, v24\n" \
M_"a[112:115], a[120:123], v[160:163], a[112:115]\n" \
B_"a[240:243], v20, s[56:59], s98 offen offset:1024 sc1\n" \
"v_max_f32_e32 v62, v62, v56\n" \
"v_rcp_f32_e32 v56, v56\n" \
M_"a[112:115], a[120:123], v[168:171], a[112:115]\n" \
"v_mfma_f32_32x32x16_bf16 v[160:175], a[120:123], a[120:123], 0\n" \
B_"a[248:251], v17, s[56:59], s98 offen offset:1024 sc1\n" \
"v_mul_f32_e32 v56, v56, v60\n" \
M_"a[112:115], a[120:123], v[176:179], a[112:115]\n" \
"v_mul_f32_e32 v192, v56, v192\n" \
"v_mul_f32_e32 v193, v56, v193\n" \
"s_waitcnt vmcnt(17) lgkmcnt(0)\n" \
"s_barrier\n" \
M_"v[64:67], a[128:131], a[64:67], v[32:35]\n" \
"s_add_u32 m0, s93, 0x0\n" \
C_"v192, v192, v193\n" \
M_"v[64:67], a[132:135], a[68:71], v[64:67]\n" \
B_"v0, s[84:87], s92 offen offset:0 lds\n" \
M_"v[64:67], a[136:139], a[72:75], v[64:67]\n" \
"v_mul_f32_e32 v194, v56, v194\n" \
"v_mul_f32_e32 v195, v56, v195\n" \
M_"v[64:67], a[140:143], a[76:79], v[64:67]\n" \
B_"v1, s[84:87], s92 offen offset:1024 lds\n" \
M_"v[68:71], a[156:159], a[76:79], v[32:35]\n" \
M_"a[112:115], a[120:123], v[184:187], a[112:115]\n" \
"v_mfma_f32_32x32x16_bf16 v[176:191], a[120:123], a[120:123], 0\n" \
B_"v2, s[84:87], s92 offen offset:2048 lds\n" \
C_"v193, v194, v195\n" \
"v_mul_f32_e32 v196, v56, v196\n" \
"v_mul_f32_e32 v197, v56, v197\n" \
"v_mul_f32_e32 v198, v56, v198\n" \
M_"v[68:71], a[152:155], a[72:75], v[68:71]\n" \
B_"v3, s[84:87], s92 offen offset:3072 lds\n" \
M_"v[68:71], a[148:151], a[68:71], v[68:71]\n" \
"s_add_u32 m0, s91, 0x1000\n" \
"v_mul_f32_e32 v199, v56, v199\n" \
M_"v[68:71], a[144:147], a[64:67], v[68:71]\n" \
B_"v4, s[84:87], s92 offen offset:0 lds\n" \
M_"v[76:79], a[144:147], a[80:83], v[36:39]\n" \
R_"a[144:147], v48 offset:12288\n" \
"v_mul_f32_e32 v200, v56, v200\n" \
M_"v[76:79], a[148:151], a[84:87], v[76:79]\n" \
B_"v5, s[84:87], s92 offen offset:1024 lds\n" \
M_"v[76:79], a[152:155], a[88:91], v[76:79]\n" \
R_"a[148:151], v48 offset:13312\n" \
R_"a[152:155], v48 offset:14336\n" \
M_"v[76:79], a[156:159], a[92:95], v[76:79]\n" \
B_"v6, s[84:87], s92 offen offset:2048 lds\n" \
M_"v[72:75], a[140:143], a[92:95], v[36:39]\n" \
R_"a[140:143], v48 offset:11264\n" \
R_"a[156:159], v48 offset:15360\n" \
M_"v[72:75], a[136:139], a[88:91], v[72:75]\n" \
B_"v7, s[84:87], s92 offen offset:3072 lds\n" \
M_"v[72:75], a[132:135], a[84:87], v[72:75]\n" \
R_"a[132:135], v48 offset:9216\n" \
R_"a[136:139], v48 offset:10240\n" \
M_"v[72:75], a[128:131], a[80:83], v[72:75]\n" \
R_"a[128:131], v48 offset:8192\n" \
"v_mul_f32_e32 v201, v56, v201\n" \
"v_accvgpr_read_b32 v57, a102\n" \
"v_accvgpr_read_b32 v25, a103\n" \
M_"a[100:103], a[120:123], a[120:123], 0\n" \
C_"v194, v196, v197\n" \
C_"v195, v198, v199\n" \
"buffer_store_dwordx4 v[192:195], v20, s[56:59], s70 offen offset:0 sc1\n" \
C_"v200, v200, v201\n" \
"v_mul_f32_e32 v202, v56, v202\n" \
"v_mul_f32_e32 v203, v56, v203\n" \
"v_add_f32_e32 v57, v57, v25\n" \
M_"a[112:115], a[120:123], v[192:195], a[112:115]\n" \
C_"v201, v202, v203\n" \
"v_mul_f32_e32 v204, v56, v204\n" \
"v_mul_f32_e32 v205, v56, v205\n" \
"v_mul_f32_e32 v206, v56, v206\n" \
"v_mul_f32_e32 v207, v56, v207\n" \
"v_mul_f32_e32 v208, v56, v208\n" \
"v_mul_f32_e32 v209, v56, v209\n" \
"v_max_f32_e32 v62, v62, v57\n" \
C_"v202, v204, v205\n" \
C_"v203, v206, v207\n" \
"buffer_store_dwordx4 v[200:203], v20, s[56:59], s70 offen offset:1024 sc1\n" \
".fill (L_ab + 28672 - .) / 4, 4, 0xBF800000\n" \
"L_ap7:\n" \
"s_cbranch_execz L_ap0\n" \
C_"v208, v208, v209\n" \
"v_mul_f32_e32 v210, v56, v210\n" \
"v_mul_f32_e32 v211, v56, v211\n" \
"v_rcp_f32_e32 v57, v57\n" \
M_"a[112:115], a[120:123], v[200:203], a[112:115]\n" \
"v_mfma_f32_32x32x16_bf16 v[192:207], a[120:123], a[120:123], 0\n" \
C_"v209, v210, v211\n" \
"v_mul_f32_e32 v212, v56, v212\n" \
"v_mul_f32_e32 v213, v56, v213\n" \
"v_mul_f32_e32 v214, v56, v214\n" \
"v_mul_f32_e32 v215, v56, v215\n" \
"v_mul_f32_e32 v216, v56, v216\n" \
"v_mul_f32_e32 v217, v56, v217\n" \
C_"v210, v212, v213\n" \
C_"v211, v214, v215\n" \
"buffer_store_dwordx4 v[208:211], v20, s[56:59], s70 offen offset:2048 sc1\n" \
C_"v216, v216, v217\n" \
"v_mul_f32_e32 v218, v56, v218\n" \
"v_mul_f32_e32 v219, v56, v219\n" \
"v_mul_f32_e32 v57, v57, v61\n" \
M_"a[112:115], a[120:123], v[208:211], a[112:115]\n" \
C_"v217, v218, v219\n" \
"v_mul_f32_e32 v220, v56, v220\n" \
"v_mul_f32_e32 v221, v56, v221\n" \
"v_mul_f32_e32 v222, v56, v222\n" \
"v_mul_f32_e32 v223, v56, v223\n" \
"v_mul_f32_e32 v224, v57, v224\n" \
"v_mul_f32_e32 v225, v57, v225\n" \
C_"v218, v220, v221\n" \
C_"v219, v222, v223\n" \
"buffer_store_dwordx4 v[216:219], v20, s[56:59], s70 offen offset:3072 sc1\n" \
C_"v224, v224, v225\n" \
"v_mul_f32_e32 v226, v57, v226\n" \
"v_mul_f32_e32 v227, v57, v227\n" \
M_"a[112:115], a[120:123], v[216:219], a[112:115]\n" \
"v_mfma_f32_32x32x16_bf16 v[208:223], a[120:123], a[120:123], 0\n" \
C_"v225, v226, v227\n" \
"v_mul_f32_e32 v228, v57, v228\n" \
"v_mul_f32_e32 v229, v57, v229\n" \
"v_mul_f32_e32 v230, v57, v230\n" \
"v_mul_f32_e32 v231, v57, v231\n" \
"v_mul_f32_e32 v232, v57, v232\n" \
"v_mul_f32_e32 v233, v57, v233\n" \
C_"v226, v228, v229\n" \
C_"v227, v230, v231\n" \
"buffer_store_dwordx4 v[224:227], v20, s[56:59], s71 offen offset:0 sc1\n" \
C_"v232, v232, v233\n" \
"v_mul_f32_e32 v234, v57, v234\n" \
"v_mul_f32_e32 v235, v57, v235\n" \
M_"a[112:115], a[120:123], v[224:227], a[112:115]\n" \
C_"v233, v234, v235\n" \
"v_mul_f32_e32 v236, v57, v236\n" \
"v_mul_f32_e32 v237, v57, v237\n" \
"v_mul_f32_e32 v238, v57, v238\n" \
"v_mul_f32_e32 v239, v57, v239\n" \
"v_mul_f32_e32 v240, v57, v240\n" \
"v_mul_f32_e32 v241, v57, v241\n" \
C_"v234, v236, v237\n" \
C_"v235, v238, v239\n" \
"buffer_store_dwordx4 v[232:235], v20, s[56:59], s71 offen offset:1024 sc1\n" \
C_"v240, v240, v241\n" \
"v_mul_f32_e32 v242, v57, v242\n" \
"v_mul_f32_e32 v243, v57, v243\n" \
M_"a[112:115], a[120:123], v[232:235], a[112:115]\n" \
"v_mfma_f32_32x32x16_bf16 v[224:239], a[120:123], a[120:123], 0\n" \
C_"v241, v242, v243\n" \
"v_mul_f32_e32 v244, v57, v244\n" \
"v_mul_f32_e32 v245, v57, v245\n" \
"v_mul_f32_e32 v246, v57, v246\n" \
"v_mul_f32_e32 v247, v57, v247\n" \
"v_mul_f32_e32 v248, v57, v248\n" \
"v_mul_f32_e32 v249, v57, v249\n" \
C_"v242, v244, v245\n" \
C_"v243, v246, v247\n" \
"buffer_store_dwordx4 v[240:243], v20, s[56:59], s71 offen offset:2048 sc1\n" \
C_"v248, v248, v249\n" \
"v_mul_f32_e32 v250, v57, v250\n" \
"v_mul_f32_e32 v251, v57, v251\n" \
M_"a[112:115], a[120:123], v[240:243], a[112:115]\n" \
C_"v249, v250, v251\n" \
"v_mul_f32_e32 v252, v57, v252\n" \
"v_mul_f32_e32 v253, v57, v253\n" \
"v_mul_f32_e32 v254, v57, v254\n" \
"v_mul_f32_e32 v255, v57, v255\n" \
C_"v250, v252, v253\n" \
C_"v251, v254, v255\n" \
"buffer_store_dwordx4 v[248:251], v20, s[56:59], s71 offen offset:3072 sc1\n" \
"s_add_u32 m0, s91, 0x2000\n" \
M_"a[112:115], a[120:123], v[248:251], a[112:115]\n" \
"v_mfma_f32_32x32x16_bf16 v[240:255], a[120:123], a[120:123], 0\n" \
"v_add_u32_e32 v53, 0x20000, v63\n" \
B_"v8, s[84:87], s92 offen offset:0 lds\n" \
E_"v70, v70\n" \
E_"v71, v71\n" \
E_"v72, v72\n" \
E_"v73, v73\n" \
"s_add_u32 s64, s64, 0x10000\n" \
"v_accvgpr_read_b32 v63, a112\n" \
"v_cmp_u_f32_e32 vcc, v63, v63\n" \
"v_cmp_lt_f32_e64 s[72:73], s21, v62\n" \
"v_readlane_b32 s95, v11, 3\n" \
E_"v68, v68\n" \
E_"v69, v69\n" \
E_"v74, v74\n" \
E_"v75, v75\n" \
"s_or_b64 vcc, s[72:73], vcc\n" \
"s_cselect_b32 s95, 0x40000000, s95\n" \
"s_cselect_b32 s94, 0x7f800000, 0\n" \
C_"v71, v70, v71\n" \
E_"v66, v66\n" \
E_"v67, v67\n" \
E_"v64, v64\n" \
E_"v65, v65\n" \
C_"v72, v72, v73\n" \
E_"v76, v76\n" \
E_"v77, v77\n" \
E_"v78, v78\n" \
E_"v79, v79\n" \
C_"v70, v68, v69\n" \
"s_waitcnt vmcnt(17) lgkmcnt(0)\n" \
"s_barrier\n" \
C_"v73, v74, v75\n" \
"v_add_u32_e32 v49, 0x10000, v48\n" \
"s_xor_b32 s65, s65, 0x8000\n" \
C_"v69, v66, v67\n" \
C_"v68, v64, v65\n" \
C_"v74, v76, v77\n" \
C_"v75, v78, v79\n" \
"v_xor_b32_e32 v48, 0x8000, v48\n" \
"s_sub_u32 s66, s19, 2\n" \
"s_branch L_f1_%=\n" \
".fill (L_ab + 32768 - .) / 4, 4, 0xBF800000\n" \
".fill (L_ab + 32768 - .) / 4, 4, 0xBF800000\n" \
"L_ap8:\n" \
"s_setpc_b64 s[88:89]\n" \
"L_out_%=:\n" \
"v_bfe_u32 v38, v20, 2, 4\n" \
"v_readlane_b32 s64, v11, 2\n" \
"v_add_u32_e32 v32, 0x49000, v20\n" \
"v_add_u32_e32 v34, 0x20000, v10\n" \
X_"a[0:1], v34 offset:0\n" \
X_"a[2:3], v34 offset:4608\n" \
X_"a[4:5], v34 offset:9216\n" \
"buffer_load_dword v26, v38, s[56:59], s64 offen sc1\n" \
X_"a[6:7], v34 offset:13824\n" \
X_"a[8:9], v34 offset:32\n" \
X_"a[10:11], v34 offset:4640\n" \
X_"a[12:13], v34 offset:9248\n" \
"buffer_store_dwordx4 v[64:67], v20, s[56:59], s61 offen offset:0 sc1\n" \
X_"a[14:15], v34 offset:13856\n" \
"v_mov_b32_e32 v39, s62\n" \
"v_readlane_b32 s70, v11, 5\n" \
"v_readlane_b32 s71, v11, 4\n" \
"buffer_store_dwordx4 v[68:71], v20, s[56:59], s61 offen offset:1024 sc1\n" \
X_"a[16:17], v34 offset:64\n" \
X_"a[18:19], v34 offset:4672\n" \
X_"a[20:21], v34 offset:9280\n" \
X_"a[22:23], v34 offset:13888\n" \
"buffer_store_dwordx4 v[72:75], v20, s[56:59], s61 offen offset:2048 sc1\n" \
X_"a[24:25], v34 offset:96\n" \
X_"a[26:27], v34 offset:4704\n" \
X_"a[28:29], v34 offset:9312\n" \
X_"a[30:31], v34 offset:13920\n" \
"buffer_store_dwordx4 v[76:79], v20, s[56:59], s61 offen offset:3072 sc1\n" \
X_"a[96:97], v34 offset:128\n" \
X_"a[98:99], v34 offset:4736\n" \
X_"a[100:101], v34 offset:9344\n" \
X_"a[102:103], v34 offset:13952\n" \
"buffer_store_dwordx4 v[80:83], v32, s[56:59], s61 offen offset:0 sc1\n" \
X_"a[104:105], v34 offset:160\n" \
X_"a[106:107], v34 offset:4768\n" \
X_"a[108:109], v34 offset:9376\n" \
X_"a[110:111], v34 offset:13984\n" \
"buffer_store_dwordx4 v[84:87], v32, s[56:59], s61 offen offset:1024 sc1\n" \
X_"a[112:113], v34 offset:192\n" \
"s_waitcnt vmcnt(16) lgkmcnt(15)\n" \
M_"v[112:115], a[0:3], a[192:195], 0\n" \
"buffer_store_dwordx4 v[96:99], v32, s[56:59], s61 offen offset:2048 sc1\n" \
M_"v[112:115], a[4:7], a[196:199], v[112:115]\n" \
X_"a[114:115], v34 offset:4800\n" \
X_"a[116:117], v34 offset:9408\n" \
"buffer_store_dwordx4 v[100:103], v32, s[56:59], s61 offen offset:3072 sc1\n" \
M_"v[116:119], a[0:3], a[208:211], 0\n" \
X_"a[118:119], v34 offset:14016\n" \
X_"a[120:121], v34 offset:224\n" \
M_"v[116:119], a[4:7], a[212:215], v[116:119]\n" \
X_"a[122:123], v34 offset:4832\n" \
"v_lshrrev_b32_e32 v44, 9, v20\n" \
M_"v[184:187], a[0:3], v[64:67], 0\n" \
"v_mul_u32_u24_e32 v44, 0xb40, v44\n" \
"v_add_u32_e32 v35, v44, v10\n" \
M_"v[184:187], a[4:7], v[68:71], v[184:187]\n" \
"v_readlane_b32 s65, v11, 33\n" \
"v_readlane_b32 s67, v11, 26\n" \
M_"v[188:191], a[0:3], v[80:83], 0\n" \
X_"a[0:1], v34 offset:9440\n" \
X_"a[2:3], v34 offset:14048\n" \
M_"v[188:191], a[4:7], v[84:87], v[188:191]\n" \
X_"a[4:5], v34 offset:256\n" \
X_"a[6:7], v34 offset:4864\n" \
M_"v[196:199], a[8:11], v[80:83], 0\n" \
"v_readlane_b32 s80, v11, 11\n" \
"v_readlane_b32 s81, v11, 12\n" \
M_"v[196:199], a[12:15], v[84:87], v[196:199]\n" \
"s_mov_b32 s82, 0x47fca0\n" \
"s_mov_b32 s83, 0x20000\n" \
M_"v[192:195], a[8:11], v[64:67], 0\n" \
"v_readlane_b32 s66, v11, 31\n" \
"s_add_u32 m0, s67, 0x0\n" \
M_"v[192:195], a[12:15], v[68:71], v[192:195]\n" \
"v_readlane_b32 s92, v11, 27\n" \
"v_readlane_b32 s73, v11, 32\n" \
M_"v[124:127], a[8:11], a[208:211], 0\n" \
B_"v0, s[80:83], s66 offen offset:0 lds\n" \
M_"v[124:127], a[12:15], a[212:215], v[124:127]\n" \
"v_readlane_b32 s94, v11, 28\n" \
"v_readlane_b32 s84, v11, 9\n" \
M_"v[120:123], a[8:11], a[192:195], 0\n" \
X_"a[8:9], v34 offset:9472\n" \
X_"a[10:11], v34 offset:14080\n" \
M_"v[120:123], a[12:15], a[196:199], v[120:123]\n" \
X_"a[12:13], v34 offset:18432\n" \
X_"a[14:15], v34 offset:23040\n" \
M_"v[128:131], a[16:19], a[192:195], 0\n" \
"v_readlane_b32 s85, v11, 10\n" \
"s_waitcnt lgkmcnt(9)\n" \
M_"v[128:131], a[20:23], a[196:199], v[128:131]\n" \
B_"v1, s[80:83], s66 offen offset:1024 lds\n" \
M_"v[132:135], a[16:19], a[208:211], 0\n" \
"s_mov_b32 s86, 0xa1ca0\n" \
"s_mov_b32 s87, 0x20000\n" \
M_"v[132:135], a[20:23], a[212:215], v[132:135]\n" \
"v_add_u32_e32 v33, 0x10000, v10\n" \
"v_readlane_b32 s68, v11, 34\n" \
M_"v[200:203], a[16:19], v[64:67], 0\n" \
"v_readlane_b32 s69, v11, 35\n" \
"v_readlane_b32 s72, v11, 37\n" \
M_"v[200:203], a[20:23], v[68:71], v[200:203]\n" \
"v_readlane_b32 s93, v11, 38\n" \
"v_readlane_b32 s63, v11, 44\n" \
M_"v[204:207], a[16:19], v[80:83], 0\n" \
X_"a[16:17], v34 offset:18464\n" \
X_"a[18:19], v34 offset:23072\n" \
M_"v[204:207], a[20:23], v[84:87], v[204:207]\n" \
X_"a[20:21], v34 offset:18496\n" \
X_"a[22:23], v34 offset:23104\n" \
M_"v[212:215], a[24:27], v[80:83], 0\n" \
B_"v2, s[80:83], s66 offen offset:2048 lds\n" \
M_"v[212:215], a[28:31], v[84:87], v[212:215]\n" \
"v_bfe_u32 v40, v20, 2, 7\n" \
"s_add_u32 s74, s63, 0x3800\n" \
M_"v[208:211], a[24:27], v[64:67], 0\n" \
"v_lshlrev_b32_e32 v41, 12, v20\n" \
"s_add_u32 s12, s63, 0x0\n" \
M_"v[208:211], a[28:31], v[68:71], v[208:211]\n" \
"v_readlane_b32 s95, v11, 41\n" \
"v_readlane_b32 s96, v11, 42\n" \
M_"v[140:143], a[24:27], a[208:211], 0\n" \
"s_add_u32 s13, s63, 0x1000\n" \
"v_readlane_b32 s97, v11, 43\n" \
M_"v[140:143], a[28:31], a[212:215], v[140:143]\n" \
B_"v3, s[80:83], s66 offen offset:3072 lds\n" \
M_"v[136:139], a[24:27], a[192:195], 0\n" \
X_"a[24:25], v34 offset:18528\n" \
X_"a[26:27], v34 offset:23136\n" \
M_"v[136:139], a[28:31], a[196:199], v[136:139]\n" \
X_"a[28:29], v34 offset:18560\n" \
X_"a[30:31], v34 offset:23168\n" \
M_"v[144:147], a[96:99], a[192:195], 0\n" \
"s_add_u32 m0, s67, 0x1000\n" \
"s_add_u32 s14, s63, 0x2000\n" \
M_"v[144:147], a[100:103], a[196:199], v[144:147]\n" \
"s_add_u32 s15, s63, 0x3000\n" \
"v_readlane_b32 s100, v11, 7\n" \
M_"v[148:151], a[96:99], a[208:211], 0\n" \
B_"v4, s[80:83], s66 offen offset:0 lds\n" \
M_"v[148:151], a[100:103], a[212:215], v[148:151]\n" \
"v_readlane_b32 s16, v11, 45\n" \
"v_bfe_u32 v44, v20, 4, 4\n" \
M_"v[216:219], a[96:99], v[64:67], 0\n" \
"s_add_u32 s17, s16, 0x1000\n" \
"s_add_u32 s18, s16, 0x2000\n" \
M_"v[216:219], a[100:103], v[68:71], v[216:219]\n" \
"s_add_u32 s19, s16, 0x3000\n" \
"v_bfe_u32 v45, v20, 8, 2\n" \
M_"v[220:223], a[96:99], v[80:83], 0\n" \
X_"a[96:97], v34 offset:18592\n" \
X_"a[98:99], v34 offset:23200\n" \
M_"v[220:223], a[100:103], v[84:87], v[220:223]\n" \
X_"a[100:101], v34 offset:18624\n" \
X_"a[102:103], v34 offset:23232\n" \
M_"v[228:231], a[104:107], v[80:83], 0\n" \
B_"v5, s[80:83], s66 offen offset:1024 lds\n" \
M_"v[228:231], a[108:111], v[84:87], v[228:231]\n" \
"v_mul_u32_u24_e32 v42, 0x480, v44\n" \
"v_lshl_add_u32 v42, v45, 3, v42\n" \
M_"v[224:227], a[104:107], v[64:67], 0\n" \
"s_waitcnt vmcnt(14)\n" \
"v_cmp_ne_u32_e32 vcc, s62, v26\n" \
M_"v[224:227], a[108:111], v[68:71], v[224:227]\n" \
"v_lshrrev_b32_e32 v44, 1, v44\n" \
"v_lshlrev_b32_e32 v45, 1, v45\n" \
M_"v[156:159], a[104:107], a[208:211], 0\n" \
"v_sub_u32_e32 v44, v44, v45\n" \
"s_cbranch_vccnz L_sa_%=\n" \
"L_ra_%=:\n" \
M_"v[156:159], a[108:111], a[212:215], v[156:159]\n" \
B_"a[64:67], v20, s[56:59], s65 offen offset:0 sc1\n" \
M_"v[152:155], a[104:107], a[192:195], 0\n" \
X_"a[104:105], v34 offset:18656\n" \
X_"a[106:107], v34 offset:23264\n" \
M_"v[152:155], a[108:111], a[196:199], v[152:155]\n" \
X_"a[108:109], v34 offset:18688\n" \
X_"a[110:111], v34 offset:23296\n" \
M_"v[160:163], a[112:115], a[192:195], 0\n" \
"v_readlane_b32 s64, v11, 36\n" \
"v_and_b32_e32 v45, 16, v20\n" \
M_"v[160:163], a[116:119], a[196:199], v[160:163]\n" \
"v_mov_b32_e32 v46, 0x3f80\n" \
"v_lshlrev_b32_e32 v45, v45, v46\n" \
M_"v[164:167], a[112:115], a[208:211], 0\n" \
B_"a[68:71], v20, s[56:59], s65 offen offset:1024 sc1\n" \
M_"v[164:167], a[116:119], a[212:215], v[164:167]\n" \
"v_mov_b32_e32 v52, 0\n" \
"v_mov_b32_e32 v53, 0\n" \
M_"v[232:235], a[112:115], v[64:67], 0\n" \
"v_mov_b32_e32 v48, 0\n" \
"v_mov_b32_e32 v49, 0\n" \
M_"v[232:235], a[116:119], v[68:71], v[232:235]\n" \
"v_add_u32_e32 v43, 0x9000, v42\n" \
"v_readlane_b32 s88, v11, 13\n" \
M_"v[236:239], a[112:115], v[80:83], 0\n" \
X_"a[112:113], v10 offset:32768\n" \
X_"a[114:115], v10 offset:37376\n" \
M_"v[236:239], a[116:119], v[84:87], v[236:239]\n" \
X_"a[116:117], v10 offset:32800\n" \
"s_waitcnt lgkmcnt(7)\n" \
M_"v[244:247], a[120:123], v[80:83], 0\n" \
X_"a[118:119], v10 offset:37408\n" \
"v_readlane_b32 s89, v11, 14\n" \
M_"v[244:247], a[0:3], v[84:87], v[244:247]\n" \
B_"a[80:83], v32, s[56:59], s65 offen offset:0 sc1\n" \
M_"v[240:243], a[120:123], v[64:67], 0\n" \
"s_mov_b32 s90, 0x240000\n" \
"s_mov_b32 s91, 0x20000\n" \
M_"v[240:243], a[0:3], v[68:71], v[240:243]\n" \
"v_mov_b32_e32 v46, s62\n" \
M_"v[172:175], a[120:123], a[208:211], 0\n" \
M_"v[172:175], a[0:3], a[212:215], v[172:175]\n" \
M_"v[168:171], a[120:123], a[192:195], 0\n" \
X_"a[120:121], v10 offset:32832\n" \
X_"a[122:123], v10 offset:37440\n" \
M_"v[168:171], a[0:3], a[196:199], v[168:171]\n" \
X_"a[0:1], v10 offset:32864\n" \
X_"a[2:3], v10 offset:37472\n" \
M_"v[176:179], a[4:7], a[192:195], 0\n" \
B_"a[84:87], v32, s[56:59], s65 offen offset:1024 sc1\n" \
M_"v[176:179], a[8:11], a[196:199], v[176:179]\n" \
M_"v[180:183], a[4:7], a[208:211], 0\n" \
M_"v[180:183], a[8:11], a[212:215], v[180:183]\n" \
M_"v[248:251], a[4:7], v[64:67], 0\n" \
M_"v[248:251], a[8:11], v[68:71], v[248:251]\n" \
B_"a[72:75], v20, s[56:59], s65 offen offset:2048 sc1\n" \
M_"v[252:255], a[4:7], v[80:83], 0\n" \
X_"a[4:5], v10 offset:32896\n" \
X_"a[6:7], v10 offset:37504\n" \
M_"v[252:255], a[8:11], v[84:87], v[252:255]\n" \
X_"a[8:9], v10 offset:32928\n" \
X_"a[10:11], v10 offset:37536\n" \
M_"v[112:115], a[12:15], a[200:203], v[112:115]\n" \
M_"v[116:119], a[12:15], a[216:219], v[116:119]\n" \
M_"v[184:187], a[12:15], v[72:75], v[184:187]\n" \
B_"a[76:79], v20, s[56:59], s65 offen offset:3072 sc1\n" \
M_"v[188:191], a[12:15], v[96:99], v[188:191]\n" \
X_"a[12:13], v10 offset:32960\n" \
X_"a[14:15], v10 offset:37568\n" \
M_"v[196:199], a[16:19], v[96:99], v[196:199]\n" \
M_"v[192:195], a[16:19], v[72:75], v[192:195]\n" \
M_"v[124:127], a[16:19], a[216:219], v[124:127]\n" \
M_"v[120:123], a[16:19], a[200:203], v[120:123]\n" \
X_"a[16:17], v10 offset:32992\n" \
X_"a[18:19], v10 offset:37600\n" \
M_"v[128:131], a[20:23], a[200:203], v[128:131]\n" \
B_"a[88:91], v32, s[56:59], s65 offen offset:2048 sc1\n" \
M_"v[132:135], a[20:23], a[216:219], v[132:135]\n" \
M_"v[200:203], a[20:23], v[72:75], v[200:203]\n" \
M_"v[204:207], a[20:23], v[96:99], v[204:207]\n" \
X_"a[20:21], v10 offset:33024\n" \
X_"a[22:23], v10 offset:37632\n" \
"s_waitcnt lgkmcnt(10)\n" \
"s_barrier\n" \
M_"v[212:215], a[24:27], v[96:99], v[212:215]\n" \
B_"a[92:95], v32, s[56:59], s65 offen offset:3072 sc1\n" \
M_"v[208:211], a[24:27], v[72:75], v[208:211]\n" \
"v_readlane_b32 s65, v11, 49\n" \
M_"v[140:143], a[24:27], a[216:219], v[140:143]\n" \
M_"v[136:139], a[24:27], a[200:203], v[136:139]\n" \
X_"a[24:25], v35 offset:41984\n" \
X_"a[26:27], v35 offset:44288\n" \
M_"v[144:147], a[28:31], a[200:203], v[144:147]\n" \
M_"v[148:151], a[28:31], a[216:219], v[148:151]\n" \
B_"v6, s[80:83], s66 offen offset:2048 lds\n" \
M_"v[216:219], a[28:31], v[72:75], v[216:219]\n" \
M_"v[220:223], a[28:31], v[96:99], v[220:223]\n" \
X_"a[28:29], v35 offset:52352\n" \
X_"a[30:31], v35 offset:54656\n" \
M_"v[228:231], a[96:99], v[96:99], v[228:231]\n" \
M_"v[224:227], a[96:99], v[72:75], v[224:227]\n" \
M_"v[156:159], a[96:99], a[216:219], v[156:159]\n" \
B_"v7, s[80:83], s66 offen offset:3072 lds\n" \
M_"v[152:155], a[96:99], a[200:203], v[152:155]\n" \
X_"a[96:97], v35 offset:42016\n" \
X_"a[98:99], v35 offset:44320\n" \
M_"v[160:163], a[100:103], a[200:203], v[160:163]\n" \
"s_add_u32 m0, s67, 0x2000\n" \
"v_readlane_b32 s67, v11, 51\n" \
M_"v[164:167], a[100:103], a[216:219], v[164:167]\n" \
M_"v[232:235], a[100:103], v[72:75], v[232:235]\n" \
M_"v[236:239], a[100:103], v[96:99], v[236:239]\n" \
X_"a[100:101], v35 offset:52384\n" \
X_"a[102:103], v35 offset:54688\n" \
M_"v[244:247], a[104:107], v[96:99], v[244:247]\n" \
B_"v8, s[80:83], s66 offen offset:0 lds\n" \
M_"v[240:243], a[104:107], v[72:75], v[240:243]\n" \
"s_add_u32 m0, s92, 0x0\n" \
"v_readlane_b32 s66, v11, 50\n" \
M_"v[172:175], a[104:107], a[216:219], v[172:175]\n" \
M_"v[168:171], a[104:107], a[200:203], v[168:171]\n" \
X_"a[104:105], v35 offset:42048\n" \
X_"a[106:107], v35 offset:44352\n" \
M_"v[176:179], a[108:111], a[200:203], v[176:179]\n" \
M_"v[180:183], a[108:111], a[216:219], v[180:183]\n" \
B_"v0, s[80:83], s73 offen offset:0 lds\n" \
M_"v[248:251], a[108:111], v[72:75], v[248:251]\n" \
M_"v[252:255], a[108:111], v[96:99], v[252:255]\n" \
X_"a[108:109], v35 offset:52416\n" \
X_"a[110:111], v35 offset:54720\n" \
M_"v[112:115], a[112:115], a[204:207], v[112:115]\n" \
M_"v[116:119], a[112:115], a[220:223], v[116:119]\n" \
M_"v[184:187], a[112:115], v[76:79], v[184:187]\n" \
B_"v1, s[80:83], s73 offen offset:1024 lds\n" \
M_"v[188:191], a[112:115], v[100:103], v[188:191]\n" \
X_"a[112:113], v35 offset:42080\n" \
X_"a[114:115], v35 offset:44384\n" \
M_"v[196:199], a[116:119], v[100:103], v[196:199]\n" \
M_"v[192:195], a[116:119], v[76:79], v[192:195]\n" \
"s_waitcnt vmcnt(18)\n" \
"s_mov_b64 exec, 1\n" \
"buffer_store_dword v39, off, s[56:59], s70 offset:0 sc1\n" \
"s_mov_b64 exec, -1\n" \
M_"v[124:127], a[116:119], a[220:223], v[124:127]\n" \
"v_readlane_b32 s70, v11, 54\n" \
M_"v[120:123], a[116:119], a[204:207], v[120:123]\n" \
X_"a[116:117], v35 offset:52448\n" \
X_"a[118:119], v35 offset:54752\n" \
M_"v[128:131], a[120:123], a[204:207], v[128:131]\n" \
M_"v[132:135], a[120:123], a[220:223], v[132:135]\n" \
B_"v2, s[80:83], s73 offen offset:2048 lds\n" \
M_"v[200:203], a[120:123], v[76:79], v[200:203]\n" \
M_"v[204:207], a[120:123], v[100:103], v[204:207]\n" \
X_"a[120:121], v35 offset:42112\n" \
X_"a[122:123], v35 offset:44416\n" \
M_"v[212:215], a[0:3], v[100:103], v[212:215]\n" \
M_"v[208:211], a[0:3], v[76:79], v[208:211]\n" \
M_"v[140:143], a[0:3], a[220:223], v[140:143]\n" \
B_"v3, s[80:83], s73 offen offset:3072 lds\n" \
M_"v[136:139], a[0:3], a[204:207], v[136:139]\n" \
X_"a[0:1], v35 offset:52480\n" \
"s_waitcnt lgkmcnt(7)\n" \
M_"v[144:147], a[4:7], a[204:207], v[144:147]\n" \
X_"a[2:3], v35 offset:54784\n" \
"s_add_u32 m0, s92, 0x1000\n" \
M_"v[148:151], a[4:7], a[220:223], v[148:151]\n" \
M_"v[216:219], a[4:7], v[76:79], v[216:219]\n" \
M_"v[220:223], a[4:7], v[100:103], v[220:223]\n" \
X_"a[4:5], v35 offset:42144\n" \
X_"a[6:7], v35 offset:44448\n" \
M_"v[228:231], a[8:11], v[100:103], v[228:231]\n" \
B_"v4, s[80:83], s73 offen offset:0 lds\n" \
M_"v[224:227], a[8:11], v[76:79], v[224:227]\n" \
M_"v[156:159], a[8:11], a[220:223], v[156:159]\n" \
M_"v[152:155], a[8:11], a[204:207], v[152:155]\n" \
X_"a[8:9], v35 offset:52512\n" \
X_"a[10:11], v35 offset:54816\n" \
M_"v[160:163], a[12:15], a[204:207], v[160:163]\n" \
M_"v[164:167], a[12:15], a[220:223], v[164:167]\n" \
B_"v5, s[80:83], s73 offen offset:1024 lds\n" \
M_"v[232:235], a[12:15], v[76:79], v[232:235]\n" \
M_"v[236:239], a[12:15], v[100:103], v[236:239]\n" \
X_"a[12:13], v35 offset:42176\n" \
X_"a[14:15], v35 offset:44480\n" \
M_"v[244:247], a[16:19], v[100:103], v[244:247]\n" \
M_"v[240:243], a[16:19], v[76:79], v[240:243]\n" \
M_"v[172:175], a[16:19], a[220:223], v[172:175]\n" \
B_"v6, s[80:83], s73 offen offset:2048 lds\n" \
M_"v[168:171], a[16:19], a[204:207], v[168:171]\n" \
X_"a[16:17], v35 offset:52544\n" \
X_"a[18:19], v35 offset:54848\n" \
M_"v[176:179], a[20:23], a[204:207], v[176:179]\n" \
M_"v[180:183], a[20:23], a[220:223], v[180:183]\n" \
M_"v[248:251], a[20:23], v[76:79], v[248:251]\n" \
M_"v[252:255], a[20:23], v[100:103], v[252:255]\n" \
X_"a[20:21], v35 offset:42208\n" \
X_"a[22:23], v35 offset:44512\n" \
M_"v[112:115], a[24:27], a[124:127], v[112:115]\n" \
B_"v7, s[80:83], s73 offen offset:3072 lds\n" \
M_"v[112:115], a[28:31], a[224:227], v[112:115]\n" \
"s_add_u32 m0, s92, 0x2000\n" \
"v_readlane_b32 s92, v11, 49\n" \
M_"v[116:119], a[24:27], a[228:231], v[116:119]\n" \
M_"v[116:119], a[28:31], a[232:235], v[116:119]\n" \
M_"v[184:187], a[24:27], a[236:239], v[184:187]\n" \
M_"v[184:187], a[28:31], a[240:243], v[184:187]\n" \
B_"v8, s[80:83], s73 offen offset:0 lds\n" \
M_"v[188:191], a[24:27], a[244:247], v[188:191]\n" \
X_"a[24:25], v35 offset:52576\n" \
X_"a[26:27], v35 offset:54880\n" \
M_"v[188:191], a[28:31], a[248:251], v[188:191]\n" \
"s_mov_b32 m0, s94\n" \
X_"a[28:29], v35 offset:42240\n" \
M_"v[196:199], a[96:99], a[244:247], v[196:199]\n" \
X_"a[30:31], v35 offset:44544\n" \
"v_readlane_b32 s73, v11, 6\n" \
M_"v[196:199], a[100:103], a[248:251], v[196:199]\n" \
M_"v[192:195], a[96:99], a[236:239], v[192:195]\n" \
B_"v9, s[84:87], 0 offen lds\n" \
M_"v[192:195], a[100:103], a[240:243], v[192:195]\n" \
M_"v[124:127], a[96:99], a[228:231], v[124:127]\n" \
M_"v[124:127], a[100:103], a[232:235], v[124:127]\n" \
M_"v[120:123], a[96:99], a[124:127], v[120:123]\n" \
X_"a[96:97], v35 offset:52608\n" \
X_"a[98:99], v35 offset:54912\n" \
M_"v[120:123], a[100:103], a[224:227], v[120:123]\n" \
X_"a[100:101], v33 offset:0\n" \
X_"a[102:103], v33 offset:4608\n" \
M_"v[128:131], a[104:107], a[124:127], v[128:131]\n" \
B_"a[128:131], v20, s[56:59], s68 offen offset:0 sc1\n" \
M_"v[128:131], a[108:111], a[224:227], v[128:131]\n" \
M_"v[132:135], a[104:107], a[228:231], v[132:135]\n" \
M_"v[132:135], a[108:111], a[232:235], v[132:135]\n" \
M_"v[200:203], a[104:107], a[236:239], v[200:203]\n" \
M_"v[200:203], a[108:111], a[240:243], v[200:203]\n" \
B_"a[132:135], v20, s[56:59], s68 offen offset:1024 sc1\n" \
M_"v[204:207], a[104:107], a[244:247], v[204:207]\n" \
X_"a[104:105], v33 offset:9216\n" \
X_"a[106:107], v33 offset:13824\n" \
M_"v[204:207], a[108:111], a[248:251], v[204:207]\n" \
X_"a[108:109], v33 offset:32\n" \
"s_waitcnt lgkmcnt(7)\n" \
M_"v[212:215], a[112:115], a[244:247], v[212:215]\n" \
X_"a[110:111], v33 offset:4640\n" \
M_"v[212:215], a[116:119], a[248:251], v[212:215]\n" \
M_"v[208:211], a[112:115], a[236:239], v[208:211]\n" \
"buffer_load_dword v47, v38, s[56:59], s71 offen sc1\n" \
M_"v[208:211], a[116:119], a[240:243], v[208:211]\n" \
M_"v[140:143], a[112:115], a[228:231], v[140:143]\n" \
M_"v[140:143], a[116:119], a[232:235], v[140:143]\n" \
M_"v[136:139], a[112:115], a[124:127], v[136:139]\n" \
X_"a[112:113], v33 offset:9248\n" \
X_"a[114:115], v33 offset:13856\n" \
M_"v[136:139], a[116:119], a[224:227], v[136:139]\n" \
X_"a[116:117], v33 offset:64\n" \
X_"a[118:119], v33 offset:4672\n" \
M_"v[144:147], a[120:123], a[124:127], v[144:147]\n" \
B_"a[144:147], v32, s[56:59], s68 offen offset:0 sc1\n" \
M_"v[144:147], a[0:3], a[224:227], v[144:147]\n" \
M_"v[148:151], a[120:123], a[228:231], v[148:151]\n" \
M_"v[148:151], a[0:3], a[232:235], v[148:151]\n" \
M_"v[216:219], a[120:123], a[236:239], v[216:219]\n" \
M_"v[216:219], a[0:3], a[240:243], v[216:219]\n" \
B_"a[148:151], v32, s[56:59], s68 offen offset:1024 sc1\n" \
M_"v[220:223], a[120:123], a[244:247], v[220:223]\n" \
X_"a[120:121], v33 offset:9280\n" \
X_"a[122:123], v33 offset:13888\n" \
M_"v[220:223], a[0:3], a[248:251], v[220:223]\n" \
X_"a[0:1], v33 offset:96\n" \
X_"a[2:3], v33 offset:4704\n" \
M_"v[228:231], a[4:7], a[244:247], v[228:231]\n" \
M_"v[228:231], a[8:11], a[248:251], v[228:231]\n" \
M_"v[224:227], a[4:7], a[236:239], v[224:227]\n" \
B_"a[152:155], v32, s[56:59], s68 offen offset:2048 sc1\n" \
M_"v[224:227], a[8:11], a[240:243], v[224:227]\n" \
M_"v[156:159], a[4:7], a[228:231], v[156:159]\n" \
M_"v[156:159], a[8:11], a[232:235], v[156:159]\n" \
M_"v[152:155], a[4:7], a[124:127], v[152:155]\n" \
X_"a[4:5], v33 offset:9312\n" \
X_"a[6:7], v33 offset:13920\n" \
M_"v[152:155], a[8:11], a[224:227], v[152:155]\n" \
X_"a[8:9], v33 offset:128\n" \
X_"a[10:11], v33 offset:4736\n" \
M_"v[160:163], a[12:15], a[124:127], v[160:163]\n" \
B_"a[156:159], v32, s[56:59], s68 offen offset:3072 sc1\n" \
M_"v[160:163], a[16:19], a[224:227], v[160:163]\n" \
M_"v[164:167], a[12:15], a[228:231], v[164:167]\n" \
M_"v[164:167], a[16:19], a[232:235], v[164:167]\n" \
M_"v[232:235], a[12:15], a[236:239], v[232:235]\n" \
M_"v[232:235], a[16:19], a[240:243], v[232:235]\n" \
B_"a[136:139], v20, s[56:59], s68 offen offset:2048 sc1\n" \
M_"v[236:239], a[12:15], a[244:247], v[236:239]\n" \
X_"a[12:13], v33 offset:9344\n" \
X_"a[14:15], v33 offset:13952\n" \
M_"v[236:239], a[16:19], a[248:251], v[236:239]\n" \
X_"a[16:17], v33 offset:160\n" \
X_"a[18:19], v33 offset:4768\n" \
M_"v[244:247], a[20:23], a[244:247], v[244:247]\n" \
M_"v[244:247], a[24:27], a[248:251], v[244:247]\n" \
M_"v[240:243], a[20:23], a[236:239], v[240:243]\n" \
B_"a[140:143], v20, s[56:59], s68 offen offset:3072 sc1\n" \
M_"v[240:243], a[24:27], a[240:243], v[240:243]\n" \
"v_readlane_b32 s68, v11, 52\n" \
M_"v[172:175], a[20:23], a[228:231], v[172:175]\n" \
M_"v[172:175], a[24:27], a[232:235], v[172:175]\n" \
M_"v[168:171], a[20:23], a[124:127], v[168:171]\n" \
X_"a[20:21], v33 offset:9376\n" \
X_"a[22:23], v33 offset:13984\n" \
M_"v[168:171], a[24:27], a[224:227], v[168:171]\n" \
X_"a[24:25], v33 offset:192\n" \
X_"a[26:27], v33 offset:4800\n" \
M_"v[176:179], a[28:31], a[124:127], v[176:179]\n" \
"s_waitcnt lgkmcnt(8)\n" \
M_"v[176:179], a[96:99], a[224:227], v[176:179]\n" \
B_"a[32:35], v20, s[56:59], s69 offen offset:0 sc1\n" \
M_"v[180:183], a[28:31], a[228:231], v[180:183]\n" \
M_"v[180:183], a[96:99], a[232:235], v[180:183]\n" \
M_"v[248:251], a[28:31], a[236:239], v[248:251]\n" \
M_"v[248:251], a[96:99], a[240:243], v[248:251]\n" \
M_"v[252:255], a[28:31], a[244:247], v[252:255]\n" \
X_"a[28:29], v33 offset:9408\n" \
X_"a[30:31], v33 offset:14016\n" \
M_"v[252:255], a[96:99], a[248:251], v[252:255]\n" \
X_"a[96:97], v33 offset:224\n" \
"s_waitcnt vmcnt(18)\n" \
M_"v[112:115], a[100:103], a[64:67], v[112:115]\n" \
B_"a[36:39], v20, s[56:59], s69 offen offset:1024 sc1\n" \
M_"v[112:115], a[104:107], a[68:71], v[112:115]\n" \
X_"a[98:99], v33 offset:4832\n" \
M_"v[116:119], a[100:103], a[80:83], v[116:119]\n" \
X_"a[100:101], v33 offset:9440\n" \
X_"a[102:103], v33 offset:14048\n" \
M_"v[116:119], a[104:107], a[84:87], v[116:119]\n" \
X_"a[104:105], v33 offset:256\n" \
X_"a[106:107], v33 offset:4864\n" \
M_"v[124:127], a[108:111], a[80:83], v[124:127]\n" \
M_"v[124:127], a[112:115], a[84:87], v[124:127]\n" \
B_"a[48:51], v32, s[56:59], s69 offen offset:0 sc1\n" \
M_"v[120:123], a[108:111], a[64:67], v[120:123]\n" \
X_"a[108:109], v33 offset:9472\n" \
X_"a[110:111], v33 offset:14080\n" \
M_"v[120:123], a[112:115], a[68:71], v[120:123]\n" \
X_"a[112:113], v33 offset:18432\n" \
X_"a[114:115], v33 offset:23040\n" \
M_"v[128:131], a[116:119], a[64:67], v[128:131]\n" \
M_"v[128:131], a[120:123], a[68:71], v[128:131]\n" \
M_"v[132:135], a[116:119], a[80:83], v[132:135]\n" \
B_"a[52:55], v32, s[56:59], s69 offen offset:1024 sc1\n" \
M_"v[132:135], a[120:123], a[84:87], v[132:135]\n" \
X_"a[116:117], v33 offset:27648\n" \
X_"a[118:119], v33 offset:32256\n" \
M_"v[140:143], a[0:3], a[80:83], v[140:143]\n" \
X_"a[120:121], v33 offset:18464\n" \
X_"a[122:123], v33 offset:23072\n" \
M_"v[140:143], a[4:7], a[84:87], v[140:143]\n" \
"s_waitcnt vmcnt(10)\n" \
"v_cmp_ne_u32_e32 vcc, s62, v47\n" \
M_"v[136:139], a[0:3], a[64:67], v[136:139]\n" \
X_"a[0:1], v33 offset:27680\n" \
".fill (L_ab + 36864 - .) / 4, 4, 0xBF800000\n" \
"L_ap9:\n" \
"s_cbranch_execz L_ap0\n" \
X_"a[2:3], v33 offset:32288\n" \
M_"v[136:139], a[4:7], a[68:71], v[136:139]\n" \
B_"a[56:59], v32, s[56:59], s69 offen offset:2048 sc1\n" \
M_"v[144:147], a[8:11], a[64:67], v[144:147]\n" \
X_"a[4:5], v33 offset:18496\n" \
"s_waitcnt lgkmcnt(13)\n" \
M_"v[144:147], a[12:15], a[68:71], v[144:147]\n" \
X_"a[6:7], v33 offset:23104\n" \
"s_cbranch_vccnz L_sb_%=\n" \
"L_rb_%=:\n" \
M_"v[148:151], a[8:11], a[80:83], v[148:151]\n" \
X_"a[8:9], v33 offset:27712\n" \
X_"a[10:11], v33 offset:32320\n" \
M_"v[148:151], a[12:15], a[84:87], v[148:151]\n" \
X_"a[12:13], v33 offset:18528\n" \
X_"a[14:15], v33 offset:23136\n" \
B_"a[60:63], v32, s[56:59], s69 offen offset:3072 sc1\n" \
M_"v[156:159], a[16:19], a[80:83], v[156:159]\n" \
"v_readlane_b32 s71, v11, 55\n" \
"v_cmp_eq_u32_e32 vcc, 0, v44\n" \
M_"v[156:159], a[20:23], a[84:87], v[156:159]\n" \
M_"v[152:155], a[16:19], a[64:67], v[152:155]\n" \
X_"a[16:17], v33 offset:27744\n" \
X_"a[18:19], v33 offset:32352\n" \
M_"v[152:155], a[20:23], a[68:71], v[152:155]\n" \
X_"a[20:21], v33 offset:18560\n" \
X_"a[22:23], v33 offset:23168\n" \
B_"a[40:43], v20, s[56:59], s69 offen offset:2048 sc1\n" \
M_"v[160:163], a[24:27], a[64:67], v[160:163]\n" \
"v_cndmask_b32_e32 v50, 0, v45, vcc\n" \
"v_cmp_eq_u32_e32 vcc, 1, v44\n" \
M_"v[160:163], a[28:31], a[68:71], v[160:163]\n" \
"v_and_b32_e32 v44, 0x3c0, v20\n" \
"v_cndmask_b32_e32 v51, 0, v45, vcc\n" \
M_"v[164:167], a[24:27], a[80:83], v[164:167]\n" \
X_"a[24:25], v33 offset:27776\n" \
X_"a[26:27], v33 offset:32384\n" \
M_"v[164:167], a[28:31], a[84:87], v[164:167]\n" \
X_"a[28:29], v33 offset:18592\n" \
X_"a[30:31], v33 offset:23200\n" \
B_"a[44:47], v20, s[56:59], s69 offen offset:3072 sc1\n" \
M_"v[172:175], a[96:99], a[80:83], v[172:175]\n" \
"v_readlane_b32 s69, v11, 53\n" \
"v_bfe_u32 v45, v20, 1, 5\n" \
M_"v[172:175], a[100:103], a[84:87], v[172:175]\n" \
"v_and_b32_e32 v45, 24, v45\n" \
"v_add_u32_e32 v45, 0x4800, v45\n" \
M_"v[168:171], a[96:99], a[64:67], v[168:171]\n" \
X_"a[96:97], v33 offset:27808\n" \
X_"a[98:99], v33 offset:32416\n" \
M_"v[168:171], a[100:103], a[68:71], v[168:171]\n" \
X_"a[100:101], v33 offset:18624\n" \
"s_waitcnt lgkmcnt(13)\n" \
M_"v[176:179], a[104:107], a[64:67], v[176:179]\n" \
X_"a[102:103], v33 offset:23232\n" \
"v_add_u32_e32 v36, v44, v45\n" \
M_"v[176:179], a[108:111], a[68:71], v[176:179]\n" \
B_"a[124:127], v20, s[56:59], s64 offen offset:0 sc1\n" \
M_"v[180:183], a[104:107], a[80:83], v[180:183]\n" \
X_"a[104:105], v33 offset:27840\n" \
X_"a[106:107], v33 offset:32448\n" \
M_"v[180:183], a[108:111], a[84:87], v[180:183]\n" \
X_"a[108:109], v33 offset:18656\n" \
X_"a[110:111], v33 offset:23264\n" \
M_"v[112:115], a[112:115], a[72:75], v[112:115]\n" \
M_"v[112:115], a[116:119], a[76:79], v[112:115]\n" \
M_"v[116:119], a[112:115], a[88:91], v[116:119]\n" \
X_"a[112:113], v33 offset:27872\n" \
X_"a[114:115], v33 offset:32480\n" \
M_"v[116:119], a[116:119], a[92:95], v[116:119]\n" \
X_"a[116:117], v33 offset:18688\n" \
X_"a[118:119], v33 offset:23296\n" \
M_"v[124:127], a[120:123], a[88:91], v[124:127]\n" \
B_"a[224:227], v20, s[56:59], s64 offen offset:1024 sc1\n" \
M_"v[124:127], a[0:3], a[92:95], v[124:127]\n" \
M_"v[120:123], a[120:123], a[72:75], v[120:123]\n" \
X_"a[120:121], v33 offset:27904\n" \
X_"a[122:123], v33 offset:32512\n" \
M_"v[120:123], a[0:3], a[76:79], v[120:123]\n" \
"s_barrier\n" \
M_"v[128:131], a[4:7], a[72:75], v[128:131]\n" \
X_"a[0:1], v33 offset:36864\n" \
X_"a[2:3], v33 offset:41472\n" \
M_"v[128:131], a[8:11], a[76:79], v[128:131]\n" \
B_"a[236:239], v32, s[56:59], s64 offen offset:0 sc1\n" \
M_"v[132:135], a[4:7], a[88:91], v[132:135]\n" \
X_"a[4:5], v33 offset:46080\n" \
X_"a[6:7], v33 offset:50688\n" \
M_"v[132:135], a[8:11], a[92:95], v[132:135]\n" \
X_"a[8:9], v33 offset:55296\n" \
"s_waitcnt lgkmcnt(15)\n" \
M_"v[140:143], a[12:15], a[88:91], v[140:143]\n" \
X_"a[10:11], v33 offset:59904\n" \
M_"v[140:143], a[16:19], a[92:95], v[140:143]\n" \
M_"v[136:139], a[12:15], a[72:75], v[136:139]\n" \
X_"a[12:13], v10 offset:0\n" \
X_"a[14:15], v10 offset:4608\n" \
M_"v[136:139], a[16:19], a[76:79], v[136:139]\n" \
X_"a[16:17], v34 offset:0\n" \
X_"a[18:19], v34 offset:4608\n" \
M_"v[144:147], a[20:23], a[72:75], v[144:147]\n" \
B_"a[240:243], v32, s[56:59], s64 offen offset:1024 sc1\n" \
M_"v[144:147], a[24:27], a[76:79], v[144:147]\n" \
M_"v[148:151], a[20:23], a[88:91], v[148:151]\n" \
X_"a[20:21], v34 offset:9216\n" \
X_"a[22:23], v34 offset:13824\n" \
M_"v[148:151], a[24:27], a[92:95], v[148:151]\n" \
X_"a[24:25], v34 offset:18432\n" \
X_"a[26:27], v34 offset:23040\n" \
M_"v[156:159], a[28:31], a[88:91], v[156:159]\n" \
M_"v[156:159], a[96:99], a[92:95], v[156:159]\n" \
B_"a[244:247], v32, s[56:59], s64 offen offset:2048 sc1\n" \
M_"v[152:155], a[28:31], a[72:75], v[152:155]\n" \
X_"a[28:29], v10 offset:9216\n" \
X_"a[30:31], v10 offset:13824\n" \
M_"v[152:155], a[96:99], a[76:79], v[152:155]\n" \
X_"a[96:97], v33 offset:36896\n" \
X_"a[98:99], v33 offset:41504\n" \
M_"v[160:163], a[100:103], a[72:75], v[160:163]\n" \
"s_waitcnt lgkmcnt(15)\n" \
M_"v[160:163], a[104:107], a[76:79], v[160:163]\n" \
M_"v[164:167], a[100:103], a[88:91], v[164:167]\n" \
X_"a[100:101], v33 offset:46112\n" \
X_"a[102:103], v33 offset:50720\n" \
M_"v[164:167], a[104:107], a[92:95], v[164:167]\n" \
X_"a[104:105], v33 offset:55328\n" \
X_"a[106:107], v33 offset:59936\n" \
M_"v[172:175], a[108:111], a[88:91], v[172:175]\n" \
B_"a[248:251], v32, s[56:59], s64 offen offset:3072 sc1\n" \
M_"v[172:175], a[112:115], a[92:95], v[172:175]\n" \
M_"v[168:171], a[108:111], a[72:75], v[168:171]\n" \
X_"a[108:109], v10 offset:32\n" \
X_"a[110:111], v10 offset:4640\n" \
M_"v[168:171], a[112:115], a[76:79], v[168:171]\n" \
X_"a[112:113], v34 offset:32\n" \
X_"a[114:115], v34 offset:4640\n" \
M_"v[176:179], a[116:119], a[72:75], v[176:179]\n" \
M_"v[176:179], a[120:123], a[76:79], v[176:179]\n" \
B_"a[228:231], v20, s[56:59], s64 offen offset:2048 sc1\n" \
M_"v[180:183], a[116:119], a[88:91], v[180:183]\n" \
X_"a[116:117], v34 offset:9248\n" \
X_"a[118:119], v34 offset:13856\n" \
M_"v[180:183], a[120:123], a[92:95], v[180:183]\n" \
X_"a[120:121], v34 offset:18464\n" \
X_"a[122:123], v34 offset:23072\n" \
M_"v[112:115], a[0:3], a[128:131], v[112:115]\n" \
"s_waitcnt lgkmcnt(15)\n" \
M_"v[112:115], a[4:7], a[132:135], v[112:115]\n" \
"s_waitcnt vmcnt(18)\n" \
M_"v[116:119], a[0:3], a[144:147], v[116:119]\n" \
X_"a[0:1], v10 offset:9248\n" \
X_"a[2:3], v10 offset:13856\n" \
M_"v[116:119], a[4:7], a[148:151], v[116:119]\n" \
X_"a[4:5], v33 offset:36928\n" \
X_"a[6:7], v33 offset:41536\n" \
M_"v[116:119], a[8:11], a[152:155], v[116:119]\n" \
"s_waitcnt vmcnt(17)\n" \
M_"v[116:119], a[12:15], a[156:159], v[116:119]\n" \
"s_waitcnt vmcnt(16)\n" \
M_"v[112:115], a[8:11], a[136:139], v[112:115]\n" \
X_"a[8:9], v33 offset:46144\n" \
"s_waitcnt vmcnt(15)\n" \
M_"v[112:115], a[12:15], a[140:143], v[112:115]\n" \
X_"a[10:11], v33 offset:50752\n" \
"s_waitcnt vmcnt(14)\n" \
M_"v[112:115], a[16:19], a[32:35], v[112:115]\n" \
X_"a[12:13], v33 offset:55360\n" \
"s_waitcnt vmcnt(13)\n" \
M_"v[112:115], a[20:23], a[36:39], v[112:115]\n" \
X_"a[14:15], v33 offset:59968\n" \
"s_waitcnt vmcnt(12)\n" \
M_"v[116:119], a[16:19], a[48:51], v[116:119]\n" \
X_"a[16:17], v10 offset:64\n" \
"s_waitcnt vmcnt(11)\n" \
M_"v[116:119], a[20:23], a[52:55], v[116:119]\n" \
X_"a[18:19], v10 offset:4672\n" \
"s_waitcnt vmcnt(10)\n" \
M_"v[116:119], a[24:27], a[56:59], v[116:119]\n" \
X_"a[20:21], v34 offset:64\n" \
X_"a[22:23], v34 offset:4672\n" \
B_"a[232:235], v20, s[56:59], s64 offen offset:3072 sc1\n" \
"s_waitcnt vmcnt(10) lgkmcnt(12)\n" \
M_"v[116:119], a[28:31], a[60:63], v[116:119]\n" \
B_"a[160:163], v20, s[56:59], s72 offen offset:0 sc1\n" \
"s_waitcnt vmcnt(10)\n" \
M_"v[112:115], a[24:27], a[40:43], v[112:115]\n" \
X_"a[24:25], v34 offset:9280\n" \
X_"a[26:27], v34 offset:13888\n" \
B_"a[164:167], v20, s[56:59], s72 offen offset:1024 sc1\n" \
"s_waitcnt vmcnt(10)\n" \
M_"v[112:115], a[28:31], a[44:47], v[112:115]\n" \
X_"a[28:29], v34 offset:18496\n" \
X_"a[30:31], v34 offset:23104\n" \
M_"v[120:123], a[96:99], a[128:131], v[120:123]\n" \
B_"a[176:179], v32, s[56:59], s72 offen offset:0 sc1\n" \
M_"v[120:123], a[100:103], a[132:135], v[120:123]\n" \
M_"v[124:127], a[96:99], a[144:147], v[124:127]\n" \
X_"a[96:97], v10 offset:9280\n" \
X_"a[98:99], v10 offset:13888\n" \
C_"v112, v112, v113\n" \
C_"v113, v114, v115\n" \
C_"v114, v116, v117\n" \
C_"v115, v118, v119\n" \
"buffer_store_dwordx4 v[112:115], v20, s[56:59], s65 offen offset:0 sc1\n" \
"s_add_u32 s65, s65, 0x3800\n" \
M_"v[124:127], a[100:103], a[148:151], v[124:127]\n" \
X_"a[100:101], v33 offset:36960\n" \
X_"a[102:103], v33 offset:41568\n" \
M_"v[124:127], a[104:107], a[152:155], v[124:127]\n" \
M_"v[124:127], a[108:111], a[156:159], v[124:127]\n" \
M_"v[120:123], a[104:107], a[136:139], v[120:123]\n" \
X_"a[104:105], v33 offset:46176\n" \
X_"a[106:107], v33 offset:50784\n" \
M_"v[120:123], a[108:111], a[140:143], v[120:123]\n" \
X_"a[108:109], v33 offset:55392\n" \
X_"a[110:111], v33 offset:60000\n" \
M_"v[120:123], a[112:115], a[32:35], v[120:123]\n" \
B_"a[180:183], v32, s[56:59], s72 offen offset:1024 sc1\n" \
M_"v[120:123], a[116:119], a[36:39], v[120:123]\n" \
M_"v[124:127], a[112:115], a[48:51], v[124:127]\n" \
X_"a[112:113], v10 offset:96\n" \
X_"a[114:115], v10 offset:4704\n" \
M_"v[124:127], a[116:119], a[52:55], v[124:127]\n" \
X_"a[116:117], v34 offset:96\n" \
X_"a[118:119], v34 offset:4704\n" \
M_"v[124:127], a[120:123], a[56:59], v[124:127]\n" \
"s_waitcnt lgkmcnt(12)\n" \
M_"v[124:127], a[0:3], a[60:63], v[124:127]\n" \
B_"a[168:171], v20, s[56:59], s72 offen offset:2048 sc1\n" \
M_"v[120:123], a[120:123], a[40:43], v[120:123]\n" \
X_"a[120:121], v34 offset:9312\n" \
X_"a[122:123], v34 offset:13920\n" \
M_"v[120:123], a[0:3], a[44:47], v[120:123]\n" \
X_"a[0:1], v34 offset:18528\n" \
X_"a[2:3], v34 offset:23136\n" \
M_"v[128:131], a[4:7], a[128:131], v[128:131]\n" \
M_"v[128:131], a[8:11], a[132:135], v[128:131]\n" \
M_"v[132:135], a[4:7], a[144:147], v[132:135]\n" \
X_"a[4:5], v10 offset:9312\n" \
X_"a[6:7], v10 offset:13920\n" \
M_"v[132:135], a[8:11], a[148:151], v[132:135]\n" \
C_"v120, v120, v121\n" \
C_"v121, v122, v123\n" \
C_"v122, v124, v125\n" \
C_"v123, v126, v127\n" \
"buffer_store_dwordx4 v[120:123], v20, s[56:59], s66 offen offset:0 sc1\n" \
"s_add_u32 s66, s66, 0x3000\n" \
M_"v[132:135], a[12:15], a[152:155], v[132:135]\n" \
X_"a[8:9], v33 offset:36992\n" \
X_"a[10:11], v33 offset:41600\n" \
M_"v[132:135], a[16:19], a[156:159], v[132:135]\n" \
M_"v[128:131], a[12:15], a[136:139], v[128:131]\n" \
X_"a[12:13], v33 offset:46208\n" \
X_"a[14:15], v33 offset:50816\n" \
M_"v[128:131], a[16:19], a[140:143], v[128:131]\n" \
X_"a[16:17], v33 offset:55424\n" \
X_"a[18:19], v33 offset:60032\n" \
M_"v[128:131], a[20:23], a[32:35], v[128:131]\n" \
B_"a[184:187], v32, s[56:59], s72 offen offset:2048 sc1\n" \
M_"v[128:131], a[24:27], a[36:39], v[128:131]\n" \
M_"v[132:135], a[20:23], a[48:51], v[132:135]\n" \
X_"a[20:21], v10 offset:128\n" \
X_"a[22:23], v10 offset:4736\n" \
M_"v[132:135], a[24:27], a[52:55], v[132:135]\n" \
X_"a[24:25], v34 offset:128\n" \
X_"a[26:27], v34 offset:4736\n" \
M_"v[132:135], a[28:31], a[56:59], v[132:135]\n" \
"s_waitcnt lgkmcnt(12)\n" \
M_"v[132:135], a[96:99], a[60:63], v[132:135]\n" \
B_"a[172:175], v20, s[56:59], s72 offen offset:3072 sc1\n" \
M_"v[128:131], a[28:31], a[40:43], v[128:131]\n" \
X_"a[28:29], v34 offset:9344\n" \
X_"a[30:31], v34 offset:13952\n" \
M_"v[128:131], a[96:99], a[44:47], v[128:131]\n" \
X_"a[96:97], v34 offset:18560\n" \
X_"a[98:99], v34 offset:23168\n" \
M_"v[136:139], a[100:103], a[128:131], v[136:139]\n" \
M_"v[136:139], a[104:107], a[132:135], v[136:139]\n" \
M_"v[140:143], a[100:103], a[144:147], v[140:143]\n" \
B_"a[188:191], v32, s[56:59], s72 offen offset:3072 sc1\n" \
M_"v[140:143], a[104:107], a[148:151], v[140:143]\n" \
"v_readlane_b32 s72, v11, 56\n" \
C_"v128, v128, v129\n" \
C_"v129, v130, v131\n" \
C_"v130, v132, v133\n" \
C_"v131, v134, v135\n" \
M_"v[140:143], a[108:111], a[152:155], v[140:143]\n" \
"buffer_store_dwordx4 v[128:131], v20, s[56:59], s67 offen offset:0 sc1\n" \
"s_add_u32 s67, s67, 0x2800\n" \
M_"v[140:143], a[112:115], a[156:159], v[140:143]\n" \
X_"a[100:101], v10 offset:9344\n" \
X_"a[102:103], v10 offset:13952\n" \
M_"v[136:139], a[108:111], a[136:139], v[136:139]\n" \
X_"a[104:105], v33 offset:37024\n" \
X_"a[106:107], v33 offset:41632\n" \
M_"v[136:139], a[112:115], a[140:143], v[136:139]\n" \
X_"a[108:109], v33 offset:46240\n" \
X_"a[110:111], v33 offset:50848\n" \
M_"v[136:139], a[116:119], a[32:35], v[136:139]\n" \
X_"a[112:113], v33 offset:55456\n" \
X_"a[114:115], v33 offset:60064\n" \
M_"v[136:139], a[120:123], a[36:39], v[136:139]\n" \
B_"a[192:195], v20, s[56:59], s93 offen offset:0 sc1\n" \
M_"v[140:143], a[116:119], a[48:51], v[140:143]\n" \
X_"a[116:117], v10 offset:160\n" \
X_"a[118:119], v10 offset:4768\n" \
M_"v[140:143], a[120:123], a[52:55], v[140:143]\n" \
X_"a[120:121], v34 offset:160\n" \
X_"a[122:123], v34 offset:4768\n" \
M_"v[140:143], a[0:3], a[56:59], v[140:143]\n" \
"s_waitcnt lgkmcnt(13)\n" \
M_"v[140:143], a[4:7], a[60:63], v[140:143]\n" \
M_"v[136:139], a[0:3], a[40:43], v[136:139]\n" \
B_"a[196:199], v20, s[56:59], s93 offen offset:1024 sc1\n" \
M_"v[136:139], a[4:7], a[44:47], v[136:139]\n" \
X_"a[0:1], v34 offset:9376\n" \
X_"a[2:3], v34 offset:13984\n" \
M_"v[144:147], a[8:11], a[128:131], v[144:147]\n" \
X_"a[4:5], v34 offset:18592\n" \
X_"a[6:7], v34 offset:23200\n" \
M_"v[144:147], a[12:15], a[132:135], v[144:147]\n" \
M_"v[148:151], a[8:11], a[144:147], v[148:151]\n" \
X_"a[8:9], v10 offset:9376\n" \
C_"v136, v136, v137\n" \
C_"v137, v138, v139\n" \
C_"v138, v140, v141\n" \
C_"v139, v142, v143\n" \
"buffer_store_dwordx4 v[136:139], v20, s[56:59], s68 offen offset:0 sc1\n" \
"s_add_u32 s68, s68, 0x2000\n" \
M_"v[148:151], a[12:15], a[148:151], v[148:151]\n" \
X_"a[10:11], v10 offset:13984\n" \
X_"a[12:13], v33 offset:37056\n" \
M_"v[148:151], a[16:19], a[152:155], v[148:151]\n" \
X_"a[14:15], v33 offset:41664\n" \
M_"v[148:151], a[20:23], a[156:159], v[148:151]\n" \
M_"v[144:147], a[16:19], a[136:139], v[144:147]\n" \
B_"a[208:211], v32, s[56:59], s93 offen offset:0 sc1\n" \
M_"v[144:147], a[20:23], a[140:143], v[144:147]\n" \
X_"a[16:17], v33 offset:46272\n" \
X_"a[18:19], v33 offset:50880\n" \
M_"v[144:147], a[24:27], a[32:35], v[144:147]\n" \
X_"a[20:21], v33 offset:55488\n" \
X_"a[22:23], v33 offset:60096\n" \
M_"v[144:147], a[28:31], a[36:39], v[144:147]\n" \
M_"v[148:151], a[24:27], a[48:51], v[148:151]\n" \
X_"a[24:25], v10 offset:192\n" \
X_"a[26:27], v10 offset:4800\n" \
B_"a[212:215], v32, s[56:59], s93 offen offset:1024 sc1\n" \
M_"v[148:151], a[28:31], a[52:55], v[148:151]\n" \
X_"a[28:29], v34 offset:192\n" \
"s_waitcnt lgkmcnt(13)\n" \
M_"v[148:151], a[96:99], a[56:59], v[148:151]\n" \
X_"a[30:31], v34 offset:4800\n" \
M_"v[148:151], a[100:103], a[60:63], v[148:151]\n" \
M_"v[144:147], a[96:99], a[40:43], v[144:147]\n" \
X_"a[96:97], v34 offset:9408\n" \
X_"a[98:99], v34 offset:14016\n" \
B_"a[216:219], v32, s[56:59], s93 offen offset:2048 sc1\n" \
M_"v[144:147], a[100:103], a[44:47], v[144:147]\n" \
X_"a[100:101], v34 offset:18624\n" \
X_"a[102:103], v34 offset:23232\n" \
M_"v[152:155], a[104:107], a[128:131], v[152:155]\n" \
M_"v[152:155], a[108:111], a[132:135], v[152:155]\n" \
M_"v[156:159], a[104:107], a[144:147], v[156:159]\n" \
X_"a[104:105], v10 offset:9408\n" \
X_"a[106:107], v10 offset:14016\n" \
B_"a[220:223], v32, s[56:59], s93 offen offset:3072 sc1\n" \
C_"v144, v144, v145\n" \
C_"v145, v146, v147\n" \
C_"v146, v148, v149\n" \
C_"v147, v150, v151\n" \
M_"v[156:159], a[108:111], a[148:151], v[156:159]\n" \
X_"a[108:109], v33 offset:256\n" \
X_"a[110:111], v33 offset:4864\n" \
"buffer_store_dwordx4 v[144:147], v20, s[56:59], s69 offen offset:0 sc1\n" \
"s_add_u32 s69, s69, 0x1800\n" \
M_"v[156:159], a[112:115], a[152:155], v[156:159]\n" \
M_"v[156:159], a[116:119], a[156:159], v[156:159]\n" \
M_"v[152:155], a[112:115], a[136:139], v[152:155]\n" \
X_"a[112:113], v33 offset:9472\n" \
X_"a[114:115], v33 offset:14080\n" \
M_"v[152:155], a[116:119], a[140:143], v[152:155]\n" \
B_"a[200:203], v20, s[56:59], s93 offen offset:2048 sc1\n" \
M_"v[152:155], a[120:123], a[32:35], v[152:155]\n" \
X_"a[116:117], v33 offset:18688\n" \
X_"a[118:119], v33 offset:23296\n" \
M_"v[152:155], a[0:3], a[36:39], v[152:155]\n" \
M_"v[156:159], a[120:123], a[48:51], v[156:159]\n" \
X_"a[120:121], v33 offset:27904\n" \
X_"a[122:123], v33 offset:32512\n" \
M_"v[156:159], a[0:3], a[52:55], v[156:159]\n" \
X_"a[0:1], v33 offset:37120\n" \
B_"a[204:207], v20, s[56:59], s93 offen offset:3072 sc1\n" \
"s_waitcnt lgkmcnt(11)\n" \
M_"v[156:159], a[4:7], a[56:59], v[156:159]\n" \
X_"a[2:3], v33 offset:41728\n" \
M_"v[156:159], a[8:11], a[60:63], v[156:159]\n" \
M_"v[152:155], a[4:7], a[40:43], v[152:155]\n" \
X_"a[4:5], v33 offset:46336\n" \
X_"a[6:7], v33 offset:50944\n" \
M_"v[152:155], a[8:11], a[44:47], v[152:155]\n" \
X_"a[8:9], v33 offset:55552\n" \
X_"a[10:11], v33 offset:60160\n" \
M_"v[160:163], a[12:15], a[128:131], v[160:163]\n" \
B_"v[64:67], v20, s[56:59], s95 offen offset:0 sc1\n" \
M_"v[160:163], a[16:19], a[132:135], v[160:163]\n" \
M_"v[164:167], a[12:15], a[144:147], v[164:167]\n" \
X_"a[12:13], v10 offset:256\n" \
X_"a[14:15], v10 offset:4864\n" \
C_"v152, v152, v153\n" \
C_"v153, v154, v155\n" \
C_"v154, v156, v157\n" \
C_"v155, v158, v159\n" \
"buffer_store_dwordx4 v[152:155], v20, s[56:59], s70 offen offset:0 sc1\n" \
"s_add_u32 s70, s70, 0x1000\n" \
M_"v[164:167], a[16:19], a[148:151], v[164:167]\n" \
X_"a[16:17], v34 offset:256\n" \
X_"a[18:19], v34 offset:4864\n" \
M_"v[164:167], a[20:23], a[152:155], v[164:167]\n" \
M_"v[164:167], a[24:27], a[156:159], v[164:167]\n" \
M_"v[160:163], a[20:23], a[136:139], v[160:163]\n" \
X_"a[20:21], v34 offset:9472\n" \
X_"a[22:23], v34 offset:14080\n" \
M_"v[160:163], a[24:27], a[140:143], v[160:163]\n" \
X_"a[24:25], v34 offset:18688\n" \
X_"a[26:27], v34 offset:23296\n" \
"s_waitcnt vmcnt(19)\n" \
"s_mov_b64 exec, 1\n" \
"buffer_store_dword v39, off, s[56:59], s65 offset:0 sc1\n" \
"s_mov_b64 exec, -1\n" \
M_"v[160:163], a[28:31], a[32:35], v[160:163]\n" \
M_"v[160:163], a[96:99], a[36:39], v[160:163]\n" \
M_"v[164:167], a[28:31], a[48:51], v[164:167]\n" \
X_"a[28:29], v10 offset:9472\n" \
X_"a[30:31], v10 offset:14080\n" \
M_"v[164:167], a[96:99], a[52:55], v[164:167]\n" \
X_"a[96:97], v33 offset:0\n" \
X_"a[98:99], v33 offset:4608\n" \
M_"v[164:167], a[100:103], a[56:59], v[164:167]\n" \
"s_waitcnt lgkmcnt(12)\n" \
M_"v[164:167], a[104:107], a[60:63], v[164:167]\n" \
B_"v[68:71], v20, s[56:59], s95 offen offset:1024 sc1\n" \
M_"v[160:163], a[100:103], a[40:43], v[160:163]\n" \
X_"a[100:101], v33 offset:9216\n" \
X_"a[102:103], v33 offset:13824\n" \
M_"v[160:163], a[104:107], a[44:47], v[160:163]\n" \
X_"a[104:105], v33 offset:18432\n" \
X_"a[106:107], v33 offset:23040\n" \
M_"v[248:251], a[108:111], a[124:127], v[248:251]\n" \
M_"v[248:251], a[112:115], a[224:227], v[248:251]\n" \
M_"v[252:255], a[108:111], a[236:239], v[252:255]\n" \
X_"a[108:109], v33 offset:27648\n" \
X_"a[110:111], v33 offset:32256\n" \
M_"v[252:255], a[112:115], a[240:243], v[252:255]\n" \
C_"v160, v160, v161\n" \
C_"v161, v162, v163\n" \
C_"v162, v164, v165\n" \
C_"v163, v166, v167\n" \
"buffer_store_dwordx4 v[160:163], v20, s[56:59], s71 offen offset:0 sc1\n" \
"s_add_u32 s71, s71, 0x800\n" \
M_"v[252:255], a[116:119], a[244:247], v[252:255]\n" \
X_"a[112:113], v33 offset:36864\n" \
X_"a[114:115], v33 offset:41472\n" \
M_"v[252:255], a[120:123], a[248:251], v[252:255]\n" \
M_"v[248:251], a[116:119], a[228:231], v[248:251]\n" \
X_"a[116:117], v33 offset:46080\n" \
X_"a[118:119], v33 offset:50688\n" \
"s_waitcnt vmcnt(19)\n" \
"s_mov_b64 exec, 1\n" \
"buffer_store_dword v39, off, s[56:59], s66 offset:4 sc1\n" \
"s_mov_b64 exec, -1\n" \
"v_readlane_b32 s66, v11, 50\n" \
M_"v[248:251], a[120:123], a[232:235], v[248:251]\n" \
X_"a[120:121], v33 offset:55296\n" \
X_"a[122:123], v33 offset:59904\n" \
M_"v[176:179], a[0:3], a[128:131], v[176:179]\n" \
M_"v[176:179], a[4:7], a[132:135], v[176:179]\n" \
M_"v[180:183], a[0:3], a[144:147], v[180:183]\n" \
B_"v[72:75], v32, s[56:59], s95 offen offset:0 sc1\n" \
M_"v[180:183], a[4:7], a[148:151], v[180:183]\n" \
M_"v[248:251], a[0:3], a[160:163], v[248:251]\n" \
M_"v[248:251], a[4:7], a[164:167], v[248:251]\n" \
M_"v[252:255], a[0:3], a[176:179], v[252:255]\n" \
X_"a[0:1], v10 offset:0\n" \
X_"a[2:3], v10 offset:4608\n" \
M_"v[252:255], a[4:7], a[180:183], v[252:255]\n" \
X_"a[4:5], v34 offset:0\n" \
"s_waitcnt vmcnt(20)\n" \
M_"v[252:255], a[8:11], a[184:187], v[252:255]\n" \
X_"a[6:7], v34 offset:4608\n" \
"s_waitcnt vmcnt(18) lgkmcnt(10)\n" \
M_"v[252:255], a[12:15], a[188:191], v[252:255]\n" \
B_"v[76:79], v32, s[56:59], s95 offen offset:1024 sc1\n" \
M_"v[248:251], a[8:11], a[168:171], v[248:251]\n" \
M_"v[248:251], a[12:15], a[172:175], v[248:251]\n" \
M_"v[180:183], a[8:11], a[152:155], v[180:183]\n" \
M_"v[180:183], a[12:15], a[156:159], v[180:183]\n" \
M_"v[176:179], a[8:11], a[136:139], v[176:179]\n" \
X_"a[8:9], v34 offset:9216\n" \
X_"a[10:11], v34 offset:13824\n" \
M_"v[176:179], a[12:15], a[140:143], v[176:179]\n" \
X_"a[12:13], v34 offset:18432\n" \
X_"a[14:15], v34 offset:23040\n" \
"s_waitcnt vmcnt(18)\n" \
"s_mov_b64 exec, 1\n" \
"buffer_store_dword v39, off, s[56:59], s67 offset:8 sc1\n" \
"s_mov_b64 exec, -1\n" \
"v_readlane_b32 s67, v11, 51\n" \
M_"v[176:179], a[16:19], a[32:35], v[176:179]\n" \
M_"v[176:179], a[20:23], a[36:39], v[176:179]\n" \
M_"v[180:183], a[16:19], a[48:51], v[180:183]\n" \
M_"v[180:183], a[20:23], a[52:55], v[180:183]\n" \
"s_waitcnt vmcnt(18)\n" \
M_"v[248:251], a[16:19], a[192:195], v[248:251]\n" \
"s_waitcnt vmcnt(17)\n" \
M_"v[248:251], a[20:23], a[196:199], v[248:251]\n" \
"s_waitcnt vmcnt(15)\n" \
M_"v[252:255], a[16:19], a[208:211], v[252:255]\n" \
X_"a[16:17], v10 offset:9216\n" \
"s_mov_b64 exec, 1\n" \
"buffer_store_dword v39, off, s[56:59], s68 offset:12 sc1\n" \
"s_mov_b64 exec, -1\n" \
"s_waitcnt vmcnt(15)\n" \
M_"v[252:255], a[20:23], a[212:215], v[252:255]\n" \
"v_readlane_b32 s68, v11, 52\n" \
"s_waitcnt vmcnt(14)\n" \
M_"v[252:255], a[24:27], a[216:219], v[252:255]\n" \
X_"a[18:19], v10 offset:13824\n" \
"s_waitcnt vmcnt(13)\n" \
M_"v[252:255], a[28:31], a[220:223], v[252:255]\n" \
X_"a[20:21], v33 offset:32\n" \
X_"a[22:23], v33 offset:4640\n" \
B_"v[80:83], v20, s[56:59], s96 offen offset:0 sc1\n" \
"s_waitcnt vmcnt(12)\n" \
M_"v[248:251], a[24:27], a[200:203], v[248:251]\n" \
"s_mov_b64 exec, 1\n" \
"buffer_store_dword v39, off, s[56:59], s69 offset:16 sc1\n" \
"s_mov_b64 exec, -1\n" \
"v_readlane_b32 s69, v11, 53\n" \
".fill (L_ab + 40960 - .) / 4, 4, 0xBF800000\n" \
"L_ap10:\n" \
"s_cbranch_execz L_ap0\n" \
"s_waitcnt vmcnt(12)\n" \
M_"v[248:251], a[28:31], a[204:207], v[248:251]\n" \
M_"v[180:183], a[24:27], a[56:59], v[180:183]\n" \
B_"v[84:87], v20, s[56:59], s96 offen offset:1024 sc1\n" \
M_"v[180:183], a[28:31], a[60:63], v[180:183]\n" \
M_"v[176:179], a[24:27], a[40:43], v[176:179]\n" \
X_"a[24:25], v33 offset:9248\n" \
X_"a[26:27], v33 offset:13856\n" \
M_"v[176:179], a[28:31], a[44:47], v[176:179]\n" \
X_"a[28:29], v33 offset:18464\n" \
C_"v248, v248, v249\n" \
C_"v249, v250, v251\n" \
C_"v250, v252, v253\n" \
C_"v251, v254, v255\n" \
"buffer_store_dwordx4 v[248:251], v20, s[56:59], s72 offen offset:1024 sc1\n" \
M_"v[184:187], a[96:99], a[124:127], v[184:187]\n" \
X_"a[30:31], v33 offset:23072\n" \
C_"v176, v176, v177\n" \
C_"v177, v178, v179\n" \
C_"v178, v180, v181\n" \
C_"v179, v182, v183\n" \
M_"v[184:187], a[100:103], a[224:227], v[184:187]\n" \
"buffer_store_dwordx4 v[176:179], v20, s[56:59], s72 offen offset:0 sc1\n" \
M_"v[188:191], a[96:99], a[236:239], v[188:191]\n" \
X_"a[96:97], v33 offset:27680\n" \
X_"a[98:99], v33 offset:32288\n" \
M_"v[188:191], a[100:103], a[240:243], v[188:191]\n" \
X_"a[100:101], v33 offset:36896\n" \
X_"a[102:103], v33 offset:41504\n" \
M_"v[188:191], a[104:107], a[244:247], v[188:191]\n" \
M_"v[188:191], a[108:111], a[248:251], v[188:191]\n" \
M_"v[184:187], a[104:107], a[228:231], v[184:187]\n" \
X_"a[104:105], v33 offset:46112\n" \
X_"a[106:107], v33 offset:50720\n" \
M_"v[184:187], a[108:111], a[232:235], v[184:187]\n" \
X_"a[108:109], v33 offset:55328\n" \
"s_waitcnt lgkmcnt(11)\n" \
M_"v[184:187], a[112:115], a[160:163], v[184:187]\n" \
B_"v[88:91], v32, s[56:59], s96 offen offset:0 sc1\n" \
M_"v[184:187], a[116:119], a[164:167], v[184:187]\n" \
X_"a[110:111], v33 offset:59936\n" \
M_"v[188:191], a[112:115], a[176:179], v[188:191]\n" \
X_"a[112:113], v10 offset:32\n" \
X_"a[114:115], v10 offset:4640\n" \
M_"v[188:191], a[116:119], a[180:183], v[188:191]\n" \
X_"a[116:117], v34 offset:32\n" \
X_"a[118:119], v34 offset:4640\n" \
M_"v[188:191], a[120:123], a[184:187], v[188:191]\n" \
M_"v[188:191], a[0:3], a[188:191], v[188:191]\n" \
B_"v[92:95], v32, s[56:59], s96 offen offset:1024 sc1\n" \
M_"v[184:187], a[120:123], a[168:171], v[184:187]\n" \
X_"a[120:121], v34 offset:9248\n" \
X_"a[122:123], v34 offset:13856\n" \
M_"v[184:187], a[0:3], a[172:175], v[184:187]\n" \
X_"a[0:1], v34 offset:18464\n" \
X_"a[2:3], v34 offset:23072\n" \
M_"v[184:187], a[4:7], a[192:195], v[184:187]\n" \
M_"v[184:187], a[8:11], a[196:199], v[184:187]\n" \
M_"v[188:191], a[4:7], a[208:211], v[188:191]\n" \
X_"a[4:5], v10 offset:9248\n" \
X_"a[6:7], v10 offset:13856\n" \
M_"v[188:191], a[8:11], a[212:215], v[188:191]\n" \
X_"a[8:9], v33 offset:64\n" \
X_"a[10:11], v33 offset:4672\n" \
M_"v[188:191], a[12:15], a[216:219], v[188:191]\n" \
B_"v[96:99], v20, s[56:59], s97 offen offset:0 sc1\n" \
M_"v[188:191], a[16:19], a[220:223], v[188:191]\n" \
M_"v[184:187], a[12:15], a[200:203], v[184:187]\n" \
X_"a[12:13], v33 offset:9280\n" \
X_"a[14:15], v33 offset:13888\n" \
M_"v[184:187], a[16:19], a[204:207], v[184:187]\n" \
"buffer_load_dword v37, v40, s[56:59], s74 offen sc1\n" \
M_"v[192:195], a[20:23], a[124:127], v[192:195]\n" \
X_"a[16:17], v33 offset:18496\n" \
"s_waitcnt lgkmcnt(13)\n" \
M_"v[192:195], a[24:27], a[224:227], v[192:195]\n" \
X_"a[18:19], v33 offset:23104\n" \
M_"v[196:199], a[20:23], a[236:239], v[196:199]\n" \
X_"a[20:21], v33 offset:27712\n" \
C_"v184, v184, v185\n" \
C_"v185, v186, v187\n" \
C_"v186, v188, v189\n" \
C_"v187, v190, v191\n" \
"buffer_store_dwordx4 v[184:187], v20, s[56:59], s92 offen offset:1024 sc1\n" \
"s_waitcnt vmcnt(18)\n" \
"s_mov_b64 exec, 1\n" \
"buffer_store_dword v39, off, s[56:59], s70 offset:20 sc1\n" \
"s_mov_b64 exec, -1\n" \
"s_add_u32 s92, s92, 0x3800\n" \
M_"v[196:199], a[24:27], a[240:243], v[196:199]\n" \
X_"a[22:23], v33 offset:32320\n" \
X_"a[24:25], v33 offset:36928\n" \
M_"v[196:199], a[28:31], a[244:247], v[196:199]\n" \
X_"a[26:27], v33 offset:41536\n" \
M_"v[196:199], a[96:99], a[248:251], v[196:199]\n" \
M_"v[192:195], a[28:31], a[228:231], v[192:195]\n" \
X_"a[28:29], v33 offset:46144\n" \
X_"a[30:31], v33 offset:50752\n" \
M_"v[192:195], a[96:99], a[232:235], v[192:195]\n" \
B_"v[100:103], v20, s[56:59], s97 offen offset:1024 sc1\n" \
M_"v[192:195], a[100:103], a[160:163], v[192:195]\n" \
X_"a[96:97], v33 offset:55360\n" \
X_"a[98:99], v33 offset:59968\n" \
M_"v[192:195], a[104:107], a[164:167], v[192:195]\n" \
M_"v[196:199], a[100:103], a[176:179], v[196:199]\n" \
X_"a[100:101], v10 offset:64\n" \
X_"a[102:103], v10 offset:4672\n" \
M_"v[196:199], a[104:107], a[180:183], v[196:199]\n" \
X_"a[104:105], v34 offset:64\n" \
X_"a[106:107], v34 offset:4672\n" \
M_"v[196:199], a[108:111], a[184:187], v[196:199]\n" \
B_"v[104:107], v32, s[56:59], s97 offen offset:0 sc1\n" \
M_"v[196:199], a[112:115], a[188:191], v[196:199]\n" \
M_"v[192:195], a[108:111], a[168:171], v[192:195]\n" \
X_"a[108:109], v34 offset:9280\n" \
X_"a[110:111], v34 offset:13888\n" \
M_"v[192:195], a[112:115], a[172:175], v[192:195]\n" \
X_"a[112:113], v34 offset:18496\n" \
"s_waitcnt lgkmcnt(14)\n" \
M_"v[192:195], a[116:119], a[192:195], v[192:195]\n" \
X_"a[114:115], v34 offset:23104\n" \
M_"v[192:195], a[120:123], a[196:199], v[192:195]\n" \
B_"v[108:111], v32, s[56:59], s97 offen offset:1024 sc1\n" \
M_"v[196:199], a[116:119], a[208:211], v[196:199]\n" \
X_"a[116:117], v10 offset:9280\n" \
X_"a[118:119], v10 offset:13888\n" \
M_"v[196:199], a[120:123], a[212:215], v[196:199]\n" \
X_"a[120:121], v33 offset:96\n" \
X_"a[122:123], v33 offset:4704\n" \
"s_waitcnt vmcnt(19)\n" \
"s_mov_b64 exec, 1\n" \
"buffer_store_dword v39, off, s[56:59], s71 offset:24 sc1\n" \
"s_mov_b64 exec, -1\n" \
M_"v[196:199], a[0:3], a[216:219], v[196:199]\n" \
M_"v[196:199], a[4:7], a[220:223], v[196:199]\n" \
M_"v[192:195], a[0:3], a[200:203], v[192:195]\n" \
X_"a[0:1], v33 offset:9312\n" \
X_"a[2:3], v33 offset:13920\n" \
M_"v[192:195], a[4:7], a[204:207], v[192:195]\n" \
X_"a[4:5], v33 offset:18528\n" \
X_"a[6:7], v33 offset:23136\n" \
M_"v[200:203], a[8:11], a[124:127], v[200:203]\n" \
M_"v[200:203], a[12:15], a[224:227], v[200:203]\n" \
M_"v[204:207], a[8:11], a[236:239], v[204:207]\n" \
X_"a[8:9], v33 offset:27744\n" \
X_"a[10:11], v33 offset:32352\n" \
M_"v[204:207], a[12:15], a[240:243], v[204:207]\n" \
C_"v192, v192, v193\n" \
C_"v193, v194, v195\n" \
C_"v194, v196, v197\n" \
C_"v195, v198, v199\n" \
"buffer_store_dwordx4 v[192:195], v20, s[56:59], s66 offen offset:1024 sc1\n" \
M_"v[204:207], a[16:19], a[244:247], v[204:207]\n" \
X_"a[12:13], v33 offset:36960\n" \
"s_waitcnt lgkmcnt(11)\n" \
M_"v[204:207], a[20:23], a[248:251], v[204:207]\n" \
X_"a[14:15], v33 offset:41568\n" \
M_"v[200:203], a[16:19], a[228:231], v[200:203]\n" \
X_"a[16:17], v33 offset:46176\n" \
X_"a[18:19], v33 offset:50784\n" \
M_"v[200:203], a[20:23], a[232:235], v[200:203]\n" \
X_"a[20:21], v33 offset:55392\n" \
X_"a[22:23], v33 offset:60000\n" \
M_"v[200:203], a[24:27], a[160:163], v[200:203]\n" \
M_"v[200:203], a[28:31], a[164:167], v[200:203]\n" \
M_"v[204:207], a[24:27], a[176:179], v[204:207]\n" \
X_"a[24:25], v10 offset:96\n" \
X_"a[26:27], v10 offset:4704\n" \
M_"v[204:207], a[28:31], a[180:183], v[204:207]\n" \
X_"a[28:29], v34 offset:96\n" \
X_"a[30:31], v34 offset:4704\n" \
M_"v[204:207], a[96:99], a[184:187], v[204:207]\n" \
M_"v[204:207], a[100:103], a[188:191], v[204:207]\n" \
M_"v[200:203], a[96:99], a[168:171], v[200:203]\n" \
X_"a[96:97], v34 offset:9312\n" \
X_"a[98:99], v34 offset:13920\n" \
M_"v[200:203], a[100:103], a[172:175], v[200:203]\n" \
X_"a[100:101], v34 offset:18528\n" \
X_"a[102:103], v34 offset:23136\n" \
M_"v[200:203], a[104:107], a[192:195], v[200:203]\n" \
M_"v[200:203], a[108:111], a[196:199], v[200:203]\n" \
M_"v[204:207], a[104:107], a[208:211], v[204:207]\n" \
X_"a[104:105], v10 offset:9312\n" \
X_"a[106:107], v10 offset:13920\n" \
M_"v[204:207], a[108:111], a[212:215], v[204:207]\n" \
X_"a[108:109], v33 offset:128\n" \
X_"a[110:111], v33 offset:4736\n" \
M_"v[204:207], a[112:115], a[216:219], v[204:207]\n" \
"s_waitcnt lgkmcnt(15)\n" \
M_"v[204:207], a[116:119], a[220:223], v[204:207]\n" \
M_"v[200:203], a[112:115], a[200:203], v[200:203]\n" \
X_"a[112:113], v33 offset:9344\n" \
X_"a[114:115], v33 offset:13952\n" \
M_"v[200:203], a[116:119], a[204:207], v[200:203]\n" \
X_"a[116:117], v33 offset:18560\n" \
X_"a[118:119], v33 offset:23168\n" \
M_"v[208:211], a[120:123], a[124:127], v[208:211]\n" \
M_"v[208:211], a[0:3], a[224:227], v[208:211]\n" \
M_"v[212:215], a[120:123], a[236:239], v[212:215]\n" \
X_"a[120:121], v33 offset:27776\n" \
X_"a[122:123], v33 offset:32384\n" \
M_"v[212:215], a[0:3], a[240:243], v[212:215]\n" \
C_"v200, v200, v201\n" \
C_"v201, v202, v203\n" \
C_"v202, v204, v205\n" \
C_"v203, v206, v207\n" \
"buffer_store_dwordx4 v[200:203], v20, s[56:59], s67 offen offset:1024 sc1\n" \
M_"v[212:215], a[4:7], a[244:247], v[212:215]\n" \
X_"a[0:1], v33 offset:36992\n" \
X_"a[2:3], v33 offset:41600\n" \
M_"v[212:215], a[8:11], a[248:251], v[212:215]\n" \
M_"v[208:211], a[4:7], a[228:231], v[208:211]\n" \
X_"a[4:5], v33 offset:46208\n" \
X_"a[6:7], v33 offset:50816\n" \
M_"v[208:211], a[8:11], a[232:235], v[208:211]\n" \
X_"a[8:9], v33 offset:55424\n" \
X_"a[10:11], v33 offset:60032\n" \
M_"v[208:211], a[12:15], a[160:163], v[208:211]\n" \
"s_waitcnt lgkmcnt(13)\n" \
M_"v[208:211], a[16:19], a[164:167], v[208:211]\n" \
M_"v[212:215], a[12:15], a[176:179], v[212:215]\n" \
"s_waitcnt vmcnt(8)\n" \
"v_cmp_eq_u32_e64 s[98:99], s62, v37\n" \
"s_mov_b64 exec, 3\n" \
"buffer_store_dword v39, v41, s[56:59], s73 offen sc1\n" \
"s_mov_b64 exec, -1\n" \
"s_andn2_b32 s75, 0x7f, s98\n" \
"s_mov_b32 s20, s75\n" \
"s_bitcmp1_b32 s20, 0\n" \
"s_cselect_b32 s93, 0x40000000, s12\n" \
"s_bitcmp1_b32 s20, 1\n" \
"s_cselect_b32 s94, 0x40000000, s12\n" \
"s_bitcmp1_b32 s20, 2\n" \
"s_cselect_b32 s95, 0x40000000, s13\n" \
"s_bitcmp1_b32 s20, 3\n" \
"s_cselect_b32 s96, 0x40000000, s13\n" \
"s_bitcmp1_b32 s20, 4\n" \
"s_cselect_b32 s97, 0x40000000, s14\n" \
"s_bitcmp1_b32 s20, 5\n" \
"s_cselect_b32 s64, 0x40000000, s14\n" \
"s_bitcmp1_b32 s20, 6\n" \
"s_cselect_b32 s65, 0x40000000, s15\n" \
"s_add_u32 s66, s66, 0x3000\n" \
"s_add_u32 s67, s67, 0x2800\n" \
M_"v[212:215], a[16:19], a[180:183], v[212:215]\n" \
B_"v[116:119], v20, s[56:59], s93 offen offset:0 sc1\n" \
M_"v[212:215], a[20:23], a[184:187], v[212:215]\n" \
X_"a[12:13], v10 offset:128\n" \
X_"a[14:15], v10 offset:4736\n" \
M_"v[212:215], a[24:27], a[188:191], v[212:215]\n" \
X_"a[16:17], v34 offset:128\n" \
X_"a[18:19], v34 offset:4736\n" \
M_"v[208:211], a[20:23], a[168:171], v[208:211]\n" \
X_"a[20:21], v34 offset:9344\n" \
X_"a[22:23], v34 offset:13952\n" \
M_"v[208:211], a[24:27], a[172:175], v[208:211]\n" \
X_"a[24:25], v34 offset:18560\n" \
X_"a[26:27], v34 offset:23168\n" \
M_"v[208:211], a[28:31], a[192:195], v[208:211]\n" \
M_"v[208:211], a[96:99], a[196:199], v[208:211]\n" \
M_"v[212:215], a[28:31], a[208:211], v[212:215]\n" \
X_"a[28:29], v10 offset:9344\n" \
X_"a[30:31], v10 offset:13952\n" \
M_"v[212:215], a[96:99], a[212:215], v[212:215]\n" \
X_"a[96:97], v33 offset:160\n" \
X_"a[98:99], v33 offset:4768\n" \
M_"v[212:215], a[100:103], a[216:219], v[212:215]\n" \
M_"v[212:215], a[104:107], a[220:223], v[212:215]\n" \
M_"v[208:211], a[100:103], a[200:203], v[208:211]\n" \
X_"a[100:101], v33 offset:9376\n" \
X_"a[102:103], v33 offset:13984\n" \
M_"v[208:211], a[104:107], a[204:207], v[208:211]\n" \
X_"a[104:105], v33 offset:18592\n" \
"s_waitcnt lgkmcnt(15)\n" \
M_"v[216:219], a[108:111], a[124:127], v[216:219]\n" \
X_"a[106:107], v33 offset:23200\n" \
M_"v[216:219], a[112:115], a[224:227], v[216:219]\n" \
B_"v[124:127], v20, s[56:59], s94 offen offset:2048 sc1\n" \
M_"v[220:223], a[108:111], a[236:239], v[220:223]\n" \
X_"a[108:109], v33 offset:27808\n" \
C_"v208, v208, v209\n" \
C_"v209, v210, v211\n" \
C_"v210, v212, v213\n" \
C_"v211, v214, v215\n" \
M_"v[220:223], a[112:115], a[240:243], v[220:223]\n" \
"buffer_store_dwordx4 v[208:211], v20, s[56:59], s68 offen offset:1024 sc1\n" \
"s_add_u32 s68, s68, 0x2000\n" \
M_"v[220:223], a[116:119], a[244:247], v[220:223]\n" \
X_"a[110:111], v33 offset:32416\n" \
X_"a[112:113], v33 offset:37024\n" \
M_"v[220:223], a[120:123], a[248:251], v[220:223]\n" \
X_"a[114:115], v33 offset:41632\n" \
M_"v[216:219], a[116:119], a[228:231], v[216:219]\n" \
X_"a[116:117], v33 offset:46240\n" \
X_"a[118:119], v33 offset:50848\n" \
M_"v[216:219], a[120:123], a[232:235], v[216:219]\n" \
X_"a[120:121], v33 offset:55456\n" \
X_"a[122:123], v33 offset:60064\n" \
M_"v[216:219], a[0:3], a[160:163], v[216:219]\n" \
M_"v[216:219], a[4:7], a[164:167], v[216:219]\n" \
M_"v[220:223], a[0:3], a[176:179], v[220:223]\n" \
X_"a[0:1], v10 offset:160\n" \
X_"a[2:3], v10 offset:4768\n" \
M_"v[220:223], a[4:7], a[180:183], v[220:223]\n" \
X_"a[4:5], v34 offset:160\n" \
X_"a[6:7], v34 offset:4768\n" \
M_"v[220:223], a[8:11], a[184:187], v[220:223]\n" \
"s_waitcnt lgkmcnt(13)\n" \
M_"v[220:223], a[12:15], a[188:191], v[220:223]\n" \
"s_waitcnt vmcnt(11)\n" \
"s_mov_b64 exec, 1\n" \
"buffer_store_dword v39, off, s[56:59], s92 offset:32 sc1\n" \
"s_mov_b64 exec, -1\n" \
M_"v[216:219], a[8:11], a[168:171], v[216:219]\n" \
X_"a[8:9], v34 offset:9376\n" \
X_"a[10:11], v34 offset:13984\n" \
M_"v[216:219], a[12:15], a[172:175], v[216:219]\n" \
X_"a[12:13], v34 offset:18592\n" \
X_"a[14:15], v34 offset:23200\n" \
M_"v[216:219], a[16:19], a[192:195], v[216:219]\n" \
M_"v[216:219], a[20:23], a[196:199], v[216:219]\n" \
B_"v[132:135], v20, s[56:59], s95 offen offset:0 sc1\n" \
M_"v[220:223], a[16:19], a[208:211], v[220:223]\n" \
X_"a[16:17], v10 offset:9376\n" \
X_"a[18:19], v10 offset:13984\n" \
M_"v[220:223], a[20:23], a[212:215], v[220:223]\n" \
X_"a[20:21], v33 offset:192\n" \
X_"a[22:23], v33 offset:4800\n" \
M_"v[220:223], a[24:27], a[216:219], v[220:223]\n" \
M_"v[220:223], a[28:31], a[220:223], v[220:223]\n" \
M_"v[216:219], a[24:27], a[200:203], v[216:219]\n" \
X_"a[24:25], v33 offset:9408\n" \
X_"a[26:27], v33 offset:14016\n" \
M_"v[216:219], a[28:31], a[204:207], v[216:219]\n" \
"buffer_load_dword v37, v40, s[56:59], s74 offen sc1\n" \
M_"v[224:227], a[96:99], a[124:127], v[224:227]\n" \
X_"a[28:29], v33 offset:18624\n" \
X_"a[30:31], v33 offset:23232\n" \
M_"v[224:227], a[100:103], a[224:227], v[224:227]\n" \
M_"v[228:231], a[96:99], a[236:239], v[228:231]\n" \
X_"a[96:97], v33 offset:27840\n" \
X_"a[98:99], v33 offset:32448\n" \
C_"v216, v216, v217\n" \
C_"v217, v218, v219\n" \
C_"v218, v220, v221\n" \
C_"v219, v222, v223\n" \
"buffer_store_dwordx4 v[216:219], v20, s[56:59], s69 offen offset:1024 sc1\n" \
"s_add_u32 s69, s69, 0x1800\n" \
M_"v[228:231], a[100:103], a[240:243], v[228:231]\n" \
X_"a[100:101], v33 offset:37056\n" \
"s_waitcnt lgkmcnt(12)\n" \
M_"v[228:231], a[104:107], a[244:247], v[228:231]\n" \
X_"a[102:103], v33 offset:41664\n" \
M_"v[228:231], a[108:111], a[248:251], v[228:231]\n" \
M_"v[224:227], a[104:107], a[228:231], v[224:227]\n" \
B_"v[140:143], v20, s[56:59], s96 offen offset:2048 sc1\n" \
M_"v[224:227], a[108:111], a[232:235], v[224:227]\n" \
X_"a[104:105], v33 offset:46272\n" \
X_"a[106:107], v33 offset:50880\n" \
M_"v[224:227], a[112:115], a[160:163], v[224:227]\n" \
X_"a[108:109], v33 offset:55488\n" \
X_"a[110:111], v33 offset:60096\n" \
M_"v[224:227], a[116:119], a[164:167], v[224:227]\n" \
M_"v[228:231], a[112:115], a[176:179], v[228:231]\n" \
X_"a[112:113], v10 offset:192\n" \
X_"a[114:115], v10 offset:4800\n" \
M_"v[228:231], a[116:119], a[180:183], v[228:231]\n" \
X_"a[116:117], v34 offset:192\n" \
X_"a[118:119], v34 offset:4800\n" \
M_"v[228:231], a[120:123], a[184:187], v[228:231]\n" \
M_"v[228:231], a[0:3], a[188:191], v[228:231]\n" \
M_"v[224:227], a[120:123], a[168:171], v[224:227]\n" \
X_"a[120:121], v34 offset:9408\n" \
X_"a[122:123], v34 offset:14016\n" \
M_"v[224:227], a[0:3], a[172:175], v[224:227]\n" \
X_"a[0:1], v34 offset:18624\n" \
X_"a[2:3], v34 offset:23232\n" \
"s_waitcnt vmcnt(10)\n" \
"s_mov_b64 exec, 1\n" \
"buffer_store_dword v39, off, s[56:59], s66 offset:36 sc1\n" \
"s_mov_b64 exec, -1\n" \
M_"v[224:227], a[4:7], a[192:195], v[224:227]\n" \
M_"v[224:227], a[8:11], a[196:199], v[224:227]\n" \
M_"v[228:231], a[4:7], a[208:211], v[228:231]\n" \
X_"a[4:5], v10 offset:9408\n" \
X_"a[6:7], v10 offset:14016\n" \
M_"v[228:231], a[8:11], a[212:215], v[228:231]\n" \
X_"a[8:9], v33 offset:224\n" \
"s_waitcnt lgkmcnt(15)\n" \
M_"v[228:231], a[12:15], a[216:219], v[228:231]\n" \
B_"v[148:151], v20, s[56:59], s97 offen offset:0 sc1\n" \
M_"v[228:231], a[16:19], a[220:223], v[228:231]\n" \
X_"a[10:11], v33 offset:4832\n" \
M_"v[224:227], a[12:15], a[200:203], v[224:227]\n" \
X_"a[12:13], v33 offset:9440\n" \
X_"a[14:15], v33 offset:14048\n" \
M_"v[224:227], a[16:19], a[204:207], v[224:227]\n" \
X_"a[16:17], v33 offset:18656\n" \
X_"a[18:19], v33 offset:23264\n" \
M_"v[232:235], a[20:23], a[124:127], v[232:235]\n" \
M_"v[232:235], a[24:27], a[224:227], v[232:235]\n" \
M_"v[236:239], a[20:23], a[236:239], v[236:239]\n" \
X_"a[20:21], v33 offset:27872\n" \
X_"a[22:23], v33 offset:32480\n" \
M_"v[236:239], a[24:27], a[240:243], v[236:239]\n" \
C_"v224, v224, v225\n" \
C_"v225, v226, v227\n" \
C_"v226, v228, v229\n" \
C_"v227, v230, v231\n" \
M_"v[236:239], a[28:31], a[244:247], v[236:239]\n" \
X_"a[24:25], v33 offset:37088\n" \
X_"a[26:27], v33 offset:41696\n" \
M_"v[236:239], a[96:99], a[248:251], v[236:239]\n" \
M_"v[232:235], a[28:31], a[228:231], v[232:235]\n" \
X_"a[28:29], v33 offset:46304\n" \
X_"a[30:31], v33 offset:50912\n" \
M_"v[232:235], a[96:99], a[232:235], v[232:235]\n" \
X_"a[96:97], v33 offset:55520\n" \
X_"a[98:99], v33 offset:60128\n" \
M_"v[232:235], a[100:103], a[160:163], v[232:235]\n" \
"s_waitcnt lgkmcnt(14)\n" \
M_"v[232:235], a[104:107], a[164:167], v[232:235]\n" \
B_"v[156:159], v20, s[56:59], s64 offen offset:2048 sc1\n" \
"v_readlane_b32 s64, v11, 54\n" \
M_"v[236:239], a[100:103], a[176:179], v[236:239]\n" \
X_"a[100:101], v10 offset:224\n" \
X_"a[102:103], v10 offset:4832\n" \
M_"v[236:239], a[104:107], a[180:183], v[236:239]\n" \
X_"a[104:105], v34 offset:224\n" \
X_"a[106:107], v34 offset:4832\n" \
"buffer_store_dwordx4 v[224:227], v20, s[56:59], s64 offen offset:1024 sc1\n" \
M_"v[236:239], a[108:111], a[184:187], v[236:239]\n" \
M_"v[236:239], a[112:115], a[188:191], v[236:239]\n" \
M_"v[232:235], a[108:111], a[168:171], v[232:235]\n" \
X_"a[108:109], v34 offset:9440\n" \
X_"a[110:111], v34 offset:14048\n" \
"s_waitcnt vmcnt(13)\n" \
"s_mov_b64 exec, 1\n" \
"buffer_store_dword v39, off, s[56:59], s67 offset:40 sc1\n" \
"s_mov_b64 exec, -1\n" \
M_"v[232:235], a[112:115], a[172:175], v[232:235]\n" \
X_"a[112:113], v34 offset:18656\n" \
X_"a[114:115], v34 offset:23264\n" \
M_"v[232:235], a[116:119], a[192:195], v[232:235]\n" \
M_"v[232:235], a[120:123], a[196:199], v[232:235]\n" \
M_"v[236:239], a[116:119], a[208:211], v[236:239]\n" \
X_"a[116:117], v10 offset:9440\n" \
X_"a[118:119], v10 offset:14048\n" \
M_"v[236:239], a[120:123], a[212:215], v[236:239]\n" \
X_"a[120:121], v36 offset:0\n" \
X_"a[122:123], v36 offset:1024\n" \
M_"v[236:239], a[0:3], a[216:219], v[236:239]\n" \
M_"v[236:239], a[4:7], a[220:223], v[236:239]\n" \
M_"v[232:235], a[0:3], a[200:203], v[232:235]\n" \
X_"a[0:1], v36 offset:2048\n" \
X_"a[2:3], v36 offset:3072\n" \
M_"v[232:235], a[4:7], a[204:207], v[232:235]\n" \
X_"a[4:5], v36 offset:32\n" \
B_"v[164:167], v20, s[56:59], s65 offen offset:0 sc1\n" \
"v_readlane_b32 s65, v11, 55\n" \
"s_waitcnt lgkmcnt(11)\n" \
M_"v[240:243], a[8:11], a[124:127], v[240:243]\n" \
X_"a[6:7], v36 offset:1056\n" \
M_"v[240:243], a[12:15], a[224:227], v[240:243]\n" \
M_"v[244:247], a[8:11], a[236:239], v[244:247]\n" \
C_"v232, v232, v233\n" \
C_"v233, v234, v235\n" \
C_"v234, v236, v237\n" \
C_"v235, v238, v239\n" \
"buffer_store_dwordx4 v[232:235], v20, s[56:59], s65 offen offset:1024 sc1\n" \
M_"v[244:247], a[12:15], a[240:243], v[244:247]\n" \
X_"a[8:9], v36 offset:2080\n" \
X_"a[10:11], v36 offset:3104\n" \
M_"v[244:247], a[16:19], a[244:247], v[244:247]\n" \
M_"v[244:247], a[20:23], a[248:251], v[244:247]\n" \
M_"v[240:243], a[16:19], a[228:231], v[240:243]\n" \
M_"v[240:243], a[20:23], a[232:235], v[240:243]\n" \
M_"v[168:171], a[24:27], a[128:131], v[168:171]\n" \
M_"v[168:171], a[28:31], a[132:135], v[168:171]\n" \
M_"v[172:175], a[24:27], a[144:147], v[172:175]\n" \
"s_waitcnt vmcnt(9)\n" \
"v_cmp_eq_u32_e64 s[98:99], s62, v37\n" \
"buffer_load_dword v37, v40, s[56:59], s74 offen sc1\n" \
"s_mov_b64 exec, 1\n" \
"buffer_store_dword v39, off, s[56:59], s68 offset:44 sc1\n" \
"s_mov_b64 exec, -1\n" \
"s_or_b32 s75, 0x300, s100\n" \
"s_andn2_b32 s75, s75, s98\n" \
"s_mov_b32 s21, s75\n" \
"s_bitcmp1_b32 s21, 16\n" \
"s_cselect_b32 s70, 0x40000000, s16\n" \
"s_bitcmp1_b32 s21, 17\n" \
"s_cselect_b32 s71, 0x40000000, s16\n" \
"s_bitcmp1_b32 s21, 18\n" \
"s_cselect_b32 s72, 0x40000000, s17\n" \
"s_bitcmp1_b32 s21, 19\n" \
"s_cselect_b32 s73, 0x40000000, s17\n" \
"s_bitcmp1_b32 s21, 20\n" \
"s_cselect_b32 s92, 0x40000000, s18\n" \
"s_bitcmp1_b32 s21, 21\n" \
"s_cselect_b32 s93, 0x40000000, s18\n" \
"s_bitcmp1_b32 s21, 22\n" \
"s_cselect_b32 s94, 0x40000000, s19\n" \
"s_bitcmp1_b32 s21, 23\n" \
"s_cselect_b32 s95, 0x40000000, s19\n" \
"s_bitcmp1_b32 s21, 8\n" \
"s_cselect_b32 s96, 0x40000000, s12\n" \
"s_bitcmp1_b32 s21, 9\n" \
"s_cselect_b32 s97, 0x40000000, s12\n" \
"s_add_u32 s64, s64, 0x1000\n" \
"s_add_u32 s65, s65, 0x800\n" \
M_"v[172:175], a[28:31], a[148:151], v[172:175]\n" \
B_"v[112:115], v20, s[56:59], s70 offen offset:0 sc1\n" \
M_"v[240:243], a[24:27], a[160:163], v[240:243]\n" \
M_"v[240:243], a[28:31], a[164:167], v[240:243]\n" \
M_"v[244:247], a[24:27], a[176:179], v[244:247]\n" \
M_"v[244:247], a[28:31], a[180:183], v[244:247]\n" \
B_"v[184:187], v20, s[56:59], s71 offen offset:2048 sc1\n" \
M_"v[244:247], a[96:99], a[184:187], v[244:247]\n" \
"v_readlane_b32 s71, v11, 57\n" \
M_"v[244:247], a[100:103], a[188:191], v[244:247]\n" \
M_"v[240:243], a[96:99], a[168:171], v[240:243]\n" \
M_"v[240:243], a[100:103], a[172:175], v[240:243]\n" \
B_"v[120:123], v20, s[56:59], s72 offen offset:0 sc1\n" \
M_"v[172:175], a[96:99], a[152:155], v[172:175]\n" \
"v_readlane_b32 s72, v11, 48\n" \
M_"v[172:175], a[100:103], a[156:159], v[172:175]\n" \
M_"v[168:171], a[96:99], a[136:139], v[168:171]\n" \
M_"v[168:171], a[100:103], a[140:143], v[168:171]\n" \
B_"v[192:195], v20, s[56:59], s73 offen offset:2048 sc1\n" \
M_"v[168:171], a[104:107], a[32:35], v[168:171]\n" \
"v_readlane_b32 s73, v11, 46\n" \
"s_waitcnt lgkmcnt(0)\n" \
M_"v[168:171], a[108:111], a[36:39], v[168:171]\n" \
M_"v[172:175], a[104:107], a[48:51], v[172:175]\n" \
M_"v[172:175], a[108:111], a[52:55], v[172:175]\n" \
B_"v[128:131], v20, s[56:59], s92 offen offset:0 sc1\n" \
M_"v[240:243], a[104:107], a[192:195], v[240:243]\n" \
"v_readlane_b32 s92, v11, 47\n" \
M_"v[240:243], a[108:111], a[196:199], v[240:243]\n" \
M_"v[244:247], a[104:107], a[208:211], v[244:247]\n" \
M_"v[244:247], a[108:111], a[212:215], v[244:247]\n" \
B_"v[200:203], v20, s[56:59], s93 offen offset:2048 sc1\n" \
M_"v[244:247], a[112:115], a[216:219], v[244:247]\n" \
M_"v[244:247], a[116:119], a[220:223], v[244:247]\n" \
M_"v[240:243], a[112:115], a[200:203], v[240:243]\n" \
M_"v[240:243], a[116:119], a[204:207], v[240:243]\n" \
".fill (L_ab + 45056 - .) / 4, 4, 0xBF800000\n" \
"L_ap11:\n" \
"s_cbranch_execz L_ap0\n" \
B_"v[176:179], v20, s[56:59], s94 offen offset:0 sc1\n" \
"s_waitcnt vmcnt(17)\n" \
"s_mov_b64 exec, 1\n" \
"buffer_store_dword v39, off, s[56:59], s69 offset:48 sc1\n" \
"s_mov_b64 exec, -1\n" \
M_"v[172:175], a[112:115], a[56:59], v[172:175]\n" \
M_"v[172:175], a[116:119], a[60:63], v[172:175]\n" \
M_"v[168:171], a[112:115], a[40:43], v[168:171]\n" \
M_"v[168:171], a[116:119], a[44:47], v[168:171]\n" \
B_"v[248:251], v20, s[56:59], s95 offen offset:2048 sc1\n" \
M_"v[168:171], a[120:123], v[64:67], v[168:171]\n" \
M_"v[172:175], a[120:123], v[72:75], v[172:175]\n" \
M_"v[240:243], a[120:123], v[80:83], v[240:243]\n" \
M_"v[244:247], a[120:123], v[88:91], v[244:247]\n" \
B_"v[188:191], v20, s[56:59], s96 offen offset:1024 sc1\n" \
M_"v[168:171], a[0:3], v[68:71], v[168:171]\n" \
M_"v[172:175], a[0:3], v[76:79], v[172:175]\n" \
M_"v[240:243], a[0:3], v[84:87], v[240:243]\n" \
M_"v[244:247], a[0:3], v[92:95], v[244:247]\n" \
B_"v[196:199], v20, s[56:59], s97 offen offset:3072 sc1\n" \
M_"v[56:59], a[4:7], v[96:99], 0\n" \
M_"v[60:63], a[4:7], v[104:107], 0\n" \
M_"v[56:59], a[8:11], v[100:103], v[56:59]\n" \
M_"v[60:63], a[8:11], v[108:111], v[60:63]\n" \
"s_waitcnt vmcnt(16)\n" \
"s_mov_b64 exec, 1\n" \
"buffer_store_dword v39, off, s[56:59], s64 offset:52 sc1\n" \
"s_mov_b64 exec, -1\n" \
"s_waitcnt vmcnt(14)\n" \
"s_mov_b64 exec, 1\n" \
"buffer_store_dword v39, off, s[56:59], s65 offset:56 sc1\n" \
"s_mov_b64 exec, -1\n" \
"s_cmp_lg_u32 s20, 0\n" \
"s_cbranch_scc1 L_sna_%=\n" \
"L_rna_%=:\n" \
"s_cmp_lg_u32 s21, 0\n" \
"s_cbranch_scc1 L_snx_%=\n" \
"L_rnx_%=:\n" \
"s_waitcnt vmcnt(14)\n" \
"v_cmp_eq_u32_e64 s[98:99], s62, v37\n" \
M_"v[168:171], v[50:53], v[116:119], v[168:171]\n" \
"s_andn2_b32 s75, 0x400, s98\n" \
"s_cbranch_scc1 L_sx0_%=\n" \
"L_rx0_%=:\n" \
"s_add_u32 s66, s63, 0x1000\n" \
M_"v[172:175], v[48:51], v[116:119], v[172:175]\n" \
B_"v[204:207], v20, s[56:59], s66 offen offset:1024 sc1\n" \
"s_andn2_b32 s75, 0x800, s98\n" \
"s_cbranch_scc1 L_sx1_%=\n" \
"L_rx1_%=:\n" \
"s_add_u32 s67, s63, 0x1800\n" \
M_"v[168:171], v[50:53], v[124:127], v[168:171]\n" \
M_"v[172:175], v[48:51], v[124:127], v[172:175]\n" \
M_"v[168:171], v[50:53], v[132:135], v[168:171]\n" \
B_"v[212:215], v20, s[56:59], s67 offen offset:1024 sc1\n" \
"s_andn2_b32 s75, 0x1000, s98\n" \
"s_cbranch_scc1 L_sx2_%=\n" \
"L_rx2_%=:\n" \
"s_add_u32 s68, s63, 0x2000\n" \
M_"v[172:175], v[48:51], v[132:135], v[172:175]\n" \
M_"v[168:171], v[50:53], v[140:143], v[168:171]\n" \
M_"v[172:175], v[48:51], v[140:143], v[172:175]\n" \
B_"v[220:223], v20, s[56:59], s68 offen offset:1024 sc1\n" \
"s_andn2_b32 s75, 0x2000, s98\n" \
"s_cbranch_scc1 L_sx3_%=\n" \
"L_rx3_%=:\n" \
"s_add_u32 s69, s63, 0x2800\n" \
M_"v[168:171], v[50:53], v[148:151], v[168:171]\n" \
M_"v[172:175], v[48:51], v[148:151], v[172:175]\n" \
M_"v[168:171], v[50:53], v[156:159], v[168:171]\n" \
B_"v[228:231], v20, s[56:59], s69 offen offset:1024 sc1\n" \
"s_andn2_b32 s75, 0x4000, s98\n" \
"s_cbranch_scc1 L_sx4_%=\n" \
"L_rx4_%=:\n" \
"s_add_u32 s70, s63, 0x3000\n" \
M_"v[172:175], v[48:51], v[156:159], v[172:175]\n" \
M_"v[168:171], v[50:53], v[164:167], v[168:171]\n" \
M_"v[172:175], v[48:51], v[164:167], v[172:175]\n" \
B_"v[236:239], v20, s[56:59], s70 offen offset:1024 sc1\n" \
"s_waitcnt vmcnt(17)\n" \
M_"v[56:59], v[50:53], v[112:115], v[56:59]\n" \
M_"v[60:63], v[48:51], v[112:115], v[60:63]\n" \
"s_waitcnt vmcnt(16)\n" \
M_"v[56:59], v[50:53], v[184:187], v[56:59]\n" \
M_"v[60:63], v[48:51], v[184:187], v[60:63]\n" \
C_"v168, v168, v169\n" \
"s_waitcnt vmcnt(15)\n" \
M_"v[56:59], v[50:53], v[120:123], v[56:59]\n" \
C_"v172, v172, v173\n" \
C_"v169, v170, v171\n" \
M_"v[60:63], v[48:51], v[120:123], v[60:63]\n" \
C_"v173, v174, v175\n" \
"s_waitcnt vmcnt(14)\n" \
M_"v[56:59], v[50:53], v[192:195], v[56:59]\n" \
"buffer_store_dwordx2 v[168:169], v42, s[88:91], s73 offen sc1\n" \
M_"v[60:63], v[48:51], v[192:195], v[60:63]\n" \
"s_waitcnt vmcnt(14)\n" \
M_"v[56:59], v[50:53], v[128:131], v[56:59]\n" \
"buffer_store_dwordx2 v[172:173], v43, s[88:91], s73 offen sc1\n" \
M_"v[60:63], v[48:51], v[128:131], v[60:63]\n" \
"s_waitcnt vmcnt(14)\n" \
M_"v[56:59], v[50:53], v[200:203], v[56:59]\n" \
M_"v[60:63], v[48:51], v[200:203], v[60:63]\n" \
"s_waitcnt vmcnt(13)\n" \
M_"v[56:59], v[50:53], v[176:179], v[56:59]\n" \
M_"v[60:63], v[48:51], v[176:179], v[60:63]\n" \
"s_waitcnt vmcnt(11)\n" \
M_"v[56:59], v[50:53], v[248:251], v[56:59]\n" \
M_"v[60:63], v[48:51], v[248:251], v[60:63]\n" \
"s_waitcnt vmcnt(10)\n" \
M_"v[240:243], v[50:53], v[188:191], v[240:243]\n" \
M_"v[244:247], v[48:51], v[188:191], v[244:247]\n" \
"s_waitcnt vmcnt(9)\n" \
M_"v[240:243], v[50:53], v[196:199], v[240:243]\n" \
M_"v[244:247], v[48:51], v[196:199], v[244:247]\n" \
"s_mov_b64 exec, 1\n" \
"buffer_store_dword v46, off, s[56:59], s71 offset:0 sc1\n" \
"s_mov_b64 exec, -1\n" \
C_"v56, v56, v57\n" \
C_"v60, v60, v61\n" \
C_"v57, v58, v59\n" \
C_"v61, v62, v63\n" \
"buffer_store_dwordx2 v[56:57], v42, s[88:91], s72 offen sc1\n" \
"buffer_store_dwordx2 v[60:61], v43, s[88:91], s72 offen sc1\n" \
"s_waitcnt vmcnt(9)\n" \
M_"v[240:243], v[50:53], v[204:207], v[240:243]\n" \
M_"v[244:247], v[48:51], v[204:207], v[244:247]\n" \
"s_waitcnt vmcnt(8)\n" \
M_"v[240:243], v[50:53], v[212:215], v[240:243]\n" \
M_"v[244:247], v[48:51], v[212:215], v[244:247]\n" \
"s_waitcnt vmcnt(7)\n" \
M_"v[240:243], v[50:53], v[220:223], v[240:243]\n" \
M_"v[244:247], v[48:51], v[220:223], v[244:247]\n" \
"s_waitcnt vmcnt(6)\n" \
M_"v[240:243], v[50:53], v[228:231], v[240:243]\n" \
M_"v[244:247], v[48:51], v[228:231], v[244:247]\n" \
"s_waitcnt vmcnt(5)\n" \
M_"v[240:243], v[50:53], v[236:239], v[240:243]\n" \
M_"v[244:247], v[48:51], v[236:239], v[244:247]\n" \
"s_nop 6\n" \
C_"v240, v240, v241\n" \
C_"v244, v244, v245\n" \
C_"v241, v242, v243\n" \
C_"v245, v246, v247\n" \
"buffer_store_dwordx2 v[240:241], v42, s[88:91], s92 offen sc1\n" \
"buffer_store_dwordx2 v[244:245], v43, s[88:91], s92 offen sc1\n" \
"L_fin_%=:\n"
#define ATT_VCLOB "v21","v22","v23","v24","v25","v26","v27","v28","v29","v32","v33","v34","v35","v36","v37","v38","v39","v40","v41","v42","v43","v44","v45","v46","v47","v48","v49","v50","v51","v52","v53","v54","v55","v56","v57","v58","v59","v60","v61","v62","v63","v64","v65","v66","v67","v68","v69","v70","v71","v72","v73","v74","v75","v76","v77","v78","v79","v80","v81","v82","v83","v84","v85","v86","v87","v88","v89","v90","v91","v92","v93","v94","v95","v96","v97","v98","v99","v100","v101","v102","v103","v104","v105","v106","v107","v108","v109","v110","v111","v112","v113","v114","v115","v116","v117","v118","v119","v120","v121","v122","v123","v124","v125","v126","v127","v128","v129","v130","v131","v132","v133","v134","v135","v136","v137","v138","v139","v140","v141","v142","v143","v144","v145","v146","v147","v148","v149","v150","v151","v152","v153","v154","v155","v156","v157","v158","v159","v160","v161","v162","v163","v164","v165","v166","v167","v168","v169","v170","v171","v172","v173","v174","v175","v176","v177","v178","v179","v180","v181","v182","v183","v184","v185","v186","v187","v188","v189","v190","v191","v192","v193","v194","v195","v196","v197","v198","v199","v200","v201","v202","v203","v204","v205","v206","v207","v208","v209","v210","v211","v212","v213","v214","v215","v216","v217","v218","v219","v220","v221","v222","v223","v224","v225","v226","v227","v228","v229","v230","v231","v232","v233","v234","v235","v236","v237","v238","v239","v240","v241","v242","v243","v244","v245","v246","v247","v248","v249","v250","v251","v252","v253","v254","v255"
#define ATT_SCLOB "s63","s64","s65","s66","s67","s68","s69","s70","s71","s72","s73","s74","s75","s76","s77","s78","s79","s80","s81","s82","s83","s84","s85","s86","s87","s88","s89","s90","s91","s92","s93","s95","s96","s97","s98","s99","s100","s101"
#define ATT_WTCH 0
#define ATT_PEEL 1
#define ATT_PEEL_FOW 1
#define ATT_PEEL_PRE 0
#define ATT_PEEL_ODD(b) (0u)
#define ATT_PAGES 11
#define ATT_TAILB 968
enum { T_fo, T_fj, T_fa, T_fas, T_fb, T_fbs, T_f8, T_mk8, T_wt, T_wol, T_woh, T_wbl, T_wbh, T_outl, T_outh, T_r1l, T_r1h, T_r1n, T_r1s, T_r2n, T_r2s, T_m2b, T_m0, T_m1, T_m1b, T_mw, T_m3, T_m4, T_ml, T_w0, T_w1, T_w2, T_w3, T_A1, T_A2, T_A3, T_B1, T_B2, T_B3, T_Ao, T_Bo, T_Al, T_Bl, T_l8, T_xr, T_x8r, T_oA, T_oB, T_o8, T_x0, T_x1, T_x2, T_x3, T_x4, T_x5, T_x6, T_x8, T_ep };

__device__ __forceinline__ void out_consts(u32x4 (&vp)[3], const u16* wo, const u16* wob, const u16* out, const unsigned b, const unsigned w, const unsigned lane) {
    const unsigned sp = b & 7u, nt = (b >> 3) & 3u, j = b >> 5, head = 4u * sp + nt, OOR = 0x40000000u;
    unsigned vw[9];
#pragma unroll
    for (unsigned i = 0; i < 9u; ++i) {
        const unsigned q = 64u * i + lane, row = __umul24(q, 3641u) >> 16, c = q - __umul24(row, 18u);
        vw[i] = __umul24(row, DM * 2) + (c < 16u ? (c * 16u + sp * 32u + 32u) & 255u : c * 16u) - (i & 3u) * 1024u;
    }
    const unsigned row = 16u * w + (lane >> 2), r9 = __umul24(16u * (row >> 5) + (row & 12u) + 2u * ((row >> 4) & 1u) + ((row >> 1) & 1u), 18u) + 16u + (row & 1u);
    unsigned vwl = __umul24(r9, DM * 2) + ((lane & 2u) ? 256u : sp * 32u) + (lane & 1u) * 16u, vrd = __umul24(lane >> 2, 288u) + (lane & 3u) * 8u;
    unsigned tab = 0;
#define TB(n, v) asm("v_writelane_b32 %0, %1, %2" : "+v"(tab) : "s"((unsigned)(v)), "n"((int)T_##n))
    const unsigned FJ = (unsigned)WS_F + (F_J + 640u * j) * 4u, mt = 2u * (w & 2u) + (w & 1u), w2 = w < 2u ? w : 2u, hw = head * 147456u;
    const unsigned FR = (unsigned)WS_F + F_R * 4u + (8u * j + sp) * 1024u;
    TB(fo, FJ + (ATT_PEEL_FOW ? 16u * head + 4u * w : 4u * head)); TB(fj, FJ); TB(fa, FR + (w | 2u) * 256u); TB(fas, FR + w * 256u + nt * 4u);
    TB(fb, FR + 128u + w * 256u); TB(fbs, FR + 128u + w * 256u + nt * 4u);
    TB(ep, ssel(b + w, OOR, (unsigned)WS_F + F_EPOCH * 4u));
    const u16* pwo = wo + nt * 144u; const u16* pwb = wob + nt * 144u;
    TB(wol, PLO(pwo)); TB(woh, PHI(pwo) & 0xffffu); TB(wbl, PLO(pwb)); TB(wbh, PHI(pwb) & 0xffffu); TB(outl, PLO(out)); TB(outh, PHI(out) & 0xffffu);
    const unsigned odd = ATT_PEEL_ODD(b), wo1 = w + 3u * odd;
#define OSEL(a, b_) (ATT_PEEL_ODD(1u) ? ssel(odd, a, b_) : (b_))
#define WU(u) (((4u * sp + ((nt + 1u + ((u) >> 2)) & 3u)) * 4u + ((u) & 3u)) * 36864u)
    TB(r1l, sslt<3>(wo1, PLO(pwo), PLO(pwb))); TB(r1h, sslt<3>(wo1, PHI(pwo), PHI(pwb)) & 0xffffu);
    TB(r1n, sslt<3>(wo1, 72u * sp + 71u, 4095u) * 1152u + 288u); TB(r1s, sslt<3>(wo1, (72u * sp + 32u * w) * 1152u, ATT_PEEL_ODD(1u) ? sslt<2>(w, WU(w), sslt<3>(w, OOR, hw + 110592u)) : hw + 110592u));
    if (ATT_PEEL_ODD(1u)) { TB(r2n, (72u * sp + 143u) * 1152u + 288u); TB(r2s, sslt<3>(w, (72u * sp + 72u + 32u * w) * 1152u, OOR)); TB(m2b, sslt<2>(w, 41984u + 9216u * w, 32768u)); }
    TB(m0, 131072u + 9216u * w2); TB(w0, hw + 36864u * w2); TB(m1, sslt<3>(w, 41984u + 9216u * w, 32768u)); TB(m1b, ssel(w ^ 2u, sslt<3>(w, 41984u + 9216u * w, 32768u), 114688u - 4096u));
    TB(mw, 65536u + 9216u * w); TB(m3, sslt<3>(w, 102400u + 9216u * w, 0u)); TB(m4, OSEL(9216u + 13312u * (w & 1u), sslt<3>(w, 131072u + 9216u * w, 9216u))); TB(ml, 18432u + 1024u * w);
    const unsigned tA = (unsigned)WS_AF + (8u * j + mt) * (KS_TOT * 1024u), tB = (unsigned)WS_AF + (120u - 8u * j + mt) * (KS_TOT * 1024u);
#define HK(k) (4u * sp + ((nt + k) & 3u))
    TB(w1, WU(w + 2u * odd)); TB(w2, WU(4u + w + 2u * odd)); TB(w3, WU(8u + (w | (2u * odd))));
    TB(A1, tA + 18432u + HK(1u) * 4096u); TB(A2, tA + 18432u + HK(2u) * 4096u); TB(A3, tA + 18432u + HK(3u) * 4096u);
    TB(B1, tB + 18432u + HK(1u) * 4096u); TB(B2, tB + 18432u + HK(2u) * 4096u); TB(B3, tB + 18432u + HK(3u) * 4096u);
    const unsigned pre = ATT_PEEL_PRE ? sslt<7>(j, tA + 18432u + head * 4096u, OOR) : OOR;
    TB(Ao, OSEL(pre, tA + sp * 2048u)); TB(Bo, OSEL(pre + 2048u, tB + sp * 2048u)); TB(Al, tA + 16384u); TB(Bl, tB + 16384u);
    const unsigned nred = w ^ (sp >> 1), blk = sp & 1u;
    const unsigned xg = (unsigned)WS_X + (4u * j + nt) * 589824u + w * 16384u, oA = (128u * j + 16u * mt) * 1152u + nt * 288u, oB = (1920u - 128u * j + 16u * mt) * 1152u + nt * 288u;
    TB(l8, ssel(nred, OOR, ssel(blk, tB, tA) + 16384u)); TB(xr, xg + sp * 65536u); TB(x8r, ssel(nred, OOR, xg + 524288u + (sp & 1u) * 1024u));
    TB(oA, oA + sp * 32u); TB(oB, oB + sp * 32u); TB(o8, ssel(nred, OOR, ssel(blk, oB, oA) + 256u)); TB(x8, xg + 524288u + sp * 2048u);
    TB(f8, xg + w * 131072u + 14400u + sp * 4u); TB(mk8, ssel(nred, 0u, 0xff0000u));
#define XI(i) TB(x##i, xg + ((sp + 1u + i) & 7u) * 65536u + i * 2048u)
    XI(0); XI(1); XI(2); XI(3); XI(4); XI(5); XI(6);
#undef XI
#undef HK
#undef WU
#undef TB
    vp[0] = (u32x4){vw[0], vw[1], vw[2], vw[3]}; vp[1] = (u32x4){vw[4], vw[5], vw[6], vw[7]}; vp[2] = (u32x4){vw[8], vwl, vrd, tab};
}
__global__ __launch_bounds__(256, 1) __attribute__((aligned(4096))) void fused_kernel(const u16* __restrict__ x, const u16* __restrict__ ckv, const u16* __restrict__ wq, const u16* __restrict__ wuk,
                                                       const u16* __restrict__ wuv, const u16* __restrict__ wqkv, const u16* __restrict__ gw,
                                                       const u16* __restrict__ wo, const u16* __restrict__ wob, char* __restrict__ ws_, u16* __restrict__ out) {
    using namespace att;
    extern __shared__ __attribute__((aligned(16))) char lds[];
    u32x4 sa[5]; u32x2 s24, s28; char* ws; unsigned tdum; kaptr ka = (kaptr)__builtin_amdgcn_kernarg_segment_ptr();
    asm volatile("s_load_dwordx8 s[8:15], %[ka], 0x0\n\ts_load_dwordx8 s[16:23], %[ka], 0x20\n\ts_getpc_b64 s[4:5]\n\tv_lshlrev_b32_e32 v191, 7, %[t]\n\ts_bfe_u32 s6, %[b], %[cb]\n\t"
                 "v_lshl_add_u32 v191, s6, %[cs], v191\n\tv_min_u32_e32 v191, %[e], v191\n\tglobal_load_dword v191, v191, s[4:5]\n\ts_load_dwordx4 s[24:27], %[ka], 0x40\n\ts_load_dwordx2 s[28:29], %[ka], 0x50"
                 : "={s[8:11]}"(sa[0]), "={s[12:15]}"(sa[1]), "={s[16:19]}"(sa[2]), "={s[20:23]}"(sa[3]), "={s[24:25]}"(s24), "={s[26:27]}"(ws), "={s[28:29]}"(s28), "={v191}"(tdum), [ka] "+{s[0:1]}"(ka)
                 : [t] "v"(threadIdx.x), [b] "s"(blockIdx.x), [e] "i"(CT_END), [cb] "i"(CT_END < 0x20000 ? 0x20003 : 0x30003), [cs] "i"(CT_END < 0x20000 ? 15 : 14) : "s4", "s5", "s6", "scc");
    SBAR();
    const unsigned wid = rfl(threadIdx.x >> 6);
    unsigned tag, vl;
    const int b = blockIdx.x;
    u32x4 vp[5], srdA; u32x2 sd;
    proj_phase(ka, ws, wid, vp, vl, sa, s24, s28, tdum, srdA, sd, tag
    );
    unsigned* const flags = (unsigned*)(ws + WS_F);
    RTS(2);
#define AST(L) do {} while (0)
#define ATT_TAIL "s_endpgm\n"
    unsigned abad;
    asm volatile(ATT_ASM ATT_TAIL "L_end_%=:\n s_nop 1"
                 : "={s94}"(abad)
                 : "{v[0:3]}"(vp[0]), "{v[4:7]}"(vp[1]), "{v[8:11]}"(vp[2]), "{v[12:15]}"(vp[3]), "{v[16:19]}"(vp[4]), "{v20}"(vl), "{s[56:59]}"(srdA), "{s[60:61]}"(sd), "{s62}"(tag),
                   "{s[8:11]}"(sa[0]), "{s[12:15]}"(sa[1]), "{s[16:19]}"(sa[2]), "{s[20:23]}"(sa[3]), "{s24}"(sa[4][0]), "{s3}"(wid), "{s2}"(b), "{s[28:29]}"(s28)
                 : "memory", "m0", "vcc", "scc", ATT_VCLOB, ATT_SCLOB, ACLOB_LO, ACLOB_HI);
    {
        unsigned b2 = b, w2 = wid;
        asm volatile("s_waitcnt vmcnt(0)" : "+s"(ka), "+s"(b2), "+s"(w2), "+s"(ws) :: "memory");
        const unsigned j2 = b2 >> 5, p2 = w2 & 1u, h2 = (b2 & 7u) * 4u + ((b2 >> 3) & 3u);
#pragma unroll 1
        for (unsigned i = w2 & 2u; i < ((w2 & 2u) ? (abad ? 8u : 4u) : 2u); ++i)
            att_exact(ka[0], ka[2], ws, h2, (i < 4u ? 1920u - 128u * j2 : 128u * j2 - 128u) + 32u * i + 16u * p2, *reinterpret_cast<const float*>(lds + LDS_G + (32u * i + 16u * p2 + (lane_id() & 15u)) * 4u));
        asm volatile("s_waitcnt vmcnt(0)" ::: "memory");
        unsigned* fa = (unsigned*)(ws + WS_F) + F_R + (8u * j2 + (b2 & 7u)) * 256u + (2u + p2) * 64u + ((b2 >> 3) & 3u);
        if (w2 & 2u) st_flag(fa, tag);
        wait_flags(fa, ld_flag(fa), tag);
        const unsigned l2 = lane_id(); u32x4 c[3]; out_consts(c, ka[7], ka[8], ka[10], b2, w2, l2);
        asm volatile("s_waitcnt vmcnt(0) lgkmcnt(0)\n\ts_nop 7\n\ts_branch L_att_stub" :: "{v[0:3]}"(c[0]), "{v[4:7]}"(c[1]), "{v[8:11]}"(c[2]), "{v20}"(l2 * 16u), "{s[56:59]}"(SRDA), "{s60}"(SD(8u * j2, h2, w2)),
                     "{s61}"(SD(120u - 8u * j2, h2, w2)), "{s62}"(tag), "{s3}"(w2), "{s2}"(b2) : "memory");
    }
    RTS(5);
}

extern "C" void kernel_launch(void* const* d_in, const int* in_sizes, int n_in, void* d_out, int out_size, void* d_ws, size_t ws_size,
                              hipStream_t stream) {
    (void)hipFuncSetAttribute((const void*)fused_kernel, hipFuncAttributeMaxDynamicSharedMemorySize, att::LDS_BYTES);
    fused_kernel<<<dim3(256), 256, att::LDS_BYTES, stream>>>((const u16*)d_in[0], (const u16*)d_in[4], (const u16*)d_in[5], (const u16*)d_in[6], (const u16*)d_in[7], (const u16*)d_in[1],
                                                             (const u16*)d_in[9], (const u16*)d_in[2], (const u16*)d_in[8], (char*)d_ws, (u16*)d_out);
}
```

```cpp
#include <hip/hip_runtime.h>
#include <hip/hip_bf16.h>
#include <stdint.h>
#include <utility>

typedef short bf16x8 __attribute__((ext_vector_type(8)));
typedef short s16x4 __attribute__((ext_vector_type(4)));
typedef float f32x16 __attribute__((ext_vector_type(16)));
typedef float f32x4 __attribute__((ext_vector_type(4)));
typedef unsigned u32x4 __attribute__((ext_vector_type(4)));
typedef unsigned u32x2 __attribute__((ext_vector_type(2)));
typedef unsigned short u16;

constexpr int S = 2048, DM = 576, NH = 32, HD = 128, DC = 512, NQKV = 1728, DHA = 18;
constexpr int NBIG = NH * HD;
constexpr int KS_TOT = 146;

#define SBAR() __builtin_amdgcn_sched_barrier(0)
constexpr size_t WS_Q = 0, WS_K = WS_Q + (size_t)S * NBIG * 2, WS_V = WS_K + (size_t)S * NBIG * 2, WS_AF = WS_V + (size_t)S * NBIG * 2, WS_X = WS_AF + (size_t)128 * KS_TOT * 1024,
                 WS_P = WS_X + (size_t)64 * 16 * 9216, WS_F = WS_P + (size_t)128 * (4 * 32 * 256 + 1024) * 4;
constexpr size_t WS_L = (size_t)96 << 20;
static_assert(WS_F == 96206848, "workspace map is baked into the generated asm");
constexpr int F_EPOCH = 0, F_J = 1024, F_ST = 8192, F_KV = 40960, F_R = 65536;
constexpr unsigned TAG_STEP = 0x9E3779B1u;
__device__ __forceinline__ unsigned ld_flag(const unsigned* p) { return __hip_atomic_load(p, __ATOMIC_RELAXED, __HIP_MEMORY_SCOPE_AGENT); }
__device__ __forceinline__ void st_flag(unsigned* p, unsigned v) { __hip_atomic_store(p, v, __ATOMIC_RELAXED, __HIP_MEMORY_SCOPE_AGENT); }
__device__ __forceinline__ void wait_flags(const unsigned* p, unsigned v, unsigned tag) {
    if (__builtin_expect(__builtin_amdgcn_ballot_w64(v != tag) != 0ull, 0)) {
        unsigned spins = 0;
        do { __builtin_amdgcn_s_sleep(1); } while (__builtin_amdgcn_ballot_w64(ld_flag(p) != tag) != 0ull && ++spins < (1u << 22));
    }
    asm volatile("" ::: "memory");
}

__device__ __forceinline__ unsigned lane_id() { unsigned l; asm volatile("v_mbcnt_lo_u32_b32 %0, -1, 0\n\tv_mbcnt_hi_u32_b32 %0, -1, %0" : "=v"(l)); return l; }
#define RTS(slot) do {} while (0)
#define CT_END ((PROJ_PAGES + ATT_PAGES) * 4096 + (ATT_TAILB & ~127) - 128)
#define ACLOB_LO "a0","a1","a2","a3","a4","a5","a6","a7","a8","a9","a10","a11","a12","a13","a14","a15","a16","a17","a18","a19","a20","a21","a22","a23","a24","a25","a26","a27","a28","a29","a30","a31","a32","a33","a34","a35","a36","a37","a38","a39","a40","a41","a42","a43","a44","a45","a46","a47","a48","a49","a50","a51","a52","a53","a54","a55","a56","a57","a58","a59","a60","a61","a62","a63","a64","a65","a66","a67","a68","a69","a70","a71","a72","a73","a74","a75","a76","a77","a78","a79","a80","a81","a82","a83","a84","a85","a86","a87","a88","a89","a90","a91","a92","a93","a94","a95","a96","a97","a98","a99","a100","a101","a102","a103","a104","a105","a106","a107","a108","a109","a110","a111","a112","a113","a114","a115","a116","a117","a118","a119","a120","a121","a122","a123","a124","a125","a126","a127"
#define ACLOB_HI "a128","a129","a130","a131","a132","a133","a134","a135","a136","a137","a138","a139","a140","a141","a142","a143","a144","a145","a146","a147","a148","a149","a150","a151","a152","a153","a154","a155","a156","a157","a158","a159","a160","a161","a162","a163","a164","a165","a166","a167","a168","a169","a170","a171","a172","a173","a174","a175","a176","a177","a178","a179","a180","a181","a182","a183","a184","a185","a186","a187","a188","a189","a190","a191","a192","a193","a194","a195","a196","a197","a198","a199","a200","a201","a202","a203","a204","a205","a206","a207","a208","a209","a210","a211","a212","a213","a214","a215","a216","a217","a218","a219","a220","a221","a222","a223","a224","a225","a226","a227","a228","a229","a230","a231","a232","a233","a234","a235","a236","a237","a238","a239","a240","a241","a242","a243","a244","a245","a246","a247","a248","a249","a250","a251","a252","a253","a254","a255"
#define M_ "v_mfma_f32_16x16x32_bf16 "
#define X_ "ds_read_b64_tr_b16 "
#define B_ "buffer_load_dwordx4 "
#define C_ "v_cvt_pk_bf16_f32 "
#define R_ "ds_read_b128 "
#define E_ "v_exp_f32_e32 "
#define PLO(p) ((unsigned)(size_t)(p))
#define PHI(p) ((unsigned)((size_t)(p) >> 32))
__device__ __forceinline__ unsigned vsel(bool c, unsigned a, unsigned b) { asm("" : "+v"(a), "+v"(b)); return c ? a : b; }
__device__ __forceinline__ unsigned ssel(unsigned c, unsigned a, unsigned b) { unsigned r; asm("s_cmp_lg_u32 %1, 0\n\ts_cselect_b32 %0, %2, %3" : "=s"(r) : "s"(c), "s"(a), "s"(b) : "scc"); return r; }
template <int N> __device__ __forceinline__ unsigned sslt(unsigned c, unsigned a, unsigned b) { unsigned r; asm("s_cmpk_lt_u32 %1, %4\n\ts_cselect_b32 %0, %2, %3" : "=s"(r) : "s"(c), "s"(a), "s"(b), "n"(N) : "scc"); return r; }
namespace att {
constexpr int LDS_BYTES = 163840;
constexpr unsigned LDS_G = 159744u, LDS_L = 161792u;
}
#define SD(J8, H, W) ((unsigned)WS_AF + (((J8) + 2u * ((W) & 2u) + ((W) & 1u)) * KS_TOT + 18u + 4u * (H)) * 1024u)
#define SRDA (u32x4){PLO(ws), PHI(ws) & 0xffffu, (unsigned)WS_L, 0x00020000u}
#define PROJ_ASM \
".p2align 3\n" \
"L_pb:\n" \
"s_waitcnt lgkmcnt(0)\n" \
"s_mov_b32 s64, s10\n" \
"s_and_b32 s65, s11, 0xffff\n" \
"s_mov_b32 s66, 0x200000\n" \
"s_mov_b32 s67, 0x20000\n" \
"s_mov_b32 s68, s14\n" \
"s_and_b32 s69, s15, 0xffff\n" \
"s_mov_b32 s70, 0x400000\n" \
"s_mov_b32 s71, 0x20000\n" \
"s_mov_b32 s72, s16\n" \
"s_and_b32 s73, s17, 0xffff\n" \
"s_mov_b32 s74, 0x400000\n" \
"s_mov_b32 s75, 0x20000\n" \
"s_add_u32 m0, s30, 0x0\n" \
"s_movk_i32 s84, 0x0\n" \
B_"v22, s[64:67], s84 offen offset:0 lds\n" \
"s_movk_i32 s84, 0x1c00\n" \
B_"v23, s[64:67], s84 offen offset:1024 lds\n" \
"s_movk_i32 s84, 0x3800\n" \
B_"v22, s[64:67], s84 offen offset:2048 lds\n" \
"s_movk_i32 s84, 0x5400\n" \
B_"v23, s[64:67], s84 offen offset:3072 lds\n" \
"s_add_u32 m0, s31, 0x0\n" \
"s_movk_i32 s84, 0x0\n" \
B_"v26, s[68:71], s84 offen offset:0 lds\n" \
"s_movk_i32 s84, 0x7c00\n" \
B_"v26, s[68:71], s84 offen offset:1024 lds\n" \
"s_addk_i32 s84, 0x7c00\n" \
B_"v27, s[68:71], s84 offen offset:2048 lds\n" \
"s_addk_i32 s84, 0x7c00\n" \
B_"v27, s[68:71], s84 offen offset:3072 lds\n" \
"s_movk_i32 s7, 0x300\n" \
"s_lshr_b32 s4, s2, 1\n" \
"s_and_b32 s4, s4, 60\n" \
"s_or_b32 s4, s4, s3\n" \
"v_lshrrev_b32_e32 v212, 4, v20\n" \
"v_min_u32_e32 v212, 59, v212\n" \
"s_cmpk_lt_u32 s2, 0x80\n" \
"s_cselect_b32 s6, s14, s16\n" \
"v_subrev_u32_e32 v214, 48, v212\n" \
"v_mov_b32_e32 v212, s10\n" \
"v_mov_b32_e32 v215, s6\n" \
"s_cmpk_lt_u32 s2, 0x80\n" \
"s_cselect_b32 s5, s15, s17\n" \
"v_cmp_gt_u32_e32 vcc, s7, v20\n" \
"v_mov_b32_e64 v213, s11\n" \
"v_bfe_u32 v216, v20, 4, 3\n" \
"v_cndmask_b32_e32 v212, v212, v215, vcc\n" \
"v_mov_b32_e32 v215, s5\n" \
"s_lshl_b32 s5, s3, 4\n" \
"v_cndmask_b32_e32 v213, v213, v215, vcc\n" \
"v_lshlrev_b32_e64 v215, 5, v20\n" \
"v_and_b32_e32 v215, 0x7000, v215\n" \
"v_lshl_add_u32 v215, s4, 6, v215\n" \
"s_lshl_b32 s4, s2, 3\n" \
"s_and_b32 s4, s4, 56\n" \
"v_or3_b32 v215, v215, s4, v216\n" \
"s_lshl_b32 s4, s2, 6\n" \
"s_or_b32 s6, s5, s4\n" \
"v_cmp_gt_u32_e64 s[4:5], 6, v214\n" \
"v_add_u32_e32 v215, 0x2000, v215\n" \
"s_nop 0\n" \
"v_cndmask_b32_e64 v216, 4, 2, s[4:5]\n" \
"v_add3_u32 v214, s6, v214, v216\n" \
"s_lshl_b32 s4, s3, 9\n" \
"v_cndmask_b32_e32 v214, v214, v215, vcc\n" \
"v_lshlrev_b32_e32 v214, 7, v214\n" \
"v_mov_b32_e64 v215, 0\n" \
"v_lshl_add_u64 v[184:185], v[212:213], 0, v[214:215]\n" \
"s_or_b32 s32, s4, 0x25000\n" \
"s_add_u32 m0, s30, 0x1000\n" \
"s_mov_b32 s84, 0x8000\n" \
B_"v22, s[64:67], s84 offen offset:0 lds\n" \
"s_and_b32 s33, s3, 1\n" \
"v_lshrrev_b32_e32 v212, 4, v20\n" \
"v_lshrrev_b32_e64 v214, 8, v20\n" \
"v_bfe_u32 v212, v212, 1, 3\n" \
"v_lshlrev_b32_e32 v215, 2, v20\n" \
"s_addk_i32 s84, 0x1c00\n" \
B_"v23, s[64:67], s84 offen offset:1024 lds\n" \
"v_lshrrev_b32_e64 v216, 1, v20\n" \
"v_bfe_u32 v213, v20, 4, 4\n" \
"s_lshl_b32 s4, s3, 5\n" \
"v_xor_b32_e32 v212, v212, v214\n" \
"v_lshlrev_b32_e32 v214, 11, v214\n" \
"s_addk_i32 s84, 0x1c00\n" \
B_"v22, s[64:67], s84 offen offset:2048 lds\n" \
"v_and_b32_e32 v215, 0x300, v215\n" \
"v_and_b32_e64 v217, 24, v216\n" \
"v_and_or_b32 v213, s4, 64, v213\n" \
"v_or3_b32 v214, v215, v214, v217\n" \
"s_lshl_b32 s4, s33, 14\n" \
"s_addk_i32 s84, 0x1c00\n" \
B_"v23, s[64:67], s84 offen offset:3072 lds\n" \
"v_and_b32_e32 v215, 0xe0, v216\n" \
"v_lshlrev_b32_e32 v213, 7, v213\n" \
"v_lshlrev_b32_e32 v212, 4, v212\n" \
"v_or3_b32 v214, v214, s4, v215\n" \
"v_or_b32_e64 v28, v212, v213\n" \
"s_add_u32 m0, s31, 0x4000\n" \
"s_movk_i32 s84, 0x0\n" \
B_"v26, s[72:75], s84 offen offset:0 lds\n" \
"v_add_u32_e32 v32, 0x10000, v214\n" \
"v_bitop3_b32 v29, v212, 64, v213 bitop3:0x36\n" \
"v_xor_b32_e32 v33, 0x20, v32\n" \
"v_xor_b32_e64 v34, 0x40, v32\n" \
"v_xor_b32_e32 v35, 0x60, v32\n" \
"s_movk_i32 s84, 0x7c00\n" \
B_"v26, s[72:75], s84 offen offset:1024 lds\n" \
"v_xor_b32_e32 v36, 0x80, v32\n" \
"v_xor_b32_e32 v37, 0xa0, v32\n" \
"v_xor_b32_e32 v38, 0xc0, v32\n" \
"v_xor_b32_e32 v39, 0xe0, v32\n" \
"s_mov_b32 s80, s26\n" \
"s_addk_i32 s84, 0x7c00\n" \
B_"v27, s[72:75], s84 offen offset:2048 lds\n" \
"s_and_b32 s81, s27, 0xffff\n" \
"s_mov_b32 s82, 0x10000000\n" \
"s_mov_b32 s83, 0x20000\n" \
"s_mov_b32 s76, s18\n" \
"s_and_b32 s77, s19, 0xffff\n" \
"s_addk_i32 s84, 0x7c00\n" \
B_"v27, s[72:75], s84 offen offset:3072 lds\n" \
"s_mov_b32 s78, 0x1e6000\n" \
"s_mov_b32 s79, 0x20000\n" \
"s_branch L_pgo_%=\n" \
"L_pret:\n" \
"s_setpc_b64 s[88:89]\n" \
"L_pgo_%=:\n" \
"s_mul_i32 s88, s33, 0xa000\n" \
"s_lshl_b32 s89, s3, 11\n" \
"s_and_b32 s89, s89, 0x1000\n" \
"s_add_u32 s88, s88, s89\n" \
"s_getpc_b64 s[86:87]\n" \
"L_hpa_%=:\n" \
"s_add_u32 s86, s86, L_pp1-L_hpa_%=\n" \
"s_addc_u32 s87, s87, 0\n" \
"s_add_u32 s86, s86, s88\n" \
"s_addc_u32 s87, s87, 0\n" \
"s_add_u32 m0, s30, 0x8000\n" \
"s_movk_i32 s84, 0x80\n" \
B_"v22, s[64:67], s84 offen offset:0 lds\n" \
"s_lshr_b32 s4, s2, 1\n" \
"s_and_b32 s4, s4, 0x7c\n" \
"v_bfe_u32 v212, v20, 4, 5\n" \
"s_movk_i32 s84, 0x1c80\n" \
B_"v23, s[64:67], s84 offen offset:1024 lds\n" \
"s_or_b32 s4, s4, s3\n" \
"v_min_u32_e32 v212, 26, v212\n" \
"v_mad_u64_u32 v[212:213], s[4:5], s4, 27, v[212:213]\n" \
"s_movk_i32 s84, 0x3880\n" \
B_"v22, s[64:67], s84 offen offset:2048 lds\n" \
"v_mul_u32_u24_e32 v213, 0xaaab, v212\n" \
"s_lshr_b32 s7, s2, 5\n" \
"v_lshrrev_b32_e32 v213, 18, v213\n" \
"s_movk_i32 s84, 0x5480\n" \
B_"v23, s[64:67], s84 offen offset:3072 lds\n" \
"s_mov_b32 s4, 0x1fffffa\n" \
"s_and_b32 s6, s2, 7\n" \
"v_mad_u64_u32 v[214:215], s[4:5], v213, s4, v[212:213]\n" \
"s_add_u32 m0, s31, 0x8000\n" \
"s_mov_b32 s84, 0x80000\n" \
B_"v26, s[68:71], s84 offen offset:0 lds\n" \
"s_mulk_i32 s7, 0x90\n" \
"v_mul_u32_u24_e32 v213, 27, v213\n" \
"v_lshrrev_b32_e32 v214, 1, v214\n" \
"s_addk_i32 s84, 0x7c00\n" \
B_"v26, s[68:71], s84 offen offset:1024 lds\n" \
"v_and_b32_e32 v212, 1, v212\n" \
"s_add_u32 s4, s7, s20\n" \
"v_mul_u32_u24_e32 v214, 9, v214\n" \
"v_add3_u32 v212, v212, s6, v213\n" \
"s_addc_u32 s5, 0, s21\n" \
"s_addk_i32 s84, 0x7c00\n" \
B_"v27, s[68:71], s84 offen offset:2048 lds\n" \
"v_add_lshl_u32 v212, v212, v214, 7\n" \
"v_mov_b32_e64 v213, 0\n" \
"v_lshl_add_u64 v[186:187], s[18:19], 0, v[212:213]\n" \
"s_addk_i32 s84, 0x7c00\n" \
B_"v27, s[68:71], s84 offen offset:3072 lds\n" \
"s_bitcmp1_b32 s3, 0\n" \
"s_cselect_b32 s6, 0x8c, 0\n" \
"s_add_u32 s4, s4, s6\n" \
"v_mov_b32_e32 v212, v187\n" \
"s_addc_u32 s5, s5, 0\n" \
"s_add_u32 m0, s30, 0x9000\n" \
"s_mov_b32 s84, 0x8080\n" \
B_"v22, s[64:67], s84 offen offset:0 lds\n" \
"v_writelane_b32 v186, s4, 31\n" \
"v_writelane_b32 v212, s5, 31\n" \
"v_mov_b32_e32 v187, v212\n" \
"s_addk_i32 s84, 0x1c00\n" \
B_"v23, s[64:67], s84 offen offset:1024 lds\n" \
"s_lshl_b32 s35, s2, 10\n" \
"s_lshr_b32 s37, s2, 1\n" \
"s_mul_i32 s37, s37, 0x24800\n" \
"s_addk_i32 s84, 0x1c00\n" \
B_"v22, s[64:67], s84 offen offset:2048 lds\n" \
"s_and_b32 s35, s35, 0x400\n" \
"s_lshl_b32 s36, s3, 8\n" \
"s_or_b32 s35, s37, s35\n" \
"s_addk_i32 s84, 0x1c00\n" \
B_"v23, s[64:67], s84 offen offset:3072 lds\n" \
"s_and_b32 s4, s2, 7\n" \
"s_lshr_b32 s6, s2, 5\n" \
"s_lshl_b32 s34, s2, 5\n" \
"s_add_u32 m0, s31, 0xc000\n" \
"s_mov_b32 s84, 0x80000\n" \
B_"v26, s[72:75], s84 offen offset:0 lds\n" \
"s_add_i32 s35, s35, s36\n" \
"s_lshl_b32 s7, s4, 10\n" \
"s_and_b32 s34, s34, 0x300\n" \
"s_addk_i32 s84, 0x7c00\n" \
B_"v26, s[72:75], s84 offen offset:1024 lds\n" \
"s_add_i32 s35, s35, 0x3004000\n" \
"s_lshl_b32 s37, s6, 13\n" \
"s_or_b32 s34, s7, s34\n" \
"s_addk_i32 s84, 0x7c00\n" \
B_"v27, s[72:75], s84 offen offset:2048 lds\n" \
"s_or_b32 s7, s37, s7\n" \
"v_mov_b32_e32 v213, s35\n" \
"s_or_b32 s7, s7, s36\n" \
"s_addk_i32 s84, 0x7c00\n" \
B_"v27, s[72:75], s84 offen offset:3072 lds\n" \
"s_lshr_b32 s5, s2, 3\n" \
"s_or_b32 s7, s7, 0x5c00000\n" \
"v_writelane_b32 v213, s7, 2\n" \
"s_mul_i32 s5, s5, 0x90000\n" \
"v_writelane_b32 v213, s7, 3\n" \
"s_lshl_b32 s7, s3, 14\n" \
"s_lshl_b32 s4, s4, 16\n" \
"s_add_i32 s5, s5, s7\n" \
"s_or_b32 s34, s34, 0x5be8000\n" \
"v_writelane_b32 v213, s34, 4\n" \
"s_add_i32 s4, s5, s4\n" \
"v_writelane_b32 v213, s34, 5\n" \
"s_add_i32 s4, s4, 0x4243800\n" \
"v_writelane_b32 v213, s4, 6\n" \
"s_mul_i32 s4, s6, 0xa00\n" \
"v_lshlrev_b32_e32 v212, 3, v20\n" \
"s_lshl_b32 s5, s3, 7\n" \
"v_and_b32_e32 v212, 0x80, v212\n" \
"s_add_i32 s4, s4, s5\n" \
"s_add_i32 s4, s4, 0x5bc0f80\n" \
"v_writelane_b32 v213, s4, 7\n" \
"v_add_u32_e32 v49, v213, v212\n" \
"s_mov_b32 m0, s32\n" \
"s_mov_b32 exec_lo, 0xffffff\n" \
"s_mov_b32 exec_hi, 0xfff0000\n" \
"global_load_lds_dword v[184:185], off\n" \
"s_mov_b64 exec, -1\n" \
"s_mov_b32 m0, s32\n" \
"s_mov_b64 exec, 0xff\n" \
"buffer_load_dword v49, s[80:83], 0 offen lds\n" \
"s_mov_b64 exec, -1\n" \
"s_mov_b32 s84, 0x5bc0000\n" \
"buffer_load_dword v180, off, s[80:83], s84 sc1\n" \
"s_cmp_lg_u32 s33, 0\n" \
"s_cbranch_scc1 L_w1_%=\n" \
"L_w0_%=:\n" \
"s_waitcnt vmcnt(19)\n" \
"s_barrier\n" \
X_"a[160:161], v32 offset:0\n" \
X_"a[162:163], v32 offset:1024\n" \
R_"a[128:131], v28 offset:16384\n" \
X_"a[224:225], v32 offset:8192\n" \
X_"a[226:227], v32 offset:9216\n" \
R_"a[192:195], v29 offset:16384\n" \
R_"a[132:135], v28 offset:18432\n" \
R_"a[196:199], v29 offset:18432\n" \
X_"a[164:165], v33 offset:0\n" \
X_"a[166:167], v33 offset:1024\n" \
X_"a[228:229], v33 offset:8192\n" \
X_"a[230:231], v33 offset:9216\n" \
R_"a[136:139], v28 offset:20480\n" \
R_"a[200:203], v29 offset:20480\n" \
"s_waitcnt lgkmcnt(11)\n" \
M_"a[0:3], a[160:163], a[128:131], 0\n" \
X_"a[168:169], v34 offset:0\n" \
"s_lshr_b32 s4, s2, 1\n" \
"s_waitcnt lgkmcnt(9)\n" \
M_"a[0:3], a[224:227], a[192:195], a[0:3]\n" \
X_"a[170:171], v34 offset:1024\n" \
"s_and_b32 s4, s4, 0x7c\n" \
"s_waitcnt lgkmcnt(9)\n" \
M_"a[16:19], a[160:163], a[132:135], 0\n" \
X_"a[232:233], v34 offset:8192\n" \
"v_mov_b32_e32 v213, s13\n" \
"s_waitcnt lgkmcnt(9)\n" \
M_"a[16:19], a[224:227], a[196:199], a[16:19]\n" \
X_"a[234:235], v34 offset:9216\n" \
"s_or_b32 s4, s4, s3\n" \
"s_waitcnt lgkmcnt(8)\n" \
M_"a[20:23], a[164:167], a[132:135], 0\n" \
R_"a[140:143], v28 offset:22528\n" \
"v_lshrrev_b32_e32 v212, 4, v20\n" \
"s_waitcnt lgkmcnt(7)\n" \
M_"a[20:23], a[228:231], a[196:199], a[20:23]\n" \
R_"a[204:207], v29 offset:22528\n" \
"v_mad_u64_u32 v[214:215], s[4:5], s4, 36, v[212:213]\n" \
M_"a[4:7], a[164:167], a[128:131], 0\n" \
X_"a[172:173], v35 offset:0\n" \
"v_mov_b32_e32 v217, s9\n" \
M_"a[4:7], a[228:231], a[192:195], a[4:7]\n" \
X_"a[174:175], v35 offset:1024\n" \
"s_movk_i32 s4, 0x240\n" \
"s_waitcnt lgkmcnt(9)\n" \
M_"a[32:35], a[160:163], a[136:139], 0\n" \
X_"a[236:237], v35 offset:8192\n" \
"v_cmp_gt_u32_e32 vcc, s4, v20\n" \
"s_waitcnt lgkmcnt(9)\n" \
M_"a[32:35], a[224:227], a[200:203], a[32:35]\n" \
X_"a[238:239], v35 offset:9216\n" \
"s_lshl_b32 s4, s2, 3\n" \
M_"a[36:39], a[164:167], a[136:139], 0\n" \
R_"a[144:147], v28 offset:0\n" \
"s_and_b32 s4, s4, 56\n" \
M_"a[36:39], a[228:231], a[200:203], a[36:39]\n" \
R_"a[208:211], v29 offset:0\n" \
"v_cndmask_b32_e32 v217, v217, v213, vcc\n" \
"s_waitcnt lgkmcnt(10)\n" \
M_"a[40:43], a[168:171], a[136:139], 0\n" \
R_"a[148:151], v28 offset:2048\n" \
"v_lshlrev_b32_e32 v213, 3, v214\n" \
"s_waitcnt lgkmcnt(9)\n" \
M_"a[40:43], a[232:235], a[200:203], a[40:43]\n" \
R_"a[212:215], v29 offset:2048\n" \
"v_and_b32_e32 v213, 0x1ffc0, v213\n" \
M_"a[24:27], a[168:171], a[132:135], 0\n" \
R_"a[152:155], v28 offset:4096\n" \
"v_and_b32_e64 v214, 7, v214\n" \
M_"a[24:27], a[232:235], a[196:199], a[24:27]\n" \
R_"a[216:219], v29 offset:4096\n" \
"v_or3_b32 v213, v214, s4, v213\n" \
M_"a[8:11], a[168:171], a[128:131], 0\n" \
R_"a[156:159], v28 offset:6144\n" \
"s_lshl_b32 s4, s2, 2\n" \
M_"a[8:11], a[232:235], a[192:195], a[8:11]\n" \
R_"a[220:223], v29 offset:6144\n" \
"s_mov_b32 m0, s32\n" \
"s_waitcnt lgkmcnt(13)\n" \
M_"a[48:51], a[160:163], a[140:143], 0\n" \
"s_mov_b32 exec_lo, 0xff000000\n" \
"s_mov_b32 exec_hi, 0x0\n" \
"global_load_lds_dword v[184:185], off\n" \
"s_mov_b64 exec, -1\n" \
"s_waitcnt lgkmcnt(12)\n" \
M_"a[48:51], a[224:227], a[204:207], a[48:51]\n" \
X_"a[176:177], v36 offset:0\n" \
"s_or_b32 s4, s4, s3\n" \
M_"a[52:55], a[164:167], a[140:143], 0\n" \
X_"a[178:179], v36 offset:1024\n" \
"s_mul_i32 s4, s4, 18\n" \
M_"a[52:55], a[228:231], a[204:207], a[52:55]\n" \
"s_mov_b32 m0, s32\n" \
X_"a[240:241], v36 offset:8192\n" \
M_"a[56:59], a[168:171], a[140:143], 0\n" \
"s_mov_b32 exec_lo, 0xffff\n" \
"s_mov_b32 exec_hi, 0x0\n" \
"global_load_lds_dword v[186:187], off\n" \
"s_mov_b64 exec, -1\n" \
"s_mov_b64 exec, 0\n" \
"s_swappc_b64 s[88:89], s[86:87]\n" \
"s_mov_b64 exec, -1\n" \
"s_add_u32 s86, s86, 0x2000\n" \
"s_addc_u32 s87, s87, 0\n" \
M_"a[56:59], a[232:235], a[204:207], a[56:59]\n" \
X_"a[242:243], v36 offset:9216\n" \
"v_min_u32_e32 v212, 53, v212\n" \
"s_waitcnt lgkmcnt(14)\n" \
M_"a[60:63], a[172:175], a[140:143], 0\n" \
X_"a[180:181], v37 offset:0\n" \
"s_sub_i32 s4, s4, 36\n" \
"s_waitcnt lgkmcnt(13)\n" \
M_"a[60:63], a[236:239], a[204:207], a[60:63]\n" \
X_"a[182:183], v37 offset:1024\n" \
"v_add_u32_e32 v212, s4, v212\n" \
M_"a[44:47], a[172:175], a[136:139], 0\n" \
X_"a[244:245], v37 offset:8192\n" \
"v_mov_b32_e64 v215, s12\n" \
M_"a[44:47], a[236:239], a[200:203], a[44:47]\n" \
X_"a[246:247], v37 offset:9216\n" \
"v_mov_b32_e64 v216, s8\n" \
M_"a[28:31], a[172:175], a[132:135], 0\n" \
X_"a[184:185], v38 offset:0\n" \
"v_cndmask_b32_e64 v212, v212, v213, vcc\n" \
M_"a[28:31], a[236:239], a[196:199], a[28:31]\n" \
X_"a[186:187], v38 offset:1024\n" \
"v_cndmask_b32_e64 v216, v216, v215, vcc\n" \
M_"a[12:15], a[172:175], a[128:131], 0\n" \
"v_lshlrev_b32_e32 v212, 7, v212\n" \
"v_mov_b32_e32 v213, 0\n" \
M_"a[12:15], a[236:239], a[192:195], a[12:15]\n" \
"v_lshl_add_u64 v[188:189], v[216:217], 0, v[212:213]\n" \
"s_movk_i32 s5, 0xd80\n" \
"s_waitcnt lgkmcnt(15)\n" \
M_"a[76:79], a[172:175], a[144:147], 0\n" \
X_"a[248:249], v38 offset:8192\n" \
"s_lshr_b32 s6, s2, 5\n" \
M_"a[76:79], a[236:239], a[208:211], a[76:79]\n" \
"v_lshrrev_b32_e32 v212, 4, v20\n" \
"v_lshl_or_b32 v215, s3, 6, v212\n" \
M_"a[72:75], a[168:171], a[144:147], 0\n" \
"v_mul_u32_u24_e32 v216, 0x2aab, v215\n" \
"v_lshrrev_b32_e64 v216, 16, v216\n" \
M_"a[72:75], a[232:235], a[208:211], a[72:75]\n" \
X_"a[250:251], v38 offset:9216\n" \
"v_mad_i32_i24 v217, v216, -6, v215\n" \
M_"a[68:71], a[164:167], a[144:147], 0\n" \
"s_mov_b64 exec, 0\n" \
"s_swappc_b64 s[88:89], s[86:87]\n" \
"s_mov_b64 exec, -1\n" \
"s_add_u32 s86, s86, 0x2000\n" \
"s_addc_u32 s87, s87, 0\n" \
M_"a[68:71], a[228:231], a[208:211], a[68:71]\n" \
"v_add_u32_e32 v218, 0xfffffe50, v215\n" \
"s_mul_i32 s35, s6, 0x48\n" \
M_"a[64:67], a[160:163], a[144:147], 0\n" \
"v_lshrrev_b32_e32 v219, 1, v217\n" \
"v_add_u32_e32 v218, s35, v216\n" \
M_"a[64:67], a[224:227], a[208:211], a[64:67]\n" \
X_"a[188:189], v39 offset:0\n" \
"v_mul_u32_u24_e32 v219, 0x480, v219\n" \
M_"a[80:83], a[160:163], a[148:151], 0\n" \
"v_and_b32_e64 v214, 16, v20\n" \
"v_mad_u32_u24 v219, v218, s5, v219\n" \
"s_waitcnt lgkmcnt(15)\n" \
M_"a[80:83], a[224:227], a[212:215], a[80:83]\n" \
"v_or_b32_e32 v192, v219, v214\n" \
"v_mov_b32_e64 v220, 2.0\n" \
M_"a[84:87], a[164:167], a[148:151], 0\n" \
X_"a[190:191], v39 offset:1024\n" \
"v_mov_b32_e64 v222, 7\n" \
M_"a[84:87], a[228:231], a[212:215], a[84:87]\n" \
"v_lshlrev_b32_e32 v220, 7, v218\n" \
"v_lshrrev_b32_e32 v218, 1, v218\n" \
M_"a[88:91], a[168:171], a[148:151], 0\n" \
"v_and_b32_e32 v218, 4, v218\n" \
"v_and_b32_e32 v216, 2, v216\n" \
M_"a[88:91], a[232:235], a[212:215], a[88:91]\n" \
X_"a[252:253], v39 offset:8192\n" \
"s_mul_i32 s7, s6, 0x2400\n" \
M_"a[92:95], a[172:175], a[148:151], 0\n" \
"s_sub_i32 s34, 0x22000, s7\n" \
"v_bitop3_b32 v216, v217, v218, v216 bitop3:0x1e\n" \
M_"a[92:95], a[236:239], a[212:215], a[92:95]\n" \
"v_or_b32_e32 v213, 0x24400, v20\n" \
"v_lshlrev_b32_e64 v216, 4, v216\n" \
M_"a[108:111], a[172:175], a[152:155], 0\n" \
X_"a[254:255], v39 offset:9216\n" \
"v_add3_u32 v196, v220, s34, v216\n" \
M_"a[108:111], a[236:239], a[216:219], a[108:111]\n" \
"v_mov_b32_e32 v216, v213\n" \
"s_movk_i32 s36, 0x1b0\n" \
M_"a[104:107], a[168:171], a[152:155], 0\n" \
"s_mov_b64 exec, 0\n" \
"s_swappc_b64 s[88:89], s[86:87]\n" \
"s_mov_b64 exec, -1\n" \
"s_add_u32 s86, s86, 0x2000\n" \
"s_addc_u32 s87, s87, 0\n" \
M_"a[104:107], a[232:235], a[216:219], a[104:107]\n" \
"v_or_b32_e32 v216, 0x100, v215\n" \
"v_mul_u32_u24_e32 v217, 0x2aab, v216\n" \
M_"a[100:103], a[164:167], a[152:155], 0\n" \
"v_add_u32_e32 v215, 0xffffff50, v215\n" \
"v_lshrrev_b32_e64 v217, 16, v217\n" \
M_"a[100:103], a[228:231], a[216:219], a[100:103]\n" \
"v_mov_b32_e64 v220, v215\n" \
"v_mad_i32_i24 v218, v217, -6, v216\n" \
M_"a[96:99], a[160:163], a[152:155], 0\n" \
"v_cmp_gt_u32_e32 vcc, s36, v216\n" \
"v_mov_b32_e32 v221, 7\n" \
M_"a[96:99], a[224:227], a[216:219], a[96:99]\n" \
"v_lshrrev_b32_e32 v222, 1, v218\n" \
"v_cndmask_b32_e32 v217, v220, v217, vcc\n" \
"s_waitcnt lgkmcnt(15)\n" \
M_"a[112:115], a[160:163], a[156:159], 0\n" \
"s_movk_i32 s4, 0x48\n" \
"v_add_u32_e64 v220, s35, v217\n" \
M_"a[112:115], a[224:227], a[220:223], a[112:115]\n" \
"v_mul_u32_u24_e32 v222, 0x480, v222\n" \
"v_mad_u32_u24 v222, v220, s5, v222\n" \
M_"a[116:119], a[164:167], a[156:159], 0\n" \
"v_cmp_gt_u32_e64 s[4:5], s4, v215\n" \
"v_cndmask_b32_e64 v215, v221, v218, vcc\n" \
M_"a[116:119], a[228:231], a[220:223], a[116:119]\n" \
"v_lshrrev_b32_e32 v218, 1, v220\n" \
"v_or_b32_e32 v214, v222, v214\n" \
M_"a[120:123], a[168:171], a[156:159], 0\n" \
"v_mov_b32_e32 v222, 2.0\n" \
"v_and_b32_e32 v218, 4, v218\n" \
M_"a[120:123], a[232:235], a[220:223], a[120:123]\n" \
"v_and_b32_e64 v217, 2, v217\n" \
"v_bitop3_b32 v215, v215, v218, v217 bitop3:0x1e\n" \
M_"a[124:127], a[172:175], a[156:159], 0\n" \
"v_lshlrev_b32_e32 v223, 1, v220\n" \
"v_mov_b32_e32 v224, 2.0\n" \
"s_waitcnt vmcnt(5) lgkmcnt(0)\n" \
M_"a[124:127], a[236:239], a[220:223], a[124:127]\n" \
"s_barrier\n" \
M_"v[52:55], a[176:179], a[128:131], 0\n" \
"s_add_u32 m0, s30, 0x0\n" \
"s_movk_i32 s84, 0x100\n" \
M_"v[52:55], a[240:243], a[192:195], v[52:55]\n" \
B_"v22, s[64:67], s84 offen offset:0 lds\n" \
M_"v[56:59], a[180:183], a[128:131], 0\n" \
X_"a[160:161], v32 offset:32768\n" \
"v_cndmask_b32_e64 v193, v222, v214, vcc\n" \
M_"v[56:59], a[244:247], a[192:195], v[56:59]\n" \
"s_movk_i32 s84, 0x1d00\n" \
M_"v[60:63], a[184:187], a[128:131], 0\n" \
B_"v23, s[64:67], s84 offen offset:1024 lds\n" \
M_"v[60:63], a[248:251], a[192:195], v[60:63]\n" \
X_"a[162:163], v32 offset:33792\n" \
"v_lshlrev_b32_e32 v214, 7, v220\n" \
M_"v[64:67], a[188:191], a[128:131], 0\n" \
X_"a[224:225], v32 offset:40960\n" \
"v_lshlrev_b32_e64 v215, 4, v215\n" \
M_"v[64:67], a[252:255], a[192:195], v[64:67]\n" \
R_"a[128:131], v28 offset:49152\n" \
"s_mul_i32 s39, s3, 18\n" \
M_"v[80:83], a[188:191], a[132:135], 0\n" \
R_"a[192:195], v29 offset:49152\n" \
"v_add3_u32 v214, v214, s34, v215\n" \
M_"v[80:83], a[252:255], a[196:199], v[80:83]\n" \
"s_movk_i32 s84, 0x3900\n" \
M_"v[76:79], a[184:187], a[132:135], 0\n" \
B_"v22, s[64:67], s84 offen offset:2048 lds\n" \
M_"v[76:79], a[248:251], a[196:199], v[76:79]\n" \
"s_movk_i32 s84, 0x5500\n" \
M_"v[72:75], a[180:183], a[132:135], 0\n" \
B_"v23, s[64:67], s84 offen offset:3072 lds\n" \
"s_mov_b64 exec, 0\n" \
"s_swappc_b64 s[88:89], s[86:87]\n" \
"s_mov_b64 exec, -1\n" \
"s_andn2_b32 s85, 2, s3\n" \
"s_lshl_b32 s85, s85, 12\n" \
"s_add_u32 s86, s86, s85\n" \
"s_addc_u32 s87, s87, 0\n" \
M_"v[72:75], a[244:247], a[196:199], v[72:75]\n" \
"s_add_u32 m0, s31, 0x0\n" \
"s_mov_b32 s84, 0x100000\n" \
M_"v[68:71], a[176:179], a[132:135], 0\n" \
B_"v26, s[68:71], s84 offen offset:0 lds\n" \
M_"v[68:71], a[240:243], a[196:199], v[68:71]\n" \
R_"a[132:135], v28 offset:51200\n" \
"v_cndmask_b32_e64 v195, v224, v223, s[4:5]\n" \
M_"v[84:87], a[176:179], a[136:139], 0\n" \
R_"a[196:199], v29 offset:51200\n" \
"s_movk_i32 s4, 0x1f8\n" \
M_"v[84:87], a[240:243], a[200:203], v[84:87]\n" \
"s_addk_i32 s84, 0x7c00\n" \
M_"v[88:91], a[180:183], a[136:139], 0\n" \
B_"v26, s[68:71], s84 offen offset:1024 lds\n" \
M_"v[88:91], a[244:247], a[200:203], v[88:91]\n" \
X_"a[226:227], v32 offset:41984\n" \
"v_mov_b32_e32 v215, v213\n" \
M_"v[92:95], a[184:187], a[136:139], 0\n" \
"s_addk_i32 s84, 0x7c00\n" \
M_"v[92:95], a[248:251], a[200:203], v[92:95]\n" \
B_"v27, s[68:71], s84 offen offset:2048 lds\n" \
M_"v[96:99], a[188:191], a[136:139], 0\n" \
X_"a[164:165], v33 offset:32768\n" \
"s_add_i32 s35, s35, s39\n" \
M_"v[96:99], a[252:255], a[200:203], v[96:99]\n" \
R_"a[136:139], v28 offset:53248\n" \
"v_cmp_gt_u32_e64 vcc, s4, v216\n" \
M_"v[112:115], a[188:191], a[140:143], 0\n" \
R_"a[200:203], v29 offset:53248\n" \
"v_add_u32_e64 v212, s35, v212\n" \
M_"v[112:115], a[252:255], a[204:207], v[112:115]\n" \
"s_addk_i32 s84, 0x7c00\n" \
M_"v[108:111], a[184:187], a[140:143], 0\n" \
B_"v27, s[68:71], s84 offen offset:3072 lds\n" \
M_"v[108:111], a[248:251], a[204:207], v[108:111]\n" \
"s_add_u32 m0, s30, 0x1000\n" \
"s_mov_b32 s84, 0x8100\n" \
M_"v[104:107], a[180:183], a[140:143], 0\n" \
B_"v22, s[64:67], s84 offen offset:0 lds\n" \
M_"v[104:107], a[244:247], a[204:207], v[104:107]\n" \
X_"a[166:167], v33 offset:33792\n" \
"v_mov_b32_e32 v219, 2.0\n" \
M_"v[100:103], a[176:179], a[140:143], 0\n" \
X_"a[228:229], v33 offset:40960\n" \
"v_cndmask_b32_e64 v197, v215, v214, vcc\n" \
M_"v[100:103], a[240:243], a[204:207], v[100:103]\n" \
R_"a[140:143], v28 offset:55296\n" \
"s_movk_i32 s35, 0x120\n" \
M_"v[116:119], a[176:179], a[144:147], 0\n" \
R_"a[204:207], v29 offset:55296\n" \
"v_mul_u32_u24_e32 v214, 0xd80, v212\n" \
M_"v[116:119], a[240:243], a[208:211], v[116:119]\n" \
"s_addk_i32 s84, 0x1c00\n" \
M_"v[120:123], a[180:183], a[144:147], 0\n" \
B_"v23, s[64:67], s84 offen offset:1024 lds\n" \
M_"v[120:123], a[244:247], a[208:211], v[120:123]\n" \
".fill (L_pb + 3808 - .) / 4, 4, 0xBF800000\n" \
"L_pp1:\n" \
"s_cbranch_execz L_pret\n" \
"s_addk_i32 s84, 0x1c00\n" \
M_"v[124:127], a[184:187], a[144:147], 0\n" \
B_"v22, s[64:67], s84 offen offset:2048 lds\n" \
"s_mov_b64 exec, 0\n" \
"s_swappc_b64 s[88:89], s[86:87]\n" \
"s_mov_b64 exec, -1\n" \
M_"v[124:127], a[248:251], a[208:211], v[124:127]\n" \
"s_addk_i32 s84, 0x1c00\n" \
M_"v[128:131], a[188:191], a[144:147], 0\n" \
B_"v23, s[64:67], s84 offen offset:3072 lds\n" \
M_"v[128:131], a[252:255], a[208:211], v[128:131]\n" \
R_"a[144:147], v28 offset:32768\n" \
"v_cmp_gt_u32_e64 vcc, s35, v20\n" \
M_"v[144:147], a[188:191], a[148:151], 0\n" \
R_"a[208:211], v29 offset:32768\n" \
"v_lshrrev_b32_e64 v215, 1, v212\n" \
M_"v[144:147], a[252:255], a[212:215], v[144:147]\n" \
"s_add_u32 m0, s31, 0x4000\n" \
"s_mov_b32 s84, 0x100000\n" \
M_"v[140:143], a[184:187], a[148:151], 0\n" \
B_"v26, s[72:75], s84 offen offset:0 lds\n" \
M_"v[140:143], a[248:251], a[212:215], v[140:143]\n" \
X_"a[230:231], v33 offset:41984\n" \
"s_and_b32 s4, s2, 7\n" \
M_"v[136:139], a[180:183], a[148:151], 0\n" \
"s_addk_i32 s84, 0x7c00\n" \
M_"v[136:139], a[244:247], a[212:215], v[136:139]\n" \
B_"v26, s[72:75], s84 offen offset:1024 lds\n" \
M_"v[132:135], a[176:179], a[148:151], 0\n" \
X_"a[168:169], v34 offset:32768\n" \
"v_cndmask_b32_e64 v194, v219, v214, vcc\n" \
M_"v[132:135], a[240:243], a[212:215], v[132:135]\n" \
R_"a[148:151], v28 offset:34816\n" \
"v_lshlrev_b32_e64 v214, 7, v212\n" \
M_"v[148:151], a[176:179], a[152:155], 0\n" \
R_"a[212:215], v29 offset:34816\n" \
"v_lshrrev_b32_e64 v212, 2, v212\n" \
M_"v[148:151], a[240:243], a[216:219], v[148:151]\n" \
X_"a[170:171], v34 offset:33792\n" \
"v_and_b32_e64 v212, 2, v212\n" \
M_"v[152:155], a[180:183], a[152:155], 0\n" \
X_"a[232:233], v34 offset:40960\n" \
"v_and_or_b32 v212, v215, 1, v212\n" \
M_"v[152:155], a[244:247], a[216:219], v[152:155]\n" \
X_"a[234:235], v34 offset:41984\n" \
"s_lshl_b32 s5, s4, 2\n" \
M_"v[156:159], a[184:187], a[152:155], 0\n" \
X_"a[172:173], v35 offset:32768\n" \
"s_bfe_u32 s38, s2, 0x20003\n" \
M_"v[156:159], a[248:251], a[216:219], v[156:159]\n" \
X_"a[174:175], v35 offset:33792\n" \
"v_lshlrev_b32_e32 v212, 5, v212\n" \
M_"v[160:163], a[188:191], a[152:155], 0\n" \
X_"a[236:237], v35 offset:40960\n" \
"s_movk_i32 s35, 0x60\n" \
M_"v[160:163], a[252:255], a[216:219], v[160:163]\n" \
R_"a[152:155], v28 offset:36864\n" \
"s_or_b32 s5, s5, s38\n" \
M_"v[176:179], a[188:191], a[156:159], 0\n" \
R_"a[216:219], v29 offset:36864\n" \
"v_bitop3_b32 v212, v212, s35, v214 bitop3:0x36\n" \
M_"v[176:179], a[252:255], a[220:223], v[176:179]\n" \
X_"a[238:239], v35 offset:41984\n" \
"s_lshr_b32 s36, s2, 3\n" \
M_"v[172:175], a[184:187], a[156:159], 0\n" \
"v_add_u32_e32 v212, s34, v212\n" \
"s_mul_i32 s34, s5, 0x12000\n" \
M_"v[172:175], a[248:251], a[220:223], v[172:175]\n" \
"s_or_b32 s40, s34, 0x6000000\n" \
"s_mul_i32 s34, s36, 0x90000\n" \
M_"v[168:171], a[180:183], a[156:159], 0\n" \
"s_lshl_b32 s36, s3, 14\n" \
"s_add_i32 s34, s34, s36\n" \
M_"v[168:171], a[244:247], a[220:223], v[168:171]\n" \
"s_lshl_b32 s38, s5, 8\n" \
"s_add_i32 s41, s34, 0x4240000\n" \
M_"v[164:167], a[176:179], a[156:159], 0\n" \
"s_mul_i32 s34, s5, 36\n" \
"s_add_i32 s5, s7, s40\n" \
M_"v[164:167], a[240:243], a[220:223], v[164:167]\n" \
R_"a[156:159], v28 offset:38912\n" \
R_"a[220:223], v29 offset:38912\n" \
"s_waitcnt lgkmcnt(15)\n" \
M_"a[0:3], a[160:163], a[128:131], a[0:3]\n" \
"s_addk_i32 s84, 0x7c00\n" \
M_"a[0:3], a[224:227], a[192:195], a[0:3]\n" \
B_"v27, s[72:75], s84 offen offset:2048 lds\n" \
M_"a[4:7], a[164:167], a[128:131], a[4:7]\n" \
X_"a[176:177], v36 offset:32768\n" \
"s_addk_i32 s5, 0x2000\n" \
"s_waitcnt lgkmcnt(15)\n" \
M_"a[4:7], a[228:231], a[192:195], a[4:7]\n" \
"s_addk_i32 s84, 0x7c00\n" \
"s_waitcnt lgkmcnt(11)\n" \
M_"a[8:11], a[168:171], a[128:131], a[8:11]\n" \
B_"v27, s[72:75], s84 offen offset:3072 lds\n" \
"s_waitcnt lgkmcnt(9)\n" \
M_"a[8:11], a[232:235], a[192:195], a[8:11]\n" \
X_"a[178:179], v36 offset:33792\n" \
"s_mov_b32 s37, 2.0\n" \
"s_waitcnt lgkmcnt(8)\n" \
M_"a[12:15], a[172:175], a[128:131], a[12:15]\n" \
X_"a[240:241], v36 offset:40960\n" \
"s_or_b32 s38, s38, 0x5be8000\n" \
"s_waitcnt lgkmcnt(5)\n" \
M_"a[12:15], a[236:239], a[192:195], a[12:15]\n" \
"s_cmp_lg_u32 s3, 0\n" \
"s_cselect_b32 s36, s37, s5\n" \
M_"a[28:31], a[172:175], a[132:135], a[28:31]\n" \
"s_lshl_b32 s5, s6, 2\n" \
"s_or_b32 s5, s38, s5\n" \
M_"a[28:31], a[236:239], a[196:199], a[28:31]\n" \
X_"a[242:243], v36 offset:41984\n" \
"s_lshl_b32 s35, s3, 10\n" \
M_"a[24:27], a[168:171], a[132:135], a[24:27]\n" \
"s_cmpk_lt_u32 s3, 3\n" \
"s_cselect_b32 s37, s37, s5\n" \
M_"a[24:27], a[232:235], a[196:199], a[24:27]\n" \
"s_lshl_b32 s5, s4, 16\n" \
"s_lshl_b32 s4, s4, 11\n" \
M_"a[20:23], a[164:167], a[132:135], a[20:23]\n" \
"v_cndmask_b32_e32 v198, v213, v212, vcc\n" \
"v_or_b32_e32 v212, s35, v20\n" \
M_"a[20:23], a[228:231], a[196:199], a[20:23]\n" \
X_"a[180:181], v37 offset:32768\n" \
"s_or_b32 s39, s40, s35\n" \
M_"a[16:19], a[160:163], a[132:135], a[16:19]\n" \
"s_or_b32 s55, s41, s4\n" \
"v_or_b32_e32 v199, 0x22000, v212\n" \
M_"a[16:19], a[224:227], a[196:199], a[16:19]\n" \
"s_add_i32 s35, s39, s7\n" \
"s_add_i32 s54, s41, s5\n" \
M_"a[32:35], a[160:163], a[136:139], a[32:35]\n" \
"s_mov_b32 m0, s32\n" \
X_"a[182:183], v37 offset:33792\n" \
M_"a[32:35], a[224:227], a[200:203], a[32:35]\n" \
"s_mov_b32 exec_lo, 0x0\n" \
"s_mov_b32 exec_hi, 0xff\n" \
"global_load_lds_dword v[184:185], off\n" \
"s_mov_b64 exec, -1\n" \
M_"a[36:39], a[164:167], a[136:139], a[36:39]\n" \
"s_add_i32 s55, s55, 0x80000\n" \
"v_mov_b32_e32 v215, 0xffff\n" \
M_"a[36:39], a[228:231], a[200:203], a[36:39]\n" \
X_"a[244:245], v37 offset:40960\n" \
"s_and_b32 s4, s3, 1\n" \
M_"a[40:43], a[168:171], a[136:139], a[40:43]\n" \
"s_lshl_b32 s5, s4, 4\n" \
"v_lshrrev_b32_e32 v212, 4, v20\n" \
M_"a[40:43], a[232:235], a[200:203], a[40:43]\n" \
"s_mov_b32 m0, s32\n" \
"v_lshrrev_b32_e32 v218, 6, v20\n" \
M_"a[44:47], a[172:175], a[136:139], a[44:47]\n" \
"s_mov_b32 exec_lo, 0xffff0000\n" \
"s_mov_b32 exec_hi, 0x0\n" \
"global_load_lds_dword v[186:187], off\n" \
"s_mov_b64 exec, -1\n" \
M_"a[44:47], a[236:239], a[200:203], a[44:47]\n" \
X_"a[246:247], v37 offset:41984\n" \
"v_and_b32_e64 v219, 12, v218\n" \
M_"a[60:63], a[172:175], a[140:143], a[60:63]\n" \
"v_and_or_b32 v214, v212, 15, s5\n" \
"v_cmp_gt_u32_e64 vcc, v219, v214\n" \
M_"a[60:63], a[236:239], a[204:207], a[60:63]\n" \
"v_mov_b32_e32 v220, 0xffff0000\n" \
"s_lshl_b32 s5, s2, 2\n" \
M_"a[56:59], a[168:171], a[140:143], a[56:59]\n" \
X_"a[184:185], v38 offset:32768\n" \
"v_cndmask_b32_e64 v217, v215, 0, vcc\n" \
M_"a[56:59], a[232:235], a[204:207], a[56:59]\n" \
"v_cmp_lt_u32_e32 vcc, v219, v214\n" \
"v_mov_b32_e64 v213, s23\n" \
M_"a[52:55], a[164:167], a[140:143], a[52:55]\n" \
"s_or_b32 s5, s5, s3\n" \
"v_cndmask_b32_e32 v221, 0, v220, vcc\n" \
M_"a[52:55], a[228:231], a[204:207], a[52:55]\n" \
X_"a[186:187], v38 offset:33792\n" \
"v_or_b32_e64 v12, v217, v221\n" \
M_"a[48:51], a[160:163], a[140:143], a[48:51]\n" \
"v_or_b32_e32 v217, 2, v219\n" \
"v_cmp_gt_u32_e32 vcc, v217, v214\n" \
M_"a[48:51], a[224:227], a[204:207], a[48:51]\n" \
"v_mov_b32_e32 v216, s25\n" \
"s_movk_i32 s40, 0xf400\n" \
M_"a[64:67], a[160:163], a[144:147], a[64:67]\n" \
X_"a[248:249], v38 offset:40960\n" \
"v_cndmask_b32_e64 v221, v215, 0, vcc\n" \
M_"a[64:67], a[224:227], a[208:211], a[64:67]\n" \
"v_cmp_lt_u32_e32 vcc, v217, v214\n" \
"s_lshl_b32 s4, s4, 6\n" \
M_"a[68:71], a[164:167], a[144:147], a[68:71]\n" \
"v_cndmask_b32_e32 v217, 0, v220, vcc\n" \
"v_or_b32_e32 v13, v221, v217\n" \
M_"a[68:71], a[228:231], a[208:211], a[68:71]\n" \
X_"a[250:251], v38 offset:41984\n" \
"v_or_b32_e64 v217, 16, v219\n" \
M_"a[72:75], a[168:171], a[144:147], a[72:75]\n" \
"v_cmp_gt_u32_e32 vcc, v217, v214\n" \
"s_nop 1\n" \
"v_cndmask_b32_e64 v221, v215, 0, vcc\n" \
M_"a[72:75], a[232:235], a[208:211], a[72:75]\n" \
"v_cmp_lt_u32_e32 vcc, v217, v214\n" \
"s_nop 1\n" \
"v_cndmask_b32_e64 v217, 0, v220, vcc\n" \
M_"a[76:79], a[172:175], a[144:147], a[76:79]\n" \
"v_or_b32_e32 v14, v221, v217\n" \
"v_or_b32_e32 v217, 18, v219\n" \
M_"a[76:79], a[236:239], a[208:211], a[76:79]\n" \
X_"a[188:189], v39 offset:32768\n" \
"v_cmp_gt_u32_e64 vcc, v217, v214\n" \
M_"a[92:95], a[172:175], a[148:151], a[92:95]\n" \
"s_nop 0\n" \
"v_cndmask_b32_e64 v215, v215, 0, vcc\n" \
"v_cmp_lt_u32_e32 vcc, v217, v214\n" \
M_"a[92:95], a[236:239], a[212:215], a[92:95]\n" \
"s_nop 0\n" \
"v_cndmask_b32_e32 v214, 0, v220, vcc\n" \
"v_or_b32_e64 v15, v215, v214\n" \
M_"a[88:91], a[168:171], a[148:151], a[88:91]\n" \
X_"a[190:191], v39 offset:33792\n" \
"v_mad_u64_u32 v[214:215], s[6:7], s5, 42, v[212:213]\n" \
M_"a[88:91], a[232:235], a[212:215], a[88:91]\n" \
"s_movk_i32 s5, 0x1440\n" \
"v_cmp_gt_u32_e32 vcc, s5, v214\n" \
M_"a[84:87], a[164:167], a[148:151], a[84:87]\n" \
"v_min_u32_e32 v217, 0xa43f, v214\n" \
"v_mov_b32_e64 v214, s24\n" \
M_"a[84:87], a[228:231], a[212:215], a[84:87]\n" \
X_"a[252:253], v39 offset:40960\n" \
"v_cndmask_b32_e64 v215, v216, v213, vcc\n" \
M_"a[80:83], a[160:163], a[148:151], a[80:83]\n" \
"v_mov_b32_e32 v213, s22\n" \
"s_movk_i32 s6, 0xffee\n" \
M_"a[80:83], a[224:227], a[212:215], a[80:83]\n" \
"v_cndmask_b32_e64 v214, v214, v213, vcc\n" \
"v_add_u32_e32 v213, 0xffffebc0, v217\n" \
M_"a[96:99], a[160:163], a[152:155], a[96:99]\n" \
X_"a[254:255], v39 offset:41984\n" \
"s_lshl_b32 s5, s2, 5\n" \
M_"a[96:99], a[224:227], a[216:219], a[96:99]\n" \
"v_cndmask_b32_e32 v213, v213, v217, vcc\n" \
"v_lshlrev_b32_e64 v216, 7, v213\n" \
M_"a[100:103], a[164:167], a[152:155], a[100:103]\n" \
"v_mul_u32_u24_e32 v213, 0xe39, v212\n" \
"v_mov_b32_e64 v217, 0\n" \
M_"a[100:103], a[228:231], a[216:219], a[100:103]\n" \
"v_lshrrev_b32_e64 v213, 16, v213\n" \
"v_lshl_add_u64 v[16:17], v[214:215], 0, v[216:217]\n" \
M_"a[104:107], a[168:171], a[152:155], a[104:107]\n" \
"v_mad_i32_i24 v214, v213, s6, v212\n" \
"s_add_i32 s5, s5, 32\n" \
M_"a[104:107], a[232:235], a[216:219], a[104:107]\n" \
"v_lshlrev_b32_e32 v215, 4, v214\n" \
"v_add_u32_e64 v216, s5, v215\n" \
M_"a[108:111], a[172:175], a[152:155], a[108:111]\n" \
"v_and_b32_e32 v216, 0xf0, v216\n" \
"v_cmp_gt_u32_e64 vcc, 16, v214\n" \
M_"a[108:111], a[236:239], a[216:219], a[108:111]\n" \
"s_movk_i32 s7, 0xf800\n" \
"v_cndmask_b32_e32 v214, v215, v216, vcc\n" \
"s_waitcnt lgkmcnt(15)\n" \
M_"a[124:127], a[172:175], a[156:159], a[124:127]\n" \
"v_or_b32_e32 v215, 64, v212\n" \
"v_mul_u32_u24_e32 v216, 0xe39, v215\n" \
M_"a[124:127], a[236:239], a[220:223], a[124:127]\n" \
"v_lshrrev_b32_e64 v216, 16, v216\n" \
"v_mad_i32_i24 v215, v216, s6, v215\n" \
M_"a[120:123], a[168:171], a[156:159], a[120:123]\n" \
"v_lshlrev_b32_e32 v217, 4, v215\n" \
"v_add_u32_e32 v220, s5, v217\n" \
M_"a[120:123], a[232:235], a[220:223], a[120:123]\n" \
"v_and_b32_e32 v220, 0xf0, v220\n" \
"v_cmp_gt_u32_e64 vcc, 16, v215\n" \
M_"a[116:119], a[164:167], a[156:159], a[116:119]\n" \
"s_nop 0\n" \
"v_cndmask_b32_e32 v215, v217, v220, vcc\n" \
"v_or_b32_e32 v217, 0x80, v212\n" \
M_"a[116:119], a[228:231], a[220:223], a[116:119]\n" \
"v_mul_u32_u24_e32 v220, 0xe39, v217\n" \
"v_lshrrev_b32_e64 v220, 16, v220\n" \
M_"a[112:115], a[160:163], a[156:159], a[112:115]\n" \
"v_mad_i32_i24 v217, v220, s6, v217\n" \
"v_lshlrev_b32_e64 v221, 4, v217\n" \
M_"a[112:115], a[224:227], a[220:223], a[112:115]\n" \
"s_waitcnt vmcnt(20)\n" \
"v_readfirstlane_b32 s62, v180\n" \
"s_nop 3\n" \
"s_add_u32 s62, s62, 0x9E3779B1\n" \
"s_waitcnt vmcnt(2) lgkmcnt(0)\n" \
"s_barrier\n" \
M_"v[52:55], a[176:179], a[128:131], v[52:55]\n" \
"s_add_u32 m0, s30, 0x8000\n" \
"s_movk_i32 s84, 0x180\n" \
M_"v[52:55], a[240:243], a[192:195], v[52:55]\n" \
B_"v22, s[64:67], s84 offen offset:0 lds\n" \
M_"v[56:59], a[180:183], a[128:131], v[56:59]\n" \
X_"a[160:161], v32 offset:0\n" \
"v_add_u32_e64 v222, s5, v221\n" \
M_"v[56:59], a[244:247], a[192:195], v[56:59]\n" \
"s_movk_i32 s84, 0x1d80\n" \
M_"v[60:63], a[184:187], a[128:131], v[60:63]\n" \
B_"v23, s[64:67], s84 offen offset:1024 lds\n" \
M_"v[60:63], a[248:251], a[192:195], v[60:63]\n" \
X_"a[162:163], v32 offset:1024\n" \
"v_and_b32_e32 v222, 0xf0, v222\n" \
M_"v[64:67], a[188:191], a[128:131], v[64:67]\n" \
X_"a[224:225], v32 offset:8192\n" \
"v_cmp_gt_u32_e32 vcc, 16, v217\n" \
M_"v[64:67], a[252:255], a[192:195], v[64:67]\n" \
R_"a[128:131], v28 offset:16384\n" \
"v_cndmask_b32_e64 v217, v221, v222, vcc\n" \
M_"v[80:83], a[188:191], a[132:135], v[80:83]\n" \
R_"a[192:195], v29 offset:16384\n" \
"v_or_b32_e32 v221, 0xc0, v212\n" \
M_"v[80:83], a[252:255], a[196:199], v[80:83]\n" \
"s_movk_i32 s84, 0x3980\n" \
M_"v[76:79], a[184:187], a[132:135], v[76:79]\n" \
B_"v22, s[64:67], s84 offen offset:2048 lds\n" \
M_"v[76:79], a[248:251], a[196:199], v[76:79]\n" \
"s_movk_i32 s84, 0x5580\n" \
M_"v[72:75], a[180:183], a[132:135], v[72:75]\n" \
B_"v23, s[64:67], s84 offen offset:3072 lds\n" \
M_"v[72:75], a[244:247], a[196:199], v[72:75]\n" \
"s_add_u32 m0, s31, 0x8000\n" \
"s_mov_b32 s84, 0x180000\n" \
M_"v[68:71], a[176:179], a[132:135], v[68:71]\n" \
B_"v26, s[68:71], s84 offen offset:0 lds\n" \
M_"v[68:71], a[240:243], a[196:199], v[68:71]\n" \
R_"a[132:135], v28 offset:18432\n" \
"v_mul_u32_u24_e32 v222, 0xe39, v221\n" \
M_"v[84:87], a[176:179], a[136:139], v[84:87]\n" \
R_"a[196:199], v29 offset:18432\n" \
"v_lshrrev_b32_e64 v222, 16, v222\n" \
M_"v[84:87], a[240:243], a[200:203], v[84:87]\n" \
"s_addk_i32 s84, 0x7c00\n" \
M_"v[88:91], a[180:183], a[136:139], v[88:91]\n" \
B_"v26, s[68:71], s84 offen offset:1024 lds\n" \
M_"v[88:91], a[244:247], a[200:203], v[88:91]\n" \
X_"a[226:227], v32 offset:9216\n" \
"v_mad_i32_i24 v221, v222, s6, v221\n" \
M_"v[92:95], a[184:187], a[136:139], v[92:95]\n" \
"s_addk_i32 s84, 0x7c00\n" \
M_"v[92:95], a[248:251], a[200:203], v[92:95]\n" \
B_"v27, s[68:71], s84 offen offset:2048 lds\n" \
M_"v[96:99], a[188:191], a[136:139], v[96:99]\n" \
X_"a[164:165], v33 offset:0\n" \
"v_lshlrev_b32_e64 v223, 4, v221\n" \
M_"v[96:99], a[252:255], a[200:203], v[96:99]\n" \
R_"a[136:139], v28 offset:20480\n" \
"v_add_u32_e64 v224, s5, v223\n" \
M_"v[112:115], a[188:191], a[140:143], v[112:115]\n" \
R_"a[200:203], v29 offset:20480\n" \
"v_and_b32_e32 v224, 0xf0, v224\n" \
M_"v[112:115], a[252:255], a[204:207], v[112:115]\n" \
"s_addk_i32 s84, 0x7c00\n" \
M_"v[108:111], a[184:187], a[140:143], v[108:111]\n" \
B_"v27, s[68:71], s84 offen offset:3072 lds\n" \
M_"v[108:111], a[248:251], a[204:207], v[108:111]\n" \
"s_add_u32 m0, s30, 0x9000\n" \
"s_mov_b32 s84, 0x8180\n" \
M_"v[104:107], a[180:183], a[140:143], v[104:107]\n" \
B_"v22, s[64:67], s84 offen offset:0 lds\n" \
M_"v[104:107], a[244:247], a[204:207], v[104:107]\n" \
X_"a[166:167], v33 offset:1024\n" \
"v_cmp_gt_u32_e32 vcc, 16, v221\n" \
M_"v[100:103], a[176:179], a[140:143], v[100:103]\n" \
X_"a[228:229], v33 offset:8192\n" \
"v_cndmask_b32_e64 v221, v223, v224, vcc\n" \
M_"v[100:103], a[240:243], a[204:207], v[100:103]\n" \
R_"a[140:143], v28 offset:22528\n" \
"v_or_b32_e32 v223, 0x100, v212\n" \
M_"v[116:119], a[176:179], a[144:147], v[116:119]\n" \
R_"a[204:207], v29 offset:22528\n" \
"v_mul_u32_u24_e32 v224, 0xe39, v223\n" \
M_"v[116:119], a[240:243], a[208:211], v[116:119]\n" \
"s_addk_i32 s84, 0x1c00\n" \
M_"v[120:123], a[180:183], a[144:147], v[120:123]\n" \
B_"v23, s[64:67], s84 offen offset:1024 lds\n" \
M_"v[120:123], a[244:247], a[208:211], v[120:123]\n" \
"s_addk_i32 s84, 0x1c00\n" \
M_"v[124:127], a[184:187], a[144:147], v[124:127]\n" \
B_"v22, s[64:67], s84 offen offset:2048 lds\n" \
M_"v[124:127], a[248:251], a[208:211], v[124:127]\n" \
"s_addk_i32 s84, 0x1c00\n" \
M_"v[128:131], a[188:191], a[144:147], v[128:131]\n" \
B_"v23, s[64:67], s84 offen offset:3072 lds\n" \
M_"v[128:131], a[252:255], a[208:211], v[128:131]\n" \
R_"a[144:147], v28 offset:0\n" \
"v_lshrrev_b32_e32 v224, 16, v224\n" \
M_"v[144:147], a[188:191], a[148:151], v[144:147]\n" \
R_"a[208:211], v29 offset:0\n" \
"v_mad_i32_i24 v223, v224, s6, v223\n" \
M_"v[144:147], a[252:255], a[212:215], v[144:147]\n" \
"s_add_u32 m0, s31, 0xc000\n" \
"s_mov_b32 s84, 0x180000\n" \
M_"v[140:143], a[184:187], a[148:151], v[140:143]\n" \
B_"v26, s[72:75], s84 offen offset:0 lds\n" \
M_"v[140:143], a[248:251], a[212:215], v[140:143]\n" \
X_"a[230:231], v33 offset:9216\n" \
"v_lshlrev_b32_e64 v225, 4, v223\n" \
M_"v[136:139], a[180:183], a[148:151], v[136:139]\n" \
"s_addk_i32 s84, 0x7c00\n" \
M_"v[136:139], a[244:247], a[212:215], v[136:139]\n" \
B_"v26, s[72:75], s84 offen offset:1024 lds\n" \
M_"v[132:135], a[176:179], a[148:151], v[132:135]\n" \
X_"a[168:169], v34 offset:0\n" \
"v_add_u32_e32 v226, s5, v225\n" \
M_"v[132:135], a[240:243], a[212:215], v[132:135]\n" \
R_"a[148:151], v28 offset:2048\n" \
"v_and_b32_e32 v226, 0xf0, v226\n" \
M_"v[148:151], a[176:179], a[152:155], v[148:151]\n" \
R_"a[212:215], v29 offset:2048\n" \
"v_cmp_gt_u32_e64 vcc, 16, v223\n" \
M_"v[148:151], a[240:243], a[216:219], v[148:151]\n" \
X_"a[170:171], v34 offset:1024\n" \
"v_cndmask_b32_e64 v223, v225, v226, vcc\n" \
M_"v[152:155], a[180:183], a[152:155], v[152:155]\n" \
X_"a[232:233], v34 offset:8192\n" \
"v_or_b32_e32 v225, 0x140, v212\n" \
M_"v[152:155], a[244:247], a[216:219], v[152:155]\n" \
X_"a[234:235], v34 offset:9216\n" \
"v_mul_u32_u24_e32 v226, 0xe39, v225\n" \
M_"v[156:159], a[184:187], a[152:155], v[156:159]\n" \
X_"a[172:173], v35 offset:0\n" \
"v_lshrrev_b32_e64 v226, 16, v226\n" \
M_"v[156:159], a[248:251], a[216:219], v[156:159]\n" \
X_"a[174:175], v35 offset:1024\n" \
"v_mad_i32_i24 v225, v226, s6, v225\n" \
M_"v[160:163], a[188:191], a[152:155], v[160:163]\n" \
X_"a[236:237], v35 offset:8192\n" \
"v_lshlrev_b32_e64 v227, 4, v225\n" \
M_"v[160:163], a[252:255], a[216:219], v[160:163]\n" \
R_"a[152:155], v28 offset:4096\n" \
"v_add_u32_e64 v228, s5, v227\n" \
M_"v[176:179], a[188:191], a[156:159], v[176:179]\n" \
R_"a[216:219], v29 offset:4096\n" \
"v_and_b32_e32 v228, 0xf0, v228\n" \
M_"v[176:179], a[252:255], a[220:223], v[176:179]\n" \
X_"a[238:239], v35 offset:9216\n" \
"v_cmp_gt_u32_e64 vcc, 16, v225\n" \
M_"v[172:175], a[184:187], a[156:159], v[172:175]\n" \
"s_nop 0\n" \
"v_cndmask_b32_e32 v225, v227, v228, vcc\n" \
"v_or_b32_e32 v227, 0x180, v212\n" \
M_"v[172:175], a[248:251], a[220:223], v[172:175]\n" \
"v_mul_u32_u24_e32 v228, 0xe39, v227\n" \
"v_lshrrev_b32_e64 v228, 16, v228\n" \
M_"v[168:171], a[180:183], a[156:159], v[168:171]\n" \
"v_mad_i32_i24 v227, v228, s6, v227\n" \
"v_lshlrev_b32_e64 v229, 4, v227\n" \
M_"v[168:171], a[244:247], a[220:223], v[168:171]\n" \
"v_add_u32_e64 v230, s5, v229\n" \
"v_and_b32_e32 v230, 0xf0, v230\n" \
M_"v[164:167], a[176:179], a[156:159], v[164:167]\n" \
"v_cmp_gt_u32_e32 vcc, 16, v227\n" \
"s_nop 1\n" \
"v_cndmask_b32_e64 v227, v229, v230, vcc\n" \
M_"v[164:167], a[240:243], a[220:223], v[164:167]\n" \
R_"a[156:159], v28 offset:6144\n" \
R_"a[220:223], v29 offset:6144\n" \
"s_waitcnt lgkmcnt(15)\n" \
M_"a[0:3], a[160:163], a[128:131], a[0:3]\n" \
"s_addk_i32 s84, 0x7c00\n" \
M_"a[0:3], a[224:227], a[192:195], a[0:3]\n" \
B_"v27, s[72:75], s84 offen offset:2048 lds\n" \
M_"a[4:7], a[164:167], a[128:131], a[4:7]\n" \
X_"a[176:177], v36 offset:0\n" \
"v_or_b32_e32 v229, 0x1c0, v212\n" \
"s_waitcnt lgkmcnt(15)\n" \
M_"a[4:7], a[228:231], a[192:195], a[4:7]\n" \
"s_addk_i32 s84, 0x7c00\n" \
"s_waitcnt lgkmcnt(11)\n" \
M_"a[8:11], a[168:171], a[128:131], a[8:11]\n" \
B_"v27, s[72:75], s84 offen offset:3072 lds\n" \
"s_waitcnt lgkmcnt(9)\n" \
M_"a[8:11], a[232:235], a[192:195], a[8:11]\n" \
X_"a[178:179], v36 offset:1024\n" \
"v_mul_u32_u24_e32 v230, 0xe39, v229\n" \
"s_waitcnt lgkmcnt(8)\n" \
M_"a[12:15], a[172:175], a[128:131], a[12:15]\n" \
X_"a[240:241], v36 offset:8192\n" \
"v_lshrrev_b32_e32 v230, 16, v230\n" \
"s_waitcnt lgkmcnt(5)\n" \
M_"a[12:15], a[236:239], a[192:195], a[12:15]\n" \
"v_mad_i32_i24 v229, v230, s6, v229\n" \
"v_lshlrev_b32_e32 v231, 4, v229\n" \
M_"a[28:31], a[172:175], a[132:135], a[28:31]\n" \
"v_add_u32_e64 v232, s5, v231\n" \
"v_and_b32_e32 v232, 0xf0, v232\n" \
M_"a[28:31], a[236:239], a[196:199], a[28:31]\n" \
X_"a[242:243], v36 offset:9216\n" \
"v_cmp_gt_u32_e64 vcc, 16, v229\n" \
M_"a[24:27], a[168:171], a[132:135], a[24:27]\n" \
B_"v[200:203], v192, s[76:79], s34 offen offset:0\n" \
M_"a[24:27], a[232:235], a[196:199], a[24:27]\n" \
"v_cndmask_b32_e64 v229, v231, v232, vcc\n" \
"v_or_b32_e32 v231, 0x200, v212\n" \
M_"a[20:23], a[164:167], a[132:135], a[20:23]\n" \
"v_mul_u32_u24_e32 v232, 0xe39, v231\n" \
"v_lshrrev_b32_e64 v232, 16, v232\n" \
M_"a[20:23], a[228:231], a[196:199], a[20:23]\n" \
X_"a[180:181], v37 offset:0\n" \
"v_mad_i32_i24 v231, v232, s6, v231\n" \
M_"a[16:19], a[160:163], a[132:135], a[16:19]\n" \
"v_lshlrev_b32_e32 v233, 4, v231\n" \
"v_add_u32_e32 v234, s5, v233\n" \
M_"a[16:19], a[224:227], a[196:199], a[16:19]\n" \
"s_movk_i32 s5, 0x480\n" \
"v_mad_u32_u24 v0, v213, s5, v214\n" \
M_"a[32:35], a[160:163], a[136:139], a[32:35]\n" \
B_"v[204:207], v193, s[76:79], s34 offen offset:0\n" \
M_"a[32:35], a[224:227], a[200:203], a[32:35]\n" \
"s_mov_b32 m0, s32\n" \
X_"a[182:183], v37 offset:1024\n" \
M_"a[36:39], a[164:167], a[136:139], a[36:39]\n" \
"s_mov_b32 exec_lo, 0x0\n" \
"s_mov_b32 exec_hi, 0xff00\n" \
"global_load_lds_dword v[184:185], off\n" \
"s_mov_b64 exec, -1\n" \
M_"a[36:39], a[228:231], a[200:203], a[36:39]\n" \
X_"a[244:245], v37 offset:8192\n" \
"v_mul_u32_u24_e32 v213, 0x480, v216\n" \
M_"a[40:43], a[168:171], a[136:139], a[40:43]\n" \
"s_movk_i32 s6, 0xfc00\n" \
"s_nop 0\n" \
"v_add3_u32 v1, v213, v215, s6\n" \
M_"a[40:43], a[232:235], a[200:203], a[40:43]\n" \
"buffer_load_dword v208, v194, s[76:79], s34 offen offset:32\n" \
M_"a[44:47], a[172:175], a[136:139], a[44:47]\n" \
X_"a[246:247], v37 offset:9216\n" \
"v_mul_u32_u24_e32 v213, 0x480, v220\n" \
M_"a[44:47], a[236:239], a[200:203], a[44:47]\n" \
"v_add3_u32 v2, v213, v217, s7\n" \
"v_mul_u32_u24_e32 v213, 0x480, v222\n" \
M_"a[60:63], a[172:175], a[140:143], a[60:63]\n" \
"v_add3_u32 v3, v213, v221, s40\n" \
"v_mul_u32_u24_e32 v213, 0x480, v226\n" \
M_"a[60:63], a[236:239], a[204:207], a[60:63]\n" \
"v_add3_u32 v5, v213, v225, s6\n" \
"v_mul_u32_u24_e32 v213, 0x480, v228\n" \
M_"a[56:59], a[168:171], a[140:143], a[56:59]\n" \
"buffer_load_dword v209, v194, s[76:79], s34 offen offset:1184\n" \
M_"a[56:59], a[232:235], a[204:207], a[56:59]\n" \
X_"a[184:185], v38 offset:0\n" \
"v_add3_u32 v6, v213, v227, s7\n" \
M_"a[52:55], a[164:167], a[140:143], a[52:55]\n" \
"v_mul_u32_u24_e32 v213, 0x480, v230\n" \
"v_add3_u32 v7, v213, v229, s40\n" \
M_"a[52:55], a[228:231], a[204:207], a[52:55]\n" \
X_"a[186:187], v38 offset:1024\n" \
"s_lshl_b32 s7, s3, 3\n" \
M_"a[48:51], a[160:163], a[140:143], a[48:51]\n" \
"s_lshl_b32 s40, s3, 1\n" \
"s_and_b32 s7, s7, 16\n" \
M_"a[48:51], a[224:227], a[204:207], a[48:51]\n" \
"s_and_b32 s40, s40, 2\n" \
"s_and_b32 s6, s2, 7\n" \
M_"a[64:67], a[160:163], a[144:147], a[64:67]\n" \
"buffer_load_dword v211, v194, s[76:79], s34 offen offset:2336\n" \
M_"a[64:67], a[224:227], a[208:211], a[64:67]\n" \
X_"a[248:249], v38 offset:8192\n" \
"v_bfe_u32 v213, v20, 7, 1\n" \
M_"a[68:71], a[164:167], a[144:147], a[68:71]\n" \
"s_or_b32 s7, s40, s7\n" \
"v_and_b32_e32 v234, 0xf0, v234\n" \
M_"a[68:71], a[228:231], a[208:211], a[68:71]\n" \
X_"a[250:251], v38 offset:9216\n" \
"v_cmp_gt_u32_e64 vcc, 16, v231\n" \
M_"a[72:75], a[168:171], a[144:147], a[72:75]\n" \
"v_or3_b32 v213, s7, v213, v219\n" \
"v_and_b32_e64 v214, 32, v20\n" \
M_"a[72:75], a[232:235], a[208:211], a[72:75]\n" \
"s_mov_b32 s76, s20\n" \
"s_and_b32 s77, s21, 0xffff\n" \
M_"a[76:79], a[172:175], a[144:147], a[76:79]\n" \
"s_mov_b32 s78, 0x480\n" \
"s_lshl_b32 s6, s6, 5\n" \
M_"a[76:79], a[236:239], a[208:211], a[76:79]\n" \
"buffer_load_ushort v210, v195, s[76:79], 0 offen\n" \
M_"a[92:95], a[172:175], a[148:151], a[92:95]\n" \
X_"a[188:189], v39 offset:0\n" \
"v_cndmask_b32_e32 v231, v233, v234, vcc\n" \
".fill (L_pb + 7904 - .) / 4, 4, 0xBF800000\n" \
"L_pp2:\n" \
"s_cbranch_execz L_pret\n" \
M_"a[92:95], a[236:239], a[212:215], a[92:95]\n" \
"v_mul_u32_u24_e32 v213, 18, v213\n" \
"v_mov_b32_e32 v215, 0x100\n" \
M_"a[88:91], a[168:171], a[148:151], a[88:91]\n" \
"s_mov_b32 s76, s8\n" \
"s_and_b32 s77, s9, 0xffff\n" \
M_"a[88:91], a[232:235], a[212:215], a[88:91]\n" \
"s_mov_b32 s78, 0x240000\n" \
X_"a[190:191], v39 offset:1024\n" \
M_"a[84:87], a[164:167], a[148:151], a[84:87]\n" \
"v_mov_b32_e32 v216, s6\n" \
"v_cmp_eq_u32_e64 vcc, 0, v214\n" \
M_"a[84:87], a[228:231], a[212:215], a[84:87]\n" \
X_"a[252:253], v39 offset:8192\n" \
"v_and_or_b32 v213, v218, 1, v213\n" \
M_"a[80:83], a[160:163], a[148:151], a[80:83]\n" \
"v_lshlrev_b32_e32 v212, 3, v212\n" \
"v_cndmask_b32_e32 v214, v215, v216, vcc\n" \
M_"a[80:83], a[224:227], a[212:215], a[80:83]\n" \
"v_mad_u32_u24 v213, v213, s5, v214\n" \
"v_and_or_b32 v213, v20, 16, v213\n" \
M_"a[96:99], a[160:163], a[152:155], a[96:99]\n" \
X_"a[254:255], v39 offset:9216\n" \
"v_add_u32_e32 v9, 0x4800, v213\n" \
M_"a[96:99], a[224:227], a[216:219], a[96:99]\n" \
"v_mul_u32_u24_e32 v213, 0x120, v218\n" \
"v_and_or_b32 v10, v212, 24, v213\n" \
M_"a[100:103], a[164:167], a[152:155], a[100:103]\n" \
"v_lshrrev_b32_e64 v213, 2, v20\n" \
"v_and_or_b32 v213, v213, 60, s4\n" \
M_"a[100:103], a[228:231], a[216:219], a[100:103]\n" \
"v_lshl_or_b32 v212, s3, 9, v212\n" \
"v_mad_u32_u24 v4, v224, s5, v223\n" \
M_"a[104:107], a[168:171], a[152:155], a[104:107]\n" \
"v_mad_u32_u24 v8, v232, s5, v231\n" \
"v_or_b32_e32 v18, 0x27000, v213\n" \
M_"a[104:107], a[232:235], a[216:219], a[104:107]\n" \
"v_or_b32_e32 v19, 0x27800, v212\n" \
"v_mov_b32_e32 v215, 0x1000c0c\n" \
M_"a[108:111], a[172:175], a[152:155], a[108:111]\n" \
"v_mov_b32_e32 v216, 0xc0c0100\n" \
"v_bfe_u32 v214, v20, 4, 1\n" \
M_"a[108:111], a[236:239], a[216:219], a[108:111]\n" \
"v_lshrrev_b32_e32 v212, 8, v20\n" \
"v_cmp_eq_u32_e32 vcc, 0, v214\n" \
"s_waitcnt lgkmcnt(15)\n" \
M_"a[124:127], a[172:175], a[156:159], a[124:127]\n" \
"v_bfe_u32 v213, v20, 4, 4\n" \
"v_mov_b32_e32 v217, 0xc0c0302\n" \
M_"a[124:127], a[236:239], a[220:223], a[124:127]\n" \
"v_cndmask_b32_e32 v215, v215, v216, vcc\n" \
"v_mov_b32_e32 v216, 0x3020c0c\n" \
M_"a[120:123], a[168:171], a[156:159], a[120:123]\n" \
"v_lshl_or_b32 v214, v212, 2, v214\n" \
"v_cndmask_b32_e64 v216, v216, v217, vcc\n" \
M_"a[120:123], a[232:235], a[220:223], a[120:123]\n" \
"v_mov_b32_e32 v217, 0xc0c0c0c\n" \
"v_cmp_gt_u32_e64 vcc, v214, v213\n" \
M_"a[116:119], a[164:167], a[156:159], a[116:119]\n" \
"v_or_b32_e32 v214, 2, v214\n" \
"s_lshr_b32 s4, s2, 5\n" \
M_"a[116:119], a[228:231], a[220:223], a[116:119]\n" \
"v_cndmask_b32_e32 v40, v217, v215, vcc\n" \
"v_cndmask_b32_e32 v42, v215, v217, vcc\n" \
M_"a[112:115], a[160:163], a[156:159], a[112:115]\n" \
"v_cmp_gt_u32_e32 vcc, v214, v213\n" \
"v_and_b32_e32 v214, 16, v20\n" \
"s_waitcnt vmcnt(7) lgkmcnt(0)\n" \
M_"a[112:115], a[224:227], a[220:223], a[112:115]\n" \
"s_barrier\n" \
M_"v[52:55], a[176:179], a[128:131], v[52:55]\n" \
"s_add_u32 m0, s30, 0x0\n" \
"s_movk_i32 s84, 0x200\n" \
M_"v[52:55], a[240:243], a[192:195], v[52:55]\n" \
B_"v22, s[64:67], s84 offen offset:0 lds\n" \
M_"v[56:59], a[180:183], a[128:131], v[56:59]\n" \
X_"a[160:161], v32 offset:32768\n" \
"s_movk_i32 s42, 0x3f80\n" \
M_"v[56:59], a[244:247], a[192:195], v[56:59]\n" \
"s_movk_i32 s84, 0x1e00\n" \
M_"v[60:63], a[184:187], a[128:131], v[60:63]\n" \
B_"v23, s[64:67], s84 offen offset:1024 lds\n" \
M_"v[60:63], a[248:251], a[192:195], v[60:63]\n" \
X_"a[162:163], v32 offset:33792\n" \
"s_xor_b32 s5, s4, 15\n" \
M_"v[64:67], a[188:191], a[128:131], v[64:67]\n" \
X_"a[224:225], v32 offset:40960\n" \
M_"v[64:67], a[252:255], a[192:195], v[64:67]\n" \
R_"a[128:131], v28 offset:49152\n" \
M_"v[80:83], a[188:191], a[132:135], v[80:83]\n" \
R_"a[192:195], v29 offset:49152\n" \
M_"v[80:83], a[252:255], a[196:199], v[80:83]\n" \
"s_movk_i32 s84, 0x3a00\n" \
M_"v[76:79], a[184:187], a[132:135], v[76:79]\n" \
B_"v22, s[64:67], s84 offen offset:2048 lds\n" \
M_"v[76:79], a[248:251], a[196:199], v[76:79]\n" \
"s_movk_i32 s84, 0x5600\n" \
M_"v[72:75], a[180:183], a[132:135], v[72:75]\n" \
B_"v23, s[64:67], s84 offen offset:3072 lds\n" \
M_"v[72:75], a[244:247], a[196:199], v[72:75]\n" \
"s_add_u32 m0, s31, 0x0\n" \
"s_mov_b32 s84, 0x200000\n" \
M_"v[68:71], a[176:179], a[132:135], v[68:71]\n" \
B_"v26, s[68:71], s84 offen offset:0 lds\n" \
M_"v[68:71], a[240:243], a[196:199], v[68:71]\n" \
R_"a[132:135], v28 offset:51200\n" \
M_"v[84:87], a[176:179], a[136:139], v[84:87]\n" \
R_"a[196:199], v29 offset:51200\n" \
M_"v[84:87], a[240:243], a[200:203], v[84:87]\n" \
"s_addk_i32 s84, 0x7c00\n" \
M_"v[88:91], a[180:183], a[136:139], v[88:91]\n" \
B_"v26, s[68:71], s84 offen offset:1024 lds\n" \
M_"v[88:91], a[244:247], a[200:203], v[88:91]\n" \
X_"a[226:227], v32 offset:41984\n" \
M_"v[92:95], a[184:187], a[136:139], v[92:95]\n" \
"s_addk_i32 s84, 0x7c00\n" \
M_"v[92:95], a[248:251], a[200:203], v[92:95]\n" \
B_"v27, s[68:71], s84 offen offset:2048 lds\n" \
M_"v[96:99], a[188:191], a[136:139], v[96:99]\n" \
X_"a[164:165], v33 offset:32768\n" \
M_"v[96:99], a[252:255], a[200:203], v[96:99]\n" \
R_"a[136:139], v28 offset:53248\n" \
M_"v[112:115], a[188:191], a[140:143], v[112:115]\n" \
R_"a[200:203], v29 offset:53248\n" \
M_"v[112:115], a[252:255], a[204:207], v[112:115]\n" \
"s_addk_i32 s84, 0x7c00\n" \
"s_nop 0\n" \
M_"v[108:111], a[184:187], a[140:143], v[108:111]\n" \
B_"v27, s[68:71], s84 offen offset:3072 lds\n" \
M_"v[108:111], a[248:251], a[204:207], v[108:111]\n" \
"s_add_u32 m0, s30, 0x1000\n" \
"s_mov_b32 s84, 0x8200\n" \
M_"v[104:107], a[180:183], a[140:143], v[104:107]\n" \
B_"v22, s[64:67], s84 offen offset:0 lds\n" \
M_"v[104:107], a[244:247], a[204:207], v[104:107]\n" \
X_"a[166:167], v33 offset:33792\n" \
M_"v[100:103], a[176:179], a[140:143], v[100:103]\n" \
X_"a[228:229], v33 offset:40960\n" \
M_"v[100:103], a[240:243], a[204:207], v[100:103]\n" \
R_"a[140:143], v28 offset:55296\n" \
M_"v[116:119], a[176:179], a[144:147], v[116:119]\n" \
R_"a[204:207], v29 offset:55296\n" \
M_"v[116:119], a[240:243], a[208:211], v[116:119]\n" \
"s_addk_i32 s84, 0x1c00\n" \
M_"v[120:123], a[180:183], a[144:147], v[120:123]\n" \
B_"v23, s[64:67], s84 offen offset:1024 lds\n" \
M_"v[120:123], a[244:247], a[208:211], v[120:123]\n" \
"s_addk_i32 s84, 0x1c00\n" \
M_"v[124:127], a[184:187], a[144:147], v[124:127]\n" \
B_"v22, s[64:67], s84 offen offset:2048 lds\n" \
M_"v[124:127], a[248:251], a[208:211], v[124:127]\n" \
"s_addk_i32 s84, 0x1c00\n" \
"s_nop 0\n" \
M_"v[128:131], a[188:191], a[144:147], v[128:131]\n" \
B_"v23, s[64:67], s84 offen offset:3072 lds\n" \
M_"v[128:131], a[252:255], a[208:211], v[128:131]\n" \
R_"a[144:147], v28 offset:32768\n" \
M_"v[144:147], a[188:191], a[148:151], v[144:147]\n" \
R_"a[208:211], v29 offset:32768\n" \
M_"v[144:147], a[252:255], a[212:215], v[144:147]\n" \
"s_add_u32 m0, s31, 0x4000\n" \
"s_mov_b32 s84, 0x200000\n" \
M_"v[140:143], a[184:187], a[148:151], v[140:143]\n" \
B_"v26, s[72:75], s84 offen offset:0 lds\n" \
M_"v[140:143], a[248:251], a[212:215], v[140:143]\n" \
X_"a[230:231], v33 offset:41984\n" \
M_"v[136:139], a[180:183], a[148:151], v[136:139]\n" \
"s_addk_i32 s84, 0x7c00\n" \
"s_nop 0\n" \
M_"v[136:139], a[244:247], a[212:215], v[136:139]\n" \
B_"v26, s[72:75], s84 offen offset:1024 lds\n" \
M_"v[132:135], a[176:179], a[148:151], v[132:135]\n" \
X_"a[168:169], v34 offset:32768\n" \
M_"v[132:135], a[240:243], a[212:215], v[132:135]\n" \
R_"a[148:151], v28 offset:34816\n" \
M_"v[148:151], a[176:179], a[152:155], v[148:151]\n" \
R_"a[212:215], v29 offset:34816\n" \
M_"v[148:151], a[240:243], a[216:219], v[148:151]\n" \
X_"a[170:171], v34 offset:33792\n" \
M_"v[152:155], a[180:183], a[152:155], v[152:155]\n" \
X_"a[232:233], v34 offset:40960\n" \
M_"v[152:155], a[244:247], a[216:219], v[152:155]\n" \
X_"a[234:235], v34 offset:41984\n" \
M_"v[156:159], a[184:187], a[152:155], v[156:159]\n" \
X_"a[172:173], v35 offset:32768\n" \
M_"v[156:159], a[248:251], a[216:219], v[156:159]\n" \
X_"a[174:175], v35 offset:33792\n" \
M_"v[160:163], a[188:191], a[152:155], v[160:163]\n" \
X_"a[236:237], v35 offset:40960\n" \
M_"v[160:163], a[252:255], a[216:219], v[160:163]\n" \
R_"a[152:155], v28 offset:36864\n" \
M_"v[176:179], a[188:191], a[156:159], v[176:179]\n" \
R_"a[216:219], v29 offset:36864\n" \
M_"v[176:179], a[252:255], a[220:223], v[176:179]\n" \
X_"a[238:239], v35 offset:41984\n" \
M_"v[172:175], a[184:187], a[156:159], v[172:175]\n" \
"s_cmpk_lt_u32 s3, 2\n" \
"s_cselect_b32 s6, s5, s4\n" \
M_"v[172:175], a[248:251], a[220:223], v[172:175]\n" \
"s_lshl_b32 s7, s6, 7\n" \
"v_and_b32_e32 v213, 0x320, v20\n" \
M_"v[168:171], a[180:183], a[156:159], v[168:171]\n" \
"s_movk_i32 s41, 0x100\n" \
"v_lshlrev_b32_e64 v214, v214, s42\n" \
M_"v[168:171], a[244:247], a[220:223], v[168:171]\n" \
"v_mov_b32_e32 v215, 0\n" \
"s_add_i32 s40, s7, -16\n" \
M_"v[164:167], a[176:179], a[156:159], v[164:167]\n" \
"v_cndmask_b32_e32 v41, v217, v216, vcc\n" \
"v_cndmask_b32_e32 v43, v216, v217, vcc\n" \
M_"v[164:167], a[240:243], a[220:223], v[164:167]\n" \
R_"a[156:159], v28 offset:38912\n" \
R_"a[220:223], v29 offset:38912\n" \
"s_waitcnt lgkmcnt(15)\n" \
M_"a[0:3], a[160:163], a[128:131], a[0:3]\n" \
"s_addk_i32 s84, 0x7c00\n" \
M_"a[0:3], a[224:227], a[192:195], a[0:3]\n" \
B_"v27, s[72:75], s84 offen offset:2048 lds\n" \
M_"a[4:7], a[164:167], a[128:131], a[4:7]\n" \
X_"a[176:177], v36 offset:32768\n" \
"v_cmp_eq_u32_e32 vcc, s41, v213\n" \
"s_waitcnt lgkmcnt(15)\n" \
M_"a[4:7], a[228:231], a[192:195], a[4:7]\n" \
"s_addk_i32 s84, 0x7c00\n" \
"s_waitcnt lgkmcnt(11)\n" \
M_"a[8:11], a[168:171], a[128:131], a[8:11]\n" \
B_"v27, s[72:75], s84 offen offset:3072 lds\n" \
"s_waitcnt lgkmcnt(9)\n" \
M_"a[8:11], a[232:235], a[192:195], a[8:11]\n" \
X_"a[178:179], v36 offset:33792\n" \
"s_mov_b32 s41, 0\n" \
"s_waitcnt lgkmcnt(8)\n" \
M_"a[12:15], a[172:175], a[128:131], a[12:15]\n" \
X_"a[240:241], v36 offset:40960\n" \
"s_and_b32 s43, s3, 1\n" \
"s_waitcnt lgkmcnt(5)\n" \
M_"a[12:15], a[236:239], a[192:195], a[12:15]\n" \
"v_cndmask_b32_e32 v46, v215, v214, vcc\n" \
"v_and_b32_e32 v213, 0x330, v20\n" \
M_"a[28:31], a[172:175], a[132:135], a[28:31]\n" \
"s_cmp_lg_u32 s7, 0\n" \
"s_cselect_b32 s40, s40, s41\n" \
M_"a[28:31], a[236:239], a[196:199], a[28:31]\n" \
X_"a[242:243], v36 offset:41984\n" \
"s_mov_b32 s42, 0xff800000\n" \
M_"a[24:27], a[168:171], a[132:135], a[24:27]\n" \
"s_cmp_lg_u32 s7, 0\n" \
"s_cselect_b32 s47, s41, s42\n" \
M_"a[24:27], a[232:235], a[196:199], a[24:27]\n" \
"s_lshl_b32 s41, s43, 6\n" \
"v_lshrrev_b32_e32 v215, 6, v20\n" \
M_"a[20:23], a[164:167], a[132:135], a[20:23]\n" \
"v_mov_b32_e32 v214, 0x3f80\n" \
"v_cmp_eq_u32_e64 vcc, 32, v213\n" \
M_"a[20:23], a[228:231], a[196:199], a[20:23]\n" \
X_"a[180:181], v37 offset:32768\n" \
"s_or_b32 s7, s41, s7\n" \
M_"a[16:19], a[160:163], a[132:135], a[16:19]\n" \
"v_lshrrev_b32_e32 v213, 7, v20\n" \
"v_bfe_u32 v216, v20, 6, 2\n" \
M_"a[16:19], a[224:227], a[196:199], a[16:19]\n" \
"v_lshrrev_b32_e64 v219, 1, v20\n" \
"v_bfe_u32 v215, v215, 1, 1\n" \
M_"a[32:35], a[160:163], a[136:139], a[32:35]\n" \
X_"a[182:183], v37 offset:33792\n" \
"v_cndmask_b32_e64 v47, 0, v214, vcc\n" \
M_"a[32:35], a[224:227], a[200:203], a[32:35]\n" \
"v_or_b32_e64 v214, s7, v213\n" \
"v_lshl_or_b32 v217, s43, 3, v213\n" \
M_"a[36:39], a[164:167], a[136:139], a[36:39]\n" \
"v_lshlrev_b32_e32 v218, 10, v212\n" \
"v_lshlrev_b32_e32 v216, 7, v216\n" \
M_"a[36:39], a[228:231], a[200:203], a[36:39]\n" \
X_"a[244:245], v37 offset:40960\n" \
"v_and_b32_e64 v219, 24, v219\n" \
M_"a[40:43], a[168:171], a[136:139], a[40:43]\n" \
"s_mov_b32 m0, s32\n" \
"v_and_or_b32 v213, v213, 2, v215\n" \
M_"a[40:43], a[232:235], a[200:203], a[40:43]\n" \
"s_mov_b32 exec_lo, 0xffffffff\n" \
"s_mov_b32 exec_hi, 0x0\n" \
"global_load_lds_dword v[188:189], off\n" \
"s_mov_b64 exec, -1\n" \
M_"a[44:47], a[172:175], a[136:139], a[44:47]\n" \
X_"a[246:247], v37 offset:41984\n" \
"v_add_u32_e64 v217, s40, v217\n" \
M_"a[44:47], a[236:239], a[200:203], a[44:47]\n" \
"v_or3_b32 v216, v216, v218, v219\n" \
"v_lshlrev_b32_e64 v213, 5, v213\n" \
M_"a[60:63], a[172:175], a[140:143], a[60:63]\n" \
"s_mov_b32 s40, 0x14000\n" \
"s_lshl_b32 s7, s2, 2\n" \
M_"a[60:63], a[236:239], a[204:207], a[60:63]\n" \
"v_or3_b32 v48, v216, v213, s40\n" \
"s_lshl_b32 s40, s43, 2\n" \
M_"a[56:59], a[168:171], a[140:143], a[56:59]\n" \
X_"a[184:185], v38 offset:32768\n" \
"s_lshl_b32 s6, s6, 3\n" \
M_"a[56:59], a[232:235], a[204:207], a[56:59]\n" \
"v_or_b32_e32 v213, s40, v212\n" \
"s_or_b32 s6, s6, s40\n" \
M_"a[52:55], a[164:167], a[140:143], a[52:55]\n" \
"s_and_b32 s40, s7, 0x7c\n" \
"s_or_b32 s40, s40, s3\n" \
M_"a[52:55], a[228:231], a[204:207], a[52:55]\n" \
X_"a[186:187], v38 offset:33792\n" \
M_"a[48:51], a[160:163], a[140:143], a[48:51]\n" \
"s_cmpk_lt_u32 s40, 64\n" \
"s_cselect_b32 s41, s4, s5\n" \
M_"a[48:51], a[224:227], a[204:207], a[48:51]\n" \
"s_lshl_b32 s40, s40, 1\n" \
"s_lshl_b32 s41, s41, 7\n" \
M_"a[64:67], a[160:163], a[144:147], a[64:67]\n" \
X_"a[248:249], v38 offset:40960\n" \
"s_and_b32 s40, s40, 0x7e\n" \
M_"a[64:67], a[224:227], a[208:211], a[64:67]\n" \
"s_or_b32 s40, s41, s40\n" \
"s_mulk_i32 s40, 0x480\n" \
M_"a[68:71], a[164:167], a[144:147], a[68:71]\n" \
"v_add_u32_e64 v49, s40, v20\n" \
"s_mov_b32 s40, 0xc000\n" \
M_"a[68:71], a[228:231], a[208:211], a[68:71]\n" \
X_"a[250:251], v38 offset:41984\n" \
"s_and_b32 s7, s7, 28\n" \
M_"a[72:75], a[168:171], a[144:147], a[72:75]\n" \
"s_bfe_u32 s45, s2, 0x20003\n" \
"s_mov_b32 s41, 0x8000\n" \
M_"a[72:75], a[232:235], a[208:211], a[72:75]\n" \
"s_cmp_lg_u32 s4, 0\n" \
"s_cselect_b32 s51, s40, s41\n" \
M_"a[76:79], a[172:175], a[144:147], a[76:79]\n" \
"s_movk_i32 s40, 0x4000\n" \
"s_lshr_b32 s44, s3, 1\n" \
M_"a[76:79], a[236:239], a[208:211], a[76:79]\n" \
X_"a[188:189], v39 offset:32768\n" \
"s_or_b32 s46, s7, s45\n" \
M_"a[92:95], a[172:175], a[148:151], a[92:95]\n" \
"s_cmp_lg_u32 s4, 0\n" \
"s_cselect_b32 s53, s41, s40\n" \
M_"a[92:95], a[236:239], a[212:215], a[92:95]\n" \
"s_lshl_b32 s40, s44, 15\n" \
"s_lshl_b32 s41, s43, 14\n" \
M_"a[88:91], a[168:171], a[148:151], a[88:91]\n" \
X_"a[190:191], v39 offset:33792\n" \
"s_lshl_b32 s48, s46, 20\n" \
M_"a[88:91], a[232:235], a[212:215], a[88:91]\n" \
"s_or_b32 s41, s40, s41\n" \
"s_add_i32 s49, s48, 0x1000000\n" \
M_"a[84:87], a[164:167], a[148:151], a[84:87]\n" \
"s_waitcnt vmcnt(17)\n" \
"v_or_b32_e32 v204, v210, v204\n" \
"v_or_b32_e64 v206, v210, v206\n" \
M_"a[84:87], a[228:231], a[212:215], a[84:87]\n" \
"v_mov_b32_e64 v210, 0\n" \
"s_add_u32 s88, s35, 0x1000\n" \
M_"a[80:83], a[160:163], a[148:151], a[80:83]\n" \
X_"a[252:253], v39 offset:40960\n" \
"s_or_b32 s42, s49, s41\n" \
M_"a[80:83], a[224:227], a[212:215], a[80:83]\n" \
"ds_write_b128 v196, v[200:203]\n" \
M_"a[96:99], a[160:163], a[152:155], a[96:99]\n" \
X_"a[254:255], v39 offset:41984\n" \
"s_lshl_b32 s4, s4, 16\n" \
M_"a[96:99], a[224:227], a[216:219], a[96:99]\n" \
"s_or_b32 s41, s42, s4\n" \
"s_lshl_b32 s4, s5, 16\n" \
M_"a[100:103], a[164:167], a[152:155], a[100:103]\n" \
"ds_write_b128 v197, v[204:207]\n" \
M_"a[100:103], a[228:231], a[216:219], a[100:103]\n" \
"s_mul_i32 s5, s6, 0x24800\n" \
"s_lshl_b32 s6, s46, 9\n" \
M_"a[104:107], a[168:171], a[152:155], a[104:107]\n" \
"s_lshl_b32 s7, s7, 6\n" \
"s_add_i32 s6, s5, s6\n" \
M_"a[104:107], a[232:235], a[216:219], a[104:107]\n" \
"ds_write_b128 v198, v[208:211]\n" \
M_"a[108:111], a[172:175], a[152:155], a[108:111]\n" \
"s_or_b32 s5, s5, s7\n" \
"s_lshl_b32 s7, s45, 2\n" \
M_"a[108:111], a[236:239], a[216:219], a[108:111]\n" \
"s_or_b32 s5, s5, s7\n" \
"s_lshl_b32 s7, s3, 12\n" \
"s_waitcnt lgkmcnt(15)\n" \
M_"a[124:127], a[172:175], a[156:159], a[124:127]\n" \
"s_or_b32 s50, s49, s7\n" \
"s_lshl_b32 s7, s3, 11\n" \
M_"a[124:127], a[236:239], a[220:223], a[124:127]\n" \
"v_lshrrev_b32_e64 v215, 4, v20\n" \
"v_bfe_u32 v216, v20, 4, 3\n" \
M_"a[120:123], a[168:171], a[156:159], a[120:123]\n" \
"s_or_b32 s42, s42, s4\n" \
"s_lshl_b32 s4, s43, 11\n" \
M_"a[120:123], a[232:235], a[220:223], a[120:123]\n" \
"s_add_i32 s52, s48, s7\n" \
"s_add_i32 s53, s53, s7\n" \
M_"a[116:119], a[164:167], a[156:159], a[116:119]\n" \
"v_mul_u32_u24_e32 v214, 0x480, v214\n" \
"v_bitop3_b32 v218, v212, v215, 7 bitop3:0x78\n" \
M_"a[116:119], a[228:231], a[220:223], a[116:119]\n" \
"v_bitop3_b32 v212, v212, v216, 4 bitop3:0x36\n" \
"s_lshl_b32 s7, s3, 10\n" \
M_"a[112:115], a[160:163], a[156:159], a[112:115]\n" \
"s_or_b32 s4, s40, s4\n" \
"v_lshl_or_b32 v25, v212, 4, v214\n" \
"s_waitcnt vmcnt(1) lgkmcnt(0)\n" \
M_"a[112:115], a[224:227], a[220:223], a[112:115]\n" \
"s_barrier\n" \
M_"v[52:55], a[176:179], a[128:131], v[52:55]\n" \
"s_add_u32 m0, s30, 0x8000\n" \
"s_movk_i32 s84, 0x280\n" \
M_"v[52:55], a[240:243], a[192:195], v[52:55]\n" \
B_"v22, s[64:67], s84 offen offset:0 lds\n" \
M_"v[56:59], a[180:183], a[128:131], v[56:59]\n" \
"s_movk_i32 s84, 0x1e80\n" \
M_"v[56:59], a[244:247], a[192:195], v[56:59]\n" \
B_"v23, s[64:67], s84 offen offset:1024 lds\n" \
M_"v[60:63], a[184:187], a[128:131], v[60:63]\n" \
X_"a[160:161], v32 offset:0\n" \
"v_mul_u32_u24_e32 v212, 0x480, v217\n" \
M_"v[60:63], a[248:251], a[192:195], v[60:63]\n" \
"s_movk_i32 s84, 0x3a80\n" \
M_"v[64:67], a[188:191], a[128:131], v[64:67]\n" \
B_"v22, s[64:67], s84 offen offset:2048 lds\n" \
M_"v[64:67], a[252:255], a[192:195], v[64:67]\n" \
R_"a[128:131], v28 offset:16384\n" \
"v_bitop3_b32 v213, v215, v213, 7 bitop3:0x6c\n" \
M_"v[80:83], a[188:191], a[132:135], v[80:83]\n" \
R_"a[192:195], v29 offset:16384\n" \
"s_or_b32 s40, s7, 0x14000\n" \
M_"v[80:83], a[252:255], a[196:199], v[80:83]\n" \
"s_movk_i32 s84, 0x5680\n" \
M_"v[76:79], a[184:187], a[132:135], v[76:79]\n" \
B_"v23, s[64:67], s84 offen offset:3072 lds\n" \
M_"v[76:79], a[248:251], a[196:199], v[76:79]\n" \
"s_add_u32 m0, s31, 0x8000\n" \
"s_mov_b32 s84, 0x280000\n" \
M_"v[72:75], a[180:183], a[132:135], v[72:75]\n" \
B_"v26, s[68:71], s84 offen offset:0 lds\n" \
M_"v[72:75], a[244:247], a[196:199], v[72:75]\n" \
X_"a[162:163], v32 offset:1024\n" \
"s_lshl_b32 s43, s44, 13\n" \
M_"v[68:71], a[176:179], a[132:135], v[68:71]\n" \
X_"a[224:225], v32 offset:8192\n" \
"s_mulk_i32 s44, 0xe800\n" \
M_"v[68:71], a[240:243], a[196:199], v[68:71]\n" \
R_"a[132:135], v28 offset:18432\n" \
"s_lshl_b32 s7, s3, 8\n" \
M_"v[84:87], a[176:179], a[136:139], v[84:87]\n" \
R_"a[196:199], v29 offset:18432\n" \
"v_lshl_or_b32 v24, v218, 4, v214\n" \
M_"v[84:87], a[240:243], a[200:203], v[84:87]\n" \
"s_addk_i32 s84, 0x7c00\n" \
M_"v[88:91], a[180:183], a[136:139], v[88:91]\n" \
B_"v26, s[68:71], s84 offen offset:1024 lds\n" \
M_"v[88:91], a[244:247], a[200:203], v[88:91]\n" \
"s_addk_i32 s84, 0x7c00\n" \
M_"v[92:95], a[184:187], a[136:139], v[92:95]\n" \
B_"v27, s[68:71], s84 offen offset:2048 lds\n" \
M_"v[92:95], a[248:251], a[200:203], v[92:95]\n" \
"s_addk_i32 s84, 0x7c00\n" \
M_"v[96:99], a[188:191], a[136:139], v[96:99]\n" \
B_"v27, s[68:71], s84 offen offset:3072 lds\n" \
M_"v[96:99], a[252:255], a[200:203], v[96:99]\n" \
R_"a[136:139], v28 offset:20480\n" \
"v_lshl_or_b32 v21, v213, 4, v212\n" \
M_"v[112:115], a[188:191], a[140:143], v[112:115]\n" \
R_"a[200:203], v29 offset:20480\n" \
"s_add_i32 s44, s44, 0x17000\n" \
M_"v[112:115], a[252:255], a[204:207], v[112:115]\n" \
"s_add_u32 m0, s30, 0x9000\n" \
"s_mov_b32 s84, 0x8280\n" \
M_"v[108:111], a[184:187], a[140:143], v[108:111]\n" \
B_"v22, s[64:67], s84 offen offset:0 lds\n" \
M_"v[108:111], a[248:251], a[204:207], v[108:111]\n" \
"s_addk_i32 s84, 0x1c00\n" \
M_"v[104:107], a[180:183], a[140:143], v[104:107]\n" \
B_"v23, s[64:67], s84 offen offset:1024 lds\n" \
M_"v[104:107], a[244:247], a[204:207], v[104:107]\n" \
"s_addk_i32 s84, 0x1c00\n" \
M_"v[100:103], a[176:179], a[140:143], v[100:103]\n" \
B_"v22, s[64:67], s84 offen offset:2048 lds\n" \
M_"v[100:103], a[240:243], a[204:207], v[100:103]\n" \
R_"a[140:143], v28 offset:22528\n" \
"s_or_b32 s45, s7, 0x27000\n" \
M_"v[116:119], a[176:179], a[144:147], v[116:119]\n" \
R_"a[204:207], v29 offset:22528\n" \
"s_or_b32 s46, s4, 0x10000\n" \
M_"v[116:119], a[240:243], a[208:211], v[116:119]\n" \
"s_addk_i32 s84, 0x1c00\n" \
M_"v[120:123], a[180:183], a[144:147], v[120:123]\n" \
B_"v23, s[64:67], s84 offen offset:3072 lds\n" \
M_"v[120:123], a[244:247], a[208:211], v[120:123]\n" \
"s_add_u32 m0, s31, 0xc000\n" \
"s_mov_b32 s84, 0x280000\n" \
M_"v[124:127], a[184:187], a[144:147], v[124:127]\n" \
B_"v26, s[72:75], s84 offen offset:0 lds\n" \
M_"v[124:127], a[248:251], a[208:211], v[124:127]\n" \
"s_addk_i32 s84, 0x7c00\n" \
M_"v[128:131], a[188:191], a[144:147], v[128:131]\n" \
B_"v26, s[72:75], s84 offen offset:1024 lds\n" \
M_"v[128:131], a[252:255], a[208:211], v[128:131]\n" \
R_"a[144:147], v28 offset:0\n" \
"s_add_i32 s48, s6, 0x3000000\n" \
M_"v[144:147], a[188:191], a[148:151], v[144:147]\n" \
R_"a[208:211], v29 offset:0\n" \
"s_add_i32 s49, s5, 0x3004000\n" \
M_"v[144:147], a[252:255], a[212:215], v[144:147]\n" \
R_"v[200:203], v199 offset:0\n" \
"s_add_i32 s52, s52, 0x1010000\n" \
M_"v[140:143], a[184:187], a[148:151], v[140:143]\n" \
R_"v[204:207], v199 offset:4096\n" \
M_"v[140:143], a[248:251], a[212:215], v[140:143]\n" \
X_"a[226:227], v32 offset:9216\n" \
M_"v[136:139], a[180:183], a[148:151], v[136:139]\n" \
X_"a[164:165], v33 offset:0\n" \
M_"v[136:139], a[244:247], a[212:215], v[136:139]\n" \
R_"v[208:211], v199 offset:8192\n" \
M_"v[132:135], a[176:179], a[148:151], v[132:135]\n" \
X_"a[166:167], v33 offset:1024\n" \
M_"v[132:135], a[240:243], a[212:215], v[132:135]\n" \
R_"a[148:151], v28 offset:2048\n" \
M_"v[148:151], a[176:179], a[152:155], v[148:151]\n" \
R_"a[212:215], v29 offset:2048\n" \
M_"v[148:151], a[240:243], a[216:219], v[148:151]\n" \
X_"a[228:229], v33 offset:8192\n" \
M_"v[152:155], a[180:183], a[152:155], v[152:155]\n" \
X_"a[230:231], v33 offset:9216\n" \
M_"v[152:155], a[244:247], a[216:219], v[152:155]\n" \
X_"a[168:169], v34 offset:0\n" \
M_"v[156:159], a[184:187], a[152:155], v[156:159]\n" \
X_"a[170:171], v34 offset:1024\n" \
M_"v[156:159], a[248:251], a[216:219], v[156:159]\n" \
X_"a[232:233], v34 offset:8192\n" \
M_"v[160:163], a[188:191], a[152:155], v[160:163]\n" \
X_"a[234:235], v34 offset:9216\n" \
M_"v[160:163], a[252:255], a[216:219], v[160:163]\n" \
R_"a[152:155], v28 offset:4096\n" \
M_"v[176:179], a[188:191], a[156:159], v[176:179]\n" \
R_"a[216:219], v29 offset:4096\n" \
M_"v[176:179], a[252:255], a[220:223], v[176:179]\n" \
X_"a[172:173], v35 offset:0\n" \
M_"v[172:175], a[184:187], a[156:159], v[172:175]\n" \
X_"a[174:175], v35 offset:1024\n" \
M_"v[172:175], a[248:251], a[220:223], v[172:175]\n" \
X_"a[236:237], v35 offset:8192\n" \
M_"v[168:171], a[180:183], a[156:159], v[168:171]\n" \
X_"a[238:239], v35 offset:9216\n" \
M_"v[168:171], a[244:247], a[220:223], v[168:171]\n" \
M_"v[164:167], a[176:179], a[156:159], v[164:167]\n" \
M_"v[164:167], a[240:243], a[220:223], v[164:167]\n" \
R_"a[156:159], v28 offset:6144\n" \
R_"a[220:223], v29 offset:6144\n" \
"s_waitcnt lgkmcnt(15)\n" \
M_"a[0:3], a[160:163], a[128:131], a[0:3]\n" \
"s_addk_i32 s84, 0x7c00\n" \
M_"a[0:3], a[224:227], a[192:195], a[0:3]\n" \
B_"v27, s[72:75], s84 offen offset:2048 lds\n" \
M_"a[4:7], a[164:167], a[128:131], a[4:7]\n" \
X_"a[176:177], v36 offset:0\n" \
"s_waitcnt lgkmcnt(13)\n" \
M_"a[4:7], a[228:231], a[192:195], a[4:7]\n" \
"s_addk_i32 s84, 0x7c00\n" \
"s_waitcnt lgkmcnt(11)\n" \
M_"a[8:11], a[168:171], a[128:131], a[8:11]\n" \
B_"v27, s[72:75], s84 offen offset:3072 lds\n" \
"s_waitcnt lgkmcnt(9)\n" \
M_"a[8:11], a[232:235], a[192:195], a[8:11]\n" \
X_"a[178:179], v36 offset:1024\n" \
"s_waitcnt lgkmcnt(6)\n" \
M_"a[12:15], a[172:175], a[128:131], a[12:15]\n" \
X_"a[240:241], v36 offset:8192\n" \
"s_waitcnt lgkmcnt(5)\n" \
M_"a[12:15], a[236:239], a[192:195], a[12:15]\n" \
M_"a[28:31], a[172:175], a[132:135], a[28:31]\n" \
M_"a[28:31], a[236:239], a[196:199], a[28:31]\n" \
X_"a[242:243], v36 offset:9216\n" \
M_"a[24:27], a[168:171], a[132:135], a[24:27]\n" \
M_"a[24:27], a[232:235], a[196:199], a[24:27]\n" \
M_"a[20:23], a[164:167], a[132:135], a[20:23]\n" \
M_"a[20:23], a[228:231], a[196:199], a[20:23]\n" \
X_"a[180:181], v37 offset:0\n" \
".fill (L_pb + 12000 - .) / 4, 4, 0xBF800000\n" \
"L_pp3:\n" \
"s_cbranch_execz L_pret\n" \
M_"a[16:19], a[160:163], a[132:135], a[16:19]\n" \
M_"a[16:19], a[224:227], a[196:199], a[16:19]\n" \
M_"a[32:35], a[160:163], a[136:139], a[32:35]\n" \
X_"a[182:183], v37 offset:1024\n" \
M_"a[32:35], a[224:227], a[200:203], a[32:35]\n" \
M_"a[36:39], a[164:167], a[136:139], a[36:39]\n" \
M_"a[36:39], a[228:231], a[200:203], a[36:39]\n" \
"buffer_store_dwordx4 v[200:203], v20, s[80:83], s35 offen sc1\n" \
M_"a[40:43], a[168:171], a[136:139], a[40:43]\n" \
"s_mov_b32 m0, s32\n" \
X_"a[244:245], v37 offset:8192\n" \
M_"a[40:43], a[232:235], a[200:203], a[40:43]\n" \
"s_mov_b32 exec_lo, 0x0\n" \
"s_mov_b32 exec_hi, 0xffffffff\n" \
"global_load_lds_dword v[188:189], off\n" \
"s_mov_b64 exec, -1\n" \
"s_nop 0\n" \
M_"a[44:47], a[172:175], a[136:139], a[44:47]\n" \
X_"a[246:247], v37 offset:9216\n" \
M_"a[44:47], a[236:239], a[200:203], a[44:47]\n" \
M_"a[60:63], a[172:175], a[140:143], a[60:63]\n" \
M_"a[60:63], a[236:239], a[204:207], a[60:63]\n" \
M_"a[56:59], a[168:171], a[140:143], a[56:59]\n" \
X_"a[184:185], v38 offset:0\n" \
M_"a[56:59], a[232:235], a[204:207], a[56:59]\n" \
M_"a[52:55], a[164:167], a[140:143], a[52:55]\n" \
"buffer_store_dwordx4 v[204:207], v20, s[80:83], s88 offen sc1\n" \
M_"a[52:55], a[228:231], a[204:207], a[52:55]\n" \
X_"a[186:187], v38 offset:1024\n" \
M_"a[48:51], a[160:163], a[140:143], a[48:51]\n" \
M_"a[48:51], a[224:227], a[204:207], a[48:51]\n" \
M_"a[64:67], a[160:163], a[144:147], a[64:67]\n" \
X_"a[248:249], v38 offset:8192\n" \
M_"a[64:67], a[224:227], a[208:211], a[64:67]\n" \
M_"a[68:71], a[164:167], a[144:147], a[68:71]\n" \
M_"a[68:71], a[228:231], a[208:211], a[68:71]\n" \
X_"a[250:251], v38 offset:9216\n" \
M_"a[72:75], a[168:171], a[144:147], a[72:75]\n" \
M_"a[72:75], a[232:235], a[208:211], a[72:75]\n" \
M_"a[76:79], a[172:175], a[144:147], a[76:79]\n" \
"buffer_store_dwordx4 v[208:211], v20, s[80:83], s36 offen sc1\n" \
M_"a[76:79], a[236:239], a[208:211], a[76:79]\n" \
X_"a[188:189], v39 offset:0\n" \
M_"a[92:95], a[172:175], a[148:151], a[92:95]\n" \
M_"a[92:95], a[236:239], a[212:215], a[92:95]\n" \
M_"a[88:91], a[168:171], a[148:151], a[88:91]\n" \
X_"a[190:191], v39 offset:1024\n" \
M_"a[88:91], a[232:235], a[212:215], a[88:91]\n" \
M_"a[84:87], a[164:167], a[148:151], a[84:87]\n" \
M_"a[84:87], a[228:231], a[212:215], a[84:87]\n" \
X_"a[252:253], v39 offset:8192\n" \
M_"a[80:83], a[160:163], a[148:151], a[80:83]\n" \
M_"a[80:83], a[224:227], a[212:215], a[80:83]\n" \
M_"a[96:99], a[160:163], a[152:155], a[96:99]\n" \
X_"a[254:255], v39 offset:9216\n" \
M_"a[96:99], a[224:227], a[216:219], a[96:99]\n" \
M_"a[100:103], a[164:167], a[152:155], a[100:103]\n" \
M_"a[100:103], a[228:231], a[216:219], a[100:103]\n" \
M_"a[104:107], a[168:171], a[152:155], a[104:107]\n" \
M_"a[104:107], a[232:235], a[216:219], a[104:107]\n" \
M_"a[108:111], a[172:175], a[152:155], a[108:111]\n" \
M_"a[108:111], a[236:239], a[216:219], a[108:111]\n" \
"s_waitcnt lgkmcnt(15)\n" \
M_"a[124:127], a[172:175], a[156:159], a[124:127]\n" \
M_"a[124:127], a[236:239], a[220:223], a[124:127]\n" \
M_"a[120:123], a[168:171], a[156:159], a[120:123]\n" \
M_"a[120:123], a[232:235], a[220:223], a[120:123]\n" \
M_"a[116:119], a[164:167], a[156:159], a[116:119]\n" \
M_"a[116:119], a[228:231], a[220:223], a[116:119]\n" \
M_"a[112:115], a[160:163], a[156:159], a[112:115]\n" \
"s_waitcnt vmcnt(4) lgkmcnt(0)\n" \
M_"a[112:115], a[224:227], a[220:223], a[112:115]\n" \
"s_barrier\n" \
M_"v[52:55], a[176:179], a[128:131], v[52:55]\n" \
"s_add_u32 m0, s30, 0x0\n" \
"s_movk_i32 s84, 0x300\n" \
M_"v[52:55], a[240:243], a[192:195], v[52:55]\n" \
B_"v22, s[64:67], s84 offen offset:0 lds\n" \
M_"v[56:59], a[180:183], a[128:131], v[56:59]\n" \
X_"a[160:161], v32 offset:32768\n" \
M_"v[56:59], a[244:247], a[192:195], v[56:59]\n" \
"s_movk_i32 s84, 0x1f00\n" \
M_"v[60:63], a[184:187], a[128:131], v[60:63]\n" \
B_"v23, s[64:67], s84 offen offset:1024 lds\n" \
M_"v[60:63], a[248:251], a[192:195], v[60:63]\n" \
X_"a[162:163], v32 offset:33792\n" \
M_"v[64:67], a[188:191], a[128:131], v[64:67]\n" \
X_"a[224:225], v32 offset:40960\n" \
M_"v[64:67], a[252:255], a[192:195], v[64:67]\n" \
R_"a[128:131], v28 offset:49152\n" \
M_"v[80:83], a[188:191], a[132:135], v[80:83]\n" \
R_"a[192:195], v29 offset:49152\n" \
M_"v[80:83], a[252:255], a[196:199], v[80:83]\n" \
"s_movk_i32 s84, 0x3b00\n" \
M_"v[76:79], a[184:187], a[132:135], v[76:79]\n" \
B_"v22, s[64:67], s84 offen offset:2048 lds\n" \
M_"v[76:79], a[248:251], a[196:199], v[76:79]\n" \
"s_movk_i32 s84, 0x5700\n" \
M_"v[72:75], a[180:183], a[132:135], v[72:75]\n" \
B_"v23, s[64:67], s84 offen offset:3072 lds\n" \
M_"v[72:75], a[244:247], a[196:199], v[72:75]\n" \
"s_add_u32 m0, s31, 0x0\n" \
"s_mov_b32 s84, 0x300000\n" \
M_"v[68:71], a[176:179], a[132:135], v[68:71]\n" \
B_"v26, s[68:71], s84 offen offset:0 lds\n" \
M_"v[68:71], a[240:243], a[196:199], v[68:71]\n" \
R_"a[132:135], v28 offset:51200\n" \
M_"v[84:87], a[176:179], a[136:139], v[84:87]\n" \
R_"a[196:199], v29 offset:51200\n" \
M_"v[84:87], a[240:243], a[200:203], v[84:87]\n" \
"s_addk_i32 s84, 0x7c00\n" \
M_"v[88:91], a[180:183], a[136:139], v[88:91]\n" \
B_"v26, s[68:71], s84 offen offset:1024 lds\n" \
M_"v[88:91], a[244:247], a[200:203], v[88:91]\n" \
X_"a[226:227], v32 offset:41984\n" \
M_"v[92:95], a[184:187], a[136:139], v[92:95]\n" \
"s_addk_i32 s84, 0x7c00\n" \
M_"v[92:95], a[248:251], a[200:203], v[92:95]\n" \
B_"v27, s[68:71], s84 offen offset:2048 lds\n" \
M_"v[96:99], a[188:191], a[136:139], v[96:99]\n" \
X_"a[164:165], v33 offset:32768\n" \
M_"v[96:99], a[252:255], a[200:203], v[96:99]\n" \
R_"a[136:139], v28 offset:53248\n" \
M_"v[112:115], a[188:191], a[140:143], v[112:115]\n" \
R_"a[200:203], v29 offset:53248\n" \
M_"v[112:115], a[252:255], a[204:207], v[112:115]\n" \
"s_addk_i32 s84, 0x7c00\n" \
M_"v[108:111], a[184:187], a[140:143], v[108:111]\n" \
B_"v27, s[68:71], s84 offen offset:3072 lds\n" \
M_"v[108:111], a[248:251], a[204:207], v[108:111]\n" \
"s_add_u32 m0, s30, 0x1000\n" \
"s_mov_b32 s84, 0x8300\n" \
M_"v[104:107], a[180:183], a[140:143], v[104:107]\n" \
B_"v22, s[64:67], s84 offen offset:0 lds\n" \
M_"v[104:107], a[244:247], a[204:207], v[104:107]\n" \
X_"a[166:167], v33 offset:33792\n" \
M_"v[100:103], a[176:179], a[140:143], v[100:103]\n" \
X_"a[228:229], v33 offset:40960\n" \
M_"v[100:103], a[240:243], a[204:207], v[100:103]\n" \
R_"a[140:143], v28 offset:55296\n" \
M_"v[116:119], a[176:179], a[144:147], v[116:119]\n" \
R_"a[204:207], v29 offset:55296\n" \
M_"v[116:119], a[240:243], a[208:211], v[116:119]\n" \
"s_addk_i32 s84, 0x1c00\n" \
M_"v[120:123], a[180:183], a[144:147], v[120:123]\n" \
B_"v23, s[64:67], s84 offen offset:1024 lds\n" \
M_"v[120:123], a[244:247], a[208:211], v[120:123]\n" \
"s_addk_i32 s84, 0x1c00\n" \
M_"v[124:127], a[184:187], a[144:147], v[124:127]\n" \
B_"v22, s[64:67], s84 offen offset:2048 lds\n" \
M_"v[124:127], a[248:251], a[208:211], v[124:127]\n" \
"s_addk_i32 s84, 0x1c00\n" \
"s_nop 0\n" \
M_"v[128:131], a[188:191], a[144:147], v[128:131]\n" \
B_"v23, s[64:67], s84 offen offset:3072 lds\n" \
M_"v[128:131], a[252:255], a[208:211], v[128:131]\n" \
R_"a[144:147], v28 offset:32768\n" \
M_"v[144:147], a[188:191], a[148:151], v[144:147]\n" \
R_"a[208:211], v29 offset:32768\n" \
M_"v[144:147], a[252:255], a[212:215], v[144:147]\n" \
"s_add_u32 m0, s31, 0x4000\n" \
"s_mov_b32 s84, 0x300000\n" \
M_"v[140:143], a[184:187], a[148:151], v[140:143]\n" \
B_"v26, s[72:75], s84 offen offset:0 lds\n" \
M_"v[140:143], a[248:251], a[212:215], v[140:143]\n" \
X_"a[230:231], v33 offset:41984\n" \
M_"v[136:139], a[180:183], a[148:151], v[136:139]\n" \
"s_addk_i32 s84, 0x7c00\n" \
"s_nop 0\n" \
M_"v[136:139], a[244:247], a[212:215], v[136:139]\n" \
B_"v26, s[72:75], s84 offen offset:1024 lds\n" \
M_"v[132:135], a[176:179], a[148:151], v[132:135]\n" \
X_"a[168:169], v34 offset:32768\n" \
M_"v[132:135], a[240:243], a[212:215], v[132:135]\n" \
R_"a[148:151], v28 offset:34816\n" \
M_"v[148:151], a[176:179], a[152:155], v[148:151]\n" \
R_"a[212:215], v29 offset:34816\n" \
M_"v[148:151], a[240:243], a[216:219], v[148:151]\n" \
X_"a[170:171], v34 offset:33792\n" \
M_"v[152:155], a[180:183], a[152:155], v[152:155]\n" \
X_"a[232:233], v34 offset:40960\n" \
M_"v[152:155], a[244:247], a[216:219], v[152:155]\n" \
X_"a[234:235], v34 offset:41984\n" \
M_"v[156:159], a[184:187], a[152:155], v[156:159]\n" \
X_"a[172:173], v35 offset:32768\n" \
M_"v[156:159], a[248:251], a[216:219], v[156:159]\n" \
X_"a[174:175], v35 offset:33792\n" \
M_"v[160:163], a[188:191], a[152:155], v[160:163]\n" \
X_"a[236:237], v35 offset:40960\n" \
M_"v[160:163], a[252:255], a[216:219], v[160:163]\n" \
R_"a[152:155], v28 offset:36864\n" \
M_"v[176:179], a[188:191], a[156:159], v[176:179]\n" \
R_"a[216:219], v29 offset:36864\n" \
M_"v[176:179], a[252:255], a[220:223], v[176:179]\n" \
X_"a[238:239], v35 offset:41984\n" \
M_"v[172:175], a[184:187], a[156:159], v[172:175]\n" \
M_"v[172:175], a[248:251], a[220:223], v[172:175]\n" \
M_"v[168:171], a[180:183], a[156:159], v[168:171]\n" \
M_"v[168:171], a[244:247], a[220:223], v[168:171]\n" \
M_"v[164:167], a[176:179], a[156:159], v[164:167]\n" \
M_"v[164:167], a[240:243], a[220:223], v[164:167]\n" \
R_"a[156:159], v28 offset:38912\n" \
R_"a[220:223], v29 offset:38912\n" \
"s_waitcnt lgkmcnt(15)\n" \
M_"a[0:3], a[160:163], a[128:131], a[0:3]\n" \
"s_addk_i32 s84, 0x7c00\n" \
M_"a[0:3], a[224:227], a[192:195], a[0:3]\n" \
B_"v27, s[72:75], s84 offen offset:2048 lds\n" \
M_"a[4:7], a[164:167], a[128:131], a[4:7]\n" \
X_"a[176:177], v36 offset:32768\n" \
"s_waitcnt lgkmcnt(15)\n" \
M_"a[4:7], a[228:231], a[192:195], a[4:7]\n" \
"s_addk_i32 s84, 0x7c00\n" \
"s_waitcnt lgkmcnt(11)\n" \
M_"a[8:11], a[168:171], a[128:131], a[8:11]\n" \
B_"v27, s[72:75], s84 offen offset:3072 lds\n" \
"s_waitcnt lgkmcnt(9)\n" \
M_"a[8:11], a[232:235], a[192:195], a[8:11]\n" \
X_"a[178:179], v36 offset:33792\n" \
"s_waitcnt lgkmcnt(8)\n" \
M_"a[12:15], a[172:175], a[128:131], a[12:15]\n" \
X_"a[240:241], v36 offset:40960\n" \
"s_waitcnt lgkmcnt(5)\n" \
M_"a[12:15], a[236:239], a[192:195], a[12:15]\n" \
M_"a[28:31], a[172:175], a[132:135], a[28:31]\n" \
M_"a[28:31], a[236:239], a[196:199], a[28:31]\n" \
X_"a[242:243], v36 offset:41984\n" \
M_"a[24:27], a[168:171], a[132:135], a[24:27]\n" \
M_"a[24:27], a[232:235], a[196:199], a[24:27]\n" \
M_"a[20:23], a[164:167], a[132:135], a[20:23]\n" \
M_"a[20:23], a[228:231], a[196:199], a[20:23]\n" \
X_"a[180:181], v37 offset:32768\n" \
M_"a[16:19], a[160:163], a[132:135], a[16:19]\n" \
M_"a[16:19], a[224:227], a[196:199], a[16:19]\n" \
M_"a[32:35], a[160:163], a[136:139], a[32:35]\n" \
X_"a[182:183], v37 offset:33792\n" \
M_"a[32:35], a[224:227], a[200:203], a[32:35]\n" \
M_"a[36:39], a[164:167], a[136:139], a[36:39]\n" \
"v_lshlrev_b32_e32 v45, 3, v20\n" \
"v_add_u32_e32 v45, s31, v45\n" \
M_"a[36:39], a[228:231], a[200:203], a[36:39]\n" \
"s_getpc_b64 s[86:87]\n" \
"s_sub_u32 s86, s86, 0xf800\n" \
"s_subb_u32 s87, s87, 0\n" \
"s_mov_b32 m0, s32\n" \
"s_nop 0\n" \
M_"a[40:43], a[168:171], a[136:139], a[40:43]\n" \
"global_load_lds_dword v45, s[86:87]\n" \
M_"a[40:43], a[232:235], a[200:203], a[40:43]\n" \
X_"a[244:245], v37 offset:40960\n" \
M_"a[44:47], a[172:175], a[136:139], a[44:47]\n" \
X_"a[246:247], v37 offset:41984\n" \
M_"a[44:47], a[236:239], a[200:203], a[44:47]\n" \
M_"a[60:63], a[172:175], a[140:143], a[60:63]\n" \
M_"a[60:63], a[236:239], a[204:207], a[60:63]\n" \
M_"a[56:59], a[168:171], a[140:143], a[56:59]\n" \
X_"a[184:185], v38 offset:32768\n" \
M_"a[56:59], a[232:235], a[204:207], a[56:59]\n" \
M_"a[52:55], a[164:167], a[140:143], a[52:55]\n" \
M_"a[52:55], a[228:231], a[204:207], a[52:55]\n" \
X_"a[186:187], v38 offset:33792\n" \
M_"a[48:51], a[160:163], a[140:143], a[48:51]\n" \
M_"a[48:51], a[224:227], a[204:207], a[48:51]\n" \
M_"a[64:67], a[160:163], a[144:147], a[64:67]\n" \
X_"a[248:249], v38 offset:40960\n" \
M_"a[64:67], a[224:227], a[208:211], a[64:67]\n" \
M_"a[68:71], a[164:167], a[144:147], a[68:71]\n" \
M_"a[68:71], a[228:231], a[208:211], a[68:71]\n" \
X_"a[250:251], v38 offset:41984\n" \
M_"a[72:75], a[168:171], a[144:147], a[72:75]\n" \
M_"a[72:75], a[232:235], a[208:211], a[72:75]\n" \
M_"a[76:79], a[172:175], a[144:147], a[76:79]\n" \
M_"a[76:79], a[236:239], a[208:211], a[76:79]\n" \
X_"a[188:189], v39 offset:32768\n" \
M_"a[92:95], a[172:175], a[148:151], a[92:95]\n" \
M_"a[92:95], a[236:239], a[212:215], a[92:95]\n" \
M_"a[88:91], a[168:171], a[148:151], a[88:91]\n" \
X_"a[190:191], v39 offset:33792\n" \
M_"a[88:91], a[232:235], a[212:215], a[88:91]\n" \
M_"a[84:87], a[164:167], a[148:151], a[84:87]\n" \
M_"a[84:87], a[228:231], a[212:215], a[84:87]\n" \
X_"a[252:253], v39 offset:40960\n" \
M_"a[80:83], a[160:163], a[148:151], a[80:83]\n" \
M_"a[80:83], a[224:227], a[212:215], a[80:83]\n" \
M_"a[96:99], a[160:163], a[152:155], a[96:99]\n" \
X_"a[254:255], v39 offset:41984\n" \
M_"a[96:99], a[224:227], a[216:219], a[96:99]\n" \
M_"a[100:103], a[164:167], a[152:155], a[100:103]\n" \
M_"a[100:103], a[228:231], a[216:219], a[100:103]\n" \
M_"a[104:107], a[168:171], a[152:155], a[104:107]\n" \
M_"a[104:107], a[232:235], a[216:219], a[104:107]\n" \
M_"a[108:111], a[172:175], a[152:155], a[108:111]\n" \
M_"a[108:111], a[236:239], a[216:219], a[108:111]\n" \
"s_waitcnt lgkmcnt(15)\n" \
M_"a[124:127], a[172:175], a[156:159], a[124:127]\n" \
M_"a[124:127], a[236:239], a[220:223], a[124:127]\n" \
M_"a[120:123], a[168:171], a[156:159], a[120:123]\n" \
M_"a[120:123], a[232:235], a[220:223], a[120:123]\n" \
M_"a[116:119], a[164:167], a[156:159], a[116:119]\n" \
M_"a[116:119], a[228:231], a[220:223], a[116:119]\n" \
M_"a[112:115], a[160:163], a[156:159], a[112:115]\n" \
"s_waitcnt vmcnt(1) lgkmcnt(0)\n" \
M_"a[112:115], a[224:227], a[220:223], a[112:115]\n" \
"s_barrier\n" \
"v_mov_b32_e32 v45, s62\n" \
"s_mov_b64 exec, 1\n" \
"buffer_store_dword v45, off, s[80:83], s37 offset:128 sc1\n" \
"s_mov_b64 exec, -1\n" \
M_"v[52:55], a[176:179], a[128:131], v[52:55]\n" \
"s_add_u32 m0, s30, 0x8000\n" \
"s_movk_i32 s84, 0x380\n" \
M_"v[52:55], a[240:243], a[192:195], v[52:55]\n" \
B_"v22, s[64:67], s84 offen offset:0 lds\n" \
M_"v[56:59], a[180:183], a[128:131], v[56:59]\n" \
X_"a[160:161], v32 offset:0\n" \
M_"v[56:59], a[244:247], a[192:195], v[56:59]\n" \
"s_movk_i32 s84, 0x1f80\n" \
M_"v[60:63], a[184:187], a[128:131], v[60:63]\n" \
B_"v23, s[64:67], s84 offen offset:1024 lds\n" \
M_"v[60:63], a[248:251], a[192:195], v[60:63]\n" \
X_"a[162:163], v32 offset:1024\n" \
M_"v[64:67], a[188:191], a[128:131], v[64:67]\n" \
X_"a[224:225], v32 offset:8192\n" \
M_"v[64:67], a[252:255], a[192:195], v[64:67]\n" \
R_"a[128:131], v28 offset:16384\n" \
M_"v[80:83], a[188:191], a[132:135], v[80:83]\n" \
R_"a[192:195], v29 offset:16384\n" \
M_"v[80:83], a[252:255], a[196:199], v[80:83]\n" \
"s_movk_i32 s84, 0x3b80\n" \
M_"v[76:79], a[184:187], a[132:135], v[76:79]\n" \
B_"v22, s[64:67], s84 offen offset:2048 lds\n" \
M_"v[76:79], a[248:251], a[196:199], v[76:79]\n" \
"s_movk_i32 s84, 0x5780\n" \
M_"v[72:75], a[180:183], a[132:135], v[72:75]\n" \
B_"v23, s[64:67], s84 offen offset:3072 lds\n" \
M_"v[72:75], a[244:247], a[196:199], v[72:75]\n" \
"s_add_u32 m0, s31, 0x8000\n" \
"s_mov_b32 s84, 0x380000\n" \
M_"v[68:71], a[176:179], a[132:135], v[68:71]\n" \
B_"v26, s[68:71], s84 offen offset:0 lds\n" \
M_"v[68:71], a[240:243], a[196:199], v[68:71]\n" \
R_"a[132:135], v28 offset:18432\n" \
M_"v[84:87], a[176:179], a[136:139], v[84:87]\n" \
R_"a[196:199], v29 offset:18432\n" \
M_"v[84:87], a[240:243], a[200:203], v[84:87]\n" \
"s_addk_i32 s84, 0x7c00\n" \
M_"v[88:91], a[180:183], a[136:139], v[88:91]\n" \
B_"v26, s[68:71], s84 offen offset:1024 lds\n" \
M_"v[88:91], a[244:247], a[200:203], v[88:91]\n" \
X_"a[226:227], v32 offset:9216\n" \
M_"v[92:95], a[184:187], a[136:139], v[92:95]\n" \
"s_addk_i32 s84, 0x7c00\n" \
M_"v[92:95], a[248:251], a[200:203], v[92:95]\n" \
B_"v27, s[68:71], s84 offen offset:2048 lds\n" \
M_"v[96:99], a[188:191], a[136:139], v[96:99]\n" \
X_"a[164:165], v33 offset:0\n" \
M_"v[96:99], a[252:255], a[200:203], v[96:99]\n" \
R_"a[136:139], v28 offset:20480\n" \
M_"v[112:115], a[188:191], a[140:143], v[112:115]\n" \
R_"a[200:203], v29 offset:20480\n" \
M_"v[112:115], a[252:255], a[204:207], v[112:115]\n" \
"s_addk_i32 s84, 0x7c00\n" \
"s_nop 0\n" \
M_"v[108:111], a[184:187], a[140:143], v[108:111]\n" \
B_"v27, s[68:71], s84 offen offset:3072 lds\n" \
M_"v[108:111], a[248:251], a[204:207], v[108:111]\n" \
"s_add_u32 m0, s30, 0x9000\n" \
"s_mov_b32 s84, 0x8380\n" \
M_"v[104:107], a[180:183], a[140:143], v[104:107]\n" \
B_"v22, s[64:67], s84 offen offset:0 lds\n" \
M_"v[104:107], a[244:247], a[204:207], v[104:107]\n" \
X_"a[166:167], v33 offset:1024\n" \
M_"v[100:103], a[176:179], a[140:143], v[100:103]\n" \
X_"a[228:229], v33 offset:8192\n" \
M_"v[100:103], a[240:243], a[204:207], v[100:103]\n" \
R_"a[140:143], v28 offset:22528\n" \
M_"v[116:119], a[176:179], a[144:147], v[116:119]\n" \
R_"a[204:207], v29 offset:22528\n" \
M_"v[116:119], a[240:243], a[208:211], v[116:119]\n" \
"s_addk_i32 s84, 0x1c00\n" \
M_"v[120:123], a[180:183], a[144:147], v[120:123]\n" \
B_"v23, s[64:67], s84 offen offset:1024 lds\n" \
M_"v[120:123], a[244:247], a[208:211], v[120:123]\n" \
"s_addk_i32 s84, 0x1c00\n" \
M_"v[124:127], a[184:187], a[144:147], v[124:127]\n" \
B_"v22, s[64:67], s84 offen offset:2048 lds\n" \
M_"v[124:127], a[248:251], a[208:211], v[124:127]\n" \
"s_addk_i32 s84, 0x1c00\n" \
"s_nop 0\n" \
M_"v[128:131], a[188:191], a[144:147], v[128:131]\n" \
B_"v23, s[64:67], s84 offen offset:3072 lds\n" \
M_"v[128:131], a[252:255], a[208:211], v[128:131]\n" \
R_"a[144:147], v28 offset:0\n" \
M_"v[144:147], a[188:191], a[148:151], v[144:147]\n" \
R_"a[208:211], v29 offset:0\n" \
M_"v[144:147], a[252:255], a[212:215], v[144:147]\n" \
"s_add_u32 m0, s31, 0xc000\n" \
"s_mov_b32 s84, 0x380000\n" \
M_"v[140:143], a[184:187], a[148:151], v[140:143]\n" \
B_"v26, s[72:75], s84 offen offset:0 lds\n" \
M_"v[140:143], a[248:251], a[212:215], v[140:143]\n" \
X_"a[230:231], v33 offset:9216\n" \
M_"v[136:139], a[180:183], a[148:151], v[136:139]\n" \
"s_addk_i32 s84, 0x7c00\n" \
"s_nop 0\n" \
M_"v[136:139], a[244:247], a[212:215], v[136:139]\n" \
B_"v26, s[72:75], s84 offen offset:1024 lds\n" \
M_"v[132:135], a[176:179], a[148:151], v[132:135]\n" \
X_"a[168:169], v34 offset:0\n" \
M_"v[132:135], a[240:243], a[212:215], v[132:135]\n" \
R_"a[148:151], v28 offset:2048\n" \
M_"v[148:151], a[176:179], a[152:155], v[148:151]\n" \
R_"a[212:215], v29 offset:2048\n" \
M_"v[148:151], a[240:243], a[216:219], v[148:151]\n" \
X_"a[170:171], v34 offset:1024\n" \
M_"v[152:155], a[180:183], a[152:155], v[152:155]\n" \
X_"a[232:233], v34 offset:8192\n" \
M_"v[152:155], a[244:247], a[216:219], v[152:155]\n" \
X_"a[234:235], v34 offset:9216\n" \
M_"v[156:159], a[184:187], a[152:155], v[156:159]\n" \
X_"a[172:173], v35 offset:0\n" \
M_"v[156:159], a[248:251], a[216:219], v[156:159]\n" \
X_"a[174:175], v35 offset:1024\n" \
M_"v[160:163], a[188:191], a[152:155], v[160:163]\n" \
X_"a[236:237], v35 offset:8192\n" \
M_"v[160:163], a[252:255], a[216:219], v[160:163]\n" \
R_"a[152:155], v28 offset:4096\n" \
M_"v[176:179], a[188:191], a[156:159], v[176:179]\n" \
R_"a[216:219], v29 offset:4096\n" \
M_"v[176:179], a[252:255], a[220:223], v[176:179]\n" \
X_"a[238:239], v35 offset:9216\n" \
M_"v[172:175], a[184:187], a[156:159], v[172:175]\n" \
M_"v[172:175], a[248:251], a[220:223], v[172:175]\n" \
M_"v[168:171], a[180:183], a[156:159], v[168:171]\n" \
M_"v[168:171], a[244:247], a[220:223], v[168:171]\n" \
M_"v[164:167], a[176:179], a[156:159], v[164:167]\n" \
M_"v[164:167], a[240:243], a[220:223], v[164:167]\n" \
R_"a[156:159], v28 offset:6144\n" \
R_"a[220:223], v29 offset:6144\n" \
"s_waitcnt lgkmcnt(15)\n" \
M_"a[0:3], a[160:163], a[128:131], a[0:3]\n" \
"s_addk_i32 s84, 0x7c00\n" \
M_"a[0:3], a[224:227], a[192:195], a[0:3]\n" \
B_"v27, s[72:75], s84 offen offset:2048 lds\n" \
M_"a[4:7], a[164:167], a[128:131], a[4:7]\n" \
X_"a[176:177], v36 offset:0\n" \
"s_waitcnt lgkmcnt(15)\n" \
M_"a[4:7], a[228:231], a[192:195], a[4:7]\n" \
"s_addk_i32 s84, 0x7c00\n" \
"s_waitcnt lgkmcnt(11)\n" \
M_"a[8:11], a[168:171], a[128:131], a[8:11]\n" \
B_"v27, s[72:75], s84 offen offset:3072 lds\n" \
"s_waitcnt lgkmcnt(9)\n" \
M_"a[8:11], a[232:235], a[192:195], a[8:11]\n" \
X_"a[178:179], v36 offset:1024\n" \
"s_waitcnt lgkmcnt(8)\n" \
M_"a[12:15], a[172:175], a[128:131], a[12:15]\n" \
X_"a[240:241], v36 offset:8192\n" \
"s_waitcnt lgkmcnt(5)\n" \
M_"a[12:15], a[236:239], a[192:195], a[12:15]\n" \
M_"a[28:31], a[172:175], a[132:135], a[28:31]\n" \
M_"a[28:31], a[236:239], a[196:199], a[28:31]\n" \
X_"a[242:243], v36 offset:9216\n" \
M_"a[24:27], a[168:171], a[132:135], a[24:27]\n" \
M_"a[24:27], a[232:235], a[196:199], a[24:27]\n" \
M_"a[20:23], a[164:167], a[132:135], a[20:23]\n" \
M_"a[20:23], a[228:231], a[196:199], a[20:23]\n" \
X_"a[180:181], v37 offset:0\n" \
M_"a[16:19], a[160:163], a[132:135], a[16:19]\n" \
M_"a[16:19], a[224:227], a[196:199], a[16:19]\n" \
M_"a[32:35], a[160:163], a[136:139], a[32:35]\n" \
"v_bfe_u32 v44, v20, 2, 5\n" \
X_"a[182:183], v37 offset:1024\n" \
M_"a[32:35], a[224:227], a[200:203], a[32:35]\n" \
"buffer_load_dword v44, v44, s[80:83], s38 offen offset:128 sc1\n" \
M_"a[36:39], a[164:167], a[136:139], a[36:39]\n" \
M_"a[36:39], a[228:231], a[200:203], a[36:39]\n" \
X_"a[244:245], v37 offset:8192\n" \
M_"a[40:43], a[168:171], a[136:139], a[40:43]\n" \
M_"a[40:43], a[232:235], a[200:203], a[40:43]\n" \
M_"a[44:47], a[172:175], a[136:139], a[44:47]\n" \
X_"a[246:247], v37 offset:9216\n" \
M_"a[44:47], a[236:239], a[200:203], a[44:47]\n" \
M_"a[60:63], a[172:175], a[140:143], a[60:63]\n" \
"global_store_dwordx4 v49, v[0:3], s[28:29] offset:0 sc1\n" \
M_"a[60:63], a[236:239], a[204:207], a[60:63]\n" \
M_"a[56:59], a[168:171], a[140:143], a[56:59]\n" \
X_"a[184:185], v38 offset:0\n" \
M_"a[56:59], a[232:235], a[204:207], a[56:59]\n" \
M_"a[52:55], a[164:167], a[140:143], a[52:55]\n" \
M_"a[52:55], a[228:231], a[204:207], a[52:55]\n" \
X_"a[186:187], v38 offset:1024\n" \
M_"a[48:51], a[160:163], a[140:143], a[48:51]\n" \
M_"a[48:51], a[224:227], a[204:207], a[48:51]\n" \
M_"a[64:67], a[160:163], a[144:147], a[64:67]\n" \
"global_store_dwordx4 v49, v[0:3], s[28:29] offset:1024 sc1\n" \
M_"a[64:67], a[224:227], a[208:211], a[64:67]\n" \
X_"a[248:249], v38 offset:8192\n" \
M_"a[68:71], a[164:167], a[144:147], a[68:71]\n" \
M_"a[68:71], a[228:231], a[208:211], a[68:71]\n" \
X_"a[250:251], v38 offset:9216\n" \
M_"a[72:75], a[168:171], a[144:147], a[72:75]\n" \
M_"a[72:75], a[232:235], a[208:211], a[72:75]\n" \
M_"a[76:79], a[172:175], a[144:147], a[76:79]\n" \
M_"a[76:79], a[236:239], a[208:211], a[76:79]\n" \
X_"a[188:189], v39 offset:0\n" \
M_"a[92:95], a[172:175], a[148:151], a[92:95]\n" \
"s_mov_b64 exec, 0xffff\n" \
"global_store_dwordx4 v49, v[0:3], s[28:29] offset:2048 sc1\n" \
"s_mov_b64 exec, -1\n" \
"s_nop 0\n" \
M_"a[92:95], a[236:239], a[212:215], a[92:95]\n" \
M_"a[88:91], a[168:171], a[148:151], a[88:91]\n" \
X_"a[190:191], v39 offset:1024\n" \
M_"a[88:91], a[232:235], a[212:215], a[88:91]\n" \
M_"a[84:87], a[164:167], a[148:151], a[84:87]\n" \
M_"a[84:87], a[228:231], a[212:215], a[84:87]\n" \
X_"a[252:253], v39 offset:8192\n" \
M_"a[80:83], a[160:163], a[148:151], a[80:83]\n" \
M_"a[80:83], a[224:227], a[212:215], a[80:83]\n" \
M_"a[96:99], a[160:163], a[152:155], a[96:99]\n" \
X_"a[254:255], v39 offset:9216\n" \
M_"a[96:99], a[224:227], a[216:219], a[96:99]\n" \
M_"a[100:103], a[164:167], a[152:155], a[100:103]\n" \
M_"a[100:103], a[228:231], a[216:219], a[100:103]\n" \
M_"a[104:107], a[168:171], a[152:155], a[104:107]\n" \
M_"a[104:107], a[232:235], a[216:219], a[104:107]\n" \
M_"a[108:111], a[172:175], a[152:155], a[108:111]\n" \
M_"a[108:111], a[236:239], a[216:219], a[108:111]\n" \
"s_waitcnt lgkmcnt(15)\n" \
M_"a[124:127], a[172:175], a[156:159], a[124:127]\n" \
M_"a[124:127], a[236:239], a[220:223], a[124:127]\n" \
M_"a[120:123], a[168:171], a[156:159], a[120:123]\n" \
M_"a[120:123], a[232:235], a[220:223], a[120:123]\n" \
M_"a[116:119], a[164:167], a[156:159], a[116:119]\n" \
M_"a[116:119], a[228:231], a[220:223], a[116:119]\n" \
M_"a[112:115], a[160:163], a[156:159], a[112:115]\n" \
"s_waitcnt vmcnt(3) lgkmcnt(0)\n" \
M_"a[112:115], a[224:227], a[220:223], a[112:115]\n" \
"s_barrier\n" \
"v_cmp_ne_u32_e32 vcc, s62, v44\n" \
"s_nop 4\n" \
"s_cbranch_vccnz L_poll60_%=\n" \
"L_pollr60_%=:\n" \
"s_mov_b32 s68, s12\n" \
"s_and_b32 s69, s13, 0xffff\n" \
"s_mov_b32 s70, 0x480000\n" \
"s_mov_b32 s71, 0x20000\n" \
M_"v[52:55], a[176:179], a[128:131], v[52:55]\n" \
"s_add_u32 m0, s40, 0x0\n" \
".fill (L_pb + 16096 - .) / 4, 4, 0xBF800000\n" \
"L_pp4:\n" \
"s_cbranch_execz L_pret\n" \
"s_add_u32 s85, s39, 0x0\n" \
M_"v[52:55], a[240:243], a[192:195], v[52:55]\n" \
B_"v20, s[80:83], s85 offen sc1 lds\n" \
M_"v[56:59], a[180:183], a[128:131], v[56:59]\n" \
X_"a[160:161], v32 offset:32768\n" \
M_"v[56:59], a[244:247], a[192:195], v[56:59]\n" \
"s_add_u32 m0, s40, 0x1000\n" \
"s_add_u32 s85, s39, 0x1000\n" \
M_"v[60:63], a[184:187], a[128:131], v[60:63]\n" \
B_"v20, s[80:83], s85 offen sc1 lds\n" \
M_"v[60:63], a[248:251], a[192:195], v[60:63]\n" \
X_"a[162:163], v32 offset:33792\n" \
M_"v[64:67], a[188:191], a[128:131], v[64:67]\n" \
X_"a[224:225], v32 offset:40960\n" \
M_"v[64:67], a[252:255], a[192:195], v[64:67]\n" \
R_"a[128:131], v28 offset:49152\n" \
M_"v[80:83], a[188:191], a[132:135], v[80:83]\n" \
R_"a[192:195], v29 offset:49152\n" \
M_"v[80:83], a[252:255], a[196:199], v[80:83]\n" \
"s_add_u32 m0, s40, 0x3000\n" \
"s_movk_i32 s84, 0x0\n" \
M_"v[76:79], a[184:187], a[132:135], v[76:79]\n" \
B_"v21, s[76:79], s84 offen offset:0 lds\n" \
M_"v[76:79], a[248:251], a[196:199], v[76:79]\n" \
"s_add_u32 m0, s30, 0x0\n" \
"s_movk_i32 s84, 0x0\n" \
"s_nop 0\n" \
M_"v[72:75], a[180:183], a[132:135], v[72:75]\n" \
B_"v24, s[76:79], s84 offen offset:0 lds\n" \
M_"v[72:75], a[244:247], a[196:199], v[72:75]\n" \
X_"a[226:227], v32 offset:41984\n" \
M_"v[68:71], a[176:179], a[132:135], v[68:71]\n" \
X_"a[164:165], v33 offset:32768\n" \
M_"v[68:71], a[240:243], a[196:199], v[68:71]\n" \
R_"a[132:135], v28 offset:51200\n" \
M_"v[84:87], a[176:179], a[136:139], v[84:87]\n" \
R_"a[196:199], v29 offset:51200\n" \
M_"v[84:87], a[240:243], a[200:203], v[84:87]\n" \
"s_movk_i32 s84, 0x2000\n" \
M_"v[88:91], a[180:183], a[136:139], v[88:91]\n" \
B_"v25, s[76:79], s84 offen offset:1024 lds\n" \
M_"v[88:91], a[244:247], a[200:203], v[88:91]\n" \
"s_movk_i32 s84, 0x4000\n" \
M_"v[92:95], a[184:187], a[136:139], v[92:95]\n" \
B_"v24, s[76:79], s84 offen offset:2048 lds\n" \
M_"v[92:95], a[248:251], a[200:203], v[92:95]\n" \
X_"a[166:167], v33 offset:33792\n" \
M_"v[96:99], a[188:191], a[136:139], v[96:99]\n" \
X_"a[228:229], v33 offset:40960\n" \
M_"v[96:99], a[252:255], a[200:203], v[96:99]\n" \
R_"a[136:139], v28 offset:53248\n" \
M_"v[112:115], a[188:191], a[140:143], v[112:115]\n" \
R_"a[200:203], v29 offset:53248\n" \
M_"v[112:115], a[252:255], a[204:207], v[112:115]\n" \
"s_movk_i32 s84, 0x6000\n" \
M_"v[108:111], a[184:187], a[140:143], v[108:111]\n" \
B_"v25, s[76:79], s84 offen offset:3072 lds\n" \
M_"v[108:111], a[248:251], a[204:207], v[108:111]\n" \
"s_add_u32 m0, s31, 0x0\n" \
"s_movk_i32 s84, 0x0\n" \
M_"v[104:107], a[180:183], a[140:143], v[104:107]\n" \
B_"v26, s[68:71], s84 offen offset:0 lds\n" \
M_"v[104:107], a[244:247], a[204:207], v[104:107]\n" \
"s_movk_i32 s84, 0x7c00\n" \
M_"v[100:103], a[176:179], a[140:143], v[100:103]\n" \
B_"v26, s[68:71], s84 offen offset:1024 lds\n" \
M_"v[100:103], a[240:243], a[204:207], v[100:103]\n" \
R_"a[140:143], v28 offset:55296\n" \
M_"v[116:119], a[176:179], a[144:147], v[116:119]\n" \
R_"a[204:207], v29 offset:55296\n" \
M_"v[116:119], a[240:243], a[208:211], v[116:119]\n" \
"s_addk_i32 s84, 0x7c00\n" \
M_"v[120:123], a[180:183], a[144:147], v[120:123]\n" \
B_"v27, s[68:71], s84 offen offset:2048 lds\n" \
M_"v[120:123], a[244:247], a[208:211], v[120:123]\n" \
X_"a[230:231], v33 offset:41984\n" \
M_"v[124:127], a[184:187], a[144:147], v[124:127]\n" \
"s_addk_i32 s84, 0x7c00\n" \
M_"v[124:127], a[248:251], a[208:211], v[124:127]\n" \
B_"v27, s[68:71], s84 offen offset:3072 lds\n" \
M_"v[128:131], a[188:191], a[144:147], v[128:131]\n" \
X_"a[168:169], v34 offset:32768\n" \
M_"v[128:131], a[252:255], a[208:211], v[128:131]\n" \
R_"a[144:147], v28 offset:32768\n" \
M_"v[144:147], a[188:191], a[148:151], v[144:147]\n" \
R_"a[208:211], v29 offset:32768\n" \
M_"v[144:147], a[252:255], a[212:215], v[144:147]\n" \
"s_add_u32 m0, s30, 0x1000\n" \
"s_mov_b32 s84, 0x9000\n" \
M_"v[140:143], a[184:187], a[148:151], v[140:143]\n" \
B_"v24, s[76:79], s84 offen offset:0 lds\n" \
M_"v[140:143], a[248:251], a[212:215], v[140:143]\n" \
X_"a[170:171], v34 offset:33792\n" \
M_"v[136:139], a[180:183], a[148:151], v[136:139]\n" \
"s_addk_i32 s84, 0x2000\n" \
"s_nop 0\n" \
M_"v[136:139], a[244:247], a[212:215], v[136:139]\n" \
B_"v25, s[76:79], s84 offen offset:1024 lds\n" \
M_"v[132:135], a[176:179], a[148:151], v[132:135]\n" \
X_"a[232:233], v34 offset:40960\n" \
M_"v[132:135], a[240:243], a[212:215], v[132:135]\n" \
R_"a[148:151], v28 offset:34816\n" \
M_"v[148:151], a[176:179], a[152:155], v[148:151]\n" \
R_"a[212:215], v29 offset:34816\n" \
M_"v[148:151], a[240:243], a[216:219], v[148:151]\n" \
X_"a[234:235], v34 offset:41984\n" \
M_"v[152:155], a[180:183], a[152:155], v[152:155]\n" \
X_"a[172:173], v35 offset:32768\n" \
M_"v[152:155], a[244:247], a[216:219], v[152:155]\n" \
X_"a[174:175], v35 offset:33792\n" \
M_"v[156:159], a[184:187], a[152:155], v[156:159]\n" \
X_"a[236:237], v35 offset:40960\n" \
M_"v[156:159], a[248:251], a[216:219], v[156:159]\n" \
X_"a[238:239], v35 offset:41984\n" \
M_"v[160:163], a[188:191], a[152:155], v[160:163]\n" \
M_"v[160:163], a[252:255], a[216:219], v[160:163]\n" \
R_"a[152:155], v28 offset:36864\n" \
M_"v[176:179], a[188:191], a[156:159], v[176:179]\n" \
R_"a[216:219], v29 offset:36864\n" \
M_"v[176:179], a[252:255], a[220:223], v[176:179]\n" \
M_"v[172:175], a[184:187], a[156:159], v[172:175]\n" \
M_"v[172:175], a[248:251], a[220:223], v[172:175]\n" \
M_"v[168:171], a[180:183], a[156:159], v[168:171]\n" \
M_"v[168:171], a[244:247], a[220:223], v[168:171]\n" \
M_"v[164:167], a[176:179], a[156:159], v[164:167]\n" \
M_"v[164:167], a[240:243], a[220:223], v[164:167]\n" \
R_"a[156:159], v28 offset:38912\n" \
R_"a[220:223], v29 offset:38912\n" \
"s_waitcnt lgkmcnt(15)\n" \
M_"a[0:3], a[160:163], a[128:131], a[0:3]\n" \
"s_addk_i32 s84, 0x2000\n" \
M_"a[0:3], a[224:227], a[192:195], a[0:3]\n" \
B_"v24, s[76:79], s84 offen offset:2048 lds\n" \
M_"a[4:7], a[164:167], a[128:131], a[4:7]\n" \
X_"a[176:177], v36 offset:32768\n" \
M_"a[4:7], a[228:231], a[192:195], a[4:7]\n" \
"s_addk_i32 s84, 0x2000\n" \
"s_waitcnt lgkmcnt(13)\n" \
M_"a[8:11], a[168:171], a[128:131], a[8:11]\n" \
B_"v25, s[76:79], s84 offen offset:3072 lds\n" \
"s_waitcnt lgkmcnt(9)\n" \
M_"a[8:11], a[232:235], a[192:195], a[8:11]\n" \
X_"a[178:179], v36 offset:33792\n" \
"s_waitcnt lgkmcnt(8)\n" \
M_"a[12:15], a[172:175], a[128:131], a[12:15]\n" \
X_"a[240:241], v36 offset:40960\n" \
"v_accvgpr_read_b32 v184, a0\n" \
"s_waitcnt lgkmcnt(7)\n" \
M_"a[12:15], a[236:239], a[192:195], a[12:15]\n" \
"v_accvgpr_read_b32 v185, a1\n" \
"v_accvgpr_read_b32 v186, a2\n" \
M_"a[28:31], a[172:175], a[132:135], a[28:31]\n" \
"v_accvgpr_read_b32 v187, a3\n" \
"v_accvgpr_read_b32 v180, a4\n" \
M_"a[28:31], a[236:239], a[196:199], a[28:31]\n" \
"s_add_u32 m0, s30, 0x20000\n" \
"s_movk_i32 s84, 0x100\n" \
M_"a[24:27], a[168:171], a[132:135], a[24:27]\n" \
B_"v24, s[76:79], s84 offen offset:0 lds\n" \
M_"a[24:27], a[232:235], a[196:199], a[24:27]\n" \
X_"a[242:243], v36 offset:41984\n" \
"v_accvgpr_read_b32 v181, a5\n" \
M_"a[20:23], a[164:167], a[132:135], a[20:23]\n" \
"v_accvgpr_read_b32 v182, a6\n" \
"v_accvgpr_read_b32 v183, a7\n" \
M_"a[20:23], a[228:231], a[196:199], a[20:23]\n" \
X_"a[180:181], v37 offset:32768\n" \
C_"v184, v184, v185\n" \
M_"a[16:19], a[160:163], a[132:135], a[16:19]\n" \
"s_movk_i32 s84, 0x2100\n" \
"s_nop 0\n" \
M_"a[16:19], a[224:227], a[196:199], a[16:19]\n" \
B_"v25, s[76:79], s84 offen offset:1024 lds\n" \
M_"a[32:35], a[160:163], a[136:139], a[32:35]\n" \
X_"a[182:183], v37 offset:33792\n" \
C_"v185, v186, v187\n" \
M_"a[32:35], a[224:227], a[200:203], a[32:35]\n" \
C_"v186, v180, v181\n" \
C_"v187, v182, v183\n" \
M_"a[36:39], a[164:167], a[136:139], a[36:39]\n" \
"v_accvgpr_read_b32 v188, a8\n" \
"v_accvgpr_read_b32 v189, a9\n" \
M_"a[36:39], a[228:231], a[200:203], a[36:39]\n" \
"s_movk_i32 s84, 0x4100\n" \
"s_nop 0\n" \
M_"a[40:43], a[168:171], a[136:139], a[40:43]\n" \
B_"v24, s[76:79], s84 offen offset:2048 lds\n" \
M_"a[40:43], a[232:235], a[200:203], a[40:43]\n" \
X_"a[244:245], v37 offset:40960\n" \
"v_accvgpr_read_b32 v190, a10\n" \
M_"a[44:47], a[172:175], a[136:139], a[44:47]\n" \
X_"a[246:247], v37 offset:41984\n" \
"v_accvgpr_read_b32 v191, a11\n" \
M_"a[44:47], a[236:239], a[200:203], a[44:47]\n" \
"v_accvgpr_read_b32 v180, a12\n" \
"v_accvgpr_read_b32 v181, a13\n" \
M_"a[60:63], a[172:175], a[140:143], a[60:63]\n" \
"s_movk_i32 s84, 0x6100\n" \
M_"a[60:63], a[236:239], a[204:207], a[60:63]\n" \
B_"v25, s[76:79], s84 offen offset:3072 lds\n" \
M_"a[56:59], a[168:171], a[140:143], a[56:59]\n" \
X_"a[184:185], v38 offset:32768\n" \
"v_accvgpr_read_b32 v182, a14\n" \
M_"a[56:59], a[232:235], a[204:207], a[56:59]\n" \
"v_accvgpr_read_b32 v183, a15\n" \
C_"v188, v188, v189\n" \
M_"a[52:55], a[164:167], a[140:143], a[52:55]\n" \
"s_add_u32 m0, s30, 0x21000\n" \
"s_addk_i32 s84, 0x3000\n" \
M_"a[52:55], a[228:231], a[204:207], a[52:55]\n" \
B_"v24, s[76:79], s84 offen offset:0 lds\n" \
M_"a[48:51], a[160:163], a[140:143], a[48:51]\n" \
X_"a[186:187], v38 offset:33792\n" \
C_"v189, v190, v191\n" \
M_"a[48:51], a[224:227], a[204:207], a[48:51]\n" \
C_"v190, v180, v181\n" \
C_"v191, v182, v183\n" \
M_"a[64:67], a[160:163], a[144:147], a[64:67]\n" \
X_"a[248:249], v38 offset:40960\n" \
"v_accvgpr_read_b32 v192, a24\n" \
M_"a[64:67], a[224:227], a[208:211], a[64:67]\n" \
"s_addk_i32 s84, 0x2000\n" \
"s_nop 0\n" \
M_"a[68:71], a[164:167], a[144:147], a[68:71]\n" \
B_"v25, s[76:79], s84 offen offset:1024 lds\n" \
M_"a[68:71], a[228:231], a[208:211], a[68:71]\n" \
X_"a[250:251], v38 offset:41984\n" \
"v_accvgpr_read_b32 v193, a25\n" \
M_"a[72:75], a[168:171], a[144:147], a[72:75]\n" \
"v_accvgpr_read_b32 v194, a26\n" \
"v_accvgpr_read_b32 v195, a27\n" \
M_"a[72:75], a[232:235], a[208:211], a[72:75]\n" \
"v_accvgpr_read_b32 v180, a28\n" \
"v_accvgpr_read_b32 v181, a29\n" \
M_"a[76:79], a[172:175], a[144:147], a[76:79]\n" \
"s_addk_i32 s84, 0x2000\n" \
M_"a[76:79], a[236:239], a[208:211], a[76:79]\n" \
B_"v24, s[76:79], s84 offen offset:2048 lds\n" \
M_"a[92:95], a[172:175], a[148:151], a[92:95]\n" \
"s_add_u32 s89, s41, 0x0\n" \
"buffer_store_dwordx4 v[184:187], v20, s[80:83], s89 offen offset:0 sc1\n" \
M_"a[92:95], a[236:239], a[212:215], a[92:95]\n" \
X_"a[188:189], v39 offset:32768\n" \
"v_accvgpr_read_b32 v182, a30\n" \
M_"a[88:91], a[168:171], a[148:151], a[88:91]\n" \
X_"a[190:191], v39 offset:33792\n" \
"v_accvgpr_read_b32 v183, a31\n" \
M_"a[88:91], a[232:235], a[212:215], a[88:91]\n" \
"s_addk_i32 s84, 0x2000\n" \
M_"a[84:87], a[164:167], a[148:151], a[84:87]\n" \
B_"v25, s[76:79], s84 offen offset:3072 lds\n" \
M_"a[84:87], a[228:231], a[212:215], a[84:87]\n" \
X_"a[252:253], v39 offset:40960\n" \
C_"v192, v192, v193\n" \
M_"a[80:83], a[160:163], a[148:151], a[80:83]\n" \
"s_add_u32 s88, s41, 0x0\n" \
"buffer_store_dwordx4 v[188:191], v20, s[80:83], s88 offen offset:1024 sc1\n" \
M_"a[80:83], a[224:227], a[212:215], a[80:83]\n" \
C_"v193, v194, v195\n" \
C_"v194, v180, v181\n" \
"s_waitcnt lgkmcnt(15)\n" \
"s_nop 0\n" \
M_"a[96:99], a[160:163], a[152:155], a[96:99]\n" \
X_"a[254:255], v39 offset:41984\n" \
C_"v195, v182, v183\n" \
M_"a[96:99], a[224:227], a[216:219], a[96:99]\n" \
"v_accvgpr_read_b32 v196, a16\n" \
"v_accvgpr_read_b32 v197, a17\n" \
M_"a[100:103], a[164:167], a[152:155], a[100:103]\n" \
"v_accvgpr_read_b32 v198, a18\n" \
"v_accvgpr_read_b32 v199, a19\n" \
M_"a[100:103], a[228:231], a[216:219], a[100:103]\n" \
"v_accvgpr_read_b32 v180, a20\n" \
"v_accvgpr_read_b32 v181, a21\n" \
M_"a[104:107], a[168:171], a[152:155], a[104:107]\n" \
"s_add_u32 s89, s41, 0x1000\n" \
"buffer_store_dwordx4 v[192:195], v20, s[80:83], s89 offen offset:1024 sc1\n" \
M_"a[104:107], a[232:235], a[216:219], a[104:107]\n" \
"v_accvgpr_read_b32 v182, a22\n" \
"v_accvgpr_read_b32 v183, a23\n" \
M_"a[108:111], a[172:175], a[152:155], a[108:111]\n" \
C_"v196, v196, v197\n" \
C_"v197, v198, v199\n" \
M_"a[108:111], a[236:239], a[216:219], a[108:111]\n" \
C_"v198, v180, v181\n" \
C_"v199, v182, v183\n" \
M_"a[124:127], a[172:175], a[156:159], a[124:127]\n" \
"v_accvgpr_read_b32 v200, a32\n" \
"v_accvgpr_read_b32 v201, a33\n" \
M_"a[124:127], a[236:239], a[220:223], a[124:127]\n" \
"v_accvgpr_read_b32 v202, a34\n" \
"v_accvgpr_read_b32 v203, a35\n" \
M_"a[120:123], a[168:171], a[156:159], a[120:123]\n" \
"s_add_u32 s88, s41, 0x1000\n" \
"buffer_store_dwordx4 v[196:199], v20, s[80:83], s88 offen offset:0 sc1\n" \
M_"a[120:123], a[232:235], a[220:223], a[120:123]\n" \
"v_accvgpr_read_b32 v180, a36\n" \
"v_accvgpr_read_b32 v181, a37\n" \
M_"a[116:119], a[164:167], a[156:159], a[116:119]\n" \
"v_accvgpr_read_b32 v182, a38\n" \
"v_accvgpr_read_b32 v183, a39\n" \
M_"a[116:119], a[228:231], a[220:223], a[116:119]\n" \
C_"v200, v200, v201\n" \
C_"v201, v202, v203\n" \
M_"a[112:115], a[160:163], a[156:159], a[112:115]\n" \
C_"v202, v180, v181\n" \
C_"v203, v182, v183\n" \
M_"a[112:115], a[224:227], a[220:223], a[112:115]\n" \
"v_accvgpr_read_b32 v204, a40\n" \
"v_accvgpr_read_b32 v205, a41\n" \
"s_waitcnt vmcnt(12) lgkmcnt(0)\n" \
"s_barrier\n" \
"v_mov_b32_e32 v36, v32\n" \
"v_xor_b32_e32 v32, 0x0, v48\n" \
"v_mov_b32_e32 v37, v33\n" \
"v_xor_b32_e32 v33, 0x20, v48\n" \
"v_mov_b32_e32 v38, v34\n" \
"v_xor_b32_e32 v34, 0x40, v48\n" \
"v_mov_b32_e64 v39, v35\n" \
"v_xor_b32_e32 v35, 0x60, v48\n" \
"v_add_u32_e32 v22, s44, v28\n" \
"v_add_u32_e32 v28, s43, v28\n" \
"v_add_u32_e32 v23, s44, v29\n" \
"v_add_u32_e32 v29, s43, v29\n" \
M_"v[52:55], a[176:179], a[128:131], v[52:55]\n" \
"s_add_u32 m0, s40, 0x8000\n" \
"s_add_u32 s85, s39, 0x2000\n" \
M_"v[52:55], a[240:243], a[192:195], v[52:55]\n" \
B_"v20, s[80:83], s85 offen sc1 lds\n" \
M_"v[56:59], a[180:183], a[128:131], v[56:59]\n" \
X_"a[160:161], v32 offset:0\n" \
"v_accvgpr_read_b32 v206, a42\n" \
M_"v[56:59], a[244:247], a[192:195], v[56:59]\n" \
"s_add_u32 m0, s40, 0x9000\n" \
"s_add_u32 s85, s39, 0x3000\n" \
M_"v[60:63], a[184:187], a[128:131], v[60:63]\n" \
B_"v20, s[80:83], s85 offen sc1 lds\n" \
M_"v[60:63], a[248:251], a[192:195], v[60:63]\n" \
X_"a[162:163], v32 offset:512\n" \
"v_accvgpr_read_b32 v207, a43\n" \
M_"v[64:67], a[188:191], a[128:131], v[64:67]\n" \
X_"a[224:225], v32 offset:4096\n" \
"v_accvgpr_read_b32 v180, a44\n" \
M_"v[64:67], a[252:255], a[192:195], v[64:67]\n" \
R_"a[128:131], v28 offset:0\n" \
"v_accvgpr_read_b32 v181, a45\n" \
M_"v[80:83], a[188:191], a[132:135], v[80:83]\n" \
R_"a[192:195], v29 offset:0\n" \
"v_accvgpr_read_b32 v182, a46\n" \
M_"v[80:83], a[252:255], a[196:199], v[80:83]\n" \
"s_add_u32 m0, s40, 0xb000\n" \
"s_movk_i32 s84, 0x80\n" \
M_"v[76:79], a[184:187], a[132:135], v[76:79]\n" \
B_"v21, s[76:79], s84 offen offset:0 lds\n" \
M_"v[76:79], a[248:251], a[196:199], v[76:79]\n" \
"s_add_u32 m0, s30, 0x8000\n" \
"s_movk_i32 s84, 0x80\n" \
M_"v[72:75], a[180:183], a[132:135], v[72:75]\n" \
B_"v24, s[76:79], s84 offen offset:0 lds\n" \
M_"v[72:75], a[244:247], a[196:199], v[72:75]\n" \
X_"a[226:227], v32 offset:4608\n" \
"v_accvgpr_read_b32 v183, a47\n" \
M_"v[68:71], a[176:179], a[132:135], v[68:71]\n" \
X_"a[164:165], v33 offset:0\n" \
C_"v204, v204, v205\n" \
M_"v[68:71], a[240:243], a[196:199], v[68:71]\n" \
R_"a[132:135], v28 offset:2048\n" \
C_"v205, v206, v207\n" \
M_"v[84:87], a[176:179], a[136:139], v[84:87]\n" \
R_"a[196:199], v29 offset:2048\n" \
C_"v206, v180, v181\n" \
M_"v[84:87], a[240:243], a[200:203], v[84:87]\n" \
"s_movk_i32 s84, 0x2080\n" \
M_"v[88:91], a[180:183], a[136:139], v[88:91]\n" \
B_"v25, s[76:79], s84 offen offset:1024 lds\n" \
M_"v[88:91], a[244:247], a[200:203], v[88:91]\n" \
"s_movk_i32 s84, 0x4080\n" \
M_"v[92:95], a[184:187], a[136:139], v[92:95]\n" \
B_"v24, s[76:79], s84 offen offset:2048 lds\n" \
M_"v[92:95], a[248:251], a[200:203], v[92:95]\n" \
X_"a[166:167], v33 offset:512\n" \
C_"v207, v182, v183\n" \
M_"v[96:99], a[188:191], a[136:139], v[96:99]\n" \
X_"a[228:229], v33 offset:4096\n" \
"v_accvgpr_read_b32 v208, a56\n" \
M_"v[96:99], a[252:255], a[200:203], v[96:99]\n" \
R_"a[136:139], v28 offset:4096\n" \
"v_accvgpr_read_b32 v209, a57\n" \
M_"v[112:115], a[188:191], a[140:143], v[112:115]\n" \
R_"a[200:203], v29 offset:4096\n" \
"v_accvgpr_read_b32 v210, a58\n" \
M_"v[112:115], a[252:255], a[204:207], v[112:115]\n" \
"s_movk_i32 s84, 0x6080\n" \
M_"v[108:111], a[184:187], a[140:143], v[108:111]\n" \
B_"v25, s[76:79], s84 offen offset:3072 lds\n" \
M_"v[108:111], a[248:251], a[204:207], v[108:111]\n" \
"s_add_u32 m0, s31, 0x8000\n" \
"s_mov_b32 s84, 0x80000\n" \
M_"v[104:107], a[180:183], a[140:143], v[104:107]\n" \
B_"v26, s[68:71], s84 offen offset:0 lds\n" \
M_"v[104:107], a[244:247], a[204:207], v[104:107]\n" \
"s_addk_i32 s84, 0x7c00\n" \
M_"v[100:103], a[176:179], a[140:143], v[100:103]\n" \
B_"v26, s[68:71], s84 offen offset:1024 lds\n" \
M_"v[100:103], a[240:243], a[204:207], v[100:103]\n" \
R_"a[140:143], v28 offset:6144\n" \
"v_accvgpr_read_b32 v211, a59\n" \
M_"v[116:119], a[176:179], a[144:147], v[116:119]\n" \
R_"a[204:207], v29 offset:6144\n" \
"v_accvgpr_read_b32 v180, a60\n" \
M_"v[116:119], a[240:243], a[208:211], v[116:119]\n" \
"s_addk_i32 s84, 0x7c00\n" \
M_"v[120:123], a[180:183], a[144:147], v[120:123]\n" \
B_"v27, s[68:71], s84 offen offset:2048 lds\n" \
M_"v[120:123], a[244:247], a[208:211], v[120:123]\n" \
X_"a[230:231], v33 offset:4608\n" \
"v_accvgpr_read_b32 v181, a61\n" \
M_"v[124:127], a[184:187], a[144:147], v[124:127]\n" \
"s_addk_i32 s84, 0x7c00\n" \
M_"v[124:127], a[248:251], a[208:211], v[124:127]\n" \
B_"v27, s[68:71], s84 offen offset:3072 lds\n" \
M_"v[128:131], a[188:191], a[144:147], v[128:131]\n" \
X_"a[172:173], v35 offset:0\n" \
"v_accvgpr_read_b32 v182, a62\n" \
M_"v[128:131], a[252:255], a[208:211], v[128:131]\n" \
R_"a[144:147], v28 offset:8192\n" \
"v_accvgpr_read_b32 v183, a63\n" \
M_"v[144:147], a[188:191], a[148:151], v[144:147]\n" \
R_"a[208:211], v29 offset:8192\n" \
C_"v208, v208, v209\n" \
M_"v[144:147], a[252:255], a[212:215], v[144:147]\n" \
"s_add_u32 m0, s30, 0x9000\n" \
"s_mov_b32 s84, 0x9080\n" \
M_"v[140:143], a[184:187], a[148:151], v[140:143]\n" \
B_"v24, s[76:79], s84 offen offset:0 lds\n" \
M_"v[140:143], a[248:251], a[212:215], v[140:143]\n" \
X_"a[174:175], v35 offset:512\n" \
C_"v209, v210, v211\n" \
M_"v[136:139], a[180:183], a[148:151], v[136:139]\n" \
"s_addk_i32 s84, 0x2000\n" \
"s_nop 0\n" \
M_"v[136:139], a[244:247], a[212:215], v[136:139]\n" \
B_"v25, s[76:79], s84 offen offset:1024 lds\n" \
M_"v[132:135], a[176:179], a[148:151], v[132:135]\n" \
X_"a[236:237], v35 offset:4096\n" \
C_"v210, v180, v181\n" \
M_"v[132:135], a[240:243], a[212:215], v[132:135]\n" \
R_"a[148:151], v28 offset:10240\n" \
C_"v211, v182, v183\n" \
M_"v[148:151], a[176:179], a[152:155], v[148:151]\n" \
R_"a[212:215], v29 offset:10240\n" \
"v_accvgpr_read_b32 v212, a48\n" \
M_"v[148:151], a[240:243], a[216:219], v[148:151]\n" \
X_"a[238:239], v35 offset:4608\n" \
"v_accvgpr_read_b32 v213, a49\n" \
M_"v[152:155], a[180:183], a[152:155], v[152:155]\n" \
X_"a[168:169], v34 offset:0\n" \
"v_accvgpr_read_b32 v214, a50\n" \
M_"v[152:155], a[244:247], a[216:219], v[152:155]\n" \
X_"a[170:171], v34 offset:512\n" \
"v_accvgpr_read_b32 v215, a51\n" \
M_"v[156:159], a[184:187], a[152:155], v[156:159]\n" \
X_"a[232:233], v34 offset:4096\n" \
"v_accvgpr_read_b32 v180, a52\n" \
M_"v[156:159], a[248:251], a[216:219], v[156:159]\n" \
X_"a[234:235], v34 offset:4608\n" \
"v_accvgpr_read_b32 v181, a53\n" \
M_"v[160:163], a[188:191], a[152:155], v[160:163]\n" \
R_"v[248:251], v22 offset:0\n" \
"v_accvgpr_read_b32 v182, a54\n" \
M_"v[160:163], a[252:255], a[216:219], v[160:163]\n" \
R_"a[152:155], v28 offset:12288\n" \
"v_accvgpr_read_b32 v183, a55\n" \
M_"v[176:179], a[188:191], a[156:159], v[176:179]\n" \
R_"a[216:219], v29 offset:12288\n" \
C_"v212, v212, v213\n" \
M_"v[176:179], a[252:255], a[220:223], v[176:179]\n" \
R_"v[48:51], v23 offset:0\n" \
C_"v213, v214, v215\n" \
M_"v[172:175], a[184:187], a[156:159], v[172:175]\n" \
C_"v214, v180, v181\n" \
C_"v215, v182, v183\n" \
M_"v[172:175], a[248:251], a[220:223], v[172:175]\n" \
"v_accvgpr_read_b32 v216, a64\n" \
"v_accvgpr_read_b32 v217, a65\n" \
M_"v[168:171], a[180:183], a[156:159], v[168:171]\n" \
"v_accvgpr_read_b32 v218, a66\n" \
"v_accvgpr_read_b32 v219, a67\n" \
M_"v[168:171], a[244:247], a[220:223], v[168:171]\n" \
"v_accvgpr_read_b32 v180, a68\n" \
"v_accvgpr_read_b32 v181, a69\n" \
M_"v[164:167], a[176:179], a[156:159], v[164:167]\n" \
"v_accvgpr_read_b32 v182, a70\n" \
"v_accvgpr_read_b32 v183, a71\n" \
M_"v[164:167], a[240:243], a[220:223], v[164:167]\n" \
R_"a[156:159], v28 offset:14336\n" \
R_"a[220:223], v29 offset:14336\n" \
"s_waitcnt lgkmcnt(15)\n" \
M_"a[0:3], a[160:163], a[128:131], 0\n" \
"s_addk_i32 s84, 0x2000\n" \
C_"v216, v216, v217\n" \
M_"a[0:3], a[224:227], a[192:195], a[0:3]\n" \
B_"v24, s[76:79], s84 offen offset:2048 lds\n" \
C_"v217, v218, v219\n" \
M_"a[4:7], a[164:167], a[128:131], 0\n" \
X_"a[176:177], v36 offset:0\n" \
C_"v218, v180, v181\n" \
M_"a[4:7], a[228:231], a[192:195], a[4:7]\n" \
"s_addk_i32 s84, 0x2000\n" \
C_"v219, v182, v183\n" \
"s_waitcnt lgkmcnt(15)\n" \
M_"a[12:15], a[172:175], a[128:131], 0\n" \
B_"v25, s[76:79], s84 offen offset:3072 lds\n" \
"v_accvgpr_read_b32 v220, a80\n" \
"s_waitcnt lgkmcnt(11)\n" \
M_"a[12:15], a[236:239], a[192:195], a[12:15]\n" \
X_"a[178:179], v36 offset:1024\n" \
"v_accvgpr_read_b32 v221, a81\n" \
"s_waitcnt lgkmcnt(10)\n" \
M_"a[8:11], a[128:131], a[168:171], 0\n" \
X_"a[240:241], v36 offset:8192\n" \
"v_accvgpr_read_b32 v222, a82\n" \
"s_waitcnt lgkmcnt(9)\n" \
"s_nop 0\n" \
M_"a[8:11], a[192:195], a[232:235], a[8:11]\n" \
X_"a[242:243], v36 offset:9216\n" \
"v_accvgpr_read_b32 v223, a83\n" \
M_"a[24:27], a[132:135], a[168:171], 0\n" \
X_"a[180:181], v37 offset:0\n" \
"v_accvgpr_read_b32 v180, a84\n" \
M_"a[24:27], a[196:199], a[232:235], a[24:27]\n" \
X_"a[182:183], v37 offset:1024\n" \
"v_accvgpr_read_b32 v181, a85\n" \
M_"a[28:31], a[172:175], a[132:135], 0\n" \
X_"a[244:245], v37 offset:8192\n" \
"v_accvgpr_read_b32 v182, a86\n" \
M_"a[28:31], a[236:239], a[196:199], a[28:31]\n" \
"s_add_u32 s89, s41, 0x2000\n" \
"buffer_store_dwordx4 v[200:203], v20, s[80:83], s89 offen offset:0 sc1\n" \
M_"a[20:23], a[164:167], a[132:135], 0\n" \
X_"a[246:247], v37 offset:9216\n" \
"v_accvgpr_read_b32 v183, a87\n" \
M_"a[20:23], a[228:231], a[196:199], a[20:23]\n" \
X_"a[184:185], v38 offset:0\n" \
C_"v220, v220, v221\n" \
".fill (L_pb + 20192 - .) / 4, 4, 0xBF800000\n" \
"L_pp5:\n" \
"s_cbranch_execz L_pret\n" \
"s_nop 0\n" \
M_"a[16:19], a[160:163], a[132:135], 0\n" \
X_"a[186:187], v38 offset:1024\n" \
C_"v221, v222, v223\n" \
M_"a[16:19], a[224:227], a[196:199], a[16:19]\n" \
C_"v222, v180, v181\n" \
C_"v223, v182, v183\n" \
C_"v224, v52, v53\n" \
M_"a[32:35], a[160:163], a[136:139], 0\n" \
"s_add_u32 s88, s41, 0x2000\n" \
"buffer_store_dwordx4 v[204:207], v20, s[80:83], s88 offen offset:1024 sc1\n" \
M_"a[32:35], a[224:227], a[200:203], a[32:35]\n" \
X_"a[248:249], v38 offset:8192\n" \
C_"v225, v54, v55\n" \
M_"a[36:39], a[164:167], a[136:139], 0\n" \
X_"a[250:251], v38 offset:9216\n" \
C_"v226, v56, v57\n" \
M_"a[36:39], a[228:231], a[200:203], a[36:39]\n" \
X_"a[188:189], v39 offset:0\n" \
C_"v227, v58, v59\n" \
M_"a[44:47], a[172:175], a[136:139], 0\n" \
C_"v228, v60, v61\n" \
C_"v229, v62, v63\n" \
C_"v230, v64, v65\n" \
M_"a[44:47], a[236:239], a[200:203], a[44:47]\n" \
X_"a[190:191], v39 offset:1024\n" \
C_"v231, v66, v67\n" \
M_"a[40:43], a[136:139], a[168:171], 0\n" \
"s_add_u32 s89, s41, 0x3000\n" \
"buffer_store_dwordx4 v[208:211], v20, s[80:83], s89 offen offset:1024 sc1\n" \
M_"a[40:43], a[200:203], a[232:235], a[40:43]\n" \
X_"a[252:253], v39 offset:8192\n" \
C_"v232, v76, v77\n" \
M_"a[56:59], a[140:143], a[168:171], 0\n" \
X_"a[254:255], v39 offset:9216\n" \
C_"v233, v78, v79\n" \
M_"a[56:59], a[204:207], a[232:235], a[56:59]\n" \
C_"v234, v80, v81\n" \
C_"v235, v82, v83\n" \
C_"v236, v68, v69\n" \
M_"a[60:63], a[172:175], a[140:143], 0\n" \
C_"v237, v70, v71\n" \
C_"v238, v72, v73\n" \
C_"v239, v74, v75\n" \
M_"a[60:63], a[236:239], a[204:207], a[60:63]\n" \
"s_add_u32 s88, s41, 0x3000\n" \
"buffer_store_dwordx4 v[212:215], v20, s[80:83], s88 offen offset:0 sc1\n" \
M_"a[52:55], a[164:167], a[140:143], 0\n" \
C_"v240, v84, v85\n" \
C_"v241, v86, v87\n" \
C_"v242, v88, v89\n" \
M_"a[52:55], a[228:231], a[204:207], a[52:55]\n" \
C_"v243, v90, v91\n" \
C_"v244, v92, v93\n" \
C_"v245, v94, v95\n" \
M_"a[48:51], a[160:163], a[140:143], 0\n" \
C_"v246, v96, v97\n" \
C_"v247, v98, v99\n" \
C_"v184, v108, v109\n" \
M_"a[48:51], a[224:227], a[204:207], a[48:51]\n" \
C_"v185, v110, v111\n" \
C_"v186, v112, v113\n" \
C_"v187, v114, v115\n" \
"s_waitcnt lgkmcnt(15)\n" \
"s_nop 0\n" \
M_"a[64:67], a[164:167], v[248:251], 0\n" \
C_"v188, v100, v101\n" \
C_"v189, v102, v103\n" \
C_"v190, v104, v105\n" \
M_"a[64:67], a[228:231], v[48:51], a[64:67]\n" \
C_"v191, v106, v107\n" \
C_"v192, v116, v117\n" \
C_"v193, v118, v119\n" \
M_"a[80:83], a[172:175], v[248:251], 0\n" \
C_"v194, v120, v121\n" \
C_"v195, v122, v123\n" \
C_"v196, v124, v125\n" \
M_"a[80:83], a[236:239], v[48:51], a[80:83]\n" \
C_"v197, v126, v127\n" \
C_"v198, v128, v129\n" \
C_"v199, v130, v131\n" \
M_"a[68:71], v[248:251], a[168:171], 0\n" \
C_"v252, v140, v141\n" \
C_"v253, v142, v143\n" \
C_"v254, v144, v145\n" \
M_"a[68:71], v[48:51], a[232:235], a[68:71]\n" \
C_"v255, v146, v147\n" \
"v_accvgpr_write_b32 a84, v252\n" \
"v_accvgpr_write_b32 a85, v253\n" \
"s_waitcnt vmcnt(4) lgkmcnt(0)\n" \
"s_barrier\n" \
M_"v[52:55], a[176:179], a[128:131], 0\n" \
"s_add_u32 m0, s40, 0x0\n" \
"s_nop 0\n" \
"s_add_u32 s85, s39, 0x4000\n" \
"v_accvgpr_write_b32 a86, v254\n" \
M_"v[52:55], a[240:243], a[192:195], v[52:55]\n" \
B_"v20, s[80:83], s85 offen sc1 lds\n" \
"v_accvgpr_write_b32 a87, v255\n" \
M_"v[56:59], a[180:183], a[128:131], 0\n" \
X_"a[160:161], v32 offset:32768\n" \
C_"v252, v132, v133\n" \
M_"v[56:59], a[244:247], a[192:195], v[56:59]\n" \
X_"a[162:163], v32 offset:33280\n" \
C_"v253, v134, v135\n" \
M_"v[60:63], a[184:187], a[128:131], 0\n" \
"s_add_u32 m0, s40, 0x1000\n" \
"s_add_u32 s85, s39, 0x5000\n" \
C_"v254, v136, v137\n" \
M_"v[60:63], a[248:251], a[192:195], v[60:63]\n" \
B_"v20, s[80:83], s85 offen sc1 lds\n" \
C_"v255, v138, v139\n" \
M_"v[64:67], a[188:191], a[128:131], 0\n" \
X_"a[224:225], v32 offset:36864\n" \
C_"v200, v148, v149\n" \
M_"v[64:67], a[252:255], a[192:195], v[64:67]\n" \
R_"a[128:131], v28 offset:32768\n" \
C_"v201, v150, v151\n" \
M_"v[80:83], a[188:191], a[132:135], 0\n" \
R_"a[192:195], v29 offset:32768\n" \
C_"v202, v152, v153\n" \
M_"v[80:83], a[252:255], a[196:199], v[80:83]\n" \
"s_add_u32 m0, s40, 0x3000\n" \
"s_movk_i32 s84, 0x100\n" \
"s_nop 0\n" \
C_"v203, v154, v155\n" \
M_"v[76:79], a[184:187], a[132:135], 0\n" \
B_"v21, s[76:79], s84 offen offset:0 lds\n" \
C_"v204, v156, v157\n" \
M_"v[76:79], a[248:251], a[196:199], v[76:79]\n" \
X_"a[226:227], v32 offset:37376\n" \
C_"v205, v158, v159\n" \
M_"v[72:75], a[180:183], a[132:135], 0\n" \
X_"a[164:165], v33 offset:32768\n" \
C_"v206, v160, v161\n" \
M_"v[72:75], a[244:247], a[196:199], v[72:75]\n" \
C_"v207, v162, v163\n" \
C_"v208, v172, v173\n" \
C_"v209, v174, v175\n" \
M_"v[68:71], a[176:179], a[132:135], 0\n" \
X_"a[166:167], v33 offset:33280\n" \
C_"v210, v176, v177\n" \
M_"v[68:71], a[240:243], a[196:199], v[68:71]\n" \
R_"a[132:135], v28 offset:34816\n" \
C_"v211, v178, v179\n" \
M_"v[84:87], a[176:179], a[136:139], 0\n" \
R_"a[196:199], v29 offset:34816\n" \
C_"v212, v164, v165\n" \
M_"v[84:87], a[240:243], a[200:203], v[84:87]\n" \
"s_add_u32 m0, s31, 0x0\n" \
"s_mov_b32 s84, 0x100000\n" \
C_"v213, v166, v167\n" \
M_"v[88:91], a[180:183], a[136:139], 0\n" \
B_"v26, s[68:71], s84 offen offset:0 lds\n" \
C_"v214, v168, v169\n" \
M_"v[88:91], a[244:247], a[200:203], v[88:91]\n" \
"s_add_u32 s89, s41, 0x0\n" \
"buffer_store_dwordx4 v[224:227], v20, s[80:83], s89 offen offset:2048 sc1\n" \
M_"v[92:95], a[184:187], a[136:139], 0\n" \
X_"a[228:229], v33 offset:36864\n" \
C_"v215, v170, v171\n" \
M_"v[92:95], a[248:251], a[200:203], v[92:95]\n" \
X_"a[230:231], v33 offset:37376\n" \
M_"v[96:99], a[188:191], a[136:139], 0\n" \
M_"v[96:99], a[252:255], a[200:203], v[96:99]\n" \
R_"a[136:139], v28 offset:36864\n" \
M_"v[112:115], a[188:191], a[140:143], 0\n" \
R_"a[200:203], v29 offset:36864\n" \
M_"v[112:115], a[252:255], a[204:207], v[112:115]\n" \
"s_addk_i32 s84, 0x7c00\n" \
M_"v[108:111], a[184:187], a[140:143], 0\n" \
B_"v26, s[68:71], s84 offen offset:1024 lds\n" \
M_"v[108:111], a[248:251], a[204:207], v[108:111]\n" \
X_"a[172:173], v35 offset:32768\n" \
M_"v[104:107], a[180:183], a[140:143], 0\n" \
"s_addk_i32 s84, 0x7c00\n" \
M_"v[104:107], a[244:247], a[204:207], v[104:107]\n" \
B_"v27, s[68:71], s84 offen offset:2048 lds\n" \
M_"v[100:103], a[176:179], a[140:143], 0\n" \
X_"a[174:175], v35 offset:33280\n" \
M_"v[100:103], a[240:243], a[204:207], v[100:103]\n" \
R_"a[140:143], v28 offset:38912\n" \
M_"v[116:119], a[176:179], a[144:147], 0\n" \
R_"a[204:207], v29 offset:38912\n" \
M_"v[116:119], a[240:243], a[208:211], v[116:119]\n" \
"s_addk_i32 s84, 0x7c00\n" \
M_"v[120:123], a[180:183], a[144:147], 0\n" \
B_"v27, s[68:71], s84 offen offset:3072 lds\n" \
M_"v[120:123], a[244:247], a[208:211], v[120:123]\n" \
X_"a[236:237], v35 offset:36864\n" \
M_"v[124:127], a[184:187], a[144:147], 0\n" \
X_"a[238:239], v35 offset:37376\n" \
M_"v[124:127], a[248:251], a[208:211], v[124:127]\n" \
"s_add_u32 m0, s30, 0x0\n" \
"s_movk_i32 s84, 0x180\n" \
M_"v[128:131], a[188:191], a[144:147], 0\n" \
B_"v24, s[76:79], s84 offen offset:0 lds\n" \
M_"v[128:131], a[252:255], a[208:211], v[128:131]\n" \
R_"a[144:147], v28 offset:40960\n" \
M_"v[144:147], a[188:191], a[148:151], 0\n" \
R_"a[208:211], v29 offset:40960\n" \
M_"v[144:147], a[252:255], a[212:215], v[144:147]\n" \
X_"a[168:169], v34 offset:32768\n" \
M_"v[140:143], a[184:187], a[148:151], 0\n" \
"s_movk_i32 s84, 0x2180\n" \
M_"v[140:143], a[248:251], a[212:215], v[140:143]\n" \
B_"v25, s[76:79], s84 offen offset:1024 lds\n" \
M_"v[136:139], a[180:183], a[148:151], 0\n" \
X_"a[170:171], v34 offset:33280\n" \
M_"v[136:139], a[244:247], a[212:215], v[136:139]\n" \
X_"a[232:233], v34 offset:36864\n" \
M_"v[132:135], a[176:179], a[148:151], 0\n" \
X_"a[234:235], v34 offset:37376\n" \
M_"v[132:135], a[240:243], a[212:215], v[132:135]\n" \
R_"a[148:151], v28 offset:43008\n" \
M_"v[148:151], a[176:179], a[152:155], 0\n" \
R_"a[212:215], v29 offset:43008\n" \
M_"v[148:151], a[240:243], a[216:219], v[148:151]\n" \
"s_movk_i32 s84, 0x4180\n" \
M_"v[152:155], a[180:183], a[152:155], 0\n" \
B_"v24, s[76:79], s84 offen offset:2048 lds\n" \
M_"v[152:155], a[244:247], a[216:219], v[152:155]\n" \
"s_movk_i32 s84, 0x6180\n" \
M_"v[156:159], a[184:187], a[152:155], 0\n" \
B_"v25, s[76:79], s84 offen offset:3072 lds\n" \
M_"v[156:159], a[248:251], a[216:219], v[156:159]\n" \
R_"v[248:251], v22 offset:32768\n" \
M_"v[160:163], a[188:191], a[152:155], 0\n" \
R_"v[48:51], v23 offset:32768\n" \
M_"v[160:163], a[252:255], a[216:219], v[160:163]\n" \
R_"a[152:155], v28 offset:45056\n" \
M_"v[176:179], a[188:191], a[156:159], 0\n" \
R_"a[216:219], v29 offset:45056\n" \
M_"v[176:179], a[252:255], a[220:223], v[176:179]\n" \
M_"v[172:175], a[184:187], a[156:159], 0\n" \
M_"v[172:175], a[248:251], a[220:223], v[172:175]\n" \
M_"v[168:171], a[180:183], a[156:159], 0\n" \
M_"v[168:171], a[244:247], a[220:223], v[168:171]\n" \
M_"v[164:167], a[176:179], a[156:159], 0\n" \
M_"v[164:167], a[240:243], a[220:223], v[164:167]\n" \
R_"a[156:159], v28 offset:47104\n" \
R_"a[220:223], v29 offset:47104\n" \
"s_waitcnt lgkmcnt(15)\n" \
M_"a[0:3], a[160:163], a[128:131], a[0:3]\n" \
"s_add_u32 m0, s30, 0x1000\n" \
"s_addk_i32 s84, 0x3000\n" \
M_"a[0:3], a[224:227], a[192:195], a[0:3]\n" \
B_"v24, s[76:79], s84 offen offset:0 lds\n" \
M_"a[4:7], a[164:167], a[128:131], a[4:7]\n" \
X_"a[176:177], v36 offset:32768\n" \
"v_mov_b32_e64 v11, 0\n" \
M_"a[4:7], a[228:231], a[192:195], a[4:7]\n" \
X_"a[178:179], v36 offset:33792\n" \
"s_and_b32 s5, s2, 7\n" \
M_"a[12:15], a[172:175], a[128:131], a[12:15]\n" \
X_"a[240:241], v36 offset:40960\n" \
"s_lshl_b32 s6, s3, 1\n" \
"s_waitcnt lgkmcnt(15)\n" \
M_"a[12:15], a[236:239], a[192:195], a[12:15]\n" \
"s_add_u32 s88, s41, 0x0\n" \
"buffer_store_dwordx4 v[228:231], v20, s[80:83], s88 offen offset:3072 sc1\n" \
"s_waitcnt lgkmcnt(13)\n" \
M_"a[8:11], a[128:131], a[168:171], a[8:11]\n" \
X_"a[242:243], v36 offset:41984\n" \
"s_lshl_b32 s4, s5, 2\n" \
"s_waitcnt lgkmcnt(12)\n" \
M_"a[8:11], a[192:195], a[232:235], a[8:11]\n" \
X_"a[180:181], v37 offset:32768\n" \
"s_bfe_u32 s7, s2, 0x20003\n" \
M_"a[24:27], a[132:135], a[168:171], a[24:27]\n" \
X_"a[182:183], v37 offset:33792\n" \
"s_or_b32 s12, s4, s7\n" \
M_"a[24:27], a[196:199], a[232:235], a[24:27]\n" \
X_"a[244:245], v37 offset:40960\n" \
"s_and_b32 s6, s6, 4\n" \
M_"a[28:31], a[172:175], a[132:135], a[28:31]\n" \
"s_and_b32 s8, s3, 1\n" \
"s_or_b32 s8, s6, s8\n" \
M_"a[28:31], a[236:239], a[196:199], a[28:31]\n" \
"s_addk_i32 s84, 0x2000\n" \
M_"a[20:23], a[164:167], a[132:135], a[20:23]\n" \
B_"v25, s[76:79], s84 offen offset:1024 lds\n" \
M_"a[20:23], a[228:231], a[196:199], a[20:23]\n" \
X_"a[246:247], v37 offset:41984\n" \
"s_lshl_b32 s6, s12, 4\n" \
M_"a[16:19], a[160:163], a[132:135], a[16:19]\n" \
X_"a[184:185], v38 offset:32768\n" \
"s_lshl_b32 s9, s3, 2\n" \
M_"a[16:19], a[224:227], a[196:199], a[16:19]\n" \
X_"a[186:187], v38 offset:33792\n" \
"s_or_b32 s6, s6, s9\n" \
M_"a[32:35], a[160:163], a[136:139], a[32:35]\n" \
"s_addk_i32 s84, 0x2000\n" \
M_"a[32:35], a[224:227], a[200:203], a[32:35]\n" \
B_"v24, s[76:79], s84 offen offset:2048 lds\n" \
M_"a[36:39], a[164:167], a[136:139], a[36:39]\n" \
X_"a[248:249], v38 offset:40960\n" \
"s_lshr_b32 s9, s2, 5\n" \
M_"a[36:39], a[228:231], a[200:203], a[36:39]\n" \
X_"a[250:251], v38 offset:41984\n" \
"s_mul_i32 s10, s9, 0xa00\n" \
M_"a[44:47], a[172:175], a[136:139], a[44:47]\n" \
X_"a[188:189], v39 offset:32768\n" \
"s_add_i32 s10, s10, 0x5bc1000\n" \
M_"a[44:47], a[236:239], a[200:203], a[44:47]\n" \
X_"a[190:191], v39 offset:33792\n" \
"s_or_b32 s6, s6, s10\n" \
M_"a[40:43], a[136:139], a[168:171], a[40:43]\n" \
"s_add_u32 s89, s41, 0x1000\n" \
"buffer_store_dwordx4 v[232:235], v20, s[80:83], s89 offen offset:3072 sc1\n" \
M_"a[40:43], a[200:203], a[232:235], a[40:43]\n" \
X_"a[252:253], v39 offset:40960\n" \
"s_lshl_b32 s13, s9, 3\n" \
M_"a[56:59], a[140:143], a[168:171], a[56:59]\n" \
X_"a[254:255], v39 offset:41984\n" \
"v_writelane_b32 v11, s6, 0\n" \
M_"a[56:59], a[204:207], a[232:235], a[56:59]\n" \
"s_or_b32 s6, s13, s5\n" \
"v_writelane_b32 v11, s10, 1\n" \
M_"a[60:63], a[172:175], a[140:143], a[60:63]\n" \
"s_lshl_b32 s6, s6, 10\n" \
"s_lshl_b32 s10, s3, 8\n" \
M_"a[60:63], a[236:239], a[204:207], a[60:63]\n" \
"s_addk_i32 s84, 0x2000\n" \
M_"a[52:55], a[164:167], a[140:143], a[52:55]\n" \
B_"v25, s[76:79], s84 offen offset:3072 lds\n" \
M_"a[52:55], a[228:231], a[204:207], a[52:55]\n" \
"s_or_b32 s6, s6, s10\n" \
"s_or_b32 s10, s6, 0x5c00200\n" \
M_"a[48:51], a[160:163], a[140:143], a[48:51]\n" \
"v_writelane_b32 v11, s10, 2\n" \
"s_lshl_b32 s10, s7, 2\n" \
M_"a[48:51], a[224:227], a[204:207], a[48:51]\n" \
"s_or_b32 s11, s10, s6\n" \
"s_or_b32 s11, s11, 0x5c00000\n" \
"s_waitcnt lgkmcnt(15)\n" \
M_"a[64:67], a[164:167], v[248:251], a[64:67]\n" \
"v_writelane_b32 v11, s11, 3\n" \
"s_or_b32 s6, s6, 0x5c00080\n" \
M_"a[64:67], a[228:231], v[48:51], a[64:67]\n" \
"v_writelane_b32 v11, s6, 4\n" \
"s_or_b32 s6, s6, s10\n" \
M_"a[80:83], a[172:175], v[248:251], a[80:83]\n" \
"s_add_i32 s10, s3, s2\n" \
"v_writelane_b32 v11, s6, 5\n" \
M_"a[80:83], a[236:239], v[48:51], a[80:83]\n" \
"s_mov_b32 s6, 2.0\n" \
"s_mov_b32 s11, 0x5bc0000\n" \
M_"a[68:71], v[248:251], a[168:171], a[68:71]\n" \
"s_cmp_lg_u32 s10, 0\n" \
"s_cselect_b32 s10, s6, s11\n" \
"s_waitcnt vmcnt(10) lgkmcnt(0)\n" \
M_"a[68:71], v[48:51], a[232:235], a[68:71]\n" \
"s_barrier\n" \
"v_xor_b32_e32 v28, 0x20000, v28\n" \
"v_xor_b32_e32 v29, 0x20000, v29\n" \
M_"v[52:55], a[176:179], a[128:131], v[52:55]\n" \
"s_add_u32 m0, s40, 0x8000\n" \
"s_add_u32 s85, s39, 0x6000\n" \
M_"v[52:55], a[240:243], a[192:195], v[52:55]\n" \
B_"v20, s[80:83], s85 offen sc1 lds\n" \
M_"v[56:59], a[180:183], a[128:131], v[56:59]\n" \
X_"a[160:161], v32 offset:0\n" \
"s_lshr_b32 s14, s2, 3\n" \
M_"v[56:59], a[244:247], a[192:195], v[56:59]\n" \
X_"a[162:163], v32 offset:512\n" \
"v_writelane_b32 v11, s10, 57\n" \
M_"v[60:63], a[184:187], a[128:131], v[60:63]\n" \
"s_add_u32 m0, s40, 0x9000\n" \
"s_add_u32 s85, s39, 0x7000\n" \
M_"v[60:63], a[248:251], a[192:195], v[60:63]\n" \
B_"v20, s[80:83], s85 offen sc1 lds\n" \
M_"v[64:67], a[188:191], a[128:131], v[64:67]\n" \
X_"a[224:225], v32 offset:4096\n" \
"s_lshl_b32 s10, s5, 5\n" \
M_"v[64:67], a[252:255], a[192:195], v[64:67]\n" \
R_"a[128:131], v28 offset:0\n" \
"s_min_u32 s15, s3, 2\n" \
"s_nop 0\n" \
M_"v[80:83], a[188:191], a[132:135], v[80:83]\n" \
R_"a[192:195], v29 offset:0\n" \
"s_mul_i32 s11, s7, 0x120\n" \
M_"v[80:83], a[252:255], a[196:199], v[80:83]\n" \
"s_add_u32 s88, s41, 0x1000\n" \
"buffer_store_dwordx4 v[236:239], v20, s[80:83], s88 offen offset:2048 sc1\n" \
M_"v[76:79], a[184:187], a[132:135], v[76:79]\n" \
X_"a[226:227], v32 offset:4608\n" \
M_"v[76:79], a[248:251], a[196:199], v[76:79]\n" \
X_"a[164:165], v33 offset:0\n" \
M_"v[72:75], a[180:183], a[132:135], v[72:75]\n" \
"s_add_u32 m0, s40, 0xb000\n" \
"s_movk_i32 s84, 0x180\n" \
"s_nop 0\n" \
M_"v[72:75], a[244:247], a[196:199], v[72:75]\n" \
B_"v21, s[76:79], s84 offen offset:0 lds\n" \
M_"v[68:71], a[176:179], a[132:135], v[68:71]\n" \
X_"a[166:167], v33 offset:512\n" \
M_"v[68:71], a[240:243], a[196:199], v[68:71]\n" \
R_"a[132:135], v28 offset:2048\n" \
M_"v[84:87], a[176:179], a[136:139], v[84:87]\n" \
R_"a[196:199], v29 offset:2048\n" \
M_"v[84:87], a[240:243], a[200:203], v[84:87]\n" \
"s_add_u32 m0, s31, 0x8000\n" \
"s_mov_b32 s84, 0x180000\n" \
M_"v[88:91], a[180:183], a[136:139], v[88:91]\n" \
B_"v26, s[68:71], s84 offen offset:0 lds\n" \
M_"v[88:91], a[244:247], a[200:203], v[88:91]\n" \
X_"a[228:229], v33 offset:4096\n" \
M_"v[92:95], a[184:187], a[136:139], v[92:95]\n" \
X_"a[230:231], v33 offset:4608\n" \
M_"v[92:95], a[248:251], a[200:203], v[92:95]\n" \
"s_addk_i32 s84, 0x7c00\n" \
M_"v[96:99], a[188:191], a[136:139], v[96:99]\n" \
B_"v26, s[68:71], s84 offen offset:1024 lds\n" \
M_"v[96:99], a[252:255], a[200:203], v[96:99]\n" \
R_"a[136:139], v28 offset:4096\n" \
M_"v[112:115], a[188:191], a[140:143], v[112:115]\n" \
R_"a[200:203], v29 offset:4096\n" \
M_"v[112:115], a[252:255], a[204:207], v[112:115]\n" \
"s_addk_i32 s84, 0x7c00\n" \
M_"v[108:111], a[184:187], a[140:143], v[108:111]\n" \
B_"v27, s[68:71], s84 offen offset:2048 lds\n" \
M_"v[108:111], a[248:251], a[204:207], v[108:111]\n" \
X_"a[172:173], v35 offset:0\n" \
M_"v[104:107], a[180:183], a[140:143], v[104:107]\n" \
X_"a[174:175], v35 offset:512\n" \
M_"v[104:107], a[244:247], a[204:207], v[104:107]\n" \
X_"a[236:237], v35 offset:4096\n" \
M_"v[100:103], a[176:179], a[140:143], v[100:103]\n" \
"s_add_u32 s16, s22, s11\n" \
"s_addc_u32 s17, s23, 0\n" \
M_"v[100:103], a[240:243], a[204:207], v[100:103]\n" \
R_"a[140:143], v28 offset:6144\n" \
M_"v[116:119], a[176:179], a[144:147], v[116:119]\n" \
R_"a[204:207], v29 offset:6144\n" \
M_"v[116:119], a[240:243], a[208:211], v[116:119]\n" \
"s_addk_i32 s84, 0x7c00\n" \
M_"v[120:123], a[180:183], a[144:147], v[120:123]\n" \
B_"v27, s[68:71], s84 offen offset:3072 lds\n" \
M_"v[120:123], a[244:247], a[208:211], v[120:123]\n" \
"s_add_u32 m0, s30, 0x8000\n" \
"s_movk_i32 s84, 0x200\n" \
M_"v[124:127], a[184:187], a[144:147], v[124:127]\n" \
B_"v24, s[76:79], s84 offen offset:0 lds\n" \
M_"v[124:127], a[248:251], a[208:211], v[124:127]\n" \
X_"a[238:239], v35 offset:4608\n" \
M_"v[128:131], a[188:191], a[144:147], v[128:131]\n" \
"s_add_u32 s89, s41, 0x2000\n" \
"buffer_store_dwordx4 v[240:243], v20, s[80:83], s89 offen offset:2048 sc1\n" \
M_"v[128:131], a[252:255], a[208:211], v[128:131]\n" \
R_"a[144:147], v28 offset:8192\n" \
M_"v[144:147], a[188:191], a[148:151], v[144:147]\n" \
R_"a[208:211], v29 offset:8192\n" \
M_"v[144:147], a[252:255], a[212:215], v[144:147]\n" \
X_"a[168:169], v34 offset:0\n" \
M_"v[140:143], a[184:187], a[148:151], v[140:143]\n" \
X_"a[170:171], v34 offset:512\n" \
M_"v[140:143], a[248:251], a[212:215], v[140:143]\n" \
"s_movk_i32 s84, 0x2200\n" \
"s_nop 0\n" \
M_"v[136:139], a[180:183], a[148:151], v[136:139]\n" \
B_"v25, s[76:79], s84 offen offset:1024 lds\n" \
M_"v[136:139], a[244:247], a[212:215], v[136:139]\n" \
X_"a[232:233], v34 offset:4096\n" \
M_"v[132:135], a[176:179], a[148:151], v[132:135]\n" \
X_"a[234:235], v34 offset:4608\n" \
M_"v[132:135], a[240:243], a[212:215], v[132:135]\n" \
R_"a[148:151], v28 offset:10240\n" \
M_"v[148:151], a[176:179], a[152:155], v[148:151]\n" \
R_"a[212:215], v29 offset:10240\n" \
M_"v[148:151], a[240:243], a[216:219], v[148:151]\n" \
"s_movk_i32 s84, 0x4200\n" \
M_"v[152:155], a[180:183], a[152:155], v[152:155]\n" \
B_"v24, s[76:79], s84 offen offset:2048 lds\n" \
M_"v[152:155], a[244:247], a[216:219], v[152:155]\n" \
"s_movk_i32 s84, 0x6200\n" \
M_"v[156:159], a[184:187], a[152:155], v[156:159]\n" \
B_"v25, s[76:79], s84 offen offset:3072 lds\n" \
M_"v[156:159], a[248:251], a[216:219], v[156:159]\n" \
R_"v[248:251], v22 offset:0\n" \
M_"v[160:163], a[188:191], a[152:155], v[160:163]\n" \
R_"v[48:51], v23 offset:0\n" \
M_"v[160:163], a[252:255], a[216:219], v[160:163]\n" \
R_"a[152:155], v28 offset:12288\n" \
M_"v[176:179], a[188:191], a[156:159], v[176:179]\n" \
R_"a[216:219], v29 offset:12288\n" \
M_"v[176:179], a[252:255], a[220:223], v[176:179]\n" \
"s_add_u32 s18, s24, s11\n" \
"v_writelane_b32 v11, s16, 9\n" \
"s_addc_u32 s19, s25, 0\n" \
M_"v[172:175], a[184:187], a[156:159], v[172:175]\n" \
"s_and_b32 s20, s17, 0xffff\n" \
"v_writelane_b32 v11, s20, 10\n" \
M_"v[172:175], a[248:251], a[220:223], v[172:175]\n" \
"s_and_b32 s20, s19, 0xffff\n" \
"v_writelane_b32 v11, s18, 11\n" \
M_"v[168:171], a[180:183], a[156:159], v[168:171]\n" \
"s_cmpk_lt_u32 s3, 3\n" \
"s_cselect_b32 s16, s16, s18\n" \
M_"v[168:171], a[244:247], a[220:223], v[168:171]\n" \
"s_movk_i32 s18, 0xfff\n" \
"v_writelane_b32 v11, s20, 12\n" \
M_"v[164:167], a[176:179], a[156:159], v[164:167]\n" \
"s_and_b32 s20, s29, 0xffff\n" \
"v_writelane_b32 v11, s28, 13\n" \
M_"v[164:167], a[240:243], a[220:223], v[164:167]\n" \
R_"a[156:159], v28 offset:14336\n" \
R_"a[220:223], v29 offset:14336\n" \
"s_waitcnt lgkmcnt(15)\n" \
M_"a[0:3], a[160:163], a[128:131], a[0:3]\n" \
"s_add_u32 m0, s30, 0x9000\n" \
"s_addk_i32 s84, 0x3000\n" \
M_"a[0:3], a[224:227], a[192:195], a[0:3]\n" \
B_"v24, s[76:79], s84 offen offset:0 lds\n" \
M_"a[4:7], a[164:167], a[128:131], a[4:7]\n" \
X_"a[176:177], v36 offset:0\n" \
"v_writelane_b32 v11, s20, 14\n" \
M_"a[4:7], a[228:231], a[192:195], a[4:7]\n" \
X_"a[178:179], v36 offset:1024\n" \
"v_writelane_b32 v11, s16, 15\n" \
M_"a[12:15], a[172:175], a[128:131], a[12:15]\n" \
X_"a[240:241], v36 offset:8192\n" \
"s_waitcnt lgkmcnt(15)\n" \
M_"a[12:15], a[236:239], a[192:195], a[12:15]\n" \
"s_add_u32 s88, s41, 0x2000\n" \
"buffer_store_dwordx4 v[244:247], v20, s[80:83], s88 offen offset:3072 sc1\n" \
"s_waitcnt lgkmcnt(13)\n" \
M_"a[8:11], a[128:131], a[168:171], a[8:11]\n" \
X_"a[242:243], v36 offset:9216\n" \
"s_waitcnt lgkmcnt(12)\n" \
M_"a[8:11], a[192:195], a[232:235], a[8:11]\n" \
X_"a[180:181], v37 offset:0\n" \
M_"a[24:27], a[132:135], a[168:171], a[24:27]\n" \
X_"a[182:183], v37 offset:1024\n" \
M_"a[24:27], a[196:199], a[232:235], a[24:27]\n" \
X_"a[244:245], v37 offset:8192\n" \
M_"a[28:31], a[172:175], a[132:135], a[28:31]\n" \
"s_cmpk_lt_u32 s3, 3\n" \
"s_cselect_b32 s16, s17, s19\n" \
M_"a[28:31], a[236:239], a[196:199], a[28:31]\n" \
"s_addk_i32 s84, 0x2000\n" \
"s_nop 0\n" \
M_"a[20:23], a[164:167], a[132:135], a[20:23]\n" \
B_"v25, s[76:79], s84 offen offset:1024 lds\n" \
M_"a[20:23], a[228:231], a[196:199], a[20:23]\n" \
X_"a[246:247], v37 offset:9216\n" \
"s_and_b32 s16, s16, 0xffff\n" \
M_"a[16:19], a[160:163], a[132:135], a[16:19]\n" \
X_"a[184:185], v38 offset:0\n" \
"v_writelane_b32 v11, s16, 16\n" \
M_"a[16:19], a[224:227], a[196:199], a[16:19]\n" \
X_"a[186:187], v38 offset:1024\n" \
"s_mul_i32 s16, s5, 0x48\n" \
M_"a[32:35], a[160:163], a[136:139], a[32:35]\n" \
".fill (L_pb + 24288 - .) / 4, 4, 0xBF800000\n" \
"L_pp6:\n" \
"s_cbranch_execz L_pret\n" \
"s_addk_i32 s84, 0x2000\n" \
M_"a[32:35], a[224:227], a[200:203], a[32:35]\n" \
B_"v24, s[76:79], s84 offen offset:2048 lds\n" \
M_"a[36:39], a[164:167], a[136:139], a[36:39]\n" \
X_"a[248:249], v38 offset:8192\n" \
"s_add_i32 s17, s16, 0x47\n" \
M_"a[36:39], a[228:231], a[200:203], a[36:39]\n" \
X_"a[250:251], v38 offset:9216\n" \
M_"a[44:47], a[172:175], a[136:139], a[44:47]\n" \
X_"a[188:189], v39 offset:0\n" \
M_"a[44:47], a[236:239], a[200:203], a[44:47]\n" \
X_"a[190:191], v39 offset:1024\n" \
M_"a[40:43], a[136:139], a[168:171], a[40:43]\n" \
"s_add_u32 s89, s41, 0x3000\n" \
"buffer_store_dwordx4 v[184:187], v20, s[80:83], s89 offen offset:3072 sc1\n" \
M_"a[40:43], a[200:203], a[232:235], a[40:43]\n" \
X_"a[252:253], v39 offset:8192\n" \
M_"a[56:59], a[140:143], a[168:171], a[56:59]\n" \
X_"a[254:255], v39 offset:9216\n" \
M_"a[56:59], a[204:207], a[232:235], a[56:59]\n" \
"s_cmpk_lt_u32 s3, 3\n" \
"s_cselect_b32 s17, s17, s18\n" \
M_"a[60:63], a[172:175], a[140:143], a[60:63]\n" \
"s_mulk_i32 s17, 0x480\n" \
"s_addk_i32 s17, 0x120\n" \
M_"a[60:63], a[236:239], a[204:207], a[60:63]\n" \
"s_addk_i32 s84, 0x2000\n" \
M_"a[52:55], a[164:167], a[140:143], a[52:55]\n" \
B_"v25, s[76:79], s84 offen offset:3072 lds\n" \
M_"a[52:55], a[228:231], a[204:207], a[52:55]\n" \
"v_writelane_b32 v11, s17, 17\n" \
"s_lshl_b32 s17, s3, 5\n" \
M_"a[48:51], a[160:163], a[140:143], a[48:51]\n" \
"s_add_i32 s16, s16, s17\n" \
"s_mulk_i32 s16, 0x480\n" \
M_"a[48:51], a[224:227], a[204:207], a[48:51]\n" \
"s_mul_i32 s17, s12, 0x24000\n" \
"s_add_i32 s18, s17, 0x1b000\n" \
"s_waitcnt lgkmcnt(15)\n" \
M_"a[64:67], a[164:167], v[248:251], a[64:67]\n" \
"s_cmpk_lt_u32 s3, 3\n" \
"s_cselect_b32 s16, s16, s18\n" \
M_"a[64:67], a[228:231], v[48:51], a[64:67]\n" \
"s_xor_b32 s18, s3, 2\n" \
"v_writelane_b32 v11, s16, 18\n" \
M_"a[80:83], a[172:175], v[248:251], a[80:83]\n" \
"s_mul_i32 s16, s15, 0x2400\n" \
"s_mul_i32 s15, s15, 0x9000\n" \
M_"a[80:83], a[236:239], v[48:51], a[80:83]\n" \
"s_bitset1_b32 s16, 17\n" \
"v_writelane_b32 v11, s16, 22\n" \
M_"a[68:71], v[248:251], a[168:171], a[68:71]\n" \
"s_add_i32 s17, s17, s15\n" \
"s_mul_i32 s15, s3, 0x2400\n" \
"s_waitcnt vmcnt(11) lgkmcnt(0)\n" \
M_"a[68:71], v[48:51], a[232:235], a[68:71]\n" \
"s_barrier\n" \
"v_xor_b32_e32 v28, 0x20000, v28\n" \
"v_xor_b32_e32 v29, 0x20000, v29\n" \
M_"v[52:55], a[176:179], a[128:131], v[52:55]\n" \
"s_add_u32 m0, s40, 0x0\n" \
"s_nop 0\n" \
"s_add_u32 s85, s39, 0x8000\n" \
M_"v[52:55], a[240:243], a[192:195], v[52:55]\n" \
B_"v20, s[80:83], s85 offen sc1 lds\n" \
M_"v[56:59], a[180:183], a[128:131], v[56:59]\n" \
X_"a[160:161], v32 offset:32768\n" \
"v_writelane_b32 v11, s17, 29\n" \
M_"v[56:59], a[244:247], a[192:195], v[56:59]\n" \
X_"a[162:163], v32 offset:33280\n" \
"s_add_i32 s16, s15, 0xa400\n" \
M_"v[60:63], a[184:187], a[128:131], v[60:63]\n" \
"s_add_u32 m0, s40, 0x1000\n" \
"s_add_u32 s85, s39, 0x9000\n" \
M_"v[60:63], a[248:251], a[192:195], v[60:63]\n" \
B_"v20, s[80:83], s85 offen sc1 lds\n" \
M_"v[64:67], a[188:191], a[128:131], v[64:67]\n" \
X_"a[224:225], v32 offset:36864\n" \
"s_mov_b32 s17, 0x8000\n" \
M_"v[64:67], a[252:255], a[192:195], v[64:67]\n" \
R_"a[128:131], v28 offset:0\n" \
M_"v[80:83], a[188:191], a[132:135], v[80:83]\n" \
R_"a[192:195], v29 offset:0\n" \
M_"v[80:83], a[252:255], a[196:199], v[80:83]\n" \
"s_add_u32 s88, s41, 0x3000\n" \
"buffer_store_dwordx4 v[188:191], v20, s[80:83], s88 offen offset:2048 sc1\n" \
M_"v[76:79], a[184:187], a[132:135], v[76:79]\n" \
X_"a[226:227], v32 offset:37376\n" \
M_"v[76:79], a[248:251], a[196:199], v[76:79]\n" \
X_"a[164:165], v33 offset:32768\n" \
M_"v[72:75], a[180:183], a[132:135], v[72:75]\n" \
"s_add_u32 m0, s40, 0x3000\n" \
"s_movk_i32 s84, 0x200\n" \
M_"v[72:75], a[244:247], a[196:199], v[72:75]\n" \
B_"v21, s[76:79], s84 offen offset:0 lds\n" \
M_"v[68:71], a[176:179], a[132:135], v[68:71]\n" \
X_"a[166:167], v33 offset:33280\n" \
M_"v[68:71], a[240:243], a[196:199], v[68:71]\n" \
R_"a[132:135], v28 offset:2048\n" \
M_"v[84:87], a[176:179], a[136:139], v[84:87]\n" \
R_"a[196:199], v29 offset:2048\n" \
M_"v[84:87], a[240:243], a[200:203], v[84:87]\n" \
"s_add_u32 m0, s31, 0x0\n" \
"s_mov_b32 s84, 0x200000\n" \
M_"v[88:91], a[180:183], a[136:139], v[88:91]\n" \
B_"v26, s[68:71], s84 offen offset:0 lds\n" \
M_"v[88:91], a[244:247], a[200:203], v[88:91]\n" \
X_"a[228:229], v33 offset:36864\n" \
M_"v[92:95], a[184:187], a[136:139], v[92:95]\n" \
X_"a[230:231], v33 offset:37376\n" \
M_"v[92:95], a[248:251], a[200:203], v[92:95]\n" \
"s_addk_i32 s84, 0x7c00\n" \
M_"v[96:99], a[188:191], a[136:139], v[96:99]\n" \
B_"v26, s[68:71], s84 offen offset:1024 lds\n" \
M_"v[96:99], a[252:255], a[200:203], v[96:99]\n" \
R_"a[136:139], v28 offset:4096\n" \
M_"v[112:115], a[188:191], a[140:143], v[112:115]\n" \
R_"a[200:203], v29 offset:4096\n" \
M_"v[112:115], a[252:255], a[204:207], v[112:115]\n" \
"s_addk_i32 s84, 0x7c00\n" \
M_"v[108:111], a[184:187], a[140:143], v[108:111]\n" \
B_"v27, s[68:71], s84 offen offset:2048 lds\n" \
M_"v[108:111], a[248:251], a[204:207], v[108:111]\n" \
X_"a[172:173], v35 offset:32768\n" \
M_"v[104:107], a[180:183], a[140:143], v[104:107]\n" \
X_"a[174:175], v35 offset:33280\n" \
M_"v[104:107], a[244:247], a[204:207], v[104:107]\n" \
X_"a[236:237], v35 offset:36864\n" \
M_"v[100:103], a[176:179], a[140:143], v[100:103]\n" \
"s_cmpk_lt_u32 s3, 3\n" \
"s_cselect_b32 s16, s16, s17\n" \
M_"v[100:103], a[240:243], a[204:207], v[100:103]\n" \
R_"a[140:143], v28 offset:6144\n" \
"s_mov_b32 s17, 0x1b000\n" \
M_"v[116:119], a[176:179], a[144:147], v[116:119]\n" \
R_"a[204:207], v29 offset:6144\n" \
M_"v[116:119], a[240:243], a[208:211], v[116:119]\n" \
"s_addk_i32 s84, 0x7c00\n" \
M_"v[120:123], a[180:183], a[144:147], v[120:123]\n" \
B_"v27, s[68:71], s84 offen offset:3072 lds\n" \
M_"v[120:123], a[244:247], a[208:211], v[120:123]\n" \
"s_add_u32 m0, s30, 0x20000\n" \
"s_movk_i32 s84, 0x280\n" \
M_"v[124:127], a[184:187], a[144:147], v[124:127]\n" \
B_"v24, s[76:79], s84 offen offset:0 lds\n" \
M_"v[124:127], a[248:251], a[208:211], v[124:127]\n" \
X_"a[238:239], v35 offset:37376\n" \
M_"v[128:131], a[188:191], a[144:147], v[128:131]\n" \
"s_add_u32 s89, s42, 0x0\n" \
"buffer_store_dwordx4 v[216:219], v20, s[80:83], s89 offen offset:0 sc1\n" \
M_"v[128:131], a[252:255], a[208:211], v[128:131]\n" \
R_"a[144:147], v28 offset:8192\n" \
M_"v[144:147], a[188:191], a[148:151], v[144:147]\n" \
R_"a[208:211], v29 offset:8192\n" \
M_"v[144:147], a[252:255], a[212:215], v[144:147]\n" \
X_"a[168:169], v34 offset:32768\n" \
M_"v[140:143], a[184:187], a[148:151], v[140:143]\n" \
X_"a[170:171], v34 offset:33280\n" \
M_"v[140:143], a[248:251], a[212:215], v[140:143]\n" \
"s_movk_i32 s84, 0x2280\n" \
M_"v[136:139], a[180:183], a[148:151], v[136:139]\n" \
B_"v25, s[76:79], s84 offen offset:1024 lds\n" \
M_"v[136:139], a[244:247], a[212:215], v[136:139]\n" \
X_"a[232:233], v34 offset:36864\n" \
M_"v[132:135], a[176:179], a[148:151], v[132:135]\n" \
X_"a[234:235], v34 offset:37376\n" \
M_"v[132:135], a[240:243], a[212:215], v[132:135]\n" \
R_"a[148:151], v28 offset:10240\n" \
M_"v[148:151], a[176:179], a[152:155], v[148:151]\n" \
R_"a[212:215], v29 offset:10240\n" \
M_"v[148:151], a[240:243], a[216:219], v[148:151]\n" \
"s_movk_i32 s84, 0x4280\n" \
M_"v[152:155], a[180:183], a[152:155], v[152:155]\n" \
B_"v24, s[76:79], s84 offen offset:2048 lds\n" \
M_"v[152:155], a[244:247], a[216:219], v[152:155]\n" \
"s_movk_i32 s84, 0x6280\n" \
M_"v[156:159], a[184:187], a[152:155], v[156:159]\n" \
B_"v25, s[76:79], s84 offen offset:3072 lds\n" \
M_"v[156:159], a[248:251], a[216:219], v[156:159]\n" \
R_"v[248:251], v22 offset:32768\n" \
M_"v[160:163], a[188:191], a[152:155], v[160:163]\n" \
R_"v[48:51], v23 offset:32768\n" \
M_"v[160:163], a[252:255], a[216:219], v[160:163]\n" \
R_"a[152:155], v28 offset:12288\n" \
M_"v[176:179], a[188:191], a[156:159], v[176:179]\n" \
R_"a[216:219], v29 offset:12288\n" \
M_"v[176:179], a[252:255], a[220:223], v[176:179]\n" \
"s_cmp_lg_u32 s18, 0\n" \
"s_cselect_b32 s17, s16, s17\n" \
M_"v[172:175], a[184:187], a[156:159], v[172:175]\n" \
"v_writelane_b32 v11, s16, 23\n" \
"s_or_b32 s16, s15, 0x10000\n" \
M_"v[172:175], a[248:251], a[220:223], v[172:175]\n" \
"v_writelane_b32 v11, s17, 24\n" \
"s_add_i32 s17, s15, 0x19000\n" \
M_"v[168:171], a[180:183], a[156:159], v[168:171]\n" \
"v_writelane_b32 v11, s16, 25\n" \
"s_mov_b32 s16, 0\n" \
M_"v[168:171], a[244:247], a[220:223], v[168:171]\n" \
"s_cmpk_lt_u32 s3, 3\n" \
"s_cselect_b32 s17, s17, s16\n" \
M_"v[164:167], a[176:179], a[156:159], v[164:167]\n" \
"s_bitset1_b32 s15, 17\n" \
"v_writelane_b32 v11, s17, 26\n" \
M_"v[164:167], a[240:243], a[220:223], v[164:167]\n" \
R_"a[156:159], v28 offset:14336\n" \
R_"a[220:223], v29 offset:14336\n" \
"s_waitcnt lgkmcnt(15)\n" \
M_"a[0:3], a[160:163], a[128:131], a[0:3]\n" \
"s_add_u32 m0, s30, 0x21000\n" \
"s_addk_i32 s84, 0x3000\n" \
M_"a[0:3], a[224:227], a[192:195], a[0:3]\n" \
B_"v24, s[76:79], s84 offen offset:0 lds\n" \
M_"a[4:7], a[164:167], a[128:131], a[4:7]\n" \
X_"a[176:177], v36 offset:32768\n" \
"s_movk_i32 s17, 0x2400\n" \
M_"a[4:7], a[228:231], a[192:195], a[4:7]\n" \
X_"a[178:179], v36 offset:33792\n" \
M_"a[12:15], a[172:175], a[128:131], a[12:15]\n" \
X_"a[240:241], v36 offset:40960\n" \
"s_waitcnt lgkmcnt(15)\n" \
M_"a[12:15], a[236:239], a[192:195], a[12:15]\n" \
"s_add_u32 s88, s42, 0x1000\n" \
"buffer_store_dwordx4 v[220:223], v20, s[80:83], s88 offen offset:0 sc1\n" \
"s_waitcnt lgkmcnt(13)\n" \
M_"a[8:11], a[128:131], a[168:171], a[8:11]\n" \
X_"a[242:243], v36 offset:41984\n" \
"s_waitcnt lgkmcnt(12)\n" \
M_"a[8:11], a[192:195], a[232:235], a[8:11]\n" \
X_"a[180:181], v37 offset:32768\n" \
M_"a[24:27], a[132:135], a[168:171], a[24:27]\n" \
X_"a[182:183], v37 offset:33792\n" \
M_"a[24:27], a[196:199], a[232:235], a[24:27]\n" \
X_"a[244:245], v37 offset:40960\n" \
M_"a[28:31], a[172:175], a[132:135], a[28:31]\n" \
"s_cmpk_lt_u32 s3, 3\n" \
"s_cselect_b32 s15, s15, s17\n" \
M_"a[28:31], a[236:239], a[196:199], a[28:31]\n" \
"s_addk_i32 s84, 0x2000\n" \
M_"a[20:23], a[164:167], a[132:135], a[20:23]\n" \
B_"v25, s[76:79], s84 offen offset:1024 lds\n" \
M_"a[20:23], a[228:231], a[196:199], a[20:23]\n" \
X_"a[246:247], v37 offset:41984\n" \
"v_writelane_b32 v11, s15, 27\n" \
M_"a[16:19], a[160:163], a[132:135], a[16:19]\n" \
X_"a[184:185], v38 offset:32768\n" \
"s_lshl_b32 s15, s3, 10\n" \
M_"a[16:19], a[224:227], a[196:199], a[16:19]\n" \
X_"a[186:187], v38 offset:33792\n" \
"s_addk_i32 s15, 0x4800\n" \
M_"a[32:35], a[160:163], a[136:139], a[32:35]\n" \
"s_addk_i32 s84, 0x2000\n" \
M_"a[32:35], a[224:227], a[200:203], a[32:35]\n" \
B_"v24, s[76:79], s84 offen offset:2048 lds\n" \
M_"a[36:39], a[164:167], a[136:139], a[36:39]\n" \
X_"a[248:249], v38 offset:40960\n" \
"v_writelane_b32 v11, s15, 28\n" \
M_"a[36:39], a[228:231], a[200:203], a[36:39]\n" \
X_"a[250:251], v38 offset:41984\n" \
"s_add_i32 s15, s14, 1\n" \
M_"a[44:47], a[172:175], a[136:139], a[44:47]\n" \
X_"a[188:189], v39 offset:32768\n" \
"s_and_b32 s15, s15, 3\n" \
M_"a[44:47], a[236:239], a[200:203], a[44:47]\n" \
X_"a[190:191], v39 offset:33792\n" \
"s_or_b32 s15, s15, s4\n" \
M_"a[40:43], a[136:139], a[168:171], a[40:43]\n" \
"s_add_u32 s89, s42, 0x0\n" \
"buffer_store_dwordx4 v[192:195], v20, s[80:83], s89 offen offset:2048 sc1\n" \
M_"a[40:43], a[200:203], a[232:235], a[40:43]\n" \
X_"a[252:253], v39 offset:40960\n" \
"s_lshl_b32 s17, s15, 2\n" \
M_"a[56:59], a[140:143], a[168:171], a[56:59]\n" \
X_"a[254:255], v39 offset:41984\n" \
"s_or_b32 s17, s17, s3\n" \
M_"a[56:59], a[204:207], a[232:235], a[56:59]\n" \
"s_mul_i32 s17, s17, 0x9000\n" \
"v_writelane_b32 v11, s17, 30\n" \
M_"a[60:63], a[172:175], a[140:143], a[60:63]\n" \
"s_lshl_b32 s17, s12, 2\n" \
"s_or_b32 s17, s17, s3\n" \
M_"a[60:63], a[236:239], a[204:207], a[60:63]\n" \
"s_addk_i32 s84, 0x2000\n" \
M_"a[52:55], a[164:167], a[140:143], a[52:55]\n" \
B_"v25, s[76:79], s84 offen offset:3072 lds\n" \
M_"a[52:55], a[228:231], a[204:207], a[52:55]\n" \
"s_add_i32 s14, s14, -1\n" \
"s_xor_b32 s17, s17, 8\n" \
M_"a[48:51], a[160:163], a[140:143], a[48:51]\n" \
"s_and_b32 s14, s14, 3\n" \
"s_mul_i32 s17, s17, 0x9000\n" \
M_"a[48:51], a[224:227], a[204:207], a[48:51]\n" \
"s_or_b32 s14, s14, s4\n" \
"v_writelane_b32 v11, s17, 31\n" \
"s_waitcnt lgkmcnt(15)\n" \
M_"a[64:67], a[164:167], v[248:251], a[64:67]\n" \
"s_lshl_b32 s17, s14, 2\n" \
"s_or_b32 s17, s17, s3\n" \
M_"a[64:67], a[228:231], v[48:51], a[64:67]\n" \
"s_mul_i32 s17, s17, 0x9000\n" \
"v_writelane_b32 v11, s17, 32\n" \
M_"a[80:83], a[172:175], v[248:251], a[80:83]\n" \
"s_or_b32 s17, s8, s13\n" \
"s_mul_i32 s17, s17, 0x24800\n" \
M_"a[80:83], a[236:239], v[48:51], a[80:83]\n" \
"s_lshl_b32 s15, s15, 12\n" \
"s_add_i32 s18, s17, 0x3004800\n" \
M_"a[68:71], v[248:251], a[168:171], a[68:71]\n" \
"s_lshl_b32 s12, s12, 12\n" \
"s_add_i32 s19, s18, s15\n" \
"s_waitcnt vmcnt(11) lgkmcnt(0)\n" \
M_"a[68:71], v[48:51], a[232:235], a[68:71]\n" \
"s_barrier\n" \
M_"v[52:55], a[176:179], a[128:131], v[52:55]\n" \
"s_add_u32 m0, s40, 0x8000\n" \
"s_add_u32 s85, s39, 0xa000\n" \
M_"v[52:55], a[240:243], a[192:195], v[52:55]\n" \
B_"v20, s[80:83], s85 offen sc1 lds\n" \
M_"v[56:59], a[180:183], a[128:131], v[56:59]\n" \
X_"a[160:161], v32 offset:0\n" \
"v_accvgpr_read_b32 v224, a120\n" \
M_"v[56:59], a[244:247], a[192:195], v[56:59]\n" \
X_"a[162:163], v32 offset:512\n" \
"v_accvgpr_read_b32 v225, a121\n" \
M_"v[60:63], a[184:187], a[128:131], v[60:63]\n" \
"s_add_u32 m0, s40, 0x9000\n" \
"s_add_u32 s85, s39, 0xb000\n" \
M_"v[60:63], a[248:251], a[192:195], v[60:63]\n" \
B_"v20, s[80:83], s85 offen sc1 lds\n" \
M_"v[64:67], a[188:191], a[128:131], v[64:67]\n" \
X_"a[224:225], v32 offset:4096\n" \
"v_accvgpr_read_b32 v226, a122\n" \
M_"v[64:67], a[252:255], a[192:195], v[64:67]\n" \
R_"a[128:131], v28 offset:32768\n" \
"v_accvgpr_read_b32 v227, a123\n" \
M_"v[80:83], a[188:191], a[132:135], v[80:83]\n" \
R_"a[192:195], v29 offset:32768\n" \
"v_accvgpr_read_b32 v180, a124\n" \
M_"v[80:83], a[252:255], a[196:199], v[80:83]\n" \
"s_add_u32 s88, s42, 0x0\n" \
"buffer_store_dwordx4 v[196:199], v20, s[80:83], s88 offen offset:3072 sc1\n" \
M_"v[76:79], a[184:187], a[132:135], v[76:79]\n" \
X_"a[226:227], v32 offset:4608\n" \
"v_accvgpr_read_b32 v181, a125\n" \
M_"v[76:79], a[248:251], a[196:199], v[76:79]\n" \
X_"a[164:165], v33 offset:0\n" \
"v_accvgpr_read_b32 v182, a126\n" \
M_"v[72:75], a[180:183], a[132:135], v[72:75]\n" \
"s_add_u32 m0, s40, 0xb000\n" \
"s_movk_i32 s84, 0x280\n" \
M_"v[72:75], a[244:247], a[196:199], v[72:75]\n" \
B_"v21, s[76:79], s84 offen offset:0 lds\n" \
M_"v[68:71], a[176:179], a[132:135], v[68:71]\n" \
X_"a[166:167], v33 offset:512\n" \
"v_accvgpr_read_b32 v183, a127\n" \
M_"v[68:71], a[240:243], a[196:199], v[68:71]\n" \
R_"a[132:135], v28 offset:34816\n" \
C_"v224, v224, v225\n" \
M_"v[84:87], a[176:179], a[136:139], v[84:87]\n" \
R_"a[196:199], v29 offset:34816\n" \
C_"v225, v226, v227\n" \
M_"v[84:87], a[240:243], a[200:203], v[84:87]\n" \
"s_add_u32 m0, s31, 0x8000\n" \
"s_mov_b32 s84, 0x280000\n" \
M_"v[88:91], a[180:183], a[136:139], v[88:91]\n" \
B_"v26, s[68:71], s84 offen offset:0 lds\n" \
M_"v[88:91], a[244:247], a[200:203], v[88:91]\n" \
X_"a[228:229], v33 offset:4096\n" \
C_"v226, v180, v181\n" \
M_"v[92:95], a[184:187], a[136:139], v[92:95]\n" \
X_"a[230:231], v33 offset:4608\n" \
C_"v227, v182, v183\n" \
M_"v[92:95], a[248:251], a[200:203], v[92:95]\n" \
"s_addk_i32 s84, 0x7c00\n" \
M_"v[96:99], a[188:191], a[136:139], v[96:99]\n" \
B_"v26, s[68:71], s84 offen offset:1024 lds\n" \
M_"v[96:99], a[252:255], a[200:203], v[96:99]\n" \
R_"a[136:139], v28 offset:36864\n" \
"v_accvgpr_read_b32 v220, a112\n" \
M_"v[112:115], a[188:191], a[140:143], v[112:115]\n" \
R_"a[200:203], v29 offset:36864\n" \
"v_accvgpr_read_b32 v221, a113\n" \
M_"v[112:115], a[252:255], a[204:207], v[112:115]\n" \
"s_addk_i32 s84, 0x7c00\n" \
M_"v[108:111], a[184:187], a[140:143], v[108:111]\n" \
B_"v27, s[68:71], s84 offen offset:2048 lds\n" \
M_"v[108:111], a[248:251], a[204:207], v[108:111]\n" \
X_"a[172:173], v35 offset:0\n" \
"v_accvgpr_read_b32 v222, a114\n" \
M_"v[104:107], a[180:183], a[140:143], v[104:107]\n" \
X_"a[174:175], v35 offset:512\n" \
"v_accvgpr_read_b32 v223, a115\n" \
M_"v[104:107], a[244:247], a[204:207], v[104:107]\n" \
X_"a[236:237], v35 offset:4096\n" \
"v_accvgpr_read_b32 v180, a116\n" \
M_"v[100:103], a[176:179], a[140:143], v[100:103]\n" \
"v_accvgpr_read_b32 v181, a117\n" \
"v_accvgpr_read_b32 v182, a118\n" \
M_"v[100:103], a[240:243], a[204:207], v[100:103]\n" \
R_"a[140:143], v28 offset:38912\n" \
"v_accvgpr_read_b32 v183, a119\n" \
M_"v[116:119], a[176:179], a[144:147], v[116:119]\n" \
R_"a[204:207], v29 offset:38912\n" \
C_"v220, v220, v221\n" \
M_"v[116:119], a[240:243], a[208:211], v[116:119]\n" \
"s_addk_i32 s84, 0x7c00\n" \
M_"v[120:123], a[180:183], a[144:147], v[120:123]\n" \
B_"v27, s[68:71], s84 offen offset:3072 lds\n" \
M_"v[120:123], a[244:247], a[208:211], v[120:123]\n" \
"s_add_u32 m0, s30, 0x0\n" \
"s_movk_i32 s84, 0x300\n" \
"s_nop 0\n" \
M_"v[124:127], a[184:187], a[144:147], v[124:127]\n" \
B_"v24, s[76:79], s84 offen offset:0 lds\n" \
M_"v[124:127], a[248:251], a[208:211], v[124:127]\n" \
X_"a[238:239], v35 offset:4608\n" \
C_"v221, v222, v223\n" \
M_"v[128:131], a[188:191], a[144:147], v[128:131]\n" \
"s_add_u32 s89, s42, 0x1000\n" \
"buffer_store_dwordx4 a[84:87], v20, s[80:83], s89 offen offset:3072 sc1\n" \
M_"v[128:131], a[252:255], a[208:211], v[128:131]\n" \
R_"a[144:147], v28 offset:40960\n" \
C_"v222, v180, v181\n" \
M_"v[144:147], a[188:191], a[148:151], v[144:147]\n" \
R_"a[208:211], v29 offset:40960\n" \
C_"v223, v182, v183\n" \
M_"v[144:147], a[252:255], a[212:215], v[144:147]\n" \
X_"a[168:169], v34 offset:0\n" \
"v_accvgpr_read_b32 v216, a104\n" \
M_"v[140:143], a[184:187], a[148:151], v[140:143]\n" \
X_"a[170:171], v34 offset:512\n" \
"v_accvgpr_read_b32 v217, a105\n" \
M_"v[140:143], a[248:251], a[212:215], v[140:143]\n" \
"s_movk_i32 s84, 0x2300\n" \
"s_nop 0\n" \
M_"v[136:139], a[180:183], a[148:151], v[136:139]\n" \
B_"v25, s[76:79], s84 offen offset:1024 lds\n" \
M_"v[136:139], a[244:247], a[212:215], v[136:139]\n" \
X_"a[232:233], v34 offset:4096\n" \
"v_accvgpr_read_b32 v218, a106\n" \
M_"v[132:135], a[176:179], a[148:151], v[132:135]\n" \
X_"a[234:235], v34 offset:4608\n" \
"v_accvgpr_read_b32 v219, a107\n" \
M_"v[132:135], a[240:243], a[212:215], v[132:135]\n" \
R_"a[148:151], v28 offset:43008\n" \
"v_accvgpr_read_b32 v180, a108\n" \
M_"v[148:151], a[176:179], a[152:155], v[148:151]\n" \
R_"a[212:215], v29 offset:43008\n" \
"v_accvgpr_read_b32 v181, a109\n" \
M_"v[148:151], a[240:243], a[216:219], v[148:151]\n" \
"s_movk_i32 s84, 0x4300\n" \
M_"v[152:155], a[180:183], a[152:155], v[152:155]\n" \
B_"v24, s[76:79], s84 offen offset:2048 lds\n" \
M_"v[152:155], a[244:247], a[216:219], v[152:155]\n" \
"s_movk_i32 s84, 0x6300\n" \
M_"v[156:159], a[184:187], a[152:155], v[156:159]\n" \
B_"v25, s[76:79], s84 offen offset:3072 lds\n" \
M_"v[156:159], a[248:251], a[216:219], v[156:159]\n" \
R_"v[248:251], v22 offset:0\n" \
"v_accvgpr_read_b32 v182, a110\n" \
M_"v[160:163], a[188:191], a[152:155], v[160:163]\n" \
R_"v[48:51], v23 offset:0\n" \
"v_accvgpr_read_b32 v183, a111\n" \
M_"v[160:163], a[252:255], a[216:219], v[160:163]\n" \
R_"a[152:155], v28 offset:45056\n" \
C_"v216, v216, v217\n" \
M_"v[176:179], a[188:191], a[156:159], v[176:179]\n" \
R_"a[216:219], v29 offset:45056\n" \
C_"v217, v218, v219\n" \
M_"v[176:179], a[252:255], a[220:223], v[176:179]\n" \
C_"v218, v180, v181\n" \
C_"v219, v182, v183\n" \
M_"v[172:175], a[184:187], a[156:159], v[172:175]\n" \
"v_accvgpr_read_b32 v192, a96\n" \
"v_accvgpr_read_b32 v193, a97\n" \
M_"v[172:175], a[248:251], a[220:223], v[172:175]\n" \
"v_accvgpr_read_b32 v194, a98\n" \
"v_accvgpr_read_b32 v195, a99\n" \
M_"v[168:171], a[180:183], a[156:159], v[168:171]\n" \
"v_accvgpr_read_b32 v180, a100\n" \
"v_accvgpr_read_b32 v181, a101\n" \
M_"v[168:171], a[244:247], a[220:223], v[168:171]\n" \
"v_accvgpr_read_b32 v182, a102\n" \
"v_accvgpr_read_b32 v183, a103\n" \
M_"v[164:167], a[176:179], a[156:159], v[164:167]\n" \
C_"v192, v192, v193\n" \
C_"v193, v194, v195\n" \
M_"v[164:167], a[240:243], a[220:223], v[164:167]\n" \
R_"a[156:159], v28 offset:47104\n" \
R_"a[220:223], v29 offset:47104\n" \
"s_waitcnt lgkmcnt(15)\n" \
M_"a[0:3], a[160:163], a[128:131], a[0:3]\n" \
"s_add_u32 m0, s30, 0x1000\n" \
"s_addk_i32 s84, 0x3000\n" \
M_"a[0:3], a[224:227], a[192:195], a[0:3]\n" \
B_"v24, s[76:79], s84 offen offset:0 lds\n" \
M_"a[4:7], a[164:167], a[128:131], a[4:7]\n" \
X_"a[176:177], v36 offset:0\n" \
C_"v194, v180, v181\n" \
M_"a[4:7], a[228:231], a[192:195], a[4:7]\n" \
X_"a[178:179], v36 offset:1024\n" \
C_"v195, v182, v183\n" \
M_"a[12:15], a[172:175], a[128:131], a[12:15]\n" \
X_"a[240:241], v36 offset:8192\n" \
"v_accvgpr_read_b32 v188, a88\n" \
"s_waitcnt lgkmcnt(15)\n" \
M_"a[12:15], a[236:239], a[192:195], a[12:15]\n" \
"s_add_u32 s88, s42, 0x1000\n" \
"buffer_store_dwordx4 v[252:255], v20, s[80:83], s88 offen offset:2048 sc1\n" \
"s_waitcnt lgkmcnt(13)\n" \
M_"a[8:11], a[128:131], a[168:171], a[8:11]\n" \
X_"a[242:243], v36 offset:9216\n" \
"v_accvgpr_read_b32 v189, a89\n" \
"s_waitcnt lgkmcnt(12)\n" \
"s_nop 0\n" \
M_"a[8:11], a[192:195], a[232:235], a[8:11]\n" \
X_"a[180:181], v37 offset:0\n" \
"v_accvgpr_read_b32 v190, a90\n" \
M_"a[24:27], a[132:135], a[168:171], a[24:27]\n" \
X_"a[182:183], v37 offset:1024\n" \
"v_accvgpr_read_b32 v191, a91\n" \
M_"a[24:27], a[196:199], a[232:235], a[24:27]\n" \
X_"a[244:245], v37 offset:8192\n" \
"v_accvgpr_read_b32 v180, a92\n" \
M_"a[28:31], a[172:175], a[132:135], a[28:31]\n" \
"v_accvgpr_read_b32 v181, a93\n" \
"v_accvgpr_read_b32 v182, a94\n" \
M_"a[28:31], a[236:239], a[196:199], a[28:31]\n" \
"s_addk_i32 s84, 0x2000\n" \
"s_nop 0\n" \
M_"a[20:23], a[164:167], a[132:135], a[20:23]\n" \
B_"v25, s[76:79], s84 offen offset:1024 lds\n" \
M_"a[20:23], a[228:231], a[196:199], a[20:23]\n" \
X_"a[246:247], v37 offset:9216\n" \
"v_accvgpr_read_b32 v183, a95\n" \
M_"a[16:19], a[160:163], a[132:135], a[16:19]\n" \
X_"a[184:185], v38 offset:0\n" \
C_"v188, v188, v189\n" \
M_"a[16:19], a[224:227], a[196:199], a[16:19]\n" \
X_"a[186:187], v38 offset:1024\n" \
C_"v189, v190, v191\n" \
M_"a[32:35], a[160:163], a[136:139], a[32:35]\n" \
"s_addk_i32 s84, 0x2000\n" \
"s_nop 0\n" \
M_"a[32:35], a[224:227], a[200:203], a[32:35]\n" \
B_"v24, s[76:79], s84 offen offset:2048 lds\n" \
M_"a[36:39], a[164:167], a[136:139], a[36:39]\n" \
X_"a[248:249], v38 offset:8192\n" \
C_"v190, v180, v181\n" \
M_"a[36:39], a[228:231], a[200:203], a[36:39]\n" \
X_"a[250:251], v38 offset:9216\n" \
C_"v191, v182, v183\n" \
M_"a[44:47], a[172:175], a[136:139], a[44:47]\n" \
X_"a[188:189], v39 offset:0\n" \
"v_accvgpr_read_b32 v184, a72\n" \
M_"a[44:47], a[236:239], a[200:203], a[44:47]\n" \
X_"a[190:191], v39 offset:1024\n" \
"v_accvgpr_read_b32 v185, a73\n" \
".fill (L_pb + 28384 - .) / 4, 4, 0xBF800000\n" \
"L_pp7:\n" \
"s_cbranch_execz L_pret\n" \
"s_nop 0\n" \
M_"a[40:43], a[136:139], a[168:171], a[40:43]\n" \
"s_add_u32 s89, s42, 0x2000\n" \
"buffer_store_dwordx4 v[200:203], v20, s[80:83], s89 offen offset:2048 sc1\n" \
M_"a[40:43], a[200:203], a[232:235], a[40:43]\n" \
X_"a[252:253], v39 offset:8192\n" \
"v_accvgpr_read_b32 v186, a74\n" \
M_"a[56:59], a[140:143], a[168:171], a[56:59]\n" \
X_"a[254:255], v39 offset:9216\n" \
"v_accvgpr_read_b32 v187, a75\n" \
M_"a[56:59], a[204:207], a[232:235], a[56:59]\n" \
"v_accvgpr_read_b32 v180, a76\n" \
"v_accvgpr_read_b32 v181, a77\n" \
M_"a[60:63], a[172:175], a[140:143], a[60:63]\n" \
"v_accvgpr_read_b32 v182, a78\n" \
"v_accvgpr_read_b32 v183, a79\n" \
M_"a[60:63], a[236:239], a[204:207], a[60:63]\n" \
"s_addk_i32 s84, 0x2000\n" \
"s_nop 0\n" \
M_"a[52:55], a[164:167], a[140:143], a[52:55]\n" \
B_"v25, s[76:79], s84 offen offset:3072 lds\n" \
M_"a[52:55], a[228:231], a[204:207], a[52:55]\n" \
C_"v184, v184, v185\n" \
C_"v185, v186, v187\n" \
M_"a[48:51], a[160:163], a[140:143], a[48:51]\n" \
C_"v186, v180, v181\n" \
C_"v187, v182, v183\n" \
M_"a[48:51], a[224:227], a[204:207], a[48:51]\n" \
"v_writelane_b32 v11, s19, 33\n" \
"s_xor_b32 s12, s12, 0x2000\n" \
"s_waitcnt lgkmcnt(15)\n" \
M_"a[64:67], a[164:167], v[248:251], a[64:67]\n" \
"s_lshl_b32 s14, s14, 12\n" \
"s_sub_i32 s13, s8, s13\n" \
M_"a[64:67], a[228:231], v[48:51], a[64:67]\n" \
"s_add_i32 s19, s18, s12\n" \
"v_writelane_b32 v11, s19, 34\n" \
M_"a[80:83], a[172:175], v[248:251], a[80:83]\n" \
"s_add_i32 s18, s18, s14\n" \
"s_mul_i32 s13, s13, 0x24800\n" \
M_"a[80:83], a[236:239], v[48:51], a[80:83]\n" \
"v_writelane_b32 v11, s18, 35\n" \
"s_add_i32 s18, s13, 0x4120800\n" \
M_"a[68:71], v[248:251], a[168:171], a[68:71]\n" \
"s_add_i32 s15, s18, s15\n" \
"v_writelane_b32 v11, s15, 36\n" \
"s_waitcnt vmcnt(11) lgkmcnt(0)\n" \
M_"a[68:71], v[48:51], a[232:235], a[68:71]\n" \
"s_barrier\n" \
"v_mov_b32_e32 v45, s62\n" \
"s_mov_b64 exec, 1\n" \
"buffer_store_dword v45, off, s[80:83], s37 offset:0 sc1\n" \
"s_mov_b64 exec, -1\n" \
"v_xor_b32_e32 v28, 0x20000, v28\n" \
"v_xor_b32_e32 v29, 0x20000, v29\n" \
M_"v[52:55], a[176:179], a[128:131], v[52:55]\n" \
"s_add_u32 m0, s40, 0x0\n" \
"s_add_u32 s85, s39, 0xc000\n" \
M_"v[52:55], a[240:243], a[192:195], v[52:55]\n" \
B_"v20, s[80:83], s85 offen sc1 lds\n" \
M_"v[56:59], a[180:183], a[128:131], v[56:59]\n" \
X_"a[160:161], v32 offset:32768\n" \
"s_add_i32 s12, s18, s12\n" \
M_"v[56:59], a[244:247], a[192:195], v[56:59]\n" \
X_"a[162:163], v32 offset:33280\n" \
"v_writelane_b32 v11, s12, 37\n" \
M_"v[60:63], a[184:187], a[128:131], v[60:63]\n" \
"s_add_u32 m0, s40, 0x1000\n" \
"s_add_u32 s85, s39, 0xd000\n" \
M_"v[60:63], a[248:251], a[192:195], v[60:63]\n" \
B_"v20, s[80:83], s85 offen sc1 lds\n" \
M_"v[64:67], a[188:191], a[128:131], v[64:67]\n" \
X_"a[224:225], v32 offset:36864\n" \
"s_add_i32 s18, s18, s14\n" \
M_"v[64:67], a[252:255], a[192:195], v[64:67]\n" \
R_"a[128:131], v28 offset:0\n" \
"s_or_b32 s12, s17, 0x3000000\n" \
M_"v[80:83], a[188:191], a[132:135], v[80:83]\n" \
R_"a[192:195], v29 offset:0\n" \
"s_lshl_b32 s14, s5, 11\n" \
M_"v[80:83], a[252:255], a[196:199], v[80:83]\n" \
"s_add_u32 m0, s40, 0x3000\n" \
"s_movk_i32 s84, 0x300\n" \
M_"v[76:79], a[184:187], a[132:135], v[76:79]\n" \
B_"v21, s[76:79], s84 offen offset:0 lds\n" \
M_"v[76:79], a[248:251], a[196:199], v[76:79]\n" \
X_"a[226:227], v32 offset:37376\n" \
"v_writelane_b32 v11, s18, 38\n" \
M_"v[72:75], a[180:183], a[132:135], v[72:75]\n" \
X_"a[164:165], v33 offset:32768\n" \
"s_add_i32 s15, s12, s14\n" \
M_"v[72:75], a[244:247], a[196:199], v[72:75]\n" \
"v_writelane_b32 v11, s15, 39\n" \
"s_add_i32 s15, s13, 0x411c000\n" \
M_"v[68:71], a[176:179], a[132:135], v[68:71]\n" \
X_"a[166:167], v33 offset:33280\n" \
"s_add_i32 s18, s15, s14\n" \
M_"v[68:71], a[240:243], a[196:199], v[68:71]\n" \
R_"a[132:135], v28 offset:2048\n" \
"v_writelane_b32 v11, s18, 40\n" \
M_"v[84:87], a[176:179], a[136:139], v[84:87]\n" \
R_"a[196:199], v29 offset:2048\n" \
"s_add_i32 s17, s17, 0x3004000\n" \
M_"v[84:87], a[240:243], a[200:203], v[84:87]\n" \
"s_add_u32 m0, s31, 0x0\n" \
"s_nop 0\n" \
"s_mov_b32 s84, 0x300000\n" \
M_"v[88:91], a[180:183], a[136:139], v[88:91]\n" \
B_"v26, s[68:71], s84 offen offset:0 lds\n" \
M_"v[88:91], a[244:247], a[200:203], v[88:91]\n" \
"s_add_u32 s88, s42, 0x2000\n" \
"buffer_store_dwordx4 v[204:207], v20, s[80:83], s88 offen offset:3072 sc1\n" \
M_"v[92:95], a[184:187], a[136:139], v[92:95]\n" \
X_"a[228:229], v33 offset:36864\n" \
"v_writelane_b32 v11, s17, 41\n" \
M_"v[92:95], a[248:251], a[200:203], v[92:95]\n" \
X_"a[230:231], v33 offset:37376\n" \
"s_add_i32 s13, s13, 0x4120000\n" \
M_"v[96:99], a[188:191], a[136:139], v[96:99]\n" \
"v_writelane_b32 v11, s13, 42\n" \
"s_lshl_b32 s13, s9, 2\n" \
M_"v[96:99], a[252:255], a[200:203], v[96:99]\n" \
R_"a[136:139], v28 offset:4096\n" \
"s_or_b32 s7, s13, s7\n" \
M_"v[112:115], a[188:191], a[140:143], v[112:115]\n" \
R_"a[200:203], v29 offset:4096\n" \
"s_mul_i32 s7, s7, 0x90000\n" \
M_"v[112:115], a[252:255], a[204:207], v[112:115]\n" \
"s_addk_i32 s84, 0x7c00\n" \
M_"v[108:111], a[184:187], a[140:143], v[108:111]\n" \
B_"v26, s[68:71], s84 offen offset:1024 lds\n" \
M_"v[108:111], a[248:251], a[204:207], v[108:111]\n" \
X_"a[172:173], v35 offset:32768\n" \
"s_lshl_b32 s13, s3, 14\n" \
M_"v[104:107], a[180:183], a[140:143], v[104:107]\n" \
"s_addk_i32 s84, 0x7c00\n" \
M_"v[104:107], a[244:247], a[204:207], v[104:107]\n" \
B_"v27, s[68:71], s84 offen offset:2048 lds\n" \
M_"v[100:103], a[176:179], a[140:143], v[100:103]\n" \
X_"a[174:175], v35 offset:33280\n" \
"s_lshl_b32 s9, s9, 7\n" \
M_"v[100:103], a[240:243], a[204:207], v[100:103]\n" \
R_"a[140:143], v28 offset:6144\n" \
"s_lshl_b32 s8, s8, 4\n" \
M_"v[116:119], a[176:179], a[144:147], v[116:119]\n" \
R_"a[204:207], v29 offset:6144\n" \
"s_add_i32 s7, s7, s13\n" \
M_"v[116:119], a[240:243], a[208:211], v[116:119]\n" \
"s_addk_i32 s84, 0x7c00\n" \
M_"v[120:123], a[180:183], a[144:147], v[120:123]\n" \
B_"v27, s[68:71], s84 offen offset:3072 lds\n" \
M_"v[120:123], a[244:247], a[208:211], v[120:123]\n" \
X_"a[236:237], v35 offset:36864\n" \
"s_or_b32 s13, s8, s9\n" \
M_"v[124:127], a[184:187], a[144:147], v[124:127]\n" \
X_"a[238:239], v35 offset:37376\n" \
"s_sub_i32 s8, s8, s9\n" \
M_"v[124:127], a[248:251], a[208:211], v[124:127]\n" \
"s_add_u32 m0, s30, 0x8000\n" \
"s_movk_i32 s84, 0x380\n" \
M_"v[128:131], a[188:191], a[144:147], v[128:131]\n" \
B_"v24, s[76:79], s84 offen offset:0 lds\n" \
M_"v[128:131], a[252:255], a[208:211], v[128:131]\n" \
R_"a[144:147], v28 offset:8192\n" \
"s_mulk_i32 s13, 0x480\n" \
M_"v[144:147], a[188:191], a[148:151], v[144:147]\n" \
R_"a[208:211], v29 offset:8192\n" \
"s_mulk_i32 s8, 0x480\n" \
M_"v[144:147], a[252:255], a[212:215], v[144:147]\n" \
X_"a[168:169], v34 offset:32768\n" \
"s_or_b32 s13, s13, s11\n" \
M_"v[140:143], a[184:187], a[148:151], v[140:143]\n" \
"s_movk_i32 s84, 0x2380\n" \
M_"v[140:143], a[248:251], a[212:215], v[140:143]\n" \
B_"v25, s[76:79], s84 offen offset:1024 lds\n" \
M_"v[136:139], a[180:183], a[148:151], v[136:139]\n" \
X_"a[170:171], v34 offset:33280\n" \
"s_or_b32 s8, s8, s11\n" \
M_"v[136:139], a[244:247], a[212:215], v[136:139]\n" \
X_"a[232:233], v34 offset:36864\n" \
"s_and_b32 s11, s2, 1\n" \
M_"v[132:135], a[176:179], a[148:151], v[132:135]\n" \
X_"a[234:235], v34 offset:37376\n" \
M_"v[132:135], a[240:243], a[212:215], v[132:135]\n" \
R_"a[148:151], v28 offset:10240\n" \
M_"v[148:151], a[176:179], a[152:155], v[148:151]\n" \
R_"a[212:215], v29 offset:10240\n" \
M_"v[148:151], a[240:243], a[216:219], v[148:151]\n" \
"s_movk_i32 s84, 0x4380\n" \
M_"v[152:155], a[180:183], a[152:155], v[152:155]\n" \
B_"v24, s[76:79], s84 offen offset:2048 lds\n" \
M_"v[152:155], a[244:247], a[216:219], v[152:155]\n" \
"s_movk_i32 s84, 0x6380\n" \
M_"v[156:159], a[184:187], a[152:155], v[156:159]\n" \
B_"v25, s[76:79], s84 offen offset:3072 lds\n" \
M_"v[156:159], a[248:251], a[216:219], v[156:159]\n" \
R_"v[248:251], v22 offset:32768\n" \
M_"v[160:163], a[188:191], a[152:155], v[160:163]\n" \
R_"v[48:51], v23 offset:32768\n" \
M_"v[160:163], a[252:255], a[216:219], v[160:163]\n" \
R_"a[152:155], v28 offset:12288\n" \
M_"v[176:179], a[188:191], a[156:159], v[176:179]\n" \
R_"a[216:219], v29 offset:12288\n" \
M_"v[176:179], a[252:255], a[220:223], v[176:179]\n" \
"s_cmp_lg_u32 s11, 0\n" \
"s_cselect_b32 s12, s15, s12\n" \
M_"v[172:175], a[184:187], a[156:159], v[172:175]\n" \
"s_bfe_u32 s9, s2, 0x20001\n" \
"s_add_i32 s15, s7, 0x4240000\n" \
M_"v[172:175], a[248:251], a[220:223], v[172:175]\n" \
"s_addk_i32 s12, 0x4000\n" \
"s_lshl_b32 s5, s5, 16\n" \
M_"v[168:171], a[180:183], a[156:159], v[168:171]\n" \
"s_xor_b32 s9, s9, s3\n" \
M_"v[168:171], a[244:247], a[220:223], v[168:171]\n" \
"s_cmp_lg_u32 s9, 0\n" \
"s_cselect_b32 s12, s6, s12\n" \
M_"v[164:167], a[176:179], a[156:159], v[164:167]\n" \
"s_add_i32 s5, s15, s5\n" \
"v_writelane_b32 v11, s12, 43\n" \
M_"v[164:167], a[240:243], a[220:223], v[164:167]\n" \
R_"a[156:159], v28 offset:14336\n" \
R_"a[220:223], v29 offset:14336\n" \
"s_waitcnt lgkmcnt(15)\n" \
M_"a[0:3], a[160:163], a[128:131], a[0:3]\n" \
"s_add_u32 m0, s30, 0x9000\n" \
"s_addk_i32 s84, 0x3000\n" \
M_"a[0:3], a[224:227], a[192:195], a[0:3]\n" \
B_"v24, s[76:79], s84 offen offset:0 lds\n" \
M_"a[4:7], a[164:167], a[128:131], a[4:7]\n" \
X_"a[176:177], v36 offset:32768\n" \
"s_add_i32 s7, s7, 0x42c0000\n" \
M_"a[4:7], a[228:231], a[192:195], a[4:7]\n" \
X_"a[178:179], v36 offset:33792\n" \
"v_writelane_b32 v11, s5, 44\n" \
M_"a[12:15], a[172:175], a[128:131], a[12:15]\n" \
X_"a[240:241], v36 offset:40960\n" \
"s_lshl_b32 s5, s11, 10\n" \
"s_waitcnt lgkmcnt(15)\n" \
M_"a[12:15], a[236:239], a[192:195], a[12:15]\n" \
"s_add_u32 s89, s42, 0x3000\n" \
"buffer_store_dwordx4 v[208:211], v20, s[80:83], s89 offen offset:3072 sc1\n" \
"s_waitcnt lgkmcnt(13)\n" \
M_"a[8:11], a[128:131], a[168:171], a[8:11]\n" \
X_"a[242:243], v36 offset:41984\n" \
"s_or_b32 s5, s7, s5\n" \
"s_waitcnt lgkmcnt(12)\n" \
M_"a[8:11], a[192:195], a[232:235], a[8:11]\n" \
X_"a[180:181], v37 offset:32768\n" \
M_"a[24:27], a[132:135], a[168:171], a[24:27]\n" \
X_"a[182:183], v37 offset:33792\n" \
M_"a[24:27], a[196:199], a[232:235], a[24:27]\n" \
X_"a[244:245], v37 offset:40960\n" \
M_"a[28:31], a[172:175], a[132:135], a[28:31]\n" \
"s_cmp_lg_u32 s9, 0\n" \
"s_cselect_b32 s5, s6, s5\n" \
M_"a[28:31], a[236:239], a[196:199], a[28:31]\n" \
"s_addk_i32 s84, 0x2000\n" \
M_"a[20:23], a[164:167], a[132:135], a[20:23]\n" \
B_"v25, s[76:79], s84 offen offset:1024 lds\n" \
M_"a[20:23], a[228:231], a[196:199], a[20:23]\n" \
X_"a[246:247], v37 offset:41984\n" \
"s_add_i32 s8, s8, 0x21c000\n" \
M_"a[16:19], a[160:163], a[132:135], a[16:19]\n" \
X_"a[184:185], v38 offset:32768\n" \
"v_writelane_b32 v11, s5, 45\n" \
M_"a[16:19], a[224:227], a[196:199], a[16:19]\n" \
X_"a[186:187], v38 offset:33792\n" \
"s_add_i32 s5, s13, s10\n" \
M_"a[32:35], a[160:163], a[136:139], a[32:35]\n" \
"s_addk_i32 s84, 0x2000\n" \
M_"a[32:35], a[224:227], a[200:203], a[32:35]\n" \
B_"v24, s[76:79], s84 offen offset:2048 lds\n" \
M_"a[36:39], a[164:167], a[136:139], a[36:39]\n" \
X_"a[248:249], v38 offset:40960\n" \
"v_writelane_b32 v11, s5, 46\n" \
M_"a[36:39], a[228:231], a[200:203], a[36:39]\n" \
X_"a[250:251], v38 offset:41984\n" \
"s_add_i32 s5, s8, s10\n" \
"s_nop 0\n" \
M_"a[44:47], a[172:175], a[136:139], a[44:47]\n" \
X_"a[188:189], v39 offset:32768\n" \
"v_writelane_b32 v11, s5, 47\n" \
M_"a[44:47], a[236:239], a[200:203], a[44:47]\n" \
X_"a[190:191], v39 offset:33792\n" \
M_"a[40:43], a[136:139], a[168:171], a[40:43]\n" \
"s_add_u32 s88, s42, 0x3000\n" \
"buffer_store_dwordx4 v[212:215], v20, s[80:83], s88 offen offset:2048 sc1\n" \
M_"a[40:43], a[200:203], a[232:235], a[40:43]\n" \
X_"a[252:253], v39 offset:40960\n" \
M_"a[56:59], a[140:143], a[168:171], a[56:59]\n" \
X_"a[254:255], v39 offset:41984\n" \
M_"a[56:59], a[204:207], a[232:235], a[56:59]\n" \
"s_cmp_lg_u32 s11, 0\n" \
"s_cselect_b32 s5, s8, s13\n" \
M_"a[60:63], a[172:175], a[140:143], a[60:63]\n" \
"s_addk_i32 s5, 0x100\n" \
M_"a[60:63], a[236:239], a[204:207], a[60:63]\n" \
"s_addk_i32 s84, 0x2000\n" \
M_"a[52:55], a[164:167], a[140:143], a[52:55]\n" \
B_"v25, s[76:79], s84 offen offset:3072 lds\n" \
M_"a[52:55], a[228:231], a[204:207], a[52:55]\n" \
"s_cmp_lg_u32 s9, 0\n" \
"s_cselect_b32 s5, s6, s5\n" \
M_"a[48:51], a[160:163], a[140:143], a[48:51]\n" \
"v_writelane_b32 v11, s5, 48\n" \
"s_or_b32 s5, s7, s14\n" \
M_"a[48:51], a[224:227], a[204:207], a[48:51]\n" \
"v_writelane_b32 v11, s5, 56\n" \
"s_lshl_b32 s5, s3, 17\n" \
"s_waitcnt lgkmcnt(15)\n" \
M_"a[64:67], a[164:167], v[248:251], a[64:67]\n" \
"s_add_i32 s5, s15, s5\n" \
"s_or_b32 s4, s5, s4\n" \
M_"a[64:67], a[228:231], v[48:51], a[64:67]\n" \
"s_or_b32 s4, s4, 0x3840\n" \
"v_writelane_b32 v11, s4, 6\n" \
M_"a[80:83], a[172:175], v[248:251], a[80:83]\n" \
"s_mov_b32 s4, 0xff0000\n" \
M_"a[80:83], a[236:239], v[48:51], a[80:83]\n" \
"s_cmp_lg_u32 s9, 0\n" \
"s_cselect_b32 s4, s16, s4\n" \
M_"a[68:71], v[248:251], a[168:171], a[68:71]\n" \
"v_writelane_b32 v11, s4, 7\n" \
"s_lshl_b32 s4, s2, 16\n" \
"s_waitcnt vmcnt(10) lgkmcnt(0)\n" \
M_"a[68:71], v[48:51], a[232:235], a[68:71]\n" \
"s_barrier\n" \
"v_xor_b32_e32 v28, 0x20000, v28\n" \
"v_xor_b32_e32 v29, 0x20000, v29\n" \
M_"v[52:55], a[176:179], a[128:131], v[52:55]\n" \
"s_add_u32 m0, s40, 0x8000\n" \
"s_add_u32 s85, s39, 0xe000\n" \
M_"v[52:55], a[240:243], a[192:195], v[52:55]\n" \
B_"v20, s[80:83], s85 offen sc1 lds\n" \
M_"v[56:59], a[180:183], a[128:131], v[56:59]\n" \
X_"a[160:161], v32 offset:0\n" \
"s_add_i32 s5, s4, 0x10000\n" \
M_"v[56:59], a[244:247], a[192:195], v[56:59]\n" \
X_"a[162:163], v32 offset:512\n" \
"s_and_b32 s5, s5, 0x70000\n" \
M_"v[60:63], a[184:187], a[128:131], v[60:63]\n" \
"s_add_u32 m0, s40, 0x9000\n" \
"s_add_u32 s85, s39, 0xf000\n" \
M_"v[60:63], a[248:251], a[192:195], v[60:63]\n" \
B_"v20, s[80:83], s85 offen sc1 lds\n" \
M_"v[64:67], a[188:191], a[128:131], v[64:67]\n" \
X_"a[224:225], v32 offset:4096\n" \
"s_add_i32 s5, s15, s5\n" \
M_"v[64:67], a[252:255], a[192:195], v[64:67]\n" \
R_"a[128:131], v28 offset:0\n" \
"v_writelane_b32 v11, s5, 49\n" \
M_"v[80:83], a[188:191], a[132:135], v[80:83]\n" \
R_"a[192:195], v29 offset:0\n" \
"s_add_i32 s5, s4, 0x20000\n" \
M_"v[80:83], a[252:255], a[196:199], v[80:83]\n" \
"s_add_u32 m0, s40, 0xb000\n" \
"s_movk_i32 s84, 0x380\n" \
M_"v[76:79], a[184:187], a[132:135], v[76:79]\n" \
B_"v21, s[76:79], s84 offen offset:0 lds\n" \
M_"v[76:79], a[248:251], a[196:199], v[76:79]\n" \
X_"a[226:227], v32 offset:4608\n" \
"s_and_b32 s5, s5, 0x70000\n" \
M_"v[72:75], a[180:183], a[132:135], v[72:75]\n" \
X_"a[164:165], v33 offset:0\n" \
"s_add_i32 s5, s15, s5\n" \
M_"v[72:75], a[244:247], a[196:199], v[72:75]\n" \
"s_bitset1_b32 s5, 11\n" \
"v_writelane_b32 v11, s5, 50\n" \
M_"v[68:71], a[176:179], a[132:135], v[68:71]\n" \
X_"a[166:167], v33 offset:512\n" \
"s_add_i32 s5, s4, 0x30000\n" \
M_"v[68:71], a[240:243], a[196:199], v[68:71]\n" \
R_"a[132:135], v28 offset:2048\n" \
"s_and_b32 s5, s5, 0x70000\n" \
M_"v[84:87], a[176:179], a[136:139], v[84:87]\n" \
R_"a[196:199], v29 offset:2048\n" \
"s_add_i32 s5, s15, s5\n" \
M_"v[84:87], a[240:243], a[200:203], v[84:87]\n" \
"s_add_u32 m0, s31, 0x8000\n" \
"s_mov_b32 s84, 0x380000\n" \
M_"v[88:91], a[180:183], a[136:139], v[88:91]\n" \
B_"v26, s[68:71], s84 offen offset:0 lds\n" \
M_"v[88:91], a[244:247], a[200:203], v[88:91]\n" \
"s_add_u32 s89, s42, 0x0\n" \
"buffer_store_dwordx4 v[184:187], v20, s[80:83], s89 offen offset:1024 sc1\n" \
M_"v[92:95], a[184:187], a[136:139], v[92:95]\n" \
X_"a[228:229], v33 offset:4096\n" \
"s_bitset1_b32 s5, 12\n" \
M_"v[92:95], a[248:251], a[200:203], v[92:95]\n" \
X_"a[230:231], v33 offset:4608\n" \
"v_writelane_b32 v11, s5, 51\n" \
M_"v[96:99], a[188:191], a[136:139], v[96:99]\n" \
"s_and_b32 s5, s4, 0x70000\n" \
"s_xor_b32 s5, s5, 0x40000\n" \
M_"v[96:99], a[252:255], a[200:203], v[96:99]\n" \
R_"a[136:139], v28 offset:4096\n" \
"s_add_i32 s5, s15, s5\n" \
M_"v[112:115], a[188:191], a[140:143], v[112:115]\n" \
R_"a[200:203], v29 offset:4096\n" \
"s_or_b32 s5, s5, 0x1800\n" \
M_"v[112:115], a[252:255], a[204:207], v[112:115]\n" \
"s_addk_i32 s84, 0x7c00\n" \
M_"v[108:111], a[184:187], a[140:143], v[108:111]\n" \
B_"v26, s[68:71], s84 offen offset:1024 lds\n" \
M_"v[108:111], a[248:251], a[204:207], v[108:111]\n" \
X_"a[172:173], v35 offset:0\n" \
"v_writelane_b32 v11, s5, 52\n" \
M_"v[104:107], a[180:183], a[140:143], v[104:107]\n" \
"s_addk_i32 s84, 0x7c00\n" \
M_"v[104:107], a[244:247], a[204:207], v[104:107]\n" \
B_"v27, s[68:71], s84 offen offset:2048 lds\n" \
M_"v[100:103], a[176:179], a[140:143], v[100:103]\n" \
X_"a[174:175], v35 offset:512\n" \
"s_add_i32 s5, s4, 0x50000\n" \
M_"v[100:103], a[240:243], a[204:207], v[100:103]\n" \
R_"a[140:143], v28 offset:6144\n" \
"s_and_b32 s5, s5, 0x70000\n" \
M_"v[116:119], a[176:179], a[144:147], v[116:119]\n" \
R_"a[204:207], v29 offset:6144\n" \
"s_add_i32 s5, s15, s5\n" \
M_"v[116:119], a[240:243], a[208:211], v[116:119]\n" \
"s_addk_i32 s84, 0x7c00\n" \
M_"v[120:123], a[180:183], a[144:147], v[120:123]\n" \
B_"v27, s[68:71], s84 offen offset:3072 lds\n" \
M_"v[120:123], a[244:247], a[208:211], v[120:123]\n" \
X_"a[236:237], v35 offset:4096\n" \
"s_bitset1_b32 s5, 13\n" \
M_"v[124:127], a[184:187], a[144:147], v[124:127]\n" \
X_"a[238:239], v35 offset:4608\n" \
"v_writelane_b32 v11, s5, 53\n" \
M_"v[124:127], a[248:251], a[208:211], v[124:127]\n" \
"s_add_u32 m0, s30, 0x20000\n" \
"s_movk_i32 s84, 0x400\n" \
M_"v[128:131], a[188:191], a[144:147], v[128:131]\n" \
B_"v24, s[76:79], s84 offen offset:0 lds\n" \
M_"v[128:131], a[252:255], a[208:211], v[128:131]\n" \
R_"a[144:147], v28 offset:8192\n" \
"s_add_i32 s5, s4, 0x60000\n" \
M_"v[144:147], a[188:191], a[148:151], v[144:147]\n" \
R_"a[208:211], v29 offset:8192\n" \
"s_and_b32 s5, s5, 0x70000\n" \
M_"v[144:147], a[252:255], a[212:215], v[144:147]\n" \
X_"a[168:169], v34 offset:0\n" \
"s_add_i32 s4, s4, 0x70000\n" \
M_"v[140:143], a[184:187], a[148:151], v[140:143]\n" \
"s_movk_i32 s84, 0x2400\n" \
M_"v[140:143], a[248:251], a[212:215], v[140:143]\n" \
B_"v25, s[76:79], s84 offen offset:1024 lds\n" \
M_"v[136:139], a[180:183], a[148:151], v[136:139]\n" \
X_"a[170:171], v34 offset:512\n" \
"s_add_i32 s5, s15, s5\n" \
M_"v[136:139], a[244:247], a[212:215], v[136:139]\n" \
X_"a[232:233], v34 offset:4096\n" \
"s_and_b32 s4, s4, 0x70000\n" \
M_"v[132:135], a[176:179], a[148:151], v[132:135]\n" \
X_"a[234:235], v34 offset:4608\n" \
"s_or_b32 s5, s5, 0x2800\n" \
M_"v[132:135], a[240:243], a[212:215], v[132:135]\n" \
R_"a[148:151], v28 offset:10240\n" \
"v_writelane_b32 v11, s5, 54\n" \
M_"v[148:151], a[176:179], a[152:155], v[148:151]\n" \
R_"a[212:215], v29 offset:10240\n" \
"s_add_i32 s15, s15, s4\n" \
M_"v[148:151], a[240:243], a[216:219], v[148:151]\n" \
"s_movk_i32 s84, 0x4400\n" \
M_"v[152:155], a[180:183], a[152:155], v[152:155]\n" \
B_"v24, s[76:79], s84 offen offset:2048 lds\n" \
M_"v[152:155], a[244:247], a[216:219], v[152:155]\n" \
"s_movk_i32 s84, 0x6400\n" \
M_"v[156:159], a[184:187], a[152:155], v[156:159]\n" \
B_"v25, s[76:79], s84 offen offset:3072 lds\n" \
M_"v[156:159], a[248:251], a[216:219], v[156:159]\n" \
R_"v[248:251], v22 offset:0\n" \
"s_or_b32 s4, s15, 0x3000\n" \
M_"v[160:163], a[188:191], a[152:155], v[160:163]\n" \
R_"v[48:51], v23 offset:0\n" \
"v_writelane_b32 v11, s4, 55\n" \
M_"v[160:163], a[252:255], a[216:219], v[160:163]\n" \
R_"a[152:155], v28 offset:12288\n" \
"s_movk_i32 s11, 0x4000\n" \
M_"v[176:179], a[188:191], a[156:159], v[176:179]\n" \
R_"a[216:219], v29 offset:12288\n" \
"s_lshl_b32 s4, s2, 2\n" \
M_"v[176:179], a[252:255], a[220:223], v[176:179]\n" \
"s_and_b32 s4, s4, 28\n" \
"s_bfe_u32 s5, s2, 0x20003\n" \
M_"v[172:175], a[184:187], a[156:159], v[172:175]\n" \
"s_or_b32 s4, s4, s5\n" \
"s_lshr_b32 s5, s2, 5\n" \
M_"v[172:175], a[248:251], a[220:223], v[172:175]\n" \
"s_lshr_b32 s19, s3, 1\n" \
"s_lshl_b32 s18, s5, 1\n" \
M_"v[168:171], a[180:183], a[156:159], v[168:171]\n" \
"s_lshl_b32 s6, s4, 20\n" \
"s_lshl_b32 s7, s4, 8\n" \
M_"v[168:171], a[244:247], a[220:223], v[168:171]\n" \
"s_and_b32 s13, s3, 1\n" \
"s_add_i32 s14, s18, s19\n" \
M_"v[164:167], a[176:179], a[156:159], v[164:167]\n" \
"s_add_u32 s6, s26, s6\n" \
"s_addc_u32 s9, s27, 0\n" \
M_"v[164:167], a[240:243], a[220:223], v[164:167]\n" \
R_"a[156:159], v28 offset:14336\n" \
R_"a[220:223], v29 offset:14336\n" \
"s_waitcnt lgkmcnt(15)\n" \
M_"a[0:3], a[160:163], a[128:131], a[0:3]\n" \
"s_add_u32 m0, s30, 0x21000\n" \
"s_addk_i32 s84, 0x3000\n" \
M_"a[0:3], a[224:227], a[192:195], a[0:3]\n" \
B_"v24, s[76:79], s84 offen offset:0 lds\n" \
M_"a[4:7], a[164:167], a[128:131], a[4:7]\n" \
X_"a[176:177], v36 offset:0\n" \
M_"a[4:7], a[228:231], a[192:195], a[4:7]\n" \
X_"a[178:179], v36 offset:1024\n" \
M_"a[12:15], a[172:175], a[128:131], a[12:15]\n" \
X_"a[240:241], v36 offset:8192\n" \
"s_waitcnt lgkmcnt(15)\n" \
M_"a[12:15], a[236:239], a[192:195], a[12:15]\n" \
"s_add_u32 s88, s42, 0x1000\n" \
"buffer_store_dwordx4 v[188:191], v20, s[80:83], s88 offen offset:1024 sc1\n" \
"s_waitcnt lgkmcnt(13)\n" \
M_"a[8:11], a[128:131], a[168:171], a[8:11]\n" \
X_"a[242:243], v36 offset:9216\n" \
"s_waitcnt lgkmcnt(12)\n" \
M_"a[8:11], a[192:195], a[232:235], a[8:11]\n" \
X_"a[180:181], v37 offset:0\n" \
M_"a[24:27], a[132:135], a[168:171], a[24:27]\n" \
X_"a[182:183], v37 offset:1024\n" \
M_"a[24:27], a[196:199], a[232:235], a[24:27]\n" \
X_"a[244:245], v37 offset:8192\n" \
M_"a[28:31], a[172:175], a[132:135], a[28:31]\n" \
"s_add_u32 s8, s6, 0x1000000\n" \
"s_addc_u32 s6, s9, 0\n" \
M_"a[28:31], a[236:239], a[196:199], a[28:31]\n" \
"s_addk_i32 s84, 0x2000\n" \
M_"a[20:23], a[164:167], a[132:135], a[20:23]\n" \
B_"v25, s[76:79], s84 offen offset:1024 lds\n" \
M_"a[20:23], a[228:231], a[196:199], a[20:23]\n" \
X_"a[246:247], v37 offset:9216\n" \
"s_and_b32 s9, s6, 0xffff\n" \
M_"a[16:19], a[160:163], a[132:135], a[16:19]\n" \
X_"a[184:185], v38 offset:0\n" \
"s_lshl_b32 s6, s5, 16\n" \
M_"a[16:19], a[224:227], a[196:199], a[16:19]\n" \
X_"a[186:187], v38 offset:1024\n" \
"s_sub_i32 s10, 0x100000, s6\n" \
M_"a[32:35], a[160:163], a[136:139], a[32:35]\n" \
"s_addk_i32 s84, 0x2000\n" \
"s_nop 0\n" \
M_"a[32:35], a[224:227], a[200:203], a[32:35]\n" \
B_"v24, s[76:79], s84 offen offset:2048 lds\n" \
M_"a[36:39], a[164:167], a[136:139], a[36:39]\n" \
X_"a[248:249], v38 offset:8192\n" \
"s_mov_b32 s6, 0x8000\n" \
M_"a[36:39], a[228:231], a[200:203], a[36:39]\n" \
X_"a[250:251], v38 offset:9216\n" \
M_"a[44:47], a[172:175], a[136:139], a[44:47]\n" \
X_"a[188:189], v39 offset:0\n" \
M_"a[44:47], a[236:239], a[200:203], a[44:47]\n" \
X_"a[190:191], v39 offset:1024\n" \
M_"a[40:43], a[136:139], a[168:171], a[40:43]\n" \
"s_add_u32 s89, s42, 0x2000\n" \
"buffer_store_dwordx4 v[192:195], v20, s[80:83], s89 offen offset:0 sc1\n" \
M_"a[40:43], a[200:203], a[232:235], a[40:43]\n" \
X_"a[252:253], v39 offset:8192\n" \
M_"a[56:59], a[140:143], a[168:171], a[56:59]\n" \
X_"a[254:255], v39 offset:9216\n" \
M_"a[56:59], a[204:207], a[232:235], a[56:59]\n" \
"s_cmp_lg_u32 s5, 0\n" \
"s_cselect_b32 s17, s11, s6\n" \
M_"a[60:63], a[172:175], a[140:143], a[60:63]\n" \
"s_lshl_b32 s6, s3, 1\n" \
"s_lshl_b32 s5, s5, 3\n" \
M_"a[60:63], a[236:239], a[204:207], a[60:63]\n" \
".fill (L_pb + 32480 - .) / 4, 4, 0xBF800000\n" \
"L_pp8:\n" \
"s_cbranch_execz L_pret\n" \
"s_addk_i32 s84, 0x2000\n" \
M_"a[52:55], a[164:167], a[140:143], a[52:55]\n" \
B_"v25, s[76:79], s84 offen offset:3072 lds\n" \
M_"a[52:55], a[228:231], a[204:207], a[52:55]\n" \
"s_and_b32 s6, s6, 4\n" \
"s_or_b32 s12, s5, s6\n" \
M_"a[48:51], a[160:163], a[140:143], a[48:51]\n" \
"s_sub_i32 s5, s13, s5\n" \
"s_or_b32 s5, s5, s6\n" \
M_"a[48:51], a[224:227], a[204:207], a[48:51]\n" \
"s_or_b32 s6, s12, s13\n" \
"s_lshl_b32 s4, s4, 2\n" \
"s_waitcnt lgkmcnt(15)\n" \
M_"a[64:67], a[164:167], v[248:251], a[64:67]\n" \
"s_mulk_i32 s6, 0x92\n" \
"s_add_i32 s4, s4, 18\n" \
M_"a[64:67], a[228:231], v[48:51], a[64:67]\n" \
"s_mulk_i32 s5, 0x92\n" \
"s_add_i32 s6, s4, s6\n" \
M_"a[80:83], a[172:175], v[248:251], a[80:83]\n" \
"s_add_i32 s5, s5, s4\n" \
"s_lshl_b32 s4, s3, 11\n" \
M_"a[80:83], a[236:239], v[48:51], a[80:83]\n" \
"s_lshl_b32 s15, s13, 12\n" \
"s_lshl_b32 s16, s19, 14\n" \
M_"a[68:71], v[248:251], a[168:171], a[68:71]\n" \
"s_mul_i32 s19, s19, 0xe000\n" \
"s_mov_b32 s11, 0x20000\n" \
"s_waitcnt vmcnt(10) lgkmcnt(0)\n" \
M_"a[68:71], v[48:51], a[232:235], a[68:71]\n" \
"s_barrier\n" \
M_"v[52:55], a[176:179], a[128:131], v[52:55]\n" \
"s_add_u32 m0, s40, 0x0\n" \
"s_add_u32 s85, s39, 0x10000\n" \
M_"v[52:55], a[240:243], a[192:195], v[52:55]\n" \
B_"v20, s[80:83], s85 offen sc1 lds\n" \
M_"v[56:59], a[180:183], a[128:131], v[56:59]\n" \
X_"a[160:161], v32 offset:32768\n" \
"s_or_b32 s12, s4, 0xc000\n" \
M_"v[56:59], a[244:247], a[192:195], v[56:59]\n" \
X_"a[162:163], v32 offset:33280\n" \
"s_lshl_b32 s4, s3, 8\n" \
M_"v[60:63], a[184:187], a[128:131], v[60:63]\n" \
"s_or_b32 s22, s15, s16\n" \
"s_or_b32 s23, s19, s15\n" \
M_"v[60:63], a[248:251], a[192:195], v[60:63]\n" \
X_"a[224:225], v32 offset:36864\n" \
"s_lshl_b32 s60, s6, 10\n" \
M_"v[64:67], a[188:191], a[128:131], v[64:67]\n" \
"s_lshl_b32 s61, s5, 10\n" \
"s_lshl_b32 s14, s14, 15\n" \
M_"v[64:67], a[252:255], a[192:195], v[64:67]\n" \
R_"a[128:131], v28 offset:32768\n" \
"s_or_b32 s20, s4, 0x26c00\n" \
M_"v[80:83], a[188:191], a[132:135], v[80:83]\n" \
R_"a[192:195], v29 offset:32768\n" \
"s_or_b32 s13, s22, 0x8000\n" \
M_"v[80:83], a[252:255], a[196:199], v[80:83]\n" \
"s_add_u32 m0, s40, 0x1000\n" \
"s_add_u32 s85, s39, 0x11000\n" \
M_"v[76:79], a[184:187], a[132:135], v[76:79]\n" \
B_"v20, s[80:83], s85 offen sc1 lds\n" \
M_"v[76:79], a[248:251], a[196:199], v[76:79]\n" \
X_"a[226:227], v32 offset:37376\n" \
"s_add_i32 s22, s22, 0x14000\n" \
M_"v[72:75], a[180:183], a[132:135], v[72:75]\n" \
X_"a[164:165], v33 offset:32768\n" \
"s_bitset1_b32 s23, 16\n" \
M_"v[72:75], a[244:247], a[196:199], v[72:75]\n" \
"s_or_b32 s24, s7, 0x5be8020\n" \
"s_and_b32 s57, s27, 0xffff\n" \
M_"v[68:71], a[176:179], a[132:135], v[68:71]\n" \
X_"a[166:167], v33 offset:33280\n" \
"s_mov_b32 s58, 0x6000000\n" \
M_"v[68:71], a[240:243], a[196:199], v[68:71]\n" \
R_"a[132:135], v28 offset:34816\n" \
"s_mov_b32 s56, s26\n" \
M_"v[84:87], a[176:179], a[136:139], v[84:87]\n" \
R_"a[196:199], v29 offset:34816\n" \
"s_mov_b32 s59, s11\n" \
M_"v[84:87], a[240:243], a[200:203], v[84:87]\n" \
"s_add_u32 m0, s40, 0x3000\n" \
"s_movk_i32 s84, 0x400\n" \
M_"v[88:91], a[180:183], a[136:139], v[88:91]\n" \
B_"v21, s[76:79], s84 offen offset:0 lds\n" \
M_"v[88:91], a[244:247], a[200:203], v[88:91]\n" \
X_"a[228:229], v33 offset:36864\n" \
"s_add_i32 s60, s60, 0x3000000\n" \
M_"v[92:95], a[184:187], a[136:139], v[92:95]\n" \
X_"a[230:231], v33 offset:37376\n" \
"s_add_i32 s61, s61, 0x411c000\n" \
M_"v[92:95], a[248:251], a[200:203], v[92:95]\n" \
"s_add_u32 s88, s42, 0x2000\n" \
"buffer_store_dwordx4 v[216:219], v20, s[80:83], s88 offen offset:1024 sc1\n" \
M_"v[96:99], a[188:191], a[136:139], v[96:99]\n" \
"s_sub_i32 s19, 14, s18\n" \
"s_nop 0\n" \
"s_mov_b32 s21, 0x71800000\n" \
M_"v[96:99], a[252:255], a[200:203], v[96:99]\n" \
R_"a[136:139], v28 offset:36864\n" \
"s_mov_b32 s4, L_gsw-L_gtr\n" \
M_"v[112:115], a[188:191], a[140:143], v[112:115]\n" \
R_"a[200:203], v29 offset:36864\n" \
M_"v[112:115], a[252:255], a[204:207], v[112:115]\n" \
X_"a[172:173], v35 offset:32768\n" \
M_"v[108:111], a[184:187], a[140:143], v[108:111]\n" \
X_"a[174:175], v35 offset:33280\n" \
M_"v[108:111], a[248:251], a[204:207], v[108:111]\n" \
"s_cmp_eq_u32 s19, 0\n" \
"s_cselect_b32 s4, L_gs7-L_gtr, s4\n" \
M_"v[104:107], a[180:183], a[140:143], v[104:107]\n" \
"s_add_u32 m0, s31, 0x0\n" \
"s_mov_b32 s84, 0x400000\n" \
M_"v[104:107], a[244:247], a[204:207], v[104:107]\n" \
B_"v26, s[68:71], s84 offen offset:0 lds\n" \
M_"v[100:103], a[176:179], a[140:143], v[100:103]\n" \
X_"a[236:237], v35 offset:36864\n" \
M_"v[100:103], a[240:243], a[204:207], v[100:103]\n" \
R_"a[140:143], v28 offset:38912\n" \
M_"v[116:119], a[176:179], a[144:147], v[116:119]\n" \
R_"a[204:207], v29 offset:38912\n" \
M_"v[116:119], a[240:243], a[208:211], v[116:119]\n" \
X_"a[238:239], v35 offset:37376\n" \
M_"v[120:123], a[180:183], a[144:147], v[120:123]\n" \
X_"a[168:169], v34 offset:32768\n" \
M_"v[120:123], a[244:247], a[208:211], v[120:123]\n" \
"s_addk_i32 s84, 0x7c00\n" \
M_"v[124:127], a[184:187], a[144:147], v[124:127]\n" \
B_"v26, s[68:71], s84 offen offset:1024 lds\n" \
M_"v[124:127], a[248:251], a[208:211], v[124:127]\n" \
X_"a[170:171], v34 offset:33280\n" \
M_"v[128:131], a[188:191], a[144:147], v[128:131]\n" \
"s_cmp_eq_u32 s16, 0\n" \
"s_cselect_b32 s4, 0, s4\n" \
M_"v[128:131], a[252:255], a[208:211], v[128:131]\n" \
R_"a[144:147], v28 offset:40960\n" \
M_"v[144:147], a[188:191], a[148:151], v[144:147]\n" \
R_"a[208:211], v29 offset:40960\n" \
M_"v[144:147], a[252:255], a[212:215], v[144:147]\n" \
X_"a[232:233], v34 offset:36864\n" \
M_"v[140:143], a[184:187], a[148:151], v[140:143]\n" \
"s_addk_i32 s84, 0x7c00\n" \
M_"v[140:143], a[248:251], a[212:215], v[140:143]\n" \
B_"v27, s[68:71], s84 offen offset:2048 lds\n" \
M_"v[136:139], a[180:183], a[148:151], v[136:139]\n" \
X_"a[234:235], v34 offset:37376\n" \
M_"v[136:139], a[244:247], a[212:215], v[136:139]\n" \
"s_getpc_b64 s[28:29]\n" \
"L_gp0_%=:\n" \
"s_add_u32 s28, s28, L_gtr-L_gp0_%=\n" \
"s_addc_u32 s29, s29, 0\n" \
M_"v[132:135], a[176:179], a[148:151], v[132:135]\n" \
R_"v[248:251], v22 offset:32768\n" \
M_"v[132:135], a[240:243], a[212:215], v[132:135]\n" \
R_"a[148:151], v28 offset:43008\n" \
M_"v[148:151], a[176:179], a[152:155], v[148:151]\n" \
R_"a[212:215], v29 offset:43008\n" \
M_"v[148:151], a[240:243], a[216:219], v[148:151]\n" \
"s_add_u32 m0, s31, 0x0\n" \
"s_mov_b32 s84, 0x417400\n" \
M_"v[152:155], a[180:183], a[152:155], v[152:155]\n" \
B_"v27, s[68:71], s84 offen offset:3072 lds\n" \
M_"v[152:155], a[244:247], a[216:219], v[152:155]\n" \
R_"v[48:51], v23 offset:32768\n" \
M_"v[156:159], a[184:187], a[152:155], v[156:159]\n" \
"s_add_u32 s28, s28, s4\n" \
"s_addc_u32 s29, s29, 0\n" \
"s_nop 0\n" \
M_"v[156:159], a[248:251], a[216:219], v[156:159]\n" \
"v_accvgpr_write_b32 a96, 0\n" \
"v_accvgpr_write_b32 a97, 0\n" \
M_"v[160:163], a[188:191], a[152:155], v[160:163]\n" \
"v_accvgpr_write_b32 a98, 0\n" \
"v_accvgpr_write_b32 a99, 0\n" \
M_"v[160:163], a[252:255], a[216:219], v[160:163]\n" \
R_"a[152:155], v28 offset:45056\n" \
"v_accvgpr_write_b32 a100, 0\n" \
M_"v[176:179], a[188:191], a[156:159], v[176:179]\n" \
R_"a[216:219], v29 offset:45056\n" \
"v_accvgpr_write_b32 a101, 0\n" \
M_"v[176:179], a[252:255], a[220:223], v[176:179]\n" \
"v_accvgpr_write_b32 a102, 0\n" \
"v_accvgpr_write_b32 a103, 0\n" \
M_"v[172:175], a[184:187], a[156:159], v[172:175]\n" \
"v_accvgpr_write_b32 a112, 0\n" \
"v_accvgpr_write_b32 a113, 0\n" \
M_"v[172:175], a[248:251], a[220:223], v[172:175]\n" \
"v_accvgpr_write_b32 a114, 0\n" \
"v_accvgpr_write_b32 a115, 0\n" \
M_"v[168:171], a[180:183], a[156:159], v[168:171]\n" \
"v_accvgpr_write_b32 a120, 0\n" \
"v_accvgpr_write_b32 a121, 0\n" \
M_"v[168:171], a[244:247], a[220:223], v[168:171]\n" \
"v_accvgpr_write_b32 a122, 0\n" \
"v_accvgpr_write_b32 a123, 0\n" \
M_"v[164:167], a[176:179], a[156:159], v[164:167]\n" \
"v_mov_b32_e32 v180, 0x3f803f80\n" \
"s_mov_b32 s6, 0x33333333\n" \
M_"v[164:167], a[240:243], a[220:223], v[164:167]\n" \
R_"a[156:159], v28 offset:47104\n" \
R_"a[220:223], v29 offset:47104\n" \
"s_waitcnt lgkmcnt(15)\n" \
"s_nop 0\n" \
M_"a[0:3], a[160:163], a[128:131], a[0:3]\n" \
"s_add_u32 s89, s42, 0x3000\n" \
"buffer_store_dwordx4 v[220:223], v20, s[80:83], s89 offen offset:0 sc1\n" \
M_"a[0:3], a[224:227], a[192:195], a[0:3]\n" \
X_"a[176:177], v36 offset:32768\n" \
"s_mov_b32 s7, 0xcccccccc\n" \
M_"a[4:7], a[164:167], a[128:131], a[4:7]\n" \
X_"a[178:179], v36 offset:33792\n" \
"v_cndmask_b32_e64 v181, 0, v180, s[6:7]\n" \
M_"a[4:7], a[228:231], a[192:195], a[4:7]\n" \
X_"a[240:241], v36 offset:40960\n" \
"v_accvgpr_write_b32 a104, v181\n" \
M_"a[12:15], a[172:175], a[128:131], a[12:15]\n" \
X_"a[242:243], v36 offset:41984\n" \
"v_accvgpr_write_b32 a105, v181\n" \
"s_waitcnt lgkmcnt(15)\n" \
M_"a[12:15], a[236:239], a[192:195], a[12:15]\n" \
"v_accvgpr_write_b32 a106, v181\n" \
"v_accvgpr_write_b32 a107, v181\n" \
M_"a[8:11], a[128:131], a[168:171], a[8:11]\n" \
X_"a[180:181], v37 offset:32768\n" \
"s_mov_b32 s6, 0x11111111\n" \
"s_waitcnt lgkmcnt(13)\n" \
M_"a[8:11], a[192:195], a[232:235], a[8:11]\n" \
"s_mov_b32 s7, 0x11111111\n" \
"v_cndmask_b32_e64 v181, 0, v180, s[6:7]\n" \
M_"a[24:27], a[132:135], a[168:171], a[24:27]\n" \
X_"a[182:183], v37 offset:33792\n" \
"v_accvgpr_write_b32 a108, v181\n" \
M_"a[24:27], a[196:199], a[232:235], a[24:27]\n" \
X_"a[244:245], v37 offset:40960\n" \
"v_accvgpr_write_b32 a109, v181\n" \
M_"a[28:31], a[172:175], a[132:135], a[28:31]\n" \
"v_accvgpr_write_b32 a110, v181\n" \
"v_accvgpr_write_b32 a111, v181\n" \
M_"a[28:31], a[236:239], a[196:199], a[28:31]\n" \
X_"a[246:247], v37 offset:41984\n" \
"s_mov_b32 s6, 0x44444444\n" \
M_"a[20:23], a[164:167], a[132:135], a[20:23]\n" \
"s_mov_b32 s7, 0x44444444\n" \
"v_cndmask_b32_e64 v181, 0, v180, s[6:7]\n" \
M_"a[20:23], a[228:231], a[196:199], a[20:23]\n" \
X_"a[184:185], v38 offset:32768\n" \
"v_accvgpr_write_b32 a116, v181\n" \
M_"a[16:19], a[160:163], a[132:135], a[16:19]\n" \
X_"a[186:187], v38 offset:33792\n" \
"v_accvgpr_write_b32 a117, v181\n" \
M_"a[16:19], a[224:227], a[196:199], a[16:19]\n" \
"v_accvgpr_write_b32 a118, v181\n" \
"v_accvgpr_write_b32 a119, v181\n" \
M_"a[32:35], a[160:163], a[136:139], a[32:35]\n" \
"s_add_u32 s88, s42, 0x3000\n" \
"buffer_store_dwordx4 v[224:227], v20, s[80:83], s88 offen offset:1024 sc1\n" \
M_"a[32:35], a[224:227], a[200:203], a[32:35]\n" \
X_"a[248:249], v38 offset:40960\n" \
"v_bfe_i32 v181, v20, 4, 1\n" \
M_"a[36:39], a[164:167], a[136:139], a[36:39]\n" \
"v_lshlrev_b32_e32 v45, 3, v20\n" \
"v_add_u32_e32 v45, s30, v45\n" \
M_"a[36:39], a[228:231], a[200:203], a[36:39]\n" \
"s_getpc_b64 s[86:87]\n" \
"L_ct0_%=:\n" \
"s_add_u32 s86, s86, L_ap0-L_ct0_%=\n" \
"s_addc_u32 s87, s87, 0\n" \
"s_add_u32 m0, s40, 0x2000\n" \
M_"a[44:47], a[172:175], a[136:139], a[44:47]\n" \
"global_load_lds_dword v45, s[86:87]\n" \
M_"a[44:47], a[236:239], a[200:203], a[44:47]\n" \
X_"a[250:251], v38 offset:41984\n" \
"v_and_b32_e32 v181, 0xaaaaaaaa, v181\n" \
M_"a[40:43], a[136:139], a[168:171], a[40:43]\n" \
X_"a[188:189], v39 offset:32768\n" \
"v_or_b32_e32 v181, 0x44444444, v181\n" \
M_"a[40:43], a[200:203], a[232:235], a[40:43]\n" \
X_"a[190:191], v39 offset:33792\n" \
"v_accvgpr_write_b32 a126, v181\n" \
M_"a[56:59], a[140:143], a[168:171], a[56:59]\n" \
X_"a[252:253], v39 offset:40960\n" \
"v_bfe_u32 v180, v20, 5, 3\n" \
M_"a[56:59], a[204:207], a[232:235], a[56:59]\n" \
X_"a[254:255], v39 offset:41984\n" \
"v_bfe_u32 v181, v20, 8, 2\n" \
M_"a[60:63], a[172:175], a[140:143], a[60:63]\n" \
"v_lshlrev_b32_e32 v181, 1, v181\n" \
"v_sub_u32_e32 v180, v180, v181\n" \
M_"a[60:63], a[236:239], a[204:207], a[60:63]\n" \
"v_and_b32_e64 v181, 16, v20\n" \
"v_mov_b32_e32 v182, 0x3f80\n" \
M_"a[52:55], a[164:167], a[140:143], a[52:55]\n" \
"v_lshlrev_b32_e32 v181, v181, v182\n" \
"v_cmp_eq_u32_e32 vcc, 0, v180\n" \
M_"a[52:55], a[228:231], a[204:207], a[52:55]\n" \
"v_cmp_eq_u32_e64 s[6:7], 1, v180\n" \
"v_cndmask_b32_e64 v182, 0, v181, vcc\n" \
M_"a[48:51], a[160:163], a[140:143], a[48:51]\n" \
"v_cndmask_b32_e64 v183, 0, v181, s[6:7]\n" \
"v_accvgpr_write_b32 a124, v182\n" \
M_"a[48:51], a[224:227], a[204:207], a[48:51]\n" \
"v_accvgpr_write_b32 a125, v183\n" \
"s_waitcnt lgkmcnt(15)\n" \
M_"a[64:67], a[164:167], v[248:251], a[64:67]\n" \
M_"a[64:67], a[228:231], v[48:51], a[64:67]\n" \
M_"a[80:83], a[172:175], v[248:251], a[80:83]\n" \
M_"a[80:83], a[236:239], v[48:51], a[80:83]\n" \
M_"a[68:71], v[248:251], a[168:171], a[68:71]\n" \
"s_waitcnt vmcnt(3) lgkmcnt(0)\n" \
M_"a[68:71], v[48:51], a[232:235], a[68:71]\n" \
"s_barrier\n" \
"v_xor_b32_e32 v28, 0x20000, v28\n" \
"v_xor_b32_e32 v29, 0x20000, v29\n" \
M_"v[52:55], a[176:179], a[128:131], v[52:55]\n" \
X_"a[160:161], v32 offset:0\n" \
"v_accvgpr_read_b32 v184, a0\n" \
M_"v[52:55], a[240:243], a[192:195], v[52:55]\n" \
"v_accvgpr_read_b32 v185, a1\n" \
"v_accvgpr_read_b32 v186, a2\n" \
M_"v[56:59], a[180:183], a[128:131], v[56:59]\n" \
X_"a[162:163], v32 offset:512\n" \
"v_accvgpr_read_b32 v187, a3\n" \
M_"v[56:59], a[244:247], a[192:195], v[56:59]\n" \
"v_accvgpr_read_b32 v188, a4\n" \
"v_accvgpr_read_b32 v189, a5\n" \
M_"v[60:63], a[184:187], a[128:131], v[60:63]\n" \
"v_accvgpr_read_b32 v190, a6\n" \
"v_accvgpr_read_b32 v191, a7\n" \
M_"v[60:63], a[248:251], a[192:195], v[60:63]\n" \
X_"a[224:225], v32 offset:4096\n" \
"v_accvgpr_read_b32 v196, a12\n" \
M_"v[64:67], a[188:191], a[128:131], v[64:67]\n" \
"v_accvgpr_read_b32 v197, a13\n" \
"v_accvgpr_read_b32 v198, a14\n" \
M_"v[64:67], a[252:255], a[192:195], v[64:67]\n" \
R_"a[128:131], v28 offset:0\n" \
"v_accvgpr_read_b32 v199, a15\n" \
M_"v[80:83], a[188:191], a[132:135], v[80:83]\n" \
R_"a[192:195], v29 offset:0\n" \
"v_accvgpr_read_b32 v192, a8\n" \
M_"v[80:83], a[252:255], a[196:199], v[80:83]\n" \
X_"a[226:227], v32 offset:4608\n" \
"v_accvgpr_read_b32 v193, a9\n" \
M_"v[76:79], a[184:187], a[132:135], v[76:79]\n" \
"v_accvgpr_read_b32 v194, a10\n" \
"v_accvgpr_read_b32 v195, a11\n" \
M_"v[76:79], a[248:251], a[196:199], v[76:79]\n" \
X_"a[164:165], v33 offset:0\n" \
"v_accvgpr_read_b32 v208, a24\n" \
M_"v[72:75], a[180:183], a[132:135], v[72:75]\n" \
"v_accvgpr_read_b32 v209, a25\n" \
"v_accvgpr_read_b32 v210, a26\n" \
M_"v[72:75], a[244:247], a[196:199], v[72:75]\n" \
"v_accvgpr_read_b32 v211, a27\n" \
"v_accvgpr_read_b32 v212, a28\n" \
M_"v[68:71], a[176:179], a[132:135], v[68:71]\n" \
X_"a[166:167], v33 offset:512\n" \
"v_accvgpr_read_b32 v213, a29\n" \
M_"v[68:71], a[240:243], a[196:199], v[68:71]\n" \
R_"a[132:135], v28 offset:2048\n" \
"v_accvgpr_read_b32 v214, a30\n" \
M_"v[84:87], a[176:179], a[136:139], v[84:87]\n" \
R_"a[196:199], v29 offset:2048\n" \
"v_accvgpr_read_b32 v215, a31\n" \
M_"v[84:87], a[240:243], a[200:203], v[84:87]\n" \
X_"a[228:229], v33 offset:4096\n" \
"v_accvgpr_read_b32 v204, a20\n" \
M_"v[88:91], a[180:183], a[136:139], v[88:91]\n" \
"v_accvgpr_read_b32 v205, a21\n" \
"v_accvgpr_read_b32 v206, a22\n" \
M_"v[88:91], a[244:247], a[200:203], v[88:91]\n" \
"v_accvgpr_read_b32 v207, a23\n" \
"v_accvgpr_read_b32 v200, a16\n" \
M_"v[92:95], a[184:187], a[136:139], v[92:95]\n" \
X_"a[230:231], v33 offset:4608\n" \
"v_accvgpr_read_b32 v201, a17\n" \
M_"v[92:95], a[248:251], a[200:203], v[92:95]\n" \
"v_accvgpr_read_b32 v202, a18\n" \
"v_accvgpr_read_b32 v203, a19\n" \
M_"v[96:99], a[188:191], a[136:139], v[96:99]\n" \
"v_accvgpr_read_b32 v216, a32\n" \
"v_accvgpr_read_b32 v217, a33\n" \
M_"v[96:99], a[252:255], a[200:203], v[96:99]\n" \
R_"a[136:139], v28 offset:4096\n" \
"v_accvgpr_read_b32 v218, a34\n" \
M_"v[112:115], a[188:191], a[140:143], v[112:115]\n" \
R_"a[200:203], v29 offset:4096\n" \
"v_accvgpr_read_b32 v219, a35\n" \
M_"v[112:115], a[252:255], a[204:207], v[112:115]\n" \
X_"a[172:173], v35 offset:0\n" \
"v_accvgpr_read_b32 v220, a36\n" \
M_"v[108:111], a[184:187], a[140:143], v[108:111]\n" \
X_"a[174:175], v35 offset:512\n" \
"v_accvgpr_read_b32 v221, a37\n" \
M_"v[108:111], a[248:251], a[204:207], v[108:111]\n" \
"v_accvgpr_read_b32 v222, a38\n" \
"v_accvgpr_read_b32 v223, a39\n" \
M_"v[104:107], a[180:183], a[140:143], v[104:107]\n" \
X_"a[236:237], v35 offset:4096\n" \
"v_accvgpr_read_b32 v228, a44\n" \
M_"v[104:107], a[244:247], a[204:207], v[104:107]\n" \
"v_accvgpr_read_b32 v229, a45\n" \
"v_accvgpr_read_b32 v230, a46\n" \
M_"v[100:103], a[176:179], a[140:143], v[100:103]\n" \
"v_accvgpr_read_b32 v231, a47\n" \
"v_accvgpr_read_b32 v224, a40\n" \
M_"v[100:103], a[240:243], a[204:207], v[100:103]\n" \
R_"a[140:143], v28 offset:6144\n" \
"v_accvgpr_read_b32 v225, a41\n" \
M_"v[116:119], a[176:179], a[144:147], v[116:119]\n" \
R_"a[204:207], v29 offset:6144\n" \
"v_accvgpr_read_b32 v226, a42\n" \
M_"v[116:119], a[240:243], a[208:211], v[116:119]\n" \
X_"a[238:239], v35 offset:4608\n" \
"v_accvgpr_read_b32 v227, a43\n" \
M_"v[120:123], a[180:183], a[144:147], v[120:123]\n" \
X_"a[168:169], v34 offset:0\n" \
"v_accvgpr_read_b32 v240, a56\n" \
M_"v[120:123], a[244:247], a[208:211], v[120:123]\n" \
"v_accvgpr_read_b32 v241, a57\n" \
"v_accvgpr_read_b32 v242, a58\n" \
M_"v[124:127], a[184:187], a[144:147], v[124:127]\n" \
"v_accvgpr_read_b32 v243, a59\n" \
"v_accvgpr_read_b32 v244, a60\n" \
M_"v[124:127], a[248:251], a[208:211], v[124:127]\n" \
X_"a[170:171], v34 offset:512\n" \
"v_accvgpr_read_b32 v245, a61\n" \
M_"v[128:131], a[188:191], a[144:147], v[128:131]\n" \
"v_accvgpr_read_b32 v246, a62\n" \
"v_accvgpr_read_b32 v247, a63\n" \
M_"v[128:131], a[252:255], a[208:211], v[128:131]\n" \
R_"a[144:147], v28 offset:8192\n" \
"v_accvgpr_read_b32 v236, a52\n" \
M_"v[144:147], a[188:191], a[148:151], v[144:147]\n" \
R_"a[208:211], v29 offset:8192\n" \
"v_accvgpr_read_b32 v237, a53\n" \
M_"v[144:147], a[252:255], a[212:215], v[144:147]\n" \
X_"a[232:233], v34 offset:4096\n" \
"v_accvgpr_read_b32 v238, a54\n" \
M_"v[140:143], a[184:187], a[148:151], v[140:143]\n" \
"v_accvgpr_read_b32 v239, a55\n" \
"v_accvgpr_read_b32 v232, a48\n" \
M_"v[140:143], a[248:251], a[212:215], v[140:143]\n" \
X_"a[234:235], v34 offset:4608\n" \
"v_accvgpr_read_b32 v233, a49\n" \
M_"v[136:139], a[180:183], a[148:151], v[136:139]\n" \
"v_accvgpr_read_b32 v234, a50\n" \
"v_accvgpr_read_b32 v235, a51\n" \
M_"v[136:139], a[244:247], a[212:215], v[136:139]\n" \
"v_accvgpr_read_b32 v180, a64\n" \
"v_accvgpr_read_b32 v181, a65\n" \
M_"v[132:135], a[176:179], a[148:151], v[132:135]\n" \
R_"v[248:251], v22 offset:0\n" \
"v_accvgpr_read_b32 v182, a66\n" \
M_"v[132:135], a[240:243], a[212:215], v[132:135]\n" \
R_"a[148:151], v28 offset:10240\n" \
"v_accvgpr_read_b32 v183, a67\n" \
M_"v[148:151], a[176:179], a[152:155], v[148:151]\n" \
R_"a[212:215], v29 offset:10240\n" \
"v_accvgpr_read_b32 v252, a68\n" \
M_"v[148:151], a[240:243], a[216:219], v[148:151]\n" \
R_"v[48:51], v23 offset:0\n" \
"v_accvgpr_read_b32 v253, a69\n" \
M_"v[152:155], a[180:183], a[152:155], v[152:155]\n" \
"v_accvgpr_read_b32 v254, a70\n" \
"v_accvgpr_read_b32 v255, a71\n" \
M_"v[152:155], a[244:247], a[216:219], v[152:155]\n" \
"v_lshrrev_b32_e32 v24, 9, v20\n" \
"v_and_b32_e32 v26, 0x1ff, v20\n" \
M_"v[156:159], a[184:187], a[152:155], v[156:159]\n" \
"v_lshl_add_u32 v24, v24, 3, v26\n" \
"v_lshrrev_b32_e64 v25, 8, v20\n" \
M_"v[156:159], a[248:251], a[216:219], v[156:159]\n" \
"v_mul_u32_u24_e32 v25, 0x24700, v25\n" \
"v_add_u32_e64 v25, v25, v20\n" \
M_"v[160:163], a[188:191], a[152:155], v[160:163]\n" \
"v_lshrrev_b32_e32 v26, 2, v20\n" \
"v_add_u32_e32 v26, s45, v26\n" \
M_"v[160:163], a[252:255], a[216:219], v[160:163]\n" \
R_"a[152:155], v28 offset:12288\n" \
"v_add_u32_e64 v27, s46, v20\n" \
M_"v[176:179], a[188:191], a[156:159], v[176:179]\n" \
R_"a[216:219], v29 offset:12288\n" \
"v_add_u32_e64 v21, s43, v27\n" \
M_"v[176:179], a[252:255], a[220:223], v[176:179]\n" \
M_"v[172:175], a[184:187], a[156:159], v[172:175]\n" \
M_"v[172:175], a[248:251], a[220:223], v[172:175]\n" \
M_"v[168:171], a[180:183], a[156:159], v[168:171]\n" \
M_"v[168:171], a[244:247], a[220:223], v[168:171]\n" \
M_"v[164:167], a[176:179], a[156:159], v[164:167]\n" \
M_"v[164:167], a[240:243], a[220:223], v[164:167]\n" \
R_"a[156:159], v28 offset:14336\n" \
R_"a[220:223], v29 offset:14336\n" \
"s_waitcnt lgkmcnt(15)\n" \
"s_nop 0\n" \
M_"v[184:187], a[160:163], a[128:131], v[184:187]\n" \
"v_bfe_u32 v44, v20, 2, 5\n" \
X_"a[176:177], v36 offset:0\n" \
M_"v[184:187], a[224:227], a[192:195], v[184:187]\n" \
"buffer_load_dword v44, v44, s[80:83], s38 offen offset:0 sc1\n" \
M_"v[188:191], a[164:167], a[128:131], v[188:191]\n" \
X_"a[178:179], v36 offset:1024\n" \
M_"v[188:191], a[228:231], a[192:195], v[188:191]\n" \
X_"a[240:241], v36 offset:8192\n" \
M_"v[196:199], a[172:175], a[128:131], v[196:199]\n" \
X_"a[242:243], v36 offset:9216\n" \
"s_waitcnt lgkmcnt(15)\n" \
M_"v[196:199], a[236:239], a[192:195], v[196:199]\n" \
M_"v[192:195], a[128:131], a[168:171], v[192:195]\n" \
X_"a[180:181], v37 offset:0\n" \
"s_waitcnt lgkmcnt(13)\n" \
M_"v[192:195], a[192:195], a[232:235], v[192:195]\n" \
M_"v[208:211], a[132:135], a[168:171], v[208:211]\n" \
X_"a[182:183], v37 offset:1024\n" \
M_"v[208:211], a[196:199], a[232:235], v[208:211]\n" \
X_"a[244:245], v37 offset:8192\n" \
M_"v[212:215], a[172:175], a[132:135], v[212:215]\n" \
M_"v[212:215], a[236:239], a[196:199], v[212:215]\n" \
X_"a[246:247], v37 offset:9216\n" \
C_"v188, v188, v189\n" \
M_"v[204:207], a[164:167], a[132:135], v[204:207]\n" \
C_"v189, v190, v191\n" \
C_"v190, v198, v199\n" \
M_"v[204:207], a[228:231], a[196:199], v[204:207]\n" \
X_"a[184:185], v38 offset:0\n" \
"v_mov_b32_e32 v191, 0x3f80\n" \
M_"v[200:203], a[160:163], a[132:135], v[200:203]\n" \
X_"a[186:187], v38 offset:1024\n" \
C_"v184, v184, v185\n" \
M_"v[200:203], a[224:227], a[196:199], v[200:203]\n" \
C_"v185, v186, v187\n" \
C_"v186, v196, v197\n" \
M_"v[216:219], a[160:163], a[136:139], v[216:219]\n" \
X_"a[248:249], v38 offset:8192\n" \
"v_mov_b32_e64 v187, 0\n" \
M_"v[216:219], a[224:227], a[200:203], v[216:219]\n" \
X_"a[250:251], v38 offset:9216\n" \
M_"v[220:223], a[164:167], a[136:139], v[220:223]\n" \
M_"v[220:223], a[228:231], a[200:203], v[220:223]\n" \
X_"a[188:189], v39 offset:0\n" \
C_"v204, v204, v205\n" \
M_"v[228:231], a[172:175], a[136:139], v[228:231]\n" \
C_"v205, v206, v207\n" \
C_"v206, v214, v215\n" \
M_"v[228:231], a[236:239], a[200:203], v[228:231]\n" \
X_"a[190:191], v39 offset:1024\n" \
".fill (L_pb + 36576 - .) / 4, 4, 0xBF800000\n" \
"L_pp9:\n" \
"s_cbranch_execz L_pret\n" \
"v_mov_b32_e32 v207, 0x3f80\n" \
M_"v[224:227], a[136:139], a[168:171], v[224:227]\n" \
X_"a[252:253], v39 offset:8192\n" \
C_"v200, v200, v201\n" \
M_"v[224:227], a[200:203], a[232:235], v[224:227]\n" \
C_"v201, v202, v203\n" \
C_"v202, v212, v213\n" \
M_"v[240:243], a[140:143], a[168:171], v[240:243]\n" \
X_"a[254:255], v39 offset:9216\n" \
"v_mov_b32_e32 v203, 0\n" \
M_"v[240:243], a[204:207], a[232:235], v[240:243]\n" \
M_"v[244:247], a[172:175], a[140:143], v[244:247]\n" \
M_"v[244:247], a[236:239], a[204:207], v[244:247]\n" \
C_"v220, v220, v221\n" \
C_"v221, v222, v223\n" \
M_"v[236:239], a[164:167], a[140:143], v[236:239]\n" \
C_"v222, v230, v231\n" \
"v_mov_b32_e32 v223, 0x3f80\n" \
M_"v[236:239], a[228:231], a[204:207], v[236:239]\n" \
C_"v216, v216, v217\n" \
C_"v217, v218, v219\n" \
M_"v[232:235], a[160:163], a[140:143], v[232:235]\n" \
C_"v218, v228, v229\n" \
"v_mov_b32_e64 v219, 0\n" \
M_"v[232:235], a[224:227], a[204:207], v[232:235]\n" \
"s_waitcnt lgkmcnt(15)\n" \
"s_nop 0\n" \
M_"v[180:183], a[164:167], v[248:251], v[180:183]\n" \
M_"v[180:183], a[228:231], v[48:51], v[180:183]\n" \
M_"a[80:83], a[172:175], v[248:251], a[80:83]\n" \
M_"a[80:83], a[236:239], v[48:51], a[80:83]\n" \
C_"v236, v236, v237\n" \
C_"v237, v238, v239\n" \
M_"v[252:255], v[248:251], a[168:171], v[252:255]\n" \
C_"v238, v246, v247\n" \
"v_mov_b32_e32 v239, 0x3f80\n" \
M_"v[252:255], v[48:51], a[232:235], v[252:255]\n" \
C_"v232, v232, v233\n" \
C_"v233, v234, v235\n" \
"s_waitcnt vmcnt(0) lgkmcnt(0)\n" \
"s_barrier\n" \
"v_mov_b32_e32 v45, s62\n" \
"s_mov_b64 exec, 1\n" \
"buffer_store_dword v45, off, s[80:83], s37 offset:32 sc1\n" \
"s_mov_b64 exec, -1\n" \
"v_cmp_ne_u32_e32 vcc, s62, v44\n" \
"s_nop 4\n" \
"s_cbranch_vccnz L_poll160_%=\n" \
"L_pollr160_%=:\n" \
M_"v[52:55], a[176:179], a[128:131], v[52:55]\n" \
"s_mov_b32 s73, s50\n" \
"s_sub_u32 m0, s31, 0x10000\n" \
M_"v[52:55], a[240:243], a[192:195], v[52:55]\n" \
B_"v20, s[80:83], s73 offen offset:0 sc1 lds\n" \
M_"v[56:59], a[180:183], a[128:131], v[56:59]\n" \
"s_add_u32 s74, s50, 0x4000\n" \
"s_add_u32 s75, s50, 0x8000\n" \
M_"v[56:59], a[244:247], a[192:195], v[56:59]\n" \
"s_mov_b32 s70, 0x3e0293ee\n" \
"s_mov_b32 s76, s52\n" \
M_"v[60:63], a[184:187], a[128:131], v[60:63]\n" \
"v_mul_f32_e32 v52, s70, v52\n" \
"v_mul_f32_e32 v53, s70, v53\n" \
M_"v[60:63], a[248:251], a[192:195], v[60:63]\n" \
B_"v20, s[80:83], s73 offen offset:1024 sc1 lds\n" \
M_"v[64:67], a[188:191], a[128:131], v[64:67]\n" \
"v_mul_f32_e32 v54, s70, v54\n" \
"v_mul_f32_e32 v55, s70, v55\n" \
M_"v[64:67], a[252:255], a[192:195], v[64:67]\n" \
C_"v52, v52, v53\n" \
"v_mul_f32_e64 v56, s70, v56\n" \
M_"v[80:83], a[188:191], a[132:135], v[80:83]\n" \
"v_mul_f32_e32 v60, s70, v60\n" \
"v_mul_f32_e32 v61, s70, v61\n" \
M_"v[80:83], a[252:255], a[196:199], v[80:83]\n" \
B_"v20, s[80:83], s73 offen offset:2048 sc1 lds\n" \
M_"v[76:79], a[184:187], a[132:135], v[76:79]\n" \
"v_mul_f32_e32 v62, s70, v62\n" \
"v_mul_f32_e32 v63, s70, v63\n" \
M_"v[76:79], a[248:251], a[196:199], v[76:79]\n" \
C_"v60, v60, v61\n" \
"v_mul_f32_e64 v57, s70, v57\n" \
M_"v[72:75], a[180:183], a[132:135], v[72:75]\n" \
"v_mul_f32_e32 v58, s70, v58\n" \
"v_mul_f32_e32 v59, s70, v59\n" \
M_"v[72:75], a[244:247], a[196:199], v[72:75]\n" \
B_"v20, s[80:83], s73 offen offset:3072 sc1 lds\n" \
M_"v[68:71], a[176:179], a[132:135], v[68:71]\n" \
"s_sub_u32 m0, s31, s51\n" \
C_"v53, v54, v55\n" \
M_"v[68:71], a[240:243], a[196:199], v[68:71]\n" \
"v_mul_f32_e32 v64, s70, v64\n" \
"v_mul_f32_e64 v65, s70, v65\n" \
M_"v[84:87], a[176:179], a[136:139], v[84:87]\n" \
"v_mul_f32_e32 v66, s70, v66\n" \
"v_mul_f32_e32 v67, s70, v67\n" \
M_"v[84:87], a[240:243], a[200:203], v[84:87]\n" \
B_"v20, s[80:83], s74 offen offset:0 sc1 lds\n" \
M_"v[88:91], a[180:183], a[136:139], v[88:91]\n" \
C_"v61, v62, v63\n" \
C_"v54, v56, v57\n" \
M_"v[88:91], a[244:247], a[200:203], v[88:91]\n" \
C_"v55, v58, v59\n" \
C_"v62, v64, v65\n" \
M_"v[92:95], a[184:187], a[136:139], v[92:95]\n" \
C_"v63, v66, v67\n" \
"v_mul_f32_e64 v68, s70, v68\n" \
M_"v[92:95], a[248:251], a[200:203], v[92:95]\n" \
B_"v20, s[80:83], s74 offen offset:1024 sc1 lds\n" \
M_"v[96:99], a[188:191], a[136:139], v[96:99]\n" \
"ds_write_b128 v27, v[52:55] offset:0\n" \
M_"v[96:99], a[252:255], a[200:203], v[96:99]\n" \
"v_mul_f32_e32 v69, s70, v69\n" \
"v_mul_f32_e32 v76, s70, v76\n" \
M_"v[112:115], a[188:191], a[140:143], v[112:115]\n" \
"v_mul_f32_e32 v77, s70, v77\n" \
"v_mul_f32_e32 v70, s70, v70\n" \
M_"v[112:115], a[252:255], a[204:207], v[112:115]\n" \
B_"v20, s[80:83], s74 offen offset:2048 sc1 lds\n" \
M_"v[108:111], a[184:187], a[140:143], v[108:111]\n" \
"ds_write_b128 v27, v[60:63] offset:1024\n" \
M_"v[108:111], a[248:251], a[204:207], v[108:111]\n" \
"v_mul_f32_e64 v71, s70, v71\n" \
C_"v68, v68, v69\n" \
M_"v[104:107], a[180:183], a[140:143], v[104:107]\n" \
"v_mul_f32_e32 v78, s70, v78\n" \
"v_mul_f32_e32 v79, s70, v79\n" \
M_"v[104:107], a[244:247], a[204:207], v[104:107]\n" \
B_"v20, s[80:83], s74 offen offset:3072 sc1 lds\n" \
M_"v[100:103], a[176:179], a[140:143], v[100:103]\n" \
"s_sub_u32 m0, s31, 0x4000\n" \
C_"v76, v76, v77\n" \
M_"v[100:103], a[240:243], a[204:207], v[100:103]\n" \
"v_mul_f32_e32 v72, s70, v72\n" \
"v_mul_f32_e32 v73, s70, v73\n" \
M_"v[116:119], a[176:179], a[144:147], v[116:119]\n" \
"v_mul_f32_e32 v74, s70, v74\n" \
"v_mul_f32_e32 v75, s70, v75\n" \
M_"v[116:119], a[240:243], a[208:211], v[116:119]\n" \
B_"v20, s[80:83], s75 offen offset:0 sc1 lds\n" \
M_"v[120:123], a[180:183], a[144:147], v[120:123]\n" \
C_"v69, v70, v71\n" \
"v_mul_f32_e64 v80, s70, v80\n" \
M_"v[120:123], a[244:247], a[208:211], v[120:123]\n" \
"v_mul_f32_e32 v81, s70, v81\n" \
"v_mul_f32_e32 v82, s70, v82\n" \
M_"v[124:127], a[184:187], a[144:147], v[124:127]\n" \
"v_mul_f32_e64 v83, s70, v83\n" \
C_"v77, v78, v79\n" \
M_"v[124:127], a[248:251], a[208:211], v[124:127]\n" \
B_"v20, s[80:83], s75 offen offset:1024 sc1 lds\n" \
M_"v[128:131], a[188:191], a[144:147], v[128:131]\n" \
C_"v70, v72, v73\n" \
C_"v71, v74, v75\n" \
M_"v[128:131], a[252:255], a[208:211], v[128:131]\n" \
C_"v78, v80, v81\n" \
C_"v79, v82, v83\n" \
M_"v[144:147], a[188:191], a[148:151], v[144:147]\n" \
"ds_write_b128 v27, v[68:71] offset:4096\n" \
M_"v[144:147], a[252:255], a[212:215], v[144:147]\n" \
B_"v20, s[80:83], s75 offen offset:2048 sc1 lds\n" \
M_"v[140:143], a[184:187], a[148:151], v[140:143]\n" \
C_"v180, v180, v181\n" \
"v_accvgpr_read_b32 v50, a82\n" \
M_"v[140:143], a[248:251], a[212:215], v[140:143]\n" \
"ds_write_b128 v27, v[76:79] offset:5120\n" \
M_"v[136:139], a[180:183], a[148:151], v[136:139]\n" \
"v_accvgpr_read_b32 v51, a83\n" \
C_"v181, v182, v183\n" \
M_"v[136:139], a[244:247], a[212:215], v[136:139]\n" \
B_"v20, s[80:83], s75 offen offset:3072 sc1 lds\n" \
M_"v[132:135], a[176:179], a[148:151], v[132:135]\n" \
"s_mov_b32 m0, s53\n" \
C_"v182, v50, v51\n" \
M_"v[132:135], a[240:243], a[212:215], v[132:135]\n" \
"v_mov_b32_e32 v183, 0x3f80\n" \
C_"v234, v244, v245\n" \
M_"v[148:151], a[176:179], a[152:155], v[148:151]\n" \
"v_mov_b32_e32 v235, 0\n" \
C_"v54, v192, v193\n" \
M_"v[148:151], a[240:243], a[216:219], v[148:151]\n" \
B_"v20, s[80:83], s76 offen offset:0 sc1 lds\n" \
M_"v[152:155], a[180:183], a[152:155], v[152:155]\n" \
C_"v55, v194, v195\n" \
C_"v56, v208, v209\n" \
M_"v[152:155], a[244:247], a[216:219], v[152:155]\n" \
C_"v57, v210, v211\n" \
C_"v58, v224, v225\n" \
C_"v59, v226, v227\n" \
C_"v60, v240, v241\n" \
C_"v61, v242, v243\n" \
M_"v[32:35], v[180:183], v[184:187], 0\n" \
B_"v20, s[80:83], s76 offen offset:1024 sc1 lds\n" \
M_"v[156:159], a[184:187], a[152:155], v[156:159]\n" \
M_"v[192:195], v[188:191], v[184:187], 0\n" \
M_"v[208:211], v[204:207], v[200:203], 0\n" \
M_"v[36:39], v[188:191], v[200:203], 0\n" \
M_"v[224:227], v[220:223], v[216:219], 0\n" \
M_"v[248:251], v[204:207], v[216:219], 0\n" \
M_"v[240:243], v[236:239], v[232:235], 0\n" \
M_"v[64:67], v[220:223], v[232:235], 0\n" \
"s_mov_b32 s64, 0xaaaaaaaa\n" \
"s_mov_b32 s65, 0xaaaaaaaa\n" \
M_"v[156:159], a[248:251], a[216:219], v[156:159]\n" \
"v_cndmask_b32_e64 v32, v32, v33, s[64:65]\n" \
"v_cndmask_b32_e64 v34, v34, v35, s[64:65]\n" \
M_"v[160:163], a[188:191], a[152:155], v[160:163]\n" \
"s_mov_b32 s66, 0xfff0ffff\n" \
"s_mov_b32 s67, 0xf000ff00\n" \
"s_mov_b32 s68, 0xffc0fffc\n" \
"s_mov_b32 s69, 0xc000fc00\n" \
"v_cndmask_b32_e64 v192, v192, v193, s[64:65]\n" \
"v_add_f32_e64 v32, s47, v32\n" \
"v_cndmask_b32_e64 v194, v194, v195, s[64:65]\n" \
"v_add_f32_e64 v34, s47, v34\n" \
"v_cndmask_b32_e64 v208, v208, v209, s[64:65]\n" \
"v_cndmask_b32_e64 v36, v36, v37, s[64:65]\n" \
"v_cndmask_b32_e64 v210, v210, v211, s[64:65]\n" \
"v_cndmask_b32_e64 v38, v38, v39, s[64:65]\n" \
"v_cndmask_b32_e64 v224, v224, v225, s[64:65]\n" \
"v_cndmask_b32_e64 v248, v248, v249, s[64:65]\n" \
"v_cndmask_b32_e64 v226, v226, v227, s[64:65]\n" \
"v_cndmask_b32_e64 v250, v250, v251, s[64:65]\n" \
"v_cndmask_b32_e64 v240, v240, v241, s[64:65]\n" \
"v_cndmask_b32_e64 v64, v64, v65, s[64:65]\n" \
"v_cndmask_b32_e64 v242, v242, v243, s[64:65]\n" \
"v_cndmask_b32_e64 v66, v66, v67, s[64:65]\n" \
"v_cndmask_b32_e64 v192, v32, v192, s[66:67]\n" \
"v_cndmask_b32_e64 v194, v34, v194, s[68:69]\n" \
"v_cndmask_b32_e64 v208, v36, v208, s[66:67]\n" \
"v_cndmask_b32_e64 v210, v38, v210, s[68:69]\n" \
"v_cndmask_b32_e64 v224, v248, v224, s[66:67]\n" \
"v_cndmask_b32_e64 v226, v250, v226, s[68:69]\n" \
"v_cndmask_b32_e64 v240, v64, v240, s[66:67]\n" \
"v_cndmask_b32_e64 v242, v66, v242, s[68:69]\n" \
M_"v[160:163], a[252:255], a[216:219], v[160:163]\n" \
"v_max_f32_e32 v193, v192, v194\n" \
"v_max_f32_e32 v209, v208, v210\n" \
"v_max_f32_e32 v225, v224, v226\n" \
"v_max_f32_e32 v241, v240, v242\n" \
"v_mul_f32_e32 v84, s70, v84\n" \
"v_permlane32_swap_b32_e32 v193, v225\n" \
"v_permlane32_swap_b32_e32 v209, v241\n" \
"v_mul_f32_e32 v85, s70, v85\n" \
"v_mul_f32_e32 v92, s70, v92\n" \
"v_mul_f32_e32 v93, s70, v93\n" \
"v_permlane32_swap_b32_e32 v196, v228\n" \
"v_permlane32_swap_b32_e32 v212, v244\n" \
"v_max_f32_e32 v193, v193, v225\n" \
"v_max_f32_e32 v209, v209, v241\n" \
"v_accvgpr_read_b32 v48, a80\n" \
"v_accvgpr_read_b32 v49, a81\n" \
"v_mul_f32_e32 v86, s70, v86\n" \
"v_mul_f32_e32 v87, s70, v87\n" \
C_"v84, v84, v85\n" \
"v_mul_f32_e32 v94, s70, v94\n" \
"v_mul_f32_e32 v95, s70, v95\n" \
C_"v92, v92, v93\n" \
"v_permlane16_swap_b32_e32 v228, v244\n" \
"v_permlane16_swap_b32_e32 v193, v209\n" \
M_"v[176:179], a[188:191], a[156:159], v[176:179]\n" \
"s_mov_b32 s72, 0x3eae1a93\n" \
"v_mul_f32_e32 v88, s70, v88\n" \
"v_mul_f32_e32 v89, s70, v89\n" \
"v_mul_f32_e32 v90, s70, v90\n" \
"v_mul_f32_e32 v91, s70, v91\n" \
C_"v85, v86, v87\n" \
"v_mul_f32_e32 v96, s70, v96\n" \
"v_mul_f32_e32 v97, s70, v97\n" \
"v_mul_f32_e32 v98, s70, v98\n" \
"v_mul_f32_e32 v99, s70, v99\n" \
C_"v93, v94, v95\n" \
"v_mul_f32_e32 v45, 0xbfb8aa3b, v228\n" \
"v_mfma_f32_16x16x16_bf16 v[48:51], v[182:183], v[46:47], 0\n" \
"v_mfma_f32_16x16x16_bf16 v[196:199], v[190:191], v[46:47], 0\n" \
"v_mfma_f32_16x16x16_bf16 v[212:215], v[206:207], v[46:47], 0\n" \
"v_max_f32_e64 v193, v193, v209\n" \
C_"v86, v88, v89\n" \
C_"v87, v90, v91\n" \
C_"v94, v96, v97\n" \
C_"v95, v98, v99\n" \
"v_mfma_f32_16x16x16_bf16 v[228:231], v[222:223], v[46:47], 0\n" \
"v_mul_f32_e32 v193, s72, v193\n" \
"v_mov_b32_e32 v209, v193\n" \
"ds_write_b128 v27, v[84:87] offset:8192\n" \
"v_mfma_f32_16x16x16_bf16 v[244:247], v[238:239], v[46:47], 0\n" \
"v_permlane16_swap_b32_e32 v193, v209\n" \
"s_mov_b32 s71, 0x3e0293ee\n" \
M_"v[176:179], a[252:255], a[220:223], v[176:179]\n" \
"v_mov_b32_e32 v225, v193\n" \
"v_mov_b32_e32 v241, v209\n" \
"v_mul_f32_e32 v100, s70, v100\n" \
"v_mul_f32_e64 v101, s70, v101\n" \
"ds_write_b128 v27, v[92:95] offset:9216\n" \
"v_mul_f32_e32 v108, s70, v108\n" \
"v_mul_f32_e32 v109, s70, v109\n" \
"v_pk_mul_f32 v[116:117], v[116:117], s[70:71]\n" \
"v_pk_mul_f32 v[124:125], v[124:125], s[70:71]\n" \
"v_pk_mul_f32 v[132:133], v[132:133], s[70:71]\n" \
"v_pk_mul_f32 v[140:141], v[140:141], s[70:71]\n" \
"v_pk_mul_f32 v[148:149], v[148:149], s[70:71]\n" \
"v_pk_mul_f32 v[156:157], v[156:157], s[70:71]\n" \
"v_permlane32_swap_b32_e32 v193, v225\n" \
"v_permlane32_swap_b32_e32 v209, v241\n" \
"v_mul_f32_e32 v102, s70, v102\n" \
"v_mul_f32_e32 v103, s70, v103\n" \
C_"v100, v100, v101\n" \
"v_mul_f32_e32 v110, s70, v110\n" \
"v_mul_f32_e32 v111, s70, v111\n" \
C_"v108, v108, v109\n" \
"v_pk_mul_f32 v[118:119], v[118:119], s[70:71]\n" \
C_"v116, v116, v117\n" \
"v_pk_mul_f32 v[126:127], v[126:127], s[70:71]\n" \
C_"v124, v124, v125\n" \
"v_pk_mul_f32 v[134:135], v[134:135], s[70:71]\n" \
C_"v132, v132, v133\n" \
"v_pk_mul_f32 v[142:143], v[142:143], s[70:71]\n" \
C_"v140, v140, v141\n" \
"v_pk_mul_f32 v[150:151], v[150:151], s[70:71]\n" \
C_"v148, v148, v149\n" \
"v_pk_mul_f32 v[158:159], v[158:159], s[70:71]\n" \
C_"v156, v156, v157\n" \
"v_fma_f32 v192, v192, s72, -v193\n" \
"v_fma_f32 v194, v194, s72, -v193\n" \
"v_fma_f32 v208, v208, s72, -v209\n" \
"v_fma_f32 v210, v210, s72, -v209\n" \
"v_mul_f32_e32 v104, s70, v104\n" \
"v_mul_f32_e32 v105, s70, v105\n" \
"v_mul_f32_e32 v106, s70, v106\n" \
"v_mul_f32_e32 v107, s70, v107\n" \
C_"v101, v102, v103\n" \
"v_mul_f32_e32 v112, s70, v112\n" \
"v_mul_f32_e32 v113, s70, v113\n" \
"v_mul_f32_e32 v114, s70, v114\n" \
"v_mul_f32_e32 v115, s70, v115\n" \
C_"v109, v110, v111\n" \
"v_pk_mul_f32 v[120:121], v[120:121], s[70:71]\n" \
"v_pk_mul_f32 v[122:123], v[122:123], s[70:71]\n" \
C_"v117, v118, v119\n" \
"v_pk_mul_f32 v[128:129], v[128:129], s[70:71]\n" \
"v_pk_mul_f32 v[130:131], v[130:131], s[70:71]\n" \
C_"v125, v126, v127\n" \
"v_pk_mul_f32 v[136:137], v[136:137], s[70:71]\n" \
"v_pk_mul_f32 v[138:139], v[138:139], s[70:71]\n" \
C_"v133, v134, v135\n" \
"v_pk_mul_f32 v[144:145], v[144:145], s[70:71]\n" \
"v_pk_mul_f32 v[146:147], v[146:147], s[70:71]\n" \
C_"v141, v142, v143\n" \
"v_pk_mul_f32 v[152:153], v[152:153], s[70:71]\n" \
"v_pk_mul_f32 v[154:155], v[154:155], s[70:71]\n" \
C_"v149, v150, v151\n" \
"v_pk_mul_f32 v[160:161], v[160:161], s[70:71]\n" \
"v_pk_mul_f32 v[162:163], v[162:163], s[70:71]\n" \
C_"v157, v158, v159\n" \
"v_pk_mul_f32 v[176:177], v[176:177], s[70:71]\n" \
"v_pk_mul_f32 v[178:179], v[178:179], s[70:71]\n" \
E_"v192, v192\n" \
E_"v194, v194\n" \
E_"v208, v208\n" \
E_"v210, v210\n" \
"v_fma_f32 v224, v224, s72, -v225\n" \
"v_fma_f32 v226, v226, s72, -v225\n" \
M_"v[172:175], a[184:187], a[156:159], v[172:175]\n" \
C_"v102, v104, v105\n" \
C_"v103, v106, v107\n" \
C_"v110, v112, v113\n" \
C_"v111, v114, v115\n" \
C_"v118, v120, v121\n" \
C_"v119, v122, v123\n" \
C_"v126, v128, v129\n" \
C_"v127, v130, v131\n" \
C_"v134, v136, v137\n" \
C_"v135, v138, v139\n" \
C_"v142, v144, v145\n" \
C_"v143, v146, v147\n" \
C_"v150, v152, v153\n" \
C_"v151, v154, v155\n" \
C_"v158, v160, v161\n" \
C_"v159, v162, v163\n" \
E_"v224, v224\n" \
E_"v226, v226\n" \
"v_fma_f32 v240, v240, s72, -v241\n" \
"v_fma_f32 v242, v242, s72, -v241\n" \
"ds_write_b128 v27, v[100:103] offset:12288\n" \
C_"v192, v192, v194\n" \
C_"v208, v208, v210\n" \
E_"v240, v240\n" \
E_"v242, v242\n" \
C_"v68, v48, v49\n" \
C_"v69, v50, v51\n" \
C_"v70, v196, v197\n" \
C_"v71, v198, v199\n" \
"ds_write_b128 v27, v[108:111] offset:13312\n" \
C_"v72, v212, v213\n" \
C_"v73, v214, v215\n" \
"v_perm_b32 v184, v192, v192, v40\n" \
"v_perm_b32 v185, v192, v192, v41\n" \
"v_perm_b32 v186, v192, v192, v42\n" \
"v_perm_b32 v187, v192, v192, v43\n" \
"v_perm_b32 v200, v208, v208, v40\n" \
"v_perm_b32 v201, v208, v208, v41\n" \
"v_perm_b32 v202, v208, v208, v42\n" \
"v_perm_b32 v203, v208, v208, v43\n" \
"ds_write_b128 v21, v[116:119] offset:16384\n" \
C_"v224, v224, v226\n" \
C_"v74, v228, v229\n" \
C_"v75, v230, v231\n" \
M_"v[32:35], v[68:71], v[184:187], 0\n" \
M_"v[36:39], v[70:73], v[200:203], 0\n" \
"v_perm_b32 v216, v224, v224, v40\n" \
"v_perm_b32 v217, v224, v224, v41\n" \
"ds_write_b128 v21, v[124:127] offset:17408\n" \
"v_perm_b32 v218, v224, v224, v42\n" \
"v_perm_b32 v219, v224, v224, v43\n" \
C_"v240, v240, v242\n" \
M_"v[172:175], a[248:251], a[220:223], v[172:175]\n" \
C_"v76, v244, v245\n" \
C_"v77, v246, v247\n" \
M_"v[248:251], v[72:75], v[216:219], 0\n" \
"v_perm_b32 v232, v240, v240, v40\n" \
"v_perm_b32 v233, v240, v240, v41\n" \
"ds_write_b128 v21, v[132:135] offset:20480\n" \
"v_perm_b32 v234, v240, v240, v42\n" \
"v_perm_b32 v235, v240, v240, v43\n" \
"v_pk_mul_f32 v[172:173], v[172:173], s[70:71]\n" \
"v_pk_mul_f32 v[174:175], v[174:175], s[70:71]\n" \
C_"v172, v172, v173\n" \
C_"v173, v174, v175\n" \
C_"v174, v176, v177\n" \
C_"v175, v178, v179\n" \
M_"v[64:67], v[74:77], v[232:235], 0\n" \
"ds_write_b128 v21, v[140:143] offset:21504\n" \
M_"v[168:171], a[180:183], a[156:159], v[168:171]\n" \
C_"v52, v252, v253\n" \
C_"v53, v254, v255\n" \
M_"v[168:171], a[244:247], a[220:223], v[168:171]\n" \
"v_exp_f32_e64 v45, v45\n" \
M_"v[192:195], v[52:55], v[184:187], 0\n" \
"ds_write_b128 v21, v[148:151] offset:24576\n" \
M_"v[208:211], v[54:57], v[200:203], 0\n" \
M_"v[224:227], v[56:59], v[216:219], 0\n" \
M_"v[240:243], v[58:61], v[232:235], 0\n" \
"ds_write_b128 v21, v[156:159] offset:25600\n" \
"v_mov_b32_dpp v34, v38 quad_perm:[0,1,2,3] row_mask:0x2 bank_mask:0xf\n" \
"v_pk_mul_f32 v[168:169], v[168:169], s[70:71]\n" \
"v_pk_mul_f32 v[170:171], v[170:171], s[70:71]\n" \
"v_mov_b32_dpp v34, v250 quad_perm:[0,1,2,3] row_mask:0x4 bank_mask:0xf\n" \
"v_add_f32_e64 v45, 1.0, v45\n" \
M_"v[164:167], a[176:179], a[156:159], v[164:167]\n" \
"v_mov_b32_dpp v34, v66 quad_perm:[0,1,2,3] row_mask:0x8 bank_mask:0xf\n" \
"v_mov_b32_dpp v32, v36 quad_perm:[0,1,2,3] row_mask:0x2 bank_mask:0xf\n" \
"v_rcp_f32_e64 v45, v45\n" \
"ds_write_b128 v21, v[172:175] offset:29696\n" \
"v_mov_b32_dpp v33, v37 quad_perm:[0,1,2,3] row_mask:0x2 bank_mask:0xf\n" \
"v_rcp_f32_e64 v34, v34\n" \
"v_mov_b32_dpp v32, v248 quad_perm:[0,1,2,3] row_mask:0x4 bank_mask:0xf\n" \
"v_mov_b32_dpp v33, v249 quad_perm:[0,1,2,3] row_mask:0x4 bank_mask:0xf\n" \
"v_mul_f32_e64 v34, v45, v34\n" \
"v_mov_b32_dpp v32, v64 quad_perm:[0,1,2,3] row_mask:0x8 bank_mask:0xf\n" \
"v_mov_b32_dpp v33, v65 quad_perm:[0,1,2,3] row_mask:0x8 bank_mask:0xf\n" \
"v_mul_f32_e32 v32, v34, v32\n" \
"v_mul_f32_e32 v33, v34, v33\n" \
"v_mov_b32_e64 v38, v34\n" \
M_"v[164:167], a[240:243], a[220:223], v[164:167]\n" \
"v_sub_f32_e32 v44, 1.0, v45\n" \
"v_permlane16_swap_b32_e32 v34, v38\n" \
"v_mov_b32_e32 v250, v34\n" \
"v_mov_b32_e32 v66, v38\n" \
C_"v32, v32, v33\n" \
"v_permlane32_swap_b32_e32 v34, v250\n" \
"v_permlane32_swap_b32_e32 v38, v66\n" \
"v_mul_f32_e64 v192, v34, v192\n" \
"v_pk_mul_f32 v[164:165], v[164:165], s[70:71]\n" \
"v_pk_mul_f32 v[166:167], v[166:167], s[70:71]\n" \
C_"v164, v164, v165\n" \
C_"v165, v166, v167\n" \
C_"v166, v168, v169\n" \
C_"v167, v170, v171\n" \
"ds_write_b128 v21, v[164:167] offset:28672\n" \
"v_mul_f32_e32 v193, v34, v193\n" \
"v_mul_f32_e32 v208, v38, v208\n" \
"v_mul_f32_e32 v209, v38, v209\n" \
"v_mul_f32_e32 v224, v250, v224\n" \
"v_mul_f32_e32 v225, v250, v225\n" \
"v_mul_f32_e32 v240, v66, v240\n" \
"v_mul_f32_e32 v241, v66, v241\n" \
"v_mul_f32_e32 v194, v34, v194\n" \
"v_mul_f32_e64 v195, v34, v195\n" \
C_"v192, v192, v193\n" \
"v_mul_f32_e32 v210, v38, v210\n" \
"v_mul_f32_e32 v211, v38, v211\n" \
C_"v208, v208, v209\n" \
"v_mul_f32_e32 v226, v250, v226\n" \
"v_mul_f32_e32 v227, v250, v227\n" \
C_"v224, v224, v225\n" \
"v_mul_f32_e32 v242, v66, v242\n" \
"v_mul_f32_e32 v243, v66, v243\n" \
C_"v240, v240, v241\n" \
C_"v193, v194, v195\n" \
"s_add_u32 s84, s48, 0x0\n" \
"s_nop 0\n" \
C_"v209, v210, v211\n" \
"s_add_u32 s85, s48, 0x24800\n" \
C_"v225, v226, v227\n" \
"s_add_u32 s86, s48, 0x49000\n" \
C_"v241, v242, v243\n" \
"s_add_u32 s87, s48, 0x6d800\n" \
"ds_write_b32 v26, v44\n" \
"buffer_store_dword v32, v25, s[80:83], s49 offen sc1\n" \
"buffer_store_dwordx2 v[192:193], v24, s[80:83], s84 offen sc1\n" \
"buffer_store_dwordx2 v[208:209], v24, s[80:83], s85 offen sc1\n" \
"buffer_store_dwordx2 v[224:225], v24, s[80:83], s86 offen sc1\n" \
"buffer_store_dwordx2 v[240:241], v24, s[80:83], s87 offen sc1\n" \
"s_branch L_end_%=\n" \
"L_poll60_%=:\n" \
"s_mov_b32 s84, 0\n" \
"L_pl60_%=:\n" \
"s_sleep 1\n" \
"v_bfe_u32 v44, v20, 2, 5\n" \
"buffer_load_dword v44, v44, s[80:83], s38 offen offset:128 sc1\n" \
"s_waitcnt vmcnt(0)\n" \
"v_cmp_ne_u32_e32 vcc, s62, v44\n" \
"s_add_u32 s84, s84, 1\n" \
"s_nop 3\n" \
"s_cbranch_vccz L_pollr60_%=\n" \
"s_cmp_gt_u32 s84, 0x400000\n" \
"s_cbranch_scc0 L_pl60_%=\n" \
"s_branch L_pollr60_%=\n" \
"L_poll160_%=:\n" \
"s_mov_b32 s84, 0\n" \
"L_pl160_%=:\n" \
"s_sleep 1\n" \
"v_bfe_u32 v44, v20, 2, 5\n" \
"buffer_load_dword v44, v44, s[80:83], s38 offen offset:0 sc1\n" \
"s_waitcnt vmcnt(0)\n" \
"v_cmp_ne_u32_e32 vcc, s62, v44\n" \
"s_add_u32 s84, s84, 1\n" \
"s_nop 3\n" \
"s_cbranch_vccz L_pollr160_%=\n" \
"s_cmp_gt_u32 s84, 0x400000\n" \
"s_cbranch_scc0 L_pl160_%=\n" \
"s_branch L_pollr160_%=\n" \
".fill (L_pb + 42500 - .) / 4, 4, 0xBF800000\n" \
"L_w1_%=:\n" \
"s_waitcnt vmcnt(19)\n" \
"s_barrier\n" \
X_"a[160:161], v32 offset:0\n" \
X_"a[162:163], v32 offset:1024\n" \
R_"a[128:131], v28 offset:16384\n" \
X_"a[224:225], v32 offset:8192\n" \
X_"a[226:227], v32 offset:9216\n" \
R_"a[192:195], v29 offset:16384\n" \
R_"a[132:135], v28 offset:18432\n" \
R_"a[196:199], v29 offset:18432\n" \
X_"a[164:165], v33 offset:0\n" \
X_"a[166:167], v33 offset:1024\n" \
X_"a[228:229], v33 offset:8192\n" \
X_"a[230:231], v33 offset:9216\n" \
R_"a[136:139], v28 offset:20480\n" \
R_"a[200:203], v29 offset:20480\n" \
"s_waitcnt lgkmcnt(11)\n" \
M_"a[0:3], a[128:131], a[160:163], 0\n" \
X_"a[168:169], v34 offset:0\n" \
"s_lshr_b32 s4, s2, 1\n" \
"s_waitcnt lgkmcnt(9)\n" \
M_"a[0:3], a[192:195], a[224:227], a[0:3]\n" \
X_"a[170:171], v34 offset:1024\n" \
"s_and_b32 s4, s4, 0x7c\n" \
"s_waitcnt lgkmcnt(9)\n" \
M_"a[16:19], a[132:135], a[160:163], 0\n" \
X_"a[232:233], v34 offset:8192\n" \
"v_mov_b32_e32 v213, s13\n" \
"s_waitcnt lgkmcnt(9)\n" \
M_"a[16:19], a[196:199], a[224:227], a[16:19]\n" \
X_"a[234:235], v34 offset:9216\n" \
"s_or_b32 s4, s4, s3\n" \
"s_waitcnt lgkmcnt(8)\n" \
M_"a[20:23], a[132:135], a[164:167], 0\n" \
R_"a[140:143], v28 offset:22528\n" \
"v_lshrrev_b32_e32 v212, 4, v20\n" \
"s_waitcnt lgkmcnt(7)\n" \
M_"a[20:23], a[196:199], a[228:231], a[20:23]\n" \
R_"a[204:207], v29 offset:22528\n" \
"v_mad_u64_u32 v[214:215], s[4:5], s4, 36, v[212:213]\n" \
M_"a[4:7], a[128:131], a[164:167], 0\n" \
X_"a[172:173], v35 offset:0\n" \
"v_mov_b32_e32 v217, s9\n" \
M_"a[4:7], a[192:195], a[228:231], a[4:7]\n" \
X_"a[174:175], v35 offset:1024\n" \
"s_movk_i32 s4, 0x240\n" \
"s_waitcnt lgkmcnt(9)\n" \
M_"a[32:35], a[136:139], a[160:163], 0\n" \
X_"a[236:237], v35 offset:8192\n" \
"v_cmp_gt_u32_e32 vcc, s4, v20\n" \
"s_waitcnt lgkmcnt(9)\n" \
M_"a[32:35], a[200:203], a[224:227], a[32:35]\n" \
X_"a[238:239], v35 offset:9216\n" \
"s_lshl_b32 s4, s2, 3\n" \
M_"a[36:39], a[136:139], a[164:167], 0\n" \
R_"a[144:147], v28 offset:0\n" \
"s_and_b32 s4, s4, 56\n" \
M_"a[36:39], a[200:203], a[228:231], a[36:39]\n" \
R_"a[208:211], v29 offset:0\n" \
"v_cndmask_b32_e32 v217, v217, v213, vcc\n" \
"s_waitcnt lgkmcnt(10)\n" \
M_"a[40:43], a[136:139], a[168:171], 0\n" \
R_"a[148:151], v28 offset:2048\n" \
"v_lshlrev_b32_e32 v213, 3, v214\n" \
"s_waitcnt lgkmcnt(9)\n" \
M_"a[40:43], a[200:203], a[232:235], a[40:43]\n" \
R_"a[212:215], v29 offset:2048\n" \
"v_and_b32_e32 v213, 0x1ffc0, v213\n" \
M_"a[24:27], a[132:135], a[168:171], 0\n" \
R_"a[152:155], v28 offset:4096\n" \
"v_and_b32_e64 v214, 7, v214\n" \
M_"a[24:27], a[196:199], a[232:235], a[24:27]\n" \
R_"a[216:219], v29 offset:4096\n" \
"v_or3_b32 v213, v214, s4, v213\n" \
M_"a[8:11], a[128:131], a[168:171], 0\n" \
R_"a[156:159], v28 offset:6144\n" \
"s_lshl_b32 s4, s2, 2\n" \
M_"a[8:11], a[192:195], a[232:235], a[8:11]\n" \
R_"a[220:223], v29 offset:6144\n" \
"s_mov_b32 m0, s32\n" \
"s_waitcnt lgkmcnt(13)\n" \
M_"a[48:51], a[140:143], a[160:163], 0\n" \
"s_mov_b32 exec_lo, 0xff000000\n" \
"s_mov_b32 exec_hi, 0x0\n" \
"global_load_lds_dword v[184:185], off\n" \
"s_mov_b64 exec, -1\n" \
"s_waitcnt lgkmcnt(12)\n" \
M_"a[48:51], a[204:207], a[224:227], a[48:51]\n" \
X_"a[176:177], v36 offset:0\n" \
"s_or_b32 s4, s4, s3\n" \
M_"a[52:55], a[140:143], a[164:167], 0\n" \
X_"a[178:179], v36 offset:1024\n" \
"s_mul_i32 s4, s4, 18\n" \
M_"a[52:55], a[204:207], a[228:231], a[52:55]\n" \
"s_mov_b32 m0, s32\n" \
X_"a[240:241], v36 offset:8192\n" \
M_"a[56:59], a[140:143], a[168:171], 0\n" \
"s_mov_b32 exec_lo, 0xffff\n" \
"s_mov_b32 exec_hi, 0x0\n" \
"global_load_lds_dword v[186:187], off\n" \
"s_mov_b64 exec, -1\n" \
"s_mov_b64 exec, 0\n" \
"s_swappc_b64 s[88:89], s[86:87]\n" \
"s_mov_b64 exec, -1\n" \
"s_add_u32 s86, s86, 0x2000\n" \
"s_addc_u32 s87, s87, 0\n" \
M_"a[56:59], a[204:207], a[232:235], a[56:59]\n" \
X_"a[242:243], v36 offset:9216\n" \
"v_min_u32_e32 v212, 53, v212\n" \
"s_waitcnt lgkmcnt(14)\n" \
M_"a[60:63], a[140:143], a[172:175], 0\n" \
X_"a[180:181], v37 offset:0\n" \
"s_sub_i32 s4, s4, 36\n" \
"s_waitcnt lgkmcnt(13)\n" \
M_"a[60:63], a[204:207], a[236:239], a[60:63]\n" \
X_"a[182:183], v37 offset:1024\n" \
"v_add_u32_e32 v212, s4, v212\n" \
M_"a[44:47], a[136:139], a[172:175], 0\n" \
X_"a[244:245], v37 offset:8192\n" \
"v_mov_b32_e64 v215, s12\n" \
M_"a[44:47], a[200:203], a[236:239], a[44:47]\n" \
X_"a[246:247], v37 offset:9216\n" \
"v_mov_b32_e64 v216, s8\n" \
M_"a[28:31], a[132:135], a[172:175], 0\n" \
X_"a[184:185], v38 offset:0\n" \
"v_cndmask_b32_e64 v212, v212, v213, vcc\n" \
M_"a[28:31], a[196:199], a[236:239], a[28:31]\n" \
X_"a[186:187], v38 offset:1024\n" \
"v_cndmask_b32_e64 v216, v216, v215, vcc\n" \
M_"a[12:15], a[128:131], a[172:175], 0\n" \
"v_lshlrev_b32_e32 v212, 7, v212\n" \
"v_mov_b32_e32 v213, 0\n" \
M_"a[12:15], a[192:195], a[236:239], a[12:15]\n" \
"v_lshl_add_u64 v[188:189], v[216:217], 0, v[212:213]\n" \
"s_movk_i32 s5, 0xd80\n" \
"s_waitcnt lgkmcnt(15)\n" \
M_"a[76:79], a[144:147], a[172:175], 0\n" \
X_"a[248:249], v38 offset:8192\n" \
"s_lshr_b32 s6, s2, 5\n" \
M_"a[76:79], a[208:211], a[236:239], a[76:79]\n" \
"v_lshrrev_b32_e32 v212, 4, v20\n" \
"v_lshl_or_b32 v215, s3, 6, v212\n" \
M_"a[72:75], a[144:147], a[168:171], 0\n" \
"v_mul_u32_u24_e32 v216, 0x2aab, v215\n" \
"v_lshrrev_b32_e64 v216, 16, v216\n" \
M_"a[72:75], a[208:211], a[232:235], a[72:75]\n" \
X_"a[250:251], v38 offset:9216\n" \
"v_mad_i32_i24 v217, v216, -6, v215\n" \
M_"a[68:71], a[144:147], a[164:167], 0\n" \
"s_mov_b64 exec, 0\n" \
"s_swappc_b64 s[88:89], s[86:87]\n" \
"s_mov_b64 exec, -1\n" \
"s_add_u32 s86, s86, 0x2000\n" \
"s_addc_u32 s87, s87, 0\n" \
M_"a[68:71], a[208:211], a[228:231], a[68:71]\n" \
"v_add_u32_e32 v218, 0xfffffe50, v215\n" \
"s_mul_i32 s35, s6, 0x48\n" \
M_"a[64:67], a[144:147], a[160:163], 0\n" \
"v_lshrrev_b32_e32 v219, 1, v217\n" \
"v_add_u32_e32 v218, s35, v216\n" \
M_"a[64:67], a[208:211], a[224:227], a[64:67]\n" \
X_"a[188:189], v39 offset:0\n" \
"v_mul_u32_u24_e32 v219, 0x480, v219\n" \
M_"a[80:83], a[148:151], a[160:163], 0\n" \
"v_and_b32_e64 v214, 16, v20\n" \
"v_mad_u32_u24 v219, v218, s5, v219\n" \
"s_waitcnt lgkmcnt(15)\n" \
M_"a[80:83], a[212:215], a[224:227], a[80:83]\n" \
"v_or_b32_e32 v192, v219, v214\n" \
"v_mov_b32_e64 v220, 2.0\n" \
M_"a[84:87], a[148:151], a[164:167], 0\n" \
X_"a[190:191], v39 offset:1024\n" \
"v_mov_b32_e64 v222, 7\n" \
M_"a[84:87], a[212:215], a[228:231], a[84:87]\n" \
"v_lshlrev_b32_e32 v220, 7, v218\n" \
"v_lshrrev_b32_e32 v218, 1, v218\n" \
M_"a[88:91], a[148:151], a[168:171], 0\n" \
"v_and_b32_e32 v218, 4, v218\n" \
"v_and_b32_e32 v216, 2, v216\n" \
M_"a[88:91], a[212:215], a[232:235], a[88:91]\n" \
X_"a[252:253], v39 offset:8192\n" \
"s_mul_i32 s7, s6, 0x2400\n" \
M_"a[92:95], a[148:151], a[172:175], 0\n" \
"s_sub_i32 s34, 0x22000, s7\n" \
"v_bitop3_b32 v216, v217, v218, v216 bitop3:0x1e\n" \
M_"a[92:95], a[212:215], a[236:239], a[92:95]\n" \
"v_or_b32_e32 v213, 0x24400, v20\n" \
"v_lshlrev_b32_e64 v216, 4, v216\n" \
M_"a[108:111], a[152:155], a[172:175], 0\n" \
X_"a[254:255], v39 offset:9216\n" \
"v_add3_u32 v196, v220, s34, v216\n" \
M_"a[108:111], a[216:219], a[236:239], a[108:111]\n" \
"v_mov_b32_e32 v216, v213\n" \
"s_movk_i32 s36, 0x1b0\n" \
M_"a[104:107], a[152:155], a[168:171], 0\n" \
"s_mov_b64 exec, 0\n" \
"s_swappc_b64 s[88:89], s[86:87]\n" \
"s_mov_b64 exec, -1\n" \
"s_add_u32 s86, s86, 0x2000\n" \
"s_addc_u32 s87, s87, 0\n" \
M_"a[104:107], a[216:219], a[232:235], a[104:107]\n" \
"v_or_b32_e32 v216, 0x100, v215\n" \
"v_mul_u32_u24_e32 v217, 0x2aab, v216\n" \
M_"a[100:103], a[152:155], a[164:167], 0\n" \
"v_add_u32_e32 v215, 0xffffff50, v215\n" \
"v_lshrrev_b32_e64 v217, 16, v217\n" \
M_"a[100:103], a[216:219], a[228:231], a[100:103]\n" \
"v_mov_b32_e64 v220, v215\n" \
"v_mad_i32_i24 v218, v217, -6, v216\n" \
M_"a[96:99], a[152:155], a[160:163], 0\n" \
"v_cmp_gt_u32_e32 vcc, s36, v216\n" \
"v_mov_b32_e32 v221, 7\n" \
M_"a[96:99], a[216:219], a[224:227], a[96:99]\n" \
"v_lshrrev_b32_e32 v222, 1, v218\n" \
"v_cndmask_b32_e32 v217, v220, v217, vcc\n" \
"s_waitcnt lgkmcnt(15)\n" \
M_"a[112:115], a[156:159], a[160:163], 0\n" \
"s_movk_i32 s4, 0x48\n" \
"v_add_u32_e64 v220, s35, v217\n" \
M_"a[112:115], a[220:223], a[224:227], a[112:115]\n" \
"v_mul_u32_u24_e32 v222, 0x480, v222\n" \
"v_mad_u32_u24 v222, v220, s5, v222\n" \
M_"a[116:119], a[156:159], a[164:167], 0\n" \
"v_cmp_gt_u32_e64 s[4:5], s4, v215\n" \
"v_cndmask_b32_e64 v215, v221, v218, vcc\n" \
M_"a[116:119], a[220:223], a[228:231], a[116:119]\n" \
"v_lshrrev_b32_e32 v218, 1, v220\n" \
"v_or_b32_e32 v214, v222, v214\n" \
M_"a[120:123], a[156:159], a[168:171], 0\n" \
"v_mov_b32_e32 v222, 2.0\n" \
"v_and_b32_e32 v218, 4, v218\n" \
M_"a[120:123], a[220:223], a[232:235], a[120:123]\n" \
"v_and_b32_e64 v217, 2, v217\n" \
"v_bitop3_b32 v215, v215, v218, v217 bitop3:0x1e\n" \
M_"a[124:127], a[156:159], a[172:175], 0\n" \
"v_lshlrev_b32_e32 v223, 1, v220\n" \
"v_mov_b32_e32 v224, 2.0\n" \
"s_waitcnt vmcnt(5) lgkmcnt(0)\n" \
M_"a[124:127], a[220:223], a[236:239], a[124:127]\n" \
"s_barrier\n" \
M_"v[52:55], a[128:131], a[176:179], 0\n" \
"s_add_u32 m0, s30, 0x0\n" \
"s_movk_i32 s84, 0x100\n" \
M_"v[52:55], a[192:195], a[240:243], v[52:55]\n" \
B_"v22, s[64:67], s84 offen offset:0 lds\n" \
M_"v[56:59], a[128:131], a[180:183], 0\n" \
X_"a[160:161], v32 offset:32768\n" \
"v_cndmask_b32_e64 v193, v222, v214, vcc\n" \
M_"v[56:59], a[192:195], a[244:247], v[56:59]\n" \
"s_movk_i32 s84, 0x1d00\n" \
M_"v[60:63], a[128:131], a[184:187], 0\n" \
B_"v23, s[64:67], s84 offen offset:1024 lds\n" \
M_"v[60:63], a[192:195], a[248:251], v[60:63]\n" \
X_"a[162:163], v32 offset:33792\n" \
"v_lshlrev_b32_e32 v214, 7, v220\n" \
M_"v[64:67], a[128:131], a[188:191], 0\n" \
X_"a[224:225], v32 offset:40960\n" \
"v_lshlrev_b32_e64 v215, 4, v215\n" \
M_"v[64:67], a[192:195], a[252:255], v[64:67]\n" \
R_"a[128:131], v28 offset:49152\n" \
"s_mul_i32 s39, s3, 18\n" \
M_"v[80:83], a[132:135], a[188:191], 0\n" \
R_"a[192:195], v29 offset:49152\n" \
"v_add3_u32 v214, v214, s34, v215\n" \
M_"v[80:83], a[196:199], a[252:255], v[80:83]\n" \
"s_movk_i32 s84, 0x3900\n" \
M_"v[76:79], a[132:135], a[184:187], 0\n" \
B_"v22, s[64:67], s84 offen offset:2048 lds\n" \
M_"v[76:79], a[196:199], a[248:251], v[76:79]\n" \
"s_movk_i32 s84, 0x5500\n" \
M_"v[72:75], a[132:135], a[180:183], 0\n" \
B_"v23, s[64:67], s84 offen offset:3072 lds\n" \
"s_mov_b64 exec, 0\n" \
"s_swappc_b64 s[88:89], s[86:87]\n" \
"s_mov_b64 exec, -1\n" \
"s_andn2_b32 s85, 2, s3\n" \
"s_lshl_b32 s85, s85, 12\n" \
"s_add_u32 s86, s86, s85\n" \
"s_addc_u32 s87, s87, 0\n" \
M_"v[72:75], a[196:199], a[244:247], v[72:75]\n" \
"s_add_u32 m0, s31, 0x0\n" \
"s_mov_b32 s84, 0x100000\n" \
M_"v[68:71], a[132:135], a[176:179], 0\n" \
B_"v26, s[68:71], s84 offen offset:0 lds\n" \
M_"v[68:71], a[196:199], a[240:243], v[68:71]\n" \
R_"a[132:135], v28 offset:51200\n" \
"v_cndmask_b32_e64 v195, v224, v223, s[4:5]\n" \
M_"v[84:87], a[136:139], a[176:179], 0\n" \
R_"a[196:199], v29 offset:51200\n" \
"s_movk_i32 s4, 0x1f8\n" \
M_"v[84:87], a[200:203], a[240:243], v[84:87]\n" \
"s_addk_i32 s84, 0x7c00\n" \
M_"v[88:91], a[136:139], a[180:183], 0\n" \
B_"v26, s[68:71], s84 offen offset:1024 lds\n" \
M_"v[88:91], a[200:203], a[244:247], v[88:91]\n" \
X_"a[226:227], v32 offset:41984\n" \
"v_mov_b32_e32 v215, v213\n" \
M_"v[92:95], a[136:139], a[184:187], 0\n" \
"s_addk_i32 s84, 0x7c00\n" \
M_"v[92:95], a[200:203], a[248:251], v[92:95]\n" \
B_"v27, s[68:71], s84 offen offset:2048 lds\n" \
M_"v[96:99], a[136:139], a[188:191], 0\n" \
X_"a[164:165], v33 offset:32768\n" \
"s_add_i32 s35, s35, s39\n" \
M_"v[96:99], a[200:203], a[252:255], v[96:99]\n" \
R_"a[136:139], v28 offset:53248\n" \
"v_cmp_gt_u32_e64 vcc, s4, v216\n" \
M_"v[112:115], a[140:143], a[188:191], 0\n" \
R_"a[200:203], v29 offset:53248\n" \
"v_add_u32_e64 v212, s35, v212\n" \
M_"v[112:115], a[204:207], a[252:255], v[112:115]\n" \
"s_addk_i32 s84, 0x7c00\n" \
M_"v[108:111], a[140:143], a[184:187], 0\n" \
B_"v27, s[68:71], s84 offen offset:3072 lds\n" \
M_"v[108:111], a[204:207], a[248:251], v[108:111]\n" \
"s_add_u32 m0, s30, 0x1000\n" \
"s_mov_b32 s84, 0x8100\n" \
M_"v[104:107], a[140:143], a[180:183], 0\n" \
B_"v22, s[64:67], s84 offen offset:0 lds\n" \
M_"v[104:107], a[204:207], a[244:247], v[104:107]\n" \
X_"a[166:167], v33 offset:33792\n" \
"v_mov_b32_e32 v219, 2.0\n" \
M_"v[100:103], a[140:143], a[176:179], 0\n" \
X_"a[228:229], v33 offset:40960\n" \
"v_cndmask_b32_e64 v197, v215, v214, vcc\n" \
M_"v[100:103], a[204:207], a[240:243], v[100:103]\n" \
R_"a[140:143], v28 offset:55296\n" \
"s_movk_i32 s35, 0x120\n" \
M_"v[116:119], a[144:147], a[176:179], 0\n" \
R_"a[204:207], v29 offset:55296\n" \
"v_mul_u32_u24_e32 v214, 0xd80, v212\n" \
M_"v[116:119], a[208:211], a[240:243], v[116:119]\n" \
"s_addk_i32 s84, 0x1c00\n" \
M_"v[120:123], a[144:147], a[180:183], 0\n" \
B_"v23, s[64:67], s84 offen offset:1024 lds\n" \
M_"v[120:123], a[208:211], a[244:247], v[120:123]\n" \
".fill (L_pb + 44768 - .) / 4, 4, 0xBF800000\n" \
"L_pp11:\n" \
"s_cbranch_execz L_pret\n" \
"s_addk_i32 s84, 0x1c00\n" \
M_"v[124:127], a[144:147], a[184:187], 0\n" \
B_"v22, s[64:67], s84 offen offset:2048 lds\n" \
"s_mov_b64 exec, 0\n" \
"s_swappc_b64 s[88:89], s[86:87]\n" \
"s_mov_b64 exec, -1\n" \
M_"v[124:127], a[208:211], a[248:251], v[124:127]\n" \
"s_addk_i32 s84, 0x1c00\n" \
M_"v[128:131], a[144:147], a[188:191], 0\n" \
B_"v23, s[64:67], s84 offen offset:3072 lds\n" \
M_"v[128:131], a[208:211], a[252:255], v[128:131]\n" \
R_"a[144:147], v28 offset:32768\n" \
"v_cmp_gt_u32_e64 vcc, s35, v20\n" \
M_"v[144:147], a[148:151], a[188:191], 0\n" \
R_"a[208:211], v29 offset:32768\n" \
"v_lshrrev_b32_e64 v215, 1, v212\n" \
M_"v[144:147], a[212:215], a[252:255], v[144:147]\n" \
"s_add_u32 m0, s31, 0x4000\n" \
"s_mov_b32 s84, 0x100000\n" \
M_"v[140:143], a[148:151], a[184:187], 0\n" \
B_"v26, s[72:75], s84 offen offset:0 lds\n" \
M_"v[140:143], a[212:215], a[248:251], v[140:143]\n" \
X_"a[230:231], v33 offset:41984\n" \
"s_and_b32 s4, s2, 7\n" \
M_"v[136:139], a[148:151], a[180:183], 0\n" \
"s_addk_i32 s84, 0x7c00\n" \
M_"v[136:139], a[212:215], a[244:247], v[136:139]\n" \
B_"v26, s[72:75], s84 offen offset:1024 lds\n" \
M_"v[132:135], a[148:151], a[176:179], 0\n" \
X_"a[168:169], v34 offset:32768\n" \
"v_cndmask_b32_e64 v194, v219, v214, vcc\n" \
M_"v[132:135], a[212:215], a[240:243], v[132:135]\n" \
R_"a[148:151], v28 offset:34816\n" \
"v_lshlrev_b32_e64 v214, 7, v212\n" \
M_"v[148:151], a[152:155], a[176:179], 0\n" \
R_"a[212:215], v29 offset:34816\n" \
"v_lshrrev_b32_e64 v212, 2, v212\n" \
M_"v[148:151], a[216:219], a[240:243], v[148:151]\n" \
X_"a[170:171], v34 offset:33792\n" \
"v_and_b32_e64 v212, 2, v212\n" \
M_"v[152:155], a[152:155], a[180:183], 0\n" \
X_"a[232:233], v34 offset:40960\n" \
"v_and_or_b32 v212, v215, 1, v212\n" \
M_"v[152:155], a[216:219], a[244:247], v[152:155]\n" \
X_"a[234:235], v34 offset:41984\n" \
"s_lshl_b32 s5, s4, 2\n" \
M_"v[156:159], a[152:155], a[184:187], 0\n" \
X_"a[172:173], v35 offset:32768\n" \
"s_bfe_u32 s38, s2, 0x20003\n" \
M_"v[156:159], a[216:219], a[248:251], v[156:159]\n" \
X_"a[174:175], v35 offset:33792\n" \
"v_lshlrev_b32_e32 v212, 5, v212\n" \
M_"v[160:163], a[152:155], a[188:191], 0\n" \
X_"a[236:237], v35 offset:40960\n" \
"s_movk_i32 s35, 0x60\n" \
M_"v[160:163], a[216:219], a[252:255], v[160:163]\n" \
R_"a[152:155], v28 offset:36864\n" \
"s_or_b32 s5, s5, s38\n" \
M_"v[176:179], a[156:159], a[188:191], 0\n" \
R_"a[216:219], v29 offset:36864\n" \
"v_bitop3_b32 v212, v212, s35, v214 bitop3:0x36\n" \
M_"v[176:179], a[220:223], a[252:255], v[176:179]\n" \
X_"a[238:239], v35 offset:41984\n" \
"s_lshr_b32 s36, s2, 3\n" \
M_"v[172:175], a[156:159], a[184:187], 0\n" \
"v_add_u32_e32 v212, s34, v212\n" \
"s_mul_i32 s34, s5, 0x12000\n" \
M_"v[172:175], a[220:223], a[248:251], v[172:175]\n" \
"s_or_b32 s40, s34, 0x6000000\n" \
"s_mul_i32 s34, s36, 0x90000\n" \
M_"v[168:171], a[156:159], a[180:183], 0\n" \
"s_lshl_b32 s36, s3, 14\n" \
"s_add_i32 s34, s34, s36\n" \
M_"v[168:171], a[220:223], a[244:247], v[168:171]\n" \
"s_lshl_b32 s38, s5, 8\n" \
"s_add_i32 s41, s34, 0x4240000\n" \
M_"v[164:167], a[156:159], a[176:179], 0\n" \
"s_mul_i32 s34, s5, 36\n" \
"s_add_i32 s5, s7, s40\n" \
M_"v[164:167], a[220:223], a[240:243], v[164:167]\n" \
R_"a[156:159], v28 offset:38912\n" \
R_"a[220:223], v29 offset:38912\n" \
"s_waitcnt lgkmcnt(15)\n" \
M_"a[0:3], a[128:131], a[160:163], a[0:3]\n" \
"s_addk_i32 s84, 0x7c00\n" \
M_"a[0:3], a[192:195], a[224:227], a[0:3]\n" \
B_"v27, s[72:75], s84 offen offset:2048 lds\n" \
M_"a[4:7], a[128:131], a[164:167], a[4:7]\n" \
X_"a[176:177], v36 offset:32768\n" \
"s_addk_i32 s5, 0x2000\n" \
"s_waitcnt lgkmcnt(15)\n" \
M_"a[4:7], a[192:195], a[228:231], a[4:7]\n" \
"s_addk_i32 s84, 0x7c00\n" \
"s_waitcnt lgkmcnt(11)\n" \
M_"a[8:11], a[128:131], a[168:171], a[8:11]\n" \
B_"v27, s[72:75], s84 offen offset:3072 lds\n" \
"s_waitcnt lgkmcnt(9)\n" \
M_"a[8:11], a[192:195], a[232:235], a[8:11]\n" \
X_"a[178:179], v36 offset:33792\n" \
"s_mov_b32 s37, 2.0\n" \
"s_waitcnt lgkmcnt(8)\n" \
M_"a[12:15], a[128:131], a[172:175], a[12:15]\n" \
X_"a[240:241], v36 offset:40960\n" \
"s_or_b32 s38, s38, 0x5be8000\n" \
"s_waitcnt lgkmcnt(5)\n" \
M_"a[12:15], a[192:195], a[236:239], a[12:15]\n" \
"s_cmp_lg_u32 s3, 0\n" \
"s_cselect_b32 s36, s37, s5\n" \
M_"a[28:31], a[132:135], a[172:175], a[28:31]\n" \
"s_lshl_b32 s5, s6, 2\n" \
"s_or_b32 s5, s38, s5\n" \
M_"a[28:31], a[196:199], a[236:239], a[28:31]\n" \
X_"a[242:243], v36 offset:41984\n" \
"s_lshl_b32 s35, s3, 10\n" \
M_"a[24:27], a[132:135], a[168:171], a[24:27]\n" \
"s_cmpk_lt_u32 s3, 3\n" \
"s_cselect_b32 s37, s37, s5\n" \
M_"a[24:27], a[196:199], a[232:235], a[24:27]\n" \
"s_lshl_b32 s5, s4, 16\n" \
"s_lshl_b32 s4, s4, 11\n" \
M_"a[20:23], a[132:135], a[164:167], a[20:23]\n" \
"v_cndmask_b32_e32 v198, v213, v212, vcc\n" \
"v_or_b32_e32 v212, s35, v20\n" \
M_"a[20:23], a[196:199], a[228:231], a[20:23]\n" \
X_"a[180:181], v37 offset:32768\n" \
"s_or_b32 s39, s40, s35\n" \
M_"a[16:19], a[132:135], a[160:163], a[16:19]\n" \
"s_or_b32 s55, s41, s4\n" \
"v_or_b32_e32 v199, 0x22000, v212\n" \
M_"a[16:19], a[196:199], a[224:227], a[16:19]\n" \
"s_add_i32 s35, s39, s7\n" \
"s_add_i32 s54, s41, s5\n" \
M_"a[32:35], a[136:139], a[160:163], a[32:35]\n" \
"s_mov_b32 m0, s32\n" \
X_"a[182:183], v37 offset:33792\n" \
M_"a[32:35], a[200:203], a[224:227], a[32:35]\n" \
"s_mov_b32 exec_lo, 0x0\n" \
"s_mov_b32 exec_hi, 0xff\n" \
"global_load_lds_dword v[184:185], off\n" \
"s_mov_b64 exec, -1\n" \
M_"a[36:39], a[136:139], a[164:167], a[36:39]\n" \
"s_add_i32 s55, s55, 0x80000\n" \
"v_mov_b32_e32 v215, 0xffff\n" \
M_"a[36:39], a[200:203], a[228:231], a[36:39]\n" \
X_"a[244:245], v37 offset:40960\n" \
"s_and_b32 s4, s3, 1\n" \
M_"a[40:43], a[136:139], a[168:171], a[40:43]\n" \
"s_lshl_b32 s5, s4, 4\n" \
"v_lshrrev_b32_e32 v212, 4, v20\n" \
M_"a[40:43], a[200:203], a[232:235], a[40:43]\n" \
"s_mov_b32 m0, s32\n" \
"v_lshrrev_b32_e32 v218, 6, v20\n" \
M_"a[44:47], a[136:139], a[172:175], a[44:47]\n" \
"s_mov_b32 exec_lo, 0xffff0000\n" \
"s_mov_b32 exec_hi, 0x0\n" \
"global_load_lds_dword v[186:187], off\n" \
"s_mov_b64 exec, -1\n" \
M_"a[44:47], a[200:203], a[236:239], a[44:47]\n" \
X_"a[246:247], v37 offset:41984\n" \
"v_and_b32_e64 v219, 12, v218\n" \
M_"a[60:63], a[140:143], a[172:175], a[60:63]\n" \
"v_and_or_b32 v214, v212, 15, s5\n" \
"v_cmp_gt_u32_e64 vcc, v219, v214\n" \
M_"a[60:63], a[204:207], a[236:239], a[60:63]\n" \
"v_mov_b32_e32 v220, 0xffff0000\n" \
"s_lshl_b32 s5, s2, 2\n" \
M_"a[56:59], a[140:143], a[168:171], a[56:59]\n" \
X_"a[184:185], v38 offset:32768\n" \
"v_cndmask_b32_e64 v217, v215, 0, vcc\n" \
M_"a[56:59], a[204:207], a[232:235], a[56:59]\n" \
"v_cmp_lt_u32_e32 vcc, v219, v214\n" \
"v_mov_b32_e64 v213, s23\n" \
M_"a[52:55], a[140:143], a[164:167], a[52:55]\n" \
"s_or_b32 s5, s5, s3\n" \
"v_cndmask_b32_e32 v221, 0, v220, vcc\n" \
M_"a[52:55], a[204:207], a[228:231], a[52:55]\n" \
X_"a[186:187], v38 offset:33792\n" \
"v_or_b32_e64 v12, v217, v221\n" \
M_"a[48:51], a[140:143], a[160:163], a[48:51]\n" \
"v_or_b32_e32 v217, 2, v219\n" \
"v_cmp_gt_u32_e32 vcc, v217, v214\n" \
M_"a[48:51], a[204:207], a[224:227], a[48:51]\n" \
"v_mov_b32_e32 v216, s25\n" \
"s_movk_i32 s40, 0xf400\n" \
M_"a[64:67], a[144:147], a[160:163], a[64:67]\n" \
X_"a[248:249], v38 offset:40960\n" \
"v_cndmask_b32_e64 v221, v215, 0, vcc\n" \
M_"a[64:67], a[208:211], a[224:227], a[64:67]\n" \
"v_cmp_lt_u32_e32 vcc, v217, v214\n" \
"s_lshl_b32 s4, s4, 6\n" \
M_"a[68:71], a[144:147], a[164:167], a[68:71]\n" \
"v_cndmask_b32_e32 v217, 0, v220, vcc\n" \
"v_or_b32_e32 v13, v221, v217\n" \
M_"a[68:71], a[208:211], a[228:231], a[68:71]\n" \
X_"a[250:251], v38 offset:41984\n" \
"v_or_b32_e64 v217, 16, v219\n" \
M_"a[72:75], a[144:147], a[168:171], a[72:75]\n" \
"v_cmp_gt_u32_e32 vcc, v217, v214\n" \
"s_nop 1\n" \
"v_cndmask_b32_e64 v221, v215, 0, vcc\n" \
M_"a[72:75], a[208:211], a[232:235], a[72:75]\n" \
"v_cmp_lt_u32_e32 vcc, v217, v214\n" \
"s_nop 1\n" \
"v_cndmask_b32_e64 v217, 0, v220, vcc\n" \
M_"a[76:79], a[144:147], a[172:175], a[76:79]\n" \
"v_or_b32_e32 v14, v221, v217\n" \
"v_or_b32_e32 v217, 18, v219\n" \
M_"a[76:79], a[208:211], a[236:239], a[76:79]\n" \
X_"a[188:189], v39 offset:32768\n" \
"v_cmp_gt_u32_e64 vcc, v217, v214\n" \
M_"a[92:95], a[148:151], a[172:175], a[92:95]\n" \
"s_nop 0\n" \
"v_cndmask_b32_e64 v215, v215, 0, vcc\n" \
"v_cmp_lt_u32_e32 vcc, v217, v214\n" \
M_"a[92:95], a[212:215], a[236:239], a[92:95]\n" \
"s_nop 0\n" \
"v_cndmask_b32_e32 v214, 0, v220, vcc\n" \
"v_or_b32_e64 v15, v215, v214\n" \
M_"a[88:91], a[148:151], a[168:171], a[88:91]\n" \
X_"a[190:191], v39 offset:33792\n" \
"v_mad_u64_u32 v[214:215], s[6:7], s5, 42, v[212:213]\n" \
M_"a[88:91], a[212:215], a[232:235], a[88:91]\n" \
"s_movk_i32 s5, 0x1440\n" \
"v_cmp_gt_u32_e32 vcc, s5, v214\n" \
M_"a[84:87], a[148:151], a[164:167], a[84:87]\n" \
"v_min_u32_e32 v217, 0xa43f, v214\n" \
"v_mov_b32_e64 v214, s24\n" \
M_"a[84:87], a[212:215], a[228:231], a[84:87]\n" \
X_"a[252:253], v39 offset:40960\n" \
"v_cndmask_b32_e64 v215, v216, v213, vcc\n" \
M_"a[80:83], a[148:151], a[160:163], a[80:83]\n" \
"v_mov_b32_e32 v213, s22\n" \
"s_movk_i32 s6, 0xffee\n" \
M_"a[80:83], a[212:215], a[224:227], a[80:83]\n" \
"v_cndmask_b32_e64 v214, v214, v213, vcc\n" \
"v_add_u32_e32 v213, 0xffffebc0, v217\n" \
M_"a[96:99], a[152:155], a[160:163], a[96:99]\n" \
X_"a[254:255], v39 offset:41984\n" \
"s_lshl_b32 s5, s2, 5\n" \
M_"a[96:99], a[216:219], a[224:227], a[96:99]\n" \
"v_cndmask_b32_e32 v213, v213, v217, vcc\n" \
"v_lshlrev_b32_e64 v216, 7, v213\n" \
M_"a[100:103], a[152:155], a[164:167], a[100:103]\n" \
"v_mul_u32_u24_e32 v213, 0xe39, v212\n" \
"v_mov_b32_e64 v217, 0\n" \
M_"a[100:103], a[216:219], a[228:231], a[100:103]\n" \
"v_lshrrev_b32_e64 v213, 16, v213\n" \
"v_lshl_add_u64 v[16:17], v[214:215], 0, v[216:217]\n" \
M_"a[104:107], a[152:155], a[168:171], a[104:107]\n" \
"v_mad_i32_i24 v214, v213, s6, v212\n" \
"s_add_i32 s5, s5, 32\n" \
M_"a[104:107], a[216:219], a[232:235], a[104:107]\n" \
"v_lshlrev_b32_e32 v215, 4, v214\n" \
"v_add_u32_e64 v216, s5, v215\n" \
M_"a[108:111], a[152:155], a[172:175], a[108:111]\n" \
"v_and_b32_e32 v216, 0xf0, v216\n" \
"v_cmp_gt_u32_e64 vcc, 16, v214\n" \
M_"a[108:111], a[216:219], a[236:239], a[108:111]\n" \
"s_movk_i32 s7, 0xf800\n" \
"v_cndmask_b32_e32 v214, v215, v216, vcc\n" \
"s_waitcnt lgkmcnt(15)\n" \
M_"a[124:127], a[156:159], a[172:175], a[124:127]\n" \
"v_or_b32_e32 v215, 64, v212\n" \
"v_mul_u32_u24_e32 v216, 0xe39, v215\n" \
M_"a[124:127], a[220:223], a[236:239], a[124:127]\n" \
"v_lshrrev_b32_e64 v216, 16, v216\n" \
"v_mad_i32_i24 v215, v216, s6, v215\n" \
M_"a[120:123], a[156:159], a[168:171], a[120:123]\n" \
"v_lshlrev_b32_e32 v217, 4, v215\n" \
"v_add_u32_e32 v220, s5, v217\n" \
M_"a[120:123], a[220:223], a[232:235], a[120:123]\n" \
"v_and_b32_e32 v220, 0xf0, v220\n" \
"v_cmp_gt_u32_e64 vcc, 16, v215\n" \
M_"a[116:119], a[156:159], a[164:167], a[116:119]\n" \
"s_nop 0\n" \
"v_cndmask_b32_e32 v215, v217, v220, vcc\n" \
"v_or_b32_e32 v217, 0x80, v212\n" \
M_"a[116:119], a[220:223], a[228:231], a[116:119]\n" \
"v_mul_u32_u24_e32 v220, 0xe39, v217\n" \
"v_lshrrev_b32_e64 v220, 16, v220\n" \
M_"a[112:115], a[156:159], a[160:163], a[112:115]\n" \
"v_mad_i32_i24 v217, v220, s6, v217\n" \
"v_lshlrev_b32_e64 v221, 4, v217\n" \
M_"a[112:115], a[220:223], a[224:227], a[112:115]\n" \
"s_waitcnt vmcnt(20)\n" \
"v_readfirstlane_b32 s62, v180\n" \
"s_nop 3\n" \
"s_add_u32 s62, s62, 0x9E3779B1\n" \
"s_waitcnt vmcnt(2) lgkmcnt(0)\n" \
"s_barrier\n" \
M_"v[52:55], a[128:131], a[176:179], v[52:55]\n" \
"s_add_u32 m0, s30, 0x8000\n" \
"s_movk_i32 s84, 0x180\n" \
M_"v[52:55], a[192:195], a[240:243], v[52:55]\n" \
B_"v22, s[64:67], s84 offen offset:0 lds\n" \
M_"v[56:59], a[128:131], a[180:183], v[56:59]\n" \
X_"a[160:161], v32 offset:0\n" \
"v_add_u32_e64 v222, s5, v221\n" \
M_"v[56:59], a[192:195], a[244:247], v[56:59]\n" \
"s_movk_i32 s84, 0x1d80\n" \
M_"v[60:63], a[128:131], a[184:187], v[60:63]\n" \
B_"v23, s[64:67], s84 offen offset:1024 lds\n" \
M_"v[60:63], a[192:195], a[248:251], v[60:63]\n" \
X_"a[162:163], v32 offset:1024\n" \
"v_and_b32_e32 v222, 0xf0, v222\n" \
M_"v[64:67], a[128:131], a[188:191], v[64:67]\n" \
X_"a[224:225], v32 offset:8192\n" \
"v_cmp_gt_u32_e32 vcc, 16, v217\n" \
M_"v[64:67], a[192:195], a[252:255], v[64:67]\n" \
R_"a[128:131], v28 offset:16384\n" \
"v_cndmask_b32_e64 v217, v221, v222, vcc\n" \
M_"v[80:83], a[132:135], a[188:191], v[80:83]\n" \
R_"a[192:195], v29 offset:16384\n" \
"v_or_b32_e32 v221, 0xc0, v212\n" \
M_"v[80:83], a[196:199], a[252:255], v[80:83]\n" \
"s_movk_i32 s84, 0x3980\n" \
M_"v[76:79], a[132:135], a[184:187], v[76:79]\n" \
B_"v22, s[64:67], s84 offen offset:2048 lds\n" \
M_"v[76:79], a[196:199], a[248:251], v[76:79]\n" \
"s_movk_i32 s84, 0x5580\n" \
M_"v[72:75], a[132:135], a[180:183], v[72:75]\n" \
B_"v23, s[64:67], s84 offen offset:3072 lds\n" \
M_"v[72:75], a[196:199], a[244:247], v[72:75]\n" \
"s_add_u32 m0, s31, 0x8000\n" \
"s_mov_b32 s84, 0x180000\n" \
M_"v[68:71], a[132:135], a[176:179], v[68:71]\n" \
B_"v26, s[68:71], s84 offen offset:0 lds\n" \
M_"v[68:71], a[196:199], a[240:243], v[68:71]\n" \
R_"a[132:135], v28 offset:18432\n" \
"v_mul_u32_u24_e32 v222, 0xe39, v221\n" \
M_"v[84:87], a[136:139], a[176:179], v[84:87]\n" \
R_"a[196:199], v29 offset:18432\n" \
"v_lshrrev_b32_e64 v222, 16, v222\n" \
M_"v[84:87], a[200:203], a[240:243], v[84:87]\n" \
"s_addk_i32 s84, 0x7c00\n" \
M_"v[88:91], a[136:139], a[180:183], v[88:91]\n" \
B_"v26, s[68:71], s84 offen offset:1024 lds\n" \
M_"v[88:91], a[200:203], a[244:247], v[88:91]\n" \
X_"a[226:227], v32 offset:9216\n" \
"v_mad_i32_i24 v221, v222, s6, v221\n" \
M_"v[92:95], a[136:139], a[184:187], v[92:95]\n" \
"s_addk_i32 s84, 0x7c00\n" \
M_"v[92:95], a[200:203], a[248:251], v[92:95]\n" \
B_"v27, s[68:71], s84 offen offset:2048 lds\n" \
M_"v[96:99], a[136:139], a[188:191], v[96:99]\n" \
X_"a[164:165], v33 offset:0\n" \
"v_lshlrev_b32_e64 v223, 4, v221\n" \
M_"v[96:99], a[200:203], a[252:255], v[96:99]\n" \
R_"a[136:139], v28 offset:20480\n" \
"v_add_u32_e64 v224, s5, v223\n" \
M_"v[112:115], a[140:143], a[188:191], v[112:115]\n" \
R_"a[200:203], v29 offset:20480\n" \
"v_and_b32_e32 v224, 0xf0, v224\n" \
M_"v[112:115], a[204:207], a[252:255], v[112:115]\n" \
"s_addk_i32 s84, 0x7c00\n" \
M_"v[108:111], a[140:143], a[184:187], v[108:111]\n" \
B_"v27, s[68:71], s84 offen offset:3072 lds\n" \
M_"v[108:111], a[204:207], a[248:251], v[108:111]\n" \
"s_add_u32 m0, s30, 0x9000\n" \
"s_mov_b32 s84, 0x8180\n" \
M_"v[104:107], a[140:143], a[180:183], v[104:107]\n" \
B_"v22, s[64:67], s84 offen offset:0 lds\n" \
M_"v[104:107], a[204:207], a[244:247], v[104:107]\n" \
X_"a[166:167], v33 offset:1024\n" \
"v_cmp_gt_u32_e32 vcc, 16, v221\n" \
M_"v[100:103], a[140:143], a[176:179], v[100:103]\n" \
X_"a[228:229], v33 offset:8192\n" \
"v_cndmask_b32_e64 v221, v223, v224, vcc\n" \
M_"v[100:103], a[204:207], a[240:243], v[100:103]\n" \
R_"a[140:143], v28 offset:22528\n" \
"v_or_b32_e32 v223, 0x100, v212\n" \
M_"v[116:119], a[144:147], a[176:179], v[116:119]\n" \
R_"a[204:207], v29 offset:22528\n" \
"v_mul_u32_u24_e32 v224, 0xe39, v223\n" \
M_"v[116:119], a[208:211], a[240:243], v[116:119]\n" \
"s_addk_i32 s84, 0x1c00\n" \
M_"v[120:123], a[144:147], a[180:183], v[120:123]\n" \
B_"v23, s[64:67], s84 offen offset:1024 lds\n" \
M_"v[120:123], a[208:211], a[244:247], v[120:123]\n" \
"s_addk_i32 s84, 0x1c00\n" \
M_"v[124:127], a[144:147], a[184:187], v[124:127]\n" \
B_"v22, s[64:67], s84 offen offset:2048 lds\n" \
M_"v[124:127], a[208:211], a[248:251], v[124:127]\n" \
"s_addk_i32 s84, 0x1c00\n" \
M_"v[128:131], a[144:147], a[188:191], v[128:131]\n" \
B_"v23, s[64:67], s84 offen offset:3072 lds\n" \
M_"v[128:131], a[208:211], a[252:255], v[128:131]\n" \
R_"a[144:147], v28 offset:0\n" \
"v_lshrrev_b32_e32 v224, 16, v224\n" \
M_"v[144:147], a[148:151], a[188:191], v[144:147]\n" \
R_"a[208:211], v29 offset:0\n" \
"v_mad_i32_i24 v223, v224, s6, v223\n" \
M_"v[144:147], a[212:215], a[252:255], v[144:147]\n" \
"s_add_u32 m0, s31, 0xc000\n" \
"s_mov_b32 s84, 0x180000\n" \
M_"v[140:143], a[148:151], a[184:187], v[140:143]\n" \
B_"v26, s[72:75], s84 offen offset:0 lds\n" \
M_"v[140:143], a[212:215], a[248:251], v[140:143]\n" \
X_"a[230:231], v33 offset:9216\n" \
"v_lshlrev_b32_e64 v225, 4, v223\n" \
M_"v[136:139], a[148:151], a[180:183], v[136:139]\n" \
"s_addk_i32 s84, 0x7c00\n" \
M_"v[136:139], a[212:215], a[244:247], v[136:139]\n" \
B_"v26, s[72:75], s84 offen offset:1024 lds\n" \
M_"v[132:135], a[148:151], a[176:179], v[132:135]\n" \
X_"a[168:169], v34 offset:0\n" \
"v_add_u32_e32 v226, s5, v225\n" \
M_"v[132:135], a[212:215], a[240:243], v[132:135]\n" \
R_"a[148:151], v28 offset:2048\n" \
"v_and_b32_e32 v226, 0xf0, v226\n" \
M_"v[148:151], a[152:155], a[176:179], v[148:151]\n" \
R_"a[212:215], v29 offset:2048\n" \
"v_cmp_gt_u32_e64 vcc, 16, v223\n" \
M_"v[148:151], a[216:219], a[240:243], v[148:151]\n" \
X_"a[170:171], v34 offset:1024\n" \
"v_cndmask_b32_e64 v223, v225, v226, vcc\n" \
M_"v[152:155], a[152:155], a[180:183], v[152:155]\n" \
X_"a[232:233], v34 offset:8192\n" \
"v_or_b32_e32 v225, 0x140, v212\n" \
M_"v[152:155], a[216:219], a[244:247], v[152:155]\n" \
X_"a[234:235], v34 offset:9216\n" \
"v_mul_u32_u24_e32 v226, 0xe39, v225\n" \
M_"v[156:159], a[152:155], a[184:187], v[156:159]\n" \
X_"a[172:173], v35 offset:0\n" \
"v_lshrrev_b32_e64 v226, 16, v226\n" \
M_"v[156:159], a[216:219], a[248:251], v[156:159]\n" \
X_"a[174:175], v35 offset:1024\n" \
"v_mad_i32_i24 v225, v226, s6, v225\n" \
M_"v[160:163], a[152:155], a[188:191], v[160:163]\n" \
X_"a[236:237], v35 offset:8192\n" \
"v_lshlrev_b32_e64 v227, 4, v225\n" \
M_"v[160:163], a[216:219], a[252:255], v[160:163]\n" \
R_"a[152:155], v28 offset:4096\n" \
"v_add_u32_e64 v228, s5, v227\n" \
M_"v[176:179], a[156:159], a[188:191], v[176:179]\n" \
R_"a[216:219], v29 offset:4096\n" \
"v_and_b32_e32 v228, 0xf0, v228\n" \
M_"v[176:179], a[220:223], a[252:255], v[176:179]\n" \
X_"a[238:239], v35 offset:9216\n" \
"v_cmp_gt_u32_e64 vcc, 16, v225\n" \
M_"v[172:175], a[156:159], a[184:187], v[172:175]\n" \
"s_nop 0\n" \
"v_cndmask_b32_e32 v225, v227, v228, vcc\n" \
"v_or_b32_e32 v227, 0x180, v212\n" \
M_"v[172:175], a[220:223], a[248:251], v[172:175]\n" \
"v_mul_u32_u24_e32 v228, 0xe39, v227\n" \
"v_lshrrev_b32_e64 v228, 16, v228\n" \
M_"v[168:171], a[156:159], a[180:183], v[168:171]\n" \
"v_mad_i32_i24 v227, v228, s6, v227\n" \
"v_lshlrev_b32_e64 v229, 4, v227\n" \
M_"v[168:171], a[220:223], a[244:247], v[168:171]\n" \
"v_add_u32_e64 v230, s5, v229\n" \
"v_and_b32_e32 v230, 0xf0, v230\n" \
M_"v[164:167], a[156:159], a[176:179], v[164:167]\n" \
"v_cmp_gt_u32_e32 vcc, 16, v227\n" \
"s_nop 1\n" \
"v_cndmask_b32_e64 v227, v229, v230, vcc\n" \
M_"v[164:167], a[220:223], a[240:243], v[164:167]\n" \
R_"a[156:159], v28 offset:6144\n" \
R_"a[220:223], v29 offset:6144\n" \
"s_waitcnt lgkmcnt(15)\n" \
M_"a[0:3], a[128:131], a[160:163], a[0:3]\n" \
"s_addk_i32 s84, 0x7c00\n" \
M_"a[0:3], a[192:195], a[224:227], a[0:3]\n" \
B_"v27, s[72:75], s84 offen offset:2048 lds\n" \
M_"a[4:7], a[128:131], a[164:167], a[4:7]\n" \
X_"a[176:177], v36 offset:0\n" \
"v_or_b32_e32 v229, 0x1c0, v212\n" \
"s_waitcnt lgkmcnt(15)\n" \
M_"a[4:7], a[192:195], a[228:231], a[4:7]\n" \
"s_addk_i32 s84, 0x7c00\n" \
"s_waitcnt lgkmcnt(11)\n" \
M_"a[8:11], a[128:131], a[168:171], a[8:11]\n" \
B_"v27, s[72:75], s84 offen offset:3072 lds\n" \
"s_waitcnt lgkmcnt(9)\n" \
M_"a[8:11], a[192:195], a[232:235], a[8:11]\n" \
X_"a[178:179], v36 offset:1024\n" \
"v_mul_u32_u24_e32 v230, 0xe39, v229\n" \
"s_waitcnt lgkmcnt(8)\n" \
M_"a[12:15], a[128:131], a[172:175], a[12:15]\n" \
X_"a[240:241], v36 offset:8192\n" \
"v_lshrrev_b32_e32 v230, 16, v230\n" \
"s_waitcnt lgkmcnt(5)\n" \
M_"a[12:15], a[192:195], a[236:239], a[12:15]\n" \
"v_mad_i32_i24 v229, v230, s6, v229\n" \
"v_lshlrev_b32_e32 v231, 4, v229\n" \
M_"a[28:31], a[132:135], a[172:175], a[28:31]\n" \
"v_add_u32_e64 v232, s5, v231\n" \
"v_and_b32_e32 v232, 0xf0, v232\n" \
M_"a[28:31], a[196:199], a[236:239], a[28:31]\n" \
X_"a[242:243], v36 offset:9216\n" \
"v_cmp_gt_u32_e64 vcc, 16, v229\n" \
M_"a[24:27], a[132:135], a[168:171], a[24:27]\n" \
B_"v[200:203], v192, s[76:79], s34 offen offset:0\n" \
M_"a[24:27], a[196:199], a[232:235], a[24:27]\n" \
"v_cndmask_b32_e64 v229, v231, v232, vcc\n" \
"v_or_b32_e32 v231, 0x200, v212\n" \
M_"a[20:23], a[132:135], a[164:167], a[20:23]\n" \
"v_mul_u32_u24_e32 v232, 0xe39, v231\n" \
"v_lshrrev_b32_e64 v232, 16, v232\n" \
M_"a[20:23], a[196:199], a[228:231], a[20:23]\n" \
X_"a[180:181], v37 offset:0\n" \
"v_mad_i32_i24 v231, v232, s6, v231\n" \
M_"a[16:19], a[132:135], a[160:163], a[16:19]\n" \
"v_lshlrev_b32_e32 v233, 4, v231\n" \
"v_add_u32_e32 v234, s5, v233\n" \
M_"a[16:19], a[196:199], a[224:227], a[16:19]\n" \
"s_movk_i32 s5, 0x480\n" \
"v_mad_u32_u24 v0, v213, s5, v214\n" \
M_"a[32:35], a[136:139], a[160:163], a[32:35]\n" \
B_"v[204:207], v193, s[76:79], s34 offen offset:0\n" \
M_"a[32:35], a[200:203], a[224:227], a[32:35]\n" \
"s_mov_b32 m0, s32\n" \
X_"a[182:183], v37 offset:1024\n" \
M_"a[36:39], a[136:139], a[164:167], a[36:39]\n" \
"s_mov_b32 exec_lo, 0x0\n" \
"s_mov_b32 exec_hi, 0xff00\n" \
"global_load_lds_dword v[184:185], off\n" \
"s_mov_b64 exec, -1\n" \
M_"a[36:39], a[200:203], a[228:231], a[36:39]\n" \
X_"a[244:245], v37 offset:8192\n" \
"v_mul_u32_u24_e32 v213, 0x480, v216\n" \
M_"a[40:43], a[136:139], a[168:171], a[40:43]\n" \
"s_movk_i32 s6, 0xfc00\n" \
"s_nop 0\n" \
"v_add3_u32 v1, v213, v215, s6\n" \
M_"a[40:43], a[200:203], a[232:235], a[40:43]\n" \
"buffer_load_dword v208, v194, s[76:79], s34 offen offset:32\n" \
M_"a[44:47], a[136:139], a[172:175], a[44:47]\n" \
X_"a[246:247], v37 offset:9216\n" \
"v_mul_u32_u24_e32 v213, 0x480, v220\n" \
M_"a[44:47], a[200:203], a[236:239], a[44:47]\n" \
"v_add3_u32 v2, v213, v217, s7\n" \
"v_mul_u32_u24_e32 v213, 0x480, v222\n" \
M_"a[60:63], a[140:143], a[172:175], a[60:63]\n" \
"v_add3_u32 v3, v213, v221, s40\n" \
"v_mul_u32_u24_e32 v213, 0x480, v226\n" \
M_"a[60:63], a[204:207], a[236:239], a[60:63]\n" \
"v_add3_u32 v5, v213, v225, s6\n" \
"v_mul_u32_u24_e32 v213, 0x480, v228\n" \
M_"a[56:59], a[140:143], a[168:171], a[56:59]\n" \
"buffer_load_dword v209, v194, s[76:79], s34 offen offset:1184\n" \
M_"a[56:59], a[204:207], a[232:235], a[56:59]\n" \
X_"a[184:185], v38 offset:0\n" \
"v_add3_u32 v6, v213, v227, s7\n" \
M_"a[52:55], a[140:143], a[164:167], a[52:55]\n" \
"v_mul_u32_u24_e32 v213, 0x480, v230\n" \
"v_add3_u32 v7, v213, v229, s40\n" \
M_"a[52:55], a[204:207], a[228:231], a[52:55]\n" \
X_"a[186:187], v38 offset:1024\n" \
"s_lshl_b32 s7, s3, 3\n" \
M_"a[48:51], a[140:143], a[160:163], a[48:51]\n" \
"s_lshl_b32 s40, s3, 1\n" \
"s_and_b32 s7, s7, 16\n" \
M_"a[48:51], a[204:207], a[224:227], a[48:51]\n" \
"s_and_b32 s40, s40, 2\n" \
"s_and_b32 s6, s2, 7\n" \
M_"a[64:67], a[144:147], a[160:163], a[64:67]\n" \
"buffer_load_dword v211, v194, s[76:79], s34 offen offset:2336\n" \
M_"a[64:67], a[208:211], a[224:227], a[64:67]\n" \
X_"a[248:249], v38 offset:8192\n" \
"v_bfe_u32 v213, v20, 7, 1\n" \
M_"a[68:71], a[144:147], a[164:167], a[68:71]\n" \
"s_or_b32 s7, s40, s7\n" \
"v_and_b32_e32 v234, 0xf0, v234\n" \
M_"a[68:71], a[208:211], a[228:231], a[68:71]\n" \
X_"a[250:251], v38 offset:9216\n" \
"v_cmp_gt_u32_e64 vcc, 16, v231\n" \
M_"a[72:75], a[144:147], a[168:171], a[72:75]\n" \
"v_or3_b32 v213, s7, v213, v219\n" \
"v_and_b32_e64 v214, 32, v20\n" \
M_"a[72:75], a[208:211], a[232:235], a[72:75]\n" \
"s_mov_b32 s76, s20\n" \
"s_and_b32 s77, s21, 0xffff\n" \
M_"a[76:79], a[144:147], a[172:175], a[76:79]\n" \
"s_mov_b32 s78, 0x480\n" \
"s_lshl_b32 s6, s6, 5\n" \
M_"a[76:79], a[208:211], a[236:239], a[76:79]\n" \
"buffer_load_ushort v210, v195, s[76:79], 0 offen\n" \
M_"a[92:95], a[148:151], a[172:175], a[92:95]\n" \
X_"a[188:189], v39 offset:0\n" \
"v_cndmask_b32_e32 v231, v233, v234, vcc\n" \
".fill (L_pb + 48864 - .) / 4, 4, 0xBF800000\n" \
"L_pp12:\n" \
"s_cbranch_execz L_pret\n" \
M_"a[92:95], a[212:215], a[236:239], a[92:95]\n" \
"v_mul_u32_u24_e32 v213, 18, v213\n" \
"v_mov_b32_e32 v215, 0x100\n" \
M_"a[88:91], a[148:151], a[168:171], a[88:91]\n" \
"s_mov_b32 s76, s8\n" \
"s_and_b32 s77, s9, 0xffff\n" \
M_"a[88:91], a[212:215], a[232:235], a[88:91]\n" \
"s_mov_b32 s78, 0x240000\n" \
X_"a[190:191], v39 offset:1024\n" \
M_"a[84:87], a[148:151], a[164:167], a[84:87]\n" \
"v_mov_b32_e32 v216, s6\n" \
"v_cmp_eq_u32_e64 vcc, 0, v214\n" \
M_"a[84:87], a[212:215], a[228:231], a[84:87]\n" \
X_"a[252:253], v39 offset:8192\n" \
"v_and_or_b32 v213, v218, 1, v213\n" \
M_"a[80:83], a[148:151], a[160:163], a[80:83]\n" \
"v_lshlrev_b32_e32 v212, 3, v212\n" \
"v_cndmask_b32_e32 v214, v215, v216, vcc\n" \
M_"a[80:83], a[212:215], a[224:227], a[80:83]\n" \
"v_mad_u32_u24 v213, v213, s5, v214\n" \
"v_and_or_b32 v213, v20, 16, v213\n" \
M_"a[96:99], a[152:155], a[160:163], a[96:99]\n" \
X_"a[254:255], v39 offset:9216\n" \
"v_add_u32_e32 v9, 0x4800, v213\n" \
M_"a[96:99], a[216:219], a[224:227], a[96:99]\n" \
"v_mul_u32_u24_e32 v213, 0x120, v218\n" \
"v_and_or_b32 v10, v212, 24, v213\n" \
M_"a[100:103], a[152:155], a[164:167], a[100:103]\n" \
"v_lshrrev_b32_e64 v213, 2, v20\n" \
"v_and_or_b32 v213, v213, 60, s4\n" \
M_"a[100:103], a[216:219], a[228:231], a[100:103]\n" \
"v_lshl_or_b32 v212, s3, 9, v212\n" \
"v_mad_u32_u24 v4, v224, s5, v223\n" \
M_"a[104:107], a[152:155], a[168:171], a[104:107]\n" \
"v_mad_u32_u24 v8, v232, s5, v231\n" \
"v_or_b32_e32 v18, 0x27000, v213\n" \
M_"a[104:107], a[216:219], a[232:235], a[104:107]\n" \
"v_or_b32_e32 v19, 0x27800, v212\n" \
"v_mov_b32_e32 v215, 0x1000c0c\n" \
M_"a[108:111], a[152:155], a[172:175], a[108:111]\n" \
"v_mov_b32_e32 v216, 0xc0c0100\n" \
"v_bfe_u32 v214, v20, 4, 1\n" \
M_"a[108:111], a[216:219], a[236:239], a[108:111]\n" \
"v_lshrrev_b32_e32 v212, 8, v20\n" \
"v_cmp_eq_u32_e32 vcc, 0, v214\n" \
"s_waitcnt lgkmcnt(15)\n" \
M_"a[124:127], a[156:159], a[172:175], a[124:127]\n" \
"v_bfe_u32 v213, v20, 4, 4\n" \
"v_mov_b32_e32 v217, 0xc0c0302\n" \
M_"a[124:127], a[220:223], a[236:239], a[124:127]\n" \
"v_cndmask_b32_e32 v215, v215, v216, vcc\n" \
"v_mov_b32_e32 v216, 0x3020c0c\n" \
M_"a[120:123], a[156:159], a[168:171], a[120:123]\n" \
"v_lshl_or_b32 v214, v212, 2, v214\n" \
"v_cndmask_b32_e64 v216, v216, v217, vcc\n" \
M_"a[120:123], a[220:223], a[232:235], a[120:123]\n" \
"v_mov_b32_e32 v217, 0xc0c0c0c\n" \
"v_cmp_gt_u32_e64 vcc, v214, v213\n" \
M_"a[116:119], a[156:159], a[164:167], a[116:119]\n" \
"v_or_b32_e32 v214, 2, v214\n" \
"s_lshr_b32 s4, s2, 5\n" \
M_"a[116:119], a[220:223], a[228:231], a[116:119]\n" \
"v_cndmask_b32_e32 v40, v217, v215, vcc\n" \
"v_cndmask_b32_e32 v42, v215, v217, vcc\n" \
M_"a[112:115], a[156:159], a[160:163], a[112:115]\n" \
"v_cmp_gt_u32_e32 vcc, v214, v213\n" \
"v_and_b32_e32 v214, 16, v20\n" \
"s_waitcnt vmcnt(7) lgkmcnt(0)\n" \
M_"a[112:115], a[220:223], a[224:227], a[112:115]\n" \
"s_barrier\n" \
M_"v[52:55], a[128:131], a[176:179], v[52:55]\n" \
"s_add_u32 m0, s30, 0x0\n" \
"s_movk_i32 s84, 0x200\n" \
M_"v[52:55], a[192:195], a[240:243], v[52:55]\n" \
B_"v22, s[64:67], s84 offen offset:0 lds\n" \
M_"v[56:59], a[128:131], a[180:183], v[56:59]\n" \
X_"a[160:161], v32 offset:32768\n" \
"s_movk_i32 s42, 0x3f80\n" \
M_"v[56:59], a[192:195], a[244:247], v[56:59]\n" \
"s_movk_i32 s84, 0x1e00\n" \
M_"v[60:63], a[128:131], a[184:187], v[60:63]\n" \
B_"v23, s[64:67], s84 offen offset:1024 lds\n" \
M_"v[60:63], a[192:195], a[248:251], v[60:63]\n" \
X_"a[162:163], v32 offset:33792\n" \
"s_xor_b32 s5, s4, 15\n" \
M_"v[64:67], a[128:131], a[188:191], v[64:67]\n" \
X_"a[224:225], v32 offset:40960\n" \
M_"v[64:67], a[192:195], a[252:255], v[64:67]\n" \
R_"a[128:131], v28 offset:49152\n" \
M_"v[80:83], a[132:135], a[188:191], v[80:83]\n" \
R_"a[192:195], v29 offset:49152\n" \
M_"v[80:83], a[196:199], a[252:255], v[80:83]\n" \
"s_movk_i32 s84, 0x3a00\n" \
M_"v[76:79], a[132:135], a[184:187], v[76:79]\n" \
B_"v22, s[64:67], s84 offen offset:2048 lds\n" \
M_"v[76:79], a[196:199], a[248:251], v[76:79]\n" \
"s_movk_i32 s84, 0x5600\n" \
M_"v[72:75], a[132:135], a[180:183], v[72:75]\n" \
B_"v23, s[64:67], s84 offen offset:3072 lds\n" \
M_"v[72:75], a[196:199], a[244:247], v[72:75]\n" \
"s_add_u32 m0, s31, 0x0\n" \
"s_mov_b32 s84, 0x200000\n" \
M_"v[68:71], a[132:135], a[176:179], v[68:71]\n" \
B_"v26, s[68:71], s84 offen offset:0 lds\n" \
M_"v[68:71], a[196:199], a[240:243], v[68:71]\n" \
R_"a[132:135], v28 offset:51200\n" \
M_"v[84:87], a[136:139], a[176:179], v[84:87]\n" \
R_"a[196:199], v29 offset:51200\n" \
M_"v[84:87], a[200:203], a[240:243], v[84:87]\n" \
"s_addk_i32 s84, 0x7c00\n" \
M_"v[88:91], a[136:139], a[180:183], v[88:91]\n" \
B_"v26, s[68:71], s84 offen offset:1024 lds\n" \
M_"v[88:91], a[200:203], a[244:247], v[88:91]\n" \
X_"a[226:227], v32 offset:41984\n" \
M_"v[92:95], a[136:139], a[184:187], v[92:95]\n" \
"s_addk_i32 s84, 0x7c00\n" \
M_"v[92:95], a[200:203], a[248:251], v[92:95]\n" \
B_"v27, s[68:71], s84 offen offset:2048 lds\n" \
M_"v[96:99], a[136:139], a[188:191], v[96:99]\n" \
X_"a[164:165], v33 offset:32768\n" \
M_"v[96:99], a[200:203], a[252:255], v[96:99]\n" \
R_"a[136:139], v28 offset:53248\n" \
M_"v[112:115], a[140:143], a[188:191], v[112:115]\n" \
R_"a[200:203], v29 offset:53248\n" \
M_"v[112:115], a[204:207], a[252:255], v[112:115]\n" \
"s_addk_i32 s84, 0x7c00\n" \
"s_nop 0\n" \
M_"v[108:111], a[140:143], a[184:187], v[108:111]\n" \
B_"v27, s[68:71], s84 offen offset:3072 lds\n" \
M_"v[108:111], a[204:207], a[248:251], v[108:111]\n" \
"s_add_u32 m0, s30, 0x1000\n" \
"s_mov_b32 s84, 0x8200\n" \
M_"v[104:107], a[140:143], a[180:183], v[104:107]\n" \
B_"v22, s[64:67], s84 offen offset:0 lds\n" \
M_"v[104:107], a[204:207], a[244:247], v[104:107]\n" \
X_"a[166:167], v33 offset:33792\n" \
M_"v[100:103], a[140:143], a[176:179], v[100:103]\n" \
X_"a[228:229], v33 offset:40960\n" \
M_"v[100:103], a[204:207], a[240:243], v[100:103]\n" \
R_"a[140:143], v28 offset:55296\n" \
M_"v[116:119], a[144:147], a[176:179], v[116:119]\n" \
R_"a[204:207], v29 offset:55296\n" \
M_"v[116:119], a[208:211], a[240:243], v[116:119]\n" \
"s_addk_i32 s84, 0x1c00\n" \
M_"v[120:123], a[144:147], a[180:183], v[120:123]\n" \
B_"v23, s[64:67], s84 offen offset:1024 lds\n" \
M_"v[120:123], a[208:211], a[244:247], v[120:123]\n" \
"s_addk_i32 s84, 0x1c00\n" \
M_"v[124:127], a[144:147], a[184:187], v[124:127]\n" \
B_"v22, s[64:67], s84 offen offset:2048 lds\n" \
M_"v[124:127], a[208:211], a[248:251], v[124:127]\n" \
"s_addk_i32 s84, 0x1c00\n" \
"s_nop 0\n" \
M_"v[128:131], a[144:147], a[188:191], v[128:131]\n" \
B_"v23, s[64:67], s84 offen offset:3072 lds\n" \
M_"v[128:131], a[208:211], a[252:255], v[128:131]\n" \
R_"a[144:147], v28 offset:32768\n" \
M_"v[144:147], a[148:151], a[188:191], v[144:147]\n" \
R_"a[208:211], v29 offset:32768\n" \
M_"v[144:147], a[212:215], a[252:255], v[144:147]\n" \
"s_add_u32 m0, s31, 0x4000\n" \
"s_mov_b32 s84, 0x200000\n" \
M_"v[140:143], a[148:151], a[184:187], v[140:143]\n" \
B_"v26, s[72:75], s84 offen offset:0 lds\n" \
M_"v[140:143], a[212:215], a[248:251], v[140:143]\n" \
X_"a[230:231], v33 offset:41984\n" \
M_"v[136:139], a[148:151], a[180:183], v[136:139]\n" \
"s_addk_i32 s84, 0x7c00\n" \
"s_nop 0\n" \
M_"v[136:139], a[212:215], a[244:247], v[136:139]\n" \
B_"v26, s[72:75], s84 offen offset:1024 lds\n" \
M_"v[132:135], a[148:151], a[176:179], v[132:135]\n" \
X_"a[168:169], v34 offset:32768\n" \
M_"v[132:135], a[212:215], a[240:243], v[132:135]\n" \
R_"a[148:151], v28 offset:34816\n" \
M_"v[148:151], a[152:155], a[176:179], v[148:151]\n" \
R_"a[212:215], v29 offset:34816\n" \
M_"v[148:151], a[216:219], a[240:243], v[148:151]\n" \
X_"a[170:171], v34 offset:33792\n" \
M_"v[152:155], a[152:155], a[180:183], v[152:155]\n" \
X_"a[232:233], v34 offset:40960\n" \
M_"v[152:155], a[216:219], a[244:247], v[152:155]\n" \
X_"a[234:235], v34 offset:41984\n" \
M_"v[156:159], a[152:155], a[184:187], v[156:159]\n" \
X_"a[172:173], v35 offset:32768\n" \
M_"v[156:159], a[216:219], a[248:251], v[156:159]\n" \
X_"a[174:175], v35 offset:33792\n" \
M_"v[160:163], a[152:155], a[188:191], v[160:163]\n" \
X_"a[236:237], v35 offset:40960\n" \
M_"v[160:163], a[216:219], a[252:255], v[160:163]\n" \
R_"a[152:155], v28 offset:36864\n" \
M_"v[176:179], a[156:159], a[188:191], v[176:179]\n" \
R_"a[216:219], v29 offset:36864\n" \
M_"v[176:179], a[220:223], a[252:255], v[176:179]\n" \
X_"a[238:239], v35 offset:41984\n" \
M_"v[172:175], a[156:159], a[184:187], v[172:175]\n" \
"s_cmpk_lt_u32 s3, 2\n" \
"s_cselect_b32 s6, s5, s4\n" \
M_"v[172:175], a[220:223], a[248:251], v[172:175]\n" \
"s_lshl_b32 s7, s6, 7\n" \
"v_and_b32_e32 v213, 0x320, v20\n" \
M_"v[168:171], a[156:159], a[180:183], v[168:171]\n" \
"s_movk_i32 s41, 0x100\n" \
"v_lshlrev_b32_e64 v214, v214, s42\n" \
M_"v[168:171], a[220:223], a[244:247], v[168:171]\n" \
"v_mov_b32_e32 v215, 0\n" \
"s_add_i32 s40, s7, -16\n" \
M_"v[164:167], a[156:159], a[176:179], v[164:167]\n" \
"v_cndmask_b32_e32 v41, v217, v216, vcc\n" \
"v_cndmask_b32_e32 v43, v216, v217, vcc\n" \
M_"v[164:167], a[220:223], a[240:243], v[164:167]\n" \
R_"a[156:159], v28 offset:38912\n" \
R_"a[220:223], v29 offset:38912\n" \
"s_waitcnt lgkmcnt(15)\n" \
M_"a[0:3], a[128:131], a[160:163], a[0:3]\n" \
"s_addk_i32 s84, 0x7c00\n" \
M_"a[0:3], a[192:195], a[224:227], a[0:3]\n" \
B_"v27, s[72:75], s84 offen offset:2048 lds\n" \
M_"a[4:7], a[128:131], a[164:167], a[4:7]\n" \
X_"a[176:177], v36 offset:32768\n" \
"v_cmp_eq_u32_e32 vcc, s41, v213\n" \
"s_waitcnt lgkmcnt(15)\n" \
M_"a[4:7], a[192:195], a[228:231], a[4:7]\n" \
"s_addk_i32 s84, 0x7c00\n" \
"s_waitcnt lgkmcnt(11)\n" \
M_"a[8:11], a[128:131], a[168:171], a[8:11]\n" \
B_"v27, s[72:75], s84 offen offset:3072 lds\n" \
"s_waitcnt lgkmcnt(9)\n" \
M_"a[8:11], a[192:195], a[232:235], a[8:11]\n" \
X_"a[178:179], v36 offset:33792\n" \
"s_mov_b32 s41, 0\n" \
"s_waitcnt lgkmcnt(8)\n" \
M_"a[12:15], a[128:131], a[172:175], a[12:15]\n" \
X_"a[240:241], v36 offset:40960\n" \
"s_and_b32 s43, s3, 1\n" \
"s_waitcnt lgkmcnt(5)\n" \
M_"a[12:15], a[192:195], a[236:239], a[12:15]\n" \
"v_cndmask_b32_e32 v46, v215, v214, vcc\n" \
"v_and_b32_e32 v213, 0x330, v20\n" \
M_"a[28:31], a[132:135], a[172:175], a[28:31]\n" \
"s_cmp_lg_u32 s7, 0\n" \
"s_cselect_b32 s40, s40, s41\n" \
M_"a[28:31], a[196:199], a[236:239], a[28:31]\n" \
X_"a[242:243], v36 offset:41984\n" \
"s_mov_b32 s42, 0xff800000\n" \
M_"a[24:27], a[132:135], a[168:171], a[24:27]\n" \
"s_cmp_lg_u32 s7, 0\n" \
"s_cselect_b32 s47, s41, s42\n" \
M_"a[24:27], a[196:199], a[232:235], a[24:27]\n" \
"s_lshl_b32 s41, s43, 6\n" \
"v_lshrrev_b32_e32 v215, 6, v20\n" \
M_"a[20:23], a[132:135], a[164:167], a[20:23]\n" \
"v_mov_b32_e32 v214, 0x3f80\n" \
"v_cmp_eq_u32_e64 vcc, 32, v213\n" \
M_"a[20:23], a[196:199], a[228:231], a[20:23]\n" \
X_"a[180:181], v37 offset:32768\n" \
"s_or_b32 s7, s41, s7\n" \
M_"a[16:19], a[132:135], a[160:163], a[16:19]\n" \
"v_lshrrev_b32_e32 v213, 7, v20\n" \
"v_bfe_u32 v216, v20, 6, 2\n" \
M_"a[16:19], a[196:199], a[224:227], a[16:19]\n" \
"v_lshrrev_b32_e64 v219, 1, v20\n" \
"v_bfe_u32 v215, v215, 1, 1\n" \
M_"a[32:35], a[136:139], a[160:163], a[32:35]\n" \
X_"a[182:183], v37 offset:33792\n" \
"v_cndmask_b32_e64 v47, 0, v214, vcc\n" \
M_"a[32:35], a[200:203], a[224:227], a[32:35]\n" \
"v_or_b32_e64 v214, s7, v213\n" \
"v_lshl_or_b32 v217, s43, 3, v213\n" \
M_"a[36:39], a[136:139], a[164:167], a[36:39]\n" \
"v_lshlrev_b32_e32 v218, 10, v212\n" \
"v_lshlrev_b32_e32 v216, 7, v216\n" \
M_"a[36:39], a[200:203], a[228:231], a[36:39]\n" \
X_"a[244:245], v37 offset:40960\n" \
"v_and_b32_e64 v219, 24, v219\n" \
M_"a[40:43], a[136:139], a[168:171], a[40:43]\n" \
"s_mov_b32 m0, s32\n" \
"v_and_or_b32 v213, v213, 2, v215\n" \
M_"a[40:43], a[200:203], a[232:235], a[40:43]\n" \
"s_mov_b32 exec_lo, 0xffffffff\n" \
"s_mov_b32 exec_hi, 0x0\n" \
"global_load_lds_dword v[188:189], off\n" \
"s_mov_b64 exec, -1\n" \
M_"a[44:47], a[136:139], a[172:175], a[44:47]\n" \
X_"a[246:247], v37 offset:41984\n" \
"v_add_u32_e64 v217, s40, v217\n" \
M_"a[44:47], a[200:203], a[236:239], a[44:47]\n" \
"v_or3_b32 v216, v216, v218, v219\n" \
"v_lshlrev_b32_e64 v213, 5, v213\n" \
M_"a[60:63], a[140:143], a[172:175], a[60:63]\n" \
"s_mov_b32 s40, 0x14000\n" \
"s_lshl_b32 s7, s2, 2\n" \
M_"a[60:63], a[204:207], a[236:239], a[60:63]\n" \
"v_or3_b32 v48, v216, v213, s40\n" \
"s_lshl_b32 s40, s43, 2\n" \
M_"a[56:59], a[140:143], a[168:171], a[56:59]\n" \
X_"a[184:185], v38 offset:32768\n" \
"s_lshl_b32 s6, s6, 3\n" \
M_"a[56:59], a[204:207], a[232:235], a[56:59]\n" \
"v_or_b32_e32 v213, s40, v212\n" \
"s_or_b32 s6, s6, s40\n" \
M_"a[52:55], a[140:143], a[164:167], a[52:55]\n" \
"s_and_b32 s40, s7, 0x7c\n" \
"s_or_b32 s40, s40, s3\n" \
M_"a[52:55], a[204:207], a[228:231], a[52:55]\n" \
X_"a[186:187], v38 offset:33792\n" \
M_"a[48:51], a[140:143], a[160:163], a[48:51]\n" \
"s_cmpk_lt_u32 s40, 64\n" \
"s_cselect_b32 s41, s4, s5\n" \
M_"a[48:51], a[204:207], a[224:227], a[48:51]\n" \
"s_lshl_b32 s40, s40, 1\n" \
"s_lshl_b32 s41, s41, 7\n" \
M_"a[64:67], a[144:147], a[160:163], a[64:67]\n" \
X_"a[248:249], v38 offset:40960\n" \
"s_and_b32 s40, s40, 0x7e\n" \
M_"a[64:67], a[208:211], a[224:227], a[64:67]\n" \
"s_or_b32 s40, s41, s40\n" \
"s_mulk_i32 s40, 0x480\n" \
M_"a[68:71], a[144:147], a[164:167], a[68:71]\n" \
"v_add_u32_e64 v49, s40, v20\n" \
"s_mov_b32 s40, 0xc000\n" \
M_"a[68:71], a[208:211], a[228:231], a[68:71]\n" \
X_"a[250:251], v38 offset:41984\n" \
"s_and_b32 s7, s7, 28\n" \
M_"a[72:75], a[144:147], a[168:171], a[72:75]\n" \
"s_bfe_u32 s45, s2, 0x20003\n" \
"s_mov_b32 s41, 0x8000\n" \
M_"a[72:75], a[208:211], a[232:235], a[72:75]\n" \
"s_cmp_lg_u32 s4, 0\n" \
"s_cselect_b32 s51, s40, s41\n" \
M_"a[76:79], a[144:147], a[172:175], a[76:79]\n" \
"s_movk_i32 s40, 0x4000\n" \
"s_lshr_b32 s44, s3, 1\n" \
M_"a[76:79], a[208:211], a[236:239], a[76:79]\n" \
X_"a[188:189], v39 offset:32768\n" \
"s_or_b32 s46, s7, s45\n" \
M_"a[92:95], a[148:151], a[172:175], a[92:95]\n" \
"s_cmp_lg_u32 s4, 0\n" \
"s_cselect_b32 s53, s41, s40\n" \
M_"a[92:95], a[212:215], a[236:239], a[92:95]\n" \
"s_lshl_b32 s40, s44, 15\n" \
"s_lshl_b32 s41, s43, 14\n" \
M_"a[88:91], a[148:151], a[168:171], a[88:91]\n" \
X_"a[190:191], v39 offset:33792\n" \
"s_lshl_b32 s48, s46, 20\n" \
M_"a[88:91], a[212:215], a[232:235], a[88:91]\n" \
"s_or_b32 s41, s40, s41\n" \
"s_add_i32 s49, s48, 0x1000000\n" \
M_"a[84:87], a[148:151], a[164:167], a[84:87]\n" \
"s_waitcnt vmcnt(17)\n" \
"v_or_b32_e32 v204, v210, v204\n" \
"v_or_b32_e64 v206, v210, v206\n" \
M_"a[84:87], a[212:215], a[228:231], a[84:87]\n" \
"v_mov_b32_e64 v210, 0\n" \
"s_add_u32 s88, s35, 0x1000\n" \
M_"a[80:83], a[148:151], a[160:163], a[80:83]\n" \
X_"a[252:253], v39 offset:40960\n" \
"s_or_b32 s42, s49, s41\n" \
M_"a[80:83], a[212:215], a[224:227], a[80:83]\n" \
"ds_write_b128 v196, v[200:203]\n" \
M_"a[96:99], a[152:155], a[160:163], a[96:99]\n" \
X_"a[254:255], v39 offset:41984\n" \
"s_lshl_b32 s4, s4, 16\n" \
M_"a[96:99], a[216:219], a[224:227], a[96:99]\n" \
"s_or_b32 s41, s42, s4\n" \
"s_lshl_b32 s4, s5, 16\n" \
M_"a[100:103], a[152:155], a[164:167], a[100:103]\n" \
"ds_write_b128 v197, v[204:207]\n" \
M_"a[100:103], a[216:219], a[228:231], a[100:103]\n" \
"s_mul_i32 s5, s6, 0x24800\n" \
"s_lshl_b32 s6, s46, 9\n" \
M_"a[104:107], a[152:155], a[168:171], a[104:107]\n" \
"s_lshl_b32 s7, s7, 6\n" \
"s_add_i32 s6, s5, s6\n" \
M_"a[104:107], a[216:219], a[232:235], a[104:107]\n" \
"ds_write_b128 v198, v[208:211]\n" \
M_"a[108:111], a[152:155], a[172:175], a[108:111]\n" \
"s_or_b32 s5, s5, s7\n" \
"s_lshl_b32 s7, s45, 2\n" \
M_"a[108:111], a[216:219], a[236:239], a[108:111]\n" \
"s_or_b32 s5, s5, s7\n" \
"s_lshl_b32 s7, s3, 12\n" \
"s_waitcnt lgkmcnt(15)\n" \
M_"a[124:127], a[156:159], a[172:175], a[124:127]\n" \
"s_or_b32 s50, s49, s7\n" \
"s_lshl_b32 s7, s3, 11\n" \
M_"a[124:127], a[220:223], a[236:239], a[124:127]\n" \
"v_lshrrev_b32_e64 v215, 4, v20\n" \
"v_bfe_u32 v216, v20, 4, 3\n" \
M_"a[120:123], a[156:159], a[168:171], a[120:123]\n" \
"s_or_b32 s42, s42, s4\n" \
"s_lshl_b32 s4, s43, 11\n" \
M_"a[120:123], a[220:223], a[232:235], a[120:123]\n" \
"s_add_i32 s52, s48, s7\n" \
"s_add_i32 s53, s53, s7\n" \
M_"a[116:119], a[156:159], a[164:167], a[116:119]\n" \
"v_mul_u32_u24_e32 v214, 0x480, v214\n" \
"v_bitop3_b32 v218, v212, v215, 7 bitop3:0x78\n" \
M_"a[116:119], a[220:223], a[228:231], a[116:119]\n" \
"v_bitop3_b32 v212, v212, v216, 4 bitop3:0x36\n" \
"s_lshl_b32 s7, s3, 10\n" \
M_"a[112:115], a[156:159], a[160:163], a[112:115]\n" \
"s_or_b32 s4, s40, s4\n" \
"v_lshl_or_b32 v25, v212, 4, v214\n" \
"s_waitcnt vmcnt(1) lgkmcnt(0)\n" \
M_"a[112:115], a[220:223], a[224:227], a[112:115]\n" \
"s_barrier\n" \
M_"v[52:55], a[128:131], a[176:179], v[52:55]\n" \
"s_add_u32 m0, s30, 0x8000\n" \
"s_movk_i32 s84, 0x280\n" \
M_"v[52:55], a[192:195], a[240:243], v[52:55]\n" \
B_"v22, s[64:67], s84 offen offset:0 lds\n" \
M_"v[56:59], a[128:131], a[180:183], v[56:59]\n" \
"s_movk_i32 s84, 0x1e80\n" \
M_"v[56:59], a[192:195], a[244:247], v[56:59]\n" \
B_"v23, s[64:67], s84 offen offset:1024 lds\n" \
M_"v[60:63], a[128:131], a[184:187], v[60:63]\n" \
X_"a[160:161], v32 offset:0\n" \
"v_mul_u32_u24_e32 v212, 0x480, v217\n" \
M_"v[60:63], a[192:195], a[248:251], v[60:63]\n" \
"s_movk_i32 s84, 0x3a80\n" \
M_"v[64:67], a[128:131], a[188:191], v[64:67]\n" \
B_"v22, s[64:67], s84 offen offset:2048 lds\n" \
M_"v[64:67], a[192:195], a[252:255], v[64:67]\n" \
R_"a[128:131], v28 offset:16384\n" \
"v_bitop3_b32 v213, v215, v213, 7 bitop3:0x6c\n" \
M_"v[80:83], a[132:135], a[188:191], v[80:83]\n" \
R_"a[192:195], v29 offset:16384\n" \
"s_or_b32 s40, s7, 0x14000\n" \
M_"v[80:83], a[196:199], a[252:255], v[80:83]\n" \
"s_movk_i32 s84, 0x5680\n" \
M_"v[76:79], a[132:135], a[184:187], v[76:79]\n" \
B_"v23, s[64:67], s84 offen offset:3072 lds\n" \
M_"v[76:79], a[196:199], a[248:251], v[76:79]\n" \
"s_add_u32 m0, s31, 0x8000\n" \
"s_mov_b32 s84, 0x280000\n" \
M_"v[72:75], a[132:135], a[180:183], v[72:75]\n" \
B_"v26, s[68:71], s84 offen offset:0 lds\n" \
M_"v[72:75], a[196:199], a[244:247], v[72:75]\n" \
X_"a[162:163], v32 offset:1024\n" \
"s_lshl_b32 s43, s44, 13\n" \
M_"v[68:71], a[132:135], a[176:179], v[68:71]\n" \
X_"a[224:225], v32 offset:8192\n" \
"s_mulk_i32 s44, 0xe800\n" \
M_"v[68:71], a[196:199], a[240:243], v[68:71]\n" \
R_"a[132:135], v28 offset:18432\n" \
"s_lshl_b32 s7, s3, 8\n" \
M_"v[84:87], a[136:139], a[176:179], v[84:87]\n" \
R_"a[196:199], v29 offset:18432\n" \
"v_lshl_or_b32 v24, v218, 4, v214\n" \
M_"v[84:87], a[200:203], a[240:243], v[84:87]\n" \
"s_addk_i32 s84, 0x7c00\n" \
M_"v[88:91], a[136:139], a[180:183], v[88:91]\n" \
B_"v26, s[68:71], s84 offen offset:1024 lds\n" \
M_"v[88:91], a[200:203], a[244:247], v[88:91]\n" \
"s_addk_i32 s84, 0x7c00\n" \
M_"v[92:95], a[136:139], a[184:187], v[92:95]\n" \
B_"v27, s[68:71], s84 offen offset:2048 lds\n" \
M_"v[92:95], a[200:203], a[248:251], v[92:95]\n" \
"s_addk_i32 s84, 0x7c00\n" \
M_"v[96:99], a[136:139], a[188:191], v[96:99]\n" \
B_"v27, s[68:71], s84 offen offset:3072 lds\n" \
M_"v[96:99], a[200:203], a[252:255], v[96:99]\n" \
R_"a[136:139], v28 offset:20480\n" \
"v_lshl_or_b32 v21, v213, 4, v212\n" \
M_"v[112:115], a[140:143], a[188:191], v[112:115]\n" \
R_"a[200:203], v29 offset:20480\n" \
"s_add_i32 s44, s44, 0x17000\n" \
M_"v[112:115], a[204:207], a[252:255], v[112:115]\n" \
"s_add_u32 m0, s30, 0x9000\n" \
"s_mov_b32 s84, 0x8280\n" \
M_"v[108:111], a[140:143], a[184:187], v[108:111]\n" \
B_"v22, s[64:67], s84 offen offset:0 lds\n" \
M_"v[108:111], a[204:207], a[248:251], v[108:111]\n" \
"s_addk_i32 s84, 0x1c00\n" \
M_"v[104:107], a[140:143], a[180:183], v[104:107]\n" \
B_"v23, s[64:67], s84 offen offset:1024 lds\n" \
M_"v[104:107], a[204:207], a[244:247], v[104:107]\n" \
"s_addk_i32 s84, 0x1c00\n" \
M_"v[100:103], a[140:143], a[176:179], v[100:103]\n" \
B_"v22, s[64:67], s84 offen offset:2048 lds\n" \
M_"v[100:103], a[204:207], a[240:243], v[100:103]\n" \
R_"a[140:143], v28 offset:22528\n" \
"s_or_b32 s45, s7, 0x27000\n" \
M_"v[116:119], a[144:147], a[176:179], v[116:119]\n" \
R_"a[204:207], v29 offset:22528\n" \
"s_or_b32 s46, s4, 0x10000\n" \
M_"v[116:119], a[208:211], a[240:243], v[116:119]\n" \
"s_addk_i32 s84, 0x1c00\n" \
M_"v[120:123], a[144:147], a[180:183], v[120:123]\n" \
B_"v23, s[64:67], s84 offen offset:3072 lds\n" \
M_"v[120:123], a[208:211], a[244:247], v[120:123]\n" \
"s_add_u32 m0, s31, 0xc000\n" \
"s_mov_b32 s84, 0x280000\n" \
M_"v[124:127], a[144:147], a[184:187], v[124:127]\n" \
B_"v26, s[72:75], s84 offen offset:0 lds\n" \
M_"v[124:127], a[208:211], a[248:251], v[124:127]\n" \
"s_addk_i32 s84, 0x7c00\n" \
M_"v[128:131], a[144:147], a[188:191], v[128:131]\n" \
B_"v26, s[72:75], s84 offen offset:1024 lds\n" \
M_"v[128:131], a[208:211], a[252:255], v[128:131]\n" \
R_"a[144:147], v28 offset:0\n" \
"s_add_i32 s48, s6, 0x3000000\n" \
M_"v[144:147], a[148:151], a[188:191], v[144:147]\n" \
R_"a[208:211], v29 offset:0\n" \
"s_add_i32 s49, s5, 0x3004000\n" \
M_"v[144:147], a[212:215], a[252:255], v[144:147]\n" \
R_"v[200:203], v199 offset:0\n" \
"s_add_i32 s52, s52, 0x1010000\n" \
M_"v[140:143], a[148:151], a[184:187], v[140:143]\n" \
R_"v[204:207], v199 offset:4096\n" \
M_"v[140:143], a[212:215], a[248:251], v[140:143]\n" \
X_"a[226:227], v32 offset:9216\n" \
M_"v[136:139], a[148:151], a[180:183], v[136:139]\n" \
X_"a[164:165], v33 offset:0\n" \
M_"v[136:139], a[212:215], a[244:247], v[136:139]\n" \
R_"v[208:211], v199 offset:8192\n" \
M_"v[132:135], a[148:151], a[176:179], v[132:135]\n" \
X_"a[166:167], v33 offset:1024\n" \
M_"v[132:135], a[212:215], a[240:243], v[132:135]\n" \
R_"a[148:151], v28 offset:2048\n" \
M_"v[148:151], a[152:155], a[176:179], v[148:151]\n" \
R_"a[212:215], v29 offset:2048\n" \
M_"v[148:151], a[216:219], a[240:243], v[148:151]\n" \
X_"a[228:229], v33 offset:8192\n" \
M_"v[152:155], a[152:155], a[180:183], v[152:155]\n" \
X_"a[230:231], v33 offset:9216\n" \
M_"v[152:155], a[216:219], a[244:247], v[152:155]\n" \
X_"a[168:169], v34 offset:0\n" \
M_"v[156:159], a[152:155], a[184:187], v[156:159]\n" \
X_"a[170:171], v34 offset:1024\n" \
M_"v[156:159], a[216:219], a[248:251], v[156:159]\n" \
X_"a[232:233], v34 offset:8192\n" \
M_"v[160:163], a[152:155], a[188:191], v[160:163]\n" \
X_"a[234:235], v34 offset:9216\n" \
M_"v[160:163], a[216:219], a[252:255], v[160:163]\n" \
R_"a[152:155], v28 offset:4096\n" \
M_"v[176:179], a[156:159], a[188:191], v[176:179]\n" \
R_"a[216:219], v29 offset:4096\n" \
M_"v[176:179], a[220:223], a[252:255], v[176:179]\n" \
X_"a[172:173], v35 offset:0\n" \
M_"v[172:175], a[156:159], a[184:187], v[172:175]\n" \
X_"a[174:175], v35 offset:1024\n" \
M_"v[172:175], a[220:223], a[248:251], v[172:175]\n" \
X_"a[236:237], v35 offset:8192\n" \
M_"v[168:171], a[156:159], a[180:183], v[168:171]\n" \
X_"a[238:239], v35 offset:9216\n" \
M_"v[168:171], a[220:223], a[244:247], v[168:171]\n" \
M_"v[164:167], a[156:159], a[176:179], v[164:167]\n" \
M_"v[164:167], a[220:223], a[240:243], v[164:167]\n" \
R_"a[156:159], v28 offset:6144\n" \
R_"a[220:223], v29 offset:6144\n" \
"s_waitcnt lgkmcnt(15)\n" \
M_"a[0:3], a[128:131], a[160:163], a[0:3]\n" \
"s_addk_i32 s84, 0x7c00\n" \
M_"a[0:3], a[192:195], a[224:227], a[0:3]\n" \
B_"v27, s[72:75], s84 offen offset:2048 lds\n" \
M_"a[4:7], a[128:131], a[164:167], a[4:7]\n" \
X_"a[176:177], v36 offset:0\n" \
"s_waitcnt lgkmcnt(13)\n" \
M_"a[4:7], a[192:195], a[228:231], a[4:7]\n" \
"s_addk_i32 s84, 0x7c00\n" \
"s_waitcnt lgkmcnt(11)\n" \
M_"a[8:11], a[128:131], a[168:171], a[8:11]\n" \
B_"v27, s[72:75], s84 offen offset:3072 lds\n" \
"s_waitcnt lgkmcnt(9)\n" \
M_"a[8:11], a[192:195], a[232:235], a[8:11]\n" \
X_"a[178:179], v36 offset:1024\n" \
"s_waitcnt lgkmcnt(6)\n" \
M_"a[12:15], a[128:131], a[172:175], a[12:15]\n" \
X_"a[240:241], v36 offset:8192\n" \
"s_waitcnt lgkmcnt(5)\n" \
M_"a[12:15], a[192:195], a[236:239], a[12:15]\n" \
M_"a[28:31], a[132:135], a[172:175], a[28:31]\n" \
M_"a[28:31], a[196:199], a[236:239], a[28:31]\n" \
X_"a[242:243], v36 offset:9216\n" \
M_"a[24:27], a[132:135], a[168:171], a[24:27]\n" \
M_"a[24:27], a[196:199], a[232:235], a[24:27]\n" \
M_"a[20:23], a[132:135], a[164:167], a[20:23]\n" \
M_"a[20:23], a[196:199], a[228:231], a[20:23]\n" \
X_"a[180:181], v37 offset:0\n" \
".fill (L_pb + 52960 - .) / 4, 4, 0xBF800000\n" \
"L_pp13:\n" \
"s_cbranch_execz L_pret\n" \
M_"a[16:19], a[132:135], a[160:163], a[16:19]\n" \
M_"a[16:19], a[196:199], a[224:227], a[16:19]\n" \
M_"a[32:35], a[136:139], a[160:163], a[32:35]\n" \
X_"a[182:183], v37 offset:1024\n" \
M_"a[32:35], a[200:203], a[224:227], a[32:35]\n" \
M_"a[36:39], a[136:139], a[164:167], a[36:39]\n" \
M_"a[36:39], a[200:203], a[228:231], a[36:39]\n" \
"buffer_store_dwordx4 v[200:203], v20, s[80:83], s35 offen sc1\n" \
M_"a[40:43], a[136:139], a[168:171], a[40:43]\n" \
"s_mov_b32 m0, s32\n" \
X_"a[244:245], v37 offset:8192\n" \
M_"a[40:43], a[200:203], a[232:235], a[40:43]\n" \
"s_mov_b32 exec_lo, 0x0\n" \
"s_mov_b32 exec_hi, 0xffffffff\n" \
"global_load_lds_dword v[188:189], off\n" \
"s_mov_b64 exec, -1\n" \
"s_nop 0\n" \
M_"a[44:47], a[136:139], a[172:175], a[44:47]\n" \
X_"a[246:247], v37 offset:9216\n" \
M_"a[44:47], a[200:203], a[236:239], a[44:47]\n" \
M_"a[60:63], a[140:143], a[172:175], a[60:63]\n" \
M_"a[60:63], a[204:207], a[236:239], a[60:63]\n" \
M_"a[56:59], a[140:143], a[168:171], a[56:59]\n" \
X_"a[184:185], v38 offset:0\n" \
M_"a[56:59], a[204:207], a[232:235], a[56:59]\n" \
M_"a[52:55], a[140:143], a[164:167], a[52:55]\n" \
"buffer_store_dwordx4 v[204:207], v20, s[80:83], s88 offen sc1\n" \
M_"a[52:55], a[204:207], a[228:231], a[52:55]\n" \
X_"a[186:187], v38 offset:1024\n" \
M_"a[48:51], a[140:143], a[160:163], a[48:51]\n" \
M_"a[48:51], a[204:207], a[224:227], a[48:51]\n" \
M_"a[64:67], a[144:147], a[160:163], a[64:67]\n" \
X_"a[248:249], v38 offset:8192\n" \
M_"a[64:67], a[208:211], a[224:227], a[64:67]\n" \
M_"a[68:71], a[144:147], a[164:167], a[68:71]\n" \
M_"a[68:71], a[208:211], a[228:231], a[68:71]\n" \
X_"a[250:251], v38 offset:9216\n" \
M_"a[72:75], a[144:147], a[168:171], a[72:75]\n" \
M_"a[72:75], a[208:211], a[232:235], a[72:75]\n" \
M_"a[76:79], a[144:147], a[172:175], a[76:79]\n" \
"buffer_store_dwordx4 v[208:211], v20, s[80:83], s36 offen sc1\n" \
M_"a[76:79], a[208:211], a[236:239], a[76:79]\n" \
X_"a[188:189], v39 offset:0\n" \
M_"a[92:95], a[148:151], a[172:175], a[92:95]\n" \
M_"a[92:95], a[212:215], a[236:239], a[92:95]\n" \
M_"a[88:91], a[148:151], a[168:171], a[88:91]\n" \
X_"a[190:191], v39 offset:1024\n" \
M_"a[88:91], a[212:215], a[232:235], a[88:91]\n" \
M_"a[84:87], a[148:151], a[164:167], a[84:87]\n" \
M_"a[84:87], a[212:215], a[228:231], a[84:87]\n" \
X_"a[252:253], v39 offset:8192\n" \
M_"a[80:83], a[148:151], a[160:163], a[80:83]\n" \
M_"a[80:83], a[212:215], a[224:227], a[80:83]\n" \
M_"a[96:99], a[152:155], a[160:163], a[96:99]\n" \
X_"a[254:255], v39 offset:9216\n" \
M_"a[96:99], a[216:219], a[224:227], a[96:99]\n" \
M_"a[100:103], a[152:155], a[164:167], a[100:103]\n" \
M_"a[100:103], a[216:219], a[228:231], a[100:103]\n" \
M_"a[104:107], a[152:155], a[168:171], a[104:107]\n" \
M_"a[104:107], a[216:219], a[232:235], a[104:107]\n" \
M_"a[108:111], a[152:155], a[172:175], a[108:111]\n" \
M_"a[108:111], a[216:219], a[236:239], a[108:111]\n" \
"s_waitcnt lgkmcnt(15)\n" \
M_"a[124:127], a[156:159], a[172:175], a[124:127]\n" \
M_"a[124:127], a[220:223], a[236:239], a[124:127]\n" \
M_"a[120:123], a[156:159], a[168:171], a[120:123]\n" \
M_"a[120:123], a[220:223], a[232:235], a[120:123]\n" \
M_"a[116:119], a[156:159], a[164:167], a[116:119]\n" \
M_"a[116:119], a[220:223], a[228:231], a[116:119]\n" \
M_"a[112:115], a[156:159], a[160:163], a[112:115]\n" \
"s_waitcnt vmcnt(4) lgkmcnt(0)\n" \
M_"a[112:115], a[220:223], a[224:227], a[112:115]\n" \
"s_barrier\n" \
M_"v[52:55], a[128:131], a[176:179], v[52:55]\n" \
"s_add_u32 m0, s30, 0x0\n" \
"s_movk_i32 s84, 0x300\n" \
M_"v[52:55], a[192:195], a[240:243], v[52:55]\n" \
B_"v22, s[64:67], s84 offen offset:0 lds\n" \
M_"v[56:59], a[128:131], a[180:183], v[56:59]\n" \
X_"a[160:161], v32 offset:32768\n" \
M_"v[56:59], a[192:195], a[244:247], v[56:59]\n" \
"s_movk_i32 s84, 0x1f00\n" \
M_"v[60:63], a[128:131], a[184:187], v[60:63]\n" \
B_"v23, s[64:67], s84 offen offset:1024 lds\n" \
M_"v[60:63], a[192:195], a[248:251], v[60:63]\n" \
X_"a[162:163], v32 offset:33792\n" \
M_"v[64:67], a[128:131], a[188:191], v[64:67]\n" \
X_"a[224:225], v32 offset:40960\n" \
M_"v[64:67], a[192:195], a[252:255], v[64:67]\n" \
R_"a[128:131], v28 offset:49152\n" \
M_"v[80:83], a[132:135], a[188:191], v[80:83]\n" \
R_"a[192:195], v29 offset:49152\n" \
M_"v[80:83], a[196:199], a[252:255], v[80:83]\n" \
"s_movk_i32 s84, 0x3b00\n" \
M_"v[76:79], a[132:135], a[184:187], v[76:79]\n" \
B_"v22, s[64:67], s84 offen offset:2048 lds\n" \
M_"v[76:79], a[196:199], a[248:251], v[76:79]\n" \
"s_movk_i32 s84, 0x5700\n" \
M_"v[72:75], a[132:135], a[180:183], v[72:75]\n" \
B_"v23, s[64:67], s84 offen offset:3072 lds\n" \
M_"v[72:75], a[196:199], a[244:247], v[72:75]\n" \
"s_add_u32 m0, s31, 0x0\n" \
"s_mov_b32 s84, 0x300000\n" \
M_"v[68:71], a[132:135], a[176:179], v[68:71]\n" \
B_"v26, s[68:71], s84 offen offset:0 lds\n" \
M_"v[68:71], a[196:199], a[240:243], v[68:71]\n" \
R_"a[132:135], v28 offset:51200\n" \
M_"v[84:87], a[136:139], a[176:179], v[84:87]\n" \
R_"a[196:199], v29 offset:51200\n" \
M_"v[84:87], a[200:203], a[240:243], v[84:87]\n" \
"s_addk_i32 s84, 0x7c00\n" \
M_"v[88:91], a[136:139], a[180:183], v[88:91]\n" \
B_"v26, s[68:71], s84 offen offset:1024 lds\n" \
M_"v[88:91], a[200:203], a[244:247], v[88:91]\n" \
X_"a[226:227], v32 offset:41984\n" \
M_"v[92:95], a[136:139], a[184:187], v[92:95]\n" \
"s_addk_i32 s84, 0x7c00\n" \
M_"v[92:95], a[200:203], a[248:251], v[92:95]\n" \
B_"v27, s[68:71], s84 offen offset:2048 lds\n" \
M_"v[96:99], a[136:139], a[188:191], v[96:99]\n" \
X_"a[164:165], v33 offset:32768\n" \
M_"v[96:99], a[200:203], a[252:255], v[96:99]\n" \
R_"a[136:139], v28 offset:53248\n" \
M_"v[112:115], a[140:143], a[188:191], v[112:115]\n" \
R_"a[200:203], v29 offset:53248\n" \
M_"v[112:115], a[204:207], a[252:255], v[112:115]\n" \
"s_addk_i32 s84, 0x7c00\n" \
M_"v[108:111], a[140:143], a[184:187], v[108:111]\n" \
B_"v27, s[68:71], s84 offen offset:3072 lds\n" \
M_"v[108:111], a[204:207], a[248:251], v[108:111]\n" \
"s_add_u32 m0, s30, 0x1000\n" \
"s_mov_b32 s84, 0x8300\n" \
M_"v[104:107], a[140:143], a[180:183], v[104:107]\n" \
B_"v22, s[64:67], s84 offen offset:0 lds\n" \
M_"v[104:107], a[204:207], a[244:247], v[104:107]\n" \
X_"a[166:167], v33 offset:33792\n" \
M_"v[100:103], a[140:143], a[176:179], v[100:103]\n" \
X_"a[228:229], v33 offset:40960\n" \
M_"v[100:103], a[204:207], a[240:243], v[100:103]\n" \
R_"a[140:143], v28 offset:55296\n" \
M_"v[116:119], a[144:147], a[176:179], v[116:119]\n" \
R_"a[204:207], v29 offset:55296\n" \
M_"v[116:119], a[208:211], a[240:243], v[116:119]\n" \
"s_addk_i32 s84, 0x1c00\n" \
M_"v[120:123], a[144:147], a[180:183], v[120:123]\n" \
B_"v23, s[64:67], s84 offen offset:1024 lds\n" \
M_"v[120:123], a[208:211], a[244:247], v[120:123]\n" \
"s_addk_i32 s84, 0x1c00\n" \
M_"v[124:127], a[144:147], a[184:187], v[124:127]\n" \
B_"v22, s[64:67], s84 offen offset:2048 lds\n" \
M_"v[124:127], a[208:211], a[248:251], v[124:127]\n" \
"s_addk_i32 s84, 0x1c00\n" \
"s_nop 0\n" \
M_"v[128:131], a[144:147], a[188:191], v[128:131]\n" \
B_"v23, s[64:67], s84 offen offset:3072 lds\n" \
M_"v[128:131], a[208:211], a[252:255], v[128:131]\n" \
R_"a[144:147], v28 offset:32768\n" \
M_"v[144:147], a[148:151], a[188:191], v[144:147]\n" \
R_"a[208:211], v29 offset:32768\n" \
M_"v[144:147], a[212:215], a[252:255], v[144:147]\n" \
"s_add_u32 m0, s31, 0x4000\n" \
"s_mov_b32 s84, 0x300000\n" \
M_"v[140:143], a[148:151], a[184:187], v[140:143]\n" \
B_"v26, s[72:75], s84 offen offset:0 lds\n" \
M_"v[140:143], a[212:215], a[248:251], v[140:143]\n" \
X_"a[230:231], v33 offset:41984\n" \
M_"v[136:139], a[148:151], a[180:183], v[136:139]\n" \
"s_addk_i32 s84, 0x7c00\n" \
"s_nop 0\n" \
M_"v[136:139], a[212:215], a[244:247], v[136:139]\n" \
B_"v26, s[72:75], s84 offen offset:1024 lds\n" \
M_"v[132:135], a[148:151], a[176:179], v[132:135]\n" \
X_"a[168:169], v34 offset:32768\n" \
M_"v[132:135], a[212:215], a[240:243], v[132:135]\n" \
R_"a[148:151], v28 offset:34816\n" \
M_"v[148:151], a[152:155], a[176:179], v[148:151]\n" \
R_"a[212:215], v29 offset:34816\n" \
M_"v[148:151], a[216:219], a[240:243], v[148:151]\n" \
X_"a[170:171], v34 offset:33792\n" \
M_"v[152:155], a[152:155], a[180:183], v[152:155]\n" \
X_"a[232:233], v34 offset:40960\n" \
M_"v[152:155], a[216:219], a[244:247], v[152:155]\n" \
X_"a[234:235], v34 offset:41984\n" \
M_"v[156:159], a[152:155], a[184:187], v[156:159]\n" \
X_"a[172:173], v35 offset:32768\n" \
M_"v[156:159], a[216:219], a[248:251], v[156:159]\n" \
X_"a[174:175], v35 offset:33792\n" \
M_"v[160:163], a[152:155], a[188:191], v[160:163]\n" \
X_"a[236:237], v35 offset:40960\n" \
M_"v[160:163], a[216:219], a[252:255], v[160:163]\n" \
R_"a[152:155], v28 offset:36864\n" \
M_"v[176:179], a[156:159], a[188:191], v[176:179]\n" \
R_"a[216:219], v29 offset:36864\n" \
M_"v[176:179], a[220:223], a[252:255], v[176:179]\n" \
X_"a[238:239], v35 offset:41984\n" \
M_"v[172:175], a[156:159], a[184:187], v[172:175]\n" \
M_"v[172:175], a[220:223], a[248:251], v[172:175]\n" \
M_"v[168:171], a[156:159], a[180:183], v[168:171]\n" \
M_"v[168:171], a[220:223], a[244:247], v[168:171]\n" \
M_"v[164:167], a[156:159], a[176:179], v[164:167]\n" \
M_"v[164:167], a[220:223], a[240:243], v[164:167]\n" \
R_"a[156:159], v28 offset:38912\n" \
R_"a[220:223], v29 offset:38912\n" \
"s_waitcnt lgkmcnt(15)\n" \
M_"a[0:3], a[128:131], a[160:163], a[0:3]\n" \
"s_addk_i32 s84, 0x7c00\n" \
M_"a[0:3], a[192:195], a[224:227], a[0:3]\n" \
B_"v27, s[72:75], s84 offen offset:2048 lds\n" \
M_"a[4:7], a[128:131], a[164:167], a[4:7]\n" \
X_"a[176:177], v36 offset:32768\n" \
"s_waitcnt lgkmcnt(15)\n" \
M_"a[4:7], a[192:195], a[228:231], a[4:7]\n" \
"s_addk_i32 s84, 0x7c00\n" \
"s_waitcnt lgkmcnt(11)\n" \
M_"a[8:11], a[128:131], a[168:171], a[8:11]\n" \
B_"v27, s[72:75], s84 offen offset:3072 lds\n" \
"s_waitcnt lgkmcnt(9)\n" \
M_"a[8:11], a[192:195], a[232:235], a[8:11]\n" \
X_"a[178:179], v36 offset:33792\n" \
"s_waitcnt lgkmcnt(8)\n" \
M_"a[12:15], a[128:131], a[172:175], a[12:15]\n" \
X_"a[240:241], v36 offset:40960\n" \
"s_waitcnt lgkmcnt(5)\n" \
M_"a[12:15], a[192:195], a[236:239], a[12:15]\n" \
M_"a[28:31], a[132:135], a[172:175], a[28:31]\n" \
M_"a[28:31], a[196:199], a[236:239], a[28:31]\n" \
X_"a[242:243], v36 offset:41984\n" \
M_"a[24:27], a[132:135], a[168:171], a[24:27]\n" \
M_"a[24:27], a[196:199], a[232:235], a[24:27]\n" \
M_"a[20:23], a[132:135], a[164:167], a[20:23]\n" \
M_"a[20:23], a[196:199], a[228:231], a[20:23]\n" \
X_"a[180:181], v37 offset:32768\n" \
M_"a[16:19], a[132:135], a[160:163], a[16:19]\n" \
M_"a[16:19], a[196:199], a[224:227], a[16:19]\n" \
M_"a[32:35], a[136:139], a[160:163], a[32:35]\n" \
X_"a[182:183], v37 offset:33792\n" \
M_"a[32:35], a[200:203], a[224:227], a[32:35]\n" \
M_"a[36:39], a[136:139], a[164:167], a[36:39]\n" \
"v_lshlrev_b32_e32 v45, 3, v20\n" \
"v_add_u32_e32 v45, s31, v45\n" \
M_"a[36:39], a[200:203], a[228:231], a[36:39]\n" \
"s_getpc_b64 s[86:87]\n" \
"s_sub_u32 s86, s86, 0x10800\n" \
"s_subb_u32 s87, s87, 0\n" \
"s_mov_b32 m0, s32\n" \
"s_nop 0\n" \
M_"a[40:43], a[136:139], a[168:171], a[40:43]\n" \
"global_load_lds_dword v45, s[86:87]\n" \
M_"a[40:43], a[200:203], a[232:235], a[40:43]\n" \
X_"a[244:245], v37 offset:40960\n" \
M_"a[44:47], a[136:139], a[172:175], a[44:47]\n" \
X_"a[246:247], v37 offset:41984\n" \
M_"a[44:47], a[200:203], a[236:239], a[44:47]\n" \
M_"a[60:63], a[140:143], a[172:175], a[60:63]\n" \
M_"a[60:63], a[204:207], a[236:239], a[60:63]\n" \
M_"a[56:59], a[140:143], a[168:171], a[56:59]\n" \
X_"a[184:185], v38 offset:32768\n" \
M_"a[56:59], a[204:207], a[232:235], a[56:59]\n" \
M_"a[52:55], a[140:143], a[164:167], a[52:55]\n" \
M_"a[52:55], a[204:207], a[228:231], a[52:55]\n" \
X_"a[186:187], v38 offset:33792\n" \
M_"a[48:51], a[140:143], a[160:163], a[48:51]\n" \
M_"a[48:51], a[204:207], a[224:227], a[48:51]\n" \
M_"a[64:67], a[144:147], a[160:163], a[64:67]\n" \
X_"a[248:249], v38 offset:40960\n" \
M_"a[64:67], a[208:211], a[224:227], a[64:67]\n" \
M_"a[68:71], a[144:147], a[164:167], a[68:71]\n" \
M_"a[68:71], a[208:211], a[228:231], a[68:71]\n" \
X_"a[250:251], v38 offset:41984\n" \
M_"a[72:75], a[144:147], a[168:171], a[72:75]\n" \
M_"a[72:75], a[208:211], a[232:235], a[72:75]\n" \
M_"a[76:79], a[144:147], a[172:175], a[76:79]\n" \
M_"a[76:79], a[208:211], a[236:239], a[76:79]\n" \
X_"a[188:189], v39 offset:32768\n" \
M_"a[92:95], a[148:151], a[172:175], a[92:95]\n" \
M_"a[92:95], a[212:215], a[236:239], a[92:95]\n" \
M_"a[88:91], a[148:151], a[168:171], a[88:91]\n" \
X_"a[190:191], v39 offset:33792\n" \
M_"a[88:91], a[212:215], a[232:235], a[88:91]\n" \
M_"a[84:87], a[148:151], a[164:167], a[84:87]\n" \
M_"a[84:87], a[212:215], a[228:231], a[84:87]\n" \
X_"a[252:253], v39 offset:40960\n" \
M_"a[80:83], a[148:151], a[160:163], a[80:83]\n" \
M_"a[80:83], a[212:215], a[224:227], a[80:83]\n" \
M_"a[96:99], a[152:155], a[160:163], a[96:99]\n" \
X_"a[254:255], v39 offset:41984\n" \
M_"a[96:99], a[216:219], a[224:227], a[96:99]\n" \
M_"a[100:103], a[152:155], a[164:167], a[100:103]\n" \
M_"a[100:103], a[216:219], a[228:231], a[100:103]\n" \
M_"a[104:107], a[152:155], a[168:171], a[104:107]\n" \
M_"a[104:107], a[216:219], a[232:235], a[104:107]\n" \
M_"a[108:111], a[152:155], a[172:175], a[108:111]\n" \
M_"a[108:111], a[216:219], a[236:239], a[108:111]\n" \
"s_waitcnt lgkmcnt(15)\n" \
M_"a[124:127], a[156:159], a[172:175], a[124:127]\n" \
M_"a[124:127], a[220:223], a[236:239], a[124:127]\n" \
M_"a[120:123], a[156:159], a[168:171], a[120:123]\n" \
M_"a[120:123], a[220:223], a[232:235], a[120:123]\n" \
M_"a[116:119], a[156:159], a[164:167], a[116:119]\n" \
M_"a[116:119], a[220:223], a[228:231], a[116:119]\n" \
M_"a[112:115], a[156:159], a[160:163], a[112:115]\n" \
"s_waitcnt vmcnt(1) lgkmcnt(0)\n" \
M_"a[112:115], a[220:223], a[224:227], a[112:115]\n" \
"s_barrier\n" \
"v_mov_b32_e32 v45, s62\n" \
"s_mov_b64 exec, 1\n" \
"buffer_store_dword v45, off, s[80:83], s37 offset:128 sc1\n" \
"s_mov_b64 exec, -1\n" \
M_"v[52:55], a[128:131], a[176:179], v[52:55]\n" \
"s_add_u32 m0, s30, 0x8000\n" \
"s_movk_i32 s84, 0x380\n" \
M_"v[52:55], a[192:195], a[240:243], v[52:55]\n" \
B_"v22, s[64:67], s84 offen offset:0 lds\n" \
M_"v[56:59], a[128:131], a[180:183], v[56:59]\n" \
X_"a[160:161], v32 offset:0\n" \
M_"v[56:59], a[192:195], a[244:247], v[56:59]\n" \
"s_movk_i32 s84, 0x1f80\n" \
M_"v[60:63], a[128:131], a[184:187], v[60:63]\n" \
B_"v23, s[64:67], s84 offen offset:1024 lds\n" \
M_"v[60:63], a[192:195], a[248:251], v[60:63]\n" \
X_"a[162:163], v32 offset:1024\n" \
M_"v[64:67], a[128:131], a[188:191], v[64:67]\n" \
X_"a[224:225], v32 offset:8192\n" \
M_"v[64:67], a[192:195], a[252:255], v[64:67]\n" \
R_"a[128:131], v28 offset:16384\n" \
M_"v[80:83], a[132:135], a[188:191], v[80:83]\n" \
R_"a[192:195], v29 offset:16384\n" \
M_"v[80:83], a[196:199], a[252:255], v[80:83]\n" \
"s_movk_i32 s84, 0x3b80\n" \
M_"v[76:79], a[132:135], a[184:187], v[76:79]\n" \
B_"v22, s[64:67], s84 offen offset:2048 lds\n" \
M_"v[76:79], a[196:199], a[248:251], v[76:79]\n" \
"s_movk_i32 s84, 0x5780\n" \
M_"v[72:75], a[132:135], a[180:183], v[72:75]\n" \
B_"v23, s[64:67], s84 offen offset:3072 lds\n" \
M_"v[72:75], a[196:199], a[244:247], v[72:75]\n" \
"s_add_u32 m0, s31, 0x8000\n" \
"s_mov_b32 s84, 0x380000\n" \
M_"v[68:71], a[132:135], a[176:179], v[68:71]\n" \
B_"v26, s[68:71], s84 offen offset:0 lds\n" \
M_"v[68:71], a[196:199], a[240:243], v[68:71]\n" \
R_"a[132:135], v28 offset:18432\n" \
M_"v[84:87], a[136:139], a[176:179], v[84:87]\n" \
R_"a[196:199], v29 offset:18432\n" \
M_"v[84:87], a[200:203], a[240:243], v[84:87]\n" \
"s_addk_i32 s84, 0x7c00\n" \
M_"v[88:91], a[136:139], a[180:183], v[88:91]\n" \
B_"v26, s[68:71], s84 offen offset:1024 lds\n" \
M_"v[88:91], a[200:203], a[244:247], v[88:91]\n" \
X_"a[226:227], v32 offset:9216\n" \
M_"v[92:95], a[136:139], a[184:187], v[92:95]\n" \
"s_addk_i32 s84, 0x7c00\n" \
M_"v[92:95], a[200:203], a[248:251], v[92:95]\n" \
B_"v27, s[68:71], s84 offen offset:2048 lds\n" \
M_"v[96:99], a[136:139], a[188:191], v[96:99]\n" \
X_"a[164:165], v33 offset:0\n" \
M_"v[96:99], a[200:203], a[252:255], v[96:99]\n" \
R_"a[136:139], v28 offset:20480\n" \
M_"v[112:115], a[140:143], a[188:191], v[112:115]\n" \
R_"a[200:203], v29 offset:20480\n" \
M_"v[112:115], a[204:207], a[252:255], v[112:115]\n" \
"s_addk_i32 s84, 0x7c00\n" \
"s_nop 0\n" \
M_"v[108:111], a[140:143], a[184:187], v[108:111]\n" \
B_"v27, s[68:71], s84 offen offset:3072 lds\n" \
M_"v[108:111], a[204:207], a[248:251], v[108:111]\n" \
"s_add_u32 m0, s30, 0x9000\n" \
"s_mov_b32 s84, 0x8380\n" \
M_"v[104:107], a[140:143], a[180:183], v[104:107]\n" \
B_"v22, s[64:67], s84 offen offset:0 lds\n" \
M_"v[104:107], a[204:207], a[244:247], v[104:107]\n" \
X_"a[166:167], v33 offset:1024\n" \
M_"v[100:103], a[140:143], a[176:179], v[100:103]\n" \
X_"a[228:229], v33 offset:8192\n" \
M_"v[100:103], a[204:207], a[240:243], v[100:103]\n" \
R_"a[140:143], v28 offset:22528\n" \
M_"v[116:119], a[144:147], a[176:179], v[116:119]\n" \
R_"a[204:207], v29 offset:22528\n" \
M_"v[116:119], a[208:211], a[240:243], v[116:119]\n" \
"s_addk_i32 s84, 0x1c00\n" \
M_"v[120:123], a[144:147], a[180:183], v[120:123]\n" \
B_"v23, s[64:67], s84 offen offset:1024 lds\n" \
M_"v[120:123], a[208:211], a[244:247], v[120:123]\n" \
"s_addk_i32 s84, 0x1c00\n" \
M_"v[124:127], a[144:147], a[184:187], v[124:127]\n" \
B_"v22, s[64:67], s84 offen offset:2048 lds\n" \
M_"v[124:127], a[208:211], a[248:251], v[124:127]\n" \
"s_addk_i32 s84, 0x1c00\n" \
"s_nop 0\n" \
M_"v[128:131], a[144:147], a[188:191], v[128:131]\n" \
B_"v23, s[64:67], s84 offen offset:3072 lds\n" \
M_"v[128:131], a[208:211], a[252:255], v[128:131]\n" \
R_"a[144:147], v28 offset:0\n" \
M_"v[144:147], a[148:151], a[188:191], v[144:147]\n" \
R_"a[208:211], v29 offset:0\n" \
M_"v[144:147], a[212:215], a[252:255], v[144:147]\n" \
"s_add_u32 m0, s31, 0xc000\n" \
"s_mov_b32 s84, 0x380000\n" \
M_"v[140:143], a[148:151], a[184:187], v[140:143]\n" \
B_"v26, s[72:75], s84 offen offset:0 lds\n" \
M_"v[140:143], a[212:215], a[248:251], v[140:143]\n" \
X_"a[230:231], v33 offset:9216\n" \
M_"v[136:139], a[148:151], a[180:183], v[136:139]\n" \
"s_addk_i32 s84, 0x7c00\n" \
"s_nop 0\n" \
M_"v[136:139], a[212:215], a[244:247], v[136:139]\n" \
B_"v26, s[72:75], s84 offen offset:1024 lds\n" \
M_"v[132:135], a[148:151], a[176:179], v[132:135]\n" \
X_"a[168:169], v34 offset:0\n" \
M_"v[132:135], a[212:215], a[240:243], v[132:135]\n" \
R_"a[148:151], v28 offset:2048\n" \
M_"v[148:151], a[152:155], a[176:179], v[148:151]\n" \
R_"a[212:215], v29 offset:2048\n" \
M_"v[148:151], a[216:219], a[240:243], v[148:151]\n" \
X_"a[170:171], v34 offset:1024\n" \
M_"v[152:155], a[152:155], a[180:183], v[152:155]\n" \
X_"a[232:233], v34 offset:8192\n" \
M_"v[152:155], a[216:219], a[244:247], v[152:155]\n" \
X_"a[234:235], v34 offset:9216\n" \
M_"v[156:159], a[152:155], a[184:187], v[156:159]\n" \
X_"a[172:173], v35 offset:0\n" \
M_"v[156:159], a[216:219], a[248:251], v[156:159]\n" \
X_"a[174:175], v35 offset:1024\n" \
M_"v[160:163], a[152:155], a[188:191], v[160:163]\n" \
X_"a[236:237], v35 offset:8192\n" \
M_"v[160:163], a[216:219], a[252:255], v[160:163]\n" \
R_"a[152:155], v28 offset:4096\n" \
M_"v[176:179], a[156:159], a[188:191], v[176:179]\n" \
R_"a[216:219], v29 offset:4096\n" \
M_"v[176:179], a[220:223], a[252:255], v[176:179]\n" \
X_"a[238:239], v35 offset:9216\n" \
M_"v[172:175], a[156:159], a[184:187], v[172:175]\n" \
M_"v[172:175], a[220:223], a[248:251], v[172:175]\n" \
M_"v[168:171], a[156:159], a[180:183], v[168:171]\n" \
M_"v[168:171], a[220:223], a[244:247], v[168:171]\n" \
M_"v[164:167], a[156:159], a[176:179], v[164:167]\n" \
M_"v[164:167], a[220:223], a[240:243], v[164:167]\n" \
R_"a[156:159], v28 offset:6144\n" \
R_"a[220:223], v29 offset:6144\n" \
"s_waitcnt lgkmcnt(15)\n" \
M_"a[0:3], a[128:131], a[160:163], a[0:3]\n" \
"s_addk_i32 s84, 0x7c00\n" \
M_"a[0:3], a[192:195], a[224:227], a[0:3]\n" \
B_"v27, s[72:75], s84 offen offset:2048 lds\n" \
M_"a[4:7], a[128:131], a[164:167], a[4:7]\n" \
X_"a[176:177], v36 offset:0\n" \
"s_waitcnt lgkmcnt(15)\n" \
M_"a[4:7], a[192:195], a[228:231], a[4:7]\n" \
"s_addk_i32 s84, 0x7c00\n" \
"s_waitcnt lgkmcnt(11)\n" \
M_"a[8:11], a[128:131], a[168:171], a[8:11]\n" \
B_"v27, s[72:75], s84 offen offset:3072 lds\n" \
"s_waitcnt lgkmcnt(9)\n" \
M_"a[8:11], a[192:195], a[232:235], a[8:11]\n" \
X_"a[178:179], v36 offset:1024\n" \
"s_waitcnt lgkmcnt(8)\n" \
M_"a[12:15], a[128:131], a[172:175], a[12:15]\n" \
X_"a[240:241], v36 offset:8192\n" \
"s_waitcnt lgkmcnt(5)\n" \
M_"a[12:15], a[192:195], a[236:239], a[12:15]\n" \
M_"a[28:31], a[132:135], a[172:175], a[28:31]\n" \
M_"a[28:31], a[196:199], a[236:239], a[28:31]\n" \
X_"a[242:243], v36 offset:9216\n" \
M_"a[24:27], a[132:135], a[168:171], a[24:27]\n" \
M_"a[24:27], a[196:199], a[232:235], a[24:27]\n" \
M_"a[20:23], a[132:135], a[164:167], a[20:23]\n" \
M_"a[20:23], a[196:199], a[228:231], a[20:23]\n" \
X_"a[180:181], v37 offset:0\n" \
M_"a[16:19], a[132:135], a[160:163], a[16:19]\n" \
M_"a[16:19], a[196:199], a[224:227], a[16:19]\n" \
M_"a[32:35], a[136:139], a[160:163], a[32:35]\n" \
"v_bfe_u32 v44, v20, 2, 5\n" \
X_"a[182:183], v37 offset:1024\n" \
M_"a[32:35], a[200:203], a[224:227], a[32:35]\n" \
"buffer_load_dword v44, v44, s[80:83], s38 offen offset:128 sc1\n" \
M_"a[36:39], a[136:139], a[164:167], a[36:39]\n" \
M_"a[36:39], a[200:203], a[228:231], a[36:39]\n" \
X_"a[244:245], v37 offset:8192\n" \
M_"a[40:43], a[136:139], a[168:171], a[40:43]\n" \
M_"a[40:43], a[200:203], a[232:235], a[40:43]\n" \
M_"a[44:47], a[136:139], a[172:175], a[44:47]\n" \
X_"a[246:247], v37 offset:9216\n" \
M_"a[44:47], a[200:203], a[236:239], a[44:47]\n" \
M_"a[60:63], a[140:143], a[172:175], a[60:63]\n" \
"global_store_dwordx4 v49, v[0:3], s[28:29] offset:0 sc1\n" \
M_"a[60:63], a[204:207], a[236:239], a[60:63]\n" \
M_"a[56:59], a[140:143], a[168:171], a[56:59]\n" \
X_"a[184:185], v38 offset:0\n" \
M_"a[56:59], a[204:207], a[232:235], a[56:59]\n" \
M_"a[52:55], a[140:143], a[164:167], a[52:55]\n" \
M_"a[52:55], a[204:207], a[228:231], a[52:55]\n" \
X_"a[186:187], v38 offset:1024\n" \
M_"a[48:51], a[140:143], a[160:163], a[48:51]\n" \
M_"a[48:51], a[204:207], a[224:227], a[48:51]\n" \
M_"a[64:67], a[144:147], a[160:163], a[64:67]\n" \
"global_store_dwordx4 v49, v[0:3], s[28:29] offset:1024 sc1\n" \
M_"a[64:67], a[208:211], a[224:227], a[64:67]\n" \
X_"a[248:249], v38 offset:8192\n" \
M_"a[68:71], a[144:147], a[164:167], a[68:71]\n" \
M_"a[68:71], a[208:211], a[228:231], a[68:71]\n" \
X_"a[250:251], v38 offset:9216\n" \
M_"a[72:75], a[144:147], a[168:171], a[72:75]\n" \
M_"a[72:75], a[208:211], a[232:235], a[72:75]\n" \
M_"a[76:79], a[144:147], a[172:175], a[76:79]\n" \
M_"a[76:79], a[208:211], a[236:239], a[76:79]\n" \
X_"a[188:189], v39 offset:0\n" \
M_"a[92:95], a[148:151], a[172:175], a[92:95]\n" \
"s_mov_b64 exec, 0xffff\n" \
"global_store_dwordx4 v49, v[0:3], s[28:29] offset:2048 sc1\n" \
"s_mov_b64 exec, -1\n" \
"s_nop 0\n" \
M_"a[92:95], a[212:215], a[236:239], a[92:95]\n" \
M_"a[88:91], a[148:151], a[168:171], a[88:91]\n" \
X_"a[190:191], v39 offset:1024\n" \
M_"a[88:91], a[212:215], a[232:235], a[88:91]\n" \
M_"a[84:87], a[148:151], a[164:167], a[84:87]\n" \
M_"a[84:87], a[212:215], a[228:231], a[84:87]\n" \
X_"a[252:253], v39 offset:8192\n" \
M_"a[80:83], a[148:151], a[160:163], a[80:83]\n" \
M_"a[80:83], a[212:215], a[224:227], a[80:83]\n" \
M_"a[96:99], a[152:155], a[160:163], a[96:99]\n" \
X_"a[254:255], v39 offset:9216\n" \
M_"a[96:99], a[216:219], a[224:227], a[96:99]\n" \
M_"a[100:103], a[152:155], a[164:167], a[100:103]\n" \
M_"a[100:103], a[216:219], a[228:231], a[100:103]\n" \
M_"a[104:107], a[152:155], a[168:171], a[104:107]\n" \
M_"a[104:107], a[216:219], a[232:235], a[104:107]\n" \
M_"a[108:111], a[152:155], a[172:175], a[108:111]\n" \
M_"a[108:111], a[216:219], a[236:239], a[108:111]\n" \
"s_waitcnt lgkmcnt(15)\n" \
M_"a[124:127], a[156:159], a[172:175], a[124:127]\n" \
M_"a[124:127], a[220:223], a[236:239], a[124:127]\n" \
M_"a[120:123], a[156:159], a[168:171], a[120:123]\n" \
M_"a[120:123], a[220:223], a[232:235], a[120:123]\n" \
M_"a[116:119], a[156:159], a[164:167], a[116:119]\n" \
M_"a[116:119], a[220:223], a[228:231], a[116:119]\n" \
M_"a[112:115], a[156:159], a[160:163], a[112:115]\n" \
"s_waitcnt vmcnt(3) lgkmcnt(0)\n" \
M_"a[112:115], a[220:223], a[224:227], a[112:115]\n" \
"s_barrier\n" \
"v_cmp_ne_u32_e32 vcc, s62, v44\n" \
"s_nop 4\n" \
"s_cbranch_vccnz L_poll61_%=\n" \
"L_pollr61_%=:\n" \
"s_mov_b32 s68, s12\n" \
"s_and_b32 s69, s13, 0xffff\n" \
"s_mov_b32 s70, 0x480000\n" \
"s_mov_b32 s71, 0x20000\n" \
M_"v[52:55], a[128:131], a[176:179], v[52:55]\n" \
"s_add_u32 m0, s40, 0x0\n" \
".fill (L_pb + 57056 - .) / 4, 4, 0xBF800000\n" \
"L_pp14:\n" \
"s_cbranch_execz L_pret\n" \
"s_add_u32 s85, s39, 0x0\n" \
M_"v[52:55], a[192:195], a[240:243], v[52:55]\n" \
B_"v20, s[80:83], s85 offen sc1 lds\n" \
M_"v[56:59], a[128:131], a[180:183], v[56:59]\n" \
X_"a[160:161], v32 offset:32768\n" \
M_"v[56:59], a[192:195], a[244:247], v[56:59]\n" \
"s_add_u32 m0, s40, 0x1000\n" \
"s_add_u32 s85, s39, 0x1000\n" \
M_"v[60:63], a[128:131], a[184:187], v[60:63]\n" \
B_"v20, s[80:83], s85 offen sc1 lds\n" \
M_"v[60:63], a[192:195], a[248:251], v[60:63]\n" \
X_"a[162:163], v32 offset:33792\n" \
M_"v[64:67], a[128:131], a[188:191], v[64:67]\n" \
X_"a[224:225], v32 offset:40960\n" \
M_"v[64:67], a[192:195], a[252:255], v[64:67]\n" \
R_"a[128:131], v28 offset:49152\n" \
M_"v[80:83], a[132:135], a[188:191], v[80:83]\n" \
R_"a[192:195], v29 offset:49152\n" \
M_"v[80:83], a[196:199], a[252:255], v[80:83]\n" \
"s_add_u32 m0, s40, 0x3000\n" \
"s_movk_i32 s84, 0x0\n" \
M_"v[76:79], a[132:135], a[184:187], v[76:79]\n" \
B_"v21, s[76:79], s84 offen offset:0 lds\n" \
M_"v[76:79], a[196:199], a[248:251], v[76:79]\n" \
"s_add_u32 m0, s30, 0x0\n" \
"s_movk_i32 s84, 0x0\n" \
"s_nop 0\n" \
M_"v[72:75], a[132:135], a[180:183], v[72:75]\n" \
B_"v24, s[76:79], s84 offen offset:0 lds\n" \
M_"v[72:75], a[196:199], a[244:247], v[72:75]\n" \
X_"a[226:227], v32 offset:41984\n" \
M_"v[68:71], a[132:135], a[176:179], v[68:71]\n" \
X_"a[164:165], v33 offset:32768\n" \
M_"v[68:71], a[196:199], a[240:243], v[68:71]\n" \
R_"a[132:135], v28 offset:51200\n" \
M_"v[84:87], a[136:139], a[176:179], v[84:87]\n" \
R_"a[196:199], v29 offset:51200\n" \
M_"v[84:87], a[200:203], a[240:243], v[84:87]\n" \
"s_movk_i32 s84, 0x2000\n" \
M_"v[88:91], a[136:139], a[180:183], v[88:91]\n" \
B_"v25, s[76:79], s84 offen offset:1024 lds\n" \
M_"v[88:91], a[200:203], a[244:247], v[88:91]\n" \
"s_movk_i32 s84, 0x4000\n" \
M_"v[92:95], a[136:139], a[184:187], v[92:95]\n" \
B_"v24, s[76:79], s84 offen offset:2048 lds\n" \
M_"v[92:95], a[200:203], a[248:251], v[92:95]\n" \
X_"a[166:167], v33 offset:33792\n" \
M_"v[96:99], a[136:139], a[188:191], v[96:99]\n" \
X_"a[228:229], v33 offset:40960\n" \
M_"v[96:99], a[200:203], a[252:255], v[96:99]\n" \
R_"a[136:139], v28 offset:53248\n" \
M_"v[112:115], a[140:143], a[188:191], v[112:115]\n" \
R_"a[200:203], v29 offset:53248\n" \
M_"v[112:115], a[204:207], a[252:255], v[112:115]\n" \
"s_movk_i32 s84, 0x6000\n" \
M_"v[108:111], a[140:143], a[184:187], v[108:111]\n" \
B_"v25, s[76:79], s84 offen offset:3072 lds\n" \
M_"v[108:111], a[204:207], a[248:251], v[108:111]\n" \
"s_add_u32 m0, s31, 0x0\n" \
"s_movk_i32 s84, 0x0\n" \
M_"v[104:107], a[140:143], a[180:183], v[104:107]\n" \
B_"v26, s[68:71], s84 offen offset:0 lds\n" \
M_"v[104:107], a[204:207], a[244:247], v[104:107]\n" \
"s_movk_i32 s84, 0x7c00\n" \
M_"v[100:103], a[140:143], a[176:179], v[100:103]\n" \
B_"v26, s[68:71], s84 offen offset:1024 lds\n" \
M_"v[100:103], a[204:207], a[240:243], v[100:103]\n" \
R_"a[140:143], v28 offset:55296\n" \
M_"v[116:119], a[144:147], a[176:179], v[116:119]\n" \
R_"a[204:207], v29 offset:55296\n" \
M_"v[116:119], a[208:211], a[240:243], v[116:119]\n" \
"s_addk_i32 s84, 0x7c00\n" \
M_"v[120:123], a[144:147], a[180:183], v[120:123]\n" \
B_"v27, s[68:71], s84 offen offset:2048 lds\n" \
M_"v[120:123], a[208:211], a[244:247], v[120:123]\n" \
X_"a[230:231], v33 offset:41984\n" \
M_"v[124:127], a[144:147], a[184:187], v[124:127]\n" \
"s_addk_i32 s84, 0x7c00\n" \
M_"v[124:127], a[208:211], a[248:251], v[124:127]\n" \
B_"v27, s[68:71], s84 offen offset:3072 lds\n" \
M_"v[128:131], a[144:147], a[188:191], v[128:131]\n" \
X_"a[168:169], v34 offset:32768\n" \
M_"v[128:131], a[208:211], a[252:255], v[128:131]\n" \
R_"a[144:147], v28 offset:32768\n" \
M_"v[144:147], a[148:151], a[188:191], v[144:147]\n" \
R_"a[208:211], v29 offset:32768\n" \
M_"v[144:147], a[212:215], a[252:255], v[144:147]\n" \
"s_add_u32 m0, s30, 0x1000\n" \
"s_mov_b32 s84, 0x9000\n" \
M_"v[140:143], a[148:151], a[184:187], v[140:143]\n" \
B_"v24, s[76:79], s84 offen offset:0 lds\n" \
M_"v[140:143], a[212:215], a[248:251], v[140:143]\n" \
X_"a[170:171], v34 offset:33792\n" \
M_"v[136:139], a[148:151], a[180:183], v[136:139]\n" \
"s_addk_i32 s84, 0x2000\n" \
"s_nop 0\n" \
M_"v[136:139], a[212:215], a[244:247], v[136:139]\n" \
B_"v25, s[76:79], s84 offen offset:1024 lds\n" \
M_"v[132:135], a[148:151], a[176:179], v[132:135]\n" \
X_"a[232:233], v34 offset:40960\n" \
M_"v[132:135], a[212:215], a[240:243], v[132:135]\n" \
R_"a[148:151], v28 offset:34816\n" \
M_"v[148:151], a[152:155], a[176:179], v[148:151]\n" \
R_"a[212:215], v29 offset:34816\n" \
M_"v[148:151], a[216:219], a[240:243], v[148:151]\n" \
X_"a[234:235], v34 offset:41984\n" \
M_"v[152:155], a[152:155], a[180:183], v[152:155]\n" \
X_"a[172:173], v35 offset:32768\n" \
M_"v[152:155], a[216:219], a[244:247], v[152:155]\n" \
X_"a[174:175], v35 offset:33792\n" \
M_"v[156:159], a[152:155], a[184:187], v[156:159]\n" \
X_"a[236:237], v35 offset:40960\n" \
M_"v[156:159], a[216:219], a[248:251], v[156:159]\n" \
X_"a[238:239], v35 offset:41984\n" \
M_"v[160:163], a[152:155], a[188:191], v[160:163]\n" \
M_"v[160:163], a[216:219], a[252:255], v[160:163]\n" \
R_"a[152:155], v28 offset:36864\n" \
M_"v[176:179], a[156:159], a[188:191], v[176:179]\n" \
R_"a[216:219], v29 offset:36864\n" \
M_"v[176:179], a[220:223], a[252:255], v[176:179]\n" \
M_"v[172:175], a[156:159], a[184:187], v[172:175]\n" \
M_"v[172:175], a[220:223], a[248:251], v[172:175]\n" \
M_"v[168:171], a[156:159], a[180:183], v[168:171]\n" \
M_"v[168:171], a[220:223], a[244:247], v[168:171]\n" \
M_"v[164:167], a[156:159], a[176:179], v[164:167]\n" \
M_"v[164:167], a[220:223], a[240:243], v[164:167]\n" \
R_"a[156:159], v28 offset:38912\n" \
R_"a[220:223], v29 offset:38912\n" \
"s_waitcnt lgkmcnt(15)\n" \
M_"a[0:3], a[128:131], a[160:163], a[0:3]\n" \
"s_addk_i32 s84, 0x2000\n" \
M_"a[0:3], a[192:195], a[224:227], a[0:3]\n" \
B_"v24, s[76:79], s84 offen offset:2048 lds\n" \
M_"a[4:7], a[128:131], a[164:167], a[4:7]\n" \
X_"a[176:177], v36 offset:32768\n" \
M_"a[4:7], a[192:195], a[228:231], a[4:7]\n" \
"s_addk_i32 s84, 0x2000\n" \
"s_waitcnt lgkmcnt(13)\n" \
M_"a[8:11], a[128:131], a[168:171], a[8:11]\n" \
B_"v25, s[76:79], s84 offen offset:3072 lds\n" \
"s_waitcnt lgkmcnt(9)\n" \
M_"a[8:11], a[192:195], a[232:235], a[8:11]\n" \
X_"a[178:179], v36 offset:33792\n" \
"s_waitcnt lgkmcnt(8)\n" \
M_"a[12:15], a[128:131], a[172:175], a[12:15]\n" \
X_"a[240:241], v36 offset:40960\n" \
"s_waitcnt lgkmcnt(7)\n" \
M_"a[12:15], a[192:195], a[236:239], a[12:15]\n" \
M_"a[28:31], a[132:135], a[172:175], a[28:31]\n" \
M_"a[28:31], a[196:199], a[236:239], a[28:31]\n" \
"s_add_u32 m0, s30, 0x20000\n" \
"s_movk_i32 s84, 0x100\n" \
M_"a[24:27], a[132:135], a[168:171], a[24:27]\n" \
B_"v24, s[76:79], s84 offen offset:0 lds\n" \
M_"a[24:27], a[196:199], a[232:235], a[24:27]\n" \
X_"a[242:243], v36 offset:41984\n" \
M_"a[20:23], a[132:135], a[164:167], a[20:23]\n" \
"v_accvgpr_read_b32 v184, a12\n" \
"v_accvgpr_read_b32 v185, a13\n" \
M_"a[20:23], a[196:199], a[228:231], a[20:23]\n" \
X_"a[180:181], v37 offset:32768\n" \
"v_accvgpr_read_b32 v186, a14\n" \
M_"a[16:19], a[132:135], a[160:163], a[16:19]\n" \
"s_movk_i32 s84, 0x2100\n" \
"s_nop 0\n" \
M_"a[16:19], a[196:199], a[224:227], a[16:19]\n" \
B_"v25, s[76:79], s84 offen offset:1024 lds\n" \
M_"a[32:35], a[136:139], a[160:163], a[32:35]\n" \
X_"a[182:183], v37 offset:33792\n" \
"v_accvgpr_read_b32 v187, a15\n" \
M_"a[32:35], a[200:203], a[224:227], a[32:35]\n" \
"v_accvgpr_read_b32 v180, a28\n" \
"v_accvgpr_read_b32 v181, a29\n" \
M_"a[36:39], a[136:139], a[164:167], a[36:39]\n" \
"v_accvgpr_read_b32 v182, a30\n" \
"v_accvgpr_read_b32 v183, a31\n" \
M_"a[36:39], a[200:203], a[228:231], a[36:39]\n" \
"s_movk_i32 s84, 0x4100\n" \
"s_nop 0\n" \
M_"a[40:43], a[136:139], a[168:171], a[40:43]\n" \
B_"v24, s[76:79], s84 offen offset:2048 lds\n" \
M_"a[40:43], a[200:203], a[232:235], a[40:43]\n" \
X_"a[244:245], v37 offset:40960\n" \
C_"v184, v184, v185\n" \
M_"a[44:47], a[136:139], a[172:175], a[44:47]\n" \
X_"a[246:247], v37 offset:41984\n" \
C_"v185, v186, v187\n" \
M_"a[44:47], a[200:203], a[236:239], a[44:47]\n" \
C_"v186, v180, v181\n" \
C_"v187, v182, v183\n" \
M_"a[60:63], a[140:143], a[172:175], a[60:63]\n" \
"s_movk_i32 s84, 0x6100\n" \
M_"a[60:63], a[204:207], a[236:239], a[60:63]\n" \
B_"v25, s[76:79], s84 offen offset:3072 lds\n" \
M_"a[56:59], a[140:143], a[168:171], a[56:59]\n" \
X_"a[184:185], v38 offset:32768\n" \
"v_accvgpr_read_b32 v188, a8\n" \
M_"a[56:59], a[204:207], a[232:235], a[56:59]\n" \
"v_accvgpr_read_b32 v189, a9\n" \
"v_accvgpr_read_b32 v190, a10\n" \
M_"a[52:55], a[140:143], a[164:167], a[52:55]\n" \
"s_add_u32 m0, s30, 0x21000\n" \
"s_addk_i32 s84, 0x3000\n" \
M_"a[52:55], a[204:207], a[228:231], a[52:55]\n" \
B_"v24, s[76:79], s84 offen offset:0 lds\n" \
M_"a[48:51], a[140:143], a[160:163], a[48:51]\n" \
X_"a[186:187], v38 offset:33792\n" \
"v_accvgpr_read_b32 v191, a11\n" \
M_"a[48:51], a[204:207], a[224:227], a[48:51]\n" \
"v_accvgpr_read_b32 v180, a24\n" \
"v_accvgpr_read_b32 v181, a25\n" \
M_"a[64:67], a[144:147], a[160:163], a[64:67]\n" \
X_"a[248:249], v38 offset:40960\n" \
"v_accvgpr_read_b32 v182, a26\n" \
M_"a[64:67], a[208:211], a[224:227], a[64:67]\n" \
"s_addk_i32 s84, 0x2000\n" \
"s_nop 0\n" \
M_"a[68:71], a[144:147], a[164:167], a[68:71]\n" \
B_"v25, s[76:79], s84 offen offset:1024 lds\n" \
M_"a[68:71], a[208:211], a[228:231], a[68:71]\n" \
X_"a[250:251], v38 offset:41984\n" \
"v_accvgpr_read_b32 v183, a27\n" \
M_"a[72:75], a[144:147], a[168:171], a[72:75]\n" \
C_"v188, v188, v189\n" \
C_"v189, v190, v191\n" \
M_"a[72:75], a[208:211], a[232:235], a[72:75]\n" \
C_"v190, v180, v181\n" \
C_"v191, v182, v183\n" \
M_"a[76:79], a[144:147], a[172:175], a[76:79]\n" \
"s_addk_i32 s84, 0x2000\n" \
M_"a[76:79], a[208:211], a[236:239], a[76:79]\n" \
B_"v24, s[76:79], s84 offen offset:2048 lds\n" \
M_"a[92:95], a[148:151], a[172:175], a[92:95]\n" \
"s_add_u32 s89, s41, 0x0\n" \
"buffer_store_dwordx4 v[184:187], v20, s[80:83], s89 offen offset:3072 sc1\n" \
M_"a[92:95], a[212:215], a[236:239], a[92:95]\n" \
X_"a[188:189], v39 offset:32768\n" \
"v_accvgpr_read_b32 v192, a4\n" \
M_"a[88:91], a[148:151], a[168:171], a[88:91]\n" \
X_"a[190:191], v39 offset:33792\n" \
"v_accvgpr_read_b32 v193, a5\n" \
M_"a[88:91], a[212:215], a[232:235], a[88:91]\n" \
"s_addk_i32 s84, 0x2000\n" \
M_"a[84:87], a[148:151], a[164:167], a[84:87]\n" \
B_"v25, s[76:79], s84 offen offset:3072 lds\n" \
M_"a[84:87], a[212:215], a[228:231], a[84:87]\n" \
X_"a[252:253], v39 offset:40960\n" \
"v_accvgpr_read_b32 v194, a6\n" \
M_"a[80:83], a[148:151], a[160:163], a[80:83]\n" \
"s_add_u32 s88, s41, 0x0\n" \
"buffer_store_dwordx4 v[188:191], v20, s[80:83], s88 offen offset:2048 sc1\n" \
M_"a[80:83], a[212:215], a[224:227], a[80:83]\n" \
"v_accvgpr_read_b32 v195, a7\n" \
"v_accvgpr_read_b32 v180, a20\n" \
"s_waitcnt lgkmcnt(15)\n" \
"s_nop 0\n" \
M_"a[96:99], a[152:155], a[160:163], a[96:99]\n" \
X_"a[254:255], v39 offset:41984\n" \
"v_accvgpr_read_b32 v181, a21\n" \
M_"a[96:99], a[216:219], a[224:227], a[96:99]\n" \
"v_accvgpr_read_b32 v182, a22\n" \
"v_accvgpr_read_b32 v183, a23\n" \
M_"a[100:103], a[152:155], a[164:167], a[100:103]\n" \
C_"v192, v192, v193\n" \
C_"v193, v194, v195\n" \
M_"a[100:103], a[216:219], a[228:231], a[100:103]\n" \
C_"v194, v180, v181\n" \
C_"v195, v182, v183\n" \
M_"a[104:107], a[152:155], a[168:171], a[104:107]\n" \
"s_add_u32 s89, s41, 0x0\n" \
"s_nop 0\n" \
"buffer_store_dwordx4 v[192:195], v20, s[80:83], s89 offen offset:1024 sc1\n" \
M_"a[104:107], a[216:219], a[232:235], a[104:107]\n" \
"v_accvgpr_read_b32 v196, a0\n" \
"v_accvgpr_read_b32 v197, a1\n" \
M_"a[108:111], a[152:155], a[172:175], a[108:111]\n" \
"v_accvgpr_read_b32 v198, a2\n" \
"v_accvgpr_read_b32 v199, a3\n" \
M_"a[108:111], a[216:219], a[236:239], a[108:111]\n" \
"v_accvgpr_read_b32 v180, a16\n" \
"v_accvgpr_read_b32 v181, a17\n" \
M_"a[124:127], a[156:159], a[172:175], a[124:127]\n" \
"v_accvgpr_read_b32 v182, a18\n" \
"v_accvgpr_read_b32 v183, a19\n" \
M_"a[124:127], a[220:223], a[236:239], a[124:127]\n" \
C_"v196, v196, v197\n" \
C_"v197, v198, v199\n" \
M_"a[120:123], a[156:159], a[168:171], a[120:123]\n" \
C_"v198, v180, v181\n" \
C_"v199, v182, v183\n" \
"s_add_u32 s88, s41, 0x0\n" \
"s_nop 0\n" \
"buffer_store_dwordx4 v[196:199], v20, s[80:83], s88 offen offset:0 sc1\n" \
M_"a[120:123], a[220:223], a[232:235], a[120:123]\n" \
"v_accvgpr_read_b32 v200, a44\n" \
"v_accvgpr_read_b32 v201, a45\n" \
M_"a[116:119], a[156:159], a[164:167], a[116:119]\n" \
"v_accvgpr_read_b32 v202, a46\n" \
"v_accvgpr_read_b32 v203, a47\n" \
M_"a[116:119], a[220:223], a[228:231], a[116:119]\n" \
"v_accvgpr_read_b32 v180, a60\n" \
"v_accvgpr_read_b32 v181, a61\n" \
M_"a[112:115], a[156:159], a[160:163], a[112:115]\n" \
"v_accvgpr_read_b32 v182, a62\n" \
"v_accvgpr_read_b32 v183, a63\n" \
M_"a[112:115], a[220:223], a[224:227], a[112:115]\n" \
C_"v200, v200, v201\n" \
C_"v201, v202, v203\n" \
"s_waitcnt vmcnt(12) lgkmcnt(0)\n" \
"s_barrier\n" \
"v_xor_b32_e32 v36, 0x4000, v36\n" \
"v_xor_b32_e64 v32, 0x0, v48\n" \
"v_xor_b32_e32 v37, 0x4000, v37\n" \
"v_xor_b32_e64 v33, 0x20, v48\n" \
"v_xor_b32_e32 v38, 0x4000, v38\n" \
"v_xor_b32_e64 v34, 0x40, v48\n" \
"v_xor_b32_e32 v39, 0x4000, v39\n" \
"v_xor_b32_e32 v35, 0x60, v48\n" \
"v_add_u32_e32 v28, s43, v28\n" \
"v_add_u32_e32 v29, s43, v29\n" \
M_"v[116:119], a[144:147], a[176:179], v[116:119]\n" \
"s_add_u32 m0, s40, 0x8000\n" \
"s_add_u32 s85, s39, 0x2000\n" \
M_"v[116:119], a[208:211], a[240:243], v[116:119]\n" \
B_"v20, s[80:83], s85 offen sc1 lds\n" \
M_"v[120:123], a[144:147], a[180:183], v[120:123]\n" \
X_"a[160:161], v32 offset:0\n" \
C_"v202, v180, v181\n" \
M_"v[120:123], a[208:211], a[244:247], v[120:123]\n" \
"s_add_u32 m0, s40, 0x9000\n" \
"s_add_u32 s85, s39, 0x3000\n" \
M_"v[124:127], a[144:147], a[184:187], v[124:127]\n" \
B_"v20, s[80:83], s85 offen sc1 lds\n" \
M_"v[124:127], a[208:211], a[248:251], v[124:127]\n" \
X_"a[162:163], v32 offset:512\n" \
C_"v203, v182, v183\n" \
M_"v[128:131], a[144:147], a[188:191], v[128:131]\n" \
X_"a[224:225], v32 offset:4096\n" \
"v_accvgpr_read_b32 v204, a40\n" \
M_"v[128:131], a[208:211], a[252:255], v[128:131]\n" \
R_"a[144:147], v28 offset:8192\n" \
"v_accvgpr_read_b32 v205, a41\n" \
M_"v[144:147], a[148:151], a[188:191], v[144:147]\n" \
R_"a[208:211], v29 offset:8192\n" \
"v_accvgpr_read_b32 v206, a42\n" \
M_"v[144:147], a[212:215], a[252:255], v[144:147]\n" \
"s_add_u32 m0, s40, 0xb000\n" \
"s_movk_i32 s84, 0x80\n" \
M_"v[140:143], a[148:151], a[184:187], v[140:143]\n" \
B_"v21, s[76:79], s84 offen offset:0 lds\n" \
M_"v[140:143], a[212:215], a[248:251], v[140:143]\n" \
"s_add_u32 m0, s30, 0x8000\n" \
"s_movk_i32 s84, 0x80\n" \
M_"v[136:139], a[148:151], a[180:183], v[136:139]\n" \
B_"v24, s[76:79], s84 offen offset:0 lds\n" \
M_"v[136:139], a[212:215], a[244:247], v[136:139]\n" \
X_"a[226:227], v32 offset:4608\n" \
"v_accvgpr_read_b32 v207, a43\n" \
M_"v[132:135], a[148:151], a[176:179], v[132:135]\n" \
X_"a[164:165], v33 offset:0\n" \
"v_accvgpr_read_b32 v180, a56\n" \
M_"v[132:135], a[212:215], a[240:243], v[132:135]\n" \
R_"a[148:151], v28 offset:10240\n" \
"v_accvgpr_read_b32 v181, a57\n" \
M_"v[148:151], a[152:155], a[176:179], v[148:151]\n" \
R_"a[212:215], v29 offset:10240\n" \
"v_accvgpr_read_b32 v182, a58\n" \
M_"v[148:151], a[216:219], a[240:243], v[148:151]\n" \
"s_movk_i32 s84, 0x2080\n" \
M_"v[152:155], a[152:155], a[180:183], v[152:155]\n" \
B_"v25, s[76:79], s84 offen offset:1024 lds\n" \
M_"v[152:155], a[216:219], a[244:247], v[152:155]\n" \
"s_movk_i32 s84, 0x4080\n" \
M_"v[156:159], a[152:155], a[184:187], v[156:159]\n" \
B_"v24, s[76:79], s84 offen offset:2048 lds\n" \
M_"v[156:159], a[216:219], a[248:251], v[156:159]\n" \
X_"a[166:167], v33 offset:512\n" \
"v_accvgpr_read_b32 v183, a59\n" \
M_"v[160:163], a[152:155], a[188:191], v[160:163]\n" \
X_"a[228:229], v33 offset:4096\n" \
C_"v204, v204, v205\n" \
M_"v[160:163], a[216:219], a[252:255], v[160:163]\n" \
R_"a[152:155], v28 offset:12288\n" \
C_"v205, v206, v207\n" \
M_"v[176:179], a[156:159], a[188:191], v[176:179]\n" \
R_"a[216:219], v29 offset:12288\n" \
C_"v206, v180, v181\n" \
M_"v[176:179], a[220:223], a[252:255], v[176:179]\n" \
"s_movk_i32 s84, 0x6080\n" \
M_"v[172:175], a[156:159], a[184:187], v[172:175]\n" \
B_"v25, s[76:79], s84 offen offset:3072 lds\n" \
M_"v[172:175], a[220:223], a[248:251], v[172:175]\n" \
"s_add_u32 m0, s31, 0x8000\n" \
"s_mov_b32 s84, 0x80000\n" \
M_"v[168:171], a[156:159], a[180:183], v[168:171]\n" \
B_"v26, s[68:71], s84 offen offset:0 lds\n" \
M_"v[168:171], a[220:223], a[244:247], v[168:171]\n" \
"s_addk_i32 s84, 0x7c00\n" \
M_"v[164:167], a[156:159], a[176:179], v[164:167]\n" \
B_"v26, s[68:71], s84 offen offset:1024 lds\n" \
M_"v[164:167], a[220:223], a[240:243], v[164:167]\n" \
R_"a[156:159], v28 offset:14336\n" \
C_"v207, v182, v183\n" \
M_"v[84:87], a[136:139], a[176:179], v[84:87]\n" \
R_"a[220:223], v29 offset:14336\n" \
"v_accvgpr_read_b32 v208, a36\n" \
M_"v[84:87], a[200:203], a[240:243], v[84:87]\n" \
"s_addk_i32 s84, 0x7c00\n" \
M_"v[88:91], a[136:139], a[180:183], v[88:91]\n" \
B_"v27, s[68:71], s84 offen offset:2048 lds\n" \
M_"v[88:91], a[200:203], a[244:247], v[88:91]\n" \
X_"a[230:231], v33 offset:4608\n" \
"v_accvgpr_read_b32 v209, a37\n" \
M_"v[92:95], a[136:139], a[184:187], v[92:95]\n" \
"s_addk_i32 s84, 0x7c00\n" \
M_"v[92:95], a[200:203], a[248:251], v[92:95]\n" \
B_"v27, s[68:71], s84 offen offset:3072 lds\n" \
M_"v[96:99], a[136:139], a[188:191], v[96:99]\n" \
X_"a[172:173], v35 offset:0\n" \
"v_accvgpr_read_b32 v210, a38\n" \
M_"v[96:99], a[200:203], a[252:255], v[96:99]\n" \
R_"a[136:139], v28 offset:4096\n" \
"v_accvgpr_read_b32 v211, a39\n" \
M_"v[112:115], a[140:143], a[188:191], v[112:115]\n" \
R_"a[200:203], v29 offset:4096\n" \
"v_accvgpr_read_b32 v180, a52\n" \
M_"v[112:115], a[204:207], a[252:255], v[112:115]\n" \
"s_add_u32 m0, s30, 0x9000\n" \
"s_mov_b32 s84, 0x9080\n" \
M_"v[108:111], a[140:143], a[184:187], v[108:111]\n" \
B_"v24, s[76:79], s84 offen offset:0 lds\n" \
M_"v[108:111], a[204:207], a[248:251], v[108:111]\n" \
X_"a[174:175], v35 offset:512\n" \
"v_accvgpr_read_b32 v181, a53\n" \
M_"v[104:107], a[140:143], a[180:183], v[104:107]\n" \
"s_addk_i32 s84, 0x2000\n" \
"s_nop 0\n" \
M_"v[104:107], a[204:207], a[244:247], v[104:107]\n" \
B_"v25, s[76:79], s84 offen offset:1024 lds\n" \
M_"v[100:103], a[140:143], a[176:179], v[100:103]\n" \
X_"a[236:237], v35 offset:4096\n" \
"v_accvgpr_read_b32 v182, a54\n" \
M_"v[100:103], a[204:207], a[240:243], v[100:103]\n" \
R_"a[140:143], v28 offset:6144\n" \
"v_accvgpr_read_b32 v183, a55\n" \
M_"v[52:55], a[128:131], a[176:179], v[52:55]\n" \
R_"a[204:207], v29 offset:6144\n" \
C_"v208, v208, v209\n" \
M_"v[52:55], a[192:195], a[240:243], v[52:55]\n" \
X_"a[238:239], v35 offset:4608\n" \
C_"v209, v210, v211\n" \
M_"v[56:59], a[128:131], a[180:183], v[56:59]\n" \
X_"a[168:169], v34 offset:0\n" \
C_"v210, v180, v181\n" \
M_"v[56:59], a[192:195], a[244:247], v[56:59]\n" \
X_"a[170:171], v34 offset:512\n" \
C_"v211, v182, v183\n" \
M_"v[60:63], a[128:131], a[184:187], v[60:63]\n" \
X_"a[232:233], v34 offset:4096\n" \
"v_accvgpr_read_b32 v212, a32\n" \
M_"v[60:63], a[192:195], a[248:251], v[60:63]\n" \
X_"a[234:235], v34 offset:4608\n" \
"v_accvgpr_read_b32 v213, a33\n" \
M_"v[64:67], a[128:131], a[188:191], v[64:67]\n" \
"v_accvgpr_read_b32 v214, a34\n" \
"v_accvgpr_read_b32 v215, a35\n" \
M_"v[64:67], a[192:195], a[252:255], v[64:67]\n" \
R_"a[128:131], v28 offset:0\n" \
"v_accvgpr_read_b32 v180, a48\n" \
M_"v[80:83], a[132:135], a[188:191], v[80:83]\n" \
R_"a[192:195], v29 offset:0\n" \
"v_accvgpr_read_b32 v181, a49\n" \
M_"v[80:83], a[196:199], a[252:255], v[80:83]\n" \
"v_accvgpr_read_b32 v182, a50\n" \
"v_accvgpr_read_b32 v183, a51\n" \
M_"v[76:79], a[132:135], a[184:187], v[76:79]\n" \
C_"v212, v212, v213\n" \
C_"v213, v214, v215\n" \
M_"v[76:79], a[196:199], a[248:251], v[76:79]\n" \
C_"v214, v180, v181\n" \
C_"v215, v182, v183\n" \
M_"v[72:75], a[132:135], a[180:183], v[72:75]\n" \
"v_accvgpr_read_b32 v216, a68\n" \
"v_accvgpr_read_b32 v217, a69\n" \
M_"v[72:75], a[196:199], a[244:247], v[72:75]\n" \
"v_accvgpr_read_b32 v218, a70\n" \
"v_accvgpr_read_b32 v219, a71\n" \
M_"v[68:71], a[132:135], a[176:179], v[68:71]\n" \
"v_accvgpr_read_b32 v180, a84\n" \
"v_accvgpr_read_b32 v181, a85\n" \
M_"v[68:71], a[196:199], a[240:243], v[68:71]\n" \
R_"a[132:135], v28 offset:2048\n" \
R_"a[196:199], v29 offset:2048\n" \
"s_waitcnt lgkmcnt(15)\n" \
M_"a[0:3], a[160:163], a[144:147], 0\n" \
"s_addk_i32 s84, 0x2000\n" \
"v_accvgpr_read_b32 v182, a86\n" \
M_"a[0:3], a[224:227], a[208:211], a[0:3]\n" \
B_"v24, s[76:79], s84 offen offset:2048 lds\n" \
"v_accvgpr_read_b32 v183, a87\n" \
M_"a[4:7], a[164:167], a[144:147], 0\n" \
X_"a[176:177], v36 offset:0\n" \
C_"v216, v216, v217\n" \
M_"a[4:7], a[228:231], a[208:211], a[4:7]\n" \
"s_addk_i32 s84, 0x2000\n" \
C_"v217, v218, v219\n" \
"s_waitcnt lgkmcnt(13)\n" \
M_"a[12:15], a[172:175], a[144:147], 0\n" \
B_"v25, s[76:79], s84 offen offset:3072 lds\n" \
C_"v218, v180, v181\n" \
"s_waitcnt lgkmcnt(9)\n" \
M_"a[12:15], a[236:239], a[208:211], a[12:15]\n" \
X_"a[178:179], v36 offset:1024\n" \
C_"v219, v182, v183\n" \
"s_waitcnt lgkmcnt(8)\n" \
M_"a[8:11], a[144:147], a[168:171], 0\n" \
X_"a[240:241], v36 offset:8192\n" \
"v_accvgpr_read_b32 v220, a64\n" \
"s_waitcnt lgkmcnt(7)\n" \
"s_nop 0\n" \
M_"a[8:11], a[208:211], a[232:235], a[8:11]\n" \
X_"a[242:243], v36 offset:9216\n" \
"v_accvgpr_read_b32 v221, a65\n" \
M_"a[24:27], a[148:151], a[168:171], 0\n" \
X_"a[180:181], v37 offset:0\n" \
"v_accvgpr_read_b32 v222, a66\n" \
M_"a[24:27], a[212:215], a[232:235], a[24:27]\n" \
X_"a[182:183], v37 offset:1024\n" \
"v_accvgpr_read_b32 v223, a67\n" \
M_"a[28:31], a[172:175], a[148:151], 0\n" \
X_"a[244:245], v37 offset:8192\n" \
"v_accvgpr_read_b32 v180, a80\n" \
M_"a[28:31], a[236:239], a[212:215], a[28:31]\n" \
"s_add_u32 s89, s41, 0x2000\n" \
"buffer_store_dwordx4 v[200:203], v20, s[80:83], s89 offen offset:3072 sc1\n" \
M_"a[20:23], a[164:167], a[148:151], 0\n" \
X_"a[246:247], v37 offset:9216\n" \
"v_accvgpr_read_b32 v181, a81\n" \
M_"a[20:23], a[228:231], a[212:215], a[20:23]\n" \
X_"a[184:185], v38 offset:0\n" \
"v_accvgpr_read_b32 v182, a82\n" \
M_"a[16:19], a[160:163], a[148:151], 0\n" \
X_"a[186:187], v38 offset:1024\n" \
".fill (L_pb + 61152 - .) / 4, 4, 0xBF800000\n" \
"L_pp15:\n" \
"s_cbranch_execz L_pret\n" \
"s_nop 0\n" \
"v_accvgpr_read_b32 v183, a83\n" \
M_"a[16:19], a[224:227], a[212:215], a[16:19]\n" \
C_"v220, v220, v221\n" \
C_"v221, v222, v223\n" \
C_"v222, v180, v181\n" \
M_"a[32:35], a[160:163], a[152:155], 0\n" \
"s_add_u32 s88, s41, 0x2000\n" \
"buffer_store_dwordx4 v[204:207], v20, s[80:83], s88 offen offset:2048 sc1\n" \
M_"a[32:35], a[224:227], a[216:219], a[32:35]\n" \
X_"a[248:249], v38 offset:8192\n" \
C_"v223, v182, v183\n" \
M_"a[36:39], a[164:167], a[152:155], 0\n" \
X_"a[250:251], v38 offset:9216\n" \
C_"v224, v128, v129\n" \
M_"a[36:39], a[228:231], a[216:219], a[36:39]\n" \
X_"a[188:189], v39 offset:0\n" \
C_"v225, v130, v131\n" \
M_"a[44:47], a[172:175], a[152:155], 0\n" \
C_"v226, v144, v145\n" \
C_"v227, v146, v147\n" \
C_"v228, v124, v125\n" \
M_"a[44:47], a[236:239], a[216:219], a[44:47]\n" \
X_"a[190:191], v39 offset:1024\n" \
C_"v229, v126, v127\n" \
M_"a[40:43], a[152:155], a[168:171], 0\n" \
"s_add_u32 s89, s41, 0x2000\n" \
"buffer_store_dwordx4 v[208:211], v20, s[80:83], s89 offen offset:1024 sc1\n" \
M_"a[40:43], a[216:219], a[232:235], a[40:43]\n" \
X_"a[252:253], v39 offset:8192\n" \
C_"v230, v140, v141\n" \
M_"a[56:59], a[156:159], a[168:171], 0\n" \
X_"a[254:255], v39 offset:9216\n" \
C_"v231, v142, v143\n" \
M_"a[56:59], a[220:223], a[232:235], a[56:59]\n" \
C_"v232, v120, v121\n" \
C_"v233, v122, v123\n" \
C_"v234, v136, v137\n" \
M_"a[60:63], a[172:175], a[156:159], 0\n" \
C_"v235, v138, v139\n" \
C_"v236, v116, v117\n" \
C_"v237, v118, v119\n" \
M_"a[60:63], a[236:239], a[220:223], a[60:63]\n" \
"s_add_u32 s88, s41, 0x2000\n" \
"buffer_store_dwordx4 v[212:215], v20, s[80:83], s88 offen offset:0 sc1\n" \
M_"a[52:55], a[164:167], a[156:159], 0\n" \
C_"v238, v132, v133\n" \
C_"v239, v134, v135\n" \
C_"v240, v160, v161\n" \
M_"a[52:55], a[228:231], a[220:223], a[52:55]\n" \
C_"v241, v162, v163\n" \
C_"v242, v176, v177\n" \
C_"v243, v178, v179\n" \
M_"a[48:51], a[160:163], a[156:159], 0\n" \
C_"v244, v156, v157\n" \
C_"v245, v158, v159\n" \
C_"v246, v172, v173\n" \
M_"a[48:51], a[224:227], a[220:223], a[48:51]\n" \
C_"v247, v174, v175\n" \
C_"v184, v152, v153\n" \
C_"v185, v154, v155\n" \
M_"a[64:67], a[164:167], a[140:143], 0\n" \
C_"v186, v168, v169\n" \
C_"v187, v170, v171\n" \
C_"v188, v148, v149\n" \
M_"a[64:67], a[228:231], a[204:207], a[64:67]\n" \
C_"v189, v150, v151\n" \
C_"v190, v164, v165\n" \
C_"v191, v166, v167\n" \
M_"a[80:83], a[172:175], a[140:143], 0\n" \
C_"v192, v96, v97\n" \
C_"v193, v98, v99\n" \
C_"v194, v112, v113\n" \
M_"a[80:83], a[236:239], a[204:207], a[80:83]\n" \
C_"v195, v114, v115\n" \
C_"v196, v92, v93\n" \
C_"v197, v94, v95\n" \
M_"a[68:71], a[140:143], a[168:171], 0\n" \
C_"v198, v108, v109\n" \
C_"v199, v110, v111\n" \
C_"v252, v88, v89\n" \
M_"a[68:71], a[204:207], a[232:235], a[68:71]\n" \
C_"v253, v90, v91\n" \
C_"v254, v104, v105\n" \
C_"v255, v106, v107\n" \
"s_waitcnt vmcnt(4) lgkmcnt(0)\n" \
"s_barrier\n" \
M_"v[116:119], a[176:179], a[144:147], 0\n" \
"s_add_u32 m0, s40, 0x0\n" \
"s_nop 0\n" \
"s_add_u32 s85, s39, 0x4000\n" \
"v_accvgpr_write_b32 a84, v252\n" \
M_"v[116:119], a[240:243], a[208:211], v[116:119]\n" \
B_"v20, s[80:83], s85 offen sc1 lds\n" \
"v_accvgpr_write_b32 a85, v253\n" \
M_"v[120:123], a[180:183], a[144:147], 0\n" \
X_"a[160:161], v32 offset:32768\n" \
"v_accvgpr_write_b32 a86, v254\n" \
M_"v[120:123], a[244:247], a[208:211], v[120:123]\n" \
X_"a[162:163], v32 offset:33280\n" \
"v_accvgpr_write_b32 a87, v255\n" \
M_"v[124:127], a[184:187], a[144:147], 0\n" \
"s_add_u32 m0, s40, 0x1000\n" \
"s_add_u32 s85, s39, 0x5000\n" \
C_"v252, v84, v85\n" \
M_"v[124:127], a[248:251], a[208:211], v[124:127]\n" \
B_"v20, s[80:83], s85 offen sc1 lds\n" \
C_"v253, v86, v87\n" \
M_"v[128:131], a[188:191], a[144:147], 0\n" \
X_"a[224:225], v32 offset:36864\n" \
C_"v254, v100, v101\n" \
M_"v[128:131], a[252:255], a[208:211], v[128:131]\n" \
R_"a[144:147], v28 offset:40960\n" \
C_"v255, v102, v103\n" \
M_"v[144:147], a[188:191], a[148:151], 0\n" \
R_"a[208:211], v29 offset:40960\n" \
C_"v200, v64, v65\n" \
M_"v[144:147], a[252:255], a[212:215], v[144:147]\n" \
"s_add_u32 m0, s40, 0x3000\n" \
"s_movk_i32 s84, 0x100\n" \
"s_nop 0\n" \
C_"v201, v66, v67\n" \
M_"v[140:143], a[184:187], a[148:151], 0\n" \
B_"v21, s[76:79], s84 offen offset:0 lds\n" \
C_"v202, v80, v81\n" \
M_"v[140:143], a[248:251], a[212:215], v[140:143]\n" \
X_"a[226:227], v32 offset:37376\n" \
C_"v203, v82, v83\n" \
M_"v[136:139], a[180:183], a[148:151], 0\n" \
C_"v204, v60, v61\n" \
C_"v205, v62, v63\n" \
C_"v206, v76, v77\n" \
M_"v[136:139], a[244:247], a[212:215], v[136:139]\n" \
X_"a[164:165], v33 offset:32768\n" \
C_"v207, v78, v79\n" \
M_"v[132:135], a[176:179], a[148:151], 0\n" \
C_"v208, v56, v57\n" \
C_"v209, v58, v59\n" \
C_"v210, v72, v73\n" \
M_"v[132:135], a[240:243], a[212:215], v[132:135]\n" \
R_"a[148:151], v28 offset:43008\n" \
C_"v211, v74, v75\n" \
M_"v[148:151], a[176:179], a[152:155], 0\n" \
R_"a[212:215], v29 offset:43008\n" \
C_"v212, v52, v53\n" \
M_"v[148:151], a[240:243], a[216:219], v[148:151]\n" \
"s_add_u32 m0, s31, 0x0\n" \
"s_mov_b32 s84, 0x100000\n" \
C_"v213, v54, v55\n" \
M_"v[152:155], a[180:183], a[152:155], 0\n" \
B_"v26, s[68:71], s84 offen offset:0 lds\n" \
C_"v214, v68, v69\n" \
M_"v[152:155], a[244:247], a[216:219], v[152:155]\n" \
"s_addk_i32 s84, 0x7c00\n" \
C_"v215, v70, v71\n" \
M_"v[156:159], a[184:187], a[152:155], 0\n" \
B_"v26, s[68:71], s84 offen offset:1024 lds\n" \
M_"v[156:159], a[248:251], a[216:219], v[156:159]\n" \
X_"a[166:167], v33 offset:33280\n" \
M_"v[160:163], a[188:191], a[152:155], 0\n" \
X_"a[228:229], v33 offset:36864\n" \
M_"v[160:163], a[252:255], a[216:219], v[160:163]\n" \
R_"a[152:155], v28 offset:45056\n" \
M_"v[176:179], a[188:191], a[156:159], 0\n" \
R_"a[216:219], v29 offset:45056\n" \
M_"v[176:179], a[252:255], a[220:223], v[176:179]\n" \
"s_addk_i32 s84, 0x7c00\n" \
M_"v[172:175], a[184:187], a[156:159], 0\n" \
B_"v27, s[68:71], s84 offen offset:2048 lds\n" \
M_"v[172:175], a[248:251], a[220:223], v[172:175]\n" \
X_"a[230:231], v33 offset:37376\n" \
M_"v[168:171], a[180:183], a[156:159], 0\n" \
"s_addk_i32 s84, 0x7c00\n" \
M_"v[168:171], a[244:247], a[220:223], v[168:171]\n" \
B_"v27, s[68:71], s84 offen offset:3072 lds\n" \
M_"v[164:167], a[176:179], a[156:159], 0\n" \
X_"a[172:173], v35 offset:32768\n" \
M_"v[164:167], a[240:243], a[220:223], v[164:167]\n" \
R_"a[156:159], v28 offset:47104\n" \
M_"v[84:87], a[176:179], a[136:139], 0\n" \
R_"a[220:223], v29 offset:47104\n" \
M_"v[84:87], a[240:243], a[200:203], v[84:87]\n" \
"s_add_u32 s89, s41, 0x3000\n" \
"buffer_store_dwordx4 v[192:195], v20, s[80:83], s89 offen offset:3072 sc1\n" \
M_"v[88:91], a[180:183], a[136:139], 0\n" \
X_"a[174:175], v35 offset:33280\n" \
M_"v[88:91], a[244:247], a[200:203], v[88:91]\n" \
X_"a[236:237], v35 offset:36864\n" \
M_"v[92:95], a[184:187], a[136:139], 0\n" \
X_"a[238:239], v35 offset:37376\n" \
M_"v[92:95], a[248:251], a[200:203], v[92:95]\n" \
"s_add_u32 m0, s30, 0x0\n" \
"s_movk_i32 s84, 0x180\n" \
M_"v[96:99], a[188:191], a[136:139], 0\n" \
B_"v24, s[76:79], s84 offen offset:0 lds\n" \
M_"v[96:99], a[252:255], a[200:203], v[96:99]\n" \
R_"a[136:139], v28 offset:36864\n" \
M_"v[112:115], a[188:191], a[140:143], 0\n" \
R_"a[200:203], v29 offset:36864\n" \
M_"v[112:115], a[252:255], a[204:207], v[112:115]\n" \
X_"a[168:169], v34 offset:32768\n" \
M_"v[108:111], a[184:187], a[140:143], 0\n" \
"s_movk_i32 s84, 0x2180\n" \
"s_nop 0\n" \
M_"v[108:111], a[248:251], a[204:207], v[108:111]\n" \
B_"v25, s[76:79], s84 offen offset:1024 lds\n" \
M_"v[104:107], a[180:183], a[140:143], 0\n" \
X_"a[170:171], v34 offset:33280\n" \
M_"v[104:107], a[244:247], a[204:207], v[104:107]\n" \
X_"a[232:233], v34 offset:36864\n" \
M_"v[100:103], a[176:179], a[140:143], 0\n" \
M_"v[100:103], a[240:243], a[204:207], v[100:103]\n" \
R_"a[140:143], v28 offset:38912\n" \
M_"v[52:55], a[176:179], a[128:131], 0\n" \
R_"a[204:207], v29 offset:38912\n" \
M_"v[52:55], a[240:243], a[192:195], v[52:55]\n" \
"s_movk_i32 s84, 0x4180\n" \
M_"v[56:59], a[180:183], a[128:131], 0\n" \
B_"v24, s[76:79], s84 offen offset:2048 lds\n" \
M_"v[56:59], a[244:247], a[192:195], v[56:59]\n" \
"s_movk_i32 s84, 0x6180\n" \
M_"v[60:63], a[184:187], a[128:131], 0\n" \
B_"v25, s[76:79], s84 offen offset:3072 lds\n" \
M_"v[60:63], a[248:251], a[192:195], v[60:63]\n" \
X_"a[234:235], v34 offset:37376\n" \
M_"v[64:67], a[188:191], a[128:131], 0\n" \
M_"v[64:67], a[252:255], a[192:195], v[64:67]\n" \
R_"a[128:131], v28 offset:32768\n" \
M_"v[80:83], a[188:191], a[132:135], 0\n" \
R_"a[192:195], v29 offset:32768\n" \
M_"v[80:83], a[252:255], a[196:199], v[80:83]\n" \
M_"v[76:79], a[184:187], a[132:135], 0\n" \
M_"v[76:79], a[248:251], a[196:199], v[76:79]\n" \
M_"v[72:75], a[180:183], a[132:135], 0\n" \
M_"v[72:75], a[244:247], a[196:199], v[72:75]\n" \
M_"v[68:71], a[176:179], a[132:135], 0\n" \
M_"v[68:71], a[240:243], a[196:199], v[68:71]\n" \
R_"a[132:135], v28 offset:34816\n" \
R_"a[196:199], v29 offset:34816\n" \
"s_waitcnt lgkmcnt(15)\n" \
M_"a[0:3], a[160:163], a[144:147], a[0:3]\n" \
"s_add_u32 m0, s30, 0x1000\n" \
"s_addk_i32 s84, 0x3000\n" \
M_"a[0:3], a[224:227], a[208:211], a[0:3]\n" \
B_"v24, s[76:79], s84 offen offset:0 lds\n" \
M_"a[4:7], a[164:167], a[144:147], a[4:7]\n" \
X_"a[176:177], v36 offset:32768\n" \
"v_mov_b32_e64 v11, 0\n" \
M_"a[4:7], a[228:231], a[208:211], a[4:7]\n" \
X_"a[178:179], v36 offset:33792\n" \
"s_and_b32 s5, s2, 7\n" \
"s_waitcnt lgkmcnt(15)\n" \
M_"a[12:15], a[172:175], a[144:147], a[12:15]\n" \
X_"a[240:241], v36 offset:40960\n" \
"s_lshl_b32 s6, s3, 1\n" \
"s_waitcnt lgkmcnt(15)\n" \
M_"a[12:15], a[236:239], a[208:211], a[12:15]\n" \
"s_add_u32 s88, s41, 0x3000\n" \
"buffer_store_dwordx4 v[196:199], v20, s[80:83], s88 offen offset:2048 sc1\n" \
"s_waitcnt lgkmcnt(11)\n" \
M_"a[8:11], a[144:147], a[168:171], a[8:11]\n" \
X_"a[242:243], v36 offset:41984\n" \
"s_lshl_b32 s4, s5, 2\n" \
"s_waitcnt lgkmcnt(8)\n" \
M_"a[8:11], a[208:211], a[232:235], a[8:11]\n" \
X_"a[180:181], v37 offset:32768\n" \
"s_bfe_u32 s7, s2, 0x20003\n" \
M_"a[24:27], a[148:151], a[168:171], a[24:27]\n" \
X_"a[182:183], v37 offset:33792\n" \
"s_or_b32 s12, s4, s7\n" \
M_"a[24:27], a[212:215], a[232:235], a[24:27]\n" \
X_"a[244:245], v37 offset:40960\n" \
"s_and_b32 s6, s6, 4\n" \
M_"a[28:31], a[172:175], a[148:151], a[28:31]\n" \
"s_and_b32 s8, s3, 1\n" \
"s_or_b32 s8, s6, s8\n" \
M_"a[28:31], a[236:239], a[212:215], a[28:31]\n" \
"s_addk_i32 s84, 0x2000\n" \
M_"a[20:23], a[164:167], a[148:151], a[20:23]\n" \
B_"v25, s[76:79], s84 offen offset:1024 lds\n" \
M_"a[20:23], a[228:231], a[212:215], a[20:23]\n" \
X_"a[246:247], v37 offset:41984\n" \
"s_lshl_b32 s6, s12, 4\n" \
M_"a[16:19], a[160:163], a[148:151], a[16:19]\n" \
X_"a[184:185], v38 offset:32768\n" \
"s_lshl_b32 s9, s3, 2\n" \
M_"a[16:19], a[224:227], a[212:215], a[16:19]\n" \
X_"a[186:187], v38 offset:33792\n" \
"s_or_b32 s6, s6, s9\n" \
M_"a[32:35], a[160:163], a[152:155], a[32:35]\n" \
"s_addk_i32 s84, 0x2000\n" \
M_"a[32:35], a[224:227], a[216:219], a[32:35]\n" \
B_"v24, s[76:79], s84 offen offset:2048 lds\n" \
M_"a[36:39], a[164:167], a[152:155], a[36:39]\n" \
X_"a[248:249], v38 offset:40960\n" \
"s_lshr_b32 s9, s2, 5\n" \
M_"a[36:39], a[228:231], a[216:219], a[36:39]\n" \
X_"a[250:251], v38 offset:41984\n" \
"s_mul_i32 s10, s9, 0xa00\n" \
M_"a[44:47], a[172:175], a[152:155], a[44:47]\n" \
X_"a[188:189], v39 offset:32768\n" \
"s_add_i32 s10, s10, 0x5bc1000\n" \
M_"a[44:47], a[236:239], a[216:219], a[44:47]\n" \
X_"a[190:191], v39 offset:33792\n" \
"s_or_b32 s6, s6, s10\n" \
M_"a[40:43], a[152:155], a[168:171], a[40:43]\n" \
"s_add_u32 s89, s41, 0x3000\n" \
"buffer_store_dwordx4 a[84:87], v20, s[80:83], s89 offen offset:1024 sc1\n" \
M_"a[40:43], a[216:219], a[232:235], a[40:43]\n" \
X_"a[252:253], v39 offset:40960\n" \
"s_lshl_b32 s13, s9, 3\n" \
M_"a[56:59], a[156:159], a[168:171], a[56:59]\n" \
X_"a[254:255], v39 offset:41984\n" \
"v_writelane_b32 v11, s6, 0\n" \
M_"a[56:59], a[220:223], a[232:235], a[56:59]\n" \
"s_or_b32 s6, s13, s5\n" \
"v_writelane_b32 v11, s10, 1\n" \
M_"a[60:63], a[172:175], a[156:159], a[60:63]\n" \
"s_lshl_b32 s6, s6, 10\n" \
"s_lshl_b32 s10, s3, 8\n" \
M_"a[60:63], a[236:239], a[220:223], a[60:63]\n" \
"s_addk_i32 s84, 0x2000\n" \
M_"a[52:55], a[164:167], a[156:159], a[52:55]\n" \
B_"v25, s[76:79], s84 offen offset:3072 lds\n" \
M_"a[52:55], a[228:231], a[220:223], a[52:55]\n" \
"s_or_b32 s6, s6, s10\n" \
"s_or_b32 s10, s6, 0x5c00200\n" \
M_"a[48:51], a[160:163], a[156:159], a[48:51]\n" \
"v_writelane_b32 v11, s10, 2\n" \
"s_lshl_b32 s10, s7, 2\n" \
M_"a[48:51], a[224:227], a[220:223], a[48:51]\n" \
"s_or_b32 s11, s10, s6\n" \
"s_or_b32 s11, s11, 0x5c00000\n" \
M_"a[64:67], a[164:167], a[140:143], a[64:67]\n" \
"v_writelane_b32 v11, s11, 3\n" \
"s_or_b32 s6, s6, 0x5c00080\n" \
M_"a[64:67], a[228:231], a[204:207], a[64:67]\n" \
"v_writelane_b32 v11, s6, 4\n" \
"s_or_b32 s6, s6, s10\n" \
M_"a[80:83], a[172:175], a[140:143], a[80:83]\n" \
"s_add_i32 s10, s3, s2\n" \
"v_writelane_b32 v11, s6, 5\n" \
M_"a[80:83], a[236:239], a[204:207], a[80:83]\n" \
"s_mov_b32 s6, 2.0\n" \
"s_mov_b32 s11, 0x5bc0000\n" \
M_"a[68:71], a[140:143], a[168:171], a[68:71]\n" \
"s_cmp_lg_u32 s10, 0\n" \
"s_cselect_b32 s10, s6, s11\n" \
"s_waitcnt vmcnt(11) lgkmcnt(0)\n" \
M_"a[68:71], a[204:207], a[232:235], a[68:71]\n" \
"s_barrier\n" \
"v_xor_b32_e32 v28, 0x20000, v28\n" \
"v_xor_b32_e32 v29, 0x20000, v29\n" \
M_"v[116:119], a[176:179], a[144:147], v[116:119]\n" \
"s_add_u32 m0, s40, 0x8000\n" \
"s_add_u32 s85, s39, 0x6000\n" \
M_"v[116:119], a[240:243], a[208:211], v[116:119]\n" \
B_"v20, s[80:83], s85 offen sc1 lds\n" \
M_"v[120:123], a[180:183], a[144:147], v[120:123]\n" \
X_"a[160:161], v32 offset:0\n" \
"s_lshr_b32 s14, s2, 3\n" \
M_"v[120:123], a[244:247], a[208:211], v[120:123]\n" \
X_"a[162:163], v32 offset:512\n" \
"v_writelane_b32 v11, s10, 57\n" \
M_"v[124:127], a[184:187], a[144:147], v[124:127]\n" \
"s_add_u32 m0, s40, 0x9000\n" \
"s_add_u32 s85, s39, 0x7000\n" \
M_"v[124:127], a[248:251], a[208:211], v[124:127]\n" \
B_"v20, s[80:83], s85 offen sc1 lds\n" \
M_"v[128:131], a[188:191], a[144:147], v[128:131]\n" \
X_"a[224:225], v32 offset:4096\n" \
"s_lshl_b32 s10, s5, 5\n" \
M_"v[128:131], a[252:255], a[208:211], v[128:131]\n" \
R_"a[144:147], v28 offset:8192\n" \
"s_min_u32 s15, s3, 2\n" \
M_"v[144:147], a[188:191], a[148:151], v[144:147]\n" \
R_"a[208:211], v29 offset:8192\n" \
"s_mul_i32 s11, s7, 0x120\n" \
M_"v[144:147], a[252:255], a[212:215], v[144:147]\n" \
"s_add_u32 m0, s40, 0xb000\n" \
"s_movk_i32 s84, 0x180\n" \
M_"v[140:143], a[184:187], a[148:151], v[140:143]\n" \
B_"v21, s[76:79], s84 offen offset:0 lds\n" \
M_"v[140:143], a[248:251], a[212:215], v[140:143]\n" \
X_"a[226:227], v32 offset:4608\n" \
M_"v[136:139], a[180:183], a[148:151], v[136:139]\n" \
"s_add_u32 s88, s41, 0x3000\n" \
"buffer_store_dwordx4 v[252:255], v20, s[80:83], s88 offen offset:0 sc1\n" \
M_"v[136:139], a[244:247], a[212:215], v[136:139]\n" \
X_"a[164:165], v33 offset:0\n" \
M_"v[132:135], a[176:179], a[148:151], v[132:135]\n" \
"s_add_u32 s16, s22, s11\n" \
"s_addc_u32 s17, s23, 0\n" \
"s_nop 0\n" \
M_"v[132:135], a[240:243], a[212:215], v[132:135]\n" \
R_"a[148:151], v28 offset:10240\n" \
M_"v[148:151], a[176:179], a[152:155], v[148:151]\n" \
R_"a[212:215], v29 offset:10240\n" \
M_"v[148:151], a[240:243], a[216:219], v[148:151]\n" \
"s_add_u32 m0, s31, 0x8000\n" \
"s_mov_b32 s84, 0x180000\n" \
M_"v[152:155], a[180:183], a[152:155], v[152:155]\n" \
B_"v26, s[68:71], s84 offen offset:0 lds\n" \
M_"v[152:155], a[244:247], a[216:219], v[152:155]\n" \
X_"a[166:167], v33 offset:512\n" \
M_"v[156:159], a[184:187], a[152:155], v[156:159]\n" \
X_"a[228:229], v33 offset:4096\n" \
M_"v[156:159], a[248:251], a[216:219], v[156:159]\n" \
"s_addk_i32 s84, 0x7c00\n" \
M_"v[160:163], a[188:191], a[152:155], v[160:163]\n" \
B_"v26, s[68:71], s84 offen offset:1024 lds\n" \
M_"v[160:163], a[252:255], a[216:219], v[160:163]\n" \
R_"a[152:155], v28 offset:12288\n" \
M_"v[176:179], a[188:191], a[156:159], v[176:179]\n" \
R_"a[216:219], v29 offset:12288\n" \
M_"v[176:179], a[252:255], a[220:223], v[176:179]\n" \
"s_addk_i32 s84, 0x7c00\n" \
M_"v[172:175], a[184:187], a[156:159], v[172:175]\n" \
B_"v27, s[68:71], s84 offen offset:2048 lds\n" \
M_"v[172:175], a[248:251], a[220:223], v[172:175]\n" \
X_"a[230:231], v33 offset:4608\n" \
M_"v[168:171], a[180:183], a[156:159], v[168:171]\n" \
X_"a[172:173], v35 offset:0\n" \
M_"v[168:171], a[244:247], a[220:223], v[168:171]\n" \
X_"a[174:175], v35 offset:512\n" \
M_"v[164:167], a[176:179], a[156:159], v[164:167]\n" \
"s_add_u32 s18, s24, s11\n" \
"v_writelane_b32 v11, s16, 9\n" \
"s_addc_u32 s19, s25, 0\n" \
M_"v[164:167], a[240:243], a[220:223], v[164:167]\n" \
R_"a[156:159], v28 offset:14336\n" \
"s_and_b32 s20, s17, 0xffff\n" \
M_"v[84:87], a[176:179], a[136:139], v[84:87]\n" \
R_"a[220:223], v29 offset:14336\n" \
"v_writelane_b32 v11, s20, 10\n" \
M_"v[84:87], a[240:243], a[200:203], v[84:87]\n" \
"s_addk_i32 s84, 0x7c00\n" \
M_"v[88:91], a[180:183], a[136:139], v[88:91]\n" \
B_"v27, s[68:71], s84 offen offset:3072 lds\n" \
M_"v[88:91], a[244:247], a[200:203], v[88:91]\n" \
"s_add_u32 m0, s30, 0x8000\n" \
"s_movk_i32 s84, 0x200\n" \
M_"v[92:95], a[184:187], a[136:139], v[92:95]\n" \
B_"v24, s[76:79], s84 offen offset:0 lds\n" \
M_"v[92:95], a[248:251], a[200:203], v[92:95]\n" \
X_"a[236:237], v35 offset:4096\n" \
"s_and_b32 s20, s19, 0xffff\n" \
M_"v[96:99], a[188:191], a[136:139], v[96:99]\n" \
X_"a[238:239], v35 offset:4608\n" \
"v_writelane_b32 v11, s18, 11\n" \
M_"v[96:99], a[252:255], a[200:203], v[96:99]\n" \
R_"a[136:139], v28 offset:4096\n" \
M_"v[112:115], a[188:191], a[140:143], v[112:115]\n" \
R_"a[200:203], v29 offset:4096\n" \
M_"v[112:115], a[252:255], a[204:207], v[112:115]\n" \
"s_movk_i32 s84, 0x2200\n" \
"s_nop 0\n" \
M_"v[108:111], a[184:187], a[140:143], v[108:111]\n" \
B_"v25, s[76:79], s84 offen offset:1024 lds\n" \
M_"v[108:111], a[248:251], a[204:207], v[108:111]\n" \
"s_add_u32 s89, s41, 0x1000\n" \
"buffer_store_dwordx4 v[200:203], v20, s[80:83], s89 offen offset:3072 sc1\n" \
M_"v[104:107], a[180:183], a[140:143], v[104:107]\n" \
X_"a[168:169], v34 offset:0\n" \
M_"v[104:107], a[244:247], a[204:207], v[104:107]\n" \
X_"a[170:171], v34 offset:512\n" \
M_"v[100:103], a[176:179], a[140:143], v[100:103]\n" \
X_"a[232:233], v34 offset:4096\n" \
M_"v[100:103], a[240:243], a[204:207], v[100:103]\n" \
R_"a[140:143], v28 offset:6144\n" \
M_"v[52:55], a[176:179], a[128:131], v[52:55]\n" \
R_"a[204:207], v29 offset:6144\n" \
M_"v[52:55], a[240:243], a[192:195], v[52:55]\n" \
"s_movk_i32 s84, 0x4200\n" \
M_"v[56:59], a[180:183], a[128:131], v[56:59]\n" \
B_"v24, s[76:79], s84 offen offset:2048 lds\n" \
M_"v[56:59], a[244:247], a[192:195], v[56:59]\n" \
"s_movk_i32 s84, 0x6200\n" \
M_"v[60:63], a[184:187], a[128:131], v[60:63]\n" \
B_"v25, s[76:79], s84 offen offset:3072 lds\n" \
M_"v[60:63], a[248:251], a[192:195], v[60:63]\n" \
X_"a[234:235], v34 offset:4608\n" \
M_"v[64:67], a[188:191], a[128:131], v[64:67]\n" \
"s_cmpk_lt_u32 s3, 3\n" \
"s_cselect_b32 s16, s16, s18\n" \
M_"v[64:67], a[252:255], a[192:195], v[64:67]\n" \
R_"a[128:131], v28 offset:0\n" \
"s_movk_i32 s18, 0xfff\n" \
M_"v[80:83], a[188:191], a[132:135], v[80:83]\n" \
R_"a[192:195], v29 offset:0\n" \
"v_writelane_b32 v11, s20, 12\n" \
M_"v[80:83], a[252:255], a[196:199], v[80:83]\n" \
"s_and_b32 s20, s29, 0xffff\n" \
"v_writelane_b32 v11, s28, 13\n" \
M_"v[76:79], a[184:187], a[132:135], v[76:79]\n" \
"v_writelane_b32 v11, s20, 14\n" \
"v_writelane_b32 v11, s16, 15\n" \
M_"v[76:79], a[248:251], a[196:199], v[76:79]\n" \
"s_cmpk_lt_u32 s3, 3\n" \
"s_cselect_b32 s16, s17, s19\n" \
M_"v[72:75], a[180:183], a[132:135], v[72:75]\n" \
"s_and_b32 s16, s16, 0xffff\n" \
"v_writelane_b32 v11, s16, 16\n" \
M_"v[72:75], a[244:247], a[196:199], v[72:75]\n" \
"s_mul_i32 s16, s5, 0x48\n" \
"s_add_i32 s17, s16, 0x47\n" \
M_"v[68:71], a[176:179], a[132:135], v[68:71]\n" \
"s_cmpk_lt_u32 s3, 3\n" \
"s_cselect_b32 s17, s17, s18\n" \
M_"v[68:71], a[240:243], a[196:199], v[68:71]\n" \
R_"a[132:135], v28 offset:2048\n" \
R_"a[196:199], v29 offset:2048\n" \
"s_waitcnt lgkmcnt(15)\n" \
M_"a[0:3], a[160:163], a[144:147], a[0:3]\n" \
"s_add_u32 m0, s30, 0x9000\n" \
"s_addk_i32 s84, 0x3000\n" \
M_"a[0:3], a[224:227], a[208:211], a[0:3]\n" \
B_"v24, s[76:79], s84 offen offset:0 lds\n" \
M_"a[4:7], a[164:167], a[144:147], a[4:7]\n" \
X_"a[176:177], v36 offset:0\n" \
"s_mulk_i32 s17, 0x480\n" \
M_"a[4:7], a[228:231], a[208:211], a[4:7]\n" \
X_"a[178:179], v36 offset:1024\n" \
"s_addk_i32 s17, 0x120\n" \
M_"a[12:15], a[172:175], a[144:147], a[12:15]\n" \
X_"a[240:241], v36 offset:8192\n" \
"v_writelane_b32 v11, s17, 17\n" \
"s_waitcnt lgkmcnt(15)\n" \
M_"a[12:15], a[236:239], a[208:211], a[12:15]\n" \
"s_add_u32 s88, s41, 0x1000\n" \
"buffer_store_dwordx4 v[204:207], v20, s[80:83], s88 offen offset:2048 sc1\n" \
"s_waitcnt lgkmcnt(11)\n" \
M_"a[8:11], a[144:147], a[168:171], a[8:11]\n" \
X_"a[242:243], v36 offset:9216\n" \
"s_lshl_b32 s17, s3, 5\n" \
"s_waitcnt lgkmcnt(8)\n" \
M_"a[8:11], a[208:211], a[232:235], a[8:11]\n" \
X_"a[180:181], v37 offset:0\n" \
"s_add_i32 s16, s16, s17\n" \
M_"a[24:27], a[148:151], a[168:171], a[24:27]\n" \
X_"a[182:183], v37 offset:1024\n" \
"s_mulk_i32 s16, 0x480\n" \
M_"a[24:27], a[212:215], a[232:235], a[24:27]\n" \
X_"a[244:245], v37 offset:8192\n" \
"s_mul_i32 s17, s12, 0x24000\n" \
M_"a[28:31], a[172:175], a[148:151], a[28:31]\n" \
"s_add_i32 s18, s17, 0x1b000\n" \
M_"a[28:31], a[236:239], a[212:215], a[28:31]\n" \
"s_addk_i32 s84, 0x2000\n" \
M_"a[20:23], a[164:167], a[148:151], a[20:23]\n" \
B_"v25, s[76:79], s84 offen offset:1024 lds\n" \
M_"a[20:23], a[228:231], a[212:215], a[20:23]\n" \
X_"a[246:247], v37 offset:9216\n" \
M_"a[16:19], a[160:163], a[148:151], a[16:19]\n" \
X_"a[184:185], v38 offset:0\n" \
".fill (L_pb + 65248 - .) / 4, 4, 0xBF800000\n" \
"L_pp16:\n" \
"s_cbranch_execz L_pret\n" \
M_"a[16:19], a[224:227], a[212:215], a[16:19]\n" \
X_"a[186:187], v38 offset:1024\n" \
M_"a[32:35], a[160:163], a[152:155], a[32:35]\n" \
"s_addk_i32 s84, 0x2000\n" \
M_"a[32:35], a[224:227], a[216:219], a[32:35]\n" \
B_"v24, s[76:79], s84 offen offset:2048 lds\n" \
M_"a[36:39], a[164:167], a[152:155], a[36:39]\n" \
X_"a[248:249], v38 offset:8192\n" \
M_"a[36:39], a[228:231], a[216:219], a[36:39]\n" \
X_"a[250:251], v38 offset:9216\n" \
M_"a[44:47], a[172:175], a[152:155], a[44:47]\n" \
X_"a[188:189], v39 offset:0\n" \
M_"a[44:47], a[236:239], a[216:219], a[44:47]\n" \
X_"a[190:191], v39 offset:1024\n" \
M_"a[40:43], a[152:155], a[168:171], a[40:43]\n" \
"s_add_u32 s89, s41, 0x1000\n" \
"buffer_store_dwordx4 v[208:211], v20, s[80:83], s89 offen offset:1024 sc1\n" \
M_"a[40:43], a[216:219], a[232:235], a[40:43]\n" \
X_"a[252:253], v39 offset:8192\n" \
M_"a[56:59], a[156:159], a[168:171], a[56:59]\n" \
X_"a[254:255], v39 offset:9216\n" \
M_"a[56:59], a[220:223], a[232:235], a[56:59]\n" \
"s_cmpk_lt_u32 s3, 3\n" \
"s_cselect_b32 s16, s16, s18\n" \
M_"a[60:63], a[172:175], a[156:159], a[60:63]\n" \
"s_xor_b32 s18, s3, 2\n" \
"v_writelane_b32 v11, s16, 18\n" \
M_"a[60:63], a[236:239], a[220:223], a[60:63]\n" \
"s_addk_i32 s84, 0x2000\n" \
M_"a[52:55], a[164:167], a[156:159], a[52:55]\n" \
B_"v25, s[76:79], s84 offen offset:3072 lds\n" \
M_"a[52:55], a[228:231], a[220:223], a[52:55]\n" \
"s_mul_i32 s16, s15, 0x2400\n" \
"s_mul_i32 s15, s15, 0x9000\n" \
M_"a[48:51], a[160:163], a[156:159], a[48:51]\n" \
"s_bitset1_b32 s16, 17\n" \
"v_writelane_b32 v11, s16, 22\n" \
M_"a[48:51], a[224:227], a[220:223], a[48:51]\n" \
"s_add_i32 s17, s17, s15\n" \
"s_mul_i32 s15, s3, 0x2400\n" \
M_"a[64:67], a[164:167], a[140:143], a[64:67]\n" \
"v_writelane_b32 v11, s17, 29\n" \
"s_add_i32 s16, s15, 0xa400\n" \
M_"a[64:67], a[228:231], a[204:207], a[64:67]\n" \
"s_mov_b32 s17, 0x8000\n" \
M_"a[80:83], a[172:175], a[140:143], a[80:83]\n" \
"s_cmpk_lt_u32 s3, 3\n" \
"s_cselect_b32 s16, s16, s17\n" \
M_"a[80:83], a[236:239], a[204:207], a[80:83]\n" \
"s_mov_b32 s17, 0x1b000\n" \
M_"a[68:71], a[140:143], a[168:171], a[68:71]\n" \
"s_cmp_lg_u32 s18, 0\n" \
"s_cselect_b32 s17, s16, s17\n" \
"s_waitcnt vmcnt(11) lgkmcnt(0)\n" \
M_"a[68:71], a[204:207], a[232:235], a[68:71]\n" \
"s_barrier\n" \
"v_xor_b32_e32 v28, 0x20000, v28\n" \
"v_xor_b32_e32 v29, 0x20000, v29\n" \
M_"v[116:119], a[176:179], a[144:147], v[116:119]\n" \
"s_add_u32 m0, s40, 0x0\n" \
"s_nop 0\n" \
"s_add_u32 s85, s39, 0x8000\n" \
M_"v[116:119], a[240:243], a[208:211], v[116:119]\n" \
B_"v20, s[80:83], s85 offen sc1 lds\n" \
M_"v[120:123], a[180:183], a[144:147], v[120:123]\n" \
X_"a[160:161], v32 offset:32768\n" \
"v_writelane_b32 v11, s16, 23\n" \
M_"v[120:123], a[244:247], a[208:211], v[120:123]\n" \
X_"a[162:163], v32 offset:33280\n" \
"s_or_b32 s16, s15, 0x10000\n" \
M_"v[124:127], a[184:187], a[144:147], v[124:127]\n" \
"s_add_u32 m0, s40, 0x1000\n" \
"s_add_u32 s85, s39, 0x9000\n" \
M_"v[124:127], a[248:251], a[208:211], v[124:127]\n" \
B_"v20, s[80:83], s85 offen sc1 lds\n" \
M_"v[128:131], a[188:191], a[144:147], v[128:131]\n" \
X_"a[224:225], v32 offset:36864\n" \
"v_writelane_b32 v11, s17, 24\n" \
M_"v[128:131], a[252:255], a[208:211], v[128:131]\n" \
R_"a[144:147], v28 offset:8192\n" \
"s_add_i32 s17, s15, 0x19000\n" \
M_"v[144:147], a[188:191], a[148:151], v[144:147]\n" \
R_"a[208:211], v29 offset:8192\n" \
"v_writelane_b32 v11, s16, 25\n" \
M_"v[144:147], a[252:255], a[212:215], v[144:147]\n" \
"s_add_u32 m0, s40, 0x3000\n" \
"s_movk_i32 s84, 0x200\n" \
M_"v[140:143], a[184:187], a[148:151], v[140:143]\n" \
B_"v21, s[76:79], s84 offen offset:0 lds\n" \
M_"v[140:143], a[248:251], a[212:215], v[140:143]\n" \
X_"a[226:227], v32 offset:37376\n" \
"s_mov_b32 s16, 0\n" \
M_"v[136:139], a[180:183], a[148:151], v[136:139]\n" \
"s_add_u32 s88, s41, 0x1000\n" \
"buffer_store_dwordx4 v[212:215], v20, s[80:83], s88 offen offset:0 sc1\n" \
M_"v[136:139], a[244:247], a[212:215], v[136:139]\n" \
X_"a[164:165], v33 offset:32768\n" \
M_"v[132:135], a[176:179], a[148:151], v[132:135]\n" \
"s_cmpk_lt_u32 s3, 3\n" \
"s_cselect_b32 s17, s17, s16\n" \
M_"v[132:135], a[240:243], a[212:215], v[132:135]\n" \
R_"a[148:151], v28 offset:10240\n" \
"s_bitset1_b32 s15, 17\n" \
M_"v[148:151], a[176:179], a[152:155], v[148:151]\n" \
R_"a[212:215], v29 offset:10240\n" \
"v_writelane_b32 v11, s17, 26\n" \
M_"v[148:151], a[240:243], a[216:219], v[148:151]\n" \
"s_add_u32 m0, s31, 0x0\n" \
"s_mov_b32 s84, 0x200000\n" \
M_"v[152:155], a[180:183], a[152:155], v[152:155]\n" \
B_"v26, s[68:71], s84 offen offset:0 lds\n" \
M_"v[152:155], a[244:247], a[216:219], v[152:155]\n" \
X_"a[166:167], v33 offset:33280\n" \
"s_movk_i32 s17, 0x2400\n" \
M_"v[156:159], a[184:187], a[152:155], v[156:159]\n" \
X_"a[228:229], v33 offset:36864\n" \
M_"v[156:159], a[248:251], a[216:219], v[156:159]\n" \
"s_addk_i32 s84, 0x7c00\n" \
M_"v[160:163], a[188:191], a[152:155], v[160:163]\n" \
B_"v26, s[68:71], s84 offen offset:1024 lds\n" \
M_"v[160:163], a[252:255], a[216:219], v[160:163]\n" \
R_"a[152:155], v28 offset:12288\n" \
M_"v[176:179], a[188:191], a[156:159], v[176:179]\n" \
R_"a[216:219], v29 offset:12288\n" \
M_"v[176:179], a[252:255], a[220:223], v[176:179]\n" \
"s_addk_i32 s84, 0x7c00\n" \
M_"v[172:175], a[184:187], a[156:159], v[172:175]\n" \
B_"v27, s[68:71], s84 offen offset:2048 lds\n" \
M_"v[172:175], a[248:251], a[220:223], v[172:175]\n" \
X_"a[230:231], v33 offset:37376\n" \
M_"v[168:171], a[180:183], a[156:159], v[168:171]\n" \
X_"a[172:173], v35 offset:32768\n" \
M_"v[168:171], a[244:247], a[220:223], v[168:171]\n" \
X_"a[174:175], v35 offset:33280\n" \
M_"v[164:167], a[176:179], a[156:159], v[164:167]\n" \
"s_cmpk_lt_u32 s3, 3\n" \
"s_cselect_b32 s15, s15, s17\n" \
M_"v[164:167], a[240:243], a[220:223], v[164:167]\n" \
R_"a[156:159], v28 offset:14336\n" \
"v_writelane_b32 v11, s15, 27\n" \
M_"v[84:87], a[176:179], a[136:139], v[84:87]\n" \
R_"a[220:223], v29 offset:14336\n" \
"s_lshl_b32 s15, s3, 10\n" \
M_"v[84:87], a[240:243], a[200:203], v[84:87]\n" \
"s_addk_i32 s84, 0x7c00\n" \
M_"v[88:91], a[180:183], a[136:139], v[88:91]\n" \
B_"v27, s[68:71], s84 offen offset:3072 lds\n" \
M_"v[88:91], a[244:247], a[200:203], v[88:91]\n" \
"s_add_u32 m0, s30, 0x20000\n" \
"s_movk_i32 s84, 0x280\n" \
M_"v[92:95], a[184:187], a[136:139], v[92:95]\n" \
B_"v24, s[76:79], s84 offen offset:0 lds\n" \
M_"v[92:95], a[248:251], a[200:203], v[92:95]\n" \
X_"a[236:237], v35 offset:36864\n" \
"s_addk_i32 s15, 0x4800\n" \
M_"v[96:99], a[188:191], a[136:139], v[96:99]\n" \
X_"a[238:239], v35 offset:37376\n" \
"v_writelane_b32 v11, s15, 28\n" \
M_"v[96:99], a[252:255], a[200:203], v[96:99]\n" \
R_"a[136:139], v28 offset:4096\n" \
"s_add_i32 s15, s14, 1\n" \
M_"v[112:115], a[188:191], a[140:143], v[112:115]\n" \
R_"a[200:203], v29 offset:4096\n" \
"s_and_b32 s15, s15, 3\n" \
M_"v[112:115], a[252:255], a[204:207], v[112:115]\n" \
"s_movk_i32 s84, 0x2280\n" \
M_"v[108:111], a[184:187], a[140:143], v[108:111]\n" \
B_"v25, s[76:79], s84 offen offset:1024 lds\n" \
M_"v[108:111], a[248:251], a[204:207], v[108:111]\n" \
"s_add_u32 s89, s42, 0x0\n" \
"buffer_store_dwordx4 v[216:219], v20, s[80:83], s89 offen offset:1024 sc1\n" \
M_"v[104:107], a[180:183], a[140:143], v[104:107]\n" \
X_"a[168:169], v34 offset:32768\n" \
"s_or_b32 s15, s15, s4\n" \
M_"v[104:107], a[244:247], a[204:207], v[104:107]\n" \
X_"a[170:171], v34 offset:33280\n" \
"s_lshl_b32 s17, s15, 2\n" \
M_"v[100:103], a[176:179], a[140:143], v[100:103]\n" \
X_"a[232:233], v34 offset:36864\n" \
"s_or_b32 s17, s17, s3\n" \
M_"v[100:103], a[240:243], a[204:207], v[100:103]\n" \
R_"a[140:143], v28 offset:6144\n" \
"s_mul_i32 s17, s17, 0x9000\n" \
M_"v[52:55], a[176:179], a[128:131], v[52:55]\n" \
R_"a[204:207], v29 offset:6144\n" \
"v_writelane_b32 v11, s17, 30\n" \
M_"v[52:55], a[240:243], a[192:195], v[52:55]\n" \
"s_movk_i32 s84, 0x4280\n" \
M_"v[56:59], a[180:183], a[128:131], v[56:59]\n" \
B_"v24, s[76:79], s84 offen offset:2048 lds\n" \
M_"v[56:59], a[244:247], a[192:195], v[56:59]\n" \
"s_movk_i32 s84, 0x6280\n" \
M_"v[60:63], a[184:187], a[128:131], v[60:63]\n" \
B_"v25, s[76:79], s84 offen offset:3072 lds\n" \
M_"v[60:63], a[248:251], a[192:195], v[60:63]\n" \
X_"a[234:235], v34 offset:37376\n" \
"s_lshl_b32 s17, s12, 2\n" \
M_"v[64:67], a[188:191], a[128:131], v[64:67]\n" \
"s_or_b32 s17, s17, s3\n" \
"s_add_i32 s14, s14, -1\n" \
M_"v[64:67], a[252:255], a[192:195], v[64:67]\n" \
R_"a[128:131], v28 offset:0\n" \
"s_xor_b32 s17, s17, 8\n" \
M_"v[80:83], a[188:191], a[132:135], v[80:83]\n" \
R_"a[192:195], v29 offset:0\n" \
"s_and_b32 s14, s14, 3\n" \
M_"v[80:83], a[252:255], a[196:199], v[80:83]\n" \
"s_mul_i32 s17, s17, 0x9000\n" \
"s_or_b32 s14, s14, s4\n" \
M_"v[76:79], a[184:187], a[132:135], v[76:79]\n" \
"v_writelane_b32 v11, s17, 31\n" \
"s_lshl_b32 s17, s14, 2\n" \
M_"v[76:79], a[248:251], a[196:199], v[76:79]\n" \
"s_or_b32 s17, s17, s3\n" \
"s_mul_i32 s17, s17, 0x9000\n" \
M_"v[72:75], a[180:183], a[132:135], v[72:75]\n" \
"v_writelane_b32 v11, s17, 32\n" \
"s_or_b32 s17, s8, s13\n" \
M_"v[72:75], a[244:247], a[196:199], v[72:75]\n" \
"s_mul_i32 s17, s17, 0x24800\n" \
"s_lshl_b32 s15, s15, 12\n" \
M_"v[68:71], a[176:179], a[132:135], v[68:71]\n" \
"s_add_i32 s18, s17, 0x3004800\n" \
"s_lshl_b32 s12, s12, 12\n" \
M_"v[68:71], a[240:243], a[196:199], v[68:71]\n" \
R_"a[132:135], v28 offset:2048\n" \
R_"a[196:199], v29 offset:2048\n" \
"s_waitcnt lgkmcnt(15)\n" \
M_"a[0:3], a[160:163], a[144:147], a[0:3]\n" \
"s_add_u32 m0, s30, 0x21000\n" \
"s_addk_i32 s84, 0x3000\n" \
M_"a[0:3], a[224:227], a[208:211], a[0:3]\n" \
B_"v24, s[76:79], s84 offen offset:0 lds\n" \
M_"a[4:7], a[164:167], a[144:147], a[4:7]\n" \
X_"a[176:177], v36 offset:32768\n" \
"s_add_i32 s19, s18, s15\n" \
M_"a[4:7], a[228:231], a[208:211], a[4:7]\n" \
X_"a[178:179], v36 offset:33792\n" \
"v_writelane_b32 v11, s19, 33\n" \
M_"a[12:15], a[172:175], a[144:147], a[12:15]\n" \
X_"a[240:241], v36 offset:40960\n" \
"s_xor_b32 s12, s12, 0x2000\n" \
"s_waitcnt lgkmcnt(15)\n" \
M_"a[12:15], a[236:239], a[208:211], a[12:15]\n" \
"s_add_u32 s88, s42, 0x0\n" \
"buffer_store_dwordx4 v[220:223], v20, s[80:83], s88 offen offset:0 sc1\n" \
"s_waitcnt lgkmcnt(11)\n" \
M_"a[8:11], a[144:147], a[168:171], a[8:11]\n" \
X_"a[242:243], v36 offset:41984\n" \
"s_lshl_b32 s14, s14, 12\n" \
"s_waitcnt lgkmcnt(8)\n" \
M_"a[8:11], a[208:211], a[232:235], a[8:11]\n" \
X_"a[180:181], v37 offset:32768\n" \
"s_sub_i32 s13, s8, s13\n" \
M_"a[24:27], a[148:151], a[168:171], a[24:27]\n" \
X_"a[182:183], v37 offset:33792\n" \
"s_add_i32 s19, s18, s12\n" \
M_"a[24:27], a[212:215], a[232:235], a[24:27]\n" \
X_"a[244:245], v37 offset:40960\n" \
"v_writelane_b32 v11, s19, 34\n" \
M_"a[28:31], a[172:175], a[148:151], a[28:31]\n" \
"s_add_i32 s18, s18, s14\n" \
"s_mul_i32 s13, s13, 0x24800\n" \
M_"a[28:31], a[236:239], a[212:215], a[28:31]\n" \
"s_addk_i32 s84, 0x2000\n" \
M_"a[20:23], a[164:167], a[148:151], a[20:23]\n" \
B_"v25, s[76:79], s84 offen offset:1024 lds\n" \
M_"a[20:23], a[228:231], a[212:215], a[20:23]\n" \
X_"a[246:247], v37 offset:41984\n" \
"v_writelane_b32 v11, s18, 35\n" \
M_"a[16:19], a[160:163], a[148:151], a[16:19]\n" \
X_"a[184:185], v38 offset:32768\n" \
"s_add_i32 s18, s13, 0x4120800\n" \
M_"a[16:19], a[224:227], a[212:215], a[16:19]\n" \
X_"a[186:187], v38 offset:33792\n" \
"s_add_i32 s15, s18, s15\n" \
M_"a[32:35], a[160:163], a[152:155], a[32:35]\n" \
"s_addk_i32 s84, 0x2000\n" \
M_"a[32:35], a[224:227], a[216:219], a[32:35]\n" \
B_"v24, s[76:79], s84 offen offset:2048 lds\n" \
M_"a[36:39], a[164:167], a[152:155], a[36:39]\n" \
X_"a[248:249], v38 offset:40960\n" \
"v_writelane_b32 v11, s15, 36\n" \
M_"a[36:39], a[228:231], a[216:219], a[36:39]\n" \
X_"a[250:251], v38 offset:41984\n" \
"s_add_i32 s12, s18, s12\n" \
M_"a[44:47], a[172:175], a[152:155], a[44:47]\n" \
X_"a[188:189], v39 offset:32768\n" \
"v_writelane_b32 v11, s12, 37\n" \
M_"a[44:47], a[236:239], a[216:219], a[44:47]\n" \
X_"a[190:191], v39 offset:33792\n" \
"s_add_i32 s18, s18, s14\n" \
M_"a[40:43], a[152:155], a[168:171], a[40:43]\n" \
"s_add_u32 s89, s42, 0x1000\n" \
"buffer_store_dwordx4 v[224:227], v20, s[80:83], s89 offen offset:3072 sc1\n" \
M_"a[40:43], a[216:219], a[232:235], a[40:43]\n" \
X_"a[252:253], v39 offset:40960\n" \
"s_or_b32 s12, s17, 0x3000000\n" \
M_"a[56:59], a[156:159], a[168:171], a[56:59]\n" \
X_"a[254:255], v39 offset:41984\n" \
"s_lshl_b32 s14, s5, 11\n" \
M_"a[56:59], a[220:223], a[232:235], a[56:59]\n" \
"v_writelane_b32 v11, s18, 38\n" \
"s_add_i32 s15, s12, s14\n" \
M_"a[60:63], a[172:175], a[156:159], a[60:63]\n" \
"v_writelane_b32 v11, s15, 39\n" \
"s_add_i32 s15, s13, 0x411c000\n" \
M_"a[60:63], a[236:239], a[220:223], a[60:63]\n" \
"s_addk_i32 s84, 0x2000\n" \
M_"a[52:55], a[164:167], a[156:159], a[52:55]\n" \
B_"v25, s[76:79], s84 offen offset:3072 lds\n" \
M_"a[52:55], a[228:231], a[220:223], a[52:55]\n" \
"s_add_i32 s18, s15, s14\n" \
"v_writelane_b32 v11, s18, 40\n" \
M_"a[48:51], a[160:163], a[156:159], a[48:51]\n" \
"s_add_i32 s17, s17, 0x3004000\n" \
"v_writelane_b32 v11, s17, 41\n" \
M_"a[48:51], a[224:227], a[220:223], a[48:51]\n" \
"s_add_i32 s13, s13, 0x4120000\n" \
"v_writelane_b32 v11, s13, 42\n" \
M_"a[64:67], a[164:167], a[140:143], a[64:67]\n" \
"s_lshl_b32 s13, s9, 2\n" \
"s_or_b32 s7, s13, s7\n" \
M_"a[64:67], a[228:231], a[204:207], a[64:67]\n" \
"s_mul_i32 s7, s7, 0x90000\n" \
"s_lshl_b32 s13, s3, 14\n" \
M_"a[80:83], a[172:175], a[140:143], a[80:83]\n" \
"s_lshl_b32 s9, s9, 7\n" \
"s_lshl_b32 s8, s8, 4\n" \
M_"a[80:83], a[236:239], a[204:207], a[80:83]\n" \
"s_add_i32 s7, s7, s13\n" \
"s_or_b32 s13, s8, s9\n" \
M_"a[68:71], a[140:143], a[168:171], a[68:71]\n" \
"s_sub_i32 s8, s8, s9\n" \
"s_mulk_i32 s13, 0x480\n" \
"s_waitcnt vmcnt(11) lgkmcnt(0)\n" \
M_"a[68:71], a[204:207], a[232:235], a[68:71]\n" \
"s_barrier\n" \
M_"v[116:119], a[176:179], a[144:147], v[116:119]\n" \
"s_add_u32 m0, s40, 0x8000\n" \
"s_add_u32 s85, s39, 0xa000\n" \
M_"v[116:119], a[240:243], a[208:211], v[116:119]\n" \
B_"v20, s[80:83], s85 offen sc1 lds\n" \
M_"v[120:123], a[180:183], a[144:147], v[120:123]\n" \
X_"a[160:161], v32 offset:0\n" \
"v_accvgpr_read_b32 v212, a108\n" \
M_"v[120:123], a[244:247], a[208:211], v[120:123]\n" \
X_"a[162:163], v32 offset:512\n" \
"v_accvgpr_read_b32 v213, a109\n" \
M_"v[124:127], a[184:187], a[144:147], v[124:127]\n" \
"s_add_u32 m0, s40, 0x9000\n" \
"s_add_u32 s85, s39, 0xb000\n" \
M_"v[124:127], a[248:251], a[208:211], v[124:127]\n" \
B_"v20, s[80:83], s85 offen sc1 lds\n" \
M_"v[128:131], a[188:191], a[144:147], v[128:131]\n" \
X_"a[224:225], v32 offset:4096\n" \
"v_accvgpr_read_b32 v214, a110\n" \
M_"v[128:131], a[252:255], a[208:211], v[128:131]\n" \
R_"a[144:147], v28 offset:40960\n" \
"v_accvgpr_read_b32 v215, a111\n" \
M_"v[144:147], a[188:191], a[148:151], v[144:147]\n" \
R_"a[208:211], v29 offset:40960\n" \
"v_accvgpr_read_b32 v180, a124\n" \
M_"v[144:147], a[252:255], a[212:215], v[144:147]\n" \
"s_add_u32 m0, s40, 0xb000\n" \
"s_movk_i32 s84, 0x280\n" \
"s_nop 0\n" \
M_"v[140:143], a[184:187], a[148:151], v[140:143]\n" \
B_"v21, s[76:79], s84 offen offset:0 lds\n" \
M_"v[140:143], a[248:251], a[212:215], v[140:143]\n" \
X_"a[226:227], v32 offset:4608\n" \
"v_accvgpr_read_b32 v181, a125\n" \
M_"v[136:139], a[180:183], a[148:151], v[136:139]\n" \
"s_add_u32 s88, s42, 0x1000\n" \
"buffer_store_dwordx4 v[228:231], v20, s[80:83], s88 offen offset:2048 sc1\n" \
M_"v[136:139], a[244:247], a[212:215], v[136:139]\n" \
X_"a[164:165], v33 offset:0\n" \
"v_accvgpr_read_b32 v182, a126\n" \
M_"v[132:135], a[176:179], a[148:151], v[132:135]\n" \
"v_accvgpr_read_b32 v183, a127\n" \
C_"v212, v212, v213\n" \
M_"v[132:135], a[240:243], a[212:215], v[132:135]\n" \
R_"a[148:151], v28 offset:43008\n" \
C_"v213, v214, v215\n" \
M_"v[148:151], a[176:179], a[152:155], v[148:151]\n" \
R_"a[212:215], v29 offset:43008\n" \
C_"v214, v180, v181\n" \
M_"v[148:151], a[240:243], a[216:219], v[148:151]\n" \
"s_add_u32 m0, s31, 0x8000\n" \
"s_mov_b32 s84, 0x280000\n" \
M_"v[152:155], a[180:183], a[152:155], v[152:155]\n" \
B_"v26, s[68:71], s84 offen offset:0 lds\n" \
M_"v[152:155], a[244:247], a[216:219], v[152:155]\n" \
X_"a[166:167], v33 offset:512\n" \
C_"v215, v182, v183\n" \
M_"v[156:159], a[184:187], a[152:155], v[156:159]\n" \
X_"a[228:229], v33 offset:4096\n" \
"v_accvgpr_read_b32 v208, a104\n" \
M_"v[156:159], a[248:251], a[216:219], v[156:159]\n" \
"s_addk_i32 s84, 0x7c00\n" \
M_"v[160:163], a[188:191], a[152:155], v[160:163]\n" \
B_"v26, s[68:71], s84 offen offset:1024 lds\n" \
M_"v[160:163], a[252:255], a[216:219], v[160:163]\n" \
R_"a[152:155], v28 offset:45056\n" \
"v_accvgpr_read_b32 v209, a105\n" \
M_"v[176:179], a[188:191], a[156:159], v[176:179]\n" \
R_"a[216:219], v29 offset:45056\n" \
"v_accvgpr_read_b32 v210, a106\n" \
M_"v[176:179], a[252:255], a[220:223], v[176:179]\n" \
"s_addk_i32 s84, 0x7c00\n" \
M_"v[172:175], a[184:187], a[156:159], v[172:175]\n" \
B_"v27, s[68:71], s84 offen offset:2048 lds\n" \
M_"v[172:175], a[248:251], a[220:223], v[172:175]\n" \
X_"a[230:231], v33 offset:4608\n" \
"v_accvgpr_read_b32 v211, a107\n" \
M_"v[168:171], a[180:183], a[156:159], v[168:171]\n" \
X_"a[172:173], v35 offset:0\n" \
"v_accvgpr_read_b32 v180, a120\n" \
M_"v[168:171], a[244:247], a[220:223], v[168:171]\n" \
X_"a[174:175], v35 offset:512\n" \
"v_accvgpr_read_b32 v181, a121\n" \
M_"v[164:167], a[176:179], a[156:159], v[164:167]\n" \
"v_accvgpr_read_b32 v182, a122\n" \
"v_accvgpr_read_b32 v183, a123\n" \
M_"v[164:167], a[240:243], a[220:223], v[164:167]\n" \
R_"a[156:159], v28 offset:47104\n" \
C_"v208, v208, v209\n" \
M_"v[84:87], a[176:179], a[136:139], v[84:87]\n" \
R_"a[220:223], v29 offset:47104\n" \
C_"v209, v210, v211\n" \
M_"v[84:87], a[240:243], a[200:203], v[84:87]\n" \
"s_addk_i32 s84, 0x7c00\n" \
M_"v[88:91], a[180:183], a[136:139], v[88:91]\n" \
B_"v27, s[68:71], s84 offen offset:3072 lds\n" \
M_"v[88:91], a[244:247], a[200:203], v[88:91]\n" \
"s_add_u32 m0, s30, 0x0\n" \
"s_movk_i32 s84, 0x300\n" \
"s_nop 0\n" \
M_"v[92:95], a[184:187], a[136:139], v[92:95]\n" \
B_"v24, s[76:79], s84 offen offset:0 lds\n" \
M_"v[92:95], a[248:251], a[200:203], v[92:95]\n" \
X_"a[236:237], v35 offset:4096\n" \
C_"v210, v180, v181\n" \
M_"v[96:99], a[188:191], a[136:139], v[96:99]\n" \
X_"a[238:239], v35 offset:4608\n" \
C_"v211, v182, v183\n" \
M_"v[96:99], a[252:255], a[200:203], v[96:99]\n" \
R_"a[136:139], v28 offset:36864\n" \
"v_accvgpr_read_b32 v204, a100\n" \
M_"v[112:115], a[188:191], a[140:143], v[112:115]\n" \
R_"a[200:203], v29 offset:36864\n" \
"v_accvgpr_read_b32 v205, a101\n" \
M_"v[112:115], a[252:255], a[204:207], v[112:115]\n" \
"s_movk_i32 s84, 0x2300\n" \
"s_nop 0\n" \
M_"v[108:111], a[184:187], a[140:143], v[108:111]\n" \
B_"v25, s[76:79], s84 offen offset:1024 lds\n" \
M_"v[108:111], a[248:251], a[204:207], v[108:111]\n" \
"s_add_u32 s89, s42, 0x1000\n" \
"buffer_store_dwordx4 v[232:235], v20, s[80:83], s89 offen offset:1024 sc1\n" \
M_"v[104:107], a[180:183], a[140:143], v[104:107]\n" \
X_"a[168:169], v34 offset:0\n" \
"v_accvgpr_read_b32 v206, a102\n" \
M_"v[104:107], a[244:247], a[204:207], v[104:107]\n" \
X_"a[170:171], v34 offset:512\n" \
"v_accvgpr_read_b32 v207, a103\n" \
M_"v[100:103], a[176:179], a[140:143], v[100:103]\n" \
X_"a[232:233], v34 offset:4096\n" \
"v_accvgpr_read_b32 v180, a116\n" \
M_"v[100:103], a[240:243], a[204:207], v[100:103]\n" \
R_"a[140:143], v28 offset:38912\n" \
"v_accvgpr_read_b32 v181, a117\n" \
M_"v[52:55], a[176:179], a[128:131], v[52:55]\n" \
R_"a[204:207], v29 offset:38912\n" \
"v_accvgpr_read_b32 v182, a118\n" \
M_"v[52:55], a[240:243], a[192:195], v[52:55]\n" \
"s_movk_i32 s84, 0x4300\n" \
M_"v[56:59], a[180:183], a[128:131], v[56:59]\n" \
B_"v24, s[76:79], s84 offen offset:2048 lds\n" \
M_"v[56:59], a[244:247], a[192:195], v[56:59]\n" \
"s_movk_i32 s84, 0x6300\n" \
M_"v[60:63], a[184:187], a[128:131], v[60:63]\n" \
B_"v25, s[76:79], s84 offen offset:3072 lds\n" \
M_"v[60:63], a[248:251], a[192:195], v[60:63]\n" \
X_"a[234:235], v34 offset:4608\n" \
"v_accvgpr_read_b32 v183, a119\n" \
M_"v[64:67], a[188:191], a[128:131], v[64:67]\n" \
C_"v204, v204, v205\n" \
C_"v205, v206, v207\n" \
M_"v[64:67], a[252:255], a[192:195], v[64:67]\n" \
R_"a[128:131], v28 offset:32768\n" \
C_"v206, v180, v181\n" \
M_"v[80:83], a[188:191], a[132:135], v[80:83]\n" \
R_"a[192:195], v29 offset:32768\n" \
C_"v207, v182, v183\n" \
M_"v[80:83], a[252:255], a[196:199], v[80:83]\n" \
"v_accvgpr_read_b32 v200, a96\n" \
"v_accvgpr_read_b32 v201, a97\n" \
M_"v[76:79], a[184:187], a[132:135], v[76:79]\n" \
"v_accvgpr_read_b32 v202, a98\n" \
"v_accvgpr_read_b32 v203, a99\n" \
M_"v[76:79], a[248:251], a[196:199], v[76:79]\n" \
"v_accvgpr_read_b32 v180, a112\n" \
"v_accvgpr_read_b32 v181, a113\n" \
M_"v[72:75], a[180:183], a[132:135], v[72:75]\n" \
"v_accvgpr_read_b32 v182, a114\n" \
"v_accvgpr_read_b32 v183, a115\n" \
M_"v[72:75], a[244:247], a[196:199], v[72:75]\n" \
C_"v200, v200, v201\n" \
C_"v201, v202, v203\n" \
M_"v[68:71], a[176:179], a[132:135], v[68:71]\n" \
C_"v202, v180, v181\n" \
C_"v203, v182, v183\n" \
M_"v[68:71], a[240:243], a[196:199], v[68:71]\n" \
R_"a[132:135], v28 offset:34816\n" \
R_"a[196:199], v29 offset:34816\n" \
"s_waitcnt lgkmcnt(15)\n" \
M_"a[0:3], a[160:163], a[144:147], a[0:3]\n" \
"s_add_u32 m0, s30, 0x1000\n" \
"s_addk_i32 s84, 0x3000\n" \
M_"a[0:3], a[224:227], a[208:211], a[0:3]\n" \
B_"v24, s[76:79], s84 offen offset:0 lds\n" \
M_"a[4:7], a[164:167], a[144:147], a[4:7]\n" \
X_"a[176:177], v36 offset:0\n" \
"v_accvgpr_read_b32 v196, a76\n" \
M_"a[4:7], a[228:231], a[208:211], a[4:7]\n" \
X_"a[178:179], v36 offset:1024\n" \
"v_accvgpr_read_b32 v197, a77\n" \
M_"a[12:15], a[172:175], a[144:147], a[12:15]\n" \
X_"a[240:241], v36 offset:8192\n" \
"v_accvgpr_read_b32 v198, a78\n" \
"s_waitcnt lgkmcnt(15)\n" \
M_"a[12:15], a[236:239], a[208:211], a[12:15]\n" \
"s_add_u32 s88, s42, 0x1000\n" \
"buffer_store_dwordx4 v[236:239], v20, s[80:83], s88 offen offset:0 sc1\n" \
"s_waitcnt lgkmcnt(11)\n" \
M_"a[8:11], a[144:147], a[168:171], a[8:11]\n" \
X_"a[242:243], v36 offset:9216\n" \
"v_accvgpr_read_b32 v199, a79\n" \
"s_waitcnt lgkmcnt(8)\n" \
"s_nop 0\n" \
M_"a[8:11], a[208:211], a[232:235], a[8:11]\n" \
X_"a[180:181], v37 offset:0\n" \
"v_accvgpr_read_b32 v180, a92\n" \
M_"a[24:27], a[148:151], a[168:171], a[24:27]\n" \
X_"a[182:183], v37 offset:1024\n" \
"v_accvgpr_read_b32 v181, a93\n" \
M_"a[24:27], a[212:215], a[232:235], a[24:27]\n" \
X_"a[244:245], v37 offset:8192\n" \
"v_accvgpr_read_b32 v182, a94\n" \
M_"a[28:31], a[172:175], a[148:151], a[28:31]\n" \
"v_accvgpr_read_b32 v183, a95\n" \
C_"v196, v196, v197\n" \
M_"a[28:31], a[236:239], a[212:215], a[28:31]\n" \
"s_addk_i32 s84, 0x2000\n" \
"s_nop 0\n" \
M_"a[20:23], a[164:167], a[148:151], a[20:23]\n" \
B_"v25, s[76:79], s84 offen offset:1024 lds\n" \
M_"a[20:23], a[228:231], a[212:215], a[20:23]\n" \
X_"a[246:247], v37 offset:9216\n" \
C_"v197, v198, v199\n" \
M_"a[16:19], a[160:163], a[148:151], a[16:19]\n" \
".fill (L_pb + 69344 - .) / 4, 4, 0xBF800000\n" \
"L_pp17:\n" \
"s_cbranch_execz L_pret\n" \
X_"a[184:185], v38 offset:0\n" \
C_"v198, v180, v181\n" \
M_"a[16:19], a[224:227], a[212:215], a[16:19]\n" \
X_"a[186:187], v38 offset:1024\n" \
C_"v199, v182, v183\n" \
M_"a[32:35], a[160:163], a[152:155], a[32:35]\n" \
"s_addk_i32 s84, 0x2000\n" \
M_"a[32:35], a[224:227], a[216:219], a[32:35]\n" \
B_"v24, s[76:79], s84 offen offset:2048 lds\n" \
M_"a[36:39], a[164:167], a[152:155], a[36:39]\n" \
X_"a[248:249], v38 offset:8192\n" \
"v_accvgpr_read_b32 v192, a72\n" \
M_"a[36:39], a[228:231], a[216:219], a[36:39]\n" \
X_"a[250:251], v38 offset:9216\n" \
"v_accvgpr_read_b32 v193, a73\n" \
M_"a[44:47], a[172:175], a[152:155], a[44:47]\n" \
X_"a[188:189], v39 offset:0\n" \
"v_accvgpr_read_b32 v194, a74\n" \
M_"a[44:47], a[236:239], a[216:219], a[44:47]\n" \
X_"a[190:191], v39 offset:1024\n" \
"v_accvgpr_read_b32 v195, a75\n" \
M_"a[40:43], a[152:155], a[168:171], a[40:43]\n" \
"s_add_u32 s89, s42, 0x3000\n" \
"buffer_store_dwordx4 v[240:243], v20, s[80:83], s89 offen offset:3072 sc1\n" \
M_"a[40:43], a[216:219], a[232:235], a[40:43]\n" \
X_"a[252:253], v39 offset:8192\n" \
"v_accvgpr_read_b32 v180, a88\n" \
M_"a[56:59], a[156:159], a[168:171], a[56:59]\n" \
X_"a[254:255], v39 offset:9216\n" \
"v_accvgpr_read_b32 v181, a89\n" \
M_"a[56:59], a[220:223], a[232:235], a[56:59]\n" \
"v_accvgpr_read_b32 v182, a90\n" \
"v_accvgpr_read_b32 v183, a91\n" \
M_"a[60:63], a[172:175], a[156:159], a[60:63]\n" \
C_"v192, v192, v193\n" \
C_"v193, v194, v195\n" \
M_"a[60:63], a[236:239], a[220:223], a[60:63]\n" \
"s_addk_i32 s84, 0x2000\n" \
M_"a[52:55], a[164:167], a[156:159], a[52:55]\n" \
B_"v25, s[76:79], s84 offen offset:3072 lds\n" \
M_"a[52:55], a[228:231], a[220:223], a[52:55]\n" \
C_"v194, v180, v181\n" \
C_"v195, v182, v183\n" \
M_"a[48:51], a[160:163], a[156:159], a[48:51]\n" \
"s_mulk_i32 s8, 0x480\n" \
"s_or_b32 s13, s13, s11\n" \
M_"a[48:51], a[224:227], a[220:223], a[48:51]\n" \
"s_or_b32 s8, s8, s11\n" \
"s_and_b32 s11, s2, 1\n" \
M_"a[64:67], a[164:167], a[140:143], a[64:67]\n" \
"s_cmp_lg_u32 s11, 0\n" \
"s_cselect_b32 s12, s15, s12\n" \
M_"a[64:67], a[228:231], a[204:207], a[64:67]\n" \
"s_bfe_u32 s9, s2, 0x20001\n" \
"s_add_i32 s15, s7, 0x4240000\n" \
M_"a[80:83], a[172:175], a[140:143], a[80:83]\n" \
"s_addk_i32 s12, 0x4000\n" \
"s_lshl_b32 s5, s5, 16\n" \
M_"a[80:83], a[236:239], a[204:207], a[80:83]\n" \
"s_xor_b32 s9, s9, s3\n" \
M_"a[68:71], a[140:143], a[168:171], a[68:71]\n" \
"s_cmp_lg_u32 s9, 0\n" \
"s_cselect_b32 s12, s6, s12\n" \
"s_waitcnt vmcnt(11) lgkmcnt(0)\n" \
M_"a[68:71], a[204:207], a[232:235], a[68:71]\n" \
"s_barrier\n" \
"v_mov_b32_e32 v45, s62\n" \
"s_mov_b64 exec, 1\n" \
"buffer_store_dword v45, off, s[80:83], s37 offset:0 sc1\n" \
"s_mov_b64 exec, -1\n" \
"v_xor_b32_e32 v28, 0x20000, v28\n" \
"v_xor_b32_e32 v29, 0x20000, v29\n" \
M_"v[116:119], a[176:179], a[144:147], v[116:119]\n" \
"s_add_u32 m0, s40, 0x0\n" \
"s_add_u32 s85, s39, 0xc000\n" \
M_"v[116:119], a[240:243], a[208:211], v[116:119]\n" \
B_"v20, s[80:83], s85 offen sc1 lds\n" \
M_"v[120:123], a[180:183], a[144:147], v[120:123]\n" \
X_"a[160:161], v32 offset:32768\n" \
"s_add_i32 s5, s15, s5\n" \
"s_nop 0\n" \
M_"v[120:123], a[244:247], a[208:211], v[120:123]\n" \
X_"a[162:163], v32 offset:33280\n" \
"v_writelane_b32 v11, s12, 43\n" \
M_"v[124:127], a[184:187], a[144:147], v[124:127]\n" \
"s_add_u32 m0, s40, 0x1000\n" \
"s_add_u32 s85, s39, 0xd000\n" \
M_"v[124:127], a[248:251], a[208:211], v[124:127]\n" \
B_"v20, s[80:83], s85 offen sc1 lds\n" \
M_"v[128:131], a[188:191], a[144:147], v[128:131]\n" \
X_"a[224:225], v32 offset:36864\n" \
"s_add_i32 s7, s7, 0x42c0000\n" \
M_"v[128:131], a[252:255], a[208:211], v[128:131]\n" \
R_"a[144:147], v28 offset:8192\n" \
"v_writelane_b32 v11, s5, 44\n" \
M_"v[144:147], a[188:191], a[148:151], v[144:147]\n" \
R_"a[208:211], v29 offset:8192\n" \
"s_lshl_b32 s5, s11, 10\n" \
M_"v[144:147], a[252:255], a[212:215], v[144:147]\n" \
"s_add_u32 m0, s40, 0x3000\n" \
"s_movk_i32 s84, 0x300\n" \
M_"v[140:143], a[184:187], a[148:151], v[140:143]\n" \
B_"v21, s[76:79], s84 offen offset:0 lds\n" \
M_"v[140:143], a[248:251], a[212:215], v[140:143]\n" \
X_"a[226:227], v32 offset:37376\n" \
"s_or_b32 s5, s7, s5\n" \
M_"v[136:139], a[180:183], a[148:151], v[136:139]\n" \
"s_cmp_lg_u32 s9, 0\n" \
"s_cselect_b32 s5, s6, s5\n" \
M_"v[136:139], a[244:247], a[212:215], v[136:139]\n" \
X_"a[164:165], v33 offset:32768\n" \
"s_add_i32 s8, s8, 0x21c000\n" \
M_"v[132:135], a[176:179], a[148:151], v[132:135]\n" \
"v_writelane_b32 v11, s5, 45\n" \
"s_add_i32 s5, s13, s10\n" \
M_"v[132:135], a[240:243], a[212:215], v[132:135]\n" \
R_"a[148:151], v28 offset:10240\n" \
"v_writelane_b32 v11, s5, 46\n" \
M_"v[148:151], a[176:179], a[152:155], v[148:151]\n" \
R_"a[212:215], v29 offset:10240\n" \
"s_add_i32 s5, s8, s10\n" \
M_"v[148:151], a[240:243], a[216:219], v[148:151]\n" \
"s_add_u32 m0, s31, 0x0\n" \
"s_mov_b32 s84, 0x300000\n" \
M_"v[152:155], a[180:183], a[152:155], v[152:155]\n" \
B_"v26, s[68:71], s84 offen offset:0 lds\n" \
M_"v[152:155], a[244:247], a[216:219], v[152:155]\n" \
"s_addk_i32 s84, 0x7c00\n" \
"s_nop 0\n" \
M_"v[156:159], a[184:187], a[152:155], v[156:159]\n" \
B_"v26, s[68:71], s84 offen offset:1024 lds\n" \
M_"v[156:159], a[248:251], a[216:219], v[156:159]\n" \
X_"a[166:167], v33 offset:33280\n" \
"v_writelane_b32 v11, s5, 47\n" \
M_"v[160:163], a[188:191], a[152:155], v[160:163]\n" \
X_"a[228:229], v33 offset:36864\n" \
M_"v[160:163], a[252:255], a[216:219], v[160:163]\n" \
R_"a[152:155], v28 offset:12288\n" \
M_"v[176:179], a[188:191], a[156:159], v[176:179]\n" \
R_"a[216:219], v29 offset:12288\n" \
M_"v[176:179], a[252:255], a[220:223], v[176:179]\n" \
"s_addk_i32 s84, 0x7c00\n" \
M_"v[172:175], a[184:187], a[156:159], v[172:175]\n" \
B_"v27, s[68:71], s84 offen offset:2048 lds\n" \
M_"v[172:175], a[248:251], a[220:223], v[172:175]\n" \
X_"a[230:231], v33 offset:37376\n" \
M_"v[168:171], a[180:183], a[156:159], v[168:171]\n" \
"s_addk_i32 s84, 0x7c00\n" \
M_"v[168:171], a[244:247], a[220:223], v[168:171]\n" \
B_"v27, s[68:71], s84 offen offset:3072 lds\n" \
M_"v[164:167], a[176:179], a[156:159], v[164:167]\n" \
X_"a[172:173], v35 offset:32768\n" \
M_"v[164:167], a[240:243], a[220:223], v[164:167]\n" \
R_"a[156:159], v28 offset:14336\n" \
M_"v[84:87], a[176:179], a[136:139], v[84:87]\n" \
R_"a[220:223], v29 offset:14336\n" \
M_"v[84:87], a[240:243], a[200:203], v[84:87]\n" \
"s_add_u32 s88, s42, 0x3000\n" \
"buffer_store_dwordx4 v[244:247], v20, s[80:83], s88 offen offset:2048 sc1\n" \
M_"v[88:91], a[180:183], a[136:139], v[88:91]\n" \
X_"a[174:175], v35 offset:33280\n" \
M_"v[88:91], a[244:247], a[200:203], v[88:91]\n" \
X_"a[236:237], v35 offset:36864\n" \
M_"v[92:95], a[184:187], a[136:139], v[92:95]\n" \
X_"a[238:239], v35 offset:37376\n" \
M_"v[92:95], a[248:251], a[200:203], v[92:95]\n" \
"s_add_u32 m0, s30, 0x8000\n" \
"s_movk_i32 s84, 0x380\n" \
M_"v[96:99], a[188:191], a[136:139], v[96:99]\n" \
B_"v24, s[76:79], s84 offen offset:0 lds\n" \
M_"v[96:99], a[252:255], a[200:203], v[96:99]\n" \
R_"a[136:139], v28 offset:4096\n" \
M_"v[112:115], a[188:191], a[140:143], v[112:115]\n" \
R_"a[200:203], v29 offset:4096\n" \
M_"v[112:115], a[252:255], a[204:207], v[112:115]\n" \
X_"a[168:169], v34 offset:32768\n" \
M_"v[108:111], a[184:187], a[140:143], v[108:111]\n" \
"s_movk_i32 s84, 0x2380\n" \
M_"v[108:111], a[248:251], a[204:207], v[108:111]\n" \
B_"v25, s[76:79], s84 offen offset:1024 lds\n" \
M_"v[104:107], a[180:183], a[140:143], v[104:107]\n" \
X_"a[170:171], v34 offset:33280\n" \
M_"v[104:107], a[244:247], a[204:207], v[104:107]\n" \
X_"a[232:233], v34 offset:36864\n" \
M_"v[100:103], a[176:179], a[140:143], v[100:103]\n" \
"s_cmp_lg_u32 s11, 0\n" \
"s_cselect_b32 s5, s8, s13\n" \
M_"v[100:103], a[240:243], a[204:207], v[100:103]\n" \
R_"a[140:143], v28 offset:6144\n" \
"s_addk_i32 s5, 0x100\n" \
M_"v[52:55], a[176:179], a[128:131], v[52:55]\n" \
R_"a[204:207], v29 offset:6144\n" \
M_"v[52:55], a[240:243], a[192:195], v[52:55]\n" \
"s_movk_i32 s84, 0x4380\n" \
M_"v[56:59], a[180:183], a[128:131], v[56:59]\n" \
B_"v24, s[76:79], s84 offen offset:2048 lds\n" \
M_"v[56:59], a[244:247], a[192:195], v[56:59]\n" \
"s_movk_i32 s84, 0x6380\n" \
M_"v[60:63], a[184:187], a[128:131], v[60:63]\n" \
B_"v25, s[76:79], s84 offen offset:3072 lds\n" \
M_"v[60:63], a[248:251], a[192:195], v[60:63]\n" \
X_"a[234:235], v34 offset:37376\n" \
M_"v[64:67], a[188:191], a[128:131], v[64:67]\n" \
"s_cmp_lg_u32 s9, 0\n" \
"s_cselect_b32 s5, s6, s5\n" \
M_"v[64:67], a[252:255], a[192:195], v[64:67]\n" \
R_"a[128:131], v28 offset:0\n" \
"v_writelane_b32 v11, s5, 48\n" \
M_"v[80:83], a[188:191], a[132:135], v[80:83]\n" \
R_"a[192:195], v29 offset:0\n" \
"s_or_b32 s5, s7, s14\n" \
M_"v[80:83], a[252:255], a[196:199], v[80:83]\n" \
"v_writelane_b32 v11, s5, 56\n" \
"s_lshl_b32 s5, s3, 17\n" \
M_"v[76:79], a[184:187], a[132:135], v[76:79]\n" \
"s_add_i32 s5, s15, s5\n" \
"s_or_b32 s4, s5, s4\n" \
M_"v[76:79], a[248:251], a[196:199], v[76:79]\n" \
"s_or_b32 s4, s4, 0x3840\n" \
"v_writelane_b32 v11, s4, 6\n" \
M_"v[72:75], a[180:183], a[132:135], v[72:75]\n" \
"s_mov_b32 s4, 0xff0000\n" \
M_"v[72:75], a[244:247], a[196:199], v[72:75]\n" \
"s_cmp_lg_u32 s9, 0\n" \
"s_cselect_b32 s4, s16, s4\n" \
M_"v[68:71], a[176:179], a[132:135], v[68:71]\n" \
"v_writelane_b32 v11, s4, 7\n" \
"s_lshl_b32 s4, s2, 16\n" \
M_"v[68:71], a[240:243], a[196:199], v[68:71]\n" \
R_"a[132:135], v28 offset:2048\n" \
R_"a[196:199], v29 offset:2048\n" \
"s_waitcnt lgkmcnt(15)\n" \
M_"a[0:3], a[160:163], a[144:147], a[0:3]\n" \
"s_add_u32 m0, s30, 0x9000\n" \
"s_addk_i32 s84, 0x3000\n" \
M_"a[0:3], a[224:227], a[208:211], a[0:3]\n" \
B_"v24, s[76:79], s84 offen offset:0 lds\n" \
M_"a[4:7], a[164:167], a[144:147], a[4:7]\n" \
X_"a[176:177], v36 offset:32768\n" \
"s_add_i32 s5, s4, 0x10000\n" \
M_"a[4:7], a[228:231], a[208:211], a[4:7]\n" \
X_"a[178:179], v36 offset:33792\n" \
"s_and_b32 s5, s5, 0x70000\n" \
"s_waitcnt lgkmcnt(15)\n" \
M_"a[12:15], a[172:175], a[144:147], a[12:15]\n" \
X_"a[240:241], v36 offset:40960\n" \
"s_add_i32 s5, s15, s5\n" \
"s_waitcnt lgkmcnt(15)\n" \
M_"a[12:15], a[236:239], a[208:211], a[12:15]\n" \
"s_add_u32 s89, s42, 0x3000\n" \
"buffer_store_dwordx4 v[184:187], v20, s[80:83], s89 offen offset:1024 sc1\n" \
"s_waitcnt lgkmcnt(11)\n" \
M_"a[8:11], a[144:147], a[168:171], a[8:11]\n" \
X_"a[242:243], v36 offset:41984\n" \
"v_writelane_b32 v11, s5, 49\n" \
"s_waitcnt lgkmcnt(8)\n" \
M_"a[8:11], a[208:211], a[232:235], a[8:11]\n" \
X_"a[180:181], v37 offset:32768\n" \
"s_add_i32 s5, s4, 0x20000\n" \
M_"a[24:27], a[148:151], a[168:171], a[24:27]\n" \
X_"a[182:183], v37 offset:33792\n" \
"s_and_b32 s5, s5, 0x70000\n" \
M_"a[24:27], a[212:215], a[232:235], a[24:27]\n" \
X_"a[244:245], v37 offset:40960\n" \
"s_add_i32 s5, s15, s5\n" \
M_"a[28:31], a[172:175], a[148:151], a[28:31]\n" \
"s_bitset1_b32 s5, 11\n" \
"v_writelane_b32 v11, s5, 50\n" \
M_"a[28:31], a[236:239], a[212:215], a[28:31]\n" \
"s_addk_i32 s84, 0x2000\n" \
M_"a[20:23], a[164:167], a[148:151], a[20:23]\n" \
B_"v25, s[76:79], s84 offen offset:1024 lds\n" \
M_"a[20:23], a[228:231], a[212:215], a[20:23]\n" \
X_"a[246:247], v37 offset:41984\n" \
"s_add_i32 s5, s4, 0x30000\n" \
M_"a[16:19], a[160:163], a[148:151], a[16:19]\n" \
X_"a[184:185], v38 offset:32768\n" \
"s_and_b32 s5, s5, 0x70000\n" \
M_"a[16:19], a[224:227], a[212:215], a[16:19]\n" \
X_"a[186:187], v38 offset:33792\n" \
"s_add_i32 s5, s15, s5\n" \
M_"a[32:35], a[160:163], a[152:155], a[32:35]\n" \
"s_addk_i32 s84, 0x2000\n" \
M_"a[32:35], a[224:227], a[216:219], a[32:35]\n" \
B_"v24, s[76:79], s84 offen offset:2048 lds\n" \
M_"a[36:39], a[164:167], a[152:155], a[36:39]\n" \
X_"a[248:249], v38 offset:40960\n" \
"s_bitset1_b32 s5, 12\n" \
"s_nop 0\n" \
M_"a[36:39], a[228:231], a[216:219], a[36:39]\n" \
X_"a[250:251], v38 offset:41984\n" \
"v_writelane_b32 v11, s5, 51\n" \
M_"a[44:47], a[172:175], a[152:155], a[44:47]\n" \
X_"a[188:189], v39 offset:32768\n" \
"s_and_b32 s5, s4, 0x70000\n" \
M_"a[44:47], a[236:239], a[216:219], a[44:47]\n" \
X_"a[190:191], v39 offset:33792\n" \
"s_xor_b32 s5, s5, 0x40000\n" \
M_"a[40:43], a[152:155], a[168:171], a[40:43]\n" \
"s_add_u32 s88, s42, 0x3000\n" \
"buffer_store_dwordx4 v[188:191], v20, s[80:83], s88 offen offset:0 sc1\n" \
M_"a[40:43], a[216:219], a[232:235], a[40:43]\n" \
X_"a[252:253], v39 offset:40960\n" \
"s_add_i32 s5, s15, s5\n" \
M_"a[56:59], a[156:159], a[168:171], a[56:59]\n" \
X_"a[254:255], v39 offset:41984\n" \
"s_or_b32 s5, s5, 0x1800\n" \
M_"a[56:59], a[220:223], a[232:235], a[56:59]\n" \
"v_writelane_b32 v11, s5, 52\n" \
"s_add_i32 s5, s4, 0x50000\n" \
M_"a[60:63], a[172:175], a[156:159], a[60:63]\n" \
"s_and_b32 s5, s5, 0x70000\n" \
"s_add_i32 s5, s15, s5\n" \
M_"a[60:63], a[236:239], a[220:223], a[60:63]\n" \
"s_addk_i32 s84, 0x2000\n" \
M_"a[52:55], a[164:167], a[156:159], a[52:55]\n" \
B_"v25, s[76:79], s84 offen offset:3072 lds\n" \
M_"a[52:55], a[228:231], a[220:223], a[52:55]\n" \
"s_bitset1_b32 s5, 13\n" \
"v_writelane_b32 v11, s5, 53\n" \
M_"a[48:51], a[160:163], a[156:159], a[48:51]\n" \
"s_add_i32 s5, s4, 0x60000\n" \
"s_and_b32 s5, s5, 0x70000\n" \
M_"a[48:51], a[224:227], a[220:223], a[48:51]\n" \
"s_add_i32 s4, s4, 0x70000\n" \
"s_add_i32 s5, s15, s5\n" \
M_"a[64:67], a[164:167], a[140:143], a[64:67]\n" \
"s_and_b32 s4, s4, 0x70000\n" \
"s_or_b32 s5, s5, 0x2800\n" \
M_"a[64:67], a[228:231], a[204:207], a[64:67]\n" \
"v_writelane_b32 v11, s5, 54\n" \
"s_add_i32 s15, s15, s4\n" \
M_"a[80:83], a[172:175], a[140:143], a[80:83]\n" \
"s_or_b32 s4, s15, 0x3000\n" \
"v_writelane_b32 v11, s4, 55\n" \
M_"a[80:83], a[236:239], a[204:207], a[80:83]\n" \
"s_movk_i32 s11, 0x4000\n" \
"s_lshl_b32 s4, s2, 2\n" \
M_"a[68:71], a[140:143], a[168:171], a[68:71]\n" \
"s_and_b32 s4, s4, 28\n" \
"s_bfe_u32 s5, s2, 0x20003\n" \
"s_waitcnt vmcnt(11) lgkmcnt(0)\n" \
M_"a[68:71], a[204:207], a[232:235], a[68:71]\n" \
"s_barrier\n" \
"v_xor_b32_e32 v28, 0x20000, v28\n" \
"v_xor_b32_e32 v29, 0x20000, v29\n" \
M_"v[116:119], a[176:179], a[144:147], v[116:119]\n" \
"s_add_u32 m0, s40, 0x8000\n" \
"s_add_u32 s85, s39, 0xe000\n" \
M_"v[116:119], a[240:243], a[208:211], v[116:119]\n" \
B_"v20, s[80:83], s85 offen sc1 lds\n" \
M_"v[120:123], a[180:183], a[144:147], v[120:123]\n" \
X_"a[160:161], v32 offset:0\n" \
"s_or_b32 s4, s4, s5\n" \
M_"v[120:123], a[244:247], a[208:211], v[120:123]\n" \
X_"a[162:163], v32 offset:512\n" \
"s_lshr_b32 s5, s2, 5\n" \
M_"v[124:127], a[184:187], a[144:147], v[124:127]\n" \
"s_add_u32 m0, s40, 0x9000\n" \
"s_add_u32 s85, s39, 0xf000\n" \
M_"v[124:127], a[248:251], a[208:211], v[124:127]\n" \
B_"v20, s[80:83], s85 offen sc1 lds\n" \
M_"v[128:131], a[188:191], a[144:147], v[128:131]\n" \
X_"a[224:225], v32 offset:4096\n" \
"s_lshr_b32 s19, s3, 1\n" \
M_"v[128:131], a[252:255], a[208:211], v[128:131]\n" \
R_"a[144:147], v28 offset:8192\n" \
"s_lshl_b32 s18, s5, 1\n" \
M_"v[144:147], a[188:191], a[148:151], v[144:147]\n" \
R_"a[208:211], v29 offset:8192\n" \
"s_lshl_b32 s6, s4, 20\n" \
M_"v[144:147], a[252:255], a[212:215], v[144:147]\n" \
"s_add_u32 m0, s40, 0xb000\n" \
"s_movk_i32 s84, 0x380\n" \
M_"v[140:143], a[184:187], a[148:151], v[140:143]\n" \
B_"v21, s[76:79], s84 offen offset:0 lds\n" \
M_"v[140:143], a[248:251], a[212:215], v[140:143]\n" \
X_"a[226:227], v32 offset:4608\n" \
"s_lshl_b32 s7, s4, 8\n" \
M_"v[136:139], a[180:183], a[148:151], v[136:139]\n" \
"s_and_b32 s13, s3, 1\n" \
"s_add_i32 s14, s18, s19\n" \
M_"v[136:139], a[244:247], a[212:215], v[136:139]\n" \
X_"a[164:165], v33 offset:0\n" \
M_"v[132:135], a[176:179], a[148:151], v[132:135]\n" \
"s_add_u32 s6, s26, s6\n" \
"s_addc_u32 s9, s27, 0\n" \
M_"v[132:135], a[240:243], a[212:215], v[132:135]\n" \
R_"a[148:151], v28 offset:10240\n" \
M_"v[148:151], a[176:179], a[152:155], v[148:151]\n" \
R_"a[212:215], v29 offset:10240\n" \
M_"v[148:151], a[240:243], a[216:219], v[148:151]\n" \
"s_add_u32 m0, s31, 0x8000\n" \
"s_mov_b32 s84, 0x380000\n" \
M_"v[152:155], a[180:183], a[152:155], v[152:155]\n" \
B_"v26, s[68:71], s84 offen offset:0 lds\n" \
M_"v[152:155], a[244:247], a[216:219], v[152:155]\n" \
"s_addk_i32 s84, 0x7c00\n" \
"s_nop 0\n" \
M_"v[156:159], a[184:187], a[152:155], v[156:159]\n" \
B_"v26, s[68:71], s84 offen offset:1024 lds\n" \
M_"v[156:159], a[248:251], a[216:219], v[156:159]\n" \
X_"a[166:167], v33 offset:512\n" \
M_"v[160:163], a[188:191], a[152:155], v[160:163]\n" \
X_"a[228:229], v33 offset:4096\n" \
M_"v[160:163], a[252:255], a[216:219], v[160:163]\n" \
R_"a[152:155], v28 offset:12288\n" \
M_"v[176:179], a[188:191], a[156:159], v[176:179]\n" \
R_"a[216:219], v29 offset:12288\n" \
M_"v[176:179], a[252:255], a[220:223], v[176:179]\n" \
"s_addk_i32 s84, 0x7c00\n" \
M_"v[172:175], a[184:187], a[156:159], v[172:175]\n" \
B_"v27, s[68:71], s84 offen offset:2048 lds\n" \
M_"v[172:175], a[248:251], a[220:223], v[172:175]\n" \
X_"a[230:231], v33 offset:4608\n" \
M_"v[168:171], a[180:183], a[156:159], v[168:171]\n" \
"s_addk_i32 s84, 0x7c00\n" \
M_"v[168:171], a[244:247], a[220:223], v[168:171]\n" \
B_"v27, s[68:71], s84 offen offset:3072 lds\n" \
M_"v[164:167], a[176:179], a[156:159], v[164:167]\n" \
X_"a[172:173], v35 offset:0\n" \
M_"v[164:167], a[240:243], a[220:223], v[164:167]\n" \
R_"a[156:159], v28 offset:14336\n" \
M_"v[84:87], a[176:179], a[136:139], v[84:87]\n" \
R_"a[220:223], v29 offset:14336\n" \
M_"v[84:87], a[240:243], a[200:203], v[84:87]\n" \
"s_add_u32 s89, s42, 0x0\n" \
"buffer_store_dwordx4 v[192:195], v20, s[80:83], s89 offen offset:2048 sc1\n" \
M_"v[88:91], a[180:183], a[136:139], v[88:91]\n" \
X_"a[174:175], v35 offset:512\n" \
M_"v[88:91], a[244:247], a[200:203], v[88:91]\n" \
X_"a[236:237], v35 offset:4096\n" \
M_"v[92:95], a[184:187], a[136:139], v[92:95]\n" \
X_"a[238:239], v35 offset:4608\n" \
M_"v[92:95], a[248:251], a[200:203], v[92:95]\n" \
"s_add_u32 m0, s30, 0x20000\n" \
"s_movk_i32 s84, 0x400\n" \
M_"v[96:99], a[188:191], a[136:139], v[96:99]\n" \
B_"v24, s[76:79], s84 offen offset:0 lds\n" \
M_"v[96:99], a[252:255], a[200:203], v[96:99]\n" \
R_"a[136:139], v28 offset:4096\n" \
M_"v[112:115], a[188:191], a[140:143], v[112:115]\n" \
R_"a[200:203], v29 offset:4096\n" \
M_"v[112:115], a[252:255], a[204:207], v[112:115]\n" \
X_"a[168:169], v34 offset:0\n" \
M_"v[108:111], a[184:187], a[140:143], v[108:111]\n" \
"s_movk_i32 s84, 0x2400\n" \
M_"v[108:111], a[248:251], a[204:207], v[108:111]\n" \
B_"v25, s[76:79], s84 offen offset:1024 lds\n" \
M_"v[104:107], a[180:183], a[140:143], v[104:107]\n" \
X_"a[170:171], v34 offset:512\n" \
M_"v[104:107], a[244:247], a[204:207], v[104:107]\n" \
X_"a[232:233], v34 offset:4096\n" \
M_"v[100:103], a[176:179], a[140:143], v[100:103]\n" \
"s_add_u32 s8, s6, 0x1000000\n" \
"s_addc_u32 s6, s9, 0\n" \
M_"v[100:103], a[240:243], a[204:207], v[100:103]\n" \
R_"a[140:143], v28 offset:6144\n" \
"s_and_b32 s9, s6, 0xffff\n" \
M_"v[52:55], a[176:179], a[128:131], v[52:55]\n" \
R_"a[204:207], v29 offset:6144\n" \
"s_lshl_b32 s6, s5, 16\n" \
M_"v[52:55], a[240:243], a[192:195], v[52:55]\n" \
"s_movk_i32 s84, 0x4400\n" \
M_"v[56:59], a[180:183], a[128:131], v[56:59]\n" \
B_"v24, s[76:79], s84 offen offset:2048 lds\n" \
M_"v[56:59], a[244:247], a[192:195], v[56:59]\n" \
"s_movk_i32 s84, 0x6400\n" \
"s_nop 0\n" \
M_"v[60:63], a[184:187], a[128:131], v[60:63]\n" \
B_"v25, s[76:79], s84 offen offset:3072 lds\n" \
M_"v[60:63], a[248:251], a[192:195], v[60:63]\n" \
X_"a[234:235], v34 offset:4608\n" \
"s_sub_i32 s10, 0x100000, s6\n" \
M_"v[64:67], a[188:191], a[128:131], v[64:67]\n" \
"s_mov_b32 s6, 0x8000\n" \
M_"v[64:67], a[252:255], a[192:195], v[64:67]\n" \
R_"a[128:131], v28 offset:0\n" \
M_"v[80:83], a[188:191], a[132:135], v[80:83]\n" \
R_"a[192:195], v29 offset:0\n" \
M_"v[80:83], a[252:255], a[196:199], v[80:83]\n" \
"s_cmp_lg_u32 s5, 0\n" \
"s_cselect_b32 s17, s11, s6\n" \
M_"v[76:79], a[184:187], a[132:135], v[76:79]\n" \
"s_lshl_b32 s6, s3, 1\n" \
"s_lshl_b32 s5, s5, 3\n" \
M_"v[76:79], a[248:251], a[196:199], v[76:79]\n" \
"s_and_b32 s6, s6, 4\n" \
"s_or_b32 s12, s5, s6\n" \
M_"v[72:75], a[180:183], a[132:135], v[72:75]\n" \
"s_sub_i32 s5, s13, s5\n" \
"s_or_b32 s5, s5, s6\n" \
M_"v[72:75], a[244:247], a[196:199], v[72:75]\n" \
"s_or_b32 s6, s12, s13\n" \
"s_lshl_b32 s4, s4, 2\n" \
M_"v[68:71], a[176:179], a[132:135], v[68:71]\n" \
"s_mulk_i32 s6, 0x92\n" \
"s_add_i32 s4, s4, 18\n" \
M_"v[68:71], a[240:243], a[196:199], v[68:71]\n" \
R_"a[132:135], v28 offset:2048\n" \
R_"a[196:199], v29 offset:2048\n" \
"s_waitcnt lgkmcnt(15)\n" \
M_"a[0:3], a[160:163], a[144:147], a[0:3]\n" \
"s_add_u32 m0, s30, 0x21000\n" \
"s_addk_i32 s84, 0x3000\n" \
M_"a[0:3], a[224:227], a[208:211], a[0:3]\n" \
B_"v24, s[76:79], s84 offen offset:0 lds\n" \
M_"a[4:7], a[164:167], a[144:147], a[4:7]\n" \
X_"a[176:177], v36 offset:0\n" \
"s_mulk_i32 s5, 0x92\n" \
M_"a[4:7], a[228:231], a[208:211], a[4:7]\n" \
X_"a[178:179], v36 offset:1024\n" \
"s_add_i32 s6, s4, s6\n" \
"s_waitcnt lgkmcnt(15)\n" \
M_"a[12:15], a[172:175], a[144:147], a[12:15]\n" \
X_"a[240:241], v36 offset:8192\n" \
"s_add_i32 s5, s5, s4\n" \
"s_waitcnt lgkmcnt(15)\n" \
M_"a[12:15], a[236:239], a[208:211], a[12:15]\n" \
"s_add_u32 s88, s42, 0x0\n" \
"buffer_store_dwordx4 v[196:199], v20, s[80:83], s88 offen offset:3072 sc1\n" \
"s_waitcnt lgkmcnt(11)\n" \
M_"a[8:11], a[144:147], a[168:171], a[8:11]\n" \
X_"a[242:243], v36 offset:9216\n" \
"s_lshl_b32 s4, s3, 11\n" \
"s_waitcnt lgkmcnt(8)\n" \
M_"a[8:11], a[208:211], a[232:235], a[8:11]\n" \
X_"a[180:181], v37 offset:0\n" \
"s_lshl_b32 s15, s13, 12\n" \
M_"a[24:27], a[148:151], a[168:171], a[24:27]\n" \
X_"a[182:183], v37 offset:1024\n" \
"s_lshl_b32 s16, s19, 14\n" \
M_"a[24:27], a[212:215], a[232:235], a[24:27]\n" \
X_"a[244:245], v37 offset:8192\n" \
"s_mul_i32 s19, s19, 0xe000\n" \
M_"a[28:31], a[172:175], a[148:151], a[28:31]\n" \
"s_mov_b32 s11, 0x20000\n" \
"s_or_b32 s12, s4, 0xc000\n" \
M_"a[28:31], a[236:239], a[212:215], a[28:31]\n" \
"s_addk_i32 s84, 0x2000\n" \
M_"a[20:23], a[164:167], a[148:151], a[20:23]\n" \
B_"v25, s[76:79], s84 offen offset:1024 lds\n" \
M_"a[20:23], a[228:231], a[212:215], a[20:23]\n" \
X_"a[246:247], v37 offset:9216\n" \
"s_lshl_b32 s4, s3, 8\n" \
M_"a[16:19], a[160:163], a[148:151], a[16:19]\n" \
X_"a[184:185], v38 offset:0\n" \
"s_or_b32 s22, s15, s16\n" \
M_"a[16:19], a[224:227], a[212:215], a[16:19]\n" \
X_"a[186:187], v38 offset:1024\n" \
"s_or_b32 s23, s19, s15\n" \
M_"a[32:35], a[160:163], a[152:155], a[32:35]\n" \
"s_addk_i32 s84, 0x2000\n" \
M_"a[32:35], a[224:227], a[216:219], a[32:35]\n" \
B_"v24, s[76:79], s84 offen offset:2048 lds\n" \
M_"a[36:39], a[164:167], a[152:155], a[36:39]\n" \
X_"a[248:249], v38 offset:8192\n" \
"s_lshl_b32 s60, s6, 10\n" \
M_"a[36:39], a[228:231], a[216:219], a[36:39]\n" \
X_"a[250:251], v38 offset:9216\n" \
"s_lshl_b32 s61, s5, 10\n" \
M_"a[44:47], a[172:175], a[152:155], a[44:47]\n" \
X_"a[188:189], v39 offset:0\n" \
"s_lshl_b32 s14, s14, 15\n" \
"s_nop 0\n" \
M_"a[44:47], a[236:239], a[216:219], a[44:47]\n" \
X_"a[190:191], v39 offset:1024\n" \
"s_or_b32 s20, s4, 0x26c00\n" \
M_"a[40:43], a[152:155], a[168:171], a[40:43]\n" \
"s_add_u32 s89, s42, 0x2000\n" \
"buffer_store_dwordx4 v[200:203], v20, s[80:83], s89 offen offset:0 sc1\n" \
M_"a[40:43], a[216:219], a[232:235], a[40:43]\n" \
X_"a[252:253], v39 offset:8192\n" \
"s_or_b32 s13, s22, 0x8000\n" \
M_"a[56:59], a[156:159], a[168:171], a[56:59]\n" \
X_"a[254:255], v39 offset:9216\n" \
"s_add_i32 s22, s22, 0x14000\n" \
".fill (L_pb + 73440 - .) / 4, 4, 0xBF800000\n" \
"L_pp18:\n" \
"s_cbranch_execz L_pret\n" \
M_"a[56:59], a[220:223], a[232:235], a[56:59]\n" \
"s_bitset1_b32 s23, 16\n" \
"s_or_b32 s24, s7, 0x5be8020\n" \
M_"a[60:63], a[172:175], a[156:159], a[60:63]\n" \
"s_and_b32 s57, s27, 0xffff\n" \
"s_mov_b32 s58, 0x6000000\n" \
M_"a[60:63], a[236:239], a[220:223], a[60:63]\n" \
"s_addk_i32 s84, 0x2000\n" \
M_"a[52:55], a[164:167], a[156:159], a[52:55]\n" \
B_"v25, s[76:79], s84 offen offset:3072 lds\n" \
M_"a[52:55], a[228:231], a[220:223], a[52:55]\n" \
"s_mov_b32 s56, s26\n" \
"s_mov_b32 s59, s11\n" \
M_"a[48:51], a[160:163], a[156:159], a[48:51]\n" \
"s_add_i32 s60, s60, 0x3000000\n" \
"s_add_i32 s61, s61, 0x411c000\n" \
M_"a[48:51], a[224:227], a[220:223], a[48:51]\n" \
"s_sub_i32 s19, 14, s18\n" \
"s_mov_b32 s21, 0x71800000\n" \
M_"a[64:67], a[164:167], a[140:143], a[64:67]\n" \
"s_mov_b32 s4, L_gsw-L_gtr\n" \
M_"a[64:67], a[228:231], a[204:207], a[64:67]\n" \
"s_cmp_eq_u32 s19, 0\n" \
"s_cselect_b32 s4, L_gs7-L_gtr, s4\n" \
M_"a[80:83], a[172:175], a[140:143], a[80:83]\n" \
"s_cmp_eq_u32 s16, 0\n" \
"s_cselect_b32 s4, 0, s4\n" \
M_"a[80:83], a[236:239], a[204:207], a[80:83]\n" \
"s_getpc_b64 s[28:29]\n" \
"L_gp1_%=:\n" \
"s_add_u32 s28, s28, L_gtr-L_gp1_%=\n" \
"s_addc_u32 s29, s29, 0\n" \
M_"a[68:71], a[140:143], a[168:171], a[68:71]\n" \
"s_add_u32 s28, s28, s4\n" \
"s_addc_u32 s29, s29, 0\n" \
"s_waitcnt vmcnt(11) lgkmcnt(0)\n" \
M_"a[68:71], a[204:207], a[232:235], a[68:71]\n" \
"s_barrier\n" \
M_"v[116:119], a[176:179], a[144:147], v[116:119]\n" \
"s_add_u32 m0, s40, 0x0\n" \
"s_add_u32 s85, s39, 0x10000\n" \
M_"v[116:119], a[240:243], a[208:211], v[116:119]\n" \
B_"v20, s[80:83], s85 offen sc1 lds\n" \
M_"v[120:123], a[180:183], a[144:147], v[120:123]\n" \
X_"a[160:161], v32 offset:32768\n" \
"v_accvgpr_write_b32 a96, 0\n" \
M_"v[120:123], a[244:247], a[208:211], v[120:123]\n" \
X_"a[162:163], v32 offset:33280\n" \
"v_accvgpr_write_b32 a97, 0\n" \
M_"v[124:127], a[184:187], a[144:147], v[124:127]\n" \
"v_accvgpr_write_b32 a98, 0\n" \
"v_accvgpr_write_b32 a99, 0\n" \
M_"v[124:127], a[248:251], a[208:211], v[124:127]\n" \
"v_accvgpr_write_b32 a100, 0\n" \
"v_accvgpr_write_b32 a101, 0\n" \
M_"v[128:131], a[188:191], a[144:147], v[128:131]\n" \
X_"a[224:225], v32 offset:36864\n" \
"v_accvgpr_write_b32 a102, 0\n" \
M_"v[128:131], a[252:255], a[208:211], v[128:131]\n" \
R_"a[144:147], v28 offset:40960\n" \
"v_accvgpr_write_b32 a103, 0\n" \
M_"v[144:147], a[188:191], a[148:151], v[144:147]\n" \
R_"a[208:211], v29 offset:40960\n" \
"v_accvgpr_write_b32 a112, 0\n" \
M_"v[144:147], a[252:255], a[212:215], v[144:147]\n" \
"s_add_u32 m0, s40, 0x1000\n" \
"s_add_u32 s85, s39, 0x11000\n" \
M_"v[140:143], a[184:187], a[148:151], v[140:143]\n" \
B_"v20, s[80:83], s85 offen sc1 lds\n" \
M_"v[140:143], a[248:251], a[212:215], v[140:143]\n" \
X_"a[226:227], v32 offset:37376\n" \
"v_accvgpr_write_b32 a113, 0\n" \
M_"v[136:139], a[180:183], a[148:151], v[136:139]\n" \
"v_accvgpr_write_b32 a114, 0\n" \
"v_accvgpr_write_b32 a115, 0\n" \
M_"v[136:139], a[244:247], a[212:215], v[136:139]\n" \
X_"a[164:165], v33 offset:32768\n" \
"v_accvgpr_write_b32 a120, 0\n" \
M_"v[132:135], a[176:179], a[148:151], v[132:135]\n" \
"v_accvgpr_write_b32 a121, 0\n" \
"v_accvgpr_write_b32 a122, 0\n" \
M_"v[132:135], a[240:243], a[212:215], v[132:135]\n" \
R_"a[148:151], v28 offset:43008\n" \
"v_accvgpr_write_b32 a123, 0\n" \
M_"v[148:151], a[176:179], a[152:155], v[148:151]\n" \
R_"a[212:215], v29 offset:43008\n" \
"v_mov_b32_e32 v180, 0x3f803f80\n" \
M_"v[148:151], a[240:243], a[216:219], v[148:151]\n" \
"s_add_u32 m0, s40, 0x3000\n" \
"s_movk_i32 s84, 0x400\n" \
M_"v[152:155], a[180:183], a[152:155], v[152:155]\n" \
B_"v21, s[76:79], s84 offen offset:0 lds\n" \
M_"v[152:155], a[244:247], a[216:219], v[152:155]\n" \
X_"a[166:167], v33 offset:33280\n" \
"s_mov_b32 s6, 0x33333333\n" \
M_"v[156:159], a[184:187], a[152:155], v[156:159]\n" \
X_"a[228:229], v33 offset:36864\n" \
"s_mov_b32 s7, 0xcccccccc\n" \
M_"v[156:159], a[248:251], a[216:219], v[156:159]\n" \
"s_add_u32 m0, s31, 0x0\n" \
"s_mov_b32 s84, 0x400000\n" \
M_"v[160:163], a[188:191], a[152:155], v[160:163]\n" \
B_"v26, s[68:71], s84 offen offset:0 lds\n" \
M_"v[160:163], a[252:255], a[216:219], v[160:163]\n" \
R_"a[152:155], v28 offset:45056\n" \
"v_cndmask_b32_e64 v181, 0, v180, s[6:7]\n" \
M_"v[176:179], a[188:191], a[156:159], v[176:179]\n" \
R_"a[216:219], v29 offset:45056\n" \
"v_accvgpr_write_b32 a104, v181\n" \
M_"v[176:179], a[252:255], a[220:223], v[176:179]\n" \
X_"a[230:231], v33 offset:37376\n" \
"v_accvgpr_write_b32 a105, v181\n" \
M_"v[172:175], a[184:187], a[156:159], v[172:175]\n" \
X_"a[172:173], v35 offset:32768\n" \
"v_accvgpr_write_b32 a106, v181\n" \
M_"v[172:175], a[248:251], a[220:223], v[172:175]\n" \
"v_accvgpr_write_b32 a107, v181\n" \
"s_mov_b32 s6, 0x11111111\n" \
M_"v[168:171], a[180:183], a[156:159], v[168:171]\n" \
"s_add_u32 s88, s42, 0x2000\n" \
"buffer_store_dwordx4 v[204:207], v20, s[80:83], s88 offen offset:1024 sc1\n" \
M_"v[168:171], a[244:247], a[220:223], v[168:171]\n" \
X_"a[174:175], v35 offset:33280\n" \
"s_mov_b32 s7, 0x11111111\n" \
M_"v[164:167], a[176:179], a[156:159], v[164:167]\n" \
"v_cndmask_b32_e64 v181, 0, v180, s[6:7]\n" \
"v_accvgpr_write_b32 a108, v181\n" \
M_"v[164:167], a[240:243], a[220:223], v[164:167]\n" \
R_"a[156:159], v28 offset:47104\n" \
"v_accvgpr_write_b32 a109, v181\n" \
M_"v[84:87], a[176:179], a[136:139], v[84:87]\n" \
R_"a[220:223], v29 offset:47104\n" \
"v_accvgpr_write_b32 a110, v181\n" \
M_"v[84:87], a[240:243], a[200:203], v[84:87]\n" \
X_"a[236:237], v35 offset:36864\n" \
"v_accvgpr_write_b32 a111, v181\n" \
M_"v[88:91], a[180:183], a[136:139], v[88:91]\n" \
"s_mov_b32 s6, 0x44444444\n" \
"s_mov_b32 s7, 0x44444444\n" \
M_"v[88:91], a[244:247], a[200:203], v[88:91]\n" \
"s_addk_i32 s84, 0x7c00\n" \
"s_nop 0\n" \
M_"v[92:95], a[184:187], a[136:139], v[92:95]\n" \
B_"v26, s[68:71], s84 offen offset:1024 lds\n" \
M_"v[92:95], a[248:251], a[200:203], v[92:95]\n" \
X_"a[238:239], v35 offset:37376\n" \
"v_cndmask_b32_e64 v181, 0, v180, s[6:7]\n" \
M_"v[96:99], a[188:191], a[136:139], v[96:99]\n" \
"v_accvgpr_write_b32 a116, v181\n" \
"v_accvgpr_write_b32 a117, v181\n" \
M_"v[96:99], a[252:255], a[200:203], v[96:99]\n" \
R_"a[136:139], v28 offset:36864\n" \
"v_accvgpr_write_b32 a118, v181\n" \
M_"v[112:115], a[188:191], a[140:143], v[112:115]\n" \
R_"a[200:203], v29 offset:36864\n" \
"v_accvgpr_write_b32 a119, v181\n" \
M_"v[112:115], a[252:255], a[204:207], v[112:115]\n" \
X_"a[168:169], v34 offset:32768\n" \
"v_bfe_i32 v181, v20, 4, 1\n" \
M_"v[108:111], a[184:187], a[140:143], v[108:111]\n" \
"s_addk_i32 s84, 0x7c00\n" \
"s_nop 0\n" \
M_"v[108:111], a[248:251], a[204:207], v[108:111]\n" \
B_"v27, s[68:71], s84 offen offset:2048 lds\n" \
M_"v[104:107], a[180:183], a[140:143], v[104:107]\n" \
X_"a[170:171], v34 offset:33280\n" \
"v_and_b32_e32 v181, 0xaaaaaaaa, v181\n" \
M_"v[104:107], a[244:247], a[204:207], v[104:107]\n" \
X_"a[232:233], v34 offset:36864\n" \
"v_or_b32_e32 v181, 0x44444444, v181\n" \
M_"v[100:103], a[176:179], a[140:143], v[100:103]\n" \
"v_accvgpr_write_b32 a126, v181\n" \
"v_bfe_u32 v180, v20, 5, 3\n" \
M_"v[100:103], a[240:243], a[204:207], v[100:103]\n" \
R_"a[140:143], v28 offset:38912\n" \
"v_bfe_u32 v181, v20, 8, 2\n" \
M_"v[52:55], a[176:179], a[128:131], v[52:55]\n" \
R_"a[204:207], v29 offset:38912\n" \
"v_lshlrev_b32_e64 v181, 1, v181\n" \
M_"v[52:55], a[240:243], a[192:195], v[52:55]\n" \
"s_addk_i32 s84, 0x7c00\n" \
M_"v[56:59], a[180:183], a[128:131], v[56:59]\n" \
B_"v27, s[68:71], s84 offen offset:3072 lds\n" \
M_"v[56:59], a[244:247], a[192:195], v[56:59]\n" \
X_"a[234:235], v34 offset:37376\n" \
"v_sub_u32_e32 v180, v180, v181\n" \
M_"v[60:63], a[184:187], a[128:131], v[60:63]\n" \
"v_and_b32_e64 v181, 16, v20\n" \
"v_mov_b32_e32 v182, 0x3f80\n" \
M_"v[60:63], a[248:251], a[192:195], v[60:63]\n" \
"v_lshlrev_b32_e32 v181, v181, v182\n" \
"v_cmp_eq_u32_e32 vcc, 0, v180\n" \
M_"v[64:67], a[188:191], a[128:131], v[64:67]\n" \
"v_cmp_eq_u32_e64 s[6:7], 1, v180\n" \
"v_cndmask_b32_e64 v182, 0, v181, vcc\n" \
M_"v[64:67], a[252:255], a[192:195], v[64:67]\n" \
R_"a[128:131], v28 offset:32768\n" \
"v_cndmask_b32_e64 v183, 0, v181, s[6:7]\n" \
M_"v[80:83], a[188:191], a[132:135], v[80:83]\n" \
R_"a[192:195], v29 offset:32768\n" \
"v_accvgpr_write_b32 a124, v182\n" \
M_"v[80:83], a[252:255], a[196:199], v[80:83]\n" \
"v_accvgpr_write_b32 a125, v183\n" \
M_"v[76:79], a[184:187], a[132:135], v[76:79]\n" \
M_"v[76:79], a[248:251], a[196:199], v[76:79]\n" \
M_"v[72:75], a[180:183], a[132:135], v[72:75]\n" \
M_"v[72:75], a[244:247], a[196:199], v[72:75]\n" \
M_"v[68:71], a[176:179], a[132:135], v[68:71]\n" \
M_"v[68:71], a[240:243], a[196:199], v[68:71]\n" \
R_"a[132:135], v28 offset:34816\n" \
R_"a[196:199], v29 offset:34816\n" \
"s_waitcnt lgkmcnt(15)\n" \
"s_nop 0\n" \
M_"a[0:3], a[160:163], a[144:147], a[0:3]\n" \
"s_add_u32 s89, s42, 0x2000\n" \
"buffer_store_dwordx4 v[208:211], v20, s[80:83], s89 offen offset:2048 sc1\n" \
M_"a[0:3], a[224:227], a[208:211], a[0:3]\n" \
X_"a[176:177], v36 offset:32768\n" \
M_"a[4:7], a[164:167], a[144:147], a[4:7]\n" \
X_"a[178:179], v36 offset:33792\n" \
M_"a[4:7], a[228:231], a[208:211], a[4:7]\n" \
X_"a[240:241], v36 offset:40960\n" \
M_"a[12:15], a[172:175], a[144:147], a[12:15]\n" \
X_"a[242:243], v36 offset:41984\n" \
"s_waitcnt lgkmcnt(15)\n" \
M_"a[12:15], a[236:239], a[208:211], a[12:15]\n" \
"s_waitcnt lgkmcnt(12)\n" \
M_"a[8:11], a[144:147], a[168:171], a[8:11]\n" \
X_"a[180:181], v37 offset:32768\n" \
"s_waitcnt lgkmcnt(9)\n" \
"s_nop 0\n" \
M_"a[8:11], a[208:211], a[232:235], a[8:11]\n" \
M_"a[24:27], a[148:151], a[168:171], a[24:27]\n" \
X_"a[182:183], v37 offset:33792\n" \
M_"a[24:27], a[212:215], a[232:235], a[24:27]\n" \
X_"a[244:245], v37 offset:40960\n" \
M_"a[28:31], a[172:175], a[148:151], a[28:31]\n" \
M_"a[28:31], a[236:239], a[212:215], a[28:31]\n" \
X_"a[246:247], v37 offset:41984\n" \
M_"a[20:23], a[164:167], a[148:151], a[20:23]\n" \
M_"a[20:23], a[228:231], a[212:215], a[20:23]\n" \
X_"a[184:185], v38 offset:32768\n" \
M_"a[16:19], a[160:163], a[148:151], a[16:19]\n" \
X_"a[186:187], v38 offset:33792\n" \
M_"a[16:19], a[224:227], a[212:215], a[16:19]\n" \
M_"a[32:35], a[160:163], a[152:155], a[32:35]\n" \
"s_add_u32 s88, s42, 0x2000\n" \
"buffer_store_dwordx4 v[212:215], v20, s[80:83], s88 offen offset:3072 sc1\n" \
M_"a[32:35], a[224:227], a[216:219], a[32:35]\n" \
X_"a[248:249], v38 offset:40960\n" \
M_"a[36:39], a[164:167], a[152:155], a[36:39]\n" \
"v_lshlrev_b32_e32 v45, 3, v20\n" \
"v_add_u32_e32 v45, s30, v45\n" \
M_"a[36:39], a[228:231], a[216:219], a[36:39]\n" \
"s_getpc_b64 s[86:87]\n" \
"L_ct1_%=:\n" \
"s_add_u32 s86, s86, L_ap0-L_ct1_%=\n" \
"s_addc_u32 s87, s87, 0\n" \
"s_add_u32 m0, s40, 0x2000\n" \
M_"a[44:47], a[172:175], a[152:155], a[44:47]\n" \
"global_load_lds_dword v45, s[86:87]\n" \
M_"a[44:47], a[236:239], a[216:219], a[44:47]\n" \
X_"a[250:251], v38 offset:41984\n" \
M_"a[40:43], a[152:155], a[168:171], a[40:43]\n" \
X_"a[188:189], v39 offset:32768\n" \
M_"a[40:43], a[216:219], a[232:235], a[40:43]\n" \
X_"a[190:191], v39 offset:33792\n" \
M_"a[56:59], a[156:159], a[168:171], a[56:59]\n" \
X_"a[252:253], v39 offset:40960\n" \
M_"a[56:59], a[220:223], a[232:235], a[56:59]\n" \
X_"a[254:255], v39 offset:41984\n" \
M_"a[60:63], a[172:175], a[156:159], a[60:63]\n" \
M_"a[60:63], a[236:239], a[220:223], a[60:63]\n" \
M_"a[52:55], a[164:167], a[156:159], a[52:55]\n" \
M_"a[52:55], a[228:231], a[220:223], a[52:55]\n" \
M_"a[48:51], a[160:163], a[156:159], a[48:51]\n" \
M_"a[48:51], a[224:227], a[220:223], a[48:51]\n" \
M_"a[64:67], a[164:167], a[140:143], a[64:67]\n" \
M_"a[64:67], a[228:231], a[204:207], a[64:67]\n" \
M_"a[80:83], a[172:175], a[140:143], a[80:83]\n" \
M_"a[80:83], a[236:239], a[204:207], a[80:83]\n" \
M_"a[68:71], a[140:143], a[168:171], a[68:71]\n" \
"s_waitcnt vmcnt(3) lgkmcnt(0)\n" \
M_"a[68:71], a[204:207], a[232:235], a[68:71]\n" \
"s_barrier\n" \
"v_xor_b32_e32 v28, 0x20000, v28\n" \
"v_xor_b32_e32 v29, 0x20000, v29\n" \
M_"v[116:119], a[176:179], a[144:147], v[116:119]\n" \
X_"a[160:161], v32 offset:0\n" \
"v_accvgpr_read_b32 v184, a0\n" \
M_"v[116:119], a[240:243], a[208:211], v[116:119]\n" \
"v_accvgpr_read_b32 v185, a1\n" \
"v_accvgpr_read_b32 v186, a2\n" \
M_"v[120:123], a[180:183], a[144:147], v[120:123]\n" \
"v_accvgpr_read_b32 v187, a3\n" \
"v_accvgpr_read_b32 v188, a4\n" \
M_"v[120:123], a[244:247], a[208:211], v[120:123]\n" \
X_"a[162:163], v32 offset:512\n" \
"v_accvgpr_read_b32 v189, a5\n" \
M_"v[124:127], a[184:187], a[144:147], v[124:127]\n" \
"v_accvgpr_read_b32 v190, a6\n" \
"v_accvgpr_read_b32 v191, a7\n" \
M_"v[124:127], a[248:251], a[208:211], v[124:127]\n" \
"v_accvgpr_read_b32 v196, a12\n" \
"v_accvgpr_read_b32 v197, a13\n" \
M_"v[128:131], a[188:191], a[144:147], v[128:131]\n" \
X_"a[224:225], v32 offset:4096\n" \
"v_accvgpr_read_b32 v198, a14\n" \
M_"v[128:131], a[252:255], a[208:211], v[128:131]\n" \
R_"a[144:147], v28 offset:8192\n" \
"v_accvgpr_read_b32 v199, a15\n" \
M_"v[144:147], a[188:191], a[148:151], v[144:147]\n" \
R_"a[208:211], v29 offset:8192\n" \
"v_accvgpr_read_b32 v192, a8\n" \
M_"v[144:147], a[252:255], a[212:215], v[144:147]\n" \
X_"a[226:227], v32 offset:4608\n" \
"v_accvgpr_read_b32 v193, a9\n" \
M_"v[140:143], a[184:187], a[148:151], v[140:143]\n" \
"v_accvgpr_read_b32 v194, a10\n" \
"v_accvgpr_read_b32 v195, a11\n" \
M_"v[140:143], a[248:251], a[212:215], v[140:143]\n" \
"v_accvgpr_read_b32 v208, a24\n" \
"v_accvgpr_read_b32 v209, a25\n" \
M_"v[136:139], a[180:183], a[148:151], v[136:139]\n" \
"v_accvgpr_read_b32 v210, a26\n" \
"v_accvgpr_read_b32 v211, a27\n" \
M_"v[136:139], a[244:247], a[212:215], v[136:139]\n" \
X_"a[164:165], v33 offset:0\n" \
"v_accvgpr_read_b32 v212, a28\n" \
M_"v[132:135], a[176:179], a[148:151], v[132:135]\n" \
"v_accvgpr_read_b32 v213, a29\n" \
"v_accvgpr_read_b32 v214, a30\n" \
M_"v[132:135], a[240:243], a[212:215], v[132:135]\n" \
R_"a[148:151], v28 offset:10240\n" \
"v_accvgpr_read_b32 v215, a31\n" \
M_"v[148:151], a[176:179], a[152:155], v[148:151]\n" \
R_"a[212:215], v29 offset:10240\n" \
"v_accvgpr_read_b32 v204, a20\n" \
M_"v[148:151], a[240:243], a[216:219], v[148:151]\n" \
X_"a[166:167], v33 offset:512\n" \
"v_accvgpr_read_b32 v205, a21\n" \
M_"v[152:155], a[180:183], a[152:155], v[152:155]\n" \
"v_accvgpr_read_b32 v206, a22\n" \
"v_accvgpr_read_b32 v207, a23\n" \
M_"v[152:155], a[244:247], a[216:219], v[152:155]\n" \
X_"a[228:229], v33 offset:4096\n" \
"v_accvgpr_read_b32 v200, a16\n" \
M_"v[156:159], a[184:187], a[152:155], v[156:159]\n" \
"v_accvgpr_read_b32 v201, a17\n" \
"v_accvgpr_read_b32 v202, a18\n" \
M_"v[156:159], a[248:251], a[216:219], v[156:159]\n" \
"v_accvgpr_read_b32 v203, a19\n" \
"v_accvgpr_read_b32 v216, a32\n" \
M_"v[160:163], a[188:191], a[152:155], v[160:163]\n" \
X_"a[230:231], v33 offset:4608\n" \
"v_accvgpr_read_b32 v217, a33\n" \
M_"v[160:163], a[252:255], a[216:219], v[160:163]\n" \
R_"a[152:155], v28 offset:12288\n" \
"v_accvgpr_read_b32 v218, a34\n" \
M_"v[176:179], a[188:191], a[156:159], v[176:179]\n" \
R_"a[216:219], v29 offset:12288\n" \
"v_accvgpr_read_b32 v219, a35\n" \
M_"v[176:179], a[252:255], a[220:223], v[176:179]\n" \
"v_accvgpr_read_b32 v220, a36\n" \
"v_accvgpr_read_b32 v221, a37\n" \
M_"v[172:175], a[184:187], a[156:159], v[172:175]\n" \
X_"a[172:173], v35 offset:0\n" \
"v_accvgpr_read_b32 v222, a38\n" \
M_"v[172:175], a[248:251], a[220:223], v[172:175]\n" \
"v_accvgpr_read_b32 v223, a39\n" \
"v_accvgpr_read_b32 v228, a44\n" \
M_"v[168:171], a[180:183], a[156:159], v[168:171]\n" \
"v_accvgpr_read_b32 v229, a45\n" \
"v_accvgpr_read_b32 v230, a46\n" \
M_"v[168:171], a[244:247], a[220:223], v[168:171]\n" \
X_"a[174:175], v35 offset:512\n" \
"v_accvgpr_read_b32 v231, a47\n" \
M_"v[164:167], a[176:179], a[156:159], v[164:167]\n" \
"v_accvgpr_read_b32 v224, a40\n" \
"v_accvgpr_read_b32 v225, a41\n" \
M_"v[164:167], a[240:243], a[220:223], v[164:167]\n" \
R_"a[156:159], v28 offset:14336\n" \
"v_accvgpr_read_b32 v226, a42\n" \
M_"v[84:87], a[176:179], a[136:139], v[84:87]\n" \
R_"a[220:223], v29 offset:14336\n" \
"v_accvgpr_read_b32 v227, a43\n" \
M_"v[84:87], a[240:243], a[200:203], v[84:87]\n" \
X_"a[236:237], v35 offset:4096\n" \
"v_accvgpr_read_b32 v240, a56\n" \
M_"v[88:91], a[180:183], a[136:139], v[88:91]\n" \
"v_accvgpr_read_b32 v241, a57\n" \
"v_accvgpr_read_b32 v242, a58\n" \
M_"v[88:91], a[244:247], a[200:203], v[88:91]\n" \
X_"a[238:239], v35 offset:4608\n" \
"v_accvgpr_read_b32 v243, a59\n" \
M_"v[92:95], a[184:187], a[136:139], v[92:95]\n" \
"v_accvgpr_read_b32 v244, a60\n" \
"v_accvgpr_read_b32 v245, a61\n" \
M_"v[92:95], a[248:251], a[200:203], v[92:95]\n" \
"v_accvgpr_read_b32 v246, a62\n" \
"v_accvgpr_read_b32 v247, a63\n" \
M_"v[96:99], a[188:191], a[136:139], v[96:99]\n" \
"v_accvgpr_read_b32 v236, a52\n" \
"v_accvgpr_read_b32 v237, a53\n" \
M_"v[96:99], a[252:255], a[200:203], v[96:99]\n" \
R_"a[136:139], v28 offset:4096\n" \
"v_accvgpr_read_b32 v238, a54\n" \
M_"v[112:115], a[188:191], a[140:143], v[112:115]\n" \
R_"a[200:203], v29 offset:4096\n" \
"v_accvgpr_read_b32 v239, a55\n" \
M_"v[112:115], a[252:255], a[204:207], v[112:115]\n" \
X_"a[168:169], v34 offset:0\n" \
"v_accvgpr_read_b32 v232, a48\n" \
M_"v[108:111], a[184:187], a[140:143], v[108:111]\n" \
X_"a[170:171], v34 offset:512\n" \
"v_accvgpr_read_b32 v233, a49\n" \
M_"v[108:111], a[248:251], a[204:207], v[108:111]\n" \
"v_accvgpr_read_b32 v234, a50\n" \
"v_accvgpr_read_b32 v235, a51\n" \
M_"v[104:107], a[180:183], a[140:143], v[104:107]\n" \
"v_accvgpr_read_b32 v180, a64\n" \
"v_accvgpr_read_b32 v181, a65\n" \
M_"v[104:107], a[244:247], a[204:207], v[104:107]\n" \
X_"a[232:233], v34 offset:4096\n" \
"v_accvgpr_read_b32 v182, a66\n" \
M_"v[100:103], a[176:179], a[140:143], v[100:103]\n" \
"v_accvgpr_read_b32 v183, a67\n" \
"v_accvgpr_read_b32 v48, a80\n" \
M_"v[100:103], a[240:243], a[204:207], v[100:103]\n" \
R_"a[140:143], v28 offset:6144\n" \
"v_accvgpr_read_b32 v49, a81\n" \
M_"v[52:55], a[176:179], a[128:131], v[52:55]\n" \
R_"a[204:207], v29 offset:6144\n" \
"v_accvgpr_read_b32 v50, a82\n" \
M_"v[52:55], a[240:243], a[192:195], v[52:55]\n" \
X_"a[234:235], v34 offset:4608\n" \
"v_accvgpr_read_b32 v51, a83\n" \
M_"v[56:59], a[180:183], a[128:131], v[56:59]\n" \
"v_accvgpr_read_b32 v252, a68\n" \
"v_accvgpr_read_b32 v253, a69\n" \
M_"v[56:59], a[244:247], a[192:195], v[56:59]\n" \
"v_accvgpr_read_b32 v254, a70\n" \
"v_accvgpr_read_b32 v255, a71\n" \
M_"v[60:63], a[184:187], a[128:131], v[60:63]\n" \
"v_lshrrev_b32_e64 v24, 9, v20\n" \
"v_and_b32_e32 v26, 0x1ff, v20\n" \
M_"v[60:63], a[248:251], a[192:195], v[60:63]\n" \
"v_lshl_add_u32 v24, v24, 3, v26\n" \
"v_lshrrev_b32_e64 v25, 8, v20\n" \
M_"v[64:67], a[188:191], a[128:131], v[64:67]\n" \
"v_mul_u32_u24_e32 v25, 0x24700, v25\n" \
"v_add_u32_e64 v25, v25, v20\n" \
M_"v[64:67], a[252:255], a[192:195], v[64:67]\n" \
R_"a[128:131], v28 offset:0\n" \
"v_lshrrev_b32_e64 v26, 2, v20\n" \
M_"v[80:83], a[188:191], a[132:135], v[80:83]\n" \
R_"a[192:195], v29 offset:0\n" \
"v_add_u32_e64 v26, s45, v26\n" \
M_"v[80:83], a[252:255], a[196:199], v[80:83]\n" \
"v_add_u32_e32 v27, s46, v20\n" \
"v_add_u32_e32 v21, s43, v27\n" \
M_"v[76:79], a[184:187], a[132:135], v[76:79]\n" \
M_"v[76:79], a[248:251], a[196:199], v[76:79]\n" \
M_"v[72:75], a[180:183], a[132:135], v[72:75]\n" \
M_"v[72:75], a[244:247], a[196:199], v[72:75]\n" \
M_"v[68:71], a[176:179], a[132:135], v[68:71]\n" \
M_"v[68:71], a[240:243], a[196:199], v[68:71]\n" \
R_"a[132:135], v28 offset:2048\n" \
R_"a[196:199], v29 offset:2048\n" \
"s_waitcnt lgkmcnt(15)\n" \
"s_nop 0\n" \
M_"v[184:187], a[160:163], a[144:147], v[184:187]\n" \
"v_bfe_u32 v44, v20, 2, 5\n" \
X_"a[176:177], v36 offset:0\n" \
M_"v[184:187], a[224:227], a[208:211], v[184:187]\n" \
"buffer_load_dword v44, v44, s[80:83], s38 offen offset:0 sc1\n" \
M_"v[188:191], a[164:167], a[144:147], v[188:191]\n" \
X_"a[178:179], v36 offset:1024\n" \
M_"v[188:191], a[228:231], a[208:211], v[188:191]\n" \
X_"a[240:241], v36 offset:8192\n" \
M_"v[196:199], a[172:175], a[144:147], v[196:199]\n" \
X_"a[242:243], v36 offset:9216\n" \
"s_waitcnt lgkmcnt(15)\n" \
M_"v[196:199], a[236:239], a[208:211], v[196:199]\n" \
"s_waitcnt lgkmcnt(12)\n" \
M_"v[192:195], a[144:147], a[168:171], v[192:195]\n" \
X_"a[180:181], v37 offset:0\n" \
"s_waitcnt lgkmcnt(9)\n" \
"s_nop 0\n" \
M_"v[192:195], a[208:211], a[232:235], v[192:195]\n" \
M_"v[208:211], a[148:151], a[168:171], v[208:211]\n" \
X_"a[182:183], v37 offset:1024\n" \
M_"v[208:211], a[212:215], a[232:235], v[208:211]\n" \
X_"a[244:245], v37 offset:8192\n" \
M_"v[212:215], a[172:175], a[148:151], v[212:215]\n" \
M_"v[212:215], a[236:239], a[212:215], v[212:215]\n" \
X_"a[246:247], v37 offset:9216\n" \
C_"v188, v188, v189\n" \
M_"v[204:207], a[164:167], a[148:151], v[204:207]\n" \
C_"v189, v190, v191\n" \
C_"v190, v198, v199\n" \
M_"v[204:207], a[228:231], a[212:215], v[204:207]\n" \
X_"a[184:185], v38 offset:0\n" \
"v_mov_b32_e32 v191, 0x3f80\n" \
M_"v[200:203], a[160:163], a[148:151], v[200:203]\n" \
X_"a[186:187], v38 offset:1024\n" \
C_"v184, v184, v185\n" \
M_"v[200:203], a[224:227], a[212:215], v[200:203]\n" \
C_"v185, v186, v187\n" \
C_"v186, v196, v197\n" \
M_"v[216:219], a[160:163], a[152:155], v[216:219]\n" \
X_"a[248:249], v38 offset:8192\n" \
"v_mov_b32_e64 v187, 0\n" \
M_"v[216:219], a[224:227], a[216:219], v[216:219]\n" \
X_"a[250:251], v38 offset:9216\n" \
M_"v[220:223], a[164:167], a[152:155], v[220:223]\n" \
M_"v[220:223], a[228:231], a[216:219], v[220:223]\n" \
X_"a[188:189], v39 offset:0\n" \
C_"v204, v204, v205\n" \
M_"v[228:231], a[172:175], a[152:155], v[228:231]\n" \
C_"v205, v206, v207\n" \
C_"v206, v214, v215\n" \
M_"v[228:231], a[236:239], a[216:219], v[228:231]\n" \
X_"a[190:191], v39 offset:1024\n" \
"v_mov_b32_e32 v207, 0x3f80\n" \
M_"v[224:227], a[152:155], a[168:171], v[224:227]\n" \
X_"a[252:253], v39 offset:8192\n" \
C_"v200, v200, v201\n" \
M_"v[224:227], a[216:219], a[232:235], v[224:227]\n" \
C_"v201, v202, v203\n" \
C_"v202, v212, v213\n" \
M_"v[240:243], a[156:159], a[168:171], v[240:243]\n" \
X_"a[254:255], v39 offset:9216\n" \
"v_mov_b32_e64 v203, 0\n" \
M_"v[240:243], a[220:223], a[232:235], v[240:243]\n" \
M_"v[244:247], a[172:175], a[156:159], v[244:247]\n" \
".fill (L_pb + 77536 - .) / 4, 4, 0xBF800000\n" \
"L_pp19:\n" \
"s_cbranch_execz L_pret\n" \
"s_nop 0\n" \
M_"v[244:247], a[236:239], a[220:223], v[244:247]\n" \
C_"v220, v220, v221\n" \
C_"v221, v222, v223\n" \
M_"v[236:239], a[164:167], a[156:159], v[236:239]\n" \
C_"v222, v230, v231\n" \
"v_mov_b32_e32 v223, 0x3f80\n" \
M_"v[236:239], a[228:231], a[220:223], v[236:239]\n" \
C_"v216, v216, v217\n" \
C_"v217, v218, v219\n" \
M_"v[232:235], a[160:163], a[156:159], v[232:235]\n" \
C_"v218, v228, v229\n" \
"v_mov_b32_e64 v219, 0\n" \
M_"v[232:235], a[224:227], a[220:223], v[232:235]\n" \
M_"v[180:183], a[164:167], a[140:143], v[180:183]\n" \
M_"v[180:183], a[228:231], a[204:207], v[180:183]\n" \
M_"v[48:51], a[172:175], a[140:143], v[48:51]\n" \
M_"v[48:51], a[236:239], a[204:207], v[48:51]\n" \
C_"v236, v236, v237\n" \
C_"v237, v238, v239\n" \
M_"v[252:255], a[140:143], a[168:171], v[252:255]\n" \
C_"v238, v246, v247\n" \
"v_mov_b32_e32 v239, 0x3f80\n" \
M_"v[252:255], a[204:207], a[232:235], v[252:255]\n" \
C_"v232, v232, v233\n" \
C_"v233, v234, v235\n" \
"s_waitcnt vmcnt(0) lgkmcnt(0)\n" \
"s_barrier\n" \
"v_mov_b32_e32 v45, s62\n" \
"s_mov_b64 exec, 1\n" \
"buffer_store_dword v45, off, s[80:83], s37 offset:32 sc1\n" \
"s_mov_b64 exec, -1\n" \
"v_cmp_ne_u32_e32 vcc, s62, v44\n" \
"s_nop 4\n" \
"s_cbranch_vccnz L_poll161_%=\n" \
"L_pollr161_%=:\n" \
M_"v[116:119], a[176:179], a[144:147], v[116:119]\n" \
"s_mov_b32 s73, s50\n" \
"s_sub_u32 m0, s31, 0x10000\n" \
M_"v[116:119], a[240:243], a[208:211], v[116:119]\n" \
B_"v20, s[80:83], s73 offen offset:0 sc1 lds\n" \
M_"v[120:123], a[180:183], a[144:147], v[120:123]\n" \
"s_add_u32 s74, s50, 0x4000\n" \
"s_add_u32 s75, s50, 0x8000\n" \
M_"v[120:123], a[244:247], a[208:211], v[120:123]\n" \
"s_mov_b32 s70, 0x3e0293ee\n" \
"s_mov_b32 s76, s52\n" \
M_"v[124:127], a[184:187], a[144:147], v[124:127]\n" \
"v_mul_f32_e32 v116, s70, v116\n" \
"v_mul_f32_e32 v117, s70, v117\n" \
M_"v[124:127], a[248:251], a[208:211], v[124:127]\n" \
B_"v20, s[80:83], s73 offen offset:1024 sc1 lds\n" \
M_"v[128:131], a[188:191], a[144:147], v[128:131]\n" \
"v_mul_f32_e32 v118, s70, v118\n" \
"v_mul_f32_e32 v119, s70, v119\n" \
M_"v[128:131], a[252:255], a[208:211], v[128:131]\n" \
C_"v116, v116, v117\n" \
"v_mul_f32_e64 v120, s70, v120\n" \
M_"v[144:147], a[188:191], a[148:151], v[144:147]\n" \
"v_mul_f32_e32 v124, s70, v124\n" \
"v_mul_f32_e32 v125, s70, v125\n" \
M_"v[144:147], a[252:255], a[212:215], v[144:147]\n" \
B_"v20, s[80:83], s73 offen offset:2048 sc1 lds\n" \
M_"v[140:143], a[184:187], a[148:151], v[140:143]\n" \
"v_mul_f32_e32 v126, s70, v126\n" \
"v_mul_f32_e32 v127, s70, v127\n" \
M_"v[140:143], a[248:251], a[212:215], v[140:143]\n" \
C_"v124, v124, v125\n" \
"v_mul_f32_e64 v121, s70, v121\n" \
M_"v[136:139], a[180:183], a[148:151], v[136:139]\n" \
"v_mul_f32_e32 v122, s70, v122\n" \
"v_mul_f32_e32 v123, s70, v123\n" \
M_"v[136:139], a[244:247], a[212:215], v[136:139]\n" \
B_"v20, s[80:83], s73 offen offset:3072 sc1 lds\n" \
M_"v[132:135], a[176:179], a[148:151], v[132:135]\n" \
"s_sub_u32 m0, s31, s51\n" \
C_"v117, v118, v119\n" \
M_"v[132:135], a[240:243], a[212:215], v[132:135]\n" \
"v_mul_f32_e32 v128, s70, v128\n" \
"v_mul_f32_e64 v129, s70, v129\n" \
M_"v[148:151], a[176:179], a[152:155], v[148:151]\n" \
"v_mul_f32_e32 v130, s70, v130\n" \
"v_mul_f32_e32 v131, s70, v131\n" \
M_"v[148:151], a[240:243], a[216:219], v[148:151]\n" \
B_"v20, s[80:83], s74 offen offset:0 sc1 lds\n" \
M_"v[152:155], a[180:183], a[152:155], v[152:155]\n" \
C_"v125, v126, v127\n" \
C_"v118, v120, v121\n" \
M_"v[152:155], a[244:247], a[216:219], v[152:155]\n" \
C_"v119, v122, v123\n" \
C_"v126, v128, v129\n" \
M_"v[156:159], a[184:187], a[152:155], v[156:159]\n" \
C_"v127, v130, v131\n" \
"v_mul_f32_e64 v132, s70, v132\n" \
M_"v[156:159], a[248:251], a[216:219], v[156:159]\n" \
B_"v20, s[80:83], s74 offen offset:1024 sc1 lds\n" \
M_"v[160:163], a[188:191], a[152:155], v[160:163]\n" \
"ds_write_b128 v21, v[116:119] offset:16384\n" \
M_"v[160:163], a[252:255], a[216:219], v[160:163]\n" \
"v_mul_f32_e32 v133, s70, v133\n" \
"v_mul_f32_e32 v140, s70, v140\n" \
M_"v[176:179], a[188:191], a[156:159], v[176:179]\n" \
"v_mul_f32_e32 v141, s70, v141\n" \
"v_mul_f32_e32 v134, s70, v134\n" \
M_"v[176:179], a[252:255], a[220:223], v[176:179]\n" \
B_"v20, s[80:83], s74 offen offset:2048 sc1 lds\n" \
M_"v[172:175], a[184:187], a[156:159], v[172:175]\n" \
"ds_write_b128 v21, v[124:127] offset:17408\n" \
M_"v[172:175], a[248:251], a[220:223], v[172:175]\n" \
"v_mul_f32_e64 v135, s70, v135\n" \
C_"v132, v132, v133\n" \
M_"v[168:171], a[180:183], a[156:159], v[168:171]\n" \
"v_mul_f32_e32 v142, s70, v142\n" \
"v_mul_f32_e32 v143, s70, v143\n" \
M_"v[168:171], a[244:247], a[220:223], v[168:171]\n" \
B_"v20, s[80:83], s74 offen offset:3072 sc1 lds\n" \
M_"v[164:167], a[176:179], a[156:159], v[164:167]\n" \
"s_sub_u32 m0, s31, 0x4000\n" \
C_"v140, v140, v141\n" \
M_"v[164:167], a[240:243], a[220:223], v[164:167]\n" \
"v_mul_f32_e32 v136, s70, v136\n" \
"v_mul_f32_e32 v137, s70, v137\n" \
M_"v[84:87], a[176:179], a[136:139], v[84:87]\n" \
"v_mul_f32_e32 v138, s70, v138\n" \
"v_mul_f32_e32 v139, s70, v139\n" \
M_"v[84:87], a[240:243], a[200:203], v[84:87]\n" \
B_"v20, s[80:83], s75 offen offset:0 sc1 lds\n" \
M_"v[88:91], a[180:183], a[136:139], v[88:91]\n" \
C_"v133, v134, v135\n" \
"v_mul_f32_e64 v144, s70, v144\n" \
M_"v[88:91], a[244:247], a[200:203], v[88:91]\n" \
"v_mul_f32_e32 v145, s70, v145\n" \
"v_mul_f32_e32 v146, s70, v146\n" \
M_"v[92:95], a[184:187], a[136:139], v[92:95]\n" \
"v_mul_f32_e64 v147, s70, v147\n" \
C_"v141, v142, v143\n" \
M_"v[92:95], a[248:251], a[200:203], v[92:95]\n" \
B_"v20, s[80:83], s75 offen offset:1024 sc1 lds\n" \
M_"v[96:99], a[188:191], a[136:139], v[96:99]\n" \
C_"v134, v136, v137\n" \
C_"v135, v138, v139\n" \
M_"v[96:99], a[252:255], a[200:203], v[96:99]\n" \
C_"v142, v144, v145\n" \
C_"v143, v146, v147\n" \
M_"v[112:115], a[188:191], a[140:143], v[112:115]\n" \
"ds_write_b128 v21, v[132:135] offset:20480\n" \
M_"v[112:115], a[252:255], a[204:207], v[112:115]\n" \
B_"v20, s[80:83], s75 offen offset:2048 sc1 lds\n" \
M_"v[108:111], a[184:187], a[140:143], v[108:111]\n" \
C_"v180, v180, v181\n" \
C_"v181, v182, v183\n" \
M_"v[108:111], a[248:251], a[204:207], v[108:111]\n" \
"ds_write_b128 v21, v[140:143] offset:21504\n" \
M_"v[104:107], a[180:183], a[140:143], v[104:107]\n" \
C_"v234, v244, v245\n" \
"v_mov_b32_e64 v235, 0\n" \
M_"v[104:107], a[244:247], a[204:207], v[104:107]\n" \
B_"v20, s[80:83], s75 offen offset:3072 sc1 lds\n" \
M_"v[100:103], a[176:179], a[140:143], v[100:103]\n" \
"s_mov_b32 m0, s53\n" \
"s_nop 0\n" \
C_"v182, v50, v51\n" \
M_"v[100:103], a[240:243], a[204:207], v[100:103]\n" \
"v_mov_b32_e32 v183, 0x3f80\n" \
C_"v118, v192, v193\n" \
M_"v[52:55], a[176:179], a[128:131], v[52:55]\n" \
C_"v119, v194, v195\n" \
C_"v120, v208, v209\n" \
M_"v[52:55], a[240:243], a[192:195], v[52:55]\n" \
B_"v20, s[80:83], s76 offen offset:0 sc1 lds\n" \
M_"v[56:59], a[180:183], a[128:131], v[56:59]\n" \
C_"v121, v210, v211\n" \
C_"v122, v224, v225\n" \
M_"v[56:59], a[244:247], a[192:195], v[56:59]\n" \
C_"v123, v226, v227\n" \
C_"v124, v240, v241\n" \
C_"v125, v242, v243\n" \
M_"v[60:63], a[184:187], a[128:131], v[60:63]\n" \
M_"v[192:195], v[188:191], v[184:187], 0\n" \
B_"v20, s[80:83], s76 offen offset:1024 sc1 lds\n" \
M_"v[32:35], v[180:183], v[184:187], 0\n" \
M_"v[208:211], v[204:207], v[200:203], 0\n" \
M_"v[36:39], v[188:191], v[200:203], 0\n" \
M_"v[224:227], v[220:223], v[216:219], 0\n" \
M_"v[248:251], v[204:207], v[216:219], 0\n" \
M_"v[240:243], v[236:239], v[232:235], 0\n" \
M_"v[128:131], v[220:223], v[232:235], 0\n" \
"s_mov_b32 s64, 0xaaaaaaaa\n" \
"s_mov_b32 s65, 0xaaaaaaaa\n" \
M_"v[60:63], a[248:251], a[192:195], v[60:63]\n" \
"s_mov_b32 s66, 0xfff0ffff\n" \
"s_mov_b32 s67, 0xf000ff00\n" \
M_"v[64:67], a[188:191], a[128:131], v[64:67]\n" \
"s_mov_b32 s68, 0xffc0fffc\n" \
"s_mov_b32 s69, 0xc000fc00\n" \
"v_cndmask_b32_e64 v192, v192, v193, s[64:65]\n" \
"v_cndmask_b32_e64 v32, v32, v33, s[64:65]\n" \
"v_cndmask_b32_e64 v194, v194, v195, s[64:65]\n" \
"v_cndmask_b32_e64 v34, v34, v35, s[64:65]\n" \
"v_cndmask_b32_e64 v208, v208, v209, s[64:65]\n" \
"v_cndmask_b32_e64 v36, v36, v37, s[64:65]\n" \
"v_cndmask_b32_e64 v210, v210, v211, s[64:65]\n" \
"v_cndmask_b32_e64 v38, v38, v39, s[64:65]\n" \
"v_cndmask_b32_e64 v224, v224, v225, s[64:65]\n" \
"v_cndmask_b32_e64 v248, v248, v249, s[64:65]\n" \
"v_cndmask_b32_e64 v226, v226, v227, s[64:65]\n" \
"v_cndmask_b32_e64 v250, v250, v251, s[64:65]\n" \
"v_cndmask_b32_e64 v240, v240, v241, s[64:65]\n" \
"v_cndmask_b32_e64 v128, v128, v129, s[64:65]\n" \
"v_cndmask_b32_e64 v242, v242, v243, s[64:65]\n" \
"v_cndmask_b32_e64 v130, v130, v131, s[64:65]\n" \
"v_cndmask_b32_e64 v192, v32, v192, s[66:67]\n" \
"v_cndmask_b32_e64 v194, v34, v194, s[68:69]\n" \
"v_cndmask_b32_e64 v208, v36, v208, s[66:67]\n" \
"v_cndmask_b32_e64 v210, v38, v210, s[68:69]\n" \
"v_cndmask_b32_e64 v224, v248, v224, s[66:67]\n" \
"v_cndmask_b32_e64 v226, v250, v226, s[68:69]\n" \
"v_cndmask_b32_e64 v240, v128, v240, s[66:67]\n" \
"v_cndmask_b32_e64 v242, v130, v242, s[68:69]\n" \
M_"v[64:67], a[252:255], a[192:195], v[64:67]\n" \
"v_max_f32_e32 v193, v192, v194\n" \
"v_max_f32_e32 v209, v208, v210\n" \
"v_max_f32_e32 v225, v224, v226\n" \
"v_max_f32_e32 v241, v240, v242\n" \
"v_mul_f32_e32 v148, s70, v148\n" \
"v_permlane32_swap_b32_e32 v193, v225\n" \
"v_permlane32_swap_b32_e32 v209, v241\n" \
"v_mul_f32_e32 v149, s70, v149\n" \
"v_mul_f32_e32 v156, s70, v156\n" \
"v_mul_f32_e32 v157, s70, v157\n" \
"v_permlane32_swap_b32_e32 v196, v228\n" \
"v_permlane32_swap_b32_e32 v212, v244\n" \
"v_max_f32_e32 v193, v193, v225\n" \
"v_max_f32_e32 v209, v209, v241\n" \
"v_mul_f32_e32 v150, s70, v150\n" \
"v_mul_f32_e32 v151, s70, v151\n" \
C_"v148, v148, v149\n" \
"v_mul_f32_e32 v158, s70, v158\n" \
"v_mul_f32_e32 v159, s70, v159\n" \
C_"v156, v156, v157\n" \
"v_permlane16_swap_b32_e32 v228, v244\n" \
"v_permlane16_swap_b32_e32 v193, v209\n" \
M_"v[80:83], a[188:191], a[132:135], v[80:83]\n" \
"s_mov_b32 s72, 0x3eae1a93\n" \
"v_mul_f32_e32 v152, s70, v152\n" \
"v_mul_f32_e32 v153, s70, v153\n" \
"v_mul_f32_e32 v154, s70, v154\n" \
"v_mul_f32_e32 v155, s70, v155\n" \
C_"v149, v150, v151\n" \
"v_mul_f32_e32 v160, s70, v160\n" \
"v_mul_f32_e32 v161, s70, v161\n" \
"v_mul_f32_e32 v162, s70, v162\n" \
"v_mul_f32_e32 v163, s70, v163\n" \
C_"v157, v158, v159\n" \
"v_mul_f32_e32 v45, 0xbfb8aa3b, v228\n" \
"v_mfma_f32_16x16x16_bf16 v[48:51], v[182:183], v[46:47], 0\n" \
"v_mfma_f32_16x16x16_bf16 v[196:199], v[190:191], v[46:47], 0\n" \
"v_mfma_f32_16x16x16_bf16 v[212:215], v[206:207], v[46:47], 0\n" \
"v_max_f32_e64 v193, v193, v209\n" \
C_"v150, v152, v153\n" \
C_"v151, v154, v155\n" \
C_"v158, v160, v161\n" \
C_"v159, v162, v163\n" \
"v_mfma_f32_16x16x16_bf16 v[228:231], v[222:223], v[46:47], 0\n" \
"v_mul_f32_e32 v193, s72, v193\n" \
"v_mov_b32_e32 v209, v193\n" \
"ds_write_b128 v21, v[148:151] offset:24576\n" \
"v_mfma_f32_16x16x16_bf16 v[244:247], v[238:239], v[46:47], 0\n" \
"v_permlane16_swap_b32_e32 v193, v209\n" \
"s_mov_b32 s71, 0x3e0293ee\n" \
M_"v[80:83], a[252:255], a[196:199], v[80:83]\n" \
"v_mov_b32_e32 v225, v193\n" \
"v_mov_b32_e32 v241, v209\n" \
"v_mul_f32_e32 v164, s70, v164\n" \
"v_mul_f32_e64 v165, s70, v165\n" \
"ds_write_b128 v21, v[156:159] offset:25600\n" \
"v_mul_f32_e32 v172, s70, v172\n" \
"v_mul_f32_e32 v173, s70, v173\n" \
"v_pk_mul_f32 v[84:85], v[84:85], s[70:71]\n" \
"v_pk_mul_f32 v[92:93], v[92:93], s[70:71]\n" \
"v_pk_mul_f32 v[100:101], v[100:101], s[70:71]\n" \
"v_pk_mul_f32 v[108:109], v[108:109], s[70:71]\n" \
"v_pk_mul_f32 v[52:53], v[52:53], s[70:71]\n" \
"v_pk_mul_f32 v[60:61], v[60:61], s[70:71]\n" \
"v_permlane32_swap_b32_e32 v193, v225\n" \
"v_permlane32_swap_b32_e32 v209, v241\n" \
"v_mul_f32_e32 v166, s70, v166\n" \
"v_mul_f32_e32 v167, s70, v167\n" \
C_"v164, v164, v165\n" \
"v_mul_f32_e32 v174, s70, v174\n" \
"v_mul_f32_e32 v175, s70, v175\n" \
C_"v172, v172, v173\n" \
"v_pk_mul_f32 v[86:87], v[86:87], s[70:71]\n" \
C_"v84, v84, v85\n" \
"v_pk_mul_f32 v[94:95], v[94:95], s[70:71]\n" \
C_"v92, v92, v93\n" \
"v_pk_mul_f32 v[102:103], v[102:103], s[70:71]\n" \
C_"v100, v100, v101\n" \
"v_pk_mul_f32 v[110:111], v[110:111], s[70:71]\n" \
C_"v108, v108, v109\n" \
"v_pk_mul_f32 v[54:55], v[54:55], s[70:71]\n" \
C_"v52, v52, v53\n" \
"v_pk_mul_f32 v[62:63], v[62:63], s[70:71]\n" \
C_"v60, v60, v61\n" \
"v_fma_f32 v192, v192, s72, -v193\n" \
"v_fma_f32 v194, v194, s72, -v193\n" \
"v_fma_f32 v208, v208, s72, -v209\n" \
"v_fma_f32 v210, v210, s72, -v209\n" \
"v_mul_f32_e32 v168, s70, v168\n" \
"v_mul_f32_e32 v169, s70, v169\n" \
"v_mul_f32_e32 v170, s70, v170\n" \
"v_mul_f32_e32 v171, s70, v171\n" \
C_"v165, v166, v167\n" \
"v_mul_f32_e32 v176, s70, v176\n" \
"v_mul_f32_e32 v177, s70, v177\n" \
"v_mul_f32_e32 v178, s70, v178\n" \
"v_mul_f32_e32 v179, s70, v179\n" \
C_"v173, v174, v175\n" \
"v_pk_mul_f32 v[88:89], v[88:89], s[70:71]\n" \
"v_pk_mul_f32 v[90:91], v[90:91], s[70:71]\n" \
C_"v85, v86, v87\n" \
"v_pk_mul_f32 v[96:97], v[96:97], s[70:71]\n" \
"v_pk_mul_f32 v[98:99], v[98:99], s[70:71]\n" \
C_"v93, v94, v95\n" \
"v_pk_mul_f32 v[104:105], v[104:105], s[70:71]\n" \
"v_pk_mul_f32 v[106:107], v[106:107], s[70:71]\n" \
C_"v101, v102, v103\n" \
"v_pk_mul_f32 v[112:113], v[112:113], s[70:71]\n" \
"v_pk_mul_f32 v[114:115], v[114:115], s[70:71]\n" \
C_"v109, v110, v111\n" \
"v_pk_mul_f32 v[56:57], v[56:57], s[70:71]\n" \
"v_pk_mul_f32 v[58:59], v[58:59], s[70:71]\n" \
C_"v53, v54, v55\n" \
"v_pk_mul_f32 v[64:65], v[64:65], s[70:71]\n" \
"v_pk_mul_f32 v[66:67], v[66:67], s[70:71]\n" \
C_"v61, v62, v63\n" \
"v_pk_mul_f32 v[80:81], v[80:81], s[70:71]\n" \
"v_pk_mul_f32 v[82:83], v[82:83], s[70:71]\n" \
E_"v192, v192\n" \
E_"v194, v194\n" \
E_"v208, v208\n" \
E_"v210, v210\n" \
"v_fma_f32 v224, v224, s72, -v225\n" \
"v_fma_f32 v226, v226, s72, -v225\n" \
M_"v[76:79], a[184:187], a[132:135], v[76:79]\n" \
C_"v166, v168, v169\n" \
C_"v167, v170, v171\n" \
C_"v174, v176, v177\n" \
C_"v175, v178, v179\n" \
C_"v86, v88, v89\n" \
C_"v87, v90, v91\n" \
C_"v94, v96, v97\n" \
C_"v95, v98, v99\n" \
C_"v102, v104, v105\n" \
C_"v103, v106, v107\n" \
C_"v110, v112, v113\n" \
C_"v111, v114, v115\n" \
C_"v54, v56, v57\n" \
C_"v55, v58, v59\n" \
C_"v62, v64, v65\n" \
C_"v63, v66, v67\n" \
E_"v224, v224\n" \
E_"v226, v226\n" \
"v_fma_f32 v240, v240, s72, -v241\n" \
"v_fma_f32 v242, v242, s72, -v241\n" \
"ds_write_b128 v21, v[164:167] offset:28672\n" \
C_"v192, v192, v194\n" \
C_"v208, v208, v210\n" \
E_"v240, v240\n" \
E_"v242, v242\n" \
C_"v132, v48, v49\n" \
C_"v133, v50, v51\n" \
C_"v134, v196, v197\n" \
C_"v135, v198, v199\n" \
"ds_write_b128 v21, v[172:175] offset:29696\n" \
C_"v136, v212, v213\n" \
C_"v137, v214, v215\n" \
"v_perm_b32 v184, v192, v192, v40\n" \
"v_perm_b32 v185, v192, v192, v41\n" \
"v_perm_b32 v186, v192, v192, v42\n" \
"v_perm_b32 v187, v192, v192, v43\n" \
"v_perm_b32 v200, v208, v208, v40\n" \
"v_perm_b32 v201, v208, v208, v41\n" \
"v_perm_b32 v202, v208, v208, v42\n" \
"v_perm_b32 v203, v208, v208, v43\n" \
"ds_write_b128 v27, v[84:87] offset:8192\n" \
C_"v224, v224, v226\n" \
C_"v138, v228, v229\n" \
C_"v139, v230, v231\n" \
M_"v[32:35], v[132:135], v[184:187], 0\n" \
M_"v[36:39], v[134:137], v[200:203], 0\n" \
"v_perm_b32 v216, v224, v224, v40\n" \
"v_perm_b32 v217, v224, v224, v41\n" \
"ds_write_b128 v27, v[92:95] offset:9216\n" \
"v_perm_b32 v218, v224, v224, v42\n" \
"v_perm_b32 v219, v224, v224, v43\n" \
C_"v240, v240, v242\n" \
M_"v[76:79], a[248:251], a[196:199], v[76:79]\n" \
C_"v140, v244, v245\n" \
C_"v141, v246, v247\n" \
M_"v[248:251], v[136:139], v[216:219], 0\n" \
"v_perm_b32 v232, v240, v240, v40\n" \
"v_perm_b32 v233, v240, v240, v41\n" \
"ds_write_b128 v27, v[100:103] offset:12288\n" \
"v_perm_b32 v234, v240, v240, v42\n" \
"v_perm_b32 v235, v240, v240, v43\n" \
"v_pk_mul_f32 v[76:77], v[76:77], s[70:71]\n" \
"v_pk_mul_f32 v[78:79], v[78:79], s[70:71]\n" \
C_"v76, v76, v77\n" \
C_"v77, v78, v79\n" \
C_"v78, v80, v81\n" \
C_"v79, v82, v83\n" \
M_"v[128:131], v[138:141], v[232:235], 0\n" \
"ds_write_b128 v27, v[108:111] offset:13312\n" \
M_"v[72:75], a[180:183], a[132:135], v[72:75]\n" \
C_"v116, v252, v253\n" \
C_"v117, v254, v255\n" \
M_"v[72:75], a[244:247], a[196:199], v[72:75]\n" \
"v_exp_f32_e64 v45, v45\n" \
M_"v[192:195], v[116:119], v[184:187], 0\n" \
"ds_write_b128 v27, v[52:55] offset:0\n" \
M_"v[208:211], v[118:121], v[200:203], 0\n" \
M_"v[224:227], v[120:123], v[216:219], 0\n" \
M_"v[240:243], v[122:125], v[232:235], 0\n" \
"ds_write_b128 v27, v[60:63] offset:1024\n" \
"v_mov_b32_dpp v34, v38 quad_perm:[0,1,2,3] row_mask:0x2 bank_mask:0xf\n" \
"v_pk_mul_f32 v[72:73], v[72:73], s[70:71]\n" \
"v_pk_mul_f32 v[74:75], v[74:75], s[70:71]\n" \
"v_mov_b32_dpp v34, v250 quad_perm:[0,1,2,3] row_mask:0x4 bank_mask:0xf\n" \
"v_add_f32_e64 v45, 1.0, v45\n" \
M_"v[68:71], a[176:179], a[132:135], v[68:71]\n" \
"v_mov_b32_dpp v34, v130 quad_perm:[0,1,2,3] row_mask:0x8 bank_mask:0xf\n" \
"v_mov_b32_dpp v32, v36 quad_perm:[0,1,2,3] row_mask:0x2 bank_mask:0xf\n" \
"v_rcp_f32_e64 v45, v45\n" \
"ds_write_b128 v27, v[76:79] offset:5120\n" \
"v_mov_b32_dpp v33, v37 quad_perm:[0,1,2,3] row_mask:0x2 bank_mask:0xf\n" \
"v_rcp_f32_e64 v34, v34\n" \
"v_mov_b32_dpp v32, v248 quad_perm:[0,1,2,3] row_mask:0x4 bank_mask:0xf\n" \
"v_mov_b32_dpp v33, v249 quad_perm:[0,1,2,3] row_mask:0x4 bank_mask:0xf\n" \
"v_mul_f32_e64 v34, v45, v34\n" \
"v_mov_b32_dpp v32, v128 quad_perm:[0,1,2,3] row_mask:0x8 bank_mask:0xf\n" \
"v_mov_b32_dpp v33, v129 quad_perm:[0,1,2,3] row_mask:0x8 bank_mask:0xf\n" \
"v_mul_f32_e32 v32, v34, v32\n" \
"v_mul_f32_e32 v33, v34, v33\n" \
"v_mov_b32_e64 v38, v34\n" \
M_"v[68:71], a[240:243], a[196:199], v[68:71]\n" \
"v_sub_f32_e32 v44, 1.0, v45\n" \
"v_permlane16_swap_b32_e32 v34, v38\n" \
"v_mov_b32_e32 v250, v34\n" \
"v_mov_b32_e32 v130, v38\n" \
C_"v32, v32, v33\n" \
"v_permlane32_swap_b32_e32 v34, v250\n" \
"v_permlane32_swap_b32_e32 v38, v130\n" \
"v_mul_f32_e64 v192, v34, v192\n" \
"v_pk_mul_f32 v[68:69], v[68:69], s[70:71]\n" \
"v_pk_mul_f32 v[70:71], v[70:71], s[70:71]\n" \
C_"v68, v68, v69\n" \
C_"v69, v70, v71\n" \
C_"v70, v72, v73\n" \
C_"v71, v74, v75\n" \
"ds_write_b128 v27, v[68:71] offset:4096\n" \
"v_mul_f32_e32 v193, v34, v193\n" \
"v_mul_f32_e32 v208, v38, v208\n" \
"v_mul_f32_e32 v209, v38, v209\n" \
"v_mul_f32_e32 v224, v250, v224\n" \
"v_mul_f32_e32 v225, v250, v225\n" \
"v_mul_f32_e32 v240, v130, v240\n" \
"v_mul_f32_e32 v241, v130, v241\n" \
"v_mul_f32_e32 v194, v34, v194\n" \
"v_mul_f32_e64 v195, v34, v195\n" \
C_"v192, v192, v193\n" \
"v_mul_f32_e32 v210, v38, v210\n" \
"v_mul_f32_e32 v211, v38, v211\n" \
C_"v208, v208, v209\n" \
"v_mul_f32_e32 v226, v250, v226\n" \
"v_mul_f32_e32 v227, v250, v227\n" \
C_"v224, v224, v225\n" \
"v_mul_f32_e32 v242, v130, v242\n" \
"v_mul_f32_e32 v243, v130, v243\n" \
C_"v240, v240, v241\n" \
C_"v193, v194, v195\n" \
"s_add_u32 s84, s48, 0x0\n" \
"s_nop 0\n" \
C_"v209, v210, v211\n" \
"s_add_u32 s85, s48, 0x24800\n" \
C_"v225, v226, v227\n" \
"s_add_u32 s86, s48, 0x49000\n" \
C_"v241, v242, v243\n" \
"s_add_u32 s87, s48, 0x6d800\n" \
"ds_write_b32 v26, v44\n" \
"buffer_store_dword v32, v25, s[80:83], s49 offen sc1\n" \
"buffer_store_dwordx2 v[192:193], v24, s[80:83], s84 offen sc1\n" \
"buffer_store_dwordx2 v[208:209], v24, s[80:83], s85 offen sc1\n" \
"buffer_store_dwordx2 v[224:225], v24, s[80:83], s86 offen sc1\n" \
"buffer_store_dwordx2 v[240:241], v24, s[80:83], s87 offen sc1\n" \
"s_branch L_end_%=\n" \
"L_poll61_%=:\n" \
"s_mov_b32 s84, 0\n" \
"L_pl61_%=:\n" \
"s_sleep 1\n" \
"v_bfe_u32 v44, v20, 2, 5\n" \
"buffer_load_dword v44, v44, s[80:83], s38 offen offset:128 sc1\n" \
"s_waitcnt vmcnt(0)\n" \
"v_cmp_ne_u32_e32 vcc, s62, v44\n" \
"s_add_u32 s84, s84, 1\n" \
"s_nop 3\n" \
"s_cbranch_vccz L_pollr61_%=\n" \
"s_cmp_gt_u32 s84, 0x400000\n" \
"s_cbranch_scc0 L_pl61_%=\n" \
"s_branch L_pollr61_%=\n" \
"L_poll161_%=:\n" \
"s_mov_b32 s84, 0\n" \
"L_pl161_%=:\n" \
"s_sleep 1\n" \
"v_bfe_u32 v44, v20, 2, 5\n" \
"buffer_load_dword v44, v44, s[80:83], s38 offen offset:0 sc1\n" \
"s_waitcnt vmcnt(0)\n" \
"v_cmp_ne_u32_e32 vcc, s62, v44\n" \
"s_add_u32 s84, s84, 1\n" \
"s_nop 3\n" \
"s_cbranch_vccz L_pollr161_%=\n" \
"s_cmp_gt_u32 s84, 0x400000\n" \
"s_cbranch_scc0 L_pl161_%=\n" \
"s_branch L_pollr161_%=\n" \
"L_end_%=:\n"
#define PROJ_PAGES 20
#define PROJ_VCLOB "v21","v24","v25","v28","v29","v32","v33","v34","v35","v36","v37","v38","v39","v40","v41","v42","v43","v44","v45","v46","v47","v48","v49","v50","v51","v52","v53","v54","v55","v56","v57","v58","v59","v60","v61","v62","v63","v64","v65","v66","v67","v68","v69","v70","v71","v72","v73","v74","v75","v76","v77","v78","v79","v80","v81","v82","v83","v84","v85","v86","v87","v88","v89","v90","v91","v92","v93","v94","v95","v96","v97","v98","v99","v100","v101","v102","v103","v104","v105","v106","v107","v108","v109","v110","v111","v112","v113","v114","v115","v116","v117","v118","v119","v120","v121","v122","v123","v124","v125","v126","v127","v128","v129","v130","v131","v132","v133","v134","v135","v136","v137","v138","v139","v140","v141","v142","v143","v144","v145","v146","v147","v148","v149","v150","v151","v152","v153","v154","v155","v156","v157","v158","v159","v160","v161","v162","v163","v164","v165","v166","v167","v168","v169","v170","v171","v172","v173","v174","v175","v176","v177","v178","v179","v180","v181","v182","v183","v184","v185","v186","v187","v188","v189","v190","v192","v193","v194","v195","v196","v197","v198","v199","v200","v201","v202","v203","v204","v205","v206","v207","v208","v209","v210","v211","v212","v213","v214","v215","v216","v217","v218","v219","v220","v221","v222","v223","v224","v225","v226","v227","v228","v229","v230","v231","v232","v233","v234","v235","v236","v237","v238","v239","v240","v241","v242","v243","v244","v245","v246","v247","v248","v249","v250","v251","v252","v253","v254","v255"
#define PROJ_SCLOB "s4","s5","s6","s7","s32","s33","s34","s35","s36","s37","s38","s39","s40","s41","s42","s43","s44","s45","s46","s47","s48","s49","s50","s51","s52","s53","s54","s55","s63","s64","s65","s66","s67","s68","s69","s70","s71","s72","s73","s74","s75","s76","s77","s78","s79","s80","s81","s82","s83","s84","s85","s86","s87","s88","s89","s90","s91","s92","s93","s94","s95","s96","s97","s98","s99","s100","s101"

typedef const u16* const __attribute__((address_space(4)))* kaptr;
__device__ __forceinline__ void proj_phase(kaptr& ka, char*& ws, const unsigned wid, u32x4 (&vp)[5], unsigned& vl, u32x4 (&sa)[5], u32x2 s24, u32x2& s28, unsigned tdum, u32x4& srdA, u32x2& sd, unsigned& tag
    ) {
    const unsigned b = blockIdx.x, h = (b & 7) * 4 + ((b >> 3) & 3), mt = b >> 5;
    const unsigned rb = (wid < 2u ? 15u - mt : mt) * 128u;
    const unsigned lane = threadIdx.x & 63u, wc = wid & 1u;
    u32x2 vAc, vB;
#pragma unroll
    for (int par = 0; par < 2; ++par) {
        const unsigned lr = lane >> 3, cc = (lane & 7u) ^ (4u * par + (lr >> 1)), r0 = rb + wc * 64u + lr;
        vAc[par] = r0 * (DC * 2) + cc * 16u;
        const unsigned br = lane >> 4, p16 = lane & 15u, sc = (((p16 >> 1) ^ (br | (par << 2))) << 1) | (p16 & 1u);
        vB[par] = (16u * wid + br) * (NBIG * 2) + h * (HD * 2) + sc * 16u;
    }
    vl = lane * 16u;
    u32x2 s30 = {wid * 8192u, 65536u + wid * 4096u};
    asm volatile(PROJ_ASM
                 : "={s62}"(tag), "={s[56:59]}"(srdA), "={s[60:61]}"(sd), "+{s[8:11]}"(sa[0]), "+{s[12:15]}"(sa[1]), "+{s[16:19]}"(sa[2]), "+{s[20:23]}"(sa[3]), "+{s[24:25]}"(s24), "+{s[28:29]}"(s28), "+{s[30:31]}"(s30),
                   "={v[0:3]}"(vp[0]), "={v[4:7]}"(vp[1]), "={v[8:11]}"(vp[2]), "={v[12:15]}"(vp[3]), "={v[16:19]}"(vp[4]), "+{v[22:23]}"(vAc), "+{v[26:27]}"(vB), "+{v191}"(tdum), "+{s[26:27]}"(ws), "+{s[0:1]}"(ka)
                 : "{s2}"(b), "{s3}"(wid), "{v20}"(vl)
                 : "memory", "m0", "vcc", "scc", PROJ_VCLOB, PROJ_SCLOB, ACLOB_LO, ACLOB_HI);
    sa[4] = (u32x4){s24[0], 0u, 0u, 0u};
}

namespace att {
#define DI __device__ __forceinline__
typedef __bf16 bfx8 __attribute__((ext_vector_type(8)));
DI unsigned rfl(unsigned x) { return __builtin_amdgcn_readfirstlane(x); }
DI f32x4 mfma(u32x4 a, u32x4 b, f32x4 c) { return __builtin_amdgcn_mfma_f32_16x16x32_bf16(__builtin_bit_cast(bfx8, a), __builtin_bit_cast(bfx8, b), c, 0, 0, 0); }
typedef __bf16 bfx2 __attribute__((ext_vector_type(2)));
typedef float f32x2 __attribute__((ext_vector_type(2)));
DI unsigned cvtpk2(float lo, float hi) { f32x2 v = {lo, hi}; bfx2 b = __builtin_convertvector(v, bfx2); return __builtin_bit_cast(unsigned, b); }
DI void att_exact(const u16* __restrict__ x, const u16* __restrict__ wq, char* __restrict__ ws, unsigned h, unsigned r0, float g1) {
    const unsigned lane = lane_id(), g = lane >> 4, c = lane & 15u;
    f32x4 o[8];
#pragma unroll
    for (int n = 0; n < 8; ++n) o[n] = (f32x4){0.f, 0.f, 0.f, 0.f};
#pragma unroll 1
    for (unsigned kk = 0; kk < (unsigned)DM; kk += 32u) {
        const unsigned k = kk + 8u * g;
        const u32x4 xf = *reinterpret_cast<const u32x4*>(x + (r0 + c) * DM + k);
#pragma unroll
        for (int n = 0; n < 8; ++n) {
            const u16* w = wq + k * NBIG + h * HD + n * 16 + c; u32x4 wf;
#pragma unroll
            for (int e = 0; e < 4; ++e) wf[e] = (unsigned)w[2 * e * NBIG] | ((unsigned)w[(2 * e + 1) * NBIG] << 16);
            o[n] = mfma(wf, xf, o[n]);
        }
    }
    u32x4 q[4];
#pragma unroll
    for (int p = 0; p < 4; ++p) {
        const f32x4 a = o[2 * p] * 0.12751743082f, b = o[2 * p + 1] * 0.12751743082f;
        q[p] = (u32x4){cvtpk2(a[0], a[1]), cvtpk2(a[2], a[3]), cvtpk2(b[0], b[1]), cvtpk2(b[2], b[3])};
    }
#pragma unroll
    for (int n = 0; n < 8; ++n) o[n] = (f32x4){0.f, 0.f, 0.f, 0.f};
    const __amdgpu_buffer_rsrc_t srd = __builtin_amdgcn_make_buffer_rsrc((void*)(ws + WS_K + (size_t)h * 1048576), (short)0, 1048576, 0x00020000);
    float m = -3.0e38f, l = 0.f;
#pragma unroll 1
    for (unsigned k0 = 0; k0 <= r0 + 15u; k0 += 32u) {
        const unsigned tb = (k0 >> 6) * 32768u + (k0 & 32u) * 256u;
        f32x4 s[2]; float mx = m;
#pragma unroll
        for (int kt = 0; kt < 2; ++kt) {
            s[kt] = (f32x4){0.f, 0.f, 0.f, 0.f};
#pragma unroll
            for (int p = 0; p < 4; ++p) s[kt] = mfma(__builtin_amdgcn_raw_buffer_load_b128(srd, lane * 16u + (kt * 4 + p) * 1024u, tb, 16), q[p], s[kt]);
#pragma unroll
            for (int e = 0; e < 4; ++e) { if (k0 + 16u * kt + 4u * g + e > r0 + c) s[kt][e] = -3.0e38f; mx = fmaxf(mx, s[kt][e]); }
        }
        mx = fmaxf(mx, __shfl_xor(mx, 16)); mx = fmaxf(mx, __shfl_xor(mx, 32));
        const float al = __builtin_amdgcn_exp2f(m - mx); m = mx;
        u32x4 pf;
#pragma unroll
        for (int e = 0; e < 4; ++e) pf[e] = cvtpk2(__builtin_amdgcn_exp2f(s[e >> 1][2 * (e & 1)] - m), __builtin_amdgcn_exp2f(s[e >> 1][2 * (e & 1) + 1] - m));
        l *= al;
#pragma unroll
        for (int e = 0; e < 4; ++e) l += __uint_as_float(pf[e] << 16) + __uint_as_float(pf[e] & 0xffff0000u);
#pragma unroll
        for (int n = 0; n < 8; ++n) o[n] = mfma(__builtin_amdgcn_raw_buffer_load_b128(srd, lane * 16u + 16384u + n * 1024u, tb, 16), pf, o[n] * al);
    }
    l += __shfl_xor(l, 16); l += __shfl_xor(l, 32);
    const float f = g1 / l;
    const __amdgpu_buffer_rsrc_t srdA = __builtin_amdgcn_make_buffer_rsrc((void*)(ws + WS_AF), (short)0, 128 * KS_TOT * 1024, 0x00020000);
#pragma unroll
    for (int p = 0; p < 4; ++p) {
        const f32x4 a = o[2 * p] * f, b = o[2 * p + 1] * f;
        __builtin_amdgcn_raw_buffer_store_b128((u32x4){cvtpk2(a[0], a[1]), cvtpk2(a[2], a[3]), cvtpk2(b[0], b[1]), cvtpk2(b[2], b[3])}, srdA, lane * 16u + p * 1024u, ((r0 >> 4) * KS_TOT + 18u + 4u * h) * 1024u, 16);
    }
}
}

#define ATT_ASM \
"s_branch L_ago_%=\n" \
".p2align 12\n" \
"L_ab:\n" \
".fill (L_ab + 0 - .) / 4, 4, 0xBF800000\n" \
"L_ap0:\n" \
"s_setpc_b64 s[88:89]\n" \
"L_ago_%=:\n" \
"s_mov_b32 s101, 0\n" \
"s_waitcnt vmcnt(15) lgkmcnt(0)\n" \
"s_barrier\n" \
"v_mfma_f32_32x32x16_bf16 v[128:143], a[120:123], a[120:123], 0\n" \
"v_and_b32_e32 v63, 0x300, v20\n" \
R_"v[96:99], v63 offset:0\n" \
"v_bfe_u32 v61, v20, 2, 5\n" \
"s_mov_b32 s96, s24\n" \
"v_add_u32_e32 v58, s22, v20\n" \
R_"a[0:3], v58 offset:0\n" \
"v_mfma_f32_32x32x16_bf16 v[144:159], a[120:123], a[120:123], 0\n" \
"buffer_load_dword v57, v61, s[56:59], s96 offen sc1\n" \
R_"a[16:19], v58 offset:8192\n" \
"v_add_u32_e32 v59, s23, v20\n" \
R_"a[32:35], v59 offset:0\n" \
R_"a[48:51], v59 offset:8192\n" \
"v_mfma_f32_32x32x16_bf16 v[160:175], a[120:123], a[120:123], 0\n" \
R_"v[100:103], v63 offset:1024\n" \
R_"a[4:7], v58 offset:1024\n" \
R_"a[20:23], v58 offset:9216\n" \
"s_sub_u32 s67, s12, 0xc000\n" \
"s_mov_b32 s65, s12\n" \
"v_add_u32_e32 v55, s67, v20\n" \
"v_mfma_f32_32x32x16_bf16 v[176:191], a[120:123], a[120:123], 0\n" \
"v_add_u32_e32 v52, 0xc000, v55\n" \
"s_xor_b32 m0, s65, 0x10000\n" \
R_"a[36:39], v59 offset:1024\n" \
B_"v52, s[8:11], 0 offen offset:0 sc1 lds\n" \
R_"a[52:55], v59 offset:9216\n" \
"v_mfma_f32_32x32x16_bf16 v[192:207], a[120:123], a[120:123], 0\n" \
R_"v[104:107], v63 offset:2048\n" \
R_"a[8:11], v58 offset:2048\n" \
R_"a[24:27], v58 offset:10240\n" \
B_"v52, s[8:11], 0 offen offset:1024 sc1 lds\n" \
R_"a[40:43], v59 offset:2048\n" \
"v_mfma_f32_32x32x16_bf16 v[208:223], a[120:123], a[120:123], 0\n" \
R_"a[56:59], v59 offset:10240\n" \
R_"v[108:111], v63 offset:3072\n" \
R_"a[12:15], v58 offset:3072\n" \
R_"a[28:31], v58 offset:11264\n" \
"s_waitcnt lgkmcnt(15)\n" \
"v_xor_b32_e32 v96, 0x80008000, v96\n" \
"v_mfma_f32_32x32x16_bf16 v[224:239], a[120:123], a[120:123], 0\n" \
"v_xor_b32_e32 v97, 0x80008000, v97\n" \
"v_xor_b32_e32 v98, 0x80008000, v98\n" \
"v_xor_b32_e32 v99, 0x80008000, v99\n" \
R_"a[44:47], v59 offset:3072\n" \
R_"a[60:63], v59 offset:11264\n" \
R_"a[160:163], v20 offset:0\n" \
"v_mfma_f32_32x32x16_bf16 v[240:255], a[120:123], a[120:123], 0\n" \
R_"a[164:167], v20 offset:1024\n" \
"s_waitcnt lgkmcnt(15)\n" \
"v_xor_b32_e32 v100, 0x80008000, v100\n" \
"v_xor_b32_e32 v101, 0x80008000, v101\n" \
"v_xor_b32_e32 v102, 0x80008000, v102\n" \
"v_xor_b32_e32 v103, 0x80008000, v103\n" \
M_"v[32:35], v[96:99], a[0:3], 0\n" \
R_"a[168:171], v20 offset:2048\n" \
R_"a[172:175], v20 offset:3072\n" \
M_"v[36:39], v[96:99], a[16:19], 0\n" \
R_"a[188:191], v20 offset:7168\n" \
R_"a[184:187], v20 offset:6144\n" \
M_"v[40:43], v[96:99], a[32:35], 0\n" \
"s_waitcnt lgkmcnt(15)\n" \
"v_xor_b32_e32 v104, 0x80008000, v104\n" \
M_"v[44:47], v[96:99], a[48:51], 0\n" \
"v_xor_b32_e32 v105, 0x80008000, v105\n" \
"v_xor_b32_e32 v106, 0x80008000, v106\n" \
M_"v[32:35], v[100:103], a[4:7], v[32:35]\n" \
"v_xor_b32_e32 v107, 0x80008000, v107\n" \
R_"a[180:183], v20 offset:5120\n" \
M_"v[36:39], v[100:103], a[20:23], v[36:39]\n" \
R_"a[176:179], v20 offset:4096\n" \
"s_waitcnt lgkmcnt(12)\n" \
"v_xor_b32_e32 v108, 0x80008000, v108\n" \
M_"v[40:43], v[100:103], a[36:39], v[40:43]\n" \
"v_xor_b32_e32 v109, 0x80008000, v109\n" \
"v_xor_b32_e32 v110, 0x80008000, v110\n" \
M_"v[44:47], v[100:103], a[52:55], v[44:47]\n" \
"v_xor_b32_e32 v111, 0x80008000, v111\n" \
"v_add_u32_e32 v60, s15, v20\n" \
M_"v[32:35], v[104:107], a[8:11], v[32:35]\n" \
"v_add_u32_e32 v60, 0x10000, v60\n" \
R_"a[128:131], v20 offset:8192\n" \
M_"v[36:39], v[104:107], a[24:27], v[36:39]\n" \
R_"a[144:147], v20 offset:12288\n" \
R_"a[132:135], v20 offset:9216\n" \
M_"v[40:43], v[104:107], a[40:43], v[40:43]\n" \
R_"a[148:151], v20 offset:13312\n" \
R_"a[136:139], v20 offset:10240\n" \
M_"v[44:47], v[104:107], a[56:59], v[44:47]\n" \
R_"a[152:155], v20 offset:14336\n" \
"s_waitcnt lgkmcnt(13)\n" \
M_"v[32:35], v[108:111], a[12:15], v[32:35]\n" \
R_"a[140:143], v20 offset:11264\n" \
R_"a[156:159], v20 offset:15360\n" \
M_"v[36:39], v[108:111], a[28:31], v[36:39]\n" \
R_"a[64:67], v60 offset:16384\n" \
R_"a[68:71], v60 offset:17408\n" \
M_"v[40:43], v[108:111], a[44:47], v[40:43]\n" \
R_"a[72:75], v60 offset:18432\n" \
R_"a[76:79], v60 offset:19456\n" \
M_"v[44:47], v[108:111], a[60:63], v[44:47]\n" \
R_"a[80:83], v60 offset:24576\n" \
R_"a[84:87], v60 offset:25600\n" \
M_"v[64:67], a[160:163], a[0:3], v[32:35]\n" \
R_"a[88:91], v60 offset:26624\n" \
"s_waitcnt lgkmcnt(15)\n" \
M_"v[64:67], a[164:167], a[4:7], v[64:67]\n" \
R_"a[92:95], v60 offset:27648\n" \
R_"a[192:195], v60 offset:0\n" \
M_"v[64:67], a[168:171], a[8:11], v[64:67]\n" \
R_"a[196:199], v60 offset:1024\n" \
R_"a[200:203], v60 offset:2048\n" \
M_"v[64:67], a[172:175], a[12:15], v[64:67]\n" \
R_"a[204:207], v60 offset:3072\n" \
R_"a[208:211], v60 offset:8192\n" \
M_"v[68:71], a[188:191], a[12:15], v[32:35]\n" \
R_"a[212:215], v60 offset:9216\n" \
R_"a[216:219], v60 offset:10240\n" \
M_"v[68:71], a[184:187], a[8:11], v[68:71]\n" \
R_"a[220:223], v60 offset:11264\n" \
"v_mov_b32_e32 v56, s62\n" \
M_"v[68:71], a[180:183], a[4:7], v[68:71]\n" \
"v_readlane_b32 s68, v11, 0\n" \
"s_mov_b32 m0, s20\n" \
M_"v[68:71], a[176:179], a[0:3], v[68:71]\n" \
"global_load_lds_dword v[16:17], off\n" \
M_"v[76:79], a[176:179], a[16:19], v[36:39]\n" \
E_"v66, v66\n" \
M_"v[76:79], a[180:183], a[20:23], v[76:79]\n" \
E_"v67, v67\n" \
M_"v[76:79], a[184:187], a[24:27], v[76:79]\n" \
E_"v64, v64\n" \
M_"v[76:79], a[188:191], a[28:31], v[76:79]\n" \
E_"v70, v70\n" \
M_"v[72:75], a[172:175], a[28:31], v[36:39]\n" \
E_"v71, v71\n" \
M_"v[72:75], a[168:171], a[24:27], v[72:75]\n" \
E_"v68, v68\n" \
M_"v[72:75], a[164:167], a[20:23], v[72:75]\n" \
E_"v69, v69\n" \
M_"v[72:75], a[160:163], a[16:19], v[72:75]\n" \
C_"v71, v70, v71\n" \
"v_accvgpr_read_b32 v28, a124\n" \
M_"v[80:83], a[160:163], a[32:35], v[40:43]\n" \
E_"v65, v65\n" \
M_"v[80:83], a[164:167], a[36:39], v[80:83]\n" \
E_"v76, v76\n" \
M_"v[80:83], a[168:171], a[40:43], v[80:83]\n" \
E_"v77, v77\n" \
M_"v[80:83], a[172:175], a[44:47], v[80:83]\n" \
E_"v72, v72\n" \
M_"v[84:87], a[188:191], a[44:47], v[40:43]\n" \
E_"v73, v73\n" \
M_"v[84:87], a[184:187], a[40:43], v[84:87]\n" \
E_"v74, v74\n" \
M_"v[84:87], a[180:183], a[36:39], v[84:87]\n" \
E_"v75, v75\n" \
M_"v[84:87], a[176:179], a[32:35], v[84:87]\n" \
C_"v72, v72, v73\n" \
"v_accvgpr_read_b32 v29, a125\n" \
M_"v[92:95], a[176:179], a[48:51], v[44:47]\n" \
E_"v78, v78\n" \
M_"v[92:95], a[180:183], a[52:55], v[92:95]\n" \
"s_waitcnt vmcnt(3)\n" \
"v_cmp_ne_u32_e32 vcc, s62, v57\n" \
M_"v[92:95], a[184:187], a[56:59], v[92:95]\n" \
E_"v79, v79\n" \
M_"v[92:95], a[188:191], a[60:63], v[92:95]\n" \
"v_accvgpr_read_b32 v21, a126\n" \
C_"v70, v68, v69\n" \
M_"v[88:91], a[172:175], a[60:63], v[44:47]\n" \
"s_cbranch_vccnz L_skb_%=\n" \
"L_rkb_%=:\n" \
C_"v73, v74, v75\n" \
"s_mov_b64 exec, 1\n" \
"buffer_store_dword v56, off, s[56:59], s68 offset:0 sc1\n" \
"s_mov_b64 exec, -1\n" \
M_"v[88:91], a[168:171], a[56:59], v[88:91]\n" \
"v_mov_b32_e32 v62, 0\n" \
"s_mov_b32 s64, 0x8000\n" \
M_"v[88:91], a[164:167], a[52:55], v[88:91]\n" \
"v_add_u32_e32 v50, 0x6000, v55\n" \
"v_add_u32_e32 v51, 0xa000, v55\n" \
M_"v[88:91], a[160:163], a[48:51], v[88:91]\n" \
"v_add_u32_e32 v53, 0x10000, v55\n" \
"v_add_u32_e32 v48, 0xc000, v20\n" \
"s_waitcnt vmcnt(5) lgkmcnt(0)\n" \
"s_barrier\n" \
"v_add_u32_e32 v49, s17, v20\n" \
"v_accvgpr_write_b32 a124, v96\n" \
"v_accvgpr_write_b32 a125, v97\n" \
"v_accvgpr_write_b32 a126, v98\n" \
"v_accvgpr_write_b32 a127, v99\n" \
"v_accvgpr_write_b32 a224, v100\n" \
"v_accvgpr_write_b32 a225, v101\n" \
"v_accvgpr_write_b32 a226, v102\n" \
"v_accvgpr_write_b32 a227, v103\n" \
"v_accvgpr_write_b32 a228, v104\n" \
"v_accvgpr_write_b32 a229, v105\n" \
"v_accvgpr_write_b32 a230, v106\n" \
"v_accvgpr_write_b32 a231, v107\n" \
"v_accvgpr_write_b32 a232, v108\n" \
"v_accvgpr_write_b32 a233, v109\n" \
"v_accvgpr_write_b32 a234, v110\n" \
"v_accvgpr_write_b32 a235, v111\n" \
C_"v69, v66, v67\n" \
C_"v68, v64, v65\n" \
C_"v74, v76, v77\n" \
C_"v75, v78, v79\n" \
"s_sub_u32 s66, s18, 1\n" \
"s_cbranch_scc1 L_tr_%=\n" \
"s_branch L_1_%=\n" \
".fill (L_ab + 1528 - .) / 4, 4, 0xBF800000\n" \
"L_tL_1_%=:\n" \
"s_waitcnt vmcnt(4) lgkmcnt(0)\n" \
"v_smfmac_f32_16x16x64_bf16 a[100:103], a[104:107], v[116:123], v21\n" \
"s_barrier\n" \
"L_1_%=:\n" \
M_"v[96:99], a[128:131], a[0:3], v[32:35]\n" \
"s_xor_b32 m0, s65, 0x12000\n" \
R_"a[160:163], v49 offset:0\n" \
M_"v[96:99], a[132:135], a[4:7], v[96:99]\n" \
B_"v50, s[8:11], s64 offen offset:0 sc1 lds\n" \
M_"v[96:99], a[136:139], a[8:11], v[96:99]\n" \
R_"a[164:167], v49 offset:1024\n" \
R_"a[168:171], v49 offset:2048\n" \
M_"v[96:99], a[140:143], a[12:15], v[96:99]\n" \
R_"a[172:175], v49 offset:3072\n" \
R_"a[176:179], v49 offset:4096\n" \
M_"v[100:103], a[156:159], a[12:15], v[32:35]\n" \
R_"a[180:183], v49 offset:5120\n" \
R_"a[184:187], v49 offset:6144\n" \
M_"v[100:103], a[152:155], a[8:11], v[100:103]\n" \
"v_exp_f32_e64 v86, v86\n" \
M_"v[100:103], a[148:151], a[4:7], v[100:103]\n" \
"v_exp_f32_e64 v87, v87\n" \
M_"v[100:103], a[144:147], a[0:3], v[100:103]\n" \
R_"a[188:191], v49 offset:7168\n" \
C_"v87, v86, v87\n" \
M_"v[108:111], a[144:147], a[16:19], v[36:39]\n" \
"v_exp_f32_e64 v84, v84\n" \
M_"v[108:111], a[148:151], a[20:23], v[108:111]\n" \
"v_exp_f32_e64 v85, v85\n" \
M_"v[108:111], a[152:155], a[24:27], v[108:111]\n" \
"v_exp_f32_e64 v82, v82\n" \
M_"v[108:111], a[156:159], a[28:31], v[108:111]\n" \
"v_exp_f32_e64 v83, v83\n" \
M_"v[104:107], a[140:143], a[28:31], v[36:39]\n" \
"v_exp_f32_e64 v80, v80\n" \
M_"v[104:107], a[136:139], a[24:27], v[104:107]\n" \
"v_exp_f32_e64 v81, v81\n" \
M_"v[104:107], a[132:135], a[20:23], v[104:107]\n" \
"v_exp_f32_e64 v88, v88\n" \
M_"v[104:107], a[128:131], a[16:19], v[104:107]\n" \
B_"v50, s[8:11], s64 offen offset:1024 sc1 lds\n" \
M_"v[112:115], a[128:131], a[32:35], v[40:43]\n" \
"s_xor_b32 m0, s65, 0x6000\n" \
C_"v86, v84, v85\n" \
M_"v[112:115], a[132:135], a[36:39], v[112:115]\n" \
C_"v85, v82, v83\n" \
C_"v84, v80, v81\n" \
M_"v[112:115], a[136:139], a[40:43], v[112:115]\n" \
"v_exp_f32_e64 v89, v89\n" \
M_"v[112:115], a[140:143], a[44:47], v[112:115]\n" \
"v_exp_f32_e64 v90, v90\n" \
M_"v[116:119], a[156:159], a[44:47], v[40:43]\n" \
"v_exp_f32_e64 v91, v91\n" \
M_"v[116:119], a[152:155], a[40:43], v[116:119]\n" \
"v_exp_f32_e64 v92, v92\n" \
M_"v[116:119], a[148:151], a[36:39], v[116:119]\n" \
"v_exp_f32_e64 v93, v93\n" \
M_"v[116:119], a[144:147], a[32:35], v[116:119]\n" \
"v_exp_f32_e64 v94, v94\n" \
M_"v[124:127], a[144:147], a[48:51], v[44:47]\n" \
C_"v88, v88, v89\n" \
C_"v89, v90, v91\n" \
M_"v[124:127], a[148:151], a[52:55], v[124:127]\n" \
"v_exp_f32_e64 v95, v95\n" \
M_"v[124:127], a[152:155], a[56:59], v[124:127]\n" \
C_"v90, v92, v93\n" \
C_"v91, v94, v95\n" \
M_"v[124:127], a[156:159], a[60:63], v[124:127]\n" \
R_"a[144:147], v48 offset:4096\n" \
R_"a[148:151], v48 offset:5120\n" \
M_"v[120:123], a[140:143], a[60:63], v[44:47]\n" \
R_"a[152:155], v48 offset:6144\n" \
R_"a[140:143], v48 offset:3072\n" \
M_"v[120:123], a[136:139], a[56:59], v[120:123]\n" \
R_"a[136:139], v48 offset:2048\n" \
R_"a[156:159], v48 offset:7168\n" \
M_"v[120:123], a[132:135], a[52:55], v[120:123]\n" \
B_"v51, s[8:11], s64 offen offset:0 sc1 lds\n" \
M_"v[120:123], a[128:131], a[48:51], v[120:123]\n" \
R_"a[128:131], v48 offset:0\n" \
"s_waitcnt lgkmcnt(7)\n" \
M_"v[128:131], a[160:163], v[68:71], v[128:131]\n" \
E_"v102, v102\n" \
M_"v[160:163], a[160:163], v[72:75], v[160:163]\n" \
"v_exp_f32_e64 v103, v103\n" \
M_"v[132:135], a[164:167], v[68:71], v[132:135]\n" \
R_"a[132:135], v48 offset:1024\n" \
C_"v103, v102, v103\n" \
M_"v[164:167], a[164:167], v[72:75], v[164:167]\n" \
"v_exp_f32_e64 v100, v100\n" \
M_"v[136:139], a[168:171], v[68:71], v[136:139]\n" \
"v_exp_f32_e64 v101, v101\n" \
M_"v[168:171], a[168:171], v[72:75], v[168:171]\n" \
"v_exp_f32_e64 v98, v98\n" \
M_"v[140:143], a[172:175], v[68:71], v[140:143]\n" \
"v_exp_f32_e64 v99, v99\n" \
M_"v[172:175], a[172:175], v[72:75], v[172:175]\n" \
"v_exp_f32_e64 v96, v96\n" \
M_"v[144:147], a[176:179], v[68:71], v[144:147]\n" \
"v_exp_f32_e64 v97, v97\n" \
M_"v[176:179], a[176:179], v[72:75], v[176:179]\n" \
"v_exp_f32_e64 v104, v104\n" \
M_"v[148:151], a[180:183], v[68:71], v[148:151]\n" \
"v_exp_f32_e64 v105, v105\n" \
M_"v[180:183], a[180:183], v[72:75], v[180:183]\n" \
C_"v102, v100, v101\n" \
C_"v101, v98, v99\n" \
M_"v[152:155], a[184:187], v[68:71], v[152:155]\n" \
C_"v100, v96, v97\n" \
C_"v104, v104, v105\n" \
M_"v[184:187], a[184:187], v[72:75], v[184:187]\n" \
B_"v51, s[8:11], s64 offen offset:1024 sc1 lds\n" \
M_"v[156:159], a[188:191], v[68:71], v[156:159]\n" \
"v_exp_f32_e64 v106, v106\n" \
M_"v[188:191], a[188:191], v[72:75], v[188:191]\n" \
"v_exp_f32_e64 v107, v107\n" \
"v_smfmac_f32_16x16x64_bf16 a[96:99], a[104:107], v[68:75], v21\n" \
"v_exp_f32_e64 v108, v108\n" \
M_"v[220:223], a[188:191], v[84:87], v[220:223]\n" \
"v_exp_f32_e64 v109, v109\n" \
M_"v[252:255], a[188:191], v[88:91], v[252:255]\n" \
"v_exp_f32_e64 v110, v110\n" \
M_"v[216:219], a[184:187], v[84:87], v[216:219]\n" \
"v_exp_f32_e64 v111, v111\n" \
M_"v[248:251], a[184:187], v[88:91], v[248:251]\n" \
"v_exp_f32_e64 v118, v118\n" \
M_"v[212:215], a[180:183], v[84:87], v[212:215]\n" \
"v_exp_f32_e64 v119, v119\n" \
M_"v[244:247], a[180:183], v[88:91], v[244:247]\n" \
C_"v105, v106, v107\n" \
C_"v106, v108, v109\n" \
M_"v[208:211], a[176:179], v[84:87], v[208:211]\n" \
C_"v107, v110, v111\n" \
C_"v119, v118, v119\n" \
M_"v[240:243], a[176:179], v[88:91], v[240:243]\n" \
"v_exp_f32_e64 v116, v116\n" \
M_"v[204:207], a[172:175], v[84:87], v[204:207]\n" \
"v_exp_f32_e64 v117, v117\n" \
M_"v[236:239], a[172:175], v[88:91], v[236:239]\n" \
"v_exp_f32_e64 v114, v114\n" \
M_"v[200:203], a[168:171], v[84:87], v[200:203]\n" \
"v_exp_f32_e64 v115, v115\n" \
M_"v[232:235], a[168:171], v[88:91], v[232:235]\n" \
"v_exp_f32_e64 v112, v112\n" \
M_"v[196:199], a[164:167], v[84:87], v[196:199]\n" \
"v_exp_f32_e64 v113, v113\n" \
M_"v[228:231], a[164:167], v[88:91], v[228:231]\n" \
"v_exp_f32_e64 v120, v120\n" \
M_"v[192:195], a[160:163], v[84:87], v[192:195]\n" \
"v_exp_f32_e64 v121, v121\n" \
M_"v[224:227], a[160:163], v[88:91], v[224:227]\n" \
C_"v118, v116, v117\n" \
"s_waitcnt vmcnt(4) lgkmcnt(0)\n" \
"v_smfmac_f32_16x16x64_bf16 a[100:103], a[104:107], v[84:91], v21\n" \
"s_barrier\n" \
M_"v[64:67], a[128:131], a[0:3], v[32:35]\n" \
"s_xor_b32 m0, s65, 0x14000\n" \
R_"a[160:163], v49 offset:8192\n" \
M_"v[64:67], a[132:135], a[4:7], v[64:67]\n" \
B_"v52, s[8:11], s64 offen offset:0 sc1 lds\n" \
M_"v[64:67], a[136:139], a[8:11], v[64:67]\n" \
R_"a[164:167], v49 offset:9216\n" \
R_"a[168:171], v49 offset:10240\n" \
M_"v[64:67], a[140:143], a[12:15], v[64:67]\n" \
R_"a[172:175], v49 offset:11264\n" \
R_"a[176:179], v49 offset:12288\n" \
M_"v[68:71], a[156:159], a[12:15], v[32:35]\n" \
R_"a[180:183], v49 offset:13312\n" \
R_"a[184:187], v49 offset:14336\n" \
M_"v[68:71], a[152:155], a[8:11], v[68:71]\n" \
R_"a[188:191], v49 offset:15360\n" \
C_"v117, v114, v115\n" \
M_"v[68:71], a[148:151], a[4:7], v[68:71]\n" \
C_"v116, v112, v113\n" \
C_"v120, v120, v121\n" \
M_"v[68:71], a[144:147], a[0:3], v[68:71]\n" \
"v_exp_f32_e64 v122, v122\n" \
M_"v[76:79], a[144:147], a[16:19], v[36:39]\n" \
"v_exp_f32_e64 v123, v123\n" \
M_"v[76:79], a[148:151], a[20:23], v[76:79]\n" \
"v_exp_f32_e64 v124, v124\n" \
M_"v[76:79], a[152:155], a[24:27], v[76:79]\n" \
"v_exp_f32_e64 v125, v125\n" \
M_"v[76:79], a[156:159], a[28:31], v[76:79]\n" \
"v_exp_f32_e64 v126, v126\n" \
M_"v[72:75], a[140:143], a[28:31], v[36:39]\n" \
"v_exp_f32_e64 v127, v127\n" \
M_"v[72:75], a[136:139], a[24:27], v[72:75]\n" \
C_"v121, v122, v123\n" \
C_"v122, v124, v125\n" \
M_"v[72:75], a[132:135], a[20:23], v[72:75]\n" \
C_"v123, v126, v127\n" \
"v_add_u32_e32 v49, 0x10000, v48\n" \
M_"v[72:75], a[128:131], a[16:19], v[72:75]\n" \
B_"v52, s[8:11], s64 offen offset:1024 sc1 lds\n" \
M_"v[80:83], a[128:131], a[32:35], v[40:43]\n" \
"v_exp_f32_e64 v70, v70\n" \
M_"v[80:83], a[132:135], a[36:39], v[80:83]\n" \
"v_exp_f32_e64 v71, v71\n" \
M_"v[80:83], a[136:139], a[40:43], v[80:83]\n" \
"s_mov_b32 m0, s65\n" \
C_"v71, v70, v71\n" \
M_"v[80:83], a[140:143], a[44:47], v[80:83]\n" \
E_"v72, v72\n" \
M_"v[84:87], a[156:159], a[44:47], v[40:43]\n" \
"v_exp_f32_e64 v73, v73\n" \
M_"v[84:87], a[152:155], a[40:43], v[84:87]\n" \
"v_exp_f32_e64 v68, v68\n" \
M_"v[84:87], a[148:151], a[36:39], v[84:87]\n" \
"v_exp_f32_e64 v69, v69\n" \
M_"v[84:87], a[144:147], a[32:35], v[84:87]\n" \
"v_exp_f32_e64 v74, v74\n" \
M_"v[92:95], a[144:147], a[48:51], v[44:47]\n" \
R_"a[144:147], v48 offset:12288\n" \
C_"v72, v72, v73\n" \
M_"v[92:95], a[148:151], a[52:55], v[92:95]\n" \
R_"a[148:151], v48 offset:13312\n" \
C_"v70, v68, v69\n" \
M_"v[92:95], a[152:155], a[56:59], v[92:95]\n" \
R_"a[152:155], v48 offset:14336\n" \
"s_xor_b32 s65, s65, 0x4000\n" \
M_"v[92:95], a[156:159], a[60:63], v[92:95]\n" \
"v_exp_f32_e64 v75, v75\n" \
M_"v[88:91], a[140:143], a[60:63], v[44:47]\n" \
R_"a[140:143], v48 offset:11264\n" \
R_"a[156:159], v48 offset:15360\n" \
M_"v[88:91], a[136:139], a[56:59], v[88:91]\n" \
R_"a[136:139], v48 offset:10240\n" \
C_"v73, v74, v75\n" \
M_"v[88:91], a[132:135], a[52:55], v[88:91]\n" \
B_"v53, s[8:11], s64 offen offset:0 sc1 lds\n" \
M_"v[88:91], a[128:131], a[48:51], v[88:91]\n" \
R_"a[128:131], v48 offset:8192\n" \
"s_waitcnt lgkmcnt(7)\n" \
M_"v[128:131], a[160:163], v[100:103], v[128:131]\n" \
E_"v66, v66\n" \
M_"v[160:163], a[160:163], v[104:107], v[160:163]\n" \
"v_exp_f32_e64 v67, v67\n" \
M_"v[132:135], a[164:167], v[100:103], v[132:135]\n" \
R_"a[132:135], v48 offset:9216\n" \
C_"v69, v66, v67\n" \
M_"v[164:167], a[164:167], v[104:107], v[164:167]\n" \
"v_exp_f32_e64 v64, v64\n" \
M_"v[136:139], a[168:171], v[100:103], v[136:139]\n" \
"v_exp_f32_e64 v65, v65\n" \
M_"v[168:171], a[168:171], v[104:107], v[168:171]\n" \
"v_exp_f32_e64 v76, v76\n" \
M_"v[140:143], a[172:175], v[100:103], v[140:143]\n" \
"v_exp_f32_e64 v77, v77\n" \
M_"v[172:175], a[172:175], v[104:107], v[172:175]\n" \
"v_exp_f32_e64 v78, v78\n" \
M_"v[144:147], a[176:179], v[100:103], v[144:147]\n" \
"v_exp_f32_e64 v79, v79\n" \
M_"v[176:179], a[176:179], v[104:107], v[176:179]\n" \
C_"v68, v64, v65\n" \
C_"v74, v76, v77\n" \
M_"v[148:151], a[180:183], v[100:103], v[148:151]\n" \
C_"v75, v78, v79\n" \
"v_xor_b32_e32 v48, 0x4000, v48\n" \
M_"v[180:183], a[180:183], v[104:107], v[180:183]\n" \
M_"v[152:155], a[184:187], v[100:103], v[152:155]\n" \
M_"v[184:187], a[184:187], v[104:107], v[184:187]\n" \
B_"v53, s[8:11], s64 offen offset:1024 sc1 lds\n" \
M_"v[156:159], a[188:191], v[100:103], v[156:159]\n" \
"s_add_u32 s64, s64, 0x8000\n" \
"s_sub_u32 s66, s66, 1\n" \
"s_nop 0\n" \
M_"v[188:191], a[188:191], v[104:107], v[188:191]\n" \
"v_smfmac_f32_16x16x64_bf16 a[96:99], a[104:107], v[100:107], v21\n" \
M_"v[220:223], a[188:191], v[116:119], v[220:223]\n" \
M_"v[252:255], a[188:191], v[120:123], v[252:255]\n" \
M_"v[216:219], a[184:187], v[116:119], v[216:219]\n" \
M_"v[248:251], a[184:187], v[120:123], v[248:251]\n" \
M_"v[212:215], a[180:183], v[116:119], v[212:215]\n" \
M_"v[244:247], a[180:183], v[120:123], v[244:247]\n" \
M_"v[208:211], a[176:179], v[116:119], v[208:211]\n" \
M_"v[240:243], a[176:179], v[120:123], v[240:243]\n" \
M_"v[204:207], a[172:175], v[116:119], v[204:207]\n" \
M_"v[236:239], a[172:175], v[120:123], v[236:239]\n" \
M_"v[200:203], a[168:171], v[116:119], v[200:203]\n" \
M_"v[232:235], a[168:171], v[120:123], v[232:235]\n" \
M_"v[196:199], a[164:167], v[116:119], v[196:199]\n" \
M_"v[228:231], a[164:167], v[120:123], v[228:231]\n" \
M_"v[192:195], a[160:163], v[116:119], v[192:195]\n" \
M_"v[224:227], a[160:163], v[120:123], v[224:227]\n" \
"s_cbranch_scc0 L_tL_1_%=\n" \
"s_waitcnt vmcnt(4) lgkmcnt(0)\n" \
"v_smfmac_f32_16x16x64_bf16 a[100:103], a[104:107], v[116:123], v21\n" \
"s_barrier\n" \
"L_tr_%=:\n" \
"s_setpc_b64 s[28:29]\n" \
".fill (L_ab + 4096 - .) / 4, 4, 0xBF800000\n" \
".fill (L_ab + 4096 - .) / 4, 4, 0xBF800000\n" \
"L_ap1:\n" \
"s_setpc_b64 s[88:89]\n" \
"L_gtr:\n" \
M_"v[96:99], a[128:131], a[0:3], v[32:35]\n" \
"s_xor_b32 m0, s65, 0x12000\n" \
"s_mov_b32 s65, s13\n" \
M_"v[96:99], a[132:135], a[4:7], v[96:99]\n" \
B_"v50, s[8:11], s64 offen offset:0 sc1 lds\n" \
M_"v[96:99], a[136:139], a[8:11], v[96:99]\n" \
"v_add_u32_e32 v63, s15, v20\n" \
"s_add_u32 s67, s14, 40960\n" \
M_"v[96:99], a[140:143], a[12:15], v[96:99]\n" \
R_"a[160:163], v49 offset:0\n" \
R_"a[164:167], v49 offset:1024\n" \
M_"v[100:103], a[156:159], a[12:15], v[32:35]\n" \
B_"v50, s[8:11], s64 offen offset:1024 sc1 lds\n" \
M_"v[100:103], a[152:155], a[8:11], v[100:103]\n" \
"v_add_u32_e32 v50, 0x6000, v63\n" \
"s_xor_b32 m0, s65, 0x8000\n" \
M_"v[100:103], a[148:151], a[4:7], v[100:103]\n" \
R_"a[168:171], v49 offset:2048\n" \
R_"a[172:175], v49 offset:3072\n" \
M_"v[100:103], a[144:147], a[0:3], v[100:103]\n" \
R_"a[176:179], v49 offset:4096\n" \
R_"a[180:183], v49 offset:5120\n" \
M_"v[108:111], a[144:147], a[16:19], v[36:39]\n" \
B_"v50, s[8:11], s67 offen offset:0 sc1 lds\n" \
M_"v[108:111], a[148:151], a[20:23], v[108:111]\n" \
R_"a[184:187], v49 offset:6144\n" \
R_"a[188:191], v49 offset:7168\n" \
M_"v[108:111], a[152:155], a[24:27], v[108:111]\n" \
"s_mov_b32 s64, s14\n" \
"v_add_u32_e32 v51, 0x12000, v63\n" \
M_"v[108:111], a[156:159], a[28:31], v[108:111]\n" \
E_"v86, v86\n" \
M_"v[104:107], a[140:143], a[28:31], v[36:39]\n" \
B_"v50, s[8:11], s67 offen offset:1024 sc1 lds\n" \
M_"v[104:107], a[136:139], a[24:27], v[104:107]\n" \
E_"v87, v87\n" \
M_"v[104:107], a[132:135], a[20:23], v[104:107]\n" \
E_"v84, v84\n" \
M_"v[104:107], a[128:131], a[16:19], v[104:107]\n" \
E_"v85, v85\n" \
M_"v[112:115], a[128:131], a[32:35], v[40:43]\n" \
C_"v87, v86, v87\n" \
C_"v86, v84, v85\n" \
M_"v[112:115], a[132:135], a[36:39], v[112:115]\n" \
B_"v50, s[8:11], s67 offen offset:2048 sc1 lds\n" \
M_"v[112:115], a[136:139], a[40:43], v[112:115]\n" \
E_"v82, v82\n" \
M_"v[112:115], a[140:143], a[44:47], v[112:115]\n" \
E_"v83, v83\n" \
M_"v[116:119], a[156:159], a[44:47], v[40:43]\n" \
E_"v80, v80\n" \
M_"v[116:119], a[152:155], a[40:43], v[116:119]\n" \
B_"v50, s[8:11], s67 offen offset:3072 sc1 lds\n" \
M_"v[116:119], a[148:151], a[36:39], v[116:119]\n" \
"s_xor_b32 m0, s65, 0xa000\n" \
C_"v85, v82, v83\n" \
M_"v[116:119], a[144:147], a[32:35], v[116:119]\n" \
E_"v81, v81\n" \
M_"v[124:127], a[144:147], a[48:51], v[44:47]\n" \
C_"v84, v80, v81\n" \
"v_readlane_b32 s93, v11, 22\n" \
M_"v[124:127], a[148:151], a[52:55], v[124:127]\n" \
B_"v51, s[8:11], s64 offen offset:0 sc1 lds\n" \
M_"v[124:127], a[152:155], a[56:59], v[124:127]\n" \
E_"v88, v88\n" \
M_"v[124:127], a[156:159], a[60:63], v[124:127]\n" \
E_"v89, v89\n" \
M_"v[120:123], a[140:143], a[60:63], v[44:47]\n" \
E_"v90, v90\n" \
M_"v[120:123], a[136:139], a[56:59], v[120:123]\n" \
E_"v91, v91\n" \
M_"v[120:123], a[132:135], a[52:55], v[120:123]\n" \
B_"v51, s[8:11], s64 offen offset:1024 sc1 lds\n" \
M_"v[120:123], a[128:131], a[48:51], v[120:123]\n" \
C_"v88, v88, v89\n" \
"s_waitcnt lgkmcnt(0)\n" \
M_"v[128:131], a[160:163], v[68:71], v[128:131]\n" \
E_"v92, v92\n" \
M_"v[160:163], a[160:163], v[72:75], v[160:163]\n" \
E_"v93, v93\n" \
M_"v[132:135], a[164:167], v[68:71], v[132:135]\n" \
B_"v51, s[8:11], s64 offen offset:2048 sc1 lds\n" \
M_"v[164:167], a[164:167], v[72:75], v[164:167]\n" \
E_"v94, v94\n" \
M_"v[136:139], a[168:171], v[68:71], v[136:139]\n" \
E_"v95, v95\n" \
M_"v[168:171], a[168:171], v[72:75], v[168:171]\n" \
C_"v89, v90, v91\n" \
C_"v90, v92, v93\n" \
M_"v[140:143], a[172:175], v[68:71], v[140:143]\n" \
B_"v51, s[8:11], s64 offen offset:3072 sc1 lds\n" \
M_"v[172:175], a[172:175], v[72:75], v[172:175]\n" \
C_"v91, v94, v95\n" \
"v_readlane_b32 s80, v11, 11\n" \
M_"v[144:147], a[176:179], v[68:71], v[144:147]\n" \
"v_readlane_b32 s81, v11, 12\n" \
"s_mov_b32 s82, 0x47fca0\n" \
M_"v[176:179], a[176:179], v[72:75], v[176:179]\n" \
"s_mov_b32 s83, 0x20000\n" \
"v_readlane_b32 s92, v11, 29\n" \
M_"v[148:151], a[180:183], v[68:71], v[148:151]\n" \
"s_add_u32 m0, s93, 0x0\n" \
R_"a[128:131], v48 offset:0\n" \
M_"v[180:183], a[180:183], v[72:75], v[180:183]\n" \
R_"a[144:147], v48 offset:4096\n" \
R_"a[132:135], v48 offset:1024\n" \
M_"v[152:155], a[184:187], v[68:71], v[152:155]\n" \
B_"v0, s[80:83], s92 offen offset:0 lds\n" \
M_"v[184:187], a[184:187], v[72:75], v[184:187]\n" \
R_"a[148:151], v48 offset:5120\n" \
R_"a[136:139], v48 offset:2048\n" \
M_"v[156:159], a[188:191], v[68:71], v[156:159]\n" \
R_"a[152:155], v48 offset:6144\n" \
R_"a[140:143], v48 offset:3072\n" \
M_"v[188:191], a[188:191], v[72:75], v[188:191]\n" \
R_"a[156:159], v48 offset:7168\n" \
"v_add_u32_e32 v52, 0x14000, v63\n" \
"v_smfmac_f32_16x16x64_bf16 a[96:99], a[104:107], v[68:75], v21\n" \
B_"v1, s[80:83], s92 offen offset:1024 lds\n" \
M_"v[220:223], a[188:191], v[84:87], v[220:223]\n" \
E_"v102, v102\n" \
M_"v[252:255], a[188:191], v[88:91], v[252:255]\n" \
E_"v103, v103\n" \
M_"v[216:219], a[184:187], v[84:87], v[216:219]\n" \
B_"v2, s[80:83], s92 offen offset:2048 lds\n" \
M_"v[248:251], a[184:187], v[88:91], v[248:251]\n" \
"v_add_u32_e32 v53, 0x20000, v63\n" \
C_"v103, v102, v103\n" \
M_"v[212:215], a[180:183], v[84:87], v[212:215]\n" \
E_"v100, v100\n" \
M_"v[244:247], a[180:183], v[88:91], v[244:247]\n" \
E_"v101, v101\n" \
M_"v[208:211], a[176:179], v[84:87], v[208:211]\n" \
B_"v3, s[80:83], s92 offen offset:3072 lds\n" \
M_"v[240:243], a[176:179], v[88:91], v[240:243]\n" \
E_"v98, v98\n" \
M_"v[204:207], a[172:175], v[84:87], v[204:207]\n" \
E_"v99, v99\n" \
M_"v[236:239], a[172:175], v[88:91], v[236:239]\n" \
E_"v96, v96\n" \
M_"v[200:203], a[168:171], v[84:87], v[200:203]\n" \
E_"v97, v97\n" \
M_"v[232:235], a[168:171], v[88:91], v[232:235]\n" \
E_"v104, v104\n" \
M_"v[196:199], a[164:167], v[84:87], v[196:199]\n" \
E_"v105, v105\n" \
M_"v[228:231], a[164:167], v[88:91], v[228:231]\n" \
C_"v102, v100, v101\n" \
C_"v101, v98, v99\n" \
M_"v[192:195], a[160:163], v[84:87], v[192:195]\n" \
C_"v100, v96, v97\n" \
C_"v104, v104, v105\n" \
M_"v[224:227], a[160:163], v[88:91], v[224:227]\n" \
E_"v106, v106\n" \
"v_smfmac_f32_16x16x64_bf16 a[100:103], a[104:107], v[84:91], v21\n" \
E_"v107, v107\n" \
"s_waitcnt vmcnt(14) lgkmcnt(0)\n" \
"s_barrier\n" \
M_"v[64:67], a[128:131], a[0:3], v[32:35]\n" \
"s_xor_b32 m0, s65, 0x18000\n" \
R_"a[160:163], v49 offset:8192\n" \
M_"v[64:67], a[132:135], a[4:7], v[64:67]\n" \
B_"v52, s[8:11], s64 offen offset:0 sc1 lds\n" \
M_"v[64:67], a[136:139], a[8:11], v[64:67]\n" \
R_"a[164:167], v49 offset:9216\n" \
R_"a[168:171], v49 offset:10240\n" \
M_"v[64:67], a[140:143], a[12:15], v[64:67]\n" \
R_"a[172:175], v49 offset:11264\n" \
R_"a[176:179], v49 offset:12288\n" \
M_"v[68:71], a[156:159], a[12:15], v[32:35]\n" \
B_"v52, s[8:11], s64 offen offset:1024 sc1 lds\n" \
M_"v[68:71], a[152:155], a[8:11], v[68:71]\n" \
R_"a[180:183], v49 offset:13312\n" \
R_"a[184:187], v49 offset:14336\n" \
M_"v[68:71], a[148:151], a[4:7], v[68:71]\n" \
E_"v108, v108\n" \
M_"v[68:71], a[144:147], a[0:3], v[68:71]\n" \
E_"v109, v109\n" \
M_"v[76:79], a[144:147], a[16:19], v[36:39]\n" \
E_"v110, v110\n" \
M_"v[76:79], a[148:151], a[20:23], v[76:79]\n" \
B_"v52, s[8:11], s64 offen offset:2048 sc1 lds\n" \
M_"v[76:79], a[152:155], a[24:27], v[76:79]\n" \
E_"v111, v111\n" \
M_"v[76:79], a[156:159], a[28:31], v[76:79]\n" \
C_"v105, v106, v107\n" \
C_"v106, v108, v109\n" \
M_"v[72:75], a[140:143], a[28:31], v[36:39]\n" \
C_"v107, v110, v111\n" \
R_"a[188:191], v49 offset:15360\n" \
M_"v[72:75], a[136:139], a[24:27], v[72:75]\n" \
B_"v52, s[8:11], s64 offen offset:3072 sc1 lds\n" \
M_"v[72:75], a[132:135], a[20:23], v[72:75]\n" \
"s_mov_b32 m0, s65\n" \
"v_add_u32_e32 v49, 0x10000, v48\n" \
M_"v[72:75], a[128:131], a[16:19], v[72:75]\n" \
E_"v118, v118\n" \
M_"v[80:83], a[128:131], a[32:35], v[40:43]\n" \
E_"v119, v119\n" \
M_"v[80:83], a[132:135], a[36:39], v[80:83]\n" \
E_"v116, v116\n" \
M_"v[80:83], a[136:139], a[40:43], v[80:83]\n" \
B_"v53, s[8:11], s64 offen offset:0 sc1 lds\n" \
M_"v[80:83], a[140:143], a[44:47], v[80:83]\n" \
E_"v117, v117\n" \
M_"v[84:87], a[156:159], a[44:47], v[40:43]\n" \
C_"v119, v118, v119\n" \
C_"v118, v116, v117\n" \
M_"v[84:87], a[152:155], a[40:43], v[84:87]\n" \
E_"v114, v114\n" \
M_"v[84:87], a[148:151], a[36:39], v[84:87]\n" \
E_"v115, v115\n" \
M_"v[84:87], a[144:147], a[32:35], v[84:87]\n" \
B_"v53, s[8:11], s64 offen offset:1024 sc1 lds\n" \
M_"v[92:95], a[144:147], a[48:51], v[44:47]\n" \
E_"v112, v112\n" \
M_"v[92:95], a[148:151], a[52:55], v[92:95]\n" \
E_"v113, v113\n" \
M_"v[92:95], a[152:155], a[56:59], v[92:95]\n" \
C_"v117, v114, v115\n" \
C_"v116, v112, v113\n" \
M_"v[92:95], a[156:159], a[60:63], v[92:95]\n" \
B_"v53, s[8:11], s64 offen offset:2048 sc1 lds\n" \
M_"v[88:91], a[140:143], a[60:63], v[44:47]\n" \
E_"v120, v120\n" \
M_"v[88:91], a[136:139], a[56:59], v[88:91]\n" \
E_"v121, v121\n" \
M_"v[88:91], a[132:135], a[52:55], v[88:91]\n" \
E_"v122, v122\n" \
M_"v[88:91], a[128:131], a[48:51], v[88:91]\n" \
C_"v120, v120, v121\n" \
"s_waitcnt lgkmcnt(0)\n" \
M_"v[128:131], a[160:163], v[100:103], v[128:131]\n" \
B_"v53, s[8:11], s64 offen offset:3072 sc1 lds\n" \
M_"v[160:163], a[160:163], v[104:107], v[160:163]\n" \
"s_add_u32 m0, s93, 0x1000\n" \
R_"a[128:131], v48 offset:8192\n" \
M_"v[132:135], a[164:167], v[100:103], v[132:135]\n" \
E_"v123, v123\n" \
M_"v[164:167], a[164:167], v[104:107], v[164:167]\n" \
E_"v124, v124\n" \
M_"v[136:139], a[168:171], v[100:103], v[136:139]\n" \
B_"v4, s[80:83], s92 offen offset:0 lds\n" \
M_"v[168:171], a[168:171], v[104:107], v[168:171]\n" \
E_"v125, v125\n" \
M_"v[140:143], a[172:175], v[100:103], v[140:143]\n" \
E_"v126, v126\n" \
M_"v[172:175], a[172:175], v[104:107], v[172:175]\n" \
E_"v127, v127\n" \
M_"v[144:147], a[176:179], v[100:103], v[144:147]\n" \
C_"v121, v122, v123\n" \
C_"v122, v124, v125\n" \
M_"v[176:179], a[176:179], v[104:107], v[176:179]\n" \
B_"v5, s[80:83], s92 offen offset:1024 lds\n" \
M_"v[148:151], a[180:183], v[100:103], v[148:151]\n" \
C_"v123, v126, v127\n" \
R_"a[144:147], v48 offset:12288\n" \
M_"v[180:183], a[180:183], v[104:107], v[180:183]\n" \
R_"a[132:135], v48 offset:9216\n" \
R_"a[148:151], v48 offset:13312\n" \
M_"v[152:155], a[184:187], v[100:103], v[152:155]\n" \
R_"a[136:139], v48 offset:10240\n" \
R_"a[152:155], v48 offset:14336\n" \
M_"v[184:187], a[184:187], v[104:107], v[184:187]\n" \
R_"a[140:143], v48 offset:11264\n" \
R_"a[156:159], v48 offset:15360\n" \
M_"v[156:159], a[188:191], v[100:103], v[156:159]\n" \
B_"v6, s[80:83], s92 offen offset:2048 lds\n" \
M_"v[188:191], a[188:191], v[104:107], v[188:191]\n" \
E_"v70, v70\n" \
"v_smfmac_f32_16x16x64_bf16 a[96:99], a[104:107], v[100:107], v21\n" \
E_"v71, v71\n" \
M_"v[220:223], a[188:191], v[116:119], v[220:223]\n" \
E_"v72, v72\n" \
M_"v[252:255], a[188:191], v[120:123], v[252:255]\n" \
B_"v7, s[80:83], s92 offen offset:3072 lds\n" \
M_"v[216:219], a[184:187], v[116:119], v[216:219]\n" \
"s_add_u32 m0, s93, 0x2000\n" \
C_"v71, v70, v71\n" \
M_"v[248:251], a[184:187], v[120:123], v[248:251]\n" \
E_"v73, v73\n" \
M_"v[212:215], a[180:183], v[116:119], v[212:215]\n" \
E_"v68, v68\n" \
M_"v[244:247], a[180:183], v[120:123], v[244:247]\n" \
E_"v69, v69\n" \
M_"v[208:211], a[176:179], v[116:119], v[208:211]\n" \
B_"v8, s[80:83], s92 offen offset:0 lds\n" \
M_"v[240:243], a[176:179], v[120:123], v[240:243]\n" \
E_"v74, v74\n" \
M_"v[204:207], a[172:175], v[116:119], v[204:207]\n" \
E_"v75, v75\n" \
M_"v[236:239], a[172:175], v[120:123], v[236:239]\n" \
E_"v66, v66\n" \
M_"v[200:203], a[168:171], v[116:119], v[200:203]\n" \
E_"v67, v67\n" \
M_"v[232:235], a[168:171], v[120:123], v[232:235]\n" \
E_"v64, v64\n" \
M_"v[196:199], a[164:167], v[116:119], v[196:199]\n" \
E_"v65, v65\n" \
M_"v[228:231], a[164:167], v[120:123], v[228:231]\n" \
C_"v72, v72, v73\n" \
C_"v70, v68, v69\n" \
M_"v[192:195], a[160:163], v[116:119], v[192:195]\n" \
E_"v76, v76\n" \
M_"v[224:227], a[160:163], v[120:123], v[224:227]\n" \
E_"v77, v77\n" \
"v_smfmac_f32_16x16x64_bf16 a[100:103], a[104:107], v[116:123], v21\n" \
E_"v78, v78\n" \
"s_waitcnt vmcnt(13) lgkmcnt(0)\n" \
"s_barrier\n" \
E_"v79, v79\n" \
C_"v73, v74, v75\n" \
C_"v69, v66, v67\n" \
C_"v68, v64, v65\n" \
C_"v74, v76, v77\n" \
C_"v75, v78, v79\n" \
"v_add_u32_e32 v48, s16, v20\n" \
"s_xor_b32 s65, s65, 0x8000\n" \
"s_add_u32 s64, s64, 0x10000\n" \
"s_getpc_b64 s[86:87]\n" \
"L_hpt_%=:\n" \
"s_add_u32 s86, s86, L_ap8-L_hpt_%=\n" \
"s_addc_u32 s87, s87, 0\n" \
"s_add_u32 s86, s86, s15\n" \
"s_addc_u32 s87, s87, 0\n" \
"s_mov_b64 exec, 0\n" \
"s_swappc_b64 s[88:89], s[86:87]\n" \
"s_mov_b64 exec, -1\n" \
"s_sub_u32 s66, s19, 1\n" \
"s_cbranch_scc1 L_la_%=\n" \
"s_branch L_2_%=\n" \
".fill (L_ab + 6400 - .) / 4, 4, 0xBF800000\n" \
"L_tL_2_%=:\n" \
"s_waitcnt vmcnt(8) lgkmcnt(0)\n" \
"v_smfmac_f32_16x16x64_bf16 a[100:103], a[104:107], v[116:123], v21\n" \
"s_barrier\n" \
"L_2_%=:\n" \
M_"v[96:99], a[128:131], a[64:67], v[32:35]\n" \
"s_xor_b32 m0, s65, 0x12000\n" \
R_"a[160:163], v49 offset:0\n" \
M_"v[96:99], a[132:135], a[68:71], v[96:99]\n" \
B_"v50, s[8:11], s64 offen offset:0 sc1 lds\n" \
M_"v[96:99], a[136:139], a[72:75], v[96:99]\n" \
R_"a[164:167], v49 offset:1024\n" \
R_"a[168:171], v49 offset:2048\n" \
M_"v[96:99], a[140:143], a[76:79], v[96:99]\n" \
R_"a[172:175], v49 offset:3072\n" \
R_"a[176:179], v49 offset:4096\n" \
M_"v[100:103], a[156:159], a[76:79], v[32:35]\n" \
R_"a[180:183], v49 offset:5120\n" \
R_"a[184:187], v49 offset:6144\n" \
M_"v[100:103], a[152:155], a[72:75], v[100:103]\n" \
"v_exp_f32_e64 v86, v86\n" \
M_"v[100:103], a[148:151], a[68:71], v[100:103]\n" \
"v_exp_f32_e64 v87, v87\n" \
M_"v[100:103], a[144:147], a[64:67], v[100:103]\n" \
B_"v50, s[8:11], s64 offen offset:1024 sc1 lds\n" \
M_"v[108:111], a[144:147], a[80:83], v[36:39]\n" \
R_"a[188:191], v49 offset:7168\n" \
C_"v87, v86, v87\n" \
M_"v[108:111], a[148:151], a[84:87], v[108:111]\n" \
"v_exp_f32_e64 v84, v84\n" \
M_"v[108:111], a[152:155], a[88:91], v[108:111]\n" \
"v_exp_f32_e64 v85, v85\n" \
M_"v[108:111], a[156:159], a[92:95], v[108:111]\n" \
"v_exp_f32_e64 v82, v82\n" \
M_"v[104:107], a[140:143], a[92:95], v[36:39]\n" \
"v_exp_f32_e64 v83, v83\n" \
M_"v[104:107], a[136:139], a[88:91], v[104:107]\n" \
"v_exp_f32_e64 v80, v80\n" \
M_"v[104:107], a[132:135], a[84:87], v[104:107]\n" \
"v_exp_f32_e64 v81, v81\n" \
M_"v[104:107], a[128:131], a[80:83], v[104:107]\n" \
B_"v50, s[8:11], s64 offen offset:2048 sc1 lds\n" \
M_"v[112:115], a[128:131], a[192:195], v[40:43]\n" \
"v_exp_f32_e64 v88, v88\n" \
M_"v[112:115], a[132:135], a[196:199], v[112:115]\n" \
"v_exp_f32_e64 v89, v89\n" \
M_"v[112:115], a[136:139], a[200:203], v[112:115]\n" \
C_"v86, v84, v85\n" \
C_"v85, v82, v83\n" \
M_"v[112:115], a[140:143], a[204:207], v[112:115]\n" \
C_"v84, v80, v81\n" \
C_"v88, v88, v89\n" \
M_"v[116:119], a[156:159], a[204:207], v[40:43]\n" \
"v_exp_f32_e64 v90, v90\n" \
M_"v[116:119], a[152:155], a[200:203], v[116:119]\n" \
"v_exp_f32_e64 v91, v91\n" \
M_"v[116:119], a[148:151], a[196:199], v[116:119]\n" \
B_"v50, s[8:11], s64 offen offset:3072 sc1 lds\n" \
M_"v[116:119], a[144:147], a[192:195], v[116:119]\n" \
"s_xor_b32 m0, s65, 0xa000\n" \
C_"v89, v90, v91\n" \
M_"v[124:127], a[144:147], a[208:211], v[44:47]\n" \
"v_exp_f32_e64 v92, v92\n" \
M_"v[124:127], a[148:151], a[212:215], v[124:127]\n" \
"v_exp_f32_e64 v93, v93\n" \
M_"v[124:127], a[152:155], a[216:219], v[124:127]\n" \
"v_exp_f32_e64 v94, v94\n" \
M_"v[124:127], a[156:159], a[220:223], v[124:127]\n" \
"v_exp_f32_e64 v95, v95\n" \
M_"v[120:123], a[140:143], a[220:223], v[44:47]\n" \
C_"v90, v92, v93\n" \
C_"v91, v94, v95\n" \
M_"v[120:123], a[136:139], a[216:219], v[120:123]\n" \
R_"a[144:147], v48 offset:4096\n" \
R_"a[148:151], v48 offset:5120\n" \
M_"v[120:123], a[132:135], a[212:215], v[120:123]\n" \
B_"v51, s[8:11], s64 offen offset:0 sc1 lds\n" \
M_"v[120:123], a[128:131], a[208:211], v[120:123]\n" \
R_"a[128:131], v48 offset:0\n" \
"s_waitcnt lgkmcnt(3)\n" \
M_"v[128:131], a[160:163], v[68:71], v[128:131]\n" \
R_"a[132:135], v48 offset:1024\n" \
R_"a[136:139], v48 offset:2048\n" \
M_"v[160:163], a[160:163], v[72:75], v[160:163]\n" \
R_"a[152:155], v48 offset:6144\n" \
R_"a[140:143], v48 offset:3072\n" \
M_"v[132:135], a[164:167], v[68:71], v[132:135]\n" \
E_"v102, v102\n" \
M_"v[164:167], a[164:167], v[72:75], v[164:167]\n" \
"v_exp_f32_e64 v103, v103\n" \
M_"v[136:139], a[168:171], v[68:71], v[136:139]\n" \
R_"a[156:159], v48 offset:7168\n" \
C_"v103, v102, v103\n" \
M_"v[168:171], a[168:171], v[72:75], v[168:171]\n" \
B_"v51, s[8:11], s64 offen offset:1024 sc1 lds\n" \
M_"v[140:143], a[172:175], v[68:71], v[140:143]\n" \
"v_exp_f32_e64 v100, v100\n" \
M_"v[172:175], a[172:175], v[72:75], v[172:175]\n" \
"v_exp_f32_e64 v101, v101\n" \
M_"v[144:147], a[176:179], v[68:71], v[144:147]\n" \
"v_exp_f32_e64 v98, v98\n" \
M_"v[176:179], a[176:179], v[72:75], v[176:179]\n" \
"v_exp_f32_e64 v99, v99\n" \
M_"v[148:151], a[180:183], v[68:71], v[148:151]\n" \
"v_exp_f32_e64 v96, v96\n" \
M_"v[180:183], a[180:183], v[72:75], v[180:183]\n" \
"v_exp_f32_e64 v97, v97\n" \
M_"v[152:155], a[184:187], v[68:71], v[152:155]\n" \
"v_exp_f32_e64 v104, v104\n" \
M_"v[184:187], a[184:187], v[72:75], v[184:187]\n" \
B_"v51, s[8:11], s64 offen offset:2048 sc1 lds\n" \
M_"v[156:159], a[188:191], v[68:71], v[156:159]\n" \
"v_exp_f32_e64 v105, v105\n" \
M_"v[188:191], a[188:191], v[72:75], v[188:191]\n" \
C_"v102, v100, v101\n" \
C_"v101, v98, v99\n" \
"v_smfmac_f32_16x16x64_bf16 a[96:99], a[104:107], v[68:75], v21\n" \
C_"v100, v96, v97\n" \
C_"v104, v104, v105\n" \
M_"v[220:223], a[188:191], v[84:87], v[220:223]\n" \
"v_exp_f32_e64 v106, v106\n" \
M_"v[252:255], a[188:191], v[88:91], v[252:255]\n" \
"v_exp_f32_e64 v107, v107\n" \
M_"v[216:219], a[184:187], v[84:87], v[216:219]\n" \
"v_exp_f32_e64 v108, v108\n" \
M_"v[248:251], a[184:187], v[88:91], v[248:251]\n" \
B_"v51, s[8:11], s64 offen offset:3072 sc1 lds\n" \
M_"v[212:215], a[180:183], v[84:87], v[212:215]\n" \
"v_exp_f32_e64 v109, v109\n" \
M_"v[244:247], a[180:183], v[88:91], v[244:247]\n" \
"v_exp_f32_e64 v110, v110\n" \
M_"v[208:211], a[176:179], v[84:87], v[208:211]\n" \
"v_exp_f32_e64 v111, v111\n" \
M_"v[240:243], a[176:179], v[88:91], v[240:243]\n" \
"v_exp_f32_e64 v118, v118\n" \
M_"v[204:207], a[172:175], v[84:87], v[204:207]\n" \
"v_exp_f32_e64 v119, v119\n" \
M_"v[236:239], a[172:175], v[88:91], v[236:239]\n" \
C_"v105, v106, v107\n" \
C_"v106, v108, v109\n" \
M_"v[200:203], a[168:171], v[84:87], v[200:203]\n" \
C_"v107, v110, v111\n" \
C_"v119, v118, v119\n" \
M_"v[232:235], a[168:171], v[88:91], v[232:235]\n" \
"v_exp_f32_e64 v116, v116\n" \
M_"v[196:199], a[164:167], v[84:87], v[196:199]\n" \
"v_exp_f32_e64 v117, v117\n" \
M_"v[228:231], a[164:167], v[88:91], v[228:231]\n" \
"v_exp_f32_e64 v114, v114\n" \
M_"v[192:195], a[160:163], v[84:87], v[192:195]\n" \
"v_exp_f32_e64 v115, v115\n" \
M_"v[224:227], a[160:163], v[88:91], v[224:227]\n" \
C_"v118, v116, v117\n" \
"s_waitcnt vmcnt(8) lgkmcnt(0)\n" \
"v_smfmac_f32_16x16x64_bf16 a[100:103], a[104:107], v[84:91], v21\n" \
"s_barrier\n" \
M_"v[64:67], a[128:131], a[64:67], v[32:35]\n" \
"s_xor_b32 m0, s65, 0x18000\n" \
R_"a[160:163], v49 offset:8192\n" \
M_"v[64:67], a[132:135], a[68:71], v[64:67]\n" \
B_"v52, s[8:11], s64 offen offset:0 sc1 lds\n" \
M_"v[64:67], a[136:139], a[72:75], v[64:67]\n" \
R_"a[164:167], v49 offset:9216\n" \
R_"a[168:171], v49 offset:10240\n" \
M_"v[64:67], a[140:143], a[76:79], v[64:67]\n" \
R_"a[172:175], v49 offset:11264\n" \
R_"a[176:179], v49 offset:12288\n" \
M_"v[68:71], a[156:159], a[76:79], v[32:35]\n" \
R_"a[180:183], v49 offset:13312\n" \
R_"a[184:187], v49 offset:14336\n" \
M_"v[68:71], a[152:155], a[72:75], v[68:71]\n" \
R_"a[188:191], v49 offset:15360\n" \
C_"v117, v114, v115\n" \
M_"v[68:71], a[148:151], a[68:71], v[68:71]\n" \
"v_exp_f32_e64 v112, v112\n" \
M_"v[68:71], a[144:147], a[64:67], v[68:71]\n" \
B_"v52, s[8:11], s64 offen offset:1024 sc1 lds\n" \
M_"v[76:79], a[144:147], a[80:83], v[36:39]\n" \
"v_exp_f32_e64 v113, v113\n" \
M_"v[76:79], a[148:151], a[84:87], v[76:79]\n" \
C_"v116, v112, v113\n" \
"v_add_u32_e32 v49, 0x10000, v48\n" \
M_"v[76:79], a[152:155], a[88:91], v[76:79]\n" \
"v_exp_f32_e64 v120, v120\n" \
M_"v[76:79], a[156:159], a[92:95], v[76:79]\n" \
"v_exp_f32_e64 v121, v121\n" \
M_"v[72:75], a[140:143], a[92:95], v[36:39]\n" \
"v_exp_f32_e64 v122, v122\n" \
M_"v[72:75], a[136:139], a[88:91], v[72:75]\n" \
"v_exp_f32_e64 v123, v123\n" \
M_"v[72:75], a[132:135], a[84:87], v[72:75]\n" \
"v_exp_f32_e64 v124, v124\n" \
M_"v[72:75], a[128:131], a[80:83], v[72:75]\n" \
B_"v52, s[8:11], s64 offen offset:2048 sc1 lds\n" \
M_"v[80:83], a[128:131], a[192:195], v[40:43]\n" \
"v_exp_f32_e64 v125, v125\n" \
M_"v[80:83], a[132:135], a[196:199], v[80:83]\n" \
"v_exp_f32_e64 v126, v126\n" \
M_"v[80:83], a[136:139], a[200:203], v[80:83]\n" \
"v_exp_f32_e64 v127, v127\n" \
M_"v[80:83], a[140:143], a[204:207], v[80:83]\n" \
"v_exp_f32_e64 v70, v70\n" \
M_"v[84:87], a[156:159], a[204:207], v[40:43]\n" \
"v_exp_f32_e64 v71, v71\n" \
M_"v[84:87], a[152:155], a[200:203], v[84:87]\n" \
C_"v120, v120, v121\n" \
C_"v121, v122, v123\n" \
M_"v[84:87], a[148:151], a[196:199], v[84:87]\n" \
B_"v52, s[8:11], s64 offen offset:3072 sc1 lds\n" \
M_"v[84:87], a[144:147], a[192:195], v[84:87]\n" \
"s_mov_b32 m0, s65\n" \
"s_nop 0\n" \
C_"v122, v124, v125\n" \
M_"v[92:95], a[144:147], a[208:211], v[44:47]\n" \
C_"v123, v126, v127\n" \
R_"a[144:147], v48 offset:12288\n" \
M_"v[92:95], a[148:151], a[212:215], v[92:95]\n" \
R_"a[148:151], v48 offset:13312\n" \
C_"v71, v70, v71\n" \
M_"v[92:95], a[152:155], a[216:219], v[92:95]\n" \
R_"a[152:155], v48 offset:14336\n" \
"s_xor_b32 s65, s65, 0x8000\n" \
M_"v[92:95], a[156:159], a[220:223], v[92:95]\n" \
"v_exp_f32_e64 v72, v72\n" \
M_"v[88:91], a[140:143], a[220:223], v[44:47]\n" \
R_"a[140:143], v48 offset:11264\n" \
R_"a[156:159], v48 offset:15360\n" \
M_"v[88:91], a[136:139], a[216:219], v[88:91]\n" \
"v_exp_f32_e64 v73, v73\n" \
M_"v[88:91], a[132:135], a[212:215], v[88:91]\n" \
".fill (L_ab + 8192 - .) / 4, 4, 0xBF800000\n" \
"L_ap2:\n" \
"s_cbranch_execz L_ap0\n" \
B_"v53, s[8:11], s64 offen offset:0 sc1 lds\n" \
M_"v[88:91], a[128:131], a[208:211], v[88:91]\n" \
R_"a[128:131], v48 offset:8192\n" \
"s_waitcnt lgkmcnt(6)\n" \
M_"v[128:131], a[160:163], v[100:103], v[128:131]\n" \
R_"a[132:135], v48 offset:9216\n" \
R_"a[136:139], v48 offset:10240\n" \
M_"v[160:163], a[160:163], v[104:107], v[160:163]\n" \
"v_exp_f32_e64 v68, v68\n" \
M_"v[132:135], a[164:167], v[100:103], v[132:135]\n" \
"v_exp_f32_e64 v69, v69\n" \
M_"v[164:167], a[164:167], v[104:107], v[164:167]\n" \
"v_exp_f32_e64 v74, v74\n" \
M_"v[136:139], a[168:171], v[100:103], v[136:139]\n" \
"v_exp_f32_e64 v75, v75\n" \
M_"v[168:171], a[168:171], v[104:107], v[168:171]\n" \
B_"v53, s[8:11], s64 offen offset:1024 sc1 lds\n" \
M_"v[140:143], a[172:175], v[100:103], v[140:143]\n" \
"v_exp_f32_e64 v66, v66\n" \
M_"v[172:175], a[172:175], v[104:107], v[172:175]\n" \
"v_exp_f32_e64 v67, v67\n" \
M_"v[144:147], a[176:179], v[100:103], v[144:147]\n" \
"v_exp_f32_e64 v64, v64\n" \
M_"v[176:179], a[176:179], v[104:107], v[176:179]\n" \
"v_exp_f32_e64 v65, v65\n" \
M_"v[148:151], a[180:183], v[100:103], v[148:151]\n" \
C_"v72, v72, v73\n" \
C_"v70, v68, v69\n" \
M_"v[180:183], a[180:183], v[104:107], v[180:183]\n" \
"v_exp_f32_e64 v76, v76\n" \
M_"v[152:155], a[184:187], v[100:103], v[152:155]\n" \
"v_exp_f32_e64 v77, v77\n" \
M_"v[184:187], a[184:187], v[104:107], v[184:187]\n" \
B_"v53, s[8:11], s64 offen offset:2048 sc1 lds\n" \
M_"v[156:159], a[188:191], v[100:103], v[156:159]\n" \
"v_exp_f32_e64 v78, v78\n" \
M_"v[188:191], a[188:191], v[104:107], v[188:191]\n" \
"v_exp_f32_e64 v79, v79\n" \
"v_smfmac_f32_16x16x64_bf16 a[96:99], a[104:107], v[100:107], v21\n" \
C_"v73, v74, v75\n" \
C_"v69, v66, v67\n" \
M_"v[220:223], a[188:191], v[116:119], v[220:223]\n" \
C_"v68, v64, v65\n" \
C_"v74, v76, v77\n" \
M_"v[252:255], a[188:191], v[120:123], v[252:255]\n" \
C_"v75, v78, v79\n" \
"v_xor_b32_e32 v48, 0x8000, v48\n" \
M_"v[216:219], a[184:187], v[116:119], v[216:219]\n" \
M_"v[248:251], a[184:187], v[120:123], v[248:251]\n" \
B_"v53, s[8:11], s64 offen offset:3072 sc1 lds\n" \
M_"v[212:215], a[180:183], v[116:119], v[212:215]\n" \
"s_add_u32 s64, s64, 0x10000\n" \
"s_sub_u32 s66, s66, 1\n" \
"s_nop 0\n" \
M_"v[244:247], a[180:183], v[120:123], v[244:247]\n" \
M_"v[208:211], a[176:179], v[116:119], v[208:211]\n" \
M_"v[240:243], a[176:179], v[120:123], v[240:243]\n" \
M_"v[204:207], a[172:175], v[116:119], v[204:207]\n" \
M_"v[236:239], a[172:175], v[120:123], v[236:239]\n" \
M_"v[200:203], a[168:171], v[116:119], v[200:203]\n" \
M_"v[232:235], a[168:171], v[120:123], v[232:235]\n" \
M_"v[196:199], a[164:167], v[116:119], v[196:199]\n" \
M_"v[228:231], a[164:167], v[120:123], v[228:231]\n" \
M_"v[192:195], a[160:163], v[116:119], v[192:195]\n" \
M_"v[224:227], a[160:163], v[120:123], v[224:227]\n" \
"s_cbranch_scc0 L_tL_2_%=\n" \
"s_waitcnt vmcnt(8) lgkmcnt(0)\n" \
"v_smfmac_f32_16x16x64_bf16 a[100:103], a[104:107], v[116:123], v21\n" \
"s_barrier\n" \
"L_la_%=:\n" \
M_"v[96:99], a[128:131], a[64:67], v[32:35]\n" \
"v_readlane_b32 s96, v11, 1\n" \
"v_lshrrev_b32_e32 v16, 1, v20\n" \
M_"v[96:99], a[132:135], a[68:71], v[96:99]\n" \
"s_xor_b32 m0, s65, 0x12000\n" \
R_"a[160:163], v49 offset:0\n" \
M_"v[96:99], a[136:139], a[72:75], v[96:99]\n" \
"buffer_load_dwordx2 v[26:27], v16, s[56:59], s96 offen sc1\n" \
M_"v[96:99], a[140:143], a[76:79], v[96:99]\n" \
B_"v50, s[8:11], s64 offen offset:0 sc1 lds\n" \
M_"v[100:103], a[156:159], a[76:79], v[32:35]\n" \
R_"a[164:167], v49 offset:1024\n" \
R_"a[168:171], v49 offset:2048\n" \
M_"v[100:103], a[152:155], a[72:75], v[100:103]\n" \
R_"a[172:175], v49 offset:3072\n" \
R_"a[176:179], v49 offset:4096\n" \
M_"v[100:103], a[148:151], a[68:71], v[100:103]\n" \
R_"a[180:183], v49 offset:5120\n" \
R_"a[184:187], v49 offset:6144\n" \
M_"v[100:103], a[144:147], a[64:67], v[100:103]\n" \
R_"a[188:191], v49 offset:7168\n" \
"s_mov_b32 s63, s62\n" \
M_"v[108:111], a[144:147], a[80:83], v[36:39]\n" \
E_"v86, v86\n" \
M_"v[108:111], a[148:151], a[84:87], v[108:111]\n" \
"v_exp_f32_e64 v87, v87\n" \
M_"v[108:111], a[152:155], a[88:91], v[108:111]\n" \
B_"v50, s[8:11], s64 offen offset:1024 sc1 lds\n" \
M_"v[108:111], a[156:159], a[92:95], v[108:111]\n" \
"v_exp_f32_e64 v84, v84\n" \
M_"v[104:107], a[140:143], a[92:95], v[36:39]\n" \
"v_exp_f32_e64 v85, v85\n" \
M_"v[104:107], a[136:139], a[88:91], v[104:107]\n" \
C_"v87, v86, v87\n" \
C_"v86, v84, v85\n" \
M_"v[104:107], a[132:135], a[84:87], v[104:107]\n" \
"v_exp_f32_e64 v82, v82\n" \
M_"v[104:107], a[128:131], a[80:83], v[104:107]\n" \
"v_exp_f32_e64 v83, v83\n" \
M_"v[112:115], a[128:131], a[192:195], v[40:43]\n" \
"v_exp_f32_e64 v80, v80\n" \
M_"v[112:115], a[132:135], a[196:199], v[112:115]\n" \
"v_exp_f32_e64 v81, v81\n" \
M_"v[112:115], a[136:139], a[200:203], v[112:115]\n" \
B_"v50, s[8:11], s64 offen offset:2048 sc1 lds\n" \
M_"v[112:115], a[140:143], a[204:207], v[112:115]\n" \
C_"v85, v82, v83\n" \
C_"v84, v80, v81\n" \
M_"v[116:119], a[156:159], a[204:207], v[40:43]\n" \
"v_exp_f32_e64 v88, v88\n" \
M_"v[116:119], a[152:155], a[200:203], v[116:119]\n" \
"v_exp_f32_e64 v89, v89\n" \
M_"v[116:119], a[148:151], a[196:199], v[116:119]\n" \
"v_exp_f32_e64 v90, v90\n" \
M_"v[116:119], a[144:147], a[192:195], v[116:119]\n" \
"v_exp_f32_e64 v91, v91\n" \
M_"v[124:127], a[144:147], a[208:211], v[44:47]\n" \
C_"v88, v88, v89\n" \
C_"v89, v90, v91\n" \
M_"v[124:127], a[148:151], a[212:215], v[124:127]\n" \
B_"v50, s[8:11], s64 offen offset:3072 sc1 lds\n" \
M_"v[124:127], a[152:155], a[216:219], v[124:127]\n" \
"v_exp_f32_e64 v92, v92\n" \
M_"v[124:127], a[156:159], a[220:223], v[124:127]\n" \
"v_exp_f32_e64 v93, v93\n" \
M_"v[120:123], a[140:143], a[220:223], v[44:47]\n" \
"v_exp_f32_e64 v94, v94\n" \
M_"v[120:123], a[136:139], a[216:219], v[120:123]\n" \
"v_exp_f32_e64 v95, v95\n" \
M_"v[120:123], a[132:135], a[212:215], v[120:123]\n" \
C_"v90, v92, v93\n" \
C_"v91, v94, v95\n" \
M_"v[120:123], a[128:131], a[208:211], v[120:123]\n" \
"v_readlane_b32 s97, v11, 39\n" \
"s_waitcnt lgkmcnt(0)\n" \
"s_nop 0\n" \
M_"v[128:131], a[160:163], v[68:71], v[128:131]\n" \
R_"a[128:131], v48 offset:0\n" \
R_"a[144:147], v48 offset:4096\n" \
M_"v[160:163], a[160:163], v[72:75], v[160:163]\n" \
R_"a[132:135], v48 offset:1024\n" \
R_"a[148:151], v48 offset:5120\n" \
M_"v[132:135], a[164:167], v[68:71], v[132:135]\n" \
R_"a[136:139], v48 offset:2048\n" \
R_"a[152:155], v48 offset:6144\n" \
M_"v[164:167], a[164:167], v[72:75], v[164:167]\n" \
R_"a[140:143], v48 offset:3072\n" \
R_"a[156:159], v48 offset:7168\n" \
M_"v[136:139], a[168:171], v[68:71], v[136:139]\n" \
"v_add_u32_e32 v17, 0x49000, v20\n" \
"v_readlane_b32 s98, v11, 40\n" \
M_"v[168:171], a[168:171], v[72:75], v[168:171]\n" \
"v_readlane_b32 s93, v11, 23\n" \
"v_readlane_b32 s84, v11, 15\n" \
M_"v[140:143], a[172:175], v[68:71], v[140:143]\n" \
"v_readlane_b32 s85, v11, 16\n" \
"v_readlane_b32 s86, v11, 17\n" \
M_"v[172:175], a[172:175], v[72:75], v[172:175]\n" \
"s_mov_b32 s87, 0x20000\n" \
"v_readlane_b32 s92, v11, 18\n" \
M_"v[144:147], a[176:179], v[68:71], v[144:147]\n" \
"v_exp_f32_e64 v102, v102\n" \
M_"v[176:179], a[176:179], v[72:75], v[176:179]\n" \
"v_exp_f32_e64 v103, v103\n" \
M_"v[148:151], a[180:183], v[68:71], v[148:151]\n" \
"v_exp_f32_e64 v100, v100\n" \
M_"v[180:183], a[180:183], v[72:75], v[180:183]\n" \
"v_exp_f32_e64 v101, v101\n" \
M_"v[152:155], a[184:187], v[68:71], v[152:155]\n" \
C_"v103, v102, v103\n" \
C_"v102, v100, v101\n" \
M_"v[184:187], a[184:187], v[72:75], v[184:187]\n" \
"v_exp_f32_e64 v98, v98\n" \
M_"v[156:159], a[188:191], v[68:71], v[156:159]\n" \
"v_exp_f32_e64 v99, v99\n" \
M_"v[188:191], a[188:191], v[72:75], v[188:191]\n" \
"s_waitcnt vmcnt(4)\n" \
"v_cmp_ne_u64_e32 vcc, s[62:63], v[26:27]\n" \
"v_smfmac_f32_16x16x64_bf16 a[96:99], a[104:107], v[68:75], v21\n" \
"v_exp_f32_e64 v96, v96\n" \
M_"v[220:223], a[188:191], v[84:87], v[220:223]\n" \
"v_exp_f32_e64 v97, v97\n" \
M_"v[252:255], a[188:191], v[88:91], v[252:255]\n" \
"s_cbranch_vccnz L_sol_%=\n" \
"L_rol_%=:\n" \
C_"v101, v98, v99\n" \
B_"a[124:127], v20, s[56:59], s97 offen offset:0 sc1\n" \
M_"v[216:219], a[184:187], v[84:87], v[216:219]\n" \
C_"v100, v96, v97\n" \
"v_readlane_b32 s91, v11, 24\n" \
M_"v[248:251], a[184:187], v[88:91], v[248:251]\n" \
E_"v104, v104\n" \
M_"v[212:215], a[180:183], v[84:87], v[212:215]\n" \
"v_exp_f32_e64 v105, v105\n" \
M_"v[244:247], a[180:183], v[88:91], v[244:247]\n" \
"v_exp_f32_e64 v106, v106\n" \
M_"v[208:211], a[176:179], v[84:87], v[208:211]\n" \
"v_exp_f32_e64 v107, v107\n" \
M_"v[240:243], a[176:179], v[88:91], v[240:243]\n" \
B_"a[228:231], v17, s[56:59], s97 offen offset:0 sc1\n" \
M_"v[204:207], a[172:175], v[84:87], v[204:207]\n" \
C_"v104, v104, v105\n" \
C_"v105, v106, v107\n" \
M_"v[236:239], a[172:175], v[88:91], v[236:239]\n" \
"v_exp_f32_e64 v108, v108\n" \
M_"v[200:203], a[168:171], v[84:87], v[200:203]\n" \
"v_exp_f32_e64 v109, v109\n" \
M_"v[232:235], a[168:171], v[88:91], v[232:235]\n" \
"v_exp_f32_e64 v110, v110\n" \
M_"v[196:199], a[164:167], v[84:87], v[196:199]\n" \
"v_exp_f32_e64 v111, v111\n" \
M_"v[228:231], a[164:167], v[88:91], v[228:231]\n" \
C_"v106, v108, v109\n" \
C_"v107, v110, v111\n" \
M_"v[192:195], a[160:163], v[84:87], v[192:195]\n" \
"v_exp_f32_e64 v118, v118\n" \
M_"v[224:227], a[160:163], v[88:91], v[224:227]\n" \
"s_xor_b32 s65, s65, 0x8000\n" \
"s_waitcnt vmcnt(7) lgkmcnt(0)\n" \
"v_smfmac_f32_16x16x64_bf16 a[100:103], a[104:107], v[84:91], v21\n" \
"s_barrier\n" \
M_"v[64:67], a[128:131], a[64:67], v[32:35]\n" \
B_"a[236:239], v20, s[56:59], s98 offen offset:0 sc1\n" \
M_"v[64:67], a[132:135], a[68:71], v[64:67]\n" \
"s_add_u32 m0, s93, 0x0\n" \
"s_nop 0\n" \
R_"a[160:163], v49 offset:8192\n" \
M_"v[64:67], a[136:139], a[72:75], v[64:67]\n" \
B_"v0, s[84:87], s92 offen offset:0 lds\n" \
M_"v[64:67], a[140:143], a[76:79], v[64:67]\n" \
R_"a[164:167], v49 offset:9216\n" \
R_"a[168:171], v49 offset:10240\n" \
M_"v[68:71], a[156:159], a[76:79], v[32:35]\n" \
R_"a[172:175], v49 offset:11264\n" \
R_"a[176:179], v49 offset:12288\n" \
M_"v[68:71], a[152:155], a[72:75], v[68:71]\n" \
R_"a[180:183], v49 offset:13312\n" \
R_"a[184:187], v49 offset:14336\n" \
M_"v[68:71], a[148:151], a[68:71], v[68:71]\n" \
B_"v1, s[84:87], s92 offen offset:1024 lds\n" \
M_"v[68:71], a[144:147], a[64:67], v[68:71]\n" \
B_"a[244:247], v17, s[56:59], s98 offen offset:0 sc1\n" \
M_"v[76:79], a[144:147], a[80:83], v[36:39]\n" \
R_"a[188:191], v49 offset:15360\n" \
"v_add_u32_e32 v49, 0x10000, v48\n" \
M_"v[76:79], a[148:151], a[84:87], v[76:79]\n" \
"v_exp_f32_e64 v119, v119\n" \
M_"v[76:79], a[152:155], a[88:91], v[76:79]\n" \
"v_exp_f32_e64 v116, v116\n" \
M_"v[76:79], a[156:159], a[92:95], v[76:79]\n" \
"v_exp_f32_e64 v117, v117\n" \
M_"v[72:75], a[140:143], a[92:95], v[36:39]\n" \
C_"v119, v118, v119\n" \
C_"v118, v116, v117\n" \
M_"v[72:75], a[136:139], a[88:91], v[72:75]\n" \
B_"v2, s[84:87], s92 offen offset:2048 lds\n" \
M_"v[72:75], a[132:135], a[84:87], v[72:75]\n" \
"v_exp_f32_e64 v114, v114\n" \
M_"v[72:75], a[128:131], a[80:83], v[72:75]\n" \
"v_exp_f32_e64 v115, v115\n" \
M_"v[80:83], a[128:131], a[192:195], v[40:43]\n" \
B_"a[224:227], v20, s[56:59], s97 offen offset:1024 sc1\n" \
M_"v[80:83], a[132:135], a[196:199], v[80:83]\n" \
"v_exp_f32_e64 v112, v112\n" \
M_"v[80:83], a[136:139], a[200:203], v[80:83]\n" \
"v_exp_f32_e64 v113, v113\n" \
M_"v[80:83], a[140:143], a[204:207], v[80:83]\n" \
C_"v117, v114, v115\n" \
C_"v116, v112, v113\n" \
M_"v[84:87], a[156:159], a[204:207], v[40:43]\n" \
B_"v3, s[84:87], s92 offen offset:3072 lds\n" \
M_"v[84:87], a[152:155], a[200:203], v[84:87]\n" \
"s_add_u32 m0, s91, 0x1000\n" \
"s_add_u32 s64, s64, 0x10000\n" \
M_"v[84:87], a[148:151], a[196:199], v[84:87]\n" \
"v_exp_f32_e64 v120, v120\n" \
M_"v[84:87], a[144:147], a[192:195], v[84:87]\n" \
"v_exp_f32_e64 v121, v121\n" \
M_"v[92:95], a[144:147], a[208:211], v[44:47]\n" \
"v_exp_f32_e64 v122, v122\n" \
M_"v[92:95], a[148:151], a[212:215], v[92:95]\n" \
B_"a[232:235], v17, s[56:59], s97 offen offset:1024 sc1\n" \
M_"v[92:95], a[152:155], a[216:219], v[92:95]\n" \
B_"v4, s[84:87], s92 offen offset:0 lds\n" \
M_"v[92:95], a[156:159], a[220:223], v[92:95]\n" \
"v_exp_f32_e64 v123, v123\n" \
M_"v[88:91], a[140:143], a[220:223], v[44:47]\n" \
C_"v120, v120, v121\n" \
C_"v121, v122, v123\n" \
M_"v[88:91], a[136:139], a[216:219], v[88:91]\n" \
"v_exp_f32_e64 v124, v124\n" \
M_"v[88:91], a[132:135], a[212:215], v[88:91]\n" \
"v_exp_f32_e64 v125, v125\n" \
M_"v[88:91], a[128:131], a[208:211], v[88:91]\n" \
C_"v122, v124, v125\n" \
"s_waitcnt lgkmcnt(0)\n" \
M_"v[128:131], a[160:163], v[100:103], v[128:131]\n" \
E_"v126, v126\n" \
M_"v[160:163], a[160:163], v[104:107], v[160:163]\n" \
B_"v5, s[84:87], s92 offen offset:1024 lds\n" \
M_"v[132:135], a[164:167], v[100:103], v[132:135]\n" \
B_"a[240:243], v20, s[56:59], s98 offen offset:1024 sc1\n" \
M_"v[164:167], a[164:167], v[104:107], v[164:167]\n" \
"v_exp_f32_e64 v127, v127\n" \
M_"v[136:139], a[168:171], v[100:103], v[136:139]\n" \
C_"v123, v126, v127\n" \
R_"a[128:131], v48 offset:8192\n" \
M_"v[168:171], a[168:171], v[104:107], v[168:171]\n" \
R_"a[144:147], v48 offset:12288\n" \
R_"a[132:135], v48 offset:9216\n" \
M_"v[140:143], a[172:175], v[100:103], v[140:143]\n" \
R_"a[148:151], v48 offset:13312\n" \
R_"a[136:139], v48 offset:10240\n" \
M_"v[172:175], a[172:175], v[104:107], v[172:175]\n" \
R_"a[152:155], v48 offset:14336\n" \
R_"a[140:143], v48 offset:11264\n" \
M_"v[144:147], a[176:179], v[100:103], v[144:147]\n" \
B_"v6, s[84:87], s92 offen offset:2048 lds\n" \
M_"v[176:179], a[176:179], v[104:107], v[176:179]\n" \
"v_exp_f32_e64 v70, v70\n" \
M_"v[148:151], a[180:183], v[100:103], v[148:151]\n" \
"v_exp_f32_e64 v71, v71\n" \
M_"v[180:183], a[180:183], v[104:107], v[180:183]\n" \
B_"a[248:251], v17, s[56:59], s98 offen offset:1024 sc1\n" \
M_"v[152:155], a[184:187], v[100:103], v[152:155]\n" \
R_"a[156:159], v48 offset:15360\n" \
C_"v71, v70, v71\n" \
M_"v[184:187], a[184:187], v[104:107], v[184:187]\n" \
"v_exp_f32_e64 v72, v72\n" \
M_"v[156:159], a[188:191], v[100:103], v[156:159]\n" \
B_"v7, s[84:87], s92 offen offset:3072 lds\n" \
M_"v[188:191], a[188:191], v[104:107], v[188:191]\n" \
"s_add_u32 m0, s91, 0x2000\n" \
"v_xor_b32_e32 v48, 0x8000, v48\n" \
"v_smfmac_f32_16x16x64_bf16 a[96:99], a[104:107], v[100:107], v21\n" \
"v_exp_f32_e64 v73, v73\n" \
M_"v[220:223], a[188:191], v[116:119], v[220:223]\n" \
"v_exp_f32_e64 v68, v68\n" \
M_"v[252:255], a[188:191], v[120:123], v[252:255]\n" \
"v_exp_f32_e64 v69, v69\n" \
M_"v[216:219], a[184:187], v[116:119], v[216:219]\n" \
"v_exp_f32_e64 v74, v74\n" \
M_"v[248:251], a[184:187], v[120:123], v[248:251]\n" \
"v_exp_f32_e64 v75, v75\n" \
M_"v[212:215], a[180:183], v[116:119], v[212:215]\n" \
B_"v8, s[84:87], s92 offen offset:0 lds\n" \
M_"v[244:247], a[180:183], v[120:123], v[244:247]\n" \
"v_exp_f32_e64 v66, v66\n" \
M_"v[208:211], a[176:179], v[116:119], v[208:211]\n" \
"v_exp_f32_e64 v67, v67\n" \
M_"v[240:243], a[176:179], v[120:123], v[240:243]\n" \
"v_exp_f32_e64 v64, v64\n" \
M_"v[204:207], a[172:175], v[116:119], v[204:207]\n" \
"v_exp_f32_e64 v65, v65\n" \
M_"v[236:239], a[172:175], v[120:123], v[236:239]\n" \
C_"v72, v72, v73\n" \
C_"v70, v68, v69\n" \
M_"v[200:203], a[168:171], v[116:119], v[200:203]\n" \
"v_exp_f32_e64 v76, v76\n" \
M_"v[232:235], a[168:171], v[120:123], v[232:235]\n" \
"v_exp_f32_e64 v77, v77\n" \
M_"v[196:199], a[164:167], v[116:119], v[196:199]\n" \
"v_exp_f32_e64 v78, v78\n" \
M_"v[228:231], a[164:167], v[120:123], v[228:231]\n" \
"v_exp_f32_e64 v79, v79\n" \
M_"v[192:195], a[160:163], v[116:119], v[192:195]\n" \
C_"v73, v74, v75\n" \
C_"v69, v66, v67\n" \
M_"v[224:227], a[160:163], v[120:123], v[224:227]\n" \
C_"v68, v64, v65\n" \
"s_waitcnt vmcnt(15) lgkmcnt(0)\n" \
"v_smfmac_f32_16x16x64_bf16 a[100:103], a[104:107], v[116:123], v21\n" \
"s_barrier\n" \
C_"v74, v76, v77\n" \
C_"v75, v78, v79\n" \
"s_cmp_lg_u32 s16, 0\n" \
"s_cbranch_scc1 L_f1_%=\n" \
R_"a[160:163], v49 offset:0\n" \
R_"a[164:167], v49 offset:1024\n" \
R_"a[168:171], v49 offset:2048\n" \
R_"a[172:175], v49 offset:3072\n" \
R_"a[176:179], v49 offset:4096\n" \
R_"a[180:183], v49 offset:5120\n" \
R_"a[184:187], v49 offset:6144\n" \
R_"a[188:191], v49 offset:7168\n" \
"v_add_u32_e32 v54, 0x49000, v20\n" \
B_"a[192:195], v20, s[56:59], s60 offen offset:0 sc1\n" \
"v_add_u32_e32 v50, s15, v20\n" \
"v_add_u32_e32 v51, 0x10000, v50\n" \
"ds_read_b32 v60, v18 offset:0\n" \
"ds_read_b32 v61, v18 offset:128\n" \
R_"a[0:3], v49 offset:8192\n" \
R_"a[4:7], v49 offset:9216\n" \
R_"a[8:11], v49 offset:10240\n" \
R_"a[12:15], v49 offset:11264\n" \
R_"a[16:19], v49 offset:12288\n" \
R_"a[20:23], v49 offset:13312\n" \
R_"a[24:27], v49 offset:14336\n" \
R_"a[28:31], v49 offset:15360\n" \
E_"v86, v86\n" \
E_"v87, v87\n" \
E_"v84, v84\n" \
E_"v85, v85\n" \
C_"v87, v86, v87\n" \
B_"a[196:199], v20, s[56:59], s60 offen offset:1024 sc1\n" \
E_"v82, v82\n" \
E_"v83, v83\n" \
E_"v80, v80\n" \
E_"v81, v81\n" \
"s_waitcnt lgkmcnt(15)\n" \
M_"v[128:131], a[160:163], v[68:71], v[128:131]\n" \
C_"v86, v84, v85\n" \
C_"v85, v82, v83\n" \
M_"v[160:163], a[160:163], v[72:75], v[160:163]\n" \
C_"v84, v80, v81\n" \
"v_and_b32_e32 v84, v12, v84\n" \
M_"v[132:135], a[164:167], v[68:71], v[132:135]\n" \
"v_and_b32_e32 v85, v13, v85\n" \
"v_and_b32_e32 v86, v14, v86\n" \
M_"v[164:167], a[164:167], v[72:75], v[164:167]\n" \
B_"a[200:203], v20, s[56:59], s60 offen offset:2048 sc1\n" \
M_"v[136:139], a[168:171], v[68:71], v[136:139]\n" \
C_"v128, v128, v129\n" \
C_"v160, v160, v161\n" \
M_"v[168:171], a[168:171], v[72:75], v[168:171]\n" \
C_"v129, v130, v131\n" \
"s_waitcnt lgkmcnt(10)\n" \
M_"v[140:143], a[172:175], v[68:71], v[140:143]\n" \
C_"v161, v162, v163\n" \
C_"v130, v132, v133\n" \
M_"v[172:175], a[172:175], v[72:75], v[172:175]\n" \
C_"v131, v134, v135\n" \
C_"v162, v164, v165\n" \
M_"v[144:147], a[176:179], v[68:71], v[144:147]\n" \
C_"v163, v166, v167\n" \
C_"v132, v136, v137\n" \
M_"v[176:179], a[176:179], v[72:75], v[176:179]\n" \
B_"a[204:207], v20, s[56:59], s60 offen offset:3072 sc1\n" \
M_"v[148:151], a[180:183], v[68:71], v[148:151]\n" \
C_"v133, v138, v139\n" \
C_"v134, v140, v141\n" \
M_"v[180:183], a[180:183], v[72:75], v[180:183]\n" \
C_"v135, v142, v143\n" \
C_"v164, v168, v169\n" \
M_"v[152:155], a[184:187], v[68:71], v[152:155]\n" \
C_"v165, v170, v171\n" \
C_"v166, v172, v173\n" \
M_"v[184:187], a[184:187], v[72:75], v[184:187]\n" \
C_"v167, v174, v175\n" \
C_"v136, v144, v145\n" \
M_"v[156:159], a[188:191], v[68:71], v[156:159]\n" \
C_"v137, v146, v147\n" \
C_"v138, v148, v149\n" \
M_"v[188:191], a[188:191], v[72:75], v[188:191]\n" \
C_"v139, v150, v151\n" \
C_"v140, v152, v153\n" \
"v_smfmac_f32_16x16x64_bf16 a[96:99], a[104:107], v[68:75], v21\n" \
C_"v141, v154, v155\n" \
C_"v168, v176, v177\n" \
M_"v[120:123], a[128:131], a[208:211], v[44:47]\n" \
C_"v142, v156, v157\n" \
C_"v143, v158, v159\n" \
M_"v[120:123], a[132:135], a[212:215], v[120:123]\n" \
C_"v169, v178, v179\n" \
C_"v170, v180, v181\n" \
C_"v171, v182, v183\n" \
C_"v172, v184, v185\n" \
C_"v173, v186, v187\n" \
C_"v174, v188, v189\n" \
C_"v175, v190, v191\n" \
"ds_write_b128 v50, v[128:131] offset:0\n" \
M_"v[120:123], a[136:139], a[216:219], v[120:123]\n" \
"v_and_b32_e32 v87, v15, v87\n" \
"v_accvgpr_read_b32 v40, a96\n" \
M_"v[120:123], a[140:143], a[220:223], v[120:123]\n" \
"ds_write_b128 v50, v[132:135] offset:1024\n" \
M_"v[124:127], a[156:159], a[220:223], v[44:47]\n" \
B_"a[220:223], v54, s[56:59], s60 offen offset:3072 sc1\n" \
M_"v[124:127], a[152:155], a[216:219], v[124:127]\n" \
"ds_write_b128 v50, v[136:139] offset:2048\n" \
M_"v[124:127], a[148:151], a[212:215], v[124:127]\n" \
"v_accvgpr_read_b32 v22, a97\n" \
"v_accvgpr_read_b32 v41, a98\n" \
M_"v[124:127], a[144:147], a[208:211], v[124:127]\n" \
"ds_write_b128 v50, v[140:143] offset:3072\n" \
M_"a[100:103], a[108:111], v[84:87], a[100:103]\n" \
"v_accvgpr_read_b32 v23, a99\n" \
"v_add_f32_e32 v40, v40, v22\n" \
M_"v[192:195], a[160:163], v[84:87], v[192:195]\n" \
"ds_write_b128 v51, v[160:163] offset:32768\n" \
M_"v[196:199], a[164:167], v[84:87], v[196:199]\n" \
B_"a[208:211], v54, s[56:59], s60 offen offset:0 sc1\n" \
M_"v[200:203], a[168:171], v[84:87], v[200:203]\n" \
"ds_write_b128 v51, v[164:167] offset:33792\n" \
"v_add_f32_e32 v41, v41, v23\n" \
"ds_write_b64 v19, v[40:41]\n" \
M_"v[204:207], a[172:175], v[84:87], v[204:207]\n" \
"v_lshl_add_u32 v52, s15, 2, v20\n" \
"v_add_u32_e32 v52, 0x1a000, v52\n" \
"ds_write_b128 v51, v[168:171] offset:34816\n" \
M_"v[208:211], a[176:179], v[84:87], v[208:211]\n" \
E_"v88, v88\n" \
M_"v[212:215], a[180:183], v[84:87], v[212:215]\n" \
"ds_write_b128 v51, v[172:175] offset:35840\n" \
M_"v[216:219], a[184:187], v[84:87], v[216:219]\n" \
B_"a[212:215], v54, s[56:59], s60 offen offset:1024 sc1\n" \
M_"v[220:223], a[188:191], v[84:87], v[220:223]\n" \
E_"v89, v89\n" \
E_"v90, v90\n" \
E_"v91, v91\n" \
C_"v88, v88, v89\n" \
E_"v92, v92\n" \
E_"v93, v93\n" \
E_"v94, v94\n" \
E_"v95, v95\n" \
C_"v89, v90, v91\n" \
C_"v90, v92, v93\n" \
C_"v91, v94, v95\n" \
"v_xor_b32_e32 v63, 0x400, v19\n" \
B_"a[216:219], v54, s[56:59], s60 offen offset:2048 sc1\n" \
E_"v120, v120\n" \
E_"v121, v121\n" \
E_"v122, v122\n" \
E_"v123, v123\n" \
C_"v120, v120, v121\n" \
E_"v124, v124\n" \
E_"v125, v125\n" \
"s_waitcnt lgkmcnt(0)\n" \
"s_barrier\n" \
R_"v[144:147], v52 offset:0\n" \
R_"v[148:151], v52 offset:1024\n" \
R_"v[152:155], v52 offset:2048\n" \
".fill (L_ab + 12288 - .) / 4, 4, 0xBF800000\n" \
"L_ap3:\n" \
"s_cbranch_execz L_ap0\n" \
R_"v[156:159], v52 offset:3072\n" \
R_"v[176:179], v52 offset:4096\n" \
R_"v[180:183], v52 offset:5120\n" \
R_"v[184:187], v52 offset:6144\n" \
R_"v[188:191], v52 offset:7168\n" \
M_"a[100:103], a[116:119], v[88:91], a[100:103]\n" \
"ds_read_b64 v[42:43], v63\n" \
C_"v121, v122, v123\n" \
M_"v[224:227], a[160:163], v[88:91], v[224:227]\n" \
E_"v126, v126\n" \
M_"v[228:231], a[164:167], v[88:91], v[228:231]\n" \
E_"v127, v127\n" \
M_"v[232:235], a[168:171], v[88:91], v[232:235]\n" \
C_"v122, v124, v125\n" \
C_"v123, v126, v127\n" \
M_"v[236:239], a[172:175], v[88:91], v[236:239]\n" \
"v_and_b32_e32 v120, v12, v120\n" \
"v_and_b32_e32 v121, v13, v121\n" \
M_"v[240:243], a[176:179], v[88:91], v[240:243]\n" \
"v_and_b32_e32 v122, v14, v122\n" \
"v_and_b32_e32 v123, v15, v123\n" \
M_"v[244:247], a[180:183], v[88:91], v[244:247]\n" \
"v_readlane_b32 s93, v11, 25\n" \
"v_readlane_b32 s92, v11, 30\n" \
M_"v[248:251], a[184:187], v[88:91], v[248:251]\n" \
"s_add_u32 m0, s93, 0x0\n" \
"v_mov_b32_e32 v130, v28\n" \
M_"v[252:255], a[188:191], v[88:91], v[252:255]\n" \
"v_mov_b32_e32 v131, v29\n" \
"v_mov_b32_e32 v132, 0\n" \
"v_mov_b32_e32 v133, 0\n" \
"v_mov_b32_e32 v128, 0\n" \
"v_mov_b32_e32 v129, 0\n" \
"s_waitcnt vmcnt(8) lgkmcnt(1)\n" \
"s_barrier\n" \
M_"a[100:103], a[116:119], v[120:123], a[100:103]\n" \
B_"v0, s[80:83], s92 offen offset:0 lds\n" \
M_"v[224:227], a[0:3], v[120:123], v[224:227]\n" \
M_"v[228:231], a[4:7], v[120:123], v[228:231]\n" \
M_"v[232:235], a[8:11], v[120:123], v[232:235]\n" \
M_"v[236:239], a[12:15], v[120:123], v[236:239]\n" \
M_"v[240:243], a[16:19], v[120:123], v[240:243]\n" \
M_"v[244:247], a[20:23], v[120:123], v[244:247]\n" \
B_"v1, s[80:83], s92 offen offset:1024 lds\n" \
M_"v[248:251], a[24:27], v[120:123], v[248:251]\n" \
"v_accvgpr_read_b32 v56, a100\n" \
"v_accvgpr_read_b32 v24, a101\n" \
M_"v[252:255], a[28:31], v[120:123], v[252:255]\n" \
"v_add_f32_e32 v56, v56, v24\n" \
"s_waitcnt lgkmcnt(0)\n" \
"v_add_f32_e32 v56, v56, v42\n" \
M_"v[192:195], v[130:133], v[144:147], v[192:195]\n" \
"v_max_f32_e32 v62, v62, v56\n" \
"v_accvgpr_read_b32 v57, a102\n" \
M_"v[196:199], v[128:131], v[144:147], v[196:199]\n" \
"v_rcp_f32_e32 v56, v56\n" \
M_"v[200:203], v[130:133], v[148:151], v[200:203]\n" \
"v_mul_f32_e32 v56, v56, v60\n" \
"v_accvgpr_read_b32 v25, a103\n" \
B_"v2, s[80:83], s92 offen offset:2048 lds\n" \
M_"v[204:207], v[128:131], v[148:151], v[204:207]\n" \
"v_mul_f32_e32 v192, v56, v192\n" \
"v_mul_f32_e32 v193, v56, v193\n" \
M_"v[208:211], v[130:133], v[152:155], v[208:211]\n" \
"v_mul_f32_e32 v194, v56, v194\n" \
"v_mul_f32_e32 v195, v56, v195\n" \
M_"v[212:215], v[128:131], v[152:155], v[212:215]\n" \
"v_mul_f32_e32 v196, v56, v196\n" \
"v_mul_f32_e32 v197, v56, v197\n" \
M_"v[216:219], v[130:133], v[156:159], v[216:219]\n" \
"v_mul_f32_e32 v198, v56, v198\n" \
"v_mul_f32_e32 v199, v56, v199\n" \
M_"v[220:223], v[128:131], v[156:159], v[220:223]\n" \
C_"v64, v192, v193\n" \
C_"v65, v194, v195\n" \
M_"v[224:227], v[130:133], v[176:179], v[224:227]\n" \
B_"v3, s[80:83], s92 offen offset:3072 lds\n" \
"s_add_u32 m0, s93, 0x1000\n" \
M_"v[228:231], v[128:131], v[176:179], v[228:231]\n" \
C_"v66, v196, v197\n" \
C_"v67, v198, v199\n" \
M_"v[232:235], v[130:133], v[180:183], v[232:235]\n" \
"v_mul_f32_e32 v200, v56, v200\n" \
"v_mul_f32_e32 v201, v56, v201\n" \
M_"v[236:239], v[128:131], v[180:183], v[236:239]\n" \
M_"a[112:115], a[120:123], v[64:67], a[112:115]\n" \
"v_mul_f32_e32 v202, v56, v202\n" \
"v_mul_f32_e32 v203, v56, v203\n" \
M_"v[240:243], v[130:133], v[184:187], v[240:243]\n" \
"v_mul_f32_e32 v204, v56, v204\n" \
"v_mul_f32_e32 v205, v56, v205\n" \
B_"v4, s[80:83], s92 offen offset:0 lds\n" \
"v_mul_f32_e32 v206, v56, v206\n" \
"v_mul_f32_e32 v207, v56, v207\n" \
"v_add_f32_e32 v57, v57, v25\n" \
C_"v68, v200, v201\n" \
C_"v69, v202, v203\n" \
C_"v70, v204, v205\n" \
C_"v71, v206, v207\n" \
"v_add_f32_e32 v57, v57, v43\n" \
"v_mul_f32_e32 v208, v56, v208\n" \
M_"a[112:115], a[120:123], v[68:71], a[112:115]\n" \
"v_mul_f32_e32 v209, v56, v209\n" \
"v_mul_f32_e32 v210, v56, v210\n" \
"v_mul_f32_e32 v211, v56, v211\n" \
"v_mul_f32_e32 v212, v56, v212\n" \
"v_mul_f32_e32 v213, v56, v213\n" \
"v_mul_f32_e32 v214, v56, v214\n" \
"v_mul_f32_e32 v215, v56, v215\n" \
"v_max_f32_e32 v62, v62, v57\n" \
C_"v72, v208, v209\n" \
C_"v73, v210, v211\n" \
B_"v5, s[80:83], s92 offen offset:1024 lds\n" \
C_"v74, v212, v213\n" \
C_"v75, v214, v215\n" \
"v_rcp_f32_e32 v57, v57\n" \
M_"v[244:247], v[128:131], v[184:187], v[244:247]\n" \
M_"a[112:115], a[120:123], v[72:75], a[112:115]\n" \
"v_mul_f32_e32 v216, v56, v216\n" \
"v_mul_f32_e32 v217, v56, v217\n" \
"v_mul_f32_e32 v218, v56, v218\n" \
"v_mul_f32_e32 v219, v56, v219\n" \
"v_mul_f32_e32 v220, v56, v220\n" \
"v_mul_f32_e32 v221, v56, v221\n" \
"v_mul_f32_e32 v222, v56, v222\n" \
"v_mul_f32_e32 v223, v56, v223\n" \
C_"v76, v216, v217\n" \
C_"v77, v218, v219\n" \
C_"v78, v220, v221\n" \
B_"v6, s[80:83], s92 offen offset:2048 lds\n" \
C_"v79, v222, v223\n" \
"v_mul_f32_e32 v57, v57, v61\n" \
"v_mul_f32_e32 v224, v57, v224\n" \
M_"a[112:115], a[120:123], v[76:79], a[112:115]\n" \
"v_mul_f32_e32 v225, v57, v225\n" \
"v_mul_f32_e32 v226, v57, v226\n" \
"v_mul_f32_e32 v227, v57, v227\n" \
"v_mul_f32_e32 v228, v57, v228\n" \
"v_mul_f32_e32 v229, v57, v229\n" \
"v_mul_f32_e32 v230, v57, v230\n" \
"v_mul_f32_e32 v231, v57, v231\n" \
C_"v80, v224, v225\n" \
C_"v81, v226, v227\n" \
C_"v82, v228, v229\n" \
C_"v83, v230, v231\n" \
M_"v[248:251], v[130:133], v[188:191], v[248:251]\n" \
"v_mul_f32_e32 v232, v57, v232\n" \
"v_mul_f32_e32 v233, v57, v233\n" \
M_"a[112:115], a[120:123], v[80:83], a[112:115]\n" \
B_"v7, s[80:83], s92 offen offset:3072 lds\n" \
"s_add_u32 m0, s93, 0x2000\n" \
"v_mul_f32_e32 v234, v57, v234\n" \
"v_mul_f32_e32 v235, v57, v235\n" \
"v_mul_f32_e32 v236, v57, v236\n" \
"v_mul_f32_e32 v237, v57, v237\n" \
"v_mul_f32_e32 v238, v57, v238\n" \
"v_mul_f32_e32 v239, v57, v239\n" \
C_"v84, v232, v233\n" \
C_"v85, v234, v235\n" \
C_"v86, v236, v237\n" \
C_"v87, v238, v239\n" \
"v_mul_f32_e32 v240, v57, v240\n" \
"v_mul_f32_e32 v241, v57, v241\n" \
M_"a[112:115], a[120:123], v[84:87], a[112:115]\n" \
"v_mul_f32_e32 v242, v57, v242\n" \
"v_mul_f32_e32 v243, v57, v243\n" \
"v_mul_f32_e32 v244, v57, v244\n" \
"v_mul_f32_e32 v245, v57, v245\n" \
"v_mul_f32_e32 v246, v57, v246\n" \
"v_mul_f32_e32 v247, v57, v247\n" \
B_"v8, s[80:83], s92 offen offset:0 lds\n" \
C_"v96, v240, v241\n" \
C_"v97, v242, v243\n" \
C_"v98, v244, v245\n" \
C_"v99, v246, v247\n" \
M_"v[252:255], v[128:131], v[188:191], v[252:255]\n" \
"v_mul_f32_e32 v248, v57, v248\n" \
M_"a[112:115], a[120:123], v[96:99], a[112:115]\n" \
"v_mul_f32_e32 v249, v57, v249\n" \
"v_mul_f32_e32 v250, v57, v250\n" \
"v_mul_f32_e32 v251, v57, v251\n" \
C_"v100, v248, v249\n" \
C_"v101, v250, v251\n" \
"v_cmp_lt_f32_e64 s[72:73], s21, v62\n" \
"v_mul_f32_e32 v252, v57, v252\n" \
"v_mul_f32_e32 v253, v57, v253\n" \
"v_mul_f32_e32 v254, v57, v254\n" \
"v_mul_f32_e32 v255, v57, v255\n" \
C_"v102, v252, v253\n" \
C_"v103, v254, v255\n" \
"s_nop 1\n" \
M_"a[112:115], a[120:123], v[100:103], a[112:115]\n" \
"s_nop 7\n" \
"v_accvgpr_read_b32 v63, a112\n" \
"v_cmp_u_f32_e32 vcc, v63, v63\n" \
"s_nop 0\n" \
"s_or_b64 vcc, s[72:73], vcc\n" \
"s_cbranch_scc1 L_end_%=\n" \
"s_branch L_out_%=\n" \
"L_skb_%=:\n" \
"s_sleep 1\n" \
"buffer_load_dword v57, v61, s[56:59], s96 offen sc1\n" \
"s_add_u32 s101, s101, 1\n" \
"s_waitcnt vmcnt(0)\n" \
"v_cmp_ne_u32_e32 vcc, s62, v57\n" \
"s_cmp_gt_u32 s101, 0x100000\n" \
"s_cbranch_scc1 L_rkb_%=\n" \
"s_nop 2\n" \
"s_cbranch_vccnz L_skb_%=\n" \
"s_branch L_rkb_%=\n" \
"L_sol_%=:\n" \
"s_sleep 1\n" \
"buffer_load_dwordx2 v[26:27], v16, s[56:59], s96 offen sc1\n" \
"s_add_u32 s101, s101, 1\n" \
"s_waitcnt vmcnt(0)\n" \
"v_cmp_ne_u64_e32 vcc, s[62:63], v[26:27]\n" \
"s_cmp_gt_u32 s101, 0x100000\n" \
"s_cbranch_scc1 L_rol_%=\n" \
"s_nop 2\n" \
"s_cbranch_vccnz L_sol_%=\n" \
"s_branch L_rol_%=\n" \
"L_sos_%=:\n" \
"s_sleep 1\n" \
"buffer_load_dwordx2 v[26:27], v16, s[56:59], s96 offen sc1\n" \
"s_add_u32 s101, s101, 1\n" \
"s_waitcnt vmcnt(0)\n" \
"v_cmp_ne_u64_e32 vcc, s[62:63], v[26:27]\n" \
"s_cmp_gt_u32 s101, 0x100000\n" \
"s_cbranch_scc1 L_ros_%=\n" \
"s_nop 2\n" \
"s_cbranch_vccnz L_sos_%=\n" \
"s_branch L_ros_%=\n" \
"L_sa_%=:\n" \
"s_sleep 1\n" \
"buffer_load_dword v26, v38, s[56:59], s64 offen sc1\n" \
"s_add_u32 s101, s101, 1\n" \
"s_waitcnt vmcnt(0)\n" \
"v_cmp_ne_u32_e32 vcc, s62, v26\n" \
"s_cmp_gt_u32 s101, 0x100000\n" \
"s_cbranch_scc1 L_ra_%=\n" \
"s_nop 2\n" \
"s_cbranch_vccnz L_sa_%=\n" \
"s_branch L_ra_%=\n" \
"L_sb_%=:\n" \
"s_sleep 1\n" \
"buffer_load_dword v47, v38, s[56:59], s71 offen sc1\n" \
"s_add_u32 s101, s101, 1\n" \
"s_waitcnt vmcnt(0)\n" \
"v_cmp_ne_u32_e32 vcc, s62, v47\n" \
"s_cmp_gt_u32 s101, 0x100000\n" \
"s_cbranch_scc1 L_rb_%=\n" \
"s_nop 2\n" \
"s_cbranch_vccnz L_sb_%=\n" \
"s_branch L_rb_%=\n" \
"L_sna_%=:\n" \
"buffer_load_dword v37, v40, s[56:59], s74 offen sc1\n" \
"s_add_u32 s101, s101, 1\n" \
"s_waitcnt vmcnt(0)\n" \
"v_cmp_eq_u32_e64 s[98:99], s62, v37\n" \
"s_cmp_gt_u32 s101, 0x100000\n" \
"s_cbranch_scc1 L_rna_%=\n" \
"s_andn2_b32 s75, s20, s98\n" \
"s_cbranch_scc0 L_gna_%=\n" \
"s_sleep 1\n" \
"s_branch L_sna_%=\n" \
"L_gna_%=:\n" \
"s_bitcmp1_b32 s20, 0\n" \
"s_cbranch_scc0 L_kna0_%=\n" \
B_"v[116:119], v20, s[56:59], s12 offen offset:0 sc1\n" \
"L_kna0_%=:\n" \
"s_bitcmp1_b32 s20, 1\n" \
"s_cbranch_scc0 L_kna1_%=\n" \
B_"v[124:127], v20, s[56:59], s12 offen offset:2048 sc1\n" \
"L_kna1_%=:\n" \
"s_bitcmp1_b32 s20, 2\n" \
"s_cbranch_scc0 L_kna2_%=\n" \
B_"v[132:135], v20, s[56:59], s13 offen offset:0 sc1\n" \
"L_kna2_%=:\n" \
"s_bitcmp1_b32 s20, 3\n" \
"s_cbranch_scc0 L_kna3_%=\n" \
B_"v[140:143], v20, s[56:59], s13 offen offset:2048 sc1\n" \
"L_kna3_%=:\n" \
"s_bitcmp1_b32 s20, 4\n" \
"s_cbranch_scc0 L_kna4_%=\n" \
B_"v[148:151], v20, s[56:59], s14 offen offset:0 sc1\n" \
"L_kna4_%=:\n" \
"s_bitcmp1_b32 s20, 5\n" \
"s_cbranch_scc0 L_kna5_%=\n" \
B_"v[156:159], v20, s[56:59], s14 offen offset:2048 sc1\n" \
"L_kna5_%=:\n" \
"s_bitcmp1_b32 s20, 6\n" \
"s_cbranch_scc0 L_kna6_%=\n" \
B_"v[164:167], v20, s[56:59], s15 offen offset:0 sc1\n" \
"L_kna6_%=:\n" \
"s_waitcnt vmcnt(0)\n" \
"s_branch L_rna_%=\n" \
"L_snx_%=:\n" \
"buffer_load_dword v37, v40, s[56:59], s74 offen sc1\n" \
"s_add_u32 s101, s101, 1\n" \
"s_waitcnt vmcnt(0)\n" \
"v_cmp_eq_u32_e64 s[98:99], s62, v37\n" \
"s_cmp_gt_u32 s101, 0x100000\n" \
"s_cbranch_scc1 L_rnx_%=\n" \
"s_andn2_b32 s75, s21, s98\n" \
"s_cbranch_scc0 L_gnx_%=\n" \
"s_sleep 1\n" \
"s_branch L_snx_%=\n" \
"L_gnx_%=:\n" \
"s_bitcmp1_b32 s21, 16\n" \
"s_cbranch_scc0 L_knx16_%=\n" \
B_"v[112:115], v20, s[56:59], s16 offen offset:0 sc1\n" \
"L_knx16_%=:\n" \
"s_bitcmp1_b32 s21, 17\n" \
"s_cbranch_scc0 L_knx17_%=\n" \
B_"v[184:187], v20, s[56:59], s16 offen offset:2048 sc1\n" \
"L_knx17_%=:\n" \
"s_bitcmp1_b32 s21, 18\n" \
"s_cbranch_scc0 L_knx18_%=\n" \
B_"v[120:123], v20, s[56:59], s17 offen offset:0 sc1\n" \
"L_knx18_%=:\n" \
"s_bitcmp1_b32 s21, 19\n" \
"s_cbranch_scc0 L_knx19_%=\n" \
B_"v[192:195], v20, s[56:59], s17 offen offset:2048 sc1\n" \
"L_knx19_%=:\n" \
"s_bitcmp1_b32 s21, 20\n" \
"s_cbranch_scc0 L_knx20_%=\n" \
B_"v[128:131], v20, s[56:59], s18 offen offset:0 sc1\n" \
"L_knx20_%=:\n" \
"s_bitcmp1_b32 s21, 21\n" \
"s_cbranch_scc0 L_knx21_%=\n" \
B_"v[200:203], v20, s[56:59], s18 offen offset:2048 sc1\n" \
"L_knx21_%=:\n" \
"s_bitcmp1_b32 s21, 22\n" \
"s_cbranch_scc0 L_knx22_%=\n" \
B_"v[176:179], v20, s[56:59], s19 offen offset:0 sc1\n" \
"L_knx22_%=:\n" \
"s_bitcmp1_b32 s21, 23\n" \
"s_cbranch_scc0 L_knx23_%=\n" \
B_"v[248:251], v20, s[56:59], s19 offen offset:2048 sc1\n" \
"L_knx23_%=:\n" \
"s_bitcmp1_b32 s21, 8\n" \
"s_cbranch_scc0 L_knx8_%=\n" \
B_"v[188:191], v20, s[56:59], s12 offen offset:1024 sc1\n" \
"L_knx8_%=:\n" \
"s_bitcmp1_b32 s21, 9\n" \
"s_cbranch_scc0 L_knx9_%=\n" \
B_"v[196:199], v20, s[56:59], s12 offen offset:3072 sc1\n" \
"L_knx9_%=:\n" \
"s_waitcnt vmcnt(0)\n" \
"s_branch L_rnx_%=\n" \
"L_sx0_%=:\n" \
"s_sleep 1\n" \
"buffer_load_dword v37, v40, s[56:59], s74 offen sc1\n" \
"s_add_u32 s101, s101, 1\n" \
"s_waitcnt vmcnt(0)\n" \
"v_cmp_eq_u32_e64 s[98:99], s62, v37\n" \
"s_cmp_gt_u32 s101, 0x100000\n" \
"s_cbranch_scc1 L_rx0_%=\n" \
"s_andn2_b32 s75, 0x400, s98\n" \
"s_cbranch_scc1 L_sx0_%=\n" \
"s_branch L_rx0_%=\n" \
"L_sx1_%=:\n" \
"s_sleep 1\n" \
"buffer_load_dword v37, v40, s[56:59], s74 offen sc1\n" \
"s_add_u32 s101, s101, 1\n" \
"s_waitcnt vmcnt(0)\n" \
"v_cmp_eq_u32_e64 s[98:99], s62, v37\n" \
"s_cmp_gt_u32 s101, 0x100000\n" \
"s_cbranch_scc1 L_rx1_%=\n" \
"s_andn2_b32 s75, 0x800, s98\n" \
"s_cbranch_scc1 L_sx1_%=\n" \
"s_branch L_rx1_%=\n" \
"L_sx2_%=:\n" \
"s_sleep 1\n" \
"buffer_load_dword v37, v40, s[56:59], s74 offen sc1\n" \
"s_add_u32 s101, s101, 1\n" \
"s_waitcnt vmcnt(0)\n" \
"v_cmp_eq_u32_e64 s[98:99], s62, v37\n" \
"s_cmp_gt_u32 s101, 0x100000\n" \
"s_cbranch_scc1 L_rx2_%=\n" \
"s_andn2_b32 s75, 0x1000, s98\n" \
"s_cbranch_scc1 L_sx2_%=\n" \
"s_branch L_rx2_%=\n" \
"L_sx3_%=:\n" \
"s_sleep 1\n" \
"buffer_load_dword v37, v40, s[56:59], s74 offen sc1\n" \
"s_add_u32 s101, s101, 1\n" \
"s_waitcnt vmcnt(0)\n" \
"v_cmp_eq_u32_e64 s[98:99], s62, v37\n" \
"s_cmp_gt_u32 s101, 0x100000\n" \
"s_cbranch_scc1 L_rx3_%=\n" \
"s_andn2_b32 s75, 0x2000, s98\n" \
"s_cbranch_scc1 L_sx3_%=\n" \
"s_branch L_rx3_%=\n" \
"L_sx4_%=:\n" \
"s_sleep 1\n" \
"buffer_load_dword v37, v40, s[56:59], s74 offen sc1\n" \
"s_add_u32 s101, s101, 1\n" \
"s_waitcnt vmcnt(0)\n" \
"v_cmp_eq_u32_e64 s[98:99], s62, v37\n" \
"s_cmp_gt_u32 s101, 0x100000\n" \
"s_cbranch_scc1 L_rx4_%=\n" \
"s_andn2_b32 s75, 0x4000, s98\n" \
"s_cbranch_scc1 L_sx4_%=\n" \
"s_branch L_rx4_%=\n" \
"L_att_stub:\n" \
"s_mov_b32 s101, 0\n" \
"v_add_u32_e32 v32, 0x49000, v20\n" \
"v_readlane_b32 s96, v11, 39\n" \
"v_readlane_b32 s97, v11, 40\n" \
B_"a[192:195], v20, s[56:59], s60 offen offset:0 sc1\n" \
B_"a[208:211], v32, s[56:59], s60 offen offset:0 sc1\n" \
B_"a[196:199], v20, s[56:59], s60 offen offset:1024 sc1\n" \
B_"a[212:215], v32, s[56:59], s60 offen offset:1024 sc1\n" \
B_"a[200:203], v20, s[56:59], s60 offen offset:2048 sc1\n" \
B_"a[216:219], v32, s[56:59], s60 offen offset:2048 sc1\n" \
B_"a[204:207], v20, s[56:59], s60 offen offset:3072 sc1\n" \
B_"a[220:223], v32, s[56:59], s60 offen offset:3072 sc1\n" \
B_"a[124:127], v20, s[56:59], s96 offen offset:0 sc1\n" \
B_"a[228:231], v32, s[56:59], s96 offen offset:0 sc1\n" \
B_"a[236:239], v20, s[56:59], s97 offen offset:0 sc1\n" \
B_"a[244:247], v32, s[56:59], s97 offen offset:0 sc1\n" \
B_"a[224:227], v20, s[56:59], s96 offen offset:1024 sc1\n" \
B_"a[232:235], v32, s[56:59], s96 offen offset:1024 sc1\n" \
B_"a[240:243], v20, s[56:59], s97 offen offset:1024 sc1\n" \
B_"a[248:251], v32, s[56:59], s97 offen offset:1024 sc1\n" \
B_"v[64:67], v20, s[56:59], s61 offen offset:0 sc1\n" \
B_"v[68:71], v20, s[56:59], s61 offen offset:1024 sc1\n" \
B_"v[72:75], v20, s[56:59], s61 offen offset:2048 sc1\n" \
B_"v[76:79], v20, s[56:59], s61 offen offset:3072 sc1\n" \
B_"v[80:83], v32, s[56:59], s61 offen offset:0 sc1\n" \
B_"v[84:87], v32, s[56:59], s61 offen offset:1024 sc1\n" \
B_"v[96:99], v32, s[56:59], s61 offen offset:2048 sc1\n" \
B_"v[100:103], v32, s[56:59], s61 offen offset:3072 sc1\n" \
"v_not_b32_e32 v26, s62\n" \
"s_waitcnt vmcnt(0)\n" \
"s_branch L_out_%=\n" \
".fill (L_ab + 16384 - .) / 4, 4, 0xBF800000\n" \
".fill (L_ab + 16384 - .) / 4, 4, 0xBF800000\n" \
"L_ap4:\n" \
"s_setpc_b64 s[88:89]\n" \
"L_gsw:\n" \
M_"v[104:107], a[128:131], a[16:19], v[36:39]\n" \
"s_add_u32 s68, s60, 0xfff6e000\n" \
"s_add_u32 s69, s60, 0xfffb7000\n" \
"s_add_u32 s70, s60, 0x0\n" \
"s_add_u32 s71, s60, 0x49000\n" \
M_"v[104:107], a[132:135], a[20:23], v[104:107]\n" \
"s_xor_b32 m0, s65, 0x12000\n" \
R_"a[160:163], v49 offset:0\n" \
M_"v[104:107], a[136:139], a[24:27], v[104:107]\n" \
B_"v50, s[8:11], s64 offen offset:0 sc1 lds\n" \
M_"v[104:107], a[140:143], a[28:31], v[104:107]\n" \
R_"a[164:167], v49 offset:1024\n" \
R_"a[168:171], v49 offset:2048\n" \
M_"v[108:111], a[156:159], a[28:31], v[36:39]\n" \
R_"a[172:175], v49 offset:3072\n" \
R_"a[176:179], v49 offset:4096\n" \
M_"v[108:111], a[152:155], a[24:27], v[108:111]\n" \
B_"v50, s[8:11], s64 offen offset:1024 sc1 lds\n" \
M_"v[108:111], a[148:151], a[20:23], v[108:111]\n" \
"s_mov_b32 s65, s13\n" \
"v_add_u32_e32 v63, s15, v20\n" \
M_"v[108:111], a[144:147], a[16:19], v[108:111]\n" \
"s_add_u32 s67, s14, 40960\n" \
"v_add_u32_e32 v50, 0x6000, v63\n" \
M_"v[116:119], a[144:147], a[32:35], v[40:43]\n" \
"s_xor_b32 m0, s65, 0x8000\n" \
R_"a[180:183], v49 offset:5120\n" \
M_"v[116:119], a[148:151], a[36:39], v[116:119]\n" \
R_"a[184:187], v49 offset:6144\n" \
"v_and_b32_e32 v68, v12, v68\n" \
M_"v[116:119], a[152:155], a[40:43], v[116:119]\n" \
"v_and_b32_e32 v69, v13, v69\n" \
"v_and_b32_e32 v70, v14, v70\n" \
M_"v[116:119], a[156:159], a[44:47], v[116:119]\n" \
B_"v50, s[8:11], s67 offen offset:0 sc1 lds\n" \
M_"v[112:115], a[140:143], a[44:47], v[40:43]\n" \
"v_and_b32_e32 v71, v15, v71\n" \
R_"a[188:191], v49 offset:7168\n" \
M_"v[112:115], a[136:139], a[40:43], v[112:115]\n" \
E_"v86, v86\n" \
M_"v[112:115], a[132:135], a[36:39], v[112:115]\n" \
E_"v87, v87\n" \
M_"v[112:115], a[128:131], a[32:35], v[112:115]\n" \
E_"v84, v84\n" \
M_"v[120:123], a[128:131], a[48:51], v[44:47]\n" \
B_"v50, s[8:11], s67 offen offset:1024 sc1 lds\n" \
M_"v[120:123], a[132:135], a[52:55], v[120:123]\n" \
E_"v85, v85\n" \
M_"v[120:123], a[136:139], a[56:59], v[120:123]\n" \
C_"v87, v86, v87\n" \
C_"v86, v84, v85\n" \
M_"v[120:123], a[140:143], a[60:63], v[120:123]\n" \
E_"v82, v82\n" \
M_"v[124:127], a[156:159], a[60:63], v[44:47]\n" \
E_"v83, v83\n" \
M_"v[124:127], a[152:155], a[56:59], v[124:127]\n" \
E_"v80, v80\n" \
M_"v[124:127], a[148:151], a[52:55], v[124:127]\n" \
B_"v50, s[8:11], s67 offen offset:2048 sc1 lds\n" \
M_"v[124:127], a[144:147], a[48:51], v[124:127]\n" \
C_"v85, v82, v83\n" \
"s_waitcnt lgkmcnt(1)\n" \
M_"v[128:131], a[160:163], v[68:71], v[128:131]\n" \
E_"v81, v81\n" \
M_"v[160:163], a[160:163], v[72:75], v[160:163]\n" \
C_"v84, v80, v81\n" \
"s_mov_b32 s64, s14\n" \
M_"v[132:135], a[164:167], v[68:71], v[132:135]\n" \
E_"v88, v88\n" \
M_"v[164:167], a[164:167], v[72:75], v[164:167]\n" \
E_"v89, v89\n" \
M_"v[136:139], a[168:171], v[68:71], v[136:139]\n" \
B_"v50, s[8:11], s67 offen offset:3072 sc1 lds\n" \
M_"v[168:171], a[168:171], v[72:75], v[168:171]\n" \
E_"v90, v90\n" \
M_"v[140:143], a[172:175], v[68:71], v[140:143]\n" \
E_"v91, v91\n" \
M_"v[172:175], a[172:175], v[72:75], v[172:175]\n" \
C_"v88, v88, v89\n" \
C_"v89, v90, v91\n" \
M_"v[144:147], a[176:179], v[68:71], v[144:147]\n" \
E_"v92, v92\n" \
M_"v[176:179], a[176:179], v[72:75], v[176:179]\n" \
E_"v93, v93\n" \
M_"v[148:151], a[180:183], v[68:71], v[148:151]\n" \
E_"v94, v94\n" \
M_"v[180:183], a[180:183], v[72:75], v[180:183]\n" \
E_"v95, v95\n" \
M_"v[152:155], a[184:187], v[68:71], v[152:155]\n" \
C_"v90, v92, v93\n" \
C_"v91, v94, v95\n" \
M_"v[184:187], a[184:187], v[72:75], v[184:187]\n" \
"v_add_u32_e32 v51, 0x12000, v63\n" \
"s_waitcnt lgkmcnt(0)\n" \
M_"v[156:159], a[188:191], v[68:71], v[156:159]\n" \
"s_xor_b32 m0, s65, 0xa000\n" \
"ds_read_b32 v58, v18 offset:512\n" \
M_"v[188:191], a[188:191], v[72:75], v[188:191]\n" \
B_"v51, s[8:11], s64 offen offset:0 sc1 lds\n" \
"v_smfmac_f32_16x16x64_bf16 a[96:99], a[104:107], v[68:75], v21\n" \
B_"v51, s[8:11], s64 offen offset:1024 sc1 lds\n" \
M_"v[220:223], a[188:191], v[84:87], v[220:223]\n" \
"ds_read_b32 v59, v18 offset:640\n" \
"ds_read_b32 v60, v18 offset:768\n" \
M_"v[252:255], a[188:191], v[88:91], v[252:255]\n" \
"ds_read_b32 v61, v18 offset:896\n" \
R_"a[128:131], v48 offset:0\n" \
M_"v[216:219], a[184:187], v[84:87], v[216:219]\n" \
R_"a[144:147], v48 offset:4096\n" \
R_"a[132:135], v48 offset:1024\n" \
M_"v[248:251], a[184:187], v[88:91], v[248:251]\n" \
R_"a[148:151], v48 offset:5120\n" \
R_"a[136:139], v48 offset:2048\n" \
M_"v[212:215], a[180:183], v[84:87], v[212:215]\n" \
R_"a[152:155], v48 offset:6144\n" \
R_"a[140:143], v48 offset:3072\n" \
M_"v[244:247], a[180:183], v[88:91], v[244:247]\n" \
R_"a[156:159], v48 offset:7168\n" \
"v_add_u32_e32 v52, 0x14000, v63\n" \
M_"v[208:211], a[176:179], v[84:87], v[208:211]\n" \
B_"v51, s[8:11], s64 offen offset:2048 sc1 lds\n" \
M_"v[240:243], a[176:179], v[88:91], v[240:243]\n" \
E_"v104, v104\n" \
M_"v[204:207], a[172:175], v[84:87], v[204:207]\n" \
E_"v105, v105\n" \
M_"v[236:239], a[172:175], v[88:91], v[236:239]\n" \
B_"v51, s[8:11], s64 offen offset:3072 sc1 lds\n" \
M_"v[200:203], a[168:171], v[84:87], v[200:203]\n" \
E_"v106, v106\n" \
M_"v[232:235], a[168:171], v[88:91], v[232:235]\n" \
E_"v107, v107\n" \
M_"v[196:199], a[164:167], v[84:87], v[196:199]\n" \
C_"v104, v104, v105\n" \
C_"v105, v106, v107\n" \
M_"v[228:231], a[164:167], v[88:91], v[228:231]\n" \
E_"v108, v108\n" \
M_"v[192:195], a[160:163], v[84:87], v[192:195]\n" \
E_"v109, v109\n" \
M_"v[224:227], a[160:163], v[88:91], v[224:227]\n" \
E_"v110, v110\n" \
"v_smfmac_f32_16x16x64_bf16 a[100:103], a[104:107], v[84:91], v21\n" \
E_"v111, v111\n" \
C_"v106, v108, v109\n" \
C_"v107, v110, v111\n" \
"v_and_b32_e32 v104, v12, v104\n" \
"v_and_b32_e32 v105, v13, v105\n" \
"v_and_b32_e32 v106, v14, v106\n" \
"v_and_b32_e32 v107, v15, v107\n" \
"v_accvgpr_read_b32 v54, a96\n" \
"v_accvgpr_read_b32 v22, a97\n" \
"v_add_u32_e32 v53, 0x20000, v63\n" \
E_"v118, v118\n" \
E_"v119, v119\n" \
E_"v116, v116\n" \
E_"v117, v117\n" \
C_"v119, v118, v119\n" \
E_"v114, v114\n" \
E_"v115, v115\n" \
E_"v112, v112\n" \
E_"v113, v113\n" \
C_"v118, v116, v117\n" \
C_"v117, v114, v115\n" \
C_"v116, v112, v113\n" \
"v_readlane_b32 s93, v11, 22\n" \
"s_waitcnt vmcnt(10) lgkmcnt(0)\n" \
"s_barrier\n" \
M_"v[80:83], a[128:131], a[32:35], v[40:43]\n" \
R_"a[160:163], v49 offset:8192\n" \
R_"a[164:167], v49 offset:9216\n" \
M_"v[80:83], a[132:135], a[36:39], v[80:83]\n" \
R_"a[168:171], v49 offset:10240\n" \
"s_xor_b32 m0, s65, 0x18000\n" \
M_"v[80:83], a[136:139], a[40:43], v[80:83]\n" \
B_"v52, s[8:11], s64 offen offset:0 sc1 lds\n" \
M_"v[80:83], a[140:143], a[44:47], v[80:83]\n" \
R_"a[172:175], v49 offset:11264\n" \
R_"a[176:179], v49 offset:12288\n" \
M_"v[84:87], a[156:159], a[44:47], v[40:43]\n" \
R_"a[180:183], v49 offset:13312\n" \
R_"a[184:187], v49 offset:14336\n" \
M_"v[84:87], a[152:155], a[40:43], v[84:87]\n" \
B_"v52, s[8:11], s64 offen offset:1024 sc1 lds\n" \
M_"v[84:87], a[148:151], a[36:39], v[84:87]\n" \
R_"a[188:191], v49 offset:15360\n" \
"v_readlane_b32 s80, v11, 11\n" \
M_"v[84:87], a[144:147], a[32:35], v[84:87]\n" \
B_"v52, s[8:11], s64 offen offset:2048 sc1 lds\n" \
M_"v[92:95], a[144:147], a[48:51], v[44:47]\n" \
E_"v120, v120\n" \
M_"v[92:95], a[148:151], a[52:55], v[92:95]\n" \
E_"v121, v121\n" \
M_"v[92:95], a[152:155], a[56:59], v[92:95]\n" \
B_"v52, s[8:11], s64 offen offset:3072 sc1 lds\n" \
M_"v[92:95], a[156:159], a[60:63], v[92:95]\n" \
"s_mov_b32 m0, s65\n" \
C_"v120, v120, v121\n" \
M_"v[88:91], a[140:143], a[60:63], v[44:47]\n" \
E_"v122, v122\n" \
M_"v[88:91], a[136:139], a[56:59], v[88:91]\n" \
B_"v53, s[8:11], s64 offen offset:0 sc1 lds\n" \
M_"v[88:91], a[132:135], a[52:55], v[88:91]\n" \
E_"v123, v123\n" \
M_"v[88:91], a[128:131], a[48:51], v[88:91]\n" \
E_"v124, v124\n" \
M_"a[96:99], a[116:119], v[104:107], a[96:99]\n" \
C_"v121, v122, v123\n" \
"s_waitcnt lgkmcnt(5)\n" \
M_"v[160:163], a[160:163], v[104:107], v[160:163]\n" \
B_"v53, s[8:11], s64 offen offset:1024 sc1 lds\n" \
M_"v[164:167], a[164:167], v[104:107], v[164:167]\n" \
E_"v125, v125\n" \
M_"v[168:171], a[168:171], v[104:107], v[168:171]\n" \
C_"v122, v124, v125\n" \
"s_waitcnt lgkmcnt(1)\n" \
M_"v[172:175], a[172:175], v[104:107], v[172:175]\n" \
E_"v126, v126\n" \
M_"v[176:179], a[176:179], v[104:107], v[176:179]\n" \
E_"v127, v127\n" \
M_"v[180:183], a[180:183], v[104:107], v[180:183]\n" \
C_"v123, v126, v127\n" \
"v_readlane_b32 s81, v11, 12\n" \
M_"v[184:187], a[184:187], v[104:107], v[184:187]\n" \
"s_mov_b32 s82, 0x47fca0\n" \
"s_waitcnt lgkmcnt(0)\n" \
M_"v[188:191], a[188:191], v[104:107], v[188:191]\n" \
B_"v53, s[8:11], s64 offen offset:2048 sc1 lds\n" \
M_"v[220:223], a[188:191], v[116:119], v[220:223]\n" \
B_"v53, s[8:11], s64 offen offset:3072 sc1 lds\n" \
M_"v[252:255], a[188:191], v[120:123], v[252:255]\n" \
"s_mov_b32 s83, 0x20000\n" \
"v_readlane_b32 s92, v11, 29\n" \
M_"v[216:219], a[184:187], v[116:119], v[216:219]\n" \
"s_add_u32 m0, s93, 0x0\n" \
R_"a[128:131], v48 offset:8192\n" \
M_"v[248:251], a[184:187], v[120:123], v[248:251]\n" \
R_"a[144:147], v48 offset:12288\n" \
R_"a[132:135], v48 offset:9216\n" \
M_"v[212:215], a[180:183], v[116:119], v[212:215]\n" \
B_"v0, s[80:83], s92 offen offset:0 lds\n" \
M_"v[244:247], a[180:183], v[120:123], v[244:247]\n" \
B_"v1, s[80:83], s92 offen offset:1024 lds\n" \
M_"v[208:211], a[176:179], v[116:119], v[208:211]\n" \
R_"a[148:151], v48 offset:13312\n" \
R_"a[136:139], v48 offset:10240\n" \
M_"v[240:243], a[176:179], v[120:123], v[240:243]\n" \
R_"a[152:155], v48 offset:14336\n" \
R_"a[140:143], v48 offset:11264\n" \
M_"v[204:207], a[172:175], v[116:119], v[204:207]\n" \
B_"v2, s[80:83], s92 offen offset:2048 lds\n" \
M_"v[236:239], a[172:175], v[120:123], v[236:239]\n" \
R_"a[156:159], v48 offset:15360\n" \
"s_xor_b32 s65, s65, 0x8000\n" \
M_"v[200:203], a[168:171], v[116:119], v[200:203]\n" \
"s_add_u32 s64, s64, 0x10000\n" \
"v_add_u32_e32 v49, 0x10000, v48\n" \
M_"v[232:235], a[168:171], v[120:123], v[232:235]\n" \
B_"v3, s[80:83], s92 offen offset:3072 lds\n" \
M_"v[196:199], a[164:167], v[116:119], v[196:199]\n" \
E_"v86, v86\n" \
M_"v[228:231], a[164:167], v[120:123], v[228:231]\n" \
E_"v87, v87\n" \
M_"v[192:195], a[160:163], v[116:119], v[192:195]\n" \
E_"v84, v84\n" \
M_"v[224:227], a[160:163], v[120:123], v[224:227]\n" \
E_"v85, v85\n" \
"v_smfmac_f32_16x16x64_bf16 a[100:103], a[104:107], v[116:123], v21\n" \
E_"v88, v88\n" \
E_"v89, v89\n" \
C_"v87, v86, v87\n" \
E_"v82, v82\n" \
E_"v83, v83\n" \
E_"v80, v80\n" \
E_"v81, v81\n" \
E_"v90, v90\n" \
E_"v91, v91\n" \
C_"v86, v84, v85\n" \
C_"v88, v88, v89\n" \
E_"v92, v92\n" \
E_"v93, v93\n" \
E_"v94, v94\n" \
E_"v95, v95\n" \
C_"v85, v82, v83\n" \
C_"v84, v80, v81\n" \
C_"v89, v90, v91\n" \
"v_and_b32_e32 v84, v12, v84\n" \
"v_and_b32_e32 v85, v13, v85\n" \
"v_and_b32_e32 v86, v14, v86\n" \
"v_and_b32_e32 v87, v15, v87\n" \
C_"v90, v92, v93\n" \
C_"v91, v94, v95\n" \
"v_add_u32_e32 v48, s16, v20\n" \
"v_add_f32_e32 v54, v54, v22\n" \
"v_accvgpr_read_b32 v55, a98\n" \
"v_accvgpr_read_b32 v23, a99\n" \
M_"a[96:99], a[120:123], a[120:123], 0\n" \
"v_max_f32_e32 v62, v62, v54\n" \
"v_add_f32_e32 v55, v55, v23\n" \
"v_max_f32_e32 v62, v62, v55\n" \
"v_rcp_f32_e32 v54, v54\n" \
"v_rcp_f32_e32 v55, v55\n" \
"v_mul_f32_e32 v54, v54, v58\n" \
"v_mul_f32_e32 v55, v55, v59\n" \
"v_mul_f32_e32 v128, v54, v128\n" \
"v_mul_f32_e32 v129, v54, v129\n" \
"v_mul_f32_e32 v136, v54, v136\n" \
"v_mul_f32_e32 v137, v54, v137\n" \
"v_mul_f32_e32 v144, v54, v144\n" \
"v_mul_f32_e32 v145, v54, v145\n" \
"v_mul_f32_e32 v152, v54, v152\n" \
"v_mul_f32_e32 v153, v54, v153\n" \
"v_mul_f32_e32 v160, v55, v160\n" \
"v_mul_f32_e32 v161, v55, v161\n" \
"v_mul_f32_e32 v168, v55, v168\n" \
"v_mul_f32_e32 v169, v55, v169\n" \
"v_mul_f32_e32 v176, v55, v176\n" \
"v_mul_f32_e32 v177, v55, v177\n" \
"v_mul_f32_e32 v184, v55, v184\n" \
"v_mul_f32_e32 v185, v55, v185\n" \
C_"v128, v128, v129\n" \
"v_mul_f32_e32 v130, v54, v130\n" \
"v_mul_f32_e32 v131, v54, v131\n" \
C_"v136, v136, v137\n" \
"v_mul_f32_e32 v138, v54, v138\n" \
"v_mul_f32_e32 v139, v54, v139\n" \
C_"v144, v144, v145\n" \
"v_mul_f32_e32 v146, v54, v146\n" \
"v_mul_f32_e32 v147, v54, v147\n" \
C_"v152, v152, v153\n" \
"v_mul_f32_e32 v154, v54, v154\n" \
"v_mul_f32_e32 v155, v54, v155\n" \
C_"v160, v160, v161\n" \
"v_mul_f32_e32 v162, v55, v162\n" \
"v_mul_f32_e32 v163, v55, v163\n" \
C_"v168, v168, v169\n" \
"v_mul_f32_e32 v170, v55, v170\n" \
"v_mul_f32_e32 v171, v55, v171\n" \
C_"v176, v176, v177\n" \
"v_mul_f32_e32 v178, v55, v178\n" \
"v_mul_f32_e32 v179, v55, v179\n" \
C_"v184, v184, v185\n" \
"v_mul_f32_e32 v186, v55, v186\n" \
"v_mul_f32_e32 v187, v55, v187\n" \
C_"v129, v130, v131\n" \
"v_mul_f32_e32 v132, v54, v132\n" \
"v_mul_f32_e32 v133, v54, v133\n" \
"v_mul_f32_e32 v134, v54, v134\n" \
"v_mul_f32_e32 v135, v54, v135\n" \
C_"v137, v138, v139\n" \
"v_mul_f32_e32 v140, v54, v140\n" \
"s_waitcnt vmcnt(12) lgkmcnt(0)\n" \
"s_barrier\n" \
M_"v[120:123], a[128:131], a[48:51], v[44:47]\n" \
"s_xor_b32 m0, s65, 0x12000\n" \
R_"a[188:191], v49 offset:7168\n" \
M_"v[120:123], a[132:135], a[52:55], v[120:123]\n" \
B_"v50, s[8:11], s64 offen offset:0 sc1 lds\n" \
M_"v[120:123], a[136:139], a[56:59], v[120:123]\n" \
R_"a[184:187], v49 offset:6144\n" \
R_"a[180:183], v49 offset:5120\n" \
M_"v[120:123], a[140:143], a[60:63], v[120:123]\n" \
B_"v50, s[8:11], s64 offen offset:1024 sc1 lds\n" \
M_"v[124:127], a[156:159], a[60:63], v[44:47]\n" \
R_"a[176:179], v49 offset:4096\n" \
R_"a[172:175], v49 offset:3072\n" \
M_"v[124:127], a[152:155], a[56:59], v[124:127]\n" \
R_"a[168:171], v49 offset:2048\n" \
R_"a[164:167], v49 offset:1024\n" \
M_"v[124:127], a[148:151], a[52:55], v[124:127]\n" \
R_"a[160:163], v49 offset:0\n" \
R_"a[0:3], v49 offset:8192\n" \
M_"v[124:127], a[144:147], a[48:51], v[124:127]\n" \
B_"v50, s[8:11], s64 offen offset:2048 sc1 lds\n" \
M_"v[32:35], a[124:127], a[64:67], 0\n" \
R_"a[4:7], v49 offset:9216\n" \
R_"a[8:11], v49 offset:10240\n" \
M_"v[36:39], a[124:127], a[80:83], 0\n" \
R_"a[12:15], v49 offset:11264\n" \
R_"a[16:19], v49 offset:12288\n" \
M_"v[40:43], a[124:127], a[192:195], 0\n" \
R_"a[20:23], v49 offset:13312\n" \
R_"a[24:27], v49 offset:14336\n" \
M_"v[44:47], a[124:127], a[208:211], 0\n" \
B_"v50, s[8:11], s64 offen offset:3072 sc1 lds\n" \
M_"v[32:35], a[224:227], a[68:71], v[32:35]\n" \
"s_xor_b32 m0, s65, 0xa000\n" \
R_"a[28:31], v49 offset:15360\n" \
M_"v[36:39], a[224:227], a[84:87], v[36:39]\n" \
E_"v120, v120\n" \
M_"v[40:43], a[224:227], a[196:199], v[40:43]\n" \
B_"v51, s[8:11], s64 offen offset:0 sc1 lds\n" \
M_"v[44:47], a[224:227], a[212:215], v[44:47]\n" \
E_"v121, v121\n" \
M_"v[32:35], a[228:231], a[72:75], v[32:35]\n" \
E_"v122, v122\n" \
M_"v[36:39], a[228:231], a[88:91], v[36:39]\n" \
E_"v123, v123\n" \
M_"v[40:43], a[228:231], a[200:203], v[40:43]\n" \
B_"v51, s[8:11], s64 offen offset:1024 sc1 lds\n" \
M_"v[44:47], a[228:231], a[216:219], v[44:47]\n" \
C_"v120, v120, v121\n" \
C_"v121, v122, v123\n" \
M_"v[32:35], a[232:235], a[76:79], v[32:35]\n" \
E_"v124, v124\n" \
M_"v[36:39], a[232:235], a[92:95], v[36:39]\n" \
E_"v125, v125\n" \
M_"v[40:43], a[232:235], a[204:207], v[40:43]\n" \
B_"v51, s[8:11], s64 offen offset:2048 sc1 lds\n" \
M_"v[44:47], a[232:235], a[220:223], v[44:47]\n" \
E_"v126, v126\n" \
"v_smfmac_f32_16x16x64_bf16 a[100:103], a[104:107], v[84:91], v21\n" \
C_"v122, v124, v125\n" \
"s_waitcnt lgkmcnt(1)\n" \
M_"v[220:223], a[188:191], v[84:87], v[220:223]\n" \
B_"v51, s[8:11], s64 offen offset:3072 sc1 lds\n" \
M_"v[252:255], a[188:191], v[88:91], v[252:255]\n" \
"s_add_u32 m0, s93, 0x1000\n" \
"v_and_b32_e32 v120, v12, v120\n" \
M_"v[216:219], a[184:187], v[84:87], v[216:219]\n" \
E_"v127, v127\n" \
M_"v[248:251], a[184:187], v[88:91], v[248:251]\n" \
C_"v123, v126, v127\n" \
"v_and_b32_e32 v121, v13, v121\n" \
M_"v[212:215], a[180:183], v[84:87], v[212:215]\n" \
B_"v4, s[80:83], s92 offen offset:0 lds\n" \
M_"v[244:247], a[180:183], v[88:91], v[244:247]\n" \
"v_and_b32_e32 v122, v14, v122\n" \
"v_and_b32_e32 v123, v15, v123\n" \
M_"v[208:211], a[176:179], v[84:87], v[208:211]\n" \
"v_accvgpr_read_b32 v56, a100\n" \
"v_accvgpr_read_b32 v24, a101\n" \
M_"v[240:243], a[176:179], v[88:91], v[240:243]\n" \
R_"a[128:131], v48 offset:0\n" \
R_"a[144:147], v48 offset:4096\n" \
M_"v[204:207], a[172:175], v[84:87], v[204:207]\n" \
B_"v5, s[80:83], s92 offen offset:1024 lds\n" \
M_"v[236:239], a[172:175], v[88:91], v[236:239]\n" \
R_"a[132:135], v48 offset:1024\n" \
R_"a[148:151], v48 offset:5120\n" \
M_"v[200:203], a[168:171], v[84:87], v[200:203]\n" \
R_"a[136:139], v48 offset:2048\n" \
R_"a[152:155], v48 offset:6144\n" \
M_"v[232:235], a[168:171], v[88:91], v[232:235]\n" \
B_"v6, s[80:83], s92 offen offset:2048 lds\n" \
M_"v[196:199], a[164:167], v[84:87], v[196:199]\n" \
R_"a[140:143], v48 offset:3072\n" \
R_"a[156:159], v48 offset:7168\n" \
M_"v[228:231], a[164:167], v[88:91], v[228:231]\n" \
"v_mul_f32_e32 v141, v54, v141\n" \
"v_mul_f32_e32 v142, v54, v142\n" \
M_"v[192:195], a[160:163], v[84:87], v[192:195]\n" \
"v_mul_f32_e32 v143, v54, v143\n" \
C_"v145, v146, v147\n" \
M_"v[224:227], a[160:163], v[88:91], v[224:227]\n" \
B_"v7, s[80:83], s92 offen offset:3072 lds\n" \
M_"a[100:103], a[116:119], v[120:123], a[100:103]\n" \
"v_mul_f32_e32 v148, v54, v148\n" \
"v_mul_f32_e32 v149, v54, v149\n" \
M_"v[224:227], a[0:3], v[120:123], v[224:227]\n" \
"v_mul_f32_e32 v150, v54, v150\n" \
"v_mul_f32_e32 v151, v54, v151\n" \
M_"v[228:231], a[4:7], v[120:123], v[228:231]\n" \
C_"v153, v154, v155\n" \
"v_mul_f32_e32 v156, v54, v156\n" \
M_"v[232:235], a[8:11], v[120:123], v[232:235]\n" \
"v_mul_f32_e32 v157, v54, v157\n" \
"v_mul_f32_e32 v158, v54, v158\n" \
M_"v[236:239], a[12:15], v[120:123], v[236:239]\n" \
"v_mul_f32_e32 v159, v54, v159\n" \
C_"v161, v162, v163\n" \
M_"v[240:243], a[16:19], v[120:123], v[240:243]\n" \
"v_mul_f32_e32 v164, v55, v164\n" \
"v_mul_f32_e32 v165, v55, v165\n" \
M_"v[244:247], a[20:23], v[120:123], v[244:247]\n" \
"v_mul_f32_e32 v166, v55, v166\n" \
"v_mul_f32_e32 v167, v55, v167\n" \
M_"v[248:251], a[24:27], v[120:123], v[248:251]\n" \
C_"v169, v170, v171\n" \
"s_waitcnt lgkmcnt(4)\n" \
M_"v[252:255], a[28:31], v[120:123], v[252:255]\n" \
"v_mul_f32_e32 v172, v55, v172\n" \
"v_mul_f32_e32 v173, v55, v173\n" \
"v_mul_f32_e32 v174, v55, v174\n" \
"v_mul_f32_e32 v175, v55, v175\n" \
C_"v177, v178, v179\n" \
"v_mul_f32_e32 v180, v55, v180\n" \
"v_mul_f32_e32 v181, v55, v181\n" \
"v_mul_f32_e32 v182, v55, v182\n" \
"v_mul_f32_e32 v183, v55, v183\n" \
C_"v185, v186, v187\n" \
"v_mul_f32_e32 v188, v55, v188\n" \
"v_mul_f32_e32 v189, v55, v189\n" \
"v_mul_f32_e32 v190, v55, v190\n" \
"v_mul_f32_e32 v191, v55, v191\n" \
C_"v130, v132, v133\n" \
C_"v131, v134, v135\n" \
"buffer_store_dwordx4 v[128:131], v20, s[56:59], s68 offen offset:0 sc1\n" \
C_"v138, v140, v141\n" \
C_"v139, v142, v143\n" \
"buffer_store_dwordx4 v[136:139], v20, s[56:59], s68 offen offset:1024 sc1\n" \
C_"v146, v148, v149\n" \
C_"v147, v150, v151\n" \
"buffer_store_dwordx4 v[144:147], v20, s[56:59], s68 offen offset:2048 sc1\n" \
C_"v154, v156, v157\n" \
C_"v155, v158, v159\n" \
"buffer_store_dwordx4 v[152:155], v20, s[56:59], s68 offen offset:3072 sc1\n" \
C_"v162, v164, v165\n" \
C_"v163, v166, v167\n" \
"buffer_store_dwordx4 v[160:163], v20, s[56:59], s69 offen offset:0 sc1\n" \
C_"v170, v172, v173\n" \
C_"v171, v174, v175\n" \
"buffer_store_dwordx4 v[168:171], v20, s[56:59], s69 offen offset:1024 sc1\n" \
C_"v178, v180, v181\n" \
C_"v179, v182, v183\n" \
"buffer_store_dwordx4 v[176:179], v20, s[56:59], s69 offen offset:2048 sc1\n" \
C_"v186, v188, v189\n" \
C_"v187, v190, v191\n" \
"buffer_store_dwordx4 v[184:187], v20, s[56:59], s69 offen offset:3072 sc1\n" \
"s_add_u32 m0, s93, 0x2000\n" \
M_"a[112:115], a[120:123], v[128:131], a[112:115]\n" \
B_"v8, s[80:83], s92 offen offset:0 lds\n" \
M_"a[112:115], a[120:123], v[136:139], a[112:115]\n" \
"v_mfma_f32_32x32x16_bf16 v[128:143], a[120:123], a[120:123], 0\n" \
M_"a[112:115], a[120:123], v[144:147], a[112:115]\n" \
M_"a[112:115], a[120:123], v[152:155], a[112:115]\n" \
"v_mfma_f32_32x32x16_bf16 v[144:159], a[120:123], a[120:123], 0\n" \
"v_add_f32_e32 v56, v56, v24\n" \
M_"a[112:115], a[120:123], v[160:163], a[112:115]\n" \
"v_max_f32_e32 v62, v62, v56\n" \
"v_accvgpr_read_b32 v57, a102\n" \
"s_waitcnt vmcnt(21) lgkmcnt(0)\n" \
"s_barrier\n" \
M_"v[64:67], a[128:131], a[64:67], v[32:35]\n" \
"s_xor_b32 m0, s65, 0x18000\n" \
"v_accvgpr_read_b32 v25, a103\n" \
M_"a[100:103], a[120:123], a[120:123], 0\n" \
B_"v52, s[8:11], s64 offen offset:0 sc1 lds\n" \
M_"v[64:67], a[132:135], a[68:71], v[64:67]\n" \
"v_rcp_f32_e32 v56, v56\n" \
M_"v[64:67], a[136:139], a[72:75], v[64:67]\n" \
M_"a[112:115], a[120:123], v[168:171], a[112:115]\n" \
"v_mfma_f32_32x32x16_bf16 v[160:175], a[120:123], a[120:123], 0\n" \
"v_mul_f32_e32 v56, v56, v60\n" \
M_"a[112:115], a[120:123], v[176:179], a[112:115]\n" \
"v_mul_f32_e32 v192, v56, v192\n" \
"v_mul_f32_e32 v193, v56, v193\n" \
M_"v[64:67], a[140:143], a[76:79], v[64:67]\n" \
B_"v52, s[8:11], s64 offen offset:1024 sc1 lds\n" \
M_"v[68:71], a[156:159], a[76:79], v[32:35]\n" \
C_"v192, v192, v193\n" \
"v_mul_f32_e32 v194, v56, v194\n" \
M_"v[68:71], a[152:155], a[72:75], v[68:71]\n" \
"v_mul_f32_e32 v195, v56, v195\n" \
C_"v193, v194, v195\n" \
M_"v[68:71], a[148:151], a[68:71], v[68:71]\n" \
M_"a[112:115], a[120:123], v[184:187], a[112:115]\n" \
"v_mfma_f32_32x32x16_bf16 v[176:191], a[120:123], a[120:123], 0\n" \
"v_mul_f32_e32 v196, v56, v196\n" \
"v_mul_f32_e32 v197, v56, v197\n" \
"v_mul_f32_e32 v198, v56, v198\n" \
"v_mul_f32_e32 v199, v56, v199\n" \
"v_mul_f32_e32 v200, v56, v200\n" \
"v_mul_f32_e32 v201, v56, v201\n" \
M_"v[68:71], a[144:147], a[64:67], v[68:71]\n" \
B_"v52, s[8:11], s64 offen offset:2048 sc1 lds\n" \
M_"v[76:79], a[144:147], a[80:83], v[36:39]\n" \
C_"v194, v196, v197\n" \
C_"v195, v198, v199\n" \
"buffer_store_dwordx4 v[192:195], v20, s[56:59], s70 offen offset:0 sc1\n" \
M_"v[76:79], a[148:151], a[84:87], v[76:79]\n" \
C_"v200, v200, v201\n" \
"v_mul_f32_e32 v202, v56, v202\n" \
M_"v[76:79], a[152:155], a[88:91], v[76:79]\n" \
"v_mul_f32_e32 v203, v56, v203\n" \
"v_add_f32_e32 v57, v57, v25\n" \
M_"v[76:79], a[156:159], a[92:95], v[76:79]\n" \
B_"v52, s[8:11], s64 offen offset:3072 sc1 lds\n" \
M_"v[72:75], a[140:143], a[92:95], v[36:39]\n" \
"s_mov_b32 m0, s65\n" \
C_"v201, v202, v203\n" \
M_"v[72:75], a[136:139], a[88:91], v[72:75]\n" \
M_"a[112:115], a[120:123], v[192:195], a[112:115]\n" \
"v_mul_f32_e32 v204, v56, v204\n" \
"v_mul_f32_e32 v205, v56, v205\n" \
M_"v[72:75], a[132:135], a[84:87], v[72:75]\n" \
"v_mul_f32_e32 v206, v56, v206\n" \
"v_mul_f32_e32 v207, v56, v207\n" \
M_"v[72:75], a[128:131], a[80:83], v[72:75]\n" \
B_"v53, s[8:11], s64 offen offset:0 sc1 lds\n" \
M_"v[80:83], a[128:131], a[192:195], v[40:43]\n" \
"v_mul_f32_e32 v208, v56, v208\n" \
"v_mul_f32_e32 v209, v56, v209\n" \
M_"v[80:83], a[132:135], a[196:199], v[80:83]\n" \
"v_max_f32_e32 v62, v62, v57\n" \
C_"v202, v204, v205\n" \
M_"v[80:83], a[136:139], a[200:203], v[80:83]\n" \
B_"v53, s[8:11], s64 offen offset:1024 sc1 lds\n" \
M_"v[80:83], a[140:143], a[204:207], v[80:83]\n" \
C_"v203, v206, v207\n" \
C_"v208, v208, v209\n" \
"buffer_store_dwordx4 v[200:203], v20, s[56:59], s70 offen offset:1024 sc1\n" \
M_"v[84:87], a[156:159], a[204:207], v[40:43]\n" \
"v_mul_f32_e32 v210, v56, v210\n" \
"v_mul_f32_e32 v211, v56, v211\n" \
M_"v[84:87], a[152:155], a[200:203], v[84:87]\n" \
"v_rcp_f32_e32 v57, v57\n" \
M_"v[84:87], a[148:151], a[196:199], v[84:87]\n" \
B_"v53, s[8:11], s64 offen offset:2048 sc1 lds\n" \
M_"v[84:87], a[144:147], a[192:195], v[84:87]\n" \
M_"a[112:115], a[120:123], v[200:203], a[112:115]\n" \
"v_mfma_f32_32x32x16_bf16 v[192:207], a[120:123], a[120:123], 0\n" \
C_"v209, v210, v211\n" \
"v_mul_f32_e32 v212, v56, v212\n" \
".fill (L_ab + 20480 - .) / 4, 4, 0xBF800000\n" \
"L_ap5:\n" \
"s_cbranch_execz L_ap0\n" \
"v_mul_f32_e32 v213, v56, v213\n" \
"v_mul_f32_e32 v214, v56, v214\n" \
"v_mul_f32_e32 v215, v56, v215\n" \
"v_mul_f32_e32 v216, v56, v216\n" \
M_"v[92:95], a[144:147], a[208:211], v[44:47]\n" \
R_"a[144:147], v48 offset:12288\n" \
"v_mul_f32_e32 v217, v56, v217\n" \
M_"v[92:95], a[148:151], a[212:215], v[92:95]\n" \
R_"a[148:151], v48 offset:13312\n" \
C_"v210, v212, v213\n" \
M_"v[92:95], a[152:155], a[216:219], v[92:95]\n" \
R_"a[152:155], v48 offset:14336\n" \
C_"v211, v214, v215\n" \
"buffer_store_dwordx4 v[208:211], v20, s[56:59], s70 offen offset:2048 sc1\n" \
M_"v[92:95], a[156:159], a[220:223], v[92:95]\n" \
R_"a[156:159], v48 offset:15360\n" \
C_"v216, v216, v217\n" \
M_"v[88:91], a[140:143], a[220:223], v[44:47]\n" \
R_"a[140:143], v48 offset:11264\n" \
"v_mul_f32_e32 v218, v56, v218\n" \
M_"v[88:91], a[136:139], a[216:219], v[88:91]\n" \
R_"a[136:139], v48 offset:10240\n" \
"v_mul_f32_e32 v219, v56, v219\n" \
M_"v[88:91], a[132:135], a[212:215], v[88:91]\n" \
R_"a[132:135], v48 offset:9216\n" \
"v_mul_f32_e32 v57, v57, v61\n" \
M_"v[88:91], a[128:131], a[208:211], v[88:91]\n" \
R_"a[128:131], v48 offset:8192\n" \
M_"a[112:115], a[120:123], v[208:211], a[112:115]\n" \
C_"v217, v218, v219\n" \
"v_mul_f32_e32 v220, v56, v220\n" \
"v_mul_f32_e32 v221, v56, v221\n" \
"v_mul_f32_e32 v222, v56, v222\n" \
"v_mul_f32_e32 v223, v56, v223\n" \
"v_mul_f32_e32 v224, v57, v224\n" \
"v_mul_f32_e32 v225, v57, v225\n" \
C_"v218, v220, v221\n" \
C_"v219, v222, v223\n" \
"buffer_store_dwordx4 v[216:219], v20, s[56:59], s70 offen offset:3072 sc1\n" \
C_"v224, v224, v225\n" \
"v_mul_f32_e32 v226, v57, v226\n" \
"v_mul_f32_e32 v227, v57, v227\n" \
M_"a[112:115], a[120:123], v[216:219], a[112:115]\n" \
"v_mfma_f32_32x32x16_bf16 v[208:223], a[120:123], a[120:123], 0\n" \
C_"v225, v226, v227\n" \
"v_mul_f32_e32 v228, v57, v228\n" \
"v_mul_f32_e32 v229, v57, v229\n" \
"v_mul_f32_e32 v230, v57, v230\n" \
"v_mul_f32_e32 v231, v57, v231\n" \
"v_mul_f32_e32 v232, v57, v232\n" \
"v_mul_f32_e32 v233, v57, v233\n" \
C_"v226, v228, v229\n" \
C_"v227, v230, v231\n" \
"buffer_store_dwordx4 v[224:227], v20, s[56:59], s71 offen offset:0 sc1\n" \
C_"v232, v232, v233\n" \
"v_mul_f32_e32 v234, v57, v234\n" \
"v_mul_f32_e32 v235, v57, v235\n" \
M_"a[112:115], a[120:123], v[224:227], a[112:115]\n" \
C_"v233, v234, v235\n" \
"v_mul_f32_e32 v236, v57, v236\n" \
"v_mul_f32_e32 v237, v57, v237\n" \
"v_mul_f32_e32 v238, v57, v238\n" \
"v_mul_f32_e32 v239, v57, v239\n" \
"v_mul_f32_e32 v240, v57, v240\n" \
"v_mul_f32_e32 v241, v57, v241\n" \
C_"v234, v236, v237\n" \
C_"v235, v238, v239\n" \
"buffer_store_dwordx4 v[232:235], v20, s[56:59], s71 offen offset:1024 sc1\n" \
C_"v240, v240, v241\n" \
"v_mul_f32_e32 v242, v57, v242\n" \
"v_mul_f32_e32 v243, v57, v243\n" \
M_"a[112:115], a[120:123], v[232:235], a[112:115]\n" \
"v_mfma_f32_32x32x16_bf16 v[224:239], a[120:123], a[120:123], 0\n" \
C_"v241, v242, v243\n" \
"v_mul_f32_e32 v244, v57, v244\n" \
"v_mul_f32_e32 v245, v57, v245\n" \
"v_mul_f32_e32 v246, v57, v246\n" \
"v_mul_f32_e32 v247, v57, v247\n" \
"v_mul_f32_e32 v248, v57, v248\n" \
"v_mul_f32_e32 v249, v57, v249\n" \
C_"v242, v244, v245\n" \
C_"v243, v246, v247\n" \
"buffer_store_dwordx4 v[240:243], v20, s[56:59], s71 offen offset:2048 sc1\n" \
C_"v248, v248, v249\n" \
"v_mul_f32_e32 v250, v57, v250\n" \
"v_mul_f32_e32 v251, v57, v251\n" \
M_"a[112:115], a[120:123], v[240:243], a[112:115]\n" \
C_"v249, v250, v251\n" \
"v_mul_f32_e32 v252, v57, v252\n" \
"v_mul_f32_e32 v253, v57, v253\n" \
"v_mul_f32_e32 v254, v57, v254\n" \
"v_mul_f32_e32 v255, v57, v255\n" \
C_"v250, v252, v253\n" \
C_"v251, v254, v255\n" \
"buffer_store_dwordx4 v[248:251], v20, s[56:59], s71 offen offset:3072 sc1\n" \
B_"v53, s[8:11], s64 offen offset:3072 sc1 lds\n" \
"s_waitcnt vmcnt(16) lgkmcnt(0)\n" \
"s_barrier\n" \
M_"a[112:115], a[120:123], v[248:251], a[112:115]\n" \
"v_mfma_f32_32x32x16_bf16 v[240:255], a[120:123], a[120:123], 0\n" \
E_"v70, v70\n" \
E_"v71, v71\n" \
E_"v72, v72\n" \
E_"v73, v73\n" \
"s_add_u32 s64, s64, 0x10000\n" \
"v_cmp_lt_f32_e64 s[72:73], s21, v62\n" \
"v_readlane_b32 s95, v11, 3\n" \
"v_accvgpr_read_b32 v63, a112\n" \
"v_cmp_u_f32_e32 vcc, v63, v63\n" \
E_"v68, v68\n" \
E_"v69, v69\n" \
E_"v74, v74\n" \
E_"v75, v75\n" \
"s_or_b64 vcc, s[72:73], vcc\n" \
"s_cselect_b32 s95, 0x40000000, s95\n" \
"s_cselect_b32 s94, 0x7f800000, 0\n" \
C_"v71, v70, v71\n" \
E_"v66, v66\n" \
E_"v67, v67\n" \
E_"v64, v64\n" \
E_"v65, v65\n" \
C_"v72, v72, v73\n" \
E_"v76, v76\n" \
E_"v77, v77\n" \
E_"v78, v78\n" \
E_"v79, v79\n" \
C_"v70, v68, v69\n" \
C_"v73, v74, v75\n" \
"v_add_u32_e32 v49, 0x10000, v48\n" \
"s_xor_b32 s65, s65, 0x8000\n" \
C_"v69, v66, v67\n" \
C_"v68, v64, v65\n" \
C_"v74, v76, v77\n" \
C_"v75, v78, v79\n" \
"v_xor_b32_e32 v48, 0x8000, v48\n" \
"s_sub_u32 s66, s19, 2\n" \
"s_branch L_2_%=\n" \
"L_f1_%=:\n" \
R_"a[160:163], v49 offset:0\n" \
R_"a[164:167], v49 offset:1024\n" \
R_"a[168:171], v49 offset:2048\n" \
M_"v[104:107], a[128:131], a[80:83], v[36:39]\n" \
C_"v192, v192, v193\n" \
C_"v224, v224, v225\n" \
C_"v193, v194, v195\n" \
C_"v225, v226, v227\n" \
R_"a[172:175], v49 offset:3072\n" \
C_"v194, v196, v197\n" \
C_"v195, v198, v199\n" \
C_"v226, v228, v229\n" \
C_"v227, v230, v231\n" \
M_"v[104:107], a[132:135], a[84:87], v[104:107]\n" \
"v_lshl_add_u32 v50, s15, 2, v20\n" \
C_"v196, v200, v201\n" \
C_"v197, v202, v203\n" \
C_"v198, v204, v205\n" \
C_"v199, v206, v207\n" \
C_"v228, v232, v233\n" \
C_"v229, v234, v235\n" \
C_"v230, v236, v237\n" \
C_"v231, v238, v239\n" \
"v_add_u32_e32 v50, 0x1a000, v50\n" \
"v_mov_b32_e32 v61, s62\n" \
C_"v200, v208, v209\n" \
C_"v201, v210, v211\n" \
C_"v202, v212, v213\n" \
C_"v203, v214, v215\n" \
C_"v204, v216, v217\n" \
C_"v205, v218, v219\n" \
C_"v206, v220, v221\n" \
C_"v207, v222, v223\n" \
C_"v232, v240, v241\n" \
C_"v233, v242, v243\n" \
C_"v234, v244, v245\n" \
C_"v235, v246, v247\n" \
C_"v236, v248, v249\n" \
C_"v237, v250, v251\n" \
C_"v238, v252, v253\n" \
C_"v239, v254, v255\n" \
"ds_write_b128 v50, v[192:195] offset:0\n" \
M_"v[104:107], a[136:139], a[88:91], v[104:107]\n" \
R_"a[176:179], v49 offset:4096\n" \
"v_add_u32_e32 v56, 0x49000, v20\n" \
M_"v[104:107], a[140:143], a[92:95], v[104:107]\n" \
"ds_write_b128 v50, v[196:199] offset:1024\n" \
M_"v[108:111], a[156:159], a[92:95], v[36:39]\n" \
R_"a[180:183], v49 offset:5120\n" \
R_"a[184:187], v49 offset:6144\n" \
M_"v[108:111], a[152:155], a[88:91], v[108:111]\n" \
"ds_write_b128 v50, v[200:203] offset:2048\n" \
M_"v[108:111], a[148:151], a[84:87], v[108:111]\n" \
"v_and_b32_e32 v68, v12, v68\n" \
"v_and_b32_e32 v69, v13, v69\n" \
M_"v[108:111], a[144:147], a[80:83], v[108:111]\n" \
"ds_write_b128 v50, v[204:207] offset:3072\n" \
"v_and_b32_e32 v70, v14, v70\n" \
"v_and_b32_e32 v71, v15, v71\n" \
R_"a[188:191], v49 offset:7168\n" \
R_"a[0:3], v20 offset:8192\n" \
"s_waitcnt lgkmcnt(9)\n" \
M_"v[128:131], a[160:163], v[68:71], v[128:131]\n" \
"ds_write_b128 v50, v[224:227] offset:4096\n" \
M_"v[160:163], a[160:163], v[72:75], v[160:163]\n" \
R_"a[4:7], v20 offset:9216\n" \
R_"a[8:11], v20 offset:10240\n" \
M_"v[132:135], a[164:167], v[68:71], v[132:135]\n" \
"ds_write_b128 v50, v[228:231] offset:5120\n" \
M_"v[164:167], a[164:167], v[72:75], v[164:167]\n" \
R_"a[12:15], v20 offset:11264\n" \
R_"a[28:31], v20 offset:15360\n" \
M_"v[136:139], a[168:171], v[68:71], v[136:139]\n" \
"ds_write_b128 v50, v[232:235] offset:6144\n" \
M_"v[168:171], a[168:171], v[72:75], v[168:171]\n" \
R_"a[24:27], v20 offset:14336\n" \
R_"a[20:23], v20 offset:13312\n" \
M_"v[140:143], a[172:175], v[68:71], v[140:143]\n" \
"ds_write_b128 v50, v[236:239] offset:7168\n" \
M_"v[172:175], a[172:175], v[72:75], v[172:175]\n" \
R_"a[16:19], v20 offset:12288\n" \
"s_waitcnt lgkmcnt(15)\n" \
M_"v[144:147], a[176:179], v[68:71], v[144:147]\n" \
"v_xor_b32_e32 v48, 0x4000, v49\n" \
"ds_read_b32 v58, v18 offset:256\n" \
M_"v[176:179], a[176:179], v[72:75], v[176:179]\n" \
"ds_read_b32 v59, v18 offset:384\n" \
R_"a[32:35], v48 offset:8192\n" \
M_"v[148:151], a[180:183], v[68:71], v[148:151]\n" \
R_"a[36:39], v48 offset:9216\n" \
R_"a[40:43], v48 offset:10240\n" \
M_"v[180:183], a[180:183], v[72:75], v[180:183]\n" \
R_"a[44:47], v48 offset:11264\n" \
R_"a[48:51], v48 offset:12288\n" \
M_"v[152:155], a[184:187], v[68:71], v[152:155]\n" \
R_"a[52:55], v48 offset:13312\n" \
R_"a[56:59], v48 offset:14336\n" \
M_"v[184:187], a[184:187], v[72:75], v[184:187]\n" \
R_"a[60:63], v48 offset:15360\n" \
"s_waitcnt lgkmcnt(15)\n" \
M_"v[156:159], a[188:191], v[68:71], v[156:159]\n" \
R_"a[128:131], v49 offset:8192\n" \
R_"a[132:135], v49 offset:9216\n" \
M_"v[188:191], a[188:191], v[72:75], v[188:191]\n" \
R_"a[136:139], v49 offset:10240\n" \
R_"a[140:143], v49 offset:11264\n" \
"v_smfmac_f32_16x16x64_bf16 a[96:99], a[104:107], v[68:75], v21\n" \
R_"a[144:147], v49 offset:12288\n" \
R_"a[148:151], v49 offset:13312\n" \
M_"v[112:115], a[0:3], a[64:67], v[32:35]\n" \
R_"a[152:155], v49 offset:14336\n" \
R_"a[156:159], v49 offset:15360\n" \
M_"v[112:115], a[4:7], a[68:71], v[112:115]\n" \
"v_accvgpr_read_b32 v40, a100\n" \
"v_accvgpr_read_b32 v24, a101\n" \
M_"v[112:115], a[8:11], a[72:75], v[112:115]\n" \
"v_accvgpr_read_b32 v41, a102\n" \
"v_accvgpr_read_b32 v25, a103\n" \
M_"v[112:115], a[12:15], a[76:79], v[112:115]\n" \
"v_add_f32_e32 v40, v40, v24\n" \
"v_add_f32_e32 v41, v41, v25\n" \
M_"v[116:119], a[28:31], a[76:79], v[32:35]\n" \
"v_add_f32_e32 v40, s94, v40\n" \
"s_waitcnt lgkmcnt(15)\n" \
M_"v[116:119], a[24:27], a[72:75], v[116:119]\n" \
"v_add_f32_e32 v41, s94, v41\n" \
"ds_write_b64 v19, v[40:41]\n" \
M_"v[116:119], a[20:23], a[68:71], v[116:119]\n" \
"v_add_u32_e32 v52, s15, v20\n" \
"v_add_u32_e32 v51, 0x10000, v52\n" \
M_"v[116:119], a[16:19], a[64:67], v[116:119]\n" \
E_"v104, v104\n" \
M_"v[124:127], a[16:19], a[80:83], v[36:39]\n" \
E_"v105, v105\n" \
M_"v[124:127], a[20:23], a[84:87], v[124:127]\n" \
E_"v106, v106\n" \
M_"v[124:127], a[24:27], a[88:91], v[124:127]\n" \
E_"v107, v107\n" \
M_"v[124:127], a[28:31], a[92:95], v[124:127]\n" \
C_"v104, v104, v105\n" \
C_"v105, v106, v107\n" \
M_"v[120:123], a[12:15], a[92:95], v[36:39]\n" \
E_"v108, v108\n" \
M_"v[120:123], a[8:11], a[88:91], v[120:123]\n" \
E_"v109, v109\n" \
M_"v[120:123], a[4:7], a[84:87], v[120:123]\n" \
E_"v110, v110\n" \
M_"v[120:123], a[0:3], a[80:83], v[120:123]\n" \
E_"v111, v111\n" \
"s_waitcnt lgkmcnt(0)\n" \
"s_barrier\n" \
R_"v[208:211], v52 offset:0\n" \
R_"v[212:215], v52 offset:1024\n" \
R_"v[216:219], v52 offset:2048\n" \
R_"v[220:223], v52 offset:3072\n" \
R_"v[240:243], v51 offset:32768\n" \
R_"v[244:247], v51 offset:33792\n" \
R_"v[248:251], v51 offset:34816\n" \
R_"v[252:255], v51 offset:35840\n" \
C_"v106, v108, v109\n" \
C_"v107, v110, v111\n" \
"v_and_b32_e32 v104, v12, v104\n" \
"v_and_b32_e32 v105, v13, v105\n" \
"v_and_b32_e32 v106, v14, v106\n" \
"s_waitcnt vmcnt(0)\n" \
"s_mov_b64 exec, 1\n" \
"buffer_store_dword v61, off, s[56:59], s95 offset:0 sc1\n" \
"s_mov_b64 exec, -1\n" \
"v_and_b32_e32 v107, v15, v107\n" \
"v_xor_b32_e32 v63, 0x400, v19\n" \
"ds_read_b64 v[42:43], v63\n" \
M_"a[96:99], a[116:119], v[104:107], a[96:99]\n" \
"v_mov_b32_e32 v194, v28\n" \
"v_mov_b32_e32 v195, v29\n" \
M_"v[160:163], a[128:131], v[104:107], v[160:163]\n" \
"v_mov_b32_e32 v196, 0\n" \
"v_mov_b32_e32 v197, 0\n" \
M_"v[164:167], a[132:135], v[104:107], v[164:167]\n" \
"v_mov_b32_e32 v192, 0\n" \
"v_mov_b32_e32 v193, 0\n" \
M_"v[168:171], a[136:139], v[104:107], v[168:171]\n" \
"v_readlane_b32 s93, v11, 25\n" \
"v_readlane_b32 s92, v11, 30\n" \
M_"v[172:175], a[140:143], v[104:107], v[172:175]\n" \
B_"a[192:195], v20, s[56:59], s60 offen offset:0 sc1\n" \
M_"v[176:179], a[144:147], v[104:107], v[176:179]\n" \
E_"v118, v118\n" \
M_"v[180:183], a[148:151], v[104:107], v[180:183]\n" \
E_"v119, v119\n" \
M_"v[184:187], a[152:155], v[104:107], v[184:187]\n" \
"s_add_u32 m0, s93, 0x0\n" \
C_"v119, v118, v119\n" \
M_"v[188:191], a[156:159], v[104:107], v[188:191]\n" \
E_"v120, v120\n" \
"s_waitcnt lgkmcnt(1)\n" \
"s_barrier\n" \
B_"a[208:211], v56, s[56:59], s60 offen offset:0 sc1\n" \
M_"v[128:131], v[194:197], v[208:211], v[128:131]\n" \
E_"v121, v121\n" \
M_"v[132:135], v[192:195], v[208:211], v[132:135]\n" \
E_"v116, v116\n" \
M_"v[136:139], v[194:197], v[212:215], v[136:139]\n" \
E_"v117, v117\n" \
M_"v[140:143], v[192:195], v[212:215], v[140:143]\n" \
E_"v122, v122\n" \
M_"v[144:147], v[194:197], v[216:219], v[144:147]\n" \
E_"v123, v123\n" \
M_"v[148:151], v[192:195], v[216:219], v[148:151]\n" \
B_"a[196:199], v20, s[56:59], s60 offen offset:1024 sc1\n" \
M_"v[152:155], v[194:197], v[220:223], v[152:155]\n" \
E_"v114, v114\n" \
M_"v[156:159], v[192:195], v[220:223], v[156:159]\n" \
E_"v115, v115\n" \
M_"v[160:163], v[194:197], v[240:243], v[160:163]\n" \
E_"v112, v112\n" \
M_"v[164:167], v[192:195], v[240:243], v[164:167]\n" \
E_"v113, v113\n" \
M_"v[168:171], v[194:197], v[244:247], v[168:171]\n" \
C_"v120, v120, v121\n" \
C_"v118, v116, v117\n" \
M_"v[172:175], v[192:195], v[244:247], v[172:175]\n" \
B_"a[212:215], v56, s[56:59], s60 offen offset:1024 sc1\n" \
M_"v[176:179], v[194:197], v[248:251], v[176:179]\n" \
E_"v124, v124\n" \
M_"v[180:183], v[192:195], v[248:251], v[180:183]\n" \
E_"v125, v125\n" \
M_"v[184:187], v[194:197], v[252:255], v[184:187]\n" \
E_"v126, v126\n" \
M_"v[188:191], v[192:195], v[252:255], v[188:191]\n" \
E_"v127, v127\n" \
C_"v121, v122, v123\n" \
C_"v117, v114, v115\n" \
C_"v116, v112, v113\n" \
C_"v122, v124, v125\n" \
C_"v123, v126, v127\n" \
B_"a[200:203], v20, s[56:59], s60 offen offset:2048 sc1\n" \
"s_nop 0\n" \
"v_smfmac_f32_16x16x64_bf16 a[96:99], a[104:107], v[116:123], v21\n" \
M_"v[128:131], a[32:35], v[116:119], v[128:131]\n" \
M_"v[160:163], a[32:35], v[120:123], v[160:163]\n" \
M_"v[132:135], a[36:39], v[116:119], v[132:135]\n" \
M_"v[164:167], a[36:39], v[120:123], v[164:167]\n" \
M_"v[136:139], a[40:43], v[116:119], v[136:139]\n" \
B_"a[216:219], v56, s[56:59], s60 offen offset:2048 sc1\n" \
M_"v[168:171], a[40:43], v[120:123], v[168:171]\n" \
M_"v[140:143], a[44:47], v[116:119], v[140:143]\n" \
"v_accvgpr_read_b32 v54, a96\n" \
"v_accvgpr_read_b32 v22, a97\n" \
M_"v[172:175], a[44:47], v[120:123], v[172:175]\n" \
"v_add_f32_e32 v54, v54, v22\n" \
"s_waitcnt lgkmcnt(0)\n" \
"v_add_f32_e32 v54, v54, v42\n" \
M_"v[144:147], a[48:51], v[116:119], v[144:147]\n" \
"v_max_f32_e32 v62, v62, v54\n" \
"v_accvgpr_read_b32 v55, a98\n" \
M_"v[176:179], a[48:51], v[120:123], v[176:179]\n" \
"v_rcp_f32_e32 v54, v54\n" \
B_"a[204:207], v20, s[56:59], s60 offen offset:3072 sc1\n" \
M_"v[148:151], a[52:55], v[116:119], v[148:151]\n" \
"v_mul_f32_e32 v54, v54, v58\n" \
"v_mul_f32_e32 v128, v54, v128\n" \
M_"v[180:183], a[52:55], v[120:123], v[180:183]\n" \
"v_mul_f32_e32 v129, v54, v129\n" \
"v_mul_f32_e32 v130, v54, v130\n" \
"v_mul_f32_e32 v131, v54, v131\n" \
"v_mul_f32_e32 v132, v54, v132\n" \
"v_mul_f32_e32 v133, v54, v133\n" \
"v_mul_f32_e32 v134, v54, v134\n" \
"v_mul_f32_e32 v135, v54, v135\n" \
C_"v64, v128, v129\n" \
C_"v65, v130, v131\n" \
C_"v66, v132, v133\n" \
C_"v67, v134, v135\n" \
"v_accvgpr_read_b32 v23, a99\n" \
M_"v[152:155], a[56:59], v[116:119], v[152:155]\n" \
M_"a[112:115], a[120:123], v[64:67], a[112:115]\n" \
B_"a[220:223], v56, s[56:59], s60 offen offset:3072 sc1\n" \
"v_mul_f32_e32 v136, v54, v136\n" \
"v_mul_f32_e32 v137, v54, v137\n" \
"v_mul_f32_e32 v138, v54, v138\n" \
"v_mul_f32_e32 v139, v54, v139\n" \
"v_mul_f32_e32 v140, v54, v140\n" \
"v_mul_f32_e32 v141, v54, v141\n" \
"v_mul_f32_e32 v142, v54, v142\n" \
"v_mul_f32_e32 v143, v54, v143\n" \
"v_add_f32_e32 v55, v55, v23\n" \
C_"v68, v136, v137\n" \
C_"v69, v138, v139\n" \
C_"v70, v140, v141\n" \
C_"v71, v142, v143\n" \
"v_add_f32_e32 v55, v55, v43\n" \
"v_mul_f32_e32 v144, v54, v144\n" \
M_"a[112:115], a[120:123], v[68:71], a[112:115]\n" \
"v_mul_f32_e32 v145, v54, v145\n" \
"v_mul_f32_e32 v146, v54, v146\n" \
"v_mul_f32_e32 v147, v54, v147\n" \
"v_mul_f32_e32 v148, v54, v148\n" \
B_"v0, s[80:83], s92 offen offset:0 lds\n" \
"v_mul_f32_e32 v149, v54, v149\n" \
"v_mul_f32_e32 v150, v54, v150\n" \
"v_mul_f32_e32 v151, v54, v151\n" \
"v_max_f32_e32 v62, v62, v55\n" \
C_"v72, v144, v145\n" \
C_"v73, v146, v147\n" \
C_"v74, v148, v149\n" \
C_"v75, v150, v151\n" \
"v_rcp_f32_e32 v55, v55\n" \
M_"v[184:187], a[56:59], v[120:123], v[184:187]\n" \
M_"a[112:115], a[120:123], v[72:75], a[112:115]\n" \
"v_mul_f32_e32 v152, v54, v152\n" \
"v_mul_f32_e32 v153, v54, v153\n" \
"v_mul_f32_e32 v154, v54, v154\n" \
"v_mul_f32_e32 v155, v54, v155\n" \
C_"v76, v152, v153\n" \
B_"v1, s[80:83], s92 offen offset:1024 lds\n" \
C_"v77, v154, v155\n" \
"v_mul_f32_e32 v55, v55, v59\n" \
"v_mul_f32_e32 v160, v55, v160\n" \
"v_mul_f32_e32 v161, v55, v161\n" \
"v_mul_f32_e32 v162, v55, v162\n" \
"v_mul_f32_e32 v163, v55, v163\n" \
"v_mul_f32_e32 v164, v55, v164\n" \
"v_mul_f32_e32 v165, v55, v165\n" \
"v_mul_f32_e32 v166, v55, v166\n" \
"v_mul_f32_e32 v167, v55, v167\n" \
C_"v80, v160, v161\n" \
C_"v81, v162, v163\n" \
C_"v82, v164, v165\n" \
C_"v83, v166, v167\n" \
M_"v[156:159], a[60:63], v[116:119], v[156:159]\n" \
"v_mul_f32_e32 v168, v55, v168\n" \
"v_mul_f32_e32 v169, v55, v169\n" \
"v_mul_f32_e32 v170, v55, v170\n" \
"v_mul_f32_e32 v171, v55, v171\n" \
"v_mul_f32_e32 v172, v55, v172\n" \
B_"v2, s[80:83], s92 offen offset:2048 lds\n" \
"v_mul_f32_e32 v173, v55, v173\n" \
"v_mul_f32_e32 v174, v55, v174\n" \
"v_mul_f32_e32 v156, v54, v156\n" \
"v_mul_f32_e32 v157, v54, v157\n" \
"v_mul_f32_e32 v158, v54, v158\n" \
"v_mul_f32_e32 v159, v54, v159\n" \
C_"v78, v156, v157\n" \
C_"v79, v158, v159\n" \
"v_mul_f32_e32 v175, v55, v175\n" \
C_"v84, v168, v169\n" \
M_"a[112:115], a[120:123], v[76:79], a[112:115]\n" \
M_"a[112:115], a[120:123], v[80:83], a[112:115]\n" \
C_"v85, v170, v171\n" \
C_"v86, v172, v173\n" \
C_"v87, v174, v175\n" \
"v_mul_f32_e32 v176, v55, v176\n" \
"v_mul_f32_e32 v177, v55, v177\n" \
B_"v3, s[80:83], s92 offen offset:3072 lds\n" \
"s_add_u32 m0, s93, 0x1000\n" \
M_"a[112:115], a[120:123], v[84:87], a[112:115]\n" \
"v_mul_f32_e32 v178, v55, v178\n" \
"v_mul_f32_e32 v179, v55, v179\n" \
"v_mul_f32_e32 v180, v55, v180\n" \
"v_mul_f32_e32 v181, v55, v181\n" \
"v_mul_f32_e32 v182, v55, v182\n" \
"v_mul_f32_e32 v183, v55, v183\n" \
C_"v96, v176, v177\n" \
C_"v97, v178, v179\n" \
C_"v98, v180, v181\n" \
C_"v99, v182, v183\n" \
M_"v[188:191], a[60:63], v[120:123], v[188:191]\n" \
"v_mul_f32_e32 v184, v55, v184\n" \
M_"a[112:115], a[120:123], v[96:99], a[112:115]\n" \
"v_mul_f32_e32 v185, v55, v185\n" \
"v_mul_f32_e32 v186, v55, v186\n" \
B_"v4, s[80:83], s92 offen offset:0 lds\n" \
"v_mul_f32_e32 v187, v55, v187\n" \
C_"v100, v184, v185\n" \
C_"v101, v186, v187\n" \
"v_mul_f32_e32 v188, v55, v188\n" \
"v_mul_f32_e32 v189, v55, v189\n" \
"v_mul_f32_e32 v190, v55, v190\n" \
"v_mul_f32_e32 v191, v55, v191\n" \
C_"v102, v188, v189\n" \
C_"v103, v190, v191\n" \
"v_cmp_lt_f32_e64 s[72:73], s21, v62\n" \
B_"v5, s[80:83], s92 offen offset:1024 lds\n" \
M_"a[112:115], a[120:123], v[100:103], a[112:115]\n" \
B_"v6, s[80:83], s92 offen offset:2048 lds\n" \
B_"v7, s[80:83], s92 offen offset:3072 lds\n" \
"s_add_u32 m0, s93, 0x2000\n" \
"s_nop 0\n" \
B_"v8, s[80:83], s92 offen offset:0 lds\n" \
"s_nop 2\n" \
"v_accvgpr_read_b32 v63, a112\n" \
"v_cmp_u_f32_e32 vcc, v63, v63\n" \
"s_nop 0\n" \
"s_or_b64 vcc, s[72:73], vcc\n" \
"s_cbranch_scc1 L_end_%=\n" \
"s_branch L_out_%=\n" \
".fill (L_ab + 24576 - .) / 4, 4, 0xBF800000\n" \
".fill (L_ab + 24576 - .) / 4, 4, 0xBF800000\n" \
"L_ap6:\n" \
"s_setpc_b64 s[88:89]\n" \
"L_gs7:\n" \
M_"v[104:107], a[128:131], a[16:19], v[36:39]\n" \
"s_add_u32 s68, s60, 0xfff6e000\n" \
"s_add_u32 s69, s60, 0xfffb7000\n" \
"s_add_u32 s70, s60, 0x0\n" \
"s_add_u32 s71, s60, 0x49000\n" \
M_"v[104:107], a[132:135], a[20:23], v[104:107]\n" \
"s_xor_b32 m0, s65, 0x12000\n" \
R_"a[160:163], v49 offset:0\n" \
M_"v[104:107], a[136:139], a[24:27], v[104:107]\n" \
B_"v50, s[8:11], s64 offen offset:0 sc1 lds\n" \
M_"v[104:107], a[140:143], a[28:31], v[104:107]\n" \
R_"a[164:167], v49 offset:1024\n" \
R_"a[168:171], v49 offset:2048\n" \
M_"v[108:111], a[156:159], a[28:31], v[36:39]\n" \
R_"a[172:175], v49 offset:3072\n" \
R_"a[176:179], v49 offset:4096\n" \
M_"v[108:111], a[152:155], a[24:27], v[108:111]\n" \
B_"v50, s[8:11], s64 offen offset:1024 sc1 lds\n" \
M_"v[108:111], a[148:151], a[20:23], v[108:111]\n" \
"s_mov_b32 s65, s13\n" \
"v_add_u32_e32 v63, s15, v20\n" \
M_"v[108:111], a[144:147], a[16:19], v[108:111]\n" \
"s_add_u32 s67, s14, 40960\n" \
"v_add_u32_e32 v50, 0x6000, v63\n" \
M_"v[116:119], a[144:147], a[32:35], v[40:43]\n" \
"s_xor_b32 m0, s65, 0x8000\n" \
R_"a[180:183], v49 offset:5120\n" \
M_"v[116:119], a[148:151], a[36:39], v[116:119]\n" \
R_"a[184:187], v49 offset:6144\n" \
"v_and_b32_e32 v68, v12, v68\n" \
M_"v[116:119], a[152:155], a[40:43], v[116:119]\n" \
"v_and_b32_e32 v69, v13, v69\n" \
"v_and_b32_e32 v70, v14, v70\n" \
M_"v[116:119], a[156:159], a[44:47], v[116:119]\n" \
B_"v50, s[8:11], s67 offen offset:0 sc1 lds\n" \
M_"v[112:115], a[140:143], a[44:47], v[40:43]\n" \
"v_and_b32_e32 v71, v15, v71\n" \
R_"a[188:191], v49 offset:7168\n" \
M_"v[112:115], a[136:139], a[40:43], v[112:115]\n" \
E_"v86, v86\n" \
M_"v[112:115], a[132:135], a[36:39], v[112:115]\n" \
E_"v87, v87\n" \
M_"v[112:115], a[128:131], a[32:35], v[112:115]\n" \
E_"v84, v84\n" \
M_"v[120:123], a[128:131], a[48:51], v[44:47]\n" \
B_"v50, s[8:11], s67 offen offset:1024 sc1 lds\n" \
M_"v[120:123], a[132:135], a[52:55], v[120:123]\n" \
E_"v85, v85\n" \
M_"v[120:123], a[136:139], a[56:59], v[120:123]\n" \
C_"v87, v86, v87\n" \
C_"v86, v84, v85\n" \
M_"v[120:123], a[140:143], a[60:63], v[120:123]\n" \
E_"v82, v82\n" \
M_"v[124:127], a[156:159], a[60:63], v[44:47]\n" \
E_"v83, v83\n" \
M_"v[124:127], a[152:155], a[56:59], v[124:127]\n" \
E_"v80, v80\n" \
M_"v[124:127], a[148:151], a[52:55], v[124:127]\n" \
B_"v50, s[8:11], s67 offen offset:2048 sc1 lds\n" \
M_"v[124:127], a[144:147], a[48:51], v[124:127]\n" \
C_"v85, v82, v83\n" \
"s_waitcnt lgkmcnt(1)\n" \
M_"v[128:131], a[160:163], v[68:71], v[128:131]\n" \
E_"v81, v81\n" \
M_"v[160:163], a[160:163], v[72:75], v[160:163]\n" \
C_"v84, v80, v81\n" \
"s_mov_b32 s64, s14\n" \
M_"v[132:135], a[164:167], v[68:71], v[132:135]\n" \
E_"v88, v88\n" \
M_"v[164:167], a[164:167], v[72:75], v[164:167]\n" \
E_"v89, v89\n" \
M_"v[136:139], a[168:171], v[68:71], v[136:139]\n" \
B_"v50, s[8:11], s67 offen offset:3072 sc1 lds\n" \
M_"v[168:171], a[168:171], v[72:75], v[168:171]\n" \
E_"v90, v90\n" \
M_"v[140:143], a[172:175], v[68:71], v[140:143]\n" \
E_"v91, v91\n" \
M_"v[172:175], a[172:175], v[72:75], v[172:175]\n" \
C_"v88, v88, v89\n" \
C_"v89, v90, v91\n" \
M_"v[144:147], a[176:179], v[68:71], v[144:147]\n" \
E_"v92, v92\n" \
M_"v[176:179], a[176:179], v[72:75], v[176:179]\n" \
E_"v93, v93\n" \
M_"v[148:151], a[180:183], v[68:71], v[148:151]\n" \
E_"v94, v94\n" \
M_"v[180:183], a[180:183], v[72:75], v[180:183]\n" \
E_"v95, v95\n" \
M_"v[152:155], a[184:187], v[68:71], v[152:155]\n" \
C_"v90, v92, v93\n" \
C_"v91, v94, v95\n" \
M_"v[184:187], a[184:187], v[72:75], v[184:187]\n" \
"v_add_u32_e32 v51, 0x12000, v63\n" \
"s_waitcnt lgkmcnt(0)\n" \
M_"v[156:159], a[188:191], v[68:71], v[156:159]\n" \
"s_xor_b32 m0, s65, 0xa000\n" \
"ds_read_b32 v58, v18 offset:512\n" \
M_"v[188:191], a[188:191], v[72:75], v[188:191]\n" \
B_"v51, s[8:11], s64 offen offset:0 sc1 lds\n" \
"v_smfmac_f32_16x16x64_bf16 a[96:99], a[104:107], v[68:75], v21\n" \
"ds_read_b32 v59, v18 offset:640\n" \
"ds_read_b32 v60, v18 offset:768\n" \
M_"v[220:223], a[188:191], v[84:87], v[220:223]\n" \
B_"v51, s[8:11], s64 offen offset:1024 sc1 lds\n" \
M_"v[252:255], a[188:191], v[88:91], v[252:255]\n" \
"ds_read_b32 v61, v18 offset:896\n" \
R_"a[128:131], v48 offset:0\n" \
M_"v[216:219], a[184:187], v[84:87], v[216:219]\n" \
R_"a[144:147], v48 offset:4096\n" \
R_"a[132:135], v48 offset:1024\n" \
M_"v[248:251], a[184:187], v[88:91], v[248:251]\n" \
R_"a[148:151], v48 offset:5120\n" \
R_"a[136:139], v48 offset:2048\n" \
M_"v[212:215], a[180:183], v[84:87], v[212:215]\n" \
R_"a[152:155], v48 offset:6144\n" \
R_"a[140:143], v48 offset:3072\n" \
M_"v[244:247], a[180:183], v[88:91], v[244:247]\n" \
R_"a[156:159], v48 offset:7168\n" \
"v_add_u32_e32 v52, 0x14000, v63\n" \
M_"v[208:211], a[176:179], v[84:87], v[208:211]\n" \
B_"v51, s[8:11], s64 offen offset:2048 sc1 lds\n" \
M_"v[240:243], a[176:179], v[88:91], v[240:243]\n" \
E_"v104, v104\n" \
M_"v[204:207], a[172:175], v[84:87], v[204:207]\n" \
E_"v105, v105\n" \
M_"v[236:239], a[172:175], v[88:91], v[236:239]\n" \
B_"v51, s[8:11], s64 offen offset:3072 sc1 lds\n" \
M_"v[200:203], a[168:171], v[84:87], v[200:203]\n" \
E_"v106, v106\n" \
M_"v[232:235], a[168:171], v[88:91], v[232:235]\n" \
E_"v107, v107\n" \
M_"v[196:199], a[164:167], v[84:87], v[196:199]\n" \
C_"v104, v104, v105\n" \
C_"v105, v106, v107\n" \
M_"v[228:231], a[164:167], v[88:91], v[228:231]\n" \
E_"v108, v108\n" \
M_"v[192:195], a[160:163], v[84:87], v[192:195]\n" \
E_"v109, v109\n" \
M_"v[224:227], a[160:163], v[88:91], v[224:227]\n" \
E_"v110, v110\n" \
"v_smfmac_f32_16x16x64_bf16 a[100:103], a[104:107], v[84:91], v21\n" \
E_"v111, v111\n" \
C_"v106, v108, v109\n" \
C_"v107, v110, v111\n" \
"v_and_b32_e32 v104, v12, v104\n" \
"v_and_b32_e32 v105, v13, v105\n" \
"v_and_b32_e32 v106, v14, v106\n" \
"v_and_b32_e32 v107, v15, v107\n" \
"v_accvgpr_read_b32 v54, a96\n" \
"v_accvgpr_read_b32 v22, a97\n" \
"v_readlane_b32 s93, v11, 22\n" \
"v_readlane_b32 s80, v11, 11\n" \
"v_readlane_b32 s81, v11, 12\n" \
"s_mov_b32 s82, 0x47fca0\n" \
"s_mov_b32 s83, 0x20000\n" \
"v_readlane_b32 s92, v11, 29\n" \
E_"v118, v118\n" \
E_"v119, v119\n" \
E_"v116, v116\n" \
E_"v117, v117\n" \
C_"v119, v118, v119\n" \
E_"v114, v114\n" \
E_"v115, v115\n" \
E_"v112, v112\n" \
C_"v118, v116, v117\n" \
"s_waitcnt vmcnt(10) lgkmcnt(0)\n" \
"s_barrier\n" \
M_"v[80:83], a[128:131], a[32:35], v[40:43]\n" \
R_"a[160:163], v49 offset:8192\n" \
R_"a[164:167], v49 offset:9216\n" \
M_"v[80:83], a[132:135], a[36:39], v[80:83]\n" \
R_"a[168:171], v49 offset:10240\n" \
"s_xor_b32 m0, s65, 0x18000\n" \
M_"v[80:83], a[136:139], a[40:43], v[80:83]\n" \
B_"v52, s[8:11], s64 offen offset:0 sc1 lds\n" \
M_"v[80:83], a[140:143], a[44:47], v[80:83]\n" \
R_"a[172:175], v49 offset:11264\n" \
R_"a[176:179], v49 offset:12288\n" \
M_"v[84:87], a[156:159], a[44:47], v[40:43]\n" \
R_"a[180:183], v49 offset:13312\n" \
R_"a[184:187], v49 offset:14336\n" \
M_"v[84:87], a[152:155], a[40:43], v[84:87]\n" \
B_"v52, s[8:11], s64 offen offset:1024 sc1 lds\n" \
M_"v[84:87], a[148:151], a[36:39], v[84:87]\n" \
R_"a[188:191], v49 offset:15360\n" \
C_"v117, v114, v115\n" \
M_"v[84:87], a[144:147], a[32:35], v[84:87]\n" \
B_"v52, s[8:11], s64 offen offset:2048 sc1 lds\n" \
M_"v[92:95], a[144:147], a[48:51], v[44:47]\n" \
E_"v113, v113\n" \
M_"v[92:95], a[148:151], a[52:55], v[92:95]\n" \
B_"v52, s[8:11], s64 offen offset:3072 sc1 lds\n" \
M_"v[92:95], a[152:155], a[56:59], v[92:95]\n" \
"s_add_u32 m0, s93, 0x0\n" \
C_"v116, v112, v113\n" \
M_"v[92:95], a[156:159], a[60:63], v[92:95]\n" \
E_"v120, v120\n" \
M_"v[88:91], a[140:143], a[60:63], v[44:47]\n" \
B_"v0, s[80:83], s92 offen offset:0 lds\n" \
M_"v[88:91], a[136:139], a[56:59], v[88:91]\n" \
E_"v121, v121\n" \
M_"v[88:91], a[132:135], a[52:55], v[88:91]\n" \
E_"v122, v122\n" \
M_"v[88:91], a[128:131], a[48:51], v[88:91]\n" \
B_"v1, s[80:83], s92 offen offset:1024 lds\n" \
M_"a[96:99], a[116:119], v[104:107], a[96:99]\n" \
C_"v120, v120, v121\n" \
"s_waitcnt lgkmcnt(5)\n" \
M_"v[160:163], a[160:163], v[104:107], v[160:163]\n" \
E_"v123, v123\n" \
M_"v[164:167], a[164:167], v[104:107], v[164:167]\n" \
B_"v2, s[80:83], s92 offen offset:2048 lds\n" \
M_"v[168:171], a[168:171], v[104:107], v[168:171]\n" \
C_"v121, v122, v123\n" \
"s_waitcnt lgkmcnt(1)\n" \
M_"v[172:175], a[172:175], v[104:107], v[172:175]\n" \
E_"v124, v124\n" \
M_"v[176:179], a[176:179], v[104:107], v[176:179]\n" \
E_"v125, v125\n" \
M_"v[180:183], a[180:183], v[104:107], v[180:183]\n" \
E_"v126, v126\n" \
M_"v[184:187], a[184:187], v[104:107], v[184:187]\n" \
C_"v122, v124, v125\n" \
"s_waitcnt lgkmcnt(0)\n" \
M_"v[188:191], a[188:191], v[104:107], v[188:191]\n" \
E_"v127, v127\n" \
M_"v[220:223], a[188:191], v[116:119], v[220:223]\n" \
C_"v123, v126, v127\n" \
B_"v3, s[80:83], s92 offen offset:3072 lds\n" \
"s_add_u32 m0, s93, 0x1000\n" \
M_"v[252:255], a[188:191], v[120:123], v[252:255]\n" \
R_"a[128:131], v48 offset:8192\n" \
R_"a[144:147], v48 offset:12288\n" \
M_"v[216:219], a[184:187], v[116:119], v[216:219]\n" \
B_"v4, s[80:83], s92 offen offset:0 lds\n" \
M_"v[248:251], a[184:187], v[120:123], v[248:251]\n" \
R_"a[132:135], v48 offset:9216\n" \
R_"a[148:151], v48 offset:13312\n" \
M_"v[212:215], a[180:183], v[116:119], v[212:215]\n" \
B_"v5, s[80:83], s92 offen offset:1024 lds\n" \
M_"v[244:247], a[180:183], v[120:123], v[244:247]\n" \
R_"a[136:139], v48 offset:10240\n" \
R_"a[152:155], v48 offset:14336\n" \
M_"v[208:211], a[176:179], v[116:119], v[208:211]\n" \
B_"v6, s[80:83], s92 offen offset:2048 lds\n" \
M_"v[240:243], a[176:179], v[120:123], v[240:243]\n" \
R_"a[140:143], v48 offset:11264\n" \
R_"a[156:159], v48 offset:15360\n" \
M_"v[204:207], a[172:175], v[116:119], v[204:207]\n" \
B_"v7, s[80:83], s92 offen offset:3072 lds\n" \
M_"v[236:239], a[172:175], v[120:123], v[236:239]\n" \
"v_readlane_b32 s96, v11, 1\n" \
"v_lshrrev_b32_e32 v16, 1, v20\n" \
M_"v[200:203], a[168:171], v[116:119], v[200:203]\n" \
"v_add_f32_e32 v54, v54, v22\n" \
"v_max_f32_e32 v62, v62, v54\n" \
M_"v[232:235], a[168:171], v[120:123], v[232:235]\n" \
"v_rcp_f32_e32 v54, v54\n" \
M_"v[196:199], a[164:167], v[116:119], v[196:199]\n" \
"v_mul_f32_e32 v54, v54, v58\n" \
"v_mul_f32_e32 v128, v54, v128\n" \
M_"v[228:231], a[164:167], v[120:123], v[228:231]\n" \
"v_mul_f32_e32 v129, v54, v129\n" \
"v_mul_f32_e32 v136, v54, v136\n" \
M_"v[192:195], a[160:163], v[116:119], v[192:195]\n" \
"v_mul_f32_e32 v137, v54, v137\n" \
"v_mul_f32_e32 v144, v54, v144\n" \
M_"v[224:227], a[160:163], v[120:123], v[224:227]\n" \
"v_mul_f32_e32 v145, v54, v145\n" \
"v_mul_f32_e32 v152, v54, v152\n" \
"v_smfmac_f32_16x16x64_bf16 a[100:103], a[104:107], v[116:123], v21\n" \
"v_mul_f32_e32 v153, v54, v153\n" \
C_"v128, v128, v129\n" \
"v_mul_f32_e32 v130, v54, v130\n" \
"v_mul_f32_e32 v131, v54, v131\n" \
C_"v136, v136, v137\n" \
"v_mul_f32_e32 v138, v54, v138\n" \
"v_mul_f32_e32 v139, v54, v139\n" \
C_"v144, v144, v145\n" \
"v_mul_f32_e32 v146, v54, v146\n" \
"v_mul_f32_e32 v147, v54, v147\n" \
C_"v152, v152, v153\n" \
"v_mul_f32_e32 v154, v54, v154\n" \
"v_mul_f32_e32 v155, v54, v155\n" \
C_"v129, v130, v131\n" \
"v_mul_f32_e32 v132, v54, v132\n" \
"v_mul_f32_e32 v133, v54, v133\n" \
"v_mul_f32_e32 v134, v54, v134\n" \
"v_mul_f32_e32 v135, v54, v135\n" \
C_"v137, v138, v139\n" \
"v_mul_f32_e32 v140, v54, v140\n" \
"v_mul_f32_e32 v141, v54, v141\n" \
"v_mul_f32_e32 v142, v54, v142\n" \
"v_mul_f32_e32 v143, v54, v143\n" \
C_"v145, v146, v147\n" \
"v_mul_f32_e32 v148, v54, v148\n" \
"v_mul_f32_e32 v149, v54, v149\n" \
"v_mul_f32_e32 v150, v54, v150\n" \
"v_mul_f32_e32 v151, v54, v151\n" \
C_"v153, v154, v155\n" \
"v_mul_f32_e32 v156, v54, v156\n" \
"v_mul_f32_e32 v157, v54, v157\n" \
"v_mul_f32_e32 v158, v54, v158\n" \
"v_mul_f32_e32 v159, v54, v159\n" \
C_"v130, v132, v133\n" \
C_"v131, v134, v135\n" \
"buffer_store_dwordx4 v[128:131], v20, s[56:59], s68 offen offset:0 sc1\n" \
C_"v138, v140, v141\n" \
C_"v139, v142, v143\n" \
C_"v146, v148, v149\n" \
C_"v147, v150, v151\n" \
C_"v154, v156, v157\n" \
C_"v155, v158, v159\n" \
"buffer_store_dwordx4 v[136:139], v20, s[56:59], s68 offen offset:1024 sc1\n" \
"s_add_u32 m0, s93, 0x2000\n" \
"v_add_u32_e32 v49, 0x10000, v48\n" \
"s_xor_b32 s65, s65, 0x8000\n" \
"s_add_u32 s64, s64, 0x10000\n" \
E_"v86, v86\n" \
"buffer_store_dwordx4 v[144:147], v20, s[56:59], s68 offen offset:2048 sc1\n" \
E_"v87, v87\n" \
E_"v84, v84\n" \
E_"v85, v85\n" \
"buffer_store_dwordx4 v[152:155], v20, s[56:59], s68 offen offset:3072 sc1\n" \
E_"v88, v88\n" \
E_"v89, v89\n" \
C_"v87, v86, v87\n" \
E_"v82, v82\n" \
B_"v8, s[80:83], s92 offen offset:0 lds\n" \
E_"v83, v83\n" \
E_"v80, v80\n" \
E_"v81, v81\n" \
E_"v90, v90\n" \
E_"v91, v91\n" \
C_"v86, v84, v85\n" \
C_"v88, v88, v89\n" \
E_"v92, v92\n" \
E_"v93, v93\n" \
E_"v94, v94\n" \
E_"v95, v95\n" \
C_"v85, v82, v83\n" \
C_"v84, v80, v81\n" \
C_"v89, v90, v91\n" \
"v_and_b32_e32 v84, v12, v84\n" \
"v_and_b32_e32 v85, v13, v85\n" \
"v_and_b32_e32 v86, v14, v86\n" \
"s_waitcnt vmcnt(17) lgkmcnt(0)\n" \
"s_barrier\n" \
M_"v[120:123], a[128:131], a[48:51], v[44:47]\n" \
"buffer_load_dwordx2 v[26:27], v16, s[56:59], s96 offen sc1\n" \
M_"v[120:123], a[132:135], a[52:55], v[120:123]\n" \
R_"a[188:191], v49 offset:7168\n" \
R_"a[184:187], v49 offset:6144\n" \
M_"v[120:123], a[136:139], a[56:59], v[120:123]\n" \
"s_xor_b32 m0, s65, 0x12000\n" \
R_"a[180:183], v49 offset:5120\n" \
M_"v[120:123], a[140:143], a[60:63], v[120:123]\n" \
B_"v50, s[8:11], s64 offen offset:0 sc1 lds\n" \
M_"v[124:127], a[156:159], a[60:63], v[44:47]\n" \
R_"a[176:179], v49 offset:4096\n" \
R_"a[172:175], v49 offset:3072\n" \
M_"v[124:127], a[152:155], a[56:59], v[124:127]\n" \
B_"v50, s[8:11], s64 offen offset:1024 sc1 lds\n" \
M_"v[124:127], a[148:151], a[52:55], v[124:127]\n" \
R_"a[168:171], v49 offset:2048\n" \
"v_and_b32_e32 v87, v15, v87\n" \
M_"v[124:127], a[144:147], a[48:51], v[124:127]\n" \
C_"v90, v92, v93\n" \
C_"v91, v94, v95\n" \
M_"v[32:35], a[124:127], a[64:67], 0\n" \
B_"v50, s[8:11], s64 offen offset:2048 sc1 lds\n" \
M_"v[36:39], a[124:127], a[80:83], 0\n" \
R_"a[164:167], v49 offset:1024\n" \
R_"a[160:163], v49 offset:0\n" \
M_"v[32:35], a[224:227], a[68:71], v[32:35]\n" \
R_"a[0:3], v49 offset:8192\n" \
R_"a[4:7], v49 offset:9216\n" \
M_"v[36:39], a[224:227], a[84:87], v[36:39]\n" \
R_"a[8:11], v49 offset:10240\n" \
R_"a[12:15], v49 offset:11264\n" \
M_"v[32:35], a[228:231], a[72:75], v[32:35]\n" \
B_"v50, s[8:11], s64 offen offset:3072 sc1 lds\n" \
M_"v[36:39], a[228:231], a[88:91], v[36:39]\n" \
R_"a[16:19], v49 offset:12288\n" \
R_"a[20:23], v49 offset:13312\n" \
M_"v[32:35], a[232:235], a[76:79], v[32:35]\n" \
R_"a[24:27], v49 offset:14336\n" \
R_"a[28:31], v49 offset:15360\n" \
M_"v[36:39], a[232:235], a[92:95], v[36:39]\n" \
E_"v120, v120\n" \
"v_smfmac_f32_16x16x64_bf16 a[100:103], a[104:107], v[84:91], v21\n" \
"s_mov_b32 s63, s62\n" \
"s_waitcnt lgkmcnt(13)\n" \
M_"v[220:223], a[188:191], v[84:87], v[220:223]\n" \
E_"v121, v121\n" \
M_"v[252:255], a[188:191], v[88:91], v[252:255]\n" \
E_"v122, v122\n" \
M_"v[216:219], a[184:187], v[84:87], v[216:219]\n" \
E_"v123, v123\n" \
M_"v[248:251], a[184:187], v[88:91], v[248:251]\n" \
C_"v120, v120, v121\n" \
C_"v121, v122, v123\n" \
M_"v[212:215], a[180:183], v[84:87], v[212:215]\n" \
E_"v124, v124\n" \
M_"v[244:247], a[180:183], v[88:91], v[244:247]\n" \
"v_and_b32_e32 v120, v12, v120\n" \
"s_waitcnt lgkmcnt(10)\n" \
M_"v[208:211], a[176:179], v[84:87], v[208:211]\n" \
E_"v125, v125\n" \
M_"v[240:243], a[176:179], v[88:91], v[240:243]\n" \
E_"v126, v126\n" \
M_"v[204:207], a[172:175], v[84:87], v[204:207]\n" \
E_"v127, v127\n" \
M_"v[236:239], a[172:175], v[88:91], v[236:239]\n" \
C_"v122, v124, v125\n" \
C_"v123, v126, v127\n" \
M_"v[200:203], a[168:171], v[84:87], v[200:203]\n" \
"v_and_b32_e32 v121, v13, v121\n" \
"v_and_b32_e32 v122, v14, v122\n" \
M_"v[232:235], a[168:171], v[88:91], v[232:235]\n" \
"v_and_b32_e32 v123, v15, v123\n" \
"s_waitcnt lgkmcnt(0)\n" \
M_"v[196:199], a[164:167], v[84:87], v[196:199]\n" \
"v_accvgpr_read_b32 v56, a100\n" \
"v_accvgpr_read_b32 v24, a101\n" \
M_"v[228:231], a[164:167], v[88:91], v[228:231]\n" \
"v_readlane_b32 s97, v11, 39\n" \
"v_add_u32_e32 v17, 0x49000, v20\n" \
M_"v[192:195], a[160:163], v[84:87], v[192:195]\n" \
"v_readlane_b32 s98, v11, 40\n" \
"v_add_u32_e32 v48, s16, v20\n" \
M_"v[224:227], a[160:163], v[88:91], v[224:227]\n" \
R_"a[128:131], v48 offset:0\n" \
R_"a[144:147], v48 offset:4096\n" \
M_"a[100:103], a[116:119], v[120:123], a[100:103]\n" \
R_"a[132:135], v48 offset:1024\n" \
R_"a[148:151], v48 offset:5120\n" \
M_"v[224:227], a[0:3], v[120:123], v[224:227]\n" \
R_"a[136:139], v48 offset:2048\n" \
R_"a[152:155], v48 offset:6144\n" \
M_"v[228:231], a[4:7], v[120:123], v[228:231]\n" \
R_"a[140:143], v48 offset:3072\n" \
R_"a[156:159], v48 offset:7168\n" \
M_"v[232:235], a[8:11], v[120:123], v[232:235]\n" \
"v_accvgpr_read_b32 v55, a98\n" \
"v_accvgpr_read_b32 v23, a99\n" \
M_"a[96:99], a[120:123], a[120:123], 0\n" \
"v_add_f32_e32 v55, v55, v23\n" \
"v_max_f32_e32 v62, v62, v55\n" \
M_"v[236:239], a[12:15], v[120:123], v[236:239]\n" \
"v_rcp_f32_e32 v55, v55\n" \
M_"v[240:243], a[16:19], v[120:123], v[240:243]\n" \
"v_mul_f32_e32 v55, v55, v59\n" \
"v_mul_f32_e32 v160, v55, v160\n" \
M_"v[244:247], a[20:23], v[120:123], v[244:247]\n" \
"v_mul_f32_e32 v161, v55, v161\n" \
"v_mul_f32_e32 v168, v55, v168\n" \
M_"v[248:251], a[24:27], v[120:123], v[248:251]\n" \
"v_mul_f32_e32 v169, v55, v169\n" \
"v_mul_f32_e32 v176, v55, v176\n" \
M_"v[252:255], a[28:31], v[120:123], v[252:255]\n" \
"v_mul_f32_e32 v177, v55, v177\n" \
"v_mul_f32_e32 v184, v55, v184\n" \
"v_mul_f32_e32 v185, v55, v185\n" \
C_"v160, v160, v161\n" \
"v_mul_f32_e32 v162, v55, v162\n" \
"v_mul_f32_e32 v163, v55, v163\n" \
C_"v168, v168, v169\n" \
"v_mul_f32_e32 v170, v55, v170\n" \
"v_mul_f32_e32 v171, v55, v171\n" \
"s_waitcnt vmcnt(4)\n" \
"v_cmp_ne_u64_e32 vcc, s[62:63], v[26:27]\n" \
C_"v176, v176, v177\n" \
"v_mul_f32_e32 v178, v55, v178\n" \
"v_mul_f32_e32 v179, v55, v179\n" \
C_"v184, v184, v185\n" \
"v_mul_f32_e32 v186, v55, v186\n" \
"s_cbranch_vccnz L_sos_%=\n" \
"L_ros_%=:\n" \
B_"a[124:127], v20, s[56:59], s97 offen offset:0 sc1\n" \
"v_mul_f32_e32 v187, v55, v187\n" \
C_"v161, v162, v163\n" \
"v_mul_f32_e32 v164, v55, v164\n" \
"v_mul_f32_e32 v165, v55, v165\n" \
"v_mul_f32_e32 v166, v55, v166\n" \
"v_mul_f32_e32 v167, v55, v167\n" \
B_"a[228:231], v17, s[56:59], s97 offen offset:0 sc1\n" \
C_"v169, v170, v171\n" \
"v_mul_f32_e32 v172, v55, v172\n" \
"v_mul_f32_e32 v173, v55, v173\n" \
"v_mul_f32_e32 v174, v55, v174\n" \
"v_mul_f32_e32 v175, v55, v175\n" \
C_"v177, v178, v179\n" \
B_"a[236:239], v20, s[56:59], s98 offen offset:0 sc1\n" \
"v_mul_f32_e32 v180, v55, v180\n" \
"v_mul_f32_e32 v181, v55, v181\n" \
"v_mul_f32_e32 v182, v55, v182\n" \
"v_mul_f32_e32 v183, v55, v183\n" \
C_"v185, v186, v187\n" \
"v_mul_f32_e32 v188, v55, v188\n" \
B_"a[244:247], v17, s[56:59], s98 offen offset:0 sc1\n" \
"v_mul_f32_e32 v189, v55, v189\n" \
"v_mul_f32_e32 v190, v55, v190\n" \
"v_mul_f32_e32 v191, v55, v191\n" \
C_"v162, v164, v165\n" \
C_"v163, v166, v167\n" \
C_"v170, v172, v173\n" \
"buffer_store_dwordx4 v[160:163], v20, s[56:59], s69 offen offset:0 sc1\n" \
C_"v171, v174, v175\n" \
C_"v178, v180, v181\n" \
C_"v179, v182, v183\n" \
C_"v186, v188, v189\n" \
C_"v187, v190, v191\n" \
"v_readlane_b32 s93, v11, 23\n" \
"buffer_store_dwordx4 v[168:171], v20, s[56:59], s69 offen offset:1024 sc1\n" \
"v_readlane_b32 s84, v11, 15\n" \
"v_readlane_b32 s85, v11, 16\n" \
"v_readlane_b32 s86, v11, 17\n" \
"s_mov_b32 s87, 0x20000\n" \
"v_readlane_b32 s92, v11, 18\n" \
"v_readlane_b32 s91, v11, 24\n" \
"buffer_store_dwordx4 v[176:179], v20, s[56:59], s69 offen offset:2048 sc1\n" \
M_"a[112:115], a[120:123], v[128:131], a[112:115]\n" \
M_"a[112:115], a[120:123], v[136:139], a[112:115]\n" \
"buffer_store_dwordx4 v[184:187], v20, s[56:59], s69 offen offset:3072 sc1\n" \
"v_mfma_f32_32x32x16_bf16 v[128:143], a[120:123], a[120:123], 0\n" \
M_"a[112:115], a[120:123], v[144:147], a[112:115]\n" \
B_"a[224:227], v20, s[56:59], s97 offen offset:1024 sc1\n" \
M_"a[112:115], a[120:123], v[152:155], a[112:115]\n" \
"v_mfma_f32_32x32x16_bf16 v[144:159], a[120:123], a[120:123], 0\n" \
B_"a[232:235], v17, s[56:59], s97 offen offset:1024 sc1\n" \
"v_add_f32_e32 v56, v56, v24\n" \
M_"a[112:115], a[120:123], v[160:163], a[112:115]\n" \
B_"a[240:243], v20, s[56:59], s98 offen offset:1024 sc1\n" \
"v_max_f32_e32 v62, v62, v56\n" \
"v_rcp_f32_e32 v56, v56\n" \
M_"a[112:115], a[120:123], v[168:171], a[112:115]\n" \
"v_mfma_f32_32x32x16_bf16 v[160:175], a[120:123], a[120:123], 0\n" \
B_"a[248:251], v17, s[56:59], s98 offen offset:1024 sc1\n" \
"v_mul_f32_e32 v56, v56, v60\n" \
M_"a[112:115], a[120:123], v[176:179], a[112:115]\n" \
"v_mul_f32_e32 v192, v56, v192\n" \
"v_mul_f32_e32 v193, v56, v193\n" \
"s_waitcnt vmcnt(17) lgkmcnt(0)\n" \
"s_barrier\n" \
M_"v[64:67], a[128:131], a[64:67], v[32:35]\n" \
"s_add_u32 m0, s93, 0x0\n" \
C_"v192, v192, v193\n" \
M_"v[64:67], a[132:135], a[68:71], v[64:67]\n" \
B_"v0, s[84:87], s92 offen offset:0 lds\n" \
M_"v[64:67], a[136:139], a[72:75], v[64:67]\n" \
"v_mul_f32_e32 v194, v56, v194\n" \
"v_mul_f32_e32 v195, v56, v195\n" \
M_"v[64:67], a[140:143], a[76:79], v[64:67]\n" \
B_"v1, s[84:87], s92 offen offset:1024 lds\n" \
M_"v[68:71], a[156:159], a[76:79], v[32:35]\n" \
M_"a[112:115], a[120:123], v[184:187], a[112:115]\n" \
"v_mfma_f32_32x32x16_bf16 v[176:191], a[120:123], a[120:123], 0\n" \
B_"v2, s[84:87], s92 offen offset:2048 lds\n" \
C_"v193, v194, v195\n" \
"v_mul_f32_e32 v196, v56, v196\n" \
"v_mul_f32_e32 v197, v56, v197\n" \
"v_mul_f32_e32 v198, v56, v198\n" \
M_"v[68:71], a[152:155], a[72:75], v[68:71]\n" \
B_"v3, s[84:87], s92 offen offset:3072 lds\n" \
M_"v[68:71], a[148:151], a[68:71], v[68:71]\n" \
"s_add_u32 m0, s91, 0x1000\n" \
"v_mul_f32_e32 v199, v56, v199\n" \
M_"v[68:71], a[144:147], a[64:67], v[68:71]\n" \
B_"v4, s[84:87], s92 offen offset:0 lds\n" \
M_"v[76:79], a[144:147], a[80:83], v[36:39]\n" \
R_"a[144:147], v48 offset:12288\n" \
"v_mul_f32_e32 v200, v56, v200\n" \
M_"v[76:79], a[148:151], a[84:87], v[76:79]\n" \
B_"v5, s[84:87], s92 offen offset:1024 lds\n" \
M_"v[76:79], a[152:155], a[88:91], v[76:79]\n" \
R_"a[148:151], v48 offset:13312\n" \
R_"a[152:155], v48 offset:14336\n" \
M_"v[76:79], a[156:159], a[92:95], v[76:79]\n" \
B_"v6, s[84:87], s92 offen offset:2048 lds\n" \
M_"v[72:75], a[140:143], a[92:95], v[36:39]\n" \
R_"a[140:143], v48 offset:11264\n" \
R_"a[156:159], v48 offset:15360\n" \
M_"v[72:75], a[136:139], a[88:91], v[72:75]\n" \
B_"v7, s[84:87], s92 offen offset:3072 lds\n" \
M_"v[72:75], a[132:135], a[84:87], v[72:75]\n" \
R_"a[132:135], v48 offset:9216\n" \
R_"a[136:139], v48 offset:10240\n" \
M_"v[72:75], a[128:131], a[80:83], v[72:75]\n" \
R_"a[128:131], v48 offset:8192\n" \
"v_mul_f32_e32 v201, v56, v201\n" \
"v_accvgpr_read_b32 v57, a102\n" \
"v_accvgpr_read_b32 v25, a103\n" \
M_"a[100:103], a[120:123], a[120:123], 0\n" \
C_"v194, v196, v197\n" \
C_"v195, v198, v199\n" \
"buffer_store_dwordx4 v[192:195], v20, s[56:59], s70 offen offset:0 sc1\n" \
C_"v200, v200, v201\n" \
"v_mul_f32_e32 v202, v56, v202\n" \
"v_mul_f32_e32 v203, v56, v203\n" \
"v_add_f32_e32 v57, v57, v25\n" \
M_"a[112:115], a[120:123], v[192:195], a[112:115]\n" \
C_"v201, v202, v203\n" \
"v_mul_f32_e32 v204, v56, v204\n" \
"v_mul_f32_e32 v205, v56, v205\n" \
"v_mul_f32_e32 v206, v56, v206\n" \
"v_mul_f32_e32 v207, v56, v207\n" \
"v_mul_f32_e32 v208, v56, v208\n" \
"v_mul_f32_e32 v209, v56, v209\n" \
"v_max_f32_e32 v62, v62, v57\n" \
C_"v202, v204, v205\n" \
C_"v203, v206, v207\n" \
"buffer_store_dwordx4 v[200:203], v20, s[56:59], s70 offen offset:1024 sc1\n" \
".fill (L_ab + 28672 - .) / 4, 4, 0xBF800000\n" \
"L_ap7:\n" \
"s_cbranch_execz L_ap0\n" \
C_"v208, v208, v209\n" \
"v_mul_f32_e32 v210, v56, v210\n" \
"v_mul_f32_e32 v211, v56, v211\n" \
"v_rcp_f32_e32 v57, v57\n" \
M_"a[112:115], a[120:123], v[200:203], a[112:115]\n" \
"v_mfma_f32_32x32x16_bf16 v[192:207], a[120:123], a[120:123], 0\n" \
C_"v209, v210, v211\n" \
"v_mul_f32_e32 v212, v56, v212\n" \
"v_mul_f32_e32 v213, v56, v213\n" \
"v_mul_f32_e32 v214, v56, v214\n" \
"v_mul_f32_e32 v215, v56, v215\n" \
"v_mul_f32_e32 v216, v56, v216\n" \
"v_mul_f32_e32 v217, v56, v217\n" \
C_"v210, v212, v213\n" \
C_"v211, v214, v215\n" \
"buffer_store_dwordx4 v[208:211], v20, s[56:59], s70 offen offset:2048 sc1\n" \
C_"v216, v216, v217\n" \
"v_mul_f32_e32 v218, v56, v218\n" \
"v_mul_f32_e32 v219, v56, v219\n" \
"v_mul_f32_e32 v57, v57, v61\n" \
M_"a[112:115], a[120:123], v[208:211], a[112:115]\n" \
C_"v217, v218, v219\n" \
"v_mul_f32_e32 v220, v56, v220\n" \
"v_mul_f32_e32 v221, v56, v221\n" \
"v_mul_f32_e32 v222, v56, v222\n" \
"v_mul_f32_e32 v223, v56, v223\n" \
"v_mul_f32_e32 v224, v57, v224\n" \
"v_mul_f32_e32 v225, v57, v225\n" \
C_"v218, v220, v221\n" \
C_"v219, v222, v223\n" \
"buffer_store_dwordx4 v[216:219], v20, s[56:59], s70 offen offset:3072 sc1\n" \
C_"v224, v224, v225\n" \
"v_mul_f32_e32 v226, v57, v226\n" \
"v_mul_f32_e32 v227, v57, v227\n" \
M_"a[112:115], a[120:123], v[216:219], a[112:115]\n" \
"v_mfma_f32_32x32x16_bf16 v[208:223], a[120:123], a[120:123], 0\n" \
C_"v225, v226, v227\n" \
"v_mul_f32_e32 v228, v57, v228\n" \
"v_mul_f32_e32 v229, v57, v229\n" \
"v_mul_f32_e32 v230, v57, v230\n" \
"v_mul_f32_e32 v231, v57, v231\n" \
"v_mul_f32_e32 v232, v57, v232\n" \
"v_mul_f32_e32 v233, v57, v233\n" \
C_"v226, v228, v229\n" \
C_"v227, v230, v231\n" \
"buffer_store_dwordx4 v[224:227], v20, s[56:59], s71 offen offset:0 sc1\n" \
C_"v232, v232, v233\n" \
"v_mul_f32_e32 v234, v57, v234\n" \
"v_mul_f32_e32 v235, v57, v235\n" \
M_"a[112:115], a[120:123], v[224:227], a[112:115]\n" \
C_"v233, v234, v235\n" \
"v_mul_f32_e32 v236, v57, v236\n" \
"v_mul_f32_e32 v237, v57, v237\n" \
"v_mul_f32_e32 v238, v57, v238\n" \
"v_mul_f32_e32 v239, v57, v239\n" \
"v_mul_f32_e32 v240, v57, v240\n" \
"v_mul_f32_e32 v241, v57, v241\n" \
C_"v234, v236, v237\n" \
C_"v235, v238, v239\n" \
"buffer_store_dwordx4 v[232:235], v20, s[56:59], s71 offen offset:1024 sc1\n" \
C_"v240, v240, v241\n" \
"v_mul_f32_e32 v242, v57, v242\n" \
"v_mul_f32_e32 v243, v57, v243\n" \
M_"a[112:115], a[120:123], v[232:235], a[112:115]\n" \
"v_mfma_f32_32x32x16_bf16 v[224:239], a[120:123], a[120:123], 0\n" \
C_"v241, v242, v243\n" \
"v_mul_f32_e32 v244, v57, v244\n" \
"v_mul_f32_e32 v245, v57, v245\n" \
"v_mul_f32_e32 v246, v57, v246\n" \
"v_mul_f32_e32 v247, v57, v247\n" \
"v_mul_f32_e32 v248, v57, v248\n" \
"v_mul_f32_e32 v249, v57, v249\n" \
C_"v242, v244, v245\n" \
C_"v243, v246, v247\n" \
"buffer_store_dwordx4 v[240:243], v20, s[56:59], s71 offen offset:2048 sc1\n" \
C_"v248, v248, v249\n" \
"v_mul_f32_e32 v250, v57, v250\n" \
"v_mul_f32_e32 v251, v57, v251\n" \
M_"a[112:115], a[120:123], v[240:243], a[112:115]\n" \
C_"v249, v250, v251\n" \
"v_mul_f32_e32 v252, v57, v252\n" \
"v_mul_f32_e32 v253, v57, v253\n" \
"v_mul_f32_e32 v254, v57, v254\n" \
"v_mul_f32_e32 v255, v57, v255\n" \
C_"v250, v252, v253\n" \
C_"v251, v254, v255\n" \
"buffer_store_dwordx4 v[248:251], v20, s[56:59], s71 offen offset:3072 sc1\n" \
"s_add_u32 m0, s91, 0x2000\n" \
M_"a[112:115], a[120:123], v[248:251], a[112:115]\n" \
"v_mfma_f32_32x32x16_bf16 v[240:255], a[120:123], a[120:123], 0\n" \
"v_add_u32_e32 v53, 0x20000, v63\n" \
B_"v8, s[84:87], s92 offen offset:0 lds\n" \
E_"v70, v70\n" \
E_"v71, v71\n" \
E_"v72, v72\n" \
E_"v73, v73\n" \
"s_add_u32 s64, s64, 0x10000\n" \
"v_accvgpr_read_b32 v63, a112\n" \
"v_cmp_u_f32_e32 vcc, v63, v63\n" \
"v_cmp_lt_f32_e64 s[72:73], s21, v62\n" \
"v_readlane_b32 s95, v11, 3\n" \
E_"v68, v68\n" \
E_"v69, v69\n" \
E_"v74, v74\n" \
E_"v75, v75\n" \
"s_or_b64 vcc, s[72:73], vcc\n" \
"s_cselect_b32 s95, 0x40000000, s95\n" \
"s_cselect_b32 s94, 0x7f800000, 0\n" \
C_"v71, v70, v71\n" \
E_"v66, v66\n" \
E_"v67, v67\n" \
E_"v64, v64\n" \
E_"v65, v65\n" \
C_"v72, v72, v73\n" \
E_"v76, v76\n" \
E_"v77, v77\n" \
E_"v78, v78\n" \
E_"v79, v79\n" \
C_"v70, v68, v69\n" \
"s_waitcnt vmcnt(17) lgkmcnt(0)\n" \
"s_barrier\n" \
C_"v73, v74, v75\n" \
"v_add_u32_e32 v49, 0x10000, v48\n" \
"s_xor_b32 s65, s65, 0x8000\n" \
C_"v69, v66, v67\n" \
C_"v68, v64, v65\n" \
C_"v74, v76, v77\n" \
C_"v75, v78, v79\n" \
"v_xor_b32_e32 v48, 0x8000, v48\n" \
"s_sub_u32 s66, s19, 2\n" \
"s_branch L_f1_%=\n" \
".fill (L_ab + 32768 - .) / 4, 4, 0xBF800000\n" \
".fill (L_ab + 32768 - .) / 4, 4, 0xBF800000\n" \
"L_ap8:\n" \
"s_setpc_b64 s[88:89]\n" \
"L_out_%=:\n" \
"v_bfe_u32 v38, v20, 2, 4\n" \
"v_readlane_b32 s64, v11, 2\n" \
"v_add_u32_e32 v32, 0x49000, v20\n" \
"v_add_u32_e32 v34, 0x20000, v10\n" \
X_"a[0:1], v34 offset:0\n" \
X_"a[2:3], v34 offset:4608\n" \
X_"a[4:5], v34 offset:9216\n" \
"buffer_load_dword v26, v38, s[56:59], s64 offen sc1\n" \
X_"a[6:7], v34 offset:13824\n" \
X_"a[8:9], v34 offset:32\n" \
X_"a[10:11], v34 offset:4640\n" \
X_"a[12:13], v34 offset:9248\n" \
"buffer_store_dwordx4 v[64:67], v20, s[56:59], s61 offen offset:0 sc1\n" \
X_"a[14:15], v34 offset:13856\n" \
"v_mov_b32_e32 v39, s62\n" \
"v_readlane_b32 s70, v11, 5\n" \
"v_readlane_b32 s71, v11, 4\n" \
"buffer_store_dwordx4 v[68:71], v20, s[56:59], s61 offen offset:1024 sc1\n" \
X_"a[16:17], v34 offset:64\n" \
X_"a[18:19], v34 offset:4672\n" \
X_"a[20:21], v34 offset:9280\n" \
X_"a[22:23], v34 offset:13888\n" \
"buffer_store_dwordx4 v[72:75], v20, s[56:59], s61 offen offset:2048 sc1\n" \
X_"a[24:25], v34 offset:96\n" \
X_"a[26:27], v34 offset:4704\n" \
X_"a[28:29], v34 offset:9312\n" \
X_"a[30:31], v34 offset:13920\n" \
"buffer_store_dwordx4 v[76:79], v20, s[56:59], s61 offen offset:3072 sc1\n" \
X_"a[96:97], v34 offset:128\n" \
X_"a[98:99], v34 offset:4736\n" \
X_"a[100:101], v34 offset:9344\n" \
X_"a[102:103], v34 offset:13952\n" \
"buffer_store_dwordx4 v[80:83], v32, s[56:59], s61 offen offset:0 sc1\n" \
X_"a[104:105], v34 offset:160\n" \
X_"a[106:107], v34 offset:4768\n" \
X_"a[108:109], v34 offset:9376\n" \
X_"a[110:111], v34 offset:13984\n" \
"buffer_store_dwordx4 v[84:87], v32, s[56:59], s61 offen offset:1024 sc1\n" \
X_"a[112:113], v34 offset:192\n" \
"s_waitcnt vmcnt(16) lgkmcnt(15)\n" \
M_"v[112:115], a[0:3], a[192:195], 0\n" \
"buffer_store_dwordx4 v[96:99], v32, s[56:59], s61 offen offset:2048 sc1\n" \
M_"v[112:115], a[4:7], a[196:199], v[112:115]\n" \
X_"a[114:115], v34 offset:4800\n" \
X_"a[116:117], v34 offset:9408\n" \
"buffer_store_dwordx4 v[100:103], v32, s[56:59], s61 offen offset:3072 sc1\n" \
M_"v[116:119], a[0:3], a[208:211], 0\n" \
X_"a[118:119], v34 offset:14016\n" \
X_"a[120:121], v34 offset:224\n" \
M_"v[116:119], a[4:7], a[212:215], v[116:119]\n" \
X_"a[122:123], v34 offset:4832\n" \
"v_lshrrev_b32_e32 v44, 9, v20\n" \
M_"v[184:187], a[0:3], v[64:67], 0\n" \
"v_mul_u32_u24_e32 v44, 0xb40, v44\n" \
"v_add_u32_e32 v35, v44, v10\n" \
M_"v[184:187], a[4:7], v[68:71], v[184:187]\n" \
"v_readlane_b32 s65, v11, 33\n" \
"v_readlane_b32 s67, v11, 26\n" \
M_"v[188:191], a[0:3], v[80:83], 0\n" \
X_"a[0:1], v34 offset:9440\n" \
X_"a[2:3], v34 offset:14048\n" \
M_"v[188:191], a[4:7], v[84:87], v[188:191]\n" \
X_"a[4:5], v34 offset:256\n" \
X_"a[6:7], v34 offset:4864\n" \
M_"v[196:199], a[8:11], v[80:83], 0\n" \
"v_readlane_b32 s80, v11, 11\n" \
"v_readlane_b32 s81, v11, 12\n" \
M_"v[196:199], a[12:15], v[84:87], v[196:199]\n" \
"s_mov_b32 s82, 0x47fca0\n" \
"s_mov_b32 s83, 0x20000\n" \
M_"v[192:195], a[8:11], v[64:67], 0\n" \
"v_readlane_b32 s66, v11, 31\n" \
"s_add_u32 m0, s67, 0x0\n" \
M_"v[192:195], a[12:15], v[68:71], v[192:195]\n" \
"v_readlane_b32 s92, v11, 27\n" \
"v_readlane_b32 s73, v11, 32\n" \
M_"v[124:127], a[8:11], a[208:211], 0\n" \
B_"v0, s[80:83], s66 offen offset:0 lds\n" \
M_"v[124:127], a[12:15], a[212:215], v[124:127]\n" \
"v_readlane_b32 s94, v11, 28\n" \
"v_readlane_b32 s84, v11, 9\n" \
M_"v[120:123], a[8:11], a[192:195], 0\n" \
X_"a[8:9], v34 offset:9472\n" \
X_"a[10:11], v34 offset:14080\n" \
M_"v[120:123], a[12:15], a[196:199], v[120:123]\n" \
X_"a[12:13], v34 offset:18432\n" \
X_"a[14:15], v34 offset:23040\n" \
M_"v[128:131], a[16:19], a[192:195], 0\n" \
"v_readlane_b32 s85, v11, 10\n" \
"s_waitcnt lgkmcnt(9)\n" \
M_"v[128:131], a[20:23], a[196:199], v[128:131]\n" \
B_"v1, s[80:83], s66 offen offset:1024 lds\n" \
M_"v[132:135], a[16:19], a[208:211], 0\n" \
"s_mov_b32 s86, 0xa1ca0\n" \
"s_mov_b32 s87, 0x20000\n" \
M_"v[132:135], a[20:23], a[212:215], v[132:135]\n" \
"v_add_u32_e32 v33, 0x10000, v10\n" \
"v_readlane_b32 s68, v11, 34\n" \
M_"v[200:203], a[16:19], v[64:67], 0\n" \
"v_readlane_b32 s69, v11, 35\n" \
"v_readlane_b32 s72, v11, 37\n" \
M_"v[200:203], a[20:23], v[68:71], v[200:203]\n" \
"v_readlane_b32 s93, v11, 38\n" \
"v_readlane_b32 s63, v11, 44\n" \
M_"v[204:207], a[16:19], v[80:83], 0\n" \
X_"a[16:17], v34 offset:18464\n" \
X_"a[18:19], v34 offset:23072\n" \
M_"v[204:207], a[20:23], v[84:87], v[204:207]\n" \
X_"a[20:21], v34 offset:18496\n" \
X_"a[22:23], v34 offset:23104\n" \
M_"v[212:215], a[24:27], v[80:83], 0\n" \
B_"v2, s[80:83], s66 offen offset:2048 lds\n" \
M_"v[212:215], a[28:31], v[84:87], v[212:215]\n" \
"v_bfe_u32 v40, v20, 2, 7\n" \
"s_add_u32 s74, s63, 0x3800\n" \
M_"v[208:211], a[24:27], v[64:67], 0\n" \
"v_lshlrev_b32_e32 v41, 12, v20\n" \
"s_add_u32 s12, s63, 0x0\n" \
M_"v[208:211], a[28:31], v[68:71], v[208:211]\n" \
"v_readlane_b32 s95, v11, 41\n" \
"v_readlane_b32 s96, v11, 42\n" \
M_"v[140:143], a[24:27], a[208:211], 0\n" \
"s_add_u32 s13, s63, 0x1000\n" \
"v_readlane_b32 s97, v11, 43\n" \
M_"v[140:143], a[28:31], a[212:215], v[140:143]\n" \
B_"v3, s[80:83], s66 offen offset:3072 lds\n" \
M_"v[136:139], a[24:27], a[192:195], 0\n" \
X_"a[24:25], v34 offset:18528\n" \
X_"a[26:27], v34 offset:23136\n" \
M_"v[136:139], a[28:31], a[196:199], v[136:139]\n" \
X_"a[28:29], v34 offset:18560\n" \
X_"a[30:31], v34 offset:23168\n" \
M_"v[144:147], a[96:99], a[192:195], 0\n" \
"s_add_u32 m0, s67, 0x1000\n" \
"s_add_u32 s14, s63, 0x2000\n" \
M_"v[144:147], a[100:103], a[196:199], v[144:147]\n" \
"s_add_u32 s15, s63, 0x3000\n" \
"v_readlane_b32 s100, v11, 7\n" \
M_"v[148:151], a[96:99], a[208:211], 0\n" \
B_"v4, s[80:83], s66 offen offset:0 lds\n" \
M_"v[148:151], a[100:103], a[212:215], v[148:151]\n" \
"v_readlane_b32 s16, v11, 45\n" \
"v_bfe_u32 v44, v20, 4, 4\n" \
M_"v[216:219], a[96:99], v[64:67], 0\n" \
"s_add_u32 s17, s16, 0x1000\n" \
"s_add_u32 s18, s16, 0x2000\n" \
M_"v[216:219], a[100:103], v[68:71], v[216:219]\n" \
"s_add_u32 s19, s16, 0x3000\n" \
"v_bfe_u32 v45, v20, 8, 2\n" \
M_"v[220:223], a[96:99], v[80:83], 0\n" \
X_"a[96:97], v34 offset:18592\n" \
X_"a[98:99], v34 offset:23200\n" \
M_"v[220:223], a[100:103], v[84:87], v[220:223]\n" \
X_"a[100:101], v34 offset:18624\n" \
X_"a[102:103], v34 offset:23232\n" \
M_"v[228:231], a[104:107], v[80:83], 0\n" \
B_"v5, s[80:83], s66 offen offset:1024 lds\n" \
M_"v[228:231], a[108:111], v[84:87], v[228:231]\n" \
"v_mul_u32_u24_e32 v42, 0x480, v44\n" \
"v_lshl_add_u32 v42, v45, 3, v42\n" \
M_"v[224:227], a[104:107], v[64:67], 0\n" \
"s_waitcnt vmcnt(14)\n" \
"v_cmp_ne_u32_e32 vcc, s62, v26\n" \
M_"v[224:227], a[108:111], v[68:71], v[224:227]\n" \
"v_lshrrev_b32_e32 v44, 1, v44\n" \
"v_lshlrev_b32_e32 v45, 1, v45\n" \
M_"v[156:159], a[104:107], a[208:211], 0\n" \
"v_sub_u32_e32 v44, v44, v45\n" \
"s_cbranch_vccnz L_sa_%=\n" \
"L_ra_%=:\n" \
M_"v[156:159], a[108:111], a[212:215], v[156:159]\n" \
B_"a[64:67], v20, s[56:59], s65 offen offset:0 sc1\n" \
M_"v[152:155], a[104:107], a[192:195], 0\n" \
X_"a[104:105], v34 offset:18656\n" \
X_"a[106:107], v34 offset:23264\n" \
M_"v[152:155], a[108:111], a[196:199], v[152:155]\n" \
X_"a[108:109], v34 offset:18688\n" \
X_"a[110:111], v34 offset:23296\n" \
M_"v[160:163], a[112:115], a[192:195], 0\n" \
"v_readlane_b32 s64, v11, 36\n" \
"v_and_b32_e32 v45, 16, v20\n" \
M_"v[160:163], a[116:119], a[196:199], v[160:163]\n" \
"v_mov_b32_e32 v46, 0x3f80\n" \
"v_lshlrev_b32_e32 v45, v45, v46\n" \
M_"v[164:167], a[112:115], a[208:211], 0\n" \
B_"a[68:71], v20, s[56:59], s65 offen offset:1024 sc1\n" \
M_"v[164:167], a[116:119], a[212:215], v[164:167]\n" \
"v_mov_b32_e32 v52, 0\n" \
"v_mov_b32_e32 v53, 0\n" \
M_"v[232:235], a[112:115], v[64:67], 0\n" \
"v_mov_b32_e32 v48, 0\n" \
"v_mov_b32_e32 v49, 0\n" \
M_"v[232:235], a[116:119], v[68:71], v[232:235]\n" \
"v_add_u32_e32 v43, 0x9000, v42\n" \
"v_readlane_b32 s88, v11, 13\n" \
M_"v[236:239], a[112:115], v[80:83], 0\n" \
X_"a[112:113], v10 offset:32768\n" \
X_"a[114:115], v10 offset:37376\n" \
M_"v[236:239], a[116:119], v[84:87], v[236:239]\n" \
X_"a[116:117], v10 offset:32800\n" \
"s_waitcnt lgkmcnt(7)\n" \
M_"v[244:247], a[120:123], v[80:83], 0\n" \
X_"a[118:119], v10 offset:37408\n" \
"v_readlane_b32 s89, v11, 14\n" \
M_"v[244:247], a[0:3], v[84:87], v[244:247]\n" \
B_"a[80:83], v32, s[56:59], s65 offen offset:0 sc1\n" \
M_"v[240:243], a[120:123], v[64:67], 0\n" \
"s_mov_b32 s90, 0x240000\n" \
"s_mov_b32 s91, 0x20000\n" \
M_"v[240:243], a[0:3], v[68:71], v[240:243]\n" \
"v_mov_b32_e32 v46, s62\n" \
M_"v[172:175], a[120:123], a[208:211], 0\n" \
M_"v[172:175], a[0:3], a[212:215], v[172:175]\n" \
M_"v[168:171], a[120:123], a[192:195], 0\n" \
X_"a[120:121], v10 offset:32832\n" \
X_"a[122:123], v10 offset:37440\n" \
M_"v[168:171], a[0:3], a[196:199], v[168:171]\n" \
X_"a[0:1], v10 offset:32864\n" \
X_"a[2:3], v10 offset:37472\n" \
M_"v[176:179], a[4:7], a[192:195], 0\n" \
B_"a[84:87], v32, s[56:59], s65 offen offset:1024 sc1\n" \
M_"v[176:179], a[8:11], a[196:199], v[176:179]\n" \
M_"v[180:183], a[4:7], a[208:211], 0\n" \
M_"v[180:183], a[8:11], a[212:215], v[180:183]\n" \
M_"v[248:251], a[4:7], v[64:67], 0\n" \
M_"v[248:251], a[8:11], v[68:71], v[248:251]\n" \
B_"a[72:75], v20, s[56:59], s65 offen offset:2048 sc1\n" \
M_"v[252:255], a[4:7], v[80:83], 0\n" \
X_"a[4:5], v10 offset:32896\n" \
X_"a[6:7], v10 offset:37504\n" \
M_"v[252:255], a[8:11], v[84:87], v[252:255]\n" \
X_"a[8:9], v10 offset:32928\n" \
X_"a[10:11], v10 offset:37536\n" \
M_"v[112:115], a[12:15], a[200:203], v[112:115]\n" \
M_"v[116:119], a[12:15], a[216:219], v[116:119]\n" \
M_"v[184:187], a[12:15], v[72:75], v[184:187]\n" \
B_"a[76:79], v20, s[56:59], s65 offen offset:3072 sc1\n" \
M_"v[188:191], a[12:15], v[96:99], v[188:191]\n" \
X_"a[12:13], v10 offset:32960\n" \
X_"a[14:15], v10 offset:37568\n" \
M_"v[196:199], a[16:19], v[96:99], v[196:199]\n" \
M_"v[192:195], a[16:19], v[72:75], v[192:195]\n" \
M_"v[124:127], a[16:19], a[216:219], v[124:127]\n" \
M_"v[120:123], a[16:19], a[200:203], v[120:123]\n" \
X_"a[16:17], v10 offset:32992\n" \
X_"a[18:19], v10 offset:37600\n" \
M_"v[128:131], a[20:23], a[200:203], v[128:131]\n" \
B_"a[88:91], v32, s[56:59], s65 offen offset:2048 sc1\n" \
M_"v[132:135], a[20:23], a[216:219], v[132:135]\n" \
M_"v[200:203], a[20:23], v[72:75], v[200:203]\n" \
M_"v[204:207], a[20:23], v[96:99], v[204:207]\n" \
X_"a[20:21], v10 offset:33024\n" \
X_"a[22:23], v10 offset:37632\n" \
"s_waitcnt lgkmcnt(10)\n" \
"s_barrier\n" \
M_"v[212:215], a[24:27], v[96:99], v[212:215]\n" \
B_"a[92:95], v32, s[56:59], s65 offen offset:3072 sc1\n" \
M_"v[208:211], a[24:27], v[72:75], v[208:211]\n" \
"v_readlane_b32 s65, v11, 49\n" \
M_"v[140:143], a[24:27], a[216:219], v[140:143]\n" \
M_"v[136:139], a[24:27], a[200:203], v[136:139]\n" \
X_"a[24:25], v35 offset:41984\n" \
X_"a[26:27], v35 offset:44288\n" \
M_"v[144:147], a[28:31], a[200:203], v[144:147]\n" \
M_"v[148:151], a[28:31], a[216:219], v[148:151]\n" \
B_"v6, s[80:83], s66 offen offset:2048 lds\n" \
M_"v[216:219], a[28:31], v[72:75], v[216:219]\n" \
M_"v[220:223], a[28:31], v[96:99], v[220:223]\n" \
X_"a[28:29], v35 offset:52352\n" \
X_"a[30:31], v35 offset:54656\n" \
M_"v[228:231], a[96:99], v[96:99], v[228:231]\n" \
M_"v[224:227], a[96:99], v[72:75], v[224:227]\n" \
M_"v[156:159], a[96:99], a[216:219], v[156:159]\n" \
B_"v7, s[80:83], s66 offen offset:3072 lds\n" \
M_"v[152:155], a[96:99], a[200:203], v[152:155]\n" \
X_"a[96:97], v35 offset:42016\n" \
X_"a[98:99], v35 offset:44320\n" \
M_"v[160:163], a[100:103], a[200:203], v[160:163]\n" \
"s_add_u32 m0, s67, 0x2000\n" \
"v_readlane_b32 s67, v11, 51\n" \
M_"v[164:167], a[100:103], a[216:219], v[164:167]\n" \
M_"v[232:235], a[100:103], v[72:75], v[232:235]\n" \
M_"v[236:239], a[100:103], v[96:99], v[236:239]\n" \
X_"a[100:101], v35 offset:52384\n" \
X_"a[102:103], v35 offset:54688\n" \
M_"v[244:247], a[104:107], v[96:99], v[244:247]\n" \
B_"v8, s[80:83], s66 offen offset:0 lds\n" \
M_"v[240:243], a[104:107], v[72:75], v[240:243]\n" \
"s_add_u32 m0, s92, 0x0\n" \
"v_readlane_b32 s66, v11, 50\n" \
M_"v[172:175], a[104:107], a[216:219], v[172:175]\n" \
M_"v[168:171], a[104:107], a[200:203], v[168:171]\n" \
X_"a[104:105], v35 offset:42048\n" \
X_"a[106:107], v35 offset:44352\n" \
M_"v[176:179], a[108:111], a[200:203], v[176:179]\n" \
M_"v[180:183], a[108:111], a[216:219], v[180:183]\n" \
B_"v0, s[80:83], s73 offen offset:0 lds\n" \
M_"v[248:251], a[108:111], v[72:75], v[248:251]\n" \
M_"v[252:255], a[108:111], v[96:99], v[252:255]\n" \
X_"a[108:109], v35 offset:52416\n" \
X_"a[110:111], v35 offset:54720\n" \
M_"v[112:115], a[112:115], a[204:207], v[112:115]\n" \
M_"v[116:119], a[112:115], a[220:223], v[116:119]\n" \
M_"v[184:187], a[112:115], v[76:79], v[184:187]\n" \
B_"v1, s[80:83], s73 offen offset:1024 lds\n" \
M_"v[188:191], a[112:115], v[100:103], v[188:191]\n" \
X_"a[112:113], v35 offset:42080\n" \
X_"a[114:115], v35 offset:44384\n" \
M_"v[196:199], a[116:119], v[100:103], v[196:199]\n" \
M_"v[192:195], a[116:119], v[76:79], v[192:195]\n" \
"s_waitcnt vmcnt(18)\n" \
"s_mov_b64 exec, 1\n" \
"buffer_store_dword v39, off, s[56:59], s70 offset:0 sc1\n" \
"s_mov_b64 exec, -1\n" \
M_"v[124:127], a[116:119], a[220:223], v[124:127]\n" \
"v_readlane_b32 s70, v11, 54\n" \
M_"v[120:123], a[116:119], a[204:207], v[120:123]\n" \
X_"a[116:117], v35 offset:52448\n" \
X_"a[118:119], v35 offset:54752\n" \
M_"v[128:131], a[120:123], a[204:207], v[128:131]\n" \
M_"v[132:135], a[120:123], a[220:223], v[132:135]\n" \
B_"v2, s[80:83], s73 offen offset:2048 lds\n" \
M_"v[200:203], a[120:123], v[76:79], v[200:203]\n" \
M_"v[204:207], a[120:123], v[100:103], v[204:207]\n" \
X_"a[120:121], v35 offset:42112\n" \
X_"a[122:123], v35 offset:44416\n" \
M_"v[212:215], a[0:3], v[100:103], v[212:215]\n" \
M_"v[208:211], a[0:3], v[76:79], v[208:211]\n" \
M_"v[140:143], a[0:3], a[220:223], v[140:143]\n" \
B_"v3, s[80:83], s73 offen offset:3072 lds\n" \
M_"v[136:139], a[0:3], a[204:207], v[136:139]\n" \
X_"a[0:1], v35 offset:52480\n" \
"s_waitcnt lgkmcnt(7)\n" \
M_"v[144:147], a[4:7], a[204:207], v[144:147]\n" \
X_"a[2:3], v35 offset:54784\n" \
"s_add_u32 m0, s92, 0x1000\n" \
M_"v[148:151], a[4:7], a[220:223], v[148:151]\n" \
M_"v[216:219], a[4:7], v[76:79], v[216:219]\n" \
M_"v[220:223], a[4:7], v[100:103], v[220:223]\n" \
X_"a[4:5], v35 offset:42144\n" \
X_"a[6:7], v35 offset:44448\n" \
M_"v[228:231], a[8:11], v[100:103], v[228:231]\n" \
B_"v4, s[80:83], s73 offen offset:0 lds\n" \
M_"v[224:227], a[8:11], v[76:79], v[224:227]\n" \
M_"v[156:159], a[8:11], a[220:223], v[156:159]\n" \
M_"v[152:155], a[8:11], a[204:207], v[152:155]\n" \
X_"a[8:9], v35 offset:52512\n" \
X_"a[10:11], v35 offset:54816\n" \
M_"v[160:163], a[12:15], a[204:207], v[160:163]\n" \
M_"v[164:167], a[12:15], a[220:223], v[164:167]\n" \
B_"v5, s[80:83], s73 offen offset:1024 lds\n" \
M_"v[232:235], a[12:15], v[76:79], v[232:235]\n" \
M_"v[236:239], a[12:15], v[100:103], v[236:239]\n" \
X_"a[12:13], v35 offset:42176\n" \
X_"a[14:15], v35 offset:44480\n" \
M_"v[244:247], a[16:19], v[100:103], v[244:247]\n" \
M_"v[240:243], a[16:19], v[76:79], v[240:243]\n" \
M_"v[172:175], a[16:19], a[220:223], v[172:175]\n" \
B_"v6, s[80:83], s73 offen offset:2048 lds\n" \
M_"v[168:171], a[16:19], a[204:207], v[168:171]\n" \
X_"a[16:17], v35 offset:52544\n" \
X_"a[18:19], v35 offset:54848\n" \
M_"v[176:179], a[20:23], a[204:207], v[176:179]\n" \
M_"v[180:183], a[20:23], a[220:223], v[180:183]\n" \
M_"v[248:251], a[20:23], v[76:79], v[248:251]\n" \
M_"v[252:255], a[20:23], v[100:103], v[252:255]\n" \
X_"a[20:21], v35 offset:42208\n" \
X_"a[22:23], v35 offset:44512\n" \
M_"v[112:115], a[24:27], a[124:127], v[112:115]\n" \
B_"v7, s[80:83], s73 offen offset:3072 lds\n" \
M_"v[112:115], a[28:31], a[224:227], v[112:115]\n" \
"s_add_u32 m0, s92, 0x2000\n" \
"v_readlane_b32 s92, v11, 49\n" \
M_"v[116:119], a[24:27], a[228:231], v[116:119]\n" \
M_"v[116:119], a[28:31], a[232:235], v[116:119]\n" \
M_"v[184:187], a[24:27], a[236:239], v[184:187]\n" \
M_"v[184:187], a[28:31], a[240:243], v[184:187]\n" \
B_"v8, s[80:83], s73 offen offset:0 lds\n" \
M_"v[188:191], a[24:27], a[244:247], v[188:191]\n" \
X_"a[24:25], v35 offset:52576\n" \
X_"a[26:27], v35 offset:54880\n" \
M_"v[188:191], a[28:31], a[248:251], v[188:191]\n" \
"s_mov_b32 m0, s94\n" \
X_"a[28:29], v35 offset:42240\n" \
M_"v[196:199], a[96:99], a[244:247], v[196:199]\n" \
X_"a[30:31], v35 offset:44544\n" \
"v_readlane_b32 s73, v11, 6\n" \
M_"v[196:199], a[100:103], a[248:251], v[196:199]\n" \
M_"v[192:195], a[96:99], a[236:239], v[192:195]\n" \
B_"v9, s[84:87], 0 offen lds\n" \
M_"v[192:195], a[100:103], a[240:243], v[192:195]\n" \
M_"v[124:127], a[96:99], a[228:231], v[124:127]\n" \
M_"v[124:127], a[100:103], a[232:235], v[124:127]\n" \
M_"v[120:123], a[96:99], a[124:127], v[120:123]\n" \
X_"a[96:97], v35 offset:52608\n" \
X_"a[98:99], v35 offset:54912\n" \
M_"v[120:123], a[100:103], a[224:227], v[120:123]\n" \
X_"a[100:101], v33 offset:0\n" \
X_"a[102:103], v33 offset:4608\n" \
M_"v[128:131], a[104:107], a[124:127], v[128:131]\n" \
B_"a[128:131], v20, s[56:59], s68 offen offset:0 sc1\n" \
M_"v[128:131], a[108:111], a[224:227], v[128:131]\n" \
M_"v[132:135], a[104:107], a[228:231], v[132:135]\n" \
M_"v[132:135], a[108:111], a[232:235], v[132:135]\n" \
M_"v[200:203], a[104:107], a[236:239], v[200:203]\n" \
M_"v[200:203], a[108:111], a[240:243], v[200:203]\n" \
B_"a[132:135], v20, s[56:59], s68 offen offset:1024 sc1\n" \
M_"v[204:207], a[104:107], a[244:247], v[204:207]\n" \
X_"a[104:105], v33 offset:9216\n" \
X_"a[106:107], v33 offset:13824\n" \
M_"v[204:207], a[108:111], a[248:251], v[204:207]\n" \
X_"a[108:109], v33 offset:32\n" \
"s_waitcnt lgkmcnt(7)\n" \
M_"v[212:215], a[112:115], a[244:247], v[212:215]\n" \
X_"a[110:111], v33 offset:4640\n" \
M_"v[212:215], a[116:119], a[248:251], v[212:215]\n" \
M_"v[208:211], a[112:115], a[236:239], v[208:211]\n" \
"buffer_load_dword v47, v38, s[56:59], s71 offen sc1\n" \
M_"v[208:211], a[116:119], a[240:243], v[208:211]\n" \
M_"v[140:143], a[112:115], a[228:231], v[140:143]\n" \
M_"v[140:143], a[116:119], a[232:235], v[140:143]\n" \
M_"v[136:139], a[112:115], a[124:127], v[136:139]\n" \
X_"a[112:113], v33 offset:9248\n" \
X_"a[114:115], v33 offset:13856\n" \
M_"v[136:139], a[116:119], a[224:227], v[136:139]\n" \
X_"a[116:117], v33 offset:64\n" \
X_"a[118:119], v33 offset:4672\n" \
M_"v[144:147], a[120:123], a[124:127], v[144:147]\n" \
B_"a[144:147], v32, s[56:59], s68 offen offset:0 sc1\n" \
M_"v[144:147], a[0:3], a[224:227], v[144:147]\n" \
M_"v[148:151], a[120:123], a[228:231], v[148:151]\n" \
M_"v[148:151], a[0:3], a[232:235], v[148:151]\n" \
M_"v[216:219], a[120:123], a[236:239], v[216:219]\n" \
M_"v[216:219], a[0:3], a[240:243], v[216:219]\n" \
B_"a[148:151], v32, s[56:59], s68 offen offset:1024 sc1\n" \
M_"v[220:223], a[120:123], a[244:247], v[220:223]\n" \
X_"a[120:121], v33 offset:9280\n" \
X_"a[122:123], v33 offset:13888\n" \
M_"v[220:223], a[0:3], a[248:251], v[220:223]\n" \
X_"a[0:1], v33 offset:96\n" \
X_"a[2:3], v33 offset:4704\n" \
M_"v[228:231], a[4:7], a[244:247], v[228:231]\n" \
M_"v[228:231], a[8:11], a[248:251], v[228:231]\n" \
M_"v[224:227], a[4:7], a[236:239], v[224:227]\n" \
B_"a[152:155], v32, s[56:59], s68 offen offset:2048 sc1\n" \
M_"v[224:227], a[8:11], a[240:243], v[224:227]\n" \
M_"v[156:159], a[4:7], a[228:231], v[156:159]\n" \
M_"v[156:159], a[8:11], a[232:235], v[156:159]\n" \
M_"v[152:155], a[4:7], a[124:127], v[152:155]\n" \
X_"a[4:5], v33 offset:9312\n" \
X_"a[6:7], v33 offset:13920\n" \
M_"v[152:155], a[8:11], a[224:227], v[152:155]\n" \
X_"a[8:9], v33 offset:128\n" \
X_"a[10:11], v33 offset:4736\n" \
M_"v[160:163], a[12:15], a[124:127], v[160:163]\n" \
B_"a[156:159], v32, s[56:59], s68 offen offset:3072 sc1\n" \
M_"v[160:163], a[16:19], a[224:227], v[160:163]\n" \
M_"v[164:167], a[12:15], a[228:231], v[164:167]\n" \
M_"v[164:167], a[16:19], a[232:235], v[164:167]\n" \
M_"v[232:235], a[12:15], a[236:239], v[232:235]\n" \
M_"v[232:235], a[16:19], a[240:243], v[232:235]\n" \
B_"a[136:139], v20, s[56:59], s68 offen offset:2048 sc1\n" \
M_"v[236:239], a[12:15], a[244:247], v[236:239]\n" \
X_"a[12:13], v33 offset:9344\n" \
X_"a[14:15], v33 offset:13952\n" \
M_"v[236:239], a[16:19], a[248:251], v[236:239]\n" \
X_"a[16:17], v33 offset:160\n" \
X_"a[18:19], v33 offset:4768\n" \
M_"v[244:247], a[20:23], a[244:247], v[244:247]\n" \
M_"v[244:247], a[24:27], a[248:251], v[244:247]\n" \
M_"v[240:243], a[20:23], a[236:239], v[240:243]\n" \
B_"a[140:143], v20, s[56:59], s68 offen offset:3072 sc1\n" \
M_"v[240:243], a[24:27], a[240:243], v[240:243]\n" \
"v_readlane_b32 s68, v11, 52\n" \
M_"v[172:175], a[20:23], a[228:231], v[172:175]\n" \
M_"v[172:175], a[24:27], a[232:235], v[172:175]\n" \
M_"v[168:171], a[20:23], a[124:127], v[168:171]\n" \
X_"a[20:21], v33 offset:9376\n" \
X_"a[22:23], v33 offset:13984\n" \
M_"v[168:171], a[24:27], a[224:227], v[168:171]\n" \
X_"a[24:25], v33 offset:192\n" \
X_"a[26:27], v33 offset:4800\n" \
M_"v[176:179], a[28:31], a[124:127], v[176:179]\n" \
"s_waitcnt lgkmcnt(8)\n" \
M_"v[176:179], a[96:99], a[224:227], v[176:179]\n" \
B_"a[32:35], v20, s[56:59], s69 offen offset:0 sc1\n" \
M_"v[180:183], a[28:31], a[228:231], v[180:183]\n" \
M_"v[180:183], a[96:99], a[232:235], v[180:183]\n" \
M_"v[248:251], a[28:31], a[236:239], v[248:251]\n" \
M_"v[248:251], a[96:99], a[240:243], v[248:251]\n" \
M_"v[252:255], a[28:31], a[244:247], v[252:255]\n" \
X_"a[28:29], v33 offset:9408\n" \
X_"a[30:31], v33 offset:14016\n" \
M_"v[252:255], a[96:99], a[248:251], v[252:255]\n" \
X_"a[96:97], v33 offset:224\n" \
"s_waitcnt vmcnt(18)\n" \
M_"v[112:115], a[100:103], a[64:67], v[112:115]\n" \
B_"a[36:39], v20, s[56:59], s69 offen offset:1024 sc1\n" \
M_"v[112:115], a[104:107], a[68:71], v[112:115]\n" \
X_"a[98:99], v33 offset:4832\n" \
M_"v[116:119], a[100:103], a[80:83], v[116:119]\n" \
X_"a[100:101], v33 offset:9440\n" \
X_"a[102:103], v33 offset:14048\n" \
M_"v[116:119], a[104:107], a[84:87], v[116:119]\n" \
X_"a[104:105], v33 offset:256\n" \
X_"a[106:107], v33 offset:4864\n" \
M_"v[124:127], a[108:111], a[80:83], v[124:127]\n" \
M_"v[124:127], a[112:115], a[84:87], v[124:127]\n" \
B_"a[48:51], v32, s[56:59], s69 offen offset:0 sc1\n" \
M_"v[120:123], a[108:111], a[64:67], v[120:123]\n" \
X_"a[108:109], v33 offset:9472\n" \
X_"a[110:111], v33 offset:14080\n" \
M_"v[120:123], a[112:115], a[68:71], v[120:123]\n" \
X_"a[112:113], v33 offset:18432\n" \
X_"a[114:115], v33 offset:23040\n" \
M_"v[128:131], a[116:119], a[64:67], v[128:131]\n" \
M_"v[128:131], a[120:123], a[68:71], v[128:131]\n" \
M_"v[132:135], a[116:119], a[80:83], v[132:135]\n" \
B_"a[52:55], v32, s[56:59], s69 offen offset:1024 sc1\n" \
M_"v[132:135], a[120:123], a[84:87], v[132:135]\n" \
X_"a[116:117], v33 offset:27648\n" \
X_"a[118:119], v33 offset:32256\n" \
M_"v[140:143], a[0:3], a[80:83], v[140:143]\n" \
X_"a[120:121], v33 offset:18464\n" \
X_"a[122:123], v33 offset:23072\n" \
M_"v[140:143], a[4:7], a[84:87], v[140:143]\n" \
"s_waitcnt vmcnt(10)\n" \
"v_cmp_ne_u32_e32 vcc, s62, v47\n" \
M_"v[136:139], a[0:3], a[64:67], v[136:139]\n" \
X_"a[0:1], v33 offset:27680\n" \
".fill (L_ab + 36864 - .) / 4, 4, 0xBF800000\n" \
"L_ap9:\n" \
"s_cbranch_execz L_ap0\n" \
X_"a[2:3], v33 offset:32288\n" \
M_"v[136:139], a[4:7], a[68:71], v[136:139]\n" \
B_"a[56:59], v32, s[56:59], s69 offen offset:2048 sc1\n" \
M_"v[144:147], a[8:11], a[64:67], v[144:147]\n" \
X_"a[4:5], v33 offset:18496\n" \
"s_waitcnt lgkmcnt(13)\n" \
M_"v[144:147], a[12:15], a[68:71], v[144:147]\n" \
X_"a[6:7], v33 offset:23104\n" \
"s_cbranch_vccnz L_sb_%=\n" \
"L_rb_%=:\n" \
M_"v[148:151], a[8:11], a[80:83], v[148:151]\n" \
X_"a[8:9], v33 offset:27712\n" \
X_"a[10:11], v33 offset:32320\n" \
M_"v[148:151], a[12:15], a[84:87], v[148:151]\n" \
X_"a[12:13], v33 offset:18528\n" \
X_"a[14:15], v33 offset:23136\n" \
B_"a[60:63], v32, s[56:59], s69 offen offset:3072 sc1\n" \
M_"v[156:159], a[16:19], a[80:83], v[156:159]\n" \
"v_readlane_b32 s71, v11, 55\n" \
"v_cmp_eq_u32_e32 vcc, 0, v44\n" \
M_"v[156:159], a[20:23], a[84:87], v[156:159]\n" \
M_"v[152:155], a[16:19], a[64:67], v[152:155]\n" \
X_"a[16:17], v33 offset:27744\n" \
X_"a[18:19], v33 offset:32352\n" \
M_"v[152:155], a[20:23], a[68:71], v[152:155]\n" \
X_"a[20:21], v33 offset:18560\n" \
X_"a[22:23], v33 offset:23168\n" \
B_"a[40:43], v20, s[56:59], s69 offen offset:2048 sc1\n" \
M_"v[160:163], a[24:27], a[64:67], v[160:163]\n" \
"v_cndmask_b32_e32 v50, 0, v45, vcc\n" \
"v_cmp_eq_u32_e32 vcc, 1, v44\n" \
M_"v[160:163], a[28:31], a[68:71], v[160:163]\n" \
"v_and_b32_e32 v44, 0x3c0, v20\n" \
"v_cndmask_b32_e32 v51, 0, v45, vcc\n" \
M_"v[164:167], a[24:27], a[80:83], v[164:167]\n" \
X_"a[24:25], v33 offset:27776\n" \
X_"a[26:27], v33 offset:32384\n" \
M_"v[164:167], a[28:31], a[84:87], v[164:167]\n" \
X_"a[28:29], v33 offset:18592\n" \
X_"a[30:31], v33 offset:23200\n" \
B_"a[44:47], v20, s[56:59], s69 offen offset:3072 sc1\n" \
M_"v[172:175], a[96:99], a[80:83], v[172:175]\n" \
"v_readlane_b32 s69, v11, 53\n" \
"v_bfe_u32 v45, v20, 1, 5\n" \
M_"v[172:175], a[100:103], a[84:87], v[172:175]\n" \
"v_and_b32_e32 v45, 24, v45\n" \
"v_add_u32_e32 v45, 0x4800, v45\n" \
M_"v[168:171], a[96:99], a[64:67], v[168:171]\n" \
X_"a[96:97], v33 offset:27808\n" \
X_"a[98:99], v33 offset:32416\n" \
M_"v[168:171], a[100:103], a[68:71], v[168:171]\n" \
X_"a[100:101], v33 offset:18624\n" \
"s_waitcnt lgkmcnt(13)\n" \
M_"v[176:179], a[104:107], a[64:67], v[176:179]\n" \
X_"a[102:103], v33 offset:23232\n" \
"v_add_u32_e32 v36, v44, v45\n" \
M_"v[176:179], a[108:111], a[68:71], v[176:179]\n" \
B_"a[124:127], v20, s[56:59], s64 offen offset:0 sc1\n" \
M_"v[180:183], a[104:107], a[80:83], v[180:183]\n" \
X_"a[104:105], v33 offset:27840\n" \
X_"a[106:107], v33 offset:32448\n" \
M_"v[180:183], a[108:111], a[84:87], v[180:183]\n" \
X_"a[108:109], v33 offset:18656\n" \
X_"a[110:111], v33 offset:23264\n" \
M_"v[112:115], a[112:115], a[72:75], v[112:115]\n" \
M_"v[112:115], a[116:119], a[76:79], v[112:115]\n" \
M_"v[116:119], a[112:115], a[88:91], v[116:119]\n" \
X_"a[112:113], v33 offset:27872\n" \
X_"a[114:115], v33 offset:32480\n" \
M_"v[116:119], a[116:119], a[92:95], v[116:119]\n" \
X_"a[116:117], v33 offset:18688\n" \
X_"a[118:119], v33 offset:23296\n" \
M_"v[124:127], a[120:123], a[88:91], v[124:127]\n" \
B_"a[224:227], v20, s[56:59], s64 offen offset:1024 sc1\n" \
M_"v[124:127], a[0:3], a[92:95], v[124:127]\n" \
M_"v[120:123], a[120:123], a[72:75], v[120:123]\n" \
X_"a[120:121], v33 offset:27904\n" \
X_"a[122:123], v33 offset:32512\n" \
M_"v[120:123], a[0:3], a[76:79], v[120:123]\n" \
"s_barrier\n" \
M_"v[128:131], a[4:7], a[72:75], v[128:131]\n" \
X_"a[0:1], v33 offset:36864\n" \
X_"a[2:3], v33 offset:41472\n" \
M_"v[128:131], a[8:11], a[76:79], v[128:131]\n" \
B_"a[236:239], v32, s[56:59], s64 offen offset:0 sc1\n" \
M_"v[132:135], a[4:7], a[88:91], v[132:135]\n" \
X_"a[4:5], v33 offset:46080\n" \
X_"a[6:7], v33 offset:50688\n" \
M_"v[132:135], a[8:11], a[92:95], v[132:135]\n" \
X_"a[8:9], v33 offset:55296\n" \
"s_waitcnt lgkmcnt(15)\n" \
M_"v[140:143], a[12:15], a[88:91], v[140:143]\n" \
X_"a[10:11], v33 offset:59904\n" \
M_"v[140:143], a[16:19], a[92:95], v[140:143]\n" \
M_"v[136:139], a[12:15], a[72:75], v[136:139]\n" \
X_"a[12:13], v10 offset:0\n" \
X_"a[14:15], v10 offset:4608\n" \
M_"v[136:139], a[16:19], a[76:79], v[136:139]\n" \
X_"a[16:17], v34 offset:0\n" \
X_"a[18:19], v34 offset:4608\n" \
M_"v[144:147], a[20:23], a[72:75], v[144:147]\n" \
B_"a[240:243], v32, s[56:59], s64 offen offset:1024 sc1\n" \
M_"v[144:147], a[24:27], a[76:79], v[144:147]\n" \
M_"v[148:151], a[20:23], a[88:91], v[148:151]\n" \
X_"a[20:21], v34 offset:9216\n" \
X_"a[22:23], v34 offset:13824\n" \
M_"v[148:151], a[24:27], a[92:95], v[148:151]\n" \
X_"a[24:25], v34 offset:18432\n" \
X_"a[26:27], v34 offset:23040\n" \
M_"v[156:159], a[28:31], a[88:91], v[156:159]\n" \
M_"v[156:159], a[96:99], a[92:95], v[156:159]\n" \
B_"a[244:247], v32, s[56:59], s64 offen offset:2048 sc1\n" \
M_"v[152:155], a[28:31], a[72:75], v[152:155]\n" \
X_"a[28:29], v10 offset:9216\n" \
X_"a[30:31], v10 offset:13824\n" \
M_"v[152:155], a[96:99], a[76:79], v[152:155]\n" \
X_"a[96:97], v33 offset:36896\n" \
X_"a[98:99], v33 offset:41504\n" \
M_"v[160:163], a[100:103], a[72:75], v[160:163]\n" \
"s_waitcnt lgkmcnt(15)\n" \
M_"v[160:163], a[104:107], a[76:79], v[160:163]\n" \
M_"v[164:167], a[100:103], a[88:91], v[164:167]\n" \
X_"a[100:101], v33 offset:46112\n" \
X_"a[102:103], v33 offset:50720\n" \
M_"v[164:167], a[104:107], a[92:95], v[164:167]\n" \
X_"a[104:105], v33 offset:55328\n" \
X_"a[106:107], v33 offset:59936\n" \
M_"v[172:175], a[108:111], a[88:91], v[172:175]\n" \
B_"a[248:251], v32, s[56:59], s64 offen offset:3072 sc1\n" \
M_"v[172:175], a[112:115], a[92:95], v[172:175]\n" \
M_"v[168:171], a[108:111], a[72:75], v[168:171]\n" \
X_"a[108:109], v10 offset:32\n" \
X_"a[110:111], v10 offset:4640\n" \
M_"v[168:171], a[112:115], a[76:79], v[168:171]\n" \
X_"a[112:113], v34 offset:32\n" \
X_"a[114:115], v34 offset:4640\n" \
M_"v[176:179], a[116:119], a[72:75], v[176:179]\n" \
M_"v[176:179], a[120:123], a[76:79], v[176:179]\n" \
B_"a[228:231], v20, s[56:59], s64 offen offset:2048 sc1\n" \
M_"v[180:183], a[116:119], a[88:91], v[180:183]\n" \
X_"a[116:117], v34 offset:9248\n" \
X_"a[118:119], v34 offset:13856\n" \
M_"v[180:183], a[120:123], a[92:95], v[180:183]\n" \
X_"a[120:121], v34 offset:18464\n" \
X_"a[122:123], v34 offset:23072\n" \
M_"v[112:115], a[0:3], a[128:131], v[112:115]\n" \
"s_waitcnt lgkmcnt(15)\n" \
M_"v[112:115], a[4:7], a[132:135], v[112:115]\n" \
"s_waitcnt vmcnt(18)\n" \
M_"v[116:119], a[0:3], a[144:147], v[116:119]\n" \
X_"a[0:1], v10 offset:9248\n" \
X_"a[2:3], v10 offset:13856\n" \
M_"v[116:119], a[4:7], a[148:151], v[116:119]\n" \
X_"a[4:5], v33 offset:36928\n" \
X_"a[6:7], v33 offset:41536\n" \
M_"v[116:119], a[8:11], a[152:155], v[116:119]\n" \
"s_waitcnt vmcnt(17)\n" \
M_"v[116:119], a[12:15], a[156:159], v[116:119]\n" \
"s_waitcnt vmcnt(16)\n" \
M_"v[112:115], a[8:11], a[136:139], v[112:115]\n" \
X_"a[8:9], v33 offset:46144\n" \
"s_waitcnt vmcnt(15)\n" \
M_"v[112:115], a[12:15], a[140:143], v[112:115]\n" \
X_"a[10:11], v33 offset:50752\n" \
"s_waitcnt vmcnt(14)\n" \
M_"v[112:115], a[16:19], a[32:35], v[112:115]\n" \
X_"a[12:13], v33 offset:55360\n" \
"s_waitcnt vmcnt(13)\n" \
M_"v[112:115], a[20:23], a[36:39], v[112:115]\n" \
X_"a[14:15], v33 offset:59968\n" \
"s_waitcnt vmcnt(12)\n" \
M_"v[116:119], a[16:19], a[48:51], v[116:119]\n" \
X_"a[16:17], v10 offset:64\n" \
"s_waitcnt vmcnt(11)\n" \
M_"v[116:119], a[20:23], a[52:55], v[116:119]\n" \
X_"a[18:19], v10 offset:4672\n" \
"s_waitcnt vmcnt(10)\n" \
M_"v[116:119], a[24:27], a[56:59], v[116:119]\n" \
X_"a[20:21], v34 offset:64\n" \
X_"a[22:23], v34 offset:4672\n" \
B_"a[232:235], v20, s[56:59], s64 offen offset:3072 sc1\n" \
"s_waitcnt vmcnt(10) lgkmcnt(12)\n" \
M_"v[116:119], a[28:31], a[60:63], v[116:119]\n" \
B_"a[160:163], v20, s[56:59], s72 offen offset:0 sc1\n" \
"s_waitcnt vmcnt(10)\n" \
M_"v[112:115], a[24:27], a[40:43], v[112:115]\n" \
X_"a[24:25], v34 offset:9280\n" \
X_"a[26:27], v34 offset:13888\n" \
B_"a[164:167], v20, s[56:59], s72 offen offset:1024 sc1\n" \
"s_waitcnt vmcnt(10)\n" \
M_"v[112:115], a[28:31], a[44:47], v[112:115]\n" \
X_"a[28:29], v34 offset:18496\n" \
X_"a[30:31], v34 offset:23104\n" \
M_"v[120:123], a[96:99], a[128:131], v[120:123]\n" \
B_"a[176:179], v32, s[56:59], s72 offen offset:0 sc1\n" \
M_"v[120:123], a[100:103], a[132:135], v[120:123]\n" \
M_"v[124:127], a[96:99], a[144:147], v[124:127]\n" \
X_"a[96:97], v10 offset:9280\n" \
X_"a[98:99], v10 offset:13888\n" \
C_"v112, v112, v113\n" \
C_"v113, v114, v115\n" \
C_"v114, v116, v117\n" \
C_"v115, v118, v119\n" \
"buffer_store_dwordx4 v[112:115], v20, s[56:59], s65 offen offset:0 sc1\n" \
"s_add_u32 s65, s65, 0x3800\n" \
M_"v[124:127], a[100:103], a[148:151], v[124:127]\n" \
X_"a[100:101], v33 offset:36960\n" \
X_"a[102:103], v33 offset:41568\n" \
M_"v[124:127], a[104:107], a[152:155], v[124:127]\n" \
M_"v[124:127], a[108:111], a[156:159], v[124:127]\n" \
M_"v[120:123], a[104:107], a[136:139], v[120:123]\n" \
X_"a[104:105], v33 offset:46176\n" \
X_"a[106:107], v33 offset:50784\n" \
M_"v[120:123], a[108:111], a[140:143], v[120:123]\n" \
X_"a[108:109], v33 offset:55392\n" \
X_"a[110:111], v33 offset:60000\n" \
M_"v[120:123], a[112:115], a[32:35], v[120:123]\n" \
B_"a[180:183], v32, s[56:59], s72 offen offset:1024 sc1\n" \
M_"v[120:123], a[116:119], a[36:39], v[120:123]\n" \
M_"v[124:127], a[112:115], a[48:51], v[124:127]\n" \
X_"a[112:113], v10 offset:96\n" \
X_"a[114:115], v10 offset:4704\n" \
M_"v[124:127], a[116:119], a[52:55], v[124:127]\n" \
X_"a[116:117], v34 offset:96\n" \
X_"a[118:119], v34 offset:4704\n" \
M_"v[124:127], a[120:123], a[56:59], v[124:127]\n" \
"s_waitcnt lgkmcnt(12)\n" \
M_"v[124:127], a[0:3], a[60:63], v[124:127]\n" \
B_"a[168:171], v20, s[56:59], s72 offen offset:2048 sc1\n" \
M_"v[120:123], a[120:123], a[40:43], v[120:123]\n" \
X_"a[120:121], v34 offset:9312\n" \
X_"a[122:123], v34 offset:13920\n" \
M_"v[120:123], a[0:3], a[44:47], v[120:123]\n" \
X_"a[0:1], v34 offset:18528\n" \
X_"a[2:3], v34 offset:23136\n" \
M_"v[128:131], a[4:7], a[128:131], v[128:131]\n" \
M_"v[128:131], a[8:11], a[132:135], v[128:131]\n" \
M_"v[132:135], a[4:7], a[144:147], v[132:135]\n" \
X_"a[4:5], v10 offset:9312\n" \
X_"a[6:7], v10 offset:13920\n" \
M_"v[132:135], a[8:11], a[148:151], v[132:135]\n" \
C_"v120, v120, v121\n" \
C_"v121, v122, v123\n" \
C_"v122, v124, v125\n" \
C_"v123, v126, v127\n" \
"buffer_store_dwordx4 v[120:123], v20, s[56:59], s66 offen offset:0 sc1\n" \
"s_add_u32 s66, s66, 0x3000\n" \
M_"v[132:135], a[12:15], a[152:155], v[132:135]\n" \
X_"a[8:9], v33 offset:36992\n" \
X_"a[10:11], v33 offset:41600\n" \
M_"v[132:135], a[16:19], a[156:159], v[132:135]\n" \
M_"v[128:131], a[12:15], a[136:139], v[128:131]\n" \
X_"a[12:13], v33 offset:46208\n" \
X_"a[14:15], v33 offset:50816\n" \
M_"v[128:131], a[16:19], a[140:143], v[128:131]\n" \
X_"a[16:17], v33 offset:55424\n" \
X_"a[18:19], v33 offset:60032\n" \
M_"v[128:131], a[20:23], a[32:35], v[128:131]\n" \
B_"a[184:187], v32, s[56:59], s72 offen offset:2048 sc1\n" \
M_"v[128:131], a[24:27], a[36:39], v[128:131]\n" \
M_"v[132:135], a[20:23], a[48:51], v[132:135]\n" \
X_"a[20:21], v10 offset:128\n" \
X_"a[22:23], v10 offset:4736\n" \
M_"v[132:135], a[24:27], a[52:55], v[132:135]\n" \
X_"a[24:25], v34 offset:128\n" \
X_"a[26:27], v34 offset:4736\n" \
M_"v[132:135], a[28:31], a[56:59], v[132:135]\n" \
"s_waitcnt lgkmcnt(12)\n" \
M_"v[132:135], a[96:99], a[60:63], v[132:135]\n" \
B_"a[172:175], v20, s[56:59], s72 offen offset:3072 sc1\n" \
M_"v[128:131], a[28:31], a[40:43], v[128:131]\n" \
X_"a[28:29], v34 offset:9344\n" \
X_"a[30:31], v34 offset:13952\n" \
M_"v[128:131], a[96:99], a[44:47], v[128:131]\n" \
X_"a[96:97], v34 offset:18560\n" \
X_"a[98:99], v34 offset:23168\n" \
M_"v[136:139], a[100:103], a[128:131], v[136:139]\n" \
M_"v[136:139], a[104:107], a[132:135], v[136:139]\n" \
M_"v[140:143], a[100:103], a[144:147], v[140:143]\n" \
B_"a[188:191], v32, s[56:59], s72 offen offset:3072 sc1\n" \
M_"v[140:143], a[104:107], a[148:151], v[140:143]\n" \
"v_readlane_b32 s72, v11, 56\n" \
C_"v128, v128, v129\n" \
C_"v129, v130, v131\n" \
C_"v130, v132, v133\n" \
C_"v131, v134, v135\n" \
M_"v[140:143], a[108:111], a[152:155], v[140:143]\n" \
"buffer_store_dwordx4 v[128:131], v20, s[56:59], s67 offen offset:0 sc1\n" \
"s_add_u32 s67, s67, 0x2800\n" \
M_"v[140:143], a[112:115], a[156:159], v[140:143]\n" \
X_"a[100:101], v10 offset:9344\n" \
X_"a[102:103], v10 offset:13952\n" \
M_"v[136:139], a[108:111], a[136:139], v[136:139]\n" \
X_"a[104:105], v33 offset:37024\n" \
X_"a[106:107], v33 offset:41632\n" \
M_"v[136:139], a[112:115], a[140:143], v[136:139]\n" \
X_"a[108:109], v33 offset:46240\n" \
X_"a[110:111], v33 offset:50848\n" \
M_"v[136:139], a[116:119], a[32:35], v[136:139]\n" \
X_"a[112:113], v33 offset:55456\n" \
X_"a[114:115], v33 offset:60064\n" \
M_"v[136:139], a[120:123], a[36:39], v[136:139]\n" \
B_"a[192:195], v20, s[56:59], s93 offen offset:0 sc1\n" \
M_"v[140:143], a[116:119], a[48:51], v[140:143]\n" \
X_"a[116:117], v10 offset:160\n" \
X_"a[118:119], v10 offset:4768\n" \
M_"v[140:143], a[120:123], a[52:55], v[140:143]\n" \
X_"a[120:121], v34 offset:160\n" \
X_"a[122:123], v34 offset:4768\n" \
M_"v[140:143], a[0:3], a[56:59], v[140:143]\n" \
"s_waitcnt lgkmcnt(13)\n" \
M_"v[140:143], a[4:7], a[60:63], v[140:143]\n" \
M_"v[136:139], a[0:3], a[40:43], v[136:139]\n" \
B_"a[196:199], v20, s[56:59], s93 offen offset:1024 sc1\n" \
M_"v[136:139], a[4:7], a[44:47], v[136:139]\n" \
X_"a[0:1], v34 offset:9376\n" \
X_"a[2:3], v34 offset:13984\n" \
M_"v[144:147], a[8:11], a[128:131], v[144:147]\n" \
X_"a[4:5], v34 offset:18592\n" \
X_"a[6:7], v34 offset:23200\n" \
M_"v[144:147], a[12:15], a[132:135], v[144:147]\n" \
M_"v[148:151], a[8:11], a[144:147], v[148:151]\n" \
X_"a[8:9], v10 offset:9376\n" \
C_"v136, v136, v137\n" \
C_"v137, v138, v139\n" \
C_"v138, v140, v141\n" \
C_"v139, v142, v143\n" \
"buffer_store_dwordx4 v[136:139], v20, s[56:59], s68 offen offset:0 sc1\n" \
"s_add_u32 s68, s68, 0x2000\n" \
M_"v[148:151], a[12:15], a[148:151], v[148:151]\n" \
X_"a[10:11], v10 offset:13984\n" \
X_"a[12:13], v33 offset:37056\n" \
M_"v[148:151], a[16:19], a[152:155], v[148:151]\n" \
X_"a[14:15], v33 offset:41664\n" \
M_"v[148:151], a[20:23], a[156:159], v[148:151]\n" \
M_"v[144:147], a[16:19], a[136:139], v[144:147]\n" \
B_"a[208:211], v32, s[56:59], s93 offen offset:0 sc1\n" \
M_"v[144:147], a[20:23], a[140:143], v[144:147]\n" \
X_"a[16:17], v33 offset:46272\n" \
X_"a[18:19], v33 offset:50880\n" \
M_"v[144:147], a[24:27], a[32:35], v[144:147]\n" \
X_"a[20:21], v33 offset:55488\n" \
X_"a[22:23], v33 offset:60096\n" \
M_"v[144:147], a[28:31], a[36:39], v[144:147]\n" \
M_"v[148:151], a[24:27], a[48:51], v[148:151]\n" \
X_"a[24:25], v10 offset:192\n" \
X_"a[26:27], v10 offset:4800\n" \
B_"a[212:215], v32, s[56:59], s93 offen offset:1024 sc1\n" \
M_"v[148:151], a[28:31], a[52:55], v[148:151]\n" \
X_"a[28:29], v34 offset:192\n" \
"s_waitcnt lgkmcnt(13)\n" \
M_"v[148:151], a[96:99], a[56:59], v[148:151]\n" \
X_"a[30:31], v34 offset:4800\n" \
M_"v[148:151], a[100:103], a[60:63], v[148:151]\n" \
M_"v[144:147], a[96:99], a[40:43], v[144:147]\n" \
X_"a[96:97], v34 offset:9408\n" \
X_"a[98:99], v34 offset:14016\n" \
B_"a[216:219], v32, s[56:59], s93 offen offset:2048 sc1\n" \
M_"v[144:147], a[100:103], a[44:47], v[144:147]\n" \
X_"a[100:101], v34 offset:18624\n" \
X_"a[102:103], v34 offset:23232\n" \
M_"v[152:155], a[104:107], a[128:131], v[152:155]\n" \
M_"v[152:155], a[108:111], a[132:135], v[152:155]\n" \
M_"v[156:159], a[104:107], a[144:147], v[156:159]\n" \
X_"a[104:105], v10 offset:9408\n" \
X_"a[106:107], v10 offset:14016\n" \
B_"a[220:223], v32, s[56:59], s93 offen offset:3072 sc1\n" \
C_"v144, v144, v145\n" \
C_"v145, v146, v147\n" \
C_"v146, v148, v149\n" \
C_"v147, v150, v151\n" \
M_"v[156:159], a[108:111], a[148:151], v[156:159]\n" \
X_"a[108:109], v33 offset:256\n" \
X_"a[110:111], v33 offset:4864\n" \
"buffer_store_dwordx4 v[144:147], v20, s[56:59], s69 offen offset:0 sc1\n" \
"s_add_u32 s69, s69, 0x1800\n" \
M_"v[156:159], a[112:115], a[152:155], v[156:159]\n" \
M_"v[156:159], a[116:119], a[156:159], v[156:159]\n" \
M_"v[152:155], a[112:115], a[136:139], v[152:155]\n" \
X_"a[112:113], v33 offset:9472\n" \
X_"a[114:115], v33 offset:14080\n" \
M_"v[152:155], a[116:119], a[140:143], v[152:155]\n" \
B_"a[200:203], v20, s[56:59], s93 offen offset:2048 sc1\n" \
M_"v[152:155], a[120:123], a[32:35], v[152:155]\n" \
X_"a[116:117], v33 offset:18688\n" \
X_"a[118:119], v33 offset:23296\n" \
M_"v[152:155], a[0:3], a[36:39], v[152:155]\n" \
M_"v[156:159], a[120:123], a[48:51], v[156:159]\n" \
X_"a[120:121], v33 offset:27904\n" \
X_"a[122:123], v33 offset:32512\n" \
M_"v[156:159], a[0:3], a[52:55], v[156:159]\n" \
X_"a[0:1], v33 offset:37120\n" \
B_"a[204:207], v20, s[56:59], s93 offen offset:3072 sc1\n" \
"s_waitcnt lgkmcnt(11)\n" \
M_"v[156:159], a[4:7], a[56:59], v[156:159]\n" \
X_"a[2:3], v33 offset:41728\n" \
M_"v[156:159], a[8:11], a[60:63], v[156:159]\n" \
M_"v[152:155], a[4:7], a[40:43], v[152:155]\n" \
X_"a[4:5], v33 offset:46336\n" \
X_"a[6:7], v33 offset:50944\n" \
M_"v[152:155], a[8:11], a[44:47], v[152:155]\n" \
X_"a[8:9], v33 offset:55552\n" \
X_"a[10:11], v33 offset:60160\n" \
M_"v[160:163], a[12:15], a[128:131], v[160:163]\n" \
B_"v[64:67], v20, s[56:59], s95 offen offset:0 sc1\n" \
M_"v[160:163], a[16:19], a[132:135], v[160:163]\n" \
M_"v[164:167], a[12:15], a[144:147], v[164:167]\n" \
X_"a[12:13], v10 offset:256\n" \
X_"a[14:15], v10 offset:4864\n" \
C_"v152, v152, v153\n" \
C_"v153, v154, v155\n" \
C_"v154, v156, v157\n" \
C_"v155, v158, v159\n" \
"buffer_store_dwordx4 v[152:155], v20, s[56:59], s70 offen offset:0 sc1\n" \
"s_add_u32 s70, s70, 0x1000\n" \
M_"v[164:167], a[16:19], a[148:151], v[164:167]\n" \
X_"a[16:17], v34 offset:256\n" \
X_"a[18:19], v34 offset:4864\n" \
M_"v[164:167], a[20:23], a[152:155], v[164:167]\n" \
M_"v[164:167], a[24:27], a[156:159], v[164:167]\n" \
M_"v[160:163], a[20:23], a[136:139], v[160:163]\n" \
X_"a[20:21], v34 offset:9472\n" \
X_"a[22:23], v34 offset:14080\n" \
M_"v[160:163], a[24:27], a[140:143], v[160:163]\n" \
X_"a[24:25], v34 offset:18688\n" \
X_"a[26:27], v34 offset:23296\n" \
"s_waitcnt vmcnt(19)\n" \
"s_mov_b64 exec, 1\n" \
"buffer_store_dword v39, off, s[56:59], s65 offset:0 sc1\n" \
"s_mov_b64 exec, -1\n" \
M_"v[160:163], a[28:31], a[32:35], v[160:163]\n" \
M_"v[160:163], a[96:99], a[36:39], v[160:163]\n" \
M_"v[164:167], a[28:31], a[48:51], v[164:167]\n" \
X_"a[28:29], v10 offset:9472\n" \
X_"a[30:31], v10 offset:14080\n" \
M_"v[164:167], a[96:99], a[52:55], v[164:167]\n" \
X_"a[96:97], v33 offset:0\n" \
X_"a[98:99], v33 offset:4608\n" \
M_"v[164:167], a[100:103], a[56:59], v[164:167]\n" \
"s_waitcnt lgkmcnt(12)\n" \
M_"v[164:167], a[104:107], a[60:63], v[164:167]\n" \
B_"v[68:71], v20, s[56:59], s95 offen offset:1024 sc1\n" \
M_"v[160:163], a[100:103], a[40:43], v[160:163]\n" \
X_"a[100:101], v33 offset:9216\n" \
X_"a[102:103], v33 offset:13824\n" \
M_"v[160:163], a[104:107], a[44:47], v[160:163]\n" \
X_"a[104:105], v33 offset:18432\n" \
X_"a[106:107], v33 offset:23040\n" \
M_"v[248:251], a[108:111], a[124:127], v[248:251]\n" \
M_"v[248:251], a[112:115], a[224:227], v[248:251]\n" \
M_"v[252:255], a[108:111], a[236:239], v[252:255]\n" \
X_"a[108:109], v33 offset:27648\n" \
X_"a[110:111], v33 offset:32256\n" \
M_"v[252:255], a[112:115], a[240:243], v[252:255]\n" \
C_"v160, v160, v161\n" \
C_"v161, v162, v163\n" \
C_"v162, v164, v165\n" \
C_"v163, v166, v167\n" \
"buffer_store_dwordx4 v[160:163], v20, s[56:59], s71 offen offset:0 sc1\n" \
"s_add_u32 s71, s71, 0x800\n" \
M_"v[252:255], a[116:119], a[244:247], v[252:255]\n" \
X_"a[112:113], v33 offset:36864\n" \
X_"a[114:115], v33 offset:41472\n" \
M_"v[252:255], a[120:123], a[248:251], v[252:255]\n" \
M_"v[248:251], a[116:119], a[228:231], v[248:251]\n" \
X_"a[116:117], v33 offset:46080\n" \
X_"a[118:119], v33 offset:50688\n" \
"s_waitcnt vmcnt(19)\n" \
"s_mov_b64 exec, 1\n" \
"buffer_store_dword v39, off, s[56:59], s66 offset:4 sc1\n" \
"s_mov_b64 exec, -1\n" \
"v_readlane_b32 s66, v11, 50\n" \
M_"v[248:251], a[120:123], a[232:235], v[248:251]\n" \
X_"a[120:121], v33 offset:55296\n" \
X_"a[122:123], v33 offset:59904\n" \
M_"v[176:179], a[0:3], a[128:131], v[176:179]\n" \
M_"v[176:179], a[4:7], a[132:135], v[176:179]\n" \
M_"v[180:183], a[0:3], a[144:147], v[180:183]\n" \
B_"v[72:75], v32, s[56:59], s95 offen offset:0 sc1\n" \
M_"v[180:183], a[4:7], a[148:151], v[180:183]\n" \
M_"v[248:251], a[0:3], a[160:163], v[248:251]\n" \
M_"v[248:251], a[4:7], a[164:167], v[248:251]\n" \
M_"v[252:255], a[0:3], a[176:179], v[252:255]\n" \
X_"a[0:1], v10 offset:0\n" \
X_"a[2:3], v10 offset:4608\n" \
M_"v[252:255], a[4:7], a[180:183], v[252:255]\n" \
X_"a[4:5], v34 offset:0\n" \
"s_waitcnt vmcnt(20)\n" \
M_"v[252:255], a[8:11], a[184:187], v[252:255]\n" \
X_"a[6:7], v34 offset:4608\n" \
"s_waitcnt vmcnt(18) lgkmcnt(10)\n" \
M_"v[252:255], a[12:15], a[188:191], v[252:255]\n" \
B_"v[76:79], v32, s[56:59], s95 offen offset:1024 sc1\n" \
M_"v[248:251], a[8:11], a[168:171], v[248:251]\n" \
M_"v[248:251], a[12:15], a[172:175], v[248:251]\n" \
M_"v[180:183], a[8:11], a[152:155], v[180:183]\n" \
M_"v[180:183], a[12:15], a[156:159], v[180:183]\n" \
M_"v[176:179], a[8:11], a[136:139], v[176:179]\n" \
X_"a[8:9], v34 offset:9216\n" \
X_"a[10:11], v34 offset:13824\n" \
M_"v[176:179], a[12:15], a[140:143], v[176:179]\n" \
X_"a[12:13], v34 offset:18432\n" \
X_"a[14:15], v34 offset:23040\n" \
"s_waitcnt vmcnt(18)\n" \
"s_mov_b64 exec, 1\n" \
"buffer_store_dword v39, off, s[56:59], s67 offset:8 sc1\n" \
"s_mov_b64 exec, -1\n" \
"v_readlane_b32 s67, v11, 51\n" \
M_"v[176:179], a[16:19], a[32:35], v[176:179]\n" \
M_"v[176:179], a[20:23], a[36:39], v[176:179]\n" \
M_"v[180:183], a[16:19], a[48:51], v[180:183]\n" \
M_"v[180:183], a[20:23], a[52:55], v[180:183]\n" \
"s_waitcnt vmcnt(18)\n" \
M_"v[248:251], a[16:19], a[192:195], v[248:251]\n" \
"s_waitcnt vmcnt(17)\n" \
M_"v[248:251], a[20:23], a[196:199], v[248:251]\n" \
"s_waitcnt vmcnt(15)\n" \
M_"v[252:255], a[16:19], a[208:211], v[252:255]\n" \
X_"a[16:17], v10 offset:9216\n" \
"s_mov_b64 exec, 1\n" \
"buffer_store_dword v39, off, s[56:59], s68 offset:12 sc1\n" \
"s_mov_b64 exec, -1\n" \
"s_waitcnt vmcnt(15)\n" \
M_"v[252:255], a[20:23], a[212:215], v[252:255]\n" \
"v_readlane_b32 s68, v11, 52\n" \
"s_waitcnt vmcnt(14)\n" \
M_"v[252:255], a[24:27], a[216:219], v[252:255]\n" \
X_"a[18:19], v10 offset:13824\n" \
"s_waitcnt vmcnt(13)\n" \
M_"v[252:255], a[28:31], a[220:223], v[252:255]\n" \
X_"a[20:21], v33 offset:32\n" \
X_"a[22:23], v33 offset:4640\n" \
B_"v[80:83], v20, s[56:59], s96 offen offset:0 sc1\n" \
"s_waitcnt vmcnt(12)\n" \
M_"v[248:251], a[24:27], a[200:203], v[248:251]\n" \
"s_mov_b64 exec, 1\n" \
"buffer_store_dword v39, off, s[56:59], s69 offset:16 sc1\n" \
"s_mov_b64 exec, -1\n" \
"v_readlane_b32 s69, v11, 53\n" \
".fill (L_ab + 40960 - .) / 4, 4, 0xBF800000\n" \
"L_ap10:\n" \
"s_cbranch_execz L_ap0\n" \
"s_waitcnt vmcnt(12)\n" \
M_"v[248:251], a[28:31], a[204:207], v[248:251]\n" \
M_"v[180:183], a[24:27], a[56:59], v[180:183]\n" \
B_"v[84:87], v20, s[56:59], s96 offen offset:1024 sc1\n" \
M_"v[180:183], a[28:31], a[60:63], v[180:183]\n" \
M_"v[176:179], a[24:27], a[40:43], v[176:179]\n" \
X_"a[24:25], v33 offset:9248\n" \
X_"a[26:27], v33 offset:13856\n" \
M_"v[176:179], a[28:31], a[44:47], v[176:179]\n" \
X_"a[28:29], v33 offset:18464\n" \
C_"v248, v248, v249\n" \
C_"v249, v250, v251\n" \
C_"v250, v252, v253\n" \
C_"v251, v254, v255\n" \
"buffer_store_dwordx4 v[248:251], v20, s[56:59], s72 offen offset:1024 sc1\n" \
M_"v[184:187], a[96:99], a[124:127], v[184:187]\n" \
X_"a[30:31], v33 offset:23072\n" \
C_"v176, v176, v177\n" \
C_"v177, v178, v179\n" \
C_"v178, v180, v181\n" \
C_"v179, v182, v183\n" \
M_"v[184:187], a[100:103], a[224:227], v[184:187]\n" \
"buffer_store_dwordx4 v[176:179], v20, s[56:59], s72 offen offset:0 sc1\n" \
M_"v[188:191], a[96:99], a[236:239], v[188:191]\n" \
X_"a[96:97], v33 offset:27680\n" \
X_"a[98:99], v33 offset:32288\n" \
M_"v[188:191], a[100:103], a[240:243], v[188:191]\n" \
X_"a[100:101], v33 offset:36896\n" \
X_"a[102:103], v33 offset:41504\n" \
M_"v[188:191], a[104:107], a[244:247], v[188:191]\n" \
M_"v[188:191], a[108:111], a[248:251], v[188:191]\n" \
M_"v[184:187], a[104:107], a[228:231], v[184:187]\n" \
X_"a[104:105], v33 offset:46112\n" \
X_"a[106:107], v33 offset:50720\n" \
M_"v[184:187], a[108:111], a[232:235], v[184:187]\n" \
X_"a[108:109], v33 offset:55328\n" \
"s_waitcnt lgkmcnt(11)\n" \
M_"v[184:187], a[112:115], a[160:163], v[184:187]\n" \
B_"v[88:91], v32, s[56:59], s96 offen offset:0 sc1\n" \
M_"v[184:187], a[116:119], a[164:167], v[184:187]\n" \
X_"a[110:111], v33 offset:59936\n" \
M_"v[188:191], a[112:115], a[176:179], v[188:191]\n" \
X_"a[112:113], v10 offset:32\n" \
X_"a[114:115], v10 offset:4640\n" \
M_"v[188:191], a[116:119], a[180:183], v[188:191]\n" \
X_"a[116:117], v34 offset:32\n" \
X_"a[118:119], v34 offset:4640\n" \
M_"v[188:191], a[120:123], a[184:187], v[188:191]\n" \
M_"v[188:191], a[0:3], a[188:191], v[188:191]\n" \
B_"v[92:95], v32, s[56:59], s96 offen offset:1024 sc1\n" \
M_"v[184:187], a[120:123], a[168:171], v[184:187]\n" \
X_"a[120:121], v34 offset:9248\n" \
X_"a[122:123], v34 offset:13856\n" \
M_"v[184:187], a[0:3], a[172:175], v[184:187]\n" \
X_"a[0:1], v34 offset:18464\n" \
X_"a[2:3], v34 offset:23072\n" \
M_"v[184:187], a[4:7], a[192:195], v[184:187]\n" \
M_"v[184:187], a[8:11], a[196:199], v[184:187]\n" \
M_"v[188:191], a[4:7], a[208:211], v[188:191]\n" \
X_"a[4:5], v10 offset:9248\n" \
X_"a[6:7], v10 offset:13856\n" \
M_"v[188:191], a[8:11], a[212:215], v[188:191]\n" \
X_"a[8:9], v33 offset:64\n" \
X_"a[10:11], v33 offset:4672\n" \
M_"v[188:191], a[12:15], a[216:219], v[188:191]\n" \
B_"v[96:99], v20, s[56:59], s97 offen offset:0 sc1\n" \
M_"v[188:191], a[16:19], a[220:223], v[188:191]\n" \
M_"v[184:187], a[12:15], a[200:203], v[184:187]\n" \
X_"a[12:13], v33 offset:9280\n" \
X_"a[14:15], v33 offset:13888\n" \
M_"v[184:187], a[16:19], a[204:207], v[184:187]\n" \
"buffer_load_dword v37, v40, s[56:59], s74 offen sc1\n" \
M_"v[192:195], a[20:23], a[124:127], v[192:195]\n" \
X_"a[16:17], v33 offset:18496\n" \
"s_waitcnt lgkmcnt(13)\n" \
M_"v[192:195], a[24:27], a[224:227], v[192:195]\n" \
X_"a[18:19], v33 offset:23104\n" \
M_"v[196:199], a[20:23], a[236:239], v[196:199]\n" \
X_"a[20:21], v33 offset:27712\n" \
C_"v184, v184, v185\n" \
C_"v185, v186, v187\n" \
C_"v186, v188, v189\n" \
C_"v187, v190, v191\n" \
"buffer_store_dwordx4 v[184:187], v20, s[56:59], s92 offen offset:1024 sc1\n" \
"s_waitcnt vmcnt(18)\n" \
"s_mov_b64 exec, 1\n" \
"buffer_store_dword v39, off, s[56:59], s70 offset:20 sc1\n" \
"s_mov_b64 exec, -1\n" \
"s_add_u32 s92, s92, 0x3800\n" \
M_"v[196:199], a[24:27], a[240:243], v[196:199]\n" \
X_"a[22:23], v33 offset:32320\n" \
X_"a[24:25], v33 offset:36928\n" \
M_"v[196:199], a[28:31], a[244:247], v[196:199]\n" \
X_"a[26:27], v33 offset:41536\n" \
M_"v[196:199], a[96:99], a[248:251], v[196:199]\n" \
M_"v[192:195], a[28:31], a[228:231], v[192:195]\n" \
X_"a[28:29], v33 offset:46144\n" \
X_"a[30:31], v33 offset:50752\n" \
M_"v[192:195], a[96:99], a[232:235], v[192:195]\n" \
B_"v[100:103], v20, s[56:59], s97 offen offset:1024 sc1\n" \
M_"v[192:195], a[100:103], a[160:163], v[192:195]\n" \
X_"a[96:97], v33 offset:55360\n" \
X_"a[98:99], v33 offset:59968\n" \
M_"v[192:195], a[104:107], a[164:167], v[192:195]\n" \
M_"v[196:199], a[100:103], a[176:179], v[196:199]\n" \
X_"a[100:101], v10 offset:64\n" \
X_"a[102:103], v10 offset:4672\n" \
M_"v[196:199], a[104:107], a[180:183], v[196:199]\n" \
X_"a[104:105], v34 offset:64\n" \
X_"a[106:107], v34 offset:4672\n" \
M_"v[196:199], a[108:111], a[184:187], v[196:199]\n" \
B_"v[104:107], v32, s[56:59], s97 offen offset:0 sc1\n" \
M_"v[196:199], a[112:115], a[188:191], v[196:199]\n" \
M_"v[192:195], a[108:111], a[168:171], v[192:195]\n" \
X_"a[108:109], v34 offset:9280\n" \
X_"a[110:111], v34 offset:13888\n" \
M_"v[192:195], a[112:115], a[172:175], v[192:195]\n" \
X_"a[112:113], v34 offset:18496\n" \
"s_waitcnt lgkmcnt(14)\n" \
M_"v[192:195], a[116:119], a[192:195], v[192:195]\n" \
X_"a[114:115], v34 offset:23104\n" \
M_"v[192:195], a[120:123], a[196:199], v[192:195]\n" \
B_"v[108:111], v32, s[56:59], s97 offen offset:1024 sc1\n" \
M_"v[196:199], a[116:119], a[208:211], v[196:199]\n" \
X_"a[116:117], v10 offset:9280\n" \
X_"a[118:119], v10 offset:13888\n" \
M_"v[196:199], a[120:123], a[212:215], v[196:199]\n" \
X_"a[120:121], v33 offset:96\n" \
X_"a[122:123], v33 offset:4704\n" \
"s_waitcnt vmcnt(19)\n" \
"s_mov_b64 exec, 1\n" \
"buffer_store_dword v39, off, s[56:59], s71 offset:24 sc1\n" \
"s_mov_b64 exec, -1\n" \
M_"v[196:199], a[0:3], a[216:219], v[196:199]\n" \
M_"v[196:199], a[4:7], a[220:223], v[196:199]\n" \
M_"v[192:195], a[0:3], a[200:203], v[192:195]\n" \
X_"a[0:1], v33 offset:9312\n" \
X_"a[2:3], v33 offset:13920\n" \
M_"v[192:195], a[4:7], a[204:207], v[192:195]\n" \
X_"a[4:5], v33 offset:18528\n" \
X_"a[6:7], v33 offset:23136\n" \
M_"v[200:203], a[8:11], a[124:127], v[200:203]\n" \
M_"v[200:203], a[12:15], a[224:227], v[200:203]\n" \
M_"v[204:207], a[8:11], a[236:239], v[204:207]\n" \
X_"a[8:9], v33 offset:27744\n" \
X_"a[10:11], v33 offset:32352\n" \
M_"v[204:207], a[12:15], a[240:243], v[204:207]\n" \
C_"v192, v192, v193\n" \
C_"v193, v194, v195\n" \
C_"v194, v196, v197\n" \
C_"v195, v198, v199\n" \
"buffer_store_dwordx4 v[192:195], v20, s[56:59], s66 offen offset:1024 sc1\n" \
M_"v[204:207], a[16:19], a[244:247], v[204:207]\n" \
X_"a[12:13], v33 offset:36960\n" \
"s_waitcnt lgkmcnt(11)\n" \
M_"v[204:207], a[20:23], a[248:251], v[204:207]\n" \
X_"a[14:15], v33 offset:41568\n" \
M_"v[200:203], a[16:19], a[228:231], v[200:203]\n" \
X_"a[16:17], v33 offset:46176\n" \
X_"a[18:19], v33 offset:50784\n" \
M_"v[200:203], a[20:23], a[232:235], v[200:203]\n" \
X_"a[20:21], v33 offset:55392\n" \
X_"a[22:23], v33 offset:60000\n" \
M_"v[200:203], a[24:27], a[160:163], v[200:203]\n" \
M_"v[200:203], a[28:31], a[164:167], v[200:203]\n" \
M_"v[204:207], a[24:27], a[176:179], v[204:207]\n" \
X_"a[24:25], v10 offset:96\n" \
X_"a[26:27], v10 offset:4704\n" \
M_"v[204:207], a[28:31], a[180:183], v[204:207]\n" \
X_"a[28:29], v34 offset:96\n" \
X_"a[30:31], v34 offset:4704\n" \
M_"v[204:207], a[96:99], a[184:187], v[204:207]\n" \
M_"v[204:207], a[100:103], a[188:191], v[204:207]\n" \
M_"v[200:203], a[96:99], a[168:171], v[200:203]\n" \
X_"a[96:97], v34 offset:9312\n" \
X_"a[98:99], v34 offset:13920\n" \
M_"v[200:203], a[100:103], a[172:175], v[200:203]\n" \
X_"a[100:101], v34 offset:18528\n" \
X_"a[102:103], v34 offset:23136\n" \
M_"v[200:203], a[104:107], a[192:195], v[200:203]\n" \
M_"v[200:203], a[108:111], a[196:199], v[200:203]\n" \
M_"v[204:207], a[104:107], a[208:211], v[204:207]\n" \
X_"a[104:105], v10 offset:9312\n" \
X_"a[106:107], v10 offset:13920\n" \
M_"v[204:207], a[108:111], a[212:215], v[204:207]\n" \
X_"a[108:109], v33 offset:128\n" \
X_"a[110:111], v33 offset:4736\n" \
M_"v[204:207], a[112:115], a[216:219], v[204:207]\n" \
"s_waitcnt lgkmcnt(15)\n" \
M_"v[204:207], a[116:119], a[220:223], v[204:207]\n" \
M_"v[200:203], a[112:115], a[200:203], v[200:203]\n" \
X_"a[112:113], v33 offset:9344\n" \
X_"a[114:115], v33 offset:13952\n" \
M_"v[200:203], a[116:119], a[204:207], v[200:203]\n" \
X_"a[116:117], v33 offset:18560\n" \
X_"a[118:119], v33 offset:23168\n" \
M_"v[208:211], a[120:123], a[124:127], v[208:211]\n" \
M_"v[208:211], a[0:3], a[224:227], v[208:211]\n" \
M_"v[212:215], a[120:123], a[236:239], v[212:215]\n" \
X_"a[120:121], v33 offset:27776\n" \
X_"a[122:123], v33 offset:32384\n" \
M_"v[212:215], a[0:3], a[240:243], v[212:215]\n" \
C_"v200, v200, v201\n" \
C_"v201, v202, v203\n" \
C_"v202, v204, v205\n" \
C_"v203, v206, v207\n" \
"buffer_store_dwordx4 v[200:203], v20, s[56:59], s67 offen offset:1024 sc1\n" \
M_"v[212:215], a[4:7], a[244:247], v[212:215]\n" \
X_"a[0:1], v33 offset:36992\n" \
X_"a[2:3], v33 offset:41600\n" \
M_"v[212:215], a[8:11], a[248:251], v[212:215]\n" \
M_"v[208:211], a[4:7], a[228:231], v[208:211]\n" \
X_"a[4:5], v33 offset:46208\n" \
X_"a[6:7], v33 offset:50816\n" \
M_"v[208:211], a[8:11], a[232:235], v[208:211]\n" \
X_"a[8:9], v33 offset:55424\n" \
X_"a[10:11], v33 offset:60032\n" \
M_"v[208:211], a[12:15], a[160:163], v[208:211]\n" \
"s_waitcnt lgkmcnt(13)\n" \
M_"v[208:211], a[16:19], a[164:167], v[208:211]\n" \
M_"v[212:215], a[12:15], a[176:179], v[212:215]\n" \
"s_waitcnt vmcnt(8)\n" \
"v_cmp_eq_u32_e64 s[98:99], s62, v37\n" \
"s_mov_b64 exec, 3\n" \
"buffer_store_dword v39, v41, s[56:59], s73 offen sc1\n" \
"s_mov_b64 exec, -1\n" \
"s_andn2_b32 s75, 0x7f, s98\n" \
"s_mov_b32 s20, s75\n" \
"s_bitcmp1_b32 s20, 0\n" \
"s_cselect_b32 s93, 0x40000000, s12\n" \
"s_bitcmp1_b32 s20, 1\n" \
"s_cselect_b32 s94, 0x40000000, s12\n" \
"s_bitcmp1_b32 s20, 2\n" \
"s_cselect_b32 s95, 0x40000000, s13\n" \
"s_bitcmp1_b32 s20, 3\n" \
"s_cselect_b32 s96, 0x40000000, s13\n" \
"s_bitcmp1_b32 s20, 4\n" \
"s_cselect_b32 s97, 0x40000000, s14\n" \
"s_bitcmp1_b32 s20, 5\n" \
"s_cselect_b32 s64, 0x40000000, s14\n" \
"s_bitcmp1_b32 s20, 6\n" \
"s_cselect_b32 s65, 0x40000000, s15\n" \
"s_add_u32 s66, s66, 0x3000\n" \
"s_add_u32 s67, s67, 0x2800\n" \
M_"v[212:215], a[16:19], a[180:183], v[212:215]\n" \
B_"v[116:119], v20, s[56:59], s93 offen offset:0 sc1\n" \
M_"v[212:215], a[20:23], a[184:187], v[212:215]\n" \
X_"a[12:13], v10 offset:128\n" \
X_"a[14:15], v10 offset:4736\n" \
M_"v[212:215], a[24:27], a[188:191], v[212:215]\n" \
X_"a[16:17], v34 offset:128\n" \
X_"a[18:19], v34 offset:4736\n" \
M_"v[208:211], a[20:23], a[168:171], v[208:211]\n" \
X_"a[20:21], v34 offset:9344\n" \
X_"a[22:23], v34 offset:13952\n" \
M_"v[208:211], a[24:27], a[172:175], v[208:211]\n" \
X_"a[24:25], v34 offset:18560\n" \
X_"a[26:27], v34 offset:23168\n" \
M_"v[208:211], a[28:31], a[192:195], v[208:211]\n" \
M_"v[208:211], a[96:99], a[196:199], v[208:211]\n" \
M_"v[212:215], a[28:31], a[208:211], v[212:215]\n" \
X_"a[28:29], v10 offset:9344\n" \
X_"a[30:31], v10 offset:13952\n" \
M_"v[212:215], a[96:99], a[212:215], v[212:215]\n" \
X_"a[96:97], v33 offset:160\n" \
X_"a[98:99], v33 offset:4768\n" \
M_"v[212:215], a[100:103], a[216:219], v[212:215]\n" \
M_"v[212:215], a[104:107], a[220:223], v[212:215]\n" \
M_"v[208:211], a[100:103], a[200:203], v[208:211]\n" \
X_"a[100:101], v33 offset:9376\n" \
X_"a[102:103], v33 offset:13984\n" \
M_"v[208:211], a[104:107], a[204:207], v[208:211]\n" \
X_"a[104:105], v33 offset:18592\n" \
"s_waitcnt lgkmcnt(15)\n" \
M_"v[216:219], a[108:111], a[124:127], v[216:219]\n" \
X_"a[106:107], v33 offset:23200\n" \
M_"v[216:219], a[112:115], a[224:227], v[216:219]\n" \
B_"v[124:127], v20, s[56:59], s94 offen offset:2048 sc1\n" \
M_"v[220:223], a[108:111], a[236:239], v[220:223]\n" \
X_"a[108:109], v33 offset:27808\n" \
C_"v208, v208, v209\n" \
C_"v209, v210, v211\n" \
C_"v210, v212, v213\n" \
C_"v211, v214, v215\n" \
M_"v[220:223], a[112:115], a[240:243], v[220:223]\n" \
"buffer_store_dwordx4 v[208:211], v20, s[56:59], s68 offen offset:1024 sc1\n" \
"s_add_u32 s68, s68, 0x2000\n" \
M_"v[220:223], a[116:119], a[244:247], v[220:223]\n" \
X_"a[110:111], v33 offset:32416\n" \
X_"a[112:113], v33 offset:37024\n" \
M_"v[220:223], a[120:123], a[248:251], v[220:223]\n" \
X_"a[114:115], v33 offset:41632\n" \
M_"v[216:219], a[116:119], a[228:231], v[216:219]\n" \
X_"a[116:117], v33 offset:46240\n" \
X_"a[118:119], v33 offset:50848\n" \
M_"v[216:219], a[120:123], a[232:235], v[216:219]\n" \
X_"a[120:121], v33 offset:55456\n" \
X_"a[122:123], v33 offset:60064\n" \
M_"v[216:219], a[0:3], a[160:163], v[216:219]\n" \
M_"v[216:219], a[4:7], a[164:167], v[216:219]\n" \
M_"v[220:223], a[0:3], a[176:179], v[220:223]\n" \
X_"a[0:1], v10 offset:160\n" \
X_"a[2:3], v10 offset:4768\n" \
M_"v[220:223], a[4:7], a[180:183], v[220:223]\n" \
X_"a[4:5], v34 offset:160\n" \
X_"a[6:7], v34 offset:4768\n" \
M_"v[220:223], a[8:11], a[184:187], v[220:223]\n" \
"s_waitcnt lgkmcnt(13)\n" \
M_"v[220:223], a[12:15], a[188:191], v[220:223]\n" \
"s_waitcnt vmcnt(11)\n" \
"s_mov_b64 exec, 1\n" \
"buffer_store_dword v39, off, s[56:59], s92 offset:32 sc1\n" \
"s_mov_b64 exec, -1\n" \
M_"v[216:219], a[8:11], a[168:171], v[216:219]\n" \
X_"a[8:9], v34 offset:9376\n" \
X_"a[10:11], v34 offset:13984\n" \
M_"v[216:219], a[12:15], a[172:175], v[216:219]\n" \
X_"a[12:13], v34 offset:18592\n" \
X_"a[14:15], v34 offset:23200\n" \
M_"v[216:219], a[16:19], a[192:195], v[216:219]\n" \
M_"v[216:219], a[20:23], a[196:199], v[216:219]\n" \
B_"v[132:135], v20, s[56:59], s95 offen offset:0 sc1\n" \
M_"v[220:223], a[16:19], a[208:211], v[220:223]\n" \
X_"a[16:17], v10 offset:9376\n" \
X_"a[18:19], v10 offset:13984\n" \
M_"v[220:223], a[20:23], a[212:215], v[220:223]\n" \
X_"a[20:21], v33 offset:192\n" \
X_"a[22:23], v33 offset:4800\n" \
M_"v[220:223], a[24:27], a[216:219], v[220:223]\n" \
M_"v[220:223], a[28:31], a[220:223], v[220:223]\n" \
M_"v[216:219], a[24:27], a[200:203], v[216:219]\n" \
X_"a[24:25], v33 offset:9408\n" \
X_"a[26:27], v33 offset:14016\n" \
M_"v[216:219], a[28:31], a[204:207], v[216:219]\n" \
"buffer_load_dword v37, v40, s[56:59], s74 offen sc1\n" \
M_"v[224:227], a[96:99], a[124:127], v[224:227]\n" \
X_"a[28:29], v33 offset:18624\n" \
X_"a[30:31], v33 offset:23232\n" \
M_"v[224:227], a[100:103], a[224:227], v[224:227]\n" \
M_"v[228:231], a[96:99], a[236:239], v[228:231]\n" \
X_"a[96:97], v33 offset:27840\n" \
X_"a[98:99], v33 offset:32448\n" \
C_"v216, v216, v217\n" \
C_"v217, v218, v219\n" \
C_"v218, v220, v221\n" \
C_"v219, v222, v223\n" \
"buffer_store_dwordx4 v[216:219], v20, s[56:59], s69 offen offset:1024 sc1\n" \
"s_add_u32 s69, s69, 0x1800\n" \
M_"v[228:231], a[100:103], a[240:243], v[228:231]\n" \
X_"a[100:101], v33 offset:37056\n" \
"s_waitcnt lgkmcnt(12)\n" \
M_"v[228:231], a[104:107], a[244:247], v[228:231]\n" \
X_"a[102:103], v33 offset:41664\n" \
M_"v[228:231], a[108:111], a[248:251], v[228:231]\n" \
M_"v[224:227], a[104:107], a[228:231], v[224:227]\n" \
B_"v[140:143], v20, s[56:59], s96 offen offset:2048 sc1\n" \
M_"v[224:227], a[108:111], a[232:235], v[224:227]\n" \
X_"a[104:105], v33 offset:46272\n" \
X_"a[106:107], v33 offset:50880\n" \
M_"v[224:227], a[112:115], a[160:163], v[224:227]\n" \
X_"a[108:109], v33 offset:55488\n" \
X_"a[110:111], v33 offset:60096\n" \
M_"v[224:227], a[116:119], a[164:167], v[224:227]\n" \
M_"v[228:231], a[112:115], a[176:179], v[228:231]\n" \
X_"a[112:113], v10 offset:192\n" \
X_"a[114:115], v10 offset:4800\n" \
M_"v[228:231], a[116:119], a[180:183], v[228:231]\n" \
X_"a[116:117], v34 offset:192\n" \
X_"a[118:119], v34 offset:4800\n" \
M_"v[228:231], a[120:123], a[184:187], v[228:231]\n" \
M_"v[228:231], a[0:3], a[188:191], v[228:231]\n" \
M_"v[224:227], a[120:123], a[168:171], v[224:227]\n" \
X_"a[120:121], v34 offset:9408\n" \
X_"a[122:123], v34 offset:14016\n" \
M_"v[224:227], a[0:3], a[172:175], v[224:227]\n" \
X_"a[0:1], v34 offset:18624\n" \
X_"a[2:3], v34 offset:23232\n" \
"s_waitcnt vmcnt(10)\n" \
"s_mov_b64 exec, 1\n" \
"buffer_store_dword v39, off, s[56:59], s66 offset:36 sc1\n" \
"s_mov_b64 exec, -1\n" \
M_"v[224:227], a[4:7], a[192:195], v[224:227]\n" \
M_"v[224:227], a[8:11], a[196:199], v[224:227]\n" \
M_"v[228:231], a[4:7], a[208:211], v[228:231]\n" \
X_"a[4:5], v10 offset:9408\n" \
X_"a[6:7], v10 offset:14016\n" \
M_"v[228:231], a[8:11], a[212:215], v[228:231]\n" \
X_"a[8:9], v33 offset:224\n" \
"s_waitcnt lgkmcnt(15)\n" \
M_"v[228:231], a[12:15], a[216:219], v[228:231]\n" \
B_"v[148:151], v20, s[56:59], s97 offen offset:0 sc1\n" \
M_"v[228:231], a[16:19], a[220:223], v[228:231]\n" \
X_"a[10:11], v33 offset:4832\n" \
M_"v[224:227], a[12:15], a[200:203], v[224:227]\n" \
X_"a[12:13], v33 offset:9440\n" \
X_"a[14:15], v33 offset:14048\n" \
M_"v[224:227], a[16:19], a[204:207], v[224:227]\n" \
X_"a[16:17], v33 offset:18656\n" \
X_"a[18:19], v33 offset:23264\n" \
M_"v[232:235], a[20:23], a[124:127], v[232:235]\n" \
M_"v[232:235], a[24:27], a[224:227], v[232:235]\n" \
M_"v[236:239], a[20:23], a[236:239], v[236:239]\n" \
X_"a[20:21], v33 offset:27872\n" \
X_"a[22:23], v33 offset:32480\n" \
M_"v[236:239], a[24:27], a[240:243], v[236:239]\n" \
C_"v224, v224, v225\n" \
C_"v225, v226, v227\n" \
C_"v226, v228, v229\n" \
C_"v227, v230, v231\n" \
M_"v[236:239], a[28:31], a[244:247], v[236:239]\n" \
X_"a[24:25], v33 offset:37088\n" \
X_"a[26:27], v33 offset:41696\n" \
M_"v[236:239], a[96:99], a[248:251], v[236:239]\n" \
M_"v[232:235], a[28:31], a[228:231], v[232:235]\n" \
X_"a[28:29], v33 offset:46304\n" \
X_"a[30:31], v33 offset:50912\n" \
M_"v[232:235], a[96:99], a[232:235], v[232:235]\n" \
X_"a[96:97], v33 offset:55520\n" \
X_"a[98:99], v33 offset:60128\n" \
M_"v[232:235], a[100:103], a[160:163], v[232:235]\n" \
"s_waitcnt lgkmcnt(14)\n" \
M_"v[232:235], a[104:107], a[164:167], v[232:235]\n" \
B_"v[156:159], v20, s[56:59], s64 offen offset:2048 sc1\n" \
"v_readlane_b32 s64, v11, 54\n" \
M_"v[236:239], a[100:103], a[176:179], v[236:239]\n" \
X_"a[100:101], v10 offset:224\n" \
X_"a[102:103], v10 offset:4832\n" \
M_"v[236:239], a[104:107], a[180:183], v[236:239]\n" \
X_"a[104:105], v34 offset:224\n" \
X_"a[106:107], v34 offset:4832\n" \
"buffer_store_dwordx4 v[224:227], v20, s[56:59], s64 offen offset:1024 sc1\n" \
M_"v[236:239], a[108:111], a[184:187], v[236:239]\n" \
M_"v[236:239], a[112:115], a[188:191], v[236:239]\n" \
M_"v[232:235], a[108:111], a[168:171], v[232:235]\n" \
X_"a[108:109], v34 offset:9440\n" \
X_"a[110:111], v34 offset:14048\n" \
"s_waitcnt vmcnt(13)\n" \
"s_mov_b64 exec, 1\n" \
"buffer_store_dword v39, off, s[56:59], s67 offset:40 sc1\n" \
"s_mov_b64 exec, -1\n" \
M_"v[232:235], a[112:115], a[172:175], v[232:235]\n" \
X_"a[112:113], v34 offset:18656\n" \
X_"a[114:115], v34 offset:23264\n" \
M_"v[232:235], a[116:119], a[192:195], v[232:235]\n" \
M_"v[232:235], a[120:123], a[196:199], v[232:235]\n" \
M_"v[236:239], a[116:119], a[208:211], v[236:239]\n" \
X_"a[116:117], v10 offset:9440\n" \
X_"a[118:119], v10 offset:14048\n" \
M_"v[236:239], a[120:123], a[212:215], v[236:239]\n" \
X_"a[120:121], v36 offset:0\n" \
X_"a[122:123], v36 offset:1024\n" \
M_"v[236:239], a[0:3], a[216:219], v[236:239]\n" \
M_"v[236:239], a[4:7], a[220:223], v[236:239]\n" \
M_"v[232:235], a[0:3], a[200:203], v[232:235]\n" \
X_"a[0:1], v36 offset:2048\n" \
X_"a[2:3], v36 offset:3072\n" \
M_"v[232:235], a[4:7], a[204:207], v[232:235]\n" \
X_"a[4:5], v36 offset:32\n" \
B_"v[164:167], v20, s[56:59], s65 offen offset:0 sc1\n" \
"v_readlane_b32 s65, v11, 55\n" \
"s_waitcnt lgkmcnt(11)\n" \
M_"v[240:243], a[8:11], a[124:127], v[240:243]\n" \
X_"a[6:7], v36 offset:1056\n" \
M_"v[240:243], a[12:15], a[224:227], v[240:243]\n" \
M_"v[244:247], a[8:11], a[236:239], v[244:247]\n" \
C_"v232, v232, v233\n" \
C_"v233, v234, v235\n" \
C_"v234, v236, v237\n" \
C_"v235, v238, v239\n" \
"buffer_store_dwordx4 v[232:235], v20, s[56:59], s65 offen offset:1024 sc1\n" \
M_"v[244:247], a[12:15], a[240:243], v[244:247]\n" \
X_"a[8:9], v36 offset:2080\n" \
X_"a[10:11], v36 offset:3104\n" \
M_"v[244:247], a[16:19], a[244:247], v[244:247]\n" \
M_"v[244:247], a[20:23], a[248:251], v[244:247]\n" \
M_"v[240:243], a[16:19], a[228:231], v[240:243]\n" \
M_"v[240:243], a[20:23], a[232:235], v[240:243]\n" \
M_"v[168:171], a[24:27], a[128:131], v[168:171]\n" \
M_"v[168:171], a[28:31], a[132:135], v[168:171]\n" \
M_"v[172:175], a[24:27], a[144:147], v[172:175]\n" \
"s_waitcnt vmcnt(9)\n" \
"v_cmp_eq_u32_e64 s[98:99], s62, v37\n" \
"buffer_load_dword v37, v40, s[56:59], s74 offen sc1\n" \
"s_mov_b64 exec, 1\n" \
"buffer_store_dword v39, off, s[56:59], s68 offset:44 sc1\n" \
"s_mov_b64 exec, -1\n" \
"s_or_b32 s75, 0x300, s100\n" \
"s_andn2_b32 s75, s75, s98\n" \
"s_mov_b32 s21, s75\n" \
"s_bitcmp1_b32 s21, 16\n" \
"s_cselect_b32 s70, 0x40000000, s16\n" \
"s_bitcmp1_b32 s21, 17\n" \
"s_cselect_b32 s71, 0x40000000, s16\n" \
"s_bitcmp1_b32 s21, 18\n" \
"s_cselect_b32 s72, 0x40000000, s17\n" \
"s_bitcmp1_b32 s21, 19\n" \
"s_cselect_b32 s73, 0x40000000, s17\n" \
"s_bitcmp1_b32 s21, 20\n" \
"s_cselect_b32 s92, 0x40000000, s18\n" \
"s_bitcmp1_b32 s21, 21\n" \
"s_cselect_b32 s93, 0x40000000, s18\n" \
"s_bitcmp1_b32 s21, 22\n" \
"s_cselect_b32 s94, 0x40000000, s19\n" \
"s_bitcmp1_b32 s21, 23\n" \
"s_cselect_b32 s95, 0x40000000, s19\n" \
"s_bitcmp1_b32 s21, 8\n" \
"s_cselect_b32 s96, 0x40000000, s12\n" \
"s_bitcmp1_b32 s21, 9\n" \
"s_cselect_b32 s97, 0x40000000, s12\n" \
"s_add_u32 s64, s64, 0x1000\n" \
"s_add_u32 s65, s65, 0x800\n" \
M_"v[172:175], a[28:31], a[148:151], v[172:175]\n" \
B_"v[112:115], v20, s[56:59], s70 offen offset:0 sc1\n" \
M_"v[240:243], a[24:27], a[160:163], v[240:243]\n" \
M_"v[240:243], a[28:31], a[164:167], v[240:243]\n" \
M_"v[244:247], a[24:27], a[176:179], v[244:247]\n" \
M_"v[244:247], a[28:31], a[180:183], v[244:247]\n" \
B_"v[184:187], v20, s[56:59], s71 offen offset:2048 sc1\n" \
M_"v[244:247], a[96:99], a[184:187], v[244:247]\n" \
"v_readlane_b32 s71, v11, 57\n" \
M_"v[244:247], a[100:103], a[188:191], v[244:247]\n" \
M_"v[240:243], a[96:99], a[168:171], v[240:243]\n" \
M_"v[240:243], a[100:103], a[172:175], v[240:243]\n" \
B_"v[120:123], v20, s[56:59], s72 offen offset:0 sc1\n" \
M_"v[172:175], a[96:99], a[152:155], v[172:175]\n" \
"v_readlane_b32 s72, v11, 48\n" \
M_"v[172:175], a[100:103], a[156:159], v[172:175]\n" \
M_"v[168:171], a[96:99], a[136:139], v[168:171]\n" \
M_"v[168:171], a[100:103], a[140:143], v[168:171]\n" \
B_"v[192:195], v20, s[56:59], s73 offen offset:2048 sc1\n" \
M_"v[168:171], a[104:107], a[32:35], v[168:171]\n" \
"v_readlane_b32 s73, v11, 46\n" \
"s_waitcnt lgkmcnt(0)\n" \
M_"v[168:171], a[108:111], a[36:39], v[168:171]\n" \
M_"v[172:175], a[104:107], a[48:51], v[172:175]\n" \
M_"v[172:175], a[108:111], a[52:55], v[172:175]\n" \
B_"v[128:131], v20, s[56:59], s92 offen offset:0 sc1\n" \
M_"v[240:243], a[104:107], a[192:195], v[240:243]\n" \
"v_readlane_b32 s92, v11, 47\n" \
M_"v[240:243], a[108:111], a[196:199], v[240:243]\n" \
M_"v[244:247], a[104:107], a[208:211], v[244:247]\n" \
M_"v[244:247], a[108:111], a[212:215], v[244:247]\n" \
B_"v[200:203], v20, s[56:59], s93 offen offset:2048 sc1\n" \
M_"v[244:247], a[112:115], a[216:219], v[244:247]\n" \
M_"v[244:247], a[116:119], a[220:223], v[244:247]\n" \
M_"v[240:243], a[112:115], a[200:203], v[240:243]\n" \
M_"v[240:243], a[116:119], a[204:207], v[240:243]\n" \
".fill (L_ab + 45056 - .) / 4, 4, 0xBF800000\n" \
"L_ap11:\n" \
"s_cbranch_execz L_ap0\n" \
B_"v[176:179], v20, s[56:59], s94 offen offset:0 sc1\n" \
"s_waitcnt vmcnt(17)\n" \
"s_mov_b64 exec, 1\n" \
"buffer_store_dword v39, off, s[56:59], s69 offset:48 sc1\n" \
"s_mov_b64 exec, -1\n" \
M_"v[172:175], a[112:115], a[56:59], v[172:175]\n" \
M_"v[172:175], a[116:119], a[60:63], v[172:175]\n" \
M_"v[168:171], a[112:115], a[40:43], v[168:171]\n" \
M_"v[168:171], a[116:119], a[44:47], v[168:171]\n" \
B_"v[248:251], v20, s[56:59], s95 offen offset:2048 sc1\n" \
M_"v[168:171], a[120:123], v[64:67], v[168:171]\n" \
M_"v[172:175], a[120:123], v[72:75], v[172:175]\n" \
M_"v[240:243], a[120:123], v[80:83], v[240:243]\n" \
M_"v[244:247], a[120:123], v[88:91], v[244:247]\n" \
B_"v[188:191], v20, s[56:59], s96 offen offset:1024 sc1\n" \
M_"v[168:171], a[0:3], v[68:71], v[168:171]\n" \
M_"v[172:175], a[0:3], v[76:79], v[172:175]\n" \
M_"v[240:243], a[0:3], v[84:87], v[240:243]\n" \
M_"v[244:247], a[0:3], v[92:95], v[244:247]\n" \
B_"v[196:199], v20, s[56:59], s97 offen offset:3072 sc1\n" \
M_"v[56:59], a[4:7], v[96:99], 0\n" \
M_"v[60:63], a[4:7], v[104:107], 0\n" \
M_"v[56:59], a[8:11], v[100:103], v[56:59]\n" \
M_"v[60:63], a[8:11], v[108:111], v[60:63]\n" \
"s_waitcnt vmcnt(16)\n" \
"s_mov_b64 exec, 1\n" \
"buffer_store_dword v39, off, s[56:59], s64 offset:52 sc1\n" \
"s_mov_b64 exec, -1\n" \
"s_waitcnt vmcnt(14)\n" \
"s_mov_b64 exec, 1\n" \
"buffer_store_dword v39, off, s[56:59], s65 offset:56 sc1\n" \
"s_mov_b64 exec, -1\n" \
"s_cmp_lg_u32 s20, 0\n" \
"s_cbranch_scc1 L_sna_%=\n" \
"L_rna_%=:\n" \
"s_cmp_lg_u32 s21, 0\n" \
"s_cbranch_scc1 L_snx_%=\n" \
"L_rnx_%=:\n" \
"s_waitcnt vmcnt(14)\n" \
"v_cmp_eq_u32_e64 s[98:99], s62, v37\n" \
M_"v[168:171], v[50:53], v[116:119], v[168:171]\n" \
"s_andn2_b32 s75, 0x400, s98\n" \
"s_cbranch_scc1 L_sx0_%=\n" \
"L_rx0_%=:\n" \
"s_add_u32 s66, s63, 0x1000\n" \
M_"v[172:175], v[48:51], v[116:119], v[172:175]\n" \
B_"v[204:207], v20, s[56:59], s66 offen offset:1024 sc1\n" \
"s_andn2_b32 s75, 0x800, s98\n" \
"s_cbranch_scc1 L_sx1_%=\n" \
"L_rx1_%=:\n" \
"s_add_u32 s67, s63, 0x1800\n" \
M_"v[168:171], v[50:53], v[124:127], v[168:171]\n" \
M_"v[172:175], v[48:51], v[124:127], v[172:175]\n" \
M_"v[168:171], v[50:53], v[132:135], v[168:171]\n" \
B_"v[212:215], v20, s[56:59], s67 offen offset:1024 sc1\n" \
"s_andn2_b32 s75, 0x1000, s98\n" \
"s_cbranch_scc1 L_sx2_%=\n" \
"L_rx2_%=:\n" \
"s_add_u32 s68, s63, 0x2000\n" \
M_"v[172:175], v[48:51], v[132:135], v[172:175]\n" \
M_"v[168:171], v[50:53], v[140:143], v[168:171]\n" \
M_"v[172:175], v[48:51], v[140:143], v[172:175]\n" \
B_"v[220:223], v20, s[56:59], s68 offen offset:1024 sc1\n" \
"s_andn2_b32 s75, 0x2000, s98\n" \
"s_cbranch_scc1 L_sx3_%=\n" \
"L_rx3_%=:\n" \
"s_add_u32 s69, s63, 0x2800\n" \
M_"v[168:171], v[50:53], v[148:151], v[168:171]\n" \
M_"v[172:175], v[48:51], v[148:151], v[172:175]\n" \
M_"v[168:171], v[50:53], v[156:159], v[168:171]\n" \
B_"v[228:231], v20, s[56:59], s69 offen offset:1024 sc1\n" \
"s_andn2_b32 s75, 0x4000, s98\n" \
"s_cbranch_scc1 L_sx4_%=\n" \
"L_rx4_%=:\n" \
"s_add_u32 s70, s63, 0x3000\n" \
M_"v[172:175], v[48:51], v[156:159], v[172:175]\n" \
M_"v[168:171], v[50:53], v[164:167], v[168:171]\n" \
M_"v[172:175], v[48:51], v[164:167], v[172:175]\n" \
B_"v[236:239], v20, s[56:59], s70 offen offset:1024 sc1\n" \
"s_waitcnt vmcnt(17)\n" \
M_"v[56:59], v[50:53], v[112:115], v[56:59]\n" \
M_"v[60:63], v[48:51], v[112:115], v[60:63]\n" \
"s_waitcnt vmcnt(16)\n" \
M_"v[56:59], v[50:53], v[184:187], v[56:59]\n" \
M_"v[60:63], v[48:51], v[184:187], v[60:63]\n" \
C_"v168, v168, v169\n" \
"s_waitcnt vmcnt(15)\n" \
M_"v[56:59], v[50:53], v[120:123], v[56:59]\n" \
C_"v172, v172, v173\n" \
C_"v169, v170, v171\n" \
M_"v[60:63], v[48:51], v[120:123], v[60:63]\n" \
C_"v173, v174, v175\n" \
"s_waitcnt vmcnt(14)\n" \
M_"v[56:59], v[50:53], v[192:195], v[56:59]\n" \
"buffer_store_dwordx2 v[168:169], v42, s[88:91], s73 offen sc1\n" \
M_"v[60:63], v[48:51], v[192:195], v[60:63]\n" \
"s_waitcnt vmcnt(14)\n" \
M_"v[56:59], v[50:53], v[128:131], v[56:59]\n" \
"buffer_store_dwordx2 v[172:173], v43, s[88:91], s73 offen sc1\n" \
M_"v[60:63], v[48:51], v[128:131], v[60:63]\n" \
"s_waitcnt vmcnt(14)\n" \
M_"v[56:59], v[50:53], v[200:203], v[56:59]\n" \
M_"v[60:63], v[48:51], v[200:203], v[60:63]\n" \
"s_waitcnt vmcnt(13)\n" \
M_"v[56:59], v[50:53], v[176:179], v[56:59]\n" \
M_"v[60:63], v[48:51], v[176:179], v[60:63]\n" \
"s_waitcnt vmcnt(11)\n" \
M_"v[56:59], v[50:53], v[248:251], v[56:59]\n" \
M_"v[60:63], v[48:51], v[248:251], v[60:63]\n" \
"s_waitcnt vmcnt(10)\n" \
M_"v[240:243], v[50:53], v[188:191], v[240:243]\n" \
M_"v[244:247], v[48:51], v[188:191], v[244:247]\n" \
"s_waitcnt vmcnt(9)\n" \
M_"v[240:243], v[50:53], v[196:199], v[240:243]\n" \
M_"v[244:247], v[48:51], v[196:199], v[244:247]\n" \
"s_mov_b64 exec, 1\n" \
"buffer_store_dword v46, off, s[56:59], s71 offset:0 sc1\n" \
"s_mov_b64 exec, -1\n" \
C_"v56, v56, v57\n" \
C_"v60, v60, v61\n" \
C_"v57, v58, v59\n" \
C_"v61, v62, v63\n" \
"buffer_store_dwordx2 v[56:57], v42, s[88:91], s72 offen sc1\n" \
"buffer_store_dwordx2 v[60:61], v43, s[88:91], s72 offen sc1\n" \
"s_waitcnt vmcnt(9)\n" \
M_"v[240:243], v[50:53], v[204:207], v[240:243]\n" \
M_"v[244:247], v[48:51], v[204:207], v[244:247]\n" \
"s_waitcnt vmcnt(8)\n" \
M_"v[240:243], v[50:53], v[212:215], v[240:243]\n" \
M_"v[244:247], v[48:51], v[212:215], v[244:247]\n" \
"s_waitcnt vmcnt(7)\n" \
M_"v[240:243], v[50:53], v[220:223], v[240:243]\n" \
M_"v[244:247], v[48:51], v[220:223], v[244:247]\n" \
"s_waitcnt vmcnt(6)\n" \
M_"v[240:243], v[50:53], v[228:231], v[240:243]\n" \
M_"v[244:247], v[48:51], v[228:231], v[244:247]\n" \
"s_waitcnt vmcnt(5)\n" \
M_"v[240:243], v[50:53], v[236:239], v[240:243]\n" \
M_"v[244:247], v[48:51], v[236:239], v[244:247]\n" \
"s_nop 6\n" \
C_"v240, v240, v241\n" \
C_"v244, v244, v245\n" \
C_"v241, v242, v243\n" \
C_"v245, v246, v247\n" \
"buffer_store_dwordx2 v[240:241], v42, s[88:91], s92 offen sc1\n" \
"buffer_store_dwordx2 v[244:245], v43, s[88:91], s92 offen sc1\n" \
"L_fin_%=:\n"
#define ATT_VCLOB "v21","v22","v23","v24","v25","v26","v27","v28","v29","v32","v33","v34","v35","v36","v37","v38","v39","v40","v41","v42","v43","v44","v45","v46","v47","v48","v49","v50","v51","v52","v53","v54","v55","v56","v57","v58","v59","v60","v61","v62","v63","v64","v65","v66","v67","v68","v69","v70","v71","v72","v73","v74","v75","v76","v77","v78","v79","v80","v81","v82","v83","v84","v85","v86","v87","v88","v89","v90","v91","v92","v93","v94","v95","v96","v97","v98","v99","v100","v101","v102","v103","v104","v105","v106","v107","v108","v109","v110","v111","v112","v113","v114","v115","v116","v117","v118","v119","v120","v121","v122","v123","v124","v125","v126","v127","v128","v129","v130","v131","v132","v133","v134","v135","v136","v137","v138","v139","v140","v141","v142","v143","v144","v145","v146","v147","v148","v149","v150","v151","v152","v153","v154","v155","v156","v157","v158","v159","v160","v161","v162","v163","v164","v165","v166","v167","v168","v169","v170","v171","v172","v173","v174","v175","v176","v177","v178","v179","v180","v181","v182","v183","v184","v185","v186","v187","v188","v189","v190","v191","v192","v193","v194","v195","v196","v197","v198","v199","v200","v201","v202","v203","v204","v205","v206","v207","v208","v209","v210","v211","v212","v213","v214","v215","v216","v217","v218","v219","v220","v221","v222","v223","v224","v225","v226","v227","v228","v229","v230","v231","v232","v233","v234","v235","v236","v237","v238","v239","v240","v241","v242","v243","v244","v245","v246","v247","v248","v249","v250","v251","v252","v253","v254","v255"
#define ATT_SCLOB "s63","s64","s65","s66","s67","s68","s69","s70","s71","s72","s73","s74","s75","s76","s77","s78","s79","s80","s81","s82","s83","s84","s85","s86","s87","s88","s89","s90","s91","s92","s93","s95","s96","s97","s98","s99","s100","s101"
#define ATT_WTCH 0
#define ATT_PEEL 1
#define ATT_PEEL_FOW 1
#define ATT_PEEL_PRE 0
#define ATT_PEEL_ODD(b) (0u)
#define ATT_PAGES 11
#define ATT_TAILB 968
enum { T_fo, T_fj, T_fa, T_fas, T_fb, T_fbs, T_f8, T_mk8, T_wt, T_wol, T_woh, T_wbl, T_wbh, T_outl, T_outh, T_r1l, T_r1h, T_r1n, T_r1s, T_r2n, T_r2s, T_m2b, T_m0, T_m1, T_m1b, T_mw, T_m3, T_m4, T_ml, T_w0, T_w1, T_w2, T_w3, T_A1, T_A2, T_A3, T_B1, T_B2, T_B3, T_Ao, T_Bo, T_Al, T_Bl, T_l8, T_xr, T_x8r, T_oA, T_oB, T_o8, T_x0, T_x1, T_x2, T_x3, T_x4, T_x5, T_x6, T_x8, T_ep };

__device__ __forceinline__ void out_consts(u32x4 (&vp)[3], const u16* wo, const u16* wob, const u16* out, const unsigned b, const unsigned w, const unsigned lane) {
    const unsigned sp = b & 7u, nt = (b >> 3) & 3u, j = b >> 5, head = 4u * sp + nt, OOR = 0x40000000u;
    unsigned vw[9];
#pragma unroll
    for (unsigned i = 0; i < 9u; ++i) {
        const unsigned q = 64u * i + lane, row = __umul24(q, 3641u) >> 16, c = q - __umul24(row, 18u);
        vw[i] = __umul24(row, DM * 2) + (c < 16u ? (c * 16u + sp * 32u + 32u) & 255u : c * 16u) - (i & 3u) * 1024u;
    }
    const unsigned row = 16u * w + (lane >> 2), r9 = __umul24(16u * (row >> 5) + (row & 12u) + 2u * ((row >> 4) & 1u) + ((row >> 1) & 1u), 18u) + 16u + (row & 1u);
    unsigned vwl = __umul24(r9, DM * 2) + ((lane & 2u) ? 256u : sp * 32u) + (lane & 1u) * 16u, vrd = __umul24(lane >> 2, 288u) + (lane & 3u) * 8u;
    unsigned tab = 0;
#define TB(n, v) asm("v_writelane_b32 %0, %1, %2" : "+v"(tab) : "s"((unsigned)(v)), "n"((int)T_##n))
    const unsigned FJ = (unsigned)WS_F + (F_J + 640u * j) * 4u, mt = 2u * (w & 2u) + (w & 1u), w2 = w < 2u ? w : 2u, hw = head * 147456u;
    const unsigned FR = (unsigned)WS_F + F_R * 4u + (8u * j + sp) * 1024u;
    TB(fo, FJ + (ATT_PEEL_FOW ? 16u * head + 4u * w : 4u * head)); TB(fj, FJ); TB(fa, FR + (w | 2u) * 256u); TB(fas, FR + w * 256u + nt * 4u);
    TB(fb, FR + 128u + w * 256u); TB(fbs, FR + 128u + w * 256u + nt * 4u);
    TB(ep, ssel(b + w, OOR, (unsigned)WS_F + F_EPOCH * 4u));
    const u16* pwo = wo + nt * 144u; const u16* pwb = wob + nt * 144u;
    TB(wol, PLO(pwo)); TB(woh, PHI(pwo) & 0xffffu); TB(wbl, PLO(pwb)); TB(wbh, PHI(pwb) & 0xffffu); TB(outl, PLO(out)); TB(outh, PHI(out) & 0xffffu);
    const unsigned odd = ATT_PEEL_ODD(b), wo1 = w + 3u * odd;
#define OSEL(a, b_) (ATT_PEEL_ODD(1u) ? ssel(odd, a, b_) : (b_))
#define WU(u) (((4u * sp + ((nt + 1u + ((u) >> 2)) & 3u)) * 4u + ((u) & 3u)) * 36864u)
    TB(r1l, sslt<3>(wo1, PLO(pwo), PLO(pwb))); TB(r1h, sslt<3>(wo1, PHI(pwo), PHI(pwb)) & 0xffffu);
    TB(r1n, sslt<3>(wo1, 72u * sp + 71u, 4095u) * 1152u + 288u); TB(r1s, sslt<3>(wo1, (72u * sp + 32u * w) * 1152u, ATT_PEEL_ODD(1u) ? sslt<2>(w, WU(w), sslt<3>(w, OOR, hw + 110592u)) : hw + 110592u));
    if (ATT_PEEL_ODD(1u)) { TB(r2n, (72u * sp + 143u) * 1152u + 288u); TB(r2s, sslt<3>(w, (72u * sp + 72u + 32u * w) * 1152u, OOR)); TB(m2b, sslt<2>(w, 41984u + 9216u * w, 32768u)); }
    TB(m0, 131072u + 9216u * w2); TB(w0, hw + 36864u * w2); TB(m1, sslt<3>(w, 41984u + 9216u * w, 32768u)); TB(m1b, ssel(w ^ 2u, sslt<3>(w, 41984u + 9216u * w, 32768u), 114688u - 4096u));
    TB(mw, 65536u + 9216u * w); TB(m3, sslt<3>(w, 102400u + 9216u * w, 0u)); TB(m4, OSEL(9216u + 13312u * (w & 1u), sslt<3>(w, 131072u + 9216u * w, 9216u))); TB(ml, 18432u + 1024u * w);
    const unsigned tA = (unsigned)WS_AF + (8u * j + mt) * (KS_TOT * 1024u), tB = (unsigned)WS_AF + (120u - 8u * j + mt) * (KS_TOT * 1024u);
#define HK(k) (4u * sp + ((nt + k) & 3u))
    TB(w1, WU(w + 2u * odd)); TB(w2, WU(4u + w + 2u * odd)); TB(w3, WU(8u + (w | (2u * odd))));
    TB(A1, tA + 18432u + HK(1u) * 4096u); TB(A2, tA + 18432u + HK(2u) * 4096u); TB(A3, tA + 18432u + HK(3u) * 4096u);
    TB(B1, tB + 18432u + HK(1u) * 4096u); TB(B2, tB + 18432u + HK(2u) * 4096u); TB(B3, tB + 18432u + HK(3u) * 4096u);
    const unsigned pre = ATT_PEEL_PRE ? sslt<7>(j, tA + 18432u + head * 4096u, OOR) : OOR;
    TB(Ao, OSEL(pre, tA + sp * 2048u)); TB(Bo, OSEL(pre + 2048u, tB + sp * 2048u)); TB(Al, tA + 16384u); TB(Bl, tB + 16384u);
    const unsigned nred = w ^ (sp >> 1), blk = sp & 1u;
    const unsigned xg = (unsigned)WS_X + (4u * j + nt) * 589824u + w * 16384u, oA = (128u * j + 16u * mt) * 1152u + nt * 288u, oB = (1920u - 128u * j + 16u * mt) * 1152u + nt * 288u;
    TB(l8, ssel(nred, OOR, ssel(blk, tB, tA) + 16384u)); TB(xr, xg + sp * 65536u); TB(x8r, ssel(nred, OOR, xg + 524288u + (sp & 1u) * 1024u));
    TB(oA, oA + sp * 32u); TB(oB, oB + sp * 32u); TB(o8, ssel(nred, OOR, ssel(blk, oB, oA) + 256u)); TB(x8, xg + 524288u + sp * 2048u);
    TB(f8, xg + w * 131072u + 14400u + sp * 4u); TB(mk8, ssel(nred, 0u, 0xff0000u));
#define XI(i) TB(x##i, xg + ((sp + 1u + i) & 7u) * 65536u + i * 2048u)
    XI(0); XI(1); XI(2); XI(3); XI(4); XI(5); XI(6);
#undef XI
#undef HK
#undef WU
#undef TB
    vp[0] = (u32x4){vw[0], vw[1], vw[2], vw[3]}; vp[1] = (u32x4){vw[4], vw[5], vw[6], vw[7]}; vp[2] = (u32x4){vw[8], vwl, vrd, tab};
}
__global__ __launch_bounds__(256, 1) __attribute__((aligned(4096))) void fused_kernel(const u16* __restrict__ x, const u16* __restrict__ ckv, const u16* __restrict__ wq, const u16* __restrict__ wuk,
                                                       const u16* __restrict__ wuv, const u16* __restrict__ wqkv, const u16* __restrict__ gw,
                                                       const u16* __restrict__ wo, const u16* __restrict__ wob, char* __restrict__ ws_, u16* __restrict__ out) {
    using namespace att;
    extern __shared__ __attribute__((aligned(16))) char lds[];
    u32x4 sa[5]; u32x2 s24, s28; char* ws; unsigned tdum; kaptr ka = (kaptr)__builtin_amdgcn_kernarg_segment_ptr();
    asm volatile("s_load_dwordx8 s[8:15], %[ka], 0x0\n\ts_load_dwordx8 s[16:23], %[ka], 0x20\n\ts_getpc_b64 s[4:5]\n\tv_lshlrev_b32_e32 v191, 7, %[t]\n\ts_bfe_u32 s6, %[b], %[cb]\n\t"
                 "v_lshl_add_u32 v191, s6, %[cs], v191\n\tv_min_u32_e32 v191, %[e], v191\n\tglobal_load_dword v191, v191, s[4:5]\n\ts_load_dwordx4 s[24:27], %[ka], 0x40\n\ts_load_dwordx2 s[28:29], %[ka], 0x50"
                 : "={s[8:11]}"(sa[0]), "={s[12:15]}"(sa[1]), "={s[16:19]}"(sa[2]), "={s[20:23]}"(sa[3]), "={s[24:25]}"(s24), "={s[26:27]}"(ws), "={s[28:29]}"(s28), "={v191}"(tdum), [ka] "+{s[0:1]}"(ka)
                 : [t] "v"(threadIdx.x), [b] "s"(blockIdx.x), [e] "i"(CT_END), [cb] "i"(CT_END < 0x20000 ? 0x20003 : 0x30003), [cs] "i"(CT_END < 0x20000 ? 15 : 14) : "s4", "s5", "s6", "scc");
    SBAR();
    const unsigned wid = rfl(threadIdx.x >> 6);
    unsigned tag, vl;
    const int b = blockIdx.x;
    u32x4 vp[5], srdA; u32x2 sd;
    proj_phase(ka, ws, wid, vp, vl, sa, s24, s28, tdum, srdA, sd, tag
    );
    unsigned* const flags = (unsigned*)(ws + WS_F);
    RTS(2);
#define AST(L) do {} while (0)
#define ATT_TAIL "s_endpgm\n"
    unsigned abad;
    asm volatile(ATT_ASM ATT_TAIL "L_end_%=:\n s_nop 1"
                 : "={s94}"(abad)
                 : "{v[0:3]}"(vp[0]), "{v[4:7]}"(vp[1]), "{v[8:11]}"(vp[2]), "{v[12:15]}"(vp[3]), "{v[16:19]}"(vp[4]), "{v20}"(vl), "{s[56:59]}"(srdA), "{s[60:61]}"(sd), "{s62}"(tag),
                   "{s[8:11]}"(sa[0]), "{s[12:15]}"(sa[1]), "{s[16:19]}"(sa[2]), "{s[20:23]}"(sa[3]), "{s24}"(sa[4][0]), "{s3}"(wid), "{s2}"(b), "{s[28:29]}"(s28)
                 : "memory", "m0", "vcc", "scc", ATT_VCLOB, ATT_SCLOB, ACLOB_LO, ACLOB_HI);
    {
        unsigned b2 = b, w2 = wid;
        asm volatile("s_waitcnt vmcnt(0)" : "+s"(ka), "+s"(b2), "+s"(w2), "+s"(ws) :: "memory");
        const unsigned j2 = b2 >> 5, p2 = w2 & 1u, h2 = (b2 & 7u) * 4u + ((b2 >> 3) & 3u);
#pragma unroll 1
        for (unsigned i = w2 & 2u; i < ((w2 & 2u) ? (abad ? 8u : 4u) : 2u); ++i)
            att_exact(ka[0], ka[2], ws, h2, (i < 4u ? 1920u - 128u * j2 : 128u * j2 - 128u) + 32u * i + 16u * p2, *reinterpret_cast<const float*>(lds + LDS_G + (32u * i + 16u * p2 + (lane_id() & 15u)) * 4u));
        asm volatile("s_waitcnt vmcnt(0)" ::: "memory");
        unsigned* fa = (unsigned*)(ws + WS_F) + F_R + (8u * j2 + (b2 & 7u)) * 256u + (2u + p2) * 64u + ((b2 >> 3) & 3u);
        if (w2 & 2u) st_flag(fa, tag);
        wait_flags(fa, ld_flag(fa), tag);
        const unsigned l2 = lane_id(); u32x4 c[3]; out_consts(c, ka[7], ka[8], ka[10], b2, w2, l2);
        asm volatile("s_waitcnt vmcnt(0) lgkmcnt(0)\n\ts_nop 7\n\ts_branch L_att_stub" :: "{v[0:3]}"(c[0]), "{v[4:7]}"(c[1]), "{v[8:11]}"(c[2]), "{v20}"(l2 * 16u), "{s[56:59]}"(SRDA), "{s60}"(SD(8u * j2, h2, w2)),
                     "{s61}"(SD(120u - 8u * j2, h2, w2)), "{s62}"(tag), "{s3}"(w2), "{s2}"(b2) : "memory");
    }
    RTS(5);
}

extern "C" void kernel_launch(void* const* d_in, const int* in_sizes, int n_in, void* d_out, int out_size, void* d_ws, size_t ws_size,
                              hipStream_t stream) {
    (void)hipFuncSetAttribute((const void*)fused_kernel, hipFuncAttributeMaxDynamicSharedMemorySize, att::LDS_BYTES);
    fused_kernel<<<dim3(256), 256, att::LDS_BYTES, stream>>>((const u16*)d_in[0], (const u16*)d_in[4], (const u16*)d_in[5], (const u16*)d_in[6], (const u16*)d_in[7], (const u16*)d_in[1],
                                                             (const u16*)d_in[9], (const u16*)d_in[2], (const u16*)d_in[8], (char*)d_ws, (u16*)d_out);
}
```

```cpp
#include <hip/hip_runtime.h>
#include <hip/hip_bf16.h>
#include <stdint.h>
#include <utility>

typedef short bf16x8 __attribute__((ext_vector_type(8)));
typedef short s16x4 __attribute__((ext_vector_type(4)));
typedef float f32x16 __attribute__((ext_vector_type(16)));
typedef float f32x4 __attribute__((ext_vector_type(4)));
typedef unsigned u32x4 __attribute__((ext_vector_type(4)));
typedef unsigned u32x2 __attribute__((ext_vector_type(2)));
typedef unsigned short u16;

constexpr int S = 2048, DM = 576, NH = 32, HD = 128, DC = 512, NQKV = 1728, DHA = 18;
constexpr int NBIG = NH * HD;
constexpr int KS_TOT = 146;

#define SBAR() __builtin_amdgcn_sched_barrier(0)
constexpr size_t WS_Q = 0, WS_K = WS_Q + (size_t)S * NBIG * 2, WS_V = WS_K + (size_t)S * NBIG * 2, WS_AF = WS_V + (size_t)S * NBIG * 2, WS_X = WS_AF + (size_t)128 * KS_TOT * 1024,
                 WS_P = WS_X + (size_t)64 * 16 * 9216, WS_F = WS_P + (size_t)128 * (4 * 32 * 256 + 1024) * 4;
constexpr size_t WS_L = (size_t)96 << 20;
static_assert(WS_F == 96206848, "workspace map is baked into the generated asm");
constexpr int F_EPOCH = 0, F_J = 1024, F_ST = 8192, F_KV = 40960, F_R = 65536;
constexpr unsigned TAG_STEP = 0x9E3779B1u;
__device__ __forceinline__ unsigned ld_flag(const unsigned* p) { return __hip_atomic_load(p, __ATOMIC_RELAXED, __HIP_MEMORY_SCOPE_AGENT); }
__device__ __forceinline__ void st_flag(unsigned* p, unsigned v) { __hip_atomic_store(p, v, __ATOMIC_RELAXED, __HIP_MEMORY_SCOPE_AGENT); }
__device__ __forceinline__ void wait_flags(const unsigned* p, unsigned v, unsigned tag) {
    if (__builtin_expect(__builtin_amdgcn_ballot_w64(v != tag) != 0ull, 0)) {
        unsigned spins = 0;
        do { __builtin_amdgcn_s_sleep(1); } while (__builtin_amdgcn_ballot_w64(ld_flag(p) != tag) != 0ull && ++spins < (1u << 22));
    }
    asm volatile("" ::: "memory");
}

__device__ __forceinline__ unsigned lane_id() { unsigned l; asm volatile("v_mbcnt_lo_u32_b32 %0, -1, 0\n\tv_mbcnt_hi_u32_b32 %0, -1, %0" : "=v"(l)); return l; }
#define RTS(slot) do {} while (0)
#define CT_END ((PROJ_PAGES + ATT_PAGES) * 4096 + (ATT_TAILB & ~127) - 128)
#define ACLOB_LO "a0","a1","a2","a3","a4","a5","a6","a7","a8","a9","a10","a11","a12","a13","a14","a15","a16","a17","a18","a19","a20","a21","a22","a23","a24","a25","a26","a27","a28","a29","a30","a31","a32","a33","a34","a35","a36","a37","a38","a39","a40","a41","a42","a43","a44","a45","a46","a47","a48","a49","a50","a51","a52","a53","a54","a55","a56","a57","a58","a59","a60","a61","a62","a63","a64","a65","a66","a67","a68","a69","a70","a71","a72","a73","a74","a75","a76","a77","a78","a79","a80","a81","a82","a83","a84","a85","a86","a87","a88","a89","a90","a91","a92","a93","a94","a95","a96","a97","a98","a99","a100","a101","a102","a103","a104","a105","a106","a107","a108","a109","a110","a111","a112","a113","a114","a115","a116","a117","a118","a119","a120","a121","a122","a123","a124","a125","a126","a127"
#define ACLOB_HI "a128","a129","a130","a131","a132","a133","a134","a135","a136","a137","a138","a139","a140","a141","a142","a143","a144","a145","a146","a147","a148","a149","a150","a151","a152","a153","a154","a155","a156","a157","a158","a159","a160","a161","a162","a163","a164","a165","a166","a167","a168","a169","a170","a171","a172","a173","a174","a175","a176","a177","a178","a179","a180","a181","a182","a183","a184","a185","a186","a187","a188","a189","a190","a191","a192","a193","a194","a195","a196","a197","a198","a199","a200","a201","a202","a203","a204","a205","a206","a207","a208","a209","a210","a211","a212","a213","a214","a215","a216","a217","a218","a219","a220","a221","a222","a223","a224","a225","a226","a227","a228","a229","a230","a231","a232","a233","a234","a235","a236","a237","a238","a239","a240","a241","a242","a243","a244","a245","a246","a247","a248","a249","a250","a251","a252","a253","a254","a255"
#define M_ "v_mfma_f32_16x16x32_bf16 "
#define X_ "ds_read_b64_tr_b16 "
#define B_ "buffer_load_dwordx4 "
#define C_ "v_cvt_pk_bf16_f32 "
#define R_ "ds_read_b128 "
#define E_ "v_exp_f32_e32 "
#define PLO(p) ((unsigned)(size_t)(p))
#define PHI(p) ((unsigned)((size_t)(p) >> 32))
__device__ __forceinline__ unsigned vsel(bool c, unsigned a, unsigned b) { asm("" : "+v"(a), "+v"(b)); return c ? a : b; }
__device__ __forceinline__ unsigned ssel(unsigned c, unsigned a, unsigned b) { unsigned r; asm("s_cmp_lg_u32 %1, 0\n\ts_cselect_b32 %0, %2, %3" : "=s"(r) : "s"(c), "s"(a), "s"(b) : "scc"); return r; }
template <int N> __device__ __forceinline__ unsigned sslt(unsigned c, unsigned a, unsigned b) { unsigned r; asm("s_cmpk_lt_u32 %1, %4\n\ts_cselect_b32 %0, %2, %3" : "=s"(r) : "s"(c), "s"(a), "s"(b), "n"(N) : "scc"); return r; }
namespace att {
constexpr int LDS_BYTES = 163840;
constexpr unsigned LDS_G = 159744u, LDS_L = 161792u;
}
#define SD(J8, H, W) ((unsigned)WS_AF + (((J8) + 2u * ((W) & 2u) + ((W) & 1u)) * KS_TOT + 18u + 4u * (H)) * 1024u)
#define SRDA (u32x4){PLO(ws), PHI(ws) & 0xffffu, (unsigned)WS_L, 0x00020000u}
#define PROJ_ASM \
".p2align 3\n" \
"L_pb:\n" \
"s_waitcnt lgkmcnt(0)\n" \
"s_mov_b32 s64, s10\n" \
"s_and_b32 s65, s11, 0xffff\n" \
"s_mov_b32 s66, 0x200000\n" \
"s_mov_b32 s67, 0x20000\n" \
"s_mov_b32 s68, s14\n" \
"s_and_b32 s69, s15, 0xffff\n" \
"s_mov_b32 s70, 0x400000\n" \
"s_mov_b32 s71, 0x20000\n" \
"s_mov_b32 s72, s16\n" \
"s_and_b32 s73, s17, 0xffff\n" \
"s_mov_b32 s74, 0x400000\n" \
"s_mov_b32 s75, 0x20000\n" \
"s_add_u32 m0, s30, 0x0\n" \
"s_movk_i32 s84, 0x0\n" \
B_"v22, s[64:67], s84 offen offset:0 lds\n" \
"s_movk_i32 s84, 0x1c00\n" \
B_"v23, s[64:67], s84 offen offset:1024 lds\n" \
"s_movk_i32 s84, 0x3800\n" \
B_"v22, s[64:67], s84 offen offset:2048 lds\n" \
"s_movk_i32 s84, 0x5400\n" \
B_"v23, s[64:67], s84 offen offset:3072 lds\n" \
"s_add_u32 m0, s31, 0x0\n" \
"s_movk_i32 s84, 0x0\n" \
B_"v26, s[68:71], s84 offen offset:0 lds\n" \
"s_movk_i32 s84, 0x7c00\n" \
B_"v26, s[68:71], s84 offen offset:1024 lds\n" \
"s_addk_i32 s84, 0x7c00\n" \
B_"v27, s[68:71], s84 offen offset:2048 lds\n" \
"s_addk_i32 s84, 0x7c00\n" \
B_"v27, s[68:71], s84 offen offset:3072 lds\n" \
"s_movk_i32 s7, 0x300\n" \
"s_lshr_b32 s4, s2, 1\n" \
"s_and_b32 s4, s4, 60\n" \
"s_or_b32 s4, s4, s3\n" \
"v_lshrrev_b32_e32 v212, 4, v20\n" \
"v_min_u32_e32 v212, 59, v212\n" \
"s_cmpk_lt_u32 s2, 0x80\n" \
"s_cselect_b32 s6, s14, s16\n" \
"v_subrev_u32_e32 v214, 48, v212\n" \
"v_mov_b32_e32 v212, s10\n" \
"v_mov_b32_e32 v215, s6\n" \
"s_cmpk_lt_u32 s2, 0x80\n" \
"s_cselect_b32 s5, s15, s17\n" \
"v_cmp_gt_u32_e32 vcc, s7, v20\n" \
"v_mov_b32_e64 v213, s11\n" \
"v_bfe_u32 v216, v20, 4, 3\n" \
"v_cndmask_b32_e32 v212, v212, v215, vcc\n" \
"v_mov_b32_e32 v215, s5\n" \
"s_lshl_b32 s5, s3, 4\n" \
"v_cndmask_b32_e32 v213, v213, v215, vcc\n" \
"v_lshlrev_b32_e64 v215, 5, v20\n" \
"v_and_b32_e32 v215, 0x7000, v215\n" \
"v_lshl_add_u32 v215, s4, 6, v215\n" \
"s_lshl_b32 s4, s2, 3\n" \
"s_and_b32 s4, s4, 56\n" \
"v_or3_b32 v215, v215, s4, v216\n" \
"s_lshl_b32 s4, s2, 6\n" \
"s_or_b32 s6, s5, s4\n" \
"v_cmp_gt_u32_e64 s[4:5], 6, v214\n" \
"v_add_u32_e32 v215, 0x2000, v215\n" \
"s_nop 0\n" \
"v_cndmask_b32_e64 v216, 4, 2, s[4:5]\n" \
"v_add3_u32 v214, s6, v214, v216\n" \
"s_lshl_b32 s4, s3, 9\n" \
"v_cndmask_b32_e32 v214, v214, v215, vcc\n" \
"v_lshlrev_b32_e32 v214, 7, v214\n" \
"v_mov_b32_e64 v215, 0\n" \
"v_lshl_add_u64 v[184:185], v[212:213], 0, v[214:215]\n" \
"s_or_b32 s32, s4, 0x25000\n" \
"s_add_u32 m0, s30, 0x1000\n" \
"s_mov_b32 s84, 0x8000\n" \
B_"v22, s[64:67], s84 offen offset:0 lds\n" \
"s_and_b32 s33, s3, 1\n" \
"v_lshrrev_b32_e32 v212, 4, v20\n" \
"v_lshrrev_b32_e64 v214, 8, v20\n" \
"v_bfe_u32 v212, v212, 1, 3\n" \
"v_lshlrev_b32_e32 v215, 2, v20\n" \
"s_addk_i32 s84, 0x1c00\n" \
B_"v23, s[64:67], s84 offen offset:1024 lds\n" \
"v_lshrrev_b32_e64 v216, 1, v20\n" \
"v_bfe_u32 v213, v20, 4, 4\n" \
"s_lshl_b32 s4, s3, 5\n" \
"v_xor_b32_e32 v212, v212, v214\n" \
"v_lshlrev_b32_e32 v214, 11, v214\n" \
"s_addk_i32 s84, 0x1c00\n" \
B_"v22, s[64:67], s84 offen offset:2048 lds\n" \
"v_and_b32_e32 v215, 0x300, v215\n" \
"v_and_b32_e64 v217, 24, v216\n" \
"v_and_or_b32 v213, s4, 64, v213\n" \
"v_or3_b32 v214, v215, v214, v217\n" \
"s_lshl_b32 s4, s33, 14\n" \
"s_addk_i32 s84, 0x1c00\n" \
B_"v23, s[64:67], s84 offen offset:3072 lds\n" \
"v_and_b32_e32 v215, 0xe0, v216\n" \
"v_lshlrev_b32_e32 v213, 7, v213\n" \
"v_lshlrev_b32_e32 v212, 4, v212\n" \
"v_or3_b32 v214, v214, s4, v215\n" \
"v_or_b32_e64 v28, v212, v213\n" \
"s_add_u32 m0, s31, 0x4000\n" \
"s_movk_i32 s84, 0x0\n" \
B_"v26, s[72:75], s84 offen offset:0 lds\n" \
"v_add_u32_e32 v32, 0x10000, v214\n" \
"v_bitop3_b32 v29, v212, 64, v213 bitop3:0x36\n" \
"v_xor_b32_e32 v33, 0x20, v32\n" \
"v_xor_b32_e64 v34, 0x40, v32\n" \
"v_xor_b32_e32 v35, 0x60, v32\n" \
"s_movk_i32 s84, 0x7c00\n" \
B_"v26, s[72:75], s84 offen offset:1024 lds\n" \
"v_xor_b32_e32 v36, 0x80, v32\n" \
"v_xor_b32_e32 v37, 0xa0, v32\n" \
"v_xor_b32_e32 v38, 0xc0, v32\n" \
"v_xor_b32_e32 v39, 0xe0, v32\n" \
"s_mov_b32 s80, s26\n" \
"s_addk_i32 s84, 0x7c00\n" \
B_"v27, s[72:75], s84 offen offset:2048 lds\n" \
"s_and_b32 s81, s27, 0xffff\n" \
"s_mov_b32 s82, 0x10000000\n" \
"s_mov_b32 s83, 0x20000\n" \
"s_mov_b32 s76, s18\n" \
"s_and_b32 s77, s19, 0xffff\n" \
"s_addk_i32 s84, 0x7c00\n" \
B_"v27, s[72:75], s84 offen offset:3072 lds\n" \
"s_mov_b32 s78, 0x1e6000\n" \
"s_mov_b32 s79, 0x20000\n" \
"s_branch L_pgo_%=\n" \
"L_pret:\n" \
"s_setpc_b64 s[88:89]\n" \
"L_pgo_%=:\n" \
"s_mul_i32 s88, s33, 0xa000\n" \
"s_lshl_b32 s89, s3, 11\n" \
"s_and_b32 s89, s89, 0x1000\n" \
"s_add_u32 s88, s88, s89\n" \
"s_getpc_b64 s[86:87]\n" \
"L_hpa_%=:\n" \
"s_add_u32 s86, s86, L_pp1-L_hpa_%=\n" \
"s_addc_u32 s87, s87, 0\n" \
"s_add_u32 s86, s86, s88\n" \
"s_addc_u32 s87, s87, 0\n" \
"s_add_u32 m0, s30, 0x8000\n" \
"s_movk_i32 s84, 0x80\n" \
B_"v22, s[64:67], s84 offen offset:0 lds\n" \
"s_lshr_b32 s4, s2, 1\n" \
"s_and_b32 s4, s4, 0x7c\n" \
"v_bfe_u32 v212, v20, 4, 5\n" \
"s_movk_i32 s84, 0x1c80\n" \
B_"v23, s[64:67], s84 offen offset:1024 lds\n" \
"s_or_b32 s4, s4, s3\n" \
"v_min_u32_e32 v212, 26, v212\n" \
"v_mad_u64_u32 v[212:213], s[4:5], s4, 27, v[212:213]\n" \
"s_movk_i32 s84, 0x3880\n" \
B_"v22, s[64:67], s84 offen offset:2048 lds\n" \
"v_mul_u32_u24_e32 v213, 0xaaab, v212\n" \
"s_lshr_b32 s7, s2, 5\n" \
"v_lshrrev_b32_e32 v213, 18, v213\n" \
"s_movk_i32 s84, 0x5480\n" \
B_"v23, s[64:67], s84 offen offset:3072 lds\n" \
"s_mov_b32 s4, 0x1fffffa\n" \
"s_and_b32 s6, s2, 7\n" \
"v_mad_u64_u32 v[214:215], s[4:5], v213, s4, v[212:213]\n" \
"s_mov_b64 exec, 0\n" \
"s_swappc_b64 s[88:89], s[86:87]\n" \
"s_mov_b64 exec, -1\n" \
"s_add_u32 s86, s86, 0x2000\n" \
"s_addc_u32 s87, s87, 0\n" \
"s_add_u32 m0, s31, 0x8000\n" \
"s_mov_b32 s84, 0x80000\n" \
B_"v26, s[68:71], s84 offen offset:0 lds\n" \
"s_mulk_i32 s7, 0x90\n" \
"v_mul_u32_u24_e32 v213, 27, v213\n" \
"v_lshrrev_b32_e32 v214, 1, v214\n" \
"s_addk_i32 s84, 0x7c00\n" \
B_"v26, s[68:71], s84 offen offset:1024 lds\n" \
"v_and_b32_e32 v212, 1, v212\n" \
"s_add_u32 s4, s7, s20\n" \
"v_mul_u32_u24_e32 v214, 9, v214\n" \
"v_add3_u32 v212, v212, s6, v213\n" \
"s_addc_u32 s5, 0, s21\n" \
"s_addk_i32 s84, 0x7c00\n" \
B_"v27, s[68:71], s84 offen offset:2048 lds\n" \
"v_add_lshl_u32 v212, v212, v214, 7\n" \
"v_mov_b32_e64 v213, 0\n" \
"v_lshl_add_u64 v[186:187], s[18:19], 0, v[212:213]\n" \
"s_addk_i32 s84, 0x7c00\n" \
B_"v27, s[68:71], s84 offen offset:3072 lds\n" \
"s_bitcmp1_b32 s3, 0\n" \
"s_cselect_b32 s6, 0x8c, 0\n" \
"s_add_u32 s4, s4, s6\n" \
"v_mov_b32_e32 v212, v187\n" \
"s_addc_u32 s5, s5, 0\n" \
"s_add_u32 m0, s30, 0x9000\n" \
"s_mov_b32 s84, 0x8080\n" \
B_"v22, s[64:67], s84 offen offset:0 lds\n" \
"v_writelane_b32 v186, s4, 31\n" \
"v_writelane_b32 v212, s5, 31\n" \
"v_mov_b32_e32 v187, v212\n" \
"s_addk_i32 s84, 0x1c00\n" \
B_"v23, s[64:67], s84 offen offset:1024 lds\n" \
"s_lshl_b32 s35, s2, 10\n" \
"s_lshr_b32 s37, s2, 1\n" \
"s_mul_i32 s37, s37, 0x24800\n" \
"s_mov_b64 exec, 0\n" \
"s_swappc_b64 s[88:89], s[86:87]\n" \
"s_mov_b64 exec, -1\n" \
"s_add_u32 s86, s86, 0x2000\n" \
"s_addc_u32 s87, s87, 0\n" \
"s_addk_i32 s84, 0x1c00\n" \
B_"v22, s[64:67], s84 offen offset:2048 lds\n" \
"s_and_b32 s35, s35, 0x400\n" \
"s_lshl_b32 s36, s3, 8\n" \
"s_or_b32 s35, s37, s35\n" \
"s_addk_i32 s84, 0x1c00\n" \
B_"v23, s[64:67], s84 offen offset:3072 lds\n" \
"s_and_b32 s4, s2, 7\n" \
"s_lshr_b32 s6, s2, 5\n" \
"s_lshl_b32 s34, s2, 5\n" \
"s_add_u32 m0, s31, 0xc000\n" \
"s_mov_b32 s84, 0x80000\n" \
B_"v26, s[72:75], s84 offen offset:0 lds\n" \
"s_add_i32 s35, s35, s36\n" \
"s_lshl_b32 s7, s4, 10\n" \
"s_and_b32 s34, s34, 0x300\n" \
"s_addk_i32 s84, 0x7c00\n" \
B_"v26, s[72:75], s84 offen offset:1024 lds\n" \
"s_add_i32 s35, s35, 0x3004000\n" \
"s_lshl_b32 s37, s6, 13\n" \
"s_or_b32 s34, s7, s34\n" \
"s_addk_i32 s84, 0x7c00\n" \
B_"v27, s[72:75], s84 offen offset:2048 lds\n" \
"s_or_b32 s7, s37, s7\n" \
"v_mov_b32_e32 v213, s35\n" \
"s_or_b32 s7, s7, s36\n" \
"s_addk_i32 s84, 0x7c00\n" \
B_"v27, s[72:75], s84 offen offset:3072 lds\n" \
"s_lshr_b32 s5, s2, 3\n" \
"s_or_b32 s7, s7, 0x5c00000\n" \
"v_writelane_b32 v213, s7, 2\n" \
"s_mul_i32 s5, s5, 0x90000\n" \
"v_writelane_b32 v213, s7, 3\n" \
"s_lshl_b32 s7, s3, 14\n" \
"s_lshl_b32 s4, s4, 16\n" \
"s_add_i32 s5, s5, s7\n" \
"s_or_b32 s34, s34, 0x5be8000\n" \
"v_writelane_b32 v213, s34, 4\n" \
"s_add_i32 s4, s5, s4\n" \
"v_writelane_b32 v213, s34, 5\n" \
"s_add_i32 s4, s4, 0x4243800\n" \
"v_writelane_b32 v213, s4, 6\n" \
"s_mul_i32 s4, s6, 0xa00\n" \
"v_lshlrev_b32_e32 v212, 3, v20\n" \
"s_lshl_b32 s5, s3, 7\n" \
"v_and_b32_e32 v212, 0x80, v212\n" \
"s_add_i32 s4, s4, s5\n" \
"s_add_i32 s4, s4, 0x5bc0f80\n" \
"v_writelane_b32 v213, s4, 7\n" \
"v_add_u32_e32 v49, v213, v212\n" \
"s_mov_b32 m0, s32\n" \
"s_mov_b32 exec_lo, 0xffffff\n" \
"s_mov_b32 exec_hi, 0xfff0000\n" \
"global_load_lds_dword v[184:185], off\n" \
"s_mov_b64 exec, -1\n" \
"s_mov_b32 m0, s32\n" \
"s_mov_b64 exec, 0xff\n" \
"buffer_load_dword v49, s[80:83], 0 offen lds\n" \
"s_mov_b64 exec, -1\n" \
"s_mov_b32 s84, 0x5bc0000\n" \
"buffer_load_dword v180, off, s[80:83], s84 sc1\n" \
"s_cmp_lg_u32 s33, 0\n" \
"s_cbranch_scc1 L_w1_%=\n" \
"L_w0_%=:\n" \
"s_waitcnt vmcnt(19)\n" \
"s_barrier\n" \
X_"a[160:161], v32 offset:0\n" \
X_"a[162:163], v32 offset:1024\n" \
R_"a[128:131], v28 offset:16384\n" \
X_"a[224:225], v32 offset:8192\n" \
X_"a[226:227], v32 offset:9216\n" \
R_"a[192:195], v29 offset:16384\n" \
R_"a[132:135], v28 offset:18432\n" \
R_"a[196:199], v29 offset:18432\n" \
X_"a[164:165], v33 offset:0\n" \
X_"a[166:167], v33 offset:1024\n" \
X_"a[228:229], v33 offset:8192\n" \
X_"a[230:231], v33 offset:9216\n" \
R_"a[136:139], v28 offset:20480\n" \
R_"a[200:203], v29 offset:20480\n" \
"s_waitcnt lgkmcnt(11)\n" \
M_"a[0:3], a[160:163], a[128:131], 0\n" \
X_"a[168:169], v34 offset:0\n" \
"s_lshr_b32 s4, s2, 1\n" \
"s_waitcnt lgkmcnt(9)\n" \
M_"a[0:3], a[224:227], a[192:195], a[0:3]\n" \
X_"a[170:171], v34 offset:1024\n" \
"s_nop 5\n" \
"s_waitcnt lgkmcnt(9)\n" \
M_"a[16:19], a[160:163], a[132:135], 0\n" \
X_"a[232:233], v34 offset:8192\n" \
"s_and_b32 s4, s4, 0x7c\n" \
"s_waitcnt lgkmcnt(9)\n" \
M_"a[16:19], a[224:227], a[196:199], a[16:19]\n" \
X_"a[234:235], v34 offset:9216\n" \
"s_nop 5\n" \
"s_waitcnt lgkmcnt(8)\n" \
M_"a[20:23], a[164:167], a[132:135], 0\n" \
R_"a[140:143], v28 offset:22528\n" \
"v_mov_b32_e32 v213, s13\n" \
"s_waitcnt lgkmcnt(7)\n" \
M_"a[20:23], a[228:231], a[196:199], a[20:23]\n" \
R_"a[204:207], v29 offset:22528\n" \
"s_nop 5\n" \
M_"a[4:7], a[164:167], a[128:131], 0\n" \
X_"a[172:173], v35 offset:0\n" \
"s_or_b32 s4, s4, s3\n" \
M_"a[4:7], a[228:231], a[192:195], a[4:7]\n" \
X_"a[174:175], v35 offset:1024\n" \
"s_nop 5\n" \
"s_waitcnt lgkmcnt(9)\n" \
M_"a[32:35], a[160:163], a[136:139], 0\n" \
X_"a[236:237], v35 offset:8192\n" \
"v_lshrrev_b32_e32 v212, 4, v20\n" \
"s_waitcnt lgkmcnt(9)\n" \
M_"a[32:35], a[224:227], a[200:203], a[32:35]\n" \
X_"a[238:239], v35 offset:9216\n" \
"s_nop 5\n" \
M_"a[36:39], a[164:167], a[136:139], 0\n" \
R_"a[144:147], v28 offset:0\n" \
"v_mad_u64_u32 v[214:215], s[4:5], s4, 36, v[212:213]\n" \
M_"a[36:39], a[228:231], a[200:203], a[36:39]\n" \
R_"a[208:211], v29 offset:0\n" \
"s_nop 5\n" \
"s_waitcnt lgkmcnt(10)\n" \
M_"a[40:43], a[168:171], a[136:139], 0\n" \
R_"a[148:151], v28 offset:2048\n" \
"v_mov_b32_e32 v217, s9\n" \
"s_waitcnt lgkmcnt(9)\n" \
M_"a[40:43], a[232:235], a[200:203], a[40:43]\n" \
R_"a[212:215], v29 offset:2048\n" \
"s_nop 5\n" \
M_"a[24:27], a[168:171], a[132:135], 0\n" \
R_"a[152:155], v28 offset:4096\n" \
"s_movk_i32 s4, 0x240\n" \
M_"a[24:27], a[232:235], a[196:199], a[24:27]\n" \
R_"a[216:219], v29 offset:4096\n" \
"s_nop 5\n" \
M_"a[8:11], a[168:171], a[128:131], 0\n" \
R_"a[156:159], v28 offset:6144\n" \
"v_cmp_gt_u32_e32 vcc, s4, v20\n" \
M_"a[8:11], a[232:235], a[192:195], a[8:11]\n" \
R_"a[220:223], v29 offset:6144\n" \
"s_mov_b32 m0, s32\n" \
"s_nop 5\n" \
"s_waitcnt lgkmcnt(13)\n" \
M_"a[48:51], a[160:163], a[140:143], 0\n" \
"s_mov_b32 exec_lo, 0xff000000\n" \
"s_mov_b32 exec_hi, 0x0\n" \
"global_load_lds_dword v[184:185], off\n" \
"s_mov_b64 exec, -1\n" \
"s_waitcnt lgkmcnt(12)\n" \
M_"a[48:51], a[224:227], a[204:207], a[48:51]\n" \
"s_nop 5\n" \
X_"a[176:177], v36 offset:0\n" \
M_"a[52:55], a[164:167], a[140:143], 0\n" \
X_"a[178:179], v36 offset:1024\n" \
"s_lshl_b32 s4, s2, 3\n" \
M_"a[52:55], a[228:231], a[204:207], a[52:55]\n" \
"s_mov_b32 m0, s32\n" \
"s_nop 5\n" \
M_"a[56:59], a[168:171], a[140:143], 0\n" \
"s_mov_b32 exec_lo, 0xffff\n" \
"s_mov_b32 exec_hi, 0x0\n" \
"global_load_lds_dword v[186:187], off\n" \
"s_mov_b64 exec, -1\n" \
M_"a[56:59], a[232:235], a[204:207], a[56:59]\n" \
"s_nop 5\n" \
X_"a[240:241], v36 offset:8192\n" \
"s_waitcnt lgkmcnt(13)\n" \
M_"a[60:63], a[172:175], a[140:143], 0\n" \
X_"a[242:243], v36 offset:9216\n" \
"s_and_b32 s4, s4, 56\n" \
"s_waitcnt lgkmcnt(12)\n" \
M_"a[60:63], a[236:239], a[204:207], a[60:63]\n" \
"s_nop 5\n" \
X_"a[180:181], v37 offset:0\n" \
M_"a[44:47], a[172:175], a[136:139], 0\n" \
X_"a[182:183], v37 offset:1024\n" \
"v_cndmask_b32_e32 v217, v217, v213, vcc\n" \
M_"a[44:47], a[236:239], a[200:203], a[44:47]\n" \
"s_nop 5\n" \
X_"a[244:245], v37 offset:8192\n" \
M_"a[28:31], a[172:175], a[132:135], 0\n" \
X_"a[246:247], v37 offset:9216\n" \
"v_lshlrev_b32_e32 v213, 3, v214\n" \
M_"a[28:31], a[236:239], a[196:199], a[28:31]\n" \
"s_nop 5\n" \
X_"a[184:185], v38 offset:0\n" \
M_"a[12:15], a[172:175], a[128:131], 0\n" \
"s_mov_b64 exec, 0\n" \
"s_swappc_b64 s[88:89], s[86:87]\n" \
"s_mov_b64 exec, -1\n" \
"s_add_u32 s86, s86, 0x2000\n" \
"s_addc_u32 s87, s87, 0\n" \
M_"a[12:15], a[236:239], a[192:195], a[12:15]\n" \
"s_nop 5\n" \
X_"a[186:187], v38 offset:1024\n" \
"s_waitcnt lgkmcnt(15)\n" \
M_"a[76:79], a[172:175], a[144:147], 0\n" \
X_"a[248:249], v38 offset:8192\n" \
"v_and_b32_e32 v213, 0x1ffc0, v213\n" \
M_"a[76:79], a[236:239], a[208:211], a[76:79]\n" \
"s_nop 5\n" \
"v_and_b32_e64 v214, 7, v214\n" \
M_"a[72:75], a[168:171], a[144:147], 0\n" \
"v_or3_b32 v213, v214, s4, v213\n" \
"s_lshl_b32 s4, s2, 2\n" \
M_"a[72:75], a[232:235], a[208:211], a[72:75]\n" \
"s_nop 5\n" \
X_"a[250:251], v38 offset:9216\n" \
M_"a[68:71], a[164:167], a[144:147], 0\n" \
"s_or_b32 s4, s4, s3\n" \
"s_mul_i32 s4, s4, 18\n" \
M_"a[68:71], a[228:231], a[208:211], a[68:71]\n" \
"s_nop 5\n" \
"v_min_u32_e32 v212, 53, v212\n" \
M_"a[64:67], a[160:163], a[144:147], 0\n" \
"s_sub_i32 s4, s4, 36\n" \
"v_add_u32_e32 v212, s4, v212\n" \
M_"a[64:67], a[224:227], a[208:211], a[64:67]\n" \
"s_nop 5\n" \
X_"a[188:189], v39 offset:0\n" \
M_"a[80:83], a[160:163], a[148:151], 0\n" \
"v_mov_b32_e32 v215, s12\n" \
"v_mov_b32_e32 v216, s8\n" \
"s_waitcnt lgkmcnt(15)\n" \
M_"a[80:83], a[224:227], a[212:215], a[80:83]\n" \
"s_nop 5\n" \
"v_cndmask_b32_e32 v212, v212, v213, vcc\n" \
M_"a[84:87], a[164:167], a[148:151], 0\n" \
X_"a[190:191], v39 offset:1024\n" \
"v_cndmask_b32_e64 v216, v216, v215, vcc\n" \
M_"a[84:87], a[228:231], a[212:215], a[84:87]\n" \
"s_nop 5\n" \
"v_lshlrev_b32_e32 v212, 7, v212\n" \
M_"a[88:91], a[168:171], a[148:151], 0\n" \
"v_mov_b32_e64 v213, 0\n" \
"v_lshl_add_u64 v[188:189], v[216:217], 0, v[212:213]\n" \
M_"a[88:91], a[232:235], a[212:215], a[88:91]\n" \
"s_nop 5\n" \
X_"a[252:253], v39 offset:8192\n" \
M_"a[92:95], a[172:175], a[148:151], 0\n" \
"s_movk_i32 s5, 0xd80\n" \
"s_lshr_b32 s6, s2, 5\n" \
M_"a[92:95], a[236:239], a[212:215], a[92:95]\n" \
"s_nop 5\n" \
"v_lshrrev_b32_e64 v212, 4, v20\n" \
M_"a[108:111], a[172:175], a[152:155], 0\n" \
X_"a[254:255], v39 offset:9216\n" \
"v_lshl_or_b32 v215, s3, 6, v212\n" \
M_"a[108:111], a[236:239], a[216:219], a[108:111]\n" \
"s_nop 5\n" \
"v_mul_u32_u24_e32 v216, 0x2aab, v215\n" \
M_"a[104:107], a[168:171], a[152:155], 0\n" \
"s_mov_b64 exec, 0\n" \
"s_swappc_b64 s[88:89], s[86:87]\n" \
"s_mov_b64 exec, -1\n" \
"s_andn2_b32 s85, 2, s3\n" \
"s_lshl_b32 s85, s85, 12\n" \
"s_add_u32 s86, s86, s85\n" \
"s_addc_u32 s87, s87, 0\n" \
M_"a[104:107], a[232:235], a[216:219], a[104:107]\n" \
"s_nop 5\n" \
"v_lshrrev_b32_e32 v216, 16, v216\n" \
M_"a[100:103], a[164:167], a[152:155], 0\n" \
"v_mad_i32_i24 v217, v216, -6, v215\n" \
"v_add_u32_e32 v218, 0xfffffe50, v215\n" \
M_"a[100:103], a[228:231], a[216:219], a[100:103]\n" \
"s_nop 5\n" \
"s_mul_i32 s35, s6, 0x48\n" \
M_"a[96:99], a[160:163], a[152:155], 0\n" \
"v_lshrrev_b32_e32 v219, 1, v217\n" \
"v_add_u32_e64 v218, s35, v216\n" \
M_"a[96:99], a[224:227], a[216:219], a[96:99]\n" \
"s_nop 5\n" \
"v_mul_u32_u24_e32 v219, 0x480, v219\n" \
"s_waitcnt lgkmcnt(15)\n" \
M_"a[112:115], a[160:163], a[156:159], 0\n" \
"v_and_b32_e64 v214, 16, v20\n" \
"v_mad_u32_u24 v219, v218, s5, v219\n" \
M_"a[112:115], a[224:227], a[220:223], a[112:115]\n" \
"s_nop 5\n" \
"v_or_b32_e32 v192, v219, v214\n" \
M_"a[116:119], a[164:167], a[156:159], 0\n" \
"v_mov_b32_e32 v220, 2.0\n" \
"v_mov_b32_e32 v222, 7\n" \
M_"a[116:119], a[228:231], a[220:223], a[116:119]\n" \
"s_nop 5\n" \
"v_lshlrev_b32_e32 v220, 7, v218\n" \
M_"a[120:123], a[168:171], a[156:159], 0\n" \
"v_lshrrev_b32_e32 v218, 1, v218\n" \
"v_and_b32_e32 v218, 4, v218\n" \
M_"a[120:123], a[232:235], a[220:223], a[120:123]\n" \
"s_nop 5\n" \
"v_and_b32_e32 v216, 2, v216\n" \
M_"a[124:127], a[172:175], a[156:159], 0\n" \
"s_mul_i32 s7, s6, 0x2400\n" \
"s_sub_i32 s34, 0x22000, s7\n" \
"s_waitcnt vmcnt(5) lgkmcnt(0)\n" \
M_"a[124:127], a[236:239], a[220:223], a[124:127]\n" \
"s_barrier\n" \
M_"v[52:55], a[176:179], a[128:131], 0\n" \
"s_add_u32 m0, s30, 0x0\n" \
"s_movk_i32 s84, 0x100\n" \
M_"v[52:55], a[240:243], a[192:195], v[52:55]\n" \
B_"v22, s[64:67], s84 offen offset:0 lds\n" \
"s_nop 5\n" \
M_"v[56:59], a[180:183], a[128:131], 0\n" \
X_"a[160:161], v32 offset:32768\n" \
"v_bitop3_b32 v216, v217, v218, v216 bitop3:0x1e\n" \
M_"v[56:59], a[244:247], a[192:195], v[56:59]\n" \
"s_movk_i32 s84, 0x1d00\n" \
"s_nop 5\n" \
M_"v[60:63], a[184:187], a[128:131], 0\n" \
B_"v23, s[64:67], s84 offen offset:1024 lds\n" \
M_"v[60:63], a[248:251], a[192:195], v[60:63]\n" \
"s_nop 5\n" \
X_"a[162:163], v32 offset:33792\n" \
M_"v[64:67], a[188:191], a[128:131], 0\n" \
X_"a[224:225], v32 offset:40960\n" \
"v_or_b32_e32 v213, 0x24400, v20\n" \
M_"v[64:67], a[252:255], a[192:195], v[64:67]\n" \
R_"a[128:131], v28 offset:49152\n" \
"s_nop 5\n" \
M_"v[80:83], a[188:191], a[132:135], 0\n" \
R_"a[192:195], v29 offset:49152\n" \
"v_lshlrev_b32_e32 v216, 4, v216\n" \
M_"v[80:83], a[252:255], a[196:199], v[80:83]\n" \
"s_movk_i32 s84, 0x3900\n" \
"s_nop 5\n" \
M_"v[76:79], a[184:187], a[132:135], 0\n" \
B_"v22, s[64:67], s84 offen offset:2048 lds\n" \
M_"v[76:79], a[248:251], a[196:199], v[76:79]\n" \
"s_movk_i32 s84, 0x5500\n" \
"s_nop 5\n" \
M_"v[72:75], a[180:183], a[132:135], 0\n" \
B_"v23, s[64:67], s84 offen offset:3072 lds\n" \
M_"v[72:75], a[244:247], a[196:199], v[72:75]\n" \
"s_add_u32 m0, s31, 0x0\n" \
"s_mov_b32 s84, 0x100000\n" \
"s_nop 5\n" \
M_"v[68:71], a[176:179], a[132:135], 0\n" \
B_"v26, s[68:71], s84 offen offset:0 lds\n" \
M_"v[68:71], a[240:243], a[196:199], v[68:71]\n" \
R_"a[132:135], v28 offset:51200\n" \
"s_nop 5\n" \
M_"v[84:87], a[176:179], a[136:139], 0\n" \
R_"a[196:199], v29 offset:51200\n" \
"v_add3_u32 v196, v220, s34, v216\n" \
M_"v[84:87], a[240:243], a[200:203], v[84:87]\n" \
"s_addk_i32 s84, 0x7c00\n" \
"s_nop 5\n" \
M_"v[88:91], a[180:183], a[136:139], 0\n" \
B_"v26, s[68:71], s84 offen offset:1024 lds\n" \
M_"v[88:91], a[244:247], a[200:203], v[88:91]\n" \
"s_nop 5\n" \
X_"a[226:227], v32 offset:41984\n" \
M_"v[92:95], a[184:187], a[136:139], 0\n" \
"s_addk_i32 s84, 0x7c00\n" \
M_"v[92:95], a[248:251], a[200:203], v[92:95]\n" \
B_"v27, s[68:71], s84 offen offset:2048 lds\n" \
"s_nop 5\n" \
M_"v[96:99], a[188:191], a[136:139], 0\n" \
X_"a[164:165], v33 offset:32768\n" \
"v_mov_b32_e64 v216, v213\n" \
M_"v[96:99], a[252:255], a[200:203], v[96:99]\n" \
R_"a[136:139], v28 offset:53248\n" \
"s_nop 5\n" \
M_"v[112:115], a[188:191], a[140:143], 0\n" \
R_"a[200:203], v29 offset:53248\n" \
"s_movk_i32 s36, 0x1b0\n" \
M_"v[112:115], a[252:255], a[204:207], v[112:115]\n" \
"s_addk_i32 s84, 0x7c00\n" \
"s_nop 5\n" \
M_"v[108:111], a[184:187], a[140:143], 0\n" \
B_"v27, s[68:71], s84 offen offset:3072 lds\n" \
"s_mov_b64 exec, 0\n" \
"s_swappc_b64 s[88:89], s[86:87]\n" \
"s_mov_b64 exec, -1\n" \
M_"v[108:111], a[248:251], a[204:207], v[108:111]\n" \
"s_add_u32 m0, s30, 0x1000\n" \
"s_mov_b32 s84, 0x8100\n" \
"s_nop 5\n" \
M_"v[104:107], a[180:183], a[140:143], 0\n" \
B_"v22, s[64:67], s84 offen offset:0 lds\n" \
M_"v[104:107], a[244:247], a[204:207], v[104:107]\n" \
"s_nop 5\n" \
X_"a[166:167], v33 offset:33792\n" \
M_"v[100:103], a[176:179], a[140:143], 0\n" \
X_"a[228:229], v33 offset:40960\n" \
"v_or_b32_e32 v216, 0x100, v215\n" \
M_"v[100:103], a[240:243], a[204:207], v[100:103]\n" \
R_"a[140:143], v28 offset:55296\n" \
"s_nop 5\n" \
M_"v[116:119], a[176:179], a[144:147], 0\n" \
R_"a[204:207], v29 offset:55296\n" \
"v_mul_u32_u24_e32 v217, 0x2aab, v216\n" \
M_"v[116:119], a[240:243], a[208:211], v[116:119]\n" \
"s_addk_i32 s84, 0x1c00\n" \
"s_nop 5\n" \
M_"v[120:123], a[180:183], a[144:147], 0\n" \
B_"v23, s[64:67], s84 offen offset:1024 lds\n" \
M_"v[120:123], a[244:247], a[208:211], v[120:123]\n" \
"s_addk_i32 s84, 0x1c00\n" \
"s_nop 5\n" \
M_"v[124:127], a[184:187], a[144:147], 0\n" \
B_"v22, s[64:67], s84 offen offset:2048 lds\n" \
M_"v[124:127], a[248:251], a[208:211], v[124:127]\n" \
".fill (L_pb + 3808 - .) / 4, 4, 0xBF800000\n" \
"L_pp1:\n" \
"s_cbranch_execz L_pret\n" \
"s_addk_i32 s84, 0x1c00\n" \
"s_nop 5\n" \
M_"v[128:131], a[188:191], a[144:147], 0\n" \
B_"v23, s[64:67], s84 offen offset:3072 lds\n" \
M_"v[128:131], a[252:255], a[208:211], v[128:131]\n" \
R_"a[144:147], v28 offset:32768\n" \
"s_nop 5\n" \
M_"v[144:147], a[188:191], a[148:151], 0\n" \
R_"a[208:211], v29 offset:32768\n" \
"v_add_u32_e32 v215, 0xffffff50, v215\n" \
M_"v[144:147], a[252:255], a[212:215], v[144:147]\n" \
"s_add_u32 m0, s31, 0x4000\n" \
"s_mov_b32 s84, 0x100000\n" \
"s_nop 5\n" \
M_"v[140:143], a[184:187], a[148:151], 0\n" \
B_"v26, s[72:75], s84 offen offset:0 lds\n" \
M_"v[140:143], a[248:251], a[212:215], v[140:143]\n" \
"s_nop 5\n" \
X_"a[230:231], v33 offset:41984\n" \
M_"v[136:139], a[180:183], a[148:151], 0\n" \
"s_addk_i32 s84, 0x7c00\n" \
M_"v[136:139], a[244:247], a[212:215], v[136:139]\n" \
B_"v26, s[72:75], s84 offen offset:1024 lds\n" \
"s_nop 5\n" \
M_"v[132:135], a[176:179], a[148:151], 0\n" \
X_"a[168:169], v34 offset:32768\n" \
"v_lshrrev_b32_e64 v217, 16, v217\n" \
M_"v[132:135], a[240:243], a[212:215], v[132:135]\n" \
R_"a[148:151], v28 offset:34816\n" \
"s_nop 5\n" \
M_"v[148:151], a[176:179], a[152:155], 0\n" \
R_"a[212:215], v29 offset:34816\n" \
"v_mov_b32_e32 v220, v215\n" \
M_"v[148:151], a[240:243], a[216:219], v[148:151]\n" \
"s_nop 5\n" \
X_"a[170:171], v34 offset:33792\n" \
M_"v[152:155], a[180:183], a[152:155], 0\n" \
X_"a[232:233], v34 offset:40960\n" \
"v_mad_i32_i24 v218, v217, -6, v216\n" \
M_"v[152:155], a[244:247], a[216:219], v[152:155]\n" \
"s_nop 5\n" \
X_"a[234:235], v34 offset:41984\n" \
M_"v[156:159], a[184:187], a[152:155], 0\n" \
X_"a[172:173], v35 offset:32768\n" \
"v_cmp_gt_u32_e64 vcc, s36, v216\n" \
M_"v[156:159], a[248:251], a[216:219], v[156:159]\n" \
"s_nop 5\n" \
X_"a[174:175], v35 offset:33792\n" \
M_"v[160:163], a[188:191], a[152:155], 0\n" \
X_"a[236:237], v35 offset:40960\n" \
"v_mov_b32_e32 v221, 7\n" \
M_"v[160:163], a[252:255], a[216:219], v[160:163]\n" \
R_"a[152:155], v28 offset:36864\n" \
"s_nop 5\n" \
M_"v[176:179], a[188:191], a[156:159], 0\n" \
R_"a[216:219], v29 offset:36864\n" \
"v_lshrrev_b32_e32 v222, 1, v218\n" \
M_"v[176:179], a[252:255], a[220:223], v[176:179]\n" \
"s_nop 5\n" \
X_"a[238:239], v35 offset:41984\n" \
M_"v[172:175], a[184:187], a[156:159], 0\n" \
"v_cndmask_b32_e32 v217, v220, v217, vcc\n" \
"s_movk_i32 s4, 0x48\n" \
M_"v[172:175], a[248:251], a[220:223], v[172:175]\n" \
"s_nop 5\n" \
"v_add_u32_e64 v220, s35, v217\n" \
M_"v[168:171], a[180:183], a[156:159], 0\n" \
"v_mul_u32_u24_e32 v222, 0x480, v222\n" \
"v_mad_u32_u24 v222, v220, s5, v222\n" \
M_"v[168:171], a[244:247], a[220:223], v[168:171]\n" \
"s_nop 5\n" \
"v_cmp_gt_u32_e64 s[4:5], s4, v215\n" \
M_"v[164:167], a[176:179], a[156:159], 0\n" \
"v_cndmask_b32_e32 v215, v221, v218, vcc\n" \
"v_lshrrev_b32_e64 v218, 1, v220\n" \
M_"v[164:167], a[240:243], a[220:223], v[164:167]\n" \
R_"a[156:159], v28 offset:38912\n" \
R_"a[220:223], v29 offset:38912\n" \
"s_waitcnt lgkmcnt(15)\n" \
M_"a[0:3], a[160:163], a[128:131], a[0:3]\n" \
"s_addk_i32 s84, 0x7c00\n" \
M_"a[0:3], a[224:227], a[192:195], a[0:3]\n" \
B_"v27, s[72:75], s84 offen offset:2048 lds\n" \
M_"a[4:7], a[164:167], a[128:131], a[4:7]\n" \
X_"a[176:177], v36 offset:32768\n" \
"v_or_b32_e32 v214, v222, v214\n" \
"s_waitcnt lgkmcnt(15)\n" \
M_"a[4:7], a[228:231], a[192:195], a[4:7]\n" \
"s_addk_i32 s84, 0x7c00\n" \
"s_waitcnt lgkmcnt(11)\n" \
M_"a[8:11], a[168:171], a[128:131], a[8:11]\n" \
B_"v27, s[72:75], s84 offen offset:3072 lds\n" \
"s_waitcnt lgkmcnt(9)\n" \
M_"a[8:11], a[232:235], a[192:195], a[8:11]\n" \
X_"a[178:179], v36 offset:33792\n" \
"v_mov_b32_e32 v222, 2.0\n" \
"s_waitcnt lgkmcnt(8)\n" \
M_"a[12:15], a[172:175], a[128:131], a[12:15]\n" \
X_"a[240:241], v36 offset:40960\n" \
"v_and_b32_e32 v218, 4, v218\n" \
"s_waitcnt lgkmcnt(5)\n" \
M_"a[12:15], a[236:239], a[192:195], a[12:15]\n" \
"v_and_b32_e32 v217, 2, v217\n" \
"v_bitop3_b32 v215, v215, v218, v217 bitop3:0x1e\n" \
M_"a[28:31], a[172:175], a[132:135], a[28:31]\n" \
"v_lshlrev_b32_e32 v223, 1, v220\n" \
"v_mov_b32_e32 v224, 2.0\n" \
M_"a[28:31], a[236:239], a[196:199], a[28:31]\n" \
X_"a[242:243], v36 offset:41984\n" \
"v_cndmask_b32_e64 v193, v222, v214, vcc\n" \
M_"a[24:27], a[168:171], a[132:135], a[24:27]\n" \
"v_lshlrev_b32_e32 v214, 7, v220\n" \
"v_lshlrev_b32_e32 v215, 4, v215\n" \
M_"a[24:27], a[232:235], a[196:199], a[24:27]\n" \
"s_mul_i32 s39, s3, 18\n" \
"v_add3_u32 v214, v214, s34, v215\n" \
M_"a[20:23], a[164:167], a[132:135], a[20:23]\n" \
"v_cndmask_b32_e64 v195, v224, v223, s[4:5]\n" \
"s_movk_i32 s4, 0x1f8\n" \
M_"a[20:23], a[228:231], a[196:199], a[20:23]\n" \
X_"a[180:181], v37 offset:32768\n" \
"v_mov_b32_e64 v215, v213\n" \
M_"a[16:19], a[160:163], a[132:135], a[16:19]\n" \
"s_add_i32 s35, s35, s39\n" \
"v_cmp_gt_u32_e32 vcc, s4, v216\n" \
M_"a[16:19], a[224:227], a[196:199], a[16:19]\n" \
"v_add_u32_e32 v212, s35, v212\n" \
"v_mov_b32_e32 v219, 2.0\n" \
M_"a[32:35], a[160:163], a[136:139], a[32:35]\n" \
"s_mov_b32 m0, s32\n" \
X_"a[182:183], v37 offset:33792\n" \
M_"a[32:35], a[224:227], a[200:203], a[32:35]\n" \
"s_mov_b32 exec_lo, 0x0\n" \
"s_mov_b32 exec_hi, 0xff\n" \
"global_load_lds_dword v[184:185], off\n" \
"s_mov_b64 exec, -1\n" \
M_"a[36:39], a[164:167], a[136:139], a[36:39]\n" \
"v_cndmask_b32_e32 v197, v215, v214, vcc\n" \
"s_movk_i32 s35, 0x120\n" \
M_"a[36:39], a[228:231], a[200:203], a[36:39]\n" \
X_"a[244:245], v37 offset:40960\n" \
"v_mul_u32_u24_e32 v214, 0xd80, v212\n" \
M_"a[40:43], a[168:171], a[136:139], a[40:43]\n" \
"v_cmp_gt_u32_e32 vcc, s35, v20\n" \
"v_lshrrev_b32_e64 v215, 1, v212\n" \
M_"a[40:43], a[232:235], a[200:203], a[40:43]\n" \
"s_mov_b32 m0, s32\n" \
"s_and_b32 s4, s2, 7\n" \
M_"a[44:47], a[172:175], a[136:139], a[44:47]\n" \
"s_mov_b32 exec_lo, 0xffff0000\n" \
"s_mov_b32 exec_hi, 0x0\n" \
"global_load_lds_dword v[186:187], off\n" \
"s_mov_b64 exec, -1\n" \
M_"a[44:47], a[236:239], a[200:203], a[44:47]\n" \
X_"a[246:247], v37 offset:41984\n" \
"v_cndmask_b32_e64 v194, v219, v214, vcc\n" \
M_"a[60:63], a[172:175], a[140:143], a[60:63]\n" \
"v_lshlrev_b32_e32 v214, 7, v212\n" \
"v_lshrrev_b32_e32 v212, 2, v212\n" \
M_"a[60:63], a[236:239], a[204:207], a[60:63]\n" \
"v_and_b32_e64 v212, 2, v212\n" \
"v_and_or_b32 v212, v215, 1, v212\n" \
M_"a[56:59], a[168:171], a[140:143], a[56:59]\n" \
X_"a[184:185], v38 offset:32768\n" \
"s_lshl_b32 s5, s4, 2\n" \
M_"a[56:59], a[232:235], a[204:207], a[56:59]\n" \
"s_bfe_u32 s38, s2, 0x20003\n" \
"v_lshlrev_b32_e32 v212, 5, v212\n" \
M_"a[52:55], a[164:167], a[140:143], a[52:55]\n" \
"s_movk_i32 s35, 0x60\n" \
"s_or_b32 s5, s5, s38\n" \
M_"a[52:55], a[228:231], a[204:207], a[52:55]\n" \
X_"a[186:187], v38 offset:33792\n" \
"v_bitop3_b32 v212, v212, s35, v214 bitop3:0x36\n" \
M_"a[48:51], a[160:163], a[140:143], a[48:51]\n" \
"s_lshr_b32 s36, s2, 3\n" \
"v_add_u32_e32 v212, s34, v212\n" \
M_"a[48:51], a[224:227], a[204:207], a[48:51]\n" \
"s_mul_i32 s34, s5, 0x12000\n" \
"s_or_b32 s40, s34, 0x6000000\n" \
M_"a[64:67], a[160:163], a[144:147], a[64:67]\n" \
X_"a[248:249], v38 offset:40960\n" \
"s_mul_i32 s34, s36, 0x90000\n" \
M_"a[64:67], a[224:227], a[208:211], a[64:67]\n" \
"s_lshl_b32 s36, s3, 14\n" \
"s_add_i32 s34, s34, s36\n" \
M_"a[68:71], a[164:167], a[144:147], a[68:71]\n" \
"s_lshl_b32 s38, s5, 8\n" \
"s_add_i32 s41, s34, 0x4240000\n" \
M_"a[68:71], a[228:231], a[208:211], a[68:71]\n" \
X_"a[250:251], v38 offset:41984\n" \
"s_mul_i32 s34, s5, 36\n" \
M_"a[72:75], a[168:171], a[144:147], a[72:75]\n" \
"s_add_i32 s5, s7, s40\n" \
"s_addk_i32 s5, 0x2000\n" \
M_"a[72:75], a[232:235], a[208:211], a[72:75]\n" \
"s_mov_b32 s37, 2.0\n" \
"s_or_b32 s38, s38, 0x5be8000\n" \
M_"a[76:79], a[172:175], a[144:147], a[76:79]\n" \
"s_cmp_lg_u32 s3, 0\n" \
"s_cselect_b32 s36, s37, s5\n" \
M_"a[76:79], a[236:239], a[208:211], a[76:79]\n" \
X_"a[188:189], v39 offset:32768\n" \
"s_lshl_b32 s5, s6, 2\n" \
M_"a[92:95], a[172:175], a[148:151], a[92:95]\n" \
"s_or_b32 s5, s38, s5\n" \
"s_lshl_b32 s35, s3, 10\n" \
M_"a[92:95], a[236:239], a[212:215], a[92:95]\n" \
"s_cmpk_lt_u32 s3, 3\n" \
"s_cselect_b32 s37, s37, s5\n" \
M_"a[88:91], a[168:171], a[148:151], a[88:91]\n" \
X_"a[190:191], v39 offset:33792\n" \
"s_lshl_b32 s5, s4, 16\n" \
M_"a[88:91], a[232:235], a[212:215], a[88:91]\n" \
"s_lshl_b32 s4, s4, 11\n" \
"v_cndmask_b32_e64 v198, v213, v212, vcc\n" \
M_"a[84:87], a[164:167], a[148:151], a[84:87]\n" \
"v_or_b32_e32 v212, s35, v20\n" \
"s_or_b32 s39, s40, s35\n" \
M_"a[84:87], a[228:231], a[212:215], a[84:87]\n" \
X_"a[252:253], v39 offset:40960\n" \
"s_or_b32 s55, s41, s4\n" \
M_"a[80:83], a[160:163], a[148:151], a[80:83]\n" \
"v_or_b32_e32 v199, 0x22000, v212\n" \
"s_add_i32 s35, s39, s7\n" \
M_"a[80:83], a[224:227], a[212:215], a[80:83]\n" \
"s_add_i32 s54, s41, s5\n" \
"s_add_i32 s55, s55, 0x80000\n" \
M_"a[96:99], a[160:163], a[152:155], a[96:99]\n" \
X_"a[254:255], v39 offset:41984\n" \
"v_mov_b32_e32 v215, 0xffff\n" \
M_"a[96:99], a[224:227], a[216:219], a[96:99]\n" \
"s_and_b32 s4, s3, 1\n" \
"s_lshl_b32 s5, s4, 4\n" \
M_"a[100:103], a[164:167], a[152:155], a[100:103]\n" \
"v_lshrrev_b32_e32 v212, 4, v20\n" \
"v_lshrrev_b32_e64 v218, 6, v20\n" \
M_"a[100:103], a[228:231], a[216:219], a[100:103]\n" \
"v_and_b32_e64 v219, 12, v218\n" \
"v_and_or_b32 v214, v212, 15, s5\n" \
M_"a[104:107], a[168:171], a[152:155], a[104:107]\n" \
"v_cmp_gt_u32_e64 vcc, v219, v214\n" \
"v_mov_b32_e32 v220, 0xffff0000\n" \
M_"a[104:107], a[232:235], a[216:219], a[104:107]\n" \
"s_lshl_b32 s5, s2, 2\n" \
"v_cndmask_b32_e64 v217, v215, 0, vcc\n" \
M_"a[108:111], a[172:175], a[152:155], a[108:111]\n" \
"v_cmp_lt_u32_e32 vcc, v219, v214\n" \
"v_mov_b32_e64 v213, s23\n" \
M_"a[108:111], a[236:239], a[216:219], a[108:111]\n" \
"s_or_b32 s5, s5, s3\n" \
"v_cndmask_b32_e32 v221, 0, v220, vcc\n" \
"s_waitcnt lgkmcnt(15)\n" \
M_"a[124:127], a[172:175], a[156:159], a[124:127]\n" \
"v_or_b32_e32 v12, v217, v221\n" \
"v_or_b32_e64 v217, 2, v219\n" \
M_"a[124:127], a[236:239], a[220:223], a[124:127]\n" \
"v_cmp_gt_u32_e32 vcc, v217, v214\n" \
"v_mov_b32_e32 v216, s25\n" \
M_"a[120:123], a[168:171], a[156:159], a[120:123]\n" \
"s_movk_i32 s40, 0xf400\n" \
"v_cndmask_b32_e64 v221, v215, 0, vcc\n" \
M_"a[120:123], a[232:235], a[220:223], a[120:123]\n" \
"v_cmp_lt_u32_e32 vcc, v217, v214\n" \
"s_lshl_b32 s4, s4, 6\n" \
M_"a[116:119], a[164:167], a[156:159], a[116:119]\n" \
"v_cndmask_b32_e32 v217, 0, v220, vcc\n" \
"v_or_b32_e64 v13, v221, v217\n" \
M_"a[116:119], a[228:231], a[220:223], a[116:119]\n" \
"v_or_b32_e32 v217, 16, v219\n" \
"v_cmp_gt_u32_e32 vcc, v217, v214\n" \
M_"a[112:115], a[160:163], a[156:159], a[112:115]\n" \
"s_nop 0\n" \
"v_cndmask_b32_e64 v221, v215, 0, vcc\n" \
"v_cmp_lt_u32_e32 vcc, v217, v214\n" \
M_"a[112:115], a[224:227], a[220:223], a[112:115]\n" \
"s_waitcnt vmcnt(20)\n" \
"v_readfirstlane_b32 s62, v180\n" \
"s_nop 3\n" \
"s_add_u32 s62, s62, 0x9E3779B1\n" \
"s_waitcnt vmcnt(2) lgkmcnt(0)\n" \
"s_barrier\n" \
M_"v[52:55], a[176:179], a[128:131], v[52:55]\n" \
"s_add_u32 m0, s30, 0x8000\n" \
"s_movk_i32 s84, 0x180\n" \
M_"v[52:55], a[240:243], a[192:195], v[52:55]\n" \
B_"v22, s[64:67], s84 offen offset:0 lds\n" \
M_"v[56:59], a[180:183], a[128:131], v[56:59]\n" \
X_"a[160:161], v32 offset:0\n" \
"v_cndmask_b32_e64 v217, 0, v220, vcc\n" \
M_"v[56:59], a[244:247], a[192:195], v[56:59]\n" \
"s_movk_i32 s84, 0x1d80\n" \
M_"v[60:63], a[184:187], a[128:131], v[60:63]\n" \
B_"v23, s[64:67], s84 offen offset:1024 lds\n" \
M_"v[60:63], a[248:251], a[192:195], v[60:63]\n" \
X_"a[162:163], v32 offset:1024\n" \
"v_or_b32_e32 v14, v221, v217\n" \
M_"v[64:67], a[188:191], a[128:131], v[64:67]\n" \
X_"a[224:225], v32 offset:8192\n" \
"v_or_b32_e64 v217, 18, v219\n" \
M_"v[64:67], a[252:255], a[192:195], v[64:67]\n" \
R_"a[128:131], v28 offset:16384\n" \
"v_cmp_gt_u32_e64 vcc, v217, v214\n" \
M_"v[80:83], a[188:191], a[132:135], v[80:83]\n" \
R_"a[192:195], v29 offset:16384\n" \
"v_cndmask_b32_e64 v215, v215, 0, vcc\n" \
M_"v[80:83], a[252:255], a[196:199], v[80:83]\n" \
"s_movk_i32 s84, 0x3980\n" \
M_"v[76:79], a[184:187], a[132:135], v[76:79]\n" \
B_"v22, s[64:67], s84 offen offset:2048 lds\n" \
M_"v[76:79], a[248:251], a[196:199], v[76:79]\n" \
"s_movk_i32 s84, 0x5580\n" \
M_"v[72:75], a[180:183], a[132:135], v[72:75]\n" \
B_"v23, s[64:67], s84 offen offset:3072 lds\n" \
M_"v[72:75], a[244:247], a[196:199], v[72:75]\n" \
"s_add_u32 m0, s31, 0x8000\n" \
"s_mov_b32 s84, 0x180000\n" \
M_"v[68:71], a[176:179], a[132:135], v[68:71]\n" \
B_"v26, s[68:71], s84 offen offset:0 lds\n" \
M_"v[68:71], a[240:243], a[196:199], v[68:71]\n" \
R_"a[132:135], v28 offset:18432\n" \
"v_cmp_lt_u32_e64 vcc, v217, v214\n" \
M_"v[84:87], a[176:179], a[136:139], v[84:87]\n" \
R_"a[196:199], v29 offset:18432\n" \
"v_cndmask_b32_e64 v214, 0, v220, vcc\n" \
M_"v[84:87], a[240:243], a[200:203], v[84:87]\n" \
"s_addk_i32 s84, 0x7c00\n" \
M_"v[88:91], a[180:183], a[136:139], v[88:91]\n" \
B_"v26, s[68:71], s84 offen offset:1024 lds\n" \
M_"v[88:91], a[244:247], a[200:203], v[88:91]\n" \
X_"a[226:227], v32 offset:9216\n" \
"v_or_b32_e32 v15, v215, v214\n" \
M_"v[92:95], a[184:187], a[136:139], v[92:95]\n" \
"s_addk_i32 s84, 0x7c00\n" \
M_"v[92:95], a[248:251], a[200:203], v[92:95]\n" \
B_"v27, s[68:71], s84 offen offset:2048 lds\n" \
M_"v[96:99], a[188:191], a[136:139], v[96:99]\n" \
X_"a[164:165], v33 offset:0\n" \
"v_mad_u64_u32 v[214:215], s[6:7], s5, 42, v[212:213]\n" \
M_"v[96:99], a[252:255], a[200:203], v[96:99]\n" \
R_"a[136:139], v28 offset:20480\n" \
"s_movk_i32 s5, 0x1440\n" \
M_"v[112:115], a[188:191], a[140:143], v[112:115]\n" \
R_"a[200:203], v29 offset:20480\n" \
"v_cmp_gt_u32_e64 vcc, s5, v214\n" \
M_"v[112:115], a[252:255], a[204:207], v[112:115]\n" \
"s_addk_i32 s84, 0x7c00\n" \
"s_nop 0\n" \
M_"v[108:111], a[184:187], a[140:143], v[108:111]\n" \
B_"v27, s[68:71], s84 offen offset:3072 lds\n" \
M_"v[108:111], a[248:251], a[204:207], v[108:111]\n" \
"s_add_u32 m0, s30, 0x9000\n" \
"s_mov_b32 s84, 0x8180\n" \
M_"v[104:107], a[180:183], a[140:143], v[104:107]\n" \
B_"v22, s[64:67], s84 offen offset:0 lds\n" \
M_"v[104:107], a[244:247], a[204:207], v[104:107]\n" \
X_"a[166:167], v33 offset:1024\n" \
"v_min_u32_e32 v217, 0xa43f, v214\n" \
M_"v[100:103], a[176:179], a[140:143], v[100:103]\n" \
X_"a[228:229], v33 offset:8192\n" \
"v_mov_b32_e64 v214, s24\n" \
M_"v[100:103], a[240:243], a[204:207], v[100:103]\n" \
R_"a[140:143], v28 offset:22528\n" \
"v_cndmask_b32_e64 v215, v216, v213, vcc\n" \
M_"v[116:119], a[176:179], a[144:147], v[116:119]\n" \
R_"a[204:207], v29 offset:22528\n" \
"v_mov_b32_e64 v213, s22\n" \
M_"v[116:119], a[240:243], a[208:211], v[116:119]\n" \
"s_addk_i32 s84, 0x1c00\n" \
M_"v[120:123], a[180:183], a[144:147], v[120:123]\n" \
B_"v23, s[64:67], s84 offen offset:1024 lds\n" \
M_"v[120:123], a[244:247], a[208:211], v[120:123]\n" \
"s_addk_i32 s84, 0x1c00\n" \
M_"v[124:127], a[184:187], a[144:147], v[124:127]\n" \
B_"v22, s[64:67], s84 offen offset:2048 lds\n" \
M_"v[124:127], a[248:251], a[208:211], v[124:127]\n" \
"s_addk_i32 s84, 0x1c00\n" \
M_"v[128:131], a[188:191], a[144:147], v[128:131]\n" \
B_"v23, s[64:67], s84 offen offset:3072 lds\n" \
M_"v[128:131], a[252:255], a[208:211], v[128:131]\n" \
R_"a[144:147], v28 offset:0\n" \
"s_movk_i32 s6, 0xffee\n" \
M_"v[144:147], a[188:191], a[148:151], v[144:147]\n" \
R_"a[208:211], v29 offset:0\n" \
"v_cndmask_b32_e64 v214, v214, v213, vcc\n" \
M_"v[144:147], a[252:255], a[212:215], v[144:147]\n" \
"s_add_u32 m0, s31, 0xc000\n" \
"s_mov_b32 s84, 0x180000\n" \
M_"v[140:143], a[184:187], a[148:151], v[140:143]\n" \
B_"v26, s[72:75], s84 offen offset:0 lds\n" \
M_"v[140:143], a[248:251], a[212:215], v[140:143]\n" \
X_"a[230:231], v33 offset:9216\n" \
"v_add_u32_e32 v213, 0xffffebc0, v217\n" \
M_"v[136:139], a[180:183], a[148:151], v[136:139]\n" \
"s_addk_i32 s84, 0x7c00\n" \
M_"v[136:139], a[244:247], a[212:215], v[136:139]\n" \
B_"v26, s[72:75], s84 offen offset:1024 lds\n" \
M_"v[132:135], a[176:179], a[148:151], v[132:135]\n" \
X_"a[168:169], v34 offset:0\n" \
"s_lshl_b32 s5, s2, 5\n" \
M_"v[132:135], a[240:243], a[212:215], v[132:135]\n" \
R_"a[148:151], v28 offset:2048\n" \
"v_cndmask_b32_e64 v213, v213, v217, vcc\n" \
M_"v[148:151], a[176:179], a[152:155], v[148:151]\n" \
R_"a[212:215], v29 offset:2048\n" \
"v_lshlrev_b32_e64 v216, 7, v213\n" \
M_"v[148:151], a[240:243], a[216:219], v[148:151]\n" \
X_"a[170:171], v34 offset:1024\n" \
"v_mul_u32_u24_e32 v213, 0xe39, v212\n" \
M_"v[152:155], a[180:183], a[152:155], v[152:155]\n" \
X_"a[232:233], v34 offset:8192\n" \
"v_mov_b32_e64 v217, 0\n" \
M_"v[152:155], a[244:247], a[216:219], v[152:155]\n" \
X_"a[234:235], v34 offset:9216\n" \
"v_lshrrev_b32_e64 v213, 16, v213\n" \
M_"v[156:159], a[184:187], a[152:155], v[156:159]\n" \
X_"a[172:173], v35 offset:0\n" \
"v_lshl_add_u64 v[16:17], v[214:215], 0, v[216:217]\n" \
M_"v[156:159], a[248:251], a[216:219], v[156:159]\n" \
X_"a[174:175], v35 offset:1024\n" \
"v_mad_i32_i24 v214, v213, s6, v212\n" \
M_"v[160:163], a[188:191], a[152:155], v[160:163]\n" \
X_"a[236:237], v35 offset:8192\n" \
"s_add_i32 s5, s5, 32\n" \
M_"v[160:163], a[252:255], a[216:219], v[160:163]\n" \
R_"a[152:155], v28 offset:4096\n" \
"v_lshlrev_b32_e32 v215, 4, v214\n" \
M_"v[176:179], a[188:191], a[156:159], v[176:179]\n" \
R_"a[216:219], v29 offset:4096\n" \
"v_add_u32_e64 v216, s5, v215\n" \
M_"v[176:179], a[252:255], a[220:223], v[176:179]\n" \
X_"a[238:239], v35 offset:9216\n" \
"v_and_b32_e32 v216, 0xf0, v216\n" \
M_"v[172:175], a[184:187], a[156:159], v[172:175]\n" \
"v_cmp_gt_u32_e32 vcc, 16, v214\n" \
"s_movk_i32 s7, 0xf800\n" \
M_"v[172:175], a[248:251], a[220:223], v[172:175]\n" \
"v_cndmask_b32_e32 v214, v215, v216, vcc\n" \
"v_or_b32_e32 v215, 64, v212\n" \
M_"v[168:171], a[180:183], a[156:159], v[168:171]\n" \
"v_mul_u32_u24_e32 v216, 0xe39, v215\n" \
"v_lshrrev_b32_e64 v216, 16, v216\n" \
M_"v[168:171], a[244:247], a[220:223], v[168:171]\n" \
"v_mad_i32_i24 v215, v216, s6, v215\n" \
"v_lshlrev_b32_e64 v217, 4, v215\n" \
M_"v[164:167], a[176:179], a[156:159], v[164:167]\n" \
"v_add_u32_e64 v220, s5, v217\n" \
"v_and_b32_e32 v220, 0xf0, v220\n" \
M_"v[164:167], a[240:243], a[220:223], v[164:167]\n" \
R_"a[156:159], v28 offset:6144\n" \
R_"a[220:223], v29 offset:6144\n" \
"s_waitcnt lgkmcnt(15)\n" \
M_"a[0:3], a[160:163], a[128:131], a[0:3]\n" \
"s_addk_i32 s84, 0x7c00\n" \
M_"a[0:3], a[224:227], a[192:195], a[0:3]\n" \
B_"v27, s[72:75], s84 offen offset:2048 lds\n" \
M_"a[4:7], a[164:167], a[128:131], a[4:7]\n" \
X_"a[176:177], v36 offset:0\n" \
"v_cmp_gt_u32_e32 vcc, 16, v215\n" \
"s_waitcnt lgkmcnt(15)\n" \
M_"a[4:7], a[228:231], a[192:195], a[4:7]\n" \
"s_addk_i32 s84, 0x7c00\n" \
"s_waitcnt lgkmcnt(11)\n" \
M_"a[8:11], a[168:171], a[128:131], a[8:11]\n" \
B_"v27, s[72:75], s84 offen offset:3072 lds\n" \
"s_waitcnt lgkmcnt(9)\n" \
M_"a[8:11], a[232:235], a[192:195], a[8:11]\n" \
X_"a[178:179], v36 offset:1024\n" \
"v_cndmask_b32_e32 v215, v217, v220, vcc\n" \
"s_waitcnt lgkmcnt(8)\n" \
M_"a[12:15], a[172:175], a[128:131], a[12:15]\n" \
X_"a[240:241], v36 offset:8192\n" \
"v_or_b32_e32 v217, 0x80, v212\n" \
"s_waitcnt lgkmcnt(5)\n" \
M_"a[12:15], a[236:239], a[192:195], a[12:15]\n" \
"v_mul_u32_u24_e32 v220, 0xe39, v217\n" \
"v_lshrrev_b32_e64 v220, 16, v220\n" \
M_"a[28:31], a[172:175], a[132:135], a[28:31]\n" \
"v_mad_i32_i24 v217, v220, s6, v217\n" \
"v_lshlrev_b32_e64 v221, 4, v217\n" \
M_"a[28:31], a[236:239], a[196:199], a[28:31]\n" \
X_"a[242:243], v36 offset:9216\n" \
"v_add_u32_e64 v222, s5, v221\n" \
M_"a[24:27], a[168:171], a[132:135], a[24:27]\n" \
B_"v[200:203], v192, s[76:79], s34 offen offset:0\n" \
M_"a[24:27], a[232:235], a[196:199], a[24:27]\n" \
"v_and_b32_e32 v222, 0xf0, v222\n" \
"v_cmp_gt_u32_e64 vcc, 16, v217\n" \
M_"a[20:23], a[164:167], a[132:135], a[20:23]\n" \
"s_nop 0\n" \
"v_cndmask_b32_e32 v217, v221, v222, vcc\n" \
"v_or_b32_e32 v221, 0xc0, v212\n" \
M_"a[20:23], a[228:231], a[196:199], a[20:23]\n" \
X_"a[180:181], v37 offset:0\n" \
"v_mul_u32_u24_e32 v222, 0xe39, v221\n" \
M_"a[16:19], a[160:163], a[132:135], a[16:19]\n" \
"v_lshrrev_b32_e64 v222, 16, v222\n" \
"v_mad_i32_i24 v221, v222, s6, v221\n" \
M_"a[16:19], a[224:227], a[196:199], a[16:19]\n" \
"v_lshlrev_b32_e32 v223, 4, v221\n" \
"v_add_u32_e32 v224, s5, v223\n" \
M_"a[32:35], a[160:163], a[136:139], a[32:35]\n" \
B_"v[204:207], v193, s[76:79], s34 offen offset:0\n" \
M_"a[32:35], a[224:227], a[200:203], a[32:35]\n" \
"s_mov_b32 m0, s32\n" \
X_"a[182:183], v37 offset:1024\n" \
M_"a[36:39], a[164:167], a[136:139], a[36:39]\n" \
"s_mov_b32 exec_lo, 0x0\n" \
"s_mov_b32 exec_hi, 0xff00\n" \
"global_load_lds_dword v[184:185], off\n" \
"s_mov_b64 exec, -1\n" \
M_"a[36:39], a[228:231], a[200:203], a[36:39]\n" \
X_"a[244:245], v37 offset:8192\n" \
"v_and_b32_e32 v224, 0xf0, v224\n" \
M_"a[40:43], a[168:171], a[136:139], a[40:43]\n" \
"v_cmp_gt_u32_e32 vcc, 16, v221\n" \
"s_nop 1\n" \
"v_cndmask_b32_e32 v221, v223, v224, vcc\n" \
M_"a[40:43], a[232:235], a[200:203], a[40:43]\n" \
"buffer_load_dword v208, v194, s[76:79], s34 offen offset:32\n" \
M_"a[44:47], a[172:175], a[136:139], a[44:47]\n" \
X_"a[246:247], v37 offset:9216\n" \
"v_or_b32_e32 v223, 0x100, v212\n" \
M_"a[44:47], a[236:239], a[200:203], a[44:47]\n" \
"v_mul_u32_u24_e32 v224, 0xe39, v223\n" \
"v_lshrrev_b32_e64 v224, 16, v224\n" \
M_"a[60:63], a[172:175], a[140:143], a[60:63]\n" \
"v_mad_i32_i24 v223, v224, s6, v223\n" \
"v_lshlrev_b32_e64 v225, 4, v223\n" \
M_"a[60:63], a[236:239], a[204:207], a[60:63]\n" \
"v_add_u32_e64 v226, s5, v225\n" \
"v_and_b32_e32 v226, 0xf0, v226\n" \
M_"a[56:59], a[168:171], a[140:143], a[56:59]\n" \
"buffer_load_dword v209, v194, s[76:79], s34 offen offset:1184\n" \
M_"a[56:59], a[232:235], a[204:207], a[56:59]\n" \
X_"a[184:185], v38 offset:0\n" \
"v_cmp_gt_u32_e64 vcc, 16, v223\n" \
M_"a[52:55], a[164:167], a[140:143], a[52:55]\n" \
"s_nop 0\n" \
"v_cndmask_b32_e32 v223, v225, v226, vcc\n" \
"v_or_b32_e32 v225, 0x140, v212\n" \
M_"a[52:55], a[228:231], a[204:207], a[52:55]\n" \
X_"a[186:187], v38 offset:1024\n" \
"v_mul_u32_u24_e32 v226, 0xe39, v225\n" \
M_"a[48:51], a[160:163], a[140:143], a[48:51]\n" \
"v_lshrrev_b32_e64 v226, 16, v226\n" \
"v_mad_i32_i24 v225, v226, s6, v225\n" \
M_"a[48:51], a[224:227], a[204:207], a[48:51]\n" \
"v_lshlrev_b32_e32 v227, 4, v225\n" \
"v_add_u32_e32 v228, s5, v227\n" \
M_"a[64:67], a[160:163], a[144:147], a[64:67]\n" \
"buffer_load_dword v211, v194, s[76:79], s34 offen offset:2336\n" \
M_"a[64:67], a[224:227], a[208:211], a[64:67]\n" \
X_"a[248:249], v38 offset:8192\n" \
"v_and_b32_e32 v228, 0xf0, v228\n" \
M_"a[68:71], a[164:167], a[144:147], a[68:71]\n" \
"v_cmp_gt_u32_e32 vcc, 16, v225\n" \
"s_nop 1\n" \
"v_cndmask_b32_e64 v225, v227, v228, vcc\n" \
M_"a[68:71], a[228:231], a[208:211], a[68:71]\n" \
X_"a[250:251], v38 offset:9216\n" \
"v_or_b32_e32 v227, 0x180, v212\n" \
M_"a[72:75], a[168:171], a[144:147], a[72:75]\n" \
"v_mul_u32_u24_e32 v228, 0xe39, v227\n" \
"v_lshrrev_b32_e64 v228, 16, v228\n" \
M_"a[72:75], a[232:235], a[208:211], a[72:75]\n" \
"s_mov_b32 s76, s20\n" \
"s_and_b32 s77, s21, 0xffff\n" \
M_"a[76:79], a[172:175], a[144:147], a[76:79]\n" \
"s_mov_b32 s78, 0x480\n" \
"v_mad_i32_i24 v227, v228, s6, v227\n" \
M_"a[76:79], a[236:239], a[208:211], a[76:79]\n" \
"buffer_load_ushort v210, v195, s[76:79], 0 offen\n" \
M_"a[92:95], a[172:175], a[148:151], a[92:95]\n" \
X_"a[188:189], v39 offset:0\n" \
"v_lshlrev_b32_e32 v229, 4, v227\n" \
M_"a[92:95], a[236:239], a[212:215], a[92:95]\n" \
"v_add_u32_e64 v230, s5, v229\n" \
"v_and_b32_e32 v230, 0xf0, v230\n" \
M_"a[88:91], a[168:171], a[148:151], a[88:91]\n" \
"s_mov_b32 s76, s8\n" \
"s_and_b32 s77, s9, 0xffff\n" \
M_"a[88:91], a[232:235], a[212:215], a[88:91]\n" \
"s_mov_b32 s78, 0x240000\n" \
X_"a[190:191], v39 offset:1024\n" \
M_"a[84:87], a[164:167], a[148:151], a[84:87]\n" \
"v_cmp_gt_u32_e32 vcc, 16, v227\n" \
"s_nop 1\n" \
"v_cndmask_b32_e32 v227, v229, v230, vcc\n" \
M_"a[84:87], a[228:231], a[212:215], a[84:87]\n" \
X_"a[252:253], v39 offset:8192\n" \
"v_or_b32_e32 v229, 0x1c0, v212\n" \
".fill (L_pb + 7904 - .) / 4, 4, 0xBF800000\n" \
"L_pp2:\n" \
"s_cbranch_execz L_pret\n" \
M_"a[80:83], a[160:163], a[148:151], a[80:83]\n" \
"v_mul_u32_u24_e32 v230, 0xe39, v229\n" \
"v_lshrrev_b32_e32 v230, 16, v230\n" \
M_"a[80:83], a[224:227], a[212:215], a[80:83]\n" \
"v_mad_i32_i24 v229, v230, s6, v229\n" \
"v_lshlrev_b32_e64 v231, 4, v229\n" \
M_"a[96:99], a[160:163], a[152:155], a[96:99]\n" \
X_"a[254:255], v39 offset:9216\n" \
"v_add_u32_e64 v232, s5, v231\n" \
M_"a[96:99], a[224:227], a[216:219], a[96:99]\n" \
"v_and_b32_e32 v232, 0xf0, v232\n" \
"v_cmp_gt_u32_e64 vcc, 16, v229\n" \
M_"a[100:103], a[164:167], a[152:155], a[100:103]\n" \
"s_nop 0\n" \
"v_cndmask_b32_e32 v229, v231, v232, vcc\n" \
"v_or_b32_e32 v231, 0x200, v212\n" \
M_"a[100:103], a[228:231], a[216:219], a[100:103]\n" \
"v_mul_u32_u24_e32 v232, 0xe39, v231\n" \
"v_lshrrev_b32_e64 v232, 16, v232\n" \
M_"a[104:107], a[168:171], a[152:155], a[104:107]\n" \
"v_mad_i32_i24 v231, v232, s6, v231\n" \
"v_lshlrev_b32_e64 v233, 4, v231\n" \
M_"a[104:107], a[232:235], a[216:219], a[104:107]\n" \
"v_add_u32_e32 v234, s5, v233\n" \
"s_movk_i32 s5, 0x480\n" \
M_"a[108:111], a[172:175], a[152:155], a[108:111]\n" \
"v_mad_u32_u24 v0, v213, s5, v214\n" \
"v_mul_u32_u24_e32 v213, 0x480, v216\n" \
M_"a[108:111], a[236:239], a[216:219], a[108:111]\n" \
"s_movk_i32 s6, 0xfc00\n" \
"v_add3_u32 v1, v213, v215, s6\n" \
"s_waitcnt lgkmcnt(15)\n" \
M_"a[124:127], a[172:175], a[156:159], a[124:127]\n" \
"v_mul_u32_u24_e32 v213, 0x480, v220\n" \
"v_add3_u32 v2, v213, v217, s7\n" \
M_"a[124:127], a[236:239], a[220:223], a[124:127]\n" \
"v_mul_u32_u24_e32 v213, 0x480, v222\n" \
"v_add3_u32 v3, v213, v221, s40\n" \
M_"a[120:123], a[168:171], a[156:159], a[120:123]\n" \
"v_mul_u32_u24_e32 v213, 0x480, v226\n" \
"v_add3_u32 v5, v213, v225, s6\n" \
M_"a[120:123], a[232:235], a[220:223], a[120:123]\n" \
"v_mul_u32_u24_e32 v213, 0x480, v228\n" \
"v_add3_u32 v6, v213, v227, s7\n" \
M_"a[116:119], a[164:167], a[156:159], a[116:119]\n" \
"v_mul_u32_u24_e32 v213, 0x480, v230\n" \
"v_add3_u32 v7, v213, v229, s40\n" \
M_"a[116:119], a[228:231], a[220:223], a[116:119]\n" \
"s_lshl_b32 s7, s3, 3\n" \
"s_lshl_b32 s40, s3, 1\n" \
M_"a[112:115], a[160:163], a[156:159], a[112:115]\n" \
"s_and_b32 s7, s7, 16\n" \
"s_and_b32 s40, s40, 2\n" \
"s_waitcnt vmcnt(7) lgkmcnt(0)\n" \
M_"a[112:115], a[224:227], a[220:223], a[112:115]\n" \
"s_barrier\n" \
M_"v[52:55], a[176:179], a[128:131], v[52:55]\n" \
"s_add_u32 m0, s30, 0x0\n" \
"s_movk_i32 s84, 0x200\n" \
M_"v[52:55], a[240:243], a[192:195], v[52:55]\n" \
B_"v22, s[64:67], s84 offen offset:0 lds\n" \
M_"v[56:59], a[180:183], a[128:131], v[56:59]\n" \
X_"a[160:161], v32 offset:32768\n" \
"s_and_b32 s6, s2, 7\n" \
M_"v[56:59], a[244:247], a[192:195], v[56:59]\n" \
"s_movk_i32 s84, 0x1e00\n" \
M_"v[60:63], a[184:187], a[128:131], v[60:63]\n" \
B_"v23, s[64:67], s84 offen offset:1024 lds\n" \
M_"v[60:63], a[248:251], a[192:195], v[60:63]\n" \
X_"a[162:163], v32 offset:33792\n" \
"v_bfe_u32 v213, v20, 7, 1\n" \
M_"v[64:67], a[188:191], a[128:131], v[64:67]\n" \
X_"a[224:225], v32 offset:40960\n" \
"s_or_b32 s7, s40, s7\n" \
M_"v[64:67], a[252:255], a[192:195], v[64:67]\n" \
R_"a[128:131], v28 offset:49152\n" \
"v_and_b32_e32 v234, 0xf0, v234\n" \
M_"v[80:83], a[188:191], a[132:135], v[80:83]\n" \
R_"a[192:195], v29 offset:49152\n" \
"v_cmp_gt_u32_e32 vcc, 16, v231\n" \
M_"v[80:83], a[252:255], a[196:199], v[80:83]\n" \
"s_movk_i32 s84, 0x3a00\n" \
M_"v[76:79], a[184:187], a[132:135], v[76:79]\n" \
B_"v22, s[64:67], s84 offen offset:2048 lds\n" \
M_"v[76:79], a[248:251], a[196:199], v[76:79]\n" \
"s_movk_i32 s84, 0x5600\n" \
M_"v[72:75], a[180:183], a[132:135], v[72:75]\n" \
B_"v23, s[64:67], s84 offen offset:3072 lds\n" \
M_"v[72:75], a[244:247], a[196:199], v[72:75]\n" \
"s_add_u32 m0, s31, 0x0\n" \
"s_mov_b32 s84, 0x200000\n" \
M_"v[68:71], a[176:179], a[132:135], v[68:71]\n" \
B_"v26, s[68:71], s84 offen offset:0 lds\n" \
M_"v[68:71], a[240:243], a[196:199], v[68:71]\n" \
R_"a[132:135], v28 offset:51200\n" \
"v_or3_b32 v213, s7, v213, v219\n" \
M_"v[84:87], a[176:179], a[136:139], v[84:87]\n" \
R_"a[196:199], v29 offset:51200\n" \
"v_and_b32_e32 v214, 32, v20\n" \
M_"v[84:87], a[240:243], a[200:203], v[84:87]\n" \
"s_addk_i32 s84, 0x7c00\n" \
M_"v[88:91], a[180:183], a[136:139], v[88:91]\n" \
B_"v26, s[68:71], s84 offen offset:1024 lds\n" \
M_"v[88:91], a[244:247], a[200:203], v[88:91]\n" \
X_"a[226:227], v32 offset:41984\n" \
"s_lshl_b32 s6, s6, 5\n" \
M_"v[92:95], a[184:187], a[136:139], v[92:95]\n" \
"s_addk_i32 s84, 0x7c00\n" \
M_"v[92:95], a[248:251], a[200:203], v[92:95]\n" \
B_"v27, s[68:71], s84 offen offset:2048 lds\n" \
M_"v[96:99], a[188:191], a[136:139], v[96:99]\n" \
X_"a[164:165], v33 offset:32768\n" \
"v_cndmask_b32_e32 v231, v233, v234, vcc\n" \
M_"v[96:99], a[252:255], a[200:203], v[96:99]\n" \
R_"a[136:139], v28 offset:53248\n" \
"v_mul_u32_u24_e64 v213, 18, v213\n" \
M_"v[112:115], a[188:191], a[140:143], v[112:115]\n" \
R_"a[200:203], v29 offset:53248\n" \
"v_mov_b32_e32 v215, 0x100\n" \
M_"v[112:115], a[252:255], a[204:207], v[112:115]\n" \
"s_addk_i32 s84, 0x7c00\n" \
M_"v[108:111], a[184:187], a[140:143], v[108:111]\n" \
B_"v27, s[68:71], s84 offen offset:3072 lds\n" \
M_"v[108:111], a[248:251], a[204:207], v[108:111]\n" \
"s_add_u32 m0, s30, 0x1000\n" \
"s_mov_b32 s84, 0x8200\n" \
M_"v[104:107], a[180:183], a[140:143], v[104:107]\n" \
B_"v22, s[64:67], s84 offen offset:0 lds\n" \
M_"v[104:107], a[244:247], a[204:207], v[104:107]\n" \
X_"a[166:167], v33 offset:33792\n" \
"v_mov_b32_e32 v216, s6\n" \
M_"v[100:103], a[176:179], a[140:143], v[100:103]\n" \
X_"a[228:229], v33 offset:40960\n" \
"v_cmp_eq_u32_e64 vcc, 0, v214\n" \
M_"v[100:103], a[240:243], a[204:207], v[100:103]\n" \
R_"a[140:143], v28 offset:55296\n" \
"v_and_or_b32 v213, v218, 1, v213\n" \
M_"v[116:119], a[176:179], a[144:147], v[116:119]\n" \
R_"a[204:207], v29 offset:55296\n" \
"v_lshlrev_b32_e64 v212, 3, v212\n" \
M_"v[116:119], a[240:243], a[208:211], v[116:119]\n" \
"s_addk_i32 s84, 0x1c00\n" \
M_"v[120:123], a[180:183], a[144:147], v[120:123]\n" \
B_"v23, s[64:67], s84 offen offset:1024 lds\n" \
M_"v[120:123], a[244:247], a[208:211], v[120:123]\n" \
"s_addk_i32 s84, 0x1c00\n" \
M_"v[124:127], a[184:187], a[144:147], v[124:127]\n" \
B_"v22, s[64:67], s84 offen offset:2048 lds\n" \
M_"v[124:127], a[248:251], a[208:211], v[124:127]\n" \
"s_addk_i32 s84, 0x1c00\n" \
M_"v[128:131], a[188:191], a[144:147], v[128:131]\n" \
B_"v23, s[64:67], s84 offen offset:3072 lds\n" \
M_"v[128:131], a[252:255], a[208:211], v[128:131]\n" \
R_"a[144:147], v28 offset:32768\n" \
"v_cndmask_b32_e32 v214, v215, v216, vcc\n" \
M_"v[144:147], a[188:191], a[148:151], v[144:147]\n" \
R_"a[208:211], v29 offset:32768\n" \
"v_mad_u32_u24 v213, v213, s5, v214\n" \
M_"v[144:147], a[252:255], a[212:215], v[144:147]\n" \
"s_add_u32 m0, s31, 0x4000\n" \
"s_mov_b32 s84, 0x200000\n" \
M_"v[140:143], a[184:187], a[148:151], v[140:143]\n" \
B_"v26, s[72:75], s84 offen offset:0 lds\n" \
M_"v[140:143], a[248:251], a[212:215], v[140:143]\n" \
X_"a[230:231], v33 offset:41984\n" \
"v_and_or_b32 v213, v20, 16, v213\n" \
M_"v[136:139], a[180:183], a[148:151], v[136:139]\n" \
"s_addk_i32 s84, 0x7c00\n" \
"s_nop 0\n" \
M_"v[136:139], a[244:247], a[212:215], v[136:139]\n" \
B_"v26, s[72:75], s84 offen offset:1024 lds\n" \
M_"v[132:135], a[176:179], a[148:151], v[132:135]\n" \
X_"a[168:169], v34 offset:32768\n" \
"v_add_u32_e32 v9, 0x4800, v213\n" \
M_"v[132:135], a[240:243], a[212:215], v[132:135]\n" \
R_"a[148:151], v28 offset:34816\n" \
"v_mul_u32_u24_e32 v213, 0x120, v218\n" \
M_"v[148:151], a[176:179], a[152:155], v[148:151]\n" \
R_"a[212:215], v29 offset:34816\n" \
"v_and_or_b32 v10, v212, 24, v213\n" \
M_"v[148:151], a[240:243], a[216:219], v[148:151]\n" \
X_"a[170:171], v34 offset:33792\n" \
"v_lshrrev_b32_e64 v213, 2, v20\n" \
M_"v[152:155], a[180:183], a[152:155], v[152:155]\n" \
X_"a[232:233], v34 offset:40960\n" \
"v_and_or_b32 v213, v213, 60, s4\n" \
M_"v[152:155], a[244:247], a[216:219], v[152:155]\n" \
X_"a[234:235], v34 offset:41984\n" \
"v_lshl_or_b32 v212, s3, 9, v212\n" \
M_"v[156:159], a[184:187], a[152:155], v[156:159]\n" \
X_"a[172:173], v35 offset:32768\n" \
"v_mad_u32_u24 v4, v224, s5, v223\n" \
M_"v[156:159], a[248:251], a[216:219], v[156:159]\n" \
X_"a[174:175], v35 offset:33792\n" \
"v_mad_u32_u24 v8, v232, s5, v231\n" \
M_"v[160:163], a[188:191], a[152:155], v[160:163]\n" \
X_"a[236:237], v35 offset:40960\n" \
"v_or_b32_e32 v18, 0x27000, v213\n" \
M_"v[160:163], a[252:255], a[216:219], v[160:163]\n" \
R_"a[152:155], v28 offset:36864\n" \
"v_or_b32_e32 v19, 0x27800, v212\n" \
M_"v[176:179], a[188:191], a[156:159], v[176:179]\n" \
R_"a[216:219], v29 offset:36864\n" \
"v_mov_b32_e32 v215, 0x1000c0c\n" \
M_"v[176:179], a[252:255], a[220:223], v[176:179]\n" \
X_"a[238:239], v35 offset:41984\n" \
"v_mov_b32_e32 v216, 0xc0c0100\n" \
M_"v[172:175], a[184:187], a[156:159], v[172:175]\n" \
"v_bfe_u32 v214, v20, 4, 1\n" \
"v_lshrrev_b32_e64 v212, 8, v20\n" \
M_"v[172:175], a[248:251], a[220:223], v[172:175]\n" \
"v_cmp_eq_u32_e64 vcc, 0, v214\n" \
"v_bfe_u32 v213, v20, 4, 4\n" \
M_"v[168:171], a[180:183], a[156:159], v[168:171]\n" \
"v_mov_b32_e32 v217, 0xc0c0302\n" \
"v_cndmask_b32_e64 v215, v215, v216, vcc\n" \
M_"v[168:171], a[244:247], a[220:223], v[168:171]\n" \
"v_mov_b32_e32 v216, 0x3020c0c\n" \
"v_lshl_or_b32 v214, v212, 2, v214\n" \
M_"v[164:167], a[176:179], a[156:159], v[164:167]\n" \
"v_cndmask_b32_e64 v216, v216, v217, vcc\n" \
"v_mov_b32_e32 v217, 0xc0c0c0c\n" \
M_"v[164:167], a[240:243], a[220:223], v[164:167]\n" \
R_"a[156:159], v28 offset:38912\n" \
R_"a[220:223], v29 offset:38912\n" \
"s_waitcnt lgkmcnt(15)\n" \
M_"a[0:3], a[160:163], a[128:131], a[0:3]\n" \
"s_addk_i32 s84, 0x7c00\n" \
M_"a[0:3], a[224:227], a[192:195], a[0:3]\n" \
B_"v27, s[72:75], s84 offen offset:2048 lds\n" \
M_"a[4:7], a[164:167], a[128:131], a[4:7]\n" \
X_"a[176:177], v36 offset:32768\n" \
"v_cmp_gt_u32_e32 vcc, v214, v213\n" \
"s_waitcnt lgkmcnt(15)\n" \
M_"a[4:7], a[228:231], a[192:195], a[4:7]\n" \
"s_addk_i32 s84, 0x7c00\n" \
"s_waitcnt lgkmcnt(11)\n" \
M_"a[8:11], a[168:171], a[128:131], a[8:11]\n" \
B_"v27, s[72:75], s84 offen offset:3072 lds\n" \
"s_waitcnt lgkmcnt(9)\n" \
M_"a[8:11], a[232:235], a[192:195], a[8:11]\n" \
X_"a[178:179], v36 offset:33792\n" \
"v_or_b32_e32 v214, 2, v214\n" \
"s_waitcnt lgkmcnt(8)\n" \
M_"a[12:15], a[172:175], a[128:131], a[12:15]\n" \
X_"a[240:241], v36 offset:40960\n" \
"s_lshr_b32 s4, s2, 5\n" \
"s_waitcnt lgkmcnt(5)\n" \
M_"a[12:15], a[236:239], a[192:195], a[12:15]\n" \
"v_cndmask_b32_e32 v40, v217, v215, vcc\n" \
"v_cndmask_b32_e64 v42, v215, v217, vcc\n" \
M_"a[28:31], a[172:175], a[132:135], a[28:31]\n" \
"v_cmp_gt_u32_e32 vcc, v214, v213\n" \
"v_and_b32_e32 v214, 16, v20\n" \
M_"a[28:31], a[236:239], a[196:199], a[28:31]\n" \
X_"a[242:243], v36 offset:41984\n" \
"s_movk_i32 s42, 0x3f80\n" \
M_"a[24:27], a[168:171], a[132:135], a[24:27]\n" \
"s_xor_b32 s5, s4, 15\n" \
M_"a[24:27], a[232:235], a[196:199], a[24:27]\n" \
"s_cmpk_lt_u32 s3, 2\n" \
"s_cselect_b32 s6, s5, s4\n" \
M_"a[20:23], a[164:167], a[132:135], a[20:23]\n" \
"s_lshl_b32 s7, s6, 7\n" \
"v_and_b32_e32 v213, 0x320, v20\n" \
M_"a[20:23], a[228:231], a[196:199], a[20:23]\n" \
X_"a[180:181], v37 offset:32768\n" \
"s_movk_i32 s41, 0x100\n" \
M_"a[16:19], a[160:163], a[132:135], a[16:19]\n" \
"v_lshlrev_b32_e64 v214, v214, s42\n" \
"v_mov_b32_e64 v215, 0\n" \
M_"a[16:19], a[224:227], a[196:199], a[16:19]\n" \
"s_add_i32 s40, s7, -16\n" \
"v_cndmask_b32_e32 v41, v217, v216, vcc\n" \
M_"a[32:35], a[160:163], a[136:139], a[32:35]\n" \
X_"a[182:183], v37 offset:33792\n" \
"v_cndmask_b32_e64 v43, v216, v217, vcc\n" \
M_"a[32:35], a[224:227], a[200:203], a[32:35]\n" \
"v_cmp_eq_u32_e32 vcc, s41, v213\n" \
"s_mov_b32 s41, 0\n" \
M_"a[36:39], a[164:167], a[136:139], a[36:39]\n" \
"s_and_b32 s43, s3, 1\n" \
"v_cndmask_b32_e32 v46, v215, v214, vcc\n" \
M_"a[36:39], a[228:231], a[200:203], a[36:39]\n" \
X_"a[244:245], v37 offset:40960\n" \
"v_and_b32_e32 v213, 0x330, v20\n" \
M_"a[40:43], a[168:171], a[136:139], a[40:43]\n" \
"s_mov_b32 m0, s32\n" \
M_"a[40:43], a[232:235], a[200:203], a[40:43]\n" \
"s_mov_b32 exec_lo, 0xffffffff\n" \
"s_mov_b32 exec_hi, 0x0\n" \
"global_load_lds_dword v[188:189], off\n" \
"s_mov_b64 exec, -1\n" \
M_"a[44:47], a[172:175], a[136:139], a[44:47]\n" \
X_"a[246:247], v37 offset:41984\n" \
M_"a[44:47], a[236:239], a[200:203], a[44:47]\n" \
"s_cmp_lg_u32 s7, 0\n" \
"s_cselect_b32 s40, s40, s41\n" \
M_"a[60:63], a[172:175], a[140:143], a[60:63]\n" \
"s_mov_b32 s42, 0xff800000\n" \
M_"a[60:63], a[236:239], a[204:207], a[60:63]\n" \
"s_cmp_lg_u32 s7, 0\n" \
"s_cselect_b32 s47, s41, s42\n" \
M_"a[56:59], a[168:171], a[140:143], a[56:59]\n" \
X_"a[184:185], v38 offset:32768\n" \
"s_lshl_b32 s41, s43, 6\n" \
M_"a[56:59], a[232:235], a[204:207], a[56:59]\n" \
"v_lshrrev_b32_e32 v215, 6, v20\n" \
"v_mov_b32_e32 v214, 0x3f80\n" \
M_"a[52:55], a[164:167], a[140:143], a[52:55]\n" \
"v_cmp_eq_u32_e32 vcc, 32, v213\n" \
"s_or_b32 s7, s41, s7\n" \
M_"a[52:55], a[228:231], a[204:207], a[52:55]\n" \
X_"a[186:187], v38 offset:33792\n" \
"v_lshrrev_b32_e64 v213, 7, v20\n" \
M_"a[48:51], a[160:163], a[140:143], a[48:51]\n" \
"v_bfe_u32 v216, v20, 6, 2\n" \
"v_lshrrev_b32_e64 v219, 1, v20\n" \
M_"a[48:51], a[224:227], a[204:207], a[48:51]\n" \
"v_bfe_u32 v215, v215, 1, 1\n" \
"v_cndmask_b32_e64 v47, 0, v214, vcc\n" \
M_"a[64:67], a[160:163], a[144:147], a[64:67]\n" \
X_"a[248:249], v38 offset:40960\n" \
"v_or_b32_e64 v214, s7, v213\n" \
M_"a[64:67], a[224:227], a[208:211], a[64:67]\n" \
"v_lshl_or_b32 v217, s43, 3, v213\n" \
"v_lshlrev_b32_e64 v218, 10, v212\n" \
M_"a[68:71], a[164:167], a[144:147], a[68:71]\n" \
"v_lshlrev_b32_e32 v216, 7, v216\n" \
"v_and_b32_e32 v219, 24, v219\n" \
M_"a[68:71], a[228:231], a[208:211], a[68:71]\n" \
X_"a[250:251], v38 offset:41984\n" \
"v_and_or_b32 v213, v213, 2, v215\n" \
M_"a[72:75], a[168:171], a[144:147], a[72:75]\n" \
"v_add_u32_e64 v217, s40, v217\n" \
"v_or3_b32 v216, v216, v218, v219\n" \
M_"a[72:75], a[232:235], a[208:211], a[72:75]\n" \
"v_lshlrev_b32_e64 v213, 5, v213\n" \
"s_mov_b32 s40, 0x14000\n" \
M_"a[76:79], a[172:175], a[144:147], a[76:79]\n" \
"s_lshl_b32 s7, s2, 2\n" \
"v_or3_b32 v48, v216, v213, s40\n" \
M_"a[76:79], a[236:239], a[208:211], a[76:79]\n" \
X_"a[188:189], v39 offset:32768\n" \
"s_lshl_b32 s40, s43, 2\n" \
M_"a[92:95], a[172:175], a[148:151], a[92:95]\n" \
"s_lshl_b32 s6, s6, 3\n" \
"v_or_b32_e32 v213, s40, v212\n" \
M_"a[92:95], a[236:239], a[212:215], a[92:95]\n" \
"s_or_b32 s6, s6, s40\n" \
"s_and_b32 s40, s7, 0x7c\n" \
M_"a[88:91], a[168:171], a[148:151], a[88:91]\n" \
X_"a[190:191], v39 offset:33792\n" \
"s_or_b32 s40, s40, s3\n" \
M_"a[88:91], a[232:235], a[212:215], a[88:91]\n" \
"s_cmpk_lt_u32 s40, 64\n" \
"s_cselect_b32 s41, s4, s5\n" \
M_"a[84:87], a[164:167], a[148:151], a[84:87]\n" \
"s_waitcnt vmcnt(17)\n" \
"v_or_b32_e32 v204, v210, v204\n" \
"v_or_b32_e64 v206, v210, v206\n" \
M_"a[84:87], a[228:231], a[212:215], a[84:87]\n" \
"v_mov_b32_e64 v210, 0\n" \
"s_add_u32 s88, s35, 0x1000\n" \
M_"a[80:83], a[160:163], a[148:151], a[80:83]\n" \
X_"a[252:253], v39 offset:40960\n" \
"s_lshl_b32 s40, s40, 1\n" \
M_"a[80:83], a[224:227], a[212:215], a[80:83]\n" \
"ds_write_b128 v196, v[200:203]\n" \
M_"a[96:99], a[160:163], a[152:155], a[96:99]\n" \
X_"a[254:255], v39 offset:41984\n" \
"s_lshl_b32 s41, s41, 7\n" \
M_"a[96:99], a[224:227], a[216:219], a[96:99]\n" \
"s_and_b32 s40, s40, 0x7e\n" \
"s_or_b32 s40, s41, s40\n" \
M_"a[100:103], a[164:167], a[152:155], a[100:103]\n" \
"ds_write_b128 v197, v[204:207]\n" \
M_"a[100:103], a[228:231], a[216:219], a[100:103]\n" \
"s_mulk_i32 s40, 0x480\n" \
"v_add_u32_e64 v49, s40, v20\n" \
M_"a[104:107], a[168:171], a[152:155], a[104:107]\n" \
"s_mov_b32 s40, 0xc000\n" \
"s_and_b32 s7, s7, 28\n" \
M_"a[104:107], a[232:235], a[216:219], a[104:107]\n" \
"ds_write_b128 v198, v[208:211]\n" \
M_"a[108:111], a[172:175], a[152:155], a[108:111]\n" \
"s_bfe_u32 s45, s2, 0x20003\n" \
"s_mov_b32 s41, 0x8000\n" \
M_"a[108:111], a[236:239], a[216:219], a[108:111]\n" \
"s_cmp_lg_u32 s4, 0\n" \
"s_cselect_b32 s51, s40, s41\n" \
"s_waitcnt lgkmcnt(15)\n" \
M_"a[124:127], a[172:175], a[156:159], a[124:127]\n" \
"s_movk_i32 s40, 0x4000\n" \
"s_lshr_b32 s44, s3, 1\n" \
M_"a[124:127], a[236:239], a[220:223], a[124:127]\n" \
"s_or_b32 s46, s7, s45\n" \
M_"a[120:123], a[168:171], a[156:159], a[120:123]\n" \
"s_cmp_lg_u32 s4, 0\n" \
"s_cselect_b32 s53, s41, s40\n" \
M_"a[120:123], a[232:235], a[220:223], a[120:123]\n" \
"s_lshl_b32 s40, s44, 15\n" \
"s_lshl_b32 s41, s43, 14\n" \
M_"a[116:119], a[164:167], a[156:159], a[116:119]\n" \
"s_lshl_b32 s48, s46, 20\n" \
"s_or_b32 s41, s40, s41\n" \
M_"a[116:119], a[228:231], a[220:223], a[116:119]\n" \
"s_add_i32 s49, s48, 0x1000000\n" \
"s_or_b32 s42, s49, s41\n" \
M_"a[112:115], a[160:163], a[156:159], a[112:115]\n" \
"s_lshl_b32 s4, s4, 16\n" \
"s_or_b32 s41, s42, s4\n" \
"s_waitcnt vmcnt(1) lgkmcnt(0)\n" \
M_"a[112:115], a[224:227], a[220:223], a[112:115]\n" \
"s_barrier\n" \
M_"v[52:55], a[176:179], a[128:131], v[52:55]\n" \
"s_add_u32 m0, s30, 0x8000\n" \
"s_movk_i32 s84, 0x280\n" \
M_"v[52:55], a[240:243], a[192:195], v[52:55]\n" \
B_"v22, s[64:67], s84 offen offset:0 lds\n" \
M_"v[56:59], a[180:183], a[128:131], v[56:59]\n" \
"s_movk_i32 s84, 0x1e80\n" \
M_"v[56:59], a[244:247], a[192:195], v[56:59]\n" \
B_"v23, s[64:67], s84 offen offset:1024 lds\n" \
M_"v[60:63], a[184:187], a[128:131], v[60:63]\n" \
X_"a[160:161], v32 offset:0\n" \
"s_lshl_b32 s4, s5, 16\n" \
M_"v[60:63], a[248:251], a[192:195], v[60:63]\n" \
"s_movk_i32 s84, 0x3a80\n" \
M_"v[64:67], a[188:191], a[128:131], v[64:67]\n" \
B_"v22, s[64:67], s84 offen offset:2048 lds\n" \
M_"v[64:67], a[252:255], a[192:195], v[64:67]\n" \
R_"a[128:131], v28 offset:16384\n" \
"s_mul_i32 s5, s6, 0x24800\n" \
M_"v[80:83], a[188:191], a[132:135], v[80:83]\n" \
R_"a[192:195], v29 offset:16384\n" \
"s_lshl_b32 s6, s46, 9\n" \
M_"v[80:83], a[252:255], a[196:199], v[80:83]\n" \
"s_movk_i32 s84, 0x5680\n" \
M_"v[76:79], a[184:187], a[132:135], v[76:79]\n" \
B_"v23, s[64:67], s84 offen offset:3072 lds\n" \
M_"v[76:79], a[248:251], a[196:199], v[76:79]\n" \
"s_add_u32 m0, s31, 0x8000\n" \
"s_mov_b32 s84, 0x280000\n" \
M_"v[72:75], a[180:183], a[132:135], v[72:75]\n" \
B_"v26, s[68:71], s84 offen offset:0 lds\n" \
M_"v[72:75], a[244:247], a[196:199], v[72:75]\n" \
X_"a[162:163], v32 offset:1024\n" \
"s_lshl_b32 s7, s7, 6\n" \
M_"v[68:71], a[176:179], a[132:135], v[68:71]\n" \
X_"a[224:225], v32 offset:8192\n" \
"s_add_i32 s6, s5, s6\n" \
M_"v[68:71], a[240:243], a[196:199], v[68:71]\n" \
R_"a[132:135], v28 offset:18432\n" \
"s_or_b32 s5, s5, s7\n" \
M_"v[84:87], a[176:179], a[136:139], v[84:87]\n" \
R_"a[196:199], v29 offset:18432\n" \
"s_lshl_b32 s7, s45, 2\n" \
M_"v[84:87], a[240:243], a[200:203], v[84:87]\n" \
"s_addk_i32 s84, 0x7c00\n" \
M_"v[88:91], a[180:183], a[136:139], v[88:91]\n" \
B_"v26, s[68:71], s84 offen offset:1024 lds\n" \
M_"v[88:91], a[244:247], a[200:203], v[88:91]\n" \
"s_addk_i32 s84, 0x7c00\n" \
M_"v[92:95], a[184:187], a[136:139], v[92:95]\n" \
B_"v27, s[68:71], s84 offen offset:2048 lds\n" \
M_"v[92:95], a[248:251], a[200:203], v[92:95]\n" \
"s_addk_i32 s84, 0x7c00\n" \
M_"v[96:99], a[188:191], a[136:139], v[96:99]\n" \
B_"v27, s[68:71], s84 offen offset:3072 lds\n" \
M_"v[96:99], a[252:255], a[200:203], v[96:99]\n" \
R_"a[136:139], v28 offset:20480\n" \
"s_or_b32 s5, s5, s7\n" \
M_"v[112:115], a[188:191], a[140:143], v[112:115]\n" \
R_"a[200:203], v29 offset:20480\n" \
"s_lshl_b32 s7, s3, 12\n" \
M_"v[112:115], a[252:255], a[204:207], v[112:115]\n" \
"s_add_u32 m0, s30, 0x9000\n" \
"s_mov_b32 s84, 0x8280\n" \
M_"v[108:111], a[184:187], a[140:143], v[108:111]\n" \
B_"v22, s[64:67], s84 offen offset:0 lds\n" \
M_"v[108:111], a[248:251], a[204:207], v[108:111]\n" \
"s_addk_i32 s84, 0x1c00\n" \
M_"v[104:107], a[180:183], a[140:143], v[104:107]\n" \
B_"v23, s[64:67], s84 offen offset:1024 lds\n" \
M_"v[104:107], a[244:247], a[204:207], v[104:107]\n" \
"s_addk_i32 s84, 0x1c00\n" \
M_"v[100:103], a[176:179], a[140:143], v[100:103]\n" \
B_"v22, s[64:67], s84 offen offset:2048 lds\n" \
M_"v[100:103], a[240:243], a[204:207], v[100:103]\n" \
R_"a[140:143], v28 offset:22528\n" \
"s_or_b32 s50, s49, s7\n" \
M_"v[116:119], a[176:179], a[144:147], v[116:119]\n" \
R_"a[204:207], v29 offset:22528\n" \
"s_lshl_b32 s7, s3, 11\n" \
M_"v[116:119], a[240:243], a[208:211], v[116:119]\n" \
"s_addk_i32 s84, 0x1c00\n" \
M_"v[120:123], a[180:183], a[144:147], v[120:123]\n" \
B_"v23, s[64:67], s84 offen offset:3072 lds\n" \
M_"v[120:123], a[244:247], a[208:211], v[120:123]\n" \
"s_add_u32 m0, s31, 0xc000\n" \
"s_mov_b32 s84, 0x280000\n" \
M_"v[124:127], a[184:187], a[144:147], v[124:127]\n" \
B_"v26, s[72:75], s84 offen offset:0 lds\n" \
M_"v[124:127], a[248:251], a[208:211], v[124:127]\n" \
"s_addk_i32 s84, 0x7c00\n" \
M_"v[128:131], a[188:191], a[144:147], v[128:131]\n" \
B_"v26, s[72:75], s84 offen offset:1024 lds\n" \
M_"v[128:131], a[252:255], a[208:211], v[128:131]\n" \
R_"a[144:147], v28 offset:0\n" \
"v_lshrrev_b32_e32 v215, 4, v20\n" \
M_"v[144:147], a[188:191], a[148:151], v[144:147]\n" \
R_"a[208:211], v29 offset:0\n" \
"v_bfe_u32 v216, v20, 4, 3\n" \
M_"v[144:147], a[252:255], a[212:215], v[144:147]\n" \
R_"v[200:203], v199 offset:0\n" \
"s_or_b32 s42, s42, s4\n" \
M_"v[140:143], a[184:187], a[148:151], v[140:143]\n" \
R_"v[204:207], v199 offset:4096\n" \
"s_lshl_b32 s4, s43, 11\n" \
M_"v[140:143], a[248:251], a[212:215], v[140:143]\n" \
X_"a[226:227], v32 offset:9216\n" \
"s_add_i32 s52, s48, s7\n" \
M_"v[136:139], a[180:183], a[148:151], v[136:139]\n" \
X_"a[164:165], v33 offset:0\n" \
"s_add_i32 s53, s53, s7\n" \
M_"v[136:139], a[244:247], a[212:215], v[136:139]\n" \
R_"v[208:211], v199 offset:8192\n" \
"v_mul_u32_u24_e32 v214, 0x480, v214\n" \
M_"v[132:135], a[176:179], a[148:151], v[132:135]\n" \
X_"a[166:167], v33 offset:1024\n" \
"v_bitop3_b32 v218, v212, v215, 7 bitop3:0x78\n" \
M_"v[132:135], a[240:243], a[212:215], v[132:135]\n" \
R_"a[148:151], v28 offset:2048\n" \
"v_bitop3_b32 v212, v212, v216, 4 bitop3:0x36\n" \
M_"v[148:151], a[176:179], a[152:155], v[148:151]\n" \
R_"a[212:215], v29 offset:2048\n" \
"s_lshl_b32 s7, s3, 10\n" \
M_"v[148:151], a[240:243], a[216:219], v[148:151]\n" \
X_"a[228:229], v33 offset:8192\n" \
"s_or_b32 s4, s40, s4\n" \
M_"v[152:155], a[180:183], a[152:155], v[152:155]\n" \
X_"a[230:231], v33 offset:9216\n" \
"v_lshl_or_b32 v25, v212, 4, v214\n" \
M_"v[152:155], a[244:247], a[216:219], v[152:155]\n" \
X_"a[168:169], v34 offset:0\n" \
"v_mul_u32_u24_e32 v212, 0x480, v217\n" \
M_"v[156:159], a[184:187], a[152:155], v[156:159]\n" \
X_"a[170:171], v34 offset:1024\n" \
"v_bitop3_b32 v213, v215, v213, 7 bitop3:0x6c\n" \
M_"v[156:159], a[248:251], a[216:219], v[156:159]\n" \
X_"a[232:233], v34 offset:8192\n" \
"s_or_b32 s40, s7, 0x14000\n" \
M_"v[160:163], a[188:191], a[152:155], v[160:163]\n" \
X_"a[234:235], v34 offset:9216\n" \
"s_lshl_b32 s43, s44, 13\n" \
M_"v[160:163], a[252:255], a[216:219], v[160:163]\n" \
R_"a[152:155], v28 offset:4096\n" \
"s_mulk_i32 s44, 0xe800\n" \
M_"v[176:179], a[188:191], a[156:159], v[176:179]\n" \
R_"a[216:219], v29 offset:4096\n" \
"s_lshl_b32 s7, s3, 8\n" \
"s_nop 0\n" \
M_"v[176:179], a[252:255], a[220:223], v[176:179]\n" \
X_"a[172:173], v35 offset:0\n" \
"v_lshl_or_b32 v24, v218, 4, v214\n" \
M_"v[172:175], a[184:187], a[156:159], v[172:175]\n" \
X_"a[174:175], v35 offset:1024\n" \
"v_lshl_or_b32 v21, v213, 4, v212\n" \
M_"v[172:175], a[248:251], a[220:223], v[172:175]\n" \
X_"a[236:237], v35 offset:8192\n" \
"s_add_i32 s44, s44, 0x17000\n" \
M_"v[168:171], a[180:183], a[156:159], v[168:171]\n" \
X_"a[238:239], v35 offset:9216\n" \
"s_or_b32 s45, s7, 0x27000\n" \
M_"v[168:171], a[244:247], a[220:223], v[168:171]\n" \
"s_or_b32 s46, s4, 0x10000\n" \
"s_add_i32 s48, s6, 0x3000000\n" \
M_"v[164:167], a[176:179], a[156:159], v[164:167]\n" \
"s_add_i32 s49, s5, 0x3004000\n" \
"s_add_i32 s52, s52, 0x1010000\n" \
".fill (L_pb + 12000 - .) / 4, 4, 0xBF800000\n" \
"L_pp3:\n" \
"s_cbranch_execz L_pret\n" \
M_"v[164:167], a[240:243], a[220:223], v[164:167]\n" \
R_"a[156:159], v28 offset:6144\n" \
R_"a[220:223], v29 offset:6144\n" \
"s_waitcnt lgkmcnt(15)\n" \
M_"a[0:3], a[160:163], a[128:131], a[0:3]\n" \
"s_addk_i32 s84, 0x7c00\n" \
M_"a[0:3], a[224:227], a[192:195], a[0:3]\n" \
B_"v27, s[72:75], s84 offen offset:2048 lds\n" \
M_"a[4:7], a[164:167], a[128:131], a[4:7]\n" \
X_"a[176:177], v36 offset:0\n" \
"s_waitcnt lgkmcnt(13)\n" \
M_"a[4:7], a[228:231], a[192:195], a[4:7]\n" \
"s_addk_i32 s84, 0x7c00\n" \
"s_waitcnt lgkmcnt(11)\n" \
M_"a[8:11], a[168:171], a[128:131], a[8:11]\n" \
B_"v27, s[72:75], s84 offen offset:3072 lds\n" \
"s_waitcnt lgkmcnt(9)\n" \
M_"a[8:11], a[232:235], a[192:195], a[8:11]\n" \
X_"a[178:179], v36 offset:1024\n" \
"s_waitcnt lgkmcnt(6)\n" \
M_"a[12:15], a[172:175], a[128:131], a[12:15]\n" \
X_"a[240:241], v36 offset:8192\n" \
"s_waitcnt lgkmcnt(5)\n" \
"s_nop 0\n" \
M_"a[12:15], a[236:239], a[192:195], a[12:15]\n" \
M_"a[28:31], a[172:175], a[132:135], a[28:31]\n" \
M_"a[28:31], a[236:239], a[196:199], a[28:31]\n" \
X_"a[242:243], v36 offset:9216\n" \
M_"a[24:27], a[168:171], a[132:135], a[24:27]\n" \
M_"a[24:27], a[232:235], a[196:199], a[24:27]\n" \
M_"a[20:23], a[164:167], a[132:135], a[20:23]\n" \
M_"a[20:23], a[228:231], a[196:199], a[20:23]\n" \
X_"a[180:181], v37 offset:0\n" \
M_"a[16:19], a[160:163], a[132:135], a[16:19]\n" \
M_"a[16:19], a[224:227], a[196:199], a[16:19]\n" \
M_"a[32:35], a[160:163], a[136:139], a[32:35]\n" \
X_"a[182:183], v37 offset:1024\n" \
M_"a[32:35], a[224:227], a[200:203], a[32:35]\n" \
M_"a[36:39], a[164:167], a[136:139], a[36:39]\n" \
M_"a[36:39], a[228:231], a[200:203], a[36:39]\n" \
"buffer_store_dwordx4 v[200:203], v20, s[80:83], s35 offen sc1\n" \
M_"a[40:43], a[168:171], a[136:139], a[40:43]\n" \
"s_mov_b32 m0, s32\n" \
X_"a[244:245], v37 offset:8192\n" \
M_"a[40:43], a[232:235], a[200:203], a[40:43]\n" \
"s_mov_b32 exec_lo, 0x0\n" \
"s_mov_b32 exec_hi, 0xffffffff\n" \
"global_load_lds_dword v[188:189], off\n" \
"s_mov_b64 exec, -1\n" \
M_"a[44:47], a[172:175], a[136:139], a[44:47]\n" \
X_"a[246:247], v37 offset:9216\n" \
M_"a[44:47], a[236:239], a[200:203], a[44:47]\n" \
M_"a[60:63], a[172:175], a[140:143], a[60:63]\n" \
M_"a[60:63], a[236:239], a[204:207], a[60:63]\n" \
M_"a[56:59], a[168:171], a[140:143], a[56:59]\n" \
X_"a[184:185], v38 offset:0\n" \
M_"a[56:59], a[232:235], a[204:207], a[56:59]\n" \
M_"a[52:55], a[164:167], a[140:143], a[52:55]\n" \
"buffer_store_dwordx4 v[204:207], v20, s[80:83], s88 offen sc1\n" \
M_"a[52:55], a[228:231], a[204:207], a[52:55]\n" \
X_"a[186:187], v38 offset:1024\n" \
M_"a[48:51], a[160:163], a[140:143], a[48:51]\n" \
M_"a[48:51], a[224:227], a[204:207], a[48:51]\n" \
M_"a[64:67], a[160:163], a[144:147], a[64:67]\n" \
X_"a[248:249], v38 offset:8192\n" \
M_"a[64:67], a[224:227], a[208:211], a[64:67]\n" \
M_"a[68:71], a[164:167], a[144:147], a[68:71]\n" \
M_"a[68:71], a[228:231], a[208:211], a[68:71]\n" \
X_"a[250:251], v38 offset:9216\n" \
M_"a[72:75], a[168:171], a[144:147], a[72:75]\n" \
M_"a[72:75], a[232:235], a[208:211], a[72:75]\n" \
M_"a[76:79], a[172:175], a[144:147], a[76:79]\n" \
"buffer_store_dwordx4 v[208:211], v20, s[80:83], s36 offen sc1\n" \
M_"a[76:79], a[236:239], a[208:211], a[76:79]\n" \
X_"a[188:189], v39 offset:0\n" \
M_"a[92:95], a[172:175], a[148:151], a[92:95]\n" \
M_"a[92:95], a[236:239], a[212:215], a[92:95]\n" \
M_"a[88:91], a[168:171], a[148:151], a[88:91]\n" \
X_"a[190:191], v39 offset:1024\n" \
M_"a[88:91], a[232:235], a[212:215], a[88:91]\n" \
M_"a[84:87], a[164:167], a[148:151], a[84:87]\n" \
M_"a[84:87], a[228:231], a[212:215], a[84:87]\n" \
X_"a[252:253], v39 offset:8192\n" \
M_"a[80:83], a[160:163], a[148:151], a[80:83]\n" \
M_"a[80:83], a[224:227], a[212:215], a[80:83]\n" \
M_"a[96:99], a[160:163], a[152:155], a[96:99]\n" \
X_"a[254:255], v39 offset:9216\n" \
M_"a[96:99], a[224:227], a[216:219], a[96:99]\n" \
M_"a[100:103], a[164:167], a[152:155], a[100:103]\n" \
M_"a[100:103], a[228:231], a[216:219], a[100:103]\n" \
M_"a[104:107], a[168:171], a[152:155], a[104:107]\n" \
M_"a[104:107], a[232:235], a[216:219], a[104:107]\n" \
M_"a[108:111], a[172:175], a[152:155], a[108:111]\n" \
M_"a[108:111], a[236:239], a[216:219], a[108:111]\n" \
"s_waitcnt lgkmcnt(15)\n" \
M_"a[124:127], a[172:175], a[156:159], a[124:127]\n" \
M_"a[124:127], a[236:239], a[220:223], a[124:127]\n" \
M_"a[120:123], a[168:171], a[156:159], a[120:123]\n" \
M_"a[120:123], a[232:235], a[220:223], a[120:123]\n" \
M_"a[116:119], a[164:167], a[156:159], a[116:119]\n" \
M_"a[116:119], a[228:231], a[220:223], a[116:119]\n" \
M_"a[112:115], a[160:163], a[156:159], a[112:115]\n" \
"s_waitcnt vmcnt(4) lgkmcnt(0)\n" \
M_"a[112:115], a[224:227], a[220:223], a[112:115]\n" \
"s_barrier\n" \
M_"v[52:55], a[176:179], a[128:131], v[52:55]\n" \
"s_add_u32 m0, s30, 0x0\n" \
"s_movk_i32 s84, 0x300\n" \
M_"v[52:55], a[240:243], a[192:195], v[52:55]\n" \
B_"v22, s[64:67], s84 offen offset:0 lds\n" \
M_"v[56:59], a[180:183], a[128:131], v[56:59]\n" \
X_"a[160:161], v32 offset:32768\n" \
M_"v[56:59], a[244:247], a[192:195], v[56:59]\n" \
"s_movk_i32 s84, 0x1f00\n" \
M_"v[60:63], a[184:187], a[128:131], v[60:63]\n" \
B_"v23, s[64:67], s84 offen offset:1024 lds\n" \
M_"v[60:63], a[248:251], a[192:195], v[60:63]\n" \
X_"a[162:163], v32 offset:33792\n" \
M_"v[64:67], a[188:191], a[128:131], v[64:67]\n" \
X_"a[224:225], v32 offset:40960\n" \
M_"v[64:67], a[252:255], a[192:195], v[64:67]\n" \
R_"a[128:131], v28 offset:49152\n" \
M_"v[80:83], a[188:191], a[132:135], v[80:83]\n" \
R_"a[192:195], v29 offset:49152\n" \
M_"v[80:83], a[252:255], a[196:199], v[80:83]\n" \
"s_movk_i32 s84, 0x3b00\n" \
M_"v[76:79], a[184:187], a[132:135], v[76:79]\n" \
B_"v22, s[64:67], s84 offen offset:2048 lds\n" \
M_"v[76:79], a[248:251], a[196:199], v[76:79]\n" \
"s_movk_i32 s84, 0x5700\n" \
M_"v[72:75], a[180:183], a[132:135], v[72:75]\n" \
B_"v23, s[64:67], s84 offen offset:3072 lds\n" \
M_"v[72:75], a[244:247], a[196:199], v[72:75]\n" \
"s_add_u32 m0, s31, 0x0\n" \
"s_mov_b32 s84, 0x300000\n" \
M_"v[68:71], a[176:179], a[132:135], v[68:71]\n" \
B_"v26, s[68:71], s84 offen offset:0 lds\n" \
M_"v[68:71], a[240:243], a[196:199], v[68:71]\n" \
R_"a[132:135], v28 offset:51200\n" \
M_"v[84:87], a[176:179], a[136:139], v[84:87]\n" \
R_"a[196:199], v29 offset:51200\n" \
M_"v[84:87], a[240:243], a[200:203], v[84:87]\n" \
"s_addk_i32 s84, 0x7c00\n" \
M_"v[88:91], a[180:183], a[136:139], v[88:91]\n" \
B_"v26, s[68:71], s84 offen offset:1024 lds\n" \
M_"v[88:91], a[244:247], a[200:203], v[88:91]\n" \
X_"a[226:227], v32 offset:41984\n" \
M_"v[92:95], a[184:187], a[136:139], v[92:95]\n" \
"s_addk_i32 s84, 0x7c00\n" \
M_"v[92:95], a[248:251], a[200:203], v[92:95]\n" \
B_"v27, s[68:71], s84 offen offset:2048 lds\n" \
M_"v[96:99], a[188:191], a[136:139], v[96:99]\n" \
X_"a[164:165], v33 offset:32768\n" \
M_"v[96:99], a[252:255], a[200:203], v[96:99]\n" \
R_"a[136:139], v28 offset:53248\n" \
M_"v[112:115], a[188:191], a[140:143], v[112:115]\n" \
R_"a[200:203], v29 offset:53248\n" \
M_"v[112:115], a[252:255], a[204:207], v[112:115]\n" \
"s_addk_i32 s84, 0x7c00\n" \
M_"v[108:111], a[184:187], a[140:143], v[108:111]\n" \
B_"v27, s[68:71], s84 offen offset:3072 lds\n" \
M_"v[108:111], a[248:251], a[204:207], v[108:111]\n" \
"s_add_u32 m0, s30, 0x1000\n" \
"s_mov_b32 s84, 0x8300\n" \
M_"v[104:107], a[180:183], a[140:143], v[104:107]\n" \
B_"v22, s[64:67], s84 offen offset:0 lds\n" \
M_"v[104:107], a[244:247], a[204:207], v[104:107]\n" \
X_"a[166:167], v33 offset:33792\n" \
M_"v[100:103], a[176:179], a[140:143], v[100:103]\n" \
X_"a[228:229], v33 offset:40960\n" \
M_"v[100:103], a[240:243], a[204:207], v[100:103]\n" \
R_"a[140:143], v28 offset:55296\n" \
M_"v[116:119], a[176:179], a[144:147], v[116:119]\n" \
R_"a[204:207], v29 offset:55296\n" \
M_"v[116:119], a[240:243], a[208:211], v[116:119]\n" \
"s_addk_i32 s84, 0x1c00\n" \
M_"v[120:123], a[180:183], a[144:147], v[120:123]\n" \
B_"v23, s[64:67], s84 offen offset:1024 lds\n" \
M_"v[120:123], a[244:247], a[208:211], v[120:123]\n" \
"s_addk_i32 s84, 0x1c00\n" \
M_"v[124:127], a[184:187], a[144:147], v[124:127]\n" \
B_"v22, s[64:67], s84 offen offset:2048 lds\n" \
M_"v[124:127], a[248:251], a[208:211], v[124:127]\n" \
"s_addk_i32 s84, 0x1c00\n" \
"s_nop 0\n" \
M_"v[128:131], a[188:191], a[144:147], v[128:131]\n" \
B_"v23, s[64:67], s84 offen offset:3072 lds\n" \
M_"v[128:131], a[252:255], a[208:211], v[128:131]\n" \
R_"a[144:147], v28 offset:32768\n" \
M_"v[144:147], a[188:191], a[148:151], v[144:147]\n" \
R_"a[208:211], v29 offset:32768\n" \
M_"v[144:147], a[252:255], a[212:215], v[144:147]\n" \
"s_add_u32 m0, s31, 0x4000\n" \
"s_mov_b32 s84, 0x300000\n" \
M_"v[140:143], a[184:187], a[148:151], v[140:143]\n" \
B_"v26, s[72:75], s84 offen offset:0 lds\n" \
M_"v[140:143], a[248:251], a[212:215], v[140:143]\n" \
X_"a[230:231], v33 offset:41984\n" \
M_"v[136:139], a[180:183], a[148:151], v[136:139]\n" \
"s_addk_i32 s84, 0x7c00\n" \
"s_nop 0\n" \
M_"v[136:139], a[244:247], a[212:215], v[136:139]\n" \
B_"v26, s[72:75], s84 offen offset:1024 lds\n" \
M_"v[132:135], a[176:179], a[148:151], v[132:135]\n" \
X_"a[168:169], v34 offset:32768\n" \
M_"v[132:135], a[240:243], a[212:215], v[132:135]\n" \
R_"a[148:151], v28 offset:34816\n" \
M_"v[148:151], a[176:179], a[152:155], v[148:151]\n" \
R_"a[212:215], v29 offset:34816\n" \
M_"v[148:151], a[240:243], a[216:219], v[148:151]\n" \
X_"a[170:171], v34 offset:33792\n" \
M_"v[152:155], a[180:183], a[152:155], v[152:155]\n" \
X_"a[232:233], v34 offset:40960\n" \
M_"v[152:155], a[244:247], a[216:219], v[152:155]\n" \
X_"a[234:235], v34 offset:41984\n" \
M_"v[156:159], a[184:187], a[152:155], v[156:159]\n" \
X_"a[172:173], v35 offset:32768\n" \
M_"v[156:159], a[248:251], a[216:219], v[156:159]\n" \
X_"a[174:175], v35 offset:33792\n" \
M_"v[160:163], a[188:191], a[152:155], v[160:163]\n" \
X_"a[236:237], v35 offset:40960\n" \
M_"v[160:163], a[252:255], a[216:219], v[160:163]\n" \
R_"a[152:155], v28 offset:36864\n" \
M_"v[176:179], a[188:191], a[156:159], v[176:179]\n" \
R_"a[216:219], v29 offset:36864\n" \
M_"v[176:179], a[252:255], a[220:223], v[176:179]\n" \
X_"a[238:239], v35 offset:41984\n" \
M_"v[172:175], a[184:187], a[156:159], v[172:175]\n" \
M_"v[172:175], a[248:251], a[220:223], v[172:175]\n" \
M_"v[168:171], a[180:183], a[156:159], v[168:171]\n" \
M_"v[168:171], a[244:247], a[220:223], v[168:171]\n" \
M_"v[164:167], a[176:179], a[156:159], v[164:167]\n" \
M_"v[164:167], a[240:243], a[220:223], v[164:167]\n" \
R_"a[156:159], v28 offset:38912\n" \
R_"a[220:223], v29 offset:38912\n" \
"s_waitcnt lgkmcnt(15)\n" \
M_"a[0:3], a[160:163], a[128:131], a[0:3]\n" \
"s_addk_i32 s84, 0x7c00\n" \
M_"a[0:3], a[224:227], a[192:195], a[0:3]\n" \
B_"v27, s[72:75], s84 offen offset:2048 lds\n" \
M_"a[4:7], a[164:167], a[128:131], a[4:7]\n" \
X_"a[176:177], v36 offset:32768\n" \
"s_waitcnt lgkmcnt(15)\n" \
M_"a[4:7], a[228:231], a[192:195], a[4:7]\n" \
"s_addk_i32 s84, 0x7c00\n" \
"s_waitcnt lgkmcnt(11)\n" \
M_"a[8:11], a[168:171], a[128:131], a[8:11]\n" \
B_"v27, s[72:75], s84 offen offset:3072 lds\n" \
"s_waitcnt lgkmcnt(9)\n" \
M_"a[8:11], a[232:235], a[192:195], a[8:11]\n" \
X_"a[178:179], v36 offset:33792\n" \
"s_waitcnt lgkmcnt(8)\n" \
M_"a[12:15], a[172:175], a[128:131], a[12:15]\n" \
X_"a[240:241], v36 offset:40960\n" \
"s_waitcnt lgkmcnt(5)\n" \
M_"a[12:15], a[236:239], a[192:195], a[12:15]\n" \
M_"a[28:31], a[172:175], a[132:135], a[28:31]\n" \
M_"a[28:31], a[236:239], a[196:199], a[28:31]\n" \
X_"a[242:243], v36 offset:41984\n" \
M_"a[24:27], a[168:171], a[132:135], a[24:27]\n" \
M_"a[24:27], a[232:235], a[196:199], a[24:27]\n" \
M_"a[20:23], a[164:167], a[132:135], a[20:23]\n" \
M_"a[20:23], a[228:231], a[196:199], a[20:23]\n" \
X_"a[180:181], v37 offset:32768\n" \
M_"a[16:19], a[160:163], a[132:135], a[16:19]\n" \
M_"a[16:19], a[224:227], a[196:199], a[16:19]\n" \
M_"a[32:35], a[160:163], a[136:139], a[32:35]\n" \
X_"a[182:183], v37 offset:33792\n" \
M_"a[32:35], a[224:227], a[200:203], a[32:35]\n" \
M_"a[36:39], a[164:167], a[136:139], a[36:39]\n" \
"v_lshlrev_b32_e32 v45, 3, v20\n" \
"v_add_u32_e32 v45, s31, v45\n" \
M_"a[36:39], a[228:231], a[200:203], a[36:39]\n" \
"s_getpc_b64 s[86:87]\n" \
"s_sub_u32 s86, s86, 0xf800\n" \
"s_subb_u32 s87, s87, 0\n" \
"s_mov_b32 m0, s32\n" \
"s_nop 0\n" \
M_"a[40:43], a[168:171], a[136:139], a[40:43]\n" \
"global_load_lds_dword v45, s[86:87]\n" \
M_"a[40:43], a[232:235], a[200:203], a[40:43]\n" \
X_"a[244:245], v37 offset:40960\n" \
M_"a[44:47], a[172:175], a[136:139], a[44:47]\n" \
X_"a[246:247], v37 offset:41984\n" \
M_"a[44:47], a[236:239], a[200:203], a[44:47]\n" \
M_"a[60:63], a[172:175], a[140:143], a[60:63]\n" \
M_"a[60:63], a[236:239], a[204:207], a[60:63]\n" \
M_"a[56:59], a[168:171], a[140:143], a[56:59]\n" \
X_"a[184:185], v38 offset:32768\n" \
M_"a[56:59], a[232:235], a[204:207], a[56:59]\n" \
M_"a[52:55], a[164:167], a[140:143], a[52:55]\n" \
M_"a[52:55], a[228:231], a[204:207], a[52:55]\n" \
X_"a[186:187], v38 offset:33792\n" \
M_"a[48:51], a[160:163], a[140:143], a[48:51]\n" \
M_"a[48:51], a[224:227], a[204:207], a[48:51]\n" \
M_"a[64:67], a[160:163], a[144:147], a[64:67]\n" \
X_"a[248:249], v38 offset:40960\n" \
M_"a[64:67], a[224:227], a[208:211], a[64:67]\n" \
M_"a[68:71], a[164:167], a[144:147], a[68:71]\n" \
M_"a[68:71], a[228:231], a[208:211], a[68:71]\n" \
X_"a[250:251], v38 offset:41984\n" \
M_"a[72:75], a[168:171], a[144:147], a[72:75]\n" \
M_"a[72:75], a[232:235], a[208:211], a[72:75]\n" \
M_"a[76:79], a[172:175], a[144:147], a[76:79]\n" \
M_"a[76:79], a[236:239], a[208:211], a[76:79]\n" \
X_"a[188:189], v39 offset:32768\n" \
M_"a[92:95], a[172:175], a[148:151], a[92:95]\n" \
M_"a[92:95], a[236:239], a[212:215], a[92:95]\n" \
M_"a[88:91], a[168:171], a[148:151], a[88:91]\n" \
X_"a[190:191], v39 offset:33792\n" \
M_"a[88:91], a[232:235], a[212:215], a[88:91]\n" \
M_"a[84:87], a[164:167], a[148:151], a[84:87]\n" \
M_"a[84:87], a[228:231], a[212:215], a[84:87]\n" \
X_"a[252:253], v39 offset:40960\n" \
M_"a[80:83], a[160:163], a[148:151], a[80:83]\n" \
M_"a[80:83], a[224:227], a[212:215], a[80:83]\n" \
M_"a[96:99], a[160:163], a[152:155], a[96:99]\n" \
X_"a[254:255], v39 offset:41984\n" \
M_"a[96:99], a[224:227], a[216:219], a[96:99]\n" \
M_"a[100:103], a[164:167], a[152:155], a[100:103]\n" \
M_"a[100:103], a[228:231], a[216:219], a[100:103]\n" \
M_"a[104:107], a[168:171], a[152:155], a[104:107]\n" \
M_"a[104:107], a[232:235], a[216:219], a[104:107]\n" \
M_"a[108:111], a[172:175], a[152:155], a[108:111]\n" \
M_"a[108:111], a[236:239], a[216:219], a[108:111]\n" \
"s_waitcnt lgkmcnt(15)\n" \
M_"a[124:127], a[172:175], a[156:159], a[124:127]\n" \
M_"a[124:127], a[236:239], a[220:223], a[124:127]\n" \
M_"a[120:123], a[168:171], a[156:159], a[120:123]\n" \
M_"a[120:123], a[232:235], a[220:223], a[120:123]\n" \
M_"a[116:119], a[164:167], a[156:159], a[116:119]\n" \
M_"a[116:119], a[228:231], a[220:223], a[116:119]\n" \
M_"a[112:115], a[160:163], a[156:159], a[112:115]\n" \
"s_waitcnt vmcnt(1) lgkmcnt(0)\n" \
M_"a[112:115], a[224:227], a[220:223], a[112:115]\n" \
"s_barrier\n" \
"v_mov_b32_e32 v45, s62\n" \
"s_mov_b64 exec, 1\n" \
"buffer_store_dword v45, off, s[80:83], s37 offset:128 sc1\n" \
"s_mov_b64 exec, -1\n" \
M_"v[52:55], a[176:179], a[128:131], v[52:55]\n" \
"s_add_u32 m0, s30, 0x8000\n" \
"s_movk_i32 s84, 0x380\n" \
M_"v[52:55], a[240:243], a[192:195], v[52:55]\n" \
B_"v22, s[64:67], s84 offen offset:0 lds\n" \
M_"v[56:59], a[180:183], a[128:131], v[56:59]\n" \
X_"a[160:161], v32 offset:0\n" \
M_"v[56:59], a[244:247], a[192:195], v[56:59]\n" \
"s_movk_i32 s84, 0x1f80\n" \
M_"v[60:63], a[184:187], a[128:131], v[60:63]\n" \
B_"v23, s[64:67], s84 offen offset:1024 lds\n" \
M_"v[60:63], a[248:251], a[192:195], v[60:63]\n" \
X_"a[162:163], v32 offset:1024\n" \
M_"v[64:67], a[188:191], a[128:131], v[64:67]\n" \
X_"a[224:225], v32 offset:8192\n" \
M_"v[64:67], a[252:255], a[192:195], v[64:67]\n" \
R_"a[128:131], v28 offset:16384\n" \
M_"v[80:83], a[188:191], a[132:135], v[80:83]\n" \
R_"a[192:195], v29 offset:16384\n" \
M_"v[80:83], a[252:255], a[196:199], v[80:83]\n" \
"s_movk_i32 s84, 0x3b80\n" \
M_"v[76:79], a[184:187], a[132:135], v[76:79]\n" \
B_"v22, s[64:67], s84 offen offset:2048 lds\n" \
M_"v[76:79], a[248:251], a[196:199], v[76:79]\n" \
"s_movk_i32 s84, 0x5780\n" \
M_"v[72:75], a[180:183], a[132:135], v[72:75]\n" \
B_"v23, s[64:67], s84 offen offset:3072 lds\n" \
M_"v[72:75], a[244:247], a[196:199], v[72:75]\n" \
"s_add_u32 m0, s31, 0x8000\n" \
"s_mov_b32 s84, 0x380000\n" \
M_"v[68:71], a[176:179], a[132:135], v[68:71]\n" \
B_"v26, s[68:71], s84 offen offset:0 lds\n" \
M_"v[68:71], a[240:243], a[196:199], v[68:71]\n" \
R_"a[132:135], v28 offset:18432\n" \
M_"v[84:87], a[176:179], a[136:139], v[84:87]\n" \
R_"a[196:199], v29 offset:18432\n" \
M_"v[84:87], a[240:243], a[200:203], v[84:87]\n" \
"s_addk_i32 s84, 0x7c00\n" \
M_"v[88:91], a[180:183], a[136:139], v[88:91]\n" \
B_"v26, s[68:71], s84 offen offset:1024 lds\n" \
M_"v[88:91], a[244:247], a[200:203], v[88:91]\n" \
X_"a[226:227], v32 offset:9216\n" \
M_"v[92:95], a[184:187], a[136:139], v[92:95]\n" \
"s_addk_i32 s84, 0x7c00\n" \
M_"v[92:95], a[248:251], a[200:203], v[92:95]\n" \
B_"v27, s[68:71], s84 offen offset:2048 lds\n" \
M_"v[96:99], a[188:191], a[136:139], v[96:99]\n" \
X_"a[164:165], v33 offset:0\n" \
M_"v[96:99], a[252:255], a[200:203], v[96:99]\n" \
R_"a[136:139], v28 offset:20480\n" \
M_"v[112:115], a[188:191], a[140:143], v[112:115]\n" \
R_"a[200:203], v29 offset:20480\n" \
M_"v[112:115], a[252:255], a[204:207], v[112:115]\n" \
"s_addk_i32 s84, 0x7c00\n" \
"s_nop 0\n" \
M_"v[108:111], a[184:187], a[140:143], v[108:111]\n" \
B_"v27, s[68:71], s84 offen offset:3072 lds\n" \
M_"v[108:111], a[248:251], a[204:207], v[108:111]\n" \
"s_add_u32 m0, s30, 0x9000\n" \
"s_mov_b32 s84, 0x8380\n" \
M_"v[104:107], a[180:183], a[140:143], v[104:107]\n" \
B_"v22, s[64:67], s84 offen offset:0 lds\n" \
M_"v[104:107], a[244:247], a[204:207], v[104:107]\n" \
X_"a[166:167], v33 offset:1024\n" \
M_"v[100:103], a[176:179], a[140:143], v[100:103]\n" \
X_"a[228:229], v33 offset:8192\n" \
M_"v[100:103], a[240:243], a[204:207], v[100:103]\n" \
R_"a[140:143], v28 offset:22528\n" \
M_"v[116:119], a[176:179], a[144:147], v[116:119]\n" \
R_"a[204:207], v29 offset:22528\n" \
M_"v[116:119], a[240:243], a[208:211], v[116:119]\n" \
"s_addk_i32 s84, 0x1c00\n" \
M_"v[120:123], a[180:183], a[144:147], v[120:123]\n" \
B_"v23, s[64:67], s84 offen offset:1024 lds\n" \
M_"v[120:123], a[244:247], a[208:211], v[120:123]\n" \
"s_addk_i32 s84, 0x1c00\n" \
M_"v[124:127], a[184:187], a[144:147], v[124:127]\n" \
B_"v22, s[64:67], s84 offen offset:2048 lds\n" \
M_"v[124:127], a[248:251], a[208:211], v[124:127]\n" \
"s_addk_i32 s84, 0x1c00\n" \
"s_nop 0\n" \
M_"v[128:131], a[188:191], a[144:147], v[128:131]\n" \
B_"v23, s[64:67], s84 offen offset:3072 lds\n" \
M_"v[128:131], a[252:255], a[208:211], v[128:131]\n" \
R_"a[144:147], v28 offset:0\n" \
M_"v[144:147], a[188:191], a[148:151], v[144:147]\n" \
R_"a[208:211], v29 offset:0\n" \
M_"v[144:147], a[252:255], a[212:215], v[144:147]\n" \
"s_add_u32 m0, s31, 0xc000\n" \
"s_mov_b32 s84, 0x380000\n" \
M_"v[140:143], a[184:187], a[148:151], v[140:143]\n" \
B_"v26, s[72:75], s84 offen offset:0 lds\n" \
M_"v[140:143], a[248:251], a[212:215], v[140:143]\n" \
X_"a[230:231], v33 offset:9216\n" \
M_"v[136:139], a[180:183], a[148:151], v[136:139]\n" \
"s_addk_i32 s84, 0x7c00\n" \
"s_nop 0\n" \
M_"v[136:139], a[244:247], a[212:215], v[136:139]\n" \
B_"v26, s[72:75], s84 offen offset:1024 lds\n" \
M_"v[132:135], a[176:179], a[148:151], v[132:135]\n" \
X_"a[168:169], v34 offset:0\n" \
M_"v[132:135], a[240:243], a[212:215], v[132:135]\n" \
R_"a[148:151], v28 offset:2048\n" \
M_"v[148:151], a[176:179], a[152:155], v[148:151]\n" \
R_"a[212:215], v29 offset:2048\n" \
M_"v[148:151], a[240:243], a[216:219], v[148:151]\n" \
X_"a[170:171], v34 offset:1024\n" \
M_"v[152:155], a[180:183], a[152:155], v[152:155]\n" \
X_"a[232:233], v34 offset:8192\n" \
M_"v[152:155], a[244:247], a[216:219], v[152:155]\n" \
X_"a[234:235], v34 offset:9216\n" \
M_"v[156:159], a[184:187], a[152:155], v[156:159]\n" \
X_"a[172:173], v35 offset:0\n" \
M_"v[156:159], a[248:251], a[216:219], v[156:159]\n" \
X_"a[174:175], v35 offset:1024\n" \
M_"v[160:163], a[188:191], a[152:155], v[160:163]\n" \
X_"a[236:237], v35 offset:8192\n" \
M_"v[160:163], a[252:255], a[216:219], v[160:163]\n" \
R_"a[152:155], v28 offset:4096\n" \
M_"v[176:179], a[188:191], a[156:159], v[176:179]\n" \
R_"a[216:219], v29 offset:4096\n" \
M_"v[176:179], a[252:255], a[220:223], v[176:179]\n" \
X_"a[238:239], v35 offset:9216\n" \
M_"v[172:175], a[184:187], a[156:159], v[172:175]\n" \
M_"v[172:175], a[248:251], a[220:223], v[172:175]\n" \
M_"v[168:171], a[180:183], a[156:159], v[168:171]\n" \
M_"v[168:171], a[244:247], a[220:223], v[168:171]\n" \
M_"v[164:167], a[176:179], a[156:159], v[164:167]\n" \
M_"v[164:167], a[240:243], a[220:223], v[164:167]\n" \
R_"a[156:159], v28 offset:6144\n" \
R_"a[220:223], v29 offset:6144\n" \
"s_waitcnt lgkmcnt(15)\n" \
M_"a[0:3], a[160:163], a[128:131], a[0:3]\n" \
"s_addk_i32 s84, 0x7c00\n" \
M_"a[0:3], a[224:227], a[192:195], a[0:3]\n" \
B_"v27, s[72:75], s84 offen offset:2048 lds\n" \
M_"a[4:7], a[164:167], a[128:131], a[4:7]\n" \
X_"a[176:177], v36 offset:0\n" \
"s_waitcnt lgkmcnt(15)\n" \
M_"a[4:7], a[228:231], a[192:195], a[4:7]\n" \
"s_addk_i32 s84, 0x7c00\n" \
"s_waitcnt lgkmcnt(11)\n" \
M_"a[8:11], a[168:171], a[128:131], a[8:11]\n" \
B_"v27, s[72:75], s84 offen offset:3072 lds\n" \
"s_waitcnt lgkmcnt(9)\n" \
M_"a[8:11], a[232:235], a[192:195], a[8:11]\n" \
X_"a[178:179], v36 offset:1024\n" \
"s_waitcnt lgkmcnt(8)\n" \
M_"a[12:15], a[172:175], a[128:131], a[12:15]\n" \
X_"a[240:241], v36 offset:8192\n" \
"s_waitcnt lgkmcnt(5)\n" \
M_"a[12:15], a[236:239], a[192:195], a[12:15]\n" \
M_"a[28:31], a[172:175], a[132:135], a[28:31]\n" \
M_"a[28:31], a[236:239], a[196:199], a[28:31]\n" \
X_"a[242:243], v36 offset:9216\n" \
M_"a[24:27], a[168:171], a[132:135], a[24:27]\n" \
M_"a[24:27], a[232:235], a[196:199], a[24:27]\n" \
M_"a[20:23], a[164:167], a[132:135], a[20:23]\n" \
M_"a[20:23], a[228:231], a[196:199], a[20:23]\n" \
X_"a[180:181], v37 offset:0\n" \
M_"a[16:19], a[160:163], a[132:135], a[16:19]\n" \
M_"a[16:19], a[224:227], a[196:199], a[16:19]\n" \
M_"a[32:35], a[160:163], a[136:139], a[32:35]\n" \
"v_bfe_u32 v44, v20, 2, 5\n" \
X_"a[182:183], v37 offset:1024\n" \
M_"a[32:35], a[224:227], a[200:203], a[32:35]\n" \
"buffer_load_dword v44, v44, s[80:83], s38 offen offset:128 sc1\n" \
M_"a[36:39], a[164:167], a[136:139], a[36:39]\n" \
M_"a[36:39], a[228:231], a[200:203], a[36:39]\n" \
X_"a[244:245], v37 offset:8192\n" \
M_"a[40:43], a[168:171], a[136:139], a[40:43]\n" \
M_"a[40:43], a[232:235], a[200:203], a[40:43]\n" \
M_"a[44:47], a[172:175], a[136:139], a[44:47]\n" \
X_"a[246:247], v37 offset:9216\n" \
M_"a[44:47], a[236:239], a[200:203], a[44:47]\n" \
M_"a[60:63], a[172:175], a[140:143], a[60:63]\n" \
"global_store_dwordx4 v49, v[0:3], s[28:29] offset:0 sc1\n" \
M_"a[60:63], a[236:239], a[204:207], a[60:63]\n" \
M_"a[56:59], a[168:171], a[140:143], a[56:59]\n" \
X_"a[184:185], v38 offset:0\n" \
M_"a[56:59], a[232:235], a[204:207], a[56:59]\n" \
M_"a[52:55], a[164:167], a[140:143], a[52:55]\n" \
M_"a[52:55], a[228:231], a[204:207], a[52:55]\n" \
X_"a[186:187], v38 offset:1024\n" \
M_"a[48:51], a[160:163], a[140:143], a[48:51]\n" \
M_"a[48:51], a[224:227], a[204:207], a[48:51]\n" \
M_"a[64:67], a[160:163], a[144:147], a[64:67]\n" \
"global_store_dwordx4 v49, v[0:3], s[28:29] offset:1024 sc1\n" \
M_"a[64:67], a[224:227], a[208:211], a[64:67]\n" \
X_"a[248:249], v38 offset:8192\n" \
M_"a[68:71], a[164:167], a[144:147], a[68:71]\n" \
M_"a[68:71], a[228:231], a[208:211], a[68:71]\n" \
X_"a[250:251], v38 offset:9216\n" \
M_"a[72:75], a[168:171], a[144:147], a[72:75]\n" \
M_"a[72:75], a[232:235], a[208:211], a[72:75]\n" \
M_"a[76:79], a[172:175], a[144:147], a[76:79]\n" \
M_"a[76:79], a[236:239], a[208:211], a[76:79]\n" \
X_"a[188:189], v39 offset:0\n" \
M_"a[92:95], a[172:175], a[148:151], a[92:95]\n" \
"s_mov_b64 exec, 0xffff\n" \
"global_store_dwordx4 v49, v[0:3], s[28:29] offset:2048 sc1\n" \
"s_mov_b64 exec, -1\n" \
"s_nop 0\n" \
M_"a[92:95], a[236:239], a[212:215], a[92:95]\n" \
M_"a[88:91], a[168:171], a[148:151], a[88:91]\n" \
X_"a[190:191], v39 offset:1024\n" \
M_"a[88:91], a[232:235], a[212:215], a[88:91]\n" \
M_"a[84:87], a[164:167], a[148:151], a[84:87]\n" \
M_"a[84:87], a[228:231], a[212:215], a[84:87]\n" \
".fill (L_pb + 16096 - .) / 4, 4, 0xBF800000\n" \
"L_pp4:\n" \
"s_cbranch_execz L_pret\n" \
"s_nop 0\n" \
X_"a[252:253], v39 offset:8192\n" \
M_"a[80:83], a[160:163], a[148:151], a[80:83]\n" \
M_"a[80:83], a[224:227], a[212:215], a[80:83]\n" \
M_"a[96:99], a[160:163], a[152:155], a[96:99]\n" \
X_"a[254:255], v39 offset:9216\n" \
M_"a[96:99], a[224:227], a[216:219], a[96:99]\n" \
M_"a[100:103], a[164:167], a[152:155], a[100:103]\n" \
M_"a[100:103], a[228:231], a[216:219], a[100:103]\n" \
M_"a[104:107], a[168:171], a[152:155], a[104:107]\n" \
M_"a[104:107], a[232:235], a[216:219], a[104:107]\n" \
M_"a[108:111], a[172:175], a[152:155], a[108:111]\n" \
M_"a[108:111], a[236:239], a[216:219], a[108:111]\n" \
"s_waitcnt lgkmcnt(15)\n" \
M_"a[124:127], a[172:175], a[156:159], a[124:127]\n" \
M_"a[124:127], a[236:239], a[220:223], a[124:127]\n" \
M_"a[120:123], a[168:171], a[156:159], a[120:123]\n" \
M_"a[120:123], a[232:235], a[220:223], a[120:123]\n" \
M_"a[116:119], a[164:167], a[156:159], a[116:119]\n" \
M_"a[116:119], a[228:231], a[220:223], a[116:119]\n" \
M_"a[112:115], a[160:163], a[156:159], a[112:115]\n" \
"s_waitcnt vmcnt(3) lgkmcnt(0)\n" \
M_"a[112:115], a[224:227], a[220:223], a[112:115]\n" \
"s_barrier\n" \
"v_cmp_ne_u32_e32 vcc, s62, v44\n" \
"s_nop 4\n" \
"s_cbranch_vccnz L_poll60_%=\n" \
"L_pollr60_%=:\n" \
"s_mov_b32 s68, s12\n" \
"s_and_b32 s69, s13, 0xffff\n" \
"s_mov_b32 s70, 0x480000\n" \
"s_mov_b32 s71, 0x20000\n" \
M_"v[52:55], a[176:179], a[128:131], v[52:55]\n" \
"s_add_u32 m0, s40, 0x0\n" \
"s_add_u32 s85, s39, 0x0\n" \
"s_nop 0\n" \
M_"v[52:55], a[240:243], a[192:195], v[52:55]\n" \
B_"v20, s[80:83], s85 offen sc1 lds\n" \
M_"v[56:59], a[180:183], a[128:131], v[56:59]\n" \
X_"a[160:161], v32 offset:32768\n" \
M_"v[56:59], a[244:247], a[192:195], v[56:59]\n" \
"s_add_u32 m0, s40, 0x1000\n" \
"s_add_u32 s85, s39, 0x1000\n" \
M_"v[60:63], a[184:187], a[128:131], v[60:63]\n" \
B_"v20, s[80:83], s85 offen sc1 lds\n" \
M_"v[60:63], a[248:251], a[192:195], v[60:63]\n" \
X_"a[162:163], v32 offset:33792\n" \
M_"v[64:67], a[188:191], a[128:131], v[64:67]\n" \
X_"a[224:225], v32 offset:40960\n" \
M_"v[64:67], a[252:255], a[192:195], v[64:67]\n" \
R_"a[128:131], v28 offset:49152\n" \
M_"v[80:83], a[188:191], a[132:135], v[80:83]\n" \
R_"a[192:195], v29 offset:49152\n" \
M_"v[80:83], a[252:255], a[196:199], v[80:83]\n" \
"s_add_u32 m0, s40, 0x3000\n" \
"s_movk_i32 s84, 0x0\n" \
M_"v[76:79], a[184:187], a[132:135], v[76:79]\n" \
B_"v21, s[76:79], s84 offen offset:0 lds\n" \
M_"v[76:79], a[248:251], a[196:199], v[76:79]\n" \
"s_add_u32 m0, s30, 0x0\n" \
"s_movk_i32 s84, 0x0\n" \
"s_nop 0\n" \
M_"v[72:75], a[180:183], a[132:135], v[72:75]\n" \
B_"v24, s[76:79], s84 offen offset:0 lds\n" \
M_"v[72:75], a[244:247], a[196:199], v[72:75]\n" \
X_"a[226:227], v32 offset:41984\n" \
M_"v[68:71], a[176:179], a[132:135], v[68:71]\n" \
X_"a[164:165], v33 offset:32768\n" \
M_"v[68:71], a[240:243], a[196:199], v[68:71]\n" \
R_"a[132:135], v28 offset:51200\n" \
M_"v[84:87], a[176:179], a[136:139], v[84:87]\n" \
R_"a[196:199], v29 offset:51200\n" \
M_"v[84:87], a[240:243], a[200:203], v[84:87]\n" \
"s_movk_i32 s84, 0x2000\n" \
M_"v[88:91], a[180:183], a[136:139], v[88:91]\n" \
B_"v25, s[76:79], s84 offen offset:1024 lds\n" \
M_"v[88:91], a[244:247], a[200:203], v[88:91]\n" \
"s_movk_i32 s84, 0x4000\n" \
M_"v[92:95], a[184:187], a[136:139], v[92:95]\n" \
B_"v24, s[76:79], s84 offen offset:2048 lds\n" \
M_"v[92:95], a[248:251], a[200:203], v[92:95]\n" \
X_"a[166:167], v33 offset:33792\n" \
M_"v[96:99], a[188:191], a[136:139], v[96:99]\n" \
X_"a[228:229], v33 offset:40960\n" \
M_"v[96:99], a[252:255], a[200:203], v[96:99]\n" \
R_"a[136:139], v28 offset:53248\n" \
M_"v[112:115], a[188:191], a[140:143], v[112:115]\n" \
R_"a[200:203], v29 offset:53248\n" \
M_"v[112:115], a[252:255], a[204:207], v[112:115]\n" \
"s_movk_i32 s84, 0x6000\n" \
M_"v[108:111], a[184:187], a[140:143], v[108:111]\n" \
B_"v25, s[76:79], s84 offen offset:3072 lds\n" \
M_"v[108:111], a[248:251], a[204:207], v[108:111]\n" \
"s_add_u32 m0, s31, 0x0\n" \
"s_movk_i32 s84, 0x0\n" \
M_"v[104:107], a[180:183], a[140:143], v[104:107]\n" \
B_"v26, s[68:71], s84 offen offset:0 lds\n" \
M_"v[104:107], a[244:247], a[204:207], v[104:107]\n" \
"s_movk_i32 s84, 0x7c00\n" \
M_"v[100:103], a[176:179], a[140:143], v[100:103]\n" \
B_"v26, s[68:71], s84 offen offset:1024 lds\n" \
M_"v[100:103], a[240:243], a[204:207], v[100:103]\n" \
R_"a[140:143], v28 offset:55296\n" \
M_"v[116:119], a[176:179], a[144:147], v[116:119]\n" \
R_"a[204:207], v29 offset:55296\n" \
M_"v[116:119], a[240:243], a[208:211], v[116:119]\n" \
"s_addk_i32 s84, 0x7c00\n" \
M_"v[120:123], a[180:183], a[144:147], v[120:123]\n" \
B_"v27, s[68:71], s84 offen offset:2048 lds\n" \
M_"v[120:123], a[244:247], a[208:211], v[120:123]\n" \
X_"a[230:231], v33 offset:41984\n" \
M_"v[124:127], a[184:187], a[144:147], v[124:127]\n" \
"s_addk_i32 s84, 0x7c00\n" \
M_"v[124:127], a[248:251], a[208:211], v[124:127]\n" \
B_"v27, s[68:71], s84 offen offset:3072 lds\n" \
M_"v[128:131], a[188:191], a[144:147], v[128:131]\n" \
X_"a[168:169], v34 offset:32768\n" \
M_"v[128:131], a[252:255], a[208:211], v[128:131]\n" \
R_"a[144:147], v28 offset:32768\n" \
M_"v[144:147], a[188:191], a[148:151], v[144:147]\n" \
R_"a[208:211], v29 offset:32768\n" \
M_"v[144:147], a[252:255], a[212:215], v[144:147]\n" \
"s_add_u32 m0, s30, 0x1000\n" \
"s_mov_b32 s84, 0x9000\n" \
M_"v[140:143], a[184:187], a[148:151], v[140:143]\n" \
B_"v24, s[76:79], s84 offen offset:0 lds\n" \
M_"v[140:143], a[248:251], a[212:215], v[140:143]\n" \
X_"a[170:171], v34 offset:33792\n" \
M_"v[136:139], a[180:183], a[148:151], v[136:139]\n" \
"s_addk_i32 s84, 0x2000\n" \
"s_nop 0\n" \
M_"v[136:139], a[244:247], a[212:215], v[136:139]\n" \
B_"v25, s[76:79], s84 offen offset:1024 lds\n" \
M_"v[132:135], a[176:179], a[148:151], v[132:135]\n" \
X_"a[232:233], v34 offset:40960\n" \
M_"v[132:135], a[240:243], a[212:215], v[132:135]\n" \
R_"a[148:151], v28 offset:34816\n" \
M_"v[148:151], a[176:179], a[152:155], v[148:151]\n" \
R_"a[212:215], v29 offset:34816\n" \
M_"v[148:151], a[240:243], a[216:219], v[148:151]\n" \
X_"a[234:235], v34 offset:41984\n" \
M_"v[152:155], a[180:183], a[152:155], v[152:155]\n" \
X_"a[172:173], v35 offset:32768\n" \
M_"v[152:155], a[244:247], a[216:219], v[152:155]\n" \
X_"a[174:175], v35 offset:33792\n" \
M_"v[156:159], a[184:187], a[152:155], v[156:159]\n" \
X_"a[236:237], v35 offset:40960\n" \
M_"v[156:159], a[248:251], a[216:219], v[156:159]\n" \
X_"a[238:239], v35 offset:41984\n" \
M_"v[160:163], a[188:191], a[152:155], v[160:163]\n" \
M_"v[160:163], a[252:255], a[216:219], v[160:163]\n" \
R_"a[152:155], v28 offset:36864\n" \
M_"v[176:179], a[188:191], a[156:159], v[176:179]\n" \
R_"a[216:219], v29 offset:36864\n" \
M_"v[176:179], a[252:255], a[220:223], v[176:179]\n" \
M_"v[172:175], a[184:187], a[156:159], v[172:175]\n" \
M_"v[172:175], a[248:251], a[220:223], v[172:175]\n" \
M_"v[168:171], a[180:183], a[156:159], v[168:171]\n" \
M_"v[168:171], a[244:247], a[220:223], v[168:171]\n" \
M_"v[164:167], a[176:179], a[156:159], v[164:167]\n" \
M_"v[164:167], a[240:243], a[220:223], v[164:167]\n" \
R_"a[156:159], v28 offset:38912\n" \
R_"a[220:223], v29 offset:38912\n" \
"s_waitcnt lgkmcnt(15)\n" \
M_"a[0:3], a[160:163], a[128:131], a[0:3]\n" \
"s_addk_i32 s84, 0x2000\n" \
M_"a[0:3], a[224:227], a[192:195], a[0:3]\n" \
B_"v24, s[76:79], s84 offen offset:2048 lds\n" \
M_"a[4:7], a[164:167], a[128:131], a[4:7]\n" \
X_"a[176:177], v36 offset:32768\n" \
M_"a[4:7], a[228:231], a[192:195], a[4:7]\n" \
"s_addk_i32 s84, 0x2000\n" \
"s_waitcnt lgkmcnt(13)\n" \
M_"a[8:11], a[168:171], a[128:131], a[8:11]\n" \
B_"v25, s[76:79], s84 offen offset:3072 lds\n" \
"s_waitcnt lgkmcnt(9)\n" \
M_"a[8:11], a[232:235], a[192:195], a[8:11]\n" \
X_"a[178:179], v36 offset:33792\n" \
"s_waitcnt lgkmcnt(8)\n" \
M_"a[12:15], a[172:175], a[128:131], a[12:15]\n" \
X_"a[240:241], v36 offset:40960\n" \
"v_accvgpr_read_b32 v184, a0\n" \
"s_waitcnt lgkmcnt(7)\n" \
M_"a[12:15], a[236:239], a[192:195], a[12:15]\n" \
"v_accvgpr_read_b32 v185, a1\n" \
"v_accvgpr_read_b32 v186, a2\n" \
M_"a[28:31], a[172:175], a[132:135], a[28:31]\n" \
"v_accvgpr_read_b32 v187, a3\n" \
"v_accvgpr_read_b32 v180, a4\n" \
M_"a[28:31], a[236:239], a[196:199], a[28:31]\n" \
"s_add_u32 m0, s30, 0x20000\n" \
"s_movk_i32 s84, 0x100\n" \
M_"a[24:27], a[168:171], a[132:135], a[24:27]\n" \
B_"v24, s[76:79], s84 offen offset:0 lds\n" \
M_"a[24:27], a[232:235], a[196:199], a[24:27]\n" \
X_"a[242:243], v36 offset:41984\n" \
"v_accvgpr_read_b32 v181, a5\n" \
M_"a[20:23], a[164:167], a[132:135], a[20:23]\n" \
"v_accvgpr_read_b32 v182, a6\n" \
"v_accvgpr_read_b32 v183, a7\n" \
M_"a[20:23], a[228:231], a[196:199], a[20:23]\n" \
X_"a[180:181], v37 offset:32768\n" \
C_"v184, v184, v185\n" \
M_"a[16:19], a[160:163], a[132:135], a[16:19]\n" \
"s_movk_i32 s84, 0x2100\n" \
"s_nop 0\n" \
M_"a[16:19], a[224:227], a[196:199], a[16:19]\n" \
B_"v25, s[76:79], s84 offen offset:1024 lds\n" \
M_"a[32:35], a[160:163], a[136:139], a[32:35]\n" \
X_"a[182:183], v37 offset:33792\n" \
C_"v185, v186, v187\n" \
M_"a[32:35], a[224:227], a[200:203], a[32:35]\n" \
C_"v186, v180, v181\n" \
C_"v187, v182, v183\n" \
M_"a[36:39], a[164:167], a[136:139], a[36:39]\n" \
"v_accvgpr_read_b32 v188, a8\n" \
"v_accvgpr_read_b32 v189, a9\n" \
M_"a[36:39], a[228:231], a[200:203], a[36:39]\n" \
"s_movk_i32 s84, 0x4100\n" \
"s_nop 0\n" \
M_"a[40:43], a[168:171], a[136:139], a[40:43]\n" \
B_"v24, s[76:79], s84 offen offset:2048 lds\n" \
M_"a[40:43], a[232:235], a[200:203], a[40:43]\n" \
X_"a[244:245], v37 offset:40960\n" \
"v_accvgpr_read_b32 v190, a10\n" \
M_"a[44:47], a[172:175], a[136:139], a[44:47]\n" \
X_"a[246:247], v37 offset:41984\n" \
"v_accvgpr_read_b32 v191, a11\n" \
M_"a[44:47], a[236:239], a[200:203], a[44:47]\n" \
"v_accvgpr_read_b32 v180, a12\n" \
"v_accvgpr_read_b32 v181, a13\n" \
M_"a[60:63], a[172:175], a[140:143], a[60:63]\n" \
"s_movk_i32 s84, 0x6100\n" \
M_"a[60:63], a[236:239], a[204:207], a[60:63]\n" \
B_"v25, s[76:79], s84 offen offset:3072 lds\n" \
M_"a[56:59], a[168:171], a[140:143], a[56:59]\n" \
X_"a[184:185], v38 offset:32768\n" \
"v_accvgpr_read_b32 v182, a14\n" \
M_"a[56:59], a[232:235], a[204:207], a[56:59]\n" \
"v_accvgpr_read_b32 v183, a15\n" \
C_"v188, v188, v189\n" \
M_"a[52:55], a[164:167], a[140:143], a[52:55]\n" \
"s_add_u32 m0, s30, 0x21000\n" \
"s_addk_i32 s84, 0x3000\n" \
M_"a[52:55], a[228:231], a[204:207], a[52:55]\n" \
B_"v24, s[76:79], s84 offen offset:0 lds\n" \
M_"a[48:51], a[160:163], a[140:143], a[48:51]\n" \
X_"a[186:187], v38 offset:33792\n" \
C_"v189, v190, v191\n" \
M_"a[48:51], a[224:227], a[204:207], a[48:51]\n" \
C_"v190, v180, v181\n" \
C_"v191, v182, v183\n" \
M_"a[64:67], a[160:163], a[144:147], a[64:67]\n" \
X_"a[248:249], v38 offset:40960\n" \
"v_accvgpr_read_b32 v192, a24\n" \
M_"a[64:67], a[224:227], a[208:211], a[64:67]\n" \
"s_addk_i32 s84, 0x2000\n" \
"s_nop 0\n" \
M_"a[68:71], a[164:167], a[144:147], a[68:71]\n" \
B_"v25, s[76:79], s84 offen offset:1024 lds\n" \
M_"a[68:71], a[228:231], a[208:211], a[68:71]\n" \
X_"a[250:251], v38 offset:41984\n" \
"v_accvgpr_read_b32 v193, a25\n" \
M_"a[72:75], a[168:171], a[144:147], a[72:75]\n" \
"v_accvgpr_read_b32 v194, a26\n" \
"v_accvgpr_read_b32 v195, a27\n" \
M_"a[72:75], a[232:235], a[208:211], a[72:75]\n" \
"v_accvgpr_read_b32 v180, a28\n" \
"v_accvgpr_read_b32 v181, a29\n" \
M_"a[76:79], a[172:175], a[144:147], a[76:79]\n" \
"s_addk_i32 s84, 0x2000\n" \
M_"a[76:79], a[236:239], a[208:211], a[76:79]\n" \
B_"v24, s[76:79], s84 offen offset:2048 lds\n" \
M_"a[92:95], a[172:175], a[148:151], a[92:95]\n" \
"s_add_u32 s89, s41, 0x0\n" \
"buffer_store_dwordx4 v[184:187], v20, s[80:83], s89 offen offset:0 sc1\n" \
M_"a[92:95], a[236:239], a[212:215], a[92:95]\n" \
X_"a[188:189], v39 offset:32768\n" \
"v_accvgpr_read_b32 v182, a30\n" \
M_"a[88:91], a[168:171], a[148:151], a[88:91]\n" \
X_"a[190:191], v39 offset:33792\n" \
"v_accvgpr_read_b32 v183, a31\n" \
M_"a[88:91], a[232:235], a[212:215], a[88:91]\n" \
"s_addk_i32 s84, 0x2000\n" \
M_"a[84:87], a[164:167], a[148:151], a[84:87]\n" \
B_"v25, s[76:79], s84 offen offset:3072 lds\n" \
M_"a[84:87], a[228:231], a[212:215], a[84:87]\n" \
X_"a[252:253], v39 offset:40960\n" \
C_"v192, v192, v193\n" \
M_"a[80:83], a[160:163], a[148:151], a[80:83]\n" \
"s_add_u32 s88, s41, 0x0\n" \
"buffer_store_dwordx4 v[188:191], v20, s[80:83], s88 offen offset:1024 sc1\n" \
M_"a[80:83], a[224:227], a[212:215], a[80:83]\n" \
C_"v193, v194, v195\n" \
C_"v194, v180, v181\n" \
"s_waitcnt lgkmcnt(15)\n" \
"s_nop 0\n" \
M_"a[96:99], a[160:163], a[152:155], a[96:99]\n" \
X_"a[254:255], v39 offset:41984\n" \
C_"v195, v182, v183\n" \
M_"a[96:99], a[224:227], a[216:219], a[96:99]\n" \
"v_accvgpr_read_b32 v196, a16\n" \
"v_accvgpr_read_b32 v197, a17\n" \
M_"a[100:103], a[164:167], a[152:155], a[100:103]\n" \
"v_accvgpr_read_b32 v198, a18\n" \
"v_accvgpr_read_b32 v199, a19\n" \
M_"a[100:103], a[228:231], a[216:219], a[100:103]\n" \
"v_accvgpr_read_b32 v180, a20\n" \
"v_accvgpr_read_b32 v181, a21\n" \
M_"a[104:107], a[168:171], a[152:155], a[104:107]\n" \
"s_add_u32 s89, s41, 0x1000\n" \
"buffer_store_dwordx4 v[192:195], v20, s[80:83], s89 offen offset:1024 sc1\n" \
M_"a[104:107], a[232:235], a[216:219], a[104:107]\n" \
"v_accvgpr_read_b32 v182, a22\n" \
"v_accvgpr_read_b32 v183, a23\n" \
M_"a[108:111], a[172:175], a[152:155], a[108:111]\n" \
C_"v196, v196, v197\n" \
C_"v197, v198, v199\n" \
M_"a[108:111], a[236:239], a[216:219], a[108:111]\n" \
C_"v198, v180, v181\n" \
C_"v199, v182, v183\n" \
M_"a[124:127], a[172:175], a[156:159], a[124:127]\n" \
"v_accvgpr_read_b32 v200, a32\n" \
"v_accvgpr_read_b32 v201, a33\n" \
M_"a[124:127], a[236:239], a[220:223], a[124:127]\n" \
"v_accvgpr_read_b32 v202, a34\n" \
"v_accvgpr_read_b32 v203, a35\n" \
M_"a[120:123], a[168:171], a[156:159], a[120:123]\n" \
"s_add_u32 s88, s41, 0x1000\n" \
"buffer_store_dwordx4 v[196:199], v20, s[80:83], s88 offen offset:0 sc1\n" \
M_"a[120:123], a[232:235], a[220:223], a[120:123]\n" \
"v_accvgpr_read_b32 v180, a36\n" \
"v_accvgpr_read_b32 v181, a37\n" \
M_"a[116:119], a[164:167], a[156:159], a[116:119]\n" \
"v_accvgpr_read_b32 v182, a38\n" \
"v_accvgpr_read_b32 v183, a39\n" \
M_"a[116:119], a[228:231], a[220:223], a[116:119]\n" \
C_"v200, v200, v201\n" \
C_"v201, v202, v203\n" \
M_"a[112:115], a[160:163], a[156:159], a[112:115]\n" \
C_"v202, v180, v181\n" \
C_"v203, v182, v183\n" \
M_"a[112:115], a[224:227], a[220:223], a[112:115]\n" \
"v_accvgpr_read_b32 v204, a40\n" \
"v_accvgpr_read_b32 v205, a41\n" \
"s_waitcnt vmcnt(12) lgkmcnt(0)\n" \
"s_barrier\n" \
"v_mov_b32_e32 v36, v32\n" \
"v_xor_b32_e32 v32, 0x0, v48\n" \
"v_mov_b32_e32 v37, v33\n" \
"v_xor_b32_e32 v33, 0x20, v48\n" \
"v_mov_b32_e32 v38, v34\n" \
"v_xor_b32_e32 v34, 0x40, v48\n" \
"v_mov_b32_e64 v39, v35\n" \
"v_xor_b32_e32 v35, 0x60, v48\n" \
"v_add_u32_e32 v22, s44, v28\n" \
"v_add_u32_e32 v28, s43, v28\n" \
"v_add_u32_e32 v23, s44, v29\n" \
"v_add_u32_e32 v29, s43, v29\n" \
M_"v[52:55], a[176:179], a[128:131], v[52:55]\n" \
"s_add_u32 m0, s40, 0x8000\n" \
"s_add_u32 s85, s39, 0x2000\n" \
M_"v[52:55], a[240:243], a[192:195], v[52:55]\n" \
B_"v20, s[80:83], s85 offen sc1 lds\n" \
M_"v[56:59], a[180:183], a[128:131], v[56:59]\n" \
X_"a[160:161], v32 offset:0\n" \
"v_accvgpr_read_b32 v206, a42\n" \
M_"v[56:59], a[244:247], a[192:195], v[56:59]\n" \
"s_add_u32 m0, s40, 0x9000\n" \
"s_add_u32 s85, s39, 0x3000\n" \
M_"v[60:63], a[184:187], a[128:131], v[60:63]\n" \
B_"v20, s[80:83], s85 offen sc1 lds\n" \
M_"v[60:63], a[248:251], a[192:195], v[60:63]\n" \
X_"a[162:163], v32 offset:512\n" \
"v_accvgpr_read_b32 v207, a43\n" \
M_"v[64:67], a[188:191], a[128:131], v[64:67]\n" \
X_"a[224:225], v32 offset:4096\n" \
"v_accvgpr_read_b32 v180, a44\n" \
M_"v[64:67], a[252:255], a[192:195], v[64:67]\n" \
R_"a[128:131], v28 offset:0\n" \
"v_accvgpr_read_b32 v181, a45\n" \
M_"v[80:83], a[188:191], a[132:135], v[80:83]\n" \
R_"a[192:195], v29 offset:0\n" \
"v_accvgpr_read_b32 v182, a46\n" \
M_"v[80:83], a[252:255], a[196:199], v[80:83]\n" \
"s_add_u32 m0, s40, 0xb000\n" \
"s_movk_i32 s84, 0x80\n" \
M_"v[76:79], a[184:187], a[132:135], v[76:79]\n" \
B_"v21, s[76:79], s84 offen offset:0 lds\n" \
M_"v[76:79], a[248:251], a[196:199], v[76:79]\n" \
"s_add_u32 m0, s30, 0x8000\n" \
"s_movk_i32 s84, 0x80\n" \
M_"v[72:75], a[180:183], a[132:135], v[72:75]\n" \
B_"v24, s[76:79], s84 offen offset:0 lds\n" \
M_"v[72:75], a[244:247], a[196:199], v[72:75]\n" \
X_"a[226:227], v32 offset:4608\n" \
"v_accvgpr_read_b32 v183, a47\n" \
M_"v[68:71], a[176:179], a[132:135], v[68:71]\n" \
X_"a[164:165], v33 offset:0\n" \
C_"v204, v204, v205\n" \
M_"v[68:71], a[240:243], a[196:199], v[68:71]\n" \
R_"a[132:135], v28 offset:2048\n" \
C_"v205, v206, v207\n" \
M_"v[84:87], a[176:179], a[136:139], v[84:87]\n" \
R_"a[196:199], v29 offset:2048\n" \
C_"v206, v180, v181\n" \
M_"v[84:87], a[240:243], a[200:203], v[84:87]\n" \
"s_movk_i32 s84, 0x2080\n" \
M_"v[88:91], a[180:183], a[136:139], v[88:91]\n" \
B_"v25, s[76:79], s84 offen offset:1024 lds\n" \
M_"v[88:91], a[244:247], a[200:203], v[88:91]\n" \
"s_movk_i32 s84, 0x4080\n" \
M_"v[92:95], a[184:187], a[136:139], v[92:95]\n" \
B_"v24, s[76:79], s84 offen offset:2048 lds\n" \
M_"v[92:95], a[248:251], a[200:203], v[92:95]\n" \
X_"a[166:167], v33 offset:512\n" \
C_"v207, v182, v183\n" \
M_"v[96:99], a[188:191], a[136:139], v[96:99]\n" \
X_"a[228:229], v33 offset:4096\n" \
"v_accvgpr_read_b32 v208, a56\n" \
M_"v[96:99], a[252:255], a[200:203], v[96:99]\n" \
R_"a[136:139], v28 offset:4096\n" \
"v_accvgpr_read_b32 v209, a57\n" \
M_"v[112:115], a[188:191], a[140:143], v[112:115]\n" \
R_"a[200:203], v29 offset:4096\n" \
"v_accvgpr_read_b32 v210, a58\n" \
M_"v[112:115], a[252:255], a[204:207], v[112:115]\n" \
"s_movk_i32 s84, 0x6080\n" \
M_"v[108:111], a[184:187], a[140:143], v[108:111]\n" \
B_"v25, s[76:79], s84 offen offset:3072 lds\n" \
M_"v[108:111], a[248:251], a[204:207], v[108:111]\n" \
"s_add_u32 m0, s31, 0x8000\n" \
"s_mov_b32 s84, 0x80000\n" \
M_"v[104:107], a[180:183], a[140:143], v[104:107]\n" \
B_"v26, s[68:71], s84 offen offset:0 lds\n" \
M_"v[104:107], a[244:247], a[204:207], v[104:107]\n" \
"s_addk_i32 s84, 0x7c00\n" \
M_"v[100:103], a[176:179], a[140:143], v[100:103]\n" \
B_"v26, s[68:71], s84 offen offset:1024 lds\n" \
M_"v[100:103], a[240:243], a[204:207], v[100:103]\n" \
R_"a[140:143], v28 offset:6144\n" \
"v_accvgpr_read_b32 v211, a59\n" \
M_"v[116:119], a[176:179], a[144:147], v[116:119]\n" \
R_"a[204:207], v29 offset:6144\n" \
"v_accvgpr_read_b32 v180, a60\n" \
M_"v[116:119], a[240:243], a[208:211], v[116:119]\n" \
"s_addk_i32 s84, 0x7c00\n" \
M_"v[120:123], a[180:183], a[144:147], v[120:123]\n" \
B_"v27, s[68:71], s84 offen offset:2048 lds\n" \
M_"v[120:123], a[244:247], a[208:211], v[120:123]\n" \
X_"a[230:231], v33 offset:4608\n" \
"v_accvgpr_read_b32 v181, a61\n" \
M_"v[124:127], a[184:187], a[144:147], v[124:127]\n" \
"s_addk_i32 s84, 0x7c00\n" \
M_"v[124:127], a[248:251], a[208:211], v[124:127]\n" \
B_"v27, s[68:71], s84 offen offset:3072 lds\n" \
M_"v[128:131], a[188:191], a[144:147], v[128:131]\n" \
X_"a[172:173], v35 offset:0\n" \
"v_accvgpr_read_b32 v182, a62\n" \
M_"v[128:131], a[252:255], a[208:211], v[128:131]\n" \
R_"a[144:147], v28 offset:8192\n" \
"v_accvgpr_read_b32 v183, a63\n" \
M_"v[144:147], a[188:191], a[148:151], v[144:147]\n" \
R_"a[208:211], v29 offset:8192\n" \
C_"v208, v208, v209\n" \
M_"v[144:147], a[252:255], a[212:215], v[144:147]\n" \
"s_add_u32 m0, s30, 0x9000\n" \
"s_mov_b32 s84, 0x9080\n" \
M_"v[140:143], a[184:187], a[148:151], v[140:143]\n" \
B_"v24, s[76:79], s84 offen offset:0 lds\n" \
M_"v[140:143], a[248:251], a[212:215], v[140:143]\n" \
X_"a[174:175], v35 offset:512\n" \
C_"v209, v210, v211\n" \
M_"v[136:139], a[180:183], a[148:151], v[136:139]\n" \
"s_addk_i32 s84, 0x2000\n" \
"s_nop 0\n" \
M_"v[136:139], a[244:247], a[212:215], v[136:139]\n" \
B_"v25, s[76:79], s84 offen offset:1024 lds\n" \
M_"v[132:135], a[176:179], a[148:151], v[132:135]\n" \
X_"a[236:237], v35 offset:4096\n" \
C_"v210, v180, v181\n" \
M_"v[132:135], a[240:243], a[212:215], v[132:135]\n" \
R_"a[148:151], v28 offset:10240\n" \
C_"v211, v182, v183\n" \
M_"v[148:151], a[176:179], a[152:155], v[148:151]\n" \
R_"a[212:215], v29 offset:10240\n" \
"v_accvgpr_read_b32 v212, a48\n" \
M_"v[148:151], a[240:243], a[216:219], v[148:151]\n" \
X_"a[238:239], v35 offset:4608\n" \
"v_accvgpr_read_b32 v213, a49\n" \
M_"v[152:155], a[180:183], a[152:155], v[152:155]\n" \
X_"a[168:169], v34 offset:0\n" \
"v_accvgpr_read_b32 v214, a50\n" \
M_"v[152:155], a[244:247], a[216:219], v[152:155]\n" \
X_"a[170:171], v34 offset:512\n" \
"v_accvgpr_read_b32 v215, a51\n" \
M_"v[156:159], a[184:187], a[152:155], v[156:159]\n" \
X_"a[232:233], v34 offset:4096\n" \
"v_accvgpr_read_b32 v180, a52\n" \
M_"v[156:159], a[248:251], a[216:219], v[156:159]\n" \
X_"a[234:235], v34 offset:4608\n" \
"v_accvgpr_read_b32 v181, a53\n" \
M_"v[160:163], a[188:191], a[152:155], v[160:163]\n" \
R_"v[248:251], v22 offset:0\n" \
"v_accvgpr_read_b32 v182, a54\n" \
M_"v[160:163], a[252:255], a[216:219], v[160:163]\n" \
R_"a[152:155], v28 offset:12288\n" \
"v_accvgpr_read_b32 v183, a55\n" \
M_"v[176:179], a[188:191], a[156:159], v[176:179]\n" \
R_"a[216:219], v29 offset:12288\n" \
C_"v212, v212, v213\n" \
M_"v[176:179], a[252:255], a[220:223], v[176:179]\n" \
R_"v[48:51], v23 offset:0\n" \
C_"v213, v214, v215\n" \
M_"v[172:175], a[184:187], a[156:159], v[172:175]\n" \
C_"v214, v180, v181\n" \
C_"v215, v182, v183\n" \
M_"v[172:175], a[248:251], a[220:223], v[172:175]\n" \
"v_accvgpr_read_b32 v216, a64\n" \
"v_accvgpr_read_b32 v217, a65\n" \
M_"v[168:171], a[180:183], a[156:159], v[168:171]\n" \
"v_accvgpr_read_b32 v218, a66\n" \
"v_accvgpr_read_b32 v219, a67\n" \
M_"v[168:171], a[244:247], a[220:223], v[168:171]\n" \
"v_accvgpr_read_b32 v180, a68\n" \
"v_accvgpr_read_b32 v181, a69\n" \
M_"v[164:167], a[176:179], a[156:159], v[164:167]\n" \
"v_accvgpr_read_b32 v182, a70\n" \
"v_accvgpr_read_b32 v183, a71\n" \
M_"v[164:167], a[240:243], a[220:223], v[164:167]\n" \
R_"a[156:159], v28 offset:14336\n" \
R_"a[220:223], v29 offset:14336\n" \
"s_waitcnt lgkmcnt(15)\n" \
M_"a[0:3], a[160:163], a[128:131], 0\n" \
"s_addk_i32 s84, 0x2000\n" \
C_"v216, v216, v217\n" \
M_"a[0:3], a[224:227], a[192:195], a[0:3]\n" \
B_"v24, s[76:79], s84 offen offset:2048 lds\n" \
C_"v217, v218, v219\n" \
M_"a[4:7], a[164:167], a[128:131], 0\n" \
X_"a[176:177], v36 offset:0\n" \
C_"v218, v180, v181\n" \
M_"a[4:7], a[228:231], a[192:195], a[4:7]\n" \
"s_addk_i32 s84, 0x2000\n" \
C_"v219, v182, v183\n" \
"s_waitcnt lgkmcnt(15)\n" \
M_"a[12:15], a[172:175], a[128:131], 0\n" \
B_"v25, s[76:79], s84 offen offset:3072 lds\n" \
"v_accvgpr_read_b32 v220, a80\n" \
".fill (L_pb + 20192 - .) / 4, 4, 0xBF800000\n" \
"L_pp5:\n" \
"s_cbranch_execz L_pret\n" \
"s_waitcnt lgkmcnt(11)\n" \
M_"a[12:15], a[236:239], a[192:195], a[12:15]\n" \
X_"a[178:179], v36 offset:1024\n" \
"v_accvgpr_read_b32 v221, a81\n" \
"s_waitcnt lgkmcnt(10)\n" \
M_"a[8:11], a[128:131], a[168:171], 0\n" \
X_"a[240:241], v36 offset:8192\n" \
"v_accvgpr_read_b32 v222, a82\n" \
"s_waitcnt lgkmcnt(9)\n" \
M_"a[8:11], a[192:195], a[232:235], a[8:11]\n" \
X_"a[242:243], v36 offset:9216\n" \
"v_accvgpr_read_b32 v223, a83\n" \
M_"a[24:27], a[132:135], a[168:171], 0\n" \
X_"a[180:181], v37 offset:0\n" \
"v_accvgpr_read_b32 v180, a84\n" \
M_"a[24:27], a[196:199], a[232:235], a[24:27]\n" \
X_"a[182:183], v37 offset:1024\n" \
"v_accvgpr_read_b32 v181, a85\n" \
M_"a[28:31], a[172:175], a[132:135], 0\n" \
X_"a[244:245], v37 offset:8192\n" \
"v_accvgpr_read_b32 v182, a86\n" \
M_"a[28:31], a[236:239], a[196:199], a[28:31]\n" \
"s_add_u32 s89, s41, 0x2000\n" \
"buffer_store_dwordx4 v[200:203], v20, s[80:83], s89 offen offset:0 sc1\n" \
M_"a[20:23], a[164:167], a[132:135], 0\n" \
X_"a[246:247], v37 offset:9216\n" \
"v_accvgpr_read_b32 v183, a87\n" \
M_"a[20:23], a[228:231], a[196:199], a[20:23]\n" \
X_"a[184:185], v38 offset:0\n" \
C_"v220, v220, v221\n" \
M_"a[16:19], a[160:163], a[132:135], 0\n" \
X_"a[186:187], v38 offset:1024\n" \
C_"v221, v222, v223\n" \
M_"a[16:19], a[224:227], a[196:199], a[16:19]\n" \
C_"v222, v180, v181\n" \
C_"v223, v182, v183\n" \
C_"v224, v52, v53\n" \
M_"a[32:35], a[160:163], a[136:139], 0\n" \
"s_add_u32 s88, s41, 0x2000\n" \
"buffer_store_dwordx4 v[204:207], v20, s[80:83], s88 offen offset:1024 sc1\n" \
M_"a[32:35], a[224:227], a[200:203], a[32:35]\n" \
X_"a[248:249], v38 offset:8192\n" \
C_"v225, v54, v55\n" \
M_"a[36:39], a[164:167], a[136:139], 0\n" \
X_"a[250:251], v38 offset:9216\n" \
C_"v226, v56, v57\n" \
M_"a[36:39], a[228:231], a[200:203], a[36:39]\n" \
X_"a[188:189], v39 offset:0\n" \
C_"v227, v58, v59\n" \
M_"a[44:47], a[172:175], a[136:139], 0\n" \
C_"v228, v60, v61\n" \
C_"v229, v62, v63\n" \
C_"v230, v64, v65\n" \
M_"a[44:47], a[236:239], a[200:203], a[44:47]\n" \
X_"a[190:191], v39 offset:1024\n" \
C_"v231, v66, v67\n" \
M_"a[40:43], a[136:139], a[168:171], 0\n" \
"s_add_u32 s89, s41, 0x3000\n" \
"buffer_store_dwordx4 v[208:211], v20, s[80:83], s89 offen offset:1024 sc1\n" \
M_"a[40:43], a[200:203], a[232:235], a[40:43]\n" \
X_"a[252:253], v39 offset:8192\n" \
C_"v232, v76, v77\n" \
M_"a[56:59], a[140:143], a[168:171], 0\n" \
X_"a[254:255], v39 offset:9216\n" \
C_"v233, v78, v79\n" \
M_"a[56:59], a[204:207], a[232:235], a[56:59]\n" \
C_"v234, v80, v81\n" \
C_"v235, v82, v83\n" \
C_"v236, v68, v69\n" \
M_"a[60:63], a[172:175], a[140:143], 0\n" \
C_"v237, v70, v71\n" \
C_"v238, v72, v73\n" \
C_"v239, v74, v75\n" \
M_"a[60:63], a[236:239], a[204:207], a[60:63]\n" \
"s_add_u32 s88, s41, 0x3000\n" \
"buffer_store_dwordx4 v[212:215], v20, s[80:83], s88 offen offset:0 sc1\n" \
M_"a[52:55], a[164:167], a[140:143], 0\n" \
C_"v240, v84, v85\n" \
C_"v241, v86, v87\n" \
C_"v242, v88, v89\n" \
M_"a[52:55], a[228:231], a[204:207], a[52:55]\n" \
C_"v243, v90, v91\n" \
C_"v244, v92, v93\n" \
C_"v245, v94, v95\n" \
M_"a[48:51], a[160:163], a[140:143], 0\n" \
C_"v246, v96, v97\n" \
C_"v247, v98, v99\n" \
C_"v184, v108, v109\n" \
M_"a[48:51], a[224:227], a[204:207], a[48:51]\n" \
C_"v185, v110, v111\n" \
C_"v186, v112, v113\n" \
C_"v187, v114, v115\n" \
"s_waitcnt lgkmcnt(15)\n" \
"s_nop 0\n" \
M_"a[64:67], a[164:167], v[248:251], 0\n" \
C_"v188, v100, v101\n" \
C_"v189, v102, v103\n" \
C_"v190, v104, v105\n" \
M_"a[64:67], a[228:231], v[48:51], a[64:67]\n" \
C_"v191, v106, v107\n" \
C_"v192, v116, v117\n" \
C_"v193, v118, v119\n" \
M_"a[80:83], a[172:175], v[248:251], 0\n" \
C_"v194, v120, v121\n" \
C_"v195, v122, v123\n" \
C_"v196, v124, v125\n" \
M_"a[80:83], a[236:239], v[48:51], a[80:83]\n" \
C_"v197, v126, v127\n" \
C_"v198, v128, v129\n" \
C_"v199, v130, v131\n" \
M_"a[68:71], v[248:251], a[168:171], 0\n" \
C_"v252, v140, v141\n" \
C_"v253, v142, v143\n" \
C_"v254, v144, v145\n" \
M_"a[68:71], v[48:51], a[232:235], a[68:71]\n" \
C_"v255, v146, v147\n" \
"v_accvgpr_write_b32 a84, v252\n" \
"v_accvgpr_write_b32 a85, v253\n" \
"s_waitcnt vmcnt(4) lgkmcnt(0)\n" \
"s_barrier\n" \
M_"v[52:55], a[176:179], a[128:131], 0\n" \
"s_add_u32 m0, s40, 0x0\n" \
"s_nop 0\n" \
"s_add_u32 s85, s39, 0x4000\n" \
"v_accvgpr_write_b32 a86, v254\n" \
M_"v[52:55], a[240:243], a[192:195], v[52:55]\n" \
B_"v20, s[80:83], s85 offen sc1 lds\n" \
"v_accvgpr_write_b32 a87, v255\n" \
M_"v[56:59], a[180:183], a[128:131], 0\n" \
X_"a[160:161], v32 offset:32768\n" \
C_"v252, v132, v133\n" \
M_"v[56:59], a[244:247], a[192:195], v[56:59]\n" \
X_"a[162:163], v32 offset:33280\n" \
C_"v253, v134, v135\n" \
M_"v[60:63], a[184:187], a[128:131], 0\n" \
"s_add_u32 m0, s40, 0x1000\n" \
"s_add_u32 s85, s39, 0x5000\n" \
C_"v254, v136, v137\n" \
M_"v[60:63], a[248:251], a[192:195], v[60:63]\n" \
B_"v20, s[80:83], s85 offen sc1 lds\n" \
C_"v255, v138, v139\n" \
M_"v[64:67], a[188:191], a[128:131], 0\n" \
X_"a[224:225], v32 offset:36864\n" \
C_"v200, v148, v149\n" \
M_"v[64:67], a[252:255], a[192:195], v[64:67]\n" \
R_"a[128:131], v28 offset:32768\n" \
C_"v201, v150, v151\n" \
M_"v[80:83], a[188:191], a[132:135], 0\n" \
R_"a[192:195], v29 offset:32768\n" \
C_"v202, v152, v153\n" \
M_"v[80:83], a[252:255], a[196:199], v[80:83]\n" \
"s_add_u32 m0, s40, 0x3000\n" \
"s_movk_i32 s84, 0x100\n" \
"s_nop 0\n" \
C_"v203, v154, v155\n" \
M_"v[76:79], a[184:187], a[132:135], 0\n" \
B_"v21, s[76:79], s84 offen offset:0 lds\n" \
C_"v204, v156, v157\n" \
M_"v[76:79], a[248:251], a[196:199], v[76:79]\n" \
X_"a[226:227], v32 offset:37376\n" \
C_"v205, v158, v159\n" \
M_"v[72:75], a[180:183], a[132:135], 0\n" \
X_"a[164:165], v33 offset:32768\n" \
C_"v206, v160, v161\n" \
M_"v[72:75], a[244:247], a[196:199], v[72:75]\n" \
C_"v207, v162, v163\n" \
C_"v208, v172, v173\n" \
C_"v209, v174, v175\n" \
M_"v[68:71], a[176:179], a[132:135], 0\n" \
X_"a[166:167], v33 offset:33280\n" \
C_"v210, v176, v177\n" \
M_"v[68:71], a[240:243], a[196:199], v[68:71]\n" \
R_"a[132:135], v28 offset:34816\n" \
C_"v211, v178, v179\n" \
M_"v[84:87], a[176:179], a[136:139], 0\n" \
R_"a[196:199], v29 offset:34816\n" \
C_"v212, v164, v165\n" \
M_"v[84:87], a[240:243], a[200:203], v[84:87]\n" \
"s_add_u32 m0, s31, 0x0\n" \
"s_mov_b32 s84, 0x100000\n" \
C_"v213, v166, v167\n" \
M_"v[88:91], a[180:183], a[136:139], 0\n" \
B_"v26, s[68:71], s84 offen offset:0 lds\n" \
C_"v214, v168, v169\n" \
M_"v[88:91], a[244:247], a[200:203], v[88:91]\n" \
"s_add_u32 s89, s41, 0x0\n" \
"buffer_store_dwordx4 v[224:227], v20, s[80:83], s89 offen offset:2048 sc1\n" \
M_"v[92:95], a[184:187], a[136:139], 0\n" \
X_"a[228:229], v33 offset:36864\n" \
C_"v215, v170, v171\n" \
M_"v[92:95], a[248:251], a[200:203], v[92:95]\n" \
X_"a[230:231], v33 offset:37376\n" \
M_"v[96:99], a[188:191], a[136:139], 0\n" \
M_"v[96:99], a[252:255], a[200:203], v[96:99]\n" \
R_"a[136:139], v28 offset:36864\n" \
M_"v[112:115], a[188:191], a[140:143], 0\n" \
R_"a[200:203], v29 offset:36864\n" \
M_"v[112:115], a[252:255], a[204:207], v[112:115]\n" \
"s_addk_i32 s84, 0x7c00\n" \
M_"v[108:111], a[184:187], a[140:143], 0\n" \
B_"v26, s[68:71], s84 offen offset:1024 lds\n" \
M_"v[108:111], a[248:251], a[204:207], v[108:111]\n" \
X_"a[172:173], v35 offset:32768\n" \
M_"v[104:107], a[180:183], a[140:143], 0\n" \
"s_addk_i32 s84, 0x7c00\n" \
M_"v[104:107], a[244:247], a[204:207], v[104:107]\n" \
B_"v27, s[68:71], s84 offen offset:2048 lds\n" \
M_"v[100:103], a[176:179], a[140:143], 0\n" \
X_"a[174:175], v35 offset:33280\n" \
M_"v[100:103], a[240:243], a[204:207], v[100:103]\n" \
R_"a[140:143], v28 offset:38912\n" \
M_"v[116:119], a[176:179], a[144:147], 0\n" \
R_"a[204:207], v29 offset:38912\n" \
M_"v[116:119], a[240:243], a[208:211], v[116:119]\n" \
"s_addk_i32 s84, 0x7c00\n" \
M_"v[120:123], a[180:183], a[144:147], 0\n" \
B_"v27, s[68:71], s84 offen offset:3072 lds\n" \
M_"v[120:123], a[244:247], a[208:211], v[120:123]\n" \
X_"a[236:237], v35 offset:36864\n" \
M_"v[124:127], a[184:187], a[144:147], 0\n" \
X_"a[238:239], v35 offset:37376\n" \
M_"v[124:127], a[248:251], a[208:211], v[124:127]\n" \
"s_add_u32 m0, s30, 0x0\n" \
"s_movk_i32 s84, 0x180\n" \
M_"v[128:131], a[188:191], a[144:147], 0\n" \
B_"v24, s[76:79], s84 offen offset:0 lds\n" \
M_"v[128:131], a[252:255], a[208:211], v[128:131]\n" \
R_"a[144:147], v28 offset:40960\n" \
M_"v[144:147], a[188:191], a[148:151], 0\n" \
R_"a[208:211], v29 offset:40960\n" \
M_"v[144:147], a[252:255], a[212:215], v[144:147]\n" \
X_"a[168:169], v34 offset:32768\n" \
M_"v[140:143], a[184:187], a[148:151], 0\n" \
"s_movk_i32 s84, 0x2180\n" \
M_"v[140:143], a[248:251], a[212:215], v[140:143]\n" \
B_"v25, s[76:79], s84 offen offset:1024 lds\n" \
M_"v[136:139], a[180:183], a[148:151], 0\n" \
X_"a[170:171], v34 offset:33280\n" \
M_"v[136:139], a[244:247], a[212:215], v[136:139]\n" \
X_"a[232:233], v34 offset:36864\n" \
M_"v[132:135], a[176:179], a[148:151], 0\n" \
X_"a[234:235], v34 offset:37376\n" \
M_"v[132:135], a[240:243], a[212:215], v[132:135]\n" \
R_"a[148:151], v28 offset:43008\n" \
M_"v[148:151], a[176:179], a[152:155], 0\n" \
R_"a[212:215], v29 offset:43008\n" \
M_"v[148:151], a[240:243], a[216:219], v[148:151]\n" \
"s_movk_i32 s84, 0x4180\n" \
M_"v[152:155], a[180:183], a[152:155], 0\n" \
B_"v24, s[76:79], s84 offen offset:2048 lds\n" \
M_"v[152:155], a[244:247], a[216:219], v[152:155]\n" \
"s_movk_i32 s84, 0x6180\n" \
M_"v[156:159], a[184:187], a[152:155], 0\n" \
B_"v25, s[76:79], s84 offen offset:3072 lds\n" \
M_"v[156:159], a[248:251], a[216:219], v[156:159]\n" \
R_"v[248:251], v22 offset:32768\n" \
M_"v[160:163], a[188:191], a[152:155], 0\n" \
R_"v[48:51], v23 offset:32768\n" \
M_"v[160:163], a[252:255], a[216:219], v[160:163]\n" \
R_"a[152:155], v28 offset:45056\n" \
M_"v[176:179], a[188:191], a[156:159], 0\n" \
R_"a[216:219], v29 offset:45056\n" \
M_"v[176:179], a[252:255], a[220:223], v[176:179]\n" \
M_"v[172:175], a[184:187], a[156:159], 0\n" \
M_"v[172:175], a[248:251], a[220:223], v[172:175]\n" \
M_"v[168:171], a[180:183], a[156:159], 0\n" \
M_"v[168:171], a[244:247], a[220:223], v[168:171]\n" \
M_"v[164:167], a[176:179], a[156:159], 0\n" \
M_"v[164:167], a[240:243], a[220:223], v[164:167]\n" \
R_"a[156:159], v28 offset:47104\n" \
R_"a[220:223], v29 offset:47104\n" \
"s_waitcnt lgkmcnt(15)\n" \
M_"a[0:3], a[160:163], a[128:131], a[0:3]\n" \
"s_add_u32 m0, s30, 0x1000\n" \
"s_addk_i32 s84, 0x3000\n" \
M_"a[0:3], a[224:227], a[192:195], a[0:3]\n" \
B_"v24, s[76:79], s84 offen offset:0 lds\n" \
M_"a[4:7], a[164:167], a[128:131], a[4:7]\n" \
X_"a[176:177], v36 offset:32768\n" \
"v_mov_b32_e64 v11, 0\n" \
M_"a[4:7], a[228:231], a[192:195], a[4:7]\n" \
X_"a[178:179], v36 offset:33792\n" \
"s_and_b32 s5, s2, 7\n" \
M_"a[12:15], a[172:175], a[128:131], a[12:15]\n" \
X_"a[240:241], v36 offset:40960\n" \
"s_lshl_b32 s6, s3, 1\n" \
"s_waitcnt lgkmcnt(15)\n" \
M_"a[12:15], a[236:239], a[192:195], a[12:15]\n" \
"s_add_u32 s88, s41, 0x0\n" \
"buffer_store_dwordx4 v[228:231], v20, s[80:83], s88 offen offset:3072 sc1\n" \
"s_waitcnt lgkmcnt(13)\n" \
M_"a[8:11], a[128:131], a[168:171], a[8:11]\n" \
X_"a[242:243], v36 offset:41984\n" \
"s_lshl_b32 s4, s5, 2\n" \
"s_waitcnt lgkmcnt(12)\n" \
M_"a[8:11], a[192:195], a[232:235], a[8:11]\n" \
X_"a[180:181], v37 offset:32768\n" \
"s_bfe_u32 s7, s2, 0x20003\n" \
M_"a[24:27], a[132:135], a[168:171], a[24:27]\n" \
X_"a[182:183], v37 offset:33792\n" \
"s_or_b32 s12, s4, s7\n" \
M_"a[24:27], a[196:199], a[232:235], a[24:27]\n" \
X_"a[244:245], v37 offset:40960\n" \
"s_and_b32 s6, s6, 4\n" \
M_"a[28:31], a[172:175], a[132:135], a[28:31]\n" \
"s_and_b32 s8, s3, 1\n" \
"s_or_b32 s8, s6, s8\n" \
M_"a[28:31], a[236:239], a[196:199], a[28:31]\n" \
"s_addk_i32 s84, 0x2000\n" \
M_"a[20:23], a[164:167], a[132:135], a[20:23]\n" \
B_"v25, s[76:79], s84 offen offset:1024 lds\n" \
M_"a[20:23], a[228:231], a[196:199], a[20:23]\n" \
X_"a[246:247], v37 offset:41984\n" \
"s_lshl_b32 s6, s12, 4\n" \
M_"a[16:19], a[160:163], a[132:135], a[16:19]\n" \
X_"a[184:185], v38 offset:32768\n" \
"s_lshl_b32 s9, s3, 2\n" \
M_"a[16:19], a[224:227], a[196:199], a[16:19]\n" \
X_"a[186:187], v38 offset:33792\n" \
"s_or_b32 s6, s6, s9\n" \
M_"a[32:35], a[160:163], a[136:139], a[32:35]\n" \
"s_addk_i32 s84, 0x2000\n" \
M_"a[32:35], a[224:227], a[200:203], a[32:35]\n" \
B_"v24, s[76:79], s84 offen offset:2048 lds\n" \
M_"a[36:39], a[164:167], a[136:139], a[36:39]\n" \
X_"a[248:249], v38 offset:40960\n" \
"s_lshr_b32 s9, s2, 5\n" \
M_"a[36:39], a[228:231], a[200:203], a[36:39]\n" \
X_"a[250:251], v38 offset:41984\n" \
"s_mul_i32 s10, s9, 0xa00\n" \
M_"a[44:47], a[172:175], a[136:139], a[44:47]\n" \
X_"a[188:189], v39 offset:32768\n" \
"s_add_i32 s10, s10, 0x5bc1000\n" \
M_"a[44:47], a[236:239], a[200:203], a[44:47]\n" \
X_"a[190:191], v39 offset:33792\n" \
"s_or_b32 s6, s6, s10\n" \
M_"a[40:43], a[136:139], a[168:171], a[40:43]\n" \
"s_add_u32 s89, s41, 0x1000\n" \
"buffer_store_dwordx4 v[232:235], v20, s[80:83], s89 offen offset:3072 sc1\n" \
M_"a[40:43], a[200:203], a[232:235], a[40:43]\n" \
X_"a[252:253], v39 offset:40960\n" \
"s_lshl_b32 s13, s9, 3\n" \
M_"a[56:59], a[140:143], a[168:171], a[56:59]\n" \
X_"a[254:255], v39 offset:41984\n" \
"v_writelane_b32 v11, s6, 0\n" \
M_"a[56:59], a[204:207], a[232:235], a[56:59]\n" \
"s_or_b32 s6, s13, s5\n" \
"v_writelane_b32 v11, s10, 1\n" \
M_"a[60:63], a[172:175], a[140:143], a[60:63]\n" \
"s_lshl_b32 s6, s6, 10\n" \
"s_lshl_b32 s10, s3, 8\n" \
M_"a[60:63], a[236:239], a[204:207], a[60:63]\n" \
"s_addk_i32 s84, 0x2000\n" \
M_"a[52:55], a[164:167], a[140:143], a[52:55]\n" \
B_"v25, s[76:79], s84 offen offset:3072 lds\n" \
M_"a[52:55], a[228:231], a[204:207], a[52:55]\n" \
"s_or_b32 s6, s6, s10\n" \
"s_or_b32 s10, s6, 0x5c00200\n" \
M_"a[48:51], a[160:163], a[140:143], a[48:51]\n" \
"v_writelane_b32 v11, s10, 2\n" \
"s_lshl_b32 s10, s7, 2\n" \
M_"a[48:51], a[224:227], a[204:207], a[48:51]\n" \
"s_or_b32 s11, s10, s6\n" \
"s_or_b32 s11, s11, 0x5c00000\n" \
"s_waitcnt lgkmcnt(15)\n" \
M_"a[64:67], a[164:167], v[248:251], a[64:67]\n" \
"v_writelane_b32 v11, s11, 3\n" \
"s_or_b32 s6, s6, 0x5c00080\n" \
M_"a[64:67], a[228:231], v[48:51], a[64:67]\n" \
"v_writelane_b32 v11, s6, 4\n" \
"s_or_b32 s6, s6, s10\n" \
M_"a[80:83], a[172:175], v[248:251], a[80:83]\n" \
"s_add_i32 s10, s3, s2\n" \
"v_writelane_b32 v11, s6, 5\n" \
M_"a[80:83], a[236:239], v[48:51], a[80:83]\n" \
"s_mov_b32 s6, 2.0\n" \
"s_mov_b32 s11, 0x5bc0000\n" \
M_"a[68:71], v[248:251], a[168:171], a[68:71]\n" \
"s_cmp_lg_u32 s10, 0\n" \
"s_cselect_b32 s10, s6, s11\n" \
"s_waitcnt vmcnt(10) lgkmcnt(0)\n" \
M_"a[68:71], v[48:51], a[232:235], a[68:71]\n" \
"s_barrier\n" \
"v_xor_b32_e32 v28, 0x20000, v28\n" \
"v_xor_b32_e32 v29, 0x20000, v29\n" \
M_"v[52:55], a[176:179], a[128:131], v[52:55]\n" \
"s_add_u32 m0, s40, 0x8000\n" \
"s_add_u32 s85, s39, 0x6000\n" \
M_"v[52:55], a[240:243], a[192:195], v[52:55]\n" \
B_"v20, s[80:83], s85 offen sc1 lds\n" \
M_"v[56:59], a[180:183], a[128:131], v[56:59]\n" \
X_"a[160:161], v32 offset:0\n" \
"s_lshr_b32 s14, s2, 3\n" \
M_"v[56:59], a[244:247], a[192:195], v[56:59]\n" \
X_"a[162:163], v32 offset:512\n" \
"v_writelane_b32 v11, s10, 57\n" \
M_"v[60:63], a[184:187], a[128:131], v[60:63]\n" \
"s_add_u32 m0, s40, 0x9000\n" \
"s_add_u32 s85, s39, 0x7000\n" \
M_"v[60:63], a[248:251], a[192:195], v[60:63]\n" \
B_"v20, s[80:83], s85 offen sc1 lds\n" \
M_"v[64:67], a[188:191], a[128:131], v[64:67]\n" \
X_"a[224:225], v32 offset:4096\n" \
"s_lshl_b32 s10, s5, 5\n" \
M_"v[64:67], a[252:255], a[192:195], v[64:67]\n" \
R_"a[128:131], v28 offset:0\n" \
"s_min_u32 s15, s3, 2\n" \
"s_nop 0\n" \
M_"v[80:83], a[188:191], a[132:135], v[80:83]\n" \
R_"a[192:195], v29 offset:0\n" \
"s_mul_i32 s11, s7, 0x120\n" \
M_"v[80:83], a[252:255], a[196:199], v[80:83]\n" \
"s_add_u32 s88, s41, 0x1000\n" \
"buffer_store_dwordx4 v[236:239], v20, s[80:83], s88 offen offset:2048 sc1\n" \
M_"v[76:79], a[184:187], a[132:135], v[76:79]\n" \
X_"a[226:227], v32 offset:4608\n" \
M_"v[76:79], a[248:251], a[196:199], v[76:79]\n" \
X_"a[164:165], v33 offset:0\n" \
M_"v[72:75], a[180:183], a[132:135], v[72:75]\n" \
"s_add_u32 m0, s40, 0xb000\n" \
"s_movk_i32 s84, 0x180\n" \
"s_nop 0\n" \
M_"v[72:75], a[244:247], a[196:199], v[72:75]\n" \
B_"v21, s[76:79], s84 offen offset:0 lds\n" \
M_"v[68:71], a[176:179], a[132:135], v[68:71]\n" \
X_"a[166:167], v33 offset:512\n" \
M_"v[68:71], a[240:243], a[196:199], v[68:71]\n" \
R_"a[132:135], v28 offset:2048\n" \
M_"v[84:87], a[176:179], a[136:139], v[84:87]\n" \
R_"a[196:199], v29 offset:2048\n" \
M_"v[84:87], a[240:243], a[200:203], v[84:87]\n" \
"s_add_u32 m0, s31, 0x8000\n" \
"s_mov_b32 s84, 0x180000\n" \
M_"v[88:91], a[180:183], a[136:139], v[88:91]\n" \
B_"v26, s[68:71], s84 offen offset:0 lds\n" \
M_"v[88:91], a[244:247], a[200:203], v[88:91]\n" \
X_"a[228:229], v33 offset:4096\n" \
M_"v[92:95], a[184:187], a[136:139], v[92:95]\n" \
X_"a[230:231], v33 offset:4608\n" \
M_"v[92:95], a[248:251], a[200:203], v[92:95]\n" \
"s_addk_i32 s84, 0x7c00\n" \
M_"v[96:99], a[188:191], a[136:139], v[96:99]\n" \
B_"v26, s[68:71], s84 offen offset:1024 lds\n" \
M_"v[96:99], a[252:255], a[200:203], v[96:99]\n" \
R_"a[136:139], v28 offset:4096\n" \
M_"v[112:115], a[188:191], a[140:143], v[112:115]\n" \
R_"a[200:203], v29 offset:4096\n" \
M_"v[112:115], a[252:255], a[204:207], v[112:115]\n" \
"s_addk_i32 s84, 0x7c00\n" \
M_"v[108:111], a[184:187], a[140:143], v[108:111]\n" \
B_"v27, s[68:71], s84 offen offset:2048 lds\n" \
M_"v[108:111], a[248:251], a[204:207], v[108:111]\n" \
X_"a[172:173], v35 offset:0\n" \
M_"v[104:107], a[180:183], a[140:143], v[104:107]\n" \
X_"a[174:175], v35 offset:512\n" \
M_"v[104:107], a[244:247], a[204:207], v[104:107]\n" \
X_"a[236:237], v35 offset:4096\n" \
M_"v[100:103], a[176:179], a[140:143], v[100:103]\n" \
"s_add_u32 s16, s22, s11\n" \
"s_addc_u32 s17, s23, 0\n" \
M_"v[100:103], a[240:243], a[204:207], v[100:103]\n" \
R_"a[140:143], v28 offset:6144\n" \
M_"v[116:119], a[176:179], a[144:147], v[116:119]\n" \
R_"a[204:207], v29 offset:6144\n" \
M_"v[116:119], a[240:243], a[208:211], v[116:119]\n" \
"s_addk_i32 s84, 0x7c00\n" \
M_"v[120:123], a[180:183], a[144:147], v[120:123]\n" \
B_"v27, s[68:71], s84 offen offset:3072 lds\n" \
M_"v[120:123], a[244:247], a[208:211], v[120:123]\n" \
"s_add_u32 m0, s30, 0x8000\n" \
"s_movk_i32 s84, 0x200\n" \
M_"v[124:127], a[184:187], a[144:147], v[124:127]\n" \
B_"v24, s[76:79], s84 offen offset:0 lds\n" \
M_"v[124:127], a[248:251], a[208:211], v[124:127]\n" \
X_"a[238:239], v35 offset:4608\n" \
M_"v[128:131], a[188:191], a[144:147], v[128:131]\n" \
"s_add_u32 s89, s41, 0x2000\n" \
"buffer_store_dwordx4 v[240:243], v20, s[80:83], s89 offen offset:2048 sc1\n" \
M_"v[128:131], a[252:255], a[208:211], v[128:131]\n" \
R_"a[144:147], v28 offset:8192\n" \
M_"v[144:147], a[188:191], a[148:151], v[144:147]\n" \
R_"a[208:211], v29 offset:8192\n" \
M_"v[144:147], a[252:255], a[212:215], v[144:147]\n" \
X_"a[168:169], v34 offset:0\n" \
M_"v[140:143], a[184:187], a[148:151], v[140:143]\n" \
X_"a[170:171], v34 offset:512\n" \
M_"v[140:143], a[248:251], a[212:215], v[140:143]\n" \
"s_movk_i32 s84, 0x2200\n" \
"s_nop 0\n" \
M_"v[136:139], a[180:183], a[148:151], v[136:139]\n" \
B_"v25, s[76:79], s84 offen offset:1024 lds\n" \
M_"v[136:139], a[244:247], a[212:215], v[136:139]\n" \
X_"a[232:233], v34 offset:4096\n" \
M_"v[132:135], a[176:179], a[148:151], v[132:135]\n" \
X_"a[234:235], v34 offset:4608\n" \
M_"v[132:135], a[240:243], a[212:215], v[132:135]\n" \
R_"a[148:151], v28 offset:10240\n" \
M_"v[148:151], a[176:179], a[152:155], v[148:151]\n" \
R_"a[212:215], v29 offset:10240\n" \
M_"v[148:151], a[240:243], a[216:219], v[148:151]\n" \
"s_movk_i32 s84, 0x4200\n" \
M_"v[152:155], a[180:183], a[152:155], v[152:155]\n" \
B_"v24, s[76:79], s84 offen offset:2048 lds\n" \
M_"v[152:155], a[244:247], a[216:219], v[152:155]\n" \
"s_movk_i32 s84, 0x6200\n" \
M_"v[156:159], a[184:187], a[152:155], v[156:159]\n" \
B_"v25, s[76:79], s84 offen offset:3072 lds\n" \
M_"v[156:159], a[248:251], a[216:219], v[156:159]\n" \
R_"v[248:251], v22 offset:0\n" \
M_"v[160:163], a[188:191], a[152:155], v[160:163]\n" \
R_"v[48:51], v23 offset:0\n" \
M_"v[160:163], a[252:255], a[216:219], v[160:163]\n" \
R_"a[152:155], v28 offset:12288\n" \
M_"v[176:179], a[188:191], a[156:159], v[176:179]\n" \
R_"a[216:219], v29 offset:12288\n" \
M_"v[176:179], a[252:255], a[220:223], v[176:179]\n" \
"s_add_u32 s18, s24, s11\n" \
"v_writelane_b32 v11, s16, 9\n" \
"s_addc_u32 s19, s25, 0\n" \
M_"v[172:175], a[184:187], a[156:159], v[172:175]\n" \
"s_and_b32 s20, s17, 0xffff\n" \
"v_writelane_b32 v11, s20, 10\n" \
M_"v[172:175], a[248:251], a[220:223], v[172:175]\n" \
"s_and_b32 s20, s19, 0xffff\n" \
"v_writelane_b32 v11, s18, 11\n" \
M_"v[168:171], a[180:183], a[156:159], v[168:171]\n" \
"s_cmpk_lt_u32 s3, 3\n" \
"s_cselect_b32 s16, s16, s18\n" \
M_"v[168:171], a[244:247], a[220:223], v[168:171]\n" \
"s_movk_i32 s18, 0xfff\n" \
"v_writelane_b32 v11, s20, 12\n" \
M_"v[164:167], a[176:179], a[156:159], v[164:167]\n" \
"s_and_b32 s20, s29, 0xffff\n" \
"v_writelane_b32 v11, s28, 13\n" \
M_"v[164:167], a[240:243], a[220:223], v[164:167]\n" \
R_"a[156:159], v28 offset:14336\n" \
R_"a[220:223], v29 offset:14336\n" \
"s_waitcnt lgkmcnt(15)\n" \
M_"a[0:3], a[160:163], a[128:131], a[0:3]\n" \
"s_add_u32 m0, s30, 0x9000\n" \
"s_addk_i32 s84, 0x3000\n" \
M_"a[0:3], a[224:227], a[192:195], a[0:3]\n" \
B_"v24, s[76:79], s84 offen offset:0 lds\n" \
M_"a[4:7], a[164:167], a[128:131], a[4:7]\n" \
X_"a[176:177], v36 offset:0\n" \
"v_writelane_b32 v11, s20, 14\n" \
M_"a[4:7], a[228:231], a[192:195], a[4:7]\n" \
X_"a[178:179], v36 offset:1024\n" \
"v_writelane_b32 v11, s16, 15\n" \
M_"a[12:15], a[172:175], a[128:131], a[12:15]\n" \
X_"a[240:241], v36 offset:8192\n" \
"s_waitcnt lgkmcnt(15)\n" \
".fill (L_pb + 24288 - .) / 4, 4, 0xBF800000\n" \
"L_pp6:\n" \
"s_cbranch_execz L_pret\n" \
M_"a[12:15], a[236:239], a[192:195], a[12:15]\n" \
"s_add_u32 s88, s41, 0x2000\n" \
"buffer_store_dwordx4 v[244:247], v20, s[80:83], s88 offen offset:3072 sc1\n" \
"s_waitcnt lgkmcnt(13)\n" \
M_"a[8:11], a[128:131], a[168:171], a[8:11]\n" \
X_"a[242:243], v36 offset:9216\n" \
"s_waitcnt lgkmcnt(12)\n" \
M_"a[8:11], a[192:195], a[232:235], a[8:11]\n" \
X_"a[180:181], v37 offset:0\n" \
M_"a[24:27], a[132:135], a[168:171], a[24:27]\n" \
X_"a[182:183], v37 offset:1024\n" \
M_"a[24:27], a[196:199], a[232:235], a[24:27]\n" \
X_"a[244:245], v37 offset:8192\n" \
M_"a[28:31], a[172:175], a[132:135], a[28:31]\n" \
"s_cmpk_lt_u32 s3, 3\n" \
"s_cselect_b32 s16, s17, s19\n" \
M_"a[28:31], a[236:239], a[196:199], a[28:31]\n" \
"s_addk_i32 s84, 0x2000\n" \
M_"a[20:23], a[164:167], a[132:135], a[20:23]\n" \
B_"v25, s[76:79], s84 offen offset:1024 lds\n" \
M_"a[20:23], a[228:231], a[196:199], a[20:23]\n" \
X_"a[246:247], v37 offset:9216\n" \
"s_and_b32 s16, s16, 0xffff\n" \
M_"a[16:19], a[160:163], a[132:135], a[16:19]\n" \
X_"a[184:185], v38 offset:0\n" \
"v_writelane_b32 v11, s16, 16\n" \
M_"a[16:19], a[224:227], a[196:199], a[16:19]\n" \
X_"a[186:187], v38 offset:1024\n" \
"s_mul_i32 s16, s5, 0x48\n" \
M_"a[32:35], a[160:163], a[136:139], a[32:35]\n" \
"s_addk_i32 s84, 0x2000\n" \
"s_nop 0\n" \
M_"a[32:35], a[224:227], a[200:203], a[32:35]\n" \
B_"v24, s[76:79], s84 offen offset:2048 lds\n" \
M_"a[36:39], a[164:167], a[136:139], a[36:39]\n" \
X_"a[248:249], v38 offset:8192\n" \
"s_add_i32 s17, s16, 0x47\n" \
M_"a[36:39], a[228:231], a[200:203], a[36:39]\n" \
X_"a[250:251], v38 offset:9216\n" \
M_"a[44:47], a[172:175], a[136:139], a[44:47]\n" \
X_"a[188:189], v39 offset:0\n" \
M_"a[44:47], a[236:239], a[200:203], a[44:47]\n" \
X_"a[190:191], v39 offset:1024\n" \
M_"a[40:43], a[136:139], a[168:171], a[40:43]\n" \
"s_add_u32 s89, s41, 0x3000\n" \
"buffer_store_dwordx4 v[184:187], v20, s[80:83], s89 offen offset:3072 sc1\n" \
M_"a[40:43], a[200:203], a[232:235], a[40:43]\n" \
X_"a[252:253], v39 offset:8192\n" \
M_"a[56:59], a[140:143], a[168:171], a[56:59]\n" \
X_"a[254:255], v39 offset:9216\n" \
M_"a[56:59], a[204:207], a[232:235], a[56:59]\n" \
"s_cmpk_lt_u32 s3, 3\n" \
"s_cselect_b32 s17, s17, s18\n" \
M_"a[60:63], a[172:175], a[140:143], a[60:63]\n" \
"s_mulk_i32 s17, 0x480\n" \
"s_addk_i32 s17, 0x120\n" \
M_"a[60:63], a[236:239], a[204:207], a[60:63]\n" \
"s_addk_i32 s84, 0x2000\n" \
M_"a[52:55], a[164:167], a[140:143], a[52:55]\n" \
B_"v25, s[76:79], s84 offen offset:3072 lds\n" \
M_"a[52:55], a[228:231], a[204:207], a[52:55]\n" \
"v_writelane_b32 v11, s17, 17\n" \
"s_lshl_b32 s17, s3, 5\n" \
M_"a[48:51], a[160:163], a[140:143], a[48:51]\n" \
"s_add_i32 s16, s16, s17\n" \
"s_mulk_i32 s16, 0x480\n" \
M_"a[48:51], a[224:227], a[204:207], a[48:51]\n" \
"s_mul_i32 s17, s12, 0x24000\n" \
"s_add_i32 s18, s17, 0x1b000\n" \
"s_waitcnt lgkmcnt(15)\n" \
M_"a[64:67], a[164:167], v[248:251], a[64:67]\n" \
"s_cmpk_lt_u32 s3, 3\n" \
"s_cselect_b32 s16, s16, s18\n" \
M_"a[64:67], a[228:231], v[48:51], a[64:67]\n" \
"s_xor_b32 s18, s3, 2\n" \
"v_writelane_b32 v11, s16, 18\n" \
M_"a[80:83], a[172:175], v[248:251], a[80:83]\n" \
"s_mul_i32 s16, s15, 0x2400\n" \
"s_mul_i32 s15, s15, 0x9000\n" \
M_"a[80:83], a[236:239], v[48:51], a[80:83]\n" \
"s_bitset1_b32 s16, 17\n" \
"v_writelane_b32 v11, s16, 22\n" \
M_"a[68:71], v[248:251], a[168:171], a[68:71]\n" \
"s_add_i32 s17, s17, s15\n" \
"s_mul_i32 s15, s3, 0x2400\n" \
"s_waitcnt vmcnt(11) lgkmcnt(0)\n" \
M_"a[68:71], v[48:51], a[232:235], a[68:71]\n" \
"s_barrier\n" \
"v_xor_b32_e32 v28, 0x20000, v28\n" \
"v_xor_b32_e32 v29, 0x20000, v29\n" \
M_"v[52:55], a[176:179], a[128:131], v[52:55]\n" \
"s_add_u32 m0, s40, 0x0\n" \
"s_nop 0\n" \
"s_add_u32 s85, s39, 0x8000\n" \
M_"v[52:55], a[240:243], a[192:195], v[52:55]\n" \
B_"v20, s[80:83], s85 offen sc1 lds\n" \
M_"v[56:59], a[180:183], a[128:131], v[56:59]\n" \
X_"a[160:161], v32 offset:32768\n" \
"v_writelane_b32 v11, s17, 29\n" \
M_"v[56:59], a[244:247], a[192:195], v[56:59]\n" \
X_"a[162:163], v32 offset:33280\n" \
"s_add_i32 s16, s15, 0xa400\n" \
M_"v[60:63], a[184:187], a[128:131], v[60:63]\n" \
"s_add_u32 m0, s40, 0x1000\n" \
"s_add_u32 s85, s39, 0x9000\n" \
M_"v[60:63], a[248:251], a[192:195], v[60:63]\n" \
B_"v20, s[80:83], s85 offen sc1 lds\n" \
M_"v[64:67], a[188:191], a[128:131], v[64:67]\n" \
X_"a[224:225], v32 offset:36864\n" \
"s_mov_b32 s17, 0x8000\n" \
M_"v[64:67], a[252:255], a[192:195], v[64:67]\n" \
R_"a[128:131], v28 offset:0\n" \
M_"v[80:83], a[188:191], a[132:135], v[80:83]\n" \
R_"a[192:195], v29 offset:0\n" \
M_"v[80:83], a[252:255], a[196:199], v[80:83]\n" \
"s_add_u32 s88, s41, 0x3000\n" \
"buffer_store_dwordx4 v[188:191], v20, s[80:83], s88 offen offset:2048 sc1\n" \
M_"v[76:79], a[184:187], a[132:135], v[76:79]\n" \
X_"a[226:227], v32 offset:37376\n" \
M_"v[76:79], a[248:251], a[196:199], v[76:79]\n" \
X_"a[164:165], v33 offset:32768\n" \
M_"v[72:75], a[180:183], a[132:135], v[72:75]\n" \
"s_add_u32 m0, s40, 0x3000\n" \
"s_movk_i32 s84, 0x200\n" \
M_"v[72:75], a[244:247], a[196:199], v[72:75]\n" \
B_"v21, s[76:79], s84 offen offset:0 lds\n" \
M_"v[68:71], a[176:179], a[132:135], v[68:71]\n" \
X_"a[166:167], v33 offset:33280\n" \
M_"v[68:71], a[240:243], a[196:199], v[68:71]\n" \
R_"a[132:135], v28 offset:2048\n" \
M_"v[84:87], a[176:179], a[136:139], v[84:87]\n" \
R_"a[196:199], v29 offset:2048\n" \
M_"v[84:87], a[240:243], a[200:203], v[84:87]\n" \
"s_add_u32 m0, s31, 0x0\n" \
"s_mov_b32 s84, 0x200000\n" \
M_"v[88:91], a[180:183], a[136:139], v[88:91]\n" \
B_"v26, s[68:71], s84 offen offset:0 lds\n" \
M_"v[88:91], a[244:247], a[200:203], v[88:91]\n" \
X_"a[228:229], v33 offset:36864\n" \
M_"v[92:95], a[184:187], a[136:139], v[92:95]\n" \
X_"a[230:231], v33 offset:37376\n" \
M_"v[92:95], a[248:251], a[200:203], v[92:95]\n" \
"s_addk_i32 s84, 0x7c00\n" \
M_"v[96:99], a[188:191], a[136:139], v[96:99]\n" \
B_"v26, s[68:71], s84 offen offset:1024 lds\n" \
M_"v[96:99], a[252:255], a[200:203], v[96:99]\n" \
R_"a[136:139], v28 offset:4096\n" \
M_"v[112:115], a[188:191], a[140:143], v[112:115]\n" \
R_"a[200:203], v29 offset:4096\n" \
M_"v[112:115], a[252:255], a[204:207], v[112:115]\n" \
"s_addk_i32 s84, 0x7c00\n" \
M_"v[108:111], a[184:187], a[140:143], v[108:111]\n" \
B_"v27, s[68:71], s84 offen offset:2048 lds\n" \
M_"v[108:111], a[248:251], a[204:207], v[108:111]\n" \
X_"a[172:173], v35 offset:32768\n" \
M_"v[104:107], a[180:183], a[140:143], v[104:107]\n" \
X_"a[174:175], v35 offset:33280\n" \
M_"v[104:107], a[244:247], a[204:207], v[104:107]\n" \
X_"a[236:237], v35 offset:36864\n" \
M_"v[100:103], a[176:179], a[140:143], v[100:103]\n" \
"s_cmpk_lt_u32 s3, 3\n" \
"s_cselect_b32 s16, s16, s17\n" \
M_"v[100:103], a[240:243], a[204:207], v[100:103]\n" \
R_"a[140:143], v28 offset:6144\n" \
"s_mov_b32 s17, 0x1b000\n" \
M_"v[116:119], a[176:179], a[144:147], v[116:119]\n" \
R_"a[204:207], v29 offset:6144\n" \
M_"v[116:119], a[240:243], a[208:211], v[116:119]\n" \
"s_addk_i32 s84, 0x7c00\n" \
M_"v[120:123], a[180:183], a[144:147], v[120:123]\n" \
B_"v27, s[68:71], s84 offen offset:3072 lds\n" \
M_"v[120:123], a[244:247], a[208:211], v[120:123]\n" \
"s_add_u32 m0, s30, 0x20000\n" \
"s_movk_i32 s84, 0x280\n" \
M_"v[124:127], a[184:187], a[144:147], v[124:127]\n" \
B_"v24, s[76:79], s84 offen offset:0 lds\n" \
M_"v[124:127], a[248:251], a[208:211], v[124:127]\n" \
X_"a[238:239], v35 offset:37376\n" \
M_"v[128:131], a[188:191], a[144:147], v[128:131]\n" \
"s_add_u32 s89, s42, 0x0\n" \
"buffer_store_dwordx4 v[216:219], v20, s[80:83], s89 offen offset:0 sc1\n" \
M_"v[128:131], a[252:255], a[208:211], v[128:131]\n" \
R_"a[144:147], v28 offset:8192\n" \
M_"v[144:147], a[188:191], a[148:151], v[144:147]\n" \
R_"a[208:211], v29 offset:8192\n" \
M_"v[144:147], a[252:255], a[212:215], v[144:147]\n" \
X_"a[168:169], v34 offset:32768\n" \
M_"v[140:143], a[184:187], a[148:151], v[140:143]\n" \
X_"a[170:171], v34 offset:33280\n" \
M_"v[140:143], a[248:251], a[212:215], v[140:143]\n" \
"s_movk_i32 s84, 0x2280\n" \
M_"v[136:139], a[180:183], a[148:151], v[136:139]\n" \
B_"v25, s[76:79], s84 offen offset:1024 lds\n" \
M_"v[136:139], a[244:247], a[212:215], v[136:139]\n" \
X_"a[232:233], v34 offset:36864\n" \
M_"v[132:135], a[176:179], a[148:151], v[132:135]\n" \
X_"a[234:235], v34 offset:37376\n" \
M_"v[132:135], a[240:243], a[212:215], v[132:135]\n" \
R_"a[148:151], v28 offset:10240\n" \
M_"v[148:151], a[176:179], a[152:155], v[148:151]\n" \
R_"a[212:215], v29 offset:10240\n" \
M_"v[148:151], a[240:243], a[216:219], v[148:151]\n" \
"s_movk_i32 s84, 0x4280\n" \
M_"v[152:155], a[180:183], a[152:155], v[152:155]\n" \
B_"v24, s[76:79], s84 offen offset:2048 lds\n" \
M_"v[152:155], a[244:247], a[216:219], v[152:155]\n" \
"s_movk_i32 s84, 0x6280\n" \
M_"v[156:159], a[184:187], a[152:155], v[156:159]\n" \
B_"v25, s[76:79], s84 offen offset:3072 lds\n" \
M_"v[156:159], a[248:251], a[216:219], v[156:159]\n" \
R_"v[248:251], v22 offset:32768\n" \
M_"v[160:163], a[188:191], a[152:155], v[160:163]\n" \
R_"v[48:51], v23 offset:32768\n" \
M_"v[160:163], a[252:255], a[216:219], v[160:163]\n" \
R_"a[152:155], v28 offset:12288\n" \
M_"v[176:179], a[188:191], a[156:159], v[176:179]\n" \
R_"a[216:219], v29 offset:12288\n" \
M_"v[176:179], a[252:255], a[220:223], v[176:179]\n" \
"s_cmp_lg_u32 s18, 0\n" \
"s_cselect_b32 s17, s16, s17\n" \
M_"v[172:175], a[184:187], a[156:159], v[172:175]\n" \
"v_writelane_b32 v11, s16, 23\n" \
"s_or_b32 s16, s15, 0x10000\n" \
M_"v[172:175], a[248:251], a[220:223], v[172:175]\n" \
"v_writelane_b32 v11, s17, 24\n" \
"s_add_i32 s17, s15, 0x19000\n" \
M_"v[168:171], a[180:183], a[156:159], v[168:171]\n" \
"v_writelane_b32 v11, s16, 25\n" \
"s_mov_b32 s16, 0\n" \
M_"v[168:171], a[244:247], a[220:223], v[168:171]\n" \
"s_cmpk_lt_u32 s3, 3\n" \
"s_cselect_b32 s17, s17, s16\n" \
M_"v[164:167], a[176:179], a[156:159], v[164:167]\n" \
"s_bitset1_b32 s15, 17\n" \
"v_writelane_b32 v11, s17, 26\n" \
M_"v[164:167], a[240:243], a[220:223], v[164:167]\n" \
R_"a[156:159], v28 offset:14336\n" \
R_"a[220:223], v29 offset:14336\n" \
"s_waitcnt lgkmcnt(15)\n" \
M_"a[0:3], a[160:163], a[128:131], a[0:3]\n" \
"s_add_u32 m0, s30, 0x21000\n" \
"s_addk_i32 s84, 0x3000\n" \
M_"a[0:3], a[224:227], a[192:195], a[0:3]\n" \
B_"v24, s[76:79], s84 offen offset:0 lds\n" \
M_"a[4:7], a[164:167], a[128:131], a[4:7]\n" \
X_"a[176:177], v36 offset:32768\n" \
"s_movk_i32 s17, 0x2400\n" \
M_"a[4:7], a[228:231], a[192:195], a[4:7]\n" \
X_"a[178:179], v36 offset:33792\n" \
M_"a[12:15], a[172:175], a[128:131], a[12:15]\n" \
X_"a[240:241], v36 offset:40960\n" \
"s_waitcnt lgkmcnt(15)\n" \
M_"a[12:15], a[236:239], a[192:195], a[12:15]\n" \
"s_add_u32 s88, s42, 0x1000\n" \
"buffer_store_dwordx4 v[220:223], v20, s[80:83], s88 offen offset:0 sc1\n" \
"s_waitcnt lgkmcnt(13)\n" \
M_"a[8:11], a[128:131], a[168:171], a[8:11]\n" \
X_"a[242:243], v36 offset:41984\n" \
"s_waitcnt lgkmcnt(12)\n" \
M_"a[8:11], a[192:195], a[232:235], a[8:11]\n" \
X_"a[180:181], v37 offset:32768\n" \
M_"a[24:27], a[132:135], a[168:171], a[24:27]\n" \
X_"a[182:183], v37 offset:33792\n" \
M_"a[24:27], a[196:199], a[232:235], a[24:27]\n" \
X_"a[244:245], v37 offset:40960\n" \
M_"a[28:31], a[172:175], a[132:135], a[28:31]\n" \
"s_cmpk_lt_u32 s3, 3\n" \
"s_cselect_b32 s15, s15, s17\n" \
M_"a[28:31], a[236:239], a[196:199], a[28:31]\n" \
"s_addk_i32 s84, 0x2000\n" \
M_"a[20:23], a[164:167], a[132:135], a[20:23]\n" \
B_"v25, s[76:79], s84 offen offset:1024 lds\n" \
M_"a[20:23], a[228:231], a[196:199], a[20:23]\n" \
X_"a[246:247], v37 offset:41984\n" \
"v_writelane_b32 v11, s15, 27\n" \
M_"a[16:19], a[160:163], a[132:135], a[16:19]\n" \
X_"a[184:185], v38 offset:32768\n" \
"s_lshl_b32 s15, s3, 10\n" \
M_"a[16:19], a[224:227], a[196:199], a[16:19]\n" \
X_"a[186:187], v38 offset:33792\n" \
"s_addk_i32 s15, 0x4800\n" \
M_"a[32:35], a[160:163], a[136:139], a[32:35]\n" \
"s_addk_i32 s84, 0x2000\n" \
M_"a[32:35], a[224:227], a[200:203], a[32:35]\n" \
B_"v24, s[76:79], s84 offen offset:2048 lds\n" \
M_"a[36:39], a[164:167], a[136:139], a[36:39]\n" \
X_"a[248:249], v38 offset:40960\n" \
"v_writelane_b32 v11, s15, 28\n" \
M_"a[36:39], a[228:231], a[200:203], a[36:39]\n" \
X_"a[250:251], v38 offset:41984\n" \
"s_add_i32 s15, s14, 1\n" \
M_"a[44:47], a[172:175], a[136:139], a[44:47]\n" \
X_"a[188:189], v39 offset:32768\n" \
"s_and_b32 s15, s15, 3\n" \
M_"a[44:47], a[236:239], a[200:203], a[44:47]\n" \
X_"a[190:191], v39 offset:33792\n" \
"s_or_b32 s15, s15, s4\n" \
M_"a[40:43], a[136:139], a[168:171], a[40:43]\n" \
"s_add_u32 s89, s42, 0x0\n" \
"buffer_store_dwordx4 v[192:195], v20, s[80:83], s89 offen offset:2048 sc1\n" \
M_"a[40:43], a[200:203], a[232:235], a[40:43]\n" \
X_"a[252:253], v39 offset:40960\n" \
"s_lshl_b32 s17, s15, 2\n" \
M_"a[56:59], a[140:143], a[168:171], a[56:59]\n" \
X_"a[254:255], v39 offset:41984\n" \
"s_or_b32 s17, s17, s3\n" \
M_"a[56:59], a[204:207], a[232:235], a[56:59]\n" \
"s_mul_i32 s17, s17, 0x9000\n" \
"v_writelane_b32 v11, s17, 30\n" \
M_"a[60:63], a[172:175], a[140:143], a[60:63]\n" \
"s_lshl_b32 s17, s12, 2\n" \
"s_or_b32 s17, s17, s3\n" \
M_"a[60:63], a[236:239], a[204:207], a[60:63]\n" \
"s_addk_i32 s84, 0x2000\n" \
M_"a[52:55], a[164:167], a[140:143], a[52:55]\n" \
B_"v25, s[76:79], s84 offen offset:3072 lds\n" \
M_"a[52:55], a[228:231], a[204:207], a[52:55]\n" \
"s_add_i32 s14, s14, -1\n" \
"s_xor_b32 s17, s17, 8\n" \
M_"a[48:51], a[160:163], a[140:143], a[48:51]\n" \
"s_and_b32 s14, s14, 3\n" \
"s_mul_i32 s17, s17, 0x9000\n" \
M_"a[48:51], a[224:227], a[204:207], a[48:51]\n" \
"s_or_b32 s14, s14, s4\n" \
"v_writelane_b32 v11, s17, 31\n" \
"s_waitcnt lgkmcnt(15)\n" \
M_"a[64:67], a[164:167], v[248:251], a[64:67]\n" \
"s_lshl_b32 s17, s14, 2\n" \
"s_or_b32 s17, s17, s3\n" \
M_"a[64:67], a[228:231], v[48:51], a[64:67]\n" \
"s_mul_i32 s17, s17, 0x9000\n" \
"v_writelane_b32 v11, s17, 32\n" \
M_"a[80:83], a[172:175], v[248:251], a[80:83]\n" \
"s_or_b32 s17, s8, s13\n" \
"s_mul_i32 s17, s17, 0x24800\n" \
M_"a[80:83], a[236:239], v[48:51], a[80:83]\n" \
"s_lshl_b32 s15, s15, 12\n" \
"s_add_i32 s18, s17, 0x3004800\n" \
M_"a[68:71], v[248:251], a[168:171], a[68:71]\n" \
"s_lshl_b32 s12, s12, 12\n" \
"s_add_i32 s19, s18, s15\n" \
"s_waitcnt vmcnt(11) lgkmcnt(0)\n" \
M_"a[68:71], v[48:51], a[232:235], a[68:71]\n" \
"s_barrier\n" \
M_"v[52:55], a[176:179], a[128:131], v[52:55]\n" \
"s_add_u32 m0, s40, 0x8000\n" \
"s_add_u32 s85, s39, 0xa000\n" \
M_"v[52:55], a[240:243], a[192:195], v[52:55]\n" \
B_"v20, s[80:83], s85 offen sc1 lds\n" \
M_"v[56:59], a[180:183], a[128:131], v[56:59]\n" \
X_"a[160:161], v32 offset:0\n" \
"v_accvgpr_read_b32 v224, a120\n" \
M_"v[56:59], a[244:247], a[192:195], v[56:59]\n" \
X_"a[162:163], v32 offset:512\n" \
"v_accvgpr_read_b32 v225, a121\n" \
M_"v[60:63], a[184:187], a[128:131], v[60:63]\n" \
"s_add_u32 m0, s40, 0x9000\n" \
"s_add_u32 s85, s39, 0xb000\n" \
M_"v[60:63], a[248:251], a[192:195], v[60:63]\n" \
B_"v20, s[80:83], s85 offen sc1 lds\n" \
M_"v[64:67], a[188:191], a[128:131], v[64:67]\n" \
X_"a[224:225], v32 offset:4096\n" \
"v_accvgpr_read_b32 v226, a122\n" \
M_"v[64:67], a[252:255], a[192:195], v[64:67]\n" \
R_"a[128:131], v28 offset:32768\n" \
"v_accvgpr_read_b32 v227, a123\n" \
M_"v[80:83], a[188:191], a[132:135], v[80:83]\n" \
R_"a[192:195], v29 offset:32768\n" \
"v_accvgpr_read_b32 v180, a124\n" \
M_"v[80:83], a[252:255], a[196:199], v[80:83]\n" \
"s_add_u32 s88, s42, 0x0\n" \
"buffer_store_dwordx4 v[196:199], v20, s[80:83], s88 offen offset:3072 sc1\n" \
M_"v[76:79], a[184:187], a[132:135], v[76:79]\n" \
X_"a[226:227], v32 offset:4608\n" \
"v_accvgpr_read_b32 v181, a125\n" \
M_"v[76:79], a[248:251], a[196:199], v[76:79]\n" \
X_"a[164:165], v33 offset:0\n" \
"v_accvgpr_read_b32 v182, a126\n" \
M_"v[72:75], a[180:183], a[132:135], v[72:75]\n" \
"s_add_u32 m0, s40, 0xb000\n" \
"s_movk_i32 s84, 0x280\n" \
M_"v[72:75], a[244:247], a[196:199], v[72:75]\n" \
B_"v21, s[76:79], s84 offen offset:0 lds\n" \
M_"v[68:71], a[176:179], a[132:135], v[68:71]\n" \
X_"a[166:167], v33 offset:512\n" \
"v_accvgpr_read_b32 v183, a127\n" \
M_"v[68:71], a[240:243], a[196:199], v[68:71]\n" \
R_"a[132:135], v28 offset:34816\n" \
C_"v224, v224, v225\n" \
M_"v[84:87], a[176:179], a[136:139], v[84:87]\n" \
R_"a[196:199], v29 offset:34816\n" \
C_"v225, v226, v227\n" \
M_"v[84:87], a[240:243], a[200:203], v[84:87]\n" \
"s_add_u32 m0, s31, 0x8000\n" \
"s_mov_b32 s84, 0x280000\n" \
M_"v[88:91], a[180:183], a[136:139], v[88:91]\n" \
B_"v26, s[68:71], s84 offen offset:0 lds\n" \
M_"v[88:91], a[244:247], a[200:203], v[88:91]\n" \
X_"a[228:229], v33 offset:4096\n" \
C_"v226, v180, v181\n" \
M_"v[92:95], a[184:187], a[136:139], v[92:95]\n" \
X_"a[230:231], v33 offset:4608\n" \
C_"v227, v182, v183\n" \
M_"v[92:95], a[248:251], a[200:203], v[92:95]\n" \
"s_addk_i32 s84, 0x7c00\n" \
M_"v[96:99], a[188:191], a[136:139], v[96:99]\n" \
B_"v26, s[68:71], s84 offen offset:1024 lds\n" \
M_"v[96:99], a[252:255], a[200:203], v[96:99]\n" \
R_"a[136:139], v28 offset:36864\n" \
"v_accvgpr_read_b32 v220, a112\n" \
M_"v[112:115], a[188:191], a[140:143], v[112:115]\n" \
R_"a[200:203], v29 offset:36864\n" \
"v_accvgpr_read_b32 v221, a113\n" \
M_"v[112:115], a[252:255], a[204:207], v[112:115]\n" \
"s_addk_i32 s84, 0x7c00\n" \
M_"v[108:111], a[184:187], a[140:143], v[108:111]\n" \
B_"v27, s[68:71], s84 offen offset:2048 lds\n" \
M_"v[108:111], a[248:251], a[204:207], v[108:111]\n" \
X_"a[172:173], v35 offset:0\n" \
"v_accvgpr_read_b32 v222, a114\n" \
M_"v[104:107], a[180:183], a[140:143], v[104:107]\n" \
X_"a[174:175], v35 offset:512\n" \
"v_accvgpr_read_b32 v223, a115\n" \
M_"v[104:107], a[244:247], a[204:207], v[104:107]\n" \
X_"a[236:237], v35 offset:4096\n" \
"v_accvgpr_read_b32 v180, a116\n" \
M_"v[100:103], a[176:179], a[140:143], v[100:103]\n" \
"v_accvgpr_read_b32 v181, a117\n" \
"v_accvgpr_read_b32 v182, a118\n" \
M_"v[100:103], a[240:243], a[204:207], v[100:103]\n" \
R_"a[140:143], v28 offset:38912\n" \
"v_accvgpr_read_b32 v183, a119\n" \
M_"v[116:119], a[176:179], a[144:147], v[116:119]\n" \
R_"a[204:207], v29 offset:38912\n" \
C_"v220, v220, v221\n" \
M_"v[116:119], a[240:243], a[208:211], v[116:119]\n" \
"s_addk_i32 s84, 0x7c00\n" \
M_"v[120:123], a[180:183], a[144:147], v[120:123]\n" \
B_"v27, s[68:71], s84 offen offset:3072 lds\n" \
M_"v[120:123], a[244:247], a[208:211], v[120:123]\n" \
"s_add_u32 m0, s30, 0x0\n" \
"s_movk_i32 s84, 0x300\n" \
"s_nop 0\n" \
M_"v[124:127], a[184:187], a[144:147], v[124:127]\n" \
B_"v24, s[76:79], s84 offen offset:0 lds\n" \
M_"v[124:127], a[248:251], a[208:211], v[124:127]\n" \
X_"a[238:239], v35 offset:4608\n" \
C_"v221, v222, v223\n" \
M_"v[128:131], a[188:191], a[144:147], v[128:131]\n" \
"s_add_u32 s89, s42, 0x1000\n" \
"buffer_store_dwordx4 a[84:87], v20, s[80:83], s89 offen offset:3072 sc1\n" \
M_"v[128:131], a[252:255], a[208:211], v[128:131]\n" \
R_"a[144:147], v28 offset:40960\n" \
C_"v222, v180, v181\n" \
M_"v[144:147], a[188:191], a[148:151], v[144:147]\n" \
R_"a[208:211], v29 offset:40960\n" \
C_"v223, v182, v183\n" \
M_"v[144:147], a[252:255], a[212:215], v[144:147]\n" \
X_"a[168:169], v34 offset:0\n" \
"v_accvgpr_read_b32 v216, a104\n" \
M_"v[140:143], a[184:187], a[148:151], v[140:143]\n" \
X_"a[170:171], v34 offset:512\n" \
"v_accvgpr_read_b32 v217, a105\n" \
M_"v[140:143], a[248:251], a[212:215], v[140:143]\n" \
"s_movk_i32 s84, 0x2300\n" \
"s_nop 0\n" \
M_"v[136:139], a[180:183], a[148:151], v[136:139]\n" \
B_"v25, s[76:79], s84 offen offset:1024 lds\n" \
M_"v[136:139], a[244:247], a[212:215], v[136:139]\n" \
X_"a[232:233], v34 offset:4096\n" \
"v_accvgpr_read_b32 v218, a106\n" \
M_"v[132:135], a[176:179], a[148:151], v[132:135]\n" \
X_"a[234:235], v34 offset:4608\n" \
"v_accvgpr_read_b32 v219, a107\n" \
M_"v[132:135], a[240:243], a[212:215], v[132:135]\n" \
R_"a[148:151], v28 offset:43008\n" \
"v_accvgpr_read_b32 v180, a108\n" \
M_"v[148:151], a[176:179], a[152:155], v[148:151]\n" \
R_"a[212:215], v29 offset:43008\n" \
"v_accvgpr_read_b32 v181, a109\n" \
M_"v[148:151], a[240:243], a[216:219], v[148:151]\n" \
"s_movk_i32 s84, 0x4300\n" \
M_"v[152:155], a[180:183], a[152:155], v[152:155]\n" \
B_"v24, s[76:79], s84 offen offset:2048 lds\n" \
M_"v[152:155], a[244:247], a[216:219], v[152:155]\n" \
"s_movk_i32 s84, 0x6300\n" \
M_"v[156:159], a[184:187], a[152:155], v[156:159]\n" \
B_"v25, s[76:79], s84 offen offset:3072 lds\n" \
M_"v[156:159], a[248:251], a[216:219], v[156:159]\n" \
R_"v[248:251], v22 offset:0\n" \
"v_accvgpr_read_b32 v182, a110\n" \
M_"v[160:163], a[188:191], a[152:155], v[160:163]\n" \
R_"v[48:51], v23 offset:0\n" \
"v_accvgpr_read_b32 v183, a111\n" \
M_"v[160:163], a[252:255], a[216:219], v[160:163]\n" \
R_"a[152:155], v28 offset:45056\n" \
C_"v216, v216, v217\n" \
M_"v[176:179], a[188:191], a[156:159], v[176:179]\n" \
R_"a[216:219], v29 offset:45056\n" \
C_"v217, v218, v219\n" \
M_"v[176:179], a[252:255], a[220:223], v[176:179]\n" \
C_"v218, v180, v181\n" \
C_"v219, v182, v183\n" \
M_"v[172:175], a[184:187], a[156:159], v[172:175]\n" \
"v_accvgpr_read_b32 v192, a96\n" \
"v_accvgpr_read_b32 v193, a97\n" \
M_"v[172:175], a[248:251], a[220:223], v[172:175]\n" \
"v_accvgpr_read_b32 v194, a98\n" \
"v_accvgpr_read_b32 v195, a99\n" \
M_"v[168:171], a[180:183], a[156:159], v[168:171]\n" \
"v_accvgpr_read_b32 v180, a100\n" \
"v_accvgpr_read_b32 v181, a101\n" \
M_"v[168:171], a[244:247], a[220:223], v[168:171]\n" \
"v_accvgpr_read_b32 v182, a102\n" \
"v_accvgpr_read_b32 v183, a103\n" \
M_"v[164:167], a[176:179], a[156:159], v[164:167]\n" \
C_"v192, v192, v193\n" \
C_"v193, v194, v195\n" \
M_"v[164:167], a[240:243], a[220:223], v[164:167]\n" \
R_"a[156:159], v28 offset:47104\n" \
R_"a[220:223], v29 offset:47104\n" \
"s_waitcnt lgkmcnt(15)\n" \
M_"a[0:3], a[160:163], a[128:131], a[0:3]\n" \
"s_add_u32 m0, s30, 0x1000\n" \
"s_addk_i32 s84, 0x3000\n" \
M_"a[0:3], a[224:227], a[192:195], a[0:3]\n" \
B_"v24, s[76:79], s84 offen offset:0 lds\n" \
M_"a[4:7], a[164:167], a[128:131], a[4:7]\n" \
X_"a[176:177], v36 offset:0\n" \
C_"v194, v180, v181\n" \
M_"a[4:7], a[228:231], a[192:195], a[4:7]\n" \
X_"a[178:179], v36 offset:1024\n" \
C_"v195, v182, v183\n" \
M_"a[12:15], a[172:175], a[128:131], a[12:15]\n" \
X_"a[240:241], v36 offset:8192\n" \
"v_accvgpr_read_b32 v188, a88\n" \
"s_waitcnt lgkmcnt(15)\n" \
M_"a[12:15], a[236:239], a[192:195], a[12:15]\n" \
"s_add_u32 s88, s42, 0x1000\n" \
"buffer_store_dwordx4 v[252:255], v20, s[80:83], s88 offen offset:2048 sc1\n" \
"s_waitcnt lgkmcnt(13)\n" \
M_"a[8:11], a[128:131], a[168:171], a[8:11]\n" \
X_"a[242:243], v36 offset:9216\n" \
"v_accvgpr_read_b32 v189, a89\n" \
"s_waitcnt lgkmcnt(12)\n" \
"s_nop 0\n" \
M_"a[8:11], a[192:195], a[232:235], a[8:11]\n" \
X_"a[180:181], v37 offset:0\n" \
"v_accvgpr_read_b32 v190, a90\n" \
M_"a[24:27], a[132:135], a[168:171], a[24:27]\n" \
X_"a[182:183], v37 offset:1024\n" \
"v_accvgpr_read_b32 v191, a91\n" \
M_"a[24:27], a[196:199], a[232:235], a[24:27]\n" \
X_"a[244:245], v37 offset:8192\n" \
"v_accvgpr_read_b32 v180, a92\n" \
M_"a[28:31], a[172:175], a[132:135], a[28:31]\n" \
"v_accvgpr_read_b32 v181, a93\n" \
"v_accvgpr_read_b32 v182, a94\n" \
M_"a[28:31], a[236:239], a[196:199], a[28:31]\n" \
".fill (L_pb + 28384 - .) / 4, 4, 0xBF800000\n" \
"L_pp7:\n" \
"s_cbranch_execz L_pret\n" \
"s_addk_i32 s84, 0x2000\n" \
M_"a[20:23], a[164:167], a[132:135], a[20:23]\n" \
B_"v25, s[76:79], s84 offen offset:1024 lds\n" \
M_"a[20:23], a[228:231], a[196:199], a[20:23]\n" \
X_"a[246:247], v37 offset:9216\n" \
"v_accvgpr_read_b32 v183, a95\n" \
M_"a[16:19], a[160:163], a[132:135], a[16:19]\n" \
X_"a[184:185], v38 offset:0\n" \
C_"v188, v188, v189\n" \
M_"a[16:19], a[224:227], a[196:199], a[16:19]\n" \
X_"a[186:187], v38 offset:1024\n" \
C_"v189, v190, v191\n" \
M_"a[32:35], a[160:163], a[136:139], a[32:35]\n" \
"s_addk_i32 s84, 0x2000\n" \
"s_nop 0\n" \
M_"a[32:35], a[224:227], a[200:203], a[32:35]\n" \
B_"v24, s[76:79], s84 offen offset:2048 lds\n" \
M_"a[36:39], a[164:167], a[136:139], a[36:39]\n" \
X_"a[248:249], v38 offset:8192\n" \
C_"v190, v180, v181\n" \
M_"a[36:39], a[228:231], a[200:203], a[36:39]\n" \
X_"a[250:251], v38 offset:9216\n" \
C_"v191, v182, v183\n" \
M_"a[44:47], a[172:175], a[136:139], a[44:47]\n" \
X_"a[188:189], v39 offset:0\n" \
"v_accvgpr_read_b32 v184, a72\n" \
M_"a[44:47], a[236:239], a[200:203], a[44:47]\n" \
X_"a[190:191], v39 offset:1024\n" \
"v_accvgpr_read_b32 v185, a73\n" \
M_"a[40:43], a[136:139], a[168:171], a[40:43]\n" \
"s_add_u32 s89, s42, 0x2000\n" \
"buffer_store_dwordx4 v[200:203], v20, s[80:83], s89 offen offset:2048 sc1\n" \
M_"a[40:43], a[200:203], a[232:235], a[40:43]\n" \
X_"a[252:253], v39 offset:8192\n" \
"v_accvgpr_read_b32 v186, a74\n" \
M_"a[56:59], a[140:143], a[168:171], a[56:59]\n" \
X_"a[254:255], v39 offset:9216\n" \
"v_accvgpr_read_b32 v187, a75\n" \
M_"a[56:59], a[204:207], a[232:235], a[56:59]\n" \
"v_accvgpr_read_b32 v180, a76\n" \
"v_accvgpr_read_b32 v181, a77\n" \
M_"a[60:63], a[172:175], a[140:143], a[60:63]\n" \
"v_accvgpr_read_b32 v182, a78\n" \
"v_accvgpr_read_b32 v183, a79\n" \
M_"a[60:63], a[236:239], a[204:207], a[60:63]\n" \
"s_addk_i32 s84, 0x2000\n" \
"s_nop 0\n" \
M_"a[52:55], a[164:167], a[140:143], a[52:55]\n" \
B_"v25, s[76:79], s84 offen offset:3072 lds\n" \
M_"a[52:55], a[228:231], a[204:207], a[52:55]\n" \
C_"v184, v184, v185\n" \
C_"v185, v186, v187\n" \
M_"a[48:51], a[160:163], a[140:143], a[48:51]\n" \
C_"v186, v180, v181\n" \
C_"v187, v182, v183\n" \
M_"a[48:51], a[224:227], a[204:207], a[48:51]\n" \
"v_writelane_b32 v11, s19, 33\n" \
"s_xor_b32 s12, s12, 0x2000\n" \
"s_waitcnt lgkmcnt(15)\n" \
M_"a[64:67], a[164:167], v[248:251], a[64:67]\n" \
"s_lshl_b32 s14, s14, 12\n" \
"s_sub_i32 s13, s8, s13\n" \
M_"a[64:67], a[228:231], v[48:51], a[64:67]\n" \
"s_add_i32 s19, s18, s12\n" \
"v_writelane_b32 v11, s19, 34\n" \
M_"a[80:83], a[172:175], v[248:251], a[80:83]\n" \
"s_add_i32 s18, s18, s14\n" \
"s_mul_i32 s13, s13, 0x24800\n" \
M_"a[80:83], a[236:239], v[48:51], a[80:83]\n" \
"v_writelane_b32 v11, s18, 35\n" \
"s_add_i32 s18, s13, 0x4120800\n" \
M_"a[68:71], v[248:251], a[168:171], a[68:71]\n" \
"s_add_i32 s15, s18, s15\n" \
"v_writelane_b32 v11, s15, 36\n" \
"s_waitcnt vmcnt(11) lgkmcnt(0)\n" \
M_"a[68:71], v[48:51], a[232:235], a[68:71]\n" \
"s_barrier\n" \
"v_mov_b32_e32 v45, s62\n" \
"s_mov_b64 exec, 1\n" \
"buffer_store_dword v45, off, s[80:83], s37 offset:0 sc1\n" \
"s_mov_b64 exec, -1\n" \
"v_xor_b32_e32 v28, 0x20000, v28\n" \
"v_xor_b32_e32 v29, 0x20000, v29\n" \
M_"v[52:55], a[176:179], a[128:131], v[52:55]\n" \
"s_add_u32 m0, s40, 0x0\n" \
"s_add_u32 s85, s39, 0xc000\n" \
M_"v[52:55], a[240:243], a[192:195], v[52:55]\n" \
B_"v20, s[80:83], s85 offen sc1 lds\n" \
M_"v[56:59], a[180:183], a[128:131], v[56:59]\n" \
X_"a[160:161], v32 offset:32768\n" \
"s_add_i32 s12, s18, s12\n" \
M_"v[56:59], a[244:247], a[192:195], v[56:59]\n" \
X_"a[162:163], v32 offset:33280\n" \
"v_writelane_b32 v11, s12, 37\n" \
M_"v[60:63], a[184:187], a[128:131], v[60:63]\n" \
"s_add_u32 m0, s40, 0x1000\n" \
"s_add_u32 s85, s39, 0xd000\n" \
M_"v[60:63], a[248:251], a[192:195], v[60:63]\n" \
B_"v20, s[80:83], s85 offen sc1 lds\n" \
M_"v[64:67], a[188:191], a[128:131], v[64:67]\n" \
X_"a[224:225], v32 offset:36864\n" \
"s_add_i32 s18, s18, s14\n" \
M_"v[64:67], a[252:255], a[192:195], v[64:67]\n" \
R_"a[128:131], v28 offset:0\n" \
"s_or_b32 s12, s17, 0x3000000\n" \
M_"v[80:83], a[188:191], a[132:135], v[80:83]\n" \
R_"a[192:195], v29 offset:0\n" \
"s_lshl_b32 s14, s5, 11\n" \
M_"v[80:83], a[252:255], a[196:199], v[80:83]\n" \
"s_add_u32 m0, s40, 0x3000\n" \
"s_movk_i32 s84, 0x300\n" \
M_"v[76:79], a[184:187], a[132:135], v[76:79]\n" \
B_"v21, s[76:79], s84 offen offset:0 lds\n" \
M_"v[76:79], a[248:251], a[196:199], v[76:79]\n" \
X_"a[226:227], v32 offset:37376\n" \
"v_writelane_b32 v11, s18, 38\n" \
M_"v[72:75], a[180:183], a[132:135], v[72:75]\n" \
X_"a[164:165], v33 offset:32768\n" \
"s_add_i32 s15, s12, s14\n" \
M_"v[72:75], a[244:247], a[196:199], v[72:75]\n" \
"v_writelane_b32 v11, s15, 39\n" \
"s_add_i32 s15, s13, 0x411c000\n" \
M_"v[68:71], a[176:179], a[132:135], v[68:71]\n" \
X_"a[166:167], v33 offset:33280\n" \
"s_add_i32 s18, s15, s14\n" \
M_"v[68:71], a[240:243], a[196:199], v[68:71]\n" \
R_"a[132:135], v28 offset:2048\n" \
"v_writelane_b32 v11, s18, 40\n" \
M_"v[84:87], a[176:179], a[136:139], v[84:87]\n" \
R_"a[196:199], v29 offset:2048\n" \
"s_add_i32 s17, s17, 0x3004000\n" \
M_"v[84:87], a[240:243], a[200:203], v[84:87]\n" \
"s_add_u32 m0, s31, 0x0\n" \
"s_nop 0\n" \
"s_mov_b32 s84, 0x300000\n" \
M_"v[88:91], a[180:183], a[136:139], v[88:91]\n" \
B_"v26, s[68:71], s84 offen offset:0 lds\n" \
M_"v[88:91], a[244:247], a[200:203], v[88:91]\n" \
"s_add_u32 s88, s42, 0x2000\n" \
"buffer_store_dwordx4 v[204:207], v20, s[80:83], s88 offen offset:3072 sc1\n" \
M_"v[92:95], a[184:187], a[136:139], v[92:95]\n" \
X_"a[228:229], v33 offset:36864\n" \
"v_writelane_b32 v11, s17, 41\n" \
M_"v[92:95], a[248:251], a[200:203], v[92:95]\n" \
X_"a[230:231], v33 offset:37376\n" \
"s_add_i32 s13, s13, 0x4120000\n" \
M_"v[96:99], a[188:191], a[136:139], v[96:99]\n" \
"v_writelane_b32 v11, s13, 42\n" \
"s_lshl_b32 s13, s9, 2\n" \
M_"v[96:99], a[252:255], a[200:203], v[96:99]\n" \
R_"a[136:139], v28 offset:4096\n" \
"s_or_b32 s7, s13, s7\n" \
M_"v[112:115], a[188:191], a[140:143], v[112:115]\n" \
R_"a[200:203], v29 offset:4096\n" \
"s_mul_i32 s7, s7, 0x90000\n" \
M_"v[112:115], a[252:255], a[204:207], v[112:115]\n" \
"s_addk_i32 s84, 0x7c00\n" \
M_"v[108:111], a[184:187], a[140:143], v[108:111]\n" \
B_"v26, s[68:71], s84 offen offset:1024 lds\n" \
M_"v[108:111], a[248:251], a[204:207], v[108:111]\n" \
X_"a[172:173], v35 offset:32768\n" \
"s_lshl_b32 s13, s3, 14\n" \
M_"v[104:107], a[180:183], a[140:143], v[104:107]\n" \
"s_addk_i32 s84, 0x7c00\n" \
M_"v[104:107], a[244:247], a[204:207], v[104:107]\n" \
B_"v27, s[68:71], s84 offen offset:2048 lds\n" \
M_"v[100:103], a[176:179], a[140:143], v[100:103]\n" \
X_"a[174:175], v35 offset:33280\n" \
"s_lshl_b32 s9, s9, 7\n" \
M_"v[100:103], a[240:243], a[204:207], v[100:103]\n" \
R_"a[140:143], v28 offset:6144\n" \
"s_lshl_b32 s8, s8, 4\n" \
M_"v[116:119], a[176:179], a[144:147], v[116:119]\n" \
R_"a[204:207], v29 offset:6144\n" \
"s_add_i32 s7, s7, s13\n" \
M_"v[116:119], a[240:243], a[208:211], v[116:119]\n" \
"s_addk_i32 s84, 0x7c00\n" \
M_"v[120:123], a[180:183], a[144:147], v[120:123]\n" \
B_"v27, s[68:71], s84 offen offset:3072 lds\n" \
M_"v[120:123], a[244:247], a[208:211], v[120:123]\n" \
X_"a[236:237], v35 offset:36864\n" \
"s_or_b32 s13, s8, s9\n" \
M_"v[124:127], a[184:187], a[144:147], v[124:127]\n" \
X_"a[238:239], v35 offset:37376\n" \
"s_sub_i32 s8, s8, s9\n" \
M_"v[124:127], a[248:251], a[208:211], v[124:127]\n" \
"s_add_u32 m0, s30, 0x8000\n" \
"s_movk_i32 s84, 0x380\n" \
M_"v[128:131], a[188:191], a[144:147], v[128:131]\n" \
B_"v24, s[76:79], s84 offen offset:0 lds\n" \
M_"v[128:131], a[252:255], a[208:211], v[128:131]\n" \
R_"a[144:147], v28 offset:8192\n" \
"s_mulk_i32 s13, 0x480\n" \
M_"v[144:147], a[188:191], a[148:151], v[144:147]\n" \
R_"a[208:211], v29 offset:8192\n" \
"s_mulk_i32 s8, 0x480\n" \
M_"v[144:147], a[252:255], a[212:215], v[144:147]\n" \
X_"a[168:169], v34 offset:32768\n" \
"s_or_b32 s13, s13, s11\n" \
M_"v[140:143], a[184:187], a[148:151], v[140:143]\n" \
"s_movk_i32 s84, 0x2380\n" \
M_"v[140:143], a[248:251], a[212:215], v[140:143]\n" \
B_"v25, s[76:79], s84 offen offset:1024 lds\n" \
M_"v[136:139], a[180:183], a[148:151], v[136:139]\n" \
X_"a[170:171], v34 offset:33280\n" \
"s_or_b32 s8, s8, s11\n" \
M_"v[136:139], a[244:247], a[212:215], v[136:139]\n" \
X_"a[232:233], v34 offset:36864\n" \
"s_and_b32 s11, s2, 1\n" \
M_"v[132:135], a[176:179], a[148:151], v[132:135]\n" \
X_"a[234:235], v34 offset:37376\n" \
M_"v[132:135], a[240:243], a[212:215], v[132:135]\n" \
R_"a[148:151], v28 offset:10240\n" \
M_"v[148:151], a[176:179], a[152:155], v[148:151]\n" \
R_"a[212:215], v29 offset:10240\n" \
M_"v[148:151], a[240:243], a[216:219], v[148:151]\n" \
"s_movk_i32 s84, 0x4380\n" \
M_"v[152:155], a[180:183], a[152:155], v[152:155]\n" \
B_"v24, s[76:79], s84 offen offset:2048 lds\n" \
M_"v[152:155], a[244:247], a[216:219], v[152:155]\n" \
"s_movk_i32 s84, 0x6380\n" \
M_"v[156:159], a[184:187], a[152:155], v[156:159]\n" \
B_"v25, s[76:79], s84 offen offset:3072 lds\n" \
M_"v[156:159], a[248:251], a[216:219], v[156:159]\n" \
R_"v[248:251], v22 offset:32768\n" \
M_"v[160:163], a[188:191], a[152:155], v[160:163]\n" \
R_"v[48:51], v23 offset:32768\n" \
M_"v[160:163], a[252:255], a[216:219], v[160:163]\n" \
R_"a[152:155], v28 offset:12288\n" \
M_"v[176:179], a[188:191], a[156:159], v[176:179]\n" \
R_"a[216:219], v29 offset:12288\n" \
M_"v[176:179], a[252:255], a[220:223], v[176:179]\n" \
"s_cmp_lg_u32 s11, 0\n" \
"s_cselect_b32 s12, s15, s12\n" \
M_"v[172:175], a[184:187], a[156:159], v[172:175]\n" \
"s_bfe_u32 s9, s2, 0x20001\n" \
"s_add_i32 s15, s7, 0x4240000\n" \
M_"v[172:175], a[248:251], a[220:223], v[172:175]\n" \
"s_addk_i32 s12, 0x4000\n" \
"s_lshl_b32 s5, s5, 16\n" \
M_"v[168:171], a[180:183], a[156:159], v[168:171]\n" \
"s_xor_b32 s9, s9, s3\n" \
M_"v[168:171], a[244:247], a[220:223], v[168:171]\n" \
"s_cmp_lg_u32 s9, 0\n" \
"s_cselect_b32 s12, s6, s12\n" \
M_"v[164:167], a[176:179], a[156:159], v[164:167]\n" \
"s_add_i32 s5, s15, s5\n" \
"v_writelane_b32 v11, s12, 43\n" \
M_"v[164:167], a[240:243], a[220:223], v[164:167]\n" \
R_"a[156:159], v28 offset:14336\n" \
R_"a[220:223], v29 offset:14336\n" \
"s_waitcnt lgkmcnt(15)\n" \
M_"a[0:3], a[160:163], a[128:131], a[0:3]\n" \
"s_add_u32 m0, s30, 0x9000\n" \
"s_addk_i32 s84, 0x3000\n" \
M_"a[0:3], a[224:227], a[192:195], a[0:3]\n" \
B_"v24, s[76:79], s84 offen offset:0 lds\n" \
M_"a[4:7], a[164:167], a[128:131], a[4:7]\n" \
X_"a[176:177], v36 offset:32768\n" \
"s_add_i32 s7, s7, 0x42c0000\n" \
M_"a[4:7], a[228:231], a[192:195], a[4:7]\n" \
X_"a[178:179], v36 offset:33792\n" \
"v_writelane_b32 v11, s5, 44\n" \
M_"a[12:15], a[172:175], a[128:131], a[12:15]\n" \
X_"a[240:241], v36 offset:40960\n" \
"s_lshl_b32 s5, s11, 10\n" \
"s_waitcnt lgkmcnt(15)\n" \
M_"a[12:15], a[236:239], a[192:195], a[12:15]\n" \
"s_add_u32 s89, s42, 0x3000\n" \
"buffer_store_dwordx4 v[208:211], v20, s[80:83], s89 offen offset:3072 sc1\n" \
"s_waitcnt lgkmcnt(13)\n" \
M_"a[8:11], a[128:131], a[168:171], a[8:11]\n" \
X_"a[242:243], v36 offset:41984\n" \
"s_or_b32 s5, s7, s5\n" \
"s_waitcnt lgkmcnt(12)\n" \
M_"a[8:11], a[192:195], a[232:235], a[8:11]\n" \
X_"a[180:181], v37 offset:32768\n" \
M_"a[24:27], a[132:135], a[168:171], a[24:27]\n" \
X_"a[182:183], v37 offset:33792\n" \
M_"a[24:27], a[196:199], a[232:235], a[24:27]\n" \
X_"a[244:245], v37 offset:40960\n" \
M_"a[28:31], a[172:175], a[132:135], a[28:31]\n" \
"s_cmp_lg_u32 s9, 0\n" \
"s_cselect_b32 s5, s6, s5\n" \
M_"a[28:31], a[236:239], a[196:199], a[28:31]\n" \
"s_addk_i32 s84, 0x2000\n" \
M_"a[20:23], a[164:167], a[132:135], a[20:23]\n" \
B_"v25, s[76:79], s84 offen offset:1024 lds\n" \
M_"a[20:23], a[228:231], a[196:199], a[20:23]\n" \
X_"a[246:247], v37 offset:41984\n" \
"s_add_i32 s8, s8, 0x21c000\n" \
M_"a[16:19], a[160:163], a[132:135], a[16:19]\n" \
X_"a[184:185], v38 offset:32768\n" \
"v_writelane_b32 v11, s5, 45\n" \
M_"a[16:19], a[224:227], a[196:199], a[16:19]\n" \
X_"a[186:187], v38 offset:33792\n" \
"s_add_i32 s5, s13, s10\n" \
M_"a[32:35], a[160:163], a[136:139], a[32:35]\n" \
"s_addk_i32 s84, 0x2000\n" \
M_"a[32:35], a[224:227], a[200:203], a[32:35]\n" \
B_"v24, s[76:79], s84 offen offset:2048 lds\n" \
M_"a[36:39], a[164:167], a[136:139], a[36:39]\n" \
X_"a[248:249], v38 offset:40960\n" \
"v_writelane_b32 v11, s5, 46\n" \
M_"a[36:39], a[228:231], a[200:203], a[36:39]\n" \
X_"a[250:251], v38 offset:41984\n" \
"s_add_i32 s5, s8, s10\n" \
"s_nop 0\n" \
M_"a[44:47], a[172:175], a[136:139], a[44:47]\n" \
X_"a[188:189], v39 offset:32768\n" \
"v_writelane_b32 v11, s5, 47\n" \
M_"a[44:47], a[236:239], a[200:203], a[44:47]\n" \
X_"a[190:191], v39 offset:33792\n" \
M_"a[40:43], a[136:139], a[168:171], a[40:43]\n" \
"s_add_u32 s88, s42, 0x3000\n" \
"buffer_store_dwordx4 v[212:215], v20, s[80:83], s88 offen offset:2048 sc1\n" \
M_"a[40:43], a[200:203], a[232:235], a[40:43]\n" \
X_"a[252:253], v39 offset:40960\n" \
M_"a[56:59], a[140:143], a[168:171], a[56:59]\n" \
X_"a[254:255], v39 offset:41984\n" \
M_"a[56:59], a[204:207], a[232:235], a[56:59]\n" \
"s_cmp_lg_u32 s11, 0\n" \
"s_cselect_b32 s5, s8, s13\n" \
M_"a[60:63], a[172:175], a[140:143], a[60:63]\n" \
"s_addk_i32 s5, 0x100\n" \
M_"a[60:63], a[236:239], a[204:207], a[60:63]\n" \
"s_addk_i32 s84, 0x2000\n" \
M_"a[52:55], a[164:167], a[140:143], a[52:55]\n" \
B_"v25, s[76:79], s84 offen offset:3072 lds\n" \
M_"a[52:55], a[228:231], a[204:207], a[52:55]\n" \
"s_cmp_lg_u32 s9, 0\n" \
"s_cselect_b32 s5, s6, s5\n" \
M_"a[48:51], a[160:163], a[140:143], a[48:51]\n" \
"v_writelane_b32 v11, s5, 48\n" \
"s_or_b32 s5, s7, s14\n" \
M_"a[48:51], a[224:227], a[204:207], a[48:51]\n" \
"v_writelane_b32 v11, s5, 56\n" \
"s_lshl_b32 s5, s3, 17\n" \
"s_waitcnt lgkmcnt(15)\n" \
M_"a[64:67], a[164:167], v[248:251], a[64:67]\n" \
"s_add_i32 s5, s15, s5\n" \
"s_or_b32 s4, s5, s4\n" \
M_"a[64:67], a[228:231], v[48:51], a[64:67]\n" \
"s_or_b32 s4, s4, 0x3840\n" \
"v_writelane_b32 v11, s4, 6\n" \
M_"a[80:83], a[172:175], v[248:251], a[80:83]\n" \
"s_mov_b32 s4, 0xff0000\n" \
M_"a[80:83], a[236:239], v[48:51], a[80:83]\n" \
"s_cmp_lg_u32 s9, 0\n" \
"s_cselect_b32 s4, s16, s4\n" \
M_"a[68:71], v[248:251], a[168:171], a[68:71]\n" \
"v_writelane_b32 v11, s4, 7\n" \
"s_lshl_b32 s4, s2, 16\n" \
"s_waitcnt vmcnt(10) lgkmcnt(0)\n" \
M_"a[68:71], v[48:51], a[232:235], a[68:71]\n" \
"s_barrier\n" \
"v_xor_b32_e32 v28, 0x20000, v28\n" \
"v_xor_b32_e32 v29, 0x20000, v29\n" \
M_"v[52:55], a[176:179], a[128:131], v[52:55]\n" \
"s_add_u32 m0, s40, 0x8000\n" \
"s_add_u32 s85, s39, 0xe000\n" \
M_"v[52:55], a[240:243], a[192:195], v[52:55]\n" \
B_"v20, s[80:83], s85 offen sc1 lds\n" \
M_"v[56:59], a[180:183], a[128:131], v[56:59]\n" \
X_"a[160:161], v32 offset:0\n" \
"s_add_i32 s5, s4, 0x10000\n" \
M_"v[56:59], a[244:247], a[192:195], v[56:59]\n" \
X_"a[162:163], v32 offset:512\n" \
"s_and_b32 s5, s5, 0x70000\n" \
M_"v[60:63], a[184:187], a[128:131], v[60:63]\n" \
"s_add_u32 m0, s40, 0x9000\n" \
"s_add_u32 s85, s39, 0xf000\n" \
M_"v[60:63], a[248:251], a[192:195], v[60:63]\n" \
B_"v20, s[80:83], s85 offen sc1 lds\n" \
M_"v[64:67], a[188:191], a[128:131], v[64:67]\n" \
X_"a[224:225], v32 offset:4096\n" \
"s_add_i32 s5, s15, s5\n" \
M_"v[64:67], a[252:255], a[192:195], v[64:67]\n" \
R_"a[128:131], v28 offset:0\n" \
"v_writelane_b32 v11, s5, 49\n" \
M_"v[80:83], a[188:191], a[132:135], v[80:83]\n" \
R_"a[192:195], v29 offset:0\n" \
"s_add_i32 s5, s4, 0x20000\n" \
M_"v[80:83], a[252:255], a[196:199], v[80:83]\n" \
"s_add_u32 m0, s40, 0xb000\n" \
"s_movk_i32 s84, 0x380\n" \
M_"v[76:79], a[184:187], a[132:135], v[76:79]\n" \
B_"v21, s[76:79], s84 offen offset:0 lds\n" \
M_"v[76:79], a[248:251], a[196:199], v[76:79]\n" \
X_"a[226:227], v32 offset:4608\n" \
"s_and_b32 s5, s5, 0x70000\n" \
M_"v[72:75], a[180:183], a[132:135], v[72:75]\n" \
X_"a[164:165], v33 offset:0\n" \
"s_add_i32 s5, s15, s5\n" \
M_"v[72:75], a[244:247], a[196:199], v[72:75]\n" \
"s_bitset1_b32 s5, 11\n" \
"v_writelane_b32 v11, s5, 50\n" \
M_"v[68:71], a[176:179], a[132:135], v[68:71]\n" \
X_"a[166:167], v33 offset:512\n" \
"s_add_i32 s5, s4, 0x30000\n" \
M_"v[68:71], a[240:243], a[196:199], v[68:71]\n" \
R_"a[132:135], v28 offset:2048\n" \
"s_and_b32 s5, s5, 0x70000\n" \
M_"v[84:87], a[176:179], a[136:139], v[84:87]\n" \
R_"a[196:199], v29 offset:2048\n" \
"s_add_i32 s5, s15, s5\n" \
M_"v[84:87], a[240:243], a[200:203], v[84:87]\n" \
"s_add_u32 m0, s31, 0x8000\n" \
"s_mov_b32 s84, 0x380000\n" \
M_"v[88:91], a[180:183], a[136:139], v[88:91]\n" \
B_"v26, s[68:71], s84 offen offset:0 lds\n" \
M_"v[88:91], a[244:247], a[200:203], v[88:91]\n" \
"s_add_u32 s89, s42, 0x0\n" \
"buffer_store_dwordx4 v[184:187], v20, s[80:83], s89 offen offset:1024 sc1\n" \
M_"v[92:95], a[184:187], a[136:139], v[92:95]\n" \
X_"a[228:229], v33 offset:4096\n" \
"s_bitset1_b32 s5, 12\n" \
M_"v[92:95], a[248:251], a[200:203], v[92:95]\n" \
X_"a[230:231], v33 offset:4608\n" \
"v_writelane_b32 v11, s5, 51\n" \
M_"v[96:99], a[188:191], a[136:139], v[96:99]\n" \
"s_and_b32 s5, s4, 0x70000\n" \
"s_xor_b32 s5, s5, 0x40000\n" \
M_"v[96:99], a[252:255], a[200:203], v[96:99]\n" \
R_"a[136:139], v28 offset:4096\n" \
"s_add_i32 s5, s15, s5\n" \
M_"v[112:115], a[188:191], a[140:143], v[112:115]\n" \
R_"a[200:203], v29 offset:4096\n" \
"s_or_b32 s5, s5, 0x1800\n" \
M_"v[112:115], a[252:255], a[204:207], v[112:115]\n" \
"s_addk_i32 s84, 0x7c00\n" \
M_"v[108:111], a[184:187], a[140:143], v[108:111]\n" \
B_"v26, s[68:71], s84 offen offset:1024 lds\n" \
M_"v[108:111], a[248:251], a[204:207], v[108:111]\n" \
X_"a[172:173], v35 offset:0\n" \
"v_writelane_b32 v11, s5, 52\n" \
M_"v[104:107], a[180:183], a[140:143], v[104:107]\n" \
"s_addk_i32 s84, 0x7c00\n" \
M_"v[104:107], a[244:247], a[204:207], v[104:107]\n" \
B_"v27, s[68:71], s84 offen offset:2048 lds\n" \
M_"v[100:103], a[176:179], a[140:143], v[100:103]\n" \
X_"a[174:175], v35 offset:512\n" \
"s_add_i32 s5, s4, 0x50000\n" \
M_"v[100:103], a[240:243], a[204:207], v[100:103]\n" \
R_"a[140:143], v28 offset:6144\n" \
"s_and_b32 s5, s5, 0x70000\n" \
M_"v[116:119], a[176:179], a[144:147], v[116:119]\n" \
R_"a[204:207], v29 offset:6144\n" \
"s_add_i32 s5, s15, s5\n" \
M_"v[116:119], a[240:243], a[208:211], v[116:119]\n" \
"s_addk_i32 s84, 0x7c00\n" \
M_"v[120:123], a[180:183], a[144:147], v[120:123]\n" \
B_"v27, s[68:71], s84 offen offset:3072 lds\n" \
M_"v[120:123], a[244:247], a[208:211], v[120:123]\n" \
X_"a[236:237], v35 offset:4096\n" \
"s_bitset1_b32 s5, 13\n" \
M_"v[124:127], a[184:187], a[144:147], v[124:127]\n" \
X_"a[238:239], v35 offset:4608\n" \
"v_writelane_b32 v11, s5, 53\n" \
M_"v[124:127], a[248:251], a[208:211], v[124:127]\n" \
"s_add_u32 m0, s30, 0x20000\n" \
"s_movk_i32 s84, 0x400\n" \
M_"v[128:131], a[188:191], a[144:147], v[128:131]\n" \
B_"v24, s[76:79], s84 offen offset:0 lds\n" \
M_"v[128:131], a[252:255], a[208:211], v[128:131]\n" \
R_"a[144:147], v28 offset:8192\n" \
"s_add_i32 s5, s4, 0x60000\n" \
M_"v[144:147], a[188:191], a[148:151], v[144:147]\n" \
R_"a[208:211], v29 offset:8192\n" \
"s_and_b32 s5, s5, 0x70000\n" \
M_"v[144:147], a[252:255], a[212:215], v[144:147]\n" \
X_"a[168:169], v34 offset:0\n" \
"s_add_i32 s4, s4, 0x70000\n" \
M_"v[140:143], a[184:187], a[148:151], v[140:143]\n" \
"s_movk_i32 s84, 0x2400\n" \
M_"v[140:143], a[248:251], a[212:215], v[140:143]\n" \
B_"v25, s[76:79], s84 offen offset:1024 lds\n" \
M_"v[136:139], a[180:183], a[148:151], v[136:139]\n" \
X_"a[170:171], v34 offset:512\n" \
"s_add_i32 s5, s15, s5\n" \
M_"v[136:139], a[244:247], a[212:215], v[136:139]\n" \
X_"a[232:233], v34 offset:4096\n" \
"s_and_b32 s4, s4, 0x70000\n" \
M_"v[132:135], a[176:179], a[148:151], v[132:135]\n" \
X_"a[234:235], v34 offset:4608\n" \
"s_or_b32 s5, s5, 0x2800\n" \
M_"v[132:135], a[240:243], a[212:215], v[132:135]\n" \
R_"a[148:151], v28 offset:10240\n" \
"v_writelane_b32 v11, s5, 54\n" \
M_"v[148:151], a[176:179], a[152:155], v[148:151]\n" \
R_"a[212:215], v29 offset:10240\n" \
"s_add_i32 s15, s15, s4\n" \
M_"v[148:151], a[240:243], a[216:219], v[148:151]\n" \
"s_movk_i32 s84, 0x4400\n" \
M_"v[152:155], a[180:183], a[152:155], v[152:155]\n" \
B_"v24, s[76:79], s84 offen offset:2048 lds\n" \
M_"v[152:155], a[244:247], a[216:219], v[152:155]\n" \
"s_movk_i32 s84, 0x6400\n" \
M_"v[156:159], a[184:187], a[152:155], v[156:159]\n" \
B_"v25, s[76:79], s84 offen offset:3072 lds\n" \
M_"v[156:159], a[248:251], a[216:219], v[156:159]\n" \
R_"v[248:251], v22 offset:0\n" \
"s_or_b32 s4, s15, 0x3000\n" \
M_"v[160:163], a[188:191], a[152:155], v[160:163]\n" \
R_"v[48:51], v23 offset:0\n" \
"v_writelane_b32 v11, s4, 55\n" \
M_"v[160:163], a[252:255], a[216:219], v[160:163]\n" \
R_"a[152:155], v28 offset:12288\n" \
"s_movk_i32 s11, 0x4000\n" \
M_"v[176:179], a[188:191], a[156:159], v[176:179]\n" \
R_"a[216:219], v29 offset:12288\n" \
"s_lshl_b32 s4, s2, 2\n" \
M_"v[176:179], a[252:255], a[220:223], v[176:179]\n" \
"s_and_b32 s4, s4, 28\n" \
"s_bfe_u32 s5, s2, 0x20003\n" \
M_"v[172:175], a[184:187], a[156:159], v[172:175]\n" \
"s_or_b32 s4, s4, s5\n" \
"s_lshr_b32 s5, s2, 5\n" \
M_"v[172:175], a[248:251], a[220:223], v[172:175]\n" \
"s_lshr_b32 s19, s3, 1\n" \
"s_lshl_b32 s18, s5, 1\n" \
M_"v[168:171], a[180:183], a[156:159], v[168:171]\n" \
"s_lshl_b32 s6, s4, 20\n" \
"s_lshl_b32 s7, s4, 8\n" \
M_"v[168:171], a[244:247], a[220:223], v[168:171]\n" \
"s_and_b32 s13, s3, 1\n" \
"s_add_i32 s14, s18, s19\n" \
M_"v[164:167], a[176:179], a[156:159], v[164:167]\n" \
"s_add_u32 s6, s26, s6\n" \
"s_addc_u32 s9, s27, 0\n" \
M_"v[164:167], a[240:243], a[220:223], v[164:167]\n" \
R_"a[156:159], v28 offset:14336\n" \
R_"a[220:223], v29 offset:14336\n" \
"s_waitcnt lgkmcnt(15)\n" \
M_"a[0:3], a[160:163], a[128:131], a[0:3]\n" \
"s_add_u32 m0, s30, 0x21000\n" \
"s_addk_i32 s84, 0x3000\n" \
M_"a[0:3], a[224:227], a[192:195], a[0:3]\n" \
B_"v24, s[76:79], s84 offen offset:0 lds\n" \
M_"a[4:7], a[164:167], a[128:131], a[4:7]\n" \
X_"a[176:177], v36 offset:0\n" \
M_"a[4:7], a[228:231], a[192:195], a[4:7]\n" \
X_"a[178:179], v36 offset:1024\n" \
M_"a[12:15], a[172:175], a[128:131], a[12:15]\n" \
X_"a[240:241], v36 offset:8192\n" \
"s_waitcnt lgkmcnt(15)\n" \
M_"a[12:15], a[236:239], a[192:195], a[12:15]\n" \
"s_add_u32 s88, s42, 0x1000\n" \
"buffer_store_dwordx4 v[188:191], v20, s[80:83], s88 offen offset:1024 sc1\n" \
"s_waitcnt lgkmcnt(13)\n" \
M_"a[8:11], a[128:131], a[168:171], a[8:11]\n" \
X_"a[242:243], v36 offset:9216\n" \
"s_waitcnt lgkmcnt(12)\n" \
M_"a[8:11], a[192:195], a[232:235], a[8:11]\n" \
X_"a[180:181], v37 offset:0\n" \
M_"a[24:27], a[132:135], a[168:171], a[24:27]\n" \
X_"a[182:183], v37 offset:1024\n" \
M_"a[24:27], a[196:199], a[232:235], a[24:27]\n" \
X_"a[244:245], v37 offset:8192\n" \
M_"a[28:31], a[172:175], a[132:135], a[28:31]\n" \
"s_add_u32 s8, s6, 0x1000000\n" \
"s_addc_u32 s6, s9, 0\n" \
M_"a[28:31], a[236:239], a[196:199], a[28:31]\n" \
"s_addk_i32 s84, 0x2000\n" \
M_"a[20:23], a[164:167], a[132:135], a[20:23]\n" \
B_"v25, s[76:79], s84 offen offset:1024 lds\n" \
M_"a[20:23], a[228:231], a[196:199], a[20:23]\n" \
X_"a[246:247], v37 offset:9216\n" \
"s_and_b32 s9, s6, 0xffff\n" \
M_"a[16:19], a[160:163], a[132:135], a[16:19]\n" \
X_"a[184:185], v38 offset:0\n" \
"s_lshl_b32 s6, s5, 16\n" \
M_"a[16:19], a[224:227], a[196:199], a[16:19]\n" \
".fill (L_pb + 32480 - .) / 4, 4, 0xBF800000\n" \
"L_pp8:\n" \
"s_cbranch_execz L_pret\n" \
X_"a[186:187], v38 offset:1024\n" \
"s_sub_i32 s10, 0x100000, s6\n" \
M_"a[32:35], a[160:163], a[136:139], a[32:35]\n" \
"s_addk_i32 s84, 0x2000\n" \
M_"a[32:35], a[224:227], a[200:203], a[32:35]\n" \
B_"v24, s[76:79], s84 offen offset:2048 lds\n" \
M_"a[36:39], a[164:167], a[136:139], a[36:39]\n" \
X_"a[248:249], v38 offset:8192\n" \
"s_mov_b32 s6, 0x8000\n" \
M_"a[36:39], a[228:231], a[200:203], a[36:39]\n" \
X_"a[250:251], v38 offset:9216\n" \
M_"a[44:47], a[172:175], a[136:139], a[44:47]\n" \
X_"a[188:189], v39 offset:0\n" \
M_"a[44:47], a[236:239], a[200:203], a[44:47]\n" \
X_"a[190:191], v39 offset:1024\n" \
M_"a[40:43], a[136:139], a[168:171], a[40:43]\n" \
"s_add_u32 s89, s42, 0x2000\n" \
"buffer_store_dwordx4 v[192:195], v20, s[80:83], s89 offen offset:0 sc1\n" \
M_"a[40:43], a[200:203], a[232:235], a[40:43]\n" \
X_"a[252:253], v39 offset:8192\n" \
M_"a[56:59], a[140:143], a[168:171], a[56:59]\n" \
X_"a[254:255], v39 offset:9216\n" \
M_"a[56:59], a[204:207], a[232:235], a[56:59]\n" \
"s_cmp_lg_u32 s5, 0\n" \
"s_cselect_b32 s17, s11, s6\n" \
M_"a[60:63], a[172:175], a[140:143], a[60:63]\n" \
"s_lshl_b32 s6, s3, 1\n" \
"s_lshl_b32 s5, s5, 3\n" \
M_"a[60:63], a[236:239], a[204:207], a[60:63]\n" \
"s_addk_i32 s84, 0x2000\n" \
M_"a[52:55], a[164:167], a[140:143], a[52:55]\n" \
B_"v25, s[76:79], s84 offen offset:3072 lds\n" \
M_"a[52:55], a[228:231], a[204:207], a[52:55]\n" \
"s_and_b32 s6, s6, 4\n" \
"s_or_b32 s12, s5, s6\n" \
M_"a[48:51], a[160:163], a[140:143], a[48:51]\n" \
"s_sub_i32 s5, s13, s5\n" \
"s_or_b32 s5, s5, s6\n" \
M_"a[48:51], a[224:227], a[204:207], a[48:51]\n" \
"s_or_b32 s6, s12, s13\n" \
"s_lshl_b32 s4, s4, 2\n" \
"s_waitcnt lgkmcnt(15)\n" \
M_"a[64:67], a[164:167], v[248:251], a[64:67]\n" \
"s_mulk_i32 s6, 0x92\n" \
"s_add_i32 s4, s4, 18\n" \
M_"a[64:67], a[228:231], v[48:51], a[64:67]\n" \
"s_mulk_i32 s5, 0x92\n" \
"s_add_i32 s6, s4, s6\n" \
M_"a[80:83], a[172:175], v[248:251], a[80:83]\n" \
"s_add_i32 s5, s5, s4\n" \
"s_lshl_b32 s4, s3, 11\n" \
M_"a[80:83], a[236:239], v[48:51], a[80:83]\n" \
"s_lshl_b32 s15, s13, 12\n" \
"s_lshl_b32 s16, s19, 14\n" \
M_"a[68:71], v[248:251], a[168:171], a[68:71]\n" \
"s_mul_i32 s19, s19, 0xe000\n" \
"s_mov_b32 s11, 0x20000\n" \
"s_waitcnt vmcnt(10) lgkmcnt(0)\n" \
M_"a[68:71], v[48:51], a[232:235], a[68:71]\n" \
"s_barrier\n" \
M_"v[52:55], a[176:179], a[128:131], v[52:55]\n" \
"s_add_u32 m0, s40, 0x0\n" \
"s_add_u32 s85, s39, 0x10000\n" \
M_"v[52:55], a[240:243], a[192:195], v[52:55]\n" \
B_"v20, s[80:83], s85 offen sc1 lds\n" \
M_"v[56:59], a[180:183], a[128:131], v[56:59]\n" \
X_"a[160:161], v32 offset:32768\n" \
"s_or_b32 s12, s4, 0xc000\n" \
M_"v[56:59], a[244:247], a[192:195], v[56:59]\n" \
X_"a[162:163], v32 offset:33280\n" \
"s_lshl_b32 s4, s3, 8\n" \
M_"v[60:63], a[184:187], a[128:131], v[60:63]\n" \
"s_or_b32 s22, s15, s16\n" \
"s_or_b32 s23, s19, s15\n" \
M_"v[60:63], a[248:251], a[192:195], v[60:63]\n" \
X_"a[224:225], v32 offset:36864\n" \
"s_lshl_b32 s60, s6, 10\n" \
M_"v[64:67], a[188:191], a[128:131], v[64:67]\n" \
"s_lshl_b32 s61, s5, 10\n" \
"s_lshl_b32 s14, s14, 15\n" \
"s_nop 0\n" \
M_"v[64:67], a[252:255], a[192:195], v[64:67]\n" \
R_"a[128:131], v28 offset:32768\n" \
"s_or_b32 s20, s4, 0x26c00\n" \
M_"v[80:83], a[188:191], a[132:135], v[80:83]\n" \
R_"a[192:195], v29 offset:32768\n" \
"s_or_b32 s13, s22, 0x8000\n" \
M_"v[80:83], a[252:255], a[196:199], v[80:83]\n" \
"s_add_u32 m0, s40, 0x1000\n" \
"s_add_u32 s85, s39, 0x11000\n" \
M_"v[76:79], a[184:187], a[132:135], v[76:79]\n" \
B_"v20, s[80:83], s85 offen sc1 lds\n" \
M_"v[76:79], a[248:251], a[196:199], v[76:79]\n" \
X_"a[226:227], v32 offset:37376\n" \
"s_add_i32 s22, s22, 0x14000\n" \
M_"v[72:75], a[180:183], a[132:135], v[72:75]\n" \
X_"a[164:165], v33 offset:32768\n" \
"s_bitset1_b32 s23, 16\n" \
M_"v[72:75], a[244:247], a[196:199], v[72:75]\n" \
"s_or_b32 s24, s7, 0x5be8020\n" \
"s_and_b32 s57, s27, 0xffff\n" \
M_"v[68:71], a[176:179], a[132:135], v[68:71]\n" \
X_"a[166:167], v33 offset:33280\n" \
"s_mov_b32 s58, 0x6000000\n" \
M_"v[68:71], a[240:243], a[196:199], v[68:71]\n" \
R_"a[132:135], v28 offset:34816\n" \
"s_mov_b32 s56, s26\n" \
M_"v[84:87], a[176:179], a[136:139], v[84:87]\n" \
R_"a[196:199], v29 offset:34816\n" \
"s_mov_b32 s59, s11\n" \
M_"v[84:87], a[240:243], a[200:203], v[84:87]\n" \
"s_add_u32 m0, s40, 0x3000\n" \
"s_movk_i32 s84, 0x400\n" \
M_"v[88:91], a[180:183], a[136:139], v[88:91]\n" \
B_"v21, s[76:79], s84 offen offset:0 lds\n" \
M_"v[88:91], a[244:247], a[200:203], v[88:91]\n" \
X_"a[228:229], v33 offset:36864\n" \
"s_add_i32 s60, s60, 0x3000000\n" \
M_"v[92:95], a[184:187], a[136:139], v[92:95]\n" \
X_"a[230:231], v33 offset:37376\n" \
"s_add_i32 s61, s61, 0x411c000\n" \
M_"v[92:95], a[248:251], a[200:203], v[92:95]\n" \
"s_add_u32 s88, s42, 0x2000\n" \
"buffer_store_dwordx4 v[216:219], v20, s[80:83], s88 offen offset:1024 sc1\n" \
M_"v[96:99], a[188:191], a[136:139], v[96:99]\n" \
"s_sub_i32 s19, 14, s18\n" \
"s_nop 0\n" \
"s_mov_b32 s21, 0x71800000\n" \
M_"v[96:99], a[252:255], a[200:203], v[96:99]\n" \
R_"a[136:139], v28 offset:36864\n" \
"s_mov_b32 s4, L_gsw-L_gtr\n" \
M_"v[112:115], a[188:191], a[140:143], v[112:115]\n" \
R_"a[200:203], v29 offset:36864\n" \
M_"v[112:115], a[252:255], a[204:207], v[112:115]\n" \
X_"a[172:173], v35 offset:32768\n" \
M_"v[108:111], a[184:187], a[140:143], v[108:111]\n" \
X_"a[174:175], v35 offset:33280\n" \
M_"v[108:111], a[248:251], a[204:207], v[108:111]\n" \
"s_cmp_eq_u32 s19, 0\n" \
"s_cselect_b32 s4, L_gs7-L_gtr, s4\n" \
M_"v[104:107], a[180:183], a[140:143], v[104:107]\n" \
"s_add_u32 m0, s31, 0x0\n" \
"s_mov_b32 s84, 0x400000\n" \
M_"v[104:107], a[244:247], a[204:207], v[104:107]\n" \
B_"v26, s[68:71], s84 offen offset:0 lds\n" \
M_"v[100:103], a[176:179], a[140:143], v[100:103]\n" \
X_"a[236:237], v35 offset:36864\n" \
M_"v[100:103], a[240:243], a[204:207], v[100:103]\n" \
R_"a[140:143], v28 offset:38912\n" \
M_"v[116:119], a[176:179], a[144:147], v[116:119]\n" \
R_"a[204:207], v29 offset:38912\n" \
M_"v[116:119], a[240:243], a[208:211], v[116:119]\n" \
X_"a[238:239], v35 offset:37376\n" \
M_"v[120:123], a[180:183], a[144:147], v[120:123]\n" \
X_"a[168:169], v34 offset:32768\n" \
M_"v[120:123], a[244:247], a[208:211], v[120:123]\n" \
"s_addk_i32 s84, 0x7c00\n" \
M_"v[124:127], a[184:187], a[144:147], v[124:127]\n" \
B_"v26, s[68:71], s84 offen offset:1024 lds\n" \
M_"v[124:127], a[248:251], a[208:211], v[124:127]\n" \
X_"a[170:171], v34 offset:33280\n" \
M_"v[128:131], a[188:191], a[144:147], v[128:131]\n" \
"s_cmp_eq_u32 s16, 0\n" \
"s_cselect_b32 s4, 0, s4\n" \
M_"v[128:131], a[252:255], a[208:211], v[128:131]\n" \
R_"a[144:147], v28 offset:40960\n" \
M_"v[144:147], a[188:191], a[148:151], v[144:147]\n" \
R_"a[208:211], v29 offset:40960\n" \
M_"v[144:147], a[252:255], a[212:215], v[144:147]\n" \
X_"a[232:233], v34 offset:36864\n" \
M_"v[140:143], a[184:187], a[148:151], v[140:143]\n" \
"s_addk_i32 s84, 0x7c00\n" \
M_"v[140:143], a[248:251], a[212:215], v[140:143]\n" \
B_"v27, s[68:71], s84 offen offset:2048 lds\n" \
M_"v[136:139], a[180:183], a[148:151], v[136:139]\n" \
X_"a[234:235], v34 offset:37376\n" \
M_"v[136:139], a[244:247], a[212:215], v[136:139]\n" \
"s_getpc_b64 s[28:29]\n" \
"L_gp0_%=:\n" \
"s_add_u32 s28, s28, L_gtr-L_gp0_%=\n" \
"s_addc_u32 s29, s29, 0\n" \
M_"v[132:135], a[176:179], a[148:151], v[132:135]\n" \
R_"v[248:251], v22 offset:32768\n" \
M_"v[132:135], a[240:243], a[212:215], v[132:135]\n" \
R_"a[148:151], v28 offset:43008\n" \
M_"v[148:151], a[176:179], a[152:155], v[148:151]\n" \
R_"a[212:215], v29 offset:43008\n" \
M_"v[148:151], a[240:243], a[216:219], v[148:151]\n" \
"s_add_u32 m0, s31, 0x0\n" \
"s_mov_b32 s84, 0x417400\n" \
M_"v[152:155], a[180:183], a[152:155], v[152:155]\n" \
B_"v27, s[68:71], s84 offen offset:3072 lds\n" \
M_"v[152:155], a[244:247], a[216:219], v[152:155]\n" \
R_"v[48:51], v23 offset:32768\n" \
M_"v[156:159], a[184:187], a[152:155], v[156:159]\n" \
"s_add_u32 s28, s28, s4\n" \
"s_addc_u32 s29, s29, 0\n" \
"s_nop 0\n" \
M_"v[156:159], a[248:251], a[216:219], v[156:159]\n" \
"v_accvgpr_write_b32 a96, 0\n" \
"v_accvgpr_write_b32 a97, 0\n" \
M_"v[160:163], a[188:191], a[152:155], v[160:163]\n" \
"v_accvgpr_write_b32 a98, 0\n" \
"v_accvgpr_write_b32 a99, 0\n" \
M_"v[160:163], a[252:255], a[216:219], v[160:163]\n" \
R_"a[152:155], v28 offset:45056\n" \
"v_accvgpr_write_b32 a100, 0\n" \
M_"v[176:179], a[188:191], a[156:159], v[176:179]\n" \
R_"a[216:219], v29 offset:45056\n" \
"v_accvgpr_write_b32 a101, 0\n" \
M_"v[176:179], a[252:255], a[220:223], v[176:179]\n" \
"v_accvgpr_write_b32 a102, 0\n" \
"v_accvgpr_write_b32 a103, 0\n" \
M_"v[172:175], a[184:187], a[156:159], v[172:175]\n" \
"v_accvgpr_write_b32 a112, 0\n" \
"v_accvgpr_write_b32 a113, 0\n" \
M_"v[172:175], a[248:251], a[220:223], v[172:175]\n" \
"v_accvgpr_write_b32 a114, 0\n" \
"v_accvgpr_write_b32 a115, 0\n" \
M_"v[168:171], a[180:183], a[156:159], v[168:171]\n" \
"v_accvgpr_write_b32 a120, 0\n" \
"v_accvgpr_write_b32 a121, 0\n" \
M_"v[168:171], a[244:247], a[220:223], v[168:171]\n" \
"v_accvgpr_write_b32 a122, 0\n" \
"v_accvgpr_write_b32 a123, 0\n" \
M_"v[164:167], a[176:179], a[156:159], v[164:167]\n" \
"v_mov_b32_e32 v180, 0x3f803f80\n" \
"s_mov_b32 s6, 0x33333333\n" \
M_"v[164:167], a[240:243], a[220:223], v[164:167]\n" \
R_"a[156:159], v28 offset:47104\n" \
R_"a[220:223], v29 offset:47104\n" \
"s_waitcnt lgkmcnt(15)\n" \
"s_nop 0\n" \
M_"a[0:3], a[160:163], a[128:131], a[0:3]\n" \
"s_add_u32 s89, s42, 0x3000\n" \
"buffer_store_dwordx4 v[220:223], v20, s[80:83], s89 offen offset:0 sc1\n" \
M_"a[0:3], a[224:227], a[192:195], a[0:3]\n" \
X_"a[176:177], v36 offset:32768\n" \
"s_mov_b32 s7, 0xcccccccc\n" \
M_"a[4:7], a[164:167], a[128:131], a[4:7]\n" \
X_"a[178:179], v36 offset:33792\n" \
"v_cndmask_b32_e64 v181, 0, v180, s[6:7]\n" \
M_"a[4:7], a[228:231], a[192:195], a[4:7]\n" \
X_"a[240:241], v36 offset:40960\n" \
"v_accvgpr_write_b32 a104, v181\n" \
M_"a[12:15], a[172:175], a[128:131], a[12:15]\n" \
X_"a[242:243], v36 offset:41984\n" \
"v_accvgpr_write_b32 a105, v181\n" \
"s_waitcnt lgkmcnt(15)\n" \
M_"a[12:15], a[236:239], a[192:195], a[12:15]\n" \
"v_accvgpr_write_b32 a106, v181\n" \
"v_accvgpr_write_b32 a107, v181\n" \
M_"a[8:11], a[128:131], a[168:171], a[8:11]\n" \
X_"a[180:181], v37 offset:32768\n" \
"s_mov_b32 s6, 0x11111111\n" \
"s_waitcnt lgkmcnt(13)\n" \
M_"a[8:11], a[192:195], a[232:235], a[8:11]\n" \
"s_mov_b32 s7, 0x11111111\n" \
"v_cndmask_b32_e64 v181, 0, v180, s[6:7]\n" \
M_"a[24:27], a[132:135], a[168:171], a[24:27]\n" \
X_"a[182:183], v37 offset:33792\n" \
"v_accvgpr_write_b32 a108, v181\n" \
M_"a[24:27], a[196:199], a[232:235], a[24:27]\n" \
X_"a[244:245], v37 offset:40960\n" \
"v_accvgpr_write_b32 a109, v181\n" \
M_"a[28:31], a[172:175], a[132:135], a[28:31]\n" \
"v_accvgpr_write_b32 a110, v181\n" \
"v_accvgpr_write_b32 a111, v181\n" \
M_"a[28:31], a[236:239], a[196:199], a[28:31]\n" \
X_"a[246:247], v37 offset:41984\n" \
"s_mov_b32 s6, 0x44444444\n" \
M_"a[20:23], a[164:167], a[132:135], a[20:23]\n" \
"s_mov_b32 s7, 0x44444444\n" \
"v_cndmask_b32_e64 v181, 0, v180, s[6:7]\n" \
M_"a[20:23], a[228:231], a[196:199], a[20:23]\n" \
X_"a[184:185], v38 offset:32768\n" \
"v_accvgpr_write_b32 a116, v181\n" \
M_"a[16:19], a[160:163], a[132:135], a[16:19]\n" \
X_"a[186:187], v38 offset:33792\n" \
"v_accvgpr_write_b32 a117, v181\n" \
M_"a[16:19], a[224:227], a[196:199], a[16:19]\n" \
"v_accvgpr_write_b32 a118, v181\n" \
"v_accvgpr_write_b32 a119, v181\n" \
M_"a[32:35], a[160:163], a[136:139], a[32:35]\n" \
"s_add_u32 s88, s42, 0x3000\n" \
"buffer_store_dwordx4 v[224:227], v20, s[80:83], s88 offen offset:1024 sc1\n" \
M_"a[32:35], a[224:227], a[200:203], a[32:35]\n" \
X_"a[248:249], v38 offset:40960\n" \
"v_bfe_i32 v181, v20, 4, 1\n" \
M_"a[36:39], a[164:167], a[136:139], a[36:39]\n" \
"v_lshlrev_b32_e32 v45, 3, v20\n" \
"v_add_u32_e32 v45, s30, v45\n" \
M_"a[36:39], a[228:231], a[200:203], a[36:39]\n" \
"s_getpc_b64 s[86:87]\n" \
"L_ct0_%=:\n" \
"s_add_u32 s86, s86, L_ap0-L_ct0_%=\n" \
"s_addc_u32 s87, s87, 0\n" \
"s_add_u32 m0, s40, 0x2000\n" \
M_"a[44:47], a[172:175], a[136:139], a[44:47]\n" \
"global_load_lds_dword v45, s[86:87]\n" \
M_"a[44:47], a[236:239], a[200:203], a[44:47]\n" \
X_"a[250:251], v38 offset:41984\n" \
"v_and_b32_e32 v181, 0xaaaaaaaa, v181\n" \
M_"a[40:43], a[136:139], a[168:171], a[40:43]\n" \
X_"a[188:189], v39 offset:32768\n" \
"v_or_b32_e32 v181, 0x44444444, v181\n" \
M_"a[40:43], a[200:203], a[232:235], a[40:43]\n" \
X_"a[190:191], v39 offset:33792\n" \
"v_accvgpr_write_b32 a126, v181\n" \
M_"a[56:59], a[140:143], a[168:171], a[56:59]\n" \
X_"a[252:253], v39 offset:40960\n" \
"v_bfe_u32 v180, v20, 5, 3\n" \
M_"a[56:59], a[204:207], a[232:235], a[56:59]\n" \
X_"a[254:255], v39 offset:41984\n" \
"v_bfe_u32 v181, v20, 8, 2\n" \
M_"a[60:63], a[172:175], a[140:143], a[60:63]\n" \
"v_lshlrev_b32_e32 v181, 1, v181\n" \
"v_sub_u32_e32 v180, v180, v181\n" \
M_"a[60:63], a[236:239], a[204:207], a[60:63]\n" \
"v_and_b32_e64 v181, 16, v20\n" \
"v_mov_b32_e32 v182, 0x3f80\n" \
M_"a[52:55], a[164:167], a[140:143], a[52:55]\n" \
"v_lshlrev_b32_e32 v181, v181, v182\n" \
"v_cmp_eq_u32_e32 vcc, 0, v180\n" \
M_"a[52:55], a[228:231], a[204:207], a[52:55]\n" \
"v_cmp_eq_u32_e64 s[6:7], 1, v180\n" \
"v_cndmask_b32_e64 v182, 0, v181, vcc\n" \
M_"a[48:51], a[160:163], a[140:143], a[48:51]\n" \
"v_cndmask_b32_e64 v183, 0, v181, s[6:7]\n" \
"v_accvgpr_write_b32 a124, v182\n" \
M_"a[48:51], a[224:227], a[204:207], a[48:51]\n" \
"v_accvgpr_write_b32 a125, v183\n" \
"s_waitcnt lgkmcnt(15)\n" \
M_"a[64:67], a[164:167], v[248:251], a[64:67]\n" \
M_"a[64:67], a[228:231], v[48:51], a[64:67]\n" \
M_"a[80:83], a[172:175], v[248:251], a[80:83]\n" \
M_"a[80:83], a[236:239], v[48:51], a[80:83]\n" \
M_"a[68:71], v[248:251], a[168:171], a[68:71]\n" \
"s_waitcnt vmcnt(3) lgkmcnt(0)\n" \
M_"a[68:71], v[48:51], a[232:235], a[68:71]\n" \
"s_barrier\n" \
"v_xor_b32_e32 v28, 0x20000, v28\n" \
"v_xor_b32_e32 v29, 0x20000, v29\n" \
M_"v[52:55], a[176:179], a[128:131], v[52:55]\n" \
X_"a[160:161], v32 offset:0\n" \
"v_accvgpr_read_b32 v184, a0\n" \
M_"v[52:55], a[240:243], a[192:195], v[52:55]\n" \
"v_accvgpr_read_b32 v185, a1\n" \
"v_accvgpr_read_b32 v186, a2\n" \
M_"v[56:59], a[180:183], a[128:131], v[56:59]\n" \
X_"a[162:163], v32 offset:512\n" \
"v_accvgpr_read_b32 v187, a3\n" \
M_"v[56:59], a[244:247], a[192:195], v[56:59]\n" \
"v_accvgpr_read_b32 v188, a4\n" \
"v_accvgpr_read_b32 v189, a5\n" \
M_"v[60:63], a[184:187], a[128:131], v[60:63]\n" \
"v_accvgpr_read_b32 v190, a6\n" \
"v_accvgpr_read_b32 v191, a7\n" \
M_"v[60:63], a[248:251], a[192:195], v[60:63]\n" \
X_"a[224:225], v32 offset:4096\n" \
"v_accvgpr_read_b32 v196, a12\n" \
M_"v[64:67], a[188:191], a[128:131], v[64:67]\n" \
"v_accvgpr_read_b32 v197, a13\n" \
"v_accvgpr_read_b32 v198, a14\n" \
M_"v[64:67], a[252:255], a[192:195], v[64:67]\n" \
R_"a[128:131], v28 offset:0\n" \
"v_accvgpr_read_b32 v199, a15\n" \
M_"v[80:83], a[188:191], a[132:135], v[80:83]\n" \
R_"a[192:195], v29 offset:0\n" \
"v_accvgpr_read_b32 v192, a8\n" \
M_"v[80:83], a[252:255], a[196:199], v[80:83]\n" \
X_"a[226:227], v32 offset:4608\n" \
"v_accvgpr_read_b32 v193, a9\n" \
M_"v[76:79], a[184:187], a[132:135], v[76:79]\n" \
"v_accvgpr_read_b32 v194, a10\n" \
"v_accvgpr_read_b32 v195, a11\n" \
M_"v[76:79], a[248:251], a[196:199], v[76:79]\n" \
X_"a[164:165], v33 offset:0\n" \
"v_accvgpr_read_b32 v208, a24\n" \
M_"v[72:75], a[180:183], a[132:135], v[72:75]\n" \
"v_accvgpr_read_b32 v209, a25\n" \
"v_accvgpr_read_b32 v210, a26\n" \
M_"v[72:75], a[244:247], a[196:199], v[72:75]\n" \
"v_accvgpr_read_b32 v211, a27\n" \
"v_accvgpr_read_b32 v212, a28\n" \
M_"v[68:71], a[176:179], a[132:135], v[68:71]\n" \
X_"a[166:167], v33 offset:512\n" \
"v_accvgpr_read_b32 v213, a29\n" \
M_"v[68:71], a[240:243], a[196:199], v[68:71]\n" \
R_"a[132:135], v28 offset:2048\n" \
"v_accvgpr_read_b32 v214, a30\n" \
M_"v[84:87], a[176:179], a[136:139], v[84:87]\n" \
R_"a[196:199], v29 offset:2048\n" \
"v_accvgpr_read_b32 v215, a31\n" \
M_"v[84:87], a[240:243], a[200:203], v[84:87]\n" \
X_"a[228:229], v33 offset:4096\n" \
"v_accvgpr_read_b32 v204, a20\n" \
M_"v[88:91], a[180:183], a[136:139], v[88:91]\n" \
"v_accvgpr_read_b32 v205, a21\n" \
"v_accvgpr_read_b32 v206, a22\n" \
M_"v[88:91], a[244:247], a[200:203], v[88:91]\n" \
"v_accvgpr_read_b32 v207, a23\n" \
"v_accvgpr_read_b32 v200, a16\n" \
M_"v[92:95], a[184:187], a[136:139], v[92:95]\n" \
X_"a[230:231], v33 offset:4608\n" \
"v_accvgpr_read_b32 v201, a17\n" \
M_"v[92:95], a[248:251], a[200:203], v[92:95]\n" \
"v_accvgpr_read_b32 v202, a18\n" \
"v_accvgpr_read_b32 v203, a19\n" \
M_"v[96:99], a[188:191], a[136:139], v[96:99]\n" \
"v_accvgpr_read_b32 v216, a32\n" \
"v_accvgpr_read_b32 v217, a33\n" \
M_"v[96:99], a[252:255], a[200:203], v[96:99]\n" \
R_"a[136:139], v28 offset:4096\n" \
"v_accvgpr_read_b32 v218, a34\n" \
M_"v[112:115], a[188:191], a[140:143], v[112:115]\n" \
R_"a[200:203], v29 offset:4096\n" \
"v_accvgpr_read_b32 v219, a35\n" \
M_"v[112:115], a[252:255], a[204:207], v[112:115]\n" \
X_"a[172:173], v35 offset:0\n" \
"v_accvgpr_read_b32 v220, a36\n" \
M_"v[108:111], a[184:187], a[140:143], v[108:111]\n" \
X_"a[174:175], v35 offset:512\n" \
"v_accvgpr_read_b32 v221, a37\n" \
M_"v[108:111], a[248:251], a[204:207], v[108:111]\n" \
"v_accvgpr_read_b32 v222, a38\n" \
"v_accvgpr_read_b32 v223, a39\n" \
M_"v[104:107], a[180:183], a[140:143], v[104:107]\n" \
X_"a[236:237], v35 offset:4096\n" \
"v_accvgpr_read_b32 v228, a44\n" \
M_"v[104:107], a[244:247], a[204:207], v[104:107]\n" \
"v_accvgpr_read_b32 v229, a45\n" \
"v_accvgpr_read_b32 v230, a46\n" \
M_"v[100:103], a[176:179], a[140:143], v[100:103]\n" \
"v_accvgpr_read_b32 v231, a47\n" \
"v_accvgpr_read_b32 v224, a40\n" \
M_"v[100:103], a[240:243], a[204:207], v[100:103]\n" \
R_"a[140:143], v28 offset:6144\n" \
"v_accvgpr_read_b32 v225, a41\n" \
M_"v[116:119], a[176:179], a[144:147], v[116:119]\n" \
R_"a[204:207], v29 offset:6144\n" \
"v_accvgpr_read_b32 v226, a42\n" \
M_"v[116:119], a[240:243], a[208:211], v[116:119]\n" \
X_"a[238:239], v35 offset:4608\n" \
"v_accvgpr_read_b32 v227, a43\n" \
M_"v[120:123], a[180:183], a[144:147], v[120:123]\n" \
X_"a[168:169], v34 offset:0\n" \
"v_accvgpr_read_b32 v240, a56\n" \
M_"v[120:123], a[244:247], a[208:211], v[120:123]\n" \
"v_accvgpr_read_b32 v241, a57\n" \
"v_accvgpr_read_b32 v242, a58\n" \
M_"v[124:127], a[184:187], a[144:147], v[124:127]\n" \
"v_accvgpr_read_b32 v243, a59\n" \
"v_accvgpr_read_b32 v244, a60\n" \
M_"v[124:127], a[248:251], a[208:211], v[124:127]\n" \
X_"a[170:171], v34 offset:512\n" \
"v_accvgpr_read_b32 v245, a61\n" \
M_"v[128:131], a[188:191], a[144:147], v[128:131]\n" \
"v_accvgpr_read_b32 v246, a62\n" \
"v_accvgpr_read_b32 v247, a63\n" \
M_"v[128:131], a[252:255], a[208:211], v[128:131]\n" \
R_"a[144:147], v28 offset:8192\n" \
"v_accvgpr_read_b32 v236, a52\n" \
M_"v[144:147], a[188:191], a[148:151], v[144:147]\n" \
R_"a[208:211], v29 offset:8192\n" \
"v_accvgpr_read_b32 v237, a53\n" \
M_"v[144:147], a[252:255], a[212:215], v[144:147]\n" \
X_"a[232:233], v34 offset:4096\n" \
"v_accvgpr_read_b32 v238, a54\n" \
M_"v[140:143], a[184:187], a[148:151], v[140:143]\n" \
"v_accvgpr_read_b32 v239, a55\n" \
"v_accvgpr_read_b32 v232, a48\n" \
M_"v[140:143], a[248:251], a[212:215], v[140:143]\n" \
X_"a[234:235], v34 offset:4608\n" \
"v_accvgpr_read_b32 v233, a49\n" \
M_"v[136:139], a[180:183], a[148:151], v[136:139]\n" \
"v_accvgpr_read_b32 v234, a50\n" \
"v_accvgpr_read_b32 v235, a51\n" \
M_"v[136:139], a[244:247], a[212:215], v[136:139]\n" \
"v_accvgpr_read_b32 v180, a64\n" \
"v_accvgpr_read_b32 v181, a65\n" \
M_"v[132:135], a[176:179], a[148:151], v[132:135]\n" \
R_"v[248:251], v22 offset:0\n" \
"v_accvgpr_read_b32 v182, a66\n" \
M_"v[132:135], a[240:243], a[212:215], v[132:135]\n" \
R_"a[148:151], v28 offset:10240\n" \
"v_accvgpr_read_b32 v183, a67\n" \
M_"v[148:151], a[176:179], a[152:155], v[148:151]\n" \
R_"a[212:215], v29 offset:10240\n" \
"v_accvgpr_read_b32 v252, a68\n" \
M_"v[148:151], a[240:243], a[216:219], v[148:151]\n" \
R_"v[48:51], v23 offset:0\n" \
"v_accvgpr_read_b32 v253, a69\n" \
M_"v[152:155], a[180:183], a[152:155], v[152:155]\n" \
"v_accvgpr_read_b32 v254, a70\n" \
"v_accvgpr_read_b32 v255, a71\n" \
M_"v[152:155], a[244:247], a[216:219], v[152:155]\n" \
"v_lshrrev_b32_e32 v24, 9, v20\n" \
"v_and_b32_e32 v26, 0x1ff, v20\n" \
M_"v[156:159], a[184:187], a[152:155], v[156:159]\n" \
"v_lshl_add_u32 v24, v24, 3, v26\n" \
"v_lshrrev_b32_e64 v25, 8, v20\n" \
M_"v[156:159], a[248:251], a[216:219], v[156:159]\n" \
"v_mul_u32_u24_e32 v25, 0x24700, v25\n" \
"v_add_u32_e64 v25, v25, v20\n" \
M_"v[160:163], a[188:191], a[152:155], v[160:163]\n" \
"v_lshrrev_b32_e32 v26, 2, v20\n" \
"v_add_u32_e32 v26, s45, v26\n" \
M_"v[160:163], a[252:255], a[216:219], v[160:163]\n" \
R_"a[152:155], v28 offset:12288\n" \
"v_add_u32_e64 v27, s46, v20\n" \
M_"v[176:179], a[188:191], a[156:159], v[176:179]\n" \
R_"a[216:219], v29 offset:12288\n" \
"v_add_u32_e64 v21, s43, v27\n" \
M_"v[176:179], a[252:255], a[220:223], v[176:179]\n" \
M_"v[172:175], a[184:187], a[156:159], v[172:175]\n" \
M_"v[172:175], a[248:251], a[220:223], v[172:175]\n" \
M_"v[168:171], a[180:183], a[156:159], v[168:171]\n" \
M_"v[168:171], a[244:247], a[220:223], v[168:171]\n" \
M_"v[164:167], a[176:179], a[156:159], v[164:167]\n" \
M_"v[164:167], a[240:243], a[220:223], v[164:167]\n" \
R_"a[156:159], v28 offset:14336\n" \
R_"a[220:223], v29 offset:14336\n" \
"s_waitcnt lgkmcnt(15)\n" \
"s_nop 0\n" \
M_"v[184:187], a[160:163], a[128:131], v[184:187]\n" \
"v_bfe_u32 v44, v20, 2, 5\n" \
X_"a[176:177], v36 offset:0\n" \
M_"v[184:187], a[224:227], a[192:195], v[184:187]\n" \
"buffer_load_dword v44, v44, s[80:83], s38 offen offset:0 sc1\n" \
M_"v[188:191], a[164:167], a[128:131], v[188:191]\n" \
X_"a[178:179], v36 offset:1024\n" \
M_"v[188:191], a[228:231], a[192:195], v[188:191]\n" \
X_"a[240:241], v36 offset:8192\n" \
M_"v[196:199], a[172:175], a[128:131], v[196:199]\n" \
X_"a[242:243], v36 offset:9216\n" \
"s_waitcnt lgkmcnt(15)\n" \
M_"v[196:199], a[236:239], a[192:195], v[196:199]\n" \
M_"v[192:195], a[128:131], a[168:171], v[192:195]\n" \
X_"a[180:181], v37 offset:0\n" \
"s_waitcnt lgkmcnt(13)\n" \
M_"v[192:195], a[192:195], a[232:235], v[192:195]\n" \
M_"v[208:211], a[132:135], a[168:171], v[208:211]\n" \
X_"a[182:183], v37 offset:1024\n" \
M_"v[208:211], a[196:199], a[232:235], v[208:211]\n" \
X_"a[244:245], v37 offset:8192\n" \
M_"v[212:215], a[172:175], a[132:135], v[212:215]\n" \
M_"v[212:215], a[236:239], a[196:199], v[212:215]\n" \
X_"a[246:247], v37 offset:9216\n" \
".fill (L_pb + 36576 - .) / 4, 4, 0xBF800000\n" \
"L_pp9:\n" \
"s_cbranch_execz L_pret\n" \
"s_nop 0\n" \
C_"v188, v188, v189\n" \
M_"v[204:207], a[164:167], a[132:135], v[204:207]\n" \
C_"v189, v190, v191\n" \
C_"v190, v198, v199\n" \
M_"v[204:207], a[228:231], a[196:199], v[204:207]\n" \
X_"a[184:185], v38 offset:0\n" \
"v_mov_b32_e32 v191, 0x3f80\n" \
M_"v[200:203], a[160:163], a[132:135], v[200:203]\n" \
X_"a[186:187], v38 offset:1024\n" \
C_"v184, v184, v185\n" \
M_"v[200:203], a[224:227], a[196:199], v[200:203]\n" \
C_"v185, v186, v187\n" \
C_"v186, v196, v197\n" \
M_"v[216:219], a[160:163], a[136:139], v[216:219]\n" \
X_"a[248:249], v38 offset:8192\n" \
"v_mov_b32_e64 v187, 0\n" \
M_"v[216:219], a[224:227], a[200:203], v[216:219]\n" \
X_"a[250:251], v38 offset:9216\n" \
M_"v[220:223], a[164:167], a[136:139], v[220:223]\n" \
M_"v[220:223], a[228:231], a[200:203], v[220:223]\n" \
X_"a[188:189], v39 offset:0\n" \
C_"v204, v204, v205\n" \
M_"v[228:231], a[172:175], a[136:139], v[228:231]\n" \
C_"v205, v206, v207\n" \
C_"v206, v214, v215\n" \
M_"v[228:231], a[236:239], a[200:203], v[228:231]\n" \
X_"a[190:191], v39 offset:1024\n" \
"v_mov_b32_e32 v207, 0x3f80\n" \
M_"v[224:227], a[136:139], a[168:171], v[224:227]\n" \
X_"a[252:253], v39 offset:8192\n" \
C_"v200, v200, v201\n" \
M_"v[224:227], a[200:203], a[232:235], v[224:227]\n" \
C_"v201, v202, v203\n" \
C_"v202, v212, v213\n" \
M_"v[240:243], a[140:143], a[168:171], v[240:243]\n" \
X_"a[254:255], v39 offset:9216\n" \
"v_mov_b32_e64 v203, 0\n" \
M_"v[240:243], a[204:207], a[232:235], v[240:243]\n" \
M_"v[244:247], a[172:175], a[140:143], v[244:247]\n" \
M_"v[244:247], a[236:239], a[204:207], v[244:247]\n" \
C_"v220, v220, v221\n" \
C_"v221, v222, v223\n" \
M_"v[236:239], a[164:167], a[140:143], v[236:239]\n" \
C_"v222, v230, v231\n" \
"v_mov_b32_e32 v223, 0x3f80\n" \
M_"v[236:239], a[228:231], a[204:207], v[236:239]\n" \
C_"v216, v216, v217\n" \
C_"v217, v218, v219\n" \
M_"v[232:235], a[160:163], a[140:143], v[232:235]\n" \
C_"v218, v228, v229\n" \
"v_mov_b32_e64 v219, 0\n" \
M_"v[232:235], a[224:227], a[204:207], v[232:235]\n" \
"s_waitcnt lgkmcnt(15)\n" \
"s_nop 0\n" \
M_"v[180:183], a[164:167], v[248:251], v[180:183]\n" \
M_"v[180:183], a[228:231], v[48:51], v[180:183]\n" \
M_"a[80:83], a[172:175], v[248:251], a[80:83]\n" \
M_"a[80:83], a[236:239], v[48:51], a[80:83]\n" \
C_"v236, v236, v237\n" \
C_"v237, v238, v239\n" \
M_"v[252:255], v[248:251], a[168:171], v[252:255]\n" \
C_"v238, v246, v247\n" \
"v_mov_b32_e32 v239, 0x3f80\n" \
M_"v[252:255], v[48:51], a[232:235], v[252:255]\n" \
C_"v232, v232, v233\n" \
C_"v233, v234, v235\n" \
"s_waitcnt vmcnt(0) lgkmcnt(0)\n" \
"s_barrier\n" \
"v_mov_b32_e32 v45, s62\n" \
"s_mov_b64 exec, 1\n" \
"buffer_store_dword v45, off, s[80:83], s37 offset:32 sc1\n" \
"s_mov_b64 exec, -1\n" \
"v_cmp_ne_u32_e32 vcc, s62, v44\n" \
"s_nop 4\n" \
"s_cbranch_vccnz L_poll160_%=\n" \
"L_pollr160_%=:\n" \
M_"v[52:55], a[176:179], a[128:131], v[52:55]\n" \
"s_mov_b32 s73, s50\n" \
"s_sub_u32 m0, s31, 0x10000\n" \
M_"v[52:55], a[240:243], a[192:195], v[52:55]\n" \
B_"v20, s[80:83], s73 offen offset:0 sc1 lds\n" \
M_"v[56:59], a[180:183], a[128:131], v[56:59]\n" \
"s_add_u32 s74, s50, 0x4000\n" \
"s_add_u32 s75, s50, 0x8000\n" \
M_"v[56:59], a[244:247], a[192:195], v[56:59]\n" \
"s_mov_b32 s70, 0x3e0293ee\n" \
"s_mov_b32 s76, s52\n" \
M_"v[60:63], a[184:187], a[128:131], v[60:63]\n" \
"v_mul_f32_e32 v52, s70, v52\n" \
"v_mul_f32_e32 v53, s70, v53\n" \
M_"v[60:63], a[248:251], a[192:195], v[60:63]\n" \
B_"v20, s[80:83], s73 offen offset:1024 sc1 lds\n" \
M_"v[64:67], a[188:191], a[128:131], v[64:67]\n" \
"v_mul_f32_e32 v54, s70, v54\n" \
"v_mul_f32_e32 v55, s70, v55\n" \
M_"v[64:67], a[252:255], a[192:195], v[64:67]\n" \
C_"v52, v52, v53\n" \
"v_mul_f32_e64 v56, s70, v56\n" \
M_"v[80:83], a[188:191], a[132:135], v[80:83]\n" \
"v_mul_f32_e32 v60, s70, v60\n" \
"v_mul_f32_e32 v61, s70, v61\n" \
M_"v[80:83], a[252:255], a[196:199], v[80:83]\n" \
B_"v20, s[80:83], s73 offen offset:2048 sc1 lds\n" \
M_"v[76:79], a[184:187], a[132:135], v[76:79]\n" \
"v_mul_f32_e32 v62, s70, v62\n" \
"v_mul_f32_e32 v63, s70, v63\n" \
M_"v[76:79], a[248:251], a[196:199], v[76:79]\n" \
C_"v60, v60, v61\n" \
"v_mul_f32_e64 v57, s70, v57\n" \
M_"v[72:75], a[180:183], a[132:135], v[72:75]\n" \
"v_mul_f32_e32 v58, s70, v58\n" \
"v_mul_f32_e32 v59, s70, v59\n" \
M_"v[72:75], a[244:247], a[196:199], v[72:75]\n" \
B_"v20, s[80:83], s73 offen offset:3072 sc1 lds\n" \
M_"v[68:71], a[176:179], a[132:135], v[68:71]\n" \
"s_sub_u32 m0, s31, s51\n" \
C_"v53, v54, v55\n" \
M_"v[68:71], a[240:243], a[196:199], v[68:71]\n" \
"v_mul_f32_e32 v64, s70, v64\n" \
"v_mul_f32_e64 v65, s70, v65\n" \
M_"v[84:87], a[176:179], a[136:139], v[84:87]\n" \
"v_mul_f32_e32 v66, s70, v66\n" \
"v_mul_f32_e32 v67, s70, v67\n" \
M_"v[84:87], a[240:243], a[200:203], v[84:87]\n" \
B_"v20, s[80:83], s74 offen offset:0 sc1 lds\n" \
M_"v[88:91], a[180:183], a[136:139], v[88:91]\n" \
C_"v61, v62, v63\n" \
C_"v54, v56, v57\n" \
M_"v[88:91], a[244:247], a[200:203], v[88:91]\n" \
C_"v55, v58, v59\n" \
C_"v62, v64, v65\n" \
M_"v[92:95], a[184:187], a[136:139], v[92:95]\n" \
C_"v63, v66, v67\n" \
"v_mul_f32_e64 v68, s70, v68\n" \
M_"v[92:95], a[248:251], a[200:203], v[92:95]\n" \
B_"v20, s[80:83], s74 offen offset:1024 sc1 lds\n" \
M_"v[96:99], a[188:191], a[136:139], v[96:99]\n" \
"ds_write_b128 v27, v[52:55] offset:0\n" \
M_"v[96:99], a[252:255], a[200:203], v[96:99]\n" \
"v_mul_f32_e32 v69, s70, v69\n" \
"v_mul_f32_e32 v76, s70, v76\n" \
M_"v[112:115], a[188:191], a[140:143], v[112:115]\n" \
"v_mul_f32_e32 v77, s70, v77\n" \
"v_mul_f32_e32 v70, s70, v70\n" \
M_"v[112:115], a[252:255], a[204:207], v[112:115]\n" \
B_"v20, s[80:83], s74 offen offset:2048 sc1 lds\n" \
M_"v[108:111], a[184:187], a[140:143], v[108:111]\n" \
"ds_write_b128 v27, v[60:63] offset:1024\n" \
M_"v[108:111], a[248:251], a[204:207], v[108:111]\n" \
"v_mul_f32_e64 v71, s70, v71\n" \
C_"v68, v68, v69\n" \
M_"v[104:107], a[180:183], a[140:143], v[104:107]\n" \
"v_mul_f32_e32 v78, s70, v78\n" \
"v_mul_f32_e32 v79, s70, v79\n" \
M_"v[104:107], a[244:247], a[204:207], v[104:107]\n" \
B_"v20, s[80:83], s74 offen offset:3072 sc1 lds\n" \
M_"v[100:103], a[176:179], a[140:143], v[100:103]\n" \
"s_sub_u32 m0, s31, 0x4000\n" \
C_"v76, v76, v77\n" \
M_"v[100:103], a[240:243], a[204:207], v[100:103]\n" \
"v_mul_f32_e32 v72, s70, v72\n" \
"v_mul_f32_e32 v73, s70, v73\n" \
M_"v[116:119], a[176:179], a[144:147], v[116:119]\n" \
"v_mul_f32_e32 v74, s70, v74\n" \
"v_mul_f32_e32 v75, s70, v75\n" \
M_"v[116:119], a[240:243], a[208:211], v[116:119]\n" \
B_"v20, s[80:83], s75 offen offset:0 sc1 lds\n" \
M_"v[120:123], a[180:183], a[144:147], v[120:123]\n" \
C_"v69, v70, v71\n" \
"v_mul_f32_e64 v80, s70, v80\n" \
M_"v[120:123], a[244:247], a[208:211], v[120:123]\n" \
"v_mul_f32_e32 v81, s70, v81\n" \
"v_mul_f32_e32 v82, s70, v82\n" \
M_"v[124:127], a[184:187], a[144:147], v[124:127]\n" \
"v_mul_f32_e64 v83, s70, v83\n" \
C_"v77, v78, v79\n" \
M_"v[124:127], a[248:251], a[208:211], v[124:127]\n" \
B_"v20, s[80:83], s75 offen offset:1024 sc1 lds\n" \
M_"v[128:131], a[188:191], a[144:147], v[128:131]\n" \
C_"v70, v72, v73\n" \
C_"v71, v74, v75\n" \
M_"v[128:131], a[252:255], a[208:211], v[128:131]\n" \
C_"v78, v80, v81\n" \
C_"v79, v82, v83\n" \
M_"v[144:147], a[188:191], a[148:151], v[144:147]\n" \
"ds_write_b128 v27, v[68:71] offset:4096\n" \
M_"v[144:147], a[252:255], a[212:215], v[144:147]\n" \
B_"v20, s[80:83], s75 offen offset:2048 sc1 lds\n" \
M_"v[140:143], a[184:187], a[148:151], v[140:143]\n" \
C_"v180, v180, v181\n" \
"v_accvgpr_read_b32 v50, a82\n" \
M_"v[140:143], a[248:251], a[212:215], v[140:143]\n" \
"ds_write_b128 v27, v[76:79] offset:5120\n" \
M_"v[136:139], a[180:183], a[148:151], v[136:139]\n" \
"v_accvgpr_read_b32 v51, a83\n" \
C_"v181, v182, v183\n" \
M_"v[136:139], a[244:247], a[212:215], v[136:139]\n" \
B_"v20, s[80:83], s75 offen offset:3072 sc1 lds\n" \
M_"v[132:135], a[176:179], a[148:151], v[132:135]\n" \
"s_mov_b32 m0, s53\n" \
C_"v182, v50, v51\n" \
M_"v[132:135], a[240:243], a[212:215], v[132:135]\n" \
"v_mov_b32_e32 v183, 0x3f80\n" \
C_"v234, v244, v245\n" \
M_"v[148:151], a[176:179], a[152:155], v[148:151]\n" \
"v_mov_b32_e32 v235, 0\n" \
C_"v54, v192, v193\n" \
M_"v[148:151], a[240:243], a[216:219], v[148:151]\n" \
B_"v20, s[80:83], s76 offen offset:0 sc1 lds\n" \
M_"v[152:155], a[180:183], a[152:155], v[152:155]\n" \
C_"v55, v194, v195\n" \
C_"v56, v208, v209\n" \
M_"v[152:155], a[244:247], a[216:219], v[152:155]\n" \
C_"v57, v210, v211\n" \
C_"v58, v224, v225\n" \
C_"v59, v226, v227\n" \
C_"v60, v240, v241\n" \
C_"v61, v242, v243\n" \
M_"v[32:35], v[180:183], v[184:187], 0\n" \
B_"v20, s[80:83], s76 offen offset:1024 sc1 lds\n" \
M_"v[156:159], a[184:187], a[152:155], v[156:159]\n" \
M_"v[192:195], v[188:191], v[184:187], 0\n" \
M_"v[208:211], v[204:207], v[200:203], 0\n" \
M_"v[36:39], v[188:191], v[200:203], 0\n" \
M_"v[224:227], v[220:223], v[216:219], 0\n" \
M_"v[248:251], v[204:207], v[216:219], 0\n" \
M_"v[240:243], v[236:239], v[232:235], 0\n" \
M_"v[64:67], v[220:223], v[232:235], 0\n" \
"s_mov_b32 s64, 0xaaaaaaaa\n" \
"s_mov_b32 s65, 0xaaaaaaaa\n" \
M_"v[156:159], a[248:251], a[216:219], v[156:159]\n" \
"v_cndmask_b32_e64 v32, v32, v33, s[64:65]\n" \
"v_cndmask_b32_e64 v34, v34, v35, s[64:65]\n" \
M_"v[160:163], a[188:191], a[152:155], v[160:163]\n" \
"s_mov_b32 s66, 0xfff0ffff\n" \
"s_mov_b32 s67, 0xf000ff00\n" \
"s_mov_b32 s68, 0xffc0fffc\n" \
"s_mov_b32 s69, 0xc000fc00\n" \
"v_cndmask_b32_e64 v192, v192, v193, s[64:65]\n" \
"v_add_f32_e64 v32, s47, v32\n" \
"v_cndmask_b32_e64 v194, v194, v195, s[64:65]\n" \
"v_add_f32_e64 v34, s47, v34\n" \
"v_cndmask_b32_e64 v208, v208, v209, s[64:65]\n" \
"v_cndmask_b32_e64 v36, v36, v37, s[64:65]\n" \
"v_cndmask_b32_e64 v210, v210, v211, s[64:65]\n" \
"v_cndmask_b32_e64 v38, v38, v39, s[64:65]\n" \
"v_cndmask_b32_e64 v224, v224, v225, s[64:65]\n" \
"v_cndmask_b32_e64 v248, v248, v249, s[64:65]\n" \
"v_cndmask_b32_e64 v226, v226, v227, s[64:65]\n" \
"v_cndmask_b32_e64 v250, v250, v251, s[64:65]\n" \
"v_cndmask_b32_e64 v240, v240, v241, s[64:65]\n" \
"v_cndmask_b32_e64 v64, v64, v65, s[64:65]\n" \
"v_cndmask_b32_e64 v242, v242, v243, s[64:65]\n" \
"v_cndmask_b32_e64 v66, v66, v67, s[64:65]\n" \
"v_cndmask_b32_e64 v192, v32, v192, s[66:67]\n" \
"v_cndmask_b32_e64 v194, v34, v194, s[68:69]\n" \
"v_cndmask_b32_e64 v208, v36, v208, s[66:67]\n" \
"v_cndmask_b32_e64 v210, v38, v210, s[68:69]\n" \
"v_cndmask_b32_e64 v224, v248, v224, s[66:67]\n" \
"v_cndmask_b32_e64 v226, v250, v226, s[68:69]\n" \
"v_cndmask_b32_e64 v240, v64, v240, s[66:67]\n" \
"v_cndmask_b32_e64 v242, v66, v242, s[68:69]\n" \
M_"v[160:163], a[252:255], a[216:219], v[160:163]\n" \
"v_max_f32_e32 v193, v192, v194\n" \
"v_max_f32_e32 v209, v208, v210\n" \
"v_max_f32_e32 v225, v224, v226\n" \
"v_max_f32_e32 v241, v240, v242\n" \
"v_mul_f32_e32 v84, s70, v84\n" \
"v_permlane32_swap_b32_e32 v193, v225\n" \
"v_permlane32_swap_b32_e32 v209, v241\n" \
"v_mul_f32_e32 v85, s70, v85\n" \
"v_mul_f32_e32 v92, s70, v92\n" \
"v_mul_f32_e32 v93, s70, v93\n" \
"v_permlane32_swap_b32_e32 v196, v228\n" \
"v_permlane32_swap_b32_e32 v212, v244\n" \
"v_max_f32_e32 v193, v193, v225\n" \
"v_max_f32_e32 v209, v209, v241\n" \
"v_accvgpr_read_b32 v48, a80\n" \
"v_accvgpr_read_b32 v49, a81\n" \
"v_mul_f32_e32 v86, s70, v86\n" \
"v_mul_f32_e32 v87, s70, v87\n" \
C_"v84, v84, v85\n" \
"v_mul_f32_e32 v94, s70, v94\n" \
"v_mul_f32_e32 v95, s70, v95\n" \
C_"v92, v92, v93\n" \
"v_permlane16_swap_b32_e32 v228, v244\n" \
"v_permlane16_swap_b32_e32 v193, v209\n" \
M_"v[176:179], a[188:191], a[156:159], v[176:179]\n" \
"s_mov_b32 s72, 0x3eae1a93\n" \
"v_mul_f32_e32 v88, s70, v88\n" \
"v_mul_f32_e32 v89, s70, v89\n" \
"v_mul_f32_e32 v90, s70, v90\n" \
"v_mul_f32_e32 v91, s70, v91\n" \
C_"v85, v86, v87\n" \
"v_mul_f32_e32 v96, s70, v96\n" \
"v_mul_f32_e32 v97, s70, v97\n" \
"v_mul_f32_e32 v98, s70, v98\n" \
"v_mul_f32_e32 v99, s70, v99\n" \
C_"v93, v94, v95\n" \
"v_mul_f32_e32 v45, 0xbfb8aa3b, v228\n" \
"v_mfma_f32_16x16x16_bf16 v[48:51], v[182:183], v[46:47], 0\n" \
"v_mfma_f32_16x16x16_bf16 v[196:199], v[190:191], v[46:47], 0\n" \
"v_mfma_f32_16x16x16_bf16 v[212:215], v[206:207], v[46:47], 0\n" \
"v_max_f32_e64 v193, v193, v209\n" \
C_"v86, v88, v89\n" \
C_"v87, v90, v91\n" \
C_"v94, v96, v97\n" \
C_"v95, v98, v99\n" \
"v_mfma_f32_16x16x16_bf16 v[228:231], v[222:223], v[46:47], 0\n" \
"v_mul_f32_e32 v193, s72, v193\n" \
"v_mov_b32_e32 v209, v193\n" \
"ds_write_b128 v27, v[84:87] offset:8192\n" \
"v_mfma_f32_16x16x16_bf16 v[244:247], v[238:239], v[46:47], 0\n" \
"v_permlane16_swap_b32_e32 v193, v209\n" \
"s_mov_b32 s71, 0x3e0293ee\n" \
M_"v[176:179], a[252:255], a[220:223], v[176:179]\n" \
"v_mov_b32_e32 v225, v193\n" \
"v_mov_b32_e32 v241, v209\n" \
"v_mul_f32_e32 v100, s70, v100\n" \
"v_mul_f32_e64 v101, s70, v101\n" \
"ds_write_b128 v27, v[92:95] offset:9216\n" \
"v_mul_f32_e32 v108, s70, v108\n" \
"v_mul_f32_e32 v109, s70, v109\n" \
"v_pk_mul_f32 v[116:117], v[116:117], s[70:71]\n" \
"v_pk_mul_f32 v[124:125], v[124:125], s[70:71]\n" \
"v_pk_mul_f32 v[132:133], v[132:133], s[70:71]\n" \
"v_pk_mul_f32 v[140:141], v[140:141], s[70:71]\n" \
"v_pk_mul_f32 v[148:149], v[148:149], s[70:71]\n" \
"v_pk_mul_f32 v[156:157], v[156:157], s[70:71]\n" \
"v_permlane32_swap_b32_e32 v193, v225\n" \
"v_permlane32_swap_b32_e32 v209, v241\n" \
"v_mul_f32_e32 v102, s70, v102\n" \
"v_mul_f32_e32 v103, s70, v103\n" \
C_"v100, v100, v101\n" \
"v_mul_f32_e32 v110, s70, v110\n" \
"v_mul_f32_e32 v111, s70, v111\n" \
C_"v108, v108, v109\n" \
"v_pk_mul_f32 v[118:119], v[118:119], s[70:71]\n" \
C_"v116, v116, v117\n" \
"v_pk_mul_f32 v[126:127], v[126:127], s[70:71]\n" \
C_"v124, v124, v125\n" \
"v_pk_mul_f32 v[134:135], v[134:135], s[70:71]\n" \
C_"v132, v132, v133\n" \
"v_pk_mul_f32 v[142:143], v[142:143], s[70:71]\n" \
C_"v140, v140, v141\n" \
"v_pk_mul_f32 v[150:151], v[150:151], s[70:71]\n" \
C_"v148, v148, v149\n" \
"v_pk_mul_f32 v[158:159], v[158:159], s[70:71]\n" \
C_"v156, v156, v157\n" \
"v_fma_f32 v192, v192, s72, -v193\n" \
"v_fma_f32 v194, v194, s72, -v193\n" \
"v_fma_f32 v208, v208, s72, -v209\n" \
"v_fma_f32 v210, v210, s72, -v209\n" \
"v_mul_f32_e32 v104, s70, v104\n" \
"v_mul_f32_e32 v105, s70, v105\n" \
"v_mul_f32_e32 v106, s70, v106\n" \
"v_mul_f32_e32 v107, s70, v107\n" \
C_"v101, v102, v103\n" \
"v_mul_f32_e32 v112, s70, v112\n" \
"v_mul_f32_e32 v113, s70, v113\n" \
"v_mul_f32_e32 v114, s70, v114\n" \
"v_mul_f32_e32 v115, s70, v115\n" \
C_"v109, v110, v111\n" \
"v_pk_mul_f32 v[120:121], v[120:121], s[70:71]\n" \
"v_pk_mul_f32 v[122:123], v[122:123], s[70:71]\n" \
C_"v117, v118, v119\n" \
"v_pk_mul_f32 v[128:129], v[128:129], s[70:71]\n" \
"v_pk_mul_f32 v[130:131], v[130:131], s[70:71]\n" \
C_"v125, v126, v127\n" \
"v_pk_mul_f32 v[136:137], v[136:137], s[70:71]\n" \
"v_pk_mul_f32 v[138:139], v[138:139], s[70:71]\n" \
C_"v133, v134, v135\n" \
"v_pk_mul_f32 v[144:145], v[144:145], s[70:71]\n" \
"v_pk_mul_f32 v[146:147], v[146:147], s[70:71]\n" \
C_"v141, v142, v143\n" \
"v_pk_mul_f32 v[152:153], v[152:153], s[70:71]\n" \
"v_pk_mul_f32 v[154:155], v[154:155], s[70:71]\n" \
C_"v149, v150, v151\n" \
"v_pk_mul_f32 v[160:161], v[160:161], s[70:71]\n" \
"v_pk_mul_f32 v[162:163], v[162:163], s[70:71]\n" \
C_"v157, v158, v159\n" \
"v_pk_mul_f32 v[176:177], v[176:177], s[70:71]\n" \
"v_pk_mul_f32 v[178:179], v[178:179], s[70:71]\n" \
E_"v192, v192\n" \
E_"v194, v194\n" \
E_"v208, v208\n" \
E_"v210, v210\n" \
"v_fma_f32 v224, v224, s72, -v225\n" \
"v_fma_f32 v226, v226, s72, -v225\n" \
M_"v[172:175], a[184:187], a[156:159], v[172:175]\n" \
C_"v102, v104, v105\n" \
C_"v103, v106, v107\n" \
C_"v110, v112, v113\n" \
C_"v111, v114, v115\n" \
C_"v118, v120, v121\n" \
C_"v119, v122, v123\n" \
C_"v126, v128, v129\n" \
C_"v127, v130, v131\n" \
C_"v134, v136, v137\n" \
C_"v135, v138, v139\n" \
C_"v142, v144, v145\n" \
C_"v143, v146, v147\n" \
C_"v150, v152, v153\n" \
C_"v151, v154, v155\n" \
C_"v158, v160, v161\n" \
C_"v159, v162, v163\n" \
E_"v224, v224\n" \
E_"v226, v226\n" \
"v_fma_f32 v240, v240, s72, -v241\n" \
"v_fma_f32 v242, v242, s72, -v241\n" \
"ds_write_b128 v27, v[100:103] offset:12288\n" \
C_"v192, v192, v194\n" \
C_"v208, v208, v210\n" \
E_"v240, v240\n" \
E_"v242, v242\n" \
C_"v68, v48, v49\n" \
C_"v69, v50, v51\n" \
C_"v70, v196, v197\n" \
C_"v71, v198, v199\n" \
"ds_write_b128 v27, v[108:111] offset:13312\n" \
C_"v72, v212, v213\n" \
C_"v73, v214, v215\n" \
"v_perm_b32 v184, v192, v192, v40\n" \
"v_perm_b32 v185, v192, v192, v41\n" \
"v_perm_b32 v186, v192, v192, v42\n" \
"v_perm_b32 v187, v192, v192, v43\n" \
"v_perm_b32 v200, v208, v208, v40\n" \
"v_perm_b32 v201, v208, v208, v41\n" \
"v_perm_b32 v202, v208, v208, v42\n" \
"v_perm_b32 v203, v208, v208, v43\n" \
"ds_write_b128 v21, v[116:119] offset:16384\n" \
C_"v224, v224, v226\n" \
C_"v74, v228, v229\n" \
C_"v75, v230, v231\n" \
M_"v[32:35], v[68:71], v[184:187], 0\n" \
M_"v[36:39], v[70:73], v[200:203], 0\n" \
"v_perm_b32 v216, v224, v224, v40\n" \
"v_perm_b32 v217, v224, v224, v41\n" \
"ds_write_b128 v21, v[124:127] offset:17408\n" \
"v_perm_b32 v218, v224, v224, v42\n" \
"v_perm_b32 v219, v224, v224, v43\n" \
C_"v240, v240, v242\n" \
M_"v[172:175], a[248:251], a[220:223], v[172:175]\n" \
C_"v76, v244, v245\n" \
C_"v77, v246, v247\n" \
M_"v[248:251], v[72:75], v[216:219], 0\n" \
"v_perm_b32 v232, v240, v240, v40\n" \
"v_perm_b32 v233, v240, v240, v41\n" \
"ds_write_b128 v21, v[132:135] offset:20480\n" \
"v_perm_b32 v234, v240, v240, v42\n" \
"v_perm_b32 v235, v240, v240, v43\n" \
"v_pk_mul_f32 v[172:173], v[172:173], s[70:71]\n" \
"v_pk_mul_f32 v[174:175], v[174:175], s[70:71]\n" \
C_"v172, v172, v173\n" \
C_"v173, v174, v175\n" \
C_"v174, v176, v177\n" \
C_"v175, v178, v179\n" \
M_"v[64:67], v[74:77], v[232:235], 0\n" \
"ds_write_b128 v21, v[140:143] offset:21504\n" \
M_"v[168:171], a[180:183], a[156:159], v[168:171]\n" \
C_"v52, v252, v253\n" \
C_"v53, v254, v255\n" \
M_"v[168:171], a[244:247], a[220:223], v[168:171]\n" \
"v_exp_f32_e64 v45, v45\n" \
M_"v[192:195], v[52:55], v[184:187], 0\n" \
"ds_write_b128 v21, v[148:151] offset:24576\n" \
M_"v[208:211], v[54:57], v[200:203], 0\n" \
M_"v[224:227], v[56:59], v[216:219], 0\n" \
M_"v[240:243], v[58:61], v[232:235], 0\n" \
"ds_write_b128 v21, v[156:159] offset:25600\n" \
"v_mov_b32_dpp v34, v38 quad_perm:[0,1,2,3] row_mask:0x2 bank_mask:0xf\n" \
"v_pk_mul_f32 v[168:169], v[168:169], s[70:71]\n" \
"v_pk_mul_f32 v[170:171], v[170:171], s[70:71]\n" \
"v_mov_b32_dpp v34, v250 quad_perm:[0,1,2,3] row_mask:0x4 bank_mask:0xf\n" \
"v_add_f32_e64 v45, 1.0, v45\n" \
M_"v[164:167], a[176:179], a[156:159], v[164:167]\n" \
"v_mov_b32_dpp v34, v66 quad_perm:[0,1,2,3] row_mask:0x8 bank_mask:0xf\n" \
"v_mov_b32_dpp v32, v36 quad_perm:[0,1,2,3] row_mask:0x2 bank_mask:0xf\n" \
"v_rcp_f32_e64 v45, v45\n" \
"ds_write_b128 v21, v[172:175] offset:29696\n" \
"v_mov_b32_dpp v33, v37 quad_perm:[0,1,2,3] row_mask:0x2 bank_mask:0xf\n" \
"v_rcp_f32_e64 v34, v34\n" \
"v_mov_b32_dpp v32, v248 quad_perm:[0,1,2,3] row_mask:0x4 bank_mask:0xf\n" \
"v_mov_b32_dpp v33, v249 quad_perm:[0,1,2,3] row_mask:0x4 bank_mask:0xf\n" \
"v_mul_f32_e64 v34, v45, v34\n" \
"v_mov_b32_dpp v32, v64 quad_perm:[0,1,2,3] row_mask:0x8 bank_mask:0xf\n" \
"v_mov_b32_dpp v33, v65 quad_perm:[0,1,2,3] row_mask:0x8 bank_mask:0xf\n" \
"v_mul_f32_e32 v32, v34, v32\n" \
"v_mul_f32_e32 v33, v34, v33\n" \
"v_mov_b32_e64 v38, v34\n" \
M_"v[164:167], a[240:243], a[220:223], v[164:167]\n" \
"v_sub_f32_e32 v44, 1.0, v45\n" \
"v_permlane16_swap_b32_e32 v34, v38\n" \
"v_mov_b32_e32 v250, v34\n" \
"v_mov_b32_e32 v66, v38\n" \
C_"v32, v32, v33\n" \
"v_permlane32_swap_b32_e32 v34, v250\n" \
"v_permlane32_swap_b32_e32 v38, v66\n" \
"v_mul_f32_e64 v192, v34, v192\n" \
"v_pk_mul_f32 v[164:165], v[164:165], s[70:71]\n" \
"v_pk_mul_f32 v[166:167], v[166:167], s[70:71]\n" \
C_"v164, v164, v165\n" \
C_"v165, v166, v167\n" \
C_"v166, v168, v169\n" \
C_"v167, v170, v171\n" \
"ds_write_b128 v21, v[164:167] offset:28672\n" \
"v_mul_f32_e32 v193, v34, v193\n" \
"v_mul_f32_e32 v208, v38, v208\n" \
"v_mul_f32_e32 v209, v38, v209\n" \
"v_mul_f32_e32 v224, v250, v224\n" \
"v_mul_f32_e32 v225, v250, v225\n" \
"v_mul_f32_e32 v240, v66, v240\n" \
"v_mul_f32_e32 v241, v66, v241\n" \
"v_mul_f32_e32 v194, v34, v194\n" \
"v_mul_f32_e64 v195, v34, v195\n" \
C_"v192, v192, v193\n" \
"v_mul_f32_e32 v210, v38, v210\n" \
"v_mul_f32_e32 v211, v38, v211\n" \
C_"v208, v208, v209\n" \
"v_mul_f32_e32 v226, v250, v226\n" \
"v_mul_f32_e32 v227, v250, v227\n" \
C_"v224, v224, v225\n" \
"v_mul_f32_e32 v242, v66, v242\n" \
"v_mul_f32_e32 v243, v66, v243\n" \
C_"v240, v240, v241\n" \
C_"v193, v194, v195\n" \
"s_add_u32 s84, s48, 0x0\n" \
"s_nop 0\n" \
C_"v209, v210, v211\n" \
"s_add_u32 s85, s48, 0x24800\n" \
C_"v225, v226, v227\n" \
"s_add_u32 s86, s48, 0x49000\n" \
C_"v241, v242, v243\n" \
"s_add_u32 s87, s48, 0x6d800\n" \
"ds_write_b32 v26, v44\n" \
"buffer_store_dword v32, v25, s[80:83], s49 offen sc1\n" \
"buffer_store_dwordx2 v[192:193], v24, s[80:83], s84 offen sc1\n" \
"buffer_store_dwordx2 v[208:209], v24, s[80:83], s85 offen sc1\n" \
"buffer_store_dwordx2 v[224:225], v24, s[80:83], s86 offen sc1\n" \
"buffer_store_dwordx2 v[240:241], v24, s[80:83], s87 offen sc1\n" \
"s_branch L_end_%=\n" \
"L_poll60_%=:\n" \
"s_mov_b32 s84, 0\n" \
"L_pl60_%=:\n" \
"s_sleep 1\n" \
"v_bfe_u32 v44, v20, 2, 5\n" \
"buffer_load_dword v44, v44, s[80:83], s38 offen offset:128 sc1\n" \
"s_waitcnt vmcnt(0)\n" \
"v_cmp_ne_u32_e32 vcc, s62, v44\n" \
"s_add_u32 s84, s84, 1\n" \
"s_nop 3\n" \
"s_cbranch_vccz L_pollr60_%=\n" \
"s_cmp_gt_u32 s84, 0x400000\n" \
"s_cbranch_scc0 L_pl60_%=\n" \
"s_branch L_pollr60_%=\n" \
"L_poll160_%=:\n" \
"s_mov_b32 s84, 0\n" \
"L_pl160_%=:\n" \
"s_sleep 1\n" \
"v_bfe_u32 v44, v20, 2, 5\n" \
"buffer_load_dword v44, v44, s[80:83], s38 offen offset:0 sc1\n" \
"s_waitcnt vmcnt(0)\n" \
"v_cmp_ne_u32_e32 vcc, s62, v44\n" \
"s_add_u32 s84, s84, 1\n" \
"s_nop 3\n" \
"s_cbranch_vccz L_pollr160_%=\n" \
"s_cmp_gt_u32 s84, 0x400000\n" \
"s_cbranch_scc0 L_pl160_%=\n" \
"s_branch L_pollr160_%=\n" \
".fill (L_pb + 42548 - .) / 4, 4, 0xBF800000\n" \
"L_w1_%=:\n" \
"s_waitcnt vmcnt(19)\n" \
"s_barrier\n" \
X_"a[160:161], v32 offset:0\n" \
X_"a[162:163], v32 offset:1024\n" \
R_"a[128:131], v28 offset:16384\n" \
X_"a[224:225], v32 offset:8192\n" \
X_"a[226:227], v32 offset:9216\n" \
R_"a[192:195], v29 offset:16384\n" \
R_"a[132:135], v28 offset:18432\n" \
R_"a[196:199], v29 offset:18432\n" \
X_"a[164:165], v33 offset:0\n" \
X_"a[166:167], v33 offset:1024\n" \
X_"a[228:229], v33 offset:8192\n" \
X_"a[230:231], v33 offset:9216\n" \
R_"a[136:139], v28 offset:20480\n" \
R_"a[200:203], v29 offset:20480\n" \
"s_waitcnt lgkmcnt(11)\n" \
M_"a[0:3], a[128:131], a[160:163], 0\n" \
X_"a[168:169], v34 offset:0\n" \
"s_lshr_b32 s4, s2, 1\n" \
"s_waitcnt lgkmcnt(9)\n" \
M_"a[0:3], a[192:195], a[224:227], a[0:3]\n" \
X_"a[170:171], v34 offset:1024\n" \
"s_nop 5\n" \
"s_waitcnt lgkmcnt(9)\n" \
M_"a[16:19], a[132:135], a[160:163], 0\n" \
X_"a[232:233], v34 offset:8192\n" \
"s_and_b32 s4, s4, 0x7c\n" \
"s_waitcnt lgkmcnt(9)\n" \
M_"a[16:19], a[196:199], a[224:227], a[16:19]\n" \
X_"a[234:235], v34 offset:9216\n" \
"s_nop 5\n" \
"s_waitcnt lgkmcnt(8)\n" \
M_"a[20:23], a[132:135], a[164:167], 0\n" \
R_"a[140:143], v28 offset:22528\n" \
"v_mov_b32_e32 v213, s13\n" \
"s_waitcnt lgkmcnt(7)\n" \
M_"a[20:23], a[196:199], a[228:231], a[20:23]\n" \
R_"a[204:207], v29 offset:22528\n" \
"s_nop 5\n" \
M_"a[4:7], a[128:131], a[164:167], 0\n" \
X_"a[172:173], v35 offset:0\n" \
"s_or_b32 s4, s4, s3\n" \
M_"a[4:7], a[192:195], a[228:231], a[4:7]\n" \
X_"a[174:175], v35 offset:1024\n" \
"s_nop 5\n" \
"s_waitcnt lgkmcnt(9)\n" \
M_"a[32:35], a[136:139], a[160:163], 0\n" \
X_"a[236:237], v35 offset:8192\n" \
"v_lshrrev_b32_e32 v212, 4, v20\n" \
"s_waitcnt lgkmcnt(9)\n" \
M_"a[32:35], a[200:203], a[224:227], a[32:35]\n" \
X_"a[238:239], v35 offset:9216\n" \
"s_nop 5\n" \
M_"a[36:39], a[136:139], a[164:167], 0\n" \
R_"a[144:147], v28 offset:0\n" \
"v_mad_u64_u32 v[214:215], s[4:5], s4, 36, v[212:213]\n" \
M_"a[36:39], a[200:203], a[228:231], a[36:39]\n" \
R_"a[208:211], v29 offset:0\n" \
"s_nop 5\n" \
"s_waitcnt lgkmcnt(10)\n" \
M_"a[40:43], a[136:139], a[168:171], 0\n" \
R_"a[148:151], v28 offset:2048\n" \
"v_mov_b32_e32 v217, s9\n" \
"s_waitcnt lgkmcnt(9)\n" \
M_"a[40:43], a[200:203], a[232:235], a[40:43]\n" \
R_"a[212:215], v29 offset:2048\n" \
"s_nop 5\n" \
M_"a[24:27], a[132:135], a[168:171], 0\n" \
R_"a[152:155], v28 offset:4096\n" \
"s_movk_i32 s4, 0x240\n" \
M_"a[24:27], a[196:199], a[232:235], a[24:27]\n" \
R_"a[216:219], v29 offset:4096\n" \
"s_nop 5\n" \
M_"a[8:11], a[128:131], a[168:171], 0\n" \
R_"a[156:159], v28 offset:6144\n" \
"v_cmp_gt_u32_e32 vcc, s4, v20\n" \
M_"a[8:11], a[192:195], a[232:235], a[8:11]\n" \
R_"a[220:223], v29 offset:6144\n" \
"s_mov_b32 m0, s32\n" \
"s_nop 5\n" \
"s_waitcnt lgkmcnt(13)\n" \
M_"a[48:51], a[140:143], a[160:163], 0\n" \
"s_mov_b32 exec_lo, 0xff000000\n" \
"s_mov_b32 exec_hi, 0x0\n" \
"global_load_lds_dword v[184:185], off\n" \
"s_mov_b64 exec, -1\n" \
"s_waitcnt lgkmcnt(12)\n" \
M_"a[48:51], a[204:207], a[224:227], a[48:51]\n" \
"s_nop 5\n" \
X_"a[176:177], v36 offset:0\n" \
M_"a[52:55], a[140:143], a[164:167], 0\n" \
X_"a[178:179], v36 offset:1024\n" \
"s_lshl_b32 s4, s2, 3\n" \
M_"a[52:55], a[204:207], a[228:231], a[52:55]\n" \
"s_mov_b32 m0, s32\n" \
"s_nop 5\n" \
M_"a[56:59], a[140:143], a[168:171], 0\n" \
"s_mov_b32 exec_lo, 0xffff\n" \
"s_mov_b32 exec_hi, 0x0\n" \
"global_load_lds_dword v[186:187], off\n" \
"s_mov_b64 exec, -1\n" \
M_"a[56:59], a[204:207], a[232:235], a[56:59]\n" \
"s_nop 5\n" \
X_"a[240:241], v36 offset:8192\n" \
"s_waitcnt lgkmcnt(13)\n" \
M_"a[60:63], a[140:143], a[172:175], 0\n" \
X_"a[242:243], v36 offset:9216\n" \
"s_and_b32 s4, s4, 56\n" \
"s_waitcnt lgkmcnt(12)\n" \
M_"a[60:63], a[204:207], a[236:239], a[60:63]\n" \
"s_nop 5\n" \
X_"a[180:181], v37 offset:0\n" \
M_"a[44:47], a[136:139], a[172:175], 0\n" \
X_"a[182:183], v37 offset:1024\n" \
"v_cndmask_b32_e32 v217, v217, v213, vcc\n" \
M_"a[44:47], a[200:203], a[236:239], a[44:47]\n" \
"s_nop 5\n" \
X_"a[244:245], v37 offset:8192\n" \
M_"a[28:31], a[132:135], a[172:175], 0\n" \
X_"a[246:247], v37 offset:9216\n" \
"v_lshlrev_b32_e32 v213, 3, v214\n" \
M_"a[28:31], a[196:199], a[236:239], a[28:31]\n" \
"s_nop 5\n" \
X_"a[184:185], v38 offset:0\n" \
M_"a[12:15], a[128:131], a[172:175], 0\n" \
"s_mov_b64 exec, 0\n" \
"s_swappc_b64 s[88:89], s[86:87]\n" \
"s_mov_b64 exec, -1\n" \
"s_add_u32 s86, s86, 0x2000\n" \
"s_addc_u32 s87, s87, 0\n" \
M_"a[12:15], a[192:195], a[236:239], a[12:15]\n" \
"s_nop 5\n" \
X_"a[186:187], v38 offset:1024\n" \
"s_waitcnt lgkmcnt(15)\n" \
M_"a[76:79], a[144:147], a[172:175], 0\n" \
X_"a[248:249], v38 offset:8192\n" \
"v_and_b32_e32 v213, 0x1ffc0, v213\n" \
M_"a[76:79], a[208:211], a[236:239], a[76:79]\n" \
"s_nop 5\n" \
"v_and_b32_e64 v214, 7, v214\n" \
M_"a[72:75], a[144:147], a[168:171], 0\n" \
"v_or3_b32 v213, v214, s4, v213\n" \
"s_lshl_b32 s4, s2, 2\n" \
M_"a[72:75], a[208:211], a[232:235], a[72:75]\n" \
"s_nop 5\n" \
X_"a[250:251], v38 offset:9216\n" \
M_"a[68:71], a[144:147], a[164:167], 0\n" \
"s_or_b32 s4, s4, s3\n" \
"s_mul_i32 s4, s4, 18\n" \
M_"a[68:71], a[208:211], a[228:231], a[68:71]\n" \
"s_nop 5\n" \
"v_min_u32_e32 v212, 53, v212\n" \
M_"a[64:67], a[144:147], a[160:163], 0\n" \
"s_sub_i32 s4, s4, 36\n" \
"v_add_u32_e32 v212, s4, v212\n" \
M_"a[64:67], a[208:211], a[224:227], a[64:67]\n" \
"s_nop 5\n" \
X_"a[188:189], v39 offset:0\n" \
M_"a[80:83], a[148:151], a[160:163], 0\n" \
"v_mov_b32_e32 v215, s12\n" \
"v_mov_b32_e32 v216, s8\n" \
"s_waitcnt lgkmcnt(15)\n" \
M_"a[80:83], a[212:215], a[224:227], a[80:83]\n" \
"s_nop 5\n" \
"v_cndmask_b32_e32 v212, v212, v213, vcc\n" \
M_"a[84:87], a[148:151], a[164:167], 0\n" \
X_"a[190:191], v39 offset:1024\n" \
"v_cndmask_b32_e64 v216, v216, v215, vcc\n" \
M_"a[84:87], a[212:215], a[228:231], a[84:87]\n" \
"s_nop 5\n" \
"v_lshlrev_b32_e32 v212, 7, v212\n" \
M_"a[88:91], a[148:151], a[168:171], 0\n" \
"v_mov_b32_e64 v213, 0\n" \
"v_lshl_add_u64 v[188:189], v[216:217], 0, v[212:213]\n" \
M_"a[88:91], a[212:215], a[232:235], a[88:91]\n" \
"s_nop 5\n" \
X_"a[252:253], v39 offset:8192\n" \
M_"a[92:95], a[148:151], a[172:175], 0\n" \
"s_movk_i32 s5, 0xd80\n" \
"s_lshr_b32 s6, s2, 5\n" \
M_"a[92:95], a[212:215], a[236:239], a[92:95]\n" \
"s_nop 5\n" \
"v_lshrrev_b32_e64 v212, 4, v20\n" \
M_"a[108:111], a[152:155], a[172:175], 0\n" \
X_"a[254:255], v39 offset:9216\n" \
"v_lshl_or_b32 v215, s3, 6, v212\n" \
M_"a[108:111], a[216:219], a[236:239], a[108:111]\n" \
"s_nop 5\n" \
"v_mul_u32_u24_e32 v216, 0x2aab, v215\n" \
M_"a[104:107], a[152:155], a[168:171], 0\n" \
"s_mov_b64 exec, 0\n" \
"s_swappc_b64 s[88:89], s[86:87]\n" \
"s_mov_b64 exec, -1\n" \
"s_andn2_b32 s85, 2, s3\n" \
"s_lshl_b32 s85, s85, 12\n" \
"s_add_u32 s86, s86, s85\n" \
"s_addc_u32 s87, s87, 0\n" \
M_"a[104:107], a[216:219], a[232:235], a[104:107]\n" \
"s_nop 5\n" \
"v_lshrrev_b32_e32 v216, 16, v216\n" \
M_"a[100:103], a[152:155], a[164:167], 0\n" \
"v_mad_i32_i24 v217, v216, -6, v215\n" \
"v_add_u32_e32 v218, 0xfffffe50, v215\n" \
M_"a[100:103], a[216:219], a[228:231], a[100:103]\n" \
"s_nop 5\n" \
"s_mul_i32 s35, s6, 0x48\n" \
M_"a[96:99], a[152:155], a[160:163], 0\n" \
"v_lshrrev_b32_e32 v219, 1, v217\n" \
"v_add_u32_e64 v218, s35, v216\n" \
M_"a[96:99], a[216:219], a[224:227], a[96:99]\n" \
"s_nop 5\n" \
"v_mul_u32_u24_e32 v219, 0x480, v219\n" \
"s_waitcnt lgkmcnt(15)\n" \
M_"a[112:115], a[156:159], a[160:163], 0\n" \
"v_and_b32_e64 v214, 16, v20\n" \
"v_mad_u32_u24 v219, v218, s5, v219\n" \
M_"a[112:115], a[220:223], a[224:227], a[112:115]\n" \
"s_nop 5\n" \
"v_or_b32_e32 v192, v219, v214\n" \
M_"a[116:119], a[156:159], a[164:167], 0\n" \
"v_mov_b32_e32 v220, 2.0\n" \
"v_mov_b32_e32 v222, 7\n" \
M_"a[116:119], a[220:223], a[228:231], a[116:119]\n" \
"s_nop 5\n" \
"v_lshlrev_b32_e32 v220, 7, v218\n" \
M_"a[120:123], a[156:159], a[168:171], 0\n" \
"v_lshrrev_b32_e32 v218, 1, v218\n" \
"v_and_b32_e32 v218, 4, v218\n" \
M_"a[120:123], a[220:223], a[232:235], a[120:123]\n" \
"s_nop 5\n" \
"v_and_b32_e32 v216, 2, v216\n" \
M_"a[124:127], a[156:159], a[172:175], 0\n" \
"s_mul_i32 s7, s6, 0x2400\n" \
"s_sub_i32 s34, 0x22000, s7\n" \
"s_waitcnt vmcnt(5) lgkmcnt(0)\n" \
M_"a[124:127], a[220:223], a[236:239], a[124:127]\n" \
"s_barrier\n" \
M_"v[52:55], a[128:131], a[176:179], 0\n" \
"s_add_u32 m0, s30, 0x0\n" \
"s_movk_i32 s84, 0x100\n" \
M_"v[52:55], a[192:195], a[240:243], v[52:55]\n" \
B_"v22, s[64:67], s84 offen offset:0 lds\n" \
"s_nop 5\n" \
M_"v[56:59], a[128:131], a[180:183], 0\n" \
X_"a[160:161], v32 offset:32768\n" \
"v_bitop3_b32 v216, v217, v218, v216 bitop3:0x1e\n" \
M_"v[56:59], a[192:195], a[244:247], v[56:59]\n" \
"s_movk_i32 s84, 0x1d00\n" \
"s_nop 5\n" \
M_"v[60:63], a[128:131], a[184:187], 0\n" \
B_"v23, s[64:67], s84 offen offset:1024 lds\n" \
M_"v[60:63], a[192:195], a[248:251], v[60:63]\n" \
"s_nop 5\n" \
X_"a[162:163], v32 offset:33792\n" \
M_"v[64:67], a[128:131], a[188:191], 0\n" \
X_"a[224:225], v32 offset:40960\n" \
"v_or_b32_e32 v213, 0x24400, v20\n" \
M_"v[64:67], a[192:195], a[252:255], v[64:67]\n" \
R_"a[128:131], v28 offset:49152\n" \
"s_nop 5\n" \
M_"v[80:83], a[132:135], a[188:191], 0\n" \
R_"a[192:195], v29 offset:49152\n" \
"v_lshlrev_b32_e32 v216, 4, v216\n" \
M_"v[80:83], a[196:199], a[252:255], v[80:83]\n" \
"s_movk_i32 s84, 0x3900\n" \
"s_nop 5\n" \
M_"v[76:79], a[132:135], a[184:187], 0\n" \
B_"v22, s[64:67], s84 offen offset:2048 lds\n" \
M_"v[76:79], a[196:199], a[248:251], v[76:79]\n" \
"s_movk_i32 s84, 0x5500\n" \
"s_nop 5\n" \
M_"v[72:75], a[132:135], a[180:183], 0\n" \
B_"v23, s[64:67], s84 offen offset:3072 lds\n" \
M_"v[72:75], a[196:199], a[244:247], v[72:75]\n" \
"s_add_u32 m0, s31, 0x0\n" \
"s_mov_b32 s84, 0x100000\n" \
"s_nop 5\n" \
M_"v[68:71], a[132:135], a[176:179], 0\n" \
B_"v26, s[68:71], s84 offen offset:0 lds\n" \
M_"v[68:71], a[196:199], a[240:243], v[68:71]\n" \
R_"a[132:135], v28 offset:51200\n" \
"s_nop 5\n" \
M_"v[84:87], a[136:139], a[176:179], 0\n" \
R_"a[196:199], v29 offset:51200\n" \
"v_add3_u32 v196, v220, s34, v216\n" \
M_"v[84:87], a[200:203], a[240:243], v[84:87]\n" \
"s_addk_i32 s84, 0x7c00\n" \
"s_nop 5\n" \
M_"v[88:91], a[136:139], a[180:183], 0\n" \
B_"v26, s[68:71], s84 offen offset:1024 lds\n" \
M_"v[88:91], a[200:203], a[244:247], v[88:91]\n" \
"s_nop 5\n" \
X_"a[226:227], v32 offset:41984\n" \
M_"v[92:95], a[136:139], a[184:187], 0\n" \
"s_addk_i32 s84, 0x7c00\n" \
M_"v[92:95], a[200:203], a[248:251], v[92:95]\n" \
B_"v27, s[68:71], s84 offen offset:2048 lds\n" \
"s_nop 5\n" \
M_"v[96:99], a[136:139], a[188:191], 0\n" \
X_"a[164:165], v33 offset:32768\n" \
"v_mov_b32_e64 v216, v213\n" \
M_"v[96:99], a[200:203], a[252:255], v[96:99]\n" \
R_"a[136:139], v28 offset:53248\n" \
"s_nop 5\n" \
M_"v[112:115], a[140:143], a[188:191], 0\n" \
R_"a[200:203], v29 offset:53248\n" \
"s_movk_i32 s36, 0x1b0\n" \
M_"v[112:115], a[204:207], a[252:255], v[112:115]\n" \
"s_addk_i32 s84, 0x7c00\n" \
"s_nop 5\n" \
M_"v[108:111], a[140:143], a[184:187], 0\n" \
B_"v27, s[68:71], s84 offen offset:3072 lds\n" \
"s_mov_b64 exec, 0\n" \
"s_swappc_b64 s[88:89], s[86:87]\n" \
"s_mov_b64 exec, -1\n" \
M_"v[108:111], a[204:207], a[248:251], v[108:111]\n" \
"s_add_u32 m0, s30, 0x1000\n" \
"s_mov_b32 s84, 0x8100\n" \
"s_nop 5\n" \
M_"v[104:107], a[140:143], a[180:183], 0\n" \
B_"v22, s[64:67], s84 offen offset:0 lds\n" \
M_"v[104:107], a[204:207], a[244:247], v[104:107]\n" \
"s_nop 5\n" \
X_"a[166:167], v33 offset:33792\n" \
M_"v[100:103], a[140:143], a[176:179], 0\n" \
X_"a[228:229], v33 offset:40960\n" \
"v_or_b32_e32 v216, 0x100, v215\n" \
M_"v[100:103], a[204:207], a[240:243], v[100:103]\n" \
R_"a[140:143], v28 offset:55296\n" \
"s_nop 5\n" \
M_"v[116:119], a[144:147], a[176:179], 0\n" \
R_"a[204:207], v29 offset:55296\n" \
"v_mul_u32_u24_e32 v217, 0x2aab, v216\n" \
M_"v[116:119], a[208:211], a[240:243], v[116:119]\n" \
"s_addk_i32 s84, 0x1c00\n" \
"s_nop 5\n" \
M_"v[120:123], a[144:147], a[180:183], 0\n" \
B_"v23, s[64:67], s84 offen offset:1024 lds\n" \
M_"v[120:123], a[208:211], a[244:247], v[120:123]\n" \
"s_addk_i32 s84, 0x1c00\n" \
"s_nop 5\n" \
M_"v[124:127], a[144:147], a[184:187], 0\n" \
B_"v22, s[64:67], s84 offen offset:2048 lds\n" \
M_"v[124:127], a[208:211], a[248:251], v[124:127]\n" \
".fill (L_pb + 44768 - .) / 4, 4, 0xBF800000\n" \
"L_pp11:\n" \
"s_cbranch_execz L_pret\n" \
"s_addk_i32 s84, 0x1c00\n" \
"s_nop 5\n" \
M_"v[128:131], a[144:147], a[188:191], 0\n" \
B_"v23, s[64:67], s84 offen offset:3072 lds\n" \
M_"v[128:131], a[208:211], a[252:255], v[128:131]\n" \
R_"a[144:147], v28 offset:32768\n" \
"s_nop 5\n" \
M_"v[144:147], a[148:151], a[188:191], 0\n" \
R_"a[208:211], v29 offset:32768\n" \
"v_add_u32_e32 v215, 0xffffff50, v215\n" \
M_"v[144:147], a[212:215], a[252:255], v[144:147]\n" \
"s_add_u32 m0, s31, 0x4000\n" \
"s_mov_b32 s84, 0x100000\n" \
"s_nop 5\n" \
M_"v[140:143], a[148:151], a[184:187], 0\n" \
B_"v26, s[72:75], s84 offen offset:0 lds\n" \
M_"v[140:143], a[212:215], a[248:251], v[140:143]\n" \
"s_nop 5\n" \
X_"a[230:231], v33 offset:41984\n" \
M_"v[136:139], a[148:151], a[180:183], 0\n" \
"s_addk_i32 s84, 0x7c00\n" \
M_"v[136:139], a[212:215], a[244:247], v[136:139]\n" \
B_"v26, s[72:75], s84 offen offset:1024 lds\n" \
"s_nop 5\n" \
M_"v[132:135], a[148:151], a[176:179], 0\n" \
X_"a[168:169], v34 offset:32768\n" \
"v_lshrrev_b32_e64 v217, 16, v217\n" \
M_"v[132:135], a[212:215], a[240:243], v[132:135]\n" \
R_"a[148:151], v28 offset:34816\n" \
"s_nop 5\n" \
M_"v[148:151], a[152:155], a[176:179], 0\n" \
R_"a[212:215], v29 offset:34816\n" \
"v_mov_b32_e32 v220, v215\n" \
M_"v[148:151], a[216:219], a[240:243], v[148:151]\n" \
"s_nop 5\n" \
X_"a[170:171], v34 offset:33792\n" \
M_"v[152:155], a[152:155], a[180:183], 0\n" \
X_"a[232:233], v34 offset:40960\n" \
"v_mad_i32_i24 v218, v217, -6, v216\n" \
M_"v[152:155], a[216:219], a[244:247], v[152:155]\n" \
"s_nop 5\n" \
X_"a[234:235], v34 offset:41984\n" \
M_"v[156:159], a[152:155], a[184:187], 0\n" \
X_"a[172:173], v35 offset:32768\n" \
"v_cmp_gt_u32_e64 vcc, s36, v216\n" \
M_"v[156:159], a[216:219], a[248:251], v[156:159]\n" \
"s_nop 5\n" \
X_"a[174:175], v35 offset:33792\n" \
M_"v[160:163], a[152:155], a[188:191], 0\n" \
X_"a[236:237], v35 offset:40960\n" \
"v_mov_b32_e32 v221, 7\n" \
M_"v[160:163], a[216:219], a[252:255], v[160:163]\n" \
R_"a[152:155], v28 offset:36864\n" \
"s_nop 5\n" \
M_"v[176:179], a[156:159], a[188:191], 0\n" \
R_"a[216:219], v29 offset:36864\n" \
"v_lshrrev_b32_e32 v222, 1, v218\n" \
M_"v[176:179], a[220:223], a[252:255], v[176:179]\n" \
"s_nop 5\n" \
X_"a[238:239], v35 offset:41984\n" \
M_"v[172:175], a[156:159], a[184:187], 0\n" \
"v_cndmask_b32_e32 v217, v220, v217, vcc\n" \
"s_movk_i32 s4, 0x48\n" \
M_"v[172:175], a[220:223], a[248:251], v[172:175]\n" \
"s_nop 5\n" \
"v_add_u32_e64 v220, s35, v217\n" \
M_"v[168:171], a[156:159], a[180:183], 0\n" \
"v_mul_u32_u24_e32 v222, 0x480, v222\n" \
"v_mad_u32_u24 v222, v220, s5, v222\n" \
M_"v[168:171], a[220:223], a[244:247], v[168:171]\n" \
"s_nop 5\n" \
"v_cmp_gt_u32_e64 s[4:5], s4, v215\n" \
M_"v[164:167], a[156:159], a[176:179], 0\n" \
"v_cndmask_b32_e32 v215, v221, v218, vcc\n" \
"v_lshrrev_b32_e64 v218, 1, v220\n" \
M_"v[164:167], a[220:223], a[240:243], v[164:167]\n" \
R_"a[156:159], v28 offset:38912\n" \
R_"a[220:223], v29 offset:38912\n" \
"s_waitcnt lgkmcnt(15)\n" \
M_"a[0:3], a[128:131], a[160:163], a[0:3]\n" \
"s_addk_i32 s84, 0x7c00\n" \
M_"a[0:3], a[192:195], a[224:227], a[0:3]\n" \
B_"v27, s[72:75], s84 offen offset:2048 lds\n" \
M_"a[4:7], a[128:131], a[164:167], a[4:7]\n" \
X_"a[176:177], v36 offset:32768\n" \
"v_or_b32_e32 v214, v222, v214\n" \
"s_waitcnt lgkmcnt(15)\n" \
M_"a[4:7], a[192:195], a[228:231], a[4:7]\n" \
"s_addk_i32 s84, 0x7c00\n" \
"s_waitcnt lgkmcnt(11)\n" \
M_"a[8:11], a[128:131], a[168:171], a[8:11]\n" \
B_"v27, s[72:75], s84 offen offset:3072 lds\n" \
"s_waitcnt lgkmcnt(9)\n" \
M_"a[8:11], a[192:195], a[232:235], a[8:11]\n" \
X_"a[178:179], v36 offset:33792\n" \
"v_mov_b32_e32 v222, 2.0\n" \
"s_waitcnt lgkmcnt(8)\n" \
M_"a[12:15], a[128:131], a[172:175], a[12:15]\n" \
X_"a[240:241], v36 offset:40960\n" \
"v_and_b32_e32 v218, 4, v218\n" \
"s_waitcnt lgkmcnt(5)\n" \
M_"a[12:15], a[192:195], a[236:239], a[12:15]\n" \
"v_and_b32_e32 v217, 2, v217\n" \
"v_bitop3_b32 v215, v215, v218, v217 bitop3:0x1e\n" \
M_"a[28:31], a[132:135], a[172:175], a[28:31]\n" \
"v_lshlrev_b32_e32 v223, 1, v220\n" \
"v_mov_b32_e32 v224, 2.0\n" \
M_"a[28:31], a[196:199], a[236:239], a[28:31]\n" \
X_"a[242:243], v36 offset:41984\n" \
"v_cndmask_b32_e64 v193, v222, v214, vcc\n" \
M_"a[24:27], a[132:135], a[168:171], a[24:27]\n" \
"v_lshlrev_b32_e32 v214, 7, v220\n" \
"v_lshlrev_b32_e32 v215, 4, v215\n" \
M_"a[24:27], a[196:199], a[232:235], a[24:27]\n" \
"s_mul_i32 s39, s3, 18\n" \
"v_add3_u32 v214, v214, s34, v215\n" \
M_"a[20:23], a[132:135], a[164:167], a[20:23]\n" \
"v_cndmask_b32_e64 v195, v224, v223, s[4:5]\n" \
"s_movk_i32 s4, 0x1f8\n" \
M_"a[20:23], a[196:199], a[228:231], a[20:23]\n" \
X_"a[180:181], v37 offset:32768\n" \
"v_mov_b32_e64 v215, v213\n" \
M_"a[16:19], a[132:135], a[160:163], a[16:19]\n" \
"s_add_i32 s35, s35, s39\n" \
"v_cmp_gt_u32_e32 vcc, s4, v216\n" \
M_"a[16:19], a[196:199], a[224:227], a[16:19]\n" \
"v_add_u32_e32 v212, s35, v212\n" \
"v_mov_b32_e32 v219, 2.0\n" \
M_"a[32:35], a[136:139], a[160:163], a[32:35]\n" \
"s_mov_b32 m0, s32\n" \
X_"a[182:183], v37 offset:33792\n" \
M_"a[32:35], a[200:203], a[224:227], a[32:35]\n" \
"s_mov_b32 exec_lo, 0x0\n" \
"s_mov_b32 exec_hi, 0xff\n" \
"global_load_lds_dword v[184:185], off\n" \
"s_mov_b64 exec, -1\n" \
M_"a[36:39], a[136:139], a[164:167], a[36:39]\n" \
"v_cndmask_b32_e32 v197, v215, v214, vcc\n" \
"s_movk_i32 s35, 0x120\n" \
M_"a[36:39], a[200:203], a[228:231], a[36:39]\n" \
X_"a[244:245], v37 offset:40960\n" \
"v_mul_u32_u24_e32 v214, 0xd80, v212\n" \
M_"a[40:43], a[136:139], a[168:171], a[40:43]\n" \
"v_cmp_gt_u32_e32 vcc, s35, v20\n" \
"v_lshrrev_b32_e64 v215, 1, v212\n" \
M_"a[40:43], a[200:203], a[232:235], a[40:43]\n" \
"s_mov_b32 m0, s32\n" \
"s_and_b32 s4, s2, 7\n" \
M_"a[44:47], a[136:139], a[172:175], a[44:47]\n" \
"s_mov_b32 exec_lo, 0xffff0000\n" \
"s_mov_b32 exec_hi, 0x0\n" \
"global_load_lds_dword v[186:187], off\n" \
"s_mov_b64 exec, -1\n" \
M_"a[44:47], a[200:203], a[236:239], a[44:47]\n" \
X_"a[246:247], v37 offset:41984\n" \
"v_cndmask_b32_e64 v194, v219, v214, vcc\n" \
M_"a[60:63], a[140:143], a[172:175], a[60:63]\n" \
"v_lshlrev_b32_e32 v214, 7, v212\n" \
"v_lshrrev_b32_e32 v212, 2, v212\n" \
M_"a[60:63], a[204:207], a[236:239], a[60:63]\n" \
"v_and_b32_e64 v212, 2, v212\n" \
"v_and_or_b32 v212, v215, 1, v212\n" \
M_"a[56:59], a[140:143], a[168:171], a[56:59]\n" \
X_"a[184:185], v38 offset:32768\n" \
"s_lshl_b32 s5, s4, 2\n" \
M_"a[56:59], a[204:207], a[232:235], a[56:59]\n" \
"s_bfe_u32 s38, s2, 0x20003\n" \
"v_lshlrev_b32_e32 v212, 5, v212\n" \
M_"a[52:55], a[140:143], a[164:167], a[52:55]\n" \
"s_movk_i32 s35, 0x60\n" \
"s_or_b32 s5, s5, s38\n" \
M_"a[52:55], a[204:207], a[228:231], a[52:55]\n" \
X_"a[186:187], v38 offset:33792\n" \
"v_bitop3_b32 v212, v212, s35, v214 bitop3:0x36\n" \
M_"a[48:51], a[140:143], a[160:163], a[48:51]\n" \
"s_lshr_b32 s36, s2, 3\n" \
"v_add_u32_e32 v212, s34, v212\n" \
M_"a[48:51], a[204:207], a[224:227], a[48:51]\n" \
"s_mul_i32 s34, s5, 0x12000\n" \
"s_or_b32 s40, s34, 0x6000000\n" \
M_"a[64:67], a[144:147], a[160:163], a[64:67]\n" \
X_"a[248:249], v38 offset:40960\n" \
"s_mul_i32 s34, s36, 0x90000\n" \
M_"a[64:67], a[208:211], a[224:227], a[64:67]\n" \
"s_lshl_b32 s36, s3, 14\n" \
"s_add_i32 s34, s34, s36\n" \
M_"a[68:71], a[144:147], a[164:167], a[68:71]\n" \
"s_lshl_b32 s38, s5, 8\n" \
"s_add_i32 s41, s34, 0x4240000\n" \
M_"a[68:71], a[208:211], a[228:231], a[68:71]\n" \
X_"a[250:251], v38 offset:41984\n" \
"s_mul_i32 s34, s5, 36\n" \
M_"a[72:75], a[144:147], a[168:171], a[72:75]\n" \
"s_add_i32 s5, s7, s40\n" \
"s_addk_i32 s5, 0x2000\n" \
M_"a[72:75], a[208:211], a[232:235], a[72:75]\n" \
"s_mov_b32 s37, 2.0\n" \
"s_or_b32 s38, s38, 0x5be8000\n" \
M_"a[76:79], a[144:147], a[172:175], a[76:79]\n" \
"s_cmp_lg_u32 s3, 0\n" \
"s_cselect_b32 s36, s37, s5\n" \
M_"a[76:79], a[208:211], a[236:239], a[76:79]\n" \
X_"a[188:189], v39 offset:32768\n" \
"s_lshl_b32 s5, s6, 2\n" \
M_"a[92:95], a[148:151], a[172:175], a[92:95]\n" \
"s_or_b32 s5, s38, s5\n" \
"s_lshl_b32 s35, s3, 10\n" \
M_"a[92:95], a[212:215], a[236:239], a[92:95]\n" \
"s_cmpk_lt_u32 s3, 3\n" \
"s_cselect_b32 s37, s37, s5\n" \
M_"a[88:91], a[148:151], a[168:171], a[88:91]\n" \
X_"a[190:191], v39 offset:33792\n" \
"s_lshl_b32 s5, s4, 16\n" \
M_"a[88:91], a[212:215], a[232:235], a[88:91]\n" \
"s_lshl_b32 s4, s4, 11\n" \
"v_cndmask_b32_e64 v198, v213, v212, vcc\n" \
M_"a[84:87], a[148:151], a[164:167], a[84:87]\n" \
"v_or_b32_e32 v212, s35, v20\n" \
"s_or_b32 s39, s40, s35\n" \
M_"a[84:87], a[212:215], a[228:231], a[84:87]\n" \
X_"a[252:253], v39 offset:40960\n" \
"s_or_b32 s55, s41, s4\n" \
M_"a[80:83], a[148:151], a[160:163], a[80:83]\n" \
"v_or_b32_e32 v199, 0x22000, v212\n" \
"s_add_i32 s35, s39, s7\n" \
M_"a[80:83], a[212:215], a[224:227], a[80:83]\n" \
"s_add_i32 s54, s41, s5\n" \
"s_add_i32 s55, s55, 0x80000\n" \
M_"a[96:99], a[152:155], a[160:163], a[96:99]\n" \
X_"a[254:255], v39 offset:41984\n" \
"v_mov_b32_e32 v215, 0xffff\n" \
M_"a[96:99], a[216:219], a[224:227], a[96:99]\n" \
"s_and_b32 s4, s3, 1\n" \
"s_lshl_b32 s5, s4, 4\n" \
M_"a[100:103], a[152:155], a[164:167], a[100:103]\n" \
"v_lshrrev_b32_e32 v212, 4, v20\n" \
"v_lshrrev_b32_e64 v218, 6, v20\n" \
M_"a[100:103], a[216:219], a[228:231], a[100:103]\n" \
"v_and_b32_e64 v219, 12, v218\n" \
"v_and_or_b32 v214, v212, 15, s5\n" \
M_"a[104:107], a[152:155], a[168:171], a[104:107]\n" \
"v_cmp_gt_u32_e64 vcc, v219, v214\n" \
"v_mov_b32_e32 v220, 0xffff0000\n" \
M_"a[104:107], a[216:219], a[232:235], a[104:107]\n" \
"s_lshl_b32 s5, s2, 2\n" \
"v_cndmask_b32_e64 v217, v215, 0, vcc\n" \
M_"a[108:111], a[152:155], a[172:175], a[108:111]\n" \
"v_cmp_lt_u32_e32 vcc, v219, v214\n" \
"v_mov_b32_e64 v213, s23\n" \
M_"a[108:111], a[216:219], a[236:239], a[108:111]\n" \
"s_or_b32 s5, s5, s3\n" \
"v_cndmask_b32_e32 v221, 0, v220, vcc\n" \
"s_waitcnt lgkmcnt(15)\n" \
M_"a[124:127], a[156:159], a[172:175], a[124:127]\n" \
"v_or_b32_e32 v12, v217, v221\n" \
"v_or_b32_e64 v217, 2, v219\n" \
M_"a[124:127], a[220:223], a[236:239], a[124:127]\n" \
"v_cmp_gt_u32_e32 vcc, v217, v214\n" \
"v_mov_b32_e32 v216, s25\n" \
M_"a[120:123], a[156:159], a[168:171], a[120:123]\n" \
"s_movk_i32 s40, 0xf400\n" \
"v_cndmask_b32_e64 v221, v215, 0, vcc\n" \
M_"a[120:123], a[220:223], a[232:235], a[120:123]\n" \
"v_cmp_lt_u32_e32 vcc, v217, v214\n" \
"s_lshl_b32 s4, s4, 6\n" \
M_"a[116:119], a[156:159], a[164:167], a[116:119]\n" \
"v_cndmask_b32_e32 v217, 0, v220, vcc\n" \
"v_or_b32_e64 v13, v221, v217\n" \
M_"a[116:119], a[220:223], a[228:231], a[116:119]\n" \
"v_or_b32_e32 v217, 16, v219\n" \
"v_cmp_gt_u32_e32 vcc, v217, v214\n" \
M_"a[112:115], a[156:159], a[160:163], a[112:115]\n" \
"s_nop 0\n" \
"v_cndmask_b32_e64 v221, v215, 0, vcc\n" \
"v_cmp_lt_u32_e32 vcc, v217, v214\n" \
M_"a[112:115], a[220:223], a[224:227], a[112:115]\n" \
"s_waitcnt vmcnt(20)\n" \
"v_readfirstlane_b32 s62, v180\n" \
"s_nop 3\n" \
"s_add_u32 s62, s62, 0x9E3779B1\n" \
"s_waitcnt vmcnt(2) lgkmcnt(0)\n" \
"s_barrier\n" \
M_"v[52:55], a[128:131], a[176:179], v[52:55]\n" \
"s_add_u32 m0, s30, 0x8000\n" \
"s_movk_i32 s84, 0x180\n" \
M_"v[52:55], a[192:195], a[240:243], v[52:55]\n" \
B_"v22, s[64:67], s84 offen offset:0 lds\n" \
M_"v[56:59], a[128:131], a[180:183], v[56:59]\n" \
X_"a[160:161], v32 offset:0\n" \
"v_cndmask_b32_e64 v217, 0, v220, vcc\n" \
M_"v[56:59], a[192:195], a[244:247], v[56:59]\n" \
"s_movk_i32 s84, 0x1d80\n" \
M_"v[60:63], a[128:131], a[184:187], v[60:63]\n" \
B_"v23, s[64:67], s84 offen offset:1024 lds\n" \
M_"v[60:63], a[192:195], a[248:251], v[60:63]\n" \
X_"a[162:163], v32 offset:1024\n" \
"v_or_b32_e32 v14, v221, v217\n" \
M_"v[64:67], a[128:131], a[188:191], v[64:67]\n" \
X_"a[224:225], v32 offset:8192\n" \
"v_or_b32_e64 v217, 18, v219\n" \
M_"v[64:67], a[192:195], a[252:255], v[64:67]\n" \
R_"a[128:131], v28 offset:16384\n" \
"v_cmp_gt_u32_e64 vcc, v217, v214\n" \
M_"v[80:83], a[132:135], a[188:191], v[80:83]\n" \
R_"a[192:195], v29 offset:16384\n" \
"v_cndmask_b32_e64 v215, v215, 0, vcc\n" \
M_"v[80:83], a[196:199], a[252:255], v[80:83]\n" \
"s_movk_i32 s84, 0x3980\n" \
M_"v[76:79], a[132:135], a[184:187], v[76:79]\n" \
B_"v22, s[64:67], s84 offen offset:2048 lds\n" \
M_"v[76:79], a[196:199], a[248:251], v[76:79]\n" \
"s_movk_i32 s84, 0x5580\n" \
M_"v[72:75], a[132:135], a[180:183], v[72:75]\n" \
B_"v23, s[64:67], s84 offen offset:3072 lds\n" \
M_"v[72:75], a[196:199], a[244:247], v[72:75]\n" \
"s_add_u32 m0, s31, 0x8000\n" \
"s_mov_b32 s84, 0x180000\n" \
M_"v[68:71], a[132:135], a[176:179], v[68:71]\n" \
B_"v26, s[68:71], s84 offen offset:0 lds\n" \
M_"v[68:71], a[196:199], a[240:243], v[68:71]\n" \
R_"a[132:135], v28 offset:18432\n" \
"v_cmp_lt_u32_e64 vcc, v217, v214\n" \
M_"v[84:87], a[136:139], a[176:179], v[84:87]\n" \
R_"a[196:199], v29 offset:18432\n" \
"v_cndmask_b32_e64 v214, 0, v220, vcc\n" \
M_"v[84:87], a[200:203], a[240:243], v[84:87]\n" \
"s_addk_i32 s84, 0x7c00\n" \
M_"v[88:91], a[136:139], a[180:183], v[88:91]\n" \
B_"v26, s[68:71], s84 offen offset:1024 lds\n" \
M_"v[88:91], a[200:203], a[244:247], v[88:91]\n" \
X_"a[226:227], v32 offset:9216\n" \
"v_or_b32_e32 v15, v215, v214\n" \
M_"v[92:95], a[136:139], a[184:187], v[92:95]\n" \
"s_addk_i32 s84, 0x7c00\n" \
M_"v[92:95], a[200:203], a[248:251], v[92:95]\n" \
B_"v27, s[68:71], s84 offen offset:2048 lds\n" \
M_"v[96:99], a[136:139], a[188:191], v[96:99]\n" \
X_"a[164:165], v33 offset:0\n" \
"v_mad_u64_u32 v[214:215], s[6:7], s5, 42, v[212:213]\n" \
M_"v[96:99], a[200:203], a[252:255], v[96:99]\n" \
R_"a[136:139], v28 offset:20480\n" \
"s_movk_i32 s5, 0x1440\n" \
M_"v[112:115], a[140:143], a[188:191], v[112:115]\n" \
R_"a[200:203], v29 offset:20480\n" \
"v_cmp_gt_u32_e64 vcc, s5, v214\n" \
M_"v[112:115], a[204:207], a[252:255], v[112:115]\n" \
"s_addk_i32 s84, 0x7c00\n" \
"s_nop 0\n" \
M_"v[108:111], a[140:143], a[184:187], v[108:111]\n" \
B_"v27, s[68:71], s84 offen offset:3072 lds\n" \
M_"v[108:111], a[204:207], a[248:251], v[108:111]\n" \
"s_add_u32 m0, s30, 0x9000\n" \
"s_mov_b32 s84, 0x8180\n" \
M_"v[104:107], a[140:143], a[180:183], v[104:107]\n" \
B_"v22, s[64:67], s84 offen offset:0 lds\n" \
M_"v[104:107], a[204:207], a[244:247], v[104:107]\n" \
X_"a[166:167], v33 offset:1024\n" \
"v_min_u32_e32 v217, 0xa43f, v214\n" \
M_"v[100:103], a[140:143], a[176:179], v[100:103]\n" \
X_"a[228:229], v33 offset:8192\n" \
"v_mov_b32_e64 v214, s24\n" \
M_"v[100:103], a[204:207], a[240:243], v[100:103]\n" \
R_"a[140:143], v28 offset:22528\n" \
"v_cndmask_b32_e64 v215, v216, v213, vcc\n" \
M_"v[116:119], a[144:147], a[176:179], v[116:119]\n" \
R_"a[204:207], v29 offset:22528\n" \
"v_mov_b32_e64 v213, s22\n" \
M_"v[116:119], a[208:211], a[240:243], v[116:119]\n" \
"s_addk_i32 s84, 0x1c00\n" \
M_"v[120:123], a[144:147], a[180:183], v[120:123]\n" \
B_"v23, s[64:67], s84 offen offset:1024 lds\n" \
M_"v[120:123], a[208:211], a[244:247], v[120:123]\n" \
"s_addk_i32 s84, 0x1c00\n" \
M_"v[124:127], a[144:147], a[184:187], v[124:127]\n" \
B_"v22, s[64:67], s84 offen offset:2048 lds\n" \
M_"v[124:127], a[208:211], a[248:251], v[124:127]\n" \
"s_addk_i32 s84, 0x1c00\n" \
M_"v[128:131], a[144:147], a[188:191], v[128:131]\n" \
B_"v23, s[64:67], s84 offen offset:3072 lds\n" \
M_"v[128:131], a[208:211], a[252:255], v[128:131]\n" \
R_"a[144:147], v28 offset:0\n" \
"s_movk_i32 s6, 0xffee\n" \
M_"v[144:147], a[148:151], a[188:191], v[144:147]\n" \
R_"a[208:211], v29 offset:0\n" \
"v_cndmask_b32_e64 v214, v214, v213, vcc\n" \
M_"v[144:147], a[212:215], a[252:255], v[144:147]\n" \
"s_add_u32 m0, s31, 0xc000\n" \
"s_mov_b32 s84, 0x180000\n" \
M_"v[140:143], a[148:151], a[184:187], v[140:143]\n" \
B_"v26, s[72:75], s84 offen offset:0 lds\n" \
M_"v[140:143], a[212:215], a[248:251], v[140:143]\n" \
X_"a[230:231], v33 offset:9216\n" \
"v_add_u32_e32 v213, 0xffffebc0, v217\n" \
M_"v[136:139], a[148:151], a[180:183], v[136:139]\n" \
"s_addk_i32 s84, 0x7c00\n" \
M_"v[136:139], a[212:215], a[244:247], v[136:139]\n" \
B_"v26, s[72:75], s84 offen offset:1024 lds\n" \
M_"v[132:135], a[148:151], a[176:179], v[132:135]\n" \
X_"a[168:169], v34 offset:0\n" \
"s_lshl_b32 s5, s2, 5\n" \
M_"v[132:135], a[212:215], a[240:243], v[132:135]\n" \
R_"a[148:151], v28 offset:2048\n" \
"v_cndmask_b32_e64 v213, v213, v217, vcc\n" \
M_"v[148:151], a[152:155], a[176:179], v[148:151]\n" \
R_"a[212:215], v29 offset:2048\n" \
"v_lshlrev_b32_e64 v216, 7, v213\n" \
M_"v[148:151], a[216:219], a[240:243], v[148:151]\n" \
X_"a[170:171], v34 offset:1024\n" \
"v_mul_u32_u24_e32 v213, 0xe39, v212\n" \
M_"v[152:155], a[152:155], a[180:183], v[152:155]\n" \
X_"a[232:233], v34 offset:8192\n" \
"v_mov_b32_e64 v217, 0\n" \
M_"v[152:155], a[216:219], a[244:247], v[152:155]\n" \
X_"a[234:235], v34 offset:9216\n" \
"v_lshrrev_b32_e64 v213, 16, v213\n" \
M_"v[156:159], a[152:155], a[184:187], v[156:159]\n" \
X_"a[172:173], v35 offset:0\n" \
"v_lshl_add_u64 v[16:17], v[214:215], 0, v[216:217]\n" \
M_"v[156:159], a[216:219], a[248:251], v[156:159]\n" \
X_"a[174:175], v35 offset:1024\n" \
"v_mad_i32_i24 v214, v213, s6, v212\n" \
M_"v[160:163], a[152:155], a[188:191], v[160:163]\n" \
X_"a[236:237], v35 offset:8192\n" \
"s_add_i32 s5, s5, 32\n" \
M_"v[160:163], a[216:219], a[252:255], v[160:163]\n" \
R_"a[152:155], v28 offset:4096\n" \
"v_lshlrev_b32_e32 v215, 4, v214\n" \
M_"v[176:179], a[156:159], a[188:191], v[176:179]\n" \
R_"a[216:219], v29 offset:4096\n" \
"v_add_u32_e64 v216, s5, v215\n" \
M_"v[176:179], a[220:223], a[252:255], v[176:179]\n" \
X_"a[238:239], v35 offset:9216\n" \
"v_and_b32_e32 v216, 0xf0, v216\n" \
M_"v[172:175], a[156:159], a[184:187], v[172:175]\n" \
"v_cmp_gt_u32_e32 vcc, 16, v214\n" \
"s_movk_i32 s7, 0xf800\n" \
M_"v[172:175], a[220:223], a[248:251], v[172:175]\n" \
"v_cndmask_b32_e32 v214, v215, v216, vcc\n" \
"v_or_b32_e32 v215, 64, v212\n" \
M_"v[168:171], a[156:159], a[180:183], v[168:171]\n" \
"v_mul_u32_u24_e32 v216, 0xe39, v215\n" \
"v_lshrrev_b32_e64 v216, 16, v216\n" \
M_"v[168:171], a[220:223], a[244:247], v[168:171]\n" \
"v_mad_i32_i24 v215, v216, s6, v215\n" \
"v_lshlrev_b32_e64 v217, 4, v215\n" \
M_"v[164:167], a[156:159], a[176:179], v[164:167]\n" \
"v_add_u32_e64 v220, s5, v217\n" \
"v_and_b32_e32 v220, 0xf0, v220\n" \
M_"v[164:167], a[220:223], a[240:243], v[164:167]\n" \
R_"a[156:159], v28 offset:6144\n" \
R_"a[220:223], v29 offset:6144\n" \
"s_waitcnt lgkmcnt(15)\n" \
M_"a[0:3], a[128:131], a[160:163], a[0:3]\n" \
"s_addk_i32 s84, 0x7c00\n" \
M_"a[0:3], a[192:195], a[224:227], a[0:3]\n" \
B_"v27, s[72:75], s84 offen offset:2048 lds\n" \
M_"a[4:7], a[128:131], a[164:167], a[4:7]\n" \
X_"a[176:177], v36 offset:0\n" \
"v_cmp_gt_u32_e32 vcc, 16, v215\n" \
"s_waitcnt lgkmcnt(15)\n" \
M_"a[4:7], a[192:195], a[228:231], a[4:7]\n" \
"s_addk_i32 s84, 0x7c00\n" \
"s_waitcnt lgkmcnt(11)\n" \
M_"a[8:11], a[128:131], a[168:171], a[8:11]\n" \
B_"v27, s[72:75], s84 offen offset:3072 lds\n" \
"s_waitcnt lgkmcnt(9)\n" \
M_"a[8:11], a[192:195], a[232:235], a[8:11]\n" \
X_"a[178:179], v36 offset:1024\n" \
"v_cndmask_b32_e32 v215, v217, v220, vcc\n" \
"s_waitcnt lgkmcnt(8)\n" \
M_"a[12:15], a[128:131], a[172:175], a[12:15]\n" \
X_"a[240:241], v36 offset:8192\n" \
"v_or_b32_e32 v217, 0x80, v212\n" \
"s_waitcnt lgkmcnt(5)\n" \
M_"a[12:15], a[192:195], a[236:239], a[12:15]\n" \
"v_mul_u32_u24_e32 v220, 0xe39, v217\n" \
"v_lshrrev_b32_e64 v220, 16, v220\n" \
M_"a[28:31], a[132:135], a[172:175], a[28:31]\n" \
"v_mad_i32_i24 v217, v220, s6, v217\n" \
"v_lshlrev_b32_e64 v221, 4, v217\n" \
M_"a[28:31], a[196:199], a[236:239], a[28:31]\n" \
X_"a[242:243], v36 offset:9216\n" \
"v_add_u32_e64 v222, s5, v221\n" \
M_"a[24:27], a[132:135], a[168:171], a[24:27]\n" \
B_"v[200:203], v192, s[76:79], s34 offen offset:0\n" \
M_"a[24:27], a[196:199], a[232:235], a[24:27]\n" \
"v_and_b32_e32 v222, 0xf0, v222\n" \
"v_cmp_gt_u32_e64 vcc, 16, v217\n" \
M_"a[20:23], a[132:135], a[164:167], a[20:23]\n" \
"s_nop 0\n" \
"v_cndmask_b32_e32 v217, v221, v222, vcc\n" \
"v_or_b32_e32 v221, 0xc0, v212\n" \
M_"a[20:23], a[196:199], a[228:231], a[20:23]\n" \
X_"a[180:181], v37 offset:0\n" \
"v_mul_u32_u24_e32 v222, 0xe39, v221\n" \
M_"a[16:19], a[132:135], a[160:163], a[16:19]\n" \
"v_lshrrev_b32_e64 v222, 16, v222\n" \
"v_mad_i32_i24 v221, v222, s6, v221\n" \
M_"a[16:19], a[196:199], a[224:227], a[16:19]\n" \
"v_lshlrev_b32_e32 v223, 4, v221\n" \
"v_add_u32_e32 v224, s5, v223\n" \
M_"a[32:35], a[136:139], a[160:163], a[32:35]\n" \
B_"v[204:207], v193, s[76:79], s34 offen offset:0\n" \
M_"a[32:35], a[200:203], a[224:227], a[32:35]\n" \
"s_mov_b32 m0, s32\n" \
X_"a[182:183], v37 offset:1024\n" \
M_"a[36:39], a[136:139], a[164:167], a[36:39]\n" \
"s_mov_b32 exec_lo, 0x0\n" \
"s_mov_b32 exec_hi, 0xff00\n" \
"global_load_lds_dword v[184:185], off\n" \
"s_mov_b64 exec, -1\n" \
M_"a[36:39], a[200:203], a[228:231], a[36:39]\n" \
X_"a[244:245], v37 offset:8192\n" \
"v_and_b32_e32 v224, 0xf0, v224\n" \
M_"a[40:43], a[136:139], a[168:171], a[40:43]\n" \
"v_cmp_gt_u32_e32 vcc, 16, v221\n" \
"s_nop 1\n" \
"v_cndmask_b32_e32 v221, v223, v224, vcc\n" \
M_"a[40:43], a[200:203], a[232:235], a[40:43]\n" \
"buffer_load_dword v208, v194, s[76:79], s34 offen offset:32\n" \
M_"a[44:47], a[136:139], a[172:175], a[44:47]\n" \
X_"a[246:247], v37 offset:9216\n" \
"v_or_b32_e32 v223, 0x100, v212\n" \
M_"a[44:47], a[200:203], a[236:239], a[44:47]\n" \
"v_mul_u32_u24_e32 v224, 0xe39, v223\n" \
"v_lshrrev_b32_e64 v224, 16, v224\n" \
M_"a[60:63], a[140:143], a[172:175], a[60:63]\n" \
"v_mad_i32_i24 v223, v224, s6, v223\n" \
"v_lshlrev_b32_e64 v225, 4, v223\n" \
M_"a[60:63], a[204:207], a[236:239], a[60:63]\n" \
"v_add_u32_e64 v226, s5, v225\n" \
"v_and_b32_e32 v226, 0xf0, v226\n" \
M_"a[56:59], a[140:143], a[168:171], a[56:59]\n" \
"buffer_load_dword v209, v194, s[76:79], s34 offen offset:1184\n" \
M_"a[56:59], a[204:207], a[232:235], a[56:59]\n" \
X_"a[184:185], v38 offset:0\n" \
"v_cmp_gt_u32_e64 vcc, 16, v223\n" \
M_"a[52:55], a[140:143], a[164:167], a[52:55]\n" \
"s_nop 0\n" \
"v_cndmask_b32_e32 v223, v225, v226, vcc\n" \
"v_or_b32_e32 v225, 0x140, v212\n" \
M_"a[52:55], a[204:207], a[228:231], a[52:55]\n" \
X_"a[186:187], v38 offset:1024\n" \
"v_mul_u32_u24_e32 v226, 0xe39, v225\n" \
M_"a[48:51], a[140:143], a[160:163], a[48:51]\n" \
"v_lshrrev_b32_e64 v226, 16, v226\n" \
"v_mad_i32_i24 v225, v226, s6, v225\n" \
M_"a[48:51], a[204:207], a[224:227], a[48:51]\n" \
"v_lshlrev_b32_e32 v227, 4, v225\n" \
"v_add_u32_e32 v228, s5, v227\n" \
M_"a[64:67], a[144:147], a[160:163], a[64:67]\n" \
"buffer_load_dword v211, v194, s[76:79], s34 offen offset:2336\n" \
M_"a[64:67], a[208:211], a[224:227], a[64:67]\n" \
X_"a[248:249], v38 offset:8192\n" \
"v_and_b32_e32 v228, 0xf0, v228\n" \
M_"a[68:71], a[144:147], a[164:167], a[68:71]\n" \
"v_cmp_gt_u32_e32 vcc, 16, v225\n" \
"s_nop 1\n" \
"v_cndmask_b32_e64 v225, v227, v228, vcc\n" \
M_"a[68:71], a[208:211], a[228:231], a[68:71]\n" \
X_"a[250:251], v38 offset:9216\n" \
"v_or_b32_e32 v227, 0x180, v212\n" \
M_"a[72:75], a[144:147], a[168:171], a[72:75]\n" \
"v_mul_u32_u24_e32 v228, 0xe39, v227\n" \
"v_lshrrev_b32_e64 v228, 16, v228\n" \
M_"a[72:75], a[208:211], a[232:235], a[72:75]\n" \
"s_mov_b32 s76, s20\n" \
"s_and_b32 s77, s21, 0xffff\n" \
M_"a[76:79], a[144:147], a[172:175], a[76:79]\n" \
"s_mov_b32 s78, 0x480\n" \
"v_mad_i32_i24 v227, v228, s6, v227\n" \
M_"a[76:79], a[208:211], a[236:239], a[76:79]\n" \
"buffer_load_ushort v210, v195, s[76:79], 0 offen\n" \
M_"a[92:95], a[148:151], a[172:175], a[92:95]\n" \
X_"a[188:189], v39 offset:0\n" \
"v_lshlrev_b32_e32 v229, 4, v227\n" \
M_"a[92:95], a[212:215], a[236:239], a[92:95]\n" \
"v_add_u32_e64 v230, s5, v229\n" \
"v_and_b32_e32 v230, 0xf0, v230\n" \
M_"a[88:91], a[148:151], a[168:171], a[88:91]\n" \
"s_mov_b32 s76, s8\n" \
"s_and_b32 s77, s9, 0xffff\n" \
M_"a[88:91], a[212:215], a[232:235], a[88:91]\n" \
"s_mov_b32 s78, 0x240000\n" \
X_"a[190:191], v39 offset:1024\n" \
M_"a[84:87], a[148:151], a[164:167], a[84:87]\n" \
"v_cmp_gt_u32_e32 vcc, 16, v227\n" \
"s_nop 1\n" \
"v_cndmask_b32_e32 v227, v229, v230, vcc\n" \
M_"a[84:87], a[212:215], a[228:231], a[84:87]\n" \
X_"a[252:253], v39 offset:8192\n" \
"v_or_b32_e32 v229, 0x1c0, v212\n" \
".fill (L_pb + 48864 - .) / 4, 4, 0xBF800000\n" \
"L_pp12:\n" \
"s_cbranch_execz L_pret\n" \
M_"a[80:83], a[148:151], a[160:163], a[80:83]\n" \
"v_mul_u32_u24_e32 v230, 0xe39, v229\n" \
"v_lshrrev_b32_e32 v230, 16, v230\n" \
M_"a[80:83], a[212:215], a[224:227], a[80:83]\n" \
"v_mad_i32_i24 v229, v230, s6, v229\n" \
"v_lshlrev_b32_e64 v231, 4, v229\n" \
M_"a[96:99], a[152:155], a[160:163], a[96:99]\n" \
X_"a[254:255], v39 offset:9216\n" \
"v_add_u32_e64 v232, s5, v231\n" \
M_"a[96:99], a[216:219], a[224:227], a[96:99]\n" \
"v_and_b32_e32 v232, 0xf0, v232\n" \
"v_cmp_gt_u32_e64 vcc, 16, v229\n" \
M_"a[100:103], a[152:155], a[164:167], a[100:103]\n" \
"s_nop 0\n" \
"v_cndmask_b32_e32 v229, v231, v232, vcc\n" \
"v_or_b32_e32 v231, 0x200, v212\n" \
M_"a[100:103], a[216:219], a[228:231], a[100:103]\n" \
"v_mul_u32_u24_e32 v232, 0xe39, v231\n" \
"v_lshrrev_b32_e64 v232, 16, v232\n" \
M_"a[104:107], a[152:155], a[168:171], a[104:107]\n" \
"v_mad_i32_i24 v231, v232, s6, v231\n" \
"v_lshlrev_b32_e64 v233, 4, v231\n" \
M_"a[104:107], a[216:219], a[232:235], a[104:107]\n" \
"v_add_u32_e32 v234, s5, v233\n" \
"s_movk_i32 s5, 0x480\n" \
M_"a[108:111], a[152:155], a[172:175], a[108:111]\n" \
"v_mad_u32_u24 v0, v213, s5, v214\n" \
"v_mul_u32_u24_e32 v213, 0x480, v216\n" \
M_"a[108:111], a[216:219], a[236:239], a[108:111]\n" \
"s_movk_i32 s6, 0xfc00\n" \
"v_add3_u32 v1, v213, v215, s6\n" \
"s_waitcnt lgkmcnt(15)\n" \
M_"a[124:127], a[156:159], a[172:175], a[124:127]\n" \
"v_mul_u32_u24_e32 v213, 0x480, v220\n" \
"v_add3_u32 v2, v213, v217, s7\n" \
M_"a[124:127], a[220:223], a[236:239], a[124:127]\n" \
"v_mul_u32_u24_e32 v213, 0x480, v222\n" \
"v_add3_u32 v3, v213, v221, s40\n" \
M_"a[120:123], a[156:159], a[168:171], a[120:123]\n" \
"v_mul_u32_u24_e32 v213, 0x480, v226\n" \
"v_add3_u32 v5, v213, v225, s6\n" \
M_"a[120:123], a[220:223], a[232:235], a[120:123]\n" \
"v_mul_u32_u24_e32 v213, 0x480, v228\n" \
"v_add3_u32 v6, v213, v227, s7\n" \
M_"a[116:119], a[156:159], a[164:167], a[116:119]\n" \
"v_mul_u32_u24_e32 v213, 0x480, v230\n" \
"v_add3_u32 v7, v213, v229, s40\n" \
M_"a[116:119], a[220:223], a[228:231], a[116:119]\n" \
"s_lshl_b32 s7, s3, 3\n" \
"s_lshl_b32 s40, s3, 1\n" \
M_"a[112:115], a[156:159], a[160:163], a[112:115]\n" \
"s_and_b32 s7, s7, 16\n" \
"s_and_b32 s40, s40, 2\n" \
"s_waitcnt vmcnt(7) lgkmcnt(0)\n" \
M_"a[112:115], a[220:223], a[224:227], a[112:115]\n" \
"s_barrier\n" \
M_"v[52:55], a[128:131], a[176:179], v[52:55]\n" \
"s_add_u32 m0, s30, 0x0\n" \
"s_movk_i32 s84, 0x200\n" \
M_"v[52:55], a[192:195], a[240:243], v[52:55]\n" \
B_"v22, s[64:67], s84 offen offset:0 lds\n" \
M_"v[56:59], a[128:131], a[180:183], v[56:59]\n" \
X_"a[160:161], v32 offset:32768\n" \
"s_and_b32 s6, s2, 7\n" \
M_"v[56:59], a[192:195], a[244:247], v[56:59]\n" \
"s_movk_i32 s84, 0x1e00\n" \
M_"v[60:63], a[128:131], a[184:187], v[60:63]\n" \
B_"v23, s[64:67], s84 offen offset:1024 lds\n" \
M_"v[60:63], a[192:195], a[248:251], v[60:63]\n" \
X_"a[162:163], v32 offset:33792\n" \
"v_bfe_u32 v213, v20, 7, 1\n" \
M_"v[64:67], a[128:131], a[188:191], v[64:67]\n" \
X_"a[224:225], v32 offset:40960\n" \
"s_or_b32 s7, s40, s7\n" \
M_"v[64:67], a[192:195], a[252:255], v[64:67]\n" \
R_"a[128:131], v28 offset:49152\n" \
"v_and_b32_e32 v234, 0xf0, v234\n" \
M_"v[80:83], a[132:135], a[188:191], v[80:83]\n" \
R_"a[192:195], v29 offset:49152\n" \
"v_cmp_gt_u32_e32 vcc, 16, v231\n" \
M_"v[80:83], a[196:199], a[252:255], v[80:83]\n" \
"s_movk_i32 s84, 0x3a00\n" \
M_"v[76:79], a[132:135], a[184:187], v[76:79]\n" \
B_"v22, s[64:67], s84 offen offset:2048 lds\n" \
M_"v[76:79], a[196:199], a[248:251], v[76:79]\n" \
"s_movk_i32 s84, 0x5600\n" \
M_"v[72:75], a[132:135], a[180:183], v[72:75]\n" \
B_"v23, s[64:67], s84 offen offset:3072 lds\n" \
M_"v[72:75], a[196:199], a[244:247], v[72:75]\n" \
"s_add_u32 m0, s31, 0x0\n" \
"s_mov_b32 s84, 0x200000\n" \
M_"v[68:71], a[132:135], a[176:179], v[68:71]\n" \
B_"v26, s[68:71], s84 offen offset:0 lds\n" \
M_"v[68:71], a[196:199], a[240:243], v[68:71]\n" \
R_"a[132:135], v28 offset:51200\n" \
"v_or3_b32 v213, s7, v213, v219\n" \
M_"v[84:87], a[136:139], a[176:179], v[84:87]\n" \
R_"a[196:199], v29 offset:51200\n" \
"v_and_b32_e32 v214, 32, v20\n" \
M_"v[84:87], a[200:203], a[240:243], v[84:87]\n" \
"s_addk_i32 s84, 0x7c00\n" \
M_"v[88:91], a[136:139], a[180:183], v[88:91]\n" \
B_"v26, s[68:71], s84 offen offset:1024 lds\n" \
M_"v[88:91], a[200:203], a[244:247], v[88:91]\n" \
X_"a[226:227], v32 offset:41984\n" \
"s_lshl_b32 s6, s6, 5\n" \
M_"v[92:95], a[136:139], a[184:187], v[92:95]\n" \
"s_addk_i32 s84, 0x7c00\n" \
M_"v[92:95], a[200:203], a[248:251], v[92:95]\n" \
B_"v27, s[68:71], s84 offen offset:2048 lds\n" \
M_"v[96:99], a[136:139], a[188:191], v[96:99]\n" \
X_"a[164:165], v33 offset:32768\n" \
"v_cndmask_b32_e32 v231, v233, v234, vcc\n" \
M_"v[96:99], a[200:203], a[252:255], v[96:99]\n" \
R_"a[136:139], v28 offset:53248\n" \
"v_mul_u32_u24_e64 v213, 18, v213\n" \
M_"v[112:115], a[140:143], a[188:191], v[112:115]\n" \
R_"a[200:203], v29 offset:53248\n" \
"v_mov_b32_e32 v215, 0x100\n" \
M_"v[112:115], a[204:207], a[252:255], v[112:115]\n" \
"s_addk_i32 s84, 0x7c00\n" \
M_"v[108:111], a[140:143], a[184:187], v[108:111]\n" \
B_"v27, s[68:71], s84 offen offset:3072 lds\n" \
M_"v[108:111], a[204:207], a[248:251], v[108:111]\n" \
"s_add_u32 m0, s30, 0x1000\n" \
"s_mov_b32 s84, 0x8200\n" \
M_"v[104:107], a[140:143], a[180:183], v[104:107]\n" \
B_"v22, s[64:67], s84 offen offset:0 lds\n" \
M_"v[104:107], a[204:207], a[244:247], v[104:107]\n" \
X_"a[166:167], v33 offset:33792\n" \
"v_mov_b32_e32 v216, s6\n" \
M_"v[100:103], a[140:143], a[176:179], v[100:103]\n" \
X_"a[228:229], v33 offset:40960\n" \
"v_cmp_eq_u32_e64 vcc, 0, v214\n" \
M_"v[100:103], a[204:207], a[240:243], v[100:103]\n" \
R_"a[140:143], v28 offset:55296\n" \
"v_and_or_b32 v213, v218, 1, v213\n" \
M_"v[116:119], a[144:147], a[176:179], v[116:119]\n" \
R_"a[204:207], v29 offset:55296\n" \
"v_lshlrev_b32_e64 v212, 3, v212\n" \
M_"v[116:119], a[208:211], a[240:243], v[116:119]\n" \
"s_addk_i32 s84, 0x1c00\n" \
M_"v[120:123], a[144:147], a[180:183], v[120:123]\n" \
B_"v23, s[64:67], s84 offen offset:1024 lds\n" \
M_"v[120:123], a[208:211], a[244:247], v[120:123]\n" \
"s_addk_i32 s84, 0x1c00\n" \
M_"v[124:127], a[144:147], a[184:187], v[124:127]\n" \
B_"v22, s[64:67], s84 offen offset:2048 lds\n" \
M_"v[124:127], a[208:211], a[248:251], v[124:127]\n" \
"s_addk_i32 s84, 0x1c00\n" \
M_"v[128:131], a[144:147], a[188:191], v[128:131]\n" \
B_"v23, s[64:67], s84 offen offset:3072 lds\n" \
M_"v[128:131], a[208:211], a[252:255], v[128:131]\n" \
R_"a[144:147], v28 offset:32768\n" \
"v_cndmask_b32_e32 v214, v215, v216, vcc\n" \
M_"v[144:147], a[148:151], a[188:191], v[144:147]\n" \
R_"a[208:211], v29 offset:32768\n" \
"v_mad_u32_u24 v213, v213, s5, v214\n" \
M_"v[144:147], a[212:215], a[252:255], v[144:147]\n" \
"s_add_u32 m0, s31, 0x4000\n" \
"s_mov_b32 s84, 0x200000\n" \
M_"v[140:143], a[148:151], a[184:187], v[140:143]\n" \
B_"v26, s[72:75], s84 offen offset:0 lds\n" \
M_"v[140:143], a[212:215], a[248:251], v[140:143]\n" \
X_"a[230:231], v33 offset:41984\n" \
"v_and_or_b32 v213, v20, 16, v213\n" \
M_"v[136:139], a[148:151], a[180:183], v[136:139]\n" \
"s_addk_i32 s84, 0x7c00\n" \
"s_nop 0\n" \
M_"v[136:139], a[212:215], a[244:247], v[136:139]\n" \
B_"v26, s[72:75], s84 offen offset:1024 lds\n" \
M_"v[132:135], a[148:151], a[176:179], v[132:135]\n" \
X_"a[168:169], v34 offset:32768\n" \
"v_add_u32_e32 v9, 0x4800, v213\n" \
M_"v[132:135], a[212:215], a[240:243], v[132:135]\n" \
R_"a[148:151], v28 offset:34816\n" \
"v_mul_u32_u24_e32 v213, 0x120, v218\n" \
M_"v[148:151], a[152:155], a[176:179], v[148:151]\n" \
R_"a[212:215], v29 offset:34816\n" \
"v_and_or_b32 v10, v212, 24, v213\n" \
M_"v[148:151], a[216:219], a[240:243], v[148:151]\n" \
X_"a[170:171], v34 offset:33792\n" \
"v_lshrrev_b32_e64 v213, 2, v20\n" \
M_"v[152:155], a[152:155], a[180:183], v[152:155]\n" \
X_"a[232:233], v34 offset:40960\n" \
"v_and_or_b32 v213, v213, 60, s4\n" \
M_"v[152:155], a[216:219], a[244:247], v[152:155]\n" \
X_"a[234:235], v34 offset:41984\n" \
"v_lshl_or_b32 v212, s3, 9, v212\n" \
M_"v[156:159], a[152:155], a[184:187], v[156:159]\n" \
X_"a[172:173], v35 offset:32768\n" \
"v_mad_u32_u24 v4, v224, s5, v223\n" \
M_"v[156:159], a[216:219], a[248:251], v[156:159]\n" \
X_"a[174:175], v35 offset:33792\n" \
"v_mad_u32_u24 v8, v232, s5, v231\n" \
M_"v[160:163], a[152:155], a[188:191], v[160:163]\n" \
X_"a[236:237], v35 offset:40960\n" \
"v_or_b32_e32 v18, 0x27000, v213\n" \
M_"v[160:163], a[216:219], a[252:255], v[160:163]\n" \
R_"a[152:155], v28 offset:36864\n" \
"v_or_b32_e32 v19, 0x27800, v212\n" \
M_"v[176:179], a[156:159], a[188:191], v[176:179]\n" \
R_"a[216:219], v29 offset:36864\n" \
"v_mov_b32_e32 v215, 0x1000c0c\n" \
M_"v[176:179], a[220:223], a[252:255], v[176:179]\n" \
X_"a[238:239], v35 offset:41984\n" \
"v_mov_b32_e32 v216, 0xc0c0100\n" \
M_"v[172:175], a[156:159], a[184:187], v[172:175]\n" \
"v_bfe_u32 v214, v20, 4, 1\n" \
"v_lshrrev_b32_e64 v212, 8, v20\n" \
M_"v[172:175], a[220:223], a[248:251], v[172:175]\n" \
"v_cmp_eq_u32_e64 vcc, 0, v214\n" \
"v_bfe_u32 v213, v20, 4, 4\n" \
M_"v[168:171], a[156:159], a[180:183], v[168:171]\n" \
"v_mov_b32_e32 v217, 0xc0c0302\n" \
"v_cndmask_b32_e64 v215, v215, v216, vcc\n" \
M_"v[168:171], a[220:223], a[244:247], v[168:171]\n" \
"v_mov_b32_e32 v216, 0x3020c0c\n" \
"v_lshl_or_b32 v214, v212, 2, v214\n" \
M_"v[164:167], a[156:159], a[176:179], v[164:167]\n" \
"v_cndmask_b32_e64 v216, v216, v217, vcc\n" \
"v_mov_b32_e32 v217, 0xc0c0c0c\n" \
M_"v[164:167], a[220:223], a[240:243], v[164:167]\n" \
R_"a[156:159], v28 offset:38912\n" \
R_"a[220:223], v29 offset:38912\n" \
"s_waitcnt lgkmcnt(15)\n" \
M_"a[0:3], a[128:131], a[160:163], a[0:3]\n" \
"s_addk_i32 s84, 0x7c00\n" \
M_"a[0:3], a[192:195], a[224:227], a[0:3]\n" \
B_"v27, s[72:75], s84 offen offset:2048 lds\n" \
M_"a[4:7], a[128:131], a[164:167], a[4:7]\n" \
X_"a[176:177], v36 offset:32768\n" \
"v_cmp_gt_u32_e32 vcc, v214, v213\n" \
"s_waitcnt lgkmcnt(15)\n" \
M_"a[4:7], a[192:195], a[228:231], a[4:7]\n" \
"s_addk_i32 s84, 0x7c00\n" \
"s_waitcnt lgkmcnt(11)\n" \
M_"a[8:11], a[128:131], a[168:171], a[8:11]\n" \
B_"v27, s[72:75], s84 offen offset:3072 lds\n" \
"s_waitcnt lgkmcnt(9)\n" \
M_"a[8:11], a[192:195], a[232:235], a[8:11]\n" \
X_"a[178:179], v36 offset:33792\n" \
"v_or_b32_e32 v214, 2, v214\n" \
"s_waitcnt lgkmcnt(8)\n" \
M_"a[12:15], a[128:131], a[172:175], a[12:15]\n" \
X_"a[240:241], v36 offset:40960\n" \
"s_lshr_b32 s4, s2, 5\n" \
"s_waitcnt lgkmcnt(5)\n" \
M_"a[12:15], a[192:195], a[236:239], a[12:15]\n" \
"v_cndmask_b32_e32 v40, v217, v215, vcc\n" \
"v_cndmask_b32_e64 v42, v215, v217, vcc\n" \
M_"a[28:31], a[132:135], a[172:175], a[28:31]\n" \
"v_cmp_gt_u32_e32 vcc, v214, v213\n" \
"v_and_b32_e32 v214, 16, v20\n" \
M_"a[28:31], a[196:199], a[236:239], a[28:31]\n" \
X_"a[242:243], v36 offset:41984\n" \
"s_movk_i32 s42, 0x3f80\n" \
M_"a[24:27], a[132:135], a[168:171], a[24:27]\n" \
"s_xor_b32 s5, s4, 15\n" \
M_"a[24:27], a[196:199], a[232:235], a[24:27]\n" \
"s_cmpk_lt_u32 s3, 2\n" \
"s_cselect_b32 s6, s5, s4\n" \
M_"a[20:23], a[132:135], a[164:167], a[20:23]\n" \
"s_lshl_b32 s7, s6, 7\n" \
"v_and_b32_e32 v213, 0x320, v20\n" \
M_"a[20:23], a[196:199], a[228:231], a[20:23]\n" \
X_"a[180:181], v37 offset:32768\n" \
"s_movk_i32 s41, 0x100\n" \
M_"a[16:19], a[132:135], a[160:163], a[16:19]\n" \
"v_lshlrev_b32_e64 v214, v214, s42\n" \
"v_mov_b32_e64 v215, 0\n" \
M_"a[16:19], a[196:199], a[224:227], a[16:19]\n" \
"s_add_i32 s40, s7, -16\n" \
"v_cndmask_b32_e32 v41, v217, v216, vcc\n" \
M_"a[32:35], a[136:139], a[160:163], a[32:35]\n" \
X_"a[182:183], v37 offset:33792\n" \
"v_cndmask_b32_e64 v43, v216, v217, vcc\n" \
M_"a[32:35], a[200:203], a[224:227], a[32:35]\n" \
"v_cmp_eq_u32_e32 vcc, s41, v213\n" \
"s_mov_b32 s41, 0\n" \
M_"a[36:39], a[136:139], a[164:167], a[36:39]\n" \
"s_and_b32 s43, s3, 1\n" \
"v_cndmask_b32_e32 v46, v215, v214, vcc\n" \
M_"a[36:39], a[200:203], a[228:231], a[36:39]\n" \
X_"a[244:245], v37 offset:40960\n" \
"v_and_b32_e32 v213, 0x330, v20\n" \
M_"a[40:43], a[136:139], a[168:171], a[40:43]\n" \
"s_mov_b32 m0, s32\n" \
M_"a[40:43], a[200:203], a[232:235], a[40:43]\n" \
"s_mov_b32 exec_lo, 0xffffffff\n" \
"s_mov_b32 exec_hi, 0x0\n" \
"global_load_lds_dword v[188:189], off\n" \
"s_mov_b64 exec, -1\n" \
M_"a[44:47], a[136:139], a[172:175], a[44:47]\n" \
X_"a[246:247], v37 offset:41984\n" \
M_"a[44:47], a[200:203], a[236:239], a[44:47]\n" \
"s_cmp_lg_u32 s7, 0\n" \
"s_cselect_b32 s40, s40, s41\n" \
M_"a[60:63], a[140:143], a[172:175], a[60:63]\n" \
"s_mov_b32 s42, 0xff800000\n" \
M_"a[60:63], a[204:207], a[236:239], a[60:63]\n" \
"s_cmp_lg_u32 s7, 0\n" \
"s_cselect_b32 s47, s41, s42\n" \
M_"a[56:59], a[140:143], a[168:171], a[56:59]\n" \
X_"a[184:185], v38 offset:32768\n" \
"s_lshl_b32 s41, s43, 6\n" \
M_"a[56:59], a[204:207], a[232:235], a[56:59]\n" \
"v_lshrrev_b32_e32 v215, 6, v20\n" \
"v_mov_b32_e32 v214, 0x3f80\n" \
M_"a[52:55], a[140:143], a[164:167], a[52:55]\n" \
"v_cmp_eq_u32_e32 vcc, 32, v213\n" \
"s_or_b32 s7, s41, s7\n" \
M_"a[52:55], a[204:207], a[228:231], a[52:55]\n" \
X_"a[186:187], v38 offset:33792\n" \
"v_lshrrev_b32_e64 v213, 7, v20\n" \
M_"a[48:51], a[140:143], a[160:163], a[48:51]\n" \
"v_bfe_u32 v216, v20, 6, 2\n" \
"v_lshrrev_b32_e64 v219, 1, v20\n" \
M_"a[48:51], a[204:207], a[224:227], a[48:51]\n" \
"v_bfe_u32 v215, v215, 1, 1\n" \
"v_cndmask_b32_e64 v47, 0, v214, vcc\n" \
M_"a[64:67], a[144:147], a[160:163], a[64:67]\n" \
X_"a[248:249], v38 offset:40960\n" \
"v_or_b32_e64 v214, s7, v213\n" \
M_"a[64:67], a[208:211], a[224:227], a[64:67]\n" \
"v_lshl_or_b32 v217, s43, 3, v213\n" \
"v_lshlrev_b32_e64 v218, 10, v212\n" \
M_"a[68:71], a[144:147], a[164:167], a[68:71]\n" \
"v_lshlrev_b32_e32 v216, 7, v216\n" \
"v_and_b32_e32 v219, 24, v219\n" \
M_"a[68:71], a[208:211], a[228:231], a[68:71]\n" \
X_"a[250:251], v38 offset:41984\n" \
"v_and_or_b32 v213, v213, 2, v215\n" \
M_"a[72:75], a[144:147], a[168:171], a[72:75]\n" \
"v_add_u32_e64 v217, s40, v217\n" \
"v_or3_b32 v216, v216, v218, v219\n" \
M_"a[72:75], a[208:211], a[232:235], a[72:75]\n" \
"v_lshlrev_b32_e64 v213, 5, v213\n" \
"s_mov_b32 s40, 0x14000\n" \
M_"a[76:79], a[144:147], a[172:175], a[76:79]\n" \
"s_lshl_b32 s7, s2, 2\n" \
"v_or3_b32 v48, v216, v213, s40\n" \
M_"a[76:79], a[208:211], a[236:239], a[76:79]\n" \
X_"a[188:189], v39 offset:32768\n" \
"s_lshl_b32 s40, s43, 2\n" \
M_"a[92:95], a[148:151], a[172:175], a[92:95]\n" \
"s_lshl_b32 s6, s6, 3\n" \
"v_or_b32_e32 v213, s40, v212\n" \
M_"a[92:95], a[212:215], a[236:239], a[92:95]\n" \
"s_or_b32 s6, s6, s40\n" \
"s_and_b32 s40, s7, 0x7c\n" \
M_"a[88:91], a[148:151], a[168:171], a[88:91]\n" \
X_"a[190:191], v39 offset:33792\n" \
"s_or_b32 s40, s40, s3\n" \
M_"a[88:91], a[212:215], a[232:235], a[88:91]\n" \
"s_cmpk_lt_u32 s40, 64\n" \
"s_cselect_b32 s41, s4, s5\n" \
M_"a[84:87], a[148:151], a[164:167], a[84:87]\n" \
"s_waitcnt vmcnt(17)\n" \
"v_or_b32_e32 v204, v210, v204\n" \
"v_or_b32_e64 v206, v210, v206\n" \
M_"a[84:87], a[212:215], a[228:231], a[84:87]\n" \
"v_mov_b32_e64 v210, 0\n" \
"s_add_u32 s88, s35, 0x1000\n" \
M_"a[80:83], a[148:151], a[160:163], a[80:83]\n" \
X_"a[252:253], v39 offset:40960\n" \
"s_lshl_b32 s40, s40, 1\n" \
M_"a[80:83], a[212:215], a[224:227], a[80:83]\n" \
"ds_write_b128 v196, v[200:203]\n" \
M_"a[96:99], a[152:155], a[160:163], a[96:99]\n" \
X_"a[254:255], v39 offset:41984\n" \
"s_lshl_b32 s41, s41, 7\n" \
M_"a[96:99], a[216:219], a[224:227], a[96:99]\n" \
"s_and_b32 s40, s40, 0x7e\n" \
"s_or_b32 s40, s41, s40\n" \
M_"a[100:103], a[152:155], a[164:167], a[100:103]\n" \
"ds_write_b128 v197, v[204:207]\n" \
M_"a[100:103], a[216:219], a[228:231], a[100:103]\n" \
"s_mulk_i32 s40, 0x480\n" \
"v_add_u32_e64 v49, s40, v20\n" \
M_"a[104:107], a[152:155], a[168:171], a[104:107]\n" \
"s_mov_b32 s40, 0xc000\n" \
"s_and_b32 s7, s7, 28\n" \
M_"a[104:107], a[216:219], a[232:235], a[104:107]\n" \
"ds_write_b128 v198, v[208:211]\n" \
M_"a[108:111], a[152:155], a[172:175], a[108:111]\n" \
"s_bfe_u32 s45, s2, 0x20003\n" \
"s_mov_b32 s41, 0x8000\n" \
M_"a[108:111], a[216:219], a[236:239], a[108:111]\n" \
"s_cmp_lg_u32 s4, 0\n" \
"s_cselect_b32 s51, s40, s41\n" \
"s_waitcnt lgkmcnt(15)\n" \
M_"a[124:127], a[156:159], a[172:175], a[124:127]\n" \
"s_movk_i32 s40, 0x4000\n" \
"s_lshr_b32 s44, s3, 1\n" \
M_"a[124:127], a[220:223], a[236:239], a[124:127]\n" \
"s_or_b32 s46, s7, s45\n" \
M_"a[120:123], a[156:159], a[168:171], a[120:123]\n" \
"s_cmp_lg_u32 s4, 0\n" \
"s_cselect_b32 s53, s41, s40\n" \
M_"a[120:123], a[220:223], a[232:235], a[120:123]\n" \
"s_lshl_b32 s40, s44, 15\n" \
"s_lshl_b32 s41, s43, 14\n" \
M_"a[116:119], a[156:159], a[164:167], a[116:119]\n" \
"s_lshl_b32 s48, s46, 20\n" \
"s_or_b32 s41, s40, s41\n" \
M_"a[116:119], a[220:223], a[228:231], a[116:119]\n" \
"s_add_i32 s49, s48, 0x1000000\n" \
"s_or_b32 s42, s49, s41\n" \
M_"a[112:115], a[156:159], a[160:163], a[112:115]\n" \
"s_lshl_b32 s4, s4, 16\n" \
"s_or_b32 s41, s42, s4\n" \
"s_waitcnt vmcnt(1) lgkmcnt(0)\n" \
M_"a[112:115], a[220:223], a[224:227], a[112:115]\n" \
"s_barrier\n" \
M_"v[52:55], a[128:131], a[176:179], v[52:55]\n" \
"s_add_u32 m0, s30, 0x8000\n" \
"s_movk_i32 s84, 0x280\n" \
M_"v[52:55], a[192:195], a[240:243], v[52:55]\n" \
B_"v22, s[64:67], s84 offen offset:0 lds\n" \
M_"v[56:59], a[128:131], a[180:183], v[56:59]\n" \
"s_movk_i32 s84, 0x1e80\n" \
M_"v[56:59], a[192:195], a[244:247], v[56:59]\n" \
B_"v23, s[64:67], s84 offen offset:1024 lds\n" \
M_"v[60:63], a[128:131], a[184:187], v[60:63]\n" \
X_"a[160:161], v32 offset:0\n" \
"s_lshl_b32 s4, s5, 16\n" \
M_"v[60:63], a[192:195], a[248:251], v[60:63]\n" \
"s_movk_i32 s84, 0x3a80\n" \
M_"v[64:67], a[128:131], a[188:191], v[64:67]\n" \
B_"v22, s[64:67], s84 offen offset:2048 lds\n" \
M_"v[64:67], a[192:195], a[252:255], v[64:67]\n" \
R_"a[128:131], v28 offset:16384\n" \
"s_mul_i32 s5, s6, 0x24800\n" \
M_"v[80:83], a[132:135], a[188:191], v[80:83]\n" \
R_"a[192:195], v29 offset:16384\n" \
"s_lshl_b32 s6, s46, 9\n" \
M_"v[80:83], a[196:199], a[252:255], v[80:83]\n" \
"s_movk_i32 s84, 0x5680\n" \
M_"v[76:79], a[132:135], a[184:187], v[76:79]\n" \
B_"v23, s[64:67], s84 offen offset:3072 lds\n" \
M_"v[76:79], a[196:199], a[248:251], v[76:79]\n" \
"s_add_u32 m0, s31, 0x8000\n" \
"s_mov_b32 s84, 0x280000\n" \
M_"v[72:75], a[132:135], a[180:183], v[72:75]\n" \
B_"v26, s[68:71], s84 offen offset:0 lds\n" \
M_"v[72:75], a[196:199], a[244:247], v[72:75]\n" \
X_"a[162:163], v32 offset:1024\n" \
"s_lshl_b32 s7, s7, 6\n" \
M_"v[68:71], a[132:135], a[176:179], v[68:71]\n" \
X_"a[224:225], v32 offset:8192\n" \
"s_add_i32 s6, s5, s6\n" \
M_"v[68:71], a[196:199], a[240:243], v[68:71]\n" \
R_"a[132:135], v28 offset:18432\n" \
"s_or_b32 s5, s5, s7\n" \
M_"v[84:87], a[136:139], a[176:179], v[84:87]\n" \
R_"a[196:199], v29 offset:18432\n" \
"s_lshl_b32 s7, s45, 2\n" \
M_"v[84:87], a[200:203], a[240:243], v[84:87]\n" \
"s_addk_i32 s84, 0x7c00\n" \
M_"v[88:91], a[136:139], a[180:183], v[88:91]\n" \
B_"v26, s[68:71], s84 offen offset:1024 lds\n" \
M_"v[88:91], a[200:203], a[244:247], v[88:91]\n" \
"s_addk_i32 s84, 0x7c00\n" \
M_"v[92:95], a[136:139], a[184:187], v[92:95]\n" \
B_"v27, s[68:71], s84 offen offset:2048 lds\n" \
M_"v[92:95], a[200:203], a[248:251], v[92:95]\n" \
"s_addk_i32 s84, 0x7c00\n" \
M_"v[96:99], a[136:139], a[188:191], v[96:99]\n" \
B_"v27, s[68:71], s84 offen offset:3072 lds\n" \
M_"v[96:99], a[200:203], a[252:255], v[96:99]\n" \
R_"a[136:139], v28 offset:20480\n" \
"s_or_b32 s5, s5, s7\n" \
M_"v[112:115], a[140:143], a[188:191], v[112:115]\n" \
R_"a[200:203], v29 offset:20480\n" \
"s_lshl_b32 s7, s3, 12\n" \
M_"v[112:115], a[204:207], a[252:255], v[112:115]\n" \
"s_add_u32 m0, s30, 0x9000\n" \
"s_mov_b32 s84, 0x8280\n" \
M_"v[108:111], a[140:143], a[184:187], v[108:111]\n" \
B_"v22, s[64:67], s84 offen offset:0 lds\n" \
M_"v[108:111], a[204:207], a[248:251], v[108:111]\n" \
"s_addk_i32 s84, 0x1c00\n" \
M_"v[104:107], a[140:143], a[180:183], v[104:107]\n" \
B_"v23, s[64:67], s84 offen offset:1024 lds\n" \
M_"v[104:107], a[204:207], a[244:247], v[104:107]\n" \
"s_addk_i32 s84, 0x1c00\n" \
M_"v[100:103], a[140:143], a[176:179], v[100:103]\n" \
B_"v22, s[64:67], s84 offen offset:2048 lds\n" \
M_"v[100:103], a[204:207], a[240:243], v[100:103]\n" \
R_"a[140:143], v28 offset:22528\n" \
"s_or_b32 s50, s49, s7\n" \
M_"v[116:119], a[144:147], a[176:179], v[116:119]\n" \
R_"a[204:207], v29 offset:22528\n" \
"s_lshl_b32 s7, s3, 11\n" \
M_"v[116:119], a[208:211], a[240:243], v[116:119]\n" \
"s_addk_i32 s84, 0x1c00\n" \
M_"v[120:123], a[144:147], a[180:183], v[120:123]\n" \
B_"v23, s[64:67], s84 offen offset:3072 lds\n" \
M_"v[120:123], a[208:211], a[244:247], v[120:123]\n" \
"s_add_u32 m0, s31, 0xc000\n" \
"s_mov_b32 s84, 0x280000\n" \
M_"v[124:127], a[144:147], a[184:187], v[124:127]\n" \
B_"v26, s[72:75], s84 offen offset:0 lds\n" \
M_"v[124:127], a[208:211], a[248:251], v[124:127]\n" \
"s_addk_i32 s84, 0x7c00\n" \
M_"v[128:131], a[144:147], a[188:191], v[128:131]\n" \
B_"v26, s[72:75], s84 offen offset:1024 lds\n" \
M_"v[128:131], a[208:211], a[252:255], v[128:131]\n" \
R_"a[144:147], v28 offset:0\n" \
"v_lshrrev_b32_e32 v215, 4, v20\n" \
M_"v[144:147], a[148:151], a[188:191], v[144:147]\n" \
R_"a[208:211], v29 offset:0\n" \
"v_bfe_u32 v216, v20, 4, 3\n" \
M_"v[144:147], a[212:215], a[252:255], v[144:147]\n" \
R_"v[200:203], v199 offset:0\n" \
"s_or_b32 s42, s42, s4\n" \
M_"v[140:143], a[148:151], a[184:187], v[140:143]\n" \
R_"v[204:207], v199 offset:4096\n" \
"s_lshl_b32 s4, s43, 11\n" \
M_"v[140:143], a[212:215], a[248:251], v[140:143]\n" \
X_"a[226:227], v32 offset:9216\n" \
"s_add_i32 s52, s48, s7\n" \
M_"v[136:139], a[148:151], a[180:183], v[136:139]\n" \
X_"a[164:165], v33 offset:0\n" \
"s_add_i32 s53, s53, s7\n" \
M_"v[136:139], a[212:215], a[244:247], v[136:139]\n" \
R_"v[208:211], v199 offset:8192\n" \
"v_mul_u32_u24_e32 v214, 0x480, v214\n" \
M_"v[132:135], a[148:151], a[176:179], v[132:135]\n" \
X_"a[166:167], v33 offset:1024\n" \
"v_bitop3_b32 v218, v212, v215, 7 bitop3:0x78\n" \
M_"v[132:135], a[212:215], a[240:243], v[132:135]\n" \
R_"a[148:151], v28 offset:2048\n" \
"v_bitop3_b32 v212, v212, v216, 4 bitop3:0x36\n" \
M_"v[148:151], a[152:155], a[176:179], v[148:151]\n" \
R_"a[212:215], v29 offset:2048\n" \
"s_lshl_b32 s7, s3, 10\n" \
M_"v[148:151], a[216:219], a[240:243], v[148:151]\n" \
X_"a[228:229], v33 offset:8192\n" \
"s_or_b32 s4, s40, s4\n" \
M_"v[152:155], a[152:155], a[180:183], v[152:155]\n" \
X_"a[230:231], v33 offset:9216\n" \
"v_lshl_or_b32 v25, v212, 4, v214\n" \
M_"v[152:155], a[216:219], a[244:247], v[152:155]\n" \
X_"a[168:169], v34 offset:0\n" \
"v_mul_u32_u24_e32 v212, 0x480, v217\n" \
M_"v[156:159], a[152:155], a[184:187], v[156:159]\n" \
X_"a[170:171], v34 offset:1024\n" \
"v_bitop3_b32 v213, v215, v213, 7 bitop3:0x6c\n" \
M_"v[156:159], a[216:219], a[248:251], v[156:159]\n" \
X_"a[232:233], v34 offset:8192\n" \
"s_or_b32 s40, s7, 0x14000\n" \
M_"v[160:163], a[152:155], a[188:191], v[160:163]\n" \
X_"a[234:235], v34 offset:9216\n" \
"s_lshl_b32 s43, s44, 13\n" \
M_"v[160:163], a[216:219], a[252:255], v[160:163]\n" \
R_"a[152:155], v28 offset:4096\n" \
"s_mulk_i32 s44, 0xe800\n" \
M_"v[176:179], a[156:159], a[188:191], v[176:179]\n" \
R_"a[216:219], v29 offset:4096\n" \
"s_lshl_b32 s7, s3, 8\n" \
"s_nop 0\n" \
M_"v[176:179], a[220:223], a[252:255], v[176:179]\n" \
X_"a[172:173], v35 offset:0\n" \
"v_lshl_or_b32 v24, v218, 4, v214\n" \
M_"v[172:175], a[156:159], a[184:187], v[172:175]\n" \
X_"a[174:175], v35 offset:1024\n" \
"v_lshl_or_b32 v21, v213, 4, v212\n" \
M_"v[172:175], a[220:223], a[248:251], v[172:175]\n" \
X_"a[236:237], v35 offset:8192\n" \
"s_add_i32 s44, s44, 0x17000\n" \
M_"v[168:171], a[156:159], a[180:183], v[168:171]\n" \
X_"a[238:239], v35 offset:9216\n" \
"s_or_b32 s45, s7, 0x27000\n" \
M_"v[168:171], a[220:223], a[244:247], v[168:171]\n" \
"s_or_b32 s46, s4, 0x10000\n" \
"s_add_i32 s48, s6, 0x3000000\n" \
M_"v[164:167], a[156:159], a[176:179], v[164:167]\n" \
"s_add_i32 s49, s5, 0x3004000\n" \
"s_add_i32 s52, s52, 0x1010000\n" \
".fill (L_pb + 52960 - .) / 4, 4, 0xBF800000\n" \
"L_pp13:\n" \
"s_cbranch_execz L_pret\n" \
M_"v[164:167], a[220:223], a[240:243], v[164:167]\n" \
R_"a[156:159], v28 offset:6144\n" \
R_"a[220:223], v29 offset:6144\n" \
"s_waitcnt lgkmcnt(15)\n" \
M_"a[0:3], a[128:131], a[160:163], a[0:3]\n" \
"s_addk_i32 s84, 0x7c00\n" \
M_"a[0:3], a[192:195], a[224:227], a[0:3]\n" \
B_"v27, s[72:75], s84 offen offset:2048 lds\n" \
M_"a[4:7], a[128:131], a[164:167], a[4:7]\n" \
X_"a[176:177], v36 offset:0\n" \
"s_waitcnt lgkmcnt(13)\n" \
M_"a[4:7], a[192:195], a[228:231], a[4:7]\n" \
"s_addk_i32 s84, 0x7c00\n" \
"s_waitcnt lgkmcnt(11)\n" \
M_"a[8:11], a[128:131], a[168:171], a[8:11]\n" \
B_"v27, s[72:75], s84 offen offset:3072 lds\n" \
"s_waitcnt lgkmcnt(9)\n" \
M_"a[8:11], a[192:195], a[232:235], a[8:11]\n" \
X_"a[178:179], v36 offset:1024\n" \
"s_waitcnt lgkmcnt(6)\n" \
M_"a[12:15], a[128:131], a[172:175], a[12:15]\n" \
X_"a[240:241], v36 offset:8192\n" \
"s_waitcnt lgkmcnt(5)\n" \
"s_nop 0\n" \
M_"a[12:15], a[192:195], a[236:239], a[12:15]\n" \
M_"a[28:31], a[132:135], a[172:175], a[28:31]\n" \
M_"a[28:31], a[196:199], a[236:239], a[28:31]\n" \
X_"a[242:243], v36 offset:9216\n" \
M_"a[24:27], a[132:135], a[168:171], a[24:27]\n" \
M_"a[24:27], a[196:199], a[232:235], a[24:27]\n" \
M_"a[20:23], a[132:135], a[164:167], a[20:23]\n" \
M_"a[20:23], a[196:199], a[228:231], a[20:23]\n" \
X_"a[180:181], v37 offset:0\n" \
M_"a[16:19], a[132:135], a[160:163], a[16:19]\n" \
M_"a[16:19], a[196:199], a[224:227], a[16:19]\n" \
M_"a[32:35], a[136:139], a[160:163], a[32:35]\n" \
X_"a[182:183], v37 offset:1024\n" \
M_"a[32:35], a[200:203], a[224:227], a[32:35]\n" \
M_"a[36:39], a[136:139], a[164:167], a[36:39]\n" \
M_"a[36:39], a[200:203], a[228:231], a[36:39]\n" \
"buffer_store_dwordx4 v[200:203], v20, s[80:83], s35 offen sc1\n" \
M_"a[40:43], a[136:139], a[168:171], a[40:43]\n" \
"s_mov_b32 m0, s32\n" \
X_"a[244:245], v37 offset:8192\n" \
M_"a[40:43], a[200:203], a[232:235], a[40:43]\n" \
"s_mov_b32 exec_lo, 0x0\n" \
"s_mov_b32 exec_hi, 0xffffffff\n" \
"global_load_lds_dword v[188:189], off\n" \
"s_mov_b64 exec, -1\n" \
M_"a[44:47], a[136:139], a[172:175], a[44:47]\n" \
X_"a[246:247], v37 offset:9216\n" \
M_"a[44:47], a[200:203], a[236:239], a[44:47]\n" \
M_"a[60:63], a[140:143], a[172:175], a[60:63]\n" \
M_"a[60:63], a[204:207], a[236:239], a[60:63]\n" \
M_"a[56:59], a[140:143], a[168:171], a[56:59]\n" \
X_"a[184:185], v38 offset:0\n" \
M_"a[56:59], a[204:207], a[232:235], a[56:59]\n" \
M_"a[52:55], a[140:143], a[164:167], a[52:55]\n" \
"buffer_store_dwordx4 v[204:207], v20, s[80:83], s88 offen sc1\n" \
M_"a[52:55], a[204:207], a[228:231], a[52:55]\n" \
X_"a[186:187], v38 offset:1024\n" \
M_"a[48:51], a[140:143], a[160:163], a[48:51]\n" \
M_"a[48:51], a[204:207], a[224:227], a[48:51]\n" \
M_"a[64:67], a[144:147], a[160:163], a[64:67]\n" \
X_"a[248:249], v38 offset:8192\n" \
M_"a[64:67], a[208:211], a[224:227], a[64:67]\n" \
M_"a[68:71], a[144:147], a[164:167], a[68:71]\n" \
M_"a[68:71], a[208:211], a[228:231], a[68:71]\n" \
X_"a[250:251], v38 offset:9216\n" \
M_"a[72:75], a[144:147], a[168:171], a[72:75]\n" \
M_"a[72:75], a[208:211], a[232:235], a[72:75]\n" \
M_"a[76:79], a[144:147], a[172:175], a[76:79]\n" \
"buffer_store_dwordx4 v[208:211], v20, s[80:83], s36 offen sc1\n" \
M_"a[76:79], a[208:211], a[236:239], a[76:79]\n" \
X_"a[188:189], v39 offset:0\n" \
M_"a[92:95], a[148:151], a[172:175], a[92:95]\n" \
M_"a[92:95], a[212:215], a[236:239], a[92:95]\n" \
M_"a[88:91], a[148:151], a[168:171], a[88:91]\n" \
X_"a[190:191], v39 offset:1024\n" \
M_"a[88:91], a[212:215], a[232:235], a[88:91]\n" \
M_"a[84:87], a[148:151], a[164:167], a[84:87]\n" \
M_"a[84:87], a[212:215], a[228:231], a[84:87]\n" \
X_"a[252:253], v39 offset:8192\n" \
M_"a[80:83], a[148:151], a[160:163], a[80:83]\n" \
M_"a[80:83], a[212:215], a[224:227], a[80:83]\n" \
M_"a[96:99], a[152:155], a[160:163], a[96:99]\n" \
X_"a[254:255], v39 offset:9216\n" \
M_"a[96:99], a[216:219], a[224:227], a[96:99]\n" \
M_"a[100:103], a[152:155], a[164:167], a[100:103]\n" \
M_"a[100:103], a[216:219], a[228:231], a[100:103]\n" \
M_"a[104:107], a[152:155], a[168:171], a[104:107]\n" \
M_"a[104:107], a[216:219], a[232:235], a[104:107]\n" \
M_"a[108:111], a[152:155], a[172:175], a[108:111]\n" \
M_"a[108:111], a[216:219], a[236:239], a[108:111]\n" \
"s_waitcnt lgkmcnt(15)\n" \
M_"a[124:127], a[156:159], a[172:175], a[124:127]\n" \
M_"a[124:127], a[220:223], a[236:239], a[124:127]\n" \
M_"a[120:123], a[156:159], a[168:171], a[120:123]\n" \
M_"a[120:123], a[220:223], a[232:235], a[120:123]\n" \
M_"a[116:119], a[156:159], a[164:167], a[116:119]\n" \
M_"a[116:119], a[220:223], a[228:231], a[116:119]\n" \
M_"a[112:115], a[156:159], a[160:163], a[112:115]\n" \
"s_waitcnt vmcnt(4) lgkmcnt(0)\n" \
M_"a[112:115], a[220:223], a[224:227], a[112:115]\n" \
"s_barrier\n" \
M_"v[52:55], a[128:131], a[176:179], v[52:55]\n" \
"s_add_u32 m0, s30, 0x0\n" \
"s_movk_i32 s84, 0x300\n" \
M_"v[52:55], a[192:195], a[240:243], v[52:55]\n" \
B_"v22, s[64:67], s84 offen offset:0 lds\n" \
M_"v[56:59], a[128:131], a[180:183], v[56:59]\n" \
X_"a[160:161], v32 offset:32768\n" \
M_"v[56:59], a[192:195], a[244:247], v[56:59]\n" \
"s_movk_i32 s84, 0x1f00\n" \
M_"v[60:63], a[128:131], a[184:187], v[60:63]\n" \
B_"v23, s[64:67], s84 offen offset:1024 lds\n" \
M_"v[60:63], a[192:195], a[248:251], v[60:63]\n" \
X_"a[162:163], v32 offset:33792\n" \
M_"v[64:67], a[128:131], a[188:191], v[64:67]\n" \
X_"a[224:225], v32 offset:40960\n" \
M_"v[64:67], a[192:195], a[252:255], v[64:67]\n" \
R_"a[128:131], v28 offset:49152\n" \
M_"v[80:83], a[132:135], a[188:191], v[80:83]\n" \
R_"a[192:195], v29 offset:49152\n" \
M_"v[80:83], a[196:199], a[252:255], v[80:83]\n" \
"s_movk_i32 s84, 0x3b00\n" \
M_"v[76:79], a[132:135], a[184:187], v[76:79]\n" \
B_"v22, s[64:67], s84 offen offset:2048 lds\n" \
M_"v[76:79], a[196:199], a[248:251], v[76:79]\n" \
"s_movk_i32 s84, 0x5700\n" \
M_"v[72:75], a[132:135], a[180:183], v[72:75]\n" \
B_"v23, s[64:67], s84 offen offset:3072 lds\n" \
M_"v[72:75], a[196:199], a[244:247], v[72:75]\n" \
"s_add_u32 m0, s31, 0x0\n" \
"s_mov_b32 s84, 0x300000\n" \
M_"v[68:71], a[132:135], a[176:179], v[68:71]\n" \
B_"v26, s[68:71], s84 offen offset:0 lds\n" \
M_"v[68:71], a[196:199], a[240:243], v[68:71]\n" \
R_"a[132:135], v28 offset:51200\n" \
M_"v[84:87], a[136:139], a[176:179], v[84:87]\n" \
R_"a[196:199], v29 offset:51200\n" \
M_"v[84:87], a[200:203], a[240:243], v[84:87]\n" \
"s_addk_i32 s84, 0x7c00\n" \
M_"v[88:91], a[136:139], a[180:183], v[88:91]\n" \
B_"v26, s[68:71], s84 offen offset:1024 lds\n" \
M_"v[88:91], a[200:203], a[244:247], v[88:91]\n" \
X_"a[226:227], v32 offset:41984\n" \
M_"v[92:95], a[136:139], a[184:187], v[92:95]\n" \
"s_addk_i32 s84, 0x7c00\n" \
M_"v[92:95], a[200:203], a[248:251], v[92:95]\n" \
B_"v27, s[68:71], s84 offen offset:2048 lds\n" \
M_"v[96:99], a[136:139], a[188:191], v[96:99]\n" \
X_"a[164:165], v33 offset:32768\n" \
M_"v[96:99], a[200:203], a[252:255], v[96:99]\n" \
R_"a[136:139], v28 offset:53248\n" \
M_"v[112:115], a[140:143], a[188:191], v[112:115]\n" \
R_"a[200:203], v29 offset:53248\n" \
M_"v[112:115], a[204:207], a[252:255], v[112:115]\n" \
"s_addk_i32 s84, 0x7c00\n" \
M_"v[108:111], a[140:143], a[184:187], v[108:111]\n" \
B_"v27, s[68:71], s84 offen offset:3072 lds\n" \
M_"v[108:111], a[204:207], a[248:251], v[108:111]\n" \
"s_add_u32 m0, s30, 0x1000\n" \
"s_mov_b32 s84, 0x8300\n" \
M_"v[104:107], a[140:143], a[180:183], v[104:107]\n" \
B_"v22, s[64:67], s84 offen offset:0 lds\n" \
M_"v[104:107], a[204:207], a[244:247], v[104:107]\n" \
X_"a[166:167], v33 offset:33792\n" \
M_"v[100:103], a[140:143], a[176:179], v[100:103]\n" \
X_"a[228:229], v33 offset:40960\n" \
M_"v[100:103], a[204:207], a[240:243], v[100:103]\n" \
R_"a[140:143], v28 offset:55296\n" \
M_"v[116:119], a[144:147], a[176:179], v[116:119]\n" \
R_"a[204:207], v29 offset:55296\n" \
M_"v[116:119], a[208:211], a[240:243], v[116:119]\n" \
"s_addk_i32 s84, 0x1c00\n" \
M_"v[120:123], a[144:147], a[180:183], v[120:123]\n" \
B_"v23, s[64:67], s84 offen offset:1024 lds\n" \
M_"v[120:123], a[208:211], a[244:247], v[120:123]\n" \
"s_addk_i32 s84, 0x1c00\n" \
M_"v[124:127], a[144:147], a[184:187], v[124:127]\n" \
B_"v22, s[64:67], s84 offen offset:2048 lds\n" \
M_"v[124:127], a[208:211], a[248:251], v[124:127]\n" \
"s_addk_i32 s84, 0x1c00\n" \
"s_nop 0\n" \
M_"v[128:131], a[144:147], a[188:191], v[128:131]\n" \
B_"v23, s[64:67], s84 offen offset:3072 lds\n" \
M_"v[128:131], a[208:211], a[252:255], v[128:131]\n" \
R_"a[144:147], v28 offset:32768\n" \
M_"v[144:147], a[148:151], a[188:191], v[144:147]\n" \
R_"a[208:211], v29 offset:32768\n" \
M_"v[144:147], a[212:215], a[252:255], v[144:147]\n" \
"s_add_u32 m0, s31, 0x4000\n" \
"s_mov_b32 s84, 0x300000\n" \
M_"v[140:143], a[148:151], a[184:187], v[140:143]\n" \
B_"v26, s[72:75], s84 offen offset:0 lds\n" \
M_"v[140:143], a[212:215], a[248:251], v[140:143]\n" \
X_"a[230:231], v33 offset:41984\n" \
M_"v[136:139], a[148:151], a[180:183], v[136:139]\n" \
"s_addk_i32 s84, 0x7c00\n" \
"s_nop 0\n" \
M_"v[136:139], a[212:215], a[244:247], v[136:139]\n" \
B_"v26, s[72:75], s84 offen offset:1024 lds\n" \
M_"v[132:135], a[148:151], a[176:179], v[132:135]\n" \
X_"a[168:169], v34 offset:32768\n" \
M_"v[132:135], a[212:215], a[240:243], v[132:135]\n" \
R_"a[148:151], v28 offset:34816\n" \
M_"v[148:151], a[152:155], a[176:179], v[148:151]\n" \
R_"a[212:215], v29 offset:34816\n" \
M_"v[148:151], a[216:219], a[240:243], v[148:151]\n" \
X_"a[170:171], v34 offset:33792\n" \
M_"v[152:155], a[152:155], a[180:183], v[152:155]\n" \
X_"a[232:233], v34 offset:40960\n" \
M_"v[152:155], a[216:219], a[244:247], v[152:155]\n" \
X_"a[234:235], v34 offset:41984\n" \
M_"v[156:159], a[152:155], a[184:187], v[156:159]\n" \
X_"a[172:173], v35 offset:32768\n" \
M_"v[156:159], a[216:219], a[248:251], v[156:159]\n" \
X_"a[174:175], v35 offset:33792\n" \
M_"v[160:163], a[152:155], a[188:191], v[160:163]\n" \
X_"a[236:237], v35 offset:40960\n" \
M_"v[160:163], a[216:219], a[252:255], v[160:163]\n" \
R_"a[152:155], v28 offset:36864\n" \
M_"v[176:179], a[156:159], a[188:191], v[176:179]\n" \
R_"a[216:219], v29 offset:36864\n" \
M_"v[176:179], a[220:223], a[252:255], v[176:179]\n" \
X_"a[238:239], v35 offset:41984\n" \
M_"v[172:175], a[156:159], a[184:187], v[172:175]\n" \
M_"v[172:175], a[220:223], a[248:251], v[172:175]\n" \
M_"v[168:171], a[156:159], a[180:183], v[168:171]\n" \
M_"v[168:171], a[220:223], a[244:247], v[168:171]\n" \
M_"v[164:167], a[156:159], a[176:179], v[164:167]\n" \
M_"v[164:167], a[220:223], a[240:243], v[164:167]\n" \
R_"a[156:159], v28 offset:38912\n" \
R_"a[220:223], v29 offset:38912\n" \
"s_waitcnt lgkmcnt(15)\n" \
M_"a[0:3], a[128:131], a[160:163], a[0:3]\n" \
"s_addk_i32 s84, 0x7c00\n" \
M_"a[0:3], a[192:195], a[224:227], a[0:3]\n" \
B_"v27, s[72:75], s84 offen offset:2048 lds\n" \
M_"a[4:7], a[128:131], a[164:167], a[4:7]\n" \
X_"a[176:177], v36 offset:32768\n" \
"s_waitcnt lgkmcnt(15)\n" \
M_"a[4:7], a[192:195], a[228:231], a[4:7]\n" \
"s_addk_i32 s84, 0x7c00\n" \
"s_waitcnt lgkmcnt(11)\n" \
M_"a[8:11], a[128:131], a[168:171], a[8:11]\n" \
B_"v27, s[72:75], s84 offen offset:3072 lds\n" \
"s_waitcnt lgkmcnt(9)\n" \
M_"a[8:11], a[192:195], a[232:235], a[8:11]\n" \
X_"a[178:179], v36 offset:33792\n" \
"s_waitcnt lgkmcnt(8)\n" \
M_"a[12:15], a[128:131], a[172:175], a[12:15]\n" \
X_"a[240:241], v36 offset:40960\n" \
"s_waitcnt lgkmcnt(5)\n" \
M_"a[12:15], a[192:195], a[236:239], a[12:15]\n" \
M_"a[28:31], a[132:135], a[172:175], a[28:31]\n" \
M_"a[28:31], a[196:199], a[236:239], a[28:31]\n" \
X_"a[242:243], v36 offset:41984\n" \
M_"a[24:27], a[132:135], a[168:171], a[24:27]\n" \
M_"a[24:27], a[196:199], a[232:235], a[24:27]\n" \
M_"a[20:23], a[132:135], a[164:167], a[20:23]\n" \
M_"a[20:23], a[196:199], a[228:231], a[20:23]\n" \
X_"a[180:181], v37 offset:32768\n" \
M_"a[16:19], a[132:135], a[160:163], a[16:19]\n" \
M_"a[16:19], a[196:199], a[224:227], a[16:19]\n" \
M_"a[32:35], a[136:139], a[160:163], a[32:35]\n" \
X_"a[182:183], v37 offset:33792\n" \
M_"a[32:35], a[200:203], a[224:227], a[32:35]\n" \
M_"a[36:39], a[136:139], a[164:167], a[36:39]\n" \
"v_lshlrev_b32_e32 v45, 3, v20\n" \
"v_add_u32_e32 v45, s31, v45\n" \
M_"a[36:39], a[200:203], a[228:231], a[36:39]\n" \
"s_getpc_b64 s[86:87]\n" \
"s_sub_u32 s86, s86, 0x10800\n" \
"s_subb_u32 s87, s87, 0\n" \
"s_mov_b32 m0, s32\n" \
"s_nop 0\n" \
M_"a[40:43], a[136:139], a[168:171], a[40:43]\n" \
"global_load_lds_dword v45, s[86:87]\n" \
M_"a[40:43], a[200:203], a[232:235], a[40:43]\n" \
X_"a[244:245], v37 offset:40960\n" \
M_"a[44:47], a[136:139], a[172:175], a[44:47]\n" \
X_"a[246:247], v37 offset:41984\n" \
M_"a[44:47], a[200:203], a[236:239], a[44:47]\n" \
M_"a[60:63], a[140:143], a[172:175], a[60:63]\n" \
M_"a[60:63], a[204:207], a[236:239], a[60:63]\n" \
M_"a[56:59], a[140:143], a[168:171], a[56:59]\n" \
X_"a[184:185], v38 offset:32768\n" \
M_"a[56:59], a[204:207], a[232:235], a[56:59]\n" \
M_"a[52:55], a[140:143], a[164:167], a[52:55]\n" \
M_"a[52:55], a[204:207], a[228:231], a[52:55]\n" \
X_"a[186:187], v38 offset:33792\n" \
M_"a[48:51], a[140:143], a[160:163], a[48:51]\n" \
M_"a[48:51], a[204:207], a[224:227], a[48:51]\n" \
M_"a[64:67], a[144:147], a[160:163], a[64:67]\n" \
X_"a[248:249], v38 offset:40960\n" \
M_"a[64:67], a[208:211], a[224:227], a[64:67]\n" \
M_"a[68:71], a[144:147], a[164:167], a[68:71]\n" \
M_"a[68:71], a[208:211], a[228:231], a[68:71]\n" \
X_"a[250:251], v38 offset:41984\n" \
M_"a[72:75], a[144:147], a[168:171], a[72:75]\n" \
M_"a[72:75], a[208:211], a[232:235], a[72:75]\n" \
M_"a[76:79], a[144:147], a[172:175], a[76:79]\n" \
M_"a[76:79], a[208:211], a[236:239], a[76:79]\n" \
X_"a[188:189], v39 offset:32768\n" \
M_"a[92:95], a[148:151], a[172:175], a[92:95]\n" \
M_"a[92:95], a[212:215], a[236:239], a[92:95]\n" \
M_"a[88:91], a[148:151], a[168:171], a[88:91]\n" \
X_"a[190:191], v39 offset:33792\n" \
M_"a[88:91], a[212:215], a[232:235], a[88:91]\n" \
M_"a[84:87], a[148:151], a[164:167], a[84:87]\n" \
M_"a[84:87], a[212:215], a[228:231], a[84:87]\n" \
X_"a[252:253], v39 offset:40960\n" \
M_"a[80:83], a[148:151], a[160:163], a[80:83]\n" \
M_"a[80:83], a[212:215], a[224:227], a[80:83]\n" \
M_"a[96:99], a[152:155], a[160:163], a[96:99]\n" \
X_"a[254:255], v39 offset:41984\n" \
M_"a[96:99], a[216:219], a[224:227], a[96:99]\n" \
M_"a[100:103], a[152:155], a[164:167], a[100:103]\n" \
M_"a[100:103], a[216:219], a[228:231], a[100:103]\n" \
M_"a[104:107], a[152:155], a[168:171], a[104:107]\n" \
M_"a[104:107], a[216:219], a[232:235], a[104:107]\n" \
M_"a[108:111], a[152:155], a[172:175], a[108:111]\n" \
M_"a[108:111], a[216:219], a[236:239], a[108:111]\n" \
"s_waitcnt lgkmcnt(15)\n" \
M_"a[124:127], a[156:159], a[172:175], a[124:127]\n" \
M_"a[124:127], a[220:223], a[236:239], a[124:127]\n" \
M_"a[120:123], a[156:159], a[168:171], a[120:123]\n" \
M_"a[120:123], a[220:223], a[232:235], a[120:123]\n" \
M_"a[116:119], a[156:159], a[164:167], a[116:119]\n" \
M_"a[116:119], a[220:223], a[228:231], a[116:119]\n" \
M_"a[112:115], a[156:159], a[160:163], a[112:115]\n" \
"s_waitcnt vmcnt(1) lgkmcnt(0)\n" \
M_"a[112:115], a[220:223], a[224:227], a[112:115]\n" \
"s_barrier\n" \
"v_mov_b32_e32 v45, s62\n" \
"s_mov_b64 exec, 1\n" \
"buffer_store_dword v45, off, s[80:83], s37 offset:128 sc1\n" \
"s_mov_b64 exec, -1\n" \
M_"v[52:55], a[128:131], a[176:179], v[52:55]\n" \
"s_add_u32 m0, s30, 0x8000\n" \
"s_movk_i32 s84, 0x380\n" \
M_"v[52:55], a[192:195], a[240:243], v[52:55]\n" \
B_"v22, s[64:67], s84 offen offset:0 lds\n" \
M_"v[56:59], a[128:131], a[180:183], v[56:59]\n" \
X_"a[160:161], v32 offset:0\n" \
M_"v[56:59], a[192:195], a[244:247], v[56:59]\n" \
"s_movk_i32 s84, 0x1f80\n" \
M_"v[60:63], a[128:131], a[184:187], v[60:63]\n" \
B_"v23, s[64:67], s84 offen offset:1024 lds\n" \
M_"v[60:63], a[192:195], a[248:251], v[60:63]\n" \
X_"a[162:163], v32 offset:1024\n" \
M_"v[64:67], a[128:131], a[188:191], v[64:67]\n" \
X_"a[224:225], v32 offset:8192\n" \
M_"v[64:67], a[192:195], a[252:255], v[64:67]\n" \
R_"a[128:131], v28 offset:16384\n" \
M_"v[80:83], a[132:135], a[188:191], v[80:83]\n" \
R_"a[192:195], v29 offset:16384\n" \
M_"v[80:83], a[196:199], a[252:255], v[80:83]\n" \
"s_movk_i32 s84, 0x3b80\n" \
M_"v[76:79], a[132:135], a[184:187], v[76:79]\n" \
B_"v22, s[64:67], s84 offen offset:2048 lds\n" \
M_"v[76:79], a[196:199], a[248:251], v[76:79]\n" \
"s_movk_i32 s84, 0x5780\n" \
M_"v[72:75], a[132:135], a[180:183], v[72:75]\n" \
B_"v23, s[64:67], s84 offen offset:3072 lds\n" \
M_"v[72:75], a[196:199], a[244:247], v[72:75]\n" \
"s_add_u32 m0, s31, 0x8000\n" \
"s_mov_b32 s84, 0x380000\n" \
M_"v[68:71], a[132:135], a[176:179], v[68:71]\n" \
B_"v26, s[68:71], s84 offen offset:0 lds\n" \
M_"v[68:71], a[196:199], a[240:243], v[68:71]\n" \
R_"a[132:135], v28 offset:18432\n" \
M_"v[84:87], a[136:139], a[176:179], v[84:87]\n" \
R_"a[196:199], v29 offset:18432\n" \
M_"v[84:87], a[200:203], a[240:243], v[84:87]\n" \
"s_addk_i32 s84, 0x7c00\n" \
M_"v[88:91], a[136:139], a[180:183], v[88:91]\n" \
B_"v26, s[68:71], s84 offen offset:1024 lds\n" \
M_"v[88:91], a[200:203], a[244:247], v[88:91]\n" \
X_"a[226:227], v32 offset:9216\n" \
M_"v[92:95], a[136:139], a[184:187], v[92:95]\n" \
"s_addk_i32 s84, 0x7c00\n" \
M_"v[92:95], a[200:203], a[248:251], v[92:95]\n" \
B_"v27, s[68:71], s84 offen offset:2048 lds\n" \
M_"v[96:99], a[136:139], a[188:191], v[96:99]\n" \
X_"a[164:165], v33 offset:0\n" \
M_"v[96:99], a[200:203], a[252:255], v[96:99]\n" \
R_"a[136:139], v28 offset:20480\n" \
M_"v[112:115], a[140:143], a[188:191], v[112:115]\n" \
R_"a[200:203], v29 offset:20480\n" \
M_"v[112:115], a[204:207], a[252:255], v[112:115]\n" \
"s_addk_i32 s84, 0x7c00\n" \
"s_nop 0\n" \
M_"v[108:111], a[140:143], a[184:187], v[108:111]\n" \
B_"v27, s[68:71], s84 offen offset:3072 lds\n" \
M_"v[108:111], a[204:207], a[248:251], v[108:111]\n" \
"s_add_u32 m0, s30, 0x9000\n" \
"s_mov_b32 s84, 0x8380\n" \
M_"v[104:107], a[140:143], a[180:183], v[104:107]\n" \
B_"v22, s[64:67], s84 offen offset:0 lds\n" \
M_"v[104:107], a[204:207], a[244:247], v[104:107]\n" \
X_"a[166:167], v33 offset:1024\n" \
M_"v[100:103], a[140:143], a[176:179], v[100:103]\n" \
X_"a[228:229], v33 offset:8192\n" \
M_"v[100:103], a[204:207], a[240:243], v[100:103]\n" \
R_"a[140:143], v28 offset:22528\n" \
M_"v[116:119], a[144:147], a[176:179], v[116:119]\n" \
R_"a[204:207], v29 offset:22528\n" \
M_"v[116:119], a[208:211], a[240:243], v[116:119]\n" \
"s_addk_i32 s84, 0x1c00\n" \
M_"v[120:123], a[144:147], a[180:183], v[120:123]\n" \
B_"v23, s[64:67], s84 offen offset:1024 lds\n" \
M_"v[120:123], a[208:211], a[244:247], v[120:123]\n" \
"s_addk_i32 s84, 0x1c00\n" \
M_"v[124:127], a[144:147], a[184:187], v[124:127]\n" \
B_"v22, s[64:67], s84 offen offset:2048 lds\n" \
M_"v[124:127], a[208:211], a[248:251], v[124:127]\n" \
"s_addk_i32 s84, 0x1c00\n" \
"s_nop 0\n" \
M_"v[128:131], a[144:147], a[188:191], v[128:131]\n" \
B_"v23, s[64:67], s84 offen offset:3072 lds\n" \
M_"v[128:131], a[208:211], a[252:255], v[128:131]\n" \
R_"a[144:147], v28 offset:0\n" \
M_"v[144:147], a[148:151], a[188:191], v[144:147]\n" \
R_"a[208:211], v29 offset:0\n" \
M_"v[144:147], a[212:215], a[252:255], v[144:147]\n" \
"s_add_u32 m0, s31, 0xc000\n" \
"s_mov_b32 s84, 0x380000\n" \
M_"v[140:143], a[148:151], a[184:187], v[140:143]\n" \
B_"v26, s[72:75], s84 offen offset:0 lds\n" \
M_"v[140:143], a[212:215], a[248:251], v[140:143]\n" \
X_"a[230:231], v33 offset:9216\n" \
M_"v[136:139], a[148:151], a[180:183], v[136:139]\n" \
"s_addk_i32 s84, 0x7c00\n" \
"s_nop 0\n" \
M_"v[136:139], a[212:215], a[244:247], v[136:139]\n" \
B_"v26, s[72:75], s84 offen offset:1024 lds\n" \
M_"v[132:135], a[148:151], a[176:179], v[132:135]\n" \
X_"a[168:169], v34 offset:0\n" \
M_"v[132:135], a[212:215], a[240:243], v[132:135]\n" \
R_"a[148:151], v28 offset:2048\n" \
M_"v[148:151], a[152:155], a[176:179], v[148:151]\n" \
R_"a[212:215], v29 offset:2048\n" \
M_"v[148:151], a[216:219], a[240:243], v[148:151]\n" \
X_"a[170:171], v34 offset:1024\n" \
M_"v[152:155], a[152:155], a[180:183], v[152:155]\n" \
X_"a[232:233], v34 offset:8192\n" \
M_"v[152:155], a[216:219], a[244:247], v[152:155]\n" \
X_"a[234:235], v34 offset:9216\n" \
M_"v[156:159], a[152:155], a[184:187], v[156:159]\n" \
X_"a[172:173], v35 offset:0\n" \
M_"v[156:159], a[216:219], a[248:251], v[156:159]\n" \
X_"a[174:175], v35 offset:1024\n" \
M_"v[160:163], a[152:155], a[188:191], v[160:163]\n" \
X_"a[236:237], v35 offset:8192\n" \
M_"v[160:163], a[216:219], a[252:255], v[160:163]\n" \
R_"a[152:155], v28 offset:4096\n" \
M_"v[176:179], a[156:159], a[188:191], v[176:179]\n" \
R_"a[216:219], v29 offset:4096\n" \
M_"v[176:179], a[220:223], a[252:255], v[176:179]\n" \
X_"a[238:239], v35 offset:9216\n" \
M_"v[172:175], a[156:159], a[184:187], v[172:175]\n" \
M_"v[172:175], a[220:223], a[248:251], v[172:175]\n" \
M_"v[168:171], a[156:159], a[180:183], v[168:171]\n" \
M_"v[168:171], a[220:223], a[244:247], v[168:171]\n" \
M_"v[164:167], a[156:159], a[176:179], v[164:167]\n" \
M_"v[164:167], a[220:223], a[240:243], v[164:167]\n" \
R_"a[156:159], v28 offset:6144\n" \
R_"a[220:223], v29 offset:6144\n" \
"s_waitcnt lgkmcnt(15)\n" \
M_"a[0:3], a[128:131], a[160:163], a[0:3]\n" \
"s_addk_i32 s84, 0x7c00\n" \
M_"a[0:3], a[192:195], a[224:227], a[0:3]\n" \
B_"v27, s[72:75], s84 offen offset:2048 lds\n" \
M_"a[4:7], a[128:131], a[164:167], a[4:7]\n" \
X_"a[176:177], v36 offset:0\n" \
"s_waitcnt lgkmcnt(15)\n" \
M_"a[4:7], a[192:195], a[228:231], a[4:7]\n" \
"s_addk_i32 s84, 0x7c00\n" \
"s_waitcnt lgkmcnt(11)\n" \
M_"a[8:11], a[128:131], a[168:171], a[8:11]\n" \
B_"v27, s[72:75], s84 offen offset:3072 lds\n" \
"s_waitcnt lgkmcnt(9)\n" \
M_"a[8:11], a[192:195], a[232:235], a[8:11]\n" \
X_"a[178:179], v36 offset:1024\n" \
"s_waitcnt lgkmcnt(8)\n" \
M_"a[12:15], a[128:131], a[172:175], a[12:15]\n" \
X_"a[240:241], v36 offset:8192\n" \
"s_waitcnt lgkmcnt(5)\n" \
M_"a[12:15], a[192:195], a[236:239], a[12:15]\n" \
M_"a[28:31], a[132:135], a[172:175], a[28:31]\n" \
M_"a[28:31], a[196:199], a[236:239], a[28:31]\n" \
X_"a[242:243], v36 offset:9216\n" \
M_"a[24:27], a[132:135], a[168:171], a[24:27]\n" \
M_"a[24:27], a[196:199], a[232:235], a[24:27]\n" \
M_"a[20:23], a[132:135], a[164:167], a[20:23]\n" \
M_"a[20:23], a[196:199], a[228:231], a[20:23]\n" \
X_"a[180:181], v37 offset:0\n" \
M_"a[16:19], a[132:135], a[160:163], a[16:19]\n" \
M_"a[16:19], a[196:199], a[224:227], a[16:19]\n" \
M_"a[32:35], a[136:139], a[160:163], a[32:35]\n" \
"v_bfe_u32 v44, v20, 2, 5\n" \
X_"a[182:183], v37 offset:1024\n" \
M_"a[32:35], a[200:203], a[224:227], a[32:35]\n" \
"buffer_load_dword v44, v44, s[80:83], s38 offen offset:128 sc1\n" \
M_"a[36:39], a[136:139], a[164:167], a[36:39]\n" \
M_"a[36:39], a[200:203], a[228:231], a[36:39]\n" \
X_"a[244:245], v37 offset:8192\n" \
M_"a[40:43], a[136:139], a[168:171], a[40:43]\n" \
M_"a[40:43], a[200:203], a[232:235], a[40:43]\n" \
M_"a[44:47], a[136:139], a[172:175], a[44:47]\n" \
X_"a[246:247], v37 offset:9216\n" \
M_"a[44:47], a[200:203], a[236:239], a[44:47]\n" \
M_"a[60:63], a[140:143], a[172:175], a[60:63]\n" \
"global_store_dwordx4 v49, v[0:3], s[28:29] offset:0 sc1\n" \
M_"a[60:63], a[204:207], a[236:239], a[60:63]\n" \
M_"a[56:59], a[140:143], a[168:171], a[56:59]\n" \
X_"a[184:185], v38 offset:0\n" \
M_"a[56:59], a[204:207], a[232:235], a[56:59]\n" \
M_"a[52:55], a[140:143], a[164:167], a[52:55]\n" \
M_"a[52:55], a[204:207], a[228:231], a[52:55]\n" \
X_"a[186:187], v38 offset:1024\n" \
M_"a[48:51], a[140:143], a[160:163], a[48:51]\n" \
M_"a[48:51], a[204:207], a[224:227], a[48:51]\n" \
M_"a[64:67], a[144:147], a[160:163], a[64:67]\n" \
"global_store_dwordx4 v49, v[0:3], s[28:29] offset:1024 sc1\n" \
M_"a[64:67], a[208:211], a[224:227], a[64:67]\n" \
X_"a[248:249], v38 offset:8192\n" \
M_"a[68:71], a[144:147], a[164:167], a[68:71]\n" \
M_"a[68:71], a[208:211], a[228:231], a[68:71]\n" \
X_"a[250:251], v38 offset:9216\n" \
M_"a[72:75], a[144:147], a[168:171], a[72:75]\n" \
M_"a[72:75], a[208:211], a[232:235], a[72:75]\n" \
M_"a[76:79], a[144:147], a[172:175], a[76:79]\n" \
M_"a[76:79], a[208:211], a[236:239], a[76:79]\n" \
X_"a[188:189], v39 offset:0\n" \
M_"a[92:95], a[148:151], a[172:175], a[92:95]\n" \
"s_mov_b64 exec, 0xffff\n" \
"global_store_dwordx4 v49, v[0:3], s[28:29] offset:2048 sc1\n" \
"s_mov_b64 exec, -1\n" \
"s_nop 0\n" \
M_"a[92:95], a[212:215], a[236:239], a[92:95]\n" \
M_"a[88:91], a[148:151], a[168:171], a[88:91]\n" \
X_"a[190:191], v39 offset:1024\n" \
M_"a[88:91], a[212:215], a[232:235], a[88:91]\n" \
M_"a[84:87], a[148:151], a[164:167], a[84:87]\n" \
M_"a[84:87], a[212:215], a[228:231], a[84:87]\n" \
".fill (L_pb + 57056 - .) / 4, 4, 0xBF800000\n" \
"L_pp14:\n" \
"s_cbranch_execz L_pret\n" \
"s_nop 0\n" \
X_"a[252:253], v39 offset:8192\n" \
M_"a[80:83], a[148:151], a[160:163], a[80:83]\n" \
M_"a[80:83], a[212:215], a[224:227], a[80:83]\n" \
M_"a[96:99], a[152:155], a[160:163], a[96:99]\n" \
X_"a[254:255], v39 offset:9216\n" \
M_"a[96:99], a[216:219], a[224:227], a[96:99]\n" \
M_"a[100:103], a[152:155], a[164:167], a[100:103]\n" \
M_"a[100:103], a[216:219], a[228:231], a[100:103]\n" \
M_"a[104:107], a[152:155], a[168:171], a[104:107]\n" \
M_"a[104:107], a[216:219], a[232:235], a[104:107]\n" \
M_"a[108:111], a[152:155], a[172:175], a[108:111]\n" \
M_"a[108:111], a[216:219], a[236:239], a[108:111]\n" \
"s_waitcnt lgkmcnt(15)\n" \
M_"a[124:127], a[156:159], a[172:175], a[124:127]\n" \
M_"a[124:127], a[220:223], a[236:239], a[124:127]\n" \
M_"a[120:123], a[156:159], a[168:171], a[120:123]\n" \
M_"a[120:123], a[220:223], a[232:235], a[120:123]\n" \
M_"a[116:119], a[156:159], a[164:167], a[116:119]\n" \
M_"a[116:119], a[220:223], a[228:231], a[116:119]\n" \
M_"a[112:115], a[156:159], a[160:163], a[112:115]\n" \
"s_waitcnt vmcnt(3) lgkmcnt(0)\n" \
M_"a[112:115], a[220:223], a[224:227], a[112:115]\n" \
"s_barrier\n" \
"v_cmp_ne_u32_e32 vcc, s62, v44\n" \
"s_nop 4\n" \
"s_cbranch_vccnz L_poll61_%=\n" \
"L_pollr61_%=:\n" \
"s_mov_b32 s68, s12\n" \
"s_and_b32 s69, s13, 0xffff\n" \
"s_mov_b32 s70, 0x480000\n" \
"s_mov_b32 s71, 0x20000\n" \
M_"v[52:55], a[128:131], a[176:179], v[52:55]\n" \
"s_add_u32 m0, s40, 0x0\n" \
"s_add_u32 s85, s39, 0x0\n" \
"s_nop 0\n" \
M_"v[52:55], a[192:195], a[240:243], v[52:55]\n" \
B_"v20, s[80:83], s85 offen sc1 lds\n" \
M_"v[56:59], a[128:131], a[180:183], v[56:59]\n" \
X_"a[160:161], v32 offset:32768\n" \
M_"v[56:59], a[192:195], a[244:247], v[56:59]\n" \
"s_add_u32 m0, s40, 0x1000\n" \
"s_add_u32 s85, s39, 0x1000\n" \
M_"v[60:63], a[128:131], a[184:187], v[60:63]\n" \
B_"v20, s[80:83], s85 offen sc1 lds\n" \
M_"v[60:63], a[192:195], a[248:251], v[60:63]\n" \
X_"a[162:163], v32 offset:33792\n" \
M_"v[64:67], a[128:131], a[188:191], v[64:67]\n" \
X_"a[224:225], v32 offset:40960\n" \
M_"v[64:67], a[192:195], a[252:255], v[64:67]\n" \
R_"a[128:131], v28 offset:49152\n" \
M_"v[80:83], a[132:135], a[188:191], v[80:83]\n" \
R_"a[192:195], v29 offset:49152\n" \
M_"v[80:83], a[196:199], a[252:255], v[80:83]\n" \
"s_add_u32 m0, s40, 0x3000\n" \
"s_movk_i32 s84, 0x0\n" \
M_"v[76:79], a[132:135], a[184:187], v[76:79]\n" \
B_"v21, s[76:79], s84 offen offset:0 lds\n" \
M_"v[76:79], a[196:199], a[248:251], v[76:79]\n" \
"s_add_u32 m0, s30, 0x0\n" \
"s_movk_i32 s84, 0x0\n" \
"s_nop 0\n" \
M_"v[72:75], a[132:135], a[180:183], v[72:75]\n" \
B_"v24, s[76:79], s84 offen offset:0 lds\n" \
M_"v[72:75], a[196:199], a[244:247], v[72:75]\n" \
X_"a[226:227], v32 offset:41984\n" \
M_"v[68:71], a[132:135], a[176:179], v[68:71]\n" \
X_"a[164:165], v33 offset:32768\n" \
M_"v[68:71], a[196:199], a[240:243], v[68:71]\n" \
R_"a[132:135], v28 offset:51200\n" \
M_"v[84:87], a[136:139], a[176:179], v[84:87]\n" \
R_"a[196:199], v29 offset:51200\n" \
M_"v[84:87], a[200:203], a[240:243], v[84:87]\n" \
"s_movk_i32 s84, 0x2000\n" \
M_"v[88:91], a[136:139], a[180:183], v[88:91]\n" \
B_"v25, s[76:79], s84 offen offset:1024 lds\n" \
M_"v[88:91], a[200:203], a[244:247], v[88:91]\n" \
"s_movk_i32 s84, 0x4000\n" \
M_"v[92:95], a[136:139], a[184:187], v[92:95]\n" \
B_"v24, s[76:79], s84 offen offset:2048 lds\n" \
M_"v[92:95], a[200:203], a[248:251], v[92:95]\n" \
X_"a[166:167], v33 offset:33792\n" \
M_"v[96:99], a[136:139], a[188:191], v[96:99]\n" \
X_"a[228:229], v33 offset:40960\n" \
M_"v[96:99], a[200:203], a[252:255], v[96:99]\n" \
R_"a[136:139], v28 offset:53248\n" \
M_"v[112:115], a[140:143], a[188:191], v[112:115]\n" \
R_"a[200:203], v29 offset:53248\n" \
M_"v[112:115], a[204:207], a[252:255], v[112:115]\n" \
"s_movk_i32 s84, 0x6000\n" \
M_"v[108:111], a[140:143], a[184:187], v[108:111]\n" \
B_"v25, s[76:79], s84 offen offset:3072 lds\n" \
M_"v[108:111], a[204:207], a[248:251], v[108:111]\n" \
"s_add_u32 m0, s31, 0x0\n" \
"s_movk_i32 s84, 0x0\n" \
M_"v[104:107], a[140:143], a[180:183], v[104:107]\n" \
B_"v26, s[68:71], s84 offen offset:0 lds\n" \
M_"v[104:107], a[204:207], a[244:247], v[104:107]\n" \
"s_movk_i32 s84, 0x7c00\n" \
M_"v[100:103], a[140:143], a[176:179], v[100:103]\n" \
B_"v26, s[68:71], s84 offen offset:1024 lds\n" \
M_"v[100:103], a[204:207], a[240:243], v[100:103]\n" \
R_"a[140:143], v28 offset:55296\n" \
M_"v[116:119], a[144:147], a[176:179], v[116:119]\n" \
R_"a[204:207], v29 offset:55296\n" \
M_"v[116:119], a[208:211], a[240:243], v[116:119]\n" \
"s_addk_i32 s84, 0x7c00\n" \
M_"v[120:123], a[144:147], a[180:183], v[120:123]\n" \
B_"v27, s[68:71], s84 offen offset:2048 lds\n" \
M_"v[120:123], a[208:211], a[244:247], v[120:123]\n" \
X_"a[230:231], v33 offset:41984\n" \
M_"v[124:127], a[144:147], a[184:187], v[124:127]\n" \
"s_addk_i32 s84, 0x7c00\n" \
M_"v[124:127], a[208:211], a[248:251], v[124:127]\n" \
B_"v27, s[68:71], s84 offen offset:3072 lds\n" \
M_"v[128:131], a[144:147], a[188:191], v[128:131]\n" \
X_"a[168:169], v34 offset:32768\n" \
M_"v[128:131], a[208:211], a[252:255], v[128:131]\n" \
R_"a[144:147], v28 offset:32768\n" \
M_"v[144:147], a[148:151], a[188:191], v[144:147]\n" \
R_"a[208:211], v29 offset:32768\n" \
M_"v[144:147], a[212:215], a[252:255], v[144:147]\n" \
"s_add_u32 m0, s30, 0x1000\n" \
"s_mov_b32 s84, 0x9000\n" \
M_"v[140:143], a[148:151], a[184:187], v[140:143]\n" \
B_"v24, s[76:79], s84 offen offset:0 lds\n" \
M_"v[140:143], a[212:215], a[248:251], v[140:143]\n" \
X_"a[170:171], v34 offset:33792\n" \
M_"v[136:139], a[148:151], a[180:183], v[136:139]\n" \
"s_addk_i32 s84, 0x2000\n" \
"s_nop 0\n" \
M_"v[136:139], a[212:215], a[244:247], v[136:139]\n" \
B_"v25, s[76:79], s84 offen offset:1024 lds\n" \
M_"v[132:135], a[148:151], a[176:179], v[132:135]\n" \
X_"a[232:233], v34 offset:40960\n" \
M_"v[132:135], a[212:215], a[240:243], v[132:135]\n" \
R_"a[148:151], v28 offset:34816\n" \
M_"v[148:151], a[152:155], a[176:179], v[148:151]\n" \
R_"a[212:215], v29 offset:34816\n" \
M_"v[148:151], a[216:219], a[240:243], v[148:151]\n" \
X_"a[234:235], v34 offset:41984\n" \
M_"v[152:155], a[152:155], a[180:183], v[152:155]\n" \
X_"a[172:173], v35 offset:32768\n" \
M_"v[152:155], a[216:219], a[244:247], v[152:155]\n" \
X_"a[174:175], v35 offset:33792\n" \
M_"v[156:159], a[152:155], a[184:187], v[156:159]\n" \
X_"a[236:237], v35 offset:40960\n" \
M_"v[156:159], a[216:219], a[248:251], v[156:159]\n" \
X_"a[238:239], v35 offset:41984\n" \
M_"v[160:163], a[152:155], a[188:191], v[160:163]\n" \
M_"v[160:163], a[216:219], a[252:255], v[160:163]\n" \
R_"a[152:155], v28 offset:36864\n" \
M_"v[176:179], a[156:159], a[188:191], v[176:179]\n" \
R_"a[216:219], v29 offset:36864\n" \
M_"v[176:179], a[220:223], a[252:255], v[176:179]\n" \
M_"v[172:175], a[156:159], a[184:187], v[172:175]\n" \
M_"v[172:175], a[220:223], a[248:251], v[172:175]\n" \
M_"v[168:171], a[156:159], a[180:183], v[168:171]\n" \
M_"v[168:171], a[220:223], a[244:247], v[168:171]\n" \
M_"v[164:167], a[156:159], a[176:179], v[164:167]\n" \
M_"v[164:167], a[220:223], a[240:243], v[164:167]\n" \
R_"a[156:159], v28 offset:38912\n" \
R_"a[220:223], v29 offset:38912\n" \
"s_waitcnt lgkmcnt(15)\n" \
M_"a[0:3], a[128:131], a[160:163], a[0:3]\n" \
"s_addk_i32 s84, 0x2000\n" \
M_"a[0:3], a[192:195], a[224:227], a[0:3]\n" \
B_"v24, s[76:79], s84 offen offset:2048 lds\n" \
M_"a[4:7], a[128:131], a[164:167], a[4:7]\n" \
X_"a[176:177], v36 offset:32768\n" \
M_"a[4:7], a[192:195], a[228:231], a[4:7]\n" \
"s_addk_i32 s84, 0x2000\n" \
"s_waitcnt lgkmcnt(13)\n" \
M_"a[8:11], a[128:131], a[168:171], a[8:11]\n" \
B_"v25, s[76:79], s84 offen offset:3072 lds\n" \
"s_waitcnt lgkmcnt(9)\n" \
M_"a[8:11], a[192:195], a[232:235], a[8:11]\n" \
X_"a[178:179], v36 offset:33792\n" \
"s_waitcnt lgkmcnt(8)\n" \
M_"a[12:15], a[128:131], a[172:175], a[12:15]\n" \
X_"a[240:241], v36 offset:40960\n" \
"s_waitcnt lgkmcnt(7)\n" \
M_"a[12:15], a[192:195], a[236:239], a[12:15]\n" \
M_"a[28:31], a[132:135], a[172:175], a[28:31]\n" \
M_"a[28:31], a[196:199], a[236:239], a[28:31]\n" \
"s_add_u32 m0, s30, 0x20000\n" \
"s_movk_i32 s84, 0x100\n" \
M_"a[24:27], a[132:135], a[168:171], a[24:27]\n" \
B_"v24, s[76:79], s84 offen offset:0 lds\n" \
M_"a[24:27], a[196:199], a[232:235], a[24:27]\n" \
X_"a[242:243], v36 offset:41984\n" \
M_"a[20:23], a[132:135], a[164:167], a[20:23]\n" \
"v_accvgpr_read_b32 v184, a12\n" \
"v_accvgpr_read_b32 v185, a13\n" \
M_"a[20:23], a[196:199], a[228:231], a[20:23]\n" \
X_"a[180:181], v37 offset:32768\n" \
"v_accvgpr_read_b32 v186, a14\n" \
M_"a[16:19], a[132:135], a[160:163], a[16:19]\n" \
"s_movk_i32 s84, 0x2100\n" \
"s_nop 0\n" \
M_"a[16:19], a[196:199], a[224:227], a[16:19]\n" \
B_"v25, s[76:79], s84 offen offset:1024 lds\n" \
M_"a[32:35], a[136:139], a[160:163], a[32:35]\n" \
X_"a[182:183], v37 offset:33792\n" \
"v_accvgpr_read_b32 v187, a15\n" \
M_"a[32:35], a[200:203], a[224:227], a[32:35]\n" \
"v_accvgpr_read_b32 v180, a28\n" \
"v_accvgpr_read_b32 v181, a29\n" \
M_"a[36:39], a[136:139], a[164:167], a[36:39]\n" \
"v_accvgpr_read_b32 v182, a30\n" \
"v_accvgpr_read_b32 v183, a31\n" \
M_"a[36:39], a[200:203], a[228:231], a[36:39]\n" \
"s_movk_i32 s84, 0x4100\n" \
"s_nop 0\n" \
M_"a[40:43], a[136:139], a[168:171], a[40:43]\n" \
B_"v24, s[76:79], s84 offen offset:2048 lds\n" \
M_"a[40:43], a[200:203], a[232:235], a[40:43]\n" \
X_"a[244:245], v37 offset:40960\n" \
C_"v184, v184, v185\n" \
M_"a[44:47], a[136:139], a[172:175], a[44:47]\n" \
X_"a[246:247], v37 offset:41984\n" \
C_"v185, v186, v187\n" \
M_"a[44:47], a[200:203], a[236:239], a[44:47]\n" \
C_"v186, v180, v181\n" \
C_"v187, v182, v183\n" \
M_"a[60:63], a[140:143], a[172:175], a[60:63]\n" \
"s_movk_i32 s84, 0x6100\n" \
M_"a[60:63], a[204:207], a[236:239], a[60:63]\n" \
B_"v25, s[76:79], s84 offen offset:3072 lds\n" \
M_"a[56:59], a[140:143], a[168:171], a[56:59]\n" \
X_"a[184:185], v38 offset:32768\n" \
"v_accvgpr_read_b32 v188, a8\n" \
M_"a[56:59], a[204:207], a[232:235], a[56:59]\n" \
"v_accvgpr_read_b32 v189, a9\n" \
"v_accvgpr_read_b32 v190, a10\n" \
M_"a[52:55], a[140:143], a[164:167], a[52:55]\n" \
"s_add_u32 m0, s30, 0x21000\n" \
"s_addk_i32 s84, 0x3000\n" \
M_"a[52:55], a[204:207], a[228:231], a[52:55]\n" \
B_"v24, s[76:79], s84 offen offset:0 lds\n" \
M_"a[48:51], a[140:143], a[160:163], a[48:51]\n" \
X_"a[186:187], v38 offset:33792\n" \
"v_accvgpr_read_b32 v191, a11\n" \
M_"a[48:51], a[204:207], a[224:227], a[48:51]\n" \
"v_accvgpr_read_b32 v180, a24\n" \
"v_accvgpr_read_b32 v181, a25\n" \
M_"a[64:67], a[144:147], a[160:163], a[64:67]\n" \
X_"a[248:249], v38 offset:40960\n" \
"v_accvgpr_read_b32 v182, a26\n" \
M_"a[64:67], a[208:211], a[224:227], a[64:67]\n" \
"s_addk_i32 s84, 0x2000\n" \
"s_nop 0\n" \
M_"a[68:71], a[144:147], a[164:167], a[68:71]\n" \
B_"v25, s[76:79], s84 offen offset:1024 lds\n" \
M_"a[68:71], a[208:211], a[228:231], a[68:71]\n" \
X_"a[250:251], v38 offset:41984\n" \
"v_accvgpr_read_b32 v183, a27\n" \
M_"a[72:75], a[144:147], a[168:171], a[72:75]\n" \
C_"v188, v188, v189\n" \
C_"v189, v190, v191\n" \
M_"a[72:75], a[208:211], a[232:235], a[72:75]\n" \
C_"v190, v180, v181\n" \
C_"v191, v182, v183\n" \
M_"a[76:79], a[144:147], a[172:175], a[76:79]\n" \
"s_addk_i32 s84, 0x2000\n" \
M_"a[76:79], a[208:211], a[236:239], a[76:79]\n" \
B_"v24, s[76:79], s84 offen offset:2048 lds\n" \
M_"a[92:95], a[148:151], a[172:175], a[92:95]\n" \
"s_add_u32 s89, s41, 0x0\n" \
"buffer_store_dwordx4 v[184:187], v20, s[80:83], s89 offen offset:3072 sc1\n" \
M_"a[92:95], a[212:215], a[236:239], a[92:95]\n" \
X_"a[188:189], v39 offset:32768\n" \
"v_accvgpr_read_b32 v192, a4\n" \
M_"a[88:91], a[148:151], a[168:171], a[88:91]\n" \
X_"a[190:191], v39 offset:33792\n" \
"v_accvgpr_read_b32 v193, a5\n" \
M_"a[88:91], a[212:215], a[232:235], a[88:91]\n" \
"s_addk_i32 s84, 0x2000\n" \
M_"a[84:87], a[148:151], a[164:167], a[84:87]\n" \
B_"v25, s[76:79], s84 offen offset:3072 lds\n" \
M_"a[84:87], a[212:215], a[228:231], a[84:87]\n" \
X_"a[252:253], v39 offset:40960\n" \
"v_accvgpr_read_b32 v194, a6\n" \
M_"a[80:83], a[148:151], a[160:163], a[80:83]\n" \
"s_add_u32 s88, s41, 0x0\n" \
"buffer_store_dwordx4 v[188:191], v20, s[80:83], s88 offen offset:2048 sc1\n" \
M_"a[80:83], a[212:215], a[224:227], a[80:83]\n" \
"v_accvgpr_read_b32 v195, a7\n" \
"v_accvgpr_read_b32 v180, a20\n" \
"s_waitcnt lgkmcnt(15)\n" \
"s_nop 0\n" \
M_"a[96:99], a[152:155], a[160:163], a[96:99]\n" \
X_"a[254:255], v39 offset:41984\n" \
"v_accvgpr_read_b32 v181, a21\n" \
M_"a[96:99], a[216:219], a[224:227], a[96:99]\n" \
"v_accvgpr_read_b32 v182, a22\n" \
"v_accvgpr_read_b32 v183, a23\n" \
M_"a[100:103], a[152:155], a[164:167], a[100:103]\n" \
C_"v192, v192, v193\n" \
C_"v193, v194, v195\n" \
M_"a[100:103], a[216:219], a[228:231], a[100:103]\n" \
C_"v194, v180, v181\n" \
C_"v195, v182, v183\n" \
M_"a[104:107], a[152:155], a[168:171], a[104:107]\n" \
"s_add_u32 s89, s41, 0x0\n" \
"s_nop 0\n" \
"buffer_store_dwordx4 v[192:195], v20, s[80:83], s89 offen offset:1024 sc1\n" \
M_"a[104:107], a[216:219], a[232:235], a[104:107]\n" \
"v_accvgpr_read_b32 v196, a0\n" \
"v_accvgpr_read_b32 v197, a1\n" \
M_"a[108:111], a[152:155], a[172:175], a[108:111]\n" \
"v_accvgpr_read_b32 v198, a2\n" \
"v_accvgpr_read_b32 v199, a3\n" \
M_"a[108:111], a[216:219], a[236:239], a[108:111]\n" \
"v_accvgpr_read_b32 v180, a16\n" \
"v_accvgpr_read_b32 v181, a17\n" \
M_"a[124:127], a[156:159], a[172:175], a[124:127]\n" \
"v_accvgpr_read_b32 v182, a18\n" \
"v_accvgpr_read_b32 v183, a19\n" \
M_"a[124:127], a[220:223], a[236:239], a[124:127]\n" \
C_"v196, v196, v197\n" \
C_"v197, v198, v199\n" \
M_"a[120:123], a[156:159], a[168:171], a[120:123]\n" \
C_"v198, v180, v181\n" \
C_"v199, v182, v183\n" \
"s_add_u32 s88, s41, 0x0\n" \
"s_nop 0\n" \
"buffer_store_dwordx4 v[196:199], v20, s[80:83], s88 offen offset:0 sc1\n" \
M_"a[120:123], a[220:223], a[232:235], a[120:123]\n" \
"v_accvgpr_read_b32 v200, a44\n" \
"v_accvgpr_read_b32 v201, a45\n" \
M_"a[116:119], a[156:159], a[164:167], a[116:119]\n" \
"v_accvgpr_read_b32 v202, a46\n" \
"v_accvgpr_read_b32 v203, a47\n" \
M_"a[116:119], a[220:223], a[228:231], a[116:119]\n" \
"v_accvgpr_read_b32 v180, a60\n" \
"v_accvgpr_read_b32 v181, a61\n" \
M_"a[112:115], a[156:159], a[160:163], a[112:115]\n" \
"v_accvgpr_read_b32 v182, a62\n" \
"v_accvgpr_read_b32 v183, a63\n" \
M_"a[112:115], a[220:223], a[224:227], a[112:115]\n" \
C_"v200, v200, v201\n" \
C_"v201, v202, v203\n" \
"s_waitcnt vmcnt(12) lgkmcnt(0)\n" \
"s_barrier\n" \
"v_xor_b32_e32 v36, 0x4000, v36\n" \
"v_xor_b32_e64 v32, 0x0, v48\n" \
"v_xor_b32_e32 v37, 0x4000, v37\n" \
"v_xor_b32_e64 v33, 0x20, v48\n" \
"v_xor_b32_e32 v38, 0x4000, v38\n" \
"v_xor_b32_e64 v34, 0x40, v48\n" \
"v_xor_b32_e32 v39, 0x4000, v39\n" \
"v_xor_b32_e32 v35, 0x60, v48\n" \
"v_add_u32_e32 v28, s43, v28\n" \
"v_add_u32_e32 v29, s43, v29\n" \
M_"v[116:119], a[144:147], a[176:179], v[116:119]\n" \
"s_add_u32 m0, s40, 0x8000\n" \
"s_add_u32 s85, s39, 0x2000\n" \
M_"v[116:119], a[208:211], a[240:243], v[116:119]\n" \
B_"v20, s[80:83], s85 offen sc1 lds\n" \
M_"v[120:123], a[144:147], a[180:183], v[120:123]\n" \
X_"a[160:161], v32 offset:0\n" \
C_"v202, v180, v181\n" \
M_"v[120:123], a[208:211], a[244:247], v[120:123]\n" \
"s_add_u32 m0, s40, 0x9000\n" \
"s_add_u32 s85, s39, 0x3000\n" \
M_"v[124:127], a[144:147], a[184:187], v[124:127]\n" \
B_"v20, s[80:83], s85 offen sc1 lds\n" \
M_"v[124:127], a[208:211], a[248:251], v[124:127]\n" \
X_"a[162:163], v32 offset:512\n" \
C_"v203, v182, v183\n" \
M_"v[128:131], a[144:147], a[188:191], v[128:131]\n" \
X_"a[224:225], v32 offset:4096\n" \
"v_accvgpr_read_b32 v204, a40\n" \
M_"v[128:131], a[208:211], a[252:255], v[128:131]\n" \
R_"a[144:147], v28 offset:8192\n" \
"v_accvgpr_read_b32 v205, a41\n" \
M_"v[144:147], a[148:151], a[188:191], v[144:147]\n" \
R_"a[208:211], v29 offset:8192\n" \
"v_accvgpr_read_b32 v206, a42\n" \
M_"v[144:147], a[212:215], a[252:255], v[144:147]\n" \
"s_add_u32 m0, s40, 0xb000\n" \
"s_movk_i32 s84, 0x80\n" \
M_"v[140:143], a[148:151], a[184:187], v[140:143]\n" \
B_"v21, s[76:79], s84 offen offset:0 lds\n" \
M_"v[140:143], a[212:215], a[248:251], v[140:143]\n" \
"s_add_u32 m0, s30, 0x8000\n" \
"s_movk_i32 s84, 0x80\n" \
M_"v[136:139], a[148:151], a[180:183], v[136:139]\n" \
B_"v24, s[76:79], s84 offen offset:0 lds\n" \
M_"v[136:139], a[212:215], a[244:247], v[136:139]\n" \
X_"a[226:227], v32 offset:4608\n" \
"v_accvgpr_read_b32 v207, a43\n" \
M_"v[132:135], a[148:151], a[176:179], v[132:135]\n" \
X_"a[164:165], v33 offset:0\n" \
"v_accvgpr_read_b32 v180, a56\n" \
M_"v[132:135], a[212:215], a[240:243], v[132:135]\n" \
R_"a[148:151], v28 offset:10240\n" \
"v_accvgpr_read_b32 v181, a57\n" \
M_"v[148:151], a[152:155], a[176:179], v[148:151]\n" \
R_"a[212:215], v29 offset:10240\n" \
"v_accvgpr_read_b32 v182, a58\n" \
M_"v[148:151], a[216:219], a[240:243], v[148:151]\n" \
"s_movk_i32 s84, 0x2080\n" \
M_"v[152:155], a[152:155], a[180:183], v[152:155]\n" \
B_"v25, s[76:79], s84 offen offset:1024 lds\n" \
M_"v[152:155], a[216:219], a[244:247], v[152:155]\n" \
"s_movk_i32 s84, 0x4080\n" \
M_"v[156:159], a[152:155], a[184:187], v[156:159]\n" \
B_"v24, s[76:79], s84 offen offset:2048 lds\n" \
M_"v[156:159], a[216:219], a[248:251], v[156:159]\n" \
X_"a[166:167], v33 offset:512\n" \
"v_accvgpr_read_b32 v183, a59\n" \
M_"v[160:163], a[152:155], a[188:191], v[160:163]\n" \
X_"a[228:229], v33 offset:4096\n" \
C_"v204, v204, v205\n" \
M_"v[160:163], a[216:219], a[252:255], v[160:163]\n" \
R_"a[152:155], v28 offset:12288\n" \
C_"v205, v206, v207\n" \
M_"v[176:179], a[156:159], a[188:191], v[176:179]\n" \
R_"a[216:219], v29 offset:12288\n" \
C_"v206, v180, v181\n" \
M_"v[176:179], a[220:223], a[252:255], v[176:179]\n" \
"s_movk_i32 s84, 0x6080\n" \
M_"v[172:175], a[156:159], a[184:187], v[172:175]\n" \
B_"v25, s[76:79], s84 offen offset:3072 lds\n" \
M_"v[172:175], a[220:223], a[248:251], v[172:175]\n" \
"s_add_u32 m0, s31, 0x8000\n" \
"s_mov_b32 s84, 0x80000\n" \
M_"v[168:171], a[156:159], a[180:183], v[168:171]\n" \
B_"v26, s[68:71], s84 offen offset:0 lds\n" \
M_"v[168:171], a[220:223], a[244:247], v[168:171]\n" \
"s_addk_i32 s84, 0x7c00\n" \
M_"v[164:167], a[156:159], a[176:179], v[164:167]\n" \
B_"v26, s[68:71], s84 offen offset:1024 lds\n" \
M_"v[164:167], a[220:223], a[240:243], v[164:167]\n" \
R_"a[156:159], v28 offset:14336\n" \
C_"v207, v182, v183\n" \
M_"v[84:87], a[136:139], a[176:179], v[84:87]\n" \
R_"a[220:223], v29 offset:14336\n" \
"v_accvgpr_read_b32 v208, a36\n" \
M_"v[84:87], a[200:203], a[240:243], v[84:87]\n" \
"s_addk_i32 s84, 0x7c00\n" \
M_"v[88:91], a[136:139], a[180:183], v[88:91]\n" \
B_"v27, s[68:71], s84 offen offset:2048 lds\n" \
M_"v[88:91], a[200:203], a[244:247], v[88:91]\n" \
X_"a[230:231], v33 offset:4608\n" \
"v_accvgpr_read_b32 v209, a37\n" \
M_"v[92:95], a[136:139], a[184:187], v[92:95]\n" \
"s_addk_i32 s84, 0x7c00\n" \
M_"v[92:95], a[200:203], a[248:251], v[92:95]\n" \
B_"v27, s[68:71], s84 offen offset:3072 lds\n" \
M_"v[96:99], a[136:139], a[188:191], v[96:99]\n" \
X_"a[172:173], v35 offset:0\n" \
"v_accvgpr_read_b32 v210, a38\n" \
M_"v[96:99], a[200:203], a[252:255], v[96:99]\n" \
R_"a[136:139], v28 offset:4096\n" \
"v_accvgpr_read_b32 v211, a39\n" \
M_"v[112:115], a[140:143], a[188:191], v[112:115]\n" \
R_"a[200:203], v29 offset:4096\n" \
"v_accvgpr_read_b32 v180, a52\n" \
M_"v[112:115], a[204:207], a[252:255], v[112:115]\n" \
"s_add_u32 m0, s30, 0x9000\n" \
"s_mov_b32 s84, 0x9080\n" \
M_"v[108:111], a[140:143], a[184:187], v[108:111]\n" \
B_"v24, s[76:79], s84 offen offset:0 lds\n" \
M_"v[108:111], a[204:207], a[248:251], v[108:111]\n" \
X_"a[174:175], v35 offset:512\n" \
"v_accvgpr_read_b32 v181, a53\n" \
M_"v[104:107], a[140:143], a[180:183], v[104:107]\n" \
"s_addk_i32 s84, 0x2000\n" \
"s_nop 0\n" \
M_"v[104:107], a[204:207], a[244:247], v[104:107]\n" \
B_"v25, s[76:79], s84 offen offset:1024 lds\n" \
M_"v[100:103], a[140:143], a[176:179], v[100:103]\n" \
X_"a[236:237], v35 offset:4096\n" \
"v_accvgpr_read_b32 v182, a54\n" \
M_"v[100:103], a[204:207], a[240:243], v[100:103]\n" \
R_"a[140:143], v28 offset:6144\n" \
"v_accvgpr_read_b32 v183, a55\n" \
M_"v[52:55], a[128:131], a[176:179], v[52:55]\n" \
R_"a[204:207], v29 offset:6144\n" \
C_"v208, v208, v209\n" \
M_"v[52:55], a[192:195], a[240:243], v[52:55]\n" \
X_"a[238:239], v35 offset:4608\n" \
C_"v209, v210, v211\n" \
M_"v[56:59], a[128:131], a[180:183], v[56:59]\n" \
X_"a[168:169], v34 offset:0\n" \
C_"v210, v180, v181\n" \
M_"v[56:59], a[192:195], a[244:247], v[56:59]\n" \
X_"a[170:171], v34 offset:512\n" \
C_"v211, v182, v183\n" \
M_"v[60:63], a[128:131], a[184:187], v[60:63]\n" \
X_"a[232:233], v34 offset:4096\n" \
"v_accvgpr_read_b32 v212, a32\n" \
M_"v[60:63], a[192:195], a[248:251], v[60:63]\n" \
X_"a[234:235], v34 offset:4608\n" \
"v_accvgpr_read_b32 v213, a33\n" \
M_"v[64:67], a[128:131], a[188:191], v[64:67]\n" \
"v_accvgpr_read_b32 v214, a34\n" \
"v_accvgpr_read_b32 v215, a35\n" \
M_"v[64:67], a[192:195], a[252:255], v[64:67]\n" \
R_"a[128:131], v28 offset:0\n" \
"v_accvgpr_read_b32 v180, a48\n" \
M_"v[80:83], a[132:135], a[188:191], v[80:83]\n" \
R_"a[192:195], v29 offset:0\n" \
"v_accvgpr_read_b32 v181, a49\n" \
M_"v[80:83], a[196:199], a[252:255], v[80:83]\n" \
"v_accvgpr_read_b32 v182, a50\n" \
"v_accvgpr_read_b32 v183, a51\n" \
M_"v[76:79], a[132:135], a[184:187], v[76:79]\n" \
C_"v212, v212, v213\n" \
C_"v213, v214, v215\n" \
M_"v[76:79], a[196:199], a[248:251], v[76:79]\n" \
C_"v214, v180, v181\n" \
C_"v215, v182, v183\n" \
M_"v[72:75], a[132:135], a[180:183], v[72:75]\n" \
"v_accvgpr_read_b32 v216, a68\n" \
"v_accvgpr_read_b32 v217, a69\n" \
M_"v[72:75], a[196:199], a[244:247], v[72:75]\n" \
"v_accvgpr_read_b32 v218, a70\n" \
"v_accvgpr_read_b32 v219, a71\n" \
M_"v[68:71], a[132:135], a[176:179], v[68:71]\n" \
"v_accvgpr_read_b32 v180, a84\n" \
"v_accvgpr_read_b32 v181, a85\n" \
M_"v[68:71], a[196:199], a[240:243], v[68:71]\n" \
R_"a[132:135], v28 offset:2048\n" \
R_"a[196:199], v29 offset:2048\n" \
"s_waitcnt lgkmcnt(15)\n" \
M_"a[0:3], a[160:163], a[144:147], 0\n" \
"s_addk_i32 s84, 0x2000\n" \
"v_accvgpr_read_b32 v182, a86\n" \
M_"a[0:3], a[224:227], a[208:211], a[0:3]\n" \
B_"v24, s[76:79], s84 offen offset:2048 lds\n" \
"v_accvgpr_read_b32 v183, a87\n" \
M_"a[4:7], a[164:167], a[144:147], 0\n" \
X_"a[176:177], v36 offset:0\n" \
C_"v216, v216, v217\n" \
M_"a[4:7], a[228:231], a[208:211], a[4:7]\n" \
"s_addk_i32 s84, 0x2000\n" \
C_"v217, v218, v219\n" \
"s_waitcnt lgkmcnt(13)\n" \
M_"a[12:15], a[172:175], a[144:147], 0\n" \
B_"v25, s[76:79], s84 offen offset:3072 lds\n" \
C_"v218, v180, v181\n" \
"s_waitcnt lgkmcnt(9)\n" \
M_"a[12:15], a[236:239], a[208:211], a[12:15]\n" \
".fill (L_pb + 61152 - .) / 4, 4, 0xBF800000\n" \
"L_pp15:\n" \
"s_cbranch_execz L_pret\n" \
X_"a[178:179], v36 offset:1024\n" \
C_"v219, v182, v183\n" \
"s_waitcnt lgkmcnt(8)\n" \
M_"a[8:11], a[144:147], a[168:171], 0\n" \
X_"a[240:241], v36 offset:8192\n" \
"v_accvgpr_read_b32 v220, a64\n" \
"s_waitcnt lgkmcnt(7)\n" \
"s_nop 0\n" \
M_"a[8:11], a[208:211], a[232:235], a[8:11]\n" \
X_"a[242:243], v36 offset:9216\n" \
"v_accvgpr_read_b32 v221, a65\n" \
M_"a[24:27], a[148:151], a[168:171], 0\n" \
X_"a[180:181], v37 offset:0\n" \
"v_accvgpr_read_b32 v222, a66\n" \
M_"a[24:27], a[212:215], a[232:235], a[24:27]\n" \
X_"a[182:183], v37 offset:1024\n" \
"v_accvgpr_read_b32 v223, a67\n" \
M_"a[28:31], a[172:175], a[148:151], 0\n" \
X_"a[244:245], v37 offset:8192\n" \
"v_accvgpr_read_b32 v180, a80\n" \
M_"a[28:31], a[236:239], a[212:215], a[28:31]\n" \
"s_add_u32 s89, s41, 0x2000\n" \
"buffer_store_dwordx4 v[200:203], v20, s[80:83], s89 offen offset:3072 sc1\n" \
M_"a[20:23], a[164:167], a[148:151], 0\n" \
X_"a[246:247], v37 offset:9216\n" \
"v_accvgpr_read_b32 v181, a81\n" \
M_"a[20:23], a[228:231], a[212:215], a[20:23]\n" \
X_"a[184:185], v38 offset:0\n" \
"v_accvgpr_read_b32 v182, a82\n" \
M_"a[16:19], a[160:163], a[148:151], 0\n" \
X_"a[186:187], v38 offset:1024\n" \
"v_accvgpr_read_b32 v183, a83\n" \
M_"a[16:19], a[224:227], a[212:215], a[16:19]\n" \
C_"v220, v220, v221\n" \
C_"v221, v222, v223\n" \
C_"v222, v180, v181\n" \
M_"a[32:35], a[160:163], a[152:155], 0\n" \
"s_add_u32 s88, s41, 0x2000\n" \
"buffer_store_dwordx4 v[204:207], v20, s[80:83], s88 offen offset:2048 sc1\n" \
M_"a[32:35], a[224:227], a[216:219], a[32:35]\n" \
X_"a[248:249], v38 offset:8192\n" \
C_"v223, v182, v183\n" \
M_"a[36:39], a[164:167], a[152:155], 0\n" \
X_"a[250:251], v38 offset:9216\n" \
C_"v224, v128, v129\n" \
M_"a[36:39], a[228:231], a[216:219], a[36:39]\n" \
X_"a[188:189], v39 offset:0\n" \
C_"v225, v130, v131\n" \
M_"a[44:47], a[172:175], a[152:155], 0\n" \
C_"v226, v144, v145\n" \
C_"v227, v146, v147\n" \
C_"v228, v124, v125\n" \
M_"a[44:47], a[236:239], a[216:219], a[44:47]\n" \
X_"a[190:191], v39 offset:1024\n" \
C_"v229, v126, v127\n" \
M_"a[40:43], a[152:155], a[168:171], 0\n" \
"s_add_u32 s89, s41, 0x2000\n" \
"buffer_store_dwordx4 v[208:211], v20, s[80:83], s89 offen offset:1024 sc1\n" \
M_"a[40:43], a[216:219], a[232:235], a[40:43]\n" \
X_"a[252:253], v39 offset:8192\n" \
C_"v230, v140, v141\n" \
M_"a[56:59], a[156:159], a[168:171], 0\n" \
X_"a[254:255], v39 offset:9216\n" \
C_"v231, v142, v143\n" \
M_"a[56:59], a[220:223], a[232:235], a[56:59]\n" \
C_"v232, v120, v121\n" \
C_"v233, v122, v123\n" \
C_"v234, v136, v137\n" \
M_"a[60:63], a[172:175], a[156:159], 0\n" \
C_"v235, v138, v139\n" \
C_"v236, v116, v117\n" \
C_"v237, v118, v119\n" \
M_"a[60:63], a[236:239], a[220:223], a[60:63]\n" \
"s_add_u32 s88, s41, 0x2000\n" \
"buffer_store_dwordx4 v[212:215], v20, s[80:83], s88 offen offset:0 sc1\n" \
M_"a[52:55], a[164:167], a[156:159], 0\n" \
C_"v238, v132, v133\n" \
C_"v239, v134, v135\n" \
C_"v240, v160, v161\n" \
M_"a[52:55], a[228:231], a[220:223], a[52:55]\n" \
C_"v241, v162, v163\n" \
C_"v242, v176, v177\n" \
C_"v243, v178, v179\n" \
M_"a[48:51], a[160:163], a[156:159], 0\n" \
C_"v244, v156, v157\n" \
C_"v245, v158, v159\n" \
C_"v246, v172, v173\n" \
M_"a[48:51], a[224:227], a[220:223], a[48:51]\n" \
C_"v247, v174, v175\n" \
C_"v184, v152, v153\n" \
C_"v185, v154, v155\n" \
M_"a[64:67], a[164:167], a[140:143], 0\n" \
C_"v186, v168, v169\n" \
C_"v187, v170, v171\n" \
C_"v188, v148, v149\n" \
M_"a[64:67], a[228:231], a[204:207], a[64:67]\n" \
C_"v189, v150, v151\n" \
C_"v190, v164, v165\n" \
C_"v191, v166, v167\n" \
M_"a[80:83], a[172:175], a[140:143], 0\n" \
C_"v192, v96, v97\n" \
C_"v193, v98, v99\n" \
C_"v194, v112, v113\n" \
M_"a[80:83], a[236:239], a[204:207], a[80:83]\n" \
C_"v195, v114, v115\n" \
C_"v196, v92, v93\n" \
C_"v197, v94, v95\n" \
M_"a[68:71], a[140:143], a[168:171], 0\n" \
C_"v198, v108, v109\n" \
C_"v199, v110, v111\n" \
C_"v252, v88, v89\n" \
M_"a[68:71], a[204:207], a[232:235], a[68:71]\n" \
C_"v253, v90, v91\n" \
C_"v254, v104, v105\n" \
C_"v255, v106, v107\n" \
"s_waitcnt vmcnt(4) lgkmcnt(0)\n" \
"s_barrier\n" \
M_"v[116:119], a[176:179], a[144:147], 0\n" \
"s_add_u32 m0, s40, 0x0\n" \
"s_nop 0\n" \
"s_add_u32 s85, s39, 0x4000\n" \
"v_accvgpr_write_b32 a84, v252\n" \
M_"v[116:119], a[240:243], a[208:211], v[116:119]\n" \
B_"v20, s[80:83], s85 offen sc1 lds\n" \
"v_accvgpr_write_b32 a85, v253\n" \
M_"v[120:123], a[180:183], a[144:147], 0\n" \
X_"a[160:161], v32 offset:32768\n" \
"v_accvgpr_write_b32 a86, v254\n" \
M_"v[120:123], a[244:247], a[208:211], v[120:123]\n" \
X_"a[162:163], v32 offset:33280\n" \
"v_accvgpr_write_b32 a87, v255\n" \
M_"v[124:127], a[184:187], a[144:147], 0\n" \
"s_add_u32 m0, s40, 0x1000\n" \
"s_add_u32 s85, s39, 0x5000\n" \
C_"v252, v84, v85\n" \
M_"v[124:127], a[248:251], a[208:211], v[124:127]\n" \
B_"v20, s[80:83], s85 offen sc1 lds\n" \
C_"v253, v86, v87\n" \
M_"v[128:131], a[188:191], a[144:147], 0\n" \
X_"a[224:225], v32 offset:36864\n" \
C_"v254, v100, v101\n" \
M_"v[128:131], a[252:255], a[208:211], v[128:131]\n" \
R_"a[144:147], v28 offset:40960\n" \
C_"v255, v102, v103\n" \
M_"v[144:147], a[188:191], a[148:151], 0\n" \
R_"a[208:211], v29 offset:40960\n" \
C_"v200, v64, v65\n" \
M_"v[144:147], a[252:255], a[212:215], v[144:147]\n" \
"s_add_u32 m0, s40, 0x3000\n" \
"s_movk_i32 s84, 0x100\n" \
"s_nop 0\n" \
C_"v201, v66, v67\n" \
M_"v[140:143], a[184:187], a[148:151], 0\n" \
B_"v21, s[76:79], s84 offen offset:0 lds\n" \
C_"v202, v80, v81\n" \
M_"v[140:143], a[248:251], a[212:215], v[140:143]\n" \
X_"a[226:227], v32 offset:37376\n" \
C_"v203, v82, v83\n" \
M_"v[136:139], a[180:183], a[148:151], 0\n" \
C_"v204, v60, v61\n" \
C_"v205, v62, v63\n" \
C_"v206, v76, v77\n" \
M_"v[136:139], a[244:247], a[212:215], v[136:139]\n" \
X_"a[164:165], v33 offset:32768\n" \
C_"v207, v78, v79\n" \
M_"v[132:135], a[176:179], a[148:151], 0\n" \
C_"v208, v56, v57\n" \
C_"v209, v58, v59\n" \
C_"v210, v72, v73\n" \
M_"v[132:135], a[240:243], a[212:215], v[132:135]\n" \
R_"a[148:151], v28 offset:43008\n" \
C_"v211, v74, v75\n" \
M_"v[148:151], a[176:179], a[152:155], 0\n" \
R_"a[212:215], v29 offset:43008\n" \
C_"v212, v52, v53\n" \
M_"v[148:151], a[240:243], a[216:219], v[148:151]\n" \
"s_add_u32 m0, s31, 0x0\n" \
"s_mov_b32 s84, 0x100000\n" \
C_"v213, v54, v55\n" \
M_"v[152:155], a[180:183], a[152:155], 0\n" \
B_"v26, s[68:71], s84 offen offset:0 lds\n" \
C_"v214, v68, v69\n" \
M_"v[152:155], a[244:247], a[216:219], v[152:155]\n" \
"s_addk_i32 s84, 0x7c00\n" \
C_"v215, v70, v71\n" \
M_"v[156:159], a[184:187], a[152:155], 0\n" \
B_"v26, s[68:71], s84 offen offset:1024 lds\n" \
M_"v[156:159], a[248:251], a[216:219], v[156:159]\n" \
X_"a[166:167], v33 offset:33280\n" \
M_"v[160:163], a[188:191], a[152:155], 0\n" \
X_"a[228:229], v33 offset:36864\n" \
M_"v[160:163], a[252:255], a[216:219], v[160:163]\n" \
R_"a[152:155], v28 offset:45056\n" \
M_"v[176:179], a[188:191], a[156:159], 0\n" \
R_"a[216:219], v29 offset:45056\n" \
M_"v[176:179], a[252:255], a[220:223], v[176:179]\n" \
"s_addk_i32 s84, 0x7c00\n" \
M_"v[172:175], a[184:187], a[156:159], 0\n" \
B_"v27, s[68:71], s84 offen offset:2048 lds\n" \
M_"v[172:175], a[248:251], a[220:223], v[172:175]\n" \
X_"a[230:231], v33 offset:37376\n" \
M_"v[168:171], a[180:183], a[156:159], 0\n" \
"s_addk_i32 s84, 0x7c00\n" \
M_"v[168:171], a[244:247], a[220:223], v[168:171]\n" \
B_"v27, s[68:71], s84 offen offset:3072 lds\n" \
M_"v[164:167], a[176:179], a[156:159], 0\n" \
X_"a[172:173], v35 offset:32768\n" \
M_"v[164:167], a[240:243], a[220:223], v[164:167]\n" \
R_"a[156:159], v28 offset:47104\n" \
M_"v[84:87], a[176:179], a[136:139], 0\n" \
R_"a[220:223], v29 offset:47104\n" \
M_"v[84:87], a[240:243], a[200:203], v[84:87]\n" \
"s_add_u32 s89, s41, 0x3000\n" \
"buffer_store_dwordx4 v[192:195], v20, s[80:83], s89 offen offset:3072 sc1\n" \
M_"v[88:91], a[180:183], a[136:139], 0\n" \
X_"a[174:175], v35 offset:33280\n" \
M_"v[88:91], a[244:247], a[200:203], v[88:91]\n" \
X_"a[236:237], v35 offset:36864\n" \
M_"v[92:95], a[184:187], a[136:139], 0\n" \
X_"a[238:239], v35 offset:37376\n" \
M_"v[92:95], a[248:251], a[200:203], v[92:95]\n" \
"s_add_u32 m0, s30, 0x0\n" \
"s_movk_i32 s84, 0x180\n" \
M_"v[96:99], a[188:191], a[136:139], 0\n" \
B_"v24, s[76:79], s84 offen offset:0 lds\n" \
M_"v[96:99], a[252:255], a[200:203], v[96:99]\n" \
R_"a[136:139], v28 offset:36864\n" \
M_"v[112:115], a[188:191], a[140:143], 0\n" \
R_"a[200:203], v29 offset:36864\n" \
M_"v[112:115], a[252:255], a[204:207], v[112:115]\n" \
X_"a[168:169], v34 offset:32768\n" \
M_"v[108:111], a[184:187], a[140:143], 0\n" \
"s_movk_i32 s84, 0x2180\n" \
"s_nop 0\n" \
M_"v[108:111], a[248:251], a[204:207], v[108:111]\n" \
B_"v25, s[76:79], s84 offen offset:1024 lds\n" \
M_"v[104:107], a[180:183], a[140:143], 0\n" \
X_"a[170:171], v34 offset:33280\n" \
M_"v[104:107], a[244:247], a[204:207], v[104:107]\n" \
X_"a[232:233], v34 offset:36864\n" \
M_"v[100:103], a[176:179], a[140:143], 0\n" \
M_"v[100:103], a[240:243], a[204:207], v[100:103]\n" \
R_"a[140:143], v28 offset:38912\n" \
M_"v[52:55], a[176:179], a[128:131], 0\n" \
R_"a[204:207], v29 offset:38912\n" \
M_"v[52:55], a[240:243], a[192:195], v[52:55]\n" \
"s_movk_i32 s84, 0x4180\n" \
M_"v[56:59], a[180:183], a[128:131], 0\n" \
B_"v24, s[76:79], s84 offen offset:2048 lds\n" \
M_"v[56:59], a[244:247], a[192:195], v[56:59]\n" \
"s_movk_i32 s84, 0x6180\n" \
M_"v[60:63], a[184:187], a[128:131], 0\n" \
B_"v25, s[76:79], s84 offen offset:3072 lds\n" \
M_"v[60:63], a[248:251], a[192:195], v[60:63]\n" \
X_"a[234:235], v34 offset:37376\n" \
M_"v[64:67], a[188:191], a[128:131], 0\n" \
M_"v[64:67], a[252:255], a[192:195], v[64:67]\n" \
R_"a[128:131], v28 offset:32768\n" \
M_"v[80:83], a[188:191], a[132:135], 0\n" \
R_"a[192:195], v29 offset:32768\n" \
M_"v[80:83], a[252:255], a[196:199], v[80:83]\n" \
M_"v[76:79], a[184:187], a[132:135], 0\n" \
M_"v[76:79], a[248:251], a[196:199], v[76:79]\n" \
M_"v[72:75], a[180:183], a[132:135], 0\n" \
M_"v[72:75], a[244:247], a[196:199], v[72:75]\n" \
M_"v[68:71], a[176:179], a[132:135], 0\n" \
M_"v[68:71], a[240:243], a[196:199], v[68:71]\n" \
R_"a[132:135], v28 offset:34816\n" \
R_"a[196:199], v29 offset:34816\n" \
"s_waitcnt lgkmcnt(15)\n" \
M_"a[0:3], a[160:163], a[144:147], a[0:3]\n" \
"s_add_u32 m0, s30, 0x1000\n" \
"s_addk_i32 s84, 0x3000\n" \
M_"a[0:3], a[224:227], a[208:211], a[0:3]\n" \
B_"v24, s[76:79], s84 offen offset:0 lds\n" \
M_"a[4:7], a[164:167], a[144:147], a[4:7]\n" \
X_"a[176:177], v36 offset:32768\n" \
"v_mov_b32_e64 v11, 0\n" \
M_"a[4:7], a[228:231], a[208:211], a[4:7]\n" \
X_"a[178:179], v36 offset:33792\n" \
"s_and_b32 s5, s2, 7\n" \
"s_waitcnt lgkmcnt(15)\n" \
M_"a[12:15], a[172:175], a[144:147], a[12:15]\n" \
X_"a[240:241], v36 offset:40960\n" \
"s_lshl_b32 s6, s3, 1\n" \
"s_waitcnt lgkmcnt(15)\n" \
M_"a[12:15], a[236:239], a[208:211], a[12:15]\n" \
"s_add_u32 s88, s41, 0x3000\n" \
"buffer_store_dwordx4 v[196:199], v20, s[80:83], s88 offen offset:2048 sc1\n" \
"s_waitcnt lgkmcnt(11)\n" \
M_"a[8:11], a[144:147], a[168:171], a[8:11]\n" \
X_"a[242:243], v36 offset:41984\n" \
"s_lshl_b32 s4, s5, 2\n" \
"s_waitcnt lgkmcnt(8)\n" \
M_"a[8:11], a[208:211], a[232:235], a[8:11]\n" \
X_"a[180:181], v37 offset:32768\n" \
"s_bfe_u32 s7, s2, 0x20003\n" \
M_"a[24:27], a[148:151], a[168:171], a[24:27]\n" \
X_"a[182:183], v37 offset:33792\n" \
"s_or_b32 s12, s4, s7\n" \
M_"a[24:27], a[212:215], a[232:235], a[24:27]\n" \
X_"a[244:245], v37 offset:40960\n" \
"s_and_b32 s6, s6, 4\n" \
M_"a[28:31], a[172:175], a[148:151], a[28:31]\n" \
"s_and_b32 s8, s3, 1\n" \
"s_or_b32 s8, s6, s8\n" \
M_"a[28:31], a[236:239], a[212:215], a[28:31]\n" \
"s_addk_i32 s84, 0x2000\n" \
M_"a[20:23], a[164:167], a[148:151], a[20:23]\n" \
B_"v25, s[76:79], s84 offen offset:1024 lds\n" \
M_"a[20:23], a[228:231], a[212:215], a[20:23]\n" \
X_"a[246:247], v37 offset:41984\n" \
"s_lshl_b32 s6, s12, 4\n" \
M_"a[16:19], a[160:163], a[148:151], a[16:19]\n" \
X_"a[184:185], v38 offset:32768\n" \
"s_lshl_b32 s9, s3, 2\n" \
M_"a[16:19], a[224:227], a[212:215], a[16:19]\n" \
X_"a[186:187], v38 offset:33792\n" \
"s_or_b32 s6, s6, s9\n" \
M_"a[32:35], a[160:163], a[152:155], a[32:35]\n" \
"s_addk_i32 s84, 0x2000\n" \
M_"a[32:35], a[224:227], a[216:219], a[32:35]\n" \
B_"v24, s[76:79], s84 offen offset:2048 lds\n" \
M_"a[36:39], a[164:167], a[152:155], a[36:39]\n" \
X_"a[248:249], v38 offset:40960\n" \
"s_lshr_b32 s9, s2, 5\n" \
M_"a[36:39], a[228:231], a[216:219], a[36:39]\n" \
X_"a[250:251], v38 offset:41984\n" \
"s_mul_i32 s10, s9, 0xa00\n" \
M_"a[44:47], a[172:175], a[152:155], a[44:47]\n" \
X_"a[188:189], v39 offset:32768\n" \
"s_add_i32 s10, s10, 0x5bc1000\n" \
M_"a[44:47], a[236:239], a[216:219], a[44:47]\n" \
X_"a[190:191], v39 offset:33792\n" \
"s_or_b32 s6, s6, s10\n" \
M_"a[40:43], a[152:155], a[168:171], a[40:43]\n" \
"s_add_u32 s89, s41, 0x3000\n" \
"buffer_store_dwordx4 a[84:87], v20, s[80:83], s89 offen offset:1024 sc1\n" \
M_"a[40:43], a[216:219], a[232:235], a[40:43]\n" \
X_"a[252:253], v39 offset:40960\n" \
"s_lshl_b32 s13, s9, 3\n" \
M_"a[56:59], a[156:159], a[168:171], a[56:59]\n" \
X_"a[254:255], v39 offset:41984\n" \
"v_writelane_b32 v11, s6, 0\n" \
M_"a[56:59], a[220:223], a[232:235], a[56:59]\n" \
"s_or_b32 s6, s13, s5\n" \
"v_writelane_b32 v11, s10, 1\n" \
M_"a[60:63], a[172:175], a[156:159], a[60:63]\n" \
"s_lshl_b32 s6, s6, 10\n" \
"s_lshl_b32 s10, s3, 8\n" \
M_"a[60:63], a[236:239], a[220:223], a[60:63]\n" \
"s_addk_i32 s84, 0x2000\n" \
M_"a[52:55], a[164:167], a[156:159], a[52:55]\n" \
B_"v25, s[76:79], s84 offen offset:3072 lds\n" \
M_"a[52:55], a[228:231], a[220:223], a[52:55]\n" \
"s_or_b32 s6, s6, s10\n" \
"s_or_b32 s10, s6, 0x5c00200\n" \
M_"a[48:51], a[160:163], a[156:159], a[48:51]\n" \
"v_writelane_b32 v11, s10, 2\n" \
"s_lshl_b32 s10, s7, 2\n" \
M_"a[48:51], a[224:227], a[220:223], a[48:51]\n" \
"s_or_b32 s11, s10, s6\n" \
"s_or_b32 s11, s11, 0x5c00000\n" \
M_"a[64:67], a[164:167], a[140:143], a[64:67]\n" \
"v_writelane_b32 v11, s11, 3\n" \
"s_or_b32 s6, s6, 0x5c00080\n" \
M_"a[64:67], a[228:231], a[204:207], a[64:67]\n" \
"v_writelane_b32 v11, s6, 4\n" \
"s_or_b32 s6, s6, s10\n" \
M_"a[80:83], a[172:175], a[140:143], a[80:83]\n" \
"s_add_i32 s10, s3, s2\n" \
"v_writelane_b32 v11, s6, 5\n" \
M_"a[80:83], a[236:239], a[204:207], a[80:83]\n" \
"s_mov_b32 s6, 2.0\n" \
"s_mov_b32 s11, 0x5bc0000\n" \
M_"a[68:71], a[140:143], a[168:171], a[68:71]\n" \
"s_cmp_lg_u32 s10, 0\n" \
"s_cselect_b32 s10, s6, s11\n" \
"s_waitcnt vmcnt(11) lgkmcnt(0)\n" \
M_"a[68:71], a[204:207], a[232:235], a[68:71]\n" \
"s_barrier\n" \
"v_xor_b32_e32 v28, 0x20000, v28\n" \
"v_xor_b32_e32 v29, 0x20000, v29\n" \
M_"v[116:119], a[176:179], a[144:147], v[116:119]\n" \
"s_add_u32 m0, s40, 0x8000\n" \
"s_add_u32 s85, s39, 0x6000\n" \
M_"v[116:119], a[240:243], a[208:211], v[116:119]\n" \
B_"v20, s[80:83], s85 offen sc1 lds\n" \
M_"v[120:123], a[180:183], a[144:147], v[120:123]\n" \
X_"a[160:161], v32 offset:0\n" \
"s_lshr_b32 s14, s2, 3\n" \
M_"v[120:123], a[244:247], a[208:211], v[120:123]\n" \
X_"a[162:163], v32 offset:512\n" \
"v_writelane_b32 v11, s10, 57\n" \
M_"v[124:127], a[184:187], a[144:147], v[124:127]\n" \
"s_add_u32 m0, s40, 0x9000\n" \
"s_add_u32 s85, s39, 0x7000\n" \
M_"v[124:127], a[248:251], a[208:211], v[124:127]\n" \
B_"v20, s[80:83], s85 offen sc1 lds\n" \
M_"v[128:131], a[188:191], a[144:147], v[128:131]\n" \
X_"a[224:225], v32 offset:4096\n" \
"s_lshl_b32 s10, s5, 5\n" \
M_"v[128:131], a[252:255], a[208:211], v[128:131]\n" \
R_"a[144:147], v28 offset:8192\n" \
"s_min_u32 s15, s3, 2\n" \
M_"v[144:147], a[188:191], a[148:151], v[144:147]\n" \
R_"a[208:211], v29 offset:8192\n" \
"s_mul_i32 s11, s7, 0x120\n" \
M_"v[144:147], a[252:255], a[212:215], v[144:147]\n" \
"s_add_u32 m0, s40, 0xb000\n" \
"s_movk_i32 s84, 0x180\n" \
M_"v[140:143], a[184:187], a[148:151], v[140:143]\n" \
B_"v21, s[76:79], s84 offen offset:0 lds\n" \
M_"v[140:143], a[248:251], a[212:215], v[140:143]\n" \
X_"a[226:227], v32 offset:4608\n" \
M_"v[136:139], a[180:183], a[148:151], v[136:139]\n" \
"s_add_u32 s88, s41, 0x3000\n" \
"buffer_store_dwordx4 v[252:255], v20, s[80:83], s88 offen offset:0 sc1\n" \
M_"v[136:139], a[244:247], a[212:215], v[136:139]\n" \
X_"a[164:165], v33 offset:0\n" \
M_"v[132:135], a[176:179], a[148:151], v[132:135]\n" \
"s_add_u32 s16, s22, s11\n" \
"s_addc_u32 s17, s23, 0\n" \
"s_nop 0\n" \
M_"v[132:135], a[240:243], a[212:215], v[132:135]\n" \
R_"a[148:151], v28 offset:10240\n" \
M_"v[148:151], a[176:179], a[152:155], v[148:151]\n" \
R_"a[212:215], v29 offset:10240\n" \
M_"v[148:151], a[240:243], a[216:219], v[148:151]\n" \
"s_add_u32 m0, s31, 0x8000\n" \
"s_mov_b32 s84, 0x180000\n" \
M_"v[152:155], a[180:183], a[152:155], v[152:155]\n" \
B_"v26, s[68:71], s84 offen offset:0 lds\n" \
M_"v[152:155], a[244:247], a[216:219], v[152:155]\n" \
X_"a[166:167], v33 offset:512\n" \
M_"v[156:159], a[184:187], a[152:155], v[156:159]\n" \
X_"a[228:229], v33 offset:4096\n" \
M_"v[156:159], a[248:251], a[216:219], v[156:159]\n" \
"s_addk_i32 s84, 0x7c00\n" \
M_"v[160:163], a[188:191], a[152:155], v[160:163]\n" \
B_"v26, s[68:71], s84 offen offset:1024 lds\n" \
M_"v[160:163], a[252:255], a[216:219], v[160:163]\n" \
R_"a[152:155], v28 offset:12288\n" \
M_"v[176:179], a[188:191], a[156:159], v[176:179]\n" \
R_"a[216:219], v29 offset:12288\n" \
M_"v[176:179], a[252:255], a[220:223], v[176:179]\n" \
"s_addk_i32 s84, 0x7c00\n" \
M_"v[172:175], a[184:187], a[156:159], v[172:175]\n" \
B_"v27, s[68:71], s84 offen offset:2048 lds\n" \
M_"v[172:175], a[248:251], a[220:223], v[172:175]\n" \
X_"a[230:231], v33 offset:4608\n" \
M_"v[168:171], a[180:183], a[156:159], v[168:171]\n" \
X_"a[172:173], v35 offset:0\n" \
M_"v[168:171], a[244:247], a[220:223], v[168:171]\n" \
X_"a[174:175], v35 offset:512\n" \
M_"v[164:167], a[176:179], a[156:159], v[164:167]\n" \
"s_add_u32 s18, s24, s11\n" \
"v_writelane_b32 v11, s16, 9\n" \
"s_addc_u32 s19, s25, 0\n" \
M_"v[164:167], a[240:243], a[220:223], v[164:167]\n" \
R_"a[156:159], v28 offset:14336\n" \
"s_and_b32 s20, s17, 0xffff\n" \
M_"v[84:87], a[176:179], a[136:139], v[84:87]\n" \
R_"a[220:223], v29 offset:14336\n" \
"v_writelane_b32 v11, s20, 10\n" \
M_"v[84:87], a[240:243], a[200:203], v[84:87]\n" \
"s_addk_i32 s84, 0x7c00\n" \
M_"v[88:91], a[180:183], a[136:139], v[88:91]\n" \
B_"v27, s[68:71], s84 offen offset:3072 lds\n" \
M_"v[88:91], a[244:247], a[200:203], v[88:91]\n" \
"s_add_u32 m0, s30, 0x8000\n" \
"s_movk_i32 s84, 0x200\n" \
M_"v[92:95], a[184:187], a[136:139], v[92:95]\n" \
B_"v24, s[76:79], s84 offen offset:0 lds\n" \
M_"v[92:95], a[248:251], a[200:203], v[92:95]\n" \
X_"a[236:237], v35 offset:4096\n" \
"s_and_b32 s20, s19, 0xffff\n" \
M_"v[96:99], a[188:191], a[136:139], v[96:99]\n" \
X_"a[238:239], v35 offset:4608\n" \
"v_writelane_b32 v11, s18, 11\n" \
M_"v[96:99], a[252:255], a[200:203], v[96:99]\n" \
R_"a[136:139], v28 offset:4096\n" \
M_"v[112:115], a[188:191], a[140:143], v[112:115]\n" \
R_"a[200:203], v29 offset:4096\n" \
M_"v[112:115], a[252:255], a[204:207], v[112:115]\n" \
"s_movk_i32 s84, 0x2200\n" \
"s_nop 0\n" \
M_"v[108:111], a[184:187], a[140:143], v[108:111]\n" \
B_"v25, s[76:79], s84 offen offset:1024 lds\n" \
M_"v[108:111], a[248:251], a[204:207], v[108:111]\n" \
"s_add_u32 s89, s41, 0x1000\n" \
"buffer_store_dwordx4 v[200:203], v20, s[80:83], s89 offen offset:3072 sc1\n" \
M_"v[104:107], a[180:183], a[140:143], v[104:107]\n" \
X_"a[168:169], v34 offset:0\n" \
M_"v[104:107], a[244:247], a[204:207], v[104:107]\n" \
X_"a[170:171], v34 offset:512\n" \
M_"v[100:103], a[176:179], a[140:143], v[100:103]\n" \
X_"a[232:233], v34 offset:4096\n" \
M_"v[100:103], a[240:243], a[204:207], v[100:103]\n" \
R_"a[140:143], v28 offset:6144\n" \
M_"v[52:55], a[176:179], a[128:131], v[52:55]\n" \
R_"a[204:207], v29 offset:6144\n" \
M_"v[52:55], a[240:243], a[192:195], v[52:55]\n" \
"s_movk_i32 s84, 0x4200\n" \
M_"v[56:59], a[180:183], a[128:131], v[56:59]\n" \
B_"v24, s[76:79], s84 offen offset:2048 lds\n" \
M_"v[56:59], a[244:247], a[192:195], v[56:59]\n" \
"s_movk_i32 s84, 0x6200\n" \
M_"v[60:63], a[184:187], a[128:131], v[60:63]\n" \
B_"v25, s[76:79], s84 offen offset:3072 lds\n" \
M_"v[60:63], a[248:251], a[192:195], v[60:63]\n" \
X_"a[234:235], v34 offset:4608\n" \
M_"v[64:67], a[188:191], a[128:131], v[64:67]\n" \
"s_cmpk_lt_u32 s3, 3\n" \
"s_cselect_b32 s16, s16, s18\n" \
M_"v[64:67], a[252:255], a[192:195], v[64:67]\n" \
R_"a[128:131], v28 offset:0\n" \
"s_movk_i32 s18, 0xfff\n" \
M_"v[80:83], a[188:191], a[132:135], v[80:83]\n" \
R_"a[192:195], v29 offset:0\n" \
"v_writelane_b32 v11, s20, 12\n" \
M_"v[80:83], a[252:255], a[196:199], v[80:83]\n" \
"s_and_b32 s20, s29, 0xffff\n" \
"v_writelane_b32 v11, s28, 13\n" \
M_"v[76:79], a[184:187], a[132:135], v[76:79]\n" \
"v_writelane_b32 v11, s20, 14\n" \
"v_writelane_b32 v11, s16, 15\n" \
M_"v[76:79], a[248:251], a[196:199], v[76:79]\n" \
"s_cmpk_lt_u32 s3, 3\n" \
"s_cselect_b32 s16, s17, s19\n" \
M_"v[72:75], a[180:183], a[132:135], v[72:75]\n" \
"s_and_b32 s16, s16, 0xffff\n" \
"v_writelane_b32 v11, s16, 16\n" \
M_"v[72:75], a[244:247], a[196:199], v[72:75]\n" \
"s_mul_i32 s16, s5, 0x48\n" \
"s_add_i32 s17, s16, 0x47\n" \
M_"v[68:71], a[176:179], a[132:135], v[68:71]\n" \
"s_cmpk_lt_u32 s3, 3\n" \
"s_cselect_b32 s17, s17, s18\n" \
M_"v[68:71], a[240:243], a[196:199], v[68:71]\n" \
R_"a[132:135], v28 offset:2048\n" \
R_"a[196:199], v29 offset:2048\n" \
"s_waitcnt lgkmcnt(15)\n" \
M_"a[0:3], a[160:163], a[144:147], a[0:3]\n" \
"s_add_u32 m0, s30, 0x9000\n" \
"s_addk_i32 s84, 0x3000\n" \
M_"a[0:3], a[224:227], a[208:211], a[0:3]\n" \
B_"v24, s[76:79], s84 offen offset:0 lds\n" \
M_"a[4:7], a[164:167], a[144:147], a[4:7]\n" \
X_"a[176:177], v36 offset:0\n" \
"s_mulk_i32 s17, 0x480\n" \
M_"a[4:7], a[228:231], a[208:211], a[4:7]\n" \
".fill (L_pb + 65248 - .) / 4, 4, 0xBF800000\n" \
"L_pp16:\n" \
"s_cbranch_execz L_pret\n" \
X_"a[178:179], v36 offset:1024\n" \
"s_addk_i32 s17, 0x120\n" \
M_"a[12:15], a[172:175], a[144:147], a[12:15]\n" \
X_"a[240:241], v36 offset:8192\n" \
"v_writelane_b32 v11, s17, 17\n" \
"s_waitcnt lgkmcnt(15)\n" \
M_"a[12:15], a[236:239], a[208:211], a[12:15]\n" \
"s_add_u32 s88, s41, 0x1000\n" \
"buffer_store_dwordx4 v[204:207], v20, s[80:83], s88 offen offset:2048 sc1\n" \
"s_waitcnt lgkmcnt(11)\n" \
M_"a[8:11], a[144:147], a[168:171], a[8:11]\n" \
X_"a[242:243], v36 offset:9216\n" \
"s_lshl_b32 s17, s3, 5\n" \
"s_waitcnt lgkmcnt(8)\n" \
M_"a[8:11], a[208:211], a[232:235], a[8:11]\n" \
X_"a[180:181], v37 offset:0\n" \
"s_add_i32 s16, s16, s17\n" \
M_"a[24:27], a[148:151], a[168:171], a[24:27]\n" \
X_"a[182:183], v37 offset:1024\n" \
"s_mulk_i32 s16, 0x480\n" \
M_"a[24:27], a[212:215], a[232:235], a[24:27]\n" \
X_"a[244:245], v37 offset:8192\n" \
"s_mul_i32 s17, s12, 0x24000\n" \
M_"a[28:31], a[172:175], a[148:151], a[28:31]\n" \
"s_add_i32 s18, s17, 0x1b000\n" \
M_"a[28:31], a[236:239], a[212:215], a[28:31]\n" \
"s_addk_i32 s84, 0x2000\n" \
M_"a[20:23], a[164:167], a[148:151], a[20:23]\n" \
B_"v25, s[76:79], s84 offen offset:1024 lds\n" \
M_"a[20:23], a[228:231], a[212:215], a[20:23]\n" \
X_"a[246:247], v37 offset:9216\n" \
M_"a[16:19], a[160:163], a[148:151], a[16:19]\n" \
X_"a[184:185], v38 offset:0\n" \
M_"a[16:19], a[224:227], a[212:215], a[16:19]\n" \
X_"a[186:187], v38 offset:1024\n" \
M_"a[32:35], a[160:163], a[152:155], a[32:35]\n" \
"s_addk_i32 s84, 0x2000\n" \
M_"a[32:35], a[224:227], a[216:219], a[32:35]\n" \
B_"v24, s[76:79], s84 offen offset:2048 lds\n" \
M_"a[36:39], a[164:167], a[152:155], a[36:39]\n" \
X_"a[248:249], v38 offset:8192\n" \
M_"a[36:39], a[228:231], a[216:219], a[36:39]\n" \
X_"a[250:251], v38 offset:9216\n" \
M_"a[44:47], a[172:175], a[152:155], a[44:47]\n" \
X_"a[188:189], v39 offset:0\n" \
M_"a[44:47], a[236:239], a[216:219], a[44:47]\n" \
X_"a[190:191], v39 offset:1024\n" \
M_"a[40:43], a[152:155], a[168:171], a[40:43]\n" \
"s_add_u32 s89, s41, 0x1000\n" \
"buffer_store_dwordx4 v[208:211], v20, s[80:83], s89 offen offset:1024 sc1\n" \
M_"a[40:43], a[216:219], a[232:235], a[40:43]\n" \
X_"a[252:253], v39 offset:8192\n" \
M_"a[56:59], a[156:159], a[168:171], a[56:59]\n" \
X_"a[254:255], v39 offset:9216\n" \
M_"a[56:59], a[220:223], a[232:235], a[56:59]\n" \
"s_cmpk_lt_u32 s3, 3\n" \
"s_cselect_b32 s16, s16, s18\n" \
M_"a[60:63], a[172:175], a[156:159], a[60:63]\n" \
"s_xor_b32 s18, s3, 2\n" \
"v_writelane_b32 v11, s16, 18\n" \
M_"a[60:63], a[236:239], a[220:223], a[60:63]\n" \
"s_addk_i32 s84, 0x2000\n" \
M_"a[52:55], a[164:167], a[156:159], a[52:55]\n" \
B_"v25, s[76:79], s84 offen offset:3072 lds\n" \
M_"a[52:55], a[228:231], a[220:223], a[52:55]\n" \
"s_mul_i32 s16, s15, 0x2400\n" \
"s_mul_i32 s15, s15, 0x9000\n" \
M_"a[48:51], a[160:163], a[156:159], a[48:51]\n" \
"s_bitset1_b32 s16, 17\n" \
"v_writelane_b32 v11, s16, 22\n" \
M_"a[48:51], a[224:227], a[220:223], a[48:51]\n" \
"s_add_i32 s17, s17, s15\n" \
"s_mul_i32 s15, s3, 0x2400\n" \
M_"a[64:67], a[164:167], a[140:143], a[64:67]\n" \
"v_writelane_b32 v11, s17, 29\n" \
"s_add_i32 s16, s15, 0xa400\n" \
M_"a[64:67], a[228:231], a[204:207], a[64:67]\n" \
"s_mov_b32 s17, 0x8000\n" \
M_"a[80:83], a[172:175], a[140:143], a[80:83]\n" \
"s_cmpk_lt_u32 s3, 3\n" \
"s_cselect_b32 s16, s16, s17\n" \
M_"a[80:83], a[236:239], a[204:207], a[80:83]\n" \
"s_mov_b32 s17, 0x1b000\n" \
M_"a[68:71], a[140:143], a[168:171], a[68:71]\n" \
"s_cmp_lg_u32 s18, 0\n" \
"s_cselect_b32 s17, s16, s17\n" \
"s_waitcnt vmcnt(11) lgkmcnt(0)\n" \
M_"a[68:71], a[204:207], a[232:235], a[68:71]\n" \
"s_barrier\n" \
"v_xor_b32_e32 v28, 0x20000, v28\n" \
"v_xor_b32_e32 v29, 0x20000, v29\n" \
M_"v[116:119], a[176:179], a[144:147], v[116:119]\n" \
"s_add_u32 m0, s40, 0x0\n" \
"s_nop 0\n" \
"s_add_u32 s85, s39, 0x8000\n" \
M_"v[116:119], a[240:243], a[208:211], v[116:119]\n" \
B_"v20, s[80:83], s85 offen sc1 lds\n" \
M_"v[120:123], a[180:183], a[144:147], v[120:123]\n" \
X_"a[160:161], v32 offset:32768\n" \
"v_writelane_b32 v11, s16, 23\n" \
M_"v[120:123], a[244:247], a[208:211], v[120:123]\n" \
X_"a[162:163], v32 offset:33280\n" \
"s_or_b32 s16, s15, 0x10000\n" \
M_"v[124:127], a[184:187], a[144:147], v[124:127]\n" \
"s_add_u32 m0, s40, 0x1000\n" \
"s_add_u32 s85, s39, 0x9000\n" \
M_"v[124:127], a[248:251], a[208:211], v[124:127]\n" \
B_"v20, s[80:83], s85 offen sc1 lds\n" \
M_"v[128:131], a[188:191], a[144:147], v[128:131]\n" \
X_"a[224:225], v32 offset:36864\n" \
"v_writelane_b32 v11, s17, 24\n" \
M_"v[128:131], a[252:255], a[208:211], v[128:131]\n" \
R_"a[144:147], v28 offset:8192\n" \
"s_add_i32 s17, s15, 0x19000\n" \
M_"v[144:147], a[188:191], a[148:151], v[144:147]\n" \
R_"a[208:211], v29 offset:8192\n" \
"v_writelane_b32 v11, s16, 25\n" \
M_"v[144:147], a[252:255], a[212:215], v[144:147]\n" \
"s_add_u32 m0, s40, 0x3000\n" \
"s_movk_i32 s84, 0x200\n" \
M_"v[140:143], a[184:187], a[148:151], v[140:143]\n" \
B_"v21, s[76:79], s84 offen offset:0 lds\n" \
M_"v[140:143], a[248:251], a[212:215], v[140:143]\n" \
X_"a[226:227], v32 offset:37376\n" \
"s_mov_b32 s16, 0\n" \
M_"v[136:139], a[180:183], a[148:151], v[136:139]\n" \
"s_add_u32 s88, s41, 0x1000\n" \
"buffer_store_dwordx4 v[212:215], v20, s[80:83], s88 offen offset:0 sc1\n" \
M_"v[136:139], a[244:247], a[212:215], v[136:139]\n" \
X_"a[164:165], v33 offset:32768\n" \
M_"v[132:135], a[176:179], a[148:151], v[132:135]\n" \
"s_cmpk_lt_u32 s3, 3\n" \
"s_cselect_b32 s17, s17, s16\n" \
M_"v[132:135], a[240:243], a[212:215], v[132:135]\n" \
R_"a[148:151], v28 offset:10240\n" \
"s_bitset1_b32 s15, 17\n" \
M_"v[148:151], a[176:179], a[152:155], v[148:151]\n" \
R_"a[212:215], v29 offset:10240\n" \
"v_writelane_b32 v11, s17, 26\n" \
M_"v[148:151], a[240:243], a[216:219], v[148:151]\n" \
"s_add_u32 m0, s31, 0x0\n" \
"s_mov_b32 s84, 0x200000\n" \
M_"v[152:155], a[180:183], a[152:155], v[152:155]\n" \
B_"v26, s[68:71], s84 offen offset:0 lds\n" \
M_"v[152:155], a[244:247], a[216:219], v[152:155]\n" \
X_"a[166:167], v33 offset:33280\n" \
"s_movk_i32 s17, 0x2400\n" \
M_"v[156:159], a[184:187], a[152:155], v[156:159]\n" \
X_"a[228:229], v33 offset:36864\n" \
M_"v[156:159], a[248:251], a[216:219], v[156:159]\n" \
"s_addk_i32 s84, 0x7c00\n" \
M_"v[160:163], a[188:191], a[152:155], v[160:163]\n" \
B_"v26, s[68:71], s84 offen offset:1024 lds\n" \
M_"v[160:163], a[252:255], a[216:219], v[160:163]\n" \
R_"a[152:155], v28 offset:12288\n" \
M_"v[176:179], a[188:191], a[156:159], v[176:179]\n" \
R_"a[216:219], v29 offset:12288\n" \
M_"v[176:179], a[252:255], a[220:223], v[176:179]\n" \
"s_addk_i32 s84, 0x7c00\n" \
M_"v[172:175], a[184:187], a[156:159], v[172:175]\n" \
B_"v27, s[68:71], s84 offen offset:2048 lds\n" \
M_"v[172:175], a[248:251], a[220:223], v[172:175]\n" \
X_"a[230:231], v33 offset:37376\n" \
M_"v[168:171], a[180:183], a[156:159], v[168:171]\n" \
X_"a[172:173], v35 offset:32768\n" \
M_"v[168:171], a[244:247], a[220:223], v[168:171]\n" \
X_"a[174:175], v35 offset:33280\n" \
M_"v[164:167], a[176:179], a[156:159], v[164:167]\n" \
"s_cmpk_lt_u32 s3, 3\n" \
"s_cselect_b32 s15, s15, s17\n" \
M_"v[164:167], a[240:243], a[220:223], v[164:167]\n" \
R_"a[156:159], v28 offset:14336\n" \
"v_writelane_b32 v11, s15, 27\n" \
M_"v[84:87], a[176:179], a[136:139], v[84:87]\n" \
R_"a[220:223], v29 offset:14336\n" \
"s_lshl_b32 s15, s3, 10\n" \
M_"v[84:87], a[240:243], a[200:203], v[84:87]\n" \
"s_addk_i32 s84, 0x7c00\n" \
M_"v[88:91], a[180:183], a[136:139], v[88:91]\n" \
B_"v27, s[68:71], s84 offen offset:3072 lds\n" \
M_"v[88:91], a[244:247], a[200:203], v[88:91]\n" \
"s_add_u32 m0, s30, 0x20000\n" \
"s_movk_i32 s84, 0x280\n" \
M_"v[92:95], a[184:187], a[136:139], v[92:95]\n" \
B_"v24, s[76:79], s84 offen offset:0 lds\n" \
M_"v[92:95], a[248:251], a[200:203], v[92:95]\n" \
X_"a[236:237], v35 offset:36864\n" \
"s_addk_i32 s15, 0x4800\n" \
M_"v[96:99], a[188:191], a[136:139], v[96:99]\n" \
X_"a[238:239], v35 offset:37376\n" \
"v_writelane_b32 v11, s15, 28\n" \
M_"v[96:99], a[252:255], a[200:203], v[96:99]\n" \
R_"a[136:139], v28 offset:4096\n" \
"s_add_i32 s15, s14, 1\n" \
M_"v[112:115], a[188:191], a[140:143], v[112:115]\n" \
R_"a[200:203], v29 offset:4096\n" \
"s_and_b32 s15, s15, 3\n" \
M_"v[112:115], a[252:255], a[204:207], v[112:115]\n" \
"s_movk_i32 s84, 0x2280\n" \
M_"v[108:111], a[184:187], a[140:143], v[108:111]\n" \
B_"v25, s[76:79], s84 offen offset:1024 lds\n" \
M_"v[108:111], a[248:251], a[204:207], v[108:111]\n" \
"s_add_u32 s89, s42, 0x0\n" \
"buffer_store_dwordx4 v[216:219], v20, s[80:83], s89 offen offset:1024 sc1\n" \
M_"v[104:107], a[180:183], a[140:143], v[104:107]\n" \
X_"a[168:169], v34 offset:32768\n" \
"s_or_b32 s15, s15, s4\n" \
M_"v[104:107], a[244:247], a[204:207], v[104:107]\n" \
X_"a[170:171], v34 offset:33280\n" \
"s_lshl_b32 s17, s15, 2\n" \
M_"v[100:103], a[176:179], a[140:143], v[100:103]\n" \
X_"a[232:233], v34 offset:36864\n" \
"s_or_b32 s17, s17, s3\n" \
M_"v[100:103], a[240:243], a[204:207], v[100:103]\n" \
R_"a[140:143], v28 offset:6144\n" \
"s_mul_i32 s17, s17, 0x9000\n" \
M_"v[52:55], a[176:179], a[128:131], v[52:55]\n" \
R_"a[204:207], v29 offset:6144\n" \
"v_writelane_b32 v11, s17, 30\n" \
M_"v[52:55], a[240:243], a[192:195], v[52:55]\n" \
"s_movk_i32 s84, 0x4280\n" \
M_"v[56:59], a[180:183], a[128:131], v[56:59]\n" \
B_"v24, s[76:79], s84 offen offset:2048 lds\n" \
M_"v[56:59], a[244:247], a[192:195], v[56:59]\n" \
"s_movk_i32 s84, 0x6280\n" \
M_"v[60:63], a[184:187], a[128:131], v[60:63]\n" \
B_"v25, s[76:79], s84 offen offset:3072 lds\n" \
M_"v[60:63], a[248:251], a[192:195], v[60:63]\n" \
X_"a[234:235], v34 offset:37376\n" \
"s_lshl_b32 s17, s12, 2\n" \
M_"v[64:67], a[188:191], a[128:131], v[64:67]\n" \
"s_or_b32 s17, s17, s3\n" \
"s_add_i32 s14, s14, -1\n" \
M_"v[64:67], a[252:255], a[192:195], v[64:67]\n" \
R_"a[128:131], v28 offset:0\n" \
"s_xor_b32 s17, s17, 8\n" \
M_"v[80:83], a[188:191], a[132:135], v[80:83]\n" \
R_"a[192:195], v29 offset:0\n" \
"s_and_b32 s14, s14, 3\n" \
M_"v[80:83], a[252:255], a[196:199], v[80:83]\n" \
"s_mul_i32 s17, s17, 0x9000\n" \
"s_or_b32 s14, s14, s4\n" \
M_"v[76:79], a[184:187], a[132:135], v[76:79]\n" \
"v_writelane_b32 v11, s17, 31\n" \
"s_lshl_b32 s17, s14, 2\n" \
M_"v[76:79], a[248:251], a[196:199], v[76:79]\n" \
"s_or_b32 s17, s17, s3\n" \
"s_mul_i32 s17, s17, 0x9000\n" \
M_"v[72:75], a[180:183], a[132:135], v[72:75]\n" \
"v_writelane_b32 v11, s17, 32\n" \
"s_or_b32 s17, s8, s13\n" \
M_"v[72:75], a[244:247], a[196:199], v[72:75]\n" \
"s_mul_i32 s17, s17, 0x24800\n" \
"s_lshl_b32 s15, s15, 12\n" \
M_"v[68:71], a[176:179], a[132:135], v[68:71]\n" \
"s_add_i32 s18, s17, 0x3004800\n" \
"s_lshl_b32 s12, s12, 12\n" \
M_"v[68:71], a[240:243], a[196:199], v[68:71]\n" \
R_"a[132:135], v28 offset:2048\n" \
R_"a[196:199], v29 offset:2048\n" \
"s_waitcnt lgkmcnt(15)\n" \
M_"a[0:3], a[160:163], a[144:147], a[0:3]\n" \
"s_add_u32 m0, s30, 0x21000\n" \
"s_addk_i32 s84, 0x3000\n" \
M_"a[0:3], a[224:227], a[208:211], a[0:3]\n" \
B_"v24, s[76:79], s84 offen offset:0 lds\n" \
M_"a[4:7], a[164:167], a[144:147], a[4:7]\n" \
X_"a[176:177], v36 offset:32768\n" \
"s_add_i32 s19, s18, s15\n" \
M_"a[4:7], a[228:231], a[208:211], a[4:7]\n" \
X_"a[178:179], v36 offset:33792\n" \
"v_writelane_b32 v11, s19, 33\n" \
M_"a[12:15], a[172:175], a[144:147], a[12:15]\n" \
X_"a[240:241], v36 offset:40960\n" \
"s_xor_b32 s12, s12, 0x2000\n" \
"s_waitcnt lgkmcnt(15)\n" \
M_"a[12:15], a[236:239], a[208:211], a[12:15]\n" \
"s_add_u32 s88, s42, 0x0\n" \
"buffer_store_dwordx4 v[220:223], v20, s[80:83], s88 offen offset:0 sc1\n" \
"s_waitcnt lgkmcnt(11)\n" \
M_"a[8:11], a[144:147], a[168:171], a[8:11]\n" \
X_"a[242:243], v36 offset:41984\n" \
"s_lshl_b32 s14, s14, 12\n" \
"s_waitcnt lgkmcnt(8)\n" \
M_"a[8:11], a[208:211], a[232:235], a[8:11]\n" \
X_"a[180:181], v37 offset:32768\n" \
"s_sub_i32 s13, s8, s13\n" \
M_"a[24:27], a[148:151], a[168:171], a[24:27]\n" \
X_"a[182:183], v37 offset:33792\n" \
"s_add_i32 s19, s18, s12\n" \
M_"a[24:27], a[212:215], a[232:235], a[24:27]\n" \
X_"a[244:245], v37 offset:40960\n" \
"v_writelane_b32 v11, s19, 34\n" \
M_"a[28:31], a[172:175], a[148:151], a[28:31]\n" \
"s_add_i32 s18, s18, s14\n" \
"s_mul_i32 s13, s13, 0x24800\n" \
M_"a[28:31], a[236:239], a[212:215], a[28:31]\n" \
"s_addk_i32 s84, 0x2000\n" \
M_"a[20:23], a[164:167], a[148:151], a[20:23]\n" \
B_"v25, s[76:79], s84 offen offset:1024 lds\n" \
M_"a[20:23], a[228:231], a[212:215], a[20:23]\n" \
X_"a[246:247], v37 offset:41984\n" \
"v_writelane_b32 v11, s18, 35\n" \
M_"a[16:19], a[160:163], a[148:151], a[16:19]\n" \
X_"a[184:185], v38 offset:32768\n" \
"s_add_i32 s18, s13, 0x4120800\n" \
M_"a[16:19], a[224:227], a[212:215], a[16:19]\n" \
X_"a[186:187], v38 offset:33792\n" \
"s_add_i32 s15, s18, s15\n" \
M_"a[32:35], a[160:163], a[152:155], a[32:35]\n" \
"s_addk_i32 s84, 0x2000\n" \
M_"a[32:35], a[224:227], a[216:219], a[32:35]\n" \
B_"v24, s[76:79], s84 offen offset:2048 lds\n" \
M_"a[36:39], a[164:167], a[152:155], a[36:39]\n" \
X_"a[248:249], v38 offset:40960\n" \
"v_writelane_b32 v11, s15, 36\n" \
M_"a[36:39], a[228:231], a[216:219], a[36:39]\n" \
X_"a[250:251], v38 offset:41984\n" \
"s_add_i32 s12, s18, s12\n" \
M_"a[44:47], a[172:175], a[152:155], a[44:47]\n" \
X_"a[188:189], v39 offset:32768\n" \
"v_writelane_b32 v11, s12, 37\n" \
M_"a[44:47], a[236:239], a[216:219], a[44:47]\n" \
X_"a[190:191], v39 offset:33792\n" \
"s_add_i32 s18, s18, s14\n" \
M_"a[40:43], a[152:155], a[168:171], a[40:43]\n" \
"s_add_u32 s89, s42, 0x1000\n" \
"buffer_store_dwordx4 v[224:227], v20, s[80:83], s89 offen offset:3072 sc1\n" \
M_"a[40:43], a[216:219], a[232:235], a[40:43]\n" \
X_"a[252:253], v39 offset:40960\n" \
"s_or_b32 s12, s17, 0x3000000\n" \
M_"a[56:59], a[156:159], a[168:171], a[56:59]\n" \
X_"a[254:255], v39 offset:41984\n" \
"s_lshl_b32 s14, s5, 11\n" \
M_"a[56:59], a[220:223], a[232:235], a[56:59]\n" \
"v_writelane_b32 v11, s18, 38\n" \
"s_add_i32 s15, s12, s14\n" \
M_"a[60:63], a[172:175], a[156:159], a[60:63]\n" \
"v_writelane_b32 v11, s15, 39\n" \
"s_add_i32 s15, s13, 0x411c000\n" \
M_"a[60:63], a[236:239], a[220:223], a[60:63]\n" \
"s_addk_i32 s84, 0x2000\n" \
M_"a[52:55], a[164:167], a[156:159], a[52:55]\n" \
B_"v25, s[76:79], s84 offen offset:3072 lds\n" \
M_"a[52:55], a[228:231], a[220:223], a[52:55]\n" \
"s_add_i32 s18, s15, s14\n" \
"v_writelane_b32 v11, s18, 40\n" \
M_"a[48:51], a[160:163], a[156:159], a[48:51]\n" \
"s_add_i32 s17, s17, 0x3004000\n" \
"v_writelane_b32 v11, s17, 41\n" \
M_"a[48:51], a[224:227], a[220:223], a[48:51]\n" \
"s_add_i32 s13, s13, 0x4120000\n" \
"v_writelane_b32 v11, s13, 42\n" \
M_"a[64:67], a[164:167], a[140:143], a[64:67]\n" \
"s_lshl_b32 s13, s9, 2\n" \
"s_or_b32 s7, s13, s7\n" \
M_"a[64:67], a[228:231], a[204:207], a[64:67]\n" \
"s_mul_i32 s7, s7, 0x90000\n" \
"s_lshl_b32 s13, s3, 14\n" \
M_"a[80:83], a[172:175], a[140:143], a[80:83]\n" \
"s_lshl_b32 s9, s9, 7\n" \
"s_lshl_b32 s8, s8, 4\n" \
M_"a[80:83], a[236:239], a[204:207], a[80:83]\n" \
"s_add_i32 s7, s7, s13\n" \
"s_or_b32 s13, s8, s9\n" \
M_"a[68:71], a[140:143], a[168:171], a[68:71]\n" \
"s_sub_i32 s8, s8, s9\n" \
"s_mulk_i32 s13, 0x480\n" \
"s_waitcnt vmcnt(11) lgkmcnt(0)\n" \
M_"a[68:71], a[204:207], a[232:235], a[68:71]\n" \
"s_barrier\n" \
M_"v[116:119], a[176:179], a[144:147], v[116:119]\n" \
"s_add_u32 m0, s40, 0x8000\n" \
"s_add_u32 s85, s39, 0xa000\n" \
M_"v[116:119], a[240:243], a[208:211], v[116:119]\n" \
B_"v20, s[80:83], s85 offen sc1 lds\n" \
M_"v[120:123], a[180:183], a[144:147], v[120:123]\n" \
X_"a[160:161], v32 offset:0\n" \
"v_accvgpr_read_b32 v212, a108\n" \
M_"v[120:123], a[244:247], a[208:211], v[120:123]\n" \
X_"a[162:163], v32 offset:512\n" \
"v_accvgpr_read_b32 v213, a109\n" \
M_"v[124:127], a[184:187], a[144:147], v[124:127]\n" \
"s_add_u32 m0, s40, 0x9000\n" \
"s_add_u32 s85, s39, 0xb000\n" \
M_"v[124:127], a[248:251], a[208:211], v[124:127]\n" \
B_"v20, s[80:83], s85 offen sc1 lds\n" \
M_"v[128:131], a[188:191], a[144:147], v[128:131]\n" \
X_"a[224:225], v32 offset:4096\n" \
"v_accvgpr_read_b32 v214, a110\n" \
M_"v[128:131], a[252:255], a[208:211], v[128:131]\n" \
R_"a[144:147], v28 offset:40960\n" \
"v_accvgpr_read_b32 v215, a111\n" \
M_"v[144:147], a[188:191], a[148:151], v[144:147]\n" \
R_"a[208:211], v29 offset:40960\n" \
"v_accvgpr_read_b32 v180, a124\n" \
M_"v[144:147], a[252:255], a[212:215], v[144:147]\n" \
"s_add_u32 m0, s40, 0xb000\n" \
"s_movk_i32 s84, 0x280\n" \
"s_nop 0\n" \
M_"v[140:143], a[184:187], a[148:151], v[140:143]\n" \
B_"v21, s[76:79], s84 offen offset:0 lds\n" \
M_"v[140:143], a[248:251], a[212:215], v[140:143]\n" \
X_"a[226:227], v32 offset:4608\n" \
"v_accvgpr_read_b32 v181, a125\n" \
M_"v[136:139], a[180:183], a[148:151], v[136:139]\n" \
"s_add_u32 s88, s42, 0x1000\n" \
"buffer_store_dwordx4 v[228:231], v20, s[80:83], s88 offen offset:2048 sc1\n" \
M_"v[136:139], a[244:247], a[212:215], v[136:139]\n" \
X_"a[164:165], v33 offset:0\n" \
"v_accvgpr_read_b32 v182, a126\n" \
M_"v[132:135], a[176:179], a[148:151], v[132:135]\n" \
"v_accvgpr_read_b32 v183, a127\n" \
C_"v212, v212, v213\n" \
M_"v[132:135], a[240:243], a[212:215], v[132:135]\n" \
R_"a[148:151], v28 offset:43008\n" \
C_"v213, v214, v215\n" \
M_"v[148:151], a[176:179], a[152:155], v[148:151]\n" \
R_"a[212:215], v29 offset:43008\n" \
C_"v214, v180, v181\n" \
M_"v[148:151], a[240:243], a[216:219], v[148:151]\n" \
"s_add_u32 m0, s31, 0x8000\n" \
"s_mov_b32 s84, 0x280000\n" \
M_"v[152:155], a[180:183], a[152:155], v[152:155]\n" \
B_"v26, s[68:71], s84 offen offset:0 lds\n" \
M_"v[152:155], a[244:247], a[216:219], v[152:155]\n" \
X_"a[166:167], v33 offset:512\n" \
C_"v215, v182, v183\n" \
M_"v[156:159], a[184:187], a[152:155], v[156:159]\n" \
X_"a[228:229], v33 offset:4096\n" \
"v_accvgpr_read_b32 v208, a104\n" \
M_"v[156:159], a[248:251], a[216:219], v[156:159]\n" \
"s_addk_i32 s84, 0x7c00\n" \
M_"v[160:163], a[188:191], a[152:155], v[160:163]\n" \
B_"v26, s[68:71], s84 offen offset:1024 lds\n" \
M_"v[160:163], a[252:255], a[216:219], v[160:163]\n" \
R_"a[152:155], v28 offset:45056\n" \
"v_accvgpr_read_b32 v209, a105\n" \
M_"v[176:179], a[188:191], a[156:159], v[176:179]\n" \
R_"a[216:219], v29 offset:45056\n" \
"v_accvgpr_read_b32 v210, a106\n" \
M_"v[176:179], a[252:255], a[220:223], v[176:179]\n" \
"s_addk_i32 s84, 0x7c00\n" \
M_"v[172:175], a[184:187], a[156:159], v[172:175]\n" \
B_"v27, s[68:71], s84 offen offset:2048 lds\n" \
M_"v[172:175], a[248:251], a[220:223], v[172:175]\n" \
X_"a[230:231], v33 offset:4608\n" \
"v_accvgpr_read_b32 v211, a107\n" \
M_"v[168:171], a[180:183], a[156:159], v[168:171]\n" \
X_"a[172:173], v35 offset:0\n" \
"v_accvgpr_read_b32 v180, a120\n" \
M_"v[168:171], a[244:247], a[220:223], v[168:171]\n" \
X_"a[174:175], v35 offset:512\n" \
"v_accvgpr_read_b32 v181, a121\n" \
M_"v[164:167], a[176:179], a[156:159], v[164:167]\n" \
"v_accvgpr_read_b32 v182, a122\n" \
"v_accvgpr_read_b32 v183, a123\n" \
M_"v[164:167], a[240:243], a[220:223], v[164:167]\n" \
R_"a[156:159], v28 offset:47104\n" \
C_"v208, v208, v209\n" \
M_"v[84:87], a[176:179], a[136:139], v[84:87]\n" \
R_"a[220:223], v29 offset:47104\n" \
C_"v209, v210, v211\n" \
M_"v[84:87], a[240:243], a[200:203], v[84:87]\n" \
"s_addk_i32 s84, 0x7c00\n" \
M_"v[88:91], a[180:183], a[136:139], v[88:91]\n" \
B_"v27, s[68:71], s84 offen offset:3072 lds\n" \
M_"v[88:91], a[244:247], a[200:203], v[88:91]\n" \
"s_add_u32 m0, s30, 0x0\n" \
"s_movk_i32 s84, 0x300\n" \
"s_nop 0\n" \
M_"v[92:95], a[184:187], a[136:139], v[92:95]\n" \
B_"v24, s[76:79], s84 offen offset:0 lds\n" \
M_"v[92:95], a[248:251], a[200:203], v[92:95]\n" \
X_"a[236:237], v35 offset:4096\n" \
C_"v210, v180, v181\n" \
M_"v[96:99], a[188:191], a[136:139], v[96:99]\n" \
X_"a[238:239], v35 offset:4608\n" \
C_"v211, v182, v183\n" \
M_"v[96:99], a[252:255], a[200:203], v[96:99]\n" \
R_"a[136:139], v28 offset:36864\n" \
"v_accvgpr_read_b32 v204, a100\n" \
M_"v[112:115], a[188:191], a[140:143], v[112:115]\n" \
R_"a[200:203], v29 offset:36864\n" \
"v_accvgpr_read_b32 v205, a101\n" \
M_"v[112:115], a[252:255], a[204:207], v[112:115]\n" \
"s_movk_i32 s84, 0x2300\n" \
"s_nop 0\n" \
M_"v[108:111], a[184:187], a[140:143], v[108:111]\n" \
B_"v25, s[76:79], s84 offen offset:1024 lds\n" \
M_"v[108:111], a[248:251], a[204:207], v[108:111]\n" \
"s_add_u32 s89, s42, 0x1000\n" \
"buffer_store_dwordx4 v[232:235], v20, s[80:83], s89 offen offset:1024 sc1\n" \
M_"v[104:107], a[180:183], a[140:143], v[104:107]\n" \
X_"a[168:169], v34 offset:0\n" \
"v_accvgpr_read_b32 v206, a102\n" \
M_"v[104:107], a[244:247], a[204:207], v[104:107]\n" \
X_"a[170:171], v34 offset:512\n" \
"v_accvgpr_read_b32 v207, a103\n" \
M_"v[100:103], a[176:179], a[140:143], v[100:103]\n" \
X_"a[232:233], v34 offset:4096\n" \
"v_accvgpr_read_b32 v180, a116\n" \
M_"v[100:103], a[240:243], a[204:207], v[100:103]\n" \
R_"a[140:143], v28 offset:38912\n" \
"v_accvgpr_read_b32 v181, a117\n" \
M_"v[52:55], a[176:179], a[128:131], v[52:55]\n" \
R_"a[204:207], v29 offset:38912\n" \
"v_accvgpr_read_b32 v182, a118\n" \
M_"v[52:55], a[240:243], a[192:195], v[52:55]\n" \
"s_movk_i32 s84, 0x4300\n" \
M_"v[56:59], a[180:183], a[128:131], v[56:59]\n" \
B_"v24, s[76:79], s84 offen offset:2048 lds\n" \
M_"v[56:59], a[244:247], a[192:195], v[56:59]\n" \
"s_movk_i32 s84, 0x6300\n" \
M_"v[60:63], a[184:187], a[128:131], v[60:63]\n" \
B_"v25, s[76:79], s84 offen offset:3072 lds\n" \
M_"v[60:63], a[248:251], a[192:195], v[60:63]\n" \
X_"a[234:235], v34 offset:4608\n" \
"v_accvgpr_read_b32 v183, a119\n" \
M_"v[64:67], a[188:191], a[128:131], v[64:67]\n" \
C_"v204, v204, v205\n" \
C_"v205, v206, v207\n" \
M_"v[64:67], a[252:255], a[192:195], v[64:67]\n" \
R_"a[128:131], v28 offset:32768\n" \
C_"v206, v180, v181\n" \
M_"v[80:83], a[188:191], a[132:135], v[80:83]\n" \
R_"a[192:195], v29 offset:32768\n" \
C_"v207, v182, v183\n" \
M_"v[80:83], a[252:255], a[196:199], v[80:83]\n" \
"v_accvgpr_read_b32 v200, a96\n" \
"v_accvgpr_read_b32 v201, a97\n" \
M_"v[76:79], a[184:187], a[132:135], v[76:79]\n" \
"v_accvgpr_read_b32 v202, a98\n" \
"v_accvgpr_read_b32 v203, a99\n" \
M_"v[76:79], a[248:251], a[196:199], v[76:79]\n" \
"v_accvgpr_read_b32 v180, a112\n" \
"v_accvgpr_read_b32 v181, a113\n" \
M_"v[72:75], a[180:183], a[132:135], v[72:75]\n" \
"v_accvgpr_read_b32 v182, a114\n" \
"v_accvgpr_read_b32 v183, a115\n" \
M_"v[72:75], a[244:247], a[196:199], v[72:75]\n" \
C_"v200, v200, v201\n" \
C_"v201, v202, v203\n" \
M_"v[68:71], a[176:179], a[132:135], v[68:71]\n" \
C_"v202, v180, v181\n" \
C_"v203, v182, v183\n" \
M_"v[68:71], a[240:243], a[196:199], v[68:71]\n" \
R_"a[132:135], v28 offset:34816\n" \
R_"a[196:199], v29 offset:34816\n" \
"s_waitcnt lgkmcnt(15)\n" \
M_"a[0:3], a[160:163], a[144:147], a[0:3]\n" \
"s_add_u32 m0, s30, 0x1000\n" \
"s_addk_i32 s84, 0x3000\n" \
M_"a[0:3], a[224:227], a[208:211], a[0:3]\n" \
B_"v24, s[76:79], s84 offen offset:0 lds\n" \
M_"a[4:7], a[164:167], a[144:147], a[4:7]\n" \
X_"a[176:177], v36 offset:0\n" \
"v_accvgpr_read_b32 v196, a76\n" \
M_"a[4:7], a[228:231], a[208:211], a[4:7]\n" \
X_"a[178:179], v36 offset:1024\n" \
"v_accvgpr_read_b32 v197, a77\n" \
M_"a[12:15], a[172:175], a[144:147], a[12:15]\n" \
X_"a[240:241], v36 offset:8192\n" \
".fill (L_pb + 69344 - .) / 4, 4, 0xBF800000\n" \
"L_pp17:\n" \
"s_cbranch_execz L_pret\n" \
"v_accvgpr_read_b32 v198, a78\n" \
"s_waitcnt lgkmcnt(15)\n" \
M_"a[12:15], a[236:239], a[208:211], a[12:15]\n" \
"s_add_u32 s88, s42, 0x1000\n" \
"buffer_store_dwordx4 v[236:239], v20, s[80:83], s88 offen offset:0 sc1\n" \
"s_waitcnt lgkmcnt(11)\n" \
M_"a[8:11], a[144:147], a[168:171], a[8:11]\n" \
X_"a[242:243], v36 offset:9216\n" \
"v_accvgpr_read_b32 v199, a79\n" \
"s_waitcnt lgkmcnt(8)\n" \
M_"a[8:11], a[208:211], a[232:235], a[8:11]\n" \
X_"a[180:181], v37 offset:0\n" \
"v_accvgpr_read_b32 v180, a92\n" \
M_"a[24:27], a[148:151], a[168:171], a[24:27]\n" \
X_"a[182:183], v37 offset:1024\n" \
"v_accvgpr_read_b32 v181, a93\n" \
M_"a[24:27], a[212:215], a[232:235], a[24:27]\n" \
X_"a[244:245], v37 offset:8192\n" \
"v_accvgpr_read_b32 v182, a94\n" \
M_"a[28:31], a[172:175], a[148:151], a[28:31]\n" \
"v_accvgpr_read_b32 v183, a95\n" \
C_"v196, v196, v197\n" \
M_"a[28:31], a[236:239], a[212:215], a[28:31]\n" \
"s_addk_i32 s84, 0x2000\n" \
"s_nop 0\n" \
M_"a[20:23], a[164:167], a[148:151], a[20:23]\n" \
B_"v25, s[76:79], s84 offen offset:1024 lds\n" \
M_"a[20:23], a[228:231], a[212:215], a[20:23]\n" \
X_"a[246:247], v37 offset:9216\n" \
C_"v197, v198, v199\n" \
M_"a[16:19], a[160:163], a[148:151], a[16:19]\n" \
X_"a[184:185], v38 offset:0\n" \
C_"v198, v180, v181\n" \
M_"a[16:19], a[224:227], a[212:215], a[16:19]\n" \
X_"a[186:187], v38 offset:1024\n" \
C_"v199, v182, v183\n" \
M_"a[32:35], a[160:163], a[152:155], a[32:35]\n" \
"s_addk_i32 s84, 0x2000\n" \
"s_nop 0\n" \
M_"a[32:35], a[224:227], a[216:219], a[32:35]\n" \
B_"v24, s[76:79], s84 offen offset:2048 lds\n" \
M_"a[36:39], a[164:167], a[152:155], a[36:39]\n" \
X_"a[248:249], v38 offset:8192\n" \
"v_accvgpr_read_b32 v192, a72\n" \
M_"a[36:39], a[228:231], a[216:219], a[36:39]\n" \
X_"a[250:251], v38 offset:9216\n" \
"v_accvgpr_read_b32 v193, a73\n" \
M_"a[44:47], a[172:175], a[152:155], a[44:47]\n" \
X_"a[188:189], v39 offset:0\n" \
"v_accvgpr_read_b32 v194, a74\n" \
M_"a[44:47], a[236:239], a[216:219], a[44:47]\n" \
X_"a[190:191], v39 offset:1024\n" \
"v_accvgpr_read_b32 v195, a75\n" \
M_"a[40:43], a[152:155], a[168:171], a[40:43]\n" \
"s_add_u32 s89, s42, 0x3000\n" \
"buffer_store_dwordx4 v[240:243], v20, s[80:83], s89 offen offset:3072 sc1\n" \
M_"a[40:43], a[216:219], a[232:235], a[40:43]\n" \
X_"a[252:253], v39 offset:8192\n" \
"v_accvgpr_read_b32 v180, a88\n" \
M_"a[56:59], a[156:159], a[168:171], a[56:59]\n" \
X_"a[254:255], v39 offset:9216\n" \
"v_accvgpr_read_b32 v181, a89\n" \
M_"a[56:59], a[220:223], a[232:235], a[56:59]\n" \
"v_accvgpr_read_b32 v182, a90\n" \
"v_accvgpr_read_b32 v183, a91\n" \
M_"a[60:63], a[172:175], a[156:159], a[60:63]\n" \
C_"v192, v192, v193\n" \
C_"v193, v194, v195\n" \
M_"a[60:63], a[236:239], a[220:223], a[60:63]\n" \
"s_addk_i32 s84, 0x2000\n" \
M_"a[52:55], a[164:167], a[156:159], a[52:55]\n" \
B_"v25, s[76:79], s84 offen offset:3072 lds\n" \
M_"a[52:55], a[228:231], a[220:223], a[52:55]\n" \
C_"v194, v180, v181\n" \
C_"v195, v182, v183\n" \
M_"a[48:51], a[160:163], a[156:159], a[48:51]\n" \
"s_mulk_i32 s8, 0x480\n" \
"s_or_b32 s13, s13, s11\n" \
M_"a[48:51], a[224:227], a[220:223], a[48:51]\n" \
"s_or_b32 s8, s8, s11\n" \
"s_and_b32 s11, s2, 1\n" \
M_"a[64:67], a[164:167], a[140:143], a[64:67]\n" \
"s_cmp_lg_u32 s11, 0\n" \
"s_cselect_b32 s12, s15, s12\n" \
M_"a[64:67], a[228:231], a[204:207], a[64:67]\n" \
"s_bfe_u32 s9, s2, 0x20001\n" \
"s_add_i32 s15, s7, 0x4240000\n" \
M_"a[80:83], a[172:175], a[140:143], a[80:83]\n" \
"s_addk_i32 s12, 0x4000\n" \
"s_lshl_b32 s5, s5, 16\n" \
M_"a[80:83], a[236:239], a[204:207], a[80:83]\n" \
"s_xor_b32 s9, s9, s3\n" \
M_"a[68:71], a[140:143], a[168:171], a[68:71]\n" \
"s_cmp_lg_u32 s9, 0\n" \
"s_cselect_b32 s12, s6, s12\n" \
"s_waitcnt vmcnt(11) lgkmcnt(0)\n" \
M_"a[68:71], a[204:207], a[232:235], a[68:71]\n" \
"s_barrier\n" \
"v_mov_b32_e32 v45, s62\n" \
"s_mov_b64 exec, 1\n" \
"buffer_store_dword v45, off, s[80:83], s37 offset:0 sc1\n" \
"s_mov_b64 exec, -1\n" \
"v_xor_b32_e32 v28, 0x20000, v28\n" \
"v_xor_b32_e32 v29, 0x20000, v29\n" \
M_"v[116:119], a[176:179], a[144:147], v[116:119]\n" \
"s_add_u32 m0, s40, 0x0\n" \
"s_add_u32 s85, s39, 0xc000\n" \
M_"v[116:119], a[240:243], a[208:211], v[116:119]\n" \
B_"v20, s[80:83], s85 offen sc1 lds\n" \
M_"v[120:123], a[180:183], a[144:147], v[120:123]\n" \
X_"a[160:161], v32 offset:32768\n" \
"s_add_i32 s5, s15, s5\n" \
"s_nop 0\n" \
M_"v[120:123], a[244:247], a[208:211], v[120:123]\n" \
X_"a[162:163], v32 offset:33280\n" \
"v_writelane_b32 v11, s12, 43\n" \
M_"v[124:127], a[184:187], a[144:147], v[124:127]\n" \
"s_add_u32 m0, s40, 0x1000\n" \
"s_add_u32 s85, s39, 0xd000\n" \
M_"v[124:127], a[248:251], a[208:211], v[124:127]\n" \
B_"v20, s[80:83], s85 offen sc1 lds\n" \
M_"v[128:131], a[188:191], a[144:147], v[128:131]\n" \
X_"a[224:225], v32 offset:36864\n" \
"s_add_i32 s7, s7, 0x42c0000\n" \
M_"v[128:131], a[252:255], a[208:211], v[128:131]\n" \
R_"a[144:147], v28 offset:8192\n" \
"v_writelane_b32 v11, s5, 44\n" \
M_"v[144:147], a[188:191], a[148:151], v[144:147]\n" \
R_"a[208:211], v29 offset:8192\n" \
"s_lshl_b32 s5, s11, 10\n" \
M_"v[144:147], a[252:255], a[212:215], v[144:147]\n" \
"s_add_u32 m0, s40, 0x3000\n" \
"s_movk_i32 s84, 0x300\n" \
M_"v[140:143], a[184:187], a[148:151], v[140:143]\n" \
B_"v21, s[76:79], s84 offen offset:0 lds\n" \
M_"v[140:143], a[248:251], a[212:215], v[140:143]\n" \
X_"a[226:227], v32 offset:37376\n" \
"s_or_b32 s5, s7, s5\n" \
M_"v[136:139], a[180:183], a[148:151], v[136:139]\n" \
"s_cmp_lg_u32 s9, 0\n" \
"s_cselect_b32 s5, s6, s5\n" \
M_"v[136:139], a[244:247], a[212:215], v[136:139]\n" \
X_"a[164:165], v33 offset:32768\n" \
"s_add_i32 s8, s8, 0x21c000\n" \
M_"v[132:135], a[176:179], a[148:151], v[132:135]\n" \
"v_writelane_b32 v11, s5, 45\n" \
"s_add_i32 s5, s13, s10\n" \
M_"v[132:135], a[240:243], a[212:215], v[132:135]\n" \
R_"a[148:151], v28 offset:10240\n" \
"v_writelane_b32 v11, s5, 46\n" \
M_"v[148:151], a[176:179], a[152:155], v[148:151]\n" \
R_"a[212:215], v29 offset:10240\n" \
"s_add_i32 s5, s8, s10\n" \
M_"v[148:151], a[240:243], a[216:219], v[148:151]\n" \
"s_add_u32 m0, s31, 0x0\n" \
"s_mov_b32 s84, 0x300000\n" \
M_"v[152:155], a[180:183], a[152:155], v[152:155]\n" \
B_"v26, s[68:71], s84 offen offset:0 lds\n" \
M_"v[152:155], a[244:247], a[216:219], v[152:155]\n" \
"s_addk_i32 s84, 0x7c00\n" \
"s_nop 0\n" \
M_"v[156:159], a[184:187], a[152:155], v[156:159]\n" \
B_"v26, s[68:71], s84 offen offset:1024 lds\n" \
M_"v[156:159], a[248:251], a[216:219], v[156:159]\n" \
X_"a[166:167], v33 offset:33280\n" \
"v_writelane_b32 v11, s5, 47\n" \
M_"v[160:163], a[188:191], a[152:155], v[160:163]\n" \
X_"a[228:229], v33 offset:36864\n" \
M_"v[160:163], a[252:255], a[216:219], v[160:163]\n" \
R_"a[152:155], v28 offset:12288\n" \
M_"v[176:179], a[188:191], a[156:159], v[176:179]\n" \
R_"a[216:219], v29 offset:12288\n" \
M_"v[176:179], a[252:255], a[220:223], v[176:179]\n" \
"s_addk_i32 s84, 0x7c00\n" \
M_"v[172:175], a[184:187], a[156:159], v[172:175]\n" \
B_"v27, s[68:71], s84 offen offset:2048 lds\n" \
M_"v[172:175], a[248:251], a[220:223], v[172:175]\n" \
X_"a[230:231], v33 offset:37376\n" \
M_"v[168:171], a[180:183], a[156:159], v[168:171]\n" \
"s_addk_i32 s84, 0x7c00\n" \
M_"v[168:171], a[244:247], a[220:223], v[168:171]\n" \
B_"v27, s[68:71], s84 offen offset:3072 lds\n" \
M_"v[164:167], a[176:179], a[156:159], v[164:167]\n" \
X_"a[172:173], v35 offset:32768\n" \
M_"v[164:167], a[240:243], a[220:223], v[164:167]\n" \
R_"a[156:159], v28 offset:14336\n" \
M_"v[84:87], a[176:179], a[136:139], v[84:87]\n" \
R_"a[220:223], v29 offset:14336\n" \
M_"v[84:87], a[240:243], a[200:203], v[84:87]\n" \
"s_add_u32 s88, s42, 0x3000\n" \
"buffer_store_dwordx4 v[244:247], v20, s[80:83], s88 offen offset:2048 sc1\n" \
M_"v[88:91], a[180:183], a[136:139], v[88:91]\n" \
X_"a[174:175], v35 offset:33280\n" \
M_"v[88:91], a[244:247], a[200:203], v[88:91]\n" \
X_"a[236:237], v35 offset:36864\n" \
M_"v[92:95], a[184:187], a[136:139], v[92:95]\n" \
X_"a[238:239], v35 offset:37376\n" \
M_"v[92:95], a[248:251], a[200:203], v[92:95]\n" \
"s_add_u32 m0, s30, 0x8000\n" \
"s_movk_i32 s84, 0x380\n" \
M_"v[96:99], a[188:191], a[136:139], v[96:99]\n" \
B_"v24, s[76:79], s84 offen offset:0 lds\n" \
M_"v[96:99], a[252:255], a[200:203], v[96:99]\n" \
R_"a[136:139], v28 offset:4096\n" \
M_"v[112:115], a[188:191], a[140:143], v[112:115]\n" \
R_"a[200:203], v29 offset:4096\n" \
M_"v[112:115], a[252:255], a[204:207], v[112:115]\n" \
X_"a[168:169], v34 offset:32768\n" \
M_"v[108:111], a[184:187], a[140:143], v[108:111]\n" \
"s_movk_i32 s84, 0x2380\n" \
M_"v[108:111], a[248:251], a[204:207], v[108:111]\n" \
B_"v25, s[76:79], s84 offen offset:1024 lds\n" \
M_"v[104:107], a[180:183], a[140:143], v[104:107]\n" \
X_"a[170:171], v34 offset:33280\n" \
M_"v[104:107], a[244:247], a[204:207], v[104:107]\n" \
X_"a[232:233], v34 offset:36864\n" \
M_"v[100:103], a[176:179], a[140:143], v[100:103]\n" \
"s_cmp_lg_u32 s11, 0\n" \
"s_cselect_b32 s5, s8, s13\n" \
M_"v[100:103], a[240:243], a[204:207], v[100:103]\n" \
R_"a[140:143], v28 offset:6144\n" \
"s_addk_i32 s5, 0x100\n" \
M_"v[52:55], a[176:179], a[128:131], v[52:55]\n" \
R_"a[204:207], v29 offset:6144\n" \
M_"v[52:55], a[240:243], a[192:195], v[52:55]\n" \
"s_movk_i32 s84, 0x4380\n" \
M_"v[56:59], a[180:183], a[128:131], v[56:59]\n" \
B_"v24, s[76:79], s84 offen offset:2048 lds\n" \
M_"v[56:59], a[244:247], a[192:195], v[56:59]\n" \
"s_movk_i32 s84, 0x6380\n" \
M_"v[60:63], a[184:187], a[128:131], v[60:63]\n" \
B_"v25, s[76:79], s84 offen offset:3072 lds\n" \
M_"v[60:63], a[248:251], a[192:195], v[60:63]\n" \
X_"a[234:235], v34 offset:37376\n" \
M_"v[64:67], a[188:191], a[128:131], v[64:67]\n" \
"s_cmp_lg_u32 s9, 0\n" \
"s_cselect_b32 s5, s6, s5\n" \
M_"v[64:67], a[252:255], a[192:195], v[64:67]\n" \
R_"a[128:131], v28 offset:0\n" \
"v_writelane_b32 v11, s5, 48\n" \
M_"v[80:83], a[188:191], a[132:135], v[80:83]\n" \
R_"a[192:195], v29 offset:0\n" \
"s_or_b32 s5, s7, s14\n" \
M_"v[80:83], a[252:255], a[196:199], v[80:83]\n" \
"v_writelane_b32 v11, s5, 56\n" \
"s_lshl_b32 s5, s3, 17\n" \
M_"v[76:79], a[184:187], a[132:135], v[76:79]\n" \
"s_add_i32 s5, s15, s5\n" \
"s_or_b32 s4, s5, s4\n" \
M_"v[76:79], a[248:251], a[196:199], v[76:79]\n" \
"s_or_b32 s4, s4, 0x3840\n" \
"v_writelane_b32 v11, s4, 6\n" \
M_"v[72:75], a[180:183], a[132:135], v[72:75]\n" \
"s_mov_b32 s4, 0xff0000\n" \
M_"v[72:75], a[244:247], a[196:199], v[72:75]\n" \
"s_cmp_lg_u32 s9, 0\n" \
"s_cselect_b32 s4, s16, s4\n" \
M_"v[68:71], a[176:179], a[132:135], v[68:71]\n" \
"v_writelane_b32 v11, s4, 7\n" \
"s_lshl_b32 s4, s2, 16\n" \
M_"v[68:71], a[240:243], a[196:199], v[68:71]\n" \
R_"a[132:135], v28 offset:2048\n" \
R_"a[196:199], v29 offset:2048\n" \
"s_waitcnt lgkmcnt(15)\n" \
M_"a[0:3], a[160:163], a[144:147], a[0:3]\n" \
"s_add_u32 m0, s30, 0x9000\n" \
"s_addk_i32 s84, 0x3000\n" \
M_"a[0:3], a[224:227], a[208:211], a[0:3]\n" \
B_"v24, s[76:79], s84 offen offset:0 lds\n" \
M_"a[4:7], a[164:167], a[144:147], a[4:7]\n" \
X_"a[176:177], v36 offset:32768\n" \
"s_add_i32 s5, s4, 0x10000\n" \
M_"a[4:7], a[228:231], a[208:211], a[4:7]\n" \
X_"a[178:179], v36 offset:33792\n" \
"s_and_b32 s5, s5, 0x70000\n" \
"s_waitcnt lgkmcnt(15)\n" \
M_"a[12:15], a[172:175], a[144:147], a[12:15]\n" \
X_"a[240:241], v36 offset:40960\n" \
"s_add_i32 s5, s15, s5\n" \
"s_waitcnt lgkmcnt(15)\n" \
M_"a[12:15], a[236:239], a[208:211], a[12:15]\n" \
"s_add_u32 s89, s42, 0x3000\n" \
"buffer_store_dwordx4 v[184:187], v20, s[80:83], s89 offen offset:1024 sc1\n" \
"s_waitcnt lgkmcnt(11)\n" \
M_"a[8:11], a[144:147], a[168:171], a[8:11]\n" \
X_"a[242:243], v36 offset:41984\n" \
"v_writelane_b32 v11, s5, 49\n" \
"s_waitcnt lgkmcnt(8)\n" \
M_"a[8:11], a[208:211], a[232:235], a[8:11]\n" \
X_"a[180:181], v37 offset:32768\n" \
"s_add_i32 s5, s4, 0x20000\n" \
M_"a[24:27], a[148:151], a[168:171], a[24:27]\n" \
X_"a[182:183], v37 offset:33792\n" \
"s_and_b32 s5, s5, 0x70000\n" \
M_"a[24:27], a[212:215], a[232:235], a[24:27]\n" \
X_"a[244:245], v37 offset:40960\n" \
"s_add_i32 s5, s15, s5\n" \
M_"a[28:31], a[172:175], a[148:151], a[28:31]\n" \
"s_bitset1_b32 s5, 11\n" \
"v_writelane_b32 v11, s5, 50\n" \
M_"a[28:31], a[236:239], a[212:215], a[28:31]\n" \
"s_addk_i32 s84, 0x2000\n" \
M_"a[20:23], a[164:167], a[148:151], a[20:23]\n" \
B_"v25, s[76:79], s84 offen offset:1024 lds\n" \
M_"a[20:23], a[228:231], a[212:215], a[20:23]\n" \
X_"a[246:247], v37 offset:41984\n" \
"s_add_i32 s5, s4, 0x30000\n" \
M_"a[16:19], a[160:163], a[148:151], a[16:19]\n" \
X_"a[184:185], v38 offset:32768\n" \
"s_and_b32 s5, s5, 0x70000\n" \
M_"a[16:19], a[224:227], a[212:215], a[16:19]\n" \
X_"a[186:187], v38 offset:33792\n" \
"s_add_i32 s5, s15, s5\n" \
M_"a[32:35], a[160:163], a[152:155], a[32:35]\n" \
"s_addk_i32 s84, 0x2000\n" \
M_"a[32:35], a[224:227], a[216:219], a[32:35]\n" \
B_"v24, s[76:79], s84 offen offset:2048 lds\n" \
M_"a[36:39], a[164:167], a[152:155], a[36:39]\n" \
X_"a[248:249], v38 offset:40960\n" \
"s_bitset1_b32 s5, 12\n" \
"s_nop 0\n" \
M_"a[36:39], a[228:231], a[216:219], a[36:39]\n" \
X_"a[250:251], v38 offset:41984\n" \
"v_writelane_b32 v11, s5, 51\n" \
M_"a[44:47], a[172:175], a[152:155], a[44:47]\n" \
X_"a[188:189], v39 offset:32768\n" \
"s_and_b32 s5, s4, 0x70000\n" \
M_"a[44:47], a[236:239], a[216:219], a[44:47]\n" \
X_"a[190:191], v39 offset:33792\n" \
"s_xor_b32 s5, s5, 0x40000\n" \
M_"a[40:43], a[152:155], a[168:171], a[40:43]\n" \
"s_add_u32 s88, s42, 0x3000\n" \
"buffer_store_dwordx4 v[188:191], v20, s[80:83], s88 offen offset:0 sc1\n" \
M_"a[40:43], a[216:219], a[232:235], a[40:43]\n" \
X_"a[252:253], v39 offset:40960\n" \
"s_add_i32 s5, s15, s5\n" \
M_"a[56:59], a[156:159], a[168:171], a[56:59]\n" \
X_"a[254:255], v39 offset:41984\n" \
"s_or_b32 s5, s5, 0x1800\n" \
M_"a[56:59], a[220:223], a[232:235], a[56:59]\n" \
"v_writelane_b32 v11, s5, 52\n" \
"s_add_i32 s5, s4, 0x50000\n" \
M_"a[60:63], a[172:175], a[156:159], a[60:63]\n" \
"s_and_b32 s5, s5, 0x70000\n" \
"s_add_i32 s5, s15, s5\n" \
M_"a[60:63], a[236:239], a[220:223], a[60:63]\n" \
"s_addk_i32 s84, 0x2000\n" \
M_"a[52:55], a[164:167], a[156:159], a[52:55]\n" \
B_"v25, s[76:79], s84 offen offset:3072 lds\n" \
M_"a[52:55], a[228:231], a[220:223], a[52:55]\n" \
"s_bitset1_b32 s5, 13\n" \
"v_writelane_b32 v11, s5, 53\n" \
M_"a[48:51], a[160:163], a[156:159], a[48:51]\n" \
"s_add_i32 s5, s4, 0x60000\n" \
"s_and_b32 s5, s5, 0x70000\n" \
M_"a[48:51], a[224:227], a[220:223], a[48:51]\n" \
"s_add_i32 s4, s4, 0x70000\n" \
"s_add_i32 s5, s15, s5\n" \
M_"a[64:67], a[164:167], a[140:143], a[64:67]\n" \
"s_and_b32 s4, s4, 0x70000\n" \
"s_or_b32 s5, s5, 0x2800\n" \
M_"a[64:67], a[228:231], a[204:207], a[64:67]\n" \
"v_writelane_b32 v11, s5, 54\n" \
"s_add_i32 s15, s15, s4\n" \
M_"a[80:83], a[172:175], a[140:143], a[80:83]\n" \
"s_or_b32 s4, s15, 0x3000\n" \
"v_writelane_b32 v11, s4, 55\n" \
M_"a[80:83], a[236:239], a[204:207], a[80:83]\n" \
"s_movk_i32 s11, 0x4000\n" \
"s_lshl_b32 s4, s2, 2\n" \
M_"a[68:71], a[140:143], a[168:171], a[68:71]\n" \
"s_and_b32 s4, s4, 28\n" \
"s_bfe_u32 s5, s2, 0x20003\n" \
"s_waitcnt vmcnt(11) lgkmcnt(0)\n" \
M_"a[68:71], a[204:207], a[232:235], a[68:71]\n" \
"s_barrier\n" \
"v_xor_b32_e32 v28, 0x20000, v28\n" \
"v_xor_b32_e32 v29, 0x20000, v29\n" \
M_"v[116:119], a[176:179], a[144:147], v[116:119]\n" \
"s_add_u32 m0, s40, 0x8000\n" \
"s_add_u32 s85, s39, 0xe000\n" \
M_"v[116:119], a[240:243], a[208:211], v[116:119]\n" \
B_"v20, s[80:83], s85 offen sc1 lds\n" \
M_"v[120:123], a[180:183], a[144:147], v[120:123]\n" \
X_"a[160:161], v32 offset:0\n" \
"s_or_b32 s4, s4, s5\n" \
M_"v[120:123], a[244:247], a[208:211], v[120:123]\n" \
X_"a[162:163], v32 offset:512\n" \
"s_lshr_b32 s5, s2, 5\n" \
M_"v[124:127], a[184:187], a[144:147], v[124:127]\n" \
"s_add_u32 m0, s40, 0x9000\n" \
"s_add_u32 s85, s39, 0xf000\n" \
M_"v[124:127], a[248:251], a[208:211], v[124:127]\n" \
B_"v20, s[80:83], s85 offen sc1 lds\n" \
M_"v[128:131], a[188:191], a[144:147], v[128:131]\n" \
X_"a[224:225], v32 offset:4096\n" \
"s_lshr_b32 s19, s3, 1\n" \
M_"v[128:131], a[252:255], a[208:211], v[128:131]\n" \
R_"a[144:147], v28 offset:8192\n" \
"s_lshl_b32 s18, s5, 1\n" \
M_"v[144:147], a[188:191], a[148:151], v[144:147]\n" \
R_"a[208:211], v29 offset:8192\n" \
"s_lshl_b32 s6, s4, 20\n" \
M_"v[144:147], a[252:255], a[212:215], v[144:147]\n" \
"s_add_u32 m0, s40, 0xb000\n" \
"s_movk_i32 s84, 0x380\n" \
M_"v[140:143], a[184:187], a[148:151], v[140:143]\n" \
B_"v21, s[76:79], s84 offen offset:0 lds\n" \
M_"v[140:143], a[248:251], a[212:215], v[140:143]\n" \
X_"a[226:227], v32 offset:4608\n" \
"s_lshl_b32 s7, s4, 8\n" \
M_"v[136:139], a[180:183], a[148:151], v[136:139]\n" \
"s_and_b32 s13, s3, 1\n" \
"s_add_i32 s14, s18, s19\n" \
M_"v[136:139], a[244:247], a[212:215], v[136:139]\n" \
X_"a[164:165], v33 offset:0\n" \
M_"v[132:135], a[176:179], a[148:151], v[132:135]\n" \
"s_add_u32 s6, s26, s6\n" \
"s_addc_u32 s9, s27, 0\n" \
M_"v[132:135], a[240:243], a[212:215], v[132:135]\n" \
R_"a[148:151], v28 offset:10240\n" \
M_"v[148:151], a[176:179], a[152:155], v[148:151]\n" \
R_"a[212:215], v29 offset:10240\n" \
M_"v[148:151], a[240:243], a[216:219], v[148:151]\n" \
"s_add_u32 m0, s31, 0x8000\n" \
"s_mov_b32 s84, 0x380000\n" \
M_"v[152:155], a[180:183], a[152:155], v[152:155]\n" \
B_"v26, s[68:71], s84 offen offset:0 lds\n" \
M_"v[152:155], a[244:247], a[216:219], v[152:155]\n" \
"s_addk_i32 s84, 0x7c00\n" \
"s_nop 0\n" \
M_"v[156:159], a[184:187], a[152:155], v[156:159]\n" \
B_"v26, s[68:71], s84 offen offset:1024 lds\n" \
M_"v[156:159], a[248:251], a[216:219], v[156:159]\n" \
X_"a[166:167], v33 offset:512\n" \
M_"v[160:163], a[188:191], a[152:155], v[160:163]\n" \
X_"a[228:229], v33 offset:4096\n" \
M_"v[160:163], a[252:255], a[216:219], v[160:163]\n" \
R_"a[152:155], v28 offset:12288\n" \
M_"v[176:179], a[188:191], a[156:159], v[176:179]\n" \
R_"a[216:219], v29 offset:12288\n" \
M_"v[176:179], a[252:255], a[220:223], v[176:179]\n" \
"s_addk_i32 s84, 0x7c00\n" \
M_"v[172:175], a[184:187], a[156:159], v[172:175]\n" \
B_"v27, s[68:71], s84 offen offset:2048 lds\n" \
M_"v[172:175], a[248:251], a[220:223], v[172:175]\n" \
X_"a[230:231], v33 offset:4608\n" \
M_"v[168:171], a[180:183], a[156:159], v[168:171]\n" \
"s_addk_i32 s84, 0x7c00\n" \
M_"v[168:171], a[244:247], a[220:223], v[168:171]\n" \
B_"v27, s[68:71], s84 offen offset:3072 lds\n" \
M_"v[164:167], a[176:179], a[156:159], v[164:167]\n" \
X_"a[172:173], v35 offset:0\n" \
M_"v[164:167], a[240:243], a[220:223], v[164:167]\n" \
R_"a[156:159], v28 offset:14336\n" \
M_"v[84:87], a[176:179], a[136:139], v[84:87]\n" \
R_"a[220:223], v29 offset:14336\n" \
M_"v[84:87], a[240:243], a[200:203], v[84:87]\n" \
"s_add_u32 s89, s42, 0x0\n" \
"buffer_store_dwordx4 v[192:195], v20, s[80:83], s89 offen offset:2048 sc1\n" \
M_"v[88:91], a[180:183], a[136:139], v[88:91]\n" \
X_"a[174:175], v35 offset:512\n" \
M_"v[88:91], a[244:247], a[200:203], v[88:91]\n" \
X_"a[236:237], v35 offset:4096\n" \
M_"v[92:95], a[184:187], a[136:139], v[92:95]\n" \
X_"a[238:239], v35 offset:4608\n" \
M_"v[92:95], a[248:251], a[200:203], v[92:95]\n" \
"s_add_u32 m0, s30, 0x20000\n" \
"s_movk_i32 s84, 0x400\n" \
M_"v[96:99], a[188:191], a[136:139], v[96:99]\n" \
B_"v24, s[76:79], s84 offen offset:0 lds\n" \
M_"v[96:99], a[252:255], a[200:203], v[96:99]\n" \
R_"a[136:139], v28 offset:4096\n" \
M_"v[112:115], a[188:191], a[140:143], v[112:115]\n" \
R_"a[200:203], v29 offset:4096\n" \
M_"v[112:115], a[252:255], a[204:207], v[112:115]\n" \
X_"a[168:169], v34 offset:0\n" \
M_"v[108:111], a[184:187], a[140:143], v[108:111]\n" \
"s_movk_i32 s84, 0x2400\n" \
M_"v[108:111], a[248:251], a[204:207], v[108:111]\n" \
B_"v25, s[76:79], s84 offen offset:1024 lds\n" \
M_"v[104:107], a[180:183], a[140:143], v[104:107]\n" \
X_"a[170:171], v34 offset:512\n" \
M_"v[104:107], a[244:247], a[204:207], v[104:107]\n" \
X_"a[232:233], v34 offset:4096\n" \
M_"v[100:103], a[176:179], a[140:143], v[100:103]\n" \
"s_add_u32 s8, s6, 0x1000000\n" \
"s_addc_u32 s6, s9, 0\n" \
M_"v[100:103], a[240:243], a[204:207], v[100:103]\n" \
R_"a[140:143], v28 offset:6144\n" \
"s_and_b32 s9, s6, 0xffff\n" \
M_"v[52:55], a[176:179], a[128:131], v[52:55]\n" \
R_"a[204:207], v29 offset:6144\n" \
"s_lshl_b32 s6, s5, 16\n" \
M_"v[52:55], a[240:243], a[192:195], v[52:55]\n" \
"s_movk_i32 s84, 0x4400\n" \
M_"v[56:59], a[180:183], a[128:131], v[56:59]\n" \
B_"v24, s[76:79], s84 offen offset:2048 lds\n" \
M_"v[56:59], a[244:247], a[192:195], v[56:59]\n" \
"s_movk_i32 s84, 0x6400\n" \
"s_nop 0\n" \
M_"v[60:63], a[184:187], a[128:131], v[60:63]\n" \
B_"v25, s[76:79], s84 offen offset:3072 lds\n" \
M_"v[60:63], a[248:251], a[192:195], v[60:63]\n" \
X_"a[234:235], v34 offset:4608\n" \
"s_sub_i32 s10, 0x100000, s6\n" \
M_"v[64:67], a[188:191], a[128:131], v[64:67]\n" \
"s_mov_b32 s6, 0x8000\n" \
M_"v[64:67], a[252:255], a[192:195], v[64:67]\n" \
R_"a[128:131], v28 offset:0\n" \
M_"v[80:83], a[188:191], a[132:135], v[80:83]\n" \
R_"a[192:195], v29 offset:0\n" \
M_"v[80:83], a[252:255], a[196:199], v[80:83]\n" \
"s_cmp_lg_u32 s5, 0\n" \
"s_cselect_b32 s17, s11, s6\n" \
M_"v[76:79], a[184:187], a[132:135], v[76:79]\n" \
"s_lshl_b32 s6, s3, 1\n" \
"s_lshl_b32 s5, s5, 3\n" \
M_"v[76:79], a[248:251], a[196:199], v[76:79]\n" \
"s_and_b32 s6, s6, 4\n" \
"s_or_b32 s12, s5, s6\n" \
M_"v[72:75], a[180:183], a[132:135], v[72:75]\n" \
"s_sub_i32 s5, s13, s5\n" \
"s_or_b32 s5, s5, s6\n" \
M_"v[72:75], a[244:247], a[196:199], v[72:75]\n" \
"s_or_b32 s6, s12, s13\n" \
"s_lshl_b32 s4, s4, 2\n" \
M_"v[68:71], a[176:179], a[132:135], v[68:71]\n" \
"s_mulk_i32 s6, 0x92\n" \
"s_add_i32 s4, s4, 18\n" \
M_"v[68:71], a[240:243], a[196:199], v[68:71]\n" \
R_"a[132:135], v28 offset:2048\n" \
R_"a[196:199], v29 offset:2048\n" \
"s_waitcnt lgkmcnt(15)\n" \
M_"a[0:3], a[160:163], a[144:147], a[0:3]\n" \
"s_add_u32 m0, s30, 0x21000\n" \
"s_addk_i32 s84, 0x3000\n" \
M_"a[0:3], a[224:227], a[208:211], a[0:3]\n" \
B_"v24, s[76:79], s84 offen offset:0 lds\n" \
M_"a[4:7], a[164:167], a[144:147], a[4:7]\n" \
X_"a[176:177], v36 offset:0\n" \
"s_mulk_i32 s5, 0x92\n" \
M_"a[4:7], a[228:231], a[208:211], a[4:7]\n" \
X_"a[178:179], v36 offset:1024\n" \
"s_add_i32 s6, s4, s6\n" \
"s_waitcnt lgkmcnt(15)\n" \
M_"a[12:15], a[172:175], a[144:147], a[12:15]\n" \
X_"a[240:241], v36 offset:8192\n" \
"s_add_i32 s5, s5, s4\n" \
"s_waitcnt lgkmcnt(15)\n" \
M_"a[12:15], a[236:239], a[208:211], a[12:15]\n" \
"s_add_u32 s88, s42, 0x0\n" \
"buffer_store_dwordx4 v[196:199], v20, s[80:83], s88 offen offset:3072 sc1\n" \
"s_waitcnt lgkmcnt(11)\n" \
M_"a[8:11], a[144:147], a[168:171], a[8:11]\n" \
X_"a[242:243], v36 offset:9216\n" \
"s_lshl_b32 s4, s3, 11\n" \
"s_waitcnt lgkmcnt(8)\n" \
M_"a[8:11], a[208:211], a[232:235], a[8:11]\n" \
X_"a[180:181], v37 offset:0\n" \
"s_lshl_b32 s15, s13, 12\n" \
M_"a[24:27], a[148:151], a[168:171], a[24:27]\n" \
X_"a[182:183], v37 offset:1024\n" \
"s_lshl_b32 s16, s19, 14\n" \
M_"a[24:27], a[212:215], a[232:235], a[24:27]\n" \
X_"a[244:245], v37 offset:8192\n" \
"s_mul_i32 s19, s19, 0xe000\n" \
M_"a[28:31], a[172:175], a[148:151], a[28:31]\n" \
"s_mov_b32 s11, 0x20000\n" \
"s_or_b32 s12, s4, 0xc000\n" \
M_"a[28:31], a[236:239], a[212:215], a[28:31]\n" \
"s_addk_i32 s84, 0x2000\n" \
M_"a[20:23], a[164:167], a[148:151], a[20:23]\n" \
B_"v25, s[76:79], s84 offen offset:1024 lds\n" \
M_"a[20:23], a[228:231], a[212:215], a[20:23]\n" \
X_"a[246:247], v37 offset:9216\n" \
".fill (L_pb + 73440 - .) / 4, 4, 0xBF800000\n" \
"L_pp18:\n" \
"s_cbranch_execz L_pret\n" \
"s_lshl_b32 s4, s3, 8\n" \
M_"a[16:19], a[160:163], a[148:151], a[16:19]\n" \
X_"a[184:185], v38 offset:0\n" \
"s_or_b32 s22, s15, s16\n" \
M_"a[16:19], a[224:227], a[212:215], a[16:19]\n" \
X_"a[186:187], v38 offset:1024\n" \
"s_or_b32 s23, s19, s15\n" \
M_"a[32:35], a[160:163], a[152:155], a[32:35]\n" \
"s_addk_i32 s84, 0x2000\n" \
M_"a[32:35], a[224:227], a[216:219], a[32:35]\n" \
B_"v24, s[76:79], s84 offen offset:2048 lds\n" \
M_"a[36:39], a[164:167], a[152:155], a[36:39]\n" \
X_"a[248:249], v38 offset:8192\n" \
"s_lshl_b32 s60, s6, 10\n" \
M_"a[36:39], a[228:231], a[216:219], a[36:39]\n" \
X_"a[250:251], v38 offset:9216\n" \
"s_lshl_b32 s61, s5, 10\n" \
M_"a[44:47], a[172:175], a[152:155], a[44:47]\n" \
X_"a[188:189], v39 offset:0\n" \
"s_lshl_b32 s14, s14, 15\n" \
M_"a[44:47], a[236:239], a[216:219], a[44:47]\n" \
X_"a[190:191], v39 offset:1024\n" \
"s_or_b32 s20, s4, 0x26c00\n" \
M_"a[40:43], a[152:155], a[168:171], a[40:43]\n" \
"s_add_u32 s89, s42, 0x2000\n" \
"buffer_store_dwordx4 v[200:203], v20, s[80:83], s89 offen offset:0 sc1\n" \
M_"a[40:43], a[216:219], a[232:235], a[40:43]\n" \
X_"a[252:253], v39 offset:8192\n" \
"s_or_b32 s13, s22, 0x8000\n" \
M_"a[56:59], a[156:159], a[168:171], a[56:59]\n" \
X_"a[254:255], v39 offset:9216\n" \
"s_add_i32 s22, s22, 0x14000\n" \
M_"a[56:59], a[220:223], a[232:235], a[56:59]\n" \
"s_bitset1_b32 s23, 16\n" \
"s_or_b32 s24, s7, 0x5be8020\n" \
M_"a[60:63], a[172:175], a[156:159], a[60:63]\n" \
"s_and_b32 s57, s27, 0xffff\n" \
"s_mov_b32 s58, 0x6000000\n" \
M_"a[60:63], a[236:239], a[220:223], a[60:63]\n" \
"s_addk_i32 s84, 0x2000\n" \
M_"a[52:55], a[164:167], a[156:159], a[52:55]\n" \
B_"v25, s[76:79], s84 offen offset:3072 lds\n" \
M_"a[52:55], a[228:231], a[220:223], a[52:55]\n" \
"s_mov_b32 s56, s26\n" \
"s_mov_b32 s59, s11\n" \
M_"a[48:51], a[160:163], a[156:159], a[48:51]\n" \
"s_add_i32 s60, s60, 0x3000000\n" \
"s_add_i32 s61, s61, 0x411c000\n" \
M_"a[48:51], a[224:227], a[220:223], a[48:51]\n" \
"s_sub_i32 s19, 14, s18\n" \
"s_mov_b32 s21, 0x71800000\n" \
M_"a[64:67], a[164:167], a[140:143], a[64:67]\n" \
"s_mov_b32 s4, L_gsw-L_gtr\n" \
M_"a[64:67], a[228:231], a[204:207], a[64:67]\n" \
"s_cmp_eq_u32 s19, 0\n" \
"s_cselect_b32 s4, L_gs7-L_gtr, s4\n" \
M_"a[80:83], a[172:175], a[140:143], a[80:83]\n" \
"s_cmp_eq_u32 s16, 0\n" \
"s_cselect_b32 s4, 0, s4\n" \
M_"a[80:83], a[236:239], a[204:207], a[80:83]\n" \
"s_getpc_b64 s[28:29]\n" \
"L_gp1_%=:\n" \
"s_add_u32 s28, s28, L_gtr-L_gp1_%=\n" \
"s_addc_u32 s29, s29, 0\n" \
M_"a[68:71], a[140:143], a[168:171], a[68:71]\n" \
"s_add_u32 s28, s28, s4\n" \
"s_addc_u32 s29, s29, 0\n" \
"s_waitcnt vmcnt(11) lgkmcnt(0)\n" \
M_"a[68:71], a[204:207], a[232:235], a[68:71]\n" \
"s_barrier\n" \
M_"v[116:119], a[176:179], a[144:147], v[116:119]\n" \
"s_add_u32 m0, s40, 0x0\n" \
"s_nop 0\n" \
"s_add_u32 s85, s39, 0x10000\n" \
M_"v[116:119], a[240:243], a[208:211], v[116:119]\n" \
B_"v20, s[80:83], s85 offen sc1 lds\n" \
M_"v[120:123], a[180:183], a[144:147], v[120:123]\n" \
X_"a[160:161], v32 offset:32768\n" \
"v_accvgpr_write_b32 a96, 0\n" \
M_"v[120:123], a[244:247], a[208:211], v[120:123]\n" \
X_"a[162:163], v32 offset:33280\n" \
"v_accvgpr_write_b32 a97, 0\n" \
M_"v[124:127], a[184:187], a[144:147], v[124:127]\n" \
"v_accvgpr_write_b32 a98, 0\n" \
"v_accvgpr_write_b32 a99, 0\n" \
M_"v[124:127], a[248:251], a[208:211], v[124:127]\n" \
"v_accvgpr_write_b32 a100, 0\n" \
"v_accvgpr_write_b32 a101, 0\n" \
M_"v[128:131], a[188:191], a[144:147], v[128:131]\n" \
X_"a[224:225], v32 offset:36864\n" \
"v_accvgpr_write_b32 a102, 0\n" \
M_"v[128:131], a[252:255], a[208:211], v[128:131]\n" \
R_"a[144:147], v28 offset:40960\n" \
"v_accvgpr_write_b32 a103, 0\n" \
M_"v[144:147], a[188:191], a[148:151], v[144:147]\n" \
R_"a[208:211], v29 offset:40960\n" \
"v_accvgpr_write_b32 a112, 0\n" \
M_"v[144:147], a[252:255], a[212:215], v[144:147]\n" \
"s_add_u32 m0, s40, 0x1000\n" \
"s_add_u32 s85, s39, 0x11000\n" \
M_"v[140:143], a[184:187], a[148:151], v[140:143]\n" \
B_"v20, s[80:83], s85 offen sc1 lds\n" \
M_"v[140:143], a[248:251], a[212:215], v[140:143]\n" \
X_"a[226:227], v32 offset:37376\n" \
"v_accvgpr_write_b32 a113, 0\n" \
M_"v[136:139], a[180:183], a[148:151], v[136:139]\n" \
"v_accvgpr_write_b32 a114, 0\n" \
"v_accvgpr_write_b32 a115, 0\n" \
M_"v[136:139], a[244:247], a[212:215], v[136:139]\n" \
X_"a[164:165], v33 offset:32768\n" \
"v_accvgpr_write_b32 a120, 0\n" \
M_"v[132:135], a[176:179], a[148:151], v[132:135]\n" \
"v_accvgpr_write_b32 a121, 0\n" \
"v_accvgpr_write_b32 a122, 0\n" \
M_"v[132:135], a[240:243], a[212:215], v[132:135]\n" \
R_"a[148:151], v28 offset:43008\n" \
"v_accvgpr_write_b32 a123, 0\n" \
M_"v[148:151], a[176:179], a[152:155], v[148:151]\n" \
R_"a[212:215], v29 offset:43008\n" \
"v_mov_b32_e32 v180, 0x3f803f80\n" \
M_"v[148:151], a[240:243], a[216:219], v[148:151]\n" \
"s_add_u32 m0, s40, 0x3000\n" \
"s_movk_i32 s84, 0x400\n" \
M_"v[152:155], a[180:183], a[152:155], v[152:155]\n" \
B_"v21, s[76:79], s84 offen offset:0 lds\n" \
M_"v[152:155], a[244:247], a[216:219], v[152:155]\n" \
X_"a[166:167], v33 offset:33280\n" \
"s_mov_b32 s6, 0x33333333\n" \
M_"v[156:159], a[184:187], a[152:155], v[156:159]\n" \
X_"a[228:229], v33 offset:36864\n" \
"s_mov_b32 s7, 0xcccccccc\n" \
M_"v[156:159], a[248:251], a[216:219], v[156:159]\n" \
"s_add_u32 m0, s31, 0x0\n" \
"s_mov_b32 s84, 0x400000\n" \
M_"v[160:163], a[188:191], a[152:155], v[160:163]\n" \
B_"v26, s[68:71], s84 offen offset:0 lds\n" \
M_"v[160:163], a[252:255], a[216:219], v[160:163]\n" \
R_"a[152:155], v28 offset:45056\n" \
"v_cndmask_b32_e64 v181, 0, v180, s[6:7]\n" \
M_"v[176:179], a[188:191], a[156:159], v[176:179]\n" \
R_"a[216:219], v29 offset:45056\n" \
"v_accvgpr_write_b32 a104, v181\n" \
M_"v[176:179], a[252:255], a[220:223], v[176:179]\n" \
X_"a[230:231], v33 offset:37376\n" \
"v_accvgpr_write_b32 a105, v181\n" \
M_"v[172:175], a[184:187], a[156:159], v[172:175]\n" \
X_"a[172:173], v35 offset:32768\n" \
"v_accvgpr_write_b32 a106, v181\n" \
M_"v[172:175], a[248:251], a[220:223], v[172:175]\n" \
"v_accvgpr_write_b32 a107, v181\n" \
"s_mov_b32 s6, 0x11111111\n" \
M_"v[168:171], a[180:183], a[156:159], v[168:171]\n" \
"s_add_u32 s88, s42, 0x2000\n" \
"buffer_store_dwordx4 v[204:207], v20, s[80:83], s88 offen offset:1024 sc1\n" \
M_"v[168:171], a[244:247], a[220:223], v[168:171]\n" \
X_"a[174:175], v35 offset:33280\n" \
"s_mov_b32 s7, 0x11111111\n" \
M_"v[164:167], a[176:179], a[156:159], v[164:167]\n" \
"v_cndmask_b32_e64 v181, 0, v180, s[6:7]\n" \
"v_accvgpr_write_b32 a108, v181\n" \
M_"v[164:167], a[240:243], a[220:223], v[164:167]\n" \
R_"a[156:159], v28 offset:47104\n" \
"v_accvgpr_write_b32 a109, v181\n" \
M_"v[84:87], a[176:179], a[136:139], v[84:87]\n" \
R_"a[220:223], v29 offset:47104\n" \
"v_accvgpr_write_b32 a110, v181\n" \
M_"v[84:87], a[240:243], a[200:203], v[84:87]\n" \
X_"a[236:237], v35 offset:36864\n" \
"v_accvgpr_write_b32 a111, v181\n" \
M_"v[88:91], a[180:183], a[136:139], v[88:91]\n" \
"s_mov_b32 s6, 0x44444444\n" \
"s_mov_b32 s7, 0x44444444\n" \
M_"v[88:91], a[244:247], a[200:203], v[88:91]\n" \
"s_addk_i32 s84, 0x7c00\n" \
"s_nop 0\n" \
M_"v[92:95], a[184:187], a[136:139], v[92:95]\n" \
B_"v26, s[68:71], s84 offen offset:1024 lds\n" \
M_"v[92:95], a[248:251], a[200:203], v[92:95]\n" \
X_"a[238:239], v35 offset:37376\n" \
"v_cndmask_b32_e64 v181, 0, v180, s[6:7]\n" \
M_"v[96:99], a[188:191], a[136:139], v[96:99]\n" \
"v_accvgpr_write_b32 a116, v181\n" \
"v_accvgpr_write_b32 a117, v181\n" \
M_"v[96:99], a[252:255], a[200:203], v[96:99]\n" \
R_"a[136:139], v28 offset:36864\n" \
"v_accvgpr_write_b32 a118, v181\n" \
M_"v[112:115], a[188:191], a[140:143], v[112:115]\n" \
R_"a[200:203], v29 offset:36864\n" \
"v_accvgpr_write_b32 a119, v181\n" \
M_"v[112:115], a[252:255], a[204:207], v[112:115]\n" \
X_"a[168:169], v34 offset:32768\n" \
"v_bfe_i32 v181, v20, 4, 1\n" \
M_"v[108:111], a[184:187], a[140:143], v[108:111]\n" \
"s_addk_i32 s84, 0x7c00\n" \
"s_nop 0\n" \
M_"v[108:111], a[248:251], a[204:207], v[108:111]\n" \
B_"v27, s[68:71], s84 offen offset:2048 lds\n" \
M_"v[104:107], a[180:183], a[140:143], v[104:107]\n" \
X_"a[170:171], v34 offset:33280\n" \
"v_and_b32_e32 v181, 0xaaaaaaaa, v181\n" \
M_"v[104:107], a[244:247], a[204:207], v[104:107]\n" \
X_"a[232:233], v34 offset:36864\n" \
"v_or_b32_e32 v181, 0x44444444, v181\n" \
M_"v[100:103], a[176:179], a[140:143], v[100:103]\n" \
"v_accvgpr_write_b32 a126, v181\n" \
"v_bfe_u32 v180, v20, 5, 3\n" \
M_"v[100:103], a[240:243], a[204:207], v[100:103]\n" \
R_"a[140:143], v28 offset:38912\n" \
"v_bfe_u32 v181, v20, 8, 2\n" \
M_"v[52:55], a[176:179], a[128:131], v[52:55]\n" \
R_"a[204:207], v29 offset:38912\n" \
"v_lshlrev_b32_e64 v181, 1, v181\n" \
M_"v[52:55], a[240:243], a[192:195], v[52:55]\n" \
"s_addk_i32 s84, 0x7c00\n" \
M_"v[56:59], a[180:183], a[128:131], v[56:59]\n" \
B_"v27, s[68:71], s84 offen offset:3072 lds\n" \
M_"v[56:59], a[244:247], a[192:195], v[56:59]\n" \
X_"a[234:235], v34 offset:37376\n" \
"v_sub_u32_e32 v180, v180, v181\n" \
M_"v[60:63], a[184:187], a[128:131], v[60:63]\n" \
"v_and_b32_e64 v181, 16, v20\n" \
"v_mov_b32_e32 v182, 0x3f80\n" \
M_"v[60:63], a[248:251], a[192:195], v[60:63]\n" \
"v_lshlrev_b32_e32 v181, v181, v182\n" \
"v_cmp_eq_u32_e32 vcc, 0, v180\n" \
M_"v[64:67], a[188:191], a[128:131], v[64:67]\n" \
"v_cmp_eq_u32_e64 s[6:7], 1, v180\n" \
"v_cndmask_b32_e64 v182, 0, v181, vcc\n" \
M_"v[64:67], a[252:255], a[192:195], v[64:67]\n" \
R_"a[128:131], v28 offset:32768\n" \
"v_cndmask_b32_e64 v183, 0, v181, s[6:7]\n" \
M_"v[80:83], a[188:191], a[132:135], v[80:83]\n" \
R_"a[192:195], v29 offset:32768\n" \
"v_accvgpr_write_b32 a124, v182\n" \
M_"v[80:83], a[252:255], a[196:199], v[80:83]\n" \
"v_accvgpr_write_b32 a125, v183\n" \
M_"v[76:79], a[184:187], a[132:135], v[76:79]\n" \
M_"v[76:79], a[248:251], a[196:199], v[76:79]\n" \
M_"v[72:75], a[180:183], a[132:135], v[72:75]\n" \
M_"v[72:75], a[244:247], a[196:199], v[72:75]\n" \
M_"v[68:71], a[176:179], a[132:135], v[68:71]\n" \
M_"v[68:71], a[240:243], a[196:199], v[68:71]\n" \
R_"a[132:135], v28 offset:34816\n" \
R_"a[196:199], v29 offset:34816\n" \
"s_waitcnt lgkmcnt(15)\n" \
"s_nop 0\n" \
M_"a[0:3], a[160:163], a[144:147], a[0:3]\n" \
"s_add_u32 s89, s42, 0x2000\n" \
"buffer_store_dwordx4 v[208:211], v20, s[80:83], s89 offen offset:2048 sc1\n" \
M_"a[0:3], a[224:227], a[208:211], a[0:3]\n" \
X_"a[176:177], v36 offset:32768\n" \
M_"a[4:7], a[164:167], a[144:147], a[4:7]\n" \
X_"a[178:179], v36 offset:33792\n" \
M_"a[4:7], a[228:231], a[208:211], a[4:7]\n" \
X_"a[240:241], v36 offset:40960\n" \
M_"a[12:15], a[172:175], a[144:147], a[12:15]\n" \
X_"a[242:243], v36 offset:41984\n" \
"s_waitcnt lgkmcnt(15)\n" \
M_"a[12:15], a[236:239], a[208:211], a[12:15]\n" \
"s_waitcnt lgkmcnt(12)\n" \
M_"a[8:11], a[144:147], a[168:171], a[8:11]\n" \
X_"a[180:181], v37 offset:32768\n" \
"s_waitcnt lgkmcnt(9)\n" \
"s_nop 0\n" \
M_"a[8:11], a[208:211], a[232:235], a[8:11]\n" \
M_"a[24:27], a[148:151], a[168:171], a[24:27]\n" \
X_"a[182:183], v37 offset:33792\n" \
M_"a[24:27], a[212:215], a[232:235], a[24:27]\n" \
X_"a[244:245], v37 offset:40960\n" \
M_"a[28:31], a[172:175], a[148:151], a[28:31]\n" \
M_"a[28:31], a[236:239], a[212:215], a[28:31]\n" \
X_"a[246:247], v37 offset:41984\n" \
M_"a[20:23], a[164:167], a[148:151], a[20:23]\n" \
M_"a[20:23], a[228:231], a[212:215], a[20:23]\n" \
X_"a[184:185], v38 offset:32768\n" \
M_"a[16:19], a[160:163], a[148:151], a[16:19]\n" \
X_"a[186:187], v38 offset:33792\n" \
M_"a[16:19], a[224:227], a[212:215], a[16:19]\n" \
M_"a[32:35], a[160:163], a[152:155], a[32:35]\n" \
"s_add_u32 s88, s42, 0x2000\n" \
"buffer_store_dwordx4 v[212:215], v20, s[80:83], s88 offen offset:3072 sc1\n" \
M_"a[32:35], a[224:227], a[216:219], a[32:35]\n" \
X_"a[248:249], v38 offset:40960\n" \
M_"a[36:39], a[164:167], a[152:155], a[36:39]\n" \
"v_lshlrev_b32_e32 v45, 3, v20\n" \
"v_add_u32_e32 v45, s30, v45\n" \
M_"a[36:39], a[228:231], a[216:219], a[36:39]\n" \
"s_getpc_b64 s[86:87]\n" \
"L_ct1_%=:\n" \
"s_add_u32 s86, s86, L_ap0-L_ct1_%=\n" \
"s_addc_u32 s87, s87, 0\n" \
"s_add_u32 m0, s40, 0x2000\n" \
M_"a[44:47], a[172:175], a[152:155], a[44:47]\n" \
"global_load_lds_dword v45, s[86:87]\n" \
M_"a[44:47], a[236:239], a[216:219], a[44:47]\n" \
X_"a[250:251], v38 offset:41984\n" \
M_"a[40:43], a[152:155], a[168:171], a[40:43]\n" \
X_"a[188:189], v39 offset:32768\n" \
M_"a[40:43], a[216:219], a[232:235], a[40:43]\n" \
X_"a[190:191], v39 offset:33792\n" \
M_"a[56:59], a[156:159], a[168:171], a[56:59]\n" \
X_"a[252:253], v39 offset:40960\n" \
M_"a[56:59], a[220:223], a[232:235], a[56:59]\n" \
X_"a[254:255], v39 offset:41984\n" \
M_"a[60:63], a[172:175], a[156:159], a[60:63]\n" \
M_"a[60:63], a[236:239], a[220:223], a[60:63]\n" \
M_"a[52:55], a[164:167], a[156:159], a[52:55]\n" \
M_"a[52:55], a[228:231], a[220:223], a[52:55]\n" \
M_"a[48:51], a[160:163], a[156:159], a[48:51]\n" \
M_"a[48:51], a[224:227], a[220:223], a[48:51]\n" \
M_"a[64:67], a[164:167], a[140:143], a[64:67]\n" \
M_"a[64:67], a[228:231], a[204:207], a[64:67]\n" \
M_"a[80:83], a[172:175], a[140:143], a[80:83]\n" \
M_"a[80:83], a[236:239], a[204:207], a[80:83]\n" \
M_"a[68:71], a[140:143], a[168:171], a[68:71]\n" \
"s_waitcnt vmcnt(3) lgkmcnt(0)\n" \
M_"a[68:71], a[204:207], a[232:235], a[68:71]\n" \
"s_barrier\n" \
"v_xor_b32_e32 v28, 0x20000, v28\n" \
"v_xor_b32_e32 v29, 0x20000, v29\n" \
M_"v[116:119], a[176:179], a[144:147], v[116:119]\n" \
X_"a[160:161], v32 offset:0\n" \
"v_accvgpr_read_b32 v184, a0\n" \
M_"v[116:119], a[240:243], a[208:211], v[116:119]\n" \
"v_accvgpr_read_b32 v185, a1\n" \
"v_accvgpr_read_b32 v186, a2\n" \
M_"v[120:123], a[180:183], a[144:147], v[120:123]\n" \
"v_accvgpr_read_b32 v187, a3\n" \
"v_accvgpr_read_b32 v188, a4\n" \
M_"v[120:123], a[244:247], a[208:211], v[120:123]\n" \
X_"a[162:163], v32 offset:512\n" \
"v_accvgpr_read_b32 v189, a5\n" \
M_"v[124:127], a[184:187], a[144:147], v[124:127]\n" \
"v_accvgpr_read_b32 v190, a6\n" \
"v_accvgpr_read_b32 v191, a7\n" \
M_"v[124:127], a[248:251], a[208:211], v[124:127]\n" \
"v_accvgpr_read_b32 v196, a12\n" \
"v_accvgpr_read_b32 v197, a13\n" \
M_"v[128:131], a[188:191], a[144:147], v[128:131]\n" \
X_"a[224:225], v32 offset:4096\n" \
"v_accvgpr_read_b32 v198, a14\n" \
M_"v[128:131], a[252:255], a[208:211], v[128:131]\n" \
R_"a[144:147], v28 offset:8192\n" \
"v_accvgpr_read_b32 v199, a15\n" \
M_"v[144:147], a[188:191], a[148:151], v[144:147]\n" \
R_"a[208:211], v29 offset:8192\n" \
"v_accvgpr_read_b32 v192, a8\n" \
M_"v[144:147], a[252:255], a[212:215], v[144:147]\n" \
X_"a[226:227], v32 offset:4608\n" \
"v_accvgpr_read_b32 v193, a9\n" \
M_"v[140:143], a[184:187], a[148:151], v[140:143]\n" \
"v_accvgpr_read_b32 v194, a10\n" \
"v_accvgpr_read_b32 v195, a11\n" \
M_"v[140:143], a[248:251], a[212:215], v[140:143]\n" \
"v_accvgpr_read_b32 v208, a24\n" \
"v_accvgpr_read_b32 v209, a25\n" \
M_"v[136:139], a[180:183], a[148:151], v[136:139]\n" \
"v_accvgpr_read_b32 v210, a26\n" \
"v_accvgpr_read_b32 v211, a27\n" \
M_"v[136:139], a[244:247], a[212:215], v[136:139]\n" \
X_"a[164:165], v33 offset:0\n" \
"v_accvgpr_read_b32 v212, a28\n" \
M_"v[132:135], a[176:179], a[148:151], v[132:135]\n" \
"v_accvgpr_read_b32 v213, a29\n" \
"v_accvgpr_read_b32 v214, a30\n" \
M_"v[132:135], a[240:243], a[212:215], v[132:135]\n" \
R_"a[148:151], v28 offset:10240\n" \
"v_accvgpr_read_b32 v215, a31\n" \
M_"v[148:151], a[176:179], a[152:155], v[148:151]\n" \
R_"a[212:215], v29 offset:10240\n" \
"v_accvgpr_read_b32 v204, a20\n" \
M_"v[148:151], a[240:243], a[216:219], v[148:151]\n" \
X_"a[166:167], v33 offset:512\n" \
"v_accvgpr_read_b32 v205, a21\n" \
M_"v[152:155], a[180:183], a[152:155], v[152:155]\n" \
"v_accvgpr_read_b32 v206, a22\n" \
"v_accvgpr_read_b32 v207, a23\n" \
M_"v[152:155], a[244:247], a[216:219], v[152:155]\n" \
X_"a[228:229], v33 offset:4096\n" \
"v_accvgpr_read_b32 v200, a16\n" \
M_"v[156:159], a[184:187], a[152:155], v[156:159]\n" \
"v_accvgpr_read_b32 v201, a17\n" \
"v_accvgpr_read_b32 v202, a18\n" \
M_"v[156:159], a[248:251], a[216:219], v[156:159]\n" \
"v_accvgpr_read_b32 v203, a19\n" \
"v_accvgpr_read_b32 v216, a32\n" \
M_"v[160:163], a[188:191], a[152:155], v[160:163]\n" \
X_"a[230:231], v33 offset:4608\n" \
"v_accvgpr_read_b32 v217, a33\n" \
M_"v[160:163], a[252:255], a[216:219], v[160:163]\n" \
R_"a[152:155], v28 offset:12288\n" \
"v_accvgpr_read_b32 v218, a34\n" \
M_"v[176:179], a[188:191], a[156:159], v[176:179]\n" \
R_"a[216:219], v29 offset:12288\n" \
"v_accvgpr_read_b32 v219, a35\n" \
M_"v[176:179], a[252:255], a[220:223], v[176:179]\n" \
"v_accvgpr_read_b32 v220, a36\n" \
"v_accvgpr_read_b32 v221, a37\n" \
M_"v[172:175], a[184:187], a[156:159], v[172:175]\n" \
X_"a[172:173], v35 offset:0\n" \
"v_accvgpr_read_b32 v222, a38\n" \
M_"v[172:175], a[248:251], a[220:223], v[172:175]\n" \
"v_accvgpr_read_b32 v223, a39\n" \
"v_accvgpr_read_b32 v228, a44\n" \
M_"v[168:171], a[180:183], a[156:159], v[168:171]\n" \
"v_accvgpr_read_b32 v229, a45\n" \
"v_accvgpr_read_b32 v230, a46\n" \
M_"v[168:171], a[244:247], a[220:223], v[168:171]\n" \
X_"a[174:175], v35 offset:512\n" \
"v_accvgpr_read_b32 v231, a47\n" \
M_"v[164:167], a[176:179], a[156:159], v[164:167]\n" \
"v_accvgpr_read_b32 v224, a40\n" \
"v_accvgpr_read_b32 v225, a41\n" \
M_"v[164:167], a[240:243], a[220:223], v[164:167]\n" \
R_"a[156:159], v28 offset:14336\n" \
"v_accvgpr_read_b32 v226, a42\n" \
M_"v[84:87], a[176:179], a[136:139], v[84:87]\n" \
R_"a[220:223], v29 offset:14336\n" \
"v_accvgpr_read_b32 v227, a43\n" \
M_"v[84:87], a[240:243], a[200:203], v[84:87]\n" \
X_"a[236:237], v35 offset:4096\n" \
"v_accvgpr_read_b32 v240, a56\n" \
M_"v[88:91], a[180:183], a[136:139], v[88:91]\n" \
"v_accvgpr_read_b32 v241, a57\n" \
"v_accvgpr_read_b32 v242, a58\n" \
M_"v[88:91], a[244:247], a[200:203], v[88:91]\n" \
X_"a[238:239], v35 offset:4608\n" \
"v_accvgpr_read_b32 v243, a59\n" \
M_"v[92:95], a[184:187], a[136:139], v[92:95]\n" \
"v_accvgpr_read_b32 v244, a60\n" \
"v_accvgpr_read_b32 v245, a61\n" \
M_"v[92:95], a[248:251], a[200:203], v[92:95]\n" \
"v_accvgpr_read_b32 v246, a62\n" \
"v_accvgpr_read_b32 v247, a63\n" \
M_"v[96:99], a[188:191], a[136:139], v[96:99]\n" \
"v_accvgpr_read_b32 v236, a52\n" \
"v_accvgpr_read_b32 v237, a53\n" \
M_"v[96:99], a[252:255], a[200:203], v[96:99]\n" \
R_"a[136:139], v28 offset:4096\n" \
"v_accvgpr_read_b32 v238, a54\n" \
M_"v[112:115], a[188:191], a[140:143], v[112:115]\n" \
R_"a[200:203], v29 offset:4096\n" \
"v_accvgpr_read_b32 v239, a55\n" \
M_"v[112:115], a[252:255], a[204:207], v[112:115]\n" \
X_"a[168:169], v34 offset:0\n" \
"v_accvgpr_read_b32 v232, a48\n" \
M_"v[108:111], a[184:187], a[140:143], v[108:111]\n" \
X_"a[170:171], v34 offset:512\n" \
"v_accvgpr_read_b32 v233, a49\n" \
M_"v[108:111], a[248:251], a[204:207], v[108:111]\n" \
"v_accvgpr_read_b32 v234, a50\n" \
"v_accvgpr_read_b32 v235, a51\n" \
M_"v[104:107], a[180:183], a[140:143], v[104:107]\n" \
"v_accvgpr_read_b32 v180, a64\n" \
"v_accvgpr_read_b32 v181, a65\n" \
M_"v[104:107], a[244:247], a[204:207], v[104:107]\n" \
X_"a[232:233], v34 offset:4096\n" \
"v_accvgpr_read_b32 v182, a66\n" \
M_"v[100:103], a[176:179], a[140:143], v[100:103]\n" \
"v_accvgpr_read_b32 v183, a67\n" \
"v_accvgpr_read_b32 v48, a80\n" \
M_"v[100:103], a[240:243], a[204:207], v[100:103]\n" \
R_"a[140:143], v28 offset:6144\n" \
"v_accvgpr_read_b32 v49, a81\n" \
M_"v[52:55], a[176:179], a[128:131], v[52:55]\n" \
R_"a[204:207], v29 offset:6144\n" \
"v_accvgpr_read_b32 v50, a82\n" \
M_"v[52:55], a[240:243], a[192:195], v[52:55]\n" \
X_"a[234:235], v34 offset:4608\n" \
"v_accvgpr_read_b32 v51, a83\n" \
M_"v[56:59], a[180:183], a[128:131], v[56:59]\n" \
"v_accvgpr_read_b32 v252, a68\n" \
"v_accvgpr_read_b32 v253, a69\n" \
M_"v[56:59], a[244:247], a[192:195], v[56:59]\n" \
"v_accvgpr_read_b32 v254, a70\n" \
"v_accvgpr_read_b32 v255, a71\n" \
M_"v[60:63], a[184:187], a[128:131], v[60:63]\n" \
"v_lshrrev_b32_e64 v24, 9, v20\n" \
"v_and_b32_e32 v26, 0x1ff, v20\n" \
M_"v[60:63], a[248:251], a[192:195], v[60:63]\n" \
"v_lshl_add_u32 v24, v24, 3, v26\n" \
"v_lshrrev_b32_e64 v25, 8, v20\n" \
M_"v[64:67], a[188:191], a[128:131], v[64:67]\n" \
"v_mul_u32_u24_e32 v25, 0x24700, v25\n" \
"v_add_u32_e64 v25, v25, v20\n" \
M_"v[64:67], a[252:255], a[192:195], v[64:67]\n" \
R_"a[128:131], v28 offset:0\n" \
"v_lshrrev_b32_e64 v26, 2, v20\n" \
M_"v[80:83], a[188:191], a[132:135], v[80:83]\n" \
R_"a[192:195], v29 offset:0\n" \
"v_add_u32_e64 v26, s45, v26\n" \
M_"v[80:83], a[252:255], a[196:199], v[80:83]\n" \
"v_add_u32_e32 v27, s46, v20\n" \
"v_add_u32_e32 v21, s43, v27\n" \
M_"v[76:79], a[184:187], a[132:135], v[76:79]\n" \
M_"v[76:79], a[248:251], a[196:199], v[76:79]\n" \
M_"v[72:75], a[180:183], a[132:135], v[72:75]\n" \
M_"v[72:75], a[244:247], a[196:199], v[72:75]\n" \
M_"v[68:71], a[176:179], a[132:135], v[68:71]\n" \
M_"v[68:71], a[240:243], a[196:199], v[68:71]\n" \
R_"a[132:135], v28 offset:2048\n" \
R_"a[196:199], v29 offset:2048\n" \
"s_waitcnt lgkmcnt(15)\n" \
"s_nop 0\n" \
M_"v[184:187], a[160:163], a[144:147], v[184:187]\n" \
"v_bfe_u32 v44, v20, 2, 5\n" \
X_"a[176:177], v36 offset:0\n" \
M_"v[184:187], a[224:227], a[208:211], v[184:187]\n" \
"buffer_load_dword v44, v44, s[80:83], s38 offen offset:0 sc1\n" \
M_"v[188:191], a[164:167], a[144:147], v[188:191]\n" \
X_"a[178:179], v36 offset:1024\n" \
M_"v[188:191], a[228:231], a[208:211], v[188:191]\n" \
X_"a[240:241], v36 offset:8192\n" \
M_"v[196:199], a[172:175], a[144:147], v[196:199]\n" \
X_"a[242:243], v36 offset:9216\n" \
"s_waitcnt lgkmcnt(15)\n" \
M_"v[196:199], a[236:239], a[208:211], v[196:199]\n" \
"s_waitcnt lgkmcnt(12)\n" \
M_"v[192:195], a[144:147], a[168:171], v[192:195]\n" \
X_"a[180:181], v37 offset:0\n" \
"s_waitcnt lgkmcnt(9)\n" \
"s_nop 0\n" \
M_"v[192:195], a[208:211], a[232:235], v[192:195]\n" \
M_"v[208:211], a[148:151], a[168:171], v[208:211]\n" \
X_"a[182:183], v37 offset:1024\n" \
M_"v[208:211], a[212:215], a[232:235], v[208:211]\n" \
X_"a[244:245], v37 offset:8192\n" \
M_"v[212:215], a[172:175], a[148:151], v[212:215]\n" \
M_"v[212:215], a[236:239], a[212:215], v[212:215]\n" \
X_"a[246:247], v37 offset:9216\n" \
C_"v188, v188, v189\n" \
M_"v[204:207], a[164:167], a[148:151], v[204:207]\n" \
C_"v189, v190, v191\n" \
C_"v190, v198, v199\n" \
M_"v[204:207], a[228:231], a[212:215], v[204:207]\n" \
X_"a[184:185], v38 offset:0\n" \
"v_mov_b32_e32 v191, 0x3f80\n" \
M_"v[200:203], a[160:163], a[148:151], v[200:203]\n" \
X_"a[186:187], v38 offset:1024\n" \
C_"v184, v184, v185\n" \
".fill (L_pb + 77536 - .) / 4, 4, 0xBF800000\n" \
"L_pp19:\n" \
"s_cbranch_execz L_pret\n" \
M_"v[200:203], a[224:227], a[212:215], v[200:203]\n" \
C_"v185, v186, v187\n" \
C_"v186, v196, v197\n" \
M_"v[216:219], a[160:163], a[152:155], v[216:219]\n" \
X_"a[248:249], v38 offset:8192\n" \
"v_mov_b32_e32 v187, 0\n" \
M_"v[216:219], a[224:227], a[216:219], v[216:219]\n" \
X_"a[250:251], v38 offset:9216\n" \
M_"v[220:223], a[164:167], a[152:155], v[220:223]\n" \
M_"v[220:223], a[228:231], a[216:219], v[220:223]\n" \
X_"a[188:189], v39 offset:0\n" \
C_"v204, v204, v205\n" \
M_"v[228:231], a[172:175], a[152:155], v[228:231]\n" \
C_"v205, v206, v207\n" \
C_"v206, v214, v215\n" \
M_"v[228:231], a[236:239], a[216:219], v[228:231]\n" \
X_"a[190:191], v39 offset:1024\n" \
"v_mov_b32_e32 v207, 0x3f80\n" \
M_"v[224:227], a[152:155], a[168:171], v[224:227]\n" \
X_"a[252:253], v39 offset:8192\n" \
C_"v200, v200, v201\n" \
M_"v[224:227], a[216:219], a[232:235], v[224:227]\n" \
C_"v201, v202, v203\n" \
C_"v202, v212, v213\n" \
M_"v[240:243], a[156:159], a[168:171], v[240:243]\n" \
X_"a[254:255], v39 offset:9216\n" \
"v_mov_b32_e64 v203, 0\n" \
M_"v[240:243], a[220:223], a[232:235], v[240:243]\n" \
M_"v[244:247], a[172:175], a[156:159], v[244:247]\n" \
M_"v[244:247], a[236:239], a[220:223], v[244:247]\n" \
C_"v220, v220, v221\n" \
C_"v221, v222, v223\n" \
M_"v[236:239], a[164:167], a[156:159], v[236:239]\n" \
C_"v222, v230, v231\n" \
"v_mov_b32_e32 v223, 0x3f80\n" \
M_"v[236:239], a[228:231], a[220:223], v[236:239]\n" \
C_"v216, v216, v217\n" \
C_"v217, v218, v219\n" \
M_"v[232:235], a[160:163], a[156:159], v[232:235]\n" \
C_"v218, v228, v229\n" \
"v_mov_b32_e64 v219, 0\n" \
M_"v[232:235], a[224:227], a[220:223], v[232:235]\n" \
M_"v[180:183], a[164:167], a[140:143], v[180:183]\n" \
M_"v[180:183], a[228:231], a[204:207], v[180:183]\n" \
M_"v[48:51], a[172:175], a[140:143], v[48:51]\n" \
M_"v[48:51], a[236:239], a[204:207], v[48:51]\n" \
C_"v236, v236, v237\n" \
C_"v237, v238, v239\n" \
M_"v[252:255], a[140:143], a[168:171], v[252:255]\n" \
C_"v238, v246, v247\n" \
"v_mov_b32_e32 v239, 0x3f80\n" \
M_"v[252:255], a[204:207], a[232:235], v[252:255]\n" \
C_"v232, v232, v233\n" \
C_"v233, v234, v235\n" \
"s_waitcnt vmcnt(0) lgkmcnt(0)\n" \
"s_barrier\n" \
"v_mov_b32_e32 v45, s62\n" \
"s_mov_b64 exec, 1\n" \
"buffer_store_dword v45, off, s[80:83], s37 offset:32 sc1\n" \
"s_mov_b64 exec, -1\n" \
"v_cmp_ne_u32_e32 vcc, s62, v44\n" \
"s_nop 4\n" \
"s_cbranch_vccnz L_poll161_%=\n" \
"L_pollr161_%=:\n" \
M_"v[116:119], a[176:179], a[144:147], v[116:119]\n" \
"s_mov_b32 s73, s50\n" \
"s_sub_u32 m0, s31, 0x10000\n" \
M_"v[116:119], a[240:243], a[208:211], v[116:119]\n" \
B_"v20, s[80:83], s73 offen offset:0 sc1 lds\n" \
M_"v[120:123], a[180:183], a[144:147], v[120:123]\n" \
"s_add_u32 s74, s50, 0x4000\n" \
"s_add_u32 s75, s50, 0x8000\n" \
M_"v[120:123], a[244:247], a[208:211], v[120:123]\n" \
"s_mov_b32 s70, 0x3e0293ee\n" \
"s_mov_b32 s76, s52\n" \
M_"v[124:127], a[184:187], a[144:147], v[124:127]\n" \
"v_mul_f32_e32 v116, s70, v116\n" \
"v_mul_f32_e32 v117, s70, v117\n" \
M_"v[124:127], a[248:251], a[208:211], v[124:127]\n" \
B_"v20, s[80:83], s73 offen offset:1024 sc1 lds\n" \
M_"v[128:131], a[188:191], a[144:147], v[128:131]\n" \
"v_mul_f32_e32 v118, s70, v118\n" \
"v_mul_f32_e32 v119, s70, v119\n" \
M_"v[128:131], a[252:255], a[208:211], v[128:131]\n" \
C_"v116, v116, v117\n" \
"v_mul_f32_e64 v120, s70, v120\n" \
M_"v[144:147], a[188:191], a[148:151], v[144:147]\n" \
"v_mul_f32_e32 v124, s70, v124\n" \
"v_mul_f32_e32 v125, s70, v125\n" \
M_"v[144:147], a[252:255], a[212:215], v[144:147]\n" \
B_"v20, s[80:83], s73 offen offset:2048 sc1 lds\n" \
M_"v[140:143], a[184:187], a[148:151], v[140:143]\n" \
"v_mul_f32_e32 v126, s70, v126\n" \
"v_mul_f32_e32 v127, s70, v127\n" \
M_"v[140:143], a[248:251], a[212:215], v[140:143]\n" \
C_"v124, v124, v125\n" \
"v_mul_f32_e64 v121, s70, v121\n" \
M_"v[136:139], a[180:183], a[148:151], v[136:139]\n" \
"v_mul_f32_e32 v122, s70, v122\n" \
"v_mul_f32_e32 v123, s70, v123\n" \
M_"v[136:139], a[244:247], a[212:215], v[136:139]\n" \
B_"v20, s[80:83], s73 offen offset:3072 sc1 lds\n" \
M_"v[132:135], a[176:179], a[148:151], v[132:135]\n" \
"s_sub_u32 m0, s31, s51\n" \
C_"v117, v118, v119\n" \
M_"v[132:135], a[240:243], a[212:215], v[132:135]\n" \
"v_mul_f32_e32 v128, s70, v128\n" \
"v_mul_f32_e64 v129, s70, v129\n" \
M_"v[148:151], a[176:179], a[152:155], v[148:151]\n" \
"v_mul_f32_e32 v130, s70, v130\n" \
"v_mul_f32_e32 v131, s70, v131\n" \
M_"v[148:151], a[240:243], a[216:219], v[148:151]\n" \
B_"v20, s[80:83], s74 offen offset:0 sc1 lds\n" \
M_"v[152:155], a[180:183], a[152:155], v[152:155]\n" \
C_"v125, v126, v127\n" \
C_"v118, v120, v121\n" \
M_"v[152:155], a[244:247], a[216:219], v[152:155]\n" \
C_"v119, v122, v123\n" \
C_"v126, v128, v129\n" \
M_"v[156:159], a[184:187], a[152:155], v[156:159]\n" \
C_"v127, v130, v131\n" \
"v_mul_f32_e64 v132, s70, v132\n" \
M_"v[156:159], a[248:251], a[216:219], v[156:159]\n" \
B_"v20, s[80:83], s74 offen offset:1024 sc1 lds\n" \
M_"v[160:163], a[188:191], a[152:155], v[160:163]\n" \
"ds_write_b128 v21, v[116:119] offset:16384\n" \
M_"v[160:163], a[252:255], a[216:219], v[160:163]\n" \
"v_mul_f32_e32 v133, s70, v133\n" \
"v_mul_f32_e32 v140, s70, v140\n" \
M_"v[176:179], a[188:191], a[156:159], v[176:179]\n" \
"v_mul_f32_e32 v141, s70, v141\n" \
"v_mul_f32_e32 v134, s70, v134\n" \
M_"v[176:179], a[252:255], a[220:223], v[176:179]\n" \
B_"v20, s[80:83], s74 offen offset:2048 sc1 lds\n" \
M_"v[172:175], a[184:187], a[156:159], v[172:175]\n" \
"ds_write_b128 v21, v[124:127] offset:17408\n" \
M_"v[172:175], a[248:251], a[220:223], v[172:175]\n" \
"v_mul_f32_e64 v135, s70, v135\n" \
C_"v132, v132, v133\n" \
M_"v[168:171], a[180:183], a[156:159], v[168:171]\n" \
"v_mul_f32_e32 v142, s70, v142\n" \
"v_mul_f32_e32 v143, s70, v143\n" \
M_"v[168:171], a[244:247], a[220:223], v[168:171]\n" \
B_"v20, s[80:83], s74 offen offset:3072 sc1 lds\n" \
M_"v[164:167], a[176:179], a[156:159], v[164:167]\n" \
"s_sub_u32 m0, s31, 0x4000\n" \
C_"v140, v140, v141\n" \
M_"v[164:167], a[240:243], a[220:223], v[164:167]\n" \
"v_mul_f32_e32 v136, s70, v136\n" \
"v_mul_f32_e32 v137, s70, v137\n" \
M_"v[84:87], a[176:179], a[136:139], v[84:87]\n" \
"v_mul_f32_e32 v138, s70, v138\n" \
"v_mul_f32_e32 v139, s70, v139\n" \
M_"v[84:87], a[240:243], a[200:203], v[84:87]\n" \
B_"v20, s[80:83], s75 offen offset:0 sc1 lds\n" \
M_"v[88:91], a[180:183], a[136:139], v[88:91]\n" \
C_"v133, v134, v135\n" \
"v_mul_f32_e64 v144, s70, v144\n" \
M_"v[88:91], a[244:247], a[200:203], v[88:91]\n" \
"v_mul_f32_e32 v145, s70, v145\n" \
"v_mul_f32_e32 v146, s70, v146\n" \
M_"v[92:95], a[184:187], a[136:139], v[92:95]\n" \
"v_mul_f32_e64 v147, s70, v147\n" \
C_"v141, v142, v143\n" \
M_"v[92:95], a[248:251], a[200:203], v[92:95]\n" \
B_"v20, s[80:83], s75 offen offset:1024 sc1 lds\n" \
M_"v[96:99], a[188:191], a[136:139], v[96:99]\n" \
C_"v134, v136, v137\n" \
C_"v135, v138, v139\n" \
M_"v[96:99], a[252:255], a[200:203], v[96:99]\n" \
C_"v142, v144, v145\n" \
C_"v143, v146, v147\n" \
M_"v[112:115], a[188:191], a[140:143], v[112:115]\n" \
"ds_write_b128 v21, v[132:135] offset:20480\n" \
M_"v[112:115], a[252:255], a[204:207], v[112:115]\n" \
B_"v20, s[80:83], s75 offen offset:2048 sc1 lds\n" \
M_"v[108:111], a[184:187], a[140:143], v[108:111]\n" \
C_"v180, v180, v181\n" \
C_"v181, v182, v183\n" \
M_"v[108:111], a[248:251], a[204:207], v[108:111]\n" \
"ds_write_b128 v21, v[140:143] offset:21504\n" \
M_"v[104:107], a[180:183], a[140:143], v[104:107]\n" \
C_"v234, v244, v245\n" \
"v_mov_b32_e64 v235, 0\n" \
M_"v[104:107], a[244:247], a[204:207], v[104:107]\n" \
B_"v20, s[80:83], s75 offen offset:3072 sc1 lds\n" \
M_"v[100:103], a[176:179], a[140:143], v[100:103]\n" \
"s_mov_b32 m0, s53\n" \
"s_nop 0\n" \
C_"v182, v50, v51\n" \
M_"v[100:103], a[240:243], a[204:207], v[100:103]\n" \
"v_mov_b32_e32 v183, 0x3f80\n" \
C_"v118, v192, v193\n" \
M_"v[52:55], a[176:179], a[128:131], v[52:55]\n" \
C_"v119, v194, v195\n" \
C_"v120, v208, v209\n" \
M_"v[52:55], a[240:243], a[192:195], v[52:55]\n" \
B_"v20, s[80:83], s76 offen offset:0 sc1 lds\n" \
M_"v[56:59], a[180:183], a[128:131], v[56:59]\n" \
C_"v121, v210, v211\n" \
C_"v122, v224, v225\n" \
M_"v[56:59], a[244:247], a[192:195], v[56:59]\n" \
C_"v123, v226, v227\n" \
C_"v124, v240, v241\n" \
C_"v125, v242, v243\n" \
M_"v[60:63], a[184:187], a[128:131], v[60:63]\n" \
M_"v[192:195], v[188:191], v[184:187], 0\n" \
B_"v20, s[80:83], s76 offen offset:1024 sc1 lds\n" \
M_"v[32:35], v[180:183], v[184:187], 0\n" \
M_"v[208:211], v[204:207], v[200:203], 0\n" \
M_"v[36:39], v[188:191], v[200:203], 0\n" \
M_"v[224:227], v[220:223], v[216:219], 0\n" \
M_"v[248:251], v[204:207], v[216:219], 0\n" \
M_"v[240:243], v[236:239], v[232:235], 0\n" \
M_"v[128:131], v[220:223], v[232:235], 0\n" \
"s_mov_b32 s64, 0xaaaaaaaa\n" \
"s_mov_b32 s65, 0xaaaaaaaa\n" \
M_"v[60:63], a[248:251], a[192:195], v[60:63]\n" \
"s_mov_b32 s66, 0xfff0ffff\n" \
"s_mov_b32 s67, 0xf000ff00\n" \
M_"v[64:67], a[188:191], a[128:131], v[64:67]\n" \
"s_mov_b32 s68, 0xffc0fffc\n" \
"s_mov_b32 s69, 0xc000fc00\n" \
"v_cndmask_b32_e64 v192, v192, v193, s[64:65]\n" \
"v_cndmask_b32_e64 v32, v32, v33, s[64:65]\n" \
"v_cndmask_b32_e64 v194, v194, v195, s[64:65]\n" \
"v_cndmask_b32_e64 v34, v34, v35, s[64:65]\n" \
"v_cndmask_b32_e64 v208, v208, v209, s[64:65]\n" \
"v_cndmask_b32_e64 v36, v36, v37, s[64:65]\n" \
"v_cndmask_b32_e64 v210, v210, v211, s[64:65]\n" \
"v_cndmask_b32_e64 v38, v38, v39, s[64:65]\n" \
"v_cndmask_b32_e64 v224, v224, v225, s[64:65]\n" \
"v_cndmask_b32_e64 v248, v248, v249, s[64:65]\n" \
"v_cndmask_b32_e64 v226, v226, v227, s[64:65]\n" \
"v_cndmask_b32_e64 v250, v250, v251, s[64:65]\n" \
"v_cndmask_b32_e64 v240, v240, v241, s[64:65]\n" \
"v_cndmask_b32_e64 v128, v128, v129, s[64:65]\n" \
"v_cndmask_b32_e64 v242, v242, v243, s[64:65]\n" \
"v_cndmask_b32_e64 v130, v130, v131, s[64:65]\n" \
"v_cndmask_b32_e64 v192, v32, v192, s[66:67]\n" \
"v_cndmask_b32_e64 v194, v34, v194, s[68:69]\n" \
"v_cndmask_b32_e64 v208, v36, v208, s[66:67]\n" \
"v_cndmask_b32_e64 v210, v38, v210, s[68:69]\n" \
"v_cndmask_b32_e64 v224, v248, v224, s[66:67]\n" \
"v_cndmask_b32_e64 v226, v250, v226, s[68:69]\n" \
"v_cndmask_b32_e64 v240, v128, v240, s[66:67]\n" \
"v_cndmask_b32_e64 v242, v130, v242, s[68:69]\n" \
M_"v[64:67], a[252:255], a[192:195], v[64:67]\n" \
"v_max_f32_e32 v193, v192, v194\n" \
"v_max_f32_e32 v209, v208, v210\n" \
"v_max_f32_e32 v225, v224, v226\n" \
"v_max_f32_e32 v241, v240, v242\n" \
"v_mul_f32_e32 v148, s70, v148\n" \
"v_permlane32_swap_b32_e32 v193, v225\n" \
"v_permlane32_swap_b32_e32 v209, v241\n" \
"v_mul_f32_e32 v149, s70, v149\n" \
"v_mul_f32_e32 v156, s70, v156\n" \
"v_mul_f32_e32 v157, s70, v157\n" \
"v_permlane32_swap_b32_e32 v196, v228\n" \
"v_permlane32_swap_b32_e32 v212, v244\n" \
"v_max_f32_e32 v193, v193, v225\n" \
"v_max_f32_e32 v209, v209, v241\n" \
"v_mul_f32_e32 v150, s70, v150\n" \
"v_mul_f32_e32 v151, s70, v151\n" \
C_"v148, v148, v149\n" \
"v_mul_f32_e32 v158, s70, v158\n" \
"v_mul_f32_e32 v159, s70, v159\n" \
C_"v156, v156, v157\n" \
"v_permlane16_swap_b32_e32 v228, v244\n" \
"v_permlane16_swap_b32_e32 v193, v209\n" \
M_"v[80:83], a[188:191], a[132:135], v[80:83]\n" \
"s_mov_b32 s72, 0x3eae1a93\n" \
"v_mul_f32_e32 v152, s70, v152\n" \
"v_mul_f32_e32 v153, s70, v153\n" \
"v_mul_f32_e32 v154, s70, v154\n" \
"v_mul_f32_e32 v155, s70, v155\n" \
C_"v149, v150, v151\n" \
"v_mul_f32_e32 v160, s70, v160\n" \
"v_mul_f32_e32 v161, s70, v161\n" \
"v_mul_f32_e32 v162, s70, v162\n" \
"v_mul_f32_e32 v163, s70, v163\n" \
C_"v157, v158, v159\n" \
"v_mul_f32_e32 v45, 0xbfb8aa3b, v228\n" \
"v_mfma_f32_16x16x16_bf16 v[48:51], v[182:183], v[46:47], 0\n" \
"v_mfma_f32_16x16x16_bf16 v[196:199], v[190:191], v[46:47], 0\n" \
"v_mfma_f32_16x16x16_bf16 v[212:215], v[206:207], v[46:47], 0\n" \
"v_max_f32_e64 v193, v193, v209\n" \
C_"v150, v152, v153\n" \
C_"v151, v154, v155\n" \
C_"v158, v160, v161\n" \
C_"v159, v162, v163\n" \
"v_mfma_f32_16x16x16_bf16 v[228:231], v[222:223], v[46:47], 0\n" \
"v_mul_f32_e32 v193, s72, v193\n" \
"v_mov_b32_e32 v209, v193\n" \
"ds_write_b128 v21, v[148:151] offset:24576\n" \
"v_mfma_f32_16x16x16_bf16 v[244:247], v[238:239], v[46:47], 0\n" \
"v_permlane16_swap_b32_e32 v193, v209\n" \
"s_mov_b32 s71, 0x3e0293ee\n" \
M_"v[80:83], a[252:255], a[196:199], v[80:83]\n" \
"v_mov_b32_e32 v225, v193\n" \
"v_mov_b32_e32 v241, v209\n" \
"v_mul_f32_e32 v164, s70, v164\n" \
"v_mul_f32_e64 v165, s70, v165\n" \
"ds_write_b128 v21, v[156:159] offset:25600\n" \
"v_mul_f32_e32 v172, s70, v172\n" \
"v_mul_f32_e32 v173, s70, v173\n" \
"v_pk_mul_f32 v[84:85], v[84:85], s[70:71]\n" \
"v_pk_mul_f32 v[92:93], v[92:93], s[70:71]\n" \
"v_pk_mul_f32 v[100:101], v[100:101], s[70:71]\n" \
"v_pk_mul_f32 v[108:109], v[108:109], s[70:71]\n" \
"v_pk_mul_f32 v[52:53], v[52:53], s[70:71]\n" \
"v_pk_mul_f32 v[60:61], v[60:61], s[70:71]\n" \
"v_permlane32_swap_b32_e32 v193, v225\n" \
"v_permlane32_swap_b32_e32 v209, v241\n" \
"v_mul_f32_e32 v166, s70, v166\n" \
"v_mul_f32_e32 v167, s70, v167\n" \
C_"v164, v164, v165\n" \
"v_mul_f32_e32 v174, s70, v174\n" \
"v_mul_f32_e32 v175, s70, v175\n" \
C_"v172, v172, v173\n" \
"v_pk_mul_f32 v[86:87], v[86:87], s[70:71]\n" \
C_"v84, v84, v85\n" \
"v_pk_mul_f32 v[94:95], v[94:95], s[70:71]\n" \
C_"v92, v92, v93\n" \
"v_pk_mul_f32 v[102:103], v[102:103], s[70:71]\n" \
C_"v100, v100, v101\n" \
"v_pk_mul_f32 v[110:111], v[110:111], s[70:71]\n" \
C_"v108, v108, v109\n" \
"v_pk_mul_f32 v[54:55], v[54:55], s[70:71]\n" \
C_"v52, v52, v53\n" \
"v_pk_mul_f32 v[62:63], v[62:63], s[70:71]\n" \
C_"v60, v60, v61\n" \
"v_fma_f32 v192, v192, s72, -v193\n" \
"v_fma_f32 v194, v194, s72, -v193\n" \
"v_fma_f32 v208, v208, s72, -v209\n" \
"v_fma_f32 v210, v210, s72, -v209\n" \
"v_mul_f32_e32 v168, s70, v168\n" \
"v_mul_f32_e32 v169, s70, v169\n" \
"v_mul_f32_e32 v170, s70, v170\n" \
"v_mul_f32_e32 v171, s70, v171\n" \
C_"v165, v166, v167\n" \
"v_mul_f32_e32 v176, s70, v176\n" \
"v_mul_f32_e32 v177, s70, v177\n" \
"v_mul_f32_e32 v178, s70, v178\n" \
"v_mul_f32_e32 v179, s70, v179\n" \
C_"v173, v174, v175\n" \
"v_pk_mul_f32 v[88:89], v[88:89], s[70:71]\n" \
"v_pk_mul_f32 v[90:91], v[90:91], s[70:71]\n" \
C_"v85, v86, v87\n" \
"v_pk_mul_f32 v[96:97], v[96:97], s[70:71]\n" \
"v_pk_mul_f32 v[98:99], v[98:99], s[70:71]\n" \
C_"v93, v94, v95\n" \
"v_pk_mul_f32 v[104:105], v[104:105], s[70:71]\n" \
"v_pk_mul_f32 v[106:107], v[106:107], s[70:71]\n" \
C_"v101, v102, v103\n" \
"v_pk_mul_f32 v[112:113], v[112:113], s[70:71]\n" \
"v_pk_mul_f32 v[114:115], v[114:115], s[70:71]\n" \
C_"v109, v110, v111\n" \
"v_pk_mul_f32 v[56:57], v[56:57], s[70:71]\n" \
"v_pk_mul_f32 v[58:59], v[58:59], s[70:71]\n" \
C_"v53, v54, v55\n" \
"v_pk_mul_f32 v[64:65], v[64:65], s[70:71]\n" \
"v_pk_mul_f32 v[66:67], v[66:67], s[70:71]\n" \
C_"v61, v62, v63\n" \
"v_pk_mul_f32 v[80:81], v[80:81], s[70:71]\n" \
"v_pk_mul_f32 v[82:83], v[82:83], s[70:71]\n" \
E_"v192, v192\n" \
E_"v194, v194\n" \
E_"v208, v208\n" \
E_"v210, v210\n" \
"v_fma_f32 v224, v224, s72, -v225\n" \
"v_fma_f32 v226, v226, s72, -v225\n" \
M_"v[76:79], a[184:187], a[132:135], v[76:79]\n" \
C_"v166, v168, v169\n" \
C_"v167, v170, v171\n" \
C_"v174, v176, v177\n" \
C_"v175, v178, v179\n" \
C_"v86, v88, v89\n" \
C_"v87, v90, v91\n" \
C_"v94, v96, v97\n" \
C_"v95, v98, v99\n" \
C_"v102, v104, v105\n" \
C_"v103, v106, v107\n" \
C_"v110, v112, v113\n" \
C_"v111, v114, v115\n" \
C_"v54, v56, v57\n" \
C_"v55, v58, v59\n" \
C_"v62, v64, v65\n" \
C_"v63, v66, v67\n" \
E_"v224, v224\n" \
E_"v226, v226\n" \
"v_fma_f32 v240, v240, s72, -v241\n" \
"v_fma_f32 v242, v242, s72, -v241\n" \
"ds_write_b128 v21, v[164:167] offset:28672\n" \
C_"v192, v192, v194\n" \
C_"v208, v208, v210\n" \
E_"v240, v240\n" \
E_"v242, v242\n" \
C_"v132, v48, v49\n" \
C_"v133, v50, v51\n" \
C_"v134, v196, v197\n" \
C_"v135, v198, v199\n" \
"ds_write_b128 v21, v[172:175] offset:29696\n" \
C_"v136, v212, v213\n" \
C_"v137, v214, v215\n" \
"v_perm_b32 v184, v192, v192, v40\n" \
"v_perm_b32 v185, v192, v192, v41\n" \
"v_perm_b32 v186, v192, v192, v42\n" \
"v_perm_b32 v187, v192, v192, v43\n" \
"v_perm_b32 v200, v208, v208, v40\n" \
"v_perm_b32 v201, v208, v208, v41\n" \
"v_perm_b32 v202, v208, v208, v42\n" \
"v_perm_b32 v203, v208, v208, v43\n" \
"ds_write_b128 v27, v[84:87] offset:8192\n" \
C_"v224, v224, v226\n" \
C_"v138, v228, v229\n" \
C_"v139, v230, v231\n" \
M_"v[32:35], v[132:135], v[184:187], 0\n" \
M_"v[36:39], v[134:137], v[200:203], 0\n" \
"v_perm_b32 v216, v224, v224, v40\n" \
"v_perm_b32 v217, v224, v224, v41\n" \
"ds_write_b128 v27, v[92:95] offset:9216\n" \
"v_perm_b32 v218, v224, v224, v42\n" \
"v_perm_b32 v219, v224, v224, v43\n" \
C_"v240, v240, v242\n" \
M_"v[76:79], a[248:251], a[196:199], v[76:79]\n" \
C_"v140, v244, v245\n" \
C_"v141, v246, v247\n" \
M_"v[248:251], v[136:139], v[216:219], 0\n" \
"v_perm_b32 v232, v240, v240, v40\n" \
"v_perm_b32 v233, v240, v240, v41\n" \
"ds_write_b128 v27, v[100:103] offset:12288\n" \
"v_perm_b32 v234, v240, v240, v42\n" \
"v_perm_b32 v235, v240, v240, v43\n" \
"v_pk_mul_f32 v[76:77], v[76:77], s[70:71]\n" \
"v_pk_mul_f32 v[78:79], v[78:79], s[70:71]\n" \
C_"v76, v76, v77\n" \
C_"v77, v78, v79\n" \
C_"v78, v80, v81\n" \
C_"v79, v82, v83\n" \
M_"v[128:131], v[138:141], v[232:235], 0\n" \
"ds_write_b128 v27, v[108:111] offset:13312\n" \
M_"v[72:75], a[180:183], a[132:135], v[72:75]\n" \
C_"v116, v252, v253\n" \
C_"v117, v254, v255\n" \
M_"v[72:75], a[244:247], a[196:199], v[72:75]\n" \
"v_exp_f32_e64 v45, v45\n" \
M_"v[192:195], v[116:119], v[184:187], 0\n" \
"ds_write_b128 v27, v[52:55] offset:0\n" \
M_"v[208:211], v[118:121], v[200:203], 0\n" \
M_"v[224:227], v[120:123], v[216:219], 0\n" \
M_"v[240:243], v[122:125], v[232:235], 0\n" \
"ds_write_b128 v27, v[60:63] offset:1024\n" \
"v_mov_b32_dpp v34, v38 quad_perm:[0,1,2,3] row_mask:0x2 bank_mask:0xf\n" \
"v_pk_mul_f32 v[72:73], v[72:73], s[70:71]\n" \
"v_pk_mul_f32 v[74:75], v[74:75], s[70:71]\n" \
"v_mov_b32_dpp v34, v250 quad_perm:[0,1,2,3] row_mask:0x4 bank_mask:0xf\n" \
"v_add_f32_e64 v45, 1.0, v45\n" \
M_"v[68:71], a[176:179], a[132:135], v[68:71]\n" \
"v_mov_b32_dpp v34, v130 quad_perm:[0,1,2,3] row_mask:0x8 bank_mask:0xf\n" \
"v_mov_b32_dpp v32, v36 quad_perm:[0,1,2,3] row_mask:0x2 bank_mask:0xf\n" \
"v_rcp_f32_e64 v45, v45\n" \
"ds_write_b128 v27, v[76:79] offset:5120\n" \
"v_mov_b32_dpp v33, v37 quad_perm:[0,1,2,3] row_mask:0x2 bank_mask:0xf\n" \
"v_rcp_f32_e64 v34, v34\n" \
"v_mov_b32_dpp v32, v248 quad_perm:[0,1,2,3] row_mask:0x4 bank_mask:0xf\n" \
"v_mov_b32_dpp v33, v249 quad_perm:[0,1,2,3] row_mask:0x4 bank_mask:0xf\n" \
"v_mul_f32_e64 v34, v45, v34\n" \
"v_mov_b32_dpp v32, v128 quad_perm:[0,1,2,3] row_mask:0x8 bank_mask:0xf\n" \
"v_mov_b32_dpp v33, v129 quad_perm:[0,1,2,3] row_mask:0x8 bank_mask:0xf\n" \
"v_mul_f32_e32 v32, v34, v32\n" \
"v_mul_f32_e32 v33, v34, v33\n" \
"v_mov_b32_e64 v38, v34\n" \
M_"v[68:71], a[240:243], a[196:199], v[68:71]\n" \
"v_sub_f32_e32 v44, 1.0, v45\n" \
"v_permlane16_swap_b32_e32 v34, v38\n" \
"v_mov_b32_e32 v250, v34\n" \
"v_mov_b32_e32 v130, v38\n" \
C_"v32, v32, v33\n" \
"v_permlane32_swap_b32_e32 v34, v250\n" \
"v_permlane32_swap_b32_e32 v38, v130\n" \
"v_mul_f32_e64 v192, v34, v192\n" \
"v_pk_mul_f32 v[68:69], v[68:69], s[70:71]\n" \
"v_pk_mul_f32 v[70:71], v[70:71], s[70:71]\n" \
C_"v68, v68, v69\n" \
C_"v69, v70, v71\n" \
C_"v70, v72, v73\n" \
C_"v71, v74, v75\n" \
"ds_write_b128 v27, v[68:71] offset:4096\n" \
"v_mul_f32_e32 v193, v34, v193\n" \
"v_mul_f32_e32 v208, v38, v208\n" \
"v_mul_f32_e32 v209, v38, v209\n" \
"v_mul_f32_e32 v224, v250, v224\n" \
"v_mul_f32_e32 v225, v250, v225\n" \
"v_mul_f32_e32 v240, v130, v240\n" \
"v_mul_f32_e32 v241, v130, v241\n" \
"v_mul_f32_e32 v194, v34, v194\n" \
"v_mul_f32_e64 v195, v34, v195\n" \
C_"v192, v192, v193\n" \
"v_mul_f32_e32 v210, v38, v210\n" \
"v_mul_f32_e32 v211, v38, v211\n" \
C_"v208, v208, v209\n" \
"v_mul_f32_e32 v226, v250, v226\n" \
"v_mul_f32_e32 v227, v250, v227\n" \
C_"v224, v224, v225\n" \
"v_mul_f32_e32 v242, v130, v242\n" \
"v_mul_f32_e32 v243, v130, v243\n" \
C_"v240, v240, v241\n" \
C_"v193, v194, v195\n" \
"s_add_u32 s84, s48, 0x0\n" \
"s_nop 0\n" \
C_"v209, v210, v211\n" \
"s_add_u32 s85, s48, 0x24800\n" \
C_"v225, v226, v227\n" \
"s_add_u32 s86, s48, 0x49000\n" \
C_"v241, v242, v243\n" \
"s_add_u32 s87, s48, 0x6d800\n" \
"ds_write_b32 v26, v44\n" \
"buffer_store_dword v32, v25, s[80:83], s49 offen sc1\n" \
"buffer_store_dwordx2 v[192:193], v24, s[80:83], s84 offen sc1\n" \
"buffer_store_dwordx2 v[208:209], v24, s[80:83], s85 offen sc1\n" \
"buffer_store_dwordx2 v[224:225], v24, s[80:83], s86 offen sc1\n" \
"buffer_store_dwordx2 v[240:241], v24, s[80:83], s87 offen sc1\n" \
"s_branch L_end_%=\n" \
"L_poll61_%=:\n" \
"s_mov_b32 s84, 0\n" \
"L_pl61_%=:\n" \
"s_sleep 1\n" \
"v_bfe_u32 v44, v20, 2, 5\n" \
"buffer_load_dword v44, v44, s[80:83], s38 offen offset:128 sc1\n" \
"s_waitcnt vmcnt(0)\n" \
"v_cmp_ne_u32_e32 vcc, s62, v44\n" \
"s_add_u32 s84, s84, 1\n" \
"s_nop 3\n" \
"s_cbranch_vccz L_pollr61_%=\n" \
"s_cmp_gt_u32 s84, 0x400000\n" \
"s_cbranch_scc0 L_pl61_%=\n" \
"s_branch L_pollr61_%=\n" \
"L_poll161_%=:\n" \
"s_mov_b32 s84, 0\n" \
"L_pl161_%=:\n" \
"s_sleep 1\n" \
"v_bfe_u32 v44, v20, 2, 5\n" \
"buffer_load_dword v44, v44, s[80:83], s38 offen offset:0 sc1\n" \
"s_waitcnt vmcnt(0)\n" \
"v_cmp_ne_u32_e32 vcc, s62, v44\n" \
"s_add_u32 s84, s84, 1\n" \
"s_nop 3\n" \
"s_cbranch_vccz L_pollr161_%=\n" \
"s_cmp_gt_u32 s84, 0x400000\n" \
"s_cbranch_scc0 L_pl161_%=\n" \
"s_branch L_pollr161_%=\n" \
"L_end_%=:\n"
#define PROJ_PAGES 20
#define PROJ_VCLOB "v21","v24","v25","v28","v29","v32","v33","v34","v35","v36","v37","v38","v39","v40","v41","v42","v43","v44","v45","v46","v47","v48","v49","v50","v51","v52","v53","v54","v55","v56","v57","v58","v59","v60","v61","v62","v63","v64","v65","v66","v67","v68","v69","v70","v71","v72","v73","v74","v75","v76","v77","v78","v79","v80","v81","v82","v83","v84","v85","v86","v87","v88","v89","v90","v91","v92","v93","v94","v95","v96","v97","v98","v99","v100","v101","v102","v103","v104","v105","v106","v107","v108","v109","v110","v111","v112","v113","v114","v115","v116","v117","v118","v119","v120","v121","v122","v123","v124","v125","v126","v127","v128","v129","v130","v131","v132","v133","v134","v135","v136","v137","v138","v139","v140","v141","v142","v143","v144","v145","v146","v147","v148","v149","v150","v151","v152","v153","v154","v155","v156","v157","v158","v159","v160","v161","v162","v163","v164","v165","v166","v167","v168","v169","v170","v171","v172","v173","v174","v175","v176","v177","v178","v179","v180","v181","v182","v183","v184","v185","v186","v187","v188","v189","v190","v192","v193","v194","v195","v196","v197","v198","v199","v200","v201","v202","v203","v204","v205","v206","v207","v208","v209","v210","v211","v212","v213","v214","v215","v216","v217","v218","v219","v220","v221","v222","v223","v224","v225","v226","v227","v228","v229","v230","v231","v232","v233","v234","v235","v236","v237","v238","v239","v240","v241","v242","v243","v244","v245","v246","v247","v248","v249","v250","v251","v252","v253","v254","v255"
#define PROJ_SCLOB "s4","s5","s6","s7","s32","s33","s34","s35","s36","s37","s38","s39","s40","s41","s42","s43","s44","s45","s46","s47","s48","s49","s50","s51","s52","s53","s54","s55","s63","s64","s65","s66","s67","s68","s69","s70","s71","s72","s73","s74","s75","s76","s77","s78","s79","s80","s81","s82","s83","s84","s85","s86","s87","s88","s89","s90","s91","s92","s93","s94","s95","s96","s97","s98","s99","s100","s101"

typedef const u16* const __attribute__((address_space(4)))* kaptr;
__device__ __forceinline__ void proj_phase(kaptr& ka, char*& ws, const unsigned wid, u32x4 (&vp)[5], unsigned& vl, u32x4 (&sa)[5], u32x2 s24, u32x2& s28, unsigned tdum, u32x4& srdA, u32x2& sd, unsigned& tag
    ) {
    const unsigned b = blockIdx.x, h = (b & 7) * 4 + ((b >> 3) & 3), mt = b >> 5;
    const unsigned rb = (wid < 2u ? 15u - mt : mt) * 128u;
    const unsigned lane = threadIdx.x & 63u, wc = wid & 1u;
    u32x2 vAc, vB;
#pragma unroll
    for (int par = 0; par < 2; ++par) {
        const unsigned lr = lane >> 3, cc = (lane & 7u) ^ (4u * par + (lr >> 1)), r0 = rb + wc * 64u + lr;
        vAc[par] = r0 * (DC * 2) + cc * 16u;
        const unsigned br = lane >> 4, p16 = lane & 15u, sc = (((p16 >> 1) ^ (br | (par << 2))) << 1) | (p16 & 1u);
        vB[par] = (16u * wid + br) * (NBIG * 2) + h * (HD * 2) + sc * 16u;
    }
    vl = lane * 16u;
    u32x2 s30 = {wid * 8192u, 65536u + wid * 4096u};
    asm volatile(PROJ_ASM
                 : "={s62}"(tag), "={s[56:59]}"(srdA), "={s[60:61]}"(sd), "+{s[8:11]}"(sa[0]), "+{s[12:15]}"(sa[1]), "+{s[16:19]}"(sa[2]), "+{s[20:23]}"(sa[3]), "+{s[24:25]}"(s24), "+{s[28:29]}"(s28), "+{s[30:31]}"(s30),
                   "={v[0:3]}"(vp[0]), "={v[4:7]}"(vp[1]), "={v[8:11]}"(vp[2]), "={v[12:15]}"(vp[3]), "={v[16:19]}"(vp[4]), "+{v[22:23]}"(vAc), "+{v[26:27]}"(vB), "+{v191}"(tdum), "+{s[26:27]}"(ws), "+{s[0:1]}"(ka)
                 : "{s2}"(b), "{s3}"(wid), "{v20}"(vl)
                 : "memory", "m0", "vcc", "scc", PROJ_VCLOB, PROJ_SCLOB, ACLOB_LO, ACLOB_HI);
    sa[4] = (u32x4){s24[0], 0u, 0u, 0u};
}

namespace att {
#define DI __device__ __forceinline__
typedef __bf16 bfx8 __attribute__((ext_vector_type(8)));
DI unsigned rfl(unsigned x) { return __builtin_amdgcn_readfirstlane(x); }
DI f32x4 mfma(u32x4 a, u32x4 b, f32x4 c) { return __builtin_amdgcn_mfma_f32_16x16x32_bf16(__builtin_bit_cast(bfx8, a), __builtin_bit_cast(bfx8, b), c, 0, 0, 0); }
typedef __bf16 bfx2 __attribute__((ext_vector_type(2)));
typedef float f32x2 __attribute__((ext_vector_type(2)));
DI unsigned cvtpk2(float lo, float hi) { f32x2 v = {lo, hi}; bfx2 b = __builtin_convertvector(v, bfx2); return __builtin_bit_cast(unsigned, b); }
DI void att_exact(const u16* __restrict__ x, const u16* __restrict__ wq, char* __restrict__ ws, unsigned h, unsigned r0, float g1) {
    const unsigned lane = lane_id(), g = lane >> 4, c = lane & 15u;
    f32x4 o[8];
#pragma unroll
    for (int n = 0; n < 8; ++n) o[n] = (f32x4){0.f, 0.f, 0.f, 0.f};
#pragma unroll 1
    for (unsigned kk = 0; kk < (unsigned)DM; kk += 32u) {
        const unsigned k = kk + 8u * g;
        const u32x4 xf = *reinterpret_cast<const u32x4*>(x + (r0 + c) * DM + k);
#pragma unroll
        for (int n = 0; n < 8; ++n) {
            const u16* w = wq + k * NBIG + h * HD + n * 16 + c; u32x4 wf;
#pragma unroll
            for (int e = 0; e < 4; ++e) wf[e] = (unsigned)w[2 * e * NBIG] | ((unsigned)w[(2 * e + 1) * NBIG] << 16);
            o[n] = mfma(wf, xf, o[n]);
        }
    }
    u32x4 q[4];
#pragma unroll
    for (int p = 0; p < 4; ++p) {
        const f32x4 a = o[2 * p] * 0.12751743082f, b = o[2 * p + 1] * 0.12751743082f;
        q[p] = (u32x4){cvtpk2(a[0], a[1]), cvtpk2(a[2], a[3]), cvtpk2(b[0], b[1]), cvtpk2(b[2], b[3])};
    }
#pragma unroll
    for (int n = 0; n < 8; ++n) o[n] = (f32x4){0.f, 0.f, 0.f, 0.f};
    const __amdgpu_buffer_rsrc_t srd = __builtin_amdgcn_make_buffer_rsrc((void*)(ws + WS_K + (size_t)h * 1048576), (short)0, 1048576, 0x00020000);
    float m = -3.0e38f, l = 0.f;
#pragma unroll 1
    for (unsigned k0 = 0; k0 <= r0 + 15u; k0 += 32u) {
        const unsigned tb = (k0 >> 6) * 32768u + (k0 & 32u) * 256u;
        f32x4 s[2]; float mx = m;
#pragma unroll
        for (int kt = 0; kt < 2; ++kt) {
            s[kt] = (f32x4){0.f, 0.f, 0.f, 0.f};
#pragma unroll
            for (int p = 0; p < 4; ++p) s[kt] = mfma(__builtin_amdgcn_raw_buffer_load_b128(srd, lane * 16u + (kt * 4 + p) * 1024u, tb, 16), q[p], s[kt]);
#pragma unroll
            for (int e = 0; e < 4; ++e) { if (k0 + 16u * kt + 4u * g + e > r0 + c) s[kt][e] = -3.0e38f; mx = fmaxf(mx, s[kt][e]); }
        }
        mx = fmaxf(mx, __shfl_xor(mx, 16)); mx = fmaxf(mx, __shfl_xor(mx, 32));
        const float al = __builtin_amdgcn_exp2f(m - mx); m = mx;
        u32x4 pf;
#pragma unroll
        for (int e = 0; e < 4; ++e) pf[e] = cvtpk2(__builtin_amdgcn_exp2f(s[e >> 1][2 * (e & 1)] - m), __builtin_amdgcn_exp2f(s[e >> 1][2 * (e & 1) + 1] - m));
        l *= al;
#pragma unroll
        for (int e = 0; e < 4; ++e) l += __uint_as_float(pf[e] << 16) + __uint_as_float(pf[e] & 0xffff0000u);
#pragma unroll
        for (int n = 0; n < 8; ++n) o[n] = mfma(__builtin_amdgcn_raw_buffer_load_b128(srd, lane * 16u + 16384u + n * 1024u, tb, 16), pf, o[n] * al);
    }
    l += __shfl_xor(l, 16); l += __shfl_xor(l, 32);
    const float f = g1 / l;
    const __amdgpu_buffer_rsrc_t srdA = __builtin_amdgcn_make_buffer_rsrc((void*)(ws + WS_AF), (short)0, 128 * KS_TOT * 1024, 0x00020000);
#pragma unroll
    for (int p = 0; p < 4; ++p) {
        const f32x4 a = o[2 * p] * f, b = o[2 * p + 1] * f;
        __builtin_amdgcn_raw_buffer_store_b128((u32x4){cvtpk2(a[0], a[1]), cvtpk2(a[2], a[3]), cvtpk2(b[0], b[1]), cvtpk2(b[2], b[3])}, srdA, lane * 16u + p * 1024u, ((r0 >> 4) * KS_TOT + 18u + 4u * h) * 1024u, 16);
    }
}
}

#define ATT_ASM \
"s_branch L_ago_%=\n" \
".p2align 12\n" \
"L_ab:\n" \
".fill (L_ab + 0 - .) / 4, 4, 0xBF800000\n" \
"L_ap0:\n" \
"s_setpc_b64 s[88:89]\n" \
"L_ago_%=:\n" \
"s_mov_b32 s101, 0\n" \
"s_waitcnt vmcnt(15) lgkmcnt(0)\n" \
"s_barrier\n" \
"v_mfma_f32_32x32x16_bf16 v[128:143], a[120:123], a[120:123], 0\n" \
"v_and_b32_e32 v63, 0x300, v20\n" \
R_"v[96:99], v63 offset:0\n" \
"v_bfe_u32 v61, v20, 2, 5\n" \
"s_mov_b32 s96, s24\n" \
"v_add_u32_e32 v58, s22, v20\n" \
R_"a[0:3], v58 offset:0\n" \
"v_mfma_f32_32x32x16_bf16 v[144:159], a[120:123], a[120:123], 0\n" \
"buffer_load_dword v57, v61, s[56:59], s96 offen sc1\n" \
R_"a[16:19], v58 offset:8192\n" \
"v_add_u32_e32 v59, s23, v20\n" \
R_"a[32:35], v59 offset:0\n" \
R_"a[48:51], v59 offset:8192\n" \
"v_mfma_f32_32x32x16_bf16 v[160:175], a[120:123], a[120:123], 0\n" \
R_"v[100:103], v63 offset:1024\n" \
R_"a[4:7], v58 offset:1024\n" \
R_"a[20:23], v58 offset:9216\n" \
"s_sub_u32 s67, s12, 0xc000\n" \
"s_mov_b32 s65, s12\n" \
"v_add_u32_e32 v55, s67, v20\n" \
"v_mfma_f32_32x32x16_bf16 v[176:191], a[120:123], a[120:123], 0\n" \
"v_add_u32_e32 v52, 0xc000, v55\n" \
"s_xor_b32 m0, s65, 0x10000\n" \
R_"a[36:39], v59 offset:1024\n" \
B_"v52, s[8:11], 0 offen offset:0 sc1 lds\n" \
R_"a[52:55], v59 offset:9216\n" \
"v_mfma_f32_32x32x16_bf16 v[192:207], a[120:123], a[120:123], 0\n" \
R_"v[104:107], v63 offset:2048\n" \
R_"a[8:11], v58 offset:2048\n" \
R_"a[24:27], v58 offset:10240\n" \
B_"v52, s[8:11], 0 offen offset:1024 sc1 lds\n" \
R_"a[40:43], v59 offset:2048\n" \
"v_mfma_f32_32x32x16_bf16 v[208:223], a[120:123], a[120:123], 0\n" \
R_"a[56:59], v59 offset:10240\n" \
R_"v[108:111], v63 offset:3072\n" \
R_"a[12:15], v58 offset:3072\n" \
R_"a[28:31], v58 offset:11264\n" \
"s_waitcnt lgkmcnt(15)\n" \
"v_xor_b32_e32 v96, 0x80008000, v96\n" \
"v_mfma_f32_32x32x16_bf16 v[224:239], a[120:123], a[120:123], 0\n" \
"v_xor_b32_e32 v97, 0x80008000, v97\n" \
"v_xor_b32_e32 v98, 0x80008000, v98\n" \
"v_xor_b32_e32 v99, 0x80008000, v99\n" \
R_"a[44:47], v59 offset:3072\n" \
R_"a[60:63], v59 offset:11264\n" \
R_"a[160:163], v20 offset:0\n" \
"v_mfma_f32_32x32x16_bf16 v[240:255], a[120:123], a[120:123], 0\n" \
R_"a[164:167], v20 offset:1024\n" \
"s_waitcnt lgkmcnt(15)\n" \
"v_xor_b32_e32 v100, 0x80008000, v100\n" \
"v_xor_b32_e32 v101, 0x80008000, v101\n" \
"v_xor_b32_e32 v102, 0x80008000, v102\n" \
"v_xor_b32_e32 v103, 0x80008000, v103\n" \
M_"v[32:35], v[96:99], a[0:3], 0\n" \
R_"a[168:171], v20 offset:2048\n" \
R_"a[172:175], v20 offset:3072\n" \
M_"v[36:39], v[96:99], a[16:19], 0\n" \
R_"a[188:191], v20 offset:7168\n" \
R_"a[184:187], v20 offset:6144\n" \
M_"v[40:43], v[96:99], a[32:35], 0\n" \
"s_waitcnt lgkmcnt(15)\n" \
"v_xor_b32_e32 v104, 0x80008000, v104\n" \
M_"v[44:47], v[96:99], a[48:51], 0\n" \
"v_xor_b32_e32 v105, 0x80008000, v105\n" \
"v_xor_b32_e32 v106, 0x80008000, v106\n" \
M_"v[32:35], v[100:103], a[4:7], v[32:35]\n" \
"v_xor_b32_e32 v107, 0x80008000, v107\n" \
R_"a[180:183], v20 offset:5120\n" \
M_"v[36:39], v[100:103], a[20:23], v[36:39]\n" \
R_"a[176:179], v20 offset:4096\n" \
"s_waitcnt lgkmcnt(12)\n" \
"v_xor_b32_e32 v108, 0x80008000, v108\n" \
M_"v[40:43], v[100:103], a[36:39], v[40:43]\n" \
"v_xor_b32_e32 v109, 0x80008000, v109\n" \
"v_xor_b32_e32 v110, 0x80008000, v110\n" \
M_"v[44:47], v[100:103], a[52:55], v[44:47]\n" \
"v_xor_b32_e32 v111, 0x80008000, v111\n" \
"v_add_u32_e32 v60, s15, v20\n" \
M_"v[32:35], v[104:107], a[8:11], v[32:35]\n" \
"v_add_u32_e32 v60, 0x10000, v60\n" \
R_"a[128:131], v20 offset:8192\n" \
M_"v[36:39], v[104:107], a[24:27], v[36:39]\n" \
R_"a[144:147], v20 offset:12288\n" \
R_"a[132:135], v20 offset:9216\n" \
M_"v[40:43], v[104:107], a[40:43], v[40:43]\n" \
R_"a[148:151], v20 offset:13312\n" \
R_"a[136:139], v20 offset:10240\n" \
M_"v[44:47], v[104:107], a[56:59], v[44:47]\n" \
R_"a[152:155], v20 offset:14336\n" \
"s_waitcnt lgkmcnt(13)\n" \
M_"v[32:35], v[108:111], a[12:15], v[32:35]\n" \
R_"a[140:143], v20 offset:11264\n" \
R_"a[156:159], v20 offset:15360\n" \
M_"v[36:39], v[108:111], a[28:31], v[36:39]\n" \
R_"a[64:67], v60 offset:16384\n" \
R_"a[68:71], v60 offset:17408\n" \
M_"v[40:43], v[108:111], a[44:47], v[40:43]\n" \
R_"a[72:75], v60 offset:18432\n" \
R_"a[76:79], v60 offset:19456\n" \
M_"v[44:47], v[108:111], a[60:63], v[44:47]\n" \
R_"a[80:83], v60 offset:24576\n" \
R_"a[84:87], v60 offset:25600\n" \
M_"v[64:67], a[160:163], a[0:3], v[32:35]\n" \
R_"a[88:91], v60 offset:26624\n" \
"s_waitcnt lgkmcnt(15)\n" \
M_"v[64:67], a[164:167], a[4:7], v[64:67]\n" \
R_"a[92:95], v60 offset:27648\n" \
R_"a[192:195], v60 offset:0\n" \
M_"v[64:67], a[168:171], a[8:11], v[64:67]\n" \
R_"a[196:199], v60 offset:1024\n" \
R_"a[200:203], v60 offset:2048\n" \
M_"v[64:67], a[172:175], a[12:15], v[64:67]\n" \
R_"a[204:207], v60 offset:3072\n" \
R_"a[208:211], v60 offset:8192\n" \
M_"v[68:71], a[188:191], a[12:15], v[32:35]\n" \
R_"a[212:215], v60 offset:9216\n" \
R_"a[216:219], v60 offset:10240\n" \
M_"v[68:71], a[184:187], a[8:11], v[68:71]\n" \
R_"a[220:223], v60 offset:11264\n" \
"v_mov_b32_e32 v56, s62\n" \
M_"v[68:71], a[180:183], a[4:7], v[68:71]\n" \
"v_readlane_b32 s68, v11, 0\n" \
"s_mov_b32 m0, s20\n" \
M_"v[68:71], a[176:179], a[0:3], v[68:71]\n" \
"global_load_lds_dword v[16:17], off\n" \
M_"v[76:79], a[176:179], a[16:19], v[36:39]\n" \
E_"v66, v66\n" \
M_"v[76:79], a[180:183], a[20:23], v[76:79]\n" \
E_"v67, v67\n" \
M_"v[76:79], a[184:187], a[24:27], v[76:79]\n" \
E_"v64, v64\n" \
M_"v[76:79], a[188:191], a[28:31], v[76:79]\n" \
E_"v70, v70\n" \
M_"v[72:75], a[172:175], a[28:31], v[36:39]\n" \
E_"v71, v71\n" \
M_"v[72:75], a[168:171], a[24:27], v[72:75]\n" \
E_"v68, v68\n" \
M_"v[72:75], a[164:167], a[20:23], v[72:75]\n" \
E_"v69, v69\n" \
M_"v[72:75], a[160:163], a[16:19], v[72:75]\n" \
C_"v71, v70, v71\n" \
"v_accvgpr_read_b32 v28, a124\n" \
M_"v[80:83], a[160:163], a[32:35], v[40:43]\n" \
E_"v65, v65\n" \
M_"v[80:83], a[164:167], a[36:39], v[80:83]\n" \
E_"v76, v76\n" \
M_"v[80:83], a[168:171], a[40:43], v[80:83]\n" \
E_"v77, v77\n" \
M_"v[80:83], a[172:175], a[44:47], v[80:83]\n" \
E_"v72, v72\n" \
M_"v[84:87], a[188:191], a[44:47], v[40:43]\n" \
E_"v73, v73\n" \
M_"v[84:87], a[184:187], a[40:43], v[84:87]\n" \
E_"v74, v74\n" \
M_"v[84:87], a[180:183], a[36:39], v[84:87]\n" \
E_"v75, v75\n" \
M_"v[84:87], a[176:179], a[32:35], v[84:87]\n" \
C_"v72, v72, v73\n" \
"v_accvgpr_read_b32 v29, a125\n" \
M_"v[92:95], a[176:179], a[48:51], v[44:47]\n" \
E_"v78, v78\n" \
M_"v[92:95], a[180:183], a[52:55], v[92:95]\n" \
"s_waitcnt vmcnt(3)\n" \
"v_cmp_ne_u32_e32 vcc, s62, v57\n" \
M_"v[92:95], a[184:187], a[56:59], v[92:95]\n" \
E_"v79, v79\n" \
M_"v[92:95], a[188:191], a[60:63], v[92:95]\n" \
"v_accvgpr_read_b32 v21, a126\n" \
C_"v70, v68, v69\n" \
M_"v[88:91], a[172:175], a[60:63], v[44:47]\n" \
"s_cbranch_vccnz L_skb_%=\n" \
"L_rkb_%=:\n" \
C_"v73, v74, v75\n" \
"s_mov_b64 exec, 1\n" \
"buffer_store_dword v56, off, s[56:59], s68 offset:0 sc1\n" \
"s_mov_b64 exec, -1\n" \
M_"v[88:91], a[168:171], a[56:59], v[88:91]\n" \
"v_mov_b32_e32 v62, 0\n" \
"s_mov_b32 s64, 0x8000\n" \
M_"v[88:91], a[164:167], a[52:55], v[88:91]\n" \
"v_add_u32_e32 v50, 0x6000, v55\n" \
"v_add_u32_e32 v51, 0xa000, v55\n" \
M_"v[88:91], a[160:163], a[48:51], v[88:91]\n" \
"v_add_u32_e32 v53, 0x10000, v55\n" \
"v_add_u32_e32 v48, 0xc000, v20\n" \
"s_waitcnt vmcnt(5) lgkmcnt(0)\n" \
"s_barrier\n" \
"v_add_u32_e32 v49, s17, v20\n" \
"v_accvgpr_write_b32 a124, v96\n" \
"v_accvgpr_write_b32 a125, v97\n" \
"v_accvgpr_write_b32 a126, v98\n" \
"v_accvgpr_write_b32 a127, v99\n" \
"v_accvgpr_write_b32 a224, v100\n" \
"v_accvgpr_write_b32 a225, v101\n" \
"v_accvgpr_write_b32 a226, v102\n" \
"v_accvgpr_write_b32 a227, v103\n" \
"v_accvgpr_write_b32 a228, v104\n" \
"v_accvgpr_write_b32 a229, v105\n" \
"v_accvgpr_write_b32 a230, v106\n" \
"v_accvgpr_write_b32 a231, v107\n" \
"v_accvgpr_write_b32 a232, v108\n" \
"v_accvgpr_write_b32 a233, v109\n" \
"v_accvgpr_write_b32 a234, v110\n" \
"v_accvgpr_write_b32 a235, v111\n" \
C_"v69, v66, v67\n" \
C_"v68, v64, v65\n" \
C_"v74, v76, v77\n" \
C_"v75, v78, v79\n" \
"s_sub_u32 s66, s18, 1\n" \
"s_cbranch_scc1 L_tr_%=\n" \
"s_branch L_1_%=\n" \
".fill (L_ab + 1528 - .) / 4, 4, 0xBF800000\n" \
"L_tL_1_%=:\n" \
"s_waitcnt vmcnt(4) lgkmcnt(0)\n" \
"v_smfmac_f32_16x16x64_bf16 a[100:103], a[104:107], v[116:123], v21\n" \
"s_barrier\n" \
"L_1_%=:\n" \
M_"v[96:99], a[128:131], a[0:3], v[32:35]\n" \
"s_xor_b32 m0, s65, 0x12000\n" \
R_"a[160:163], v49 offset:0\n" \
M_"v[96:99], a[132:135], a[4:7], v[96:99]\n" \
B_"v50, s[8:11], s64 offen offset:0 sc1 lds\n" \
M_"v[96:99], a[136:139], a[8:11], v[96:99]\n" \
R_"a[164:167], v49 offset:1024\n" \
R_"a[168:171], v49 offset:2048\n" \
M_"v[96:99], a[140:143], a[12:15], v[96:99]\n" \
R_"a[172:175], v49 offset:3072\n" \
R_"a[176:179], v49 offset:4096\n" \
M_"v[100:103], a[156:159], a[12:15], v[32:35]\n" \
R_"a[180:183], v49 offset:5120\n" \
R_"a[184:187], v49 offset:6144\n" \
M_"v[100:103], a[152:155], a[8:11], v[100:103]\n" \
"v_exp_f32_e64 v86, v86\n" \
M_"v[100:103], a[148:151], a[4:7], v[100:103]\n" \
"v_exp_f32_e64 v87, v87\n" \
M_"v[100:103], a[144:147], a[0:3], v[100:103]\n" \
R_"a[188:191], v49 offset:7168\n" \
C_"v87, v86, v87\n" \
M_"v[108:111], a[144:147], a[16:19], v[36:39]\n" \
"v_exp_f32_e64 v84, v84\n" \
M_"v[108:111], a[148:151], a[20:23], v[108:111]\n" \
"v_exp_f32_e64 v85, v85\n" \
M_"v[108:111], a[152:155], a[24:27], v[108:111]\n" \
"v_exp_f32_e64 v82, v82\n" \
M_"v[108:111], a[156:159], a[28:31], v[108:111]\n" \
"v_exp_f32_e64 v83, v83\n" \
M_"v[104:107], a[140:143], a[28:31], v[36:39]\n" \
"v_exp_f32_e64 v80, v80\n" \
M_"v[104:107], a[136:139], a[24:27], v[104:107]\n" \
"v_exp_f32_e64 v81, v81\n" \
M_"v[104:107], a[132:135], a[20:23], v[104:107]\n" \
"v_exp_f32_e64 v88, v88\n" \
M_"v[104:107], a[128:131], a[16:19], v[104:107]\n" \
B_"v50, s[8:11], s64 offen offset:1024 sc1 lds\n" \
M_"v[112:115], a[128:131], a[32:35], v[40:43]\n" \
"s_xor_b32 m0, s65, 0x6000\n" \
C_"v86, v84, v85\n" \
M_"v[112:115], a[132:135], a[36:39], v[112:115]\n" \
C_"v85, v82, v83\n" \
C_"v84, v80, v81\n" \
M_"v[112:115], a[136:139], a[40:43], v[112:115]\n" \
"v_exp_f32_e64 v89, v89\n" \
M_"v[112:115], a[140:143], a[44:47], v[112:115]\n" \
"v_exp_f32_e64 v90, v90\n" \
M_"v[116:119], a[156:159], a[44:47], v[40:43]\n" \
"v_exp_f32_e64 v91, v91\n" \
M_"v[116:119], a[152:155], a[40:43], v[116:119]\n" \
"v_exp_f32_e64 v92, v92\n" \
M_"v[116:119], a[148:151], a[36:39], v[116:119]\n" \
"v_exp_f32_e64 v93, v93\n" \
M_"v[116:119], a[144:147], a[32:35], v[116:119]\n" \
"v_exp_f32_e64 v94, v94\n" \
M_"v[124:127], a[144:147], a[48:51], v[44:47]\n" \
C_"v88, v88, v89\n" \
C_"v89, v90, v91\n" \
M_"v[124:127], a[148:151], a[52:55], v[124:127]\n" \
"v_exp_f32_e64 v95, v95\n" \
M_"v[124:127], a[152:155], a[56:59], v[124:127]\n" \
C_"v90, v92, v93\n" \
C_"v91, v94, v95\n" \
M_"v[124:127], a[156:159], a[60:63], v[124:127]\n" \
R_"a[144:147], v48 offset:4096\n" \
R_"a[148:151], v48 offset:5120\n" \
M_"v[120:123], a[140:143], a[60:63], v[44:47]\n" \
R_"a[152:155], v48 offset:6144\n" \
R_"a[140:143], v48 offset:3072\n" \
M_"v[120:123], a[136:139], a[56:59], v[120:123]\n" \
R_"a[136:139], v48 offset:2048\n" \
R_"a[156:159], v48 offset:7168\n" \
M_"v[120:123], a[132:135], a[52:55], v[120:123]\n" \
B_"v51, s[8:11], s64 offen offset:0 sc1 lds\n" \
M_"v[120:123], a[128:131], a[48:51], v[120:123]\n" \
R_"a[128:131], v48 offset:0\n" \
"s_waitcnt lgkmcnt(7)\n" \
M_"v[128:131], a[160:163], v[68:71], v[128:131]\n" \
E_"v102, v102\n" \
M_"v[160:163], a[160:163], v[72:75], v[160:163]\n" \
"v_exp_f32_e64 v103, v103\n" \
M_"v[132:135], a[164:167], v[68:71], v[132:135]\n" \
R_"a[132:135], v48 offset:1024\n" \
C_"v103, v102, v103\n" \
M_"v[164:167], a[164:167], v[72:75], v[164:167]\n" \
"v_exp_f32_e64 v100, v100\n" \
M_"v[136:139], a[168:171], v[68:71], v[136:139]\n" \
"v_exp_f32_e64 v101, v101\n" \
M_"v[168:171], a[168:171], v[72:75], v[168:171]\n" \
"v_exp_f32_e64 v98, v98\n" \
M_"v[140:143], a[172:175], v[68:71], v[140:143]\n" \
"v_exp_f32_e64 v99, v99\n" \
M_"v[172:175], a[172:175], v[72:75], v[172:175]\n" \
"v_exp_f32_e64 v96, v96\n" \
M_"v[144:147], a[176:179], v[68:71], v[144:147]\n" \
"v_exp_f32_e64 v97, v97\n" \
M_"v[176:179], a[176:179], v[72:75], v[176:179]\n" \
"v_exp_f32_e64 v104, v104\n" \
M_"v[148:151], a[180:183], v[68:71], v[148:151]\n" \
"v_exp_f32_e64 v105, v105\n" \
M_"v[180:183], a[180:183], v[72:75], v[180:183]\n" \
C_"v102, v100, v101\n" \
C_"v101, v98, v99\n" \
M_"v[152:155], a[184:187], v[68:71], v[152:155]\n" \
C_"v100, v96, v97\n" \
C_"v104, v104, v105\n" \
M_"v[184:187], a[184:187], v[72:75], v[184:187]\n" \
B_"v51, s[8:11], s64 offen offset:1024 sc1 lds\n" \
M_"v[156:159], a[188:191], v[68:71], v[156:159]\n" \
"v_exp_f32_e64 v106, v106\n" \
M_"v[188:191], a[188:191], v[72:75], v[188:191]\n" \
"v_exp_f32_e64 v107, v107\n" \
"v_smfmac_f32_16x16x64_bf16 a[96:99], a[104:107], v[68:75], v21\n" \
"v_exp_f32_e64 v108, v108\n" \
M_"v[220:223], a[188:191], v[84:87], v[220:223]\n" \
"v_exp_f32_e64 v109, v109\n" \
M_"v[252:255], a[188:191], v[88:91], v[252:255]\n" \
"v_exp_f32_e64 v110, v110\n" \
M_"v[216:219], a[184:187], v[84:87], v[216:219]\n" \
"v_exp_f32_e64 v111, v111\n" \
M_"v[248:251], a[184:187], v[88:91], v[248:251]\n" \
"v_exp_f32_e64 v118, v118\n" \
M_"v[212:215], a[180:183], v[84:87], v[212:215]\n" \
"v_exp_f32_e64 v119, v119\n" \
M_"v[244:247], a[180:183], v[88:91], v[244:247]\n" \
C_"v105, v106, v107\n" \
C_"v106, v108, v109\n" \
M_"v[208:211], a[176:179], v[84:87], v[208:211]\n" \
C_"v107, v110, v111\n" \
C_"v119, v118, v119\n" \
M_"v[240:243], a[176:179], v[88:91], v[240:243]\n" \
"v_exp_f32_e64 v116, v116\n" \
M_"v[204:207], a[172:175], v[84:87], v[204:207]\n" \
"v_exp_f32_e64 v117, v117\n" \
M_"v[236:239], a[172:175], v[88:91], v[236:239]\n" \
"v_exp_f32_e64 v114, v114\n" \
M_"v[200:203], a[168:171], v[84:87], v[200:203]\n" \
"v_exp_f32_e64 v115, v115\n" \
M_"v[232:235], a[168:171], v[88:91], v[232:235]\n" \
"v_exp_f32_e64 v112, v112\n" \
M_"v[196:199], a[164:167], v[84:87], v[196:199]\n" \
"v_exp_f32_e64 v113, v113\n" \
M_"v[228:231], a[164:167], v[88:91], v[228:231]\n" \
"v_exp_f32_e64 v120, v120\n" \
M_"v[192:195], a[160:163], v[84:87], v[192:195]\n" \
"v_exp_f32_e64 v121, v121\n" \
M_"v[224:227], a[160:163], v[88:91], v[224:227]\n" \
C_"v118, v116, v117\n" \
"s_waitcnt vmcnt(4) lgkmcnt(0)\n" \
"v_smfmac_f32_16x16x64_bf16 a[100:103], a[104:107], v[84:91], v21\n" \
"s_barrier\n" \
M_"v[64:67], a[128:131], a[0:3], v[32:35]\n" \
"s_xor_b32 m0, s65, 0x14000\n" \
R_"a[160:163], v49 offset:8192\n" \
M_"v[64:67], a[132:135], a[4:7], v[64:67]\n" \
B_"v52, s[8:11], s64 offen offset:0 sc1 lds\n" \
M_"v[64:67], a[136:139], a[8:11], v[64:67]\n" \
R_"a[164:167], v49 offset:9216\n" \
R_"a[168:171], v49 offset:10240\n" \
M_"v[64:67], a[140:143], a[12:15], v[64:67]\n" \
R_"a[172:175], v49 offset:11264\n" \
R_"a[176:179], v49 offset:12288\n" \
M_"v[68:71], a[156:159], a[12:15], v[32:35]\n" \
R_"a[180:183], v49 offset:13312\n" \
R_"a[184:187], v49 offset:14336\n" \
M_"v[68:71], a[152:155], a[8:11], v[68:71]\n" \
R_"a[188:191], v49 offset:15360\n" \
C_"v117, v114, v115\n" \
M_"v[68:71], a[148:151], a[4:7], v[68:71]\n" \
C_"v116, v112, v113\n" \
C_"v120, v120, v121\n" \
M_"v[68:71], a[144:147], a[0:3], v[68:71]\n" \
"v_exp_f32_e64 v122, v122\n" \
M_"v[76:79], a[144:147], a[16:19], v[36:39]\n" \
"v_exp_f32_e64 v123, v123\n" \
M_"v[76:79], a[148:151], a[20:23], v[76:79]\n" \
"v_exp_f32_e64 v124, v124\n" \
M_"v[76:79], a[152:155], a[24:27], v[76:79]\n" \
"v_exp_f32_e64 v125, v125\n" \
M_"v[76:79], a[156:159], a[28:31], v[76:79]\n" \
"v_exp_f32_e64 v126, v126\n" \
M_"v[72:75], a[140:143], a[28:31], v[36:39]\n" \
"v_exp_f32_e64 v127, v127\n" \
M_"v[72:75], a[136:139], a[24:27], v[72:75]\n" \
C_"v121, v122, v123\n" \
C_"v122, v124, v125\n" \
M_"v[72:75], a[132:135], a[20:23], v[72:75]\n" \
C_"v123, v126, v127\n" \
"v_add_u32_e32 v49, 0x10000, v48\n" \
M_"v[72:75], a[128:131], a[16:19], v[72:75]\n" \
B_"v52, s[8:11], s64 offen offset:1024 sc1 lds\n" \
M_"v[80:83], a[128:131], a[32:35], v[40:43]\n" \
"v_exp_f32_e64 v70, v70\n" \
M_"v[80:83], a[132:135], a[36:39], v[80:83]\n" \
"v_exp_f32_e64 v71, v71\n" \
M_"v[80:83], a[136:139], a[40:43], v[80:83]\n" \
"s_mov_b32 m0, s65\n" \
C_"v71, v70, v71\n" \
M_"v[80:83], a[140:143], a[44:47], v[80:83]\n" \
E_"v72, v72\n" \
M_"v[84:87], a[156:159], a[44:47], v[40:43]\n" \
"v_exp_f32_e64 v73, v73\n" \
M_"v[84:87], a[152:155], a[40:43], v[84:87]\n" \
"v_exp_f32_e64 v68, v68\n" \
M_"v[84:87], a[148:151], a[36:39], v[84:87]\n" \
"v_exp_f32_e64 v69, v69\n" \
M_"v[84:87], a[144:147], a[32:35], v[84:87]\n" \
"v_exp_f32_e64 v74, v74\n" \
M_"v[92:95], a[144:147], a[48:51], v[44:47]\n" \
R_"a[144:147], v48 offset:12288\n" \
C_"v72, v72, v73\n" \
M_"v[92:95], a[148:151], a[52:55], v[92:95]\n" \
R_"a[148:151], v48 offset:13312\n" \
C_"v70, v68, v69\n" \
M_"v[92:95], a[152:155], a[56:59], v[92:95]\n" \
R_"a[152:155], v48 offset:14336\n" \
"s_xor_b32 s65, s65, 0x4000\n" \
M_"v[92:95], a[156:159], a[60:63], v[92:95]\n" \
"v_exp_f32_e64 v75, v75\n" \
M_"v[88:91], a[140:143], a[60:63], v[44:47]\n" \
R_"a[140:143], v48 offset:11264\n" \
R_"a[156:159], v48 offset:15360\n" \
M_"v[88:91], a[136:139], a[56:59], v[88:91]\n" \
R_"a[136:139], v48 offset:10240\n" \
C_"v73, v74, v75\n" \
M_"v[88:91], a[132:135], a[52:55], v[88:91]\n" \
B_"v53, s[8:11], s64 offen offset:0 sc1 lds\n" \
M_"v[88:91], a[128:131], a[48:51], v[88:91]\n" \
R_"a[128:131], v48 offset:8192\n" \
"s_waitcnt lgkmcnt(7)\n" \
M_"v[128:131], a[160:163], v[100:103], v[128:131]\n" \
E_"v66, v66\n" \
M_"v[160:163], a[160:163], v[104:107], v[160:163]\n" \
"v_exp_f32_e64 v67, v67\n" \
M_"v[132:135], a[164:167], v[100:103], v[132:135]\n" \
R_"a[132:135], v48 offset:9216\n" \
C_"v69, v66, v67\n" \
M_"v[164:167], a[164:167], v[104:107], v[164:167]\n" \
"v_exp_f32_e64 v64, v64\n" \
M_"v[136:139], a[168:171], v[100:103], v[136:139]\n" \
"v_exp_f32_e64 v65, v65\n" \
M_"v[168:171], a[168:171], v[104:107], v[168:171]\n" \
"v_exp_f32_e64 v76, v76\n" \
M_"v[140:143], a[172:175], v[100:103], v[140:143]\n" \
"v_exp_f32_e64 v77, v77\n" \
M_"v[172:175], a[172:175], v[104:107], v[172:175]\n" \
"v_exp_f32_e64 v78, v78\n" \
M_"v[144:147], a[176:179], v[100:103], v[144:147]\n" \
"v_exp_f32_e64 v79, v79\n" \
M_"v[176:179], a[176:179], v[104:107], v[176:179]\n" \
C_"v68, v64, v65\n" \
C_"v74, v76, v77\n" \
M_"v[148:151], a[180:183], v[100:103], v[148:151]\n" \
C_"v75, v78, v79\n" \
"v_xor_b32_e32 v48, 0x4000, v48\n" \
M_"v[180:183], a[180:183], v[104:107], v[180:183]\n" \
M_"v[152:155], a[184:187], v[100:103], v[152:155]\n" \
M_"v[184:187], a[184:187], v[104:107], v[184:187]\n" \
B_"v53, s[8:11], s64 offen offset:1024 sc1 lds\n" \
M_"v[156:159], a[188:191], v[100:103], v[156:159]\n" \
"s_add_u32 s64, s64, 0x8000\n" \
"s_sub_u32 s66, s66, 1\n" \
"s_nop 0\n" \
M_"v[188:191], a[188:191], v[104:107], v[188:191]\n" \
"v_smfmac_f32_16x16x64_bf16 a[96:99], a[104:107], v[100:107], v21\n" \
M_"v[220:223], a[188:191], v[116:119], v[220:223]\n" \
M_"v[252:255], a[188:191], v[120:123], v[252:255]\n" \
M_"v[216:219], a[184:187], v[116:119], v[216:219]\n" \
M_"v[248:251], a[184:187], v[120:123], v[248:251]\n" \
M_"v[212:215], a[180:183], v[116:119], v[212:215]\n" \
M_"v[244:247], a[180:183], v[120:123], v[244:247]\n" \
M_"v[208:211], a[176:179], v[116:119], v[208:211]\n" \
M_"v[240:243], a[176:179], v[120:123], v[240:243]\n" \
M_"v[204:207], a[172:175], v[116:119], v[204:207]\n" \
M_"v[236:239], a[172:175], v[120:123], v[236:239]\n" \
M_"v[200:203], a[168:171], v[116:119], v[200:203]\n" \
M_"v[232:235], a[168:171], v[120:123], v[232:235]\n" \
M_"v[196:199], a[164:167], v[116:119], v[196:199]\n" \
M_"v[228:231], a[164:167], v[120:123], v[228:231]\n" \
M_"v[192:195], a[160:163], v[116:119], v[192:195]\n" \
M_"v[224:227], a[160:163], v[120:123], v[224:227]\n" \
"s_cbranch_scc0 L_tL_1_%=\n" \
"s_waitcnt vmcnt(4) lgkmcnt(0)\n" \
"v_smfmac_f32_16x16x64_bf16 a[100:103], a[104:107], v[116:123], v21\n" \
"s_barrier\n" \
"L_tr_%=:\n" \
"s_setpc_b64 s[28:29]\n" \
".fill (L_ab + 4096 - .) / 4, 4, 0xBF800000\n" \
".fill (L_ab + 4096 - .) / 4, 4, 0xBF800000\n" \
"L_ap1:\n" \
"s_setpc_b64 s[88:89]\n" \
"L_gtr:\n" \
M_"v[96:99], a[128:131], a[0:3], v[32:35]\n" \
"s_xor_b32 m0, s65, 0x12000\n" \
"s_mov_b32 s65, s13\n" \
M_"v[96:99], a[132:135], a[4:7], v[96:99]\n" \
B_"v50, s[8:11], s64 offen offset:0 sc1 lds\n" \
M_"v[96:99], a[136:139], a[8:11], v[96:99]\n" \
"v_add_u32_e32 v63, s15, v20\n" \
"s_add_u32 s67, s14, 40960\n" \
M_"v[96:99], a[140:143], a[12:15], v[96:99]\n" \
R_"a[160:163], v49 offset:0\n" \
R_"a[164:167], v49 offset:1024\n" \
M_"v[100:103], a[156:159], a[12:15], v[32:35]\n" \
B_"v50, s[8:11], s64 offen offset:1024 sc1 lds\n" \
M_"v[100:103], a[152:155], a[8:11], v[100:103]\n" \
"v_add_u32_e32 v50, 0x6000, v63\n" \
"s_xor_b32 m0, s65, 0x8000\n" \
M_"v[100:103], a[148:151], a[4:7], v[100:103]\n" \
R_"a[168:171], v49 offset:2048\n" \
R_"a[172:175], v49 offset:3072\n" \
M_"v[100:103], a[144:147], a[0:3], v[100:103]\n" \
R_"a[176:179], v49 offset:4096\n" \
R_"a[180:183], v49 offset:5120\n" \
M_"v[108:111], a[144:147], a[16:19], v[36:39]\n" \
B_"v50, s[8:11], s67 offen offset:0 sc1 lds\n" \
M_"v[108:111], a[148:151], a[20:23], v[108:111]\n" \
R_"a[184:187], v49 offset:6144\n" \
R_"a[188:191], v49 offset:7168\n" \
M_"v[108:111], a[152:155], a[24:27], v[108:111]\n" \
"s_mov_b32 s64, s14\n" \
"v_add_u32_e32 v51, 0x12000, v63\n" \
M_"v[108:111], a[156:159], a[28:31], v[108:111]\n" \
E_"v86, v86\n" \
M_"v[104:107], a[140:143], a[28:31], v[36:39]\n" \
B_"v50, s[8:11], s67 offen offset:1024 sc1 lds\n" \
M_"v[104:107], a[136:139], a[24:27], v[104:107]\n" \
E_"v87, v87\n" \
M_"v[104:107], a[132:135], a[20:23], v[104:107]\n" \
E_"v84, v84\n" \
M_"v[104:107], a[128:131], a[16:19], v[104:107]\n" \
E_"v85, v85\n" \
M_"v[112:115], a[128:131], a[32:35], v[40:43]\n" \
C_"v87, v86, v87\n" \
C_"v86, v84, v85\n" \
M_"v[112:115], a[132:135], a[36:39], v[112:115]\n" \
B_"v50, s[8:11], s67 offen offset:2048 sc1 lds\n" \
M_"v[112:115], a[136:139], a[40:43], v[112:115]\n" \
E_"v82, v82\n" \
M_"v[112:115], a[140:143], a[44:47], v[112:115]\n" \
E_"v83, v83\n" \
M_"v[116:119], a[156:159], a[44:47], v[40:43]\n" \
E_"v80, v80\n" \
M_"v[116:119], a[152:155], a[40:43], v[116:119]\n" \
B_"v50, s[8:11], s67 offen offset:3072 sc1 lds\n" \
M_"v[116:119], a[148:151], a[36:39], v[116:119]\n" \
"s_xor_b32 m0, s65, 0xa000\n" \
C_"v85, v82, v83\n" \
M_"v[116:119], a[144:147], a[32:35], v[116:119]\n" \
E_"v81, v81\n" \
M_"v[124:127], a[144:147], a[48:51], v[44:47]\n" \
C_"v84, v80, v81\n" \
"v_readlane_b32 s93, v11, 22\n" \
M_"v[124:127], a[148:151], a[52:55], v[124:127]\n" \
B_"v51, s[8:11], s64 offen offset:0 sc1 lds\n" \
M_"v[124:127], a[152:155], a[56:59], v[124:127]\n" \
E_"v88, v88\n" \
M_"v[124:127], a[156:159], a[60:63], v[124:127]\n" \
E_"v89, v89\n" \
M_"v[120:123], a[140:143], a[60:63], v[44:47]\n" \
E_"v90, v90\n" \
M_"v[120:123], a[136:139], a[56:59], v[120:123]\n" \
E_"v91, v91\n" \
M_"v[120:123], a[132:135], a[52:55], v[120:123]\n" \
B_"v51, s[8:11], s64 offen offset:1024 sc1 lds\n" \
M_"v[120:123], a[128:131], a[48:51], v[120:123]\n" \
C_"v88, v88, v89\n" \
"s_waitcnt lgkmcnt(0)\n" \
M_"v[128:131], a[160:163], v[68:71], v[128:131]\n" \
E_"v92, v92\n" \
M_"v[160:163], a[160:163], v[72:75], v[160:163]\n" \
E_"v93, v93\n" \
M_"v[132:135], a[164:167], v[68:71], v[132:135]\n" \
B_"v51, s[8:11], s64 offen offset:2048 sc1 lds\n" \
M_"v[164:167], a[164:167], v[72:75], v[164:167]\n" \
E_"v94, v94\n" \
M_"v[136:139], a[168:171], v[68:71], v[136:139]\n" \
E_"v95, v95\n" \
M_"v[168:171], a[168:171], v[72:75], v[168:171]\n" \
C_"v89, v90, v91\n" \
C_"v90, v92, v93\n" \
M_"v[140:143], a[172:175], v[68:71], v[140:143]\n" \
B_"v51, s[8:11], s64 offen offset:3072 sc1 lds\n" \
M_"v[172:175], a[172:175], v[72:75], v[172:175]\n" \
C_"v91, v94, v95\n" \
"v_readlane_b32 s80, v11, 11\n" \
M_"v[144:147], a[176:179], v[68:71], v[144:147]\n" \
"v_readlane_b32 s81, v11, 12\n" \
"s_mov_b32 s82, 0x47fca0\n" \
M_"v[176:179], a[176:179], v[72:75], v[176:179]\n" \
"s_mov_b32 s83, 0x20000\n" \
"v_readlane_b32 s92, v11, 29\n" \
M_"v[148:151], a[180:183], v[68:71], v[148:151]\n" \
"s_add_u32 m0, s93, 0x0\n" \
R_"a[128:131], v48 offset:0\n" \
M_"v[180:183], a[180:183], v[72:75], v[180:183]\n" \
R_"a[144:147], v48 offset:4096\n" \
R_"a[132:135], v48 offset:1024\n" \
M_"v[152:155], a[184:187], v[68:71], v[152:155]\n" \
B_"v0, s[80:83], s92 offen offset:0 lds\n" \
M_"v[184:187], a[184:187], v[72:75], v[184:187]\n" \
R_"a[148:151], v48 offset:5120\n" \
R_"a[136:139], v48 offset:2048\n" \
M_"v[156:159], a[188:191], v[68:71], v[156:159]\n" \
R_"a[152:155], v48 offset:6144\n" \
R_"a[140:143], v48 offset:3072\n" \
M_"v[188:191], a[188:191], v[72:75], v[188:191]\n" \
R_"a[156:159], v48 offset:7168\n" \
"v_add_u32_e32 v52, 0x14000, v63\n" \
"v_smfmac_f32_16x16x64_bf16 a[96:99], a[104:107], v[68:75], v21\n" \
B_"v1, s[80:83], s92 offen offset:1024 lds\n" \
M_"v[220:223], a[188:191], v[84:87], v[220:223]\n" \
E_"v102, v102\n" \
M_"v[252:255], a[188:191], v[88:91], v[252:255]\n" \
E_"v103, v103\n" \
M_"v[216:219], a[184:187], v[84:87], v[216:219]\n" \
B_"v2, s[80:83], s92 offen offset:2048 lds\n" \
M_"v[248:251], a[184:187], v[88:91], v[248:251]\n" \
"v_add_u32_e32 v53, 0x20000, v63\n" \
C_"v103, v102, v103\n" \
M_"v[212:215], a[180:183], v[84:87], v[212:215]\n" \
E_"v100, v100\n" \
M_"v[244:247], a[180:183], v[88:91], v[244:247]\n" \
E_"v101, v101\n" \
M_"v[208:211], a[176:179], v[84:87], v[208:211]\n" \
B_"v3, s[80:83], s92 offen offset:3072 lds\n" \
M_"v[240:243], a[176:179], v[88:91], v[240:243]\n" \
E_"v98, v98\n" \
M_"v[204:207], a[172:175], v[84:87], v[204:207]\n" \
E_"v99, v99\n" \
M_"v[236:239], a[172:175], v[88:91], v[236:239]\n" \
E_"v96, v96\n" \
M_"v[200:203], a[168:171], v[84:87], v[200:203]\n" \
E_"v97, v97\n" \
M_"v[232:235], a[168:171], v[88:91], v[232:235]\n" \
E_"v104, v104\n" \
M_"v[196:199], a[164:167], v[84:87], v[196:199]\n" \
E_"v105, v105\n" \
M_"v[228:231], a[164:167], v[88:91], v[228:231]\n" \
C_"v102, v100, v101\n" \
C_"v101, v98, v99\n" \
M_"v[192:195], a[160:163], v[84:87], v[192:195]\n" \
C_"v100, v96, v97\n" \
C_"v104, v104, v105\n" \
M_"v[224:227], a[160:163], v[88:91], v[224:227]\n" \
E_"v106, v106\n" \
"v_smfmac_f32_16x16x64_bf16 a[100:103], a[104:107], v[84:91], v21\n" \
E_"v107, v107\n" \
"s_waitcnt vmcnt(14) lgkmcnt(0)\n" \
"s_barrier\n" \
M_"v[64:67], a[128:131], a[0:3], v[32:35]\n" \
"s_xor_b32 m0, s65, 0x18000\n" \
R_"a[160:163], v49 offset:8192\n" \
M_"v[64:67], a[132:135], a[4:7], v[64:67]\n" \
B_"v52, s[8:11], s64 offen offset:0 sc1 lds\n" \
M_"v[64:67], a[136:139], a[8:11], v[64:67]\n" \
R_"a[164:167], v49 offset:9216\n" \
R_"a[168:171], v49 offset:10240\n" \
M_"v[64:67], a[140:143], a[12:15], v[64:67]\n" \
R_"a[172:175], v49 offset:11264\n" \
R_"a[176:179], v49 offset:12288\n" \
M_"v[68:71], a[156:159], a[12:15], v[32:35]\n" \
B_"v52, s[8:11], s64 offen offset:1024 sc1 lds\n" \
M_"v[68:71], a[152:155], a[8:11], v[68:71]\n" \
R_"a[180:183], v49 offset:13312\n" \
R_"a[184:187], v49 offset:14336\n" \
M_"v[68:71], a[148:151], a[4:7], v[68:71]\n" \
E_"v108, v108\n" \
M_"v[68:71], a[144:147], a[0:3], v[68:71]\n" \
E_"v109, v109\n" \
M_"v[76:79], a[144:147], a[16:19], v[36:39]\n" \
E_"v110, v110\n" \
M_"v[76:79], a[148:151], a[20:23], v[76:79]\n" \
B_"v52, s[8:11], s64 offen offset:2048 sc1 lds\n" \
M_"v[76:79], a[152:155], a[24:27], v[76:79]\n" \
E_"v111, v111\n" \
M_"v[76:79], a[156:159], a[28:31], v[76:79]\n" \
C_"v105, v106, v107\n" \
C_"v106, v108, v109\n" \
M_"v[72:75], a[140:143], a[28:31], v[36:39]\n" \
C_"v107, v110, v111\n" \
R_"a[188:191], v49 offset:15360\n" \
M_"v[72:75], a[136:139], a[24:27], v[72:75]\n" \
B_"v52, s[8:11], s64 offen offset:3072 sc1 lds\n" \
M_"v[72:75], a[132:135], a[20:23], v[72:75]\n" \
"s_mov_b32 m0, s65\n" \
"v_add_u32_e32 v49, 0x10000, v48\n" \
M_"v[72:75], a[128:131], a[16:19], v[72:75]\n" \
E_"v118, v118\n" \
M_"v[80:83], a[128:131], a[32:35], v[40:43]\n" \
E_"v119, v119\n" \
M_"v[80:83], a[132:135], a[36:39], v[80:83]\n" \
E_"v116, v116\n" \
M_"v[80:83], a[136:139], a[40:43], v[80:83]\n" \
B_"v53, s[8:11], s64 offen offset:0 sc1 lds\n" \
M_"v[80:83], a[140:143], a[44:47], v[80:83]\n" \
E_"v117, v117\n" \
M_"v[84:87], a[156:159], a[44:47], v[40:43]\n" \
C_"v119, v118, v119\n" \
C_"v118, v116, v117\n" \
M_"v[84:87], a[152:155], a[40:43], v[84:87]\n" \
E_"v114, v114\n" \
M_"v[84:87], a[148:151], a[36:39], v[84:87]\n" \
E_"v115, v115\n" \
M_"v[84:87], a[144:147], a[32:35], v[84:87]\n" \
B_"v53, s[8:11], s64 offen offset:1024 sc1 lds\n" \
M_"v[92:95], a[144:147], a[48:51], v[44:47]\n" \
E_"v112, v112\n" \
M_"v[92:95], a[148:151], a[52:55], v[92:95]\n" \
E_"v113, v113\n" \
M_"v[92:95], a[152:155], a[56:59], v[92:95]\n" \
C_"v117, v114, v115\n" \
C_"v116, v112, v113\n" \
M_"v[92:95], a[156:159], a[60:63], v[92:95]\n" \
B_"v53, s[8:11], s64 offen offset:2048 sc1 lds\n" \
M_"v[88:91], a[140:143], a[60:63], v[44:47]\n" \
E_"v120, v120\n" \
M_"v[88:91], a[136:139], a[56:59], v[88:91]\n" \
E_"v121, v121\n" \
M_"v[88:91], a[132:135], a[52:55], v[88:91]\n" \
E_"v122, v122\n" \
M_"v[88:91], a[128:131], a[48:51], v[88:91]\n" \
C_"v120, v120, v121\n" \
"s_waitcnt lgkmcnt(0)\n" \
M_"v[128:131], a[160:163], v[100:103], v[128:131]\n" \
B_"v53, s[8:11], s64 offen offset:3072 sc1 lds\n" \
M_"v[160:163], a[160:163], v[104:107], v[160:163]\n" \
"s_add_u32 m0, s93, 0x1000\n" \
R_"a[128:131], v48 offset:8192\n" \
M_"v[132:135], a[164:167], v[100:103], v[132:135]\n" \
E_"v123, v123\n" \
M_"v[164:167], a[164:167], v[104:107], v[164:167]\n" \
E_"v124, v124\n" \
M_"v[136:139], a[168:171], v[100:103], v[136:139]\n" \
B_"v4, s[80:83], s92 offen offset:0 lds\n" \
M_"v[168:171], a[168:171], v[104:107], v[168:171]\n" \
E_"v125, v125\n" \
M_"v[140:143], a[172:175], v[100:103], v[140:143]\n" \
E_"v126, v126\n" \
M_"v[172:175], a[172:175], v[104:107], v[172:175]\n" \
E_"v127, v127\n" \
M_"v[144:147], a[176:179], v[100:103], v[144:147]\n" \
C_"v121, v122, v123\n" \
C_"v122, v124, v125\n" \
M_"v[176:179], a[176:179], v[104:107], v[176:179]\n" \
B_"v5, s[80:83], s92 offen offset:1024 lds\n" \
M_"v[148:151], a[180:183], v[100:103], v[148:151]\n" \
C_"v123, v126, v127\n" \
R_"a[144:147], v48 offset:12288\n" \
M_"v[180:183], a[180:183], v[104:107], v[180:183]\n" \
R_"a[132:135], v48 offset:9216\n" \
R_"a[148:151], v48 offset:13312\n" \
M_"v[152:155], a[184:187], v[100:103], v[152:155]\n" \
R_"a[136:139], v48 offset:10240\n" \
R_"a[152:155], v48 offset:14336\n" \
M_"v[184:187], a[184:187], v[104:107], v[184:187]\n" \
R_"a[140:143], v48 offset:11264\n" \
R_"a[156:159], v48 offset:15360\n" \
M_"v[156:159], a[188:191], v[100:103], v[156:159]\n" \
B_"v6, s[80:83], s92 offen offset:2048 lds\n" \
M_"v[188:191], a[188:191], v[104:107], v[188:191]\n" \
E_"v70, v70\n" \
"v_smfmac_f32_16x16x64_bf16 a[96:99], a[104:107], v[100:107], v21\n" \
E_"v71, v71\n" \
M_"v[220:223], a[188:191], v[116:119], v[220:223]\n" \
E_"v72, v72\n" \
M_"v[252:255], a[188:191], v[120:123], v[252:255]\n" \
B_"v7, s[80:83], s92 offen offset:3072 lds\n" \
M_"v[216:219], a[184:187], v[116:119], v[216:219]\n" \
"s_add_u32 m0, s93, 0x2000\n" \
C_"v71, v70, v71\n" \
M_"v[248:251], a[184:187], v[120:123], v[248:251]\n" \
E_"v73, v73\n" \
M_"v[212:215], a[180:183], v[116:119], v[212:215]\n" \
E_"v68, v68\n" \
M_"v[244:247], a[180:183], v[120:123], v[244:247]\n" \
E_"v69, v69\n" \
M_"v[208:211], a[176:179], v[116:119], v[208:211]\n" \
B_"v8, s[80:83], s92 offen offset:0 lds\n" \
M_"v[240:243], a[176:179], v[120:123], v[240:243]\n" \
E_"v74, v74\n" \
M_"v[204:207], a[172:175], v[116:119], v[204:207]\n" \
E_"v75, v75\n" \
M_"v[236:239], a[172:175], v[120:123], v[236:239]\n" \
E_"v66, v66\n" \
M_"v[200:203], a[168:171], v[116:119], v[200:203]\n" \
E_"v67, v67\n" \
M_"v[232:235], a[168:171], v[120:123], v[232:235]\n" \
E_"v64, v64\n" \
M_"v[196:199], a[164:167], v[116:119], v[196:199]\n" \
E_"v65, v65\n" \
M_"v[228:231], a[164:167], v[120:123], v[228:231]\n" \
C_"v72, v72, v73\n" \
C_"v70, v68, v69\n" \
M_"v[192:195], a[160:163], v[116:119], v[192:195]\n" \
E_"v76, v76\n" \
M_"v[224:227], a[160:163], v[120:123], v[224:227]\n" \
E_"v77, v77\n" \
"v_smfmac_f32_16x16x64_bf16 a[100:103], a[104:107], v[116:123], v21\n" \
E_"v78, v78\n" \
"s_waitcnt vmcnt(13) lgkmcnt(0)\n" \
"s_barrier\n" \
E_"v79, v79\n" \
C_"v73, v74, v75\n" \
C_"v69, v66, v67\n" \
C_"v68, v64, v65\n" \
C_"v74, v76, v77\n" \
C_"v75, v78, v79\n" \
"v_add_u32_e32 v48, s16, v20\n" \
"s_xor_b32 s65, s65, 0x8000\n" \
"s_add_u32 s64, s64, 0x10000\n" \
"s_getpc_b64 s[86:87]\n" \
"L_hpt_%=:\n" \
"s_add_u32 s86, s86, L_ap8-L_hpt_%=\n" \
"s_addc_u32 s87, s87, 0\n" \
"s_add_u32 s86, s86, s15\n" \
"s_addc_u32 s87, s87, 0\n" \
"s_mov_b64 exec, 0\n" \
"s_swappc_b64 s[88:89], s[86:87]\n" \
"s_mov_b64 exec, -1\n" \
"s_sub_u32 s66, s19, 1\n" \
"s_cbranch_scc1 L_la_%=\n" \
"s_branch L_2_%=\n" \
".fill (L_ab + 6400 - .) / 4, 4, 0xBF800000\n" \
"L_tL_2_%=:\n" \
"s_waitcnt vmcnt(8) lgkmcnt(0)\n" \
"v_smfmac_f32_16x16x64_bf16 a[100:103], a[104:107], v[116:123], v21\n" \
"s_barrier\n" \
"L_2_%=:\n" \
M_"v[96:99], a[128:131], a[64:67], v[32:35]\n" \
"s_xor_b32 m0, s65, 0x12000\n" \
R_"a[160:163], v49 offset:0\n" \
M_"v[96:99], a[132:135], a[68:71], v[96:99]\n" \
B_"v50, s[8:11], s64 offen offset:0 sc1 lds\n" \
M_"v[96:99], a[136:139], a[72:75], v[96:99]\n" \
R_"a[164:167], v49 offset:1024\n" \
R_"a[168:171], v49 offset:2048\n" \
M_"v[96:99], a[140:143], a[76:79], v[96:99]\n" \
R_"a[172:175], v49 offset:3072\n" \
R_"a[176:179], v49 offset:4096\n" \
M_"v[100:103], a[156:159], a[76:79], v[32:35]\n" \
R_"a[180:183], v49 offset:5120\n" \
R_"a[184:187], v49 offset:6144\n" \
M_"v[100:103], a[152:155], a[72:75], v[100:103]\n" \
"v_exp_f32_e64 v86, v86\n" \
M_"v[100:103], a[148:151], a[68:71], v[100:103]\n" \
"v_exp_f32_e64 v87, v87\n" \
M_"v[100:103], a[144:147], a[64:67], v[100:103]\n" \
B_"v50, s[8:11], s64 offen offset:1024 sc1 lds\n" \
M_"v[108:111], a[144:147], a[80:83], v[36:39]\n" \
R_"a[188:191], v49 offset:7168\n" \
C_"v87, v86, v87\n" \
M_"v[108:111], a[148:151], a[84:87], v[108:111]\n" \
"v_exp_f32_e64 v84, v84\n" \
M_"v[108:111], a[152:155], a[88:91], v[108:111]\n" \
"v_exp_f32_e64 v85, v85\n" \
M_"v[108:111], a[156:159], a[92:95], v[108:111]\n" \
"v_exp_f32_e64 v82, v82\n" \
M_"v[104:107], a[140:143], a[92:95], v[36:39]\n" \
"v_exp_f32_e64 v83, v83\n" \
M_"v[104:107], a[136:139], a[88:91], v[104:107]\n" \
"v_exp_f32_e64 v80, v80\n" \
M_"v[104:107], a[132:135], a[84:87], v[104:107]\n" \
"v_exp_f32_e64 v81, v81\n" \
M_"v[104:107], a[128:131], a[80:83], v[104:107]\n" \
B_"v50, s[8:11], s64 offen offset:2048 sc1 lds\n" \
M_"v[112:115], a[128:131], a[192:195], v[40:43]\n" \
"v_exp_f32_e64 v88, v88\n" \
M_"v[112:115], a[132:135], a[196:199], v[112:115]\n" \
"v_exp_f32_e64 v89, v89\n" \
M_"v[112:115], a[136:139], a[200:203], v[112:115]\n" \
C_"v86, v84, v85\n" \
C_"v85, v82, v83\n" \
M_"v[112:115], a[140:143], a[204:207], v[112:115]\n" \
C_"v84, v80, v81\n" \
C_"v88, v88, v89\n" \
M_"v[116:119], a[156:159], a[204:207], v[40:43]\n" \
"v_exp_f32_e64 v90, v90\n" \
M_"v[116:119], a[152:155], a[200:203], v[116:119]\n" \
"v_exp_f32_e64 v91, v91\n" \
M_"v[116:119], a[148:151], a[196:199], v[116:119]\n" \
B_"v50, s[8:11], s64 offen offset:3072 sc1 lds\n" \
M_"v[116:119], a[144:147], a[192:195], v[116:119]\n" \
"s_xor_b32 m0, s65, 0xa000\n" \
C_"v89, v90, v91\n" \
M_"v[124:127], a[144:147], a[208:211], v[44:47]\n" \
"v_exp_f32_e64 v92, v92\n" \
M_"v[124:127], a[148:151], a[212:215], v[124:127]\n" \
"v_exp_f32_e64 v93, v93\n" \
M_"v[124:127], a[152:155], a[216:219], v[124:127]\n" \
"v_exp_f32_e64 v94, v94\n" \
M_"v[124:127], a[156:159], a[220:223], v[124:127]\n" \
"v_exp_f32_e64 v95, v95\n" \
M_"v[120:123], a[140:143], a[220:223], v[44:47]\n" \
C_"v90, v92, v93\n" \
C_"v91, v94, v95\n" \
M_"v[120:123], a[136:139], a[216:219], v[120:123]\n" \
R_"a[144:147], v48 offset:4096\n" \
R_"a[148:151], v48 offset:5120\n" \
M_"v[120:123], a[132:135], a[212:215], v[120:123]\n" \
B_"v51, s[8:11], s64 offen offset:0 sc1 lds\n" \
M_"v[120:123], a[128:131], a[208:211], v[120:123]\n" \
R_"a[128:131], v48 offset:0\n" \
"s_waitcnt lgkmcnt(3)\n" \
M_"v[128:131], a[160:163], v[68:71], v[128:131]\n" \
R_"a[132:135], v48 offset:1024\n" \
R_"a[136:139], v48 offset:2048\n" \
M_"v[160:163], a[160:163], v[72:75], v[160:163]\n" \
R_"a[152:155], v48 offset:6144\n" \
R_"a[140:143], v48 offset:3072\n" \
M_"v[132:135], a[164:167], v[68:71], v[132:135]\n" \
E_"v102, v102\n" \
M_"v[164:167], a[164:167], v[72:75], v[164:167]\n" \
"v_exp_f32_e64 v103, v103\n" \
M_"v[136:139], a[168:171], v[68:71], v[136:139]\n" \
R_"a[156:159], v48 offset:7168\n" \
C_"v103, v102, v103\n" \
M_"v[168:171], a[168:171], v[72:75], v[168:171]\n" \
B_"v51, s[8:11], s64 offen offset:1024 sc1 lds\n" \
M_"v[140:143], a[172:175], v[68:71], v[140:143]\n" \
"v_exp_f32_e64 v100, v100\n" \
M_"v[172:175], a[172:175], v[72:75], v[172:175]\n" \
"v_exp_f32_e64 v101, v101\n" \
M_"v[144:147], a[176:179], v[68:71], v[144:147]\n" \
"v_exp_f32_e64 v98, v98\n" \
M_"v[176:179], a[176:179], v[72:75], v[176:179]\n" \
"v_exp_f32_e64 v99, v99\n" \
M_"v[148:151], a[180:183], v[68:71], v[148:151]\n" \
"v_exp_f32_e64 v96, v96\n" \
M_"v[180:183], a[180:183], v[72:75], v[180:183]\n" \
"v_exp_f32_e64 v97, v97\n" \
M_"v[152:155], a[184:187], v[68:71], v[152:155]\n" \
"v_exp_f32_e64 v104, v104\n" \
M_"v[184:187], a[184:187], v[72:75], v[184:187]\n" \
B_"v51, s[8:11], s64 offen offset:2048 sc1 lds\n" \
M_"v[156:159], a[188:191], v[68:71], v[156:159]\n" \
"v_exp_f32_e64 v105, v105\n" \
M_"v[188:191], a[188:191], v[72:75], v[188:191]\n" \
C_"v102, v100, v101\n" \
C_"v101, v98, v99\n" \
"v_smfmac_f32_16x16x64_bf16 a[96:99], a[104:107], v[68:75], v21\n" \
C_"v100, v96, v97\n" \
C_"v104, v104, v105\n" \
M_"v[220:223], a[188:191], v[84:87], v[220:223]\n" \
"v_exp_f32_e64 v106, v106\n" \
M_"v[252:255], a[188:191], v[88:91], v[252:255]\n" \
"v_exp_f32_e64 v107, v107\n" \
M_"v[216:219], a[184:187], v[84:87], v[216:219]\n" \
"v_exp_f32_e64 v108, v108\n" \
M_"v[248:251], a[184:187], v[88:91], v[248:251]\n" \
B_"v51, s[8:11], s64 offen offset:3072 sc1 lds\n" \
M_"v[212:215], a[180:183], v[84:87], v[212:215]\n" \
"v_exp_f32_e64 v109, v109\n" \
M_"v[244:247], a[180:183], v[88:91], v[244:247]\n" \
"v_exp_f32_e64 v110, v110\n" \
M_"v[208:211], a[176:179], v[84:87], v[208:211]\n" \
"v_exp_f32_e64 v111, v111\n" \
M_"v[240:243], a[176:179], v[88:91], v[240:243]\n" \
"v_exp_f32_e64 v118, v118\n" \
M_"v[204:207], a[172:175], v[84:87], v[204:207]\n" \
"v_exp_f32_e64 v119, v119\n" \
M_"v[236:239], a[172:175], v[88:91], v[236:239]\n" \
C_"v105, v106, v107\n" \
C_"v106, v108, v109\n" \
M_"v[200:203], a[168:171], v[84:87], v[200:203]\n" \
C_"v107, v110, v111\n" \
C_"v119, v118, v119\n" \
M_"v[232:235], a[168:171], v[88:91], v[232:235]\n" \
"v_exp_f32_e64 v116, v116\n" \
M_"v[196:199], a[164:167], v[84:87], v[196:199]\n" \
"v_exp_f32_e64 v117, v117\n" \
M_"v[228:231], a[164:167], v[88:91], v[228:231]\n" \
"v_exp_f32_e64 v114, v114\n" \
M_"v[192:195], a[160:163], v[84:87], v[192:195]\n" \
"v_exp_f32_e64 v115, v115\n" \
M_"v[224:227], a[160:163], v[88:91], v[224:227]\n" \
C_"v118, v116, v117\n" \
"s_waitcnt vmcnt(8) lgkmcnt(0)\n" \
"v_smfmac_f32_16x16x64_bf16 a[100:103], a[104:107], v[84:91], v21\n" \
"s_barrier\n" \
M_"v[64:67], a[128:131], a[64:67], v[32:35]\n" \
"s_xor_b32 m0, s65, 0x18000\n" \
R_"a[160:163], v49 offset:8192\n" \
M_"v[64:67], a[132:135], a[68:71], v[64:67]\n" \
B_"v52, s[8:11], s64 offen offset:0 sc1 lds\n" \
M_"v[64:67], a[136:139], a[72:75], v[64:67]\n" \
R_"a[164:167], v49 offset:9216\n" \
R_"a[168:171], v49 offset:10240\n" \
M_"v[64:67], a[140:143], a[76:79], v[64:67]\n" \
R_"a[172:175], v49 offset:11264\n" \
R_"a[176:179], v49 offset:12288\n" \
M_"v[68:71], a[156:159], a[76:79], v[32:35]\n" \
R_"a[180:183], v49 offset:13312\n" \
R_"a[184:187], v49 offset:14336\n" \
M_"v[68:71], a[152:155], a[72:75], v[68:71]\n" \
R_"a[188:191], v49 offset:15360\n" \
C_"v117, v114, v115\n" \
M_"v[68:71], a[148:151], a[68:71], v[68:71]\n" \
"v_exp_f32_e64 v112, v112\n" \
M_"v[68:71], a[144:147], a[64:67], v[68:71]\n" \
B_"v52, s[8:11], s64 offen offset:1024 sc1 lds\n" \
M_"v[76:79], a[144:147], a[80:83], v[36:39]\n" \
"v_exp_f32_e64 v113, v113\n" \
M_"v[76:79], a[148:151], a[84:87], v[76:79]\n" \
C_"v116, v112, v113\n" \
"v_add_u32_e32 v49, 0x10000, v48\n" \
M_"v[76:79], a[152:155], a[88:91], v[76:79]\n" \
"v_exp_f32_e64 v120, v120\n" \
M_"v[76:79], a[156:159], a[92:95], v[76:79]\n" \
"v_exp_f32_e64 v121, v121\n" \
M_"v[72:75], a[140:143], a[92:95], v[36:39]\n" \
"v_exp_f32_e64 v122, v122\n" \
M_"v[72:75], a[136:139], a[88:91], v[72:75]\n" \
"v_exp_f32_e64 v123, v123\n" \
M_"v[72:75], a[132:135], a[84:87], v[72:75]\n" \
"v_exp_f32_e64 v124, v124\n" \
M_"v[72:75], a[128:131], a[80:83], v[72:75]\n" \
B_"v52, s[8:11], s64 offen offset:2048 sc1 lds\n" \
M_"v[80:83], a[128:131], a[192:195], v[40:43]\n" \
"v_exp_f32_e64 v125, v125\n" \
M_"v[80:83], a[132:135], a[196:199], v[80:83]\n" \
"v_exp_f32_e64 v126, v126\n" \
M_"v[80:83], a[136:139], a[200:203], v[80:83]\n" \
"v_exp_f32_e64 v127, v127\n" \
M_"v[80:83], a[140:143], a[204:207], v[80:83]\n" \
"v_exp_f32_e64 v70, v70\n" \
M_"v[84:87], a[156:159], a[204:207], v[40:43]\n" \
"v_exp_f32_e64 v71, v71\n" \
M_"v[84:87], a[152:155], a[200:203], v[84:87]\n" \
C_"v120, v120, v121\n" \
C_"v121, v122, v123\n" \
M_"v[84:87], a[148:151], a[196:199], v[84:87]\n" \
B_"v52, s[8:11], s64 offen offset:3072 sc1 lds\n" \
M_"v[84:87], a[144:147], a[192:195], v[84:87]\n" \
"s_mov_b32 m0, s65\n" \
"s_nop 0\n" \
C_"v122, v124, v125\n" \
M_"v[92:95], a[144:147], a[208:211], v[44:47]\n" \
C_"v123, v126, v127\n" \
R_"a[144:147], v48 offset:12288\n" \
M_"v[92:95], a[148:151], a[212:215], v[92:95]\n" \
R_"a[148:151], v48 offset:13312\n" \
C_"v71, v70, v71\n" \
M_"v[92:95], a[152:155], a[216:219], v[92:95]\n" \
R_"a[152:155], v48 offset:14336\n" \
"s_xor_b32 s65, s65, 0x8000\n" \
M_"v[92:95], a[156:159], a[220:223], v[92:95]\n" \
"v_exp_f32_e64 v72, v72\n" \
M_"v[88:91], a[140:143], a[220:223], v[44:47]\n" \
R_"a[140:143], v48 offset:11264\n" \
R_"a[156:159], v48 offset:15360\n" \
M_"v[88:91], a[136:139], a[216:219], v[88:91]\n" \
"v_exp_f32_e64 v73, v73\n" \
M_"v[88:91], a[132:135], a[212:215], v[88:91]\n" \
".fill (L_ab + 8192 - .) / 4, 4, 0xBF800000\n" \
"L_ap2:\n" \
"s_cbranch_execz L_ap0\n" \
B_"v53, s[8:11], s64 offen offset:0 sc1 lds\n" \
M_"v[88:91], a[128:131], a[208:211], v[88:91]\n" \
R_"a[128:131], v48 offset:8192\n" \
"s_waitcnt lgkmcnt(6)\n" \
M_"v[128:131], a[160:163], v[100:103], v[128:131]\n" \
R_"a[132:135], v48 offset:9216\n" \
R_"a[136:139], v48 offset:10240\n" \
M_"v[160:163], a[160:163], v[104:107], v[160:163]\n" \
"v_exp_f32_e64 v68, v68\n" \
M_"v[132:135], a[164:167], v[100:103], v[132:135]\n" \
"v_exp_f32_e64 v69, v69\n" \
M_"v[164:167], a[164:167], v[104:107], v[164:167]\n" \
"v_exp_f32_e64 v74, v74\n" \
M_"v[136:139], a[168:171], v[100:103], v[136:139]\n" \
"v_exp_f32_e64 v75, v75\n" \
M_"v[168:171], a[168:171], v[104:107], v[168:171]\n" \
B_"v53, s[8:11], s64 offen offset:1024 sc1 lds\n" \
M_"v[140:143], a[172:175], v[100:103], v[140:143]\n" \
"v_exp_f32_e64 v66, v66\n" \
M_"v[172:175], a[172:175], v[104:107], v[172:175]\n" \
"v_exp_f32_e64 v67, v67\n" \
M_"v[144:147], a[176:179], v[100:103], v[144:147]\n" \
"v_exp_f32_e64 v64, v64\n" \
M_"v[176:179], a[176:179], v[104:107], v[176:179]\n" \
"v_exp_f32_e64 v65, v65\n" \
M_"v[148:151], a[180:183], v[100:103], v[148:151]\n" \
C_"v72, v72, v73\n" \
C_"v70, v68, v69\n" \
M_"v[180:183], a[180:183], v[104:107], v[180:183]\n" \
"v_exp_f32_e64 v76, v76\n" \
M_"v[152:155], a[184:187], v[100:103], v[152:155]\n" \
"v_exp_f32_e64 v77, v77\n" \
M_"v[184:187], a[184:187], v[104:107], v[184:187]\n" \
B_"v53, s[8:11], s64 offen offset:2048 sc1 lds\n" \
M_"v[156:159], a[188:191], v[100:103], v[156:159]\n" \
"v_exp_f32_e64 v78, v78\n" \
M_"v[188:191], a[188:191], v[104:107], v[188:191]\n" \
"v_exp_f32_e64 v79, v79\n" \
"v_smfmac_f32_16x16x64_bf16 a[96:99], a[104:107], v[100:107], v21\n" \
C_"v73, v74, v75\n" \
C_"v69, v66, v67\n" \
M_"v[220:223], a[188:191], v[116:119], v[220:223]\n" \
C_"v68, v64, v65\n" \
C_"v74, v76, v77\n" \
M_"v[252:255], a[188:191], v[120:123], v[252:255]\n" \
C_"v75, v78, v79\n" \
"v_xor_b32_e32 v48, 0x8000, v48\n" \
M_"v[216:219], a[184:187], v[116:119], v[216:219]\n" \
M_"v[248:251], a[184:187], v[120:123], v[248:251]\n" \
B_"v53, s[8:11], s64 offen offset:3072 sc1 lds\n" \
M_"v[212:215], a[180:183], v[116:119], v[212:215]\n" \
"s_add_u32 s64, s64, 0x10000\n" \
"s_sub_u32 s66, s66, 1\n" \
"s_nop 0\n" \
M_"v[244:247], a[180:183], v[120:123], v[244:247]\n" \
M_"v[208:211], a[176:179], v[116:119], v[208:211]\n" \
M_"v[240:243], a[176:179], v[120:123], v[240:243]\n" \
M_"v[204:207], a[172:175], v[116:119], v[204:207]\n" \
M_"v[236:239], a[172:175], v[120:123], v[236:239]\n" \
M_"v[200:203], a[168:171], v[116:119], v[200:203]\n" \
M_"v[232:235], a[168:171], v[120:123], v[232:235]\n" \
M_"v[196:199], a[164:167], v[116:119], v[196:199]\n" \
M_"v[228:231], a[164:167], v[120:123], v[228:231]\n" \
M_"v[192:195], a[160:163], v[116:119], v[192:195]\n" \
M_"v[224:227], a[160:163], v[120:123], v[224:227]\n" \
"s_cbranch_scc0 L_tL_2_%=\n" \
"s_waitcnt vmcnt(8) lgkmcnt(0)\n" \
"v_smfmac_f32_16x16x64_bf16 a[100:103], a[104:107], v[116:123], v21\n" \
"s_barrier\n" \
"L_la_%=:\n" \
M_"v[96:99], a[128:131], a[64:67], v[32:35]\n" \
"v_readlane_b32 s96, v11, 1\n" \
"v_lshrrev_b32_e32 v16, 1, v20\n" \
M_"v[96:99], a[132:135], a[68:71], v[96:99]\n" \
"s_xor_b32 m0, s65, 0x12000\n" \
R_"a[160:163], v49 offset:0\n" \
M_"v[96:99], a[136:139], a[72:75], v[96:99]\n" \
"buffer_load_dwordx2 v[26:27], v16, s[56:59], s96 offen sc1\n" \
M_"v[96:99], a[140:143], a[76:79], v[96:99]\n" \
B_"v50, s[8:11], s64 offen offset:0 sc1 lds\n" \
M_"v[100:103], a[156:159], a[76:79], v[32:35]\n" \
R_"a[164:167], v49 offset:1024\n" \
R_"a[168:171], v49 offset:2048\n" \
M_"v[100:103], a[152:155], a[72:75], v[100:103]\n" \
R_"a[172:175], v49 offset:3072\n" \
R_"a[176:179], v49 offset:4096\n" \
M_"v[100:103], a[148:151], a[68:71], v[100:103]\n" \
R_"a[180:183], v49 offset:5120\n" \
R_"a[184:187], v49 offset:6144\n" \
M_"v[100:103], a[144:147], a[64:67], v[100:103]\n" \
R_"a[188:191], v49 offset:7168\n" \
"s_mov_b32 s63, s62\n" \
M_"v[108:111], a[144:147], a[80:83], v[36:39]\n" \
E_"v86, v86\n" \
M_"v[108:111], a[148:151], a[84:87], v[108:111]\n" \
"v_exp_f32_e64 v87, v87\n" \
M_"v[108:111], a[152:155], a[88:91], v[108:111]\n" \
B_"v50, s[8:11], s64 offen offset:1024 sc1 lds\n" \
M_"v[108:111], a[156:159], a[92:95], v[108:111]\n" \
"v_exp_f32_e64 v84, v84\n" \
M_"v[104:107], a[140:143], a[92:95], v[36:39]\n" \
"v_exp_f32_e64 v85, v85\n" \
M_"v[104:107], a[136:139], a[88:91], v[104:107]\n" \
C_"v87, v86, v87\n" \
C_"v86, v84, v85\n" \
M_"v[104:107], a[132:135], a[84:87], v[104:107]\n" \
"v_exp_f32_e64 v82, v82\n" \
M_"v[104:107], a[128:131], a[80:83], v[104:107]\n" \
"v_exp_f32_e64 v83, v83\n" \
M_"v[112:115], a[128:131], a[192:195], v[40:43]\n" \
"v_exp_f32_e64 v80, v80\n" \
M_"v[112:115], a[132:135], a[196:199], v[112:115]\n" \
"v_exp_f32_e64 v81, v81\n" \
M_"v[112:115], a[136:139], a[200:203], v[112:115]\n" \
B_"v50, s[8:11], s64 offen offset:2048 sc1 lds\n" \
M_"v[112:115], a[140:143], a[204:207], v[112:115]\n" \
C_"v85, v82, v83\n" \
C_"v84, v80, v81\n" \
M_"v[116:119], a[156:159], a[204:207], v[40:43]\n" \
"v_exp_f32_e64 v88, v88\n" \
M_"v[116:119], a[152:155], a[200:203], v[116:119]\n" \
"v_exp_f32_e64 v89, v89\n" \
M_"v[116:119], a[148:151], a[196:199], v[116:119]\n" \
"v_exp_f32_e64 v90, v90\n" \
M_"v[116:119], a[144:147], a[192:195], v[116:119]\n" \
"v_exp_f32_e64 v91, v91\n" \
M_"v[124:127], a[144:147], a[208:211], v[44:47]\n" \
C_"v88, v88, v89\n" \
C_"v89, v90, v91\n" \
M_"v[124:127], a[148:151], a[212:215], v[124:127]\n" \
B_"v50, s[8:11], s64 offen offset:3072 sc1 lds\n" \
M_"v[124:127], a[152:155], a[216:219], v[124:127]\n" \
"v_exp_f32_e64 v92, v92\n" \
M_"v[124:127], a[156:159], a[220:223], v[124:127]\n" \
"v_exp_f32_e64 v93, v93\n" \
M_"v[120:123], a[140:143], a[220:223], v[44:47]\n" \
"v_exp_f32_e64 v94, v94\n" \
M_"v[120:123], a[136:139], a[216:219], v[120:123]\n" \
"v_exp_f32_e64 v95, v95\n" \
M_"v[120:123], a[132:135], a[212:215], v[120:123]\n" \
C_"v90, v92, v93\n" \
C_"v91, v94, v95\n" \
M_"v[120:123], a[128:131], a[208:211], v[120:123]\n" \
"v_readlane_b32 s97, v11, 39\n" \
"s_waitcnt lgkmcnt(0)\n" \
"s_nop 0\n" \
M_"v[128:131], a[160:163], v[68:71], v[128:131]\n" \
R_"a[128:131], v48 offset:0\n" \
R_"a[144:147], v48 offset:4096\n" \
M_"v[160:163], a[160:163], v[72:75], v[160:163]\n" \
R_"a[132:135], v48 offset:1024\n" \
R_"a[148:151], v48 offset:5120\n" \
M_"v[132:135], a[164:167], v[68:71], v[132:135]\n" \
R_"a[136:139], v48 offset:2048\n" \
R_"a[152:155], v48 offset:6144\n" \
M_"v[164:167], a[164:167], v[72:75], v[164:167]\n" \
R_"a[140:143], v48 offset:3072\n" \
R_"a[156:159], v48 offset:7168\n" \
M_"v[136:139], a[168:171], v[68:71], v[136:139]\n" \
"v_add_u32_e32 v17, 0x49000, v20\n" \
"v_readlane_b32 s98, v11, 40\n" \
M_"v[168:171], a[168:171], v[72:75], v[168:171]\n" \
"v_readlane_b32 s93, v11, 23\n" \
"v_readlane_b32 s84, v11, 15\n" \
M_"v[140:143], a[172:175], v[68:71], v[140:143]\n" \
"v_readlane_b32 s85, v11, 16\n" \
"v_readlane_b32 s86, v11, 17\n" \
M_"v[172:175], a[172:175], v[72:75], v[172:175]\n" \
"s_mov_b32 s87, 0x20000\n" \
"v_readlane_b32 s92, v11, 18\n" \
M_"v[144:147], a[176:179], v[68:71], v[144:147]\n" \
"v_exp_f32_e64 v102, v102\n" \
M_"v[176:179], a[176:179], v[72:75], v[176:179]\n" \
"v_exp_f32_e64 v103, v103\n" \
M_"v[148:151], a[180:183], v[68:71], v[148:151]\n" \
"v_exp_f32_e64 v100, v100\n" \
M_"v[180:183], a[180:183], v[72:75], v[180:183]\n" \
"v_exp_f32_e64 v101, v101\n" \
M_"v[152:155], a[184:187], v[68:71], v[152:155]\n" \
C_"v103, v102, v103\n" \
C_"v102, v100, v101\n" \
M_"v[184:187], a[184:187], v[72:75], v[184:187]\n" \
"v_exp_f32_e64 v98, v98\n" \
M_"v[156:159], a[188:191], v[68:71], v[156:159]\n" \
"v_exp_f32_e64 v99, v99\n" \
M_"v[188:191], a[188:191], v[72:75], v[188:191]\n" \
"s_waitcnt vmcnt(4)\n" \
"v_cmp_ne_u64_e32 vcc, s[62:63], v[26:27]\n" \
"v_smfmac_f32_16x16x64_bf16 a[96:99], a[104:107], v[68:75], v21\n" \
"v_exp_f32_e64 v96, v96\n" \
M_"v[220:223], a[188:191], v[84:87], v[220:223]\n" \
"v_exp_f32_e64 v97, v97\n" \
M_"v[252:255], a[188:191], v[88:91], v[252:255]\n" \
"s_cbranch_vccnz L_sol_%=\n" \
"L_rol_%=:\n" \
C_"v101, v98, v99\n" \
B_"a[124:127], v20, s[56:59], s97 offen offset:0 sc1\n" \
M_"v[216:219], a[184:187], v[84:87], v[216:219]\n" \
C_"v100, v96, v97\n" \
"v_readlane_b32 s91, v11, 24\n" \
M_"v[248:251], a[184:187], v[88:91], v[248:251]\n" \
E_"v104, v104\n" \
M_"v[212:215], a[180:183], v[84:87], v[212:215]\n" \
"v_exp_f32_e64 v105, v105\n" \
M_"v[244:247], a[180:183], v[88:91], v[244:247]\n" \
"v_exp_f32_e64 v106, v106\n" \
M_"v[208:211], a[176:179], v[84:87], v[208:211]\n" \
"v_exp_f32_e64 v107, v107\n" \
M_"v[240:243], a[176:179], v[88:91], v[240:243]\n" \
B_"a[228:231], v17, s[56:59], s97 offen offset:0 sc1\n" \
M_"v[204:207], a[172:175], v[84:87], v[204:207]\n" \
C_"v104, v104, v105\n" \
C_"v105, v106, v107\n" \
M_"v[236:239], a[172:175], v[88:91], v[236:239]\n" \
"v_exp_f32_e64 v108, v108\n" \
M_"v[200:203], a[168:171], v[84:87], v[200:203]\n" \
"v_exp_f32_e64 v109, v109\n" \
M_"v[232:235], a[168:171], v[88:91], v[232:235]\n" \
"v_exp_f32_e64 v110, v110\n" \
M_"v[196:199], a[164:167], v[84:87], v[196:199]\n" \
"v_exp_f32_e64 v111, v111\n" \
M_"v[228:231], a[164:167], v[88:91], v[228:231]\n" \
C_"v106, v108, v109\n" \
C_"v107, v110, v111\n" \
M_"v[192:195], a[160:163], v[84:87], v[192:195]\n" \
"v_exp_f32_e64 v118, v118\n" \
M_"v[224:227], a[160:163], v[88:91], v[224:227]\n" \
"s_xor_b32 s65, s65, 0x8000\n" \
"s_waitcnt vmcnt(7) lgkmcnt(0)\n" \
"v_smfmac_f32_16x16x64_bf16 a[100:103], a[104:107], v[84:91], v21\n" \
"s_barrier\n" \
M_"v[64:67], a[128:131], a[64:67], v[32:35]\n" \
B_"a[236:239], v20, s[56:59], s98 offen offset:0 sc1\n" \
M_"v[64:67], a[132:135], a[68:71], v[64:67]\n" \
"s_add_u32 m0, s93, 0x0\n" \
"s_nop 0\n" \
R_"a[160:163], v49 offset:8192\n" \
M_"v[64:67], a[136:139], a[72:75], v[64:67]\n" \
B_"v0, s[84:87], s92 offen offset:0 lds\n" \
M_"v[64:67], a[140:143], a[76:79], v[64:67]\n" \
R_"a[164:167], v49 offset:9216\n" \
R_"a[168:171], v49 offset:10240\n" \
M_"v[68:71], a[156:159], a[76:79], v[32:35]\n" \
R_"a[172:175], v49 offset:11264\n" \
R_"a[176:179], v49 offset:12288\n" \
M_"v[68:71], a[152:155], a[72:75], v[68:71]\n" \
R_"a[180:183], v49 offset:13312\n" \
R_"a[184:187], v49 offset:14336\n" \
M_"v[68:71], a[148:151], a[68:71], v[68:71]\n" \
B_"v1, s[84:87], s92 offen offset:1024 lds\n" \
M_"v[68:71], a[144:147], a[64:67], v[68:71]\n" \
B_"a[244:247], v17, s[56:59], s98 offen offset:0 sc1\n" \
M_"v[76:79], a[144:147], a[80:83], v[36:39]\n" \
R_"a[188:191], v49 offset:15360\n" \
"v_add_u32_e32 v49, 0x10000, v48\n" \
M_"v[76:79], a[148:151], a[84:87], v[76:79]\n" \
"v_exp_f32_e64 v119, v119\n" \
M_"v[76:79], a[152:155], a[88:91], v[76:79]\n" \
"v_exp_f32_e64 v116, v116\n" \
M_"v[76:79], a[156:159], a[92:95], v[76:79]\n" \
"v_exp_f32_e64 v117, v117\n" \
M_"v[72:75], a[140:143], a[92:95], v[36:39]\n" \
C_"v119, v118, v119\n" \
C_"v118, v116, v117\n" \
M_"v[72:75], a[136:139], a[88:91], v[72:75]\n" \
B_"v2, s[84:87], s92 offen offset:2048 lds\n" \
M_"v[72:75], a[132:135], a[84:87], v[72:75]\n" \
"v_exp_f32_e64 v114, v114\n" \
M_"v[72:75], a[128:131], a[80:83], v[72:75]\n" \
"v_exp_f32_e64 v115, v115\n" \
M_"v[80:83], a[128:131], a[192:195], v[40:43]\n" \
B_"a[224:227], v20, s[56:59], s97 offen offset:1024 sc1\n" \
M_"v[80:83], a[132:135], a[196:199], v[80:83]\n" \
"v_exp_f32_e64 v112, v112\n" \
M_"v[80:83], a[136:139], a[200:203], v[80:83]\n" \
"v_exp_f32_e64 v113, v113\n" \
M_"v[80:83], a[140:143], a[204:207], v[80:83]\n" \
C_"v117, v114, v115\n" \
C_"v116, v112, v113\n" \
M_"v[84:87], a[156:159], a[204:207], v[40:43]\n" \
B_"v3, s[84:87], s92 offen offset:3072 lds\n" \
M_"v[84:87], a[152:155], a[200:203], v[84:87]\n" \
"s_add_u32 m0, s91, 0x1000\n" \
"s_add_u32 s64, s64, 0x10000\n" \
M_"v[84:87], a[148:151], a[196:199], v[84:87]\n" \
"v_exp_f32_e64 v120, v120\n" \
M_"v[84:87], a[144:147], a[192:195], v[84:87]\n" \
"v_exp_f32_e64 v121, v121\n" \
M_"v[92:95], a[144:147], a[208:211], v[44:47]\n" \
"v_exp_f32_e64 v122, v122\n" \
M_"v[92:95], a[148:151], a[212:215], v[92:95]\n" \
B_"a[232:235], v17, s[56:59], s97 offen offset:1024 sc1\n" \
M_"v[92:95], a[152:155], a[216:219], v[92:95]\n" \
B_"v4, s[84:87], s92 offen offset:0 lds\n" \
M_"v[92:95], a[156:159], a[220:223], v[92:95]\n" \
"v_exp_f32_e64 v123, v123\n" \
M_"v[88:91], a[140:143], a[220:223], v[44:47]\n" \
C_"v120, v120, v121\n" \
C_"v121, v122, v123\n" \
M_"v[88:91], a[136:139], a[216:219], v[88:91]\n" \
"v_exp_f32_e64 v124, v124\n" \
M_"v[88:91], a[132:135], a[212:215], v[88:91]\n" \
"v_exp_f32_e64 v125, v125\n" \
M_"v[88:91], a[128:131], a[208:211], v[88:91]\n" \
C_"v122, v124, v125\n" \
"s_waitcnt lgkmcnt(0)\n" \
M_"v[128:131], a[160:163], v[100:103], v[128:131]\n" \
E_"v126, v126\n" \
M_"v[160:163], a[160:163], v[104:107], v[160:163]\n" \
B_"v5, s[84:87], s92 offen offset:1024 lds\n" \
M_"v[132:135], a[164:167], v[100:103], v[132:135]\n" \
B_"a[240:243], v20, s[56:59], s98 offen offset:1024 sc1\n" \
M_"v[164:167], a[164:167], v[104:107], v[164:167]\n" \
"v_exp_f32_e64 v127, v127\n" \
M_"v[136:139], a[168:171], v[100:103], v[136:139]\n" \
C_"v123, v126, v127\n" \
R_"a[128:131], v48 offset:8192\n" \
M_"v[168:171], a[168:171], v[104:107], v[168:171]\n" \
R_"a[144:147], v48 offset:12288\n" \
R_"a[132:135], v48 offset:9216\n" \
M_"v[140:143], a[172:175], v[100:103], v[140:143]\n" \
R_"a[148:151], v48 offset:13312\n" \
R_"a[136:139], v48 offset:10240\n" \
M_"v[172:175], a[172:175], v[104:107], v[172:175]\n" \
R_"a[152:155], v48 offset:14336\n" \
R_"a[140:143], v48 offset:11264\n" \
M_"v[144:147], a[176:179], v[100:103], v[144:147]\n" \
B_"v6, s[84:87], s92 offen offset:2048 lds\n" \
M_"v[176:179], a[176:179], v[104:107], v[176:179]\n" \
"v_exp_f32_e64 v70, v70\n" \
M_"v[148:151], a[180:183], v[100:103], v[148:151]\n" \
"v_exp_f32_e64 v71, v71\n" \
M_"v[180:183], a[180:183], v[104:107], v[180:183]\n" \
B_"a[248:251], v17, s[56:59], s98 offen offset:1024 sc1\n" \
M_"v[152:155], a[184:187], v[100:103], v[152:155]\n" \
R_"a[156:159], v48 offset:15360\n" \
C_"v71, v70, v71\n" \
M_"v[184:187], a[184:187], v[104:107], v[184:187]\n" \
"v_exp_f32_e64 v72, v72\n" \
M_"v[156:159], a[188:191], v[100:103], v[156:159]\n" \
B_"v7, s[84:87], s92 offen offset:3072 lds\n" \
M_"v[188:191], a[188:191], v[104:107], v[188:191]\n" \
"s_add_u32 m0, s91, 0x2000\n" \
"v_xor_b32_e32 v48, 0x8000, v48\n" \
"v_smfmac_f32_16x16x64_bf16 a[96:99], a[104:107], v[100:107], v21\n" \
"v_exp_f32_e64 v73, v73\n" \
M_"v[220:223], a[188:191], v[116:119], v[220:223]\n" \
"v_exp_f32_e64 v68, v68\n" \
M_"v[252:255], a[188:191], v[120:123], v[252:255]\n" \
"v_exp_f32_e64 v69, v69\n" \
M_"v[216:219], a[184:187], v[116:119], v[216:219]\n" \
"v_exp_f32_e64 v74, v74\n" \
M_"v[248:251], a[184:187], v[120:123], v[248:251]\n" \
"v_exp_f32_e64 v75, v75\n" \
M_"v[212:215], a[180:183], v[116:119], v[212:215]\n" \
B_"v8, s[84:87], s92 offen offset:0 lds\n" \
M_"v[244:247], a[180:183], v[120:123], v[244:247]\n" \
"v_exp_f32_e64 v66, v66\n" \
M_"v[208:211], a[176:179], v[116:119], v[208:211]\n" \
"v_exp_f32_e64 v67, v67\n" \
M_"v[240:243], a[176:179], v[120:123], v[240:243]\n" \
"v_exp_f32_e64 v64, v64\n" \
M_"v[204:207], a[172:175], v[116:119], v[204:207]\n" \
"v_exp_f32_e64 v65, v65\n" \
M_"v[236:239], a[172:175], v[120:123], v[236:239]\n" \
C_"v72, v72, v73\n" \
C_"v70, v68, v69\n" \
M_"v[200:203], a[168:171], v[116:119], v[200:203]\n" \
"v_exp_f32_e64 v76, v76\n" \
M_"v[232:235], a[168:171], v[120:123], v[232:235]\n" \
"v_exp_f32_e64 v77, v77\n" \
M_"v[196:199], a[164:167], v[116:119], v[196:199]\n" \
"v_exp_f32_e64 v78, v78\n" \
M_"v[228:231], a[164:167], v[120:123], v[228:231]\n" \
"v_exp_f32_e64 v79, v79\n" \
M_"v[192:195], a[160:163], v[116:119], v[192:195]\n" \
C_"v73, v74, v75\n" \
C_"v69, v66, v67\n" \
M_"v[224:227], a[160:163], v[120:123], v[224:227]\n" \
C_"v68, v64, v65\n" \
"s_waitcnt vmcnt(15) lgkmcnt(0)\n" \
"v_smfmac_f32_16x16x64_bf16 a[100:103], a[104:107], v[116:123], v21\n" \
"s_barrier\n" \
C_"v74, v76, v77\n" \
C_"v75, v78, v79\n" \
"s_cmp_lg_u32 s16, 0\n" \
"s_cbranch_scc1 L_f1_%=\n" \
R_"a[160:163], v49 offset:0\n" \
R_"a[164:167], v49 offset:1024\n" \
R_"a[168:171], v49 offset:2048\n" \
R_"a[172:175], v49 offset:3072\n" \
R_"a[176:179], v49 offset:4096\n" \
R_"a[180:183], v49 offset:5120\n" \
R_"a[184:187], v49 offset:6144\n" \
R_"a[188:191], v49 offset:7168\n" \
"v_add_u32_e32 v54, 0x49000, v20\n" \
B_"a[192:195], v20, s[56:59], s60 offen offset:0 sc1\n" \
"v_add_u32_e32 v50, s15, v20\n" \
"v_add_u32_e32 v51, 0x10000, v50\n" \
"ds_read_b32 v60, v18 offset:0\n" \
"ds_read_b32 v61, v18 offset:128\n" \
R_"a[0:3], v49 offset:8192\n" \
R_"a[4:7], v49 offset:9216\n" \
R_"a[8:11], v49 offset:10240\n" \
R_"a[12:15], v49 offset:11264\n" \
R_"a[16:19], v49 offset:12288\n" \
R_"a[20:23], v49 offset:13312\n" \
R_"a[24:27], v49 offset:14336\n" \
R_"a[28:31], v49 offset:15360\n" \
E_"v86, v86\n" \
E_"v87, v87\n" \
E_"v84, v84\n" \
E_"v85, v85\n" \
C_"v87, v86, v87\n" \
B_"a[196:199], v20, s[56:59], s60 offen offset:1024 sc1\n" \
E_"v82, v82\n" \
E_"v83, v83\n" \
E_"v80, v80\n" \
E_"v81, v81\n" \
"s_waitcnt lgkmcnt(15)\n" \
M_"v[128:131], a[160:163], v[68:71], v[128:131]\n" \
C_"v86, v84, v85\n" \
C_"v85, v82, v83\n" \
M_"v[160:163], a[160:163], v[72:75], v[160:163]\n" \
C_"v84, v80, v81\n" \
"v_and_b32_e32 v84, v12, v84\n" \
M_"v[132:135], a[164:167], v[68:71], v[132:135]\n" \
"v_and_b32_e32 v85, v13, v85\n" \
"v_and_b32_e32 v86, v14, v86\n" \
M_"v[164:167], a[164:167], v[72:75], v[164:167]\n" \
B_"a[200:203], v20, s[56:59], s60 offen offset:2048 sc1\n" \
M_"v[136:139], a[168:171], v[68:71], v[136:139]\n" \
C_"v128, v128, v129\n" \
C_"v160, v160, v161\n" \
M_"v[168:171], a[168:171], v[72:75], v[168:171]\n" \
C_"v129, v130, v131\n" \
"s_waitcnt lgkmcnt(10)\n" \
M_"v[140:143], a[172:175], v[68:71], v[140:143]\n" \
C_"v161, v162, v163\n" \
C_"v130, v132, v133\n" \
M_"v[172:175], a[172:175], v[72:75], v[172:175]\n" \
C_"v131, v134, v135\n" \
C_"v162, v164, v165\n" \
M_"v[144:147], a[176:179], v[68:71], v[144:147]\n" \
C_"v163, v166, v167\n" \
C_"v132, v136, v137\n" \
M_"v[176:179], a[176:179], v[72:75], v[176:179]\n" \
B_"a[204:207], v20, s[56:59], s60 offen offset:3072 sc1\n" \
M_"v[148:151], a[180:183], v[68:71], v[148:151]\n" \
C_"v133, v138, v139\n" \
C_"v134, v140, v141\n" \
M_"v[180:183], a[180:183], v[72:75], v[180:183]\n" \
C_"v135, v142, v143\n" \
C_"v164, v168, v169\n" \
M_"v[152:155], a[184:187], v[68:71], v[152:155]\n" \
C_"v165, v170, v171\n" \
C_"v166, v172, v173\n" \
M_"v[184:187], a[184:187], v[72:75], v[184:187]\n" \
C_"v167, v174, v175\n" \
C_"v136, v144, v145\n" \
M_"v[156:159], a[188:191], v[68:71], v[156:159]\n" \
C_"v137, v146, v147\n" \
C_"v138, v148, v149\n" \
M_"v[188:191], a[188:191], v[72:75], v[188:191]\n" \
C_"v139, v150, v151\n" \
C_"v140, v152, v153\n" \
"v_smfmac_f32_16x16x64_bf16 a[96:99], a[104:107], v[68:75], v21\n" \
C_"v141, v154, v155\n" \
C_"v168, v176, v177\n" \
M_"v[120:123], a[128:131], a[208:211], v[44:47]\n" \
C_"v142, v156, v157\n" \
C_"v143, v158, v159\n" \
M_"v[120:123], a[132:135], a[212:215], v[120:123]\n" \
C_"v169, v178, v179\n" \
C_"v170, v180, v181\n" \
C_"v171, v182, v183\n" \
C_"v172, v184, v185\n" \
C_"v173, v186, v187\n" \
C_"v174, v188, v189\n" \
C_"v175, v190, v191\n" \
"ds_write_b128 v50, v[128:131] offset:0\n" \
M_"v[120:123], a[136:139], a[216:219], v[120:123]\n" \
"v_and_b32_e32 v87, v15, v87\n" \
"v_accvgpr_read_b32 v40, a96\n" \
M_"v[120:123], a[140:143], a[220:223], v[120:123]\n" \
"ds_write_b128 v50, v[132:135] offset:1024\n" \
M_"v[124:127], a[156:159], a[220:223], v[44:47]\n" \
B_"a[220:223], v54, s[56:59], s60 offen offset:3072 sc1\n" \
M_"v[124:127], a[152:155], a[216:219], v[124:127]\n" \
"ds_write_b128 v50, v[136:139] offset:2048\n" \
M_"v[124:127], a[148:151], a[212:215], v[124:127]\n" \
"v_accvgpr_read_b32 v22, a97\n" \
"v_accvgpr_read_b32 v41, a98\n" \
M_"v[124:127], a[144:147], a[208:211], v[124:127]\n" \
"ds_write_b128 v50, v[140:143] offset:3072\n" \
M_"a[100:103], a[108:111], v[84:87], a[100:103]\n" \
"v_accvgpr_read_b32 v23, a99\n" \
"v_add_f32_e32 v40, v40, v22\n" \
M_"v[192:195], a[160:163], v[84:87], v[192:195]\n" \
"ds_write_b128 v51, v[160:163] offset:32768\n" \
M_"v[196:199], a[164:167], v[84:87], v[196:199]\n" \
B_"a[208:211], v54, s[56:59], s60 offen offset:0 sc1\n" \
M_"v[200:203], a[168:171], v[84:87], v[200:203]\n" \
"ds_write_b128 v51, v[164:167] offset:33792\n" \
"v_add_f32_e32 v41, v41, v23\n" \
"ds_write_b64 v19, v[40:41]\n" \
M_"v[204:207], a[172:175], v[84:87], v[204:207]\n" \
"v_lshl_add_u32 v52, s15, 2, v20\n" \
"v_add_u32_e32 v52, 0x1a000, v52\n" \
"ds_write_b128 v51, v[168:171] offset:34816\n" \
M_"v[208:211], a[176:179], v[84:87], v[208:211]\n" \
E_"v88, v88\n" \
M_"v[212:215], a[180:183], v[84:87], v[212:215]\n" \
"ds_write_b128 v51, v[172:175] offset:35840\n" \
M_"v[216:219], a[184:187], v[84:87], v[216:219]\n" \
B_"a[212:215], v54, s[56:59], s60 offen offset:1024 sc1\n" \
M_"v[220:223], a[188:191], v[84:87], v[220:223]\n" \
E_"v89, v89\n" \
E_"v90, v90\n" \
E_"v91, v91\n" \
C_"v88, v88, v89\n" \
E_"v92, v92\n" \
E_"v93, v93\n" \
E_"v94, v94\n" \
E_"v95, v95\n" \
C_"v89, v90, v91\n" \
C_"v90, v92, v93\n" \
C_"v91, v94, v95\n" \
"v_xor_b32_e32 v63, 0x400, v19\n" \
B_"a[216:219], v54, s[56:59], s60 offen offset:2048 sc1\n" \
E_"v120, v120\n" \
E_"v121, v121\n" \
E_"v122, v122\n" \
E_"v123, v123\n" \
C_"v120, v120, v121\n" \
E_"v124, v124\n" \
E_"v125, v125\n" \
"s_waitcnt lgkmcnt(0)\n" \
"s_barrier\n" \
R_"v[144:147], v52 offset:0\n" \
R_"v[148:151], v52 offset:1024\n" \
R_"v[152:155], v52 offset:2048\n" \
".fill (L_ab + 12288 - .) / 4, 4, 0xBF800000\n" \
"L_ap3:\n" \
"s_cbranch_execz L_ap0\n" \
R_"v[156:159], v52 offset:3072\n" \
R_"v[176:179], v52 offset:4096\n" \
R_"v[180:183], v52 offset:5120\n" \
R_"v[184:187], v52 offset:6144\n" \
R_"v[188:191], v52 offset:7168\n" \
M_"a[100:103], a[116:119], v[88:91], a[100:103]\n" \
"ds_read_b64 v[42:43], v63\n" \
C_"v121, v122, v123\n" \
M_"v[224:227], a[160:163], v[88:91], v[224:227]\n" \
E_"v126, v126\n" \
M_"v[228:231], a[164:167], v[88:91], v[228:231]\n" \
E_"v127, v127\n" \
M_"v[232:235], a[168:171], v[88:91], v[232:235]\n" \
C_"v122, v124, v125\n" \
C_"v123, v126, v127\n" \
M_"v[236:239], a[172:175], v[88:91], v[236:239]\n" \
"v_and_b32_e32 v120, v12, v120\n" \
"v_and_b32_e32 v121, v13, v121\n" \
M_"v[240:243], a[176:179], v[88:91], v[240:243]\n" \
"v_and_b32_e32 v122, v14, v122\n" \
"v_and_b32_e32 v123, v15, v123\n" \
M_"v[244:247], a[180:183], v[88:91], v[244:247]\n" \
"v_readlane_b32 s93, v11, 25\n" \
"v_readlane_b32 s92, v11, 30\n" \
M_"v[248:251], a[184:187], v[88:91], v[248:251]\n" \
"s_add_u32 m0, s93, 0x0\n" \
"v_mov_b32_e32 v130, v28\n" \
M_"v[252:255], a[188:191], v[88:91], v[252:255]\n" \
"v_mov_b32_e32 v131, v29\n" \
"v_mov_b32_e32 v132, 0\n" \
"v_mov_b32_e32 v133, 0\n" \
"v_mov_b32_e32 v128, 0\n" \
"v_mov_b32_e32 v129, 0\n" \
"s_waitcnt vmcnt(8) lgkmcnt(1)\n" \
"s_barrier\n" \
M_"a[100:103], a[116:119], v[120:123], a[100:103]\n" \
B_"v0, s[80:83], s92 offen offset:0 lds\n" \
M_"v[224:227], a[0:3], v[120:123], v[224:227]\n" \
M_"v[228:231], a[4:7], v[120:123], v[228:231]\n" \
M_"v[232:235], a[8:11], v[120:123], v[232:235]\n" \
M_"v[236:239], a[12:15], v[120:123], v[236:239]\n" \
M_"v[240:243], a[16:19], v[120:123], v[240:243]\n" \
M_"v[244:247], a[20:23], v[120:123], v[244:247]\n" \
B_"v1, s[80:83], s92 offen offset:1024 lds\n" \
M_"v[248:251], a[24:27], v[120:123], v[248:251]\n" \
"v_accvgpr_read_b32 v56, a100\n" \
"v_accvgpr_read_b32 v24, a101\n" \
M_"v[252:255], a[28:31], v[120:123], v[252:255]\n" \
"v_add_f32_e32 v56, v56, v24\n" \
"s_waitcnt lgkmcnt(0)\n" \
"v_add_f32_e32 v56, v56, v42\n" \
M_"v[192:195], v[130:133], v[144:147], v[192:195]\n" \
"v_max_f32_e32 v62, v62, v56\n" \
"v_accvgpr_read_b32 v57, a102\n" \
M_"v[196:199], v[128:131], v[144:147], v[196:199]\n" \
"v_rcp_f32_e32 v56, v56\n" \
M_"v[200:203], v[130:133], v[148:151], v[200:203]\n" \
"v_mul_f32_e32 v56, v56, v60\n" \
"v_accvgpr_read_b32 v25, a103\n" \
B_"v2, s[80:83], s92 offen offset:2048 lds\n" \
M_"v[204:207], v[128:131], v[148:151], v[204:207]\n" \
"v_mul_f32_e32 v192, v56, v192\n" \
"v_mul_f32_e32 v193, v56, v193\n" \
M_"v[208:211], v[130:133], v[152:155], v[208:211]\n" \
"v_mul_f32_e32 v194, v56, v194\n" \
"v_mul_f32_e32 v195, v56, v195\n" \
M_"v[212:215], v[128:131], v[152:155], v[212:215]\n" \
"v_mul_f32_e32 v196, v56, v196\n" \
"v_mul_f32_e32 v197, v56, v197\n" \
M_"v[216:219], v[130:133], v[156:159], v[216:219]\n" \
"v_mul_f32_e32 v198, v56, v198\n" \
"v_mul_f32_e32 v199, v56, v199\n" \
M_"v[220:223], v[128:131], v[156:159], v[220:223]\n" \
C_"v64, v192, v193\n" \
C_"v65, v194, v195\n" \
M_"v[224:227], v[130:133], v[176:179], v[224:227]\n" \
B_"v3, s[80:83], s92 offen offset:3072 lds\n" \
"s_add_u32 m0, s93, 0x1000\n" \
M_"v[228:231], v[128:131], v[176:179], v[228:231]\n" \
C_"v66, v196, v197\n" \
C_"v67, v198, v199\n" \
M_"v[232:235], v[130:133], v[180:183], v[232:235]\n" \
"v_mul_f32_e32 v200, v56, v200\n" \
"v_mul_f32_e32 v201, v56, v201\n" \
M_"v[236:239], v[128:131], v[180:183], v[236:239]\n" \
M_"a[112:115], a[120:123], v[64:67], a[112:115]\n" \
"v_mul_f32_e32 v202, v56, v202\n" \
"v_mul_f32_e32 v203, v56, v203\n" \
M_"v[240:243], v[130:133], v[184:187], v[240:243]\n" \
"v_mul_f32_e32 v204, v56, v204\n" \
"v_mul_f32_e32 v205, v56, v205\n" \
B_"v4, s[80:83], s92 offen offset:0 lds\n" \
"v_mul_f32_e32 v206, v56, v206\n" \
"v_mul_f32_e32 v207, v56, v207\n" \
"v_add_f32_e32 v57, v57, v25\n" \
C_"v68, v200, v201\n" \
C_"v69, v202, v203\n" \
C_"v70, v204, v205\n" \
C_"v71, v206, v207\n" \
"v_add_f32_e32 v57, v57, v43\n" \
"v_mul_f32_e32 v208, v56, v208\n" \
M_"a[112:115], a[120:123], v[68:71], a[112:115]\n" \
"v_mul_f32_e32 v209, v56, v209\n" \
"v_mul_f32_e32 v210, v56, v210\n" \
"v_mul_f32_e32 v211, v56, v211\n" \
"v_mul_f32_e32 v212, v56, v212\n" \
"v_mul_f32_e32 v213, v56, v213\n" \
"v_mul_f32_e32 v214, v56, v214\n" \
"v_mul_f32_e32 v215, v56, v215\n" \
"v_max_f32_e32 v62, v62, v57\n" \
C_"v72, v208, v209\n" \
C_"v73, v210, v211\n" \
B_"v5, s[80:83], s92 offen offset:1024 lds\n" \
C_"v74, v212, v213\n" \
C_"v75, v214, v215\n" \
"v_rcp_f32_e32 v57, v57\n" \
M_"v[244:247], v[128:131], v[184:187], v[244:247]\n" \
M_"a[112:115], a[120:123], v[72:75], a[112:115]\n" \
"v_mul_f32_e32 v216, v56, v216\n" \
"v_mul_f32_e32 v217, v56, v217\n" \
"v_mul_f32_e32 v218, v56, v218\n" \
"v_mul_f32_e32 v219, v56, v219\n" \
"v_mul_f32_e32 v220, v56, v220\n" \
"v_mul_f32_e32 v221, v56, v221\n" \
"v_mul_f32_e32 v222, v56, v222\n" \
"v_mul_f32_e32 v223, v56, v223\n" \
C_"v76, v216, v217\n" \
C_"v77, v218, v219\n" \
C_"v78, v220, v221\n" \
B_"v6, s[80:83], s92 offen offset:2048 lds\n" \
C_"v79, v222, v223\n" \
"v_mul_f32_e32 v57, v57, v61\n" \
"v_mul_f32_e32 v224, v57, v224\n" \
M_"a[112:115], a[120:123], v[76:79], a[112:115]\n" \
"v_mul_f32_e32 v225, v57, v225\n" \
"v_mul_f32_e32 v226, v57, v226\n" \
"v_mul_f32_e32 v227, v57, v227\n" \
"v_mul_f32_e32 v228, v57, v228\n" \
"v_mul_f32_e32 v229, v57, v229\n" \
"v_mul_f32_e32 v230, v57, v230\n" \
"v_mul_f32_e32 v231, v57, v231\n" \
C_"v80, v224, v225\n" \
C_"v81, v226, v227\n" \
C_"v82, v228, v229\n" \
C_"v83, v230, v231\n" \
M_"v[248:251], v[130:133], v[188:191], v[248:251]\n" \
"v_mul_f32_e32 v232, v57, v232\n" \
"v_mul_f32_e32 v233, v57, v233\n" \
M_"a[112:115], a[120:123], v[80:83], a[112:115]\n" \
B_"v7, s[80:83], s92 offen offset:3072 lds\n" \
"s_add_u32 m0, s93, 0x2000\n" \
"v_mul_f32_e32 v234, v57, v234\n" \
"v_mul_f32_e32 v235, v57, v235\n" \
"v_mul_f32_e32 v236, v57, v236\n" \
"v_mul_f32_e32 v237, v57, v237\n" \
"v_mul_f32_e32 v238, v57, v238\n" \
"v_mul_f32_e32 v239, v57, v239\n" \
C_"v84, v232, v233\n" \
C_"v85, v234, v235\n" \
C_"v86, v236, v237\n" \
C_"v87, v238, v239\n" \
"v_mul_f32_e32 v240, v57, v240\n" \
"v_mul_f32_e32 v241, v57, v241\n" \
M_"a[112:115], a[120:123], v[84:87], a[112:115]\n" \
"v_mul_f32_e32 v242, v57, v242\n" \
"v_mul_f32_e32 v243, v57, v243\n" \
"v_mul_f32_e32 v244, v57, v244\n" \
"v_mul_f32_e32 v245, v57, v245\n" \
"v_mul_f32_e32 v246, v57, v246\n" \
"v_mul_f32_e32 v247, v57, v247\n" \
B_"v8, s[80:83], s92 offen offset:0 lds\n" \
C_"v96, v240, v241\n" \
C_"v97, v242, v243\n" \
C_"v98, v244, v245\n" \
C_"v99, v246, v247\n" \
M_"v[252:255], v[128:131], v[188:191], v[252:255]\n" \
"v_mul_f32_e32 v248, v57, v248\n" \
M_"a[112:115], a[120:123], v[96:99], a[112:115]\n" \
"v_mul_f32_e32 v249, v57, v249\n" \
"v_mul_f32_e32 v250, v57, v250\n" \
"v_mul_f32_e32 v251, v57, v251\n" \
C_"v100, v248, v249\n" \
C_"v101, v250, v251\n" \
"v_cmp_lt_f32_e64 s[72:73], s21, v62\n" \
"v_mul_f32_e32 v252, v57, v252\n" \
"v_mul_f32_e32 v253, v57, v253\n" \
"v_mul_f32_e32 v254, v57, v254\n" \
"v_mul_f32_e32 v255, v57, v255\n" \
C_"v102, v252, v253\n" \
C_"v103, v254, v255\n" \
"s_nop 1\n" \
M_"a[112:115], a[120:123], v[100:103], a[112:115]\n" \
"s_nop 7\n" \
"v_accvgpr_read_b32 v63, a112\n" \
"v_cmp_u_f32_e32 vcc, v63, v63\n" \
"s_nop 0\n" \
"s_or_b64 vcc, s[72:73], vcc\n" \
"s_cbranch_scc1 L_end_%=\n" \
"s_branch L_out_%=\n" \
"L_skb_%=:\n" \
"s_sleep 1\n" \
"buffer_load_dword v57, v61, s[56:59], s96 offen sc1\n" \
"s_add_u32 s101, s101, 1\n" \
"s_waitcnt vmcnt(0)\n" \
"v_cmp_ne_u32_e32 vcc, s62, v57\n" \
"s_cmp_gt_u32 s101, 0x100000\n" \
"s_cbranch_scc1 L_rkb_%=\n" \
"s_nop 2\n" \
"s_cbranch_vccnz L_skb_%=\n" \
"s_branch L_rkb_%=\n" \
"L_sol_%=:\n" \
"s_sleep 1\n" \
"buffer_load_dwordx2 v[26:27], v16, s[56:59], s96 offen sc1\n" \
"s_add_u32 s101, s101, 1\n" \
"s_waitcnt vmcnt(0)\n" \
"v_cmp_ne_u64_e32 vcc, s[62:63], v[26:27]\n" \
"s_cmp_gt_u32 s101, 0x100000\n" \
"s_cbranch_scc1 L_rol_%=\n" \
"s_nop 2\n" \
"s_cbranch_vccnz L_sol_%=\n" \
"s_branch L_rol_%=\n" \
"L_sos_%=:\n" \
"s_sleep 1\n" \
"buffer_load_dwordx2 v[26:27], v16, s[56:59], s96 offen sc1\n" \
"s_add_u32 s101, s101, 1\n" \
"s_waitcnt vmcnt(0)\n" \
"v_cmp_ne_u64_e32 vcc, s[62:63], v[26:27]\n" \
"s_cmp_gt_u32 s101, 0x100000\n" \
"s_cbranch_scc1 L_ros_%=\n" \
"s_nop 2\n" \
"s_cbranch_vccnz L_sos_%=\n" \
"s_branch L_ros_%=\n" \
"L_sa_%=:\n" \
"s_sleep 1\n" \
"buffer_load_dword v26, v38, s[56:59], s64 offen sc1\n" \
"s_add_u32 s101, s101, 1\n" \
"s_waitcnt vmcnt(0)\n" \
"v_cmp_ne_u32_e32 vcc, s62, v26\n" \
"s_cmp_gt_u32 s101, 0x100000\n" \
"s_cbranch_scc1 L_ra_%=\n" \
"s_nop 2\n" \
"s_cbranch_vccnz L_sa_%=\n" \
"s_branch L_ra_%=\n" \
"L_sb_%=:\n" \
"s_sleep 1\n" \
"buffer_load_dword v47, v38, s[56:59], s71 offen sc1\n" \
"s_add_u32 s101, s101, 1\n" \
"s_waitcnt vmcnt(0)\n" \
"v_cmp_ne_u32_e32 vcc, s62, v47\n" \
"s_cmp_gt_u32 s101, 0x100000\n" \
"s_cbranch_scc1 L_rb_%=\n" \
"s_nop 2\n" \
"s_cbranch_vccnz L_sb_%=\n" \
"s_branch L_rb_%=\n" \
"L_sna_%=:\n" \
"buffer_load_dword v37, v40, s[56:59], s74 offen sc1\n" \
"s_add_u32 s101, s101, 1\n" \
"s_waitcnt vmcnt(0)\n" \
"v_cmp_eq_u32_e64 s[98:99], s62, v37\n" \
"s_cmp_gt_u32 s101, 0x100000\n" \
"s_cbranch_scc1 L_rna_%=\n" \
"s_andn2_b32 s75, s20, s98\n" \
"s_cbranch_scc0 L_gna_%=\n" \
"s_sleep 1\n" \
"s_branch L_sna_%=\n" \
"L_gna_%=:\n" \
"s_bitcmp1_b32 s20, 0\n" \
"s_cbranch_scc0 L_kna0_%=\n" \
B_"v[116:119], v20, s[56:59], s12 offen offset:0 sc1\n" \
"L_kna0_%=:\n" \
"s_bitcmp1_b32 s20, 1\n" \
"s_cbranch_scc0 L_kna1_%=\n" \
B_"v[124:127], v20, s[56:59], s12 offen offset:2048 sc1\n" \
"L_kna1_%=:\n" \
"s_bitcmp1_b32 s20, 2\n" \
"s_cbranch_scc0 L_kna2_%=\n" \
B_"v[132:135], v20, s[56:59], s13 offen offset:0 sc1\n" \
"L_kna2_%=:\n" \
"s_bitcmp1_b32 s20, 3\n" \
"s_cbranch_scc0 L_kna3_%=\n" \
B_"v[140:143], v20, s[56:59], s13 offen offset:2048 sc1\n" \
"L_kna3_%=:\n" \
"s_bitcmp1_b32 s20, 4\n" \
"s_cbranch_scc0 L_kna4_%=\n" \
B_"v[148:151], v20, s[56:59], s14 offen offset:0 sc1\n" \
"L_kna4_%=:\n" \
"s_bitcmp1_b32 s20, 5\n" \
"s_cbranch_scc0 L_kna5_%=\n" \
B_"v[156:159], v20, s[56:59], s14 offen offset:2048 sc1\n" \
"L_kna5_%=:\n" \
"s_bitcmp1_b32 s20, 6\n" \
"s_cbranch_scc0 L_kna6_%=\n" \
B_"v[164:167], v20, s[56:59], s15 offen offset:0 sc1\n" \
"L_kna6_%=:\n" \
"s_waitcnt vmcnt(0)\n" \
"s_branch L_rna_%=\n" \
"L_snx_%=:\n" \
"buffer_load_dword v37, v40, s[56:59], s74 offen sc1\n" \
"s_add_u32 s101, s101, 1\n" \
"s_waitcnt vmcnt(0)\n" \
"v_cmp_eq_u32_e64 s[98:99], s62, v37\n" \
"s_cmp_gt_u32 s101, 0x100000\n" \
"s_cbranch_scc1 L_rnx_%=\n" \
"s_andn2_b32 s75, s21, s98\n" \
"s_cbranch_scc0 L_gnx_%=\n" \
"s_sleep 1\n" \
"s_branch L_snx_%=\n" \
"L_gnx_%=:\n" \
"s_bitcmp1_b32 s21, 16\n" \
"s_cbranch_scc0 L_knx16_%=\n" \
B_"v[112:115], v20, s[56:59], s16 offen offset:0 sc1\n" \
"L_knx16_%=:\n" \
"s_bitcmp1_b32 s21, 17\n" \
"s_cbranch_scc0 L_knx17_%=\n" \
B_"v[184:187], v20, s[56:59], s16 offen offset:2048 sc1\n" \
"L_knx17_%=:\n" \
"s_bitcmp1_b32 s21, 18\n" \
"s_cbranch_scc0 L_knx18_%=\n" \
B_"v[120:123], v20, s[56:59], s17 offen offset:0 sc1\n" \
"L_knx18_%=:\n" \
"s_bitcmp1_b32 s21, 19\n" \
"s_cbranch_scc0 L_knx19_%=\n" \
B_"v[192:195], v20, s[56:59], s17 offen offset:2048 sc1\n" \
"L_knx19_%=:\n" \
"s_bitcmp1_b32 s21, 20\n" \
"s_cbranch_scc0 L_knx20_%=\n" \
B_"v[128:131], v20, s[56:59], s18 offen offset:0 sc1\n" \
"L_knx20_%=:\n" \
"s_bitcmp1_b32 s21, 21\n" \
"s_cbranch_scc0 L_knx21_%=\n" \
B_"v[200:203], v20, s[56:59], s18 offen offset:2048 sc1\n" \
"L_knx21_%=:\n" \
"s_bitcmp1_b32 s21, 22\n" \
"s_cbranch_scc0 L_knx22_%=\n" \
B_"v[176:179], v20, s[56:59], s19 offen offset:0 sc1\n" \
"L_knx22_%=:\n" \
"s_bitcmp1_b32 s21, 23\n" \
"s_cbranch_scc0 L_knx23_%=\n" \
B_"v[248:251], v20, s[56:59], s19 offen offset:2048 sc1\n" \
"L_knx23_%=:\n" \
"s_bitcmp1_b32 s21, 8\n" \
"s_cbranch_scc0 L_knx8_%=\n" \
B_"v[188:191], v20, s[56:59], s12 offen offset:1024 sc1\n" \
"L_knx8_%=:\n" \
"s_bitcmp1_b32 s21, 9\n" \
"s_cbranch_scc0 L_knx9_%=\n" \
B_"v[196:199], v20, s[56:59], s12 offen offset:3072 sc1\n" \
"L_knx9_%=:\n" \
"s_waitcnt vmcnt(0)\n" \
"s_branch L_rnx_%=\n" \
"L_sx0_%=:\n" \
"s_sleep 1\n" \
"buffer_load_dword v37, v40, s[56:59], s74 offen sc1\n" \
"s_add_u32 s101, s101, 1\n" \
"s_waitcnt vmcnt(0)\n" \
"v_cmp_eq_u32_e64 s[98:99], s62, v37\n" \
"s_cmp_gt_u32 s101, 0x100000\n" \
"s_cbranch_scc1 L_rx0_%=\n" \
"s_andn2_b32 s75, 0x400, s98\n" \
"s_cbranch_scc1 L_sx0_%=\n" \
"s_branch L_rx0_%=\n" \
"L_sx1_%=:\n" \
"s_sleep 1\n" \
"buffer_load_dword v37, v40, s[56:59], s74 offen sc1\n" \
"s_add_u32 s101, s101, 1\n" \
"s_waitcnt vmcnt(0)\n" \
"v_cmp_eq_u32_e64 s[98:99], s62, v37\n" \
"s_cmp_gt_u32 s101, 0x100000\n" \
"s_cbranch_scc1 L_rx1_%=\n" \
"s_andn2_b32 s75, 0x800, s98\n" \
"s_cbranch_scc1 L_sx1_%=\n" \
"s_branch L_rx1_%=\n" \
"L_sx2_%=:\n" \
"s_sleep 1\n" \
"buffer_load_dword v37, v40, s[56:59], s74 offen sc1\n" \
"s_add_u32 s101, s101, 1\n" \
"s_waitcnt vmcnt(0)\n" \
"v_cmp_eq_u32_e64 s[98:99], s62, v37\n" \
"s_cmp_gt_u32 s101, 0x100000\n" \
"s_cbranch_scc1 L_rx2_%=\n" \
"s_andn2_b32 s75, 0x1000, s98\n" \
"s_cbranch_scc1 L_sx2_%=\n" \
"s_branch L_rx2_%=\n" \
"L_sx3_%=:\n" \
"s_sleep 1\n" \
"buffer_load_dword v37, v40, s[56:59], s74 offen sc1\n" \
"s_add_u32 s101, s101, 1\n" \
"s_waitcnt vmcnt(0)\n" \
"v_cmp_eq_u32_e64 s[98:99], s62, v37\n" \
"s_cmp_gt_u32 s101, 0x100000\n" \
"s_cbranch_scc1 L_rx3_%=\n" \
"s_andn2_b32 s75, 0x2000, s98\n" \
"s_cbranch_scc1 L_sx3_%=\n" \
"s_branch L_rx3_%=\n" \
"L_sx4_%=:\n" \
"s_sleep 1\n" \
"buffer_load_dword v37, v40, s[56:59], s74 offen sc1\n" \
"s_add_u32 s101, s101, 1\n" \
"s_waitcnt vmcnt(0)\n" \
"v_cmp_eq_u32_e64 s[98:99], s62, v37\n" \
"s_cmp_gt_u32 s101, 0x100000\n" \
"s_cbranch_scc1 L_rx4_%=\n" \
"s_andn2_b32 s75, 0x4000, s98\n" \
"s_cbranch_scc1 L_sx4_%=\n" \
"s_branch L_rx4_%=\n" \
"L_att_stub:\n" \
"s_mov_b32 s101, 0\n" \
"v_add_u32_e32 v32, 0x49000, v20\n" \
"v_readlane_b32 s96, v11, 39\n" \
"v_readlane_b32 s97, v11, 40\n" \
B_"a[192:195], v20, s[56:59], s60 offen offset:0 sc1\n" \
B_"a[208:211], v32, s[56:59], s60 offen offset:0 sc1\n" \
B_"a[196:199], v20, s[56:59], s60 offen offset:1024 sc1\n" \
B_"a[212:215], v32, s[56:59], s60 offen offset:1024 sc1\n" \
B_"a[200:203], v20, s[56:59], s60 offen offset:2048 sc1\n" \
B_"a[216:219], v32, s[56:59], s60 offen offset:2048 sc1\n" \
B_"a[204:207], v20, s[56:59], s60 offen offset:3072 sc1\n" \
B_"a[220:223], v32, s[56:59], s60 offen offset:3072 sc1\n" \
B_"a[124:127], v20, s[56:59], s96 offen offset:0 sc1\n" \
B_"a[228:231], v32, s[56:59], s96 offen offset:0 sc1\n" \
B_"a[236:239], v20, s[56:59], s97 offen offset:0 sc1\n" \
B_"a[244:247], v32, s[56:59], s97 offen offset:0 sc1\n" \
B_"a[224:227], v20, s[56:59], s96 offen offset:1024 sc1\n" \
B_"a[232:235], v32, s[56:59], s96 offen offset:1024 sc1\n" \
B_"a[240:243], v20, s[56:59], s97 offen offset:1024 sc1\n" \
B_"a[248:251], v32, s[56:59], s97 offen offset:1024 sc1\n" \
B_"v[64:67], v20, s[56:59], s61 offen offset:0 sc1\n" \
B_"v[68:71], v20, s[56:59], s61 offen offset:1024 sc1\n" \
B_"v[72:75], v20, s[56:59], s61 offen offset:2048 sc1\n" \
B_"v[76:79], v20, s[56:59], s61 offen offset:3072 sc1\n" \
B_"v[80:83], v32, s[56:59], s61 offen offset:0 sc1\n" \
B_"v[84:87], v32, s[56:59], s61 offen offset:1024 sc1\n" \
B_"v[96:99], v32, s[56:59], s61 offen offset:2048 sc1\n" \
B_"v[100:103], v32, s[56:59], s61 offen offset:3072 sc1\n" \
"v_not_b32_e32 v26, s62\n" \
"s_waitcnt vmcnt(0)\n" \
"s_branch L_out_%=\n" \
".fill (L_ab + 16384 - .) / 4, 4, 0xBF800000\n" \
".fill (L_ab + 16384 - .) / 4, 4, 0xBF800000\n" \
"L_ap4:\n" \
"s_setpc_b64 s[88:89]\n" \
"L_gsw:\n" \
M_"v[104:107], a[128:131], a[16:19], v[36:39]\n" \
"s_add_u32 s68, s60, 0xfff6e000\n" \
"s_add_u32 s69, s60, 0xfffb7000\n" \
"s_add_u32 s70, s60, 0x0\n" \
"s_add_u32 s71, s60, 0x49000\n" \
M_"v[104:107], a[132:135], a[20:23], v[104:107]\n" \
"s_xor_b32 m0, s65, 0x12000\n" \
R_"a[160:163], v49 offset:0\n" \
M_"v[104:107], a[136:139], a[24:27], v[104:107]\n" \
B_"v50, s[8:11], s64 offen offset:0 sc1 lds\n" \
M_"v[104:107], a[140:143], a[28:31], v[104:107]\n" \
R_"a[164:167], v49 offset:1024\n" \
R_"a[168:171], v49 offset:2048\n" \
M_"v[108:111], a[156:159], a[28:31], v[36:39]\n" \
R_"a[172:175], v49 offset:3072\n" \
R_"a[176:179], v49 offset:4096\n" \
M_"v[108:111], a[152:155], a[24:27], v[108:111]\n" \
B_"v50, s[8:11], s64 offen offset:1024 sc1 lds\n" \
M_"v[108:111], a[148:151], a[20:23], v[108:111]\n" \
"s_mov_b32 s65, s13\n" \
"v_add_u32_e32 v63, s15, v20\n" \
M_"v[108:111], a[144:147], a[16:19], v[108:111]\n" \
"s_add_u32 s67, s14, 40960\n" \
"v_add_u32_e32 v50, 0x6000, v63\n" \
M_"v[116:119], a[144:147], a[32:35], v[40:43]\n" \
"s_xor_b32 m0, s65, 0x8000\n" \
R_"a[180:183], v49 offset:5120\n" \
M_"v[116:119], a[148:151], a[36:39], v[116:119]\n" \
R_"a[184:187], v49 offset:6144\n" \
"v_and_b32_e32 v68, v12, v68\n" \
M_"v[116:119], a[152:155], a[40:43], v[116:119]\n" \
"v_and_b32_e32 v69, v13, v69\n" \
"v_and_b32_e32 v70, v14, v70\n" \
M_"v[116:119], a[156:159], a[44:47], v[116:119]\n" \
B_"v50, s[8:11], s67 offen offset:0 sc1 lds\n" \
M_"v[112:115], a[140:143], a[44:47], v[40:43]\n" \
"v_and_b32_e32 v71, v15, v71\n" \
R_"a[188:191], v49 offset:7168\n" \
M_"v[112:115], a[136:139], a[40:43], v[112:115]\n" \
E_"v86, v86\n" \
M_"v[112:115], a[132:135], a[36:39], v[112:115]\n" \
E_"v87, v87\n" \
M_"v[112:115], a[128:131], a[32:35], v[112:115]\n" \
E_"v84, v84\n" \
M_"v[120:123], a[128:131], a[48:51], v[44:47]\n" \
B_"v50, s[8:11], s67 offen offset:1024 sc1 lds\n" \
M_"v[120:123], a[132:135], a[52:55], v[120:123]\n" \
E_"v85, v85\n" \
M_"v[120:123], a[136:139], a[56:59], v[120:123]\n" \
C_"v87, v86, v87\n" \
C_"v86, v84, v85\n" \
M_"v[120:123], a[140:143], a[60:63], v[120:123]\n" \
E_"v82, v82\n" \
M_"v[124:127], a[156:159], a[60:63], v[44:47]\n" \
E_"v83, v83\n" \
M_"v[124:127], a[152:155], a[56:59], v[124:127]\n" \
E_"v80, v80\n" \
M_"v[124:127], a[148:151], a[52:55], v[124:127]\n" \
B_"v50, s[8:11], s67 offen offset:2048 sc1 lds\n" \
M_"v[124:127], a[144:147], a[48:51], v[124:127]\n" \
C_"v85, v82, v83\n" \
"s_waitcnt lgkmcnt(1)\n" \
M_"v[128:131], a[160:163], v[68:71], v[128:131]\n" \
E_"v81, v81\n" \
M_"v[160:163], a[160:163], v[72:75], v[160:163]\n" \
C_"v84, v80, v81\n" \
"s_mov_b32 s64, s14\n" \
M_"v[132:135], a[164:167], v[68:71], v[132:135]\n" \
E_"v88, v88\n" \
M_"v[164:167], a[164:167], v[72:75], v[164:167]\n" \
E_"v89, v89\n" \
M_"v[136:139], a[168:171], v[68:71], v[136:139]\n" \
B_"v50, s[8:11], s67 offen offset:3072 sc1 lds\n" \
M_"v[168:171], a[168:171], v[72:75], v[168:171]\n" \
E_"v90, v90\n" \
M_"v[140:143], a[172:175], v[68:71], v[140:143]\n" \
E_"v91, v91\n" \
M_"v[172:175], a[172:175], v[72:75], v[172:175]\n" \
C_"v88, v88, v89\n" \
C_"v89, v90, v91\n" \
M_"v[144:147], a[176:179], v[68:71], v[144:147]\n" \
E_"v92, v92\n" \
M_"v[176:179], a[176:179], v[72:75], v[176:179]\n" \
E_"v93, v93\n" \
M_"v[148:151], a[180:183], v[68:71], v[148:151]\n" \
E_"v94, v94\n" \
M_"v[180:183], a[180:183], v[72:75], v[180:183]\n" \
E_"v95, v95\n" \
M_"v[152:155], a[184:187], v[68:71], v[152:155]\n" \
C_"v90, v92, v93\n" \
C_"v91, v94, v95\n" \
M_"v[184:187], a[184:187], v[72:75], v[184:187]\n" \
"v_add_u32_e32 v51, 0x12000, v63\n" \
"s_waitcnt lgkmcnt(0)\n" \
M_"v[156:159], a[188:191], v[68:71], v[156:159]\n" \
"s_xor_b32 m0, s65, 0xa000\n" \
"ds_read_b32 v58, v18 offset:512\n" \
M_"v[188:191], a[188:191], v[72:75], v[188:191]\n" \
B_"v51, s[8:11], s64 offen offset:0 sc1 lds\n" \
"v_smfmac_f32_16x16x64_bf16 a[96:99], a[104:107], v[68:75], v21\n" \
B_"v51, s[8:11], s64 offen offset:1024 sc1 lds\n" \
M_"v[220:223], a[188:191], v[84:87], v[220:223]\n" \
"ds_read_b32 v59, v18 offset:640\n" \
"ds_read_b32 v60, v18 offset:768\n" \
M_"v[252:255], a[188:191], v[88:91], v[252:255]\n" \
"ds_read_b32 v61, v18 offset:896\n" \
R_"a[128:131], v48 offset:0\n" \
M_"v[216:219], a[184:187], v[84:87], v[216:219]\n" \
R_"a[144:147], v48 offset:4096\n" \
R_"a[132:135], v48 offset:1024\n" \
M_"v[248:251], a[184:187], v[88:91], v[248:251]\n" \
R_"a[148:151], v48 offset:5120\n" \
R_"a[136:139], v48 offset:2048\n" \
M_"v[212:215], a[180:183], v[84:87], v[212:215]\n" \
R_"a[152:155], v48 offset:6144\n" \
R_"a[140:143], v48 offset:3072\n" \
M_"v[244:247], a[180:183], v[88:91], v[244:247]\n" \
R_"a[156:159], v48 offset:7168\n" \
"v_add_u32_e32 v52, 0x14000, v63\n" \
M_"v[208:211], a[176:179], v[84:87], v[208:211]\n" \
B_"v51, s[8:11], s64 offen offset:2048 sc1 lds\n" \
M_"v[240:243], a[176:179], v[88:91], v[240:243]\n" \
E_"v104, v104\n" \
M_"v[204:207], a[172:175], v[84:87], v[204:207]\n" \
E_"v105, v105\n" \
M_"v[236:239], a[172:175], v[88:91], v[236:239]\n" \
B_"v51, s[8:11], s64 offen offset:3072 sc1 lds\n" \
M_"v[200:203], a[168:171], v[84:87], v[200:203]\n" \
E_"v106, v106\n" \
M_"v[232:235], a[168:171], v[88:91], v[232:235]\n" \
E_"v107, v107\n" \
M_"v[196:199], a[164:167], v[84:87], v[196:199]\n" \
C_"v104, v104, v105\n" \
C_"v105, v106, v107\n" \
M_"v[228:231], a[164:167], v[88:91], v[228:231]\n" \
E_"v108, v108\n" \
M_"v[192:195], a[160:163], v[84:87], v[192:195]\n" \
E_"v109, v109\n" \
M_"v[224:227], a[160:163], v[88:91], v[224:227]\n" \
E_"v110, v110\n" \
"v_smfmac_f32_16x16x64_bf16 a[100:103], a[104:107], v[84:91], v21\n" \
E_"v111, v111\n" \
C_"v106, v108, v109\n" \
C_"v107, v110, v111\n" \
"v_and_b32_e32 v104, v12, v104\n" \
"v_and_b32_e32 v105, v13, v105\n" \
"v_and_b32_e32 v106, v14, v106\n" \
"v_and_b32_e32 v107, v15, v107\n" \
"v_accvgpr_read_b32 v54, a96\n" \
"v_accvgpr_read_b32 v22, a97\n" \
"v_add_u32_e32 v53, 0x20000, v63\n" \
E_"v118, v118\n" \
E_"v119, v119\n" \
E_"v116, v116\n" \
E_"v117, v117\n" \
C_"v119, v118, v119\n" \
E_"v114, v114\n" \
E_"v115, v115\n" \
E_"v112, v112\n" \
E_"v113, v113\n" \
C_"v118, v116, v117\n" \
C_"v117, v114, v115\n" \
C_"v116, v112, v113\n" \
"v_readlane_b32 s93, v11, 22\n" \
"s_waitcnt vmcnt(10) lgkmcnt(0)\n" \
"s_barrier\n" \
M_"v[80:83], a[128:131], a[32:35], v[40:43]\n" \
R_"a[160:163], v49 offset:8192\n" \
R_"a[164:167], v49 offset:9216\n" \
M_"v[80:83], a[132:135], a[36:39], v[80:83]\n" \
R_"a[168:171], v49 offset:10240\n" \
"s_xor_b32 m0, s65, 0x18000\n" \
M_"v[80:83], a[136:139], a[40:43], v[80:83]\n" \
B_"v52, s[8:11], s64 offen offset:0 sc1 lds\n" \
M_"v[80:83], a[140:143], a[44:47], v[80:83]\n" \
R_"a[172:175], v49 offset:11264\n" \
R_"a[176:179], v49 offset:12288\n" \
M_"v[84:87], a[156:159], a[44:47], v[40:43]\n" \
R_"a[180:183], v49 offset:13312\n" \
R_"a[184:187], v49 offset:14336\n" \
M_"v[84:87], a[152:155], a[40:43], v[84:87]\n" \
B_"v52, s[8:11], s64 offen offset:1024 sc1 lds\n" \
M_"v[84:87], a[148:151], a[36:39], v[84:87]\n" \
R_"a[188:191], v49 offset:15360\n" \
"v_readlane_b32 s80, v11, 11\n" \
M_"v[84:87], a[144:147], a[32:35], v[84:87]\n" \
B_"v52, s[8:11], s64 offen offset:2048 sc1 lds\n" \
M_"v[92:95], a[144:147], a[48:51], v[44:47]\n" \
E_"v120, v120\n" \
M_"v[92:95], a[148:151], a[52:55], v[92:95]\n" \
E_"v121, v121\n" \
M_"v[92:95], a[152:155], a[56:59], v[92:95]\n" \
B_"v52, s[8:11], s64 offen offset:3072 sc1 lds\n" \
M_"v[92:95], a[156:159], a[60:63], v[92:95]\n" \
"s_mov_b32 m0, s65\n" \
C_"v120, v120, v121\n" \
M_"v[88:91], a[140:143], a[60:63], v[44:47]\n" \
E_"v122, v122\n" \
M_"v[88:91], a[136:139], a[56:59], v[88:91]\n" \
B_"v53, s[8:11], s64 offen offset:0 sc1 lds\n" \
M_"v[88:91], a[132:135], a[52:55], v[88:91]\n" \
E_"v123, v123\n" \
M_"v[88:91], a[128:131], a[48:51], v[88:91]\n" \
E_"v124, v124\n" \
M_"a[96:99], a[116:119], v[104:107], a[96:99]\n" \
C_"v121, v122, v123\n" \
"s_waitcnt lgkmcnt(5)\n" \
M_"v[160:163], a[160:163], v[104:107], v[160:163]\n" \
B_"v53, s[8:11], s64 offen offset:1024 sc1 lds\n" \
M_"v[164:167], a[164:167], v[104:107], v[164:167]\n" \
E_"v125, v125\n" \
M_"v[168:171], a[168:171], v[104:107], v[168:171]\n" \
C_"v122, v124, v125\n" \
"s_waitcnt lgkmcnt(1)\n" \
M_"v[172:175], a[172:175], v[104:107], v[172:175]\n" \
E_"v126, v126\n" \
M_"v[176:179], a[176:179], v[104:107], v[176:179]\n" \
E_"v127, v127\n" \
M_"v[180:183], a[180:183], v[104:107], v[180:183]\n" \
C_"v123, v126, v127\n" \
"v_readlane_b32 s81, v11, 12\n" \
M_"v[184:187], a[184:187], v[104:107], v[184:187]\n" \
"s_mov_b32 s82, 0x47fca0\n" \
"s_waitcnt lgkmcnt(0)\n" \
M_"v[188:191], a[188:191], v[104:107], v[188:191]\n" \
B_"v53, s[8:11], s64 offen offset:2048 sc1 lds\n" \
M_"v[220:223], a[188:191], v[116:119], v[220:223]\n" \
B_"v53, s[8:11], s64 offen offset:3072 sc1 lds\n" \
M_"v[252:255], a[188:191], v[120:123], v[252:255]\n" \
"s_mov_b32 s83, 0x20000\n" \
"v_readlane_b32 s92, v11, 29\n" \
M_"v[216:219], a[184:187], v[116:119], v[216:219]\n" \
"s_add_u32 m0, s93, 0x0\n" \
R_"a[128:131], v48 offset:8192\n" \
M_"v[248:251], a[184:187], v[120:123], v[248:251]\n" \
R_"a[144:147], v48 offset:12288\n" \
R_"a[132:135], v48 offset:9216\n" \
M_"v[212:215], a[180:183], v[116:119], v[212:215]\n" \
B_"v0, s[80:83], s92 offen offset:0 lds\n" \
M_"v[244:247], a[180:183], v[120:123], v[244:247]\n" \
B_"v1, s[80:83], s92 offen offset:1024 lds\n" \
M_"v[208:211], a[176:179], v[116:119], v[208:211]\n" \
R_"a[148:151], v48 offset:13312\n" \
R_"a[136:139], v48 offset:10240\n" \
M_"v[240:243], a[176:179], v[120:123], v[240:243]\n" \
R_"a[152:155], v48 offset:14336\n" \
R_"a[140:143], v48 offset:11264\n" \
M_"v[204:207], a[172:175], v[116:119], v[204:207]\n" \
B_"v2, s[80:83], s92 offen offset:2048 lds\n" \
M_"v[236:239], a[172:175], v[120:123], v[236:239]\n" \
R_"a[156:159], v48 offset:15360\n" \
"s_xor_b32 s65, s65, 0x8000\n" \
M_"v[200:203], a[168:171], v[116:119], v[200:203]\n" \
"s_add_u32 s64, s64, 0x10000\n" \
"v_add_u32_e32 v49, 0x10000, v48\n" \
M_"v[232:235], a[168:171], v[120:123], v[232:235]\n" \
B_"v3, s[80:83], s92 offen offset:3072 lds\n" \
M_"v[196:199], a[164:167], v[116:119], v[196:199]\n" \
E_"v86, v86\n" \
M_"v[228:231], a[164:167], v[120:123], v[228:231]\n" \
E_"v87, v87\n" \
M_"v[192:195], a[160:163], v[116:119], v[192:195]\n" \
E_"v84, v84\n" \
M_"v[224:227], a[160:163], v[120:123], v[224:227]\n" \
E_"v85, v85\n" \
"v_smfmac_f32_16x16x64_bf16 a[100:103], a[104:107], v[116:123], v21\n" \
E_"v88, v88\n" \
E_"v89, v89\n" \
C_"v87, v86, v87\n" \
E_"v82, v82\n" \
E_"v83, v83\n" \
E_"v80, v80\n" \
E_"v81, v81\n" \
E_"v90, v90\n" \
E_"v91, v91\n" \
C_"v86, v84, v85\n" \
C_"v88, v88, v89\n" \
E_"v92, v92\n" \
E_"v93, v93\n" \
E_"v94, v94\n" \
E_"v95, v95\n" \
C_"v85, v82, v83\n" \
C_"v84, v80, v81\n" \
C_"v89, v90, v91\n" \
"v_and_b32_e32 v84, v12, v84\n" \
"v_and_b32_e32 v85, v13, v85\n" \
"v_and_b32_e32 v86, v14, v86\n" \
"v_and_b32_e32 v87, v15, v87\n" \
C_"v90, v92, v93\n" \
C_"v91, v94, v95\n" \
"v_add_u32_e32 v48, s16, v20\n" \
"v_add_f32_e32 v54, v54, v22\n" \
"v_accvgpr_read_b32 v55, a98\n" \
"v_accvgpr_read_b32 v23, a99\n" \
M_"a[96:99], a[120:123], a[120:123], 0\n" \
"v_max_f32_e32 v62, v62, v54\n" \
"v_add_f32_e32 v55, v55, v23\n" \
"v_max_f32_e32 v62, v62, v55\n" \
"v_rcp_f32_e32 v54, v54\n" \
"v_rcp_f32_e32 v55, v55\n" \
"v_mul_f32_e32 v54, v54, v58\n" \
"v_mul_f32_e32 v55, v55, v59\n" \
"v_mul_f32_e32 v128, v54, v128\n" \
"v_mul_f32_e32 v129, v54, v129\n" \
"v_mul_f32_e32 v136, v54, v136\n" \
"v_mul_f32_e32 v137, v54, v137\n" \
"v_mul_f32_e32 v144, v54, v144\n" \
"v_mul_f32_e32 v145, v54, v145\n" \
"v_mul_f32_e32 v152, v54, v152\n" \
"v_mul_f32_e32 v153, v54, v153\n" \
"v_mul_f32_e32 v160, v55, v160\n" \
"v_mul_f32_e32 v161, v55, v161\n" \
"v_mul_f32_e32 v168, v55, v168\n" \
"v_mul_f32_e32 v169, v55, v169\n" \
"v_mul_f32_e32 v176, v55, v176\n" \
"v_mul_f32_e32 v177, v55, v177\n" \
"v_mul_f32_e32 v184, v55, v184\n" \
"v_mul_f32_e32 v185, v55, v185\n" \
C_"v128, v128, v129\n" \
"v_mul_f32_e32 v130, v54, v130\n" \
"v_mul_f32_e32 v131, v54, v131\n" \
C_"v136, v136, v137\n" \
"v_mul_f32_e32 v138, v54, v138\n" \
"v_mul_f32_e32 v139, v54, v139\n" \
C_"v144, v144, v145\n" \
"v_mul_f32_e32 v146, v54, v146\n" \
"v_mul_f32_e32 v147, v54, v147\n" \
C_"v152, v152, v153\n" \
"v_mul_f32_e32 v154, v54, v154\n" \
"v_mul_f32_e32 v155, v54, v155\n" \
C_"v160, v160, v161\n" \
"v_mul_f32_e32 v162, v55, v162\n" \
"v_mul_f32_e32 v163, v55, v163\n" \
C_"v168, v168, v169\n" \
"v_mul_f32_e32 v170, v55, v170\n" \
"v_mul_f32_e32 v171, v55, v171\n" \
C_"v176, v176, v177\n" \
"v_mul_f32_e32 v178, v55, v178\n" \
"v_mul_f32_e32 v179, v55, v179\n" \
C_"v184, v184, v185\n" \
"v_mul_f32_e32 v186, v55, v186\n" \
"v_mul_f32_e32 v187, v55, v187\n" \
C_"v129, v130, v131\n" \
"v_mul_f32_e32 v132, v54, v132\n" \
"v_mul_f32_e32 v133, v54, v133\n" \
"v_mul_f32_e32 v134, v54, v134\n" \
"v_mul_f32_e32 v135, v54, v135\n" \
C_"v137, v138, v139\n" \
"v_mul_f32_e32 v140, v54, v140\n" \
"s_waitcnt vmcnt(12) lgkmcnt(0)\n" \
"s_barrier\n" \
M_"v[120:123], a[128:131], a[48:51], v[44:47]\n" \
"s_xor_b32 m0, s65, 0x12000\n" \
R_"a[188:191], v49 offset:7168\n" \
M_"v[120:123], a[132:135], a[52:55], v[120:123]\n" \
B_"v50, s[8:11], s64 offen offset:0 sc1 lds\n" \
M_"v[120:123], a[136:139], a[56:59], v[120:123]\n" \
R_"a[184:187], v49 offset:6144\n" \
R_"a[180:183], v49 offset:5120\n" \
M_"v[120:123], a[140:143], a[60:63], v[120:123]\n" \
B_"v50, s[8:11], s64 offen offset:1024 sc1 lds\n" \
M_"v[124:127], a[156:159], a[60:63], v[44:47]\n" \
R_"a[176:179], v49 offset:4096\n" \
R_"a[172:175], v49 offset:3072\n" \
M_"v[124:127], a[152:155], a[56:59], v[124:127]\n" \
R_"a[168:171], v49 offset:2048\n" \
R_"a[164:167], v49 offset:1024\n" \
M_"v[124:127], a[148:151], a[52:55], v[124:127]\n" \
R_"a[160:163], v49 offset:0\n" \
R_"a[0:3], v49 offset:8192\n" \
M_"v[124:127], a[144:147], a[48:51], v[124:127]\n" \
B_"v50, s[8:11], s64 offen offset:2048 sc1 lds\n" \
M_"v[32:35], a[124:127], a[64:67], 0\n" \
R_"a[4:7], v49 offset:9216\n" \
R_"a[8:11], v49 offset:10240\n" \
M_"v[36:39], a[124:127], a[80:83], 0\n" \
R_"a[12:15], v49 offset:11264\n" \
R_"a[16:19], v49 offset:12288\n" \
M_"v[40:43], a[124:127], a[192:195], 0\n" \
R_"a[20:23], v49 offset:13312\n" \
R_"a[24:27], v49 offset:14336\n" \
M_"v[44:47], a[124:127], a[208:211], 0\n" \
B_"v50, s[8:11], s64 offen offset:3072 sc1 lds\n" \
M_"v[32:35], a[224:227], a[68:71], v[32:35]\n" \
"s_xor_b32 m0, s65, 0xa000\n" \
R_"a[28:31], v49 offset:15360\n" \
M_"v[36:39], a[224:227], a[84:87], v[36:39]\n" \
E_"v120, v120\n" \
M_"v[40:43], a[224:227], a[196:199], v[40:43]\n" \
B_"v51, s[8:11], s64 offen offset:0 sc1 lds\n" \
M_"v[44:47], a[224:227], a[212:215], v[44:47]\n" \
E_"v121, v121\n" \
M_"v[32:35], a[228:231], a[72:75], v[32:35]\n" \
E_"v122, v122\n" \
M_"v[36:39], a[228:231], a[88:91], v[36:39]\n" \
E_"v123, v123\n" \
M_"v[40:43], a[228:231], a[200:203], v[40:43]\n" \
B_"v51, s[8:11], s64 offen offset:1024 sc1 lds\n" \
M_"v[44:47], a[228:231], a[216:219], v[44:47]\n" \
C_"v120, v120, v121\n" \
C_"v121, v122, v123\n" \
M_"v[32:35], a[232:235], a[76:79], v[32:35]\n" \
E_"v124, v124\n" \
M_"v[36:39], a[232:235], a[92:95], v[36:39]\n" \
E_"v125, v125\n" \
M_"v[40:43], a[232:235], a[204:207], v[40:43]\n" \
B_"v51, s[8:11], s64 offen offset:2048 sc1 lds\n" \
M_"v[44:47], a[232:235], a[220:223], v[44:47]\n" \
E_"v126, v126\n" \
"v_smfmac_f32_16x16x64_bf16 a[100:103], a[104:107], v[84:91], v21\n" \
C_"v122, v124, v125\n" \
"s_waitcnt lgkmcnt(1)\n" \
M_"v[220:223], a[188:191], v[84:87], v[220:223]\n" \
B_"v51, s[8:11], s64 offen offset:3072 sc1 lds\n" \
M_"v[252:255], a[188:191], v[88:91], v[252:255]\n" \
"s_add_u32 m0, s93, 0x1000\n" \
"v_and_b32_e32 v120, v12, v120\n" \
M_"v[216:219], a[184:187], v[84:87], v[216:219]\n" \
E_"v127, v127\n" \
M_"v[248:251], a[184:187], v[88:91], v[248:251]\n" \
C_"v123, v126, v127\n" \
"v_and_b32_e32 v121, v13, v121\n" \
M_"v[212:215], a[180:183], v[84:87], v[212:215]\n" \
B_"v4, s[80:83], s92 offen offset:0 lds\n" \
M_"v[244:247], a[180:183], v[88:91], v[244:247]\n" \
"v_and_b32_e32 v122, v14, v122\n" \
"v_and_b32_e32 v123, v15, v123\n" \
M_"v[208:211], a[176:179], v[84:87], v[208:211]\n" \
"v_accvgpr_read_b32 v56, a100\n" \
"v_accvgpr_read_b32 v24, a101\n" \
M_"v[240:243], a[176:179], v[88:91], v[240:243]\n" \
R_"a[128:131], v48 offset:0\n" \
R_"a[144:147], v48 offset:4096\n" \
M_"v[204:207], a[172:175], v[84:87], v[204:207]\n" \
B_"v5, s[80:83], s92 offen offset:1024 lds\n" \
M_"v[236:239], a[172:175], v[88:91], v[236:239]\n" \
R_"a[132:135], v48 offset:1024\n" \
R_"a[148:151], v48 offset:5120\n" \
M_"v[200:203], a[168:171], v[84:87], v[200:203]\n" \
R_"a[136:139], v48 offset:2048\n" \
R_"a[152:155], v48 offset:6144\n" \
M_"v[232:235], a[168:171], v[88:91], v[232:235]\n" \
B_"v6, s[80:83], s92 offen offset:2048 lds\n" \
M_"v[196:199], a[164:167], v[84:87], v[196:199]\n" \
R_"a[140:143], v48 offset:3072\n" \
R_"a[156:159], v48 offset:7168\n" \
M_"v[228:231], a[164:167], v[88:91], v[228:231]\n" \
"v_mul_f32_e32 v141, v54, v141\n" \
"v_mul_f32_e32 v142, v54, v142\n" \
M_"v[192:195], a[160:163], v[84:87], v[192:195]\n" \
"v_mul_f32_e32 v143, v54, v143\n" \
C_"v145, v146, v147\n" \
M_"v[224:227], a[160:163], v[88:91], v[224:227]\n" \
B_"v7, s[80:83], s92 offen offset:3072 lds\n" \
M_"a[100:103], a[116:119], v[120:123], a[100:103]\n" \
"v_mul_f32_e32 v148, v54, v148\n" \
"v_mul_f32_e32 v149, v54, v149\n" \
M_"v[224:227], a[0:3], v[120:123], v[224:227]\n" \
"v_mul_f32_e32 v150, v54, v150\n" \
"v_mul_f32_e32 v151, v54, v151\n" \
M_"v[228:231], a[4:7], v[120:123], v[228:231]\n" \
C_"v153, v154, v155\n" \
"v_mul_f32_e32 v156, v54, v156\n" \
M_"v[232:235], a[8:11], v[120:123], v[232:235]\n" \
"v_mul_f32_e32 v157, v54, v157\n" \
"v_mul_f32_e32 v158, v54, v158\n" \
M_"v[236:239], a[12:15], v[120:123], v[236:239]\n" \
"v_mul_f32_e32 v159, v54, v159\n" \
C_"v161, v162, v163\n" \
M_"v[240:243], a[16:19], v[120:123], v[240:243]\n" \
"v_mul_f32_e32 v164, v55, v164\n" \
"v_mul_f32_e32 v165, v55, v165\n" \
M_"v[244:247], a[20:23], v[120:123], v[244:247]\n" \
"v_mul_f32_e32 v166, v55, v166\n" \
"v_mul_f32_e32 v167, v55, v167\n" \
M_"v[248:251], a[24:27], v[120:123], v[248:251]\n" \
C_"v169, v170, v171\n" \
"s_waitcnt lgkmcnt(4)\n" \
M_"v[252:255], a[28:31], v[120:123], v[252:255]\n" \
"v_mul_f32_e32 v172, v55, v172\n" \
"v_mul_f32_e32 v173, v55, v173\n" \
"v_mul_f32_e32 v174, v55, v174\n" \
"v_mul_f32_e32 v175, v55, v175\n" \
C_"v177, v178, v179\n" \
"v_mul_f32_e32 v180, v55, v180\n" \
"v_mul_f32_e32 v181, v55, v181\n" \
"v_mul_f32_e32 v182, v55, v182\n" \
"v_mul_f32_e32 v183, v55, v183\n" \
C_"v185, v186, v187\n" \
"v_mul_f32_e32 v188, v55, v188\n" \
"v_mul_f32_e32 v189, v55, v189\n" \
"v_mul_f32_e32 v190, v55, v190\n" \
"v_mul_f32_e32 v191, v55, v191\n" \
C_"v130, v132, v133\n" \
C_"v131, v134, v135\n" \
"buffer_store_dwordx4 v[128:131], v20, s[56:59], s68 offen offset:0 sc1\n" \
C_"v138, v140, v141\n" \
C_"v139, v142, v143\n" \
"buffer_store_dwordx4 v[136:139], v20, s[56:59], s68 offen offset:1024 sc1\n" \
C_"v146, v148, v149\n" \
C_"v147, v150, v151\n" \
"buffer_store_dwordx4 v[144:147], v20, s[56:59], s68 offen offset:2048 sc1\n" \
C_"v154, v156, v157\n" \
C_"v155, v158, v159\n" \
"buffer_store_dwordx4 v[152:155], v20, s[56:59], s68 offen offset:3072 sc1\n" \
C_"v162, v164, v165\n" \
C_"v163, v166, v167\n" \
"buffer_store_dwordx4 v[160:163], v20, s[56:59], s69 offen offset:0 sc1\n" \
C_"v170, v172, v173\n" \
C_"v171, v174, v175\n" \
"buffer_store_dwordx4 v[168:171], v20, s[56:59], s69 offen offset:1024 sc1\n" \
C_"v178, v180, v181\n" \
C_"v179, v182, v183\n" \
"buffer_store_dwordx4 v[176:179], v20, s[56:59], s69 offen offset:2048 sc1\n" \
C_"v186, v188, v189\n" \
C_"v187, v190, v191\n" \
"buffer_store_dwordx4 v[184:187], v20, s[56:59], s69 offen offset:3072 sc1\n" \
"s_add_u32 m0, s93, 0x2000\n" \
M_"a[112:115], a[120:123], v[128:131], a[112:115]\n" \
B_"v8, s[80:83], s92 offen offset:0 lds\n" \
M_"a[112:115], a[120:123], v[136:139], a[112:115]\n" \
"v_mfma_f32_32x32x16_bf16 v[128:143], a[120:123], a[120:123], 0\n" \
M_"a[112:115], a[120:123], v[144:147], a[112:115]\n" \
M_"a[112:115], a[120:123], v[152:155], a[112:115]\n" \
"v_mfma_f32_32x32x16_bf16 v[144:159], a[120:123], a[120:123], 0\n" \
"v_add_f32_e32 v56, v56, v24\n" \
M_"a[112:115], a[120:123], v[160:163], a[112:115]\n" \
"v_max_f32_e32 v62, v62, v56\n" \
"v_accvgpr_read_b32 v57, a102\n" \
"s_waitcnt vmcnt(21) lgkmcnt(0)\n" \
"s_barrier\n" \
M_"v[64:67], a[128:131], a[64:67], v[32:35]\n" \
"s_xor_b32 m0, s65, 0x18000\n" \
"v_accvgpr_read_b32 v25, a103\n" \
M_"a[100:103], a[120:123], a[120:123], 0\n" \
B_"v52, s[8:11], s64 offen offset:0 sc1 lds\n" \
M_"v[64:67], a[132:135], a[68:71], v[64:67]\n" \
"v_rcp_f32_e32 v56, v56\n" \
M_"v[64:67], a[136:139], a[72:75], v[64:67]\n" \
M_"a[112:115], a[120:123], v[168:171], a[112:115]\n" \
"v_mfma_f32_32x32x16_bf16 v[160:175], a[120:123], a[120:123], 0\n" \
"v_mul_f32_e32 v56, v56, v60\n" \
M_"a[112:115], a[120:123], v[176:179], a[112:115]\n" \
"v_mul_f32_e32 v192, v56, v192\n" \
"v_mul_f32_e32 v193, v56, v193\n" \
M_"v[64:67], a[140:143], a[76:79], v[64:67]\n" \
B_"v52, s[8:11], s64 offen offset:1024 sc1 lds\n" \
M_"v[68:71], a[156:159], a[76:79], v[32:35]\n" \
C_"v192, v192, v193\n" \
"v_mul_f32_e32 v194, v56, v194\n" \
M_"v[68:71], a[152:155], a[72:75], v[68:71]\n" \
"v_mul_f32_e32 v195, v56, v195\n" \
C_"v193, v194, v195\n" \
M_"v[68:71], a[148:151], a[68:71], v[68:71]\n" \
M_"a[112:115], a[120:123], v[184:187], a[112:115]\n" \
"v_mfma_f32_32x32x16_bf16 v[176:191], a[120:123], a[120:123], 0\n" \
"v_mul_f32_e32 v196, v56, v196\n" \
"v_mul_f32_e32 v197, v56, v197\n" \
"v_mul_f32_e32 v198, v56, v198\n" \
"v_mul_f32_e32 v199, v56, v199\n" \
"v_mul_f32_e32 v200, v56, v200\n" \
"v_mul_f32_e32 v201, v56, v201\n" \
M_"v[68:71], a[144:147], a[64:67], v[68:71]\n" \
B_"v52, s[8:11], s64 offen offset:2048 sc1 lds\n" \
M_"v[76:79], a[144:147], a[80:83], v[36:39]\n" \
C_"v194, v196, v197\n" \
C_"v195, v198, v199\n" \
"buffer_store_dwordx4 v[192:195], v20, s[56:59], s70 offen offset:0 sc1\n" \
M_"v[76:79], a[148:151], a[84:87], v[76:79]\n" \
C_"v200, v200, v201\n" \
"v_mul_f32_e32 v202, v56, v202\n" \
M_"v[76:79], a[152:155], a[88:91], v[76:79]\n" \
"v_mul_f32_e32 v203, v56, v203\n" \
"v_add_f32_e32 v57, v57, v25\n" \
M_"v[76:79], a[156:159], a[92:95], v[76:79]\n" \
B_"v52, s[8:11], s64 offen offset:3072 sc1 lds\n" \
M_"v[72:75], a[140:143], a[92:95], v[36:39]\n" \
"s_mov_b32 m0, s65\n" \
C_"v201, v202, v203\n" \
M_"v[72:75], a[136:139], a[88:91], v[72:75]\n" \
M_"a[112:115], a[120:123], v[192:195], a[112:115]\n" \
"v_mul_f32_e32 v204, v56, v204\n" \
"v_mul_f32_e32 v205, v56, v205\n" \
M_"v[72:75], a[132:135], a[84:87], v[72:75]\n" \
"v_mul_f32_e32 v206, v56, v206\n" \
"v_mul_f32_e32 v207, v56, v207\n" \
M_"v[72:75], a[128:131], a[80:83], v[72:75]\n" \
B_"v53, s[8:11], s64 offen offset:0 sc1 lds\n" \
M_"v[80:83], a[128:131], a[192:195], v[40:43]\n" \
"v_mul_f32_e32 v208, v56, v208\n" \
"v_mul_f32_e32 v209, v56, v209\n" \
M_"v[80:83], a[132:135], a[196:199], v[80:83]\n" \
"v_max_f32_e32 v62, v62, v57\n" \
C_"v202, v204, v205\n" \
M_"v[80:83], a[136:139], a[200:203], v[80:83]\n" \
B_"v53, s[8:11], s64 offen offset:1024 sc1 lds\n" \
M_"v[80:83], a[140:143], a[204:207], v[80:83]\n" \
C_"v203, v206, v207\n" \
C_"v208, v208, v209\n" \
"buffer_store_dwordx4 v[200:203], v20, s[56:59], s70 offen offset:1024 sc1\n" \
M_"v[84:87], a[156:159], a[204:207], v[40:43]\n" \
"v_mul_f32_e32 v210, v56, v210\n" \
"v_mul_f32_e32 v211, v56, v211\n" \
M_"v[84:87], a[152:155], a[200:203], v[84:87]\n" \
"v_rcp_f32_e32 v57, v57\n" \
M_"v[84:87], a[148:151], a[196:199], v[84:87]\n" \
B_"v53, s[8:11], s64 offen offset:2048 sc1 lds\n" \
M_"v[84:87], a[144:147], a[192:195], v[84:87]\n" \
M_"a[112:115], a[120:123], v[200:203], a[112:115]\n" \
"v_mfma_f32_32x32x16_bf16 v[192:207], a[120:123], a[120:123], 0\n" \
C_"v209, v210, v211\n" \
"v_mul_f32_e32 v212, v56, v212\n" \
".fill (L_ab + 20480 - .) / 4, 4, 0xBF800000\n" \
"L_ap5:\n" \
"s_cbranch_execz L_ap0\n" \
"v_mul_f32_e32 v213, v56, v213\n" \
"v_mul_f32_e32 v214, v56, v214\n" \
"v_mul_f32_e32 v215, v56, v215\n" \
"v_mul_f32_e32 v216, v56, v216\n" \
M_"v[92:95], a[144:147], a[208:211], v[44:47]\n" \
R_"a[144:147], v48 offset:12288\n" \
"v_mul_f32_e32 v217, v56, v217\n" \
M_"v[92:95], a[148:151], a[212:215], v[92:95]\n" \
R_"a[148:151], v48 offset:13312\n" \
C_"v210, v212, v213\n" \
M_"v[92:95], a[152:155], a[216:219], v[92:95]\n" \
R_"a[152:155], v48 offset:14336\n" \
C_"v211, v214, v215\n" \
"buffer_store_dwordx4 v[208:211], v20, s[56:59], s70 offen offset:2048 sc1\n" \
M_"v[92:95], a[156:159], a[220:223], v[92:95]\n" \
R_"a[156:159], v48 offset:15360\n" \
C_"v216, v216, v217\n" \
M_"v[88:91], a[140:143], a[220:223], v[44:47]\n" \
R_"a[140:143], v48 offset:11264\n" \
"v_mul_f32_e32 v218, v56, v218\n" \
M_"v[88:91], a[136:139], a[216:219], v[88:91]\n" \
R_"a[136:139], v48 offset:10240\n" \
"v_mul_f32_e32 v219, v56, v219\n" \
M_"v[88:91], a[132:135], a[212:215], v[88:91]\n" \
R_"a[132:135], v48 offset:9216\n" \
"v_mul_f32_e32 v57, v57, v61\n" \
M_"v[88:91], a[128:131], a[208:211], v[88:91]\n" \
R_"a[128:131], v48 offset:8192\n" \
M_"a[112:115], a[120:123], v[208:211], a[112:115]\n" \
C_"v217, v218, v219\n" \
"v_mul_f32_e32 v220, v56, v220\n" \
"v_mul_f32_e32 v221, v56, v221\n" \
"v_mul_f32_e32 v222, v56, v222\n" \
"v_mul_f32_e32 v223, v56, v223\n" \
"v_mul_f32_e32 v224, v57, v224\n" \
"v_mul_f32_e32 v225, v57, v225\n" \
C_"v218, v220, v221\n" \
C_"v219, v222, v223\n" \
"buffer_store_dwordx4 v[216:219], v20, s[56:59], s70 offen offset:3072 sc1\n" \
C_"v224, v224, v225\n" \
"v_mul_f32_e32 v226, v57, v226\n" \
"v_mul_f32_e32 v227, v57, v227\n" \
M_"a[112:115], a[120:123], v[216:219], a[112:115]\n" \
"v_mfma_f32_32x32x16_bf16 v[208:223], a[120:123], a[120:123], 0\n" \
C_"v225, v226, v227\n" \
"v_mul_f32_e32 v228, v57, v228\n" \
"v_mul_f32_e32 v229, v57, v229\n" \
"v_mul_f32_e32 v230, v57, v230\n" \
"v_mul_f32_e32 v231, v57, v231\n" \
"v_mul_f32_e32 v232, v57, v232\n" \
"v_mul_f32_e32 v233, v57, v233\n" \
C_"v226, v228, v229\n" \
C_"v227, v230, v231\n" \
"buffer_store_dwordx4 v[224:227], v20, s[56:59], s71 offen offset:0 sc1\n" \
C_"v232, v232, v233\n" \
"v_mul_f32_e32 v234, v57, v234\n" \
"v_mul_f32_e32 v235, v57, v235\n" \
M_"a[112:115], a[120:123], v[224:227], a[112:115]\n" \
C_"v233, v234, v235\n" \
"v_mul_f32_e32 v236, v57, v236\n" \
"v_mul_f32_e32 v237, v57, v237\n" \
"v_mul_f32_e32 v238, v57, v238\n" \
"v_mul_f32_e32 v239, v57, v239\n" \
"v_mul_f32_e32 v240, v57, v240\n" \
"v_mul_f32_e32 v241, v57, v241\n" \
C_"v234, v236, v237\n" \
C_"v235, v238, v239\n" \
"buffer_store_dwordx4 v[232:235], v20, s[56:59], s71 offen offset:1024 sc1\n" \
C_"v240, v240, v241\n" \
"v_mul_f32_e32 v242, v57, v242\n" \
"v_mul_f32_e32 v243, v57, v243\n" \
M_"a[112:115], a[120:123], v[232:235], a[112:115]\n" \
"v_mfma_f32_32x32x16_bf16 v[224:239], a[120:123], a[120:123], 0\n" \
C_"v241, v242, v243\n" \
"v_mul_f32_e32 v244, v57, v244\n" \
"v_mul_f32_e32 v245, v57, v245\n" \
"v_mul_f32_e32 v246, v57, v246\n" \
"v_mul_f32_e32 v247, v57, v247\n" \
"v_mul_f32_e32 v248, v57, v248\n" \
"v_mul_f32_e32 v249, v57, v249\n" \
C_"v242, v244, v245\n" \
C_"v243, v246, v247\n" \
"buffer_store_dwordx4 v[240:243], v20, s[56:59], s71 offen offset:2048 sc1\n" \
C_"v248, v248, v249\n" \
"v_mul_f32_e32 v250, v57, v250\n" \
"v_mul_f32_e32 v251, v57, v251\n" \
M_"a[112:115], a[120:123], v[240:243], a[112:115]\n" \
C_"v249, v250, v251\n" \
"v_mul_f32_e32 v252, v57, v252\n" \
"v_mul_f32_e32 v253, v57, v253\n" \
"v_mul_f32_e32 v254, v57, v254\n" \
"v_mul_f32_e32 v255, v57, v255\n" \
C_"v250, v252, v253\n" \
C_"v251, v254, v255\n" \
"buffer_store_dwordx4 v[248:251], v20, s[56:59], s71 offen offset:3072 sc1\n" \
B_"v53, s[8:11], s64 offen offset:3072 sc1 lds\n" \
"s_waitcnt vmcnt(16) lgkmcnt(0)\n" \
"s_barrier\n" \
M_"a[112:115], a[120:123], v[248:251], a[112:115]\n" \
"v_mfma_f32_32x32x16_bf16 v[240:255], a[120:123], a[120:123], 0\n" \
E_"v70, v70\n" \
E_"v71, v71\n" \
E_"v72, v72\n" \
E_"v73, v73\n" \
"s_add_u32 s64, s64, 0x10000\n" \
"v_cmp_lt_f32_e64 s[72:73], s21, v62\n" \
"v_readlane_b32 s95, v11, 3\n" \
"v_accvgpr_read_b32 v63, a112\n" \
"v_cmp_u_f32_e32 vcc, v63, v63\n" \
E_"v68, v68\n" \
E_"v69, v69\n" \
E_"v74, v74\n" \
E_"v75, v75\n" \
"s_or_b64 vcc, s[72:73], vcc\n" \
"s_cselect_b32 s95, 0x40000000, s95\n" \
"s_cselect_b32 s94, 0x7f800000, 0\n" \
C_"v71, v70, v71\n" \
E_"v66, v66\n" \
E_"v67, v67\n" \
E_"v64, v64\n" \
E_"v65, v65\n" \
C_"v72, v72, v73\n" \
E_"v76, v76\n" \
E_"v77, v77\n" \
E_"v78, v78\n" \
E_"v79, v79\n" \
C_"v70, v68, v69\n" \
C_"v73, v74, v75\n" \
"v_add_u32_e32 v49, 0x10000, v48\n" \
"s_xor_b32 s65, s65, 0x8000\n" \
C_"v69, v66, v67\n" \
C_"v68, v64, v65\n" \
C_"v74, v76, v77\n" \
C_"v75, v78, v79\n" \
"v_xor_b32_e32 v48, 0x8000, v48\n" \
"s_sub_u32 s66, s19, 2\n" \
"s_branch L_2_%=\n" \
"L_f1_%=:\n" \
R_"a[160:163], v49 offset:0\n" \
R_"a[164:167], v49 offset:1024\n" \
R_"a[168:171], v49 offset:2048\n" \
M_"v[104:107], a[128:131], a[80:83], v[36:39]\n" \
C_"v192, v192, v193\n" \
C_"v224, v224, v225\n" \
C_"v193, v194, v195\n" \
C_"v225, v226, v227\n" \
R_"a[172:175], v49 offset:3072\n" \
C_"v194, v196, v197\n" \
C_"v195, v198, v199\n" \
C_"v226, v228, v229\n" \
C_"v227, v230, v231\n" \
M_"v[104:107], a[132:135], a[84:87], v[104:107]\n" \
"v_lshl_add_u32 v50, s15, 2, v20\n" \
C_"v196, v200, v201\n" \
C_"v197, v202, v203\n" \
C_"v198, v204, v205\n" \
C_"v199, v206, v207\n" \
C_"v228, v232, v233\n" \
C_"v229, v234, v235\n" \
C_"v230, v236, v237\n" \
C_"v231, v238, v239\n" \
"v_add_u32_e32 v50, 0x1a000, v50\n" \
"v_mov_b32_e32 v61, s62\n" \
C_"v200, v208, v209\n" \
C_"v201, v210, v211\n" \
C_"v202, v212, v213\n" \
C_"v203, v214, v215\n" \
C_"v204, v216, v217\n" \
C_"v205, v218, v219\n" \
C_"v206, v220, v221\n" \
C_"v207, v222, v223\n" \
C_"v232, v240, v241\n" \
C_"v233, v242, v243\n" \
C_"v234, v244, v245\n" \
C_"v235, v246, v247\n" \
C_"v236, v248, v249\n" \
C_"v237, v250, v251\n" \
C_"v238, v252, v253\n" \
C_"v239, v254, v255\n" \
"ds_write_b128 v50, v[192:195] offset:0\n" \
M_"v[104:107], a[136:139], a[88:91], v[104:107]\n" \
R_"a[176:179], v49 offset:4096\n" \
"v_add_u32_e32 v56, 0x49000, v20\n" \
M_"v[104:107], a[140:143], a[92:95], v[104:107]\n" \
"ds_write_b128 v50, v[196:199] offset:1024\n" \
M_"v[108:111], a[156:159], a[92:95], v[36:39]\n" \
R_"a[180:183], v49 offset:5120\n" \
R_"a[184:187], v49 offset:6144\n" \
M_"v[108:111], a[152:155], a[88:91], v[108:111]\n" \
"ds_write_b128 v50, v[200:203] offset:2048\n" \
M_"v[108:111], a[148:151], a[84:87], v[108:111]\n" \
"v_and_b32_e32 v68, v12, v68\n" \
"v_and_b32_e32 v69, v13, v69\n" \
M_"v[108:111], a[144:147], a[80:83], v[108:111]\n" \
"ds_write_b128 v50, v[204:207] offset:3072\n" \
"v_and_b32_e32 v70, v14, v70\n" \
"v_and_b32_e32 v71, v15, v71\n" \
R_"a[188:191], v49 offset:7168\n" \
R_"a[0:3], v20 offset:8192\n" \
"s_waitcnt lgkmcnt(9)\n" \
M_"v[128:131], a[160:163], v[68:71], v[128:131]\n" \
"ds_write_b128 v50, v[224:227] offset:4096\n" \
M_"v[160:163], a[160:163], v[72:75], v[160:163]\n" \
R_"a[4:7], v20 offset:9216\n" \
R_"a[8:11], v20 offset:10240\n" \
M_"v[132:135], a[164:167], v[68:71], v[132:135]\n" \
"ds_write_b128 v50, v[228:231] offset:5120\n" \
M_"v[164:167], a[164:167], v[72:75], v[164:167]\n" \
R_"a[12:15], v20 offset:11264\n" \
R_"a[28:31], v20 offset:15360\n" \
M_"v[136:139], a[168:171], v[68:71], v[136:139]\n" \
"ds_write_b128 v50, v[232:235] offset:6144\n" \
M_"v[168:171], a[168:171], v[72:75], v[168:171]\n" \
R_"a[24:27], v20 offset:14336\n" \
R_"a[20:23], v20 offset:13312\n" \
M_"v[140:143], a[172:175], v[68:71], v[140:143]\n" \
"ds_write_b128 v50, v[236:239] offset:7168\n" \
M_"v[172:175], a[172:175], v[72:75], v[172:175]\n" \
R_"a[16:19], v20 offset:12288\n" \
"s_waitcnt lgkmcnt(15)\n" \
M_"v[144:147], a[176:179], v[68:71], v[144:147]\n" \
"v_xor_b32_e32 v48, 0x4000, v49\n" \
"ds_read_b32 v58, v18 offset:256\n" \
M_"v[176:179], a[176:179], v[72:75], v[176:179]\n" \
"ds_read_b32 v59, v18 offset:384\n" \
R_"a[32:35], v48 offset:8192\n" \
M_"v[148:151], a[180:183], v[68:71], v[148:151]\n" \
R_"a[36:39], v48 offset:9216\n" \
R_"a[40:43], v48 offset:10240\n" \
M_"v[180:183], a[180:183], v[72:75], v[180:183]\n" \
R_"a[44:47], v48 offset:11264\n" \
R_"a[48:51], v48 offset:12288\n" \
M_"v[152:155], a[184:187], v[68:71], v[152:155]\n" \
R_"a[52:55], v48 offset:13312\n" \
R_"a[56:59], v48 offset:14336\n" \
M_"v[184:187], a[184:187], v[72:75], v[184:187]\n" \
R_"a[60:63], v48 offset:15360\n" \
"s_waitcnt lgkmcnt(15)\n" \
M_"v[156:159], a[188:191], v[68:71], v[156:159]\n" \
R_"a[128:131], v49 offset:8192\n" \
R_"a[132:135], v49 offset:9216\n" \
M_"v[188:191], a[188:191], v[72:75], v[188:191]\n" \
R_"a[136:139], v49 offset:10240\n" \
R_"a[140:143], v49 offset:11264\n" \
"v_smfmac_f32_16x16x64_bf16 a[96:99], a[104:107], v[68:75], v21\n" \
R_"a[144:147], v49 offset:12288\n" \
R_"a[148:151], v49 offset:13312\n" \
M_"v[112:115], a[0:3], a[64:67], v[32:35]\n" \
R_"a[152:155], v49 offset:14336\n" \
R_"a[156:159], v49 offset:15360\n" \
M_"v[112:115], a[4:7], a[68:71], v[112:115]\n" \
"v_accvgpr_read_b32 v40, a100\n" \
"v_accvgpr_read_b32 v24, a101\n" \
M_"v[112:115], a[8:11], a[72:75], v[112:115]\n" \
"v_accvgpr_read_b32 v41, a102\n" \
"v_accvgpr_read_b32 v25, a103\n" \
M_"v[112:115], a[12:15], a[76:79], v[112:115]\n" \
"v_add_f32_e32 v40, v40, v24\n" \
"v_add_f32_e32 v41, v41, v25\n" \
M_"v[116:119], a[28:31], a[76:79], v[32:35]\n" \
"v_add_f32_e32 v40, s94, v40\n" \
"s_waitcnt lgkmcnt(15)\n" \
M_"v[116:119], a[24:27], a[72:75], v[116:119]\n" \
"v_add_f32_e32 v41, s94, v41\n" \
"ds_write_b64 v19, v[40:41]\n" \
M_"v[116:119], a[20:23], a[68:71], v[116:119]\n" \
"v_add_u32_e32 v52, s15, v20\n" \
"v_add_u32_e32 v51, 0x10000, v52\n" \
M_"v[116:119], a[16:19], a[64:67], v[116:119]\n" \
E_"v104, v104\n" \
M_"v[124:127], a[16:19], a[80:83], v[36:39]\n" \
E_"v105, v105\n" \
M_"v[124:127], a[20:23], a[84:87], v[124:127]\n" \
E_"v106, v106\n" \
M_"v[124:127], a[24:27], a[88:91], v[124:127]\n" \
E_"v107, v107\n" \
M_"v[124:127], a[28:31], a[92:95], v[124:127]\n" \
C_"v104, v104, v105\n" \
C_"v105, v106, v107\n" \
M_"v[120:123], a[12:15], a[92:95], v[36:39]\n" \
E_"v108, v108\n" \
M_"v[120:123], a[8:11], a[88:91], v[120:123]\n" \
E_"v109, v109\n" \
M_"v[120:123], a[4:7], a[84:87], v[120:123]\n" \
E_"v110, v110\n" \
M_"v[120:123], a[0:3], a[80:83], v[120:123]\n" \
E_"v111, v111\n" \
"s_waitcnt lgkmcnt(0)\n" \
"s_barrier\n" \
R_"v[208:211], v52 offset:0\n" \
R_"v[212:215], v52 offset:1024\n" \
R_"v[216:219], v52 offset:2048\n" \
R_"v[220:223], v52 offset:3072\n" \
R_"v[240:243], v51 offset:32768\n" \
R_"v[244:247], v51 offset:33792\n" \
R_"v[248:251], v51 offset:34816\n" \
R_"v[252:255], v51 offset:35840\n" \
C_"v106, v108, v109\n" \
C_"v107, v110, v111\n" \
"v_and_b32_e32 v104, v12, v104\n" \
"v_and_b32_e32 v105, v13, v105\n" \
"v_and_b32_e32 v106, v14, v106\n" \
"s_waitcnt vmcnt(0)\n" \
"s_mov_b64 exec, 1\n" \
"buffer_store_dword v61, off, s[56:59], s95 offset:0 sc1\n" \
"s_mov_b64 exec, -1\n" \
"v_and_b32_e32 v107, v15, v107\n" \
"v_xor_b32_e32 v63, 0x400, v19\n" \
"ds_read_b64 v[42:43], v63\n" \
M_"a[96:99], a[116:119], v[104:107], a[96:99]\n" \
"v_mov_b32_e32 v194, v28\n" \
"v_mov_b32_e32 v195, v29\n" \
M_"v[160:163], a[128:131], v[104:107], v[160:163]\n" \
"v_mov_b32_e32 v196, 0\n" \
"v_mov_b32_e32 v197, 0\n" \
M_"v[164:167], a[132:135], v[104:107], v[164:167]\n" \
"v_mov_b32_e32 v192, 0\n" \
"v_mov_b32_e32 v193, 0\n" \
M_"v[168:171], a[136:139], v[104:107], v[168:171]\n" \
"v_readlane_b32 s93, v11, 25\n" \
"v_readlane_b32 s92, v11, 30\n" \
M_"v[172:175], a[140:143], v[104:107], v[172:175]\n" \
B_"a[192:195], v20, s[56:59], s60 offen offset:0 sc1\n" \
M_"v[176:179], a[144:147], v[104:107], v[176:179]\n" \
E_"v118, v118\n" \
M_"v[180:183], a[148:151], v[104:107], v[180:183]\n" \
E_"v119, v119\n" \
M_"v[184:187], a[152:155], v[104:107], v[184:187]\n" \
"s_add_u32 m0, s93, 0x0\n" \
C_"v119, v118, v119\n" \
M_"v[188:191], a[156:159], v[104:107], v[188:191]\n" \
E_"v120, v120\n" \
"s_waitcnt lgkmcnt(1)\n" \
"s_barrier\n" \
B_"a[208:211], v56, s[56:59], s60 offen offset:0 sc1\n" \
M_"v[128:131], v[194:197], v[208:211], v[128:131]\n" \
E_"v121, v121\n" \
M_"v[132:135], v[192:195], v[208:211], v[132:135]\n" \
E_"v116, v116\n" \
M_"v[136:139], v[194:197], v[212:215], v[136:139]\n" \
E_"v117, v117\n" \
M_"v[140:143], v[192:195], v[212:215], v[140:143]\n" \
E_"v122, v122\n" \
M_"v[144:147], v[194:197], v[216:219], v[144:147]\n" \
E_"v123, v123\n" \
M_"v[148:151], v[192:195], v[216:219], v[148:151]\n" \
B_"a[196:199], v20, s[56:59], s60 offen offset:1024 sc1\n" \
M_"v[152:155], v[194:197], v[220:223], v[152:155]\n" \
E_"v114, v114\n" \
M_"v[156:159], v[192:195], v[220:223], v[156:159]\n" \
E_"v115, v115\n" \
M_"v[160:163], v[194:197], v[240:243], v[160:163]\n" \
E_"v112, v112\n" \
M_"v[164:167], v[192:195], v[240:243], v[164:167]\n" \
E_"v113, v113\n" \
M_"v[168:171], v[194:197], v[244:247], v[168:171]\n" \
C_"v120, v120, v121\n" \
C_"v118, v116, v117\n" \
M_"v[172:175], v[192:195], v[244:247], v[172:175]\n" \
B_"a[212:215], v56, s[56:59], s60 offen offset:1024 sc1\n" \
M_"v[176:179], v[194:197], v[248:251], v[176:179]\n" \
E_"v124, v124\n" \
M_"v[180:183], v[192:195], v[248:251], v[180:183]\n" \
E_"v125, v125\n" \
M_"v[184:187], v[194:197], v[252:255], v[184:187]\n" \
E_"v126, v126\n" \
M_"v[188:191], v[192:195], v[252:255], v[188:191]\n" \
E_"v127, v127\n" \
C_"v121, v122, v123\n" \
C_"v117, v114, v115\n" \
C_"v116, v112, v113\n" \
C_"v122, v124, v125\n" \
C_"v123, v126, v127\n" \
B_"a[200:203], v20, s[56:59], s60 offen offset:2048 sc1\n" \
"s_nop 0\n" \
"v_smfmac_f32_16x16x64_bf16 a[96:99], a[104:107], v[116:123], v21\n" \
M_"v[128:131], a[32:35], v[116:119], v[128:131]\n" \
M_"v[160:163], a[32:35], v[120:123], v[160:163]\n" \
M_"v[132:135], a[36:39], v[116:119], v[132:135]\n" \
M_"v[164:167], a[36:39], v[120:123], v[164:167]\n" \
M_"v[136:139], a[40:43], v[116:119], v[136:139]\n" \
B_"a[216:219], v56, s[56:59], s60 offen offset:2048 sc1\n" \
M_"v[168:171], a[40:43], v[120:123], v[168:171]\n" \
M_"v[140:143], a[44:47], v[116:119], v[140:143]\n" \
"v_accvgpr_read_b32 v54, a96\n" \
"v_accvgpr_read_b32 v22, a97\n" \
M_"v[172:175], a[44:47], v[120:123], v[172:175]\n" \
"v_add_f32_e32 v54, v54, v22\n" \
"s_waitcnt lgkmcnt(0)\n" \
"v_add_f32_e32 v54, v54, v42\n" \
M_"v[144:147], a[48:51], v[116:119], v[144:147]\n" \
"v_max_f32_e32 v62, v62, v54\n" \
"v_accvgpr_read_b32 v55, a98\n" \
M_"v[176:179], a[48:51], v[120:123], v[176:179]\n" \
"v_rcp_f32_e32 v54, v54\n" \
B_"a[204:207], v20, s[56:59], s60 offen offset:3072 sc1\n" \
M_"v[148:151], a[52:55], v[116:119], v[148:151]\n" \
"v_mul_f32_e32 v54, v54, v58\n" \
"v_mul_f32_e32 v128, v54, v128\n" \
M_"v[180:183], a[52:55], v[120:123], v[180:183]\n" \
"v_mul_f32_e32 v129, v54, v129\n" \
"v_mul_f32_e32 v130, v54, v130\n" \
"v_mul_f32_e32 v131, v54, v131\n" \
"v_mul_f32_e32 v132, v54, v132\n" \
"v_mul_f32_e32 v133, v54, v133\n" \
"v_mul_f32_e32 v134, v54, v134\n" \
"v_mul_f32_e32 v135, v54, v135\n" \
C_"v64, v128, v129\n" \
C_"v65, v130, v131\n" \
C_"v66, v132, v133\n" \
C_"v67, v134, v135\n" \
"v_accvgpr_read_b32 v23, a99\n" \
M_"v[152:155], a[56:59], v[116:119], v[152:155]\n" \
M_"a[112:115], a[120:123], v[64:67], a[112:115]\n" \
B_"a[220:223], v56, s[56:59], s60 offen offset:3072 sc1\n" \
"v_mul_f32_e32 v136, v54, v136\n" \
"v_mul_f32_e32 v137, v54, v137\n" \
"v_mul_f32_e32 v138, v54, v138\n" \
"v_mul_f32_e32 v139, v54, v139\n" \
"v_mul_f32_e32 v140, v54, v140\n" \
"v_mul_f32_e32 v141, v54, v141\n" \
"v_mul_f32_e32 v142, v54, v142\n" \
"v_mul_f32_e32 v143, v54, v143\n" \
"v_add_f32_e32 v55, v55, v23\n" \
C_"v68, v136, v137\n" \
C_"v69, v138, v139\n" \
C_"v70, v140, v141\n" \
C_"v71, v142, v143\n" \
"v_add_f32_e32 v55, v55, v43\n" \
"v_mul_f32_e32 v144, v54, v144\n" \
M_"a[112:115], a[120:123], v[68:71], a[112:115]\n" \
"v_mul_f32_e32 v145, v54, v145\n" \
"v_mul_f32_e32 v146, v54, v146\n" \
"v_mul_f32_e32 v147, v54, v147\n" \
"v_mul_f32_e32 v148, v54, v148\n" \
B_"v0, s[80:83], s92 offen offset:0 lds\n" \
"v_mul_f32_e32 v149, v54, v149\n" \
"v_mul_f32_e32 v150, v54, v150\n" \
"v_mul_f32_e32 v151, v54, v151\n" \
"v_max_f32_e32 v62, v62, v55\n" \
C_"v72, v144, v145\n" \
C_"v73, v146, v147\n" \
C_"v74, v148, v149\n" \
C_"v75, v150, v151\n" \
"v_rcp_f32_e32 v55, v55\n" \
M_"v[184:187], a[56:59], v[120:123], v[184:187]\n" \
M_"a[112:115], a[120:123], v[72:75], a[112:115]\n" \
"v_mul_f32_e32 v152, v54, v152\n" \
"v_mul_f32_e32 v153, v54, v153\n" \
"v_mul_f32_e32 v154, v54, v154\n" \
"v_mul_f32_e32 v155, v54, v155\n" \
C_"v76, v152, v153\n" \
B_"v1, s[80:83], s92 offen offset:1024 lds\n" \
C_"v77, v154, v155\n" \
"v_mul_f32_e32 v55, v55, v59\n" \
"v_mul_f32_e32 v160, v55, v160\n" \
"v_mul_f32_e32 v161, v55, v161\n" \
"v_mul_f32_e32 v162, v55, v162\n" \
"v_mul_f32_e32 v163, v55, v163\n" \
"v_mul_f32_e32 v164, v55, v164\n" \
"v_mul_f32_e32 v165, v55, v165\n" \
"v_mul_f32_e32 v166, v55, v166\n" \
"v_mul_f32_e32 v167, v55, v167\n" \
C_"v80, v160, v161\n" \
C_"v81, v162, v163\n" \
C_"v82, v164, v165\n" \
C_"v83, v166, v167\n" \
M_"v[156:159], a[60:63], v[116:119], v[156:159]\n" \
"v_mul_f32_e32 v168, v55, v168\n" \
"v_mul_f32_e32 v169, v55, v169\n" \
"v_mul_f32_e32 v170, v55, v170\n" \
"v_mul_f32_e32 v171, v55, v171\n" \
"v_mul_f32_e32 v172, v55, v172\n" \
B_"v2, s[80:83], s92 offen offset:2048 lds\n" \
"v_mul_f32_e32 v173, v55, v173\n" \
"v_mul_f32_e32 v174, v55, v174\n" \
"v_mul_f32_e32 v156, v54, v156\n" \
"v_mul_f32_e32 v157, v54, v157\n" \
"v_mul_f32_e32 v158, v54, v158\n" \
"v_mul_f32_e32 v159, v54, v159\n" \
C_"v78, v156, v157\n" \
C_"v79, v158, v159\n" \
"v_mul_f32_e32 v175, v55, v175\n" \
C_"v84, v168, v169\n" \
M_"a[112:115], a[120:123], v[76:79], a[112:115]\n" \
M_"a[112:115], a[120:123], v[80:83], a[112:115]\n" \
C_"v85, v170, v171\n" \
C_"v86, v172, v173\n" \
C_"v87, v174, v175\n" \
"v_mul_f32_e32 v176, v55, v176\n" \
"v_mul_f32_e32 v177, v55, v177\n" \
B_"v3, s[80:83], s92 offen offset:3072 lds\n" \
"s_add_u32 m0, s93, 0x1000\n" \
M_"a[112:115], a[120:123], v[84:87], a[112:115]\n" \
"v_mul_f32_e32 v178, v55, v178\n" \
"v_mul_f32_e32 v179, v55, v179\n" \
"v_mul_f32_e32 v180, v55, v180\n" \
"v_mul_f32_e32 v181, v55, v181\n" \
"v_mul_f32_e32 v182, v55, v182\n" \
"v_mul_f32_e32 v183, v55, v183\n" \
C_"v96, v176, v177\n" \
C_"v97, v178, v179\n" \
C_"v98, v180, v181\n" \
C_"v99, v182, v183\n" \
M_"v[188:191], a[60:63], v[120:123], v[188:191]\n" \
"v_mul_f32_e32 v184, v55, v184\n" \
M_"a[112:115], a[120:123], v[96:99], a[112:115]\n" \
"v_mul_f32_e32 v185, v55, v185\n" \
"v_mul_f32_e32 v186, v55, v186\n" \
B_"v4, s[80:83], s92 offen offset:0 lds\n" \
"v_mul_f32_e32 v187, v55, v187\n" \
C_"v100, v184, v185\n" \
C_"v101, v186, v187\n" \
"v_mul_f32_e32 v188, v55, v188\n" \
"v_mul_f32_e32 v189, v55, v189\n" \
"v_mul_f32_e32 v190, v55, v190\n" \
"v_mul_f32_e32 v191, v55, v191\n" \
C_"v102, v188, v189\n" \
C_"v103, v190, v191\n" \
"v_cmp_lt_f32_e64 s[72:73], s21, v62\n" \
B_"v5, s[80:83], s92 offen offset:1024 lds\n" \
M_"a[112:115], a[120:123], v[100:103], a[112:115]\n" \
B_"v6, s[80:83], s92 offen offset:2048 lds\n" \
B_"v7, s[80:83], s92 offen offset:3072 lds\n" \
"s_add_u32 m0, s93, 0x2000\n" \
"s_nop 0\n" \
B_"v8, s[80:83], s92 offen offset:0 lds\n" \
"s_nop 2\n" \
"v_accvgpr_read_b32 v63, a112\n" \
"v_cmp_u_f32_e32 vcc, v63, v63\n" \
"s_nop 0\n" \
"s_or_b64 vcc, s[72:73], vcc\n" \
"s_cbranch_scc1 L_end_%=\n" \
"s_branch L_out_%=\n" \
".fill (L_ab + 24576 - .) / 4, 4, 0xBF800000\n" \
".fill (L_ab + 24576 - .) / 4, 4, 0xBF800000\n" \
"L_ap6:\n" \
"s_setpc_b64 s[88:89]\n" \
"L_gs7:\n" \
M_"v[104:107], a[128:131], a[16:19], v[36:39]\n" \
"s_add_u32 s68, s60, 0xfff6e000\n" \
"s_add_u32 s69, s60, 0xfffb7000\n" \
"s_add_u32 s70, s60, 0x0\n" \
"s_add_u32 s71, s60, 0x49000\n" \
M_"v[104:107], a[132:135], a[20:23], v[104:107]\n" \
"s_xor_b32 m0, s65, 0x12000\n" \
R_"a[160:163], v49 offset:0\n" \
M_"v[104:107], a[136:139], a[24:27], v[104:107]\n" \
B_"v50, s[8:11], s64 offen offset:0 sc1 lds\n" \
M_"v[104:107], a[140:143], a[28:31], v[104:107]\n" \
R_"a[164:167], v49 offset:1024\n" \
R_"a[168:171], v49 offset:2048\n" \
M_"v[108:111], a[156:159], a[28:31], v[36:39]\n" \
R_"a[172:175], v49 offset:3072\n" \
R_"a[176:179], v49 offset:4096\n" \
M_"v[108:111], a[152:155], a[24:27], v[108:111]\n" \
B_"v50, s[8:11], s64 offen offset:1024 sc1 lds\n" \
M_"v[108:111], a[148:151], a[20:23], v[108:111]\n" \
"s_mov_b32 s65, s13\n" \
"v_add_u32_e32 v63, s15, v20\n" \
M_"v[108:111], a[144:147], a[16:19], v[108:111]\n" \
"s_add_u32 s67, s14, 40960\n" \
"v_add_u32_e32 v50, 0x6000, v63\n" \
M_"v[116:119], a[144:147], a[32:35], v[40:43]\n" \
"s_xor_b32 m0, s65, 0x8000\n" \
R_"a[180:183], v49 offset:5120\n" \
M_"v[116:119], a[148:151], a[36:39], v[116:119]\n" \
R_"a[184:187], v49 offset:6144\n" \
"v_and_b32_e32 v68, v12, v68\n" \
M_"v[116:119], a[152:155], a[40:43], v[116:119]\n" \
"v_and_b32_e32 v69, v13, v69\n" \
"v_and_b32_e32 v70, v14, v70\n" \
M_"v[116:119], a[156:159], a[44:47], v[116:119]\n" \
B_"v50, s[8:11], s67 offen offset:0 sc1 lds\n" \
M_"v[112:115], a[140:143], a[44:47], v[40:43]\n" \
"v_and_b32_e32 v71, v15, v71\n" \
R_"a[188:191], v49 offset:7168\n" \
M_"v[112:115], a[136:139], a[40:43], v[112:115]\n" \
E_"v86, v86\n" \
M_"v[112:115], a[132:135], a[36:39], v[112:115]\n" \
E_"v87, v87\n" \
M_"v[112:115], a[128:131], a[32:35], v[112:115]\n" \
E_"v84, v84\n" \
M_"v[120:123], a[128:131], a[48:51], v[44:47]\n" \
B_"v50, s[8:11], s67 offen offset:1024 sc1 lds\n" \
M_"v[120:123], a[132:135], a[52:55], v[120:123]\n" \
E_"v85, v85\n" \
M_"v[120:123], a[136:139], a[56:59], v[120:123]\n" \
C_"v87, v86, v87\n" \
C_"v86, v84, v85\n" \
M_"v[120:123], a[140:143], a[60:63], v[120:123]\n" \
E_"v82, v82\n" \
M_"v[124:127], a[156:159], a[60:63], v[44:47]\n" \
E_"v83, v83\n" \
M_"v[124:127], a[152:155], a[56:59], v[124:127]\n" \
E_"v80, v80\n" \
M_"v[124:127], a[148:151], a[52:55], v[124:127]\n" \
B_"v50, s[8:11], s67 offen offset:2048 sc1 lds\n" \
M_"v[124:127], a[144:147], a[48:51], v[124:127]\n" \
C_"v85, v82, v83\n" \
"s_waitcnt lgkmcnt(1)\n" \
M_"v[128:131], a[160:163], v[68:71], v[128:131]\n" \
E_"v81, v81\n" \
M_"v[160:163], a[160:163], v[72:75], v[160:163]\n" \
C_"v84, v80, v81\n" \
"s_mov_b32 s64, s14\n" \
M_"v[132:135], a[164:167], v[68:71], v[132:135]\n" \
E_"v88, v88\n" \
M_"v[164:167], a[164:167], v[72:75], v[164:167]\n" \
E_"v89, v89\n" \
M_"v[136:139], a[168:171], v[68:71], v[136:139]\n" \
B_"v50, s[8:11], s67 offen offset:3072 sc1 lds\n" \
M_"v[168:171], a[168:171], v[72:75], v[168:171]\n" \
E_"v90, v90\n" \
M_"v[140:143], a[172:175], v[68:71], v[140:143]\n" \
E_"v91, v91\n" \
M_"v[172:175], a[172:175], v[72:75], v[172:175]\n" \
C_"v88, v88, v89\n" \
C_"v89, v90, v91\n" \
M_"v[144:147], a[176:179], v[68:71], v[144:147]\n" \
E_"v92, v92\n" \
M_"v[176:179], a[176:179], v[72:75], v[176:179]\n" \
E_"v93, v93\n" \
M_"v[148:151], a[180:183], v[68:71], v[148:151]\n" \
E_"v94, v94\n" \
M_"v[180:183], a[180:183], v[72:75], v[180:183]\n" \
E_"v95, v95\n" \
M_"v[152:155], a[184:187], v[68:71], v[152:155]\n" \
C_"v90, v92, v93\n" \
C_"v91, v94, v95\n" \
M_"v[184:187], a[184:187], v[72:75], v[184:187]\n" \
"v_add_u32_e32 v51, 0x12000, v63\n" \
"s_waitcnt lgkmcnt(0)\n" \
M_"v[156:159], a[188:191], v[68:71], v[156:159]\n" \
"s_xor_b32 m0, s65, 0xa000\n" \
"ds_read_b32 v58, v18 offset:512\n" \
M_"v[188:191], a[188:191], v[72:75], v[188:191]\n" \
B_"v51, s[8:11], s64 offen offset:0 sc1 lds\n" \
"v_smfmac_f32_16x16x64_bf16 a[96:99], a[104:107], v[68:75], v21\n" \
"ds_read_b32 v59, v18 offset:640\n" \
"ds_read_b32 v60, v18 offset:768\n" \
M_"v[220:223], a[188:191], v[84:87], v[220:223]\n" \
B_"v51, s[8:11], s64 offen offset:1024 sc1 lds\n" \
M_"v[252:255], a[188:191], v[88:91], v[252:255]\n" \
"ds_read_b32 v61, v18 offset:896\n" \
R_"a[128:131], v48 offset:0\n" \
M_"v[216:219], a[184:187], v[84:87], v[216:219]\n" \
R_"a[144:147], v48 offset:4096\n" \
R_"a[132:135], v48 offset:1024\n" \
M_"v[248:251], a[184:187], v[88:91], v[248:251]\n" \
R_"a[148:151], v48 offset:5120\n" \
R_"a[136:139], v48 offset:2048\n" \
M_"v[212:215], a[180:183], v[84:87], v[212:215]\n" \
R_"a[152:155], v48 offset:6144\n" \
R_"a[140:143], v48 offset:3072\n" \
M_"v[244:247], a[180:183], v[88:91], v[244:247]\n" \
R_"a[156:159], v48 offset:7168\n" \
"v_add_u32_e32 v52, 0x14000, v63\n" \
M_"v[208:211], a[176:179], v[84:87], v[208:211]\n" \
B_"v51, s[8:11], s64 offen offset:2048 sc1 lds\n" \
M_"v[240:243], a[176:179], v[88:91], v[240:243]\n" \
E_"v104, v104\n" \
M_"v[204:207], a[172:175], v[84:87], v[204:207]\n" \
E_"v105, v105\n" \
M_"v[236:239], a[172:175], v[88:91], v[236:239]\n" \
B_"v51, s[8:11], s64 offen offset:3072 sc1 lds\n" \
M_"v[200:203], a[168:171], v[84:87], v[200:203]\n" \
E_"v106, v106\n" \
M_"v[232:235], a[168:171], v[88:91], v[232:235]\n" \
E_"v107, v107\n" \
M_"v[196:199], a[164:167], v[84:87], v[196:199]\n" \
C_"v104, v104, v105\n" \
C_"v105, v106, v107\n" \
M_"v[228:231], a[164:167], v[88:91], v[228:231]\n" \
E_"v108, v108\n" \
M_"v[192:195], a[160:163], v[84:87], v[192:195]\n" \
E_"v109, v109\n" \
M_"v[224:227], a[160:163], v[88:91], v[224:227]\n" \
E_"v110, v110\n" \
"v_smfmac_f32_16x16x64_bf16 a[100:103], a[104:107], v[84:91], v21\n" \
E_"v111, v111\n" \
C_"v106, v108, v109\n" \
C_"v107, v110, v111\n" \
"v_and_b32_e32 v104, v12, v104\n" \
"v_and_b32_e32 v105, v13, v105\n" \
"v_and_b32_e32 v106, v14, v106\n" \
"v_and_b32_e32 v107, v15, v107\n" \
"v_accvgpr_read_b32 v54, a96\n" \
"v_accvgpr_read_b32 v22, a97\n" \
"v_readlane_b32 s93, v11, 22\n" \
"v_readlane_b32 s80, v11, 11\n" \
"v_readlane_b32 s81, v11, 12\n" \
"s_mov_b32 s82, 0x47fca0\n" \
"s_mov_b32 s83, 0x20000\n" \
"v_readlane_b32 s92, v11, 29\n" \
E_"v118, v118\n" \
E_"v119, v119\n" \
E_"v116, v116\n" \
E_"v117, v117\n" \
C_"v119, v118, v119\n" \
E_"v114, v114\n" \
E_"v115, v115\n" \
E_"v112, v112\n" \
C_"v118, v116, v117\n" \
"s_waitcnt vmcnt(10) lgkmcnt(0)\n" \
"s_barrier\n" \
M_"v[80:83], a[128:131], a[32:35], v[40:43]\n" \
R_"a[160:163], v49 offset:8192\n" \
R_"a[164:167], v49 offset:9216\n" \
M_"v[80:83], a[132:135], a[36:39], v[80:83]\n" \
R_"a[168:171], v49 offset:10240\n" \
"s_xor_b32 m0, s65, 0x18000\n" \
M_"v[80:83], a[136:139], a[40:43], v[80:83]\n" \
B_"v52, s[8:11], s64 offen offset:0 sc1 lds\n" \
M_"v[80:83], a[140:143], a[44:47], v[80:83]\n" \
R_"a[172:175], v49 offset:11264\n" \
R_"a[176:179], v49 offset:12288\n" \
M_"v[84:87], a[156:159], a[44:47], v[40:43]\n" \
R_"a[180:183], v49 offset:13312\n" \
R_"a[184:187], v49 offset:14336\n" \
M_"v[84:87], a[152:155], a[40:43], v[84:87]\n" \
B_"v52, s[8:11], s64 offen offset:1024 sc1 lds\n" \
M_"v[84:87], a[148:151], a[36:39], v[84:87]\n" \
R_"a[188:191], v49 offset:15360\n" \
C_"v117, v114, v115\n" \
M_"v[84:87], a[144:147], a[32:35], v[84:87]\n" \
B_"v52, s[8:11], s64 offen offset:2048 sc1 lds\n" \
M_"v[92:95], a[144:147], a[48:51], v[44:47]\n" \
E_"v113, v113\n" \
M_"v[92:95], a[148:151], a[52:55], v[92:95]\n" \
B_"v52, s[8:11], s64 offen offset:3072 sc1 lds\n" \
M_"v[92:95], a[152:155], a[56:59], v[92:95]\n" \
"s_add_u32 m0, s93, 0x0\n" \
C_"v116, v112, v113\n" \
M_"v[92:95], a[156:159], a[60:63], v[92:95]\n" \
E_"v120, v120\n" \
M_"v[88:91], a[140:143], a[60:63], v[44:47]\n" \
B_"v0, s[80:83], s92 offen offset:0 lds\n" \
M_"v[88:91], a[136:139], a[56:59], v[88:91]\n" \
E_"v121, v121\n" \
M_"v[88:91], a[132:135], a[52:55], v[88:91]\n" \
E_"v122, v122\n" \
M_"v[88:91], a[128:131], a[48:51], v[88:91]\n" \
B_"v1, s[80:83], s92 offen offset:1024 lds\n" \
M_"a[96:99], a[116:119], v[104:107], a[96:99]\n" \
C_"v120, v120, v121\n" \
"s_waitcnt lgkmcnt(5)\n" \
M_"v[160:163], a[160:163], v[104:107], v[160:163]\n" \
E_"v123, v123\n" \
M_"v[164:167], a[164:167], v[104:107], v[164:167]\n" \
B_"v2, s[80:83], s92 offen offset:2048 lds\n" \
M_"v[168:171], a[168:171], v[104:107], v[168:171]\n" \
C_"v121, v122, v123\n" \
"s_waitcnt lgkmcnt(1)\n" \
M_"v[172:175], a[172:175], v[104:107], v[172:175]\n" \
E_"v124, v124\n" \
M_"v[176:179], a[176:179], v[104:107], v[176:179]\n" \
E_"v125, v125\n" \
M_"v[180:183], a[180:183], v[104:107], v[180:183]\n" \
E_"v126, v126\n" \
M_"v[184:187], a[184:187], v[104:107], v[184:187]\n" \
C_"v122, v124, v125\n" \
"s_waitcnt lgkmcnt(0)\n" \
M_"v[188:191], a[188:191], v[104:107], v[188:191]\n" \
E_"v127, v127\n" \
M_"v[220:223], a[188:191], v[116:119], v[220:223]\n" \
C_"v123, v126, v127\n" \
B_"v3, s[80:83], s92 offen offset:3072 lds\n" \
"s_add_u32 m0, s93, 0x1000\n" \
M_"v[252:255], a[188:191], v[120:123], v[252:255]\n" \
R_"a[128:131], v48 offset:8192\n" \
R_"a[144:147], v48 offset:12288\n" \
M_"v[216:219], a[184:187], v[116:119], v[216:219]\n" \
B_"v4, s[80:83], s92 offen offset:0 lds\n" \
M_"v[248:251], a[184:187], v[120:123], v[248:251]\n" \
R_"a[132:135], v48 offset:9216\n" \
R_"a[148:151], v48 offset:13312\n" \
M_"v[212:215], a[180:183], v[116:119], v[212:215]\n" \
B_"v5, s[80:83], s92 offen offset:1024 lds\n" \
M_"v[244:247], a[180:183], v[120:123], v[244:247]\n" \
R_"a[136:139], v48 offset:10240\n" \
R_"a[152:155], v48 offset:14336\n" \
M_"v[208:211], a[176:179], v[116:119], v[208:211]\n" \
B_"v6, s[80:83], s92 offen offset:2048 lds\n" \
M_"v[240:243], a[176:179], v[120:123], v[240:243]\n" \
R_"a[140:143], v48 offset:11264\n" \
R_"a[156:159], v48 offset:15360\n" \
M_"v[204:207], a[172:175], v[116:119], v[204:207]\n" \
B_"v7, s[80:83], s92 offen offset:3072 lds\n" \
M_"v[236:239], a[172:175], v[120:123], v[236:239]\n" \
"v_readlane_b32 s96, v11, 1\n" \
"v_lshrrev_b32_e32 v16, 1, v20\n" \
M_"v[200:203], a[168:171], v[116:119], v[200:203]\n" \
"v_add_f32_e32 v54, v54, v22\n" \
"v_max_f32_e32 v62, v62, v54\n" \
M_"v[232:235], a[168:171], v[120:123], v[232:235]\n" \
"v_rcp_f32_e32 v54, v54\n" \
M_"v[196:199], a[164:167], v[116:119], v[196:199]\n" \
"v_mul_f32_e32 v54, v54, v58\n" \
"v_mul_f32_e32 v128, v54, v128\n" \
M_"v[228:231], a[164:167], v[120:123], v[228:231]\n" \
"v_mul_f32_e32 v129, v54, v129\n" \
"v_mul_f32_e32 v136, v54, v136\n" \
M_"v[192:195], a[160:163], v[116:119], v[192:195]\n" \
"v_mul_f32_e32 v137, v54, v137\n" \
"v_mul_f32_e32 v144, v54, v144\n" \
M_"v[224:227], a[160:163], v[120:123], v[224:227]\n" \
"v_mul_f32_e32 v145, v54, v145\n" \
"v_mul_f32_e32 v152, v54, v152\n" \
"v_smfmac_f32_16x16x64_bf16 a[100:103], a[104:107], v[116:123], v21\n" \
"v_mul_f32_e32 v153, v54, v153\n" \
C_"v128, v128, v129\n" \
"v_mul_f32_e32 v130, v54, v130\n" \
"v_mul_f32_e32 v131, v54, v131\n" \
C_"v136, v136, v137\n" \
"v_mul_f32_e32 v138, v54, v138\n" \
"v_mul_f32_e32 v139, v54, v139\n" \
C_"v144, v144, v145\n" \
"v_mul_f32_e32 v146, v54, v146\n" \
"v_mul_f32_e32 v147, v54, v147\n" \
C_"v152, v152, v153\n" \
"v_mul_f32_e32 v154, v54, v154\n" \
"v_mul_f32_e32 v155, v54, v155\n" \
C_"v129, v130, v131\n" \
"v_mul_f32_e32 v132, v54, v132\n" \
"v_mul_f32_e32 v133, v54, v133\n" \
"v_mul_f32_e32 v134, v54, v134\n" \
"v_mul_f32_e32 v135, v54, v135\n" \
C_"v137, v138, v139\n" \
"v_mul_f32_e32 v140, v54, v140\n" \
"v_mul_f32_e32 v141, v54, v141\n" \
"v_mul_f32_e32 v142, v54, v142\n" \
"v_mul_f32_e32 v143, v54, v143\n" \
C_"v145, v146, v147\n" \
"v_mul_f32_e32 v148, v54, v148\n" \
"v_mul_f32_e32 v149, v54, v149\n" \
"v_mul_f32_e32 v150, v54, v150\n" \
"v_mul_f32_e32 v151, v54, v151\n" \
C_"v153, v154, v155\n" \
"v_mul_f32_e32 v156, v54, v156\n" \
"v_mul_f32_e32 v157, v54, v157\n" \
"v_mul_f32_e32 v158, v54, v158\n" \
"v_mul_f32_e32 v159, v54, v159\n" \
C_"v130, v132, v133\n" \
C_"v131, v134, v135\n" \
"buffer_store_dwordx4 v[128:131], v20, s[56:59], s68 offen offset:0 sc1\n" \
C_"v138, v140, v141\n" \
C_"v139, v142, v143\n" \
C_"v146, v148, v149\n" \
C_"v147, v150, v151\n" \
C_"v154, v156, v157\n" \
C_"v155, v158, v159\n" \
"buffer_store_dwordx4 v[136:139], v20, s[56:59], s68 offen offset:1024 sc1\n" \
"s_add_u32 m0, s93, 0x2000\n" \
"v_add_u32_e32 v49, 0x10000, v48\n" \
"s_xor_b32 s65, s65, 0x8000\n" \
"s_add_u32 s64, s64, 0x10000\n" \
E_"v86, v86\n" \
"buffer_store_dwordx4 v[144:147], v20, s[56:59], s68 offen offset:2048 sc1\n" \
E_"v87, v87\n" \
E_"v84, v84\n" \
E_"v85, v85\n" \
"buffer_store_dwordx4 v[152:155], v20, s[56:59], s68 offen offset:3072 sc1\n" \
E_"v88, v88\n" \
E_"v89, v89\n" \
C_"v87, v86, v87\n" \
E_"v82, v82\n" \
B_"v8, s[80:83], s92 offen offset:0 lds\n" \
E_"v83, v83\n" \
E_"v80, v80\n" \
E_"v81, v81\n" \
E_"v90, v90\n" \
E_"v91, v91\n" \
C_"v86, v84, v85\n" \
C_"v88, v88, v89\n" \
E_"v92, v92\n" \
E_"v93, v93\n" \
E_"v94, v94\n" \
E_"v95, v95\n" \
C_"v85, v82, v83\n" \
C_"v84, v80, v81\n" \
C_"v89, v90, v91\n" \
"v_and_b32_e32 v84, v12, v84\n" \
"v_and_b32_e32 v85, v13, v85\n" \
"v_and_b32_e32 v86, v14, v86\n" \
"s_waitcnt vmcnt(17) lgkmcnt(0)\n" \
"s_barrier\n" \
M_"v[120:123], a[128:131], a[48:51], v[44:47]\n" \
"buffer_load_dwordx2 v[26:27], v16, s[56:59], s96 offen sc1\n" \
M_"v[120:123], a[132:135], a[52:55], v[120:123]\n" \
R_"a[188:191], v49 offset:7168\n" \
R_"a[184:187], v49 offset:6144\n" \
M_"v[120:123], a[136:139], a[56:59], v[120:123]\n" \
"s_xor_b32 m0, s65, 0x12000\n" \
R_"a[180:183], v49 offset:5120\n" \
M_"v[120:123], a[140:143], a[60:63], v[120:123]\n" \
B_"v50, s[8:11], s64 offen offset:0 sc1 lds\n" \
M_"v[124:127], a[156:159], a[60:63], v[44:47]\n" \
R_"a[176:179], v49 offset:4096\n" \
R_"a[172:175], v49 offset:3072\n" \
M_"v[124:127], a[152:155], a[56:59], v[124:127]\n" \
B_"v50, s[8:11], s64 offen offset:1024 sc1 lds\n" \
M_"v[124:127], a[148:151], a[52:55], v[124:127]\n" \
R_"a[168:171], v49 offset:2048\n" \
"v_and_b32_e32 v87, v15, v87\n" \
M_"v[124:127], a[144:147], a[48:51], v[124:127]\n" \
C_"v90, v92, v93\n" \
C_"v91, v94, v95\n" \
M_"v[32:35], a[124:127], a[64:67], 0\n" \
B_"v50, s[8:11], s64 offen offset:2048 sc1 lds\n" \
M_"v[36:39], a[124:127], a[80:83], 0\n" \
R_"a[164:167], v49 offset:1024\n" \
R_"a[160:163], v49 offset:0\n" \
M_"v[32:35], a[224:227], a[68:71], v[32:35]\n" \
R_"a[0:3], v49 offset:8192\n" \
R_"a[4:7], v49 offset:9216\n" \
M_"v[36:39], a[224:227], a[84:87], v[36:39]\n" \
R_"a[8:11], v49 offset:10240\n" \
R_"a[12:15], v49 offset:11264\n" \
M_"v[32:35], a[228:231], a[72:75], v[32:35]\n" \
B_"v50, s[8:11], s64 offen offset:3072 sc1 lds\n" \
M_"v[36:39], a[228:231], a[88:91], v[36:39]\n" \
R_"a[16:19], v49 offset:12288\n" \
R_"a[20:23], v49 offset:13312\n" \
M_"v[32:35], a[232:235], a[76:79], v[32:35]\n" \
R_"a[24:27], v49 offset:14336\n" \
R_"a[28:31], v49 offset:15360\n" \
M_"v[36:39], a[232:235], a[92:95], v[36:39]\n" \
E_"v120, v120\n" \
"v_smfmac_f32_16x16x64_bf16 a[100:103], a[104:107], v[84:91], v21\n" \
"s_mov_b32 s63, s62\n" \
"s_waitcnt lgkmcnt(13)\n" \
M_"v[220:223], a[188:191], v[84:87], v[220:223]\n" \
E_"v121, v121\n" \
M_"v[252:255], a[188:191], v[88:91], v[252:255]\n" \
E_"v122, v122\n" \
M_"v[216:219], a[184:187], v[84:87], v[216:219]\n" \
E_"v123, v123\n" \
M_"v[248:251], a[184:187], v[88:91], v[248:251]\n" \
C_"v120, v120, v121\n" \
C_"v121, v122, v123\n" \
M_"v[212:215], a[180:183], v[84:87], v[212:215]\n" \
E_"v124, v124\n" \
M_"v[244:247], a[180:183], v[88:91], v[244:247]\n" \
"v_and_b32_e32 v120, v12, v120\n" \
"s_waitcnt lgkmcnt(10)\n" \
M_"v[208:211], a[176:179], v[84:87], v[208:211]\n" \
E_"v125, v125\n" \
M_"v[240:243], a[176:179], v[88:91], v[240:243]\n" \
E_"v126, v126\n" \
M_"v[204:207], a[172:175], v[84:87], v[204:207]\n" \
E_"v127, v127\n" \
M_"v[236:239], a[172:175], v[88:91], v[236:239]\n" \
C_"v122, v124, v125\n" \
C_"v123, v126, v127\n" \
M_"v[200:203], a[168:171], v[84:87], v[200:203]\n" \
"v_and_b32_e32 v121, v13, v121\n" \
"v_and_b32_e32 v122, v14, v122\n" \
M_"v[232:235], a[168:171], v[88:91], v[232:235]\n" \
"v_and_b32_e32 v123, v15, v123\n" \
"s_waitcnt lgkmcnt(0)\n" \
M_"v[196:199], a[164:167], v[84:87], v[196:199]\n" \
"v_accvgpr_read_b32 v56, a100\n" \
"v_accvgpr_read_b32 v24, a101\n" \
M_"v[228:231], a[164:167], v[88:91], v[228:231]\n" \
"v_readlane_b32 s97, v11, 39\n" \
"v_add_u32_e32 v17, 0x49000, v20\n" \
M_"v[192:195], a[160:163], v[84:87], v[192:195]\n" \
"v_readlane_b32 s98, v11, 40\n" \
"v_add_u32_e32 v48, s16, v20\n" \
M_"v[224:227], a[160:163], v[88:91], v[224:227]\n" \
R_"a[128:131], v48 offset:0\n" \
R_"a[144:147], v48 offset:4096\n" \
M_"a[100:103], a[116:119], v[120:123], a[100:103]\n" \
R_"a[132:135], v48 offset:1024\n" \
R_"a[148:151], v48 offset:5120\n" \
M_"v[224:227], a[0:3], v[120:123], v[224:227]\n" \
R_"a[136:139], v48 offset:2048\n" \
R_"a[152:155], v48 offset:6144\n" \
M_"v[228:231], a[4:7], v[120:123], v[228:231]\n" \
R_"a[140:143], v48 offset:3072\n" \
R_"a[156:159], v48 offset:7168\n" \
M_"v[232:235], a[8:11], v[120:123], v[232:235]\n" \
"v_accvgpr_read_b32 v55, a98\n" \
"v_accvgpr_read_b32 v23, a99\n" \
M_"a[96:99], a[120:123], a[120:123], 0\n" \
"v_add_f32_e32 v55, v55, v23\n" \
"v_max_f32_e32 v62, v62, v55\n" \
M_"v[236:239], a[12:15], v[120:123], v[236:239]\n" \
"v_rcp_f32_e32 v55, v55\n" \
M_"v[240:243], a[16:19], v[120:123], v[240:243]\n" \
"v_mul_f32_e32 v55, v55, v59\n" \
"v_mul_f32_e32 v160, v55, v160\n" \
M_"v[244:247], a[20:23], v[120:123], v[244:247]\n" \
"v_mul_f32_e32 v161, v55, v161\n" \
"v_mul_f32_e32 v168, v55, v168\n" \
M_"v[248:251], a[24:27], v[120:123], v[248:251]\n" \
"v_mul_f32_e32 v169, v55, v169\n" \
"v_mul_f32_e32 v176, v55, v176\n" \
M_"v[252:255], a[28:31], v[120:123], v[252:255]\n" \
"v_mul_f32_e32 v177, v55, v177\n" \
"v_mul_f32_e32 v184, v55, v184\n" \
"v_mul_f32_e32 v185, v55, v185\n" \
C_"v160, v160, v161\n" \
"v_mul_f32_e32 v162, v55, v162\n" \
"v_mul_f32_e32 v163, v55, v163\n" \
C_"v168, v168, v169\n" \
"v_mul_f32_e32 v170, v55, v170\n" \
"v_mul_f32_e32 v171, v55, v171\n" \
"s_waitcnt vmcnt(4)\n" \
"v_cmp_ne_u64_e32 vcc, s[62:63], v[26:27]\n" \
C_"v176, v176, v177\n" \
"v_mul_f32_e32 v178, v55, v178\n" \
"v_mul_f32_e32 v179, v55, v179\n" \
C_"v184, v184, v185\n" \
"v_mul_f32_e32 v186, v55, v186\n" \
"s_cbranch_vccnz L_sos_%=\n" \
"L_ros_%=:\n" \
B_"a[124:127], v20, s[56:59], s97 offen offset:0 sc1\n" \
"v_mul_f32_e32 v187, v55, v187\n" \
C_"v161, v162, v163\n" \
"v_mul_f32_e32 v164, v55, v164\n" \
"v_mul_f32_e32 v165, v55, v165\n" \
"v_mul_f32_e32 v166, v55, v166\n" \
"v_mul_f32_e32 v167, v55, v167\n" \
B_"a[228:231], v17, s[56:59], s97 offen offset:0 sc1\n" \
C_"v169, v170, v171\n" \
"v_mul_f32_e32 v172, v55, v172\n" \
"v_mul_f32_e32 v173, v55, v173\n" \
"v_mul_f32_e32 v174, v55, v174\n" \
"v_mul_f32_e32 v175, v55, v175\n" \
C_"v177, v178, v179\n" \
B_"a[236:239], v20, s[56:59], s98 offen offset:0 sc1\n" \
"v_mul_f32_e32 v180, v55, v180\n" \
"v_mul_f32_e32 v181, v55, v181\n" \
"v_mul_f32_e32 v182, v55, v182\n" \
"v_mul_f32_e32 v183, v55, v183\n" \
C_"v185, v186, v187\n" \
"v_mul_f32_e32 v188, v55, v188\n" \
B_"a[244:247], v17, s[56:59], s98 offen offset:0 sc1\n" \
"v_mul_f32_e32 v189, v55, v189\n" \
"v_mul_f32_e32 v190, v55, v190\n" \
"v_mul_f32_e32 v191, v55, v191\n" \
C_"v162, v164, v165\n" \
C_"v163, v166, v167\n" \
C_"v170, v172, v173\n" \
"buffer_store_dwordx4 v[160:163], v20, s[56:59], s69 offen offset:0 sc1\n" \
C_"v171, v174, v175\n" \
C_"v178, v180, v181\n" \
C_"v179, v182, v183\n" \
C_"v186, v188, v189\n" \
C_"v187, v190, v191\n" \
"v_readlane_b32 s93, v11, 23\n" \
"buffer_store_dwordx4 v[168:171], v20, s[56:59], s69 offen offset:1024 sc1\n" \
"v_readlane_b32 s84, v11, 15\n" \
"v_readlane_b32 s85, v11, 16\n" \
"v_readlane_b32 s86, v11, 17\n" \
"s_mov_b32 s87, 0x20000\n" \
"v_readlane_b32 s92, v11, 18\n" \
"v_readlane_b32 s91, v11, 24\n" \
"buffer_store_dwordx4 v[176:179], v20, s[56:59], s69 offen offset:2048 sc1\n" \
M_"a[112:115], a[120:123], v[128:131], a[112:115]\n" \
M_"a[112:115], a[120:123], v[136:139], a[112:115]\n" \
"buffer_store_dwordx4 v[184:187], v20, s[56:59], s69 offen offset:3072 sc1\n" \
"v_mfma_f32_32x32x16_bf16 v[128:143], a[120:123], a[120:123], 0\n" \
M_"a[112:115], a[120:123], v[144:147], a[112:115]\n" \
B_"a[224:227], v20, s[56:59], s97 offen offset:1024 sc1\n" \
M_"a[112:115], a[120:123], v[152:155], a[112:115]\n" \
"v_mfma_f32_32x32x16_bf16 v[144:159], a[120:123], a[120:123], 0\n" \
B_"a[232:235], v17, s[56:59], s97 offen offset:1024 sc1\n" \
"v_add_f32_e32 v56, v56, v24\n" \
M_"a[112:115], a[120:123], v[160:163], a[112:115]\n" \
B_"a[240:243], v20, s[56:59], s98 offen offset:1024 sc1\n" \
"v_max_f32_e32 v62, v62, v56\n" \
"v_rcp_f32_e32 v56, v56\n" \
M_"a[112:115], a[120:123], v[168:171], a[112:115]\n" \
"v_mfma_f32_32x32x16_bf16 v[160:175], a[120:123], a[120:123], 0\n" \
B_"a[248:251], v17, s[56:59], s98 offen offset:1024 sc1\n" \
"v_mul_f32_e32 v56, v56, v60\n" \
M_"a[112:115], a[120:123], v[176:179], a[112:115]\n" \
"v_mul_f32_e32 v192, v56, v192\n" \
"v_mul_f32_e32 v193, v56, v193\n" \
"s_waitcnt vmcnt(17) lgkmcnt(0)\n" \
"s_barrier\n" \
M_"v[64:67], a[128:131], a[64:67], v[32:35]\n" \
"s_add_u32 m0, s93, 0x0\n" \
C_"v192, v192, v193\n" \
M_"v[64:67], a[132:135], a[68:71], v[64:67]\n" \
B_"v0, s[84:87], s92 offen offset:0 lds\n" \
M_"v[64:67], a[136:139], a[72:75], v[64:67]\n" \
"v_mul_f32_e32 v194, v56, v194\n" \
"v_mul_f32_e32 v195, v56, v195\n" \
M_"v[64:67], a[140:143], a[76:79], v[64:67]\n" \
B_"v1, s[84:87], s92 offen offset:1024 lds\n" \
M_"v[68:71], a[156:159], a[76:79], v[32:35]\n" \
M_"a[112:115], a[120:123], v[184:187], a[112:115]\n" \
"v_mfma_f32_32x32x16_bf16 v[176:191], a[120:123], a[120:123], 0\n" \
B_"v2, s[84:87], s92 offen offset:2048 lds\n" \
C_"v193, v194, v195\n" \
"v_mul_f32_e32 v196, v56, v196\n" \
"v_mul_f32_e32 v197, v56, v197\n" \
"v_mul_f32_e32 v198, v56, v198\n" \
M_"v[68:71], a[152:155], a[72:75], v[68:71]\n" \
B_"v3, s[84:87], s92 offen offset:3072 lds\n" \
M_"v[68:71], a[148:151], a[68:71], v[68:71]\n" \
"s_add_u32 m0, s91, 0x1000\n" \
"v_mul_f32_e32 v199, v56, v199\n" \
M_"v[68:71], a[144:147], a[64:67], v[68:71]\n" \
B_"v4, s[84:87], s92 offen offset:0 lds\n" \
M_"v[76:79], a[144:147], a[80:83], v[36:39]\n" \
R_"a[144:147], v48 offset:12288\n" \
"v_mul_f32_e32 v200, v56, v200\n" \
M_"v[76:79], a[148:151], a[84:87], v[76:79]\n" \
B_"v5, s[84:87], s92 offen offset:1024 lds\n" \
M_"v[76:79], a[152:155], a[88:91], v[76:79]\n" \
R_"a[148:151], v48 offset:13312\n" \
R_"a[152:155], v48 offset:14336\n" \
M_"v[76:79], a[156:159], a[92:95], v[76:79]\n" \
B_"v6, s[84:87], s92 offen offset:2048 lds\n" \
M_"v[72:75], a[140:143], a[92:95], v[36:39]\n" \
R_"a[140:143], v48 offset:11264\n" \
R_"a[156:159], v48 offset:15360\n" \
M_"v[72:75], a[136:139], a[88:91], v[72:75]\n" \
B_"v7, s[84:87], s92 offen offset:3072 lds\n" \
M_"v[72:75], a[132:135], a[84:87], v[72:75]\n" \
R_"a[132:135], v48 offset:9216\n" \
R_"a[136:139], v48 offset:10240\n" \
M_"v[72:75], a[128:131], a[80:83], v[72:75]\n" \
R_"a[128:131], v48 offset:8192\n" \
"v_mul_f32_e32 v201, v56, v201\n" \
"v_accvgpr_read_b32 v57, a102\n" \
"v_accvgpr_read_b32 v25, a103\n" \
M_"a[100:103], a[120:123], a[120:123], 0\n" \
C_"v194, v196, v197\n" \
C_"v195, v198, v199\n" \
"buffer_store_dwordx4 v[192:195], v20, s[56:59], s70 offen offset:0 sc1\n" \
C_"v200, v200, v201\n" \
"v_mul_f32_e32 v202, v56, v202\n" \
"v_mul_f32_e32 v203, v56, v203\n" \
"v_add_f32_e32 v57, v57, v25\n" \
M_"a[112:115], a[120:123], v[192:195], a[112:115]\n" \
C_"v201, v202, v203\n" \
"v_mul_f32_e32 v204, v56, v204\n" \
"v_mul_f32_e32 v205, v56, v205\n" \
"v_mul_f32_e32 v206, v56, v206\n" \
"v_mul_f32_e32 v207, v56, v207\n" \
"v_mul_f32_e32 v208, v56, v208\n" \
"v_mul_f32_e32 v209, v56, v209\n" \
"v_max_f32_e32 v62, v62, v57\n" \
C_"v202, v204, v205\n" \
C_"v203, v206, v207\n" \
"buffer_store_dwordx4 v[200:203], v20, s[56:59], s70 offen offset:1024 sc1\n" \
".fill (L_ab + 28672 - .) / 4, 4, 0xBF800000\n" \
"L_ap7:\n" \
"s_cbranch_execz L_ap0\n" \
C_"v208, v208, v209\n" \
"v_mul_f32_e32 v210, v56, v210\n" \
"v_mul_f32_e32 v211, v56, v211\n" \
"v_rcp_f32_e32 v57, v57\n" \
M_"a[112:115], a[120:123], v[200:203], a[112:115]\n" \
"v_mfma_f32_32x32x16_bf16 v[192:207], a[120:123], a[120:123], 0\n" \
C_"v209, v210, v211\n" \
"v_mul_f32_e32 v212, v56, v212\n" \
"v_mul_f32_e32 v213, v56, v213\n" \
"v_mul_f32_e32 v214, v56, v214\n" \
"v_mul_f32_e32 v215, v56, v215\n" \
"v_mul_f32_e32 v216, v56, v216\n" \
"v_mul_f32_e32 v217, v56, v217\n" \
C_"v210, v212, v213\n" \
C_"v211, v214, v215\n" \
"buffer_store_dwordx4 v[208:211], v20, s[56:59], s70 offen offset:2048 sc1\n" \
C_"v216, v216, v217\n" \
"v_mul_f32_e32 v218, v56, v218\n" \
"v_mul_f32_e32 v219, v56, v219\n" \
"v_mul_f32_e32 v57, v57, v61\n" \
M_"a[112:115], a[120:123], v[208:211], a[112:115]\n" \
C_"v217, v218, v219\n" \
"v_mul_f32_e32 v220, v56, v220\n" \
"v_mul_f32_e32 v221, v56, v221\n" \
"v_mul_f32_e32 v222, v56, v222\n" \
"v_mul_f32_e32 v223, v56, v223\n" \
"v_mul_f32_e32 v224, v57, v224\n" \
"v_mul_f32_e32 v225, v57, v225\n" \
C_"v218, v220, v221\n" \
C_"v219, v222, v223\n" \
"buffer_store_dwordx4 v[216:219], v20, s[56:59], s70 offen offset:3072 sc1\n" \
C_"v224, v224, v225\n" \
"v_mul_f32_e32 v226, v57, v226\n" \
"v_mul_f32_e32 v227, v57, v227\n" \
M_"a[112:115], a[120:123], v[216:219], a[112:115]\n" \
"v_mfma_f32_32x32x16_bf16 v[208:223], a[120:123], a[120:123], 0\n" \
C_"v225, v226, v227\n" \
"v_mul_f32_e32 v228, v57, v228\n" \
"v_mul_f32_e32 v229, v57, v229\n" \
"v_mul_f32_e32 v230, v57, v230\n" \
"v_mul_f32_e32 v231, v57, v231\n" \
"v_mul_f32_e32 v232, v57, v232\n" \
"v_mul_f32_e32 v233, v57, v233\n" \
C_"v226, v228, v229\n" \
C_"v227, v230, v231\n" \
"buffer_store_dwordx4 v[224:227], v20, s[56:59], s71 offen offset:0 sc1\n" \
C_"v232, v232, v233\n" \
"v_mul_f32_e32 v234, v57, v234\n" \
"v_mul_f32_e32 v235, v57, v235\n" \
M_"a[112:115], a[120:123], v[224:227], a[112:115]\n" \
C_"v233, v234, v235\n" \
"v_mul_f32_e32 v236, v57, v236\n" \
"v_mul_f32_e32 v237, v57, v237\n" \
"v_mul_f32_e32 v238, v57, v238\n" \
"v_mul_f32_e32 v239, v57, v239\n" \
"v_mul_f32_e32 v240, v57, v240\n" \
"v_mul_f32_e32 v241, v57, v241\n" \
C_"v234, v236, v237\n" \
C_"v235, v238, v239\n" \
"buffer_store_dwordx4 v[232:235], v20, s[56:59], s71 offen offset:1024 sc1\n" \
C_"v240, v240, v241\n" \
"v_mul_f32_e32 v242, v57, v242\n" \
"v_mul_f32_e32 v243, v57, v243\n" \
M_"a[112:115], a[120:123], v[232:235], a[112:115]\n" \
"v_mfma_f32_32x32x16_bf16 v[224:239], a[120:123], a[120:123], 0\n" \
C_"v241, v242, v243\n" \
"v_mul_f32_e32 v244, v57, v244\n" \
"v_mul_f32_e32 v245, v57, v245\n" \
"v_mul_f32_e32 v246, v57, v246\n" \
"v_mul_f32_e32 v247, v57, v247\n" \
"v_mul_f32_e32 v248, v57, v248\n" \
"v_mul_f32_e32 v249, v57, v249\n" \
C_"v242, v244, v245\n" \
C_"v243, v246, v247\n" \
"buffer_store_dwordx4 v[240:243], v20, s[56:59], s71 offen offset:2048 sc1\n" \
C_"v248, v248, v249\n" \
"v_mul_f32_e32 v250, v57, v250\n" \
"v_mul_f32_e32 v251, v57, v251\n" \
M_"a[112:115], a[120:123], v[240:243], a[112:115]\n" \
C_"v249, v250, v251\n" \
"v_mul_f32_e32 v252, v57, v252\n" \
"v_mul_f32_e32 v253, v57, v253\n" \
"v_mul_f32_e32 v254, v57, v254\n" \
"v_mul_f32_e32 v255, v57, v255\n" \
C_"v250, v252, v253\n" \
C_"v251, v254, v255\n" \
"buffer_store_dwordx4 v[248:251], v20, s[56:59], s71 offen offset:3072 sc1\n" \
"s_add_u32 m0, s91, 0x2000\n" \
M_"a[112:115], a[120:123], v[248:251], a[112:115]\n" \
"v_mfma_f32_32x32x16_bf16 v[240:255], a[120:123], a[120:123], 0\n" \
"v_add_u32_e32 v53, 0x20000, v63\n" \
B_"v8, s[84:87], s92 offen offset:0 lds\n" \
E_"v70, v70\n" \
E_"v71, v71\n" \
E_"v72, v72\n" \
E_"v73, v73\n" \
"s_add_u32 s64, s64, 0x10000\n" \
"v_accvgpr_read_b32 v63, a112\n" \
"v_cmp_u_f32_e32 vcc, v63, v63\n" \
"v_cmp_lt_f32_e64 s[72:73], s21, v62\n" \
"v_readlane_b32 s95, v11, 3\n" \
E_"v68, v68\n" \
E_"v69, v69\n" \
E_"v74, v74\n" \
E_"v75, v75\n" \
"s_or_b64 vcc, s[72:73], vcc\n" \
"s_cselect_b32 s95, 0x40000000, s95\n" \
"s_cselect_b32 s94, 0x7f800000, 0\n" \
C_"v71, v70, v71\n" \
E_"v66, v66\n" \
E_"v67, v67\n" \
E_"v64, v64\n" \
E_"v65, v65\n" \
C_"v72, v72, v73\n" \
E_"v76, v76\n" \
E_"v77, v77\n" \
E_"v78, v78\n" \
E_"v79, v79\n" \
C_"v70, v68, v69\n" \
"s_waitcnt vmcnt(17) lgkmcnt(0)\n" \
"s_barrier\n" \
C_"v73, v74, v75\n" \
"v_add_u32_e32 v49, 0x10000, v48\n" \
"s_xor_b32 s65, s65, 0x8000\n" \
C_"v69, v66, v67\n" \
C_"v68, v64, v65\n" \
C_"v74, v76, v77\n" \
C_"v75, v78, v79\n" \
"v_xor_b32_e32 v48, 0x8000, v48\n" \
"s_sub_u32 s66, s19, 2\n" \
"s_branch L_f1_%=\n" \
".fill (L_ab + 32768 - .) / 4, 4, 0xBF800000\n" \
".fill (L_ab + 32768 - .) / 4, 4, 0xBF800000\n" \
"L_ap8:\n" \
"s_setpc_b64 s[88:89]\n" \
"L_out_%=:\n" \
"v_bfe_u32 v38, v20, 2, 4\n" \
"v_readlane_b32 s64, v11, 2\n" \
"v_add_u32_e32 v32, 0x49000, v20\n" \
"v_add_u32_e32 v34, 0x20000, v10\n" \
X_"a[0:1], v34 offset:0\n" \
X_"a[2:3], v34 offset:4608\n" \
X_"a[4:5], v34 offset:9216\n" \
"buffer_load_dword v26, v38, s[56:59], s64 offen sc1\n" \
X_"a[6:7], v34 offset:13824\n" \
X_"a[8:9], v34 offset:32\n" \
X_"a[10:11], v34 offset:4640\n" \
X_"a[12:13], v34 offset:9248\n" \
"buffer_store_dwordx4 v[64:67], v20, s[56:59], s61 offen offset:0 sc1\n" \
X_"a[14:15], v34 offset:13856\n" \
"v_mov_b32_e32 v39, s62\n" \
"v_readlane_b32 s70, v11, 5\n" \
"v_readlane_b32 s71, v11, 4\n" \
"buffer_store_dwordx4 v[68:71], v20, s[56:59], s61 offen offset:1024 sc1\n" \
X_"a[16:17], v34 offset:64\n" \
X_"a[18:19], v34 offset:4672\n" \
X_"a[20:21], v34 offset:9280\n" \
X_"a[22:23], v34 offset:13888\n" \
"buffer_store_dwordx4 v[72:75], v20, s[56:59], s61 offen offset:2048 sc1\n" \
X_"a[24:25], v34 offset:96\n" \
X_"a[26:27], v34 offset:4704\n" \
X_"a[28:29], v34 offset:9312\n" \
X_"a[30:31], v34 offset:13920\n" \
"buffer_store_dwordx4 v[76:79], v20, s[56:59], s61 offen offset:3072 sc1\n" \
X_"a[96:97], v34 offset:128\n" \
X_"a[98:99], v34 offset:4736\n" \
X_"a[100:101], v34 offset:9344\n" \
X_"a[102:103], v34 offset:13952\n" \
"buffer_store_dwordx4 v[80:83], v32, s[56:59], s61 offen offset:0 sc1\n" \
X_"a[104:105], v34 offset:160\n" \
X_"a[106:107], v34 offset:4768\n" \
X_"a[108:109], v34 offset:9376\n" \
X_"a[110:111], v34 offset:13984\n" \
"buffer_store_dwordx4 v[84:87], v32, s[56:59], s61 offen offset:1024 sc1\n" \
X_"a[112:113], v34 offset:192\n" \
"s_waitcnt vmcnt(16) lgkmcnt(15)\n" \
M_"v[112:115], a[0:3], a[192:195], 0\n" \
"buffer_store_dwordx4 v[96:99], v32, s[56:59], s61 offen offset:2048 sc1\n" \
M_"v[112:115], a[4:7], a[196:199], v[112:115]\n" \
X_"a[114:115], v34 offset:4800\n" \
X_"a[116:117], v34 offset:9408\n" \
"buffer_store_dwordx4 v[100:103], v32, s[56:59], s61 offen offset:3072 sc1\n" \
M_"v[116:119], a[0:3], a[208:211], 0\n" \
X_"a[118:119], v34 offset:14016\n" \
X_"a[120:121], v34 offset:224\n" \
M_"v[116:119], a[4:7], a[212:215], v[116:119]\n" \
X_"a[122:123], v34 offset:4832\n" \
"v_lshrrev_b32_e32 v44, 9, v20\n" \
M_"v[184:187], a[0:3], v[64:67], 0\n" \
"v_mul_u32_u24_e32 v44, 0xb40, v44\n" \
"v_add_u32_e32 v35, v44, v10\n" \
M_"v[184:187], a[4:7], v[68:71], v[184:187]\n" \
"v_readlane_b32 s65, v11, 33\n" \
"v_readlane_b32 s67, v11, 26\n" \
M_"v[188:191], a[0:3], v[80:83], 0\n" \
X_"a[0:1], v34 offset:9440\n" \
X_"a[2:3], v34 offset:14048\n" \
M_"v[188:191], a[4:7], v[84:87], v[188:191]\n" \
X_"a[4:5], v34 offset:256\n" \
X_"a[6:7], v34 offset:4864\n" \
M_"v[196:199], a[8:11], v[80:83], 0\n" \
"v_readlane_b32 s80, v11, 11\n" \
"v_readlane_b32 s81, v11, 12\n" \
M_"v[196:199], a[12:15], v[84:87], v[196:199]\n" \
"s_mov_b32 s82, 0x47fca0\n" \
"s_mov_b32 s83, 0x20000\n" \
M_"v[192:195], a[8:11], v[64:67], 0\n" \
"v_readlane_b32 s66, v11, 31\n" \
"s_add_u32 m0, s67, 0x0\n" \
M_"v[192:195], a[12:15], v[68:71], v[192:195]\n" \
"v_readlane_b32 s92, v11, 27\n" \
"v_readlane_b32 s73, v11, 32\n" \
M_"v[124:127], a[8:11], a[208:211], 0\n" \
B_"v0, s[80:83], s66 offen offset:0 lds\n" \
M_"v[124:127], a[12:15], a[212:215], v[124:127]\n" \
"v_readlane_b32 s94, v11, 28\n" \
"v_readlane_b32 s84, v11, 9\n" \
M_"v[120:123], a[8:11], a[192:195], 0\n" \
X_"a[8:9], v34 offset:9472\n" \
X_"a[10:11], v34 offset:14080\n" \
M_"v[120:123], a[12:15], a[196:199], v[120:123]\n" \
X_"a[12:13], v34 offset:18432\n" \
X_"a[14:15], v34 offset:23040\n" \
M_"v[128:131], a[16:19], a[192:195], 0\n" \
"v_readlane_b32 s85, v11, 10\n" \
"s_waitcnt lgkmcnt(9)\n" \
M_"v[128:131], a[20:23], a[196:199], v[128:131]\n" \
B_"v1, s[80:83], s66 offen offset:1024 lds\n" \
M_"v[132:135], a[16:19], a[208:211], 0\n" \
"s_mov_b32 s86, 0xa1ca0\n" \
"s_mov_b32 s87, 0x20000\n" \
M_"v[132:135], a[20:23], a[212:215], v[132:135]\n" \
"v_add_u32_e32 v33, 0x10000, v10\n" \
"v_readlane_b32 s68, v11, 34\n" \
M_"v[200:203], a[16:19], v[64:67], 0\n" \
"v_readlane_b32 s69, v11, 35\n" \
"v_readlane_b32 s72, v11, 37\n" \
M_"v[200:203], a[20:23], v[68:71], v[200:203]\n" \
"v_readlane_b32 s93, v11, 38\n" \
"v_readlane_b32 s63, v11, 44\n" \
M_"v[204:207], a[16:19], v[80:83], 0\n" \
X_"a[16:17], v34 offset:18464\n" \
X_"a[18:19], v34 offset:23072\n" \
M_"v[204:207], a[20:23], v[84:87], v[204:207]\n" \
X_"a[20:21], v34 offset:18496\n" \
X_"a[22:23], v34 offset:23104\n" \
M_"v[212:215], a[24:27], v[80:83], 0\n" \
B_"v2, s[80:83], s66 offen offset:2048 lds\n" \
M_"v[212:215], a[28:31], v[84:87], v[212:215]\n" \
"v_bfe_u32 v40, v20, 2, 7\n" \
"s_add_u32 s74, s63, 0x3800\n" \
M_"v[208:211], a[24:27], v[64:67], 0\n" \
"v_lshlrev_b32_e32 v41, 12, v20\n" \
"s_add_u32 s12, s63, 0x0\n" \
M_"v[208:211], a[28:31], v[68:71], v[208:211]\n" \
"v_readlane_b32 s95, v11, 41\n" \
"v_readlane_b32 s96, v11, 42\n" \
M_"v[140:143], a[24:27], a[208:211], 0\n" \
"s_add_u32 s13, s63, 0x1000\n" \
"v_readlane_b32 s97, v11, 43\n" \
M_"v[140:143], a[28:31], a[212:215], v[140:143]\n" \
B_"v3, s[80:83], s66 offen offset:3072 lds\n" \
M_"v[136:139], a[24:27], a[192:195], 0\n" \
X_"a[24:25], v34 offset:18528\n" \
X_"a[26:27], v34 offset:23136\n" \
M_"v[136:139], a[28:31], a[196:199], v[136:139]\n" \
X_"a[28:29], v34 offset:18560\n" \
X_"a[30:31], v34 offset:23168\n" \
M_"v[144:147], a[96:99], a[192:195], 0\n" \
"s_add_u32 m0, s67, 0x1000\n" \
"s_add_u32 s14, s63, 0x2000\n" \
M_"v[144:147], a[100:103], a[196:199], v[144:147]\n" \
"s_add_u32 s15, s63, 0x3000\n" \
"v_readlane_b32 s100, v11, 7\n" \
M_"v[148:151], a[96:99], a[208:211], 0\n" \
B_"v4, s[80:83], s66 offen offset:0 lds\n" \
M_"v[148:151], a[100:103], a[212:215], v[148:151]\n" \
"v_readlane_b32 s16, v11, 45\n" \
"v_bfe_u32 v44, v20, 4, 4\n" \
M_"v[216:219], a[96:99], v[64:67], 0\n" \
"s_add_u32 s17, s16, 0x1000\n" \
"s_add_u32 s18, s16, 0x2000\n" \
M_"v[216:219], a[100:103], v[68:71], v[216:219]\n" \
"s_add_u32 s19, s16, 0x3000\n" \
"v_bfe_u32 v45, v20, 8, 2\n" \
M_"v[220:223], a[96:99], v[80:83], 0\n" \
X_"a[96:97], v34 offset:18592\n" \
X_"a[98:99], v34 offset:23200\n" \
M_"v[220:223], a[100:103], v[84:87], v[220:223]\n" \
X_"a[100:101], v34 offset:18624\n" \
X_"a[102:103], v34 offset:23232\n" \
M_"v[228:231], a[104:107], v[80:83], 0\n" \
B_"v5, s[80:83], s66 offen offset:1024 lds\n" \
M_"v[228:231], a[108:111], v[84:87], v[228:231]\n" \
"v_mul_u32_u24_e32 v42, 0x480, v44\n" \
"v_lshl_add_u32 v42, v45, 3, v42\n" \
M_"v[224:227], a[104:107], v[64:67], 0\n" \
"s_waitcnt vmcnt(14)\n" \
"v_cmp_ne_u32_e32 vcc, s62, v26\n" \
M_"v[224:227], a[108:111], v[68:71], v[224:227]\n" \
"v_lshrrev_b32_e32 v44, 1, v44\n" \
"v_lshlrev_b32_e32 v45, 1, v45\n" \
M_"v[156:159], a[104:107], a[208:211], 0\n" \
"v_sub_u32_e32 v44, v44, v45\n" \
"s_cbranch_vccnz L_sa_%=\n" \
"L_ra_%=:\n" \
M_"v[156:159], a[108:111], a[212:215], v[156:159]\n" \
B_"a[64:67], v20, s[56:59], s65 offen offset:0 sc1\n" \
M_"v[152:155], a[104:107], a[192:195], 0\n" \
X_"a[104:105], v34 offset:18656\n" \
X_"a[106:107], v34 offset:23264\n" \
M_"v[152:155], a[108:111], a[196:199], v[152:155]\n" \
X_"a[108:109], v34 offset:18688\n" \
X_"a[110:111], v34 offset:23296\n" \
M_"v[160:163], a[112:115], a[192:195], 0\n" \
"v_readlane_b32 s64, v11, 36\n" \
"v_and_b32_e32 v45, 16, v20\n" \
M_"v[160:163], a[116:119], a[196:199], v[160:163]\n" \
"v_mov_b32_e32 v46, 0x3f80\n" \
"v_lshlrev_b32_e32 v45, v45, v46\n" \
M_"v[164:167], a[112:115], a[208:211], 0\n" \
B_"a[68:71], v20, s[56:59], s65 offen offset:1024 sc1\n" \
M_"v[164:167], a[116:119], a[212:215], v[164:167]\n" \
"v_mov_b32_e32 v52, 0\n" \
"v_mov_b32_e32 v53, 0\n" \
M_"v[232:235], a[112:115], v[64:67], 0\n" \
"v_mov_b32_e32 v48, 0\n" \
"v_mov_b32_e32 v49, 0\n" \
M_"v[232:235], a[116:119], v[68:71], v[232:235]\n" \
"v_add_u32_e32 v43, 0x9000, v42\n" \
"v_readlane_b32 s88, v11, 13\n" \
M_"v[236:239], a[112:115], v[80:83], 0\n" \
X_"a[112:113], v10 offset:32768\n" \
X_"a[114:115], v10 offset:37376\n" \
M_"v[236:239], a[116:119], v[84:87], v[236:239]\n" \
X_"a[116:117], v10 offset:32800\n" \
"s_waitcnt lgkmcnt(7)\n" \
M_"v[244:247], a[120:123], v[80:83], 0\n" \
X_"a[118:119], v10 offset:37408\n" \
"v_readlane_b32 s89, v11, 14\n" \
M_"v[244:247], a[0:3], v[84:87], v[244:247]\n" \
B_"a[80:83], v32, s[56:59], s65 offen offset:0 sc1\n" \
M_"v[240:243], a[120:123], v[64:67], 0\n" \
"s_mov_b32 s90, 0x240000\n" \
"s_mov_b32 s91, 0x20000\n" \
M_"v[240:243], a[0:3], v[68:71], v[240:243]\n" \
"v_mov_b32_e32 v46, s62\n" \
M_"v[172:175], a[120:123], a[208:211], 0\n" \
M_"v[172:175], a[0:3], a[212:215], v[172:175]\n" \
M_"v[168:171], a[120:123], a[192:195], 0\n" \
X_"a[120:121], v10 offset:32832\n" \
X_"a[122:123], v10 offset:37440\n" \
M_"v[168:171], a[0:3], a[196:199], v[168:171]\n" \
X_"a[0:1], v10 offset:32864\n" \
X_"a[2:3], v10 offset:37472\n" \
M_"v[176:179], a[4:7], a[192:195], 0\n" \
B_"a[84:87], v32, s[56:59], s65 offen offset:1024 sc1\n" \
M_"v[176:179], a[8:11], a[196:199], v[176:179]\n" \
M_"v[180:183], a[4:7], a[208:211], 0\n" \
M_"v[180:183], a[8:11], a[212:215], v[180:183]\n" \
M_"v[248:251], a[4:7], v[64:67], 0\n" \
M_"v[248:251], a[8:11], v[68:71], v[248:251]\n" \
B_"a[72:75], v20, s[56:59], s65 offen offset:2048 sc1\n" \
M_"v[252:255], a[4:7], v[80:83], 0\n" \
X_"a[4:5], v10 offset:32896\n" \
X_"a[6:7], v10 offset:37504\n" \
M_"v[252:255], a[8:11], v[84:87], v[252:255]\n" \
X_"a[8:9], v10 offset:32928\n" \
X_"a[10:11], v10 offset:37536\n" \
M_"v[112:115], a[12:15], a[200:203], v[112:115]\n" \
M_"v[116:119], a[12:15], a[216:219], v[116:119]\n" \
M_"v[184:187], a[12:15], v[72:75], v[184:187]\n" \
B_"a[76:79], v20, s[56:59], s65 offen offset:3072 sc1\n" \
M_"v[188:191], a[12:15], v[96:99], v[188:191]\n" \
X_"a[12:13], v10 offset:32960\n" \
X_"a[14:15], v10 offset:37568\n" \
M_"v[196:199], a[16:19], v[96:99], v[196:199]\n" \
M_"v[192:195], a[16:19], v[72:75], v[192:195]\n" \
M_"v[124:127], a[16:19], a[216:219], v[124:127]\n" \
M_"v[120:123], a[16:19], a[200:203], v[120:123]\n" \
X_"a[16:17], v10 offset:32992\n" \
X_"a[18:19], v10 offset:37600\n" \
M_"v[128:131], a[20:23], a[200:203], v[128:131]\n" \
B_"a[88:91], v32, s[56:59], s65 offen offset:2048 sc1\n" \
M_"v[132:135], a[20:23], a[216:219], v[132:135]\n" \
M_"v[200:203], a[20:23], v[72:75], v[200:203]\n" \
M_"v[204:207], a[20:23], v[96:99], v[204:207]\n" \
X_"a[20:21], v10 offset:33024\n" \
X_"a[22:23], v10 offset:37632\n" \
"s_waitcnt lgkmcnt(10)\n" \
"s_barrier\n" \
M_"v[212:215], a[24:27], v[96:99], v[212:215]\n" \
B_"a[92:95], v32, s[56:59], s65 offen offset:3072 sc1\n" \
M_"v[208:211], a[24:27], v[72:75], v[208:211]\n" \
"v_readlane_b32 s65, v11, 49\n" \
M_"v[140:143], a[24:27], a[216:219], v[140:143]\n" \
M_"v[136:139], a[24:27], a[200:203], v[136:139]\n" \
X_"a[24:25], v35 offset:41984\n" \
X_"a[26:27], v35 offset:44288\n" \
M_"v[144:147], a[28:31], a[200:203], v[144:147]\n" \
M_"v[148:151], a[28:31], a[216:219], v[148:151]\n" \
B_"v6, s[80:83], s66 offen offset:2048 lds\n" \
M_"v[216:219], a[28:31], v[72:75], v[216:219]\n" \
M_"v[220:223], a[28:31], v[96:99], v[220:223]\n" \
X_"a[28:29], v35 offset:52352\n" \
X_"a[30:31], v35 offset:54656\n" \
M_"v[228:231], a[96:99], v[96:99], v[228:231]\n" \
M_"v[224:227], a[96:99], v[72:75], v[224:227]\n" \
M_"v[156:159], a[96:99], a[216:219], v[156:159]\n" \
B_"v7, s[80:83], s66 offen offset:3072 lds\n" \
M_"v[152:155], a[96:99], a[200:203], v[152:155]\n" \
X_"a[96:97], v35 offset:42016\n" \
X_"a[98:99], v35 offset:44320\n" \
M_"v[160:163], a[100:103], a[200:203], v[160:163]\n" \
"s_add_u32 m0, s67, 0x2000\n" \
"v_readlane_b32 s67, v11, 51\n" \
M_"v[164:167], a[100:103], a[216:219], v[164:167]\n" \
M_"v[232:235], a[100:103], v[72:75], v[232:235]\n" \
M_"v[236:239], a[100:103], v[96:99], v[236:239]\n" \
X_"a[100:101], v35 offset:52384\n" \
X_"a[102:103], v35 offset:54688\n" \
M_"v[244:247], a[104:107], v[96:99], v[244:247]\n" \
B_"v8, s[80:83], s66 offen offset:0 lds\n" \
M_"v[240:243], a[104:107], v[72:75], v[240:243]\n" \
"s_add_u32 m0, s92, 0x0\n" \
"v_readlane_b32 s66, v11, 50\n" \
M_"v[172:175], a[104:107], a[216:219], v[172:175]\n" \
M_"v[168:171], a[104:107], a[200:203], v[168:171]\n" \
X_"a[104:105], v35 offset:42048\n" \
X_"a[106:107], v35 offset:44352\n" \
M_"v[176:179], a[108:111], a[200:203], v[176:179]\n" \
M_"v[180:183], a[108:111], a[216:219], v[180:183]\n" \
B_"v0, s[80:83], s73 offen offset:0 lds\n" \
M_"v[248:251], a[108:111], v[72:75], v[248:251]\n" \
M_"v[252:255], a[108:111], v[96:99], v[252:255]\n" \
X_"a[108:109], v35 offset:52416\n" \
X_"a[110:111], v35 offset:54720\n" \
M_"v[112:115], a[112:115], a[204:207], v[112:115]\n" \
M_"v[116:119], a[112:115], a[220:223], v[116:119]\n" \
M_"v[184:187], a[112:115], v[76:79], v[184:187]\n" \
B_"v1, s[80:83], s73 offen offset:1024 lds\n" \
M_"v[188:191], a[112:115], v[100:103], v[188:191]\n" \
X_"a[112:113], v35 offset:42080\n" \
X_"a[114:115], v35 offset:44384\n" \
M_"v[196:199], a[116:119], v[100:103], v[196:199]\n" \
M_"v[192:195], a[116:119], v[76:79], v[192:195]\n" \
"s_waitcnt vmcnt(18)\n" \
"s_mov_b64 exec, 1\n" \
"buffer_store_dword v39, off, s[56:59], s70 offset:0 sc1\n" \
"s_mov_b64 exec, -1\n" \
M_"v[124:127], a[116:119], a[220:223], v[124:127]\n" \
"v_readlane_b32 s70, v11, 54\n" \
M_"v[120:123], a[116:119], a[204:207], v[120:123]\n" \
X_"a[116:117], v35 offset:52448\n" \
X_"a[118:119], v35 offset:54752\n" \
M_"v[128:131], a[120:123], a[204:207], v[128:131]\n" \
M_"v[132:135], a[120:123], a[220:223], v[132:135]\n" \
B_"v2, s[80:83], s73 offen offset:2048 lds\n" \
M_"v[200:203], a[120:123], v[76:79], v[200:203]\n" \
M_"v[204:207], a[120:123], v[100:103], v[204:207]\n" \
X_"a[120:121], v35 offset:42112\n" \
X_"a[122:123], v35 offset:44416\n" \
M_"v[212:215], a[0:3], v[100:103], v[212:215]\n" \
M_"v[208:211], a[0:3], v[76:79], v[208:211]\n" \
M_"v[140:143], a[0:3], a[220:223], v[140:143]\n" \
B_"v3, s[80:83], s73 offen offset:3072 lds\n" \
M_"v[136:139], a[0:3], a[204:207], v[136:139]\n" \
X_"a[0:1], v35 offset:52480\n" \
"s_waitcnt lgkmcnt(7)\n" \
M_"v[144:147], a[4:7], a[204:207], v[144:147]\n" \
X_"a[2:3], v35 offset:54784\n" \
"s_add_u32 m0, s92, 0x1000\n" \
M_"v[148:151], a[4:7], a[220:223], v[148:151]\n" \
M_"v[216:219], a[4:7], v[76:79], v[216:219]\n" \
M_"v[220:223], a[4:7], v[100:103], v[220:223]\n" \
X_"a[4:5], v35 offset:42144\n" \
X_"a[6:7], v35 offset:44448\n" \
M_"v[228:231], a[8:11], v[100:103], v[228:231]\n" \
B_"v4, s[80:83], s73 offen offset:0 lds\n" \
M_"v[224:227], a[8:11], v[76:79], v[224:227]\n" \
M_"v[156:159], a[8:11], a[220:223], v[156:159]\n" \
M_"v[152:155], a[8:11], a[204:207], v[152:155]\n" \
X_"a[8:9], v35 offset:52512\n" \
X_"a[10:11], v35 offset:54816\n" \
M_"v[160:163], a[12:15], a[204:207], v[160:163]\n" \
M_"v[164:167], a[12:15], a[220:223], v[164:167]\n" \
B_"v5, s[80:83], s73 offen offset:1024 lds\n" \
M_"v[232:235], a[12:15], v[76:79], v[232:235]\n" \
M_"v[236:239], a[12:15], v[100:103], v[236:239]\n" \
X_"a[12:13], v35 offset:42176\n" \
X_"a[14:15], v35 offset:44480\n" \
M_"v[244:247], a[16:19], v[100:103], v[244:247]\n" \
M_"v[240:243], a[16:19], v[76:79], v[240:243]\n" \
M_"v[172:175], a[16:19], a[220:223], v[172:175]\n" \
B_"v6, s[80:83], s73 offen offset:2048 lds\n" \
M_"v[168:171], a[16:19], a[204:207], v[168:171]\n" \
X_"a[16:17], v35 offset:52544\n" \
X_"a[18:19], v35 offset:54848\n" \
M_"v[176:179], a[20:23], a[204:207], v[176:179]\n" \
M_"v[180:183], a[20:23], a[220:223], v[180:183]\n" \
M_"v[248:251], a[20:23], v[76:79], v[248:251]\n" \
M_"v[252:255], a[20:23], v[100:103], v[252:255]\n" \
X_"a[20:21], v35 offset:42208\n" \
X_"a[22:23], v35 offset:44512\n" \
M_"v[112:115], a[24:27], a[124:127], v[112:115]\n" \
B_"v7, s[80:83], s73 offen offset:3072 lds\n" \
M_"v[112:115], a[28:31], a[224:227], v[112:115]\n" \
"s_add_u32 m0, s92, 0x2000\n" \
"v_readlane_b32 s92, v11, 49\n" \
M_"v[116:119], a[24:27], a[228:231], v[116:119]\n" \
M_"v[116:119], a[28:31], a[232:235], v[116:119]\n" \
M_"v[184:187], a[24:27], a[236:239], v[184:187]\n" \
M_"v[184:187], a[28:31], a[240:243], v[184:187]\n" \
B_"v8, s[80:83], s73 offen offset:0 lds\n" \
M_"v[188:191], a[24:27], a[244:247], v[188:191]\n" \
X_"a[24:25], v35 offset:52576\n" \
X_"a[26:27], v35 offset:54880\n" \
M_"v[188:191], a[28:31], a[248:251], v[188:191]\n" \
"s_mov_b32 m0, s94\n" \
X_"a[28:29], v35 offset:42240\n" \
M_"v[196:199], a[96:99], a[244:247], v[196:199]\n" \
X_"a[30:31], v35 offset:44544\n" \
"v_readlane_b32 s73, v11, 6\n" \
M_"v[196:199], a[100:103], a[248:251], v[196:199]\n" \
M_"v[192:195], a[96:99], a[236:239], v[192:195]\n" \
B_"v9, s[84:87], 0 offen lds\n" \
M_"v[192:195], a[100:103], a[240:243], v[192:195]\n" \
M_"v[124:127], a[96:99], a[228:231], v[124:127]\n" \
M_"v[124:127], a[100:103], a[232:235], v[124:127]\n" \
M_"v[120:123], a[96:99], a[124:127], v[120:123]\n" \
X_"a[96:97], v35 offset:52608\n" \
X_"a[98:99], v35 offset:54912\n" \
M_"v[120:123], a[100:103], a[224:227], v[120:123]\n" \
X_"a[100:101], v33 offset:0\n" \
X_"a[102:103], v33 offset:4608\n" \
M_"v[128:131], a[104:107], a[124:127], v[128:131]\n" \
B_"a[128:131], v20, s[56:59], s68 offen offset:0 sc1\n" \
M_"v[128:131], a[108:111], a[224:227], v[128:131]\n" \
M_"v[132:135], a[104:107], a[228:231], v[132:135]\n" \
M_"v[132:135], a[108:111], a[232:235], v[132:135]\n" \
M_"v[200:203], a[104:107], a[236:239], v[200:203]\n" \
M_"v[200:203], a[108:111], a[240:243], v[200:203]\n" \
B_"a[132:135], v20, s[56:59], s68 offen offset:1024 sc1\n" \
M_"v[204:207], a[104:107], a[244:247], v[204:207]\n" \
X_"a[104:105], v33 offset:9216\n" \
X_"a[106:107], v33 offset:13824\n" \
M_"v[204:207], a[108:111], a[248:251], v[204:207]\n" \
X_"a[108:109], v33 offset:32\n" \
"s_waitcnt lgkmcnt(7)\n" \
M_"v[212:215], a[112:115], a[244:247], v[212:215]\n" \
X_"a[110:111], v33 offset:4640\n" \
M_"v[212:215], a[116:119], a[248:251], v[212:215]\n" \
M_"v[208:211], a[112:115], a[236:239], v[208:211]\n" \
"buffer_load_dword v47, v38, s[56:59], s71 offen sc1\n" \
M_"v[208:211], a[116:119], a[240:243], v[208:211]\n" \
M_"v[140:143], a[112:115], a[228:231], v[140:143]\n" \
M_"v[140:143], a[116:119], a[232:235], v[140:143]\n" \
M_"v[136:139], a[112:115], a[124:127], v[136:139]\n" \
X_"a[112:113], v33 offset:9248\n" \
X_"a[114:115], v33 offset:13856\n" \
M_"v[136:139], a[116:119], a[224:227], v[136:139]\n" \
X_"a[116:117], v33 offset:64\n" \
X_"a[118:119], v33 offset:4672\n" \
M_"v[144:147], a[120:123], a[124:127], v[144:147]\n" \
B_"a[144:147], v32, s[56:59], s68 offen offset:0 sc1\n" \
M_"v[144:147], a[0:3], a[224:227], v[144:147]\n" \
M_"v[148:151], a[120:123], a[228:231], v[148:151]\n" \
M_"v[148:151], a[0:3], a[232:235], v[148:151]\n" \
M_"v[216:219], a[120:123], a[236:239], v[216:219]\n" \
M_"v[216:219], a[0:3], a[240:243], v[216:219]\n" \
B_"a[148:151], v32, s[56:59], s68 offen offset:1024 sc1\n" \
M_"v[220:223], a[120:123], a[244:247], v[220:223]\n" \
X_"a[120:121], v33 offset:9280\n" \
X_"a[122:123], v33 offset:13888\n" \
M_"v[220:223], a[0:3], a[248:251], v[220:223]\n" \
X_"a[0:1], v33 offset:96\n" \
X_"a[2:3], v33 offset:4704\n" \
M_"v[228:231], a[4:7], a[244:247], v[228:231]\n" \
M_"v[228:231], a[8:11], a[248:251], v[228:231]\n" \
M_"v[224:227], a[4:7], a[236:239], v[224:227]\n" \
B_"a[152:155], v32, s[56:59], s68 offen offset:2048 sc1\n" \
M_"v[224:227], a[8:11], a[240:243], v[224:227]\n" \
M_"v[156:159], a[4:7], a[228:231], v[156:159]\n" \
M_"v[156:159], a[8:11], a[232:235], v[156:159]\n" \
M_"v[152:155], a[4:7], a[124:127], v[152:155]\n" \
X_"a[4:5], v33 offset:9312\n" \
X_"a[6:7], v33 offset:13920\n" \
M_"v[152:155], a[8:11], a[224:227], v[152:155]\n" \
X_"a[8:9], v33 offset:128\n" \
X_"a[10:11], v33 offset:4736\n" \
M_"v[160:163], a[12:15], a[124:127], v[160:163]\n" \
B_"a[156:159], v32, s[56:59], s68 offen offset:3072 sc1\n" \
M_"v[160:163], a[16:19], a[224:227], v[160:163]\n" \
M_"v[164:167], a[12:15], a[228:231], v[164:167]\n" \
M_"v[164:167], a[16:19], a[232:235], v[164:167]\n" \
M_"v[232:235], a[12:15], a[236:239], v[232:235]\n" \
M_"v[232:235], a[16:19], a[240:243], v[232:235]\n" \
B_"a[136:139], v20, s[56:59], s68 offen offset:2048 sc1\n" \
M_"v[236:239], a[12:15], a[244:247], v[236:239]\n" \
X_"a[12:13], v33 offset:9344\n" \
X_"a[14:15], v33 offset:13952\n" \
M_"v[236:239], a[16:19], a[248:251], v[236:239]\n" \
X_"a[16:17], v33 offset:160\n" \
X_"a[18:19], v33 offset:4768\n" \
M_"v[244:247], a[20:23], a[244:247], v[244:247]\n" \
M_"v[244:247], a[24:27], a[248:251], v[244:247]\n" \
M_"v[240:243], a[20:23], a[236:239], v[240:243]\n" \
B_"a[140:143], v20, s[56:59], s68 offen offset:3072 sc1\n" \
M_"v[240:243], a[24:27], a[240:243], v[240:243]\n" \
"v_readlane_b32 s68, v11, 52\n" \
M_"v[172:175], a[20:23], a[228:231], v[172:175]\n" \
M_"v[172:175], a[24:27], a[232:235], v[172:175]\n" \
M_"v[168:171], a[20:23], a[124:127], v[168:171]\n" \
X_"a[20:21], v33 offset:9376\n" \
X_"a[22:23], v33 offset:13984\n" \
M_"v[168:171], a[24:27], a[224:227], v[168:171]\n" \
X_"a[24:25], v33 offset:192\n" \
X_"a[26:27], v33 offset:4800\n" \
M_"v[176:179], a[28:31], a[124:127], v[176:179]\n" \
"s_waitcnt lgkmcnt(8)\n" \
M_"v[176:179], a[96:99], a[224:227], v[176:179]\n" \
B_"a[32:35], v20, s[56:59], s69 offen offset:0 sc1\n" \
M_"v[180:183], a[28:31], a[228:231], v[180:183]\n" \
M_"v[180:183], a[96:99], a[232:235], v[180:183]\n" \
M_"v[248:251], a[28:31], a[236:239], v[248:251]\n" \
M_"v[248:251], a[96:99], a[240:243], v[248:251]\n" \
M_"v[252:255], a[28:31], a[244:247], v[252:255]\n" \
X_"a[28:29], v33 offset:9408\n" \
X_"a[30:31], v33 offset:14016\n" \
M_"v[252:255], a[96:99], a[248:251], v[252:255]\n" \
X_"a[96:97], v33 offset:224\n" \
"s_waitcnt vmcnt(18)\n" \
M_"v[112:115], a[100:103], a[64:67], v[112:115]\n" \
B_"a[36:39], v20, s[56:59], s69 offen offset:1024 sc1\n" \
M_"v[112:115], a[104:107], a[68:71], v[112:115]\n" \
X_"a[98:99], v33 offset:4832\n" \
M_"v[116:119], a[100:103], a[80:83], v[116:119]\n" \
X_"a[100:101], v33 offset:9440\n" \
X_"a[102:103], v33 offset:14048\n" \
M_"v[116:119], a[104:107], a[84:87], v[116:119]\n" \
X_"a[104:105], v33 offset:256\n" \
X_"a[106:107], v33 offset:4864\n" \
M_"v[124:127], a[108:111], a[80:83], v[124:127]\n" \
M_"v[124:127], a[112:115], a[84:87], v[124:127]\n" \
B_"a[48:51], v32, s[56:59], s69 offen offset:0 sc1\n" \
M_"v[120:123], a[108:111], a[64:67], v[120:123]\n" \
X_"a[108:109], v33 offset:9472\n" \
X_"a[110:111], v33 offset:14080\n" \
M_"v[120:123], a[112:115], a[68:71], v[120:123]\n" \
X_"a[112:113], v33 offset:18432\n" \
X_"a[114:115], v33 offset:23040\n" \
M_"v[128:131], a[116:119], a[64:67], v[128:131]\n" \
M_"v[128:131], a[120:123], a[68:71], v[128:131]\n" \
M_"v[132:135], a[116:119], a[80:83], v[132:135]\n" \
B_"a[52:55], v32, s[56:59], s69 offen offset:1024 sc1\n" \
M_"v[132:135], a[120:123], a[84:87], v[132:135]\n" \
X_"a[116:117], v33 offset:27648\n" \
X_"a[118:119], v33 offset:32256\n" \
M_"v[140:143], a[0:3], a[80:83], v[140:143]\n" \
X_"a[120:121], v33 offset:18464\n" \
X_"a[122:123], v33 offset:23072\n" \
M_"v[140:143], a[4:7], a[84:87], v[140:143]\n" \
"s_waitcnt vmcnt(10)\n" \
"v_cmp_ne_u32_e32 vcc, s62, v47\n" \
M_"v[136:139], a[0:3], a[64:67], v[136:139]\n" \
X_"a[0:1], v33 offset:27680\n" \
".fill (L_ab + 36864 - .) / 4, 4, 0xBF800000\n" \
"L_ap9:\n" \
"s_cbranch_execz L_ap0\n" \
X_"a[2:3], v33 offset:32288\n" \
M_"v[136:139], a[4:7], a[68:71], v[136:139]\n" \
B_"a[56:59], v32, s[56:59], s69 offen offset:2048 sc1\n" \
M_"v[144:147], a[8:11], a[64:67], v[144:147]\n" \
X_"a[4:5], v33 offset:18496\n" \
"s_waitcnt lgkmcnt(13)\n" \
M_"v[144:147], a[12:15], a[68:71], v[144:147]\n" \
X_"a[6:7], v33 offset:23104\n" \
"s_cbranch_vccnz L_sb_%=\n" \
"L_rb_%=:\n" \
M_"v[148:151], a[8:11], a[80:83], v[148:151]\n" \
X_"a[8:9], v33 offset:27712\n" \
X_"a[10:11], v33 offset:32320\n" \
M_"v[148:151], a[12:15], a[84:87], v[148:151]\n" \
X_"a[12:13], v33 offset:18528\n" \
X_"a[14:15], v33 offset:23136\n" \
B_"a[60:63], v32, s[56:59], s69 offen offset:3072 sc1\n" \
M_"v[156:159], a[16:19], a[80:83], v[156:159]\n" \
"v_readlane_b32 s71, v11, 55\n" \
"v_cmp_eq_u32_e32 vcc, 0, v44\n" \
M_"v[156:159], a[20:23], a[84:87], v[156:159]\n" \
M_"v[152:155], a[16:19], a[64:67], v[152:155]\n" \
X_"a[16:17], v33 offset:27744\n" \
X_"a[18:19], v33 offset:32352\n" \
M_"v[152:155], a[20:23], a[68:71], v[152:155]\n" \
X_"a[20:21], v33 offset:18560\n" \
X_"a[22:23], v33 offset:23168\n" \
B_"a[40:43], v20, s[56:59], s69 offen offset:2048 sc1\n" \
M_"v[160:163], a[24:27], a[64:67], v[160:163]\n" \
"v_cndmask_b32_e32 v50, 0, v45, vcc\n" \
"v_cmp_eq_u32_e32 vcc, 1, v44\n" \
M_"v[160:163], a[28:31], a[68:71], v[160:163]\n" \
"v_and_b32_e32 v44, 0x3c0, v20\n" \
"v_cndmask_b32_e32 v51, 0, v45, vcc\n" \
M_"v[164:167], a[24:27], a[80:83], v[164:167]\n" \
X_"a[24:25], v33 offset:27776\n" \
X_"a[26:27], v33 offset:32384\n" \
M_"v[164:167], a[28:31], a[84:87], v[164:167]\n" \
X_"a[28:29], v33 offset:18592\n" \
X_"a[30:31], v33 offset:23200\n" \
B_"a[44:47], v20, s[56:59], s69 offen offset:3072 sc1\n" \
M_"v[172:175], a[96:99], a[80:83], v[172:175]\n" \
"v_readlane_b32 s69, v11, 53\n" \
"v_bfe_u32 v45, v20, 1, 5\n" \
M_"v[172:175], a[100:103], a[84:87], v[172:175]\n" \
"v_and_b32_e32 v45, 24, v45\n" \
"v_add_u32_e32 v45, 0x4800, v45\n" \
M_"v[168:171], a[96:99], a[64:67], v[168:171]\n" \
X_"a[96:97], v33 offset:27808\n" \
X_"a[98:99], v33 offset:32416\n" \
M_"v[168:171], a[100:103], a[68:71], v[168:171]\n" \
X_"a[100:101], v33 offset:18624\n" \
"s_waitcnt lgkmcnt(13)\n" \
M_"v[176:179], a[104:107], a[64:67], v[176:179]\n" \
X_"a[102:103], v33 offset:23232\n" \
"v_add_u32_e32 v36, v44, v45\n" \
M_"v[176:179], a[108:111], a[68:71], v[176:179]\n" \
B_"a[124:127], v20, s[56:59], s64 offen offset:0 sc1\n" \
M_"v[180:183], a[104:107], a[80:83], v[180:183]\n" \
X_"a[104:105], v33 offset:27840\n" \
X_"a[106:107], v33 offset:32448\n" \
M_"v[180:183], a[108:111], a[84:87], v[180:183]\n" \
X_"a[108:109], v33 offset:18656\n" \
X_"a[110:111], v33 offset:23264\n" \
M_"v[112:115], a[112:115], a[72:75], v[112:115]\n" \
M_"v[112:115], a[116:119], a[76:79], v[112:115]\n" \
M_"v[116:119], a[112:115], a[88:91], v[116:119]\n" \
X_"a[112:113], v33 offset:27872\n" \
X_"a[114:115], v33 offset:32480\n" \
M_"v[116:119], a[116:119], a[92:95], v[116:119]\n" \
X_"a[116:117], v33 offset:18688\n" \
X_"a[118:119], v33 offset:23296\n" \
M_"v[124:127], a[120:123], a[88:91], v[124:127]\n" \
B_"a[224:227], v20, s[56:59], s64 offen offset:1024 sc1\n" \
M_"v[124:127], a[0:3], a[92:95], v[124:127]\n" \
M_"v[120:123], a[120:123], a[72:75], v[120:123]\n" \
X_"a[120:121], v33 offset:27904\n" \
X_"a[122:123], v33 offset:32512\n" \
M_"v[120:123], a[0:3], a[76:79], v[120:123]\n" \
"s_barrier\n" \
M_"v[128:131], a[4:7], a[72:75], v[128:131]\n" \
X_"a[0:1], v33 offset:36864\n" \
X_"a[2:3], v33 offset:41472\n" \
M_"v[128:131], a[8:11], a[76:79], v[128:131]\n" \
B_"a[236:239], v32, s[56:59], s64 offen offset:0 sc1\n" \
M_"v[132:135], a[4:7], a[88:91], v[132:135]\n" \
X_"a[4:5], v33 offset:46080\n" \
X_"a[6:7], v33 offset:50688\n" \
M_"v[132:135], a[8:11], a[92:95], v[132:135]\n" \
X_"a[8:9], v33 offset:55296\n" \
"s_waitcnt lgkmcnt(15)\n" \
M_"v[140:143], a[12:15], a[88:91], v[140:143]\n" \
X_"a[10:11], v33 offset:59904\n" \
M_"v[140:143], a[16:19], a[92:95], v[140:143]\n" \
M_"v[136:139], a[12:15], a[72:75], v[136:139]\n" \
X_"a[12:13], v10 offset:0\n" \
X_"a[14:15], v10 offset:4608\n" \
M_"v[136:139], a[16:19], a[76:79], v[136:139]\n" \
X_"a[16:17], v34 offset:0\n" \
X_"a[18:19], v34 offset:4608\n" \
M_"v[144:147], a[20:23], a[72:75], v[144:147]\n" \
B_"a[240:243], v32, s[56:59], s64 offen offset:1024 sc1\n" \
M_"v[144:147], a[24:27], a[76:79], v[144:147]\n" \
M_"v[148:151], a[20:23], a[88:91], v[148:151]\n" \
X_"a[20:21], v34 offset:9216\n" \
X_"a[22:23], v34 offset:13824\n" \
M_"v[148:151], a[24:27], a[92:95], v[148:151]\n" \
X_"a[24:25], v34 offset:18432\n" \
X_"a[26:27], v34 offset:23040\n" \
M_"v[156:159], a[28:31], a[88:91], v[156:159]\n" \
M_"v[156:159], a[96:99], a[92:95], v[156:159]\n" \
B_"a[244:247], v32, s[56:59], s64 offen offset:2048 sc1\n" \
M_"v[152:155], a[28:31], a[72:75], v[152:155]\n" \
X_"a[28:29], v10 offset:9216\n" \
X_"a[30:31], v10 offset:13824\n" \
M_"v[152:155], a[96:99], a[76:79], v[152:155]\n" \
X_"a[96:97], v33 offset:36896\n" \
X_"a[98:99], v33 offset:41504\n" \
M_"v[160:163], a[100:103], a[72:75], v[160:163]\n" \
"s_waitcnt lgkmcnt(15)\n" \
M_"v[160:163], a[104:107], a[76:79], v[160:163]\n" \
M_"v[164:167], a[100:103], a[88:91], v[164:167]\n" \
X_"a[100:101], v33 offset:46112\n" \
X_"a[102:103], v33 offset:50720\n" \
M_"v[164:167], a[104:107], a[92:95], v[164:167]\n" \
X_"a[104:105], v33 offset:55328\n" \
X_"a[106:107], v33 offset:59936\n" \
M_"v[172:175], a[108:111], a[88:91], v[172:175]\n" \
B_"a[248:251], v32, s[56:59], s64 offen offset:3072 sc1\n" \
M_"v[172:175], a[112:115], a[92:95], v[172:175]\n" \
M_"v[168:171], a[108:111], a[72:75], v[168:171]\n" \
X_"a[108:109], v10 offset:32\n" \
X_"a[110:111], v10 offset:4640\n" \
M_"v[168:171], a[112:115], a[76:79], v[168:171]\n" \
X_"a[112:113], v34 offset:32\n" \
X_"a[114:115], v34 offset:4640\n" \
M_"v[176:179], a[116:119], a[72:75], v[176:179]\n" \
M_"v[176:179], a[120:123], a[76:79], v[176:179]\n" \
B_"a[228:231], v20, s[56:59], s64 offen offset:2048 sc1\n" \
M_"v[180:183], a[116:119], a[88:91], v[180:183]\n" \
X_"a[116:117], v34 offset:9248\n" \
X_"a[118:119], v34 offset:13856\n" \
M_"v[180:183], a[120:123], a[92:95], v[180:183]\n" \
X_"a[120:121], v34 offset:18464\n" \
X_"a[122:123], v34 offset:23072\n" \
M_"v[112:115], a[0:3], a[128:131], v[112:115]\n" \
"s_waitcnt lgkmcnt(15)\n" \
M_"v[112:115], a[4:7], a[132:135], v[112:115]\n" \
"s_waitcnt vmcnt(18)\n" \
M_"v[116:119], a[0:3], a[144:147], v[116:119]\n" \
X_"a[0:1], v10 offset:9248\n" \
X_"a[2:3], v10 offset:13856\n" \
M_"v[116:119], a[4:7], a[148:151], v[116:119]\n" \
X_"a[4:5], v33 offset:36928\n" \
X_"a[6:7], v33 offset:41536\n" \
M_"v[116:119], a[8:11], a[152:155], v[116:119]\n" \
"s_waitcnt vmcnt(17)\n" \
M_"v[116:119], a[12:15], a[156:159], v[116:119]\n" \
"s_waitcnt vmcnt(16)\n" \
M_"v[112:115], a[8:11], a[136:139], v[112:115]\n" \
X_"a[8:9], v33 offset:46144\n" \
"s_waitcnt vmcnt(15)\n" \
M_"v[112:115], a[12:15], a[140:143], v[112:115]\n" \
X_"a[10:11], v33 offset:50752\n" \
"s_waitcnt vmcnt(14)\n" \
M_"v[112:115], a[16:19], a[32:35], v[112:115]\n" \
X_"a[12:13], v33 offset:55360\n" \
"s_waitcnt vmcnt(13)\n" \
M_"v[112:115], a[20:23], a[36:39], v[112:115]\n" \
X_"a[14:15], v33 offset:59968\n" \
"s_waitcnt vmcnt(12)\n" \
M_"v[116:119], a[16:19], a[48:51], v[116:119]\n" \
X_"a[16:17], v10 offset:64\n" \
"s_waitcnt vmcnt(11)\n" \
M_"v[116:119], a[20:23], a[52:55], v[116:119]\n" \
X_"a[18:19], v10 offset:4672\n" \
"s_waitcnt vmcnt(10)\n" \
M_"v[116:119], a[24:27], a[56:59], v[116:119]\n" \
X_"a[20:21], v34 offset:64\n" \
X_"a[22:23], v34 offset:4672\n" \
B_"a[232:235], v20, s[56:59], s64 offen offset:3072 sc1\n" \
"s_waitcnt vmcnt(10) lgkmcnt(12)\n" \
M_"v[116:119], a[28:31], a[60:63], v[116:119]\n" \
B_"a[160:163], v20, s[56:59], s72 offen offset:0 sc1\n" \
"s_waitcnt vmcnt(10)\n" \
M_"v[112:115], a[24:27], a[40:43], v[112:115]\n" \
X_"a[24:25], v34 offset:9280\n" \
X_"a[26:27], v34 offset:13888\n" \
B_"a[164:167], v20, s[56:59], s72 offen offset:1024 sc1\n" \
"s_waitcnt vmcnt(10)\n" \
M_"v[112:115], a[28:31], a[44:47], v[112:115]\n" \
X_"a[28:29], v34 offset:18496\n" \
X_"a[30:31], v34 offset:23104\n" \
M_"v[120:123], a[96:99], a[128:131], v[120:123]\n" \
B_"a[176:179], v32, s[56:59], s72 offen offset:0 sc1\n" \
M_"v[120:123], a[100:103], a[132:135], v[120:123]\n" \
M_"v[124:127], a[96:99], a[144:147], v[124:127]\n" \
X_"a[96:97], v10 offset:9280\n" \
X_"a[98:99], v10 offset:13888\n" \
C_"v112, v112, v113\n" \
C_"v113, v114, v115\n" \
C_"v114, v116, v117\n" \
C_"v115, v118, v119\n" \
"buffer_store_dwordx4 v[112:115], v20, s[56:59], s65 offen offset:0 sc1\n" \
"s_add_u32 s65, s65, 0x3800\n" \
M_"v[124:127], a[100:103], a[148:151], v[124:127]\n" \
X_"a[100:101], v33 offset:36960\n" \
X_"a[102:103], v33 offset:41568\n" \
M_"v[124:127], a[104:107], a[152:155], v[124:127]\n" \
M_"v[124:127], a[108:111], a[156:159], v[124:127]\n" \
M_"v[120:123], a[104:107], a[136:139], v[120:123]\n" \
X_"a[104:105], v33 offset:46176\n" \
X_"a[106:107], v33 offset:50784\n" \
M_"v[120:123], a[108:111], a[140:143], v[120:123]\n" \
X_"a[108:109], v33 offset:55392\n" \
X_"a[110:111], v33 offset:60000\n" \
M_"v[120:123], a[112:115], a[32:35], v[120:123]\n" \
B_"a[180:183], v32, s[56:59], s72 offen offset:1024 sc1\n" \
M_"v[120:123], a[116:119], a[36:39], v[120:123]\n" \
M_"v[124:127], a[112:115], a[48:51], v[124:127]\n" \
X_"a[112:113], v10 offset:96\n" \
X_"a[114:115], v10 offset:4704\n" \
M_"v[124:127], a[116:119], a[52:55], v[124:127]\n" \
X_"a[116:117], v34 offset:96\n" \
X_"a[118:119], v34 offset:4704\n" \
M_"v[124:127], a[120:123], a[56:59], v[124:127]\n" \
"s_waitcnt lgkmcnt(12)\n" \
M_"v[124:127], a[0:3], a[60:63], v[124:127]\n" \
B_"a[168:171], v20, s[56:59], s72 offen offset:2048 sc1\n" \
M_"v[120:123], a[120:123], a[40:43], v[120:123]\n" \
X_"a[120:121], v34 offset:9312\n" \
X_"a[122:123], v34 offset:13920\n" \
M_"v[120:123], a[0:3], a[44:47], v[120:123]\n" \
X_"a[0:1], v34 offset:18528\n" \
X_"a[2:3], v34 offset:23136\n" \
M_"v[128:131], a[4:7], a[128:131], v[128:131]\n" \
M_"v[128:131], a[8:11], a[132:135], v[128:131]\n" \
M_"v[132:135], a[4:7], a[144:147], v[132:135]\n" \
X_"a[4:5], v10 offset:9312\n" \
X_"a[6:7], v10 offset:13920\n" \
M_"v[132:135], a[8:11], a[148:151], v[132:135]\n" \
C_"v120, v120, v121\n" \
C_"v121, v122, v123\n" \
C_"v122, v124, v125\n" \
C_"v123, v126, v127\n" \
"buffer_store_dwordx4 v[120:123], v20, s[56:59], s66 offen offset:0 sc1\n" \
"s_add_u32 s66, s66, 0x3000\n" \
M_"v[132:135], a[12:15], a[152:155], v[132:135]\n" \
X_"a[8:9], v33 offset:36992\n" \
X_"a[10:11], v33 offset:41600\n" \
M_"v[132:135], a[16:19], a[156:159], v[132:135]\n" \
M_"v[128:131], a[12:15], a[136:139], v[128:131]\n" \
X_"a[12:13], v33 offset:46208\n" \
X_"a[14:15], v33 offset:50816\n" \
M_"v[128:131], a[16:19], a[140:143], v[128:131]\n" \
X_"a[16:17], v33 offset:55424\n" \
X_"a[18:19], v33 offset:60032\n" \
M_"v[128:131], a[20:23], a[32:35], v[128:131]\n" \
B_"a[184:187], v32, s[56:59], s72 offen offset:2048 sc1\n" \
M_"v[128:131], a[24:27], a[36:39], v[128:131]\n" \
M_"v[132:135], a[20:23], a[48:51], v[132:135]\n" \
X_"a[20:21], v10 offset:128\n" \
X_"a[22:23], v10 offset:4736\n" \
M_"v[132:135], a[24:27], a[52:55], v[132:135]\n" \
X_"a[24:25], v34 offset:128\n" \
X_"a[26:27], v34 offset:4736\n" \
M_"v[132:135], a[28:31], a[56:59], v[132:135]\n" \
"s_waitcnt lgkmcnt(12)\n" \
M_"v[132:135], a[96:99], a[60:63], v[132:135]\n" \
B_"a[172:175], v20, s[56:59], s72 offen offset:3072 sc1\n" \
M_"v[128:131], a[28:31], a[40:43], v[128:131]\n" \
X_"a[28:29], v34 offset:9344\n" \
X_"a[30:31], v34 offset:13952\n" \
M_"v[128:131], a[96:99], a[44:47], v[128:131]\n" \
X_"a[96:97], v34 offset:18560\n" \
X_"a[98:99], v34 offset:23168\n" \
M_"v[136:139], a[100:103], a[128:131], v[136:139]\n" \
M_"v[136:139], a[104:107], a[132:135], v[136:139]\n" \
M_"v[140:143], a[100:103], a[144:147], v[140:143]\n" \
B_"a[188:191], v32, s[56:59], s72 offen offset:3072 sc1\n" \
M_"v[140:143], a[104:107], a[148:151], v[140:143]\n" \
"v_readlane_b32 s72, v11, 56\n" \
C_"v128, v128, v129\n" \
C_"v129, v130, v131\n" \
C_"v130, v132, v133\n" \
C_"v131, v134, v135\n" \
M_"v[140:143], a[108:111], a[152:155], v[140:143]\n" \
"buffer_store_dwordx4 v[128:131], v20, s[56:59], s67 offen offset:0 sc1\n" \
"s_add_u32 s67, s67, 0x2800\n" \
M_"v[140:143], a[112:115], a[156:159], v[140:143]\n" \
X_"a[100:101], v10 offset:9344\n" \
X_"a[102:103], v10 offset:13952\n" \
M_"v[136:139], a[108:111], a[136:139], v[136:139]\n" \
X_"a[104:105], v33 offset:37024\n" \
X_"a[106:107], v33 offset:41632\n" \
M_"v[136:139], a[112:115], a[140:143], v[136:139]\n" \
X_"a[108:109], v33 offset:46240\n" \
X_"a[110:111], v33 offset:50848\n" \
M_"v[136:139], a[116:119], a[32:35], v[136:139]\n" \
X_"a[112:113], v33 offset:55456\n" \
X_"a[114:115], v33 offset:60064\n" \
M_"v[136:139], a[120:123], a[36:39], v[136:139]\n" \
B_"a[192:195], v20, s[56:59], s93 offen offset:0 sc1\n" \
M_"v[140:143], a[116:119], a[48:51], v[140:143]\n" \
X_"a[116:117], v10 offset:160\n" \
X_"a[118:119], v10 offset:4768\n" \
M_"v[140:143], a[120:123], a[52:55], v[140:143]\n" \
X_"a[120:121], v34 offset:160\n" \
X_"a[122:123], v34 offset:4768\n" \
M_"v[140:143], a[0:3], a[56:59], v[140:143]\n" \
"s_waitcnt lgkmcnt(13)\n" \
M_"v[140:143], a[4:7], a[60:63], v[140:143]\n" \
M_"v[136:139], a[0:3], a[40:43], v[136:139]\n" \
B_"a[196:199], v20, s[56:59], s93 offen offset:1024 sc1\n" \
M_"v[136:139], a[4:7], a[44:47], v[136:139]\n" \
X_"a[0:1], v34 offset:9376\n" \
X_"a[2:3], v34 offset:13984\n" \
M_"v[144:147], a[8:11], a[128:131], v[144:147]\n" \
X_"a[4:5], v34 offset:18592\n" \
X_"a[6:7], v34 offset:23200\n" \
M_"v[144:147], a[12:15], a[132:135], v[144:147]\n" \
M_"v[148:151], a[8:11], a[144:147], v[148:151]\n" \
X_"a[8:9], v10 offset:9376\n" \
C_"v136, v136, v137\n" \
C_"v137, v138, v139\n" \
C_"v138, v140, v141\n" \
C_"v139, v142, v143\n" \
"buffer_store_dwordx4 v[136:139], v20, s[56:59], s68 offen offset:0 sc1\n" \
"s_add_u32 s68, s68, 0x2000\n" \
M_"v[148:151], a[12:15], a[148:151], v[148:151]\n" \
X_"a[10:11], v10 offset:13984\n" \
X_"a[12:13], v33 offset:37056\n" \
M_"v[148:151], a[16:19], a[152:155], v[148:151]\n" \
X_"a[14:15], v33 offset:41664\n" \
M_"v[148:151], a[20:23], a[156:159], v[148:151]\n" \
M_"v[144:147], a[16:19], a[136:139], v[144:147]\n" \
B_"a[208:211], v32, s[56:59], s93 offen offset:0 sc1\n" \
M_"v[144:147], a[20:23], a[140:143], v[144:147]\n" \
X_"a[16:17], v33 offset:46272\n" \
X_"a[18:19], v33 offset:50880\n" \
M_"v[144:147], a[24:27], a[32:35], v[144:147]\n" \
X_"a[20:21], v33 offset:55488\n" \
X_"a[22:23], v33 offset:60096\n" \
M_"v[144:147], a[28:31], a[36:39], v[144:147]\n" \
M_"v[148:151], a[24:27], a[48:51], v[148:151]\n" \
X_"a[24:25], v10 offset:192\n" \
X_"a[26:27], v10 offset:4800\n" \
B_"a[212:215], v32, s[56:59], s93 offen offset:1024 sc1\n" \
M_"v[148:151], a[28:31], a[52:55], v[148:151]\n" \
X_"a[28:29], v34 offset:192\n" \
"s_waitcnt lgkmcnt(13)\n" \
M_"v[148:151], a[96:99], a[56:59], v[148:151]\n" \
X_"a[30:31], v34 offset:4800\n" \
M_"v[148:151], a[100:103], a[60:63], v[148:151]\n" \
M_"v[144:147], a[96:99], a[40:43], v[144:147]\n" \
X_"a[96:97], v34 offset:9408\n" \
X_"a[98:99], v34 offset:14016\n" \
B_"a[216:219], v32, s[56:59], s93 offen offset:2048 sc1\n" \
M_"v[144:147], a[100:103], a[44:47], v[144:147]\n" \
X_"a[100:101], v34 offset:18624\n" \
X_"a[102:103], v34 offset:23232\n" \
M_"v[152:155], a[104:107], a[128:131], v[152:155]\n" \
M_"v[152:155], a[108:111], a[132:135], v[152:155]\n" \
M_"v[156:159], a[104:107], a[144:147], v[156:159]\n" \
X_"a[104:105], v10 offset:9408\n" \
X_"a[106:107], v10 offset:14016\n" \
B_"a[220:223], v32, s[56:59], s93 offen offset:3072 sc1\n" \
C_"v144, v144, v145\n" \
C_"v145, v146, v147\n" \
C_"v146, v148, v149\n" \
C_"v147, v150, v151\n" \
M_"v[156:159], a[108:111], a[148:151], v[156:159]\n" \
X_"a[108:109], v33 offset:256\n" \
X_"a[110:111], v33 offset:4864\n" \
"buffer_store_dwordx4 v[144:147], v20, s[56:59], s69 offen offset:0 sc1\n" \
"s_add_u32 s69, s69, 0x1800\n" \
M_"v[156:159], a[112:115], a[152:155], v[156:159]\n" \
M_"v[156:159], a[116:119], a[156:159], v[156:159]\n" \
M_"v[152:155], a[112:115], a[136:139], v[152:155]\n" \
X_"a[112:113], v33 offset:9472\n" \
X_"a[114:115], v33 offset:14080\n" \
M_"v[152:155], a[116:119], a[140:143], v[152:155]\n" \
B_"a[200:203], v20, s[56:59], s93 offen offset:2048 sc1\n" \
M_"v[152:155], a[120:123], a[32:35], v[152:155]\n" \
X_"a[116:117], v33 offset:18688\n" \
X_"a[118:119], v33 offset:23296\n" \
M_"v[152:155], a[0:3], a[36:39], v[152:155]\n" \
M_"v[156:159], a[120:123], a[48:51], v[156:159]\n" \
X_"a[120:121], v33 offset:27904\n" \
X_"a[122:123], v33 offset:32512\n" \
M_"v[156:159], a[0:3], a[52:55], v[156:159]\n" \
X_"a[0:1], v33 offset:37120\n" \
B_"a[204:207], v20, s[56:59], s93 offen offset:3072 sc1\n" \
"s_waitcnt lgkmcnt(11)\n" \
M_"v[156:159], a[4:7], a[56:59], v[156:159]\n" \
X_"a[2:3], v33 offset:41728\n" \
M_"v[156:159], a[8:11], a[60:63], v[156:159]\n" \
M_"v[152:155], a[4:7], a[40:43], v[152:155]\n" \
X_"a[4:5], v33 offset:46336\n" \
X_"a[6:7], v33 offset:50944\n" \
M_"v[152:155], a[8:11], a[44:47], v[152:155]\n" \
X_"a[8:9], v33 offset:55552\n" \
X_"a[10:11], v33 offset:60160\n" \
M_"v[160:163], a[12:15], a[128:131], v[160:163]\n" \
B_"v[64:67], v20, s[56:59], s95 offen offset:0 sc1\n" \
M_"v[160:163], a[16:19], a[132:135], v[160:163]\n" \
M_"v[164:167], a[12:15], a[144:147], v[164:167]\n" \
X_"a[12:13], v10 offset:256\n" \
X_"a[14:15], v10 offset:4864\n" \
C_"v152, v152, v153\n" \
C_"v153, v154, v155\n" \
C_"v154, v156, v157\n" \
C_"v155, v158, v159\n" \
"buffer_store_dwordx4 v[152:155], v20, s[56:59], s70 offen offset:0 sc1\n" \
"s_add_u32 s70, s70, 0x1000\n" \
M_"v[164:167], a[16:19], a[148:151], v[164:167]\n" \
X_"a[16:17], v34 offset:256\n" \
X_"a[18:19], v34 offset:4864\n" \
M_"v[164:167], a[20:23], a[152:155], v[164:167]\n" \
M_"v[164:167], a[24:27], a[156:159], v[164:167]\n" \
M_"v[160:163], a[20:23], a[136:139], v[160:163]\n" \
X_"a[20:21], v34 offset:9472\n" \
X_"a[22:23], v34 offset:14080\n" \
M_"v[160:163], a[24:27], a[140:143], v[160:163]\n" \
X_"a[24:25], v34 offset:18688\n" \
X_"a[26:27], v34 offset:23296\n" \
"s_waitcnt vmcnt(19)\n" \
"s_mov_b64 exec, 1\n" \
"buffer_store_dword v39, off, s[56:59], s65 offset:0 sc1\n" \
"s_mov_b64 exec, -1\n" \
M_"v[160:163], a[28:31], a[32:35], v[160:163]\n" \
M_"v[160:163], a[96:99], a[36:39], v[160:163]\n" \
M_"v[164:167], a[28:31], a[48:51], v[164:167]\n" \
X_"a[28:29], v10 offset:9472\n" \
X_"a[30:31], v10 offset:14080\n" \
M_"v[164:167], a[96:99], a[52:55], v[164:167]\n" \
X_"a[96:97], v33 offset:0\n" \
X_"a[98:99], v33 offset:4608\n" \
M_"v[164:167], a[100:103], a[56:59], v[164:167]\n" \
"s_waitcnt lgkmcnt(12)\n" \
M_"v[164:167], a[104:107], a[60:63], v[164:167]\n" \
B_"v[68:71], v20, s[56:59], s95 offen offset:1024 sc1\n" \
M_"v[160:163], a[100:103], a[40:43], v[160:163]\n" \
X_"a[100:101], v33 offset:9216\n" \
X_"a[102:103], v33 offset:13824\n" \
M_"v[160:163], a[104:107], a[44:47], v[160:163]\n" \
X_"a[104:105], v33 offset:18432\n" \
X_"a[106:107], v33 offset:23040\n" \
M_"v[248:251], a[108:111], a[124:127], v[248:251]\n" \
M_"v[248:251], a[112:115], a[224:227], v[248:251]\n" \
M_"v[252:255], a[108:111], a[236:239], v[252:255]\n" \
X_"a[108:109], v33 offset:27648\n" \
X_"a[110:111], v33 offset:32256\n" \
M_"v[252:255], a[112:115], a[240:243], v[252:255]\n" \
C_"v160, v160, v161\n" \
C_"v161, v162, v163\n" \
C_"v162, v164, v165\n" \
C_"v163, v166, v167\n" \
"buffer_store_dwordx4 v[160:163], v20, s[56:59], s71 offen offset:0 sc1\n" \
"s_add_u32 s71, s71, 0x800\n" \
M_"v[252:255], a[116:119], a[244:247], v[252:255]\n" \
X_"a[112:113], v33 offset:36864\n" \
X_"a[114:115], v33 offset:41472\n" \
M_"v[252:255], a[120:123], a[248:251], v[252:255]\n" \
M_"v[248:251], a[116:119], a[228:231], v[248:251]\n" \
X_"a[116:117], v33 offset:46080\n" \
X_"a[118:119], v33 offset:50688\n" \
"s_waitcnt vmcnt(19)\n" \
"s_mov_b64 exec, 1\n" \
"buffer_store_dword v39, off, s[56:59], s66 offset:4 sc1\n" \
"s_mov_b64 exec, -1\n" \
"v_readlane_b32 s66, v11, 50\n" \
M_"v[248:251], a[120:123], a[232:235], v[248:251]\n" \
X_"a[120:121], v33 offset:55296\n" \
X_"a[122:123], v33 offset:59904\n" \
M_"v[176:179], a[0:3], a[128:131], v[176:179]\n" \
M_"v[176:179], a[4:7], a[132:135], v[176:179]\n" \
M_"v[180:183], a[0:3], a[144:147], v[180:183]\n" \
B_"v[72:75], v32, s[56:59], s95 offen offset:0 sc1\n" \
M_"v[180:183], a[4:7], a[148:151], v[180:183]\n" \
M_"v[248:251], a[0:3], a[160:163], v[248:251]\n" \
M_"v[248:251], a[4:7], a[164:167], v[248:251]\n" \
M_"v[252:255], a[0:3], a[176:179], v[252:255]\n" \
X_"a[0:1], v10 offset:0\n" \
X_"a[2:3], v10 offset:4608\n" \
M_"v[252:255], a[4:7], a[180:183], v[252:255]\n" \
X_"a[4:5], v34 offset:0\n" \
"s_waitcnt vmcnt(20)\n" \
M_"v[252:255], a[8:11], a[184:187], v[252:255]\n" \
X_"a[6:7], v34 offset:4608\n" \
"s_waitcnt vmcnt(18) lgkmcnt(10)\n" \
M_"v[252:255], a[12:15], a[188:191], v[252:255]\n" \
B_"v[76:79], v32, s[56:59], s95 offen offset:1024 sc1\n" \
M_"v[248:251], a[8:11], a[168:171], v[248:251]\n" \
M_"v[248:251], a[12:15], a[172:175], v[248:251]\n" \
M_"v[180:183], a[8:11], a[152:155], v[180:183]\n" \
M_"v[180:183], a[12:15], a[156:159], v[180:183]\n" \
M_"v[176:179], a[8:11], a[136:139], v[176:179]\n" \
X_"a[8:9], v34 offset:9216\n" \
X_"a[10:11], v34 offset:13824\n" \
M_"v[176:179], a[12:15], a[140:143], v[176:179]\n" \
X_"a[12:13], v34 offset:18432\n" \
X_"a[14:15], v34 offset:23040\n" \
"s_waitcnt vmcnt(18)\n" \
"s_mov_b64 exec, 1\n" \
"buffer_store_dword v39, off, s[56:59], s67 offset:8 sc1\n" \
"s_mov_b64 exec, -1\n" \
"v_readlane_b32 s67, v11, 51\n" \
M_"v[176:179], a[16:19], a[32:35], v[176:179]\n" \
M_"v[176:179], a[20:23], a[36:39], v[176:179]\n" \
M_"v[180:183], a[16:19], a[48:51], v[180:183]\n" \
M_"v[180:183], a[20:23], a[52:55], v[180:183]\n" \
"s_waitcnt vmcnt(18)\n" \
M_"v[248:251], a[16:19], a[192:195], v[248:251]\n" \
"s_waitcnt vmcnt(17)\n" \
M_"v[248:251], a[20:23], a[196:199], v[248:251]\n" \
"s_waitcnt vmcnt(15)\n" \
M_"v[252:255], a[16:19], a[208:211], v[252:255]\n" \
X_"a[16:17], v10 offset:9216\n" \
"s_mov_b64 exec, 1\n" \
"buffer_store_dword v39, off, s[56:59], s68 offset:12 sc1\n" \
"s_mov_b64 exec, -1\n" \
"s_waitcnt vmcnt(15)\n" \
M_"v[252:255], a[20:23], a[212:215], v[252:255]\n" \
"v_readlane_b32 s68, v11, 52\n" \
"s_waitcnt vmcnt(14)\n" \
M_"v[252:255], a[24:27], a[216:219], v[252:255]\n" \
X_"a[18:19], v10 offset:13824\n" \
"s_waitcnt vmcnt(13)\n" \
M_"v[252:255], a[28:31], a[220:223], v[252:255]\n" \
X_"a[20:21], v33 offset:32\n" \
X_"a[22:23], v33 offset:4640\n" \
B_"v[80:83], v20, s[56:59], s96 offen offset:0 sc1\n" \
"s_waitcnt vmcnt(12)\n" \
M_"v[248:251], a[24:27], a[200:203], v[248:251]\n" \
"s_mov_b64 exec, 1\n" \
"buffer_store_dword v39, off, s[56:59], s69 offset:16 sc1\n" \
"s_mov_b64 exec, -1\n" \
"v_readlane_b32 s69, v11, 53\n" \
".fill (L_ab + 40960 - .) / 4, 4, 0xBF800000\n" \
"L_ap10:\n" \
"s_cbranch_execz L_ap0\n" \
"s_waitcnt vmcnt(12)\n" \
M_"v[248:251], a[28:31], a[204:207], v[248:251]\n" \
M_"v[180:183], a[24:27], a[56:59], v[180:183]\n" \
B_"v[84:87], v20, s[56:59], s96 offen offset:1024 sc1\n" \
M_"v[180:183], a[28:31], a[60:63], v[180:183]\n" \
M_"v[176:179], a[24:27], a[40:43], v[176:179]\n" \
X_"a[24:25], v33 offset:9248\n" \
X_"a[26:27], v33 offset:13856\n" \
M_"v[176:179], a[28:31], a[44:47], v[176:179]\n" \
X_"a[28:29], v33 offset:18464\n" \
C_"v248, v248, v249\n" \
C_"v249, v250, v251\n" \
C_"v250, v252, v253\n" \
C_"v251, v254, v255\n" \
"buffer_store_dwordx4 v[248:251], v20, s[56:59], s72 offen offset:1024 sc1\n" \
M_"v[184:187], a[96:99], a[124:127], v[184:187]\n" \
X_"a[30:31], v33 offset:23072\n" \
C_"v176, v176, v177\n" \
C_"v177, v178, v179\n" \
C_"v178, v180, v181\n" \
C_"v179, v182, v183\n" \
M_"v[184:187], a[100:103], a[224:227], v[184:187]\n" \
"buffer_store_dwordx4 v[176:179], v20, s[56:59], s72 offen offset:0 sc1\n" \
M_"v[188:191], a[96:99], a[236:239], v[188:191]\n" \
X_"a[96:97], v33 offset:27680\n" \
X_"a[98:99], v33 offset:32288\n" \
M_"v[188:191], a[100:103], a[240:243], v[188:191]\n" \
X_"a[100:101], v33 offset:36896\n" \
X_"a[102:103], v33 offset:41504\n" \
M_"v[188:191], a[104:107], a[244:247], v[188:191]\n" \
M_"v[188:191], a[108:111], a[248:251], v[188:191]\n" \
M_"v[184:187], a[104:107], a[228:231], v[184:187]\n" \
X_"a[104:105], v33 offset:46112\n" \
X_"a[106:107], v33 offset:50720\n" \
M_"v[184:187], a[108:111], a[232:235], v[184:187]\n" \
X_"a[108:109], v33 offset:55328\n" \
"s_waitcnt lgkmcnt(11)\n" \
M_"v[184:187], a[112:115], a[160:163], v[184:187]\n" \
B_"v[88:91], v32, s[56:59], s96 offen offset:0 sc1\n" \
M_"v[184:187], a[116:119], a[164:167], v[184:187]\n" \
X_"a[110:111], v33 offset:59936\n" \
M_"v[188:191], a[112:115], a[176:179], v[188:191]\n" \
X_"a[112:113], v10 offset:32\n" \
X_"a[114:115], v10 offset:4640\n" \
M_"v[188:191], a[116:119], a[180:183], v[188:191]\n" \
X_"a[116:117], v34 offset:32\n" \
X_"a[118:119], v34 offset:4640\n" \
M_"v[188:191], a[120:123], a[184:187], v[188:191]\n" \
M_"v[188:191], a[0:3], a[188:191], v[188:191]\n" \
B_"v[92:95], v32, s[56:59], s96 offen offset:1024 sc1\n" \
M_"v[184:187], a[120:123], a[168:171], v[184:187]\n" \
X_"a[120:121], v34 offset:9248\n" \
X_"a[122:123], v34 offset:13856\n" \
M_"v[184:187], a[0:3], a[172:175], v[184:187]\n" \
X_"a[0:1], v34 offset:18464\n" \
X_"a[2:3], v34 offset:23072\n" \
M_"v[184:187], a[4:7], a[192:195], v[184:187]\n" \
M_"v[184:187], a[8:11], a[196:199], v[184:187]\n" \
M_"v[188:191], a[4:7], a[208:211], v[188:191]\n" \
X_"a[4:5], v10 offset:9248\n" \
X_"a[6:7], v10 offset:13856\n" \
M_"v[188:191], a[8:11], a[212:215], v[188:191]\n" \
X_"a[8:9], v33 offset:64\n" \
X_"a[10:11], v33 offset:4672\n" \
M_"v[188:191], a[12:15], a[216:219], v[188:191]\n" \
B_"v[96:99], v20, s[56:59], s97 offen offset:0 sc1\n" \
M_"v[188:191], a[16:19], a[220:223], v[188:191]\n" \
M_"v[184:187], a[12:15], a[200:203], v[184:187]\n" \
X_"a[12:13], v33 offset:9280\n" \
X_"a[14:15], v33 offset:13888\n" \
M_"v[184:187], a[16:19], a[204:207], v[184:187]\n" \
"buffer_load_dword v37, v40, s[56:59], s74 offen sc1\n" \
M_"v[192:195], a[20:23], a[124:127], v[192:195]\n" \
X_"a[16:17], v33 offset:18496\n" \
"s_waitcnt lgkmcnt(13)\n" \
M_"v[192:195], a[24:27], a[224:227], v[192:195]\n" \
X_"a[18:19], v33 offset:23104\n" \
M_"v[196:199], a[20:23], a[236:239], v[196:199]\n" \
X_"a[20:21], v33 offset:27712\n" \
C_"v184, v184, v185\n" \
C_"v185, v186, v187\n" \
C_"v186, v188, v189\n" \
C_"v187, v190, v191\n" \
"buffer_store_dwordx4 v[184:187], v20, s[56:59], s92 offen offset:1024 sc1\n" \
"s_waitcnt vmcnt(18)\n" \
"s_mov_b64 exec, 1\n" \
"buffer_store_dword v39, off, s[56:59], s70 offset:20 sc1\n" \
"s_mov_b64 exec, -1\n" \
"s_add_u32 s92, s92, 0x3800\n" \
M_"v[196:199], a[24:27], a[240:243], v[196:199]\n" \
X_"a[22:23], v33 offset:32320\n" \
X_"a[24:25], v33 offset:36928\n" \
M_"v[196:199], a[28:31], a[244:247], v[196:199]\n" \
X_"a[26:27], v33 offset:41536\n" \
M_"v[196:199], a[96:99], a[248:251], v[196:199]\n" \
M_"v[192:195], a[28:31], a[228:231], v[192:195]\n" \
X_"a[28:29], v33 offset:46144\n" \
X_"a[30:31], v33 offset:50752\n" \
M_"v[192:195], a[96:99], a[232:235], v[192:195]\n" \
B_"v[100:103], v20, s[56:59], s97 offen offset:1024 sc1\n" \
M_"v[192:195], a[100:103], a[160:163], v[192:195]\n" \
X_"a[96:97], v33 offset:55360\n" \
X_"a[98:99], v33 offset:59968\n" \
M_"v[192:195], a[104:107], a[164:167], v[192:195]\n" \
M_"v[196:199], a[100:103], a[176:179], v[196:199]\n" \
X_"a[100:101], v10 offset:64\n" \
X_"a[102:103], v10 offset:4672\n" \
M_"v[196:199], a[104:107], a[180:183], v[196:199]\n" \
X_"a[104:105], v34 offset:64\n" \
X_"a[106:107], v34 offset:4672\n" \
M_"v[196:199], a[108:111], a[184:187], v[196:199]\n" \
B_"v[104:107], v32, s[56:59], s97 offen offset:0 sc1\n" \
M_"v[196:199], a[112:115], a[188:191], v[196:199]\n" \
M_"v[192:195], a[108:111], a[168:171], v[192:195]\n" \
X_"a[108:109], v34 offset:9280\n" \
X_"a[110:111], v34 offset:13888\n" \
M_"v[192:195], a[112:115], a[172:175], v[192:195]\n" \
X_"a[112:113], v34 offset:18496\n" \
"s_waitcnt lgkmcnt(14)\n" \
M_"v[192:195], a[116:119], a[192:195], v[192:195]\n" \
X_"a[114:115], v34 offset:23104\n" \
M_"v[192:195], a[120:123], a[196:199], v[192:195]\n" \
B_"v[108:111], v32, s[56:59], s97 offen offset:1024 sc1\n" \
M_"v[196:199], a[116:119], a[208:211], v[196:199]\n" \
X_"a[116:117], v10 offset:9280\n" \
X_"a[118:119], v10 offset:13888\n" \
M_"v[196:199], a[120:123], a[212:215], v[196:199]\n" \
X_"a[120:121], v33 offset:96\n" \
X_"a[122:123], v33 offset:4704\n" \
"s_waitcnt vmcnt(19)\n" \
"s_mov_b64 exec, 1\n" \
"buffer_store_dword v39, off, s[56:59], s71 offset:24 sc1\n" \
"s_mov_b64 exec, -1\n" \
M_"v[196:199], a[0:3], a[216:219], v[196:199]\n" \
M_"v[196:199], a[4:7], a[220:223], v[196:199]\n" \
M_"v[192:195], a[0:3], a[200:203], v[192:195]\n" \
X_"a[0:1], v33 offset:9312\n" \
X_"a[2:3], v33 offset:13920\n" \
M_"v[192:195], a[4:7], a[204:207], v[192:195]\n" \
X_"a[4:5], v33 offset:18528\n" \
X_"a[6:7], v33 offset:23136\n" \
M_"v[200:203], a[8:11], a[124:127], v[200:203]\n" \
M_"v[200:203], a[12:15], a[224:227], v[200:203]\n" \
M_"v[204:207], a[8:11], a[236:239], v[204:207]\n" \
X_"a[8:9], v33 offset:27744\n" \
X_"a[10:11], v33 offset:32352\n" \
M_"v[204:207], a[12:15], a[240:243], v[204:207]\n" \
C_"v192, v192, v193\n" \
C_"v193, v194, v195\n" \
C_"v194, v196, v197\n" \
C_"v195, v198, v199\n" \
"buffer_store_dwordx4 v[192:195], v20, s[56:59], s66 offen offset:1024 sc1\n" \
M_"v[204:207], a[16:19], a[244:247], v[204:207]\n" \
X_"a[12:13], v33 offset:36960\n" \
"s_waitcnt lgkmcnt(11)\n" \
M_"v[204:207], a[20:23], a[248:251], v[204:207]\n" \
X_"a[14:15], v33 offset:41568\n" \
M_"v[200:203], a[16:19], a[228:231], v[200:203]\n" \
X_"a[16:17], v33 offset:46176\n" \
X_"a[18:19], v33 offset:50784\n" \
M_"v[200:203], a[20:23], a[232:235], v[200:203]\n" \
X_"a[20:21], v33 offset:55392\n" \
X_"a[22:23], v33 offset:60000\n" \
M_"v[200:203], a[24:27], a[160:163], v[200:203]\n" \
M_"v[200:203], a[28:31], a[164:167], v[200:203]\n" \
M_"v[204:207], a[24:27], a[176:179], v[204:207]\n" \
X_"a[24:25], v10 offset:96\n" \
X_"a[26:27], v10 offset:4704\n" \
M_"v[204:207], a[28:31], a[180:183], v[204:207]\n" \
X_"a[28:29], v34 offset:96\n" \
X_"a[30:31], v34 offset:4704\n" \
M_"v[204:207], a[96:99], a[184:187], v[204:207]\n" \
M_"v[204:207], a[100:103], a[188:191], v[204:207]\n" \
M_"v[200:203], a[96:99], a[168:171], v[200:203]\n" \
X_"a[96:97], v34 offset:9312\n" \
X_"a[98:99], v34 offset:13920\n" \
M_"v[200:203], a[100:103], a[172:175], v[200:203]\n" \
X_"a[100:101], v34 offset:18528\n" \
X_"a[102:103], v34 offset:23136\n" \
M_"v[200:203], a[104:107], a[192:195], v[200:203]\n" \
M_"v[200:203], a[108:111], a[196:199], v[200:203]\n" \
M_"v[204:207], a[104:107], a[208:211], v[204:207]\n" \
X_"a[104:105], v10 offset:9312\n" \
X_"a[106:107], v10 offset:13920\n" \
M_"v[204:207], a[108:111], a[212:215], v[204:207]\n" \
X_"a[108:109], v33 offset:128\n" \
X_"a[110:111], v33 offset:4736\n" \
M_"v[204:207], a[112:115], a[216:219], v[204:207]\n" \
"s_waitcnt lgkmcnt(15)\n" \
M_"v[204:207], a[116:119], a[220:223], v[204:207]\n" \
M_"v[200:203], a[112:115], a[200:203], v[200:203]\n" \
X_"a[112:113], v33 offset:9344\n" \
X_"a[114:115], v33 offset:13952\n" \
M_"v[200:203], a[116:119], a[204:207], v[200:203]\n" \
X_"a[116:117], v33 offset:18560\n" \
X_"a[118:119], v33 offset:23168\n" \
M_"v[208:211], a[120:123], a[124:127], v[208:211]\n" \
M_"v[208:211], a[0:3], a[224:227], v[208:211]\n" \
M_"v[212:215], a[120:123], a[236:239], v[212:215]\n" \
X_"a[120:121], v33 offset:27776\n" \
X_"a[122:123], v33 offset:32384\n" \
M_"v[212:215], a[0:3], a[240:243], v[212:215]\n" \
C_"v200, v200, v201\n" \
C_"v201, v202, v203\n" \
C_"v202, v204, v205\n" \
C_"v203, v206, v207\n" \
"buffer_store_dwordx4 v[200:203], v20, s[56:59], s67 offen offset:1024 sc1\n" \
M_"v[212:215], a[4:7], a[244:247], v[212:215]\n" \
X_"a[0:1], v33 offset:36992\n" \
X_"a[2:3], v33 offset:41600\n" \
M_"v[212:215], a[8:11], a[248:251], v[212:215]\n" \
M_"v[208:211], a[4:7], a[228:231], v[208:211]\n" \
X_"a[4:5], v33 offset:46208\n" \
X_"a[6:7], v33 offset:50816\n" \
M_"v[208:211], a[8:11], a[232:235], v[208:211]\n" \
X_"a[8:9], v33 offset:55424\n" \
X_"a[10:11], v33 offset:60032\n" \
M_"v[208:211], a[12:15], a[160:163], v[208:211]\n" \
"s_waitcnt lgkmcnt(13)\n" \
M_"v[208:211], a[16:19], a[164:167], v[208:211]\n" \
M_"v[212:215], a[12:15], a[176:179], v[212:215]\n" \
"s_waitcnt vmcnt(8)\n" \
"v_cmp_eq_u32_e64 s[98:99], s62, v37\n" \
"s_mov_b64 exec, 3\n" \
"buffer_store_dword v39, v41, s[56:59], s73 offen sc1\n" \
"s_mov_b64 exec, -1\n" \
"s_andn2_b32 s75, 0x7f, s98\n" \
"s_mov_b32 s20, s75\n" \
"s_bitcmp1_b32 s20, 0\n" \
"s_cselect_b32 s93, 0x40000000, s12\n" \
"s_bitcmp1_b32 s20, 1\n" \
"s_cselect_b32 s94, 0x40000000, s12\n" \
"s_bitcmp1_b32 s20, 2\n" \
"s_cselect_b32 s95, 0x40000000, s13\n" \
"s_bitcmp1_b32 s20, 3\n" \
"s_cselect_b32 s96, 0x40000000, s13\n" \
"s_bitcmp1_b32 s20, 4\n" \
"s_cselect_b32 s97, 0x40000000, s14\n" \
"s_bitcmp1_b32 s20, 5\n" \
"s_cselect_b32 s64, 0x40000000, s14\n" \
"s_bitcmp1_b32 s20, 6\n" \
"s_cselect_b32 s65, 0x40000000, s15\n" \
"s_add_u32 s66, s66, 0x3000\n" \
"s_add_u32 s67, s67, 0x2800\n" \
M_"v[212:215], a[16:19], a[180:183], v[212:215]\n" \
B_"v[116:119], v20, s[56:59], s93 offen offset:0 sc1\n" \
M_"v[212:215], a[20:23], a[184:187], v[212:215]\n" \
X_"a[12:13], v10 offset:128\n" \
X_"a[14:15], v10 offset:4736\n" \
M_"v[212:215], a[24:27], a[188:191], v[212:215]\n" \
X_"a[16:17], v34 offset:128\n" \
X_"a[18:19], v34 offset:4736\n" \
M_"v[208:211], a[20:23], a[168:171], v[208:211]\n" \
X_"a[20:21], v34 offset:9344\n" \
X_"a[22:23], v34 offset:13952\n" \
M_"v[208:211], a[24:27], a[172:175], v[208:211]\n" \
X_"a[24:25], v34 offset:18560\n" \
X_"a[26:27], v34 offset:23168\n" \
M_"v[208:211], a[28:31], a[192:195], v[208:211]\n" \
M_"v[208:211], a[96:99], a[196:199], v[208:211]\n" \
M_"v[212:215], a[28:31], a[208:211], v[212:215]\n" \
X_"a[28:29], v10 offset:9344\n" \
X_"a[30:31], v10 offset:13952\n" \
M_"v[212:215], a[96:99], a[212:215], v[212:215]\n" \
X_"a[96:97], v33 offset:160\n" \
X_"a[98:99], v33 offset:4768\n" \
M_"v[212:215], a[100:103], a[216:219], v[212:215]\n" \
M_"v[212:215], a[104:107], a[220:223], v[212:215]\n" \
M_"v[208:211], a[100:103], a[200:203], v[208:211]\n" \
X_"a[100:101], v33 offset:9376\n" \
X_"a[102:103], v33 offset:13984\n" \
M_"v[208:211], a[104:107], a[204:207], v[208:211]\n" \
X_"a[104:105], v33 offset:18592\n" \
"s_waitcnt lgkmcnt(15)\n" \
M_"v[216:219], a[108:111], a[124:127], v[216:219]\n" \
X_"a[106:107], v33 offset:23200\n" \
M_"v[216:219], a[112:115], a[224:227], v[216:219]\n" \
B_"v[124:127], v20, s[56:59], s94 offen offset:2048 sc1\n" \
M_"v[220:223], a[108:111], a[236:239], v[220:223]\n" \
X_"a[108:109], v33 offset:27808\n" \
C_"v208, v208, v209\n" \
C_"v209, v210, v211\n" \
C_"v210, v212, v213\n" \
C_"v211, v214, v215\n" \
M_"v[220:223], a[112:115], a[240:243], v[220:223]\n" \
"buffer_store_dwordx4 v[208:211], v20, s[56:59], s68 offen offset:1024 sc1\n" \
"s_add_u32 s68, s68, 0x2000\n" \
M_"v[220:223], a[116:119], a[244:247], v[220:223]\n" \
X_"a[110:111], v33 offset:32416\n" \
X_"a[112:113], v33 offset:37024\n" \
M_"v[220:223], a[120:123], a[248:251], v[220:223]\n" \
X_"a[114:115], v33 offset:41632\n" \
M_"v[216:219], a[116:119], a[228:231], v[216:219]\n" \
X_"a[116:117], v33 offset:46240\n" \
X_"a[118:119], v33 offset:50848\n" \
M_"v[216:219], a[120:123], a[232:235], v[216:219]\n" \
X_"a[120:121], v33 offset:55456\n" \
X_"a[122:123], v33 offset:60064\n" \
M_"v[216:219], a[0:3], a[160:163], v[216:219]\n" \
M_"v[216:219], a[4:7], a[164:167], v[216:219]\n" \
M_"v[220:223], a[0:3], a[176:179], v[220:223]\n" \
X_"a[0:1], v10 offset:160\n" \
X_"a[2:3], v10 offset:4768\n" \
M_"v[220:223], a[4:7], a[180:183], v[220:223]\n" \
X_"a[4:5], v34 offset:160\n" \
X_"a[6:7], v34 offset:4768\n" \
M_"v[220:223], a[8:11], a[184:187], v[220:223]\n" \
"s_waitcnt lgkmcnt(13)\n" \
M_"v[220:223], a[12:15], a[188:191], v[220:223]\n" \
"s_waitcnt vmcnt(11)\n" \
"s_mov_b64 exec, 1\n" \
"buffer_store_dword v39, off, s[56:59], s92 offset:32 sc1\n" \
"s_mov_b64 exec, -1\n" \
M_"v[216:219], a[8:11], a[168:171], v[216:219]\n" \
X_"a[8:9], v34 offset:9376\n" \
X_"a[10:11], v34 offset:13984\n" \
M_"v[216:219], a[12:15], a[172:175], v[216:219]\n" \
X_"a[12:13], v34 offset:18592\n" \
X_"a[14:15], v34 offset:23200\n" \
M_"v[216:219], a[16:19], a[192:195], v[216:219]\n" \
M_"v[216:219], a[20:23], a[196:199], v[216:219]\n" \
B_"v[132:135], v20, s[56:59], s95 offen offset:0 sc1\n" \
M_"v[220:223], a[16:19], a[208:211], v[220:223]\n" \
X_"a[16:17], v10 offset:9376\n" \
X_"a[18:19], v10 offset:13984\n" \
M_"v[220:223], a[20:23], a[212:215], v[220:223]\n" \
X_"a[20:21], v33 offset:192\n" \
X_"a[22:23], v33 offset:4800\n" \
M_"v[220:223], a[24:27], a[216:219], v[220:223]\n" \
M_"v[220:223], a[28:31], a[220:223], v[220:223]\n" \
M_"v[216:219], a[24:27], a[200:203], v[216:219]\n" \
X_"a[24:25], v33 offset:9408\n" \
X_"a[26:27], v33 offset:14016\n" \
M_"v[216:219], a[28:31], a[204:207], v[216:219]\n" \
"buffer_load_dword v37, v40, s[56:59], s74 offen sc1\n" \
M_"v[224:227], a[96:99], a[124:127], v[224:227]\n" \
X_"a[28:29], v33 offset:18624\n" \
X_"a[30:31], v33 offset:23232\n" \
M_"v[224:227], a[100:103], a[224:227], v[224:227]\n" \
M_"v[228:231], a[96:99], a[236:239], v[228:231]\n" \
X_"a[96:97], v33 offset:27840\n" \
X_"a[98:99], v33 offset:32448\n" \
C_"v216, v216, v217\n" \
C_"v217, v218, v219\n" \
C_"v218, v220, v221\n" \
C_"v219, v222, v223\n" \
"buffer_store_dwordx4 v[216:219], v20, s[56:59], s69 offen offset:1024 sc1\n" \
"s_add_u32 s69, s69, 0x1800\n" \
M_"v[228:231], a[100:103], a[240:243], v[228:231]\n" \
X_"a[100:101], v33 offset:37056\n" \
"s_waitcnt lgkmcnt(12)\n" \
M_"v[228:231], a[104:107], a[244:247], v[228:231]\n" \
X_"a[102:103], v33 offset:41664\n" \
M_"v[228:231], a[108:111], a[248:251], v[228:231]\n" \
M_"v[224:227], a[104:107], a[228:231], v[224:227]\n" \
B_"v[140:143], v20, s[56:59], s96 offen offset:2048 sc1\n" \
M_"v[224:227], a[108:111], a[232:235], v[224:227]\n" \
X_"a[104:105], v33 offset:46272\n" \
X_"a[106:107], v33 offset:50880\n" \
M_"v[224:227], a[112:115], a[160:163], v[224:227]\n" \
X_"a[108:109], v33 offset:55488\n" \
X_"a[110:111], v33 offset:60096\n" \
M_"v[224:227], a[116:119], a[164:167], v[224:227]\n" \
M_"v[228:231], a[112:115], a[176:179], v[228:231]\n" \
X_"a[112:113], v10 offset:192\n" \
X_"a[114:115], v10 offset:4800\n" \
M_"v[228:231], a[116:119], a[180:183], v[228:231]\n" \
X_"a[116:117], v34 offset:192\n" \
X_"a[118:119], v34 offset:4800\n" \
M_"v[228:231], a[120:123], a[184:187], v[228:231]\n" \
M_"v[228:231], a[0:3], a[188:191], v[228:231]\n" \
M_"v[224:227], a[120:123], a[168:171], v[224:227]\n" \
X_"a[120:121], v34 offset:9408\n" \
X_"a[122:123], v34 offset:14016\n" \
M_"v[224:227], a[0:3], a[172:175], v[224:227]\n" \
X_"a[0:1], v34 offset:18624\n" \
X_"a[2:3], v34 offset:23232\n" \
"s_waitcnt vmcnt(10)\n" \
"s_mov_b64 exec, 1\n" \
"buffer_store_dword v39, off, s[56:59], s66 offset:36 sc1\n" \
"s_mov_b64 exec, -1\n" \
M_"v[224:227], a[4:7], a[192:195], v[224:227]\n" \
M_"v[224:227], a[8:11], a[196:199], v[224:227]\n" \
M_"v[228:231], a[4:7], a[208:211], v[228:231]\n" \
X_"a[4:5], v10 offset:9408\n" \
X_"a[6:7], v10 offset:14016\n" \
M_"v[228:231], a[8:11], a[212:215], v[228:231]\n" \
X_"a[8:9], v33 offset:224\n" \
"s_waitcnt lgkmcnt(15)\n" \
M_"v[228:231], a[12:15], a[216:219], v[228:231]\n" \
B_"v[148:151], v20, s[56:59], s97 offen offset:0 sc1\n" \
M_"v[228:231], a[16:19], a[220:223], v[228:231]\n" \
X_"a[10:11], v33 offset:4832\n" \
M_"v[224:227], a[12:15], a[200:203], v[224:227]\n" \
X_"a[12:13], v33 offset:9440\n" \
X_"a[14:15], v33 offset:14048\n" \
M_"v[224:227], a[16:19], a[204:207], v[224:227]\n" \
X_"a[16:17], v33 offset:18656\n" \
X_"a[18:19], v33 offset:23264\n" \
M_"v[232:235], a[20:23], a[124:127], v[232:235]\n" \
M_"v[232:235], a[24:27], a[224:227], v[232:235]\n" \
M_"v[236:239], a[20:23], a[236:239], v[236:239]\n" \
X_"a[20:21], v33 offset:27872\n" \
X_"a[22:23], v33 offset:32480\n" \
M_"v[236:239], a[24:27], a[240:243], v[236:239]\n" \
C_"v224, v224, v225\n" \
C_"v225, v226, v227\n" \
C_"v226, v228, v229\n" \
C_"v227, v230, v231\n" \
M_"v[236:239], a[28:31], a[244:247], v[236:239]\n" \
X_"a[24:25], v33 offset:37088\n" \
X_"a[26:27], v33 offset:41696\n" \
M_"v[236:239], a[96:99], a[248:251], v[236:239]\n" \
M_"v[232:235], a[28:31], a[228:231], v[232:235]\n" \
X_"a[28:29], v33 offset:46304\n" \
X_"a[30:31], v33 offset:50912\n" \
M_"v[232:235], a[96:99], a[232:235], v[232:235]\n" \
X_"a[96:97], v33 offset:55520\n" \
X_"a[98:99], v33 offset:60128\n" \
M_"v[232:235], a[100:103], a[160:163], v[232:235]\n" \
"s_waitcnt lgkmcnt(14)\n" \
M_"v[232:235], a[104:107], a[164:167], v[232:235]\n" \
B_"v[156:159], v20, s[56:59], s64 offen offset:2048 sc1\n" \
"v_readlane_b32 s64, v11, 54\n" \
M_"v[236:239], a[100:103], a[176:179], v[236:239]\n" \
X_"a[100:101], v10 offset:224\n" \
X_"a[102:103], v10 offset:4832\n" \
M_"v[236:239], a[104:107], a[180:183], v[236:239]\n" \
X_"a[104:105], v34 offset:224\n" \
X_"a[106:107], v34 offset:4832\n" \
"buffer_store_dwordx4 v[224:227], v20, s[56:59], s64 offen offset:1024 sc1\n" \
M_"v[236:239], a[108:111], a[184:187], v[236:239]\n" \
M_"v[236:239], a[112:115], a[188:191], v[236:239]\n" \
M_"v[232:235], a[108:111], a[168:171], v[232:235]\n" \
X_"a[108:109], v34 offset:9440\n" \
X_"a[110:111], v34 offset:14048\n" \
"s_waitcnt vmcnt(13)\n" \
"s_mov_b64 exec, 1\n" \
"buffer_store_dword v39, off, s[56:59], s67 offset:40 sc1\n" \
"s_mov_b64 exec, -1\n" \
M_"v[232:235], a[112:115], a[172:175], v[232:235]\n" \
X_"a[112:113], v34 offset:18656\n" \
X_"a[114:115], v34 offset:23264\n" \
M_"v[232:235], a[116:119], a[192:195], v[232:235]\n" \
M_"v[232:235], a[120:123], a[196:199], v[232:235]\n" \
M_"v[236:239], a[116:119], a[208:211], v[236:239]\n" \
X_"a[116:117], v10 offset:9440\n" \
X_"a[118:119], v10 offset:14048\n" \
M_"v[236:239], a[120:123], a[212:215], v[236:239]\n" \
X_"a[120:121], v36 offset:0\n" \
X_"a[122:123], v36 offset:1024\n" \
M_"v[236:239], a[0:3], a[216:219], v[236:239]\n" \
M_"v[236:239], a[4:7], a[220:223], v[236:239]\n" \
M_"v[232:235], a[0:3], a[200:203], v[232:235]\n" \
X_"a[0:1], v36 offset:2048\n" \
X_"a[2:3], v36 offset:3072\n" \
M_"v[232:235], a[4:7], a[204:207], v[232:235]\n" \
X_"a[4:5], v36 offset:32\n" \
B_"v[164:167], v20, s[56:59], s65 offen offset:0 sc1\n" \
"v_readlane_b32 s65, v11, 55\n" \
"s_waitcnt lgkmcnt(11)\n" \
M_"v[240:243], a[8:11], a[124:127], v[240:243]\n" \
X_"a[6:7], v36 offset:1056\n" \
M_"v[240:243], a[12:15], a[224:227], v[240:243]\n" \
M_"v[244:247], a[8:11], a[236:239], v[244:247]\n" \
C_"v232, v232, v233\n" \
C_"v233, v234, v235\n" \
C_"v234, v236, v237\n" \
C_"v235, v238, v239\n" \
"buffer_store_dwordx4 v[232:235], v20, s[56:59], s65 offen offset:1024 sc1\n" \
M_"v[244:247], a[12:15], a[240:243], v[244:247]\n" \
X_"a[8:9], v36 offset:2080\n" \
X_"a[10:11], v36 offset:3104\n" \
M_"v[244:247], a[16:19], a[244:247], v[244:247]\n" \
M_"v[244:247], a[20:23], a[248:251], v[244:247]\n" \
M_"v[240:243], a[16:19], a[228:231], v[240:243]\n" \
M_"v[240:243], a[20:23], a[232:235], v[240:243]\n" \
M_"v[168:171], a[24:27], a[128:131], v[168:171]\n" \
M_"v[168:171], a[28:31], a[132:135], v[168:171]\n" \
M_"v[172:175], a[24:27], a[144:147], v[172:175]\n" \
"s_waitcnt vmcnt(9)\n" \
"v_cmp_eq_u32_e64 s[98:99], s62, v37\n" \
"buffer_load_dword v37, v40, s[56:59], s74 offen sc1\n" \
"s_mov_b64 exec, 1\n" \
"buffer_store_dword v39, off, s[56:59], s68 offset:44 sc1\n" \
"s_mov_b64 exec, -1\n" \
"s_or_b32 s75, 0x300, s100\n" \
"s_andn2_b32 s75, s75, s98\n" \
"s_mov_b32 s21, s75\n" \
"s_bitcmp1_b32 s21, 16\n" \
"s_cselect_b32 s70, 0x40000000, s16\n" \
"s_bitcmp1_b32 s21, 17\n" \
"s_cselect_b32 s71, 0x40000000, s16\n" \
"s_bitcmp1_b32 s21, 18\n" \
"s_cselect_b32 s72, 0x40000000, s17\n" \
"s_bitcmp1_b32 s21, 19\n" \
"s_cselect_b32 s73, 0x40000000, s17\n" \
"s_bitcmp1_b32 s21, 20\n" \
"s_cselect_b32 s92, 0x40000000, s18\n" \
"s_bitcmp1_b32 s21, 21\n" \
"s_cselect_b32 s93, 0x40000000, s18\n" \
"s_bitcmp1_b32 s21, 22\n" \
"s_cselect_b32 s94, 0x40000000, s19\n" \
"s_bitcmp1_b32 s21, 23\n" \
"s_cselect_b32 s95, 0x40000000, s19\n" \
"s_bitcmp1_b32 s21, 8\n" \
"s_cselect_b32 s96, 0x40000000, s12\n" \
"s_bitcmp1_b32 s21, 9\n" \
"s_cselect_b32 s97, 0x40000000, s12\n" \
"s_add_u32 s64, s64, 0x1000\n" \
"s_add_u32 s65, s65, 0x800\n" \
M_"v[172:175], a[28:31], a[148:151], v[172:175]\n" \
B_"v[112:115], v20, s[56:59], s70 offen offset:0 sc1\n" \
M_"v[240:243], a[24:27], a[160:163], v[240:243]\n" \
M_"v[240:243], a[28:31], a[164:167], v[240:243]\n" \
M_"v[244:247], a[24:27], a[176:179], v[244:247]\n" \
M_"v[244:247], a[28:31], a[180:183], v[244:247]\n" \
B_"v[184:187], v20, s[56:59], s71 offen offset:2048 sc1\n" \
M_"v[244:247], a[96:99], a[184:187], v[244:247]\n" \
"v_readlane_b32 s71, v11, 57\n" \
M_"v[244:247], a[100:103], a[188:191], v[244:247]\n" \
M_"v[240:243], a[96:99], a[168:171], v[240:243]\n" \
M_"v[240:243], a[100:103], a[172:175], v[240:243]\n" \
B_"v[120:123], v20, s[56:59], s72 offen offset:0 sc1\n" \
M_"v[172:175], a[96:99], a[152:155], v[172:175]\n" \
"v_readlane_b32 s72, v11, 48\n" \
M_"v[172:175], a[100:103], a[156:159], v[172:175]\n" \
M_"v[168:171], a[96:99], a[136:139], v[168:171]\n" \
M_"v[168:171], a[100:103], a[140:143], v[168:171]\n" \
B_"v[192:195], v20, s[56:59], s73 offen offset:2048 sc1\n" \
M_"v[168:171], a[104:107], a[32:35], v[168:171]\n" \
"v_readlane_b32 s73, v11, 46\n" \
"s_waitcnt lgkmcnt(0)\n" \
M_"v[168:171], a[108:111], a[36:39], v[168:171]\n" \
M_"v[172:175], a[104:107], a[48:51], v[172:175]\n" \
M_"v[172:175], a[108:111], a[52:55], v[172:175]\n" \
B_"v[128:131], v20, s[56:59], s92 offen offset:0 sc1\n" \
M_"v[240:243], a[104:107], a[192:195], v[240:243]\n" \
"v_readlane_b32 s92, v11, 47\n" \
M_"v[240:243], a[108:111], a[196:199], v[240:243]\n" \
M_"v[244:247], a[104:107], a[208:211], v[244:247]\n" \
M_"v[244:247], a[108:111], a[212:215], v[244:247]\n" \
B_"v[200:203], v20, s[56:59], s93 offen offset:2048 sc1\n" \
M_"v[244:247], a[112:115], a[216:219], v[244:247]\n" \
M_"v[244:247], a[116:119], a[220:223], v[244:247]\n" \
M_"v[240:243], a[112:115], a[200:203], v[240:243]\n" \
M_"v[240:243], a[116:119], a[204:207], v[240:243]\n" \
".fill (L_ab + 45056 - .) / 4, 4, 0xBF800000\n" \
"L_ap11:\n" \
"s_cbranch_execz L_ap0\n" \
B_"v[176:179], v20, s[56:59], s94 offen offset:0 sc1\n" \
"s_waitcnt vmcnt(17)\n" \
"s_mov_b64 exec, 1\n" \
"buffer_store_dword v39, off, s[56:59], s69 offset:48 sc1\n" \
"s_mov_b64 exec, -1\n" \
M_"v[172:175], a[112:115], a[56:59], v[172:175]\n" \
M_"v[172:175], a[116:119], a[60:63], v[172:175]\n" \
M_"v[168:171], a[112:115], a[40:43], v[168:171]\n" \
M_"v[168:171], a[116:119], a[44:47], v[168:171]\n" \
B_"v[248:251], v20, s[56:59], s95 offen offset:2048 sc1\n" \
M_"v[168:171], a[120:123], v[64:67], v[168:171]\n" \
M_"v[172:175], a[120:123], v[72:75], v[172:175]\n" \
M_"v[240:243], a[120:123], v[80:83], v[240:243]\n" \
M_"v[244:247], a[120:123], v[88:91], v[244:247]\n" \
B_"v[188:191], v20, s[56:59], s96 offen offset:1024 sc1\n" \
M_"v[168:171], a[0:3], v[68:71], v[168:171]\n" \
M_"v[172:175], a[0:3], v[76:79], v[172:175]\n" \
M_"v[240:243], a[0:3], v[84:87], v[240:243]\n" \
M_"v[244:247], a[0:3], v[92:95], v[244:247]\n" \
B_"v[196:199], v20, s[56:59], s97 offen offset:3072 sc1\n" \
M_"v[56:59], a[4:7], v[96:99], 0\n" \
M_"v[60:63], a[4:7], v[104:107], 0\n" \
M_"v[56:59], a[8:11], v[100:103], v[56:59]\n" \
M_"v[60:63], a[8:11], v[108:111], v[60:63]\n" \
"s_waitcnt vmcnt(16)\n" \
"s_mov_b64 exec, 1\n" \
"buffer_store_dword v39, off, s[56:59], s64 offset:52 sc1\n" \
"s_mov_b64 exec, -1\n" \
"s_waitcnt vmcnt(14)\n" \
"s_mov_b64 exec, 1\n" \
"buffer_store_dword v39, off, s[56:59], s65 offset:56 sc1\n" \
"s_mov_b64 exec, -1\n" \
"s_cmp_lg_u32 s20, 0\n" \
"s_cbranch_scc1 L_sna_%=\n" \
"L_rna_%=:\n" \
"s_cmp_lg_u32 s21, 0\n" \
"s_cbranch_scc1 L_snx_%=\n" \
"L_rnx_%=:\n" \
"s_waitcnt vmcnt(14)\n" \
"v_cmp_eq_u32_e64 s[98:99], s62, v37\n" \
M_"v[168:171], v[50:53], v[116:119], v[168:171]\n" \
"s_andn2_b32 s75, 0x400, s98\n" \
"s_cbranch_scc1 L_sx0_%=\n" \
"L_rx0_%=:\n" \
"s_add_u32 s66, s63, 0x1000\n" \
M_"v[172:175], v[48:51], v[116:119], v[172:175]\n" \
B_"v[204:207], v20, s[56:59], s66 offen offset:1024 sc1\n" \
"s_andn2_b32 s75, 0x800, s98\n" \
"s_cbranch_scc1 L_sx1_%=\n" \
"L_rx1_%=:\n" \
"s_add_u32 s67, s63, 0x1800\n" \
M_"v[168:171], v[50:53], v[124:127], v[168:171]\n" \
M_"v[172:175], v[48:51], v[124:127], v[172:175]\n" \
M_"v[168:171], v[50:53], v[132:135], v[168:171]\n" \
B_"v[212:215], v20, s[56:59], s67 offen offset:1024 sc1\n" \
"s_andn2_b32 s75, 0x1000, s98\n" \
"s_cbranch_scc1 L_sx2_%=\n" \
"L_rx2_%=:\n" \
"s_add_u32 s68, s63, 0x2000\n" \
M_"v[172:175], v[48:51], v[132:135], v[172:175]\n" \
M_"v[168:171], v[50:53], v[140:143], v[168:171]\n" \
M_"v[172:175], v[48:51], v[140:143], v[172:175]\n" \
B_"v[220:223], v20, s[56:59], s68 offen offset:1024 sc1\n" \
"s_andn2_b32 s75, 0x2000, s98\n" \
"s_cbranch_scc1 L_sx3_%=\n" \
"L_rx3_%=:\n" \
"s_add_u32 s69, s63, 0x2800\n" \
M_"v[168:171], v[50:53], v[148:151], v[168:171]\n" \
M_"v[172:175], v[48:51], v[148:151], v[172:175]\n" \
M_"v[168:171], v[50:53], v[156:159], v[168:171]\n" \
B_"v[228:231], v20, s[56:59], s69 offen offset:1024 sc1\n" \
"s_andn2_b32 s75, 0x4000, s98\n" \
"s_cbranch_scc1 L_sx4_%=\n" \
"L_rx4_%=:\n" \
"s_add_u32 s70, s63, 0x3000\n" \
M_"v[172:175], v[48:51], v[156:159], v[172:175]\n" \
M_"v[168:171], v[50:53], v[164:167], v[168:171]\n" \
M_"v[172:175], v[48:51], v[164:167], v[172:175]\n" \
B_"v[236:239], v20, s[56:59], s70 offen offset:1024 sc1\n" \
"s_waitcnt vmcnt(17)\n" \
M_"v[56:59], v[50:53], v[112:115], v[56:59]\n" \
M_"v[60:63], v[48:51], v[112:115], v[60:63]\n" \
"s_waitcnt vmcnt(16)\n" \
M_"v[56:59], v[50:53], v[184:187], v[56:59]\n" \
M_"v[60:63], v[48:51], v[184:187], v[60:63]\n" \
C_"v168, v168, v169\n" \
"s_waitcnt vmcnt(15)\n" \
M_"v[56:59], v[50:53], v[120:123], v[56:59]\n" \
C_"v172, v172, v173\n" \
C_"v169, v170, v171\n" \
M_"v[60:63], v[48:51], v[120:123], v[60:63]\n" \
C_"v173, v174, v175\n" \
"s_waitcnt vmcnt(14)\n" \
M_"v[56:59], v[50:53], v[192:195], v[56:59]\n" \
"buffer_store_dwordx2 v[168:169], v42, s[88:91], s73 offen sc1\n" \
M_"v[60:63], v[48:51], v[192:195], v[60:63]\n" \
"s_waitcnt vmcnt(14)\n" \
M_"v[56:59], v[50:53], v[128:131], v[56:59]\n" \
"buffer_store_dwordx2 v[172:173], v43, s[88:91], s73 offen sc1\n" \
M_"v[60:63], v[48:51], v[128:131], v[60:63]\n" \
"s_waitcnt vmcnt(14)\n" \
M_"v[56:59], v[50:53], v[200:203], v[56:59]\n" \
M_"v[60:63], v[48:51], v[200:203], v[60:63]\n" \
"s_waitcnt vmcnt(13)\n" \
M_"v[56:59], v[50:53], v[176:179], v[56:59]\n" \
M_"v[60:63], v[48:51], v[176:179], v[60:63]\n" \
"s_waitcnt vmcnt(11)\n" \
M_"v[56:59], v[50:53], v[248:251], v[56:59]\n" \
M_"v[60:63], v[48:51], v[248:251], v[60:63]\n" \
"s_waitcnt vmcnt(10)\n" \
M_"v[240:243], v[50:53], v[188:191], v[240:243]\n" \
M_"v[244:247], v[48:51], v[188:191], v[244:247]\n" \
"s_waitcnt vmcnt(9)\n" \
M_"v[240:243], v[50:53], v[196:199], v[240:243]\n" \
M_"v[244:247], v[48:51], v[196:199], v[244:247]\n" \
"s_mov_b64 exec, 1\n" \
"buffer_store_dword v46, off, s[56:59], s71 offset:0 sc1\n" \
"s_mov_b64 exec, -1\n" \
C_"v56, v56, v57\n" \
C_"v60, v60, v61\n" \
C_"v57, v58, v59\n" \
C_"v61, v62, v63\n" \
"buffer_store_dwordx2 v[56:57], v42, s[88:91], s72 offen sc1\n" \
"buffer_store_dwordx2 v[60:61], v43, s[88:91], s72 offen sc1\n" \
"s_waitcnt vmcnt(9)\n" \
M_"v[240:243], v[50:53], v[204:207], v[240:243]\n" \
M_"v[244:247], v[48:51], v[204:207], v[244:247]\n" \
"s_waitcnt vmcnt(8)\n" \
M_"v[240:243], v[50:53], v[212:215], v[240:243]\n" \
M_"v[244:247], v[48:51], v[212:215], v[244:247]\n" \
"s_waitcnt vmcnt(7)\n" \
M_"v[240:243], v[50:53], v[220:223], v[240:243]\n" \
M_"v[244:247], v[48:51], v[220:223], v[244:247]\n" \
"s_waitcnt vmcnt(6)\n" \
M_"v[240:243], v[50:53], v[228:231], v[240:243]\n" \
M_"v[244:247], v[48:51], v[228:231], v[244:247]\n" \
"s_waitcnt vmcnt(5)\n" \
M_"v[240:243], v[50:53], v[236:239], v[240:243]\n" \
M_"v[244:247], v[48:51], v[236:239], v[244:247]\n" \
"s_nop 6\n" \
C_"v240, v240, v241\n" \
C_"v244, v244, v245\n" \
C_"v241, v242, v243\n" \
C_"v245, v246, v247\n" \
"buffer_store_dwordx2 v[240:241], v42, s[88:91], s92 offen sc1\n" \
"buffer_store_dwordx2 v[244:245], v43, s[88:91], s92 offen sc1\n" \
"L_fin_%=:\n"
#define ATT_VCLOB "v21","v22","v23","v24","v25","v26","v27","v28","v29","v32","v33","v34","v35","v36","v37","v38","v39","v40","v41","v42","v43","v44","v45","v46","v47","v48","v49","v50","v51","v52","v53","v54","v55","v56","v57","v58","v59","v60","v61","v62","v63","v64","v65","v66","v67","v68","v69","v70","v71","v72","v73","v74","v75","v76","v77","v78","v79","v80","v81","v82","v83","v84","v85","v86","v87","v88","v89","v90","v91","v92","v93","v94","v95","v96","v97","v98","v99","v100","v101","v102","v103","v104","v105","v106","v107","v108","v109","v110","v111","v112","v113","v114","v115","v116","v117","v118","v119","v120","v121","v122","v123","v124","v125","v126","v127","v128","v129","v130","v131","v132","v133","v134","v135","v136","v137","v138","v139","v140","v141","v142","v143","v144","v145","v146","v147","v148","v149","v150","v151","v152","v153","v154","v155","v156","v157","v158","v159","v160","v161","v162","v163","v164","v165","v166","v167","v168","v169","v170","v171","v172","v173","v174","v175","v176","v177","v178","v179","v180","v181","v182","v183","v184","v185","v186","v187","v188","v189","v190","v191","v192","v193","v194","v195","v196","v197","v198","v199","v200","v201","v202","v203","v204","v205","v206","v207","v208","v209","v210","v211","v212","v213","v214","v215","v216","v217","v218","v219","v220","v221","v222","v223","v224","v225","v226","v227","v228","v229","v230","v231","v232","v233","v234","v235","v236","v237","v238","v239","v240","v241","v242","v243","v244","v245","v246","v247","v248","v249","v250","v251","v252","v253","v254","v255"
#define ATT_SCLOB "s63","s64","s65","s66","s67","s68","s69","s70","s71","s72","s73","s74","s75","s76","s77","s78","s79","s80","s81","s82","s83","s84","s85","s86","s87","s88","s89","s90","s91","s92","s93","s95","s96","s97","s98","s99","s100","s101"
#define ATT_WTCH 0
#define ATT_PEEL 1
#define ATT_PEEL_FOW 1
#define ATT_PEEL_PRE 0
#define ATT_PEEL_ODD(b) (0u)
#define ATT_PAGES 11
#define ATT_TAILB 968
enum { T_fo, T_fj, T_fa, T_fas, T_fb, T_fbs, T_f8, T_mk8, T_wt, T_wol, T_woh, T_wbl, T_wbh, T_outl, T_outh, T_r1l, T_r1h, T_r1n, T_r1s, T_r2n, T_r2s, T_m2b, T_m0, T_m1, T_m1b, T_mw, T_m3, T_m4, T_ml, T_w0, T_w1, T_w2, T_w3, T_A1, T_A2, T_A3, T_B1, T_B2, T_B3, T_Ao, T_Bo, T_Al, T_Bl, T_l8, T_xr, T_x8r, T_oA, T_oB, T_o8, T_x0, T_x1, T_x2, T_x3, T_x4, T_x5, T_x6, T_x8, T_ep };

__device__ __forceinline__ void out_consts(u32x4 (&vp)[3], const u16* wo, const u16* wob, const u16* out, const unsigned b, const unsigned w, const unsigned lane) {
    const unsigned sp = b & 7u, nt = (b >> 3) & 3u, j = b >> 5, head = 4u * sp + nt, OOR = 0x40000000u;
    unsigned vw[9];
#pragma unroll
    for (unsigned i = 0; i < 9u; ++i) {
        const unsigned q = 64u * i + lane, row = __umul24(q, 3641u) >> 16, c = q - __umul24(row, 18u);
        vw[i] = __umul24(row, DM * 2) + (c < 16u ? (c * 16u + sp * 32u + 32u) & 255u : c * 16u) - (i & 3u) * 1024u;
    }
    const unsigned row = 16u * w + (lane >> 2), r9 = __umul24(16u * (row >> 5) + (row & 12u) + 2u * ((row >> 4) & 1u) + ((row >> 1) & 1u), 18u) + 16u + (row & 1u);
    unsigned vwl = __umul24(r9, DM * 2) + ((lane & 2u) ? 256u : sp * 32u) + (lane & 1u) * 16u, vrd = __umul24(lane >> 2, 288u) + (lane & 3u) * 8u;
    unsigned tab = 0;
#define TB(n, v) asm("v_writelane_b32 %0, %1, %2" : "+v"(tab) : "s"((unsigned)(v)), "n"((int)T_##n))
    const unsigned FJ = (unsigned)WS_F + (F_J + 640u * j) * 4u, mt = 2u * (w & 2u) + (w & 1u), w2 = w < 2u ? w : 2u, hw = head * 147456u;
    const unsigned FR = (unsigned)WS_F + F_R * 4u + (8u * j + sp) * 1024u;
    TB(fo, FJ + (ATT_PEEL_FOW ? 16u * head + 4u * w : 4u * head)); TB(fj, FJ); TB(fa, FR + (w | 2u) * 256u); TB(fas, FR + w * 256u + nt * 4u);
    TB(fb, FR + 128u + w * 256u); TB(fbs, FR + 128u + w * 256u + nt * 4u);
    TB(ep, ssel(b + w, OOR, (unsigned)WS_F + F_EPOCH * 4u));
    const u16* pwo = wo + nt * 144u; const u16* pwb = wob + nt * 144u;
    TB(wol, PLO(pwo)); TB(woh, PHI(pwo) & 0xffffu); TB(wbl, PLO(pwb)); TB(wbh, PHI(pwb) & 0xffffu); TB(outl, PLO(out)); TB(outh, PHI(out) & 0xffffu);
    const unsigned odd = ATT_PEEL_ODD(b), wo1 = w + 3u * odd;
#define OSEL(a, b_) (ATT_PEEL_ODD(1u) ? ssel(odd, a, b_) : (b_))
#define WU(u) (((4u * sp + ((nt + 1u + ((u) >> 2)) & 3u)) * 4u + ((u) & 3u)) * 36864u)
    TB(r1l, sslt<3>(wo1, PLO(pwo), PLO(pwb))); TB(r1h, sslt<3>(wo1, PHI(pwo), PHI(pwb)) & 0xffffu);
    TB(r1n, sslt<3>(wo1, 72u * sp + 71u, 4095u) * 1152u + 288u); TB(r1s, sslt<3>(wo1, (72u * sp + 32u * w) * 1152u, ATT_PEEL_ODD(1u) ? sslt<2>(w, WU(w), sslt<3>(w, OOR, hw + 110592u)) : hw + 110592u));
    if (ATT_PEEL_ODD(1u)) { TB(r2n, (72u * sp + 143u) * 1152u + 288u); TB(r2s, sslt<3>(w, (72u * sp + 72u + 32u * w) * 1152u, OOR)); TB(m2b, sslt<2>(w, 41984u + 9216u * w, 32768u)); }
    TB(m0, 131072u + 9216u * w2); TB(w0, hw + 36864u * w2); TB(m1, sslt<3>(w, 41984u + 9216u * w, 32768u)); TB(m1b, ssel(w ^ 2u, sslt<3>(w, 41984u + 9216u * w, 32768u), 114688u - 4096u));
    TB(mw, 65536u + 9216u * w); TB(m3, sslt<3>(w, 102400u + 9216u * w, 0u)); TB(m4, OSEL(9216u + 13312u * (w & 1u), sslt<3>(w, 131072u + 9216u * w, 9216u))); TB(ml, 18432u + 1024u * w);
    const unsigned tA = (unsigned)WS_AF + (8u * j + mt) * (KS_TOT * 1024u), tB = (unsigned)WS_AF + (120u - 8u * j + mt) * (KS_TOT * 1024u);
#define HK(k) (4u * sp + ((nt + k) & 3u))
    TB(w1, WU(w + 2u * odd)); TB(w2, WU(4u + w + 2u * odd)); TB(w3, WU(8u + (w | (2u * odd))));
    TB(A1, tA + 18432u + HK(1u) * 4096u); TB(A2, tA + 18432u + HK(2u) * 4096u); TB(A3, tA + 18432u + HK(3u) * 4096u);
    TB(B1, tB + 18432u + HK(1u) * 4096u); TB(B2, tB + 18432u + HK(2u) * 4096u); TB(B3, tB + 18432u + HK(3u) * 4096u);
    const unsigned pre = ATT_PEEL_PRE ? sslt<7>(j, tA + 18432u + head * 4096u, OOR) : OOR;
    TB(Ao, OSEL(pre, tA + sp * 2048u)); TB(Bo, OSEL(pre + 2048u, tB + sp * 2048u)); TB(Al, tA + 16384u); TB(Bl, tB + 16384u);
    const unsigned nred = w ^ (sp >> 1), blk = sp & 1u;
    const unsigned xg = (unsigned)WS_X + (4u * j + nt) * 589824u + w * 16384u, oA = (128u * j + 16u * mt) * 1152u + nt * 288u, oB = (1920u - 128u * j + 16u * mt) * 1152u + nt * 288u;
    TB(l8, ssel(nred, OOR, ssel(blk, tB, tA) + 16384u)); TB(xr, xg + sp * 65536u); TB(x8r, ssel(nred, OOR, xg + 524288u + (sp & 1u) * 1024u));
    TB(oA, oA + sp * 32u); TB(oB, oB + sp * 32u); TB(o8, ssel(nred, OOR, ssel(blk, oB, oA) + 256u)); TB(x8, xg + 524288u + sp * 2048u);
    TB(f8, xg + w * 131072u + 14400u + sp * 4u); TB(mk8, ssel(nred, 0u, 0xff0000u));
#define XI(i) TB(x##i, xg + ((sp + 1u + i) & 7u) * 65536u + i * 2048u)
    XI(0); XI(1); XI(2); XI(3); XI(4); XI(5); XI(6);
#undef XI
#undef HK
#undef WU
#undef TB
    vp[0] = (u32x4){vw[0], vw[1], vw[2], vw[3]}; vp[1] = (u32x4){vw[4], vw[5], vw[6], vw[7]}; vp[2] = (u32x4){vw[8], vwl, vrd, tab};
}
__global__ __launch_bounds__(256, 1) __attribute__((aligned(4096))) void fused_kernel(const u16* __restrict__ x, const u16* __restrict__ ckv, const u16* __restrict__ wq, const u16* __restrict__ wuk,
                                                       const u16* __restrict__ wuv, const u16* __restrict__ wqkv, const u16* __restrict__ gw,
                                                       const u16* __restrict__ wo, const u16* __restrict__ wob, char* __restrict__ ws_, u16* __restrict__ out) {
    using namespace att;
    extern __shared__ __attribute__((aligned(16))) char lds[];
    u32x4 sa[5]; u32x2 s24, s28; char* ws; unsigned tdum; kaptr ka = (kaptr)__builtin_amdgcn_kernarg_segment_ptr();
    asm volatile("s_load_dwordx8 s[8:15], %[ka], 0x0\n\ts_load_dwordx8 s[16:23], %[ka], 0x20\n\ts_getpc_b64 s[4:5]\n\tv_lshlrev_b32_e32 v191, 7, %[t]\n\ts_bfe_u32 s6, %[b], %[cb]\n\t"
                 "v_lshl_add_u32 v191, s6, %[cs], v191\n\tv_min_u32_e32 v191, %[e], v191\n\tglobal_load_dword v191, v191, s[4:5]\n\ts_load_dwordx4 s[24:27], %[ka], 0x40\n\ts_load_dwordx2 s[28:29], %[ka], 0x50"
                 : "={s[8:11]}"(sa[0]), "={s[12:15]}"(sa[1]), "={s[16:19]}"(sa[2]), "={s[20:23]}"(sa[3]), "={s[24:25]}"(s24), "={s[26:27]}"(ws), "={s[28:29]}"(s28), "={v191}"(tdum), [ka] "+{s[0:1]}"(ka)
                 : [t] "v"(threadIdx.x), [b] "s"(blockIdx.x), [e] "i"(CT_END), [cb] "i"(CT_END < 0x20000 ? 0x20003 : 0x30003), [cs] "i"(CT_END < 0x20000 ? 15 : 14) : "s4", "s5", "s6", "scc");
    SBAR();
    const unsigned wid = rfl(threadIdx.x >> 6);
    unsigned tag, vl;
    const int b = blockIdx.x;
    u32x4 vp[5], srdA; u32x2 sd;
    proj_phase(ka, ws, wid, vp, vl, sa, s24, s28, tdum, srdA, sd, tag
    );
    unsigned* const flags = (unsigned*)(ws + WS_F);
    RTS(2);
#define AST(L) do {} while (0)
#define ATT_TAIL "s_endpgm\n"
    unsigned abad;
    asm volatile(ATT_ASM ATT_TAIL "L_end_%=:\n s_nop 1"
                 : "={s94}"(abad)
                 : "{v[0:3]}"(vp[0]), "{v[4:7]}"(vp[1]), "{v[8:11]}"(vp[2]), "{v[12:15]}"(vp[3]), "{v[16:19]}"(vp[4]), "{v20}"(vl), "{s[56:59]}"(srdA), "{s[60:61]}"(sd), "{s62}"(tag),
                   "{s[8:11]}"(sa[0]), "{s[12:15]}"(sa[1]), "{s[16:19]}"(sa[2]), "{s[20:23]}"(sa[3]), "{s24}"(sa[4][0]), "{s3}"(wid), "{s2}"(b), "{s[28:29]}"(s28)
                 : "memory", "m0", "vcc", "scc", ATT_VCLOB, ATT_SCLOB, ACLOB_LO, ACLOB_HI);
    {
        unsigned b2 = b, w2 = wid;
        asm volatile("s_waitcnt vmcnt(0)" : "+s"(ka), "+s"(b2), "+s"(w2), "+s"(ws) :: "memory");
        const unsigned j2 = b2 >> 5, p2 = w2 & 1u, h2 = (b2 & 7u) * 4u + ((b2 >> 3) & 3u);
#pragma unroll 1
        for (unsigned i = w2 & 2u; i < ((w2 & 2u) ? (abad ? 8u : 4u) : 2u); ++i)
            att_exact(ka[0], ka[2], ws, h2, (i < 4u ? 1920u - 128u * j2 : 128u * j2 - 128u) + 32u * i + 16u * p2, *reinterpret_cast<const float*>(lds + LDS_G + (32u * i + 16u * p2 + (lane_id() & 15u)) * 4u));
        asm volatile("s_waitcnt vmcnt(0)" ::: "memory");
        unsigned* fa = (unsigned*)(ws + WS_F) + F_R + (8u * j2 + (b2 & 7u)) * 256u + (2u + p2) * 64u + ((b2 >> 3) & 3u);
        if (w2 & 2u) st_flag(fa, tag);
        wait_flags(fa, ld_flag(fa), tag);
        const unsigned l2 = lane_id(); u32x4 c[3]; out_consts(c, ka[7], ka[8], ka[10], b2, w2, l2);
        asm volatile("s_waitcnt vmcnt(0) lgkmcnt(0)\n\ts_nop 7\n\ts_branch L_att_stub" :: "{v[0:3]}"(c[0]), "{v[4:7]}"(c[1]), "{v[8:11]}"(c[2]), "{v20}"(l2 * 16u), "{s[56:59]}"(SRDA), "{s60}"(SD(8u * j2, h2, w2)),
                     "{s61}"(SD(120u - 8u * j2, h2, w2)), "{s62}"(tag), "{s3}"(w2), "{s2}"(b2) : "memory");
    }
    RTS(5);
}

extern "C" void kernel_launch(void* const* d_in, const int* in_sizes, int n_in, void* d_out, int out_size, void* d_ws, size_t ws_size,
                              hipStream_t stream) {
    (void)hipFuncSetAttribute((const void*)fused_kernel, hipFuncAttributeMaxDynamicSharedMemorySize, att::LDS_BYTES);
    fused_kernel<<<dim3(256), 256, att::LDS_BYTES, stream>>>((const u16*)d_in[0], (const u16*)d_in[4], (const u16*)d_in[5], (const u16*)d_in[6], (const u16*)d_in[7], (const u16*)d_in[1],
                                                             (const u16*)d_in[9], (const u16*)d_in[2], (const u16*)d_in[8], (char*)d_ws, (u16*)d_out);
}
```

```cpp
#include <hip/hip_runtime.h>
#include <hip/hip_bf16.h>
#include <stdint.h>
#include <utility>

typedef short bf16x8 __attribute__((ext_vector_type(8)));
typedef short s16x4 __attribute__((ext_vector_type(4)));
typedef float f32x16 __attribute__((ext_vector_type(16)));
typedef float f32x4 __attribute__((ext_vector_type(4)));
typedef unsigned u32x4 __attribute__((ext_vector_type(4)));
typedef unsigned u32x2 __attribute__((ext_vector_type(2)));
typedef unsigned short u16;

constexpr int S = 2048, DM = 576, NH = 32, HD = 128, DC = 512, NQKV = 1728, DHA = 18;
constexpr int NBIG = NH * HD;
constexpr int KS_TOT = 146;

#define SBAR() __builtin_amdgcn_sched_barrier(0)
constexpr size_t WS_Q = 0, WS_K = WS_Q + (size_t)S * NBIG * 2, WS_V = WS_K + (size_t)S * NBIG * 2, WS_AF = WS_V + (size_t)S * NBIG * 2, WS_X = WS_AF + (size_t)128 * KS_TOT * 1024,
                 WS_P = WS_X + (size_t)64 * 16 * 9216, WS_F = WS_P + (size_t)128 * (4 * 32 * 256 + 1024) * 4;
constexpr size_t WS_L = (size_t)96 << 20;
static_assert(WS_F == 96206848, "workspace map is baked into the generated asm");
constexpr int F_EPOCH = 0, F_J = 1024, F_ST = 8192, F_KV = 40960, F_R = 65536;
constexpr unsigned TAG_STEP = 0x9E3779B1u;
__device__ __forceinline__ unsigned ld_flag(const unsigned* p) { return __hip_atomic_load(p, __ATOMIC_RELAXED, __HIP_MEMORY_SCOPE_AGENT); }
__device__ __forceinline__ void st_flag(unsigned* p, unsigned v) { __hip_atomic_store(p, v, __ATOMIC_RELAXED, __HIP_MEMORY_SCOPE_AGENT); }
__device__ __forceinline__ void wait_flags(const unsigned* p, unsigned v, unsigned tag) {
    if (__builtin_expect(__builtin_amdgcn_ballot_w64(v != tag) != 0ull, 0)) {
        unsigned spins = 0;
        do { __builtin_amdgcn_s_sleep(1); } while (__builtin_amdgcn_ballot_w64(ld_flag(p) != tag) != 0ull && ++spins < (1u << 22));
    }
    asm volatile("" ::: "memory");
}

__device__ __forceinline__ unsigned lane_id() { unsigned l; asm volatile("v_mbcnt_lo_u32_b32 %0, -1, 0\n\tv_mbcnt_hi_u32_b32 %0, -1, %0" : "=v"(l)); return l; }
#define RTS(slot) do {} while (0)
#define CT_END ((PROJ_PAGES + ATT_PAGES) * 4096 + (ATT_TAILB & ~127) - 128)
#define ACLOB_LO "a0","a1","a2","a3","a4","a5","a6","a7","a8","a9","a10","a11","a12","a13","a14","a15","a16","a17","a18","a19","a20","a21","a22","a23","a24","a25","a26","a27","a28","a29","a30","a31","a32","a33","a34","a35","a36","a37","a38","a39","a40","a41","a42","a43","a44","a45","a46","a47","a48","a49","a50","a51","a52","a53","a54","a55","a56","a57","a58","a59","a60","a61","a62","a63","a64","a65","a66","a67","a68","a69","a70","a71","a72","a73","a74","a75","a76","a77","a78","a79","a80","a81","a82","a83","a84","a85","a86","a87","a88","a89","a90","a91","a92","a93","a94","a95","a96","a97","a98","a99","a100","a101","a102","a103","a104","a105","a106","a107","a108","a109","a110","a111","a112","a113","a114","a115","a116","a117","a118","a119","a120","a121","a122","a123","a124","a125","a126","a127"
#define ACLOB_HI "a128","a129","a130","a131","a132","a133","a134","a135","a136","a137","a138","a139","a140","a141","a142","a143","a144","a145","a146","a147","a148","a149","a150","a151","a152","a153","a154","a155","a156","a157","a158","a159","a160","a161","a162","a163","a164","a165","a166","a167","a168","a169","a170","a171","a172","a173","a174","a175","a176","a177","a178","a179","a180","a181","a182","a183","a184","a185","a186","a187","a188","a189","a190","a191","a192","a193","a194","a195","a196","a197","a198","a199","a200","a201","a202","a203","a204","a205","a206","a207","a208","a209","a210","a211","a212","a213","a214","a215","a216","a217","a218","a219","a220","a221","a222","a223","a224","a225","a226","a227","a228","a229","a230","a231","a232","a233","a234","a235","a236","a237","a238","a239","a240","a241","a242","a243","a244","a245","a246","a247","a248","a249","a250","a251","a252","a253","a254","a255"
#define M_ "v_mfma_f32_16x16x32_bf16 "
#define X_ "ds_read_b64_tr_b16 "
#define B_ "buffer_load_dwordx4 "
#define C_ "v_cvt_pk_bf16_f32 "
#define R_ "ds_read_b128 "
#define E_ "v_exp_f32_e32 "
#define PLO(p) ((unsigned)(size_t)(p))
#define PHI(p) ((unsigned)((size_t)(p) >> 32))
__device__ __forceinline__ unsigned vsel(bool c, unsigned a, unsigned b) { asm("" : "+v"(a), "+v"(b)); return c ? a : b; }
__device__ __forceinline__ unsigned ssel(unsigned c, unsigned a, unsigned b) { unsigned r; asm("s_cmp_lg_u32 %1, 0\n\ts_cselect_b32 %0, %2, %3" : "=s"(r) : "s"(c), "s"(a), "s"(b) : "scc"); return r; }
template <int N> __device__ __forceinline__ unsigned sslt(unsigned c, unsigned a, unsigned b) { unsigned r; asm("s_cmpk_lt_u32 %1, %4\n\ts_cselect_b32 %0, %2, %3" : "=s"(r) : "s"(c), "s"(a), "s"(b), "n"(N) : "scc"); return r; }
namespace att {
constexpr int LDS_BYTES = 163840;
constexpr unsigned LDS_G = 159744u, LDS_L = 161792u;
}
#define SD(J8, H, W) ((unsigned)WS_AF + (((J8) + 2u * ((W) & 2u) + ((W) & 1u)) * KS_TOT + 18u + 4u * (H)) * 1024u)
#define SRDA (u32x4){PLO(ws), PHI(ws) & 0xffffu, (unsigned)WS_L, 0x00020000u}
#define PROJ_ASM \
".p2align 3\n" \
"L_pb:\n" \
"s_waitcnt lgkmcnt(0)\n" \
"s_mov_b32 s64, s10\n" \
"s_and_b32 s65, s11, 0xffff\n" \
"s_mov_b32 s66, 0x200000\n" \
"s_mov_b32 s67, 0x20000\n" \
"s_mov_b32 s68, s14\n" \
"s_and_b32 s69, s15, 0xffff\n" \
"s_mov_b32 s70, 0x400000\n" \
"s_mov_b32 s71, 0x20000\n" \
"s_mov_b32 s72, s16\n" \
"s_and_b32 s73, s17, 0xffff\n" \
"s_mov_b32 s74, 0x400000\n" \
"s_mov_b32 s75, 0x20000\n" \
"s_add_u32 m0, s30, 0x0\n" \
"s_movk_i32 s84, 0x0\n" \
B_"v22, s[64:67], s84 offen offset:0 lds\n" \
"s_movk_i32 s84, 0x1c00\n" \
B_"v23, s[64:67], s84 offen offset:1024 lds\n" \
"s_movk_i32 s84, 0x3800\n" \
B_"v22, s[64:67], s84 offen offset:2048 lds\n" \
"s_movk_i32 s84, 0x5400\n" \
B_"v23, s[64:67], s84 offen offset:3072 lds\n" \
"s_add_u32 m0, s31, 0x0\n" \
"s_movk_i32 s84, 0x0\n" \
B_"v26, s[68:71], s84 offen offset:0 lds\n" \
"s_movk_i32 s84, 0x7c00\n" \
B_"v26, s[68:71], s84 offen offset:1024 lds\n" \
"s_addk_i32 s84, 0x7c00\n" \
B_"v27, s[68:71], s84 offen offset:2048 lds\n" \
"s_addk_i32 s84, 0x7c00\n" \
B_"v27, s[68:71], s84 offen offset:3072 lds\n" \
"s_movk_i32 s7, 0x300\n" \
"s_lshr_b32 s4, s2, 1\n" \
"s_and_b32 s4, s4, 60\n" \
"s_or_b32 s4, s4, s3\n" \
"v_lshrrev_b32_e32 v212, 4, v20\n" \
"v_min_u32_e32 v212, 59, v212\n" \
"s_cmpk_lt_u32 s2, 0x80\n" \
"s_cselect_b32 s6, s14, s16\n" \
"v_subrev_u32_e32 v214, 48, v212\n" \
"v_mov_b32_e32 v212, s10\n" \
"v_mov_b32_e32 v215, s6\n" \
"s_cmpk_lt_u32 s2, 0x80\n" \
"s_cselect_b32 s5, s15, s17\n" \
"v_cmp_gt_u32_e32 vcc, s7, v20\n" \
"v_mov_b32_e64 v213, s11\n" \
"v_bfe_u32 v216, v20, 4, 3\n" \
"v_cndmask_b32_e32 v212, v212, v215, vcc\n" \
"v_mov_b32_e32 v215, s5\n" \
"s_lshl_b32 s5, s3, 4\n" \
"v_cndmask_b32_e32 v213, v213, v215, vcc\n" \
"v_lshlrev_b32_e64 v215, 5, v20\n" \
"v_and_b32_e32 v215, 0x7000, v215\n" \
"v_lshl_add_u32 v215, s4, 6, v215\n" \
"s_lshl_b32 s4, s2, 3\n" \
"s_and_b32 s4, s4, 56\n" \
"v_or3_b32 v215, v215, s4, v216\n" \
"s_lshl_b32 s4, s2, 6\n" \
"s_or_b32 s6, s5, s4\n" \
"v_cmp_gt_u32_e64 s[4:5], 6, v214\n" \
"v_add_u32_e32 v215, 0x2000, v215\n" \
"s_nop 0\n" \
"v_cndmask_b32_e64 v216, 4, 2, s[4:5]\n" \
"v_add3_u32 v214, s6, v214, v216\n" \
"s_lshl_b32 s4, s3, 9\n" \
"v_cndmask_b32_e32 v214, v214, v215, vcc\n" \
"v_lshlrev_b32_e32 v214, 7, v214\n" \
"v_mov_b32_e64 v215, 0\n" \
"v_lshl_add_u64 v[184:185], v[212:213], 0, v[214:215]\n" \
"s_or_b32 s32, s4, 0x25000\n" \
"s_add_u32 m0, s30, 0x1000\n" \
"s_mov_b32 s84, 0x8000\n" \
B_"v22, s[64:67], s84 offen offset:0 lds\n" \
"s_and_b32 s33, s3, 1\n" \
"v_lshrrev_b32_e32 v212, 4, v20\n" \
"v_lshrrev_b32_e64 v214, 8, v20\n" \
"v_bfe_u32 v212, v212, 1, 3\n" \
"v_lshlrev_b32_e32 v215, 2, v20\n" \
"s_addk_i32 s84, 0x1c00\n" \
B_"v23, s[64:67], s84 offen offset:1024 lds\n" \
"v_lshrrev_b32_e64 v216, 1, v20\n" \
"v_bfe_u32 v213, v20, 4, 4\n" \
"s_lshl_b32 s4, s3, 5\n" \
"v_xor_b32_e32 v212, v212, v214\n" \
"v_lshlrev_b32_e32 v214, 11, v214\n" \
"s_addk_i32 s84, 0x1c00\n" \
B_"v22, s[64:67], s84 offen offset:2048 lds\n" \
"v_and_b32_e32 v215, 0x300, v215\n" \
"v_and_b32_e64 v217, 24, v216\n" \
"v_and_or_b32 v213, s4, 64, v213\n" \
"v_or3_b32 v214, v215, v214, v217\n" \
"s_lshl_b32 s4, s33, 14\n" \
"s_addk_i32 s84, 0x1c00\n" \
B_"v23, s[64:67], s84 offen offset:3072 lds\n" \
"v_and_b32_e32 v215, 0xe0, v216\n" \
"v_lshlrev_b32_e32 v213, 7, v213\n" \
"v_lshlrev_b32_e32 v212, 4, v212\n" \
"v_or3_b32 v214, v214, s4, v215\n" \
"v_or_b32_e64 v28, v212, v213\n" \
"s_add_u32 m0, s31, 0x4000\n" \
"s_movk_i32 s84, 0x0\n" \
B_"v26, s[72:75], s84 offen offset:0 lds\n" \
"v_add_u32_e32 v32, 0x10000, v214\n" \
"v_bitop3_b32 v29, v212, 64, v213 bitop3:0x36\n" \
"v_xor_b32_e32 v33, 0x20, v32\n" \
"v_xor_b32_e64 v34, 0x40, v32\n" \
"v_xor_b32_e32 v35, 0x60, v32\n" \
"s_movk_i32 s84, 0x7c00\n" \
B_"v26, s[72:75], s84 offen offset:1024 lds\n" \
"v_xor_b32_e32 v36, 0x80, v32\n" \
"v_xor_b32_e32 v37, 0xa0, v32\n" \
"v_xor_b32_e32 v38, 0xc0, v32\n" \
"v_xor_b32_e32 v39, 0xe0, v32\n" \
"s_mov_b32 s80, s26\n" \
"s_addk_i32 s84, 0x7c00\n" \
B_"v27, s[72:75], s84 offen offset:2048 lds\n" \
"s_and_b32 s81, s27, 0xffff\n" \
"s_mov_b32 s82, 0x10000000\n" \
"s_mov_b32 s83, 0x20000\n" \
"s_mov_b32 s76, s18\n" \
"s_and_b32 s77, s19, 0xffff\n" \
"s_addk_i32 s84, 0x7c00\n" \
B_"v27, s[72:75], s84 offen offset:3072 lds\n" \
"s_mov_b32 s78, 0x1e6000\n" \
"s_mov_b32 s79, 0x20000\n" \
"s_branch L_pgo_%=\n" \
"L_pret:\n" \
"s_setpc_b64 s[88:89]\n" \
"L_pgo_%=:\n" \
"s_mul_i32 s88, s33, 0xa000\n" \
"s_lshl_b32 s89, s3, 11\n" \
"s_and_b32 s89, s89, 0x1000\n" \
"s_add_u32 s88, s88, s89\n" \
"s_getpc_b64 s[86:87]\n" \
"L_hpa_%=:\n" \
"s_add_u32 s86, s86, L_pp1-L_hpa_%=\n" \
"s_addc_u32 s87, s87, 0\n" \
"s_add_u32 s86, s86, s88\n" \
"s_addc_u32 s87, s87, 0\n" \
"s_add_u32 m0, s30, 0x8000\n" \
"s_movk_i32 s84, 0x80\n" \
B_"v22, s[64:67], s84 offen offset:0 lds\n" \
"s_lshr_b32 s4, s2, 1\n" \
"s_and_b32 s4, s4, 0x7c\n" \
"v_bfe_u32 v212, v20, 4, 5\n" \
"s_movk_i32 s84, 0x1c80\n" \
B_"v23, s[64:67], s84 offen offset:1024 lds\n" \
"s_or_b32 s4, s4, s3\n" \
"v_min_u32_e32 v212, 26, v212\n" \
"v_mad_u64_u32 v[212:213], s[4:5], s4, 27, v[212:213]\n" \
"s_movk_i32 s84, 0x3880\n" \
B_"v22, s[64:67], s84 offen offset:2048 lds\n" \
"v_mul_u32_u24_e32 v213, 0xaaab, v212\n" \
"s_lshr_b32 s7, s2, 5\n" \
"v_lshrrev_b32_e32 v213, 18, v213\n" \
"s_movk_i32 s84, 0x5480\n" \
B_"v23, s[64:67], s84 offen offset:3072 lds\n" \
"s_mov_b32 s4, 0x1fffffa\n" \
"s_and_b32 s6, s2, 7\n" \
"v_mad_u64_u32 v[214:215], s[4:5], v213, s4, v[212:213]\n" \
"s_mov_b64 exec, 0\n" \
"s_swappc_b64 s[88:89], s[86:87]\n" \
"s_mov_b64 exec, -1\n" \
"s_add_u32 s86, s86, 0x2000\n" \
"s_addc_u32 s87, s87, 0\n" \
"s_add_u32 m0, s31, 0x8000\n" \
"s_mov_b32 s84, 0x80000\n" \
B_"v26, s[68:71], s84 offen offset:0 lds\n" \
"s_mulk_i32 s7, 0x90\n" \
"v_mul_u32_u24_e32 v213, 27, v213\n" \
"v_lshrrev_b32_e32 v214, 1, v214\n" \
"s_addk_i32 s84, 0x7c00\n" \
B_"v26, s[68:71], s84 offen offset:1024 lds\n" \
"v_and_b32_e32 v212, 1, v212\n" \
"s_add_u32 s4, s7, s20\n" \
"v_mul_u32_u24_e32 v214, 9, v214\n" \
"v_add3_u32 v212, v212, s6, v213\n" \
"s_addc_u32 s5, 0, s21\n" \
"s_addk_i32 s84, 0x7c00\n" \
B_"v27, s[68:71], s84 offen offset:2048 lds\n" \
"v_add_lshl_u32 v212, v212, v214, 7\n" \
"v_mov_b32_e64 v213, 0\n" \
"v_lshl_add_u64 v[186:187], s[18:19], 0, v[212:213]\n" \
"s_addk_i32 s84, 0x7c00\n" \
B_"v27, s[68:71], s84 offen offset:3072 lds\n" \
"s_bitcmp1_b32 s3, 0\n" \
"s_cselect_b32 s6, 0x8c, 0\n" \
"s_add_u32 s4, s4, s6\n" \
"v_mov_b32_e32 v212, v187\n" \
"s_addc_u32 s5, s5, 0\n" \
"s_add_u32 m0, s30, 0x9000\n" \
"s_mov_b32 s84, 0x8080\n" \
B_"v22, s[64:67], s84 offen offset:0 lds\n" \
"v_writelane_b32 v186, s4, 31\n" \
"v_writelane_b32 v212, s5, 31\n" \
"v_mov_b32_e32 v187, v212\n" \
"s_addk_i32 s84, 0x1c00\n" \
B_"v23, s[64:67], s84 offen offset:1024 lds\n" \
"s_lshl_b32 s35, s2, 10\n" \
"s_lshr_b32 s37, s2, 1\n" \
"s_mul_i32 s37, s37, 0x24800\n" \
"s_mov_b64 exec, 0\n" \
"s_swappc_b64 s[88:89], s[86:87]\n" \
"s_mov_b64 exec, -1\n" \
"s_add_u32 s86, s86, 0x2000\n" \
"s_addc_u32 s87, s87, 0\n" \
"s_addk_i32 s84, 0x1c00\n" \
B_"v22, s[64:67], s84 offen offset:2048 lds\n" \
"s_and_b32 s35, s35, 0x400\n" \
"s_lshl_b32 s36, s3, 8\n" \
"s_or_b32 s35, s37, s35\n" \
"s_addk_i32 s84, 0x1c00\n" \
B_"v23, s[64:67], s84 offen offset:3072 lds\n" \
"s_and_b32 s4, s2, 7\n" \
"s_lshr_b32 s6, s2, 5\n" \
"s_lshl_b32 s34, s2, 5\n" \
"s_add_u32 m0, s31, 0xc000\n" \
"s_mov_b32 s84, 0x80000\n" \
B_"v26, s[72:75], s84 offen offset:0 lds\n" \
"s_add_i32 s35, s35, s36\n" \
"s_lshl_b32 s7, s4, 10\n" \
"s_and_b32 s34, s34, 0x300\n" \
"s_addk_i32 s84, 0x7c00\n" \
B_"v26, s[72:75], s84 offen offset:1024 lds\n" \
"s_add_i32 s35, s35, 0x3004000\n" \
"s_lshl_b32 s37, s6, 13\n" \
"s_or_b32 s34, s7, s34\n" \
"s_addk_i32 s84, 0x7c00\n" \
B_"v27, s[72:75], s84 offen offset:2048 lds\n" \
"s_or_b32 s7, s37, s7\n" \
"v_mov_b32_e32 v213, s35\n" \
"s_or_b32 s7, s7, s36\n" \
"s_addk_i32 s84, 0x7c00\n" \
B_"v27, s[72:75], s84 offen offset:3072 lds\n" \
"s_lshr_b32 s5, s2, 3\n" \
"s_or_b32 s7, s7, 0x5c00000\n" \
"v_writelane_b32 v213, s7, 2\n" \
"s_mul_i32 s5, s5, 0x90000\n" \
"v_writelane_b32 v213, s7, 3\n" \
"s_lshl_b32 s7, s3, 14\n" \
"s_lshl_b32 s4, s4, 16\n" \
"s_add_i32 s5, s5, s7\n" \
"s_or_b32 s34, s34, 0x5be8000\n" \
"v_writelane_b32 v213, s34, 4\n" \
"s_add_i32 s4, s5, s4\n" \
"v_writelane_b32 v213, s34, 5\n" \
"s_add_i32 s4, s4, 0x4243800\n" \
"v_writelane_b32 v213, s4, 6\n" \
"s_mul_i32 s4, s6, 0xa00\n" \
"v_lshlrev_b32_e32 v212, 3, v20\n" \
"s_lshl_b32 s5, s3, 7\n" \
"v_and_b32_e32 v212, 0x80, v212\n" \
"s_add_i32 s4, s4, s5\n" \
"s_add_i32 s4, s4, 0x5bc0f80\n" \
"v_writelane_b32 v213, s4, 7\n" \
"v_add_u32_e32 v49, v213, v212\n" \
"s_mov_b32 m0, s32\n" \
"s_mov_b32 exec_lo, 0xffffff\n" \
"s_mov_b32 exec_hi, 0xfff0000\n" \
"global_load_lds_dword v[184:185], off\n" \
"s_mov_b64 exec, -1\n" \
"s_mov_b32 m0, s32\n" \
"s_mov_b64 exec, 0xff\n" \
"buffer_load_dword v49, s[80:83], 0 offen lds\n" \
"s_mov_b64 exec, -1\n" \
"s_mov_b32 s84, 0x5bc0000\n" \
"buffer_load_dword v180, off, s[80:83], s84 sc1\n" \
"s_bitcmp1_b32 s2, 0\n" \
"s_cbranch_scc1 L_sd_%=\n" \
"s_sleep 24\n" \
"L_sd_%=:\n" \
"s_cmp_lg_u32 s33, 0\n" \
"s_cbranch_scc1 L_w1_%=\n" \
"L_w0_%=:\n" \
"s_waitcnt vmcnt(19)\n" \
"s_barrier\n" \
X_"a[160:161], v32 offset:0\n" \
X_"a[162:163], v32 offset:1024\n" \
R_"a[128:131], v28 offset:16384\n" \
X_"a[224:225], v32 offset:8192\n" \
X_"a[226:227], v32 offset:9216\n" \
R_"a[192:195], v29 offset:16384\n" \
R_"a[132:135], v28 offset:18432\n" \
R_"a[196:199], v29 offset:18432\n" \
X_"a[164:165], v33 offset:0\n" \
X_"a[166:167], v33 offset:1024\n" \
X_"a[228:229], v33 offset:8192\n" \
X_"a[230:231], v33 offset:9216\n" \
R_"a[136:139], v28 offset:20480\n" \
R_"a[200:203], v29 offset:20480\n" \
"s_waitcnt lgkmcnt(11)\n" \
M_"a[0:3], a[160:163], a[128:131], 0\n" \
X_"a[168:169], v34 offset:0\n" \
"s_lshr_b32 s4, s2, 1\n" \
"s_waitcnt lgkmcnt(9)\n" \
M_"a[0:3], a[224:227], a[192:195], a[0:3]\n" \
X_"a[170:171], v34 offset:1024\n" \
"s_and_b32 s4, s4, 0x7c\n" \
"s_waitcnt lgkmcnt(9)\n" \
M_"a[16:19], a[160:163], a[132:135], 0\n" \
X_"a[232:233], v34 offset:8192\n" \
"v_mov_b32_e32 v213, s13\n" \
"s_waitcnt lgkmcnt(9)\n" \
M_"a[16:19], a[224:227], a[196:199], a[16:19]\n" \
X_"a[234:235], v34 offset:9216\n" \
"s_or_b32 s4, s4, s3\n" \
"s_waitcnt lgkmcnt(8)\n" \
M_"a[20:23], a[164:167], a[132:135], 0\n" \
R_"a[140:143], v28 offset:22528\n" \
"v_lshrrev_b32_e32 v212, 4, v20\n" \
"s_waitcnt lgkmcnt(7)\n" \
M_"a[20:23], a[228:231], a[196:199], a[20:23]\n" \
R_"a[204:207], v29 offset:22528\n" \
"v_mad_u64_u32 v[214:215], s[4:5], s4, 36, v[212:213]\n" \
M_"a[4:7], a[164:167], a[128:131], 0\n" \
X_"a[172:173], v35 offset:0\n" \
"v_mov_b32_e64 v217, s9\n" \
M_"a[4:7], a[228:231], a[192:195], a[4:7]\n" \
X_"a[174:175], v35 offset:1024\n" \
"s_movk_i32 s4, 0x240\n" \
"s_waitcnt lgkmcnt(9)\n" \
M_"a[32:35], a[160:163], a[136:139], 0\n" \
X_"a[236:237], v35 offset:8192\n" \
"v_cmp_gt_u32_e32 vcc, s4, v20\n" \
"s_waitcnt lgkmcnt(9)\n" \
M_"a[32:35], a[224:227], a[200:203], a[32:35]\n" \
X_"a[238:239], v35 offset:9216\n" \
"s_lshl_b32 s4, s2, 3\n" \
M_"a[36:39], a[164:167], a[136:139], 0\n" \
R_"a[144:147], v28 offset:0\n" \
"s_and_b32 s4, s4, 56\n" \
M_"a[36:39], a[228:231], a[200:203], a[36:39]\n" \
R_"a[208:211], v29 offset:0\n" \
"v_cndmask_b32_e32 v217, v217, v213, vcc\n" \
"s_waitcnt lgkmcnt(10)\n" \
M_"a[40:43], a[168:171], a[136:139], 0\n" \
R_"a[148:151], v28 offset:2048\n" \
"v_lshlrev_b32_e32 v213, 3, v214\n" \
"s_waitcnt lgkmcnt(9)\n" \
M_"a[40:43], a[232:235], a[200:203], a[40:43]\n" \
R_"a[212:215], v29 offset:2048\n" \
"v_and_b32_e32 v213, 0x1ffc0, v213\n" \
M_"a[24:27], a[168:171], a[132:135], 0\n" \
R_"a[152:155], v28 offset:4096\n" \
"v_and_b32_e64 v214, 7, v214\n" \
M_"a[24:27], a[232:235], a[196:199], a[24:27]\n" \
R_"a[216:219], v29 offset:4096\n" \
"v_or3_b32 v213, v214, s4, v213\n" \
M_"a[8:11], a[168:171], a[128:131], 0\n" \
R_"a[156:159], v28 offset:6144\n" \
"s_lshl_b32 s4, s2, 2\n" \
M_"a[8:11], a[232:235], a[192:195], a[8:11]\n" \
R_"a[220:223], v29 offset:6144\n" \
"s_mov_b32 m0, s32\n" \
"s_waitcnt lgkmcnt(13)\n" \
M_"a[48:51], a[160:163], a[140:143], 0\n" \
"s_mov_b32 exec_lo, 0xff000000\n" \
"s_mov_b32 exec_hi, 0x0\n" \
"global_load_lds_dword v[184:185], off\n" \
"s_mov_b64 exec, -1\n" \
"s_waitcnt lgkmcnt(12)\n" \
M_"a[48:51], a[224:227], a[204:207], a[48:51]\n" \
X_"a[176:177], v36 offset:0\n" \
"s_or_b32 s4, s4, s3\n" \
M_"a[52:55], a[164:167], a[140:143], 0\n" \
X_"a[178:179], v36 offset:1024\n" \
"s_mul_i32 s4, s4, 18\n" \
M_"a[52:55], a[228:231], a[204:207], a[52:55]\n" \
"s_mov_b32 m0, s32\n" \
X_"a[240:241], v36 offset:8192\n" \
M_"a[56:59], a[168:171], a[140:143], 0\n" \
"s_mov_b32 exec_lo, 0xffff\n" \
"s_mov_b32 exec_hi, 0x0\n" \
"global_load_lds_dword v[186:187], off\n" \
"s_mov_b64 exec, -1\n" \
M_"a[56:59], a[232:235], a[204:207], a[56:59]\n" \
X_"a[242:243], v36 offset:9216\n" \
"v_min_u32_e32 v212, 53, v212\n" \
"s_waitcnt lgkmcnt(14)\n" \
M_"a[60:63], a[172:175], a[140:143], 0\n" \
X_"a[180:181], v37 offset:0\n" \
"s_sub_i32 s4, s4, 36\n" \
"s_waitcnt lgkmcnt(13)\n" \
M_"a[60:63], a[236:239], a[204:207], a[60:63]\n" \
X_"a[182:183], v37 offset:1024\n" \
"v_add_u32_e32 v212, s4, v212\n" \
M_"a[44:47], a[172:175], a[136:139], 0\n" \
X_"a[244:245], v37 offset:8192\n" \
"v_mov_b32_e64 v215, s12\n" \
M_"a[44:47], a[236:239], a[200:203], a[44:47]\n" \
X_"a[246:247], v37 offset:9216\n" \
"v_mov_b32_e64 v216, s8\n" \
M_"a[28:31], a[172:175], a[132:135], 0\n" \
X_"a[184:185], v38 offset:0\n" \
"v_cndmask_b32_e64 v212, v212, v213, vcc\n" \
M_"a[28:31], a[236:239], a[196:199], a[28:31]\n" \
X_"a[186:187], v38 offset:1024\n" \
"v_cndmask_b32_e64 v216, v216, v215, vcc\n" \
M_"a[12:15], a[172:175], a[128:131], 0\n" \
"s_mov_b64 exec, 0\n" \
"s_swappc_b64 s[88:89], s[86:87]\n" \
"s_mov_b64 exec, -1\n" \
"s_add_u32 s86, s86, 0x2000\n" \
"s_addc_u32 s87, s87, 0\n" \
M_"a[12:15], a[236:239], a[192:195], a[12:15]\n" \
"v_lshlrev_b32_e32 v212, 7, v212\n" \
"v_mov_b32_e32 v213, 0\n" \
"s_waitcnt lgkmcnt(15)\n" \
M_"a[76:79], a[172:175], a[144:147], 0\n" \
X_"a[248:249], v38 offset:8192\n" \
"v_lshl_add_u64 v[188:189], v[216:217], 0, v[212:213]\n" \
M_"a[76:79], a[236:239], a[208:211], a[76:79]\n" \
"s_movk_i32 s5, 0xd80\n" \
"s_lshr_b32 s6, s2, 5\n" \
M_"a[72:75], a[168:171], a[144:147], 0\n" \
"v_lshrrev_b32_e32 v212, 4, v20\n" \
"v_lshl_or_b32 v215, s3, 6, v212\n" \
M_"a[72:75], a[232:235], a[208:211], a[72:75]\n" \
X_"a[250:251], v38 offset:9216\n" \
"v_mul_u32_u24_e32 v216, 0x2aab, v215\n" \
M_"a[68:71], a[164:167], a[144:147], 0\n" \
"v_lshrrev_b32_e64 v216, 16, v216\n" \
"v_mad_i32_i24 v217, v216, -6, v215\n" \
M_"a[68:71], a[228:231], a[208:211], a[68:71]\n" \
"v_add_u32_e32 v218, 0xfffffe50, v215\n" \
"s_mul_i32 s35, s6, 0x48\n" \
M_"a[64:67], a[160:163], a[144:147], 0\n" \
"v_lshrrev_b32_e32 v219, 1, v217\n" \
"v_add_u32_e32 v218, s35, v216\n" \
M_"a[64:67], a[224:227], a[208:211], a[64:67]\n" \
X_"a[188:189], v39 offset:0\n" \
"v_mul_u32_u24_e32 v219, 0x480, v219\n" \
M_"a[80:83], a[160:163], a[148:151], 0\n" \
"v_and_b32_e64 v214, 16, v20\n" \
"v_mad_u32_u24 v219, v218, s5, v219\n" \
"s_waitcnt lgkmcnt(15)\n" \
M_"a[80:83], a[224:227], a[212:215], a[80:83]\n" \
"v_or_b32_e32 v192, v219, v214\n" \
"v_mov_b32_e64 v220, 2.0\n" \
M_"a[84:87], a[164:167], a[148:151], 0\n" \
X_"a[190:191], v39 offset:1024\n" \
"v_mov_b32_e64 v222, 7\n" \
M_"a[84:87], a[228:231], a[212:215], a[84:87]\n" \
"v_lshlrev_b32_e32 v220, 7, v218\n" \
"v_lshrrev_b32_e32 v218, 1, v218\n" \
M_"a[88:91], a[168:171], a[148:151], 0\n" \
"v_and_b32_e32 v218, 4, v218\n" \
"v_and_b32_e32 v216, 2, v216\n" \
M_"a[88:91], a[232:235], a[212:215], a[88:91]\n" \
X_"a[252:253], v39 offset:8192\n" \
"s_mul_i32 s7, s6, 0x2400\n" \
M_"a[92:95], a[172:175], a[148:151], 0\n" \
"s_sub_i32 s34, 0x22000, s7\n" \
"v_bitop3_b32 v216, v217, v218, v216 bitop3:0x1e\n" \
M_"a[92:95], a[236:239], a[212:215], a[92:95]\n" \
"v_or_b32_e32 v213, 0x24400, v20\n" \
"v_lshlrev_b32_e64 v216, 4, v216\n" \
M_"a[108:111], a[172:175], a[152:155], 0\n" \
X_"a[254:255], v39 offset:9216\n" \
"v_add3_u32 v196, v220, s34, v216\n" \
M_"a[108:111], a[236:239], a[216:219], a[108:111]\n" \
"v_mov_b32_e32 v216, v213\n" \
"s_movk_i32 s36, 0x1b0\n" \
M_"a[104:107], a[168:171], a[152:155], 0\n" \
"s_mov_b64 exec, 0\n" \
"s_swappc_b64 s[88:89], s[86:87]\n" \
"s_mov_b64 exec, -1\n" \
"s_andn2_b32 s85, 2, s3\n" \
"s_lshl_b32 s85, s85, 12\n" \
"s_add_u32 s86, s86, s85\n" \
"s_addc_u32 s87, s87, 0\n" \
M_"a[104:107], a[232:235], a[216:219], a[104:107]\n" \
"v_or_b32_e32 v216, 0x100, v215\n" \
"v_mul_u32_u24_e32 v217, 0x2aab, v216\n" \
M_"a[100:103], a[164:167], a[152:155], 0\n" \
"v_add_u32_e32 v215, 0xffffff50, v215\n" \
"v_lshrrev_b32_e32 v217, 16, v217\n" \
M_"a[100:103], a[228:231], a[216:219], a[100:103]\n" \
"v_mov_b32_e64 v220, v215\n" \
"v_mad_i32_i24 v218, v217, -6, v216\n" \
M_"a[96:99], a[160:163], a[152:155], 0\n" \
"v_cmp_gt_u32_e32 vcc, s36, v216\n" \
"v_mov_b32_e32 v221, 7\n" \
M_"a[96:99], a[224:227], a[216:219], a[96:99]\n" \
"v_lshrrev_b32_e32 v222, 1, v218\n" \
"v_cndmask_b32_e32 v217, v220, v217, vcc\n" \
"s_waitcnt lgkmcnt(15)\n" \
M_"a[112:115], a[160:163], a[156:159], 0\n" \
"s_movk_i32 s4, 0x48\n" \
"v_add_u32_e64 v220, s35, v217\n" \
M_"a[112:115], a[224:227], a[220:223], a[112:115]\n" \
"v_mul_u32_u24_e32 v222, 0x480, v222\n" \
"v_mad_u32_u24 v222, v220, s5, v222\n" \
M_"a[116:119], a[164:167], a[156:159], 0\n" \
"v_cmp_gt_u32_e64 s[4:5], s4, v215\n" \
"v_cndmask_b32_e64 v215, v221, v218, vcc\n" \
M_"a[116:119], a[228:231], a[220:223], a[116:119]\n" \
"v_lshrrev_b32_e32 v218, 1, v220\n" \
"v_or_b32_e32 v214, v222, v214\n" \
M_"a[120:123], a[168:171], a[156:159], 0\n" \
"v_mov_b32_e32 v222, 2.0\n" \
"v_and_b32_e32 v218, 4, v218\n" \
M_"a[120:123], a[232:235], a[220:223], a[120:123]\n" \
"v_and_b32_e64 v217, 2, v217\n" \
"v_bitop3_b32 v215, v215, v218, v217 bitop3:0x1e\n" \
M_"a[124:127], a[172:175], a[156:159], 0\n" \
"v_lshlrev_b32_e32 v223, 1, v220\n" \
"v_mov_b32_e32 v224, 2.0\n" \
"s_waitcnt vmcnt(5) lgkmcnt(0)\n" \
M_"a[124:127], a[236:239], a[220:223], a[124:127]\n" \
"s_barrier\n" \
M_"v[52:55], a[176:179], a[128:131], 0\n" \
"s_add_u32 m0, s30, 0x0\n" \
"s_movk_i32 s84, 0x100\n" \
M_"v[52:55], a[240:243], a[192:195], v[52:55]\n" \
B_"v22, s[64:67], s84 offen offset:0 lds\n" \
M_"v[56:59], a[180:183], a[128:131], 0\n" \
X_"a[160:161], v32 offset:32768\n" \
"v_cndmask_b32_e64 v193, v222, v214, vcc\n" \
M_"v[56:59], a[244:247], a[192:195], v[56:59]\n" \
"s_movk_i32 s84, 0x1d00\n" \
M_"v[60:63], a[184:187], a[128:131], 0\n" \
B_"v23, s[64:67], s84 offen offset:1024 lds\n" \
M_"v[60:63], a[248:251], a[192:195], v[60:63]\n" \
X_"a[162:163], v32 offset:33792\n" \
"v_lshlrev_b32_e32 v214, 7, v220\n" \
M_"v[64:67], a[188:191], a[128:131], 0\n" \
X_"a[224:225], v32 offset:40960\n" \
"v_lshlrev_b32_e64 v215, 4, v215\n" \
M_"v[64:67], a[252:255], a[192:195], v[64:67]\n" \
R_"a[128:131], v28 offset:49152\n" \
"s_mul_i32 s39, s3, 18\n" \
M_"v[80:83], a[188:191], a[132:135], 0\n" \
R_"a[192:195], v29 offset:49152\n" \
"v_add3_u32 v214, v214, s34, v215\n" \
M_"v[80:83], a[252:255], a[196:199], v[80:83]\n" \
"s_movk_i32 s84, 0x3900\n" \
M_"v[76:79], a[184:187], a[132:135], 0\n" \
B_"v22, s[64:67], s84 offen offset:2048 lds\n" \
M_"v[76:79], a[248:251], a[196:199], v[76:79]\n" \
"s_movk_i32 s84, 0x5500\n" \
M_"v[72:75], a[180:183], a[132:135], 0\n" \
B_"v23, s[64:67], s84 offen offset:3072 lds\n" \
M_"v[72:75], a[244:247], a[196:199], v[72:75]\n" \
"s_add_u32 m0, s31, 0x0\n" \
"s_mov_b32 s84, 0x100000\n" \
M_"v[68:71], a[176:179], a[132:135], 0\n" \
B_"v26, s[68:71], s84 offen offset:0 lds\n" \
M_"v[68:71], a[240:243], a[196:199], v[68:71]\n" \
R_"a[132:135], v28 offset:51200\n" \
"v_cndmask_b32_e64 v195, v224, v223, s[4:5]\n" \
M_"v[84:87], a[176:179], a[136:139], 0\n" \
R_"a[196:199], v29 offset:51200\n" \
"s_movk_i32 s4, 0x1f8\n" \
M_"v[84:87], a[240:243], a[200:203], v[84:87]\n" \
"s_addk_i32 s84, 0x7c00\n" \
M_"v[88:91], a[180:183], a[136:139], 0\n" \
B_"v26, s[68:71], s84 offen offset:1024 lds\n" \
M_"v[88:91], a[244:247], a[200:203], v[88:91]\n" \
X_"a[226:227], v32 offset:41984\n" \
"v_mov_b32_e64 v215, v213\n" \
M_"v[92:95], a[184:187], a[136:139], 0\n" \
"s_addk_i32 s84, 0x7c00\n" \
M_"v[92:95], a[248:251], a[200:203], v[92:95]\n" \
B_"v27, s[68:71], s84 offen offset:2048 lds\n" \
M_"v[96:99], a[188:191], a[136:139], 0\n" \
X_"a[164:165], v33 offset:32768\n" \
"s_add_i32 s35, s35, s39\n" \
M_"v[96:99], a[252:255], a[200:203], v[96:99]\n" \
R_"a[136:139], v28 offset:53248\n" \
"v_cmp_gt_u32_e64 vcc, s4, v216\n" \
M_"v[112:115], a[188:191], a[140:143], 0\n" \
R_"a[200:203], v29 offset:53248\n" \
"v_add_u32_e64 v212, s35, v212\n" \
M_"v[112:115], a[252:255], a[204:207], v[112:115]\n" \
"s_addk_i32 s84, 0x7c00\n" \
M_"v[108:111], a[184:187], a[140:143], 0\n" \
B_"v27, s[68:71], s84 offen offset:3072 lds\n" \
"s_mov_b64 exec, 0\n" \
"s_swappc_b64 s[88:89], s[86:87]\n" \
"s_mov_b64 exec, -1\n" \
M_"v[108:111], a[248:251], a[204:207], v[108:111]\n" \
"s_add_u32 m0, s30, 0x1000\n" \
"s_mov_b32 s84, 0x8100\n" \
M_"v[104:107], a[180:183], a[140:143], 0\n" \
B_"v22, s[64:67], s84 offen offset:0 lds\n" \
M_"v[104:107], a[244:247], a[204:207], v[104:107]\n" \
X_"a[166:167], v33 offset:33792\n" \
"v_mov_b32_e64 v219, 2.0\n" \
M_"v[100:103], a[176:179], a[140:143], 0\n" \
X_"a[228:229], v33 offset:40960\n" \
"v_cndmask_b32_e64 v197, v215, v214, vcc\n" \
M_"v[100:103], a[240:243], a[204:207], v[100:103]\n" \
R_"a[140:143], v28 offset:55296\n" \
"s_movk_i32 s35, 0x120\n" \
M_"v[116:119], a[176:179], a[144:147], 0\n" \
R_"a[204:207], v29 offset:55296\n" \
"v_mul_u32_u24_e32 v214, 0xd80, v212\n" \
".fill (L_pb + 3808 - .) / 4, 4, 0xBF800000\n" \
"L_pp1:\n" \
"s_cbranch_execz L_pret\n" \
M_"v[116:119], a[240:243], a[208:211], v[116:119]\n" \
"s_addk_i32 s84, 0x1c00\n" \
M_"v[120:123], a[180:183], a[144:147], 0\n" \
B_"v23, s[64:67], s84 offen offset:1024 lds\n" \
M_"v[120:123], a[244:247], a[208:211], v[120:123]\n" \
"s_addk_i32 s84, 0x1c00\n" \
M_"v[124:127], a[184:187], a[144:147], 0\n" \
B_"v22, s[64:67], s84 offen offset:2048 lds\n" \
M_"v[124:127], a[248:251], a[208:211], v[124:127]\n" \
"s_addk_i32 s84, 0x1c00\n" \
M_"v[128:131], a[188:191], a[144:147], 0\n" \
B_"v23, s[64:67], s84 offen offset:3072 lds\n" \
M_"v[128:131], a[252:255], a[208:211], v[128:131]\n" \
R_"a[144:147], v28 offset:32768\n" \
"v_cmp_gt_u32_e64 vcc, s35, v20\n" \
M_"v[144:147], a[188:191], a[148:151], 0\n" \
R_"a[208:211], v29 offset:32768\n" \
"v_lshrrev_b32_e64 v215, 1, v212\n" \
M_"v[144:147], a[252:255], a[212:215], v[144:147]\n" \
"s_add_u32 m0, s31, 0x4000\n" \
"s_mov_b32 s84, 0x100000\n" \
M_"v[140:143], a[184:187], a[148:151], 0\n" \
B_"v26, s[72:75], s84 offen offset:0 lds\n" \
M_"v[140:143], a[248:251], a[212:215], v[140:143]\n" \
X_"a[230:231], v33 offset:41984\n" \
"s_and_b32 s4, s2, 7\n" \
M_"v[136:139], a[180:183], a[148:151], 0\n" \
"s_addk_i32 s84, 0x7c00\n" \
M_"v[136:139], a[244:247], a[212:215], v[136:139]\n" \
B_"v26, s[72:75], s84 offen offset:1024 lds\n" \
M_"v[132:135], a[176:179], a[148:151], 0\n" \
X_"a[168:169], v34 offset:32768\n" \
"v_cndmask_b32_e64 v194, v219, v214, vcc\n" \
M_"v[132:135], a[240:243], a[212:215], v[132:135]\n" \
R_"a[148:151], v28 offset:34816\n" \
"v_lshlrev_b32_e64 v214, 7, v212\n" \
M_"v[148:151], a[176:179], a[152:155], 0\n" \
R_"a[212:215], v29 offset:34816\n" \
"v_lshrrev_b32_e64 v212, 2, v212\n" \
M_"v[148:151], a[240:243], a[216:219], v[148:151]\n" \
X_"a[170:171], v34 offset:33792\n" \
"v_and_b32_e64 v212, 2, v212\n" \
M_"v[152:155], a[180:183], a[152:155], 0\n" \
X_"a[232:233], v34 offset:40960\n" \
"v_and_or_b32 v212, v215, 1, v212\n" \
M_"v[152:155], a[244:247], a[216:219], v[152:155]\n" \
X_"a[234:235], v34 offset:41984\n" \
"s_lshl_b32 s5, s4, 2\n" \
M_"v[156:159], a[184:187], a[152:155], 0\n" \
X_"a[172:173], v35 offset:32768\n" \
"s_bfe_u32 s38, s2, 0x20003\n" \
M_"v[156:159], a[248:251], a[216:219], v[156:159]\n" \
X_"a[174:175], v35 offset:33792\n" \
"v_lshlrev_b32_e32 v212, 5, v212\n" \
M_"v[160:163], a[188:191], a[152:155], 0\n" \
X_"a[236:237], v35 offset:40960\n" \
"s_movk_i32 s35, 0x60\n" \
M_"v[160:163], a[252:255], a[216:219], v[160:163]\n" \
R_"a[152:155], v28 offset:36864\n" \
"s_or_b32 s5, s5, s38\n" \
M_"v[176:179], a[188:191], a[156:159], 0\n" \
R_"a[216:219], v29 offset:36864\n" \
"v_bitop3_b32 v212, v212, s35, v214 bitop3:0x36\n" \
M_"v[176:179], a[252:255], a[220:223], v[176:179]\n" \
X_"a[238:239], v35 offset:41984\n" \
"s_lshr_b32 s36, s2, 3\n" \
M_"v[172:175], a[184:187], a[156:159], 0\n" \
"v_add_u32_e32 v212, s34, v212\n" \
"s_mul_i32 s34, s5, 0x12000\n" \
M_"v[172:175], a[248:251], a[220:223], v[172:175]\n" \
"s_or_b32 s40, s34, 0x6000000\n" \
"s_mul_i32 s34, s36, 0x90000\n" \
M_"v[168:171], a[180:183], a[156:159], 0\n" \
"s_lshl_b32 s36, s3, 14\n" \
"s_add_i32 s34, s34, s36\n" \
M_"v[168:171], a[244:247], a[220:223], v[168:171]\n" \
"s_lshl_b32 s38, s5, 8\n" \
"s_add_i32 s41, s34, 0x4240000\n" \
M_"v[164:167], a[176:179], a[156:159], 0\n" \
"s_mul_i32 s34, s5, 36\n" \
"s_add_i32 s5, s7, s40\n" \
M_"v[164:167], a[240:243], a[220:223], v[164:167]\n" \
R_"a[156:159], v28 offset:38912\n" \
R_"a[220:223], v29 offset:38912\n" \
"s_waitcnt lgkmcnt(15)\n" \
M_"a[0:3], a[160:163], a[128:131], a[0:3]\n" \
"s_addk_i32 s84, 0x7c00\n" \
M_"a[0:3], a[224:227], a[192:195], a[0:3]\n" \
B_"v27, s[72:75], s84 offen offset:2048 lds\n" \
M_"a[4:7], a[164:167], a[128:131], a[4:7]\n" \
X_"a[176:177], v36 offset:32768\n" \
"s_addk_i32 s5, 0x2000\n" \
"s_waitcnt lgkmcnt(15)\n" \
M_"a[4:7], a[228:231], a[192:195], a[4:7]\n" \
"s_addk_i32 s84, 0x7c00\n" \
"s_waitcnt lgkmcnt(11)\n" \
M_"a[8:11], a[168:171], a[128:131], a[8:11]\n" \
B_"v27, s[72:75], s84 offen offset:3072 lds\n" \
"s_waitcnt lgkmcnt(9)\n" \
M_"a[8:11], a[232:235], a[192:195], a[8:11]\n" \
X_"a[178:179], v36 offset:33792\n" \
"s_mov_b32 s37, 2.0\n" \
"s_waitcnt lgkmcnt(8)\n" \
M_"a[12:15], a[172:175], a[128:131], a[12:15]\n" \
X_"a[240:241], v36 offset:40960\n" \
"s_or_b32 s38, s38, 0x5be8000\n" \
"s_waitcnt lgkmcnt(5)\n" \
M_"a[12:15], a[236:239], a[192:195], a[12:15]\n" \
"s_cmp_lg_u32 s3, 0\n" \
"s_cselect_b32 s36, s37, s5\n" \
M_"a[28:31], a[172:175], a[132:135], a[28:31]\n" \
"s_lshl_b32 s5, s6, 2\n" \
"s_or_b32 s5, s38, s5\n" \
M_"a[28:31], a[236:239], a[196:199], a[28:31]\n" \
X_"a[242:243], v36 offset:41984\n" \
"s_lshl_b32 s35, s3, 10\n" \
M_"a[24:27], a[168:171], a[132:135], a[24:27]\n" \
"s_cmpk_lt_u32 s3, 3\n" \
"s_cselect_b32 s37, s37, s5\n" \
M_"a[24:27], a[232:235], a[196:199], a[24:27]\n" \
"s_lshl_b32 s5, s4, 16\n" \
"s_lshl_b32 s4, s4, 11\n" \
M_"a[20:23], a[164:167], a[132:135], a[20:23]\n" \
"v_cndmask_b32_e32 v198, v213, v212, vcc\n" \
"v_or_b32_e32 v212, s35, v20\n" \
M_"a[20:23], a[228:231], a[196:199], a[20:23]\n" \
X_"a[180:181], v37 offset:32768\n" \
"s_or_b32 s39, s40, s35\n" \
M_"a[16:19], a[160:163], a[132:135], a[16:19]\n" \
"s_or_b32 s55, s41, s4\n" \
"v_or_b32_e32 v199, 0x22000, v212\n" \
M_"a[16:19], a[224:227], a[196:199], a[16:19]\n" \
"s_add_i32 s35, s39, s7\n" \
"s_add_i32 s54, s41, s5\n" \
M_"a[32:35], a[160:163], a[136:139], a[32:35]\n" \
"s_mov_b32 m0, s32\n" \
X_"a[182:183], v37 offset:33792\n" \
M_"a[32:35], a[224:227], a[200:203], a[32:35]\n" \
"s_mov_b32 exec_lo, 0x0\n" \
"s_mov_b32 exec_hi, 0xff\n" \
"global_load_lds_dword v[184:185], off\n" \
"s_mov_b64 exec, -1\n" \
M_"a[36:39], a[164:167], a[136:139], a[36:39]\n" \
"s_add_i32 s55, s55, 0x80000\n" \
"v_mov_b32_e32 v215, 0xffff\n" \
M_"a[36:39], a[228:231], a[200:203], a[36:39]\n" \
X_"a[244:245], v37 offset:40960\n" \
"s_and_b32 s4, s3, 1\n" \
M_"a[40:43], a[168:171], a[136:139], a[40:43]\n" \
"s_lshl_b32 s5, s4, 4\n" \
"v_lshrrev_b32_e32 v212, 4, v20\n" \
M_"a[40:43], a[232:235], a[200:203], a[40:43]\n" \
"s_mov_b32 m0, s32\n" \
"v_lshrrev_b32_e32 v218, 6, v20\n" \
M_"a[44:47], a[172:175], a[136:139], a[44:47]\n" \
"s_mov_b32 exec_lo, 0xffff0000\n" \
"s_mov_b32 exec_hi, 0x0\n" \
"global_load_lds_dword v[186:187], off\n" \
"s_mov_b64 exec, -1\n" \
M_"a[44:47], a[236:239], a[200:203], a[44:47]\n" \
X_"a[246:247], v37 offset:41984\n" \
"v_and_b32_e64 v219, 12, v218\n" \
M_"a[60:63], a[172:175], a[140:143], a[60:63]\n" \
"v_and_or_b32 v214, v212, 15, s5\n" \
"v_cmp_gt_u32_e64 vcc, v219, v214\n" \
M_"a[60:63], a[236:239], a[204:207], a[60:63]\n" \
"v_mov_b32_e32 v220, 0xffff0000\n" \
"s_lshl_b32 s5, s2, 2\n" \
M_"a[56:59], a[168:171], a[140:143], a[56:59]\n" \
X_"a[184:185], v38 offset:32768\n" \
"v_cndmask_b32_e64 v217, v215, 0, vcc\n" \
M_"a[56:59], a[232:235], a[204:207], a[56:59]\n" \
"v_cmp_lt_u32_e32 vcc, v219, v214\n" \
"v_mov_b32_e64 v213, s23\n" \
M_"a[52:55], a[164:167], a[140:143], a[52:55]\n" \
"s_or_b32 s5, s5, s3\n" \
"v_cndmask_b32_e32 v221, 0, v220, vcc\n" \
M_"a[52:55], a[228:231], a[204:207], a[52:55]\n" \
X_"a[186:187], v38 offset:33792\n" \
"v_or_b32_e64 v12, v217, v221\n" \
M_"a[48:51], a[160:163], a[140:143], a[48:51]\n" \
"v_or_b32_e32 v217, 2, v219\n" \
"v_cmp_gt_u32_e32 vcc, v217, v214\n" \
M_"a[48:51], a[224:227], a[204:207], a[48:51]\n" \
"v_mov_b32_e32 v216, s25\n" \
"s_movk_i32 s40, 0xf400\n" \
M_"a[64:67], a[160:163], a[144:147], a[64:67]\n" \
X_"a[248:249], v38 offset:40960\n" \
"v_cndmask_b32_e64 v221, v215, 0, vcc\n" \
M_"a[64:67], a[224:227], a[208:211], a[64:67]\n" \
"v_cmp_lt_u32_e32 vcc, v217, v214\n" \
"s_lshl_b32 s4, s4, 6\n" \
M_"a[68:71], a[164:167], a[144:147], a[68:71]\n" \
"v_cndmask_b32_e32 v217, 0, v220, vcc\n" \
"v_or_b32_e32 v13, v221, v217\n" \
M_"a[68:71], a[228:231], a[208:211], a[68:71]\n" \
X_"a[250:251], v38 offset:41984\n" \
"v_or_b32_e64 v217, 16, v219\n" \
M_"a[72:75], a[168:171], a[144:147], a[72:75]\n" \
"v_cmp_gt_u32_e32 vcc, v217, v214\n" \
"s_nop 1\n" \
"v_cndmask_b32_e64 v221, v215, 0, vcc\n" \
M_"a[72:75], a[232:235], a[208:211], a[72:75]\n" \
"v_cmp_lt_u32_e32 vcc, v217, v214\n" \
"s_nop 1\n" \
"v_cndmask_b32_e64 v217, 0, v220, vcc\n" \
M_"a[76:79], a[172:175], a[144:147], a[76:79]\n" \
"v_or_b32_e32 v14, v221, v217\n" \
"v_or_b32_e32 v217, 18, v219\n" \
M_"a[76:79], a[236:239], a[208:211], a[76:79]\n" \
X_"a[188:189], v39 offset:32768\n" \
"v_cmp_gt_u32_e64 vcc, v217, v214\n" \
M_"a[92:95], a[172:175], a[148:151], a[92:95]\n" \
"s_nop 0\n" \
"v_cndmask_b32_e64 v215, v215, 0, vcc\n" \
"v_cmp_lt_u32_e32 vcc, v217, v214\n" \
M_"a[92:95], a[236:239], a[212:215], a[92:95]\n" \
"s_nop 0\n" \
"v_cndmask_b32_e32 v214, 0, v220, vcc\n" \
"v_or_b32_e64 v15, v215, v214\n" \
M_"a[88:91], a[168:171], a[148:151], a[88:91]\n" \
X_"a[190:191], v39 offset:33792\n" \
"v_mad_u64_u32 v[214:215], s[6:7], s5, 42, v[212:213]\n" \
M_"a[88:91], a[232:235], a[212:215], a[88:91]\n" \
"s_movk_i32 s5, 0x1440\n" \
"v_cmp_gt_u32_e32 vcc, s5, v214\n" \
M_"a[84:87], a[164:167], a[148:151], a[84:87]\n" \
"v_min_u32_e32 v217, 0xa43f, v214\n" \
"v_mov_b32_e64 v214, s24\n" \
M_"a[84:87], a[228:231], a[212:215], a[84:87]\n" \
X_"a[252:253], v39 offset:40960\n" \
"v_cndmask_b32_e64 v215, v216, v213, vcc\n" \
M_"a[80:83], a[160:163], a[148:151], a[80:83]\n" \
"v_mov_b32_e32 v213, s22\n" \
"s_movk_i32 s6, 0xffee\n" \
M_"a[80:83], a[224:227], a[212:215], a[80:83]\n" \
"v_cndmask_b32_e64 v214, v214, v213, vcc\n" \
"v_add_u32_e32 v213, 0xffffebc0, v217\n" \
M_"a[96:99], a[160:163], a[152:155], a[96:99]\n" \
X_"a[254:255], v39 offset:41984\n" \
"s_lshl_b32 s5, s2, 5\n" \
M_"a[96:99], a[224:227], a[216:219], a[96:99]\n" \
"v_cndmask_b32_e32 v213, v213, v217, vcc\n" \
"v_lshlrev_b32_e64 v216, 7, v213\n" \
M_"a[100:103], a[164:167], a[152:155], a[100:103]\n" \
"v_mul_u32_u24_e32 v213, 0xe39, v212\n" \
"v_mov_b32_e64 v217, 0\n" \
M_"a[100:103], a[228:231], a[216:219], a[100:103]\n" \
"v_lshrrev_b32_e64 v213, 16, v213\n" \
"v_lshl_add_u64 v[16:17], v[214:215], 0, v[216:217]\n" \
M_"a[104:107], a[168:171], a[152:155], a[104:107]\n" \
"v_mad_i32_i24 v214, v213, s6, v212\n" \
"s_add_i32 s5, s5, 32\n" \
M_"a[104:107], a[232:235], a[216:219], a[104:107]\n" \
"v_lshlrev_b32_e32 v215, 4, v214\n" \
"v_add_u32_e64 v216, s5, v215\n" \
M_"a[108:111], a[172:175], a[152:155], a[108:111]\n" \
"v_and_b32_e32 v216, 0xf0, v216\n" \
"v_cmp_gt_u32_e64 vcc, 16, v214\n" \
M_"a[108:111], a[236:239], a[216:219], a[108:111]\n" \
"s_movk_i32 s7, 0xf800\n" \
"v_cndmask_b32_e32 v214, v215, v216, vcc\n" \
"s_waitcnt lgkmcnt(15)\n" \
M_"a[124:127], a[172:175], a[156:159], a[124:127]\n" \
"v_or_b32_e32 v215, 64, v212\n" \
"v_mul_u32_u24_e32 v216, 0xe39, v215\n" \
M_"a[124:127], a[236:239], a[220:223], a[124:127]\n" \
"v_lshrrev_b32_e64 v216, 16, v216\n" \
"v_mad_i32_i24 v215, v216, s6, v215\n" \
M_"a[120:123], a[168:171], a[156:159], a[120:123]\n" \
"v_lshlrev_b32_e32 v217, 4, v215\n" \
"v_add_u32_e32 v220, s5, v217\n" \
M_"a[120:123], a[232:235], a[220:223], a[120:123]\n" \
"v_and_b32_e32 v220, 0xf0, v220\n" \
"v_cmp_gt_u32_e64 vcc, 16, v215\n" \
M_"a[116:119], a[164:167], a[156:159], a[116:119]\n" \
"s_nop 0\n" \
"v_cndmask_b32_e32 v215, v217, v220, vcc\n" \
"v_or_b32_e32 v217, 0x80, v212\n" \
M_"a[116:119], a[228:231], a[220:223], a[116:119]\n" \
"v_mul_u32_u24_e32 v220, 0xe39, v217\n" \
"v_lshrrev_b32_e64 v220, 16, v220\n" \
M_"a[112:115], a[160:163], a[156:159], a[112:115]\n" \
"v_mad_i32_i24 v217, v220, s6, v217\n" \
"v_lshlrev_b32_e64 v221, 4, v217\n" \
M_"a[112:115], a[224:227], a[220:223], a[112:115]\n" \
"s_waitcnt vmcnt(20)\n" \
"v_readfirstlane_b32 s62, v180\n" \
"s_nop 3\n" \
"s_add_u32 s62, s62, 0x9E3779B1\n" \
"s_waitcnt vmcnt(2) lgkmcnt(0)\n" \
"s_barrier\n" \
M_"v[52:55], a[176:179], a[128:131], v[52:55]\n" \
"s_add_u32 m0, s30, 0x8000\n" \
"s_movk_i32 s84, 0x180\n" \
M_"v[52:55], a[240:243], a[192:195], v[52:55]\n" \
B_"v22, s[64:67], s84 offen offset:0 lds\n" \
M_"v[56:59], a[180:183], a[128:131], v[56:59]\n" \
X_"a[160:161], v32 offset:0\n" \
"v_add_u32_e64 v222, s5, v221\n" \
M_"v[56:59], a[244:247], a[192:195], v[56:59]\n" \
"s_movk_i32 s84, 0x1d80\n" \
M_"v[60:63], a[184:187], a[128:131], v[60:63]\n" \
B_"v23, s[64:67], s84 offen offset:1024 lds\n" \
M_"v[60:63], a[248:251], a[192:195], v[60:63]\n" \
X_"a[162:163], v32 offset:1024\n" \
"v_and_b32_e32 v222, 0xf0, v222\n" \
M_"v[64:67], a[188:191], a[128:131], v[64:67]\n" \
X_"a[224:225], v32 offset:8192\n" \
"v_cmp_gt_u32_e32 vcc, 16, v217\n" \
M_"v[64:67], a[252:255], a[192:195], v[64:67]\n" \
R_"a[128:131], v28 offset:16384\n" \
"v_cndmask_b32_e64 v217, v221, v222, vcc\n" \
M_"v[80:83], a[188:191], a[132:135], v[80:83]\n" \
R_"a[192:195], v29 offset:16384\n" \
"v_or_b32_e32 v221, 0xc0, v212\n" \
M_"v[80:83], a[252:255], a[196:199], v[80:83]\n" \
"s_movk_i32 s84, 0x3980\n" \
M_"v[76:79], a[184:187], a[132:135], v[76:79]\n" \
B_"v22, s[64:67], s84 offen offset:2048 lds\n" \
M_"v[76:79], a[248:251], a[196:199], v[76:79]\n" \
"s_movk_i32 s84, 0x5580\n" \
M_"v[72:75], a[180:183], a[132:135], v[72:75]\n" \
B_"v23, s[64:67], s84 offen offset:3072 lds\n" \
M_"v[72:75], a[244:247], a[196:199], v[72:75]\n" \
"s_add_u32 m0, s31, 0x8000\n" \
"s_mov_b32 s84, 0x180000\n" \
M_"v[68:71], a[176:179], a[132:135], v[68:71]\n" \
B_"v26, s[68:71], s84 offen offset:0 lds\n" \
M_"v[68:71], a[240:243], a[196:199], v[68:71]\n" \
R_"a[132:135], v28 offset:18432\n" \
"v_mul_u32_u24_e32 v222, 0xe39, v221\n" \
M_"v[84:87], a[176:179], a[136:139], v[84:87]\n" \
R_"a[196:199], v29 offset:18432\n" \
"v_lshrrev_b32_e64 v222, 16, v222\n" \
M_"v[84:87], a[240:243], a[200:203], v[84:87]\n" \
"s_addk_i32 s84, 0x7c00\n" \
M_"v[88:91], a[180:183], a[136:139], v[88:91]\n" \
B_"v26, s[68:71], s84 offen offset:1024 lds\n" \
M_"v[88:91], a[244:247], a[200:203], v[88:91]\n" \
X_"a[226:227], v32 offset:9216\n" \
"v_mad_i32_i24 v221, v222, s6, v221\n" \
M_"v[92:95], a[184:187], a[136:139], v[92:95]\n" \
"s_addk_i32 s84, 0x7c00\n" \
M_"v[92:95], a[248:251], a[200:203], v[92:95]\n" \
B_"v27, s[68:71], s84 offen offset:2048 lds\n" \
M_"v[96:99], a[188:191], a[136:139], v[96:99]\n" \
X_"a[164:165], v33 offset:0\n" \
"v_lshlrev_b32_e64 v223, 4, v221\n" \
M_"v[96:99], a[252:255], a[200:203], v[96:99]\n" \
R_"a[136:139], v28 offset:20480\n" \
"v_add_u32_e64 v224, s5, v223\n" \
M_"v[112:115], a[188:191], a[140:143], v[112:115]\n" \
R_"a[200:203], v29 offset:20480\n" \
"v_and_b32_e32 v224, 0xf0, v224\n" \
M_"v[112:115], a[252:255], a[204:207], v[112:115]\n" \
"s_addk_i32 s84, 0x7c00\n" \
M_"v[108:111], a[184:187], a[140:143], v[108:111]\n" \
B_"v27, s[68:71], s84 offen offset:3072 lds\n" \
M_"v[108:111], a[248:251], a[204:207], v[108:111]\n" \
"s_add_u32 m0, s30, 0x9000\n" \
"s_mov_b32 s84, 0x8180\n" \
M_"v[104:107], a[180:183], a[140:143], v[104:107]\n" \
B_"v22, s[64:67], s84 offen offset:0 lds\n" \
M_"v[104:107], a[244:247], a[204:207], v[104:107]\n" \
X_"a[166:167], v33 offset:1024\n" \
"v_cmp_gt_u32_e32 vcc, 16, v221\n" \
M_"v[100:103], a[176:179], a[140:143], v[100:103]\n" \
X_"a[228:229], v33 offset:8192\n" \
"v_cndmask_b32_e64 v221, v223, v224, vcc\n" \
M_"v[100:103], a[240:243], a[204:207], v[100:103]\n" \
R_"a[140:143], v28 offset:22528\n" \
"v_or_b32_e32 v223, 0x100, v212\n" \
M_"v[116:119], a[176:179], a[144:147], v[116:119]\n" \
R_"a[204:207], v29 offset:22528\n" \
"v_mul_u32_u24_e32 v224, 0xe39, v223\n" \
M_"v[116:119], a[240:243], a[208:211], v[116:119]\n" \
"s_addk_i32 s84, 0x1c00\n" \
M_"v[120:123], a[180:183], a[144:147], v[120:123]\n" \
B_"v23, s[64:67], s84 offen offset:1024 lds\n" \
M_"v[120:123], a[244:247], a[208:211], v[120:123]\n" \
"s_addk_i32 s84, 0x1c00\n" \
M_"v[124:127], a[184:187], a[144:147], v[124:127]\n" \
B_"v22, s[64:67], s84 offen offset:2048 lds\n" \
M_"v[124:127], a[248:251], a[208:211], v[124:127]\n" \
"s_addk_i32 s84, 0x1c00\n" \
M_"v[128:131], a[188:191], a[144:147], v[128:131]\n" \
B_"v23, s[64:67], s84 offen offset:3072 lds\n" \
M_"v[128:131], a[252:255], a[208:211], v[128:131]\n" \
R_"a[144:147], v28 offset:0\n" \
"v_lshrrev_b32_e32 v224, 16, v224\n" \
M_"v[144:147], a[188:191], a[148:151], v[144:147]\n" \
R_"a[208:211], v29 offset:0\n" \
"v_mad_i32_i24 v223, v224, s6, v223\n" \
M_"v[144:147], a[252:255], a[212:215], v[144:147]\n" \
"s_add_u32 m0, s31, 0xc000\n" \
"s_mov_b32 s84, 0x180000\n" \
M_"v[140:143], a[184:187], a[148:151], v[140:143]\n" \
B_"v26, s[72:75], s84 offen offset:0 lds\n" \
M_"v[140:143], a[248:251], a[212:215], v[140:143]\n" \
X_"a[230:231], v33 offset:9216\n" \
"v_lshlrev_b32_e64 v225, 4, v223\n" \
M_"v[136:139], a[180:183], a[148:151], v[136:139]\n" \
"s_addk_i32 s84, 0x7c00\n" \
M_"v[136:139], a[244:247], a[212:215], v[136:139]\n" \
B_"v26, s[72:75], s84 offen offset:1024 lds\n" \
M_"v[132:135], a[176:179], a[148:151], v[132:135]\n" \
X_"a[168:169], v34 offset:0\n" \
"v_add_u32_e32 v226, s5, v225\n" \
M_"v[132:135], a[240:243], a[212:215], v[132:135]\n" \
R_"a[148:151], v28 offset:2048\n" \
"v_and_b32_e32 v226, 0xf0, v226\n" \
M_"v[148:151], a[176:179], a[152:155], v[148:151]\n" \
R_"a[212:215], v29 offset:2048\n" \
"v_cmp_gt_u32_e64 vcc, 16, v223\n" \
M_"v[148:151], a[240:243], a[216:219], v[148:151]\n" \
X_"a[170:171], v34 offset:1024\n" \
"v_cndmask_b32_e64 v223, v225, v226, vcc\n" \
M_"v[152:155], a[180:183], a[152:155], v[152:155]\n" \
X_"a[232:233], v34 offset:8192\n" \
"v_or_b32_e32 v225, 0x140, v212\n" \
M_"v[152:155], a[244:247], a[216:219], v[152:155]\n" \
X_"a[234:235], v34 offset:9216\n" \
"v_mul_u32_u24_e32 v226, 0xe39, v225\n" \
M_"v[156:159], a[184:187], a[152:155], v[156:159]\n" \
X_"a[172:173], v35 offset:0\n" \
"v_lshrrev_b32_e64 v226, 16, v226\n" \
M_"v[156:159], a[248:251], a[216:219], v[156:159]\n" \
X_"a[174:175], v35 offset:1024\n" \
"v_mad_i32_i24 v225, v226, s6, v225\n" \
M_"v[160:163], a[188:191], a[152:155], v[160:163]\n" \
X_"a[236:237], v35 offset:8192\n" \
"v_lshlrev_b32_e64 v227, 4, v225\n" \
M_"v[160:163], a[252:255], a[216:219], v[160:163]\n" \
R_"a[152:155], v28 offset:4096\n" \
"v_add_u32_e64 v228, s5, v227\n" \
M_"v[176:179], a[188:191], a[156:159], v[176:179]\n" \
R_"a[216:219], v29 offset:4096\n" \
"v_and_b32_e32 v228, 0xf0, v228\n" \
M_"v[176:179], a[252:255], a[220:223], v[176:179]\n" \
X_"a[238:239], v35 offset:9216\n" \
"v_cmp_gt_u32_e64 vcc, 16, v225\n" \
M_"v[172:175], a[184:187], a[156:159], v[172:175]\n" \
"s_nop 0\n" \
"v_cndmask_b32_e32 v225, v227, v228, vcc\n" \
"v_or_b32_e32 v227, 0x180, v212\n" \
M_"v[172:175], a[248:251], a[220:223], v[172:175]\n" \
"v_mul_u32_u24_e32 v228, 0xe39, v227\n" \
"v_lshrrev_b32_e64 v228, 16, v228\n" \
M_"v[168:171], a[180:183], a[156:159], v[168:171]\n" \
"v_mad_i32_i24 v227, v228, s6, v227\n" \
"v_lshlrev_b32_e64 v229, 4, v227\n" \
M_"v[168:171], a[244:247], a[220:223], v[168:171]\n" \
"v_add_u32_e64 v230, s5, v229\n" \
"v_and_b32_e32 v230, 0xf0, v230\n" \
M_"v[164:167], a[176:179], a[156:159], v[164:167]\n" \
"v_cmp_gt_u32_e32 vcc, 16, v227\n" \
"s_nop 1\n" \
"v_cndmask_b32_e64 v227, v229, v230, vcc\n" \
M_"v[164:167], a[240:243], a[220:223], v[164:167]\n" \
R_"a[156:159], v28 offset:6144\n" \
R_"a[220:223], v29 offset:6144\n" \
"s_waitcnt lgkmcnt(15)\n" \
M_"a[0:3], a[160:163], a[128:131], a[0:3]\n" \
"s_addk_i32 s84, 0x7c00\n" \
M_"a[0:3], a[224:227], a[192:195], a[0:3]\n" \
B_"v27, s[72:75], s84 offen offset:2048 lds\n" \
M_"a[4:7], a[164:167], a[128:131], a[4:7]\n" \
X_"a[176:177], v36 offset:0\n" \
"v_or_b32_e32 v229, 0x1c0, v212\n" \
"s_waitcnt lgkmcnt(15)\n" \
M_"a[4:7], a[228:231], a[192:195], a[4:7]\n" \
"s_addk_i32 s84, 0x7c00\n" \
"s_waitcnt lgkmcnt(11)\n" \
M_"a[8:11], a[168:171], a[128:131], a[8:11]\n" \
B_"v27, s[72:75], s84 offen offset:3072 lds\n" \
"s_waitcnt lgkmcnt(9)\n" \
M_"a[8:11], a[232:235], a[192:195], a[8:11]\n" \
X_"a[178:179], v36 offset:1024\n" \
"v_mul_u32_u24_e32 v230, 0xe39, v229\n" \
"s_waitcnt lgkmcnt(8)\n" \
M_"a[12:15], a[172:175], a[128:131], a[12:15]\n" \
X_"a[240:241], v36 offset:8192\n" \
"v_lshrrev_b32_e32 v230, 16, v230\n" \
"s_waitcnt lgkmcnt(5)\n" \
M_"a[12:15], a[236:239], a[192:195], a[12:15]\n" \
"v_mad_i32_i24 v229, v230, s6, v229\n" \
"v_lshlrev_b32_e32 v231, 4, v229\n" \
M_"a[28:31], a[172:175], a[132:135], a[28:31]\n" \
"v_add_u32_e64 v232, s5, v231\n" \
"v_and_b32_e32 v232, 0xf0, v232\n" \
M_"a[28:31], a[236:239], a[196:199], a[28:31]\n" \
X_"a[242:243], v36 offset:9216\n" \
"v_cmp_gt_u32_e64 vcc, 16, v229\n" \
M_"a[24:27], a[168:171], a[132:135], a[24:27]\n" \
B_"v[200:203], v192, s[76:79], s34 offen offset:0\n" \
M_"a[24:27], a[232:235], a[196:199], a[24:27]\n" \
"v_cndmask_b32_e64 v229, v231, v232, vcc\n" \
"v_or_b32_e32 v231, 0x200, v212\n" \
M_"a[20:23], a[164:167], a[132:135], a[20:23]\n" \
"v_mul_u32_u24_e32 v232, 0xe39, v231\n" \
"v_lshrrev_b32_e64 v232, 16, v232\n" \
M_"a[20:23], a[228:231], a[196:199], a[20:23]\n" \
X_"a[180:181], v37 offset:0\n" \
"v_mad_i32_i24 v231, v232, s6, v231\n" \
M_"a[16:19], a[160:163], a[132:135], a[16:19]\n" \
"v_lshlrev_b32_e32 v233, 4, v231\n" \
"v_add_u32_e32 v234, s5, v233\n" \
M_"a[16:19], a[224:227], a[196:199], a[16:19]\n" \
"s_movk_i32 s5, 0x480\n" \
"v_mad_u32_u24 v0, v213, s5, v214\n" \
M_"a[32:35], a[160:163], a[136:139], a[32:35]\n" \
B_"v[204:207], v193, s[76:79], s34 offen offset:0\n" \
M_"a[32:35], a[224:227], a[200:203], a[32:35]\n" \
"s_mov_b32 m0, s32\n" \
X_"a[182:183], v37 offset:1024\n" \
M_"a[36:39], a[164:167], a[136:139], a[36:39]\n" \
"s_mov_b32 exec_lo, 0x0\n" \
"s_mov_b32 exec_hi, 0xff00\n" \
"global_load_lds_dword v[184:185], off\n" \
"s_mov_b64 exec, -1\n" \
M_"a[36:39], a[228:231], a[200:203], a[36:39]\n" \
X_"a[244:245], v37 offset:8192\n" \
"v_mul_u32_u24_e32 v213, 0x480, v216\n" \
M_"a[40:43], a[168:171], a[136:139], a[40:43]\n" \
"s_movk_i32 s6, 0xfc00\n" \
"s_nop 0\n" \
"v_add3_u32 v1, v213, v215, s6\n" \
M_"a[40:43], a[232:235], a[200:203], a[40:43]\n" \
"buffer_load_dword v208, v194, s[76:79], s34 offen offset:32\n" \
M_"a[44:47], a[172:175], a[136:139], a[44:47]\n" \
X_"a[246:247], v37 offset:9216\n" \
"v_mul_u32_u24_e32 v213, 0x480, v220\n" \
M_"a[44:47], a[236:239], a[200:203], a[44:47]\n" \
"v_add3_u32 v2, v213, v217, s7\n" \
"v_mul_u32_u24_e32 v213, 0x480, v222\n" \
M_"a[60:63], a[172:175], a[140:143], a[60:63]\n" \
"v_add3_u32 v3, v213, v221, s40\n" \
"v_mul_u32_u24_e32 v213, 0x480, v226\n" \
M_"a[60:63], a[236:239], a[204:207], a[60:63]\n" \
"v_add3_u32 v5, v213, v225, s6\n" \
"v_mul_u32_u24_e32 v213, 0x480, v228\n" \
M_"a[56:59], a[168:171], a[140:143], a[56:59]\n" \
"buffer_load_dword v209, v194, s[76:79], s34 offen offset:1184\n" \
M_"a[56:59], a[232:235], a[204:207], a[56:59]\n" \
X_"a[184:185], v38 offset:0\n" \
"v_add3_u32 v6, v213, v227, s7\n" \
M_"a[52:55], a[164:167], a[140:143], a[52:55]\n" \
"v_mul_u32_u24_e32 v213, 0x480, v230\n" \
"v_add3_u32 v7, v213, v229, s40\n" \
M_"a[52:55], a[228:231], a[204:207], a[52:55]\n" \
X_"a[186:187], v38 offset:1024\n" \
"s_lshl_b32 s7, s3, 3\n" \
M_"a[48:51], a[160:163], a[140:143], a[48:51]\n" \
"s_lshl_b32 s40, s3, 1\n" \
"s_and_b32 s7, s7, 16\n" \
M_"a[48:51], a[224:227], a[204:207], a[48:51]\n" \
"s_and_b32 s40, s40, 2\n" \
"s_and_b32 s6, s2, 7\n" \
M_"a[64:67], a[160:163], a[144:147], a[64:67]\n" \
"buffer_load_dword v211, v194, s[76:79], s34 offen offset:2336\n" \
M_"a[64:67], a[224:227], a[208:211], a[64:67]\n" \
X_"a[248:249], v38 offset:8192\n" \
"v_bfe_u32 v213, v20, 7, 1\n" \
M_"a[68:71], a[164:167], a[144:147], a[68:71]\n" \
"s_or_b32 s7, s40, s7\n" \
"v_and_b32_e32 v234, 0xf0, v234\n" \
M_"a[68:71], a[228:231], a[208:211], a[68:71]\n" \
X_"a[250:251], v38 offset:9216\n" \
"v_cmp_gt_u32_e64 vcc, 16, v231\n" \
M_"a[72:75], a[168:171], a[144:147], a[72:75]\n" \
"v_or3_b32 v213, s7, v213, v219\n" \
"v_and_b32_e64 v214, 32, v20\n" \
M_"a[72:75], a[232:235], a[208:211], a[72:75]\n" \
"s_mov_b32 s76, s20\n" \
"s_and_b32 s77, s21, 0xffff\n" \
M_"a[76:79], a[172:175], a[144:147], a[76:79]\n" \
"s_mov_b32 s78, 0x480\n" \
"s_lshl_b32 s6, s6, 5\n" \
M_"a[76:79], a[236:239], a[208:211], a[76:79]\n" \
"buffer_load_ushort v210, v195, s[76:79], 0 offen\n" \
".fill (L_pb + 7904 - .) / 4, 4, 0xBF800000\n" \
"L_pp2:\n" \
"s_cbranch_execz L_pret\n" \
M_"a[92:95], a[172:175], a[148:151], a[92:95]\n" \
X_"a[188:189], v39 offset:0\n" \
"v_cndmask_b32_e32 v231, v233, v234, vcc\n" \
M_"a[92:95], a[236:239], a[212:215], a[92:95]\n" \
"v_mul_u32_u24_e64 v213, 18, v213\n" \
"v_mov_b32_e32 v215, 0x100\n" \
M_"a[88:91], a[168:171], a[148:151], a[88:91]\n" \
"s_mov_b32 s76, s8\n" \
"s_and_b32 s77, s9, 0xffff\n" \
M_"a[88:91], a[232:235], a[212:215], a[88:91]\n" \
"s_mov_b32 s78, 0x240000\n" \
X_"a[190:191], v39 offset:1024\n" \
M_"a[84:87], a[164:167], a[148:151], a[84:87]\n" \
"v_mov_b32_e32 v216, s6\n" \
"v_cmp_eq_u32_e64 vcc, 0, v214\n" \
M_"a[84:87], a[228:231], a[212:215], a[84:87]\n" \
X_"a[252:253], v39 offset:8192\n" \
"v_and_or_b32 v213, v218, 1, v213\n" \
M_"a[80:83], a[160:163], a[148:151], a[80:83]\n" \
"v_lshlrev_b32_e32 v212, 3, v212\n" \
"v_cndmask_b32_e32 v214, v215, v216, vcc\n" \
M_"a[80:83], a[224:227], a[212:215], a[80:83]\n" \
"v_mad_u32_u24 v213, v213, s5, v214\n" \
"v_and_or_b32 v213, v20, 16, v213\n" \
M_"a[96:99], a[160:163], a[152:155], a[96:99]\n" \
X_"a[254:255], v39 offset:9216\n" \
"v_add_u32_e32 v9, 0x4800, v213\n" \
M_"a[96:99], a[224:227], a[216:219], a[96:99]\n" \
"v_mul_u32_u24_e32 v213, 0x120, v218\n" \
"v_and_or_b32 v10, v212, 24, v213\n" \
M_"a[100:103], a[164:167], a[152:155], a[100:103]\n" \
"v_lshrrev_b32_e64 v213, 2, v20\n" \
"v_and_or_b32 v213, v213, 60, s4\n" \
M_"a[100:103], a[228:231], a[216:219], a[100:103]\n" \
"v_lshl_or_b32 v212, s3, 9, v212\n" \
"v_mad_u32_u24 v4, v224, s5, v223\n" \
M_"a[104:107], a[168:171], a[152:155], a[104:107]\n" \
"v_mad_u32_u24 v8, v232, s5, v231\n" \
"v_or_b32_e32 v18, 0x27000, v213\n" \
M_"a[104:107], a[232:235], a[216:219], a[104:107]\n" \
"v_or_b32_e32 v19, 0x27800, v212\n" \
"v_mov_b32_e32 v215, 0x1000c0c\n" \
M_"a[108:111], a[172:175], a[152:155], a[108:111]\n" \
"v_mov_b32_e32 v216, 0xc0c0100\n" \
"v_bfe_u32 v214, v20, 4, 1\n" \
M_"a[108:111], a[236:239], a[216:219], a[108:111]\n" \
"v_lshrrev_b32_e32 v212, 8, v20\n" \
"v_cmp_eq_u32_e32 vcc, 0, v214\n" \
"s_waitcnt lgkmcnt(15)\n" \
M_"a[124:127], a[172:175], a[156:159], a[124:127]\n" \
"v_bfe_u32 v213, v20, 4, 4\n" \
"v_mov_b32_e32 v217, 0xc0c0302\n" \
M_"a[124:127], a[236:239], a[220:223], a[124:127]\n" \
"v_cndmask_b32_e32 v215, v215, v216, vcc\n" \
"v_mov_b32_e32 v216, 0x3020c0c\n" \
M_"a[120:123], a[168:171], a[156:159], a[120:123]\n" \
"v_lshl_or_b32 v214, v212, 2, v214\n" \
"v_cndmask_b32_e64 v216, v216, v217, vcc\n" \
M_"a[120:123], a[232:235], a[220:223], a[120:123]\n" \
"v_mov_b32_e32 v217, 0xc0c0c0c\n" \
"v_cmp_gt_u32_e64 vcc, v214, v213\n" \
M_"a[116:119], a[164:167], a[156:159], a[116:119]\n" \
"v_or_b32_e32 v214, 2, v214\n" \
"s_lshr_b32 s4, s2, 5\n" \
M_"a[116:119], a[228:231], a[220:223], a[116:119]\n" \
"v_cndmask_b32_e32 v40, v217, v215, vcc\n" \
"v_cndmask_b32_e32 v42, v215, v217, vcc\n" \
M_"a[112:115], a[160:163], a[156:159], a[112:115]\n" \
"v_cmp_gt_u32_e32 vcc, v214, v213\n" \
"v_and_b32_e32 v214, 16, v20\n" \
"s_waitcnt vmcnt(7) lgkmcnt(0)\n" \
M_"a[112:115], a[224:227], a[220:223], a[112:115]\n" \
"s_barrier\n" \
M_"v[52:55], a[176:179], a[128:131], v[52:55]\n" \
"s_add_u32 m0, s30, 0x0\n" \
"s_movk_i32 s84, 0x200\n" \
M_"v[52:55], a[240:243], a[192:195], v[52:55]\n" \
B_"v22, s[64:67], s84 offen offset:0 lds\n" \
M_"v[56:59], a[180:183], a[128:131], v[56:59]\n" \
X_"a[160:161], v32 offset:32768\n" \
"s_movk_i32 s42, 0x3f80\n" \
M_"v[56:59], a[244:247], a[192:195], v[56:59]\n" \
"s_movk_i32 s84, 0x1e00\n" \
M_"v[60:63], a[184:187], a[128:131], v[60:63]\n" \
B_"v23, s[64:67], s84 offen offset:1024 lds\n" \
M_"v[60:63], a[248:251], a[192:195], v[60:63]\n" \
X_"a[162:163], v32 offset:33792\n" \
"s_xor_b32 s5, s4, 15\n" \
M_"v[64:67], a[188:191], a[128:131], v[64:67]\n" \
X_"a[224:225], v32 offset:40960\n" \
M_"v[64:67], a[252:255], a[192:195], v[64:67]\n" \
R_"a[128:131], v28 offset:49152\n" \
M_"v[80:83], a[188:191], a[132:135], v[80:83]\n" \
R_"a[192:195], v29 offset:49152\n" \
M_"v[80:83], a[252:255], a[196:199], v[80:83]\n" \
"s_movk_i32 s84, 0x3a00\n" \
M_"v[76:79], a[184:187], a[132:135], v[76:79]\n" \
B_"v22, s[64:67], s84 offen offset:2048 lds\n" \
M_"v[76:79], a[248:251], a[196:199], v[76:79]\n" \
"s_movk_i32 s84, 0x5600\n" \
M_"v[72:75], a[180:183], a[132:135], v[72:75]\n" \
B_"v23, s[64:67], s84 offen offset:3072 lds\n" \
M_"v[72:75], a[244:247], a[196:199], v[72:75]\n" \
"s_add_u32 m0, s31, 0x0\n" \
"s_mov_b32 s84, 0x200000\n" \
M_"v[68:71], a[176:179], a[132:135], v[68:71]\n" \
B_"v26, s[68:71], s84 offen offset:0 lds\n" \
M_"v[68:71], a[240:243], a[196:199], v[68:71]\n" \
R_"a[132:135], v28 offset:51200\n" \
M_"v[84:87], a[176:179], a[136:139], v[84:87]\n" \
R_"a[196:199], v29 offset:51200\n" \
M_"v[84:87], a[240:243], a[200:203], v[84:87]\n" \
"s_addk_i32 s84, 0x7c00\n" \
M_"v[88:91], a[180:183], a[136:139], v[88:91]\n" \
B_"v26, s[68:71], s84 offen offset:1024 lds\n" \
M_"v[88:91], a[244:247], a[200:203], v[88:91]\n" \
X_"a[226:227], v32 offset:41984\n" \
M_"v[92:95], a[184:187], a[136:139], v[92:95]\n" \
"s_addk_i32 s84, 0x7c00\n" \
M_"v[92:95], a[248:251], a[200:203], v[92:95]\n" \
B_"v27, s[68:71], s84 offen offset:2048 lds\n" \
M_"v[96:99], a[188:191], a[136:139], v[96:99]\n" \
X_"a[164:165], v33 offset:32768\n" \
M_"v[96:99], a[252:255], a[200:203], v[96:99]\n" \
R_"a[136:139], v28 offset:53248\n" \
M_"v[112:115], a[188:191], a[140:143], v[112:115]\n" \
R_"a[200:203], v29 offset:53248\n" \
M_"v[112:115], a[252:255], a[204:207], v[112:115]\n" \
"s_addk_i32 s84, 0x7c00\n" \
"s_nop 0\n" \
M_"v[108:111], a[184:187], a[140:143], v[108:111]\n" \
B_"v27, s[68:71], s84 offen offset:3072 lds\n" \
M_"v[108:111], a[248:251], a[204:207], v[108:111]\n" \
"s_add_u32 m0, s30, 0x1000\n" \
"s_mov_b32 s84, 0x8200\n" \
M_"v[104:107], a[180:183], a[140:143], v[104:107]\n" \
B_"v22, s[64:67], s84 offen offset:0 lds\n" \
M_"v[104:107], a[244:247], a[204:207], v[104:107]\n" \
X_"a[166:167], v33 offset:33792\n" \
M_"v[100:103], a[176:179], a[140:143], v[100:103]\n" \
X_"a[228:229], v33 offset:40960\n" \
M_"v[100:103], a[240:243], a[204:207], v[100:103]\n" \
R_"a[140:143], v28 offset:55296\n" \
M_"v[116:119], a[176:179], a[144:147], v[116:119]\n" \
R_"a[204:207], v29 offset:55296\n" \
M_"v[116:119], a[240:243], a[208:211], v[116:119]\n" \
"s_addk_i32 s84, 0x1c00\n" \
M_"v[120:123], a[180:183], a[144:147], v[120:123]\n" \
B_"v23, s[64:67], s84 offen offset:1024 lds\n" \
M_"v[120:123], a[244:247], a[208:211], v[120:123]\n" \
"s_addk_i32 s84, 0x1c00\n" \
M_"v[124:127], a[184:187], a[144:147], v[124:127]\n" \
B_"v22, s[64:67], s84 offen offset:2048 lds\n" \
M_"v[124:127], a[248:251], a[208:211], v[124:127]\n" \
"s_addk_i32 s84, 0x1c00\n" \
"s_nop 0\n" \
M_"v[128:131], a[188:191], a[144:147], v[128:131]\n" \
B_"v23, s[64:67], s84 offen offset:3072 lds\n" \
M_"v[128:131], a[252:255], a[208:211], v[128:131]\n" \
R_"a[144:147], v28 offset:32768\n" \
M_"v[144:147], a[188:191], a[148:151], v[144:147]\n" \
R_"a[208:211], v29 offset:32768\n" \
M_"v[144:147], a[252:255], a[212:215], v[144:147]\n" \
"s_add_u32 m0, s31, 0x4000\n" \
"s_mov_b32 s84, 0x200000\n" \
M_"v[140:143], a[184:187], a[148:151], v[140:143]\n" \
B_"v26, s[72:75], s84 offen offset:0 lds\n" \
M_"v[140:143], a[248:251], a[212:215], v[140:143]\n" \
X_"a[230:231], v33 offset:41984\n" \
M_"v[136:139], a[180:183], a[148:151], v[136:139]\n" \
"s_addk_i32 s84, 0x7c00\n" \
"s_nop 0\n" \
M_"v[136:139], a[244:247], a[212:215], v[136:139]\n" \
B_"v26, s[72:75], s84 offen offset:1024 lds\n" \
M_"v[132:135], a[176:179], a[148:151], v[132:135]\n" \
X_"a[168:169], v34 offset:32768\n" \
M_"v[132:135], a[240:243], a[212:215], v[132:135]\n" \
R_"a[148:151], v28 offset:34816\n" \
M_"v[148:151], a[176:179], a[152:155], v[148:151]\n" \
R_"a[212:215], v29 offset:34816\n" \
M_"v[148:151], a[240:243], a[216:219], v[148:151]\n" \
X_"a[170:171], v34 offset:33792\n" \
M_"v[152:155], a[180:183], a[152:155], v[152:155]\n" \
X_"a[232:233], v34 offset:40960\n" \
M_"v[152:155], a[244:247], a[216:219], v[152:155]\n" \
X_"a[234:235], v34 offset:41984\n" \
M_"v[156:159], a[184:187], a[152:155], v[156:159]\n" \
X_"a[172:173], v35 offset:32768\n" \
M_"v[156:159], a[248:251], a[216:219], v[156:159]\n" \
X_"a[174:175], v35 offset:33792\n" \
M_"v[160:163], a[188:191], a[152:155], v[160:163]\n" \
X_"a[236:237], v35 offset:40960\n" \
M_"v[160:163], a[252:255], a[216:219], v[160:163]\n" \
R_"a[152:155], v28 offset:36864\n" \
M_"v[176:179], a[188:191], a[156:159], v[176:179]\n" \
R_"a[216:219], v29 offset:36864\n" \
M_"v[176:179], a[252:255], a[220:223], v[176:179]\n" \
X_"a[238:239], v35 offset:41984\n" \
M_"v[172:175], a[184:187], a[156:159], v[172:175]\n" \
"s_cmpk_lt_u32 s3, 2\n" \
"s_cselect_b32 s6, s5, s4\n" \
M_"v[172:175], a[248:251], a[220:223], v[172:175]\n" \
"s_lshl_b32 s7, s6, 7\n" \
"v_and_b32_e32 v213, 0x320, v20\n" \
M_"v[168:171], a[180:183], a[156:159], v[168:171]\n" \
"s_movk_i32 s41, 0x100\n" \
"v_lshlrev_b32_e64 v214, v214, s42\n" \
M_"v[168:171], a[244:247], a[220:223], v[168:171]\n" \
"v_mov_b32_e32 v215, 0\n" \
"s_add_i32 s40, s7, -16\n" \
M_"v[164:167], a[176:179], a[156:159], v[164:167]\n" \
"v_cndmask_b32_e32 v41, v217, v216, vcc\n" \
"v_cndmask_b32_e32 v43, v216, v217, vcc\n" \
M_"v[164:167], a[240:243], a[220:223], v[164:167]\n" \
R_"a[156:159], v28 offset:38912\n" \
R_"a[220:223], v29 offset:38912\n" \
"s_waitcnt lgkmcnt(15)\n" \
M_"a[0:3], a[160:163], a[128:131], a[0:3]\n" \
"s_addk_i32 s84, 0x7c00\n" \
M_"a[0:3], a[224:227], a[192:195], a[0:3]\n" \
B_"v27, s[72:75], s84 offen offset:2048 lds\n" \
M_"a[4:7], a[164:167], a[128:131], a[4:7]\n" \
X_"a[176:177], v36 offset:32768\n" \
"v_cmp_eq_u32_e32 vcc, s41, v213\n" \
"s_waitcnt lgkmcnt(15)\n" \
M_"a[4:7], a[228:231], a[192:195], a[4:7]\n" \
"s_addk_i32 s84, 0x7c00\n" \
"s_waitcnt lgkmcnt(11)\n" \
M_"a[8:11], a[168:171], a[128:131], a[8:11]\n" \
B_"v27, s[72:75], s84 offen offset:3072 lds\n" \
"s_waitcnt lgkmcnt(9)\n" \
M_"a[8:11], a[232:235], a[192:195], a[8:11]\n" \
X_"a[178:179], v36 offset:33792\n" \
"s_mov_b32 s41, 0\n" \
"s_waitcnt lgkmcnt(8)\n" \
M_"a[12:15], a[172:175], a[128:131], a[12:15]\n" \
X_"a[240:241], v36 offset:40960\n" \
"s_and_b32 s43, s3, 1\n" \
"s_waitcnt lgkmcnt(5)\n" \
M_"a[12:15], a[236:239], a[192:195], a[12:15]\n" \
"v_cndmask_b32_e32 v46, v215, v214, vcc\n" \
"v_and_b32_e32 v213, 0x330, v20\n" \
M_"a[28:31], a[172:175], a[132:135], a[28:31]\n" \
"s_cmp_lg_u32 s7, 0\n" \
"s_cselect_b32 s40, s40, s41\n" \
M_"a[28:31], a[236:239], a[196:199], a[28:31]\n" \
X_"a[242:243], v36 offset:41984\n" \
"s_mov_b32 s42, 0xff800000\n" \
M_"a[24:27], a[168:171], a[132:135], a[24:27]\n" \
"s_cmp_lg_u32 s7, 0\n" \
"s_cselect_b32 s47, s41, s42\n" \
M_"a[24:27], a[232:235], a[196:199], a[24:27]\n" \
"s_lshl_b32 s41, s43, 6\n" \
"v_lshrrev_b32_e32 v215, 6, v20\n" \
M_"a[20:23], a[164:167], a[132:135], a[20:23]\n" \
"v_mov_b32_e32 v214, 0x3f80\n" \
"v_cmp_eq_u32_e64 vcc, 32, v213\n" \
M_"a[20:23], a[228:231], a[196:199], a[20:23]\n" \
X_"a[180:181], v37 offset:32768\n" \
"s_or_b32 s7, s41, s7\n" \
M_"a[16:19], a[160:163], a[132:135], a[16:19]\n" \
"v_lshrrev_b32_e32 v213, 7, v20\n" \
"v_bfe_u32 v216, v20, 6, 2\n" \
M_"a[16:19], a[224:227], a[196:199], a[16:19]\n" \
"v_lshrrev_b32_e64 v219, 1, v20\n" \
"v_bfe_u32 v215, v215, 1, 1\n" \
M_"a[32:35], a[160:163], a[136:139], a[32:35]\n" \
X_"a[182:183], v37 offset:33792\n" \
"v_cndmask_b32_e64 v47, 0, v214, vcc\n" \
M_"a[32:35], a[224:227], a[200:203], a[32:35]\n" \
"v_or_b32_e64 v214, s7, v213\n" \
"v_lshl_or_b32 v217, s43, 3, v213\n" \
M_"a[36:39], a[164:167], a[136:139], a[36:39]\n" \
"v_lshlrev_b32_e32 v218, 10, v212\n" \
"v_lshlrev_b32_e32 v216, 7, v216\n" \
M_"a[36:39], a[228:231], a[200:203], a[36:39]\n" \
X_"a[244:245], v37 offset:40960\n" \
"v_and_b32_e64 v219, 24, v219\n" \
M_"a[40:43], a[168:171], a[136:139], a[40:43]\n" \
"s_mov_b32 m0, s32\n" \
"v_and_or_b32 v213, v213, 2, v215\n" \
M_"a[40:43], a[232:235], a[200:203], a[40:43]\n" \
"s_mov_b32 exec_lo, 0xffffffff\n" \
"s_mov_b32 exec_hi, 0x0\n" \
"global_load_lds_dword v[188:189], off\n" \
"s_mov_b64 exec, -1\n" \
M_"a[44:47], a[172:175], a[136:139], a[44:47]\n" \
X_"a[246:247], v37 offset:41984\n" \
"v_add_u32_e64 v217, s40, v217\n" \
M_"a[44:47], a[236:239], a[200:203], a[44:47]\n" \
"v_or3_b32 v216, v216, v218, v219\n" \
"v_lshlrev_b32_e64 v213, 5, v213\n" \
M_"a[60:63], a[172:175], a[140:143], a[60:63]\n" \
"s_mov_b32 s40, 0x14000\n" \
"s_lshl_b32 s7, s2, 2\n" \
M_"a[60:63], a[236:239], a[204:207], a[60:63]\n" \
"v_or3_b32 v48, v216, v213, s40\n" \
"s_lshl_b32 s40, s43, 2\n" \
M_"a[56:59], a[168:171], a[140:143], a[56:59]\n" \
X_"a[184:185], v38 offset:32768\n" \
"s_lshl_b32 s6, s6, 3\n" \
M_"a[56:59], a[232:235], a[204:207], a[56:59]\n" \
"v_or_b32_e32 v213, s40, v212\n" \
"s_or_b32 s6, s6, s40\n" \
M_"a[52:55], a[164:167], a[140:143], a[52:55]\n" \
"s_and_b32 s40, s7, 0x7c\n" \
"s_or_b32 s40, s40, s3\n" \
M_"a[52:55], a[228:231], a[204:207], a[52:55]\n" \
X_"a[186:187], v38 offset:33792\n" \
M_"a[48:51], a[160:163], a[140:143], a[48:51]\n" \
"s_cmpk_lt_u32 s40, 64\n" \
"s_cselect_b32 s41, s4, s5\n" \
M_"a[48:51], a[224:227], a[204:207], a[48:51]\n" \
"s_lshl_b32 s40, s40, 1\n" \
"s_lshl_b32 s41, s41, 7\n" \
M_"a[64:67], a[160:163], a[144:147], a[64:67]\n" \
X_"a[248:249], v38 offset:40960\n" \
"s_and_b32 s40, s40, 0x7e\n" \
M_"a[64:67], a[224:227], a[208:211], a[64:67]\n" \
"s_or_b32 s40, s41, s40\n" \
"s_mulk_i32 s40, 0x480\n" \
M_"a[68:71], a[164:167], a[144:147], a[68:71]\n" \
"v_add_u32_e64 v49, s40, v20\n" \
"s_mov_b32 s40, 0xc000\n" \
M_"a[68:71], a[228:231], a[208:211], a[68:71]\n" \
X_"a[250:251], v38 offset:41984\n" \
"s_and_b32 s7, s7, 28\n" \
M_"a[72:75], a[168:171], a[144:147], a[72:75]\n" \
"s_bfe_u32 s45, s2, 0x20003\n" \
"s_mov_b32 s41, 0x8000\n" \
M_"a[72:75], a[232:235], a[208:211], a[72:75]\n" \
"s_cmp_lg_u32 s4, 0\n" \
"s_cselect_b32 s51, s40, s41\n" \
M_"a[76:79], a[172:175], a[144:147], a[76:79]\n" \
"s_movk_i32 s40, 0x4000\n" \
"s_lshr_b32 s44, s3, 1\n" \
M_"a[76:79], a[236:239], a[208:211], a[76:79]\n" \
X_"a[188:189], v39 offset:32768\n" \
"s_or_b32 s46, s7, s45\n" \
M_"a[92:95], a[172:175], a[148:151], a[92:95]\n" \
"s_cmp_lg_u32 s4, 0\n" \
"s_cselect_b32 s53, s41, s40\n" \
M_"a[92:95], a[236:239], a[212:215], a[92:95]\n" \
"s_lshl_b32 s40, s44, 15\n" \
"s_lshl_b32 s41, s43, 14\n" \
M_"a[88:91], a[168:171], a[148:151], a[88:91]\n" \
X_"a[190:191], v39 offset:33792\n" \
"s_lshl_b32 s48, s46, 20\n" \
M_"a[88:91], a[232:235], a[212:215], a[88:91]\n" \
"s_or_b32 s41, s40, s41\n" \
"s_add_i32 s49, s48, 0x1000000\n" \
M_"a[84:87], a[164:167], a[148:151], a[84:87]\n" \
"s_waitcnt vmcnt(17)\n" \
"v_or_b32_e32 v204, v210, v204\n" \
"v_or_b32_e64 v206, v210, v206\n" \
M_"a[84:87], a[228:231], a[212:215], a[84:87]\n" \
"v_mov_b32_e64 v210, 0\n" \
"s_add_u32 s88, s35, 0x1000\n" \
M_"a[80:83], a[160:163], a[148:151], a[80:83]\n" \
X_"a[252:253], v39 offset:40960\n" \
"s_or_b32 s42, s49, s41\n" \
M_"a[80:83], a[224:227], a[212:215], a[80:83]\n" \
"ds_write_b128 v196, v[200:203]\n" \
M_"a[96:99], a[160:163], a[152:155], a[96:99]\n" \
X_"a[254:255], v39 offset:41984\n" \
"s_lshl_b32 s4, s4, 16\n" \
M_"a[96:99], a[224:227], a[216:219], a[96:99]\n" \
"s_or_b32 s41, s42, s4\n" \
"s_lshl_b32 s4, s5, 16\n" \
M_"a[100:103], a[164:167], a[152:155], a[100:103]\n" \
"ds_write_b128 v197, v[204:207]\n" \
M_"a[100:103], a[228:231], a[216:219], a[100:103]\n" \
"s_mul_i32 s5, s6, 0x24800\n" \
"s_lshl_b32 s6, s46, 9\n" \
M_"a[104:107], a[168:171], a[152:155], a[104:107]\n" \
"s_lshl_b32 s7, s7, 6\n" \
"s_add_i32 s6, s5, s6\n" \
M_"a[104:107], a[232:235], a[216:219], a[104:107]\n" \
"ds_write_b128 v198, v[208:211]\n" \
M_"a[108:111], a[172:175], a[152:155], a[108:111]\n" \
"s_or_b32 s5, s5, s7\n" \
"s_lshl_b32 s7, s45, 2\n" \
M_"a[108:111], a[236:239], a[216:219], a[108:111]\n" \
"s_or_b32 s5, s5, s7\n" \
"s_lshl_b32 s7, s3, 12\n" \
"s_waitcnt lgkmcnt(15)\n" \
M_"a[124:127], a[172:175], a[156:159], a[124:127]\n" \
"s_or_b32 s50, s49, s7\n" \
"s_lshl_b32 s7, s3, 11\n" \
M_"a[124:127], a[236:239], a[220:223], a[124:127]\n" \
"v_lshrrev_b32_e64 v215, 4, v20\n" \
"v_bfe_u32 v216, v20, 4, 3\n" \
M_"a[120:123], a[168:171], a[156:159], a[120:123]\n" \
"s_or_b32 s42, s42, s4\n" \
"s_lshl_b32 s4, s43, 11\n" \
M_"a[120:123], a[232:235], a[220:223], a[120:123]\n" \
"s_add_i32 s52, s48, s7\n" \
"s_add_i32 s53, s53, s7\n" \
M_"a[116:119], a[164:167], a[156:159], a[116:119]\n" \
"v_mul_u32_u24_e32 v214, 0x480, v214\n" \
"v_bitop3_b32 v218, v212, v215, 7 bitop3:0x78\n" \
M_"a[116:119], a[228:231], a[220:223], a[116:119]\n" \
"v_bitop3_b32 v212, v212, v216, 4 bitop3:0x36\n" \
"s_lshl_b32 s7, s3, 10\n" \
M_"a[112:115], a[160:163], a[156:159], a[112:115]\n" \
"s_or_b32 s4, s40, s4\n" \
"v_lshl_or_b32 v25, v212, 4, v214\n" \
"s_waitcnt vmcnt(1) lgkmcnt(0)\n" \
M_"a[112:115], a[224:227], a[220:223], a[112:115]\n" \
"s_barrier\n" \
M_"v[52:55], a[176:179], a[128:131], v[52:55]\n" \
"s_add_u32 m0, s30, 0x8000\n" \
"s_movk_i32 s84, 0x280\n" \
M_"v[52:55], a[240:243], a[192:195], v[52:55]\n" \
B_"v22, s[64:67], s84 offen offset:0 lds\n" \
M_"v[56:59], a[180:183], a[128:131], v[56:59]\n" \
"s_movk_i32 s84, 0x1e80\n" \
M_"v[56:59], a[244:247], a[192:195], v[56:59]\n" \
B_"v23, s[64:67], s84 offen offset:1024 lds\n" \
M_"v[60:63], a[184:187], a[128:131], v[60:63]\n" \
X_"a[160:161], v32 offset:0\n" \
"v_mul_u32_u24_e32 v212, 0x480, v217\n" \
M_"v[60:63], a[248:251], a[192:195], v[60:63]\n" \
"s_movk_i32 s84, 0x3a80\n" \
M_"v[64:67], a[188:191], a[128:131], v[64:67]\n" \
B_"v22, s[64:67], s84 offen offset:2048 lds\n" \
M_"v[64:67], a[252:255], a[192:195], v[64:67]\n" \
R_"a[128:131], v28 offset:16384\n" \
"v_bitop3_b32 v213, v215, v213, 7 bitop3:0x6c\n" \
M_"v[80:83], a[188:191], a[132:135], v[80:83]\n" \
R_"a[192:195], v29 offset:16384\n" \
"s_or_b32 s40, s7, 0x14000\n" \
M_"v[80:83], a[252:255], a[196:199], v[80:83]\n" \
"s_movk_i32 s84, 0x5680\n" \
M_"v[76:79], a[184:187], a[132:135], v[76:79]\n" \
B_"v23, s[64:67], s84 offen offset:3072 lds\n" \
M_"v[76:79], a[248:251], a[196:199], v[76:79]\n" \
"s_add_u32 m0, s31, 0x8000\n" \
"s_mov_b32 s84, 0x280000\n" \
M_"v[72:75], a[180:183], a[132:135], v[72:75]\n" \
B_"v26, s[68:71], s84 offen offset:0 lds\n" \
M_"v[72:75], a[244:247], a[196:199], v[72:75]\n" \
X_"a[162:163], v32 offset:1024\n" \
"s_lshl_b32 s43, s44, 13\n" \
M_"v[68:71], a[176:179], a[132:135], v[68:71]\n" \
X_"a[224:225], v32 offset:8192\n" \
"s_mulk_i32 s44, 0xe800\n" \
M_"v[68:71], a[240:243], a[196:199], v[68:71]\n" \
R_"a[132:135], v28 offset:18432\n" \
"s_lshl_b32 s7, s3, 8\n" \
M_"v[84:87], a[176:179], a[136:139], v[84:87]\n" \
R_"a[196:199], v29 offset:18432\n" \
"v_lshl_or_b32 v24, v218, 4, v214\n" \
M_"v[84:87], a[240:243], a[200:203], v[84:87]\n" \
"s_addk_i32 s84, 0x7c00\n" \
M_"v[88:91], a[180:183], a[136:139], v[88:91]\n" \
B_"v26, s[68:71], s84 offen offset:1024 lds\n" \
M_"v[88:91], a[244:247], a[200:203], v[88:91]\n" \
"s_addk_i32 s84, 0x7c00\n" \
M_"v[92:95], a[184:187], a[136:139], v[92:95]\n" \
B_"v27, s[68:71], s84 offen offset:2048 lds\n" \
M_"v[92:95], a[248:251], a[200:203], v[92:95]\n" \
"s_addk_i32 s84, 0x7c00\n" \
M_"v[96:99], a[188:191], a[136:139], v[96:99]\n" \
B_"v27, s[68:71], s84 offen offset:3072 lds\n" \
M_"v[96:99], a[252:255], a[200:203], v[96:99]\n" \
R_"a[136:139], v28 offset:20480\n" \
"v_lshl_or_b32 v21, v213, 4, v212\n" \
M_"v[112:115], a[188:191], a[140:143], v[112:115]\n" \
R_"a[200:203], v29 offset:20480\n" \
"s_add_i32 s44, s44, 0x17000\n" \
M_"v[112:115], a[252:255], a[204:207], v[112:115]\n" \
"s_add_u32 m0, s30, 0x9000\n" \
"s_mov_b32 s84, 0x8280\n" \
M_"v[108:111], a[184:187], a[140:143], v[108:111]\n" \
B_"v22, s[64:67], s84 offen offset:0 lds\n" \
M_"v[108:111], a[248:251], a[204:207], v[108:111]\n" \
"s_addk_i32 s84, 0x1c00\n" \
M_"v[104:107], a[180:183], a[140:143], v[104:107]\n" \
B_"v23, s[64:67], s84 offen offset:1024 lds\n" \
M_"v[104:107], a[244:247], a[204:207], v[104:107]\n" \
"s_addk_i32 s84, 0x1c00\n" \
M_"v[100:103], a[176:179], a[140:143], v[100:103]\n" \
B_"v22, s[64:67], s84 offen offset:2048 lds\n" \
M_"v[100:103], a[240:243], a[204:207], v[100:103]\n" \
R_"a[140:143], v28 offset:22528\n" \
"s_or_b32 s45, s7, 0x27000\n" \
M_"v[116:119], a[176:179], a[144:147], v[116:119]\n" \
R_"a[204:207], v29 offset:22528\n" \
"s_or_b32 s46, s4, 0x10000\n" \
M_"v[116:119], a[240:243], a[208:211], v[116:119]\n" \
"s_addk_i32 s84, 0x1c00\n" \
M_"v[120:123], a[180:183], a[144:147], v[120:123]\n" \
B_"v23, s[64:67], s84 offen offset:3072 lds\n" \
M_"v[120:123], a[244:247], a[208:211], v[120:123]\n" \
"s_add_u32 m0, s31, 0xc000\n" \
"s_mov_b32 s84, 0x280000\n" \
M_"v[124:127], a[184:187], a[144:147], v[124:127]\n" \
B_"v26, s[72:75], s84 offen offset:0 lds\n" \
M_"v[124:127], a[248:251], a[208:211], v[124:127]\n" \
"s_addk_i32 s84, 0x7c00\n" \
M_"v[128:131], a[188:191], a[144:147], v[128:131]\n" \
B_"v26, s[72:75], s84 offen offset:1024 lds\n" \
M_"v[128:131], a[252:255], a[208:211], v[128:131]\n" \
R_"a[144:147], v28 offset:0\n" \
"s_add_i32 s48, s6, 0x3000000\n" \
M_"v[144:147], a[188:191], a[148:151], v[144:147]\n" \
R_"a[208:211], v29 offset:0\n" \
"s_add_i32 s49, s5, 0x3004000\n" \
M_"v[144:147], a[252:255], a[212:215], v[144:147]\n" \
R_"v[200:203], v199 offset:0\n" \
"s_add_i32 s52, s52, 0x1010000\n" \
M_"v[140:143], a[184:187], a[148:151], v[140:143]\n" \
R_"v[204:207], v199 offset:4096\n" \
M_"v[140:143], a[248:251], a[212:215], v[140:143]\n" \
X_"a[226:227], v32 offset:9216\n" \
M_"v[136:139], a[180:183], a[148:151], v[136:139]\n" \
X_"a[164:165], v33 offset:0\n" \
M_"v[136:139], a[244:247], a[212:215], v[136:139]\n" \
R_"v[208:211], v199 offset:8192\n" \
M_"v[132:135], a[176:179], a[148:151], v[132:135]\n" \
X_"a[166:167], v33 offset:1024\n" \
M_"v[132:135], a[240:243], a[212:215], v[132:135]\n" \
R_"a[148:151], v28 offset:2048\n" \
M_"v[148:151], a[176:179], a[152:155], v[148:151]\n" \
R_"a[212:215], v29 offset:2048\n" \
M_"v[148:151], a[240:243], a[216:219], v[148:151]\n" \
X_"a[228:229], v33 offset:8192\n" \
M_"v[152:155], a[180:183], a[152:155], v[152:155]\n" \
X_"a[230:231], v33 offset:9216\n" \
M_"v[152:155], a[244:247], a[216:219], v[152:155]\n" \
X_"a[168:169], v34 offset:0\n" \
M_"v[156:159], a[184:187], a[152:155], v[156:159]\n" \
X_"a[170:171], v34 offset:1024\n" \
M_"v[156:159], a[248:251], a[216:219], v[156:159]\n" \
X_"a[232:233], v34 offset:8192\n" \
M_"v[160:163], a[188:191], a[152:155], v[160:163]\n" \
X_"a[234:235], v34 offset:9216\n" \
M_"v[160:163], a[252:255], a[216:219], v[160:163]\n" \
R_"a[152:155], v28 offset:4096\n" \
M_"v[176:179], a[188:191], a[156:159], v[176:179]\n" \
R_"a[216:219], v29 offset:4096\n" \
M_"v[176:179], a[252:255], a[220:223], v[176:179]\n" \
X_"a[172:173], v35 offset:0\n" \
M_"v[172:175], a[184:187], a[156:159], v[172:175]\n" \
X_"a[174:175], v35 offset:1024\n" \
M_"v[172:175], a[248:251], a[220:223], v[172:175]\n" \
X_"a[236:237], v35 offset:8192\n" \
M_"v[168:171], a[180:183], a[156:159], v[168:171]\n" \
X_"a[238:239], v35 offset:9216\n" \
M_"v[168:171], a[244:247], a[220:223], v[168:171]\n" \
M_"v[164:167], a[176:179], a[156:159], v[164:167]\n" \
M_"v[164:167], a[240:243], a[220:223], v[164:167]\n" \
R_"a[156:159], v28 offset:6144\n" \
R_"a[220:223], v29 offset:6144\n" \
"s_waitcnt lgkmcnt(15)\n" \
M_"a[0:3], a[160:163], a[128:131], a[0:3]\n" \
"s_addk_i32 s84, 0x7c00\n" \
M_"a[0:3], a[224:227], a[192:195], a[0:3]\n" \
B_"v27, s[72:75], s84 offen offset:2048 lds\n" \
M_"a[4:7], a[164:167], a[128:131], a[4:7]\n" \
X_"a[176:177], v36 offset:0\n" \
"s_waitcnt lgkmcnt(13)\n" \
M_"a[4:7], a[228:231], a[192:195], a[4:7]\n" \
"s_addk_i32 s84, 0x7c00\n" \
"s_waitcnt lgkmcnt(11)\n" \
M_"a[8:11], a[168:171], a[128:131], a[8:11]\n" \
B_"v27, s[72:75], s84 offen offset:3072 lds\n" \
"s_waitcnt lgkmcnt(9)\n" \
M_"a[8:11], a[232:235], a[192:195], a[8:11]\n" \
X_"a[178:179], v36 offset:1024\n" \
"s_waitcnt lgkmcnt(6)\n" \
M_"a[12:15], a[172:175], a[128:131], a[12:15]\n" \
X_"a[240:241], v36 offset:8192\n" \
"s_waitcnt lgkmcnt(5)\n" \
M_"a[12:15], a[236:239], a[192:195], a[12:15]\n" \
M_"a[28:31], a[172:175], a[132:135], a[28:31]\n" \
M_"a[28:31], a[236:239], a[196:199], a[28:31]\n" \
X_"a[242:243], v36 offset:9216\n" \
M_"a[24:27], a[168:171], a[132:135], a[24:27]\n" \
M_"a[24:27], a[232:235], a[196:199], a[24:27]\n" \
".fill (L_pb + 12000 - .) / 4, 4, 0xBF800000\n" \
"L_pp3:\n" \
"s_cbranch_execz L_pret\n" \
"s_nop 0\n" \
M_"a[20:23], a[164:167], a[132:135], a[20:23]\n" \
M_"a[20:23], a[228:231], a[196:199], a[20:23]\n" \
X_"a[180:181], v37 offset:0\n" \
M_"a[16:19], a[160:163], a[132:135], a[16:19]\n" \
M_"a[16:19], a[224:227], a[196:199], a[16:19]\n" \
M_"a[32:35], a[160:163], a[136:139], a[32:35]\n" \
X_"a[182:183], v37 offset:1024\n" \
M_"a[32:35], a[224:227], a[200:203], a[32:35]\n" \
M_"a[36:39], a[164:167], a[136:139], a[36:39]\n" \
M_"a[36:39], a[228:231], a[200:203], a[36:39]\n" \
"buffer_store_dwordx4 v[200:203], v20, s[80:83], s35 offen sc1\n" \
M_"a[40:43], a[168:171], a[136:139], a[40:43]\n" \
"s_mov_b32 m0, s32\n" \
X_"a[244:245], v37 offset:8192\n" \
M_"a[40:43], a[232:235], a[200:203], a[40:43]\n" \
"s_mov_b32 exec_lo, 0x0\n" \
"s_mov_b32 exec_hi, 0xffffffff\n" \
"global_load_lds_dword v[188:189], off\n" \
"s_mov_b64 exec, -1\n" \
M_"a[44:47], a[172:175], a[136:139], a[44:47]\n" \
X_"a[246:247], v37 offset:9216\n" \
M_"a[44:47], a[236:239], a[200:203], a[44:47]\n" \
M_"a[60:63], a[172:175], a[140:143], a[60:63]\n" \
M_"a[60:63], a[236:239], a[204:207], a[60:63]\n" \
M_"a[56:59], a[168:171], a[140:143], a[56:59]\n" \
X_"a[184:185], v38 offset:0\n" \
M_"a[56:59], a[232:235], a[204:207], a[56:59]\n" \
M_"a[52:55], a[164:167], a[140:143], a[52:55]\n" \
"buffer_store_dwordx4 v[204:207], v20, s[80:83], s88 offen sc1\n" \
M_"a[52:55], a[228:231], a[204:207], a[52:55]\n" \
X_"a[186:187], v38 offset:1024\n" \
M_"a[48:51], a[160:163], a[140:143], a[48:51]\n" \
M_"a[48:51], a[224:227], a[204:207], a[48:51]\n" \
M_"a[64:67], a[160:163], a[144:147], a[64:67]\n" \
X_"a[248:249], v38 offset:8192\n" \
M_"a[64:67], a[224:227], a[208:211], a[64:67]\n" \
M_"a[68:71], a[164:167], a[144:147], a[68:71]\n" \
M_"a[68:71], a[228:231], a[208:211], a[68:71]\n" \
X_"a[250:251], v38 offset:9216\n" \
M_"a[72:75], a[168:171], a[144:147], a[72:75]\n" \
M_"a[72:75], a[232:235], a[208:211], a[72:75]\n" \
M_"a[76:79], a[172:175], a[144:147], a[76:79]\n" \
"buffer_store_dwordx4 v[208:211], v20, s[80:83], s36 offen sc1\n" \
M_"a[76:79], a[236:239], a[208:211], a[76:79]\n" \
X_"a[188:189], v39 offset:0\n" \
M_"a[92:95], a[172:175], a[148:151], a[92:95]\n" \
M_"a[92:95], a[236:239], a[212:215], a[92:95]\n" \
M_"a[88:91], a[168:171], a[148:151], a[88:91]\n" \
X_"a[190:191], v39 offset:1024\n" \
M_"a[88:91], a[232:235], a[212:215], a[88:91]\n" \
M_"a[84:87], a[164:167], a[148:151], a[84:87]\n" \
M_"a[84:87], a[228:231], a[212:215], a[84:87]\n" \
X_"a[252:253], v39 offset:8192\n" \
M_"a[80:83], a[160:163], a[148:151], a[80:83]\n" \
M_"a[80:83], a[224:227], a[212:215], a[80:83]\n" \
M_"a[96:99], a[160:163], a[152:155], a[96:99]\n" \
X_"a[254:255], v39 offset:9216\n" \
M_"a[96:99], a[224:227], a[216:219], a[96:99]\n" \
M_"a[100:103], a[164:167], a[152:155], a[100:103]\n" \
M_"a[100:103], a[228:231], a[216:219], a[100:103]\n" \
M_"a[104:107], a[168:171], a[152:155], a[104:107]\n" \
M_"a[104:107], a[232:235], a[216:219], a[104:107]\n" \
M_"a[108:111], a[172:175], a[152:155], a[108:111]\n" \
M_"a[108:111], a[236:239], a[216:219], a[108:111]\n" \
"s_waitcnt lgkmcnt(15)\n" \
M_"a[124:127], a[172:175], a[156:159], a[124:127]\n" \
M_"a[124:127], a[236:239], a[220:223], a[124:127]\n" \
M_"a[120:123], a[168:171], a[156:159], a[120:123]\n" \
M_"a[120:123], a[232:235], a[220:223], a[120:123]\n" \
M_"a[116:119], a[164:167], a[156:159], a[116:119]\n" \
M_"a[116:119], a[228:231], a[220:223], a[116:119]\n" \
M_"a[112:115], a[160:163], a[156:159], a[112:115]\n" \
"s_waitcnt vmcnt(4) lgkmcnt(0)\n" \
M_"a[112:115], a[224:227], a[220:223], a[112:115]\n" \
"s_barrier\n" \
M_"v[52:55], a[176:179], a[128:131], v[52:55]\n" \
"s_add_u32 m0, s30, 0x0\n" \
"s_movk_i32 s84, 0x300\n" \
M_"v[52:55], a[240:243], a[192:195], v[52:55]\n" \
B_"v22, s[64:67], s84 offen offset:0 lds\n" \
M_"v[56:59], a[180:183], a[128:131], v[56:59]\n" \
X_"a[160:161], v32 offset:32768\n" \
M_"v[56:59], a[244:247], a[192:195], v[56:59]\n" \
"s_movk_i32 s84, 0x1f00\n" \
M_"v[60:63], a[184:187], a[128:131], v[60:63]\n" \
B_"v23, s[64:67], s84 offen offset:1024 lds\n" \
M_"v[60:63], a[248:251], a[192:195], v[60:63]\n" \
X_"a[162:163], v32 offset:33792\n" \
M_"v[64:67], a[188:191], a[128:131], v[64:67]\n" \
X_"a[224:225], v32 offset:40960\n" \
M_"v[64:67], a[252:255], a[192:195], v[64:67]\n" \
R_"a[128:131], v28 offset:49152\n" \
M_"v[80:83], a[188:191], a[132:135], v[80:83]\n" \
R_"a[192:195], v29 offset:49152\n" \
M_"v[80:83], a[252:255], a[196:199], v[80:83]\n" \
"s_movk_i32 s84, 0x3b00\n" \
M_"v[76:79], a[184:187], a[132:135], v[76:79]\n" \
B_"v22, s[64:67], s84 offen offset:2048 lds\n" \
M_"v[76:79], a[248:251], a[196:199], v[76:79]\n" \
"s_movk_i32 s84, 0x5700\n" \
M_"v[72:75], a[180:183], a[132:135], v[72:75]\n" \
B_"v23, s[64:67], s84 offen offset:3072 lds\n" \
M_"v[72:75], a[244:247], a[196:199], v[72:75]\n" \
"s_add_u32 m0, s31, 0x0\n" \
"s_mov_b32 s84, 0x300000\n" \
M_"v[68:71], a[176:179], a[132:135], v[68:71]\n" \
B_"v26, s[68:71], s84 offen offset:0 lds\n" \
M_"v[68:71], a[240:243], a[196:199], v[68:71]\n" \
R_"a[132:135], v28 offset:51200\n" \
M_"v[84:87], a[176:179], a[136:139], v[84:87]\n" \
R_"a[196:199], v29 offset:51200\n" \
M_"v[84:87], a[240:243], a[200:203], v[84:87]\n" \
"s_addk_i32 s84, 0x7c00\n" \
M_"v[88:91], a[180:183], a[136:139], v[88:91]\n" \
B_"v26, s[68:71], s84 offen offset:1024 lds\n" \
M_"v[88:91], a[244:247], a[200:203], v[88:91]\n" \
X_"a[226:227], v32 offset:41984\n" \
M_"v[92:95], a[184:187], a[136:139], v[92:95]\n" \
"s_addk_i32 s84, 0x7c00\n" \
M_"v[92:95], a[248:251], a[200:203], v[92:95]\n" \
B_"v27, s[68:71], s84 offen offset:2048 lds\n" \
M_"v[96:99], a[188:191], a[136:139], v[96:99]\n" \
X_"a[164:165], v33 offset:32768\n" \
M_"v[96:99], a[252:255], a[200:203], v[96:99]\n" \
R_"a[136:139], v28 offset:53248\n" \
M_"v[112:115], a[188:191], a[140:143], v[112:115]\n" \
R_"a[200:203], v29 offset:53248\n" \
M_"v[112:115], a[252:255], a[204:207], v[112:115]\n" \
"s_addk_i32 s84, 0x7c00\n" \
M_"v[108:111], a[184:187], a[140:143], v[108:111]\n" \
B_"v27, s[68:71], s84 offen offset:3072 lds\n" \
M_"v[108:111], a[248:251], a[204:207], v[108:111]\n" \
"s_add_u32 m0, s30, 0x1000\n" \
"s_mov_b32 s84, 0x8300\n" \
M_"v[104:107], a[180:183], a[140:143], v[104:107]\n" \
B_"v22, s[64:67], s84 offen offset:0 lds\n" \
M_"v[104:107], a[244:247], a[204:207], v[104:107]\n" \
X_"a[166:167], v33 offset:33792\n" \
M_"v[100:103], a[176:179], a[140:143], v[100:103]\n" \
X_"a[228:229], v33 offset:40960\n" \
M_"v[100:103], a[240:243], a[204:207], v[100:103]\n" \
R_"a[140:143], v28 offset:55296\n" \
M_"v[116:119], a[176:179], a[144:147], v[116:119]\n" \
R_"a[204:207], v29 offset:55296\n" \
M_"v[116:119], a[240:243], a[208:211], v[116:119]\n" \
"s_addk_i32 s84, 0x1c00\n" \
M_"v[120:123], a[180:183], a[144:147], v[120:123]\n" \
B_"v23, s[64:67], s84 offen offset:1024 lds\n" \
M_"v[120:123], a[244:247], a[208:211], v[120:123]\n" \
"s_addk_i32 s84, 0x1c00\n" \
M_"v[124:127], a[184:187], a[144:147], v[124:127]\n" \
B_"v22, s[64:67], s84 offen offset:2048 lds\n" \
M_"v[124:127], a[248:251], a[208:211], v[124:127]\n" \
"s_addk_i32 s84, 0x1c00\n" \
"s_nop 0\n" \
M_"v[128:131], a[188:191], a[144:147], v[128:131]\n" \
B_"v23, s[64:67], s84 offen offset:3072 lds\n" \
M_"v[128:131], a[252:255], a[208:211], v[128:131]\n" \
R_"a[144:147], v28 offset:32768\n" \
M_"v[144:147], a[188:191], a[148:151], v[144:147]\n" \
R_"a[208:211], v29 offset:32768\n" \
M_"v[144:147], a[252:255], a[212:215], v[144:147]\n" \
"s_add_u32 m0, s31, 0x4000\n" \
"s_mov_b32 s84, 0x300000\n" \
M_"v[140:143], a[184:187], a[148:151], v[140:143]\n" \
B_"v26, s[72:75], s84 offen offset:0 lds\n" \
M_"v[140:143], a[248:251], a[212:215], v[140:143]\n" \
X_"a[230:231], v33 offset:41984\n" \
M_"v[136:139], a[180:183], a[148:151], v[136:139]\n" \
"s_addk_i32 s84, 0x7c00\n" \
"s_nop 0\n" \
M_"v[136:139], a[244:247], a[212:215], v[136:139]\n" \
B_"v26, s[72:75], s84 offen offset:1024 lds\n" \
M_"v[132:135], a[176:179], a[148:151], v[132:135]\n" \
X_"a[168:169], v34 offset:32768\n" \
M_"v[132:135], a[240:243], a[212:215], v[132:135]\n" \
R_"a[148:151], v28 offset:34816\n" \
M_"v[148:151], a[176:179], a[152:155], v[148:151]\n" \
R_"a[212:215], v29 offset:34816\n" \
M_"v[148:151], a[240:243], a[216:219], v[148:151]\n" \
X_"a[170:171], v34 offset:33792\n" \
M_"v[152:155], a[180:183], a[152:155], v[152:155]\n" \
X_"a[232:233], v34 offset:40960\n" \
M_"v[152:155], a[244:247], a[216:219], v[152:155]\n" \
X_"a[234:235], v34 offset:41984\n" \
M_"v[156:159], a[184:187], a[152:155], v[156:159]\n" \
X_"a[172:173], v35 offset:32768\n" \
M_"v[156:159], a[248:251], a[216:219], v[156:159]\n" \
X_"a[174:175], v35 offset:33792\n" \
M_"v[160:163], a[188:191], a[152:155], v[160:163]\n" \
X_"a[236:237], v35 offset:40960\n" \
M_"v[160:163], a[252:255], a[216:219], v[160:163]\n" \
R_"a[152:155], v28 offset:36864\n" \
M_"v[176:179], a[188:191], a[156:159], v[176:179]\n" \
R_"a[216:219], v29 offset:36864\n" \
M_"v[176:179], a[252:255], a[220:223], v[176:179]\n" \
X_"a[238:239], v35 offset:41984\n" \
M_"v[172:175], a[184:187], a[156:159], v[172:175]\n" \
M_"v[172:175], a[248:251], a[220:223], v[172:175]\n" \
M_"v[168:171], a[180:183], a[156:159], v[168:171]\n" \
M_"v[168:171], a[244:247], a[220:223], v[168:171]\n" \
M_"v[164:167], a[176:179], a[156:159], v[164:167]\n" \
M_"v[164:167], a[240:243], a[220:223], v[164:167]\n" \
R_"a[156:159], v28 offset:38912\n" \
R_"a[220:223], v29 offset:38912\n" \
"s_waitcnt lgkmcnt(15)\n" \
M_"a[0:3], a[160:163], a[128:131], a[0:3]\n" \
"s_addk_i32 s84, 0x7c00\n" \
M_"a[0:3], a[224:227], a[192:195], a[0:3]\n" \
B_"v27, s[72:75], s84 offen offset:2048 lds\n" \
M_"a[4:7], a[164:167], a[128:131], a[4:7]\n" \
X_"a[176:177], v36 offset:32768\n" \
"s_waitcnt lgkmcnt(15)\n" \
M_"a[4:7], a[228:231], a[192:195], a[4:7]\n" \
"s_addk_i32 s84, 0x7c00\n" \
"s_waitcnt lgkmcnt(11)\n" \
M_"a[8:11], a[168:171], a[128:131], a[8:11]\n" \
B_"v27, s[72:75], s84 offen offset:3072 lds\n" \
"s_waitcnt lgkmcnt(9)\n" \
M_"a[8:11], a[232:235], a[192:195], a[8:11]\n" \
X_"a[178:179], v36 offset:33792\n" \
"s_waitcnt lgkmcnt(8)\n" \
M_"a[12:15], a[172:175], a[128:131], a[12:15]\n" \
X_"a[240:241], v36 offset:40960\n" \
"s_waitcnt lgkmcnt(5)\n" \
M_"a[12:15], a[236:239], a[192:195], a[12:15]\n" \
M_"a[28:31], a[172:175], a[132:135], a[28:31]\n" \
M_"a[28:31], a[236:239], a[196:199], a[28:31]\n" \
X_"a[242:243], v36 offset:41984\n" \
M_"a[24:27], a[168:171], a[132:135], a[24:27]\n" \
M_"a[24:27], a[232:235], a[196:199], a[24:27]\n" \
M_"a[20:23], a[164:167], a[132:135], a[20:23]\n" \
M_"a[20:23], a[228:231], a[196:199], a[20:23]\n" \
X_"a[180:181], v37 offset:32768\n" \
M_"a[16:19], a[160:163], a[132:135], a[16:19]\n" \
M_"a[16:19], a[224:227], a[196:199], a[16:19]\n" \
M_"a[32:35], a[160:163], a[136:139], a[32:35]\n" \
X_"a[182:183], v37 offset:33792\n" \
M_"a[32:35], a[224:227], a[200:203], a[32:35]\n" \
M_"a[36:39], a[164:167], a[136:139], a[36:39]\n" \
"v_lshlrev_b32_e32 v45, 3, v20\n" \
"v_add_u32_e32 v45, s31, v45\n" \
M_"a[36:39], a[228:231], a[200:203], a[36:39]\n" \
"s_getpc_b64 s[86:87]\n" \
"s_sub_u32 s86, s86, 0xf800\n" \
"s_subb_u32 s87, s87, 0\n" \
"s_mov_b32 m0, s32\n" \
"s_nop 0\n" \
M_"a[40:43], a[168:171], a[136:139], a[40:43]\n" \
"global_load_lds_dword v45, s[86:87]\n" \
M_"a[40:43], a[232:235], a[200:203], a[40:43]\n" \
X_"a[244:245], v37 offset:40960\n" \
M_"a[44:47], a[172:175], a[136:139], a[44:47]\n" \
X_"a[246:247], v37 offset:41984\n" \
M_"a[44:47], a[236:239], a[200:203], a[44:47]\n" \
M_"a[60:63], a[172:175], a[140:143], a[60:63]\n" \
M_"a[60:63], a[236:239], a[204:207], a[60:63]\n" \
M_"a[56:59], a[168:171], a[140:143], a[56:59]\n" \
X_"a[184:185], v38 offset:32768\n" \
M_"a[56:59], a[232:235], a[204:207], a[56:59]\n" \
M_"a[52:55], a[164:167], a[140:143], a[52:55]\n" \
M_"a[52:55], a[228:231], a[204:207], a[52:55]\n" \
X_"a[186:187], v38 offset:33792\n" \
M_"a[48:51], a[160:163], a[140:143], a[48:51]\n" \
M_"a[48:51], a[224:227], a[204:207], a[48:51]\n" \
M_"a[64:67], a[160:163], a[144:147], a[64:67]\n" \
X_"a[248:249], v38 offset:40960\n" \
M_"a[64:67], a[224:227], a[208:211], a[64:67]\n" \
M_"a[68:71], a[164:167], a[144:147], a[68:71]\n" \
M_"a[68:71], a[228:231], a[208:211], a[68:71]\n" \
X_"a[250:251], v38 offset:41984\n" \
M_"a[72:75], a[168:171], a[144:147], a[72:75]\n" \
M_"a[72:75], a[232:235], a[208:211], a[72:75]\n" \
M_"a[76:79], a[172:175], a[144:147], a[76:79]\n" \
M_"a[76:79], a[236:239], a[208:211], a[76:79]\n" \
X_"a[188:189], v39 offset:32768\n" \
M_"a[92:95], a[172:175], a[148:151], a[92:95]\n" \
M_"a[92:95], a[236:239], a[212:215], a[92:95]\n" \
M_"a[88:91], a[168:171], a[148:151], a[88:91]\n" \
X_"a[190:191], v39 offset:33792\n" \
M_"a[88:91], a[232:235], a[212:215], a[88:91]\n" \
M_"a[84:87], a[164:167], a[148:151], a[84:87]\n" \
M_"a[84:87], a[228:231], a[212:215], a[84:87]\n" \
X_"a[252:253], v39 offset:40960\n" \
M_"a[80:83], a[160:163], a[148:151], a[80:83]\n" \
M_"a[80:83], a[224:227], a[212:215], a[80:83]\n" \
M_"a[96:99], a[160:163], a[152:155], a[96:99]\n" \
X_"a[254:255], v39 offset:41984\n" \
M_"a[96:99], a[224:227], a[216:219], a[96:99]\n" \
M_"a[100:103], a[164:167], a[152:155], a[100:103]\n" \
M_"a[100:103], a[228:231], a[216:219], a[100:103]\n" \
M_"a[104:107], a[168:171], a[152:155], a[104:107]\n" \
M_"a[104:107], a[232:235], a[216:219], a[104:107]\n" \
M_"a[108:111], a[172:175], a[152:155], a[108:111]\n" \
M_"a[108:111], a[236:239], a[216:219], a[108:111]\n" \
"s_waitcnt lgkmcnt(15)\n" \
M_"a[124:127], a[172:175], a[156:159], a[124:127]\n" \
M_"a[124:127], a[236:239], a[220:223], a[124:127]\n" \
M_"a[120:123], a[168:171], a[156:159], a[120:123]\n" \
M_"a[120:123], a[232:235], a[220:223], a[120:123]\n" \
M_"a[116:119], a[164:167], a[156:159], a[116:119]\n" \
M_"a[116:119], a[228:231], a[220:223], a[116:119]\n" \
M_"a[112:115], a[160:163], a[156:159], a[112:115]\n" \
"s_waitcnt vmcnt(1) lgkmcnt(0)\n" \
M_"a[112:115], a[224:227], a[220:223], a[112:115]\n" \
"s_barrier\n" \
"v_mov_b32_e32 v45, s62\n" \
"s_mov_b64 exec, 1\n" \
"buffer_store_dword v45, off, s[80:83], s37 offset:128 sc1\n" \
"s_mov_b64 exec, -1\n" \
M_"v[52:55], a[176:179], a[128:131], v[52:55]\n" \
"s_add_u32 m0, s30, 0x8000\n" \
"s_movk_i32 s84, 0x380\n" \
M_"v[52:55], a[240:243], a[192:195], v[52:55]\n" \
B_"v22, s[64:67], s84 offen offset:0 lds\n" \
M_"v[56:59], a[180:183], a[128:131], v[56:59]\n" \
X_"a[160:161], v32 offset:0\n" \
M_"v[56:59], a[244:247], a[192:195], v[56:59]\n" \
"s_movk_i32 s84, 0x1f80\n" \
M_"v[60:63], a[184:187], a[128:131], v[60:63]\n" \
B_"v23, s[64:67], s84 offen offset:1024 lds\n" \
M_"v[60:63], a[248:251], a[192:195], v[60:63]\n" \
X_"a[162:163], v32 offset:1024\n" \
M_"v[64:67], a[188:191], a[128:131], v[64:67]\n" \
X_"a[224:225], v32 offset:8192\n" \
M_"v[64:67], a[252:255], a[192:195], v[64:67]\n" \
R_"a[128:131], v28 offset:16384\n" \
M_"v[80:83], a[188:191], a[132:135], v[80:83]\n" \
R_"a[192:195], v29 offset:16384\n" \
M_"v[80:83], a[252:255], a[196:199], v[80:83]\n" \
"s_movk_i32 s84, 0x3b80\n" \
M_"v[76:79], a[184:187], a[132:135], v[76:79]\n" \
B_"v22, s[64:67], s84 offen offset:2048 lds\n" \
M_"v[76:79], a[248:251], a[196:199], v[76:79]\n" \
"s_movk_i32 s84, 0x5780\n" \
M_"v[72:75], a[180:183], a[132:135], v[72:75]\n" \
B_"v23, s[64:67], s84 offen offset:3072 lds\n" \
M_"v[72:75], a[244:247], a[196:199], v[72:75]\n" \
"s_add_u32 m0, s31, 0x8000\n" \
"s_mov_b32 s84, 0x380000\n" \
M_"v[68:71], a[176:179], a[132:135], v[68:71]\n" \
B_"v26, s[68:71], s84 offen offset:0 lds\n" \
M_"v[68:71], a[240:243], a[196:199], v[68:71]\n" \
R_"a[132:135], v28 offset:18432\n" \
M_"v[84:87], a[176:179], a[136:139], v[84:87]\n" \
R_"a[196:199], v29 offset:18432\n" \
M_"v[84:87], a[240:243], a[200:203], v[84:87]\n" \
"s_addk_i32 s84, 0x7c00\n" \
M_"v[88:91], a[180:183], a[136:139], v[88:91]\n" \
B_"v26, s[68:71], s84 offen offset:1024 lds\n" \
M_"v[88:91], a[244:247], a[200:203], v[88:91]\n" \
X_"a[226:227], v32 offset:9216\n" \
M_"v[92:95], a[184:187], a[136:139], v[92:95]\n" \
"s_addk_i32 s84, 0x7c00\n" \
M_"v[92:95], a[248:251], a[200:203], v[92:95]\n" \
B_"v27, s[68:71], s84 offen offset:2048 lds\n" \
M_"v[96:99], a[188:191], a[136:139], v[96:99]\n" \
X_"a[164:165], v33 offset:0\n" \
M_"v[96:99], a[252:255], a[200:203], v[96:99]\n" \
R_"a[136:139], v28 offset:20480\n" \
M_"v[112:115], a[188:191], a[140:143], v[112:115]\n" \
R_"a[200:203], v29 offset:20480\n" \
M_"v[112:115], a[252:255], a[204:207], v[112:115]\n" \
"s_addk_i32 s84, 0x7c00\n" \
"s_nop 0\n" \
M_"v[108:111], a[184:187], a[140:143], v[108:111]\n" \
B_"v27, s[68:71], s84 offen offset:3072 lds\n" \
M_"v[108:111], a[248:251], a[204:207], v[108:111]\n" \
"s_add_u32 m0, s30, 0x9000\n" \
"s_mov_b32 s84, 0x8380\n" \
M_"v[104:107], a[180:183], a[140:143], v[104:107]\n" \
B_"v22, s[64:67], s84 offen offset:0 lds\n" \
M_"v[104:107], a[244:247], a[204:207], v[104:107]\n" \
X_"a[166:167], v33 offset:1024\n" \
M_"v[100:103], a[176:179], a[140:143], v[100:103]\n" \
X_"a[228:229], v33 offset:8192\n" \
M_"v[100:103], a[240:243], a[204:207], v[100:103]\n" \
R_"a[140:143], v28 offset:22528\n" \
M_"v[116:119], a[176:179], a[144:147], v[116:119]\n" \
R_"a[204:207], v29 offset:22528\n" \
M_"v[116:119], a[240:243], a[208:211], v[116:119]\n" \
"s_addk_i32 s84, 0x1c00\n" \
M_"v[120:123], a[180:183], a[144:147], v[120:123]\n" \
B_"v23, s[64:67], s84 offen offset:1024 lds\n" \
M_"v[120:123], a[244:247], a[208:211], v[120:123]\n" \
"s_addk_i32 s84, 0x1c00\n" \
M_"v[124:127], a[184:187], a[144:147], v[124:127]\n" \
B_"v22, s[64:67], s84 offen offset:2048 lds\n" \
M_"v[124:127], a[248:251], a[208:211], v[124:127]\n" \
"s_addk_i32 s84, 0x1c00\n" \
"s_nop 0\n" \
M_"v[128:131], a[188:191], a[144:147], v[128:131]\n" \
B_"v23, s[64:67], s84 offen offset:3072 lds\n" \
M_"v[128:131], a[252:255], a[208:211], v[128:131]\n" \
R_"a[144:147], v28 offset:0\n" \
M_"v[144:147], a[188:191], a[148:151], v[144:147]\n" \
R_"a[208:211], v29 offset:0\n" \
M_"v[144:147], a[252:255], a[212:215], v[144:147]\n" \
"s_add_u32 m0, s31, 0xc000\n" \
"s_mov_b32 s84, 0x380000\n" \
M_"v[140:143], a[184:187], a[148:151], v[140:143]\n" \
B_"v26, s[72:75], s84 offen offset:0 lds\n" \
M_"v[140:143], a[248:251], a[212:215], v[140:143]\n" \
X_"a[230:231], v33 offset:9216\n" \
M_"v[136:139], a[180:183], a[148:151], v[136:139]\n" \
"s_addk_i32 s84, 0x7c00\n" \
"s_nop 0\n" \
M_"v[136:139], a[244:247], a[212:215], v[136:139]\n" \
B_"v26, s[72:75], s84 offen offset:1024 lds\n" \
M_"v[132:135], a[176:179], a[148:151], v[132:135]\n" \
X_"a[168:169], v34 offset:0\n" \
M_"v[132:135], a[240:243], a[212:215], v[132:135]\n" \
R_"a[148:151], v28 offset:2048\n" \
M_"v[148:151], a[176:179], a[152:155], v[148:151]\n" \
R_"a[212:215], v29 offset:2048\n" \
M_"v[148:151], a[240:243], a[216:219], v[148:151]\n" \
X_"a[170:171], v34 offset:1024\n" \
M_"v[152:155], a[180:183], a[152:155], v[152:155]\n" \
X_"a[232:233], v34 offset:8192\n" \
M_"v[152:155], a[244:247], a[216:219], v[152:155]\n" \
X_"a[234:235], v34 offset:9216\n" \
M_"v[156:159], a[184:187], a[152:155], v[156:159]\n" \
X_"a[172:173], v35 offset:0\n" \
M_"v[156:159], a[248:251], a[216:219], v[156:159]\n" \
X_"a[174:175], v35 offset:1024\n" \
M_"v[160:163], a[188:191], a[152:155], v[160:163]\n" \
X_"a[236:237], v35 offset:8192\n" \
M_"v[160:163], a[252:255], a[216:219], v[160:163]\n" \
R_"a[152:155], v28 offset:4096\n" \
M_"v[176:179], a[188:191], a[156:159], v[176:179]\n" \
R_"a[216:219], v29 offset:4096\n" \
M_"v[176:179], a[252:255], a[220:223], v[176:179]\n" \
X_"a[238:239], v35 offset:9216\n" \
M_"v[172:175], a[184:187], a[156:159], v[172:175]\n" \
M_"v[172:175], a[248:251], a[220:223], v[172:175]\n" \
M_"v[168:171], a[180:183], a[156:159], v[168:171]\n" \
M_"v[168:171], a[244:247], a[220:223], v[168:171]\n" \
M_"v[164:167], a[176:179], a[156:159], v[164:167]\n" \
M_"v[164:167], a[240:243], a[220:223], v[164:167]\n" \
R_"a[156:159], v28 offset:6144\n" \
R_"a[220:223], v29 offset:6144\n" \
"s_waitcnt lgkmcnt(15)\n" \
M_"a[0:3], a[160:163], a[128:131], a[0:3]\n" \
"s_addk_i32 s84, 0x7c00\n" \
M_"a[0:3], a[224:227], a[192:195], a[0:3]\n" \
B_"v27, s[72:75], s84 offen offset:2048 lds\n" \
M_"a[4:7], a[164:167], a[128:131], a[4:7]\n" \
X_"a[176:177], v36 offset:0\n" \
"s_waitcnt lgkmcnt(15)\n" \
M_"a[4:7], a[228:231], a[192:195], a[4:7]\n" \
"s_addk_i32 s84, 0x7c00\n" \
"s_waitcnt lgkmcnt(11)\n" \
M_"a[8:11], a[168:171], a[128:131], a[8:11]\n" \
B_"v27, s[72:75], s84 offen offset:3072 lds\n" \
"s_waitcnt lgkmcnt(9)\n" \
M_"a[8:11], a[232:235], a[192:195], a[8:11]\n" \
X_"a[178:179], v36 offset:1024\n" \
"s_waitcnt lgkmcnt(8)\n" \
M_"a[12:15], a[172:175], a[128:131], a[12:15]\n" \
X_"a[240:241], v36 offset:8192\n" \
"s_waitcnt lgkmcnt(5)\n" \
M_"a[12:15], a[236:239], a[192:195], a[12:15]\n" \
M_"a[28:31], a[172:175], a[132:135], a[28:31]\n" \
M_"a[28:31], a[236:239], a[196:199], a[28:31]\n" \
X_"a[242:243], v36 offset:9216\n" \
M_"a[24:27], a[168:171], a[132:135], a[24:27]\n" \
M_"a[24:27], a[232:235], a[196:199], a[24:27]\n" \
M_"a[20:23], a[164:167], a[132:135], a[20:23]\n" \
M_"a[20:23], a[228:231], a[196:199], a[20:23]\n" \
X_"a[180:181], v37 offset:0\n" \
M_"a[16:19], a[160:163], a[132:135], a[16:19]\n" \
M_"a[16:19], a[224:227], a[196:199], a[16:19]\n" \
M_"a[32:35], a[160:163], a[136:139], a[32:35]\n" \
"v_bfe_u32 v44, v20, 2, 5\n" \
X_"a[182:183], v37 offset:1024\n" \
M_"a[32:35], a[224:227], a[200:203], a[32:35]\n" \
"buffer_load_dword v44, v44, s[80:83], s38 offen offset:128 sc1\n" \
M_"a[36:39], a[164:167], a[136:139], a[36:39]\n" \
M_"a[36:39], a[228:231], a[200:203], a[36:39]\n" \
X_"a[244:245], v37 offset:8192\n" \
M_"a[40:43], a[168:171], a[136:139], a[40:43]\n" \
M_"a[40:43], a[232:235], a[200:203], a[40:43]\n" \
M_"a[44:47], a[172:175], a[136:139], a[44:47]\n" \
X_"a[246:247], v37 offset:9216\n" \
M_"a[44:47], a[236:239], a[200:203], a[44:47]\n" \
M_"a[60:63], a[172:175], a[140:143], a[60:63]\n" \
"global_store_dwordx4 v49, v[0:3], s[28:29] offset:0 sc1\n" \
M_"a[60:63], a[236:239], a[204:207], a[60:63]\n" \
M_"a[56:59], a[168:171], a[140:143], a[56:59]\n" \
X_"a[184:185], v38 offset:0\n" \
M_"a[56:59], a[232:235], a[204:207], a[56:59]\n" \
M_"a[52:55], a[164:167], a[140:143], a[52:55]\n" \
M_"a[52:55], a[228:231], a[204:207], a[52:55]\n" \
X_"a[186:187], v38 offset:1024\n" \
M_"a[48:51], a[160:163], a[140:143], a[48:51]\n" \
M_"a[48:51], a[224:227], a[204:207], a[48:51]\n" \
M_"a[64:67], a[160:163], a[144:147], a[64:67]\n" \
"global_store_dwordx4 v49, v[0:3], s[28:29] offset:1024 sc1\n" \
M_"a[64:67], a[224:227], a[208:211], a[64:67]\n" \
X_"a[248:249], v38 offset:8192\n" \
M_"a[68:71], a[164:167], a[144:147], a[68:71]\n" \
M_"a[68:71], a[228:231], a[208:211], a[68:71]\n" \
X_"a[250:251], v38 offset:9216\n" \
M_"a[72:75], a[168:171], a[144:147], a[72:75]\n" \
M_"a[72:75], a[232:235], a[208:211], a[72:75]\n" \
M_"a[76:79], a[172:175], a[144:147], a[76:79]\n" \
M_"a[76:79], a[236:239], a[208:211], a[76:79]\n" \
X_"a[188:189], v39 offset:0\n" \
M_"a[92:95], a[172:175], a[148:151], a[92:95]\n" \
"s_mov_b64 exec, 0xffff\n" \
"global_store_dwordx4 v49, v[0:3], s[28:29] offset:2048 sc1\n" \
"s_mov_b64 exec, -1\n" \
"s_nop 0\n" \
M_"a[92:95], a[236:239], a[212:215], a[92:95]\n" \
M_"a[88:91], a[168:171], a[148:151], a[88:91]\n" \
X_"a[190:191], v39 offset:1024\n" \
M_"a[88:91], a[232:235], a[212:215], a[88:91]\n" \
M_"a[84:87], a[164:167], a[148:151], a[84:87]\n" \
M_"a[84:87], a[228:231], a[212:215], a[84:87]\n" \
X_"a[252:253], v39 offset:8192\n" \
M_"a[80:83], a[160:163], a[148:151], a[80:83]\n" \
M_"a[80:83], a[224:227], a[212:215], a[80:83]\n" \
M_"a[96:99], a[160:163], a[152:155], a[96:99]\n" \
X_"a[254:255], v39 offset:9216\n" \
M_"a[96:99], a[224:227], a[216:219], a[96:99]\n" \
M_"a[100:103], a[164:167], a[152:155], a[100:103]\n" \
M_"a[100:103], a[228:231], a[216:219], a[100:103]\n" \
M_"a[104:107], a[168:171], a[152:155], a[104:107]\n" \
M_"a[104:107], a[232:235], a[216:219], a[104:107]\n" \
M_"a[108:111], a[172:175], a[152:155], a[108:111]\n" \
M_"a[108:111], a[236:239], a[216:219], a[108:111]\n" \
"s_waitcnt lgkmcnt(15)\n" \
M_"a[124:127], a[172:175], a[156:159], a[124:127]\n" \
M_"a[124:127], a[236:239], a[220:223], a[124:127]\n" \
M_"a[120:123], a[168:171], a[156:159], a[120:123]\n" \
M_"a[120:123], a[232:235], a[220:223], a[120:123]\n" \
M_"a[116:119], a[164:167], a[156:159], a[116:119]\n" \
M_"a[116:119], a[228:231], a[220:223], a[116:119]\n" \
M_"a[112:115], a[160:163], a[156:159], a[112:115]\n" \
"s_waitcnt vmcnt(3) lgkmcnt(0)\n" \
M_"a[112:115], a[224:227], a[220:223], a[112:115]\n" \
"s_barrier\n" \
"v_cmp_ne_u32_e32 vcc, s62, v44\n" \
"s_nop 4\n" \
"s_cbranch_vccnz L_poll60_%=\n" \
"L_pollr60_%=:\n" \
"s_mov_b32 s68, s12\n" \
"s_and_b32 s69, s13, 0xffff\n" \
".fill (L_pb + 16096 - .) / 4, 4, 0xBF800000\n" \
"L_pp4:\n" \
"s_cbranch_execz L_pret\n" \
"s_mov_b32 s70, 0x480000\n" \
"s_mov_b32 s71, 0x20000\n" \
M_"v[52:55], a[176:179], a[128:131], v[52:55]\n" \
"s_add_u32 m0, s40, 0x0\n" \
"s_add_u32 s85, s39, 0x0\n" \
"s_nop 0\n" \
M_"v[52:55], a[240:243], a[192:195], v[52:55]\n" \
B_"v20, s[80:83], s85 offen sc1 lds\n" \
M_"v[56:59], a[180:183], a[128:131], v[56:59]\n" \
X_"a[160:161], v32 offset:32768\n" \
M_"v[56:59], a[244:247], a[192:195], v[56:59]\n" \
"s_add_u32 m0, s40, 0x1000\n" \
"s_add_u32 s85, s39, 0x1000\n" \
M_"v[60:63], a[184:187], a[128:131], v[60:63]\n" \
B_"v20, s[80:83], s85 offen sc1 lds\n" \
M_"v[60:63], a[248:251], a[192:195], v[60:63]\n" \
X_"a[162:163], v32 offset:33792\n" \
M_"v[64:67], a[188:191], a[128:131], v[64:67]\n" \
X_"a[224:225], v32 offset:40960\n" \
M_"v[64:67], a[252:255], a[192:195], v[64:67]\n" \
R_"a[128:131], v28 offset:49152\n" \
M_"v[80:83], a[188:191], a[132:135], v[80:83]\n" \
R_"a[192:195], v29 offset:49152\n" \
M_"v[80:83], a[252:255], a[196:199], v[80:83]\n" \
"s_add_u32 m0, s40, 0x3000\n" \
"s_movk_i32 s84, 0x0\n" \
M_"v[76:79], a[184:187], a[132:135], v[76:79]\n" \
B_"v21, s[76:79], s84 offen offset:0 lds\n" \
M_"v[76:79], a[248:251], a[196:199], v[76:79]\n" \
"s_add_u32 m0, s30, 0x0\n" \
"s_movk_i32 s84, 0x0\n" \
"s_nop 0\n" \
M_"v[72:75], a[180:183], a[132:135], v[72:75]\n" \
B_"v24, s[76:79], s84 offen offset:0 lds\n" \
M_"v[72:75], a[244:247], a[196:199], v[72:75]\n" \
X_"a[226:227], v32 offset:41984\n" \
M_"v[68:71], a[176:179], a[132:135], v[68:71]\n" \
X_"a[164:165], v33 offset:32768\n" \
M_"v[68:71], a[240:243], a[196:199], v[68:71]\n" \
R_"a[132:135], v28 offset:51200\n" \
M_"v[84:87], a[176:179], a[136:139], v[84:87]\n" \
R_"a[196:199], v29 offset:51200\n" \
M_"v[84:87], a[240:243], a[200:203], v[84:87]\n" \
"s_movk_i32 s84, 0x2000\n" \
M_"v[88:91], a[180:183], a[136:139], v[88:91]\n" \
B_"v25, s[76:79], s84 offen offset:1024 lds\n" \
M_"v[88:91], a[244:247], a[200:203], v[88:91]\n" \
"s_movk_i32 s84, 0x4000\n" \
M_"v[92:95], a[184:187], a[136:139], v[92:95]\n" \
B_"v24, s[76:79], s84 offen offset:2048 lds\n" \
M_"v[92:95], a[248:251], a[200:203], v[92:95]\n" \
X_"a[166:167], v33 offset:33792\n" \
M_"v[96:99], a[188:191], a[136:139], v[96:99]\n" \
X_"a[228:229], v33 offset:40960\n" \
M_"v[96:99], a[252:255], a[200:203], v[96:99]\n" \
R_"a[136:139], v28 offset:53248\n" \
M_"v[112:115], a[188:191], a[140:143], v[112:115]\n" \
R_"a[200:203], v29 offset:53248\n" \
M_"v[112:115], a[252:255], a[204:207], v[112:115]\n" \
"s_movk_i32 s84, 0x6000\n" \
M_"v[108:111], a[184:187], a[140:143], v[108:111]\n" \
B_"v25, s[76:79], s84 offen offset:3072 lds\n" \
M_"v[108:111], a[248:251], a[204:207], v[108:111]\n" \
"s_add_u32 m0, s31, 0x0\n" \
"s_movk_i32 s84, 0x0\n" \
M_"v[104:107], a[180:183], a[140:143], v[104:107]\n" \
B_"v26, s[68:71], s84 offen offset:0 lds\n" \
M_"v[104:107], a[244:247], a[204:207], v[104:107]\n" \
"s_movk_i32 s84, 0x7c00\n" \
M_"v[100:103], a[176:179], a[140:143], v[100:103]\n" \
B_"v26, s[68:71], s84 offen offset:1024 lds\n" \
M_"v[100:103], a[240:243], a[204:207], v[100:103]\n" \
R_"a[140:143], v28 offset:55296\n" \
M_"v[116:119], a[176:179], a[144:147], v[116:119]\n" \
R_"a[204:207], v29 offset:55296\n" \
M_"v[116:119], a[240:243], a[208:211], v[116:119]\n" \
"s_addk_i32 s84, 0x7c00\n" \
M_"v[120:123], a[180:183], a[144:147], v[120:123]\n" \
B_"v27, s[68:71], s84 offen offset:2048 lds\n" \
M_"v[120:123], a[244:247], a[208:211], v[120:123]\n" \
X_"a[230:231], v33 offset:41984\n" \
M_"v[124:127], a[184:187], a[144:147], v[124:127]\n" \
"s_addk_i32 s84, 0x7c00\n" \
M_"v[124:127], a[248:251], a[208:211], v[124:127]\n" \
B_"v27, s[68:71], s84 offen offset:3072 lds\n" \
M_"v[128:131], a[188:191], a[144:147], v[128:131]\n" \
X_"a[168:169], v34 offset:32768\n" \
M_"v[128:131], a[252:255], a[208:211], v[128:131]\n" \
R_"a[144:147], v28 offset:32768\n" \
M_"v[144:147], a[188:191], a[148:151], v[144:147]\n" \
R_"a[208:211], v29 offset:32768\n" \
M_"v[144:147], a[252:255], a[212:215], v[144:147]\n" \
"s_add_u32 m0, s30, 0x1000\n" \
"s_mov_b32 s84, 0x9000\n" \
M_"v[140:143], a[184:187], a[148:151], v[140:143]\n" \
B_"v24, s[76:79], s84 offen offset:0 lds\n" \
M_"v[140:143], a[248:251], a[212:215], v[140:143]\n" \
X_"a[170:171], v34 offset:33792\n" \
M_"v[136:139], a[180:183], a[148:151], v[136:139]\n" \
"s_addk_i32 s84, 0x2000\n" \
"s_nop 0\n" \
M_"v[136:139], a[244:247], a[212:215], v[136:139]\n" \
B_"v25, s[76:79], s84 offen offset:1024 lds\n" \
M_"v[132:135], a[176:179], a[148:151], v[132:135]\n" \
X_"a[232:233], v34 offset:40960\n" \
M_"v[132:135], a[240:243], a[212:215], v[132:135]\n" \
R_"a[148:151], v28 offset:34816\n" \
M_"v[148:151], a[176:179], a[152:155], v[148:151]\n" \
R_"a[212:215], v29 offset:34816\n" \
M_"v[148:151], a[240:243], a[216:219], v[148:151]\n" \
X_"a[234:235], v34 offset:41984\n" \
M_"v[152:155], a[180:183], a[152:155], v[152:155]\n" \
X_"a[172:173], v35 offset:32768\n" \
M_"v[152:155], a[244:247], a[216:219], v[152:155]\n" \
X_"a[174:175], v35 offset:33792\n" \
M_"v[156:159], a[184:187], a[152:155], v[156:159]\n" \
X_"a[236:237], v35 offset:40960\n" \
M_"v[156:159], a[248:251], a[216:219], v[156:159]\n" \
X_"a[238:239], v35 offset:41984\n" \
M_"v[160:163], a[188:191], a[152:155], v[160:163]\n" \
M_"v[160:163], a[252:255], a[216:219], v[160:163]\n" \
R_"a[152:155], v28 offset:36864\n" \
M_"v[176:179], a[188:191], a[156:159], v[176:179]\n" \
R_"a[216:219], v29 offset:36864\n" \
M_"v[176:179], a[252:255], a[220:223], v[176:179]\n" \
M_"v[172:175], a[184:187], a[156:159], v[172:175]\n" \
M_"v[172:175], a[248:251], a[220:223], v[172:175]\n" \
M_"v[168:171], a[180:183], a[156:159], v[168:171]\n" \
M_"v[168:171], a[244:247], a[220:223], v[168:171]\n" \
M_"v[164:167], a[176:179], a[156:159], v[164:167]\n" \
M_"v[164:167], a[240:243], a[220:223], v[164:167]\n" \
R_"a[156:159], v28 offset:38912\n" \
R_"a[220:223], v29 offset:38912\n" \
"s_waitcnt lgkmcnt(15)\n" \
M_"a[0:3], a[160:163], a[128:131], a[0:3]\n" \
"s_addk_i32 s84, 0x2000\n" \
M_"a[0:3], a[224:227], a[192:195], a[0:3]\n" \
B_"v24, s[76:79], s84 offen offset:2048 lds\n" \
M_"a[4:7], a[164:167], a[128:131], a[4:7]\n" \
X_"a[176:177], v36 offset:32768\n" \
M_"a[4:7], a[228:231], a[192:195], a[4:7]\n" \
"s_addk_i32 s84, 0x2000\n" \
"s_waitcnt lgkmcnt(13)\n" \
M_"a[8:11], a[168:171], a[128:131], a[8:11]\n" \
B_"v25, s[76:79], s84 offen offset:3072 lds\n" \
"s_waitcnt lgkmcnt(9)\n" \
M_"a[8:11], a[232:235], a[192:195], a[8:11]\n" \
X_"a[178:179], v36 offset:33792\n" \
"s_waitcnt lgkmcnt(8)\n" \
M_"a[12:15], a[172:175], a[128:131], a[12:15]\n" \
X_"a[240:241], v36 offset:40960\n" \
"v_accvgpr_read_b32 v184, a0\n" \
"s_waitcnt lgkmcnt(7)\n" \
M_"a[12:15], a[236:239], a[192:195], a[12:15]\n" \
"v_accvgpr_read_b32 v185, a1\n" \
"v_accvgpr_read_b32 v186, a2\n" \
M_"a[28:31], a[172:175], a[132:135], a[28:31]\n" \
"v_accvgpr_read_b32 v187, a3\n" \
"v_accvgpr_read_b32 v180, a4\n" \
M_"a[28:31], a[236:239], a[196:199], a[28:31]\n" \
"s_add_u32 m0, s30, 0x20000\n" \
"s_movk_i32 s84, 0x100\n" \
M_"a[24:27], a[168:171], a[132:135], a[24:27]\n" \
B_"v24, s[76:79], s84 offen offset:0 lds\n" \
M_"a[24:27], a[232:235], a[196:199], a[24:27]\n" \
X_"a[242:243], v36 offset:41984\n" \
"v_accvgpr_read_b32 v181, a5\n" \
M_"a[20:23], a[164:167], a[132:135], a[20:23]\n" \
"v_accvgpr_read_b32 v182, a6\n" \
"v_accvgpr_read_b32 v183, a7\n" \
M_"a[20:23], a[228:231], a[196:199], a[20:23]\n" \
X_"a[180:181], v37 offset:32768\n" \
C_"v184, v184, v185\n" \
M_"a[16:19], a[160:163], a[132:135], a[16:19]\n" \
"s_movk_i32 s84, 0x2100\n" \
"s_nop 0\n" \
M_"a[16:19], a[224:227], a[196:199], a[16:19]\n" \
B_"v25, s[76:79], s84 offen offset:1024 lds\n" \
M_"a[32:35], a[160:163], a[136:139], a[32:35]\n" \
X_"a[182:183], v37 offset:33792\n" \
C_"v185, v186, v187\n" \
M_"a[32:35], a[224:227], a[200:203], a[32:35]\n" \
C_"v186, v180, v181\n" \
C_"v187, v182, v183\n" \
M_"a[36:39], a[164:167], a[136:139], a[36:39]\n" \
"v_accvgpr_read_b32 v188, a8\n" \
"v_accvgpr_read_b32 v189, a9\n" \
M_"a[36:39], a[228:231], a[200:203], a[36:39]\n" \
"s_movk_i32 s84, 0x4100\n" \
"s_nop 0\n" \
M_"a[40:43], a[168:171], a[136:139], a[40:43]\n" \
B_"v24, s[76:79], s84 offen offset:2048 lds\n" \
M_"a[40:43], a[232:235], a[200:203], a[40:43]\n" \
X_"a[244:245], v37 offset:40960\n" \
"v_accvgpr_read_b32 v190, a10\n" \
M_"a[44:47], a[172:175], a[136:139], a[44:47]\n" \
X_"a[246:247], v37 offset:41984\n" \
"v_accvgpr_read_b32 v191, a11\n" \
M_"a[44:47], a[236:239], a[200:203], a[44:47]\n" \
"v_accvgpr_read_b32 v180, a12\n" \
"v_accvgpr_read_b32 v181, a13\n" \
M_"a[60:63], a[172:175], a[140:143], a[60:63]\n" \
"s_movk_i32 s84, 0x6100\n" \
M_"a[60:63], a[236:239], a[204:207], a[60:63]\n" \
B_"v25, s[76:79], s84 offen offset:3072 lds\n" \
M_"a[56:59], a[168:171], a[140:143], a[56:59]\n" \
X_"a[184:185], v38 offset:32768\n" \
"v_accvgpr_read_b32 v182, a14\n" \
M_"a[56:59], a[232:235], a[204:207], a[56:59]\n" \
"v_accvgpr_read_b32 v183, a15\n" \
C_"v188, v188, v189\n" \
M_"a[52:55], a[164:167], a[140:143], a[52:55]\n" \
"s_add_u32 m0, s30, 0x21000\n" \
"s_addk_i32 s84, 0x3000\n" \
M_"a[52:55], a[228:231], a[204:207], a[52:55]\n" \
B_"v24, s[76:79], s84 offen offset:0 lds\n" \
M_"a[48:51], a[160:163], a[140:143], a[48:51]\n" \
X_"a[186:187], v38 offset:33792\n" \
C_"v189, v190, v191\n" \
M_"a[48:51], a[224:227], a[204:207], a[48:51]\n" \
C_"v190, v180, v181\n" \
C_"v191, v182, v183\n" \
M_"a[64:67], a[160:163], a[144:147], a[64:67]\n" \
X_"a[248:249], v38 offset:40960\n" \
"v_accvgpr_read_b32 v192, a24\n" \
M_"a[64:67], a[224:227], a[208:211], a[64:67]\n" \
"s_addk_i32 s84, 0x2000\n" \
"s_nop 0\n" \
M_"a[68:71], a[164:167], a[144:147], a[68:71]\n" \
B_"v25, s[76:79], s84 offen offset:1024 lds\n" \
M_"a[68:71], a[228:231], a[208:211], a[68:71]\n" \
X_"a[250:251], v38 offset:41984\n" \
"v_accvgpr_read_b32 v193, a25\n" \
M_"a[72:75], a[168:171], a[144:147], a[72:75]\n" \
"v_accvgpr_read_b32 v194, a26\n" \
"v_accvgpr_read_b32 v195, a27\n" \
M_"a[72:75], a[232:235], a[208:211], a[72:75]\n" \
"v_accvgpr_read_b32 v180, a28\n" \
"v_accvgpr_read_b32 v181, a29\n" \
M_"a[76:79], a[172:175], a[144:147], a[76:79]\n" \
"s_addk_i32 s84, 0x2000\n" \
M_"a[76:79], a[236:239], a[208:211], a[76:79]\n" \
B_"v24, s[76:79], s84 offen offset:2048 lds\n" \
M_"a[92:95], a[172:175], a[148:151], a[92:95]\n" \
"s_add_u32 s89, s41, 0x0\n" \
"buffer_store_dwordx4 v[184:187], v20, s[80:83], s89 offen offset:0 sc1\n" \
M_"a[92:95], a[236:239], a[212:215], a[92:95]\n" \
X_"a[188:189], v39 offset:32768\n" \
"v_accvgpr_read_b32 v182, a30\n" \
M_"a[88:91], a[168:171], a[148:151], a[88:91]\n" \
X_"a[190:191], v39 offset:33792\n" \
"v_accvgpr_read_b32 v183, a31\n" \
M_"a[88:91], a[232:235], a[212:215], a[88:91]\n" \
"s_addk_i32 s84, 0x2000\n" \
M_"a[84:87], a[164:167], a[148:151], a[84:87]\n" \
B_"v25, s[76:79], s84 offen offset:3072 lds\n" \
M_"a[84:87], a[228:231], a[212:215], a[84:87]\n" \
X_"a[252:253], v39 offset:40960\n" \
C_"v192, v192, v193\n" \
M_"a[80:83], a[160:163], a[148:151], a[80:83]\n" \
"s_add_u32 s88, s41, 0x0\n" \
"buffer_store_dwordx4 v[188:191], v20, s[80:83], s88 offen offset:1024 sc1\n" \
M_"a[80:83], a[224:227], a[212:215], a[80:83]\n" \
C_"v193, v194, v195\n" \
C_"v194, v180, v181\n" \
"s_waitcnt lgkmcnt(15)\n" \
"s_nop 0\n" \
M_"a[96:99], a[160:163], a[152:155], a[96:99]\n" \
X_"a[254:255], v39 offset:41984\n" \
C_"v195, v182, v183\n" \
M_"a[96:99], a[224:227], a[216:219], a[96:99]\n" \
"v_accvgpr_read_b32 v196, a16\n" \
"v_accvgpr_read_b32 v197, a17\n" \
M_"a[100:103], a[164:167], a[152:155], a[100:103]\n" \
"v_accvgpr_read_b32 v198, a18\n" \
"v_accvgpr_read_b32 v199, a19\n" \
M_"a[100:103], a[228:231], a[216:219], a[100:103]\n" \
"v_accvgpr_read_b32 v180, a20\n" \
"v_accvgpr_read_b32 v181, a21\n" \
M_"a[104:107], a[168:171], a[152:155], a[104:107]\n" \
"s_add_u32 s89, s41, 0x1000\n" \
"buffer_store_dwordx4 v[192:195], v20, s[80:83], s89 offen offset:1024 sc1\n" \
M_"a[104:107], a[232:235], a[216:219], a[104:107]\n" \
"v_accvgpr_read_b32 v182, a22\n" \
"v_accvgpr_read_b32 v183, a23\n" \
M_"a[108:111], a[172:175], a[152:155], a[108:111]\n" \
C_"v196, v196, v197\n" \
C_"v197, v198, v199\n" \
M_"a[108:111], a[236:239], a[216:219], a[108:111]\n" \
C_"v198, v180, v181\n" \
C_"v199, v182, v183\n" \
M_"a[124:127], a[172:175], a[156:159], a[124:127]\n" \
"v_accvgpr_read_b32 v200, a32\n" \
"v_accvgpr_read_b32 v201, a33\n" \
M_"a[124:127], a[236:239], a[220:223], a[124:127]\n" \
"v_accvgpr_read_b32 v202, a34\n" \
"v_accvgpr_read_b32 v203, a35\n" \
M_"a[120:123], a[168:171], a[156:159], a[120:123]\n" \
"s_add_u32 s88, s41, 0x1000\n" \
"buffer_store_dwordx4 v[196:199], v20, s[80:83], s88 offen offset:0 sc1\n" \
M_"a[120:123], a[232:235], a[220:223], a[120:123]\n" \
"v_accvgpr_read_b32 v180, a36\n" \
"v_accvgpr_read_b32 v181, a37\n" \
M_"a[116:119], a[164:167], a[156:159], a[116:119]\n" \
"v_accvgpr_read_b32 v182, a38\n" \
"v_accvgpr_read_b32 v183, a39\n" \
M_"a[116:119], a[228:231], a[220:223], a[116:119]\n" \
C_"v200, v200, v201\n" \
C_"v201, v202, v203\n" \
M_"a[112:115], a[160:163], a[156:159], a[112:115]\n" \
C_"v202, v180, v181\n" \
C_"v203, v182, v183\n" \
M_"a[112:115], a[224:227], a[220:223], a[112:115]\n" \
"v_accvgpr_read_b32 v204, a40\n" \
"v_accvgpr_read_b32 v205, a41\n" \
"s_waitcnt vmcnt(12) lgkmcnt(0)\n" \
"s_barrier\n" \
"v_mov_b32_e32 v36, v32\n" \
"v_xor_b32_e32 v32, 0x0, v48\n" \
"v_mov_b32_e32 v37, v33\n" \
"v_xor_b32_e32 v33, 0x20, v48\n" \
"v_mov_b32_e32 v38, v34\n" \
"v_xor_b32_e32 v34, 0x40, v48\n" \
"v_mov_b32_e64 v39, v35\n" \
"v_xor_b32_e32 v35, 0x60, v48\n" \
"v_add_u32_e32 v22, s44, v28\n" \
"v_add_u32_e32 v28, s43, v28\n" \
"v_add_u32_e32 v23, s44, v29\n" \
"v_add_u32_e32 v29, s43, v29\n" \
M_"v[52:55], a[176:179], a[128:131], v[52:55]\n" \
"s_add_u32 m0, s40, 0x8000\n" \
"s_add_u32 s85, s39, 0x2000\n" \
M_"v[52:55], a[240:243], a[192:195], v[52:55]\n" \
B_"v20, s[80:83], s85 offen sc1 lds\n" \
M_"v[56:59], a[180:183], a[128:131], v[56:59]\n" \
X_"a[160:161], v32 offset:0\n" \
"v_accvgpr_read_b32 v206, a42\n" \
M_"v[56:59], a[244:247], a[192:195], v[56:59]\n" \
"s_add_u32 m0, s40, 0x9000\n" \
"s_add_u32 s85, s39, 0x3000\n" \
M_"v[60:63], a[184:187], a[128:131], v[60:63]\n" \
B_"v20, s[80:83], s85 offen sc1 lds\n" \
M_"v[60:63], a[248:251], a[192:195], v[60:63]\n" \
X_"a[162:163], v32 offset:512\n" \
"v_accvgpr_read_b32 v207, a43\n" \
M_"v[64:67], a[188:191], a[128:131], v[64:67]\n" \
X_"a[224:225], v32 offset:4096\n" \
"v_accvgpr_read_b32 v180, a44\n" \
M_"v[64:67], a[252:255], a[192:195], v[64:67]\n" \
R_"a[128:131], v28 offset:0\n" \
"v_accvgpr_read_b32 v181, a45\n" \
M_"v[80:83], a[188:191], a[132:135], v[80:83]\n" \
R_"a[192:195], v29 offset:0\n" \
"v_accvgpr_read_b32 v182, a46\n" \
M_"v[80:83], a[252:255], a[196:199], v[80:83]\n" \
"s_add_u32 m0, s40, 0xb000\n" \
"s_movk_i32 s84, 0x80\n" \
M_"v[76:79], a[184:187], a[132:135], v[76:79]\n" \
B_"v21, s[76:79], s84 offen offset:0 lds\n" \
M_"v[76:79], a[248:251], a[196:199], v[76:79]\n" \
"s_add_u32 m0, s30, 0x8000\n" \
"s_movk_i32 s84, 0x80\n" \
M_"v[72:75], a[180:183], a[132:135], v[72:75]\n" \
B_"v24, s[76:79], s84 offen offset:0 lds\n" \
M_"v[72:75], a[244:247], a[196:199], v[72:75]\n" \
X_"a[226:227], v32 offset:4608\n" \
"v_accvgpr_read_b32 v183, a47\n" \
M_"v[68:71], a[176:179], a[132:135], v[68:71]\n" \
X_"a[164:165], v33 offset:0\n" \
C_"v204, v204, v205\n" \
M_"v[68:71], a[240:243], a[196:199], v[68:71]\n" \
R_"a[132:135], v28 offset:2048\n" \
C_"v205, v206, v207\n" \
M_"v[84:87], a[176:179], a[136:139], v[84:87]\n" \
R_"a[196:199], v29 offset:2048\n" \
C_"v206, v180, v181\n" \
M_"v[84:87], a[240:243], a[200:203], v[84:87]\n" \
"s_movk_i32 s84, 0x2080\n" \
M_"v[88:91], a[180:183], a[136:139], v[88:91]\n" \
B_"v25, s[76:79], s84 offen offset:1024 lds\n" \
M_"v[88:91], a[244:247], a[200:203], v[88:91]\n" \
"s_movk_i32 s84, 0x4080\n" \
M_"v[92:95], a[184:187], a[136:139], v[92:95]\n" \
B_"v24, s[76:79], s84 offen offset:2048 lds\n" \
M_"v[92:95], a[248:251], a[200:203], v[92:95]\n" \
X_"a[166:167], v33 offset:512\n" \
C_"v207, v182, v183\n" \
M_"v[96:99], a[188:191], a[136:139], v[96:99]\n" \
X_"a[228:229], v33 offset:4096\n" \
"v_accvgpr_read_b32 v208, a56\n" \
M_"v[96:99], a[252:255], a[200:203], v[96:99]\n" \
R_"a[136:139], v28 offset:4096\n" \
"v_accvgpr_read_b32 v209, a57\n" \
M_"v[112:115], a[188:191], a[140:143], v[112:115]\n" \
R_"a[200:203], v29 offset:4096\n" \
"v_accvgpr_read_b32 v210, a58\n" \
M_"v[112:115], a[252:255], a[204:207], v[112:115]\n" \
"s_movk_i32 s84, 0x6080\n" \
M_"v[108:111], a[184:187], a[140:143], v[108:111]\n" \
B_"v25, s[76:79], s84 offen offset:3072 lds\n" \
M_"v[108:111], a[248:251], a[204:207], v[108:111]\n" \
"s_add_u32 m0, s31, 0x8000\n" \
"s_mov_b32 s84, 0x80000\n" \
M_"v[104:107], a[180:183], a[140:143], v[104:107]\n" \
B_"v26, s[68:71], s84 offen offset:0 lds\n" \
M_"v[104:107], a[244:247], a[204:207], v[104:107]\n" \
"s_addk_i32 s84, 0x7c00\n" \
M_"v[100:103], a[176:179], a[140:143], v[100:103]\n" \
B_"v26, s[68:71], s84 offen offset:1024 lds\n" \
M_"v[100:103], a[240:243], a[204:207], v[100:103]\n" \
R_"a[140:143], v28 offset:6144\n" \
"v_accvgpr_read_b32 v211, a59\n" \
M_"v[116:119], a[176:179], a[144:147], v[116:119]\n" \
R_"a[204:207], v29 offset:6144\n" \
"v_accvgpr_read_b32 v180, a60\n" \
M_"v[116:119], a[240:243], a[208:211], v[116:119]\n" \
"s_addk_i32 s84, 0x7c00\n" \
M_"v[120:123], a[180:183], a[144:147], v[120:123]\n" \
B_"v27, s[68:71], s84 offen offset:2048 lds\n" \
M_"v[120:123], a[244:247], a[208:211], v[120:123]\n" \
X_"a[230:231], v33 offset:4608\n" \
"v_accvgpr_read_b32 v181, a61\n" \
M_"v[124:127], a[184:187], a[144:147], v[124:127]\n" \
"s_addk_i32 s84, 0x7c00\n" \
M_"v[124:127], a[248:251], a[208:211], v[124:127]\n" \
B_"v27, s[68:71], s84 offen offset:3072 lds\n" \
M_"v[128:131], a[188:191], a[144:147], v[128:131]\n" \
X_"a[172:173], v35 offset:0\n" \
"v_accvgpr_read_b32 v182, a62\n" \
M_"v[128:131], a[252:255], a[208:211], v[128:131]\n" \
R_"a[144:147], v28 offset:8192\n" \
"v_accvgpr_read_b32 v183, a63\n" \
M_"v[144:147], a[188:191], a[148:151], v[144:147]\n" \
R_"a[208:211], v29 offset:8192\n" \
C_"v208, v208, v209\n" \
M_"v[144:147], a[252:255], a[212:215], v[144:147]\n" \
"s_add_u32 m0, s30, 0x9000\n" \
"s_mov_b32 s84, 0x9080\n" \
M_"v[140:143], a[184:187], a[148:151], v[140:143]\n" \
B_"v24, s[76:79], s84 offen offset:0 lds\n" \
M_"v[140:143], a[248:251], a[212:215], v[140:143]\n" \
X_"a[174:175], v35 offset:512\n" \
C_"v209, v210, v211\n" \
M_"v[136:139], a[180:183], a[148:151], v[136:139]\n" \
"s_addk_i32 s84, 0x2000\n" \
"s_nop 0\n" \
M_"v[136:139], a[244:247], a[212:215], v[136:139]\n" \
B_"v25, s[76:79], s84 offen offset:1024 lds\n" \
M_"v[132:135], a[176:179], a[148:151], v[132:135]\n" \
X_"a[236:237], v35 offset:4096\n" \
C_"v210, v180, v181\n" \
M_"v[132:135], a[240:243], a[212:215], v[132:135]\n" \
R_"a[148:151], v28 offset:10240\n" \
C_"v211, v182, v183\n" \
M_"v[148:151], a[176:179], a[152:155], v[148:151]\n" \
R_"a[212:215], v29 offset:10240\n" \
"v_accvgpr_read_b32 v212, a48\n" \
M_"v[148:151], a[240:243], a[216:219], v[148:151]\n" \
X_"a[238:239], v35 offset:4608\n" \
"v_accvgpr_read_b32 v213, a49\n" \
M_"v[152:155], a[180:183], a[152:155], v[152:155]\n" \
X_"a[168:169], v34 offset:0\n" \
"v_accvgpr_read_b32 v214, a50\n" \
M_"v[152:155], a[244:247], a[216:219], v[152:155]\n" \
X_"a[170:171], v34 offset:512\n" \
"v_accvgpr_read_b32 v215, a51\n" \
M_"v[156:159], a[184:187], a[152:155], v[156:159]\n" \
X_"a[232:233], v34 offset:4096\n" \
"v_accvgpr_read_b32 v180, a52\n" \
M_"v[156:159], a[248:251], a[216:219], v[156:159]\n" \
X_"a[234:235], v34 offset:4608\n" \
"v_accvgpr_read_b32 v181, a53\n" \
M_"v[160:163], a[188:191], a[152:155], v[160:163]\n" \
R_"v[248:251], v22 offset:0\n" \
"v_accvgpr_read_b32 v182, a54\n" \
M_"v[160:163], a[252:255], a[216:219], v[160:163]\n" \
R_"a[152:155], v28 offset:12288\n" \
"v_accvgpr_read_b32 v183, a55\n" \
M_"v[176:179], a[188:191], a[156:159], v[176:179]\n" \
R_"a[216:219], v29 offset:12288\n" \
C_"v212, v212, v213\n" \
M_"v[176:179], a[252:255], a[220:223], v[176:179]\n" \
R_"v[48:51], v23 offset:0\n" \
C_"v213, v214, v215\n" \
M_"v[172:175], a[184:187], a[156:159], v[172:175]\n" \
C_"v214, v180, v181\n" \
C_"v215, v182, v183\n" \
M_"v[172:175], a[248:251], a[220:223], v[172:175]\n" \
"v_accvgpr_read_b32 v216, a64\n" \
"v_accvgpr_read_b32 v217, a65\n" \
M_"v[168:171], a[180:183], a[156:159], v[168:171]\n" \
"v_accvgpr_read_b32 v218, a66\n" \
"v_accvgpr_read_b32 v219, a67\n" \
M_"v[168:171], a[244:247], a[220:223], v[168:171]\n" \
"v_accvgpr_read_b32 v180, a68\n" \
"v_accvgpr_read_b32 v181, a69\n" \
M_"v[164:167], a[176:179], a[156:159], v[164:167]\n" \
"v_accvgpr_read_b32 v182, a70\n" \
"v_accvgpr_read_b32 v183, a71\n" \
M_"v[164:167], a[240:243], a[220:223], v[164:167]\n" \
R_"a[156:159], v28 offset:14336\n" \
R_"a[220:223], v29 offset:14336\n" \
"s_waitcnt lgkmcnt(15)\n" \
M_"a[0:3], a[160:163], a[128:131], 0\n" \
"s_addk_i32 s84, 0x2000\n" \
C_"v216, v216, v217\n" \
M_"a[0:3], a[224:227], a[192:195], a[0:3]\n" \
B_"v24, s[76:79], s84 offen offset:2048 lds\n" \
C_"v217, v218, v219\n" \
M_"a[4:7], a[164:167], a[128:131], 0\n" \
X_"a[176:177], v36 offset:0\n" \
C_"v218, v180, v181\n" \
M_"a[4:7], a[228:231], a[192:195], a[4:7]\n" \
"s_addk_i32 s84, 0x2000\n" \
C_"v219, v182, v183\n" \
"s_waitcnt lgkmcnt(15)\n" \
M_"a[12:15], a[172:175], a[128:131], 0\n" \
B_"v25, s[76:79], s84 offen offset:3072 lds\n" \
"v_accvgpr_read_b32 v220, a80\n" \
"s_waitcnt lgkmcnt(11)\n" \
M_"a[12:15], a[236:239], a[192:195], a[12:15]\n" \
X_"a[178:179], v36 offset:1024\n" \
"v_accvgpr_read_b32 v221, a81\n" \
"s_waitcnt lgkmcnt(10)\n" \
M_"a[8:11], a[128:131], a[168:171], 0\n" \
X_"a[240:241], v36 offset:8192\n" \
"v_accvgpr_read_b32 v222, a82\n" \
"s_waitcnt lgkmcnt(9)\n" \
"s_nop 0\n" \
M_"a[8:11], a[192:195], a[232:235], a[8:11]\n" \
X_"a[242:243], v36 offset:9216\n" \
"v_accvgpr_read_b32 v223, a83\n" \
M_"a[24:27], a[132:135], a[168:171], 0\n" \
X_"a[180:181], v37 offset:0\n" \
"v_accvgpr_read_b32 v180, a84\n" \
M_"a[24:27], a[196:199], a[232:235], a[24:27]\n" \
X_"a[182:183], v37 offset:1024\n" \
"v_accvgpr_read_b32 v181, a85\n" \
M_"a[28:31], a[172:175], a[132:135], 0\n" \
X_"a[244:245], v37 offset:8192\n" \
"v_accvgpr_read_b32 v182, a86\n" \
M_"a[28:31], a[236:239], a[196:199], a[28:31]\n" \
"s_add_u32 s89, s41, 0x2000\n" \
"buffer_store_dwordx4 v[200:203], v20, s[80:83], s89 offen offset:0 sc1\n" \
M_"a[20:23], a[164:167], a[132:135], 0\n" \
X_"a[246:247], v37 offset:9216\n" \
".fill (L_pb + 20192 - .) / 4, 4, 0xBF800000\n" \
"L_pp5:\n" \
"s_cbranch_execz L_pret\n" \
"s_nop 0\n" \
"v_accvgpr_read_b32 v183, a87\n" \
M_"a[20:23], a[228:231], a[196:199], a[20:23]\n" \
X_"a[184:185], v38 offset:0\n" \
C_"v220, v220, v221\n" \
M_"a[16:19], a[160:163], a[132:135], 0\n" \
X_"a[186:187], v38 offset:1024\n" \
C_"v221, v222, v223\n" \
M_"a[16:19], a[224:227], a[196:199], a[16:19]\n" \
C_"v222, v180, v181\n" \
C_"v223, v182, v183\n" \
C_"v224, v52, v53\n" \
M_"a[32:35], a[160:163], a[136:139], 0\n" \
"s_add_u32 s88, s41, 0x2000\n" \
"buffer_store_dwordx4 v[204:207], v20, s[80:83], s88 offen offset:1024 sc1\n" \
M_"a[32:35], a[224:227], a[200:203], a[32:35]\n" \
X_"a[248:249], v38 offset:8192\n" \
C_"v225, v54, v55\n" \
M_"a[36:39], a[164:167], a[136:139], 0\n" \
X_"a[250:251], v38 offset:9216\n" \
C_"v226, v56, v57\n" \
M_"a[36:39], a[228:231], a[200:203], a[36:39]\n" \
X_"a[188:189], v39 offset:0\n" \
C_"v227, v58, v59\n" \
M_"a[44:47], a[172:175], a[136:139], 0\n" \
C_"v228, v60, v61\n" \
C_"v229, v62, v63\n" \
C_"v230, v64, v65\n" \
M_"a[44:47], a[236:239], a[200:203], a[44:47]\n" \
X_"a[190:191], v39 offset:1024\n" \
C_"v231, v66, v67\n" \
M_"a[40:43], a[136:139], a[168:171], 0\n" \
"s_add_u32 s89, s41, 0x3000\n" \
"buffer_store_dwordx4 v[208:211], v20, s[80:83], s89 offen offset:1024 sc1\n" \
M_"a[40:43], a[200:203], a[232:235], a[40:43]\n" \
X_"a[252:253], v39 offset:8192\n" \
C_"v232, v76, v77\n" \
M_"a[56:59], a[140:143], a[168:171], 0\n" \
X_"a[254:255], v39 offset:9216\n" \
C_"v233, v78, v79\n" \
M_"a[56:59], a[204:207], a[232:235], a[56:59]\n" \
C_"v234, v80, v81\n" \
C_"v235, v82, v83\n" \
C_"v236, v68, v69\n" \
M_"a[60:63], a[172:175], a[140:143], 0\n" \
C_"v237, v70, v71\n" \
C_"v238, v72, v73\n" \
C_"v239, v74, v75\n" \
M_"a[60:63], a[236:239], a[204:207], a[60:63]\n" \
"s_add_u32 s88, s41, 0x3000\n" \
"buffer_store_dwordx4 v[212:215], v20, s[80:83], s88 offen offset:0 sc1\n" \
M_"a[52:55], a[164:167], a[140:143], 0\n" \
C_"v240, v84, v85\n" \
C_"v241, v86, v87\n" \
C_"v242, v88, v89\n" \
M_"a[52:55], a[228:231], a[204:207], a[52:55]\n" \
C_"v243, v90, v91\n" \
C_"v244, v92, v93\n" \
C_"v245, v94, v95\n" \
M_"a[48:51], a[160:163], a[140:143], 0\n" \
C_"v246, v96, v97\n" \
C_"v247, v98, v99\n" \
C_"v184, v108, v109\n" \
M_"a[48:51], a[224:227], a[204:207], a[48:51]\n" \
C_"v185, v110, v111\n" \
C_"v186, v112, v113\n" \
C_"v187, v114, v115\n" \
"s_waitcnt lgkmcnt(15)\n" \
"s_nop 0\n" \
M_"a[64:67], a[164:167], v[248:251], 0\n" \
C_"v188, v100, v101\n" \
C_"v189, v102, v103\n" \
C_"v190, v104, v105\n" \
M_"a[64:67], a[228:231], v[48:51], a[64:67]\n" \
C_"v191, v106, v107\n" \
C_"v192, v116, v117\n" \
C_"v193, v118, v119\n" \
M_"a[80:83], a[172:175], v[248:251], 0\n" \
C_"v194, v120, v121\n" \
C_"v195, v122, v123\n" \
C_"v196, v124, v125\n" \
M_"a[80:83], a[236:239], v[48:51], a[80:83]\n" \
C_"v197, v126, v127\n" \
C_"v198, v128, v129\n" \
C_"v199, v130, v131\n" \
M_"a[68:71], v[248:251], a[168:171], 0\n" \
C_"v252, v140, v141\n" \
C_"v253, v142, v143\n" \
C_"v254, v144, v145\n" \
M_"a[68:71], v[48:51], a[232:235], a[68:71]\n" \
C_"v255, v146, v147\n" \
"v_accvgpr_write_b32 a84, v252\n" \
"v_accvgpr_write_b32 a85, v253\n" \
"s_waitcnt vmcnt(4) lgkmcnt(0)\n" \
"s_barrier\n" \
M_"v[52:55], a[176:179], a[128:131], 0\n" \
"s_add_u32 m0, s40, 0x0\n" \
"s_nop 0\n" \
"s_add_u32 s85, s39, 0x4000\n" \
"v_accvgpr_write_b32 a86, v254\n" \
M_"v[52:55], a[240:243], a[192:195], v[52:55]\n" \
B_"v20, s[80:83], s85 offen sc1 lds\n" \
"v_accvgpr_write_b32 a87, v255\n" \
M_"v[56:59], a[180:183], a[128:131], 0\n" \
X_"a[160:161], v32 offset:32768\n" \
C_"v252, v132, v133\n" \
M_"v[56:59], a[244:247], a[192:195], v[56:59]\n" \
X_"a[162:163], v32 offset:33280\n" \
C_"v253, v134, v135\n" \
M_"v[60:63], a[184:187], a[128:131], 0\n" \
"s_add_u32 m0, s40, 0x1000\n" \
"s_add_u32 s85, s39, 0x5000\n" \
C_"v254, v136, v137\n" \
M_"v[60:63], a[248:251], a[192:195], v[60:63]\n" \
B_"v20, s[80:83], s85 offen sc1 lds\n" \
C_"v255, v138, v139\n" \
M_"v[64:67], a[188:191], a[128:131], 0\n" \
X_"a[224:225], v32 offset:36864\n" \
C_"v200, v148, v149\n" \
M_"v[64:67], a[252:255], a[192:195], v[64:67]\n" \
R_"a[128:131], v28 offset:32768\n" \
C_"v201, v150, v151\n" \
M_"v[80:83], a[188:191], a[132:135], 0\n" \
R_"a[192:195], v29 offset:32768\n" \
C_"v202, v152, v153\n" \
M_"v[80:83], a[252:255], a[196:199], v[80:83]\n" \
"s_add_u32 m0, s40, 0x3000\n" \
"s_movk_i32 s84, 0x100\n" \
"s_nop 0\n" \
C_"v203, v154, v155\n" \
M_"v[76:79], a[184:187], a[132:135], 0\n" \
B_"v21, s[76:79], s84 offen offset:0 lds\n" \
C_"v204, v156, v157\n" \
M_"v[76:79], a[248:251], a[196:199], v[76:79]\n" \
X_"a[226:227], v32 offset:37376\n" \
C_"v205, v158, v159\n" \
M_"v[72:75], a[180:183], a[132:135], 0\n" \
X_"a[164:165], v33 offset:32768\n" \
C_"v206, v160, v161\n" \
M_"v[72:75], a[244:247], a[196:199], v[72:75]\n" \
C_"v207, v162, v163\n" \
C_"v208, v172, v173\n" \
C_"v209, v174, v175\n" \
M_"v[68:71], a[176:179], a[132:135], 0\n" \
X_"a[166:167], v33 offset:33280\n" \
C_"v210, v176, v177\n" \
M_"v[68:71], a[240:243], a[196:199], v[68:71]\n" \
R_"a[132:135], v28 offset:34816\n" \
C_"v211, v178, v179\n" \
M_"v[84:87], a[176:179], a[136:139], 0\n" \
R_"a[196:199], v29 offset:34816\n" \
C_"v212, v164, v165\n" \
M_"v[84:87], a[240:243], a[200:203], v[84:87]\n" \
"s_add_u32 m0, s31, 0x0\n" \
"s_mov_b32 s84, 0x100000\n" \
C_"v213, v166, v167\n" \
M_"v[88:91], a[180:183], a[136:139], 0\n" \
B_"v26, s[68:71], s84 offen offset:0 lds\n" \
C_"v214, v168, v169\n" \
M_"v[88:91], a[244:247], a[200:203], v[88:91]\n" \
"s_add_u32 s89, s41, 0x0\n" \
"buffer_store_dwordx4 v[224:227], v20, s[80:83], s89 offen offset:2048 sc1\n" \
M_"v[92:95], a[184:187], a[136:139], 0\n" \
X_"a[228:229], v33 offset:36864\n" \
C_"v215, v170, v171\n" \
M_"v[92:95], a[248:251], a[200:203], v[92:95]\n" \
X_"a[230:231], v33 offset:37376\n" \
M_"v[96:99], a[188:191], a[136:139], 0\n" \
M_"v[96:99], a[252:255], a[200:203], v[96:99]\n" \
R_"a[136:139], v28 offset:36864\n" \
M_"v[112:115], a[188:191], a[140:143], 0\n" \
R_"a[200:203], v29 offset:36864\n" \
M_"v[112:115], a[252:255], a[204:207], v[112:115]\n" \
"s_addk_i32 s84, 0x7c00\n" \
M_"v[108:111], a[184:187], a[140:143], 0\n" \
B_"v26, s[68:71], s84 offen offset:1024 lds\n" \
M_"v[108:111], a[248:251], a[204:207], v[108:111]\n" \
X_"a[172:173], v35 offset:32768\n" \
M_"v[104:107], a[180:183], a[140:143], 0\n" \
"s_addk_i32 s84, 0x7c00\n" \
M_"v[104:107], a[244:247], a[204:207], v[104:107]\n" \
B_"v27, s[68:71], s84 offen offset:2048 lds\n" \
M_"v[100:103], a[176:179], a[140:143], 0\n" \
X_"a[174:175], v35 offset:33280\n" \
M_"v[100:103], a[240:243], a[204:207], v[100:103]\n" \
R_"a[140:143], v28 offset:38912\n" \
M_"v[116:119], a[176:179], a[144:147], 0\n" \
R_"a[204:207], v29 offset:38912\n" \
M_"v[116:119], a[240:243], a[208:211], v[116:119]\n" \
"s_addk_i32 s84, 0x7c00\n" \
M_"v[120:123], a[180:183], a[144:147], 0\n" \
B_"v27, s[68:71], s84 offen offset:3072 lds\n" \
M_"v[120:123], a[244:247], a[208:211], v[120:123]\n" \
X_"a[236:237], v35 offset:36864\n" \
M_"v[124:127], a[184:187], a[144:147], 0\n" \
X_"a[238:239], v35 offset:37376\n" \
M_"v[124:127], a[248:251], a[208:211], v[124:127]\n" \
"s_add_u32 m0, s30, 0x0\n" \
"s_movk_i32 s84, 0x180\n" \
M_"v[128:131], a[188:191], a[144:147], 0\n" \
B_"v24, s[76:79], s84 offen offset:0 lds\n" \
M_"v[128:131], a[252:255], a[208:211], v[128:131]\n" \
R_"a[144:147], v28 offset:40960\n" \
M_"v[144:147], a[188:191], a[148:151], 0\n" \
R_"a[208:211], v29 offset:40960\n" \
M_"v[144:147], a[252:255], a[212:215], v[144:147]\n" \
X_"a[168:169], v34 offset:32768\n" \
M_"v[140:143], a[184:187], a[148:151], 0\n" \
"s_movk_i32 s84, 0x2180\n" \
M_"v[140:143], a[248:251], a[212:215], v[140:143]\n" \
B_"v25, s[76:79], s84 offen offset:1024 lds\n" \
M_"v[136:139], a[180:183], a[148:151], 0\n" \
X_"a[170:171], v34 offset:33280\n" \
M_"v[136:139], a[244:247], a[212:215], v[136:139]\n" \
X_"a[232:233], v34 offset:36864\n" \
M_"v[132:135], a[176:179], a[148:151], 0\n" \
X_"a[234:235], v34 offset:37376\n" \
M_"v[132:135], a[240:243], a[212:215], v[132:135]\n" \
R_"a[148:151], v28 offset:43008\n" \
M_"v[148:151], a[176:179], a[152:155], 0\n" \
R_"a[212:215], v29 offset:43008\n" \
M_"v[148:151], a[240:243], a[216:219], v[148:151]\n" \
"s_movk_i32 s84, 0x4180\n" \
M_"v[152:155], a[180:183], a[152:155], 0\n" \
B_"v24, s[76:79], s84 offen offset:2048 lds\n" \
M_"v[152:155], a[244:247], a[216:219], v[152:155]\n" \
"s_movk_i32 s84, 0x6180\n" \
M_"v[156:159], a[184:187], a[152:155], 0\n" \
B_"v25, s[76:79], s84 offen offset:3072 lds\n" \
M_"v[156:159], a[248:251], a[216:219], v[156:159]\n" \
R_"v[248:251], v22 offset:32768\n" \
M_"v[160:163], a[188:191], a[152:155], 0\n" \
R_"v[48:51], v23 offset:32768\n" \
M_"v[160:163], a[252:255], a[216:219], v[160:163]\n" \
R_"a[152:155], v28 offset:45056\n" \
M_"v[176:179], a[188:191], a[156:159], 0\n" \
R_"a[216:219], v29 offset:45056\n" \
M_"v[176:179], a[252:255], a[220:223], v[176:179]\n" \
M_"v[172:175], a[184:187], a[156:159], 0\n" \
M_"v[172:175], a[248:251], a[220:223], v[172:175]\n" \
M_"v[168:171], a[180:183], a[156:159], 0\n" \
M_"v[168:171], a[244:247], a[220:223], v[168:171]\n" \
M_"v[164:167], a[176:179], a[156:159], 0\n" \
M_"v[164:167], a[240:243], a[220:223], v[164:167]\n" \
R_"a[156:159], v28 offset:47104\n" \
R_"a[220:223], v29 offset:47104\n" \
"s_waitcnt lgkmcnt(15)\n" \
M_"a[0:3], a[160:163], a[128:131], a[0:3]\n" \
"s_add_u32 m0, s30, 0x1000\n" \
"s_addk_i32 s84, 0x3000\n" \
M_"a[0:3], a[224:227], a[192:195], a[0:3]\n" \
B_"v24, s[76:79], s84 offen offset:0 lds\n" \
M_"a[4:7], a[164:167], a[128:131], a[4:7]\n" \
X_"a[176:177], v36 offset:32768\n" \
"v_mov_b32_e64 v11, 0\n" \
M_"a[4:7], a[228:231], a[192:195], a[4:7]\n" \
X_"a[178:179], v36 offset:33792\n" \
"s_and_b32 s5, s2, 7\n" \
M_"a[12:15], a[172:175], a[128:131], a[12:15]\n" \
X_"a[240:241], v36 offset:40960\n" \
"s_lshl_b32 s6, s3, 1\n" \
"s_waitcnt lgkmcnt(15)\n" \
M_"a[12:15], a[236:239], a[192:195], a[12:15]\n" \
"s_add_u32 s88, s41, 0x0\n" \
"buffer_store_dwordx4 v[228:231], v20, s[80:83], s88 offen offset:3072 sc1\n" \
"s_waitcnt lgkmcnt(13)\n" \
M_"a[8:11], a[128:131], a[168:171], a[8:11]\n" \
X_"a[242:243], v36 offset:41984\n" \
"s_lshl_b32 s4, s5, 2\n" \
"s_waitcnt lgkmcnt(12)\n" \
M_"a[8:11], a[192:195], a[232:235], a[8:11]\n" \
X_"a[180:181], v37 offset:32768\n" \
"s_bfe_u32 s7, s2, 0x20003\n" \
M_"a[24:27], a[132:135], a[168:171], a[24:27]\n" \
X_"a[182:183], v37 offset:33792\n" \
"s_or_b32 s12, s4, s7\n" \
M_"a[24:27], a[196:199], a[232:235], a[24:27]\n" \
X_"a[244:245], v37 offset:40960\n" \
"s_and_b32 s6, s6, 4\n" \
M_"a[28:31], a[172:175], a[132:135], a[28:31]\n" \
"s_and_b32 s8, s3, 1\n" \
"s_or_b32 s8, s6, s8\n" \
M_"a[28:31], a[236:239], a[196:199], a[28:31]\n" \
"s_addk_i32 s84, 0x2000\n" \
M_"a[20:23], a[164:167], a[132:135], a[20:23]\n" \
B_"v25, s[76:79], s84 offen offset:1024 lds\n" \
M_"a[20:23], a[228:231], a[196:199], a[20:23]\n" \
X_"a[246:247], v37 offset:41984\n" \
"s_lshl_b32 s6, s12, 4\n" \
M_"a[16:19], a[160:163], a[132:135], a[16:19]\n" \
X_"a[184:185], v38 offset:32768\n" \
"s_lshl_b32 s9, s3, 2\n" \
M_"a[16:19], a[224:227], a[196:199], a[16:19]\n" \
X_"a[186:187], v38 offset:33792\n" \
"s_or_b32 s6, s6, s9\n" \
M_"a[32:35], a[160:163], a[136:139], a[32:35]\n" \
"s_addk_i32 s84, 0x2000\n" \
M_"a[32:35], a[224:227], a[200:203], a[32:35]\n" \
B_"v24, s[76:79], s84 offen offset:2048 lds\n" \
M_"a[36:39], a[164:167], a[136:139], a[36:39]\n" \
X_"a[248:249], v38 offset:40960\n" \
"s_lshr_b32 s9, s2, 5\n" \
M_"a[36:39], a[228:231], a[200:203], a[36:39]\n" \
X_"a[250:251], v38 offset:41984\n" \
"s_mul_i32 s10, s9, 0xa00\n" \
M_"a[44:47], a[172:175], a[136:139], a[44:47]\n" \
X_"a[188:189], v39 offset:32768\n" \
"s_add_i32 s10, s10, 0x5bc1000\n" \
M_"a[44:47], a[236:239], a[200:203], a[44:47]\n" \
X_"a[190:191], v39 offset:33792\n" \
"s_or_b32 s6, s6, s10\n" \
M_"a[40:43], a[136:139], a[168:171], a[40:43]\n" \
"s_add_u32 s89, s41, 0x1000\n" \
"buffer_store_dwordx4 v[232:235], v20, s[80:83], s89 offen offset:3072 sc1\n" \
M_"a[40:43], a[200:203], a[232:235], a[40:43]\n" \
X_"a[252:253], v39 offset:40960\n" \
"s_lshl_b32 s13, s9, 3\n" \
M_"a[56:59], a[140:143], a[168:171], a[56:59]\n" \
X_"a[254:255], v39 offset:41984\n" \
"v_writelane_b32 v11, s6, 0\n" \
M_"a[56:59], a[204:207], a[232:235], a[56:59]\n" \
"s_or_b32 s6, s13, s5\n" \
"v_writelane_b32 v11, s10, 1\n" \
M_"a[60:63], a[172:175], a[140:143], a[60:63]\n" \
"s_lshl_b32 s6, s6, 10\n" \
"s_lshl_b32 s10, s3, 8\n" \
M_"a[60:63], a[236:239], a[204:207], a[60:63]\n" \
"s_addk_i32 s84, 0x2000\n" \
M_"a[52:55], a[164:167], a[140:143], a[52:55]\n" \
B_"v25, s[76:79], s84 offen offset:3072 lds\n" \
M_"a[52:55], a[228:231], a[204:207], a[52:55]\n" \
"s_or_b32 s6, s6, s10\n" \
"s_or_b32 s10, s6, 0x5c00200\n" \
M_"a[48:51], a[160:163], a[140:143], a[48:51]\n" \
"v_writelane_b32 v11, s10, 2\n" \
"s_lshl_b32 s10, s7, 2\n" \
M_"a[48:51], a[224:227], a[204:207], a[48:51]\n" \
"s_or_b32 s11, s10, s6\n" \
"s_or_b32 s11, s11, 0x5c00000\n" \
"s_waitcnt lgkmcnt(15)\n" \
M_"a[64:67], a[164:167], v[248:251], a[64:67]\n" \
"v_writelane_b32 v11, s11, 3\n" \
"s_or_b32 s6, s6, 0x5c00080\n" \
M_"a[64:67], a[228:231], v[48:51], a[64:67]\n" \
"v_writelane_b32 v11, s6, 4\n" \
"s_or_b32 s6, s6, s10\n" \
M_"a[80:83], a[172:175], v[248:251], a[80:83]\n" \
"s_add_i32 s10, s3, s2\n" \
"v_writelane_b32 v11, s6, 5\n" \
M_"a[80:83], a[236:239], v[48:51], a[80:83]\n" \
"s_mov_b32 s6, 2.0\n" \
"s_mov_b32 s11, 0x5bc0000\n" \
M_"a[68:71], v[248:251], a[168:171], a[68:71]\n" \
"s_cmp_lg_u32 s10, 0\n" \
"s_cselect_b32 s10, s6, s11\n" \
"s_waitcnt vmcnt(10) lgkmcnt(0)\n" \
M_"a[68:71], v[48:51], a[232:235], a[68:71]\n" \
"s_barrier\n" \
"v_xor_b32_e32 v28, 0x20000, v28\n" \
"v_xor_b32_e32 v29, 0x20000, v29\n" \
M_"v[52:55], a[176:179], a[128:131], v[52:55]\n" \
"s_add_u32 m0, s40, 0x8000\n" \
"s_add_u32 s85, s39, 0x6000\n" \
M_"v[52:55], a[240:243], a[192:195], v[52:55]\n" \
B_"v20, s[80:83], s85 offen sc1 lds\n" \
M_"v[56:59], a[180:183], a[128:131], v[56:59]\n" \
X_"a[160:161], v32 offset:0\n" \
"s_lshr_b32 s14, s2, 3\n" \
M_"v[56:59], a[244:247], a[192:195], v[56:59]\n" \
X_"a[162:163], v32 offset:512\n" \
"v_writelane_b32 v11, s10, 57\n" \
M_"v[60:63], a[184:187], a[128:131], v[60:63]\n" \
"s_add_u32 m0, s40, 0x9000\n" \
"s_add_u32 s85, s39, 0x7000\n" \
M_"v[60:63], a[248:251], a[192:195], v[60:63]\n" \
B_"v20, s[80:83], s85 offen sc1 lds\n" \
M_"v[64:67], a[188:191], a[128:131], v[64:67]\n" \
X_"a[224:225], v32 offset:4096\n" \
"s_lshl_b32 s10, s5, 5\n" \
M_"v[64:67], a[252:255], a[192:195], v[64:67]\n" \
R_"a[128:131], v28 offset:0\n" \
"s_min_u32 s15, s3, 2\n" \
"s_nop 0\n" \
M_"v[80:83], a[188:191], a[132:135], v[80:83]\n" \
R_"a[192:195], v29 offset:0\n" \
"s_mul_i32 s11, s7, 0x120\n" \
M_"v[80:83], a[252:255], a[196:199], v[80:83]\n" \
"s_add_u32 s88, s41, 0x1000\n" \
"buffer_store_dwordx4 v[236:239], v20, s[80:83], s88 offen offset:2048 sc1\n" \
M_"v[76:79], a[184:187], a[132:135], v[76:79]\n" \
X_"a[226:227], v32 offset:4608\n" \
M_"v[76:79], a[248:251], a[196:199], v[76:79]\n" \
X_"a[164:165], v33 offset:0\n" \
M_"v[72:75], a[180:183], a[132:135], v[72:75]\n" \
"s_add_u32 m0, s40, 0xb000\n" \
"s_movk_i32 s84, 0x180\n" \
"s_nop 0\n" \
M_"v[72:75], a[244:247], a[196:199], v[72:75]\n" \
B_"v21, s[76:79], s84 offen offset:0 lds\n" \
M_"v[68:71], a[176:179], a[132:135], v[68:71]\n" \
X_"a[166:167], v33 offset:512\n" \
M_"v[68:71], a[240:243], a[196:199], v[68:71]\n" \
R_"a[132:135], v28 offset:2048\n" \
M_"v[84:87], a[176:179], a[136:139], v[84:87]\n" \
R_"a[196:199], v29 offset:2048\n" \
M_"v[84:87], a[240:243], a[200:203], v[84:87]\n" \
"s_add_u32 m0, s31, 0x8000\n" \
"s_mov_b32 s84, 0x180000\n" \
M_"v[88:91], a[180:183], a[136:139], v[88:91]\n" \
B_"v26, s[68:71], s84 offen offset:0 lds\n" \
M_"v[88:91], a[244:247], a[200:203], v[88:91]\n" \
X_"a[228:229], v33 offset:4096\n" \
M_"v[92:95], a[184:187], a[136:139], v[92:95]\n" \
X_"a[230:231], v33 offset:4608\n" \
M_"v[92:95], a[248:251], a[200:203], v[92:95]\n" \
"s_addk_i32 s84, 0x7c00\n" \
M_"v[96:99], a[188:191], a[136:139], v[96:99]\n" \
B_"v26, s[68:71], s84 offen offset:1024 lds\n" \
M_"v[96:99], a[252:255], a[200:203], v[96:99]\n" \
R_"a[136:139], v28 offset:4096\n" \
M_"v[112:115], a[188:191], a[140:143], v[112:115]\n" \
R_"a[200:203], v29 offset:4096\n" \
M_"v[112:115], a[252:255], a[204:207], v[112:115]\n" \
"s_addk_i32 s84, 0x7c00\n" \
M_"v[108:111], a[184:187], a[140:143], v[108:111]\n" \
B_"v27, s[68:71], s84 offen offset:2048 lds\n" \
M_"v[108:111], a[248:251], a[204:207], v[108:111]\n" \
X_"a[172:173], v35 offset:0\n" \
M_"v[104:107], a[180:183], a[140:143], v[104:107]\n" \
X_"a[174:175], v35 offset:512\n" \
M_"v[104:107], a[244:247], a[204:207], v[104:107]\n" \
X_"a[236:237], v35 offset:4096\n" \
M_"v[100:103], a[176:179], a[140:143], v[100:103]\n" \
"s_add_u32 s16, s22, s11\n" \
"s_addc_u32 s17, s23, 0\n" \
M_"v[100:103], a[240:243], a[204:207], v[100:103]\n" \
R_"a[140:143], v28 offset:6144\n" \
M_"v[116:119], a[176:179], a[144:147], v[116:119]\n" \
R_"a[204:207], v29 offset:6144\n" \
M_"v[116:119], a[240:243], a[208:211], v[116:119]\n" \
"s_addk_i32 s84, 0x7c00\n" \
M_"v[120:123], a[180:183], a[144:147], v[120:123]\n" \
B_"v27, s[68:71], s84 offen offset:3072 lds\n" \
M_"v[120:123], a[244:247], a[208:211], v[120:123]\n" \
"s_add_u32 m0, s30, 0x8000\n" \
"s_movk_i32 s84, 0x200\n" \
M_"v[124:127], a[184:187], a[144:147], v[124:127]\n" \
B_"v24, s[76:79], s84 offen offset:0 lds\n" \
M_"v[124:127], a[248:251], a[208:211], v[124:127]\n" \
X_"a[238:239], v35 offset:4608\n" \
M_"v[128:131], a[188:191], a[144:147], v[128:131]\n" \
"s_add_u32 s89, s41, 0x2000\n" \
"buffer_store_dwordx4 v[240:243], v20, s[80:83], s89 offen offset:2048 sc1\n" \
M_"v[128:131], a[252:255], a[208:211], v[128:131]\n" \
R_"a[144:147], v28 offset:8192\n" \
M_"v[144:147], a[188:191], a[148:151], v[144:147]\n" \
R_"a[208:211], v29 offset:8192\n" \
M_"v[144:147], a[252:255], a[212:215], v[144:147]\n" \
X_"a[168:169], v34 offset:0\n" \
M_"v[140:143], a[184:187], a[148:151], v[140:143]\n" \
X_"a[170:171], v34 offset:512\n" \
M_"v[140:143], a[248:251], a[212:215], v[140:143]\n" \
"s_movk_i32 s84, 0x2200\n" \
"s_nop 0\n" \
M_"v[136:139], a[180:183], a[148:151], v[136:139]\n" \
B_"v25, s[76:79], s84 offen offset:1024 lds\n" \
M_"v[136:139], a[244:247], a[212:215], v[136:139]\n" \
X_"a[232:233], v34 offset:4096\n" \
M_"v[132:135], a[176:179], a[148:151], v[132:135]\n" \
X_"a[234:235], v34 offset:4608\n" \
M_"v[132:135], a[240:243], a[212:215], v[132:135]\n" \
R_"a[148:151], v28 offset:10240\n" \
M_"v[148:151], a[176:179], a[152:155], v[148:151]\n" \
R_"a[212:215], v29 offset:10240\n" \
M_"v[148:151], a[240:243], a[216:219], v[148:151]\n" \
"s_movk_i32 s84, 0x4200\n" \
M_"v[152:155], a[180:183], a[152:155], v[152:155]\n" \
B_"v24, s[76:79], s84 offen offset:2048 lds\n" \
M_"v[152:155], a[244:247], a[216:219], v[152:155]\n" \
"s_movk_i32 s84, 0x6200\n" \
M_"v[156:159], a[184:187], a[152:155], v[156:159]\n" \
B_"v25, s[76:79], s84 offen offset:3072 lds\n" \
M_"v[156:159], a[248:251], a[216:219], v[156:159]\n" \
R_"v[248:251], v22 offset:0\n" \
M_"v[160:163], a[188:191], a[152:155], v[160:163]\n" \
R_"v[48:51], v23 offset:0\n" \
M_"v[160:163], a[252:255], a[216:219], v[160:163]\n" \
R_"a[152:155], v28 offset:12288\n" \
M_"v[176:179], a[188:191], a[156:159], v[176:179]\n" \
R_"a[216:219], v29 offset:12288\n" \
M_"v[176:179], a[252:255], a[220:223], v[176:179]\n" \
"s_add_u32 s18, s24, s11\n" \
"v_writelane_b32 v11, s16, 9\n" \
"s_addc_u32 s19, s25, 0\n" \
M_"v[172:175], a[184:187], a[156:159], v[172:175]\n" \
"s_and_b32 s20, s17, 0xffff\n" \
"v_writelane_b32 v11, s20, 10\n" \
M_"v[172:175], a[248:251], a[220:223], v[172:175]\n" \
"s_and_b32 s20, s19, 0xffff\n" \
"v_writelane_b32 v11, s18, 11\n" \
M_"v[168:171], a[180:183], a[156:159], v[168:171]\n" \
"s_cmpk_lt_u32 s3, 3\n" \
"s_cselect_b32 s16, s16, s18\n" \
M_"v[168:171], a[244:247], a[220:223], v[168:171]\n" \
"s_movk_i32 s18, 0xfff\n" \
"v_writelane_b32 v11, s20, 12\n" \
M_"v[164:167], a[176:179], a[156:159], v[164:167]\n" \
"s_and_b32 s20, s29, 0xffff\n" \
"v_writelane_b32 v11, s28, 13\n" \
M_"v[164:167], a[240:243], a[220:223], v[164:167]\n" \
R_"a[156:159], v28 offset:14336\n" \
R_"a[220:223], v29 offset:14336\n" \
"s_waitcnt lgkmcnt(15)\n" \
M_"a[0:3], a[160:163], a[128:131], a[0:3]\n" \
"s_add_u32 m0, s30, 0x9000\n" \
"s_addk_i32 s84, 0x3000\n" \
M_"a[0:3], a[224:227], a[192:195], a[0:3]\n" \
B_"v24, s[76:79], s84 offen offset:0 lds\n" \
M_"a[4:7], a[164:167], a[128:131], a[4:7]\n" \
X_"a[176:177], v36 offset:0\n" \
"v_writelane_b32 v11, s20, 14\n" \
M_"a[4:7], a[228:231], a[192:195], a[4:7]\n" \
X_"a[178:179], v36 offset:1024\n" \
"v_writelane_b32 v11, s16, 15\n" \
M_"a[12:15], a[172:175], a[128:131], a[12:15]\n" \
X_"a[240:241], v36 offset:8192\n" \
"s_waitcnt lgkmcnt(15)\n" \
M_"a[12:15], a[236:239], a[192:195], a[12:15]\n" \
"s_add_u32 s88, s41, 0x2000\n" \
"buffer_store_dwordx4 v[244:247], v20, s[80:83], s88 offen offset:3072 sc1\n" \
"s_waitcnt lgkmcnt(13)\n" \
M_"a[8:11], a[128:131], a[168:171], a[8:11]\n" \
X_"a[242:243], v36 offset:9216\n" \
"s_waitcnt lgkmcnt(12)\n" \
M_"a[8:11], a[192:195], a[232:235], a[8:11]\n" \
X_"a[180:181], v37 offset:0\n" \
M_"a[24:27], a[132:135], a[168:171], a[24:27]\n" \
X_"a[182:183], v37 offset:1024\n" \
M_"a[24:27], a[196:199], a[232:235], a[24:27]\n" \
X_"a[244:245], v37 offset:8192\n" \
M_"a[28:31], a[172:175], a[132:135], a[28:31]\n" \
"s_cmpk_lt_u32 s3, 3\n" \
"s_cselect_b32 s16, s17, s19\n" \
M_"a[28:31], a[236:239], a[196:199], a[28:31]\n" \
"s_addk_i32 s84, 0x2000\n" \
"s_nop 0\n" \
M_"a[20:23], a[164:167], a[132:135], a[20:23]\n" \
B_"v25, s[76:79], s84 offen offset:1024 lds\n" \
M_"a[20:23], a[228:231], a[196:199], a[20:23]\n" \
X_"a[246:247], v37 offset:9216\n" \
"s_and_b32 s16, s16, 0xffff\n" \
M_"a[16:19], a[160:163], a[132:135], a[16:19]\n" \
X_"a[184:185], v38 offset:0\n" \
"v_writelane_b32 v11, s16, 16\n" \
".fill (L_pb + 24288 - .) / 4, 4, 0xBF800000\n" \
"L_pp6:\n" \
"s_cbranch_execz L_pret\n" \
M_"a[16:19], a[224:227], a[196:199], a[16:19]\n" \
X_"a[186:187], v38 offset:1024\n" \
"s_mul_i32 s16, s5, 0x48\n" \
M_"a[32:35], a[160:163], a[136:139], a[32:35]\n" \
"s_addk_i32 s84, 0x2000\n" \
M_"a[32:35], a[224:227], a[200:203], a[32:35]\n" \
B_"v24, s[76:79], s84 offen offset:2048 lds\n" \
M_"a[36:39], a[164:167], a[136:139], a[36:39]\n" \
X_"a[248:249], v38 offset:8192\n" \
"s_add_i32 s17, s16, 0x47\n" \
M_"a[36:39], a[228:231], a[200:203], a[36:39]\n" \
X_"a[250:251], v38 offset:9216\n" \
M_"a[44:47], a[172:175], a[136:139], a[44:47]\n" \
X_"a[188:189], v39 offset:0\n" \
M_"a[44:47], a[236:239], a[200:203], a[44:47]\n" \
X_"a[190:191], v39 offset:1024\n" \
M_"a[40:43], a[136:139], a[168:171], a[40:43]\n" \
"s_add_u32 s89, s41, 0x3000\n" \
"buffer_store_dwordx4 v[184:187], v20, s[80:83], s89 offen offset:3072 sc1\n" \
M_"a[40:43], a[200:203], a[232:235], a[40:43]\n" \
X_"a[252:253], v39 offset:8192\n" \
M_"a[56:59], a[140:143], a[168:171], a[56:59]\n" \
X_"a[254:255], v39 offset:9216\n" \
M_"a[56:59], a[204:207], a[232:235], a[56:59]\n" \
"s_cmpk_lt_u32 s3, 3\n" \
"s_cselect_b32 s17, s17, s18\n" \
M_"a[60:63], a[172:175], a[140:143], a[60:63]\n" \
"s_mulk_i32 s17, 0x480\n" \
"s_addk_i32 s17, 0x120\n" \
M_"a[60:63], a[236:239], a[204:207], a[60:63]\n" \
"s_addk_i32 s84, 0x2000\n" \
M_"a[52:55], a[164:167], a[140:143], a[52:55]\n" \
B_"v25, s[76:79], s84 offen offset:3072 lds\n" \
M_"a[52:55], a[228:231], a[204:207], a[52:55]\n" \
"v_writelane_b32 v11, s17, 17\n" \
"s_lshl_b32 s17, s3, 5\n" \
M_"a[48:51], a[160:163], a[140:143], a[48:51]\n" \
"s_add_i32 s16, s16, s17\n" \
"s_mulk_i32 s16, 0x480\n" \
M_"a[48:51], a[224:227], a[204:207], a[48:51]\n" \
"s_mul_i32 s17, s12, 0x24000\n" \
"s_add_i32 s18, s17, 0x1b000\n" \
"s_waitcnt lgkmcnt(15)\n" \
M_"a[64:67], a[164:167], v[248:251], a[64:67]\n" \
"s_cmpk_lt_u32 s3, 3\n" \
"s_cselect_b32 s16, s16, s18\n" \
M_"a[64:67], a[228:231], v[48:51], a[64:67]\n" \
"s_xor_b32 s18, s3, 2\n" \
"v_writelane_b32 v11, s16, 18\n" \
M_"a[80:83], a[172:175], v[248:251], a[80:83]\n" \
"s_mul_i32 s16, s15, 0x2400\n" \
"s_mul_i32 s15, s15, 0x9000\n" \
M_"a[80:83], a[236:239], v[48:51], a[80:83]\n" \
"s_bitset1_b32 s16, 17\n" \
"v_writelane_b32 v11, s16, 22\n" \
M_"a[68:71], v[248:251], a[168:171], a[68:71]\n" \
"s_add_i32 s17, s17, s15\n" \
"s_mul_i32 s15, s3, 0x2400\n" \
"s_waitcnt vmcnt(11) lgkmcnt(0)\n" \
M_"a[68:71], v[48:51], a[232:235], a[68:71]\n" \
"s_barrier\n" \
"v_xor_b32_e32 v28, 0x20000, v28\n" \
"v_xor_b32_e32 v29, 0x20000, v29\n" \
M_"v[52:55], a[176:179], a[128:131], v[52:55]\n" \
"s_add_u32 m0, s40, 0x0\n" \
"s_nop 0\n" \
"s_add_u32 s85, s39, 0x8000\n" \
M_"v[52:55], a[240:243], a[192:195], v[52:55]\n" \
B_"v20, s[80:83], s85 offen sc1 lds\n" \
M_"v[56:59], a[180:183], a[128:131], v[56:59]\n" \
X_"a[160:161], v32 offset:32768\n" \
"v_writelane_b32 v11, s17, 29\n" \
M_"v[56:59], a[244:247], a[192:195], v[56:59]\n" \
X_"a[162:163], v32 offset:33280\n" \
"s_add_i32 s16, s15, 0xa400\n" \
M_"v[60:63], a[184:187], a[128:131], v[60:63]\n" \
"s_add_u32 m0, s40, 0x1000\n" \
"s_add_u32 s85, s39, 0x9000\n" \
M_"v[60:63], a[248:251], a[192:195], v[60:63]\n" \
B_"v20, s[80:83], s85 offen sc1 lds\n" \
M_"v[64:67], a[188:191], a[128:131], v[64:67]\n" \
X_"a[224:225], v32 offset:36864\n" \
"s_mov_b32 s17, 0x8000\n" \
M_"v[64:67], a[252:255], a[192:195], v[64:67]\n" \
R_"a[128:131], v28 offset:0\n" \
M_"v[80:83], a[188:191], a[132:135], v[80:83]\n" \
R_"a[192:195], v29 offset:0\n" \
M_"v[80:83], a[252:255], a[196:199], v[80:83]\n" \
"s_add_u32 s88, s41, 0x3000\n" \
"buffer_store_dwordx4 v[188:191], v20, s[80:83], s88 offen offset:2048 sc1\n" \
M_"v[76:79], a[184:187], a[132:135], v[76:79]\n" \
X_"a[226:227], v32 offset:37376\n" \
M_"v[76:79], a[248:251], a[196:199], v[76:79]\n" \
X_"a[164:165], v33 offset:32768\n" \
M_"v[72:75], a[180:183], a[132:135], v[72:75]\n" \
"s_add_u32 m0, s40, 0x3000\n" \
"s_movk_i32 s84, 0x200\n" \
M_"v[72:75], a[244:247], a[196:199], v[72:75]\n" \
B_"v21, s[76:79], s84 offen offset:0 lds\n" \
M_"v[68:71], a[176:179], a[132:135], v[68:71]\n" \
X_"a[166:167], v33 offset:33280\n" \
M_"v[68:71], a[240:243], a[196:199], v[68:71]\n" \
R_"a[132:135], v28 offset:2048\n" \
M_"v[84:87], a[176:179], a[136:139], v[84:87]\n" \
R_"a[196:199], v29 offset:2048\n" \
M_"v[84:87], a[240:243], a[200:203], v[84:87]\n" \
"s_add_u32 m0, s31, 0x0\n" \
"s_mov_b32 s84, 0x200000\n" \
M_"v[88:91], a[180:183], a[136:139], v[88:91]\n" \
B_"v26, s[68:71], s84 offen offset:0 lds\n" \
M_"v[88:91], a[244:247], a[200:203], v[88:91]\n" \
X_"a[228:229], v33 offset:36864\n" \
M_"v[92:95], a[184:187], a[136:139], v[92:95]\n" \
X_"a[230:231], v33 offset:37376\n" \
M_"v[92:95], a[248:251], a[200:203], v[92:95]\n" \
"s_addk_i32 s84, 0x7c00\n" \
M_"v[96:99], a[188:191], a[136:139], v[96:99]\n" \
B_"v26, s[68:71], s84 offen offset:1024 lds\n" \
M_"v[96:99], a[252:255], a[200:203], v[96:99]\n" \
R_"a[136:139], v28 offset:4096\n" \
M_"v[112:115], a[188:191], a[140:143], v[112:115]\n" \
R_"a[200:203], v29 offset:4096\n" \
M_"v[112:115], a[252:255], a[204:207], v[112:115]\n" \
"s_addk_i32 s84, 0x7c00\n" \
M_"v[108:111], a[184:187], a[140:143], v[108:111]\n" \
B_"v27, s[68:71], s84 offen offset:2048 lds\n" \
M_"v[108:111], a[248:251], a[204:207], v[108:111]\n" \
X_"a[172:173], v35 offset:32768\n" \
M_"v[104:107], a[180:183], a[140:143], v[104:107]\n" \
X_"a[174:175], v35 offset:33280\n" \
M_"v[104:107], a[244:247], a[204:207], v[104:107]\n" \
X_"a[236:237], v35 offset:36864\n" \
M_"v[100:103], a[176:179], a[140:143], v[100:103]\n" \
"s_cmpk_lt_u32 s3, 3\n" \
"s_cselect_b32 s16, s16, s17\n" \
M_"v[100:103], a[240:243], a[204:207], v[100:103]\n" \
R_"a[140:143], v28 offset:6144\n" \
"s_mov_b32 s17, 0x1b000\n" \
M_"v[116:119], a[176:179], a[144:147], v[116:119]\n" \
R_"a[204:207], v29 offset:6144\n" \
M_"v[116:119], a[240:243], a[208:211], v[116:119]\n" \
"s_addk_i32 s84, 0x7c00\n" \
M_"v[120:123], a[180:183], a[144:147], v[120:123]\n" \
B_"v27, s[68:71], s84 offen offset:3072 lds\n" \
M_"v[120:123], a[244:247], a[208:211], v[120:123]\n" \
"s_add_u32 m0, s30, 0x20000\n" \
"s_movk_i32 s84, 0x280\n" \
M_"v[124:127], a[184:187], a[144:147], v[124:127]\n" \
B_"v24, s[76:79], s84 offen offset:0 lds\n" \
M_"v[124:127], a[248:251], a[208:211], v[124:127]\n" \
X_"a[238:239], v35 offset:37376\n" \
M_"v[128:131], a[188:191], a[144:147], v[128:131]\n" \
"s_add_u32 s89, s42, 0x0\n" \
"buffer_store_dwordx4 v[216:219], v20, s[80:83], s89 offen offset:0 sc1\n" \
M_"v[128:131], a[252:255], a[208:211], v[128:131]\n" \
R_"a[144:147], v28 offset:8192\n" \
M_"v[144:147], a[188:191], a[148:151], v[144:147]\n" \
R_"a[208:211], v29 offset:8192\n" \
M_"v[144:147], a[252:255], a[212:215], v[144:147]\n" \
X_"a[168:169], v34 offset:32768\n" \
M_"v[140:143], a[184:187], a[148:151], v[140:143]\n" \
X_"a[170:171], v34 offset:33280\n" \
M_"v[140:143], a[248:251], a[212:215], v[140:143]\n" \
"s_movk_i32 s84, 0x2280\n" \
M_"v[136:139], a[180:183], a[148:151], v[136:139]\n" \
B_"v25, s[76:79], s84 offen offset:1024 lds\n" \
M_"v[136:139], a[244:247], a[212:215], v[136:139]\n" \
X_"a[232:233], v34 offset:36864\n" \
M_"v[132:135], a[176:179], a[148:151], v[132:135]\n" \
X_"a[234:235], v34 offset:37376\n" \
M_"v[132:135], a[240:243], a[212:215], v[132:135]\n" \
R_"a[148:151], v28 offset:10240\n" \
M_"v[148:151], a[176:179], a[152:155], v[148:151]\n" \
R_"a[212:215], v29 offset:10240\n" \
M_"v[148:151], a[240:243], a[216:219], v[148:151]\n" \
"s_movk_i32 s84, 0x4280\n" \
M_"v[152:155], a[180:183], a[152:155], v[152:155]\n" \
B_"v24, s[76:79], s84 offen offset:2048 lds\n" \
M_"v[152:155], a[244:247], a[216:219], v[152:155]\n" \
"s_movk_i32 s84, 0x6280\n" \
M_"v[156:159], a[184:187], a[152:155], v[156:159]\n" \
B_"v25, s[76:79], s84 offen offset:3072 lds\n" \
M_"v[156:159], a[248:251], a[216:219], v[156:159]\n" \
R_"v[248:251], v22 offset:32768\n" \
M_"v[160:163], a[188:191], a[152:155], v[160:163]\n" \
R_"v[48:51], v23 offset:32768\n" \
M_"v[160:163], a[252:255], a[216:219], v[160:163]\n" \
R_"a[152:155], v28 offset:12288\n" \
M_"v[176:179], a[188:191], a[156:159], v[176:179]\n" \
R_"a[216:219], v29 offset:12288\n" \
M_"v[176:179], a[252:255], a[220:223], v[176:179]\n" \
"s_cmp_lg_u32 s18, 0\n" \
"s_cselect_b32 s17, s16, s17\n" \
M_"v[172:175], a[184:187], a[156:159], v[172:175]\n" \
"v_writelane_b32 v11, s16, 23\n" \
"s_or_b32 s16, s15, 0x10000\n" \
M_"v[172:175], a[248:251], a[220:223], v[172:175]\n" \
"v_writelane_b32 v11, s17, 24\n" \
"s_add_i32 s17, s15, 0x19000\n" \
M_"v[168:171], a[180:183], a[156:159], v[168:171]\n" \
"v_writelane_b32 v11, s16, 25\n" \
"s_mov_b32 s16, 0\n" \
M_"v[168:171], a[244:247], a[220:223], v[168:171]\n" \
"s_cmpk_lt_u32 s3, 3\n" \
"s_cselect_b32 s17, s17, s16\n" \
M_"v[164:167], a[176:179], a[156:159], v[164:167]\n" \
"s_bitset1_b32 s15, 17\n" \
"v_writelane_b32 v11, s17, 26\n" \
M_"v[164:167], a[240:243], a[220:223], v[164:167]\n" \
R_"a[156:159], v28 offset:14336\n" \
R_"a[220:223], v29 offset:14336\n" \
"s_waitcnt lgkmcnt(15)\n" \
M_"a[0:3], a[160:163], a[128:131], a[0:3]\n" \
"s_add_u32 m0, s30, 0x21000\n" \
"s_addk_i32 s84, 0x3000\n" \
M_"a[0:3], a[224:227], a[192:195], a[0:3]\n" \
B_"v24, s[76:79], s84 offen offset:0 lds\n" \
M_"a[4:7], a[164:167], a[128:131], a[4:7]\n" \
X_"a[176:177], v36 offset:32768\n" \
"s_movk_i32 s17, 0x2400\n" \
M_"a[4:7], a[228:231], a[192:195], a[4:7]\n" \
X_"a[178:179], v36 offset:33792\n" \
M_"a[12:15], a[172:175], a[128:131], a[12:15]\n" \
X_"a[240:241], v36 offset:40960\n" \
"s_waitcnt lgkmcnt(15)\n" \
M_"a[12:15], a[236:239], a[192:195], a[12:15]\n" \
"s_add_u32 s88, s42, 0x1000\n" \
"buffer_store_dwordx4 v[220:223], v20, s[80:83], s88 offen offset:0 sc1\n" \
"s_waitcnt lgkmcnt(13)\n" \
M_"a[8:11], a[128:131], a[168:171], a[8:11]\n" \
X_"a[242:243], v36 offset:41984\n" \
"s_waitcnt lgkmcnt(12)\n" \
M_"a[8:11], a[192:195], a[232:235], a[8:11]\n" \
X_"a[180:181], v37 offset:32768\n" \
M_"a[24:27], a[132:135], a[168:171], a[24:27]\n" \
X_"a[182:183], v37 offset:33792\n" \
M_"a[24:27], a[196:199], a[232:235], a[24:27]\n" \
X_"a[244:245], v37 offset:40960\n" \
M_"a[28:31], a[172:175], a[132:135], a[28:31]\n" \
"s_cmpk_lt_u32 s3, 3\n" \
"s_cselect_b32 s15, s15, s17\n" \
M_"a[28:31], a[236:239], a[196:199], a[28:31]\n" \
"s_addk_i32 s84, 0x2000\n" \
M_"a[20:23], a[164:167], a[132:135], a[20:23]\n" \
B_"v25, s[76:79], s84 offen offset:1024 lds\n" \
M_"a[20:23], a[228:231], a[196:199], a[20:23]\n" \
X_"a[246:247], v37 offset:41984\n" \
"v_writelane_b32 v11, s15, 27\n" \
M_"a[16:19], a[160:163], a[132:135], a[16:19]\n" \
X_"a[184:185], v38 offset:32768\n" \
"s_lshl_b32 s15, s3, 10\n" \
M_"a[16:19], a[224:227], a[196:199], a[16:19]\n" \
X_"a[186:187], v38 offset:33792\n" \
"s_addk_i32 s15, 0x4800\n" \
M_"a[32:35], a[160:163], a[136:139], a[32:35]\n" \
"s_addk_i32 s84, 0x2000\n" \
M_"a[32:35], a[224:227], a[200:203], a[32:35]\n" \
B_"v24, s[76:79], s84 offen offset:2048 lds\n" \
M_"a[36:39], a[164:167], a[136:139], a[36:39]\n" \
X_"a[248:249], v38 offset:40960\n" \
"v_writelane_b32 v11, s15, 28\n" \
M_"a[36:39], a[228:231], a[200:203], a[36:39]\n" \
X_"a[250:251], v38 offset:41984\n" \
"s_add_i32 s15, s14, 1\n" \
M_"a[44:47], a[172:175], a[136:139], a[44:47]\n" \
X_"a[188:189], v39 offset:32768\n" \
"s_and_b32 s15, s15, 3\n" \
M_"a[44:47], a[236:239], a[200:203], a[44:47]\n" \
X_"a[190:191], v39 offset:33792\n" \
"s_or_b32 s15, s15, s4\n" \
M_"a[40:43], a[136:139], a[168:171], a[40:43]\n" \
"s_add_u32 s89, s42, 0x0\n" \
"buffer_store_dwordx4 v[192:195], v20, s[80:83], s89 offen offset:2048 sc1\n" \
M_"a[40:43], a[200:203], a[232:235], a[40:43]\n" \
X_"a[252:253], v39 offset:40960\n" \
"s_lshl_b32 s17, s15, 2\n" \
M_"a[56:59], a[140:143], a[168:171], a[56:59]\n" \
X_"a[254:255], v39 offset:41984\n" \
"s_or_b32 s17, s17, s3\n" \
M_"a[56:59], a[204:207], a[232:235], a[56:59]\n" \
"s_mul_i32 s17, s17, 0x9000\n" \
"v_writelane_b32 v11, s17, 30\n" \
M_"a[60:63], a[172:175], a[140:143], a[60:63]\n" \
"s_lshl_b32 s17, s12, 2\n" \
"s_or_b32 s17, s17, s3\n" \
M_"a[60:63], a[236:239], a[204:207], a[60:63]\n" \
"s_addk_i32 s84, 0x2000\n" \
M_"a[52:55], a[164:167], a[140:143], a[52:55]\n" \
B_"v25, s[76:79], s84 offen offset:3072 lds\n" \
M_"a[52:55], a[228:231], a[204:207], a[52:55]\n" \
"s_add_i32 s14, s14, -1\n" \
"s_xor_b32 s17, s17, 8\n" \
M_"a[48:51], a[160:163], a[140:143], a[48:51]\n" \
"s_and_b32 s14, s14, 3\n" \
"s_mul_i32 s17, s17, 0x9000\n" \
M_"a[48:51], a[224:227], a[204:207], a[48:51]\n" \
"s_or_b32 s14, s14, s4\n" \
"v_writelane_b32 v11, s17, 31\n" \
"s_waitcnt lgkmcnt(15)\n" \
M_"a[64:67], a[164:167], v[248:251], a[64:67]\n" \
"s_lshl_b32 s17, s14, 2\n" \
"s_or_b32 s17, s17, s3\n" \
M_"a[64:67], a[228:231], v[48:51], a[64:67]\n" \
"s_mul_i32 s17, s17, 0x9000\n" \
"v_writelane_b32 v11, s17, 32\n" \
M_"a[80:83], a[172:175], v[248:251], a[80:83]\n" \
"s_or_b32 s17, s8, s13\n" \
"s_mul_i32 s17, s17, 0x24800\n" \
M_"a[80:83], a[236:239], v[48:51], a[80:83]\n" \
"s_lshl_b32 s15, s15, 12\n" \
"s_add_i32 s18, s17, 0x3004800\n" \
M_"a[68:71], v[248:251], a[168:171], a[68:71]\n" \
"s_lshl_b32 s12, s12, 12\n" \
"s_add_i32 s19, s18, s15\n" \
"s_waitcnt vmcnt(11) lgkmcnt(0)\n" \
M_"a[68:71], v[48:51], a[232:235], a[68:71]\n" \
"s_barrier\n" \
M_"v[52:55], a[176:179], a[128:131], v[52:55]\n" \
"s_add_u32 m0, s40, 0x8000\n" \
"s_add_u32 s85, s39, 0xa000\n" \
M_"v[52:55], a[240:243], a[192:195], v[52:55]\n" \
B_"v20, s[80:83], s85 offen sc1 lds\n" \
M_"v[56:59], a[180:183], a[128:131], v[56:59]\n" \
X_"a[160:161], v32 offset:0\n" \
"v_accvgpr_read_b32 v224, a120\n" \
M_"v[56:59], a[244:247], a[192:195], v[56:59]\n" \
X_"a[162:163], v32 offset:512\n" \
"v_accvgpr_read_b32 v225, a121\n" \
M_"v[60:63], a[184:187], a[128:131], v[60:63]\n" \
"s_add_u32 m0, s40, 0x9000\n" \
"s_add_u32 s85, s39, 0xb000\n" \
M_"v[60:63], a[248:251], a[192:195], v[60:63]\n" \
B_"v20, s[80:83], s85 offen sc1 lds\n" \
M_"v[64:67], a[188:191], a[128:131], v[64:67]\n" \
X_"a[224:225], v32 offset:4096\n" \
"v_accvgpr_read_b32 v226, a122\n" \
M_"v[64:67], a[252:255], a[192:195], v[64:67]\n" \
R_"a[128:131], v28 offset:32768\n" \
"v_accvgpr_read_b32 v227, a123\n" \
M_"v[80:83], a[188:191], a[132:135], v[80:83]\n" \
R_"a[192:195], v29 offset:32768\n" \
"v_accvgpr_read_b32 v180, a124\n" \
M_"v[80:83], a[252:255], a[196:199], v[80:83]\n" \
"s_add_u32 s88, s42, 0x0\n" \
"buffer_store_dwordx4 v[196:199], v20, s[80:83], s88 offen offset:3072 sc1\n" \
M_"v[76:79], a[184:187], a[132:135], v[76:79]\n" \
X_"a[226:227], v32 offset:4608\n" \
"v_accvgpr_read_b32 v181, a125\n" \
M_"v[76:79], a[248:251], a[196:199], v[76:79]\n" \
X_"a[164:165], v33 offset:0\n" \
"v_accvgpr_read_b32 v182, a126\n" \
M_"v[72:75], a[180:183], a[132:135], v[72:75]\n" \
"s_add_u32 m0, s40, 0xb000\n" \
"s_movk_i32 s84, 0x280\n" \
M_"v[72:75], a[244:247], a[196:199], v[72:75]\n" \
B_"v21, s[76:79], s84 offen offset:0 lds\n" \
M_"v[68:71], a[176:179], a[132:135], v[68:71]\n" \
X_"a[166:167], v33 offset:512\n" \
"v_accvgpr_read_b32 v183, a127\n" \
M_"v[68:71], a[240:243], a[196:199], v[68:71]\n" \
R_"a[132:135], v28 offset:34816\n" \
C_"v224, v224, v225\n" \
M_"v[84:87], a[176:179], a[136:139], v[84:87]\n" \
R_"a[196:199], v29 offset:34816\n" \
C_"v225, v226, v227\n" \
M_"v[84:87], a[240:243], a[200:203], v[84:87]\n" \
"s_add_u32 m0, s31, 0x8000\n" \
"s_mov_b32 s84, 0x280000\n" \
M_"v[88:91], a[180:183], a[136:139], v[88:91]\n" \
B_"v26, s[68:71], s84 offen offset:0 lds\n" \
M_"v[88:91], a[244:247], a[200:203], v[88:91]\n" \
X_"a[228:229], v33 offset:4096\n" \
C_"v226, v180, v181\n" \
M_"v[92:95], a[184:187], a[136:139], v[92:95]\n" \
X_"a[230:231], v33 offset:4608\n" \
C_"v227, v182, v183\n" \
M_"v[92:95], a[248:251], a[200:203], v[92:95]\n" \
"s_addk_i32 s84, 0x7c00\n" \
M_"v[96:99], a[188:191], a[136:139], v[96:99]\n" \
B_"v26, s[68:71], s84 offen offset:1024 lds\n" \
M_"v[96:99], a[252:255], a[200:203], v[96:99]\n" \
R_"a[136:139], v28 offset:36864\n" \
"v_accvgpr_read_b32 v220, a112\n" \
M_"v[112:115], a[188:191], a[140:143], v[112:115]\n" \
R_"a[200:203], v29 offset:36864\n" \
"v_accvgpr_read_b32 v221, a113\n" \
M_"v[112:115], a[252:255], a[204:207], v[112:115]\n" \
"s_addk_i32 s84, 0x7c00\n" \
M_"v[108:111], a[184:187], a[140:143], v[108:111]\n" \
B_"v27, s[68:71], s84 offen offset:2048 lds\n" \
M_"v[108:111], a[248:251], a[204:207], v[108:111]\n" \
X_"a[172:173], v35 offset:0\n" \
"v_accvgpr_read_b32 v222, a114\n" \
M_"v[104:107], a[180:183], a[140:143], v[104:107]\n" \
X_"a[174:175], v35 offset:512\n" \
"v_accvgpr_read_b32 v223, a115\n" \
M_"v[104:107], a[244:247], a[204:207], v[104:107]\n" \
X_"a[236:237], v35 offset:4096\n" \
"v_accvgpr_read_b32 v180, a116\n" \
M_"v[100:103], a[176:179], a[140:143], v[100:103]\n" \
"v_accvgpr_read_b32 v181, a117\n" \
"v_accvgpr_read_b32 v182, a118\n" \
M_"v[100:103], a[240:243], a[204:207], v[100:103]\n" \
R_"a[140:143], v28 offset:38912\n" \
"v_accvgpr_read_b32 v183, a119\n" \
M_"v[116:119], a[176:179], a[144:147], v[116:119]\n" \
R_"a[204:207], v29 offset:38912\n" \
C_"v220, v220, v221\n" \
M_"v[116:119], a[240:243], a[208:211], v[116:119]\n" \
"s_addk_i32 s84, 0x7c00\n" \
M_"v[120:123], a[180:183], a[144:147], v[120:123]\n" \
B_"v27, s[68:71], s84 offen offset:3072 lds\n" \
M_"v[120:123], a[244:247], a[208:211], v[120:123]\n" \
"s_add_u32 m0, s30, 0x0\n" \
"s_movk_i32 s84, 0x300\n" \
"s_nop 0\n" \
M_"v[124:127], a[184:187], a[144:147], v[124:127]\n" \
B_"v24, s[76:79], s84 offen offset:0 lds\n" \
M_"v[124:127], a[248:251], a[208:211], v[124:127]\n" \
X_"a[238:239], v35 offset:4608\n" \
C_"v221, v222, v223\n" \
M_"v[128:131], a[188:191], a[144:147], v[128:131]\n" \
"s_add_u32 s89, s42, 0x1000\n" \
"buffer_store_dwordx4 a[84:87], v20, s[80:83], s89 offen offset:3072 sc1\n" \
M_"v[128:131], a[252:255], a[208:211], v[128:131]\n" \
R_"a[144:147], v28 offset:40960\n" \
C_"v222, v180, v181\n" \
M_"v[144:147], a[188:191], a[148:151], v[144:147]\n" \
R_"a[208:211], v29 offset:40960\n" \
C_"v223, v182, v183\n" \
M_"v[144:147], a[252:255], a[212:215], v[144:147]\n" \
X_"a[168:169], v34 offset:0\n" \
"v_accvgpr_read_b32 v216, a104\n" \
M_"v[140:143], a[184:187], a[148:151], v[140:143]\n" \
X_"a[170:171], v34 offset:512\n" \
"v_accvgpr_read_b32 v217, a105\n" \
M_"v[140:143], a[248:251], a[212:215], v[140:143]\n" \
"s_movk_i32 s84, 0x2300\n" \
"s_nop 0\n" \
M_"v[136:139], a[180:183], a[148:151], v[136:139]\n" \
B_"v25, s[76:79], s84 offen offset:1024 lds\n" \
M_"v[136:139], a[244:247], a[212:215], v[136:139]\n" \
X_"a[232:233], v34 offset:4096\n" \
"v_accvgpr_read_b32 v218, a106\n" \
M_"v[132:135], a[176:179], a[148:151], v[132:135]\n" \
X_"a[234:235], v34 offset:4608\n" \
"v_accvgpr_read_b32 v219, a107\n" \
M_"v[132:135], a[240:243], a[212:215], v[132:135]\n" \
R_"a[148:151], v28 offset:43008\n" \
"v_accvgpr_read_b32 v180, a108\n" \
M_"v[148:151], a[176:179], a[152:155], v[148:151]\n" \
R_"a[212:215], v29 offset:43008\n" \
"v_accvgpr_read_b32 v181, a109\n" \
M_"v[148:151], a[240:243], a[216:219], v[148:151]\n" \
"s_movk_i32 s84, 0x4300\n" \
M_"v[152:155], a[180:183], a[152:155], v[152:155]\n" \
B_"v24, s[76:79], s84 offen offset:2048 lds\n" \
M_"v[152:155], a[244:247], a[216:219], v[152:155]\n" \
"s_movk_i32 s84, 0x6300\n" \
M_"v[156:159], a[184:187], a[152:155], v[156:159]\n" \
B_"v25, s[76:79], s84 offen offset:3072 lds\n" \
M_"v[156:159], a[248:251], a[216:219], v[156:159]\n" \
R_"v[248:251], v22 offset:0\n" \
"v_accvgpr_read_b32 v182, a110\n" \
M_"v[160:163], a[188:191], a[152:155], v[160:163]\n" \
R_"v[48:51], v23 offset:0\n" \
"v_accvgpr_read_b32 v183, a111\n" \
M_"v[160:163], a[252:255], a[216:219], v[160:163]\n" \
R_"a[152:155], v28 offset:45056\n" \
C_"v216, v216, v217\n" \
M_"v[176:179], a[188:191], a[156:159], v[176:179]\n" \
R_"a[216:219], v29 offset:45056\n" \
C_"v217, v218, v219\n" \
M_"v[176:179], a[252:255], a[220:223], v[176:179]\n" \
C_"v218, v180, v181\n" \
C_"v219, v182, v183\n" \
M_"v[172:175], a[184:187], a[156:159], v[172:175]\n" \
"v_accvgpr_read_b32 v192, a96\n" \
"v_accvgpr_read_b32 v193, a97\n" \
M_"v[172:175], a[248:251], a[220:223], v[172:175]\n" \
"v_accvgpr_read_b32 v194, a98\n" \
"v_accvgpr_read_b32 v195, a99\n" \
M_"v[168:171], a[180:183], a[156:159], v[168:171]\n" \
"v_accvgpr_read_b32 v180, a100\n" \
"v_accvgpr_read_b32 v181, a101\n" \
M_"v[168:171], a[244:247], a[220:223], v[168:171]\n" \
"v_accvgpr_read_b32 v182, a102\n" \
"v_accvgpr_read_b32 v183, a103\n" \
M_"v[164:167], a[176:179], a[156:159], v[164:167]\n" \
C_"v192, v192, v193\n" \
C_"v193, v194, v195\n" \
M_"v[164:167], a[240:243], a[220:223], v[164:167]\n" \
R_"a[156:159], v28 offset:47104\n" \
R_"a[220:223], v29 offset:47104\n" \
"s_waitcnt lgkmcnt(15)\n" \
M_"a[0:3], a[160:163], a[128:131], a[0:3]\n" \
"s_add_u32 m0, s30, 0x1000\n" \
"s_addk_i32 s84, 0x3000\n" \
M_"a[0:3], a[224:227], a[192:195], a[0:3]\n" \
B_"v24, s[76:79], s84 offen offset:0 lds\n" \
M_"a[4:7], a[164:167], a[128:131], a[4:7]\n" \
X_"a[176:177], v36 offset:0\n" \
C_"v194, v180, v181\n" \
M_"a[4:7], a[228:231], a[192:195], a[4:7]\n" \
X_"a[178:179], v36 offset:1024\n" \
C_"v195, v182, v183\n" \
M_"a[12:15], a[172:175], a[128:131], a[12:15]\n" \
X_"a[240:241], v36 offset:8192\n" \
"v_accvgpr_read_b32 v188, a88\n" \
"s_waitcnt lgkmcnt(15)\n" \
M_"a[12:15], a[236:239], a[192:195], a[12:15]\n" \
"s_add_u32 s88, s42, 0x1000\n" \
"buffer_store_dwordx4 v[252:255], v20, s[80:83], s88 offen offset:2048 sc1\n" \
"s_waitcnt lgkmcnt(13)\n" \
M_"a[8:11], a[128:131], a[168:171], a[8:11]\n" \
X_"a[242:243], v36 offset:9216\n" \
"v_accvgpr_read_b32 v189, a89\n" \
"s_waitcnt lgkmcnt(12)\n" \
"s_nop 0\n" \
M_"a[8:11], a[192:195], a[232:235], a[8:11]\n" \
X_"a[180:181], v37 offset:0\n" \
"v_accvgpr_read_b32 v190, a90\n" \
M_"a[24:27], a[132:135], a[168:171], a[24:27]\n" \
X_"a[182:183], v37 offset:1024\n" \
"v_accvgpr_read_b32 v191, a91\n" \
M_"a[24:27], a[196:199], a[232:235], a[24:27]\n" \
X_"a[244:245], v37 offset:8192\n" \
"v_accvgpr_read_b32 v180, a92\n" \
M_"a[28:31], a[172:175], a[132:135], a[28:31]\n" \
"v_accvgpr_read_b32 v181, a93\n" \
"v_accvgpr_read_b32 v182, a94\n" \
M_"a[28:31], a[236:239], a[196:199], a[28:31]\n" \
"s_addk_i32 s84, 0x2000\n" \
"s_nop 0\n" \
M_"a[20:23], a[164:167], a[132:135], a[20:23]\n" \
B_"v25, s[76:79], s84 offen offset:1024 lds\n" \
M_"a[20:23], a[228:231], a[196:199], a[20:23]\n" \
X_"a[246:247], v37 offset:9216\n" \
"v_accvgpr_read_b32 v183, a95\n" \
M_"a[16:19], a[160:163], a[132:135], a[16:19]\n" \
X_"a[184:185], v38 offset:0\n" \
C_"v188, v188, v189\n" \
M_"a[16:19], a[224:227], a[196:199], a[16:19]\n" \
X_"a[186:187], v38 offset:1024\n" \
C_"v189, v190, v191\n" \
M_"a[32:35], a[160:163], a[136:139], a[32:35]\n" \
"s_addk_i32 s84, 0x2000\n" \
"s_nop 0\n" \
M_"a[32:35], a[224:227], a[200:203], a[32:35]\n" \
B_"v24, s[76:79], s84 offen offset:2048 lds\n" \
M_"a[36:39], a[164:167], a[136:139], a[36:39]\n" \
X_"a[248:249], v38 offset:8192\n" \
C_"v190, v180, v181\n" \
M_"a[36:39], a[228:231], a[200:203], a[36:39]\n" \
X_"a[250:251], v38 offset:9216\n" \
C_"v191, v182, v183\n" \
M_"a[44:47], a[172:175], a[136:139], a[44:47]\n" \
X_"a[188:189], v39 offset:0\n" \
".fill (L_pb + 28384 - .) / 4, 4, 0xBF800000\n" \
"L_pp7:\n" \
"s_cbranch_execz L_pret\n" \
"s_nop 0\n" \
"v_accvgpr_read_b32 v184, a72\n" \
M_"a[44:47], a[236:239], a[200:203], a[44:47]\n" \
X_"a[190:191], v39 offset:1024\n" \
"v_accvgpr_read_b32 v185, a73\n" \
M_"a[40:43], a[136:139], a[168:171], a[40:43]\n" \
"s_add_u32 s89, s42, 0x2000\n" \
"buffer_store_dwordx4 v[200:203], v20, s[80:83], s89 offen offset:2048 sc1\n" \
M_"a[40:43], a[200:203], a[232:235], a[40:43]\n" \
X_"a[252:253], v39 offset:8192\n" \
"v_accvgpr_read_b32 v186, a74\n" \
M_"a[56:59], a[140:143], a[168:171], a[56:59]\n" \
X_"a[254:255], v39 offset:9216\n" \
"v_accvgpr_read_b32 v187, a75\n" \
M_"a[56:59], a[204:207], a[232:235], a[56:59]\n" \
"v_accvgpr_read_b32 v180, a76\n" \
"v_accvgpr_read_b32 v181, a77\n" \
M_"a[60:63], a[172:175], a[140:143], a[60:63]\n" \
"v_accvgpr_read_b32 v182, a78\n" \
"v_accvgpr_read_b32 v183, a79\n" \
M_"a[60:63], a[236:239], a[204:207], a[60:63]\n" \
"s_addk_i32 s84, 0x2000\n" \
"s_nop 0\n" \
M_"a[52:55], a[164:167], a[140:143], a[52:55]\n" \
B_"v25, s[76:79], s84 offen offset:3072 lds\n" \
M_"a[52:55], a[228:231], a[204:207], a[52:55]\n" \
C_"v184, v184, v185\n" \
C_"v185, v186, v187\n" \
M_"a[48:51], a[160:163], a[140:143], a[48:51]\n" \
C_"v186, v180, v181\n" \
C_"v187, v182, v183\n" \
M_"a[48:51], a[224:227], a[204:207], a[48:51]\n" \
"v_writelane_b32 v11, s19, 33\n" \
"s_xor_b32 s12, s12, 0x2000\n" \
"s_waitcnt lgkmcnt(15)\n" \
M_"a[64:67], a[164:167], v[248:251], a[64:67]\n" \
"s_lshl_b32 s14, s14, 12\n" \
"s_sub_i32 s13, s8, s13\n" \
M_"a[64:67], a[228:231], v[48:51], a[64:67]\n" \
"s_add_i32 s19, s18, s12\n" \
"v_writelane_b32 v11, s19, 34\n" \
M_"a[80:83], a[172:175], v[248:251], a[80:83]\n" \
"s_add_i32 s18, s18, s14\n" \
"s_mul_i32 s13, s13, 0x24800\n" \
M_"a[80:83], a[236:239], v[48:51], a[80:83]\n" \
"v_writelane_b32 v11, s18, 35\n" \
"s_add_i32 s18, s13, 0x4120800\n" \
M_"a[68:71], v[248:251], a[168:171], a[68:71]\n" \
"s_add_i32 s15, s18, s15\n" \
"v_writelane_b32 v11, s15, 36\n" \
"s_waitcnt vmcnt(11) lgkmcnt(0)\n" \
M_"a[68:71], v[48:51], a[232:235], a[68:71]\n" \
"s_barrier\n" \
"v_mov_b32_e32 v45, s62\n" \
"s_mov_b64 exec, 1\n" \
"buffer_store_dword v45, off, s[80:83], s37 offset:0 sc1\n" \
"s_mov_b64 exec, -1\n" \
"v_xor_b32_e32 v28, 0x20000, v28\n" \
"v_xor_b32_e32 v29, 0x20000, v29\n" \
M_"v[52:55], a[176:179], a[128:131], v[52:55]\n" \
"s_add_u32 m0, s40, 0x0\n" \
"s_add_u32 s85, s39, 0xc000\n" \
M_"v[52:55], a[240:243], a[192:195], v[52:55]\n" \
B_"v20, s[80:83], s85 offen sc1 lds\n" \
M_"v[56:59], a[180:183], a[128:131], v[56:59]\n" \
X_"a[160:161], v32 offset:32768\n" \
"s_add_i32 s12, s18, s12\n" \
M_"v[56:59], a[244:247], a[192:195], v[56:59]\n" \
X_"a[162:163], v32 offset:33280\n" \
"v_writelane_b32 v11, s12, 37\n" \
M_"v[60:63], a[184:187], a[128:131], v[60:63]\n" \
"s_add_u32 m0, s40, 0x1000\n" \
"s_add_u32 s85, s39, 0xd000\n" \
M_"v[60:63], a[248:251], a[192:195], v[60:63]\n" \
B_"v20, s[80:83], s85 offen sc1 lds\n" \
M_"v[64:67], a[188:191], a[128:131], v[64:67]\n" \
X_"a[224:225], v32 offset:36864\n" \
"s_add_i32 s18, s18, s14\n" \
M_"v[64:67], a[252:255], a[192:195], v[64:67]\n" \
R_"a[128:131], v28 offset:0\n" \
"s_or_b32 s12, s17, 0x3000000\n" \
M_"v[80:83], a[188:191], a[132:135], v[80:83]\n" \
R_"a[192:195], v29 offset:0\n" \
"s_lshl_b32 s14, s5, 11\n" \
M_"v[80:83], a[252:255], a[196:199], v[80:83]\n" \
"s_add_u32 m0, s40, 0x3000\n" \
"s_movk_i32 s84, 0x300\n" \
M_"v[76:79], a[184:187], a[132:135], v[76:79]\n" \
B_"v21, s[76:79], s84 offen offset:0 lds\n" \
M_"v[76:79], a[248:251], a[196:199], v[76:79]\n" \
X_"a[226:227], v32 offset:37376\n" \
"v_writelane_b32 v11, s18, 38\n" \
M_"v[72:75], a[180:183], a[132:135], v[72:75]\n" \
X_"a[164:165], v33 offset:32768\n" \
"s_add_i32 s15, s12, s14\n" \
M_"v[72:75], a[244:247], a[196:199], v[72:75]\n" \
"v_writelane_b32 v11, s15, 39\n" \
"s_add_i32 s15, s13, 0x411c000\n" \
M_"v[68:71], a[176:179], a[132:135], v[68:71]\n" \
X_"a[166:167], v33 offset:33280\n" \
"s_add_i32 s18, s15, s14\n" \
M_"v[68:71], a[240:243], a[196:199], v[68:71]\n" \
R_"a[132:135], v28 offset:2048\n" \
"v_writelane_b32 v11, s18, 40\n" \
M_"v[84:87], a[176:179], a[136:139], v[84:87]\n" \
R_"a[196:199], v29 offset:2048\n" \
"s_add_i32 s17, s17, 0x3004000\n" \
M_"v[84:87], a[240:243], a[200:203], v[84:87]\n" \
"s_add_u32 m0, s31, 0x0\n" \
"s_nop 0\n" \
"s_mov_b32 s84, 0x300000\n" \
M_"v[88:91], a[180:183], a[136:139], v[88:91]\n" \
B_"v26, s[68:71], s84 offen offset:0 lds\n" \
M_"v[88:91], a[244:247], a[200:203], v[88:91]\n" \
"s_add_u32 s88, s42, 0x2000\n" \
"buffer_store_dwordx4 v[204:207], v20, s[80:83], s88 offen offset:3072 sc1\n" \
M_"v[92:95], a[184:187], a[136:139], v[92:95]\n" \
X_"a[228:229], v33 offset:36864\n" \
"v_writelane_b32 v11, s17, 41\n" \
M_"v[92:95], a[248:251], a[200:203], v[92:95]\n" \
X_"a[230:231], v33 offset:37376\n" \
"s_add_i32 s13, s13, 0x4120000\n" \
M_"v[96:99], a[188:191], a[136:139], v[96:99]\n" \
"v_writelane_b32 v11, s13, 42\n" \
"s_lshl_b32 s13, s9, 2\n" \
M_"v[96:99], a[252:255], a[200:203], v[96:99]\n" \
R_"a[136:139], v28 offset:4096\n" \
"s_or_b32 s7, s13, s7\n" \
M_"v[112:115], a[188:191], a[140:143], v[112:115]\n" \
R_"a[200:203], v29 offset:4096\n" \
"s_mul_i32 s7, s7, 0x90000\n" \
M_"v[112:115], a[252:255], a[204:207], v[112:115]\n" \
"s_addk_i32 s84, 0x7c00\n" \
M_"v[108:111], a[184:187], a[140:143], v[108:111]\n" \
B_"v26, s[68:71], s84 offen offset:1024 lds\n" \
M_"v[108:111], a[248:251], a[204:207], v[108:111]\n" \
X_"a[172:173], v35 offset:32768\n" \
"s_lshl_b32 s13, s3, 14\n" \
M_"v[104:107], a[180:183], a[140:143], v[104:107]\n" \
"s_addk_i32 s84, 0x7c00\n" \
M_"v[104:107], a[244:247], a[204:207], v[104:107]\n" \
B_"v27, s[68:71], s84 offen offset:2048 lds\n" \
M_"v[100:103], a[176:179], a[140:143], v[100:103]\n" \
X_"a[174:175], v35 offset:33280\n" \
"s_lshl_b32 s9, s9, 7\n" \
M_"v[100:103], a[240:243], a[204:207], v[100:103]\n" \
R_"a[140:143], v28 offset:6144\n" \
"s_lshl_b32 s8, s8, 4\n" \
M_"v[116:119], a[176:179], a[144:147], v[116:119]\n" \
R_"a[204:207], v29 offset:6144\n" \
"s_add_i32 s7, s7, s13\n" \
M_"v[116:119], a[240:243], a[208:211], v[116:119]\n" \
"s_addk_i32 s84, 0x7c00\n" \
M_"v[120:123], a[180:183], a[144:147], v[120:123]\n" \
B_"v27, s[68:71], s84 offen offset:3072 lds\n" \
M_"v[120:123], a[244:247], a[208:211], v[120:123]\n" \
X_"a[236:237], v35 offset:36864\n" \
"s_or_b32 s13, s8, s9\n" \
M_"v[124:127], a[184:187], a[144:147], v[124:127]\n" \
X_"a[238:239], v35 offset:37376\n" \
"s_sub_i32 s8, s8, s9\n" \
M_"v[124:127], a[248:251], a[208:211], v[124:127]\n" \
"s_add_u32 m0, s30, 0x8000\n" \
"s_movk_i32 s84, 0x380\n" \
M_"v[128:131], a[188:191], a[144:147], v[128:131]\n" \
B_"v24, s[76:79], s84 offen offset:0 lds\n" \
M_"v[128:131], a[252:255], a[208:211], v[128:131]\n" \
R_"a[144:147], v28 offset:8192\n" \
"s_mulk_i32 s13, 0x480\n" \
M_"v[144:147], a[188:191], a[148:151], v[144:147]\n" \
R_"a[208:211], v29 offset:8192\n" \
"s_mulk_i32 s8, 0x480\n" \
M_"v[144:147], a[252:255], a[212:215], v[144:147]\n" \
X_"a[168:169], v34 offset:32768\n" \
"s_or_b32 s13, s13, s11\n" \
M_"v[140:143], a[184:187], a[148:151], v[140:143]\n" \
"s_movk_i32 s84, 0x2380\n" \
M_"v[140:143], a[248:251], a[212:215], v[140:143]\n" \
B_"v25, s[76:79], s84 offen offset:1024 lds\n" \
M_"v[136:139], a[180:183], a[148:151], v[136:139]\n" \
X_"a[170:171], v34 offset:33280\n" \
"s_or_b32 s8, s8, s11\n" \
M_"v[136:139], a[244:247], a[212:215], v[136:139]\n" \
X_"a[232:233], v34 offset:36864\n" \
"s_and_b32 s11, s2, 1\n" \
M_"v[132:135], a[176:179], a[148:151], v[132:135]\n" \
X_"a[234:235], v34 offset:37376\n" \
M_"v[132:135], a[240:243], a[212:215], v[132:135]\n" \
R_"a[148:151], v28 offset:10240\n" \
M_"v[148:151], a[176:179], a[152:155], v[148:151]\n" \
R_"a[212:215], v29 offset:10240\n" \
M_"v[148:151], a[240:243], a[216:219], v[148:151]\n" \
"s_movk_i32 s84, 0x4380\n" \
M_"v[152:155], a[180:183], a[152:155], v[152:155]\n" \
B_"v24, s[76:79], s84 offen offset:2048 lds\n" \
M_"v[152:155], a[244:247], a[216:219], v[152:155]\n" \
"s_movk_i32 s84, 0x6380\n" \
M_"v[156:159], a[184:187], a[152:155], v[156:159]\n" \
B_"v25, s[76:79], s84 offen offset:3072 lds\n" \
M_"v[156:159], a[248:251], a[216:219], v[156:159]\n" \
R_"v[248:251], v22 offset:32768\n" \
M_"v[160:163], a[188:191], a[152:155], v[160:163]\n" \
R_"v[48:51], v23 offset:32768\n" \
M_"v[160:163], a[252:255], a[216:219], v[160:163]\n" \
R_"a[152:155], v28 offset:12288\n" \
M_"v[176:179], a[188:191], a[156:159], v[176:179]\n" \
R_"a[216:219], v29 offset:12288\n" \
M_"v[176:179], a[252:255], a[220:223], v[176:179]\n" \
"s_cmp_lg_u32 s11, 0\n" \
"s_cselect_b32 s12, s15, s12\n" \
M_"v[172:175], a[184:187], a[156:159], v[172:175]\n" \
"s_bfe_u32 s9, s2, 0x20001\n" \
"s_add_i32 s15, s7, 0x4240000\n" \
M_"v[172:175], a[248:251], a[220:223], v[172:175]\n" \
"s_addk_i32 s12, 0x4000\n" \
"s_lshl_b32 s5, s5, 16\n" \
M_"v[168:171], a[180:183], a[156:159], v[168:171]\n" \
"s_xor_b32 s9, s9, s3\n" \
M_"v[168:171], a[244:247], a[220:223], v[168:171]\n" \
"s_cmp_lg_u32 s9, 0\n" \
"s_cselect_b32 s12, s6, s12\n" \
M_"v[164:167], a[176:179], a[156:159], v[164:167]\n" \
"s_add_i32 s5, s15, s5\n" \
"v_writelane_b32 v11, s12, 43\n" \
M_"v[164:167], a[240:243], a[220:223], v[164:167]\n" \
R_"a[156:159], v28 offset:14336\n" \
R_"a[220:223], v29 offset:14336\n" \
"s_waitcnt lgkmcnt(15)\n" \
M_"a[0:3], a[160:163], a[128:131], a[0:3]\n" \
"s_add_u32 m0, s30, 0x9000\n" \
"s_addk_i32 s84, 0x3000\n" \
M_"a[0:3], a[224:227], a[192:195], a[0:3]\n" \
B_"v24, s[76:79], s84 offen offset:0 lds\n" \
M_"a[4:7], a[164:167], a[128:131], a[4:7]\n" \
X_"a[176:177], v36 offset:32768\n" \
"s_add_i32 s7, s7, 0x42c0000\n" \
M_"a[4:7], a[228:231], a[192:195], a[4:7]\n" \
X_"a[178:179], v36 offset:33792\n" \
"v_writelane_b32 v11, s5, 44\n" \
M_"a[12:15], a[172:175], a[128:131], a[12:15]\n" \
X_"a[240:241], v36 offset:40960\n" \
"s_lshl_b32 s5, s11, 10\n" \
"s_waitcnt lgkmcnt(15)\n" \
M_"a[12:15], a[236:239], a[192:195], a[12:15]\n" \
"s_add_u32 s89, s42, 0x3000\n" \
"buffer_store_dwordx4 v[208:211], v20, s[80:83], s89 offen offset:3072 sc1\n" \
"s_waitcnt lgkmcnt(13)\n" \
M_"a[8:11], a[128:131], a[168:171], a[8:11]\n" \
X_"a[242:243], v36 offset:41984\n" \
"s_or_b32 s5, s7, s5\n" \
"s_waitcnt lgkmcnt(12)\n" \
M_"a[8:11], a[192:195], a[232:235], a[8:11]\n" \
X_"a[180:181], v37 offset:32768\n" \
M_"a[24:27], a[132:135], a[168:171], a[24:27]\n" \
X_"a[182:183], v37 offset:33792\n" \
M_"a[24:27], a[196:199], a[232:235], a[24:27]\n" \
X_"a[244:245], v37 offset:40960\n" \
M_"a[28:31], a[172:175], a[132:135], a[28:31]\n" \
"s_cmp_lg_u32 s9, 0\n" \
"s_cselect_b32 s5, s6, s5\n" \
M_"a[28:31], a[236:239], a[196:199], a[28:31]\n" \
"s_addk_i32 s84, 0x2000\n" \
M_"a[20:23], a[164:167], a[132:135], a[20:23]\n" \
B_"v25, s[76:79], s84 offen offset:1024 lds\n" \
M_"a[20:23], a[228:231], a[196:199], a[20:23]\n" \
X_"a[246:247], v37 offset:41984\n" \
"s_add_i32 s8, s8, 0x21c000\n" \
M_"a[16:19], a[160:163], a[132:135], a[16:19]\n" \
X_"a[184:185], v38 offset:32768\n" \
"v_writelane_b32 v11, s5, 45\n" \
M_"a[16:19], a[224:227], a[196:199], a[16:19]\n" \
X_"a[186:187], v38 offset:33792\n" \
"s_add_i32 s5, s13, s10\n" \
M_"a[32:35], a[160:163], a[136:139], a[32:35]\n" \
"s_addk_i32 s84, 0x2000\n" \
M_"a[32:35], a[224:227], a[200:203], a[32:35]\n" \
B_"v24, s[76:79], s84 offen offset:2048 lds\n" \
M_"a[36:39], a[164:167], a[136:139], a[36:39]\n" \
X_"a[248:249], v38 offset:40960\n" \
"v_writelane_b32 v11, s5, 46\n" \
M_"a[36:39], a[228:231], a[200:203], a[36:39]\n" \
X_"a[250:251], v38 offset:41984\n" \
"s_add_i32 s5, s8, s10\n" \
"s_nop 0\n" \
M_"a[44:47], a[172:175], a[136:139], a[44:47]\n" \
X_"a[188:189], v39 offset:32768\n" \
"v_writelane_b32 v11, s5, 47\n" \
M_"a[44:47], a[236:239], a[200:203], a[44:47]\n" \
X_"a[190:191], v39 offset:33792\n" \
M_"a[40:43], a[136:139], a[168:171], a[40:43]\n" \
"s_add_u32 s88, s42, 0x3000\n" \
"buffer_store_dwordx4 v[212:215], v20, s[80:83], s88 offen offset:2048 sc1\n" \
M_"a[40:43], a[200:203], a[232:235], a[40:43]\n" \
X_"a[252:253], v39 offset:40960\n" \
M_"a[56:59], a[140:143], a[168:171], a[56:59]\n" \
X_"a[254:255], v39 offset:41984\n" \
M_"a[56:59], a[204:207], a[232:235], a[56:59]\n" \
"s_cmp_lg_u32 s11, 0\n" \
"s_cselect_b32 s5, s8, s13\n" \
M_"a[60:63], a[172:175], a[140:143], a[60:63]\n" \
"s_addk_i32 s5, 0x100\n" \
M_"a[60:63], a[236:239], a[204:207], a[60:63]\n" \
"s_addk_i32 s84, 0x2000\n" \
M_"a[52:55], a[164:167], a[140:143], a[52:55]\n" \
B_"v25, s[76:79], s84 offen offset:3072 lds\n" \
M_"a[52:55], a[228:231], a[204:207], a[52:55]\n" \
"s_cmp_lg_u32 s9, 0\n" \
"s_cselect_b32 s5, s6, s5\n" \
M_"a[48:51], a[160:163], a[140:143], a[48:51]\n" \
"v_writelane_b32 v11, s5, 48\n" \
"s_or_b32 s5, s7, s14\n" \
M_"a[48:51], a[224:227], a[204:207], a[48:51]\n" \
"v_writelane_b32 v11, s5, 56\n" \
"s_lshl_b32 s5, s3, 17\n" \
"s_waitcnt lgkmcnt(15)\n" \
M_"a[64:67], a[164:167], v[248:251], a[64:67]\n" \
"s_add_i32 s5, s15, s5\n" \
"s_or_b32 s4, s5, s4\n" \
M_"a[64:67], a[228:231], v[48:51], a[64:67]\n" \
"s_or_b32 s4, s4, 0x3840\n" \
"v_writelane_b32 v11, s4, 6\n" \
M_"a[80:83], a[172:175], v[248:251], a[80:83]\n" \
"s_mov_b32 s4, 0xff0000\n" \
M_"a[80:83], a[236:239], v[48:51], a[80:83]\n" \
"s_cmp_lg_u32 s9, 0\n" \
"s_cselect_b32 s4, s16, s4\n" \
M_"a[68:71], v[248:251], a[168:171], a[68:71]\n" \
"v_writelane_b32 v11, s4, 7\n" \
"s_lshl_b32 s4, s2, 16\n" \
"s_waitcnt vmcnt(10) lgkmcnt(0)\n" \
M_"a[68:71], v[48:51], a[232:235], a[68:71]\n" \
"s_barrier\n" \
"v_xor_b32_e32 v28, 0x20000, v28\n" \
"v_xor_b32_e32 v29, 0x20000, v29\n" \
M_"v[52:55], a[176:179], a[128:131], v[52:55]\n" \
"s_add_u32 m0, s40, 0x8000\n" \
"s_add_u32 s85, s39, 0xe000\n" \
M_"v[52:55], a[240:243], a[192:195], v[52:55]\n" \
B_"v20, s[80:83], s85 offen sc1 lds\n" \
M_"v[56:59], a[180:183], a[128:131], v[56:59]\n" \
X_"a[160:161], v32 offset:0\n" \
"s_add_i32 s5, s4, 0x10000\n" \
M_"v[56:59], a[244:247], a[192:195], v[56:59]\n" \
X_"a[162:163], v32 offset:512\n" \
"s_and_b32 s5, s5, 0x70000\n" \
M_"v[60:63], a[184:187], a[128:131], v[60:63]\n" \
"s_add_u32 m0, s40, 0x9000\n" \
"s_add_u32 s85, s39, 0xf000\n" \
M_"v[60:63], a[248:251], a[192:195], v[60:63]\n" \
B_"v20, s[80:83], s85 offen sc1 lds\n" \
M_"v[64:67], a[188:191], a[128:131], v[64:67]\n" \
X_"a[224:225], v32 offset:4096\n" \
"s_add_i32 s5, s15, s5\n" \
M_"v[64:67], a[252:255], a[192:195], v[64:67]\n" \
R_"a[128:131], v28 offset:0\n" \
"v_writelane_b32 v11, s5, 49\n" \
M_"v[80:83], a[188:191], a[132:135], v[80:83]\n" \
R_"a[192:195], v29 offset:0\n" \
"s_add_i32 s5, s4, 0x20000\n" \
M_"v[80:83], a[252:255], a[196:199], v[80:83]\n" \
"s_add_u32 m0, s40, 0xb000\n" \
"s_movk_i32 s84, 0x380\n" \
M_"v[76:79], a[184:187], a[132:135], v[76:79]\n" \
B_"v21, s[76:79], s84 offen offset:0 lds\n" \
M_"v[76:79], a[248:251], a[196:199], v[76:79]\n" \
X_"a[226:227], v32 offset:4608\n" \
"s_and_b32 s5, s5, 0x70000\n" \
M_"v[72:75], a[180:183], a[132:135], v[72:75]\n" \
X_"a[164:165], v33 offset:0\n" \
"s_add_i32 s5, s15, s5\n" \
M_"v[72:75], a[244:247], a[196:199], v[72:75]\n" \
"s_bitset1_b32 s5, 11\n" \
"v_writelane_b32 v11, s5, 50\n" \
M_"v[68:71], a[176:179], a[132:135], v[68:71]\n" \
X_"a[166:167], v33 offset:512\n" \
"s_add_i32 s5, s4, 0x30000\n" \
M_"v[68:71], a[240:243], a[196:199], v[68:71]\n" \
R_"a[132:135], v28 offset:2048\n" \
"s_and_b32 s5, s5, 0x70000\n" \
M_"v[84:87], a[176:179], a[136:139], v[84:87]\n" \
R_"a[196:199], v29 offset:2048\n" \
"s_add_i32 s5, s15, s5\n" \
M_"v[84:87], a[240:243], a[200:203], v[84:87]\n" \
"s_add_u32 m0, s31, 0x8000\n" \
"s_mov_b32 s84, 0x380000\n" \
M_"v[88:91], a[180:183], a[136:139], v[88:91]\n" \
B_"v26, s[68:71], s84 offen offset:0 lds\n" \
M_"v[88:91], a[244:247], a[200:203], v[88:91]\n" \
"s_add_u32 s89, s42, 0x0\n" \
"buffer_store_dwordx4 v[184:187], v20, s[80:83], s89 offen offset:1024 sc1\n" \
M_"v[92:95], a[184:187], a[136:139], v[92:95]\n" \
X_"a[228:229], v33 offset:4096\n" \
"s_bitset1_b32 s5, 12\n" \
M_"v[92:95], a[248:251], a[200:203], v[92:95]\n" \
X_"a[230:231], v33 offset:4608\n" \
"v_writelane_b32 v11, s5, 51\n" \
M_"v[96:99], a[188:191], a[136:139], v[96:99]\n" \
"s_and_b32 s5, s4, 0x70000\n" \
"s_xor_b32 s5, s5, 0x40000\n" \
M_"v[96:99], a[252:255], a[200:203], v[96:99]\n" \
R_"a[136:139], v28 offset:4096\n" \
"s_add_i32 s5, s15, s5\n" \
M_"v[112:115], a[188:191], a[140:143], v[112:115]\n" \
R_"a[200:203], v29 offset:4096\n" \
"s_or_b32 s5, s5, 0x1800\n" \
M_"v[112:115], a[252:255], a[204:207], v[112:115]\n" \
"s_addk_i32 s84, 0x7c00\n" \
M_"v[108:111], a[184:187], a[140:143], v[108:111]\n" \
B_"v26, s[68:71], s84 offen offset:1024 lds\n" \
M_"v[108:111], a[248:251], a[204:207], v[108:111]\n" \
X_"a[172:173], v35 offset:0\n" \
"v_writelane_b32 v11, s5, 52\n" \
M_"v[104:107], a[180:183], a[140:143], v[104:107]\n" \
"s_addk_i32 s84, 0x7c00\n" \
M_"v[104:107], a[244:247], a[204:207], v[104:107]\n" \
B_"v27, s[68:71], s84 offen offset:2048 lds\n" \
M_"v[100:103], a[176:179], a[140:143], v[100:103]\n" \
X_"a[174:175], v35 offset:512\n" \
"s_add_i32 s5, s4, 0x50000\n" \
M_"v[100:103], a[240:243], a[204:207], v[100:103]\n" \
R_"a[140:143], v28 offset:6144\n" \
"s_and_b32 s5, s5, 0x70000\n" \
M_"v[116:119], a[176:179], a[144:147], v[116:119]\n" \
R_"a[204:207], v29 offset:6144\n" \
"s_add_i32 s5, s15, s5\n" \
M_"v[116:119], a[240:243], a[208:211], v[116:119]\n" \
"s_addk_i32 s84, 0x7c00\n" \
M_"v[120:123], a[180:183], a[144:147], v[120:123]\n" \
B_"v27, s[68:71], s84 offen offset:3072 lds\n" \
M_"v[120:123], a[244:247], a[208:211], v[120:123]\n" \
X_"a[236:237], v35 offset:4096\n" \
"s_bitset1_b32 s5, 13\n" \
M_"v[124:127], a[184:187], a[144:147], v[124:127]\n" \
X_"a[238:239], v35 offset:4608\n" \
"v_writelane_b32 v11, s5, 53\n" \
M_"v[124:127], a[248:251], a[208:211], v[124:127]\n" \
"s_add_u32 m0, s30, 0x20000\n" \
"s_movk_i32 s84, 0x400\n" \
M_"v[128:131], a[188:191], a[144:147], v[128:131]\n" \
B_"v24, s[76:79], s84 offen offset:0 lds\n" \
M_"v[128:131], a[252:255], a[208:211], v[128:131]\n" \
R_"a[144:147], v28 offset:8192\n" \
"s_add_i32 s5, s4, 0x60000\n" \
M_"v[144:147], a[188:191], a[148:151], v[144:147]\n" \
R_"a[208:211], v29 offset:8192\n" \
"s_and_b32 s5, s5, 0x70000\n" \
M_"v[144:147], a[252:255], a[212:215], v[144:147]\n" \
X_"a[168:169], v34 offset:0\n" \
"s_add_i32 s4, s4, 0x70000\n" \
M_"v[140:143], a[184:187], a[148:151], v[140:143]\n" \
"s_movk_i32 s84, 0x2400\n" \
M_"v[140:143], a[248:251], a[212:215], v[140:143]\n" \
B_"v25, s[76:79], s84 offen offset:1024 lds\n" \
M_"v[136:139], a[180:183], a[148:151], v[136:139]\n" \
X_"a[170:171], v34 offset:512\n" \
"s_add_i32 s5, s15, s5\n" \
M_"v[136:139], a[244:247], a[212:215], v[136:139]\n" \
X_"a[232:233], v34 offset:4096\n" \
"s_and_b32 s4, s4, 0x70000\n" \
M_"v[132:135], a[176:179], a[148:151], v[132:135]\n" \
X_"a[234:235], v34 offset:4608\n" \
"s_or_b32 s5, s5, 0x2800\n" \
M_"v[132:135], a[240:243], a[212:215], v[132:135]\n" \
R_"a[148:151], v28 offset:10240\n" \
"v_writelane_b32 v11, s5, 54\n" \
M_"v[148:151], a[176:179], a[152:155], v[148:151]\n" \
R_"a[212:215], v29 offset:10240\n" \
"s_add_i32 s15, s15, s4\n" \
M_"v[148:151], a[240:243], a[216:219], v[148:151]\n" \
"s_movk_i32 s84, 0x4400\n" \
M_"v[152:155], a[180:183], a[152:155], v[152:155]\n" \
B_"v24, s[76:79], s84 offen offset:2048 lds\n" \
M_"v[152:155], a[244:247], a[216:219], v[152:155]\n" \
"s_movk_i32 s84, 0x6400\n" \
M_"v[156:159], a[184:187], a[152:155], v[156:159]\n" \
B_"v25, s[76:79], s84 offen offset:3072 lds\n" \
M_"v[156:159], a[248:251], a[216:219], v[156:159]\n" \
R_"v[248:251], v22 offset:0\n" \
"s_or_b32 s4, s15, 0x3000\n" \
M_"v[160:163], a[188:191], a[152:155], v[160:163]\n" \
R_"v[48:51], v23 offset:0\n" \
"v_writelane_b32 v11, s4, 55\n" \
M_"v[160:163], a[252:255], a[216:219], v[160:163]\n" \
R_"a[152:155], v28 offset:12288\n" \
"s_movk_i32 s11, 0x4000\n" \
M_"v[176:179], a[188:191], a[156:159], v[176:179]\n" \
R_"a[216:219], v29 offset:12288\n" \
"s_lshl_b32 s4, s2, 2\n" \
M_"v[176:179], a[252:255], a[220:223], v[176:179]\n" \
"s_and_b32 s4, s4, 28\n" \
"s_bfe_u32 s5, s2, 0x20003\n" \
M_"v[172:175], a[184:187], a[156:159], v[172:175]\n" \
"s_or_b32 s4, s4, s5\n" \
"s_lshr_b32 s5, s2, 5\n" \
M_"v[172:175], a[248:251], a[220:223], v[172:175]\n" \
"s_lshr_b32 s19, s3, 1\n" \
"s_lshl_b32 s18, s5, 1\n" \
M_"v[168:171], a[180:183], a[156:159], v[168:171]\n" \
"s_lshl_b32 s6, s4, 20\n" \
"s_lshl_b32 s7, s4, 8\n" \
M_"v[168:171], a[244:247], a[220:223], v[168:171]\n" \
"s_and_b32 s13, s3, 1\n" \
"s_add_i32 s14, s18, s19\n" \
M_"v[164:167], a[176:179], a[156:159], v[164:167]\n" \
"s_add_u32 s6, s26, s6\n" \
"s_addc_u32 s9, s27, 0\n" \
M_"v[164:167], a[240:243], a[220:223], v[164:167]\n" \
R_"a[156:159], v28 offset:14336\n" \
R_"a[220:223], v29 offset:14336\n" \
"s_waitcnt lgkmcnt(15)\n" \
M_"a[0:3], a[160:163], a[128:131], a[0:3]\n" \
"s_add_u32 m0, s30, 0x21000\n" \
"s_addk_i32 s84, 0x3000\n" \
M_"a[0:3], a[224:227], a[192:195], a[0:3]\n" \
B_"v24, s[76:79], s84 offen offset:0 lds\n" \
M_"a[4:7], a[164:167], a[128:131], a[4:7]\n" \
X_"a[176:177], v36 offset:0\n" \
M_"a[4:7], a[228:231], a[192:195], a[4:7]\n" \
X_"a[178:179], v36 offset:1024\n" \
M_"a[12:15], a[172:175], a[128:131], a[12:15]\n" \
X_"a[240:241], v36 offset:8192\n" \
"s_waitcnt lgkmcnt(15)\n" \
M_"a[12:15], a[236:239], a[192:195], a[12:15]\n" \
"s_add_u32 s88, s42, 0x1000\n" \
"buffer_store_dwordx4 v[188:191], v20, s[80:83], s88 offen offset:1024 sc1\n" \
"s_waitcnt lgkmcnt(13)\n" \
M_"a[8:11], a[128:131], a[168:171], a[8:11]\n" \
X_"a[242:243], v36 offset:9216\n" \
"s_waitcnt lgkmcnt(12)\n" \
M_"a[8:11], a[192:195], a[232:235], a[8:11]\n" \
X_"a[180:181], v37 offset:0\n" \
M_"a[24:27], a[132:135], a[168:171], a[24:27]\n" \
X_"a[182:183], v37 offset:1024\n" \
M_"a[24:27], a[196:199], a[232:235], a[24:27]\n" \
X_"a[244:245], v37 offset:8192\n" \
M_"a[28:31], a[172:175], a[132:135], a[28:31]\n" \
"s_add_u32 s8, s6, 0x1000000\n" \
"s_addc_u32 s6, s9, 0\n" \
M_"a[28:31], a[236:239], a[196:199], a[28:31]\n" \
"s_addk_i32 s84, 0x2000\n" \
M_"a[20:23], a[164:167], a[132:135], a[20:23]\n" \
B_"v25, s[76:79], s84 offen offset:1024 lds\n" \
M_"a[20:23], a[228:231], a[196:199], a[20:23]\n" \
X_"a[246:247], v37 offset:9216\n" \
"s_and_b32 s9, s6, 0xffff\n" \
M_"a[16:19], a[160:163], a[132:135], a[16:19]\n" \
X_"a[184:185], v38 offset:0\n" \
"s_lshl_b32 s6, s5, 16\n" \
M_"a[16:19], a[224:227], a[196:199], a[16:19]\n" \
X_"a[186:187], v38 offset:1024\n" \
"s_sub_i32 s10, 0x100000, s6\n" \
M_"a[32:35], a[160:163], a[136:139], a[32:35]\n" \
"s_addk_i32 s84, 0x2000\n" \
"s_nop 0\n" \
M_"a[32:35], a[224:227], a[200:203], a[32:35]\n" \
B_"v24, s[76:79], s84 offen offset:2048 lds\n" \
M_"a[36:39], a[164:167], a[136:139], a[36:39]\n" \
X_"a[248:249], v38 offset:8192\n" \
"s_mov_b32 s6, 0x8000\n" \
M_"a[36:39], a[228:231], a[200:203], a[36:39]\n" \
X_"a[250:251], v38 offset:9216\n" \
M_"a[44:47], a[172:175], a[136:139], a[44:47]\n" \
X_"a[188:189], v39 offset:0\n" \
M_"a[44:47], a[236:239], a[200:203], a[44:47]\n" \
X_"a[190:191], v39 offset:1024\n" \
M_"a[40:43], a[136:139], a[168:171], a[40:43]\n" \
"s_add_u32 s89, s42, 0x2000\n" \
"buffer_store_dwordx4 v[192:195], v20, s[80:83], s89 offen offset:0 sc1\n" \
M_"a[40:43], a[200:203], a[232:235], a[40:43]\n" \
X_"a[252:253], v39 offset:8192\n" \
M_"a[56:59], a[140:143], a[168:171], a[56:59]\n" \
X_"a[254:255], v39 offset:9216\n" \
M_"a[56:59], a[204:207], a[232:235], a[56:59]\n" \
".fill (L_pb + 32480 - .) / 4, 4, 0xBF800000\n" \
"L_pp8:\n" \
"s_cbranch_execz L_pret\n" \
"s_cmp_lg_u32 s5, 0\n" \
"s_cselect_b32 s17, s11, s6\n" \
M_"a[60:63], a[172:175], a[140:143], a[60:63]\n" \
"s_lshl_b32 s6, s3, 1\n" \
"s_lshl_b32 s5, s5, 3\n" \
M_"a[60:63], a[236:239], a[204:207], a[60:63]\n" \
"s_addk_i32 s84, 0x2000\n" \
M_"a[52:55], a[164:167], a[140:143], a[52:55]\n" \
B_"v25, s[76:79], s84 offen offset:3072 lds\n" \
M_"a[52:55], a[228:231], a[204:207], a[52:55]\n" \
"s_and_b32 s6, s6, 4\n" \
"s_or_b32 s12, s5, s6\n" \
M_"a[48:51], a[160:163], a[140:143], a[48:51]\n" \
"s_sub_i32 s5, s13, s5\n" \
"s_or_b32 s5, s5, s6\n" \
M_"a[48:51], a[224:227], a[204:207], a[48:51]\n" \
"s_or_b32 s6, s12, s13\n" \
"s_lshl_b32 s4, s4, 2\n" \
"s_waitcnt lgkmcnt(15)\n" \
M_"a[64:67], a[164:167], v[248:251], a[64:67]\n" \
"s_mulk_i32 s6, 0x92\n" \
"s_add_i32 s4, s4, 18\n" \
M_"a[64:67], a[228:231], v[48:51], a[64:67]\n" \
"s_mulk_i32 s5, 0x92\n" \
"s_add_i32 s6, s4, s6\n" \
M_"a[80:83], a[172:175], v[248:251], a[80:83]\n" \
"s_add_i32 s5, s5, s4\n" \
"s_lshl_b32 s4, s3, 11\n" \
M_"a[80:83], a[236:239], v[48:51], a[80:83]\n" \
"s_lshl_b32 s15, s13, 12\n" \
"s_lshl_b32 s16, s19, 14\n" \
M_"a[68:71], v[248:251], a[168:171], a[68:71]\n" \
"s_mul_i32 s19, s19, 0xe000\n" \
"s_mov_b32 s11, 0x20000\n" \
"s_waitcnt vmcnt(10) lgkmcnt(0)\n" \
M_"a[68:71], v[48:51], a[232:235], a[68:71]\n" \
"s_barrier\n" \
M_"v[52:55], a[176:179], a[128:131], v[52:55]\n" \
"s_add_u32 m0, s40, 0x0\n" \
"s_add_u32 s85, s39, 0x10000\n" \
M_"v[52:55], a[240:243], a[192:195], v[52:55]\n" \
B_"v20, s[80:83], s85 offen sc1 lds\n" \
M_"v[56:59], a[180:183], a[128:131], v[56:59]\n" \
X_"a[160:161], v32 offset:32768\n" \
"s_or_b32 s12, s4, 0xc000\n" \
M_"v[56:59], a[244:247], a[192:195], v[56:59]\n" \
X_"a[162:163], v32 offset:33280\n" \
"s_lshl_b32 s4, s3, 8\n" \
M_"v[60:63], a[184:187], a[128:131], v[60:63]\n" \
"s_or_b32 s22, s15, s16\n" \
"s_or_b32 s23, s19, s15\n" \
M_"v[60:63], a[248:251], a[192:195], v[60:63]\n" \
X_"a[224:225], v32 offset:36864\n" \
"s_lshl_b32 s60, s6, 10\n" \
M_"v[64:67], a[188:191], a[128:131], v[64:67]\n" \
"s_lshl_b32 s61, s5, 10\n" \
"s_lshl_b32 s14, s14, 15\n" \
M_"v[64:67], a[252:255], a[192:195], v[64:67]\n" \
R_"a[128:131], v28 offset:32768\n" \
"s_or_b32 s20, s4, 0x26c00\n" \
M_"v[80:83], a[188:191], a[132:135], v[80:83]\n" \
R_"a[192:195], v29 offset:32768\n" \
"s_or_b32 s13, s22, 0x8000\n" \
M_"v[80:83], a[252:255], a[196:199], v[80:83]\n" \
"s_add_u32 m0, s40, 0x1000\n" \
"s_add_u32 s85, s39, 0x11000\n" \
M_"v[76:79], a[184:187], a[132:135], v[76:79]\n" \
B_"v20, s[80:83], s85 offen sc1 lds\n" \
M_"v[76:79], a[248:251], a[196:199], v[76:79]\n" \
X_"a[226:227], v32 offset:37376\n" \
"s_add_i32 s22, s22, 0x14000\n" \
M_"v[72:75], a[180:183], a[132:135], v[72:75]\n" \
X_"a[164:165], v33 offset:32768\n" \
"s_bitset1_b32 s23, 16\n" \
M_"v[72:75], a[244:247], a[196:199], v[72:75]\n" \
"s_or_b32 s24, s7, 0x5be8020\n" \
"s_and_b32 s57, s27, 0xffff\n" \
M_"v[68:71], a[176:179], a[132:135], v[68:71]\n" \
X_"a[166:167], v33 offset:33280\n" \
"s_mov_b32 s58, 0x6000000\n" \
M_"v[68:71], a[240:243], a[196:199], v[68:71]\n" \
R_"a[132:135], v28 offset:34816\n" \
"s_mov_b32 s56, s26\n" \
M_"v[84:87], a[176:179], a[136:139], v[84:87]\n" \
R_"a[196:199], v29 offset:34816\n" \
"s_mov_b32 s59, s11\n" \
M_"v[84:87], a[240:243], a[200:203], v[84:87]\n" \
"s_add_u32 m0, s40, 0x3000\n" \
"s_movk_i32 s84, 0x400\n" \
M_"v[88:91], a[180:183], a[136:139], v[88:91]\n" \
B_"v21, s[76:79], s84 offen offset:0 lds\n" \
M_"v[88:91], a[244:247], a[200:203], v[88:91]\n" \
X_"a[228:229], v33 offset:36864\n" \
"s_add_i32 s60, s60, 0x3000000\n" \
M_"v[92:95], a[184:187], a[136:139], v[92:95]\n" \
X_"a[230:231], v33 offset:37376\n" \
"s_add_i32 s61, s61, 0x411c000\n" \
M_"v[92:95], a[248:251], a[200:203], v[92:95]\n" \
"s_add_u32 s88, s42, 0x2000\n" \
"buffer_store_dwordx4 v[216:219], v20, s[80:83], s88 offen offset:1024 sc1\n" \
M_"v[96:99], a[188:191], a[136:139], v[96:99]\n" \
"s_sub_i32 s19, 14, s18\n" \
"s_nop 0\n" \
"s_mov_b32 s21, 0x71800000\n" \
M_"v[96:99], a[252:255], a[200:203], v[96:99]\n" \
R_"a[136:139], v28 offset:36864\n" \
"s_mov_b32 s4, L_gsw-L_gtr\n" \
M_"v[112:115], a[188:191], a[140:143], v[112:115]\n" \
R_"a[200:203], v29 offset:36864\n" \
M_"v[112:115], a[252:255], a[204:207], v[112:115]\n" \
X_"a[172:173], v35 offset:32768\n" \
M_"v[108:111], a[184:187], a[140:143], v[108:111]\n" \
X_"a[174:175], v35 offset:33280\n" \
M_"v[108:111], a[248:251], a[204:207], v[108:111]\n" \
"s_cmp_eq_u32 s19, 0\n" \
"s_cselect_b32 s4, L_gs7-L_gtr, s4\n" \
M_"v[104:107], a[180:183], a[140:143], v[104:107]\n" \
"s_add_u32 m0, s31, 0x0\n" \
"s_mov_b32 s84, 0x400000\n" \
M_"v[104:107], a[244:247], a[204:207], v[104:107]\n" \
B_"v26, s[68:71], s84 offen offset:0 lds\n" \
M_"v[100:103], a[176:179], a[140:143], v[100:103]\n" \
X_"a[236:237], v35 offset:36864\n" \
M_"v[100:103], a[240:243], a[204:207], v[100:103]\n" \
R_"a[140:143], v28 offset:38912\n" \
M_"v[116:119], a[176:179], a[144:147], v[116:119]\n" \
R_"a[204:207], v29 offset:38912\n" \
M_"v[116:119], a[240:243], a[208:211], v[116:119]\n" \
X_"a[238:239], v35 offset:37376\n" \
M_"v[120:123], a[180:183], a[144:147], v[120:123]\n" \
X_"a[168:169], v34 offset:32768\n" \
M_"v[120:123], a[244:247], a[208:211], v[120:123]\n" \
"s_addk_i32 s84, 0x7c00\n" \
M_"v[124:127], a[184:187], a[144:147], v[124:127]\n" \
B_"v26, s[68:71], s84 offen offset:1024 lds\n" \
M_"v[124:127], a[248:251], a[208:211], v[124:127]\n" \
X_"a[170:171], v34 offset:33280\n" \
M_"v[128:131], a[188:191], a[144:147], v[128:131]\n" \
"s_cmp_eq_u32 s16, 0\n" \
"s_cselect_b32 s4, 0, s4\n" \
M_"v[128:131], a[252:255], a[208:211], v[128:131]\n" \
R_"a[144:147], v28 offset:40960\n" \
M_"v[144:147], a[188:191], a[148:151], v[144:147]\n" \
R_"a[208:211], v29 offset:40960\n" \
M_"v[144:147], a[252:255], a[212:215], v[144:147]\n" \
X_"a[232:233], v34 offset:36864\n" \
M_"v[140:143], a[184:187], a[148:151], v[140:143]\n" \
"s_addk_i32 s84, 0x7c00\n" \
M_"v[140:143], a[248:251], a[212:215], v[140:143]\n" \
B_"v27, s[68:71], s84 offen offset:2048 lds\n" \
M_"v[136:139], a[180:183], a[148:151], v[136:139]\n" \
X_"a[234:235], v34 offset:37376\n" \
M_"v[136:139], a[244:247], a[212:215], v[136:139]\n" \
"s_getpc_b64 s[28:29]\n" \
"L_gp0_%=:\n" \
"s_add_u32 s28, s28, L_gtr-L_gp0_%=\n" \
"s_addc_u32 s29, s29, 0\n" \
M_"v[132:135], a[176:179], a[148:151], v[132:135]\n" \
R_"v[248:251], v22 offset:32768\n" \
M_"v[132:135], a[240:243], a[212:215], v[132:135]\n" \
R_"a[148:151], v28 offset:43008\n" \
M_"v[148:151], a[176:179], a[152:155], v[148:151]\n" \
R_"a[212:215], v29 offset:43008\n" \
M_"v[148:151], a[240:243], a[216:219], v[148:151]\n" \
"s_add_u32 m0, s31, 0x0\n" \
"s_mov_b32 s84, 0x417400\n" \
M_"v[152:155], a[180:183], a[152:155], v[152:155]\n" \
B_"v27, s[68:71], s84 offen offset:3072 lds\n" \
M_"v[152:155], a[244:247], a[216:219], v[152:155]\n" \
R_"v[48:51], v23 offset:32768\n" \
M_"v[156:159], a[184:187], a[152:155], v[156:159]\n" \
"s_add_u32 s28, s28, s4\n" \
"s_addc_u32 s29, s29, 0\n" \
"s_nop 0\n" \
M_"v[156:159], a[248:251], a[216:219], v[156:159]\n" \
"v_accvgpr_write_b32 a96, 0\n" \
"v_accvgpr_write_b32 a97, 0\n" \
M_"v[160:163], a[188:191], a[152:155], v[160:163]\n" \
"v_accvgpr_write_b32 a98, 0\n" \
"v_accvgpr_write_b32 a99, 0\n" \
M_"v[160:163], a[252:255], a[216:219], v[160:163]\n" \
R_"a[152:155], v28 offset:45056\n" \
"v_accvgpr_write_b32 a100, 0\n" \
M_"v[176:179], a[188:191], a[156:159], v[176:179]\n" \
R_"a[216:219], v29 offset:45056\n" \
"v_accvgpr_write_b32 a101, 0\n" \
M_"v[176:179], a[252:255], a[220:223], v[176:179]\n" \
"v_accvgpr_write_b32 a102, 0\n" \
"v_accvgpr_write_b32 a103, 0\n" \
M_"v[172:175], a[184:187], a[156:159], v[172:175]\n" \
"v_accvgpr_write_b32 a112, 0\n" \
"v_accvgpr_write_b32 a113, 0\n" \
M_"v[172:175], a[248:251], a[220:223], v[172:175]\n" \
"v_accvgpr_write_b32 a114, 0\n" \
"v_accvgpr_write_b32 a115, 0\n" \
M_"v[168:171], a[180:183], a[156:159], v[168:171]\n" \
"v_accvgpr_write_b32 a120, 0\n" \
"v_accvgpr_write_b32 a121, 0\n" \
M_"v[168:171], a[244:247], a[220:223], v[168:171]\n" \
"v_accvgpr_write_b32 a122, 0\n" \
"v_accvgpr_write_b32 a123, 0\n" \
M_"v[164:167], a[176:179], a[156:159], v[164:167]\n" \
"v_mov_b32_e32 v180, 0x3f803f80\n" \
"s_mov_b32 s6, 0x33333333\n" \
M_"v[164:167], a[240:243], a[220:223], v[164:167]\n" \
R_"a[156:159], v28 offset:47104\n" \
R_"a[220:223], v29 offset:47104\n" \
"s_waitcnt lgkmcnt(15)\n" \
"s_nop 0\n" \
M_"a[0:3], a[160:163], a[128:131], a[0:3]\n" \
"s_add_u32 s89, s42, 0x3000\n" \
"buffer_store_dwordx4 v[220:223], v20, s[80:83], s89 offen offset:0 sc1\n" \
M_"a[0:3], a[224:227], a[192:195], a[0:3]\n" \
X_"a[176:177], v36 offset:32768\n" \
"s_mov_b32 s7, 0xcccccccc\n" \
M_"a[4:7], a[164:167], a[128:131], a[4:7]\n" \
X_"a[178:179], v36 offset:33792\n" \
"v_cndmask_b32_e64 v181, 0, v180, s[6:7]\n" \
M_"a[4:7], a[228:231], a[192:195], a[4:7]\n" \
X_"a[240:241], v36 offset:40960\n" \
"v_accvgpr_write_b32 a104, v181\n" \
M_"a[12:15], a[172:175], a[128:131], a[12:15]\n" \
X_"a[242:243], v36 offset:41984\n" \
"v_accvgpr_write_b32 a105, v181\n" \
"s_waitcnt lgkmcnt(15)\n" \
M_"a[12:15], a[236:239], a[192:195], a[12:15]\n" \
"v_accvgpr_write_b32 a106, v181\n" \
"v_accvgpr_write_b32 a107, v181\n" \
M_"a[8:11], a[128:131], a[168:171], a[8:11]\n" \
X_"a[180:181], v37 offset:32768\n" \
"s_mov_b32 s6, 0x11111111\n" \
"s_waitcnt lgkmcnt(13)\n" \
M_"a[8:11], a[192:195], a[232:235], a[8:11]\n" \
"s_mov_b32 s7, 0x11111111\n" \
"v_cndmask_b32_e64 v181, 0, v180, s[6:7]\n" \
M_"a[24:27], a[132:135], a[168:171], a[24:27]\n" \
X_"a[182:183], v37 offset:33792\n" \
"v_accvgpr_write_b32 a108, v181\n" \
M_"a[24:27], a[196:199], a[232:235], a[24:27]\n" \
X_"a[244:245], v37 offset:40960\n" \
"v_accvgpr_write_b32 a109, v181\n" \
M_"a[28:31], a[172:175], a[132:135], a[28:31]\n" \
"v_accvgpr_write_b32 a110, v181\n" \
"v_accvgpr_write_b32 a111, v181\n" \
M_"a[28:31], a[236:239], a[196:199], a[28:31]\n" \
X_"a[246:247], v37 offset:41984\n" \
"s_mov_b32 s6, 0x44444444\n" \
M_"a[20:23], a[164:167], a[132:135], a[20:23]\n" \
"s_mov_b32 s7, 0x44444444\n" \
"v_cndmask_b32_e64 v181, 0, v180, s[6:7]\n" \
M_"a[20:23], a[228:231], a[196:199], a[20:23]\n" \
X_"a[184:185], v38 offset:32768\n" \
"v_accvgpr_write_b32 a116, v181\n" \
M_"a[16:19], a[160:163], a[132:135], a[16:19]\n" \
X_"a[186:187], v38 offset:33792\n" \
"v_accvgpr_write_b32 a117, v181\n" \
M_"a[16:19], a[224:227], a[196:199], a[16:19]\n" \
"v_accvgpr_write_b32 a118, v181\n" \
"v_accvgpr_write_b32 a119, v181\n" \
M_"a[32:35], a[160:163], a[136:139], a[32:35]\n" \
"s_add_u32 s88, s42, 0x3000\n" \
"buffer_store_dwordx4 v[224:227], v20, s[80:83], s88 offen offset:1024 sc1\n" \
M_"a[32:35], a[224:227], a[200:203], a[32:35]\n" \
X_"a[248:249], v38 offset:40960\n" \
"v_bfe_i32 v181, v20, 4, 1\n" \
M_"a[36:39], a[164:167], a[136:139], a[36:39]\n" \
"v_lshlrev_b32_e32 v45, 3, v20\n" \
"v_add_u32_e32 v45, s30, v45\n" \
M_"a[36:39], a[228:231], a[200:203], a[36:39]\n" \
"s_getpc_b64 s[86:87]\n" \
"L_ct0_%=:\n" \
"s_add_u32 s86, s86, L_ap0-L_ct0_%=\n" \
"s_addc_u32 s87, s87, 0\n" \
"s_add_u32 m0, s40, 0x2000\n" \
M_"a[44:47], a[172:175], a[136:139], a[44:47]\n" \
"global_load_lds_dword v45, s[86:87]\n" \
M_"a[44:47], a[236:239], a[200:203], a[44:47]\n" \
X_"a[250:251], v38 offset:41984\n" \
"v_and_b32_e32 v181, 0xaaaaaaaa, v181\n" \
M_"a[40:43], a[136:139], a[168:171], a[40:43]\n" \
X_"a[188:189], v39 offset:32768\n" \
"v_or_b32_e32 v181, 0x44444444, v181\n" \
M_"a[40:43], a[200:203], a[232:235], a[40:43]\n" \
X_"a[190:191], v39 offset:33792\n" \
"v_accvgpr_write_b32 a126, v181\n" \
M_"a[56:59], a[140:143], a[168:171], a[56:59]\n" \
X_"a[252:253], v39 offset:40960\n" \
"v_bfe_u32 v180, v20, 5, 3\n" \
M_"a[56:59], a[204:207], a[232:235], a[56:59]\n" \
X_"a[254:255], v39 offset:41984\n" \
"v_bfe_u32 v181, v20, 8, 2\n" \
M_"a[60:63], a[172:175], a[140:143], a[60:63]\n" \
"v_lshlrev_b32_e32 v181, 1, v181\n" \
"v_sub_u32_e32 v180, v180, v181\n" \
M_"a[60:63], a[236:239], a[204:207], a[60:63]\n" \
"v_and_b32_e64 v181, 16, v20\n" \
"v_mov_b32_e32 v182, 0x3f80\n" \
M_"a[52:55], a[164:167], a[140:143], a[52:55]\n" \
"v_lshlrev_b32_e32 v181, v181, v182\n" \
"v_cmp_eq_u32_e32 vcc, 0, v180\n" \
M_"a[52:55], a[228:231], a[204:207], a[52:55]\n" \
"v_cmp_eq_u32_e64 s[6:7], 1, v180\n" \
"v_cndmask_b32_e64 v182, 0, v181, vcc\n" \
M_"a[48:51], a[160:163], a[140:143], a[48:51]\n" \
"v_cndmask_b32_e64 v183, 0, v181, s[6:7]\n" \
"v_accvgpr_write_b32 a124, v182\n" \
M_"a[48:51], a[224:227], a[204:207], a[48:51]\n" \
"v_accvgpr_write_b32 a125, v183\n" \
"s_waitcnt lgkmcnt(15)\n" \
M_"a[64:67], a[164:167], v[248:251], a[64:67]\n" \
M_"a[64:67], a[228:231], v[48:51], a[64:67]\n" \
M_"a[80:83], a[172:175], v[248:251], a[80:83]\n" \
M_"a[80:83], a[236:239], v[48:51], a[80:83]\n" \
M_"a[68:71], v[248:251], a[168:171], a[68:71]\n" \
"s_waitcnt vmcnt(3) lgkmcnt(0)\n" \
M_"a[68:71], v[48:51], a[232:235], a[68:71]\n" \
"s_barrier\n" \
"v_xor_b32_e32 v28, 0x20000, v28\n" \
"v_xor_b32_e32 v29, 0x20000, v29\n" \
M_"v[52:55], a[176:179], a[128:131], v[52:55]\n" \
X_"a[160:161], v32 offset:0\n" \
"v_accvgpr_read_b32 v184, a0\n" \
M_"v[52:55], a[240:243], a[192:195], v[52:55]\n" \
"v_accvgpr_read_b32 v185, a1\n" \
"v_accvgpr_read_b32 v186, a2\n" \
M_"v[56:59], a[180:183], a[128:131], v[56:59]\n" \
X_"a[162:163], v32 offset:512\n" \
"v_accvgpr_read_b32 v187, a3\n" \
M_"v[56:59], a[244:247], a[192:195], v[56:59]\n" \
"v_accvgpr_read_b32 v188, a4\n" \
"v_accvgpr_read_b32 v189, a5\n" \
M_"v[60:63], a[184:187], a[128:131], v[60:63]\n" \
"v_accvgpr_read_b32 v190, a6\n" \
"v_accvgpr_read_b32 v191, a7\n" \
M_"v[60:63], a[248:251], a[192:195], v[60:63]\n" \
X_"a[224:225], v32 offset:4096\n" \
"v_accvgpr_read_b32 v196, a12\n" \
M_"v[64:67], a[188:191], a[128:131], v[64:67]\n" \
"v_accvgpr_read_b32 v197, a13\n" \
"v_accvgpr_read_b32 v198, a14\n" \
M_"v[64:67], a[252:255], a[192:195], v[64:67]\n" \
R_"a[128:131], v28 offset:0\n" \
"v_accvgpr_read_b32 v199, a15\n" \
M_"v[80:83], a[188:191], a[132:135], v[80:83]\n" \
R_"a[192:195], v29 offset:0\n" \
"v_accvgpr_read_b32 v192, a8\n" \
M_"v[80:83], a[252:255], a[196:199], v[80:83]\n" \
X_"a[226:227], v32 offset:4608\n" \
"v_accvgpr_read_b32 v193, a9\n" \
M_"v[76:79], a[184:187], a[132:135], v[76:79]\n" \
"v_accvgpr_read_b32 v194, a10\n" \
"v_accvgpr_read_b32 v195, a11\n" \
M_"v[76:79], a[248:251], a[196:199], v[76:79]\n" \
X_"a[164:165], v33 offset:0\n" \
"v_accvgpr_read_b32 v208, a24\n" \
M_"v[72:75], a[180:183], a[132:135], v[72:75]\n" \
"v_accvgpr_read_b32 v209, a25\n" \
"v_accvgpr_read_b32 v210, a26\n" \
M_"v[72:75], a[244:247], a[196:199], v[72:75]\n" \
"v_accvgpr_read_b32 v211, a27\n" \
"v_accvgpr_read_b32 v212, a28\n" \
M_"v[68:71], a[176:179], a[132:135], v[68:71]\n" \
X_"a[166:167], v33 offset:512\n" \
"v_accvgpr_read_b32 v213, a29\n" \
M_"v[68:71], a[240:243], a[196:199], v[68:71]\n" \
R_"a[132:135], v28 offset:2048\n" \
"v_accvgpr_read_b32 v214, a30\n" \
M_"v[84:87], a[176:179], a[136:139], v[84:87]\n" \
R_"a[196:199], v29 offset:2048\n" \
"v_accvgpr_read_b32 v215, a31\n" \
M_"v[84:87], a[240:243], a[200:203], v[84:87]\n" \
X_"a[228:229], v33 offset:4096\n" \
"v_accvgpr_read_b32 v204, a20\n" \
M_"v[88:91], a[180:183], a[136:139], v[88:91]\n" \
"v_accvgpr_read_b32 v205, a21\n" \
"v_accvgpr_read_b32 v206, a22\n" \
M_"v[88:91], a[244:247], a[200:203], v[88:91]\n" \
"v_accvgpr_read_b32 v207, a23\n" \
"v_accvgpr_read_b32 v200, a16\n" \
M_"v[92:95], a[184:187], a[136:139], v[92:95]\n" \
X_"a[230:231], v33 offset:4608\n" \
"v_accvgpr_read_b32 v201, a17\n" \
M_"v[92:95], a[248:251], a[200:203], v[92:95]\n" \
"v_accvgpr_read_b32 v202, a18\n" \
"v_accvgpr_read_b32 v203, a19\n" \
M_"v[96:99], a[188:191], a[136:139], v[96:99]\n" \
"v_accvgpr_read_b32 v216, a32\n" \
"v_accvgpr_read_b32 v217, a33\n" \
M_"v[96:99], a[252:255], a[200:203], v[96:99]\n" \
R_"a[136:139], v28 offset:4096\n" \
"v_accvgpr_read_b32 v218, a34\n" \
M_"v[112:115], a[188:191], a[140:143], v[112:115]\n" \
R_"a[200:203], v29 offset:4096\n" \
"v_accvgpr_read_b32 v219, a35\n" \
M_"v[112:115], a[252:255], a[204:207], v[112:115]\n" \
X_"a[172:173], v35 offset:0\n" \
"v_accvgpr_read_b32 v220, a36\n" \
M_"v[108:111], a[184:187], a[140:143], v[108:111]\n" \
X_"a[174:175], v35 offset:512\n" \
"v_accvgpr_read_b32 v221, a37\n" \
M_"v[108:111], a[248:251], a[204:207], v[108:111]\n" \
"v_accvgpr_read_b32 v222, a38\n" \
"v_accvgpr_read_b32 v223, a39\n" \
M_"v[104:107], a[180:183], a[140:143], v[104:107]\n" \
X_"a[236:237], v35 offset:4096\n" \
"v_accvgpr_read_b32 v228, a44\n" \
M_"v[104:107], a[244:247], a[204:207], v[104:107]\n" \
"v_accvgpr_read_b32 v229, a45\n" \
"v_accvgpr_read_b32 v230, a46\n" \
M_"v[100:103], a[176:179], a[140:143], v[100:103]\n" \
"v_accvgpr_read_b32 v231, a47\n" \
"v_accvgpr_read_b32 v224, a40\n" \
M_"v[100:103], a[240:243], a[204:207], v[100:103]\n" \
R_"a[140:143], v28 offset:6144\n" \
"v_accvgpr_read_b32 v225, a41\n" \
M_"v[116:119], a[176:179], a[144:147], v[116:119]\n" \
R_"a[204:207], v29 offset:6144\n" \
"v_accvgpr_read_b32 v226, a42\n" \
M_"v[116:119], a[240:243], a[208:211], v[116:119]\n" \
X_"a[238:239], v35 offset:4608\n" \
"v_accvgpr_read_b32 v227, a43\n" \
M_"v[120:123], a[180:183], a[144:147], v[120:123]\n" \
X_"a[168:169], v34 offset:0\n" \
"v_accvgpr_read_b32 v240, a56\n" \
M_"v[120:123], a[244:247], a[208:211], v[120:123]\n" \
"v_accvgpr_read_b32 v241, a57\n" \
"v_accvgpr_read_b32 v242, a58\n" \
M_"v[124:127], a[184:187], a[144:147], v[124:127]\n" \
"v_accvgpr_read_b32 v243, a59\n" \
"v_accvgpr_read_b32 v244, a60\n" \
M_"v[124:127], a[248:251], a[208:211], v[124:127]\n" \
X_"a[170:171], v34 offset:512\n" \
"v_accvgpr_read_b32 v245, a61\n" \
M_"v[128:131], a[188:191], a[144:147], v[128:131]\n" \
"v_accvgpr_read_b32 v246, a62\n" \
"v_accvgpr_read_b32 v247, a63\n" \
M_"v[128:131], a[252:255], a[208:211], v[128:131]\n" \
R_"a[144:147], v28 offset:8192\n" \
"v_accvgpr_read_b32 v236, a52\n" \
M_"v[144:147], a[188:191], a[148:151], v[144:147]\n" \
R_"a[208:211], v29 offset:8192\n" \
"v_accvgpr_read_b32 v237, a53\n" \
M_"v[144:147], a[252:255], a[212:215], v[144:147]\n" \
X_"a[232:233], v34 offset:4096\n" \
"v_accvgpr_read_b32 v238, a54\n" \
M_"v[140:143], a[184:187], a[148:151], v[140:143]\n" \
"v_accvgpr_read_b32 v239, a55\n" \
"v_accvgpr_read_b32 v232, a48\n" \
M_"v[140:143], a[248:251], a[212:215], v[140:143]\n" \
X_"a[234:235], v34 offset:4608\n" \
"v_accvgpr_read_b32 v233, a49\n" \
M_"v[136:139], a[180:183], a[148:151], v[136:139]\n" \
"v_accvgpr_read_b32 v234, a50\n" \
"v_accvgpr_read_b32 v235, a51\n" \
M_"v[136:139], a[244:247], a[212:215], v[136:139]\n" \
"v_accvgpr_read_b32 v180, a64\n" \
"v_accvgpr_read_b32 v181, a65\n" \
M_"v[132:135], a[176:179], a[148:151], v[132:135]\n" \
R_"v[248:251], v22 offset:0\n" \
"v_accvgpr_read_b32 v182, a66\n" \
M_"v[132:135], a[240:243], a[212:215], v[132:135]\n" \
R_"a[148:151], v28 offset:10240\n" \
"v_accvgpr_read_b32 v183, a67\n" \
M_"v[148:151], a[176:179], a[152:155], v[148:151]\n" \
R_"a[212:215], v29 offset:10240\n" \
"v_accvgpr_read_b32 v252, a68\n" \
M_"v[148:151], a[240:243], a[216:219], v[148:151]\n" \
R_"v[48:51], v23 offset:0\n" \
"v_accvgpr_read_b32 v253, a69\n" \
M_"v[152:155], a[180:183], a[152:155], v[152:155]\n" \
"v_accvgpr_read_b32 v254, a70\n" \
"v_accvgpr_read_b32 v255, a71\n" \
M_"v[152:155], a[244:247], a[216:219], v[152:155]\n" \
"v_lshrrev_b32_e32 v24, 9, v20\n" \
"v_and_b32_e32 v26, 0x1ff, v20\n" \
M_"v[156:159], a[184:187], a[152:155], v[156:159]\n" \
"v_lshl_add_u32 v24, v24, 3, v26\n" \
"v_lshrrev_b32_e64 v25, 8, v20\n" \
M_"v[156:159], a[248:251], a[216:219], v[156:159]\n" \
"v_mul_u32_u24_e32 v25, 0x24700, v25\n" \
"v_add_u32_e64 v25, v25, v20\n" \
M_"v[160:163], a[188:191], a[152:155], v[160:163]\n" \
"v_lshrrev_b32_e32 v26, 2, v20\n" \
"v_add_u32_e32 v26, s45, v26\n" \
M_"v[160:163], a[252:255], a[216:219], v[160:163]\n" \
R_"a[152:155], v28 offset:12288\n" \
"v_add_u32_e64 v27, s46, v20\n" \
M_"v[176:179], a[188:191], a[156:159], v[176:179]\n" \
R_"a[216:219], v29 offset:12288\n" \
"v_add_u32_e64 v21, s43, v27\n" \
M_"v[176:179], a[252:255], a[220:223], v[176:179]\n" \
M_"v[172:175], a[184:187], a[156:159], v[172:175]\n" \
M_"v[172:175], a[248:251], a[220:223], v[172:175]\n" \
M_"v[168:171], a[180:183], a[156:159], v[168:171]\n" \
M_"v[168:171], a[244:247], a[220:223], v[168:171]\n" \
M_"v[164:167], a[176:179], a[156:159], v[164:167]\n" \
M_"v[164:167], a[240:243], a[220:223], v[164:167]\n" \
R_"a[156:159], v28 offset:14336\n" \
R_"a[220:223], v29 offset:14336\n" \
"s_waitcnt lgkmcnt(15)\n" \
"s_nop 0\n" \
M_"v[184:187], a[160:163], a[128:131], v[184:187]\n" \
"v_bfe_u32 v44, v20, 2, 5\n" \
X_"a[176:177], v36 offset:0\n" \
M_"v[184:187], a[224:227], a[192:195], v[184:187]\n" \
"buffer_load_dword v44, v44, s[80:83], s38 offen offset:0 sc1\n" \
M_"v[188:191], a[164:167], a[128:131], v[188:191]\n" \
X_"a[178:179], v36 offset:1024\n" \
M_"v[188:191], a[228:231], a[192:195], v[188:191]\n" \
X_"a[240:241], v36 offset:8192\n" \
M_"v[196:199], a[172:175], a[128:131], v[196:199]\n" \
X_"a[242:243], v36 offset:9216\n" \
"s_waitcnt lgkmcnt(15)\n" \
M_"v[196:199], a[236:239], a[192:195], v[196:199]\n" \
M_"v[192:195], a[128:131], a[168:171], v[192:195]\n" \
X_"a[180:181], v37 offset:0\n" \
"s_waitcnt lgkmcnt(13)\n" \
M_"v[192:195], a[192:195], a[232:235], v[192:195]\n" \
M_"v[208:211], a[132:135], a[168:171], v[208:211]\n" \
X_"a[182:183], v37 offset:1024\n" \
M_"v[208:211], a[196:199], a[232:235], v[208:211]\n" \
X_"a[244:245], v37 offset:8192\n" \
M_"v[212:215], a[172:175], a[132:135], v[212:215]\n" \
M_"v[212:215], a[236:239], a[196:199], v[212:215]\n" \
X_"a[246:247], v37 offset:9216\n" \
C_"v188, v188, v189\n" \
M_"v[204:207], a[164:167], a[132:135], v[204:207]\n" \
C_"v189, v190, v191\n" \
C_"v190, v198, v199\n" \
M_"v[204:207], a[228:231], a[196:199], v[204:207]\n" \
X_"a[184:185], v38 offset:0\n" \
"v_mov_b32_e32 v191, 0x3f80\n" \
M_"v[200:203], a[160:163], a[132:135], v[200:203]\n" \
X_"a[186:187], v38 offset:1024\n" \
C_"v184, v184, v185\n" \
M_"v[200:203], a[224:227], a[196:199], v[200:203]\n" \
C_"v185, v186, v187\n" \
C_"v186, v196, v197\n" \
M_"v[216:219], a[160:163], a[136:139], v[216:219]\n" \
X_"a[248:249], v38 offset:8192\n" \
"v_mov_b32_e64 v187, 0\n" \
M_"v[216:219], a[224:227], a[200:203], v[216:219]\n" \
X_"a[250:251], v38 offset:9216\n" \
M_"v[220:223], a[164:167], a[136:139], v[220:223]\n" \
M_"v[220:223], a[228:231], a[200:203], v[220:223]\n" \
X_"a[188:189], v39 offset:0\n" \
C_"v204, v204, v205\n" \
M_"v[228:231], a[172:175], a[136:139], v[228:231]\n" \
".fill (L_pb + 36576 - .) / 4, 4, 0xBF800000\n" \
"L_pp9:\n" \
"s_cbranch_execz L_pret\n" \
"s_nop 0\n" \
C_"v205, v206, v207\n" \
C_"v206, v214, v215\n" \
M_"v[228:231], a[236:239], a[200:203], v[228:231]\n" \
X_"a[190:191], v39 offset:1024\n" \
"v_mov_b32_e32 v207, 0x3f80\n" \
M_"v[224:227], a[136:139], a[168:171], v[224:227]\n" \
X_"a[252:253], v39 offset:8192\n" \
C_"v200, v200, v201\n" \
M_"v[224:227], a[200:203], a[232:235], v[224:227]\n" \
C_"v201, v202, v203\n" \
C_"v202, v212, v213\n" \
M_"v[240:243], a[140:143], a[168:171], v[240:243]\n" \
X_"a[254:255], v39 offset:9216\n" \
"v_mov_b32_e64 v203, 0\n" \
M_"v[240:243], a[204:207], a[232:235], v[240:243]\n" \
M_"v[244:247], a[172:175], a[140:143], v[244:247]\n" \
M_"v[244:247], a[236:239], a[204:207], v[244:247]\n" \
C_"v220, v220, v221\n" \
C_"v221, v222, v223\n" \
M_"v[236:239], a[164:167], a[140:143], v[236:239]\n" \
C_"v222, v230, v231\n" \
"v_mov_b32_e32 v223, 0x3f80\n" \
M_"v[236:239], a[228:231], a[204:207], v[236:239]\n" \
C_"v216, v216, v217\n" \
C_"v217, v218, v219\n" \
M_"v[232:235], a[160:163], a[140:143], v[232:235]\n" \
C_"v218, v228, v229\n" \
"v_mov_b32_e64 v219, 0\n" \
M_"v[232:235], a[224:227], a[204:207], v[232:235]\n" \
"s_waitcnt lgkmcnt(15)\n" \
"s_nop 0\n" \
M_"v[180:183], a[164:167], v[248:251], v[180:183]\n" \
M_"v[180:183], a[228:231], v[48:51], v[180:183]\n" \
M_"a[80:83], a[172:175], v[248:251], a[80:83]\n" \
M_"a[80:83], a[236:239], v[48:51], a[80:83]\n" \
C_"v236, v236, v237\n" \
C_"v237, v238, v239\n" \
M_"v[252:255], v[248:251], a[168:171], v[252:255]\n" \
C_"v238, v246, v247\n" \
"v_mov_b32_e32 v239, 0x3f80\n" \
M_"v[252:255], v[48:51], a[232:235], v[252:255]\n" \
C_"v232, v232, v233\n" \
C_"v233, v234, v235\n" \
"s_waitcnt vmcnt(0) lgkmcnt(0)\n" \
"s_barrier\n" \
"v_mov_b32_e32 v45, s62\n" \
"s_mov_b64 exec, 1\n" \
"buffer_store_dword v45, off, s[80:83], s37 offset:32 sc1\n" \
"s_mov_b64 exec, -1\n" \
"v_cmp_ne_u32_e32 vcc, s62, v44\n" \
"s_nop 4\n" \
"s_cbranch_vccnz L_poll160_%=\n" \
"L_pollr160_%=:\n" \
M_"v[52:55], a[176:179], a[128:131], v[52:55]\n" \
"s_mov_b32 s73, s50\n" \
"s_sub_u32 m0, s31, 0x10000\n" \
M_"v[52:55], a[240:243], a[192:195], v[52:55]\n" \
B_"v20, s[80:83], s73 offen offset:0 sc1 lds\n" \
M_"v[56:59], a[180:183], a[128:131], v[56:59]\n" \
"s_add_u32 s74, s50, 0x4000\n" \
"s_add_u32 s75, s50, 0x8000\n" \
M_"v[56:59], a[244:247], a[192:195], v[56:59]\n" \
"s_mov_b32 s70, 0x3e0293ee\n" \
"s_mov_b32 s76, s52\n" \
M_"v[60:63], a[184:187], a[128:131], v[60:63]\n" \
"v_mul_f32_e32 v52, s70, v52\n" \
"v_mul_f32_e32 v53, s70, v53\n" \
M_"v[60:63], a[248:251], a[192:195], v[60:63]\n" \
B_"v20, s[80:83], s73 offen offset:1024 sc1 lds\n" \
M_"v[64:67], a[188:191], a[128:131], v[64:67]\n" \
"v_mul_f32_e32 v54, s70, v54\n" \
"v_mul_f32_e32 v55, s70, v55\n" \
M_"v[64:67], a[252:255], a[192:195], v[64:67]\n" \
C_"v52, v52, v53\n" \
"v_mul_f32_e64 v56, s70, v56\n" \
M_"v[80:83], a[188:191], a[132:135], v[80:83]\n" \
"v_mul_f32_e32 v60, s70, v60\n" \
"v_mul_f32_e32 v61, s70, v61\n" \
M_"v[80:83], a[252:255], a[196:199], v[80:83]\n" \
B_"v20, s[80:83], s73 offen offset:2048 sc1 lds\n" \
M_"v[76:79], a[184:187], a[132:135], v[76:79]\n" \
"v_mul_f32_e32 v62, s70, v62\n" \
"v_mul_f32_e32 v63, s70, v63\n" \
M_"v[76:79], a[248:251], a[196:199], v[76:79]\n" \
C_"v60, v60, v61\n" \
"v_mul_f32_e64 v57, s70, v57\n" \
M_"v[72:75], a[180:183], a[132:135], v[72:75]\n" \
"v_mul_f32_e32 v58, s70, v58\n" \
"v_mul_f32_e32 v59, s70, v59\n" \
M_"v[72:75], a[244:247], a[196:199], v[72:75]\n" \
B_"v20, s[80:83], s73 offen offset:3072 sc1 lds\n" \
M_"v[68:71], a[176:179], a[132:135], v[68:71]\n" \
"s_sub_u32 m0, s31, s51\n" \
C_"v53, v54, v55\n" \
M_"v[68:71], a[240:243], a[196:199], v[68:71]\n" \
"v_mul_f32_e32 v64, s70, v64\n" \
"v_mul_f32_e64 v65, s70, v65\n" \
M_"v[84:87], a[176:179], a[136:139], v[84:87]\n" \
"v_mul_f32_e32 v66, s70, v66\n" \
"v_mul_f32_e32 v67, s70, v67\n" \
M_"v[84:87], a[240:243], a[200:203], v[84:87]\n" \
B_"v20, s[80:83], s74 offen offset:0 sc1 lds\n" \
M_"v[88:91], a[180:183], a[136:139], v[88:91]\n" \
C_"v61, v62, v63\n" \
C_"v54, v56, v57\n" \
M_"v[88:91], a[244:247], a[200:203], v[88:91]\n" \
C_"v55, v58, v59\n" \
C_"v62, v64, v65\n" \
M_"v[92:95], a[184:187], a[136:139], v[92:95]\n" \
C_"v63, v66, v67\n" \
"v_mul_f32_e64 v68, s70, v68\n" \
M_"v[92:95], a[248:251], a[200:203], v[92:95]\n" \
B_"v20, s[80:83], s74 offen offset:1024 sc1 lds\n" \
M_"v[96:99], a[188:191], a[136:139], v[96:99]\n" \
"ds_write_b128 v27, v[52:55] offset:0\n" \
M_"v[96:99], a[252:255], a[200:203], v[96:99]\n" \
"v_mul_f32_e32 v69, s70, v69\n" \
"v_mul_f32_e32 v76, s70, v76\n" \
M_"v[112:115], a[188:191], a[140:143], v[112:115]\n" \
"v_mul_f32_e32 v77, s70, v77\n" \
"v_mul_f32_e32 v70, s70, v70\n" \
M_"v[112:115], a[252:255], a[204:207], v[112:115]\n" \
B_"v20, s[80:83], s74 offen offset:2048 sc1 lds\n" \
M_"v[108:111], a[184:187], a[140:143], v[108:111]\n" \
"ds_write_b128 v27, v[60:63] offset:1024\n" \
M_"v[108:111], a[248:251], a[204:207], v[108:111]\n" \
"v_mul_f32_e64 v71, s70, v71\n" \
C_"v68, v68, v69\n" \
M_"v[104:107], a[180:183], a[140:143], v[104:107]\n" \
"v_mul_f32_e32 v78, s70, v78\n" \
"v_mul_f32_e32 v79, s70, v79\n" \
M_"v[104:107], a[244:247], a[204:207], v[104:107]\n" \
B_"v20, s[80:83], s74 offen offset:3072 sc1 lds\n" \
M_"v[100:103], a[176:179], a[140:143], v[100:103]\n" \
"s_sub_u32 m0, s31, 0x4000\n" \
C_"v76, v76, v77\n" \
M_"v[100:103], a[240:243], a[204:207], v[100:103]\n" \
"v_mul_f32_e32 v72, s70, v72\n" \
"v_mul_f32_e32 v73, s70, v73\n" \
M_"v[116:119], a[176:179], a[144:147], v[116:119]\n" \
"v_mul_f32_e32 v74, s70, v74\n" \
"v_mul_f32_e32 v75, s70, v75\n" \
M_"v[116:119], a[240:243], a[208:211], v[116:119]\n" \
B_"v20, s[80:83], s75 offen offset:0 sc1 lds\n" \
M_"v[120:123], a[180:183], a[144:147], v[120:123]\n" \
C_"v69, v70, v71\n" \
"v_mul_f32_e64 v80, s70, v80\n" \
M_"v[120:123], a[244:247], a[208:211], v[120:123]\n" \
"v_mul_f32_e32 v81, s70, v81\n" \
"v_mul_f32_e32 v82, s70, v82\n" \
M_"v[124:127], a[184:187], a[144:147], v[124:127]\n" \
"v_mul_f32_e64 v83, s70, v83\n" \
C_"v77, v78, v79\n" \
M_"v[124:127], a[248:251], a[208:211], v[124:127]\n" \
B_"v20, s[80:83], s75 offen offset:1024 sc1 lds\n" \
M_"v[128:131], a[188:191], a[144:147], v[128:131]\n" \
C_"v70, v72, v73\n" \
C_"v71, v74, v75\n" \
M_"v[128:131], a[252:255], a[208:211], v[128:131]\n" \
C_"v78, v80, v81\n" \
C_"v79, v82, v83\n" \
M_"v[144:147], a[188:191], a[148:151], v[144:147]\n" \
"ds_write_b128 v27, v[68:71] offset:4096\n" \
M_"v[144:147], a[252:255], a[212:215], v[144:147]\n" \
B_"v20, s[80:83], s75 offen offset:2048 sc1 lds\n" \
M_"v[140:143], a[184:187], a[148:151], v[140:143]\n" \
C_"v180, v180, v181\n" \
"v_accvgpr_read_b32 v50, a82\n" \
M_"v[140:143], a[248:251], a[212:215], v[140:143]\n" \
"ds_write_b128 v27, v[76:79] offset:5120\n" \
M_"v[136:139], a[180:183], a[148:151], v[136:139]\n" \
"v_accvgpr_read_b32 v51, a83\n" \
C_"v181, v182, v183\n" \
M_"v[136:139], a[244:247], a[212:215], v[136:139]\n" \
B_"v20, s[80:83], s75 offen offset:3072 sc1 lds\n" \
M_"v[132:135], a[176:179], a[148:151], v[132:135]\n" \
"s_mov_b32 m0, s53\n" \
C_"v182, v50, v51\n" \
M_"v[132:135], a[240:243], a[212:215], v[132:135]\n" \
"v_mov_b32_e32 v183, 0x3f80\n" \
C_"v234, v244, v245\n" \
M_"v[148:151], a[176:179], a[152:155], v[148:151]\n" \
"v_mov_b32_e32 v235, 0\n" \
C_"v54, v192, v193\n" \
M_"v[148:151], a[240:243], a[216:219], v[148:151]\n" \
B_"v20, s[80:83], s76 offen offset:0 sc1 lds\n" \
M_"v[152:155], a[180:183], a[152:155], v[152:155]\n" \
C_"v55, v194, v195\n" \
C_"v56, v208, v209\n" \
M_"v[152:155], a[244:247], a[216:219], v[152:155]\n" \
C_"v57, v210, v211\n" \
C_"v58, v224, v225\n" \
C_"v59, v226, v227\n" \
C_"v60, v240, v241\n" \
C_"v61, v242, v243\n" \
M_"v[32:35], v[180:183], v[184:187], 0\n" \
B_"v20, s[80:83], s76 offen offset:1024 sc1 lds\n" \
M_"v[156:159], a[184:187], a[152:155], v[156:159]\n" \
M_"v[192:195], v[188:191], v[184:187], 0\n" \
M_"v[208:211], v[204:207], v[200:203], 0\n" \
M_"v[36:39], v[188:191], v[200:203], 0\n" \
M_"v[224:227], v[220:223], v[216:219], 0\n" \
M_"v[248:251], v[204:207], v[216:219], 0\n" \
M_"v[240:243], v[236:239], v[232:235], 0\n" \
M_"v[64:67], v[220:223], v[232:235], 0\n" \
"s_mov_b32 s64, 0xaaaaaaaa\n" \
"s_mov_b32 s65, 0xaaaaaaaa\n" \
M_"v[156:159], a[248:251], a[216:219], v[156:159]\n" \
"v_cndmask_b32_e64 v32, v32, v33, s[64:65]\n" \
"v_cndmask_b32_e64 v34, v34, v35, s[64:65]\n" \
M_"v[160:163], a[188:191], a[152:155], v[160:163]\n" \
"s_mov_b32 s66, 0xfff0ffff\n" \
"s_mov_b32 s67, 0xf000ff00\n" \
"s_mov_b32 s68, 0xffc0fffc\n" \
"s_mov_b32 s69, 0xc000fc00\n" \
"v_cndmask_b32_e64 v192, v192, v193, s[64:65]\n" \
"v_add_f32_e64 v32, s47, v32\n" \
"v_cndmask_b32_e64 v194, v194, v195, s[64:65]\n" \
"v_add_f32_e64 v34, s47, v34\n" \
"v_cndmask_b32_e64 v208, v208, v209, s[64:65]\n" \
"v_cndmask_b32_e64 v36, v36, v37, s[64:65]\n" \
"v_cndmask_b32_e64 v210, v210, v211, s[64:65]\n" \
"v_cndmask_b32_e64 v38, v38, v39, s[64:65]\n" \
"v_cndmask_b32_e64 v224, v224, v225, s[64:65]\n" \
"v_cndmask_b32_e64 v248, v248, v249, s[64:65]\n" \
"v_cndmask_b32_e64 v226, v226, v227, s[64:65]\n" \
"v_cndmask_b32_e64 v250, v250, v251, s[64:65]\n" \
"v_cndmask_b32_e64 v240, v240, v241, s[64:65]\n" \
"v_cndmask_b32_e64 v64, v64, v65, s[64:65]\n" \
"v_cndmask_b32_e64 v242, v242, v243, s[64:65]\n" \
"v_cndmask_b32_e64 v66, v66, v67, s[64:65]\n" \
"v_cndmask_b32_e64 v192, v32, v192, s[66:67]\n" \
"v_cndmask_b32_e64 v194, v34, v194, s[68:69]\n" \
"v_cndmask_b32_e64 v208, v36, v208, s[66:67]\n" \
"v_cndmask_b32_e64 v210, v38, v210, s[68:69]\n" \
"v_cndmask_b32_e64 v224, v248, v224, s[66:67]\n" \
"v_cndmask_b32_e64 v226, v250, v226, s[68:69]\n" \
"v_cndmask_b32_e64 v240, v64, v240, s[66:67]\n" \
"v_cndmask_b32_e64 v242, v66, v242, s[68:69]\n" \
M_"v[160:163], a[252:255], a[216:219], v[160:163]\n" \
"v_max_f32_e32 v193, v192, v194\n" \
"v_max_f32_e32 v209, v208, v210\n" \
"v_max_f32_e32 v225, v224, v226\n" \
"v_max_f32_e32 v241, v240, v242\n" \
"v_mul_f32_e32 v84, s70, v84\n" \
"v_permlane32_swap_b32_e32 v193, v225\n" \
"v_permlane32_swap_b32_e32 v209, v241\n" \
"v_mul_f32_e32 v85, s70, v85\n" \
"v_mul_f32_e32 v92, s70, v92\n" \
"v_mul_f32_e32 v93, s70, v93\n" \
"v_permlane32_swap_b32_e32 v196, v228\n" \
"v_permlane32_swap_b32_e32 v212, v244\n" \
"v_max_f32_e32 v193, v193, v225\n" \
"v_max_f32_e32 v209, v209, v241\n" \
"v_accvgpr_read_b32 v48, a80\n" \
"v_accvgpr_read_b32 v49, a81\n" \
"v_mul_f32_e32 v86, s70, v86\n" \
"v_mul_f32_e32 v87, s70, v87\n" \
C_"v84, v84, v85\n" \
"v_mul_f32_e32 v94, s70, v94\n" \
"v_mul_f32_e32 v95, s70, v95\n" \
C_"v92, v92, v93\n" \
"v_permlane16_swap_b32_e32 v228, v244\n" \
"v_permlane16_swap_b32_e32 v193, v209\n" \
M_"v[176:179], a[188:191], a[156:159], v[176:179]\n" \
"s_mov_b32 s72, 0x3eae1a93\n" \
"v_mul_f32_e32 v88, s70, v88\n" \
"v_mul_f32_e32 v89, s70, v89\n" \
"v_mul_f32_e32 v90, s70, v90\n" \
"v_mul_f32_e32 v91, s70, v91\n" \
C_"v85, v86, v87\n" \
"v_mul_f32_e32 v96, s70, v96\n" \
"v_mul_f32_e32 v97, s70, v97\n" \
"v_mul_f32_e32 v98, s70, v98\n" \
"v_mul_f32_e32 v99, s70, v99\n" \
C_"v93, v94, v95\n" \
"v_mul_f32_e32 v45, 0xbfb8aa3b, v228\n" \
"v_mfma_f32_16x16x16_bf16 v[48:51], v[182:183], v[46:47], 0\n" \
"v_mfma_f32_16x16x16_bf16 v[196:199], v[190:191], v[46:47], 0\n" \
"v_mfma_f32_16x16x16_bf16 v[212:215], v[206:207], v[46:47], 0\n" \
"v_max_f32_e64 v193, v193, v209\n" \
C_"v86, v88, v89\n" \
C_"v87, v90, v91\n" \
C_"v94, v96, v97\n" \
C_"v95, v98, v99\n" \
"v_mfma_f32_16x16x16_bf16 v[228:231], v[222:223], v[46:47], 0\n" \
"v_mul_f32_e32 v193, s72, v193\n" \
"v_mov_b32_e32 v209, v193\n" \
"ds_write_b128 v27, v[84:87] offset:8192\n" \
"v_mfma_f32_16x16x16_bf16 v[244:247], v[238:239], v[46:47], 0\n" \
"v_permlane16_swap_b32_e32 v193, v209\n" \
"s_mov_b32 s71, 0x3e0293ee\n" \
M_"v[176:179], a[252:255], a[220:223], v[176:179]\n" \
"v_mov_b32_e32 v225, v193\n" \
"v_mov_b32_e32 v241, v209\n" \
"v_mul_f32_e32 v100, s70, v100\n" \
"v_mul_f32_e64 v101, s70, v101\n" \
"ds_write_b128 v27, v[92:95] offset:9216\n" \
"v_mul_f32_e32 v108, s70, v108\n" \
"v_mul_f32_e32 v109, s70, v109\n" \
"v_pk_mul_f32 v[116:117], v[116:117], s[70:71]\n" \
"v_pk_mul_f32 v[124:125], v[124:125], s[70:71]\n" \
"v_pk_mul_f32 v[132:133], v[132:133], s[70:71]\n" \
"v_pk_mul_f32 v[140:141], v[140:141], s[70:71]\n" \
"v_pk_mul_f32 v[148:149], v[148:149], s[70:71]\n" \
"v_pk_mul_f32 v[156:157], v[156:157], s[70:71]\n" \
"v_permlane32_swap_b32_e32 v193, v225\n" \
"v_permlane32_swap_b32_e32 v209, v241\n" \
"v_mul_f32_e32 v102, s70, v102\n" \
"v_mul_f32_e32 v103, s70, v103\n" \
C_"v100, v100, v101\n" \
"v_mul_f32_e32 v110, s70, v110\n" \
"v_mul_f32_e32 v111, s70, v111\n" \
C_"v108, v108, v109\n" \
"v_pk_mul_f32 v[118:119], v[118:119], s[70:71]\n" \
C_"v116, v116, v117\n" \
"v_pk_mul_f32 v[126:127], v[126:127], s[70:71]\n" \
C_"v124, v124, v125\n" \
"v_pk_mul_f32 v[134:135], v[134:135], s[70:71]\n" \
C_"v132, v132, v133\n" \
"v_pk_mul_f32 v[142:143], v[142:143], s[70:71]\n" \
C_"v140, v140, v141\n" \
"v_pk_mul_f32 v[150:151], v[150:151], s[70:71]\n" \
C_"v148, v148, v149\n" \
"v_pk_mul_f32 v[158:159], v[158:159], s[70:71]\n" \
C_"v156, v156, v157\n" \
"v_fma_f32 v192, v192, s72, -v193\n" \
"v_fma_f32 v194, v194, s72, -v193\n" \
"v_fma_f32 v208, v208, s72, -v209\n" \
"v_fma_f32 v210, v210, s72, -v209\n" \
"v_mul_f32_e32 v104, s70, v104\n" \
"v_mul_f32_e32 v105, s70, v105\n" \
"v_mul_f32_e32 v106, s70, v106\n" \
"v_mul_f32_e32 v107, s70, v107\n" \
C_"v101, v102, v103\n" \
"v_mul_f32_e32 v112, s70, v112\n" \
"v_mul_f32_e32 v113, s70, v113\n" \
"v_mul_f32_e32 v114, s70, v114\n" \
"v_mul_f32_e32 v115, s70, v115\n" \
C_"v109, v110, v111\n" \
"v_pk_mul_f32 v[120:121], v[120:121], s[70:71]\n" \
"v_pk_mul_f32 v[122:123], v[122:123], s[70:71]\n" \
C_"v117, v118, v119\n" \
"v_pk_mul_f32 v[128:129], v[128:129], s[70:71]\n" \
"v_pk_mul_f32 v[130:131], v[130:131], s[70:71]\n" \
C_"v125, v126, v127\n" \
"v_pk_mul_f32 v[136:137], v[136:137], s[70:71]\n" \
"v_pk_mul_f32 v[138:139], v[138:139], s[70:71]\n" \
C_"v133, v134, v135\n" \
"v_pk_mul_f32 v[144:145], v[144:145], s[70:71]\n" \
"v_pk_mul_f32 v[146:147], v[146:147], s[70:71]\n" \
C_"v141, v142, v143\n" \
"v_pk_mul_f32 v[152:153], v[152:153], s[70:71]\n" \
"v_pk_mul_f32 v[154:155], v[154:155], s[70:71]\n" \
C_"v149, v150, v151\n" \
"v_pk_mul_f32 v[160:161], v[160:161], s[70:71]\n" \
"v_pk_mul_f32 v[162:163], v[162:163], s[70:71]\n" \
C_"v157, v158, v159\n" \
"v_pk_mul_f32 v[176:177], v[176:177], s[70:71]\n" \
"v_pk_mul_f32 v[178:179], v[178:179], s[70:71]\n" \
E_"v192, v192\n" \
E_"v194, v194\n" \
E_"v208, v208\n" \
E_"v210, v210\n" \
"v_fma_f32 v224, v224, s72, -v225\n" \
"v_fma_f32 v226, v226, s72, -v225\n" \
M_"v[172:175], a[184:187], a[156:159], v[172:175]\n" \
C_"v102, v104, v105\n" \
C_"v103, v106, v107\n" \
C_"v110, v112, v113\n" \
C_"v111, v114, v115\n" \
C_"v118, v120, v121\n" \
C_"v119, v122, v123\n" \
C_"v126, v128, v129\n" \
C_"v127, v130, v131\n" \
C_"v134, v136, v137\n" \
C_"v135, v138, v139\n" \
C_"v142, v144, v145\n" \
C_"v143, v146, v147\n" \
C_"v150, v152, v153\n" \
C_"v151, v154, v155\n" \
C_"v158, v160, v161\n" \
C_"v159, v162, v163\n" \
E_"v224, v224\n" \
E_"v226, v226\n" \
"v_fma_f32 v240, v240, s72, -v241\n" \
"v_fma_f32 v242, v242, s72, -v241\n" \
"ds_write_b128 v27, v[100:103] offset:12288\n" \
C_"v192, v192, v194\n" \
C_"v208, v208, v210\n" \
E_"v240, v240\n" \
E_"v242, v242\n" \
C_"v68, v48, v49\n" \
C_"v69, v50, v51\n" \
C_"v70, v196, v197\n" \
C_"v71, v198, v199\n" \
"ds_write_b128 v27, v[108:111] offset:13312\n" \
C_"v72, v212, v213\n" \
C_"v73, v214, v215\n" \
"v_perm_b32 v184, v192, v192, v40\n" \
"v_perm_b32 v185, v192, v192, v41\n" \
"v_perm_b32 v186, v192, v192, v42\n" \
"v_perm_b32 v187, v192, v192, v43\n" \
"v_perm_b32 v200, v208, v208, v40\n" \
"v_perm_b32 v201, v208, v208, v41\n" \
"v_perm_b32 v202, v208, v208, v42\n" \
"v_perm_b32 v203, v208, v208, v43\n" \
"ds_write_b128 v21, v[116:119] offset:16384\n" \
C_"v224, v224, v226\n" \
C_"v74, v228, v229\n" \
C_"v75, v230, v231\n" \
M_"v[32:35], v[68:71], v[184:187], 0\n" \
M_"v[36:39], v[70:73], v[200:203], 0\n" \
"v_perm_b32 v216, v224, v224, v40\n" \
"v_perm_b32 v217, v224, v224, v41\n" \
"ds_write_b128 v21, v[124:127] offset:17408\n" \
"v_perm_b32 v218, v224, v224, v42\n" \
"v_perm_b32 v219, v224, v224, v43\n" \
C_"v240, v240, v242\n" \
M_"v[172:175], a[248:251], a[220:223], v[172:175]\n" \
C_"v76, v244, v245\n" \
C_"v77, v246, v247\n" \
M_"v[248:251], v[72:75], v[216:219], 0\n" \
"v_perm_b32 v232, v240, v240, v40\n" \
"v_perm_b32 v233, v240, v240, v41\n" \
"ds_write_b128 v21, v[132:135] offset:20480\n" \
"v_perm_b32 v234, v240, v240, v42\n" \
"v_perm_b32 v235, v240, v240, v43\n" \
"v_pk_mul_f32 v[172:173], v[172:173], s[70:71]\n" \
"v_pk_mul_f32 v[174:175], v[174:175], s[70:71]\n" \
C_"v172, v172, v173\n" \
C_"v173, v174, v175\n" \
C_"v174, v176, v177\n" \
C_"v175, v178, v179\n" \
M_"v[64:67], v[74:77], v[232:235], 0\n" \
"ds_write_b128 v21, v[140:143] offset:21504\n" \
M_"v[168:171], a[180:183], a[156:159], v[168:171]\n" \
C_"v52, v252, v253\n" \
C_"v53, v254, v255\n" \
M_"v[168:171], a[244:247], a[220:223], v[168:171]\n" \
"v_exp_f32_e64 v45, v45\n" \
M_"v[192:195], v[52:55], v[184:187], 0\n" \
"ds_write_b128 v21, v[148:151] offset:24576\n" \
M_"v[208:211], v[54:57], v[200:203], 0\n" \
M_"v[224:227], v[56:59], v[216:219], 0\n" \
M_"v[240:243], v[58:61], v[232:235], 0\n" \
"ds_write_b128 v21, v[156:159] offset:25600\n" \
"v_mov_b32_dpp v34, v38 quad_perm:[0,1,2,3] row_mask:0x2 bank_mask:0xf\n" \
"v_pk_mul_f32 v[168:169], v[168:169], s[70:71]\n" \
"v_pk_mul_f32 v[170:171], v[170:171], s[70:71]\n" \
"v_mov_b32_dpp v34, v250 quad_perm:[0,1,2,3] row_mask:0x4 bank_mask:0xf\n" \
"v_add_f32_e64 v45, 1.0, v45\n" \
M_"v[164:167], a[176:179], a[156:159], v[164:167]\n" \
"v_mov_b32_dpp v34, v66 quad_perm:[0,1,2,3] row_mask:0x8 bank_mask:0xf\n" \
"v_mov_b32_dpp v32, v36 quad_perm:[0,1,2,3] row_mask:0x2 bank_mask:0xf\n" \
"v_rcp_f32_e64 v45, v45\n" \
"ds_write_b128 v21, v[172:175] offset:29696\n" \
"v_mov_b32_dpp v33, v37 quad_perm:[0,1,2,3] row_mask:0x2 bank_mask:0xf\n" \
"v_rcp_f32_e64 v34, v34\n" \
"v_mov_b32_dpp v32, v248 quad_perm:[0,1,2,3] row_mask:0x4 bank_mask:0xf\n" \
"v_mov_b32_dpp v33, v249 quad_perm:[0,1,2,3] row_mask:0x4 bank_mask:0xf\n" \
"v_mul_f32_e64 v34, v45, v34\n" \
"v_mov_b32_dpp v32, v64 quad_perm:[0,1,2,3] row_mask:0x8 bank_mask:0xf\n" \
"v_mov_b32_dpp v33, v65 quad_perm:[0,1,2,3] row_mask:0x8 bank_mask:0xf\n" \
"v_mul_f32_e32 v32, v34, v32\n" \
"v_mul_f32_e32 v33, v34, v33\n" \
"v_mov_b32_e64 v38, v34\n" \
M_"v[164:167], a[240:243], a[220:223], v[164:167]\n" \
"v_sub_f32_e32 v44, 1.0, v45\n" \
"v_permlane16_swap_b32_e32 v34, v38\n" \
"v_mov_b32_e32 v250, v34\n" \
"v_mov_b32_e32 v66, v38\n" \
C_"v32, v32, v33\n" \
"v_permlane32_swap_b32_e32 v34, v250\n" \
"v_permlane32_swap_b32_e32 v38, v66\n" \
"v_mul_f32_e64 v192, v34, v192\n" \
"v_pk_mul_f32 v[164:165], v[164:165], s[70:71]\n" \
"v_pk_mul_f32 v[166:167], v[166:167], s[70:71]\n" \
C_"v164, v164, v165\n" \
C_"v165, v166, v167\n" \
C_"v166, v168, v169\n" \
C_"v167, v170, v171\n" \
"ds_write_b128 v21, v[164:167] offset:28672\n" \
"v_mul_f32_e32 v193, v34, v193\n" \
"v_mul_f32_e32 v208, v38, v208\n" \
"v_mul_f32_e32 v209, v38, v209\n" \
"v_mul_f32_e32 v224, v250, v224\n" \
"v_mul_f32_e32 v225, v250, v225\n" \
"v_mul_f32_e32 v240, v66, v240\n" \
"v_mul_f32_e32 v241, v66, v241\n" \
"v_mul_f32_e32 v194, v34, v194\n" \
"v_mul_f32_e64 v195, v34, v195\n" \
C_"v192, v192, v193\n" \
"v_mul_f32_e32 v210, v38, v210\n" \
"v_mul_f32_e32 v211, v38, v211\n" \
C_"v208, v208, v209\n" \
"v_mul_f32_e32 v226, v250, v226\n" \
"v_mul_f32_e32 v227, v250, v227\n" \
C_"v224, v224, v225\n" \
"v_mul_f32_e32 v242, v66, v242\n" \
"v_mul_f32_e32 v243, v66, v243\n" \
C_"v240, v240, v241\n" \
C_"v193, v194, v195\n" \
"s_add_u32 s84, s48, 0x0\n" \
"s_nop 0\n" \
C_"v209, v210, v211\n" \
"s_add_u32 s85, s48, 0x24800\n" \
C_"v225, v226, v227\n" \
"s_add_u32 s86, s48, 0x49000\n" \
C_"v241, v242, v243\n" \
"s_add_u32 s87, s48, 0x6d800\n" \
"ds_write_b32 v26, v44\n" \
"buffer_store_dword v32, v25, s[80:83], s49 offen sc1\n" \
"buffer_store_dwordx2 v[192:193], v24, s[80:83], s84 offen sc1\n" \
"buffer_store_dwordx2 v[208:209], v24, s[80:83], s85 offen sc1\n" \
"buffer_store_dwordx2 v[224:225], v24, s[80:83], s86 offen sc1\n" \
"buffer_store_dwordx2 v[240:241], v24, s[80:83], s87 offen sc1\n" \
"s_branch L_end_%=\n" \
"L_poll60_%=:\n" \
"s_mov_b32 s84, 0\n" \
"L_pl60_%=:\n" \
"s_sleep 1\n" \
"v_bfe_u32 v44, v20, 2, 5\n" \
"buffer_load_dword v44, v44, s[80:83], s38 offen offset:128 sc1\n" \
"s_waitcnt vmcnt(0)\n" \
"v_cmp_ne_u32_e32 vcc, s62, v44\n" \
"s_add_u32 s84, s84, 1\n" \
"s_nop 3\n" \
"s_cbranch_vccz L_pollr60_%=\n" \
"s_cmp_gt_u32 s84, 0x400000\n" \
"s_cbranch_scc0 L_pl60_%=\n" \
"s_branch L_pollr60_%=\n" \
"L_poll160_%=:\n" \
"s_mov_b32 s84, 0\n" \
"L_pl160_%=:\n" \
"s_sleep 1\n" \
"v_bfe_u32 v44, v20, 2, 5\n" \
"buffer_load_dword v44, v44, s[80:83], s38 offen offset:0 sc1\n" \
"s_waitcnt vmcnt(0)\n" \
"v_cmp_ne_u32_e32 vcc, s62, v44\n" \
"s_add_u32 s84, s84, 1\n" \
"s_nop 3\n" \
"s_cbranch_vccz L_pollr160_%=\n" \
"s_cmp_gt_u32 s84, 0x400000\n" \
"s_cbranch_scc0 L_pl160_%=\n" \
"s_branch L_pollr160_%=\n" \
".fill (L_pb + 42560 - .) / 4, 4, 0xBF800000\n" \
"L_w1_%=:\n" \
"s_waitcnt vmcnt(19)\n" \
"s_barrier\n" \
X_"a[160:161], v32 offset:0\n" \
X_"a[162:163], v32 offset:1024\n" \
R_"a[128:131], v28 offset:16384\n" \
X_"a[224:225], v32 offset:8192\n" \
X_"a[226:227], v32 offset:9216\n" \
R_"a[192:195], v29 offset:16384\n" \
R_"a[132:135], v28 offset:18432\n" \
R_"a[196:199], v29 offset:18432\n" \
X_"a[164:165], v33 offset:0\n" \
X_"a[166:167], v33 offset:1024\n" \
X_"a[228:229], v33 offset:8192\n" \
X_"a[230:231], v33 offset:9216\n" \
R_"a[136:139], v28 offset:20480\n" \
R_"a[200:203], v29 offset:20480\n" \
"s_waitcnt lgkmcnt(11)\n" \
M_"a[0:3], a[128:131], a[160:163], 0\n" \
X_"a[168:169], v34 offset:0\n" \
"s_lshr_b32 s4, s2, 1\n" \
"s_waitcnt lgkmcnt(9)\n" \
M_"a[0:3], a[192:195], a[224:227], a[0:3]\n" \
X_"a[170:171], v34 offset:1024\n" \
"s_and_b32 s4, s4, 0x7c\n" \
"s_waitcnt lgkmcnt(9)\n" \
M_"a[16:19], a[132:135], a[160:163], 0\n" \
X_"a[232:233], v34 offset:8192\n" \
"v_mov_b32_e32 v213, s13\n" \
"s_waitcnt lgkmcnt(9)\n" \
M_"a[16:19], a[196:199], a[224:227], a[16:19]\n" \
X_"a[234:235], v34 offset:9216\n" \
"s_or_b32 s4, s4, s3\n" \
"s_waitcnt lgkmcnt(8)\n" \
M_"a[20:23], a[132:135], a[164:167], 0\n" \
R_"a[140:143], v28 offset:22528\n" \
"v_lshrrev_b32_e32 v212, 4, v20\n" \
"s_waitcnt lgkmcnt(7)\n" \
M_"a[20:23], a[196:199], a[228:231], a[20:23]\n" \
R_"a[204:207], v29 offset:22528\n" \
"v_mad_u64_u32 v[214:215], s[4:5], s4, 36, v[212:213]\n" \
M_"a[4:7], a[128:131], a[164:167], 0\n" \
X_"a[172:173], v35 offset:0\n" \
"v_mov_b32_e64 v217, s9\n" \
M_"a[4:7], a[192:195], a[228:231], a[4:7]\n" \
X_"a[174:175], v35 offset:1024\n" \
"s_movk_i32 s4, 0x240\n" \
"s_waitcnt lgkmcnt(9)\n" \
M_"a[32:35], a[136:139], a[160:163], 0\n" \
X_"a[236:237], v35 offset:8192\n" \
"v_cmp_gt_u32_e32 vcc, s4, v20\n" \
"s_waitcnt lgkmcnt(9)\n" \
M_"a[32:35], a[200:203], a[224:227], a[32:35]\n" \
X_"a[238:239], v35 offset:9216\n" \
"s_lshl_b32 s4, s2, 3\n" \
M_"a[36:39], a[136:139], a[164:167], 0\n" \
R_"a[144:147], v28 offset:0\n" \
"s_and_b32 s4, s4, 56\n" \
M_"a[36:39], a[200:203], a[228:231], a[36:39]\n" \
R_"a[208:211], v29 offset:0\n" \
"v_cndmask_b32_e32 v217, v217, v213, vcc\n" \
"s_waitcnt lgkmcnt(10)\n" \
M_"a[40:43], a[136:139], a[168:171], 0\n" \
R_"a[148:151], v28 offset:2048\n" \
"v_lshlrev_b32_e32 v213, 3, v214\n" \
"s_waitcnt lgkmcnt(9)\n" \
M_"a[40:43], a[200:203], a[232:235], a[40:43]\n" \
R_"a[212:215], v29 offset:2048\n" \
"v_and_b32_e32 v213, 0x1ffc0, v213\n" \
M_"a[24:27], a[132:135], a[168:171], 0\n" \
R_"a[152:155], v28 offset:4096\n" \
"v_and_b32_e64 v214, 7, v214\n" \
M_"a[24:27], a[196:199], a[232:235], a[24:27]\n" \
R_"a[216:219], v29 offset:4096\n" \
"v_or3_b32 v213, v214, s4, v213\n" \
M_"a[8:11], a[128:131], a[168:171], 0\n" \
R_"a[156:159], v28 offset:6144\n" \
"s_lshl_b32 s4, s2, 2\n" \
M_"a[8:11], a[192:195], a[232:235], a[8:11]\n" \
R_"a[220:223], v29 offset:6144\n" \
"s_mov_b32 m0, s32\n" \
"s_waitcnt lgkmcnt(13)\n" \
M_"a[48:51], a[140:143], a[160:163], 0\n" \
"s_mov_b32 exec_lo, 0xff000000\n" \
"s_mov_b32 exec_hi, 0x0\n" \
"global_load_lds_dword v[184:185], off\n" \
"s_mov_b64 exec, -1\n" \
"s_waitcnt lgkmcnt(12)\n" \
M_"a[48:51], a[204:207], a[224:227], a[48:51]\n" \
X_"a[176:177], v36 offset:0\n" \
"s_or_b32 s4, s4, s3\n" \
M_"a[52:55], a[140:143], a[164:167], 0\n" \
X_"a[178:179], v36 offset:1024\n" \
"s_mul_i32 s4, s4, 18\n" \
M_"a[52:55], a[204:207], a[228:231], a[52:55]\n" \
"s_mov_b32 m0, s32\n" \
X_"a[240:241], v36 offset:8192\n" \
M_"a[56:59], a[140:143], a[168:171], 0\n" \
"s_mov_b32 exec_lo, 0xffff\n" \
"s_mov_b32 exec_hi, 0x0\n" \
"global_load_lds_dword v[186:187], off\n" \
"s_mov_b64 exec, -1\n" \
M_"a[56:59], a[204:207], a[232:235], a[56:59]\n" \
X_"a[242:243], v36 offset:9216\n" \
"v_min_u32_e32 v212, 53, v212\n" \
"s_waitcnt lgkmcnt(14)\n" \
M_"a[60:63], a[140:143], a[172:175], 0\n" \
X_"a[180:181], v37 offset:0\n" \
"s_sub_i32 s4, s4, 36\n" \
"s_waitcnt lgkmcnt(13)\n" \
M_"a[60:63], a[204:207], a[236:239], a[60:63]\n" \
X_"a[182:183], v37 offset:1024\n" \
"v_add_u32_e32 v212, s4, v212\n" \
M_"a[44:47], a[136:139], a[172:175], 0\n" \
X_"a[244:245], v37 offset:8192\n" \
"v_mov_b32_e64 v215, s12\n" \
M_"a[44:47], a[200:203], a[236:239], a[44:47]\n" \
X_"a[246:247], v37 offset:9216\n" \
"v_mov_b32_e64 v216, s8\n" \
M_"a[28:31], a[132:135], a[172:175], 0\n" \
X_"a[184:185], v38 offset:0\n" \
"v_cndmask_b32_e64 v212, v212, v213, vcc\n" \
M_"a[28:31], a[196:199], a[236:239], a[28:31]\n" \
X_"a[186:187], v38 offset:1024\n" \
"v_cndmask_b32_e64 v216, v216, v215, vcc\n" \
M_"a[12:15], a[128:131], a[172:175], 0\n" \
"s_mov_b64 exec, 0\n" \
"s_swappc_b64 s[88:89], s[86:87]\n" \
"s_mov_b64 exec, -1\n" \
"s_add_u32 s86, s86, 0x2000\n" \
"s_addc_u32 s87, s87, 0\n" \
M_"a[12:15], a[192:195], a[236:239], a[12:15]\n" \
"v_lshlrev_b32_e32 v212, 7, v212\n" \
"v_mov_b32_e32 v213, 0\n" \
"s_waitcnt lgkmcnt(15)\n" \
M_"a[76:79], a[144:147], a[172:175], 0\n" \
X_"a[248:249], v38 offset:8192\n" \
"v_lshl_add_u64 v[188:189], v[216:217], 0, v[212:213]\n" \
M_"a[76:79], a[208:211], a[236:239], a[76:79]\n" \
"s_movk_i32 s5, 0xd80\n" \
"s_lshr_b32 s6, s2, 5\n" \
M_"a[72:75], a[144:147], a[168:171], 0\n" \
"v_lshrrev_b32_e32 v212, 4, v20\n" \
"v_lshl_or_b32 v215, s3, 6, v212\n" \
M_"a[72:75], a[208:211], a[232:235], a[72:75]\n" \
X_"a[250:251], v38 offset:9216\n" \
"v_mul_u32_u24_e32 v216, 0x2aab, v215\n" \
M_"a[68:71], a[144:147], a[164:167], 0\n" \
"v_lshrrev_b32_e64 v216, 16, v216\n" \
"v_mad_i32_i24 v217, v216, -6, v215\n" \
M_"a[68:71], a[208:211], a[228:231], a[68:71]\n" \
"v_add_u32_e32 v218, 0xfffffe50, v215\n" \
"s_mul_i32 s35, s6, 0x48\n" \
M_"a[64:67], a[144:147], a[160:163], 0\n" \
"v_lshrrev_b32_e32 v219, 1, v217\n" \
"v_add_u32_e32 v218, s35, v216\n" \
M_"a[64:67], a[208:211], a[224:227], a[64:67]\n" \
X_"a[188:189], v39 offset:0\n" \
"v_mul_u32_u24_e32 v219, 0x480, v219\n" \
M_"a[80:83], a[148:151], a[160:163], 0\n" \
"v_and_b32_e64 v214, 16, v20\n" \
"v_mad_u32_u24 v219, v218, s5, v219\n" \
"s_waitcnt lgkmcnt(15)\n" \
M_"a[80:83], a[212:215], a[224:227], a[80:83]\n" \
"v_or_b32_e32 v192, v219, v214\n" \
"v_mov_b32_e64 v220, 2.0\n" \
M_"a[84:87], a[148:151], a[164:167], 0\n" \
X_"a[190:191], v39 offset:1024\n" \
"v_mov_b32_e64 v222, 7\n" \
M_"a[84:87], a[212:215], a[228:231], a[84:87]\n" \
"v_lshlrev_b32_e32 v220, 7, v218\n" \
"v_lshrrev_b32_e32 v218, 1, v218\n" \
M_"a[88:91], a[148:151], a[168:171], 0\n" \
"v_and_b32_e32 v218, 4, v218\n" \
"v_and_b32_e32 v216, 2, v216\n" \
M_"a[88:91], a[212:215], a[232:235], a[88:91]\n" \
X_"a[252:253], v39 offset:8192\n" \
"s_mul_i32 s7, s6, 0x2400\n" \
M_"a[92:95], a[148:151], a[172:175], 0\n" \
"s_sub_i32 s34, 0x22000, s7\n" \
"v_bitop3_b32 v216, v217, v218, v216 bitop3:0x1e\n" \
M_"a[92:95], a[212:215], a[236:239], a[92:95]\n" \
"v_or_b32_e32 v213, 0x24400, v20\n" \
"v_lshlrev_b32_e64 v216, 4, v216\n" \
M_"a[108:111], a[152:155], a[172:175], 0\n" \
X_"a[254:255], v39 offset:9216\n" \
"v_add3_u32 v196, v220, s34, v216\n" \
M_"a[108:111], a[216:219], a[236:239], a[108:111]\n" \
"v_mov_b32_e32 v216, v213\n" \
"s_movk_i32 s36, 0x1b0\n" \
M_"a[104:107], a[152:155], a[168:171], 0\n" \
"s_mov_b64 exec, 0\n" \
"s_swappc_b64 s[88:89], s[86:87]\n" \
"s_mov_b64 exec, -1\n" \
"s_andn2_b32 s85, 2, s3\n" \
"s_lshl_b32 s85, s85, 12\n" \
"s_add_u32 s86, s86, s85\n" \
"s_addc_u32 s87, s87, 0\n" \
M_"a[104:107], a[216:219], a[232:235], a[104:107]\n" \
"v_or_b32_e32 v216, 0x100, v215\n" \
"v_mul_u32_u24_e32 v217, 0x2aab, v216\n" \
M_"a[100:103], a[152:155], a[164:167], 0\n" \
"v_add_u32_e32 v215, 0xffffff50, v215\n" \
"v_lshrrev_b32_e32 v217, 16, v217\n" \
M_"a[100:103], a[216:219], a[228:231], a[100:103]\n" \
"v_mov_b32_e64 v220, v215\n" \
"v_mad_i32_i24 v218, v217, -6, v216\n" \
M_"a[96:99], a[152:155], a[160:163], 0\n" \
"v_cmp_gt_u32_e32 vcc, s36, v216\n" \
"v_mov_b32_e32 v221, 7\n" \
M_"a[96:99], a[216:219], a[224:227], a[96:99]\n" \
"v_lshrrev_b32_e32 v222, 1, v218\n" \
"v_cndmask_b32_e32 v217, v220, v217, vcc\n" \
"s_waitcnt lgkmcnt(15)\n" \
M_"a[112:115], a[156:159], a[160:163], 0\n" \
"s_movk_i32 s4, 0x48\n" \
"v_add_u32_e64 v220, s35, v217\n" \
M_"a[112:115], a[220:223], a[224:227], a[112:115]\n" \
"v_mul_u32_u24_e32 v222, 0x480, v222\n" \
"v_mad_u32_u24 v222, v220, s5, v222\n" \
M_"a[116:119], a[156:159], a[164:167], 0\n" \
"v_cmp_gt_u32_e64 s[4:5], s4, v215\n" \
"v_cndmask_b32_e64 v215, v221, v218, vcc\n" \
M_"a[116:119], a[220:223], a[228:231], a[116:119]\n" \
"v_lshrrev_b32_e32 v218, 1, v220\n" \
"v_or_b32_e32 v214, v222, v214\n" \
M_"a[120:123], a[156:159], a[168:171], 0\n" \
"v_mov_b32_e32 v222, 2.0\n" \
"v_and_b32_e32 v218, 4, v218\n" \
M_"a[120:123], a[220:223], a[232:235], a[120:123]\n" \
"v_and_b32_e64 v217, 2, v217\n" \
"v_bitop3_b32 v215, v215, v218, v217 bitop3:0x1e\n" \
M_"a[124:127], a[156:159], a[172:175], 0\n" \
"v_lshlrev_b32_e32 v223, 1, v220\n" \
"v_mov_b32_e32 v224, 2.0\n" \
"s_waitcnt vmcnt(5) lgkmcnt(0)\n" \
M_"a[124:127], a[220:223], a[236:239], a[124:127]\n" \
"s_barrier\n" \
M_"v[52:55], a[128:131], a[176:179], 0\n" \
"s_add_u32 m0, s30, 0x0\n" \
"s_movk_i32 s84, 0x100\n" \
M_"v[52:55], a[192:195], a[240:243], v[52:55]\n" \
B_"v22, s[64:67], s84 offen offset:0 lds\n" \
M_"v[56:59], a[128:131], a[180:183], 0\n" \
X_"a[160:161], v32 offset:32768\n" \
"v_cndmask_b32_e64 v193, v222, v214, vcc\n" \
M_"v[56:59], a[192:195], a[244:247], v[56:59]\n" \
"s_movk_i32 s84, 0x1d00\n" \
M_"v[60:63], a[128:131], a[184:187], 0\n" \
B_"v23, s[64:67], s84 offen offset:1024 lds\n" \
M_"v[60:63], a[192:195], a[248:251], v[60:63]\n" \
X_"a[162:163], v32 offset:33792\n" \
"v_lshlrev_b32_e32 v214, 7, v220\n" \
M_"v[64:67], a[128:131], a[188:191], 0\n" \
X_"a[224:225], v32 offset:40960\n" \
"v_lshlrev_b32_e64 v215, 4, v215\n" \
M_"v[64:67], a[192:195], a[252:255], v[64:67]\n" \
R_"a[128:131], v28 offset:49152\n" \
"s_mul_i32 s39, s3, 18\n" \
M_"v[80:83], a[132:135], a[188:191], 0\n" \
R_"a[192:195], v29 offset:49152\n" \
"v_add3_u32 v214, v214, s34, v215\n" \
M_"v[80:83], a[196:199], a[252:255], v[80:83]\n" \
"s_movk_i32 s84, 0x3900\n" \
M_"v[76:79], a[132:135], a[184:187], 0\n" \
B_"v22, s[64:67], s84 offen offset:2048 lds\n" \
M_"v[76:79], a[196:199], a[248:251], v[76:79]\n" \
"s_movk_i32 s84, 0x5500\n" \
M_"v[72:75], a[132:135], a[180:183], 0\n" \
B_"v23, s[64:67], s84 offen offset:3072 lds\n" \
M_"v[72:75], a[196:199], a[244:247], v[72:75]\n" \
"s_add_u32 m0, s31, 0x0\n" \
"s_mov_b32 s84, 0x100000\n" \
M_"v[68:71], a[132:135], a[176:179], 0\n" \
B_"v26, s[68:71], s84 offen offset:0 lds\n" \
M_"v[68:71], a[196:199], a[240:243], v[68:71]\n" \
R_"a[132:135], v28 offset:51200\n" \
"v_cndmask_b32_e64 v195, v224, v223, s[4:5]\n" \
M_"v[84:87], a[136:139], a[176:179], 0\n" \
R_"a[196:199], v29 offset:51200\n" \
"s_movk_i32 s4, 0x1f8\n" \
M_"v[84:87], a[200:203], a[240:243], v[84:87]\n" \
"s_addk_i32 s84, 0x7c00\n" \
M_"v[88:91], a[136:139], a[180:183], 0\n" \
B_"v26, s[68:71], s84 offen offset:1024 lds\n" \
M_"v[88:91], a[200:203], a[244:247], v[88:91]\n" \
X_"a[226:227], v32 offset:41984\n" \
"v_mov_b32_e64 v215, v213\n" \
M_"v[92:95], a[136:139], a[184:187], 0\n" \
"s_addk_i32 s84, 0x7c00\n" \
M_"v[92:95], a[200:203], a[248:251], v[92:95]\n" \
B_"v27, s[68:71], s84 offen offset:2048 lds\n" \
M_"v[96:99], a[136:139], a[188:191], 0\n" \
X_"a[164:165], v33 offset:32768\n" \
"s_add_i32 s35, s35, s39\n" \
M_"v[96:99], a[200:203], a[252:255], v[96:99]\n" \
R_"a[136:139], v28 offset:53248\n" \
"v_cmp_gt_u32_e64 vcc, s4, v216\n" \
M_"v[112:115], a[140:143], a[188:191], 0\n" \
R_"a[200:203], v29 offset:53248\n" \
"v_add_u32_e64 v212, s35, v212\n" \
M_"v[112:115], a[204:207], a[252:255], v[112:115]\n" \
"s_addk_i32 s84, 0x7c00\n" \
M_"v[108:111], a[140:143], a[184:187], 0\n" \
B_"v27, s[68:71], s84 offen offset:3072 lds\n" \
"s_mov_b64 exec, 0\n" \
"s_swappc_b64 s[88:89], s[86:87]\n" \
"s_mov_b64 exec, -1\n" \
M_"v[108:111], a[204:207], a[248:251], v[108:111]\n" \
"s_add_u32 m0, s30, 0x1000\n" \
"s_mov_b32 s84, 0x8100\n" \
M_"v[104:107], a[140:143], a[180:183], 0\n" \
B_"v22, s[64:67], s84 offen offset:0 lds\n" \
M_"v[104:107], a[204:207], a[244:247], v[104:107]\n" \
X_"a[166:167], v33 offset:33792\n" \
"v_mov_b32_e64 v219, 2.0\n" \
M_"v[100:103], a[140:143], a[176:179], 0\n" \
X_"a[228:229], v33 offset:40960\n" \
"v_cndmask_b32_e64 v197, v215, v214, vcc\n" \
M_"v[100:103], a[204:207], a[240:243], v[100:103]\n" \
R_"a[140:143], v28 offset:55296\n" \
"s_movk_i32 s35, 0x120\n" \
M_"v[116:119], a[144:147], a[176:179], 0\n" \
R_"a[204:207], v29 offset:55296\n" \
"v_mul_u32_u24_e32 v214, 0xd80, v212\n" \
".fill (L_pb + 44768 - .) / 4, 4, 0xBF800000\n" \
"L_pp11:\n" \
"s_cbranch_execz L_pret\n" \
M_"v[116:119], a[208:211], a[240:243], v[116:119]\n" \
"s_addk_i32 s84, 0x1c00\n" \
M_"v[120:123], a[144:147], a[180:183], 0\n" \
B_"v23, s[64:67], s84 offen offset:1024 lds\n" \
M_"v[120:123], a[208:211], a[244:247], v[120:123]\n" \
"s_addk_i32 s84, 0x1c00\n" \
M_"v[124:127], a[144:147], a[184:187], 0\n" \
B_"v22, s[64:67], s84 offen offset:2048 lds\n" \
M_"v[124:127], a[208:211], a[248:251], v[124:127]\n" \
"s_addk_i32 s84, 0x1c00\n" \
M_"v[128:131], a[144:147], a[188:191], 0\n" \
B_"v23, s[64:67], s84 offen offset:3072 lds\n" \
M_"v[128:131], a[208:211], a[252:255], v[128:131]\n" \
R_"a[144:147], v28 offset:32768\n" \
"v_cmp_gt_u32_e64 vcc, s35, v20\n" \
M_"v[144:147], a[148:151], a[188:191], 0\n" \
R_"a[208:211], v29 offset:32768\n" \
"v_lshrrev_b32_e64 v215, 1, v212\n" \
M_"v[144:147], a[212:215], a[252:255], v[144:147]\n" \
"s_add_u32 m0, s31, 0x4000\n" \
"s_mov_b32 s84, 0x100000\n" \
M_"v[140:143], a[148:151], a[184:187], 0\n" \
B_"v26, s[72:75], s84 offen offset:0 lds\n" \
M_"v[140:143], a[212:215], a[248:251], v[140:143]\n" \
X_"a[230:231], v33 offset:41984\n" \
"s_and_b32 s4, s2, 7\n" \
M_"v[136:139], a[148:151], a[180:183], 0\n" \
"s_addk_i32 s84, 0x7c00\n" \
M_"v[136:139], a[212:215], a[244:247], v[136:139]\n" \
B_"v26, s[72:75], s84 offen offset:1024 lds\n" \
M_"v[132:135], a[148:151], a[176:179], 0\n" \
X_"a[168:169], v34 offset:32768\n" \
"v_cndmask_b32_e64 v194, v219, v214, vcc\n" \
M_"v[132:135], a[212:215], a[240:243], v[132:135]\n" \
R_"a[148:151], v28 offset:34816\n" \
"v_lshlrev_b32_e64 v214, 7, v212\n" \
M_"v[148:151], a[152:155], a[176:179], 0\n" \
R_"a[212:215], v29 offset:34816\n" \
"v_lshrrev_b32_e64 v212, 2, v212\n" \
M_"v[148:151], a[216:219], a[240:243], v[148:151]\n" \
X_"a[170:171], v34 offset:33792\n" \
"v_and_b32_e64 v212, 2, v212\n" \
M_"v[152:155], a[152:155], a[180:183], 0\n" \
X_"a[232:233], v34 offset:40960\n" \
"v_and_or_b32 v212, v215, 1, v212\n" \
M_"v[152:155], a[216:219], a[244:247], v[152:155]\n" \
X_"a[234:235], v34 offset:41984\n" \
"s_lshl_b32 s5, s4, 2\n" \
M_"v[156:159], a[152:155], a[184:187], 0\n" \
X_"a[172:173], v35 offset:32768\n" \
"s_bfe_u32 s38, s2, 0x20003\n" \
M_"v[156:159], a[216:219], a[248:251], v[156:159]\n" \
X_"a[174:175], v35 offset:33792\n" \
"v_lshlrev_b32_e32 v212, 5, v212\n" \
M_"v[160:163], a[152:155], a[188:191], 0\n" \
X_"a[236:237], v35 offset:40960\n" \
"s_movk_i32 s35, 0x60\n" \
M_"v[160:163], a[216:219], a[252:255], v[160:163]\n" \
R_"a[152:155], v28 offset:36864\n" \
"s_or_b32 s5, s5, s38\n" \
M_"v[176:179], a[156:159], a[188:191], 0\n" \
R_"a[216:219], v29 offset:36864\n" \
"v_bitop3_b32 v212, v212, s35, v214 bitop3:0x36\n" \
M_"v[176:179], a[220:223], a[252:255], v[176:179]\n" \
X_"a[238:239], v35 offset:41984\n" \
"s_lshr_b32 s36, s2, 3\n" \
M_"v[172:175], a[156:159], a[184:187], 0\n" \
"v_add_u32_e32 v212, s34, v212\n" \
"s_mul_i32 s34, s5, 0x12000\n" \
M_"v[172:175], a[220:223], a[248:251], v[172:175]\n" \
"s_or_b32 s40, s34, 0x6000000\n" \
"s_mul_i32 s34, s36, 0x90000\n" \
M_"v[168:171], a[156:159], a[180:183], 0\n" \
"s_lshl_b32 s36, s3, 14\n" \
"s_add_i32 s34, s34, s36\n" \
M_"v[168:171], a[220:223], a[244:247], v[168:171]\n" \
"s_lshl_b32 s38, s5, 8\n" \
"s_add_i32 s41, s34, 0x4240000\n" \
M_"v[164:167], a[156:159], a[176:179], 0\n" \
"s_mul_i32 s34, s5, 36\n" \
"s_add_i32 s5, s7, s40\n" \
M_"v[164:167], a[220:223], a[240:243], v[164:167]\n" \
R_"a[156:159], v28 offset:38912\n" \
R_"a[220:223], v29 offset:38912\n" \
"s_waitcnt lgkmcnt(15)\n" \
M_"a[0:3], a[128:131], a[160:163], a[0:3]\n" \
"s_addk_i32 s84, 0x7c00\n" \
M_"a[0:3], a[192:195], a[224:227], a[0:3]\n" \
B_"v27, s[72:75], s84 offen offset:2048 lds\n" \
M_"a[4:7], a[128:131], a[164:167], a[4:7]\n" \
X_"a[176:177], v36 offset:32768\n" \
"s_addk_i32 s5, 0x2000\n" \
"s_waitcnt lgkmcnt(15)\n" \
M_"a[4:7], a[192:195], a[228:231], a[4:7]\n" \
"s_addk_i32 s84, 0x7c00\n" \
"s_waitcnt lgkmcnt(11)\n" \
M_"a[8:11], a[128:131], a[168:171], a[8:11]\n" \
B_"v27, s[72:75], s84 offen offset:3072 lds\n" \
"s_waitcnt lgkmcnt(9)\n" \
M_"a[8:11], a[192:195], a[232:235], a[8:11]\n" \
X_"a[178:179], v36 offset:33792\n" \
"s_mov_b32 s37, 2.0\n" \
"s_waitcnt lgkmcnt(8)\n" \
M_"a[12:15], a[128:131], a[172:175], a[12:15]\n" \
X_"a[240:241], v36 offset:40960\n" \
"s_or_b32 s38, s38, 0x5be8000\n" \
"s_waitcnt lgkmcnt(5)\n" \
M_"a[12:15], a[192:195], a[236:239], a[12:15]\n" \
"s_cmp_lg_u32 s3, 0\n" \
"s_cselect_b32 s36, s37, s5\n" \
M_"a[28:31], a[132:135], a[172:175], a[28:31]\n" \
"s_lshl_b32 s5, s6, 2\n" \
"s_or_b32 s5, s38, s5\n" \
M_"a[28:31], a[196:199], a[236:239], a[28:31]\n" \
X_"a[242:243], v36 offset:41984\n" \
"s_lshl_b32 s35, s3, 10\n" \
M_"a[24:27], a[132:135], a[168:171], a[24:27]\n" \
"s_cmpk_lt_u32 s3, 3\n" \
"s_cselect_b32 s37, s37, s5\n" \
M_"a[24:27], a[196:199], a[232:235], a[24:27]\n" \
"s_lshl_b32 s5, s4, 16\n" \
"s_lshl_b32 s4, s4, 11\n" \
M_"a[20:23], a[132:135], a[164:167], a[20:23]\n" \
"v_cndmask_b32_e32 v198, v213, v212, vcc\n" \
"v_or_b32_e32 v212, s35, v20\n" \
M_"a[20:23], a[196:199], a[228:231], a[20:23]\n" \
X_"a[180:181], v37 offset:32768\n" \
"s_or_b32 s39, s40, s35\n" \
M_"a[16:19], a[132:135], a[160:163], a[16:19]\n" \
"s_or_b32 s55, s41, s4\n" \
"v_or_b32_e32 v199, 0x22000, v212\n" \
M_"a[16:19], a[196:199], a[224:227], a[16:19]\n" \
"s_add_i32 s35, s39, s7\n" \
"s_add_i32 s54, s41, s5\n" \
M_"a[32:35], a[136:139], a[160:163], a[32:35]\n" \
"s_mov_b32 m0, s32\n" \
X_"a[182:183], v37 offset:33792\n" \
M_"a[32:35], a[200:203], a[224:227], a[32:35]\n" \
"s_mov_b32 exec_lo, 0x0\n" \
"s_mov_b32 exec_hi, 0xff\n" \
"global_load_lds_dword v[184:185], off\n" \
"s_mov_b64 exec, -1\n" \
M_"a[36:39], a[136:139], a[164:167], a[36:39]\n" \
"s_add_i32 s55, s55, 0x80000\n" \
"v_mov_b32_e32 v215, 0xffff\n" \
M_"a[36:39], a[200:203], a[228:231], a[36:39]\n" \
X_"a[244:245], v37 offset:40960\n" \
"s_and_b32 s4, s3, 1\n" \
M_"a[40:43], a[136:139], a[168:171], a[40:43]\n" \
"s_lshl_b32 s5, s4, 4\n" \
"v_lshrrev_b32_e32 v212, 4, v20\n" \
M_"a[40:43], a[200:203], a[232:235], a[40:43]\n" \
"s_mov_b32 m0, s32\n" \
"v_lshrrev_b32_e32 v218, 6, v20\n" \
M_"a[44:47], a[136:139], a[172:175], a[44:47]\n" \
"s_mov_b32 exec_lo, 0xffff0000\n" \
"s_mov_b32 exec_hi, 0x0\n" \
"global_load_lds_dword v[186:187], off\n" \
"s_mov_b64 exec, -1\n" \
M_"a[44:47], a[200:203], a[236:239], a[44:47]\n" \
X_"a[246:247], v37 offset:41984\n" \
"v_and_b32_e64 v219, 12, v218\n" \
M_"a[60:63], a[140:143], a[172:175], a[60:63]\n" \
"v_and_or_b32 v214, v212, 15, s5\n" \
"v_cmp_gt_u32_e64 vcc, v219, v214\n" \
M_"a[60:63], a[204:207], a[236:239], a[60:63]\n" \
"v_mov_b32_e32 v220, 0xffff0000\n" \
"s_lshl_b32 s5, s2, 2\n" \
M_"a[56:59], a[140:143], a[168:171], a[56:59]\n" \
X_"a[184:185], v38 offset:32768\n" \
"v_cndmask_b32_e64 v217, v215, 0, vcc\n" \
M_"a[56:59], a[204:207], a[232:235], a[56:59]\n" \
"v_cmp_lt_u32_e32 vcc, v219, v214\n" \
"v_mov_b32_e64 v213, s23\n" \
M_"a[52:55], a[140:143], a[164:167], a[52:55]\n" \
"s_or_b32 s5, s5, s3\n" \
"v_cndmask_b32_e32 v221, 0, v220, vcc\n" \
M_"a[52:55], a[204:207], a[228:231], a[52:55]\n" \
X_"a[186:187], v38 offset:33792\n" \
"v_or_b32_e64 v12, v217, v221\n" \
M_"a[48:51], a[140:143], a[160:163], a[48:51]\n" \
"v_or_b32_e32 v217, 2, v219\n" \
"v_cmp_gt_u32_e32 vcc, v217, v214\n" \
M_"a[48:51], a[204:207], a[224:227], a[48:51]\n" \
"v_mov_b32_e32 v216, s25\n" \
"s_movk_i32 s40, 0xf400\n" \
M_"a[64:67], a[144:147], a[160:163], a[64:67]\n" \
X_"a[248:249], v38 offset:40960\n" \
"v_cndmask_b32_e64 v221, v215, 0, vcc\n" \
M_"a[64:67], a[208:211], a[224:227], a[64:67]\n" \
"v_cmp_lt_u32_e32 vcc, v217, v214\n" \
"s_lshl_b32 s4, s4, 6\n" \
M_"a[68:71], a[144:147], a[164:167], a[68:71]\n" \
"v_cndmask_b32_e32 v217, 0, v220, vcc\n" \
"v_or_b32_e32 v13, v221, v217\n" \
M_"a[68:71], a[208:211], a[228:231], a[68:71]\n" \
X_"a[250:251], v38 offset:41984\n" \
"v_or_b32_e64 v217, 16, v219\n" \
M_"a[72:75], a[144:147], a[168:171], a[72:75]\n" \
"v_cmp_gt_u32_e32 vcc, v217, v214\n" \
"s_nop 1\n" \
"v_cndmask_b32_e64 v221, v215, 0, vcc\n" \
M_"a[72:75], a[208:211], a[232:235], a[72:75]\n" \
"v_cmp_lt_u32_e32 vcc, v217, v214\n" \
"s_nop 1\n" \
"v_cndmask_b32_e64 v217, 0, v220, vcc\n" \
M_"a[76:79], a[144:147], a[172:175], a[76:79]\n" \
"v_or_b32_e32 v14, v221, v217\n" \
"v_or_b32_e32 v217, 18, v219\n" \
M_"a[76:79], a[208:211], a[236:239], a[76:79]\n" \
X_"a[188:189], v39 offset:32768\n" \
"v_cmp_gt_u32_e64 vcc, v217, v214\n" \
M_"a[92:95], a[148:151], a[172:175], a[92:95]\n" \
"s_nop 0\n" \
"v_cndmask_b32_e64 v215, v215, 0, vcc\n" \
"v_cmp_lt_u32_e32 vcc, v217, v214\n" \
M_"a[92:95], a[212:215], a[236:239], a[92:95]\n" \
"s_nop 0\n" \
"v_cndmask_b32_e32 v214, 0, v220, vcc\n" \
"v_or_b32_e64 v15, v215, v214\n" \
M_"a[88:91], a[148:151], a[168:171], a[88:91]\n" \
X_"a[190:191], v39 offset:33792\n" \
"v_mad_u64_u32 v[214:215], s[6:7], s5, 42, v[212:213]\n" \
M_"a[88:91], a[212:215], a[232:235], a[88:91]\n" \
"s_movk_i32 s5, 0x1440\n" \
"v_cmp_gt_u32_e32 vcc, s5, v214\n" \
M_"a[84:87], a[148:151], a[164:167], a[84:87]\n" \
"v_min_u32_e32 v217, 0xa43f, v214\n" \
"v_mov_b32_e64 v214, s24\n" \
M_"a[84:87], a[212:215], a[228:231], a[84:87]\n" \
X_"a[252:253], v39 offset:40960\n" \
"v_cndmask_b32_e64 v215, v216, v213, vcc\n" \
M_"a[80:83], a[148:151], a[160:163], a[80:83]\n" \
"v_mov_b32_e32 v213, s22\n" \
"s_movk_i32 s6, 0xffee\n" \
M_"a[80:83], a[212:215], a[224:227], a[80:83]\n" \
"v_cndmask_b32_e64 v214, v214, v213, vcc\n" \
"v_add_u32_e32 v213, 0xffffebc0, v217\n" \
M_"a[96:99], a[152:155], a[160:163], a[96:99]\n" \
X_"a[254:255], v39 offset:41984\n" \
"s_lshl_b32 s5, s2, 5\n" \
M_"a[96:99], a[216:219], a[224:227], a[96:99]\n" \
"v_cndmask_b32_e32 v213, v213, v217, vcc\n" \
"v_lshlrev_b32_e64 v216, 7, v213\n" \
M_"a[100:103], a[152:155], a[164:167], a[100:103]\n" \
"v_mul_u32_u24_e32 v213, 0xe39, v212\n" \
"v_mov_b32_e64 v217, 0\n" \
M_"a[100:103], a[216:219], a[228:231], a[100:103]\n" \
"v_lshrrev_b32_e64 v213, 16, v213\n" \
"v_lshl_add_u64 v[16:17], v[214:215], 0, v[216:217]\n" \
M_"a[104:107], a[152:155], a[168:171], a[104:107]\n" \
"v_mad_i32_i24 v214, v213, s6, v212\n" \
"s_add_i32 s5, s5, 32\n" \
M_"a[104:107], a[216:219], a[232:235], a[104:107]\n" \
"v_lshlrev_b32_e32 v215, 4, v214\n" \
"v_add_u32_e64 v216, s5, v215\n" \
M_"a[108:111], a[152:155], a[172:175], a[108:111]\n" \
"v_and_b32_e32 v216, 0xf0, v216\n" \
"v_cmp_gt_u32_e64 vcc, 16, v214\n" \
M_"a[108:111], a[216:219], a[236:239], a[108:111]\n" \
"s_movk_i32 s7, 0xf800\n" \
"v_cndmask_b32_e32 v214, v215, v216, vcc\n" \
"s_waitcnt lgkmcnt(15)\n" \
M_"a[124:127], a[156:159], a[172:175], a[124:127]\n" \
"v_or_b32_e32 v215, 64, v212\n" \
"v_mul_u32_u24_e32 v216, 0xe39, v215\n" \
M_"a[124:127], a[220:223], a[236:239], a[124:127]\n" \
"v_lshrrev_b32_e64 v216, 16, v216\n" \
"v_mad_i32_i24 v215, v216, s6, v215\n" \
M_"a[120:123], a[156:159], a[168:171], a[120:123]\n" \
"v_lshlrev_b32_e32 v217, 4, v215\n" \
"v_add_u32_e32 v220, s5, v217\n" \
M_"a[120:123], a[220:223], a[232:235], a[120:123]\n" \
"v_and_b32_e32 v220, 0xf0, v220\n" \
"v_cmp_gt_u32_e64 vcc, 16, v215\n" \
M_"a[116:119], a[156:159], a[164:167], a[116:119]\n" \
"s_nop 0\n" \
"v_cndmask_b32_e32 v215, v217, v220, vcc\n" \
"v_or_b32_e32 v217, 0x80, v212\n" \
M_"a[116:119], a[220:223], a[228:231], a[116:119]\n" \
"v_mul_u32_u24_e32 v220, 0xe39, v217\n" \
"v_lshrrev_b32_e64 v220, 16, v220\n" \
M_"a[112:115], a[156:159], a[160:163], a[112:115]\n" \
"v_mad_i32_i24 v217, v220, s6, v217\n" \
"v_lshlrev_b32_e64 v221, 4, v217\n" \
M_"a[112:115], a[220:223], a[224:227], a[112:115]\n" \
"s_waitcnt vmcnt(20)\n" \
"v_readfirstlane_b32 s62, v180\n" \
"s_nop 3\n" \
"s_add_u32 s62, s62, 0x9E3779B1\n" \
"s_waitcnt vmcnt(2) lgkmcnt(0)\n" \
"s_barrier\n" \
M_"v[52:55], a[128:131], a[176:179], v[52:55]\n" \
"s_add_u32 m0, s30, 0x8000\n" \
"s_movk_i32 s84, 0x180\n" \
M_"v[52:55], a[192:195], a[240:243], v[52:55]\n" \
B_"v22, s[64:67], s84 offen offset:0 lds\n" \
M_"v[56:59], a[128:131], a[180:183], v[56:59]\n" \
X_"a[160:161], v32 offset:0\n" \
"v_add_u32_e64 v222, s5, v221\n" \
M_"v[56:59], a[192:195], a[244:247], v[56:59]\n" \
"s_movk_i32 s84, 0x1d80\n" \
M_"v[60:63], a[128:131], a[184:187], v[60:63]\n" \
B_"v23, s[64:67], s84 offen offset:1024 lds\n" \
M_"v[60:63], a[192:195], a[248:251], v[60:63]\n" \
X_"a[162:163], v32 offset:1024\n" \
"v_and_b32_e32 v222, 0xf0, v222\n" \
M_"v[64:67], a[128:131], a[188:191], v[64:67]\n" \
X_"a[224:225], v32 offset:8192\n" \
"v_cmp_gt_u32_e32 vcc, 16, v217\n" \
M_"v[64:67], a[192:195], a[252:255], v[64:67]\n" \
R_"a[128:131], v28 offset:16384\n" \
"v_cndmask_b32_e64 v217, v221, v222, vcc\n" \
M_"v[80:83], a[132:135], a[188:191], v[80:83]\n" \
R_"a[192:195], v29 offset:16384\n" \
"v_or_b32_e32 v221, 0xc0, v212\n" \
M_"v[80:83], a[196:199], a[252:255], v[80:83]\n" \
"s_movk_i32 s84, 0x3980\n" \
M_"v[76:79], a[132:135], a[184:187], v[76:79]\n" \
B_"v22, s[64:67], s84 offen offset:2048 lds\n" \
M_"v[76:79], a[196:199], a[248:251], v[76:79]\n" \
"s_movk_i32 s84, 0x5580\n" \
M_"v[72:75], a[132:135], a[180:183], v[72:75]\n" \
B_"v23, s[64:67], s84 offen offset:3072 lds\n" \
M_"v[72:75], a[196:199], a[244:247], v[72:75]\n" \
"s_add_u32 m0, s31, 0x8000\n" \
"s_mov_b32 s84, 0x180000\n" \
M_"v[68:71], a[132:135], a[176:179], v[68:71]\n" \
B_"v26, s[68:71], s84 offen offset:0 lds\n" \
M_"v[68:71], a[196:199], a[240:243], v[68:71]\n" \
R_"a[132:135], v28 offset:18432\n" \
"v_mul_u32_u24_e32 v222, 0xe39, v221\n" \
M_"v[84:87], a[136:139], a[176:179], v[84:87]\n" \
R_"a[196:199], v29 offset:18432\n" \
"v_lshrrev_b32_e64 v222, 16, v222\n" \
M_"v[84:87], a[200:203], a[240:243], v[84:87]\n" \
"s_addk_i32 s84, 0x7c00\n" \
M_"v[88:91], a[136:139], a[180:183], v[88:91]\n" \
B_"v26, s[68:71], s84 offen offset:1024 lds\n" \
M_"v[88:91], a[200:203], a[244:247], v[88:91]\n" \
X_"a[226:227], v32 offset:9216\n" \
"v_mad_i32_i24 v221, v222, s6, v221\n" \
M_"v[92:95], a[136:139], a[184:187], v[92:95]\n" \
"s_addk_i32 s84, 0x7c00\n" \
M_"v[92:95], a[200:203], a[248:251], v[92:95]\n" \
B_"v27, s[68:71], s84 offen offset:2048 lds\n" \
M_"v[96:99], a[136:139], a[188:191], v[96:99]\n" \
X_"a[164:165], v33 offset:0\n" \
"v_lshlrev_b32_e64 v223, 4, v221\n" \
M_"v[96:99], a[200:203], a[252:255], v[96:99]\n" \
R_"a[136:139], v28 offset:20480\n" \
"v_add_u32_e64 v224, s5, v223\n" \
M_"v[112:115], a[140:143], a[188:191], v[112:115]\n" \
R_"a[200:203], v29 offset:20480\n" \
"v_and_b32_e32 v224, 0xf0, v224\n" \
M_"v[112:115], a[204:207], a[252:255], v[112:115]\n" \
"s_addk_i32 s84, 0x7c00\n" \
M_"v[108:111], a[140:143], a[184:187], v[108:111]\n" \
B_"v27, s[68:71], s84 offen offset:3072 lds\n" \
M_"v[108:111], a[204:207], a[248:251], v[108:111]\n" \
"s_add_u32 m0, s30, 0x9000\n" \
"s_mov_b32 s84, 0x8180\n" \
M_"v[104:107], a[140:143], a[180:183], v[104:107]\n" \
B_"v22, s[64:67], s84 offen offset:0 lds\n" \
M_"v[104:107], a[204:207], a[244:247], v[104:107]\n" \
X_"a[166:167], v33 offset:1024\n" \
"v_cmp_gt_u32_e32 vcc, 16, v221\n" \
M_"v[100:103], a[140:143], a[176:179], v[100:103]\n" \
X_"a[228:229], v33 offset:8192\n" \
"v_cndmask_b32_e64 v221, v223, v224, vcc\n" \
M_"v[100:103], a[204:207], a[240:243], v[100:103]\n" \
R_"a[140:143], v28 offset:22528\n" \
"v_or_b32_e32 v223, 0x100, v212\n" \
M_"v[116:119], a[144:147], a[176:179], v[116:119]\n" \
R_"a[204:207], v29 offset:22528\n" \
"v_mul_u32_u24_e32 v224, 0xe39, v223\n" \
M_"v[116:119], a[208:211], a[240:243], v[116:119]\n" \
"s_addk_i32 s84, 0x1c00\n" \
M_"v[120:123], a[144:147], a[180:183], v[120:123]\n" \
B_"v23, s[64:67], s84 offen offset:1024 lds\n" \
M_"v[120:123], a[208:211], a[244:247], v[120:123]\n" \
"s_addk_i32 s84, 0x1c00\n" \
M_"v[124:127], a[144:147], a[184:187], v[124:127]\n" \
B_"v22, s[64:67], s84 offen offset:2048 lds\n" \
M_"v[124:127], a[208:211], a[248:251], v[124:127]\n" \
"s_addk_i32 s84, 0x1c00\n" \
M_"v[128:131], a[144:147], a[188:191], v[128:131]\n" \
B_"v23, s[64:67], s84 offen offset:3072 lds\n" \
M_"v[128:131], a[208:211], a[252:255], v[128:131]\n" \
R_"a[144:147], v28 offset:0\n" \
"v_lshrrev_b32_e32 v224, 16, v224\n" \
M_"v[144:147], a[148:151], a[188:191], v[144:147]\n" \
R_"a[208:211], v29 offset:0\n" \
"v_mad_i32_i24 v223, v224, s6, v223\n" \
M_"v[144:147], a[212:215], a[252:255], v[144:147]\n" \
"s_add_u32 m0, s31, 0xc000\n" \
"s_mov_b32 s84, 0x180000\n" \
M_"v[140:143], a[148:151], a[184:187], v[140:143]\n" \
B_"v26, s[72:75], s84 offen offset:0 lds\n" \
M_"v[140:143], a[212:215], a[248:251], v[140:143]\n" \
X_"a[230:231], v33 offset:9216\n" \
"v_lshlrev_b32_e64 v225, 4, v223\n" \
M_"v[136:139], a[148:151], a[180:183], v[136:139]\n" \
"s_addk_i32 s84, 0x7c00\n" \
M_"v[136:139], a[212:215], a[244:247], v[136:139]\n" \
B_"v26, s[72:75], s84 offen offset:1024 lds\n" \
M_"v[132:135], a[148:151], a[176:179], v[132:135]\n" \
X_"a[168:169], v34 offset:0\n" \
"v_add_u32_e32 v226, s5, v225\n" \
M_"v[132:135], a[212:215], a[240:243], v[132:135]\n" \
R_"a[148:151], v28 offset:2048\n" \
"v_and_b32_e32 v226, 0xf0, v226\n" \
M_"v[148:151], a[152:155], a[176:179], v[148:151]\n" \
R_"a[212:215], v29 offset:2048\n" \
"v_cmp_gt_u32_e64 vcc, 16, v223\n" \
M_"v[148:151], a[216:219], a[240:243], v[148:151]\n" \
X_"a[170:171], v34 offset:1024\n" \
"v_cndmask_b32_e64 v223, v225, v226, vcc\n" \
M_"v[152:155], a[152:155], a[180:183], v[152:155]\n" \
X_"a[232:233], v34 offset:8192\n" \
"v_or_b32_e32 v225, 0x140, v212\n" \
M_"v[152:155], a[216:219], a[244:247], v[152:155]\n" \
X_"a[234:235], v34 offset:9216\n" \
"v_mul_u32_u24_e32 v226, 0xe39, v225\n" \
M_"v[156:159], a[152:155], a[184:187], v[156:159]\n" \
X_"a[172:173], v35 offset:0\n" \
"v_lshrrev_b32_e64 v226, 16, v226\n" \
M_"v[156:159], a[216:219], a[248:251], v[156:159]\n" \
X_"a[174:175], v35 offset:1024\n" \
"v_mad_i32_i24 v225, v226, s6, v225\n" \
M_"v[160:163], a[152:155], a[188:191], v[160:163]\n" \
X_"a[236:237], v35 offset:8192\n" \
"v_lshlrev_b32_e64 v227, 4, v225\n" \
M_"v[160:163], a[216:219], a[252:255], v[160:163]\n" \
R_"a[152:155], v28 offset:4096\n" \
"v_add_u32_e64 v228, s5, v227\n" \
M_"v[176:179], a[156:159], a[188:191], v[176:179]\n" \
R_"a[216:219], v29 offset:4096\n" \
"v_and_b32_e32 v228, 0xf0, v228\n" \
M_"v[176:179], a[220:223], a[252:255], v[176:179]\n" \
X_"a[238:239], v35 offset:9216\n" \
"v_cmp_gt_u32_e64 vcc, 16, v225\n" \
M_"v[172:175], a[156:159], a[184:187], v[172:175]\n" \
"s_nop 0\n" \
"v_cndmask_b32_e32 v225, v227, v228, vcc\n" \
"v_or_b32_e32 v227, 0x180, v212\n" \
M_"v[172:175], a[220:223], a[248:251], v[172:175]\n" \
"v_mul_u32_u24_e32 v228, 0xe39, v227\n" \
"v_lshrrev_b32_e64 v228, 16, v228\n" \
M_"v[168:171], a[156:159], a[180:183], v[168:171]\n" \
"v_mad_i32_i24 v227, v228, s6, v227\n" \
"v_lshlrev_b32_e64 v229, 4, v227\n" \
M_"v[168:171], a[220:223], a[244:247], v[168:171]\n" \
"v_add_u32_e64 v230, s5, v229\n" \
"v_and_b32_e32 v230, 0xf0, v230\n" \
M_"v[164:167], a[156:159], a[176:179], v[164:167]\n" \
"v_cmp_gt_u32_e32 vcc, 16, v227\n" \
"s_nop 1\n" \
"v_cndmask_b32_e64 v227, v229, v230, vcc\n" \
M_"v[164:167], a[220:223], a[240:243], v[164:167]\n" \
R_"a[156:159], v28 offset:6144\n" \
R_"a[220:223], v29 offset:6144\n" \
"s_waitcnt lgkmcnt(15)\n" \
M_"a[0:3], a[128:131], a[160:163], a[0:3]\n" \
"s_addk_i32 s84, 0x7c00\n" \
M_"a[0:3], a[192:195], a[224:227], a[0:3]\n" \
B_"v27, s[72:75], s84 offen offset:2048 lds\n" \
M_"a[4:7], a[128:131], a[164:167], a[4:7]\n" \
X_"a[176:177], v36 offset:0\n" \
"v_or_b32_e32 v229, 0x1c0, v212\n" \
"s_waitcnt lgkmcnt(15)\n" \
M_"a[4:7], a[192:195], a[228:231], a[4:7]\n" \
"s_addk_i32 s84, 0x7c00\n" \
"s_waitcnt lgkmcnt(11)\n" \
M_"a[8:11], a[128:131], a[168:171], a[8:11]\n" \
B_"v27, s[72:75], s84 offen offset:3072 lds\n" \
"s_waitcnt lgkmcnt(9)\n" \
M_"a[8:11], a[192:195], a[232:235], a[8:11]\n" \
X_"a[178:179], v36 offset:1024\n" \
"v_mul_u32_u24_e32 v230, 0xe39, v229\n" \
"s_waitcnt lgkmcnt(8)\n" \
M_"a[12:15], a[128:131], a[172:175], a[12:15]\n" \
X_"a[240:241], v36 offset:8192\n" \
"v_lshrrev_b32_e32 v230, 16, v230\n" \
"s_waitcnt lgkmcnt(5)\n" \
M_"a[12:15], a[192:195], a[236:239], a[12:15]\n" \
"v_mad_i32_i24 v229, v230, s6, v229\n" \
"v_lshlrev_b32_e32 v231, 4, v229\n" \
M_"a[28:31], a[132:135], a[172:175], a[28:31]\n" \
"v_add_u32_e64 v232, s5, v231\n" \
"v_and_b32_e32 v232, 0xf0, v232\n" \
M_"a[28:31], a[196:199], a[236:239], a[28:31]\n" \
X_"a[242:243], v36 offset:9216\n" \
"v_cmp_gt_u32_e64 vcc, 16, v229\n" \
M_"a[24:27], a[132:135], a[168:171], a[24:27]\n" \
B_"v[200:203], v192, s[76:79], s34 offen offset:0\n" \
M_"a[24:27], a[196:199], a[232:235], a[24:27]\n" \
"v_cndmask_b32_e64 v229, v231, v232, vcc\n" \
"v_or_b32_e32 v231, 0x200, v212\n" \
M_"a[20:23], a[132:135], a[164:167], a[20:23]\n" \
"v_mul_u32_u24_e32 v232, 0xe39, v231\n" \
"v_lshrrev_b32_e64 v232, 16, v232\n" \
M_"a[20:23], a[196:199], a[228:231], a[20:23]\n" \
X_"a[180:181], v37 offset:0\n" \
"v_mad_i32_i24 v231, v232, s6, v231\n" \
M_"a[16:19], a[132:135], a[160:163], a[16:19]\n" \
"v_lshlrev_b32_e32 v233, 4, v231\n" \
"v_add_u32_e32 v234, s5, v233\n" \
M_"a[16:19], a[196:199], a[224:227], a[16:19]\n" \
"s_movk_i32 s5, 0x480\n" \
"v_mad_u32_u24 v0, v213, s5, v214\n" \
M_"a[32:35], a[136:139], a[160:163], a[32:35]\n" \
B_"v[204:207], v193, s[76:79], s34 offen offset:0\n" \
M_"a[32:35], a[200:203], a[224:227], a[32:35]\n" \
"s_mov_b32 m0, s32\n" \
X_"a[182:183], v37 offset:1024\n" \
M_"a[36:39], a[136:139], a[164:167], a[36:39]\n" \
"s_mov_b32 exec_lo, 0x0\n" \
"s_mov_b32 exec_hi, 0xff00\n" \
"global_load_lds_dword v[184:185], off\n" \
"s_mov_b64 exec, -1\n" \
M_"a[36:39], a[200:203], a[228:231], a[36:39]\n" \
X_"a[244:245], v37 offset:8192\n" \
"v_mul_u32_u24_e32 v213, 0x480, v216\n" \
M_"a[40:43], a[136:139], a[168:171], a[40:43]\n" \
"s_movk_i32 s6, 0xfc00\n" \
"s_nop 0\n" \
"v_add3_u32 v1, v213, v215, s6\n" \
M_"a[40:43], a[200:203], a[232:235], a[40:43]\n" \
"buffer_load_dword v208, v194, s[76:79], s34 offen offset:32\n" \
M_"a[44:47], a[136:139], a[172:175], a[44:47]\n" \
X_"a[246:247], v37 offset:9216\n" \
"v_mul_u32_u24_e32 v213, 0x480, v220\n" \
M_"a[44:47], a[200:203], a[236:239], a[44:47]\n" \
"v_add3_u32 v2, v213, v217, s7\n" \
"v_mul_u32_u24_e32 v213, 0x480, v222\n" \
M_"a[60:63], a[140:143], a[172:175], a[60:63]\n" \
"v_add3_u32 v3, v213, v221, s40\n" \
"v_mul_u32_u24_e32 v213, 0x480, v226\n" \
M_"a[60:63], a[204:207], a[236:239], a[60:63]\n" \
"v_add3_u32 v5, v213, v225, s6\n" \
"v_mul_u32_u24_e32 v213, 0x480, v228\n" \
M_"a[56:59], a[140:143], a[168:171], a[56:59]\n" \
"buffer_load_dword v209, v194, s[76:79], s34 offen offset:1184\n" \
M_"a[56:59], a[204:207], a[232:235], a[56:59]\n" \
X_"a[184:185], v38 offset:0\n" \
"v_add3_u32 v6, v213, v227, s7\n" \
M_"a[52:55], a[140:143], a[164:167], a[52:55]\n" \
"v_mul_u32_u24_e32 v213, 0x480, v230\n" \
"v_add3_u32 v7, v213, v229, s40\n" \
M_"a[52:55], a[204:207], a[228:231], a[52:55]\n" \
X_"a[186:187], v38 offset:1024\n" \
"s_lshl_b32 s7, s3, 3\n" \
M_"a[48:51], a[140:143], a[160:163], a[48:51]\n" \
"s_lshl_b32 s40, s3, 1\n" \
"s_and_b32 s7, s7, 16\n" \
M_"a[48:51], a[204:207], a[224:227], a[48:51]\n" \
"s_and_b32 s40, s40, 2\n" \
"s_and_b32 s6, s2, 7\n" \
M_"a[64:67], a[144:147], a[160:163], a[64:67]\n" \
"buffer_load_dword v211, v194, s[76:79], s34 offen offset:2336\n" \
M_"a[64:67], a[208:211], a[224:227], a[64:67]\n" \
X_"a[248:249], v38 offset:8192\n" \
"v_bfe_u32 v213, v20, 7, 1\n" \
M_"a[68:71], a[144:147], a[164:167], a[68:71]\n" \
"s_or_b32 s7, s40, s7\n" \
"v_and_b32_e32 v234, 0xf0, v234\n" \
M_"a[68:71], a[208:211], a[228:231], a[68:71]\n" \
X_"a[250:251], v38 offset:9216\n" \
"v_cmp_gt_u32_e64 vcc, 16, v231\n" \
M_"a[72:75], a[144:147], a[168:171], a[72:75]\n" \
"v_or3_b32 v213, s7, v213, v219\n" \
"v_and_b32_e64 v214, 32, v20\n" \
M_"a[72:75], a[208:211], a[232:235], a[72:75]\n" \
"s_mov_b32 s76, s20\n" \
"s_and_b32 s77, s21, 0xffff\n" \
M_"a[76:79], a[144:147], a[172:175], a[76:79]\n" \
"s_mov_b32 s78, 0x480\n" \
"s_lshl_b32 s6, s6, 5\n" \
M_"a[76:79], a[208:211], a[236:239], a[76:79]\n" \
"buffer_load_ushort v210, v195, s[76:79], 0 offen\n" \
".fill (L_pb + 48864 - .) / 4, 4, 0xBF800000\n" \
"L_pp12:\n" \
"s_cbranch_execz L_pret\n" \
M_"a[92:95], a[148:151], a[172:175], a[92:95]\n" \
X_"a[188:189], v39 offset:0\n" \
"v_cndmask_b32_e32 v231, v233, v234, vcc\n" \
M_"a[92:95], a[212:215], a[236:239], a[92:95]\n" \
"v_mul_u32_u24_e64 v213, 18, v213\n" \
"v_mov_b32_e32 v215, 0x100\n" \
M_"a[88:91], a[148:151], a[168:171], a[88:91]\n" \
"s_mov_b32 s76, s8\n" \
"s_and_b32 s77, s9, 0xffff\n" \
M_"a[88:91], a[212:215], a[232:235], a[88:91]\n" \
"s_mov_b32 s78, 0x240000\n" \
X_"a[190:191], v39 offset:1024\n" \
M_"a[84:87], a[148:151], a[164:167], a[84:87]\n" \
"v_mov_b32_e32 v216, s6\n" \
"v_cmp_eq_u32_e64 vcc, 0, v214\n" \
M_"a[84:87], a[212:215], a[228:231], a[84:87]\n" \
X_"a[252:253], v39 offset:8192\n" \
"v_and_or_b32 v213, v218, 1, v213\n" \
M_"a[80:83], a[148:151], a[160:163], a[80:83]\n" \
"v_lshlrev_b32_e32 v212, 3, v212\n" \
"v_cndmask_b32_e32 v214, v215, v216, vcc\n" \
M_"a[80:83], a[212:215], a[224:227], a[80:83]\n" \
"v_mad_u32_u24 v213, v213, s5, v214\n" \
"v_and_or_b32 v213, v20, 16, v213\n" \
M_"a[96:99], a[152:155], a[160:163], a[96:99]\n" \
X_"a[254:255], v39 offset:9216\n" \
"v_add_u32_e32 v9, 0x4800, v213\n" \
M_"a[96:99], a[216:219], a[224:227], a[96:99]\n" \
"v_mul_u32_u24_e32 v213, 0x120, v218\n" \
"v_and_or_b32 v10, v212, 24, v213\n" \
M_"a[100:103], a[152:155], a[164:167], a[100:103]\n" \
"v_lshrrev_b32_e64 v213, 2, v20\n" \
"v_and_or_b32 v213, v213, 60, s4\n" \
M_"a[100:103], a[216:219], a[228:231], a[100:103]\n" \
"v_lshl_or_b32 v212, s3, 9, v212\n" \
"v_mad_u32_u24 v4, v224, s5, v223\n" \
M_"a[104:107], a[152:155], a[168:171], a[104:107]\n" \
"v_mad_u32_u24 v8, v232, s5, v231\n" \
"v_or_b32_e32 v18, 0x27000, v213\n" \
M_"a[104:107], a[216:219], a[232:235], a[104:107]\n" \
"v_or_b32_e32 v19, 0x27800, v212\n" \
"v_mov_b32_e32 v215, 0x1000c0c\n" \
M_"a[108:111], a[152:155], a[172:175], a[108:111]\n" \
"v_mov_b32_e32 v216, 0xc0c0100\n" \
"v_bfe_u32 v214, v20, 4, 1\n" \
M_"a[108:111], a[216:219], a[236:239], a[108:111]\n" \
"v_lshrrev_b32_e32 v212, 8, v20\n" \
"v_cmp_eq_u32_e32 vcc, 0, v214\n" \
"s_waitcnt lgkmcnt(15)\n" \
M_"a[124:127], a[156:159], a[172:175], a[124:127]\n" \
"v_bfe_u32 v213, v20, 4, 4\n" \
"v_mov_b32_e32 v217, 0xc0c0302\n" \
M_"a[124:127], a[220:223], a[236:239], a[124:127]\n" \
"v_cndmask_b32_e32 v215, v215, v216, vcc\n" \
"v_mov_b32_e32 v216, 0x3020c0c\n" \
M_"a[120:123], a[156:159], a[168:171], a[120:123]\n" \
"v_lshl_or_b32 v214, v212, 2, v214\n" \
"v_cndmask_b32_e64 v216, v216, v217, vcc\n" \
M_"a[120:123], a[220:223], a[232:235], a[120:123]\n" \
"v_mov_b32_e32 v217, 0xc0c0c0c\n" \
"v_cmp_gt_u32_e64 vcc, v214, v213\n" \
M_"a[116:119], a[156:159], a[164:167], a[116:119]\n" \
"v_or_b32_e32 v214, 2, v214\n" \
"s_lshr_b32 s4, s2, 5\n" \
M_"a[116:119], a[220:223], a[228:231], a[116:119]\n" \
"v_cndmask_b32_e32 v40, v217, v215, vcc\n" \
"v_cndmask_b32_e32 v42, v215, v217, vcc\n" \
M_"a[112:115], a[156:159], a[160:163], a[112:115]\n" \
"v_cmp_gt_u32_e32 vcc, v214, v213\n" \
"v_and_b32_e32 v214, 16, v20\n" \
"s_waitcnt vmcnt(7) lgkmcnt(0)\n" \
M_"a[112:115], a[220:223], a[224:227], a[112:115]\n" \
"s_barrier\n" \
M_"v[52:55], a[128:131], a[176:179], v[52:55]\n" \
"s_add_u32 m0, s30, 0x0\n" \
"s_movk_i32 s84, 0x200\n" \
M_"v[52:55], a[192:195], a[240:243], v[52:55]\n" \
B_"v22, s[64:67], s84 offen offset:0 lds\n" \
M_"v[56:59], a[128:131], a[180:183], v[56:59]\n" \
X_"a[160:161], v32 offset:32768\n" \
"s_movk_i32 s42, 0x3f80\n" \
M_"v[56:59], a[192:195], a[244:247], v[56:59]\n" \
"s_movk_i32 s84, 0x1e00\n" \
M_"v[60:63], a[128:131], a[184:187], v[60:63]\n" \
B_"v23, s[64:67], s84 offen offset:1024 lds\n" \
M_"v[60:63], a[192:195], a[248:251], v[60:63]\n" \
X_"a[162:163], v32 offset:33792\n" \
"s_xor_b32 s5, s4, 15\n" \
M_"v[64:67], a[128:131], a[188:191], v[64:67]\n" \
X_"a[224:225], v32 offset:40960\n" \
M_"v[64:67], a[192:195], a[252:255], v[64:67]\n" \
R_"a[128:131], v28 offset:49152\n" \
M_"v[80:83], a[132:135], a[188:191], v[80:83]\n" \
R_"a[192:195], v29 offset:49152\n" \
M_"v[80:83], a[196:199], a[252:255], v[80:83]\n" \
"s_movk_i32 s84, 0x3a00\n" \
M_"v[76:79], a[132:135], a[184:187], v[76:79]\n" \
B_"v22, s[64:67], s84 offen offset:2048 lds\n" \
M_"v[76:79], a[196:199], a[248:251], v[76:79]\n" \
"s_movk_i32 s84, 0x5600\n" \
M_"v[72:75], a[132:135], a[180:183], v[72:75]\n" \
B_"v23, s[64:67], s84 offen offset:3072 lds\n" \
M_"v[72:75], a[196:199], a[244:247], v[72:75]\n" \
"s_add_u32 m0, s31, 0x0\n" \
"s_mov_b32 s84, 0x200000\n" \
M_"v[68:71], a[132:135], a[176:179], v[68:71]\n" \
B_"v26, s[68:71], s84 offen offset:0 lds\n" \
M_"v[68:71], a[196:199], a[240:243], v[68:71]\n" \
R_"a[132:135], v28 offset:51200\n" \
M_"v[84:87], a[136:139], a[176:179], v[84:87]\n" \
R_"a[196:199], v29 offset:51200\n" \
M_"v[84:87], a[200:203], a[240:243], v[84:87]\n" \
"s_addk_i32 s84, 0x7c00\n" \
M_"v[88:91], a[136:139], a[180:183], v[88:91]\n" \
B_"v26, s[68:71], s84 offen offset:1024 lds\n" \
M_"v[88:91], a[200:203], a[244:247], v[88:91]\n" \
X_"a[226:227], v32 offset:41984\n" \
M_"v[92:95], a[136:139], a[184:187], v[92:95]\n" \
"s_addk_i32 s84, 0x7c00\n" \
M_"v[92:95], a[200:203], a[248:251], v[92:95]\n" \
B_"v27, s[68:71], s84 offen offset:2048 lds\n" \
M_"v[96:99], a[136:139], a[188:191], v[96:99]\n" \
X_"a[164:165], v33 offset:32768\n" \
M_"v[96:99], a[200:203], a[252:255], v[96:99]\n" \
R_"a[136:139], v28 offset:53248\n" \
M_"v[112:115], a[140:143], a[188:191], v[112:115]\n" \
R_"a[200:203], v29 offset:53248\n" \
M_"v[112:115], a[204:207], a[252:255], v[112:115]\n" \
"s_addk_i32 s84, 0x7c00\n" \
"s_nop 0\n" \
M_"v[108:111], a[140:143], a[184:187], v[108:111]\n" \
B_"v27, s[68:71], s84 offen offset:3072 lds\n" \
M_"v[108:111], a[204:207], a[248:251], v[108:111]\n" \
"s_add_u32 m0, s30, 0x1000\n" \
"s_mov_b32 s84, 0x8200\n" \
M_"v[104:107], a[140:143], a[180:183], v[104:107]\n" \
B_"v22, s[64:67], s84 offen offset:0 lds\n" \
M_"v[104:107], a[204:207], a[244:247], v[104:107]\n" \
X_"a[166:167], v33 offset:33792\n" \
M_"v[100:103], a[140:143], a[176:179], v[100:103]\n" \
X_"a[228:229], v33 offset:40960\n" \
M_"v[100:103], a[204:207], a[240:243], v[100:103]\n" \
R_"a[140:143], v28 offset:55296\n" \
M_"v[116:119], a[144:147], a[176:179], v[116:119]\n" \
R_"a[204:207], v29 offset:55296\n" \
M_"v[116:119], a[208:211], a[240:243], v[116:119]\n" \
"s_addk_i32 s84, 0x1c00\n" \
M_"v[120:123], a[144:147], a[180:183], v[120:123]\n" \
B_"v23, s[64:67], s84 offen offset:1024 lds\n" \
M_"v[120:123], a[208:211], a[244:247], v[120:123]\n" \
"s_addk_i32 s84, 0x1c00\n" \
M_"v[124:127], a[144:147], a[184:187], v[124:127]\n" \
B_"v22, s[64:67], s84 offen offset:2048 lds\n" \
M_"v[124:127], a[208:211], a[248:251], v[124:127]\n" \
"s_addk_i32 s84, 0x1c00\n" \
"s_nop 0\n" \
M_"v[128:131], a[144:147], a[188:191], v[128:131]\n" \
B_"v23, s[64:67], s84 offen offset:3072 lds\n" \
M_"v[128:131], a[208:211], a[252:255], v[128:131]\n" \
R_"a[144:147], v28 offset:32768\n" \
M_"v[144:147], a[148:151], a[188:191], v[144:147]\n" \
R_"a[208:211], v29 offset:32768\n" \
M_"v[144:147], a[212:215], a[252:255], v[144:147]\n" \
"s_add_u32 m0, s31, 0x4000\n" \
"s_mov_b32 s84, 0x200000\n" \
M_"v[140:143], a[148:151], a[184:187], v[140:143]\n" \
B_"v26, s[72:75], s84 offen offset:0 lds\n" \
M_"v[140:143], a[212:215], a[248:251], v[140:143]\n" \
X_"a[230:231], v33 offset:41984\n" \
M_"v[136:139], a[148:151], a[180:183], v[136:139]\n" \
"s_addk_i32 s84, 0x7c00\n" \
"s_nop 0\n" \
M_"v[136:139], a[212:215], a[244:247], v[136:139]\n" \
B_"v26, s[72:75], s84 offen offset:1024 lds\n" \
M_"v[132:135], a[148:151], a[176:179], v[132:135]\n" \
X_"a[168:169], v34 offset:32768\n" \
M_"v[132:135], a[212:215], a[240:243], v[132:135]\n" \
R_"a[148:151], v28 offset:34816\n" \
M_"v[148:151], a[152:155], a[176:179], v[148:151]\n" \
R_"a[212:215], v29 offset:34816\n" \
M_"v[148:151], a[216:219], a[240:243], v[148:151]\n" \
X_"a[170:171], v34 offset:33792\n" \
M_"v[152:155], a[152:155], a[180:183], v[152:155]\n" \
X_"a[232:233], v34 offset:40960\n" \
M_"v[152:155], a[216:219], a[244:247], v[152:155]\n" \
X_"a[234:235], v34 offset:41984\n" \
M_"v[156:159], a[152:155], a[184:187], v[156:159]\n" \
X_"a[172:173], v35 offset:32768\n" \
M_"v[156:159], a[216:219], a[248:251], v[156:159]\n" \
X_"a[174:175], v35 offset:33792\n" \
M_"v[160:163], a[152:155], a[188:191], v[160:163]\n" \
X_"a[236:237], v35 offset:40960\n" \
M_"v[160:163], a[216:219], a[252:255], v[160:163]\n" \
R_"a[152:155], v28 offset:36864\n" \
M_"v[176:179], a[156:159], a[188:191], v[176:179]\n" \
R_"a[216:219], v29 offset:36864\n" \
M_"v[176:179], a[220:223], a[252:255], v[176:179]\n" \
X_"a[238:239], v35 offset:41984\n" \
M_"v[172:175], a[156:159], a[184:187], v[172:175]\n" \
"s_cmpk_lt_u32 s3, 2\n" \
"s_cselect_b32 s6, s5, s4\n" \
M_"v[172:175], a[220:223], a[248:251], v[172:175]\n" \
"s_lshl_b32 s7, s6, 7\n" \
"v_and_b32_e32 v213, 0x320, v20\n" \
M_"v[168:171], a[156:159], a[180:183], v[168:171]\n" \
"s_movk_i32 s41, 0x100\n" \
"v_lshlrev_b32_e64 v214, v214, s42\n" \
M_"v[168:171], a[220:223], a[244:247], v[168:171]\n" \
"v_mov_b32_e32 v215, 0\n" \
"s_add_i32 s40, s7, -16\n" \
M_"v[164:167], a[156:159], a[176:179], v[164:167]\n" \
"v_cndmask_b32_e32 v41, v217, v216, vcc\n" \
"v_cndmask_b32_e32 v43, v216, v217, vcc\n" \
M_"v[164:167], a[220:223], a[240:243], v[164:167]\n" \
R_"a[156:159], v28 offset:38912\n" \
R_"a[220:223], v29 offset:38912\n" \
"s_waitcnt lgkmcnt(15)\n" \
M_"a[0:3], a[128:131], a[160:163], a[0:3]\n" \
"s_addk_i32 s84, 0x7c00\n" \
M_"a[0:3], a[192:195], a[224:227], a[0:3]\n" \
B_"v27, s[72:75], s84 offen offset:2048 lds\n" \
M_"a[4:7], a[128:131], a[164:167], a[4:7]\n" \
X_"a[176:177], v36 offset:32768\n" \
"v_cmp_eq_u32_e32 vcc, s41, v213\n" \
"s_waitcnt lgkmcnt(15)\n" \
M_"a[4:7], a[192:195], a[228:231], a[4:7]\n" \
"s_addk_i32 s84, 0x7c00\n" \
"s_waitcnt lgkmcnt(11)\n" \
M_"a[8:11], a[128:131], a[168:171], a[8:11]\n" \
B_"v27, s[72:75], s84 offen offset:3072 lds\n" \
"s_waitcnt lgkmcnt(9)\n" \
M_"a[8:11], a[192:195], a[232:235], a[8:11]\n" \
X_"a[178:179], v36 offset:33792\n" \
"s_mov_b32 s41, 0\n" \
"s_waitcnt lgkmcnt(8)\n" \
M_"a[12:15], a[128:131], a[172:175], a[12:15]\n" \
X_"a[240:241], v36 offset:40960\n" \
"s_and_b32 s43, s3, 1\n" \
"s_waitcnt lgkmcnt(5)\n" \
M_"a[12:15], a[192:195], a[236:239], a[12:15]\n" \
"v_cndmask_b32_e32 v46, v215, v214, vcc\n" \
"v_and_b32_e32 v213, 0x330, v20\n" \
M_"a[28:31], a[132:135], a[172:175], a[28:31]\n" \
"s_cmp_lg_u32 s7, 0\n" \
"s_cselect_b32 s40, s40, s41\n" \
M_"a[28:31], a[196:199], a[236:239], a[28:31]\n" \
X_"a[242:243], v36 offset:41984\n" \
"s_mov_b32 s42, 0xff800000\n" \
M_"a[24:27], a[132:135], a[168:171], a[24:27]\n" \
"s_cmp_lg_u32 s7, 0\n" \
"s_cselect_b32 s47, s41, s42\n" \
M_"a[24:27], a[196:199], a[232:235], a[24:27]\n" \
"s_lshl_b32 s41, s43, 6\n" \
"v_lshrrev_b32_e32 v215, 6, v20\n" \
M_"a[20:23], a[132:135], a[164:167], a[20:23]\n" \
"v_mov_b32_e32 v214, 0x3f80\n" \
"v_cmp_eq_u32_e64 vcc, 32, v213\n" \
M_"a[20:23], a[196:199], a[228:231], a[20:23]\n" \
X_"a[180:181], v37 offset:32768\n" \
"s_or_b32 s7, s41, s7\n" \
M_"a[16:19], a[132:135], a[160:163], a[16:19]\n" \
"v_lshrrev_b32_e32 v213, 7, v20\n" \
"v_bfe_u32 v216, v20, 6, 2\n" \
M_"a[16:19], a[196:199], a[224:227], a[16:19]\n" \
"v_lshrrev_b32_e64 v219, 1, v20\n" \
"v_bfe_u32 v215, v215, 1, 1\n" \
M_"a[32:35], a[136:139], a[160:163], a[32:35]\n" \
X_"a[182:183], v37 offset:33792\n" \
"v_cndmask_b32_e64 v47, 0, v214, vcc\n" \
M_"a[32:35], a[200:203], a[224:227], a[32:35]\n" \
"v_or_b32_e64 v214, s7, v213\n" \
"v_lshl_or_b32 v217, s43, 3, v213\n" \
M_"a[36:39], a[136:139], a[164:167], a[36:39]\n" \
"v_lshlrev_b32_e32 v218, 10, v212\n" \
"v_lshlrev_b32_e32 v216, 7, v216\n" \
M_"a[36:39], a[200:203], a[228:231], a[36:39]\n" \
X_"a[244:245], v37 offset:40960\n" \
"v_and_b32_e64 v219, 24, v219\n" \
M_"a[40:43], a[136:139], a[168:171], a[40:43]\n" \
"s_mov_b32 m0, s32\n" \
"v_and_or_b32 v213, v213, 2, v215\n" \
M_"a[40:43], a[200:203], a[232:235], a[40:43]\n" \
"s_mov_b32 exec_lo, 0xffffffff\n" \
"s_mov_b32 exec_hi, 0x0\n" \
"global_load_lds_dword v[188:189], off\n" \
"s_mov_b64 exec, -1\n" \
M_"a[44:47], a[136:139], a[172:175], a[44:47]\n" \
X_"a[246:247], v37 offset:41984\n" \
"v_add_u32_e64 v217, s40, v217\n" \
M_"a[44:47], a[200:203], a[236:239], a[44:47]\n" \
"v_or3_b32 v216, v216, v218, v219\n" \
"v_lshlrev_b32_e64 v213, 5, v213\n" \
M_"a[60:63], a[140:143], a[172:175], a[60:63]\n" \
"s_mov_b32 s40, 0x14000\n" \
"s_lshl_b32 s7, s2, 2\n" \
M_"a[60:63], a[204:207], a[236:239], a[60:63]\n" \
"v_or3_b32 v48, v216, v213, s40\n" \
"s_lshl_b32 s40, s43, 2\n" \
M_"a[56:59], a[140:143], a[168:171], a[56:59]\n" \
X_"a[184:185], v38 offset:32768\n" \
"s_lshl_b32 s6, s6, 3\n" \
M_"a[56:59], a[204:207], a[232:235], a[56:59]\n" \
"v_or_b32_e32 v213, s40, v212\n" \
"s_or_b32 s6, s6, s40\n" \
M_"a[52:55], a[140:143], a[164:167], a[52:55]\n" \
"s_and_b32 s40, s7, 0x7c\n" \
"s_or_b32 s40, s40, s3\n" \
M_"a[52:55], a[204:207], a[228:231], a[52:55]\n" \
X_"a[186:187], v38 offset:33792\n" \
M_"a[48:51], a[140:143], a[160:163], a[48:51]\n" \
"s_cmpk_lt_u32 s40, 64\n" \
"s_cselect_b32 s41, s4, s5\n" \
M_"a[48:51], a[204:207], a[224:227], a[48:51]\n" \
"s_lshl_b32 s40, s40, 1\n" \
"s_lshl_b32 s41, s41, 7\n" \
M_"a[64:67], a[144:147], a[160:163], a[64:67]\n" \
X_"a[248:249], v38 offset:40960\n" \
"s_and_b32 s40, s40, 0x7e\n" \
M_"a[64:67], a[208:211], a[224:227], a[64:67]\n" \
"s_or_b32 s40, s41, s40\n" \
"s_mulk_i32 s40, 0x480\n" \
M_"a[68:71], a[144:147], a[164:167], a[68:71]\n" \
"v_add_u32_e64 v49, s40, v20\n" \
"s_mov_b32 s40, 0xc000\n" \
M_"a[68:71], a[208:211], a[228:231], a[68:71]\n" \
X_"a[250:251], v38 offset:41984\n" \
"s_and_b32 s7, s7, 28\n" \
M_"a[72:75], a[144:147], a[168:171], a[72:75]\n" \
"s_bfe_u32 s45, s2, 0x20003\n" \
"s_mov_b32 s41, 0x8000\n" \
M_"a[72:75], a[208:211], a[232:235], a[72:75]\n" \
"s_cmp_lg_u32 s4, 0\n" \
"s_cselect_b32 s51, s40, s41\n" \
M_"a[76:79], a[144:147], a[172:175], a[76:79]\n" \
"s_movk_i32 s40, 0x4000\n" \
"s_lshr_b32 s44, s3, 1\n" \
M_"a[76:79], a[208:211], a[236:239], a[76:79]\n" \
X_"a[188:189], v39 offset:32768\n" \
"s_or_b32 s46, s7, s45\n" \
M_"a[92:95], a[148:151], a[172:175], a[92:95]\n" \
"s_cmp_lg_u32 s4, 0\n" \
"s_cselect_b32 s53, s41, s40\n" \
M_"a[92:95], a[212:215], a[236:239], a[92:95]\n" \
"s_lshl_b32 s40, s44, 15\n" \
"s_lshl_b32 s41, s43, 14\n" \
M_"a[88:91], a[148:151], a[168:171], a[88:91]\n" \
X_"a[190:191], v39 offset:33792\n" \
"s_lshl_b32 s48, s46, 20\n" \
M_"a[88:91], a[212:215], a[232:235], a[88:91]\n" \
"s_or_b32 s41, s40, s41\n" \
"s_add_i32 s49, s48, 0x1000000\n" \
M_"a[84:87], a[148:151], a[164:167], a[84:87]\n" \
"s_waitcnt vmcnt(17)\n" \
"v_or_b32_e32 v204, v210, v204\n" \
"v_or_b32_e64 v206, v210, v206\n" \
M_"a[84:87], a[212:215], a[228:231], a[84:87]\n" \
"v_mov_b32_e64 v210, 0\n" \
"s_add_u32 s88, s35, 0x1000\n" \
M_"a[80:83], a[148:151], a[160:163], a[80:83]\n" \
X_"a[252:253], v39 offset:40960\n" \
"s_or_b32 s42, s49, s41\n" \
M_"a[80:83], a[212:215], a[224:227], a[80:83]\n" \
"ds_write_b128 v196, v[200:203]\n" \
M_"a[96:99], a[152:155], a[160:163], a[96:99]\n" \
X_"a[254:255], v39 offset:41984\n" \
"s_lshl_b32 s4, s4, 16\n" \
M_"a[96:99], a[216:219], a[224:227], a[96:99]\n" \
"s_or_b32 s41, s42, s4\n" \
"s_lshl_b32 s4, s5, 16\n" \
M_"a[100:103], a[152:155], a[164:167], a[100:103]\n" \
"ds_write_b128 v197, v[204:207]\n" \
M_"a[100:103], a[216:219], a[228:231], a[100:103]\n" \
"s_mul_i32 s5, s6, 0x24800\n" \
"s_lshl_b32 s6, s46, 9\n" \
M_"a[104:107], a[152:155], a[168:171], a[104:107]\n" \
"s_lshl_b32 s7, s7, 6\n" \
"s_add_i32 s6, s5, s6\n" \
M_"a[104:107], a[216:219], a[232:235], a[104:107]\n" \
"ds_write_b128 v198, v[208:211]\n" \
M_"a[108:111], a[152:155], a[172:175], a[108:111]\n" \
"s_or_b32 s5, s5, s7\n" \
"s_lshl_b32 s7, s45, 2\n" \
M_"a[108:111], a[216:219], a[236:239], a[108:111]\n" \
"s_or_b32 s5, s5, s7\n" \
"s_lshl_b32 s7, s3, 12\n" \
"s_waitcnt lgkmcnt(15)\n" \
M_"a[124:127], a[156:159], a[172:175], a[124:127]\n" \
"s_or_b32 s50, s49, s7\n" \
"s_lshl_b32 s7, s3, 11\n" \
M_"a[124:127], a[220:223], a[236:239], a[124:127]\n" \
"v_lshrrev_b32_e64 v215, 4, v20\n" \
"v_bfe_u32 v216, v20, 4, 3\n" \
M_"a[120:123], a[156:159], a[168:171], a[120:123]\n" \
"s_or_b32 s42, s42, s4\n" \
"s_lshl_b32 s4, s43, 11\n" \
M_"a[120:123], a[220:223], a[232:235], a[120:123]\n" \
"s_add_i32 s52, s48, s7\n" \
"s_add_i32 s53, s53, s7\n" \
M_"a[116:119], a[156:159], a[164:167], a[116:119]\n" \
"v_mul_u32_u24_e32 v214, 0x480, v214\n" \
"v_bitop3_b32 v218, v212, v215, 7 bitop3:0x78\n" \
M_"a[116:119], a[220:223], a[228:231], a[116:119]\n" \
"v_bitop3_b32 v212, v212, v216, 4 bitop3:0x36\n" \
"s_lshl_b32 s7, s3, 10\n" \
M_"a[112:115], a[156:159], a[160:163], a[112:115]\n" \
"s_or_b32 s4, s40, s4\n" \
"v_lshl_or_b32 v25, v212, 4, v214\n" \
"s_waitcnt vmcnt(1) lgkmcnt(0)\n" \
M_"a[112:115], a[220:223], a[224:227], a[112:115]\n" \
"s_barrier\n" \
M_"v[52:55], a[128:131], a[176:179], v[52:55]\n" \
"s_add_u32 m0, s30, 0x8000\n" \
"s_movk_i32 s84, 0x280\n" \
M_"v[52:55], a[192:195], a[240:243], v[52:55]\n" \
B_"v22, s[64:67], s84 offen offset:0 lds\n" \
M_"v[56:59], a[128:131], a[180:183], v[56:59]\n" \
"s_movk_i32 s84, 0x1e80\n" \
M_"v[56:59], a[192:195], a[244:247], v[56:59]\n" \
B_"v23, s[64:67], s84 offen offset:1024 lds\n" \
M_"v[60:63], a[128:131], a[184:187], v[60:63]\n" \
X_"a[160:161], v32 offset:0\n" \
"v_mul_u32_u24_e32 v212, 0x480, v217\n" \
M_"v[60:63], a[192:195], a[248:251], v[60:63]\n" \
"s_movk_i32 s84, 0x3a80\n" \
M_"v[64:67], a[128:131], a[188:191], v[64:67]\n" \
B_"v22, s[64:67], s84 offen offset:2048 lds\n" \
M_"v[64:67], a[192:195], a[252:255], v[64:67]\n" \
R_"a[128:131], v28 offset:16384\n" \
"v_bitop3_b32 v213, v215, v213, 7 bitop3:0x6c\n" \
M_"v[80:83], a[132:135], a[188:191], v[80:83]\n" \
R_"a[192:195], v29 offset:16384\n" \
"s_or_b32 s40, s7, 0x14000\n" \
M_"v[80:83], a[196:199], a[252:255], v[80:83]\n" \
"s_movk_i32 s84, 0x5680\n" \
M_"v[76:79], a[132:135], a[184:187], v[76:79]\n" \
B_"v23, s[64:67], s84 offen offset:3072 lds\n" \
M_"v[76:79], a[196:199], a[248:251], v[76:79]\n" \
"s_add_u32 m0, s31, 0x8000\n" \
"s_mov_b32 s84, 0x280000\n" \
M_"v[72:75], a[132:135], a[180:183], v[72:75]\n" \
B_"v26, s[68:71], s84 offen offset:0 lds\n" \
M_"v[72:75], a[196:199], a[244:247], v[72:75]\n" \
X_"a[162:163], v32 offset:1024\n" \
"s_lshl_b32 s43, s44, 13\n" \
M_"v[68:71], a[132:135], a[176:179], v[68:71]\n" \
X_"a[224:225], v32 offset:8192\n" \
"s_mulk_i32 s44, 0xe800\n" \
M_"v[68:71], a[196:199], a[240:243], v[68:71]\n" \
R_"a[132:135], v28 offset:18432\n" \
"s_lshl_b32 s7, s3, 8\n" \
M_"v[84:87], a[136:139], a[176:179], v[84:87]\n" \
R_"a[196:199], v29 offset:18432\n" \
"v_lshl_or_b32 v24, v218, 4, v214\n" \
M_"v[84:87], a[200:203], a[240:243], v[84:87]\n" \
"s_addk_i32 s84, 0x7c00\n" \
M_"v[88:91], a[136:139], a[180:183], v[88:91]\n" \
B_"v26, s[68:71], s84 offen offset:1024 lds\n" \
M_"v[88:91], a[200:203], a[244:247], v[88:91]\n" \
"s_addk_i32 s84, 0x7c00\n" \
M_"v[92:95], a[136:139], a[184:187], v[92:95]\n" \
B_"v27, s[68:71], s84 offen offset:2048 lds\n" \
M_"v[92:95], a[200:203], a[248:251], v[92:95]\n" \
"s_addk_i32 s84, 0x7c00\n" \
M_"v[96:99], a[136:139], a[188:191], v[96:99]\n" \
B_"v27, s[68:71], s84 offen offset:3072 lds\n" \
M_"v[96:99], a[200:203], a[252:255], v[96:99]\n" \
R_"a[136:139], v28 offset:20480\n" \
"v_lshl_or_b32 v21, v213, 4, v212\n" \
M_"v[112:115], a[140:143], a[188:191], v[112:115]\n" \
R_"a[200:203], v29 offset:20480\n" \
"s_add_i32 s44, s44, 0x17000\n" \
M_"v[112:115], a[204:207], a[252:255], v[112:115]\n" \
"s_add_u32 m0, s30, 0x9000\n" \
"s_mov_b32 s84, 0x8280\n" \
M_"v[108:111], a[140:143], a[184:187], v[108:111]\n" \
B_"v22, s[64:67], s84 offen offset:0 lds\n" \
M_"v[108:111], a[204:207], a[248:251], v[108:111]\n" \
"s_addk_i32 s84, 0x1c00\n" \
M_"v[104:107], a[140:143], a[180:183], v[104:107]\n" \
B_"v23, s[64:67], s84 offen offset:1024 lds\n" \
M_"v[104:107], a[204:207], a[244:247], v[104:107]\n" \
"s_addk_i32 s84, 0x1c00\n" \
M_"v[100:103], a[140:143], a[176:179], v[100:103]\n" \
B_"v22, s[64:67], s84 offen offset:2048 lds\n" \
M_"v[100:103], a[204:207], a[240:243], v[100:103]\n" \
R_"a[140:143], v28 offset:22528\n" \
"s_or_b32 s45, s7, 0x27000\n" \
M_"v[116:119], a[144:147], a[176:179], v[116:119]\n" \
R_"a[204:207], v29 offset:22528\n" \
"s_or_b32 s46, s4, 0x10000\n" \
M_"v[116:119], a[208:211], a[240:243], v[116:119]\n" \
"s_addk_i32 s84, 0x1c00\n" \
M_"v[120:123], a[144:147], a[180:183], v[120:123]\n" \
B_"v23, s[64:67], s84 offen offset:3072 lds\n" \
M_"v[120:123], a[208:211], a[244:247], v[120:123]\n" \
"s_add_u32 m0, s31, 0xc000\n" \
"s_mov_b32 s84, 0x280000\n" \
M_"v[124:127], a[144:147], a[184:187], v[124:127]\n" \
B_"v26, s[72:75], s84 offen offset:0 lds\n" \
M_"v[124:127], a[208:211], a[248:251], v[124:127]\n" \
"s_addk_i32 s84, 0x7c00\n" \
M_"v[128:131], a[144:147], a[188:191], v[128:131]\n" \
B_"v26, s[72:75], s84 offen offset:1024 lds\n" \
M_"v[128:131], a[208:211], a[252:255], v[128:131]\n" \
R_"a[144:147], v28 offset:0\n" \
"s_add_i32 s48, s6, 0x3000000\n" \
M_"v[144:147], a[148:151], a[188:191], v[144:147]\n" \
R_"a[208:211], v29 offset:0\n" \
"s_add_i32 s49, s5, 0x3004000\n" \
M_"v[144:147], a[212:215], a[252:255], v[144:147]\n" \
R_"v[200:203], v199 offset:0\n" \
"s_add_i32 s52, s52, 0x1010000\n" \
M_"v[140:143], a[148:151], a[184:187], v[140:143]\n" \
R_"v[204:207], v199 offset:4096\n" \
M_"v[140:143], a[212:215], a[248:251], v[140:143]\n" \
X_"a[226:227], v32 offset:9216\n" \
M_"v[136:139], a[148:151], a[180:183], v[136:139]\n" \
X_"a[164:165], v33 offset:0\n" \
M_"v[136:139], a[212:215], a[244:247], v[136:139]\n" \
R_"v[208:211], v199 offset:8192\n" \
M_"v[132:135], a[148:151], a[176:179], v[132:135]\n" \
X_"a[166:167], v33 offset:1024\n" \
M_"v[132:135], a[212:215], a[240:243], v[132:135]\n" \
R_"a[148:151], v28 offset:2048\n" \
M_"v[148:151], a[152:155], a[176:179], v[148:151]\n" \
R_"a[212:215], v29 offset:2048\n" \
M_"v[148:151], a[216:219], a[240:243], v[148:151]\n" \
X_"a[228:229], v33 offset:8192\n" \
M_"v[152:155], a[152:155], a[180:183], v[152:155]\n" \
X_"a[230:231], v33 offset:9216\n" \
M_"v[152:155], a[216:219], a[244:247], v[152:155]\n" \
X_"a[168:169], v34 offset:0\n" \
M_"v[156:159], a[152:155], a[184:187], v[156:159]\n" \
X_"a[170:171], v34 offset:1024\n" \
M_"v[156:159], a[216:219], a[248:251], v[156:159]\n" \
X_"a[232:233], v34 offset:8192\n" \
M_"v[160:163], a[152:155], a[188:191], v[160:163]\n" \
X_"a[234:235], v34 offset:9216\n" \
M_"v[160:163], a[216:219], a[252:255], v[160:163]\n" \
R_"a[152:155], v28 offset:4096\n" \
M_"v[176:179], a[156:159], a[188:191], v[176:179]\n" \
R_"a[216:219], v29 offset:4096\n" \
M_"v[176:179], a[220:223], a[252:255], v[176:179]\n" \
X_"a[172:173], v35 offset:0\n" \
M_"v[172:175], a[156:159], a[184:187], v[172:175]\n" \
X_"a[174:175], v35 offset:1024\n" \
M_"v[172:175], a[220:223], a[248:251], v[172:175]\n" \
X_"a[236:237], v35 offset:8192\n" \
M_"v[168:171], a[156:159], a[180:183], v[168:171]\n" \
X_"a[238:239], v35 offset:9216\n" \
M_"v[168:171], a[220:223], a[244:247], v[168:171]\n" \
M_"v[164:167], a[156:159], a[176:179], v[164:167]\n" \
M_"v[164:167], a[220:223], a[240:243], v[164:167]\n" \
R_"a[156:159], v28 offset:6144\n" \
R_"a[220:223], v29 offset:6144\n" \
"s_waitcnt lgkmcnt(15)\n" \
M_"a[0:3], a[128:131], a[160:163], a[0:3]\n" \
"s_addk_i32 s84, 0x7c00\n" \
M_"a[0:3], a[192:195], a[224:227], a[0:3]\n" \
B_"v27, s[72:75], s84 offen offset:2048 lds\n" \
M_"a[4:7], a[128:131], a[164:167], a[4:7]\n" \
X_"a[176:177], v36 offset:0\n" \
"s_waitcnt lgkmcnt(13)\n" \
M_"a[4:7], a[192:195], a[228:231], a[4:7]\n" \
"s_addk_i32 s84, 0x7c00\n" \
"s_waitcnt lgkmcnt(11)\n" \
M_"a[8:11], a[128:131], a[168:171], a[8:11]\n" \
B_"v27, s[72:75], s84 offen offset:3072 lds\n" \
"s_waitcnt lgkmcnt(9)\n" \
M_"a[8:11], a[192:195], a[232:235], a[8:11]\n" \
X_"a[178:179], v36 offset:1024\n" \
"s_waitcnt lgkmcnt(6)\n" \
M_"a[12:15], a[128:131], a[172:175], a[12:15]\n" \
X_"a[240:241], v36 offset:8192\n" \
"s_waitcnt lgkmcnt(5)\n" \
M_"a[12:15], a[192:195], a[236:239], a[12:15]\n" \
M_"a[28:31], a[132:135], a[172:175], a[28:31]\n" \
M_"a[28:31], a[196:199], a[236:239], a[28:31]\n" \
X_"a[242:243], v36 offset:9216\n" \
M_"a[24:27], a[132:135], a[168:171], a[24:27]\n" \
M_"a[24:27], a[196:199], a[232:235], a[24:27]\n" \
".fill (L_pb + 52960 - .) / 4, 4, 0xBF800000\n" \
"L_pp13:\n" \
"s_cbranch_execz L_pret\n" \
"s_nop 0\n" \
M_"a[20:23], a[132:135], a[164:167], a[20:23]\n" \
M_"a[20:23], a[196:199], a[228:231], a[20:23]\n" \
X_"a[180:181], v37 offset:0\n" \
M_"a[16:19], a[132:135], a[160:163], a[16:19]\n" \
M_"a[16:19], a[196:199], a[224:227], a[16:19]\n" \
M_"a[32:35], a[136:139], a[160:163], a[32:35]\n" \
X_"a[182:183], v37 offset:1024\n" \
M_"a[32:35], a[200:203], a[224:227], a[32:35]\n" \
M_"a[36:39], a[136:139], a[164:167], a[36:39]\n" \
M_"a[36:39], a[200:203], a[228:231], a[36:39]\n" \
"buffer_store_dwordx4 v[200:203], v20, s[80:83], s35 offen sc1\n" \
M_"a[40:43], a[136:139], a[168:171], a[40:43]\n" \
"s_mov_b32 m0, s32\n" \
X_"a[244:245], v37 offset:8192\n" \
M_"a[40:43], a[200:203], a[232:235], a[40:43]\n" \
"s_mov_b32 exec_lo, 0x0\n" \
"s_mov_b32 exec_hi, 0xffffffff\n" \
"global_load_lds_dword v[188:189], off\n" \
"s_mov_b64 exec, -1\n" \
M_"a[44:47], a[136:139], a[172:175], a[44:47]\n" \
X_"a[246:247], v37 offset:9216\n" \
M_"a[44:47], a[200:203], a[236:239], a[44:47]\n" \
M_"a[60:63], a[140:143], a[172:175], a[60:63]\n" \
M_"a[60:63], a[204:207], a[236:239], a[60:63]\n" \
M_"a[56:59], a[140:143], a[168:171], a[56:59]\n" \
X_"a[184:185], v38 offset:0\n" \
M_"a[56:59], a[204:207], a[232:235], a[56:59]\n" \
M_"a[52:55], a[140:143], a[164:167], a[52:55]\n" \
"buffer_store_dwordx4 v[204:207], v20, s[80:83], s88 offen sc1\n" \
M_"a[52:55], a[204:207], a[228:231], a[52:55]\n" \
X_"a[186:187], v38 offset:1024\n" \
M_"a[48:51], a[140:143], a[160:163], a[48:51]\n" \
M_"a[48:51], a[204:207], a[224:227], a[48:51]\n" \
M_"a[64:67], a[144:147], a[160:163], a[64:67]\n" \
X_"a[248:249], v38 offset:8192\n" \
M_"a[64:67], a[208:211], a[224:227], a[64:67]\n" \
M_"a[68:71], a[144:147], a[164:167], a[68:71]\n" \
M_"a[68:71], a[208:211], a[228:231], a[68:71]\n" \
X_"a[250:251], v38 offset:9216\n" \
M_"a[72:75], a[144:147], a[168:171], a[72:75]\n" \
M_"a[72:75], a[208:211], a[232:235], a[72:75]\n" \
M_"a[76:79], a[144:147], a[172:175], a[76:79]\n" \
"buffer_store_dwordx4 v[208:211], v20, s[80:83], s36 offen sc1\n" \
M_"a[76:79], a[208:211], a[236:239], a[76:79]\n" \
X_"a[188:189], v39 offset:0\n" \
M_"a[92:95], a[148:151], a[172:175], a[92:95]\n" \
M_"a[92:95], a[212:215], a[236:239], a[92:95]\n" \
M_"a[88:91], a[148:151], a[168:171], a[88:91]\n" \
X_"a[190:191], v39 offset:1024\n" \
M_"a[88:91], a[212:215], a[232:235], a[88:91]\n" \
M_"a[84:87], a[148:151], a[164:167], a[84:87]\n" \
M_"a[84:87], a[212:215], a[228:231], a[84:87]\n" \
X_"a[252:253], v39 offset:8192\n" \
M_"a[80:83], a[148:151], a[160:163], a[80:83]\n" \
M_"a[80:83], a[212:215], a[224:227], a[80:83]\n" \
M_"a[96:99], a[152:155], a[160:163], a[96:99]\n" \
X_"a[254:255], v39 offset:9216\n" \
M_"a[96:99], a[216:219], a[224:227], a[96:99]\n" \
M_"a[100:103], a[152:155], a[164:167], a[100:103]\n" \
M_"a[100:103], a[216:219], a[228:231], a[100:103]\n" \
M_"a[104:107], a[152:155], a[168:171], a[104:107]\n" \
M_"a[104:107], a[216:219], a[232:235], a[104:107]\n" \
M_"a[108:111], a[152:155], a[172:175], a[108:111]\n" \
M_"a[108:111], a[216:219], a[236:239], a[108:111]\n" \
"s_waitcnt lgkmcnt(15)\n" \
M_"a[124:127], a[156:159], a[172:175], a[124:127]\n" \
M_"a[124:127], a[220:223], a[236:239], a[124:127]\n" \
M_"a[120:123], a[156:159], a[168:171], a[120:123]\n" \
M_"a[120:123], a[220:223], a[232:235], a[120:123]\n" \
M_"a[116:119], a[156:159], a[164:167], a[116:119]\n" \
M_"a[116:119], a[220:223], a[228:231], a[116:119]\n" \
M_"a[112:115], a[156:159], a[160:163], a[112:115]\n" \
"s_waitcnt vmcnt(4) lgkmcnt(0)\n" \
M_"a[112:115], a[220:223], a[224:227], a[112:115]\n" \
"s_barrier\n" \
M_"v[52:55], a[128:131], a[176:179], v[52:55]\n" \
"s_add_u32 m0, s30, 0x0\n" \
"s_movk_i32 s84, 0x300\n" \
M_"v[52:55], a[192:195], a[240:243], v[52:55]\n" \
B_"v22, s[64:67], s84 offen offset:0 lds\n" \
M_"v[56:59], a[128:131], a[180:183], v[56:59]\n" \
X_"a[160:161], v32 offset:32768\n" \
M_"v[56:59], a[192:195], a[244:247], v[56:59]\n" \
"s_movk_i32 s84, 0x1f00\n" \
M_"v[60:63], a[128:131], a[184:187], v[60:63]\n" \
B_"v23, s[64:67], s84 offen offset:1024 lds\n" \
M_"v[60:63], a[192:195], a[248:251], v[60:63]\n" \
X_"a[162:163], v32 offset:33792\n" \
M_"v[64:67], a[128:131], a[188:191], v[64:67]\n" \
X_"a[224:225], v32 offset:40960\n" \
M_"v[64:67], a[192:195], a[252:255], v[64:67]\n" \
R_"a[128:131], v28 offset:49152\n" \
M_"v[80:83], a[132:135], a[188:191], v[80:83]\n" \
R_"a[192:195], v29 offset:49152\n" \
M_"v[80:83], a[196:199], a[252:255], v[80:83]\n" \
"s_movk_i32 s84, 0x3b00\n" \
M_"v[76:79], a[132:135], a[184:187], v[76:79]\n" \
B_"v22, s[64:67], s84 offen offset:2048 lds\n" \
M_"v[76:79], a[196:199], a[248:251], v[76:79]\n" \
"s_movk_i32 s84, 0x5700\n" \
M_"v[72:75], a[132:135], a[180:183], v[72:75]\n" \
B_"v23, s[64:67], s84 offen offset:3072 lds\n" \
M_"v[72:75], a[196:199], a[244:247], v[72:75]\n" \
"s_add_u32 m0, s31, 0x0\n" \
"s_mov_b32 s84, 0x300000\n" \
M_"v[68:71], a[132:135], a[176:179], v[68:71]\n" \
B_"v26, s[68:71], s84 offen offset:0 lds\n" \
M_"v[68:71], a[196:199], a[240:243], v[68:71]\n" \
R_"a[132:135], v28 offset:51200\n" \
M_"v[84:87], a[136:139], a[176:179], v[84:87]\n" \
R_"a[196:199], v29 offset:51200\n" \
M_"v[84:87], a[200:203], a[240:243], v[84:87]\n" \
"s_addk_i32 s84, 0x7c00\n" \
M_"v[88:91], a[136:139], a[180:183], v[88:91]\n" \
B_"v26, s[68:71], s84 offen offset:1024 lds\n" \
M_"v[88:91], a[200:203], a[244:247], v[88:91]\n" \
X_"a[226:227], v32 offset:41984\n" \
M_"v[92:95], a[136:139], a[184:187], v[92:95]\n" \
"s_addk_i32 s84, 0x7c00\n" \
M_"v[92:95], a[200:203], a[248:251], v[92:95]\n" \
B_"v27, s[68:71], s84 offen offset:2048 lds\n" \
M_"v[96:99], a[136:139], a[188:191], v[96:99]\n" \
X_"a[164:165], v33 offset:32768\n" \
M_"v[96:99], a[200:203], a[252:255], v[96:99]\n" \
R_"a[136:139], v28 offset:53248\n" \
M_"v[112:115], a[140:143], a[188:191], v[112:115]\n" \
R_"a[200:203], v29 offset:53248\n" \
M_"v[112:115], a[204:207], a[252:255], v[112:115]\n" \
"s_addk_i32 s84, 0x7c00\n" \
M_"v[108:111], a[140:143], a[184:187], v[108:111]\n" \
B_"v27, s[68:71], s84 offen offset:3072 lds\n" \
M_"v[108:111], a[204:207], a[248:251], v[108:111]\n" \
"s_add_u32 m0, s30, 0x1000\n" \
"s_mov_b32 s84, 0x8300\n" \
M_"v[104:107], a[140:143], a[180:183], v[104:107]\n" \
B_"v22, s[64:67], s84 offen offset:0 lds\n" \
M_"v[104:107], a[204:207], a[244:247], v[104:107]\n" \
X_"a[166:167], v33 offset:33792\n" \
M_"v[100:103], a[140:143], a[176:179], v[100:103]\n" \
X_"a[228:229], v33 offset:40960\n" \
M_"v[100:103], a[204:207], a[240:243], v[100:103]\n" \
R_"a[140:143], v28 offset:55296\n" \
M_"v[116:119], a[144:147], a[176:179], v[116:119]\n" \
R_"a[204:207], v29 offset:55296\n" \
M_"v[116:119], a[208:211], a[240:243], v[116:119]\n" \
"s_addk_i32 s84, 0x1c00\n" \
M_"v[120:123], a[144:147], a[180:183], v[120:123]\n" \
B_"v23, s[64:67], s84 offen offset:1024 lds\n" \
M_"v[120:123], a[208:211], a[244:247], v[120:123]\n" \
"s_addk_i32 s84, 0x1c00\n" \
M_"v[124:127], a[144:147], a[184:187], v[124:127]\n" \
B_"v22, s[64:67], s84 offen offset:2048 lds\n" \
M_"v[124:127], a[208:211], a[248:251], v[124:127]\n" \
"s_addk_i32 s84, 0x1c00\n" \
"s_nop 0\n" \
M_"v[128:131], a[144:147], a[188:191], v[128:131]\n" \
B_"v23, s[64:67], s84 offen offset:3072 lds\n" \
M_"v[128:131], a[208:211], a[252:255], v[128:131]\n" \
R_"a[144:147], v28 offset:32768\n" \
M_"v[144:147], a[148:151], a[188:191], v[144:147]\n" \
R_"a[208:211], v29 offset:32768\n" \
M_"v[144:147], a[212:215], a[252:255], v[144:147]\n" \
"s_add_u32 m0, s31, 0x4000\n" \
"s_mov_b32 s84, 0x300000\n" \
M_"v[140:143], a[148:151], a[184:187], v[140:143]\n" \
B_"v26, s[72:75], s84 offen offset:0 lds\n" \
M_"v[140:143], a[212:215], a[248:251], v[140:143]\n" \
X_"a[230:231], v33 offset:41984\n" \
M_"v[136:139], a[148:151], a[180:183], v[136:139]\n" \
"s_addk_i32 s84, 0x7c00\n" \
"s_nop 0\n" \
M_"v[136:139], a[212:215], a[244:247], v[136:139]\n" \
B_"v26, s[72:75], s84 offen offset:1024 lds\n" \
M_"v[132:135], a[148:151], a[176:179], v[132:135]\n" \
X_"a[168:169], v34 offset:32768\n" \
M_"v[132:135], a[212:215], a[240:243], v[132:135]\n" \
R_"a[148:151], v28 offset:34816\n" \
M_"v[148:151], a[152:155], a[176:179], v[148:151]\n" \
R_"a[212:215], v29 offset:34816\n" \
M_"v[148:151], a[216:219], a[240:243], v[148:151]\n" \
X_"a[170:171], v34 offset:33792\n" \
M_"v[152:155], a[152:155], a[180:183], v[152:155]\n" \
X_"a[232:233], v34 offset:40960\n" \
M_"v[152:155], a[216:219], a[244:247], v[152:155]\n" \
X_"a[234:235], v34 offset:41984\n" \
M_"v[156:159], a[152:155], a[184:187], v[156:159]\n" \
X_"a[172:173], v35 offset:32768\n" \
M_"v[156:159], a[216:219], a[248:251], v[156:159]\n" \
X_"a[174:175], v35 offset:33792\n" \
M_"v[160:163], a[152:155], a[188:191], v[160:163]\n" \
X_"a[236:237], v35 offset:40960\n" \
M_"v[160:163], a[216:219], a[252:255], v[160:163]\n" \
R_"a[152:155], v28 offset:36864\n" \
M_"v[176:179], a[156:159], a[188:191], v[176:179]\n" \
R_"a[216:219], v29 offset:36864\n" \
M_"v[176:179], a[220:223], a[252:255], v[176:179]\n" \
X_"a[238:239], v35 offset:41984\n" \
M_"v[172:175], a[156:159], a[184:187], v[172:175]\n" \
M_"v[172:175], a[220:223], a[248:251], v[172:175]\n" \
M_"v[168:171], a[156:159], a[180:183], v[168:171]\n" \
M_"v[168:171], a[220:223], a[244:247], v[168:171]\n" \
M_"v[164:167], a[156:159], a[176:179], v[164:167]\n" \
M_"v[164:167], a[220:223], a[240:243], v[164:167]\n" \
R_"a[156:159], v28 offset:38912\n" \
R_"a[220:223], v29 offset:38912\n" \
"s_waitcnt lgkmcnt(15)\n" \
M_"a[0:3], a[128:131], a[160:163], a[0:3]\n" \
"s_addk_i32 s84, 0x7c00\n" \
M_"a[0:3], a[192:195], a[224:227], a[0:3]\n" \
B_"v27, s[72:75], s84 offen offset:2048 lds\n" \
M_"a[4:7], a[128:131], a[164:167], a[4:7]\n" \
X_"a[176:177], v36 offset:32768\n" \
"s_waitcnt lgkmcnt(15)\n" \
M_"a[4:7], a[192:195], a[228:231], a[4:7]\n" \
"s_addk_i32 s84, 0x7c00\n" \
"s_waitcnt lgkmcnt(11)\n" \
M_"a[8:11], a[128:131], a[168:171], a[8:11]\n" \
B_"v27, s[72:75], s84 offen offset:3072 lds\n" \
"s_waitcnt lgkmcnt(9)\n" \
M_"a[8:11], a[192:195], a[232:235], a[8:11]\n" \
X_"a[178:179], v36 offset:33792\n" \
"s_waitcnt lgkmcnt(8)\n" \
M_"a[12:15], a[128:131], a[172:175], a[12:15]\n" \
X_"a[240:241], v36 offset:40960\n" \
"s_waitcnt lgkmcnt(5)\n" \
M_"a[12:15], a[192:195], a[236:239], a[12:15]\n" \
M_"a[28:31], a[132:135], a[172:175], a[28:31]\n" \
M_"a[28:31], a[196:199], a[236:239], a[28:31]\n" \
X_"a[242:243], v36 offset:41984\n" \
M_"a[24:27], a[132:135], a[168:171], a[24:27]\n" \
M_"a[24:27], a[196:199], a[232:235], a[24:27]\n" \
M_"a[20:23], a[132:135], a[164:167], a[20:23]\n" \
M_"a[20:23], a[196:199], a[228:231], a[20:23]\n" \
X_"a[180:181], v37 offset:32768\n" \
M_"a[16:19], a[132:135], a[160:163], a[16:19]\n" \
M_"a[16:19], a[196:199], a[224:227], a[16:19]\n" \
M_"a[32:35], a[136:139], a[160:163], a[32:35]\n" \
X_"a[182:183], v37 offset:33792\n" \
M_"a[32:35], a[200:203], a[224:227], a[32:35]\n" \
M_"a[36:39], a[136:139], a[164:167], a[36:39]\n" \
"v_lshlrev_b32_e32 v45, 3, v20\n" \
"v_add_u32_e32 v45, s31, v45\n" \
M_"a[36:39], a[200:203], a[228:231], a[36:39]\n" \
"s_getpc_b64 s[86:87]\n" \
"s_sub_u32 s86, s86, 0x10800\n" \
"s_subb_u32 s87, s87, 0\n" \
"s_mov_b32 m0, s32\n" \
"s_nop 0\n" \
M_"a[40:43], a[136:139], a[168:171], a[40:43]\n" \
"global_load_lds_dword v45, s[86:87]\n" \
M_"a[40:43], a[200:203], a[232:235], a[40:43]\n" \
X_"a[244:245], v37 offset:40960\n" \
M_"a[44:47], a[136:139], a[172:175], a[44:47]\n" \
X_"a[246:247], v37 offset:41984\n" \
M_"a[44:47], a[200:203], a[236:239], a[44:47]\n" \
M_"a[60:63], a[140:143], a[172:175], a[60:63]\n" \
M_"a[60:63], a[204:207], a[236:239], a[60:63]\n" \
M_"a[56:59], a[140:143], a[168:171], a[56:59]\n" \
X_"a[184:185], v38 offset:32768\n" \
M_"a[56:59], a[204:207], a[232:235], a[56:59]\n" \
M_"a[52:55], a[140:143], a[164:167], a[52:55]\n" \
M_"a[52:55], a[204:207], a[228:231], a[52:55]\n" \
X_"a[186:187], v38 offset:33792\n" \
M_"a[48:51], a[140:143], a[160:163], a[48:51]\n" \
M_"a[48:51], a[204:207], a[224:227], a[48:51]\n" \
M_"a[64:67], a[144:147], a[160:163], a[64:67]\n" \
X_"a[248:249], v38 offset:40960\n" \
M_"a[64:67], a[208:211], a[224:227], a[64:67]\n" \
M_"a[68:71], a[144:147], a[164:167], a[68:71]\n" \
M_"a[68:71], a[208:211], a[228:231], a[68:71]\n" \
X_"a[250:251], v38 offset:41984\n" \
M_"a[72:75], a[144:147], a[168:171], a[72:75]\n" \
M_"a[72:75], a[208:211], a[232:235], a[72:75]\n" \
M_"a[76:79], a[144:147], a[172:175], a[76:79]\n" \
M_"a[76:79], a[208:211], a[236:239], a[76:79]\n" \
X_"a[188:189], v39 offset:32768\n" \
M_"a[92:95], a[148:151], a[172:175], a[92:95]\n" \
M_"a[92:95], a[212:215], a[236:239], a[92:95]\n" \
M_"a[88:91], a[148:151], a[168:171], a[88:91]\n" \
X_"a[190:191], v39 offset:33792\n" \
M_"a[88:91], a[212:215], a[232:235], a[88:91]\n" \
M_"a[84:87], a[148:151], a[164:167], a[84:87]\n" \
M_"a[84:87], a[212:215], a[228:231], a[84:87]\n" \
X_"a[252:253], v39 offset:40960\n" \
M_"a[80:83], a[148:151], a[160:163], a[80:83]\n" \
M_"a[80:83], a[212:215], a[224:227], a[80:83]\n" \
M_"a[96:99], a[152:155], a[160:163], a[96:99]\n" \
X_"a[254:255], v39 offset:41984\n" \
M_"a[96:99], a[216:219], a[224:227], a[96:99]\n" \
M_"a[100:103], a[152:155], a[164:167], a[100:103]\n" \
M_"a[100:103], a[216:219], a[228:231], a[100:103]\n" \
M_"a[104:107], a[152:155], a[168:171], a[104:107]\n" \
M_"a[104:107], a[216:219], a[232:235], a[104:107]\n" \
M_"a[108:111], a[152:155], a[172:175], a[108:111]\n" \
M_"a[108:111], a[216:219], a[236:239], a[108:111]\n" \
"s_waitcnt lgkmcnt(15)\n" \
M_"a[124:127], a[156:159], a[172:175], a[124:127]\n" \
M_"a[124:127], a[220:223], a[236:239], a[124:127]\n" \
M_"a[120:123], a[156:159], a[168:171], a[120:123]\n" \
M_"a[120:123], a[220:223], a[232:235], a[120:123]\n" \
M_"a[116:119], a[156:159], a[164:167], a[116:119]\n" \
M_"a[116:119], a[220:223], a[228:231], a[116:119]\n" \
M_"a[112:115], a[156:159], a[160:163], a[112:115]\n" \
"s_waitcnt vmcnt(1) lgkmcnt(0)\n" \
M_"a[112:115], a[220:223], a[224:227], a[112:115]\n" \
"s_barrier\n" \
"v_mov_b32_e32 v45, s62\n" \
"s_mov_b64 exec, 1\n" \
"buffer_store_dword v45, off, s[80:83], s37 offset:128 sc1\n" \
"s_mov_b64 exec, -1\n" \
M_"v[52:55], a[128:131], a[176:179], v[52:55]\n" \
"s_add_u32 m0, s30, 0x8000\n" \
"s_movk_i32 s84, 0x380\n" \
M_"v[52:55], a[192:195], a[240:243], v[52:55]\n" \
B_"v22, s[64:67], s84 offen offset:0 lds\n" \
M_"v[56:59], a[128:131], a[180:183], v[56:59]\n" \
X_"a[160:161], v32 offset:0\n" \
M_"v[56:59], a[192:195], a[244:247], v[56:59]\n" \
"s_movk_i32 s84, 0x1f80\n" \
M_"v[60:63], a[128:131], a[184:187], v[60:63]\n" \
B_"v23, s[64:67], s84 offen offset:1024 lds\n" \
M_"v[60:63], a[192:195], a[248:251], v[60:63]\n" \
X_"a[162:163], v32 offset:1024\n" \
M_"v[64:67], a[128:131], a[188:191], v[64:67]\n" \
X_"a[224:225], v32 offset:8192\n" \
M_"v[64:67], a[192:195], a[252:255], v[64:67]\n" \
R_"a[128:131], v28 offset:16384\n" \
M_"v[80:83], a[132:135], a[188:191], v[80:83]\n" \
R_"a[192:195], v29 offset:16384\n" \
M_"v[80:83], a[196:199], a[252:255], v[80:83]\n" \
"s_movk_i32 s84, 0x3b80\n" \
M_"v[76:79], a[132:135], a[184:187], v[76:79]\n" \
B_"v22, s[64:67], s84 offen offset:2048 lds\n" \
M_"v[76:79], a[196:199], a[248:251], v[76:79]\n" \
"s_movk_i32 s84, 0x5780\n" \
M_"v[72:75], a[132:135], a[180:183], v[72:75]\n" \
B_"v23, s[64:67], s84 offen offset:3072 lds\n" \
M_"v[72:75], a[196:199], a[244:247], v[72:75]\n" \
"s_add_u32 m0, s31, 0x8000\n" \
"s_mov_b32 s84, 0x380000\n" \
M_"v[68:71], a[132:135], a[176:179], v[68:71]\n" \
B_"v26, s[68:71], s84 offen offset:0 lds\n" \
M_"v[68:71], a[196:199], a[240:243], v[68:71]\n" \
R_"a[132:135], v28 offset:18432\n" \
M_"v[84:87], a[136:139], a[176:179], v[84:87]\n" \
R_"a[196:199], v29 offset:18432\n" \
M_"v[84:87], a[200:203], a[240:243], v[84:87]\n" \
"s_addk_i32 s84, 0x7c00\n" \
M_"v[88:91], a[136:139], a[180:183], v[88:91]\n" \
B_"v26, s[68:71], s84 offen offset:1024 lds\n" \
M_"v[88:91], a[200:203], a[244:247], v[88:91]\n" \
X_"a[226:227], v32 offset:9216\n" \
M_"v[92:95], a[136:139], a[184:187], v[92:95]\n" \
"s_addk_i32 s84, 0x7c00\n" \
M_"v[92:95], a[200:203], a[248:251], v[92:95]\n" \
B_"v27, s[68:71], s84 offen offset:2048 lds\n" \
M_"v[96:99], a[136:139], a[188:191], v[96:99]\n" \
X_"a[164:165], v33 offset:0\n" \
M_"v[96:99], a[200:203], a[252:255], v[96:99]\n" \
R_"a[136:139], v28 offset:20480\n" \
M_"v[112:115], a[140:143], a[188:191], v[112:115]\n" \
R_"a[200:203], v29 offset:20480\n" \
M_"v[112:115], a[204:207], a[252:255], v[112:115]\n" \
"s_addk_i32 s84, 0x7c00\n" \
"s_nop 0\n" \
M_"v[108:111], a[140:143], a[184:187], v[108:111]\n" \
B_"v27, s[68:71], s84 offen offset:3072 lds\n" \
M_"v[108:111], a[204:207], a[248:251], v[108:111]\n" \
"s_add_u32 m0, s30, 0x9000\n" \
"s_mov_b32 s84, 0x8380\n" \
M_"v[104:107], a[140:143], a[180:183], v[104:107]\n" \
B_"v22, s[64:67], s84 offen offset:0 lds\n" \
M_"v[104:107], a[204:207], a[244:247], v[104:107]\n" \
X_"a[166:167], v33 offset:1024\n" \
M_"v[100:103], a[140:143], a[176:179], v[100:103]\n" \
X_"a[228:229], v33 offset:8192\n" \
M_"v[100:103], a[204:207], a[240:243], v[100:103]\n" \
R_"a[140:143], v28 offset:22528\n" \
M_"v[116:119], a[144:147], a[176:179], v[116:119]\n" \
R_"a[204:207], v29 offset:22528\n" \
M_"v[116:119], a[208:211], a[240:243], v[116:119]\n" \
"s_addk_i32 s84, 0x1c00\n" \
M_"v[120:123], a[144:147], a[180:183], v[120:123]\n" \
B_"v23, s[64:67], s84 offen offset:1024 lds\n" \
M_"v[120:123], a[208:211], a[244:247], v[120:123]\n" \
"s_addk_i32 s84, 0x1c00\n" \
M_"v[124:127], a[144:147], a[184:187], v[124:127]\n" \
B_"v22, s[64:67], s84 offen offset:2048 lds\n" \
M_"v[124:127], a[208:211], a[248:251], v[124:127]\n" \
"s_addk_i32 s84, 0x1c00\n" \
"s_nop 0\n" \
M_"v[128:131], a[144:147], a[188:191], v[128:131]\n" \
B_"v23, s[64:67], s84 offen offset:3072 lds\n" \
M_"v[128:131], a[208:211], a[252:255], v[128:131]\n" \
R_"a[144:147], v28 offset:0\n" \
M_"v[144:147], a[148:151], a[188:191], v[144:147]\n" \
R_"a[208:211], v29 offset:0\n" \
M_"v[144:147], a[212:215], a[252:255], v[144:147]\n" \
"s_add_u32 m0, s31, 0xc000\n" \
"s_mov_b32 s84, 0x380000\n" \
M_"v[140:143], a[148:151], a[184:187], v[140:143]\n" \
B_"v26, s[72:75], s84 offen offset:0 lds\n" \
M_"v[140:143], a[212:215], a[248:251], v[140:143]\n" \
X_"a[230:231], v33 offset:9216\n" \
M_"v[136:139], a[148:151], a[180:183], v[136:139]\n" \
"s_addk_i32 s84, 0x7c00\n" \
"s_nop 0\n" \
M_"v[136:139], a[212:215], a[244:247], v[136:139]\n" \
B_"v26, s[72:75], s84 offen offset:1024 lds\n" \
M_"v[132:135], a[148:151], a[176:179], v[132:135]\n" \
X_"a[168:169], v34 offset:0\n" \
M_"v[132:135], a[212:215], a[240:243], v[132:135]\n" \
R_"a[148:151], v28 offset:2048\n" \
M_"v[148:151], a[152:155], a[176:179], v[148:151]\n" \
R_"a[212:215], v29 offset:2048\n" \
M_"v[148:151], a[216:219], a[240:243], v[148:151]\n" \
X_"a[170:171], v34 offset:1024\n" \
M_"v[152:155], a[152:155], a[180:183], v[152:155]\n" \
X_"a[232:233], v34 offset:8192\n" \
M_"v[152:155], a[216:219], a[244:247], v[152:155]\n" \
X_"a[234:235], v34 offset:9216\n" \
M_"v[156:159], a[152:155], a[184:187], v[156:159]\n" \
X_"a[172:173], v35 offset:0\n" \
M_"v[156:159], a[216:219], a[248:251], v[156:159]\n" \
X_"a[174:175], v35 offset:1024\n" \
M_"v[160:163], a[152:155], a[188:191], v[160:163]\n" \
X_"a[236:237], v35 offset:8192\n" \
M_"v[160:163], a[216:219], a[252:255], v[160:163]\n" \
R_"a[152:155], v28 offset:4096\n" \
M_"v[176:179], a[156:159], a[188:191], v[176:179]\n" \
R_"a[216:219], v29 offset:4096\n" \
M_"v[176:179], a[220:223], a[252:255], v[176:179]\n" \
X_"a[238:239], v35 offset:9216\n" \
M_"v[172:175], a[156:159], a[184:187], v[172:175]\n" \
M_"v[172:175], a[220:223], a[248:251], v[172:175]\n" \
M_"v[168:171], a[156:159], a[180:183], v[168:171]\n" \
M_"v[168:171], a[220:223], a[244:247], v[168:171]\n" \
M_"v[164:167], a[156:159], a[176:179], v[164:167]\n" \
M_"v[164:167], a[220:223], a[240:243], v[164:167]\n" \
R_"a[156:159], v28 offset:6144\n" \
R_"a[220:223], v29 offset:6144\n" \
"s_waitcnt lgkmcnt(15)\n" \
M_"a[0:3], a[128:131], a[160:163], a[0:3]\n" \
"s_addk_i32 s84, 0x7c00\n" \
M_"a[0:3], a[192:195], a[224:227], a[0:3]\n" \
B_"v27, s[72:75], s84 offen offset:2048 lds\n" \
M_"a[4:7], a[128:131], a[164:167], a[4:7]\n" \
X_"a[176:177], v36 offset:0\n" \
"s_waitcnt lgkmcnt(15)\n" \
M_"a[4:7], a[192:195], a[228:231], a[4:7]\n" \
"s_addk_i32 s84, 0x7c00\n" \
"s_waitcnt lgkmcnt(11)\n" \
M_"a[8:11], a[128:131], a[168:171], a[8:11]\n" \
B_"v27, s[72:75], s84 offen offset:3072 lds\n" \
"s_waitcnt lgkmcnt(9)\n" \
M_"a[8:11], a[192:195], a[232:235], a[8:11]\n" \
X_"a[178:179], v36 offset:1024\n" \
"s_waitcnt lgkmcnt(8)\n" \
M_"a[12:15], a[128:131], a[172:175], a[12:15]\n" \
X_"a[240:241], v36 offset:8192\n" \
"s_waitcnt lgkmcnt(5)\n" \
M_"a[12:15], a[192:195], a[236:239], a[12:15]\n" \
M_"a[28:31], a[132:135], a[172:175], a[28:31]\n" \
M_"a[28:31], a[196:199], a[236:239], a[28:31]\n" \
X_"a[242:243], v36 offset:9216\n" \
M_"a[24:27], a[132:135], a[168:171], a[24:27]\n" \
M_"a[24:27], a[196:199], a[232:235], a[24:27]\n" \
M_"a[20:23], a[132:135], a[164:167], a[20:23]\n" \
M_"a[20:23], a[196:199], a[228:231], a[20:23]\n" \
X_"a[180:181], v37 offset:0\n" \
M_"a[16:19], a[132:135], a[160:163], a[16:19]\n" \
M_"a[16:19], a[196:199], a[224:227], a[16:19]\n" \
M_"a[32:35], a[136:139], a[160:163], a[32:35]\n" \
"v_bfe_u32 v44, v20, 2, 5\n" \
X_"a[182:183], v37 offset:1024\n" \
M_"a[32:35], a[200:203], a[224:227], a[32:35]\n" \
"buffer_load_dword v44, v44, s[80:83], s38 offen offset:128 sc1\n" \
M_"a[36:39], a[136:139], a[164:167], a[36:39]\n" \
M_"a[36:39], a[200:203], a[228:231], a[36:39]\n" \
X_"a[244:245], v37 offset:8192\n" \
M_"a[40:43], a[136:139], a[168:171], a[40:43]\n" \
M_"a[40:43], a[200:203], a[232:235], a[40:43]\n" \
M_"a[44:47], a[136:139], a[172:175], a[44:47]\n" \
X_"a[246:247], v37 offset:9216\n" \
M_"a[44:47], a[200:203], a[236:239], a[44:47]\n" \
M_"a[60:63], a[140:143], a[172:175], a[60:63]\n" \
"global_store_dwordx4 v49, v[0:3], s[28:29] offset:0 sc1\n" \
M_"a[60:63], a[204:207], a[236:239], a[60:63]\n" \
M_"a[56:59], a[140:143], a[168:171], a[56:59]\n" \
X_"a[184:185], v38 offset:0\n" \
M_"a[56:59], a[204:207], a[232:235], a[56:59]\n" \
M_"a[52:55], a[140:143], a[164:167], a[52:55]\n" \
M_"a[52:55], a[204:207], a[228:231], a[52:55]\n" \
X_"a[186:187], v38 offset:1024\n" \
M_"a[48:51], a[140:143], a[160:163], a[48:51]\n" \
M_"a[48:51], a[204:207], a[224:227], a[48:51]\n" \
M_"a[64:67], a[144:147], a[160:163], a[64:67]\n" \
"global_store_dwordx4 v49, v[0:3], s[28:29] offset:1024 sc1\n" \
M_"a[64:67], a[208:211], a[224:227], a[64:67]\n" \
X_"a[248:249], v38 offset:8192\n" \
M_"a[68:71], a[144:147], a[164:167], a[68:71]\n" \
M_"a[68:71], a[208:211], a[228:231], a[68:71]\n" \
X_"a[250:251], v38 offset:9216\n" \
M_"a[72:75], a[144:147], a[168:171], a[72:75]\n" \
M_"a[72:75], a[208:211], a[232:235], a[72:75]\n" \
M_"a[76:79], a[144:147], a[172:175], a[76:79]\n" \
M_"a[76:79], a[208:211], a[236:239], a[76:79]\n" \
X_"a[188:189], v39 offset:0\n" \
M_"a[92:95], a[148:151], a[172:175], a[92:95]\n" \
"s_mov_b64 exec, 0xffff\n" \
"global_store_dwordx4 v49, v[0:3], s[28:29] offset:2048 sc1\n" \
"s_mov_b64 exec, -1\n" \
"s_nop 0\n" \
M_"a[92:95], a[212:215], a[236:239], a[92:95]\n" \
M_"a[88:91], a[148:151], a[168:171], a[88:91]\n" \
X_"a[190:191], v39 offset:1024\n" \
M_"a[88:91], a[212:215], a[232:235], a[88:91]\n" \
M_"a[84:87], a[148:151], a[164:167], a[84:87]\n" \
M_"a[84:87], a[212:215], a[228:231], a[84:87]\n" \
X_"a[252:253], v39 offset:8192\n" \
M_"a[80:83], a[148:151], a[160:163], a[80:83]\n" \
M_"a[80:83], a[212:215], a[224:227], a[80:83]\n" \
M_"a[96:99], a[152:155], a[160:163], a[96:99]\n" \
X_"a[254:255], v39 offset:9216\n" \
M_"a[96:99], a[216:219], a[224:227], a[96:99]\n" \
M_"a[100:103], a[152:155], a[164:167], a[100:103]\n" \
M_"a[100:103], a[216:219], a[228:231], a[100:103]\n" \
M_"a[104:107], a[152:155], a[168:171], a[104:107]\n" \
M_"a[104:107], a[216:219], a[232:235], a[104:107]\n" \
M_"a[108:111], a[152:155], a[172:175], a[108:111]\n" \
M_"a[108:111], a[216:219], a[236:239], a[108:111]\n" \
"s_waitcnt lgkmcnt(15)\n" \
M_"a[124:127], a[156:159], a[172:175], a[124:127]\n" \
M_"a[124:127], a[220:223], a[236:239], a[124:127]\n" \
M_"a[120:123], a[156:159], a[168:171], a[120:123]\n" \
M_"a[120:123], a[220:223], a[232:235], a[120:123]\n" \
M_"a[116:119], a[156:159], a[164:167], a[116:119]\n" \
M_"a[116:119], a[220:223], a[228:231], a[116:119]\n" \
M_"a[112:115], a[156:159], a[160:163], a[112:115]\n" \
"s_waitcnt vmcnt(3) lgkmcnt(0)\n" \
M_"a[112:115], a[220:223], a[224:227], a[112:115]\n" \
"s_barrier\n" \
"v_cmp_ne_u32_e32 vcc, s62, v44\n" \
"s_nop 4\n" \
"s_cbranch_vccnz L_poll61_%=\n" \
"L_pollr61_%=:\n" \
"s_mov_b32 s68, s12\n" \
"s_and_b32 s69, s13, 0xffff\n" \
".fill (L_pb + 57056 - .) / 4, 4, 0xBF800000\n" \
"L_pp14:\n" \
"s_cbranch_execz L_pret\n" \
"s_mov_b32 s70, 0x480000\n" \
"s_mov_b32 s71, 0x20000\n" \
M_"v[52:55], a[128:131], a[176:179], v[52:55]\n" \
"s_add_u32 m0, s40, 0x0\n" \
"s_add_u32 s85, s39, 0x0\n" \
"s_nop 0\n" \
M_"v[52:55], a[192:195], a[240:243], v[52:55]\n" \
B_"v20, s[80:83], s85 offen sc1 lds\n" \
M_"v[56:59], a[128:131], a[180:183], v[56:59]\n" \
X_"a[160:161], v32 offset:32768\n" \
M_"v[56:59], a[192:195], a[244:247], v[56:59]\n" \
"s_add_u32 m0, s40, 0x1000\n" \
"s_add_u32 s85, s39, 0x1000\n" \
M_"v[60:63], a[128:131], a[184:187], v[60:63]\n" \
B_"v20, s[80:83], s85 offen sc1 lds\n" \
M_"v[60:63], a[192:195], a[248:251], v[60:63]\n" \
X_"a[162:163], v32 offset:33792\n" \
M_"v[64:67], a[128:131], a[188:191], v[64:67]\n" \
X_"a[224:225], v32 offset:40960\n" \
M_"v[64:67], a[192:195], a[252:255], v[64:67]\n" \
R_"a[128:131], v28 offset:49152\n" \
M_"v[80:83], a[132:135], a[188:191], v[80:83]\n" \
R_"a[192:195], v29 offset:49152\n" \
M_"v[80:83], a[196:199], a[252:255], v[80:83]\n" \
"s_add_u32 m0, s40, 0x3000\n" \
"s_movk_i32 s84, 0x0\n" \
M_"v[76:79], a[132:135], a[184:187], v[76:79]\n" \
B_"v21, s[76:79], s84 offen offset:0 lds\n" \
M_"v[76:79], a[196:199], a[248:251], v[76:79]\n" \
"s_add_u32 m0, s30, 0x0\n" \
"s_movk_i32 s84, 0x0\n" \
"s_nop 0\n" \
M_"v[72:75], a[132:135], a[180:183], v[72:75]\n" \
B_"v24, s[76:79], s84 offen offset:0 lds\n" \
M_"v[72:75], a[196:199], a[244:247], v[72:75]\n" \
X_"a[226:227], v32 offset:41984\n" \
M_"v[68:71], a[132:135], a[176:179], v[68:71]\n" \
X_"a[164:165], v33 offset:32768\n" \
M_"v[68:71], a[196:199], a[240:243], v[68:71]\n" \
R_"a[132:135], v28 offset:51200\n" \
M_"v[84:87], a[136:139], a[176:179], v[84:87]\n" \
R_"a[196:199], v29 offset:51200\n" \
M_"v[84:87], a[200:203], a[240:243], v[84:87]\n" \
"s_movk_i32 s84, 0x2000\n" \
M_"v[88:91], a[136:139], a[180:183], v[88:91]\n" \
B_"v25, s[76:79], s84 offen offset:1024 lds\n" \
M_"v[88:91], a[200:203], a[244:247], v[88:91]\n" \
"s_movk_i32 s84, 0x4000\n" \
M_"v[92:95], a[136:139], a[184:187], v[92:95]\n" \
B_"v24, s[76:79], s84 offen offset:2048 lds\n" \
M_"v[92:95], a[200:203], a[248:251], v[92:95]\n" \
X_"a[166:167], v33 offset:33792\n" \
M_"v[96:99], a[136:139], a[188:191], v[96:99]\n" \
X_"a[228:229], v33 offset:40960\n" \
M_"v[96:99], a[200:203], a[252:255], v[96:99]\n" \
R_"a[136:139], v28 offset:53248\n" \
M_"v[112:115], a[140:143], a[188:191], v[112:115]\n" \
R_"a[200:203], v29 offset:53248\n" \
M_"v[112:115], a[204:207], a[252:255], v[112:115]\n" \
"s_movk_i32 s84, 0x6000\n" \
M_"v[108:111], a[140:143], a[184:187], v[108:111]\n" \
B_"v25, s[76:79], s84 offen offset:3072 lds\n" \
M_"v[108:111], a[204:207], a[248:251], v[108:111]\n" \
"s_add_u32 m0, s31, 0x0\n" \
"s_movk_i32 s84, 0x0\n" \
M_"v[104:107], a[140:143], a[180:183], v[104:107]\n" \
B_"v26, s[68:71], s84 offen offset:0 lds\n" \
M_"v[104:107], a[204:207], a[244:247], v[104:107]\n" \
"s_movk_i32 s84, 0x7c00\n" \
M_"v[100:103], a[140:143], a[176:179], v[100:103]\n" \
B_"v26, s[68:71], s84 offen offset:1024 lds\n" \
M_"v[100:103], a[204:207], a[240:243], v[100:103]\n" \
R_"a[140:143], v28 offset:55296\n" \
M_"v[116:119], a[144:147], a[176:179], v[116:119]\n" \
R_"a[204:207], v29 offset:55296\n" \
M_"v[116:119], a[208:211], a[240:243], v[116:119]\n" \
"s_addk_i32 s84, 0x7c00\n" \
M_"v[120:123], a[144:147], a[180:183], v[120:123]\n" \
B_"v27, s[68:71], s84 offen offset:2048 lds\n" \
M_"v[120:123], a[208:211], a[244:247], v[120:123]\n" \
X_"a[230:231], v33 offset:41984\n" \
M_"v[124:127], a[144:147], a[184:187], v[124:127]\n" \
"s_addk_i32 s84, 0x7c00\n" \
M_"v[124:127], a[208:211], a[248:251], v[124:127]\n" \
B_"v27, s[68:71], s84 offen offset:3072 lds\n" \
M_"v[128:131], a[144:147], a[188:191], v[128:131]\n" \
X_"a[168:169], v34 offset:32768\n" \
M_"v[128:131], a[208:211], a[252:255], v[128:131]\n" \
R_"a[144:147], v28 offset:32768\n" \
M_"v[144:147], a[148:151], a[188:191], v[144:147]\n" \
R_"a[208:211], v29 offset:32768\n" \
M_"v[144:147], a[212:215], a[252:255], v[144:147]\n" \
"s_add_u32 m0, s30, 0x1000\n" \
"s_mov_b32 s84, 0x9000\n" \
M_"v[140:143], a[148:151], a[184:187], v[140:143]\n" \
B_"v24, s[76:79], s84 offen offset:0 lds\n" \
M_"v[140:143], a[212:215], a[248:251], v[140:143]\n" \
X_"a[170:171], v34 offset:33792\n" \
M_"v[136:139], a[148:151], a[180:183], v[136:139]\n" \
"s_addk_i32 s84, 0x2000\n" \
"s_nop 0\n" \
M_"v[136:139], a[212:215], a[244:247], v[136:139]\n" \
B_"v25, s[76:79], s84 offen offset:1024 lds\n" \
M_"v[132:135], a[148:151], a[176:179], v[132:135]\n" \
X_"a[232:233], v34 offset:40960\n" \
M_"v[132:135], a[212:215], a[240:243], v[132:135]\n" \
R_"a[148:151], v28 offset:34816\n" \
M_"v[148:151], a[152:155], a[176:179], v[148:151]\n" \
R_"a[212:215], v29 offset:34816\n" \
M_"v[148:151], a[216:219], a[240:243], v[148:151]\n" \
X_"a[234:235], v34 offset:41984\n" \
M_"v[152:155], a[152:155], a[180:183], v[152:155]\n" \
X_"a[172:173], v35 offset:32768\n" \
M_"v[152:155], a[216:219], a[244:247], v[152:155]\n" \
X_"a[174:175], v35 offset:33792\n" \
M_"v[156:159], a[152:155], a[184:187], v[156:159]\n" \
X_"a[236:237], v35 offset:40960\n" \
M_"v[156:159], a[216:219], a[248:251], v[156:159]\n" \
X_"a[238:239], v35 offset:41984\n" \
M_"v[160:163], a[152:155], a[188:191], v[160:163]\n" \
M_"v[160:163], a[216:219], a[252:255], v[160:163]\n" \
R_"a[152:155], v28 offset:36864\n" \
M_"v[176:179], a[156:159], a[188:191], v[176:179]\n" \
R_"a[216:219], v29 offset:36864\n" \
M_"v[176:179], a[220:223], a[252:255], v[176:179]\n" \
M_"v[172:175], a[156:159], a[184:187], v[172:175]\n" \
M_"v[172:175], a[220:223], a[248:251], v[172:175]\n" \
M_"v[168:171], a[156:159], a[180:183], v[168:171]\n" \
M_"v[168:171], a[220:223], a[244:247], v[168:171]\n" \
M_"v[164:167], a[156:159], a[176:179], v[164:167]\n" \
M_"v[164:167], a[220:223], a[240:243], v[164:167]\n" \
R_"a[156:159], v28 offset:38912\n" \
R_"a[220:223], v29 offset:38912\n" \
"s_waitcnt lgkmcnt(15)\n" \
M_"a[0:3], a[128:131], a[160:163], a[0:3]\n" \
"s_addk_i32 s84, 0x2000\n" \
M_"a[0:3], a[192:195], a[224:227], a[0:3]\n" \
B_"v24, s[76:79], s84 offen offset:2048 lds\n" \
M_"a[4:7], a[128:131], a[164:167], a[4:7]\n" \
X_"a[176:177], v36 offset:32768\n" \
M_"a[4:7], a[192:195], a[228:231], a[4:7]\n" \
"s_addk_i32 s84, 0x2000\n" \
"s_waitcnt lgkmcnt(13)\n" \
M_"a[8:11], a[128:131], a[168:171], a[8:11]\n" \
B_"v25, s[76:79], s84 offen offset:3072 lds\n" \
"s_waitcnt lgkmcnt(9)\n" \
M_"a[8:11], a[192:195], a[232:235], a[8:11]\n" \
X_"a[178:179], v36 offset:33792\n" \
"s_waitcnt lgkmcnt(8)\n" \
M_"a[12:15], a[128:131], a[172:175], a[12:15]\n" \
X_"a[240:241], v36 offset:40960\n" \
"s_waitcnt lgkmcnt(7)\n" \
M_"a[12:15], a[192:195], a[236:239], a[12:15]\n" \
M_"a[28:31], a[132:135], a[172:175], a[28:31]\n" \
M_"a[28:31], a[196:199], a[236:239], a[28:31]\n" \
"s_add_u32 m0, s30, 0x20000\n" \
"s_movk_i32 s84, 0x100\n" \
M_"a[24:27], a[132:135], a[168:171], a[24:27]\n" \
B_"v24, s[76:79], s84 offen offset:0 lds\n" \
M_"a[24:27], a[196:199], a[232:235], a[24:27]\n" \
X_"a[242:243], v36 offset:41984\n" \
M_"a[20:23], a[132:135], a[164:167], a[20:23]\n" \
"v_accvgpr_read_b32 v184, a12\n" \
"v_accvgpr_read_b32 v185, a13\n" \
M_"a[20:23], a[196:199], a[228:231], a[20:23]\n" \
X_"a[180:181], v37 offset:32768\n" \
"v_accvgpr_read_b32 v186, a14\n" \
M_"a[16:19], a[132:135], a[160:163], a[16:19]\n" \
"s_movk_i32 s84, 0x2100\n" \
"s_nop 0\n" \
M_"a[16:19], a[196:199], a[224:227], a[16:19]\n" \
B_"v25, s[76:79], s84 offen offset:1024 lds\n" \
M_"a[32:35], a[136:139], a[160:163], a[32:35]\n" \
X_"a[182:183], v37 offset:33792\n" \
"v_accvgpr_read_b32 v187, a15\n" \
M_"a[32:35], a[200:203], a[224:227], a[32:35]\n" \
"v_accvgpr_read_b32 v180, a28\n" \
"v_accvgpr_read_b32 v181, a29\n" \
M_"a[36:39], a[136:139], a[164:167], a[36:39]\n" \
"v_accvgpr_read_b32 v182, a30\n" \
"v_accvgpr_read_b32 v183, a31\n" \
M_"a[36:39], a[200:203], a[228:231], a[36:39]\n" \
"s_movk_i32 s84, 0x4100\n" \
"s_nop 0\n" \
M_"a[40:43], a[136:139], a[168:171], a[40:43]\n" \
B_"v24, s[76:79], s84 offen offset:2048 lds\n" \
M_"a[40:43], a[200:203], a[232:235], a[40:43]\n" \
X_"a[244:245], v37 offset:40960\n" \
C_"v184, v184, v185\n" \
M_"a[44:47], a[136:139], a[172:175], a[44:47]\n" \
X_"a[246:247], v37 offset:41984\n" \
C_"v185, v186, v187\n" \
M_"a[44:47], a[200:203], a[236:239], a[44:47]\n" \
C_"v186, v180, v181\n" \
C_"v187, v182, v183\n" \
M_"a[60:63], a[140:143], a[172:175], a[60:63]\n" \
"s_movk_i32 s84, 0x6100\n" \
M_"a[60:63], a[204:207], a[236:239], a[60:63]\n" \
B_"v25, s[76:79], s84 offen offset:3072 lds\n" \
M_"a[56:59], a[140:143], a[168:171], a[56:59]\n" \
X_"a[184:185], v38 offset:32768\n" \
"v_accvgpr_read_b32 v188, a8\n" \
M_"a[56:59], a[204:207], a[232:235], a[56:59]\n" \
"v_accvgpr_read_b32 v189, a9\n" \
"v_accvgpr_read_b32 v190, a10\n" \
M_"a[52:55], a[140:143], a[164:167], a[52:55]\n" \
"s_add_u32 m0, s30, 0x21000\n" \
"s_addk_i32 s84, 0x3000\n" \
M_"a[52:55], a[204:207], a[228:231], a[52:55]\n" \
B_"v24, s[76:79], s84 offen offset:0 lds\n" \
M_"a[48:51], a[140:143], a[160:163], a[48:51]\n" \
X_"a[186:187], v38 offset:33792\n" \
"v_accvgpr_read_b32 v191, a11\n" \
M_"a[48:51], a[204:207], a[224:227], a[48:51]\n" \
"v_accvgpr_read_b32 v180, a24\n" \
"v_accvgpr_read_b32 v181, a25\n" \
M_"a[64:67], a[144:147], a[160:163], a[64:67]\n" \
X_"a[248:249], v38 offset:40960\n" \
"v_accvgpr_read_b32 v182, a26\n" \
M_"a[64:67], a[208:211], a[224:227], a[64:67]\n" \
"s_addk_i32 s84, 0x2000\n" \
"s_nop 0\n" \
M_"a[68:71], a[144:147], a[164:167], a[68:71]\n" \
B_"v25, s[76:79], s84 offen offset:1024 lds\n" \
M_"a[68:71], a[208:211], a[228:231], a[68:71]\n" \
X_"a[250:251], v38 offset:41984\n" \
"v_accvgpr_read_b32 v183, a27\n" \
M_"a[72:75], a[144:147], a[168:171], a[72:75]\n" \
C_"v188, v188, v189\n" \
C_"v189, v190, v191\n" \
M_"a[72:75], a[208:211], a[232:235], a[72:75]\n" \
C_"v190, v180, v181\n" \
C_"v191, v182, v183\n" \
M_"a[76:79], a[144:147], a[172:175], a[76:79]\n" \
"s_addk_i32 s84, 0x2000\n" \
M_"a[76:79], a[208:211], a[236:239], a[76:79]\n" \
B_"v24, s[76:79], s84 offen offset:2048 lds\n" \
M_"a[92:95], a[148:151], a[172:175], a[92:95]\n" \
"s_add_u32 s89, s41, 0x0\n" \
"buffer_store_dwordx4 v[184:187], v20, s[80:83], s89 offen offset:3072 sc1\n" \
M_"a[92:95], a[212:215], a[236:239], a[92:95]\n" \
X_"a[188:189], v39 offset:32768\n" \
"v_accvgpr_read_b32 v192, a4\n" \
M_"a[88:91], a[148:151], a[168:171], a[88:91]\n" \
X_"a[190:191], v39 offset:33792\n" \
"v_accvgpr_read_b32 v193, a5\n" \
M_"a[88:91], a[212:215], a[232:235], a[88:91]\n" \
"s_addk_i32 s84, 0x2000\n" \
M_"a[84:87], a[148:151], a[164:167], a[84:87]\n" \
B_"v25, s[76:79], s84 offen offset:3072 lds\n" \
M_"a[84:87], a[212:215], a[228:231], a[84:87]\n" \
X_"a[252:253], v39 offset:40960\n" \
"v_accvgpr_read_b32 v194, a6\n" \
M_"a[80:83], a[148:151], a[160:163], a[80:83]\n" \
"s_add_u32 s88, s41, 0x0\n" \
"buffer_store_dwordx4 v[188:191], v20, s[80:83], s88 offen offset:2048 sc1\n" \
M_"a[80:83], a[212:215], a[224:227], a[80:83]\n" \
"v_accvgpr_read_b32 v195, a7\n" \
"v_accvgpr_read_b32 v180, a20\n" \
"s_waitcnt lgkmcnt(15)\n" \
"s_nop 0\n" \
M_"a[96:99], a[152:155], a[160:163], a[96:99]\n" \
X_"a[254:255], v39 offset:41984\n" \
"v_accvgpr_read_b32 v181, a21\n" \
M_"a[96:99], a[216:219], a[224:227], a[96:99]\n" \
"v_accvgpr_read_b32 v182, a22\n" \
"v_accvgpr_read_b32 v183, a23\n" \
M_"a[100:103], a[152:155], a[164:167], a[100:103]\n" \
C_"v192, v192, v193\n" \
C_"v193, v194, v195\n" \
M_"a[100:103], a[216:219], a[228:231], a[100:103]\n" \
C_"v194, v180, v181\n" \
C_"v195, v182, v183\n" \
M_"a[104:107], a[152:155], a[168:171], a[104:107]\n" \
"s_add_u32 s89, s41, 0x0\n" \
"s_nop 0\n" \
"buffer_store_dwordx4 v[192:195], v20, s[80:83], s89 offen offset:1024 sc1\n" \
M_"a[104:107], a[216:219], a[232:235], a[104:107]\n" \
"v_accvgpr_read_b32 v196, a0\n" \
"v_accvgpr_read_b32 v197, a1\n" \
M_"a[108:111], a[152:155], a[172:175], a[108:111]\n" \
"v_accvgpr_read_b32 v198, a2\n" \
"v_accvgpr_read_b32 v199, a3\n" \
M_"a[108:111], a[216:219], a[236:239], a[108:111]\n" \
"v_accvgpr_read_b32 v180, a16\n" \
"v_accvgpr_read_b32 v181, a17\n" \
M_"a[124:127], a[156:159], a[172:175], a[124:127]\n" \
"v_accvgpr_read_b32 v182, a18\n" \
"v_accvgpr_read_b32 v183, a19\n" \
M_"a[124:127], a[220:223], a[236:239], a[124:127]\n" \
C_"v196, v196, v197\n" \
C_"v197, v198, v199\n" \
M_"a[120:123], a[156:159], a[168:171], a[120:123]\n" \
C_"v198, v180, v181\n" \
C_"v199, v182, v183\n" \
"s_add_u32 s88, s41, 0x0\n" \
"s_nop 0\n" \
"buffer_store_dwordx4 v[196:199], v20, s[80:83], s88 offen offset:0 sc1\n" \
M_"a[120:123], a[220:223], a[232:235], a[120:123]\n" \
"v_accvgpr_read_b32 v200, a44\n" \
"v_accvgpr_read_b32 v201, a45\n" \
M_"a[116:119], a[156:159], a[164:167], a[116:119]\n" \
"v_accvgpr_read_b32 v202, a46\n" \
"v_accvgpr_read_b32 v203, a47\n" \
M_"a[116:119], a[220:223], a[228:231], a[116:119]\n" \
"v_accvgpr_read_b32 v180, a60\n" \
"v_accvgpr_read_b32 v181, a61\n" \
M_"a[112:115], a[156:159], a[160:163], a[112:115]\n" \
"v_accvgpr_read_b32 v182, a62\n" \
"v_accvgpr_read_b32 v183, a63\n" \
M_"a[112:115], a[220:223], a[224:227], a[112:115]\n" \
C_"v200, v200, v201\n" \
C_"v201, v202, v203\n" \
"s_waitcnt vmcnt(12) lgkmcnt(0)\n" \
"s_barrier\n" \
"v_xor_b32_e32 v36, 0x4000, v36\n" \
"v_xor_b32_e64 v32, 0x0, v48\n" \
"v_xor_b32_e32 v37, 0x4000, v37\n" \
"v_xor_b32_e64 v33, 0x20, v48\n" \
"v_xor_b32_e32 v38, 0x4000, v38\n" \
"v_xor_b32_e64 v34, 0x40, v48\n" \
"v_xor_b32_e32 v39, 0x4000, v39\n" \
"v_xor_b32_e32 v35, 0x60, v48\n" \
"v_add_u32_e32 v28, s43, v28\n" \
"v_add_u32_e32 v29, s43, v29\n" \
M_"v[116:119], a[144:147], a[176:179], v[116:119]\n" \
"s_add_u32 m0, s40, 0x8000\n" \
"s_add_u32 s85, s39, 0x2000\n" \
M_"v[116:119], a[208:211], a[240:243], v[116:119]\n" \
B_"v20, s[80:83], s85 offen sc1 lds\n" \
M_"v[120:123], a[144:147], a[180:183], v[120:123]\n" \
X_"a[160:161], v32 offset:0\n" \
C_"v202, v180, v181\n" \
M_"v[120:123], a[208:211], a[244:247], v[120:123]\n" \
"s_add_u32 m0, s40, 0x9000\n" \
"s_add_u32 s85, s39, 0x3000\n" \
M_"v[124:127], a[144:147], a[184:187], v[124:127]\n" \
B_"v20, s[80:83], s85 offen sc1 lds\n" \
M_"v[124:127], a[208:211], a[248:251], v[124:127]\n" \
X_"a[162:163], v32 offset:512\n" \
C_"v203, v182, v183\n" \
M_"v[128:131], a[144:147], a[188:191], v[128:131]\n" \
X_"a[224:225], v32 offset:4096\n" \
"v_accvgpr_read_b32 v204, a40\n" \
M_"v[128:131], a[208:211], a[252:255], v[128:131]\n" \
R_"a[144:147], v28 offset:8192\n" \
"v_accvgpr_read_b32 v205, a41\n" \
M_"v[144:147], a[148:151], a[188:191], v[144:147]\n" \
R_"a[208:211], v29 offset:8192\n" \
"v_accvgpr_read_b32 v206, a42\n" \
M_"v[144:147], a[212:215], a[252:255], v[144:147]\n" \
"s_add_u32 m0, s40, 0xb000\n" \
"s_movk_i32 s84, 0x80\n" \
M_"v[140:143], a[148:151], a[184:187], v[140:143]\n" \
B_"v21, s[76:79], s84 offen offset:0 lds\n" \
M_"v[140:143], a[212:215], a[248:251], v[140:143]\n" \
"s_add_u32 m0, s30, 0x8000\n" \
"s_movk_i32 s84, 0x80\n" \
M_"v[136:139], a[148:151], a[180:183], v[136:139]\n" \
B_"v24, s[76:79], s84 offen offset:0 lds\n" \
M_"v[136:139], a[212:215], a[244:247], v[136:139]\n" \
X_"a[226:227], v32 offset:4608\n" \
"v_accvgpr_read_b32 v207, a43\n" \
M_"v[132:135], a[148:151], a[176:179], v[132:135]\n" \
X_"a[164:165], v33 offset:0\n" \
"v_accvgpr_read_b32 v180, a56\n" \
M_"v[132:135], a[212:215], a[240:243], v[132:135]\n" \
R_"a[148:151], v28 offset:10240\n" \
"v_accvgpr_read_b32 v181, a57\n" \
M_"v[148:151], a[152:155], a[176:179], v[148:151]\n" \
R_"a[212:215], v29 offset:10240\n" \
"v_accvgpr_read_b32 v182, a58\n" \
M_"v[148:151], a[216:219], a[240:243], v[148:151]\n" \
"s_movk_i32 s84, 0x2080\n" \
M_"v[152:155], a[152:155], a[180:183], v[152:155]\n" \
B_"v25, s[76:79], s84 offen offset:1024 lds\n" \
M_"v[152:155], a[216:219], a[244:247], v[152:155]\n" \
"s_movk_i32 s84, 0x4080\n" \
M_"v[156:159], a[152:155], a[184:187], v[156:159]\n" \
B_"v24, s[76:79], s84 offen offset:2048 lds\n" \
M_"v[156:159], a[216:219], a[248:251], v[156:159]\n" \
X_"a[166:167], v33 offset:512\n" \
"v_accvgpr_read_b32 v183, a59\n" \
M_"v[160:163], a[152:155], a[188:191], v[160:163]\n" \
X_"a[228:229], v33 offset:4096\n" \
C_"v204, v204, v205\n" \
M_"v[160:163], a[216:219], a[252:255], v[160:163]\n" \
R_"a[152:155], v28 offset:12288\n" \
C_"v205, v206, v207\n" \
M_"v[176:179], a[156:159], a[188:191], v[176:179]\n" \
R_"a[216:219], v29 offset:12288\n" \
C_"v206, v180, v181\n" \
M_"v[176:179], a[220:223], a[252:255], v[176:179]\n" \
"s_movk_i32 s84, 0x6080\n" \
M_"v[172:175], a[156:159], a[184:187], v[172:175]\n" \
B_"v25, s[76:79], s84 offen offset:3072 lds\n" \
M_"v[172:175], a[220:223], a[248:251], v[172:175]\n" \
"s_add_u32 m0, s31, 0x8000\n" \
"s_mov_b32 s84, 0x80000\n" \
M_"v[168:171], a[156:159], a[180:183], v[168:171]\n" \
B_"v26, s[68:71], s84 offen offset:0 lds\n" \
M_"v[168:171], a[220:223], a[244:247], v[168:171]\n" \
"s_addk_i32 s84, 0x7c00\n" \
M_"v[164:167], a[156:159], a[176:179], v[164:167]\n" \
B_"v26, s[68:71], s84 offen offset:1024 lds\n" \
M_"v[164:167], a[220:223], a[240:243], v[164:167]\n" \
R_"a[156:159], v28 offset:14336\n" \
C_"v207, v182, v183\n" \
M_"v[84:87], a[136:139], a[176:179], v[84:87]\n" \
R_"a[220:223], v29 offset:14336\n" \
"v_accvgpr_read_b32 v208, a36\n" \
M_"v[84:87], a[200:203], a[240:243], v[84:87]\n" \
"s_addk_i32 s84, 0x7c00\n" \
M_"v[88:91], a[136:139], a[180:183], v[88:91]\n" \
B_"v27, s[68:71], s84 offen offset:2048 lds\n" \
M_"v[88:91], a[200:203], a[244:247], v[88:91]\n" \
X_"a[230:231], v33 offset:4608\n" \
"v_accvgpr_read_b32 v209, a37\n" \
M_"v[92:95], a[136:139], a[184:187], v[92:95]\n" \
"s_addk_i32 s84, 0x7c00\n" \
M_"v[92:95], a[200:203], a[248:251], v[92:95]\n" \
B_"v27, s[68:71], s84 offen offset:3072 lds\n" \
M_"v[96:99], a[136:139], a[188:191], v[96:99]\n" \
X_"a[172:173], v35 offset:0\n" \
"v_accvgpr_read_b32 v210, a38\n" \
M_"v[96:99], a[200:203], a[252:255], v[96:99]\n" \
R_"a[136:139], v28 offset:4096\n" \
"v_accvgpr_read_b32 v211, a39\n" \
M_"v[112:115], a[140:143], a[188:191], v[112:115]\n" \
R_"a[200:203], v29 offset:4096\n" \
"v_accvgpr_read_b32 v180, a52\n" \
M_"v[112:115], a[204:207], a[252:255], v[112:115]\n" \
"s_add_u32 m0, s30, 0x9000\n" \
"s_mov_b32 s84, 0x9080\n" \
M_"v[108:111], a[140:143], a[184:187], v[108:111]\n" \
B_"v24, s[76:79], s84 offen offset:0 lds\n" \
M_"v[108:111], a[204:207], a[248:251], v[108:111]\n" \
X_"a[174:175], v35 offset:512\n" \
"v_accvgpr_read_b32 v181, a53\n" \
M_"v[104:107], a[140:143], a[180:183], v[104:107]\n" \
"s_addk_i32 s84, 0x2000\n" \
"s_nop 0\n" \
M_"v[104:107], a[204:207], a[244:247], v[104:107]\n" \
B_"v25, s[76:79], s84 offen offset:1024 lds\n" \
M_"v[100:103], a[140:143], a[176:179], v[100:103]\n" \
X_"a[236:237], v35 offset:4096\n" \
"v_accvgpr_read_b32 v182, a54\n" \
M_"v[100:103], a[204:207], a[240:243], v[100:103]\n" \
R_"a[140:143], v28 offset:6144\n" \
"v_accvgpr_read_b32 v183, a55\n" \
M_"v[52:55], a[128:131], a[176:179], v[52:55]\n" \
R_"a[204:207], v29 offset:6144\n" \
C_"v208, v208, v209\n" \
M_"v[52:55], a[192:195], a[240:243], v[52:55]\n" \
X_"a[238:239], v35 offset:4608\n" \
C_"v209, v210, v211\n" \
M_"v[56:59], a[128:131], a[180:183], v[56:59]\n" \
X_"a[168:169], v34 offset:0\n" \
C_"v210, v180, v181\n" \
M_"v[56:59], a[192:195], a[244:247], v[56:59]\n" \
X_"a[170:171], v34 offset:512\n" \
C_"v211, v182, v183\n" \
M_"v[60:63], a[128:131], a[184:187], v[60:63]\n" \
X_"a[232:233], v34 offset:4096\n" \
"v_accvgpr_read_b32 v212, a32\n" \
M_"v[60:63], a[192:195], a[248:251], v[60:63]\n" \
X_"a[234:235], v34 offset:4608\n" \
"v_accvgpr_read_b32 v213, a33\n" \
M_"v[64:67], a[128:131], a[188:191], v[64:67]\n" \
"v_accvgpr_read_b32 v214, a34\n" \
"v_accvgpr_read_b32 v215, a35\n" \
M_"v[64:67], a[192:195], a[252:255], v[64:67]\n" \
R_"a[128:131], v28 offset:0\n" \
"v_accvgpr_read_b32 v180, a48\n" \
M_"v[80:83], a[132:135], a[188:191], v[80:83]\n" \
R_"a[192:195], v29 offset:0\n" \
"v_accvgpr_read_b32 v181, a49\n" \
M_"v[80:83], a[196:199], a[252:255], v[80:83]\n" \
"v_accvgpr_read_b32 v182, a50\n" \
"v_accvgpr_read_b32 v183, a51\n" \
M_"v[76:79], a[132:135], a[184:187], v[76:79]\n" \
C_"v212, v212, v213\n" \
C_"v213, v214, v215\n" \
M_"v[76:79], a[196:199], a[248:251], v[76:79]\n" \
C_"v214, v180, v181\n" \
C_"v215, v182, v183\n" \
M_"v[72:75], a[132:135], a[180:183], v[72:75]\n" \
"v_accvgpr_read_b32 v216, a68\n" \
"v_accvgpr_read_b32 v217, a69\n" \
M_"v[72:75], a[196:199], a[244:247], v[72:75]\n" \
"v_accvgpr_read_b32 v218, a70\n" \
"v_accvgpr_read_b32 v219, a71\n" \
M_"v[68:71], a[132:135], a[176:179], v[68:71]\n" \
"v_accvgpr_read_b32 v180, a84\n" \
"v_accvgpr_read_b32 v181, a85\n" \
M_"v[68:71], a[196:199], a[240:243], v[68:71]\n" \
R_"a[132:135], v28 offset:2048\n" \
R_"a[196:199], v29 offset:2048\n" \
"s_waitcnt lgkmcnt(15)\n" \
M_"a[0:3], a[160:163], a[144:147], 0\n" \
"s_addk_i32 s84, 0x2000\n" \
"v_accvgpr_read_b32 v182, a86\n" \
M_"a[0:3], a[224:227], a[208:211], a[0:3]\n" \
B_"v24, s[76:79], s84 offen offset:2048 lds\n" \
"v_accvgpr_read_b32 v183, a87\n" \
M_"a[4:7], a[164:167], a[144:147], 0\n" \
X_"a[176:177], v36 offset:0\n" \
C_"v216, v216, v217\n" \
M_"a[4:7], a[228:231], a[208:211], a[4:7]\n" \
"s_addk_i32 s84, 0x2000\n" \
C_"v217, v218, v219\n" \
"s_waitcnt lgkmcnt(13)\n" \
M_"a[12:15], a[172:175], a[144:147], 0\n" \
B_"v25, s[76:79], s84 offen offset:3072 lds\n" \
C_"v218, v180, v181\n" \
"s_waitcnt lgkmcnt(9)\n" \
M_"a[12:15], a[236:239], a[208:211], a[12:15]\n" \
X_"a[178:179], v36 offset:1024\n" \
C_"v219, v182, v183\n" \
"s_waitcnt lgkmcnt(8)\n" \
M_"a[8:11], a[144:147], a[168:171], 0\n" \
X_"a[240:241], v36 offset:8192\n" \
"v_accvgpr_read_b32 v220, a64\n" \
"s_waitcnt lgkmcnt(7)\n" \
"s_nop 0\n" \
M_"a[8:11], a[208:211], a[232:235], a[8:11]\n" \
X_"a[242:243], v36 offset:9216\n" \
"v_accvgpr_read_b32 v221, a65\n" \
M_"a[24:27], a[148:151], a[168:171], 0\n" \
X_"a[180:181], v37 offset:0\n" \
"v_accvgpr_read_b32 v222, a66\n" \
M_"a[24:27], a[212:215], a[232:235], a[24:27]\n" \
X_"a[182:183], v37 offset:1024\n" \
"v_accvgpr_read_b32 v223, a67\n" \
M_"a[28:31], a[172:175], a[148:151], 0\n" \
X_"a[244:245], v37 offset:8192\n" \
"v_accvgpr_read_b32 v180, a80\n" \
M_"a[28:31], a[236:239], a[212:215], a[28:31]\n" \
"s_add_u32 s89, s41, 0x2000\n" \
"buffer_store_dwordx4 v[200:203], v20, s[80:83], s89 offen offset:3072 sc1\n" \
M_"a[20:23], a[164:167], a[148:151], 0\n" \
X_"a[246:247], v37 offset:9216\n" \
"v_accvgpr_read_b32 v181, a81\n" \
M_"a[20:23], a[228:231], a[212:215], a[20:23]\n" \
".fill (L_pb + 61152 - .) / 4, 4, 0xBF800000\n" \
"L_pp15:\n" \
"s_cbranch_execz L_pret\n" \
"s_nop 0\n" \
X_"a[184:185], v38 offset:0\n" \
"v_accvgpr_read_b32 v182, a82\n" \
M_"a[16:19], a[160:163], a[148:151], 0\n" \
X_"a[186:187], v38 offset:1024\n" \
"v_accvgpr_read_b32 v183, a83\n" \
M_"a[16:19], a[224:227], a[212:215], a[16:19]\n" \
C_"v220, v220, v221\n" \
C_"v221, v222, v223\n" \
C_"v222, v180, v181\n" \
M_"a[32:35], a[160:163], a[152:155], 0\n" \
"s_add_u32 s88, s41, 0x2000\n" \
"buffer_store_dwordx4 v[204:207], v20, s[80:83], s88 offen offset:2048 sc1\n" \
M_"a[32:35], a[224:227], a[216:219], a[32:35]\n" \
X_"a[248:249], v38 offset:8192\n" \
C_"v223, v182, v183\n" \
M_"a[36:39], a[164:167], a[152:155], 0\n" \
X_"a[250:251], v38 offset:9216\n" \
C_"v224, v128, v129\n" \
M_"a[36:39], a[228:231], a[216:219], a[36:39]\n" \
X_"a[188:189], v39 offset:0\n" \
C_"v225, v130, v131\n" \
M_"a[44:47], a[172:175], a[152:155], 0\n" \
C_"v226, v144, v145\n" \
C_"v227, v146, v147\n" \
C_"v228, v124, v125\n" \
M_"a[44:47], a[236:239], a[216:219], a[44:47]\n" \
X_"a[190:191], v39 offset:1024\n" \
C_"v229, v126, v127\n" \
M_"a[40:43], a[152:155], a[168:171], 0\n" \
"s_add_u32 s89, s41, 0x2000\n" \
"buffer_store_dwordx4 v[208:211], v20, s[80:83], s89 offen offset:1024 sc1\n" \
M_"a[40:43], a[216:219], a[232:235], a[40:43]\n" \
X_"a[252:253], v39 offset:8192\n" \
C_"v230, v140, v141\n" \
M_"a[56:59], a[156:159], a[168:171], 0\n" \
X_"a[254:255], v39 offset:9216\n" \
C_"v231, v142, v143\n" \
M_"a[56:59], a[220:223], a[232:235], a[56:59]\n" \
C_"v232, v120, v121\n" \
C_"v233, v122, v123\n" \
C_"v234, v136, v137\n" \
M_"a[60:63], a[172:175], a[156:159], 0\n" \
C_"v235, v138, v139\n" \
C_"v236, v116, v117\n" \
C_"v237, v118, v119\n" \
M_"a[60:63], a[236:239], a[220:223], a[60:63]\n" \
"s_add_u32 s88, s41, 0x2000\n" \
"buffer_store_dwordx4 v[212:215], v20, s[80:83], s88 offen offset:0 sc1\n" \
M_"a[52:55], a[164:167], a[156:159], 0\n" \
C_"v238, v132, v133\n" \
C_"v239, v134, v135\n" \
C_"v240, v160, v161\n" \
M_"a[52:55], a[228:231], a[220:223], a[52:55]\n" \
C_"v241, v162, v163\n" \
C_"v242, v176, v177\n" \
C_"v243, v178, v179\n" \
M_"a[48:51], a[160:163], a[156:159], 0\n" \
C_"v244, v156, v157\n" \
C_"v245, v158, v159\n" \
C_"v246, v172, v173\n" \
M_"a[48:51], a[224:227], a[220:223], a[48:51]\n" \
C_"v247, v174, v175\n" \
C_"v184, v152, v153\n" \
C_"v185, v154, v155\n" \
M_"a[64:67], a[164:167], a[140:143], 0\n" \
C_"v186, v168, v169\n" \
C_"v187, v170, v171\n" \
C_"v188, v148, v149\n" \
M_"a[64:67], a[228:231], a[204:207], a[64:67]\n" \
C_"v189, v150, v151\n" \
C_"v190, v164, v165\n" \
C_"v191, v166, v167\n" \
M_"a[80:83], a[172:175], a[140:143], 0\n" \
C_"v192, v96, v97\n" \
C_"v193, v98, v99\n" \
C_"v194, v112, v113\n" \
M_"a[80:83], a[236:239], a[204:207], a[80:83]\n" \
C_"v195, v114, v115\n" \
C_"v196, v92, v93\n" \
C_"v197, v94, v95\n" \
M_"a[68:71], a[140:143], a[168:171], 0\n" \
C_"v198, v108, v109\n" \
C_"v199, v110, v111\n" \
C_"v252, v88, v89\n" \
M_"a[68:71], a[204:207], a[232:235], a[68:71]\n" \
C_"v253, v90, v91\n" \
C_"v254, v104, v105\n" \
C_"v255, v106, v107\n" \
"s_waitcnt vmcnt(4) lgkmcnt(0)\n" \
"s_barrier\n" \
M_"v[116:119], a[176:179], a[144:147], 0\n" \
"s_add_u32 m0, s40, 0x0\n" \
"s_nop 0\n" \
"s_add_u32 s85, s39, 0x4000\n" \
"v_accvgpr_write_b32 a84, v252\n" \
M_"v[116:119], a[240:243], a[208:211], v[116:119]\n" \
B_"v20, s[80:83], s85 offen sc1 lds\n" \
"v_accvgpr_write_b32 a85, v253\n" \
M_"v[120:123], a[180:183], a[144:147], 0\n" \
X_"a[160:161], v32 offset:32768\n" \
"v_accvgpr_write_b32 a86, v254\n" \
M_"v[120:123], a[244:247], a[208:211], v[120:123]\n" \
X_"a[162:163], v32 offset:33280\n" \
"v_accvgpr_write_b32 a87, v255\n" \
M_"v[124:127], a[184:187], a[144:147], 0\n" \
"s_add_u32 m0, s40, 0x1000\n" \
"s_add_u32 s85, s39, 0x5000\n" \
C_"v252, v84, v85\n" \
M_"v[124:127], a[248:251], a[208:211], v[124:127]\n" \
B_"v20, s[80:83], s85 offen sc1 lds\n" \
C_"v253, v86, v87\n" \
M_"v[128:131], a[188:191], a[144:147], 0\n" \
X_"a[224:225], v32 offset:36864\n" \
C_"v254, v100, v101\n" \
M_"v[128:131], a[252:255], a[208:211], v[128:131]\n" \
R_"a[144:147], v28 offset:40960\n" \
C_"v255, v102, v103\n" \
M_"v[144:147], a[188:191], a[148:151], 0\n" \
R_"a[208:211], v29 offset:40960\n" \
C_"v200, v64, v65\n" \
M_"v[144:147], a[252:255], a[212:215], v[144:147]\n" \
"s_add_u32 m0, s40, 0x3000\n" \
"s_movk_i32 s84, 0x100\n" \
"s_nop 0\n" \
C_"v201, v66, v67\n" \
M_"v[140:143], a[184:187], a[148:151], 0\n" \
B_"v21, s[76:79], s84 offen offset:0 lds\n" \
C_"v202, v80, v81\n" \
M_"v[140:143], a[248:251], a[212:215], v[140:143]\n" \
X_"a[226:227], v32 offset:37376\n" \
C_"v203, v82, v83\n" \
M_"v[136:139], a[180:183], a[148:151], 0\n" \
C_"v204, v60, v61\n" \
C_"v205, v62, v63\n" \
C_"v206, v76, v77\n" \
M_"v[136:139], a[244:247], a[212:215], v[136:139]\n" \
X_"a[164:165], v33 offset:32768\n" \
C_"v207, v78, v79\n" \
M_"v[132:135], a[176:179], a[148:151], 0\n" \
C_"v208, v56, v57\n" \
C_"v209, v58, v59\n" \
C_"v210, v72, v73\n" \
M_"v[132:135], a[240:243], a[212:215], v[132:135]\n" \
R_"a[148:151], v28 offset:43008\n" \
C_"v211, v74, v75\n" \
M_"v[148:151], a[176:179], a[152:155], 0\n" \
R_"a[212:215], v29 offset:43008\n" \
C_"v212, v52, v53\n" \
M_"v[148:151], a[240:243], a[216:219], v[148:151]\n" \
"s_add_u32 m0, s31, 0x0\n" \
"s_mov_b32 s84, 0x100000\n" \
C_"v213, v54, v55\n" \
M_"v[152:155], a[180:183], a[152:155], 0\n" \
B_"v26, s[68:71], s84 offen offset:0 lds\n" \
C_"v214, v68, v69\n" \
M_"v[152:155], a[244:247], a[216:219], v[152:155]\n" \
"s_addk_i32 s84, 0x7c00\n" \
C_"v215, v70, v71\n" \
M_"v[156:159], a[184:187], a[152:155], 0\n" \
B_"v26, s[68:71], s84 offen offset:1024 lds\n" \
M_"v[156:159], a[248:251], a[216:219], v[156:159]\n" \
X_"a[166:167], v33 offset:33280\n" \
M_"v[160:163], a[188:191], a[152:155], 0\n" \
X_"a[228:229], v33 offset:36864\n" \
M_"v[160:163], a[252:255], a[216:219], v[160:163]\n" \
R_"a[152:155], v28 offset:45056\n" \
M_"v[176:179], a[188:191], a[156:159], 0\n" \
R_"a[216:219], v29 offset:45056\n" \
M_"v[176:179], a[252:255], a[220:223], v[176:179]\n" \
"s_addk_i32 s84, 0x7c00\n" \
M_"v[172:175], a[184:187], a[156:159], 0\n" \
B_"v27, s[68:71], s84 offen offset:2048 lds\n" \
M_"v[172:175], a[248:251], a[220:223], v[172:175]\n" \
X_"a[230:231], v33 offset:37376\n" \
M_"v[168:171], a[180:183], a[156:159], 0\n" \
"s_addk_i32 s84, 0x7c00\n" \
M_"v[168:171], a[244:247], a[220:223], v[168:171]\n" \
B_"v27, s[68:71], s84 offen offset:3072 lds\n" \
M_"v[164:167], a[176:179], a[156:159], 0\n" \
X_"a[172:173], v35 offset:32768\n" \
M_"v[164:167], a[240:243], a[220:223], v[164:167]\n" \
R_"a[156:159], v28 offset:47104\n" \
M_"v[84:87], a[176:179], a[136:139], 0\n" \
R_"a[220:223], v29 offset:47104\n" \
M_"v[84:87], a[240:243], a[200:203], v[84:87]\n" \
"s_add_u32 s89, s41, 0x3000\n" \
"buffer_store_dwordx4 v[192:195], v20, s[80:83], s89 offen offset:3072 sc1\n" \
M_"v[88:91], a[180:183], a[136:139], 0\n" \
X_"a[174:175], v35 offset:33280\n" \
M_"v[88:91], a[244:247], a[200:203], v[88:91]\n" \
X_"a[236:237], v35 offset:36864\n" \
M_"v[92:95], a[184:187], a[136:139], 0\n" \
X_"a[238:239], v35 offset:37376\n" \
M_"v[92:95], a[248:251], a[200:203], v[92:95]\n" \
"s_add_u32 m0, s30, 0x0\n" \
"s_movk_i32 s84, 0x180\n" \
M_"v[96:99], a[188:191], a[136:139], 0\n" \
B_"v24, s[76:79], s84 offen offset:0 lds\n" \
M_"v[96:99], a[252:255], a[200:203], v[96:99]\n" \
R_"a[136:139], v28 offset:36864\n" \
M_"v[112:115], a[188:191], a[140:143], 0\n" \
R_"a[200:203], v29 offset:36864\n" \
M_"v[112:115], a[252:255], a[204:207], v[112:115]\n" \
X_"a[168:169], v34 offset:32768\n" \
M_"v[108:111], a[184:187], a[140:143], 0\n" \
"s_movk_i32 s84, 0x2180\n" \
"s_nop 0\n" \
M_"v[108:111], a[248:251], a[204:207], v[108:111]\n" \
B_"v25, s[76:79], s84 offen offset:1024 lds\n" \
M_"v[104:107], a[180:183], a[140:143], 0\n" \
X_"a[170:171], v34 offset:33280\n" \
M_"v[104:107], a[244:247], a[204:207], v[104:107]\n" \
X_"a[232:233], v34 offset:36864\n" \
M_"v[100:103], a[176:179], a[140:143], 0\n" \
M_"v[100:103], a[240:243], a[204:207], v[100:103]\n" \
R_"a[140:143], v28 offset:38912\n" \
M_"v[52:55], a[176:179], a[128:131], 0\n" \
R_"a[204:207], v29 offset:38912\n" \
M_"v[52:55], a[240:243], a[192:195], v[52:55]\n" \
"s_movk_i32 s84, 0x4180\n" \
M_"v[56:59], a[180:183], a[128:131], 0\n" \
B_"v24, s[76:79], s84 offen offset:2048 lds\n" \
M_"v[56:59], a[244:247], a[192:195], v[56:59]\n" \
"s_movk_i32 s84, 0x6180\n" \
M_"v[60:63], a[184:187], a[128:131], 0\n" \
B_"v25, s[76:79], s84 offen offset:3072 lds\n" \
M_"v[60:63], a[248:251], a[192:195], v[60:63]\n" \
X_"a[234:235], v34 offset:37376\n" \
M_"v[64:67], a[188:191], a[128:131], 0\n" \
M_"v[64:67], a[252:255], a[192:195], v[64:67]\n" \
R_"a[128:131], v28 offset:32768\n" \
M_"v[80:83], a[188:191], a[132:135], 0\n" \
R_"a[192:195], v29 offset:32768\n" \
M_"v[80:83], a[252:255], a[196:199], v[80:83]\n" \
M_"v[76:79], a[184:187], a[132:135], 0\n" \
M_"v[76:79], a[248:251], a[196:199], v[76:79]\n" \
M_"v[72:75], a[180:183], a[132:135], 0\n" \
M_"v[72:75], a[244:247], a[196:199], v[72:75]\n" \
M_"v[68:71], a[176:179], a[132:135], 0\n" \
M_"v[68:71], a[240:243], a[196:199], v[68:71]\n" \
R_"a[132:135], v28 offset:34816\n" \
R_"a[196:199], v29 offset:34816\n" \
"s_waitcnt lgkmcnt(15)\n" \
M_"a[0:3], a[160:163], a[144:147], a[0:3]\n" \
"s_add_u32 m0, s30, 0x1000\n" \
"s_addk_i32 s84, 0x3000\n" \
M_"a[0:3], a[224:227], a[208:211], a[0:3]\n" \
B_"v24, s[76:79], s84 offen offset:0 lds\n" \
M_"a[4:7], a[164:167], a[144:147], a[4:7]\n" \
X_"a[176:177], v36 offset:32768\n" \
"v_mov_b32_e64 v11, 0\n" \
M_"a[4:7], a[228:231], a[208:211], a[4:7]\n" \
X_"a[178:179], v36 offset:33792\n" \
"s_and_b32 s5, s2, 7\n" \
"s_waitcnt lgkmcnt(15)\n" \
M_"a[12:15], a[172:175], a[144:147], a[12:15]\n" \
X_"a[240:241], v36 offset:40960\n" \
"s_lshl_b32 s6, s3, 1\n" \
"s_waitcnt lgkmcnt(15)\n" \
M_"a[12:15], a[236:239], a[208:211], a[12:15]\n" \
"s_add_u32 s88, s41, 0x3000\n" \
"buffer_store_dwordx4 v[196:199], v20, s[80:83], s88 offen offset:2048 sc1\n" \
"s_waitcnt lgkmcnt(11)\n" \
M_"a[8:11], a[144:147], a[168:171], a[8:11]\n" \
X_"a[242:243], v36 offset:41984\n" \
"s_lshl_b32 s4, s5, 2\n" \
"s_waitcnt lgkmcnt(8)\n" \
M_"a[8:11], a[208:211], a[232:235], a[8:11]\n" \
X_"a[180:181], v37 offset:32768\n" \
"s_bfe_u32 s7, s2, 0x20003\n" \
M_"a[24:27], a[148:151], a[168:171], a[24:27]\n" \
X_"a[182:183], v37 offset:33792\n" \
"s_or_b32 s12, s4, s7\n" \
M_"a[24:27], a[212:215], a[232:235], a[24:27]\n" \
X_"a[244:245], v37 offset:40960\n" \
"s_and_b32 s6, s6, 4\n" \
M_"a[28:31], a[172:175], a[148:151], a[28:31]\n" \
"s_and_b32 s8, s3, 1\n" \
"s_or_b32 s8, s6, s8\n" \
M_"a[28:31], a[236:239], a[212:215], a[28:31]\n" \
"s_addk_i32 s84, 0x2000\n" \
M_"a[20:23], a[164:167], a[148:151], a[20:23]\n" \
B_"v25, s[76:79], s84 offen offset:1024 lds\n" \
M_"a[20:23], a[228:231], a[212:215], a[20:23]\n" \
X_"a[246:247], v37 offset:41984\n" \
"s_lshl_b32 s6, s12, 4\n" \
M_"a[16:19], a[160:163], a[148:151], a[16:19]\n" \
X_"a[184:185], v38 offset:32768\n" \
"s_lshl_b32 s9, s3, 2\n" \
M_"a[16:19], a[224:227], a[212:215], a[16:19]\n" \
X_"a[186:187], v38 offset:33792\n" \
"s_or_b32 s6, s6, s9\n" \
M_"a[32:35], a[160:163], a[152:155], a[32:35]\n" \
"s_addk_i32 s84, 0x2000\n" \
M_"a[32:35], a[224:227], a[216:219], a[32:35]\n" \
B_"v24, s[76:79], s84 offen offset:2048 lds\n" \
M_"a[36:39], a[164:167], a[152:155], a[36:39]\n" \
X_"a[248:249], v38 offset:40960\n" \
"s_lshr_b32 s9, s2, 5\n" \
M_"a[36:39], a[228:231], a[216:219], a[36:39]\n" \
X_"a[250:251], v38 offset:41984\n" \
"s_mul_i32 s10, s9, 0xa00\n" \
M_"a[44:47], a[172:175], a[152:155], a[44:47]\n" \
X_"a[188:189], v39 offset:32768\n" \
"s_add_i32 s10, s10, 0x5bc1000\n" \
M_"a[44:47], a[236:239], a[216:219], a[44:47]\n" \
X_"a[190:191], v39 offset:33792\n" \
"s_or_b32 s6, s6, s10\n" \
M_"a[40:43], a[152:155], a[168:171], a[40:43]\n" \
"s_add_u32 s89, s41, 0x3000\n" \
"buffer_store_dwordx4 a[84:87], v20, s[80:83], s89 offen offset:1024 sc1\n" \
M_"a[40:43], a[216:219], a[232:235], a[40:43]\n" \
X_"a[252:253], v39 offset:40960\n" \
"s_lshl_b32 s13, s9, 3\n" \
M_"a[56:59], a[156:159], a[168:171], a[56:59]\n" \
X_"a[254:255], v39 offset:41984\n" \
"v_writelane_b32 v11, s6, 0\n" \
M_"a[56:59], a[220:223], a[232:235], a[56:59]\n" \
"s_or_b32 s6, s13, s5\n" \
"v_writelane_b32 v11, s10, 1\n" \
M_"a[60:63], a[172:175], a[156:159], a[60:63]\n" \
"s_lshl_b32 s6, s6, 10\n" \
"s_lshl_b32 s10, s3, 8\n" \
M_"a[60:63], a[236:239], a[220:223], a[60:63]\n" \
"s_addk_i32 s84, 0x2000\n" \
M_"a[52:55], a[164:167], a[156:159], a[52:55]\n" \
B_"v25, s[76:79], s84 offen offset:3072 lds\n" \
M_"a[52:55], a[228:231], a[220:223], a[52:55]\n" \
"s_or_b32 s6, s6, s10\n" \
"s_or_b32 s10, s6, 0x5c00200\n" \
M_"a[48:51], a[160:163], a[156:159], a[48:51]\n" \
"v_writelane_b32 v11, s10, 2\n" \
"s_lshl_b32 s10, s7, 2\n" \
M_"a[48:51], a[224:227], a[220:223], a[48:51]\n" \
"s_or_b32 s11, s10, s6\n" \
"s_or_b32 s11, s11, 0x5c00000\n" \
M_"a[64:67], a[164:167], a[140:143], a[64:67]\n" \
"v_writelane_b32 v11, s11, 3\n" \
"s_or_b32 s6, s6, 0x5c00080\n" \
M_"a[64:67], a[228:231], a[204:207], a[64:67]\n" \
"v_writelane_b32 v11, s6, 4\n" \
"s_or_b32 s6, s6, s10\n" \
M_"a[80:83], a[172:175], a[140:143], a[80:83]\n" \
"s_add_i32 s10, s3, s2\n" \
"v_writelane_b32 v11, s6, 5\n" \
M_"a[80:83], a[236:239], a[204:207], a[80:83]\n" \
"s_mov_b32 s6, 2.0\n" \
"s_mov_b32 s11, 0x5bc0000\n" \
M_"a[68:71], a[140:143], a[168:171], a[68:71]\n" \
"s_cmp_lg_u32 s10, 0\n" \
"s_cselect_b32 s10, s6, s11\n" \
"s_waitcnt vmcnt(11) lgkmcnt(0)\n" \
M_"a[68:71], a[204:207], a[232:235], a[68:71]\n" \
"s_barrier\n" \
"v_xor_b32_e32 v28, 0x20000, v28\n" \
"v_xor_b32_e32 v29, 0x20000, v29\n" \
M_"v[116:119], a[176:179], a[144:147], v[116:119]\n" \
"s_add_u32 m0, s40, 0x8000\n" \
"s_add_u32 s85, s39, 0x6000\n" \
M_"v[116:119], a[240:243], a[208:211], v[116:119]\n" \
B_"v20, s[80:83], s85 offen sc1 lds\n" \
M_"v[120:123], a[180:183], a[144:147], v[120:123]\n" \
X_"a[160:161], v32 offset:0\n" \
"s_lshr_b32 s14, s2, 3\n" \
M_"v[120:123], a[244:247], a[208:211], v[120:123]\n" \
X_"a[162:163], v32 offset:512\n" \
"v_writelane_b32 v11, s10, 57\n" \
M_"v[124:127], a[184:187], a[144:147], v[124:127]\n" \
"s_add_u32 m0, s40, 0x9000\n" \
"s_add_u32 s85, s39, 0x7000\n" \
M_"v[124:127], a[248:251], a[208:211], v[124:127]\n" \
B_"v20, s[80:83], s85 offen sc1 lds\n" \
M_"v[128:131], a[188:191], a[144:147], v[128:131]\n" \
X_"a[224:225], v32 offset:4096\n" \
"s_lshl_b32 s10, s5, 5\n" \
M_"v[128:131], a[252:255], a[208:211], v[128:131]\n" \
R_"a[144:147], v28 offset:8192\n" \
"s_min_u32 s15, s3, 2\n" \
M_"v[144:147], a[188:191], a[148:151], v[144:147]\n" \
R_"a[208:211], v29 offset:8192\n" \
"s_mul_i32 s11, s7, 0x120\n" \
M_"v[144:147], a[252:255], a[212:215], v[144:147]\n" \
"s_add_u32 m0, s40, 0xb000\n" \
"s_movk_i32 s84, 0x180\n" \
M_"v[140:143], a[184:187], a[148:151], v[140:143]\n" \
B_"v21, s[76:79], s84 offen offset:0 lds\n" \
M_"v[140:143], a[248:251], a[212:215], v[140:143]\n" \
X_"a[226:227], v32 offset:4608\n" \
M_"v[136:139], a[180:183], a[148:151], v[136:139]\n" \
"s_add_u32 s88, s41, 0x3000\n" \
"buffer_store_dwordx4 v[252:255], v20, s[80:83], s88 offen offset:0 sc1\n" \
M_"v[136:139], a[244:247], a[212:215], v[136:139]\n" \
X_"a[164:165], v33 offset:0\n" \
M_"v[132:135], a[176:179], a[148:151], v[132:135]\n" \
"s_add_u32 s16, s22, s11\n" \
"s_addc_u32 s17, s23, 0\n" \
"s_nop 0\n" \
M_"v[132:135], a[240:243], a[212:215], v[132:135]\n" \
R_"a[148:151], v28 offset:10240\n" \
M_"v[148:151], a[176:179], a[152:155], v[148:151]\n" \
R_"a[212:215], v29 offset:10240\n" \
M_"v[148:151], a[240:243], a[216:219], v[148:151]\n" \
"s_add_u32 m0, s31, 0x8000\n" \
"s_mov_b32 s84, 0x180000\n" \
M_"v[152:155], a[180:183], a[152:155], v[152:155]\n" \
B_"v26, s[68:71], s84 offen offset:0 lds\n" \
M_"v[152:155], a[244:247], a[216:219], v[152:155]\n" \
X_"a[166:167], v33 offset:512\n" \
M_"v[156:159], a[184:187], a[152:155], v[156:159]\n" \
X_"a[228:229], v33 offset:4096\n" \
M_"v[156:159], a[248:251], a[216:219], v[156:159]\n" \
"s_addk_i32 s84, 0x7c00\n" \
M_"v[160:163], a[188:191], a[152:155], v[160:163]\n" \
B_"v26, s[68:71], s84 offen offset:1024 lds\n" \
M_"v[160:163], a[252:255], a[216:219], v[160:163]\n" \
R_"a[152:155], v28 offset:12288\n" \
M_"v[176:179], a[188:191], a[156:159], v[176:179]\n" \
R_"a[216:219], v29 offset:12288\n" \
M_"v[176:179], a[252:255], a[220:223], v[176:179]\n" \
"s_addk_i32 s84, 0x7c00\n" \
M_"v[172:175], a[184:187], a[156:159], v[172:175]\n" \
B_"v27, s[68:71], s84 offen offset:2048 lds\n" \
M_"v[172:175], a[248:251], a[220:223], v[172:175]\n" \
X_"a[230:231], v33 offset:4608\n" \
M_"v[168:171], a[180:183], a[156:159], v[168:171]\n" \
X_"a[172:173], v35 offset:0\n" \
M_"v[168:171], a[244:247], a[220:223], v[168:171]\n" \
X_"a[174:175], v35 offset:512\n" \
M_"v[164:167], a[176:179], a[156:159], v[164:167]\n" \
"s_add_u32 s18, s24, s11\n" \
"v_writelane_b32 v11, s16, 9\n" \
"s_addc_u32 s19, s25, 0\n" \
M_"v[164:167], a[240:243], a[220:223], v[164:167]\n" \
R_"a[156:159], v28 offset:14336\n" \
"s_and_b32 s20, s17, 0xffff\n" \
M_"v[84:87], a[176:179], a[136:139], v[84:87]\n" \
R_"a[220:223], v29 offset:14336\n" \
"v_writelane_b32 v11, s20, 10\n" \
M_"v[84:87], a[240:243], a[200:203], v[84:87]\n" \
"s_addk_i32 s84, 0x7c00\n" \
M_"v[88:91], a[180:183], a[136:139], v[88:91]\n" \
B_"v27, s[68:71], s84 offen offset:3072 lds\n" \
M_"v[88:91], a[244:247], a[200:203], v[88:91]\n" \
"s_add_u32 m0, s30, 0x8000\n" \
"s_movk_i32 s84, 0x200\n" \
M_"v[92:95], a[184:187], a[136:139], v[92:95]\n" \
B_"v24, s[76:79], s84 offen offset:0 lds\n" \
M_"v[92:95], a[248:251], a[200:203], v[92:95]\n" \
X_"a[236:237], v35 offset:4096\n" \
"s_and_b32 s20, s19, 0xffff\n" \
M_"v[96:99], a[188:191], a[136:139], v[96:99]\n" \
X_"a[238:239], v35 offset:4608\n" \
"v_writelane_b32 v11, s18, 11\n" \
M_"v[96:99], a[252:255], a[200:203], v[96:99]\n" \
R_"a[136:139], v28 offset:4096\n" \
M_"v[112:115], a[188:191], a[140:143], v[112:115]\n" \
R_"a[200:203], v29 offset:4096\n" \
M_"v[112:115], a[252:255], a[204:207], v[112:115]\n" \
"s_movk_i32 s84, 0x2200\n" \
"s_nop 0\n" \
M_"v[108:111], a[184:187], a[140:143], v[108:111]\n" \
B_"v25, s[76:79], s84 offen offset:1024 lds\n" \
M_"v[108:111], a[248:251], a[204:207], v[108:111]\n" \
"s_add_u32 s89, s41, 0x1000\n" \
"buffer_store_dwordx4 v[200:203], v20, s[80:83], s89 offen offset:3072 sc1\n" \
M_"v[104:107], a[180:183], a[140:143], v[104:107]\n" \
X_"a[168:169], v34 offset:0\n" \
M_"v[104:107], a[244:247], a[204:207], v[104:107]\n" \
X_"a[170:171], v34 offset:512\n" \
M_"v[100:103], a[176:179], a[140:143], v[100:103]\n" \
X_"a[232:233], v34 offset:4096\n" \
M_"v[100:103], a[240:243], a[204:207], v[100:103]\n" \
R_"a[140:143], v28 offset:6144\n" \
M_"v[52:55], a[176:179], a[128:131], v[52:55]\n" \
R_"a[204:207], v29 offset:6144\n" \
M_"v[52:55], a[240:243], a[192:195], v[52:55]\n" \
"s_movk_i32 s84, 0x4200\n" \
M_"v[56:59], a[180:183], a[128:131], v[56:59]\n" \
B_"v24, s[76:79], s84 offen offset:2048 lds\n" \
M_"v[56:59], a[244:247], a[192:195], v[56:59]\n" \
"s_movk_i32 s84, 0x6200\n" \
M_"v[60:63], a[184:187], a[128:131], v[60:63]\n" \
B_"v25, s[76:79], s84 offen offset:3072 lds\n" \
M_"v[60:63], a[248:251], a[192:195], v[60:63]\n" \
X_"a[234:235], v34 offset:4608\n" \
M_"v[64:67], a[188:191], a[128:131], v[64:67]\n" \
"s_cmpk_lt_u32 s3, 3\n" \
"s_cselect_b32 s16, s16, s18\n" \
M_"v[64:67], a[252:255], a[192:195], v[64:67]\n" \
R_"a[128:131], v28 offset:0\n" \
"s_movk_i32 s18, 0xfff\n" \
M_"v[80:83], a[188:191], a[132:135], v[80:83]\n" \
R_"a[192:195], v29 offset:0\n" \
"v_writelane_b32 v11, s20, 12\n" \
M_"v[80:83], a[252:255], a[196:199], v[80:83]\n" \
"s_and_b32 s20, s29, 0xffff\n" \
"v_writelane_b32 v11, s28, 13\n" \
M_"v[76:79], a[184:187], a[132:135], v[76:79]\n" \
"v_writelane_b32 v11, s20, 14\n" \
"v_writelane_b32 v11, s16, 15\n" \
M_"v[76:79], a[248:251], a[196:199], v[76:79]\n" \
"s_cmpk_lt_u32 s3, 3\n" \
"s_cselect_b32 s16, s17, s19\n" \
M_"v[72:75], a[180:183], a[132:135], v[72:75]\n" \
"s_and_b32 s16, s16, 0xffff\n" \
"v_writelane_b32 v11, s16, 16\n" \
M_"v[72:75], a[244:247], a[196:199], v[72:75]\n" \
"s_mul_i32 s16, s5, 0x48\n" \
"s_add_i32 s17, s16, 0x47\n" \
M_"v[68:71], a[176:179], a[132:135], v[68:71]\n" \
"s_cmpk_lt_u32 s3, 3\n" \
"s_cselect_b32 s17, s17, s18\n" \
M_"v[68:71], a[240:243], a[196:199], v[68:71]\n" \
R_"a[132:135], v28 offset:2048\n" \
R_"a[196:199], v29 offset:2048\n" \
"s_waitcnt lgkmcnt(15)\n" \
M_"a[0:3], a[160:163], a[144:147], a[0:3]\n" \
"s_add_u32 m0, s30, 0x9000\n" \
"s_addk_i32 s84, 0x3000\n" \
M_"a[0:3], a[224:227], a[208:211], a[0:3]\n" \
B_"v24, s[76:79], s84 offen offset:0 lds\n" \
M_"a[4:7], a[164:167], a[144:147], a[4:7]\n" \
X_"a[176:177], v36 offset:0\n" \
"s_mulk_i32 s17, 0x480\n" \
M_"a[4:7], a[228:231], a[208:211], a[4:7]\n" \
X_"a[178:179], v36 offset:1024\n" \
"s_addk_i32 s17, 0x120\n" \
M_"a[12:15], a[172:175], a[144:147], a[12:15]\n" \
X_"a[240:241], v36 offset:8192\n" \
"v_writelane_b32 v11, s17, 17\n" \
"s_waitcnt lgkmcnt(15)\n" \
M_"a[12:15], a[236:239], a[208:211], a[12:15]\n" \
"s_add_u32 s88, s41, 0x1000\n" \
"buffer_store_dwordx4 v[204:207], v20, s[80:83], s88 offen offset:2048 sc1\n" \
"s_waitcnt lgkmcnt(11)\n" \
M_"a[8:11], a[144:147], a[168:171], a[8:11]\n" \
X_"a[242:243], v36 offset:9216\n" \
"s_lshl_b32 s17, s3, 5\n" \
"s_waitcnt lgkmcnt(8)\n" \
M_"a[8:11], a[208:211], a[232:235], a[8:11]\n" \
X_"a[180:181], v37 offset:0\n" \
"s_add_i32 s16, s16, s17\n" \
M_"a[24:27], a[148:151], a[168:171], a[24:27]\n" \
X_"a[182:183], v37 offset:1024\n" \
"s_mulk_i32 s16, 0x480\n" \
M_"a[24:27], a[212:215], a[232:235], a[24:27]\n" \
X_"a[244:245], v37 offset:8192\n" \
"s_mul_i32 s17, s12, 0x24000\n" \
M_"a[28:31], a[172:175], a[148:151], a[28:31]\n" \
"s_add_i32 s18, s17, 0x1b000\n" \
M_"a[28:31], a[236:239], a[212:215], a[28:31]\n" \
"s_addk_i32 s84, 0x2000\n" \
M_"a[20:23], a[164:167], a[148:151], a[20:23]\n" \
B_"v25, s[76:79], s84 offen offset:1024 lds\n" \
".fill (L_pb + 65248 - .) / 4, 4, 0xBF800000\n" \
"L_pp16:\n" \
"s_cbranch_execz L_pret\n" \
M_"a[20:23], a[228:231], a[212:215], a[20:23]\n" \
X_"a[246:247], v37 offset:9216\n" \
M_"a[16:19], a[160:163], a[148:151], a[16:19]\n" \
X_"a[184:185], v38 offset:0\n" \
M_"a[16:19], a[224:227], a[212:215], a[16:19]\n" \
X_"a[186:187], v38 offset:1024\n" \
M_"a[32:35], a[160:163], a[152:155], a[32:35]\n" \
"s_addk_i32 s84, 0x2000\n" \
M_"a[32:35], a[224:227], a[216:219], a[32:35]\n" \
B_"v24, s[76:79], s84 offen offset:2048 lds\n" \
M_"a[36:39], a[164:167], a[152:155], a[36:39]\n" \
X_"a[248:249], v38 offset:8192\n" \
M_"a[36:39], a[228:231], a[216:219], a[36:39]\n" \
X_"a[250:251], v38 offset:9216\n" \
M_"a[44:47], a[172:175], a[152:155], a[44:47]\n" \
X_"a[188:189], v39 offset:0\n" \
M_"a[44:47], a[236:239], a[216:219], a[44:47]\n" \
X_"a[190:191], v39 offset:1024\n" \
M_"a[40:43], a[152:155], a[168:171], a[40:43]\n" \
"s_add_u32 s89, s41, 0x1000\n" \
"buffer_store_dwordx4 v[208:211], v20, s[80:83], s89 offen offset:1024 sc1\n" \
M_"a[40:43], a[216:219], a[232:235], a[40:43]\n" \
X_"a[252:253], v39 offset:8192\n" \
M_"a[56:59], a[156:159], a[168:171], a[56:59]\n" \
X_"a[254:255], v39 offset:9216\n" \
M_"a[56:59], a[220:223], a[232:235], a[56:59]\n" \
"s_cmpk_lt_u32 s3, 3\n" \
"s_cselect_b32 s16, s16, s18\n" \
M_"a[60:63], a[172:175], a[156:159], a[60:63]\n" \
"s_xor_b32 s18, s3, 2\n" \
"v_writelane_b32 v11, s16, 18\n" \
M_"a[60:63], a[236:239], a[220:223], a[60:63]\n" \
"s_addk_i32 s84, 0x2000\n" \
M_"a[52:55], a[164:167], a[156:159], a[52:55]\n" \
B_"v25, s[76:79], s84 offen offset:3072 lds\n" \
M_"a[52:55], a[228:231], a[220:223], a[52:55]\n" \
"s_mul_i32 s16, s15, 0x2400\n" \
"s_mul_i32 s15, s15, 0x9000\n" \
M_"a[48:51], a[160:163], a[156:159], a[48:51]\n" \
"s_bitset1_b32 s16, 17\n" \
"v_writelane_b32 v11, s16, 22\n" \
M_"a[48:51], a[224:227], a[220:223], a[48:51]\n" \
"s_add_i32 s17, s17, s15\n" \
"s_mul_i32 s15, s3, 0x2400\n" \
M_"a[64:67], a[164:167], a[140:143], a[64:67]\n" \
"v_writelane_b32 v11, s17, 29\n" \
"s_add_i32 s16, s15, 0xa400\n" \
M_"a[64:67], a[228:231], a[204:207], a[64:67]\n" \
"s_mov_b32 s17, 0x8000\n" \
M_"a[80:83], a[172:175], a[140:143], a[80:83]\n" \
"s_cmpk_lt_u32 s3, 3\n" \
"s_cselect_b32 s16, s16, s17\n" \
M_"a[80:83], a[236:239], a[204:207], a[80:83]\n" \
"s_mov_b32 s17, 0x1b000\n" \
M_"a[68:71], a[140:143], a[168:171], a[68:71]\n" \
"s_cmp_lg_u32 s18, 0\n" \
"s_cselect_b32 s17, s16, s17\n" \
"s_waitcnt vmcnt(11) lgkmcnt(0)\n" \
M_"a[68:71], a[204:207], a[232:235], a[68:71]\n" \
"s_barrier\n" \
"v_xor_b32_e32 v28, 0x20000, v28\n" \
"v_xor_b32_e32 v29, 0x20000, v29\n" \
M_"v[116:119], a[176:179], a[144:147], v[116:119]\n" \
"s_add_u32 m0, s40, 0x0\n" \
"s_nop 0\n" \
"s_add_u32 s85, s39, 0x8000\n" \
M_"v[116:119], a[240:243], a[208:211], v[116:119]\n" \
B_"v20, s[80:83], s85 offen sc1 lds\n" \
M_"v[120:123], a[180:183], a[144:147], v[120:123]\n" \
X_"a[160:161], v32 offset:32768\n" \
"v_writelane_b32 v11, s16, 23\n" \
M_"v[120:123], a[244:247], a[208:211], v[120:123]\n" \
X_"a[162:163], v32 offset:33280\n" \
"s_or_b32 s16, s15, 0x10000\n" \
M_"v[124:127], a[184:187], a[144:147], v[124:127]\n" \
"s_add_u32 m0, s40, 0x1000\n" \
"s_add_u32 s85, s39, 0x9000\n" \
M_"v[124:127], a[248:251], a[208:211], v[124:127]\n" \
B_"v20, s[80:83], s85 offen sc1 lds\n" \
M_"v[128:131], a[188:191], a[144:147], v[128:131]\n" \
X_"a[224:225], v32 offset:36864\n" \
"v_writelane_b32 v11, s17, 24\n" \
M_"v[128:131], a[252:255], a[208:211], v[128:131]\n" \
R_"a[144:147], v28 offset:8192\n" \
"s_add_i32 s17, s15, 0x19000\n" \
M_"v[144:147], a[188:191], a[148:151], v[144:147]\n" \
R_"a[208:211], v29 offset:8192\n" \
"v_writelane_b32 v11, s16, 25\n" \
M_"v[144:147], a[252:255], a[212:215], v[144:147]\n" \
"s_add_u32 m0, s40, 0x3000\n" \
"s_movk_i32 s84, 0x200\n" \
M_"v[140:143], a[184:187], a[148:151], v[140:143]\n" \
B_"v21, s[76:79], s84 offen offset:0 lds\n" \
M_"v[140:143], a[248:251], a[212:215], v[140:143]\n" \
X_"a[226:227], v32 offset:37376\n" \
"s_mov_b32 s16, 0\n" \
M_"v[136:139], a[180:183], a[148:151], v[136:139]\n" \
"s_add_u32 s88, s41, 0x1000\n" \
"buffer_store_dwordx4 v[212:215], v20, s[80:83], s88 offen offset:0 sc1\n" \
M_"v[136:139], a[244:247], a[212:215], v[136:139]\n" \
X_"a[164:165], v33 offset:32768\n" \
M_"v[132:135], a[176:179], a[148:151], v[132:135]\n" \
"s_cmpk_lt_u32 s3, 3\n" \
"s_cselect_b32 s17, s17, s16\n" \
M_"v[132:135], a[240:243], a[212:215], v[132:135]\n" \
R_"a[148:151], v28 offset:10240\n" \
"s_bitset1_b32 s15, 17\n" \
M_"v[148:151], a[176:179], a[152:155], v[148:151]\n" \
R_"a[212:215], v29 offset:10240\n" \
"v_writelane_b32 v11, s17, 26\n" \
M_"v[148:151], a[240:243], a[216:219], v[148:151]\n" \
"s_add_u32 m0, s31, 0x0\n" \
"s_mov_b32 s84, 0x200000\n" \
M_"v[152:155], a[180:183], a[152:155], v[152:155]\n" \
B_"v26, s[68:71], s84 offen offset:0 lds\n" \
M_"v[152:155], a[244:247], a[216:219], v[152:155]\n" \
X_"a[166:167], v33 offset:33280\n" \
"s_movk_i32 s17, 0x2400\n" \
M_"v[156:159], a[184:187], a[152:155], v[156:159]\n" \
X_"a[228:229], v33 offset:36864\n" \
M_"v[156:159], a[248:251], a[216:219], v[156:159]\n" \
"s_addk_i32 s84, 0x7c00\n" \
M_"v[160:163], a[188:191], a[152:155], v[160:163]\n" \
B_"v26, s[68:71], s84 offen offset:1024 lds\n" \
M_"v[160:163], a[252:255], a[216:219], v[160:163]\n" \
R_"a[152:155], v28 offset:12288\n" \
M_"v[176:179], a[188:191], a[156:159], v[176:179]\n" \
R_"a[216:219], v29 offset:12288\n" \
M_"v[176:179], a[252:255], a[220:223], v[176:179]\n" \
"s_addk_i32 s84, 0x7c00\n" \
M_"v[172:175], a[184:187], a[156:159], v[172:175]\n" \
B_"v27, s[68:71], s84 offen offset:2048 lds\n" \
M_"v[172:175], a[248:251], a[220:223], v[172:175]\n" \
X_"a[230:231], v33 offset:37376\n" \
M_"v[168:171], a[180:183], a[156:159], v[168:171]\n" \
X_"a[172:173], v35 offset:32768\n" \
M_"v[168:171], a[244:247], a[220:223], v[168:171]\n" \
X_"a[174:175], v35 offset:33280\n" \
M_"v[164:167], a[176:179], a[156:159], v[164:167]\n" \
"s_cmpk_lt_u32 s3, 3\n" \
"s_cselect_b32 s15, s15, s17\n" \
M_"v[164:167], a[240:243], a[220:223], v[164:167]\n" \
R_"a[156:159], v28 offset:14336\n" \
"v_writelane_b32 v11, s15, 27\n" \
M_"v[84:87], a[176:179], a[136:139], v[84:87]\n" \
R_"a[220:223], v29 offset:14336\n" \
"s_lshl_b32 s15, s3, 10\n" \
M_"v[84:87], a[240:243], a[200:203], v[84:87]\n" \
"s_addk_i32 s84, 0x7c00\n" \
M_"v[88:91], a[180:183], a[136:139], v[88:91]\n" \
B_"v27, s[68:71], s84 offen offset:3072 lds\n" \
M_"v[88:91], a[244:247], a[200:203], v[88:91]\n" \
"s_add_u32 m0, s30, 0x20000\n" \
"s_movk_i32 s84, 0x280\n" \
M_"v[92:95], a[184:187], a[136:139], v[92:95]\n" \
B_"v24, s[76:79], s84 offen offset:0 lds\n" \
M_"v[92:95], a[248:251], a[200:203], v[92:95]\n" \
X_"a[236:237], v35 offset:36864\n" \
"s_addk_i32 s15, 0x4800\n" \
M_"v[96:99], a[188:191], a[136:139], v[96:99]\n" \
X_"a[238:239], v35 offset:37376\n" \
"v_writelane_b32 v11, s15, 28\n" \
M_"v[96:99], a[252:255], a[200:203], v[96:99]\n" \
R_"a[136:139], v28 offset:4096\n" \
"s_add_i32 s15, s14, 1\n" \
M_"v[112:115], a[188:191], a[140:143], v[112:115]\n" \
R_"a[200:203], v29 offset:4096\n" \
"s_and_b32 s15, s15, 3\n" \
M_"v[112:115], a[252:255], a[204:207], v[112:115]\n" \
"s_movk_i32 s84, 0x2280\n" \
M_"v[108:111], a[184:187], a[140:143], v[108:111]\n" \
B_"v25, s[76:79], s84 offen offset:1024 lds\n" \
M_"v[108:111], a[248:251], a[204:207], v[108:111]\n" \
"s_add_u32 s89, s42, 0x0\n" \
"buffer_store_dwordx4 v[216:219], v20, s[80:83], s89 offen offset:1024 sc1\n" \
M_"v[104:107], a[180:183], a[140:143], v[104:107]\n" \
X_"a[168:169], v34 offset:32768\n" \
"s_or_b32 s15, s15, s4\n" \
M_"v[104:107], a[244:247], a[204:207], v[104:107]\n" \
X_"a[170:171], v34 offset:33280\n" \
"s_lshl_b32 s17, s15, 2\n" \
M_"v[100:103], a[176:179], a[140:143], v[100:103]\n" \
X_"a[232:233], v34 offset:36864\n" \
"s_or_b32 s17, s17, s3\n" \
M_"v[100:103], a[240:243], a[204:207], v[100:103]\n" \
R_"a[140:143], v28 offset:6144\n" \
"s_mul_i32 s17, s17, 0x9000\n" \
M_"v[52:55], a[176:179], a[128:131], v[52:55]\n" \
R_"a[204:207], v29 offset:6144\n" \
"v_writelane_b32 v11, s17, 30\n" \
M_"v[52:55], a[240:243], a[192:195], v[52:55]\n" \
"s_movk_i32 s84, 0x4280\n" \
M_"v[56:59], a[180:183], a[128:131], v[56:59]\n" \
B_"v24, s[76:79], s84 offen offset:2048 lds\n" \
M_"v[56:59], a[244:247], a[192:195], v[56:59]\n" \
"s_movk_i32 s84, 0x6280\n" \
M_"v[60:63], a[184:187], a[128:131], v[60:63]\n" \
B_"v25, s[76:79], s84 offen offset:3072 lds\n" \
M_"v[60:63], a[248:251], a[192:195], v[60:63]\n" \
X_"a[234:235], v34 offset:37376\n" \
"s_lshl_b32 s17, s12, 2\n" \
M_"v[64:67], a[188:191], a[128:131], v[64:67]\n" \
"s_or_b32 s17, s17, s3\n" \
"s_add_i32 s14, s14, -1\n" \
M_"v[64:67], a[252:255], a[192:195], v[64:67]\n" \
R_"a[128:131], v28 offset:0\n" \
"s_xor_b32 s17, s17, 8\n" \
M_"v[80:83], a[188:191], a[132:135], v[80:83]\n" \
R_"a[192:195], v29 offset:0\n" \
"s_and_b32 s14, s14, 3\n" \
M_"v[80:83], a[252:255], a[196:199], v[80:83]\n" \
"s_mul_i32 s17, s17, 0x9000\n" \
"s_or_b32 s14, s14, s4\n" \
M_"v[76:79], a[184:187], a[132:135], v[76:79]\n" \
"v_writelane_b32 v11, s17, 31\n" \
"s_lshl_b32 s17, s14, 2\n" \
M_"v[76:79], a[248:251], a[196:199], v[76:79]\n" \
"s_or_b32 s17, s17, s3\n" \
"s_mul_i32 s17, s17, 0x9000\n" \
M_"v[72:75], a[180:183], a[132:135], v[72:75]\n" \
"v_writelane_b32 v11, s17, 32\n" \
"s_or_b32 s17, s8, s13\n" \
M_"v[72:75], a[244:247], a[196:199], v[72:75]\n" \
"s_mul_i32 s17, s17, 0x24800\n" \
"s_lshl_b32 s15, s15, 12\n" \
M_"v[68:71], a[176:179], a[132:135], v[68:71]\n" \
"s_add_i32 s18, s17, 0x3004800\n" \
"s_lshl_b32 s12, s12, 12\n" \
M_"v[68:71], a[240:243], a[196:199], v[68:71]\n" \
R_"a[132:135], v28 offset:2048\n" \
R_"a[196:199], v29 offset:2048\n" \
"s_waitcnt lgkmcnt(15)\n" \
M_"a[0:3], a[160:163], a[144:147], a[0:3]\n" \
"s_add_u32 m0, s30, 0x21000\n" \
"s_addk_i32 s84, 0x3000\n" \
M_"a[0:3], a[224:227], a[208:211], a[0:3]\n" \
B_"v24, s[76:79], s84 offen offset:0 lds\n" \
M_"a[4:7], a[164:167], a[144:147], a[4:7]\n" \
X_"a[176:177], v36 offset:32768\n" \
"s_add_i32 s19, s18, s15\n" \
M_"a[4:7], a[228:231], a[208:211], a[4:7]\n" \
X_"a[178:179], v36 offset:33792\n" \
"v_writelane_b32 v11, s19, 33\n" \
M_"a[12:15], a[172:175], a[144:147], a[12:15]\n" \
X_"a[240:241], v36 offset:40960\n" \
"s_xor_b32 s12, s12, 0x2000\n" \
"s_waitcnt lgkmcnt(15)\n" \
M_"a[12:15], a[236:239], a[208:211], a[12:15]\n" \
"s_add_u32 s88, s42, 0x0\n" \
"buffer_store_dwordx4 v[220:223], v20, s[80:83], s88 offen offset:0 sc1\n" \
"s_waitcnt lgkmcnt(11)\n" \
M_"a[8:11], a[144:147], a[168:171], a[8:11]\n" \
X_"a[242:243], v36 offset:41984\n" \
"s_lshl_b32 s14, s14, 12\n" \
"s_waitcnt lgkmcnt(8)\n" \
M_"a[8:11], a[208:211], a[232:235], a[8:11]\n" \
X_"a[180:181], v37 offset:32768\n" \
"s_sub_i32 s13, s8, s13\n" \
M_"a[24:27], a[148:151], a[168:171], a[24:27]\n" \
X_"a[182:183], v37 offset:33792\n" \
"s_add_i32 s19, s18, s12\n" \
M_"a[24:27], a[212:215], a[232:235], a[24:27]\n" \
X_"a[244:245], v37 offset:40960\n" \
"v_writelane_b32 v11, s19, 34\n" \
M_"a[28:31], a[172:175], a[148:151], a[28:31]\n" \
"s_add_i32 s18, s18, s14\n" \
"s_mul_i32 s13, s13, 0x24800\n" \
M_"a[28:31], a[236:239], a[212:215], a[28:31]\n" \
"s_addk_i32 s84, 0x2000\n" \
M_"a[20:23], a[164:167], a[148:151], a[20:23]\n" \
B_"v25, s[76:79], s84 offen offset:1024 lds\n" \
M_"a[20:23], a[228:231], a[212:215], a[20:23]\n" \
X_"a[246:247], v37 offset:41984\n" \
"v_writelane_b32 v11, s18, 35\n" \
M_"a[16:19], a[160:163], a[148:151], a[16:19]\n" \
X_"a[184:185], v38 offset:32768\n" \
"s_add_i32 s18, s13, 0x4120800\n" \
M_"a[16:19], a[224:227], a[212:215], a[16:19]\n" \
X_"a[186:187], v38 offset:33792\n" \
"s_add_i32 s15, s18, s15\n" \
M_"a[32:35], a[160:163], a[152:155], a[32:35]\n" \
"s_addk_i32 s84, 0x2000\n" \
M_"a[32:35], a[224:227], a[216:219], a[32:35]\n" \
B_"v24, s[76:79], s84 offen offset:2048 lds\n" \
M_"a[36:39], a[164:167], a[152:155], a[36:39]\n" \
X_"a[248:249], v38 offset:40960\n" \
"v_writelane_b32 v11, s15, 36\n" \
M_"a[36:39], a[228:231], a[216:219], a[36:39]\n" \
X_"a[250:251], v38 offset:41984\n" \
"s_add_i32 s12, s18, s12\n" \
M_"a[44:47], a[172:175], a[152:155], a[44:47]\n" \
X_"a[188:189], v39 offset:32768\n" \
"v_writelane_b32 v11, s12, 37\n" \
M_"a[44:47], a[236:239], a[216:219], a[44:47]\n" \
X_"a[190:191], v39 offset:33792\n" \
"s_add_i32 s18, s18, s14\n" \
M_"a[40:43], a[152:155], a[168:171], a[40:43]\n" \
"s_add_u32 s89, s42, 0x1000\n" \
"buffer_store_dwordx4 v[224:227], v20, s[80:83], s89 offen offset:3072 sc1\n" \
M_"a[40:43], a[216:219], a[232:235], a[40:43]\n" \
X_"a[252:253], v39 offset:40960\n" \
"s_or_b32 s12, s17, 0x3000000\n" \
M_"a[56:59], a[156:159], a[168:171], a[56:59]\n" \
X_"a[254:255], v39 offset:41984\n" \
"s_lshl_b32 s14, s5, 11\n" \
M_"a[56:59], a[220:223], a[232:235], a[56:59]\n" \
"v_writelane_b32 v11, s18, 38\n" \
"s_add_i32 s15, s12, s14\n" \
M_"a[60:63], a[172:175], a[156:159], a[60:63]\n" \
"v_writelane_b32 v11, s15, 39\n" \
"s_add_i32 s15, s13, 0x411c000\n" \
M_"a[60:63], a[236:239], a[220:223], a[60:63]\n" \
"s_addk_i32 s84, 0x2000\n" \
M_"a[52:55], a[164:167], a[156:159], a[52:55]\n" \
B_"v25, s[76:79], s84 offen offset:3072 lds\n" \
M_"a[52:55], a[228:231], a[220:223], a[52:55]\n" \
"s_add_i32 s18, s15, s14\n" \
"v_writelane_b32 v11, s18, 40\n" \
M_"a[48:51], a[160:163], a[156:159], a[48:51]\n" \
"s_add_i32 s17, s17, 0x3004000\n" \
"v_writelane_b32 v11, s17, 41\n" \
M_"a[48:51], a[224:227], a[220:223], a[48:51]\n" \
"s_add_i32 s13, s13, 0x4120000\n" \
"v_writelane_b32 v11, s13, 42\n" \
M_"a[64:67], a[164:167], a[140:143], a[64:67]\n" \
"s_lshl_b32 s13, s9, 2\n" \
"s_or_b32 s7, s13, s7\n" \
M_"a[64:67], a[228:231], a[204:207], a[64:67]\n" \
"s_mul_i32 s7, s7, 0x90000\n" \
"s_lshl_b32 s13, s3, 14\n" \
M_"a[80:83], a[172:175], a[140:143], a[80:83]\n" \
"s_lshl_b32 s9, s9, 7\n" \
"s_lshl_b32 s8, s8, 4\n" \
M_"a[80:83], a[236:239], a[204:207], a[80:83]\n" \
"s_add_i32 s7, s7, s13\n" \
"s_or_b32 s13, s8, s9\n" \
M_"a[68:71], a[140:143], a[168:171], a[68:71]\n" \
"s_sub_i32 s8, s8, s9\n" \
"s_mulk_i32 s13, 0x480\n" \
"s_waitcnt vmcnt(11) lgkmcnt(0)\n" \
M_"a[68:71], a[204:207], a[232:235], a[68:71]\n" \
"s_barrier\n" \
M_"v[116:119], a[176:179], a[144:147], v[116:119]\n" \
"s_add_u32 m0, s40, 0x8000\n" \
"s_add_u32 s85, s39, 0xa000\n" \
M_"v[116:119], a[240:243], a[208:211], v[116:119]\n" \
B_"v20, s[80:83], s85 offen sc1 lds\n" \
M_"v[120:123], a[180:183], a[144:147], v[120:123]\n" \
X_"a[160:161], v32 offset:0\n" \
"v_accvgpr_read_b32 v212, a108\n" \
M_"v[120:123], a[244:247], a[208:211], v[120:123]\n" \
X_"a[162:163], v32 offset:512\n" \
"v_accvgpr_read_b32 v213, a109\n" \
M_"v[124:127], a[184:187], a[144:147], v[124:127]\n" \
"s_add_u32 m0, s40, 0x9000\n" \
"s_add_u32 s85, s39, 0xb000\n" \
M_"v[124:127], a[248:251], a[208:211], v[124:127]\n" \
B_"v20, s[80:83], s85 offen sc1 lds\n" \
M_"v[128:131], a[188:191], a[144:147], v[128:131]\n" \
X_"a[224:225], v32 offset:4096\n" \
"v_accvgpr_read_b32 v214, a110\n" \
M_"v[128:131], a[252:255], a[208:211], v[128:131]\n" \
R_"a[144:147], v28 offset:40960\n" \
"v_accvgpr_read_b32 v215, a111\n" \
M_"v[144:147], a[188:191], a[148:151], v[144:147]\n" \
R_"a[208:211], v29 offset:40960\n" \
"v_accvgpr_read_b32 v180, a124\n" \
M_"v[144:147], a[252:255], a[212:215], v[144:147]\n" \
"s_add_u32 m0, s40, 0xb000\n" \
"s_movk_i32 s84, 0x280\n" \
"s_nop 0\n" \
M_"v[140:143], a[184:187], a[148:151], v[140:143]\n" \
B_"v21, s[76:79], s84 offen offset:0 lds\n" \
M_"v[140:143], a[248:251], a[212:215], v[140:143]\n" \
X_"a[226:227], v32 offset:4608\n" \
"v_accvgpr_read_b32 v181, a125\n" \
M_"v[136:139], a[180:183], a[148:151], v[136:139]\n" \
"s_add_u32 s88, s42, 0x1000\n" \
"buffer_store_dwordx4 v[228:231], v20, s[80:83], s88 offen offset:2048 sc1\n" \
M_"v[136:139], a[244:247], a[212:215], v[136:139]\n" \
X_"a[164:165], v33 offset:0\n" \
"v_accvgpr_read_b32 v182, a126\n" \
M_"v[132:135], a[176:179], a[148:151], v[132:135]\n" \
"v_accvgpr_read_b32 v183, a127\n" \
C_"v212, v212, v213\n" \
M_"v[132:135], a[240:243], a[212:215], v[132:135]\n" \
R_"a[148:151], v28 offset:43008\n" \
C_"v213, v214, v215\n" \
M_"v[148:151], a[176:179], a[152:155], v[148:151]\n" \
R_"a[212:215], v29 offset:43008\n" \
C_"v214, v180, v181\n" \
M_"v[148:151], a[240:243], a[216:219], v[148:151]\n" \
"s_add_u32 m0, s31, 0x8000\n" \
"s_mov_b32 s84, 0x280000\n" \
M_"v[152:155], a[180:183], a[152:155], v[152:155]\n" \
B_"v26, s[68:71], s84 offen offset:0 lds\n" \
M_"v[152:155], a[244:247], a[216:219], v[152:155]\n" \
X_"a[166:167], v33 offset:512\n" \
C_"v215, v182, v183\n" \
M_"v[156:159], a[184:187], a[152:155], v[156:159]\n" \
X_"a[228:229], v33 offset:4096\n" \
"v_accvgpr_read_b32 v208, a104\n" \
M_"v[156:159], a[248:251], a[216:219], v[156:159]\n" \
"s_addk_i32 s84, 0x7c00\n" \
M_"v[160:163], a[188:191], a[152:155], v[160:163]\n" \
B_"v26, s[68:71], s84 offen offset:1024 lds\n" \
M_"v[160:163], a[252:255], a[216:219], v[160:163]\n" \
R_"a[152:155], v28 offset:45056\n" \
"v_accvgpr_read_b32 v209, a105\n" \
M_"v[176:179], a[188:191], a[156:159], v[176:179]\n" \
R_"a[216:219], v29 offset:45056\n" \
"v_accvgpr_read_b32 v210, a106\n" \
M_"v[176:179], a[252:255], a[220:223], v[176:179]\n" \
"s_addk_i32 s84, 0x7c00\n" \
M_"v[172:175], a[184:187], a[156:159], v[172:175]\n" \
B_"v27, s[68:71], s84 offen offset:2048 lds\n" \
M_"v[172:175], a[248:251], a[220:223], v[172:175]\n" \
X_"a[230:231], v33 offset:4608\n" \
"v_accvgpr_read_b32 v211, a107\n" \
M_"v[168:171], a[180:183], a[156:159], v[168:171]\n" \
X_"a[172:173], v35 offset:0\n" \
"v_accvgpr_read_b32 v180, a120\n" \
M_"v[168:171], a[244:247], a[220:223], v[168:171]\n" \
X_"a[174:175], v35 offset:512\n" \
"v_accvgpr_read_b32 v181, a121\n" \
M_"v[164:167], a[176:179], a[156:159], v[164:167]\n" \
"v_accvgpr_read_b32 v182, a122\n" \
"v_accvgpr_read_b32 v183, a123\n" \
M_"v[164:167], a[240:243], a[220:223], v[164:167]\n" \
R_"a[156:159], v28 offset:47104\n" \
C_"v208, v208, v209\n" \
M_"v[84:87], a[176:179], a[136:139], v[84:87]\n" \
R_"a[220:223], v29 offset:47104\n" \
C_"v209, v210, v211\n" \
M_"v[84:87], a[240:243], a[200:203], v[84:87]\n" \
"s_addk_i32 s84, 0x7c00\n" \
M_"v[88:91], a[180:183], a[136:139], v[88:91]\n" \
B_"v27, s[68:71], s84 offen offset:3072 lds\n" \
M_"v[88:91], a[244:247], a[200:203], v[88:91]\n" \
"s_add_u32 m0, s30, 0x0\n" \
"s_movk_i32 s84, 0x300\n" \
"s_nop 0\n" \
M_"v[92:95], a[184:187], a[136:139], v[92:95]\n" \
B_"v24, s[76:79], s84 offen offset:0 lds\n" \
M_"v[92:95], a[248:251], a[200:203], v[92:95]\n" \
X_"a[236:237], v35 offset:4096\n" \
C_"v210, v180, v181\n" \
M_"v[96:99], a[188:191], a[136:139], v[96:99]\n" \
X_"a[238:239], v35 offset:4608\n" \
C_"v211, v182, v183\n" \
M_"v[96:99], a[252:255], a[200:203], v[96:99]\n" \
R_"a[136:139], v28 offset:36864\n" \
"v_accvgpr_read_b32 v204, a100\n" \
M_"v[112:115], a[188:191], a[140:143], v[112:115]\n" \
R_"a[200:203], v29 offset:36864\n" \
"v_accvgpr_read_b32 v205, a101\n" \
M_"v[112:115], a[252:255], a[204:207], v[112:115]\n" \
"s_movk_i32 s84, 0x2300\n" \
"s_nop 0\n" \
M_"v[108:111], a[184:187], a[140:143], v[108:111]\n" \
B_"v25, s[76:79], s84 offen offset:1024 lds\n" \
M_"v[108:111], a[248:251], a[204:207], v[108:111]\n" \
"s_add_u32 s89, s42, 0x1000\n" \
"buffer_store_dwordx4 v[232:235], v20, s[80:83], s89 offen offset:1024 sc1\n" \
M_"v[104:107], a[180:183], a[140:143], v[104:107]\n" \
X_"a[168:169], v34 offset:0\n" \
"v_accvgpr_read_b32 v206, a102\n" \
M_"v[104:107], a[244:247], a[204:207], v[104:107]\n" \
X_"a[170:171], v34 offset:512\n" \
"v_accvgpr_read_b32 v207, a103\n" \
M_"v[100:103], a[176:179], a[140:143], v[100:103]\n" \
X_"a[232:233], v34 offset:4096\n" \
"v_accvgpr_read_b32 v180, a116\n" \
M_"v[100:103], a[240:243], a[204:207], v[100:103]\n" \
R_"a[140:143], v28 offset:38912\n" \
"v_accvgpr_read_b32 v181, a117\n" \
M_"v[52:55], a[176:179], a[128:131], v[52:55]\n" \
R_"a[204:207], v29 offset:38912\n" \
"v_accvgpr_read_b32 v182, a118\n" \
M_"v[52:55], a[240:243], a[192:195], v[52:55]\n" \
"s_movk_i32 s84, 0x4300\n" \
M_"v[56:59], a[180:183], a[128:131], v[56:59]\n" \
B_"v24, s[76:79], s84 offen offset:2048 lds\n" \
M_"v[56:59], a[244:247], a[192:195], v[56:59]\n" \
"s_movk_i32 s84, 0x6300\n" \
M_"v[60:63], a[184:187], a[128:131], v[60:63]\n" \
B_"v25, s[76:79], s84 offen offset:3072 lds\n" \
M_"v[60:63], a[248:251], a[192:195], v[60:63]\n" \
X_"a[234:235], v34 offset:4608\n" \
"v_accvgpr_read_b32 v183, a119\n" \
M_"v[64:67], a[188:191], a[128:131], v[64:67]\n" \
C_"v204, v204, v205\n" \
C_"v205, v206, v207\n" \
M_"v[64:67], a[252:255], a[192:195], v[64:67]\n" \
R_"a[128:131], v28 offset:32768\n" \
C_"v206, v180, v181\n" \
M_"v[80:83], a[188:191], a[132:135], v[80:83]\n" \
R_"a[192:195], v29 offset:32768\n" \
C_"v207, v182, v183\n" \
M_"v[80:83], a[252:255], a[196:199], v[80:83]\n" \
"v_accvgpr_read_b32 v200, a96\n" \
"v_accvgpr_read_b32 v201, a97\n" \
M_"v[76:79], a[184:187], a[132:135], v[76:79]\n" \
"v_accvgpr_read_b32 v202, a98\n" \
"v_accvgpr_read_b32 v203, a99\n" \
M_"v[76:79], a[248:251], a[196:199], v[76:79]\n" \
"v_accvgpr_read_b32 v180, a112\n" \
"v_accvgpr_read_b32 v181, a113\n" \
M_"v[72:75], a[180:183], a[132:135], v[72:75]\n" \
"v_accvgpr_read_b32 v182, a114\n" \
"v_accvgpr_read_b32 v183, a115\n" \
M_"v[72:75], a[244:247], a[196:199], v[72:75]\n" \
C_"v200, v200, v201\n" \
C_"v201, v202, v203\n" \
M_"v[68:71], a[176:179], a[132:135], v[68:71]\n" \
C_"v202, v180, v181\n" \
C_"v203, v182, v183\n" \
M_"v[68:71], a[240:243], a[196:199], v[68:71]\n" \
R_"a[132:135], v28 offset:34816\n" \
R_"a[196:199], v29 offset:34816\n" \
"s_waitcnt lgkmcnt(15)\n" \
M_"a[0:3], a[160:163], a[144:147], a[0:3]\n" \
"s_add_u32 m0, s30, 0x1000\n" \
"s_addk_i32 s84, 0x3000\n" \
M_"a[0:3], a[224:227], a[208:211], a[0:3]\n" \
B_"v24, s[76:79], s84 offen offset:0 lds\n" \
M_"a[4:7], a[164:167], a[144:147], a[4:7]\n" \
X_"a[176:177], v36 offset:0\n" \
"v_accvgpr_read_b32 v196, a76\n" \
M_"a[4:7], a[228:231], a[208:211], a[4:7]\n" \
X_"a[178:179], v36 offset:1024\n" \
"v_accvgpr_read_b32 v197, a77\n" \
M_"a[12:15], a[172:175], a[144:147], a[12:15]\n" \
X_"a[240:241], v36 offset:8192\n" \
"v_accvgpr_read_b32 v198, a78\n" \
"s_waitcnt lgkmcnt(15)\n" \
M_"a[12:15], a[236:239], a[208:211], a[12:15]\n" \
"s_add_u32 s88, s42, 0x1000\n" \
"buffer_store_dwordx4 v[236:239], v20, s[80:83], s88 offen offset:0 sc1\n" \
"s_waitcnt lgkmcnt(11)\n" \
M_"a[8:11], a[144:147], a[168:171], a[8:11]\n" \
X_"a[242:243], v36 offset:9216\n" \
"v_accvgpr_read_b32 v199, a79\n" \
"s_waitcnt lgkmcnt(8)\n" \
"s_nop 0\n" \
M_"a[8:11], a[208:211], a[232:235], a[8:11]\n" \
X_"a[180:181], v37 offset:0\n" \
"v_accvgpr_read_b32 v180, a92\n" \
M_"a[24:27], a[148:151], a[168:171], a[24:27]\n" \
X_"a[182:183], v37 offset:1024\n" \
"v_accvgpr_read_b32 v181, a93\n" \
M_"a[24:27], a[212:215], a[232:235], a[24:27]\n" \
X_"a[244:245], v37 offset:8192\n" \
"v_accvgpr_read_b32 v182, a94\n" \
M_"a[28:31], a[172:175], a[148:151], a[28:31]\n" \
"v_accvgpr_read_b32 v183, a95\n" \
C_"v196, v196, v197\n" \
M_"a[28:31], a[236:239], a[212:215], a[28:31]\n" \
"s_addk_i32 s84, 0x2000\n" \
"s_nop 0\n" \
M_"a[20:23], a[164:167], a[148:151], a[20:23]\n" \
B_"v25, s[76:79], s84 offen offset:1024 lds\n" \
".fill (L_pb + 69344 - .) / 4, 4, 0xBF800000\n" \
"L_pp17:\n" \
"s_cbranch_execz L_pret\n" \
M_"a[20:23], a[228:231], a[212:215], a[20:23]\n" \
X_"a[246:247], v37 offset:9216\n" \
C_"v197, v198, v199\n" \
M_"a[16:19], a[160:163], a[148:151], a[16:19]\n" \
X_"a[184:185], v38 offset:0\n" \
C_"v198, v180, v181\n" \
M_"a[16:19], a[224:227], a[212:215], a[16:19]\n" \
X_"a[186:187], v38 offset:1024\n" \
C_"v199, v182, v183\n" \
M_"a[32:35], a[160:163], a[152:155], a[32:35]\n" \
"s_addk_i32 s84, 0x2000\n" \
M_"a[32:35], a[224:227], a[216:219], a[32:35]\n" \
B_"v24, s[76:79], s84 offen offset:2048 lds\n" \
M_"a[36:39], a[164:167], a[152:155], a[36:39]\n" \
X_"a[248:249], v38 offset:8192\n" \
"v_accvgpr_read_b32 v192, a72\n" \
M_"a[36:39], a[228:231], a[216:219], a[36:39]\n" \
X_"a[250:251], v38 offset:9216\n" \
"v_accvgpr_read_b32 v193, a73\n" \
M_"a[44:47], a[172:175], a[152:155], a[44:47]\n" \
X_"a[188:189], v39 offset:0\n" \
"v_accvgpr_read_b32 v194, a74\n" \
M_"a[44:47], a[236:239], a[216:219], a[44:47]\n" \
X_"a[190:191], v39 offset:1024\n" \
"v_accvgpr_read_b32 v195, a75\n" \
M_"a[40:43], a[152:155], a[168:171], a[40:43]\n" \
"s_add_u32 s89, s42, 0x3000\n" \
"buffer_store_dwordx4 v[240:243], v20, s[80:83], s89 offen offset:3072 sc1\n" \
M_"a[40:43], a[216:219], a[232:235], a[40:43]\n" \
X_"a[252:253], v39 offset:8192\n" \
"v_accvgpr_read_b32 v180, a88\n" \
M_"a[56:59], a[156:159], a[168:171], a[56:59]\n" \
X_"a[254:255], v39 offset:9216\n" \
"v_accvgpr_read_b32 v181, a89\n" \
M_"a[56:59], a[220:223], a[232:235], a[56:59]\n" \
"v_accvgpr_read_b32 v182, a90\n" \
"v_accvgpr_read_b32 v183, a91\n" \
M_"a[60:63], a[172:175], a[156:159], a[60:63]\n" \
C_"v192, v192, v193\n" \
C_"v193, v194, v195\n" \
M_"a[60:63], a[236:239], a[220:223], a[60:63]\n" \
"s_addk_i32 s84, 0x2000\n" \
M_"a[52:55], a[164:167], a[156:159], a[52:55]\n" \
B_"v25, s[76:79], s84 offen offset:3072 lds\n" \
M_"a[52:55], a[228:231], a[220:223], a[52:55]\n" \
C_"v194, v180, v181\n" \
C_"v195, v182, v183\n" \
M_"a[48:51], a[160:163], a[156:159], a[48:51]\n" \
"s_mulk_i32 s8, 0x480\n" \
"s_or_b32 s13, s13, s11\n" \
M_"a[48:51], a[224:227], a[220:223], a[48:51]\n" \
"s_or_b32 s8, s8, s11\n" \
"s_and_b32 s11, s2, 1\n" \
M_"a[64:67], a[164:167], a[140:143], a[64:67]\n" \
"s_cmp_lg_u32 s11, 0\n" \
"s_cselect_b32 s12, s15, s12\n" \
M_"a[64:67], a[228:231], a[204:207], a[64:67]\n" \
"s_bfe_u32 s9, s2, 0x20001\n" \
"s_add_i32 s15, s7, 0x4240000\n" \
M_"a[80:83], a[172:175], a[140:143], a[80:83]\n" \
"s_addk_i32 s12, 0x4000\n" \
"s_lshl_b32 s5, s5, 16\n" \
M_"a[80:83], a[236:239], a[204:207], a[80:83]\n" \
"s_xor_b32 s9, s9, s3\n" \
M_"a[68:71], a[140:143], a[168:171], a[68:71]\n" \
"s_cmp_lg_u32 s9, 0\n" \
"s_cselect_b32 s12, s6, s12\n" \
"s_waitcnt vmcnt(11) lgkmcnt(0)\n" \
M_"a[68:71], a[204:207], a[232:235], a[68:71]\n" \
"s_barrier\n" \
"v_mov_b32_e32 v45, s62\n" \
"s_mov_b64 exec, 1\n" \
"buffer_store_dword v45, off, s[80:83], s37 offset:0 sc1\n" \
"s_mov_b64 exec, -1\n" \
"v_xor_b32_e32 v28, 0x20000, v28\n" \
"v_xor_b32_e32 v29, 0x20000, v29\n" \
M_"v[116:119], a[176:179], a[144:147], v[116:119]\n" \
"s_add_u32 m0, s40, 0x0\n" \
"s_add_u32 s85, s39, 0xc000\n" \
M_"v[116:119], a[240:243], a[208:211], v[116:119]\n" \
B_"v20, s[80:83], s85 offen sc1 lds\n" \
M_"v[120:123], a[180:183], a[144:147], v[120:123]\n" \
X_"a[160:161], v32 offset:32768\n" \
"s_add_i32 s5, s15, s5\n" \
"s_nop 0\n" \
M_"v[120:123], a[244:247], a[208:211], v[120:123]\n" \
X_"a[162:163], v32 offset:33280\n" \
"v_writelane_b32 v11, s12, 43\n" \
M_"v[124:127], a[184:187], a[144:147], v[124:127]\n" \
"s_add_u32 m0, s40, 0x1000\n" \
"s_add_u32 s85, s39, 0xd000\n" \
M_"v[124:127], a[248:251], a[208:211], v[124:127]\n" \
B_"v20, s[80:83], s85 offen sc1 lds\n" \
M_"v[128:131], a[188:191], a[144:147], v[128:131]\n" \
X_"a[224:225], v32 offset:36864\n" \
"s_add_i32 s7, s7, 0x42c0000\n" \
M_"v[128:131], a[252:255], a[208:211], v[128:131]\n" \
R_"a[144:147], v28 offset:8192\n" \
"v_writelane_b32 v11, s5, 44\n" \
M_"v[144:147], a[188:191], a[148:151], v[144:147]\n" \
R_"a[208:211], v29 offset:8192\n" \
"s_lshl_b32 s5, s11, 10\n" \
M_"v[144:147], a[252:255], a[212:215], v[144:147]\n" \
"s_add_u32 m0, s40, 0x3000\n" \
"s_movk_i32 s84, 0x300\n" \
M_"v[140:143], a[184:187], a[148:151], v[140:143]\n" \
B_"v21, s[76:79], s84 offen offset:0 lds\n" \
M_"v[140:143], a[248:251], a[212:215], v[140:143]\n" \
X_"a[226:227], v32 offset:37376\n" \
"s_or_b32 s5, s7, s5\n" \
M_"v[136:139], a[180:183], a[148:151], v[136:139]\n" \
"s_cmp_lg_u32 s9, 0\n" \
"s_cselect_b32 s5, s6, s5\n" \
M_"v[136:139], a[244:247], a[212:215], v[136:139]\n" \
X_"a[164:165], v33 offset:32768\n" \
"s_add_i32 s8, s8, 0x21c000\n" \
M_"v[132:135], a[176:179], a[148:151], v[132:135]\n" \
"v_writelane_b32 v11, s5, 45\n" \
"s_add_i32 s5, s13, s10\n" \
M_"v[132:135], a[240:243], a[212:215], v[132:135]\n" \
R_"a[148:151], v28 offset:10240\n" \
"v_writelane_b32 v11, s5, 46\n" \
M_"v[148:151], a[176:179], a[152:155], v[148:151]\n" \
R_"a[212:215], v29 offset:10240\n" \
"s_add_i32 s5, s8, s10\n" \
M_"v[148:151], a[240:243], a[216:219], v[148:151]\n" \
"s_add_u32 m0, s31, 0x0\n" \
"s_mov_b32 s84, 0x300000\n" \
M_"v[152:155], a[180:183], a[152:155], v[152:155]\n" \
B_"v26, s[68:71], s84 offen offset:0 lds\n" \
M_"v[152:155], a[244:247], a[216:219], v[152:155]\n" \
"s_addk_i32 s84, 0x7c00\n" \
"s_nop 0\n" \
M_"v[156:159], a[184:187], a[152:155], v[156:159]\n" \
B_"v26, s[68:71], s84 offen offset:1024 lds\n" \
M_"v[156:159], a[248:251], a[216:219], v[156:159]\n" \
X_"a[166:167], v33 offset:33280\n" \
"v_writelane_b32 v11, s5, 47\n" \
M_"v[160:163], a[188:191], a[152:155], v[160:163]\n" \
X_"a[228:229], v33 offset:36864\n" \
M_"v[160:163], a[252:255], a[216:219], v[160:163]\n" \
R_"a[152:155], v28 offset:12288\n" \
M_"v[176:179], a[188:191], a[156:159], v[176:179]\n" \
R_"a[216:219], v29 offset:12288\n" \
M_"v[176:179], a[252:255], a[220:223], v[176:179]\n" \
"s_addk_i32 s84, 0x7c00\n" \
M_"v[172:175], a[184:187], a[156:159], v[172:175]\n" \
B_"v27, s[68:71], s84 offen offset:2048 lds\n" \
M_"v[172:175], a[248:251], a[220:223], v[172:175]\n" \
X_"a[230:231], v33 offset:37376\n" \
M_"v[168:171], a[180:183], a[156:159], v[168:171]\n" \
"s_addk_i32 s84, 0x7c00\n" \
M_"v[168:171], a[244:247], a[220:223], v[168:171]\n" \
B_"v27, s[68:71], s84 offen offset:3072 lds\n" \
M_"v[164:167], a[176:179], a[156:159], v[164:167]\n" \
X_"a[172:173], v35 offset:32768\n" \
M_"v[164:167], a[240:243], a[220:223], v[164:167]\n" \
R_"a[156:159], v28 offset:14336\n" \
M_"v[84:87], a[176:179], a[136:139], v[84:87]\n" \
R_"a[220:223], v29 offset:14336\n" \
M_"v[84:87], a[240:243], a[200:203], v[84:87]\n" \
"s_add_u32 s88, s42, 0x3000\n" \
"buffer_store_dwordx4 v[244:247], v20, s[80:83], s88 offen offset:2048 sc1\n" \
M_"v[88:91], a[180:183], a[136:139], v[88:91]\n" \
X_"a[174:175], v35 offset:33280\n" \
M_"v[88:91], a[244:247], a[200:203], v[88:91]\n" \
X_"a[236:237], v35 offset:36864\n" \
M_"v[92:95], a[184:187], a[136:139], v[92:95]\n" \
X_"a[238:239], v35 offset:37376\n" \
M_"v[92:95], a[248:251], a[200:203], v[92:95]\n" \
"s_add_u32 m0, s30, 0x8000\n" \
"s_movk_i32 s84, 0x380\n" \
M_"v[96:99], a[188:191], a[136:139], v[96:99]\n" \
B_"v24, s[76:79], s84 offen offset:0 lds\n" \
M_"v[96:99], a[252:255], a[200:203], v[96:99]\n" \
R_"a[136:139], v28 offset:4096\n" \
M_"v[112:115], a[188:191], a[140:143], v[112:115]\n" \
R_"a[200:203], v29 offset:4096\n" \
M_"v[112:115], a[252:255], a[204:207], v[112:115]\n" \
X_"a[168:169], v34 offset:32768\n" \
M_"v[108:111], a[184:187], a[140:143], v[108:111]\n" \
"s_movk_i32 s84, 0x2380\n" \
M_"v[108:111], a[248:251], a[204:207], v[108:111]\n" \
B_"v25, s[76:79], s84 offen offset:1024 lds\n" \
M_"v[104:107], a[180:183], a[140:143], v[104:107]\n" \
X_"a[170:171], v34 offset:33280\n" \
M_"v[104:107], a[244:247], a[204:207], v[104:107]\n" \
X_"a[232:233], v34 offset:36864\n" \
M_"v[100:103], a[176:179], a[140:143], v[100:103]\n" \
"s_cmp_lg_u32 s11, 0\n" \
"s_cselect_b32 s5, s8, s13\n" \
M_"v[100:103], a[240:243], a[204:207], v[100:103]\n" \
R_"a[140:143], v28 offset:6144\n" \
"s_addk_i32 s5, 0x100\n" \
M_"v[52:55], a[176:179], a[128:131], v[52:55]\n" \
R_"a[204:207], v29 offset:6144\n" \
M_"v[52:55], a[240:243], a[192:195], v[52:55]\n" \
"s_movk_i32 s84, 0x4380\n" \
M_"v[56:59], a[180:183], a[128:131], v[56:59]\n" \
B_"v24, s[76:79], s84 offen offset:2048 lds\n" \
M_"v[56:59], a[244:247], a[192:195], v[56:59]\n" \
"s_movk_i32 s84, 0x6380\n" \
M_"v[60:63], a[184:187], a[128:131], v[60:63]\n" \
B_"v25, s[76:79], s84 offen offset:3072 lds\n" \
M_"v[60:63], a[248:251], a[192:195], v[60:63]\n" \
X_"a[234:235], v34 offset:37376\n" \
M_"v[64:67], a[188:191], a[128:131], v[64:67]\n" \
"s_cmp_lg_u32 s9, 0\n" \
"s_cselect_b32 s5, s6, s5\n" \
M_"v[64:67], a[252:255], a[192:195], v[64:67]\n" \
R_"a[128:131], v28 offset:0\n" \
"v_writelane_b32 v11, s5, 48\n" \
M_"v[80:83], a[188:191], a[132:135], v[80:83]\n" \
R_"a[192:195], v29 offset:0\n" \
"s_or_b32 s5, s7, s14\n" \
M_"v[80:83], a[252:255], a[196:199], v[80:83]\n" \
"v_writelane_b32 v11, s5, 56\n" \
"s_lshl_b32 s5, s3, 17\n" \
M_"v[76:79], a[184:187], a[132:135], v[76:79]\n" \
"s_add_i32 s5, s15, s5\n" \
"s_or_b32 s4, s5, s4\n" \
M_"v[76:79], a[248:251], a[196:199], v[76:79]\n" \
"s_or_b32 s4, s4, 0x3840\n" \
"v_writelane_b32 v11, s4, 6\n" \
M_"v[72:75], a[180:183], a[132:135], v[72:75]\n" \
"s_mov_b32 s4, 0xff0000\n" \
M_"v[72:75], a[244:247], a[196:199], v[72:75]\n" \
"s_cmp_lg_u32 s9, 0\n" \
"s_cselect_b32 s4, s16, s4\n" \
M_"v[68:71], a[176:179], a[132:135], v[68:71]\n" \
"v_writelane_b32 v11, s4, 7\n" \
"s_lshl_b32 s4, s2, 16\n" \
M_"v[68:71], a[240:243], a[196:199], v[68:71]\n" \
R_"a[132:135], v28 offset:2048\n" \
R_"a[196:199], v29 offset:2048\n" \
"s_waitcnt lgkmcnt(15)\n" \
M_"a[0:3], a[160:163], a[144:147], a[0:3]\n" \
"s_add_u32 m0, s30, 0x9000\n" \
"s_addk_i32 s84, 0x3000\n" \
M_"a[0:3], a[224:227], a[208:211], a[0:3]\n" \
B_"v24, s[76:79], s84 offen offset:0 lds\n" \
M_"a[4:7], a[164:167], a[144:147], a[4:7]\n" \
X_"a[176:177], v36 offset:32768\n" \
"s_add_i32 s5, s4, 0x10000\n" \
M_"a[4:7], a[228:231], a[208:211], a[4:7]\n" \
X_"a[178:179], v36 offset:33792\n" \
"s_and_b32 s5, s5, 0x70000\n" \
"s_waitcnt lgkmcnt(15)\n" \
M_"a[12:15], a[172:175], a[144:147], a[12:15]\n" \
X_"a[240:241], v36 offset:40960\n" \
"s_add_i32 s5, s15, s5\n" \
"s_waitcnt lgkmcnt(15)\n" \
M_"a[12:15], a[236:239], a[208:211], a[12:15]\n" \
"s_add_u32 s89, s42, 0x3000\n" \
"buffer_store_dwordx4 v[184:187], v20, s[80:83], s89 offen offset:1024 sc1\n" \
"s_waitcnt lgkmcnt(11)\n" \
M_"a[8:11], a[144:147], a[168:171], a[8:11]\n" \
X_"a[242:243], v36 offset:41984\n" \
"v_writelane_b32 v11, s5, 49\n" \
"s_waitcnt lgkmcnt(8)\n" \
M_"a[8:11], a[208:211], a[232:235], a[8:11]\n" \
X_"a[180:181], v37 offset:32768\n" \
"s_add_i32 s5, s4, 0x20000\n" \
M_"a[24:27], a[148:151], a[168:171], a[24:27]\n" \
X_"a[182:183], v37 offset:33792\n" \
"s_and_b32 s5, s5, 0x70000\n" \
M_"a[24:27], a[212:215], a[232:235], a[24:27]\n" \
X_"a[244:245], v37 offset:40960\n" \
"s_add_i32 s5, s15, s5\n" \
M_"a[28:31], a[172:175], a[148:151], a[28:31]\n" \
"s_bitset1_b32 s5, 11\n" \
"v_writelane_b32 v11, s5, 50\n" \
M_"a[28:31], a[236:239], a[212:215], a[28:31]\n" \
"s_addk_i32 s84, 0x2000\n" \
M_"a[20:23], a[164:167], a[148:151], a[20:23]\n" \
B_"v25, s[76:79], s84 offen offset:1024 lds\n" \
M_"a[20:23], a[228:231], a[212:215], a[20:23]\n" \
X_"a[246:247], v37 offset:41984\n" \
"s_add_i32 s5, s4, 0x30000\n" \
M_"a[16:19], a[160:163], a[148:151], a[16:19]\n" \
X_"a[184:185], v38 offset:32768\n" \
"s_and_b32 s5, s5, 0x70000\n" \
M_"a[16:19], a[224:227], a[212:215], a[16:19]\n" \
X_"a[186:187], v38 offset:33792\n" \
"s_add_i32 s5, s15, s5\n" \
M_"a[32:35], a[160:163], a[152:155], a[32:35]\n" \
"s_addk_i32 s84, 0x2000\n" \
M_"a[32:35], a[224:227], a[216:219], a[32:35]\n" \
B_"v24, s[76:79], s84 offen offset:2048 lds\n" \
M_"a[36:39], a[164:167], a[152:155], a[36:39]\n" \
X_"a[248:249], v38 offset:40960\n" \
"s_bitset1_b32 s5, 12\n" \
"s_nop 0\n" \
M_"a[36:39], a[228:231], a[216:219], a[36:39]\n" \
X_"a[250:251], v38 offset:41984\n" \
"v_writelane_b32 v11, s5, 51\n" \
M_"a[44:47], a[172:175], a[152:155], a[44:47]\n" \
X_"a[188:189], v39 offset:32768\n" \
"s_and_b32 s5, s4, 0x70000\n" \
M_"a[44:47], a[236:239], a[216:219], a[44:47]\n" \
X_"a[190:191], v39 offset:33792\n" \
"s_xor_b32 s5, s5, 0x40000\n" \
M_"a[40:43], a[152:155], a[168:171], a[40:43]\n" \
"s_add_u32 s88, s42, 0x3000\n" \
"buffer_store_dwordx4 v[188:191], v20, s[80:83], s88 offen offset:0 sc1\n" \
M_"a[40:43], a[216:219], a[232:235], a[40:43]\n" \
X_"a[252:253], v39 offset:40960\n" \
"s_add_i32 s5, s15, s5\n" \
M_"a[56:59], a[156:159], a[168:171], a[56:59]\n" \
X_"a[254:255], v39 offset:41984\n" \
"s_or_b32 s5, s5, 0x1800\n" \
M_"a[56:59], a[220:223], a[232:235], a[56:59]\n" \
"v_writelane_b32 v11, s5, 52\n" \
"s_add_i32 s5, s4, 0x50000\n" \
M_"a[60:63], a[172:175], a[156:159], a[60:63]\n" \
"s_and_b32 s5, s5, 0x70000\n" \
"s_add_i32 s5, s15, s5\n" \
M_"a[60:63], a[236:239], a[220:223], a[60:63]\n" \
"s_addk_i32 s84, 0x2000\n" \
M_"a[52:55], a[164:167], a[156:159], a[52:55]\n" \
B_"v25, s[76:79], s84 offen offset:3072 lds\n" \
M_"a[52:55], a[228:231], a[220:223], a[52:55]\n" \
"s_bitset1_b32 s5, 13\n" \
"v_writelane_b32 v11, s5, 53\n" \
M_"a[48:51], a[160:163], a[156:159], a[48:51]\n" \
"s_add_i32 s5, s4, 0x60000\n" \
"s_and_b32 s5, s5, 0x70000\n" \
M_"a[48:51], a[224:227], a[220:223], a[48:51]\n" \
"s_add_i32 s4, s4, 0x70000\n" \
"s_add_i32 s5, s15, s5\n" \
M_"a[64:67], a[164:167], a[140:143], a[64:67]\n" \
"s_and_b32 s4, s4, 0x70000\n" \
"s_or_b32 s5, s5, 0x2800\n" \
M_"a[64:67], a[228:231], a[204:207], a[64:67]\n" \
"v_writelane_b32 v11, s5, 54\n" \
"s_add_i32 s15, s15, s4\n" \
M_"a[80:83], a[172:175], a[140:143], a[80:83]\n" \
"s_or_b32 s4, s15, 0x3000\n" \
"v_writelane_b32 v11, s4, 55\n" \
M_"a[80:83], a[236:239], a[204:207], a[80:83]\n" \
"s_movk_i32 s11, 0x4000\n" \
"s_lshl_b32 s4, s2, 2\n" \
M_"a[68:71], a[140:143], a[168:171], a[68:71]\n" \
"s_and_b32 s4, s4, 28\n" \
"s_bfe_u32 s5, s2, 0x20003\n" \
"s_waitcnt vmcnt(11) lgkmcnt(0)\n" \
M_"a[68:71], a[204:207], a[232:235], a[68:71]\n" \
"s_barrier\n" \
"v_xor_b32_e32 v28, 0x20000, v28\n" \
"v_xor_b32_e32 v29, 0x20000, v29\n" \
M_"v[116:119], a[176:179], a[144:147], v[116:119]\n" \
"s_add_u32 m0, s40, 0x8000\n" \
"s_add_u32 s85, s39, 0xe000\n" \
M_"v[116:119], a[240:243], a[208:211], v[116:119]\n" \
B_"v20, s[80:83], s85 offen sc1 lds\n" \
M_"v[120:123], a[180:183], a[144:147], v[120:123]\n" \
X_"a[160:161], v32 offset:0\n" \
"s_or_b32 s4, s4, s5\n" \
M_"v[120:123], a[244:247], a[208:211], v[120:123]\n" \
X_"a[162:163], v32 offset:512\n" \
"s_lshr_b32 s5, s2, 5\n" \
M_"v[124:127], a[184:187], a[144:147], v[124:127]\n" \
"s_add_u32 m0, s40, 0x9000\n" \
"s_add_u32 s85, s39, 0xf000\n" \
M_"v[124:127], a[248:251], a[208:211], v[124:127]\n" \
B_"v20, s[80:83], s85 offen sc1 lds\n" \
M_"v[128:131], a[188:191], a[144:147], v[128:131]\n" \
X_"a[224:225], v32 offset:4096\n" \
"s_lshr_b32 s19, s3, 1\n" \
M_"v[128:131], a[252:255], a[208:211], v[128:131]\n" \
R_"a[144:147], v28 offset:8192\n" \
"s_lshl_b32 s18, s5, 1\n" \
M_"v[144:147], a[188:191], a[148:151], v[144:147]\n" \
R_"a[208:211], v29 offset:8192\n" \
"s_lshl_b32 s6, s4, 20\n" \
M_"v[144:147], a[252:255], a[212:215], v[144:147]\n" \
"s_add_u32 m0, s40, 0xb000\n" \
"s_movk_i32 s84, 0x380\n" \
M_"v[140:143], a[184:187], a[148:151], v[140:143]\n" \
B_"v21, s[76:79], s84 offen offset:0 lds\n" \
M_"v[140:143], a[248:251], a[212:215], v[140:143]\n" \
X_"a[226:227], v32 offset:4608\n" \
"s_lshl_b32 s7, s4, 8\n" \
M_"v[136:139], a[180:183], a[148:151], v[136:139]\n" \
"s_and_b32 s13, s3, 1\n" \
"s_add_i32 s14, s18, s19\n" \
M_"v[136:139], a[244:247], a[212:215], v[136:139]\n" \
X_"a[164:165], v33 offset:0\n" \
M_"v[132:135], a[176:179], a[148:151], v[132:135]\n" \
"s_add_u32 s6, s26, s6\n" \
"s_addc_u32 s9, s27, 0\n" \
M_"v[132:135], a[240:243], a[212:215], v[132:135]\n" \
R_"a[148:151], v28 offset:10240\n" \
M_"v[148:151], a[176:179], a[152:155], v[148:151]\n" \
R_"a[212:215], v29 offset:10240\n" \
M_"v[148:151], a[240:243], a[216:219], v[148:151]\n" \
"s_add_u32 m0, s31, 0x8000\n" \
"s_mov_b32 s84, 0x380000\n" \
M_"v[152:155], a[180:183], a[152:155], v[152:155]\n" \
B_"v26, s[68:71], s84 offen offset:0 lds\n" \
M_"v[152:155], a[244:247], a[216:219], v[152:155]\n" \
"s_addk_i32 s84, 0x7c00\n" \
"s_nop 0\n" \
M_"v[156:159], a[184:187], a[152:155], v[156:159]\n" \
B_"v26, s[68:71], s84 offen offset:1024 lds\n" \
M_"v[156:159], a[248:251], a[216:219], v[156:159]\n" \
X_"a[166:167], v33 offset:512\n" \
M_"v[160:163], a[188:191], a[152:155], v[160:163]\n" \
X_"a[228:229], v33 offset:4096\n" \
M_"v[160:163], a[252:255], a[216:219], v[160:163]\n" \
R_"a[152:155], v28 offset:12288\n" \
M_"v[176:179], a[188:191], a[156:159], v[176:179]\n" \
R_"a[216:219], v29 offset:12288\n" \
M_"v[176:179], a[252:255], a[220:223], v[176:179]\n" \
"s_addk_i32 s84, 0x7c00\n" \
M_"v[172:175], a[184:187], a[156:159], v[172:175]\n" \
B_"v27, s[68:71], s84 offen offset:2048 lds\n" \
M_"v[172:175], a[248:251], a[220:223], v[172:175]\n" \
X_"a[230:231], v33 offset:4608\n" \
M_"v[168:171], a[180:183], a[156:159], v[168:171]\n" \
"s_addk_i32 s84, 0x7c00\n" \
M_"v[168:171], a[244:247], a[220:223], v[168:171]\n" \
B_"v27, s[68:71], s84 offen offset:3072 lds\n" \
M_"v[164:167], a[176:179], a[156:159], v[164:167]\n" \
X_"a[172:173], v35 offset:0\n" \
M_"v[164:167], a[240:243], a[220:223], v[164:167]\n" \
R_"a[156:159], v28 offset:14336\n" \
M_"v[84:87], a[176:179], a[136:139], v[84:87]\n" \
R_"a[220:223], v29 offset:14336\n" \
M_"v[84:87], a[240:243], a[200:203], v[84:87]\n" \
"s_add_u32 s89, s42, 0x0\n" \
"buffer_store_dwordx4 v[192:195], v20, s[80:83], s89 offen offset:2048 sc1\n" \
M_"v[88:91], a[180:183], a[136:139], v[88:91]\n" \
X_"a[174:175], v35 offset:512\n" \
M_"v[88:91], a[244:247], a[200:203], v[88:91]\n" \
X_"a[236:237], v35 offset:4096\n" \
M_"v[92:95], a[184:187], a[136:139], v[92:95]\n" \
X_"a[238:239], v35 offset:4608\n" \
M_"v[92:95], a[248:251], a[200:203], v[92:95]\n" \
"s_add_u32 m0, s30, 0x20000\n" \
"s_movk_i32 s84, 0x400\n" \
M_"v[96:99], a[188:191], a[136:139], v[96:99]\n" \
B_"v24, s[76:79], s84 offen offset:0 lds\n" \
M_"v[96:99], a[252:255], a[200:203], v[96:99]\n" \
R_"a[136:139], v28 offset:4096\n" \
M_"v[112:115], a[188:191], a[140:143], v[112:115]\n" \
R_"a[200:203], v29 offset:4096\n" \
M_"v[112:115], a[252:255], a[204:207], v[112:115]\n" \
X_"a[168:169], v34 offset:0\n" \
M_"v[108:111], a[184:187], a[140:143], v[108:111]\n" \
"s_movk_i32 s84, 0x2400\n" \
M_"v[108:111], a[248:251], a[204:207], v[108:111]\n" \
B_"v25, s[76:79], s84 offen offset:1024 lds\n" \
M_"v[104:107], a[180:183], a[140:143], v[104:107]\n" \
X_"a[170:171], v34 offset:512\n" \
M_"v[104:107], a[244:247], a[204:207], v[104:107]\n" \
X_"a[232:233], v34 offset:4096\n" \
M_"v[100:103], a[176:179], a[140:143], v[100:103]\n" \
"s_add_u32 s8, s6, 0x1000000\n" \
"s_addc_u32 s6, s9, 0\n" \
M_"v[100:103], a[240:243], a[204:207], v[100:103]\n" \
R_"a[140:143], v28 offset:6144\n" \
"s_and_b32 s9, s6, 0xffff\n" \
M_"v[52:55], a[176:179], a[128:131], v[52:55]\n" \
R_"a[204:207], v29 offset:6144\n" \
"s_lshl_b32 s6, s5, 16\n" \
M_"v[52:55], a[240:243], a[192:195], v[52:55]\n" \
"s_movk_i32 s84, 0x4400\n" \
M_"v[56:59], a[180:183], a[128:131], v[56:59]\n" \
B_"v24, s[76:79], s84 offen offset:2048 lds\n" \
M_"v[56:59], a[244:247], a[192:195], v[56:59]\n" \
"s_movk_i32 s84, 0x6400\n" \
"s_nop 0\n" \
M_"v[60:63], a[184:187], a[128:131], v[60:63]\n" \
B_"v25, s[76:79], s84 offen offset:3072 lds\n" \
M_"v[60:63], a[248:251], a[192:195], v[60:63]\n" \
X_"a[234:235], v34 offset:4608\n" \
"s_sub_i32 s10, 0x100000, s6\n" \
M_"v[64:67], a[188:191], a[128:131], v[64:67]\n" \
"s_mov_b32 s6, 0x8000\n" \
M_"v[64:67], a[252:255], a[192:195], v[64:67]\n" \
R_"a[128:131], v28 offset:0\n" \
M_"v[80:83], a[188:191], a[132:135], v[80:83]\n" \
R_"a[192:195], v29 offset:0\n" \
M_"v[80:83], a[252:255], a[196:199], v[80:83]\n" \
"s_cmp_lg_u32 s5, 0\n" \
"s_cselect_b32 s17, s11, s6\n" \
M_"v[76:79], a[184:187], a[132:135], v[76:79]\n" \
"s_lshl_b32 s6, s3, 1\n" \
"s_lshl_b32 s5, s5, 3\n" \
M_"v[76:79], a[248:251], a[196:199], v[76:79]\n" \
"s_and_b32 s6, s6, 4\n" \
"s_or_b32 s12, s5, s6\n" \
M_"v[72:75], a[180:183], a[132:135], v[72:75]\n" \
"s_sub_i32 s5, s13, s5\n" \
"s_or_b32 s5, s5, s6\n" \
M_"v[72:75], a[244:247], a[196:199], v[72:75]\n" \
"s_or_b32 s6, s12, s13\n" \
"s_lshl_b32 s4, s4, 2\n" \
M_"v[68:71], a[176:179], a[132:135], v[68:71]\n" \
"s_mulk_i32 s6, 0x92\n" \
"s_add_i32 s4, s4, 18\n" \
M_"v[68:71], a[240:243], a[196:199], v[68:71]\n" \
R_"a[132:135], v28 offset:2048\n" \
R_"a[196:199], v29 offset:2048\n" \
"s_waitcnt lgkmcnt(15)\n" \
M_"a[0:3], a[160:163], a[144:147], a[0:3]\n" \
"s_add_u32 m0, s30, 0x21000\n" \
"s_addk_i32 s84, 0x3000\n" \
M_"a[0:3], a[224:227], a[208:211], a[0:3]\n" \
B_"v24, s[76:79], s84 offen offset:0 lds\n" \
M_"a[4:7], a[164:167], a[144:147], a[4:7]\n" \
X_"a[176:177], v36 offset:0\n" \
"s_mulk_i32 s5, 0x92\n" \
M_"a[4:7], a[228:231], a[208:211], a[4:7]\n" \
X_"a[178:179], v36 offset:1024\n" \
"s_add_i32 s6, s4, s6\n" \
"s_waitcnt lgkmcnt(15)\n" \
M_"a[12:15], a[172:175], a[144:147], a[12:15]\n" \
X_"a[240:241], v36 offset:8192\n" \
"s_add_i32 s5, s5, s4\n" \
"s_waitcnt lgkmcnt(15)\n" \
M_"a[12:15], a[236:239], a[208:211], a[12:15]\n" \
"s_add_u32 s88, s42, 0x0\n" \
"buffer_store_dwordx4 v[196:199], v20, s[80:83], s88 offen offset:3072 sc1\n" \
"s_waitcnt lgkmcnt(11)\n" \
M_"a[8:11], a[144:147], a[168:171], a[8:11]\n" \
X_"a[242:243], v36 offset:9216\n" \
"s_lshl_b32 s4, s3, 11\n" \
"s_waitcnt lgkmcnt(8)\n" \
M_"a[8:11], a[208:211], a[232:235], a[8:11]\n" \
X_"a[180:181], v37 offset:0\n" \
"s_lshl_b32 s15, s13, 12\n" \
M_"a[24:27], a[148:151], a[168:171], a[24:27]\n" \
X_"a[182:183], v37 offset:1024\n" \
"s_lshl_b32 s16, s19, 14\n" \
M_"a[24:27], a[212:215], a[232:235], a[24:27]\n" \
X_"a[244:245], v37 offset:8192\n" \
"s_mul_i32 s19, s19, 0xe000\n" \
M_"a[28:31], a[172:175], a[148:151], a[28:31]\n" \
"s_mov_b32 s11, 0x20000\n" \
"s_or_b32 s12, s4, 0xc000\n" \
M_"a[28:31], a[236:239], a[212:215], a[28:31]\n" \
"s_addk_i32 s84, 0x2000\n" \
M_"a[20:23], a[164:167], a[148:151], a[20:23]\n" \
B_"v25, s[76:79], s84 offen offset:1024 lds\n" \
M_"a[20:23], a[228:231], a[212:215], a[20:23]\n" \
X_"a[246:247], v37 offset:9216\n" \
"s_lshl_b32 s4, s3, 8\n" \
M_"a[16:19], a[160:163], a[148:151], a[16:19]\n" \
X_"a[184:185], v38 offset:0\n" \
"s_or_b32 s22, s15, s16\n" \
M_"a[16:19], a[224:227], a[212:215], a[16:19]\n" \
X_"a[186:187], v38 offset:1024\n" \
"s_or_b32 s23, s19, s15\n" \
M_"a[32:35], a[160:163], a[152:155], a[32:35]\n" \
"s_addk_i32 s84, 0x2000\n" \
M_"a[32:35], a[224:227], a[216:219], a[32:35]\n" \
B_"v24, s[76:79], s84 offen offset:2048 lds\n" \
M_"a[36:39], a[164:167], a[152:155], a[36:39]\n" \
X_"a[248:249], v38 offset:8192\n" \
"s_lshl_b32 s60, s6, 10\n" \
M_"a[36:39], a[228:231], a[216:219], a[36:39]\n" \
X_"a[250:251], v38 offset:9216\n" \
"s_lshl_b32 s61, s5, 10\n" \
M_"a[44:47], a[172:175], a[152:155], a[44:47]\n" \
X_"a[188:189], v39 offset:0\n" \
"s_lshl_b32 s14, s14, 15\n" \
"s_nop 0\n" \
M_"a[44:47], a[236:239], a[216:219], a[44:47]\n" \
X_"a[190:191], v39 offset:1024\n" \
"s_or_b32 s20, s4, 0x26c00\n" \
M_"a[40:43], a[152:155], a[168:171], a[40:43]\n" \
"s_add_u32 s89, s42, 0x2000\n" \
"buffer_store_dwordx4 v[200:203], v20, s[80:83], s89 offen offset:0 sc1\n" \
M_"a[40:43], a[216:219], a[232:235], a[40:43]\n" \
X_"a[252:253], v39 offset:8192\n" \
".fill (L_pb + 73440 - .) / 4, 4, 0xBF800000\n" \
"L_pp18:\n" \
"s_cbranch_execz L_pret\n" \
"s_or_b32 s13, s22, 0x8000\n" \
M_"a[56:59], a[156:159], a[168:171], a[56:59]\n" \
X_"a[254:255], v39 offset:9216\n" \
"s_add_i32 s22, s22, 0x14000\n" \
M_"a[56:59], a[220:223], a[232:235], a[56:59]\n" \
"s_bitset1_b32 s23, 16\n" \
"s_or_b32 s24, s7, 0x5be8020\n" \
M_"a[60:63], a[172:175], a[156:159], a[60:63]\n" \
"s_and_b32 s57, s27, 0xffff\n" \
"s_mov_b32 s58, 0x6000000\n" \
M_"a[60:63], a[236:239], a[220:223], a[60:63]\n" \
"s_addk_i32 s84, 0x2000\n" \
M_"a[52:55], a[164:167], a[156:159], a[52:55]\n" \
B_"v25, s[76:79], s84 offen offset:3072 lds\n" \
M_"a[52:55], a[228:231], a[220:223], a[52:55]\n" \
"s_mov_b32 s56, s26\n" \
"s_mov_b32 s59, s11\n" \
M_"a[48:51], a[160:163], a[156:159], a[48:51]\n" \
"s_add_i32 s60, s60, 0x3000000\n" \
"s_add_i32 s61, s61, 0x411c000\n" \
M_"a[48:51], a[224:227], a[220:223], a[48:51]\n" \
"s_sub_i32 s19, 14, s18\n" \
"s_mov_b32 s21, 0x71800000\n" \
M_"a[64:67], a[164:167], a[140:143], a[64:67]\n" \
"s_mov_b32 s4, L_gsw-L_gtr\n" \
M_"a[64:67], a[228:231], a[204:207], a[64:67]\n" \
"s_cmp_eq_u32 s19, 0\n" \
"s_cselect_b32 s4, L_gs7-L_gtr, s4\n" \
M_"a[80:83], a[172:175], a[140:143], a[80:83]\n" \
"s_cmp_eq_u32 s16, 0\n" \
"s_cselect_b32 s4, 0, s4\n" \
M_"a[80:83], a[236:239], a[204:207], a[80:83]\n" \
"s_getpc_b64 s[28:29]\n" \
"L_gp1_%=:\n" \
"s_add_u32 s28, s28, L_gtr-L_gp1_%=\n" \
"s_addc_u32 s29, s29, 0\n" \
M_"a[68:71], a[140:143], a[168:171], a[68:71]\n" \
"s_add_u32 s28, s28, s4\n" \
"s_addc_u32 s29, s29, 0\n" \
"s_waitcnt vmcnt(11) lgkmcnt(0)\n" \
M_"a[68:71], a[204:207], a[232:235], a[68:71]\n" \
"s_barrier\n" \
M_"v[116:119], a[176:179], a[144:147], v[116:119]\n" \
"s_add_u32 m0, s40, 0x0\n" \
"s_add_u32 s85, s39, 0x10000\n" \
M_"v[116:119], a[240:243], a[208:211], v[116:119]\n" \
B_"v20, s[80:83], s85 offen sc1 lds\n" \
M_"v[120:123], a[180:183], a[144:147], v[120:123]\n" \
X_"a[160:161], v32 offset:32768\n" \
"v_accvgpr_write_b32 a96, 0\n" \
M_"v[120:123], a[244:247], a[208:211], v[120:123]\n" \
X_"a[162:163], v32 offset:33280\n" \
"v_accvgpr_write_b32 a97, 0\n" \
M_"v[124:127], a[184:187], a[144:147], v[124:127]\n" \
"v_accvgpr_write_b32 a98, 0\n" \
"v_accvgpr_write_b32 a99, 0\n" \
M_"v[124:127], a[248:251], a[208:211], v[124:127]\n" \
"v_accvgpr_write_b32 a100, 0\n" \
"v_accvgpr_write_b32 a101, 0\n" \
M_"v[128:131], a[188:191], a[144:147], v[128:131]\n" \
X_"a[224:225], v32 offset:36864\n" \
"v_accvgpr_write_b32 a102, 0\n" \
M_"v[128:131], a[252:255], a[208:211], v[128:131]\n" \
R_"a[144:147], v28 offset:40960\n" \
"v_accvgpr_write_b32 a103, 0\n" \
M_"v[144:147], a[188:191], a[148:151], v[144:147]\n" \
R_"a[208:211], v29 offset:40960\n" \
"v_accvgpr_write_b32 a112, 0\n" \
M_"v[144:147], a[252:255], a[212:215], v[144:147]\n" \
"s_add_u32 m0, s40, 0x1000\n" \
"s_add_u32 s85, s39, 0x11000\n" \
M_"v[140:143], a[184:187], a[148:151], v[140:143]\n" \
B_"v20, s[80:83], s85 offen sc1 lds\n" \
M_"v[140:143], a[248:251], a[212:215], v[140:143]\n" \
X_"a[226:227], v32 offset:37376\n" \
"v_accvgpr_write_b32 a113, 0\n" \
M_"v[136:139], a[180:183], a[148:151], v[136:139]\n" \
"v_accvgpr_write_b32 a114, 0\n" \
"v_accvgpr_write_b32 a115, 0\n" \
M_"v[136:139], a[244:247], a[212:215], v[136:139]\n" \
X_"a[164:165], v33 offset:32768\n" \
"v_accvgpr_write_b32 a120, 0\n" \
M_"v[132:135], a[176:179], a[148:151], v[132:135]\n" \
"v_accvgpr_write_b32 a121, 0\n" \
"v_accvgpr_write_b32 a122, 0\n" \
M_"v[132:135], a[240:243], a[212:215], v[132:135]\n" \
R_"a[148:151], v28 offset:43008\n" \
"v_accvgpr_write_b32 a123, 0\n" \
M_"v[148:151], a[176:179], a[152:155], v[148:151]\n" \
R_"a[212:215], v29 offset:43008\n" \
"v_mov_b32_e32 v180, 0x3f803f80\n" \
M_"v[148:151], a[240:243], a[216:219], v[148:151]\n" \
"s_add_u32 m0, s40, 0x3000\n" \
"s_movk_i32 s84, 0x400\n" \
M_"v[152:155], a[180:183], a[152:155], v[152:155]\n" \
B_"v21, s[76:79], s84 offen offset:0 lds\n" \
M_"v[152:155], a[244:247], a[216:219], v[152:155]\n" \
X_"a[166:167], v33 offset:33280\n" \
"s_mov_b32 s6, 0x33333333\n" \
M_"v[156:159], a[184:187], a[152:155], v[156:159]\n" \
X_"a[228:229], v33 offset:36864\n" \
"s_mov_b32 s7, 0xcccccccc\n" \
M_"v[156:159], a[248:251], a[216:219], v[156:159]\n" \
"s_add_u32 m0, s31, 0x0\n" \
"s_mov_b32 s84, 0x400000\n" \
M_"v[160:163], a[188:191], a[152:155], v[160:163]\n" \
B_"v26, s[68:71], s84 offen offset:0 lds\n" \
M_"v[160:163], a[252:255], a[216:219], v[160:163]\n" \
R_"a[152:155], v28 offset:45056\n" \
"v_cndmask_b32_e64 v181, 0, v180, s[6:7]\n" \
M_"v[176:179], a[188:191], a[156:159], v[176:179]\n" \
R_"a[216:219], v29 offset:45056\n" \
"v_accvgpr_write_b32 a104, v181\n" \
M_"v[176:179], a[252:255], a[220:223], v[176:179]\n" \
X_"a[230:231], v33 offset:37376\n" \
"v_accvgpr_write_b32 a105, v181\n" \
M_"v[172:175], a[184:187], a[156:159], v[172:175]\n" \
X_"a[172:173], v35 offset:32768\n" \
"v_accvgpr_write_b32 a106, v181\n" \
M_"v[172:175], a[248:251], a[220:223], v[172:175]\n" \
"v_accvgpr_write_b32 a107, v181\n" \
"s_mov_b32 s6, 0x11111111\n" \
M_"v[168:171], a[180:183], a[156:159], v[168:171]\n" \
"s_add_u32 s88, s42, 0x2000\n" \
"buffer_store_dwordx4 v[204:207], v20, s[80:83], s88 offen offset:1024 sc1\n" \
M_"v[168:171], a[244:247], a[220:223], v[168:171]\n" \
X_"a[174:175], v35 offset:33280\n" \
"s_mov_b32 s7, 0x11111111\n" \
M_"v[164:167], a[176:179], a[156:159], v[164:167]\n" \
"v_cndmask_b32_e64 v181, 0, v180, s[6:7]\n" \
"v_accvgpr_write_b32 a108, v181\n" \
M_"v[164:167], a[240:243], a[220:223], v[164:167]\n" \
R_"a[156:159], v28 offset:47104\n" \
"v_accvgpr_write_b32 a109, v181\n" \
M_"v[84:87], a[176:179], a[136:139], v[84:87]\n" \
R_"a[220:223], v29 offset:47104\n" \
"v_accvgpr_write_b32 a110, v181\n" \
M_"v[84:87], a[240:243], a[200:203], v[84:87]\n" \
X_"a[236:237], v35 offset:36864\n" \
"v_accvgpr_write_b32 a111, v181\n" \
M_"v[88:91], a[180:183], a[136:139], v[88:91]\n" \
"s_mov_b32 s6, 0x44444444\n" \
"s_mov_b32 s7, 0x44444444\n" \
M_"v[88:91], a[244:247], a[200:203], v[88:91]\n" \
"s_addk_i32 s84, 0x7c00\n" \
"s_nop 0\n" \
M_"v[92:95], a[184:187], a[136:139], v[92:95]\n" \
B_"v26, s[68:71], s84 offen offset:1024 lds\n" \
M_"v[92:95], a[248:251], a[200:203], v[92:95]\n" \
X_"a[238:239], v35 offset:37376\n" \
"v_cndmask_b32_e64 v181, 0, v180, s[6:7]\n" \
M_"v[96:99], a[188:191], a[136:139], v[96:99]\n" \
"v_accvgpr_write_b32 a116, v181\n" \
"v_accvgpr_write_b32 a117, v181\n" \
M_"v[96:99], a[252:255], a[200:203], v[96:99]\n" \
R_"a[136:139], v28 offset:36864\n" \
"v_accvgpr_write_b32 a118, v181\n" \
M_"v[112:115], a[188:191], a[140:143], v[112:115]\n" \
R_"a[200:203], v29 offset:36864\n" \
"v_accvgpr_write_b32 a119, v181\n" \
M_"v[112:115], a[252:255], a[204:207], v[112:115]\n" \
X_"a[168:169], v34 offset:32768\n" \
"v_bfe_i32 v181, v20, 4, 1\n" \
M_"v[108:111], a[184:187], a[140:143], v[108:111]\n" \
"s_addk_i32 s84, 0x7c00\n" \
"s_nop 0\n" \
M_"v[108:111], a[248:251], a[204:207], v[108:111]\n" \
B_"v27, s[68:71], s84 offen offset:2048 lds\n" \
M_"v[104:107], a[180:183], a[140:143], v[104:107]\n" \
X_"a[170:171], v34 offset:33280\n" \
"v_and_b32_e32 v181, 0xaaaaaaaa, v181\n" \
M_"v[104:107], a[244:247], a[204:207], v[104:107]\n" \
X_"a[232:233], v34 offset:36864\n" \
"v_or_b32_e32 v181, 0x44444444, v181\n" \
M_"v[100:103], a[176:179], a[140:143], v[100:103]\n" \
"v_accvgpr_write_b32 a126, v181\n" \
"v_bfe_u32 v180, v20, 5, 3\n" \
M_"v[100:103], a[240:243], a[204:207], v[100:103]\n" \
R_"a[140:143], v28 offset:38912\n" \
"v_bfe_u32 v181, v20, 8, 2\n" \
M_"v[52:55], a[176:179], a[128:131], v[52:55]\n" \
R_"a[204:207], v29 offset:38912\n" \
"v_lshlrev_b32_e64 v181, 1, v181\n" \
M_"v[52:55], a[240:243], a[192:195], v[52:55]\n" \
"s_addk_i32 s84, 0x7c00\n" \
M_"v[56:59], a[180:183], a[128:131], v[56:59]\n" \
B_"v27, s[68:71], s84 offen offset:3072 lds\n" \
M_"v[56:59], a[244:247], a[192:195], v[56:59]\n" \
X_"a[234:235], v34 offset:37376\n" \
"v_sub_u32_e32 v180, v180, v181\n" \
M_"v[60:63], a[184:187], a[128:131], v[60:63]\n" \
"v_and_b32_e64 v181, 16, v20\n" \
"v_mov_b32_e32 v182, 0x3f80\n" \
M_"v[60:63], a[248:251], a[192:195], v[60:63]\n" \
"v_lshlrev_b32_e32 v181, v181, v182\n" \
"v_cmp_eq_u32_e32 vcc, 0, v180\n" \
M_"v[64:67], a[188:191], a[128:131], v[64:67]\n" \
"v_cmp_eq_u32_e64 s[6:7], 1, v180\n" \
"v_cndmask_b32_e64 v182, 0, v181, vcc\n" \
M_"v[64:67], a[252:255], a[192:195], v[64:67]\n" \
R_"a[128:131], v28 offset:32768\n" \
"v_cndmask_b32_e64 v183, 0, v181, s[6:7]\n" \
M_"v[80:83], a[188:191], a[132:135], v[80:83]\n" \
R_"a[192:195], v29 offset:32768\n" \
"v_accvgpr_write_b32 a124, v182\n" \
M_"v[80:83], a[252:255], a[196:199], v[80:83]\n" \
"v_accvgpr_write_b32 a125, v183\n" \
M_"v[76:79], a[184:187], a[132:135], v[76:79]\n" \
M_"v[76:79], a[248:251], a[196:199], v[76:79]\n" \
M_"v[72:75], a[180:183], a[132:135], v[72:75]\n" \
M_"v[72:75], a[244:247], a[196:199], v[72:75]\n" \
M_"v[68:71], a[176:179], a[132:135], v[68:71]\n" \
M_"v[68:71], a[240:243], a[196:199], v[68:71]\n" \
R_"a[132:135], v28 offset:34816\n" \
R_"a[196:199], v29 offset:34816\n" \
"s_waitcnt lgkmcnt(15)\n" \
"s_nop 0\n" \
M_"a[0:3], a[160:163], a[144:147], a[0:3]\n" \
"s_add_u32 s89, s42, 0x2000\n" \
"buffer_store_dwordx4 v[208:211], v20, s[80:83], s89 offen offset:2048 sc1\n" \
M_"a[0:3], a[224:227], a[208:211], a[0:3]\n" \
X_"a[176:177], v36 offset:32768\n" \
M_"a[4:7], a[164:167], a[144:147], a[4:7]\n" \
X_"a[178:179], v36 offset:33792\n" \
M_"a[4:7], a[228:231], a[208:211], a[4:7]\n" \
X_"a[240:241], v36 offset:40960\n" \
M_"a[12:15], a[172:175], a[144:147], a[12:15]\n" \
X_"a[242:243], v36 offset:41984\n" \
"s_waitcnt lgkmcnt(15)\n" \
M_"a[12:15], a[236:239], a[208:211], a[12:15]\n" \
"s_waitcnt lgkmcnt(12)\n" \
M_"a[8:11], a[144:147], a[168:171], a[8:11]\n" \
X_"a[180:181], v37 offset:32768\n" \
"s_waitcnt lgkmcnt(9)\n" \
"s_nop 0\n" \
M_"a[8:11], a[208:211], a[232:235], a[8:11]\n" \
M_"a[24:27], a[148:151], a[168:171], a[24:27]\n" \
X_"a[182:183], v37 offset:33792\n" \
M_"a[24:27], a[212:215], a[232:235], a[24:27]\n" \
X_"a[244:245], v37 offset:40960\n" \
M_"a[28:31], a[172:175], a[148:151], a[28:31]\n" \
M_"a[28:31], a[236:239], a[212:215], a[28:31]\n" \
X_"a[246:247], v37 offset:41984\n" \
M_"a[20:23], a[164:167], a[148:151], a[20:23]\n" \
M_"a[20:23], a[228:231], a[212:215], a[20:23]\n" \
X_"a[184:185], v38 offset:32768\n" \
M_"a[16:19], a[160:163], a[148:151], a[16:19]\n" \
X_"a[186:187], v38 offset:33792\n" \
M_"a[16:19], a[224:227], a[212:215], a[16:19]\n" \
M_"a[32:35], a[160:163], a[152:155], a[32:35]\n" \
"s_add_u32 s88, s42, 0x2000\n" \
"buffer_store_dwordx4 v[212:215], v20, s[80:83], s88 offen offset:3072 sc1\n" \
M_"a[32:35], a[224:227], a[216:219], a[32:35]\n" \
X_"a[248:249], v38 offset:40960\n" \
M_"a[36:39], a[164:167], a[152:155], a[36:39]\n" \
"v_lshlrev_b32_e32 v45, 3, v20\n" \
"v_add_u32_e32 v45, s30, v45\n" \
M_"a[36:39], a[228:231], a[216:219], a[36:39]\n" \
"s_getpc_b64 s[86:87]\n" \
"L_ct1_%=:\n" \
"s_add_u32 s86, s86, L_ap0-L_ct1_%=\n" \
"s_addc_u32 s87, s87, 0\n" \
"s_add_u32 m0, s40, 0x2000\n" \
M_"a[44:47], a[172:175], a[152:155], a[44:47]\n" \
"global_load_lds_dword v45, s[86:87]\n" \
M_"a[44:47], a[236:239], a[216:219], a[44:47]\n" \
X_"a[250:251], v38 offset:41984\n" \
M_"a[40:43], a[152:155], a[168:171], a[40:43]\n" \
X_"a[188:189], v39 offset:32768\n" \
M_"a[40:43], a[216:219], a[232:235], a[40:43]\n" \
X_"a[190:191], v39 offset:33792\n" \
M_"a[56:59], a[156:159], a[168:171], a[56:59]\n" \
X_"a[252:253], v39 offset:40960\n" \
M_"a[56:59], a[220:223], a[232:235], a[56:59]\n" \
X_"a[254:255], v39 offset:41984\n" \
M_"a[60:63], a[172:175], a[156:159], a[60:63]\n" \
M_"a[60:63], a[236:239], a[220:223], a[60:63]\n" \
M_"a[52:55], a[164:167], a[156:159], a[52:55]\n" \
M_"a[52:55], a[228:231], a[220:223], a[52:55]\n" \
M_"a[48:51], a[160:163], a[156:159], a[48:51]\n" \
M_"a[48:51], a[224:227], a[220:223], a[48:51]\n" \
M_"a[64:67], a[164:167], a[140:143], a[64:67]\n" \
M_"a[64:67], a[228:231], a[204:207], a[64:67]\n" \
M_"a[80:83], a[172:175], a[140:143], a[80:83]\n" \
M_"a[80:83], a[236:239], a[204:207], a[80:83]\n" \
M_"a[68:71], a[140:143], a[168:171], a[68:71]\n" \
"s_waitcnt vmcnt(3) lgkmcnt(0)\n" \
M_"a[68:71], a[204:207], a[232:235], a[68:71]\n" \
"s_barrier\n" \
"v_xor_b32_e32 v28, 0x20000, v28\n" \
"v_xor_b32_e32 v29, 0x20000, v29\n" \
M_"v[116:119], a[176:179], a[144:147], v[116:119]\n" \
X_"a[160:161], v32 offset:0\n" \
"v_accvgpr_read_b32 v184, a0\n" \
M_"v[116:119], a[240:243], a[208:211], v[116:119]\n" \
"v_accvgpr_read_b32 v185, a1\n" \
"v_accvgpr_read_b32 v186, a2\n" \
M_"v[120:123], a[180:183], a[144:147], v[120:123]\n" \
"v_accvgpr_read_b32 v187, a3\n" \
"v_accvgpr_read_b32 v188, a4\n" \
M_"v[120:123], a[244:247], a[208:211], v[120:123]\n" \
X_"a[162:163], v32 offset:512\n" \
"v_accvgpr_read_b32 v189, a5\n" \
M_"v[124:127], a[184:187], a[144:147], v[124:127]\n" \
"v_accvgpr_read_b32 v190, a6\n" \
"v_accvgpr_read_b32 v191, a7\n" \
M_"v[124:127], a[248:251], a[208:211], v[124:127]\n" \
"v_accvgpr_read_b32 v196, a12\n" \
"v_accvgpr_read_b32 v197, a13\n" \
M_"v[128:131], a[188:191], a[144:147], v[128:131]\n" \
X_"a[224:225], v32 offset:4096\n" \
"v_accvgpr_read_b32 v198, a14\n" \
M_"v[128:131], a[252:255], a[208:211], v[128:131]\n" \
R_"a[144:147], v28 offset:8192\n" \
"v_accvgpr_read_b32 v199, a15\n" \
M_"v[144:147], a[188:191], a[148:151], v[144:147]\n" \
R_"a[208:211], v29 offset:8192\n" \
"v_accvgpr_read_b32 v192, a8\n" \
M_"v[144:147], a[252:255], a[212:215], v[144:147]\n" \
X_"a[226:227], v32 offset:4608\n" \
"v_accvgpr_read_b32 v193, a9\n" \
M_"v[140:143], a[184:187], a[148:151], v[140:143]\n" \
"v_accvgpr_read_b32 v194, a10\n" \
"v_accvgpr_read_b32 v195, a11\n" \
M_"v[140:143], a[248:251], a[212:215], v[140:143]\n" \
"v_accvgpr_read_b32 v208, a24\n" \
"v_accvgpr_read_b32 v209, a25\n" \
M_"v[136:139], a[180:183], a[148:151], v[136:139]\n" \
"v_accvgpr_read_b32 v210, a26\n" \
"v_accvgpr_read_b32 v211, a27\n" \
M_"v[136:139], a[244:247], a[212:215], v[136:139]\n" \
X_"a[164:165], v33 offset:0\n" \
"v_accvgpr_read_b32 v212, a28\n" \
M_"v[132:135], a[176:179], a[148:151], v[132:135]\n" \
"v_accvgpr_read_b32 v213, a29\n" \
"v_accvgpr_read_b32 v214, a30\n" \
M_"v[132:135], a[240:243], a[212:215], v[132:135]\n" \
R_"a[148:151], v28 offset:10240\n" \
"v_accvgpr_read_b32 v215, a31\n" \
M_"v[148:151], a[176:179], a[152:155], v[148:151]\n" \
R_"a[212:215], v29 offset:10240\n" \
"v_accvgpr_read_b32 v204, a20\n" \
M_"v[148:151], a[240:243], a[216:219], v[148:151]\n" \
X_"a[166:167], v33 offset:512\n" \
"v_accvgpr_read_b32 v205, a21\n" \
M_"v[152:155], a[180:183], a[152:155], v[152:155]\n" \
"v_accvgpr_read_b32 v206, a22\n" \
"v_accvgpr_read_b32 v207, a23\n" \
M_"v[152:155], a[244:247], a[216:219], v[152:155]\n" \
X_"a[228:229], v33 offset:4096\n" \
"v_accvgpr_read_b32 v200, a16\n" \
M_"v[156:159], a[184:187], a[152:155], v[156:159]\n" \
"v_accvgpr_read_b32 v201, a17\n" \
"v_accvgpr_read_b32 v202, a18\n" \
M_"v[156:159], a[248:251], a[216:219], v[156:159]\n" \
"v_accvgpr_read_b32 v203, a19\n" \
"v_accvgpr_read_b32 v216, a32\n" \
M_"v[160:163], a[188:191], a[152:155], v[160:163]\n" \
X_"a[230:231], v33 offset:4608\n" \
"v_accvgpr_read_b32 v217, a33\n" \
M_"v[160:163], a[252:255], a[216:219], v[160:163]\n" \
R_"a[152:155], v28 offset:12288\n" \
"v_accvgpr_read_b32 v218, a34\n" \
M_"v[176:179], a[188:191], a[156:159], v[176:179]\n" \
R_"a[216:219], v29 offset:12288\n" \
"v_accvgpr_read_b32 v219, a35\n" \
M_"v[176:179], a[252:255], a[220:223], v[176:179]\n" \
"v_accvgpr_read_b32 v220, a36\n" \
"v_accvgpr_read_b32 v221, a37\n" \
M_"v[172:175], a[184:187], a[156:159], v[172:175]\n" \
X_"a[172:173], v35 offset:0\n" \
"v_accvgpr_read_b32 v222, a38\n" \
M_"v[172:175], a[248:251], a[220:223], v[172:175]\n" \
"v_accvgpr_read_b32 v223, a39\n" \
"v_accvgpr_read_b32 v228, a44\n" \
M_"v[168:171], a[180:183], a[156:159], v[168:171]\n" \
"v_accvgpr_read_b32 v229, a45\n" \
"v_accvgpr_read_b32 v230, a46\n" \
M_"v[168:171], a[244:247], a[220:223], v[168:171]\n" \
X_"a[174:175], v35 offset:512\n" \
"v_accvgpr_read_b32 v231, a47\n" \
M_"v[164:167], a[176:179], a[156:159], v[164:167]\n" \
"v_accvgpr_read_b32 v224, a40\n" \
"v_accvgpr_read_b32 v225, a41\n" \
M_"v[164:167], a[240:243], a[220:223], v[164:167]\n" \
R_"a[156:159], v28 offset:14336\n" \
"v_accvgpr_read_b32 v226, a42\n" \
M_"v[84:87], a[176:179], a[136:139], v[84:87]\n" \
R_"a[220:223], v29 offset:14336\n" \
"v_accvgpr_read_b32 v227, a43\n" \
M_"v[84:87], a[240:243], a[200:203], v[84:87]\n" \
X_"a[236:237], v35 offset:4096\n" \
"v_accvgpr_read_b32 v240, a56\n" \
M_"v[88:91], a[180:183], a[136:139], v[88:91]\n" \
"v_accvgpr_read_b32 v241, a57\n" \
"v_accvgpr_read_b32 v242, a58\n" \
M_"v[88:91], a[244:247], a[200:203], v[88:91]\n" \
X_"a[238:239], v35 offset:4608\n" \
"v_accvgpr_read_b32 v243, a59\n" \
M_"v[92:95], a[184:187], a[136:139], v[92:95]\n" \
"v_accvgpr_read_b32 v244, a60\n" \
"v_accvgpr_read_b32 v245, a61\n" \
M_"v[92:95], a[248:251], a[200:203], v[92:95]\n" \
"v_accvgpr_read_b32 v246, a62\n" \
"v_accvgpr_read_b32 v247, a63\n" \
M_"v[96:99], a[188:191], a[136:139], v[96:99]\n" \
"v_accvgpr_read_b32 v236, a52\n" \
"v_accvgpr_read_b32 v237, a53\n" \
M_"v[96:99], a[252:255], a[200:203], v[96:99]\n" \
R_"a[136:139], v28 offset:4096\n" \
"v_accvgpr_read_b32 v238, a54\n" \
M_"v[112:115], a[188:191], a[140:143], v[112:115]\n" \
R_"a[200:203], v29 offset:4096\n" \
"v_accvgpr_read_b32 v239, a55\n" \
M_"v[112:115], a[252:255], a[204:207], v[112:115]\n" \
X_"a[168:169], v34 offset:0\n" \
"v_accvgpr_read_b32 v232, a48\n" \
M_"v[108:111], a[184:187], a[140:143], v[108:111]\n" \
X_"a[170:171], v34 offset:512\n" \
"v_accvgpr_read_b32 v233, a49\n" \
M_"v[108:111], a[248:251], a[204:207], v[108:111]\n" \
"v_accvgpr_read_b32 v234, a50\n" \
"v_accvgpr_read_b32 v235, a51\n" \
M_"v[104:107], a[180:183], a[140:143], v[104:107]\n" \
"v_accvgpr_read_b32 v180, a64\n" \
"v_accvgpr_read_b32 v181, a65\n" \
M_"v[104:107], a[244:247], a[204:207], v[104:107]\n" \
X_"a[232:233], v34 offset:4096\n" \
"v_accvgpr_read_b32 v182, a66\n" \
M_"v[100:103], a[176:179], a[140:143], v[100:103]\n" \
"v_accvgpr_read_b32 v183, a67\n" \
"v_accvgpr_read_b32 v48, a80\n" \
M_"v[100:103], a[240:243], a[204:207], v[100:103]\n" \
R_"a[140:143], v28 offset:6144\n" \
"v_accvgpr_read_b32 v49, a81\n" \
M_"v[52:55], a[176:179], a[128:131], v[52:55]\n" \
R_"a[204:207], v29 offset:6144\n" \
"v_accvgpr_read_b32 v50, a82\n" \
M_"v[52:55], a[240:243], a[192:195], v[52:55]\n" \
X_"a[234:235], v34 offset:4608\n" \
"v_accvgpr_read_b32 v51, a83\n" \
M_"v[56:59], a[180:183], a[128:131], v[56:59]\n" \
"v_accvgpr_read_b32 v252, a68\n" \
"v_accvgpr_read_b32 v253, a69\n" \
M_"v[56:59], a[244:247], a[192:195], v[56:59]\n" \
"v_accvgpr_read_b32 v254, a70\n" \
"v_accvgpr_read_b32 v255, a71\n" \
M_"v[60:63], a[184:187], a[128:131], v[60:63]\n" \
"v_lshrrev_b32_e64 v24, 9, v20\n" \
"v_and_b32_e32 v26, 0x1ff, v20\n" \
M_"v[60:63], a[248:251], a[192:195], v[60:63]\n" \
"v_lshl_add_u32 v24, v24, 3, v26\n" \
"v_lshrrev_b32_e64 v25, 8, v20\n" \
M_"v[64:67], a[188:191], a[128:131], v[64:67]\n" \
"v_mul_u32_u24_e32 v25, 0x24700, v25\n" \
"v_add_u32_e64 v25, v25, v20\n" \
M_"v[64:67], a[252:255], a[192:195], v[64:67]\n" \
R_"a[128:131], v28 offset:0\n" \
"v_lshrrev_b32_e64 v26, 2, v20\n" \
M_"v[80:83], a[188:191], a[132:135], v[80:83]\n" \
R_"a[192:195], v29 offset:0\n" \
"v_add_u32_e64 v26, s45, v26\n" \
M_"v[80:83], a[252:255], a[196:199], v[80:83]\n" \
"v_add_u32_e32 v27, s46, v20\n" \
"v_add_u32_e32 v21, s43, v27\n" \
M_"v[76:79], a[184:187], a[132:135], v[76:79]\n" \
M_"v[76:79], a[248:251], a[196:199], v[76:79]\n" \
M_"v[72:75], a[180:183], a[132:135], v[72:75]\n" \
M_"v[72:75], a[244:247], a[196:199], v[72:75]\n" \
M_"v[68:71], a[176:179], a[132:135], v[68:71]\n" \
M_"v[68:71], a[240:243], a[196:199], v[68:71]\n" \
R_"a[132:135], v28 offset:2048\n" \
R_"a[196:199], v29 offset:2048\n" \
"s_waitcnt lgkmcnt(15)\n" \
"s_nop 0\n" \
M_"v[184:187], a[160:163], a[144:147], v[184:187]\n" \
"v_bfe_u32 v44, v20, 2, 5\n" \
X_"a[176:177], v36 offset:0\n" \
M_"v[184:187], a[224:227], a[208:211], v[184:187]\n" \
"buffer_load_dword v44, v44, s[80:83], s38 offen offset:0 sc1\n" \
M_"v[188:191], a[164:167], a[144:147], v[188:191]\n" \
X_"a[178:179], v36 offset:1024\n" \
M_"v[188:191], a[228:231], a[208:211], v[188:191]\n" \
X_"a[240:241], v36 offset:8192\n" \
M_"v[196:199], a[172:175], a[144:147], v[196:199]\n" \
X_"a[242:243], v36 offset:9216\n" \
"s_waitcnt lgkmcnt(15)\n" \
M_"v[196:199], a[236:239], a[208:211], v[196:199]\n" \
"s_waitcnt lgkmcnt(12)\n" \
M_"v[192:195], a[144:147], a[168:171], v[192:195]\n" \
X_"a[180:181], v37 offset:0\n" \
"s_waitcnt lgkmcnt(9)\n" \
"s_nop 0\n" \
M_"v[192:195], a[208:211], a[232:235], v[192:195]\n" \
M_"v[208:211], a[148:151], a[168:171], v[208:211]\n" \
X_"a[182:183], v37 offset:1024\n" \
M_"v[208:211], a[212:215], a[232:235], v[208:211]\n" \
X_"a[244:245], v37 offset:8192\n" \
M_"v[212:215], a[172:175], a[148:151], v[212:215]\n" \
M_"v[212:215], a[236:239], a[212:215], v[212:215]\n" \
X_"a[246:247], v37 offset:9216\n" \
C_"v188, v188, v189\n" \
M_"v[204:207], a[164:167], a[148:151], v[204:207]\n" \
C_"v189, v190, v191\n" \
C_"v190, v198, v199\n" \
M_"v[204:207], a[228:231], a[212:215], v[204:207]\n" \
X_"a[184:185], v38 offset:0\n" \
"v_mov_b32_e32 v191, 0x3f80\n" \
M_"v[200:203], a[160:163], a[148:151], v[200:203]\n" \
X_"a[186:187], v38 offset:1024\n" \
C_"v184, v184, v185\n" \
M_"v[200:203], a[224:227], a[212:215], v[200:203]\n" \
C_"v185, v186, v187\n" \
C_"v186, v196, v197\n" \
M_"v[216:219], a[160:163], a[152:155], v[216:219]\n" \
X_"a[248:249], v38 offset:8192\n" \
"v_mov_b32_e64 v187, 0\n" \
M_"v[216:219], a[224:227], a[216:219], v[216:219]\n" \
X_"a[250:251], v38 offset:9216\n" \
M_"v[220:223], a[164:167], a[152:155], v[220:223]\n" \
M_"v[220:223], a[228:231], a[216:219], v[220:223]\n" \
X_"a[188:189], v39 offset:0\n" \
C_"v204, v204, v205\n" \
M_"v[228:231], a[172:175], a[152:155], v[228:231]\n" \
C_"v205, v206, v207\n" \
C_"v206, v214, v215\n" \
M_"v[228:231], a[236:239], a[216:219], v[228:231]\n" \
X_"a[190:191], v39 offset:1024\n" \
"v_mov_b32_e32 v207, 0x3f80\n" \
M_"v[224:227], a[152:155], a[168:171], v[224:227]\n" \
X_"a[252:253], v39 offset:8192\n" \
C_"v200, v200, v201\n" \
M_"v[224:227], a[216:219], a[232:235], v[224:227]\n" \
C_"v201, v202, v203\n" \
C_"v202, v212, v213\n" \
M_"v[240:243], a[156:159], a[168:171], v[240:243]\n" \
".fill (L_pb + 77536 - .) / 4, 4, 0xBF800000\n" \
"L_pp19:\n" \
"s_cbranch_execz L_pret\n" \
X_"a[254:255], v39 offset:9216\n" \
"v_mov_b32_e32 v203, 0\n" \
M_"v[240:243], a[220:223], a[232:235], v[240:243]\n" \
M_"v[244:247], a[172:175], a[156:159], v[244:247]\n" \
M_"v[244:247], a[236:239], a[220:223], v[244:247]\n" \
C_"v220, v220, v221\n" \
C_"v221, v222, v223\n" \
M_"v[236:239], a[164:167], a[156:159], v[236:239]\n" \
C_"v222, v230, v231\n" \
"v_mov_b32_e32 v223, 0x3f80\n" \
M_"v[236:239], a[228:231], a[220:223], v[236:239]\n" \
C_"v216, v216, v217\n" \
C_"v217, v218, v219\n" \
M_"v[232:235], a[160:163], a[156:159], v[232:235]\n" \
C_"v218, v228, v229\n" \
"v_mov_b32_e64 v219, 0\n" \
M_"v[232:235], a[224:227], a[220:223], v[232:235]\n" \
M_"v[180:183], a[164:167], a[140:143], v[180:183]\n" \
M_"v[180:183], a[228:231], a[204:207], v[180:183]\n" \
M_"v[48:51], a[172:175], a[140:143], v[48:51]\n" \
M_"v[48:51], a[236:239], a[204:207], v[48:51]\n" \
C_"v236, v236, v237\n" \
C_"v237, v238, v239\n" \
M_"v[252:255], a[140:143], a[168:171], v[252:255]\n" \
C_"v238, v246, v247\n" \
"v_mov_b32_e32 v239, 0x3f80\n" \
M_"v[252:255], a[204:207], a[232:235], v[252:255]\n" \
C_"v232, v232, v233\n" \
C_"v233, v234, v235\n" \
"s_waitcnt vmcnt(0) lgkmcnt(0)\n" \
"s_barrier\n" \
"v_mov_b32_e32 v45, s62\n" \
"s_mov_b64 exec, 1\n" \
"buffer_store_dword v45, off, s[80:83], s37 offset:32 sc1\n" \
"s_mov_b64 exec, -1\n" \
"v_cmp_ne_u32_e32 vcc, s62, v44\n" \
"s_nop 4\n" \
"s_cbranch_vccnz L_poll161_%=\n" \
"L_pollr161_%=:\n" \
M_"v[116:119], a[176:179], a[144:147], v[116:119]\n" \
"s_mov_b32 s73, s50\n" \
"s_sub_u32 m0, s31, 0x10000\n" \
M_"v[116:119], a[240:243], a[208:211], v[116:119]\n" \
B_"v20, s[80:83], s73 offen offset:0 sc1 lds\n" \
M_"v[120:123], a[180:183], a[144:147], v[120:123]\n" \
"s_add_u32 s74, s50, 0x4000\n" \
"s_add_u32 s75, s50, 0x8000\n" \
M_"v[120:123], a[244:247], a[208:211], v[120:123]\n" \
"s_mov_b32 s70, 0x3e0293ee\n" \
"s_mov_b32 s76, s52\n" \
M_"v[124:127], a[184:187], a[144:147], v[124:127]\n" \
"v_mul_f32_e32 v116, s70, v116\n" \
"v_mul_f32_e32 v117, s70, v117\n" \
M_"v[124:127], a[248:251], a[208:211], v[124:127]\n" \
B_"v20, s[80:83], s73 offen offset:1024 sc1 lds\n" \
M_"v[128:131], a[188:191], a[144:147], v[128:131]\n" \
"v_mul_f32_e32 v118, s70, v118\n" \
"v_mul_f32_e32 v119, s70, v119\n" \
M_"v[128:131], a[252:255], a[208:211], v[128:131]\n" \
C_"v116, v116, v117\n" \
"v_mul_f32_e64 v120, s70, v120\n" \
M_"v[144:147], a[188:191], a[148:151], v[144:147]\n" \
"v_mul_f32_e32 v124, s70, v124\n" \
"v_mul_f32_e32 v125, s70, v125\n" \
M_"v[144:147], a[252:255], a[212:215], v[144:147]\n" \
B_"v20, s[80:83], s73 offen offset:2048 sc1 lds\n" \
M_"v[140:143], a[184:187], a[148:151], v[140:143]\n" \
"v_mul_f32_e32 v126, s70, v126\n" \
"v_mul_f32_e32 v127, s70, v127\n" \
M_"v[140:143], a[248:251], a[212:215], v[140:143]\n" \
C_"v124, v124, v125\n" \
"v_mul_f32_e64 v121, s70, v121\n" \
M_"v[136:139], a[180:183], a[148:151], v[136:139]\n" \
"v_mul_f32_e32 v122, s70, v122\n" \
"v_mul_f32_e32 v123, s70, v123\n" \
M_"v[136:139], a[244:247], a[212:215], v[136:139]\n" \
B_"v20, s[80:83], s73 offen offset:3072 sc1 lds\n" \
M_"v[132:135], a[176:179], a[148:151], v[132:135]\n" \
"s_sub_u32 m0, s31, s51\n" \
C_"v117, v118, v119\n" \
M_"v[132:135], a[240:243], a[212:215], v[132:135]\n" \
"v_mul_f32_e32 v128, s70, v128\n" \
"v_mul_f32_e64 v129, s70, v129\n" \
M_"v[148:151], a[176:179], a[152:155], v[148:151]\n" \
"v_mul_f32_e32 v130, s70, v130\n" \
"v_mul_f32_e32 v131, s70, v131\n" \
M_"v[148:151], a[240:243], a[216:219], v[148:151]\n" \
B_"v20, s[80:83], s74 offen offset:0 sc1 lds\n" \
M_"v[152:155], a[180:183], a[152:155], v[152:155]\n" \
C_"v125, v126, v127\n" \
C_"v118, v120, v121\n" \
M_"v[152:155], a[244:247], a[216:219], v[152:155]\n" \
C_"v119, v122, v123\n" \
C_"v126, v128, v129\n" \
M_"v[156:159], a[184:187], a[152:155], v[156:159]\n" \
C_"v127, v130, v131\n" \
"v_mul_f32_e64 v132, s70, v132\n" \
M_"v[156:159], a[248:251], a[216:219], v[156:159]\n" \
B_"v20, s[80:83], s74 offen offset:1024 sc1 lds\n" \
M_"v[160:163], a[188:191], a[152:155], v[160:163]\n" \
"ds_write_b128 v21, v[116:119] offset:16384\n" \
M_"v[160:163], a[252:255], a[216:219], v[160:163]\n" \
"v_mul_f32_e32 v133, s70, v133\n" \
"v_mul_f32_e32 v140, s70, v140\n" \
M_"v[176:179], a[188:191], a[156:159], v[176:179]\n" \
"v_mul_f32_e32 v141, s70, v141\n" \
"v_mul_f32_e32 v134, s70, v134\n" \
M_"v[176:179], a[252:255], a[220:223], v[176:179]\n" \
B_"v20, s[80:83], s74 offen offset:2048 sc1 lds\n" \
M_"v[172:175], a[184:187], a[156:159], v[172:175]\n" \
"ds_write_b128 v21, v[124:127] offset:17408\n" \
M_"v[172:175], a[248:251], a[220:223], v[172:175]\n" \
"v_mul_f32_e64 v135, s70, v135\n" \
C_"v132, v132, v133\n" \
M_"v[168:171], a[180:183], a[156:159], v[168:171]\n" \
"v_mul_f32_e32 v142, s70, v142\n" \
"v_mul_f32_e32 v143, s70, v143\n" \
M_"v[168:171], a[244:247], a[220:223], v[168:171]\n" \
B_"v20, s[80:83], s74 offen offset:3072 sc1 lds\n" \
M_"v[164:167], a[176:179], a[156:159], v[164:167]\n" \
"s_sub_u32 m0, s31, 0x4000\n" \
C_"v140, v140, v141\n" \
M_"v[164:167], a[240:243], a[220:223], v[164:167]\n" \
"v_mul_f32_e32 v136, s70, v136\n" \
"v_mul_f32_e32 v137, s70, v137\n" \
M_"v[84:87], a[176:179], a[136:139], v[84:87]\n" \
"v_mul_f32_e32 v138, s70, v138\n" \
"v_mul_f32_e32 v139, s70, v139\n" \
M_"v[84:87], a[240:243], a[200:203], v[84:87]\n" \
B_"v20, s[80:83], s75 offen offset:0 sc1 lds\n" \
M_"v[88:91], a[180:183], a[136:139], v[88:91]\n" \
C_"v133, v134, v135\n" \
"v_mul_f32_e64 v144, s70, v144\n" \
M_"v[88:91], a[244:247], a[200:203], v[88:91]\n" \
"v_mul_f32_e32 v145, s70, v145\n" \
"v_mul_f32_e32 v146, s70, v146\n" \
M_"v[92:95], a[184:187], a[136:139], v[92:95]\n" \
"v_mul_f32_e64 v147, s70, v147\n" \
C_"v141, v142, v143\n" \
M_"v[92:95], a[248:251], a[200:203], v[92:95]\n" \
B_"v20, s[80:83], s75 offen offset:1024 sc1 lds\n" \
M_"v[96:99], a[188:191], a[136:139], v[96:99]\n" \
C_"v134, v136, v137\n" \
C_"v135, v138, v139\n" \
M_"v[96:99], a[252:255], a[200:203], v[96:99]\n" \
C_"v142, v144, v145\n" \
C_"v143, v146, v147\n" \
M_"v[112:115], a[188:191], a[140:143], v[112:115]\n" \
"ds_write_b128 v21, v[132:135] offset:20480\n" \
M_"v[112:115], a[252:255], a[204:207], v[112:115]\n" \
B_"v20, s[80:83], s75 offen offset:2048 sc1 lds\n" \
M_"v[108:111], a[184:187], a[140:143], v[108:111]\n" \
C_"v180, v180, v181\n" \
C_"v181, v182, v183\n" \
M_"v[108:111], a[248:251], a[204:207], v[108:111]\n" \
"ds_write_b128 v21, v[140:143] offset:21504\n" \
M_"v[104:107], a[180:183], a[140:143], v[104:107]\n" \
C_"v234, v244, v245\n" \
"v_mov_b32_e64 v235, 0\n" \
M_"v[104:107], a[244:247], a[204:207], v[104:107]\n" \
B_"v20, s[80:83], s75 offen offset:3072 sc1 lds\n" \
M_"v[100:103], a[176:179], a[140:143], v[100:103]\n" \
"s_mov_b32 m0, s53\n" \
"s_nop 0\n" \
C_"v182, v50, v51\n" \
M_"v[100:103], a[240:243], a[204:207], v[100:103]\n" \
"v_mov_b32_e32 v183, 0x3f80\n" \
C_"v118, v192, v193\n" \
M_"v[52:55], a[176:179], a[128:131], v[52:55]\n" \
C_"v119, v194, v195\n" \
C_"v120, v208, v209\n" \
M_"v[52:55], a[240:243], a[192:195], v[52:55]\n" \
B_"v20, s[80:83], s76 offen offset:0 sc1 lds\n" \
M_"v[56:59], a[180:183], a[128:131], v[56:59]\n" \
C_"v121, v210, v211\n" \
C_"v122, v224, v225\n" \
M_"v[56:59], a[244:247], a[192:195], v[56:59]\n" \
C_"v123, v226, v227\n" \
C_"v124, v240, v241\n" \
C_"v125, v242, v243\n" \
M_"v[60:63], a[184:187], a[128:131], v[60:63]\n" \
M_"v[192:195], v[188:191], v[184:187], 0\n" \
B_"v20, s[80:83], s76 offen offset:1024 sc1 lds\n" \
M_"v[32:35], v[180:183], v[184:187], 0\n" \
M_"v[208:211], v[204:207], v[200:203], 0\n" \
M_"v[36:39], v[188:191], v[200:203], 0\n" \
M_"v[224:227], v[220:223], v[216:219], 0\n" \
M_"v[248:251], v[204:207], v[216:219], 0\n" \
M_"v[240:243], v[236:239], v[232:235], 0\n" \
M_"v[128:131], v[220:223], v[232:235], 0\n" \
"s_mov_b32 s64, 0xaaaaaaaa\n" \
"s_mov_b32 s65, 0xaaaaaaaa\n" \
M_"v[60:63], a[248:251], a[192:195], v[60:63]\n" \
"s_mov_b32 s66, 0xfff0ffff\n" \
"s_mov_b32 s67, 0xf000ff00\n" \
M_"v[64:67], a[188:191], a[128:131], v[64:67]\n" \
"s_mov_b32 s68, 0xffc0fffc\n" \
"s_mov_b32 s69, 0xc000fc00\n" \
"v_cndmask_b32_e64 v192, v192, v193, s[64:65]\n" \
"v_cndmask_b32_e64 v32, v32, v33, s[64:65]\n" \
"v_cndmask_b32_e64 v194, v194, v195, s[64:65]\n" \
"v_cndmask_b32_e64 v34, v34, v35, s[64:65]\n" \
"v_cndmask_b32_e64 v208, v208, v209, s[64:65]\n" \
"v_cndmask_b32_e64 v36, v36, v37, s[64:65]\n" \
"v_cndmask_b32_e64 v210, v210, v211, s[64:65]\n" \
"v_cndmask_b32_e64 v38, v38, v39, s[64:65]\n" \
"v_cndmask_b32_e64 v224, v224, v225, s[64:65]\n" \
"v_cndmask_b32_e64 v248, v248, v249, s[64:65]\n" \
"v_cndmask_b32_e64 v226, v226, v227, s[64:65]\n" \
"v_cndmask_b32_e64 v250, v250, v251, s[64:65]\n" \
"v_cndmask_b32_e64 v240, v240, v241, s[64:65]\n" \
"v_cndmask_b32_e64 v128, v128, v129, s[64:65]\n" \
"v_cndmask_b32_e64 v242, v242, v243, s[64:65]\n" \
"v_cndmask_b32_e64 v130, v130, v131, s[64:65]\n" \
"v_cndmask_b32_e64 v192, v32, v192, s[66:67]\n" \
"v_cndmask_b32_e64 v194, v34, v194, s[68:69]\n" \
"v_cndmask_b32_e64 v208, v36, v208, s[66:67]\n" \
"v_cndmask_b32_e64 v210, v38, v210, s[68:69]\n" \
"v_cndmask_b32_e64 v224, v248, v224, s[66:67]\n" \
"v_cndmask_b32_e64 v226, v250, v226, s[68:69]\n" \
"v_cndmask_b32_e64 v240, v128, v240, s[66:67]\n" \
"v_cndmask_b32_e64 v242, v130, v242, s[68:69]\n" \
M_"v[64:67], a[252:255], a[192:195], v[64:67]\n" \
"v_max_f32_e32 v193, v192, v194\n" \
"v_max_f32_e32 v209, v208, v210\n" \
"v_max_f32_e32 v225, v224, v226\n" \
"v_max_f32_e32 v241, v240, v242\n" \
"v_mul_f32_e32 v148, s70, v148\n" \
"v_permlane32_swap_b32_e32 v193, v225\n" \
"v_permlane32_swap_b32_e32 v209, v241\n" \
"v_mul_f32_e32 v149, s70, v149\n" \
"v_mul_f32_e32 v156, s70, v156\n" \
"v_mul_f32_e32 v157, s70, v157\n" \
"v_permlane32_swap_b32_e32 v196, v228\n" \
"v_permlane32_swap_b32_e32 v212, v244\n" \
"v_max_f32_e32 v193, v193, v225\n" \
"v_max_f32_e32 v209, v209, v241\n" \
"v_mul_f32_e32 v150, s70, v150\n" \
"v_mul_f32_e32 v151, s70, v151\n" \
C_"v148, v148, v149\n" \
"v_mul_f32_e32 v158, s70, v158\n" \
"v_mul_f32_e32 v159, s70, v159\n" \
C_"v156, v156, v157\n" \
"v_permlane16_swap_b32_e32 v228, v244\n" \
"v_permlane16_swap_b32_e32 v193, v209\n" \
M_"v[80:83], a[188:191], a[132:135], v[80:83]\n" \
"s_mov_b32 s72, 0x3eae1a93\n" \
"v_mul_f32_e32 v152, s70, v152\n" \
"v_mul_f32_e32 v153, s70, v153\n" \
"v_mul_f32_e32 v154, s70, v154\n" \
"v_mul_f32_e32 v155, s70, v155\n" \
C_"v149, v150, v151\n" \
"v_mul_f32_e32 v160, s70, v160\n" \
"v_mul_f32_e32 v161, s70, v161\n" \
"v_mul_f32_e32 v162, s70, v162\n" \
"v_mul_f32_e32 v163, s70, v163\n" \
C_"v157, v158, v159\n" \
"v_mul_f32_e32 v45, 0xbfb8aa3b, v228\n" \
"v_mfma_f32_16x16x16_bf16 v[48:51], v[182:183], v[46:47], 0\n" \
"v_mfma_f32_16x16x16_bf16 v[196:199], v[190:191], v[46:47], 0\n" \
"v_mfma_f32_16x16x16_bf16 v[212:215], v[206:207], v[46:47], 0\n" \
"v_max_f32_e64 v193, v193, v209\n" \
C_"v150, v152, v153\n" \
C_"v151, v154, v155\n" \
C_"v158, v160, v161\n" \
C_"v159, v162, v163\n" \
"v_mfma_f32_16x16x16_bf16 v[228:231], v[222:223], v[46:47], 0\n" \
"v_mul_f32_e32 v193, s72, v193\n" \
"v_mov_b32_e32 v209, v193\n" \
"ds_write_b128 v21, v[148:151] offset:24576\n" \
"v_mfma_f32_16x16x16_bf16 v[244:247], v[238:239], v[46:47], 0\n" \
"v_permlane16_swap_b32_e32 v193, v209\n" \
"s_mov_b32 s71, 0x3e0293ee\n" \
M_"v[80:83], a[252:255], a[196:199], v[80:83]\n" \
"v_mov_b32_e32 v225, v193\n" \
"v_mov_b32_e32 v241, v209\n" \
"v_mul_f32_e32 v164, s70, v164\n" \
"v_mul_f32_e64 v165, s70, v165\n" \
"ds_write_b128 v21, v[156:159] offset:25600\n" \
"v_mul_f32_e32 v172, s70, v172\n" \
"v_mul_f32_e32 v173, s70, v173\n" \
"v_pk_mul_f32 v[84:85], v[84:85], s[70:71]\n" \
"v_pk_mul_f32 v[92:93], v[92:93], s[70:71]\n" \
"v_pk_mul_f32 v[100:101], v[100:101], s[70:71]\n" \
"v_pk_mul_f32 v[108:109], v[108:109], s[70:71]\n" \
"v_pk_mul_f32 v[52:53], v[52:53], s[70:71]\n" \
"v_pk_mul_f32 v[60:61], v[60:61], s[70:71]\n" \
"v_permlane32_swap_b32_e32 v193, v225\n" \
"v_permlane32_swap_b32_e32 v209, v241\n" \
"v_mul_f32_e32 v166, s70, v166\n" \
"v_mul_f32_e32 v167, s70, v167\n" \
C_"v164, v164, v165\n" \
"v_mul_f32_e32 v174, s70, v174\n" \
"v_mul_f32_e32 v175, s70, v175\n" \
C_"v172, v172, v173\n" \
"v_pk_mul_f32 v[86:87], v[86:87], s[70:71]\n" \
C_"v84, v84, v85\n" \
"v_pk_mul_f32 v[94:95], v[94:95], s[70:71]\n" \
C_"v92, v92, v93\n" \
"v_pk_mul_f32 v[102:103], v[102:103], s[70:71]\n" \
C_"v100, v100, v101\n" \
"v_pk_mul_f32 v[110:111], v[110:111], s[70:71]\n" \
C_"v108, v108, v109\n" \
"v_pk_mul_f32 v[54:55], v[54:55], s[70:71]\n" \
C_"v52, v52, v53\n" \
"v_pk_mul_f32 v[62:63], v[62:63], s[70:71]\n" \
C_"v60, v60, v61\n" \
"v_fma_f32 v192, v192, s72, -v193\n" \
"v_fma_f32 v194, v194, s72, -v193\n" \
"v_fma_f32 v208, v208, s72, -v209\n" \
"v_fma_f32 v210, v210, s72, -v209\n" \
"v_mul_f32_e32 v168, s70, v168\n" \
"v_mul_f32_e32 v169, s70, v169\n" \
"v_mul_f32_e32 v170, s70, v170\n" \
"v_mul_f32_e32 v171, s70, v171\n" \
C_"v165, v166, v167\n" \
"v_mul_f32_e32 v176, s70, v176\n" \
"v_mul_f32_e32 v177, s70, v177\n" \
"v_mul_f32_e32 v178, s70, v178\n" \
"v_mul_f32_e32 v179, s70, v179\n" \
C_"v173, v174, v175\n" \
"v_pk_mul_f32 v[88:89], v[88:89], s[70:71]\n" \
"v_pk_mul_f32 v[90:91], v[90:91], s[70:71]\n" \
C_"v85, v86, v87\n" \
"v_pk_mul_f32 v[96:97], v[96:97], s[70:71]\n" \
"v_pk_mul_f32 v[98:99], v[98:99], s[70:71]\n" \
C_"v93, v94, v95\n" \
"v_pk_mul_f32 v[104:105], v[104:105], s[70:71]\n" \
"v_pk_mul_f32 v[106:107], v[106:107], s[70:71]\n" \
C_"v101, v102, v103\n" \
"v_pk_mul_f32 v[112:113], v[112:113], s[70:71]\n" \
"v_pk_mul_f32 v[114:115], v[114:115], s[70:71]\n" \
C_"v109, v110, v111\n" \
"v_pk_mul_f32 v[56:57], v[56:57], s[70:71]\n" \
"v_pk_mul_f32 v[58:59], v[58:59], s[70:71]\n" \
C_"v53, v54, v55\n" \
"v_pk_mul_f32 v[64:65], v[64:65], s[70:71]\n" \
"v_pk_mul_f32 v[66:67], v[66:67], s[70:71]\n" \
C_"v61, v62, v63\n" \
"v_pk_mul_f32 v[80:81], v[80:81], s[70:71]\n" \
"v_pk_mul_f32 v[82:83], v[82:83], s[70:71]\n" \
E_"v192, v192\n" \
E_"v194, v194\n" \
E_"v208, v208\n" \
E_"v210, v210\n" \
"v_fma_f32 v224, v224, s72, -v225\n" \
"v_fma_f32 v226, v226, s72, -v225\n" \
M_"v[76:79], a[184:187], a[132:135], v[76:79]\n" \
C_"v166, v168, v169\n" \
C_"v167, v170, v171\n" \
C_"v174, v176, v177\n" \
C_"v175, v178, v179\n" \
C_"v86, v88, v89\n" \
C_"v87, v90, v91\n" \
C_"v94, v96, v97\n" \
C_"v95, v98, v99\n" \
C_"v102, v104, v105\n" \
C_"v103, v106, v107\n" \
C_"v110, v112, v113\n" \
C_"v111, v114, v115\n" \
C_"v54, v56, v57\n" \
C_"v55, v58, v59\n" \
C_"v62, v64, v65\n" \
C_"v63, v66, v67\n" \
E_"v224, v224\n" \
E_"v226, v226\n" \
"v_fma_f32 v240, v240, s72, -v241\n" \
"v_fma_f32 v242, v242, s72, -v241\n" \
"ds_write_b128 v21, v[164:167] offset:28672\n" \
C_"v192, v192, v194\n" \
C_"v208, v208, v210\n" \
E_"v240, v240\n" \
E_"v242, v242\n" \
C_"v132, v48, v49\n" \
C_"v133, v50, v51\n" \
C_"v134, v196, v197\n" \
C_"v135, v198, v199\n" \
"ds_write_b128 v21, v[172:175] offset:29696\n" \
C_"v136, v212, v213\n" \
C_"v137, v214, v215\n" \
"v_perm_b32 v184, v192, v192, v40\n" \
"v_perm_b32 v185, v192, v192, v41\n" \
"v_perm_b32 v186, v192, v192, v42\n" \
"v_perm_b32 v187, v192, v192, v43\n" \
"v_perm_b32 v200, v208, v208, v40\n" \
"v_perm_b32 v201, v208, v208, v41\n" \
"v_perm_b32 v202, v208, v208, v42\n" \
"v_perm_b32 v203, v208, v208, v43\n" \
"ds_write_b128 v27, v[84:87] offset:8192\n" \
C_"v224, v224, v226\n" \
C_"v138, v228, v229\n" \
C_"v139, v230, v231\n" \
M_"v[32:35], v[132:135], v[184:187], 0\n" \
M_"v[36:39], v[134:137], v[200:203], 0\n" \
"v_perm_b32 v216, v224, v224, v40\n" \
"v_perm_b32 v217, v224, v224, v41\n" \
"ds_write_b128 v27, v[92:95] offset:9216\n" \
"v_perm_b32 v218, v224, v224, v42\n" \
"v_perm_b32 v219, v224, v224, v43\n" \
C_"v240, v240, v242\n" \
M_"v[76:79], a[248:251], a[196:199], v[76:79]\n" \
C_"v140, v244, v245\n" \
C_"v141, v246, v247\n" \
M_"v[248:251], v[136:139], v[216:219], 0\n" \
"v_perm_b32 v232, v240, v240, v40\n" \
"v_perm_b32 v233, v240, v240, v41\n" \
"ds_write_b128 v27, v[100:103] offset:12288\n" \
"v_perm_b32 v234, v240, v240, v42\n" \
"v_perm_b32 v235, v240, v240, v43\n" \
"v_pk_mul_f32 v[76:77], v[76:77], s[70:71]\n" \
"v_pk_mul_f32 v[78:79], v[78:79], s[70:71]\n" \
C_"v76, v76, v77\n" \
C_"v77, v78, v79\n" \
C_"v78, v80, v81\n" \
C_"v79, v82, v83\n" \
M_"v[128:131], v[138:141], v[232:235], 0\n" \
"ds_write_b128 v27, v[108:111] offset:13312\n" \
M_"v[72:75], a[180:183], a[132:135], v[72:75]\n" \
C_"v116, v252, v253\n" \
C_"v117, v254, v255\n" \
M_"v[72:75], a[244:247], a[196:199], v[72:75]\n" \
"v_exp_f32_e64 v45, v45\n" \
M_"v[192:195], v[116:119], v[184:187], 0\n" \
"ds_write_b128 v27, v[52:55] offset:0\n" \
M_"v[208:211], v[118:121], v[200:203], 0\n" \
M_"v[224:227], v[120:123], v[216:219], 0\n" \
M_"v[240:243], v[122:125], v[232:235], 0\n" \
"ds_write_b128 v27, v[60:63] offset:1024\n" \
"v_mov_b32_dpp v34, v38 quad_perm:[0,1,2,3] row_mask:0x2 bank_mask:0xf\n" \
"v_pk_mul_f32 v[72:73], v[72:73], s[70:71]\n" \
"v_pk_mul_f32 v[74:75], v[74:75], s[70:71]\n" \
"v_mov_b32_dpp v34, v250 quad_perm:[0,1,2,3] row_mask:0x4 bank_mask:0xf\n" \
"v_add_f32_e64 v45, 1.0, v45\n" \
M_"v[68:71], a[176:179], a[132:135], v[68:71]\n" \
"v_mov_b32_dpp v34, v130 quad_perm:[0,1,2,3] row_mask:0x8 bank_mask:0xf\n" \
"v_mov_b32_dpp v32, v36 quad_perm:[0,1,2,3] row_mask:0x2 bank_mask:0xf\n" \
"v_rcp_f32_e64 v45, v45\n" \
"ds_write_b128 v27, v[76:79] offset:5120\n" \
"v_mov_b32_dpp v33, v37 quad_perm:[0,1,2,3] row_mask:0x2 bank_mask:0xf\n" \
"v_rcp_f32_e64 v34, v34\n" \
"v_mov_b32_dpp v32, v248 quad_perm:[0,1,2,3] row_mask:0x4 bank_mask:0xf\n" \
"v_mov_b32_dpp v33, v249 quad_perm:[0,1,2,3] row_mask:0x4 bank_mask:0xf\n" \
"v_mul_f32_e64 v34, v45, v34\n" \
"v_mov_b32_dpp v32, v128 quad_perm:[0,1,2,3] row_mask:0x8 bank_mask:0xf\n" \
"v_mov_b32_dpp v33, v129 quad_perm:[0,1,2,3] row_mask:0x8 bank_mask:0xf\n" \
"v_mul_f32_e32 v32, v34, v32\n" \
"v_mul_f32_e32 v33, v34, v33\n" \
"v_mov_b32_e64 v38, v34\n" \
M_"v[68:71], a[240:243], a[196:199], v[68:71]\n" \
"v_sub_f32_e32 v44, 1.0, v45\n" \
"v_permlane16_swap_b32_e32 v34, v38\n" \
"v_mov_b32_e32 v250, v34\n" \
"v_mov_b32_e32 v130, v38\n" \
C_"v32, v32, v33\n" \
"v_permlane32_swap_b32_e32 v34, v250\n" \
"v_permlane32_swap_b32_e32 v38, v130\n" \
"v_mul_f32_e64 v192, v34, v192\n" \
"v_pk_mul_f32 v[68:69], v[68:69], s[70:71]\n" \
"v_pk_mul_f32 v[70:71], v[70:71], s[70:71]\n" \
C_"v68, v68, v69\n" \
C_"v69, v70, v71\n" \
C_"v70, v72, v73\n" \
C_"v71, v74, v75\n" \
"ds_write_b128 v27, v[68:71] offset:4096\n" \
"v_mul_f32_e32 v193, v34, v193\n" \
"v_mul_f32_e32 v208, v38, v208\n" \
"v_mul_f32_e32 v209, v38, v209\n" \
"v_mul_f32_e32 v224, v250, v224\n" \
"v_mul_f32_e32 v225, v250, v225\n" \
"v_mul_f32_e32 v240, v130, v240\n" \
"v_mul_f32_e32 v241, v130, v241\n" \
"v_mul_f32_e32 v194, v34, v194\n" \
"v_mul_f32_e64 v195, v34, v195\n" \
C_"v192, v192, v193\n" \
"v_mul_f32_e32 v210, v38, v210\n" \
"v_mul_f32_e32 v211, v38, v211\n" \
C_"v208, v208, v209\n" \
"v_mul_f32_e32 v226, v250, v226\n" \
"v_mul_f32_e32 v227, v250, v227\n" \
C_"v224, v224, v225\n" \
"v_mul_f32_e32 v242, v130, v242\n" \
"v_mul_f32_e32 v243, v130, v243\n" \
C_"v240, v240, v241\n" \
C_"v193, v194, v195\n" \
"s_add_u32 s84, s48, 0x0\n" \
"s_nop 0\n" \
C_"v209, v210, v211\n" \
"s_add_u32 s85, s48, 0x24800\n" \
C_"v225, v226, v227\n" \
"s_add_u32 s86, s48, 0x49000\n" \
C_"v241, v242, v243\n" \
"s_add_u32 s87, s48, 0x6d800\n" \
"ds_write_b32 v26, v44\n" \
"buffer_store_dword v32, v25, s[80:83], s49 offen sc1\n" \
"buffer_store_dwordx2 v[192:193], v24, s[80:83], s84 offen sc1\n" \
"buffer_store_dwordx2 v[208:209], v24, s[80:83], s85 offen sc1\n" \
"buffer_store_dwordx2 v[224:225], v24, s[80:83], s86 offen sc1\n" \
"buffer_store_dwordx2 v[240:241], v24, s[80:83], s87 offen sc1\n" \
"s_branch L_end_%=\n" \
"L_poll61_%=:\n" \
"s_mov_b32 s84, 0\n" \
"L_pl61_%=:\n" \
"s_sleep 1\n" \
"v_bfe_u32 v44, v20, 2, 5\n" \
"buffer_load_dword v44, v44, s[80:83], s38 offen offset:128 sc1\n" \
"s_waitcnt vmcnt(0)\n" \
"v_cmp_ne_u32_e32 vcc, s62, v44\n" \
"s_add_u32 s84, s84, 1\n" \
"s_nop 3\n" \
"s_cbranch_vccz L_pollr61_%=\n" \
"s_cmp_gt_u32 s84, 0x400000\n" \
"s_cbranch_scc0 L_pl61_%=\n" \
"s_branch L_pollr61_%=\n" \
"L_poll161_%=:\n" \
"s_mov_b32 s84, 0\n" \
"L_pl161_%=:\n" \
"s_sleep 1\n" \
"v_bfe_u32 v44, v20, 2, 5\n" \
"buffer_load_dword v44, v44, s[80:83], s38 offen offset:0 sc1\n" \
"s_waitcnt vmcnt(0)\n" \
"v_cmp_ne_u32_e32 vcc, s62, v44\n" \
"s_add_u32 s84, s84, 1\n" \
"s_nop 3\n" \
"s_cbranch_vccz L_pollr161_%=\n" \
"s_cmp_gt_u32 s84, 0x400000\n" \
"s_cbranch_scc0 L_pl161_%=\n" \
"s_branch L_pollr161_%=\n" \
"L_end_%=:\n"
#define PROJ_PAGES 20
#define PROJ_VCLOB "v21","v24","v25","v28","v29","v32","v33","v34","v35","v36","v37","v38","v39","v40","v41","v42","v43","v44","v45","v46","v47","v48","v49","v50","v51","v52","v53","v54","v55","v56","v57","v58","v59","v60","v61","v62","v63","v64","v65","v66","v67","v68","v69","v70","v71","v72","v73","v74","v75","v76","v77","v78","v79","v80","v81","v82","v83","v84","v85","v86","v87","v88","v89","v90","v91","v92","v93","v94","v95","v96","v97","v98","v99","v100","v101","v102","v103","v104","v105","v106","v107","v108","v109","v110","v111","v112","v113","v114","v115","v116","v117","v118","v119","v120","v121","v122","v123","v124","v125","v126","v127","v128","v129","v130","v131","v132","v133","v134","v135","v136","v137","v138","v139","v140","v141","v142","v143","v144","v145","v146","v147","v148","v149","v150","v151","v152","v153","v154","v155","v156","v157","v158","v159","v160","v161","v162","v163","v164","v165","v166","v167","v168","v169","v170","v171","v172","v173","v174","v175","v176","v177","v178","v179","v180","v181","v182","v183","v184","v185","v186","v187","v188","v189","v190","v192","v193","v194","v195","v196","v197","v198","v199","v200","v201","v202","v203","v204","v205","v206","v207","v208","v209","v210","v211","v212","v213","v214","v215","v216","v217","v218","v219","v220","v221","v222","v223","v224","v225","v226","v227","v228","v229","v230","v231","v232","v233","v234","v235","v236","v237","v238","v239","v240","v241","v242","v243","v244","v245","v246","v247","v248","v249","v250","v251","v252","v253","v254","v255"
#define PROJ_SCLOB "s4","s5","s6","s7","s32","s33","s34","s35","s36","s37","s38","s39","s40","s41","s42","s43","s44","s45","s46","s47","s48","s49","s50","s51","s52","s53","s54","s55","s63","s64","s65","s66","s67","s68","s69","s70","s71","s72","s73","s74","s75","s76","s77","s78","s79","s80","s81","s82","s83","s84","s85","s86","s87","s88","s89","s90","s91","s92","s93","s94","s95","s96","s97","s98","s99","s100","s101"

typedef const u16* const __attribute__((address_space(4)))* kaptr;
__device__ __forceinline__ void proj_phase(kaptr& ka, char*& ws, const unsigned wid, u32x4 (&vp)[5], unsigned& vl, u32x4 (&sa)[5], u32x2 s24, u32x2& s28, unsigned tdum, u32x4& srdA, u32x2& sd, unsigned& tag
    ) {
    const unsigned b = blockIdx.x, h = (b & 7) * 4 + ((b >> 3) & 3), mt = b >> 5;
    const unsigned rb = (wid < 2u ? 15u - mt : mt) * 128u;
    const unsigned lane = threadIdx.x & 63u, wc = wid & 1u;
    u32x2 vAc, vB;
#pragma unroll
    for (int par = 0; par < 2; ++par) {
        const unsigned lr = lane >> 3, cc = (lane & 7u) ^ (4u * par + (lr >> 1)), r0 = rb + wc * 64u + lr;
        vAc[par] = r0 * (DC * 2) + cc * 16u;
        const unsigned br = lane >> 4, p16 = lane & 15u, sc = (((p16 >> 1) ^ (br | (par << 2))) << 1) | (p16 & 1u);
        vB[par] = (16u * wid + br) * (NBIG * 2) + h * (HD * 2) + sc * 16u;
    }
    vl = lane * 16u;
    u32x2 s30 = {wid * 8192u, 65536u + wid * 4096u};
    asm volatile(PROJ_ASM
                 : "={s62}"(tag), "={s[56:59]}"(srdA), "={s[60:61]}"(sd), "+{s[8:11]}"(sa[0]), "+{s[12:15]}"(sa[1]), "+{s[16:19]}"(sa[2]), "+{s[20:23]}"(sa[3]), "+{s[24:25]}"(s24), "+{s[28:29]}"(s28), "+{s[30:31]}"(s30),
                   "={v[0:3]}"(vp[0]), "={v[4:7]}"(vp[1]), "={v[8:11]}"(vp[2]), "={v[12:15]}"(vp[3]), "={v[16:19]}"(vp[4]), "+{v[22:23]}"(vAc), "+{v[26:27]}"(vB), "+{v191}"(tdum), "+{s[26:27]}"(ws), "+{s[0:1]}"(ka)
                 : "{s2}"(b), "{s3}"(wid), "{v20}"(vl)
                 : "memory", "m0", "vcc", "scc", PROJ_VCLOB, PROJ_SCLOB, ACLOB_LO, ACLOB_HI);
    sa[4] = (u32x4){s24[0], 0u, 0u, 0u};
}

namespace att {
#define DI __device__ __forceinline__
typedef __bf16 bfx8 __attribute__((ext_vector_type(8)));
DI unsigned rfl(unsigned x) { return __builtin_amdgcn_readfirstlane(x); }
DI f32x4 mfma(u32x4 a, u32x4 b, f32x4 c) { return __builtin_amdgcn_mfma_f32_16x16x32_bf16(__builtin_bit_cast(bfx8, a), __builtin_bit_cast(bfx8, b), c, 0, 0, 0); }
typedef __bf16 bfx2 __attribute__((ext_vector_type(2)));
typedef float f32x2 __attribute__((ext_vector_type(2)));
DI unsigned cvtpk2(float lo, float hi) { f32x2 v = {lo, hi}; bfx2 b = __builtin_convertvector(v, bfx2); return __builtin_bit_cast(unsigned, b); }
DI void att_exact(const u16* __restrict__ x, const u16* __restrict__ wq, char* __restrict__ ws, unsigned h, unsigned r0, float g1) {
    const unsigned lane = lane_id(), g = lane >> 4, c = lane & 15u;
    f32x4 o[8];
#pragma unroll
    for (int n = 0; n < 8; ++n) o[n] = (f32x4){0.f, 0.f, 0.f, 0.f};
#pragma unroll 1
    for (unsigned kk = 0; kk < (unsigned)DM; kk += 32u) {
        const unsigned k = kk + 8u * g;
        const u32x4 xf = *reinterpret_cast<const u32x4*>(x + (r0 + c) * DM + k);
#pragma unroll
        for (int n = 0; n < 8; ++n) {
            const u16* w = wq + k * NBIG + h * HD + n * 16 + c; u32x4 wf;
#pragma unroll
            for (int e = 0; e < 4; ++e) wf[e] = (unsigned)w[2 * e * NBIG] | ((unsigned)w[(2 * e + 1) * NBIG] << 16);
            o[n] = mfma(wf, xf, o[n]);
        }
    }
    u32x4 q[4];
#pragma unroll
    for (int p = 0; p < 4; ++p) {
        const f32x4 a = o[2 * p] * 0.12751743082f, b = o[2 * p + 1] * 0.12751743082f;
        q[p] = (u32x4){cvtpk2(a[0], a[1]), cvtpk2(a[2], a[3]), cvtpk2(b[0], b[1]), cvtpk2(b[2], b[3])};
    }
#pragma unroll
    for (int n = 0; n < 8; ++n) o[n] = (f32x4){0.f, 0.f, 0.f, 0.f};
    const __amdgpu_buffer_rsrc_t srd = __builtin_amdgcn_make_buffer_rsrc((void*)(ws + WS_K + (size_t)h * 1048576), (short)0, 1048576, 0x00020000);
    float m = -3.0e38f, l = 0.f;
#pragma unroll 1
    for (unsigned k0 = 0; k0 <= r0 + 15u; k0 += 32u) {
        const unsigned tb = (k0 >> 6) * 32768u + (k0 & 32u) * 256u;
        f32x4 s[2]; float mx = m;
#pragma unroll
        for (int kt = 0; kt < 2; ++kt) {
            s[kt] = (f32x4){0.f, 0.f, 0.f, 0.f};
#pragma unroll
            for (int p = 0; p < 4; ++p) s[kt] = mfma(__builtin_amdgcn_raw_buffer_load_b128(srd, lane * 16u + (kt * 4 + p) * 1024u, tb, 16), q[p], s[kt]);
#pragma unroll
            for (int e = 0; e < 4; ++e) { if (k0 + 16u * kt + 4u * g + e > r0 + c) s[kt][e] = -3.0e38f; mx = fmaxf(mx, s[kt][e]); }
        }
        mx = fmaxf(mx, __shfl_xor(mx, 16)); mx = fmaxf(mx, __shfl_xor(mx, 32));
        const float al = __builtin_amdgcn_exp2f(m - mx); m = mx;
        u32x4 pf;
#pragma unroll
        for (int e = 0; e < 4; ++e) pf[e] = cvtpk2(__builtin_amdgcn_exp2f(s[e >> 1][2 * (e & 1)] - m), __builtin_amdgcn_exp2f(s[e >> 1][2 * (e & 1) + 1] - m));
        l *= al;
#pragma unroll
        for (int e = 0; e < 4; ++e) l += __uint_as_float(pf[e] << 16) + __uint_as_float(pf[e] & 0xffff0000u);
#pragma unroll
        for (int n = 0; n < 8; ++n) o[n] = mfma(__builtin_amdgcn_raw_buffer_load_b128(srd, lane * 16u + 16384u + n * 1024u, tb, 16), pf, o[n] * al);
    }
    l += __shfl_xor(l, 16); l += __shfl_xor(l, 32);
    const float f = g1 / l;
    const __amdgpu_buffer_rsrc_t srdA = __builtin_amdgcn_make_buffer_rsrc((void*)(ws + WS_AF), (short)0, 128 * KS_TOT * 1024, 0x00020000);
#pragma unroll
    for (int p = 0; p < 4; ++p) {
        const f32x4 a = o[2 * p] * f, b = o[2 * p + 1] * f;
        __builtin_amdgcn_raw_buffer_store_b128((u32x4){cvtpk2(a[0], a[1]), cvtpk2(a[2], a[3]), cvtpk2(b[0], b[1]), cvtpk2(b[2], b[3])}, srdA, lane * 16u + p * 1024u, ((r0 >> 4) * KS_TOT + 18u + 4u * h) * 1024u, 16);
    }
}
}

#define ATT_ASM \
"s_branch L_ago_%=\n" \
".p2align 12\n" \
"L_ab:\n" \
".fill (L_ab + 0 - .) / 4, 4, 0xBF800000\n" \
"L_ap0:\n" \
"s_setpc_b64 s[88:89]\n" \
"L_ago_%=:\n" \
"s_mov_b32 s101, 0\n" \
"s_waitcnt vmcnt(15) lgkmcnt(0)\n" \
"s_barrier\n" \
"v_mfma_f32_32x32x16_bf16 v[128:143], a[120:123], a[120:123], 0\n" \
"v_and_b32_e32 v63, 0x300, v20\n" \
R_"v[96:99], v63 offset:0\n" \
"v_bfe_u32 v61, v20, 2, 5\n" \
"s_mov_b32 s96, s24\n" \
"v_add_u32_e32 v58, s22, v20\n" \
R_"a[0:3], v58 offset:0\n" \
"v_mfma_f32_32x32x16_bf16 v[144:159], a[120:123], a[120:123], 0\n" \
"buffer_load_dword v57, v61, s[56:59], s96 offen sc1\n" \
R_"a[16:19], v58 offset:8192\n" \
"v_add_u32_e32 v59, s23, v20\n" \
R_"a[32:35], v59 offset:0\n" \
R_"a[48:51], v59 offset:8192\n" \
"v_mfma_f32_32x32x16_bf16 v[160:175], a[120:123], a[120:123], 0\n" \
R_"v[100:103], v63 offset:1024\n" \
R_"a[4:7], v58 offset:1024\n" \
R_"a[20:23], v58 offset:9216\n" \
"s_sub_u32 s67, s12, 0xc000\n" \
"s_mov_b32 s65, s12\n" \
"v_add_u32_e32 v55, s67, v20\n" \
"v_mfma_f32_32x32x16_bf16 v[176:191], a[120:123], a[120:123], 0\n" \
"v_add_u32_e32 v52, 0xc000, v55\n" \
"s_xor_b32 m0, s65, 0x10000\n" \
R_"a[36:39], v59 offset:1024\n" \
B_"v52, s[8:11], 0 offen offset:0 sc1 lds\n" \
R_"a[52:55], v59 offset:9216\n" \
"v_mfma_f32_32x32x16_bf16 v[192:207], a[120:123], a[120:123], 0\n" \
R_"v[104:107], v63 offset:2048\n" \
R_"a[8:11], v58 offset:2048\n" \
R_"a[24:27], v58 offset:10240\n" \
B_"v52, s[8:11], 0 offen offset:1024 sc1 lds\n" \
R_"a[40:43], v59 offset:2048\n" \
"v_mfma_f32_32x32x16_bf16 v[208:223], a[120:123], a[120:123], 0\n" \
R_"a[56:59], v59 offset:10240\n" \
R_"v[108:111], v63 offset:3072\n" \
R_"a[12:15], v58 offset:3072\n" \
R_"a[28:31], v58 offset:11264\n" \
"s_waitcnt lgkmcnt(15)\n" \
"v_xor_b32_e32 v96, 0x80008000, v96\n" \
"v_mfma_f32_32x32x16_bf16 v[224:239], a[120:123], a[120:123], 0\n" \
"v_xor_b32_e32 v97, 0x80008000, v97\n" \
"v_xor_b32_e32 v98, 0x80008000, v98\n" \
"v_xor_b32_e32 v99, 0x80008000, v99\n" \
R_"a[44:47], v59 offset:3072\n" \
R_"a[60:63], v59 offset:11264\n" \
R_"a[160:163], v20 offset:0\n" \
"v_mfma_f32_32x32x16_bf16 v[240:255], a[120:123], a[120:123], 0\n" \
R_"a[164:167], v20 offset:1024\n" \
"s_waitcnt lgkmcnt(15)\n" \
"v_xor_b32_e32 v100, 0x80008000, v100\n" \
"v_xor_b32_e32 v101, 0x80008000, v101\n" \
"v_xor_b32_e32 v102, 0x80008000, v102\n" \
"v_xor_b32_e32 v103, 0x80008000, v103\n" \
M_"v[32:35], v[96:99], a[0:3], 0\n" \
R_"a[168:171], v20 offset:2048\n" \
R_"a[172:175], v20 offset:3072\n" \
M_"v[36:39], v[96:99], a[16:19], 0\n" \
R_"a[188:191], v20 offset:7168\n" \
R_"a[184:187], v20 offset:6144\n" \
M_"v[40:43], v[96:99], a[32:35], 0\n" \
"s_waitcnt lgkmcnt(15)\n" \
"v_xor_b32_e32 v104, 0x80008000, v104\n" \
M_"v[44:47], v[96:99], a[48:51], 0\n" \
"v_xor_b32_e32 v105, 0x80008000, v105\n" \
"v_xor_b32_e32 v106, 0x80008000, v106\n" \
M_"v[32:35], v[100:103], a[4:7], v[32:35]\n" \
"v_xor_b32_e32 v107, 0x80008000, v107\n" \
R_"a[180:183], v20 offset:5120\n" \
M_"v[36:39], v[100:103], a[20:23], v[36:39]\n" \
R_"a[176:179], v20 offset:4096\n" \
"s_waitcnt lgkmcnt(12)\n" \
"v_xor_b32_e32 v108, 0x80008000, v108\n" \
M_"v[40:43], v[100:103], a[36:39], v[40:43]\n" \
"v_xor_b32_e32 v109, 0x80008000, v109\n" \
"v_xor_b32_e32 v110, 0x80008000, v110\n" \
M_"v[44:47], v[100:103], a[52:55], v[44:47]\n" \
"v_xor_b32_e32 v111, 0x80008000, v111\n" \
"v_add_u32_e32 v60, s15, v20\n" \
M_"v[32:35], v[104:107], a[8:11], v[32:35]\n" \
"v_add_u32_e32 v60, 0x10000, v60\n" \
R_"a[128:131], v20 offset:8192\n" \
M_"v[36:39], v[104:107], a[24:27], v[36:39]\n" \
R_"a[144:147], v20 offset:12288\n" \
R_"a[132:135], v20 offset:9216\n" \
M_"v[40:43], v[104:107], a[40:43], v[40:43]\n" \
R_"a[148:151], v20 offset:13312\n" \
R_"a[136:139], v20 offset:10240\n" \
M_"v[44:47], v[104:107], a[56:59], v[44:47]\n" \
R_"a[152:155], v20 offset:14336\n" \
"s_waitcnt lgkmcnt(13)\n" \
M_"v[32:35], v[108:111], a[12:15], v[32:35]\n" \
R_"a[140:143], v20 offset:11264\n" \
R_"a[156:159], v20 offset:15360\n" \
M_"v[36:39], v[108:111], a[28:31], v[36:39]\n" \
R_"a[64:67], v60 offset:16384\n" \
R_"a[68:71], v60 offset:17408\n" \
M_"v[40:43], v[108:111], a[44:47], v[40:43]\n" \
R_"a[72:75], v60 offset:18432\n" \
R_"a[76:79], v60 offset:19456\n" \
M_"v[44:47], v[108:111], a[60:63], v[44:47]\n" \
R_"a[80:83], v60 offset:24576\n" \
R_"a[84:87], v60 offset:25600\n" \
M_"v[64:67], a[160:163], a[0:3], v[32:35]\n" \
R_"a[88:91], v60 offset:26624\n" \
"s_waitcnt lgkmcnt(15)\n" \
M_"v[64:67], a[164:167], a[4:7], v[64:67]\n" \
R_"a[92:95], v60 offset:27648\n" \
R_"a[192:195], v60 offset:0\n" \
M_"v[64:67], a[168:171], a[8:11], v[64:67]\n" \
R_"a[196:199], v60 offset:1024\n" \
R_"a[200:203], v60 offset:2048\n" \
M_"v[64:67], a[172:175], a[12:15], v[64:67]\n" \
R_"a[204:207], v60 offset:3072\n" \
R_"a[208:211], v60 offset:8192\n" \
M_"v[68:71], a[188:191], a[12:15], v[32:35]\n" \
R_"a[212:215], v60 offset:9216\n" \
R_"a[216:219], v60 offset:10240\n" \
M_"v[68:71], a[184:187], a[8:11], v[68:71]\n" \
R_"a[220:223], v60 offset:11264\n" \
"v_mov_b32_e32 v56, s62\n" \
M_"v[68:71], a[180:183], a[4:7], v[68:71]\n" \
"v_readlane_b32 s68, v11, 0\n" \
"s_mov_b32 m0, s20\n" \
M_"v[68:71], a[176:179], a[0:3], v[68:71]\n" \
"global_load_lds_dword v[16:17], off\n" \
M_"v[76:79], a[176:179], a[16:19], v[36:39]\n" \
E_"v66, v66\n" \
M_"v[76:79], a[180:183], a[20:23], v[76:79]\n" \
E_"v67, v67\n" \
M_"v[76:79], a[184:187], a[24:27], v[76:79]\n" \
E_"v64, v64\n" \
M_"v[76:79], a[188:191], a[28:31], v[76:79]\n" \
E_"v70, v70\n" \
M_"v[72:75], a[172:175], a[28:31], v[36:39]\n" \
E_"v71, v71\n" \
M_"v[72:75], a[168:171], a[24:27], v[72:75]\n" \
E_"v68, v68\n" \
M_"v[72:75], a[164:167], a[20:23], v[72:75]\n" \
E_"v69, v69\n" \
M_"v[72:75], a[160:163], a[16:19], v[72:75]\n" \
C_"v71, v70, v71\n" \
"v_accvgpr_read_b32 v28, a124\n" \
M_"v[80:83], a[160:163], a[32:35], v[40:43]\n" \
E_"v65, v65\n" \
M_"v[80:83], a[164:167], a[36:39], v[80:83]\n" \
E_"v76, v76\n" \
M_"v[80:83], a[168:171], a[40:43], v[80:83]\n" \
E_"v77, v77\n" \
M_"v[80:83], a[172:175], a[44:47], v[80:83]\n" \
E_"v72, v72\n" \
M_"v[84:87], a[188:191], a[44:47], v[40:43]\n" \
E_"v73, v73\n" \
M_"v[84:87], a[184:187], a[40:43], v[84:87]\n" \
E_"v74, v74\n" \
M_"v[84:87], a[180:183], a[36:39], v[84:87]\n" \
E_"v75, v75\n" \
M_"v[84:87], a[176:179], a[32:35], v[84:87]\n" \
C_"v72, v72, v73\n" \
"v_accvgpr_read_b32 v29, a125\n" \
M_"v[92:95], a[176:179], a[48:51], v[44:47]\n" \
E_"v78, v78\n" \
M_"v[92:95], a[180:183], a[52:55], v[92:95]\n" \
"s_waitcnt vmcnt(3)\n" \
"v_cmp_ne_u32_e32 vcc, s62, v57\n" \
M_"v[92:95], a[184:187], a[56:59], v[92:95]\n" \
E_"v79, v79\n" \
M_"v[92:95], a[188:191], a[60:63], v[92:95]\n" \
"v_accvgpr_read_b32 v21, a126\n" \
C_"v70, v68, v69\n" \
M_"v[88:91], a[172:175], a[60:63], v[44:47]\n" \
"s_cbranch_vccnz L_skb_%=\n" \
"L_rkb_%=:\n" \
C_"v73, v74, v75\n" \
"s_mov_b64 exec, 1\n" \
"buffer_store_dword v56, off, s[56:59], s68 offset:0 sc1\n" \
"s_mov_b64 exec, -1\n" \
M_"v[88:91], a[168:171], a[56:59], v[88:91]\n" \
"v_mov_b32_e32 v62, 0\n" \
"s_mov_b32 s64, 0x8000\n" \
M_"v[88:91], a[164:167], a[52:55], v[88:91]\n" \
"v_add_u32_e32 v50, 0x6000, v55\n" \
"v_add_u32_e32 v51, 0xa000, v55\n" \
M_"v[88:91], a[160:163], a[48:51], v[88:91]\n" \
"v_add_u32_e32 v53, 0x10000, v55\n" \
"v_add_u32_e32 v48, 0xc000, v20\n" \
"s_waitcnt vmcnt(5) lgkmcnt(0)\n" \
"s_barrier\n" \
"v_add_u32_e32 v49, s17, v20\n" \
"v_accvgpr_write_b32 a124, v96\n" \
"v_accvgpr_write_b32 a125, v97\n" \
"v_accvgpr_write_b32 a126, v98\n" \
"v_accvgpr_write_b32 a127, v99\n" \
"v_accvgpr_write_b32 a224, v100\n" \
"v_accvgpr_write_b32 a225, v101\n" \
"v_accvgpr_write_b32 a226, v102\n" \
"v_accvgpr_write_b32 a227, v103\n" \
"v_accvgpr_write_b32 a228, v104\n" \
"v_accvgpr_write_b32 a229, v105\n" \
"v_accvgpr_write_b32 a230, v106\n" \
"v_accvgpr_write_b32 a231, v107\n" \
"v_accvgpr_write_b32 a232, v108\n" \
"v_accvgpr_write_b32 a233, v109\n" \
"v_accvgpr_write_b32 a234, v110\n" \
"v_accvgpr_write_b32 a235, v111\n" \
C_"v69, v66, v67\n" \
C_"v68, v64, v65\n" \
C_"v74, v76, v77\n" \
C_"v75, v78, v79\n" \
"s_sub_u32 s66, s18, 1\n" \
"s_cbranch_scc1 L_tr_%=\n" \
"s_branch L_1_%=\n" \
".fill (L_ab + 1528 - .) / 4, 4, 0xBF800000\n" \
"L_tL_1_%=:\n" \
"s_waitcnt vmcnt(4) lgkmcnt(0)\n" \
"v_smfmac_f32_16x16x64_bf16 a[100:103], a[104:107], v[116:123], v21\n" \
"s_barrier\n" \
"L_1_%=:\n" \
M_"v[96:99], a[128:131], a[0:3], v[32:35]\n" \
"s_xor_b32 m0, s65, 0x12000\n" \
R_"a[160:163], v49 offset:0\n" \
M_"v[96:99], a[132:135], a[4:7], v[96:99]\n" \
B_"v50, s[8:11], s64 offen offset:0 sc1 lds\n" \
M_"v[96:99], a[136:139], a[8:11], v[96:99]\n" \
R_"a[164:167], v49 offset:1024\n" \
R_"a[168:171], v49 offset:2048\n" \
M_"v[96:99], a[140:143], a[12:15], v[96:99]\n" \
R_"a[172:175], v49 offset:3072\n" \
R_"a[176:179], v49 offset:4096\n" \
M_"v[100:103], a[156:159], a[12:15], v[32:35]\n" \
R_"a[180:183], v49 offset:5120\n" \
R_"a[184:187], v49 offset:6144\n" \
M_"v[100:103], a[152:155], a[8:11], v[100:103]\n" \
"v_exp_f32_e64 v86, v86\n" \
M_"v[100:103], a[148:151], a[4:7], v[100:103]\n" \
"v_exp_f32_e64 v87, v87\n" \
M_"v[100:103], a[144:147], a[0:3], v[100:103]\n" \
R_"a[188:191], v49 offset:7168\n" \
C_"v87, v86, v87\n" \
M_"v[108:111], a[144:147], a[16:19], v[36:39]\n" \
"v_exp_f32_e64 v84, v84\n" \
M_"v[108:111], a[148:151], a[20:23], v[108:111]\n" \
"v_exp_f32_e64 v85, v85\n" \
M_"v[108:111], a[152:155], a[24:27], v[108:111]\n" \
"v_exp_f32_e64 v82, v82\n" \
M_"v[108:111], a[156:159], a[28:31], v[108:111]\n" \
"v_exp_f32_e64 v83, v83\n" \
M_"v[104:107], a[140:143], a[28:31], v[36:39]\n" \
"v_exp_f32_e64 v80, v80\n" \
M_"v[104:107], a[136:139], a[24:27], v[104:107]\n" \
"v_exp_f32_e64 v81, v81\n" \
M_"v[104:107], a[132:135], a[20:23], v[104:107]\n" \
"v_exp_f32_e64 v88, v88\n" \
M_"v[104:107], a[128:131], a[16:19], v[104:107]\n" \
B_"v50, s[8:11], s64 offen offset:1024 sc1 lds\n" \
M_"v[112:115], a[128:131], a[32:35], v[40:43]\n" \
"s_xor_b32 m0, s65, 0x6000\n" \
C_"v86, v84, v85\n" \
M_"v[112:115], a[132:135], a[36:39], v[112:115]\n" \
C_"v85, v82, v83\n" \
C_"v84, v80, v81\n" \
M_"v[112:115], a[136:139], a[40:43], v[112:115]\n" \
"v_exp_f32_e64 v89, v89\n" \
M_"v[112:115], a[140:143], a[44:47], v[112:115]\n" \
"v_exp_f32_e64 v90, v90\n" \
M_"v[116:119], a[156:159], a[44:47], v[40:43]\n" \
"v_exp_f32_e64 v91, v91\n" \
M_"v[116:119], a[152:155], a[40:43], v[116:119]\n" \
"v_exp_f32_e64 v92, v92\n" \
M_"v[116:119], a[148:151], a[36:39], v[116:119]\n" \
"v_exp_f32_e64 v93, v93\n" \
M_"v[116:119], a[144:147], a[32:35], v[116:119]\n" \
"v_exp_f32_e64 v94, v94\n" \
M_"v[124:127], a[144:147], a[48:51], v[44:47]\n" \
C_"v88, v88, v89\n" \
C_"v89, v90, v91\n" \
M_"v[124:127], a[148:151], a[52:55], v[124:127]\n" \
"v_exp_f32_e64 v95, v95\n" \
M_"v[124:127], a[152:155], a[56:59], v[124:127]\n" \
C_"v90, v92, v93\n" \
C_"v91, v94, v95\n" \
M_"v[124:127], a[156:159], a[60:63], v[124:127]\n" \
R_"a[144:147], v48 offset:4096\n" \
R_"a[148:151], v48 offset:5120\n" \
M_"v[120:123], a[140:143], a[60:63], v[44:47]\n" \
R_"a[152:155], v48 offset:6144\n" \
R_"a[140:143], v48 offset:3072\n" \
M_"v[120:123], a[136:139], a[56:59], v[120:123]\n" \
R_"a[136:139], v48 offset:2048\n" \
R_"a[156:159], v48 offset:7168\n" \
M_"v[120:123], a[132:135], a[52:55], v[120:123]\n" \
B_"v51, s[8:11], s64 offen offset:0 sc1 lds\n" \
M_"v[120:123], a[128:131], a[48:51], v[120:123]\n" \
R_"a[128:131], v48 offset:0\n" \
"s_waitcnt lgkmcnt(7)\n" \
M_"v[128:131], a[160:163], v[68:71], v[128:131]\n" \
E_"v102, v102\n" \
M_"v[160:163], a[160:163], v[72:75], v[160:163]\n" \
"v_exp_f32_e64 v103, v103\n" \
M_"v[132:135], a[164:167], v[68:71], v[132:135]\n" \
R_"a[132:135], v48 offset:1024\n" \
C_"v103, v102, v103\n" \
M_"v[164:167], a[164:167], v[72:75], v[164:167]\n" \
"v_exp_f32_e64 v100, v100\n" \
M_"v[136:139], a[168:171], v[68:71], v[136:139]\n" \
"v_exp_f32_e64 v101, v101\n" \
M_"v[168:171], a[168:171], v[72:75], v[168:171]\n" \
"v_exp_f32_e64 v98, v98\n" \
M_"v[140:143], a[172:175], v[68:71], v[140:143]\n" \
"v_exp_f32_e64 v99, v99\n" \
M_"v[172:175], a[172:175], v[72:75], v[172:175]\n" \
"v_exp_f32_e64 v96, v96\n" \
M_"v[144:147], a[176:179], v[68:71], v[144:147]\n" \
"v_exp_f32_e64 v97, v97\n" \
M_"v[176:179], a[176:179], v[72:75], v[176:179]\n" \
"v_exp_f32_e64 v104, v104\n" \
M_"v[148:151], a[180:183], v[68:71], v[148:151]\n" \
"v_exp_f32_e64 v105, v105\n" \
M_"v[180:183], a[180:183], v[72:75], v[180:183]\n" \
C_"v102, v100, v101\n" \
C_"v101, v98, v99\n" \
M_"v[152:155], a[184:187], v[68:71], v[152:155]\n" \
C_"v100, v96, v97\n" \
C_"v104, v104, v105\n" \
M_"v[184:187], a[184:187], v[72:75], v[184:187]\n" \
B_"v51, s[8:11], s64 offen offset:1024 sc1 lds\n" \
M_"v[156:159], a[188:191], v[68:71], v[156:159]\n" \
"v_exp_f32_e64 v106, v106\n" \
M_"v[188:191], a[188:191], v[72:75], v[188:191]\n" \
"v_exp_f32_e64 v107, v107\n" \
"v_smfmac_f32_16x16x64_bf16 a[96:99], a[104:107], v[68:75], v21\n" \
"v_exp_f32_e64 v108, v108\n" \
M_"v[220:223], a[188:191], v[84:87], v[220:223]\n" \
"v_exp_f32_e64 v109, v109\n" \
M_"v[252:255], a[188:191], v[88:91], v[252:255]\n" \
"v_exp_f32_e64 v110, v110\n" \
M_"v[216:219], a[184:187], v[84:87], v[216:219]\n" \
"v_exp_f32_e64 v111, v111\n" \
M_"v[248:251], a[184:187], v[88:91], v[248:251]\n" \
"v_exp_f32_e64 v118, v118\n" \
M_"v[212:215], a[180:183], v[84:87], v[212:215]\n" \
"v_exp_f32_e64 v119, v119\n" \
M_"v[244:247], a[180:183], v[88:91], v[244:247]\n" \
C_"v105, v106, v107\n" \
C_"v106, v108, v109\n" \
M_"v[208:211], a[176:179], v[84:87], v[208:211]\n" \
C_"v107, v110, v111\n" \
C_"v119, v118, v119\n" \
M_"v[240:243], a[176:179], v[88:91], v[240:243]\n" \
"v_exp_f32_e64 v116, v116\n" \
M_"v[204:207], a[172:175], v[84:87], v[204:207]\n" \
"v_exp_f32_e64 v117, v117\n" \
M_"v[236:239], a[172:175], v[88:91], v[236:239]\n" \
"v_exp_f32_e64 v114, v114\n" \
M_"v[200:203], a[168:171], v[84:87], v[200:203]\n" \
"v_exp_f32_e64 v115, v115\n" \
M_"v[232:235], a[168:171], v[88:91], v[232:235]\n" \
"v_exp_f32_e64 v112, v112\n" \
M_"v[196:199], a[164:167], v[84:87], v[196:199]\n" \
"v_exp_f32_e64 v113, v113\n" \
M_"v[228:231], a[164:167], v[88:91], v[228:231]\n" \
"v_exp_f32_e64 v120, v120\n" \
M_"v[192:195], a[160:163], v[84:87], v[192:195]\n" \
"v_exp_f32_e64 v121, v121\n" \
M_"v[224:227], a[160:163], v[88:91], v[224:227]\n" \
C_"v118, v116, v117\n" \
"s_waitcnt vmcnt(4) lgkmcnt(0)\n" \
"v_smfmac_f32_16x16x64_bf16 a[100:103], a[104:107], v[84:91], v21\n" \
"s_barrier\n" \
M_"v[64:67], a[128:131], a[0:3], v[32:35]\n" \
"s_xor_b32 m0, s65, 0x14000\n" \
R_"a[160:163], v49 offset:8192\n" \
M_"v[64:67], a[132:135], a[4:7], v[64:67]\n" \
B_"v52, s[8:11], s64 offen offset:0 sc1 lds\n" \
M_"v[64:67], a[136:139], a[8:11], v[64:67]\n" \
R_"a[164:167], v49 offset:9216\n" \
R_"a[168:171], v49 offset:10240\n" \
M_"v[64:67], a[140:143], a[12:15], v[64:67]\n" \
R_"a[172:175], v49 offset:11264\n" \
R_"a[176:179], v49 offset:12288\n" \
M_"v[68:71], a[156:159], a[12:15], v[32:35]\n" \
R_"a[180:183], v49 offset:13312\n" \
R_"a[184:187], v49 offset:14336\n" \
M_"v[68:71], a[152:155], a[8:11], v[68:71]\n" \
R_"a[188:191], v49 offset:15360\n" \
C_"v117, v114, v115\n" \
M_"v[68:71], a[148:151], a[4:7], v[68:71]\n" \
C_"v116, v112, v113\n" \
C_"v120, v120, v121\n" \
M_"v[68:71], a[144:147], a[0:3], v[68:71]\n" \
"v_exp_f32_e64 v122, v122\n" \
M_"v[76:79], a[144:147], a[16:19], v[36:39]\n" \
"v_exp_f32_e64 v123, v123\n" \
M_"v[76:79], a[148:151], a[20:23], v[76:79]\n" \
"v_exp_f32_e64 v124, v124\n" \
M_"v[76:79], a[152:155], a[24:27], v[76:79]\n" \
"v_exp_f32_e64 v125, v125\n" \
M_"v[76:79], a[156:159], a[28:31], v[76:79]\n" \
"v_exp_f32_e64 v126, v126\n" \
M_"v[72:75], a[140:143], a[28:31], v[36:39]\n" \
"v_exp_f32_e64 v127, v127\n" \
M_"v[72:75], a[136:139], a[24:27], v[72:75]\n" \
C_"v121, v122, v123\n" \
C_"v122, v124, v125\n" \
M_"v[72:75], a[132:135], a[20:23], v[72:75]\n" \
C_"v123, v126, v127\n" \
"v_add_u32_e32 v49, 0x10000, v48\n" \
M_"v[72:75], a[128:131], a[16:19], v[72:75]\n" \
B_"v52, s[8:11], s64 offen offset:1024 sc1 lds\n" \
M_"v[80:83], a[128:131], a[32:35], v[40:43]\n" \
"v_exp_f32_e64 v70, v70\n" \
M_"v[80:83], a[132:135], a[36:39], v[80:83]\n" \
"v_exp_f32_e64 v71, v71\n" \
M_"v[80:83], a[136:139], a[40:43], v[80:83]\n" \
"s_mov_b32 m0, s65\n" \
C_"v71, v70, v71\n" \
M_"v[80:83], a[140:143], a[44:47], v[80:83]\n" \
E_"v72, v72\n" \
M_"v[84:87], a[156:159], a[44:47], v[40:43]\n" \
"v_exp_f32_e64 v73, v73\n" \
M_"v[84:87], a[152:155], a[40:43], v[84:87]\n" \
"v_exp_f32_e64 v68, v68\n" \
M_"v[84:87], a[148:151], a[36:39], v[84:87]\n" \
"v_exp_f32_e64 v69, v69\n" \
M_"v[84:87], a[144:147], a[32:35], v[84:87]\n" \
"v_exp_f32_e64 v74, v74\n" \
M_"v[92:95], a[144:147], a[48:51], v[44:47]\n" \
R_"a[144:147], v48 offset:12288\n" \
C_"v72, v72, v73\n" \
M_"v[92:95], a[148:151], a[52:55], v[92:95]\n" \
R_"a[148:151], v48 offset:13312\n" \
C_"v70, v68, v69\n" \
M_"v[92:95], a[152:155], a[56:59], v[92:95]\n" \
R_"a[152:155], v48 offset:14336\n" \
"s_xor_b32 s65, s65, 0x4000\n" \
M_"v[92:95], a[156:159], a[60:63], v[92:95]\n" \
"v_exp_f32_e64 v75, v75\n" \
M_"v[88:91], a[140:143], a[60:63], v[44:47]\n" \
R_"a[140:143], v48 offset:11264\n" \
R_"a[156:159], v48 offset:15360\n" \
M_"v[88:91], a[136:139], a[56:59], v[88:91]\n" \
R_"a[136:139], v48 offset:10240\n" \
C_"v73, v74, v75\n" \
M_"v[88:91], a[132:135], a[52:55], v[88:91]\n" \
B_"v53, s[8:11], s64 offen offset:0 sc1 lds\n" \
M_"v[88:91], a[128:131], a[48:51], v[88:91]\n" \
R_"a[128:131], v48 offset:8192\n" \
"s_waitcnt lgkmcnt(7)\n" \
M_"v[128:131], a[160:163], v[100:103], v[128:131]\n" \
E_"v66, v66\n" \
M_"v[160:163], a[160:163], v[104:107], v[160:163]\n" \
"v_exp_f32_e64 v67, v67\n" \
M_"v[132:135], a[164:167], v[100:103], v[132:135]\n" \
R_"a[132:135], v48 offset:9216\n" \
C_"v69, v66, v67\n" \
M_"v[164:167], a[164:167], v[104:107], v[164:167]\n" \
"v_exp_f32_e64 v64, v64\n" \
M_"v[136:139], a[168:171], v[100:103], v[136:139]\n" \
"v_exp_f32_e64 v65, v65\n" \
M_"v[168:171], a[168:171], v[104:107], v[168:171]\n" \
"v_exp_f32_e64 v76, v76\n" \
M_"v[140:143], a[172:175], v[100:103], v[140:143]\n" \
"v_exp_f32_e64 v77, v77\n" \
M_"v[172:175], a[172:175], v[104:107], v[172:175]\n" \
"v_exp_f32_e64 v78, v78\n" \
M_"v[144:147], a[176:179], v[100:103], v[144:147]\n" \
"v_exp_f32_e64 v79, v79\n" \
M_"v[176:179], a[176:179], v[104:107], v[176:179]\n" \
C_"v68, v64, v65\n" \
C_"v74, v76, v77\n" \
M_"v[148:151], a[180:183], v[100:103], v[148:151]\n" \
C_"v75, v78, v79\n" \
"v_xor_b32_e32 v48, 0x4000, v48\n" \
M_"v[180:183], a[180:183], v[104:107], v[180:183]\n" \
M_"v[152:155], a[184:187], v[100:103], v[152:155]\n" \
M_"v[184:187], a[184:187], v[104:107], v[184:187]\n" \
B_"v53, s[8:11], s64 offen offset:1024 sc1 lds\n" \
M_"v[156:159], a[188:191], v[100:103], v[156:159]\n" \
"s_add_u32 s64, s64, 0x8000\n" \
"s_sub_u32 s66, s66, 1\n" \
"s_nop 0\n" \
M_"v[188:191], a[188:191], v[104:107], v[188:191]\n" \
"v_smfmac_f32_16x16x64_bf16 a[96:99], a[104:107], v[100:107], v21\n" \
M_"v[220:223], a[188:191], v[116:119], v[220:223]\n" \
M_"v[252:255], a[188:191], v[120:123], v[252:255]\n" \
M_"v[216:219], a[184:187], v[116:119], v[216:219]\n" \
M_"v[248:251], a[184:187], v[120:123], v[248:251]\n" \
M_"v[212:215], a[180:183], v[116:119], v[212:215]\n" \
M_"v[244:247], a[180:183], v[120:123], v[244:247]\n" \
M_"v[208:211], a[176:179], v[116:119], v[208:211]\n" \
M_"v[240:243], a[176:179], v[120:123], v[240:243]\n" \
M_"v[204:207], a[172:175], v[116:119], v[204:207]\n" \
M_"v[236:239], a[172:175], v[120:123], v[236:239]\n" \
M_"v[200:203], a[168:171], v[116:119], v[200:203]\n" \
M_"v[232:235], a[168:171], v[120:123], v[232:235]\n" \
M_"v[196:199], a[164:167], v[116:119], v[196:199]\n" \
M_"v[228:231], a[164:167], v[120:123], v[228:231]\n" \
M_"v[192:195], a[160:163], v[116:119], v[192:195]\n" \
M_"v[224:227], a[160:163], v[120:123], v[224:227]\n" \
"s_cbranch_scc0 L_tL_1_%=\n" \
"s_waitcnt vmcnt(4) lgkmcnt(0)\n" \
"v_smfmac_f32_16x16x64_bf16 a[100:103], a[104:107], v[116:123], v21\n" \
"s_barrier\n" \
"L_tr_%=:\n" \
"s_setpc_b64 s[28:29]\n" \
".fill (L_ab + 4096 - .) / 4, 4, 0xBF800000\n" \
".fill (L_ab + 4096 - .) / 4, 4, 0xBF800000\n" \
"L_ap1:\n" \
"s_setpc_b64 s[88:89]\n" \
"L_gtr:\n" \
M_"v[96:99], a[128:131], a[0:3], v[32:35]\n" \
"s_xor_b32 m0, s65, 0x12000\n" \
"s_mov_b32 s65, s13\n" \
M_"v[96:99], a[132:135], a[4:7], v[96:99]\n" \
B_"v50, s[8:11], s64 offen offset:0 sc1 lds\n" \
M_"v[96:99], a[136:139], a[8:11], v[96:99]\n" \
"v_add_u32_e32 v63, s15, v20\n" \
"s_add_u32 s67, s14, 40960\n" \
M_"v[96:99], a[140:143], a[12:15], v[96:99]\n" \
R_"a[160:163], v49 offset:0\n" \
R_"a[164:167], v49 offset:1024\n" \
M_"v[100:103], a[156:159], a[12:15], v[32:35]\n" \
B_"v50, s[8:11], s64 offen offset:1024 sc1 lds\n" \
M_"v[100:103], a[152:155], a[8:11], v[100:103]\n" \
"v_add_u32_e32 v50, 0x6000, v63\n" \
"s_xor_b32 m0, s65, 0x8000\n" \
M_"v[100:103], a[148:151], a[4:7], v[100:103]\n" \
R_"a[168:171], v49 offset:2048\n" \
R_"a[172:175], v49 offset:3072\n" \
M_"v[100:103], a[144:147], a[0:3], v[100:103]\n" \
R_"a[176:179], v49 offset:4096\n" \
R_"a[180:183], v49 offset:5120\n" \
M_"v[108:111], a[144:147], a[16:19], v[36:39]\n" \
B_"v50, s[8:11], s67 offen offset:0 sc1 lds\n" \
M_"v[108:111], a[148:151], a[20:23], v[108:111]\n" \
R_"a[184:187], v49 offset:6144\n" \
R_"a[188:191], v49 offset:7168\n" \
M_"v[108:111], a[152:155], a[24:27], v[108:111]\n" \
"s_mov_b32 s64, s14\n" \
"v_add_u32_e32 v51, 0x12000, v63\n" \
M_"v[108:111], a[156:159], a[28:31], v[108:111]\n" \
E_"v86, v86\n" \
M_"v[104:107], a[140:143], a[28:31], v[36:39]\n" \
B_"v50, s[8:11], s67 offen offset:1024 sc1 lds\n" \
M_"v[104:107], a[136:139], a[24:27], v[104:107]\n" \
E_"v87, v87\n" \
M_"v[104:107], a[132:135], a[20:23], v[104:107]\n" \
E_"v84, v84\n" \
M_"v[104:107], a[128:131], a[16:19], v[104:107]\n" \
E_"v85, v85\n" \
M_"v[112:115], a[128:131], a[32:35], v[40:43]\n" \
C_"v87, v86, v87\n" \
C_"v86, v84, v85\n" \
M_"v[112:115], a[132:135], a[36:39], v[112:115]\n" \
B_"v50, s[8:11], s67 offen offset:2048 sc1 lds\n" \
M_"v[112:115], a[136:139], a[40:43], v[112:115]\n" \
E_"v82, v82\n" \
M_"v[112:115], a[140:143], a[44:47], v[112:115]\n" \
E_"v83, v83\n" \
M_"v[116:119], a[156:159], a[44:47], v[40:43]\n" \
E_"v80, v80\n" \
M_"v[116:119], a[152:155], a[40:43], v[116:119]\n" \
B_"v50, s[8:11], s67 offen offset:3072 sc1 lds\n" \
M_"v[116:119], a[148:151], a[36:39], v[116:119]\n" \
"s_xor_b32 m0, s65, 0xa000\n" \
C_"v85, v82, v83\n" \
M_"v[116:119], a[144:147], a[32:35], v[116:119]\n" \
E_"v81, v81\n" \
M_"v[124:127], a[144:147], a[48:51], v[44:47]\n" \
C_"v84, v80, v81\n" \
"v_readlane_b32 s93, v11, 22\n" \
M_"v[124:127], a[148:151], a[52:55], v[124:127]\n" \
B_"v51, s[8:11], s64 offen offset:0 sc1 lds\n" \
M_"v[124:127], a[152:155], a[56:59], v[124:127]\n" \
E_"v88, v88\n" \
M_"v[124:127], a[156:159], a[60:63], v[124:127]\n" \
E_"v89, v89\n" \
M_"v[120:123], a[140:143], a[60:63], v[44:47]\n" \
E_"v90, v90\n" \
M_"v[120:123], a[136:139], a[56:59], v[120:123]\n" \
E_"v91, v91\n" \
M_"v[120:123], a[132:135], a[52:55], v[120:123]\n" \
B_"v51, s[8:11], s64 offen offset:1024 sc1 lds\n" \
M_"v[120:123], a[128:131], a[48:51], v[120:123]\n" \
C_"v88, v88, v89\n" \
"s_waitcnt lgkmcnt(0)\n" \
M_"v[128:131], a[160:163], v[68:71], v[128:131]\n" \
E_"v92, v92\n" \
M_"v[160:163], a[160:163], v[72:75], v[160:163]\n" \
E_"v93, v93\n" \
M_"v[132:135], a[164:167], v[68:71], v[132:135]\n" \
B_"v51, s[8:11], s64 offen offset:2048 sc1 lds\n" \
M_"v[164:167], a[164:167], v[72:75], v[164:167]\n" \
E_"v94, v94\n" \
M_"v[136:139], a[168:171], v[68:71], v[136:139]\n" \
E_"v95, v95\n" \
M_"v[168:171], a[168:171], v[72:75], v[168:171]\n" \
C_"v89, v90, v91\n" \
C_"v90, v92, v93\n" \
M_"v[140:143], a[172:175], v[68:71], v[140:143]\n" \
B_"v51, s[8:11], s64 offen offset:3072 sc1 lds\n" \
M_"v[172:175], a[172:175], v[72:75], v[172:175]\n" \
C_"v91, v94, v95\n" \
"v_readlane_b32 s80, v11, 11\n" \
M_"v[144:147], a[176:179], v[68:71], v[144:147]\n" \
"v_readlane_b32 s81, v11, 12\n" \
"s_mov_b32 s82, 0x47fca0\n" \
M_"v[176:179], a[176:179], v[72:75], v[176:179]\n" \
"s_mov_b32 s83, 0x20000\n" \
"v_readlane_b32 s92, v11, 29\n" \
M_"v[148:151], a[180:183], v[68:71], v[148:151]\n" \
"s_add_u32 m0, s93, 0x0\n" \
R_"a[128:131], v48 offset:0\n" \
M_"v[180:183], a[180:183], v[72:75], v[180:183]\n" \
R_"a[144:147], v48 offset:4096\n" \
R_"a[132:135], v48 offset:1024\n" \
M_"v[152:155], a[184:187], v[68:71], v[152:155]\n" \
B_"v0, s[80:83], s92 offen offset:0 lds\n" \
M_"v[184:187], a[184:187], v[72:75], v[184:187]\n" \
R_"a[148:151], v48 offset:5120\n" \
R_"a[136:139], v48 offset:2048\n" \
M_"v[156:159], a[188:191], v[68:71], v[156:159]\n" \
R_"a[152:155], v48 offset:6144\n" \
R_"a[140:143], v48 offset:3072\n" \
M_"v[188:191], a[188:191], v[72:75], v[188:191]\n" \
R_"a[156:159], v48 offset:7168\n" \
"v_add_u32_e32 v52, 0x14000, v63\n" \
"v_smfmac_f32_16x16x64_bf16 a[96:99], a[104:107], v[68:75], v21\n" \
B_"v1, s[80:83], s92 offen offset:1024 lds\n" \
M_"v[220:223], a[188:191], v[84:87], v[220:223]\n" \
E_"v102, v102\n" \
M_"v[252:255], a[188:191], v[88:91], v[252:255]\n" \
E_"v103, v103\n" \
M_"v[216:219], a[184:187], v[84:87], v[216:219]\n" \
B_"v2, s[80:83], s92 offen offset:2048 lds\n" \
M_"v[248:251], a[184:187], v[88:91], v[248:251]\n" \
"v_add_u32_e32 v53, 0x20000, v63\n" \
C_"v103, v102, v103\n" \
M_"v[212:215], a[180:183], v[84:87], v[212:215]\n" \
E_"v100, v100\n" \
M_"v[244:247], a[180:183], v[88:91], v[244:247]\n" \
E_"v101, v101\n" \
M_"v[208:211], a[176:179], v[84:87], v[208:211]\n" \
B_"v3, s[80:83], s92 offen offset:3072 lds\n" \
M_"v[240:243], a[176:179], v[88:91], v[240:243]\n" \
E_"v98, v98\n" \
M_"v[204:207], a[172:175], v[84:87], v[204:207]\n" \
E_"v99, v99\n" \
M_"v[236:239], a[172:175], v[88:91], v[236:239]\n" \
E_"v96, v96\n" \
M_"v[200:203], a[168:171], v[84:87], v[200:203]\n" \
E_"v97, v97\n" \
M_"v[232:235], a[168:171], v[88:91], v[232:235]\n" \
E_"v104, v104\n" \
M_"v[196:199], a[164:167], v[84:87], v[196:199]\n" \
E_"v105, v105\n" \
M_"v[228:231], a[164:167], v[88:91], v[228:231]\n" \
C_"v102, v100, v101\n" \
C_"v101, v98, v99\n" \
M_"v[192:195], a[160:163], v[84:87], v[192:195]\n" \
C_"v100, v96, v97\n" \
C_"v104, v104, v105\n" \
M_"v[224:227], a[160:163], v[88:91], v[224:227]\n" \
E_"v106, v106\n" \
"v_smfmac_f32_16x16x64_bf16 a[100:103], a[104:107], v[84:91], v21\n" \
E_"v107, v107\n" \
"s_waitcnt vmcnt(14) lgkmcnt(0)\n" \
"s_barrier\n" \
M_"v[64:67], a[128:131], a[0:3], v[32:35]\n" \
"s_xor_b32 m0, s65, 0x18000\n" \
R_"a[160:163], v49 offset:8192\n" \
M_"v[64:67], a[132:135], a[4:7], v[64:67]\n" \
B_"v52, s[8:11], s64 offen offset:0 sc1 lds\n" \
M_"v[64:67], a[136:139], a[8:11], v[64:67]\n" \
R_"a[164:167], v49 offset:9216\n" \
R_"a[168:171], v49 offset:10240\n" \
M_"v[64:67], a[140:143], a[12:15], v[64:67]\n" \
R_"a[172:175], v49 offset:11264\n" \
R_"a[176:179], v49 offset:12288\n" \
M_"v[68:71], a[156:159], a[12:15], v[32:35]\n" \
B_"v52, s[8:11], s64 offen offset:1024 sc1 lds\n" \
M_"v[68:71], a[152:155], a[8:11], v[68:71]\n" \
R_"a[180:183], v49 offset:13312\n" \
R_"a[184:187], v49 offset:14336\n" \
M_"v[68:71], a[148:151], a[4:7], v[68:71]\n" \
E_"v108, v108\n" \
M_"v[68:71], a[144:147], a[0:3], v[68:71]\n" \
E_"v109, v109\n" \
M_"v[76:79], a[144:147], a[16:19], v[36:39]\n" \
E_"v110, v110\n" \
M_"v[76:79], a[148:151], a[20:23], v[76:79]\n" \
B_"v52, s[8:11], s64 offen offset:2048 sc1 lds\n" \
M_"v[76:79], a[152:155], a[24:27], v[76:79]\n" \
E_"v111, v111\n" \
M_"v[76:79], a[156:159], a[28:31], v[76:79]\n" \
C_"v105, v106, v107\n" \
C_"v106, v108, v109\n" \
M_"v[72:75], a[140:143], a[28:31], v[36:39]\n" \
C_"v107, v110, v111\n" \
R_"a[188:191], v49 offset:15360\n" \
M_"v[72:75], a[136:139], a[24:27], v[72:75]\n" \
B_"v52, s[8:11], s64 offen offset:3072 sc1 lds\n" \
M_"v[72:75], a[132:135], a[20:23], v[72:75]\n" \
"s_mov_b32 m0, s65\n" \
"v_add_u32_e32 v49, 0x10000, v48\n" \
M_"v[72:75], a[128:131], a[16:19], v[72:75]\n" \
E_"v118, v118\n" \
M_"v[80:83], a[128:131], a[32:35], v[40:43]\n" \
E_"v119, v119\n" \
M_"v[80:83], a[132:135], a[36:39], v[80:83]\n" \
E_"v116, v116\n" \
M_"v[80:83], a[136:139], a[40:43], v[80:83]\n" \
B_"v53, s[8:11], s64 offen offset:0 sc1 lds\n" \
M_"v[80:83], a[140:143], a[44:47], v[80:83]\n" \
E_"v117, v117\n" \
M_"v[84:87], a[156:159], a[44:47], v[40:43]\n" \
C_"v119, v118, v119\n" \
C_"v118, v116, v117\n" \
M_"v[84:87], a[152:155], a[40:43], v[84:87]\n" \
E_"v114, v114\n" \
M_"v[84:87], a[148:151], a[36:39], v[84:87]\n" \
E_"v115, v115\n" \
M_"v[84:87], a[144:147], a[32:35], v[84:87]\n" \
B_"v53, s[8:11], s64 offen offset:1024 sc1 lds\n" \
M_"v[92:95], a[144:147], a[48:51], v[44:47]\n" \
E_"v112, v112\n" \
M_"v[92:95], a[148:151], a[52:55], v[92:95]\n" \
E_"v113, v113\n" \
M_"v[92:95], a[152:155], a[56:59], v[92:95]\n" \
C_"v117, v114, v115\n" \
C_"v116, v112, v113\n" \
M_"v[92:95], a[156:159], a[60:63], v[92:95]\n" \
B_"v53, s[8:11], s64 offen offset:2048 sc1 lds\n" \
M_"v[88:91], a[140:143], a[60:63], v[44:47]\n" \
E_"v120, v120\n" \
M_"v[88:91], a[136:139], a[56:59], v[88:91]\n" \
E_"v121, v121\n" \
M_"v[88:91], a[132:135], a[52:55], v[88:91]\n" \
E_"v122, v122\n" \
M_"v[88:91], a[128:131], a[48:51], v[88:91]\n" \
C_"v120, v120, v121\n" \
"s_waitcnt lgkmcnt(0)\n" \
M_"v[128:131], a[160:163], v[100:103], v[128:131]\n" \
B_"v53, s[8:11], s64 offen offset:3072 sc1 lds\n" \
M_"v[160:163], a[160:163], v[104:107], v[160:163]\n" \
"s_add_u32 m0, s93, 0x1000\n" \
R_"a[128:131], v48 offset:8192\n" \
M_"v[132:135], a[164:167], v[100:103], v[132:135]\n" \
E_"v123, v123\n" \
M_"v[164:167], a[164:167], v[104:107], v[164:167]\n" \
E_"v124, v124\n" \
M_"v[136:139], a[168:171], v[100:103], v[136:139]\n" \
B_"v4, s[80:83], s92 offen offset:0 lds\n" \
M_"v[168:171], a[168:171], v[104:107], v[168:171]\n" \
E_"v125, v125\n" \
M_"v[140:143], a[172:175], v[100:103], v[140:143]\n" \
E_"v126, v126\n" \
M_"v[172:175], a[172:175], v[104:107], v[172:175]\n" \
E_"v127, v127\n" \
M_"v[144:147], a[176:179], v[100:103], v[144:147]\n" \
C_"v121, v122, v123\n" \
C_"v122, v124, v125\n" \
M_"v[176:179], a[176:179], v[104:107], v[176:179]\n" \
B_"v5, s[80:83], s92 offen offset:1024 lds\n" \
M_"v[148:151], a[180:183], v[100:103], v[148:151]\n" \
C_"v123, v126, v127\n" \
R_"a[144:147], v48 offset:12288\n" \
M_"v[180:183], a[180:183], v[104:107], v[180:183]\n" \
R_"a[132:135], v48 offset:9216\n" \
R_"a[148:151], v48 offset:13312\n" \
M_"v[152:155], a[184:187], v[100:103], v[152:155]\n" \
R_"a[136:139], v48 offset:10240\n" \
R_"a[152:155], v48 offset:14336\n" \
M_"v[184:187], a[184:187], v[104:107], v[184:187]\n" \
R_"a[140:143], v48 offset:11264\n" \
R_"a[156:159], v48 offset:15360\n" \
M_"v[156:159], a[188:191], v[100:103], v[156:159]\n" \
B_"v6, s[80:83], s92 offen offset:2048 lds\n" \
M_"v[188:191], a[188:191], v[104:107], v[188:191]\n" \
E_"v70, v70\n" \
"v_smfmac_f32_16x16x64_bf16 a[96:99], a[104:107], v[100:107], v21\n" \
E_"v71, v71\n" \
M_"v[220:223], a[188:191], v[116:119], v[220:223]\n" \
E_"v72, v72\n" \
M_"v[252:255], a[188:191], v[120:123], v[252:255]\n" \
B_"v7, s[80:83], s92 offen offset:3072 lds\n" \
M_"v[216:219], a[184:187], v[116:119], v[216:219]\n" \
"s_add_u32 m0, s93, 0x2000\n" \
C_"v71, v70, v71\n" \
M_"v[248:251], a[184:187], v[120:123], v[248:251]\n" \
E_"v73, v73\n" \
M_"v[212:215], a[180:183], v[116:119], v[212:215]\n" \
E_"v68, v68\n" \
M_"v[244:247], a[180:183], v[120:123], v[244:247]\n" \
E_"v69, v69\n" \
M_"v[208:211], a[176:179], v[116:119], v[208:211]\n" \
B_"v8, s[80:83], s92 offen offset:0 lds\n" \
M_"v[240:243], a[176:179], v[120:123], v[240:243]\n" \
E_"v74, v74\n" \
M_"v[204:207], a[172:175], v[116:119], v[204:207]\n" \
E_"v75, v75\n" \
M_"v[236:239], a[172:175], v[120:123], v[236:239]\n" \
E_"v66, v66\n" \
M_"v[200:203], a[168:171], v[116:119], v[200:203]\n" \
E_"v67, v67\n" \
M_"v[232:235], a[168:171], v[120:123], v[232:235]\n" \
E_"v64, v64\n" \
M_"v[196:199], a[164:167], v[116:119], v[196:199]\n" \
E_"v65, v65\n" \
M_"v[228:231], a[164:167], v[120:123], v[228:231]\n" \
C_"v72, v72, v73\n" \
C_"v70, v68, v69\n" \
M_"v[192:195], a[160:163], v[116:119], v[192:195]\n" \
E_"v76, v76\n" \
M_"v[224:227], a[160:163], v[120:123], v[224:227]\n" \
E_"v77, v77\n" \
"v_smfmac_f32_16x16x64_bf16 a[100:103], a[104:107], v[116:123], v21\n" \
E_"v78, v78\n" \
"s_waitcnt vmcnt(13) lgkmcnt(0)\n" \
"s_barrier\n" \
E_"v79, v79\n" \
C_"v73, v74, v75\n" \
C_"v69, v66, v67\n" \
C_"v68, v64, v65\n" \
C_"v74, v76, v77\n" \
C_"v75, v78, v79\n" \
"v_add_u32_e32 v48, s16, v20\n" \
"s_xor_b32 s65, s65, 0x8000\n" \
"s_add_u32 s64, s64, 0x10000\n" \
"s_getpc_b64 s[86:87]\n" \
"L_hpt_%=:\n" \
"s_add_u32 s86, s86, L_ap8-L_hpt_%=\n" \
"s_addc_u32 s87, s87, 0\n" \
"s_add_u32 s86, s86, s15\n" \
"s_addc_u32 s87, s87, 0\n" \
"s_mov_b64 exec, 0\n" \
"s_swappc_b64 s[88:89], s[86:87]\n" \
"s_mov_b64 exec, -1\n" \
"s_sub_u32 s66, s19, 1\n" \
"s_cbranch_scc1 L_la_%=\n" \
"s_branch L_2_%=\n" \
".fill (L_ab + 6400 - .) / 4, 4, 0xBF800000\n" \
"L_tL_2_%=:\n" \
"s_waitcnt vmcnt(8) lgkmcnt(0)\n" \
"v_smfmac_f32_16x16x64_bf16 a[100:103], a[104:107], v[116:123], v21\n" \
"s_barrier\n" \
"L_2_%=:\n" \
M_"v[96:99], a[128:131], a[64:67], v[32:35]\n" \
"s_xor_b32 m0, s65, 0x12000\n" \
R_"a[160:163], v49 offset:0\n" \
M_"v[96:99], a[132:135], a[68:71], v[96:99]\n" \
B_"v50, s[8:11], s64 offen offset:0 sc1 lds\n" \
M_"v[96:99], a[136:139], a[72:75], v[96:99]\n" \
R_"a[164:167], v49 offset:1024\n" \
R_"a[168:171], v49 offset:2048\n" \
M_"v[96:99], a[140:143], a[76:79], v[96:99]\n" \
R_"a[172:175], v49 offset:3072\n" \
R_"a[176:179], v49 offset:4096\n" \
M_"v[100:103], a[156:159], a[76:79], v[32:35]\n" \
R_"a[180:183], v49 offset:5120\n" \
R_"a[184:187], v49 offset:6144\n" \
M_"v[100:103], a[152:155], a[72:75], v[100:103]\n" \
"v_exp_f32_e64 v86, v86\n" \
M_"v[100:103], a[148:151], a[68:71], v[100:103]\n" \
"v_exp_f32_e64 v87, v87\n" \
M_"v[100:103], a[144:147], a[64:67], v[100:103]\n" \
B_"v50, s[8:11], s64 offen offset:1024 sc1 lds\n" \
M_"v[108:111], a[144:147], a[80:83], v[36:39]\n" \
R_"a[188:191], v49 offset:7168\n" \
C_"v87, v86, v87\n" \
M_"v[108:111], a[148:151], a[84:87], v[108:111]\n" \
"v_exp_f32_e64 v84, v84\n" \
M_"v[108:111], a[152:155], a[88:91], v[108:111]\n" \
"v_exp_f32_e64 v85, v85\n" \
M_"v[108:111], a[156:159], a[92:95], v[108:111]\n" \
"v_exp_f32_e64 v82, v82\n" \
M_"v[104:107], a[140:143], a[92:95], v[36:39]\n" \
"v_exp_f32_e64 v83, v83\n" \
M_"v[104:107], a[136:139], a[88:91], v[104:107]\n" \
"v_exp_f32_e64 v80, v80\n" \
M_"v[104:107], a[132:135], a[84:87], v[104:107]\n" \
"v_exp_f32_e64 v81, v81\n" \
M_"v[104:107], a[128:131], a[80:83], v[104:107]\n" \
B_"v50, s[8:11], s64 offen offset:2048 sc1 lds\n" \
M_"v[112:115], a[128:131], a[192:195], v[40:43]\n" \
"v_exp_f32_e64 v88, v88\n" \
M_"v[112:115], a[132:135], a[196:199], v[112:115]\n" \
"v_exp_f32_e64 v89, v89\n" \
M_"v[112:115], a[136:139], a[200:203], v[112:115]\n" \
C_"v86, v84, v85\n" \
C_"v85, v82, v83\n" \
M_"v[112:115], a[140:143], a[204:207], v[112:115]\n" \
C_"v84, v80, v81\n" \
C_"v88, v88, v89\n" \
M_"v[116:119], a[156:159], a[204:207], v[40:43]\n" \
"v_exp_f32_e64 v90, v90\n" \
M_"v[116:119], a[152:155], a[200:203], v[116:119]\n" \
"v_exp_f32_e64 v91, v91\n" \
M_"v[116:119], a[148:151], a[196:199], v[116:119]\n" \
B_"v50, s[8:11], s64 offen offset:3072 sc1 lds\n" \
M_"v[116:119], a[144:147], a[192:195], v[116:119]\n" \
"s_xor_b32 m0, s65, 0xa000\n" \
C_"v89, v90, v91\n" \
M_"v[124:127], a[144:147], a[208:211], v[44:47]\n" \
"v_exp_f32_e64 v92, v92\n" \
M_"v[124:127], a[148:151], a[212:215], v[124:127]\n" \
"v_exp_f32_e64 v93, v93\n" \
M_"v[124:127], a[152:155], a[216:219], v[124:127]\n" \
"v_exp_f32_e64 v94, v94\n" \
M_"v[124:127], a[156:159], a[220:223], v[124:127]\n" \
"v_exp_f32_e64 v95, v95\n" \
M_"v[120:123], a[140:143], a[220:223], v[44:47]\n" \
C_"v90, v92, v93\n" \
C_"v91, v94, v95\n" \
M_"v[120:123], a[136:139], a[216:219], v[120:123]\n" \
R_"a[144:147], v48 offset:4096\n" \
R_"a[148:151], v48 offset:5120\n" \
M_"v[120:123], a[132:135], a[212:215], v[120:123]\n" \
B_"v51, s[8:11], s64 offen offset:0 sc1 lds\n" \
M_"v[120:123], a[128:131], a[208:211], v[120:123]\n" \
R_"a[128:131], v48 offset:0\n" \
"s_waitcnt lgkmcnt(3)\n" \
M_"v[128:131], a[160:163], v[68:71], v[128:131]\n" \
R_"a[132:135], v48 offset:1024\n" \
R_"a[136:139], v48 offset:2048\n" \
M_"v[160:163], a[160:163], v[72:75], v[160:163]\n" \
R_"a[152:155], v48 offset:6144\n" \
R_"a[140:143], v48 offset:3072\n" \
M_"v[132:135], a[164:167], v[68:71], v[132:135]\n" \
E_"v102, v102\n" \
M_"v[164:167], a[164:167], v[72:75], v[164:167]\n" \
"v_exp_f32_e64 v103, v103\n" \
M_"v[136:139], a[168:171], v[68:71], v[136:139]\n" \
R_"a[156:159], v48 offset:7168\n" \
C_"v103, v102, v103\n" \
M_"v[168:171], a[168:171], v[72:75], v[168:171]\n" \
B_"v51, s[8:11], s64 offen offset:1024 sc1 lds\n" \
M_"v[140:143], a[172:175], v[68:71], v[140:143]\n" \
"v_exp_f32_e64 v100, v100\n" \
M_"v[172:175], a[172:175], v[72:75], v[172:175]\n" \
"v_exp_f32_e64 v101, v101\n" \
M_"v[144:147], a[176:179], v[68:71], v[144:147]\n" \
"v_exp_f32_e64 v98, v98\n" \
M_"v[176:179], a[176:179], v[72:75], v[176:179]\n" \
"v_exp_f32_e64 v99, v99\n" \
M_"v[148:151], a[180:183], v[68:71], v[148:151]\n" \
"v_exp_f32_e64 v96, v96\n" \
M_"v[180:183], a[180:183], v[72:75], v[180:183]\n" \
"v_exp_f32_e64 v97, v97\n" \
M_"v[152:155], a[184:187], v[68:71], v[152:155]\n" \
"v_exp_f32_e64 v104, v104\n" \
M_"v[184:187], a[184:187], v[72:75], v[184:187]\n" \
B_"v51, s[8:11], s64 offen offset:2048 sc1 lds\n" \
M_"v[156:159], a[188:191], v[68:71], v[156:159]\n" \
"v_exp_f32_e64 v105, v105\n" \
M_"v[188:191], a[188:191], v[72:75], v[188:191]\n" \
C_"v102, v100, v101\n" \
C_"v101, v98, v99\n" \
"v_smfmac_f32_16x16x64_bf16 a[96:99], a[104:107], v[68:75], v21\n" \
C_"v100, v96, v97\n" \
C_"v104, v104, v105\n" \
M_"v[220:223], a[188:191], v[84:87], v[220:223]\n" \
"v_exp_f32_e64 v106, v106\n" \
M_"v[252:255], a[188:191], v[88:91], v[252:255]\n" \
"v_exp_f32_e64 v107, v107\n" \
M_"v[216:219], a[184:187], v[84:87], v[216:219]\n" \
"v_exp_f32_e64 v108, v108\n" \
M_"v[248:251], a[184:187], v[88:91], v[248:251]\n" \
B_"v51, s[8:11], s64 offen offset:3072 sc1 lds\n" \
M_"v[212:215], a[180:183], v[84:87], v[212:215]\n" \
"v_exp_f32_e64 v109, v109\n" \
M_"v[244:247], a[180:183], v[88:91], v[244:247]\n" \
"v_exp_f32_e64 v110, v110\n" \
M_"v[208:211], a[176:179], v[84:87], v[208:211]\n" \
"v_exp_f32_e64 v111, v111\n" \
M_"v[240:243], a[176:179], v[88:91], v[240:243]\n" \
"v_exp_f32_e64 v118, v118\n" \
M_"v[204:207], a[172:175], v[84:87], v[204:207]\n" \
"v_exp_f32_e64 v119, v119\n" \
M_"v[236:239], a[172:175], v[88:91], v[236:239]\n" \
C_"v105, v106, v107\n" \
C_"v106, v108, v109\n" \
M_"v[200:203], a[168:171], v[84:87], v[200:203]\n" \
C_"v107, v110, v111\n" \
C_"v119, v118, v119\n" \
M_"v[232:235], a[168:171], v[88:91], v[232:235]\n" \
"v_exp_f32_e64 v116, v116\n" \
M_"v[196:199], a[164:167], v[84:87], v[196:199]\n" \
"v_exp_f32_e64 v117, v117\n" \
M_"v[228:231], a[164:167], v[88:91], v[228:231]\n" \
"v_exp_f32_e64 v114, v114\n" \
M_"v[192:195], a[160:163], v[84:87], v[192:195]\n" \
"v_exp_f32_e64 v115, v115\n" \
M_"v[224:227], a[160:163], v[88:91], v[224:227]\n" \
C_"v118, v116, v117\n" \
"s_waitcnt vmcnt(8) lgkmcnt(0)\n" \
"v_smfmac_f32_16x16x64_bf16 a[100:103], a[104:107], v[84:91], v21\n" \
"s_barrier\n" \
M_"v[64:67], a[128:131], a[64:67], v[32:35]\n" \
"s_xor_b32 m0, s65, 0x18000\n" \
R_"a[160:163], v49 offset:8192\n" \
M_"v[64:67], a[132:135], a[68:71], v[64:67]\n" \
B_"v52, s[8:11], s64 offen offset:0 sc1 lds\n" \
M_"v[64:67], a[136:139], a[72:75], v[64:67]\n" \
R_"a[164:167], v49 offset:9216\n" \
R_"a[168:171], v49 offset:10240\n" \
M_"v[64:67], a[140:143], a[76:79], v[64:67]\n" \
R_"a[172:175], v49 offset:11264\n" \
R_"a[176:179], v49 offset:12288\n" \
M_"v[68:71], a[156:159], a[76:79], v[32:35]\n" \
R_"a[180:183], v49 offset:13312\n" \
R_"a[184:187], v49 offset:14336\n" \
M_"v[68:71], a[152:155], a[72:75], v[68:71]\n" \
R_"a[188:191], v49 offset:15360\n" \
C_"v117, v114, v115\n" \
M_"v[68:71], a[148:151], a[68:71], v[68:71]\n" \
"v_exp_f32_e64 v112, v112\n" \
M_"v[68:71], a[144:147], a[64:67], v[68:71]\n" \
B_"v52, s[8:11], s64 offen offset:1024 sc1 lds\n" \
M_"v[76:79], a[144:147], a[80:83], v[36:39]\n" \
"v_exp_f32_e64 v113, v113\n" \
M_"v[76:79], a[148:151], a[84:87], v[76:79]\n" \
C_"v116, v112, v113\n" \
"v_add_u32_e32 v49, 0x10000, v48\n" \
M_"v[76:79], a[152:155], a[88:91], v[76:79]\n" \
"v_exp_f32_e64 v120, v120\n" \
M_"v[76:79], a[156:159], a[92:95], v[76:79]\n" \
"v_exp_f32_e64 v121, v121\n" \
M_"v[72:75], a[140:143], a[92:95], v[36:39]\n" \
"v_exp_f32_e64 v122, v122\n" \
M_"v[72:75], a[136:139], a[88:91], v[72:75]\n" \
"v_exp_f32_e64 v123, v123\n" \
M_"v[72:75], a[132:135], a[84:87], v[72:75]\n" \
"v_exp_f32_e64 v124, v124\n" \
M_"v[72:75], a[128:131], a[80:83], v[72:75]\n" \
B_"v52, s[8:11], s64 offen offset:2048 sc1 lds\n" \
M_"v[80:83], a[128:131], a[192:195], v[40:43]\n" \
"v_exp_f32_e64 v125, v125\n" \
M_"v[80:83], a[132:135], a[196:199], v[80:83]\n" \
"v_exp_f32_e64 v126, v126\n" \
M_"v[80:83], a[136:139], a[200:203], v[80:83]\n" \
"v_exp_f32_e64 v127, v127\n" \
M_"v[80:83], a[140:143], a[204:207], v[80:83]\n" \
"v_exp_f32_e64 v70, v70\n" \
M_"v[84:87], a[156:159], a[204:207], v[40:43]\n" \
"v_exp_f32_e64 v71, v71\n" \
M_"v[84:87], a[152:155], a[200:203], v[84:87]\n" \
C_"v120, v120, v121\n" \
C_"v121, v122, v123\n" \
M_"v[84:87], a[148:151], a[196:199], v[84:87]\n" \
B_"v52, s[8:11], s64 offen offset:3072 sc1 lds\n" \
M_"v[84:87], a[144:147], a[192:195], v[84:87]\n" \
"s_mov_b32 m0, s65\n" \
"s_nop 0\n" \
C_"v122, v124, v125\n" \
M_"v[92:95], a[144:147], a[208:211], v[44:47]\n" \
C_"v123, v126, v127\n" \
R_"a[144:147], v48 offset:12288\n" \
M_"v[92:95], a[148:151], a[212:215], v[92:95]\n" \
R_"a[148:151], v48 offset:13312\n" \
C_"v71, v70, v71\n" \
M_"v[92:95], a[152:155], a[216:219], v[92:95]\n" \
R_"a[152:155], v48 offset:14336\n" \
"s_xor_b32 s65, s65, 0x8000\n" \
M_"v[92:95], a[156:159], a[220:223], v[92:95]\n" \
"v_exp_f32_e64 v72, v72\n" \
M_"v[88:91], a[140:143], a[220:223], v[44:47]\n" \
R_"a[140:143], v48 offset:11264\n" \
R_"a[156:159], v48 offset:15360\n" \
M_"v[88:91], a[136:139], a[216:219], v[88:91]\n" \
"v_exp_f32_e64 v73, v73\n" \
M_"v[88:91], a[132:135], a[212:215], v[88:91]\n" \
".fill (L_ab + 8192 - .) / 4, 4, 0xBF800000\n" \
"L_ap2:\n" \
"s_cbranch_execz L_ap0\n" \
B_"v53, s[8:11], s64 offen offset:0 sc1 lds\n" \
M_"v[88:91], a[128:131], a[208:211], v[88:91]\n" \
R_"a[128:131], v48 offset:8192\n" \
"s_waitcnt lgkmcnt(6)\n" \
M_"v[128:131], a[160:163], v[100:103], v[128:131]\n" \
R_"a[132:135], v48 offset:9216\n" \
R_"a[136:139], v48 offset:10240\n" \
M_"v[160:163], a[160:163], v[104:107], v[160:163]\n" \
"v_exp_f32_e64 v68, v68\n" \
M_"v[132:135], a[164:167], v[100:103], v[132:135]\n" \
"v_exp_f32_e64 v69, v69\n" \
M_"v[164:167], a[164:167], v[104:107], v[164:167]\n" \
"v_exp_f32_e64 v74, v74\n" \
M_"v[136:139], a[168:171], v[100:103], v[136:139]\n" \
"v_exp_f32_e64 v75, v75\n" \
M_"v[168:171], a[168:171], v[104:107], v[168:171]\n" \
B_"v53, s[8:11], s64 offen offset:1024 sc1 lds\n" \
M_"v[140:143], a[172:175], v[100:103], v[140:143]\n" \
"v_exp_f32_e64 v66, v66\n" \
M_"v[172:175], a[172:175], v[104:107], v[172:175]\n" \
"v_exp_f32_e64 v67, v67\n" \
M_"v[144:147], a[176:179], v[100:103], v[144:147]\n" \
"v_exp_f32_e64 v64, v64\n" \
M_"v[176:179], a[176:179], v[104:107], v[176:179]\n" \
"v_exp_f32_e64 v65, v65\n" \
M_"v[148:151], a[180:183], v[100:103], v[148:151]\n" \
C_"v72, v72, v73\n" \
C_"v70, v68, v69\n" \
M_"v[180:183], a[180:183], v[104:107], v[180:183]\n" \
"v_exp_f32_e64 v76, v76\n" \
M_"v[152:155], a[184:187], v[100:103], v[152:155]\n" \
"v_exp_f32_e64 v77, v77\n" \
M_"v[184:187], a[184:187], v[104:107], v[184:187]\n" \
B_"v53, s[8:11], s64 offen offset:2048 sc1 lds\n" \
M_"v[156:159], a[188:191], v[100:103], v[156:159]\n" \
"v_exp_f32_e64 v78, v78\n" \
M_"v[188:191], a[188:191], v[104:107], v[188:191]\n" \
"v_exp_f32_e64 v79, v79\n" \
"v_smfmac_f32_16x16x64_bf16 a[96:99], a[104:107], v[100:107], v21\n" \
C_"v73, v74, v75\n" \
C_"v69, v66, v67\n" \
M_"v[220:223], a[188:191], v[116:119], v[220:223]\n" \
C_"v68, v64, v65\n" \
C_"v74, v76, v77\n" \
M_"v[252:255], a[188:191], v[120:123], v[252:255]\n" \
C_"v75, v78, v79\n" \
"v_xor_b32_e32 v48, 0x8000, v48\n" \
M_"v[216:219], a[184:187], v[116:119], v[216:219]\n" \
M_"v[248:251], a[184:187], v[120:123], v[248:251]\n" \
B_"v53, s[8:11], s64 offen offset:3072 sc1 lds\n" \
M_"v[212:215], a[180:183], v[116:119], v[212:215]\n" \
"s_add_u32 s64, s64, 0x10000\n" \
"s_sub_u32 s66, s66, 1\n" \
"s_nop 0\n" \
M_"v[244:247], a[180:183], v[120:123], v[244:247]\n" \
M_"v[208:211], a[176:179], v[116:119], v[208:211]\n" \
M_"v[240:243], a[176:179], v[120:123], v[240:243]\n" \
M_"v[204:207], a[172:175], v[116:119], v[204:207]\n" \
M_"v[236:239], a[172:175], v[120:123], v[236:239]\n" \
M_"v[200:203], a[168:171], v[116:119], v[200:203]\n" \
M_"v[232:235], a[168:171], v[120:123], v[232:235]\n" \
M_"v[196:199], a[164:167], v[116:119], v[196:199]\n" \
M_"v[228:231], a[164:167], v[120:123], v[228:231]\n" \
M_"v[192:195], a[160:163], v[116:119], v[192:195]\n" \
M_"v[224:227], a[160:163], v[120:123], v[224:227]\n" \
"s_cbranch_scc0 L_tL_2_%=\n" \
"s_waitcnt vmcnt(8) lgkmcnt(0)\n" \
"v_smfmac_f32_16x16x64_bf16 a[100:103], a[104:107], v[116:123], v21\n" \
"s_barrier\n" \
"L_la_%=:\n" \
M_"v[96:99], a[128:131], a[64:67], v[32:35]\n" \
"v_readlane_b32 s96, v11, 1\n" \
"v_lshrrev_b32_e32 v16, 1, v20\n" \
M_"v[96:99], a[132:135], a[68:71], v[96:99]\n" \
"s_xor_b32 m0, s65, 0x12000\n" \
R_"a[160:163], v49 offset:0\n" \
M_"v[96:99], a[136:139], a[72:75], v[96:99]\n" \
"buffer_load_dwordx2 v[26:27], v16, s[56:59], s96 offen sc1\n" \
M_"v[96:99], a[140:143], a[76:79], v[96:99]\n" \
B_"v50, s[8:11], s64 offen offset:0 sc1 lds\n" \
M_"v[100:103], a[156:159], a[76:79], v[32:35]\n" \
R_"a[164:167], v49 offset:1024\n" \
R_"a[168:171], v49 offset:2048\n" \
M_"v[100:103], a[152:155], a[72:75], v[100:103]\n" \
R_"a[172:175], v49 offset:3072\n" \
R_"a[176:179], v49 offset:4096\n" \
M_"v[100:103], a[148:151], a[68:71], v[100:103]\n" \
R_"a[180:183], v49 offset:5120\n" \
R_"a[184:187], v49 offset:6144\n" \
M_"v[100:103], a[144:147], a[64:67], v[100:103]\n" \
R_"a[188:191], v49 offset:7168\n" \
"s_mov_b32 s63, s62\n" \
M_"v[108:111], a[144:147], a[80:83], v[36:39]\n" \
E_"v86, v86\n" \
M_"v[108:111], a[148:151], a[84:87], v[108:111]\n" \
"v_exp_f32_e64 v87, v87\n" \
M_"v[108:111], a[152:155], a[88:91], v[108:111]\n" \
B_"v50, s[8:11], s64 offen offset:1024 sc1 lds\n" \
M_"v[108:111], a[156:159], a[92:95], v[108:111]\n" \
"v_exp_f32_e64 v84, v84\n" \
M_"v[104:107], a[140:143], a[92:95], v[36:39]\n" \
"v_exp_f32_e64 v85, v85\n" \
M_"v[104:107], a[136:139], a[88:91], v[104:107]\n" \
C_"v87, v86, v87\n" \
C_"v86, v84, v85\n" \
M_"v[104:107], a[132:135], a[84:87], v[104:107]\n" \
"v_exp_f32_e64 v82, v82\n" \
M_"v[104:107], a[128:131], a[80:83], v[104:107]\n" \
"v_exp_f32_e64 v83, v83\n" \
M_"v[112:115], a[128:131], a[192:195], v[40:43]\n" \
"v_exp_f32_e64 v80, v80\n" \
M_"v[112:115], a[132:135], a[196:199], v[112:115]\n" \
"v_exp_f32_e64 v81, v81\n" \
M_"v[112:115], a[136:139], a[200:203], v[112:115]\n" \
B_"v50, s[8:11], s64 offen offset:2048 sc1 lds\n" \
M_"v[112:115], a[140:143], a[204:207], v[112:115]\n" \
C_"v85, v82, v83\n" \
C_"v84, v80, v81\n" \
M_"v[116:119], a[156:159], a[204:207], v[40:43]\n" \
"v_exp_f32_e64 v88, v88\n" \
M_"v[116:119], a[152:155], a[200:203], v[116:119]\n" \
"v_exp_f32_e64 v89, v89\n" \
M_"v[116:119], a[148:151], a[196:199], v[116:119]\n" \
"v_exp_f32_e64 v90, v90\n" \
M_"v[116:119], a[144:147], a[192:195], v[116:119]\n" \
"v_exp_f32_e64 v91, v91\n" \
M_"v[124:127], a[144:147], a[208:211], v[44:47]\n" \
C_"v88, v88, v89\n" \
C_"v89, v90, v91\n" \
M_"v[124:127], a[148:151], a[212:215], v[124:127]\n" \
B_"v50, s[8:11], s64 offen offset:3072 sc1 lds\n" \
M_"v[124:127], a[152:155], a[216:219], v[124:127]\n" \
"v_exp_f32_e64 v92, v92\n" \
M_"v[124:127], a[156:159], a[220:223], v[124:127]\n" \
"v_exp_f32_e64 v93, v93\n" \
M_"v[120:123], a[140:143], a[220:223], v[44:47]\n" \
"v_exp_f32_e64 v94, v94\n" \
M_"v[120:123], a[136:139], a[216:219], v[120:123]\n" \
"v_exp_f32_e64 v95, v95\n" \
M_"v[120:123], a[132:135], a[212:215], v[120:123]\n" \
C_"v90, v92, v93\n" \
C_"v91, v94, v95\n" \
M_"v[120:123], a[128:131], a[208:211], v[120:123]\n" \
"v_readlane_b32 s97, v11, 39\n" \
"s_waitcnt lgkmcnt(0)\n" \
"s_nop 0\n" \
M_"v[128:131], a[160:163], v[68:71], v[128:131]\n" \
R_"a[128:131], v48 offset:0\n" \
R_"a[144:147], v48 offset:4096\n" \
M_"v[160:163], a[160:163], v[72:75], v[160:163]\n" \
R_"a[132:135], v48 offset:1024\n" \
R_"a[148:151], v48 offset:5120\n" \
M_"v[132:135], a[164:167], v[68:71], v[132:135]\n" \
R_"a[136:139], v48 offset:2048\n" \
R_"a[152:155], v48 offset:6144\n" \
M_"v[164:167], a[164:167], v[72:75], v[164:167]\n" \
R_"a[140:143], v48 offset:3072\n" \
R_"a[156:159], v48 offset:7168\n" \
M_"v[136:139], a[168:171], v[68:71], v[136:139]\n" \
"v_add_u32_e32 v17, 0x49000, v20\n" \
"v_readlane_b32 s98, v11, 40\n" \
M_"v[168:171], a[168:171], v[72:75], v[168:171]\n" \
"v_readlane_b32 s93, v11, 23\n" \
"v_readlane_b32 s84, v11, 15\n" \
M_"v[140:143], a[172:175], v[68:71], v[140:143]\n" \
"v_readlane_b32 s85, v11, 16\n" \
"v_readlane_b32 s86, v11, 17\n" \
M_"v[172:175], a[172:175], v[72:75], v[172:175]\n" \
"s_mov_b32 s87, 0x20000\n" \
"v_readlane_b32 s92, v11, 18\n" \
M_"v[144:147], a[176:179], v[68:71], v[144:147]\n" \
"v_exp_f32_e64 v102, v102\n" \
M_"v[176:179], a[176:179], v[72:75], v[176:179]\n" \
"v_exp_f32_e64 v103, v103\n" \
M_"v[148:151], a[180:183], v[68:71], v[148:151]\n" \
"v_exp_f32_e64 v100, v100\n" \
M_"v[180:183], a[180:183], v[72:75], v[180:183]\n" \
"v_exp_f32_e64 v101, v101\n" \
M_"v[152:155], a[184:187], v[68:71], v[152:155]\n" \
C_"v103, v102, v103\n" \
C_"v102, v100, v101\n" \
M_"v[184:187], a[184:187], v[72:75], v[184:187]\n" \
"v_exp_f32_e64 v98, v98\n" \
M_"v[156:159], a[188:191], v[68:71], v[156:159]\n" \
"v_exp_f32_e64 v99, v99\n" \
M_"v[188:191], a[188:191], v[72:75], v[188:191]\n" \
"s_waitcnt vmcnt(4)\n" \
"v_cmp_ne_u64_e32 vcc, s[62:63], v[26:27]\n" \
"v_smfmac_f32_16x16x64_bf16 a[96:99], a[104:107], v[68:75], v21\n" \
"v_exp_f32_e64 v96, v96\n" \
M_"v[220:223], a[188:191], v[84:87], v[220:223]\n" \
"v_exp_f32_e64 v97, v97\n" \
M_"v[252:255], a[188:191], v[88:91], v[252:255]\n" \
"s_cbranch_vccnz L_sol_%=\n" \
"L_rol_%=:\n" \
C_"v101, v98, v99\n" \
B_"a[124:127], v20, s[56:59], s97 offen offset:0 sc1\n" \
M_"v[216:219], a[184:187], v[84:87], v[216:219]\n" \
C_"v100, v96, v97\n" \
"v_readlane_b32 s91, v11, 24\n" \
M_"v[248:251], a[184:187], v[88:91], v[248:251]\n" \
E_"v104, v104\n" \
M_"v[212:215], a[180:183], v[84:87], v[212:215]\n" \
"v_exp_f32_e64 v105, v105\n" \
M_"v[244:247], a[180:183], v[88:91], v[244:247]\n" \
"v_exp_f32_e64 v106, v106\n" \
M_"v[208:211], a[176:179], v[84:87], v[208:211]\n" \
"v_exp_f32_e64 v107, v107\n" \
M_"v[240:243], a[176:179], v[88:91], v[240:243]\n" \
B_"a[228:231], v17, s[56:59], s97 offen offset:0 sc1\n" \
M_"v[204:207], a[172:175], v[84:87], v[204:207]\n" \
C_"v104, v104, v105\n" \
C_"v105, v106, v107\n" \
M_"v[236:239], a[172:175], v[88:91], v[236:239]\n" \
"v_exp_f32_e64 v108, v108\n" \
M_"v[200:203], a[168:171], v[84:87], v[200:203]\n" \
"v_exp_f32_e64 v109, v109\n" \
M_"v[232:235], a[168:171], v[88:91], v[232:235]\n" \
"v_exp_f32_e64 v110, v110\n" \
M_"v[196:199], a[164:167], v[84:87], v[196:199]\n" \
"v_exp_f32_e64 v111, v111\n" \
M_"v[228:231], a[164:167], v[88:91], v[228:231]\n" \
C_"v106, v108, v109\n" \
C_"v107, v110, v111\n" \
M_"v[192:195], a[160:163], v[84:87], v[192:195]\n" \
"v_exp_f32_e64 v118, v118\n" \
M_"v[224:227], a[160:163], v[88:91], v[224:227]\n" \
"s_xor_b32 s65, s65, 0x8000\n" \
"s_waitcnt vmcnt(7) lgkmcnt(0)\n" \
"v_smfmac_f32_16x16x64_bf16 a[100:103], a[104:107], v[84:91], v21\n" \
"s_barrier\n" \
M_"v[64:67], a[128:131], a[64:67], v[32:35]\n" \
B_"a[236:239], v20, s[56:59], s98 offen offset:0 sc1\n" \
M_"v[64:67], a[132:135], a[68:71], v[64:67]\n" \
"s_add_u32 m0, s93, 0x0\n" \
"s_nop 0\n" \
R_"a[160:163], v49 offset:8192\n" \
M_"v[64:67], a[136:139], a[72:75], v[64:67]\n" \
B_"v0, s[84:87], s92 offen offset:0 lds\n" \
M_"v[64:67], a[140:143], a[76:79], v[64:67]\n" \
R_"a[164:167], v49 offset:9216\n" \
R_"a[168:171], v49 offset:10240\n" \
M_"v[68:71], a[156:159], a[76:79], v[32:35]\n" \
R_"a[172:175], v49 offset:11264\n" \
R_"a[176:179], v49 offset:12288\n" \
M_"v[68:71], a[152:155], a[72:75], v[68:71]\n" \
R_"a[180:183], v49 offset:13312\n" \
R_"a[184:187], v49 offset:14336\n" \
M_"v[68:71], a[148:151], a[68:71], v[68:71]\n" \
B_"v1, s[84:87], s92 offen offset:1024 lds\n" \
M_"v[68:71], a[144:147], a[64:67], v[68:71]\n" \
B_"a[244:247], v17, s[56:59], s98 offen offset:0 sc1\n" \
M_"v[76:79], a[144:147], a[80:83], v[36:39]\n" \
R_"a[188:191], v49 offset:15360\n" \
"v_add_u32_e32 v49, 0x10000, v48\n" \
M_"v[76:79], a[148:151], a[84:87], v[76:79]\n" \
"v_exp_f32_e64 v119, v119\n" \
M_"v[76:79], a[152:155], a[88:91], v[76:79]\n" \
"v_exp_f32_e64 v116, v116\n" \
M_"v[76:79], a[156:159], a[92:95], v[76:79]\n" \
"v_exp_f32_e64 v117, v117\n" \
M_"v[72:75], a[140:143], a[92:95], v[36:39]\n" \
C_"v119, v118, v119\n" \
C_"v118, v116, v117\n" \
M_"v[72:75], a[136:139], a[88:91], v[72:75]\n" \
B_"v2, s[84:87], s92 offen offset:2048 lds\n" \
M_"v[72:75], a[132:135], a[84:87], v[72:75]\n" \
"v_exp_f32_e64 v114, v114\n" \
M_"v[72:75], a[128:131], a[80:83], v[72:75]\n" \
"v_exp_f32_e64 v115, v115\n" \
M_"v[80:83], a[128:131], a[192:195], v[40:43]\n" \
B_"a[224:227], v20, s[56:59], s97 offen offset:1024 sc1\n" \
M_"v[80:83], a[132:135], a[196:199], v[80:83]\n" \
"v_exp_f32_e64 v112, v112\n" \
M_"v[80:83], a[136:139], a[200:203], v[80:83]\n" \
"v_exp_f32_e64 v113, v113\n" \
M_"v[80:83], a[140:143], a[204:207], v[80:83]\n" \
C_"v117, v114, v115\n" \
C_"v116, v112, v113\n" \
M_"v[84:87], a[156:159], a[204:207], v[40:43]\n" \
B_"v3, s[84:87], s92 offen offset:3072 lds\n" \
M_"v[84:87], a[152:155], a[200:203], v[84:87]\n" \
"s_add_u32 m0, s91, 0x1000\n" \
"s_add_u32 s64, s64, 0x10000\n" \
M_"v[84:87], a[148:151], a[196:199], v[84:87]\n" \
"v_exp_f32_e64 v120, v120\n" \
M_"v[84:87], a[144:147], a[192:195], v[84:87]\n" \
"v_exp_f32_e64 v121, v121\n" \
M_"v[92:95], a[144:147], a[208:211], v[44:47]\n" \
"v_exp_f32_e64 v122, v122\n" \
M_"v[92:95], a[148:151], a[212:215], v[92:95]\n" \
B_"a[232:235], v17, s[56:59], s97 offen offset:1024 sc1\n" \
M_"v[92:95], a[152:155], a[216:219], v[92:95]\n" \
B_"v4, s[84:87], s92 offen offset:0 lds\n" \
M_"v[92:95], a[156:159], a[220:223], v[92:95]\n" \
"v_exp_f32_e64 v123, v123\n" \
M_"v[88:91], a[140:143], a[220:223], v[44:47]\n" \
C_"v120, v120, v121\n" \
C_"v121, v122, v123\n" \
M_"v[88:91], a[136:139], a[216:219], v[88:91]\n" \
"v_exp_f32_e64 v124, v124\n" \
M_"v[88:91], a[132:135], a[212:215], v[88:91]\n" \
"v_exp_f32_e64 v125, v125\n" \
M_"v[88:91], a[128:131], a[208:211], v[88:91]\n" \
C_"v122, v124, v125\n" \
"s_waitcnt lgkmcnt(0)\n" \
M_"v[128:131], a[160:163], v[100:103], v[128:131]\n" \
E_"v126, v126\n" \
M_"v[160:163], a[160:163], v[104:107], v[160:163]\n" \
B_"v5, s[84:87], s92 offen offset:1024 lds\n" \
M_"v[132:135], a[164:167], v[100:103], v[132:135]\n" \
B_"a[240:243], v20, s[56:59], s98 offen offset:1024 sc1\n" \
M_"v[164:167], a[164:167], v[104:107], v[164:167]\n" \
"v_exp_f32_e64 v127, v127\n" \
M_"v[136:139], a[168:171], v[100:103], v[136:139]\n" \
C_"v123, v126, v127\n" \
R_"a[128:131], v48 offset:8192\n" \
M_"v[168:171], a[168:171], v[104:107], v[168:171]\n" \
R_"a[144:147], v48 offset:12288\n" \
R_"a[132:135], v48 offset:9216\n" \
M_"v[140:143], a[172:175], v[100:103], v[140:143]\n" \
R_"a[148:151], v48 offset:13312\n" \
R_"a[136:139], v48 offset:10240\n" \
M_"v[172:175], a[172:175], v[104:107], v[172:175]\n" \
R_"a[152:155], v48 offset:14336\n" \
R_"a[140:143], v48 offset:11264\n" \
M_"v[144:147], a[176:179], v[100:103], v[144:147]\n" \
B_"v6, s[84:87], s92 offen offset:2048 lds\n" \
M_"v[176:179], a[176:179], v[104:107], v[176:179]\n" \
"v_exp_f32_e64 v70, v70\n" \
M_"v[148:151], a[180:183], v[100:103], v[148:151]\n" \
"v_exp_f32_e64 v71, v71\n" \
M_"v[180:183], a[180:183], v[104:107], v[180:183]\n" \
B_"a[248:251], v17, s[56:59], s98 offen offset:1024 sc1\n" \
M_"v[152:155], a[184:187], v[100:103], v[152:155]\n" \
R_"a[156:159], v48 offset:15360\n" \
C_"v71, v70, v71\n" \
M_"v[184:187], a[184:187], v[104:107], v[184:187]\n" \
"v_exp_f32_e64 v72, v72\n" \
M_"v[156:159], a[188:191], v[100:103], v[156:159]\n" \
B_"v7, s[84:87], s92 offen offset:3072 lds\n" \
M_"v[188:191], a[188:191], v[104:107], v[188:191]\n" \
"s_add_u32 m0, s91, 0x2000\n" \
"v_xor_b32_e32 v48, 0x8000, v48\n" \
"v_smfmac_f32_16x16x64_bf16 a[96:99], a[104:107], v[100:107], v21\n" \
"v_exp_f32_e64 v73, v73\n" \
M_"v[220:223], a[188:191], v[116:119], v[220:223]\n" \
"v_exp_f32_e64 v68, v68\n" \
M_"v[252:255], a[188:191], v[120:123], v[252:255]\n" \
"v_exp_f32_e64 v69, v69\n" \
M_"v[216:219], a[184:187], v[116:119], v[216:219]\n" \
"v_exp_f32_e64 v74, v74\n" \
M_"v[248:251], a[184:187], v[120:123], v[248:251]\n" \
"v_exp_f32_e64 v75, v75\n" \
M_"v[212:215], a[180:183], v[116:119], v[212:215]\n" \
B_"v8, s[84:87], s92 offen offset:0 lds\n" \
M_"v[244:247], a[180:183], v[120:123], v[244:247]\n" \
"v_exp_f32_e64 v66, v66\n" \
M_"v[208:211], a[176:179], v[116:119], v[208:211]\n" \
"v_exp_f32_e64 v67, v67\n" \
M_"v[240:243], a[176:179], v[120:123], v[240:243]\n" \
"v_exp_f32_e64 v64, v64\n" \
M_"v[204:207], a[172:175], v[116:119], v[204:207]\n" \
"v_exp_f32_e64 v65, v65\n" \
M_"v[236:239], a[172:175], v[120:123], v[236:239]\n" \
C_"v72, v72, v73\n" \
C_"v70, v68, v69\n" \
M_"v[200:203], a[168:171], v[116:119], v[200:203]\n" \
"v_exp_f32_e64 v76, v76\n" \
M_"v[232:235], a[168:171], v[120:123], v[232:235]\n" \
"v_exp_f32_e64 v77, v77\n" \
M_"v[196:199], a[164:167], v[116:119], v[196:199]\n" \
"v_exp_f32_e64 v78, v78\n" \
M_"v[228:231], a[164:167], v[120:123], v[228:231]\n" \
"v_exp_f32_e64 v79, v79\n" \
M_"v[192:195], a[160:163], v[116:119], v[192:195]\n" \
C_"v73, v74, v75\n" \
C_"v69, v66, v67\n" \
M_"v[224:227], a[160:163], v[120:123], v[224:227]\n" \
C_"v68, v64, v65\n" \
"s_waitcnt vmcnt(15) lgkmcnt(0)\n" \
"v_smfmac_f32_16x16x64_bf16 a[100:103], a[104:107], v[116:123], v21\n" \
"s_barrier\n" \
C_"v74, v76, v77\n" \
C_"v75, v78, v79\n" \
"s_cmp_lg_u32 s16, 0\n" \
"s_cbranch_scc1 L_f1_%=\n" \
R_"a[160:163], v49 offset:0\n" \
R_"a[164:167], v49 offset:1024\n" \
R_"a[168:171], v49 offset:2048\n" \
R_"a[172:175], v49 offset:3072\n" \
R_"a[176:179], v49 offset:4096\n" \
R_"a[180:183], v49 offset:5120\n" \
R_"a[184:187], v49 offset:6144\n" \
R_"a[188:191], v49 offset:7168\n" \
"v_add_u32_e32 v54, 0x49000, v20\n" \
B_"a[192:195], v20, s[56:59], s60 offen offset:0 sc1\n" \
"v_add_u32_e32 v50, s15, v20\n" \
"v_add_u32_e32 v51, 0x10000, v50\n" \
"ds_read_b32 v60, v18 offset:0\n" \
"ds_read_b32 v61, v18 offset:128\n" \
R_"a[0:3], v49 offset:8192\n" \
R_"a[4:7], v49 offset:9216\n" \
R_"a[8:11], v49 offset:10240\n" \
R_"a[12:15], v49 offset:11264\n" \
R_"a[16:19], v49 offset:12288\n" \
R_"a[20:23], v49 offset:13312\n" \
R_"a[24:27], v49 offset:14336\n" \
R_"a[28:31], v49 offset:15360\n" \
E_"v86, v86\n" \
E_"v87, v87\n" \
E_"v84, v84\n" \
E_"v85, v85\n" \
C_"v87, v86, v87\n" \
B_"a[196:199], v20, s[56:59], s60 offen offset:1024 sc1\n" \
E_"v82, v82\n" \
E_"v83, v83\n" \
E_"v80, v80\n" \
E_"v81, v81\n" \
"s_waitcnt lgkmcnt(15)\n" \
M_"v[128:131], a[160:163], v[68:71], v[128:131]\n" \
C_"v86, v84, v85\n" \
C_"v85, v82, v83\n" \
M_"v[160:163], a[160:163], v[72:75], v[160:163]\n" \
C_"v84, v80, v81\n" \
"v_and_b32_e32 v84, v12, v84\n" \
M_"v[132:135], a[164:167], v[68:71], v[132:135]\n" \
"v_and_b32_e32 v85, v13, v85\n" \
"v_and_b32_e32 v86, v14, v86\n" \
M_"v[164:167], a[164:167], v[72:75], v[164:167]\n" \
B_"a[200:203], v20, s[56:59], s60 offen offset:2048 sc1\n" \
M_"v[136:139], a[168:171], v[68:71], v[136:139]\n" \
C_"v128, v128, v129\n" \
C_"v160, v160, v161\n" \
M_"v[168:171], a[168:171], v[72:75], v[168:171]\n" \
C_"v129, v130, v131\n" \
"s_waitcnt lgkmcnt(10)\n" \
M_"v[140:143], a[172:175], v[68:71], v[140:143]\n" \
C_"v161, v162, v163\n" \
C_"v130, v132, v133\n" \
M_"v[172:175], a[172:175], v[72:75], v[172:175]\n" \
C_"v131, v134, v135\n" \
C_"v162, v164, v165\n" \
M_"v[144:147], a[176:179], v[68:71], v[144:147]\n" \
C_"v163, v166, v167\n" \
C_"v132, v136, v137\n" \
M_"v[176:179], a[176:179], v[72:75], v[176:179]\n" \
B_"a[204:207], v20, s[56:59], s60 offen offset:3072 sc1\n" \
M_"v[148:151], a[180:183], v[68:71], v[148:151]\n" \
C_"v133, v138, v139\n" \
C_"v134, v140, v141\n" \
M_"v[180:183], a[180:183], v[72:75], v[180:183]\n" \
C_"v135, v142, v143\n" \
C_"v164, v168, v169\n" \
M_"v[152:155], a[184:187], v[68:71], v[152:155]\n" \
C_"v165, v170, v171\n" \
C_"v166, v172, v173\n" \
M_"v[184:187], a[184:187], v[72:75], v[184:187]\n" \
C_"v167, v174, v175\n" \
C_"v136, v144, v145\n" \
M_"v[156:159], a[188:191], v[68:71], v[156:159]\n" \
C_"v137, v146, v147\n" \
C_"v138, v148, v149\n" \
M_"v[188:191], a[188:191], v[72:75], v[188:191]\n" \
C_"v139, v150, v151\n" \
C_"v140, v152, v153\n" \
"v_smfmac_f32_16x16x64_bf16 a[96:99], a[104:107], v[68:75], v21\n" \
C_"v141, v154, v155\n" \
C_"v168, v176, v177\n" \
M_"v[120:123], a[128:131], a[208:211], v[44:47]\n" \
C_"v142, v156, v157\n" \
C_"v143, v158, v159\n" \
M_"v[120:123], a[132:135], a[212:215], v[120:123]\n" \
C_"v169, v178, v179\n" \
C_"v170, v180, v181\n" \
C_"v171, v182, v183\n" \
C_"v172, v184, v185\n" \
C_"v173, v186, v187\n" \
C_"v174, v188, v189\n" \
C_"v175, v190, v191\n" \
"ds_write_b128 v50, v[128:131] offset:0\n" \
M_"v[120:123], a[136:139], a[216:219], v[120:123]\n" \
"v_and_b32_e32 v87, v15, v87\n" \
"v_accvgpr_read_b32 v40, a96\n" \
M_"v[120:123], a[140:143], a[220:223], v[120:123]\n" \
"ds_write_b128 v50, v[132:135] offset:1024\n" \
M_"v[124:127], a[156:159], a[220:223], v[44:47]\n" \
B_"a[220:223], v54, s[56:59], s60 offen offset:3072 sc1\n" \
M_"v[124:127], a[152:155], a[216:219], v[124:127]\n" \
"ds_write_b128 v50, v[136:139] offset:2048\n" \
M_"v[124:127], a[148:151], a[212:215], v[124:127]\n" \
"v_accvgpr_read_b32 v22, a97\n" \
"v_accvgpr_read_b32 v41, a98\n" \
M_"v[124:127], a[144:147], a[208:211], v[124:127]\n" \
"ds_write_b128 v50, v[140:143] offset:3072\n" \
M_"a[100:103], a[108:111], v[84:87], a[100:103]\n" \
"v_accvgpr_read_b32 v23, a99\n" \
"v_add_f32_e32 v40, v40, v22\n" \
M_"v[192:195], a[160:163], v[84:87], v[192:195]\n" \
"ds_write_b128 v51, v[160:163] offset:32768\n" \
M_"v[196:199], a[164:167], v[84:87], v[196:199]\n" \
B_"a[208:211], v54, s[56:59], s60 offen offset:0 sc1\n" \
M_"v[200:203], a[168:171], v[84:87], v[200:203]\n" \
"ds_write_b128 v51, v[164:167] offset:33792\n" \
"v_add_f32_e32 v41, v41, v23\n" \
"ds_write_b64 v19, v[40:41]\n" \
M_"v[204:207], a[172:175], v[84:87], v[204:207]\n" \
"v_lshl_add_u32 v52, s15, 2, v20\n" \
"v_add_u32_e32 v52, 0x1a000, v52\n" \
"ds_write_b128 v51, v[168:171] offset:34816\n" \
M_"v[208:211], a[176:179], v[84:87], v[208:211]\n" \
E_"v88, v88\n" \
M_"v[212:215], a[180:183], v[84:87], v[212:215]\n" \
"ds_write_b128 v51, v[172:175] offset:35840\n" \
M_"v[216:219], a[184:187], v[84:87], v[216:219]\n" \
B_"a[212:215], v54, s[56:59], s60 offen offset:1024 sc1\n" \
M_"v[220:223], a[188:191], v[84:87], v[220:223]\n" \
E_"v89, v89\n" \
E_"v90, v90\n" \
E_"v91, v91\n" \
C_"v88, v88, v89\n" \
E_"v92, v92\n" \
E_"v93, v93\n" \
E_"v94, v94\n" \
E_"v95, v95\n" \
C_"v89, v90, v91\n" \
C_"v90, v92, v93\n" \
C_"v91, v94, v95\n" \
"v_xor_b32_e32 v63, 0x400, v19\n" \
B_"a[216:219], v54, s[56:59], s60 offen offset:2048 sc1\n" \
E_"v120, v120\n" \
E_"v121, v121\n" \
E_"v122, v122\n" \
E_"v123, v123\n" \
C_"v120, v120, v121\n" \
E_"v124, v124\n" \
E_"v125, v125\n" \
"s_waitcnt lgkmcnt(0)\n" \
"s_barrier\n" \
R_"v[144:147], v52 offset:0\n" \
R_"v[148:151], v52 offset:1024\n" \
R_"v[152:155], v52 offset:2048\n" \
".fill (L_ab + 12288 - .) / 4, 4, 0xBF800000\n" \
"L_ap3:\n" \
"s_cbranch_execz L_ap0\n" \
R_"v[156:159], v52 offset:3072\n" \
R_"v[176:179], v52 offset:4096\n" \
R_"v[180:183], v52 offset:5120\n" \
R_"v[184:187], v52 offset:6144\n" \
R_"v[188:191], v52 offset:7168\n" \
M_"a[100:103], a[116:119], v[88:91], a[100:103]\n" \
"ds_read_b64 v[42:43], v63\n" \
C_"v121, v122, v123\n" \
M_"v[224:227], a[160:163], v[88:91], v[224:227]\n" \
E_"v126, v126\n" \
M_"v[228:231], a[164:167], v[88:91], v[228:231]\n" \
E_"v127, v127\n" \
M_"v[232:235], a[168:171], v[88:91], v[232:235]\n" \
C_"v122, v124, v125\n" \
C_"v123, v126, v127\n" \
M_"v[236:239], a[172:175], v[88:91], v[236:239]\n" \
"v_and_b32_e32 v120, v12, v120\n" \
"v_and_b32_e32 v121, v13, v121\n" \
M_"v[240:243], a[176:179], v[88:91], v[240:243]\n" \
"v_and_b32_e32 v122, v14, v122\n" \
"v_and_b32_e32 v123, v15, v123\n" \
M_"v[244:247], a[180:183], v[88:91], v[244:247]\n" \
"v_readlane_b32 s93, v11, 25\n" \
"v_readlane_b32 s92, v11, 30\n" \
M_"v[248:251], a[184:187], v[88:91], v[248:251]\n" \
"s_add_u32 m0, s93, 0x0\n" \
"v_mov_b32_e32 v130, v28\n" \
M_"v[252:255], a[188:191], v[88:91], v[252:255]\n" \
"v_mov_b32_e32 v131, v29\n" \
"v_mov_b32_e32 v132, 0\n" \
"v_mov_b32_e32 v133, 0\n" \
"v_mov_b32_e32 v128, 0\n" \
"v_mov_b32_e32 v129, 0\n" \
"s_waitcnt vmcnt(8) lgkmcnt(1)\n" \
"s_barrier\n" \
M_"a[100:103], a[116:119], v[120:123], a[100:103]\n" \
B_"v0, s[80:83], s92 offen offset:0 lds\n" \
M_"v[224:227], a[0:3], v[120:123], v[224:227]\n" \
M_"v[228:231], a[4:7], v[120:123], v[228:231]\n" \
M_"v[232:235], a[8:11], v[120:123], v[232:235]\n" \
M_"v[236:239], a[12:15], v[120:123], v[236:239]\n" \
M_"v[240:243], a[16:19], v[120:123], v[240:243]\n" \
M_"v[244:247], a[20:23], v[120:123], v[244:247]\n" \
B_"v1, s[80:83], s92 offen offset:1024 lds\n" \
M_"v[248:251], a[24:27], v[120:123], v[248:251]\n" \
"v_accvgpr_read_b32 v56, a100\n" \
"v_accvgpr_read_b32 v24, a101\n" \
M_"v[252:255], a[28:31], v[120:123], v[252:255]\n" \
"v_add_f32_e32 v56, v56, v24\n" \
"s_waitcnt lgkmcnt(0)\n" \
"v_add_f32_e32 v56, v56, v42\n" \
M_"v[192:195], v[130:133], v[144:147], v[192:195]\n" \
"v_max_f32_e32 v62, v62, v56\n" \
"v_accvgpr_read_b32 v57, a102\n" \
M_"v[196:199], v[128:131], v[144:147], v[196:199]\n" \
"v_rcp_f32_e32 v56, v56\n" \
M_"v[200:203], v[130:133], v[148:151], v[200:203]\n" \
"v_mul_f32_e32 v56, v56, v60\n" \
"v_accvgpr_read_b32 v25, a103\n" \
B_"v2, s[80:83], s92 offen offset:2048 lds\n" \
M_"v[204:207], v[128:131], v[148:151], v[204:207]\n" \
"v_mul_f32_e32 v192, v56, v192\n" \
"v_mul_f32_e32 v193, v56, v193\n" \
M_"v[208:211], v[130:133], v[152:155], v[208:211]\n" \
"v_mul_f32_e32 v194, v56, v194\n" \
"v_mul_f32_e32 v195, v56, v195\n" \
M_"v[212:215], v[128:131], v[152:155], v[212:215]\n" \
"v_mul_f32_e32 v196, v56, v196\n" \
"v_mul_f32_e32 v197, v56, v197\n" \
M_"v[216:219], v[130:133], v[156:159], v[216:219]\n" \
"v_mul_f32_e32 v198, v56, v198\n" \
"v_mul_f32_e32 v199, v56, v199\n" \
M_"v[220:223], v[128:131], v[156:159], v[220:223]\n" \
C_"v64, v192, v193\n" \
C_"v65, v194, v195\n" \
M_"v[224:227], v[130:133], v[176:179], v[224:227]\n" \
B_"v3, s[80:83], s92 offen offset:3072 lds\n" \
"s_add_u32 m0, s93, 0x1000\n" \
M_"v[228:231], v[128:131], v[176:179], v[228:231]\n" \
C_"v66, v196, v197\n" \
C_"v67, v198, v199\n" \
M_"v[232:235], v[130:133], v[180:183], v[232:235]\n" \
"v_mul_f32_e32 v200, v56, v200\n" \
"v_mul_f32_e32 v201, v56, v201\n" \
M_"v[236:239], v[128:131], v[180:183], v[236:239]\n" \
M_"a[112:115], a[120:123], v[64:67], a[112:115]\n" \
"v_mul_f32_e32 v202, v56, v202\n" \
"v_mul_f32_e32 v203, v56, v203\n" \
M_"v[240:243], v[130:133], v[184:187], v[240:243]\n" \
"v_mul_f32_e32 v204, v56, v204\n" \
"v_mul_f32_e32 v205, v56, v205\n" \
B_"v4, s[80:83], s92 offen offset:0 lds\n" \
"v_mul_f32_e32 v206, v56, v206\n" \
"v_mul_f32_e32 v207, v56, v207\n" \
"v_add_f32_e32 v57, v57, v25\n" \
C_"v68, v200, v201\n" \
C_"v69, v202, v203\n" \
C_"v70, v204, v205\n" \
C_"v71, v206, v207\n" \
"v_add_f32_e32 v57, v57, v43\n" \
"v_mul_f32_e32 v208, v56, v208\n" \
M_"a[112:115], a[120:123], v[68:71], a[112:115]\n" \
"v_mul_f32_e32 v209, v56, v209\n" \
"v_mul_f32_e32 v210, v56, v210\n" \
"v_mul_f32_e32 v211, v56, v211\n" \
"v_mul_f32_e32 v212, v56, v212\n" \
"v_mul_f32_e32 v213, v56, v213\n" \
"v_mul_f32_e32 v214, v56, v214\n" \
"v_mul_f32_e32 v215, v56, v215\n" \
"v_max_f32_e32 v62, v62, v57\n" \
C_"v72, v208, v209\n" \
C_"v73, v210, v211\n" \
B_"v5, s[80:83], s92 offen offset:1024 lds\n" \
C_"v74, v212, v213\n" \
C_"v75, v214, v215\n" \
"v_rcp_f32_e32 v57, v57\n" \
M_"v[244:247], v[128:131], v[184:187], v[244:247]\n" \
M_"a[112:115], a[120:123], v[72:75], a[112:115]\n" \
"v_mul_f32_e32 v216, v56, v216\n" \
"v_mul_f32_e32 v217, v56, v217\n" \
"v_mul_f32_e32 v218, v56, v218\n" \
"v_mul_f32_e32 v219, v56, v219\n" \
"v_mul_f32_e32 v220, v56, v220\n" \
"v_mul_f32_e32 v221, v56, v221\n" \
"v_mul_f32_e32 v222, v56, v222\n" \
"v_mul_f32_e32 v223, v56, v223\n" \
C_"v76, v216, v217\n" \
C_"v77, v218, v219\n" \
C_"v78, v220, v221\n" \
B_"v6, s[80:83], s92 offen offset:2048 lds\n" \
C_"v79, v222, v223\n" \
"v_mul_f32_e32 v57, v57, v61\n" \
"v_mul_f32_e32 v224, v57, v224\n" \
M_"a[112:115], a[120:123], v[76:79], a[112:115]\n" \
"v_mul_f32_e32 v225, v57, v225\n" \
"v_mul_f32_e32 v226, v57, v226\n" \
"v_mul_f32_e32 v227, v57, v227\n" \
"v_mul_f32_e32 v228, v57, v228\n" \
"v_mul_f32_e32 v229, v57, v229\n" \
"v_mul_f32_e32 v230, v57, v230\n" \
"v_mul_f32_e32 v231, v57, v231\n" \
C_"v80, v224, v225\n" \
C_"v81, v226, v227\n" \
C_"v82, v228, v229\n" \
C_"v83, v230, v231\n" \
M_"v[248:251], v[130:133], v[188:191], v[248:251]\n" \
"v_mul_f32_e32 v232, v57, v232\n" \
"v_mul_f32_e32 v233, v57, v233\n" \
M_"a[112:115], a[120:123], v[80:83], a[112:115]\n" \
B_"v7, s[80:83], s92 offen offset:3072 lds\n" \
"s_add_u32 m0, s93, 0x2000\n" \
"v_mul_f32_e32 v234, v57, v234\n" \
"v_mul_f32_e32 v235, v57, v235\n" \
"v_mul_f32_e32 v236, v57, v236\n" \
"v_mul_f32_e32 v237, v57, v237\n" \
"v_mul_f32_e32 v238, v57, v238\n" \
"v_mul_f32_e32 v239, v57, v239\n" \
C_"v84, v232, v233\n" \
C_"v85, v234, v235\n" \
C_"v86, v236, v237\n" \
C_"v87, v238, v239\n" \
"v_mul_f32_e32 v240, v57, v240\n" \
"v_mul_f32_e32 v241, v57, v241\n" \
M_"a[112:115], a[120:123], v[84:87], a[112:115]\n" \
"v_mul_f32_e32 v242, v57, v242\n" \
"v_mul_f32_e32 v243, v57, v243\n" \
"v_mul_f32_e32 v244, v57, v244\n" \
"v_mul_f32_e32 v245, v57, v245\n" \
"v_mul_f32_e32 v246, v57, v246\n" \
"v_mul_f32_e32 v247, v57, v247\n" \
B_"v8, s[80:83], s92 offen offset:0 lds\n" \
C_"v96, v240, v241\n" \
C_"v97, v242, v243\n" \
C_"v98, v244, v245\n" \
C_"v99, v246, v247\n" \
M_"v[252:255], v[128:131], v[188:191], v[252:255]\n" \
"v_mul_f32_e32 v248, v57, v248\n" \
M_"a[112:115], a[120:123], v[96:99], a[112:115]\n" \
"v_mul_f32_e32 v249, v57, v249\n" \
"v_mul_f32_e32 v250, v57, v250\n" \
"v_mul_f32_e32 v251, v57, v251\n" \
C_"v100, v248, v249\n" \
C_"v101, v250, v251\n" \
"v_cmp_lt_f32_e64 s[72:73], s21, v62\n" \
"v_mul_f32_e32 v252, v57, v252\n" \
"v_mul_f32_e32 v253, v57, v253\n" \
"v_mul_f32_e32 v254, v57, v254\n" \
"v_mul_f32_e32 v255, v57, v255\n" \
C_"v102, v252, v253\n" \
C_"v103, v254, v255\n" \
"s_nop 1\n" \
M_"a[112:115], a[120:123], v[100:103], a[112:115]\n" \
"s_nop 7\n" \
"v_accvgpr_read_b32 v63, a112\n" \
"v_cmp_u_f32_e32 vcc, v63, v63\n" \
"s_nop 0\n" \
"s_or_b64 vcc, s[72:73], vcc\n" \
"s_cbranch_scc1 L_end_%=\n" \
"s_branch L_out_%=\n" \
"L_skb_%=:\n" \
"s_sleep 1\n" \
"buffer_load_dword v57, v61, s[56:59], s96 offen sc1\n" \
"s_add_u32 s101, s101, 1\n" \
"s_waitcnt vmcnt(0)\n" \
"v_cmp_ne_u32_e32 vcc, s62, v57\n" \
"s_cmp_gt_u32 s101, 0x100000\n" \
"s_cbranch_scc1 L_rkb_%=\n" \
"s_nop 2\n" \
"s_cbranch_vccnz L_skb_%=\n" \
"s_branch L_rkb_%=\n" \
"L_sol_%=:\n" \
"s_sleep 1\n" \
"buffer_load_dwordx2 v[26:27], v16, s[56:59], s96 offen sc1\n" \
"s_add_u32 s101, s101, 1\n" \
"s_waitcnt vmcnt(0)\n" \
"v_cmp_ne_u64_e32 vcc, s[62:63], v[26:27]\n" \
"s_cmp_gt_u32 s101, 0x100000\n" \
"s_cbranch_scc1 L_rol_%=\n" \
"s_nop 2\n" \
"s_cbranch_vccnz L_sol_%=\n" \
"s_branch L_rol_%=\n" \
"L_sos_%=:\n" \
"s_sleep 1\n" \
"buffer_load_dwordx2 v[26:27], v16, s[56:59], s96 offen sc1\n" \
"s_add_u32 s101, s101, 1\n" \
"s_waitcnt vmcnt(0)\n" \
"v_cmp_ne_u64_e32 vcc, s[62:63], v[26:27]\n" \
"s_cmp_gt_u32 s101, 0x100000\n" \
"s_cbranch_scc1 L_ros_%=\n" \
"s_nop 2\n" \
"s_cbranch_vccnz L_sos_%=\n" \
"s_branch L_ros_%=\n" \
"L_sa_%=:\n" \
"s_sleep 1\n" \
"buffer_load_dword v26, v38, s[56:59], s64 offen sc1\n" \
"s_add_u32 s101, s101, 1\n" \
"s_waitcnt vmcnt(0)\n" \
"v_cmp_ne_u32_e32 vcc, s62, v26\n" \
"s_cmp_gt_u32 s101, 0x100000\n" \
"s_cbranch_scc1 L_ra_%=\n" \
"s_nop 2\n" \
"s_cbranch_vccnz L_sa_%=\n" \
"s_branch L_ra_%=\n" \
"L_sb_%=:\n" \
"s_sleep 1\n" \
"buffer_load_dword v47, v38, s[56:59], s71 offen sc1\n" \
"s_add_u32 s101, s101, 1\n" \
"s_waitcnt vmcnt(0)\n" \
"v_cmp_ne_u32_e32 vcc, s62, v47\n" \
"s_cmp_gt_u32 s101, 0x100000\n" \
"s_cbranch_scc1 L_rb_%=\n" \
"s_nop 2\n" \
"s_cbranch_vccnz L_sb_%=\n" \
"s_branch L_rb_%=\n" \
"L_sna_%=:\n" \
"buffer_load_dword v37, v40, s[56:59], s74 offen sc1\n" \
"s_add_u32 s101, s101, 1\n" \
"s_waitcnt vmcnt(0)\n" \
"v_cmp_eq_u32_e64 s[98:99], s62, v37\n" \
"s_cmp_gt_u32 s101, 0x100000\n" \
"s_cbranch_scc1 L_rna_%=\n" \
"s_andn2_b32 s75, s20, s98\n" \
"s_cbranch_scc0 L_gna_%=\n" \
"s_sleep 1\n" \
"s_branch L_sna_%=\n" \
"L_gna_%=:\n" \
"s_bitcmp1_b32 s20, 0\n" \
"s_cbranch_scc0 L_kna0_%=\n" \
B_"v[116:119], v20, s[56:59], s12 offen offset:0 sc1\n" \
"L_kna0_%=:\n" \
"s_bitcmp1_b32 s20, 1\n" \
"s_cbranch_scc0 L_kna1_%=\n" \
B_"v[124:127], v20, s[56:59], s12 offen offset:2048 sc1\n" \
"L_kna1_%=:\n" \
"s_bitcmp1_b32 s20, 2\n" \
"s_cbranch_scc0 L_kna2_%=\n" \
B_"v[132:135], v20, s[56:59], s13 offen offset:0 sc1\n" \
"L_kna2_%=:\n" \
"s_bitcmp1_b32 s20, 3\n" \
"s_cbranch_scc0 L_kna3_%=\n" \
B_"v[140:143], v20, s[56:59], s13 offen offset:2048 sc1\n" \
"L_kna3_%=:\n" \
"s_bitcmp1_b32 s20, 4\n" \
"s_cbranch_scc0 L_kna4_%=\n" \
B_"v[148:151], v20, s[56:59], s14 offen offset:0 sc1\n" \
"L_kna4_%=:\n" \
"s_bitcmp1_b32 s20, 5\n" \
"s_cbranch_scc0 L_kna5_%=\n" \
B_"v[156:159], v20, s[56:59], s14 offen offset:2048 sc1\n" \
"L_kna5_%=:\n" \
"s_bitcmp1_b32 s20, 6\n" \
"s_cbranch_scc0 L_kna6_%=\n" \
B_"v[164:167], v20, s[56:59], s15 offen offset:0 sc1\n" \
"L_kna6_%=:\n" \
"s_waitcnt vmcnt(0)\n" \
"s_branch L_rna_%=\n" \
"L_snx_%=:\n" \
"buffer_load_dword v37, v40, s[56:59], s74 offen sc1\n" \
"s_add_u32 s101, s101, 1\n" \
"s_waitcnt vmcnt(0)\n" \
"v_cmp_eq_u32_e64 s[98:99], s62, v37\n" \
"s_cmp_gt_u32 s101, 0x100000\n" \
"s_cbranch_scc1 L_rnx_%=\n" \
"s_andn2_b32 s75, s21, s98\n" \
"s_cbranch_scc0 L_gnx_%=\n" \
"s_sleep 1\n" \
"s_branch L_snx_%=\n" \
"L_gnx_%=:\n" \
"s_bitcmp1_b32 s21, 16\n" \
"s_cbranch_scc0 L_knx16_%=\n" \
B_"v[112:115], v20, s[56:59], s16 offen offset:0 sc1\n" \
"L_knx16_%=:\n" \
"s_bitcmp1_b32 s21, 17\n" \
"s_cbranch_scc0 L_knx17_%=\n" \
B_"v[184:187], v20, s[56:59], s16 offen offset:2048 sc1\n" \
"L_knx17_%=:\n" \
"s_bitcmp1_b32 s21, 18\n" \
"s_cbranch_scc0 L_knx18_%=\n" \
B_"v[120:123], v20, s[56:59], s17 offen offset:0 sc1\n" \
"L_knx18_%=:\n" \
"s_bitcmp1_b32 s21, 19\n" \
"s_cbranch_scc0 L_knx19_%=\n" \
B_"v[192:195], v20, s[56:59], s17 offen offset:2048 sc1\n" \
"L_knx19_%=:\n" \
"s_bitcmp1_b32 s21, 20\n" \
"s_cbranch_scc0 L_knx20_%=\n" \
B_"v[128:131], v20, s[56:59], s18 offen offset:0 sc1\n" \
"L_knx20_%=:\n" \
"s_bitcmp1_b32 s21, 21\n" \
"s_cbranch_scc0 L_knx21_%=\n" \
B_"v[200:203], v20, s[56:59], s18 offen offset:2048 sc1\n" \
"L_knx21_%=:\n" \
"s_bitcmp1_b32 s21, 22\n" \
"s_cbranch_scc0 L_knx22_%=\n" \
B_"v[176:179], v20, s[56:59], s19 offen offset:0 sc1\n" \
"L_knx22_%=:\n" \
"s_bitcmp1_b32 s21, 23\n" \
"s_cbranch_scc0 L_knx23_%=\n" \
B_"v[248:251], v20, s[56:59], s19 offen offset:2048 sc1\n" \
"L_knx23_%=:\n" \
"s_bitcmp1_b32 s21, 8\n" \
"s_cbranch_scc0 L_knx8_%=\n" \
B_"v[188:191], v20, s[56:59], s12 offen offset:1024 sc1\n" \
"L_knx8_%=:\n" \
"s_bitcmp1_b32 s21, 9\n" \
"s_cbranch_scc0 L_knx9_%=\n" \
B_"v[196:199], v20, s[56:59], s12 offen offset:3072 sc1\n" \
"L_knx9_%=:\n" \
"s_waitcnt vmcnt(0)\n" \
"s_branch L_rnx_%=\n" \
"L_sx0_%=:\n" \
"s_sleep 1\n" \
"buffer_load_dword v37, v40, s[56:59], s74 offen sc1\n" \
"s_add_u32 s101, s101, 1\n" \
"s_waitcnt vmcnt(0)\n" \
"v_cmp_eq_u32_e64 s[98:99], s62, v37\n" \
"s_cmp_gt_u32 s101, 0x100000\n" \
"s_cbranch_scc1 L_rx0_%=\n" \
"s_andn2_b32 s75, 0x400, s98\n" \
"s_cbranch_scc1 L_sx0_%=\n" \
"s_branch L_rx0_%=\n" \
"L_sx1_%=:\n" \
"s_sleep 1\n" \
"buffer_load_dword v37, v40, s[56:59], s74 offen sc1\n" \
"s_add_u32 s101, s101, 1\n" \
"s_waitcnt vmcnt(0)\n" \
"v_cmp_eq_u32_e64 s[98:99], s62, v37\n" \
"s_cmp_gt_u32 s101, 0x100000\n" \
"s_cbranch_scc1 L_rx1_%=\n" \
"s_andn2_b32 s75, 0x800, s98\n" \
"s_cbranch_scc1 L_sx1_%=\n" \
"s_branch L_rx1_%=\n" \
"L_sx2_%=:\n" \
"s_sleep 1\n" \
"buffer_load_dword v37, v40, s[56:59], s74 offen sc1\n" \
"s_add_u32 s101, s101, 1\n" \
"s_waitcnt vmcnt(0)\n" \
"v_cmp_eq_u32_e64 s[98:99], s62, v37\n" \
"s_cmp_gt_u32 s101, 0x100000\n" \
"s_cbranch_scc1 L_rx2_%=\n" \
"s_andn2_b32 s75, 0x1000, s98\n" \
"s_cbranch_scc1 L_sx2_%=\n" \
"s_branch L_rx2_%=\n" \
"L_sx3_%=:\n" \
"s_sleep 1\n" \
"buffer_load_dword v37, v40, s[56:59], s74 offen sc1\n" \
"s_add_u32 s101, s101, 1\n" \
"s_waitcnt vmcnt(0)\n" \
"v_cmp_eq_u32_e64 s[98:99], s62, v37\n" \
"s_cmp_gt_u32 s101, 0x100000\n" \
"s_cbranch_scc1 L_rx3_%=\n" \
"s_andn2_b32 s75, 0x2000, s98\n" \
"s_cbranch_scc1 L_sx3_%=\n" \
"s_branch L_rx3_%=\n" \
"L_sx4_%=:\n" \
"s_sleep 1\n" \
"buffer_load_dword v37, v40, s[56:59], s74 offen sc1\n" \
"s_add_u32 s101, s101, 1\n" \
"s_waitcnt vmcnt(0)\n" \
"v_cmp_eq_u32_e64 s[98:99], s62, v37\n" \
"s_cmp_gt_u32 s101, 0x100000\n" \
"s_cbranch_scc1 L_rx4_%=\n" \
"s_andn2_b32 s75, 0x4000, s98\n" \
"s_cbranch_scc1 L_sx4_%=\n" \
"s_branch L_rx4_%=\n" \
"L_att_stub:\n" \
"s_mov_b32 s101, 0\n" \
"v_add_u32_e32 v32, 0x49000, v20\n" \
"v_readlane_b32 s96, v11, 39\n" \
"v_readlane_b32 s97, v11, 40\n" \
B_"a[192:195], v20, s[56:59], s60 offen offset:0 sc1\n" \
B_"a[208:211], v32, s[56:59], s60 offen offset:0 sc1\n" \
B_"a[196:199], v20, s[56:59], s60 offen offset:1024 sc1\n" \
B_"a[212:215], v32, s[56:59], s60 offen offset:1024 sc1\n" \
B_"a[200:203], v20, s[56:59], s60 offen offset:2048 sc1\n" \
B_"a[216:219], v32, s[56:59], s60 offen offset:2048 sc1\n" \
B_"a[204:207], v20, s[56:59], s60 offen offset:3072 sc1\n" \
B_"a[220:223], v32, s[56:59], s60 offen offset:3072 sc1\n" \
B_"a[124:127], v20, s[56:59], s96 offen offset:0 sc1\n" \
B_"a[228:231], v32, s[56:59], s96 offen offset:0 sc1\n" \
B_"a[236:239], v20, s[56:59], s97 offen offset:0 sc1\n" \
B_"a[244:247], v32, s[56:59], s97 offen offset:0 sc1\n" \
B_"a[224:227], v20, s[56:59], s96 offen offset:1024 sc1\n" \
B_"a[232:235], v32, s[56:59], s96 offen offset:1024 sc1\n" \
B_"a[240:243], v20, s[56:59], s97 offen offset:1024 sc1\n" \
B_"a[248:251], v32, s[56:59], s97 offen offset:1024 sc1\n" \
B_"v[64:67], v20, s[56:59], s61 offen offset:0 sc1\n" \
B_"v[68:71], v20, s[56:59], s61 offen offset:1024 sc1\n" \
B_"v[72:75], v20, s[56:59], s61 offen offset:2048 sc1\n" \
B_"v[76:79], v20, s[56:59], s61 offen offset:3072 sc1\n" \
B_"v[80:83], v32, s[56:59], s61 offen offset:0 sc1\n" \
B_"v[84:87], v32, s[56:59], s61 offen offset:1024 sc1\n" \
B_"v[96:99], v32, s[56:59], s61 offen offset:2048 sc1\n" \
B_"v[100:103], v32, s[56:59], s61 offen offset:3072 sc1\n" \
"v_not_b32_e32 v26, s62\n" \
"s_waitcnt vmcnt(0)\n" \
"s_branch L_out_%=\n" \
".fill (L_ab + 16384 - .) / 4, 4, 0xBF800000\n" \
".fill (L_ab + 16384 - .) / 4, 4, 0xBF800000\n" \
"L_ap4:\n" \
"s_setpc_b64 s[88:89]\n" \
"L_gsw:\n" \
M_"v[104:107], a[128:131], a[16:19], v[36:39]\n" \
"s_add_u32 s68, s60, 0xfff6e000\n" \
"s_add_u32 s69, s60, 0xfffb7000\n" \
"s_add_u32 s70, s60, 0x0\n" \
"s_add_u32 s71, s60, 0x49000\n" \
M_"v[104:107], a[132:135], a[20:23], v[104:107]\n" \
"s_xor_b32 m0, s65, 0x12000\n" \
R_"a[160:163], v49 offset:0\n" \
M_"v[104:107], a[136:139], a[24:27], v[104:107]\n" \
B_"v50, s[8:11], s64 offen offset:0 sc1 lds\n" \
M_"v[104:107], a[140:143], a[28:31], v[104:107]\n" \
R_"a[164:167], v49 offset:1024\n" \
R_"a[168:171], v49 offset:2048\n" \
M_"v[108:111], a[156:159], a[28:31], v[36:39]\n" \
R_"a[172:175], v49 offset:3072\n" \
R_"a[176:179], v49 offset:4096\n" \
M_"v[108:111], a[152:155], a[24:27], v[108:111]\n" \
B_"v50, s[8:11], s64 offen offset:1024 sc1 lds\n" \
M_"v[108:111], a[148:151], a[20:23], v[108:111]\n" \
"s_mov_b32 s65, s13\n" \
"v_add_u32_e32 v63, s15, v20\n" \
M_"v[108:111], a[144:147], a[16:19], v[108:111]\n" \
"s_add_u32 s67, s14, 40960\n" \
"v_add_u32_e32 v50, 0x6000, v63\n" \
M_"v[116:119], a[144:147], a[32:35], v[40:43]\n" \
"s_xor_b32 m0, s65, 0x8000\n" \
R_"a[180:183], v49 offset:5120\n" \
M_"v[116:119], a[148:151], a[36:39], v[116:119]\n" \
R_"a[184:187], v49 offset:6144\n" \
"v_and_b32_e32 v68, v12, v68\n" \
M_"v[116:119], a[152:155], a[40:43], v[116:119]\n" \
"v_and_b32_e32 v69, v13, v69\n" \
"v_and_b32_e32 v70, v14, v70\n" \
M_"v[116:119], a[156:159], a[44:47], v[116:119]\n" \
B_"v50, s[8:11], s67 offen offset:0 sc1 lds\n" \
M_"v[112:115], a[140:143], a[44:47], v[40:43]\n" \
"v_and_b32_e32 v71, v15, v71\n" \
R_"a[188:191], v49 offset:7168\n" \
M_"v[112:115], a[136:139], a[40:43], v[112:115]\n" \
E_"v86, v86\n" \
M_"v[112:115], a[132:135], a[36:39], v[112:115]\n" \
E_"v87, v87\n" \
M_"v[112:115], a[128:131], a[32:35], v[112:115]\n" \
E_"v84, v84\n" \
M_"v[120:123], a[128:131], a[48:51], v[44:47]\n" \
B_"v50, s[8:11], s67 offen offset:1024 sc1 lds\n" \
M_"v[120:123], a[132:135], a[52:55], v[120:123]\n" \
E_"v85, v85\n" \
M_"v[120:123], a[136:139], a[56:59], v[120:123]\n" \
C_"v87, v86, v87\n" \
C_"v86, v84, v85\n" \
M_"v[120:123], a[140:143], a[60:63], v[120:123]\n" \
E_"v82, v82\n" \
M_"v[124:127], a[156:159], a[60:63], v[44:47]\n" \
E_"v83, v83\n" \
M_"v[124:127], a[152:155], a[56:59], v[124:127]\n" \
E_"v80, v80\n" \
M_"v[124:127], a[148:151], a[52:55], v[124:127]\n" \
B_"v50, s[8:11], s67 offen offset:2048 sc1 lds\n" \
M_"v[124:127], a[144:147], a[48:51], v[124:127]\n" \
C_"v85, v82, v83\n" \
"s_waitcnt lgkmcnt(1)\n" \
M_"v[128:131], a[160:163], v[68:71], v[128:131]\n" \
E_"v81, v81\n" \
M_"v[160:163], a[160:163], v[72:75], v[160:163]\n" \
C_"v84, v80, v81\n" \
"s_mov_b32 s64, s14\n" \
M_"v[132:135], a[164:167], v[68:71], v[132:135]\n" \
E_"v88, v88\n" \
M_"v[164:167], a[164:167], v[72:75], v[164:167]\n" \
E_"v89, v89\n" \
M_"v[136:139], a[168:171], v[68:71], v[136:139]\n" \
B_"v50, s[8:11], s67 offen offset:3072 sc1 lds\n" \
M_"v[168:171], a[168:171], v[72:75], v[168:171]\n" \
E_"v90, v90\n" \
M_"v[140:143], a[172:175], v[68:71], v[140:143]\n" \
E_"v91, v91\n" \
M_"v[172:175], a[172:175], v[72:75], v[172:175]\n" \
C_"v88, v88, v89\n" \
C_"v89, v90, v91\n" \
M_"v[144:147], a[176:179], v[68:71], v[144:147]\n" \
E_"v92, v92\n" \
M_"v[176:179], a[176:179], v[72:75], v[176:179]\n" \
E_"v93, v93\n" \
M_"v[148:151], a[180:183], v[68:71], v[148:151]\n" \
E_"v94, v94\n" \
M_"v[180:183], a[180:183], v[72:75], v[180:183]\n" \
E_"v95, v95\n" \
M_"v[152:155], a[184:187], v[68:71], v[152:155]\n" \
C_"v90, v92, v93\n" \
C_"v91, v94, v95\n" \
M_"v[184:187], a[184:187], v[72:75], v[184:187]\n" \
"v_add_u32_e32 v51, 0x12000, v63\n" \
"s_waitcnt lgkmcnt(0)\n" \
M_"v[156:159], a[188:191], v[68:71], v[156:159]\n" \
"s_xor_b32 m0, s65, 0xa000\n" \
"ds_read_b32 v58, v18 offset:512\n" \
M_"v[188:191], a[188:191], v[72:75], v[188:191]\n" \
B_"v51, s[8:11], s64 offen offset:0 sc1 lds\n" \
"v_smfmac_f32_16x16x64_bf16 a[96:99], a[104:107], v[68:75], v21\n" \
B_"v51, s[8:11], s64 offen offset:1024 sc1 lds\n" \
M_"v[220:223], a[188:191], v[84:87], v[220:223]\n" \
"ds_read_b32 v59, v18 offset:640\n" \
"ds_read_b32 v60, v18 offset:768\n" \
M_"v[252:255], a[188:191], v[88:91], v[252:255]\n" \
"ds_read_b32 v61, v18 offset:896\n" \
R_"a[128:131], v48 offset:0\n" \
M_"v[216:219], a[184:187], v[84:87], v[216:219]\n" \
R_"a[144:147], v48 offset:4096\n" \
R_"a[132:135], v48 offset:1024\n" \
M_"v[248:251], a[184:187], v[88:91], v[248:251]\n" \
R_"a[148:151], v48 offset:5120\n" \
R_"a[136:139], v48 offset:2048\n" \
M_"v[212:215], a[180:183], v[84:87], v[212:215]\n" \
R_"a[152:155], v48 offset:6144\n" \
R_"a[140:143], v48 offset:3072\n" \
M_"v[244:247], a[180:183], v[88:91], v[244:247]\n" \
R_"a[156:159], v48 offset:7168\n" \
"v_add_u32_e32 v52, 0x14000, v63\n" \
M_"v[208:211], a[176:179], v[84:87], v[208:211]\n" \
B_"v51, s[8:11], s64 offen offset:2048 sc1 lds\n" \
M_"v[240:243], a[176:179], v[88:91], v[240:243]\n" \
E_"v104, v104\n" \
M_"v[204:207], a[172:175], v[84:87], v[204:207]\n" \
E_"v105, v105\n" \
M_"v[236:239], a[172:175], v[88:91], v[236:239]\n" \
B_"v51, s[8:11], s64 offen offset:3072 sc1 lds\n" \
M_"v[200:203], a[168:171], v[84:87], v[200:203]\n" \
E_"v106, v106\n" \
M_"v[232:235], a[168:171], v[88:91], v[232:235]\n" \
E_"v107, v107\n" \
M_"v[196:199], a[164:167], v[84:87], v[196:199]\n" \
C_"v104, v104, v105\n" \
C_"v105, v106, v107\n" \
M_"v[228:231], a[164:167], v[88:91], v[228:231]\n" \
E_"v108, v108\n" \
M_"v[192:195], a[160:163], v[84:87], v[192:195]\n" \
E_"v109, v109\n" \
M_"v[224:227], a[160:163], v[88:91], v[224:227]\n" \
E_"v110, v110\n" \
"v_smfmac_f32_16x16x64_bf16 a[100:103], a[104:107], v[84:91], v21\n" \
E_"v111, v111\n" \
C_"v106, v108, v109\n" \
C_"v107, v110, v111\n" \
"v_and_b32_e32 v104, v12, v104\n" \
"v_and_b32_e32 v105, v13, v105\n" \
"v_and_b32_e32 v106, v14, v106\n" \
"v_and_b32_e32 v107, v15, v107\n" \
"v_accvgpr_read_b32 v54, a96\n" \
"v_accvgpr_read_b32 v22, a97\n" \
"v_add_u32_e32 v53, 0x20000, v63\n" \
E_"v118, v118\n" \
E_"v119, v119\n" \
E_"v116, v116\n" \
E_"v117, v117\n" \
C_"v119, v118, v119\n" \
E_"v114, v114\n" \
E_"v115, v115\n" \
E_"v112, v112\n" \
E_"v113, v113\n" \
C_"v118, v116, v117\n" \
C_"v117, v114, v115\n" \
C_"v116, v112, v113\n" \
"v_readlane_b32 s93, v11, 22\n" \
"s_waitcnt vmcnt(10) lgkmcnt(0)\n" \
"s_barrier\n" \
M_"v[80:83], a[128:131], a[32:35], v[40:43]\n" \
R_"a[160:163], v49 offset:8192\n" \
R_"a[164:167], v49 offset:9216\n" \
M_"v[80:83], a[132:135], a[36:39], v[80:83]\n" \
R_"a[168:171], v49 offset:10240\n" \
"s_xor_b32 m0, s65, 0x18000\n" \
M_"v[80:83], a[136:139], a[40:43], v[80:83]\n" \
B_"v52, s[8:11], s64 offen offset:0 sc1 lds\n" \
M_"v[80:83], a[140:143], a[44:47], v[80:83]\n" \
R_"a[172:175], v49 offset:11264\n" \
R_"a[176:179], v49 offset:12288\n" \
M_"v[84:87], a[156:159], a[44:47], v[40:43]\n" \
R_"a[180:183], v49 offset:13312\n" \
R_"a[184:187], v49 offset:14336\n" \
M_"v[84:87], a[152:155], a[40:43], v[84:87]\n" \
B_"v52, s[8:11], s64 offen offset:1024 sc1 lds\n" \
M_"v[84:87], a[148:151], a[36:39], v[84:87]\n" \
R_"a[188:191], v49 offset:15360\n" \
"v_readlane_b32 s80, v11, 11\n" \
M_"v[84:87], a[144:147], a[32:35], v[84:87]\n" \
B_"v52, s[8:11], s64 offen offset:2048 sc1 lds\n" \
M_"v[92:95], a[144:147], a[48:51], v[44:47]\n" \
E_"v120, v120\n" \
M_"v[92:95], a[148:151], a[52:55], v[92:95]\n" \
E_"v121, v121\n" \
M_"v[92:95], a[152:155], a[56:59], v[92:95]\n" \
B_"v52, s[8:11], s64 offen offset:3072 sc1 lds\n" \
M_"v[92:95], a[156:159], a[60:63], v[92:95]\n" \
"s_mov_b32 m0, s65\n" \
C_"v120, v120, v121\n" \
M_"v[88:91], a[140:143], a[60:63], v[44:47]\n" \
E_"v122, v122\n" \
M_"v[88:91], a[136:139], a[56:59], v[88:91]\n" \
B_"v53, s[8:11], s64 offen offset:0 sc1 lds\n" \
M_"v[88:91], a[132:135], a[52:55], v[88:91]\n" \
E_"v123, v123\n" \
M_"v[88:91], a[128:131], a[48:51], v[88:91]\n" \
E_"v124, v124\n" \
M_"a[96:99], a[116:119], v[104:107], a[96:99]\n" \
C_"v121, v122, v123\n" \
"s_waitcnt lgkmcnt(5)\n" \
M_"v[160:163], a[160:163], v[104:107], v[160:163]\n" \
B_"v53, s[8:11], s64 offen offset:1024 sc1 lds\n" \
M_"v[164:167], a[164:167], v[104:107], v[164:167]\n" \
E_"v125, v125\n" \
M_"v[168:171], a[168:171], v[104:107], v[168:171]\n" \
C_"v122, v124, v125\n" \
"s_waitcnt lgkmcnt(1)\n" \
M_"v[172:175], a[172:175], v[104:107], v[172:175]\n" \
E_"v126, v126\n" \
M_"v[176:179], a[176:179], v[104:107], v[176:179]\n" \
E_"v127, v127\n" \
M_"v[180:183], a[180:183], v[104:107], v[180:183]\n" \
C_"v123, v126, v127\n" \
"v_readlane_b32 s81, v11, 12\n" \
M_"v[184:187], a[184:187], v[104:107], v[184:187]\n" \
"s_mov_b32 s82, 0x47fca0\n" \
"s_waitcnt lgkmcnt(0)\n" \
M_"v[188:191], a[188:191], v[104:107], v[188:191]\n" \
B_"v53, s[8:11], s64 offen offset:2048 sc1 lds\n" \
M_"v[220:223], a[188:191], v[116:119], v[220:223]\n" \
B_"v53, s[8:11], s64 offen offset:3072 sc1 lds\n" \
M_"v[252:255], a[188:191], v[120:123], v[252:255]\n" \
"s_mov_b32 s83, 0x20000\n" \
"v_readlane_b32 s92, v11, 29\n" \
M_"v[216:219], a[184:187], v[116:119], v[216:219]\n" \
"s_add_u32 m0, s93, 0x0\n" \
R_"a[128:131], v48 offset:8192\n" \
M_"v[248:251], a[184:187], v[120:123], v[248:251]\n" \
R_"a[144:147], v48 offset:12288\n" \
R_"a[132:135], v48 offset:9216\n" \
M_"v[212:215], a[180:183], v[116:119], v[212:215]\n" \
B_"v0, s[80:83], s92 offen offset:0 lds\n" \
M_"v[244:247], a[180:183], v[120:123], v[244:247]\n" \
B_"v1, s[80:83], s92 offen offset:1024 lds\n" \
M_"v[208:211], a[176:179], v[116:119], v[208:211]\n" \
R_"a[148:151], v48 offset:13312\n" \
R_"a[136:139], v48 offset:10240\n" \
M_"v[240:243], a[176:179], v[120:123], v[240:243]\n" \
R_"a[152:155], v48 offset:14336\n" \
R_"a[140:143], v48 offset:11264\n" \
M_"v[204:207], a[172:175], v[116:119], v[204:207]\n" \
B_"v2, s[80:83], s92 offen offset:2048 lds\n" \
M_"v[236:239], a[172:175], v[120:123], v[236:239]\n" \
R_"a[156:159], v48 offset:15360\n" \
"s_xor_b32 s65, s65, 0x8000\n" \
M_"v[200:203], a[168:171], v[116:119], v[200:203]\n" \
"s_add_u32 s64, s64, 0x10000\n" \
"v_add_u32_e32 v49, 0x10000, v48\n" \
M_"v[232:235], a[168:171], v[120:123], v[232:235]\n" \
B_"v3, s[80:83], s92 offen offset:3072 lds\n" \
M_"v[196:199], a[164:167], v[116:119], v[196:199]\n" \
E_"v86, v86\n" \
M_"v[228:231], a[164:167], v[120:123], v[228:231]\n" \
E_"v87, v87\n" \
M_"v[192:195], a[160:163], v[116:119], v[192:195]\n" \
E_"v84, v84\n" \
M_"v[224:227], a[160:163], v[120:123], v[224:227]\n" \
E_"v85, v85\n" \
"v_smfmac_f32_16x16x64_bf16 a[100:103], a[104:107], v[116:123], v21\n" \
E_"v88, v88\n" \
E_"v89, v89\n" \
C_"v87, v86, v87\n" \
E_"v82, v82\n" \
E_"v83, v83\n" \
E_"v80, v80\n" \
E_"v81, v81\n" \
E_"v90, v90\n" \
E_"v91, v91\n" \
C_"v86, v84, v85\n" \
C_"v88, v88, v89\n" \
E_"v92, v92\n" \
E_"v93, v93\n" \
E_"v94, v94\n" \
E_"v95, v95\n" \
C_"v85, v82, v83\n" \
C_"v84, v80, v81\n" \
C_"v89, v90, v91\n" \
"v_and_b32_e32 v84, v12, v84\n" \
"v_and_b32_e32 v85, v13, v85\n" \
"v_and_b32_e32 v86, v14, v86\n" \
"v_and_b32_e32 v87, v15, v87\n" \
C_"v90, v92, v93\n" \
C_"v91, v94, v95\n" \
"v_add_u32_e32 v48, s16, v20\n" \
"v_add_f32_e32 v54, v54, v22\n" \
"v_accvgpr_read_b32 v55, a98\n" \
"v_accvgpr_read_b32 v23, a99\n" \
M_"a[96:99], a[120:123], a[120:123], 0\n" \
"v_max_f32_e32 v62, v62, v54\n" \
"v_add_f32_e32 v55, v55, v23\n" \
"v_max_f32_e32 v62, v62, v55\n" \
"v_rcp_f32_e32 v54, v54\n" \
"v_rcp_f32_e32 v55, v55\n" \
"v_mul_f32_e32 v54, v54, v58\n" \
"v_mul_f32_e32 v55, v55, v59\n" \
"v_mul_f32_e32 v128, v54, v128\n" \
"v_mul_f32_e32 v129, v54, v129\n" \
"v_mul_f32_e32 v136, v54, v136\n" \
"v_mul_f32_e32 v137, v54, v137\n" \
"v_mul_f32_e32 v144, v54, v144\n" \
"v_mul_f32_e32 v145, v54, v145\n" \
"v_mul_f32_e32 v152, v54, v152\n" \
"v_mul_f32_e32 v153, v54, v153\n" \
"v_mul_f32_e32 v160, v55, v160\n" \
"v_mul_f32_e32 v161, v55, v161\n" \
"v_mul_f32_e32 v168, v55, v168\n" \
"v_mul_f32_e32 v169, v55, v169\n" \
"v_mul_f32_e32 v176, v55, v176\n" \
"v_mul_f32_e32 v177, v55, v177\n" \
"v_mul_f32_e32 v184, v55, v184\n" \
"v_mul_f32_e32 v185, v55, v185\n" \
C_"v128, v128, v129\n" \
"v_mul_f32_e32 v130, v54, v130\n" \
"v_mul_f32_e32 v131, v54, v131\n" \
C_"v136, v136, v137\n" \
"v_mul_f32_e32 v138, v54, v138\n" \
"v_mul_f32_e32 v139, v54, v139\n" \
C_"v144, v144, v145\n" \
"v_mul_f32_e32 v146, v54, v146\n" \
"v_mul_f32_e32 v147, v54, v147\n" \
C_"v152, v152, v153\n" \
"v_mul_f32_e32 v154, v54, v154\n" \
"v_mul_f32_e32 v155, v54, v155\n" \
C_"v160, v160, v161\n" \
"v_mul_f32_e32 v162, v55, v162\n" \
"v_mul_f32_e32 v163, v55, v163\n" \
C_"v168, v168, v169\n" \
"v_mul_f32_e32 v170, v55, v170\n" \
"v_mul_f32_e32 v171, v55, v171\n" \
C_"v176, v176, v177\n" \
"v_mul_f32_e32 v178, v55, v178\n" \
"v_mul_f32_e32 v179, v55, v179\n" \
C_"v184, v184, v185\n" \
"v_mul_f32_e32 v186, v55, v186\n" \
"v_mul_f32_e32 v187, v55, v187\n" \
C_"v129, v130, v131\n" \
"v_mul_f32_e32 v132, v54, v132\n" \
"v_mul_f32_e32 v133, v54, v133\n" \
"v_mul_f32_e32 v134, v54, v134\n" \
"v_mul_f32_e32 v135, v54, v135\n" \
C_"v137, v138, v139\n" \
"v_mul_f32_e32 v140, v54, v140\n" \
"s_waitcnt vmcnt(12) lgkmcnt(0)\n" \
"s_barrier\n" \
M_"v[120:123], a[128:131], a[48:51], v[44:47]\n" \
"s_xor_b32 m0, s65, 0x12000\n" \
R_"a[188:191], v49 offset:7168\n" \
M_"v[120:123], a[132:135], a[52:55], v[120:123]\n" \
B_"v50, s[8:11], s64 offen offset:0 sc1 lds\n" \
M_"v[120:123], a[136:139], a[56:59], v[120:123]\n" \
R_"a[184:187], v49 offset:6144\n" \
R_"a[180:183], v49 offset:5120\n" \
M_"v[120:123], a[140:143], a[60:63], v[120:123]\n" \
B_"v50, s[8:11], s64 offen offset:1024 sc1 lds\n" \
M_"v[124:127], a[156:159], a[60:63], v[44:47]\n" \
R_"a[176:179], v49 offset:4096\n" \
R_"a[172:175], v49 offset:3072\n" \
M_"v[124:127], a[152:155], a[56:59], v[124:127]\n" \
R_"a[168:171], v49 offset:2048\n" \
R_"a[164:167], v49 offset:1024\n" \
M_"v[124:127], a[148:151], a[52:55], v[124:127]\n" \
R_"a[160:163], v49 offset:0\n" \
R_"a[0:3], v49 offset:8192\n" \
M_"v[124:127], a[144:147], a[48:51], v[124:127]\n" \
B_"v50, s[8:11], s64 offen offset:2048 sc1 lds\n" \
M_"v[32:35], a[124:127], a[64:67], 0\n" \
R_"a[4:7], v49 offset:9216\n" \
R_"a[8:11], v49 offset:10240\n" \
M_"v[36:39], a[124:127], a[80:83], 0\n" \
R_"a[12:15], v49 offset:11264\n" \
R_"a[16:19], v49 offset:12288\n" \
M_"v[40:43], a[124:127], a[192:195], 0\n" \
R_"a[20:23], v49 offset:13312\n" \
R_"a[24:27], v49 offset:14336\n" \
M_"v[44:47], a[124:127], a[208:211], 0\n" \
B_"v50, s[8:11], s64 offen offset:3072 sc1 lds\n" \
M_"v[32:35], a[224:227], a[68:71], v[32:35]\n" \
"s_xor_b32 m0, s65, 0xa000\n" \
R_"a[28:31], v49 offset:15360\n" \
M_"v[36:39], a[224:227], a[84:87], v[36:39]\n" \
E_"v120, v120\n" \
M_"v[40:43], a[224:227], a[196:199], v[40:43]\n" \
B_"v51, s[8:11], s64 offen offset:0 sc1 lds\n" \
M_"v[44:47], a[224:227], a[212:215], v[44:47]\n" \
E_"v121, v121\n" \
M_"v[32:35], a[228:231], a[72:75], v[32:35]\n" \
E_"v122, v122\n" \
M_"v[36:39], a[228:231], a[88:91], v[36:39]\n" \
E_"v123, v123\n" \
M_"v[40:43], a[228:231], a[200:203], v[40:43]\n" \
B_"v51, s[8:11], s64 offen offset:1024 sc1 lds\n" \
M_"v[44:47], a[228:231], a[216:219], v[44:47]\n" \
C_"v120, v120, v121\n" \
C_"v121, v122, v123\n" \
M_"v[32:35], a[232:235], a[76:79], v[32:35]\n" \
E_"v124, v124\n" \
M_"v[36:39], a[232:235], a[92:95], v[36:39]\n" \
E_"v125, v125\n" \
M_"v[40:43], a[232:235], a[204:207], v[40:43]\n" \
B_"v51, s[8:11], s64 offen offset:2048 sc1 lds\n" \
M_"v[44:47], a[232:235], a[220:223], v[44:47]\n" \
E_"v126, v126\n" \
"v_smfmac_f32_16x16x64_bf16 a[100:103], a[104:107], v[84:91], v21\n" \
C_"v122, v124, v125\n" \
"s_waitcnt lgkmcnt(1)\n" \
M_"v[220:223], a[188:191], v[84:87], v[220:223]\n" \
B_"v51, s[8:11], s64 offen offset:3072 sc1 lds\n" \
M_"v[252:255], a[188:191], v[88:91], v[252:255]\n" \
"s_add_u32 m0, s93, 0x1000\n" \
"v_and_b32_e32 v120, v12, v120\n" \
M_"v[216:219], a[184:187], v[84:87], v[216:219]\n" \
E_"v127, v127\n" \
M_"v[248:251], a[184:187], v[88:91], v[248:251]\n" \
C_"v123, v126, v127\n" \
"v_and_b32_e32 v121, v13, v121\n" \
M_"v[212:215], a[180:183], v[84:87], v[212:215]\n" \
B_"v4, s[80:83], s92 offen offset:0 lds\n" \
M_"v[244:247], a[180:183], v[88:91], v[244:247]\n" \
"v_and_b32_e32 v122, v14, v122\n" \
"v_and_b32_e32 v123, v15, v123\n" \
M_"v[208:211], a[176:179], v[84:87], v[208:211]\n" \
"v_accvgpr_read_b32 v56, a100\n" \
"v_accvgpr_read_b32 v24, a101\n" \
M_"v[240:243], a[176:179], v[88:91], v[240:243]\n" \
R_"a[128:131], v48 offset:0\n" \
R_"a[144:147], v48 offset:4096\n" \
M_"v[204:207], a[172:175], v[84:87], v[204:207]\n" \
B_"v5, s[80:83], s92 offen offset:1024 lds\n" \
M_"v[236:239], a[172:175], v[88:91], v[236:239]\n" \
R_"a[132:135], v48 offset:1024\n" \
R_"a[148:151], v48 offset:5120\n" \
M_"v[200:203], a[168:171], v[84:87], v[200:203]\n" \
R_"a[136:139], v48 offset:2048\n" \
R_"a[152:155], v48 offset:6144\n" \
M_"v[232:235], a[168:171], v[88:91], v[232:235]\n" \
B_"v6, s[80:83], s92 offen offset:2048 lds\n" \
M_"v[196:199], a[164:167], v[84:87], v[196:199]\n" \
R_"a[140:143], v48 offset:3072\n" \
R_"a[156:159], v48 offset:7168\n" \
M_"v[228:231], a[164:167], v[88:91], v[228:231]\n" \
"v_mul_f32_e32 v141, v54, v141\n" \
"v_mul_f32_e32 v142, v54, v142\n" \
M_"v[192:195], a[160:163], v[84:87], v[192:195]\n" \
"v_mul_f32_e32 v143, v54, v143\n" \
C_"v145, v146, v147\n" \
M_"v[224:227], a[160:163], v[88:91], v[224:227]\n" \
B_"v7, s[80:83], s92 offen offset:3072 lds\n" \
M_"a[100:103], a[116:119], v[120:123], a[100:103]\n" \
"v_mul_f32_e32 v148, v54, v148\n" \
"v_mul_f32_e32 v149, v54, v149\n" \
M_"v[224:227], a[0:3], v[120:123], v[224:227]\n" \
"v_mul_f32_e32 v150, v54, v150\n" \
"v_mul_f32_e32 v151, v54, v151\n" \
M_"v[228:231], a[4:7], v[120:123], v[228:231]\n" \
C_"v153, v154, v155\n" \
"v_mul_f32_e32 v156, v54, v156\n" \
M_"v[232:235], a[8:11], v[120:123], v[232:235]\n" \
"v_mul_f32_e32 v157, v54, v157\n" \
"v_mul_f32_e32 v158, v54, v158\n" \
M_"v[236:239], a[12:15], v[120:123], v[236:239]\n" \
"v_mul_f32_e32 v159, v54, v159\n" \
C_"v161, v162, v163\n" \
M_"v[240:243], a[16:19], v[120:123], v[240:243]\n" \
"v_mul_f32_e32 v164, v55, v164\n" \
"v_mul_f32_e32 v165, v55, v165\n" \
M_"v[244:247], a[20:23], v[120:123], v[244:247]\n" \
"v_mul_f32_e32 v166, v55, v166\n" \
"v_mul_f32_e32 v167, v55, v167\n" \
M_"v[248:251], a[24:27], v[120:123], v[248:251]\n" \
C_"v169, v170, v171\n" \
"s_waitcnt lgkmcnt(4)\n" \
M_"v[252:255], a[28:31], v[120:123], v[252:255]\n" \
"v_mul_f32_e32 v172, v55, v172\n" \
"v_mul_f32_e32 v173, v55, v173\n" \
"v_mul_f32_e32 v174, v55, v174\n" \
"v_mul_f32_e32 v175, v55, v175\n" \
C_"v177, v178, v179\n" \
"v_mul_f32_e32 v180, v55, v180\n" \
"v_mul_f32_e32 v181, v55, v181\n" \
"v_mul_f32_e32 v182, v55, v182\n" \
"v_mul_f32_e32 v183, v55, v183\n" \
C_"v185, v186, v187\n" \
"v_mul_f32_e32 v188, v55, v188\n" \
"v_mul_f32_e32 v189, v55, v189\n" \
"v_mul_f32_e32 v190, v55, v190\n" \
"v_mul_f32_e32 v191, v55, v191\n" \
C_"v130, v132, v133\n" \
C_"v131, v134, v135\n" \
"buffer_store_dwordx4 v[128:131], v20, s[56:59], s68 offen offset:0 sc1\n" \
C_"v138, v140, v141\n" \
C_"v139, v142, v143\n" \
"buffer_store_dwordx4 v[136:139], v20, s[56:59], s68 offen offset:1024 sc1\n" \
C_"v146, v148, v149\n" \
C_"v147, v150, v151\n" \
"buffer_store_dwordx4 v[144:147], v20, s[56:59], s68 offen offset:2048 sc1\n" \
C_"v154, v156, v157\n" \
C_"v155, v158, v159\n" \
"buffer_store_dwordx4 v[152:155], v20, s[56:59], s68 offen offset:3072 sc1\n" \
C_"v162, v164, v165\n" \
C_"v163, v166, v167\n" \
"buffer_store_dwordx4 v[160:163], v20, s[56:59], s69 offen offset:0 sc1\n" \
C_"v170, v172, v173\n" \
C_"v171, v174, v175\n" \
"buffer_store_dwordx4 v[168:171], v20, s[56:59], s69 offen offset:1024 sc1\n" \
C_"v178, v180, v181\n" \
C_"v179, v182, v183\n" \
"buffer_store_dwordx4 v[176:179], v20, s[56:59], s69 offen offset:2048 sc1\n" \
C_"v186, v188, v189\n" \
C_"v187, v190, v191\n" \
"buffer_store_dwordx4 v[184:187], v20, s[56:59], s69 offen offset:3072 sc1\n" \
"s_add_u32 m0, s93, 0x2000\n" \
M_"a[112:115], a[120:123], v[128:131], a[112:115]\n" \
B_"v8, s[80:83], s92 offen offset:0 lds\n" \
M_"a[112:115], a[120:123], v[136:139], a[112:115]\n" \
"v_mfma_f32_32x32x16_bf16 v[128:143], a[120:123], a[120:123], 0\n" \
M_"a[112:115], a[120:123], v[144:147], a[112:115]\n" \
M_"a[112:115], a[120:123], v[152:155], a[112:115]\n" \
"v_mfma_f32_32x32x16_bf16 v[144:159], a[120:123], a[120:123], 0\n" \
"v_add_f32_e32 v56, v56, v24\n" \
M_"a[112:115], a[120:123], v[160:163], a[112:115]\n" \
"v_max_f32_e32 v62, v62, v56\n" \
"v_accvgpr_read_b32 v57, a102\n" \
"s_waitcnt vmcnt(21) lgkmcnt(0)\n" \
"s_barrier\n" \
M_"v[64:67], a[128:131], a[64:67], v[32:35]\n" \
"s_xor_b32 m0, s65, 0x18000\n" \
"v_accvgpr_read_b32 v25, a103\n" \
M_"a[100:103], a[120:123], a[120:123], 0\n" \
B_"v52, s[8:11], s64 offen offset:0 sc1 lds\n" \
M_"v[64:67], a[132:135], a[68:71], v[64:67]\n" \
"v_rcp_f32_e32 v56, v56\n" \
M_"v[64:67], a[136:139], a[72:75], v[64:67]\n" \
M_"a[112:115], a[120:123], v[168:171], a[112:115]\n" \
"v_mfma_f32_32x32x16_bf16 v[160:175], a[120:123], a[120:123], 0\n" \
"v_mul_f32_e32 v56, v56, v60\n" \
M_"a[112:115], a[120:123], v[176:179], a[112:115]\n" \
"v_mul_f32_e32 v192, v56, v192\n" \
"v_mul_f32_e32 v193, v56, v193\n" \
M_"v[64:67], a[140:143], a[76:79], v[64:67]\n" \
B_"v52, s[8:11], s64 offen offset:1024 sc1 lds\n" \
M_"v[68:71], a[156:159], a[76:79], v[32:35]\n" \
C_"v192, v192, v193\n" \
"v_mul_f32_e32 v194, v56, v194\n" \
M_"v[68:71], a[152:155], a[72:75], v[68:71]\n" \
"v_mul_f32_e32 v195, v56, v195\n" \
C_"v193, v194, v195\n" \
M_"v[68:71], a[148:151], a[68:71], v[68:71]\n" \
M_"a[112:115], a[120:123], v[184:187], a[112:115]\n" \
"v_mfma_f32_32x32x16_bf16 v[176:191], a[120:123], a[120:123], 0\n" \
"v_mul_f32_e32 v196, v56, v196\n" \
"v_mul_f32_e32 v197, v56, v197\n" \
"v_mul_f32_e32 v198, v56, v198\n" \
"v_mul_f32_e32 v199, v56, v199\n" \
"v_mul_f32_e32 v200, v56, v200\n" \
"v_mul_f32_e32 v201, v56, v201\n" \
M_"v[68:71], a[144:147], a[64:67], v[68:71]\n" \
B_"v52, s[8:11], s64 offen offset:2048 sc1 lds\n" \
M_"v[76:79], a[144:147], a[80:83], v[36:39]\n" \
C_"v194, v196, v197\n" \
C_"v195, v198, v199\n" \
"buffer_store_dwordx4 v[192:195], v20, s[56:59], s70 offen offset:0 sc1\n" \
M_"v[76:79], a[148:151], a[84:87], v[76:79]\n" \
C_"v200, v200, v201\n" \
"v_mul_f32_e32 v202, v56, v202\n" \
M_"v[76:79], a[152:155], a[88:91], v[76:79]\n" \
"v_mul_f32_e32 v203, v56, v203\n" \
"v_add_f32_e32 v57, v57, v25\n" \
M_"v[76:79], a[156:159], a[92:95], v[76:79]\n" \
B_"v52, s[8:11], s64 offen offset:3072 sc1 lds\n" \
M_"v[72:75], a[140:143], a[92:95], v[36:39]\n" \
"s_mov_b32 m0, s65\n" \
C_"v201, v202, v203\n" \
M_"v[72:75], a[136:139], a[88:91], v[72:75]\n" \
M_"a[112:115], a[120:123], v[192:195], a[112:115]\n" \
"v_mul_f32_e32 v204, v56, v204\n" \
"v_mul_f32_e32 v205, v56, v205\n" \
M_"v[72:75], a[132:135], a[84:87], v[72:75]\n" \
"v_mul_f32_e32 v206, v56, v206\n" \
"v_mul_f32_e32 v207, v56, v207\n" \
M_"v[72:75], a[128:131], a[80:83], v[72:75]\n" \
B_"v53, s[8:11], s64 offen offset:0 sc1 lds\n" \
M_"v[80:83], a[128:131], a[192:195], v[40:43]\n" \
"v_mul_f32_e32 v208, v56, v208\n" \
"v_mul_f32_e32 v209, v56, v209\n" \
M_"v[80:83], a[132:135], a[196:199], v[80:83]\n" \
"v_max_f32_e32 v62, v62, v57\n" \
C_"v202, v204, v205\n" \
M_"v[80:83], a[136:139], a[200:203], v[80:83]\n" \
B_"v53, s[8:11], s64 offen offset:1024 sc1 lds\n" \
M_"v[80:83], a[140:143], a[204:207], v[80:83]\n" \
C_"v203, v206, v207\n" \
C_"v208, v208, v209\n" \
"buffer_store_dwordx4 v[200:203], v20, s[56:59], s70 offen offset:1024 sc1\n" \
M_"v[84:87], a[156:159], a[204:207], v[40:43]\n" \
"v_mul_f32_e32 v210, v56, v210\n" \
"v_mul_f32_e32 v211, v56, v211\n" \
M_"v[84:87], a[152:155], a[200:203], v[84:87]\n" \
"v_rcp_f32_e32 v57, v57\n" \
M_"v[84:87], a[148:151], a[196:199], v[84:87]\n" \
B_"v53, s[8:11], s64 offen offset:2048 sc1 lds\n" \
M_"v[84:87], a[144:147], a[192:195], v[84:87]\n" \
M_"a[112:115], a[120:123], v[200:203], a[112:115]\n" \
"v_mfma_f32_32x32x16_bf16 v[192:207], a[120:123], a[120:123], 0\n" \
C_"v209, v210, v211\n" \
"v_mul_f32_e32 v212, v56, v212\n" \
".fill (L_ab + 20480 - .) / 4, 4, 0xBF800000\n" \
"L_ap5:\n" \
"s_cbranch_execz L_ap0\n" \
"v_mul_f32_e32 v213, v56, v213\n" \
"v_mul_f32_e32 v214, v56, v214\n" \
"v_mul_f32_e32 v215, v56, v215\n" \
"v_mul_f32_e32 v216, v56, v216\n" \
M_"v[92:95], a[144:147], a[208:211], v[44:47]\n" \
R_"a[144:147], v48 offset:12288\n" \
"v_mul_f32_e32 v217, v56, v217\n" \
M_"v[92:95], a[148:151], a[212:215], v[92:95]\n" \
R_"a[148:151], v48 offset:13312\n" \
C_"v210, v212, v213\n" \
M_"v[92:95], a[152:155], a[216:219], v[92:95]\n" \
R_"a[152:155], v48 offset:14336\n" \
C_"v211, v214, v215\n" \
"buffer_store_dwordx4 v[208:211], v20, s[56:59], s70 offen offset:2048 sc1\n" \
M_"v[92:95], a[156:159], a[220:223], v[92:95]\n" \
R_"a[156:159], v48 offset:15360\n" \
C_"v216, v216, v217\n" \
M_"v[88:91], a[140:143], a[220:223], v[44:47]\n" \
R_"a[140:143], v48 offset:11264\n" \
"v_mul_f32_e32 v218, v56, v218\n" \
M_"v[88:91], a[136:139], a[216:219], v[88:91]\n" \
R_"a[136:139], v48 offset:10240\n" \
"v_mul_f32_e32 v219, v56, v219\n" \
M_"v[88:91], a[132:135], a[212:215], v[88:91]\n" \
R_"a[132:135], v48 offset:9216\n" \
"v_mul_f32_e32 v57, v57, v61\n" \
M_"v[88:91], a[128:131], a[208:211], v[88:91]\n" \
R_"a[128:131], v48 offset:8192\n" \
M_"a[112:115], a[120:123], v[208:211], a[112:115]\n" \
C_"v217, v218, v219\n" \
"v_mul_f32_e32 v220, v56, v220\n" \
"v_mul_f32_e32 v221, v56, v221\n" \
"v_mul_f32_e32 v222, v56, v222\n" \
"v_mul_f32_e32 v223, v56, v223\n" \
"v_mul_f32_e32 v224, v57, v224\n" \
"v_mul_f32_e32 v225, v57, v225\n" \
C_"v218, v220, v221\n" \
C_"v219, v222, v223\n" \
"buffer_store_dwordx4 v[216:219], v20, s[56:59], s70 offen offset:3072 sc1\n" \
C_"v224, v224, v225\n" \
"v_mul_f32_e32 v226, v57, v226\n" \
"v_mul_f32_e32 v227, v57, v227\n" \
M_"a[112:115], a[120:123], v[216:219], a[112:115]\n" \
"v_mfma_f32_32x32x16_bf16 v[208:223], a[120:123], a[120:123], 0\n" \
C_"v225, v226, v227\n" \
"v_mul_f32_e32 v228, v57, v228\n" \
"v_mul_f32_e32 v229, v57, v229\n" \
"v_mul_f32_e32 v230, v57, v230\n" \
"v_mul_f32_e32 v231, v57, v231\n" \
"v_mul_f32_e32 v232, v57, v232\n" \
"v_mul_f32_e32 v233, v57, v233\n" \
C_"v226, v228, v229\n" \
C_"v227, v230, v231\n" \
"buffer_store_dwordx4 v[224:227], v20, s[56:59], s71 offen offset:0 sc1\n" \
C_"v232, v232, v233\n" \
"v_mul_f32_e32 v234, v57, v234\n" \
"v_mul_f32_e32 v235, v57, v235\n" \
M_"a[112:115], a[120:123], v[224:227], a[112:115]\n" \
C_"v233, v234, v235\n" \
"v_mul_f32_e32 v236, v57, v236\n" \
"v_mul_f32_e32 v237, v57, v237\n" \
"v_mul_f32_e32 v238, v57, v238\n" \
"v_mul_f32_e32 v239, v57, v239\n" \
"v_mul_f32_e32 v240, v57, v240\n" \
"v_mul_f32_e32 v241, v57, v241\n" \
C_"v234, v236, v237\n" \
C_"v235, v238, v239\n" \
"buffer_store_dwordx4 v[232:235], v20, s[56:59], s71 offen offset:1024 sc1\n" \
C_"v240, v240, v241\n" \
"v_mul_f32_e32 v242, v57, v242\n" \
"v_mul_f32_e32 v243, v57, v243\n" \
M_"a[112:115], a[120:123], v[232:235], a[112:115]\n" \
"v_mfma_f32_32x32x16_bf16 v[224:239], a[120:123], a[120:123], 0\n" \
C_"v241, v242, v243\n" \
"v_mul_f32_e32 v244, v57, v244\n" \
"v_mul_f32_e32 v245, v57, v245\n" \
"v_mul_f32_e32 v246, v57, v246\n" \
"v_mul_f32_e32 v247, v57, v247\n" \
"v_mul_f32_e32 v248, v57, v248\n" \
"v_mul_f32_e32 v249, v57, v249\n" \
C_"v242, v244, v245\n" \
C_"v243, v246, v247\n" \
"buffer_store_dwordx4 v[240:243], v20, s[56:59], s71 offen offset:2048 sc1\n" \
C_"v248, v248, v249\n" \
"v_mul_f32_e32 v250, v57, v250\n" \
"v_mul_f32_e32 v251, v57, v251\n" \
M_"a[112:115], a[120:123], v[240:243], a[112:115]\n" \
C_"v249, v250, v251\n" \
"v_mul_f32_e32 v252, v57, v252\n" \
"v_mul_f32_e32 v253, v57, v253\n" \
"v_mul_f32_e32 v254, v57, v254\n" \
"v_mul_f32_e32 v255, v57, v255\n" \
C_"v250, v252, v253\n" \
C_"v251, v254, v255\n" \
"buffer_store_dwordx4 v[248:251], v20, s[56:59], s71 offen offset:3072 sc1\n" \
B_"v53, s[8:11], s64 offen offset:3072 sc1 lds\n" \
"s_waitcnt vmcnt(16) lgkmcnt(0)\n" \
"s_barrier\n" \
M_"a[112:115], a[120:123], v[248:251], a[112:115]\n" \
"v_mfma_f32_32x32x16_bf16 v[240:255], a[120:123], a[120:123], 0\n" \
E_"v70, v70\n" \
E_"v71, v71\n" \
E_"v72, v72\n" \
E_"v73, v73\n" \
"s_add_u32 s64, s64, 0x10000\n" \
"v_cmp_lt_f32_e64 s[72:73], s21, v62\n" \
"v_readlane_b32 s95, v11, 3\n" \
"v_accvgpr_read_b32 v63, a112\n" \
"v_cmp_u_f32_e32 vcc, v63, v63\n" \
E_"v68, v68\n" \
E_"v69, v69\n" \
E_"v74, v74\n" \
E_"v75, v75\n" \
"s_or_b64 vcc, s[72:73], vcc\n" \
"s_cselect_b32 s95, 0x40000000, s95\n" \
"s_cselect_b32 s94, 0x7f800000, 0\n" \
C_"v71, v70, v71\n" \
E_"v66, v66\n" \
E_"v67, v67\n" \
E_"v64, v64\n" \
E_"v65, v65\n" \
C_"v72, v72, v73\n" \
E_"v76, v76\n" \
E_"v77, v77\n" \
E_"v78, v78\n" \
E_"v79, v79\n" \
C_"v70, v68, v69\n" \
C_"v73, v74, v75\n" \
"v_add_u32_e32 v49, 0x10000, v48\n" \
"s_xor_b32 s65, s65, 0x8000\n" \
C_"v69, v66, v67\n" \
C_"v68, v64, v65\n" \
C_"v74, v76, v77\n" \
C_"v75, v78, v79\n" \
"v_xor_b32_e32 v48, 0x8000, v48\n" \
"s_sub_u32 s66, s19, 2\n" \
"s_branch L_2_%=\n" \
"L_f1_%=:\n" \
R_"a[160:163], v49 offset:0\n" \
R_"a[164:167], v49 offset:1024\n" \
R_"a[168:171], v49 offset:2048\n" \
M_"v[104:107], a[128:131], a[80:83], v[36:39]\n" \
C_"v192, v192, v193\n" \
C_"v224, v224, v225\n" \
C_"v193, v194, v195\n" \
C_"v225, v226, v227\n" \
R_"a[172:175], v49 offset:3072\n" \
C_"v194, v196, v197\n" \
C_"v195, v198, v199\n" \
C_"v226, v228, v229\n" \
C_"v227, v230, v231\n" \
M_"v[104:107], a[132:135], a[84:87], v[104:107]\n" \
"v_lshl_add_u32 v50, s15, 2, v20\n" \
C_"v196, v200, v201\n" \
C_"v197, v202, v203\n" \
C_"v198, v204, v205\n" \
C_"v199, v206, v207\n" \
C_"v228, v232, v233\n" \
C_"v229, v234, v235\n" \
C_"v230, v236, v237\n" \
C_"v231, v238, v239\n" \
"v_add_u32_e32 v50, 0x1a000, v50\n" \
"v_mov_b32_e32 v61, s62\n" \
C_"v200, v208, v209\n" \
C_"v201, v210, v211\n" \
C_"v202, v212, v213\n" \
C_"v203, v214, v215\n" \
C_"v204, v216, v217\n" \
C_"v205, v218, v219\n" \
C_"v206, v220, v221\n" \
C_"v207, v222, v223\n" \
C_"v232, v240, v241\n" \
C_"v233, v242, v243\n" \
C_"v234, v244, v245\n" \
C_"v235, v246, v247\n" \
C_"v236, v248, v249\n" \
C_"v237, v250, v251\n" \
C_"v238, v252, v253\n" \
C_"v239, v254, v255\n" \
"ds_write_b128 v50, v[192:195] offset:0\n" \
M_"v[104:107], a[136:139], a[88:91], v[104:107]\n" \
R_"a[176:179], v49 offset:4096\n" \
"v_add_u32_e32 v56, 0x49000, v20\n" \
M_"v[104:107], a[140:143], a[92:95], v[104:107]\n" \
"ds_write_b128 v50, v[196:199] offset:1024\n" \
M_"v[108:111], a[156:159], a[92:95], v[36:39]\n" \
R_"a[180:183], v49 offset:5120\n" \
R_"a[184:187], v49 offset:6144\n" \
M_"v[108:111], a[152:155], a[88:91], v[108:111]\n" \
"ds_write_b128 v50, v[200:203] offset:2048\n" \
M_"v[108:111], a[148:151], a[84:87], v[108:111]\n" \
"v_and_b32_e32 v68, v12, v68\n" \
"v_and_b32_e32 v69, v13, v69\n" \
M_"v[108:111], a[144:147], a[80:83], v[108:111]\n" \
"ds_write_b128 v50, v[204:207] offset:3072\n" \
"v_and_b32_e32 v70, v14, v70\n" \
"v_and_b32_e32 v71, v15, v71\n" \
R_"a[188:191], v49 offset:7168\n" \
R_"a[0:3], v20 offset:8192\n" \
"s_waitcnt lgkmcnt(9)\n" \
M_"v[128:131], a[160:163], v[68:71], v[128:131]\n" \
"ds_write_b128 v50, v[224:227] offset:4096\n" \
M_"v[160:163], a[160:163], v[72:75], v[160:163]\n" \
R_"a[4:7], v20 offset:9216\n" \
R_"a[8:11], v20 offset:10240\n" \
M_"v[132:135], a[164:167], v[68:71], v[132:135]\n" \
"ds_write_b128 v50, v[228:231] offset:5120\n" \
M_"v[164:167], a[164:167], v[72:75], v[164:167]\n" \
R_"a[12:15], v20 offset:11264\n" \
R_"a[28:31], v20 offset:15360\n" \
M_"v[136:139], a[168:171], v[68:71], v[136:139]\n" \
"ds_write_b128 v50, v[232:235] offset:6144\n" \
M_"v[168:171], a[168:171], v[72:75], v[168:171]\n" \
R_"a[24:27], v20 offset:14336\n" \
R_"a[20:23], v20 offset:13312\n" \
M_"v[140:143], a[172:175], v[68:71], v[140:143]\n" \
"ds_write_b128 v50, v[236:239] offset:7168\n" \
M_"v[172:175], a[172:175], v[72:75], v[172:175]\n" \
R_"a[16:19], v20 offset:12288\n" \
"s_waitcnt lgkmcnt(15)\n" \
M_"v[144:147], a[176:179], v[68:71], v[144:147]\n" \
"v_xor_b32_e32 v48, 0x4000, v49\n" \
"ds_read_b32 v58, v18 offset:256\n" \
M_"v[176:179], a[176:179], v[72:75], v[176:179]\n" \
"ds_read_b32 v59, v18 offset:384\n" \
R_"a[32:35], v48 offset:8192\n" \
M_"v[148:151], a[180:183], v[68:71], v[148:151]\n" \
R_"a[36:39], v48 offset:9216\n" \
R_"a[40:43], v48 offset:10240\n" \
M_"v[180:183], a[180:183], v[72:75], v[180:183]\n" \
R_"a[44:47], v48 offset:11264\n" \
R_"a[48:51], v48 offset:12288\n" \
M_"v[152:155], a[184:187], v[68:71], v[152:155]\n" \
R_"a[52:55], v48 offset:13312\n" \
R_"a[56:59], v48 offset:14336\n" \
M_"v[184:187], a[184:187], v[72:75], v[184:187]\n" \
R_"a[60:63], v48 offset:15360\n" \
"s_waitcnt lgkmcnt(15)\n" \
M_"v[156:159], a[188:191], v[68:71], v[156:159]\n" \
R_"a[128:131], v49 offset:8192\n" \
R_"a[132:135], v49 offset:9216\n" \
M_"v[188:191], a[188:191], v[72:75], v[188:191]\n" \
R_"a[136:139], v49 offset:10240\n" \
R_"a[140:143], v49 offset:11264\n" \
"v_smfmac_f32_16x16x64_bf16 a[96:99], a[104:107], v[68:75], v21\n" \
R_"a[144:147], v49 offset:12288\n" \
R_"a[148:151], v49 offset:13312\n" \
M_"v[112:115], a[0:3], a[64:67], v[32:35]\n" \
R_"a[152:155], v49 offset:14336\n" \
R_"a[156:159], v49 offset:15360\n" \
M_"v[112:115], a[4:7], a[68:71], v[112:115]\n" \
"v_accvgpr_read_b32 v40, a100\n" \
"v_accvgpr_read_b32 v24, a101\n" \
M_"v[112:115], a[8:11], a[72:75], v[112:115]\n" \
"v_accvgpr_read_b32 v41, a102\n" \
"v_accvgpr_read_b32 v25, a103\n" \
M_"v[112:115], a[12:15], a[76:79], v[112:115]\n" \
"v_add_f32_e32 v40, v40, v24\n" \
"v_add_f32_e32 v41, v41, v25\n" \
M_"v[116:119], a[28:31], a[76:79], v[32:35]\n" \
"v_add_f32_e32 v40, s94, v40\n" \
"s_waitcnt lgkmcnt(15)\n" \
M_"v[116:119], a[24:27], a[72:75], v[116:119]\n" \
"v_add_f32_e32 v41, s94, v41\n" \
"ds_write_b64 v19, v[40:41]\n" \
M_"v[116:119], a[20:23], a[68:71], v[116:119]\n" \
"v_add_u32_e32 v52, s15, v20\n" \
"v_add_u32_e32 v51, 0x10000, v52\n" \
M_"v[116:119], a[16:19], a[64:67], v[116:119]\n" \
E_"v104, v104\n" \
M_"v[124:127], a[16:19], a[80:83], v[36:39]\n" \
E_"v105, v105\n" \
M_"v[124:127], a[20:23], a[84:87], v[124:127]\n" \
E_"v106, v106\n" \
M_"v[124:127], a[24:27], a[88:91], v[124:127]\n" \
E_"v107, v107\n" \
M_"v[124:127], a[28:31], a[92:95], v[124:127]\n" \
C_"v104, v104, v105\n" \
C_"v105, v106, v107\n" \
M_"v[120:123], a[12:15], a[92:95], v[36:39]\n" \
E_"v108, v108\n" \
M_"v[120:123], a[8:11], a[88:91], v[120:123]\n" \
E_"v109, v109\n" \
M_"v[120:123], a[4:7], a[84:87], v[120:123]\n" \
E_"v110, v110\n" \
M_"v[120:123], a[0:3], a[80:83], v[120:123]\n" \
E_"v111, v111\n" \
"s_waitcnt lgkmcnt(0)\n" \
"s_barrier\n" \
R_"v[208:211], v52 offset:0\n" \
R_"v[212:215], v52 offset:1024\n" \
R_"v[216:219], v52 offset:2048\n" \
R_"v[220:223], v52 offset:3072\n" \
R_"v[240:243], v51 offset:32768\n" \
R_"v[244:247], v51 offset:33792\n" \
R_"v[248:251], v51 offset:34816\n" \
R_"v[252:255], v51 offset:35840\n" \
C_"v106, v108, v109\n" \
C_"v107, v110, v111\n" \
"v_and_b32_e32 v104, v12, v104\n" \
"v_and_b32_e32 v105, v13, v105\n" \
"v_and_b32_e32 v106, v14, v106\n" \
"s_waitcnt vmcnt(0)\n" \
"s_mov_b64 exec, 1\n" \
"buffer_store_dword v61, off, s[56:59], s95 offset:0 sc1\n" \
"s_mov_b64 exec, -1\n" \
"v_and_b32_e32 v107, v15, v107\n" \
"v_xor_b32_e32 v63, 0x400, v19\n" \
"ds_read_b64 v[42:43], v63\n" \
M_"a[96:99], a[116:119], v[104:107], a[96:99]\n" \
"v_mov_b32_e32 v194, v28\n" \
"v_mov_b32_e32 v195, v29\n" \
M_"v[160:163], a[128:131], v[104:107], v[160:163]\n" \
"v_mov_b32_e32 v196, 0\n" \
"v_mov_b32_e32 v197, 0\n" \
M_"v[164:167], a[132:135], v[104:107], v[164:167]\n" \
"v_mov_b32_e32 v192, 0\n" \
"v_mov_b32_e32 v193, 0\n" \
M_"v[168:171], a[136:139], v[104:107], v[168:171]\n" \
"v_readlane_b32 s93, v11, 25\n" \
"v_readlane_b32 s92, v11, 30\n" \
M_"v[172:175], a[140:143], v[104:107], v[172:175]\n" \
B_"a[192:195], v20, s[56:59], s60 offen offset:0 sc1\n" \
M_"v[176:179], a[144:147], v[104:107], v[176:179]\n" \
E_"v118, v118\n" \
M_"v[180:183], a[148:151], v[104:107], v[180:183]\n" \
E_"v119, v119\n" \
M_"v[184:187], a[152:155], v[104:107], v[184:187]\n" \
"s_add_u32 m0, s93, 0x0\n" \
C_"v119, v118, v119\n" \
M_"v[188:191], a[156:159], v[104:107], v[188:191]\n" \
E_"v120, v120\n" \
"s_waitcnt lgkmcnt(1)\n" \
"s_barrier\n" \
B_"a[208:211], v56, s[56:59], s60 offen offset:0 sc1\n" \
M_"v[128:131], v[194:197], v[208:211], v[128:131]\n" \
E_"v121, v121\n" \
M_"v[132:135], v[192:195], v[208:211], v[132:135]\n" \
E_"v116, v116\n" \
M_"v[136:139], v[194:197], v[212:215], v[136:139]\n" \
E_"v117, v117\n" \
M_"v[140:143], v[192:195], v[212:215], v[140:143]\n" \
E_"v122, v122\n" \
M_"v[144:147], v[194:197], v[216:219], v[144:147]\n" \
E_"v123, v123\n" \
M_"v[148:151], v[192:195], v[216:219], v[148:151]\n" \
B_"a[196:199], v20, s[56:59], s60 offen offset:1024 sc1\n" \
M_"v[152:155], v[194:197], v[220:223], v[152:155]\n" \
E_"v114, v114\n" \
M_"v[156:159], v[192:195], v[220:223], v[156:159]\n" \
E_"v115, v115\n" \
M_"v[160:163], v[194:197], v[240:243], v[160:163]\n" \
E_"v112, v112\n" \
M_"v[164:167], v[192:195], v[240:243], v[164:167]\n" \
E_"v113, v113\n" \
M_"v[168:171], v[194:197], v[244:247], v[168:171]\n" \
C_"v120, v120, v121\n" \
C_"v118, v116, v117\n" \
M_"v[172:175], v[192:195], v[244:247], v[172:175]\n" \
B_"a[212:215], v56, s[56:59], s60 offen offset:1024 sc1\n" \
M_"v[176:179], v[194:197], v[248:251], v[176:179]\n" \
E_"v124, v124\n" \
M_"v[180:183], v[192:195], v[248:251], v[180:183]\n" \
E_"v125, v125\n" \
M_"v[184:187], v[194:197], v[252:255], v[184:187]\n" \
E_"v126, v126\n" \
M_"v[188:191], v[192:195], v[252:255], v[188:191]\n" \
E_"v127, v127\n" \
C_"v121, v122, v123\n" \
C_"v117, v114, v115\n" \
C_"v116, v112, v113\n" \
C_"v122, v124, v125\n" \
C_"v123, v126, v127\n" \
B_"a[200:203], v20, s[56:59], s60 offen offset:2048 sc1\n" \
"s_nop 0\n" \
"v_smfmac_f32_16x16x64_bf16 a[96:99], a[104:107], v[116:123], v21\n" \
M_"v[128:131], a[32:35], v[116:119], v[128:131]\n" \
M_"v[160:163], a[32:35], v[120:123], v[160:163]\n" \
M_"v[132:135], a[36:39], v[116:119], v[132:135]\n" \
M_"v[164:167], a[36:39], v[120:123], v[164:167]\n" \
M_"v[136:139], a[40:43], v[116:119], v[136:139]\n" \
B_"a[216:219], v56, s[56:59], s60 offen offset:2048 sc1\n" \
M_"v[168:171], a[40:43], v[120:123], v[168:171]\n" \
M_"v[140:143], a[44:47], v[116:119], v[140:143]\n" \
"v_accvgpr_read_b32 v54, a96\n" \
"v_accvgpr_read_b32 v22, a97\n" \
M_"v[172:175], a[44:47], v[120:123], v[172:175]\n" \
"v_add_f32_e32 v54, v54, v22\n" \
"s_waitcnt lgkmcnt(0)\n" \
"v_add_f32_e32 v54, v54, v42\n" \
M_"v[144:147], a[48:51], v[116:119], v[144:147]\n" \
"v_max_f32_e32 v62, v62, v54\n" \
"v_accvgpr_read_b32 v55, a98\n" \
M_"v[176:179], a[48:51], v[120:123], v[176:179]\n" \
"v_rcp_f32_e32 v54, v54\n" \
B_"a[204:207], v20, s[56:59], s60 offen offset:3072 sc1\n" \
M_"v[148:151], a[52:55], v[116:119], v[148:151]\n" \
"v_mul_f32_e32 v54, v54, v58\n" \
"v_mul_f32_e32 v128, v54, v128\n" \
M_"v[180:183], a[52:55], v[120:123], v[180:183]\n" \
"v_mul_f32_e32 v129, v54, v129\n" \
"v_mul_f32_e32 v130, v54, v130\n" \
"v_mul_f32_e32 v131, v54, v131\n" \
"v_mul_f32_e32 v132, v54, v132\n" \
"v_mul_f32_e32 v133, v54, v133\n" \
"v_mul_f32_e32 v134, v54, v134\n" \
"v_mul_f32_e32 v135, v54, v135\n" \
C_"v64, v128, v129\n" \
C_"v65, v130, v131\n" \
C_"v66, v132, v133\n" \
C_"v67, v134, v135\n" \
"v_accvgpr_read_b32 v23, a99\n" \
M_"v[152:155], a[56:59], v[116:119], v[152:155]\n" \
M_"a[112:115], a[120:123], v[64:67], a[112:115]\n" \
B_"a[220:223], v56, s[56:59], s60 offen offset:3072 sc1\n" \
"v_mul_f32_e32 v136, v54, v136\n" \
"v_mul_f32_e32 v137, v54, v137\n" \
"v_mul_f32_e32 v138, v54, v138\n" \
"v_mul_f32_e32 v139, v54, v139\n" \
"v_mul_f32_e32 v140, v54, v140\n" \
"v_mul_f32_e32 v141, v54, v141\n" \
"v_mul_f32_e32 v142, v54, v142\n" \
"v_mul_f32_e32 v143, v54, v143\n" \
"v_add_f32_e32 v55, v55, v23\n" \
C_"v68, v136, v137\n" \
C_"v69, v138, v139\n" \
C_"v70, v140, v141\n" \
C_"v71, v142, v143\n" \
"v_add_f32_e32 v55, v55, v43\n" \
"v_mul_f32_e32 v144, v54, v144\n" \
M_"a[112:115], a[120:123], v[68:71], a[112:115]\n" \
"v_mul_f32_e32 v145, v54, v145\n" \
"v_mul_f32_e32 v146, v54, v146\n" \
"v_mul_f32_e32 v147, v54, v147\n" \
"v_mul_f32_e32 v148, v54, v148\n" \
B_"v0, s[80:83], s92 offen offset:0 lds\n" \
"v_mul_f32_e32 v149, v54, v149\n" \
"v_mul_f32_e32 v150, v54, v150\n" \
"v_mul_f32_e32 v151, v54, v151\n" \
"v_max_f32_e32 v62, v62, v55\n" \
C_"v72, v144, v145\n" \
C_"v73, v146, v147\n" \
C_"v74, v148, v149\n" \
C_"v75, v150, v151\n" \
"v_rcp_f32_e32 v55, v55\n" \
M_"v[184:187], a[56:59], v[120:123], v[184:187]\n" \
M_"a[112:115], a[120:123], v[72:75], a[112:115]\n" \
"v_mul_f32_e32 v152, v54, v152\n" \
"v_mul_f32_e32 v153, v54, v153\n" \
"v_mul_f32_e32 v154, v54, v154\n" \
"v_mul_f32_e32 v155, v54, v155\n" \
C_"v76, v152, v153\n" \
B_"v1, s[80:83], s92 offen offset:1024 lds\n" \
C_"v77, v154, v155\n" \
"v_mul_f32_e32 v55, v55, v59\n" \
"v_mul_f32_e32 v160, v55, v160\n" \
"v_mul_f32_e32 v161, v55, v161\n" \
"v_mul_f32_e32 v162, v55, v162\n" \
"v_mul_f32_e32 v163, v55, v163\n" \
"v_mul_f32_e32 v164, v55, v164\n" \
"v_mul_f32_e32 v165, v55, v165\n" \
"v_mul_f32_e32 v166, v55, v166\n" \
"v_mul_f32_e32 v167, v55, v167\n" \
C_"v80, v160, v161\n" \
C_"v81, v162, v163\n" \
C_"v82, v164, v165\n" \
C_"v83, v166, v167\n" \
M_"v[156:159], a[60:63], v[116:119], v[156:159]\n" \
"v_mul_f32_e32 v168, v55, v168\n" \
"v_mul_f32_e32 v169, v55, v169\n" \
"v_mul_f32_e32 v170, v55, v170\n" \
"v_mul_f32_e32 v171, v55, v171\n" \
"v_mul_f32_e32 v172, v55, v172\n" \
B_"v2, s[80:83], s92 offen offset:2048 lds\n" \
"v_mul_f32_e32 v173, v55, v173\n" \
"v_mul_f32_e32 v174, v55, v174\n" \
"v_mul_f32_e32 v156, v54, v156\n" \
"v_mul_f32_e32 v157, v54, v157\n" \
"v_mul_f32_e32 v158, v54, v158\n" \
"v_mul_f32_e32 v159, v54, v159\n" \
C_"v78, v156, v157\n" \
C_"v79, v158, v159\n" \
"v_mul_f32_e32 v175, v55, v175\n" \
C_"v84, v168, v169\n" \
M_"a[112:115], a[120:123], v[76:79], a[112:115]\n" \
M_"a[112:115], a[120:123], v[80:83], a[112:115]\n" \
C_"v85, v170, v171\n" \
C_"v86, v172, v173\n" \
C_"v87, v174, v175\n" \
"v_mul_f32_e32 v176, v55, v176\n" \
"v_mul_f32_e32 v177, v55, v177\n" \
B_"v3, s[80:83], s92 offen offset:3072 lds\n" \
"s_add_u32 m0, s93, 0x1000\n" \
M_"a[112:115], a[120:123], v[84:87], a[112:115]\n" \
"v_mul_f32_e32 v178, v55, v178\n" \
"v_mul_f32_e32 v179, v55, v179\n" \
"v_mul_f32_e32 v180, v55, v180\n" \
"v_mul_f32_e32 v181, v55, v181\n" \
"v_mul_f32_e32 v182, v55, v182\n" \
"v_mul_f32_e32 v183, v55, v183\n" \
C_"v96, v176, v177\n" \
C_"v97, v178, v179\n" \
C_"v98, v180, v181\n" \
C_"v99, v182, v183\n" \
M_"v[188:191], a[60:63], v[120:123], v[188:191]\n" \
"v_mul_f32_e32 v184, v55, v184\n" \
M_"a[112:115], a[120:123], v[96:99], a[112:115]\n" \
"v_mul_f32_e32 v185, v55, v185\n" \
"v_mul_f32_e32 v186, v55, v186\n" \
B_"v4, s[80:83], s92 offen offset:0 lds\n" \
"v_mul_f32_e32 v187, v55, v187\n" \
C_"v100, v184, v185\n" \
C_"v101, v186, v187\n" \
"v_mul_f32_e32 v188, v55, v188\n" \
"v_mul_f32_e32 v189, v55, v189\n" \
"v_mul_f32_e32 v190, v55, v190\n" \
"v_mul_f32_e32 v191, v55, v191\n" \
C_"v102, v188, v189\n" \
C_"v103, v190, v191\n" \
"v_cmp_lt_f32_e64 s[72:73], s21, v62\n" \
B_"v5, s[80:83], s92 offen offset:1024 lds\n" \
M_"a[112:115], a[120:123], v[100:103], a[112:115]\n" \
B_"v6, s[80:83], s92 offen offset:2048 lds\n" \
B_"v7, s[80:83], s92 offen offset:3072 lds\n" \
"s_add_u32 m0, s93, 0x2000\n" \
"s_nop 0\n" \
B_"v8, s[80:83], s92 offen offset:0 lds\n" \
"s_nop 2\n" \
"v_accvgpr_read_b32 v63, a112\n" \
"v_cmp_u_f32_e32 vcc, v63, v63\n" \
"s_nop 0\n" \
"s_or_b64 vcc, s[72:73], vcc\n" \
"s_cbranch_scc1 L_end_%=\n" \
"s_branch L_out_%=\n" \
".fill (L_ab + 24576 - .) / 4, 4, 0xBF800000\n" \
".fill (L_ab + 24576 - .) / 4, 4, 0xBF800000\n" \
"L_ap6:\n" \
"s_setpc_b64 s[88:89]\n" \
"L_gs7:\n" \
M_"v[104:107], a[128:131], a[16:19], v[36:39]\n" \
"s_add_u32 s68, s60, 0xfff6e000\n" \
"s_add_u32 s69, s60, 0xfffb7000\n" \
"s_add_u32 s70, s60, 0x0\n" \
"s_add_u32 s71, s60, 0x49000\n" \
M_"v[104:107], a[132:135], a[20:23], v[104:107]\n" \
"s_xor_b32 m0, s65, 0x12000\n" \
R_"a[160:163], v49 offset:0\n" \
M_"v[104:107], a[136:139], a[24:27], v[104:107]\n" \
B_"v50, s[8:11], s64 offen offset:0 sc1 lds\n" \
M_"v[104:107], a[140:143], a[28:31], v[104:107]\n" \
R_"a[164:167], v49 offset:1024\n" \
R_"a[168:171], v49 offset:2048\n" \
M_"v[108:111], a[156:159], a[28:31], v[36:39]\n" \
R_"a[172:175], v49 offset:3072\n" \
R_"a[176:179], v49 offset:4096\n" \
M_"v[108:111], a[152:155], a[24:27], v[108:111]\n" \
B_"v50, s[8:11], s64 offen offset:1024 sc1 lds\n" \
M_"v[108:111], a[148:151], a[20:23], v[108:111]\n" \
"s_mov_b32 s65, s13\n" \
"v_add_u32_e32 v63, s15, v20\n" \
M_"v[108:111], a[144:147], a[16:19], v[108:111]\n" \
"s_add_u32 s67, s14, 40960\n" \
"v_add_u32_e32 v50, 0x6000, v63\n" \
M_"v[116:119], a[144:147], a[32:35], v[40:43]\n" \
"s_xor_b32 m0, s65, 0x8000\n" \
R_"a[180:183], v49 offset:5120\n" \
M_"v[116:119], a[148:151], a[36:39], v[116:119]\n" \
R_"a[184:187], v49 offset:6144\n" \
"v_and_b32_e32 v68, v12, v68\n" \
M_"v[116:119], a[152:155], a[40:43], v[116:119]\n" \
"v_and_b32_e32 v69, v13, v69\n" \
"v_and_b32_e32 v70, v14, v70\n" \
M_"v[116:119], a[156:159], a[44:47], v[116:119]\n" \
B_"v50, s[8:11], s67 offen offset:0 sc1 lds\n" \
M_"v[112:115], a[140:143], a[44:47], v[40:43]\n" \
"v_and_b32_e32 v71, v15, v71\n" \
R_"a[188:191], v49 offset:7168\n" \
M_"v[112:115], a[136:139], a[40:43], v[112:115]\n" \
E_"v86, v86\n" \
M_"v[112:115], a[132:135], a[36:39], v[112:115]\n" \
E_"v87, v87\n" \
M_"v[112:115], a[128:131], a[32:35], v[112:115]\n" \
E_"v84, v84\n" \
M_"v[120:123], a[128:131], a[48:51], v[44:47]\n" \
B_"v50, s[8:11], s67 offen offset:1024 sc1 lds\n" \
M_"v[120:123], a[132:135], a[52:55], v[120:123]\n" \
E_"v85, v85\n" \
M_"v[120:123], a[136:139], a[56:59], v[120:123]\n" \
C_"v87, v86, v87\n" \
C_"v86, v84, v85\n" \
M_"v[120:123], a[140:143], a[60:63], v[120:123]\n" \
E_"v82, v82\n" \
M_"v[124:127], a[156:159], a[60:63], v[44:47]\n" \
E_"v83, v83\n" \
M_"v[124:127], a[152:155], a[56:59], v[124:127]\n" \
E_"v80, v80\n" \
M_"v[124:127], a[148:151], a[52:55], v[124:127]\n" \
B_"v50, s[8:11], s67 offen offset:2048 sc1 lds\n" \
M_"v[124:127], a[144:147], a[48:51], v[124:127]\n" \
C_"v85, v82, v83\n" \
"s_waitcnt lgkmcnt(1)\n" \
M_"v[128:131], a[160:163], v[68:71], v[128:131]\n" \
E_"v81, v81\n" \
M_"v[160:163], a[160:163], v[72:75], v[160:163]\n" \
C_"v84, v80, v81\n" \
"s_mov_b32 s64, s14\n" \
M_"v[132:135], a[164:167], v[68:71], v[132:135]\n" \
E_"v88, v88\n" \
M_"v[164:167], a[164:167], v[72:75], v[164:167]\n" \
E_"v89, v89\n" \
M_"v[136:139], a[168:171], v[68:71], v[136:139]\n" \
B_"v50, s[8:11], s67 offen offset:3072 sc1 lds\n" \
M_"v[168:171], a[168:171], v[72:75], v[168:171]\n" \
E_"v90, v90\n" \
M_"v[140:143], a[172:175], v[68:71], v[140:143]\n" \
E_"v91, v91\n" \
M_"v[172:175], a[172:175], v[72:75], v[172:175]\n" \
C_"v88, v88, v89\n" \
C_"v89, v90, v91\n" \
M_"v[144:147], a[176:179], v[68:71], v[144:147]\n" \
E_"v92, v92\n" \
M_"v[176:179], a[176:179], v[72:75], v[176:179]\n" \
E_"v93, v93\n" \
M_"v[148:151], a[180:183], v[68:71], v[148:151]\n" \
E_"v94, v94\n" \
M_"v[180:183], a[180:183], v[72:75], v[180:183]\n" \
E_"v95, v95\n" \
M_"v[152:155], a[184:187], v[68:71], v[152:155]\n" \
C_"v90, v92, v93\n" \
C_"v91, v94, v95\n" \
M_"v[184:187], a[184:187], v[72:75], v[184:187]\n" \
"v_add_u32_e32 v51, 0x12000, v63\n" \
"s_waitcnt lgkmcnt(0)\n" \
M_"v[156:159], a[188:191], v[68:71], v[156:159]\n" \
"s_xor_b32 m0, s65, 0xa000\n" \
"ds_read_b32 v58, v18 offset:512\n" \
M_"v[188:191], a[188:191], v[72:75], v[188:191]\n" \
B_"v51, s[8:11], s64 offen offset:0 sc1 lds\n" \
"v_smfmac_f32_16x16x64_bf16 a[96:99], a[104:107], v[68:75], v21\n" \
"ds_read_b32 v59, v18 offset:640\n" \
"ds_read_b32 v60, v18 offset:768\n" \
M_"v[220:223], a[188:191], v[84:87], v[220:223]\n" \
B_"v51, s[8:11], s64 offen offset:1024 sc1 lds\n" \
M_"v[252:255], a[188:191], v[88:91], v[252:255]\n" \
"ds_read_b32 v61, v18 offset:896\n" \
R_"a[128:131], v48 offset:0\n" \
M_"v[216:219], a[184:187], v[84:87], v[216:219]\n" \
R_"a[144:147], v48 offset:4096\n" \
R_"a[132:135], v48 offset:1024\n" \
M_"v[248:251], a[184:187], v[88:91], v[248:251]\n" \
R_"a[148:151], v48 offset:5120\n" \
R_"a[136:139], v48 offset:2048\n" \
M_"v[212:215], a[180:183], v[84:87], v[212:215]\n" \
R_"a[152:155], v48 offset:6144\n" \
R_"a[140:143], v48 offset:3072\n" \
M_"v[244:247], a[180:183], v[88:91], v[244:247]\n" \
R_"a[156:159], v48 offset:7168\n" \
"v_add_u32_e32 v52, 0x14000, v63\n" \
M_"v[208:211], a[176:179], v[84:87], v[208:211]\n" \
B_"v51, s[8:11], s64 offen offset:2048 sc1 lds\n" \
M_"v[240:243], a[176:179], v[88:91], v[240:243]\n" \
E_"v104, v104\n" \
M_"v[204:207], a[172:175], v[84:87], v[204:207]\n" \
E_"v105, v105\n" \
M_"v[236:239], a[172:175], v[88:91], v[236:239]\n" \
B_"v51, s[8:11], s64 offen offset:3072 sc1 lds\n" \
M_"v[200:203], a[168:171], v[84:87], v[200:203]\n" \
E_"v106, v106\n" \
M_"v[232:235], a[168:171], v[88:91], v[232:235]\n" \
E_"v107, v107\n" \
M_"v[196:199], a[164:167], v[84:87], v[196:199]\n" \
C_"v104, v104, v105\n" \
C_"v105, v106, v107\n" \
M_"v[228:231], a[164:167], v[88:91], v[228:231]\n" \
E_"v108, v108\n" \
M_"v[192:195], a[160:163], v[84:87], v[192:195]\n" \
E_"v109, v109\n" \
M_"v[224:227], a[160:163], v[88:91], v[224:227]\n" \
E_"v110, v110\n" \
"v_smfmac_f32_16x16x64_bf16 a[100:103], a[104:107], v[84:91], v21\n" \
E_"v111, v111\n" \
C_"v106, v108, v109\n" \
C_"v107, v110, v111\n" \
"v_and_b32_e32 v104, v12, v104\n" \
"v_and_b32_e32 v105, v13, v105\n" \
"v_and_b32_e32 v106, v14, v106\n" \
"v_and_b32_e32 v107, v15, v107\n" \
"v_accvgpr_read_b32 v54, a96\n" \
"v_accvgpr_read_b32 v22, a97\n" \
"v_readlane_b32 s93, v11, 22\n" \
"v_readlane_b32 s80, v11, 11\n" \
"v_readlane_b32 s81, v11, 12\n" \
"s_mov_b32 s82, 0x47fca0\n" \
"s_mov_b32 s83, 0x20000\n" \
"v_readlane_b32 s92, v11, 29\n" \
E_"v118, v118\n" \
E_"v119, v119\n" \
E_"v116, v116\n" \
E_"v117, v117\n" \
C_"v119, v118, v119\n" \
E_"v114, v114\n" \
E_"v115, v115\n" \
E_"v112, v112\n" \
C_"v118, v116, v117\n" \
"s_waitcnt vmcnt(10) lgkmcnt(0)\n" \
"s_barrier\n" \
M_"v[80:83], a[128:131], a[32:35], v[40:43]\n" \
R_"a[160:163], v49 offset:8192\n" \
R_"a[164:167], v49 offset:9216\n" \
M_"v[80:83], a[132:135], a[36:39], v[80:83]\n" \
R_"a[168:171], v49 offset:10240\n" \
"s_xor_b32 m0, s65, 0x18000\n" \
M_"v[80:83], a[136:139], a[40:43], v[80:83]\n" \
B_"v52, s[8:11], s64 offen offset:0 sc1 lds\n" \
M_"v[80:83], a[140:143], a[44:47], v[80:83]\n" \
R_"a[172:175], v49 offset:11264\n" \
R_"a[176:179], v49 offset:12288\n" \
M_"v[84:87], a[156:159], a[44:47], v[40:43]\n" \
R_"a[180:183], v49 offset:13312\n" \
R_"a[184:187], v49 offset:14336\n" \
M_"v[84:87], a[152:155], a[40:43], v[84:87]\n" \
B_"v52, s[8:11], s64 offen offset:1024 sc1 lds\n" \
M_"v[84:87], a[148:151], a[36:39], v[84:87]\n" \
R_"a[188:191], v49 offset:15360\n" \
C_"v117, v114, v115\n" \
M_"v[84:87], a[144:147], a[32:35], v[84:87]\n" \
B_"v52, s[8:11], s64 offen offset:2048 sc1 lds\n" \
M_"v[92:95], a[144:147], a[48:51], v[44:47]\n" \
E_"v113, v113\n" \
M_"v[92:95], a[148:151], a[52:55], v[92:95]\n" \
B_"v52, s[8:11], s64 offen offset:3072 sc1 lds\n" \
M_"v[92:95], a[152:155], a[56:59], v[92:95]\n" \
"s_add_u32 m0, s93, 0x0\n" \
C_"v116, v112, v113\n" \
M_"v[92:95], a[156:159], a[60:63], v[92:95]\n" \
E_"v120, v120\n" \
M_"v[88:91], a[140:143], a[60:63], v[44:47]\n" \
B_"v0, s[80:83], s92 offen offset:0 lds\n" \
M_"v[88:91], a[136:139], a[56:59], v[88:91]\n" \
E_"v121, v121\n" \
M_"v[88:91], a[132:135], a[52:55], v[88:91]\n" \
E_"v122, v122\n" \
M_"v[88:91], a[128:131], a[48:51], v[88:91]\n" \
B_"v1, s[80:83], s92 offen offset:1024 lds\n" \
M_"a[96:99], a[116:119], v[104:107], a[96:99]\n" \
C_"v120, v120, v121\n" \
"s_waitcnt lgkmcnt(5)\n" \
M_"v[160:163], a[160:163], v[104:107], v[160:163]\n" \
E_"v123, v123\n" \
M_"v[164:167], a[164:167], v[104:107], v[164:167]\n" \
B_"v2, s[80:83], s92 offen offset:2048 lds\n" \
M_"v[168:171], a[168:171], v[104:107], v[168:171]\n" \
C_"v121, v122, v123\n" \
"s_waitcnt lgkmcnt(1)\n" \
M_"v[172:175], a[172:175], v[104:107], v[172:175]\n" \
E_"v124, v124\n" \
M_"v[176:179], a[176:179], v[104:107], v[176:179]\n" \
E_"v125, v125\n" \
M_"v[180:183], a[180:183], v[104:107], v[180:183]\n" \
E_"v126, v126\n" \
M_"v[184:187], a[184:187], v[104:107], v[184:187]\n" \
C_"v122, v124, v125\n" \
"s_waitcnt lgkmcnt(0)\n" \
M_"v[188:191], a[188:191], v[104:107], v[188:191]\n" \
E_"v127, v127\n" \
M_"v[220:223], a[188:191], v[116:119], v[220:223]\n" \
C_"v123, v126, v127\n" \
B_"v3, s[80:83], s92 offen offset:3072 lds\n" \
"s_add_u32 m0, s93, 0x1000\n" \
M_"v[252:255], a[188:191], v[120:123], v[252:255]\n" \
R_"a[128:131], v48 offset:8192\n" \
R_"a[144:147], v48 offset:12288\n" \
M_"v[216:219], a[184:187], v[116:119], v[216:219]\n" \
B_"v4, s[80:83], s92 offen offset:0 lds\n" \
M_"v[248:251], a[184:187], v[120:123], v[248:251]\n" \
R_"a[132:135], v48 offset:9216\n" \
R_"a[148:151], v48 offset:13312\n" \
M_"v[212:215], a[180:183], v[116:119], v[212:215]\n" \
B_"v5, s[80:83], s92 offen offset:1024 lds\n" \
M_"v[244:247], a[180:183], v[120:123], v[244:247]\n" \
R_"a[136:139], v48 offset:10240\n" \
R_"a[152:155], v48 offset:14336\n" \
M_"v[208:211], a[176:179], v[116:119], v[208:211]\n" \
B_"v6, s[80:83], s92 offen offset:2048 lds\n" \
M_"v[240:243], a[176:179], v[120:123], v[240:243]\n" \
R_"a[140:143], v48 offset:11264\n" \
R_"a[156:159], v48 offset:15360\n" \
M_"v[204:207], a[172:175], v[116:119], v[204:207]\n" \
B_"v7, s[80:83], s92 offen offset:3072 lds\n" \
M_"v[236:239], a[172:175], v[120:123], v[236:239]\n" \
"v_readlane_b32 s96, v11, 1\n" \
"v_lshrrev_b32_e32 v16, 1, v20\n" \
M_"v[200:203], a[168:171], v[116:119], v[200:203]\n" \
"v_add_f32_e32 v54, v54, v22\n" \
"v_max_f32_e32 v62, v62, v54\n" \
M_"v[232:235], a[168:171], v[120:123], v[232:235]\n" \
"v_rcp_f32_e32 v54, v54\n" \
M_"v[196:199], a[164:167], v[116:119], v[196:199]\n" \
"v_mul_f32_e32 v54, v54, v58\n" \
"v_mul_f32_e32 v128, v54, v128\n" \
M_"v[228:231], a[164:167], v[120:123], v[228:231]\n" \
"v_mul_f32_e32 v129, v54, v129\n" \
"v_mul_f32_e32 v136, v54, v136\n" \
M_"v[192:195], a[160:163], v[116:119], v[192:195]\n" \
"v_mul_f32_e32 v137, v54, v137\n" \
"v_mul_f32_e32 v144, v54, v144\n" \
M_"v[224:227], a[160:163], v[120:123], v[224:227]\n" \
"v_mul_f32_e32 v145, v54, v145\n" \
"v_mul_f32_e32 v152, v54, v152\n" \
"v_smfmac_f32_16x16x64_bf16 a[100:103], a[104:107], v[116:123], v21\n" \
"v_mul_f32_e32 v153, v54, v153\n" \
C_"v128, v128, v129\n" \
"v_mul_f32_e32 v130, v54, v130\n" \
"v_mul_f32_e32 v131, v54, v131\n" \
C_"v136, v136, v137\n" \
"v_mul_f32_e32 v138, v54, v138\n" \
"v_mul_f32_e32 v139, v54, v139\n" \
C_"v144, v144, v145\n" \
"v_mul_f32_e32 v146, v54, v146\n" \
"v_mul_f32_e32 v147, v54, v147\n" \
C_"v152, v152, v153\n" \
"v_mul_f32_e32 v154, v54, v154\n" \
"v_mul_f32_e32 v155, v54, v155\n" \
C_"v129, v130, v131\n" \
"v_mul_f32_e32 v132, v54, v132\n" \
"v_mul_f32_e32 v133, v54, v133\n" \
"v_mul_f32_e32 v134, v54, v134\n" \
"v_mul_f32_e32 v135, v54, v135\n" \
C_"v137, v138, v139\n" \
"v_mul_f32_e32 v140, v54, v140\n" \
"v_mul_f32_e32 v141, v54, v141\n" \
"v_mul_f32_e32 v142, v54, v142\n" \
"v_mul_f32_e32 v143, v54, v143\n" \
C_"v145, v146, v147\n" \
"v_mul_f32_e32 v148, v54, v148\n" \
"v_mul_f32_e32 v149, v54, v149\n" \
"v_mul_f32_e32 v150, v54, v150\n" \
"v_mul_f32_e32 v151, v54, v151\n" \
C_"v153, v154, v155\n" \
"v_mul_f32_e32 v156, v54, v156\n" \
"v_mul_f32_e32 v157, v54, v157\n" \
"v_mul_f32_e32 v158, v54, v158\n" \
"v_mul_f32_e32 v159, v54, v159\n" \
C_"v130, v132, v133\n" \
C_"v131, v134, v135\n" \
"buffer_store_dwordx4 v[128:131], v20, s[56:59], s68 offen offset:0 sc1\n" \
C_"v138, v140, v141\n" \
C_"v139, v142, v143\n" \
C_"v146, v148, v149\n" \
C_"v147, v150, v151\n" \
C_"v154, v156, v157\n" \
C_"v155, v158, v159\n" \
"buffer_store_dwordx4 v[136:139], v20, s[56:59], s68 offen offset:1024 sc1\n" \
"s_add_u32 m0, s93, 0x2000\n" \
"v_add_u32_e32 v49, 0x10000, v48\n" \
"s_xor_b32 s65, s65, 0x8000\n" \
"s_add_u32 s64, s64, 0x10000\n" \
E_"v86, v86\n" \
"buffer_store_dwordx4 v[144:147], v20, s[56:59], s68 offen offset:2048 sc1\n" \
E_"v87, v87\n" \
E_"v84, v84\n" \
E_"v85, v85\n" \
"buffer_store_dwordx4 v[152:155], v20, s[56:59], s68 offen offset:3072 sc1\n" \
E_"v88, v88\n" \
E_"v89, v89\n" \
C_"v87, v86, v87\n" \
E_"v82, v82\n" \
B_"v8, s[80:83], s92 offen offset:0 lds\n" \
E_"v83, v83\n" \
E_"v80, v80\n" \
E_"v81, v81\n" \
E_"v90, v90\n" \
E_"v91, v91\n" \
C_"v86, v84, v85\n" \
C_"v88, v88, v89\n" \
E_"v92, v92\n" \
E_"v93, v93\n" \
E_"v94, v94\n" \
E_"v95, v95\n" \
C_"v85, v82, v83\n" \
C_"v84, v80, v81\n" \
C_"v89, v90, v91\n" \
"v_and_b32_e32 v84, v12, v84\n" \
"v_and_b32_e32 v85, v13, v85\n" \
"v_and_b32_e32 v86, v14, v86\n" \
"s_waitcnt vmcnt(17) lgkmcnt(0)\n" \
"s_barrier\n" \
M_"v[120:123], a[128:131], a[48:51], v[44:47]\n" \
"buffer_load_dwordx2 v[26:27], v16, s[56:59], s96 offen sc1\n" \
M_"v[120:123], a[132:135], a[52:55], v[120:123]\n" \
R_"a[188:191], v49 offset:7168\n" \
R_"a[184:187], v49 offset:6144\n" \
M_"v[120:123], a[136:139], a[56:59], v[120:123]\n" \
"s_xor_b32 m0, s65, 0x12000\n" \
R_"a[180:183], v49 offset:5120\n" \
M_"v[120:123], a[140:143], a[60:63], v[120:123]\n" \
B_"v50, s[8:11], s64 offen offset:0 sc1 lds\n" \
M_"v[124:127], a[156:159], a[60:63], v[44:47]\n" \
R_"a[176:179], v49 offset:4096\n" \
R_"a[172:175], v49 offset:3072\n" \
M_"v[124:127], a[152:155], a[56:59], v[124:127]\n" \
B_"v50, s[8:11], s64 offen offset:1024 sc1 lds\n" \
M_"v[124:127], a[148:151], a[52:55], v[124:127]\n" \
R_"a[168:171], v49 offset:2048\n" \
"v_and_b32_e32 v87, v15, v87\n" \
M_"v[124:127], a[144:147], a[48:51], v[124:127]\n" \
C_"v90, v92, v93\n" \
C_"v91, v94, v95\n" \
M_"v[32:35], a[124:127], a[64:67], 0\n" \
B_"v50, s[8:11], s64 offen offset:2048 sc1 lds\n" \
M_"v[36:39], a[124:127], a[80:83], 0\n" \
R_"a[164:167], v49 offset:1024\n" \
R_"a[160:163], v49 offset:0\n" \
M_"v[32:35], a[224:227], a[68:71], v[32:35]\n" \
R_"a[0:3], v49 offset:8192\n" \
R_"a[4:7], v49 offset:9216\n" \
M_"v[36:39], a[224:227], a[84:87], v[36:39]\n" \
R_"a[8:11], v49 offset:10240\n" \
R_"a[12:15], v49 offset:11264\n" \
M_"v[32:35], a[228:231], a[72:75], v[32:35]\n" \
B_"v50, s[8:11], s64 offen offset:3072 sc1 lds\n" \
M_"v[36:39], a[228:231], a[88:91], v[36:39]\n" \
R_"a[16:19], v49 offset:12288\n" \
R_"a[20:23], v49 offset:13312\n" \
M_"v[32:35], a[232:235], a[76:79], v[32:35]\n" \
R_"a[24:27], v49 offset:14336\n" \
R_"a[28:31], v49 offset:15360\n" \
M_"v[36:39], a[232:235], a[92:95], v[36:39]\n" \
E_"v120, v120\n" \
"v_smfmac_f32_16x16x64_bf16 a[100:103], a[104:107], v[84:91], v21\n" \
"s_mov_b32 s63, s62\n" \
"s_waitcnt lgkmcnt(13)\n" \
M_"v[220:223], a[188:191], v[84:87], v[220:223]\n" \
E_"v121, v121\n" \
M_"v[252:255], a[188:191], v[88:91], v[252:255]\n" \
E_"v122, v122\n" \
M_"v[216:219], a[184:187], v[84:87], v[216:219]\n" \
E_"v123, v123\n" \
M_"v[248:251], a[184:187], v[88:91], v[248:251]\n" \
C_"v120, v120, v121\n" \
C_"v121, v122, v123\n" \
M_"v[212:215], a[180:183], v[84:87], v[212:215]\n" \
E_"v124, v124\n" \
M_"v[244:247], a[180:183], v[88:91], v[244:247]\n" \
"v_and_b32_e32 v120, v12, v120\n" \
"s_waitcnt lgkmcnt(10)\n" \
M_"v[208:211], a[176:179], v[84:87], v[208:211]\n" \
E_"v125, v125\n" \
M_"v[240:243], a[176:179], v[88:91], v[240:243]\n" \
E_"v126, v126\n" \
M_"v[204:207], a[172:175], v[84:87], v[204:207]\n" \
E_"v127, v127\n" \
M_"v[236:239], a[172:175], v[88:91], v[236:239]\n" \
C_"v122, v124, v125\n" \
C_"v123, v126, v127\n" \
M_"v[200:203], a[168:171], v[84:87], v[200:203]\n" \
"v_and_b32_e32 v121, v13, v121\n" \
"v_and_b32_e32 v122, v14, v122\n" \
M_"v[232:235], a[168:171], v[88:91], v[232:235]\n" \
"v_and_b32_e32 v123, v15, v123\n" \
"s_waitcnt lgkmcnt(0)\n" \
M_"v[196:199], a[164:167], v[84:87], v[196:199]\n" \
"v_accvgpr_read_b32 v56, a100\n" \
"v_accvgpr_read_b32 v24, a101\n" \
M_"v[228:231], a[164:167], v[88:91], v[228:231]\n" \
"v_readlane_b32 s97, v11, 39\n" \
"v_add_u32_e32 v17, 0x49000, v20\n" \
M_"v[192:195], a[160:163], v[84:87], v[192:195]\n" \
"v_readlane_b32 s98, v11, 40\n" \
"v_add_u32_e32 v48, s16, v20\n" \
M_"v[224:227], a[160:163], v[88:91], v[224:227]\n" \
R_"a[128:131], v48 offset:0\n" \
R_"a[144:147], v48 offset:4096\n" \
M_"a[100:103], a[116:119], v[120:123], a[100:103]\n" \
R_"a[132:135], v48 offset:1024\n" \
R_"a[148:151], v48 offset:5120\n" \
M_"v[224:227], a[0:3], v[120:123], v[224:227]\n" \
R_"a[136:139], v48 offset:2048\n" \
R_"a[152:155], v48 offset:6144\n" \
M_"v[228:231], a[4:7], v[120:123], v[228:231]\n" \
R_"a[140:143], v48 offset:3072\n" \
R_"a[156:159], v48 offset:7168\n" \
M_"v[232:235], a[8:11], v[120:123], v[232:235]\n" \
"v_accvgpr_read_b32 v55, a98\n" \
"v_accvgpr_read_b32 v23, a99\n" \
M_"a[96:99], a[120:123], a[120:123], 0\n" \
"v_add_f32_e32 v55, v55, v23\n" \
"v_max_f32_e32 v62, v62, v55\n" \
M_"v[236:239], a[12:15], v[120:123], v[236:239]\n" \
"v_rcp_f32_e32 v55, v55\n" \
M_"v[240:243], a[16:19], v[120:123], v[240:243]\n" \
"v_mul_f32_e32 v55, v55, v59\n" \
"v_mul_f32_e32 v160, v55, v160\n" \
M_"v[244:247], a[20:23], v[120:123], v[244:247]\n" \
"v_mul_f32_e32 v161, v55, v161\n" \
"v_mul_f32_e32 v168, v55, v168\n" \
M_"v[248:251], a[24:27], v[120:123], v[248:251]\n" \
"v_mul_f32_e32 v169, v55, v169\n" \
"v_mul_f32_e32 v176, v55, v176\n" \
M_"v[252:255], a[28:31], v[120:123], v[252:255]\n" \
"v_mul_f32_e32 v177, v55, v177\n" \
"v_mul_f32_e32 v184, v55, v184\n" \
"v_mul_f32_e32 v185, v55, v185\n" \
C_"v160, v160, v161\n" \
"v_mul_f32_e32 v162, v55, v162\n" \
"v_mul_f32_e32 v163, v55, v163\n" \
C_"v168, v168, v169\n" \
"v_mul_f32_e32 v170, v55, v170\n" \
"v_mul_f32_e32 v171, v55, v171\n" \
"s_waitcnt vmcnt(4)\n" \
"v_cmp_ne_u64_e32 vcc, s[62:63], v[26:27]\n" \
C_"v176, v176, v177\n" \
"v_mul_f32_e32 v178, v55, v178\n" \
"v_mul_f32_e32 v179, v55, v179\n" \
C_"v184, v184, v185\n" \
"v_mul_f32_e32 v186, v55, v186\n" \
"s_cbranch_vccnz L_sos_%=\n" \
"L_ros_%=:\n" \
B_"a[124:127], v20, s[56:59], s97 offen offset:0 sc1\n" \
"v_mul_f32_e32 v187, v55, v187\n" \
C_"v161, v162, v163\n" \
"v_mul_f32_e32 v164, v55, v164\n" \
"v_mul_f32_e32 v165, v55, v165\n" \
"v_mul_f32_e32 v166, v55, v166\n" \
"v_mul_f32_e32 v167, v55, v167\n" \
B_"a[228:231], v17, s[56:59], s97 offen offset:0 sc1\n" \
C_"v169, v170, v171\n" \
"v_mul_f32_e32 v172, v55, v172\n" \
"v_mul_f32_e32 v173, v55, v173\n" \
"v_mul_f32_e32 v174, v55, v174\n" \
"v_mul_f32_e32 v175, v55, v175\n" \
C_"v177, v178, v179\n" \
B_"a[236:239], v20, s[56:59], s98 offen offset:0 sc1\n" \
"v_mul_f32_e32 v180, v55, v180\n" \
"v_mul_f32_e32 v181, v55, v181\n" \
"v_mul_f32_e32 v182, v55, v182\n" \
"v_mul_f32_e32 v183, v55, v183\n" \
C_"v185, v186, v187\n" \
"v_mul_f32_e32 v188, v55, v188\n" \
B_"a[244:247], v17, s[56:59], s98 offen offset:0 sc1\n" \
"v_mul_f32_e32 v189, v55, v189\n" \
"v_mul_f32_e32 v190, v55, v190\n" \
"v_mul_f32_e32 v191, v55, v191\n" \
C_"v162, v164, v165\n" \
C_"v163, v166, v167\n" \
C_"v170, v172, v173\n" \
"buffer_store_dwordx4 v[160:163], v20, s[56:59], s69 offen offset:0 sc1\n" \
C_"v171, v174, v175\n" \
C_"v178, v180, v181\n" \
C_"v179, v182, v183\n" \
C_"v186, v188, v189\n" \
C_"v187, v190, v191\n" \
"v_readlane_b32 s93, v11, 23\n" \
"buffer_store_dwordx4 v[168:171], v20, s[56:59], s69 offen offset:1024 sc1\n" \
"v_readlane_b32 s84, v11, 15\n" \
"v_readlane_b32 s85, v11, 16\n" \
"v_readlane_b32 s86, v11, 17\n" \
"s_mov_b32 s87, 0x20000\n" \
"v_readlane_b32 s92, v11, 18\n" \
"v_readlane_b32 s91, v11, 24\n" \
"buffer_store_dwordx4 v[176:179], v20, s[56:59], s69 offen offset:2048 sc1\n" \
M_"a[112:115], a[120:123], v[128:131], a[112:115]\n" \
M_"a[112:115], a[120:123], v[136:139], a[112:115]\n" \
"buffer_store_dwordx4 v[184:187], v20, s[56:59], s69 offen offset:3072 sc1\n" \
"v_mfma_f32_32x32x16_bf16 v[128:143], a[120:123], a[120:123], 0\n" \
M_"a[112:115], a[120:123], v[144:147], a[112:115]\n" \
B_"a[224:227], v20, s[56:59], s97 offen offset:1024 sc1\n" \
M_"a[112:115], a[120:123], v[152:155], a[112:115]\n" \
"v_mfma_f32_32x32x16_bf16 v[144:159], a[120:123], a[120:123], 0\n" \
B_"a[232:235], v17, s[56:59], s97 offen offset:1024 sc1\n" \
"v_add_f32_e32 v56, v56, v24\n" \
M_"a[112:115], a[120:123], v[160:163], a[112:115]\n" \
B_"a[240:243], v20, s[56:59], s98 offen offset:1024 sc1\n" \
"v_max_f32_e32 v62, v62, v56\n" \
"v_rcp_f32_e32 v56, v56\n" \
M_"a[112:115], a[120:123], v[168:171], a[112:115]\n" \
"v_mfma_f32_32x32x16_bf16 v[160:175], a[120:123], a[120:123], 0\n" \
B_"a[248:251], v17, s[56:59], s98 offen offset:1024 sc1\n" \
"v_mul_f32_e32 v56, v56, v60\n" \
M_"a[112:115], a[120:123], v[176:179], a[112:115]\n" \
"v_mul_f32_e32 v192, v56, v192\n" \
"v_mul_f32_e32 v193, v56, v193\n" \
"s_waitcnt vmcnt(17) lgkmcnt(0)\n" \
"s_barrier\n" \
M_"v[64:67], a[128:131], a[64:67], v[32:35]\n" \
"s_add_u32 m0, s93, 0x0\n" \
C_"v192, v192, v193\n" \
M_"v[64:67], a[132:135], a[68:71], v[64:67]\n" \
B_"v0, s[84:87], s92 offen offset:0 lds\n" \
M_"v[64:67], a[136:139], a[72:75], v[64:67]\n" \
"v_mul_f32_e32 v194, v56, v194\n" \
"v_mul_f32_e32 v195, v56, v195\n" \
M_"v[64:67], a[140:143], a[76:79], v[64:67]\n" \
B_"v1, s[84:87], s92 offen offset:1024 lds\n" \
M_"v[68:71], a[156:159], a[76:79], v[32:35]\n" \
M_"a[112:115], a[120:123], v[184:187], a[112:115]\n" \
"v_mfma_f32_32x32x16_bf16 v[176:191], a[120:123], a[120:123], 0\n" \
B_"v2, s[84:87], s92 offen offset:2048 lds\n" \
C_"v193, v194, v195\n" \
"v_mul_f32_e32 v196, v56, v196\n" \
"v_mul_f32_e32 v197, v56, v197\n" \
"v_mul_f32_e32 v198, v56, v198\n" \
M_"v[68:71], a[152:155], a[72:75], v[68:71]\n" \
B_"v3, s[84:87], s92 offen offset:3072 lds\n" \
M_"v[68:71], a[148:151], a[68:71], v[68:71]\n" \
"s_add_u32 m0, s91, 0x1000\n" \
"v_mul_f32_e32 v199, v56, v199\n" \
M_"v[68:71], a[144:147], a[64:67], v[68:71]\n" \
B_"v4, s[84:87], s92 offen offset:0 lds\n" \
M_"v[76:79], a[144:147], a[80:83], v[36:39]\n" \
R_"a[144:147], v48 offset:12288\n" \
"v_mul_f32_e32 v200, v56, v200\n" \
M_"v[76:79], a[148:151], a[84:87], v[76:79]\n" \
B_"v5, s[84:87], s92 offen offset:1024 lds\n" \
M_"v[76:79], a[152:155], a[88:91], v[76:79]\n" \
R_"a[148:151], v48 offset:13312\n" \
R_"a[152:155], v48 offset:14336\n" \
M_"v[76:79], a[156:159], a[92:95], v[76:79]\n" \
B_"v6, s[84:87], s92 offen offset:2048 lds\n" \
M_"v[72:75], a[140:143], a[92:95], v[36:39]\n" \
R_"a[140:143], v48 offset:11264\n" \
R_"a[156:159], v48 offset:15360\n" \
M_"v[72:75], a[136:139], a[88:91], v[72:75]\n" \
B_"v7, s[84:87], s92 offen offset:3072 lds\n" \
M_"v[72:75], a[132:135], a[84:87], v[72:75]\n" \
R_"a[132:135], v48 offset:9216\n" \
R_"a[136:139], v48 offset:10240\n" \
M_"v[72:75], a[128:131], a[80:83], v[72:75]\n" \
R_"a[128:131], v48 offset:8192\n" \
"v_mul_f32_e32 v201, v56, v201\n" \
"v_accvgpr_read_b32 v57, a102\n" \
"v_accvgpr_read_b32 v25, a103\n" \
M_"a[100:103], a[120:123], a[120:123], 0\n" \
C_"v194, v196, v197\n" \
C_"v195, v198, v199\n" \
"buffer_store_dwordx4 v[192:195], v20, s[56:59], s70 offen offset:0 sc1\n" \
C_"v200, v200, v201\n" \
"v_mul_f32_e32 v202, v56, v202\n" \
"v_mul_f32_e32 v203, v56, v203\n" \
"v_add_f32_e32 v57, v57, v25\n" \
M_"a[112:115], a[120:123], v[192:195], a[112:115]\n" \
C_"v201, v202, v203\n" \
"v_mul_f32_e32 v204, v56, v204\n" \
"v_mul_f32_e32 v205, v56, v205\n" \
"v_mul_f32_e32 v206, v56, v206\n" \
"v_mul_f32_e32 v207, v56, v207\n" \
"v_mul_f32_e32 v208, v56, v208\n" \
"v_mul_f32_e32 v209, v56, v209\n" \
"v_max_f32_e32 v62, v62, v57\n" \
C_"v202, v204, v205\n" \
C_"v203, v206, v207\n" \
"buffer_store_dwordx4 v[200:203], v20, s[56:59], s70 offen offset:1024 sc1\n" \
".fill (L_ab + 28672 - .) / 4, 4, 0xBF800000\n" \
"L_ap7:\n" \
"s_cbranch_execz L_ap0\n" \
C_"v208, v208, v209\n" \
"v_mul_f32_e32 v210, v56, v210\n" \
"v_mul_f32_e32 v211, v56, v211\n" \
"v_rcp_f32_e32 v57, v57\n" \
M_"a[112:115], a[120:123], v[200:203], a[112:115]\n" \
"v_mfma_f32_32x32x16_bf16 v[192:207], a[120:123], a[120:123], 0\n" \
C_"v209, v210, v211\n" \
"v_mul_f32_e32 v212, v56, v212\n" \
"v_mul_f32_e32 v213, v56, v213\n" \
"v_mul_f32_e32 v214, v56, v214\n" \
"v_mul_f32_e32 v215, v56, v215\n" \
"v_mul_f32_e32 v216, v56, v216\n" \
"v_mul_f32_e32 v217, v56, v217\n" \
C_"v210, v212, v213\n" \
C_"v211, v214, v215\n" \
"buffer_store_dwordx4 v[208:211], v20, s[56:59], s70 offen offset:2048 sc1\n" \
C_"v216, v216, v217\n" \
"v_mul_f32_e32 v218, v56, v218\n" \
"v_mul_f32_e32 v219, v56, v219\n" \
"v_mul_f32_e32 v57, v57, v61\n" \
M_"a[112:115], a[120:123], v[208:211], a[112:115]\n" \
C_"v217, v218, v219\n" \
"v_mul_f32_e32 v220, v56, v220\n" \
"v_mul_f32_e32 v221, v56, v221\n" \
"v_mul_f32_e32 v222, v56, v222\n" \
"v_mul_f32_e32 v223, v56, v223\n" \
"v_mul_f32_e32 v224, v57, v224\n" \
"v_mul_f32_e32 v225, v57, v225\n" \
C_"v218, v220, v221\n" \
C_"v219, v222, v223\n" \
"buffer_store_dwordx4 v[216:219], v20, s[56:59], s70 offen offset:3072 sc1\n" \
C_"v224, v224, v225\n" \
"v_mul_f32_e32 v226, v57, v226\n" \
"v_mul_f32_e32 v227, v57, v227\n" \
M_"a[112:115], a[120:123], v[216:219], a[112:115]\n" \
"v_mfma_f32_32x32x16_bf16 v[208:223], a[120:123], a[120:123], 0\n" \
C_"v225, v226, v227\n" \
"v_mul_f32_e32 v228, v57, v228\n" \
"v_mul_f32_e32 v229, v57, v229\n" \
"v_mul_f32_e32 v230, v57, v230\n" \
"v_mul_f32_e32 v231, v57, v231\n" \
"v_mul_f32_e32 v232, v57, v232\n" \
"v_mul_f32_e32 v233, v57, v233\n" \
C_"v226, v228, v229\n" \
C_"v227, v230, v231\n" \
"buffer_store_dwordx4 v[224:227], v20, s[56:59], s71 offen offset:0 sc1\n" \
C_"v232, v232, v233\n" \
"v_mul_f32_e32 v234, v57, v234\n" \
"v_mul_f32_e32 v235, v57, v235\n" \
M_"a[112:115], a[120:123], v[224:227], a[112:115]\n" \
C_"v233, v234, v235\n" \
"v_mul_f32_e32 v236, v57, v236\n" \
"v_mul_f32_e32 v237, v57, v237\n" \
"v_mul_f32_e32 v238, v57, v238\n" \
"v_mul_f32_e32 v239, v57, v239\n" \
"v_mul_f32_e32 v240, v57, v240\n" \
"v_mul_f32_e32 v241, v57, v241\n" \
C_"v234, v236, v237\n" \
C_"v235, v238, v239\n" \
"buffer_store_dwordx4 v[232:235], v20, s[56:59], s71 offen offset:1024 sc1\n" \
C_"v240, v240, v241\n" \
"v_mul_f32_e32 v242, v57, v242\n" \
"v_mul_f32_e32 v243, v57, v243\n" \
M_"a[112:115], a[120:123], v[232:235], a[112:115]\n" \
"v_mfma_f32_32x32x16_bf16 v[224:239], a[120:123], a[120:123], 0\n" \
C_"v241, v242, v243\n" \
"v_mul_f32_e32 v244, v57, v244\n" \
"v_mul_f32_e32 v245, v57, v245\n" \
"v_mul_f32_e32 v246, v57, v246\n" \
"v_mul_f32_e32 v247, v57, v247\n" \
"v_mul_f32_e32 v248, v57, v248\n" \
"v_mul_f32_e32 v249, v57, v249\n" \
C_"v242, v244, v245\n" \
C_"v243, v246, v247\n" \
"buffer_store_dwordx4 v[240:243], v20, s[56:59], s71 offen offset:2048 sc1\n" \
C_"v248, v248, v249\n" \
"v_mul_f32_e32 v250, v57, v250\n" \
"v_mul_f32_e32 v251, v57, v251\n" \
M_"a[112:115], a[120:123], v[240:243], a[112:115]\n" \
C_"v249, v250, v251\n" \
"v_mul_f32_e32 v252, v57, v252\n" \
"v_mul_f32_e32 v253, v57, v253\n" \
"v_mul_f32_e32 v254, v57, v254\n" \
"v_mul_f32_e32 v255, v57, v255\n" \
C_"v250, v252, v253\n" \
C_"v251, v254, v255\n" \
"buffer_store_dwordx4 v[248:251], v20, s[56:59], s71 offen offset:3072 sc1\n" \
"s_add_u32 m0, s91, 0x2000\n" \
M_"a[112:115], a[120:123], v[248:251], a[112:115]\n" \
"v_mfma_f32_32x32x16_bf16 v[240:255], a[120:123], a[120:123], 0\n" \
"v_add_u32_e32 v53, 0x20000, v63\n" \
B_"v8, s[84:87], s92 offen offset:0 lds\n" \
E_"v70, v70\n" \
E_"v71, v71\n" \
E_"v72, v72\n" \
E_"v73, v73\n" \
"s_add_u32 s64, s64, 0x10000\n" \
"v_accvgpr_read_b32 v63, a112\n" \
"v_cmp_u_f32_e32 vcc, v63, v63\n" \
"v_cmp_lt_f32_e64 s[72:73], s21, v62\n" \
"v_readlane_b32 s95, v11, 3\n" \
E_"v68, v68\n" \
E_"v69, v69\n" \
E_"v74, v74\n" \
E_"v75, v75\n" \
"s_or_b64 vcc, s[72:73], vcc\n" \
"s_cselect_b32 s95, 0x40000000, s95\n" \
"s_cselect_b32 s94, 0x7f800000, 0\n" \
C_"v71, v70, v71\n" \
E_"v66, v66\n" \
E_"v67, v67\n" \
E_"v64, v64\n" \
E_"v65, v65\n" \
C_"v72, v72, v73\n" \
E_"v76, v76\n" \
E_"v77, v77\n" \
E_"v78, v78\n" \
E_"v79, v79\n" \
C_"v70, v68, v69\n" \
"s_waitcnt vmcnt(17) lgkmcnt(0)\n" \
"s_barrier\n" \
C_"v73, v74, v75\n" \
"v_add_u32_e32 v49, 0x10000, v48\n" \
"s_xor_b32 s65, s65, 0x8000\n" \
C_"v69, v66, v67\n" \
C_"v68, v64, v65\n" \
C_"v74, v76, v77\n" \
C_"v75, v78, v79\n" \
"v_xor_b32_e32 v48, 0x8000, v48\n" \
"s_sub_u32 s66, s19, 2\n" \
"s_branch L_f1_%=\n" \
".fill (L_ab + 32768 - .) / 4, 4, 0xBF800000\n" \
".fill (L_ab + 32768 - .) / 4, 4, 0xBF800000\n" \
"L_ap8:\n" \
"s_setpc_b64 s[88:89]\n" \
"L_out_%=:\n" \
"v_bfe_u32 v38, v20, 2, 4\n" \
"v_readlane_b32 s64, v11, 2\n" \
"v_add_u32_e32 v32, 0x49000, v20\n" \
"v_add_u32_e32 v34, 0x20000, v10\n" \
X_"a[0:1], v34 offset:0\n" \
X_"a[2:3], v34 offset:4608\n" \
X_"a[4:5], v34 offset:9216\n" \
"buffer_load_dword v26, v38, s[56:59], s64 offen sc1\n" \
X_"a[6:7], v34 offset:13824\n" \
X_"a[8:9], v34 offset:32\n" \
X_"a[10:11], v34 offset:4640\n" \
X_"a[12:13], v34 offset:9248\n" \
"buffer_store_dwordx4 v[64:67], v20, s[56:59], s61 offen offset:0 sc1\n" \
X_"a[14:15], v34 offset:13856\n" \
"v_mov_b32_e32 v39, s62\n" \
"v_readlane_b32 s70, v11, 5\n" \
"v_readlane_b32 s71, v11, 4\n" \
"buffer_store_dwordx4 v[68:71], v20, s[56:59], s61 offen offset:1024 sc1\n" \
X_"a[16:17], v34 offset:64\n" \
X_"a[18:19], v34 offset:4672\n" \
X_"a[20:21], v34 offset:9280\n" \
X_"a[22:23], v34 offset:13888\n" \
"buffer_store_dwordx4 v[72:75], v20, s[56:59], s61 offen offset:2048 sc1\n" \
X_"a[24:25], v34 offset:96\n" \
X_"a[26:27], v34 offset:4704\n" \
X_"a[28:29], v34 offset:9312\n" \
X_"a[30:31], v34 offset:13920\n" \
"buffer_store_dwordx4 v[76:79], v20, s[56:59], s61 offen offset:3072 sc1\n" \
X_"a[96:97], v34 offset:128\n" \
X_"a[98:99], v34 offset:4736\n" \
X_"a[100:101], v34 offset:9344\n" \
X_"a[102:103], v34 offset:13952\n" \
"buffer_store_dwordx4 v[80:83], v32, s[56:59], s61 offen offset:0 sc1\n" \
X_"a[104:105], v34 offset:160\n" \
X_"a[106:107], v34 offset:4768\n" \
X_"a[108:109], v34 offset:9376\n" \
X_"a[110:111], v34 offset:13984\n" \
"buffer_store_dwordx4 v[84:87], v32, s[56:59], s61 offen offset:1024 sc1\n" \
X_"a[112:113], v34 offset:192\n" \
"s_waitcnt vmcnt(16) lgkmcnt(15)\n" \
M_"v[112:115], a[0:3], a[192:195], 0\n" \
"buffer_store_dwordx4 v[96:99], v32, s[56:59], s61 offen offset:2048 sc1\n" \
M_"v[112:115], a[4:7], a[196:199], v[112:115]\n" \
X_"a[114:115], v34 offset:4800\n" \
X_"a[116:117], v34 offset:9408\n" \
"buffer_store_dwordx4 v[100:103], v32, s[56:59], s61 offen offset:3072 sc1\n" \
M_"v[116:119], a[0:3], a[208:211], 0\n" \
X_"a[118:119], v34 offset:14016\n" \
X_"a[120:121], v34 offset:224\n" \
M_"v[116:119], a[4:7], a[212:215], v[116:119]\n" \
X_"a[122:123], v34 offset:4832\n" \
"v_lshrrev_b32_e32 v44, 9, v20\n" \
M_"v[184:187], a[0:3], v[64:67], 0\n" \
"v_mul_u32_u24_e32 v44, 0xb40, v44\n" \
"v_add_u32_e32 v35, v44, v10\n" \
M_"v[184:187], a[4:7], v[68:71], v[184:187]\n" \
"v_readlane_b32 s65, v11, 33\n" \
"v_readlane_b32 s67, v11, 26\n" \
M_"v[188:191], a[0:3], v[80:83], 0\n" \
X_"a[0:1], v34 offset:9440\n" \
X_"a[2:3], v34 offset:14048\n" \
M_"v[188:191], a[4:7], v[84:87], v[188:191]\n" \
X_"a[4:5], v34 offset:256\n" \
X_"a[6:7], v34 offset:4864\n" \
M_"v[196:199], a[8:11], v[80:83], 0\n" \
"v_readlane_b32 s80, v11, 11\n" \
"v_readlane_b32 s81, v11, 12\n" \
M_"v[196:199], a[12:15], v[84:87], v[196:199]\n" \
"s_mov_b32 s82, 0x47fca0\n" \
"s_mov_b32 s83, 0x20000\n" \
M_"v[192:195], a[8:11], v[64:67], 0\n" \
"v_readlane_b32 s66, v11, 31\n" \
"s_add_u32 m0, s67, 0x0\n" \
M_"v[192:195], a[12:15], v[68:71], v[192:195]\n" \
"v_readlane_b32 s92, v11, 27\n" \
"v_readlane_b32 s73, v11, 32\n" \
M_"v[124:127], a[8:11], a[208:211], 0\n" \
B_"v0, s[80:83], s66 offen offset:0 lds\n" \
M_"v[124:127], a[12:15], a[212:215], v[124:127]\n" \
"v_readlane_b32 s94, v11, 28\n" \
"v_readlane_b32 s84, v11, 9\n" \
M_"v[120:123], a[8:11], a[192:195], 0\n" \
X_"a[8:9], v34 offset:9472\n" \
X_"a[10:11], v34 offset:14080\n" \
M_"v[120:123], a[12:15], a[196:199], v[120:123]\n" \
X_"a[12:13], v34 offset:18432\n" \
X_"a[14:15], v34 offset:23040\n" \
M_"v[128:131], a[16:19], a[192:195], 0\n" \
"v_readlane_b32 s85, v11, 10\n" \
"s_waitcnt lgkmcnt(9)\n" \
M_"v[128:131], a[20:23], a[196:199], v[128:131]\n" \
B_"v1, s[80:83], s66 offen offset:1024 lds\n" \
M_"v[132:135], a[16:19], a[208:211], 0\n" \
"s_mov_b32 s86, 0xa1ca0\n" \
"s_mov_b32 s87, 0x20000\n" \
M_"v[132:135], a[20:23], a[212:215], v[132:135]\n" \
"v_add_u32_e32 v33, 0x10000, v10\n" \
"v_readlane_b32 s68, v11, 34\n" \
M_"v[200:203], a[16:19], v[64:67], 0\n" \
"v_readlane_b32 s69, v11, 35\n" \
"v_readlane_b32 s72, v11, 37\n" \
M_"v[200:203], a[20:23], v[68:71], v[200:203]\n" \
"v_readlane_b32 s93, v11, 38\n" \
"v_readlane_b32 s63, v11, 44\n" \
M_"v[204:207], a[16:19], v[80:83], 0\n" \
X_"a[16:17], v34 offset:18464\n" \
X_"a[18:19], v34 offset:23072\n" \
M_"v[204:207], a[20:23], v[84:87], v[204:207]\n" \
X_"a[20:21], v34 offset:18496\n" \
X_"a[22:23], v34 offset:23104\n" \
M_"v[212:215], a[24:27], v[80:83], 0\n" \
B_"v2, s[80:83], s66 offen offset:2048 lds\n" \
M_"v[212:215], a[28:31], v[84:87], v[212:215]\n" \
"v_bfe_u32 v40, v20, 2, 7\n" \
"s_add_u32 s74, s63, 0x3800\n" \
M_"v[208:211], a[24:27], v[64:67], 0\n" \
"v_lshlrev_b32_e32 v41, 12, v20\n" \
"s_add_u32 s12, s63, 0x0\n" \
M_"v[208:211], a[28:31], v[68:71], v[208:211]\n" \
"v_readlane_b32 s95, v11, 41\n" \
"v_readlane_b32 s96, v11, 42\n" \
M_"v[140:143], a[24:27], a[208:211], 0\n" \
"s_add_u32 s13, s63, 0x1000\n" \
"v_readlane_b32 s97, v11, 43\n" \
M_"v[140:143], a[28:31], a[212:215], v[140:143]\n" \
B_"v3, s[80:83], s66 offen offset:3072 lds\n" \
M_"v[136:139], a[24:27], a[192:195], 0\n" \
X_"a[24:25], v34 offset:18528\n" \
X_"a[26:27], v34 offset:23136\n" \
M_"v[136:139], a[28:31], a[196:199], v[136:139]\n" \
X_"a[28:29], v34 offset:18560\n" \
X_"a[30:31], v34 offset:23168\n" \
M_"v[144:147], a[96:99], a[192:195], 0\n" \
"s_add_u32 m0, s67, 0x1000\n" \
"s_add_u32 s14, s63, 0x2000\n" \
M_"v[144:147], a[100:103], a[196:199], v[144:147]\n" \
"s_add_u32 s15, s63, 0x3000\n" \
"v_readlane_b32 s100, v11, 7\n" \
M_"v[148:151], a[96:99], a[208:211], 0\n" \
B_"v4, s[80:83], s66 offen offset:0 lds\n" \
M_"v[148:151], a[100:103], a[212:215], v[148:151]\n" \
"v_readlane_b32 s16, v11, 45\n" \
"v_bfe_u32 v44, v20, 4, 4\n" \
M_"v[216:219], a[96:99], v[64:67], 0\n" \
"s_add_u32 s17, s16, 0x1000\n" \
"s_add_u32 s18, s16, 0x2000\n" \
M_"v[216:219], a[100:103], v[68:71], v[216:219]\n" \
"s_add_u32 s19, s16, 0x3000\n" \
"v_bfe_u32 v45, v20, 8, 2\n" \
M_"v[220:223], a[96:99], v[80:83], 0\n" \
X_"a[96:97], v34 offset:18592\n" \
X_"a[98:99], v34 offset:23200\n" \
M_"v[220:223], a[100:103], v[84:87], v[220:223]\n" \
X_"a[100:101], v34 offset:18624\n" \
X_"a[102:103], v34 offset:23232\n" \
M_"v[228:231], a[104:107], v[80:83], 0\n" \
B_"v5, s[80:83], s66 offen offset:1024 lds\n" \
M_"v[228:231], a[108:111], v[84:87], v[228:231]\n" \
"v_mul_u32_u24_e32 v42, 0x480, v44\n" \
"v_lshl_add_u32 v42, v45, 3, v42\n" \
M_"v[224:227], a[104:107], v[64:67], 0\n" \
"s_waitcnt vmcnt(14)\n" \
"v_cmp_ne_u32_e32 vcc, s62, v26\n" \
M_"v[224:227], a[108:111], v[68:71], v[224:227]\n" \
"v_lshrrev_b32_e32 v44, 1, v44\n" \
"v_lshlrev_b32_e32 v45, 1, v45\n" \
M_"v[156:159], a[104:107], a[208:211], 0\n" \
"v_sub_u32_e32 v44, v44, v45\n" \
"s_cbranch_vccnz L_sa_%=\n" \
"L_ra_%=:\n" \
M_"v[156:159], a[108:111], a[212:215], v[156:159]\n" \
B_"a[64:67], v20, s[56:59], s65 offen offset:0 sc1\n" \
M_"v[152:155], a[104:107], a[192:195], 0\n" \
X_"a[104:105], v34 offset:18656\n" \
X_"a[106:107], v34 offset:23264\n" \
M_"v[152:155], a[108:111], a[196:199], v[152:155]\n" \
X_"a[108:109], v34 offset:18688\n" \
X_"a[110:111], v34 offset:23296\n" \
M_"v[160:163], a[112:115], a[192:195], 0\n" \
"v_readlane_b32 s64, v11, 36\n" \
"v_and_b32_e32 v45, 16, v20\n" \
M_"v[160:163], a[116:119], a[196:199], v[160:163]\n" \
"v_mov_b32_e32 v46, 0x3f80\n" \
"v_lshlrev_b32_e32 v45, v45, v46\n" \
M_"v[164:167], a[112:115], a[208:211], 0\n" \
B_"a[68:71], v20, s[56:59], s65 offen offset:1024 sc1\n" \
M_"v[164:167], a[116:119], a[212:215], v[164:167]\n" \
"v_mov_b32_e32 v52, 0\n" \
"v_mov_b32_e32 v53, 0\n" \
M_"v[232:235], a[112:115], v[64:67], 0\n" \
"v_mov_b32_e32 v48, 0\n" \
"v_mov_b32_e32 v49, 0\n" \
M_"v[232:235], a[116:119], v[68:71], v[232:235]\n" \
"v_add_u32_e32 v43, 0x9000, v42\n" \
"v_readlane_b32 s88, v11, 13\n" \
M_"v[236:239], a[112:115], v[80:83], 0\n" \
X_"a[112:113], v10 offset:32768\n" \
X_"a[114:115], v10 offset:37376\n" \
M_"v[236:239], a[116:119], v[84:87], v[236:239]\n" \
X_"a[116:117], v10 offset:32800\n" \
"s_waitcnt lgkmcnt(7)\n" \
M_"v[244:247], a[120:123], v[80:83], 0\n" \
X_"a[118:119], v10 offset:37408\n" \
"v_readlane_b32 s89, v11, 14\n" \
M_"v[244:247], a[0:3], v[84:87], v[244:247]\n" \
B_"a[80:83], v32, s[56:59], s65 offen offset:0 sc1\n" \
M_"v[240:243], a[120:123], v[64:67], 0\n" \
"s_mov_b32 s90, 0x240000\n" \
"s_mov_b32 s91, 0x20000\n" \
M_"v[240:243], a[0:3], v[68:71], v[240:243]\n" \
"v_mov_b32_e32 v46, s62\n" \
M_"v[172:175], a[120:123], a[208:211], 0\n" \
M_"v[172:175], a[0:3], a[212:215], v[172:175]\n" \
M_"v[168:171], a[120:123], a[192:195], 0\n" \
X_"a[120:121], v10 offset:32832\n" \
X_"a[122:123], v10 offset:37440\n" \
M_"v[168:171], a[0:3], a[196:199], v[168:171]\n" \
X_"a[0:1], v10 offset:32864\n" \
X_"a[2:3], v10 offset:37472\n" \
M_"v[176:179], a[4:7], a[192:195], 0\n" \
B_"a[84:87], v32, s[56:59], s65 offen offset:1024 sc1\n" \
M_"v[176:179], a[8:11], a[196:199], v[176:179]\n" \
M_"v[180:183], a[4:7], a[208:211], 0\n" \
M_"v[180:183], a[8:11], a[212:215], v[180:183]\n" \
M_"v[248:251], a[4:7], v[64:67], 0\n" \
M_"v[248:251], a[8:11], v[68:71], v[248:251]\n" \
B_"a[72:75], v20, s[56:59], s65 offen offset:2048 sc1\n" \
M_"v[252:255], a[4:7], v[80:83], 0\n" \
X_"a[4:5], v10 offset:32896\n" \
X_"a[6:7], v10 offset:37504\n" \
M_"v[252:255], a[8:11], v[84:87], v[252:255]\n" \
X_"a[8:9], v10 offset:32928\n" \
X_"a[10:11], v10 offset:37536\n" \
M_"v[112:115], a[12:15], a[200:203], v[112:115]\n" \
M_"v[116:119], a[12:15], a[216:219], v[116:119]\n" \
M_"v[184:187], a[12:15], v[72:75], v[184:187]\n" \
B_"a[76:79], v20, s[56:59], s65 offen offset:3072 sc1\n" \
M_"v[188:191], a[12:15], v[96:99], v[188:191]\n" \
X_"a[12:13], v10 offset:32960\n" \
X_"a[14:15], v10 offset:37568\n" \
M_"v[196:199], a[16:19], v[96:99], v[196:199]\n" \
M_"v[192:195], a[16:19], v[72:75], v[192:195]\n" \
M_"v[124:127], a[16:19], a[216:219], v[124:127]\n" \
M_"v[120:123], a[16:19], a[200:203], v[120:123]\n" \
X_"a[16:17], v10 offset:32992\n" \
X_"a[18:19], v10 offset:37600\n" \
M_"v[128:131], a[20:23], a[200:203], v[128:131]\n" \
B_"a[88:91], v32, s[56:59], s65 offen offset:2048 sc1\n" \
M_"v[132:135], a[20:23], a[216:219], v[132:135]\n" \
M_"v[200:203], a[20:23], v[72:75], v[200:203]\n" \
M_"v[204:207], a[20:23], v[96:99], v[204:207]\n" \
X_"a[20:21], v10 offset:33024\n" \
X_"a[22:23], v10 offset:37632\n" \
"s_waitcnt lgkmcnt(10)\n" \
"s_barrier\n" \
M_"v[212:215], a[24:27], v[96:99], v[212:215]\n" \
B_"a[92:95], v32, s[56:59], s65 offen offset:3072 sc1\n" \
M_"v[208:211], a[24:27], v[72:75], v[208:211]\n" \
"v_readlane_b32 s65, v11, 49\n" \
M_"v[140:143], a[24:27], a[216:219], v[140:143]\n" \
M_"v[136:139], a[24:27], a[200:203], v[136:139]\n" \
X_"a[24:25], v35 offset:41984\n" \
X_"a[26:27], v35 offset:44288\n" \
M_"v[144:147], a[28:31], a[200:203], v[144:147]\n" \
M_"v[148:151], a[28:31], a[216:219], v[148:151]\n" \
B_"v6, s[80:83], s66 offen offset:2048 lds\n" \
M_"v[216:219], a[28:31], v[72:75], v[216:219]\n" \
M_"v[220:223], a[28:31], v[96:99], v[220:223]\n" \
X_"a[28:29], v35 offset:52352\n" \
X_"a[30:31], v35 offset:54656\n" \
M_"v[228:231], a[96:99], v[96:99], v[228:231]\n" \
M_"v[224:227], a[96:99], v[72:75], v[224:227]\n" \
M_"v[156:159], a[96:99], a[216:219], v[156:159]\n" \
B_"v7, s[80:83], s66 offen offset:3072 lds\n" \
M_"v[152:155], a[96:99], a[200:203], v[152:155]\n" \
X_"a[96:97], v35 offset:42016\n" \
X_"a[98:99], v35 offset:44320\n" \
M_"v[160:163], a[100:103], a[200:203], v[160:163]\n" \
"s_add_u32 m0, s67, 0x2000\n" \
"v_readlane_b32 s67, v11, 51\n" \
M_"v[164:167], a[100:103], a[216:219], v[164:167]\n" \
M_"v[232:235], a[100:103], v[72:75], v[232:235]\n" \
M_"v[236:239], a[100:103], v[96:99], v[236:239]\n" \
X_"a[100:101], v35 offset:52384\n" \
X_"a[102:103], v35 offset:54688\n" \
M_"v[244:247], a[104:107], v[96:99], v[244:247]\n" \
B_"v8, s[80:83], s66 offen offset:0 lds\n" \
M_"v[240:243], a[104:107], v[72:75], v[240:243]\n" \
"s_add_u32 m0, s92, 0x0\n" \
"v_readlane_b32 s66, v11, 50\n" \
M_"v[172:175], a[104:107], a[216:219], v[172:175]\n" \
M_"v[168:171], a[104:107], a[200:203], v[168:171]\n" \
X_"a[104:105], v35 offset:42048\n" \
X_"a[106:107], v35 offset:44352\n" \
M_"v[176:179], a[108:111], a[200:203], v[176:179]\n" \
M_"v[180:183], a[108:111], a[216:219], v[180:183]\n" \
B_"v0, s[80:83], s73 offen offset:0 lds\n" \
M_"v[248:251], a[108:111], v[72:75], v[248:251]\n" \
M_"v[252:255], a[108:111], v[96:99], v[252:255]\n" \
X_"a[108:109], v35 offset:52416\n" \
X_"a[110:111], v35 offset:54720\n" \
M_"v[112:115], a[112:115], a[204:207], v[112:115]\n" \
M_"v[116:119], a[112:115], a[220:223], v[116:119]\n" \
M_"v[184:187], a[112:115], v[76:79], v[184:187]\n" \
B_"v1, s[80:83], s73 offen offset:1024 lds\n" \
M_"v[188:191], a[112:115], v[100:103], v[188:191]\n" \
X_"a[112:113], v35 offset:42080\n" \
X_"a[114:115], v35 offset:44384\n" \
M_"v[196:199], a[116:119], v[100:103], v[196:199]\n" \
M_"v[192:195], a[116:119], v[76:79], v[192:195]\n" \
"s_waitcnt vmcnt(18)\n" \
"s_mov_b64 exec, 1\n" \
"buffer_store_dword v39, off, s[56:59], s70 offset:0 sc1\n" \
"s_mov_b64 exec, -1\n" \
M_"v[124:127], a[116:119], a[220:223], v[124:127]\n" \
"v_readlane_b32 s70, v11, 54\n" \
M_"v[120:123], a[116:119], a[204:207], v[120:123]\n" \
X_"a[116:117], v35 offset:52448\n" \
X_"a[118:119], v35 offset:54752\n" \
M_"v[128:131], a[120:123], a[204:207], v[128:131]\n" \
M_"v[132:135], a[120:123], a[220:223], v[132:135]\n" \
B_"v2, s[80:83], s73 offen offset:2048 lds\n" \
M_"v[200:203], a[120:123], v[76:79], v[200:203]\n" \
M_"v[204:207], a[120:123], v[100:103], v[204:207]\n" \
X_"a[120:121], v35 offset:42112\n" \
X_"a[122:123], v35 offset:44416\n" \
M_"v[212:215], a[0:3], v[100:103], v[212:215]\n" \
M_"v[208:211], a[0:3], v[76:79], v[208:211]\n" \
M_"v[140:143], a[0:3], a[220:223], v[140:143]\n" \
B_"v3, s[80:83], s73 offen offset:3072 lds\n" \
M_"v[136:139], a[0:3], a[204:207], v[136:139]\n" \
X_"a[0:1], v35 offset:52480\n" \
"s_waitcnt lgkmcnt(7)\n" \
M_"v[144:147], a[4:7], a[204:207], v[144:147]\n" \
X_"a[2:3], v35 offset:54784\n" \
"s_add_u32 m0, s92, 0x1000\n" \
M_"v[148:151], a[4:7], a[220:223], v[148:151]\n" \
M_"v[216:219], a[4:7], v[76:79], v[216:219]\n" \
M_"v[220:223], a[4:7], v[100:103], v[220:223]\n" \
X_"a[4:5], v35 offset:42144\n" \
X_"a[6:7], v35 offset:44448\n" \
M_"v[228:231], a[8:11], v[100:103], v[228:231]\n" \
B_"v4, s[80:83], s73 offen offset:0 lds\n" \
M_"v[224:227], a[8:11], v[76:79], v[224:227]\n" \
M_"v[156:159], a[8:11], a[220:223], v[156:159]\n" \
M_"v[152:155], a[8:11], a[204:207], v[152:155]\n" \
X_"a[8:9], v35 offset:52512\n" \
X_"a[10:11], v35 offset:54816\n" \
M_"v[160:163], a[12:15], a[204:207], v[160:163]\n" \
M_"v[164:167], a[12:15], a[220:223], v[164:167]\n" \
B_"v5, s[80:83], s73 offen offset:1024 lds\n" \
M_"v[232:235], a[12:15], v[76:79], v[232:235]\n" \
M_"v[236:239], a[12:15], v[100:103], v[236:239]\n" \
X_"a[12:13], v35 offset:42176\n" \
X_"a[14:15], v35 offset:44480\n" \
M_"v[244:247], a[16:19], v[100:103], v[244:247]\n" \
M_"v[240:243], a[16:19], v[76:79], v[240:243]\n" \
M_"v[172:175], a[16:19], a[220:223], v[172:175]\n" \
B_"v6, s[80:83], s73 offen offset:2048 lds\n" \
M_"v[168:171], a[16:19], a[204:207], v[168:171]\n" \
X_"a[16:17], v35 offset:52544\n" \
X_"a[18:19], v35 offset:54848\n" \
M_"v[176:179], a[20:23], a[204:207], v[176:179]\n" \
M_"v[180:183], a[20:23], a[220:223], v[180:183]\n" \
M_"v[248:251], a[20:23], v[76:79], v[248:251]\n" \
M_"v[252:255], a[20:23], v[100:103], v[252:255]\n" \
X_"a[20:21], v35 offset:42208\n" \
X_"a[22:23], v35 offset:44512\n" \
M_"v[112:115], a[24:27], a[124:127], v[112:115]\n" \
B_"v7, s[80:83], s73 offen offset:3072 lds\n" \
M_"v[112:115], a[28:31], a[224:227], v[112:115]\n" \
"s_add_u32 m0, s92, 0x2000\n" \
"v_readlane_b32 s92, v11, 49\n" \
M_"v[116:119], a[24:27], a[228:231], v[116:119]\n" \
M_"v[116:119], a[28:31], a[232:235], v[116:119]\n" \
M_"v[184:187], a[24:27], a[236:239], v[184:187]\n" \
M_"v[184:187], a[28:31], a[240:243], v[184:187]\n" \
B_"v8, s[80:83], s73 offen offset:0 lds\n" \
M_"v[188:191], a[24:27], a[244:247], v[188:191]\n" \
X_"a[24:25], v35 offset:52576\n" \
X_"a[26:27], v35 offset:54880\n" \
M_"v[188:191], a[28:31], a[248:251], v[188:191]\n" \
"s_mov_b32 m0, s94\n" \
X_"a[28:29], v35 offset:42240\n" \
M_"v[196:199], a[96:99], a[244:247], v[196:199]\n" \
X_"a[30:31], v35 offset:44544\n" \
"v_readlane_b32 s73, v11, 6\n" \
M_"v[196:199], a[100:103], a[248:251], v[196:199]\n" \
M_"v[192:195], a[96:99], a[236:239], v[192:195]\n" \
B_"v9, s[84:87], 0 offen lds\n" \
M_"v[192:195], a[100:103], a[240:243], v[192:195]\n" \
M_"v[124:127], a[96:99], a[228:231], v[124:127]\n" \
M_"v[124:127], a[100:103], a[232:235], v[124:127]\n" \
M_"v[120:123], a[96:99], a[124:127], v[120:123]\n" \
X_"a[96:97], v35 offset:52608\n" \
X_"a[98:99], v35 offset:54912\n" \
M_"v[120:123], a[100:103], a[224:227], v[120:123]\n" \
X_"a[100:101], v33 offset:0\n" \
X_"a[102:103], v33 offset:4608\n" \
M_"v[128:131], a[104:107], a[124:127], v[128:131]\n" \
B_"a[128:131], v20, s[56:59], s68 offen offset:0 sc1\n" \
M_"v[128:131], a[108:111], a[224:227], v[128:131]\n" \
M_"v[132:135], a[104:107], a[228:231], v[132:135]\n" \
M_"v[132:135], a[108:111], a[232:235], v[132:135]\n" \
M_"v[200:203], a[104:107], a[236:239], v[200:203]\n" \
M_"v[200:203], a[108:111], a[240:243], v[200:203]\n" \
B_"a[132:135], v20, s[56:59], s68 offen offset:1024 sc1\n" \
M_"v[204:207], a[104:107], a[244:247], v[204:207]\n" \
X_"a[104:105], v33 offset:9216\n" \
X_"a[106:107], v33 offset:13824\n" \
M_"v[204:207], a[108:111], a[248:251], v[204:207]\n" \
X_"a[108:109], v33 offset:32\n" \
"s_waitcnt lgkmcnt(7)\n" \
M_"v[212:215], a[112:115], a[244:247], v[212:215]\n" \
X_"a[110:111], v33 offset:4640\n" \
M_"v[212:215], a[116:119], a[248:251], v[212:215]\n" \
M_"v[208:211], a[112:115], a[236:239], v[208:211]\n" \
"buffer_load_dword v47, v38, s[56:59], s71 offen sc1\n" \
M_"v[208:211], a[116:119], a[240:243], v[208:211]\n" \
M_"v[140:143], a[112:115], a[228:231], v[140:143]\n" \
M_"v[140:143], a[116:119], a[232:235], v[140:143]\n" \
M_"v[136:139], a[112:115], a[124:127], v[136:139]\n" \
X_"a[112:113], v33 offset:9248\n" \
X_"a[114:115], v33 offset:13856\n" \
M_"v[136:139], a[116:119], a[224:227], v[136:139]\n" \
X_"a[116:117], v33 offset:64\n" \
X_"a[118:119], v33 offset:4672\n" \
M_"v[144:147], a[120:123], a[124:127], v[144:147]\n" \
B_"a[144:147], v32, s[56:59], s68 offen offset:0 sc1\n" \
M_"v[144:147], a[0:3], a[224:227], v[144:147]\n" \
M_"v[148:151], a[120:123], a[228:231], v[148:151]\n" \
M_"v[148:151], a[0:3], a[232:235], v[148:151]\n" \
M_"v[216:219], a[120:123], a[236:239], v[216:219]\n" \
M_"v[216:219], a[0:3], a[240:243], v[216:219]\n" \
B_"a[148:151], v32, s[56:59], s68 offen offset:1024 sc1\n" \
M_"v[220:223], a[120:123], a[244:247], v[220:223]\n" \
X_"a[120:121], v33 offset:9280\n" \
X_"a[122:123], v33 offset:13888\n" \
M_"v[220:223], a[0:3], a[248:251], v[220:223]\n" \
X_"a[0:1], v33 offset:96\n" \
X_"a[2:3], v33 offset:4704\n" \
M_"v[228:231], a[4:7], a[244:247], v[228:231]\n" \
M_"v[228:231], a[8:11], a[248:251], v[228:231]\n" \
M_"v[224:227], a[4:7], a[236:239], v[224:227]\n" \
B_"a[152:155], v32, s[56:59], s68 offen offset:2048 sc1\n" \
M_"v[224:227], a[8:11], a[240:243], v[224:227]\n" \
M_"v[156:159], a[4:7], a[228:231], v[156:159]\n" \
M_"v[156:159], a[8:11], a[232:235], v[156:159]\n" \
M_"v[152:155], a[4:7], a[124:127], v[152:155]\n" \
X_"a[4:5], v33 offset:9312\n" \
X_"a[6:7], v33 offset:13920\n" \
M_"v[152:155], a[8:11], a[224:227], v[152:155]\n" \
X_"a[8:9], v33 offset:128\n" \
X_"a[10:11], v33 offset:4736\n" \
M_"v[160:163], a[12:15], a[124:127], v[160:163]\n" \
B_"a[156:159], v32, s[56:59], s68 offen offset:3072 sc1\n" \
M_"v[160:163], a[16:19], a[224:227], v[160:163]\n" \
M_"v[164:167], a[12:15], a[228:231], v[164:167]\n" \
M_"v[164:167], a[16:19], a[232:235], v[164:167]\n" \
M_"v[232:235], a[12:15], a[236:239], v[232:235]\n" \
M_"v[232:235], a[16:19], a[240:243], v[232:235]\n" \
B_"a[136:139], v20, s[56:59], s68 offen offset:2048 sc1\n" \
M_"v[236:239], a[12:15], a[244:247], v[236:239]\n" \
X_"a[12:13], v33 offset:9344\n" \
X_"a[14:15], v33 offset:13952\n" \
M_"v[236:239], a[16:19], a[248:251], v[236:239]\n" \
X_"a[16:17], v33 offset:160\n" \
X_"a[18:19], v33 offset:4768\n" \
M_"v[244:247], a[20:23], a[244:247], v[244:247]\n" \
M_"v[244:247], a[24:27], a[248:251], v[244:247]\n" \
M_"v[240:243], a[20:23], a[236:239], v[240:243]\n" \
B_"a[140:143], v20, s[56:59], s68 offen offset:3072 sc1\n" \
M_"v[240:243], a[24:27], a[240:243], v[240:243]\n" \
"v_readlane_b32 s68, v11, 52\n" \
M_"v[172:175], a[20:23], a[228:231], v[172:175]\n" \
M_"v[172:175], a[24:27], a[232:235], v[172:175]\n" \
M_"v[168:171], a[20:23], a[124:127], v[168:171]\n" \
X_"a[20:21], v33 offset:9376\n" \
X_"a[22:23], v33 offset:13984\n" \
M_"v[168:171], a[24:27], a[224:227], v[168:171]\n" \
X_"a[24:25], v33 offset:192\n" \
X_"a[26:27], v33 offset:4800\n" \
M_"v[176:179], a[28:31], a[124:127], v[176:179]\n" \
"s_waitcnt lgkmcnt(8)\n" \
M_"v[176:179], a[96:99], a[224:227], v[176:179]\n" \
B_"a[32:35], v20, s[56:59], s69 offen offset:0 sc1\n" \
M_"v[180:183], a[28:31], a[228:231], v[180:183]\n" \
M_"v[180:183], a[96:99], a[232:235], v[180:183]\n" \
M_"v[248:251], a[28:31], a[236:239], v[248:251]\n" \
M_"v[248:251], a[96:99], a[240:243], v[248:251]\n" \
M_"v[252:255], a[28:31], a[244:247], v[252:255]\n" \
X_"a[28:29], v33 offset:9408\n" \
X_"a[30:31], v33 offset:14016\n" \
M_"v[252:255], a[96:99], a[248:251], v[252:255]\n" \
X_"a[96:97], v33 offset:224\n" \
"s_waitcnt vmcnt(18)\n" \
M_"v[112:115], a[100:103], a[64:67], v[112:115]\n" \
B_"a[36:39], v20, s[56:59], s69 offen offset:1024 sc1\n" \
M_"v[112:115], a[104:107], a[68:71], v[112:115]\n" \
X_"a[98:99], v33 offset:4832\n" \
M_"v[116:119], a[100:103], a[80:83], v[116:119]\n" \
X_"a[100:101], v33 offset:9440\n" \
X_"a[102:103], v33 offset:14048\n" \
M_"v[116:119], a[104:107], a[84:87], v[116:119]\n" \
X_"a[104:105], v33 offset:256\n" \
X_"a[106:107], v33 offset:4864\n" \
M_"v[124:127], a[108:111], a[80:83], v[124:127]\n" \
M_"v[124:127], a[112:115], a[84:87], v[124:127]\n" \
B_"a[48:51], v32, s[56:59], s69 offen offset:0 sc1\n" \
M_"v[120:123], a[108:111], a[64:67], v[120:123]\n" \
X_"a[108:109], v33 offset:9472\n" \
X_"a[110:111], v33 offset:14080\n" \
M_"v[120:123], a[112:115], a[68:71], v[120:123]\n" \
X_"a[112:113], v33 offset:18432\n" \
X_"a[114:115], v33 offset:23040\n" \
M_"v[128:131], a[116:119], a[64:67], v[128:131]\n" \
M_"v[128:131], a[120:123], a[68:71], v[128:131]\n" \
M_"v[132:135], a[116:119], a[80:83], v[132:135]\n" \
B_"a[52:55], v32, s[56:59], s69 offen offset:1024 sc1\n" \
M_"v[132:135], a[120:123], a[84:87], v[132:135]\n" \
X_"a[116:117], v33 offset:27648\n" \
X_"a[118:119], v33 offset:32256\n" \
M_"v[140:143], a[0:3], a[80:83], v[140:143]\n" \
X_"a[120:121], v33 offset:18464\n" \
X_"a[122:123], v33 offset:23072\n" \
M_"v[140:143], a[4:7], a[84:87], v[140:143]\n" \
"s_waitcnt vmcnt(10)\n" \
"v_cmp_ne_u32_e32 vcc, s62, v47\n" \
M_"v[136:139], a[0:3], a[64:67], v[136:139]\n" \
X_"a[0:1], v33 offset:27680\n" \
".fill (L_ab + 36864 - .) / 4, 4, 0xBF800000\n" \
"L_ap9:\n" \
"s_cbranch_execz L_ap0\n" \
X_"a[2:3], v33 offset:32288\n" \
M_"v[136:139], a[4:7], a[68:71], v[136:139]\n" \
B_"a[56:59], v32, s[56:59], s69 offen offset:2048 sc1\n" \
M_"v[144:147], a[8:11], a[64:67], v[144:147]\n" \
X_"a[4:5], v33 offset:18496\n" \
"s_waitcnt lgkmcnt(13)\n" \
M_"v[144:147], a[12:15], a[68:71], v[144:147]\n" \
X_"a[6:7], v33 offset:23104\n" \
"s_cbranch_vccnz L_sb_%=\n" \
"L_rb_%=:\n" \
M_"v[148:151], a[8:11], a[80:83], v[148:151]\n" \
X_"a[8:9], v33 offset:27712\n" \
X_"a[10:11], v33 offset:32320\n" \
M_"v[148:151], a[12:15], a[84:87], v[148:151]\n" \
X_"a[12:13], v33 offset:18528\n" \
X_"a[14:15], v33 offset:23136\n" \
B_"a[60:63], v32, s[56:59], s69 offen offset:3072 sc1\n" \
M_"v[156:159], a[16:19], a[80:83], v[156:159]\n" \
"v_readlane_b32 s71, v11, 55\n" \
"v_cmp_eq_u32_e32 vcc, 0, v44\n" \
M_"v[156:159], a[20:23], a[84:87], v[156:159]\n" \
M_"v[152:155], a[16:19], a[64:67], v[152:155]\n" \
X_"a[16:17], v33 offset:27744\n" \
X_"a[18:19], v33 offset:32352\n" \
M_"v[152:155], a[20:23], a[68:71], v[152:155]\n" \
X_"a[20:21], v33 offset:18560\n" \
X_"a[22:23], v33 offset:23168\n" \
B_"a[40:43], v20, s[56:59], s69 offen offset:2048 sc1\n" \
M_"v[160:163], a[24:27], a[64:67], v[160:163]\n" \
"v_cndmask_b32_e32 v50, 0, v45, vcc\n" \
"v_cmp_eq_u32_e32 vcc, 1, v44\n" \
M_"v[160:163], a[28:31], a[68:71], v[160:163]\n" \
"v_and_b32_e32 v44, 0x3c0, v20\n" \
"v_cndmask_b32_e32 v51, 0, v45, vcc\n" \
M_"v[164:167], a[24:27], a[80:83], v[164:167]\n" \
X_"a[24:25], v33 offset:27776\n" \
X_"a[26:27], v33 offset:32384\n" \
M_"v[164:167], a[28:31], a[84:87], v[164:167]\n" \
X_"a[28:29], v33 offset:18592\n" \
X_"a[30:31], v33 offset:23200\n" \
B_"a[44:47], v20, s[56:59], s69 offen offset:3072 sc1\n" \
M_"v[172:175], a[96:99], a[80:83], v[172:175]\n" \
"v_readlane_b32 s69, v11, 53\n" \
"v_bfe_u32 v45, v20, 1, 5\n" \
M_"v[172:175], a[100:103], a[84:87], v[172:175]\n" \
"v_and_b32_e32 v45, 24, v45\n" \
"v_add_u32_e32 v45, 0x4800, v45\n" \
M_"v[168:171], a[96:99], a[64:67], v[168:171]\n" \
X_"a[96:97], v33 offset:27808\n" \
X_"a[98:99], v33 offset:32416\n" \
M_"v[168:171], a[100:103], a[68:71], v[168:171]\n" \
X_"a[100:101], v33 offset:18624\n" \
"s_waitcnt lgkmcnt(13)\n" \
M_"v[176:179], a[104:107], a[64:67], v[176:179]\n" \
X_"a[102:103], v33 offset:23232\n" \
"v_add_u32_e32 v36, v44, v45\n" \
M_"v[176:179], a[108:111], a[68:71], v[176:179]\n" \
B_"a[124:127], v20, s[56:59], s64 offen offset:0 sc1\n" \
M_"v[180:183], a[104:107], a[80:83], v[180:183]\n" \
X_"a[104:105], v33 offset:27840\n" \
X_"a[106:107], v33 offset:32448\n" \
M_"v[180:183], a[108:111], a[84:87], v[180:183]\n" \
X_"a[108:109], v33 offset:18656\n" \
X_"a[110:111], v33 offset:23264\n" \
M_"v[112:115], a[112:115], a[72:75], v[112:115]\n" \
M_"v[112:115], a[116:119], a[76:79], v[112:115]\n" \
M_"v[116:119], a[112:115], a[88:91], v[116:119]\n" \
X_"a[112:113], v33 offset:27872\n" \
X_"a[114:115], v33 offset:32480\n" \
M_"v[116:119], a[116:119], a[92:95], v[116:119]\n" \
X_"a[116:117], v33 offset:18688\n" \
X_"a[118:119], v33 offset:23296\n" \
M_"v[124:127], a[120:123], a[88:91], v[124:127]\n" \
B_"a[224:227], v20, s[56:59], s64 offen offset:1024 sc1\n" \
M_"v[124:127], a[0:3], a[92:95], v[124:127]\n" \
M_"v[120:123], a[120:123], a[72:75], v[120:123]\n" \
X_"a[120:121], v33 offset:27904\n" \
X_"a[122:123], v33 offset:32512\n" \
M_"v[120:123], a[0:3], a[76:79], v[120:123]\n" \
"s_barrier\n" \
M_"v[128:131], a[4:7], a[72:75], v[128:131]\n" \
X_"a[0:1], v33 offset:36864\n" \
X_"a[2:3], v33 offset:41472\n" \
M_"v[128:131], a[8:11], a[76:79], v[128:131]\n" \
B_"a[236:239], v32, s[56:59], s64 offen offset:0 sc1\n" \
M_"v[132:135], a[4:7], a[88:91], v[132:135]\n" \
X_"a[4:5], v33 offset:46080\n" \
X_"a[6:7], v33 offset:50688\n" \
M_"v[132:135], a[8:11], a[92:95], v[132:135]\n" \
X_"a[8:9], v33 offset:55296\n" \
"s_waitcnt lgkmcnt(15)\n" \
M_"v[140:143], a[12:15], a[88:91], v[140:143]\n" \
X_"a[10:11], v33 offset:59904\n" \
M_"v[140:143], a[16:19], a[92:95], v[140:143]\n" \
M_"v[136:139], a[12:15], a[72:75], v[136:139]\n" \
X_"a[12:13], v10 offset:0\n" \
X_"a[14:15], v10 offset:4608\n" \
M_"v[136:139], a[16:19], a[76:79], v[136:139]\n" \
X_"a[16:17], v34 offset:0\n" \
X_"a[18:19], v34 offset:4608\n" \
M_"v[144:147], a[20:23], a[72:75], v[144:147]\n" \
B_"a[240:243], v32, s[56:59], s64 offen offset:1024 sc1\n" \
M_"v[144:147], a[24:27], a[76:79], v[144:147]\n" \
M_"v[148:151], a[20:23], a[88:91], v[148:151]\n" \
X_"a[20:21], v34 offset:9216\n" \
X_"a[22:23], v34 offset:13824\n" \
M_"v[148:151], a[24:27], a[92:95], v[148:151]\n" \
X_"a[24:25], v34 offset:18432\n" \
X_"a[26:27], v34 offset:23040\n" \
M_"v[156:159], a[28:31], a[88:91], v[156:159]\n" \
M_"v[156:159], a[96:99], a[92:95], v[156:159]\n" \
B_"a[244:247], v32, s[56:59], s64 offen offset:2048 sc1\n" \
M_"v[152:155], a[28:31], a[72:75], v[152:155]\n" \
X_"a[28:29], v10 offset:9216\n" \
X_"a[30:31], v10 offset:13824\n" \
M_"v[152:155], a[96:99], a[76:79], v[152:155]\n" \
X_"a[96:97], v33 offset:36896\n" \
X_"a[98:99], v33 offset:41504\n" \
M_"v[160:163], a[100:103], a[72:75], v[160:163]\n" \
"s_waitcnt lgkmcnt(15)\n" \
M_"v[160:163], a[104:107], a[76:79], v[160:163]\n" \
M_"v[164:167], a[100:103], a[88:91], v[164:167]\n" \
X_"a[100:101], v33 offset:46112\n" \
X_"a[102:103], v33 offset:50720\n" \
M_"v[164:167], a[104:107], a[92:95], v[164:167]\n" \
X_"a[104:105], v33 offset:55328\n" \
X_"a[106:107], v33 offset:59936\n" \
M_"v[172:175], a[108:111], a[88:91], v[172:175]\n" \
B_"a[248:251], v32, s[56:59], s64 offen offset:3072 sc1\n" \
M_"v[172:175], a[112:115], a[92:95], v[172:175]\n" \
M_"v[168:171], a[108:111], a[72:75], v[168:171]\n" \
X_"a[108:109], v10 offset:32\n" \
X_"a[110:111], v10 offset:4640\n" \
M_"v[168:171], a[112:115], a[76:79], v[168:171]\n" \
X_"a[112:113], v34 offset:32\n" \
X_"a[114:115], v34 offset:4640\n" \
M_"v[176:179], a[116:119], a[72:75], v[176:179]\n" \
M_"v[176:179], a[120:123], a[76:79], v[176:179]\n" \
B_"a[228:231], v20, s[56:59], s64 offen offset:2048 sc1\n" \
M_"v[180:183], a[116:119], a[88:91], v[180:183]\n" \
X_"a[116:117], v34 offset:9248\n" \
X_"a[118:119], v34 offset:13856\n" \
M_"v[180:183], a[120:123], a[92:95], v[180:183]\n" \
X_"a[120:121], v34 offset:18464\n" \
X_"a[122:123], v34 offset:23072\n" \
M_"v[112:115], a[0:3], a[128:131], v[112:115]\n" \
"s_waitcnt lgkmcnt(15)\n" \
M_"v[112:115], a[4:7], a[132:135], v[112:115]\n" \
"s_waitcnt vmcnt(18)\n" \
M_"v[116:119], a[0:3], a[144:147], v[116:119]\n" \
X_"a[0:1], v10 offset:9248\n" \
X_"a[2:3], v10 offset:13856\n" \
M_"v[116:119], a[4:7], a[148:151], v[116:119]\n" \
X_"a[4:5], v33 offset:36928\n" \
X_"a[6:7], v33 offset:41536\n" \
M_"v[116:119], a[8:11], a[152:155], v[116:119]\n" \
"s_waitcnt vmcnt(17)\n" \
M_"v[116:119], a[12:15], a[156:159], v[116:119]\n" \
"s_waitcnt vmcnt(16)\n" \
M_"v[112:115], a[8:11], a[136:139], v[112:115]\n" \
X_"a[8:9], v33 offset:46144\n" \
"s_waitcnt vmcnt(15)\n" \
M_"v[112:115], a[12:15], a[140:143], v[112:115]\n" \
X_"a[10:11], v33 offset:50752\n" \
"s_waitcnt vmcnt(14)\n" \
M_"v[112:115], a[16:19], a[32:35], v[112:115]\n" \
X_"a[12:13], v33 offset:55360\n" \
"s_waitcnt vmcnt(13)\n" \
M_"v[112:115], a[20:23], a[36:39], v[112:115]\n" \
X_"a[14:15], v33 offset:59968\n" \
"s_waitcnt vmcnt(12)\n" \
M_"v[116:119], a[16:19], a[48:51], v[116:119]\n" \
X_"a[16:17], v10 offset:64\n" \
"s_waitcnt vmcnt(11)\n" \
M_"v[116:119], a[20:23], a[52:55], v[116:119]\n" \
X_"a[18:19], v10 offset:4672\n" \
"s_waitcnt vmcnt(10)\n" \
M_"v[116:119], a[24:27], a[56:59], v[116:119]\n" \
X_"a[20:21], v34 offset:64\n" \
X_"a[22:23], v34 offset:4672\n" \
B_"a[232:235], v20, s[56:59], s64 offen offset:3072 sc1\n" \
"s_waitcnt vmcnt(10) lgkmcnt(12)\n" \
M_"v[116:119], a[28:31], a[60:63], v[116:119]\n" \
B_"a[160:163], v20, s[56:59], s72 offen offset:0 sc1\n" \
"s_waitcnt vmcnt(10)\n" \
M_"v[112:115], a[24:27], a[40:43], v[112:115]\n" \
X_"a[24:25], v34 offset:9280\n" \
X_"a[26:27], v34 offset:13888\n" \
B_"a[164:167], v20, s[56:59], s72 offen offset:1024 sc1\n" \
"s_waitcnt vmcnt(10)\n" \
M_"v[112:115], a[28:31], a[44:47], v[112:115]\n" \
X_"a[28:29], v34 offset:18496\n" \
X_"a[30:31], v34 offset:23104\n" \
M_"v[120:123], a[96:99], a[128:131], v[120:123]\n" \
B_"a[176:179], v32, s[56:59], s72 offen offset:0 sc1\n" \
M_"v[120:123], a[100:103], a[132:135], v[120:123]\n" \
M_"v[124:127], a[96:99], a[144:147], v[124:127]\n" \
X_"a[96:97], v10 offset:9280\n" \
X_"a[98:99], v10 offset:13888\n" \
C_"v112, v112, v113\n" \
C_"v113, v114, v115\n" \
C_"v114, v116, v117\n" \
C_"v115, v118, v119\n" \
"buffer_store_dwordx4 v[112:115], v20, s[56:59], s65 offen offset:0 sc1\n" \
"s_add_u32 s65, s65, 0x3800\n" \
M_"v[124:127], a[100:103], a[148:151], v[124:127]\n" \
X_"a[100:101], v33 offset:36960\n" \
X_"a[102:103], v33 offset:41568\n" \
M_"v[124:127], a[104:107], a[152:155], v[124:127]\n" \
M_"v[124:127], a[108:111], a[156:159], v[124:127]\n" \
M_"v[120:123], a[104:107], a[136:139], v[120:123]\n" \
X_"a[104:105], v33 offset:46176\n" \
X_"a[106:107], v33 offset:50784\n" \
M_"v[120:123], a[108:111], a[140:143], v[120:123]\n" \
X_"a[108:109], v33 offset:55392\n" \
X_"a[110:111], v33 offset:60000\n" \
M_"v[120:123], a[112:115], a[32:35], v[120:123]\n" \
B_"a[180:183], v32, s[56:59], s72 offen offset:1024 sc1\n" \
M_"v[120:123], a[116:119], a[36:39], v[120:123]\n" \
M_"v[124:127], a[112:115], a[48:51], v[124:127]\n" \
X_"a[112:113], v10 offset:96\n" \
X_"a[114:115], v10 offset:4704\n" \
M_"v[124:127], a[116:119], a[52:55], v[124:127]\n" \
X_"a[116:117], v34 offset:96\n" \
X_"a[118:119], v34 offset:4704\n" \
M_"v[124:127], a[120:123], a[56:59], v[124:127]\n" \
"s_waitcnt lgkmcnt(12)\n" \
M_"v[124:127], a[0:3], a[60:63], v[124:127]\n" \
B_"a[168:171], v20, s[56:59], s72 offen offset:2048 sc1\n" \
M_"v[120:123], a[120:123], a[40:43], v[120:123]\n" \
X_"a[120:121], v34 offset:9312\n" \
X_"a[122:123], v34 offset:13920\n" \
M_"v[120:123], a[0:3], a[44:47], v[120:123]\n" \
X_"a[0:1], v34 offset:18528\n" \
X_"a[2:3], v34 offset:23136\n" \
M_"v[128:131], a[4:7], a[128:131], v[128:131]\n" \
M_"v[128:131], a[8:11], a[132:135], v[128:131]\n" \
M_"v[132:135], a[4:7], a[144:147], v[132:135]\n" \
X_"a[4:5], v10 offset:9312\n" \
X_"a[6:7], v10 offset:13920\n" \
M_"v[132:135], a[8:11], a[148:151], v[132:135]\n" \
C_"v120, v120, v121\n" \
C_"v121, v122, v123\n" \
C_"v122, v124, v125\n" \
C_"v123, v126, v127\n" \
"buffer_store_dwordx4 v[120:123], v20, s[56:59], s66 offen offset:0 sc1\n" \
"s_add_u32 s66, s66, 0x3000\n" \
M_"v[132:135], a[12:15], a[152:155], v[132:135]\n" \
X_"a[8:9], v33 offset:36992\n" \
X_"a[10:11], v33 offset:41600\n" \
M_"v[132:135], a[16:19], a[156:159], v[132:135]\n" \
M_"v[128:131], a[12:15], a[136:139], v[128:131]\n" \
X_"a[12:13], v33 offset:46208\n" \
X_"a[14:15], v33 offset:50816\n" \
M_"v[128:131], a[16:19], a[140:143], v[128:131]\n" \
X_"a[16:17], v33 offset:55424\n" \
X_"a[18:19], v33 offset:60032\n" \
M_"v[128:131], a[20:23], a[32:35], v[128:131]\n" \
B_"a[184:187], v32, s[56:59], s72 offen offset:2048 sc1\n" \
M_"v[128:131], a[24:27], a[36:39], v[128:131]\n" \
M_"v[132:135], a[20:23], a[48:51], v[132:135]\n" \
X_"a[20:21], v10 offset:128\n" \
X_"a[22:23], v10 offset:4736\n" \
M_"v[132:135], a[24:27], a[52:55], v[132:135]\n" \
X_"a[24:25], v34 offset:128\n" \
X_"a[26:27], v34 offset:4736\n" \
M_"v[132:135], a[28:31], a[56:59], v[132:135]\n" \
"s_waitcnt lgkmcnt(12)\n" \
M_"v[132:135], a[96:99], a[60:63], v[132:135]\n" \
B_"a[172:175], v20, s[56:59], s72 offen offset:3072 sc1\n" \
M_"v[128:131], a[28:31], a[40:43], v[128:131]\n" \
X_"a[28:29], v34 offset:9344\n" \
X_"a[30:31], v34 offset:13952\n" \
M_"v[128:131], a[96:99], a[44:47], v[128:131]\n" \
X_"a[96:97], v34 offset:18560\n" \
X_"a[98:99], v34 offset:23168\n" \
M_"v[136:139], a[100:103], a[128:131], v[136:139]\n" \
M_"v[136:139], a[104:107], a[132:135], v[136:139]\n" \
M_"v[140:143], a[100:103], a[144:147], v[140:143]\n" \
B_"a[188:191], v32, s[56:59], s72 offen offset:3072 sc1\n" \
M_"v[140:143], a[104:107], a[148:151], v[140:143]\n" \
"v_readlane_b32 s72, v11, 56\n" \
C_"v128, v128, v129\n" \
C_"v129, v130, v131\n" \
C_"v130, v132, v133\n" \
C_"v131, v134, v135\n" \
M_"v[140:143], a[108:111], a[152:155], v[140:143]\n" \
"buffer_store_dwordx4 v[128:131], v20, s[56:59], s67 offen offset:0 sc1\n" \
"s_add_u32 s67, s67, 0x2800\n" \
M_"v[140:143], a[112:115], a[156:159], v[140:143]\n" \
X_"a[100:101], v10 offset:9344\n" \
X_"a[102:103], v10 offset:13952\n" \
M_"v[136:139], a[108:111], a[136:139], v[136:139]\n" \
X_"a[104:105], v33 offset:37024\n" \
X_"a[106:107], v33 offset:41632\n" \
M_"v[136:139], a[112:115], a[140:143], v[136:139]\n" \
X_"a[108:109], v33 offset:46240\n" \
X_"a[110:111], v33 offset:50848\n" \
M_"v[136:139], a[116:119], a[32:35], v[136:139]\n" \
X_"a[112:113], v33 offset:55456\n" \
X_"a[114:115], v33 offset:60064\n" \
M_"v[136:139], a[120:123], a[36:39], v[136:139]\n" \
B_"a[192:195], v20, s[56:59], s93 offen offset:0 sc1\n" \
M_"v[140:143], a[116:119], a[48:51], v[140:143]\n" \
X_"a[116:117], v10 offset:160\n" \
X_"a[118:119], v10 offset:4768\n" \
M_"v[140:143], a[120:123], a[52:55], v[140:143]\n" \
X_"a[120:121], v34 offset:160\n" \
X_"a[122:123], v34 offset:4768\n" \
M_"v[140:143], a[0:3], a[56:59], v[140:143]\n" \
"s_waitcnt lgkmcnt(13)\n" \
M_"v[140:143], a[4:7], a[60:63], v[140:143]\n" \
M_"v[136:139], a[0:3], a[40:43], v[136:139]\n" \
B_"a[196:199], v20, s[56:59], s93 offen offset:1024 sc1\n" \
M_"v[136:139], a[4:7], a[44:47], v[136:139]\n" \
X_"a[0:1], v34 offset:9376\n" \
X_"a[2:3], v34 offset:13984\n" \
M_"v[144:147], a[8:11], a[128:131], v[144:147]\n" \
X_"a[4:5], v34 offset:18592\n" \
X_"a[6:7], v34 offset:23200\n" \
M_"v[144:147], a[12:15], a[132:135], v[144:147]\n" \
M_"v[148:151], a[8:11], a[144:147], v[148:151]\n" \
X_"a[8:9], v10 offset:9376\n" \
C_"v136, v136, v137\n" \
C_"v137, v138, v139\n" \
C_"v138, v140, v141\n" \
C_"v139, v142, v143\n" \
"buffer_store_dwordx4 v[136:139], v20, s[56:59], s68 offen offset:0 sc1\n" \
"s_add_u32 s68, s68, 0x2000\n" \
M_"v[148:151], a[12:15], a[148:151], v[148:151]\n" \
X_"a[10:11], v10 offset:13984\n" \
X_"a[12:13], v33 offset:37056\n" \
M_"v[148:151], a[16:19], a[152:155], v[148:151]\n" \
X_"a[14:15], v33 offset:41664\n" \
M_"v[148:151], a[20:23], a[156:159], v[148:151]\n" \
M_"v[144:147], a[16:19], a[136:139], v[144:147]\n" \
B_"a[208:211], v32, s[56:59], s93 offen offset:0 sc1\n" \
M_"v[144:147], a[20:23], a[140:143], v[144:147]\n" \
X_"a[16:17], v33 offset:46272\n" \
X_"a[18:19], v33 offset:50880\n" \
M_"v[144:147], a[24:27], a[32:35], v[144:147]\n" \
X_"a[20:21], v33 offset:55488\n" \
X_"a[22:23], v33 offset:60096\n" \
M_"v[144:147], a[28:31], a[36:39], v[144:147]\n" \
M_"v[148:151], a[24:27], a[48:51], v[148:151]\n" \
X_"a[24:25], v10 offset:192\n" \
X_"a[26:27], v10 offset:4800\n" \
B_"a[212:215], v32, s[56:59], s93 offen offset:1024 sc1\n" \
M_"v[148:151], a[28:31], a[52:55], v[148:151]\n" \
X_"a[28:29], v34 offset:192\n" \
"s_waitcnt lgkmcnt(13)\n" \
M_"v[148:151], a[96:99], a[56:59], v[148:151]\n" \
X_"a[30:31], v34 offset:4800\n" \
M_"v[148:151], a[100:103], a[60:63], v[148:151]\n" \
M_"v[144:147], a[96:99], a[40:43], v[144:147]\n" \
X_"a[96:97], v34 offset:9408\n" \
X_"a[98:99], v34 offset:14016\n" \
B_"a[216:219], v32, s[56:59], s93 offen offset:2048 sc1\n" \
M_"v[144:147], a[100:103], a[44:47], v[144:147]\n" \
X_"a[100:101], v34 offset:18624\n" \
X_"a[102:103], v34 offset:23232\n" \
M_"v[152:155], a[104:107], a[128:131], v[152:155]\n" \
M_"v[152:155], a[108:111], a[132:135], v[152:155]\n" \
M_"v[156:159], a[104:107], a[144:147], v[156:159]\n" \
X_"a[104:105], v10 offset:9408\n" \
X_"a[106:107], v10 offset:14016\n" \
B_"a[220:223], v32, s[56:59], s93 offen offset:3072 sc1\n" \
C_"v144, v144, v145\n" \
C_"v145, v146, v147\n" \
C_"v146, v148, v149\n" \
C_"v147, v150, v151\n" \
M_"v[156:159], a[108:111], a[148:151], v[156:159]\n" \
X_"a[108:109], v33 offset:256\n" \
X_"a[110:111], v33 offset:4864\n" \
"buffer_store_dwordx4 v[144:147], v20, s[56:59], s69 offen offset:0 sc1\n" \
"s_add_u32 s69, s69, 0x1800\n" \
M_"v[156:159], a[112:115], a[152:155], v[156:159]\n" \
M_"v[156:159], a[116:119], a[156:159], v[156:159]\n" \
M_"v[152:155], a[112:115], a[136:139], v[152:155]\n" \
X_"a[112:113], v33 offset:9472\n" \
X_"a[114:115], v33 offset:14080\n" \
M_"v[152:155], a[116:119], a[140:143], v[152:155]\n" \
B_"a[200:203], v20, s[56:59], s93 offen offset:2048 sc1\n" \
M_"v[152:155], a[120:123], a[32:35], v[152:155]\n" \
X_"a[116:117], v33 offset:18688\n" \
X_"a[118:119], v33 offset:23296\n" \
M_"v[152:155], a[0:3], a[36:39], v[152:155]\n" \
M_"v[156:159], a[120:123], a[48:51], v[156:159]\n" \
X_"a[120:121], v33 offset:27904\n" \
X_"a[122:123], v33 offset:32512\n" \
M_"v[156:159], a[0:3], a[52:55], v[156:159]\n" \
X_"a[0:1], v33 offset:37120\n" \
B_"a[204:207], v20, s[56:59], s93 offen offset:3072 sc1\n" \
"s_waitcnt lgkmcnt(11)\n" \
M_"v[156:159], a[4:7], a[56:59], v[156:159]\n" \
X_"a[2:3], v33 offset:41728\n" \
M_"v[156:159], a[8:11], a[60:63], v[156:159]\n" \
M_"v[152:155], a[4:7], a[40:43], v[152:155]\n" \
X_"a[4:5], v33 offset:46336\n" \
X_"a[6:7], v33 offset:50944\n" \
M_"v[152:155], a[8:11], a[44:47], v[152:155]\n" \
X_"a[8:9], v33 offset:55552\n" \
X_"a[10:11], v33 offset:60160\n" \
M_"v[160:163], a[12:15], a[128:131], v[160:163]\n" \
B_"v[64:67], v20, s[56:59], s95 offen offset:0 sc1\n" \
M_"v[160:163], a[16:19], a[132:135], v[160:163]\n" \
M_"v[164:167], a[12:15], a[144:147], v[164:167]\n" \
X_"a[12:13], v10 offset:256\n" \
X_"a[14:15], v10 offset:4864\n" \
C_"v152, v152, v153\n" \
C_"v153, v154, v155\n" \
C_"v154, v156, v157\n" \
C_"v155, v158, v159\n" \
"buffer_store_dwordx4 v[152:155], v20, s[56:59], s70 offen offset:0 sc1\n" \
"s_add_u32 s70, s70, 0x1000\n" \
M_"v[164:167], a[16:19], a[148:151], v[164:167]\n" \
X_"a[16:17], v34 offset:256\n" \
X_"a[18:19], v34 offset:4864\n" \
M_"v[164:167], a[20:23], a[152:155], v[164:167]\n" \
M_"v[164:167], a[24:27], a[156:159], v[164:167]\n" \
M_"v[160:163], a[20:23], a[136:139], v[160:163]\n" \
X_"a[20:21], v34 offset:9472\n" \
X_"a[22:23], v34 offset:14080\n" \
M_"v[160:163], a[24:27], a[140:143], v[160:163]\n" \
X_"a[24:25], v34 offset:18688\n" \
X_"a[26:27], v34 offset:23296\n" \
"s_waitcnt vmcnt(19)\n" \
"s_mov_b64 exec, 1\n" \
"buffer_store_dword v39, off, s[56:59], s65 offset:0 sc1\n" \
"s_mov_b64 exec, -1\n" \
M_"v[160:163], a[28:31], a[32:35], v[160:163]\n" \
M_"v[160:163], a[96:99], a[36:39], v[160:163]\n" \
M_"v[164:167], a[28:31], a[48:51], v[164:167]\n" \
X_"a[28:29], v10 offset:9472\n" \
X_"a[30:31], v10 offset:14080\n" \
M_"v[164:167], a[96:99], a[52:55], v[164:167]\n" \
X_"a[96:97], v33 offset:0\n" \
X_"a[98:99], v33 offset:4608\n" \
M_"v[164:167], a[100:103], a[56:59], v[164:167]\n" \
"s_waitcnt lgkmcnt(12)\n" \
M_"v[164:167], a[104:107], a[60:63], v[164:167]\n" \
B_"v[68:71], v20, s[56:59], s95 offen offset:1024 sc1\n" \
M_"v[160:163], a[100:103], a[40:43], v[160:163]\n" \
X_"a[100:101], v33 offset:9216\n" \
X_"a[102:103], v33 offset:13824\n" \
M_"v[160:163], a[104:107], a[44:47], v[160:163]\n" \
X_"a[104:105], v33 offset:18432\n" \
X_"a[106:107], v33 offset:23040\n" \
M_"v[248:251], a[108:111], a[124:127], v[248:251]\n" \
M_"v[248:251], a[112:115], a[224:227], v[248:251]\n" \
M_"v[252:255], a[108:111], a[236:239], v[252:255]\n" \
X_"a[108:109], v33 offset:27648\n" \
X_"a[110:111], v33 offset:32256\n" \
M_"v[252:255], a[112:115], a[240:243], v[252:255]\n" \
C_"v160, v160, v161\n" \
C_"v161, v162, v163\n" \
C_"v162, v164, v165\n" \
C_"v163, v166, v167\n" \
"buffer_store_dwordx4 v[160:163], v20, s[56:59], s71 offen offset:0 sc1\n" \
"s_add_u32 s71, s71, 0x800\n" \
M_"v[252:255], a[116:119], a[244:247], v[252:255]\n" \
X_"a[112:113], v33 offset:36864\n" \
X_"a[114:115], v33 offset:41472\n" \
M_"v[252:255], a[120:123], a[248:251], v[252:255]\n" \
M_"v[248:251], a[116:119], a[228:231], v[248:251]\n" \
X_"a[116:117], v33 offset:46080\n" \
X_"a[118:119], v33 offset:50688\n" \
"s_waitcnt vmcnt(19)\n" \
"s_mov_b64 exec, 1\n" \
"buffer_store_dword v39, off, s[56:59], s66 offset:4 sc1\n" \
"s_mov_b64 exec, -1\n" \
"v_readlane_b32 s66, v11, 50\n" \
M_"v[248:251], a[120:123], a[232:235], v[248:251]\n" \
X_"a[120:121], v33 offset:55296\n" \
X_"a[122:123], v33 offset:59904\n" \
M_"v[176:179], a[0:3], a[128:131], v[176:179]\n" \
M_"v[176:179], a[4:7], a[132:135], v[176:179]\n" \
M_"v[180:183], a[0:3], a[144:147], v[180:183]\n" \
B_"v[72:75], v32, s[56:59], s95 offen offset:0 sc1\n" \
M_"v[180:183], a[4:7], a[148:151], v[180:183]\n" \
M_"v[248:251], a[0:3], a[160:163], v[248:251]\n" \
M_"v[248:251], a[4:7], a[164:167], v[248:251]\n" \
M_"v[252:255], a[0:3], a[176:179], v[252:255]\n" \
X_"a[0:1], v10 offset:0\n" \
X_"a[2:3], v10 offset:4608\n" \
M_"v[252:255], a[4:7], a[180:183], v[252:255]\n" \
X_"a[4:5], v34 offset:0\n" \
"s_waitcnt vmcnt(20)\n" \
M_"v[252:255], a[8:11], a[184:187], v[252:255]\n" \
X_"a[6:7], v34 offset:4608\n" \
"s_waitcnt vmcnt(18) lgkmcnt(10)\n" \
M_"v[252:255], a[12:15], a[188:191], v[252:255]\n" \
B_"v[76:79], v32, s[56:59], s95 offen offset:1024 sc1\n" \
M_"v[248:251], a[8:11], a[168:171], v[248:251]\n" \
M_"v[248:251], a[12:15], a[172:175], v[248:251]\n" \
M_"v[180:183], a[8:11], a[152:155], v[180:183]\n" \
M_"v[180:183], a[12:15], a[156:159], v[180:183]\n" \
M_"v[176:179], a[8:11], a[136:139], v[176:179]\n" \
X_"a[8:9], v34 offset:9216\n" \
X_"a[10:11], v34 offset:13824\n" \
M_"v[176:179], a[12:15], a[140:143], v[176:179]\n" \
X_"a[12:13], v34 offset:18432\n" \
X_"a[14:15], v34 offset:23040\n" \
"s_waitcnt vmcnt(18)\n" \
"s_mov_b64 exec, 1\n" \
"buffer_store_dword v39, off, s[56:59], s67 offset:8 sc1\n" \
"s_mov_b64 exec, -1\n" \
"v_readlane_b32 s67, v11, 51\n" \
M_"v[176:179], a[16:19], a[32:35], v[176:179]\n" \
M_"v[176:179], a[20:23], a[36:39], v[176:179]\n" \
M_"v[180:183], a[16:19], a[48:51], v[180:183]\n" \
M_"v[180:183], a[20:23], a[52:55], v[180:183]\n" \
"s_waitcnt vmcnt(18)\n" \
M_"v[248:251], a[16:19], a[192:195], v[248:251]\n" \
"s_waitcnt vmcnt(17)\n" \
M_"v[248:251], a[20:23], a[196:199], v[248:251]\n" \
"s_waitcnt vmcnt(15)\n" \
M_"v[252:255], a[16:19], a[208:211], v[252:255]\n" \
X_"a[16:17], v10 offset:9216\n" \
"s_mov_b64 exec, 1\n" \
"buffer_store_dword v39, off, s[56:59], s68 offset:12 sc1\n" \
"s_mov_b64 exec, -1\n" \
"s_waitcnt vmcnt(15)\n" \
M_"v[252:255], a[20:23], a[212:215], v[252:255]\n" \
"v_readlane_b32 s68, v11, 52\n" \
"s_waitcnt vmcnt(14)\n" \
M_"v[252:255], a[24:27], a[216:219], v[252:255]\n" \
X_"a[18:19], v10 offset:13824\n" \
"s_waitcnt vmcnt(13)\n" \
M_"v[252:255], a[28:31], a[220:223], v[252:255]\n" \
X_"a[20:21], v33 offset:32\n" \
X_"a[22:23], v33 offset:4640\n" \
B_"v[80:83], v20, s[56:59], s96 offen offset:0 sc1\n" \
"s_waitcnt vmcnt(12)\n" \
M_"v[248:251], a[24:27], a[200:203], v[248:251]\n" \
"s_mov_b64 exec, 1\n" \
"buffer_store_dword v39, off, s[56:59], s69 offset:16 sc1\n" \
"s_mov_b64 exec, -1\n" \
"v_readlane_b32 s69, v11, 53\n" \
".fill (L_ab + 40960 - .) / 4, 4, 0xBF800000\n" \
"L_ap10:\n" \
"s_cbranch_execz L_ap0\n" \
"s_waitcnt vmcnt(12)\n" \
M_"v[248:251], a[28:31], a[204:207], v[248:251]\n" \
M_"v[180:183], a[24:27], a[56:59], v[180:183]\n" \
B_"v[84:87], v20, s[56:59], s96 offen offset:1024 sc1\n" \
M_"v[180:183], a[28:31], a[60:63], v[180:183]\n" \
M_"v[176:179], a[24:27], a[40:43], v[176:179]\n" \
X_"a[24:25], v33 offset:9248\n" \
X_"a[26:27], v33 offset:13856\n" \
M_"v[176:179], a[28:31], a[44:47], v[176:179]\n" \
X_"a[28:29], v33 offset:18464\n" \
C_"v248, v248, v249\n" \
C_"v249, v250, v251\n" \
C_"v250, v252, v253\n" \
C_"v251, v254, v255\n" \
"buffer_store_dwordx4 v[248:251], v20, s[56:59], s72 offen offset:1024 sc1\n" \
M_"v[184:187], a[96:99], a[124:127], v[184:187]\n" \
X_"a[30:31], v33 offset:23072\n" \
C_"v176, v176, v177\n" \
C_"v177, v178, v179\n" \
C_"v178, v180, v181\n" \
C_"v179, v182, v183\n" \
M_"v[184:187], a[100:103], a[224:227], v[184:187]\n" \
"buffer_store_dwordx4 v[176:179], v20, s[56:59], s72 offen offset:0 sc1\n" \
M_"v[188:191], a[96:99], a[236:239], v[188:191]\n" \
X_"a[96:97], v33 offset:27680\n" \
X_"a[98:99], v33 offset:32288\n" \
M_"v[188:191], a[100:103], a[240:243], v[188:191]\n" \
X_"a[100:101], v33 offset:36896\n" \
X_"a[102:103], v33 offset:41504\n" \
M_"v[188:191], a[104:107], a[244:247], v[188:191]\n" \
M_"v[188:191], a[108:111], a[248:251], v[188:191]\n" \
M_"v[184:187], a[104:107], a[228:231], v[184:187]\n" \
X_"a[104:105], v33 offset:46112\n" \
X_"a[106:107], v33 offset:50720\n" \
M_"v[184:187], a[108:111], a[232:235], v[184:187]\n" \
X_"a[108:109], v33 offset:55328\n" \
"s_waitcnt lgkmcnt(11)\n" \
M_"v[184:187], a[112:115], a[160:163], v[184:187]\n" \
B_"v[88:91], v32, s[56:59], s96 offen offset:0 sc1\n" \
M_"v[184:187], a[116:119], a[164:167], v[184:187]\n" \
X_"a[110:111], v33 offset:59936\n" \
M_"v[188:191], a[112:115], a[176:179], v[188:191]\n" \
X_"a[112:113], v10 offset:32\n" \
X_"a[114:115], v10 offset:4640\n" \
M_"v[188:191], a[116:119], a[180:183], v[188:191]\n" \
X_"a[116:117], v34 offset:32\n" \
X_"a[118:119], v34 offset:4640\n" \
M_"v[188:191], a[120:123], a[184:187], v[188:191]\n" \
M_"v[188:191], a[0:3], a[188:191], v[188:191]\n" \
B_"v[92:95], v32, s[56:59], s96 offen offset:1024 sc1\n" \
M_"v[184:187], a[120:123], a[168:171], v[184:187]\n" \
X_"a[120:121], v34 offset:9248\n" \
X_"a[122:123], v34 offset:13856\n" \
M_"v[184:187], a[0:3], a[172:175], v[184:187]\n" \
X_"a[0:1], v34 offset:18464\n" \
X_"a[2:3], v34 offset:23072\n" \
M_"v[184:187], a[4:7], a[192:195], v[184:187]\n" \
M_"v[184:187], a[8:11], a[196:199], v[184:187]\n" \
M_"v[188:191], a[4:7], a[208:211], v[188:191]\n" \
X_"a[4:5], v10 offset:9248\n" \
X_"a[6:7], v10 offset:13856\n" \
M_"v[188:191], a[8:11], a[212:215], v[188:191]\n" \
X_"a[8:9], v33 offset:64\n" \
X_"a[10:11], v33 offset:4672\n" \
M_"v[188:191], a[12:15], a[216:219], v[188:191]\n" \
B_"v[96:99], v20, s[56:59], s97 offen offset:0 sc1\n" \
M_"v[188:191], a[16:19], a[220:223], v[188:191]\n" \
M_"v[184:187], a[12:15], a[200:203], v[184:187]\n" \
X_"a[12:13], v33 offset:9280\n" \
X_"a[14:15], v33 offset:13888\n" \
M_"v[184:187], a[16:19], a[204:207], v[184:187]\n" \
"buffer_load_dword v37, v40, s[56:59], s74 offen sc1\n" \
M_"v[192:195], a[20:23], a[124:127], v[192:195]\n" \
X_"a[16:17], v33 offset:18496\n" \
"s_waitcnt lgkmcnt(13)\n" \
M_"v[192:195], a[24:27], a[224:227], v[192:195]\n" \
X_"a[18:19], v33 offset:23104\n" \
M_"v[196:199], a[20:23], a[236:239], v[196:199]\n" \
X_"a[20:21], v33 offset:27712\n" \
C_"v184, v184, v185\n" \
C_"v185, v186, v187\n" \
C_"v186, v188, v189\n" \
C_"v187, v190, v191\n" \
"buffer_store_dwordx4 v[184:187], v20, s[56:59], s92 offen offset:1024 sc1\n" \
"s_waitcnt vmcnt(18)\n" \
"s_mov_b64 exec, 1\n" \
"buffer_store_dword v39, off, s[56:59], s70 offset:20 sc1\n" \
"s_mov_b64 exec, -1\n" \
"s_add_u32 s92, s92, 0x3800\n" \
M_"v[196:199], a[24:27], a[240:243], v[196:199]\n" \
X_"a[22:23], v33 offset:32320\n" \
X_"a[24:25], v33 offset:36928\n" \
M_"v[196:199], a[28:31], a[244:247], v[196:199]\n" \
X_"a[26:27], v33 offset:41536\n" \
M_"v[196:199], a[96:99], a[248:251], v[196:199]\n" \
M_"v[192:195], a[28:31], a[228:231], v[192:195]\n" \
X_"a[28:29], v33 offset:46144\n" \
X_"a[30:31], v33 offset:50752\n" \
M_"v[192:195], a[96:99], a[232:235], v[192:195]\n" \
B_"v[100:103], v20, s[56:59], s97 offen offset:1024 sc1\n" \
M_"v[192:195], a[100:103], a[160:163], v[192:195]\n" \
X_"a[96:97], v33 offset:55360\n" \
X_"a[98:99], v33 offset:59968\n" \
M_"v[192:195], a[104:107], a[164:167], v[192:195]\n" \
M_"v[196:199], a[100:103], a[176:179], v[196:199]\n" \
X_"a[100:101], v10 offset:64\n" \
X_"a[102:103], v10 offset:4672\n" \
M_"v[196:199], a[104:107], a[180:183], v[196:199]\n" \
X_"a[104:105], v34 offset:64\n" \
X_"a[106:107], v34 offset:4672\n" \
M_"v[196:199], a[108:111], a[184:187], v[196:199]\n" \
B_"v[104:107], v32, s[56:59], s97 offen offset:0 sc1\n" \
M_"v[196:199], a[112:115], a[188:191], v[196:199]\n" \
M_"v[192:195], a[108:111], a[168:171], v[192:195]\n" \
X_"a[108:109], v34 offset:9280\n" \
X_"a[110:111], v34 offset:13888\n" \
M_"v[192:195], a[112:115], a[172:175], v[192:195]\n" \
X_"a[112:113], v34 offset:18496\n" \
"s_waitcnt lgkmcnt(14)\n" \
M_"v[192:195], a[116:119], a[192:195], v[192:195]\n" \
X_"a[114:115], v34 offset:23104\n" \
M_"v[192:195], a[120:123], a[196:199], v[192:195]\n" \
B_"v[108:111], v32, s[56:59], s97 offen offset:1024 sc1\n" \
M_"v[196:199], a[116:119], a[208:211], v[196:199]\n" \
X_"a[116:117], v10 offset:9280\n" \
X_"a[118:119], v10 offset:13888\n" \
M_"v[196:199], a[120:123], a[212:215], v[196:199]\n" \
X_"a[120:121], v33 offset:96\n" \
X_"a[122:123], v33 offset:4704\n" \
"s_waitcnt vmcnt(19)\n" \
"s_mov_b64 exec, 1\n" \
"buffer_store_dword v39, off, s[56:59], s71 offset:24 sc1\n" \
"s_mov_b64 exec, -1\n" \
M_"v[196:199], a[0:3], a[216:219], v[196:199]\n" \
M_"v[196:199], a[4:7], a[220:223], v[196:199]\n" \
M_"v[192:195], a[0:3], a[200:203], v[192:195]\n" \
X_"a[0:1], v33 offset:9312\n" \
X_"a[2:3], v33 offset:13920\n" \
M_"v[192:195], a[4:7], a[204:207], v[192:195]\n" \
X_"a[4:5], v33 offset:18528\n" \
X_"a[6:7], v33 offset:23136\n" \
M_"v[200:203], a[8:11], a[124:127], v[200:203]\n" \
M_"v[200:203], a[12:15], a[224:227], v[200:203]\n" \
M_"v[204:207], a[8:11], a[236:239], v[204:207]\n" \
X_"a[8:9], v33 offset:27744\n" \
X_"a[10:11], v33 offset:32352\n" \
M_"v[204:207], a[12:15], a[240:243], v[204:207]\n" \
C_"v192, v192, v193\n" \
C_"v193, v194, v195\n" \
C_"v194, v196, v197\n" \
C_"v195, v198, v199\n" \
"buffer_store_dwordx4 v[192:195], v20, s[56:59], s66 offen offset:1024 sc1\n" \
M_"v[204:207], a[16:19], a[244:247], v[204:207]\n" \
X_"a[12:13], v33 offset:36960\n" \
"s_waitcnt lgkmcnt(11)\n" \
M_"v[204:207], a[20:23], a[248:251], v[204:207]\n" \
X_"a[14:15], v33 offset:41568\n" \
M_"v[200:203], a[16:19], a[228:231], v[200:203]\n" \
X_"a[16:17], v33 offset:46176\n" \
X_"a[18:19], v33 offset:50784\n" \
M_"v[200:203], a[20:23], a[232:235], v[200:203]\n" \
X_"a[20:21], v33 offset:55392\n" \
X_"a[22:23], v33 offset:60000\n" \
M_"v[200:203], a[24:27], a[160:163], v[200:203]\n" \
M_"v[200:203], a[28:31], a[164:167], v[200:203]\n" \
M_"v[204:207], a[24:27], a[176:179], v[204:207]\n" \
X_"a[24:25], v10 offset:96\n" \
X_"a[26:27], v10 offset:4704\n" \
M_"v[204:207], a[28:31], a[180:183], v[204:207]\n" \
X_"a[28:29], v34 offset:96\n" \
X_"a[30:31], v34 offset:4704\n" \
M_"v[204:207], a[96:99], a[184:187], v[204:207]\n" \
M_"v[204:207], a[100:103], a[188:191], v[204:207]\n" \
M_"v[200:203], a[96:99], a[168:171], v[200:203]\n" \
X_"a[96:97], v34 offset:9312\n" \
X_"a[98:99], v34 offset:13920\n" \
M_"v[200:203], a[100:103], a[172:175], v[200:203]\n" \
X_"a[100:101], v34 offset:18528\n" \
X_"a[102:103], v34 offset:23136\n" \
M_"v[200:203], a[104:107], a[192:195], v[200:203]\n" \
M_"v[200:203], a[108:111], a[196:199], v[200:203]\n" \
M_"v[204:207], a[104:107], a[208:211], v[204:207]\n" \
X_"a[104:105], v10 offset:9312\n" \
X_"a[106:107], v10 offset:13920\n" \
M_"v[204:207], a[108:111], a[212:215], v[204:207]\n" \
X_"a[108:109], v33 offset:128\n" \
X_"a[110:111], v33 offset:4736\n" \
M_"v[204:207], a[112:115], a[216:219], v[204:207]\n" \
"s_waitcnt lgkmcnt(15)\n" \
M_"v[204:207], a[116:119], a[220:223], v[204:207]\n" \
M_"v[200:203], a[112:115], a[200:203], v[200:203]\n" \
X_"a[112:113], v33 offset:9344\n" \
X_"a[114:115], v33 offset:13952\n" \
M_"v[200:203], a[116:119], a[204:207], v[200:203]\n" \
X_"a[116:117], v33 offset:18560\n" \
X_"a[118:119], v33 offset:23168\n" \
M_"v[208:211], a[120:123], a[124:127], v[208:211]\n" \
M_"v[208:211], a[0:3], a[224:227], v[208:211]\n" \
M_"v[212:215], a[120:123], a[236:239], v[212:215]\n" \
X_"a[120:121], v33 offset:27776\n" \
X_"a[122:123], v33 offset:32384\n" \
M_"v[212:215], a[0:3], a[240:243], v[212:215]\n" \
C_"v200, v200, v201\n" \
C_"v201, v202, v203\n" \
C_"v202, v204, v205\n" \
C_"v203, v206, v207\n" \
"buffer_store_dwordx4 v[200:203], v20, s[56:59], s67 offen offset:1024 sc1\n" \
M_"v[212:215], a[4:7], a[244:247], v[212:215]\n" \
X_"a[0:1], v33 offset:36992\n" \
X_"a[2:3], v33 offset:41600\n" \
M_"v[212:215], a[8:11], a[248:251], v[212:215]\n" \
M_"v[208:211], a[4:7], a[228:231], v[208:211]\n" \
X_"a[4:5], v33 offset:46208\n" \
X_"a[6:7], v33 offset:50816\n" \
M_"v[208:211], a[8:11], a[232:235], v[208:211]\n" \
X_"a[8:9], v33 offset:55424\n" \
X_"a[10:11], v33 offset:60032\n" \
M_"v[208:211], a[12:15], a[160:163], v[208:211]\n" \
"s_waitcnt lgkmcnt(13)\n" \
M_"v[208:211], a[16:19], a[164:167], v[208:211]\n" \
M_"v[212:215], a[12:15], a[176:179], v[212:215]\n" \
"s_waitcnt vmcnt(8)\n" \
"v_cmp_eq_u32_e64 s[98:99], s62, v37\n" \
"s_mov_b64 exec, 3\n" \
"buffer_store_dword v39, v41, s[56:59], s73 offen sc1\n" \
"s_mov_b64 exec, -1\n" \
"s_andn2_b32 s75, 0x7f, s98\n" \
"s_mov_b32 s20, s75\n" \
"s_bitcmp1_b32 s20, 0\n" \
"s_cselect_b32 s93, 0x40000000, s12\n" \
"s_bitcmp1_b32 s20, 1\n" \
"s_cselect_b32 s94, 0x40000000, s12\n" \
"s_bitcmp1_b32 s20, 2\n" \
"s_cselect_b32 s95, 0x40000000, s13\n" \
"s_bitcmp1_b32 s20, 3\n" \
"s_cselect_b32 s96, 0x40000000, s13\n" \
"s_bitcmp1_b32 s20, 4\n" \
"s_cselect_b32 s97, 0x40000000, s14\n" \
"s_bitcmp1_b32 s20, 5\n" \
"s_cselect_b32 s64, 0x40000000, s14\n" \
"s_bitcmp1_b32 s20, 6\n" \
"s_cselect_b32 s65, 0x40000000, s15\n" \
"s_add_u32 s66, s66, 0x3000\n" \
"s_add_u32 s67, s67, 0x2800\n" \
M_"v[212:215], a[16:19], a[180:183], v[212:215]\n" \
B_"v[116:119], v20, s[56:59], s93 offen offset:0 sc1\n" \
M_"v[212:215], a[20:23], a[184:187], v[212:215]\n" \
X_"a[12:13], v10 offset:128\n" \
X_"a[14:15], v10 offset:4736\n" \
M_"v[212:215], a[24:27], a[188:191], v[212:215]\n" \
X_"a[16:17], v34 offset:128\n" \
X_"a[18:19], v34 offset:4736\n" \
M_"v[208:211], a[20:23], a[168:171], v[208:211]\n" \
X_"a[20:21], v34 offset:9344\n" \
X_"a[22:23], v34 offset:13952\n" \
M_"v[208:211], a[24:27], a[172:175], v[208:211]\n" \
X_"a[24:25], v34 offset:18560\n" \
X_"a[26:27], v34 offset:23168\n" \
M_"v[208:211], a[28:31], a[192:195], v[208:211]\n" \
M_"v[208:211], a[96:99], a[196:199], v[208:211]\n" \
M_"v[212:215], a[28:31], a[208:211], v[212:215]\n" \
X_"a[28:29], v10 offset:9344\n" \
X_"a[30:31], v10 offset:13952\n" \
M_"v[212:215], a[96:99], a[212:215], v[212:215]\n" \
X_"a[96:97], v33 offset:160\n" \
X_"a[98:99], v33 offset:4768\n" \
M_"v[212:215], a[100:103], a[216:219], v[212:215]\n" \
M_"v[212:215], a[104:107], a[220:223], v[212:215]\n" \
M_"v[208:211], a[100:103], a[200:203], v[208:211]\n" \
X_"a[100:101], v33 offset:9376\n" \
X_"a[102:103], v33 offset:13984\n" \
M_"v[208:211], a[104:107], a[204:207], v[208:211]\n" \
X_"a[104:105], v33 offset:18592\n" \
"s_waitcnt lgkmcnt(15)\n" \
M_"v[216:219], a[108:111], a[124:127], v[216:219]\n" \
X_"a[106:107], v33 offset:23200\n" \
M_"v[216:219], a[112:115], a[224:227], v[216:219]\n" \
B_"v[124:127], v20, s[56:59], s94 offen offset:2048 sc1\n" \
M_"v[220:223], a[108:111], a[236:239], v[220:223]\n" \
X_"a[108:109], v33 offset:27808\n" \
C_"v208, v208, v209\n" \
C_"v209, v210, v211\n" \
C_"v210, v212, v213\n" \
C_"v211, v214, v215\n" \
M_"v[220:223], a[112:115], a[240:243], v[220:223]\n" \
"buffer_store_dwordx4 v[208:211], v20, s[56:59], s68 offen offset:1024 sc1\n" \
"s_add_u32 s68, s68, 0x2000\n" \
M_"v[220:223], a[116:119], a[244:247], v[220:223]\n" \
X_"a[110:111], v33 offset:32416\n" \
X_"a[112:113], v33 offset:37024\n" \
M_"v[220:223], a[120:123], a[248:251], v[220:223]\n" \
X_"a[114:115], v33 offset:41632\n" \
M_"v[216:219], a[116:119], a[228:231], v[216:219]\n" \
X_"a[116:117], v33 offset:46240\n" \
X_"a[118:119], v33 offset:50848\n" \
M_"v[216:219], a[120:123], a[232:235], v[216:219]\n" \
X_"a[120:121], v33 offset:55456\n" \
X_"a[122:123], v33 offset:60064\n" \
M_"v[216:219], a[0:3], a[160:163], v[216:219]\n" \
M_"v[216:219], a[4:7], a[164:167], v[216:219]\n" \
M_"v[220:223], a[0:3], a[176:179], v[220:223]\n" \
X_"a[0:1], v10 offset:160\n" \
X_"a[2:3], v10 offset:4768\n" \
M_"v[220:223], a[4:7], a[180:183], v[220:223]\n" \
X_"a[4:5], v34 offset:160\n" \
X_"a[6:7], v34 offset:4768\n" \
M_"v[220:223], a[8:11], a[184:187], v[220:223]\n" \
"s_waitcnt lgkmcnt(13)\n" \
M_"v[220:223], a[12:15], a[188:191], v[220:223]\n" \
"s_waitcnt vmcnt(11)\n" \
"s_mov_b64 exec, 1\n" \
"buffer_store_dword v39, off, s[56:59], s92 offset:32 sc1\n" \
"s_mov_b64 exec, -1\n" \
M_"v[216:219], a[8:11], a[168:171], v[216:219]\n" \
X_"a[8:9], v34 offset:9376\n" \
X_"a[10:11], v34 offset:13984\n" \
M_"v[216:219], a[12:15], a[172:175], v[216:219]\n" \
X_"a[12:13], v34 offset:18592\n" \
X_"a[14:15], v34 offset:23200\n" \
M_"v[216:219], a[16:19], a[192:195], v[216:219]\n" \
M_"v[216:219], a[20:23], a[196:199], v[216:219]\n" \
B_"v[132:135], v20, s[56:59], s95 offen offset:0 sc1\n" \
M_"v[220:223], a[16:19], a[208:211], v[220:223]\n" \
X_"a[16:17], v10 offset:9376\n" \
X_"a[18:19], v10 offset:13984\n" \
M_"v[220:223], a[20:23], a[212:215], v[220:223]\n" \
X_"a[20:21], v33 offset:192\n" \
X_"a[22:23], v33 offset:4800\n" \
M_"v[220:223], a[24:27], a[216:219], v[220:223]\n" \
M_"v[220:223], a[28:31], a[220:223], v[220:223]\n" \
M_"v[216:219], a[24:27], a[200:203], v[216:219]\n" \
X_"a[24:25], v33 offset:9408\n" \
X_"a[26:27], v33 offset:14016\n" \
M_"v[216:219], a[28:31], a[204:207], v[216:219]\n" \
"buffer_load_dword v37, v40, s[56:59], s74 offen sc1\n" \
M_"v[224:227], a[96:99], a[124:127], v[224:227]\n" \
X_"a[28:29], v33 offset:18624\n" \
X_"a[30:31], v33 offset:23232\n" \
M_"v[224:227], a[100:103], a[224:227], v[224:227]\n" \
M_"v[228:231], a[96:99], a[236:239], v[228:231]\n" \
X_"a[96:97], v33 offset:27840\n" \
X_"a[98:99], v33 offset:32448\n" \
C_"v216, v216, v217\n" \
C_"v217, v218, v219\n" \
C_"v218, v220, v221\n" \
C_"v219, v222, v223\n" \
"buffer_store_dwordx4 v[216:219], v20, s[56:59], s69 offen offset:1024 sc1\n" \
"s_add_u32 s69, s69, 0x1800\n" \
M_"v[228:231], a[100:103], a[240:243], v[228:231]\n" \
X_"a[100:101], v33 offset:37056\n" \
"s_waitcnt lgkmcnt(12)\n" \
M_"v[228:231], a[104:107], a[244:247], v[228:231]\n" \
X_"a[102:103], v33 offset:41664\n" \
M_"v[228:231], a[108:111], a[248:251], v[228:231]\n" \
M_"v[224:227], a[104:107], a[228:231], v[224:227]\n" \
B_"v[140:143], v20, s[56:59], s96 offen offset:2048 sc1\n" \
M_"v[224:227], a[108:111], a[232:235], v[224:227]\n" \
X_"a[104:105], v33 offset:46272\n" \
X_"a[106:107], v33 offset:50880\n" \
M_"v[224:227], a[112:115], a[160:163], v[224:227]\n" \
X_"a[108:109], v33 offset:55488\n" \
X_"a[110:111], v33 offset:60096\n" \
M_"v[224:227], a[116:119], a[164:167], v[224:227]\n" \
M_"v[228:231], a[112:115], a[176:179], v[228:231]\n" \
X_"a[112:113], v10 offset:192\n" \
X_"a[114:115], v10 offset:4800\n" \
M_"v[228:231], a[116:119], a[180:183], v[228:231]\n" \
X_"a[116:117], v34 offset:192\n" \
X_"a[118:119], v34 offset:4800\n" \
M_"v[228:231], a[120:123], a[184:187], v[228:231]\n" \
M_"v[228:231], a[0:3], a[188:191], v[228:231]\n" \
M_"v[224:227], a[120:123], a[168:171], v[224:227]\n" \
X_"a[120:121], v34 offset:9408\n" \
X_"a[122:123], v34 offset:14016\n" \
M_"v[224:227], a[0:3], a[172:175], v[224:227]\n" \
X_"a[0:1], v34 offset:18624\n" \
X_"a[2:3], v34 offset:23232\n" \
"s_waitcnt vmcnt(10)\n" \
"s_mov_b64 exec, 1\n" \
"buffer_store_dword v39, off, s[56:59], s66 offset:36 sc1\n" \
"s_mov_b64 exec, -1\n" \
M_"v[224:227], a[4:7], a[192:195], v[224:227]\n" \
M_"v[224:227], a[8:11], a[196:199], v[224:227]\n" \
M_"v[228:231], a[4:7], a[208:211], v[228:231]\n" \
X_"a[4:5], v10 offset:9408\n" \
X_"a[6:7], v10 offset:14016\n" \
M_"v[228:231], a[8:11], a[212:215], v[228:231]\n" \
X_"a[8:9], v33 offset:224\n" \
"s_waitcnt lgkmcnt(15)\n" \
M_"v[228:231], a[12:15], a[216:219], v[228:231]\n" \
B_"v[148:151], v20, s[56:59], s97 offen offset:0 sc1\n" \
M_"v[228:231], a[16:19], a[220:223], v[228:231]\n" \
X_"a[10:11], v33 offset:4832\n" \
M_"v[224:227], a[12:15], a[200:203], v[224:227]\n" \
X_"a[12:13], v33 offset:9440\n" \
X_"a[14:15], v33 offset:14048\n" \
M_"v[224:227], a[16:19], a[204:207], v[224:227]\n" \
X_"a[16:17], v33 offset:18656\n" \
X_"a[18:19], v33 offset:23264\n" \
M_"v[232:235], a[20:23], a[124:127], v[232:235]\n" \
M_"v[232:235], a[24:27], a[224:227], v[232:235]\n" \
M_"v[236:239], a[20:23], a[236:239], v[236:239]\n" \
X_"a[20:21], v33 offset:27872\n" \
X_"a[22:23], v33 offset:32480\n" \
M_"v[236:239], a[24:27], a[240:243], v[236:239]\n" \
C_"v224, v224, v225\n" \
C_"v225, v226, v227\n" \
C_"v226, v228, v229\n" \
C_"v227, v230, v231\n" \
M_"v[236:239], a[28:31], a[244:247], v[236:239]\n" \
X_"a[24:25], v33 offset:37088\n" \
X_"a[26:27], v33 offset:41696\n" \
M_"v[236:239], a[96:99], a[248:251], v[236:239]\n" \
M_"v[232:235], a[28:31], a[228:231], v[232:235]\n" \
X_"a[28:29], v33 offset:46304\n" \
X_"a[30:31], v33 offset:50912\n" \
M_"v[232:235], a[96:99], a[232:235], v[232:235]\n" \
X_"a[96:97], v33 offset:55520\n" \
X_"a[98:99], v33 offset:60128\n" \
M_"v[232:235], a[100:103], a[160:163], v[232:235]\n" \
"s_waitcnt lgkmcnt(14)\n" \
M_"v[232:235], a[104:107], a[164:167], v[232:235]\n" \
B_"v[156:159], v20, s[56:59], s64 offen offset:2048 sc1\n" \
"v_readlane_b32 s64, v11, 54\n" \
M_"v[236:239], a[100:103], a[176:179], v[236:239]\n" \
X_"a[100:101], v10 offset:224\n" \
X_"a[102:103], v10 offset:4832\n" \
M_"v[236:239], a[104:107], a[180:183], v[236:239]\n" \
X_"a[104:105], v34 offset:224\n" \
X_"a[106:107], v34 offset:4832\n" \
"buffer_store_dwordx4 v[224:227], v20, s[56:59], s64 offen offset:1024 sc1\n" \
M_"v[236:239], a[108:111], a[184:187], v[236:239]\n" \
M_"v[236:239], a[112:115], a[188:191], v[236:239]\n" \
M_"v[232:235], a[108:111], a[168:171], v[232:235]\n" \
X_"a[108:109], v34 offset:9440\n" \
X_"a[110:111], v34 offset:14048\n" \
"s_waitcnt vmcnt(13)\n" \
"s_mov_b64 exec, 1\n" \
"buffer_store_dword v39, off, s[56:59], s67 offset:40 sc1\n" \
"s_mov_b64 exec, -1\n" \
M_"v[232:235], a[112:115], a[172:175], v[232:235]\n" \
X_"a[112:113], v34 offset:18656\n" \
X_"a[114:115], v34 offset:23264\n" \
M_"v[232:235], a[116:119], a[192:195], v[232:235]\n" \
M_"v[232:235], a[120:123], a[196:199], v[232:235]\n" \
M_"v[236:239], a[116:119], a[208:211], v[236:239]\n" \
X_"a[116:117], v10 offset:9440\n" \
X_"a[118:119], v10 offset:14048\n" \
M_"v[236:239], a[120:123], a[212:215], v[236:239]\n" \
X_"a[120:121], v36 offset:0\n" \
X_"a[122:123], v36 offset:1024\n" \
M_"v[236:239], a[0:3], a[216:219], v[236:239]\n" \
M_"v[236:239], a[4:7], a[220:223], v[236:239]\n" \
M_"v[232:235], a[0:3], a[200:203], v[232:235]\n" \
X_"a[0:1], v36 offset:2048\n" \
X_"a[2:3], v36 offset:3072\n" \
M_"v[232:235], a[4:7], a[204:207], v[232:235]\n" \
X_"a[4:5], v36 offset:32\n" \
B_"v[164:167], v20, s[56:59], s65 offen offset:0 sc1\n" \
"v_readlane_b32 s65, v11, 55\n" \
"s_waitcnt lgkmcnt(11)\n" \
M_"v[240:243], a[8:11], a[124:127], v[240:243]\n" \
X_"a[6:7], v36 offset:1056\n" \
M_"v[240:243], a[12:15], a[224:227], v[240:243]\n" \
M_"v[244:247], a[8:11], a[236:239], v[244:247]\n" \
C_"v232, v232, v233\n" \
C_"v233, v234, v235\n" \
C_"v234, v236, v237\n" \
C_"v235, v238, v239\n" \
"buffer_store_dwordx4 v[232:235], v20, s[56:59], s65 offen offset:1024 sc1\n" \
M_"v[244:247], a[12:15], a[240:243], v[244:247]\n" \
X_"a[8:9], v36 offset:2080\n" \
X_"a[10:11], v36 offset:3104\n" \
M_"v[244:247], a[16:19], a[244:247], v[244:247]\n" \
M_"v[244:247], a[20:23], a[248:251], v[244:247]\n" \
M_"v[240:243], a[16:19], a[228:231], v[240:243]\n" \
M_"v[240:243], a[20:23], a[232:235], v[240:243]\n" \
M_"v[168:171], a[24:27], a[128:131], v[168:171]\n" \
M_"v[168:171], a[28:31], a[132:135], v[168:171]\n" \
M_"v[172:175], a[24:27], a[144:147], v[172:175]\n" \
"s_waitcnt vmcnt(9)\n" \
"v_cmp_eq_u32_e64 s[98:99], s62, v37\n" \
"buffer_load_dword v37, v40, s[56:59], s74 offen sc1\n" \
"s_mov_b64 exec, 1\n" \
"buffer_store_dword v39, off, s[56:59], s68 offset:44 sc1\n" \
"s_mov_b64 exec, -1\n" \
"s_or_b32 s75, 0x300, s100\n" \
"s_andn2_b32 s75, s75, s98\n" \
"s_mov_b32 s21, s75\n" \
"s_bitcmp1_b32 s21, 16\n" \
"s_cselect_b32 s70, 0x40000000, s16\n" \
"s_bitcmp1_b32 s21, 17\n" \
"s_cselect_b32 s71, 0x40000000, s16\n" \
"s_bitcmp1_b32 s21, 18\n" \
"s_cselect_b32 s72, 0x40000000, s17\n" \
"s_bitcmp1_b32 s21, 19\n" \
"s_cselect_b32 s73, 0x40000000, s17\n" \
"s_bitcmp1_b32 s21, 20\n" \
"s_cselect_b32 s92, 0x40000000, s18\n" \
"s_bitcmp1_b32 s21, 21\n" \
"s_cselect_b32 s93, 0x40000000, s18\n" \
"s_bitcmp1_b32 s21, 22\n" \
"s_cselect_b32 s94, 0x40000000, s19\n" \
"s_bitcmp1_b32 s21, 23\n" \
"s_cselect_b32 s95, 0x40000000, s19\n" \
"s_bitcmp1_b32 s21, 8\n" \
"s_cselect_b32 s96, 0x40000000, s12\n" \
"s_bitcmp1_b32 s21, 9\n" \
"s_cselect_b32 s97, 0x40000000, s12\n" \
"s_add_u32 s64, s64, 0x1000\n" \
"s_add_u32 s65, s65, 0x800\n" \
M_"v[172:175], a[28:31], a[148:151], v[172:175]\n" \
B_"v[112:115], v20, s[56:59], s70 offen offset:0 sc1\n" \
M_"v[240:243], a[24:27], a[160:163], v[240:243]\n" \
M_"v[240:243], a[28:31], a[164:167], v[240:243]\n" \
M_"v[244:247], a[24:27], a[176:179], v[244:247]\n" \
M_"v[244:247], a[28:31], a[180:183], v[244:247]\n" \
B_"v[184:187], v20, s[56:59], s71 offen offset:2048 sc1\n" \
M_"v[244:247], a[96:99], a[184:187], v[244:247]\n" \
"v_readlane_b32 s71, v11, 57\n" \
M_"v[244:247], a[100:103], a[188:191], v[244:247]\n" \
M_"v[240:243], a[96:99], a[168:171], v[240:243]\n" \
M_"v[240:243], a[100:103], a[172:175], v[240:243]\n" \
B_"v[120:123], v20, s[56:59], s72 offen offset:0 sc1\n" \
M_"v[172:175], a[96:99], a[152:155], v[172:175]\n" \
"v_readlane_b32 s72, v11, 48\n" \
M_"v[172:175], a[100:103], a[156:159], v[172:175]\n" \
M_"v[168:171], a[96:99], a[136:139], v[168:171]\n" \
M_"v[168:171], a[100:103], a[140:143], v[168:171]\n" \
B_"v[192:195], v20, s[56:59], s73 offen offset:2048 sc1\n" \
M_"v[168:171], a[104:107], a[32:35], v[168:171]\n" \
"v_readlane_b32 s73, v11, 46\n" \
"s_waitcnt lgkmcnt(0)\n" \
M_"v[168:171], a[108:111], a[36:39], v[168:171]\n" \
M_"v[172:175], a[104:107], a[48:51], v[172:175]\n" \
M_"v[172:175], a[108:111], a[52:55], v[172:175]\n" \
B_"v[128:131], v20, s[56:59], s92 offen offset:0 sc1\n" \
M_"v[240:243], a[104:107], a[192:195], v[240:243]\n" \
"v_readlane_b32 s92, v11, 47\n" \
M_"v[240:243], a[108:111], a[196:199], v[240:243]\n" \
M_"v[244:247], a[104:107], a[208:211], v[244:247]\n" \
M_"v[244:247], a[108:111], a[212:215], v[244:247]\n" \
B_"v[200:203], v20, s[56:59], s93 offen offset:2048 sc1\n" \
M_"v[244:247], a[112:115], a[216:219], v[244:247]\n" \
M_"v[244:247], a[116:119], a[220:223], v[244:247]\n" \
M_"v[240:243], a[112:115], a[200:203], v[240:243]\n" \
M_"v[240:243], a[116:119], a[204:207], v[240:243]\n" \
".fill (L_ab + 45056 - .) / 4, 4, 0xBF800000\n" \
"L_ap11:\n" \
"s_cbranch_execz L_ap0\n" \
B_"v[176:179], v20, s[56:59], s94 offen offset:0 sc1\n" \
"s_waitcnt vmcnt(17)\n" \
"s_mov_b64 exec, 1\n" \
"buffer_store_dword v39, off, s[56:59], s69 offset:48 sc1\n" \
"s_mov_b64 exec, -1\n" \
M_"v[172:175], a[112:115], a[56:59], v[172:175]\n" \
M_"v[172:175], a[116:119], a[60:63], v[172:175]\n" \
M_"v[168:171], a[112:115], a[40:43], v[168:171]\n" \
M_"v[168:171], a[116:119], a[44:47], v[168:171]\n" \
B_"v[248:251], v20, s[56:59], s95 offen offset:2048 sc1\n" \
M_"v[168:171], a[120:123], v[64:67], v[168:171]\n" \
M_"v[172:175], a[120:123], v[72:75], v[172:175]\n" \
M_"v[240:243], a[120:123], v[80:83], v[240:243]\n" \
M_"v[244:247], a[120:123], v[88:91], v[244:247]\n" \
B_"v[188:191], v20, s[56:59], s96 offen offset:1024 sc1\n" \
M_"v[168:171], a[0:3], v[68:71], v[168:171]\n" \
M_"v[172:175], a[0:3], v[76:79], v[172:175]\n" \
M_"v[240:243], a[0:3], v[84:87], v[240:243]\n" \
M_"v[244:247], a[0:3], v[92:95], v[244:247]\n" \
B_"v[196:199], v20, s[56:59], s97 offen offset:3072 sc1\n" \
M_"v[56:59], a[4:7], v[96:99], 0\n" \
M_"v[60:63], a[4:7], v[104:107], 0\n" \
M_"v[56:59], a[8:11], v[100:103], v[56:59]\n" \
M_"v[60:63], a[8:11], v[108:111], v[60:63]\n" \
"s_waitcnt vmcnt(16)\n" \
"s_mov_b64 exec, 1\n" \
"buffer_store_dword v39, off, s[56:59], s64 offset:52 sc1\n" \
"s_mov_b64 exec, -1\n" \
"s_waitcnt vmcnt(14)\n" \
"s_mov_b64 exec, 1\n" \
"buffer_store_dword v39, off, s[56:59], s65 offset:56 sc1\n" \
"s_mov_b64 exec, -1\n" \
"s_cmp_lg_u32 s20, 0\n" \
"s_cbranch_scc1 L_sna_%=\n" \
"L_rna_%=:\n" \
"s_cmp_lg_u32 s21, 0\n" \
"s_cbranch_scc1 L_snx_%=\n" \
"L_rnx_%=:\n" \
"s_waitcnt vmcnt(14)\n" \
"v_cmp_eq_u32_e64 s[98:99], s62, v37\n" \
M_"v[168:171], v[50:53], v[116:119], v[168:171]\n" \
"s_andn2_b32 s75, 0x400, s98\n" \
"s_cbranch_scc1 L_sx0_%=\n" \
"L_rx0_%=:\n" \
"s_add_u32 s66, s63, 0x1000\n" \
M_"v[172:175], v[48:51], v[116:119], v[172:175]\n" \
B_"v[204:207], v20, s[56:59], s66 offen offset:1024 sc1\n" \
"s_andn2_b32 s75, 0x800, s98\n" \
"s_cbranch_scc1 L_sx1_%=\n" \
"L_rx1_%=:\n" \
"s_add_u32 s67, s63, 0x1800\n" \
M_"v[168:171], v[50:53], v[124:127], v[168:171]\n" \
M_"v[172:175], v[48:51], v[124:127], v[172:175]\n" \
M_"v[168:171], v[50:53], v[132:135], v[168:171]\n" \
B_"v[212:215], v20, s[56:59], s67 offen offset:1024 sc1\n" \
"s_andn2_b32 s75, 0x1000, s98\n" \
"s_cbranch_scc1 L_sx2_%=\n" \
"L_rx2_%=:\n" \
"s_add_u32 s68, s63, 0x2000\n" \
M_"v[172:175], v[48:51], v[132:135], v[172:175]\n" \
M_"v[168:171], v[50:53], v[140:143], v[168:171]\n" \
M_"v[172:175], v[48:51], v[140:143], v[172:175]\n" \
B_"v[220:223], v20, s[56:59], s68 offen offset:1024 sc1\n" \
"s_andn2_b32 s75, 0x2000, s98\n" \
"s_cbranch_scc1 L_sx3_%=\n" \
"L_rx3_%=:\n" \
"s_add_u32 s69, s63, 0x2800\n" \
M_"v[168:171], v[50:53], v[148:151], v[168:171]\n" \
M_"v[172:175], v[48:51], v[148:151], v[172:175]\n" \
M_"v[168:171], v[50:53], v[156:159], v[168:171]\n" \
B_"v[228:231], v20, s[56:59], s69 offen offset:1024 sc1\n" \
"s_andn2_b32 s75, 0x4000, s98\n" \
"s_cbranch_scc1 L_sx4_%=\n" \
"L_rx4_%=:\n" \
"s_add_u32 s70, s63, 0x3000\n" \
M_"v[172:175], v[48:51], v[156:159], v[172:175]\n" \
M_"v[168:171], v[50:53], v[164:167], v[168:171]\n" \
M_"v[172:175], v[48:51], v[164:167], v[172:175]\n" \
B_"v[236:239], v20, s[56:59], s70 offen offset:1024 sc1\n" \
"s_waitcnt vmcnt(17)\n" \
M_"v[56:59], v[50:53], v[112:115], v[56:59]\n" \
M_"v[60:63], v[48:51], v[112:115], v[60:63]\n" \
"s_waitcnt vmcnt(16)\n" \
M_"v[56:59], v[50:53], v[184:187], v[56:59]\n" \
M_"v[60:63], v[48:51], v[184:187], v[60:63]\n" \
C_"v168, v168, v169\n" \
"s_waitcnt vmcnt(15)\n" \
M_"v[56:59], v[50:53], v[120:123], v[56:59]\n" \
C_"v172, v172, v173\n" \
C_"v169, v170, v171\n" \
M_"v[60:63], v[48:51], v[120:123], v[60:63]\n" \
C_"v173, v174, v175\n" \
"s_waitcnt vmcnt(14)\n" \
M_"v[56:59], v[50:53], v[192:195], v[56:59]\n" \
"buffer_store_dwordx2 v[168:169], v42, s[88:91], s73 offen sc1\n" \
M_"v[60:63], v[48:51], v[192:195], v[60:63]\n" \
"s_waitcnt vmcnt(14)\n" \
M_"v[56:59], v[50:53], v[128:131], v[56:59]\n" \
"buffer_store_dwordx2 v[172:173], v43, s[88:91], s73 offen sc1\n" \
M_"v[60:63], v[48:51], v[128:131], v[60:63]\n" \
"s_waitcnt vmcnt(14)\n" \
M_"v[56:59], v[50:53], v[200:203], v[56:59]\n" \
M_"v[60:63], v[48:51], v[200:203], v[60:63]\n" \
"s_waitcnt vmcnt(13)\n" \
M_"v[56:59], v[50:53], v[176:179], v[56:59]\n" \
M_"v[60:63], v[48:51], v[176:179], v[60:63]\n" \
"s_waitcnt vmcnt(11)\n" \
M_"v[56:59], v[50:53], v[248:251], v[56:59]\n" \
M_"v[60:63], v[48:51], v[248:251], v[60:63]\n" \
"s_waitcnt vmcnt(10)\n" \
M_"v[240:243], v[50:53], v[188:191], v[240:243]\n" \
M_"v[244:247], v[48:51], v[188:191], v[244:247]\n" \
"s_waitcnt vmcnt(9)\n" \
M_"v[240:243], v[50:53], v[196:199], v[240:243]\n" \
M_"v[244:247], v[48:51], v[196:199], v[244:247]\n" \
"s_mov_b64 exec, 1\n" \
"buffer_store_dword v46, off, s[56:59], s71 offset:0 sc1\n" \
"s_mov_b64 exec, -1\n" \
C_"v56, v56, v57\n" \
C_"v60, v60, v61\n" \
C_"v57, v58, v59\n" \
C_"v61, v62, v63\n" \
"buffer_store_dwordx2 v[56:57], v42, s[88:91], s72 offen sc1\n" \
"buffer_store_dwordx2 v[60:61], v43, s[88:91], s72 offen sc1\n" \
"s_waitcnt vmcnt(9)\n" \
M_"v[240:243], v[50:53], v[204:207], v[240:243]\n" \
M_"v[244:247], v[48:51], v[204:207], v[244:247]\n" \
"s_waitcnt vmcnt(8)\n" \
M_"v[240:243], v[50:53], v[212:215], v[240:243]\n" \
M_"v[244:247], v[48:51], v[212:215], v[244:247]\n" \
"s_waitcnt vmcnt(7)\n" \
M_"v[240:243], v[50:53], v[220:223], v[240:243]\n" \
M_"v[244:247], v[48:51], v[220:223], v[244:247]\n" \
"s_waitcnt vmcnt(6)\n" \
M_"v[240:243], v[50:53], v[228:231], v[240:243]\n" \
M_"v[244:247], v[48:51], v[228:231], v[244:247]\n" \
"s_waitcnt vmcnt(5)\n" \
M_"v[240:243], v[50:53], v[236:239], v[240:243]\n" \
M_"v[244:247], v[48:51], v[236:239], v[244:247]\n" \
"s_nop 6\n" \
C_"v240, v240, v241\n" \
C_"v244, v244, v245\n" \
C_"v241, v242, v243\n" \
C_"v245, v246, v247\n" \
"buffer_store_dwordx2 v[240:241], v42, s[88:91], s92 offen sc1\n" \
"buffer_store_dwordx2 v[244:245], v43, s[88:91], s92 offen sc1\n" \
"L_fin_%=:\n"
#define ATT_VCLOB "v21","v22","v23","v24","v25","v26","v27","v28","v29","v32","v33","v34","v35","v36","v37","v38","v39","v40","v41","v42","v43","v44","v45","v46","v47","v48","v49","v50","v51","v52","v53","v54","v55","v56","v57","v58","v59","v60","v61","v62","v63","v64","v65","v66","v67","v68","v69","v70","v71","v72","v73","v74","v75","v76","v77","v78","v79","v80","v81","v82","v83","v84","v85","v86","v87","v88","v89","v90","v91","v92","v93","v94","v95","v96","v97","v98","v99","v100","v101","v102","v103","v104","v105","v106","v107","v108","v109","v110","v111","v112","v113","v114","v115","v116","v117","v118","v119","v120","v121","v122","v123","v124","v125","v126","v127","v128","v129","v130","v131","v132","v133","v134","v135","v136","v137","v138","v139","v140","v141","v142","v143","v144","v145","v146","v147","v148","v149","v150","v151","v152","v153","v154","v155","v156","v157","v158","v159","v160","v161","v162","v163","v164","v165","v166","v167","v168","v169","v170","v171","v172","v173","v174","v175","v176","v177","v178","v179","v180","v181","v182","v183","v184","v185","v186","v187","v188","v189","v190","v191","v192","v193","v194","v195","v196","v197","v198","v199","v200","v201","v202","v203","v204","v205","v206","v207","v208","v209","v210","v211","v212","v213","v214","v215","v216","v217","v218","v219","v220","v221","v222","v223","v224","v225","v226","v227","v228","v229","v230","v231","v232","v233","v234","v235","v236","v237","v238","v239","v240","v241","v242","v243","v244","v245","v246","v247","v248","v249","v250","v251","v252","v253","v254","v255"
#define ATT_SCLOB "s63","s64","s65","s66","s67","s68","s69","s70","s71","s72","s73","s74","s75","s76","s77","s78","s79","s80","s81","s82","s83","s84","s85","s86","s87","s88","s89","s90","s91","s92","s93","s95","s96","s97","s98","s99","s100","s101"
#define ATT_WTCH 0
#define ATT_PEEL 1
#define ATT_PEEL_FOW 1
#define ATT_PEEL_PRE 0
#define ATT_PEEL_ODD(b) (0u)
#define ATT_PAGES 11
#define ATT_TAILB 968
enum { T_fo, T_fj, T_fa, T_fas, T_fb, T_fbs, T_f8, T_mk8, T_wt, T_wol, T_woh, T_wbl, T_wbh, T_outl, T_outh, T_r1l, T_r1h, T_r1n, T_r1s, T_r2n, T_r2s, T_m2b, T_m0, T_m1, T_m1b, T_mw, T_m3, T_m4, T_ml, T_w0, T_w1, T_w2, T_w3, T_A1, T_A2, T_A3, T_B1, T_B2, T_B3, T_Ao, T_Bo, T_Al, T_Bl, T_l8, T_xr, T_x8r, T_oA, T_oB, T_o8, T_x0, T_x1, T_x2, T_x3, T_x4, T_x5, T_x6, T_x8, T_ep };

__device__ __forceinline__ void out_consts(u32x4 (&vp)[3], const u16* wo, const u16* wob, const u16* out, const unsigned b, const unsigned w, const unsigned lane) {
    const unsigned sp = b & 7u, nt = (b >> 3) & 3u, j = b >> 5, head = 4u * sp + nt, OOR = 0x40000000u;
    unsigned vw[9];
#pragma unroll
    for (unsigned i = 0; i < 9u; ++i) {
        const unsigned q = 64u * i + lane, row = __umul24(q, 3641u) >> 16, c = q - __umul24(row, 18u);
        vw[i] = __umul24(row, DM * 2) + (c < 16u ? (c * 16u + sp * 32u + 32u) & 255u : c * 16u) - (i & 3u) * 1024u;
    }
    const unsigned row = 16u * w + (lane >> 2), r9 = __umul24(16u * (row >> 5) + (row & 12u) + 2u * ((row >> 4) & 1u) + ((row >> 1) & 1u), 18u) + 16u + (row & 1u);
    unsigned vwl = __umul24(r9, DM * 2) + ((lane & 2u) ? 256u : sp * 32u) + (lane & 1u) * 16u, vrd = __umul24(lane >> 2, 288u) + (lane & 3u) * 8u;
    unsigned tab = 0;
#define TB(n, v) asm("v_writelane_b32 %0, %1, %2" : "+v"(tab) : "s"((unsigned)(v)), "n"((int)T_##n))
    const unsigned FJ = (unsigned)WS_F + (F_J + 640u * j) * 4u, mt = 2u * (w & 2u) + (w & 1u), w2 = w < 2u ? w : 2u, hw = head * 147456u;
    const unsigned FR = (unsigned)WS_F + F_R * 4u + (8u * j + sp) * 1024u;
    TB(fo, FJ + (ATT_PEEL_FOW ? 16u * head + 4u * w : 4u * head)); TB(fj, FJ); TB(fa, FR + (w | 2u) * 256u); TB(fas, FR + w * 256u + nt * 4u);
    TB(fb, FR + 128u + w * 256u); TB(fbs, FR + 128u + w * 256u + nt * 4u);
    TB(ep, ssel(b + w, OOR, (unsigned)WS_F + F_EPOCH * 4u));
    const u16* pwo = wo + nt * 144u; const u16* pwb = wob + nt * 144u;
    TB(wol, PLO(pwo)); TB(woh, PHI(pwo) & 0xffffu); TB(wbl, PLO(pwb)); TB(wbh, PHI(pwb) & 0xffffu); TB(outl, PLO(out)); TB(outh, PHI(out) & 0xffffu);
    const unsigned odd = ATT_PEEL_ODD(b), wo1 = w + 3u * odd;
#define OSEL(a, b_) (ATT_PEEL_ODD(1u) ? ssel(odd, a, b_) : (b_))
#define WU(u) (((4u * sp + ((nt + 1u + ((u) >> 2)) & 3u)) * 4u + ((u) & 3u)) * 36864u)
    TB(r1l, sslt<3>(wo1, PLO(pwo), PLO(pwb))); TB(r1h, sslt<3>(wo1, PHI(pwo), PHI(pwb)) & 0xffffu);
    TB(r1n, sslt<3>(wo1, 72u * sp + 71u, 4095u) * 1152u + 288u); TB(r1s, sslt<3>(wo1, (72u * sp + 32u * w) * 1152u, ATT_PEEL_ODD(1u) ? sslt<2>(w, WU(w), sslt<3>(w, OOR, hw + 110592u)) : hw + 110592u));
    if (ATT_PEEL_ODD(1u)) { TB(r2n, (72u * sp + 143u) * 1152u + 288u); TB(r2s, sslt<3>(w, (72u * sp + 72u + 32u * w) * 1152u, OOR)); TB(m2b, sslt<2>(w, 41984u + 9216u * w, 32768u)); }
    TB(m0, 131072u + 9216u * w2); TB(w0, hw + 36864u * w2); TB(m1, sslt<3>(w, 41984u + 9216u * w, 32768u)); TB(m1b, ssel(w ^ 2u, sslt<3>(w, 41984u + 9216u * w, 32768u), 114688u - 4096u));
    TB(mw, 65536u + 9216u * w); TB(m3, sslt<3>(w, 102400u + 9216u * w, 0u)); TB(m4, OSEL(9216u + 13312u * (w & 1u), sslt<3>(w, 131072u + 9216u * w, 9216u))); TB(ml, 18432u + 1024u * w);
    const unsigned tA = (unsigned)WS_AF + (8u * j + mt) * (KS_TOT * 1024u), tB = (unsigned)WS_AF + (120u - 8u * j + mt) * (KS_TOT * 1024u);
#define HK(k) (4u * sp + ((nt + k) & 3u))
    TB(w1, WU(w + 2u * odd)); TB(w2, WU(4u + w + 2u * odd)); TB(w3, WU(8u + (w | (2u * odd))));
    TB(A1, tA + 18432u + HK(1u) * 4096u); TB(A2, tA + 18432u + HK(2u) * 4096u); TB(A3, tA + 18432u + HK(3u) * 4096u);
    TB(B1, tB + 18432u + HK(1u) * 4096u); TB(B2, tB + 18432u + HK(2u) * 4096u); TB(B3, tB + 18432u + HK(3u) * 4096u);
    const unsigned pre = ATT_PEEL_PRE ? sslt<7>(j, tA + 18432u + head * 4096u, OOR) : OOR;
    TB(Ao, OSEL(pre, tA + sp * 2048u)); TB(Bo, OSEL(pre + 2048u, tB + sp * 2048u)); TB(Al, tA + 16384u); TB(Bl, tB + 16384u);
    const unsigned nred = w ^ (sp >> 1), blk = sp & 1u;
    const unsigned xg = (unsigned)WS_X + (4u * j + nt) * 589824u + w * 16384u, oA = (128u * j + 16u * mt) * 1152u + nt * 288u, oB = (1920u - 128u * j + 16u * mt) * 1152u + nt * 288u;
    TB(l8, ssel(nred, OOR, ssel(blk, tB, tA) + 16384u)); TB(xr, xg + sp * 65536u); TB(x8r, ssel(nred, OOR, xg + 524288u + (sp & 1u) * 1024u));
    TB(oA, oA + sp * 32u); TB(oB, oB + sp * 32u); TB(o8, ssel(nred, OOR, ssel(blk, oB, oA) + 256u)); TB(x8, xg + 524288u + sp * 2048u);
    TB(f8, xg + w * 131072u + 14400u + sp * 4u); TB(mk8, ssel(nred, 0u, 0xff0000u));
#define XI(i) TB(x##i, xg + ((sp + 1u + i) & 7u) * 65536u + i * 2048u)
    XI(0); XI(1); XI(2); XI(3); XI(4); XI(5); XI(6);
#undef XI
#undef HK
#undef WU
#undef TB
    vp[0] = (u32x4){vw[0], vw[1], vw[2], vw[3]}; vp[1] = (u32x4){vw[4], vw[5], vw[6], vw[7]}; vp[2] = (u32x4){vw[8], vwl, vrd, tab};
}
__global__ __launch_bounds__(256, 1) __attribute__((aligned(4096))) void fused_kernel(const u16* __restrict__ x, const u16* __restrict__ ckv, const u16* __restrict__ wq, const u16* __restrict__ wuk,
                                                       const u16* __restrict__ wuv, const u16* __restrict__ wqkv, const u16* __restrict__ gw,
                                                       const u16* __restrict__ wo, const u16* __restrict__ wob, char* __restrict__ ws_, u16* __restrict__ out) {
    using namespace att;
    extern __shared__ __attribute__((aligned(16))) char lds[];
    u32x4 sa[5]; u32x2 s24, s28; char* ws; unsigned tdum; kaptr ka = (kaptr)__builtin_amdgcn_kernarg_segment_ptr();
    asm volatile("s_load_dwordx8 s[8:15], %[ka], 0x0\n\ts_load_dwordx8 s[16:23], %[ka], 0x20\n\ts_getpc_b64 s[4:5]\n\tv_lshlrev_b32_e32 v191, 7, %[t]\n\ts_bfe_u32 s6, %[b], %[cb]\n\t"
                 "v_lshl_add_u32 v191, s6, %[cs], v191\n\tv_min_u32_e32 v191, %[e], v191\n\tglobal_load_dword v191, v191, s[4:5]\n\ts_load_dwordx4 s[24:27], %[ka], 0x40\n\ts_load_dwordx2 s[28:29], %[ka], 0x50"
                 : "={s[8:11]}"(sa[0]), "={s[12:15]}"(sa[1]), "={s[16:19]}"(sa[2]), "={s[20:23]}"(sa[3]), "={s[24:25]}"(s24), "={s[26:27]}"(ws), "={s[28:29]}"(s28), "={v191}"(tdum), [ka] "+{s[0:1]}"(ka)
                 : [t] "v"(threadIdx.x), [b] "s"(blockIdx.x), [e] "i"(CT_END), [cb] "i"(CT_END < 0x20000 ? 0x20003 : 0x30003), [cs] "i"(CT_END < 0x20000 ? 15 : 14) : "s4", "s5", "s6", "scc");
    SBAR();
    const unsigned wid = rfl(threadIdx.x >> 6);
    unsigned tag, vl;
    const int b = blockIdx.x;
    u32x4 vp[5], srdA; u32x2 sd;
    proj_phase(ka, ws, wid, vp, vl, sa, s24, s28, tdum, srdA, sd, tag
    );
    unsigned* const flags = (unsigned*)(ws + WS_F);
    RTS(2);
#define AST(L) do {} while (0)
#define ATT_TAIL "s_endpgm\n"
    unsigned abad;
    asm volatile(ATT_ASM ATT_TAIL "L_end_%=:\n s_nop 1"
                 : "={s94}"(abad)
                 : "{v[0:3]}"(vp[0]), "{v[4:7]}"(vp[1]), "{v[8:11]}"(vp[2]), "{v[12:15]}"(vp[3]), "{v[16:19]}"(vp[4]), "{v20}"(vl), "{s[56:59]}"(srdA), "{s[60:61]}"(sd), "{s62}"(tag),
                   "{s[8:11]}"(sa[0]), "{s[12:15]}"(sa[1]), "{s[16:19]}"(sa[2]), "{s[20:23]}"(sa[3]), "{s24}"(sa[4][0]), "{s3}"(wid), "{s2}"(b), "{s[28:29]}"(s28)
                 : "memory", "m0", "vcc", "scc", ATT_VCLOB, ATT_SCLOB, ACLOB_LO, ACLOB_HI);
    {
        unsigned b2 = b, w2 = wid;
        asm volatile("s_waitcnt vmcnt(0)" : "+s"(ka), "+s"(b2), "+s"(w2), "+s"(ws) :: "memory");
        const unsigned j2 = b2 >> 5, p2 = w2 & 1u, h2 = (b2 & 7u) * 4u + ((b2 >> 3) & 3u);
#pragma unroll 1
        for (unsigned i = w2 & 2u; i < ((w2 & 2u) ? (abad ? 8u : 4u) : 2u); ++i)
            att_exact(ka[0], ka[2], ws, h2, (i < 4u ? 1920u - 128u * j2 : 128u * j2 - 128u) + 32u * i + 16u * p2, *reinterpret_cast<const float*>(lds + LDS_G + (32u * i + 16u * p2 + (lane_id() & 15u)) * 4u));
        asm volatile("s_waitcnt vmcnt(0)" ::: "memory");
        unsigned* fa = (unsigned*)(ws + WS_F) + F_R + (8u * j2 + (b2 & 7u)) * 256u + (2u + p2) * 64u + ((b2 >> 3) & 3u);
        if (w2 & 2u) st_flag(fa, tag);
        wait_flags(fa, ld_flag(fa), tag);
        const unsigned l2 = lane_id(); u32x4 c[3]; out_consts(c, ka[7], ka[8], ka[10], b2, w2, l2);
        asm volatile("s_waitcnt vmcnt(0) lgkmcnt(0)\n\ts_nop 7\n\ts_branch L_att_stub" :: "{v[0:3]}"(c[0]), "{v[4:7]}"(c[1]), "{v[8:11]}"(c[2]), "{v20}"(l2 * 16u), "{s[56:59]}"(SRDA), "{s60}"(SD(8u * j2, h2, w2)),
                     "{s61}"(SD(120u - 8u * j2, h2, w2)), "{s62}"(tag), "{s3}"(w2), "{s2}"(b2) : "memory");
    }
    RTS(5);
}

extern "C" void kernel_launch(void* const* d_in, const int* in_sizes, int n_in, void* d_out, int out_size, void* d_ws, size_t ws_size,
                              hipStream_t stream) {
    (void)hipFuncSetAttribute((const void*)fused_kernel, hipFuncAttributeMaxDynamicSharedMemorySize, att::LDS_BYTES);
    fused_kernel<<<dim3(256), 256, att::LDS_BYTES, stream>>>((const u16*)d_in[0], (const u16*)d_in[4], (const u16*)d_in[5], (const u16*)d_in[6], (const u16*)d_in[7], (const u16*)d_in[1],
                                                             (const u16*)d_in[9], (const u16*)d_in[2], (const u16*)d_in[8], (char*)d_ws, (u16*)d_out);
}
```

```cpp
#include <hip/hip_runtime.h>
#include <hip/hip_bf16.h>
#include <stdint.h>
#include <utility>

typedef short bf16x8 __attribute__((ext_vector_type(8)));
typedef short s16x4 __attribute__((ext_vector_type(4)));
typedef float f32x16 __attribute__((ext_vector_type(16)));
typedef float f32x4 __attribute__((ext_vector_type(4)));
typedef unsigned u32x4 __attribute__((ext_vector_type(4)));
typedef unsigned u32x2 __attribute__((ext_vector_type(2)));
typedef unsigned short u16;

constexpr int S = 2048, DM = 576, NH = 32, HD = 128, DC = 512, NQKV = 1728, DHA = 18;
constexpr int NBIG = NH * HD;
constexpr int KS_TOT = 146;

#define SBAR() __builtin_amdgcn_sched_barrier(0)
constexpr size_t WS_Q = 0, WS_K = WS_Q + (size_t)S * NBIG * 2, WS_V = WS_K + (size_t)S * NBIG * 2, WS_AF = WS_V + (size_t)S * NBIG * 2, WS_X = WS_AF + (size_t)128 * KS_TOT * 1024,
                 WS_P = WS_X + (size_t)64 * 16 * 9216, WS_F = WS_P + (size_t)128 * (4 * 32 * 256 + 1024) * 4;
constexpr size_t WS_L = (size_t)96 << 20;
static_assert(WS_F == 96206848, "workspace map is baked into the generated asm");
constexpr int F_EPOCH = 0, F_J = 1024, F_ST = 8192, F_KV = 40960, F_R = 65536;
constexpr unsigned TAG_STEP = 0x9E3779B1u;
__device__ __forceinline__ unsigned ld_flag(const unsigned* p) { return __hip_atomic_load(p, __ATOMIC_RELAXED, __HIP_MEMORY_SCOPE_AGENT); }
__device__ __forceinline__ void st_flag(unsigned* p, unsigned v) { __hip_atomic_store(p, v, __ATOMIC_RELAXED, __HIP_MEMORY_SCOPE_AGENT); }
__device__ __forceinline__ void wait_flags(const unsigned* p, unsigned v, unsigned tag) {
    if (__builtin_expect(__builtin_amdgcn_ballot_w64(v != tag) != 0ull, 0)) {
        unsigned spins = 0;
        do { __builtin_amdgcn_s_sleep(1); } while (__builtin_amdgcn_ballot_w64(ld_flag(p) != tag) != 0ull && ++spins < (1u << 22));
    }
    asm volatile("" ::: "memory");
}

__device__ __forceinline__ unsigned lane_id() { unsigned l; asm volatile("v_mbcnt_lo_u32_b32 %0, -1, 0\n\tv_mbcnt_hi_u32_b32 %0, -1, %0" : "=v"(l)); return l; }
#define RTS(slot) do {} while (0)
#define CT_END ((PROJ_PAGES + ATT_PAGES) * 4096 + (ATT_TAILB & ~127) - 128)
#define ACLOB_LO "a0","a1","a2","a3","a4","a5","a6","a7","a8","a9","a10","a11","a12","a13","a14","a15","a16","a17","a18","a19","a20","a21","a22","a23","a24","a25","a26","a27","a28","a29","a30","a31","a32","a33","a34","a35","a36","a37","a38","a39","a40","a41","a42","a43","a44","a45","a46","a47","a48","a49","a50","a51","a52","a53","a54","a55","a56","a57","a58","a59","a60","a61","a62","a63","a64","a65","a66","a67","a68","a69","a70","a71","a72","a73","a74","a75","a76","a77","a78","a79","a80","a81","a82","a83","a84","a85","a86","a87","a88","a89","a90","a91","a92","a93","a94","a95","a96","a97","a98","a99","a100","a101","a102","a103","a104","a105","a106","a107","a108","a109","a110","a111","a112","a113","a114","a115","a116","a117","a118","a119","a120","a121","a122","a123","a124","a125","a126","a127"
#define ACLOB_HI "a128","a129","a130","a131","a132","a133","a134","a135","a136","a137","a138","a139","a140","a141","a142","a143","a144","a145","a146","a147","a148","a149","a150","a151","a152","a153","a154","a155","a156","a157","a158","a159","a160","a161","a162","a163","a164","a165","a166","a167","a168","a169","a170","a171","a172","a173","a174","a175","a176","a177","a178","a179","a180","a181","a182","a183","a184","a185","a186","a187","a188","a189","a190","a191","a192","a193","a194","a195","a196","a197","a198","a199","a200","a201","a202","a203","a204","a205","a206","a207","a208","a209","a210","a211","a212","a213","a214","a215","a216","a217","a218","a219","a220","a221","a222","a223","a224","a225","a226","a227","a228","a229","a230","a231","a232","a233","a234","a235","a236","a237","a238","a239","a240","a241","a242","a243","a244","a245","a246","a247","a248","a249","a250","a251","a252","a253","a254","a255"
#define M_ "v_mfma_f32_16x16x32_bf16 "
#define X_ "ds_read_b64_tr_b16 "
#define B_ "buffer_load_dwordx4 "
#define C_ "v_cvt_pk_bf16_f32 "
#define R_ "ds_read_b128 "
#define E_ "v_exp_f32_e32 "
#define PLO(p) ((unsigned)(size_t)(p))
#define PHI(p) ((unsigned)((size_t)(p) >> 32))
__device__ __forceinline__ unsigned vsel(bool c, unsigned a, unsigned b) { asm("" : "+v"(a), "+v"(b)); return c ? a : b; }
__device__ __forceinline__ unsigned ssel(unsigned c, unsigned a, unsigned b) { unsigned r; asm("s_cmp_lg_u32 %1, 0\n\ts_cselect_b32 %0, %2, %3" : "=s"(r) : "s"(c), "s"(a), "s"(b) : "scc"); return r; }
template <int N> __device__ __forceinline__ unsigned sslt(unsigned c, unsigned a, unsigned b) { unsigned r; asm("s_cmpk_lt_u32 %1, %4\n\ts_cselect_b32 %0, %2, %3" : "=s"(r) : "s"(c), "s"(a), "s"(b), "n"(N) : "scc"); return r; }
namespace att {
constexpr int LDS_BYTES = 163840;
constexpr unsigned LDS_G = 159744u, LDS_L = 161792u;
}
#define SD(J8, H, W) ((unsigned)WS_AF + (((J8) + 2u * ((W) & 2u) + ((W) & 1u)) * KS_TOT + 18u + 4u * (H)) * 1024u)
#define SRDA (u32x4){PLO(ws), PHI(ws) & 0xffffu, (unsigned)WS_L, 0x00020000u}
#define PROJ_ASM \
".p2align 3\n" \
"L_pb:\n" \
"s_waitcnt lgkmcnt(0)\n" \
"s_mov_b32 s64, s10\n" \
"s_and_b32 s65, s11, 0xffff\n" \
"s_mov_b32 s66, 0x200000\n" \
"s_mov_b32 s67, 0x20000\n" \
"s_mov_b32 s68, s14\n" \
"s_and_b32 s69, s15, 0xffff\n" \
"s_mov_b32 s70, 0x400000\n" \
"s_mov_b32 s71, 0x20000\n" \
"s_mov_b32 s72, s16\n" \
"s_and_b32 s73, s17, 0xffff\n" \
"s_mov_b32 s74, 0x400000\n" \
"s_mov_b32 s75, 0x20000\n" \
"s_add_u32 m0, s30, 0x0\n" \
"s_movk_i32 s84, 0x0\n" \
B_"v22, s[64:67], s84 offen offset:0 lds\n" \
"s_movk_i32 s84, 0x1c00\n" \
B_"v23, s[64:67], s84 offen offset:1024 lds\n" \
"s_movk_i32 s84, 0x3800\n" \
B_"v22, s[64:67], s84 offen offset:2048 lds\n" \
"s_movk_i32 s84, 0x5400\n" \
B_"v23, s[64:67], s84 offen offset:3072 lds\n" \
"s_add_u32 m0, s31, 0x0\n" \
"s_movk_i32 s84, 0x0\n" \
B_"v26, s[68:71], s84 offen offset:0 lds\n" \
"s_movk_i32 s84, 0x7c00\n" \
B_"v26, s[68:71], s84 offen offset:1024 lds\n" \
"s_addk_i32 s84, 0x7c00\n" \
B_"v27, s[68:71], s84 offen offset:2048 lds\n" \
"s_addk_i32 s84, 0x7c00\n" \
B_"v27, s[68:71], s84 offen offset:3072 lds\n" \
"s_movk_i32 s7, 0x300\n" \
"s_lshr_b32 s4, s2, 1\n" \
"s_and_b32 s4, s4, 60\n" \
"s_or_b32 s4, s4, s3\n" \
"v_lshrrev_b32_e32 v212, 4, v20\n" \
"v_min_u32_e32 v212, 59, v212\n" \
"s_cmpk_lt_u32 s2, 0x80\n" \
"s_cselect_b32 s6, s14, s16\n" \
"v_subrev_u32_e32 v214, 48, v212\n" \
"v_mov_b32_e32 v212, s10\n" \
"v_mov_b32_e32 v215, s6\n" \
"s_cmpk_lt_u32 s2, 0x80\n" \
"s_cselect_b32 s5, s15, s17\n" \
"v_cmp_gt_u32_e32 vcc, s7, v20\n" \
"v_mov_b32_e64 v213, s11\n" \
"v_bfe_u32 v216, v20, 4, 3\n" \
"v_cndmask_b32_e32 v212, v212, v215, vcc\n" \
"v_mov_b32_e32 v215, s5\n" \
"s_lshl_b32 s5, s3, 4\n" \
"v_cndmask_b32_e32 v213, v213, v215, vcc\n" \
"v_lshlrev_b32_e64 v215, 5, v20\n" \
"v_and_b32_e32 v215, 0x7000, v215\n" \
"v_lshl_add_u32 v215, s4, 6, v215\n" \
"s_lshl_b32 s4, s2, 3\n" \
"s_and_b32 s4, s4, 56\n" \
"v_or3_b32 v215, v215, s4, v216\n" \
"s_lshl_b32 s4, s2, 6\n" \
"s_or_b32 s6, s5, s4\n" \
"v_cmp_gt_u32_e64 s[4:5], 6, v214\n" \
"v_add_u32_e32 v215, 0x2000, v215\n" \
"s_nop 0\n" \
"v_cndmask_b32_e64 v216, 4, 2, s[4:5]\n" \
"v_add3_u32 v214, s6, v214, v216\n" \
"s_lshl_b32 s4, s3, 9\n" \
"v_cndmask_b32_e32 v214, v214, v215, vcc\n" \
"v_lshlrev_b32_e32 v214, 7, v214\n" \
"v_mov_b32_e64 v215, 0\n" \
"v_lshl_add_u64 v[184:185], v[212:213], 0, v[214:215]\n" \
"s_or_b32 s32, s4, 0x25000\n" \
"s_add_u32 m0, s30, 0x1000\n" \
"s_mov_b32 s84, 0x8000\n" \
B_"v22, s[64:67], s84 offen offset:0 lds\n" \
"s_and_b32 s33, s3, 1\n" \
"v_lshrrev_b32_e32 v212, 4, v20\n" \
"v_lshrrev_b32_e64 v214, 8, v20\n" \
"v_bfe_u32 v212, v212, 1, 3\n" \
"v_lshlrev_b32_e32 v215, 2, v20\n" \
"s_addk_i32 s84, 0x1c00\n" \
B_"v23, s[64:67], s84 offen offset:1024 lds\n" \
"v_lshrrev_b32_e64 v216, 1, v20\n" \
"v_bfe_u32 v213, v20, 4, 4\n" \
"s_lshl_b32 s4, s3, 5\n" \
"v_xor_b32_e32 v212, v212, v214\n" \
"v_lshlrev_b32_e32 v214, 11, v214\n" \
"s_addk_i32 s84, 0x1c00\n" \
B_"v22, s[64:67], s84 offen offset:2048 lds\n" \
"v_and_b32_e32 v215, 0x300, v215\n" \
"v_and_b32_e64 v217, 24, v216\n" \
"v_and_or_b32 v213, s4, 64, v213\n" \
"v_or3_b32 v214, v215, v214, v217\n" \
"s_lshl_b32 s4, s33, 14\n" \
"s_addk_i32 s84, 0x1c00\n" \
B_"v23, s[64:67], s84 offen offset:3072 lds\n" \
"v_and_b32_e32 v215, 0xe0, v216\n" \
"v_lshlrev_b32_e32 v213, 7, v213\n" \
"v_lshlrev_b32_e32 v212, 4, v212\n" \
"v_or3_b32 v214, v214, s4, v215\n" \
"v_or_b32_e64 v28, v212, v213\n" \
"s_add_u32 m0, s31, 0x4000\n" \
"s_movk_i32 s84, 0x0\n" \
B_"v26, s[72:75], s84 offen offset:0 lds\n" \
"v_add_u32_e32 v32, 0x10000, v214\n" \
"v_bitop3_b32 v29, v212, 64, v213 bitop3:0x36\n" \
"v_xor_b32_e32 v33, 0x20, v32\n" \
"v_xor_b32_e64 v34, 0x40, v32\n" \
"v_xor_b32_e32 v35, 0x60, v32\n" \
"s_movk_i32 s84, 0x7c00\n" \
B_"v26, s[72:75], s84 offen offset:1024 lds\n" \
"v_xor_b32_e32 v36, 0x80, v32\n" \
"v_xor_b32_e32 v37, 0xa0, v32\n" \
"v_xor_b32_e32 v38, 0xc0, v32\n" \
"v_xor_b32_e32 v39, 0xe0, v32\n" \
"s_mov_b32 s80, s26\n" \
"s_addk_i32 s84, 0x7c00\n" \
B_"v27, s[72:75], s84 offen offset:2048 lds\n" \
"s_and_b32 s81, s27, 0xffff\n" \
"s_mov_b32 s82, 0x10000000\n" \
"s_mov_b32 s83, 0x20000\n" \
"s_mov_b32 s76, s18\n" \
"s_and_b32 s77, s19, 0xffff\n" \
"s_addk_i32 s84, 0x7c00\n" \
B_"v27, s[72:75], s84 offen offset:3072 lds\n" \
"s_mov_b32 s78, 0x1e6000\n" \
"s_mov_b32 s79, 0x20000\n" \
"s_branch L_pgo_%=\n" \
"L_pret:\n" \
"s_setpc_b64 s[88:89]\n" \
"L_pgo_%=:\n" \
"s_mul_i32 s88, s33, 0xa000\n" \
"s_lshl_b32 s89, s3, 11\n" \
"s_and_b32 s89, s89, 0x1000\n" \
"s_add_u32 s88, s88, s89\n" \
"s_getpc_b64 s[86:87]\n" \
"L_hpa_%=:\n" \
"s_add_u32 s86, s86, L_pp1-L_hpa_%=\n" \
"s_addc_u32 s87, s87, 0\n" \
"s_add_u32 s86, s86, s88\n" \
"s_addc_u32 s87, s87, 0\n" \
"s_add_u32 m0, s30, 0x8000\n" \
"s_movk_i32 s84, 0x80\n" \
B_"v22, s[64:67], s84 offen offset:0 lds\n" \
"s_lshr_b32 s4, s2, 1\n" \
"s_and_b32 s4, s4, 0x7c\n" \
"v_bfe_u32 v212, v20, 4, 5\n" \
"s_movk_i32 s84, 0x1c80\n" \
B_"v23, s[64:67], s84 offen offset:1024 lds\n" \
"s_or_b32 s4, s4, s3\n" \
"v_min_u32_e32 v212, 26, v212\n" \
"v_mad_u64_u32 v[212:213], s[4:5], s4, 27, v[212:213]\n" \
"s_movk_i32 s84, 0x3880\n" \
B_"v22, s[64:67], s84 offen offset:2048 lds\n" \
"v_mul_u32_u24_e32 v213, 0xaaab, v212\n" \
"s_lshr_b32 s7, s2, 5\n" \
"v_lshrrev_b32_e32 v213, 18, v213\n" \
"s_movk_i32 s84, 0x5480\n" \
B_"v23, s[64:67], s84 offen offset:3072 lds\n" \
"s_mov_b32 s4, 0x1fffffa\n" \
"s_and_b32 s6, s2, 7\n" \
"v_mad_u64_u32 v[214:215], s[4:5], v213, s4, v[212:213]\n" \
"s_add_u32 m0, s31, 0x8000\n" \
"s_mov_b32 s84, 0x80000\n" \
B_"v26, s[68:71], s84 offen offset:0 lds\n" \
"s_mulk_i32 s7, 0x90\n" \
"v_mul_u32_u24_e32 v213, 27, v213\n" \
"v_lshrrev_b32_e32 v214, 1, v214\n" \
"s_addk_i32 s84, 0x7c00\n" \
B_"v26, s[68:71], s84 offen offset:1024 lds\n" \
"v_and_b32_e32 v212, 1, v212\n" \
"s_add_u32 s4, s7, s20\n" \
"v_mul_u32_u24_e32 v214, 9, v214\n" \
"v_add3_u32 v212, v212, s6, v213\n" \
"s_addc_u32 s5, 0, s21\n" \
"s_addk_i32 s84, 0x7c00\n" \
B_"v27, s[68:71], s84 offen offset:2048 lds\n" \
"v_add_lshl_u32 v212, v212, v214, 7\n" \
"v_mov_b32_e64 v213, 0\n" \
"v_lshl_add_u64 v[186:187], s[18:19], 0, v[212:213]\n" \
"s_addk_i32 s84, 0x7c00\n" \
B_"v27, s[68:71], s84 offen offset:3072 lds\n" \
"s_bitcmp1_b32 s3, 0\n" \
"s_cselect_b32 s6, 0x8c, 0\n" \
"s_add_u32 s4, s4, s6\n" \
"v_mov_b32_e32 v212, v187\n" \
"s_addc_u32 s5, s5, 0\n" \
"s_add_u32 m0, s30, 0x9000\n" \
"s_mov_b32 s84, 0x8080\n" \
B_"v22, s[64:67], s84 offen offset:0 lds\n" \
"v_writelane_b32 v186, s4, 31\n" \
"v_writelane_b32 v212, s5, 31\n" \
"v_mov_b32_e32 v187, v212\n" \
"s_addk_i32 s84, 0x1c00\n" \
B_"v23, s[64:67], s84 offen offset:1024 lds\n" \
"s_lshl_b32 s35, s2, 10\n" \
"s_lshr_b32 s37, s2, 1\n" \
"s_mul_i32 s37, s37, 0x24800\n" \
"s_addk_i32 s84, 0x1c00\n" \
B_"v22, s[64:67], s84 offen offset:2048 lds\n" \
"s_and_b32 s35, s35, 0x400\n" \
"s_lshl_b32 s36, s3, 8\n" \
"s_or_b32 s35, s37, s35\n" \
"s_addk_i32 s84, 0x1c00\n" \
B_"v23, s[64:67], s84 offen offset:3072 lds\n" \
"s_and_b32 s4, s2, 7\n" \
"s_lshr_b32 s6, s2, 5\n" \
"s_lshl_b32 s34, s2, 5\n" \
"s_add_u32 m0, s31, 0xc000\n" \
"s_mov_b32 s84, 0x80000\n" \
B_"v26, s[72:75], s84 offen offset:0 lds\n" \
"s_add_i32 s35, s35, s36\n" \
"s_lshl_b32 s7, s4, 10\n" \
"s_and_b32 s34, s34, 0x300\n" \
"s_addk_i32 s84, 0x7c00\n" \
B_"v26, s[72:75], s84 offen offset:1024 lds\n" \
"s_add_i32 s35, s35, 0x3004000\n" \
"s_lshl_b32 s37, s6, 13\n" \
"s_or_b32 s34, s7, s34\n" \
"s_addk_i32 s84, 0x7c00\n" \
B_"v27, s[72:75], s84 offen offset:2048 lds\n" \
"s_or_b32 s7, s37, s7\n" \
"v_mov_b32_e32 v213, s35\n" \
"s_or_b32 s7, s7, s36\n" \
"s_addk_i32 s84, 0x7c00\n" \
B_"v27, s[72:75], s84 offen offset:3072 lds\n" \
"s_lshr_b32 s5, s2, 3\n" \
"s_or_b32 s7, s7, 0x5c00000\n" \
"v_writelane_b32 v213, s7, 2\n" \
"s_mul_i32 s5, s5, 0x90000\n" \
"v_writelane_b32 v213, s7, 3\n" \
"s_lshl_b32 s7, s3, 14\n" \
"s_lshl_b32 s4, s4, 16\n" \
"s_add_i32 s5, s5, s7\n" \
"s_or_b32 s34, s34, 0x5be8000\n" \
"v_writelane_b32 v213, s34, 4\n" \
"s_add_i32 s4, s5, s4\n" \
"v_writelane_b32 v213, s34, 5\n" \
"s_add_i32 s4, s4, 0x4243800\n" \
"v_writelane_b32 v213, s4, 6\n" \
"s_mul_i32 s4, s6, 0xa00\n" \
"v_lshlrev_b32_e32 v212, 3, v20\n" \
"s_lshl_b32 s5, s3, 7\n" \
"v_and_b32_e32 v212, 0x80, v212\n" \
"s_add_i32 s4, s4, s5\n" \
"s_add_i32 s4, s4, 0x5bc0f80\n" \
"v_writelane_b32 v213, s4, 7\n" \
"v_add_u32_e32 v49, v213, v212\n" \
"s_mov_b32 m0, s32\n" \
"s_mov_b32 exec_lo, 0xffffff\n" \
"s_mov_b32 exec_hi, 0xfff0000\n" \
"global_load_lds_dword v[184:185], off\n" \
"s_mov_b64 exec, -1\n" \
"s_mov_b32 m0, s32\n" \
"s_mov_b64 exec, 0xff\n" \
"buffer_load_dword v49, s[80:83], 0 offen lds\n" \
"s_mov_b64 exec, -1\n" \
"s_mov_b32 s84, 0x5bc0000\n" \
"buffer_load_dword v180, off, s[80:83], s84 sc1\n" \
"s_cmp_lg_u32 s33, 0\n" \
"s_cbranch_scc1 L_w1_%=\n" \
"L_w0_%=:\n" \
"s_waitcnt vmcnt(19)\n" \
"s_barrier\n" \
X_"a[160:161], v32 offset:0\n" \
X_"a[162:163], v32 offset:1024\n" \
R_"a[128:131], v28 offset:16384\n" \
X_"a[224:225], v32 offset:8192\n" \
X_"a[226:227], v32 offset:9216\n" \
R_"a[192:195], v29 offset:16384\n" \
R_"a[132:135], v28 offset:18432\n" \
R_"a[196:199], v29 offset:18432\n" \
X_"a[164:165], v33 offset:0\n" \
X_"a[166:167], v33 offset:1024\n" \
X_"a[228:229], v33 offset:8192\n" \
X_"a[230:231], v33 offset:9216\n" \
R_"a[136:139], v28 offset:20480\n" \
R_"a[200:203], v29 offset:20480\n" \
"s_waitcnt lgkmcnt(11)\n" \
M_"a[0:3], a[160:163], a[128:131], 0\n" \
X_"a[168:169], v34 offset:0\n" \
"s_lshr_b32 s4, s2, 1\n" \
"s_waitcnt lgkmcnt(9)\n" \
M_"a[0:3], a[224:227], a[192:195], a[0:3]\n" \
X_"a[170:171], v34 offset:1024\n" \
"s_nop 5\n" \
"s_waitcnt lgkmcnt(9)\n" \
M_"a[16:19], a[160:163], a[132:135], 0\n" \
X_"a[232:233], v34 offset:8192\n" \
"s_and_b32 s4, s4, 0x7c\n" \
"s_waitcnt lgkmcnt(9)\n" \
M_"a[16:19], a[224:227], a[196:199], a[16:19]\n" \
X_"a[234:235], v34 offset:9216\n" \
"s_nop 5\n" \
"s_waitcnt lgkmcnt(8)\n" \
M_"a[20:23], a[164:167], a[132:135], 0\n" \
R_"a[140:143], v28 offset:22528\n" \
"v_mov_b32_e32 v213, s13\n" \
"s_waitcnt lgkmcnt(7)\n" \
M_"a[20:23], a[228:231], a[196:199], a[20:23]\n" \
R_"a[204:207], v29 offset:22528\n" \
"s_nop 5\n" \
M_"a[4:7], a[164:167], a[128:131], 0\n" \
X_"a[172:173], v35 offset:0\n" \
"s_or_b32 s4, s4, s3\n" \
M_"a[4:7], a[228:231], a[192:195], a[4:7]\n" \
X_"a[174:175], v35 offset:1024\n" \
"s_nop 5\n" \
"s_waitcnt lgkmcnt(9)\n" \
M_"a[32:35], a[160:163], a[136:139], 0\n" \
X_"a[236:237], v35 offset:8192\n" \
"v_lshrrev_b32_e32 v212, 4, v20\n" \
"s_waitcnt lgkmcnt(9)\n" \
M_"a[32:35], a[224:227], a[200:203], a[32:35]\n" \
X_"a[238:239], v35 offset:9216\n" \
"s_nop 5\n" \
M_"a[36:39], a[164:167], a[136:139], 0\n" \
R_"a[144:147], v28 offset:0\n" \
"v_mad_u64_u32 v[214:215], s[4:5], s4, 36, v[212:213]\n" \
M_"a[36:39], a[228:231], a[200:203], a[36:39]\n" \
R_"a[208:211], v29 offset:0\n" \
"s_nop 5\n" \
"s_waitcnt lgkmcnt(10)\n" \
M_"a[40:43], a[168:171], a[136:139], 0\n" \
R_"a[148:151], v28 offset:2048\n" \
"v_mov_b32_e32 v217, s9\n" \
"s_waitcnt lgkmcnt(9)\n" \
M_"a[40:43], a[232:235], a[200:203], a[40:43]\n" \
R_"a[212:215], v29 offset:2048\n" \
"s_nop 5\n" \
M_"a[24:27], a[168:171], a[132:135], 0\n" \
R_"a[152:155], v28 offset:4096\n" \
"s_movk_i32 s4, 0x240\n" \
M_"a[24:27], a[232:235], a[196:199], a[24:27]\n" \
R_"a[216:219], v29 offset:4096\n" \
"s_nop 5\n" \
M_"a[8:11], a[168:171], a[128:131], 0\n" \
R_"a[156:159], v28 offset:6144\n" \
"v_cmp_gt_u32_e32 vcc, s4, v20\n" \
M_"a[8:11], a[232:235], a[192:195], a[8:11]\n" \
R_"a[220:223], v29 offset:6144\n" \
"s_mov_b32 m0, s32\n" \
"s_nop 5\n" \
"s_waitcnt lgkmcnt(13)\n" \
M_"a[48:51], a[160:163], a[140:143], 0\n" \
"s_mov_b32 exec_lo, 0xff000000\n" \
"s_mov_b32 exec_hi, 0x0\n" \
"global_load_lds_dword v[184:185], off\n" \
"s_mov_b64 exec, -1\n" \
"s_waitcnt lgkmcnt(12)\n" \
M_"a[48:51], a[224:227], a[204:207], a[48:51]\n" \
"s_nop 5\n" \
X_"a[176:177], v36 offset:0\n" \
M_"a[52:55], a[164:167], a[140:143], 0\n" \
X_"a[178:179], v36 offset:1024\n" \
"s_lshl_b32 s4, s2, 3\n" \
M_"a[52:55], a[228:231], a[204:207], a[52:55]\n" \
"s_mov_b32 m0, s32\n" \
"s_nop 5\n" \
M_"a[56:59], a[168:171], a[140:143], 0\n" \
"s_mov_b32 exec_lo, 0xffff\n" \
"s_mov_b32 exec_hi, 0x0\n" \
"global_load_lds_dword v[186:187], off\n" \
"s_mov_b64 exec, -1\n" \
"s_mov_b64 exec, 0\n" \
"s_swappc_b64 s[88:89], s[86:87]\n" \
"s_mov_b64 exec, -1\n" \
"s_add_u32 s86, s86, 0x2000\n" \
"s_addc_u32 s87, s87, 0\n" \
M_"a[56:59], a[232:235], a[204:207], a[56:59]\n" \
"s_nop 5\n" \
X_"a[240:241], v36 offset:8192\n" \
"s_waitcnt lgkmcnt(13)\n" \
M_"a[60:63], a[172:175], a[140:143], 0\n" \
X_"a[242:243], v36 offset:9216\n" \
"s_and_b32 s4, s4, 56\n" \
"s_waitcnt lgkmcnt(12)\n" \
M_"a[60:63], a[236:239], a[204:207], a[60:63]\n" \
"s_nop 5\n" \
X_"a[180:181], v37 offset:0\n" \
M_"a[44:47], a[172:175], a[136:139], 0\n" \
X_"a[182:183], v37 offset:1024\n" \
"v_cndmask_b32_e32 v217, v217, v213, vcc\n" \
M_"a[44:47], a[236:239], a[200:203], a[44:47]\n" \
"s_nop 5\n" \
X_"a[244:245], v37 offset:8192\n" \
M_"a[28:31], a[172:175], a[132:135], 0\n" \
X_"a[246:247], v37 offset:9216\n" \
"v_lshlrev_b32_e32 v213, 3, v214\n" \
M_"a[28:31], a[236:239], a[196:199], a[28:31]\n" \
"s_nop 5\n" \
X_"a[184:185], v38 offset:0\n" \
M_"a[12:15], a[172:175], a[128:131], 0\n" \
X_"a[186:187], v38 offset:1024\n" \
"v_and_b32_e32 v213, 0x1ffc0, v213\n" \
M_"a[12:15], a[236:239], a[192:195], a[12:15]\n" \
"s_nop 5\n" \
"v_and_b32_e32 v214, 7, v214\n" \
"s_waitcnt lgkmcnt(15)\n" \
M_"a[76:79], a[172:175], a[144:147], 0\n" \
X_"a[248:249], v38 offset:8192\n" \
"v_or3_b32 v213, v214, s4, v213\n" \
M_"a[76:79], a[236:239], a[208:211], a[76:79]\n" \
"s_nop 5\n" \
"s_lshl_b32 s4, s2, 2\n" \
M_"a[72:75], a[168:171], a[144:147], 0\n" \
"s_or_b32 s4, s4, s3\n" \
"s_mul_i32 s4, s4, 18\n" \
M_"a[72:75], a[232:235], a[208:211], a[72:75]\n" \
"s_nop 5\n" \
X_"a[250:251], v38 offset:9216\n" \
M_"a[68:71], a[164:167], a[144:147], 0\n" \
"s_mov_b64 exec, 0\n" \
"s_swappc_b64 s[88:89], s[86:87]\n" \
"s_mov_b64 exec, -1\n" \
"s_add_u32 s86, s86, 0x2000\n" \
"s_addc_u32 s87, s87, 0\n" \
M_"a[68:71], a[228:231], a[208:211], a[68:71]\n" \
"s_nop 5\n" \
"v_min_u32_e64 v212, 53, v212\n" \
M_"a[64:67], a[160:163], a[144:147], 0\n" \
"s_sub_i32 s4, s4, 36\n" \
"v_add_u32_e32 v212, s4, v212\n" \
M_"a[64:67], a[224:227], a[208:211], a[64:67]\n" \
"s_nop 5\n" \
X_"a[188:189], v39 offset:0\n" \
M_"a[80:83], a[160:163], a[148:151], 0\n" \
"v_mov_b32_e32 v215, s12\n" \
"v_mov_b32_e32 v216, s8\n" \
"s_waitcnt lgkmcnt(15)\n" \
M_"a[80:83], a[224:227], a[212:215], a[80:83]\n" \
"s_nop 5\n" \
"v_cndmask_b32_e32 v212, v212, v213, vcc\n" \
M_"a[84:87], a[164:167], a[148:151], 0\n" \
X_"a[190:191], v39 offset:1024\n" \
"v_cndmask_b32_e64 v216, v216, v215, vcc\n" \
M_"a[84:87], a[228:231], a[212:215], a[84:87]\n" \
"s_nop 5\n" \
"v_lshlrev_b32_e32 v212, 7, v212\n" \
M_"a[88:91], a[168:171], a[148:151], 0\n" \
"v_mov_b32_e64 v213, 0\n" \
"v_lshl_add_u64 v[188:189], v[216:217], 0, v[212:213]\n" \
M_"a[88:91], a[232:235], a[212:215], a[88:91]\n" \
"s_nop 5\n" \
X_"a[252:253], v39 offset:8192\n" \
M_"a[92:95], a[172:175], a[148:151], 0\n" \
"s_movk_i32 s5, 0xd80\n" \
"s_lshr_b32 s6, s2, 5\n" \
M_"a[92:95], a[236:239], a[212:215], a[92:95]\n" \
"s_nop 5\n" \
"v_lshrrev_b32_e64 v212, 4, v20\n" \
M_"a[108:111], a[172:175], a[152:155], 0\n" \
X_"a[254:255], v39 offset:9216\n" \
"v_lshl_or_b32 v215, s3, 6, v212\n" \
M_"a[108:111], a[236:239], a[216:219], a[108:111]\n" \
"s_nop 5\n" \
"v_mul_u32_u24_e32 v216, 0x2aab, v215\n" \
M_"a[104:107], a[168:171], a[152:155], 0\n" \
"s_mov_b64 exec, 0\n" \
"s_swappc_b64 s[88:89], s[86:87]\n" \
"s_mov_b64 exec, -1\n" \
"s_add_u32 s86, s86, 0x2000\n" \
"s_addc_u32 s87, s87, 0\n" \
M_"a[104:107], a[232:235], a[216:219], a[104:107]\n" \
"s_nop 5\n" \
"v_lshrrev_b32_e64 v216, 16, v216\n" \
M_"a[100:103], a[164:167], a[152:155], 0\n" \
"v_mad_i32_i24 v217, v216, -6, v215\n" \
"v_add_u32_e32 v218, 0xfffffe50, v215\n" \
M_"a[100:103], a[228:231], a[216:219], a[100:103]\n" \
"s_nop 5\n" \
"s_mul_i32 s35, s6, 0x48\n" \
M_"a[96:99], a[160:163], a[152:155], 0\n" \
"v_lshrrev_b32_e32 v219, 1, v217\n" \
"v_add_u32_e64 v218, s35, v216\n" \
M_"a[96:99], a[224:227], a[216:219], a[96:99]\n" \
"s_nop 5\n" \
"v_mul_u32_u24_e32 v219, 0x480, v219\n" \
"s_waitcnt lgkmcnt(15)\n" \
M_"a[112:115], a[160:163], a[156:159], 0\n" \
"v_and_b32_e64 v214, 16, v20\n" \
"v_mad_u32_u24 v219, v218, s5, v219\n" \
M_"a[112:115], a[224:227], a[220:223], a[112:115]\n" \
"s_nop 5\n" \
"v_or_b32_e32 v192, v219, v214\n" \
M_"a[116:119], a[164:167], a[156:159], 0\n" \
"v_mov_b32_e32 v220, 2.0\n" \
"v_mov_b32_e32 v222, 7\n" \
M_"a[116:119], a[228:231], a[220:223], a[116:119]\n" \
"s_nop 5\n" \
"v_lshlrev_b32_e32 v220, 7, v218\n" \
M_"a[120:123], a[168:171], a[156:159], 0\n" \
"v_lshrrev_b32_e32 v218, 1, v218\n" \
"v_and_b32_e32 v218, 4, v218\n" \
M_"a[120:123], a[232:235], a[220:223], a[120:123]\n" \
"s_nop 5\n" \
"v_and_b32_e32 v216, 2, v216\n" \
M_"a[124:127], a[172:175], a[156:159], 0\n" \
"s_mul_i32 s7, s6, 0x2400\n" \
"s_sub_i32 s34, 0x22000, s7\n" \
"s_waitcnt vmcnt(5) lgkmcnt(0)\n" \
M_"a[124:127], a[236:239], a[220:223], a[124:127]\n" \
"s_barrier\n" \
M_"v[52:55], a[176:179], a[128:131], 0\n" \
"s_add_u32 m0, s30, 0x0\n" \
"s_movk_i32 s84, 0x100\n" \
M_"v[52:55], a[240:243], a[192:195], v[52:55]\n" \
B_"v22, s[64:67], s84 offen offset:0 lds\n" \
"s_nop 5\n" \
M_"v[56:59], a[180:183], a[128:131], 0\n" \
X_"a[160:161], v32 offset:32768\n" \
"v_bitop3_b32 v216, v217, v218, v216 bitop3:0x1e\n" \
M_"v[56:59], a[244:247], a[192:195], v[56:59]\n" \
"s_movk_i32 s84, 0x1d00\n" \
"s_nop 5\n" \
M_"v[60:63], a[184:187], a[128:131], 0\n" \
B_"v23, s[64:67], s84 offen offset:1024 lds\n" \
M_"v[60:63], a[248:251], a[192:195], v[60:63]\n" \
"s_nop 5\n" \
X_"a[162:163], v32 offset:33792\n" \
M_"v[64:67], a[188:191], a[128:131], 0\n" \
X_"a[224:225], v32 offset:40960\n" \
"v_or_b32_e32 v213, 0x24400, v20\n" \
M_"v[64:67], a[252:255], a[192:195], v[64:67]\n" \
R_"a[128:131], v28 offset:49152\n" \
"s_nop 5\n" \
M_"v[80:83], a[188:191], a[132:135], 0\n" \
R_"a[192:195], v29 offset:49152\n" \
"v_lshlrev_b32_e32 v216, 4, v216\n" \
M_"v[80:83], a[252:255], a[196:199], v[80:83]\n" \
"s_movk_i32 s84, 0x3900\n" \
"s_nop 5\n" \
M_"v[76:79], a[184:187], a[132:135], 0\n" \
B_"v22, s[64:67], s84 offen offset:2048 lds\n" \
M_"v[76:79], a[248:251], a[196:199], v[76:79]\n" \
"s_movk_i32 s84, 0x5500\n" \
"s_nop 5\n" \
M_"v[72:75], a[180:183], a[132:135], 0\n" \
B_"v23, s[64:67], s84 offen offset:3072 lds\n" \
"s_mov_b64 exec, 0\n" \
"s_swappc_b64 s[88:89], s[86:87]\n" \
"s_mov_b64 exec, -1\n" \
"s_andn2_b32 s85, 2, s3\n" \
"s_lshl_b32 s85, s85, 12\n" \
"s_add_u32 s86, s86, s85\n" \
"s_addc_u32 s87, s87, 0\n" \
M_"v[72:75], a[244:247], a[196:199], v[72:75]\n" \
"s_add_u32 m0, s31, 0x0\n" \
"s_mov_b32 s84, 0x100000\n" \
"s_nop 5\n" \
M_"v[68:71], a[176:179], a[132:135], 0\n" \
B_"v26, s[68:71], s84 offen offset:0 lds\n" \
M_"v[68:71], a[240:243], a[196:199], v[68:71]\n" \
R_"a[132:135], v28 offset:51200\n" \
"s_nop 5\n" \
M_"v[84:87], a[176:179], a[136:139], 0\n" \
R_"a[196:199], v29 offset:51200\n" \
"v_add3_u32 v196, v220, s34, v216\n" \
M_"v[84:87], a[240:243], a[200:203], v[84:87]\n" \
"s_addk_i32 s84, 0x7c00\n" \
"s_nop 5\n" \
M_"v[88:91], a[180:183], a[136:139], 0\n" \
B_"v26, s[68:71], s84 offen offset:1024 lds\n" \
M_"v[88:91], a[244:247], a[200:203], v[88:91]\n" \
"s_nop 5\n" \
X_"a[226:227], v32 offset:41984\n" \
M_"v[92:95], a[184:187], a[136:139], 0\n" \
"s_addk_i32 s84, 0x7c00\n" \
M_"v[92:95], a[248:251], a[200:203], v[92:95]\n" \
B_"v27, s[68:71], s84 offen offset:2048 lds\n" \
"s_nop 5\n" \
M_"v[96:99], a[188:191], a[136:139], 0\n" \
X_"a[164:165], v33 offset:32768\n" \
"v_mov_b32_e32 v216, v213\n" \
M_"v[96:99], a[252:255], a[200:203], v[96:99]\n" \
R_"a[136:139], v28 offset:53248\n" \
"s_nop 5\n" \
M_"v[112:115], a[188:191], a[140:143], 0\n" \
R_"a[200:203], v29 offset:53248\n" \
"s_movk_i32 s36, 0x1b0\n" \
M_"v[112:115], a[252:255], a[204:207], v[112:115]\n" \
"s_addk_i32 s84, 0x7c00\n" \
"s_nop 5\n" \
M_"v[108:111], a[184:187], a[140:143], 0\n" \
B_"v27, s[68:71], s84 offen offset:3072 lds\n" \
M_"v[108:111], a[248:251], a[204:207], v[108:111]\n" \
"s_add_u32 m0, s30, 0x1000\n" \
"s_mov_b32 s84, 0x8100\n" \
"s_nop 5\n" \
M_"v[104:107], a[180:183], a[140:143], 0\n" \
B_"v22, s[64:67], s84 offen offset:0 lds\n" \
M_"v[104:107], a[244:247], a[204:207], v[104:107]\n" \
"s_nop 5\n" \
X_"a[166:167], v33 offset:33792\n" \
M_"v[100:103], a[176:179], a[140:143], 0\n" \
X_"a[228:229], v33 offset:40960\n" \
"v_or_b32_e32 v216, 0x100, v215\n" \
M_"v[100:103], a[240:243], a[204:207], v[100:103]\n" \
R_"a[140:143], v28 offset:55296\n" \
"s_nop 5\n" \
M_"v[116:119], a[176:179], a[144:147], 0\n" \
R_"a[204:207], v29 offset:55296\n" \
"v_mul_u32_u24_e32 v217, 0x2aab, v216\n" \
M_"v[116:119], a[240:243], a[208:211], v[116:119]\n" \
"s_addk_i32 s84, 0x1c00\n" \
"s_nop 5\n" \
M_"v[120:123], a[180:183], a[144:147], 0\n" \
B_"v23, s[64:67], s84 offen offset:1024 lds\n" \
M_"v[120:123], a[244:247], a[208:211], v[120:123]\n" \
"s_addk_i32 s84, 0x1c00\n" \
"s_nop 5\n" \
M_"v[124:127], a[184:187], a[144:147], 0\n" \
B_"v22, s[64:67], s84 offen offset:2048 lds\n" \
"s_mov_b64 exec, 0\n" \
"s_swappc_b64 s[88:89], s[86:87]\n" \
"s_mov_b64 exec, -1\n" \
M_"v[124:127], a[248:251], a[208:211], v[124:127]\n" \
".fill (L_pb + 3808 - .) / 4, 4, 0xBF800000\n" \
"L_pp1:\n" \
"s_cbranch_execz L_pret\n" \
"s_addk_i32 s84, 0x1c00\n" \
"s_nop 5\n" \
M_"v[128:131], a[188:191], a[144:147], 0\n" \
B_"v23, s[64:67], s84 offen offset:3072 lds\n" \
M_"v[128:131], a[252:255], a[208:211], v[128:131]\n" \
R_"a[144:147], v28 offset:32768\n" \
"s_nop 5\n" \
M_"v[144:147], a[188:191], a[148:151], 0\n" \
R_"a[208:211], v29 offset:32768\n" \
"v_add_u32_e32 v215, 0xffffff50, v215\n" \
M_"v[144:147], a[252:255], a[212:215], v[144:147]\n" \
"s_add_u32 m0, s31, 0x4000\n" \
"s_mov_b32 s84, 0x100000\n" \
"s_nop 5\n" \
M_"v[140:143], a[184:187], a[148:151], 0\n" \
B_"v26, s[72:75], s84 offen offset:0 lds\n" \
M_"v[140:143], a[248:251], a[212:215], v[140:143]\n" \
"s_nop 5\n" \
X_"a[230:231], v33 offset:41984\n" \
M_"v[136:139], a[180:183], a[148:151], 0\n" \
"s_addk_i32 s84, 0x7c00\n" \
M_"v[136:139], a[244:247], a[212:215], v[136:139]\n" \
B_"v26, s[72:75], s84 offen offset:1024 lds\n" \
"s_nop 5\n" \
M_"v[132:135], a[176:179], a[148:151], 0\n" \
X_"a[168:169], v34 offset:32768\n" \
"v_lshrrev_b32_e64 v217, 16, v217\n" \
M_"v[132:135], a[240:243], a[212:215], v[132:135]\n" \
R_"a[148:151], v28 offset:34816\n" \
"s_nop 5\n" \
M_"v[148:151], a[176:179], a[152:155], 0\n" \
R_"a[212:215], v29 offset:34816\n" \
"v_mov_b32_e32 v220, v215\n" \
M_"v[148:151], a[240:243], a[216:219], v[148:151]\n" \
"s_nop 5\n" \
X_"a[170:171], v34 offset:33792\n" \
M_"v[152:155], a[180:183], a[152:155], 0\n" \
X_"a[232:233], v34 offset:40960\n" \
"v_mad_i32_i24 v218, v217, -6, v216\n" \
M_"v[152:155], a[244:247], a[216:219], v[152:155]\n" \
"s_nop 5\n" \
X_"a[234:235], v34 offset:41984\n" \
M_"v[156:159], a[184:187], a[152:155], 0\n" \
X_"a[172:173], v35 offset:32768\n" \
"v_cmp_gt_u32_e64 vcc, s36, v216\n" \
M_"v[156:159], a[248:251], a[216:219], v[156:159]\n" \
"s_nop 5\n" \
X_"a[174:175], v35 offset:33792\n" \
M_"v[160:163], a[188:191], a[152:155], 0\n" \
X_"a[236:237], v35 offset:40960\n" \
"v_mov_b32_e32 v221, 7\n" \
M_"v[160:163], a[252:255], a[216:219], v[160:163]\n" \
R_"a[152:155], v28 offset:36864\n" \
"s_nop 5\n" \
M_"v[176:179], a[188:191], a[156:159], 0\n" \
R_"a[216:219], v29 offset:36864\n" \
"v_lshrrev_b32_e32 v222, 1, v218\n" \
M_"v[176:179], a[252:255], a[220:223], v[176:179]\n" \
"s_nop 5\n" \
X_"a[238:239], v35 offset:41984\n" \
M_"v[172:175], a[184:187], a[156:159], 0\n" \
"v_cndmask_b32_e32 v217, v220, v217, vcc\n" \
"s_movk_i32 s4, 0x48\n" \
M_"v[172:175], a[248:251], a[220:223], v[172:175]\n" \
"s_nop 5\n" \
"v_add_u32_e64 v220, s35, v217\n" \
M_"v[168:171], a[180:183], a[156:159], 0\n" \
"v_mul_u32_u24_e32 v222, 0x480, v222\n" \
"v_mad_u32_u24 v222, v220, s5, v222\n" \
M_"v[168:171], a[244:247], a[220:223], v[168:171]\n" \
"s_nop 5\n" \
"v_cmp_gt_u32_e64 s[4:5], s4, v215\n" \
M_"v[164:167], a[176:179], a[156:159], 0\n" \
"v_cndmask_b32_e32 v215, v221, v218, vcc\n" \
"v_lshrrev_b32_e64 v218, 1, v220\n" \
M_"v[164:167], a[240:243], a[220:223], v[164:167]\n" \
R_"a[156:159], v28 offset:38912\n" \
R_"a[220:223], v29 offset:38912\n" \
"s_waitcnt lgkmcnt(15)\n" \
M_"a[0:3], a[160:163], a[128:131], a[0:3]\n" \
"s_addk_i32 s84, 0x7c00\n" \
M_"a[0:3], a[224:227], a[192:195], a[0:3]\n" \
B_"v27, s[72:75], s84 offen offset:2048 lds\n" \
M_"a[4:7], a[164:167], a[128:131], a[4:7]\n" \
X_"a[176:177], v36 offset:32768\n" \
"v_or_b32_e32 v214, v222, v214\n" \
"s_waitcnt lgkmcnt(15)\n" \
M_"a[4:7], a[228:231], a[192:195], a[4:7]\n" \
"s_addk_i32 s84, 0x7c00\n" \
"s_waitcnt lgkmcnt(11)\n" \
M_"a[8:11], a[168:171], a[128:131], a[8:11]\n" \
B_"v27, s[72:75], s84 offen offset:3072 lds\n" \
"s_waitcnt lgkmcnt(9)\n" \
M_"a[8:11], a[232:235], a[192:195], a[8:11]\n" \
X_"a[178:179], v36 offset:33792\n" \
"v_mov_b32_e32 v222, 2.0\n" \
"s_waitcnt lgkmcnt(8)\n" \
M_"a[12:15], a[172:175], a[128:131], a[12:15]\n" \
X_"a[240:241], v36 offset:40960\n" \
"v_and_b32_e32 v218, 4, v218\n" \
"s_waitcnt lgkmcnt(5)\n" \
M_"a[12:15], a[236:239], a[192:195], a[12:15]\n" \
"v_and_b32_e32 v217, 2, v217\n" \
"v_bitop3_b32 v215, v215, v218, v217 bitop3:0x1e\n" \
M_"a[28:31], a[172:175], a[132:135], a[28:31]\n" \
"v_lshlrev_b32_e32 v223, 1, v220\n" \
"v_mov_b32_e32 v224, 2.0\n" \
M_"a[28:31], a[236:239], a[196:199], a[28:31]\n" \
X_"a[242:243], v36 offset:41984\n" \
"v_cndmask_b32_e64 v193, v222, v214, vcc\n" \
M_"a[24:27], a[168:171], a[132:135], a[24:27]\n" \
"v_lshlrev_b32_e32 v214, 7, v220\n" \
"v_lshlrev_b32_e32 v215, 4, v215\n" \
M_"a[24:27], a[232:235], a[196:199], a[24:27]\n" \
"s_mul_i32 s39, s3, 18\n" \
"v_add3_u32 v214, v214, s34, v215\n" \
M_"a[20:23], a[164:167], a[132:135], a[20:23]\n" \
"v_cndmask_b32_e64 v195, v224, v223, s[4:5]\n" \
"s_movk_i32 s4, 0x1f8\n" \
M_"a[20:23], a[228:231], a[196:199], a[20:23]\n" \
X_"a[180:181], v37 offset:32768\n" \
"v_mov_b32_e64 v215, v213\n" \
M_"a[16:19], a[160:163], a[132:135], a[16:19]\n" \
"s_add_i32 s35, s35, s39\n" \
"v_cmp_gt_u32_e32 vcc, s4, v216\n" \
M_"a[16:19], a[224:227], a[196:199], a[16:19]\n" \
"v_add_u32_e32 v212, s35, v212\n" \
"v_mov_b32_e32 v219, 2.0\n" \
M_"a[32:35], a[160:163], a[136:139], a[32:35]\n" \
"s_mov_b32 m0, s32\n" \
X_"a[182:183], v37 offset:33792\n" \
M_"a[32:35], a[224:227], a[200:203], a[32:35]\n" \
"s_mov_b32 exec_lo, 0x0\n" \
"s_mov_b32 exec_hi, 0xff\n" \
"global_load_lds_dword v[184:185], off\n" \
"s_mov_b64 exec, -1\n" \
M_"a[36:39], a[164:167], a[136:139], a[36:39]\n" \
"v_cndmask_b32_e32 v197, v215, v214, vcc\n" \
"s_movk_i32 s35, 0x120\n" \
M_"a[36:39], a[228:231], a[200:203], a[36:39]\n" \
X_"a[244:245], v37 offset:40960\n" \
"v_mul_u32_u24_e32 v214, 0xd80, v212\n" \
M_"a[40:43], a[168:171], a[136:139], a[40:43]\n" \
"v_cmp_gt_u32_e32 vcc, s35, v20\n" \
"v_lshrrev_b32_e64 v215, 1, v212\n" \
M_"a[40:43], a[232:235], a[200:203], a[40:43]\n" \
"s_mov_b32 m0, s32\n" \
"s_and_b32 s4, s2, 7\n" \
M_"a[44:47], a[172:175], a[136:139], a[44:47]\n" \
"s_mov_b32 exec_lo, 0xffff0000\n" \
"s_mov_b32 exec_hi, 0x0\n" \
"global_load_lds_dword v[186:187], off\n" \
"s_mov_b64 exec, -1\n" \
M_"a[44:47], a[236:239], a[200:203], a[44:47]\n" \
X_"a[246:247], v37 offset:41984\n" \
"v_cndmask_b32_e64 v194, v219, v214, vcc\n" \
M_"a[60:63], a[172:175], a[140:143], a[60:63]\n" \
"v_lshlrev_b32_e32 v214, 7, v212\n" \
"v_lshrrev_b32_e32 v212, 2, v212\n" \
M_"a[60:63], a[236:239], a[204:207], a[60:63]\n" \
"v_and_b32_e64 v212, 2, v212\n" \
"v_and_or_b32 v212, v215, 1, v212\n" \
M_"a[56:59], a[168:171], a[140:143], a[56:59]\n" \
X_"a[184:185], v38 offset:32768\n" \
"s_lshl_b32 s5, s4, 2\n" \
M_"a[56:59], a[232:235], a[204:207], a[56:59]\n" \
"s_bfe_u32 s38, s2, 0x20003\n" \
"v_lshlrev_b32_e32 v212, 5, v212\n" \
M_"a[52:55], a[164:167], a[140:143], a[52:55]\n" \
"s_movk_i32 s35, 0x60\n" \
"s_or_b32 s5, s5, s38\n" \
M_"a[52:55], a[228:231], a[204:207], a[52:55]\n" \
X_"a[186:187], v38 offset:33792\n" \
"v_bitop3_b32 v212, v212, s35, v214 bitop3:0x36\n" \
M_"a[48:51], a[160:163], a[140:143], a[48:51]\n" \
"s_lshr_b32 s36, s2, 3\n" \
"v_add_u32_e32 v212, s34, v212\n" \
M_"a[48:51], a[224:227], a[204:207], a[48:51]\n" \
"s_mul_i32 s34, s5, 0x12000\n" \
"s_or_b32 s40, s34, 0x6000000\n" \
M_"a[64:67], a[160:163], a[144:147], a[64:67]\n" \
X_"a[248:249], v38 offset:40960\n" \
"s_mul_i32 s34, s36, 0x90000\n" \
M_"a[64:67], a[224:227], a[208:211], a[64:67]\n" \
"s_lshl_b32 s36, s3, 14\n" \
"s_add_i32 s34, s34, s36\n" \
M_"a[68:71], a[164:167], a[144:147], a[68:71]\n" \
"s_lshl_b32 s38, s5, 8\n" \
"s_add_i32 s41, s34, 0x4240000\n" \
M_"a[68:71], a[228:231], a[208:211], a[68:71]\n" \
X_"a[250:251], v38 offset:41984\n" \
"s_mul_i32 s34, s5, 36\n" \
M_"a[72:75], a[168:171], a[144:147], a[72:75]\n" \
"s_add_i32 s5, s7, s40\n" \
"s_addk_i32 s5, 0x2000\n" \
M_"a[72:75], a[232:235], a[208:211], a[72:75]\n" \
"s_mov_b32 s37, 2.0\n" \
"s_or_b32 s38, s38, 0x5be8000\n" \
M_"a[76:79], a[172:175], a[144:147], a[76:79]\n" \
"s_cmp_lg_u32 s3, 0\n" \
"s_cselect_b32 s36, s37, s5\n" \
M_"a[76:79], a[236:239], a[208:211], a[76:79]\n" \
X_"a[188:189], v39 offset:32768\n" \
"s_lshl_b32 s5, s6, 2\n" \
M_"a[92:95], a[172:175], a[148:151], a[92:95]\n" \
"s_or_b32 s5, s38, s5\n" \
"s_lshl_b32 s35, s3, 10\n" \
M_"a[92:95], a[236:239], a[212:215], a[92:95]\n" \
"s_cmpk_lt_u32 s3, 3\n" \
"s_cselect_b32 s37, s37, s5\n" \
M_"a[88:91], a[168:171], a[148:151], a[88:91]\n" \
X_"a[190:191], v39 offset:33792\n" \
"s_lshl_b32 s5, s4, 16\n" \
M_"a[88:91], a[232:235], a[212:215], a[88:91]\n" \
"s_lshl_b32 s4, s4, 11\n" \
"v_cndmask_b32_e64 v198, v213, v212, vcc\n" \
M_"a[84:87], a[164:167], a[148:151], a[84:87]\n" \
"v_or_b32_e32 v212, s35, v20\n" \
"s_or_b32 s39, s40, s35\n" \
M_"a[84:87], a[228:231], a[212:215], a[84:87]\n" \
X_"a[252:253], v39 offset:40960\n" \
"s_or_b32 s55, s41, s4\n" \
M_"a[80:83], a[160:163], a[148:151], a[80:83]\n" \
"v_or_b32_e32 v199, 0x22000, v212\n" \
"s_add_i32 s35, s39, s7\n" \
M_"a[80:83], a[224:227], a[212:215], a[80:83]\n" \
"s_add_i32 s54, s41, s5\n" \
"s_add_i32 s55, s55, 0x80000\n" \
M_"a[96:99], a[160:163], a[152:155], a[96:99]\n" \
X_"a[254:255], v39 offset:41984\n" \
"v_mov_b32_e32 v215, 0xffff\n" \
M_"a[96:99], a[224:227], a[216:219], a[96:99]\n" \
"s_and_b32 s4, s3, 1\n" \
"s_lshl_b32 s5, s4, 4\n" \
M_"a[100:103], a[164:167], a[152:155], a[100:103]\n" \
"v_lshrrev_b32_e32 v212, 4, v20\n" \
"v_lshrrev_b32_e64 v218, 6, v20\n" \
M_"a[100:103], a[228:231], a[216:219], a[100:103]\n" \
"v_and_b32_e64 v219, 12, v218\n" \
"v_and_or_b32 v214, v212, 15, s5\n" \
M_"a[104:107], a[168:171], a[152:155], a[104:107]\n" \
"v_cmp_gt_u32_e64 vcc, v219, v214\n" \
"v_mov_b32_e32 v220, 0xffff0000\n" \
M_"a[104:107], a[232:235], a[216:219], a[104:107]\n" \
"s_lshl_b32 s5, s2, 2\n" \
"v_cndmask_b32_e64 v217, v215, 0, vcc\n" \
M_"a[108:111], a[172:175], a[152:155], a[108:111]\n" \
"v_cmp_lt_u32_e32 vcc, v219, v214\n" \
"v_mov_b32_e64 v213, s23\n" \
M_"a[108:111], a[236:239], a[216:219], a[108:111]\n" \
"s_or_b32 s5, s5, s3\n" \
"v_cndmask_b32_e32 v221, 0, v220, vcc\n" \
"s_waitcnt lgkmcnt(15)\n" \
M_"a[124:127], a[172:175], a[156:159], a[124:127]\n" \
"v_or_b32_e32 v12, v217, v221\n" \
"v_or_b32_e64 v217, 2, v219\n" \
M_"a[124:127], a[236:239], a[220:223], a[124:127]\n" \
"v_cmp_gt_u32_e32 vcc, v217, v214\n" \
"v_mov_b32_e32 v216, s25\n" \
M_"a[120:123], a[168:171], a[156:159], a[120:123]\n" \
"s_movk_i32 s40, 0xf400\n" \
"v_cndmask_b32_e64 v221, v215, 0, vcc\n" \
M_"a[120:123], a[232:235], a[220:223], a[120:123]\n" \
"v_cmp_lt_u32_e32 vcc, v217, v214\n" \
"s_lshl_b32 s4, s4, 6\n" \
M_"a[116:119], a[164:167], a[156:159], a[116:119]\n" \
"v_cndmask_b32_e32 v217, 0, v220, vcc\n" \
"v_or_b32_e64 v13, v221, v217\n" \
M_"a[116:119], a[228:231], a[220:223], a[116:119]\n" \
"v_or_b32_e32 v217, 16, v219\n" \
"v_cmp_gt_u32_e32 vcc, v217, v214\n" \
M_"a[112:115], a[160:163], a[156:159], a[112:115]\n" \
"s_nop 0\n" \
"v_cndmask_b32_e64 v221, v215, 0, vcc\n" \
"v_cmp_lt_u32_e32 vcc, v217, v214\n" \
M_"a[112:115], a[224:227], a[220:223], a[112:115]\n" \
"s_waitcnt vmcnt(20)\n" \
"v_readfirstlane_b32 s62, v180\n" \
"s_nop 3\n" \
"s_add_u32 s62, s62, 0x9E3779B1\n" \
"s_waitcnt vmcnt(2) lgkmcnt(0)\n" \
"s_barrier\n" \
M_"v[52:55], a[176:179], a[128:131], v[52:55]\n" \
"s_add_u32 m0, s30, 0x8000\n" \
"s_movk_i32 s84, 0x180\n" \
M_"v[52:55], a[240:243], a[192:195], v[52:55]\n" \
B_"v22, s[64:67], s84 offen offset:0 lds\n" \
M_"v[56:59], a[180:183], a[128:131], v[56:59]\n" \
X_"a[160:161], v32 offset:0\n" \
"v_cndmask_b32_e64 v217, 0, v220, vcc\n" \
M_"v[56:59], a[244:247], a[192:195], v[56:59]\n" \
"s_movk_i32 s84, 0x1d80\n" \
M_"v[60:63], a[184:187], a[128:131], v[60:63]\n" \
B_"v23, s[64:67], s84 offen offset:1024 lds\n" \
M_"v[60:63], a[248:251], a[192:195], v[60:63]\n" \
X_"a[162:163], v32 offset:1024\n" \
"v_or_b32_e32 v14, v221, v217\n" \
M_"v[64:67], a[188:191], a[128:131], v[64:67]\n" \
X_"a[224:225], v32 offset:8192\n" \
"v_or_b32_e64 v217, 18, v219\n" \
M_"v[64:67], a[252:255], a[192:195], v[64:67]\n" \
R_"a[128:131], v28 offset:16384\n" \
"v_cmp_gt_u32_e64 vcc, v217, v214\n" \
M_"v[80:83], a[188:191], a[132:135], v[80:83]\n" \
R_"a[192:195], v29 offset:16384\n" \
"v_cndmask_b32_e64 v215, v215, 0, vcc\n" \
M_"v[80:83], a[252:255], a[196:199], v[80:83]\n" \
"s_movk_i32 s84, 0x3980\n" \
M_"v[76:79], a[184:187], a[132:135], v[76:79]\n" \
B_"v22, s[64:67], s84 offen offset:2048 lds\n" \
M_"v[76:79], a[248:251], a[196:199], v[76:79]\n" \
"s_movk_i32 s84, 0x5580\n" \
M_"v[72:75], a[180:183], a[132:135], v[72:75]\n" \
B_"v23, s[64:67], s84 offen offset:3072 lds\n" \
M_"v[72:75], a[244:247], a[196:199], v[72:75]\n" \
"s_add_u32 m0, s31, 0x8000\n" \
"s_mov_b32 s84, 0x180000\n" \
M_"v[68:71], a[176:179], a[132:135], v[68:71]\n" \
B_"v26, s[68:71], s84 offen offset:0 lds\n" \
M_"v[68:71], a[240:243], a[196:199], v[68:71]\n" \
R_"a[132:135], v28 offset:18432\n" \
"v_cmp_lt_u32_e64 vcc, v217, v214\n" \
M_"v[84:87], a[176:179], a[136:139], v[84:87]\n" \
R_"a[196:199], v29 offset:18432\n" \
"v_cndmask_b32_e64 v214, 0, v220, vcc\n" \
M_"v[84:87], a[240:243], a[200:203], v[84:87]\n" \
"s_addk_i32 s84, 0x7c00\n" \
M_"v[88:91], a[180:183], a[136:139], v[88:91]\n" \
B_"v26, s[68:71], s84 offen offset:1024 lds\n" \
M_"v[88:91], a[244:247], a[200:203], v[88:91]\n" \
X_"a[226:227], v32 offset:9216\n" \
"v_or_b32_e32 v15, v215, v214\n" \
M_"v[92:95], a[184:187], a[136:139], v[92:95]\n" \
"s_addk_i32 s84, 0x7c00\n" \
M_"v[92:95], a[248:251], a[200:203], v[92:95]\n" \
B_"v27, s[68:71], s84 offen offset:2048 lds\n" \
M_"v[96:99], a[188:191], a[136:139], v[96:99]\n" \
X_"a[164:165], v33 offset:0\n" \
"v_mad_u64_u32 v[214:215], s[6:7], s5, 42, v[212:213]\n" \
M_"v[96:99], a[252:255], a[200:203], v[96:99]\n" \
R_"a[136:139], v28 offset:20480\n" \
"s_movk_i32 s5, 0x1440\n" \
M_"v[112:115], a[188:191], a[140:143], v[112:115]\n" \
R_"a[200:203], v29 offset:20480\n" \
"v_cmp_gt_u32_e64 vcc, s5, v214\n" \
M_"v[112:115], a[252:255], a[204:207], v[112:115]\n" \
"s_addk_i32 s84, 0x7c00\n" \
"s_nop 0\n" \
M_"v[108:111], a[184:187], a[140:143], v[108:111]\n" \
B_"v27, s[68:71], s84 offen offset:3072 lds\n" \
M_"v[108:111], a[248:251], a[204:207], v[108:111]\n" \
"s_add_u32 m0, s30, 0x9000\n" \
"s_mov_b32 s84, 0x8180\n" \
M_"v[104:107], a[180:183], a[140:143], v[104:107]\n" \
B_"v22, s[64:67], s84 offen offset:0 lds\n" \
M_"v[104:107], a[244:247], a[204:207], v[104:107]\n" \
X_"a[166:167], v33 offset:1024\n" \
"v_min_u32_e32 v217, 0xa43f, v214\n" \
M_"v[100:103], a[176:179], a[140:143], v[100:103]\n" \
X_"a[228:229], v33 offset:8192\n" \
"v_mov_b32_e64 v214, s24\n" \
M_"v[100:103], a[240:243], a[204:207], v[100:103]\n" \
R_"a[140:143], v28 offset:22528\n" \
"v_cndmask_b32_e64 v215, v216, v213, vcc\n" \
M_"v[116:119], a[176:179], a[144:147], v[116:119]\n" \
R_"a[204:207], v29 offset:22528\n" \
"v_mov_b32_e64 v213, s22\n" \
M_"v[116:119], a[240:243], a[208:211], v[116:119]\n" \
"s_addk_i32 s84, 0x1c00\n" \
M_"v[120:123], a[180:183], a[144:147], v[120:123]\n" \
B_"v23, s[64:67], s84 offen offset:1024 lds\n" \
M_"v[120:123], a[244:247], a[208:211], v[120:123]\n" \
"s_addk_i32 s84, 0x1c00\n" \
M_"v[124:127], a[184:187], a[144:147], v[124:127]\n" \
B_"v22, s[64:67], s84 offen offset:2048 lds\n" \
M_"v[124:127], a[248:251], a[208:211], v[124:127]\n" \
"s_addk_i32 s84, 0x1c00\n" \
M_"v[128:131], a[188:191], a[144:147], v[128:131]\n" \
B_"v23, s[64:67], s84 offen offset:3072 lds\n" \
M_"v[128:131], a[252:255], a[208:211], v[128:131]\n" \
R_"a[144:147], v28 offset:0\n" \
"s_movk_i32 s6, 0xffee\n" \
M_"v[144:147], a[188:191], a[148:151], v[144:147]\n" \
R_"a[208:211], v29 offset:0\n" \
"v_cndmask_b32_e64 v214, v214, v213, vcc\n" \
M_"v[144:147], a[252:255], a[212:215], v[144:147]\n" \
"s_add_u32 m0, s31, 0xc000\n" \
"s_mov_b32 s84, 0x180000\n" \
M_"v[140:143], a[184:187], a[148:151], v[140:143]\n" \
B_"v26, s[72:75], s84 offen offset:0 lds\n" \
M_"v[140:143], a[248:251], a[212:215], v[140:143]\n" \
X_"a[230:231], v33 offset:9216\n" \
"v_add_u32_e32 v213, 0xffffebc0, v217\n" \
M_"v[136:139], a[180:183], a[148:151], v[136:139]\n" \
"s_addk_i32 s84, 0x7c00\n" \
M_"v[136:139], a[244:247], a[212:215], v[136:139]\n" \
B_"v26, s[72:75], s84 offen offset:1024 lds\n" \
M_"v[132:135], a[176:179], a[148:151], v[132:135]\n" \
X_"a[168:169], v34 offset:0\n" \
"s_lshl_b32 s5, s2, 5\n" \
M_"v[132:135], a[240:243], a[212:215], v[132:135]\n" \
R_"a[148:151], v28 offset:2048\n" \
"v_cndmask_b32_e64 v213, v213, v217, vcc\n" \
M_"v[148:151], a[176:179], a[152:155], v[148:151]\n" \
R_"a[212:215], v29 offset:2048\n" \
"v_lshlrev_b32_e64 v216, 7, v213\n" \
M_"v[148:151], a[240:243], a[216:219], v[148:151]\n" \
X_"a[170:171], v34 offset:1024\n" \
"v_mul_u32_u24_e32 v213, 0xe39, v212\n" \
M_"v[152:155], a[180:183], a[152:155], v[152:155]\n" \
X_"a[232:233], v34 offset:8192\n" \
"v_mov_b32_e64 v217, 0\n" \
M_"v[152:155], a[244:247], a[216:219], v[152:155]\n" \
X_"a[234:235], v34 offset:9216\n" \
"v_lshrrev_b32_e64 v213, 16, v213\n" \
M_"v[156:159], a[184:187], a[152:155], v[156:159]\n" \
X_"a[172:173], v35 offset:0\n" \
"v_lshl_add_u64 v[16:17], v[214:215], 0, v[216:217]\n" \
M_"v[156:159], a[248:251], a[216:219], v[156:159]\n" \
X_"a[174:175], v35 offset:1024\n" \
"v_mad_i32_i24 v214, v213, s6, v212\n" \
M_"v[160:163], a[188:191], a[152:155], v[160:163]\n" \
X_"a[236:237], v35 offset:8192\n" \
"s_add_i32 s5, s5, 32\n" \
M_"v[160:163], a[252:255], a[216:219], v[160:163]\n" \
R_"a[152:155], v28 offset:4096\n" \
"v_lshlrev_b32_e32 v215, 4, v214\n" \
M_"v[176:179], a[188:191], a[156:159], v[176:179]\n" \
R_"a[216:219], v29 offset:4096\n" \
"v_add_u32_e64 v216, s5, v215\n" \
M_"v[176:179], a[252:255], a[220:223], v[176:179]\n" \
X_"a[238:239], v35 offset:9216\n" \
"v_and_b32_e32 v216, 0xf0, v216\n" \
M_"v[172:175], a[184:187], a[156:159], v[172:175]\n" \
"v_cmp_gt_u32_e32 vcc, 16, v214\n" \
"s_movk_i32 s7, 0xf800\n" \
M_"v[172:175], a[248:251], a[220:223], v[172:175]\n" \
"v_cndmask_b32_e32 v214, v215, v216, vcc\n" \
"v_or_b32_e32 v215, 64, v212\n" \
M_"v[168:171], a[180:183], a[156:159], v[168:171]\n" \
"v_mul_u32_u24_e32 v216, 0xe39, v215\n" \
"v_lshrrev_b32_e64 v216, 16, v216\n" \
M_"v[168:171], a[244:247], a[220:223], v[168:171]\n" \
"v_mad_i32_i24 v215, v216, s6, v215\n" \
"v_lshlrev_b32_e64 v217, 4, v215\n" \
M_"v[164:167], a[176:179], a[156:159], v[164:167]\n" \
"v_add_u32_e64 v220, s5, v217\n" \
"v_and_b32_e32 v220, 0xf0, v220\n" \
M_"v[164:167], a[240:243], a[220:223], v[164:167]\n" \
R_"a[156:159], v28 offset:6144\n" \
R_"a[220:223], v29 offset:6144\n" \
"s_waitcnt lgkmcnt(15)\n" \
M_"a[0:3], a[160:163], a[128:131], a[0:3]\n" \
"s_addk_i32 s84, 0x7c00\n" \
M_"a[0:3], a[224:227], a[192:195], a[0:3]\n" \
B_"v27, s[72:75], s84 offen offset:2048 lds\n" \
M_"a[4:7], a[164:167], a[128:131], a[4:7]\n" \
X_"a[176:177], v36 offset:0\n" \
"v_cmp_gt_u32_e32 vcc, 16, v215\n" \
"s_waitcnt lgkmcnt(15)\n" \
M_"a[4:7], a[228:231], a[192:195], a[4:7]\n" \
"s_addk_i32 s84, 0x7c00\n" \
"s_waitcnt lgkmcnt(11)\n" \
M_"a[8:11], a[168:171], a[128:131], a[8:11]\n" \
B_"v27, s[72:75], s84 offen offset:3072 lds\n" \
"s_waitcnt lgkmcnt(9)\n" \
M_"a[8:11], a[232:235], a[192:195], a[8:11]\n" \
X_"a[178:179], v36 offset:1024\n" \
"v_cndmask_b32_e32 v215, v217, v220, vcc\n" \
"s_waitcnt lgkmcnt(8)\n" \
M_"a[12:15], a[172:175], a[128:131], a[12:15]\n" \
X_"a[240:241], v36 offset:8192\n" \
"v_or_b32_e32 v217, 0x80, v212\n" \
"s_waitcnt lgkmcnt(5)\n" \
M_"a[12:15], a[236:239], a[192:195], a[12:15]\n" \
"v_mul_u32_u24_e32 v220, 0xe39, v217\n" \
"v_lshrrev_b32_e64 v220, 16, v220\n" \
M_"a[28:31], a[172:175], a[132:135], a[28:31]\n" \
"v_mad_i32_i24 v217, v220, s6, v217\n" \
"v_lshlrev_b32_e64 v221, 4, v217\n" \
M_"a[28:31], a[236:239], a[196:199], a[28:31]\n" \
X_"a[242:243], v36 offset:9216\n" \
"v_add_u32_e64 v222, s5, v221\n" \
M_"a[24:27], a[168:171], a[132:135], a[24:27]\n" \
B_"v[200:203], v192, s[76:79], s34 offen offset:0\n" \
M_"a[24:27], a[232:235], a[196:199], a[24:27]\n" \
"v_and_b32_e32 v222, 0xf0, v222\n" \
"v_cmp_gt_u32_e64 vcc, 16, v217\n" \
M_"a[20:23], a[164:167], a[132:135], a[20:23]\n" \
"s_nop 0\n" \
"v_cndmask_b32_e32 v217, v221, v222, vcc\n" \
"v_or_b32_e32 v221, 0xc0, v212\n" \
M_"a[20:23], a[228:231], a[196:199], a[20:23]\n" \
X_"a[180:181], v37 offset:0\n" \
"v_mul_u32_u24_e32 v222, 0xe39, v221\n" \
M_"a[16:19], a[160:163], a[132:135], a[16:19]\n" \
"v_lshrrev_b32_e64 v222, 16, v222\n" \
"v_mad_i32_i24 v221, v222, s6, v221\n" \
M_"a[16:19], a[224:227], a[196:199], a[16:19]\n" \
"v_lshlrev_b32_e32 v223, 4, v221\n" \
"v_add_u32_e32 v224, s5, v223\n" \
M_"a[32:35], a[160:163], a[136:139], a[32:35]\n" \
B_"v[204:207], v193, s[76:79], s34 offen offset:0\n" \
M_"a[32:35], a[224:227], a[200:203], a[32:35]\n" \
"s_mov_b32 m0, s32\n" \
X_"a[182:183], v37 offset:1024\n" \
M_"a[36:39], a[164:167], a[136:139], a[36:39]\n" \
"s_mov_b32 exec_lo, 0x0\n" \
"s_mov_b32 exec_hi, 0xff00\n" \
"global_load_lds_dword v[184:185], off\n" \
"s_mov_b64 exec, -1\n" \
M_"a[36:39], a[228:231], a[200:203], a[36:39]\n" \
X_"a[244:245], v37 offset:8192\n" \
"v_and_b32_e32 v224, 0xf0, v224\n" \
M_"a[40:43], a[168:171], a[136:139], a[40:43]\n" \
"v_cmp_gt_u32_e32 vcc, 16, v221\n" \
"s_nop 1\n" \
"v_cndmask_b32_e32 v221, v223, v224, vcc\n" \
M_"a[40:43], a[232:235], a[200:203], a[40:43]\n" \
"buffer_load_dword v208, v194, s[76:79], s34 offen offset:32\n" \
M_"a[44:47], a[172:175], a[136:139], a[44:47]\n" \
X_"a[246:247], v37 offset:9216\n" \
"v_or_b32_e32 v223, 0x100, v212\n" \
M_"a[44:47], a[236:239], a[200:203], a[44:47]\n" \
"v_mul_u32_u24_e32 v224, 0xe39, v223\n" \
"v_lshrrev_b32_e64 v224, 16, v224\n" \
M_"a[60:63], a[172:175], a[140:143], a[60:63]\n" \
"v_mad_i32_i24 v223, v224, s6, v223\n" \
"v_lshlrev_b32_e64 v225, 4, v223\n" \
M_"a[60:63], a[236:239], a[204:207], a[60:63]\n" \
"v_add_u32_e64 v226, s5, v225\n" \
"v_and_b32_e32 v226, 0xf0, v226\n" \
M_"a[56:59], a[168:171], a[140:143], a[56:59]\n" \
"buffer_load_dword v209, v194, s[76:79], s34 offen offset:1184\n" \
M_"a[56:59], a[232:235], a[204:207], a[56:59]\n" \
X_"a[184:185], v38 offset:0\n" \
"v_cmp_gt_u32_e64 vcc, 16, v223\n" \
M_"a[52:55], a[164:167], a[140:143], a[52:55]\n" \
"s_nop 0\n" \
"v_cndmask_b32_e32 v223, v225, v226, vcc\n" \
"v_or_b32_e32 v225, 0x140, v212\n" \
M_"a[52:55], a[228:231], a[204:207], a[52:55]\n" \
X_"a[186:187], v38 offset:1024\n" \
"v_mul_u32_u24_e32 v226, 0xe39, v225\n" \
M_"a[48:51], a[160:163], a[140:143], a[48:51]\n" \
"v_lshrrev_b32_e64 v226, 16, v226\n" \
"v_mad_i32_i24 v225, v226, s6, v225\n" \
M_"a[48:51], a[224:227], a[204:207], a[48:51]\n" \
"v_lshlrev_b32_e32 v227, 4, v225\n" \
"v_add_u32_e32 v228, s5, v227\n" \
M_"a[64:67], a[160:163], a[144:147], a[64:67]\n" \
"buffer_load_dword v211, v194, s[76:79], s34 offen offset:2336\n" \
M_"a[64:67], a[224:227], a[208:211], a[64:67]\n" \
X_"a[248:249], v38 offset:8192\n" \
"v_and_b32_e32 v228, 0xf0, v228\n" \
M_"a[68:71], a[164:167], a[144:147], a[68:71]\n" \
"v_cmp_gt_u32_e32 vcc, 16, v225\n" \
"s_nop 1\n" \
"v_cndmask_b32_e64 v225, v227, v228, vcc\n" \
M_"a[68:71], a[228:231], a[208:211], a[68:71]\n" \
X_"a[250:251], v38 offset:9216\n" \
"v_or_b32_e32 v227, 0x180, v212\n" \
M_"a[72:75], a[168:171], a[144:147], a[72:75]\n" \
"v_mul_u32_u24_e32 v228, 0xe39, v227\n" \
"v_lshrrev_b32_e64 v228, 16, v228\n" \
M_"a[72:75], a[232:235], a[208:211], a[72:75]\n" \
"s_mov_b32 s76, s20\n" \
"s_and_b32 s77, s21, 0xffff\n" \
M_"a[76:79], a[172:175], a[144:147], a[76:79]\n" \
"s_mov_b32 s78, 0x480\n" \
"v_mad_i32_i24 v227, v228, s6, v227\n" \
M_"a[76:79], a[236:239], a[208:211], a[76:79]\n" \
"buffer_load_ushort v210, v195, s[76:79], 0 offen\n" \
M_"a[92:95], a[172:175], a[148:151], a[92:95]\n" \
X_"a[188:189], v39 offset:0\n" \
"v_lshlrev_b32_e32 v229, 4, v227\n" \
M_"a[92:95], a[236:239], a[212:215], a[92:95]\n" \
"v_add_u32_e64 v230, s5, v229\n" \
"v_and_b32_e32 v230, 0xf0, v230\n" \
M_"a[88:91], a[168:171], a[148:151], a[88:91]\n" \
"s_mov_b32 s76, s8\n" \
"s_and_b32 s77, s9, 0xffff\n" \
M_"a[88:91], a[232:235], a[212:215], a[88:91]\n" \
"s_mov_b32 s78, 0x240000\n" \
X_"a[190:191], v39 offset:1024\n" \
M_"a[84:87], a[164:167], a[148:151], a[84:87]\n" \
"v_cmp_gt_u32_e32 vcc, 16, v227\n" \
"s_nop 1\n" \
"v_cndmask_b32_e32 v227, v229, v230, vcc\n" \
M_"a[84:87], a[228:231], a[212:215], a[84:87]\n" \
X_"a[252:253], v39 offset:8192\n" \
"v_or_b32_e32 v229, 0x1c0, v212\n" \
".fill (L_pb + 7904 - .) / 4, 4, 0xBF800000\n" \
"L_pp2:\n" \
"s_cbranch_execz L_pret\n" \
M_"a[80:83], a[160:163], a[148:151], a[80:83]\n" \
"v_mul_u32_u24_e32 v230, 0xe39, v229\n" \
"v_lshrrev_b32_e32 v230, 16, v230\n" \
M_"a[80:83], a[224:227], a[212:215], a[80:83]\n" \
"v_mad_i32_i24 v229, v230, s6, v229\n" \
"v_lshlrev_b32_e64 v231, 4, v229\n" \
M_"a[96:99], a[160:163], a[152:155], a[96:99]\n" \
X_"a[254:255], v39 offset:9216\n" \
"v_add_u32_e64 v232, s5, v231\n" \
M_"a[96:99], a[224:227], a[216:219], a[96:99]\n" \
"v_and_b32_e32 v232, 0xf0, v232\n" \
"v_cmp_gt_u32_e64 vcc, 16, v229\n" \
M_"a[100:103], a[164:167], a[152:155], a[100:103]\n" \
"s_nop 0\n" \
"v_cndmask_b32_e32 v229, v231, v232, vcc\n" \
"v_or_b32_e32 v231, 0x200, v212\n" \
M_"a[100:103], a[228:231], a[216:219], a[100:103]\n" \
"v_mul_u32_u24_e32 v232, 0xe39, v231\n" \
"v_lshrrev_b32_e64 v232, 16, v232\n" \
M_"a[104:107], a[168:171], a[152:155], a[104:107]\n" \
"v_mad_i32_i24 v231, v232, s6, v231\n" \
"v_lshlrev_b32_e64 v233, 4, v231\n" \
M_"a[104:107], a[232:235], a[216:219], a[104:107]\n" \
"v_add_u32_e32 v234, s5, v233\n" \
"s_movk_i32 s5, 0x480\n" \
M_"a[108:111], a[172:175], a[152:155], a[108:111]\n" \
"v_mad_u32_u24 v0, v213, s5, v214\n" \
"v_mul_u32_u24_e32 v213, 0x480, v216\n" \
M_"a[108:111], a[236:239], a[216:219], a[108:111]\n" \
"s_movk_i32 s6, 0xfc00\n" \
"v_add3_u32 v1, v213, v215, s6\n" \
"s_waitcnt lgkmcnt(15)\n" \
M_"a[124:127], a[172:175], a[156:159], a[124:127]\n" \
"v_mul_u32_u24_e32 v213, 0x480, v220\n" \
"v_add3_u32 v2, v213, v217, s7\n" \
M_"a[124:127], a[236:239], a[220:223], a[124:127]\n" \
"v_mul_u32_u24_e32 v213, 0x480, v222\n" \
"v_add3_u32 v3, v213, v221, s40\n" \
M_"a[120:123], a[168:171], a[156:159], a[120:123]\n" \
"v_mul_u32_u24_e32 v213, 0x480, v226\n" \
"v_add3_u32 v5, v213, v225, s6\n" \
M_"a[120:123], a[232:235], a[220:223], a[120:123]\n" \
"v_mul_u32_u24_e32 v213, 0x480, v228\n" \
"v_add3_u32 v6, v213, v227, s7\n" \
M_"a[116:119], a[164:167], a[156:159], a[116:119]\n" \
"v_mul_u32_u24_e32 v213, 0x480, v230\n" \
"v_add3_u32 v7, v213, v229, s40\n" \
M_"a[116:119], a[228:231], a[220:223], a[116:119]\n" \
"s_lshl_b32 s7, s3, 3\n" \
"s_lshl_b32 s40, s3, 1\n" \
M_"a[112:115], a[160:163], a[156:159], a[112:115]\n" \
"s_and_b32 s7, s7, 16\n" \
"s_and_b32 s40, s40, 2\n" \
"s_waitcnt vmcnt(7) lgkmcnt(0)\n" \
M_"a[112:115], a[224:227], a[220:223], a[112:115]\n" \
"s_barrier\n" \
M_"v[52:55], a[176:179], a[128:131], v[52:55]\n" \
"s_add_u32 m0, s30, 0x0\n" \
"s_movk_i32 s84, 0x200\n" \
M_"v[52:55], a[240:243], a[192:195], v[52:55]\n" \
B_"v22, s[64:67], s84 offen offset:0 lds\n" \
M_"v[56:59], a[180:183], a[128:131], v[56:59]\n" \
X_"a[160:161], v32 offset:32768\n" \
"s_and_b32 s6, s2, 7\n" \
M_"v[56:59], a[244:247], a[192:195], v[56:59]\n" \
"s_movk_i32 s84, 0x1e00\n" \
M_"v[60:63], a[184:187], a[128:131], v[60:63]\n" \
B_"v23, s[64:67], s84 offen offset:1024 lds\n" \
M_"v[60:63], a[248:251], a[192:195], v[60:63]\n" \
X_"a[162:163], v32 offset:33792\n" \
"v_bfe_u32 v213, v20, 7, 1\n" \
M_"v[64:67], a[188:191], a[128:131], v[64:67]\n" \
X_"a[224:225], v32 offset:40960\n" \
"s_or_b32 s7, s40, s7\n" \
M_"v[64:67], a[252:255], a[192:195], v[64:67]\n" \
R_"a[128:131], v28 offset:49152\n" \
"v_and_b32_e32 v234, 0xf0, v234\n" \
M_"v[80:83], a[188:191], a[132:135], v[80:83]\n" \
R_"a[192:195], v29 offset:49152\n" \
"v_cmp_gt_u32_e32 vcc, 16, v231\n" \
M_"v[80:83], a[252:255], a[196:199], v[80:83]\n" \
"s_movk_i32 s84, 0x3a00\n" \
M_"v[76:79], a[184:187], a[132:135], v[76:79]\n" \
B_"v22, s[64:67], s84 offen offset:2048 lds\n" \
M_"v[76:79], a[248:251], a[196:199], v[76:79]\n" \
"s_movk_i32 s84, 0x5600\n" \
M_"v[72:75], a[180:183], a[132:135], v[72:75]\n" \
B_"v23, s[64:67], s84 offen offset:3072 lds\n" \
M_"v[72:75], a[244:247], a[196:199], v[72:75]\n" \
"s_add_u32 m0, s31, 0x0\n" \
"s_mov_b32 s84, 0x200000\n" \
M_"v[68:71], a[176:179], a[132:135], v[68:71]\n" \
B_"v26, s[68:71], s84 offen offset:0 lds\n" \
M_"v[68:71], a[240:243], a[196:199], v[68:71]\n" \
R_"a[132:135], v28 offset:51200\n" \
"v_or3_b32 v213, s7, v213, v219\n" \
M_"v[84:87], a[176:179], a[136:139], v[84:87]\n" \
R_"a[196:199], v29 offset:51200\n" \
"v_and_b32_e32 v214, 32, v20\n" \
M_"v[84:87], a[240:243], a[200:203], v[84:87]\n" \
"s_addk_i32 s84, 0x7c00\n" \
M_"v[88:91], a[180:183], a[136:139], v[88:91]\n" \
B_"v26, s[68:71], s84 offen offset:1024 lds\n" \
M_"v[88:91], a[244:247], a[200:203], v[88:91]\n" \
X_"a[226:227], v32 offset:41984\n" \
"s_lshl_b32 s6, s6, 5\n" \
M_"v[92:95], a[184:187], a[136:139], v[92:95]\n" \
"s_addk_i32 s84, 0x7c00\n" \
M_"v[92:95], a[248:251], a[200:203], v[92:95]\n" \
B_"v27, s[68:71], s84 offen offset:2048 lds\n" \
M_"v[96:99], a[188:191], a[136:139], v[96:99]\n" \
X_"a[164:165], v33 offset:32768\n" \
"v_cndmask_b32_e32 v231, v233, v234, vcc\n" \
M_"v[96:99], a[252:255], a[200:203], v[96:99]\n" \
R_"a[136:139], v28 offset:53248\n" \
"v_mul_u32_u24_e64 v213, 18, v213\n" \
M_"v[112:115], a[188:191], a[140:143], v[112:115]\n" \
R_"a[200:203], v29 offset:53248\n" \
"v_mov_b32_e32 v215, 0x100\n" \
M_"v[112:115], a[252:255], a[204:207], v[112:115]\n" \
"s_addk_i32 s84, 0x7c00\n" \
M_"v[108:111], a[184:187], a[140:143], v[108:111]\n" \
B_"v27, s[68:71], s84 offen offset:3072 lds\n" \
M_"v[108:111], a[248:251], a[204:207], v[108:111]\n" \
"s_add_u32 m0, s30, 0x1000\n" \
"s_mov_b32 s84, 0x8200\n" \
M_"v[104:107], a[180:183], a[140:143], v[104:107]\n" \
B_"v22, s[64:67], s84 offen offset:0 lds\n" \
M_"v[104:107], a[244:247], a[204:207], v[104:107]\n" \
X_"a[166:167], v33 offset:33792\n" \
"v_mov_b32_e32 v216, s6\n" \
M_"v[100:103], a[176:179], a[140:143], v[100:103]\n" \
X_"a[228:229], v33 offset:40960\n" \
"v_cmp_eq_u32_e64 vcc, 0, v214\n" \
M_"v[100:103], a[240:243], a[204:207], v[100:103]\n" \
R_"a[140:143], v28 offset:55296\n" \
"v_and_or_b32 v213, v218, 1, v213\n" \
M_"v[116:119], a[176:179], a[144:147], v[116:119]\n" \
R_"a[204:207], v29 offset:55296\n" \
"v_lshlrev_b32_e64 v212, 3, v212\n" \
M_"v[116:119], a[240:243], a[208:211], v[116:119]\n" \
"s_addk_i32 s84, 0x1c00\n" \
M_"v[120:123], a[180:183], a[144:147], v[120:123]\n" \
B_"v23, s[64:67], s84 offen offset:1024 lds\n" \
M_"v[120:123], a[244:247], a[208:211], v[120:123]\n" \
"s_addk_i32 s84, 0x1c00\n" \
M_"v[124:127], a[184:187], a[144:147], v[124:127]\n" \
B_"v22, s[64:67], s84 offen offset:2048 lds\n" \
M_"v[124:127], a[248:251], a[208:211], v[124:127]\n" \
"s_addk_i32 s84, 0x1c00\n" \
M_"v[128:131], a[188:191], a[144:147], v[128:131]\n" \
B_"v23, s[64:67], s84 offen offset:3072 lds\n" \
M_"v[128:131], a[252:255], a[208:211], v[128:131]\n" \
R_"a[144:147], v28 offset:32768\n" \
"v_cndmask_b32_e32 v214, v215, v216, vcc\n" \
M_"v[144:147], a[188:191], a[148:151], v[144:147]\n" \
R_"a[208:211], v29 offset:32768\n" \
"v_mad_u32_u24 v213, v213, s5, v214\n" \
M_"v[144:147], a[252:255], a[212:215], v[144:147]\n" \
"s_add_u32 m0, s31, 0x4000\n" \
"s_mov_b32 s84, 0x200000\n" \
M_"v[140:143], a[184:187], a[148:151], v[140:143]\n" \
B_"v26, s[72:75], s84 offen offset:0 lds\n" \
M_"v[140:143], a[248:251], a[212:215], v[140:143]\n" \
X_"a[230:231], v33 offset:41984\n" \
"v_and_or_b32 v213, v20, 16, v213\n" \
M_"v[136:139], a[180:183], a[148:151], v[136:139]\n" \
"s_addk_i32 s84, 0x7c00\n" \
"s_nop 0\n" \
M_"v[136:139], a[244:247], a[212:215], v[136:139]\n" \
B_"v26, s[72:75], s84 offen offset:1024 lds\n" \
M_"v[132:135], a[176:179], a[148:151], v[132:135]\n" \
X_"a[168:169], v34 offset:32768\n" \
"v_add_u32_e32 v9, 0x4800, v213\n" \
M_"v[132:135], a[240:243], a[212:215], v[132:135]\n" \
R_"a[148:151], v28 offset:34816\n" \
"v_mul_u32_u24_e32 v213, 0x120, v218\n" \
M_"v[148:151], a[176:179], a[152:155], v[148:151]\n" \
R_"a[212:215], v29 offset:34816\n" \
"v_and_or_b32 v10, v212, 24, v213\n" \
M_"v[148:151], a[240:243], a[216:219], v[148:151]\n" \
X_"a[170:171], v34 offset:33792\n" \
"v_lshrrev_b32_e64 v213, 2, v20\n" \
M_"v[152:155], a[180:183], a[152:155], v[152:155]\n" \
X_"a[232:233], v34 offset:40960\n" \
"v_and_or_b32 v213, v213, 60, s4\n" \
M_"v[152:155], a[244:247], a[216:219], v[152:155]\n" \
X_"a[234:235], v34 offset:41984\n" \
"v_lshl_or_b32 v212, s3, 9, v212\n" \
M_"v[156:159], a[184:187], a[152:155], v[156:159]\n" \
X_"a[172:173], v35 offset:32768\n" \
"v_mad_u32_u24 v4, v224, s5, v223\n" \
M_"v[156:159], a[248:251], a[216:219], v[156:159]\n" \
X_"a[174:175], v35 offset:33792\n" \
"v_mad_u32_u24 v8, v232, s5, v231\n" \
M_"v[160:163], a[188:191], a[152:155], v[160:163]\n" \
X_"a[236:237], v35 offset:40960\n" \
"v_or_b32_e32 v18, 0x27000, v213\n" \
M_"v[160:163], a[252:255], a[216:219], v[160:163]\n" \
R_"a[152:155], v28 offset:36864\n" \
"v_or_b32_e32 v19, 0x27800, v212\n" \
M_"v[176:179], a[188:191], a[156:159], v[176:179]\n" \
R_"a[216:219], v29 offset:36864\n" \
"v_mov_b32_e32 v215, 0x1000c0c\n" \
M_"v[176:179], a[252:255], a[220:223], v[176:179]\n" \
X_"a[238:239], v35 offset:41984\n" \
"v_mov_b32_e32 v216, 0xc0c0100\n" \
M_"v[172:175], a[184:187], a[156:159], v[172:175]\n" \
"v_bfe_u32 v214, v20, 4, 1\n" \
"v_lshrrev_b32_e64 v212, 8, v20\n" \
M_"v[172:175], a[248:251], a[220:223], v[172:175]\n" \
"v_cmp_eq_u32_e64 vcc, 0, v214\n" \
"v_bfe_u32 v213, v20, 4, 4\n" \
M_"v[168:171], a[180:183], a[156:159], v[168:171]\n" \
"v_mov_b32_e32 v217, 0xc0c0302\n" \
"v_cndmask_b32_e64 v215, v215, v216, vcc\n" \
M_"v[168:171], a[244:247], a[220:223], v[168:171]\n" \
"v_mov_b32_e32 v216, 0x3020c0c\n" \
"v_lshl_or_b32 v214, v212, 2, v214\n" \
M_"v[164:167], a[176:179], a[156:159], v[164:167]\n" \
"v_cndmask_b32_e64 v216, v216, v217, vcc\n" \
"v_mov_b32_e32 v217, 0xc0c0c0c\n" \
M_"v[164:167], a[240:243], a[220:223], v[164:167]\n" \
R_"a[156:159], v28 offset:38912\n" \
R_"a[220:223], v29 offset:38912\n" \
"s_waitcnt lgkmcnt(15)\n" \
M_"a[0:3], a[160:163], a[128:131], a[0:3]\n" \
"s_addk_i32 s84, 0x7c00\n" \
M_"a[0:3], a[224:227], a[192:195], a[0:3]\n" \
B_"v27, s[72:75], s84 offen offset:2048 lds\n" \
M_"a[4:7], a[164:167], a[128:131], a[4:7]\n" \
X_"a[176:177], v36 offset:32768\n" \
"v_cmp_gt_u32_e32 vcc, v214, v213\n" \
"s_waitcnt lgkmcnt(15)\n" \
M_"a[4:7], a[228:231], a[192:195], a[4:7]\n" \
"s_addk_i32 s84, 0x7c00\n" \
"s_waitcnt lgkmcnt(11)\n" \
M_"a[8:11], a[168:171], a[128:131], a[8:11]\n" \
B_"v27, s[72:75], s84 offen offset:3072 lds\n" \
"s_waitcnt lgkmcnt(9)\n" \
M_"a[8:11], a[232:235], a[192:195], a[8:11]\n" \
X_"a[178:179], v36 offset:33792\n" \
"v_or_b32_e32 v214, 2, v214\n" \
"s_waitcnt lgkmcnt(8)\n" \
M_"a[12:15], a[172:175], a[128:131], a[12:15]\n" \
X_"a[240:241], v36 offset:40960\n" \
"s_lshr_b32 s4, s2, 5\n" \
"s_waitcnt lgkmcnt(5)\n" \
M_"a[12:15], a[236:239], a[192:195], a[12:15]\n" \
"v_cndmask_b32_e32 v40, v217, v215, vcc\n" \
"v_cndmask_b32_e64 v42, v215, v217, vcc\n" \
M_"a[28:31], a[172:175], a[132:135], a[28:31]\n" \
"v_cmp_gt_u32_e32 vcc, v214, v213\n" \
"v_and_b32_e32 v214, 16, v20\n" \
M_"a[28:31], a[236:239], a[196:199], a[28:31]\n" \
X_"a[242:243], v36 offset:41984\n" \
"s_movk_i32 s42, 0x3f80\n" \
M_"a[24:27], a[168:171], a[132:135], a[24:27]\n" \
"s_xor_b32 s5, s4, 15\n" \
M_"a[24:27], a[232:235], a[196:199], a[24:27]\n" \
"s_cmpk_lt_u32 s3, 2\n" \
"s_cselect_b32 s6, s5, s4\n" \
M_"a[20:23], a[164:167], a[132:135], a[20:23]\n" \
"s_lshl_b32 s7, s6, 7\n" \
"v_and_b32_e32 v213, 0x320, v20\n" \
M_"a[20:23], a[228:231], a[196:199], a[20:23]\n" \
X_"a[180:181], v37 offset:32768\n" \
"s_movk_i32 s41, 0x100\n" \
M_"a[16:19], a[160:163], a[132:135], a[16:19]\n" \
"v_lshlrev_b32_e64 v214, v214, s42\n" \
"v_mov_b32_e64 v215, 0\n" \
M_"a[16:19], a[224:227], a[196:199], a[16:19]\n" \
"s_add_i32 s40, s7, -16\n" \
"v_cndmask_b32_e32 v41, v217, v216, vcc\n" \
M_"a[32:35], a[160:163], a[136:139], a[32:35]\n" \
X_"a[182:183], v37 offset:33792\n" \
"v_cndmask_b32_e64 v43, v216, v217, vcc\n" \
M_"a[32:35], a[224:227], a[200:203], a[32:35]\n" \
"v_cmp_eq_u32_e32 vcc, s41, v213\n" \
"s_mov_b32 s41, 0\n" \
M_"a[36:39], a[164:167], a[136:139], a[36:39]\n" \
"s_and_b32 s43, s3, 1\n" \
"v_cndmask_b32_e32 v46, v215, v214, vcc\n" \
M_"a[36:39], a[228:231], a[200:203], a[36:39]\n" \
X_"a[244:245], v37 offset:40960\n" \
"v_and_b32_e32 v213, 0x330, v20\n" \
M_"a[40:43], a[168:171], a[136:139], a[40:43]\n" \
"s_mov_b32 m0, s32\n" \
M_"a[40:43], a[232:235], a[200:203], a[40:43]\n" \
"s_mov_b32 exec_lo, 0xffffffff\n" \
"s_mov_b32 exec_hi, 0x0\n" \
"global_load_lds_dword v[188:189], off\n" \
"s_mov_b64 exec, -1\n" \
M_"a[44:47], a[172:175], a[136:139], a[44:47]\n" \
X_"a[246:247], v37 offset:41984\n" \
M_"a[44:47], a[236:239], a[200:203], a[44:47]\n" \
"s_cmp_lg_u32 s7, 0\n" \
"s_cselect_b32 s40, s40, s41\n" \
M_"a[60:63], a[172:175], a[140:143], a[60:63]\n" \
"s_mov_b32 s42, 0xff800000\n" \
M_"a[60:63], a[236:239], a[204:207], a[60:63]\n" \
"s_cmp_lg_u32 s7, 0\n" \
"s_cselect_b32 s47, s41, s42\n" \
M_"a[56:59], a[168:171], a[140:143], a[56:59]\n" \
X_"a[184:185], v38 offset:32768\n" \
"s_lshl_b32 s41, s43, 6\n" \
M_"a[56:59], a[232:235], a[204:207], a[56:59]\n" \
"v_lshrrev_b32_e32 v215, 6, v20\n" \
"v_mov_b32_e32 v214, 0x3f80\n" \
M_"a[52:55], a[164:167], a[140:143], a[52:55]\n" \
"v_cmp_eq_u32_e32 vcc, 32, v213\n" \
"s_or_b32 s7, s41, s7\n" \
M_"a[52:55], a[228:231], a[204:207], a[52:55]\n" \
X_"a[186:187], v38 offset:33792\n" \
"v_lshrrev_b32_e64 v213, 7, v20\n" \
M_"a[48:51], a[160:163], a[140:143], a[48:51]\n" \
"v_bfe_u32 v216, v20, 6, 2\n" \
"v_lshrrev_b32_e64 v219, 1, v20\n" \
M_"a[48:51], a[224:227], a[204:207], a[48:51]\n" \
"v_bfe_u32 v215, v215, 1, 1\n" \
"v_cndmask_b32_e64 v47, 0, v214, vcc\n" \
M_"a[64:67], a[160:163], a[144:147], a[64:67]\n" \
X_"a[248:249], v38 offset:40960\n" \
"v_or_b32_e64 v214, s7, v213\n" \
M_"a[64:67], a[224:227], a[208:211], a[64:67]\n" \
"v_lshl_or_b32 v217, s43, 3, v213\n" \
"v_lshlrev_b32_e64 v218, 10, v212\n" \
M_"a[68:71], a[164:167], a[144:147], a[68:71]\n" \
"v_lshlrev_b32_e32 v216, 7, v216\n" \
"v_and_b32_e32 v219, 24, v219\n" \
M_"a[68:71], a[228:231], a[208:211], a[68:71]\n" \
X_"a[250:251], v38 offset:41984\n" \
"v_and_or_b32 v213, v213, 2, v215\n" \
M_"a[72:75], a[168:171], a[144:147], a[72:75]\n" \
"v_add_u32_e64 v217, s40, v217\n" \
"v_or3_b32 v216, v216, v218, v219\n" \
M_"a[72:75], a[232:235], a[208:211], a[72:75]\n" \
"v_lshlrev_b32_e64 v213, 5, v213\n" \
"s_mov_b32 s40, 0x14000\n" \
M_"a[76:79], a[172:175], a[144:147], a[76:79]\n" \
"s_lshl_b32 s7, s2, 2\n" \
"v_or3_b32 v48, v216, v213, s40\n" \
M_"a[76:79], a[236:239], a[208:211], a[76:79]\n" \
X_"a[188:189], v39 offset:32768\n" \
"s_lshl_b32 s40, s43, 2\n" \
M_"a[92:95], a[172:175], a[148:151], a[92:95]\n" \
"s_lshl_b32 s6, s6, 3\n" \
"v_or_b32_e32 v213, s40, v212\n" \
M_"a[92:95], a[236:239], a[212:215], a[92:95]\n" \
"s_or_b32 s6, s6, s40\n" \
"s_and_b32 s40, s7, 0x7c\n" \
M_"a[88:91], a[168:171], a[148:151], a[88:91]\n" \
X_"a[190:191], v39 offset:33792\n" \
"s_or_b32 s40, s40, s3\n" \
M_"a[88:91], a[232:235], a[212:215], a[88:91]\n" \
"s_cmpk_lt_u32 s40, 64\n" \
"s_cselect_b32 s41, s4, s5\n" \
M_"a[84:87], a[164:167], a[148:151], a[84:87]\n" \
"s_waitcnt vmcnt(17)\n" \
"v_or_b32_e32 v204, v210, v204\n" \
"v_or_b32_e64 v206, v210, v206\n" \
M_"a[84:87], a[228:231], a[212:215], a[84:87]\n" \
"v_mov_b32_e64 v210, 0\n" \
"s_add_u32 s88, s35, 0x1000\n" \
M_"a[80:83], a[160:163], a[148:151], a[80:83]\n" \
X_"a[252:253], v39 offset:40960\n" \
"s_lshl_b32 s40, s40, 1\n" \
M_"a[80:83], a[224:227], a[212:215], a[80:83]\n" \
"ds_write_b128 v196, v[200:203]\n" \
M_"a[96:99], a[160:163], a[152:155], a[96:99]\n" \
X_"a[254:255], v39 offset:41984\n" \
"s_lshl_b32 s41, s41, 7\n" \
M_"a[96:99], a[224:227], a[216:219], a[96:99]\n" \
"s_and_b32 s40, s40, 0x7e\n" \
"s_or_b32 s40, s41, s40\n" \
M_"a[100:103], a[164:167], a[152:155], a[100:103]\n" \
"ds_write_b128 v197, v[204:207]\n" \
M_"a[100:103], a[228:231], a[216:219], a[100:103]\n" \
"s_mulk_i32 s40, 0x480\n" \
"v_add_u32_e64 v49, s40, v20\n" \
M_"a[104:107], a[168:171], a[152:155], a[104:107]\n" \
"s_mov_b32 s40, 0xc000\n" \
"s_and_b32 s7, s7, 28\n" \
M_"a[104:107], a[232:235], a[216:219], a[104:107]\n" \
"ds_write_b128 v198, v[208:211]\n" \
M_"a[108:111], a[172:175], a[152:155], a[108:111]\n" \
"s_bfe_u32 s45, s2, 0x20003\n" \
"s_mov_b32 s41, 0x8000\n" \
M_"a[108:111], a[236:239], a[216:219], a[108:111]\n" \
"s_cmp_lg_u32 s4, 0\n" \
"s_cselect_b32 s51, s40, s41\n" \
"s_waitcnt lgkmcnt(15)\n" \
M_"a[124:127], a[172:175], a[156:159], a[124:127]\n" \
"s_movk_i32 s40, 0x4000\n" \
"s_lshr_b32 s44, s3, 1\n" \
M_"a[124:127], a[236:239], a[220:223], a[124:127]\n" \
"s_or_b32 s46, s7, s45\n" \
M_"a[120:123], a[168:171], a[156:159], a[120:123]\n" \
"s_cmp_lg_u32 s4, 0\n" \
"s_cselect_b32 s53, s41, s40\n" \
M_"a[120:123], a[232:235], a[220:223], a[120:123]\n" \
"s_lshl_b32 s40, s44, 15\n" \
"s_lshl_b32 s41, s43, 14\n" \
M_"a[116:119], a[164:167], a[156:159], a[116:119]\n" \
"s_lshl_b32 s48, s46, 20\n" \
"s_or_b32 s41, s40, s41\n" \
M_"a[116:119], a[228:231], a[220:223], a[116:119]\n" \
"s_add_i32 s49, s48, 0x1000000\n" \
"s_or_b32 s42, s49, s41\n" \
M_"a[112:115], a[160:163], a[156:159], a[112:115]\n" \
"s_lshl_b32 s4, s4, 16\n" \
"s_or_b32 s41, s42, s4\n" \
"s_waitcnt vmcnt(1) lgkmcnt(0)\n" \
M_"a[112:115], a[224:227], a[220:223], a[112:115]\n" \
"s_barrier\n" \
M_"v[52:55], a[176:179], a[128:131], v[52:55]\n" \
"s_add_u32 m0, s30, 0x8000\n" \
"s_movk_i32 s84, 0x280\n" \
M_"v[52:55], a[240:243], a[192:195], v[52:55]\n" \
B_"v22, s[64:67], s84 offen offset:0 lds\n" \
M_"v[56:59], a[180:183], a[128:131], v[56:59]\n" \
"s_movk_i32 s84, 0x1e80\n" \
M_"v[56:59], a[244:247], a[192:195], v[56:59]\n" \
B_"v23, s[64:67], s84 offen offset:1024 lds\n" \
M_"v[60:63], a[184:187], a[128:131], v[60:63]\n" \
X_"a[160:161], v32 offset:0\n" \
"s_lshl_b32 s4, s5, 16\n" \
M_"v[60:63], a[248:251], a[192:195], v[60:63]\n" \
"s_movk_i32 s84, 0x3a80\n" \
M_"v[64:67], a[188:191], a[128:131], v[64:67]\n" \
B_"v22, s[64:67], s84 offen offset:2048 lds\n" \
M_"v[64:67], a[252:255], a[192:195], v[64:67]\n" \
R_"a[128:131], v28 offset:16384\n" \
"s_mul_i32 s5, s6, 0x24800\n" \
M_"v[80:83], a[188:191], a[132:135], v[80:83]\n" \
R_"a[192:195], v29 offset:16384\n" \
"s_lshl_b32 s6, s46, 9\n" \
M_"v[80:83], a[252:255], a[196:199], v[80:83]\n" \
"s_movk_i32 s84, 0x5680\n" \
M_"v[76:79], a[184:187], a[132:135], v[76:79]\n" \
B_"v23, s[64:67], s84 offen offset:3072 lds\n" \
M_"v[76:79], a[248:251], a[196:199], v[76:79]\n" \
"s_add_u32 m0, s31, 0x8000\n" \
"s_mov_b32 s84, 0x280000\n" \
M_"v[72:75], a[180:183], a[132:135], v[72:75]\n" \
B_"v26, s[68:71], s84 offen offset:0 lds\n" \
M_"v[72:75], a[244:247], a[196:199], v[72:75]\n" \
X_"a[162:163], v32 offset:1024\n" \
"s_lshl_b32 s7, s7, 6\n" \
M_"v[68:71], a[176:179], a[132:135], v[68:71]\n" \
X_"a[224:225], v32 offset:8192\n" \
"s_add_i32 s6, s5, s6\n" \
M_"v[68:71], a[240:243], a[196:199], v[68:71]\n" \
R_"a[132:135], v28 offset:18432\n" \
"s_or_b32 s5, s5, s7\n" \
M_"v[84:87], a[176:179], a[136:139], v[84:87]\n" \
R_"a[196:199], v29 offset:18432\n" \
"s_lshl_b32 s7, s45, 2\n" \
M_"v[84:87], a[240:243], a[200:203], v[84:87]\n" \
"s_addk_i32 s84, 0x7c00\n" \
M_"v[88:91], a[180:183], a[136:139], v[88:91]\n" \
B_"v26, s[68:71], s84 offen offset:1024 lds\n" \
M_"v[88:91], a[244:247], a[200:203], v[88:91]\n" \
"s_addk_i32 s84, 0x7c00\n" \
M_"v[92:95], a[184:187], a[136:139], v[92:95]\n" \
B_"v27, s[68:71], s84 offen offset:2048 lds\n" \
M_"v[92:95], a[248:251], a[200:203], v[92:95]\n" \
"s_addk_i32 s84, 0x7c00\n" \
M_"v[96:99], a[188:191], a[136:139], v[96:99]\n" \
B_"v27, s[68:71], s84 offen offset:3072 lds\n" \
M_"v[96:99], a[252:255], a[200:203], v[96:99]\n" \
R_"a[136:139], v28 offset:20480\n" \
"s_or_b32 s5, s5, s7\n" \
M_"v[112:115], a[188:191], a[140:143], v[112:115]\n" \
R_"a[200:203], v29 offset:20480\n" \
"s_lshl_b32 s7, s3, 12\n" \
M_"v[112:115], a[252:255], a[204:207], v[112:115]\n" \
"s_add_u32 m0, s30, 0x9000\n" \
"s_mov_b32 s84, 0x8280\n" \
M_"v[108:111], a[184:187], a[140:143], v[108:111]\n" \
B_"v22, s[64:67], s84 offen offset:0 lds\n" \
M_"v[108:111], a[248:251], a[204:207], v[108:111]\n" \
"s_addk_i32 s84, 0x1c00\n" \
M_"v[104:107], a[180:183], a[140:143], v[104:107]\n" \
B_"v23, s[64:67], s84 offen offset:1024 lds\n" \
M_"v[104:107], a[244:247], a[204:207], v[104:107]\n" \
"s_addk_i32 s84, 0x1c00\n" \
M_"v[100:103], a[176:179], a[140:143], v[100:103]\n" \
B_"v22, s[64:67], s84 offen offset:2048 lds\n" \
M_"v[100:103], a[240:243], a[204:207], v[100:103]\n" \
R_"a[140:143], v28 offset:22528\n" \
"s_or_b32 s50, s49, s7\n" \
M_"v[116:119], a[176:179], a[144:147], v[116:119]\n" \
R_"a[204:207], v29 offset:22528\n" \
"s_lshl_b32 s7, s3, 11\n" \
M_"v[116:119], a[240:243], a[208:211], v[116:119]\n" \
"s_addk_i32 s84, 0x1c00\n" \
M_"v[120:123], a[180:183], a[144:147], v[120:123]\n" \
B_"v23, s[64:67], s84 offen offset:3072 lds\n" \
M_"v[120:123], a[244:247], a[208:211], v[120:123]\n" \
"s_add_u32 m0, s31, 0xc000\n" \
"s_mov_b32 s84, 0x280000\n" \
M_"v[124:127], a[184:187], a[144:147], v[124:127]\n" \
B_"v26, s[72:75], s84 offen offset:0 lds\n" \
M_"v[124:127], a[248:251], a[208:211], v[124:127]\n" \
"s_addk_i32 s84, 0x7c00\n" \
M_"v[128:131], a[188:191], a[144:147], v[128:131]\n" \
B_"v26, s[72:75], s84 offen offset:1024 lds\n" \
M_"v[128:131], a[252:255], a[208:211], v[128:131]\n" \
R_"a[144:147], v28 offset:0\n" \
"v_lshrrev_b32_e32 v215, 4, v20\n" \
M_"v[144:147], a[188:191], a[148:151], v[144:147]\n" \
R_"a[208:211], v29 offset:0\n" \
"v_bfe_u32 v216, v20, 4, 3\n" \
M_"v[144:147], a[252:255], a[212:215], v[144:147]\n" \
R_"v[200:203], v199 offset:0\n" \
"s_or_b32 s42, s42, s4\n" \
M_"v[140:143], a[184:187], a[148:151], v[140:143]\n" \
R_"v[204:207], v199 offset:4096\n" \
"s_lshl_b32 s4, s43, 11\n" \
M_"v[140:143], a[248:251], a[212:215], v[140:143]\n" \
X_"a[226:227], v32 offset:9216\n" \
"s_add_i32 s52, s48, s7\n" \
M_"v[136:139], a[180:183], a[148:151], v[136:139]\n" \
X_"a[164:165], v33 offset:0\n" \
"s_add_i32 s53, s53, s7\n" \
M_"v[136:139], a[244:247], a[212:215], v[136:139]\n" \
R_"v[208:211], v199 offset:8192\n" \
"v_mul_u32_u24_e32 v214, 0x480, v214\n" \
M_"v[132:135], a[176:179], a[148:151], v[132:135]\n" \
X_"a[166:167], v33 offset:1024\n" \
"v_bitop3_b32 v218, v212, v215, 7 bitop3:0x78\n" \
M_"v[132:135], a[240:243], a[212:215], v[132:135]\n" \
R_"a[148:151], v28 offset:2048\n" \
"v_bitop3_b32 v212, v212, v216, 4 bitop3:0x36\n" \
M_"v[148:151], a[176:179], a[152:155], v[148:151]\n" \
R_"a[212:215], v29 offset:2048\n" \
"s_lshl_b32 s7, s3, 10\n" \
M_"v[148:151], a[240:243], a[216:219], v[148:151]\n" \
X_"a[228:229], v33 offset:8192\n" \
"s_or_b32 s4, s40, s4\n" \
M_"v[152:155], a[180:183], a[152:155], v[152:155]\n" \
X_"a[230:231], v33 offset:9216\n" \
"v_lshl_or_b32 v25, v212, 4, v214\n" \
M_"v[152:155], a[244:247], a[216:219], v[152:155]\n" \
X_"a[168:169], v34 offset:0\n" \
"v_mul_u32_u24_e32 v212, 0x480, v217\n" \
M_"v[156:159], a[184:187], a[152:155], v[156:159]\n" \
X_"a[170:171], v34 offset:1024\n" \
"v_bitop3_b32 v213, v215, v213, 7 bitop3:0x6c\n" \
M_"v[156:159], a[248:251], a[216:219], v[156:159]\n" \
X_"a[232:233], v34 offset:8192\n" \
"s_or_b32 s40, s7, 0x14000\n" \
M_"v[160:163], a[188:191], a[152:155], v[160:163]\n" \
X_"a[234:235], v34 offset:9216\n" \
"s_lshl_b32 s43, s44, 13\n" \
M_"v[160:163], a[252:255], a[216:219], v[160:163]\n" \
R_"a[152:155], v28 offset:4096\n" \
"s_mulk_i32 s44, 0xe800\n" \
M_"v[176:179], a[188:191], a[156:159], v[176:179]\n" \
R_"a[216:219], v29 offset:4096\n" \
"s_lshl_b32 s7, s3, 8\n" \
"s_nop 0\n" \
M_"v[176:179], a[252:255], a[220:223], v[176:179]\n" \
X_"a[172:173], v35 offset:0\n" \
"v_lshl_or_b32 v24, v218, 4, v214\n" \
M_"v[172:175], a[184:187], a[156:159], v[172:175]\n" \
X_"a[174:175], v35 offset:1024\n" \
"v_lshl_or_b32 v21, v213, 4, v212\n" \
M_"v[172:175], a[248:251], a[220:223], v[172:175]\n" \
X_"a[236:237], v35 offset:8192\n" \
"s_add_i32 s44, s44, 0x17000\n" \
M_"v[168:171], a[180:183], a[156:159], v[168:171]\n" \
X_"a[238:239], v35 offset:9216\n" \
"s_or_b32 s45, s7, 0x27000\n" \
M_"v[168:171], a[244:247], a[220:223], v[168:171]\n" \
"s_or_b32 s46, s4, 0x10000\n" \
"s_add_i32 s48, s6, 0x3000000\n" \
M_"v[164:167], a[176:179], a[156:159], v[164:167]\n" \
"s_add_i32 s49, s5, 0x3004000\n" \
"s_add_i32 s52, s52, 0x1010000\n" \
".fill (L_pb + 12000 - .) / 4, 4, 0xBF800000\n" \
"L_pp3:\n" \
"s_cbranch_execz L_pret\n" \
M_"v[164:167], a[240:243], a[220:223], v[164:167]\n" \
R_"a[156:159], v28 offset:6144\n" \
R_"a[220:223], v29 offset:6144\n" \
"s_waitcnt lgkmcnt(15)\n" \
M_"a[0:3], a[160:163], a[128:131], a[0:3]\n" \
"s_addk_i32 s84, 0x7c00\n" \
M_"a[0:3], a[224:227], a[192:195], a[0:3]\n" \
B_"v27, s[72:75], s84 offen offset:2048 lds\n" \
M_"a[4:7], a[164:167], a[128:131], a[4:7]\n" \
X_"a[176:177], v36 offset:0\n" \
"s_waitcnt lgkmcnt(13)\n" \
M_"a[4:7], a[228:231], a[192:195], a[4:7]\n" \
"s_addk_i32 s84, 0x7c00\n" \
"s_waitcnt lgkmcnt(11)\n" \
M_"a[8:11], a[168:171], a[128:131], a[8:11]\n" \
B_"v27, s[72:75], s84 offen offset:3072 lds\n" \
"s_waitcnt lgkmcnt(9)\n" \
M_"a[8:11], a[232:235], a[192:195], a[8:11]\n" \
X_"a[178:179], v36 offset:1024\n" \
"s_waitcnt lgkmcnt(6)\n" \
M_"a[12:15], a[172:175], a[128:131], a[12:15]\n" \
X_"a[240:241], v36 offset:8192\n" \
"s_waitcnt lgkmcnt(5)\n" \
"s_nop 0\n" \
M_"a[12:15], a[236:239], a[192:195], a[12:15]\n" \
M_"a[28:31], a[172:175], a[132:135], a[28:31]\n" \
M_"a[28:31], a[236:239], a[196:199], a[28:31]\n" \
X_"a[242:243], v36 offset:9216\n" \
M_"a[24:27], a[168:171], a[132:135], a[24:27]\n" \
M_"a[24:27], a[232:235], a[196:199], a[24:27]\n" \
M_"a[20:23], a[164:167], a[132:135], a[20:23]\n" \
M_"a[20:23], a[228:231], a[196:199], a[20:23]\n" \
X_"a[180:181], v37 offset:0\n" \
M_"a[16:19], a[160:163], a[132:135], a[16:19]\n" \
M_"a[16:19], a[224:227], a[196:199], a[16:19]\n" \
M_"a[32:35], a[160:163], a[136:139], a[32:35]\n" \
X_"a[182:183], v37 offset:1024\n" \
M_"a[32:35], a[224:227], a[200:203], a[32:35]\n" \
M_"a[36:39], a[164:167], a[136:139], a[36:39]\n" \
M_"a[36:39], a[228:231], a[200:203], a[36:39]\n" \
"buffer_store_dwordx4 v[200:203], v20, s[80:83], s35 offen sc1\n" \
M_"a[40:43], a[168:171], a[136:139], a[40:43]\n" \
"s_mov_b32 m0, s32\n" \
X_"a[244:245], v37 offset:8192\n" \
M_"a[40:43], a[232:235], a[200:203], a[40:43]\n" \
"s_mov_b32 exec_lo, 0x0\n" \
"s_mov_b32 exec_hi, 0xffffffff\n" \
"global_load_lds_dword v[188:189], off\n" \
"s_mov_b64 exec, -1\n" \
M_"a[44:47], a[172:175], a[136:139], a[44:47]\n" \
X_"a[246:247], v37 offset:9216\n" \
M_"a[44:47], a[236:239], a[200:203], a[44:47]\n" \
M_"a[60:63], a[172:175], a[140:143], a[60:63]\n" \
M_"a[60:63], a[236:239], a[204:207], a[60:63]\n" \
M_"a[56:59], a[168:171], a[140:143], a[56:59]\n" \
X_"a[184:185], v38 offset:0\n" \
M_"a[56:59], a[232:235], a[204:207], a[56:59]\n" \
M_"a[52:55], a[164:167], a[140:143], a[52:55]\n" \
"buffer_store_dwordx4 v[204:207], v20, s[80:83], s88 offen sc1\n" \
M_"a[52:55], a[228:231], a[204:207], a[52:55]\n" \
X_"a[186:187], v38 offset:1024\n" \
M_"a[48:51], a[160:163], a[140:143], a[48:51]\n" \
M_"a[48:51], a[224:227], a[204:207], a[48:51]\n" \
M_"a[64:67], a[160:163], a[144:147], a[64:67]\n" \
X_"a[248:249], v38 offset:8192\n" \
M_"a[64:67], a[224:227], a[208:211], a[64:67]\n" \
M_"a[68:71], a[164:167], a[144:147], a[68:71]\n" \
M_"a[68:71], a[228:231], a[208:211], a[68:71]\n" \
X_"a[250:251], v38 offset:9216\n" \
M_"a[72:75], a[168:171], a[144:147], a[72:75]\n" \
M_"a[72:75], a[232:235], a[208:211], a[72:75]\n" \
M_"a[76:79], a[172:175], a[144:147], a[76:79]\n" \
"buffer_store_dwordx4 v[208:211], v20, s[80:83], s36 offen sc1\n" \
M_"a[76:79], a[236:239], a[208:211], a[76:79]\n" \
X_"a[188:189], v39 offset:0\n" \
M_"a[92:95], a[172:175], a[148:151], a[92:95]\n" \
M_"a[92:95], a[236:239], a[212:215], a[92:95]\n" \
M_"a[88:91], a[168:171], a[148:151], a[88:91]\n" \
X_"a[190:191], v39 offset:1024\n" \
M_"a[88:91], a[232:235], a[212:215], a[88:91]\n" \
M_"a[84:87], a[164:167], a[148:151], a[84:87]\n" \
M_"a[84:87], a[228:231], a[212:215], a[84:87]\n" \
X_"a[252:253], v39 offset:8192\n" \
M_"a[80:83], a[160:163], a[148:151], a[80:83]\n" \
M_"a[80:83], a[224:227], a[212:215], a[80:83]\n" \
M_"a[96:99], a[160:163], a[152:155], a[96:99]\n" \
X_"a[254:255], v39 offset:9216\n" \
M_"a[96:99], a[224:227], a[216:219], a[96:99]\n" \
M_"a[100:103], a[164:167], a[152:155], a[100:103]\n" \
M_"a[100:103], a[228:231], a[216:219], a[100:103]\n" \
M_"a[104:107], a[168:171], a[152:155], a[104:107]\n" \
M_"a[104:107], a[232:235], a[216:219], a[104:107]\n" \
M_"a[108:111], a[172:175], a[152:155], a[108:111]\n" \
M_"a[108:111], a[236:239], a[216:219], a[108:111]\n" \
"s_waitcnt lgkmcnt(15)\n" \
M_"a[124:127], a[172:175], a[156:159], a[124:127]\n" \
M_"a[124:127], a[236:239], a[220:223], a[124:127]\n" \
M_"a[120:123], a[168:171], a[156:159], a[120:123]\n" \
M_"a[120:123], a[232:235], a[220:223], a[120:123]\n" \
M_"a[116:119], a[164:167], a[156:159], a[116:119]\n" \
M_"a[116:119], a[228:231], a[220:223], a[116:119]\n" \
M_"a[112:115], a[160:163], a[156:159], a[112:115]\n" \
"s_waitcnt vmcnt(4) lgkmcnt(0)\n" \
M_"a[112:115], a[224:227], a[220:223], a[112:115]\n" \
"s_barrier\n" \
M_"v[52:55], a[176:179], a[128:131], v[52:55]\n" \
"s_add_u32 m0, s30, 0x0\n" \
"s_movk_i32 s84, 0x300\n" \
M_"v[52:55], a[240:243], a[192:195], v[52:55]\n" \
B_"v22, s[64:67], s84 offen offset:0 lds\n" \
M_"v[56:59], a[180:183], a[128:131], v[56:59]\n" \
X_"a[160:161], v32 offset:32768\n" \
M_"v[56:59], a[244:247], a[192:195], v[56:59]\n" \
"s_movk_i32 s84, 0x1f00\n" \
M_"v[60:63], a[184:187], a[128:131], v[60:63]\n" \
B_"v23, s[64:67], s84 offen offset:1024 lds\n" \
M_"v[60:63], a[248:251], a[192:195], v[60:63]\n" \
X_"a[162:163], v32 offset:33792\n" \
M_"v[64:67], a[188:191], a[128:131], v[64:67]\n" \
X_"a[224:225], v32 offset:40960\n" \
M_"v[64:67], a[252:255], a[192:195], v[64:67]\n" \
R_"a[128:131], v28 offset:49152\n" \
M_"v[80:83], a[188:191], a[132:135], v[80:83]\n" \
R_"a[192:195], v29 offset:49152\n" \
M_"v[80:83], a[252:255], a[196:199], v[80:83]\n" \
"s_movk_i32 s84, 0x3b00\n" \
M_"v[76:79], a[184:187], a[132:135], v[76:79]\n" \
B_"v22, s[64:67], s84 offen offset:2048 lds\n" \
M_"v[76:79], a[248:251], a[196:199], v[76:79]\n" \
"s_movk_i32 s84, 0x5700\n" \
M_"v[72:75], a[180:183], a[132:135], v[72:75]\n" \
B_"v23, s[64:67], s84 offen offset:3072 lds\n" \
M_"v[72:75], a[244:247], a[196:199], v[72:75]\n" \
"s_add_u32 m0, s31, 0x0\n" \
"s_mov_b32 s84, 0x300000\n" \
M_"v[68:71], a[176:179], a[132:135], v[68:71]\n" \
B_"v26, s[68:71], s84 offen offset:0 lds\n" \
M_"v[68:71], a[240:243], a[196:199], v[68:71]\n" \
R_"a[132:135], v28 offset:51200\n" \
M_"v[84:87], a[176:179], a[136:139], v[84:87]\n" \
R_"a[196:199], v29 offset:51200\n" \
M_"v[84:87], a[240:243], a[200:203], v[84:87]\n" \
"s_addk_i32 s84, 0x7c00\n" \
M_"v[88:91], a[180:183], a[136:139], v[88:91]\n" \
B_"v26, s[68:71], s84 offen offset:1024 lds\n" \
M_"v[88:91], a[244:247], a[200:203], v[88:91]\n" \
X_"a[226:227], v32 offset:41984\n" \
M_"v[92:95], a[184:187], a[136:139], v[92:95]\n" \
"s_addk_i32 s84, 0x7c00\n" \
M_"v[92:95], a[248:251], a[200:203], v[92:95]\n" \
B_"v27, s[68:71], s84 offen offset:2048 lds\n" \
M_"v[96:99], a[188:191], a[136:139], v[96:99]\n" \
X_"a[164:165], v33 offset:32768\n" \
M_"v[96:99], a[252:255], a[200:203], v[96:99]\n" \
R_"a[136:139], v28 offset:53248\n" \
M_"v[112:115], a[188:191], a[140:143], v[112:115]\n" \
R_"a[200:203], v29 offset:53248\n" \
M_"v[112:115], a[252:255], a[204:207], v[112:115]\n" \
"s_addk_i32 s84, 0x7c00\n" \
M_"v[108:111], a[184:187], a[140:143], v[108:111]\n" \
B_"v27, s[68:71], s84 offen offset:3072 lds\n" \
M_"v[108:111], a[248:251], a[204:207], v[108:111]\n" \
"s_add_u32 m0, s30, 0x1000\n" \
"s_mov_b32 s84, 0x8300\n" \
M_"v[104:107], a[180:183], a[140:143], v[104:107]\n" \
B_"v22, s[64:67], s84 offen offset:0 lds\n" \
M_"v[104:107], a[244:247], a[204:207], v[104:107]\n" \
X_"a[166:167], v33 offset:33792\n" \
M_"v[100:103], a[176:179], a[140:143], v[100:103]\n" \
X_"a[228:229], v33 offset:40960\n" \
M_"v[100:103], a[240:243], a[204:207], v[100:103]\n" \
R_"a[140:143], v28 offset:55296\n" \
M_"v[116:119], a[176:179], a[144:147], v[116:119]\n" \
R_"a[204:207], v29 offset:55296\n" \
M_"v[116:119], a[240:243], a[208:211], v[116:119]\n" \
"s_addk_i32 s84, 0x1c00\n" \
M_"v[120:123], a[180:183], a[144:147], v[120:123]\n" \
B_"v23, s[64:67], s84 offen offset:1024 lds\n" \
M_"v[120:123], a[244:247], a[208:211], v[120:123]\n" \
"s_addk_i32 s84, 0x1c00\n" \
M_"v[124:127], a[184:187], a[144:147], v[124:127]\n" \
B_"v22, s[64:67], s84 offen offset:2048 lds\n" \
M_"v[124:127], a[248:251], a[208:211], v[124:127]\n" \
"s_addk_i32 s84, 0x1c00\n" \
"s_nop 0\n" \
M_"v[128:131], a[188:191], a[144:147], v[128:131]\n" \
B_"v23, s[64:67], s84 offen offset:3072 lds\n" \
M_"v[128:131], a[252:255], a[208:211], v[128:131]\n" \
R_"a[144:147], v28 offset:32768\n" \
M_"v[144:147], a[188:191], a[148:151], v[144:147]\n" \
R_"a[208:211], v29 offset:32768\n" \
M_"v[144:147], a[252:255], a[212:215], v[144:147]\n" \
"s_add_u32 m0, s31, 0x4000\n" \
"s_mov_b32 s84, 0x300000\n" \
M_"v[140:143], a[184:187], a[148:151], v[140:143]\n" \
B_"v26, s[72:75], s84 offen offset:0 lds\n" \
M_"v[140:143], a[248:251], a[212:215], v[140:143]\n" \
X_"a[230:231], v33 offset:41984\n" \
M_"v[136:139], a[180:183], a[148:151], v[136:139]\n" \
"s_addk_i32 s84, 0x7c00\n" \
"s_nop 0\n" \
M_"v[136:139], a[244:247], a[212:215], v[136:139]\n" \
B_"v26, s[72:75], s84 offen offset:1024 lds\n" \
M_"v[132:135], a[176:179], a[148:151], v[132:135]\n" \
X_"a[168:169], v34 offset:32768\n" \
M_"v[132:135], a[240:243], a[212:215], v[132:135]\n" \
R_"a[148:151], v28 offset:34816\n" \
M_"v[148:151], a[176:179], a[152:155], v[148:151]\n" \
R_"a[212:215], v29 offset:34816\n" \
M_"v[148:151], a[240:243], a[216:219], v[148:151]\n" \
X_"a[170:171], v34 offset:33792\n" \
M_"v[152:155], a[180:183], a[152:155], v[152:155]\n" \
X_"a[232:233], v34 offset:40960\n" \
M_"v[152:155], a[244:247], a[216:219], v[152:155]\n" \
X_"a[234:235], v34 offset:41984\n" \
M_"v[156:159], a[184:187], a[152:155], v[156:159]\n" \
X_"a[172:173], v35 offset:32768\n" \
M_"v[156:159], a[248:251], a[216:219], v[156:159]\n" \
X_"a[174:175], v35 offset:33792\n" \
M_"v[160:163], a[188:191], a[152:155], v[160:163]\n" \
X_"a[236:237], v35 offset:40960\n" \
M_"v[160:163], a[252:255], a[216:219], v[160:163]\n" \
R_"a[152:155], v28 offset:36864\n" \
M_"v[176:179], a[188:191], a[156:159], v[176:179]\n" \
R_"a[216:219], v29 offset:36864\n" \
M_"v[176:179], a[252:255], a[220:223], v[176:179]\n" \
X_"a[238:239], v35 offset:41984\n" \
M_"v[172:175], a[184:187], a[156:159], v[172:175]\n" \
M_"v[172:175], a[248:251], a[220:223], v[172:175]\n" \
M_"v[168:171], a[180:183], a[156:159], v[168:171]\n" \
M_"v[168:171], a[244:247], a[220:223], v[168:171]\n" \
M_"v[164:167], a[176:179], a[156:159], v[164:167]\n" \
M_"v[164:167], a[240:243], a[220:223], v[164:167]\n" \
R_"a[156:159], v28 offset:38912\n" \
R_"a[220:223], v29 offset:38912\n" \
"s_waitcnt lgkmcnt(15)\n" \
M_"a[0:3], a[160:163], a[128:131], a[0:3]\n" \
"s_addk_i32 s84, 0x7c00\n" \
M_"a[0:3], a[224:227], a[192:195], a[0:3]\n" \
B_"v27, s[72:75], s84 offen offset:2048 lds\n" \
M_"a[4:7], a[164:167], a[128:131], a[4:7]\n" \
X_"a[176:177], v36 offset:32768\n" \
"s_waitcnt lgkmcnt(15)\n" \
M_"a[4:7], a[228:231], a[192:195], a[4:7]\n" \
"s_addk_i32 s84, 0x7c00\n" \
"s_waitcnt lgkmcnt(11)\n" \
M_"a[8:11], a[168:171], a[128:131], a[8:11]\n" \
B_"v27, s[72:75], s84 offen offset:3072 lds\n" \
"s_waitcnt lgkmcnt(9)\n" \
M_"a[8:11], a[232:235], a[192:195], a[8:11]\n" \
X_"a[178:179], v36 offset:33792\n" \
"s_waitcnt lgkmcnt(8)\n" \
M_"a[12:15], a[172:175], a[128:131], a[12:15]\n" \
X_"a[240:241], v36 offset:40960\n" \
"s_waitcnt lgkmcnt(5)\n" \
M_"a[12:15], a[236:239], a[192:195], a[12:15]\n" \
M_"a[28:31], a[172:175], a[132:135], a[28:31]\n" \
M_"a[28:31], a[236:239], a[196:199], a[28:31]\n" \
X_"a[242:243], v36 offset:41984\n" \
M_"a[24:27], a[168:171], a[132:135], a[24:27]\n" \
M_"a[24:27], a[232:235], a[196:199], a[24:27]\n" \
M_"a[20:23], a[164:167], a[132:135], a[20:23]\n" \
M_"a[20:23], a[228:231], a[196:199], a[20:23]\n" \
X_"a[180:181], v37 offset:32768\n" \
M_"a[16:19], a[160:163], a[132:135], a[16:19]\n" \
M_"a[16:19], a[224:227], a[196:199], a[16:19]\n" \
M_"a[32:35], a[160:163], a[136:139], a[32:35]\n" \
X_"a[182:183], v37 offset:33792\n" \
M_"a[32:35], a[224:227], a[200:203], a[32:35]\n" \
M_"a[36:39], a[164:167], a[136:139], a[36:39]\n" \
"v_lshlrev_b32_e32 v45, 3, v20\n" \
"v_add_u32_e32 v45, s31, v45\n" \
M_"a[36:39], a[228:231], a[200:203], a[36:39]\n" \
"s_getpc_b64 s[86:87]\n" \
"s_sub_u32 s86, s86, 0xf800\n" \
"s_subb_u32 s87, s87, 0\n" \
"s_mov_b32 m0, s32\n" \
"s_nop 0\n" \
M_"a[40:43], a[168:171], a[136:139], a[40:43]\n" \
"global_load_lds_dword v45, s[86:87]\n" \
M_"a[40:43], a[232:235], a[200:203], a[40:43]\n" \
X_"a[244:245], v37 offset:40960\n" \
M_"a[44:47], a[172:175], a[136:139], a[44:47]\n" \
X_"a[246:247], v37 offset:41984\n" \
M_"a[44:47], a[236:239], a[200:203], a[44:47]\n" \
M_"a[60:63], a[172:175], a[140:143], a[60:63]\n" \
M_"a[60:63], a[236:239], a[204:207], a[60:63]\n" \
M_"a[56:59], a[168:171], a[140:143], a[56:59]\n" \
X_"a[184:185], v38 offset:32768\n" \
M_"a[56:59], a[232:235], a[204:207], a[56:59]\n" \
M_"a[52:55], a[164:167], a[140:143], a[52:55]\n" \
M_"a[52:55], a[228:231], a[204:207], a[52:55]\n" \
X_"a[186:187], v38 offset:33792\n" \
M_"a[48:51], a[160:163], a[140:143], a[48:51]\n" \
M_"a[48:51], a[224:227], a[204:207], a[48:51]\n" \
M_"a[64:67], a[160:163], a[144:147], a[64:67]\n" \
X_"a[248:249], v38 offset:40960\n" \
M_"a[64:67], a[224:227], a[208:211], a[64:67]\n" \
M_"a[68:71], a[164:167], a[144:147], a[68:71]\n" \
M_"a[68:71], a[228:231], a[208:211], a[68:71]\n" \
X_"a[250:251], v38 offset:41984\n" \
M_"a[72:75], a[168:171], a[144:147], a[72:75]\n" \
M_"a[72:75], a[232:235], a[208:211], a[72:75]\n" \
M_"a[76:79], a[172:175], a[144:147], a[76:79]\n" \
M_"a[76:79], a[236:239], a[208:211], a[76:79]\n" \
X_"a[188:189], v39 offset:32768\n" \
M_"a[92:95], a[172:175], a[148:151], a[92:95]\n" \
M_"a[92:95], a[236:239], a[212:215], a[92:95]\n" \
M_"a[88:91], a[168:171], a[148:151], a[88:91]\n" \
X_"a[190:191], v39 offset:33792\n" \
M_"a[88:91], a[232:235], a[212:215], a[88:91]\n" \
M_"a[84:87], a[164:167], a[148:151], a[84:87]\n" \
M_"a[84:87], a[228:231], a[212:215], a[84:87]\n" \
X_"a[252:253], v39 offset:40960\n" \
M_"a[80:83], a[160:163], a[148:151], a[80:83]\n" \
M_"a[80:83], a[224:227], a[212:215], a[80:83]\n" \
M_"a[96:99], a[160:163], a[152:155], a[96:99]\n" \
X_"a[254:255], v39 offset:41984\n" \
M_"a[96:99], a[224:227], a[216:219], a[96:99]\n" \
M_"a[100:103], a[164:167], a[152:155], a[100:103]\n" \
M_"a[100:103], a[228:231], a[216:219], a[100:103]\n" \
M_"a[104:107], a[168:171], a[152:155], a[104:107]\n" \
M_"a[104:107], a[232:235], a[216:219], a[104:107]\n" \
M_"a[108:111], a[172:175], a[152:155], a[108:111]\n" \
M_"a[108:111], a[236:239], a[216:219], a[108:111]\n" \
"s_waitcnt lgkmcnt(15)\n" \
M_"a[124:127], a[172:175], a[156:159], a[124:127]\n" \
M_"a[124:127], a[236:239], a[220:223], a[124:127]\n" \
M_"a[120:123], a[168:171], a[156:159], a[120:123]\n" \
M_"a[120:123], a[232:235], a[220:223], a[120:123]\n" \
M_"a[116:119], a[164:167], a[156:159], a[116:119]\n" \
M_"a[116:119], a[228:231], a[220:223], a[116:119]\n" \
M_"a[112:115], a[160:163], a[156:159], a[112:115]\n" \
"s_waitcnt vmcnt(1) lgkmcnt(0)\n" \
M_"a[112:115], a[224:227], a[220:223], a[112:115]\n" \
"s_barrier\n" \
"v_mov_b32_e32 v45, s62\n" \
"s_mov_b64 exec, 1\n" \
"buffer_store_dword v45, off, s[80:83], s37 offset:128 sc1\n" \
"s_mov_b64 exec, -1\n" \
M_"v[52:55], a[176:179], a[128:131], v[52:55]\n" \
"s_add_u32 m0, s30, 0x8000\n" \
"s_movk_i32 s84, 0x380\n" \
M_"v[52:55], a[240:243], a[192:195], v[52:55]\n" \
B_"v22, s[64:67], s84 offen offset:0 lds\n" \
M_"v[56:59], a[180:183], a[128:131], v[56:59]\n" \
X_"a[160:161], v32 offset:0\n" \
M_"v[56:59], a[244:247], a[192:195], v[56:59]\n" \
"s_movk_i32 s84, 0x1f80\n" \
M_"v[60:63], a[184:187], a[128:131], v[60:63]\n" \
B_"v23, s[64:67], s84 offen offset:1024 lds\n" \
M_"v[60:63], a[248:251], a[192:195], v[60:63]\n" \
X_"a[162:163], v32 offset:1024\n" \
M_"v[64:67], a[188:191], a[128:131], v[64:67]\n" \
X_"a[224:225], v32 offset:8192\n" \
M_"v[64:67], a[252:255], a[192:195], v[64:67]\n" \
R_"a[128:131], v28 offset:16384\n" \
M_"v[80:83], a[188:191], a[132:135], v[80:83]\n" \
R_"a[192:195], v29 offset:16384\n" \
M_"v[80:83], a[252:255], a[196:199], v[80:83]\n" \
"s_movk_i32 s84, 0x3b80\n" \
M_"v[76:79], a[184:187], a[132:135], v[76:79]\n" \
B_"v22, s[64:67], s84 offen offset:2048 lds\n" \
M_"v[76:79], a[248:251], a[196:199], v[76:79]\n" \
"s_movk_i32 s84, 0x5780\n" \
M_"v[72:75], a[180:183], a[132:135], v[72:75]\n" \
B_"v23, s[64:67], s84 offen offset:3072 lds\n" \
M_"v[72:75], a[244:247], a[196:199], v[72:75]\n" \
"s_add_u32 m0, s31, 0x8000\n" \
"s_mov_b32 s84, 0x380000\n" \
M_"v[68:71], a[176:179], a[132:135], v[68:71]\n" \
B_"v26, s[68:71], s84 offen offset:0 lds\n" \
M_"v[68:71], a[240:243], a[196:199], v[68:71]\n" \
R_"a[132:135], v28 offset:18432\n" \
M_"v[84:87], a[176:179], a[136:139], v[84:87]\n" \
R_"a[196:199], v29 offset:18432\n" \
M_"v[84:87], a[240:243], a[200:203], v[84:87]\n" \
"s_addk_i32 s84, 0x7c00\n" \
M_"v[88:91], a[180:183], a[136:139], v[88:91]\n" \
B_"v26, s[68:71], s84 offen offset:1024 lds\n" \
M_"v[88:91], a[244:247], a[200:203], v[88:91]\n" \
X_"a[226:227], v32 offset:9216\n" \
M_"v[92:95], a[184:187], a[136:139], v[92:95]\n" \
"s_addk_i32 s84, 0x7c00\n" \
M_"v[92:95], a[248:251], a[200:203], v[92:95]\n" \
B_"v27, s[68:71], s84 offen offset:2048 lds\n" \
M_"v[96:99], a[188:191], a[136:139], v[96:99]\n" \
X_"a[164:165], v33 offset:0\n" \
M_"v[96:99], a[252:255], a[200:203], v[96:99]\n" \
R_"a[136:139], v28 offset:20480\n" \
M_"v[112:115], a[188:191], a[140:143], v[112:115]\n" \
R_"a[200:203], v29 offset:20480\n" \
M_"v[112:115], a[252:255], a[204:207], v[112:115]\n" \
"s_addk_i32 s84, 0x7c00\n" \
"s_nop 0\n" \
M_"v[108:111], a[184:187], a[140:143], v[108:111]\n" \
B_"v27, s[68:71], s84 offen offset:3072 lds\n" \
M_"v[108:111], a[248:251], a[204:207], v[108:111]\n" \
"s_add_u32 m0, s30, 0x9000\n" \
"s_mov_b32 s84, 0x8380\n" \
M_"v[104:107], a[180:183], a[140:143], v[104:107]\n" \
B_"v22, s[64:67], s84 offen offset:0 lds\n" \
M_"v[104:107], a[244:247], a[204:207], v[104:107]\n" \
X_"a[166:167], v33 offset:1024\n" \
M_"v[100:103], a[176:179], a[140:143], v[100:103]\n" \
X_"a[228:229], v33 offset:8192\n" \
M_"v[100:103], a[240:243], a[204:207], v[100:103]\n" \
R_"a[140:143], v28 offset:22528\n" \
M_"v[116:119], a[176:179], a[144:147], v[116:119]\n" \
R_"a[204:207], v29 offset:22528\n" \
M_"v[116:119], a[240:243], a[208:211], v[116:119]\n" \
"s_addk_i32 s84, 0x1c00\n" \
M_"v[120:123], a[180:183], a[144:147], v[120:123]\n" \
B_"v23, s[64:67], s84 offen offset:1024 lds\n" \
M_"v[120:123], a[244:247], a[208:211], v[120:123]\n" \
"s_addk_i32 s84, 0x1c00\n" \
M_"v[124:127], a[184:187], a[144:147], v[124:127]\n" \
B_"v22, s[64:67], s84 offen offset:2048 lds\n" \
M_"v[124:127], a[248:251], a[208:211], v[124:127]\n" \
"s_addk_i32 s84, 0x1c00\n" \
"s_nop 0\n" \
M_"v[128:131], a[188:191], a[144:147], v[128:131]\n" \
B_"v23, s[64:67], s84 offen offset:3072 lds\n" \
M_"v[128:131], a[252:255], a[208:211], v[128:131]\n" \
R_"a[144:147], v28 offset:0\n" \
M_"v[144:147], a[188:191], a[148:151], v[144:147]\n" \
R_"a[208:211], v29 offset:0\n" \
M_"v[144:147], a[252:255], a[212:215], v[144:147]\n" \
"s_add_u32 m0, s31, 0xc000\n" \
"s_mov_b32 s84, 0x380000\n" \
M_"v[140:143], a[184:187], a[148:151], v[140:143]\n" \
B_"v26, s[72:75], s84 offen offset:0 lds\n" \
M_"v[140:143], a[248:251], a[212:215], v[140:143]\n" \
X_"a[230:231], v33 offset:9216\n" \
M_"v[136:139], a[180:183], a[148:151], v[136:139]\n" \
"s_addk_i32 s84, 0x7c00\n" \
"s_nop 0\n" \
M_"v[136:139], a[244:247], a[212:215], v[136:139]\n" \
B_"v26, s[72:75], s84 offen offset:1024 lds\n" \
M_"v[132:135], a[176:179], a[148:151], v[132:135]\n" \
X_"a[168:169], v34 offset:0\n" \
M_"v[132:135], a[240:243], a[212:215], v[132:135]\n" \
R_"a[148:151], v28 offset:2048\n" \
M_"v[148:151], a[176:179], a[152:155], v[148:151]\n" \
R_"a[212:215], v29 offset:2048\n" \
M_"v[148:151], a[240:243], a[216:219], v[148:151]\n" \
X_"a[170:171], v34 offset:1024\n" \
M_"v[152:155], a[180:183], a[152:155], v[152:155]\n" \
X_"a[232:233], v34 offset:8192\n" \
M_"v[152:155], a[244:247], a[216:219], v[152:155]\n" \
X_"a[234:235], v34 offset:9216\n" \
M_"v[156:159], a[184:187], a[152:155], v[156:159]\n" \
X_"a[172:173], v35 offset:0\n" \
M_"v[156:159], a[248:251], a[216:219], v[156:159]\n" \
X_"a[174:175], v35 offset:1024\n" \
M_"v[160:163], a[188:191], a[152:155], v[160:163]\n" \
X_"a[236:237], v35 offset:8192\n" \
M_"v[160:163], a[252:255], a[216:219], v[160:163]\n" \
R_"a[152:155], v28 offset:4096\n" \
M_"v[176:179], a[188:191], a[156:159], v[176:179]\n" \
R_"a[216:219], v29 offset:4096\n" \
M_"v[176:179], a[252:255], a[220:223], v[176:179]\n" \
X_"a[238:239], v35 offset:9216\n" \
M_"v[172:175], a[184:187], a[156:159], v[172:175]\n" \
M_"v[172:175], a[248:251], a[220:223], v[172:175]\n" \
M_"v[168:171], a[180:183], a[156:159], v[168:171]\n" \
M_"v[168:171], a[244:247], a[220:223], v[168:171]\n" \
M_"v[164:167], a[176:179], a[156:159], v[164:167]\n" \
M_"v[164:167], a[240:243], a[220:223], v[164:167]\n" \
R_"a[156:159], v28 offset:6144\n" \
R_"a[220:223], v29 offset:6144\n" \
"s_waitcnt lgkmcnt(15)\n" \
M_"a[0:3], a[160:163], a[128:131], a[0:3]\n" \
"s_addk_i32 s84, 0x7c00\n" \
M_"a[0:3], a[224:227], a[192:195], a[0:3]\n" \
B_"v27, s[72:75], s84 offen offset:2048 lds\n" \
M_"a[4:7], a[164:167], a[128:131], a[4:7]\n" \
X_"a[176:177], v36 offset:0\n" \
"s_waitcnt lgkmcnt(15)\n" \
M_"a[4:7], a[228:231], a[192:195], a[4:7]\n" \
"s_addk_i32 s84, 0x7c00\n" \
"s_waitcnt lgkmcnt(11)\n" \
M_"a[8:11], a[168:171], a[128:131], a[8:11]\n" \
B_"v27, s[72:75], s84 offen offset:3072 lds\n" \
"s_waitcnt lgkmcnt(9)\n" \
M_"a[8:11], a[232:235], a[192:195], a[8:11]\n" \
X_"a[178:179], v36 offset:1024\n" \
"s_waitcnt lgkmcnt(8)\n" \
M_"a[12:15], a[172:175], a[128:131], a[12:15]\n" \
X_"a[240:241], v36 offset:8192\n" \
"s_waitcnt lgkmcnt(5)\n" \
M_"a[12:15], a[236:239], a[192:195], a[12:15]\n" \
M_"a[28:31], a[172:175], a[132:135], a[28:31]\n" \
M_"a[28:31], a[236:239], a[196:199], a[28:31]\n" \
X_"a[242:243], v36 offset:9216\n" \
M_"a[24:27], a[168:171], a[132:135], a[24:27]\n" \
M_"a[24:27], a[232:235], a[196:199], a[24:27]\n" \
M_"a[20:23], a[164:167], a[132:135], a[20:23]\n" \
M_"a[20:23], a[228:231], a[196:199], a[20:23]\n" \
X_"a[180:181], v37 offset:0\n" \
M_"a[16:19], a[160:163], a[132:135], a[16:19]\n" \
M_"a[16:19], a[224:227], a[196:199], a[16:19]\n" \
M_"a[32:35], a[160:163], a[136:139], a[32:35]\n" \
"v_bfe_u32 v44, v20, 2, 5\n" \
X_"a[182:183], v37 offset:1024\n" \
M_"a[32:35], a[224:227], a[200:203], a[32:35]\n" \
"buffer_load_dword v44, v44, s[80:83], s38 offen offset:128 sc1\n" \
M_"a[36:39], a[164:167], a[136:139], a[36:39]\n" \
M_"a[36:39], a[228:231], a[200:203], a[36:39]\n" \
X_"a[244:245], v37 offset:8192\n" \
M_"a[40:43], a[168:171], a[136:139], a[40:43]\n" \
M_"a[40:43], a[232:235], a[200:203], a[40:43]\n" \
M_"a[44:47], a[172:175], a[136:139], a[44:47]\n" \
X_"a[246:247], v37 offset:9216\n" \
M_"a[44:47], a[236:239], a[200:203], a[44:47]\n" \
M_"a[60:63], a[172:175], a[140:143], a[60:63]\n" \
"global_store_dwordx4 v49, v[0:3], s[28:29] offset:0 sc1\n" \
M_"a[60:63], a[236:239], a[204:207], a[60:63]\n" \
M_"a[56:59], a[168:171], a[140:143], a[56:59]\n" \
X_"a[184:185], v38 offset:0\n" \
M_"a[56:59], a[232:235], a[204:207], a[56:59]\n" \
M_"a[52:55], a[164:167], a[140:143], a[52:55]\n" \
M_"a[52:55], a[228:231], a[204:207], a[52:55]\n" \
X_"a[186:187], v38 offset:1024\n" \
M_"a[48:51], a[160:163], a[140:143], a[48:51]\n" \
M_"a[48:51], a[224:227], a[204:207], a[48:51]\n" \
M_"a[64:67], a[160:163], a[144:147], a[64:67]\n" \
"global_store_dwordx4 v49, v[0:3], s[28:29] offset:1024 sc1\n" \
M_"a[64:67], a[224:227], a[208:211], a[64:67]\n" \
X_"a[248:249], v38 offset:8192\n" \
M_"a[68:71], a[164:167], a[144:147], a[68:71]\n" \
M_"a[68:71], a[228:231], a[208:211], a[68:71]\n" \
X_"a[250:251], v38 offset:9216\n" \
M_"a[72:75], a[168:171], a[144:147], a[72:75]\n" \
M_"a[72:75], a[232:235], a[208:211], a[72:75]\n" \
M_"a[76:79], a[172:175], a[144:147], a[76:79]\n" \
M_"a[76:79], a[236:239], a[208:211], a[76:79]\n" \
X_"a[188:189], v39 offset:0\n" \
M_"a[92:95], a[172:175], a[148:151], a[92:95]\n" \
"s_mov_b64 exec, 0xffff\n" \
"global_store_dwordx4 v49, v[0:3], s[28:29] offset:2048 sc1\n" \
"s_mov_b64 exec, -1\n" \
"s_nop 0\n" \
M_"a[92:95], a[236:239], a[212:215], a[92:95]\n" \
M_"a[88:91], a[168:171], a[148:151], a[88:91]\n" \
X_"a[190:191], v39 offset:1024\n" \
M_"a[88:91], a[232:235], a[212:215], a[88:91]\n" \
M_"a[84:87], a[164:167], a[148:151], a[84:87]\n" \
M_"a[84:87], a[228:231], a[212:215], a[84:87]\n" \
".fill (L_pb + 16096 - .) / 4, 4, 0xBF800000\n" \
"L_pp4:\n" \
"s_cbranch_execz L_pret\n" \
"s_nop 0\n" \
X_"a[252:253], v39 offset:8192\n" \
M_"a[80:83], a[160:163], a[148:151], a[80:83]\n" \
M_"a[80:83], a[224:227], a[212:215], a[80:83]\n" \
M_"a[96:99], a[160:163], a[152:155], a[96:99]\n" \
X_"a[254:255], v39 offset:9216\n" \
M_"a[96:99], a[224:227], a[216:219], a[96:99]\n" \
M_"a[100:103], a[164:167], a[152:155], a[100:103]\n" \
M_"a[100:103], a[228:231], a[216:219], a[100:103]\n" \
M_"a[104:107], a[168:171], a[152:155], a[104:107]\n" \
M_"a[104:107], a[232:235], a[216:219], a[104:107]\n" \
M_"a[108:111], a[172:175], a[152:155], a[108:111]\n" \
M_"a[108:111], a[236:239], a[216:219], a[108:111]\n" \
"s_waitcnt lgkmcnt(15)\n" \
M_"a[124:127], a[172:175], a[156:159], a[124:127]\n" \
M_"a[124:127], a[236:239], a[220:223], a[124:127]\n" \
M_"a[120:123], a[168:171], a[156:159], a[120:123]\n" \
M_"a[120:123], a[232:235], a[220:223], a[120:123]\n" \
M_"a[116:119], a[164:167], a[156:159], a[116:119]\n" \
M_"a[116:119], a[228:231], a[220:223], a[116:119]\n" \
M_"a[112:115], a[160:163], a[156:159], a[112:115]\n" \
"s_waitcnt vmcnt(3) lgkmcnt(0)\n" \
M_"a[112:115], a[224:227], a[220:223], a[112:115]\n" \
"s_barrier\n" \
"v_cmp_ne_u32_e32 vcc, s62, v44\n" \
"s_nop 4\n" \
"s_cbranch_vccnz L_poll60_%=\n" \
"L_pollr60_%=:\n" \
"s_mov_b32 s68, s12\n" \
"s_and_b32 s69, s13, 0xffff\n" \
"s_mov_b32 s70, 0x480000\n" \
"s_mov_b32 s71, 0x20000\n" \
M_"v[52:55], a[176:179], a[128:131], v[52:55]\n" \
"s_add_u32 m0, s40, 0x0\n" \
"s_add_u32 s85, s39, 0x0\n" \
"s_nop 0\n" \
M_"v[52:55], a[240:243], a[192:195], v[52:55]\n" \
B_"v20, s[80:83], s85 offen sc1 lds\n" \
M_"v[56:59], a[180:183], a[128:131], v[56:59]\n" \
X_"a[160:161], v32 offset:32768\n" \
M_"v[56:59], a[244:247], a[192:195], v[56:59]\n" \
"s_add_u32 m0, s40, 0x1000\n" \
"s_add_u32 s85, s39, 0x1000\n" \
M_"v[60:63], a[184:187], a[128:131], v[60:63]\n" \
B_"v20, s[80:83], s85 offen sc1 lds\n" \
M_"v[60:63], a[248:251], a[192:195], v[60:63]\n" \
X_"a[162:163], v32 offset:33792\n" \
M_"v[64:67], a[188:191], a[128:131], v[64:67]\n" \
X_"a[224:225], v32 offset:40960\n" \
M_"v[64:67], a[252:255], a[192:195], v[64:67]\n" \
R_"a[128:131], v28 offset:49152\n" \
M_"v[80:83], a[188:191], a[132:135], v[80:83]\n" \
R_"a[192:195], v29 offset:49152\n" \
M_"v[80:83], a[252:255], a[196:199], v[80:83]\n" \
"s_add_u32 m0, s40, 0x3000\n" \
"s_movk_i32 s84, 0x0\n" \
M_"v[76:79], a[184:187], a[132:135], v[76:79]\n" \
B_"v21, s[76:79], s84 offen offset:0 lds\n" \
M_"v[76:79], a[248:251], a[196:199], v[76:79]\n" \
"s_add_u32 m0, s30, 0x0\n" \
"s_movk_i32 s84, 0x0\n" \
"s_nop 0\n" \
M_"v[72:75], a[180:183], a[132:135], v[72:75]\n" \
B_"v24, s[76:79], s84 offen offset:0 lds\n" \
M_"v[72:75], a[244:247], a[196:199], v[72:75]\n" \
X_"a[226:227], v32 offset:41984\n" \
M_"v[68:71], a[176:179], a[132:135], v[68:71]\n" \
X_"a[164:165], v33 offset:32768\n" \
M_"v[68:71], a[240:243], a[196:199], v[68:71]\n" \
R_"a[132:135], v28 offset:51200\n" \
M_"v[84:87], a[176:179], a[136:139], v[84:87]\n" \
R_"a[196:199], v29 offset:51200\n" \
M_"v[84:87], a[240:243], a[200:203], v[84:87]\n" \
"s_movk_i32 s84, 0x2000\n" \
M_"v[88:91], a[180:183], a[136:139], v[88:91]\n" \
B_"v25, s[76:79], s84 offen offset:1024 lds\n" \
M_"v[88:91], a[244:247], a[200:203], v[88:91]\n" \
"s_movk_i32 s84, 0x4000\n" \
M_"v[92:95], a[184:187], a[136:139], v[92:95]\n" \
B_"v24, s[76:79], s84 offen offset:2048 lds\n" \
M_"v[92:95], a[248:251], a[200:203], v[92:95]\n" \
X_"a[166:167], v33 offset:33792\n" \
M_"v[96:99], a[188:191], a[136:139], v[96:99]\n" \
X_"a[228:229], v33 offset:40960\n" \
M_"v[96:99], a[252:255], a[200:203], v[96:99]\n" \
R_"a[136:139], v28 offset:53248\n" \
M_"v[112:115], a[188:191], a[140:143], v[112:115]\n" \
R_"a[200:203], v29 offset:53248\n" \
M_"v[112:115], a[252:255], a[204:207], v[112:115]\n" \
"s_movk_i32 s84, 0x6000\n" \
M_"v[108:111], a[184:187], a[140:143], v[108:111]\n" \
B_"v25, s[76:79], s84 offen offset:3072 lds\n" \
M_"v[108:111], a[248:251], a[204:207], v[108:111]\n" \
"s_add_u32 m0, s31, 0x0\n" \
"s_movk_i32 s84, 0x0\n" \
M_"v[104:107], a[180:183], a[140:143], v[104:107]\n" \
B_"v26, s[68:71], s84 offen offset:0 lds\n" \
M_"v[104:107], a[244:247], a[204:207], v[104:107]\n" \
"s_movk_i32 s84, 0x7c00\n" \
M_"v[100:103], a[176:179], a[140:143], v[100:103]\n" \
B_"v26, s[68:71], s84 offen offset:1024 lds\n" \
M_"v[100:103], a[240:243], a[204:207], v[100:103]\n" \
R_"a[140:143], v28 offset:55296\n" \
M_"v[116:119], a[176:179], a[144:147], v[116:119]\n" \
R_"a[204:207], v29 offset:55296\n" \
M_"v[116:119], a[240:243], a[208:211], v[116:119]\n" \
"s_addk_i32 s84, 0x7c00\n" \
M_"v[120:123], a[180:183], a[144:147], v[120:123]\n" \
B_"v27, s[68:71], s84 offen offset:2048 lds\n" \
M_"v[120:123], a[244:247], a[208:211], v[120:123]\n" \
X_"a[230:231], v33 offset:41984\n" \
M_"v[124:127], a[184:187], a[144:147], v[124:127]\n" \
"s_addk_i32 s84, 0x7c00\n" \
M_"v[124:127], a[248:251], a[208:211], v[124:127]\n" \
B_"v27, s[68:71], s84 offen offset:3072 lds\n" \
M_"v[128:131], a[188:191], a[144:147], v[128:131]\n" \
X_"a[168:169], v34 offset:32768\n" \
M_"v[128:131], a[252:255], a[208:211], v[128:131]\n" \
R_"a[144:147], v28 offset:32768\n" \
M_"v[144:147], a[188:191], a[148:151], v[144:147]\n" \
R_"a[208:211], v29 offset:32768\n" \
M_"v[144:147], a[252:255], a[212:215], v[144:147]\n" \
"s_add_u32 m0, s30, 0x1000\n" \
"s_mov_b32 s84, 0x9000\n" \
M_"v[140:143], a[184:187], a[148:151], v[140:143]\n" \
B_"v24, s[76:79], s84 offen offset:0 lds\n" \
M_"v[140:143], a[248:251], a[212:215], v[140:143]\n" \
X_"a[170:171], v34 offset:33792\n" \
M_"v[136:139], a[180:183], a[148:151], v[136:139]\n" \
"s_addk_i32 s84, 0x2000\n" \
"s_nop 0\n" \
M_"v[136:139], a[244:247], a[212:215], v[136:139]\n" \
B_"v25, s[76:79], s84 offen offset:1024 lds\n" \
M_"v[132:135], a[176:179], a[148:151], v[132:135]\n" \
X_"a[232:233], v34 offset:40960\n" \
M_"v[132:135], a[240:243], a[212:215], v[132:135]\n" \
R_"a[148:151], v28 offset:34816\n" \
M_"v[148:151], a[176:179], a[152:155], v[148:151]\n" \
R_"a[212:215], v29 offset:34816\n" \
M_"v[148:151], a[240:243], a[216:219], v[148:151]\n" \
X_"a[234:235], v34 offset:41984\n" \
M_"v[152:155], a[180:183], a[152:155], v[152:155]\n" \
X_"a[172:173], v35 offset:32768\n" \
M_"v[152:155], a[244:247], a[216:219], v[152:155]\n" \
X_"a[174:175], v35 offset:33792\n" \
M_"v[156:159], a[184:187], a[152:155], v[156:159]\n" \
X_"a[236:237], v35 offset:40960\n" \
M_"v[156:159], a[248:251], a[216:219], v[156:159]\n" \
X_"a[238:239], v35 offset:41984\n" \
M_"v[160:163], a[188:191], a[152:155], v[160:163]\n" \
M_"v[160:163], a[252:255], a[216:219], v[160:163]\n" \
R_"a[152:155], v28 offset:36864\n" \
M_"v[176:179], a[188:191], a[156:159], v[176:179]\n" \
R_"a[216:219], v29 offset:36864\n" \
M_"v[176:179], a[252:255], a[220:223], v[176:179]\n" \
M_"v[172:175], a[184:187], a[156:159], v[172:175]\n" \
M_"v[172:175], a[248:251], a[220:223], v[172:175]\n" \
M_"v[168:171], a[180:183], a[156:159], v[168:171]\n" \
M_"v[168:171], a[244:247], a[220:223], v[168:171]\n" \
M_"v[164:167], a[176:179], a[156:159], v[164:167]\n" \
M_"v[164:167], a[240:243], a[220:223], v[164:167]\n" \
R_"a[156:159], v28 offset:38912\n" \
R_"a[220:223], v29 offset:38912\n" \
"s_waitcnt lgkmcnt(15)\n" \
M_"a[0:3], a[160:163], a[128:131], a[0:3]\n" \
"s_addk_i32 s84, 0x2000\n" \
M_"a[0:3], a[224:227], a[192:195], a[0:3]\n" \
B_"v24, s[76:79], s84 offen offset:2048 lds\n" \
M_"a[4:7], a[164:167], a[128:131], a[4:7]\n" \
X_"a[176:177], v36 offset:32768\n" \
M_"a[4:7], a[228:231], a[192:195], a[4:7]\n" \
"s_addk_i32 s84, 0x2000\n" \
"s_waitcnt lgkmcnt(13)\n" \
M_"a[8:11], a[168:171], a[128:131], a[8:11]\n" \
B_"v25, s[76:79], s84 offen offset:3072 lds\n" \
"s_waitcnt lgkmcnt(9)\n" \
M_"a[8:11], a[232:235], a[192:195], a[8:11]\n" \
X_"a[178:179], v36 offset:33792\n" \
"s_waitcnt lgkmcnt(8)\n" \
M_"a[12:15], a[172:175], a[128:131], a[12:15]\n" \
X_"a[240:241], v36 offset:40960\n" \
"v_accvgpr_read_b32 v184, a0\n" \
"s_waitcnt lgkmcnt(7)\n" \
M_"a[12:15], a[236:239], a[192:195], a[12:15]\n" \
"v_accvgpr_read_b32 v185, a1\n" \
"v_accvgpr_read_b32 v186, a2\n" \
M_"a[28:31], a[172:175], a[132:135], a[28:31]\n" \
"v_accvgpr_read_b32 v187, a3\n" \
"v_accvgpr_read_b32 v180, a4\n" \
M_"a[28:31], a[236:239], a[196:199], a[28:31]\n" \
"s_add_u32 m0, s30, 0x20000\n" \
"s_movk_i32 s84, 0x100\n" \
M_"a[24:27], a[168:171], a[132:135], a[24:27]\n" \
B_"v24, s[76:79], s84 offen offset:0 lds\n" \
M_"a[24:27], a[232:235], a[196:199], a[24:27]\n" \
X_"a[242:243], v36 offset:41984\n" \
"v_accvgpr_read_b32 v181, a5\n" \
M_"a[20:23], a[164:167], a[132:135], a[20:23]\n" \
"v_accvgpr_read_b32 v182, a6\n" \
"v_accvgpr_read_b32 v183, a7\n" \
M_"a[20:23], a[228:231], a[196:199], a[20:23]\n" \
X_"a[180:181], v37 offset:32768\n" \
C_"v184, v184, v185\n" \
M_"a[16:19], a[160:163], a[132:135], a[16:19]\n" \
"s_movk_i32 s84, 0x2100\n" \
"s_nop 0\n" \
M_"a[16:19], a[224:227], a[196:199], a[16:19]\n" \
B_"v25, s[76:79], s84 offen offset:1024 lds\n" \
M_"a[32:35], a[160:163], a[136:139], a[32:35]\n" \
X_"a[182:183], v37 offset:33792\n" \
C_"v185, v186, v187\n" \
M_"a[32:35], a[224:227], a[200:203], a[32:35]\n" \
C_"v186, v180, v181\n" \
C_"v187, v182, v183\n" \
M_"a[36:39], a[164:167], a[136:139], a[36:39]\n" \
"v_accvgpr_read_b32 v188, a8\n" \
"v_accvgpr_read_b32 v189, a9\n" \
M_"a[36:39], a[228:231], a[200:203], a[36:39]\n" \
"s_movk_i32 s84, 0x4100\n" \
"s_nop 0\n" \
M_"a[40:43], a[168:171], a[136:139], a[40:43]\n" \
B_"v24, s[76:79], s84 offen offset:2048 lds\n" \
M_"a[40:43], a[232:235], a[200:203], a[40:43]\n" \
X_"a[244:245], v37 offset:40960\n" \
"v_accvgpr_read_b32 v190, a10\n" \
M_"a[44:47], a[172:175], a[136:139], a[44:47]\n" \
X_"a[246:247], v37 offset:41984\n" \
"v_accvgpr_read_b32 v191, a11\n" \
M_"a[44:47], a[236:239], a[200:203], a[44:47]\n" \
"v_accvgpr_read_b32 v180, a12\n" \
"v_accvgpr_read_b32 v181, a13\n" \
M_"a[60:63], a[172:175], a[140:143], a[60:63]\n" \
"s_movk_i32 s84, 0x6100\n" \
M_"a[60:63], a[236:239], a[204:207], a[60:63]\n" \
B_"v25, s[76:79], s84 offen offset:3072 lds\n" \
M_"a[56:59], a[168:171], a[140:143], a[56:59]\n" \
X_"a[184:185], v38 offset:32768\n" \
"v_accvgpr_read_b32 v182, a14\n" \
M_"a[56:59], a[232:235], a[204:207], a[56:59]\n" \
"v_accvgpr_read_b32 v183, a15\n" \
C_"v188, v188, v189\n" \
M_"a[52:55], a[164:167], a[140:143], a[52:55]\n" \
"s_add_u32 m0, s30, 0x21000\n" \
"s_addk_i32 s84, 0x3000\n" \
M_"a[52:55], a[228:231], a[204:207], a[52:55]\n" \
B_"v24, s[76:79], s84 offen offset:0 lds\n" \
M_"a[48:51], a[160:163], a[140:143], a[48:51]\n" \
X_"a[186:187], v38 offset:33792\n" \
C_"v189, v190, v191\n" \
M_"a[48:51], a[224:227], a[204:207], a[48:51]\n" \
C_"v190, v180, v181\n" \
C_"v191, v182, v183\n" \
M_"a[64:67], a[160:163], a[144:147], a[64:67]\n" \
X_"a[248:249], v38 offset:40960\n" \
"v_accvgpr_read_b32 v192, a24\n" \
M_"a[64:67], a[224:227], a[208:211], a[64:67]\n" \
"s_addk_i32 s84, 0x2000\n" \
"s_nop 0\n" \
M_"a[68:71], a[164:167], a[144:147], a[68:71]\n" \
B_"v25, s[76:79], s84 offen offset:1024 lds\n" \
M_"a[68:71], a[228:231], a[208:211], a[68:71]\n" \
X_"a[250:251], v38 offset:41984\n" \
"v_accvgpr_read_b32 v193, a25\n" \
M_"a[72:75], a[168:171], a[144:147], a[72:75]\n" \
"v_accvgpr_read_b32 v194, a26\n" \
"v_accvgpr_read_b32 v195, a27\n" \
M_"a[72:75], a[232:235], a[208:211], a[72:75]\n" \
"v_accvgpr_read_b32 v180, a28\n" \
"v_accvgpr_read_b32 v181, a29\n" \
M_"a[76:79], a[172:175], a[144:147], a[76:79]\n" \
"s_addk_i32 s84, 0x2000\n" \
M_"a[76:79], a[236:239], a[208:211], a[76:79]\n" \
B_"v24, s[76:79], s84 offen offset:2048 lds\n" \
M_"a[92:95], a[172:175], a[148:151], a[92:95]\n" \
"s_add_u32 s89, s41, 0x0\n" \
"buffer_store_dwordx4 v[184:187], v20, s[80:83], s89 offen offset:0 sc1\n" \
M_"a[92:95], a[236:239], a[212:215], a[92:95]\n" \
X_"a[188:189], v39 offset:32768\n" \
"v_accvgpr_read_b32 v182, a30\n" \
M_"a[88:91], a[168:171], a[148:151], a[88:91]\n" \
X_"a[190:191], v39 offset:33792\n" \
"v_accvgpr_read_b32 v183, a31\n" \
M_"a[88:91], a[232:235], a[212:215], a[88:91]\n" \
"s_addk_i32 s84, 0x2000\n" \
M_"a[84:87], a[164:167], a[148:151], a[84:87]\n" \
B_"v25, s[76:79], s84 offen offset:3072 lds\n" \
M_"a[84:87], a[228:231], a[212:215], a[84:87]\n" \
X_"a[252:253], v39 offset:40960\n" \
C_"v192, v192, v193\n" \
M_"a[80:83], a[160:163], a[148:151], a[80:83]\n" \
"s_add_u32 s88, s41, 0x0\n" \
"buffer_store_dwordx4 v[188:191], v20, s[80:83], s88 offen offset:1024 sc1\n" \
M_"a[80:83], a[224:227], a[212:215], a[80:83]\n" \
C_"v193, v194, v195\n" \
C_"v194, v180, v181\n" \
"s_waitcnt lgkmcnt(15)\n" \
"s_nop 0\n" \
M_"a[96:99], a[160:163], a[152:155], a[96:99]\n" \
X_"a[254:255], v39 offset:41984\n" \
C_"v195, v182, v183\n" \
M_"a[96:99], a[224:227], a[216:219], a[96:99]\n" \
"v_accvgpr_read_b32 v196, a16\n" \
"v_accvgpr_read_b32 v197, a17\n" \
M_"a[100:103], a[164:167], a[152:155], a[100:103]\n" \
"v_accvgpr_read_b32 v198, a18\n" \
"v_accvgpr_read_b32 v199, a19\n" \
M_"a[100:103], a[228:231], a[216:219], a[100:103]\n" \
"v_accvgpr_read_b32 v180, a20\n" \
"v_accvgpr_read_b32 v181, a21\n" \
M_"a[104:107], a[168:171], a[152:155], a[104:107]\n" \
"s_add_u32 s89, s41, 0x1000\n" \
"buffer_store_dwordx4 v[192:195], v20, s[80:83], s89 offen offset:1024 sc1\n" \
M_"a[104:107], a[232:235], a[216:219], a[104:107]\n" \
"v_accvgpr_read_b32 v182, a22\n" \
"v_accvgpr_read_b32 v183, a23\n" \
M_"a[108:111], a[172:175], a[152:155], a[108:111]\n" \
C_"v196, v196, v197\n" \
C_"v197, v198, v199\n" \
M_"a[108:111], a[236:239], a[216:219], a[108:111]\n" \
C_"v198, v180, v181\n" \
C_"v199, v182, v183\n" \
M_"a[124:127], a[172:175], a[156:159], a[124:127]\n" \
"v_accvgpr_read_b32 v200, a32\n" \
"v_accvgpr_read_b32 v201, a33\n" \
M_"a[124:127], a[236:239], a[220:223], a[124:127]\n" \
"v_accvgpr_read_b32 v202, a34\n" \
"v_accvgpr_read_b32 v203, a35\n" \
M_"a[120:123], a[168:171], a[156:159], a[120:123]\n" \
"s_add_u32 s88, s41, 0x1000\n" \
"buffer_store_dwordx4 v[196:199], v20, s[80:83], s88 offen offset:0 sc1\n" \
M_"a[120:123], a[232:235], a[220:223], a[120:123]\n" \
"v_accvgpr_read_b32 v180, a36\n" \
"v_accvgpr_read_b32 v181, a37\n" \
M_"a[116:119], a[164:167], a[156:159], a[116:119]\n" \
"v_accvgpr_read_b32 v182, a38\n" \
"v_accvgpr_read_b32 v183, a39\n" \
M_"a[116:119], a[228:231], a[220:223], a[116:119]\n" \
C_"v200, v200, v201\n" \
C_"v201, v202, v203\n" \
M_"a[112:115], a[160:163], a[156:159], a[112:115]\n" \
C_"v202, v180, v181\n" \
C_"v203, v182, v183\n" \
M_"a[112:115], a[224:227], a[220:223], a[112:115]\n" \
"v_accvgpr_read_b32 v204, a40\n" \
"v_accvgpr_read_b32 v205, a41\n" \
"s_waitcnt vmcnt(12) lgkmcnt(0)\n" \
"s_barrier\n" \
"v_mov_b32_e32 v36, v32\n" \
"v_xor_b32_e32 v32, 0x0, v48\n" \
"v_mov_b32_e32 v37, v33\n" \
"v_xor_b32_e32 v33, 0x20, v48\n" \
"v_mov_b32_e32 v38, v34\n" \
"v_xor_b32_e32 v34, 0x40, v48\n" \
"v_mov_b32_e64 v39, v35\n" \
"v_xor_b32_e32 v35, 0x60, v48\n" \
"v_add_u32_e32 v22, s44, v28\n" \
"v_add_u32_e32 v28, s43, v28\n" \
"v_add_u32_e32 v23, s44, v29\n" \
"v_add_u32_e32 v29, s43, v29\n" \
M_"v[52:55], a[176:179], a[128:131], v[52:55]\n" \
"s_add_u32 m0, s40, 0x8000\n" \
"s_add_u32 s85, s39, 0x2000\n" \
M_"v[52:55], a[240:243], a[192:195], v[52:55]\n" \
B_"v20, s[80:83], s85 offen sc1 lds\n" \
M_"v[56:59], a[180:183], a[128:131], v[56:59]\n" \
X_"a[160:161], v32 offset:0\n" \
"v_accvgpr_read_b32 v206, a42\n" \
M_"v[56:59], a[244:247], a[192:195], v[56:59]\n" \
"s_add_u32 m0, s40, 0x9000\n" \
"s_add_u32 s85, s39, 0x3000\n" \
M_"v[60:63], a[184:187], a[128:131], v[60:63]\n" \
B_"v20, s[80:83], s85 offen sc1 lds\n" \
M_"v[60:63], a[248:251], a[192:195], v[60:63]\n" \
X_"a[162:163], v32 offset:512\n" \
"v_accvgpr_read_b32 v207, a43\n" \
M_"v[64:67], a[188:191], a[128:131], v[64:67]\n" \
X_"a[224:225], v32 offset:4096\n" \
"v_accvgpr_read_b32 v180, a44\n" \
M_"v[64:67], a[252:255], a[192:195], v[64:67]\n" \
R_"a[128:131], v28 offset:0\n" \
"v_accvgpr_read_b32 v181, a45\n" \
M_"v[80:83], a[188:191], a[132:135], v[80:83]\n" \
R_"a[192:195], v29 offset:0\n" \
"v_accvgpr_read_b32 v182, a46\n" \
M_"v[80:83], a[252:255], a[196:199], v[80:83]\n" \
"s_add_u32 m0, s40, 0xb000\n" \
"s_movk_i32 s84, 0x80\n" \
M_"v[76:79], a[184:187], a[132:135], v[76:79]\n" \
B_"v21, s[76:79], s84 offen offset:0 lds\n" \
M_"v[76:79], a[248:251], a[196:199], v[76:79]\n" \
"s_add_u32 m0, s30, 0x8000\n" \
"s_movk_i32 s84, 0x80\n" \
M_"v[72:75], a[180:183], a[132:135], v[72:75]\n" \
B_"v24, s[76:79], s84 offen offset:0 lds\n" \
M_"v[72:75], a[244:247], a[196:199], v[72:75]\n" \
X_"a[226:227], v32 offset:4608\n" \
"v_accvgpr_read_b32 v183, a47\n" \
M_"v[68:71], a[176:179], a[132:135], v[68:71]\n" \
X_"a[164:165], v33 offset:0\n" \
C_"v204, v204, v205\n" \
M_"v[68:71], a[240:243], a[196:199], v[68:71]\n" \
R_"a[132:135], v28 offset:2048\n" \
C_"v205, v206, v207\n" \
M_"v[84:87], a[176:179], a[136:139], v[84:87]\n" \
R_"a[196:199], v29 offset:2048\n" \
C_"v206, v180, v181\n" \
M_"v[84:87], a[240:243], a[200:203], v[84:87]\n" \
"s_movk_i32 s84, 0x2080\n" \
M_"v[88:91], a[180:183], a[136:139], v[88:91]\n" \
B_"v25, s[76:79], s84 offen offset:1024 lds\n" \
M_"v[88:91], a[244:247], a[200:203], v[88:91]\n" \
"s_movk_i32 s84, 0x4080\n" \
M_"v[92:95], a[184:187], a[136:139], v[92:95]\n" \
B_"v24, s[76:79], s84 offen offset:2048 lds\n" \
M_"v[92:95], a[248:251], a[200:203], v[92:95]\n" \
X_"a[166:167], v33 offset:512\n" \
C_"v207, v182, v183\n" \
M_"v[96:99], a[188:191], a[136:139], v[96:99]\n" \
X_"a[228:229], v33 offset:4096\n" \
"v_accvgpr_read_b32 v208, a56\n" \
M_"v[96:99], a[252:255], a[200:203], v[96:99]\n" \
R_"a[136:139], v28 offset:4096\n" \
"v_accvgpr_read_b32 v209, a57\n" \
M_"v[112:115], a[188:191], a[140:143], v[112:115]\n" \
R_"a[200:203], v29 offset:4096\n" \
"v_accvgpr_read_b32 v210, a58\n" \
M_"v[112:115], a[252:255], a[204:207], v[112:115]\n" \
"s_movk_i32 s84, 0x6080\n" \
M_"v[108:111], a[184:187], a[140:143], v[108:111]\n" \
B_"v25, s[76:79], s84 offen offset:3072 lds\n" \
M_"v[108:111], a[248:251], a[204:207], v[108:111]\n" \
"s_add_u32 m0, s31, 0x8000\n" \
"s_mov_b32 s84, 0x80000\n" \
M_"v[104:107], a[180:183], a[140:143], v[104:107]\n" \
B_"v26, s[68:71], s84 offen offset:0 lds\n" \
M_"v[104:107], a[244:247], a[204:207], v[104:107]\n" \
"s_addk_i32 s84, 0x7c00\n" \
M_"v[100:103], a[176:179], a[140:143], v[100:103]\n" \
B_"v26, s[68:71], s84 offen offset:1024 lds\n" \
M_"v[100:103], a[240:243], a[204:207], v[100:103]\n" \
R_"a[140:143], v28 offset:6144\n" \
"v_accvgpr_read_b32 v211, a59\n" \
M_"v[116:119], a[176:179], a[144:147], v[116:119]\n" \
R_"a[204:207], v29 offset:6144\n" \
"v_accvgpr_read_b32 v180, a60\n" \
M_"v[116:119], a[240:243], a[208:211], v[116:119]\n" \
"s_addk_i32 s84, 0x7c00\n" \
M_"v[120:123], a[180:183], a[144:147], v[120:123]\n" \
B_"v27, s[68:71], s84 offen offset:2048 lds\n" \
M_"v[120:123], a[244:247], a[208:211], v[120:123]\n" \
X_"a[230:231], v33 offset:4608\n" \
"v_accvgpr_read_b32 v181, a61\n" \
M_"v[124:127], a[184:187], a[144:147], v[124:127]\n" \
"s_addk_i32 s84, 0x7c00\n" \
M_"v[124:127], a[248:251], a[208:211], v[124:127]\n" \
B_"v27, s[68:71], s84 offen offset:3072 lds\n" \
M_"v[128:131], a[188:191], a[144:147], v[128:131]\n" \
X_"a[172:173], v35 offset:0\n" \
"v_accvgpr_read_b32 v182, a62\n" \
M_"v[128:131], a[252:255], a[208:211], v[128:131]\n" \
R_"a[144:147], v28 offset:8192\n" \
"v_accvgpr_read_b32 v183, a63\n" \
M_"v[144:147], a[188:191], a[148:151], v[144:147]\n" \
R_"a[208:211], v29 offset:8192\n" \
C_"v208, v208, v209\n" \
M_"v[144:147], a[252:255], a[212:215], v[144:147]\n" \
"s_add_u32 m0, s30, 0x9000\n" \
"s_mov_b32 s84, 0x9080\n" \
M_"v[140:143], a[184:187], a[148:151], v[140:143]\n" \
B_"v24, s[76:79], s84 offen offset:0 lds\n" \
M_"v[140:143], a[248:251], a[212:215], v[140:143]\n" \
X_"a[174:175], v35 offset:512\n" \
C_"v209, v210, v211\n" \
M_"v[136:139], a[180:183], a[148:151], v[136:139]\n" \
"s_addk_i32 s84, 0x2000\n" \
"s_nop 0\n" \
M_"v[136:139], a[244:247], a[212:215], v[136:139]\n" \
B_"v25, s[76:79], s84 offen offset:1024 lds\n" \
M_"v[132:135], a[176:179], a[148:151], v[132:135]\n" \
X_"a[236:237], v35 offset:4096\n" \
C_"v210, v180, v181\n" \
M_"v[132:135], a[240:243], a[212:215], v[132:135]\n" \
R_"a[148:151], v28 offset:10240\n" \
C_"v211, v182, v183\n" \
M_"v[148:151], a[176:179], a[152:155], v[148:151]\n" \
R_"a[212:215], v29 offset:10240\n" \
"v_accvgpr_read_b32 v212, a48\n" \
M_"v[148:151], a[240:243], a[216:219], v[148:151]\n" \
X_"a[238:239], v35 offset:4608\n" \
"v_accvgpr_read_b32 v213, a49\n" \
M_"v[152:155], a[180:183], a[152:155], v[152:155]\n" \
X_"a[168:169], v34 offset:0\n" \
"v_accvgpr_read_b32 v214, a50\n" \
M_"v[152:155], a[244:247], a[216:219], v[152:155]\n" \
X_"a[170:171], v34 offset:512\n" \
"v_accvgpr_read_b32 v215, a51\n" \
M_"v[156:159], a[184:187], a[152:155], v[156:159]\n" \
X_"a[232:233], v34 offset:4096\n" \
"v_accvgpr_read_b32 v180, a52\n" \
M_"v[156:159], a[248:251], a[216:219], v[156:159]\n" \
X_"a[234:235], v34 offset:4608\n" \
"v_accvgpr_read_b32 v181, a53\n" \
M_"v[160:163], a[188:191], a[152:155], v[160:163]\n" \
R_"v[248:251], v22 offset:0\n" \
"v_accvgpr_read_b32 v182, a54\n" \
M_"v[160:163], a[252:255], a[216:219], v[160:163]\n" \
R_"a[152:155], v28 offset:12288\n" \
"v_accvgpr_read_b32 v183, a55\n" \
M_"v[176:179], a[188:191], a[156:159], v[176:179]\n" \
R_"a[216:219], v29 offset:12288\n" \
C_"v212, v212, v213\n" \
M_"v[176:179], a[252:255], a[220:223], v[176:179]\n" \
R_"v[48:51], v23 offset:0\n" \
C_"v213, v214, v215\n" \
M_"v[172:175], a[184:187], a[156:159], v[172:175]\n" \
C_"v214, v180, v181\n" \
C_"v215, v182, v183\n" \
M_"v[172:175], a[248:251], a[220:223], v[172:175]\n" \
"v_accvgpr_read_b32 v216, a64\n" \
"v_accvgpr_read_b32 v217, a65\n" \
M_"v[168:171], a[180:183], a[156:159], v[168:171]\n" \
"v_accvgpr_read_b32 v218, a66\n" \
"v_accvgpr_read_b32 v219, a67\n" \
M_"v[168:171], a[244:247], a[220:223], v[168:171]\n" \
"v_accvgpr_read_b32 v180, a68\n" \
"v_accvgpr_read_b32 v181, a69\n" \
M_"v[164:167], a[176:179], a[156:159], v[164:167]\n" \
"v_accvgpr_read_b32 v182, a70\n" \
"v_accvgpr_read_b32 v183, a71\n" \
M_"v[164:167], a[240:243], a[220:223], v[164:167]\n" \
R_"a[156:159], v28 offset:14336\n" \
R_"a[220:223], v29 offset:14336\n" \
"s_waitcnt lgkmcnt(15)\n" \
M_"a[0:3], a[160:163], a[128:131], 0\n" \
"s_addk_i32 s84, 0x2000\n" \
C_"v216, v216, v217\n" \
M_"a[0:3], a[224:227], a[192:195], a[0:3]\n" \
B_"v24, s[76:79], s84 offen offset:2048 lds\n" \
C_"v217, v218, v219\n" \
M_"a[4:7], a[164:167], a[128:131], 0\n" \
X_"a[176:177], v36 offset:0\n" \
C_"v218, v180, v181\n" \
M_"a[4:7], a[228:231], a[192:195], a[4:7]\n" \
"s_addk_i32 s84, 0x2000\n" \
C_"v219, v182, v183\n" \
"s_waitcnt lgkmcnt(15)\n" \
M_"a[12:15], a[172:175], a[128:131], 0\n" \
B_"v25, s[76:79], s84 offen offset:3072 lds\n" \
"v_accvgpr_read_b32 v220, a80\n" \
".fill (L_pb + 20192 - .) / 4, 4, 0xBF800000\n" \
"L_pp5:\n" \
"s_cbranch_execz L_pret\n" \
"s_waitcnt lgkmcnt(11)\n" \
M_"a[12:15], a[236:239], a[192:195], a[12:15]\n" \
X_"a[178:179], v36 offset:1024\n" \
"v_accvgpr_read_b32 v221, a81\n" \
"s_waitcnt lgkmcnt(10)\n" \
M_"a[8:11], a[128:131], a[168:171], 0\n" \
X_"a[240:241], v36 offset:8192\n" \
"v_accvgpr_read_b32 v222, a82\n" \
"s_waitcnt lgkmcnt(9)\n" \
M_"a[8:11], a[192:195], a[232:235], a[8:11]\n" \
X_"a[242:243], v36 offset:9216\n" \
"v_accvgpr_read_b32 v223, a83\n" \
M_"a[24:27], a[132:135], a[168:171], 0\n" \
X_"a[180:181], v37 offset:0\n" \
"v_accvgpr_read_b32 v180, a84\n" \
M_"a[24:27], a[196:199], a[232:235], a[24:27]\n" \
X_"a[182:183], v37 offset:1024\n" \
"v_accvgpr_read_b32 v181, a85\n" \
M_"a[28:31], a[172:175], a[132:135], 0\n" \
X_"a[244:245], v37 offset:8192\n" \
"v_accvgpr_read_b32 v182, a86\n" \
M_"a[28:31], a[236:239], a[196:199], a[28:31]\n" \
"s_add_u32 s89, s41, 0x2000\n" \
"buffer_store_dwordx4 v[200:203], v20, s[80:83], s89 offen offset:0 sc1\n" \
M_"a[20:23], a[164:167], a[132:135], 0\n" \
X_"a[246:247], v37 offset:9216\n" \
"v_accvgpr_read_b32 v183, a87\n" \
M_"a[20:23], a[228:231], a[196:199], a[20:23]\n" \
X_"a[184:185], v38 offset:0\n" \
C_"v220, v220, v221\n" \
M_"a[16:19], a[160:163], a[132:135], 0\n" \
X_"a[186:187], v38 offset:1024\n" \
C_"v221, v222, v223\n" \
M_"a[16:19], a[224:227], a[196:199], a[16:19]\n" \
C_"v222, v180, v181\n" \
C_"v223, v182, v183\n" \
C_"v224, v52, v53\n" \
M_"a[32:35], a[160:163], a[136:139], 0\n" \
"s_add_u32 s88, s41, 0x2000\n" \
"buffer_store_dwordx4 v[204:207], v20, s[80:83], s88 offen offset:1024 sc1\n" \
M_"a[32:35], a[224:227], a[200:203], a[32:35]\n" \
X_"a[248:249], v38 offset:8192\n" \
C_"v225, v54, v55\n" \
M_"a[36:39], a[164:167], a[136:139], 0\n" \
X_"a[250:251], v38 offset:9216\n" \
C_"v226, v56, v57\n" \
M_"a[36:39], a[228:231], a[200:203], a[36:39]\n" \
X_"a[188:189], v39 offset:0\n" \
C_"v227, v58, v59\n" \
M_"a[44:47], a[172:175], a[136:139], 0\n" \
C_"v228, v60, v61\n" \
C_"v229, v62, v63\n" \
C_"v230, v64, v65\n" \
M_"a[44:47], a[236:239], a[200:203], a[44:47]\n" \
X_"a[190:191], v39 offset:1024\n" \
C_"v231, v66, v67\n" \
M_"a[40:43], a[136:139], a[168:171], 0\n" \
"s_add_u32 s89, s41, 0x3000\n" \
"buffer_store_dwordx4 v[208:211], v20, s[80:83], s89 offen offset:1024 sc1\n" \
M_"a[40:43], a[200:203], a[232:235], a[40:43]\n" \
X_"a[252:253], v39 offset:8192\n" \
C_"v232, v76, v77\n" \
M_"a[56:59], a[140:143], a[168:171], 0\n" \
X_"a[254:255], v39 offset:9216\n" \
C_"v233, v78, v79\n" \
M_"a[56:59], a[204:207], a[232:235], a[56:59]\n" \
C_"v234, v80, v81\n" \
C_"v235, v82, v83\n" \
C_"v236, v68, v69\n" \
M_"a[60:63], a[172:175], a[140:143], 0\n" \
C_"v237, v70, v71\n" \
C_"v238, v72, v73\n" \
C_"v239, v74, v75\n" \
M_"a[60:63], a[236:239], a[204:207], a[60:63]\n" \
"s_add_u32 s88, s41, 0x3000\n" \
"buffer_store_dwordx4 v[212:215], v20, s[80:83], s88 offen offset:0 sc1\n" \
M_"a[52:55], a[164:167], a[140:143], 0\n" \
C_"v240, v84, v85\n" \
C_"v241, v86, v87\n" \
C_"v242, v88, v89\n" \
M_"a[52:55], a[228:231], a[204:207], a[52:55]\n" \
C_"v243, v90, v91\n" \
C_"v244, v92, v93\n" \
C_"v245, v94, v95\n" \
M_"a[48:51], a[160:163], a[140:143], 0\n" \
C_"v246, v96, v97\n" \
C_"v247, v98, v99\n" \
C_"v184, v108, v109\n" \
M_"a[48:51], a[224:227], a[204:207], a[48:51]\n" \
C_"v185, v110, v111\n" \
C_"v186, v112, v113\n" \
C_"v187, v114, v115\n" \
"s_waitcnt lgkmcnt(15)\n" \
"s_nop 0\n" \
M_"a[64:67], a[164:167], v[248:251], 0\n" \
C_"v188, v100, v101\n" \
C_"v189, v102, v103\n" \
C_"v190, v104, v105\n" \
M_"a[64:67], a[228:231], v[48:51], a[64:67]\n" \
C_"v191, v106, v107\n" \
C_"v192, v116, v117\n" \
C_"v193, v118, v119\n" \
M_"a[80:83], a[172:175], v[248:251], 0\n" \
C_"v194, v120, v121\n" \
C_"v195, v122, v123\n" \
C_"v196, v124, v125\n" \
M_"a[80:83], a[236:239], v[48:51], a[80:83]\n" \
C_"v197, v126, v127\n" \
C_"v198, v128, v129\n" \
C_"v199, v130, v131\n" \
M_"a[68:71], v[248:251], a[168:171], 0\n" \
C_"v252, v140, v141\n" \
C_"v253, v142, v143\n" \
C_"v254, v144, v145\n" \
M_"a[68:71], v[48:51], a[232:235], a[68:71]\n" \
C_"v255, v146, v147\n" \
"v_accvgpr_write_b32 a84, v252\n" \
"v_accvgpr_write_b32 a85, v253\n" \
"s_waitcnt vmcnt(4) lgkmcnt(0)\n" \
"s_barrier\n" \
M_"v[52:55], a[176:179], a[128:131], 0\n" \
"s_add_u32 m0, s40, 0x0\n" \
"s_nop 0\n" \
"s_add_u32 s85, s39, 0x4000\n" \
"v_accvgpr_write_b32 a86, v254\n" \
M_"v[52:55], a[240:243], a[192:195], v[52:55]\n" \
B_"v20, s[80:83], s85 offen sc1 lds\n" \
"v_accvgpr_write_b32 a87, v255\n" \
M_"v[56:59], a[180:183], a[128:131], 0\n" \
X_"a[160:161], v32 offset:32768\n" \
C_"v252, v132, v133\n" \
M_"v[56:59], a[244:247], a[192:195], v[56:59]\n" \
X_"a[162:163], v32 offset:33280\n" \
C_"v253, v134, v135\n" \
M_"v[60:63], a[184:187], a[128:131], 0\n" \
"s_add_u32 m0, s40, 0x1000\n" \
"s_add_u32 s85, s39, 0x5000\n" \
C_"v254, v136, v137\n" \
M_"v[60:63], a[248:251], a[192:195], v[60:63]\n" \
B_"v20, s[80:83], s85 offen sc1 lds\n" \
C_"v255, v138, v139\n" \
M_"v[64:67], a[188:191], a[128:131], 0\n" \
X_"a[224:225], v32 offset:36864\n" \
C_"v200, v148, v149\n" \
M_"v[64:67], a[252:255], a[192:195], v[64:67]\n" \
R_"a[128:131], v28 offset:32768\n" \
C_"v201, v150, v151\n" \
M_"v[80:83], a[188:191], a[132:135], 0\n" \
R_"a[192:195], v29 offset:32768\n" \
C_"v202, v152, v153\n" \
M_"v[80:83], a[252:255], a[196:199], v[80:83]\n" \
"s_add_u32 m0, s40, 0x3000\n" \
"s_movk_i32 s84, 0x100\n" \
"s_nop 0\n" \
C_"v203, v154, v155\n" \
M_"v[76:79], a[184:187], a[132:135], 0\n" \
B_"v21, s[76:79], s84 offen offset:0 lds\n" \
C_"v204, v156, v157\n" \
M_"v[76:79], a[248:251], a[196:199], v[76:79]\n" \
X_"a[226:227], v32 offset:37376\n" \
C_"v205, v158, v159\n" \
M_"v[72:75], a[180:183], a[132:135], 0\n" \
X_"a[164:165], v33 offset:32768\n" \
C_"v206, v160, v161\n" \
M_"v[72:75], a[244:247], a[196:199], v[72:75]\n" \
C_"v207, v162, v163\n" \
C_"v208, v172, v173\n" \
C_"v209, v174, v175\n" \
M_"v[68:71], a[176:179], a[132:135], 0\n" \
X_"a[166:167], v33 offset:33280\n" \
C_"v210, v176, v177\n" \
M_"v[68:71], a[240:243], a[196:199], v[68:71]\n" \
R_"a[132:135], v28 offset:34816\n" \
C_"v211, v178, v179\n" \
M_"v[84:87], a[176:179], a[136:139], 0\n" \
R_"a[196:199], v29 offset:34816\n" \
C_"v212, v164, v165\n" \
M_"v[84:87], a[240:243], a[200:203], v[84:87]\n" \
"s_add_u32 m0, s31, 0x0\n" \
"s_mov_b32 s84, 0x100000\n" \
C_"v213, v166, v167\n" \
M_"v[88:91], a[180:183], a[136:139], 0\n" \
B_"v26, s[68:71], s84 offen offset:0 lds\n" \
C_"v214, v168, v169\n" \
M_"v[88:91], a[244:247], a[200:203], v[88:91]\n" \
"s_add_u32 s89, s41, 0x0\n" \
"buffer_store_dwordx4 v[224:227], v20, s[80:83], s89 offen offset:2048 sc1\n" \
M_"v[92:95], a[184:187], a[136:139], 0\n" \
X_"a[228:229], v33 offset:36864\n" \
C_"v215, v170, v171\n" \
M_"v[92:95], a[248:251], a[200:203], v[92:95]\n" \
X_"a[230:231], v33 offset:37376\n" \
M_"v[96:99], a[188:191], a[136:139], 0\n" \
M_"v[96:99], a[252:255], a[200:203], v[96:99]\n" \
R_"a[136:139], v28 offset:36864\n" \
M_"v[112:115], a[188:191], a[140:143], 0\n" \
R_"a[200:203], v29 offset:36864\n" \
M_"v[112:115], a[252:255], a[204:207], v[112:115]\n" \
"s_addk_i32 s84, 0x7c00\n" \
M_"v[108:111], a[184:187], a[140:143], 0\n" \
B_"v26, s[68:71], s84 offen offset:1024 lds\n" \
M_"v[108:111], a[248:251], a[204:207], v[108:111]\n" \
X_"a[172:173], v35 offset:32768\n" \
M_"v[104:107], a[180:183], a[140:143], 0\n" \
"s_addk_i32 s84, 0x7c00\n" \
M_"v[104:107], a[244:247], a[204:207], v[104:107]\n" \
B_"v27, s[68:71], s84 offen offset:2048 lds\n" \
M_"v[100:103], a[176:179], a[140:143], 0\n" \
X_"a[174:175], v35 offset:33280\n" \
M_"v[100:103], a[240:243], a[204:207], v[100:103]\n" \
R_"a[140:143], v28 offset:38912\n" \
M_"v[116:119], a[176:179], a[144:147], 0\n" \
R_"a[204:207], v29 offset:38912\n" \
M_"v[116:119], a[240:243], a[208:211], v[116:119]\n" \
"s_addk_i32 s84, 0x7c00\n" \
M_"v[120:123], a[180:183], a[144:147], 0\n" \
B_"v27, s[68:71], s84 offen offset:3072 lds\n" \
M_"v[120:123], a[244:247], a[208:211], v[120:123]\n" \
X_"a[236:237], v35 offset:36864\n" \
M_"v[124:127], a[184:187], a[144:147], 0\n" \
X_"a[238:239], v35 offset:37376\n" \
M_"v[124:127], a[248:251], a[208:211], v[124:127]\n" \
"s_add_u32 m0, s30, 0x0\n" \
"s_movk_i32 s84, 0x180\n" \
M_"v[128:131], a[188:191], a[144:147], 0\n" \
B_"v24, s[76:79], s84 offen offset:0 lds\n" \
M_"v[128:131], a[252:255], a[208:211], v[128:131]\n" \
R_"a[144:147], v28 offset:40960\n" \
M_"v[144:147], a[188:191], a[148:151], 0\n" \
R_"a[208:211], v29 offset:40960\n" \
M_"v[144:147], a[252:255], a[212:215], v[144:147]\n" \
X_"a[168:169], v34 offset:32768\n" \
M_"v[140:143], a[184:187], a[148:151], 0\n" \
"s_movk_i32 s84, 0x2180\n" \
M_"v[140:143], a[248:251], a[212:215], v[140:143]\n" \
B_"v25, s[76:79], s84 offen offset:1024 lds\n" \
M_"v[136:139], a[180:183], a[148:151], 0\n" \
X_"a[170:171], v34 offset:33280\n" \
M_"v[136:139], a[244:247], a[212:215], v[136:139]\n" \
X_"a[232:233], v34 offset:36864\n" \
M_"v[132:135], a[176:179], a[148:151], 0\n" \
X_"a[234:235], v34 offset:37376\n" \
M_"v[132:135], a[240:243], a[212:215], v[132:135]\n" \
R_"a[148:151], v28 offset:43008\n" \
M_"v[148:151], a[176:179], a[152:155], 0\n" \
R_"a[212:215], v29 offset:43008\n" \
M_"v[148:151], a[240:243], a[216:219], v[148:151]\n" \
"s_movk_i32 s84, 0x4180\n" \
M_"v[152:155], a[180:183], a[152:155], 0\n" \
B_"v24, s[76:79], s84 offen offset:2048 lds\n" \
M_"v[152:155], a[244:247], a[216:219], v[152:155]\n" \
"s_movk_i32 s84, 0x6180\n" \
M_"v[156:159], a[184:187], a[152:155], 0\n" \
B_"v25, s[76:79], s84 offen offset:3072 lds\n" \
M_"v[156:159], a[248:251], a[216:219], v[156:159]\n" \
R_"v[248:251], v22 offset:32768\n" \
M_"v[160:163], a[188:191], a[152:155], 0\n" \
R_"v[48:51], v23 offset:32768\n" \
M_"v[160:163], a[252:255], a[216:219], v[160:163]\n" \
R_"a[152:155], v28 offset:45056\n" \
M_"v[176:179], a[188:191], a[156:159], 0\n" \
R_"a[216:219], v29 offset:45056\n" \
M_"v[176:179], a[252:255], a[220:223], v[176:179]\n" \
M_"v[172:175], a[184:187], a[156:159], 0\n" \
M_"v[172:175], a[248:251], a[220:223], v[172:175]\n" \
M_"v[168:171], a[180:183], a[156:159], 0\n" \
M_"v[168:171], a[244:247], a[220:223], v[168:171]\n" \
M_"v[164:167], a[176:179], a[156:159], 0\n" \
M_"v[164:167], a[240:243], a[220:223], v[164:167]\n" \
R_"a[156:159], v28 offset:47104\n" \
R_"a[220:223], v29 offset:47104\n" \
"s_waitcnt lgkmcnt(15)\n" \
M_"a[0:3], a[160:163], a[128:131], a[0:3]\n" \
"s_add_u32 m0, s30, 0x1000\n" \
"s_addk_i32 s84, 0x3000\n" \
M_"a[0:3], a[224:227], a[192:195], a[0:3]\n" \
B_"v24, s[76:79], s84 offen offset:0 lds\n" \
M_"a[4:7], a[164:167], a[128:131], a[4:7]\n" \
X_"a[176:177], v36 offset:32768\n" \
"v_mov_b32_e64 v11, 0\n" \
M_"a[4:7], a[228:231], a[192:195], a[4:7]\n" \
X_"a[178:179], v36 offset:33792\n" \
"s_and_b32 s5, s2, 7\n" \
M_"a[12:15], a[172:175], a[128:131], a[12:15]\n" \
X_"a[240:241], v36 offset:40960\n" \
"s_lshl_b32 s6, s3, 1\n" \
"s_waitcnt lgkmcnt(15)\n" \
M_"a[12:15], a[236:239], a[192:195], a[12:15]\n" \
"s_add_u32 s88, s41, 0x0\n" \
"buffer_store_dwordx4 v[228:231], v20, s[80:83], s88 offen offset:3072 sc1\n" \
"s_waitcnt lgkmcnt(13)\n" \
M_"a[8:11], a[128:131], a[168:171], a[8:11]\n" \
X_"a[242:243], v36 offset:41984\n" \
"s_lshl_b32 s4, s5, 2\n" \
"s_waitcnt lgkmcnt(12)\n" \
M_"a[8:11], a[192:195], a[232:235], a[8:11]\n" \
X_"a[180:181], v37 offset:32768\n" \
"s_bfe_u32 s7, s2, 0x20003\n" \
M_"a[24:27], a[132:135], a[168:171], a[24:27]\n" \
X_"a[182:183], v37 offset:33792\n" \
"s_or_b32 s12, s4, s7\n" \
M_"a[24:27], a[196:199], a[232:235], a[24:27]\n" \
X_"a[244:245], v37 offset:40960\n" \
"s_and_b32 s6, s6, 4\n" \
M_"a[28:31], a[172:175], a[132:135], a[28:31]\n" \
"s_and_b32 s8, s3, 1\n" \
"s_or_b32 s8, s6, s8\n" \
M_"a[28:31], a[236:239], a[196:199], a[28:31]\n" \
"s_addk_i32 s84, 0x2000\n" \
M_"a[20:23], a[164:167], a[132:135], a[20:23]\n" \
B_"v25, s[76:79], s84 offen offset:1024 lds\n" \
M_"a[20:23], a[228:231], a[196:199], a[20:23]\n" \
X_"a[246:247], v37 offset:41984\n" \
"s_lshl_b32 s6, s12, 4\n" \
M_"a[16:19], a[160:163], a[132:135], a[16:19]\n" \
X_"a[184:185], v38 offset:32768\n" \
"s_lshl_b32 s9, s3, 2\n" \
M_"a[16:19], a[224:227], a[196:199], a[16:19]\n" \
X_"a[186:187], v38 offset:33792\n" \
"s_or_b32 s6, s6, s9\n" \
M_"a[32:35], a[160:163], a[136:139], a[32:35]\n" \
"s_addk_i32 s84, 0x2000\n" \
M_"a[32:35], a[224:227], a[200:203], a[32:35]\n" \
B_"v24, s[76:79], s84 offen offset:2048 lds\n" \
M_"a[36:39], a[164:167], a[136:139], a[36:39]\n" \
X_"a[248:249], v38 offset:40960\n" \
"s_lshr_b32 s9, s2, 5\n" \
M_"a[36:39], a[228:231], a[200:203], a[36:39]\n" \
X_"a[250:251], v38 offset:41984\n" \
"s_mul_i32 s10, s9, 0xa00\n" \
M_"a[44:47], a[172:175], a[136:139], a[44:47]\n" \
X_"a[188:189], v39 offset:32768\n" \
"s_add_i32 s10, s10, 0x5bc1000\n" \
M_"a[44:47], a[236:239], a[200:203], a[44:47]\n" \
X_"a[190:191], v39 offset:33792\n" \
"s_or_b32 s6, s6, s10\n" \
M_"a[40:43], a[136:139], a[168:171], a[40:43]\n" \
"s_add_u32 s89, s41, 0x1000\n" \
"buffer_store_dwordx4 v[232:235], v20, s[80:83], s89 offen offset:3072 sc1\n" \
M_"a[40:43], a[200:203], a[232:235], a[40:43]\n" \
X_"a[252:253], v39 offset:40960\n" \
"s_lshl_b32 s13, s9, 3\n" \
M_"a[56:59], a[140:143], a[168:171], a[56:59]\n" \
X_"a[254:255], v39 offset:41984\n" \
"v_writelane_b32 v11, s6, 0\n" \
M_"a[56:59], a[204:207], a[232:235], a[56:59]\n" \
"s_or_b32 s6, s13, s5\n" \
"v_writelane_b32 v11, s10, 1\n" \
M_"a[60:63], a[172:175], a[140:143], a[60:63]\n" \
"s_lshl_b32 s6, s6, 10\n" \
"s_lshl_b32 s10, s3, 8\n" \
M_"a[60:63], a[236:239], a[204:207], a[60:63]\n" \
"s_addk_i32 s84, 0x2000\n" \
M_"a[52:55], a[164:167], a[140:143], a[52:55]\n" \
B_"v25, s[76:79], s84 offen offset:3072 lds\n" \
M_"a[52:55], a[228:231], a[204:207], a[52:55]\n" \
"s_or_b32 s6, s6, s10\n" \
"s_or_b32 s10, s6, 0x5c00200\n" \
M_"a[48:51], a[160:163], a[140:143], a[48:51]\n" \
"v_writelane_b32 v11, s10, 2\n" \
"s_lshl_b32 s10, s7, 2\n" \
M_"a[48:51], a[224:227], a[204:207], a[48:51]\n" \
"s_or_b32 s11, s10, s6\n" \
"s_or_b32 s11, s11, 0x5c00000\n" \
"s_waitcnt lgkmcnt(15)\n" \
M_"a[64:67], a[164:167], v[248:251], a[64:67]\n" \
"v_writelane_b32 v11, s11, 3\n" \
"s_or_b32 s6, s6, 0x5c00080\n" \
M_"a[64:67], a[228:231], v[48:51], a[64:67]\n" \
"v_writelane_b32 v11, s6, 4\n" \
"s_or_b32 s6, s6, s10\n" \
M_"a[80:83], a[172:175], v[248:251], a[80:83]\n" \
"s_add_i32 s10, s3, s2\n" \
"v_writelane_b32 v11, s6, 5\n" \
M_"a[80:83], a[236:239], v[48:51], a[80:83]\n" \
"s_mov_b32 s6, 2.0\n" \
"s_mov_b32 s11, 0x5bc0000\n" \
M_"a[68:71], v[248:251], a[168:171], a[68:71]\n" \
"s_cmp_lg_u32 s10, 0\n" \
"s_cselect_b32 s10, s6, s11\n" \
"s_waitcnt vmcnt(10) lgkmcnt(0)\n" \
M_"a[68:71], v[48:51], a[232:235], a[68:71]\n" \
"s_barrier\n" \
"v_xor_b32_e32 v28, 0x20000, v28\n" \
"v_xor_b32_e32 v29, 0x20000, v29\n" \
M_"v[52:55], a[176:179], a[128:131], v[52:55]\n" \
"s_add_u32 m0, s40, 0x8000\n" \
"s_add_u32 s85, s39, 0x6000\n" \
M_"v[52:55], a[240:243], a[192:195], v[52:55]\n" \
B_"v20, s[80:83], s85 offen sc1 lds\n" \
M_"v[56:59], a[180:183], a[128:131], v[56:59]\n" \
X_"a[160:161], v32 offset:0\n" \
"s_lshr_b32 s14, s2, 3\n" \
M_"v[56:59], a[244:247], a[192:195], v[56:59]\n" \
X_"a[162:163], v32 offset:512\n" \
"v_writelane_b32 v11, s10, 57\n" \
M_"v[60:63], a[184:187], a[128:131], v[60:63]\n" \
"s_add_u32 m0, s40, 0x9000\n" \
"s_add_u32 s85, s39, 0x7000\n" \
M_"v[60:63], a[248:251], a[192:195], v[60:63]\n" \
B_"v20, s[80:83], s85 offen sc1 lds\n" \
M_"v[64:67], a[188:191], a[128:131], v[64:67]\n" \
X_"a[224:225], v32 offset:4096\n" \
"s_lshl_b32 s10, s5, 5\n" \
M_"v[64:67], a[252:255], a[192:195], v[64:67]\n" \
R_"a[128:131], v28 offset:0\n" \
"s_min_u32 s15, s3, 2\n" \
"s_nop 0\n" \
M_"v[80:83], a[188:191], a[132:135], v[80:83]\n" \
R_"a[192:195], v29 offset:0\n" \
"s_mul_i32 s11, s7, 0x120\n" \
M_"v[80:83], a[252:255], a[196:199], v[80:83]\n" \
"s_add_u32 s88, s41, 0x1000\n" \
"buffer_store_dwordx4 v[236:239], v20, s[80:83], s88 offen offset:2048 sc1\n" \
M_"v[76:79], a[184:187], a[132:135], v[76:79]\n" \
X_"a[226:227], v32 offset:4608\n" \
M_"v[76:79], a[248:251], a[196:199], v[76:79]\n" \
X_"a[164:165], v33 offset:0\n" \
M_"v[72:75], a[180:183], a[132:135], v[72:75]\n" \
"s_add_u32 m0, s40, 0xb000\n" \
"s_movk_i32 s84, 0x180\n" \
"s_nop 0\n" \
M_"v[72:75], a[244:247], a[196:199], v[72:75]\n" \
B_"v21, s[76:79], s84 offen offset:0 lds\n" \
M_"v[68:71], a[176:179], a[132:135], v[68:71]\n" \
X_"a[166:167], v33 offset:512\n" \
M_"v[68:71], a[240:243], a[196:199], v[68:71]\n" \
R_"a[132:135], v28 offset:2048\n" \
M_"v[84:87], a[176:179], a[136:139], v[84:87]\n" \
R_"a[196:199], v29 offset:2048\n" \
M_"v[84:87], a[240:243], a[200:203], v[84:87]\n" \
"s_add_u32 m0, s31, 0x8000\n" \
"s_mov_b32 s84, 0x180000\n" \
M_"v[88:91], a[180:183], a[136:139], v[88:91]\n" \
B_"v26, s[68:71], s84 offen offset:0 lds\n" \
M_"v[88:91], a[244:247], a[200:203], v[88:91]\n" \
X_"a[228:229], v33 offset:4096\n" \
M_"v[92:95], a[184:187], a[136:139], v[92:95]\n" \
X_"a[230:231], v33 offset:4608\n" \
M_"v[92:95], a[248:251], a[200:203], v[92:95]\n" \
"s_addk_i32 s84, 0x7c00\n" \
M_"v[96:99], a[188:191], a[136:139], v[96:99]\n" \
B_"v26, s[68:71], s84 offen offset:1024 lds\n" \
M_"v[96:99], a[252:255], a[200:203], v[96:99]\n" \
R_"a[136:139], v28 offset:4096\n" \
M_"v[112:115], a[188:191], a[140:143], v[112:115]\n" \
R_"a[200:203], v29 offset:4096\n" \
M_"v[112:115], a[252:255], a[204:207], v[112:115]\n" \
"s_addk_i32 s84, 0x7c00\n" \
M_"v[108:111], a[184:187], a[140:143], v[108:111]\n" \
B_"v27, s[68:71], s84 offen offset:2048 lds\n" \
M_"v[108:111], a[248:251], a[204:207], v[108:111]\n" \
X_"a[172:173], v35 offset:0\n" \
M_"v[104:107], a[180:183], a[140:143], v[104:107]\n" \
X_"a[174:175], v35 offset:512\n" \
M_"v[104:107], a[244:247], a[204:207], v[104:107]\n" \
X_"a[236:237], v35 offset:4096\n" \
M_"v[100:103], a[176:179], a[140:143], v[100:103]\n" \
"s_add_u32 s16, s22, s11\n" \
"s_addc_u32 s17, s23, 0\n" \
M_"v[100:103], a[240:243], a[204:207], v[100:103]\n" \
R_"a[140:143], v28 offset:6144\n" \
M_"v[116:119], a[176:179], a[144:147], v[116:119]\n" \
R_"a[204:207], v29 offset:6144\n" \
M_"v[116:119], a[240:243], a[208:211], v[116:119]\n" \
"s_addk_i32 s84, 0x7c00\n" \
M_"v[120:123], a[180:183], a[144:147], v[120:123]\n" \
B_"v27, s[68:71], s84 offen offset:3072 lds\n" \
M_"v[120:123], a[244:247], a[208:211], v[120:123]\n" \
"s_add_u32 m0, s30, 0x8000\n" \
"s_movk_i32 s84, 0x200\n" \
M_"v[124:127], a[184:187], a[144:147], v[124:127]\n" \
B_"v24, s[76:79], s84 offen offset:0 lds\n" \
M_"v[124:127], a[248:251], a[208:211], v[124:127]\n" \
X_"a[238:239], v35 offset:4608\n" \
M_"v[128:131], a[188:191], a[144:147], v[128:131]\n" \
"s_add_u32 s89, s41, 0x2000\n" \
"buffer_store_dwordx4 v[240:243], v20, s[80:83], s89 offen offset:2048 sc1\n" \
M_"v[128:131], a[252:255], a[208:211], v[128:131]\n" \
R_"a[144:147], v28 offset:8192\n" \
M_"v[144:147], a[188:191], a[148:151], v[144:147]\n" \
R_"a[208:211], v29 offset:8192\n" \
M_"v[144:147], a[252:255], a[212:215], v[144:147]\n" \
X_"a[168:169], v34 offset:0\n" \
M_"v[140:143], a[184:187], a[148:151], v[140:143]\n" \
X_"a[170:171], v34 offset:512\n" \
M_"v[140:143], a[248:251], a[212:215], v[140:143]\n" \
"s_movk_i32 s84, 0x2200\n" \
"s_nop 0\n" \
M_"v[136:139], a[180:183], a[148:151], v[136:139]\n" \
B_"v25, s[76:79], s84 offen offset:1024 lds\n" \
M_"v[136:139], a[244:247], a[212:215], v[136:139]\n" \
X_"a[232:233], v34 offset:4096\n" \
M_"v[132:135], a[176:179], a[148:151], v[132:135]\n" \
X_"a[234:235], v34 offset:4608\n" \
M_"v[132:135], a[240:243], a[212:215], v[132:135]\n" \
R_"a[148:151], v28 offset:10240\n" \
M_"v[148:151], a[176:179], a[152:155], v[148:151]\n" \
R_"a[212:215], v29 offset:10240\n" \
M_"v[148:151], a[240:243], a[216:219], v[148:151]\n" \
"s_movk_i32 s84, 0x4200\n" \
M_"v[152:155], a[180:183], a[152:155], v[152:155]\n" \
B_"v24, s[76:79], s84 offen offset:2048 lds\n" \
M_"v[152:155], a[244:247], a[216:219], v[152:155]\n" \
"s_movk_i32 s84, 0x6200\n" \
M_"v[156:159], a[184:187], a[152:155], v[156:159]\n" \
B_"v25, s[76:79], s84 offen offset:3072 lds\n" \
M_"v[156:159], a[248:251], a[216:219], v[156:159]\n" \
R_"v[248:251], v22 offset:0\n" \
M_"v[160:163], a[188:191], a[152:155], v[160:163]\n" \
R_"v[48:51], v23 offset:0\n" \
M_"v[160:163], a[252:255], a[216:219], v[160:163]\n" \
R_"a[152:155], v28 offset:12288\n" \
M_"v[176:179], a[188:191], a[156:159], v[176:179]\n" \
R_"a[216:219], v29 offset:12288\n" \
M_"v[176:179], a[252:255], a[220:223], v[176:179]\n" \
"s_add_u32 s18, s24, s11\n" \
"v_writelane_b32 v11, s16, 9\n" \
"s_addc_u32 s19, s25, 0\n" \
M_"v[172:175], a[184:187], a[156:159], v[172:175]\n" \
"s_and_b32 s20, s17, 0xffff\n" \
"v_writelane_b32 v11, s20, 10\n" \
M_"v[172:175], a[248:251], a[220:223], v[172:175]\n" \
"s_and_b32 s20, s19, 0xffff\n" \
"v_writelane_b32 v11, s18, 11\n" \
M_"v[168:171], a[180:183], a[156:159], v[168:171]\n" \
"s_cmpk_lt_u32 s3, 3\n" \
"s_cselect_b32 s16, s16, s18\n" \
M_"v[168:171], a[244:247], a[220:223], v[168:171]\n" \
"s_movk_i32 s18, 0xfff\n" \
"v_writelane_b32 v11, s20, 12\n" \
M_"v[164:167], a[176:179], a[156:159], v[164:167]\n" \
"s_and_b32 s20, s29, 0xffff\n" \
"v_writelane_b32 v11, s28, 13\n" \
M_"v[164:167], a[240:243], a[220:223], v[164:167]\n" \
R_"a[156:159], v28 offset:14336\n" \
R_"a[220:223], v29 offset:14336\n" \
"s_waitcnt lgkmcnt(15)\n" \
M_"a[0:3], a[160:163], a[128:131], a[0:3]\n" \
"s_add_u32 m0, s30, 0x9000\n" \
"s_addk_i32 s84, 0x3000\n" \
M_"a[0:3], a[224:227], a[192:195], a[0:3]\n" \
B_"v24, s[76:79], s84 offen offset:0 lds\n" \
M_"a[4:7], a[164:167], a[128:131], a[4:7]\n" \
X_"a[176:177], v36 offset:0\n" \
"v_writelane_b32 v11, s20, 14\n" \
M_"a[4:7], a[228:231], a[192:195], a[4:7]\n" \
X_"a[178:179], v36 offset:1024\n" \
"v_writelane_b32 v11, s16, 15\n" \
M_"a[12:15], a[172:175], a[128:131], a[12:15]\n" \
X_"a[240:241], v36 offset:8192\n" \
"s_waitcnt lgkmcnt(15)\n" \
".fill (L_pb + 24288 - .) / 4, 4, 0xBF800000\n" \
"L_pp6:\n" \
"s_cbranch_execz L_pret\n" \
M_"a[12:15], a[236:239], a[192:195], a[12:15]\n" \
"s_add_u32 s88, s41, 0x2000\n" \
"buffer_store_dwordx4 v[244:247], v20, s[80:83], s88 offen offset:3072 sc1\n" \
"s_waitcnt lgkmcnt(13)\n" \
M_"a[8:11], a[128:131], a[168:171], a[8:11]\n" \
X_"a[242:243], v36 offset:9216\n" \
"s_waitcnt lgkmcnt(12)\n" \
M_"a[8:11], a[192:195], a[232:235], a[8:11]\n" \
X_"a[180:181], v37 offset:0\n" \
M_"a[24:27], a[132:135], a[168:171], a[24:27]\n" \
X_"a[182:183], v37 offset:1024\n" \
M_"a[24:27], a[196:199], a[232:235], a[24:27]\n" \
X_"a[244:245], v37 offset:8192\n" \
M_"a[28:31], a[172:175], a[132:135], a[28:31]\n" \
"s_cmpk_lt_u32 s3, 3\n" \
"s_cselect_b32 s16, s17, s19\n" \
M_"a[28:31], a[236:239], a[196:199], a[28:31]\n" \
"s_addk_i32 s84, 0x2000\n" \
M_"a[20:23], a[164:167], a[132:135], a[20:23]\n" \
B_"v25, s[76:79], s84 offen offset:1024 lds\n" \
M_"a[20:23], a[228:231], a[196:199], a[20:23]\n" \
X_"a[246:247], v37 offset:9216\n" \
"s_and_b32 s16, s16, 0xffff\n" \
M_"a[16:19], a[160:163], a[132:135], a[16:19]\n" \
X_"a[184:185], v38 offset:0\n" \
"v_writelane_b32 v11, s16, 16\n" \
M_"a[16:19], a[224:227], a[196:199], a[16:19]\n" \
X_"a[186:187], v38 offset:1024\n" \
"s_mul_i32 s16, s5, 0x48\n" \
M_"a[32:35], a[160:163], a[136:139], a[32:35]\n" \
"s_addk_i32 s84, 0x2000\n" \
"s_nop 0\n" \
M_"a[32:35], a[224:227], a[200:203], a[32:35]\n" \
B_"v24, s[76:79], s84 offen offset:2048 lds\n" \
M_"a[36:39], a[164:167], a[136:139], a[36:39]\n" \
X_"a[248:249], v38 offset:8192\n" \
"s_add_i32 s17, s16, 0x47\n" \
M_"a[36:39], a[228:231], a[200:203], a[36:39]\n" \
X_"a[250:251], v38 offset:9216\n" \
M_"a[44:47], a[172:175], a[136:139], a[44:47]\n" \
X_"a[188:189], v39 offset:0\n" \
M_"a[44:47], a[236:239], a[200:203], a[44:47]\n" \
X_"a[190:191], v39 offset:1024\n" \
M_"a[40:43], a[136:139], a[168:171], a[40:43]\n" \
"s_add_u32 s89, s41, 0x3000\n" \
"buffer_store_dwordx4 v[184:187], v20, s[80:83], s89 offen offset:3072 sc1\n" \
M_"a[40:43], a[200:203], a[232:235], a[40:43]\n" \
X_"a[252:253], v39 offset:8192\n" \
M_"a[56:59], a[140:143], a[168:171], a[56:59]\n" \
X_"a[254:255], v39 offset:9216\n" \
M_"a[56:59], a[204:207], a[232:235], a[56:59]\n" \
"s_cmpk_lt_u32 s3, 3\n" \
"s_cselect_b32 s17, s17, s18\n" \
M_"a[60:63], a[172:175], a[140:143], a[60:63]\n" \
"s_mulk_i32 s17, 0x480\n" \
"s_addk_i32 s17, 0x120\n" \
M_"a[60:63], a[236:239], a[204:207], a[60:63]\n" \
"s_addk_i32 s84, 0x2000\n" \
M_"a[52:55], a[164:167], a[140:143], a[52:55]\n" \
B_"v25, s[76:79], s84 offen offset:3072 lds\n" \
M_"a[52:55], a[228:231], a[204:207], a[52:55]\n" \
"v_writelane_b32 v11, s17, 17\n" \
"s_lshl_b32 s17, s3, 5\n" \
M_"a[48:51], a[160:163], a[140:143], a[48:51]\n" \
"s_add_i32 s16, s16, s17\n" \
"s_mulk_i32 s16, 0x480\n" \
M_"a[48:51], a[224:227], a[204:207], a[48:51]\n" \
"s_mul_i32 s17, s12, 0x24000\n" \
"s_add_i32 s18, s17, 0x1b000\n" \
"s_waitcnt lgkmcnt(15)\n" \
M_"a[64:67], a[164:167], v[248:251], a[64:67]\n" \
"s_cmpk_lt_u32 s3, 3\n" \
"s_cselect_b32 s16, s16, s18\n" \
M_"a[64:67], a[228:231], v[48:51], a[64:67]\n" \
"s_xor_b32 s18, s3, 2\n" \
"v_writelane_b32 v11, s16, 18\n" \
M_"a[80:83], a[172:175], v[248:251], a[80:83]\n" \
"s_mul_i32 s16, s15, 0x2400\n" \
"s_mul_i32 s15, s15, 0x9000\n" \
M_"a[80:83], a[236:239], v[48:51], a[80:83]\n" \
"s_bitset1_b32 s16, 17\n" \
"v_writelane_b32 v11, s16, 22\n" \
M_"a[68:71], v[248:251], a[168:171], a[68:71]\n" \
"s_add_i32 s17, s17, s15\n" \
"s_mul_i32 s15, s3, 0x2400\n" \
"s_waitcnt vmcnt(11) lgkmcnt(0)\n" \
M_"a[68:71], v[48:51], a[232:235], a[68:71]\n" \
"s_barrier\n" \
"v_xor_b32_e32 v28, 0x20000, v28\n" \
"v_xor_b32_e32 v29, 0x20000, v29\n" \
M_"v[52:55], a[176:179], a[128:131], v[52:55]\n" \
"s_add_u32 m0, s40, 0x0\n" \
"s_nop 0\n" \
"s_add_u32 s85, s39, 0x8000\n" \
M_"v[52:55], a[240:243], a[192:195], v[52:55]\n" \
B_"v20, s[80:83], s85 offen sc1 lds\n" \
M_"v[56:59], a[180:183], a[128:131], v[56:59]\n" \
X_"a[160:161], v32 offset:32768\n" \
"v_writelane_b32 v11, s17, 29\n" \
M_"v[56:59], a[244:247], a[192:195], v[56:59]\n" \
X_"a[162:163], v32 offset:33280\n" \
"s_add_i32 s16, s15, 0xa400\n" \
M_"v[60:63], a[184:187], a[128:131], v[60:63]\n" \
"s_add_u32 m0, s40, 0x1000\n" \
"s_add_u32 s85, s39, 0x9000\n" \
M_"v[60:63], a[248:251], a[192:195], v[60:63]\n" \
B_"v20, s[80:83], s85 offen sc1 lds\n" \
M_"v[64:67], a[188:191], a[128:131], v[64:67]\n" \
X_"a[224:225], v32 offset:36864\n" \
"s_mov_b32 s17, 0x8000\n" \
M_"v[64:67], a[252:255], a[192:195], v[64:67]\n" \
R_"a[128:131], v28 offset:0\n" \
M_"v[80:83], a[188:191], a[132:135], v[80:83]\n" \
R_"a[192:195], v29 offset:0\n" \
M_"v[80:83], a[252:255], a[196:199], v[80:83]\n" \
"s_add_u32 s88, s41, 0x3000\n" \
"buffer_store_dwordx4 v[188:191], v20, s[80:83], s88 offen offset:2048 sc1\n" \
M_"v[76:79], a[184:187], a[132:135], v[76:79]\n" \
X_"a[226:227], v32 offset:37376\n" \
M_"v[76:79], a[248:251], a[196:199], v[76:79]\n" \
X_"a[164:165], v33 offset:32768\n" \
M_"v[72:75], a[180:183], a[132:135], v[72:75]\n" \
"s_add_u32 m0, s40, 0x3000\n" \
"s_movk_i32 s84, 0x200\n" \
M_"v[72:75], a[244:247], a[196:199], v[72:75]\n" \
B_"v21, s[76:79], s84 offen offset:0 lds\n" \
M_"v[68:71], a[176:179], a[132:135], v[68:71]\n" \
X_"a[166:167], v33 offset:33280\n" \
M_"v[68:71], a[240:243], a[196:199], v[68:71]\n" \
R_"a[132:135], v28 offset:2048\n" \
M_"v[84:87], a[176:179], a[136:139], v[84:87]\n" \
R_"a[196:199], v29 offset:2048\n" \
M_"v[84:87], a[240:243], a[200:203], v[84:87]\n" \
"s_add_u32 m0, s31, 0x0\n" \
"s_mov_b32 s84, 0x200000\n" \
M_"v[88:91], a[180:183], a[136:139], v[88:91]\n" \
B_"v26, s[68:71], s84 offen offset:0 lds\n" \
M_"v[88:91], a[244:247], a[200:203], v[88:91]\n" \
X_"a[228:229], v33 offset:36864\n" \
M_"v[92:95], a[184:187], a[136:139], v[92:95]\n" \
X_"a[230:231], v33 offset:37376\n" \
M_"v[92:95], a[248:251], a[200:203], v[92:95]\n" \
"s_addk_i32 s84, 0x7c00\n" \
M_"v[96:99], a[188:191], a[136:139], v[96:99]\n" \
B_"v26, s[68:71], s84 offen offset:1024 lds\n" \
M_"v[96:99], a[252:255], a[200:203], v[96:99]\n" \
R_"a[136:139], v28 offset:4096\n" \
M_"v[112:115], a[188:191], a[140:143], v[112:115]\n" \
R_"a[200:203], v29 offset:4096\n" \
M_"v[112:115], a[252:255], a[204:207], v[112:115]\n" \
"s_addk_i32 s84, 0x7c00\n" \
M_"v[108:111], a[184:187], a[140:143], v[108:111]\n" \
B_"v27, s[68:71], s84 offen offset:2048 lds\n" \
M_"v[108:111], a[248:251], a[204:207], v[108:111]\n" \
X_"a[172:173], v35 offset:32768\n" \
M_"v[104:107], a[180:183], a[140:143], v[104:107]\n" \
X_"a[174:175], v35 offset:33280\n" \
M_"v[104:107], a[244:247], a[204:207], v[104:107]\n" \
X_"a[236:237], v35 offset:36864\n" \
M_"v[100:103], a[176:179], a[140:143], v[100:103]\n" \
"s_cmpk_lt_u32 s3, 3\n" \
"s_cselect_b32 s16, s16, s17\n" \
M_"v[100:103], a[240:243], a[204:207], v[100:103]\n" \
R_"a[140:143], v28 offset:6144\n" \
"s_mov_b32 s17, 0x1b000\n" \
M_"v[116:119], a[176:179], a[144:147], v[116:119]\n" \
R_"a[204:207], v29 offset:6144\n" \
M_"v[116:119], a[240:243], a[208:211], v[116:119]\n" \
"s_addk_i32 s84, 0x7c00\n" \
M_"v[120:123], a[180:183], a[144:147], v[120:123]\n" \
B_"v27, s[68:71], s84 offen offset:3072 lds\n" \
M_"v[120:123], a[244:247], a[208:211], v[120:123]\n" \
"s_add_u32 m0, s30, 0x20000\n" \
"s_movk_i32 s84, 0x280\n" \
M_"v[124:127], a[184:187], a[144:147], v[124:127]\n" \
B_"v24, s[76:79], s84 offen offset:0 lds\n" \
M_"v[124:127], a[248:251], a[208:211], v[124:127]\n" \
X_"a[238:239], v35 offset:37376\n" \
M_"v[128:131], a[188:191], a[144:147], v[128:131]\n" \
"s_add_u32 s89, s42, 0x0\n" \
"buffer_store_dwordx4 v[216:219], v20, s[80:83], s89 offen offset:0 sc1\n" \
M_"v[128:131], a[252:255], a[208:211], v[128:131]\n" \
R_"a[144:147], v28 offset:8192\n" \
M_"v[144:147], a[188:191], a[148:151], v[144:147]\n" \
R_"a[208:211], v29 offset:8192\n" \
M_"v[144:147], a[252:255], a[212:215], v[144:147]\n" \
X_"a[168:169], v34 offset:32768\n" \
M_"v[140:143], a[184:187], a[148:151], v[140:143]\n" \
X_"a[170:171], v34 offset:33280\n" \
M_"v[140:143], a[248:251], a[212:215], v[140:143]\n" \
"s_movk_i32 s84, 0x2280\n" \
M_"v[136:139], a[180:183], a[148:151], v[136:139]\n" \
B_"v25, s[76:79], s84 offen offset:1024 lds\n" \
M_"v[136:139], a[244:247], a[212:215], v[136:139]\n" \
X_"a[232:233], v34 offset:36864\n" \
M_"v[132:135], a[176:179], a[148:151], v[132:135]\n" \
X_"a[234:235], v34 offset:37376\n" \
M_"v[132:135], a[240:243], a[212:215], v[132:135]\n" \
R_"a[148:151], v28 offset:10240\n" \
M_"v[148:151], a[176:179], a[152:155], v[148:151]\n" \
R_"a[212:215], v29 offset:10240\n" \
M_"v[148:151], a[240:243], a[216:219], v[148:151]\n" \
"s_movk_i32 s84, 0x4280\n" \
M_"v[152:155], a[180:183], a[152:155], v[152:155]\n" \
B_"v24, s[76:79], s84 offen offset:2048 lds\n" \
M_"v[152:155], a[244:247], a[216:219], v[152:155]\n" \
"s_movk_i32 s84, 0x6280\n" \
M_"v[156:159], a[184:187], a[152:155], v[156:159]\n" \
B_"v25, s[76:79], s84 offen offset:3072 lds\n" \
M_"v[156:159], a[248:251], a[216:219], v[156:159]\n" \
R_"v[248:251], v22 offset:32768\n" \
M_"v[160:163], a[188:191], a[152:155], v[160:163]\n" \
R_"v[48:51], v23 offset:32768\n" \
M_"v[160:163], a[252:255], a[216:219], v[160:163]\n" \
R_"a[152:155], v28 offset:12288\n" \
M_"v[176:179], a[188:191], a[156:159], v[176:179]\n" \
R_"a[216:219], v29 offset:12288\n" \
M_"v[176:179], a[252:255], a[220:223], v[176:179]\n" \
"s_cmp_lg_u32 s18, 0\n" \
"s_cselect_b32 s17, s16, s17\n" \
M_"v[172:175], a[184:187], a[156:159], v[172:175]\n" \
"v_writelane_b32 v11, s16, 23\n" \
"s_or_b32 s16, s15, 0x10000\n" \
M_"v[172:175], a[248:251], a[220:223], v[172:175]\n" \
"v_writelane_b32 v11, s17, 24\n" \
"s_add_i32 s17, s15, 0x19000\n" \
M_"v[168:171], a[180:183], a[156:159], v[168:171]\n" \
"v_writelane_b32 v11, s16, 25\n" \
"s_mov_b32 s16, 0\n" \
M_"v[168:171], a[244:247], a[220:223], v[168:171]\n" \
"s_cmpk_lt_u32 s3, 3\n" \
"s_cselect_b32 s17, s17, s16\n" \
M_"v[164:167], a[176:179], a[156:159], v[164:167]\n" \
"s_bitset1_b32 s15, 17\n" \
"v_writelane_b32 v11, s17, 26\n" \
M_"v[164:167], a[240:243], a[220:223], v[164:167]\n" \
R_"a[156:159], v28 offset:14336\n" \
R_"a[220:223], v29 offset:14336\n" \
"s_waitcnt lgkmcnt(15)\n" \
M_"a[0:3], a[160:163], a[128:131], a[0:3]\n" \
"s_add_u32 m0, s30, 0x21000\n" \
"s_addk_i32 s84, 0x3000\n" \
M_"a[0:3], a[224:227], a[192:195], a[0:3]\n" \
B_"v24, s[76:79], s84 offen offset:0 lds\n" \
M_"a[4:7], a[164:167], a[128:131], a[4:7]\n" \
X_"a[176:177], v36 offset:32768\n" \
"s_movk_i32 s17, 0x2400\n" \
M_"a[4:7], a[228:231], a[192:195], a[4:7]\n" \
X_"a[178:179], v36 offset:33792\n" \
M_"a[12:15], a[172:175], a[128:131], a[12:15]\n" \
X_"a[240:241], v36 offset:40960\n" \
"s_waitcnt lgkmcnt(15)\n" \
M_"a[12:15], a[236:239], a[192:195], a[12:15]\n" \
"s_add_u32 s88, s42, 0x1000\n" \
"buffer_store_dwordx4 v[220:223], v20, s[80:83], s88 offen offset:0 sc1\n" \
"s_waitcnt lgkmcnt(13)\n" \
M_"a[8:11], a[128:131], a[168:171], a[8:11]\n" \
X_"a[242:243], v36 offset:41984\n" \
"s_waitcnt lgkmcnt(12)\n" \
M_"a[8:11], a[192:195], a[232:235], a[8:11]\n" \
X_"a[180:181], v37 offset:32768\n" \
M_"a[24:27], a[132:135], a[168:171], a[24:27]\n" \
X_"a[182:183], v37 offset:33792\n" \
M_"a[24:27], a[196:199], a[232:235], a[24:27]\n" \
X_"a[244:245], v37 offset:40960\n" \
M_"a[28:31], a[172:175], a[132:135], a[28:31]\n" \
"s_cmpk_lt_u32 s3, 3\n" \
"s_cselect_b32 s15, s15, s17\n" \
M_"a[28:31], a[236:239], a[196:199], a[28:31]\n" \
"s_addk_i32 s84, 0x2000\n" \
M_"a[20:23], a[164:167], a[132:135], a[20:23]\n" \
B_"v25, s[76:79], s84 offen offset:1024 lds\n" \
M_"a[20:23], a[228:231], a[196:199], a[20:23]\n" \
X_"a[246:247], v37 offset:41984\n" \
"v_writelane_b32 v11, s15, 27\n" \
M_"a[16:19], a[160:163], a[132:135], a[16:19]\n" \
X_"a[184:185], v38 offset:32768\n" \
"s_lshl_b32 s15, s3, 10\n" \
M_"a[16:19], a[224:227], a[196:199], a[16:19]\n" \
X_"a[186:187], v38 offset:33792\n" \
"s_addk_i32 s15, 0x4800\n" \
M_"a[32:35], a[160:163], a[136:139], a[32:35]\n" \
"s_addk_i32 s84, 0x2000\n" \
M_"a[32:35], a[224:227], a[200:203], a[32:35]\n" \
B_"v24, s[76:79], s84 offen offset:2048 lds\n" \
M_"a[36:39], a[164:167], a[136:139], a[36:39]\n" \
X_"a[248:249], v38 offset:40960\n" \
"v_writelane_b32 v11, s15, 28\n" \
M_"a[36:39], a[228:231], a[200:203], a[36:39]\n" \
X_"a[250:251], v38 offset:41984\n" \
"s_add_i32 s15, s14, 1\n" \
M_"a[44:47], a[172:175], a[136:139], a[44:47]\n" \
X_"a[188:189], v39 offset:32768\n" \
"s_and_b32 s15, s15, 3\n" \
M_"a[44:47], a[236:239], a[200:203], a[44:47]\n" \
X_"a[190:191], v39 offset:33792\n" \
"s_or_b32 s15, s15, s4\n" \
M_"a[40:43], a[136:139], a[168:171], a[40:43]\n" \
"s_add_u32 s89, s42, 0x0\n" \
"buffer_store_dwordx4 v[192:195], v20, s[80:83], s89 offen offset:2048 sc1\n" \
M_"a[40:43], a[200:203], a[232:235], a[40:43]\n" \
X_"a[252:253], v39 offset:40960\n" \
"s_lshl_b32 s17, s15, 2\n" \
M_"a[56:59], a[140:143], a[168:171], a[56:59]\n" \
X_"a[254:255], v39 offset:41984\n" \
"s_or_b32 s17, s17, s3\n" \
M_"a[56:59], a[204:207], a[232:235], a[56:59]\n" \
"s_mul_i32 s17, s17, 0x9000\n" \
"v_writelane_b32 v11, s17, 30\n" \
M_"a[60:63], a[172:175], a[140:143], a[60:63]\n" \
"s_lshl_b32 s17, s12, 2\n" \
"s_or_b32 s17, s17, s3\n" \
M_"a[60:63], a[236:239], a[204:207], a[60:63]\n" \
"s_addk_i32 s84, 0x2000\n" \
M_"a[52:55], a[164:167], a[140:143], a[52:55]\n" \
B_"v25, s[76:79], s84 offen offset:3072 lds\n" \
M_"a[52:55], a[228:231], a[204:207], a[52:55]\n" \
"s_add_i32 s14, s14, -1\n" \
"s_xor_b32 s17, s17, 8\n" \
M_"a[48:51], a[160:163], a[140:143], a[48:51]\n" \
"s_and_b32 s14, s14, 3\n" \
"s_mul_i32 s17, s17, 0x9000\n" \
M_"a[48:51], a[224:227], a[204:207], a[48:51]\n" \
"s_or_b32 s14, s14, s4\n" \
"v_writelane_b32 v11, s17, 31\n" \
"s_waitcnt lgkmcnt(15)\n" \
M_"a[64:67], a[164:167], v[248:251], a[64:67]\n" \
"s_lshl_b32 s17, s14, 2\n" \
"s_or_b32 s17, s17, s3\n" \
M_"a[64:67], a[228:231], v[48:51], a[64:67]\n" \
"s_mul_i32 s17, s17, 0x9000\n" \
"v_writelane_b32 v11, s17, 32\n" \
M_"a[80:83], a[172:175], v[248:251], a[80:83]\n" \
"s_or_b32 s17, s8, s13\n" \
"s_mul_i32 s17, s17, 0x24800\n" \
M_"a[80:83], a[236:239], v[48:51], a[80:83]\n" \
"s_lshl_b32 s15, s15, 12\n" \
"s_add_i32 s18, s17, 0x3004800\n" \
M_"a[68:71], v[248:251], a[168:171], a[68:71]\n" \
"s_lshl_b32 s12, s12, 12\n" \
"s_add_i32 s19, s18, s15\n" \
"s_waitcnt vmcnt(11) lgkmcnt(0)\n" \
M_"a[68:71], v[48:51], a[232:235], a[68:71]\n" \
"s_barrier\n" \
M_"v[52:55], a[176:179], a[128:131], v[52:55]\n" \
"s_add_u32 m0, s40, 0x8000\n" \
"s_add_u32 s85, s39, 0xa000\n" \
M_"v[52:55], a[240:243], a[192:195], v[52:55]\n" \
B_"v20, s[80:83], s85 offen sc1 lds\n" \
M_"v[56:59], a[180:183], a[128:131], v[56:59]\n" \
X_"a[160:161], v32 offset:0\n" \
"v_accvgpr_read_b32 v224, a120\n" \
M_"v[56:59], a[244:247], a[192:195], v[56:59]\n" \
X_"a[162:163], v32 offset:512\n" \
"v_accvgpr_read_b32 v225, a121\n" \
M_"v[60:63], a[184:187], a[128:131], v[60:63]\n" \
"s_add_u32 m0, s40, 0x9000\n" \
"s_add_u32 s85, s39, 0xb000\n" \
M_"v[60:63], a[248:251], a[192:195], v[60:63]\n" \
B_"v20, s[80:83], s85 offen sc1 lds\n" \
M_"v[64:67], a[188:191], a[128:131], v[64:67]\n" \
X_"a[224:225], v32 offset:4096\n" \
"v_accvgpr_read_b32 v226, a122\n" \
M_"v[64:67], a[252:255], a[192:195], v[64:67]\n" \
R_"a[128:131], v28 offset:32768\n" \
"v_accvgpr_read_b32 v227, a123\n" \
M_"v[80:83], a[188:191], a[132:135], v[80:83]\n" \
R_"a[192:195], v29 offset:32768\n" \
"v_accvgpr_read_b32 v180, a124\n" \
M_"v[80:83], a[252:255], a[196:199], v[80:83]\n" \
"s_add_u32 s88, s42, 0x0\n" \
"buffer_store_dwordx4 v[196:199], v20, s[80:83], s88 offen offset:3072 sc1\n" \
M_"v[76:79], a[184:187], a[132:135], v[76:79]\n" \
X_"a[226:227], v32 offset:4608\n" \
"v_accvgpr_read_b32 v181, a125\n" \
M_"v[76:79], a[248:251], a[196:199], v[76:79]\n" \
X_"a[164:165], v33 offset:0\n" \
"v_accvgpr_read_b32 v182, a126\n" \
M_"v[72:75], a[180:183], a[132:135], v[72:75]\n" \
"s_add_u32 m0, s40, 0xb000\n" \
"s_movk_i32 s84, 0x280\n" \
M_"v[72:75], a[244:247], a[196:199], v[72:75]\n" \
B_"v21, s[76:79], s84 offen offset:0 lds\n" \
M_"v[68:71], a[176:179], a[132:135], v[68:71]\n" \
X_"a[166:167], v33 offset:512\n" \
"v_accvgpr_read_b32 v183, a127\n" \
M_"v[68:71], a[240:243], a[196:199], v[68:71]\n" \
R_"a[132:135], v28 offset:34816\n" \
C_"v224, v224, v225\n" \
M_"v[84:87], a[176:179], a[136:139], v[84:87]\n" \
R_"a[196:199], v29 offset:34816\n" \
C_"v225, v226, v227\n" \
M_"v[84:87], a[240:243], a[200:203], v[84:87]\n" \
"s_add_u32 m0, s31, 0x8000\n" \
"s_mov_b32 s84, 0x280000\n" \
M_"v[88:91], a[180:183], a[136:139], v[88:91]\n" \
B_"v26, s[68:71], s84 offen offset:0 lds\n" \
M_"v[88:91], a[244:247], a[200:203], v[88:91]\n" \
X_"a[228:229], v33 offset:4096\n" \
C_"v226, v180, v181\n" \
M_"v[92:95], a[184:187], a[136:139], v[92:95]\n" \
X_"a[230:231], v33 offset:4608\n" \
C_"v227, v182, v183\n" \
M_"v[92:95], a[248:251], a[200:203], v[92:95]\n" \
"s_addk_i32 s84, 0x7c00\n" \
M_"v[96:99], a[188:191], a[136:139], v[96:99]\n" \
B_"v26, s[68:71], s84 offen offset:1024 lds\n" \
M_"v[96:99], a[252:255], a[200:203], v[96:99]\n" \
R_"a[136:139], v28 offset:36864\n" \
"v_accvgpr_read_b32 v220, a112\n" \
M_"v[112:115], a[188:191], a[140:143], v[112:115]\n" \
R_"a[200:203], v29 offset:36864\n" \
"v_accvgpr_read_b32 v221, a113\n" \
M_"v[112:115], a[252:255], a[204:207], v[112:115]\n" \
"s_addk_i32 s84, 0x7c00\n" \
M_"v[108:111], a[184:187], a[140:143], v[108:111]\n" \
B_"v27, s[68:71], s84 offen offset:2048 lds\n" \
M_"v[108:111], a[248:251], a[204:207], v[108:111]\n" \
X_"a[172:173], v35 offset:0\n" \
"v_accvgpr_read_b32 v222, a114\n" \
M_"v[104:107], a[180:183], a[140:143], v[104:107]\n" \
X_"a[174:175], v35 offset:512\n" \
"v_accvgpr_read_b32 v223, a115\n" \
M_"v[104:107], a[244:247], a[204:207], v[104:107]\n" \
X_"a[236:237], v35 offset:4096\n" \
"v_accvgpr_read_b32 v180, a116\n" \
M_"v[100:103], a[176:179], a[140:143], v[100:103]\n" \
"v_accvgpr_read_b32 v181, a117\n" \
"v_accvgpr_read_b32 v182, a118\n" \
M_"v[100:103], a[240:243], a[204:207], v[100:103]\n" \
R_"a[140:143], v28 offset:38912\n" \
"v_accvgpr_read_b32 v183, a119\n" \
M_"v[116:119], a[176:179], a[144:147], v[116:119]\n" \
R_"a[204:207], v29 offset:38912\n" \
C_"v220, v220, v221\n" \
M_"v[116:119], a[240:243], a[208:211], v[116:119]\n" \
"s_addk_i32 s84, 0x7c00\n" \
M_"v[120:123], a[180:183], a[144:147], v[120:123]\n" \
B_"v27, s[68:71], s84 offen offset:3072 lds\n" \
M_"v[120:123], a[244:247], a[208:211], v[120:123]\n" \
"s_add_u32 m0, s30, 0x0\n" \
"s_movk_i32 s84, 0x300\n" \
"s_nop 0\n" \
M_"v[124:127], a[184:187], a[144:147], v[124:127]\n" \
B_"v24, s[76:79], s84 offen offset:0 lds\n" \
M_"v[124:127], a[248:251], a[208:211], v[124:127]\n" \
X_"a[238:239], v35 offset:4608\n" \
C_"v221, v222, v223\n" \
M_"v[128:131], a[188:191], a[144:147], v[128:131]\n" \
"s_add_u32 s89, s42, 0x1000\n" \
"buffer_store_dwordx4 a[84:87], v20, s[80:83], s89 offen offset:3072 sc1\n" \
M_"v[128:131], a[252:255], a[208:211], v[128:131]\n" \
R_"a[144:147], v28 offset:40960\n" \
C_"v222, v180, v181\n" \
M_"v[144:147], a[188:191], a[148:151], v[144:147]\n" \
R_"a[208:211], v29 offset:40960\n" \
C_"v223, v182, v183\n" \
M_"v[144:147], a[252:255], a[212:215], v[144:147]\n" \
X_"a[168:169], v34 offset:0\n" \
"v_accvgpr_read_b32 v216, a104\n" \
M_"v[140:143], a[184:187], a[148:151], v[140:143]\n" \
X_"a[170:171], v34 offset:512\n" \
"v_accvgpr_read_b32 v217, a105\n" \
M_"v[140:143], a[248:251], a[212:215], v[140:143]\n" \
"s_movk_i32 s84, 0x2300\n" \
"s_nop 0\n" \
M_"v[136:139], a[180:183], a[148:151], v[136:139]\n" \
B_"v25, s[76:79], s84 offen offset:1024 lds\n" \
M_"v[136:139], a[244:247], a[212:215], v[136:139]\n" \
X_"a[232:233], v34 offset:4096\n" \
"v_accvgpr_read_b32 v218, a106\n" \
M_"v[132:135], a[176:179], a[148:151], v[132:135]\n" \
X_"a[234:235], v34 offset:4608\n" \
"v_accvgpr_read_b32 v219, a107\n" \
M_"v[132:135], a[240:243], a[212:215], v[132:135]\n" \
R_"a[148:151], v28 offset:43008\n" \
"v_accvgpr_read_b32 v180, a108\n" \
M_"v[148:151], a[176:179], a[152:155], v[148:151]\n" \
R_"a[212:215], v29 offset:43008\n" \
"v_accvgpr_read_b32 v181, a109\n" \
M_"v[148:151], a[240:243], a[216:219], v[148:151]\n" \
"s_movk_i32 s84, 0x4300\n" \
M_"v[152:155], a[180:183], a[152:155], v[152:155]\n" \
B_"v24, s[76:79], s84 offen offset:2048 lds\n" \
M_"v[152:155], a[244:247], a[216:219], v[152:155]\n" \
"s_movk_i32 s84, 0x6300\n" \
M_"v[156:159], a[184:187], a[152:155], v[156:159]\n" \
B_"v25, s[76:79], s84 offen offset:3072 lds\n" \
M_"v[156:159], a[248:251], a[216:219], v[156:159]\n" \
R_"v[248:251], v22 offset:0\n" \
"v_accvgpr_read_b32 v182, a110\n" \
M_"v[160:163], a[188:191], a[152:155], v[160:163]\n" \
R_"v[48:51], v23 offset:0\n" \
"v_accvgpr_read_b32 v183, a111\n" \
M_"v[160:163], a[252:255], a[216:219], v[160:163]\n" \
R_"a[152:155], v28 offset:45056\n" \
C_"v216, v216, v217\n" \
M_"v[176:179], a[188:191], a[156:159], v[176:179]\n" \
R_"a[216:219], v29 offset:45056\n" \
C_"v217, v218, v219\n" \
M_"v[176:179], a[252:255], a[220:223], v[176:179]\n" \
C_"v218, v180, v181\n" \
C_"v219, v182, v183\n" \
M_"v[172:175], a[184:187], a[156:159], v[172:175]\n" \
"v_accvgpr_read_b32 v192, a96\n" \
"v_accvgpr_read_b32 v193, a97\n" \
M_"v[172:175], a[248:251], a[220:223], v[172:175]\n" \
"v_accvgpr_read_b32 v194, a98\n" \
"v_accvgpr_read_b32 v195, a99\n" \
M_"v[168:171], a[180:183], a[156:159], v[168:171]\n" \
"v_accvgpr_read_b32 v180, a100\n" \
"v_accvgpr_read_b32 v181, a101\n" \
M_"v[168:171], a[244:247], a[220:223], v[168:171]\n" \
"v_accvgpr_read_b32 v182, a102\n" \
"v_accvgpr_read_b32 v183, a103\n" \
M_"v[164:167], a[176:179], a[156:159], v[164:167]\n" \
C_"v192, v192, v193\n" \
C_"v193, v194, v195\n" \
M_"v[164:167], a[240:243], a[220:223], v[164:167]\n" \
R_"a[156:159], v28 offset:47104\n" \
R_"a[220:223], v29 offset:47104\n" \
"s_waitcnt lgkmcnt(15)\n" \
M_"a[0:3], a[160:163], a[128:131], a[0:3]\n" \
"s_add_u32 m0, s30, 0x1000\n" \
"s_addk_i32 s84, 0x3000\n" \
M_"a[0:3], a[224:227], a[192:195], a[0:3]\n" \
B_"v24, s[76:79], s84 offen offset:0 lds\n" \
M_"a[4:7], a[164:167], a[128:131], a[4:7]\n" \
X_"a[176:177], v36 offset:0\n" \
C_"v194, v180, v181\n" \
M_"a[4:7], a[228:231], a[192:195], a[4:7]\n" \
X_"a[178:179], v36 offset:1024\n" \
C_"v195, v182, v183\n" \
M_"a[12:15], a[172:175], a[128:131], a[12:15]\n" \
X_"a[240:241], v36 offset:8192\n" \
"v_accvgpr_read_b32 v188, a88\n" \
"s_waitcnt lgkmcnt(15)\n" \
M_"a[12:15], a[236:239], a[192:195], a[12:15]\n" \
"s_add_u32 s88, s42, 0x1000\n" \
"buffer_store_dwordx4 v[252:255], v20, s[80:83], s88 offen offset:2048 sc1\n" \
"s_waitcnt lgkmcnt(13)\n" \
M_"a[8:11], a[128:131], a[168:171], a[8:11]\n" \
X_"a[242:243], v36 offset:9216\n" \
"v_accvgpr_read_b32 v189, a89\n" \
"s_waitcnt lgkmcnt(12)\n" \
"s_nop 0\n" \
M_"a[8:11], a[192:195], a[232:235], a[8:11]\n" \
X_"a[180:181], v37 offset:0\n" \
"v_accvgpr_read_b32 v190, a90\n" \
M_"a[24:27], a[132:135], a[168:171], a[24:27]\n" \
X_"a[182:183], v37 offset:1024\n" \
"v_accvgpr_read_b32 v191, a91\n" \
M_"a[24:27], a[196:199], a[232:235], a[24:27]\n" \
X_"a[244:245], v37 offset:8192\n" \
"v_accvgpr_read_b32 v180, a92\n" \
M_"a[28:31], a[172:175], a[132:135], a[28:31]\n" \
"v_accvgpr_read_b32 v181, a93\n" \
"v_accvgpr_read_b32 v182, a94\n" \
M_"a[28:31], a[236:239], a[196:199], a[28:31]\n" \
".fill (L_pb + 28384 - .) / 4, 4, 0xBF800000\n" \
"L_pp7:\n" \
"s_cbranch_execz L_pret\n" \
"s_addk_i32 s84, 0x2000\n" \
M_"a[20:23], a[164:167], a[132:135], a[20:23]\n" \
B_"v25, s[76:79], s84 offen offset:1024 lds\n" \
M_"a[20:23], a[228:231], a[196:199], a[20:23]\n" \
X_"a[246:247], v37 offset:9216\n" \
"v_accvgpr_read_b32 v183, a95\n" \
M_"a[16:19], a[160:163], a[132:135], a[16:19]\n" \
X_"a[184:185], v38 offset:0\n" \
C_"v188, v188, v189\n" \
M_"a[16:19], a[224:227], a[196:199], a[16:19]\n" \
X_"a[186:187], v38 offset:1024\n" \
C_"v189, v190, v191\n" \
M_"a[32:35], a[160:163], a[136:139], a[32:35]\n" \
"s_addk_i32 s84, 0x2000\n" \
"s_nop 0\n" \
M_"a[32:35], a[224:227], a[200:203], a[32:35]\n" \
B_"v24, s[76:79], s84 offen offset:2048 lds\n" \
M_"a[36:39], a[164:167], a[136:139], a[36:39]\n" \
X_"a[248:249], v38 offset:8192\n" \
C_"v190, v180, v181\n" \
M_"a[36:39], a[228:231], a[200:203], a[36:39]\n" \
X_"a[250:251], v38 offset:9216\n" \
C_"v191, v182, v183\n" \
M_"a[44:47], a[172:175], a[136:139], a[44:47]\n" \
X_"a[188:189], v39 offset:0\n" \
"v_accvgpr_read_b32 v184, a72\n" \
M_"a[44:47], a[236:239], a[200:203], a[44:47]\n" \
X_"a[190:191], v39 offset:1024\n" \
"v_accvgpr_read_b32 v185, a73\n" \
M_"a[40:43], a[136:139], a[168:171], a[40:43]\n" \
"s_add_u32 s89, s42, 0x2000\n" \
"buffer_store_dwordx4 v[200:203], v20, s[80:83], s89 offen offset:2048 sc1\n" \
M_"a[40:43], a[200:203], a[232:235], a[40:43]\n" \
X_"a[252:253], v39 offset:8192\n" \
"v_accvgpr_read_b32 v186, a74\n" \
M_"a[56:59], a[140:143], a[168:171], a[56:59]\n" \
X_"a[254:255], v39 offset:9216\n" \
"v_accvgpr_read_b32 v187, a75\n" \
M_"a[56:59], a[204:207], a[232:235], a[56:59]\n" \
"v_accvgpr_read_b32 v180, a76\n" \
"v_accvgpr_read_b32 v181, a77\n" \
M_"a[60:63], a[172:175], a[140:143], a[60:63]\n" \
"v_accvgpr_read_b32 v182, a78\n" \
"v_accvgpr_read_b32 v183, a79\n" \
M_"a[60:63], a[236:239], a[204:207], a[60:63]\n" \
"s_addk_i32 s84, 0x2000\n" \
"s_nop 0\n" \
M_"a[52:55], a[164:167], a[140:143], a[52:55]\n" \
B_"v25, s[76:79], s84 offen offset:3072 lds\n" \
M_"a[52:55], a[228:231], a[204:207], a[52:55]\n" \
C_"v184, v184, v185\n" \
C_"v185, v186, v187\n" \
M_"a[48:51], a[160:163], a[140:143], a[48:51]\n" \
C_"v186, v180, v181\n" \
C_"v187, v182, v183\n" \
M_"a[48:51], a[224:227], a[204:207], a[48:51]\n" \
"v_writelane_b32 v11, s19, 33\n" \
"s_xor_b32 s12, s12, 0x2000\n" \
"s_waitcnt lgkmcnt(15)\n" \
M_"a[64:67], a[164:167], v[248:251], a[64:67]\n" \
"s_lshl_b32 s14, s14, 12\n" \
"s_sub_i32 s13, s8, s13\n" \
M_"a[64:67], a[228:231], v[48:51], a[64:67]\n" \
"s_add_i32 s19, s18, s12\n" \
"v_writelane_b32 v11, s19, 34\n" \
M_"a[80:83], a[172:175], v[248:251], a[80:83]\n" \
"s_add_i32 s18, s18, s14\n" \
"s_mul_i32 s13, s13, 0x24800\n" \
M_"a[80:83], a[236:239], v[48:51], a[80:83]\n" \
"v_writelane_b32 v11, s18, 35\n" \
"s_add_i32 s18, s13, 0x4120800\n" \
M_"a[68:71], v[248:251], a[168:171], a[68:71]\n" \
"s_add_i32 s15, s18, s15\n" \
"v_writelane_b32 v11, s15, 36\n" \
"s_waitcnt vmcnt(11) lgkmcnt(0)\n" \
M_"a[68:71], v[48:51], a[232:235], a[68:71]\n" \
"s_barrier\n" \
"v_mov_b32_e32 v45, s62\n" \
"s_mov_b64 exec, 1\n" \
"buffer_store_dword v45, off, s[80:83], s37 offset:0 sc1\n" \
"s_mov_b64 exec, -1\n" \
"v_xor_b32_e32 v28, 0x20000, v28\n" \
"v_xor_b32_e32 v29, 0x20000, v29\n" \
M_"v[52:55], a[176:179], a[128:131], v[52:55]\n" \
"s_add_u32 m0, s40, 0x0\n" \
"s_add_u32 s85, s39, 0xc000\n" \
M_"v[52:55], a[240:243], a[192:195], v[52:55]\n" \
B_"v20, s[80:83], s85 offen sc1 lds\n" \
M_"v[56:59], a[180:183], a[128:131], v[56:59]\n" \
X_"a[160:161], v32 offset:32768\n" \
"s_add_i32 s12, s18, s12\n" \
M_"v[56:59], a[244:247], a[192:195], v[56:59]\n" \
X_"a[162:163], v32 offset:33280\n" \
"v_writelane_b32 v11, s12, 37\n" \
M_"v[60:63], a[184:187], a[128:131], v[60:63]\n" \
"s_add_u32 m0, s40, 0x1000\n" \
"s_add_u32 s85, s39, 0xd000\n" \
M_"v[60:63], a[248:251], a[192:195], v[60:63]\n" \
B_"v20, s[80:83], s85 offen sc1 lds\n" \
M_"v[64:67], a[188:191], a[128:131], v[64:67]\n" \
X_"a[224:225], v32 offset:36864\n" \
"s_add_i32 s18, s18, s14\n" \
M_"v[64:67], a[252:255], a[192:195], v[64:67]\n" \
R_"a[128:131], v28 offset:0\n" \
"s_or_b32 s12, s17, 0x3000000\n" \
M_"v[80:83], a[188:191], a[132:135], v[80:83]\n" \
R_"a[192:195], v29 offset:0\n" \
"s_lshl_b32 s14, s5, 11\n" \
M_"v[80:83], a[252:255], a[196:199], v[80:83]\n" \
"s_add_u32 m0, s40, 0x3000\n" \
"s_movk_i32 s84, 0x300\n" \
M_"v[76:79], a[184:187], a[132:135], v[76:79]\n" \
B_"v21, s[76:79], s84 offen offset:0 lds\n" \
M_"v[76:79], a[248:251], a[196:199], v[76:79]\n" \
X_"a[226:227], v32 offset:37376\n" \
"v_writelane_b32 v11, s18, 38\n" \
M_"v[72:75], a[180:183], a[132:135], v[72:75]\n" \
X_"a[164:165], v33 offset:32768\n" \
"s_add_i32 s15, s12, s14\n" \
M_"v[72:75], a[244:247], a[196:199], v[72:75]\n" \
"v_writelane_b32 v11, s15, 39\n" \
"s_add_i32 s15, s13, 0x411c000\n" \
M_"v[68:71], a[176:179], a[132:135], v[68:71]\n" \
X_"a[166:167], v33 offset:33280\n" \
"s_add_i32 s18, s15, s14\n" \
M_"v[68:71], a[240:243], a[196:199], v[68:71]\n" \
R_"a[132:135], v28 offset:2048\n" \
"v_writelane_b32 v11, s18, 40\n" \
M_"v[84:87], a[176:179], a[136:139], v[84:87]\n" \
R_"a[196:199], v29 offset:2048\n" \
"s_add_i32 s17, s17, 0x3004000\n" \
M_"v[84:87], a[240:243], a[200:203], v[84:87]\n" \
"s_add_u32 m0, s31, 0x0\n" \
"s_nop 0\n" \
"s_mov_b32 s84, 0x300000\n" \
M_"v[88:91], a[180:183], a[136:139], v[88:91]\n" \
B_"v26, s[68:71], s84 offen offset:0 lds\n" \
M_"v[88:91], a[244:247], a[200:203], v[88:91]\n" \
"s_add_u32 s88, s42, 0x2000\n" \
"buffer_store_dwordx4 v[204:207], v20, s[80:83], s88 offen offset:3072 sc1\n" \
M_"v[92:95], a[184:187], a[136:139], v[92:95]\n" \
X_"a[228:229], v33 offset:36864\n" \
"v_writelane_b32 v11, s17, 41\n" \
M_"v[92:95], a[248:251], a[200:203], v[92:95]\n" \
X_"a[230:231], v33 offset:37376\n" \
"s_add_i32 s13, s13, 0x4120000\n" \
M_"v[96:99], a[188:191], a[136:139], v[96:99]\n" \
"v_writelane_b32 v11, s13, 42\n" \
"s_lshl_b32 s13, s9, 2\n" \
M_"v[96:99], a[252:255], a[200:203], v[96:99]\n" \
R_"a[136:139], v28 offset:4096\n" \
"s_or_b32 s7, s13, s7\n" \
M_"v[112:115], a[188:191], a[140:143], v[112:115]\n" \
R_"a[200:203], v29 offset:4096\n" \
"s_mul_i32 s7, s7, 0x90000\n" \
M_"v[112:115], a[252:255], a[204:207], v[112:115]\n" \
"s_addk_i32 s84, 0x7c00\n" \
M_"v[108:111], a[184:187], a[140:143], v[108:111]\n" \
B_"v26, s[68:71], s84 offen offset:1024 lds\n" \
M_"v[108:111], a[248:251], a[204:207], v[108:111]\n" \
X_"a[172:173], v35 offset:32768\n" \
"s_lshl_b32 s13, s3, 14\n" \
M_"v[104:107], a[180:183], a[140:143], v[104:107]\n" \
"s_addk_i32 s84, 0x7c00\n" \
M_"v[104:107], a[244:247], a[204:207], v[104:107]\n" \
B_"v27, s[68:71], s84 offen offset:2048 lds\n" \
M_"v[100:103], a[176:179], a[140:143], v[100:103]\n" \
X_"a[174:175], v35 offset:33280\n" \
"s_lshl_b32 s9, s9, 7\n" \
M_"v[100:103], a[240:243], a[204:207], v[100:103]\n" \
R_"a[140:143], v28 offset:6144\n" \
"s_lshl_b32 s8, s8, 4\n" \
M_"v[116:119], a[176:179], a[144:147], v[116:119]\n" \
R_"a[204:207], v29 offset:6144\n" \
"s_add_i32 s7, s7, s13\n" \
M_"v[116:119], a[240:243], a[208:211], v[116:119]\n" \
"s_addk_i32 s84, 0x7c00\n" \
M_"v[120:123], a[180:183], a[144:147], v[120:123]\n" \
B_"v27, s[68:71], s84 offen offset:3072 lds\n" \
M_"v[120:123], a[244:247], a[208:211], v[120:123]\n" \
X_"a[236:237], v35 offset:36864\n" \
"s_or_b32 s13, s8, s9\n" \
M_"v[124:127], a[184:187], a[144:147], v[124:127]\n" \
X_"a[238:239], v35 offset:37376\n" \
"s_sub_i32 s8, s8, s9\n" \
M_"v[124:127], a[248:251], a[208:211], v[124:127]\n" \
"s_add_u32 m0, s30, 0x8000\n" \
"s_movk_i32 s84, 0x380\n" \
M_"v[128:131], a[188:191], a[144:147], v[128:131]\n" \
B_"v24, s[76:79], s84 offen offset:0 lds\n" \
M_"v[128:131], a[252:255], a[208:211], v[128:131]\n" \
R_"a[144:147], v28 offset:8192\n" \
"s_mulk_i32 s13, 0x480\n" \
M_"v[144:147], a[188:191], a[148:151], v[144:147]\n" \
R_"a[208:211], v29 offset:8192\n" \
"s_mulk_i32 s8, 0x480\n" \
M_"v[144:147], a[252:255], a[212:215], v[144:147]\n" \
X_"a[168:169], v34 offset:32768\n" \
"s_or_b32 s13, s13, s11\n" \
M_"v[140:143], a[184:187], a[148:151], v[140:143]\n" \
"s_movk_i32 s84, 0x2380\n" \
M_"v[140:143], a[248:251], a[212:215], v[140:143]\n" \
B_"v25, s[76:79], s84 offen offset:1024 lds\n" \
M_"v[136:139], a[180:183], a[148:151], v[136:139]\n" \
X_"a[170:171], v34 offset:33280\n" \
"s_or_b32 s8, s8, s11\n" \
M_"v[136:139], a[244:247], a[212:215], v[136:139]\n" \
X_"a[232:233], v34 offset:36864\n" \
"s_and_b32 s11, s2, 1\n" \
M_"v[132:135], a[176:179], a[148:151], v[132:135]\n" \
X_"a[234:235], v34 offset:37376\n" \
M_"v[132:135], a[240:243], a[212:215], v[132:135]\n" \
R_"a[148:151], v28 offset:10240\n" \
M_"v[148:151], a[176:179], a[152:155], v[148:151]\n" \
R_"a[212:215], v29 offset:10240\n" \
M_"v[148:151], a[240:243], a[216:219], v[148:151]\n" \
"s_movk_i32 s84, 0x4380\n" \
M_"v[152:155], a[180:183], a[152:155], v[152:155]\n" \
B_"v24, s[76:79], s84 offen offset:2048 lds\n" \
M_"v[152:155], a[244:247], a[216:219], v[152:155]\n" \
"s_movk_i32 s84, 0x6380\n" \
M_"v[156:159], a[184:187], a[152:155], v[156:159]\n" \
B_"v25, s[76:79], s84 offen offset:3072 lds\n" \
M_"v[156:159], a[248:251], a[216:219], v[156:159]\n" \
R_"v[248:251], v22 offset:32768\n" \
M_"v[160:163], a[188:191], a[152:155], v[160:163]\n" \
R_"v[48:51], v23 offset:32768\n" \
M_"v[160:163], a[252:255], a[216:219], v[160:163]\n" \
R_"a[152:155], v28 offset:12288\n" \
M_"v[176:179], a[188:191], a[156:159], v[176:179]\n" \
R_"a[216:219], v29 offset:12288\n" \
M_"v[176:179], a[252:255], a[220:223], v[176:179]\n" \
"s_cmp_lg_u32 s11, 0\n" \
"s_cselect_b32 s12, s15, s12\n" \
M_"v[172:175], a[184:187], a[156:159], v[172:175]\n" \
"s_bfe_u32 s9, s2, 0x20001\n" \
"s_add_i32 s15, s7, 0x4240000\n" \
M_"v[172:175], a[248:251], a[220:223], v[172:175]\n" \
"s_addk_i32 s12, 0x4000\n" \
"s_lshl_b32 s5, s5, 16\n" \
M_"v[168:171], a[180:183], a[156:159], v[168:171]\n" \
"s_xor_b32 s9, s9, s3\n" \
M_"v[168:171], a[244:247], a[220:223], v[168:171]\n" \
"s_cmp_lg_u32 s9, 0\n" \
"s_cselect_b32 s12, s6, s12\n" \
M_"v[164:167], a[176:179], a[156:159], v[164:167]\n" \
"s_add_i32 s5, s15, s5\n" \
"v_writelane_b32 v11, s12, 43\n" \
M_"v[164:167], a[240:243], a[220:223], v[164:167]\n" \
R_"a[156:159], v28 offset:14336\n" \
R_"a[220:223], v29 offset:14336\n" \
"s_waitcnt lgkmcnt(15)\n" \
M_"a[0:3], a[160:163], a[128:131], a[0:3]\n" \
"s_add_u32 m0, s30, 0x9000\n" \
"s_addk_i32 s84, 0x3000\n" \
M_"a[0:3], a[224:227], a[192:195], a[0:3]\n" \
B_"v24, s[76:79], s84 offen offset:0 lds\n" \
M_"a[4:7], a[164:167], a[128:131], a[4:7]\n" \
X_"a[176:177], v36 offset:32768\n" \
"s_add_i32 s7, s7, 0x42c0000\n" \
M_"a[4:7], a[228:231], a[192:195], a[4:7]\n" \
X_"a[178:179], v36 offset:33792\n" \
"v_writelane_b32 v11, s5, 44\n" \
M_"a[12:15], a[172:175], a[128:131], a[12:15]\n" \
X_"a[240:241], v36 offset:40960\n" \
"s_lshl_b32 s5, s11, 10\n" \
"s_waitcnt lgkmcnt(15)\n" \
M_"a[12:15], a[236:239], a[192:195], a[12:15]\n" \
"s_add_u32 s89, s42, 0x3000\n" \
"buffer_store_dwordx4 v[208:211], v20, s[80:83], s89 offen offset:3072 sc1\n" \
"s_waitcnt lgkmcnt(13)\n" \
M_"a[8:11], a[128:131], a[168:171], a[8:11]\n" \
X_"a[242:243], v36 offset:41984\n" \
"s_or_b32 s5, s7, s5\n" \
"s_waitcnt lgkmcnt(12)\n" \
M_"a[8:11], a[192:195], a[232:235], a[8:11]\n" \
X_"a[180:181], v37 offset:32768\n" \
M_"a[24:27], a[132:135], a[168:171], a[24:27]\n" \
X_"a[182:183], v37 offset:33792\n" \
M_"a[24:27], a[196:199], a[232:235], a[24:27]\n" \
X_"a[244:245], v37 offset:40960\n" \
M_"a[28:31], a[172:175], a[132:135], a[28:31]\n" \
"s_cmp_lg_u32 s9, 0\n" \
"s_cselect_b32 s5, s6, s5\n" \
M_"a[28:31], a[236:239], a[196:199], a[28:31]\n" \
"s_addk_i32 s84, 0x2000\n" \
M_"a[20:23], a[164:167], a[132:135], a[20:23]\n" \
B_"v25, s[76:79], s84 offen offset:1024 lds\n" \
M_"a[20:23], a[228:231], a[196:199], a[20:23]\n" \
X_"a[246:247], v37 offset:41984\n" \
"s_add_i32 s8, s8, 0x21c000\n" \
M_"a[16:19], a[160:163], a[132:135], a[16:19]\n" \
X_"a[184:185], v38 offset:32768\n" \
"v_writelane_b32 v11, s5, 45\n" \
M_"a[16:19], a[224:227], a[196:199], a[16:19]\n" \
X_"a[186:187], v38 offset:33792\n" \
"s_add_i32 s5, s13, s10\n" \
M_"a[32:35], a[160:163], a[136:139], a[32:35]\n" \
"s_addk_i32 s84, 0x2000\n" \
M_"a[32:35], a[224:227], a[200:203], a[32:35]\n" \
B_"v24, s[76:79], s84 offen offset:2048 lds\n" \
M_"a[36:39], a[164:167], a[136:139], a[36:39]\n" \
X_"a[248:249], v38 offset:40960\n" \
"v_writelane_b32 v11, s5, 46\n" \
M_"a[36:39], a[228:231], a[200:203], a[36:39]\n" \
X_"a[250:251], v38 offset:41984\n" \
"s_add_i32 s5, s8, s10\n" \
"s_nop 0\n" \
M_"a[44:47], a[172:175], a[136:139], a[44:47]\n" \
X_"a[188:189], v39 offset:32768\n" \
"v_writelane_b32 v11, s5, 47\n" \
M_"a[44:47], a[236:239], a[200:203], a[44:47]\n" \
X_"a[190:191], v39 offset:33792\n" \
M_"a[40:43], a[136:139], a[168:171], a[40:43]\n" \
"s_add_u32 s88, s42, 0x3000\n" \
"buffer_store_dwordx4 v[212:215], v20, s[80:83], s88 offen offset:2048 sc1\n" \
M_"a[40:43], a[200:203], a[232:235], a[40:43]\n" \
X_"a[252:253], v39 offset:40960\n" \
M_"a[56:59], a[140:143], a[168:171], a[56:59]\n" \
X_"a[254:255], v39 offset:41984\n" \
M_"a[56:59], a[204:207], a[232:235], a[56:59]\n" \
"s_cmp_lg_u32 s11, 0\n" \
"s_cselect_b32 s5, s8, s13\n" \
M_"a[60:63], a[172:175], a[140:143], a[60:63]\n" \
"s_addk_i32 s5, 0x100\n" \
M_"a[60:63], a[236:239], a[204:207], a[60:63]\n" \
"s_addk_i32 s84, 0x2000\n" \
M_"a[52:55], a[164:167], a[140:143], a[52:55]\n" \
B_"v25, s[76:79], s84 offen offset:3072 lds\n" \
M_"a[52:55], a[228:231], a[204:207], a[52:55]\n" \
"s_cmp_lg_u32 s9, 0\n" \
"s_cselect_b32 s5, s6, s5\n" \
M_"a[48:51], a[160:163], a[140:143], a[48:51]\n" \
"v_writelane_b32 v11, s5, 48\n" \
"s_or_b32 s5, s7, s14\n" \
M_"a[48:51], a[224:227], a[204:207], a[48:51]\n" \
"v_writelane_b32 v11, s5, 56\n" \
"s_lshl_b32 s5, s3, 17\n" \
"s_waitcnt lgkmcnt(15)\n" \
M_"a[64:67], a[164:167], v[248:251], a[64:67]\n" \
"s_add_i32 s5, s15, s5\n" \
"s_or_b32 s4, s5, s4\n" \
M_"a[64:67], a[228:231], v[48:51], a[64:67]\n" \
"s_or_b32 s4, s4, 0x3840\n" \
"v_writelane_b32 v11, s4, 6\n" \
M_"a[80:83], a[172:175], v[248:251], a[80:83]\n" \
"s_mov_b32 s4, 0xff0000\n" \
M_"a[80:83], a[236:239], v[48:51], a[80:83]\n" \
"s_cmp_lg_u32 s9, 0\n" \
"s_cselect_b32 s4, s16, s4\n" \
M_"a[68:71], v[248:251], a[168:171], a[68:71]\n" \
"v_writelane_b32 v11, s4, 7\n" \
"s_lshl_b32 s4, s2, 16\n" \
"s_waitcnt vmcnt(10) lgkmcnt(0)\n" \
M_"a[68:71], v[48:51], a[232:235], a[68:71]\n" \
"s_barrier\n" \
"v_xor_b32_e32 v28, 0x20000, v28\n" \
"v_xor_b32_e32 v29, 0x20000, v29\n" \
M_"v[52:55], a[176:179], a[128:131], v[52:55]\n" \
"s_add_u32 m0, s40, 0x8000\n" \
"s_add_u32 s85, s39, 0xe000\n" \
M_"v[52:55], a[240:243], a[192:195], v[52:55]\n" \
B_"v20, s[80:83], s85 offen sc1 lds\n" \
M_"v[56:59], a[180:183], a[128:131], v[56:59]\n" \
X_"a[160:161], v32 offset:0\n" \
"s_add_i32 s5, s4, 0x10000\n" \
M_"v[56:59], a[244:247], a[192:195], v[56:59]\n" \
X_"a[162:163], v32 offset:512\n" \
"s_and_b32 s5, s5, 0x70000\n" \
M_"v[60:63], a[184:187], a[128:131], v[60:63]\n" \
"s_add_u32 m0, s40, 0x9000\n" \
"s_add_u32 s85, s39, 0xf000\n" \
M_"v[60:63], a[248:251], a[192:195], v[60:63]\n" \
B_"v20, s[80:83], s85 offen sc1 lds\n" \
M_"v[64:67], a[188:191], a[128:131], v[64:67]\n" \
X_"a[224:225], v32 offset:4096\n" \
"s_add_i32 s5, s15, s5\n" \
M_"v[64:67], a[252:255], a[192:195], v[64:67]\n" \
R_"a[128:131], v28 offset:0\n" \
"v_writelane_b32 v11, s5, 49\n" \
M_"v[80:83], a[188:191], a[132:135], v[80:83]\n" \
R_"a[192:195], v29 offset:0\n" \
"s_add_i32 s5, s4, 0x20000\n" \
M_"v[80:83], a[252:255], a[196:199], v[80:83]\n" \
"s_add_u32 m0, s40, 0xb000\n" \
"s_movk_i32 s84, 0x380\n" \
M_"v[76:79], a[184:187], a[132:135], v[76:79]\n" \
B_"v21, s[76:79], s84 offen offset:0 lds\n" \
M_"v[76:79], a[248:251], a[196:199], v[76:79]\n" \
X_"a[226:227], v32 offset:4608\n" \
"s_and_b32 s5, s5, 0x70000\n" \
M_"v[72:75], a[180:183], a[132:135], v[72:75]\n" \
X_"a[164:165], v33 offset:0\n" \
"s_add_i32 s5, s15, s5\n" \
M_"v[72:75], a[244:247], a[196:199], v[72:75]\n" \
"s_bitset1_b32 s5, 11\n" \
"v_writelane_b32 v11, s5, 50\n" \
M_"v[68:71], a[176:179], a[132:135], v[68:71]\n" \
X_"a[166:167], v33 offset:512\n" \
"s_add_i32 s5, s4, 0x30000\n" \
M_"v[68:71], a[240:243], a[196:199], v[68:71]\n" \
R_"a[132:135], v28 offset:2048\n" \
"s_and_b32 s5, s5, 0x70000\n" \
M_"v[84:87], a[176:179], a[136:139], v[84:87]\n" \
R_"a[196:199], v29 offset:2048\n" \
"s_add_i32 s5, s15, s5\n" \
M_"v[84:87], a[240:243], a[200:203], v[84:87]\n" \
"s_add_u32 m0, s31, 0x8000\n" \
"s_mov_b32 s84, 0x380000\n" \
M_"v[88:91], a[180:183], a[136:139], v[88:91]\n" \
B_"v26, s[68:71], s84 offen offset:0 lds\n" \
M_"v[88:91], a[244:247], a[200:203], v[88:91]\n" \
"s_add_u32 s89, s42, 0x0\n" \
"buffer_store_dwordx4 v[184:187], v20, s[80:83], s89 offen offset:1024 sc1\n" \
M_"v[92:95], a[184:187], a[136:139], v[92:95]\n" \
X_"a[228:229], v33 offset:4096\n" \
"s_bitset1_b32 s5, 12\n" \
M_"v[92:95], a[248:251], a[200:203], v[92:95]\n" \
X_"a[230:231], v33 offset:4608\n" \
"v_writelane_b32 v11, s5, 51\n" \
M_"v[96:99], a[188:191], a[136:139], v[96:99]\n" \
"s_and_b32 s5, s4, 0x70000\n" \
"s_xor_b32 s5, s5, 0x40000\n" \
M_"v[96:99], a[252:255], a[200:203], v[96:99]\n" \
R_"a[136:139], v28 offset:4096\n" \
"s_add_i32 s5, s15, s5\n" \
M_"v[112:115], a[188:191], a[140:143], v[112:115]\n" \
R_"a[200:203], v29 offset:4096\n" \
"s_or_b32 s5, s5, 0x1800\n" \
M_"v[112:115], a[252:255], a[204:207], v[112:115]\n" \
"s_addk_i32 s84, 0x7c00\n" \
M_"v[108:111], a[184:187], a[140:143], v[108:111]\n" \
B_"v26, s[68:71], s84 offen offset:1024 lds\n" \
M_"v[108:111], a[248:251], a[204:207], v[108:111]\n" \
X_"a[172:173], v35 offset:0\n" \
"v_writelane_b32 v11, s5, 52\n" \
M_"v[104:107], a[180:183], a[140:143], v[104:107]\n" \
"s_addk_i32 s84, 0x7c00\n" \
M_"v[104:107], a[244:247], a[204:207], v[104:107]\n" \
B_"v27, s[68:71], s84 offen offset:2048 lds\n" \
M_"v[100:103], a[176:179], a[140:143], v[100:103]\n" \
X_"a[174:175], v35 offset:512\n" \
"s_add_i32 s5, s4, 0x50000\n" \
M_"v[100:103], a[240:243], a[204:207], v[100:103]\n" \
R_"a[140:143], v28 offset:6144\n" \
"s_and_b32 s5, s5, 0x70000\n" \
M_"v[116:119], a[176:179], a[144:147], v[116:119]\n" \
R_"a[204:207], v29 offset:6144\n" \
"s_add_i32 s5, s15, s5\n" \
M_"v[116:119], a[240:243], a[208:211], v[116:119]\n" \
"s_addk_i32 s84, 0x7c00\n" \
M_"v[120:123], a[180:183], a[144:147], v[120:123]\n" \
B_"v27, s[68:71], s84 offen offset:3072 lds\n" \
M_"v[120:123], a[244:247], a[208:211], v[120:123]\n" \
X_"a[236:237], v35 offset:4096\n" \
"s_bitset1_b32 s5, 13\n" \
M_"v[124:127], a[184:187], a[144:147], v[124:127]\n" \
X_"a[238:239], v35 offset:4608\n" \
"v_writelane_b32 v11, s5, 53\n" \
M_"v[124:127], a[248:251], a[208:211], v[124:127]\n" \
"s_add_u32 m0, s30, 0x20000\n" \
"s_movk_i32 s84, 0x400\n" \
M_"v[128:131], a[188:191], a[144:147], v[128:131]\n" \
B_"v24, s[76:79], s84 offen offset:0 lds\n" \
M_"v[128:131], a[252:255], a[208:211], v[128:131]\n" \
R_"a[144:147], v28 offset:8192\n" \
"s_add_i32 s5, s4, 0x60000\n" \
M_"v[144:147], a[188:191], a[148:151], v[144:147]\n" \
R_"a[208:211], v29 offset:8192\n" \
"s_and_b32 s5, s5, 0x70000\n" \
M_"v[144:147], a[252:255], a[212:215], v[144:147]\n" \
X_"a[168:169], v34 offset:0\n" \
"s_add_i32 s4, s4, 0x70000\n" \
M_"v[140:143], a[184:187], a[148:151], v[140:143]\n" \
"s_movk_i32 s84, 0x2400\n" \
M_"v[140:143], a[248:251], a[212:215], v[140:143]\n" \
B_"v25, s[76:79], s84 offen offset:1024 lds\n" \
M_"v[136:139], a[180:183], a[148:151], v[136:139]\n" \
X_"a[170:171], v34 offset:512\n" \
"s_add_i32 s5, s15, s5\n" \
M_"v[136:139], a[244:247], a[212:215], v[136:139]\n" \
X_"a[232:233], v34 offset:4096\n" \
"s_and_b32 s4, s4, 0x70000\n" \
M_"v[132:135], a[176:179], a[148:151], v[132:135]\n" \
X_"a[234:235], v34 offset:4608\n" \
"s_or_b32 s5, s5, 0x2800\n" \
M_"v[132:135], a[240:243], a[212:215], v[132:135]\n" \
R_"a[148:151], v28 offset:10240\n" \
"v_writelane_b32 v11, s5, 54\n" \
M_"v[148:151], a[176:179], a[152:155], v[148:151]\n" \
R_"a[212:215], v29 offset:10240\n" \
"s_add_i32 s15, s15, s4\n" \
M_"v[148:151], a[240:243], a[216:219], v[148:151]\n" \
"s_movk_i32 s84, 0x4400\n" \
M_"v[152:155], a[180:183], a[152:155], v[152:155]\n" \
B_"v24, s[76:79], s84 offen offset:2048 lds\n" \
M_"v[152:155], a[244:247], a[216:219], v[152:155]\n" \
"s_movk_i32 s84, 0x6400\n" \
M_"v[156:159], a[184:187], a[152:155], v[156:159]\n" \
B_"v25, s[76:79], s84 offen offset:3072 lds\n" \
M_"v[156:159], a[248:251], a[216:219], v[156:159]\n" \
R_"v[248:251], v22 offset:0\n" \
"s_or_b32 s4, s15, 0x3000\n" \
M_"v[160:163], a[188:191], a[152:155], v[160:163]\n" \
R_"v[48:51], v23 offset:0\n" \
"v_writelane_b32 v11, s4, 55\n" \
M_"v[160:163], a[252:255], a[216:219], v[160:163]\n" \
R_"a[152:155], v28 offset:12288\n" \
"s_movk_i32 s11, 0x4000\n" \
M_"v[176:179], a[188:191], a[156:159], v[176:179]\n" \
R_"a[216:219], v29 offset:12288\n" \
"s_lshl_b32 s4, s2, 2\n" \
M_"v[176:179], a[252:255], a[220:223], v[176:179]\n" \
"s_and_b32 s4, s4, 28\n" \
"s_bfe_u32 s5, s2, 0x20003\n" \
M_"v[172:175], a[184:187], a[156:159], v[172:175]\n" \
"s_or_b32 s4, s4, s5\n" \
"s_lshr_b32 s5, s2, 5\n" \
M_"v[172:175], a[248:251], a[220:223], v[172:175]\n" \
"s_lshr_b32 s19, s3, 1\n" \
"s_lshl_b32 s18, s5, 1\n" \
M_"v[168:171], a[180:183], a[156:159], v[168:171]\n" \
"s_lshl_b32 s6, s4, 20\n" \
"s_lshl_b32 s7, s4, 8\n" \
M_"v[168:171], a[244:247], a[220:223], v[168:171]\n" \
"s_and_b32 s13, s3, 1\n" \
"s_add_i32 s14, s18, s19\n" \
M_"v[164:167], a[176:179], a[156:159], v[164:167]\n" \
"s_add_u32 s6, s26, s6\n" \
"s_addc_u32 s9, s27, 0\n" \
M_"v[164:167], a[240:243], a[220:223], v[164:167]\n" \
R_"a[156:159], v28 offset:14336\n" \
R_"a[220:223], v29 offset:14336\n" \
"s_waitcnt lgkmcnt(15)\n" \
M_"a[0:3], a[160:163], a[128:131], a[0:3]\n" \
"s_add_u32 m0, s30, 0x21000\n" \
"s_addk_i32 s84, 0x3000\n" \
M_"a[0:3], a[224:227], a[192:195], a[0:3]\n" \
B_"v24, s[76:79], s84 offen offset:0 lds\n" \
M_"a[4:7], a[164:167], a[128:131], a[4:7]\n" \
X_"a[176:177], v36 offset:0\n" \
M_"a[4:7], a[228:231], a[192:195], a[4:7]\n" \
X_"a[178:179], v36 offset:1024\n" \
M_"a[12:15], a[172:175], a[128:131], a[12:15]\n" \
X_"a[240:241], v36 offset:8192\n" \
"s_waitcnt lgkmcnt(15)\n" \
M_"a[12:15], a[236:239], a[192:195], a[12:15]\n" \
"s_add_u32 s88, s42, 0x1000\n" \
"buffer_store_dwordx4 v[188:191], v20, s[80:83], s88 offen offset:1024 sc1\n" \
"s_waitcnt lgkmcnt(13)\n" \
M_"a[8:11], a[128:131], a[168:171], a[8:11]\n" \
X_"a[242:243], v36 offset:9216\n" \
"s_waitcnt lgkmcnt(12)\n" \
M_"a[8:11], a[192:195], a[232:235], a[8:11]\n" \
X_"a[180:181], v37 offset:0\n" \
M_"a[24:27], a[132:135], a[168:171], a[24:27]\n" \
X_"a[182:183], v37 offset:1024\n" \
M_"a[24:27], a[196:199], a[232:235], a[24:27]\n" \
X_"a[244:245], v37 offset:8192\n" \
M_"a[28:31], a[172:175], a[132:135], a[28:31]\n" \
"s_add_u32 s8, s6, 0x1000000\n" \
"s_addc_u32 s6, s9, 0\n" \
M_"a[28:31], a[236:239], a[196:199], a[28:31]\n" \
"s_addk_i32 s84, 0x2000\n" \
M_"a[20:23], a[164:167], a[132:135], a[20:23]\n" \
B_"v25, s[76:79], s84 offen offset:1024 lds\n" \
M_"a[20:23], a[228:231], a[196:199], a[20:23]\n" \
X_"a[246:247], v37 offset:9216\n" \
"s_and_b32 s9, s6, 0xffff\n" \
M_"a[16:19], a[160:163], a[132:135], a[16:19]\n" \
X_"a[184:185], v38 offset:0\n" \
"s_lshl_b32 s6, s5, 16\n" \
M_"a[16:19], a[224:227], a[196:199], a[16:19]\n" \
".fill (L_pb + 32480 - .) / 4, 4, 0xBF800000\n" \
"L_pp8:\n" \
"s_cbranch_execz L_pret\n" \
X_"a[186:187], v38 offset:1024\n" \
"s_sub_i32 s10, 0x100000, s6\n" \
M_"a[32:35], a[160:163], a[136:139], a[32:35]\n" \
"s_addk_i32 s84, 0x2000\n" \
M_"a[32:35], a[224:227], a[200:203], a[32:35]\n" \
B_"v24, s[76:79], s84 offen offset:2048 lds\n" \
M_"a[36:39], a[164:167], a[136:139], a[36:39]\n" \
X_"a[248:249], v38 offset:8192\n" \
"s_mov_b32 s6, 0x8000\n" \
M_"a[36:39], a[228:231], a[200:203], a[36:39]\n" \
X_"a[250:251], v38 offset:9216\n" \
M_"a[44:47], a[172:175], a[136:139], a[44:47]\n" \
X_"a[188:189], v39 offset:0\n" \
M_"a[44:47], a[236:239], a[200:203], a[44:47]\n" \
X_"a[190:191], v39 offset:1024\n" \
M_"a[40:43], a[136:139], a[168:171], a[40:43]\n" \
"s_add_u32 s89, s42, 0x2000\n" \
"buffer_store_dwordx4 v[192:195], v20, s[80:83], s89 offen offset:0 sc1\n" \
M_"a[40:43], a[200:203], a[232:235], a[40:43]\n" \
X_"a[252:253], v39 offset:8192\n" \
M_"a[56:59], a[140:143], a[168:171], a[56:59]\n" \
X_"a[254:255], v39 offset:9216\n" \
M_"a[56:59], a[204:207], a[232:235], a[56:59]\n" \
"s_cmp_lg_u32 s5, 0\n" \
"s_cselect_b32 s17, s11, s6\n" \
M_"a[60:63], a[172:175], a[140:143], a[60:63]\n" \
"s_lshl_b32 s6, s3, 1\n" \
"s_lshl_b32 s5, s5, 3\n" \
M_"a[60:63], a[236:239], a[204:207], a[60:63]\n" \
"s_addk_i32 s84, 0x2000\n" \
M_"a[52:55], a[164:167], a[140:143], a[52:55]\n" \
B_"v25, s[76:79], s84 offen offset:3072 lds\n" \
M_"a[52:55], a[228:231], a[204:207], a[52:55]\n" \
"s_and_b32 s6, s6, 4\n" \
"s_or_b32 s12, s5, s6\n" \
M_"a[48:51], a[160:163], a[140:143], a[48:51]\n" \
"s_sub_i32 s5, s13, s5\n" \
"s_or_b32 s5, s5, s6\n" \
M_"a[48:51], a[224:227], a[204:207], a[48:51]\n" \
"s_or_b32 s6, s12, s13\n" \
"s_lshl_b32 s4, s4, 2\n" \
"s_waitcnt lgkmcnt(15)\n" \
M_"a[64:67], a[164:167], v[248:251], a[64:67]\n" \
"s_mulk_i32 s6, 0x92\n" \
"s_add_i32 s4, s4, 18\n" \
M_"a[64:67], a[228:231], v[48:51], a[64:67]\n" \
"s_mulk_i32 s5, 0x92\n" \
"s_add_i32 s6, s4, s6\n" \
M_"a[80:83], a[172:175], v[248:251], a[80:83]\n" \
"s_add_i32 s5, s5, s4\n" \
"s_lshl_b32 s4, s3, 11\n" \
M_"a[80:83], a[236:239], v[48:51], a[80:83]\n" \
"s_lshl_b32 s15, s13, 12\n" \
"s_lshl_b32 s16, s19, 14\n" \
M_"a[68:71], v[248:251], a[168:171], a[68:71]\n" \
"s_mul_i32 s19, s19, 0xe000\n" \
"s_mov_b32 s11, 0x20000\n" \
"s_waitcnt vmcnt(10) lgkmcnt(0)\n" \
M_"a[68:71], v[48:51], a[232:235], a[68:71]\n" \
"s_barrier\n" \
M_"v[52:55], a[176:179], a[128:131], v[52:55]\n" \
"s_add_u32 m0, s40, 0x0\n" \
"s_add_u32 s85, s39, 0x10000\n" \
M_"v[52:55], a[240:243], a[192:195], v[52:55]\n" \
B_"v20, s[80:83], s85 offen sc1 lds\n" \
M_"v[56:59], a[180:183], a[128:131], v[56:59]\n" \
X_"a[160:161], v32 offset:32768\n" \
"s_or_b32 s12, s4, 0xc000\n" \
M_"v[56:59], a[244:247], a[192:195], v[56:59]\n" \
X_"a[162:163], v32 offset:33280\n" \
"s_lshl_b32 s4, s3, 8\n" \
M_"v[60:63], a[184:187], a[128:131], v[60:63]\n" \
"s_or_b32 s22, s15, s16\n" \
"s_or_b32 s23, s19, s15\n" \
M_"v[60:63], a[248:251], a[192:195], v[60:63]\n" \
X_"a[224:225], v32 offset:36864\n" \
"s_lshl_b32 s60, s6, 10\n" \
M_"v[64:67], a[188:191], a[128:131], v[64:67]\n" \
"s_lshl_b32 s61, s5, 10\n" \
"s_lshl_b32 s14, s14, 15\n" \
"s_nop 0\n" \
M_"v[64:67], a[252:255], a[192:195], v[64:67]\n" \
R_"a[128:131], v28 offset:32768\n" \
"s_or_b32 s20, s4, 0x26c00\n" \
M_"v[80:83], a[188:191], a[132:135], v[80:83]\n" \
R_"a[192:195], v29 offset:32768\n" \
"s_or_b32 s13, s22, 0x8000\n" \
M_"v[80:83], a[252:255], a[196:199], v[80:83]\n" \
"s_add_u32 m0, s40, 0x1000\n" \
"s_add_u32 s85, s39, 0x11000\n" \
M_"v[76:79], a[184:187], a[132:135], v[76:79]\n" \
B_"v20, s[80:83], s85 offen sc1 lds\n" \
M_"v[76:79], a[248:251], a[196:199], v[76:79]\n" \
X_"a[226:227], v32 offset:37376\n" \
"s_add_i32 s22, s22, 0x14000\n" \
M_"v[72:75], a[180:183], a[132:135], v[72:75]\n" \
X_"a[164:165], v33 offset:32768\n" \
"s_bitset1_b32 s23, 16\n" \
M_"v[72:75], a[244:247], a[196:199], v[72:75]\n" \
"s_or_b32 s24, s7, 0x5be8020\n" \
"s_and_b32 s57, s27, 0xffff\n" \
M_"v[68:71], a[176:179], a[132:135], v[68:71]\n" \
X_"a[166:167], v33 offset:33280\n" \
"s_mov_b32 s58, 0x6000000\n" \
M_"v[68:71], a[240:243], a[196:199], v[68:71]\n" \
R_"a[132:135], v28 offset:34816\n" \
"s_mov_b32 s56, s26\n" \
M_"v[84:87], a[176:179], a[136:139], v[84:87]\n" \
R_"a[196:199], v29 offset:34816\n" \
"s_mov_b32 s59, s11\n" \
M_"v[84:87], a[240:243], a[200:203], v[84:87]\n" \
"s_add_u32 m0, s40, 0x3000\n" \
"s_movk_i32 s84, 0x400\n" \
M_"v[88:91], a[180:183], a[136:139], v[88:91]\n" \
B_"v21, s[76:79], s84 offen offset:0 lds\n" \
M_"v[88:91], a[244:247], a[200:203], v[88:91]\n" \
X_"a[228:229], v33 offset:36864\n" \
"s_add_i32 s60, s60, 0x3000000\n" \
M_"v[92:95], a[184:187], a[136:139], v[92:95]\n" \
X_"a[230:231], v33 offset:37376\n" \
"s_add_i32 s61, s61, 0x411c000\n" \
M_"v[92:95], a[248:251], a[200:203], v[92:95]\n" \
"s_add_u32 s88, s42, 0x2000\n" \
"buffer_store_dwordx4 v[216:219], v20, s[80:83], s88 offen offset:1024 sc1\n" \
M_"v[96:99], a[188:191], a[136:139], v[96:99]\n" \
"s_sub_i32 s19, 14, s18\n" \
"s_nop 0\n" \
"s_mov_b32 s21, 0x71800000\n" \
M_"v[96:99], a[252:255], a[200:203], v[96:99]\n" \
R_"a[136:139], v28 offset:36864\n" \
"s_mov_b32 s4, L_gsw-L_gtr\n" \
M_"v[112:115], a[188:191], a[140:143], v[112:115]\n" \
R_"a[200:203], v29 offset:36864\n" \
M_"v[112:115], a[252:255], a[204:207], v[112:115]\n" \
X_"a[172:173], v35 offset:32768\n" \
M_"v[108:111], a[184:187], a[140:143], v[108:111]\n" \
X_"a[174:175], v35 offset:33280\n" \
M_"v[108:111], a[248:251], a[204:207], v[108:111]\n" \
"s_cmp_eq_u32 s19, 0\n" \
"s_cselect_b32 s4, L_gs7-L_gtr, s4\n" \
M_"v[104:107], a[180:183], a[140:143], v[104:107]\n" \
"s_add_u32 m0, s31, 0x0\n" \
"s_mov_b32 s84, 0x400000\n" \
M_"v[104:107], a[244:247], a[204:207], v[104:107]\n" \
B_"v26, s[68:71], s84 offen offset:0 lds\n" \
M_"v[100:103], a[176:179], a[140:143], v[100:103]\n" \
X_"a[236:237], v35 offset:36864\n" \
M_"v[100:103], a[240:243], a[204:207], v[100:103]\n" \
R_"a[140:143], v28 offset:38912\n" \
M_"v[116:119], a[176:179], a[144:147], v[116:119]\n" \
R_"a[204:207], v29 offset:38912\n" \
M_"v[116:119], a[240:243], a[208:211], v[116:119]\n" \
X_"a[238:239], v35 offset:37376\n" \
M_"v[120:123], a[180:183], a[144:147], v[120:123]\n" \
X_"a[168:169], v34 offset:32768\n" \
M_"v[120:123], a[244:247], a[208:211], v[120:123]\n" \
"s_addk_i32 s84, 0x7c00\n" \
M_"v[124:127], a[184:187], a[144:147], v[124:127]\n" \
B_"v26, s[68:71], s84 offen offset:1024 lds\n" \
M_"v[124:127], a[248:251], a[208:211], v[124:127]\n" \
X_"a[170:171], v34 offset:33280\n" \
M_"v[128:131], a[188:191], a[144:147], v[128:131]\n" \
"s_cmp_eq_u32 s16, 0\n" \
"s_cselect_b32 s4, 0, s4\n" \
M_"v[128:131], a[252:255], a[208:211], v[128:131]\n" \
R_"a[144:147], v28 offset:40960\n" \
M_"v[144:147], a[188:191], a[148:151], v[144:147]\n" \
R_"a[208:211], v29 offset:40960\n" \
M_"v[144:147], a[252:255], a[212:215], v[144:147]\n" \
X_"a[232:233], v34 offset:36864\n" \
M_"v[140:143], a[184:187], a[148:151], v[140:143]\n" \
"s_addk_i32 s84, 0x7c00\n" \
M_"v[140:143], a[248:251], a[212:215], v[140:143]\n" \
B_"v27, s[68:71], s84 offen offset:2048 lds\n" \
M_"v[136:139], a[180:183], a[148:151], v[136:139]\n" \
X_"a[234:235], v34 offset:37376\n" \
M_"v[136:139], a[244:247], a[212:215], v[136:139]\n" \
"s_getpc_b64 s[28:29]\n" \
"L_gp0_%=:\n" \
"s_add_u32 s28, s28, L_gtr-L_gp0_%=\n" \
"s_addc_u32 s29, s29, 0\n" \
M_"v[132:135], a[176:179], a[148:151], v[132:135]\n" \
R_"v[248:251], v22 offset:32768\n" \
M_"v[132:135], a[240:243], a[212:215], v[132:135]\n" \
R_"a[148:151], v28 offset:43008\n" \
M_"v[148:151], a[176:179], a[152:155], v[148:151]\n" \
R_"a[212:215], v29 offset:43008\n" \
M_"v[148:151], a[240:243], a[216:219], v[148:151]\n" \
"s_add_u32 m0, s31, 0x0\n" \
"s_mov_b32 s84, 0x417400\n" \
M_"v[152:155], a[180:183], a[152:155], v[152:155]\n" \
B_"v27, s[68:71], s84 offen offset:3072 lds\n" \
M_"v[152:155], a[244:247], a[216:219], v[152:155]\n" \
R_"v[48:51], v23 offset:32768\n" \
M_"v[156:159], a[184:187], a[152:155], v[156:159]\n" \
"s_add_u32 s28, s28, s4\n" \
"s_addc_u32 s29, s29, 0\n" \
"s_nop 0\n" \
M_"v[156:159], a[248:251], a[216:219], v[156:159]\n" \
"v_accvgpr_write_b32 a96, 0\n" \
"v_accvgpr_write_b32 a97, 0\n" \
M_"v[160:163], a[188:191], a[152:155], v[160:163]\n" \
"v_accvgpr_write_b32 a98, 0\n" \
"v_accvgpr_write_b32 a99, 0\n" \
M_"v[160:163], a[252:255], a[216:219], v[160:163]\n" \
R_"a[152:155], v28 offset:45056\n" \
"v_accvgpr_write_b32 a100, 0\n" \
M_"v[176:179], a[188:191], a[156:159], v[176:179]\n" \
R_"a[216:219], v29 offset:45056\n" \
"v_accvgpr_write_b32 a101, 0\n" \
M_"v[176:179], a[252:255], a[220:223], v[176:179]\n" \
"v_accvgpr_write_b32 a102, 0\n" \
"v_accvgpr_write_b32 a103, 0\n" \
M_"v[172:175], a[184:187], a[156:159], v[172:175]\n" \
"v_accvgpr_write_b32 a112, 0\n" \
"v_accvgpr_write_b32 a113, 0\n" \
M_"v[172:175], a[248:251], a[220:223], v[172:175]\n" \
"v_accvgpr_write_b32 a114, 0\n" \
"v_accvgpr_write_b32 a115, 0\n" \
M_"v[168:171], a[180:183], a[156:159], v[168:171]\n" \
"v_accvgpr_write_b32 a120, 0\n" \
"v_accvgpr_write_b32 a121, 0\n" \
M_"v[168:171], a[244:247], a[220:223], v[168:171]\n" \
"v_accvgpr_write_b32 a122, 0\n" \
"v_accvgpr_write_b32 a123, 0\n" \
M_"v[164:167], a[176:179], a[156:159], v[164:167]\n" \
"v_mov_b32_e32 v180, 0x3f803f80\n" \
"s_mov_b32 s6, 0x33333333\n" \
M_"v[164:167], a[240:243], a[220:223], v[164:167]\n" \
R_"a[156:159], v28 offset:47104\n" \
R_"a[220:223], v29 offset:47104\n" \
"s_waitcnt lgkmcnt(15)\n" \
"s_nop 0\n" \
M_"a[0:3], a[160:163], a[128:131], a[0:3]\n" \
"s_add_u32 s89, s42, 0x3000\n" \
"buffer_store_dwordx4 v[220:223], v20, s[80:83], s89 offen offset:0 sc1\n" \
M_"a[0:3], a[224:227], a[192:195], a[0:3]\n" \
X_"a[176:177], v36 offset:32768\n" \
"s_mov_b32 s7, 0xcccccccc\n" \
M_"a[4:7], a[164:167], a[128:131], a[4:7]\n" \
X_"a[178:179], v36 offset:33792\n" \
"v_cndmask_b32_e64 v181, 0, v180, s[6:7]\n" \
M_"a[4:7], a[228:231], a[192:195], a[4:7]\n" \
X_"a[240:241], v36 offset:40960\n" \
"v_accvgpr_write_b32 a104, v181\n" \
M_"a[12:15], a[172:175], a[128:131], a[12:15]\n" \
X_"a[242:243], v36 offset:41984\n" \
"v_accvgpr_write_b32 a105, v181\n" \
"s_waitcnt lgkmcnt(15)\n" \
M_"a[12:15], a[236:239], a[192:195], a[12:15]\n" \
"v_accvgpr_write_b32 a106, v181\n" \
"v_accvgpr_write_b32 a107, v181\n" \
M_"a[8:11], a[128:131], a[168:171], a[8:11]\n" \
X_"a[180:181], v37 offset:32768\n" \
"s_mov_b32 s6, 0x11111111\n" \
"s_waitcnt lgkmcnt(13)\n" \
M_"a[8:11], a[192:195], a[232:235], a[8:11]\n" \
"s_mov_b32 s7, 0x11111111\n" \
"v_cndmask_b32_e64 v181, 0, v180, s[6:7]\n" \
M_"a[24:27], a[132:135], a[168:171], a[24:27]\n" \
X_"a[182:183], v37 offset:33792\n" \
"v_accvgpr_write_b32 a108, v181\n" \
M_"a[24:27], a[196:199], a[232:235], a[24:27]\n" \
X_"a[244:245], v37 offset:40960\n" \
"v_accvgpr_write_b32 a109, v181\n" \
M_"a[28:31], a[172:175], a[132:135], a[28:31]\n" \
"v_accvgpr_write_b32 a110, v181\n" \
"v_accvgpr_write_b32 a111, v181\n" \
M_"a[28:31], a[236:239], a[196:199], a[28:31]\n" \
X_"a[246:247], v37 offset:41984\n" \
"s_mov_b32 s6, 0x44444444\n" \
M_"a[20:23], a[164:167], a[132:135], a[20:23]\n" \
"s_mov_b32 s7, 0x44444444\n" \
"v_cndmask_b32_e64 v181, 0, v180, s[6:7]\n" \
M_"a[20:23], a[228:231], a[196:199], a[20:23]\n" \
X_"a[184:185], v38 offset:32768\n" \
"v_accvgpr_write_b32 a116, v181\n" \
M_"a[16:19], a[160:163], a[132:135], a[16:19]\n" \
X_"a[186:187], v38 offset:33792\n" \
"v_accvgpr_write_b32 a117, v181\n" \
M_"a[16:19], a[224:227], a[196:199], a[16:19]\n" \
"v_accvgpr_write_b32 a118, v181\n" \
"v_accvgpr_write_b32 a119, v181\n" \
M_"a[32:35], a[160:163], a[136:139], a[32:35]\n" \
"s_add_u32 s88, s42, 0x3000\n" \
"buffer_store_dwordx4 v[224:227], v20, s[80:83], s88 offen offset:1024 sc1\n" \
M_"a[32:35], a[224:227], a[200:203], a[32:35]\n" \
X_"a[248:249], v38 offset:40960\n" \
"v_bfe_i32 v181, v20, 4, 1\n" \
M_"a[36:39], a[164:167], a[136:139], a[36:39]\n" \
"v_lshlrev_b32_e32 v45, 3, v20\n" \
"v_add_u32_e32 v45, s30, v45\n" \
M_"a[36:39], a[228:231], a[200:203], a[36:39]\n" \
"s_getpc_b64 s[86:87]\n" \
"L_ct0_%=:\n" \
"s_add_u32 s86, s86, L_ap0-L_ct0_%=\n" \
"s_addc_u32 s87, s87, 0\n" \
"s_add_u32 m0, s40, 0x2000\n" \
M_"a[44:47], a[172:175], a[136:139], a[44:47]\n" \
"global_load_lds_dword v45, s[86:87]\n" \
M_"a[44:47], a[236:239], a[200:203], a[44:47]\n" \
X_"a[250:251], v38 offset:41984\n" \
"v_and_b32_e32 v181, 0xaaaaaaaa, v181\n" \
M_"a[40:43], a[136:139], a[168:171], a[40:43]\n" \
X_"a[188:189], v39 offset:32768\n" \
"v_or_b32_e32 v181, 0x44444444, v181\n" \
M_"a[40:43], a[200:203], a[232:235], a[40:43]\n" \
X_"a[190:191], v39 offset:33792\n" \
"v_accvgpr_write_b32 a126, v181\n" \
M_"a[56:59], a[140:143], a[168:171], a[56:59]\n" \
X_"a[252:253], v39 offset:40960\n" \
"v_bfe_u32 v180, v20, 5, 3\n" \
M_"a[56:59], a[204:207], a[232:235], a[56:59]\n" \
X_"a[254:255], v39 offset:41984\n" \
"v_bfe_u32 v181, v20, 8, 2\n" \
M_"a[60:63], a[172:175], a[140:143], a[60:63]\n" \
"v_lshlrev_b32_e32 v181, 1, v181\n" \
"v_sub_u32_e32 v180, v180, v181\n" \
M_"a[60:63], a[236:239], a[204:207], a[60:63]\n" \
"v_and_b32_e64 v181, 16, v20\n" \
"v_mov_b32_e32 v182, 0x3f80\n" \
M_"a[52:55], a[164:167], a[140:143], a[52:55]\n" \
"v_lshlrev_b32_e32 v181, v181, v182\n" \
"v_cmp_eq_u32_e32 vcc, 0, v180\n" \
M_"a[52:55], a[228:231], a[204:207], a[52:55]\n" \
"v_cmp_eq_u32_e64 s[6:7], 1, v180\n" \
"v_cndmask_b32_e64 v182, 0, v181, vcc\n" \
M_"a[48:51], a[160:163], a[140:143], a[48:51]\n" \
"v_cndmask_b32_e64 v183, 0, v181, s[6:7]\n" \
"v_accvgpr_write_b32 a124, v182\n" \
M_"a[48:51], a[224:227], a[204:207], a[48:51]\n" \
"v_accvgpr_write_b32 a125, v183\n" \
"s_waitcnt lgkmcnt(15)\n" \
M_"a[64:67], a[164:167], v[248:251], a[64:67]\n" \
M_"a[64:67], a[228:231], v[48:51], a[64:67]\n" \
M_"a[80:83], a[172:175], v[248:251], a[80:83]\n" \
M_"a[80:83], a[236:239], v[48:51], a[80:83]\n" \
M_"a[68:71], v[248:251], a[168:171], a[68:71]\n" \
"s_waitcnt vmcnt(3) lgkmcnt(0)\n" \
M_"a[68:71], v[48:51], a[232:235], a[68:71]\n" \
"s_barrier\n" \
"v_xor_b32_e32 v28, 0x20000, v28\n" \
"v_xor_b32_e32 v29, 0x20000, v29\n" \
M_"v[52:55], a[176:179], a[128:131], v[52:55]\n" \
X_"a[160:161], v32 offset:0\n" \
"v_accvgpr_read_b32 v184, a0\n" \
M_"v[52:55], a[240:243], a[192:195], v[52:55]\n" \
"v_accvgpr_read_b32 v185, a1\n" \
"v_accvgpr_read_b32 v186, a2\n" \
M_"v[56:59], a[180:183], a[128:131], v[56:59]\n" \
X_"a[162:163], v32 offset:512\n" \
"v_accvgpr_read_b32 v187, a3\n" \
M_"v[56:59], a[244:247], a[192:195], v[56:59]\n" \
"v_accvgpr_read_b32 v188, a4\n" \
"v_accvgpr_read_b32 v189, a5\n" \
M_"v[60:63], a[184:187], a[128:131], v[60:63]\n" \
"v_accvgpr_read_b32 v190, a6\n" \
"v_accvgpr_read_b32 v191, a7\n" \
M_"v[60:63], a[248:251], a[192:195], v[60:63]\n" \
X_"a[224:225], v32 offset:4096\n" \
"v_accvgpr_read_b32 v196, a12\n" \
M_"v[64:67], a[188:191], a[128:131], v[64:67]\n" \
"v_accvgpr_read_b32 v197, a13\n" \
"v_accvgpr_read_b32 v198, a14\n" \
M_"v[64:67], a[252:255], a[192:195], v[64:67]\n" \
R_"a[128:131], v28 offset:0\n" \
"v_accvgpr_read_b32 v199, a15\n" \
M_"v[80:83], a[188:191], a[132:135], v[80:83]\n" \
R_"a[192:195], v29 offset:0\n" \
"v_accvgpr_read_b32 v192, a8\n" \
M_"v[80:83], a[252:255], a[196:199], v[80:83]\n" \
X_"a[226:227], v32 offset:4608\n" \
"v_accvgpr_read_b32 v193, a9\n" \
M_"v[76:79], a[184:187], a[132:135], v[76:79]\n" \
"v_accvgpr_read_b32 v194, a10\n" \
"v_accvgpr_read_b32 v195, a11\n" \
M_"v[76:79], a[248:251], a[196:199], v[76:79]\n" \
X_"a[164:165], v33 offset:0\n" \
"v_accvgpr_read_b32 v208, a24\n" \
M_"v[72:75], a[180:183], a[132:135], v[72:75]\n" \
"v_accvgpr_read_b32 v209, a25\n" \
"v_accvgpr_read_b32 v210, a26\n" \
M_"v[72:75], a[244:247], a[196:199], v[72:75]\n" \
"v_accvgpr_read_b32 v211, a27\n" \
"v_accvgpr_read_b32 v212, a28\n" \
M_"v[68:71], a[176:179], a[132:135], v[68:71]\n" \
X_"a[166:167], v33 offset:512\n" \
"v_accvgpr_read_b32 v213, a29\n" \
M_"v[68:71], a[240:243], a[196:199], v[68:71]\n" \
R_"a[132:135], v28 offset:2048\n" \
"v_accvgpr_read_b32 v214, a30\n" \
M_"v[84:87], a[176:179], a[136:139], v[84:87]\n" \
R_"a[196:199], v29 offset:2048\n" \
"v_accvgpr_read_b32 v215, a31\n" \
M_"v[84:87], a[240:243], a[200:203], v[84:87]\n" \
X_"a[228:229], v33 offset:4096\n" \
"v_accvgpr_read_b32 v204, a20\n" \
M_"v[88:91], a[180:183], a[136:139], v[88:91]\n" \
"v_accvgpr_read_b32 v205, a21\n" \
"v_accvgpr_read_b32 v206, a22\n" \
M_"v[88:91], a[244:247], a[200:203], v[88:91]\n" \
"v_accvgpr_read_b32 v207, a23\n" \
"v_accvgpr_read_b32 v200, a16\n" \
M_"v[92:95], a[184:187], a[136:139], v[92:95]\n" \
X_"a[230:231], v33 offset:4608\n" \
"v_accvgpr_read_b32 v201, a17\n" \
M_"v[92:95], a[248:251], a[200:203], v[92:95]\n" \
"v_accvgpr_read_b32 v202, a18\n" \
"v_accvgpr_read_b32 v203, a19\n" \
M_"v[96:99], a[188:191], a[136:139], v[96:99]\n" \
"v_accvgpr_read_b32 v216, a32\n" \
"v_accvgpr_read_b32 v217, a33\n" \
M_"v[96:99], a[252:255], a[200:203], v[96:99]\n" \
R_"a[136:139], v28 offset:4096\n" \
"v_accvgpr_read_b32 v218, a34\n" \
M_"v[112:115], a[188:191], a[140:143], v[112:115]\n" \
R_"a[200:203], v29 offset:4096\n" \
"v_accvgpr_read_b32 v219, a35\n" \
M_"v[112:115], a[252:255], a[204:207], v[112:115]\n" \
X_"a[172:173], v35 offset:0\n" \
"v_accvgpr_read_b32 v220, a36\n" \
M_"v[108:111], a[184:187], a[140:143], v[108:111]\n" \
X_"a[174:175], v35 offset:512\n" \
"v_accvgpr_read_b32 v221, a37\n" \
M_"v[108:111], a[248:251], a[204:207], v[108:111]\n" \
"v_accvgpr_read_b32 v222, a38\n" \
"v_accvgpr_read_b32 v223, a39\n" \
M_"v[104:107], a[180:183], a[140:143], v[104:107]\n" \
X_"a[236:237], v35 offset:4096\n" \
"v_accvgpr_read_b32 v228, a44\n" \
M_"v[104:107], a[244:247], a[204:207], v[104:107]\n" \
"v_accvgpr_read_b32 v229, a45\n" \
"v_accvgpr_read_b32 v230, a46\n" \
M_"v[100:103], a[176:179], a[140:143], v[100:103]\n" \
"v_accvgpr_read_b32 v231, a47\n" \
"v_accvgpr_read_b32 v224, a40\n" \
M_"v[100:103], a[240:243], a[204:207], v[100:103]\n" \
R_"a[140:143], v28 offset:6144\n" \
"v_accvgpr_read_b32 v225, a41\n" \
M_"v[116:119], a[176:179], a[144:147], v[116:119]\n" \
R_"a[204:207], v29 offset:6144\n" \
"v_accvgpr_read_b32 v226, a42\n" \
M_"v[116:119], a[240:243], a[208:211], v[116:119]\n" \
X_"a[238:239], v35 offset:4608\n" \
"v_accvgpr_read_b32 v227, a43\n" \
M_"v[120:123], a[180:183], a[144:147], v[120:123]\n" \
X_"a[168:169], v34 offset:0\n" \
"v_accvgpr_read_b32 v240, a56\n" \
M_"v[120:123], a[244:247], a[208:211], v[120:123]\n" \
"v_accvgpr_read_b32 v241, a57\n" \
"v_accvgpr_read_b32 v242, a58\n" \
M_"v[124:127], a[184:187], a[144:147], v[124:127]\n" \
"v_accvgpr_read_b32 v243, a59\n" \
"v_accvgpr_read_b32 v244, a60\n" \
M_"v[124:127], a[248:251], a[208:211], v[124:127]\n" \
X_"a[170:171], v34 offset:512\n" \
"v_accvgpr_read_b32 v245, a61\n" \
M_"v[128:131], a[188:191], a[144:147], v[128:131]\n" \
"v_accvgpr_read_b32 v246, a62\n" \
"v_accvgpr_read_b32 v247, a63\n" \
M_"v[128:131], a[252:255], a[208:211], v[128:131]\n" \
R_"a[144:147], v28 offset:8192\n" \
"v_accvgpr_read_b32 v236, a52\n" \
M_"v[144:147], a[188:191], a[148:151], v[144:147]\n" \
R_"a[208:211], v29 offset:8192\n" \
"v_accvgpr_read_b32 v237, a53\n" \
M_"v[144:147], a[252:255], a[212:215], v[144:147]\n" \
X_"a[232:233], v34 offset:4096\n" \
"v_accvgpr_read_b32 v238, a54\n" \
M_"v[140:143], a[184:187], a[148:151], v[140:143]\n" \
"v_accvgpr_read_b32 v239, a55\n" \
"v_accvgpr_read_b32 v232, a48\n" \
M_"v[140:143], a[248:251], a[212:215], v[140:143]\n" \
X_"a[234:235], v34 offset:4608\n" \
"v_accvgpr_read_b32 v233, a49\n" \
M_"v[136:139], a[180:183], a[148:151], v[136:139]\n" \
"v_accvgpr_read_b32 v234, a50\n" \
"v_accvgpr_read_b32 v235, a51\n" \
M_"v[136:139], a[244:247], a[212:215], v[136:139]\n" \
"v_accvgpr_read_b32 v180, a64\n" \
"v_accvgpr_read_b32 v181, a65\n" \
M_"v[132:135], a[176:179], a[148:151], v[132:135]\n" \
R_"v[248:251], v22 offset:0\n" \
"v_accvgpr_read_b32 v182, a66\n" \
M_"v[132:135], a[240:243], a[212:215], v[132:135]\n" \
R_"a[148:151], v28 offset:10240\n" \
"v_accvgpr_read_b32 v183, a67\n" \
M_"v[148:151], a[176:179], a[152:155], v[148:151]\n" \
R_"a[212:215], v29 offset:10240\n" \
"v_accvgpr_read_b32 v252, a68\n" \
M_"v[148:151], a[240:243], a[216:219], v[148:151]\n" \
R_"v[48:51], v23 offset:0\n" \
"v_accvgpr_read_b32 v253, a69\n" \
M_"v[152:155], a[180:183], a[152:155], v[152:155]\n" \
"v_accvgpr_read_b32 v254, a70\n" \
"v_accvgpr_read_b32 v255, a71\n" \
M_"v[152:155], a[244:247], a[216:219], v[152:155]\n" \
"v_lshrrev_b32_e32 v24, 9, v20\n" \
"v_and_b32_e32 v26, 0x1ff, v20\n" \
M_"v[156:159], a[184:187], a[152:155], v[156:159]\n" \
"v_lshl_add_u32 v24, v24, 3, v26\n" \
"v_lshrrev_b32_e64 v25, 8, v20\n" \
M_"v[156:159], a[248:251], a[216:219], v[156:159]\n" \
"v_mul_u32_u24_e32 v25, 0x24700, v25\n" \
"v_add_u32_e64 v25, v25, v20\n" \
M_"v[160:163], a[188:191], a[152:155], v[160:163]\n" \
"v_lshrrev_b32_e32 v26, 2, v20\n" \
"v_add_u32_e32 v26, s45, v26\n" \
M_"v[160:163], a[252:255], a[216:219], v[160:163]\n" \
R_"a[152:155], v28 offset:12288\n" \
"v_add_u32_e64 v27, s46, v20\n" \
M_"v[176:179], a[188:191], a[156:159], v[176:179]\n" \
R_"a[216:219], v29 offset:12288\n" \
"v_add_u32_e64 v21, s43, v27\n" \
M_"v[176:179], a[252:255], a[220:223], v[176:179]\n" \
M_"v[172:175], a[184:187], a[156:159], v[172:175]\n" \
M_"v[172:175], a[248:251], a[220:223], v[172:175]\n" \
M_"v[168:171], a[180:183], a[156:159], v[168:171]\n" \
M_"v[168:171], a[244:247], a[220:223], v[168:171]\n" \
M_"v[164:167], a[176:179], a[156:159], v[164:167]\n" \
M_"v[164:167], a[240:243], a[220:223], v[164:167]\n" \
R_"a[156:159], v28 offset:14336\n" \
R_"a[220:223], v29 offset:14336\n" \
"s_waitcnt lgkmcnt(15)\n" \
"s_nop 0\n" \
M_"v[184:187], a[160:163], a[128:131], v[184:187]\n" \
"v_bfe_u32 v44, v20, 2, 5\n" \
X_"a[176:177], v36 offset:0\n" \
M_"v[184:187], a[224:227], a[192:195], v[184:187]\n" \
"buffer_load_dword v44, v44, s[80:83], s38 offen offset:0 sc1\n" \
M_"v[188:191], a[164:167], a[128:131], v[188:191]\n" \
X_"a[178:179], v36 offset:1024\n" \
M_"v[188:191], a[228:231], a[192:195], v[188:191]\n" \
X_"a[240:241], v36 offset:8192\n" \
M_"v[196:199], a[172:175], a[128:131], v[196:199]\n" \
X_"a[242:243], v36 offset:9216\n" \
"s_waitcnt lgkmcnt(15)\n" \
M_"v[196:199], a[236:239], a[192:195], v[196:199]\n" \
M_"v[192:195], a[128:131], a[168:171], v[192:195]\n" \
X_"a[180:181], v37 offset:0\n" \
"s_waitcnt lgkmcnt(13)\n" \
M_"v[192:195], a[192:195], a[232:235], v[192:195]\n" \
M_"v[208:211], a[132:135], a[168:171], v[208:211]\n" \
X_"a[182:183], v37 offset:1024\n" \
M_"v[208:211], a[196:199], a[232:235], v[208:211]\n" \
X_"a[244:245], v37 offset:8192\n" \
M_"v[212:215], a[172:175], a[132:135], v[212:215]\n" \
M_"v[212:215], a[236:239], a[196:199], v[212:215]\n" \
X_"a[246:247], v37 offset:9216\n" \
".fill (L_pb + 36576 - .) / 4, 4, 0xBF800000\n" \
"L_pp9:\n" \
"s_cbranch_execz L_pret\n" \
"s_nop 0\n" \
C_"v188, v188, v189\n" \
M_"v[204:207], a[164:167], a[132:135], v[204:207]\n" \
C_"v189, v190, v191\n" \
C_"v190, v198, v199\n" \
M_"v[204:207], a[228:231], a[196:199], v[204:207]\n" \
X_"a[184:185], v38 offset:0\n" \
"v_mov_b32_e32 v191, 0x3f80\n" \
M_"v[200:203], a[160:163], a[132:135], v[200:203]\n" \
X_"a[186:187], v38 offset:1024\n" \
C_"v184, v184, v185\n" \
M_"v[200:203], a[224:227], a[196:199], v[200:203]\n" \
C_"v185, v186, v187\n" \
C_"v186, v196, v197\n" \
M_"v[216:219], a[160:163], a[136:139], v[216:219]\n" \
X_"a[248:249], v38 offset:8192\n" \
"v_mov_b32_e64 v187, 0\n" \
M_"v[216:219], a[224:227], a[200:203], v[216:219]\n" \
X_"a[250:251], v38 offset:9216\n" \
M_"v[220:223], a[164:167], a[136:139], v[220:223]\n" \
M_"v[220:223], a[228:231], a[200:203], v[220:223]\n" \
X_"a[188:189], v39 offset:0\n" \
C_"v204, v204, v205\n" \
M_"v[228:231], a[172:175], a[136:139], v[228:231]\n" \
C_"v205, v206, v207\n" \
C_"v206, v214, v215\n" \
M_"v[228:231], a[236:239], a[200:203], v[228:231]\n" \
X_"a[190:191], v39 offset:1024\n" \
"v_mov_b32_e32 v207, 0x3f80\n" \
M_"v[224:227], a[136:139], a[168:171], v[224:227]\n" \
X_"a[252:253], v39 offset:8192\n" \
C_"v200, v200, v201\n" \
M_"v[224:227], a[200:203], a[232:235], v[224:227]\n" \
C_"v201, v202, v203\n" \
C_"v202, v212, v213\n" \
M_"v[240:243], a[140:143], a[168:171], v[240:243]\n" \
X_"a[254:255], v39 offset:9216\n" \
"v_mov_b32_e64 v203, 0\n" \
M_"v[240:243], a[204:207], a[232:235], v[240:243]\n" \
M_"v[244:247], a[172:175], a[140:143], v[244:247]\n" \
M_"v[244:247], a[236:239], a[204:207], v[244:247]\n" \
C_"v220, v220, v221\n" \
C_"v221, v222, v223\n" \
M_"v[236:239], a[164:167], a[140:143], v[236:239]\n" \
C_"v222, v230, v231\n" \
"v_mov_b32_e32 v223, 0x3f80\n" \
M_"v[236:239], a[228:231], a[204:207], v[236:239]\n" \
C_"v216, v216, v217\n" \
C_"v217, v218, v219\n" \
M_"v[232:235], a[160:163], a[140:143], v[232:235]\n" \
C_"v218, v228, v229\n" \
"v_mov_b32_e64 v219, 0\n" \
M_"v[232:235], a[224:227], a[204:207], v[232:235]\n" \
"s_waitcnt lgkmcnt(15)\n" \
"s_nop 0\n" \
M_"v[180:183], a[164:167], v[248:251], v[180:183]\n" \
M_"v[180:183], a[228:231], v[48:51], v[180:183]\n" \
M_"a[80:83], a[172:175], v[248:251], a[80:83]\n" \
M_"a[80:83], a[236:239], v[48:51], a[80:83]\n" \
C_"v236, v236, v237\n" \
C_"v237, v238, v239\n" \
M_"v[252:255], v[248:251], a[168:171], v[252:255]\n" \
C_"v238, v246, v247\n" \
"v_mov_b32_e32 v239, 0x3f80\n" \
M_"v[252:255], v[48:51], a[232:235], v[252:255]\n" \
C_"v232, v232, v233\n" \
C_"v233, v234, v235\n" \
"s_waitcnt vmcnt(0) lgkmcnt(0)\n" \
"s_barrier\n" \
"v_mov_b32_e32 v45, s62\n" \
"s_mov_b64 exec, 1\n" \
"buffer_store_dword v45, off, s[80:83], s37 offset:32 sc1\n" \
"s_mov_b64 exec, -1\n" \
"v_cmp_ne_u32_e32 vcc, s62, v44\n" \
"s_nop 4\n" \
"s_cbranch_vccnz L_poll160_%=\n" \
"L_pollr160_%=:\n" \
M_"v[52:55], a[176:179], a[128:131], v[52:55]\n" \
"s_mov_b32 s73, s50\n" \
"s_sub_u32 m0, s31, 0x10000\n" \
M_"v[52:55], a[240:243], a[192:195], v[52:55]\n" \
B_"v20, s[80:83], s73 offen offset:0 sc1 lds\n" \
M_"v[56:59], a[180:183], a[128:131], v[56:59]\n" \
"s_add_u32 s74, s50, 0x4000\n" \
"s_add_u32 s75, s50, 0x8000\n" \
M_"v[56:59], a[244:247], a[192:195], v[56:59]\n" \
"s_mov_b32 s70, 0x3e0293ee\n" \
"s_mov_b32 s76, s52\n" \
M_"v[60:63], a[184:187], a[128:131], v[60:63]\n" \
"v_mul_f32_e32 v52, s70, v52\n" \
"v_mul_f32_e32 v53, s70, v53\n" \
M_"v[60:63], a[248:251], a[192:195], v[60:63]\n" \
B_"v20, s[80:83], s73 offen offset:1024 sc1 lds\n" \
M_"v[64:67], a[188:191], a[128:131], v[64:67]\n" \
"v_mul_f32_e32 v54, s70, v54\n" \
"v_mul_f32_e32 v55, s70, v55\n" \
M_"v[64:67], a[252:255], a[192:195], v[64:67]\n" \
C_"v52, v52, v53\n" \
"v_mul_f32_e64 v56, s70, v56\n" \
M_"v[80:83], a[188:191], a[132:135], v[80:83]\n" \
"v_mul_f32_e32 v60, s70, v60\n" \
"v_mul_f32_e32 v61, s70, v61\n" \
M_"v[80:83], a[252:255], a[196:199], v[80:83]\n" \
B_"v20, s[80:83], s73 offen offset:2048 sc1 lds\n" \
M_"v[76:79], a[184:187], a[132:135], v[76:79]\n" \
"v_mul_f32_e32 v62, s70, v62\n" \
"v_mul_f32_e32 v63, s70, v63\n" \
M_"v[76:79], a[248:251], a[196:199], v[76:79]\n" \
C_"v60, v60, v61\n" \
"v_mul_f32_e64 v57, s70, v57\n" \
M_"v[72:75], a[180:183], a[132:135], v[72:75]\n" \
"v_mul_f32_e32 v58, s70, v58\n" \
"v_mul_f32_e32 v59, s70, v59\n" \
M_"v[72:75], a[244:247], a[196:199], v[72:75]\n" \
B_"v20, s[80:83], s73 offen offset:3072 sc1 lds\n" \
M_"v[68:71], a[176:179], a[132:135], v[68:71]\n" \
"s_sub_u32 m0, s31, s51\n" \
C_"v53, v54, v55\n" \
M_"v[68:71], a[240:243], a[196:199], v[68:71]\n" \
"v_mul_f32_e32 v64, s70, v64\n" \
"v_mul_f32_e64 v65, s70, v65\n" \
M_"v[84:87], a[176:179], a[136:139], v[84:87]\n" \
"v_mul_f32_e32 v66, s70, v66\n" \
"v_mul_f32_e32 v67, s70, v67\n" \
M_"v[84:87], a[240:243], a[200:203], v[84:87]\n" \
B_"v20, s[80:83], s74 offen offset:0 sc1 lds\n" \
M_"v[88:91], a[180:183], a[136:139], v[88:91]\n" \
C_"v61, v62, v63\n" \
C_"v54, v56, v57\n" \
M_"v[88:91], a[244:247], a[200:203], v[88:91]\n" \
C_"v55, v58, v59\n" \
C_"v62, v64, v65\n" \
M_"v[92:95], a[184:187], a[136:139], v[92:95]\n" \
C_"v63, v66, v67\n" \
"v_mul_f32_e64 v68, s70, v68\n" \
M_"v[92:95], a[248:251], a[200:203], v[92:95]\n" \
B_"v20, s[80:83], s74 offen offset:1024 sc1 lds\n" \
M_"v[96:99], a[188:191], a[136:139], v[96:99]\n" \
"ds_write_b128 v27, v[52:55] offset:0\n" \
M_"v[96:99], a[252:255], a[200:203], v[96:99]\n" \
"v_mul_f32_e32 v69, s70, v69\n" \
"v_mul_f32_e32 v76, s70, v76\n" \
M_"v[112:115], a[188:191], a[140:143], v[112:115]\n" \
"v_mul_f32_e32 v77, s70, v77\n" \
"v_mul_f32_e32 v70, s70, v70\n" \
M_"v[112:115], a[252:255], a[204:207], v[112:115]\n" \
B_"v20, s[80:83], s74 offen offset:2048 sc1 lds\n" \
M_"v[108:111], a[184:187], a[140:143], v[108:111]\n" \
"ds_write_b128 v27, v[60:63] offset:1024\n" \
M_"v[108:111], a[248:251], a[204:207], v[108:111]\n" \
"v_mul_f32_e64 v71, s70, v71\n" \
C_"v68, v68, v69\n" \
M_"v[104:107], a[180:183], a[140:143], v[104:107]\n" \
"v_mul_f32_e32 v78, s70, v78\n" \
"v_mul_f32_e32 v79, s70, v79\n" \
M_"v[104:107], a[244:247], a[204:207], v[104:107]\n" \
B_"v20, s[80:83], s74 offen offset:3072 sc1 lds\n" \
M_"v[100:103], a[176:179], a[140:143], v[100:103]\n" \
"s_sub_u32 m0, s31, 0x4000\n" \
C_"v76, v76, v77\n" \
M_"v[100:103], a[240:243], a[204:207], v[100:103]\n" \
"v_mul_f32_e32 v72, s70, v72\n" \
"v_mul_f32_e32 v73, s70, v73\n" \
M_"v[116:119], a[176:179], a[144:147], v[116:119]\n" \
"v_mul_f32_e32 v74, s70, v74\n" \
"v_mul_f32_e32 v75, s70, v75\n" \
M_"v[116:119], a[240:243], a[208:211], v[116:119]\n" \
B_"v20, s[80:83], s75 offen offset:0 sc1 lds\n" \
M_"v[120:123], a[180:183], a[144:147], v[120:123]\n" \
C_"v69, v70, v71\n" \
"v_mul_f32_e64 v80, s70, v80\n" \
M_"v[120:123], a[244:247], a[208:211], v[120:123]\n" \
"v_mul_f32_e32 v81, s70, v81\n" \
"v_mul_f32_e32 v82, s70, v82\n" \
M_"v[124:127], a[184:187], a[144:147], v[124:127]\n" \
"v_mul_f32_e64 v83, s70, v83\n" \
C_"v77, v78, v79\n" \
M_"v[124:127], a[248:251], a[208:211], v[124:127]\n" \
B_"v20, s[80:83], s75 offen offset:1024 sc1 lds\n" \
M_"v[128:131], a[188:191], a[144:147], v[128:131]\n" \
C_"v70, v72, v73\n" \
C_"v71, v74, v75\n" \
M_"v[128:131], a[252:255], a[208:211], v[128:131]\n" \
C_"v78, v80, v81\n" \
C_"v79, v82, v83\n" \
M_"v[144:147], a[188:191], a[148:151], v[144:147]\n" \
"ds_write_b128 v27, v[68:71] offset:4096\n" \
M_"v[144:147], a[252:255], a[212:215], v[144:147]\n" \
B_"v20, s[80:83], s75 offen offset:2048 sc1 lds\n" \
M_"v[140:143], a[184:187], a[148:151], v[140:143]\n" \
C_"v180, v180, v181\n" \
"v_accvgpr_read_b32 v50, a82\n" \
M_"v[140:143], a[248:251], a[212:215], v[140:143]\n" \
"ds_write_b128 v27, v[76:79] offset:5120\n" \
M_"v[136:139], a[180:183], a[148:151], v[136:139]\n" \
"v_accvgpr_read_b32 v51, a83\n" \
C_"v181, v182, v183\n" \
M_"v[136:139], a[244:247], a[212:215], v[136:139]\n" \
B_"v20, s[80:83], s75 offen offset:3072 sc1 lds\n" \
M_"v[132:135], a[176:179], a[148:151], v[132:135]\n" \
"s_mov_b32 m0, s53\n" \
C_"v182, v50, v51\n" \
M_"v[132:135], a[240:243], a[212:215], v[132:135]\n" \
"v_mov_b32_e32 v183, 0x3f80\n" \
C_"v234, v244, v245\n" \
M_"v[148:151], a[176:179], a[152:155], v[148:151]\n" \
"v_mov_b32_e32 v235, 0\n" \
C_"v54, v192, v193\n" \
M_"v[148:151], a[240:243], a[216:219], v[148:151]\n" \
B_"v20, s[80:83], s76 offen offset:0 sc1 lds\n" \
M_"v[152:155], a[180:183], a[152:155], v[152:155]\n" \
C_"v55, v194, v195\n" \
C_"v56, v208, v209\n" \
M_"v[152:155], a[244:247], a[216:219], v[152:155]\n" \
C_"v57, v210, v211\n" \
C_"v58, v224, v225\n" \
C_"v59, v226, v227\n" \
C_"v60, v240, v241\n" \
C_"v61, v242, v243\n" \
M_"v[32:35], v[180:183], v[184:187], 0\n" \
B_"v20, s[80:83], s76 offen offset:1024 sc1 lds\n" \
M_"v[156:159], a[184:187], a[152:155], v[156:159]\n" \
M_"v[192:195], v[188:191], v[184:187], 0\n" \
M_"v[208:211], v[204:207], v[200:203], 0\n" \
M_"v[36:39], v[188:191], v[200:203], 0\n" \
M_"v[224:227], v[220:223], v[216:219], 0\n" \
M_"v[248:251], v[204:207], v[216:219], 0\n" \
M_"v[240:243], v[236:239], v[232:235], 0\n" \
M_"v[64:67], v[220:223], v[232:235], 0\n" \
"s_mov_b32 s64, 0xaaaaaaaa\n" \
"s_mov_b32 s65, 0xaaaaaaaa\n" \
M_"v[156:159], a[248:251], a[216:219], v[156:159]\n" \
"v_cndmask_b32_e64 v32, v32, v33, s[64:65]\n" \
"v_cndmask_b32_e64 v34, v34, v35, s[64:65]\n" \
M_"v[160:163], a[188:191], a[152:155], v[160:163]\n" \
"s_mov_b32 s66, 0xfff0ffff\n" \
"s_mov_b32 s67, 0xf000ff00\n" \
"s_mov_b32 s68, 0xffc0fffc\n" \
"s_mov_b32 s69, 0xc000fc00\n" \
"v_cndmask_b32_e64 v192, v192, v193, s[64:65]\n" \
"v_add_f32_e64 v32, s47, v32\n" \
"v_cndmask_b32_e64 v194, v194, v195, s[64:65]\n" \
"v_add_f32_e64 v34, s47, v34\n" \
"v_cndmask_b32_e64 v208, v208, v209, s[64:65]\n" \
"v_cndmask_b32_e64 v36, v36, v37, s[64:65]\n" \
"v_cndmask_b32_e64 v210, v210, v211, s[64:65]\n" \
"v_cndmask_b32_e64 v38, v38, v39, s[64:65]\n" \
"v_cndmask_b32_e64 v224, v224, v225, s[64:65]\n" \
"v_cndmask_b32_e64 v248, v248, v249, s[64:65]\n" \
"v_cndmask_b32_e64 v226, v226, v227, s[64:65]\n" \
"v_cndmask_b32_e64 v250, v250, v251, s[64:65]\n" \
"v_cndmask_b32_e64 v240, v240, v241, s[64:65]\n" \
"v_cndmask_b32_e64 v64, v64, v65, s[64:65]\n" \
"v_cndmask_b32_e64 v242, v242, v243, s[64:65]\n" \
"v_cndmask_b32_e64 v66, v66, v67, s[64:65]\n" \
"v_cndmask_b32_e64 v192, v32, v192, s[66:67]\n" \
"v_cndmask_b32_e64 v194, v34, v194, s[68:69]\n" \
"v_cndmask_b32_e64 v208, v36, v208, s[66:67]\n" \
"v_cndmask_b32_e64 v210, v38, v210, s[68:69]\n" \
"v_cndmask_b32_e64 v224, v248, v224, s[66:67]\n" \
"v_cndmask_b32_e64 v226, v250, v226, s[68:69]\n" \
"v_cndmask_b32_e64 v240, v64, v240, s[66:67]\n" \
"v_cndmask_b32_e64 v242, v66, v242, s[68:69]\n" \
M_"v[160:163], a[252:255], a[216:219], v[160:163]\n" \
"v_max_f32_e32 v193, v192, v194\n" \
"v_max_f32_e32 v209, v208, v210\n" \
"v_max_f32_e32 v225, v224, v226\n" \
"v_max_f32_e32 v241, v240, v242\n" \
"v_mul_f32_e32 v84, s70, v84\n" \
"v_permlane32_swap_b32_e32 v193, v225\n" \
"v_permlane32_swap_b32_e32 v209, v241\n" \
"v_mul_f32_e32 v85, s70, v85\n" \
"v_mul_f32_e32 v92, s70, v92\n" \
"v_mul_f32_e32 v93, s70, v93\n" \
"v_permlane32_swap_b32_e32 v196, v228\n" \
"v_permlane32_swap_b32_e32 v212, v244\n" \
"v_max_f32_e32 v193, v193, v225\n" \
"v_max_f32_e32 v209, v209, v241\n" \
"v_accvgpr_read_b32 v48, a80\n" \
"v_accvgpr_read_b32 v49, a81\n" \
"v_mul_f32_e32 v86, s70, v86\n" \
"v_mul_f32_e32 v87, s70, v87\n" \
C_"v84, v84, v85\n" \
"v_mul_f32_e32 v94, s70, v94\n" \
"v_mul_f32_e32 v95, s70, v95\n" \
C_"v92, v92, v93\n" \
"v_permlane16_swap_b32_e32 v228, v244\n" \
"v_permlane16_swap_b32_e32 v193, v209\n" \
M_"v[176:179], a[188:191], a[156:159], v[176:179]\n" \
"s_mov_b32 s72, 0x3eae1a93\n" \
"v_mul_f32_e32 v88, s70, v88\n" \
"v_mul_f32_e32 v89, s70, v89\n" \
"v_mul_f32_e32 v90, s70, v90\n" \
"v_mul_f32_e32 v91, s70, v91\n" \
C_"v85, v86, v87\n" \
"v_mul_f32_e32 v96, s70, v96\n" \
"v_mul_f32_e32 v97, s70, v97\n" \
"v_mul_f32_e32 v98, s70, v98\n" \
"v_mul_f32_e32 v99, s70, v99\n" \
C_"v93, v94, v95\n" \
"v_mul_f32_e32 v45, 0xbfb8aa3b, v228\n" \
"v_mfma_f32_16x16x16_bf16 v[48:51], v[182:183], v[46:47], 0\n" \
"v_mfma_f32_16x16x16_bf16 v[196:199], v[190:191], v[46:47], 0\n" \
"v_mfma_f32_16x16x16_bf16 v[212:215], v[206:207], v[46:47], 0\n" \
"v_max_f32_e64 v193, v193, v209\n" \
C_"v86, v88, v89\n" \
C_"v87, v90, v91\n" \
C_"v94, v96, v97\n" \
C_"v95, v98, v99\n" \
"v_mfma_f32_16x16x16_bf16 v[228:231], v[222:223], v[46:47], 0\n" \
"v_mul_f32_e32 v193, s72, v193\n" \
"v_mov_b32_e32 v209, v193\n" \
"ds_write_b128 v27, v[84:87] offset:8192\n" \
"v_mfma_f32_16x16x16_bf16 v[244:247], v[238:239], v[46:47], 0\n" \
"v_permlane16_swap_b32_e32 v193, v209\n" \
"s_mov_b32 s71, 0x3e0293ee\n" \
M_"v[176:179], a[252:255], a[220:223], v[176:179]\n" \
"v_mov_b32_e32 v225, v193\n" \
"v_mov_b32_e32 v241, v209\n" \
"v_mul_f32_e32 v100, s70, v100\n" \
"v_mul_f32_e64 v101, s70, v101\n" \
"ds_write_b128 v27, v[92:95] offset:9216\n" \
"v_mul_f32_e32 v108, s70, v108\n" \
"v_mul_f32_e32 v109, s70, v109\n" \
"v_pk_mul_f32 v[116:117], v[116:117], s[70:71]\n" \
"v_pk_mul_f32 v[124:125], v[124:125], s[70:71]\n" \
"v_pk_mul_f32 v[132:133], v[132:133], s[70:71]\n" \
"v_pk_mul_f32 v[140:141], v[140:141], s[70:71]\n" \
"v_pk_mul_f32 v[148:149], v[148:149], s[70:71]\n" \
"v_pk_mul_f32 v[156:157], v[156:157], s[70:71]\n" \
"v_permlane32_swap_b32_e32 v193, v225\n" \
"v_permlane32_swap_b32_e32 v209, v241\n" \
"v_mul_f32_e32 v102, s70, v102\n" \
"v_mul_f32_e32 v103, s70, v103\n" \
C_"v100, v100, v101\n" \
"v_mul_f32_e32 v110, s70, v110\n" \
"v_mul_f32_e32 v111, s70, v111\n" \
C_"v108, v108, v109\n" \
"v_pk_mul_f32 v[118:119], v[118:119], s[70:71]\n" \
C_"v116, v116, v117\n" \
"v_pk_mul_f32 v[126:127], v[126:127], s[70:71]\n" \
C_"v124, v124, v125\n" \
"v_pk_mul_f32 v[134:135], v[134:135], s[70:71]\n" \
C_"v132, v132, v133\n" \
"v_pk_mul_f32 v[142:143], v[142:143], s[70:71]\n" \
C_"v140, v140, v141\n" \
"v_pk_mul_f32 v[150:151], v[150:151], s[70:71]\n" \
C_"v148, v148, v149\n" \
"v_pk_mul_f32 v[158:159], v[158:159], s[70:71]\n" \
C_"v156, v156, v157\n" \
"v_fma_f32 v192, v192, s72, -v193\n" \
"v_fma_f32 v194, v194, s72, -v193\n" \
"v_fma_f32 v208, v208, s72, -v209\n" \
"v_fma_f32 v210, v210, s72, -v209\n" \
"v_mul_f32_e32 v104, s70, v104\n" \
"v_mul_f32_e32 v105, s70, v105\n" \
"v_mul_f32_e32 v106, s70, v106\n" \
"v_mul_f32_e32 v107, s70, v107\n" \
C_"v101, v102, v103\n" \
"v_mul_f32_e32 v112, s70, v112\n" \
"v_mul_f32_e32 v113, s70, v113\n" \
"v_mul_f32_e32 v114, s70, v114\n" \
"v_mul_f32_e32 v115, s70, v115\n" \
C_"v109, v110, v111\n" \
"v_pk_mul_f32 v[120:121], v[120:121], s[70:71]\n" \
"v_pk_mul_f32 v[122:123], v[122:123], s[70:71]\n" \
C_"v117, v118, v119\n" \
"v_pk_mul_f32 v[128:129], v[128:129], s[70:71]\n" \
"v_pk_mul_f32 v[130:131], v[130:131], s[70:71]\n" \
C_"v125, v126, v127\n" \
"v_pk_mul_f32 v[136:137], v[136:137], s[70:71]\n" \
"v_pk_mul_f32 v[138:139], v[138:139], s[70:71]\n" \
C_"v133, v134, v135\n" \
"v_pk_mul_f32 v[144:145], v[144:145], s[70:71]\n" \
"v_pk_mul_f32 v[146:147], v[146:147], s[70:71]\n" \
C_"v141, v142, v143\n" \
"v_pk_mul_f32 v[152:153], v[152:153], s[70:71]\n" \
"v_pk_mul_f32 v[154:155], v[154:155], s[70:71]\n" \
C_"v149, v150, v151\n" \
"v_pk_mul_f32 v[160:161], v[160:161], s[70:71]\n" \
"v_pk_mul_f32 v[162:163], v[162:163], s[70:71]\n" \
C_"v157, v158, v159\n" \
"v_pk_mul_f32 v[176:177], v[176:177], s[70:71]\n" \
"v_pk_mul_f32 v[178:179], v[178:179], s[70:71]\n" \
E_"v192, v192\n" \
E_"v194, v194\n" \
E_"v208, v208\n" \
E_"v210, v210\n" \
"v_fma_f32 v224, v224, s72, -v225\n" \
"v_fma_f32 v226, v226, s72, -v225\n" \
M_"v[172:175], a[184:187], a[156:159], v[172:175]\n" \
C_"v102, v104, v105\n" \
C_"v103, v106, v107\n" \
C_"v110, v112, v113\n" \
C_"v111, v114, v115\n" \
C_"v118, v120, v121\n" \
C_"v119, v122, v123\n" \
C_"v126, v128, v129\n" \
C_"v127, v130, v131\n" \
C_"v134, v136, v137\n" \
C_"v135, v138, v139\n" \
C_"v142, v144, v145\n" \
C_"v143, v146, v147\n" \
C_"v150, v152, v153\n" \
C_"v151, v154, v155\n" \
C_"v158, v160, v161\n" \
C_"v159, v162, v163\n" \
E_"v224, v224\n" \
E_"v226, v226\n" \
"v_fma_f32 v240, v240, s72, -v241\n" \
"v_fma_f32 v242, v242, s72, -v241\n" \
"ds_write_b128 v27, v[100:103] offset:12288\n" \
C_"v192, v192, v194\n" \
C_"v208, v208, v210\n" \
E_"v240, v240\n" \
E_"v242, v242\n" \
C_"v68, v48, v49\n" \
C_"v69, v50, v51\n" \
C_"v70, v196, v197\n" \
C_"v71, v198, v199\n" \
"ds_write_b128 v27, v[108:111] offset:13312\n" \
C_"v72, v212, v213\n" \
C_"v73, v214, v215\n" \
"v_perm_b32 v184, v192, v192, v40\n" \
"v_perm_b32 v185, v192, v192, v41\n" \
"v_perm_b32 v186, v192, v192, v42\n" \
"v_perm_b32 v187, v192, v192, v43\n" \
"v_perm_b32 v200, v208, v208, v40\n" \
"v_perm_b32 v201, v208, v208, v41\n" \
"v_perm_b32 v202, v208, v208, v42\n" \
"v_perm_b32 v203, v208, v208, v43\n" \
"ds_write_b128 v21, v[116:119] offset:16384\n" \
C_"v224, v224, v226\n" \
C_"v74, v228, v229\n" \
C_"v75, v230, v231\n" \
M_"v[32:35], v[68:71], v[184:187], 0\n" \
M_"v[36:39], v[70:73], v[200:203], 0\n" \
"v_perm_b32 v216, v224, v224, v40\n" \
"v_perm_b32 v217, v224, v224, v41\n" \
"ds_write_b128 v21, v[124:127] offset:17408\n" \
"v_perm_b32 v218, v224, v224, v42\n" \
"v_perm_b32 v219, v224, v224, v43\n" \
C_"v240, v240, v242\n" \
M_"v[172:175], a[248:251], a[220:223], v[172:175]\n" \
C_"v76, v244, v245\n" \
C_"v77, v246, v247\n" \
M_"v[248:251], v[72:75], v[216:219], 0\n" \
"v_perm_b32 v232, v240, v240, v40\n" \
"v_perm_b32 v233, v240, v240, v41\n" \
"ds_write_b128 v21, v[132:135] offset:20480\n" \
"v_perm_b32 v234, v240, v240, v42\n" \
"v_perm_b32 v235, v240, v240, v43\n" \
"v_pk_mul_f32 v[172:173], v[172:173], s[70:71]\n" \
"v_pk_mul_f32 v[174:175], v[174:175], s[70:71]\n" \
C_"v172, v172, v173\n" \
C_"v173, v174, v175\n" \
C_"v174, v176, v177\n" \
C_"v175, v178, v179\n" \
M_"v[64:67], v[74:77], v[232:235], 0\n" \
"ds_write_b128 v21, v[140:143] offset:21504\n" \
M_"v[168:171], a[180:183], a[156:159], v[168:171]\n" \
C_"v52, v252, v253\n" \
C_"v53, v254, v255\n" \
M_"v[168:171], a[244:247], a[220:223], v[168:171]\n" \
"v_exp_f32_e64 v45, v45\n" \
M_"v[192:195], v[52:55], v[184:187], 0\n" \
"ds_write_b128 v21, v[148:151] offset:24576\n" \
M_"v[208:211], v[54:57], v[200:203], 0\n" \
M_"v[224:227], v[56:59], v[216:219], 0\n" \
M_"v[240:243], v[58:61], v[232:235], 0\n" \
"ds_write_b128 v21, v[156:159] offset:25600\n" \
"v_mov_b32_dpp v34, v38 quad_perm:[0,1,2,3] row_mask:0x2 bank_mask:0xf\n" \
"v_pk_mul_f32 v[168:169], v[168:169], s[70:71]\n" \
"v_pk_mul_f32 v[170:171], v[170:171], s[70:71]\n" \
"v_mov_b32_dpp v34, v250 quad_perm:[0,1,2,3] row_mask:0x4 bank_mask:0xf\n" \
"v_add_f32_e64 v45, 1.0, v45\n" \
M_"v[164:167], a[176:179], a[156:159], v[164:167]\n" \
"v_mov_b32_dpp v34, v66 quad_perm:[0,1,2,3] row_mask:0x8 bank_mask:0xf\n" \
"v_mov_b32_dpp v32, v36 quad_perm:[0,1,2,3] row_mask:0x2 bank_mask:0xf\n" \
"v_rcp_f32_e64 v45, v45\n" \
"ds_write_b128 v21, v[172:175] offset:29696\n" \
"v_mov_b32_dpp v33, v37 quad_perm:[0,1,2,3] row_mask:0x2 bank_mask:0xf\n" \
"v_rcp_f32_e64 v34, v34\n" \
"v_mov_b32_dpp v32, v248 quad_perm:[0,1,2,3] row_mask:0x4 bank_mask:0xf\n" \
"v_mov_b32_dpp v33, v249 quad_perm:[0,1,2,3] row_mask:0x4 bank_mask:0xf\n" \
"v_mul_f32_e64 v34, v45, v34\n" \
"v_mov_b32_dpp v32, v64 quad_perm:[0,1,2,3] row_mask:0x8 bank_mask:0xf\n" \
"v_mov_b32_dpp v33, v65 quad_perm:[0,1,2,3] row_mask:0x8 bank_mask:0xf\n" \
"v_mul_f32_e32 v32, v34, v32\n" \
"v_mul_f32_e32 v33, v34, v33\n" \
"v_mov_b32_e64 v38, v34\n" \
M_"v[164:167], a[240:243], a[220:223], v[164:167]\n" \
"v_sub_f32_e32 v44, 1.0, v45\n" \
"v_permlane16_swap_b32_e32 v34, v38\n" \
"v_mov_b32_e32 v250, v34\n" \
"v_mov_b32_e32 v66, v38\n" \
C_"v32, v32, v33\n" \
"v_permlane32_swap_b32_e32 v34, v250\n" \
"v_permlane32_swap_b32_e32 v38, v66\n" \
"v_mul_f32_e64 v192, v34, v192\n" \
"v_pk_mul_f32 v[164:165], v[164:165], s[70:71]\n" \
"v_pk_mul_f32 v[166:167], v[166:167], s[70:71]\n" \
C_"v164, v164, v165\n" \
C_"v165, v166, v167\n" \
C_"v166, v168, v169\n" \
C_"v167, v170, v171\n" \
"ds_write_b128 v21, v[164:167] offset:28672\n" \
"v_mul_f32_e32 v193, v34, v193\n" \
"v_mul_f32_e32 v208, v38, v208\n" \
"v_mul_f32_e32 v209, v38, v209\n" \
"v_mul_f32_e32 v224, v250, v224\n" \
"v_mul_f32_e32 v225, v250, v225\n" \
"v_mul_f32_e32 v240, v66, v240\n" \
"v_mul_f32_e32 v241, v66, v241\n" \
"v_mul_f32_e32 v194, v34, v194\n" \
"v_mul_f32_e64 v195, v34, v195\n" \
C_"v192, v192, v193\n" \
"v_mul_f32_e32 v210, v38, v210\n" \
"v_mul_f32_e32 v211, v38, v211\n" \
C_"v208, v208, v209\n" \
"v_mul_f32_e32 v226, v250, v226\n" \
"v_mul_f32_e32 v227, v250, v227\n" \
C_"v224, v224, v225\n" \
"v_mul_f32_e32 v242, v66, v242\n" \
"v_mul_f32_e32 v243, v66, v243\n" \
C_"v240, v240, v241\n" \
C_"v193, v194, v195\n" \
"s_add_u32 s84, s48, 0x0\n" \
"s_nop 0\n" \
C_"v209, v210, v211\n" \
"s_add_u32 s85, s48, 0x24800\n" \
C_"v225, v226, v227\n" \
"s_add_u32 s86, s48, 0x49000\n" \
C_"v241, v242, v243\n" \
"s_add_u32 s87, s48, 0x6d800\n" \
"ds_write_b32 v26, v44\n" \
"buffer_store_dword v32, v25, s[80:83], s49 offen sc1\n" \
"buffer_store_dwordx2 v[192:193], v24, s[80:83], s84 offen sc1\n" \
"buffer_store_dwordx2 v[208:209], v24, s[80:83], s85 offen sc1\n" \
"buffer_store_dwordx2 v[224:225], v24, s[80:83], s86 offen sc1\n" \
"buffer_store_dwordx2 v[240:241], v24, s[80:83], s87 offen sc1\n" \
"s_branch L_end_%=\n" \
"L_poll60_%=:\n" \
"s_mov_b32 s84, 0\n" \
"L_pl60_%=:\n" \
"s_sleep 1\n" \
"v_bfe_u32 v44, v20, 2, 5\n" \
"buffer_load_dword v44, v44, s[80:83], s38 offen offset:128 sc1\n" \
"s_waitcnt vmcnt(0)\n" \
"v_cmp_ne_u32_e32 vcc, s62, v44\n" \
"s_add_u32 s84, s84, 1\n" \
"s_nop 3\n" \
"s_cbranch_vccz L_pollr60_%=\n" \
"s_cmp_gt_u32 s84, 0x400000\n" \
"s_cbranch_scc0 L_pl60_%=\n" \
"s_branch L_pollr60_%=\n" \
"L_poll160_%=:\n" \
"s_mov_b32 s84, 0\n" \
"L_pl160_%=:\n" \
"s_sleep 1\n" \
"v_bfe_u32 v44, v20, 2, 5\n" \
"buffer_load_dword v44, v44, s[80:83], s38 offen offset:0 sc1\n" \
"s_waitcnt vmcnt(0)\n" \
"v_cmp_ne_u32_e32 vcc, s62, v44\n" \
"s_add_u32 s84, s84, 1\n" \
"s_nop 3\n" \
"s_cbranch_vccz L_pollr160_%=\n" \
"s_cmp_gt_u32 s84, 0x400000\n" \
"s_cbranch_scc0 L_pl160_%=\n" \
"s_branch L_pollr160_%=\n" \
".fill (L_pb + 42500 - .) / 4, 4, 0xBF800000\n" \
"L_w1_%=:\n" \
"s_waitcnt vmcnt(19)\n" \
"s_barrier\n" \
X_"a[160:161], v32 offset:0\n" \
X_"a[162:163], v32 offset:1024\n" \
R_"a[128:131], v28 offset:16384\n" \
X_"a[224:225], v32 offset:8192\n" \
X_"a[226:227], v32 offset:9216\n" \
R_"a[192:195], v29 offset:16384\n" \
R_"a[132:135], v28 offset:18432\n" \
R_"a[196:199], v29 offset:18432\n" \
X_"a[164:165], v33 offset:0\n" \
X_"a[166:167], v33 offset:1024\n" \
X_"a[228:229], v33 offset:8192\n" \
X_"a[230:231], v33 offset:9216\n" \
R_"a[136:139], v28 offset:20480\n" \
R_"a[200:203], v29 offset:20480\n" \
"s_waitcnt lgkmcnt(11)\n" \
M_"a[0:3], a[128:131], a[160:163], 0\n" \
X_"a[168:169], v34 offset:0\n" \
"s_lshr_b32 s4, s2, 1\n" \
"s_waitcnt lgkmcnt(9)\n" \
M_"a[0:3], a[192:195], a[224:227], a[0:3]\n" \
X_"a[170:171], v34 offset:1024\n" \
"s_nop 5\n" \
"s_waitcnt lgkmcnt(9)\n" \
M_"a[16:19], a[132:135], a[160:163], 0\n" \
X_"a[232:233], v34 offset:8192\n" \
"s_and_b32 s4, s4, 0x7c\n" \
"s_waitcnt lgkmcnt(9)\n" \
M_"a[16:19], a[196:199], a[224:227], a[16:19]\n" \
X_"a[234:235], v34 offset:9216\n" \
"s_nop 5\n" \
"s_waitcnt lgkmcnt(8)\n" \
M_"a[20:23], a[132:135], a[164:167], 0\n" \
R_"a[140:143], v28 offset:22528\n" \
"v_mov_b32_e32 v213, s13\n" \
"s_waitcnt lgkmcnt(7)\n" \
M_"a[20:23], a[196:199], a[228:231], a[20:23]\n" \
R_"a[204:207], v29 offset:22528\n" \
"s_nop 5\n" \
M_"a[4:7], a[128:131], a[164:167], 0\n" \
X_"a[172:173], v35 offset:0\n" \
"s_or_b32 s4, s4, s3\n" \
M_"a[4:7], a[192:195], a[228:231], a[4:7]\n" \
X_"a[174:175], v35 offset:1024\n" \
"s_nop 5\n" \
"s_waitcnt lgkmcnt(9)\n" \
M_"a[32:35], a[136:139], a[160:163], 0\n" \
X_"a[236:237], v35 offset:8192\n" \
"v_lshrrev_b32_e32 v212, 4, v20\n" \
"s_waitcnt lgkmcnt(9)\n" \
M_"a[32:35], a[200:203], a[224:227], a[32:35]\n" \
X_"a[238:239], v35 offset:9216\n" \
"s_nop 5\n" \
M_"a[36:39], a[136:139], a[164:167], 0\n" \
R_"a[144:147], v28 offset:0\n" \
"v_mad_u64_u32 v[214:215], s[4:5], s4, 36, v[212:213]\n" \
M_"a[36:39], a[200:203], a[228:231], a[36:39]\n" \
R_"a[208:211], v29 offset:0\n" \
"s_nop 5\n" \
"s_waitcnt lgkmcnt(10)\n" \
M_"a[40:43], a[136:139], a[168:171], 0\n" \
R_"a[148:151], v28 offset:2048\n" \
"v_mov_b32_e32 v217, s9\n" \
"s_waitcnt lgkmcnt(9)\n" \
M_"a[40:43], a[200:203], a[232:235], a[40:43]\n" \
R_"a[212:215], v29 offset:2048\n" \
"s_nop 5\n" \
M_"a[24:27], a[132:135], a[168:171], 0\n" \
R_"a[152:155], v28 offset:4096\n" \
"s_movk_i32 s4, 0x240\n" \
M_"a[24:27], a[196:199], a[232:235], a[24:27]\n" \
R_"a[216:219], v29 offset:4096\n" \
"s_nop 5\n" \
M_"a[8:11], a[128:131], a[168:171], 0\n" \
R_"a[156:159], v28 offset:6144\n" \
"v_cmp_gt_u32_e32 vcc, s4, v20\n" \
M_"a[8:11], a[192:195], a[232:235], a[8:11]\n" \
R_"a[220:223], v29 offset:6144\n" \
"s_mov_b32 m0, s32\n" \
"s_nop 5\n" \
"s_waitcnt lgkmcnt(13)\n" \
M_"a[48:51], a[140:143], a[160:163], 0\n" \
"s_mov_b32 exec_lo, 0xff000000\n" \
"s_mov_b32 exec_hi, 0x0\n" \
"global_load_lds_dword v[184:185], off\n" \
"s_mov_b64 exec, -1\n" \
"s_waitcnt lgkmcnt(12)\n" \
M_"a[48:51], a[204:207], a[224:227], a[48:51]\n" \
"s_nop 5\n" \
X_"a[176:177], v36 offset:0\n" \
M_"a[52:55], a[140:143], a[164:167], 0\n" \
X_"a[178:179], v36 offset:1024\n" \
"s_lshl_b32 s4, s2, 3\n" \
M_"a[52:55], a[204:207], a[228:231], a[52:55]\n" \
"s_mov_b32 m0, s32\n" \
"s_nop 5\n" \
M_"a[56:59], a[140:143], a[168:171], 0\n" \
"s_mov_b32 exec_lo, 0xffff\n" \
"s_mov_b32 exec_hi, 0x0\n" \
"global_load_lds_dword v[186:187], off\n" \
"s_mov_b64 exec, -1\n" \
"s_mov_b64 exec, 0\n" \
"s_swappc_b64 s[88:89], s[86:87]\n" \
"s_mov_b64 exec, -1\n" \
"s_add_u32 s86, s86, 0x2000\n" \
"s_addc_u32 s87, s87, 0\n" \
M_"a[56:59], a[204:207], a[232:235], a[56:59]\n" \
"s_nop 5\n" \
X_"a[240:241], v36 offset:8192\n" \
"s_waitcnt lgkmcnt(13)\n" \
M_"a[60:63], a[140:143], a[172:175], 0\n" \
X_"a[242:243], v36 offset:9216\n" \
"s_and_b32 s4, s4, 56\n" \
"s_waitcnt lgkmcnt(12)\n" \
M_"a[60:63], a[204:207], a[236:239], a[60:63]\n" \
"s_nop 5\n" \
X_"a[180:181], v37 offset:0\n" \
M_"a[44:47], a[136:139], a[172:175], 0\n" \
X_"a[182:183], v37 offset:1024\n" \
"v_cndmask_b32_e32 v217, v217, v213, vcc\n" \
M_"a[44:47], a[200:203], a[236:239], a[44:47]\n" \
"s_nop 5\n" \
X_"a[244:245], v37 offset:8192\n" \
M_"a[28:31], a[132:135], a[172:175], 0\n" \
X_"a[246:247], v37 offset:9216\n" \
"v_lshlrev_b32_e32 v213, 3, v214\n" \
M_"a[28:31], a[196:199], a[236:239], a[28:31]\n" \
"s_nop 5\n" \
X_"a[184:185], v38 offset:0\n" \
M_"a[12:15], a[128:131], a[172:175], 0\n" \
X_"a[186:187], v38 offset:1024\n" \
"v_and_b32_e32 v213, 0x1ffc0, v213\n" \
M_"a[12:15], a[192:195], a[236:239], a[12:15]\n" \
"s_nop 5\n" \
"v_and_b32_e32 v214, 7, v214\n" \
"s_waitcnt lgkmcnt(15)\n" \
M_"a[76:79], a[144:147], a[172:175], 0\n" \
X_"a[248:249], v38 offset:8192\n" \
"v_or3_b32 v213, v214, s4, v213\n" \
M_"a[76:79], a[208:211], a[236:239], a[76:79]\n" \
"s_nop 5\n" \
"s_lshl_b32 s4, s2, 2\n" \
M_"a[72:75], a[144:147], a[168:171], 0\n" \
"s_or_b32 s4, s4, s3\n" \
"s_mul_i32 s4, s4, 18\n" \
M_"a[72:75], a[208:211], a[232:235], a[72:75]\n" \
"s_nop 5\n" \
X_"a[250:251], v38 offset:9216\n" \
M_"a[68:71], a[144:147], a[164:167], 0\n" \
"s_mov_b64 exec, 0\n" \
"s_swappc_b64 s[88:89], s[86:87]\n" \
"s_mov_b64 exec, -1\n" \
"s_add_u32 s86, s86, 0x2000\n" \
"s_addc_u32 s87, s87, 0\n" \
M_"a[68:71], a[208:211], a[228:231], a[68:71]\n" \
"s_nop 5\n" \
"v_min_u32_e64 v212, 53, v212\n" \
M_"a[64:67], a[144:147], a[160:163], 0\n" \
"s_sub_i32 s4, s4, 36\n" \
"v_add_u32_e32 v212, s4, v212\n" \
M_"a[64:67], a[208:211], a[224:227], a[64:67]\n" \
"s_nop 5\n" \
X_"a[188:189], v39 offset:0\n" \
M_"a[80:83], a[148:151], a[160:163], 0\n" \
"v_mov_b32_e32 v215, s12\n" \
"v_mov_b32_e32 v216, s8\n" \
"s_waitcnt lgkmcnt(15)\n" \
M_"a[80:83], a[212:215], a[224:227], a[80:83]\n" \
"s_nop 5\n" \
"v_cndmask_b32_e32 v212, v212, v213, vcc\n" \
M_"a[84:87], a[148:151], a[164:167], 0\n" \
X_"a[190:191], v39 offset:1024\n" \
"v_cndmask_b32_e64 v216, v216, v215, vcc\n" \
M_"a[84:87], a[212:215], a[228:231], a[84:87]\n" \
"s_nop 5\n" \
"v_lshlrev_b32_e32 v212, 7, v212\n" \
M_"a[88:91], a[148:151], a[168:171], 0\n" \
"v_mov_b32_e64 v213, 0\n" \
"v_lshl_add_u64 v[188:189], v[216:217], 0, v[212:213]\n" \
M_"a[88:91], a[212:215], a[232:235], a[88:91]\n" \
"s_nop 5\n" \
X_"a[252:253], v39 offset:8192\n" \
M_"a[92:95], a[148:151], a[172:175], 0\n" \
"s_movk_i32 s5, 0xd80\n" \
"s_lshr_b32 s6, s2, 5\n" \
M_"a[92:95], a[212:215], a[236:239], a[92:95]\n" \
"s_nop 5\n" \
"v_lshrrev_b32_e64 v212, 4, v20\n" \
M_"a[108:111], a[152:155], a[172:175], 0\n" \
X_"a[254:255], v39 offset:9216\n" \
"v_lshl_or_b32 v215, s3, 6, v212\n" \
M_"a[108:111], a[216:219], a[236:239], a[108:111]\n" \
"s_nop 5\n" \
"v_mul_u32_u24_e32 v216, 0x2aab, v215\n" \
M_"a[104:107], a[152:155], a[168:171], 0\n" \
"s_mov_b64 exec, 0\n" \
"s_swappc_b64 s[88:89], s[86:87]\n" \
"s_mov_b64 exec, -1\n" \
"s_add_u32 s86, s86, 0x2000\n" \
"s_addc_u32 s87, s87, 0\n" \
M_"a[104:107], a[216:219], a[232:235], a[104:107]\n" \
"s_nop 5\n" \
"v_lshrrev_b32_e64 v216, 16, v216\n" \
M_"a[100:103], a[152:155], a[164:167], 0\n" \
"v_mad_i32_i24 v217, v216, -6, v215\n" \
"v_add_u32_e32 v218, 0xfffffe50, v215\n" \
M_"a[100:103], a[216:219], a[228:231], a[100:103]\n" \
"s_nop 5\n" \
"s_mul_i32 s35, s6, 0x48\n" \
M_"a[96:99], a[152:155], a[160:163], 0\n" \
"v_lshrrev_b32_e32 v219, 1, v217\n" \
"v_add_u32_e64 v218, s35, v216\n" \
M_"a[96:99], a[216:219], a[224:227], a[96:99]\n" \
"s_nop 5\n" \
"v_mul_u32_u24_e32 v219, 0x480, v219\n" \
"s_waitcnt lgkmcnt(15)\n" \
M_"a[112:115], a[156:159], a[160:163], 0\n" \
"v_and_b32_e64 v214, 16, v20\n" \
"v_mad_u32_u24 v219, v218, s5, v219\n" \
M_"a[112:115], a[220:223], a[224:227], a[112:115]\n" \
"s_nop 5\n" \
"v_or_b32_e32 v192, v219, v214\n" \
M_"a[116:119], a[156:159], a[164:167], 0\n" \
"v_mov_b32_e32 v220, 2.0\n" \
"v_mov_b32_e32 v222, 7\n" \
M_"a[116:119], a[220:223], a[228:231], a[116:119]\n" \
"s_nop 5\n" \
"v_lshlrev_b32_e32 v220, 7, v218\n" \
M_"a[120:123], a[156:159], a[168:171], 0\n" \
"v_lshrrev_b32_e32 v218, 1, v218\n" \
"v_and_b32_e32 v218, 4, v218\n" \
M_"a[120:123], a[220:223], a[232:235], a[120:123]\n" \
"s_nop 5\n" \
"v_and_b32_e32 v216, 2, v216\n" \
M_"a[124:127], a[156:159], a[172:175], 0\n" \
"s_mul_i32 s7, s6, 0x2400\n" \
"s_sub_i32 s34, 0x22000, s7\n" \
"s_waitcnt vmcnt(5) lgkmcnt(0)\n" \
M_"a[124:127], a[220:223], a[236:239], a[124:127]\n" \
"s_barrier\n" \
M_"v[52:55], a[128:131], a[176:179], 0\n" \
"s_add_u32 m0, s30, 0x0\n" \
"s_movk_i32 s84, 0x100\n" \
M_"v[52:55], a[192:195], a[240:243], v[52:55]\n" \
B_"v22, s[64:67], s84 offen offset:0 lds\n" \
"s_nop 5\n" \
M_"v[56:59], a[128:131], a[180:183], 0\n" \
X_"a[160:161], v32 offset:32768\n" \
"v_bitop3_b32 v216, v217, v218, v216 bitop3:0x1e\n" \
M_"v[56:59], a[192:195], a[244:247], v[56:59]\n" \
"s_movk_i32 s84, 0x1d00\n" \
"s_nop 5\n" \
M_"v[60:63], a[128:131], a[184:187], 0\n" \
B_"v23, s[64:67], s84 offen offset:1024 lds\n" \
M_"v[60:63], a[192:195], a[248:251], v[60:63]\n" \
"s_nop 5\n" \
X_"a[162:163], v32 offset:33792\n" \
M_"v[64:67], a[128:131], a[188:191], 0\n" \
X_"a[224:225], v32 offset:40960\n" \
"v_or_b32_e32 v213, 0x24400, v20\n" \
M_"v[64:67], a[192:195], a[252:255], v[64:67]\n" \
R_"a[128:131], v28 offset:49152\n" \
"s_nop 5\n" \
M_"v[80:83], a[132:135], a[188:191], 0\n" \
R_"a[192:195], v29 offset:49152\n" \
"v_lshlrev_b32_e32 v216, 4, v216\n" \
M_"v[80:83], a[196:199], a[252:255], v[80:83]\n" \
"s_movk_i32 s84, 0x3900\n" \
"s_nop 5\n" \
M_"v[76:79], a[132:135], a[184:187], 0\n" \
B_"v22, s[64:67], s84 offen offset:2048 lds\n" \
M_"v[76:79], a[196:199], a[248:251], v[76:79]\n" \
"s_movk_i32 s84, 0x5500\n" \
"s_nop 5\n" \
M_"v[72:75], a[132:135], a[180:183], 0\n" \
B_"v23, s[64:67], s84 offen offset:3072 lds\n" \
"s_mov_b64 exec, 0\n" \
"s_swappc_b64 s[88:89], s[86:87]\n" \
"s_mov_b64 exec, -1\n" \
"s_andn2_b32 s85, 2, s3\n" \
"s_lshl_b32 s85, s85, 12\n" \
"s_add_u32 s86, s86, s85\n" \
"s_addc_u32 s87, s87, 0\n" \
M_"v[72:75], a[196:199], a[244:247], v[72:75]\n" \
"s_add_u32 m0, s31, 0x0\n" \
"s_mov_b32 s84, 0x100000\n" \
"s_nop 5\n" \
M_"v[68:71], a[132:135], a[176:179], 0\n" \
B_"v26, s[68:71], s84 offen offset:0 lds\n" \
M_"v[68:71], a[196:199], a[240:243], v[68:71]\n" \
R_"a[132:135], v28 offset:51200\n" \
"s_nop 5\n" \
M_"v[84:87], a[136:139], a[176:179], 0\n" \
R_"a[196:199], v29 offset:51200\n" \
"v_add3_u32 v196, v220, s34, v216\n" \
M_"v[84:87], a[200:203], a[240:243], v[84:87]\n" \
"s_addk_i32 s84, 0x7c00\n" \
"s_nop 5\n" \
M_"v[88:91], a[136:139], a[180:183], 0\n" \
B_"v26, s[68:71], s84 offen offset:1024 lds\n" \
M_"v[88:91], a[200:203], a[244:247], v[88:91]\n" \
"s_nop 5\n" \
X_"a[226:227], v32 offset:41984\n" \
M_"v[92:95], a[136:139], a[184:187], 0\n" \
"s_addk_i32 s84, 0x7c00\n" \
M_"v[92:95], a[200:203], a[248:251], v[92:95]\n" \
B_"v27, s[68:71], s84 offen offset:2048 lds\n" \
"s_nop 5\n" \
M_"v[96:99], a[136:139], a[188:191], 0\n" \
X_"a[164:165], v33 offset:32768\n" \
"v_mov_b32_e32 v216, v213\n" \
M_"v[96:99], a[200:203], a[252:255], v[96:99]\n" \
R_"a[136:139], v28 offset:53248\n" \
"s_nop 5\n" \
M_"v[112:115], a[140:143], a[188:191], 0\n" \
R_"a[200:203], v29 offset:53248\n" \
"s_movk_i32 s36, 0x1b0\n" \
M_"v[112:115], a[204:207], a[252:255], v[112:115]\n" \
"s_addk_i32 s84, 0x7c00\n" \
"s_nop 5\n" \
M_"v[108:111], a[140:143], a[184:187], 0\n" \
B_"v27, s[68:71], s84 offen offset:3072 lds\n" \
M_"v[108:111], a[204:207], a[248:251], v[108:111]\n" \
"s_add_u32 m0, s30, 0x1000\n" \
"s_mov_b32 s84, 0x8100\n" \
"s_nop 5\n" \
M_"v[104:107], a[140:143], a[180:183], 0\n" \
B_"v22, s[64:67], s84 offen offset:0 lds\n" \
M_"v[104:107], a[204:207], a[244:247], v[104:107]\n" \
"s_nop 5\n" \
X_"a[166:167], v33 offset:33792\n" \
M_"v[100:103], a[140:143], a[176:179], 0\n" \
X_"a[228:229], v33 offset:40960\n" \
"v_or_b32_e32 v216, 0x100, v215\n" \
M_"v[100:103], a[204:207], a[240:243], v[100:103]\n" \
R_"a[140:143], v28 offset:55296\n" \
"s_nop 5\n" \
M_"v[116:119], a[144:147], a[176:179], 0\n" \
R_"a[204:207], v29 offset:55296\n" \
"v_mul_u32_u24_e32 v217, 0x2aab, v216\n" \
M_"v[116:119], a[208:211], a[240:243], v[116:119]\n" \
"s_addk_i32 s84, 0x1c00\n" \
"s_nop 5\n" \
M_"v[120:123], a[144:147], a[180:183], 0\n" \
B_"v23, s[64:67], s84 offen offset:1024 lds\n" \
M_"v[120:123], a[208:211], a[244:247], v[120:123]\n" \
"s_addk_i32 s84, 0x1c00\n" \
"s_nop 5\n" \
M_"v[124:127], a[144:147], a[184:187], 0\n" \
B_"v22, s[64:67], s84 offen offset:2048 lds\n" \
"s_mov_b64 exec, 0\n" \
"s_swappc_b64 s[88:89], s[86:87]\n" \
"s_mov_b64 exec, -1\n" \
M_"v[124:127], a[208:211], a[248:251], v[124:127]\n" \
".fill (L_pb + 44768 - .) / 4, 4, 0xBF800000\n" \
"L_pp11:\n" \
"s_cbranch_execz L_pret\n" \
"s_addk_i32 s84, 0x1c00\n" \
"s_nop 5\n" \
M_"v[128:131], a[144:147], a[188:191], 0\n" \
B_"v23, s[64:67], s84 offen offset:3072 lds\n" \
M_"v[128:131], a[208:211], a[252:255], v[128:131]\n" \
R_"a[144:147], v28 offset:32768\n" \
"s_nop 5\n" \
M_"v[144:147], a[148:151], a[188:191], 0\n" \
R_"a[208:211], v29 offset:32768\n" \
"v_add_u32_e32 v215, 0xffffff50, v215\n" \
M_"v[144:147], a[212:215], a[252:255], v[144:147]\n" \
"s_add_u32 m0, s31, 0x4000\n" \
"s_mov_b32 s84, 0x100000\n" \
"s_nop 5\n" \
M_"v[140:143], a[148:151], a[184:187], 0\n" \
B_"v26, s[72:75], s84 offen offset:0 lds\n" \
M_"v[140:143], a[212:215], a[248:251], v[140:143]\n" \
"s_nop 5\n" \
X_"a[230:231], v33 offset:41984\n" \
M_"v[136:139], a[148:151], a[180:183], 0\n" \
"s_addk_i32 s84, 0x7c00\n" \
M_"v[136:139], a[212:215], a[244:247], v[136:139]\n" \
B_"v26, s[72:75], s84 offen offset:1024 lds\n" \
"s_nop 5\n" \
M_"v[132:135], a[148:151], a[176:179], 0\n" \
X_"a[168:169], v34 offset:32768\n" \
"v_lshrrev_b32_e64 v217, 16, v217\n" \
M_"v[132:135], a[212:215], a[240:243], v[132:135]\n" \
R_"a[148:151], v28 offset:34816\n" \
"s_nop 5\n" \
M_"v[148:151], a[152:155], a[176:179], 0\n" \
R_"a[212:215], v29 offset:34816\n" \
"v_mov_b32_e32 v220, v215\n" \
M_"v[148:151], a[216:219], a[240:243], v[148:151]\n" \
"s_nop 5\n" \
X_"a[170:171], v34 offset:33792\n" \
M_"v[152:155], a[152:155], a[180:183], 0\n" \
X_"a[232:233], v34 offset:40960\n" \
"v_mad_i32_i24 v218, v217, -6, v216\n" \
M_"v[152:155], a[216:219], a[244:247], v[152:155]\n" \
"s_nop 5\n" \
X_"a[234:235], v34 offset:41984\n" \
M_"v[156:159], a[152:155], a[184:187], 0\n" \
X_"a[172:173], v35 offset:32768\n" \
"v_cmp_gt_u32_e64 vcc, s36, v216\n" \
M_"v[156:159], a[216:219], a[248:251], v[156:159]\n" \
"s_nop 5\n" \
X_"a[174:175], v35 offset:33792\n" \
M_"v[160:163], a[152:155], a[188:191], 0\n" \
X_"a[236:237], v35 offset:40960\n" \
"v_mov_b32_e32 v221, 7\n" \
M_"v[160:163], a[216:219], a[252:255], v[160:163]\n" \
R_"a[152:155], v28 offset:36864\n" \
"s_nop 5\n" \
M_"v[176:179], a[156:159], a[188:191], 0\n" \
R_"a[216:219], v29 offset:36864\n" \
"v_lshrrev_b32_e32 v222, 1, v218\n" \
M_"v[176:179], a[220:223], a[252:255], v[176:179]\n" \
"s_nop 5\n" \
X_"a[238:239], v35 offset:41984\n" \
M_"v[172:175], a[156:159], a[184:187], 0\n" \
"v_cndmask_b32_e32 v217, v220, v217, vcc\n" \
"s_movk_i32 s4, 0x48\n" \
M_"v[172:175], a[220:223], a[248:251], v[172:175]\n" \
"s_nop 5\n" \
"v_add_u32_e64 v220, s35, v217\n" \
M_"v[168:171], a[156:159], a[180:183], 0\n" \
"v_mul_u32_u24_e32 v222, 0x480, v222\n" \
"v_mad_u32_u24 v222, v220, s5, v222\n" \
M_"v[168:171], a[220:223], a[244:247], v[168:171]\n" \
"s_nop 5\n" \
"v_cmp_gt_u32_e64 s[4:5], s4, v215\n" \
M_"v[164:167], a[156:159], a[176:179], 0\n" \
"v_cndmask_b32_e32 v215, v221, v218, vcc\n" \
"v_lshrrev_b32_e64 v218, 1, v220\n" \
M_"v[164:167], a[220:223], a[240:243], v[164:167]\n" \
R_"a[156:159], v28 offset:38912\n" \
R_"a[220:223], v29 offset:38912\n" \
"s_waitcnt lgkmcnt(15)\n" \
M_"a[0:3], a[128:131], a[160:163], a[0:3]\n" \
"s_addk_i32 s84, 0x7c00\n" \
M_"a[0:3], a[192:195], a[224:227], a[0:3]\n" \
B_"v27, s[72:75], s84 offen offset:2048 lds\n" \
M_"a[4:7], a[128:131], a[164:167], a[4:7]\n" \
X_"a[176:177], v36 offset:32768\n" \
"v_or_b32_e32 v214, v222, v214\n" \
"s_waitcnt lgkmcnt(15)\n" \
M_"a[4:7], a[192:195], a[228:231], a[4:7]\n" \
"s_addk_i32 s84, 0x7c00\n" \
"s_waitcnt lgkmcnt(11)\n" \
M_"a[8:11], a[128:131], a[168:171], a[8:11]\n" \
B_"v27, s[72:75], s84 offen offset:3072 lds\n" \
"s_waitcnt lgkmcnt(9)\n" \
M_"a[8:11], a[192:195], a[232:235], a[8:11]\n" \
X_"a[178:179], v36 offset:33792\n" \
"v_mov_b32_e32 v222, 2.0\n" \
"s_waitcnt lgkmcnt(8)\n" \
M_"a[12:15], a[128:131], a[172:175], a[12:15]\n" \
X_"a[240:241], v36 offset:40960\n" \
"v_and_b32_e32 v218, 4, v218\n" \
"s_waitcnt lgkmcnt(5)\n" \
M_"a[12:15], a[192:195], a[236:239], a[12:15]\n" \
"v_and_b32_e32 v217, 2, v217\n" \
"v_bitop3_b32 v215, v215, v218, v217 bitop3:0x1e\n" \
M_"a[28:31], a[132:135], a[172:175], a[28:31]\n" \
"v_lshlrev_b32_e32 v223, 1, v220\n" \
"v_mov_b32_e32 v224, 2.0\n" \
M_"a[28:31], a[196:199], a[236:239], a[28:31]\n" \
X_"a[242:243], v36 offset:41984\n" \
"v_cndmask_b32_e64 v193, v222, v214, vcc\n" \
M_"a[24:27], a[132:135], a[168:171], a[24:27]\n" \
"v_lshlrev_b32_e32 v214, 7, v220\n" \
"v_lshlrev_b32_e32 v215, 4, v215\n" \
M_"a[24:27], a[196:199], a[232:235], a[24:27]\n" \
"s_mul_i32 s39, s3, 18\n" \
"v_add3_u32 v214, v214, s34, v215\n" \
M_"a[20:23], a[132:135], a[164:167], a[20:23]\n" \
"v_cndmask_b32_e64 v195, v224, v223, s[4:5]\n" \
"s_movk_i32 s4, 0x1f8\n" \
M_"a[20:23], a[196:199], a[228:231], a[20:23]\n" \
X_"a[180:181], v37 offset:32768\n" \
"v_mov_b32_e64 v215, v213\n" \
M_"a[16:19], a[132:135], a[160:163], a[16:19]\n" \
"s_add_i32 s35, s35, s39\n" \
"v_cmp_gt_u32_e32 vcc, s4, v216\n" \
M_"a[16:19], a[196:199], a[224:227], a[16:19]\n" \
"v_add_u32_e32 v212, s35, v212\n" \
"v_mov_b32_e32 v219, 2.0\n" \
M_"a[32:35], a[136:139], a[160:163], a[32:35]\n" \
"s_mov_b32 m0, s32\n" \
X_"a[182:183], v37 offset:33792\n" \
M_"a[32:35], a[200:203], a[224:227], a[32:35]\n" \
"s_mov_b32 exec_lo, 0x0\n" \
"s_mov_b32 exec_hi, 0xff\n" \
"global_load_lds_dword v[184:185], off\n" \
"s_mov_b64 exec, -1\n" \
M_"a[36:39], a[136:139], a[164:167], a[36:39]\n" \
"v_cndmask_b32_e32 v197, v215, v214, vcc\n" \
"s_movk_i32 s35, 0x120\n" \
M_"a[36:39], a[200:203], a[228:231], a[36:39]\n" \
X_"a[244:245], v37 offset:40960\n" \
"v_mul_u32_u24_e32 v214, 0xd80, v212\n" \
M_"a[40:43], a[136:139], a[168:171], a[40:43]\n" \
"v_cmp_gt_u32_e32 vcc, s35, v20\n" \
"v_lshrrev_b32_e64 v215, 1, v212\n" \
M_"a[40:43], a[200:203], a[232:235], a[40:43]\n" \
"s_mov_b32 m0, s32\n" \
"s_and_b32 s4, s2, 7\n" \
M_"a[44:47], a[136:139], a[172:175], a[44:47]\n" \
"s_mov_b32 exec_lo, 0xffff0000\n" \
"s_mov_b32 exec_hi, 0x0\n" \
"global_load_lds_dword v[186:187], off\n" \
"s_mov_b64 exec, -1\n" \
M_"a[44:47], a[200:203], a[236:239], a[44:47]\n" \
X_"a[246:247], v37 offset:41984\n" \
"v_cndmask_b32_e64 v194, v219, v214, vcc\n" \
M_"a[60:63], a[140:143], a[172:175], a[60:63]\n" \
"v_lshlrev_b32_e32 v214, 7, v212\n" \
"v_lshrrev_b32_e32 v212, 2, v212\n" \
M_"a[60:63], a[204:207], a[236:239], a[60:63]\n" \
"v_and_b32_e64 v212, 2, v212\n" \
"v_and_or_b32 v212, v215, 1, v212\n" \
M_"a[56:59], a[140:143], a[168:171], a[56:59]\n" \
X_"a[184:185], v38 offset:32768\n" \
"s_lshl_b32 s5, s4, 2\n" \
M_"a[56:59], a[204:207], a[232:235], a[56:59]\n" \
"s_bfe_u32 s38, s2, 0x20003\n" \
"v_lshlrev_b32_e32 v212, 5, v212\n" \
M_"a[52:55], a[140:143], a[164:167], a[52:55]\n" \
"s_movk_i32 s35, 0x60\n" \
"s_or_b32 s5, s5, s38\n" \
M_"a[52:55], a[204:207], a[228:231], a[52:55]\n" \
X_"a[186:187], v38 offset:33792\n" \
"v_bitop3_b32 v212, v212, s35, v214 bitop3:0x36\n" \
M_"a[48:51], a[140:143], a[160:163], a[48:51]\n" \
"s_lshr_b32 s36, s2, 3\n" \
"v_add_u32_e32 v212, s34, v212\n" \
M_"a[48:51], a[204:207], a[224:227], a[48:51]\n" \
"s_mul_i32 s34, s5, 0x12000\n" \
"s_or_b32 s40, s34, 0x6000000\n" \
M_"a[64:67], a[144:147], a[160:163], a[64:67]\n" \
X_"a[248:249], v38 offset:40960\n" \
"s_mul_i32 s34, s36, 0x90000\n" \
M_"a[64:67], a[208:211], a[224:227], a[64:67]\n" \
"s_lshl_b32 s36, s3, 14\n" \
"s_add_i32 s34, s34, s36\n" \
M_"a[68:71], a[144:147], a[164:167], a[68:71]\n" \
"s_lshl_b32 s38, s5, 8\n" \
"s_add_i32 s41, s34, 0x4240000\n" \
M_"a[68:71], a[208:211], a[228:231], a[68:71]\n" \
X_"a[250:251], v38 offset:41984\n" \
"s_mul_i32 s34, s5, 36\n" \
M_"a[72:75], a[144:147], a[168:171], a[72:75]\n" \
"s_add_i32 s5, s7, s40\n" \
"s_addk_i32 s5, 0x2000\n" \
M_"a[72:75], a[208:211], a[232:235], a[72:75]\n" \
"s_mov_b32 s37, 2.0\n" \
"s_or_b32 s38, s38, 0x5be8000\n" \
M_"a[76:79], a[144:147], a[172:175], a[76:79]\n" \
"s_cmp_lg_u32 s3, 0\n" \
"s_cselect_b32 s36, s37, s5\n" \
M_"a[76:79], a[208:211], a[236:239], a[76:79]\n" \
X_"a[188:189], v39 offset:32768\n" \
"s_lshl_b32 s5, s6, 2\n" \
M_"a[92:95], a[148:151], a[172:175], a[92:95]\n" \
"s_or_b32 s5, s38, s5\n" \
"s_lshl_b32 s35, s3, 10\n" \
M_"a[92:95], a[212:215], a[236:239], a[92:95]\n" \
"s_cmpk_lt_u32 s3, 3\n" \
"s_cselect_b32 s37, s37, s5\n" \
M_"a[88:91], a[148:151], a[168:171], a[88:91]\n" \
X_"a[190:191], v39 offset:33792\n" \
"s_lshl_b32 s5, s4, 16\n" \
M_"a[88:91], a[212:215], a[232:235], a[88:91]\n" \
"s_lshl_b32 s4, s4, 11\n" \
"v_cndmask_b32_e64 v198, v213, v212, vcc\n" \
M_"a[84:87], a[148:151], a[164:167], a[84:87]\n" \
"v_or_b32_e32 v212, s35, v20\n" \
"s_or_b32 s39, s40, s35\n" \
M_"a[84:87], a[212:215], a[228:231], a[84:87]\n" \
X_"a[252:253], v39 offset:40960\n" \
"s_or_b32 s55, s41, s4\n" \
M_"a[80:83], a[148:151], a[160:163], a[80:83]\n" \
"v_or_b32_e32 v199, 0x22000, v212\n" \
"s_add_i32 s35, s39, s7\n" \
M_"a[80:83], a[212:215], a[224:227], a[80:83]\n" \
"s_add_i32 s54, s41, s5\n" \
"s_add_i32 s55, s55, 0x80000\n" \
M_"a[96:99], a[152:155], a[160:163], a[96:99]\n" \
X_"a[254:255], v39 offset:41984\n" \
"v_mov_b32_e32 v215, 0xffff\n" \
M_"a[96:99], a[216:219], a[224:227], a[96:99]\n" \
"s_and_b32 s4, s3, 1\n" \
"s_lshl_b32 s5, s4, 4\n" \
M_"a[100:103], a[152:155], a[164:167], a[100:103]\n" \
"v_lshrrev_b32_e32 v212, 4, v20\n" \
"v_lshrrev_b32_e64 v218, 6, v20\n" \
M_"a[100:103], a[216:219], a[228:231], a[100:103]\n" \
"v_and_b32_e64 v219, 12, v218\n" \
"v_and_or_b32 v214, v212, 15, s5\n" \
M_"a[104:107], a[152:155], a[168:171], a[104:107]\n" \
"v_cmp_gt_u32_e64 vcc, v219, v214\n" \
"v_mov_b32_e32 v220, 0xffff0000\n" \
M_"a[104:107], a[216:219], a[232:235], a[104:107]\n" \
"s_lshl_b32 s5, s2, 2\n" \
"v_cndmask_b32_e64 v217, v215, 0, vcc\n" \
M_"a[108:111], a[152:155], a[172:175], a[108:111]\n" \
"v_cmp_lt_u32_e32 vcc, v219, v214\n" \
"v_mov_b32_e64 v213, s23\n" \
M_"a[108:111], a[216:219], a[236:239], a[108:111]\n" \
"s_or_b32 s5, s5, s3\n" \
"v_cndmask_b32_e32 v221, 0, v220, vcc\n" \
"s_waitcnt lgkmcnt(15)\n" \
M_"a[124:127], a[156:159], a[172:175], a[124:127]\n" \
"v_or_b32_e32 v12, v217, v221\n" \
"v_or_b32_e64 v217, 2, v219\n" \
M_"a[124:127], a[220:223], a[236:239], a[124:127]\n" \
"v_cmp_gt_u32_e32 vcc, v217, v214\n" \
"v_mov_b32_e32 v216, s25\n" \
M_"a[120:123], a[156:159], a[168:171], a[120:123]\n" \
"s_movk_i32 s40, 0xf400\n" \
"v_cndmask_b32_e64 v221, v215, 0, vcc\n" \
M_"a[120:123], a[220:223], a[232:235], a[120:123]\n" \
"v_cmp_lt_u32_e32 vcc, v217, v214\n" \
"s_lshl_b32 s4, s4, 6\n" \
M_"a[116:119], a[156:159], a[164:167], a[116:119]\n" \
"v_cndmask_b32_e32 v217, 0, v220, vcc\n" \
"v_or_b32_e64 v13, v221, v217\n" \
M_"a[116:119], a[220:223], a[228:231], a[116:119]\n" \
"v_or_b32_e32 v217, 16, v219\n" \
"v_cmp_gt_u32_e32 vcc, v217, v214\n" \
M_"a[112:115], a[156:159], a[160:163], a[112:115]\n" \
"s_nop 0\n" \
"v_cndmask_b32_e64 v221, v215, 0, vcc\n" \
"v_cmp_lt_u32_e32 vcc, v217, v214\n" \
M_"a[112:115], a[220:223], a[224:227], a[112:115]\n" \
"s_waitcnt vmcnt(20)\n" \
"v_readfirstlane_b32 s62, v180\n" \
"s_nop 3\n" \
"s_add_u32 s62, s62, 0x9E3779B1\n" \
"s_waitcnt vmcnt(2) lgkmcnt(0)\n" \
"s_barrier\n" \
M_"v[52:55], a[128:131], a[176:179], v[52:55]\n" \
"s_add_u32 m0, s30, 0x8000\n" \
"s_movk_i32 s84, 0x180\n" \
M_"v[52:55], a[192:195], a[240:243], v[52:55]\n" \
B_"v22, s[64:67], s84 offen offset:0 lds\n" \
M_"v[56:59], a[128:131], a[180:183], v[56:59]\n" \
X_"a[160:161], v32 offset:0\n" \
"v_cndmask_b32_e64 v217, 0, v220, vcc\n" \
M_"v[56:59], a[192:195], a[244:247], v[56:59]\n" \
"s_movk_i32 s84, 0x1d80\n" \
M_"v[60:63], a[128:131], a[184:187], v[60:63]\n" \
B_"v23, s[64:67], s84 offen offset:1024 lds\n" \
M_"v[60:63], a[192:195], a[248:251], v[60:63]\n" \
X_"a[162:163], v32 offset:1024\n" \
"v_or_b32_e32 v14, v221, v217\n" \
M_"v[64:67], a[128:131], a[188:191], v[64:67]\n" \
X_"a[224:225], v32 offset:8192\n" \
"v_or_b32_e64 v217, 18, v219\n" \
M_"v[64:67], a[192:195], a[252:255], v[64:67]\n" \
R_"a[128:131], v28 offset:16384\n" \
"v_cmp_gt_u32_e64 vcc, v217, v214\n" \
M_"v[80:83], a[132:135], a[188:191], v[80:83]\n" \
R_"a[192:195], v29 offset:16384\n" \
"v_cndmask_b32_e64 v215, v215, 0, vcc\n" \
M_"v[80:83], a[196:199], a[252:255], v[80:83]\n" \
"s_movk_i32 s84, 0x3980\n" \
M_"v[76:79], a[132:135], a[184:187], v[76:79]\n" \
B_"v22, s[64:67], s84 offen offset:2048 lds\n" \
M_"v[76:79], a[196:199], a[248:251], v[76:79]\n" \
"s_movk_i32 s84, 0x5580\n" \
M_"v[72:75], a[132:135], a[180:183], v[72:75]\n" \
B_"v23, s[64:67], s84 offen offset:3072 lds\n" \
M_"v[72:75], a[196:199], a[244:247], v[72:75]\n" \
"s_add_u32 m0, s31, 0x8000\n" \
"s_mov_b32 s84, 0x180000\n" \
M_"v[68:71], a[132:135], a[176:179], v[68:71]\n" \
B_"v26, s[68:71], s84 offen offset:0 lds\n" \
M_"v[68:71], a[196:199], a[240:243], v[68:71]\n" \
R_"a[132:135], v28 offset:18432\n" \
"v_cmp_lt_u32_e64 vcc, v217, v214\n" \
M_"v[84:87], a[136:139], a[176:179], v[84:87]\n" \
R_"a[196:199], v29 offset:18432\n" \
"v_cndmask_b32_e64 v214, 0, v220, vcc\n" \
M_"v[84:87], a[200:203], a[240:243], v[84:87]\n" \
"s_addk_i32 s84, 0x7c00\n" \
M_"v[88:91], a[136:139], a[180:183], v[88:91]\n" \
B_"v26, s[68:71], s84 offen offset:1024 lds\n" \
M_"v[88:91], a[200:203], a[244:247], v[88:91]\n" \
X_"a[226:227], v32 offset:9216\n" \
"v_or_b32_e32 v15, v215, v214\n" \
M_"v[92:95], a[136:139], a[184:187], v[92:95]\n" \
"s_addk_i32 s84, 0x7c00\n" \
M_"v[92:95], a[200:203], a[248:251], v[92:95]\n" \
B_"v27, s[68:71], s84 offen offset:2048 lds\n" \
M_"v[96:99], a[136:139], a[188:191], v[96:99]\n" \
X_"a[164:165], v33 offset:0\n" \
"v_mad_u64_u32 v[214:215], s[6:7], s5, 42, v[212:213]\n" \
M_"v[96:99], a[200:203], a[252:255], v[96:99]\n" \
R_"a[136:139], v28 offset:20480\n" \
"s_movk_i32 s5, 0x1440\n" \
M_"v[112:115], a[140:143], a[188:191], v[112:115]\n" \
R_"a[200:203], v29 offset:20480\n" \
"v_cmp_gt_u32_e64 vcc, s5, v214\n" \
M_"v[112:115], a[204:207], a[252:255], v[112:115]\n" \
"s_addk_i32 s84, 0x7c00\n" \
"s_nop 0\n" \
M_"v[108:111], a[140:143], a[184:187], v[108:111]\n" \
B_"v27, s[68:71], s84 offen offset:3072 lds\n" \
M_"v[108:111], a[204:207], a[248:251], v[108:111]\n" \
"s_add_u32 m0, s30, 0x9000\n" \
"s_mov_b32 s84, 0x8180\n" \
M_"v[104:107], a[140:143], a[180:183], v[104:107]\n" \
B_"v22, s[64:67], s84 offen offset:0 lds\n" \
M_"v[104:107], a[204:207], a[244:247], v[104:107]\n" \
X_"a[166:167], v33 offset:1024\n" \
"v_min_u32_e32 v217, 0xa43f, v214\n" \
M_"v[100:103], a[140:143], a[176:179], v[100:103]\n" \
X_"a[228:229], v33 offset:8192\n" \
"v_mov_b32_e64 v214, s24\n" \
M_"v[100:103], a[204:207], a[240:243], v[100:103]\n" \
R_"a[140:143], v28 offset:22528\n" \
"v_cndmask_b32_e64 v215, v216, v213, vcc\n" \
M_"v[116:119], a[144:147], a[176:179], v[116:119]\n" \
R_"a[204:207], v29 offset:22528\n" \
"v_mov_b32_e64 v213, s22\n" \
M_"v[116:119], a[208:211], a[240:243], v[116:119]\n" \
"s_addk_i32 s84, 0x1c00\n" \
M_"v[120:123], a[144:147], a[180:183], v[120:123]\n" \
B_"v23, s[64:67], s84 offen offset:1024 lds\n" \
M_"v[120:123], a[208:211], a[244:247], v[120:123]\n" \
"s_addk_i32 s84, 0x1c00\n" \
M_"v[124:127], a[144:147], a[184:187], v[124:127]\n" \
B_"v22, s[64:67], s84 offen offset:2048 lds\n" \
M_"v[124:127], a[208:211], a[248:251], v[124:127]\n" \
"s_addk_i32 s84, 0x1c00\n" \
M_"v[128:131], a[144:147], a[188:191], v[128:131]\n" \
B_"v23, s[64:67], s84 offen offset:3072 lds\n" \
M_"v[128:131], a[208:211], a[252:255], v[128:131]\n" \
R_"a[144:147], v28 offset:0\n" \
"s_movk_i32 s6, 0xffee\n" \
M_"v[144:147], a[148:151], a[188:191], v[144:147]\n" \
R_"a[208:211], v29 offset:0\n" \
"v_cndmask_b32_e64 v214, v214, v213, vcc\n" \
M_"v[144:147], a[212:215], a[252:255], v[144:147]\n" \
"s_add_u32 m0, s31, 0xc000\n" \
"s_mov_b32 s84, 0x180000\n" \
M_"v[140:143], a[148:151], a[184:187], v[140:143]\n" \
B_"v26, s[72:75], s84 offen offset:0 lds\n" \
M_"v[140:143], a[212:215], a[248:251], v[140:143]\n" \
X_"a[230:231], v33 offset:9216\n" \
"v_add_u32_e32 v213, 0xffffebc0, v217\n" \
M_"v[136:139], a[148:151], a[180:183], v[136:139]\n" \
"s_addk_i32 s84, 0x7c00\n" \
M_"v[136:139], a[212:215], a[244:247], v[136:139]\n" \
B_"v26, s[72:75], s84 offen offset:1024 lds\n" \
M_"v[132:135], a[148:151], a[176:179], v[132:135]\n" \
X_"a[168:169], v34 offset:0\n" \
"s_lshl_b32 s5, s2, 5\n" \
M_"v[132:135], a[212:215], a[240:243], v[132:135]\n" \
R_"a[148:151], v28 offset:2048\n" \
"v_cndmask_b32_e64 v213, v213, v217, vcc\n" \
M_"v[148:151], a[152:155], a[176:179], v[148:151]\n" \
R_"a[212:215], v29 offset:2048\n" \
"v_lshlrev_b32_e64 v216, 7, v213\n" \
M_"v[148:151], a[216:219], a[240:243], v[148:151]\n" \
X_"a[170:171], v34 offset:1024\n" \
"v_mul_u32_u24_e32 v213, 0xe39, v212\n" \
M_"v[152:155], a[152:155], a[180:183], v[152:155]\n" \
X_"a[232:233], v34 offset:8192\n" \
"v_mov_b32_e64 v217, 0\n" \
M_"v[152:155], a[216:219], a[244:247], v[152:155]\n" \
X_"a[234:235], v34 offset:9216\n" \
"v_lshrrev_b32_e64 v213, 16, v213\n" \
M_"v[156:159], a[152:155], a[184:187], v[156:159]\n" \
X_"a[172:173], v35 offset:0\n" \
"v_lshl_add_u64 v[16:17], v[214:215], 0, v[216:217]\n" \
M_"v[156:159], a[216:219], a[248:251], v[156:159]\n" \
X_"a[174:175], v35 offset:1024\n" \
"v_mad_i32_i24 v214, v213, s6, v212\n" \
M_"v[160:163], a[152:155], a[188:191], v[160:163]\n" \
X_"a[236:237], v35 offset:8192\n" \
"s_add_i32 s5, s5, 32\n" \
M_"v[160:163], a[216:219], a[252:255], v[160:163]\n" \
R_"a[152:155], v28 offset:4096\n" \
"v_lshlrev_b32_e32 v215, 4, v214\n" \
M_"v[176:179], a[156:159], a[188:191], v[176:179]\n" \
R_"a[216:219], v29 offset:4096\n" \
"v_add_u32_e64 v216, s5, v215\n" \
M_"v[176:179], a[220:223], a[252:255], v[176:179]\n" \
X_"a[238:239], v35 offset:9216\n" \
"v_and_b32_e32 v216, 0xf0, v216\n" \
M_"v[172:175], a[156:159], a[184:187], v[172:175]\n" \
"v_cmp_gt_u32_e32 vcc, 16, v214\n" \
"s_movk_i32 s7, 0xf800\n" \
M_"v[172:175], a[220:223], a[248:251], v[172:175]\n" \
"v_cndmask_b32_e32 v214, v215, v216, vcc\n" \
"v_or_b32_e32 v215, 64, v212\n" \
M_"v[168:171], a[156:159], a[180:183], v[168:171]\n" \
"v_mul_u32_u24_e32 v216, 0xe39, v215\n" \
"v_lshrrev_b32_e64 v216, 16, v216\n" \
M_"v[168:171], a[220:223], a[244:247], v[168:171]\n" \
"v_mad_i32_i24 v215, v216, s6, v215\n" \
"v_lshlrev_b32_e64 v217, 4, v215\n" \
M_"v[164:167], a[156:159], a[176:179], v[164:167]\n" \
"v_add_u32_e64 v220, s5, v217\n" \
"v_and_b32_e32 v220, 0xf0, v220\n" \
M_"v[164:167], a[220:223], a[240:243], v[164:167]\n" \
R_"a[156:159], v28 offset:6144\n" \
R_"a[220:223], v29 offset:6144\n" \
"s_waitcnt lgkmcnt(15)\n" \
M_"a[0:3], a[128:131], a[160:163], a[0:3]\n" \
"s_addk_i32 s84, 0x7c00\n" \
M_"a[0:3], a[192:195], a[224:227], a[0:3]\n" \
B_"v27, s[72:75], s84 offen offset:2048 lds\n" \
M_"a[4:7], a[128:131], a[164:167], a[4:7]\n" \
X_"a[176:177], v36 offset:0\n" \
"v_cmp_gt_u32_e32 vcc, 16, v215\n" \
"s_waitcnt lgkmcnt(15)\n" \
M_"a[4:7], a[192:195], a[228:231], a[4:7]\n" \
"s_addk_i32 s84, 0x7c00\n" \
"s_waitcnt lgkmcnt(11)\n" \
M_"a[8:11], a[128:131], a[168:171], a[8:11]\n" \
B_"v27, s[72:75], s84 offen offset:3072 lds\n" \
"s_waitcnt lgkmcnt(9)\n" \
M_"a[8:11], a[192:195], a[232:235], a[8:11]\n" \
X_"a[178:179], v36 offset:1024\n" \
"v_cndmask_b32_e32 v215, v217, v220, vcc\n" \
"s_waitcnt lgkmcnt(8)\n" \
M_"a[12:15], a[128:131], a[172:175], a[12:15]\n" \
X_"a[240:241], v36 offset:8192\n" \
"v_or_b32_e32 v217, 0x80, v212\n" \
"s_waitcnt lgkmcnt(5)\n" \
M_"a[12:15], a[192:195], a[236:239], a[12:15]\n" \
"v_mul_u32_u24_e32 v220, 0xe39, v217\n" \
"v_lshrrev_b32_e64 v220, 16, v220\n" \
M_"a[28:31], a[132:135], a[172:175], a[28:31]\n" \
"v_mad_i32_i24 v217, v220, s6, v217\n" \
"v_lshlrev_b32_e64 v221, 4, v217\n" \
M_"a[28:31], a[196:199], a[236:239], a[28:31]\n" \
X_"a[242:243], v36 offset:9216\n" \
"v_add_u32_e64 v222, s5, v221\n" \
M_"a[24:27], a[132:135], a[168:171], a[24:27]\n" \
B_"v[200:203], v192, s[76:79], s34 offen offset:0\n" \
M_"a[24:27], a[196:199], a[232:235], a[24:27]\n" \
"v_and_b32_e32 v222, 0xf0, v222\n" \
"v_cmp_gt_u32_e64 vcc, 16, v217\n" \
M_"a[20:23], a[132:135], a[164:167], a[20:23]\n" \
"s_nop 0\n" \
"v_cndmask_b32_e32 v217, v221, v222, vcc\n" \
"v_or_b32_e32 v221, 0xc0, v212\n" \
M_"a[20:23], a[196:199], a[228:231], a[20:23]\n" \
X_"a[180:181], v37 offset:0\n" \
"v_mul_u32_u24_e32 v222, 0xe39, v221\n" \
M_"a[16:19], a[132:135], a[160:163], a[16:19]\n" \
"v_lshrrev_b32_e64 v222, 16, v222\n" \
"v_mad_i32_i24 v221, v222, s6, v221\n" \
M_"a[16:19], a[196:199], a[224:227], a[16:19]\n" \
"v_lshlrev_b32_e32 v223, 4, v221\n" \
"v_add_u32_e32 v224, s5, v223\n" \
M_"a[32:35], a[136:139], a[160:163], a[32:35]\n" \
B_"v[204:207], v193, s[76:79], s34 offen offset:0\n" \
M_"a[32:35], a[200:203], a[224:227], a[32:35]\n" \
"s_mov_b32 m0, s32\n" \
X_"a[182:183], v37 offset:1024\n" \
M_"a[36:39], a[136:139], a[164:167], a[36:39]\n" \
"s_mov_b32 exec_lo, 0x0\n" \
"s_mov_b32 exec_hi, 0xff00\n" \
"global_load_lds_dword v[184:185], off\n" \
"s_mov_b64 exec, -1\n" \
M_"a[36:39], a[200:203], a[228:231], a[36:39]\n" \
X_"a[244:245], v37 offset:8192\n" \
"v_and_b32_e32 v224, 0xf0, v224\n" \
M_"a[40:43], a[136:139], a[168:171], a[40:43]\n" \
"v_cmp_gt_u32_e32 vcc, 16, v221\n" \
"s_nop 1\n" \
"v_cndmask_b32_e32 v221, v223, v224, vcc\n" \
M_"a[40:43], a[200:203], a[232:235], a[40:43]\n" \
"buffer_load_dword v208, v194, s[76:79], s34 offen offset:32\n" \
M_"a[44:47], a[136:139], a[172:175], a[44:47]\n" \
X_"a[246:247], v37 offset:9216\n" \
"v_or_b32_e32 v223, 0x100, v212\n" \
M_"a[44:47], a[200:203], a[236:239], a[44:47]\n" \
"v_mul_u32_u24_e32 v224, 0xe39, v223\n" \
"v_lshrrev_b32_e64 v224, 16, v224\n" \
M_"a[60:63], a[140:143], a[172:175], a[60:63]\n" \
"v_mad_i32_i24 v223, v224, s6, v223\n" \
"v_lshlrev_b32_e64 v225, 4, v223\n" \
M_"a[60:63], a[204:207], a[236:239], a[60:63]\n" \
"v_add_u32_e64 v226, s5, v225\n" \
"v_and_b32_e32 v226, 0xf0, v226\n" \
M_"a[56:59], a[140:143], a[168:171], a[56:59]\n" \
"buffer_load_dword v209, v194, s[76:79], s34 offen offset:1184\n" \
M_"a[56:59], a[204:207], a[232:235], a[56:59]\n" \
X_"a[184:185], v38 offset:0\n" \
"v_cmp_gt_u32_e64 vcc, 16, v223\n" \
M_"a[52:55], a[140:143], a[164:167], a[52:55]\n" \
"s_nop 0\n" \
"v_cndmask_b32_e32 v223, v225, v226, vcc\n" \
"v_or_b32_e32 v225, 0x140, v212\n" \
M_"a[52:55], a[204:207], a[228:231], a[52:55]\n" \
X_"a[186:187], v38 offset:1024\n" \
"v_mul_u32_u24_e32 v226, 0xe39, v225\n" \
M_"a[48:51], a[140:143], a[160:163], a[48:51]\n" \
"v_lshrrev_b32_e64 v226, 16, v226\n" \
"v_mad_i32_i24 v225, v226, s6, v225\n" \
M_"a[48:51], a[204:207], a[224:227], a[48:51]\n" \
"v_lshlrev_b32_e32 v227, 4, v225\n" \
"v_add_u32_e32 v228, s5, v227\n" \
M_"a[64:67], a[144:147], a[160:163], a[64:67]\n" \
"buffer_load_dword v211, v194, s[76:79], s34 offen offset:2336\n" \
M_"a[64:67], a[208:211], a[224:227], a[64:67]\n" \
X_"a[248:249], v38 offset:8192\n" \
"v_and_b32_e32 v228, 0xf0, v228\n" \
M_"a[68:71], a[144:147], a[164:167], a[68:71]\n" \
"v_cmp_gt_u32_e32 vcc, 16, v225\n" \
"s_nop 1\n" \
"v_cndmask_b32_e64 v225, v227, v228, vcc\n" \
M_"a[68:71], a[208:211], a[228:231], a[68:71]\n" \
X_"a[250:251], v38 offset:9216\n" \
"v_or_b32_e32 v227, 0x180, v212\n" \
M_"a[72:75], a[144:147], a[168:171], a[72:75]\n" \
"v_mul_u32_u24_e32 v228, 0xe39, v227\n" \
"v_lshrrev_b32_e64 v228, 16, v228\n" \
M_"a[72:75], a[208:211], a[232:235], a[72:75]\n" \
"s_mov_b32 s76, s20\n" \
"s_and_b32 s77, s21, 0xffff\n" \
M_"a[76:79], a[144:147], a[172:175], a[76:79]\n" \
"s_mov_b32 s78, 0x480\n" \
"v_mad_i32_i24 v227, v228, s6, v227\n" \
M_"a[76:79], a[208:211], a[236:239], a[76:79]\n" \
"buffer_load_ushort v210, v195, s[76:79], 0 offen\n" \
M_"a[92:95], a[148:151], a[172:175], a[92:95]\n" \
X_"a[188:189], v39 offset:0\n" \
"v_lshlrev_b32_e32 v229, 4, v227\n" \
M_"a[92:95], a[212:215], a[236:239], a[92:95]\n" \
"v_add_u32_e64 v230, s5, v229\n" \
"v_and_b32_e32 v230, 0xf0, v230\n" \
M_"a[88:91], a[148:151], a[168:171], a[88:91]\n" \
"s_mov_b32 s76, s8\n" \
"s_and_b32 s77, s9, 0xffff\n" \
M_"a[88:91], a[212:215], a[232:235], a[88:91]\n" \
"s_mov_b32 s78, 0x240000\n" \
X_"a[190:191], v39 offset:1024\n" \
M_"a[84:87], a[148:151], a[164:167], a[84:87]\n" \
"v_cmp_gt_u32_e32 vcc, 16, v227\n" \
"s_nop 1\n" \
"v_cndmask_b32_e32 v227, v229, v230, vcc\n" \
M_"a[84:87], a[212:215], a[228:231], a[84:87]\n" \
X_"a[252:253], v39 offset:8192\n" \
"v_or_b32_e32 v229, 0x1c0, v212\n" \
".fill (L_pb + 48864 - .) / 4, 4, 0xBF800000\n" \
"L_pp12:\n" \
"s_cbranch_execz L_pret\n" \
M_"a[80:83], a[148:151], a[160:163], a[80:83]\n" \
"v_mul_u32_u24_e32 v230, 0xe39, v229\n" \
"v_lshrrev_b32_e32 v230, 16, v230\n" \
M_"a[80:83], a[212:215], a[224:227], a[80:83]\n" \
"v_mad_i32_i24 v229, v230, s6, v229\n" \
"v_lshlrev_b32_e64 v231, 4, v229\n" \
M_"a[96:99], a[152:155], a[160:163], a[96:99]\n" \
X_"a[254:255], v39 offset:9216\n" \
"v_add_u32_e64 v232, s5, v231\n" \
M_"a[96:99], a[216:219], a[224:227], a[96:99]\n" \
"v_and_b32_e32 v232, 0xf0, v232\n" \
"v_cmp_gt_u32_e64 vcc, 16, v229\n" \
M_"a[100:103], a[152:155], a[164:167], a[100:103]\n" \
"s_nop 0\n" \
"v_cndmask_b32_e32 v229, v231, v232, vcc\n" \
"v_or_b32_e32 v231, 0x200, v212\n" \
M_"a[100:103], a[216:219], a[228:231], a[100:103]\n" \
"v_mul_u32_u24_e32 v232, 0xe39, v231\n" \
"v_lshrrev_b32_e64 v232, 16, v232\n" \
M_"a[104:107], a[152:155], a[168:171], a[104:107]\n" \
"v_mad_i32_i24 v231, v232, s6, v231\n" \
"v_lshlrev_b32_e64 v233, 4, v231\n" \
M_"a[104:107], a[216:219], a[232:235], a[104:107]\n" \
"v_add_u32_e32 v234, s5, v233\n" \
"s_movk_i32 s5, 0x480\n" \
M_"a[108:111], a[152:155], a[172:175], a[108:111]\n" \
"v_mad_u32_u24 v0, v213, s5, v214\n" \
"v_mul_u32_u24_e32 v213, 0x480, v216\n" \
M_"a[108:111], a[216:219], a[236:239], a[108:111]\n" \
"s_movk_i32 s6, 0xfc00\n" \
"v_add3_u32 v1, v213, v215, s6\n" \
"s_waitcnt lgkmcnt(15)\n" \
M_"a[124:127], a[156:159], a[172:175], a[124:127]\n" \
"v_mul_u32_u24_e32 v213, 0x480, v220\n" \
"v_add3_u32 v2, v213, v217, s7\n" \
M_"a[124:127], a[220:223], a[236:239], a[124:127]\n" \
"v_mul_u32_u24_e32 v213, 0x480, v222\n" \
"v_add3_u32 v3, v213, v221, s40\n" \
M_"a[120:123], a[156:159], a[168:171], a[120:123]\n" \
"v_mul_u32_u24_e32 v213, 0x480, v226\n" \
"v_add3_u32 v5, v213, v225, s6\n" \
M_"a[120:123], a[220:223], a[232:235], a[120:123]\n" \
"v_mul_u32_u24_e32 v213, 0x480, v228\n" \
"v_add3_u32 v6, v213, v227, s7\n" \
M_"a[116:119], a[156:159], a[164:167], a[116:119]\n" \
"v_mul_u32_u24_e32 v213, 0x480, v230\n" \
"v_add3_u32 v7, v213, v229, s40\n" \
M_"a[116:119], a[220:223], a[228:231], a[116:119]\n" \
"s_lshl_b32 s7, s3, 3\n" \
"s_lshl_b32 s40, s3, 1\n" \
M_"a[112:115], a[156:159], a[160:163], a[112:115]\n" \
"s_and_b32 s7, s7, 16\n" \
"s_and_b32 s40, s40, 2\n" \
"s_waitcnt vmcnt(7) lgkmcnt(0)\n" \
M_"a[112:115], a[220:223], a[224:227], a[112:115]\n" \
"s_barrier\n" \
M_"v[52:55], a[128:131], a[176:179], v[52:55]\n" \
"s_add_u32 m0, s30, 0x0\n" \
"s_movk_i32 s84, 0x200\n" \
M_"v[52:55], a[192:195], a[240:243], v[52:55]\n" \
B_"v22, s[64:67], s84 offen offset:0 lds\n" \
M_"v[56:59], a[128:131], a[180:183], v[56:59]\n" \
X_"a[160:161], v32 offset:32768\n" \
"s_and_b32 s6, s2, 7\n" \
M_"v[56:59], a[192:195], a[244:247], v[56:59]\n" \
"s_movk_i32 s84, 0x1e00\n" \
M_"v[60:63], a[128:131], a[184:187], v[60:63]\n" \
B_"v23, s[64:67], s84 offen offset:1024 lds\n" \
M_"v[60:63], a[192:195], a[248:251], v[60:63]\n" \
X_"a[162:163], v32 offset:33792\n" \
"v_bfe_u32 v213, v20, 7, 1\n" \
M_"v[64:67], a[128:131], a[188:191], v[64:67]\n" \
X_"a[224:225], v32 offset:40960\n" \
"s_or_b32 s7, s40, s7\n" \
M_"v[64:67], a[192:195], a[252:255], v[64:67]\n" \
R_"a[128:131], v28 offset:49152\n" \
"v_and_b32_e32 v234, 0xf0, v234\n" \
M_"v[80:83], a[132:135], a[188:191], v[80:83]\n" \
R_"a[192:195], v29 offset:49152\n" \
"v_cmp_gt_u32_e32 vcc, 16, v231\n" \
M_"v[80:83], a[196:199], a[252:255], v[80:83]\n" \
"s_movk_i32 s84, 0x3a00\n" \
M_"v[76:79], a[132:135], a[184:187], v[76:79]\n" \
B_"v22, s[64:67], s84 offen offset:2048 lds\n" \
M_"v[76:79], a[196:199], a[248:251], v[76:79]\n" \
"s_movk_i32 s84, 0x5600\n" \
M_"v[72:75], a[132:135], a[180:183], v[72:75]\n" \
B_"v23, s[64:67], s84 offen offset:3072 lds\n" \
M_"v[72:75], a[196:199], a[244:247], v[72:75]\n" \
"s_add_u32 m0, s31, 0x0\n" \
"s_mov_b32 s84, 0x200000\n" \
M_"v[68:71], a[132:135], a[176:179], v[68:71]\n" \
B_"v26, s[68:71], s84 offen offset:0 lds\n" \
M_"v[68:71], a[196:199], a[240:243], v[68:71]\n" \
R_"a[132:135], v28 offset:51200\n" \
"v_or3_b32 v213, s7, v213, v219\n" \
M_"v[84:87], a[136:139], a[176:179], v[84:87]\n" \
R_"a[196:199], v29 offset:51200\n" \
"v_and_b32_e32 v214, 32, v20\n" \
M_"v[84:87], a[200:203], a[240:243], v[84:87]\n" \
"s_addk_i32 s84, 0x7c00\n" \
M_"v[88:91], a[136:139], a[180:183], v[88:91]\n" \
B_"v26, s[68:71], s84 offen offset:1024 lds\n" \
M_"v[88:91], a[200:203], a[244:247], v[88:91]\n" \
X_"a[226:227], v32 offset:41984\n" \
"s_lshl_b32 s6, s6, 5\n" \
M_"v[92:95], a[136:139], a[184:187], v[92:95]\n" \
"s_addk_i32 s84, 0x7c00\n" \
M_"v[92:95], a[200:203], a[248:251], v[92:95]\n" \
B_"v27, s[68:71], s84 offen offset:2048 lds\n" \
M_"v[96:99], a[136:139], a[188:191], v[96:99]\n" \
X_"a[164:165], v33 offset:32768\n" \
"v_cndmask_b32_e32 v231, v233, v234, vcc\n" \
M_"v[96:99], a[200:203], a[252:255], v[96:99]\n" \
R_"a[136:139], v28 offset:53248\n" \
"v_mul_u32_u24_e64 v213, 18, v213\n" \
M_"v[112:115], a[140:143], a[188:191], v[112:115]\n" \
R_"a[200:203], v29 offset:53248\n" \
"v_mov_b32_e32 v215, 0x100\n" \
M_"v[112:115], a[204:207], a[252:255], v[112:115]\n" \
"s_addk_i32 s84, 0x7c00\n" \
M_"v[108:111], a[140:143], a[184:187], v[108:111]\n" \
B_"v27, s[68:71], s84 offen offset:3072 lds\n" \
M_"v[108:111], a[204:207], a[248:251], v[108:111]\n" \
"s_add_u32 m0, s30, 0x1000\n" \
"s_mov_b32 s84, 0x8200\n" \
M_"v[104:107], a[140:143], a[180:183], v[104:107]\n" \
B_"v22, s[64:67], s84 offen offset:0 lds\n" \
M_"v[104:107], a[204:207], a[244:247], v[104:107]\n" \
X_"a[166:167], v33 offset:33792\n" \
"v_mov_b32_e32 v216, s6\n" \
M_"v[100:103], a[140:143], a[176:179], v[100:103]\n" \
X_"a[228:229], v33 offset:40960\n" \
"v_cmp_eq_u32_e64 vcc, 0, v214\n" \
M_"v[100:103], a[204:207], a[240:243], v[100:103]\n" \
R_"a[140:143], v28 offset:55296\n" \
"v_and_or_b32 v213, v218, 1, v213\n" \
M_"v[116:119], a[144:147], a[176:179], v[116:119]\n" \
R_"a[204:207], v29 offset:55296\n" \
"v_lshlrev_b32_e64 v212, 3, v212\n" \
M_"v[116:119], a[208:211], a[240:243], v[116:119]\n" \
"s_addk_i32 s84, 0x1c00\n" \
M_"v[120:123], a[144:147], a[180:183], v[120:123]\n" \
B_"v23, s[64:67], s84 offen offset:1024 lds\n" \
M_"v[120:123], a[208:211], a[244:247], v[120:123]\n" \
"s_addk_i32 s84, 0x1c00\n" \
M_"v[124:127], a[144:147], a[184:187], v[124:127]\n" \
B_"v22, s[64:67], s84 offen offset:2048 lds\n" \
M_"v[124:127], a[208:211], a[248:251], v[124:127]\n" \
"s_addk_i32 s84, 0x1c00\n" \
M_"v[128:131], a[144:147], a[188:191], v[128:131]\n" \
B_"v23, s[64:67], s84 offen offset:3072 lds\n" \
M_"v[128:131], a[208:211], a[252:255], v[128:131]\n" \
R_"a[144:147], v28 offset:32768\n" \
"v_cndmask_b32_e32 v214, v215, v216, vcc\n" \
M_"v[144:147], a[148:151], a[188:191], v[144:147]\n" \
R_"a[208:211], v29 offset:32768\n" \
"v_mad_u32_u24 v213, v213, s5, v214\n" \
M_"v[144:147], a[212:215], a[252:255], v[144:147]\n" \
"s_add_u32 m0, s31, 0x4000\n" \
"s_mov_b32 s84, 0x200000\n" \
M_"v[140:143], a[148:151], a[184:187], v[140:143]\n" \
B_"v26, s[72:75], s84 offen offset:0 lds\n" \
M_"v[140:143], a[212:215], a[248:251], v[140:143]\n" \
X_"a[230:231], v33 offset:41984\n" \
"v_and_or_b32 v213, v20, 16, v213\n" \
M_"v[136:139], a[148:151], a[180:183], v[136:139]\n" \
"s_addk_i32 s84, 0x7c00\n" \
"s_nop 0\n" \
M_"v[136:139], a[212:215], a[244:247], v[136:139]\n" \
B_"v26, s[72:75], s84 offen offset:1024 lds\n" \
M_"v[132:135], a[148:151], a[176:179], v[132:135]\n" \
X_"a[168:169], v34 offset:32768\n" \
"v_add_u32_e32 v9, 0x4800, v213\n" \
M_"v[132:135], a[212:215], a[240:243], v[132:135]\n" \
R_"a[148:151], v28 offset:34816\n" \
"v_mul_u32_u24_e32 v213, 0x120, v218\n" \
M_"v[148:151], a[152:155], a[176:179], v[148:151]\n" \
R_"a[212:215], v29 offset:34816\n" \
"v_and_or_b32 v10, v212, 24, v213\n" \
M_"v[148:151], a[216:219], a[240:243], v[148:151]\n" \
X_"a[170:171], v34 offset:33792\n" \
"v_lshrrev_b32_e64 v213, 2, v20\n" \
M_"v[152:155], a[152:155], a[180:183], v[152:155]\n" \
X_"a[232:233], v34 offset:40960\n" \
"v_and_or_b32 v213, v213, 60, s4\n" \
M_"v[152:155], a[216:219], a[244:247], v[152:155]\n" \
X_"a[234:235], v34 offset:41984\n" \
"v_lshl_or_b32 v212, s3, 9, v212\n" \
M_"v[156:159], a[152:155], a[184:187], v[156:159]\n" \
X_"a[172:173], v35 offset:32768\n" \
"v_mad_u32_u24 v4, v224, s5, v223\n" \
M_"v[156:159], a[216:219], a[248:251], v[156:159]\n" \
X_"a[174:175], v35 offset:33792\n" \
"v_mad_u32_u24 v8, v232, s5, v231\n" \
M_"v[160:163], a[152:155], a[188:191], v[160:163]\n" \
X_"a[236:237], v35 offset:40960\n" \
"v_or_b32_e32 v18, 0x27000, v213\n" \
M_"v[160:163], a[216:219], a[252:255], v[160:163]\n" \
R_"a[152:155], v28 offset:36864\n" \
"v_or_b32_e32 v19, 0x27800, v212\n" \
M_"v[176:179], a[156:159], a[188:191], v[176:179]\n" \
R_"a[216:219], v29 offset:36864\n" \
"v_mov_b32_e32 v215, 0x1000c0c\n" \
M_"v[176:179], a[220:223], a[252:255], v[176:179]\n" \
X_"a[238:239], v35 offset:41984\n" \
"v_mov_b32_e32 v216, 0xc0c0100\n" \
M_"v[172:175], a[156:159], a[184:187], v[172:175]\n" \
"v_bfe_u32 v214, v20, 4, 1\n" \
"v_lshrrev_b32_e64 v212, 8, v20\n" \
M_"v[172:175], a[220:223], a[248:251], v[172:175]\n" \
"v_cmp_eq_u32_e64 vcc, 0, v214\n" \
"v_bfe_u32 v213, v20, 4, 4\n" \
M_"v[168:171], a[156:159], a[180:183], v[168:171]\n" \
"v_mov_b32_e32 v217, 0xc0c0302\n" \
"v_cndmask_b32_e64 v215, v215, v216, vcc\n" \
M_"v[168:171], a[220:223], a[244:247], v[168:171]\n" \
"v_mov_b32_e32 v216, 0x3020c0c\n" \
"v_lshl_or_b32 v214, v212, 2, v214\n" \
M_"v[164:167], a[156:159], a[176:179], v[164:167]\n" \
"v_cndmask_b32_e64 v216, v216, v217, vcc\n" \
"v_mov_b32_e32 v217, 0xc0c0c0c\n" \
M_"v[164:167], a[220:223], a[240:243], v[164:167]\n" \
R_"a[156:159], v28 offset:38912\n" \
R_"a[220:223], v29 offset:38912\n" \
"s_waitcnt lgkmcnt(15)\n" \
M_"a[0:3], a[128:131], a[160:163], a[0:3]\n" \
"s_addk_i32 s84, 0x7c00\n" \
M_"a[0:3], a[192:195], a[224:227], a[0:3]\n" \
B_"v27, s[72:75], s84 offen offset:2048 lds\n" \
M_"a[4:7], a[128:131], a[164:167], a[4:7]\n" \
X_"a[176:177], v36 offset:32768\n" \
"v_cmp_gt_u32_e32 vcc, v214, v213\n" \
"s_waitcnt lgkmcnt(15)\n" \
M_"a[4:7], a[192:195], a[228:231], a[4:7]\n" \
"s_addk_i32 s84, 0x7c00\n" \
"s_waitcnt lgkmcnt(11)\n" \
M_"a[8:11], a[128:131], a[168:171], a[8:11]\n" \
B_"v27, s[72:75], s84 offen offset:3072 lds\n" \
"s_waitcnt lgkmcnt(9)\n" \
M_"a[8:11], a[192:195], a[232:235], a[8:11]\n" \
X_"a[178:179], v36 offset:33792\n" \
"v_or_b32_e32 v214, 2, v214\n" \
"s_waitcnt lgkmcnt(8)\n" \
M_"a[12:15], a[128:131], a[172:175], a[12:15]\n" \
X_"a[240:241], v36 offset:40960\n" \
"s_lshr_b32 s4, s2, 5\n" \
"s_waitcnt lgkmcnt(5)\n" \
M_"a[12:15], a[192:195], a[236:239], a[12:15]\n" \
"v_cndmask_b32_e32 v40, v217, v215, vcc\n" \
"v_cndmask_b32_e64 v42, v215, v217, vcc\n" \
M_"a[28:31], a[132:135], a[172:175], a[28:31]\n" \
"v_cmp_gt_u32_e32 vcc, v214, v213\n" \
"v_and_b32_e32 v214, 16, v20\n" \
M_"a[28:31], a[196:199], a[236:239], a[28:31]\n" \
X_"a[242:243], v36 offset:41984\n" \
"s_movk_i32 s42, 0x3f80\n" \
M_"a[24:27], a[132:135], a[168:171], a[24:27]\n" \
"s_xor_b32 s5, s4, 15\n" \
M_"a[24:27], a[196:199], a[232:235], a[24:27]\n" \
"s_cmpk_lt_u32 s3, 2\n" \
"s_cselect_b32 s6, s5, s4\n" \
M_"a[20:23], a[132:135], a[164:167], a[20:23]\n" \
"s_lshl_b32 s7, s6, 7\n" \
"v_and_b32_e32 v213, 0x320, v20\n" \
M_"a[20:23], a[196:199], a[228:231], a[20:23]\n" \
X_"a[180:181], v37 offset:32768\n" \
"s_movk_i32 s41, 0x100\n" \
M_"a[16:19], a[132:135], a[160:163], a[16:19]\n" \
"v_lshlrev_b32_e64 v214, v214, s42\n" \
"v_mov_b32_e64 v215, 0\n" \
M_"a[16:19], a[196:199], a[224:227], a[16:19]\n" \
"s_add_i32 s40, s7, -16\n" \
"v_cndmask_b32_e32 v41, v217, v216, vcc\n" \
M_"a[32:35], a[136:139], a[160:163], a[32:35]\n" \
X_"a[182:183], v37 offset:33792\n" \
"v_cndmask_b32_e64 v43, v216, v217, vcc\n" \
M_"a[32:35], a[200:203], a[224:227], a[32:35]\n" \
"v_cmp_eq_u32_e32 vcc, s41, v213\n" \
"s_mov_b32 s41, 0\n" \
M_"a[36:39], a[136:139], a[164:167], a[36:39]\n" \
"s_and_b32 s43, s3, 1\n" \
"v_cndmask_b32_e32 v46, v215, v214, vcc\n" \
M_"a[36:39], a[200:203], a[228:231], a[36:39]\n" \
X_"a[244:245], v37 offset:40960\n" \
"v_and_b32_e32 v213, 0x330, v20\n" \
M_"a[40:43], a[136:139], a[168:171], a[40:43]\n" \
"s_mov_b32 m0, s32\n" \
M_"a[40:43], a[200:203], a[232:235], a[40:43]\n" \
"s_mov_b32 exec_lo, 0xffffffff\n" \
"s_mov_b32 exec_hi, 0x0\n" \
"global_load_lds_dword v[188:189], off\n" \
"s_mov_b64 exec, -1\n" \
M_"a[44:47], a[136:139], a[172:175], a[44:47]\n" \
X_"a[246:247], v37 offset:41984\n" \
M_"a[44:47], a[200:203], a[236:239], a[44:47]\n" \
"s_cmp_lg_u32 s7, 0\n" \
"s_cselect_b32 s40, s40, s41\n" \
M_"a[60:63], a[140:143], a[172:175], a[60:63]\n" \
"s_mov_b32 s42, 0xff800000\n" \
M_"a[60:63], a[204:207], a[236:239], a[60:63]\n" \
"s_cmp_lg_u32 s7, 0\n" \
"s_cselect_b32 s47, s41, s42\n" \
M_"a[56:59], a[140:143], a[168:171], a[56:59]\n" \
X_"a[184:185], v38 offset:32768\n" \
"s_lshl_b32 s41, s43, 6\n" \
M_"a[56:59], a[204:207], a[232:235], a[56:59]\n" \
"v_lshrrev_b32_e32 v215, 6, v20\n" \
"v_mov_b32_e32 v214, 0x3f80\n" \
M_"a[52:55], a[140:143], a[164:167], a[52:55]\n" \
"v_cmp_eq_u32_e32 vcc, 32, v213\n" \
"s_or_b32 s7, s41, s7\n" \
M_"a[52:55], a[204:207], a[228:231], a[52:55]\n" \
X_"a[186:187], v38 offset:33792\n" \
"v_lshrrev_b32_e64 v213, 7, v20\n" \
M_"a[48:51], a[140:143], a[160:163], a[48:51]\n" \
"v_bfe_u32 v216, v20, 6, 2\n" \
"v_lshrrev_b32_e64 v219, 1, v20\n" \
M_"a[48:51], a[204:207], a[224:227], a[48:51]\n" \
"v_bfe_u32 v215, v215, 1, 1\n" \
"v_cndmask_b32_e64 v47, 0, v214, vcc\n" \
M_"a[64:67], a[144:147], a[160:163], a[64:67]\n" \
X_"a[248:249], v38 offset:40960\n" \
"v_or_b32_e64 v214, s7, v213\n" \
M_"a[64:67], a[208:211], a[224:227], a[64:67]\n" \
"v_lshl_or_b32 v217, s43, 3, v213\n" \
"v_lshlrev_b32_e64 v218, 10, v212\n" \
M_"a[68:71], a[144:147], a[164:167], a[68:71]\n" \
"v_lshlrev_b32_e32 v216, 7, v216\n" \
"v_and_b32_e32 v219, 24, v219\n" \
M_"a[68:71], a[208:211], a[228:231], a[68:71]\n" \
X_"a[250:251], v38 offset:41984\n" \
"v_and_or_b32 v213, v213, 2, v215\n" \
M_"a[72:75], a[144:147], a[168:171], a[72:75]\n" \
"v_add_u32_e64 v217, s40, v217\n" \
"v_or3_b32 v216, v216, v218, v219\n" \
M_"a[72:75], a[208:211], a[232:235], a[72:75]\n" \
"v_lshlrev_b32_e64 v213, 5, v213\n" \
"s_mov_b32 s40, 0x14000\n" \
M_"a[76:79], a[144:147], a[172:175], a[76:79]\n" \
"s_lshl_b32 s7, s2, 2\n" \
"v_or3_b32 v48, v216, v213, s40\n" \
M_"a[76:79], a[208:211], a[236:239], a[76:79]\n" \
X_"a[188:189], v39 offset:32768\n" \
"s_lshl_b32 s40, s43, 2\n" \
M_"a[92:95], a[148:151], a[172:175], a[92:95]\n" \
"s_lshl_b32 s6, s6, 3\n" \
"v_or_b32_e32 v213, s40, v212\n" \
M_"a[92:95], a[212:215], a[236:239], a[92:95]\n" \
"s_or_b32 s6, s6, s40\n" \
"s_and_b32 s40, s7, 0x7c\n" \
M_"a[88:91], a[148:151], a[168:171], a[88:91]\n" \
X_"a[190:191], v39 offset:33792\n" \
"s_or_b32 s40, s40, s3\n" \
M_"a[88:91], a[212:215], a[232:235], a[88:91]\n" \
"s_cmpk_lt_u32 s40, 64\n" \
"s_cselect_b32 s41, s4, s5\n" \
M_"a[84:87], a[148:151], a[164:167], a[84:87]\n" \
"s_waitcnt vmcnt(17)\n" \
"v_or_b32_e32 v204, v210, v204\n" \
"v_or_b32_e64 v206, v210, v206\n" \
M_"a[84:87], a[212:215], a[228:231], a[84:87]\n" \
"v_mov_b32_e64 v210, 0\n" \
"s_add_u32 s88, s35, 0x1000\n" \
M_"a[80:83], a[148:151], a[160:163], a[80:83]\n" \
X_"a[252:253], v39 offset:40960\n" \
"s_lshl_b32 s40, s40, 1\n" \
M_"a[80:83], a[212:215], a[224:227], a[80:83]\n" \
"ds_write_b128 v196, v[200:203]\n" \
M_"a[96:99], a[152:155], a[160:163], a[96:99]\n" \
X_"a[254:255], v39 offset:41984\n" \
"s_lshl_b32 s41, s41, 7\n" \
M_"a[96:99], a[216:219], a[224:227], a[96:99]\n" \
"s_and_b32 s40, s40, 0x7e\n" \
"s_or_b32 s40, s41, s40\n" \
M_"a[100:103], a[152:155], a[164:167], a[100:103]\n" \
"ds_write_b128 v197, v[204:207]\n" \
M_"a[100:103], a[216:219], a[228:231], a[100:103]\n" \
"s_mulk_i32 s40, 0x480\n" \
"v_add_u32_e64 v49, s40, v20\n" \
M_"a[104:107], a[152:155], a[168:171], a[104:107]\n" \
"s_mov_b32 s40, 0xc000\n" \
"s_and_b32 s7, s7, 28\n" \
M_"a[104:107], a[216:219], a[232:235], a[104:107]\n" \
"ds_write_b128 v198, v[208:211]\n" \
M_"a[108:111], a[152:155], a[172:175], a[108:111]\n" \
"s_bfe_u32 s45, s2, 0x20003\n" \
"s_mov_b32 s41, 0x8000\n" \
M_"a[108:111], a[216:219], a[236:239], a[108:111]\n" \
"s_cmp_lg_u32 s4, 0\n" \
"s_cselect_b32 s51, s40, s41\n" \
"s_waitcnt lgkmcnt(15)\n" \
M_"a[124:127], a[156:159], a[172:175], a[124:127]\n" \
"s_movk_i32 s40, 0x4000\n" \
"s_lshr_b32 s44, s3, 1\n" \
M_"a[124:127], a[220:223], a[236:239], a[124:127]\n" \
"s_or_b32 s46, s7, s45\n" \
M_"a[120:123], a[156:159], a[168:171], a[120:123]\n" \
"s_cmp_lg_u32 s4, 0\n" \
"s_cselect_b32 s53, s41, s40\n" \
M_"a[120:123], a[220:223], a[232:235], a[120:123]\n" \
"s_lshl_b32 s40, s44, 15\n" \
"s_lshl_b32 s41, s43, 14\n" \
M_"a[116:119], a[156:159], a[164:167], a[116:119]\n" \
"s_lshl_b32 s48, s46, 20\n" \
"s_or_b32 s41, s40, s41\n" \
M_"a[116:119], a[220:223], a[228:231], a[116:119]\n" \
"s_add_i32 s49, s48, 0x1000000\n" \
"s_or_b32 s42, s49, s41\n" \
M_"a[112:115], a[156:159], a[160:163], a[112:115]\n" \
"s_lshl_b32 s4, s4, 16\n" \
"s_or_b32 s41, s42, s4\n" \
"s_waitcnt vmcnt(1) lgkmcnt(0)\n" \
M_"a[112:115], a[220:223], a[224:227], a[112:115]\n" \
"s_barrier\n" \
M_"v[52:55], a[128:131], a[176:179], v[52:55]\n" \
"s_add_u32 m0, s30, 0x8000\n" \
"s_movk_i32 s84, 0x280\n" \
M_"v[52:55], a[192:195], a[240:243], v[52:55]\n" \
B_"v22, s[64:67], s84 offen offset:0 lds\n" \
M_"v[56:59], a[128:131], a[180:183], v[56:59]\n" \
"s_movk_i32 s84, 0x1e80\n" \
M_"v[56:59], a[192:195], a[244:247], v[56:59]\n" \
B_"v23, s[64:67], s84 offen offset:1024 lds\n" \
M_"v[60:63], a[128:131], a[184:187], v[60:63]\n" \
X_"a[160:161], v32 offset:0\n" \
"s_lshl_b32 s4, s5, 16\n" \
M_"v[60:63], a[192:195], a[248:251], v[60:63]\n" \
"s_movk_i32 s84, 0x3a80\n" \
M_"v[64:67], a[128:131], a[188:191], v[64:67]\n" \
B_"v22, s[64:67], s84 offen offset:2048 lds\n" \
M_"v[64:67], a[192:195], a[252:255], v[64:67]\n" \
R_"a[128:131], v28 offset:16384\n" \
"s_mul_i32 s5, s6, 0x24800\n" \
M_"v[80:83], a[132:135], a[188:191], v[80:83]\n" \
R_"a[192:195], v29 offset:16384\n" \
"s_lshl_b32 s6, s46, 9\n" \
M_"v[80:83], a[196:199], a[252:255], v[80:83]\n" \
"s_movk_i32 s84, 0x5680\n" \
M_"v[76:79], a[132:135], a[184:187], v[76:79]\n" \
B_"v23, s[64:67], s84 offen offset:3072 lds\n" \
M_"v[76:79], a[196:199], a[248:251], v[76:79]\n" \
"s_add_u32 m0, s31, 0x8000\n" \
"s_mov_b32 s84, 0x280000\n" \
M_"v[72:75], a[132:135], a[180:183], v[72:75]\n" \
B_"v26, s[68:71], s84 offen offset:0 lds\n" \
M_"v[72:75], a[196:199], a[244:247], v[72:75]\n" \
X_"a[162:163], v32 offset:1024\n" \
"s_lshl_b32 s7, s7, 6\n" \
M_"v[68:71], a[132:135], a[176:179], v[68:71]\n" \
X_"a[224:225], v32 offset:8192\n" \
"s_add_i32 s6, s5, s6\n" \
M_"v[68:71], a[196:199], a[240:243], v[68:71]\n" \
R_"a[132:135], v28 offset:18432\n" \
"s_or_b32 s5, s5, s7\n" \
M_"v[84:87], a[136:139], a[176:179], v[84:87]\n" \
R_"a[196:199], v29 offset:18432\n" \
"s_lshl_b32 s7, s45, 2\n" \
M_"v[84:87], a[200:203], a[240:243], v[84:87]\n" \
"s_addk_i32 s84, 0x7c00\n" \
M_"v[88:91], a[136:139], a[180:183], v[88:91]\n" \
B_"v26, s[68:71], s84 offen offset:1024 lds\n" \
M_"v[88:91], a[200:203], a[244:247], v[88:91]\n" \
"s_addk_i32 s84, 0x7c00\n" \
M_"v[92:95], a[136:139], a[184:187], v[92:95]\n" \
B_"v27, s[68:71], s84 offen offset:2048 lds\n" \
M_"v[92:95], a[200:203], a[248:251], v[92:95]\n" \
"s_addk_i32 s84, 0x7c00\n" \
M_"v[96:99], a[136:139], a[188:191], v[96:99]\n" \
B_"v27, s[68:71], s84 offen offset:3072 lds\n" \
M_"v[96:99], a[200:203], a[252:255], v[96:99]\n" \
R_"a[136:139], v28 offset:20480\n" \
"s_or_b32 s5, s5, s7\n" \
M_"v[112:115], a[140:143], a[188:191], v[112:115]\n" \
R_"a[200:203], v29 offset:20480\n" \
"s_lshl_b32 s7, s3, 12\n" \
M_"v[112:115], a[204:207], a[252:255], v[112:115]\n" \
"s_add_u32 m0, s30, 0x9000\n" \
"s_mov_b32 s84, 0x8280\n" \
M_"v[108:111], a[140:143], a[184:187], v[108:111]\n" \
B_"v22, s[64:67], s84 offen offset:0 lds\n" \
M_"v[108:111], a[204:207], a[248:251], v[108:111]\n" \
"s_addk_i32 s84, 0x1c00\n" \
M_"v[104:107], a[140:143], a[180:183], v[104:107]\n" \
B_"v23, s[64:67], s84 offen offset:1024 lds\n" \
M_"v[104:107], a[204:207], a[244:247], v[104:107]\n" \
"s_addk_i32 s84, 0x1c00\n" \
M_"v[100:103], a[140:143], a[176:179], v[100:103]\n" \
B_"v22, s[64:67], s84 offen offset:2048 lds\n" \
M_"v[100:103], a[204:207], a[240:243], v[100:103]\n" \
R_"a[140:143], v28 offset:22528\n" \
"s_or_b32 s50, s49, s7\n" \
M_"v[116:119], a[144:147], a[176:179], v[116:119]\n" \
R_"a[204:207], v29 offset:22528\n" \
"s_lshl_b32 s7, s3, 11\n" \
M_"v[116:119], a[208:211], a[240:243], v[116:119]\n" \
"s_addk_i32 s84, 0x1c00\n" \
M_"v[120:123], a[144:147], a[180:183], v[120:123]\n" \
B_"v23, s[64:67], s84 offen offset:3072 lds\n" \
M_"v[120:123], a[208:211], a[244:247], v[120:123]\n" \
"s_add_u32 m0, s31, 0xc000\n" \
"s_mov_b32 s84, 0x280000\n" \
M_"v[124:127], a[144:147], a[184:187], v[124:127]\n" \
B_"v26, s[72:75], s84 offen offset:0 lds\n" \
M_"v[124:127], a[208:211], a[248:251], v[124:127]\n" \
"s_addk_i32 s84, 0x7c00\n" \
M_"v[128:131], a[144:147], a[188:191], v[128:131]\n" \
B_"v26, s[72:75], s84 offen offset:1024 lds\n" \
M_"v[128:131], a[208:211], a[252:255], v[128:131]\n" \
R_"a[144:147], v28 offset:0\n" \
"v_lshrrev_b32_e32 v215, 4, v20\n" \
M_"v[144:147], a[148:151], a[188:191], v[144:147]\n" \
R_"a[208:211], v29 offset:0\n" \
"v_bfe_u32 v216, v20, 4, 3\n" \
M_"v[144:147], a[212:215], a[252:255], v[144:147]\n" \
R_"v[200:203], v199 offset:0\n" \
"s_or_b32 s42, s42, s4\n" \
M_"v[140:143], a[148:151], a[184:187], v[140:143]\n" \
R_"v[204:207], v199 offset:4096\n" \
"s_lshl_b32 s4, s43, 11\n" \
M_"v[140:143], a[212:215], a[248:251], v[140:143]\n" \
X_"a[226:227], v32 offset:9216\n" \
"s_add_i32 s52, s48, s7\n" \
M_"v[136:139], a[148:151], a[180:183], v[136:139]\n" \
X_"a[164:165], v33 offset:0\n" \
"s_add_i32 s53, s53, s7\n" \
M_"v[136:139], a[212:215], a[244:247], v[136:139]\n" \
R_"v[208:211], v199 offset:8192\n" \
"v_mul_u32_u24_e32 v214, 0x480, v214\n" \
M_"v[132:135], a[148:151], a[176:179], v[132:135]\n" \
X_"a[166:167], v33 offset:1024\n" \
"v_bitop3_b32 v218, v212, v215, 7 bitop3:0x78\n" \
M_"v[132:135], a[212:215], a[240:243], v[132:135]\n" \
R_"a[148:151], v28 offset:2048\n" \
"v_bitop3_b32 v212, v212, v216, 4 bitop3:0x36\n" \
M_"v[148:151], a[152:155], a[176:179], v[148:151]\n" \
R_"a[212:215], v29 offset:2048\n" \
"s_lshl_b32 s7, s3, 10\n" \
M_"v[148:151], a[216:219], a[240:243], v[148:151]\n" \
X_"a[228:229], v33 offset:8192\n" \
"s_or_b32 s4, s40, s4\n" \
M_"v[152:155], a[152:155], a[180:183], v[152:155]\n" \
X_"a[230:231], v33 offset:9216\n" \
"v_lshl_or_b32 v25, v212, 4, v214\n" \
M_"v[152:155], a[216:219], a[244:247], v[152:155]\n" \
X_"a[168:169], v34 offset:0\n" \
"v_mul_u32_u24_e32 v212, 0x480, v217\n" \
M_"v[156:159], a[152:155], a[184:187], v[156:159]\n" \
X_"a[170:171], v34 offset:1024\n" \
"v_bitop3_b32 v213, v215, v213, 7 bitop3:0x6c\n" \
M_"v[156:159], a[216:219], a[248:251], v[156:159]\n" \
X_"a[232:233], v34 offset:8192\n" \
"s_or_b32 s40, s7, 0x14000\n" \
M_"v[160:163], a[152:155], a[188:191], v[160:163]\n" \
X_"a[234:235], v34 offset:9216\n" \
"s_lshl_b32 s43, s44, 13\n" \
M_"v[160:163], a[216:219], a[252:255], v[160:163]\n" \
R_"a[152:155], v28 offset:4096\n" \
"s_mulk_i32 s44, 0xe800\n" \
M_"v[176:179], a[156:159], a[188:191], v[176:179]\n" \
R_"a[216:219], v29 offset:4096\n" \
"s_lshl_b32 s7, s3, 8\n" \
"s_nop 0\n" \
M_"v[176:179], a[220:223], a[252:255], v[176:179]\n" \
X_"a[172:173], v35 offset:0\n" \
"v_lshl_or_b32 v24, v218, 4, v214\n" \
M_"v[172:175], a[156:159], a[184:187], v[172:175]\n" \
X_"a[174:175], v35 offset:1024\n" \
"v_lshl_or_b32 v21, v213, 4, v212\n" \
M_"v[172:175], a[220:223], a[248:251], v[172:175]\n" \
X_"a[236:237], v35 offset:8192\n" \
"s_add_i32 s44, s44, 0x17000\n" \
M_"v[168:171], a[156:159], a[180:183], v[168:171]\n" \
X_"a[238:239], v35 offset:9216\n" \
"s_or_b32 s45, s7, 0x27000\n" \
M_"v[168:171], a[220:223], a[244:247], v[168:171]\n" \
"s_or_b32 s46, s4, 0x10000\n" \
"s_add_i32 s48, s6, 0x3000000\n" \
M_"v[164:167], a[156:159], a[176:179], v[164:167]\n" \
"s_add_i32 s49, s5, 0x3004000\n" \
"s_add_i32 s52, s52, 0x1010000\n" \
".fill (L_pb + 52960 - .) / 4, 4, 0xBF800000\n" \
"L_pp13:\n" \
"s_cbranch_execz L_pret\n" \
M_"v[164:167], a[220:223], a[240:243], v[164:167]\n" \
R_"a[156:159], v28 offset:6144\n" \
R_"a[220:223], v29 offset:6144\n" \
"s_waitcnt lgkmcnt(15)\n" \
M_"a[0:3], a[128:131], a[160:163], a[0:3]\n" \
"s_addk_i32 s84, 0x7c00\n" \
M_"a[0:3], a[192:195], a[224:227], a[0:3]\n" \
B_"v27, s[72:75], s84 offen offset:2048 lds\n" \
M_"a[4:7], a[128:131], a[164:167], a[4:7]\n" \
X_"a[176:177], v36 offset:0\n" \
"s_waitcnt lgkmcnt(13)\n" \
M_"a[4:7], a[192:195], a[228:231], a[4:7]\n" \
"s_addk_i32 s84, 0x7c00\n" \
"s_waitcnt lgkmcnt(11)\n" \
M_"a[8:11], a[128:131], a[168:171], a[8:11]\n" \
B_"v27, s[72:75], s84 offen offset:3072 lds\n" \
"s_waitcnt lgkmcnt(9)\n" \
M_"a[8:11], a[192:195], a[232:235], a[8:11]\n" \
X_"a[178:179], v36 offset:1024\n" \
"s_waitcnt lgkmcnt(6)\n" \
M_"a[12:15], a[128:131], a[172:175], a[12:15]\n" \
X_"a[240:241], v36 offset:8192\n" \
"s_waitcnt lgkmcnt(5)\n" \
"s_nop 0\n" \
M_"a[12:15], a[192:195], a[236:239], a[12:15]\n" \
M_"a[28:31], a[132:135], a[172:175], a[28:31]\n" \
M_"a[28:31], a[196:199], a[236:239], a[28:31]\n" \
X_"a[242:243], v36 offset:9216\n" \
M_"a[24:27], a[132:135], a[168:171], a[24:27]\n" \
M_"a[24:27], a[196:199], a[232:235], a[24:27]\n" \
M_"a[20:23], a[132:135], a[164:167], a[20:23]\n" \
M_"a[20:23], a[196:199], a[228:231], a[20:23]\n" \
X_"a[180:181], v37 offset:0\n" \
M_"a[16:19], a[132:135], a[160:163], a[16:19]\n" \
M_"a[16:19], a[196:199], a[224:227], a[16:19]\n" \
M_"a[32:35], a[136:139], a[160:163], a[32:35]\n" \
X_"a[182:183], v37 offset:1024\n" \
M_"a[32:35], a[200:203], a[224:227], a[32:35]\n" \
M_"a[36:39], a[136:139], a[164:167], a[36:39]\n" \
M_"a[36:39], a[200:203], a[228:231], a[36:39]\n" \
"buffer_store_dwordx4 v[200:203], v20, s[80:83], s35 offen sc1\n" \
M_"a[40:43], a[136:139], a[168:171], a[40:43]\n" \
"s_mov_b32 m0, s32\n" \
X_"a[244:245], v37 offset:8192\n" \
M_"a[40:43], a[200:203], a[232:235], a[40:43]\n" \
"s_mov_b32 exec_lo, 0x0\n" \
"s_mov_b32 exec_hi, 0xffffffff\n" \
"global_load_lds_dword v[188:189], off\n" \
"s_mov_b64 exec, -1\n" \
M_"a[44:47], a[136:139], a[172:175], a[44:47]\n" \
X_"a[246:247], v37 offset:9216\n" \
M_"a[44:47], a[200:203], a[236:239], a[44:47]\n" \
M_"a[60:63], a[140:143], a[172:175], a[60:63]\n" \
M_"a[60:63], a[204:207], a[236:239], a[60:63]\n" \
M_"a[56:59], a[140:143], a[168:171], a[56:59]\n" \
X_"a[184:185], v38 offset:0\n" \
M_"a[56:59], a[204:207], a[232:235], a[56:59]\n" \
M_"a[52:55], a[140:143], a[164:167], a[52:55]\n" \
"buffer_store_dwordx4 v[204:207], v20, s[80:83], s88 offen sc1\n" \
M_"a[52:55], a[204:207], a[228:231], a[52:55]\n" \
X_"a[186:187], v38 offset:1024\n" \
M_"a[48:51], a[140:143], a[160:163], a[48:51]\n" \
M_"a[48:51], a[204:207], a[224:227], a[48:51]\n" \
M_"a[64:67], a[144:147], a[160:163], a[64:67]\n" \
X_"a[248:249], v38 offset:8192\n" \
M_"a[64:67], a[208:211], a[224:227], a[64:67]\n" \
M_"a[68:71], a[144:147], a[164:167], a[68:71]\n" \
M_"a[68:71], a[208:211], a[228:231], a[68:71]\n" \
X_"a[250:251], v38 offset:9216\n" \
M_"a[72:75], a[144:147], a[168:171], a[72:75]\n" \
M_"a[72:75], a[208:211], a[232:235], a[72:75]\n" \
M_"a[76:79], a[144:147], a[172:175], a[76:79]\n" \
"buffer_store_dwordx4 v[208:211], v20, s[80:83], s36 offen sc1\n" \
M_"a[76:79], a[208:211], a[236:239], a[76:79]\n" \
X_"a[188:189], v39 offset:0\n" \
M_"a[92:95], a[148:151], a[172:175], a[92:95]\n" \
M_"a[92:95], a[212:215], a[236:239], a[92:95]\n" \
M_"a[88:91], a[148:151], a[168:171], a[88:91]\n" \
X_"a[190:191], v39 offset:1024\n" \
M_"a[88:91], a[212:215], a[232:235], a[88:91]\n" \
M_"a[84:87], a[148:151], a[164:167], a[84:87]\n" \
M_"a[84:87], a[212:215], a[228:231], a[84:87]\n" \
X_"a[252:253], v39 offset:8192\n" \
M_"a[80:83], a[148:151], a[160:163], a[80:83]\n" \
M_"a[80:83], a[212:215], a[224:227], a[80:83]\n" \
M_"a[96:99], a[152:155], a[160:163], a[96:99]\n" \
X_"a[254:255], v39 offset:9216\n" \
M_"a[96:99], a[216:219], a[224:227], a[96:99]\n" \
M_"a[100:103], a[152:155], a[164:167], a[100:103]\n" \
M_"a[100:103], a[216:219], a[228:231], a[100:103]\n" \
M_"a[104:107], a[152:155], a[168:171], a[104:107]\n" \
M_"a[104:107], a[216:219], a[232:235], a[104:107]\n" \
M_"a[108:111], a[152:155], a[172:175], a[108:111]\n" \
M_"a[108:111], a[216:219], a[236:239], a[108:111]\n" \
"s_waitcnt lgkmcnt(15)\n" \
M_"a[124:127], a[156:159], a[172:175], a[124:127]\n" \
M_"a[124:127], a[220:223], a[236:239], a[124:127]\n" \
M_"a[120:123], a[156:159], a[168:171], a[120:123]\n" \
M_"a[120:123], a[220:223], a[232:235], a[120:123]\n" \
M_"a[116:119], a[156:159], a[164:167], a[116:119]\n" \
M_"a[116:119], a[220:223], a[228:231], a[116:119]\n" \
M_"a[112:115], a[156:159], a[160:163], a[112:115]\n" \
"s_waitcnt vmcnt(4) lgkmcnt(0)\n" \
M_"a[112:115], a[220:223], a[224:227], a[112:115]\n" \
"s_barrier\n" \
M_"v[52:55], a[128:131], a[176:179], v[52:55]\n" \
"s_add_u32 m0, s30, 0x0\n" \
"s_movk_i32 s84, 0x300\n" \
M_"v[52:55], a[192:195], a[240:243], v[52:55]\n" \
B_"v22, s[64:67], s84 offen offset:0 lds\n" \
M_"v[56:59], a[128:131], a[180:183], v[56:59]\n" \
X_"a[160:161], v32 offset:32768\n" \
M_"v[56:59], a[192:195], a[244:247], v[56:59]\n" \
"s_movk_i32 s84, 0x1f00\n" \
M_"v[60:63], a[128:131], a[184:187], v[60:63]\n" \
B_"v23, s[64:67], s84 offen offset:1024 lds\n" \
M_"v[60:63], a[192:195], a[248:251], v[60:63]\n" \
X_"a[162:163], v32 offset:33792\n" \
M_"v[64:67], a[128:131], a[188:191], v[64:67]\n" \
X_"a[224:225], v32 offset:40960\n" \
M_"v[64:67], a[192:195], a[252:255], v[64:67]\n" \
R_"a[128:131], v28 offset:49152\n" \
M_"v[80:83], a[132:135], a[188:191], v[80:83]\n" \
R_"a[192:195], v29 offset:49152\n" \
M_"v[80:83], a[196:199], a[252:255], v[80:83]\n" \
"s_movk_i32 s84, 0x3b00\n" \
M_"v[76:79], a[132:135], a[184:187], v[76:79]\n" \
B_"v22, s[64:67], s84 offen offset:2048 lds\n" \
M_"v[76:79], a[196:199], a[248:251], v[76:79]\n" \
"s_movk_i32 s84, 0x5700\n" \
M_"v[72:75], a[132:135], a[180:183], v[72:75]\n" \
B_"v23, s[64:67], s84 offen offset:3072 lds\n" \
M_"v[72:75], a[196:199], a[244:247], v[72:75]\n" \
"s_add_u32 m0, s31, 0x0\n" \
"s_mov_b32 s84, 0x300000\n" \
M_"v[68:71], a[132:135], a[176:179], v[68:71]\n" \
B_"v26, s[68:71], s84 offen offset:0 lds\n" \
M_"v[68:71], a[196:199], a[240:243], v[68:71]\n" \
R_"a[132:135], v28 offset:51200\n" \
M_"v[84:87], a[136:139], a[176:179], v[84:87]\n" \
R_"a[196:199], v29 offset:51200\n" \
M_"v[84:87], a[200:203], a[240:243], v[84:87]\n" \
"s_addk_i32 s84, 0x7c00\n" \
M_"v[88:91], a[136:139], a[180:183], v[88:91]\n" \
B_"v26, s[68:71], s84 offen offset:1024 lds\n" \
M_"v[88:91], a[200:203], a[244:247], v[88:91]\n" \
X_"a[226:227], v32 offset:41984\n" \
M_"v[92:95], a[136:139], a[184:187], v[92:95]\n" \
"s_addk_i32 s84, 0x7c00\n" \
M_"v[92:95], a[200:203], a[248:251], v[92:95]\n" \
B_"v27, s[68:71], s84 offen offset:2048 lds\n" \
M_"v[96:99], a[136:139], a[188:191], v[96:99]\n" \
X_"a[164:165], v33 offset:32768\n" \
M_"v[96:99], a[200:203], a[252:255], v[96:99]\n" \
R_"a[136:139], v28 offset:53248\n" \
M_"v[112:115], a[140:143], a[188:191], v[112:115]\n" \
R_"a[200:203], v29 offset:53248\n" \
M_"v[112:115], a[204:207], a[252:255], v[112:115]\n" \
"s_addk_i32 s84, 0x7c00\n" \
M_"v[108:111], a[140:143], a[184:187], v[108:111]\n" \
B_"v27, s[68:71], s84 offen offset:3072 lds\n" \
M_"v[108:111], a[204:207], a[248:251], v[108:111]\n" \
"s_add_u32 m0, s30, 0x1000\n" \
"s_mov_b32 s84, 0x8300\n" \
M_"v[104:107], a[140:143], a[180:183], v[104:107]\n" \
B_"v22, s[64:67], s84 offen offset:0 lds\n" \
M_"v[104:107], a[204:207], a[244:247], v[104:107]\n" \
X_"a[166:167], v33 offset:33792\n" \
M_"v[100:103], a[140:143], a[176:179], v[100:103]\n" \
X_"a[228:229], v33 offset:40960\n" \
M_"v[100:103], a[204:207], a[240:243], v[100:103]\n" \
R_"a[140:143], v28 offset:55296\n" \
M_"v[116:119], a[144:147], a[176:179], v[116:119]\n" \
R_"a[204:207], v29 offset:55296\n" \
M_"v[116:119], a[208:211], a[240:243], v[116:119]\n" \
"s_addk_i32 s84, 0x1c00\n" \
M_"v[120:123], a[144:147], a[180:183], v[120:123]\n" \
B_"v23, s[64:67], s84 offen offset:1024 lds\n" \
M_"v[120:123], a[208:211], a[244:247], v[120:123]\n" \
"s_addk_i32 s84, 0x1c00\n" \
M_"v[124:127], a[144:147], a[184:187], v[124:127]\n" \
B_"v22, s[64:67], s84 offen offset:2048 lds\n" \
M_"v[124:127], a[208:211], a[248:251], v[124:127]\n" \
"s_addk_i32 s84, 0x1c00\n" \
"s_nop 0\n" \
M_"v[128:131], a[144:147], a[188:191], v[128:131]\n" \
B_"v23, s[64:67], s84 offen offset:3072 lds\n" \
M_"v[128:131], a[208:211], a[252:255], v[128:131]\n" \
R_"a[144:147], v28 offset:32768\n" \
M_"v[144:147], a[148:151], a[188:191], v[144:147]\n" \
R_"a[208:211], v29 offset:32768\n" \
M_"v[144:147], a[212:215], a[252:255], v[144:147]\n" \
"s_add_u32 m0, s31, 0x4000\n" \
"s_mov_b32 s84, 0x300000\n" \
M_"v[140:143], a[148:151], a[184:187], v[140:143]\n" \
B_"v26, s[72:75], s84 offen offset:0 lds\n" \
M_"v[140:143], a[212:215], a[248:251], v[140:143]\n" \
X_"a[230:231], v33 offset:41984\n" \
M_"v[136:139], a[148:151], a[180:183], v[136:139]\n" \
"s_addk_i32 s84, 0x7c00\n" \
"s_nop 0\n" \
M_"v[136:139], a[212:215], a[244:247], v[136:139]\n" \
B_"v26, s[72:75], s84 offen offset:1024 lds\n" \
M_"v[132:135], a[148:151], a[176:179], v[132:135]\n" \
X_"a[168:169], v34 offset:32768\n" \
M_"v[132:135], a[212:215], a[240:243], v[132:135]\n" \
R_"a[148:151], v28 offset:34816\n" \
M_"v[148:151], a[152:155], a[176:179], v[148:151]\n" \
R_"a[212:215], v29 offset:34816\n" \
M_"v[148:151], a[216:219], a[240:243], v[148:151]\n" \
X_"a[170:171], v34 offset:33792\n" \
M_"v[152:155], a[152:155], a[180:183], v[152:155]\n" \
X_"a[232:233], v34 offset:40960\n" \
M_"v[152:155], a[216:219], a[244:247], v[152:155]\n" \
X_"a[234:235], v34 offset:41984\n" \
M_"v[156:159], a[152:155], a[184:187], v[156:159]\n" \
X_"a[172:173], v35 offset:32768\n" \
M_"v[156:159], a[216:219], a[248:251], v[156:159]\n" \
X_"a[174:175], v35 offset:33792\n" \
M_"v[160:163], a[152:155], a[188:191], v[160:163]\n" \
X_"a[236:237], v35 offset:40960\n" \
M_"v[160:163], a[216:219], a[252:255], v[160:163]\n" \
R_"a[152:155], v28 offset:36864\n" \
M_"v[176:179], a[156:159], a[188:191], v[176:179]\n" \
R_"a[216:219], v29 offset:36864\n" \
M_"v[176:179], a[220:223], a[252:255], v[176:179]\n" \
X_"a[238:239], v35 offset:41984\n" \
M_"v[172:175], a[156:159], a[184:187], v[172:175]\n" \
M_"v[172:175], a[220:223], a[248:251], v[172:175]\n" \
M_"v[168:171], a[156:159], a[180:183], v[168:171]\n" \
M_"v[168:171], a[220:223], a[244:247], v[168:171]\n" \
M_"v[164:167], a[156:159], a[176:179], v[164:167]\n" \
M_"v[164:167], a[220:223], a[240:243], v[164:167]\n" \
R_"a[156:159], v28 offset:38912\n" \
R_"a[220:223], v29 offset:38912\n" \
"s_waitcnt lgkmcnt(15)\n" \
M_"a[0:3], a[128:131], a[160:163], a[0:3]\n" \
"s_addk_i32 s84, 0x7c00\n" \
M_"a[0:3], a[192:195], a[224:227], a[0:3]\n" \
B_"v27, s[72:75], s84 offen offset:2048 lds\n" \
M_"a[4:7], a[128:131], a[164:167], a[4:7]\n" \
X_"a[176:177], v36 offset:32768\n" \
"s_waitcnt lgkmcnt(15)\n" \
M_"a[4:7], a[192:195], a[228:231], a[4:7]\n" \
"s_addk_i32 s84, 0x7c00\n" \
"s_waitcnt lgkmcnt(11)\n" \
M_"a[8:11], a[128:131], a[168:171], a[8:11]\n" \
B_"v27, s[72:75], s84 offen offset:3072 lds\n" \
"s_waitcnt lgkmcnt(9)\n" \
M_"a[8:11], a[192:195], a[232:235], a[8:11]\n" \
X_"a[178:179], v36 offset:33792\n" \
"s_waitcnt lgkmcnt(8)\n" \
M_"a[12:15], a[128:131], a[172:175], a[12:15]\n" \
X_"a[240:241], v36 offset:40960\n" \
"s_waitcnt lgkmcnt(5)\n" \
M_"a[12:15], a[192:195], a[236:239], a[12:15]\n" \
M_"a[28:31], a[132:135], a[172:175], a[28:31]\n" \
M_"a[28:31], a[196:199], a[236:239], a[28:31]\n" \
X_"a[242:243], v36 offset:41984\n" \
M_"a[24:27], a[132:135], a[168:171], a[24:27]\n" \
M_"a[24:27], a[196:199], a[232:235], a[24:27]\n" \
M_"a[20:23], a[132:135], a[164:167], a[20:23]\n" \
M_"a[20:23], a[196:199], a[228:231], a[20:23]\n" \
X_"a[180:181], v37 offset:32768\n" \
M_"a[16:19], a[132:135], a[160:163], a[16:19]\n" \
M_"a[16:19], a[196:199], a[224:227], a[16:19]\n" \
M_"a[32:35], a[136:139], a[160:163], a[32:35]\n" \
X_"a[182:183], v37 offset:33792\n" \
M_"a[32:35], a[200:203], a[224:227], a[32:35]\n" \
M_"a[36:39], a[136:139], a[164:167], a[36:39]\n" \
"v_lshlrev_b32_e32 v45, 3, v20\n" \
"v_add_u32_e32 v45, s31, v45\n" \
M_"a[36:39], a[200:203], a[228:231], a[36:39]\n" \
"s_getpc_b64 s[86:87]\n" \
"s_sub_u32 s86, s86, 0x10800\n" \
"s_subb_u32 s87, s87, 0\n" \
"s_mov_b32 m0, s32\n" \
"s_nop 0\n" \
M_"a[40:43], a[136:139], a[168:171], a[40:43]\n" \
"global_load_lds_dword v45, s[86:87]\n" \
M_"a[40:43], a[200:203], a[232:235], a[40:43]\n" \
X_"a[244:245], v37 offset:40960\n" \
M_"a[44:47], a[136:139], a[172:175], a[44:47]\n" \
X_"a[246:247], v37 offset:41984\n" \
M_"a[44:47], a[200:203], a[236:239], a[44:47]\n" \
M_"a[60:63], a[140:143], a[172:175], a[60:63]\n" \
M_"a[60:63], a[204:207], a[236:239], a[60:63]\n" \
M_"a[56:59], a[140:143], a[168:171], a[56:59]\n" \
X_"a[184:185], v38 offset:32768\n" \
M_"a[56:59], a[204:207], a[232:235], a[56:59]\n" \
M_"a[52:55], a[140:143], a[164:167], a[52:55]\n" \
M_"a[52:55], a[204:207], a[228:231], a[52:55]\n" \
X_"a[186:187], v38 offset:33792\n" \
M_"a[48:51], a[140:143], a[160:163], a[48:51]\n" \
M_"a[48:51], a[204:207], a[224:227], a[48:51]\n" \
M_"a[64:67], a[144:147], a[160:163], a[64:67]\n" \
X_"a[248:249], v38 offset:40960\n" \
M_"a[64:67], a[208:211], a[224:227], a[64:67]\n" \
M_"a[68:71], a[144:147], a[164:167], a[68:71]\n" \
M_"a[68:71], a[208:211], a[228:231], a[68:71]\n" \
X_"a[250:251], v38 offset:41984\n" \
M_"a[72:75], a[144:147], a[168:171], a[72:75]\n" \
M_"a[72:75], a[208:211], a[232:235], a[72:75]\n" \
M_"a[76:79], a[144:147], a[172:175], a[76:79]\n" \
M_"a[76:79], a[208:211], a[236:239], a[76:79]\n" \
X_"a[188:189], v39 offset:32768\n" \
M_"a[92:95], a[148:151], a[172:175], a[92:95]\n" \
M_"a[92:95], a[212:215], a[236:239], a[92:95]\n" \
M_"a[88:91], a[148:151], a[168:171], a[88:91]\n" \
X_"a[190:191], v39 offset:33792\n" \
M_"a[88:91], a[212:215], a[232:235], a[88:91]\n" \
M_"a[84:87], a[148:151], a[164:167], a[84:87]\n" \
M_"a[84:87], a[212:215], a[228:231], a[84:87]\n" \
X_"a[252:253], v39 offset:40960\n" \
M_"a[80:83], a[148:151], a[160:163], a[80:83]\n" \
M_"a[80:83], a[212:215], a[224:227], a[80:83]\n" \
M_"a[96:99], a[152:155], a[160:163], a[96:99]\n" \
X_"a[254:255], v39 offset:41984\n" \
M_"a[96:99], a[216:219], a[224:227], a[96:99]\n" \
M_"a[100:103], a[152:155], a[164:167], a[100:103]\n" \
M_"a[100:103], a[216:219], a[228:231], a[100:103]\n" \
M_"a[104:107], a[152:155], a[168:171], a[104:107]\n" \
M_"a[104:107], a[216:219], a[232:235], a[104:107]\n" \
M_"a[108:111], a[152:155], a[172:175], a[108:111]\n" \
M_"a[108:111], a[216:219], a[236:239], a[108:111]\n" \
"s_waitcnt lgkmcnt(15)\n" \
M_"a[124:127], a[156:159], a[172:175], a[124:127]\n" \
M_"a[124:127], a[220:223], a[236:239], a[124:127]\n" \
M_"a[120:123], a[156:159], a[168:171], a[120:123]\n" \
M_"a[120:123], a[220:223], a[232:235], a[120:123]\n" \
M_"a[116:119], a[156:159], a[164:167], a[116:119]\n" \
M_"a[116:119], a[220:223], a[228:231], a[116:119]\n" \
M_"a[112:115], a[156:159], a[160:163], a[112:115]\n" \
"s_waitcnt vmcnt(1) lgkmcnt(0)\n" \
M_"a[112:115], a[220:223], a[224:227], a[112:115]\n" \
"s_barrier\n" \
"v_mov_b32_e32 v45, s62\n" \
"s_mov_b64 exec, 1\n" \
"buffer_store_dword v45, off, s[80:83], s37 offset:128 sc1\n" \
"s_mov_b64 exec, -1\n" \
M_"v[52:55], a[128:131], a[176:179], v[52:55]\n" \
"s_add_u32 m0, s30, 0x8000\n" \
"s_movk_i32 s84, 0x380\n" \
M_"v[52:55], a[192:195], a[240:243], v[52:55]\n" \
B_"v22, s[64:67], s84 offen offset:0 lds\n" \
M_"v[56:59], a[128:131], a[180:183], v[56:59]\n" \
X_"a[160:161], v32 offset:0\n" \
M_"v[56:59], a[192:195], a[244:247], v[56:59]\n" \
"s_movk_i32 s84, 0x1f80\n" \
M_"v[60:63], a[128:131], a[184:187], v[60:63]\n" \
B_"v23, s[64:67], s84 offen offset:1024 lds\n" \
M_"v[60:63], a[192:195], a[248:251], v[60:63]\n" \
X_"a[162:163], v32 offset:1024\n" \
M_"v[64:67], a[128:131], a[188:191], v[64:67]\n" \
X_"a[224:225], v32 offset:8192\n" \
M_"v[64:67], a[192:195], a[252:255], v[64:67]\n" \
R_"a[128:131], v28 offset:16384\n" \
M_"v[80:83], a[132:135], a[188:191], v[80:83]\n" \
R_"a[192:195], v29 offset:16384\n" \
M_"v[80:83], a[196:199], a[252:255], v[80:83]\n" \
"s_movk_i32 s84, 0x3b80\n" \
M_"v[76:79], a[132:135], a[184:187], v[76:79]\n" \
B_"v22, s[64:67], s84 offen offset:2048 lds\n" \
M_"v[76:79], a[196:199], a[248:251], v[76:79]\n" \
"s_movk_i32 s84, 0x5780\n" \
M_"v[72:75], a[132:135], a[180:183], v[72:75]\n" \
B_"v23, s[64:67], s84 offen offset:3072 lds\n" \
M_"v[72:75], a[196:199], a[244:247], v[72:75]\n" \
"s_add_u32 m0, s31, 0x8000\n" \
"s_mov_b32 s84, 0x380000\n" \
M_"v[68:71], a[132:135], a[176:179], v[68:71]\n" \
B_"v26, s[68:71], s84 offen offset:0 lds\n" \
M_"v[68:71], a[196:199], a[240:243], v[68:71]\n" \
R_"a[132:135], v28 offset:18432\n" \
M_"v[84:87], a[136:139], a[176:179], v[84:87]\n" \
R_"a[196:199], v29 offset:18432\n" \
M_"v[84:87], a[200:203], a[240:243], v[84:87]\n" \
"s_addk_i32 s84, 0x7c00\n" \
M_"v[88:91], a[136:139], a[180:183], v[88:91]\n" \
B_"v26, s[68:71], s84 offen offset:1024 lds\n" \
M_"v[88:91], a[200:203], a[244:247], v[88:91]\n" \
X_"a[226:227], v32 offset:9216\n" \
M_"v[92:95], a[136:139], a[184:187], v[92:95]\n" \
"s_addk_i32 s84, 0x7c00\n" \
M_"v[92:95], a[200:203], a[248:251], v[92:95]\n" \
B_"v27, s[68:71], s84 offen offset:2048 lds\n" \
M_"v[96:99], a[136:139], a[188:191], v[96:99]\n" \
X_"a[164:165], v33 offset:0\n" \
M_"v[96:99], a[200:203], a[252:255], v[96:99]\n" \
R_"a[136:139], v28 offset:20480\n" \
M_"v[112:115], a[140:143], a[188:191], v[112:115]\n" \
R_"a[200:203], v29 offset:20480\n" \
M_"v[112:115], a[204:207], a[252:255], v[112:115]\n" \
"s_addk_i32 s84, 0x7c00\n" \
"s_nop 0\n" \
M_"v[108:111], a[140:143], a[184:187], v[108:111]\n" \
B_"v27, s[68:71], s84 offen offset:3072 lds\n" \
M_"v[108:111], a[204:207], a[248:251], v[108:111]\n" \
"s_add_u32 m0, s30, 0x9000\n" \
"s_mov_b32 s84, 0x8380\n" \
M_"v[104:107], a[140:143], a[180:183], v[104:107]\n" \
B_"v22, s[64:67], s84 offen offset:0 lds\n" \
M_"v[104:107], a[204:207], a[244:247], v[104:107]\n" \
X_"a[166:167], v33 offset:1024\n" \
M_"v[100:103], a[140:143], a[176:179], v[100:103]\n" \
X_"a[228:229], v33 offset:8192\n" \
M_"v[100:103], a[204:207], a[240:243], v[100:103]\n" \
R_"a[140:143], v28 offset:22528\n" \
M_"v[116:119], a[144:147], a[176:179], v[116:119]\n" \
R_"a[204:207], v29 offset:22528\n" \
M_"v[116:119], a[208:211], a[240:243], v[116:119]\n" \
"s_addk_i32 s84, 0x1c00\n" \
M_"v[120:123], a[144:147], a[180:183], v[120:123]\n" \
B_"v23, s[64:67], s84 offen offset:1024 lds\n" \
M_"v[120:123], a[208:211], a[244:247], v[120:123]\n" \
"s_addk_i32 s84, 0x1c00\n" \
M_"v[124:127], a[144:147], a[184:187], v[124:127]\n" \
B_"v22, s[64:67], s84 offen offset:2048 lds\n" \
M_"v[124:127], a[208:211], a[248:251], v[124:127]\n" \
"s_addk_i32 s84, 0x1c00\n" \
"s_nop 0\n" \
M_"v[128:131], a[144:147], a[188:191], v[128:131]\n" \
B_"v23, s[64:67], s84 offen offset:3072 lds\n" \
M_"v[128:131], a[208:211], a[252:255], v[128:131]\n" \
R_"a[144:147], v28 offset:0\n" \
M_"v[144:147], a[148:151], a[188:191], v[144:147]\n" \
R_"a[208:211], v29 offset:0\n" \
M_"v[144:147], a[212:215], a[252:255], v[144:147]\n" \
"s_add_u32 m0, s31, 0xc000\n" \
"s_mov_b32 s84, 0x380000\n" \
M_"v[140:143], a[148:151], a[184:187], v[140:143]\n" \
B_"v26, s[72:75], s84 offen offset:0 lds\n" \
M_"v[140:143], a[212:215], a[248:251], v[140:143]\n" \
X_"a[230:231], v33 offset:9216\n" \
M_"v[136:139], a[148:151], a[180:183], v[136:139]\n" \
"s_addk_i32 s84, 0x7c00\n" \
"s_nop 0\n" \
M_"v[136:139], a[212:215], a[244:247], v[136:139]\n" \
B_"v26, s[72:75], s84 offen offset:1024 lds\n" \
M_"v[132:135], a[148:151], a[176:179], v[132:135]\n" \
X_"a[168:169], v34 offset:0\n" \
M_"v[132:135], a[212:215], a[240:243], v[132:135]\n" \
R_"a[148:151], v28 offset:2048\n" \
M_"v[148:151], a[152:155], a[176:179], v[148:151]\n" \
R_"a[212:215], v29 offset:2048\n" \
M_"v[148:151], a[216:219], a[240:243], v[148:151]\n" \
X_"a[170:171], v34 offset:1024\n" \
M_"v[152:155], a[152:155], a[180:183], v[152:155]\n" \
X_"a[232:233], v34 offset:8192\n" \
M_"v[152:155], a[216:219], a[244:247], v[152:155]\n" \
X_"a[234:235], v34 offset:9216\n" \
M_"v[156:159], a[152:155], a[184:187], v[156:159]\n" \
X_"a[172:173], v35 offset:0\n" \
M_"v[156:159], a[216:219], a[248:251], v[156:159]\n" \
X_"a[174:175], v35 offset:1024\n" \
M_"v[160:163], a[152:155], a[188:191], v[160:163]\n" \
X_"a[236:237], v35 offset:8192\n" \
M_"v[160:163], a[216:219], a[252:255], v[160:163]\n" \
R_"a[152:155], v28 offset:4096\n" \
M_"v[176:179], a[156:159], a[188:191], v[176:179]\n" \
R_"a[216:219], v29 offset:4096\n" \
M_"v[176:179], a[220:223], a[252:255], v[176:179]\n" \
X_"a[238:239], v35 offset:9216\n" \
M_"v[172:175], a[156:159], a[184:187], v[172:175]\n" \
M_"v[172:175], a[220:223], a[248:251], v[172:175]\n" \
M_"v[168:171], a[156:159], a[180:183], v[168:171]\n" \
M_"v[168:171], a[220:223], a[244:247], v[168:171]\n" \
M_"v[164:167], a[156:159], a[176:179], v[164:167]\n" \
M_"v[164:167], a[220:223], a[240:243], v[164:167]\n" \
R_"a[156:159], v28 offset:6144\n" \
R_"a[220:223], v29 offset:6144\n" \
"s_waitcnt lgkmcnt(15)\n" \
M_"a[0:3], a[128:131], a[160:163], a[0:3]\n" \
"s_addk_i32 s84, 0x7c00\n" \
M_"a[0:3], a[192:195], a[224:227], a[0:3]\n" \
B_"v27, s[72:75], s84 offen offset:2048 lds\n" \
M_"a[4:7], a[128:131], a[164:167], a[4:7]\n" \
X_"a[176:177], v36 offset:0\n" \
"s_waitcnt lgkmcnt(15)\n" \
M_"a[4:7], a[192:195], a[228:231], a[4:7]\n" \
"s_addk_i32 s84, 0x7c00\n" \
"s_waitcnt lgkmcnt(11)\n" \
M_"a[8:11], a[128:131], a[168:171], a[8:11]\n" \
B_"v27, s[72:75], s84 offen offset:3072 lds\n" \
"s_waitcnt lgkmcnt(9)\n" \
M_"a[8:11], a[192:195], a[232:235], a[8:11]\n" \
X_"a[178:179], v36 offset:1024\n" \
"s_waitcnt lgkmcnt(8)\n" \
M_"a[12:15], a[128:131], a[172:175], a[12:15]\n" \
X_"a[240:241], v36 offset:8192\n" \
"s_waitcnt lgkmcnt(5)\n" \
M_"a[12:15], a[192:195], a[236:239], a[12:15]\n" \
M_"a[28:31], a[132:135], a[172:175], a[28:31]\n" \
M_"a[28:31], a[196:199], a[236:239], a[28:31]\n" \
X_"a[242:243], v36 offset:9216\n" \
M_"a[24:27], a[132:135], a[168:171], a[24:27]\n" \
M_"a[24:27], a[196:199], a[232:235], a[24:27]\n" \
M_"a[20:23], a[132:135], a[164:167], a[20:23]\n" \
M_"a[20:23], a[196:199], a[228:231], a[20:23]\n" \
X_"a[180:181], v37 offset:0\n" \
M_"a[16:19], a[132:135], a[160:163], a[16:19]\n" \
M_"a[16:19], a[196:199], a[224:227], a[16:19]\n" \
M_"a[32:35], a[136:139], a[160:163], a[32:35]\n" \
"v_bfe_u32 v44, v20, 2, 5\n" \
X_"a[182:183], v37 offset:1024\n" \
M_"a[32:35], a[200:203], a[224:227], a[32:35]\n" \
"buffer_load_dword v44, v44, s[80:83], s38 offen offset:128 sc1\n" \
M_"a[36:39], a[136:139], a[164:167], a[36:39]\n" \
M_"a[36:39], a[200:203], a[228:231], a[36:39]\n" \
X_"a[244:245], v37 offset:8192\n" \
M_"a[40:43], a[136:139], a[168:171], a[40:43]\n" \
M_"a[40:43], a[200:203], a[232:235], a[40:43]\n" \
M_"a[44:47], a[136:139], a[172:175], a[44:47]\n" \
X_"a[246:247], v37 offset:9216\n" \
M_"a[44:47], a[200:203], a[236:239], a[44:47]\n" \
M_"a[60:63], a[140:143], a[172:175], a[60:63]\n" \
"global_store_dwordx4 v49, v[0:3], s[28:29] offset:0 sc1\n" \
M_"a[60:63], a[204:207], a[236:239], a[60:63]\n" \
M_"a[56:59], a[140:143], a[168:171], a[56:59]\n" \
X_"a[184:185], v38 offset:0\n" \
M_"a[56:59], a[204:207], a[232:235], a[56:59]\n" \
M_"a[52:55], a[140:143], a[164:167], a[52:55]\n" \
M_"a[52:55], a[204:207], a[228:231], a[52:55]\n" \
X_"a[186:187], v38 offset:1024\n" \
M_"a[48:51], a[140:143], a[160:163], a[48:51]\n" \
M_"a[48:51], a[204:207], a[224:227], a[48:51]\n" \
M_"a[64:67], a[144:147], a[160:163], a[64:67]\n" \
"global_store_dwordx4 v49, v[0:3], s[28:29] offset:1024 sc1\n" \
M_"a[64:67], a[208:211], a[224:227], a[64:67]\n" \
X_"a[248:249], v38 offset:8192\n" \
M_"a[68:71], a[144:147], a[164:167], a[68:71]\n" \
M_"a[68:71], a[208:211], a[228:231], a[68:71]\n" \
X_"a[250:251], v38 offset:9216\n" \
M_"a[72:75], a[144:147], a[168:171], a[72:75]\n" \
M_"a[72:75], a[208:211], a[232:235], a[72:75]\n" \
M_"a[76:79], a[144:147], a[172:175], a[76:79]\n" \
M_"a[76:79], a[208:211], a[236:239], a[76:79]\n" \
X_"a[188:189], v39 offset:0\n" \
M_"a[92:95], a[148:151], a[172:175], a[92:95]\n" \
"s_mov_b64 exec, 0xffff\n" \
"global_store_dwordx4 v49, v[0:3], s[28:29] offset:2048 sc1\n" \
"s_mov_b64 exec, -1\n" \
"s_nop 0\n" \
M_"a[92:95], a[212:215], a[236:239], a[92:95]\n" \
M_"a[88:91], a[148:151], a[168:171], a[88:91]\n" \
X_"a[190:191], v39 offset:1024\n" \
M_"a[88:91], a[212:215], a[232:235], a[88:91]\n" \
M_"a[84:87], a[148:151], a[164:167], a[84:87]\n" \
M_"a[84:87], a[212:215], a[228:231], a[84:87]\n" \
".fill (L_pb + 57056 - .) / 4, 4, 0xBF800000\n" \
"L_pp14:\n" \
"s_cbranch_execz L_pret\n" \
"s_nop 0\n" \
X_"a[252:253], v39 offset:8192\n" \
M_"a[80:83], a[148:151], a[160:163], a[80:83]\n" \
M_"a[80:83], a[212:215], a[224:227], a[80:83]\n" \
M_"a[96:99], a[152:155], a[160:163], a[96:99]\n" \
X_"a[254:255], v39 offset:9216\n" \
M_"a[96:99], a[216:219], a[224:227], a[96:99]\n" \
M_"a[100:103], a[152:155], a[164:167], a[100:103]\n" \
M_"a[100:103], a[216:219], a[228:231], a[100:103]\n" \
M_"a[104:107], a[152:155], a[168:171], a[104:107]\n" \
M_"a[104:107], a[216:219], a[232:235], a[104:107]\n" \
M_"a[108:111], a[152:155], a[172:175], a[108:111]\n" \
M_"a[108:111], a[216:219], a[236:239], a[108:111]\n" \
"s_waitcnt lgkmcnt(15)\n" \
M_"a[124:127], a[156:159], a[172:175], a[124:127]\n" \
M_"a[124:127], a[220:223], a[236:239], a[124:127]\n" \
M_"a[120:123], a[156:159], a[168:171], a[120:123]\n" \
M_"a[120:123], a[220:223], a[232:235], a[120:123]\n" \
M_"a[116:119], a[156:159], a[164:167], a[116:119]\n" \
M_"a[116:119], a[220:223], a[228:231], a[116:119]\n" \
M_"a[112:115], a[156:159], a[160:163], a[112:115]\n" \
"s_waitcnt vmcnt(3) lgkmcnt(0)\n" \
M_"a[112:115], a[220:223], a[224:227], a[112:115]\n" \
"s_barrier\n" \
"v_cmp_ne_u32_e32 vcc, s62, v44\n" \
"s_nop 4\n" \
"s_cbranch_vccnz L_poll61_%=\n" \
"L_pollr61_%=:\n" \
"s_mov_b32 s68, s12\n" \
"s_and_b32 s69, s13, 0xffff\n" \
"s_mov_b32 s70, 0x480000\n" \
"s_mov_b32 s71, 0x20000\n" \
M_"v[52:55], a[128:131], a[176:179], v[52:55]\n" \
"s_add_u32 m0, s40, 0x0\n" \
"s_add_u32 s85, s39, 0x0\n" \
"s_nop 0\n" \
M_"v[52:55], a[192:195], a[240:243], v[52:55]\n" \
B_"v20, s[80:83], s85 offen sc1 lds\n" \
M_"v[56:59], a[128:131], a[180:183], v[56:59]\n" \
X_"a[160:161], v32 offset:32768\n" \
M_"v[56:59], a[192:195], a[244:247], v[56:59]\n" \
"s_add_u32 m0, s40, 0x1000\n" \
"s_add_u32 s85, s39, 0x1000\n" \
M_"v[60:63], a[128:131], a[184:187], v[60:63]\n" \
B_"v20, s[80:83], s85 offen sc1 lds\n" \
M_"v[60:63], a[192:195], a[248:251], v[60:63]\n" \
X_"a[162:163], v32 offset:33792\n" \
M_"v[64:67], a[128:131], a[188:191], v[64:67]\n" \
X_"a[224:225], v32 offset:40960\n" \
M_"v[64:67], a[192:195], a[252:255], v[64:67]\n" \
R_"a[128:131], v28 offset:49152\n" \
M_"v[80:83], a[132:135], a[188:191], v[80:83]\n" \
R_"a[192:195], v29 offset:49152\n" \
M_"v[80:83], a[196:199], a[252:255], v[80:83]\n" \
"s_add_u32 m0, s40, 0x3000\n" \
"s_movk_i32 s84, 0x0\n" \
M_"v[76:79], a[132:135], a[184:187], v[76:79]\n" \
B_"v21, s[76:79], s84 offen offset:0 lds\n" \
M_"v[76:79], a[196:199], a[248:251], v[76:79]\n" \
"s_add_u32 m0, s30, 0x0\n" \
"s_movk_i32 s84, 0x0\n" \
"s_nop 0\n" \
M_"v[72:75], a[132:135], a[180:183], v[72:75]\n" \
B_"v24, s[76:79], s84 offen offset:0 lds\n" \
M_"v[72:75], a[196:199], a[244:247], v[72:75]\n" \
X_"a[226:227], v32 offset:41984\n" \
M_"v[68:71], a[132:135], a[176:179], v[68:71]\n" \
X_"a[164:165], v33 offset:32768\n" \
M_"v[68:71], a[196:199], a[240:243], v[68:71]\n" \
R_"a[132:135], v28 offset:51200\n" \
M_"v[84:87], a[136:139], a[176:179], v[84:87]\n" \
R_"a[196:199], v29 offset:51200\n" \
M_"v[84:87], a[200:203], a[240:243], v[84:87]\n" \
"s_movk_i32 s84, 0x2000\n" \
M_"v[88:91], a[136:139], a[180:183], v[88:91]\n" \
B_"v25, s[76:79], s84 offen offset:1024 lds\n" \
M_"v[88:91], a[200:203], a[244:247], v[88:91]\n" \
"s_movk_i32 s84, 0x4000\n" \
M_"v[92:95], a[136:139], a[184:187], v[92:95]\n" \
B_"v24, s[76:79], s84 offen offset:2048 lds\n" \
M_"v[92:95], a[200:203], a[248:251], v[92:95]\n" \
X_"a[166:167], v33 offset:33792\n" \
M_"v[96:99], a[136:139], a[188:191], v[96:99]\n" \
X_"a[228:229], v33 offset:40960\n" \
M_"v[96:99], a[200:203], a[252:255], v[96:99]\n" \
R_"a[136:139], v28 offset:53248\n" \
M_"v[112:115], a[140:143], a[188:191], v[112:115]\n" \
R_"a[200:203], v29 offset:53248\n" \
M_"v[112:115], a[204:207], a[252:255], v[112:115]\n" \
"s_movk_i32 s84, 0x6000\n" \
M_"v[108:111], a[140:143], a[184:187], v[108:111]\n" \
B_"v25, s[76:79], s84 offen offset:3072 lds\n" \
M_"v[108:111], a[204:207], a[248:251], v[108:111]\n" \
"s_add_u32 m0, s31, 0x0\n" \
"s_movk_i32 s84, 0x0\n" \
M_"v[104:107], a[140:143], a[180:183], v[104:107]\n" \
B_"v26, s[68:71], s84 offen offset:0 lds\n" \
M_"v[104:107], a[204:207], a[244:247], v[104:107]\n" \
"s_movk_i32 s84, 0x7c00\n" \
M_"v[100:103], a[140:143], a[176:179], v[100:103]\n" \
B_"v26, s[68:71], s84 offen offset:1024 lds\n" \
M_"v[100:103], a[204:207], a[240:243], v[100:103]\n" \
R_"a[140:143], v28 offset:55296\n" \
M_"v[116:119], a[144:147], a[176:179], v[116:119]\n" \
R_"a[204:207], v29 offset:55296\n" \
M_"v[116:119], a[208:211], a[240:243], v[116:119]\n" \
"s_addk_i32 s84, 0x7c00\n" \
M_"v[120:123], a[144:147], a[180:183], v[120:123]\n" \
B_"v27, s[68:71], s84 offen offset:2048 lds\n" \
M_"v[120:123], a[208:211], a[244:247], v[120:123]\n" \
X_"a[230:231], v33 offset:41984\n" \
M_"v[124:127], a[144:147], a[184:187], v[124:127]\n" \
"s_addk_i32 s84, 0x7c00\n" \
M_"v[124:127], a[208:211], a[248:251], v[124:127]\n" \
B_"v27, s[68:71], s84 offen offset:3072 lds\n" \
M_"v[128:131], a[144:147], a[188:191], v[128:131]\n" \
X_"a[168:169], v34 offset:32768\n" \
M_"v[128:131], a[208:211], a[252:255], v[128:131]\n" \
R_"a[144:147], v28 offset:32768\n" \
M_"v[144:147], a[148:151], a[188:191], v[144:147]\n" \
R_"a[208:211], v29 offset:32768\n" \
M_"v[144:147], a[212:215], a[252:255], v[144:147]\n" \
"s_add_u32 m0, s30, 0x1000\n" \
"s_mov_b32 s84, 0x9000\n" \
M_"v[140:143], a[148:151], a[184:187], v[140:143]\n" \
B_"v24, s[76:79], s84 offen offset:0 lds\n" \
M_"v[140:143], a[212:215], a[248:251], v[140:143]\n" \
X_"a[170:171], v34 offset:33792\n" \
M_"v[136:139], a[148:151], a[180:183], v[136:139]\n" \
"s_addk_i32 s84, 0x2000\n" \
"s_nop 0\n" \
M_"v[136:139], a[212:215], a[244:247], v[136:139]\n" \
B_"v25, s[76:79], s84 offen offset:1024 lds\n" \
M_"v[132:135], a[148:151], a[176:179], v[132:135]\n" \
X_"a[232:233], v34 offset:40960\n" \
M_"v[132:135], a[212:215], a[240:243], v[132:135]\n" \
R_"a[148:151], v28 offset:34816\n" \
M_"v[148:151], a[152:155], a[176:179], v[148:151]\n" \
R_"a[212:215], v29 offset:34816\n" \
M_"v[148:151], a[216:219], a[240:243], v[148:151]\n" \
X_"a[234:235], v34 offset:41984\n" \
M_"v[152:155], a[152:155], a[180:183], v[152:155]\n" \
X_"a[172:173], v35 offset:32768\n" \
M_"v[152:155], a[216:219], a[244:247], v[152:155]\n" \
X_"a[174:175], v35 offset:33792\n" \
M_"v[156:159], a[152:155], a[184:187], v[156:159]\n" \
X_"a[236:237], v35 offset:40960\n" \
M_"v[156:159], a[216:219], a[248:251], v[156:159]\n" \
X_"a[238:239], v35 offset:41984\n" \
M_"v[160:163], a[152:155], a[188:191], v[160:163]\n" \
M_"v[160:163], a[216:219], a[252:255], v[160:163]\n" \
R_"a[152:155], v28 offset:36864\n" \
M_"v[176:179], a[156:159], a[188:191], v[176:179]\n" \
R_"a[216:219], v29 offset:36864\n" \
M_"v[176:179], a[220:223], a[252:255], v[176:179]\n" \
M_"v[172:175], a[156:159], a[184:187], v[172:175]\n" \
M_"v[172:175], a[220:223], a[248:251], v[172:175]\n" \
M_"v[168:171], a[156:159], a[180:183], v[168:171]\n" \
M_"v[168:171], a[220:223], a[244:247], v[168:171]\n" \
M_"v[164:167], a[156:159], a[176:179], v[164:167]\n" \
M_"v[164:167], a[220:223], a[240:243], v[164:167]\n" \
R_"a[156:159], v28 offset:38912\n" \
R_"a[220:223], v29 offset:38912\n" \
"s_waitcnt lgkmcnt(15)\n" \
M_"a[0:3], a[128:131], a[160:163], a[0:3]\n" \
"s_addk_i32 s84, 0x2000\n" \
M_"a[0:3], a[192:195], a[224:227], a[0:3]\n" \
B_"v24, s[76:79], s84 offen offset:2048 lds\n" \
M_"a[4:7], a[128:131], a[164:167], a[4:7]\n" \
X_"a[176:177], v36 offset:32768\n" \
M_"a[4:7], a[192:195], a[228:231], a[4:7]\n" \
"s_addk_i32 s84, 0x2000\n" \
"s_waitcnt lgkmcnt(13)\n" \
M_"a[8:11], a[128:131], a[168:171], a[8:11]\n" \
B_"v25, s[76:79], s84 offen offset:3072 lds\n" \
"s_waitcnt lgkmcnt(9)\n" \
M_"a[8:11], a[192:195], a[232:235], a[8:11]\n" \
X_"a[178:179], v36 offset:33792\n" \
"s_waitcnt lgkmcnt(8)\n" \
M_"a[12:15], a[128:131], a[172:175], a[12:15]\n" \
X_"a[240:241], v36 offset:40960\n" \
"s_waitcnt lgkmcnt(7)\n" \
M_"a[12:15], a[192:195], a[236:239], a[12:15]\n" \
M_"a[28:31], a[132:135], a[172:175], a[28:31]\n" \
M_"a[28:31], a[196:199], a[236:239], a[28:31]\n" \
"s_add_u32 m0, s30, 0x20000\n" \
"s_movk_i32 s84, 0x100\n" \
M_"a[24:27], a[132:135], a[168:171], a[24:27]\n" \
B_"v24, s[76:79], s84 offen offset:0 lds\n" \
M_"a[24:27], a[196:199], a[232:235], a[24:27]\n" \
X_"a[242:243], v36 offset:41984\n" \
M_"a[20:23], a[132:135], a[164:167], a[20:23]\n" \
"v_accvgpr_read_b32 v184, a12\n" \
"v_accvgpr_read_b32 v185, a13\n" \
M_"a[20:23], a[196:199], a[228:231], a[20:23]\n" \
X_"a[180:181], v37 offset:32768\n" \
"v_accvgpr_read_b32 v186, a14\n" \
M_"a[16:19], a[132:135], a[160:163], a[16:19]\n" \
"s_movk_i32 s84, 0x2100\n" \
"s_nop 0\n" \
M_"a[16:19], a[196:199], a[224:227], a[16:19]\n" \
B_"v25, s[76:79], s84 offen offset:1024 lds\n" \
M_"a[32:35], a[136:139], a[160:163], a[32:35]\n" \
X_"a[182:183], v37 offset:33792\n" \
"v_accvgpr_read_b32 v187, a15\n" \
M_"a[32:35], a[200:203], a[224:227], a[32:35]\n" \
"v_accvgpr_read_b32 v180, a28\n" \
"v_accvgpr_read_b32 v181, a29\n" \
M_"a[36:39], a[136:139], a[164:167], a[36:39]\n" \
"v_accvgpr_read_b32 v182, a30\n" \
"v_accvgpr_read_b32 v183, a31\n" \
M_"a[36:39], a[200:203], a[228:231], a[36:39]\n" \
"s_movk_i32 s84, 0x4100\n" \
"s_nop 0\n" \
M_"a[40:43], a[136:139], a[168:171], a[40:43]\n" \
B_"v24, s[76:79], s84 offen offset:2048 lds\n" \
M_"a[40:43], a[200:203], a[232:235], a[40:43]\n" \
X_"a[244:245], v37 offset:40960\n" \
C_"v184, v184, v185\n" \
M_"a[44:47], a[136:139], a[172:175], a[44:47]\n" \
X_"a[246:247], v37 offset:41984\n" \
C_"v185, v186, v187\n" \
M_"a[44:47], a[200:203], a[236:239], a[44:47]\n" \
C_"v186, v180, v181\n" \
C_"v187, v182, v183\n" \
M_"a[60:63], a[140:143], a[172:175], a[60:63]\n" \
"s_movk_i32 s84, 0x6100\n" \
M_"a[60:63], a[204:207], a[236:239], a[60:63]\n" \
B_"v25, s[76:79], s84 offen offset:3072 lds\n" \
M_"a[56:59], a[140:143], a[168:171], a[56:59]\n" \
X_"a[184:185], v38 offset:32768\n" \
"v_accvgpr_read_b32 v188, a8\n" \
M_"a[56:59], a[204:207], a[232:235], a[56:59]\n" \
"v_accvgpr_read_b32 v189, a9\n" \
"v_accvgpr_read_b32 v190, a10\n" \
M_"a[52:55], a[140:143], a[164:167], a[52:55]\n" \
"s_add_u32 m0, s30, 0x21000\n" \
"s_addk_i32 s84, 0x3000\n" \
M_"a[52:55], a[204:207], a[228:231], a[52:55]\n" \
B_"v24, s[76:79], s84 offen offset:0 lds\n" \
M_"a[48:51], a[140:143], a[160:163], a[48:51]\n" \
X_"a[186:187], v38 offset:33792\n" \
"v_accvgpr_read_b32 v191, a11\n" \
M_"a[48:51], a[204:207], a[224:227], a[48:51]\n" \
"v_accvgpr_read_b32 v180, a24\n" \
"v_accvgpr_read_b32 v181, a25\n" \
M_"a[64:67], a[144:147], a[160:163], a[64:67]\n" \
X_"a[248:249], v38 offset:40960\n" \
"v_accvgpr_read_b32 v182, a26\n" \
M_"a[64:67], a[208:211], a[224:227], a[64:67]\n" \
"s_addk_i32 s84, 0x2000\n" \
"s_nop 0\n" \
M_"a[68:71], a[144:147], a[164:167], a[68:71]\n" \
B_"v25, s[76:79], s84 offen offset:1024 lds\n" \
M_"a[68:71], a[208:211], a[228:231], a[68:71]\n" \
X_"a[250:251], v38 offset:41984\n" \
"v_accvgpr_read_b32 v183, a27\n" \
M_"a[72:75], a[144:147], a[168:171], a[72:75]\n" \
C_"v188, v188, v189\n" \
C_"v189, v190, v191\n" \
M_"a[72:75], a[208:211], a[232:235], a[72:75]\n" \
C_"v190, v180, v181\n" \
C_"v191, v182, v183\n" \
M_"a[76:79], a[144:147], a[172:175], a[76:79]\n" \
"s_addk_i32 s84, 0x2000\n" \
M_"a[76:79], a[208:211], a[236:239], a[76:79]\n" \
B_"v24, s[76:79], s84 offen offset:2048 lds\n" \
M_"a[92:95], a[148:151], a[172:175], a[92:95]\n" \
"s_add_u32 s89, s41, 0x0\n" \
"buffer_store_dwordx4 v[184:187], v20, s[80:83], s89 offen offset:3072 sc1\n" \
M_"a[92:95], a[212:215], a[236:239], a[92:95]\n" \
X_"a[188:189], v39 offset:32768\n" \
"v_accvgpr_read_b32 v192, a4\n" \
M_"a[88:91], a[148:151], a[168:171], a[88:91]\n" \
X_"a[190:191], v39 offset:33792\n" \
"v_accvgpr_read_b32 v193, a5\n" \
M_"a[88:91], a[212:215], a[232:235], a[88:91]\n" \
"s_addk_i32 s84, 0x2000\n" \
M_"a[84:87], a[148:151], a[164:167], a[84:87]\n" \
B_"v25, s[76:79], s84 offen offset:3072 lds\n" \
M_"a[84:87], a[212:215], a[228:231], a[84:87]\n" \
X_"a[252:253], v39 offset:40960\n" \
"v_accvgpr_read_b32 v194, a6\n" \
M_"a[80:83], a[148:151], a[160:163], a[80:83]\n" \
"s_add_u32 s88, s41, 0x0\n" \
"buffer_store_dwordx4 v[188:191], v20, s[80:83], s88 offen offset:2048 sc1\n" \
M_"a[80:83], a[212:215], a[224:227], a[80:83]\n" \
"v_accvgpr_read_b32 v195, a7\n" \
"v_accvgpr_read_b32 v180, a20\n" \
"s_waitcnt lgkmcnt(15)\n" \
"s_nop 0\n" \
M_"a[96:99], a[152:155], a[160:163], a[96:99]\n" \
X_"a[254:255], v39 offset:41984\n" \
"v_accvgpr_read_b32 v181, a21\n" \
M_"a[96:99], a[216:219], a[224:227], a[96:99]\n" \
"v_accvgpr_read_b32 v182, a22\n" \
"v_accvgpr_read_b32 v183, a23\n" \
M_"a[100:103], a[152:155], a[164:167], a[100:103]\n" \
C_"v192, v192, v193\n" \
C_"v193, v194, v195\n" \
M_"a[100:103], a[216:219], a[228:231], a[100:103]\n" \
C_"v194, v180, v181\n" \
C_"v195, v182, v183\n" \
M_"a[104:107], a[152:155], a[168:171], a[104:107]\n" \
"s_add_u32 s89, s41, 0x0\n" \
"s_nop 0\n" \
"buffer_store_dwordx4 v[192:195], v20, s[80:83], s89 offen offset:1024 sc1\n" \
M_"a[104:107], a[216:219], a[232:235], a[104:107]\n" \
"v_accvgpr_read_b32 v196, a0\n" \
"v_accvgpr_read_b32 v197, a1\n" \
M_"a[108:111], a[152:155], a[172:175], a[108:111]\n" \
"v_accvgpr_read_b32 v198, a2\n" \
"v_accvgpr_read_b32 v199, a3\n" \
M_"a[108:111], a[216:219], a[236:239], a[108:111]\n" \
"v_accvgpr_read_b32 v180, a16\n" \
"v_accvgpr_read_b32 v181, a17\n" \
M_"a[124:127], a[156:159], a[172:175], a[124:127]\n" \
"v_accvgpr_read_b32 v182, a18\n" \
"v_accvgpr_read_b32 v183, a19\n" \
M_"a[124:127], a[220:223], a[236:239], a[124:127]\n" \
C_"v196, v196, v197\n" \
C_"v197, v198, v199\n" \
M_"a[120:123], a[156:159], a[168:171], a[120:123]\n" \
C_"v198, v180, v181\n" \
C_"v199, v182, v183\n" \
"s_add_u32 s88, s41, 0x0\n" \
"s_nop 0\n" \
"buffer_store_dwordx4 v[196:199], v20, s[80:83], s88 offen offset:0 sc1\n" \
M_"a[120:123], a[220:223], a[232:235], a[120:123]\n" \
"v_accvgpr_read_b32 v200, a44\n" \
"v_accvgpr_read_b32 v201, a45\n" \
M_"a[116:119], a[156:159], a[164:167], a[116:119]\n" \
"v_accvgpr_read_b32 v202, a46\n" \
"v_accvgpr_read_b32 v203, a47\n" \
M_"a[116:119], a[220:223], a[228:231], a[116:119]\n" \
"v_accvgpr_read_b32 v180, a60\n" \
"v_accvgpr_read_b32 v181, a61\n" \
M_"a[112:115], a[156:159], a[160:163], a[112:115]\n" \
"v_accvgpr_read_b32 v182, a62\n" \
"v_accvgpr_read_b32 v183, a63\n" \
M_"a[112:115], a[220:223], a[224:227], a[112:115]\n" \
C_"v200, v200, v201\n" \
C_"v201, v202, v203\n" \
"s_waitcnt vmcnt(12) lgkmcnt(0)\n" \
"s_barrier\n" \
"v_xor_b32_e32 v36, 0x4000, v36\n" \
"v_xor_b32_e64 v32, 0x0, v48\n" \
"v_xor_b32_e32 v37, 0x4000, v37\n" \
"v_xor_b32_e64 v33, 0x20, v48\n" \
"v_xor_b32_e32 v38, 0x4000, v38\n" \
"v_xor_b32_e64 v34, 0x40, v48\n" \
"v_xor_b32_e32 v39, 0x4000, v39\n" \
"v_xor_b32_e32 v35, 0x60, v48\n" \
"v_add_u32_e32 v28, s43, v28\n" \
"v_add_u32_e32 v29, s43, v29\n" \
M_"v[116:119], a[144:147], a[176:179], v[116:119]\n" \
"s_add_u32 m0, s40, 0x8000\n" \
"s_add_u32 s85, s39, 0x2000\n" \
M_"v[116:119], a[208:211], a[240:243], v[116:119]\n" \
B_"v20, s[80:83], s85 offen sc1 lds\n" \
M_"v[120:123], a[144:147], a[180:183], v[120:123]\n" \
X_"a[160:161], v32 offset:0\n" \
C_"v202, v180, v181\n" \
M_"v[120:123], a[208:211], a[244:247], v[120:123]\n" \
"s_add_u32 m0, s40, 0x9000\n" \
"s_add_u32 s85, s39, 0x3000\n" \
M_"v[124:127], a[144:147], a[184:187], v[124:127]\n" \
B_"v20, s[80:83], s85 offen sc1 lds\n" \
M_"v[124:127], a[208:211], a[248:251], v[124:127]\n" \
X_"a[162:163], v32 offset:512\n" \
C_"v203, v182, v183\n" \
M_"v[128:131], a[144:147], a[188:191], v[128:131]\n" \
X_"a[224:225], v32 offset:4096\n" \
"v_accvgpr_read_b32 v204, a40\n" \
M_"v[128:131], a[208:211], a[252:255], v[128:131]\n" \
R_"a[144:147], v28 offset:8192\n" \
"v_accvgpr_read_b32 v205, a41\n" \
M_"v[144:147], a[148:151], a[188:191], v[144:147]\n" \
R_"a[208:211], v29 offset:8192\n" \
"v_accvgpr_read_b32 v206, a42\n" \
M_"v[144:147], a[212:215], a[252:255], v[144:147]\n" \
"s_add_u32 m0, s40, 0xb000\n" \
"s_movk_i32 s84, 0x80\n" \
M_"v[140:143], a[148:151], a[184:187], v[140:143]\n" \
B_"v21, s[76:79], s84 offen offset:0 lds\n" \
M_"v[140:143], a[212:215], a[248:251], v[140:143]\n" \
"s_add_u32 m0, s30, 0x8000\n" \
"s_movk_i32 s84, 0x80\n" \
M_"v[136:139], a[148:151], a[180:183], v[136:139]\n" \
B_"v24, s[76:79], s84 offen offset:0 lds\n" \
M_"v[136:139], a[212:215], a[244:247], v[136:139]\n" \
X_"a[226:227], v32 offset:4608\n" \
"v_accvgpr_read_b32 v207, a43\n" \
M_"v[132:135], a[148:151], a[176:179], v[132:135]\n" \
X_"a[164:165], v33 offset:0\n" \
"v_accvgpr_read_b32 v180, a56\n" \
M_"v[132:135], a[212:215], a[240:243], v[132:135]\n" \
R_"a[148:151], v28 offset:10240\n" \
"v_accvgpr_read_b32 v181, a57\n" \
M_"v[148:151], a[152:155], a[176:179], v[148:151]\n" \
R_"a[212:215], v29 offset:10240\n" \
"v_accvgpr_read_b32 v182, a58\n" \
M_"v[148:151], a[216:219], a[240:243], v[148:151]\n" \
"s_movk_i32 s84, 0x2080\n" \
M_"v[152:155], a[152:155], a[180:183], v[152:155]\n" \
B_"v25, s[76:79], s84 offen offset:1024 lds\n" \
M_"v[152:155], a[216:219], a[244:247], v[152:155]\n" \
"s_movk_i32 s84, 0x4080\n" \
M_"v[156:159], a[152:155], a[184:187], v[156:159]\n" \
B_"v24, s[76:79], s84 offen offset:2048 lds\n" \
M_"v[156:159], a[216:219], a[248:251], v[156:159]\n" \
X_"a[166:167], v33 offset:512\n" \
"v_accvgpr_read_b32 v183, a59\n" \
M_"v[160:163], a[152:155], a[188:191], v[160:163]\n" \
X_"a[228:229], v33 offset:4096\n" \
C_"v204, v204, v205\n" \
M_"v[160:163], a[216:219], a[252:255], v[160:163]\n" \
R_"a[152:155], v28 offset:12288\n" \
C_"v205, v206, v207\n" \
M_"v[176:179], a[156:159], a[188:191], v[176:179]\n" \
R_"a[216:219], v29 offset:12288\n" \
C_"v206, v180, v181\n" \
M_"v[176:179], a[220:223], a[252:255], v[176:179]\n" \
"s_movk_i32 s84, 0x6080\n" \
M_"v[172:175], a[156:159], a[184:187], v[172:175]\n" \
B_"v25, s[76:79], s84 offen offset:3072 lds\n" \
M_"v[172:175], a[220:223], a[248:251], v[172:175]\n" \
"s_add_u32 m0, s31, 0x8000\n" \
"s_mov_b32 s84, 0x80000\n" \
M_"v[168:171], a[156:159], a[180:183], v[168:171]\n" \
B_"v26, s[68:71], s84 offen offset:0 lds\n" \
M_"v[168:171], a[220:223], a[244:247], v[168:171]\n" \
"s_addk_i32 s84, 0x7c00\n" \
M_"v[164:167], a[156:159], a[176:179], v[164:167]\n" \
B_"v26, s[68:71], s84 offen offset:1024 lds\n" \
M_"v[164:167], a[220:223], a[240:243], v[164:167]\n" \
R_"a[156:159], v28 offset:14336\n" \
C_"v207, v182, v183\n" \
M_"v[84:87], a[136:139], a[176:179], v[84:87]\n" \
R_"a[220:223], v29 offset:14336\n" \
"v_accvgpr_read_b32 v208, a36\n" \
M_"v[84:87], a[200:203], a[240:243], v[84:87]\n" \
"s_addk_i32 s84, 0x7c00\n" \
M_"v[88:91], a[136:139], a[180:183], v[88:91]\n" \
B_"v27, s[68:71], s84 offen offset:2048 lds\n" \
M_"v[88:91], a[200:203], a[244:247], v[88:91]\n" \
X_"a[230:231], v33 offset:4608\n" \
"v_accvgpr_read_b32 v209, a37\n" \
M_"v[92:95], a[136:139], a[184:187], v[92:95]\n" \
"s_addk_i32 s84, 0x7c00\n" \
M_"v[92:95], a[200:203], a[248:251], v[92:95]\n" \
B_"v27, s[68:71], s84 offen offset:3072 lds\n" \
M_"v[96:99], a[136:139], a[188:191], v[96:99]\n" \
X_"a[172:173], v35 offset:0\n" \
"v_accvgpr_read_b32 v210, a38\n" \
M_"v[96:99], a[200:203], a[252:255], v[96:99]\n" \
R_"a[136:139], v28 offset:4096\n" \
"v_accvgpr_read_b32 v211, a39\n" \
M_"v[112:115], a[140:143], a[188:191], v[112:115]\n" \
R_"a[200:203], v29 offset:4096\n" \
"v_accvgpr_read_b32 v180, a52\n" \
M_"v[112:115], a[204:207], a[252:255], v[112:115]\n" \
"s_add_u32 m0, s30, 0x9000\n" \
"s_mov_b32 s84, 0x9080\n" \
M_"v[108:111], a[140:143], a[184:187], v[108:111]\n" \
B_"v24, s[76:79], s84 offen offset:0 lds\n" \
M_"v[108:111], a[204:207], a[248:251], v[108:111]\n" \
X_"a[174:175], v35 offset:512\n" \
"v_accvgpr_read_b32 v181, a53\n" \
M_"v[104:107], a[140:143], a[180:183], v[104:107]\n" \
"s_addk_i32 s84, 0x2000\n" \
"s_nop 0\n" \
M_"v[104:107], a[204:207], a[244:247], v[104:107]\n" \
B_"v25, s[76:79], s84 offen offset:1024 lds\n" \
M_"v[100:103], a[140:143], a[176:179], v[100:103]\n" \
X_"a[236:237], v35 offset:4096\n" \
"v_accvgpr_read_b32 v182, a54\n" \
M_"v[100:103], a[204:207], a[240:243], v[100:103]\n" \
R_"a[140:143], v28 offset:6144\n" \
"v_accvgpr_read_b32 v183, a55\n" \
M_"v[52:55], a[128:131], a[176:179], v[52:55]\n" \
R_"a[204:207], v29 offset:6144\n" \
C_"v208, v208, v209\n" \
M_"v[52:55], a[192:195], a[240:243], v[52:55]\n" \
X_"a[238:239], v35 offset:4608\n" \
C_"v209, v210, v211\n" \
M_"v[56:59], a[128:131], a[180:183], v[56:59]\n" \
X_"a[168:169], v34 offset:0\n" \
C_"v210, v180, v181\n" \
M_"v[56:59], a[192:195], a[244:247], v[56:59]\n" \
X_"a[170:171], v34 offset:512\n" \
C_"v211, v182, v183\n" \
M_"v[60:63], a[128:131], a[184:187], v[60:63]\n" \
X_"a[232:233], v34 offset:4096\n" \
"v_accvgpr_read_b32 v212, a32\n" \
M_"v[60:63], a[192:195], a[248:251], v[60:63]\n" \
X_"a[234:235], v34 offset:4608\n" \
"v_accvgpr_read_b32 v213, a33\n" \
M_"v[64:67], a[128:131], a[188:191], v[64:67]\n" \
"v_accvgpr_read_b32 v214, a34\n" \
"v_accvgpr_read_b32 v215, a35\n" \
M_"v[64:67], a[192:195], a[252:255], v[64:67]\n" \
R_"a[128:131], v28 offset:0\n" \
"v_accvgpr_read_b32 v180, a48\n" \
M_"v[80:83], a[132:135], a[188:191], v[80:83]\n" \
R_"a[192:195], v29 offset:0\n" \
"v_accvgpr_read_b32 v181, a49\n" \
M_"v[80:83], a[196:199], a[252:255], v[80:83]\n" \
"v_accvgpr_read_b32 v182, a50\n" \
"v_accvgpr_read_b32 v183, a51\n" \
M_"v[76:79], a[132:135], a[184:187], v[76:79]\n" \
C_"v212, v212, v213\n" \
C_"v213, v214, v215\n" \
M_"v[76:79], a[196:199], a[248:251], v[76:79]\n" \
C_"v214, v180, v181\n" \
C_"v215, v182, v183\n" \
M_"v[72:75], a[132:135], a[180:183], v[72:75]\n" \
"v_accvgpr_read_b32 v216, a68\n" \
"v_accvgpr_read_b32 v217, a69\n" \
M_"v[72:75], a[196:199], a[244:247], v[72:75]\n" \
"v_accvgpr_read_b32 v218, a70\n" \
"v_accvgpr_read_b32 v219, a71\n" \
M_"v[68:71], a[132:135], a[176:179], v[68:71]\n" \
"v_accvgpr_read_b32 v180, a84\n" \
"v_accvgpr_read_b32 v181, a85\n" \
M_"v[68:71], a[196:199], a[240:243], v[68:71]\n" \
R_"a[132:135], v28 offset:2048\n" \
R_"a[196:199], v29 offset:2048\n" \
"s_waitcnt lgkmcnt(15)\n" \
M_"a[0:3], a[160:163], a[144:147], 0\n" \
"s_addk_i32 s84, 0x2000\n" \
"v_accvgpr_read_b32 v182, a86\n" \
M_"a[0:3], a[224:227], a[208:211], a[0:3]\n" \
B_"v24, s[76:79], s84 offen offset:2048 lds\n" \
"v_accvgpr_read_b32 v183, a87\n" \
M_"a[4:7], a[164:167], a[144:147], 0\n" \
X_"a[176:177], v36 offset:0\n" \
C_"v216, v216, v217\n" \
M_"a[4:7], a[228:231], a[208:211], a[4:7]\n" \
"s_addk_i32 s84, 0x2000\n" \
C_"v217, v218, v219\n" \
"s_waitcnt lgkmcnt(13)\n" \
M_"a[12:15], a[172:175], a[144:147], 0\n" \
B_"v25, s[76:79], s84 offen offset:3072 lds\n" \
C_"v218, v180, v181\n" \
"s_waitcnt lgkmcnt(9)\n" \
M_"a[12:15], a[236:239], a[208:211], a[12:15]\n" \
".fill (L_pb + 61152 - .) / 4, 4, 0xBF800000\n" \
"L_pp15:\n" \
"s_cbranch_execz L_pret\n" \
X_"a[178:179], v36 offset:1024\n" \
C_"v219, v182, v183\n" \
"s_waitcnt lgkmcnt(8)\n" \
M_"a[8:11], a[144:147], a[168:171], 0\n" \
X_"a[240:241], v36 offset:8192\n" \
"v_accvgpr_read_b32 v220, a64\n" \
"s_waitcnt lgkmcnt(7)\n" \
"s_nop 0\n" \
M_"a[8:11], a[208:211], a[232:235], a[8:11]\n" \
X_"a[242:243], v36 offset:9216\n" \
"v_accvgpr_read_b32 v221, a65\n" \
M_"a[24:27], a[148:151], a[168:171], 0\n" \
X_"a[180:181], v37 offset:0\n" \
"v_accvgpr_read_b32 v222, a66\n" \
M_"a[24:27], a[212:215], a[232:235], a[24:27]\n" \
X_"a[182:183], v37 offset:1024\n" \
"v_accvgpr_read_b32 v223, a67\n" \
M_"a[28:31], a[172:175], a[148:151], 0\n" \
X_"a[244:245], v37 offset:8192\n" \
"v_accvgpr_read_b32 v180, a80\n" \
M_"a[28:31], a[236:239], a[212:215], a[28:31]\n" \
"s_add_u32 s89, s41, 0x2000\n" \
"buffer_store_dwordx4 v[200:203], v20, s[80:83], s89 offen offset:3072 sc1\n" \
M_"a[20:23], a[164:167], a[148:151], 0\n" \
X_"a[246:247], v37 offset:9216\n" \
"v_accvgpr_read_b32 v181, a81\n" \
M_"a[20:23], a[228:231], a[212:215], a[20:23]\n" \
X_"a[184:185], v38 offset:0\n" \
"v_accvgpr_read_b32 v182, a82\n" \
M_"a[16:19], a[160:163], a[148:151], 0\n" \
X_"a[186:187], v38 offset:1024\n" \
"v_accvgpr_read_b32 v183, a83\n" \
M_"a[16:19], a[224:227], a[212:215], a[16:19]\n" \
C_"v220, v220, v221\n" \
C_"v221, v222, v223\n" \
C_"v222, v180, v181\n" \
M_"a[32:35], a[160:163], a[152:155], 0\n" \
"s_add_u32 s88, s41, 0x2000\n" \
"buffer_store_dwordx4 v[204:207], v20, s[80:83], s88 offen offset:2048 sc1\n" \
M_"a[32:35], a[224:227], a[216:219], a[32:35]\n" \
X_"a[248:249], v38 offset:8192\n" \
C_"v223, v182, v183\n" \
M_"a[36:39], a[164:167], a[152:155], 0\n" \
X_"a[250:251], v38 offset:9216\n" \
C_"v224, v128, v129\n" \
M_"a[36:39], a[228:231], a[216:219], a[36:39]\n" \
X_"a[188:189], v39 offset:0\n" \
C_"v225, v130, v131\n" \
M_"a[44:47], a[172:175], a[152:155], 0\n" \
C_"v226, v144, v145\n" \
C_"v227, v146, v147\n" \
C_"v228, v124, v125\n" \
M_"a[44:47], a[236:239], a[216:219], a[44:47]\n" \
X_"a[190:191], v39 offset:1024\n" \
C_"v229, v126, v127\n" \
M_"a[40:43], a[152:155], a[168:171], 0\n" \
"s_add_u32 s89, s41, 0x2000\n" \
"buffer_store_dwordx4 v[208:211], v20, s[80:83], s89 offen offset:1024 sc1\n" \
M_"a[40:43], a[216:219], a[232:235], a[40:43]\n" \
X_"a[252:253], v39 offset:8192\n" \
C_"v230, v140, v141\n" \
M_"a[56:59], a[156:159], a[168:171], 0\n" \
X_"a[254:255], v39 offset:9216\n" \
C_"v231, v142, v143\n" \
M_"a[56:59], a[220:223], a[232:235], a[56:59]\n" \
C_"v232, v120, v121\n" \
C_"v233, v122, v123\n" \
C_"v234, v136, v137\n" \
M_"a[60:63], a[172:175], a[156:159], 0\n" \
C_"v235, v138, v139\n" \
C_"v236, v116, v117\n" \
C_"v237, v118, v119\n" \
M_"a[60:63], a[236:239], a[220:223], a[60:63]\n" \
"s_add_u32 s88, s41, 0x2000\n" \
"buffer_store_dwordx4 v[212:215], v20, s[80:83], s88 offen offset:0 sc1\n" \
M_"a[52:55], a[164:167], a[156:159], 0\n" \
C_"v238, v132, v133\n" \
C_"v239, v134, v135\n" \
C_"v240, v160, v161\n" \
M_"a[52:55], a[228:231], a[220:223], a[52:55]\n" \
C_"v241, v162, v163\n" \
C_"v242, v176, v177\n" \
C_"v243, v178, v179\n" \
M_"a[48:51], a[160:163], a[156:159], 0\n" \
C_"v244, v156, v157\n" \
C_"v245, v158, v159\n" \
C_"v246, v172, v173\n" \
M_"a[48:51], a[224:227], a[220:223], a[48:51]\n" \
C_"v247, v174, v175\n" \
C_"v184, v152, v153\n" \
C_"v185, v154, v155\n" \
M_"a[64:67], a[164:167], a[140:143], 0\n" \
C_"v186, v168, v169\n" \
C_"v187, v170, v171\n" \
C_"v188, v148, v149\n" \
M_"a[64:67], a[228:231], a[204:207], a[64:67]\n" \
C_"v189, v150, v151\n" \
C_"v190, v164, v165\n" \
C_"v191, v166, v167\n" \
M_"a[80:83], a[172:175], a[140:143], 0\n" \
C_"v192, v96, v97\n" \
C_"v193, v98, v99\n" \
C_"v194, v112, v113\n" \
M_"a[80:83], a[236:239], a[204:207], a[80:83]\n" \
C_"v195, v114, v115\n" \
C_"v196, v92, v93\n" \
C_"v197, v94, v95\n" \
M_"a[68:71], a[140:143], a[168:171], 0\n" \
C_"v198, v108, v109\n" \
C_"v199, v110, v111\n" \
C_"v252, v88, v89\n" \
M_"a[68:71], a[204:207], a[232:235], a[68:71]\n" \
C_"v253, v90, v91\n" \
C_"v254, v104, v105\n" \
C_"v255, v106, v107\n" \
"s_waitcnt vmcnt(4) lgkmcnt(0)\n" \
"s_barrier\n" \
M_"v[116:119], a[176:179], a[144:147], 0\n" \
"s_add_u32 m0, s40, 0x0\n" \
"s_nop 0\n" \
"s_add_u32 s85, s39, 0x4000\n" \
"v_accvgpr_write_b32 a84, v252\n" \
M_"v[116:119], a[240:243], a[208:211], v[116:119]\n" \
B_"v20, s[80:83], s85 offen sc1 lds\n" \
"v_accvgpr_write_b32 a85, v253\n" \
M_"v[120:123], a[180:183], a[144:147], 0\n" \
X_"a[160:161], v32 offset:32768\n" \
"v_accvgpr_write_b32 a86, v254\n" \
M_"v[120:123], a[244:247], a[208:211], v[120:123]\n" \
X_"a[162:163], v32 offset:33280\n" \
"v_accvgpr_write_b32 a87, v255\n" \
M_"v[124:127], a[184:187], a[144:147], 0\n" \
"s_add_u32 m0, s40, 0x1000\n" \
"s_add_u32 s85, s39, 0x5000\n" \
C_"v252, v84, v85\n" \
M_"v[124:127], a[248:251], a[208:211], v[124:127]\n" \
B_"v20, s[80:83], s85 offen sc1 lds\n" \
C_"v253, v86, v87\n" \
M_"v[128:131], a[188:191], a[144:147], 0\n" \
X_"a[224:225], v32 offset:36864\n" \
C_"v254, v100, v101\n" \
M_"v[128:131], a[252:255], a[208:211], v[128:131]\n" \
R_"a[144:147], v28 offset:40960\n" \
C_"v255, v102, v103\n" \
M_"v[144:147], a[188:191], a[148:151], 0\n" \
R_"a[208:211], v29 offset:40960\n" \
C_"v200, v64, v65\n" \
M_"v[144:147], a[252:255], a[212:215], v[144:147]\n" \
"s_add_u32 m0, s40, 0x3000\n" \
"s_movk_i32 s84, 0x100\n" \
"s_nop 0\n" \
C_"v201, v66, v67\n" \
M_"v[140:143], a[184:187], a[148:151], 0\n" \
B_"v21, s[76:79], s84 offen offset:0 lds\n" \
C_"v202, v80, v81\n" \
M_"v[140:143], a[248:251], a[212:215], v[140:143]\n" \
X_"a[226:227], v32 offset:37376\n" \
C_"v203, v82, v83\n" \
M_"v[136:139], a[180:183], a[148:151], 0\n" \
C_"v204, v60, v61\n" \
C_"v205, v62, v63\n" \
C_"v206, v76, v77\n" \
M_"v[136:139], a[244:247], a[212:215], v[136:139]\n" \
X_"a[164:165], v33 offset:32768\n" \
C_"v207, v78, v79\n" \
M_"v[132:135], a[176:179], a[148:151], 0\n" \
C_"v208, v56, v57\n" \
C_"v209, v58, v59\n" \
C_"v210, v72, v73\n" \
M_"v[132:135], a[240:243], a[212:215], v[132:135]\n" \
R_"a[148:151], v28 offset:43008\n" \
C_"v211, v74, v75\n" \
M_"v[148:151], a[176:179], a[152:155], 0\n" \
R_"a[212:215], v29 offset:43008\n" \
C_"v212, v52, v53\n" \
M_"v[148:151], a[240:243], a[216:219], v[148:151]\n" \
"s_add_u32 m0, s31, 0x0\n" \
"s_mov_b32 s84, 0x100000\n" \
C_"v213, v54, v55\n" \
M_"v[152:155], a[180:183], a[152:155], 0\n" \
B_"v26, s[68:71], s84 offen offset:0 lds\n" \
C_"v214, v68, v69\n" \
M_"v[152:155], a[244:247], a[216:219], v[152:155]\n" \
"s_addk_i32 s84, 0x7c00\n" \
C_"v215, v70, v71\n" \
M_"v[156:159], a[184:187], a[152:155], 0\n" \
B_"v26, s[68:71], s84 offen offset:1024 lds\n" \
M_"v[156:159], a[248:251], a[216:219], v[156:159]\n" \
X_"a[166:167], v33 offset:33280\n" \
M_"v[160:163], a[188:191], a[152:155], 0\n" \
X_"a[228:229], v33 offset:36864\n" \
M_"v[160:163], a[252:255], a[216:219], v[160:163]\n" \
R_"a[152:155], v28 offset:45056\n" \
M_"v[176:179], a[188:191], a[156:159], 0\n" \
R_"a[216:219], v29 offset:45056\n" \
M_"v[176:179], a[252:255], a[220:223], v[176:179]\n" \
"s_addk_i32 s84, 0x7c00\n" \
M_"v[172:175], a[184:187], a[156:159], 0\n" \
B_"v27, s[68:71], s84 offen offset:2048 lds\n" \
M_"v[172:175], a[248:251], a[220:223], v[172:175]\n" \
X_"a[230:231], v33 offset:37376\n" \
M_"v[168:171], a[180:183], a[156:159], 0\n" \
"s_addk_i32 s84, 0x7c00\n" \
M_"v[168:171], a[244:247], a[220:223], v[168:171]\n" \
B_"v27, s[68:71], s84 offen offset:3072 lds\n" \
M_"v[164:167], a[176:179], a[156:159], 0\n" \
X_"a[172:173], v35 offset:32768\n" \
M_"v[164:167], a[240:243], a[220:223], v[164:167]\n" \
R_"a[156:159], v28 offset:47104\n" \
M_"v[84:87], a[176:179], a[136:139], 0\n" \
R_"a[220:223], v29 offset:47104\n" \
M_"v[84:87], a[240:243], a[200:203], v[84:87]\n" \
"s_add_u32 s89, s41, 0x3000\n" \
"buffer_store_dwordx4 v[192:195], v20, s[80:83], s89 offen offset:3072 sc1\n" \
M_"v[88:91], a[180:183], a[136:139], 0\n" \
X_"a[174:175], v35 offset:33280\n" \
M_"v[88:91], a[244:247], a[200:203], v[88:91]\n" \
X_"a[236:237], v35 offset:36864\n" \
M_"v[92:95], a[184:187], a[136:139], 0\n" \
X_"a[238:239], v35 offset:37376\n" \
M_"v[92:95], a[248:251], a[200:203], v[92:95]\n" \
"s_add_u32 m0, s30, 0x0\n" \
"s_movk_i32 s84, 0x180\n" \
M_"v[96:99], a[188:191], a[136:139], 0\n" \
B_"v24, s[76:79], s84 offen offset:0 lds\n" \
M_"v[96:99], a[252:255], a[200:203], v[96:99]\n" \
R_"a[136:139], v28 offset:36864\n" \
M_"v[112:115], a[188:191], a[140:143], 0\n" \
R_"a[200:203], v29 offset:36864\n" \
M_"v[112:115], a[252:255], a[204:207], v[112:115]\n" \
X_"a[168:169], v34 offset:32768\n" \
M_"v[108:111], a[184:187], a[140:143], 0\n" \
"s_movk_i32 s84, 0x2180\n" \
"s_nop 0\n" \
M_"v[108:111], a[248:251], a[204:207], v[108:111]\n" \
B_"v25, s[76:79], s84 offen offset:1024 lds\n" \
M_"v[104:107], a[180:183], a[140:143], 0\n" \
X_"a[170:171], v34 offset:33280\n" \
M_"v[104:107], a[244:247], a[204:207], v[104:107]\n" \
X_"a[232:233], v34 offset:36864\n" \
M_"v[100:103], a[176:179], a[140:143], 0\n" \
M_"v[100:103], a[240:243], a[204:207], v[100:103]\n" \
R_"a[140:143], v28 offset:38912\n" \
M_"v[52:55], a[176:179], a[128:131], 0\n" \
R_"a[204:207], v29 offset:38912\n" \
M_"v[52:55], a[240:243], a[192:195], v[52:55]\n" \
"s_movk_i32 s84, 0x4180\n" \
M_"v[56:59], a[180:183], a[128:131], 0\n" \
B_"v24, s[76:79], s84 offen offset:2048 lds\n" \
M_"v[56:59], a[244:247], a[192:195], v[56:59]\n" \
"s_movk_i32 s84, 0x6180\n" \
M_"v[60:63], a[184:187], a[128:131], 0\n" \
B_"v25, s[76:79], s84 offen offset:3072 lds\n" \
M_"v[60:63], a[248:251], a[192:195], v[60:63]\n" \
X_"a[234:235], v34 offset:37376\n" \
M_"v[64:67], a[188:191], a[128:131], 0\n" \
M_"v[64:67], a[252:255], a[192:195], v[64:67]\n" \
R_"a[128:131], v28 offset:32768\n" \
M_"v[80:83], a[188:191], a[132:135], 0\n" \
R_"a[192:195], v29 offset:32768\n" \
M_"v[80:83], a[252:255], a[196:199], v[80:83]\n" \
M_"v[76:79], a[184:187], a[132:135], 0\n" \
M_"v[76:79], a[248:251], a[196:199], v[76:79]\n" \
M_"v[72:75], a[180:183], a[132:135], 0\n" \
M_"v[72:75], a[244:247], a[196:199], v[72:75]\n" \
M_"v[68:71], a[176:179], a[132:135], 0\n" \
M_"v[68:71], a[240:243], a[196:199], v[68:71]\n" \
R_"a[132:135], v28 offset:34816\n" \
R_"a[196:199], v29 offset:34816\n" \
"s_waitcnt lgkmcnt(15)\n" \
M_"a[0:3], a[160:163], a[144:147], a[0:3]\n" \
"s_add_u32 m0, s30, 0x1000\n" \
"s_addk_i32 s84, 0x3000\n" \
M_"a[0:3], a[224:227], a[208:211], a[0:3]\n" \
B_"v24, s[76:79], s84 offen offset:0 lds\n" \
M_"a[4:7], a[164:167], a[144:147], a[4:7]\n" \
X_"a[176:177], v36 offset:32768\n" \
"v_mov_b32_e64 v11, 0\n" \
M_"a[4:7], a[228:231], a[208:211], a[4:7]\n" \
X_"a[178:179], v36 offset:33792\n" \
"s_and_b32 s5, s2, 7\n" \
"s_waitcnt lgkmcnt(15)\n" \
M_"a[12:15], a[172:175], a[144:147], a[12:15]\n" \
X_"a[240:241], v36 offset:40960\n" \
"s_lshl_b32 s6, s3, 1\n" \
"s_waitcnt lgkmcnt(15)\n" \
M_"a[12:15], a[236:239], a[208:211], a[12:15]\n" \
"s_add_u32 s88, s41, 0x3000\n" \
"buffer_store_dwordx4 v[196:199], v20, s[80:83], s88 offen offset:2048 sc1\n" \
"s_waitcnt lgkmcnt(11)\n" \
M_"a[8:11], a[144:147], a[168:171], a[8:11]\n" \
X_"a[242:243], v36 offset:41984\n" \
"s_lshl_b32 s4, s5, 2\n" \
"s_waitcnt lgkmcnt(8)\n" \
M_"a[8:11], a[208:211], a[232:235], a[8:11]\n" \
X_"a[180:181], v37 offset:32768\n" \
"s_bfe_u32 s7, s2, 0x20003\n" \
M_"a[24:27], a[148:151], a[168:171], a[24:27]\n" \
X_"a[182:183], v37 offset:33792\n" \
"s_or_b32 s12, s4, s7\n" \
M_"a[24:27], a[212:215], a[232:235], a[24:27]\n" \
X_"a[244:245], v37 offset:40960\n" \
"s_and_b32 s6, s6, 4\n" \
M_"a[28:31], a[172:175], a[148:151], a[28:31]\n" \
"s_and_b32 s8, s3, 1\n" \
"s_or_b32 s8, s6, s8\n" \
M_"a[28:31], a[236:239], a[212:215], a[28:31]\n" \
"s_addk_i32 s84, 0x2000\n" \
M_"a[20:23], a[164:167], a[148:151], a[20:23]\n" \
B_"v25, s[76:79], s84 offen offset:1024 lds\n" \
M_"a[20:23], a[228:231], a[212:215], a[20:23]\n" \
X_"a[246:247], v37 offset:41984\n" \
"s_lshl_b32 s6, s12, 4\n" \
M_"a[16:19], a[160:163], a[148:151], a[16:19]\n" \
X_"a[184:185], v38 offset:32768\n" \
"s_lshl_b32 s9, s3, 2\n" \
M_"a[16:19], a[224:227], a[212:215], a[16:19]\n" \
X_"a[186:187], v38 offset:33792\n" \
"s_or_b32 s6, s6, s9\n" \
M_"a[32:35], a[160:163], a[152:155], a[32:35]\n" \
"s_addk_i32 s84, 0x2000\n" \
M_"a[32:35], a[224:227], a[216:219], a[32:35]\n" \
B_"v24, s[76:79], s84 offen offset:2048 lds\n" \
M_"a[36:39], a[164:167], a[152:155], a[36:39]\n" \
X_"a[248:249], v38 offset:40960\n" \
"s_lshr_b32 s9, s2, 5\n" \
M_"a[36:39], a[228:231], a[216:219], a[36:39]\n" \
X_"a[250:251], v38 offset:41984\n" \
"s_mul_i32 s10, s9, 0xa00\n" \
M_"a[44:47], a[172:175], a[152:155], a[44:47]\n" \
X_"a[188:189], v39 offset:32768\n" \
"s_add_i32 s10, s10, 0x5bc1000\n" \
M_"a[44:47], a[236:239], a[216:219], a[44:47]\n" \
X_"a[190:191], v39 offset:33792\n" \
"s_or_b32 s6, s6, s10\n" \
M_"a[40:43], a[152:155], a[168:171], a[40:43]\n" \
"s_add_u32 s89, s41, 0x3000\n" \
"buffer_store_dwordx4 a[84:87], v20, s[80:83], s89 offen offset:1024 sc1\n" \
M_"a[40:43], a[216:219], a[232:235], a[40:43]\n" \
X_"a[252:253], v39 offset:40960\n" \
"s_lshl_b32 s13, s9, 3\n" \
M_"a[56:59], a[156:159], a[168:171], a[56:59]\n" \
X_"a[254:255], v39 offset:41984\n" \
"v_writelane_b32 v11, s6, 0\n" \
M_"a[56:59], a[220:223], a[232:235], a[56:59]\n" \
"s_or_b32 s6, s13, s5\n" \
"v_writelane_b32 v11, s10, 1\n" \
M_"a[60:63], a[172:175], a[156:159], a[60:63]\n" \
"s_lshl_b32 s6, s6, 10\n" \
"s_lshl_b32 s10, s3, 8\n" \
M_"a[60:63], a[236:239], a[220:223], a[60:63]\n" \
"s_addk_i32 s84, 0x2000\n" \
M_"a[52:55], a[164:167], a[156:159], a[52:55]\n" \
B_"v25, s[76:79], s84 offen offset:3072 lds\n" \
M_"a[52:55], a[228:231], a[220:223], a[52:55]\n" \
"s_or_b32 s6, s6, s10\n" \
"s_or_b32 s10, s6, 0x5c00200\n" \
M_"a[48:51], a[160:163], a[156:159], a[48:51]\n" \
"v_writelane_b32 v11, s10, 2\n" \
"s_lshl_b32 s10, s7, 2\n" \
M_"a[48:51], a[224:227], a[220:223], a[48:51]\n" \
"s_or_b32 s11, s10, s6\n" \
"s_or_b32 s11, s11, 0x5c00000\n" \
M_"a[64:67], a[164:167], a[140:143], a[64:67]\n" \
"v_writelane_b32 v11, s11, 3\n" \
"s_or_b32 s6, s6, 0x5c00080\n" \
M_"a[64:67], a[228:231], a[204:207], a[64:67]\n" \
"v_writelane_b32 v11, s6, 4\n" \
"s_or_b32 s6, s6, s10\n" \
M_"a[80:83], a[172:175], a[140:143], a[80:83]\n" \
"s_add_i32 s10, s3, s2\n" \
"v_writelane_b32 v11, s6, 5\n" \
M_"a[80:83], a[236:239], a[204:207], a[80:83]\n" \
"s_mov_b32 s6, 2.0\n" \
"s_mov_b32 s11, 0x5bc0000\n" \
M_"a[68:71], a[140:143], a[168:171], a[68:71]\n" \
"s_cmp_lg_u32 s10, 0\n" \
"s_cselect_b32 s10, s6, s11\n" \
"s_waitcnt vmcnt(11) lgkmcnt(0)\n" \
M_"a[68:71], a[204:207], a[232:235], a[68:71]\n" \
"s_barrier\n" \
"v_xor_b32_e32 v28, 0x20000, v28\n" \
"v_xor_b32_e32 v29, 0x20000, v29\n" \
M_"v[116:119], a[176:179], a[144:147], v[116:119]\n" \
"s_add_u32 m0, s40, 0x8000\n" \
"s_add_u32 s85, s39, 0x6000\n" \
M_"v[116:119], a[240:243], a[208:211], v[116:119]\n" \
B_"v20, s[80:83], s85 offen sc1 lds\n" \
M_"v[120:123], a[180:183], a[144:147], v[120:123]\n" \
X_"a[160:161], v32 offset:0\n" \
"s_lshr_b32 s14, s2, 3\n" \
M_"v[120:123], a[244:247], a[208:211], v[120:123]\n" \
X_"a[162:163], v32 offset:512\n" \
"v_writelane_b32 v11, s10, 57\n" \
M_"v[124:127], a[184:187], a[144:147], v[124:127]\n" \
"s_add_u32 m0, s40, 0x9000\n" \
"s_add_u32 s85, s39, 0x7000\n" \
M_"v[124:127], a[248:251], a[208:211], v[124:127]\n" \
B_"v20, s[80:83], s85 offen sc1 lds\n" \
M_"v[128:131], a[188:191], a[144:147], v[128:131]\n" \
X_"a[224:225], v32 offset:4096\n" \
"s_lshl_b32 s10, s5, 5\n" \
M_"v[128:131], a[252:255], a[208:211], v[128:131]\n" \
R_"a[144:147], v28 offset:8192\n" \
"s_min_u32 s15, s3, 2\n" \
M_"v[144:147], a[188:191], a[148:151], v[144:147]\n" \
R_"a[208:211], v29 offset:8192\n" \
"s_mul_i32 s11, s7, 0x120\n" \
M_"v[144:147], a[252:255], a[212:215], v[144:147]\n" \
"s_add_u32 m0, s40, 0xb000\n" \
"s_movk_i32 s84, 0x180\n" \
M_"v[140:143], a[184:187], a[148:151], v[140:143]\n" \
B_"v21, s[76:79], s84 offen offset:0 lds\n" \
M_"v[140:143], a[248:251], a[212:215], v[140:143]\n" \
X_"a[226:227], v32 offset:4608\n" \
M_"v[136:139], a[180:183], a[148:151], v[136:139]\n" \
"s_add_u32 s88, s41, 0x3000\n" \
"buffer_store_dwordx4 v[252:255], v20, s[80:83], s88 offen offset:0 sc1\n" \
M_"v[136:139], a[244:247], a[212:215], v[136:139]\n" \
X_"a[164:165], v33 offset:0\n" \
M_"v[132:135], a[176:179], a[148:151], v[132:135]\n" \
"s_add_u32 s16, s22, s11\n" \
"s_addc_u32 s17, s23, 0\n" \
"s_nop 0\n" \
M_"v[132:135], a[240:243], a[212:215], v[132:135]\n" \
R_"a[148:151], v28 offset:10240\n" \
M_"v[148:151], a[176:179], a[152:155], v[148:151]\n" \
R_"a[212:215], v29 offset:10240\n" \
M_"v[148:151], a[240:243], a[216:219], v[148:151]\n" \
"s_add_u32 m0, s31, 0x8000\n" \
"s_mov_b32 s84, 0x180000\n" \
M_"v[152:155], a[180:183], a[152:155], v[152:155]\n" \
B_"v26, s[68:71], s84 offen offset:0 lds\n" \
M_"v[152:155], a[244:247], a[216:219], v[152:155]\n" \
X_"a[166:167], v33 offset:512\n" \
M_"v[156:159], a[184:187], a[152:155], v[156:159]\n" \
X_"a[228:229], v33 offset:4096\n" \
M_"v[156:159], a[248:251], a[216:219], v[156:159]\n" \
"s_addk_i32 s84, 0x7c00\n" \
M_"v[160:163], a[188:191], a[152:155], v[160:163]\n" \
B_"v26, s[68:71], s84 offen offset:1024 lds\n" \
M_"v[160:163], a[252:255], a[216:219], v[160:163]\n" \
R_"a[152:155], v28 offset:12288\n" \
M_"v[176:179], a[188:191], a[156:159], v[176:179]\n" \
R_"a[216:219], v29 offset:12288\n" \
M_"v[176:179], a[252:255], a[220:223], v[176:179]\n" \
"s_addk_i32 s84, 0x7c00\n" \
M_"v[172:175], a[184:187], a[156:159], v[172:175]\n" \
B_"v27, s[68:71], s84 offen offset:2048 lds\n" \
M_"v[172:175], a[248:251], a[220:223], v[172:175]\n" \
X_"a[230:231], v33 offset:4608\n" \
M_"v[168:171], a[180:183], a[156:159], v[168:171]\n" \
X_"a[172:173], v35 offset:0\n" \
M_"v[168:171], a[244:247], a[220:223], v[168:171]\n" \
X_"a[174:175], v35 offset:512\n" \
M_"v[164:167], a[176:179], a[156:159], v[164:167]\n" \
"s_add_u32 s18, s24, s11\n" \
"v_writelane_b32 v11, s16, 9\n" \
"s_addc_u32 s19, s25, 0\n" \
M_"v[164:167], a[240:243], a[220:223], v[164:167]\n" \
R_"a[156:159], v28 offset:14336\n" \
"s_and_b32 s20, s17, 0xffff\n" \
M_"v[84:87], a[176:179], a[136:139], v[84:87]\n" \
R_"a[220:223], v29 offset:14336\n" \
"v_writelane_b32 v11, s20, 10\n" \
M_"v[84:87], a[240:243], a[200:203], v[84:87]\n" \
"s_addk_i32 s84, 0x7c00\n" \
M_"v[88:91], a[180:183], a[136:139], v[88:91]\n" \
B_"v27, s[68:71], s84 offen offset:3072 lds\n" \
M_"v[88:91], a[244:247], a[200:203], v[88:91]\n" \
"s_add_u32 m0, s30, 0x8000\n" \
"s_movk_i32 s84, 0x200\n" \
M_"v[92:95], a[184:187], a[136:139], v[92:95]\n" \
B_"v24, s[76:79], s84 offen offset:0 lds\n" \
M_"v[92:95], a[248:251], a[200:203], v[92:95]\n" \
X_"a[236:237], v35 offset:4096\n" \
"s_and_b32 s20, s19, 0xffff\n" \
M_"v[96:99], a[188:191], a[136:139], v[96:99]\n" \
X_"a[238:239], v35 offset:4608\n" \
"v_writelane_b32 v11, s18, 11\n" \
M_"v[96:99], a[252:255], a[200:203], v[96:99]\n" \
R_"a[136:139], v28 offset:4096\n" \
M_"v[112:115], a[188:191], a[140:143], v[112:115]\n" \
R_"a[200:203], v29 offset:4096\n" \
M_"v[112:115], a[252:255], a[204:207], v[112:115]\n" \
"s_movk_i32 s84, 0x2200\n" \
"s_nop 0\n" \
M_"v[108:111], a[184:187], a[140:143], v[108:111]\n" \
B_"v25, s[76:79], s84 offen offset:1024 lds\n" \
M_"v[108:111], a[248:251], a[204:207], v[108:111]\n" \
"s_add_u32 s89, s41, 0x1000\n" \
"buffer_store_dwordx4 v[200:203], v20, s[80:83], s89 offen offset:3072 sc1\n" \
M_"v[104:107], a[180:183], a[140:143], v[104:107]\n" \
X_"a[168:169], v34 offset:0\n" \
M_"v[104:107], a[244:247], a[204:207], v[104:107]\n" \
X_"a[170:171], v34 offset:512\n" \
M_"v[100:103], a[176:179], a[140:143], v[100:103]\n" \
X_"a[232:233], v34 offset:4096\n" \
M_"v[100:103], a[240:243], a[204:207], v[100:103]\n" \
R_"a[140:143], v28 offset:6144\n" \
M_"v[52:55], a[176:179], a[128:131], v[52:55]\n" \
R_"a[204:207], v29 offset:6144\n" \
M_"v[52:55], a[240:243], a[192:195], v[52:55]\n" \
"s_movk_i32 s84, 0x4200\n" \
M_"v[56:59], a[180:183], a[128:131], v[56:59]\n" \
B_"v24, s[76:79], s84 offen offset:2048 lds\n" \
M_"v[56:59], a[244:247], a[192:195], v[56:59]\n" \
"s_movk_i32 s84, 0x6200\n" \
M_"v[60:63], a[184:187], a[128:131], v[60:63]\n" \
B_"v25, s[76:79], s84 offen offset:3072 lds\n" \
M_"v[60:63], a[248:251], a[192:195], v[60:63]\n" \
X_"a[234:235], v34 offset:4608\n" \
M_"v[64:67], a[188:191], a[128:131], v[64:67]\n" \
"s_cmpk_lt_u32 s3, 3\n" \
"s_cselect_b32 s16, s16, s18\n" \
M_"v[64:67], a[252:255], a[192:195], v[64:67]\n" \
R_"a[128:131], v28 offset:0\n" \
"s_movk_i32 s18, 0xfff\n" \
M_"v[80:83], a[188:191], a[132:135], v[80:83]\n" \
R_"a[192:195], v29 offset:0\n" \
"v_writelane_b32 v11, s20, 12\n" \
M_"v[80:83], a[252:255], a[196:199], v[80:83]\n" \
"s_and_b32 s20, s29, 0xffff\n" \
"v_writelane_b32 v11, s28, 13\n" \
M_"v[76:79], a[184:187], a[132:135], v[76:79]\n" \
"v_writelane_b32 v11, s20, 14\n" \
"v_writelane_b32 v11, s16, 15\n" \
M_"v[76:79], a[248:251], a[196:199], v[76:79]\n" \
"s_cmpk_lt_u32 s3, 3\n" \
"s_cselect_b32 s16, s17, s19\n" \
M_"v[72:75], a[180:183], a[132:135], v[72:75]\n" \
"s_and_b32 s16, s16, 0xffff\n" \
"v_writelane_b32 v11, s16, 16\n" \
M_"v[72:75], a[244:247], a[196:199], v[72:75]\n" \
"s_mul_i32 s16, s5, 0x48\n" \
"s_add_i32 s17, s16, 0x47\n" \
M_"v[68:71], a[176:179], a[132:135], v[68:71]\n" \
"s_cmpk_lt_u32 s3, 3\n" \
"s_cselect_b32 s17, s17, s18\n" \
M_"v[68:71], a[240:243], a[196:199], v[68:71]\n" \
R_"a[132:135], v28 offset:2048\n" \
R_"a[196:199], v29 offset:2048\n" \
"s_waitcnt lgkmcnt(15)\n" \
M_"a[0:3], a[160:163], a[144:147], a[0:3]\n" \
"s_add_u32 m0, s30, 0x9000\n" \
"s_addk_i32 s84, 0x3000\n" \
M_"a[0:3], a[224:227], a[208:211], a[0:3]\n" \
B_"v24, s[76:79], s84 offen offset:0 lds\n" \
M_"a[4:7], a[164:167], a[144:147], a[4:7]\n" \
X_"a[176:177], v36 offset:0\n" \
"s_mulk_i32 s17, 0x480\n" \
M_"a[4:7], a[228:231], a[208:211], a[4:7]\n" \
".fill (L_pb + 65248 - .) / 4, 4, 0xBF800000\n" \
"L_pp16:\n" \
"s_cbranch_execz L_pret\n" \
X_"a[178:179], v36 offset:1024\n" \
"s_addk_i32 s17, 0x120\n" \
M_"a[12:15], a[172:175], a[144:147], a[12:15]\n" \
X_"a[240:241], v36 offset:8192\n" \
"v_writelane_b32 v11, s17, 17\n" \
"s_waitcnt lgkmcnt(15)\n" \
M_"a[12:15], a[236:239], a[208:211], a[12:15]\n" \
"s_add_u32 s88, s41, 0x1000\n" \
"buffer_store_dwordx4 v[204:207], v20, s[80:83], s88 offen offset:2048 sc1\n" \
"s_waitcnt lgkmcnt(11)\n" \
M_"a[8:11], a[144:147], a[168:171], a[8:11]\n" \
X_"a[242:243], v36 offset:9216\n" \
"s_lshl_b32 s17, s3, 5\n" \
"s_waitcnt lgkmcnt(8)\n" \
M_"a[8:11], a[208:211], a[232:235], a[8:11]\n" \
X_"a[180:181], v37 offset:0\n" \
"s_add_i32 s16, s16, s17\n" \
M_"a[24:27], a[148:151], a[168:171], a[24:27]\n" \
X_"a[182:183], v37 offset:1024\n" \
"s_mulk_i32 s16, 0x480\n" \
M_"a[24:27], a[212:215], a[232:235], a[24:27]\n" \
X_"a[244:245], v37 offset:8192\n" \
"s_mul_i32 s17, s12, 0x24000\n" \
M_"a[28:31], a[172:175], a[148:151], a[28:31]\n" \
"s_add_i32 s18, s17, 0x1b000\n" \
M_"a[28:31], a[236:239], a[212:215], a[28:31]\n" \
"s_addk_i32 s84, 0x2000\n" \
M_"a[20:23], a[164:167], a[148:151], a[20:23]\n" \
B_"v25, s[76:79], s84 offen offset:1024 lds\n" \
M_"a[20:23], a[228:231], a[212:215], a[20:23]\n" \
X_"a[246:247], v37 offset:9216\n" \
M_"a[16:19], a[160:163], a[148:151], a[16:19]\n" \
X_"a[184:185], v38 offset:0\n" \
M_"a[16:19], a[224:227], a[212:215], a[16:19]\n" \
X_"a[186:187], v38 offset:1024\n" \
M_"a[32:35], a[160:163], a[152:155], a[32:35]\n" \
"s_addk_i32 s84, 0x2000\n" \
M_"a[32:35], a[224:227], a[216:219], a[32:35]\n" \
B_"v24, s[76:79], s84 offen offset:2048 lds\n" \
M_"a[36:39], a[164:167], a[152:155], a[36:39]\n" \
X_"a[248:249], v38 offset:8192\n" \
M_"a[36:39], a[228:231], a[216:219], a[36:39]\n" \
X_"a[250:251], v38 offset:9216\n" \
M_"a[44:47], a[172:175], a[152:155], a[44:47]\n" \
X_"a[188:189], v39 offset:0\n" \
M_"a[44:47], a[236:239], a[216:219], a[44:47]\n" \
X_"a[190:191], v39 offset:1024\n" \
M_"a[40:43], a[152:155], a[168:171], a[40:43]\n" \
"s_add_u32 s89, s41, 0x1000\n" \
"buffer_store_dwordx4 v[208:211], v20, s[80:83], s89 offen offset:1024 sc1\n" \
M_"a[40:43], a[216:219], a[232:235], a[40:43]\n" \
X_"a[252:253], v39 offset:8192\n" \
M_"a[56:59], a[156:159], a[168:171], a[56:59]\n" \
X_"a[254:255], v39 offset:9216\n" \
M_"a[56:59], a[220:223], a[232:235], a[56:59]\n" \
"s_cmpk_lt_u32 s3, 3\n" \
"s_cselect_b32 s16, s16, s18\n" \
M_"a[60:63], a[172:175], a[156:159], a[60:63]\n" \
"s_xor_b32 s18, s3, 2\n" \
"v_writelane_b32 v11, s16, 18\n" \
M_"a[60:63], a[236:239], a[220:223], a[60:63]\n" \
"s_addk_i32 s84, 0x2000\n" \
M_"a[52:55], a[164:167], a[156:159], a[52:55]\n" \
B_"v25, s[76:79], s84 offen offset:3072 lds\n" \
M_"a[52:55], a[228:231], a[220:223], a[52:55]\n" \
"s_mul_i32 s16, s15, 0x2400\n" \
"s_mul_i32 s15, s15, 0x9000\n" \
M_"a[48:51], a[160:163], a[156:159], a[48:51]\n" \
"s_bitset1_b32 s16, 17\n" \
"v_writelane_b32 v11, s16, 22\n" \
M_"a[48:51], a[224:227], a[220:223], a[48:51]\n" \
"s_add_i32 s17, s17, s15\n" \
"s_mul_i32 s15, s3, 0x2400\n" \
M_"a[64:67], a[164:167], a[140:143], a[64:67]\n" \
"v_writelane_b32 v11, s17, 29\n" \
"s_add_i32 s16, s15, 0xa400\n" \
M_"a[64:67], a[228:231], a[204:207], a[64:67]\n" \
"s_mov_b32 s17, 0x8000\n" \
M_"a[80:83], a[172:175], a[140:143], a[80:83]\n" \
"s_cmpk_lt_u32 s3, 3\n" \
"s_cselect_b32 s16, s16, s17\n" \
M_"a[80:83], a[236:239], a[204:207], a[80:83]\n" \
"s_mov_b32 s17, 0x1b000\n" \
M_"a[68:71], a[140:143], a[168:171], a[68:71]\n" \
"s_cmp_lg_u32 s18, 0\n" \
"s_cselect_b32 s17, s16, s17\n" \
"s_waitcnt vmcnt(11) lgkmcnt(0)\n" \
M_"a[68:71], a[204:207], a[232:235], a[68:71]\n" \
"s_barrier\n" \
"v_xor_b32_e32 v28, 0x20000, v28\n" \
"v_xor_b32_e32 v29, 0x20000, v29\n" \
M_"v[116:119], a[176:179], a[144:147], v[116:119]\n" \
"s_add_u32 m0, s40, 0x0\n" \
"s_nop 0\n" \
"s_add_u32 s85, s39, 0x8000\n" \
M_"v[116:119], a[240:243], a[208:211], v[116:119]\n" \
B_"v20, s[80:83], s85 offen sc1 lds\n" \
M_"v[120:123], a[180:183], a[144:147], v[120:123]\n" \
X_"a[160:161], v32 offset:32768\n" \
"v_writelane_b32 v11, s16, 23\n" \
M_"v[120:123], a[244:247], a[208:211], v[120:123]\n" \
X_"a[162:163], v32 offset:33280\n" \
"s_or_b32 s16, s15, 0x10000\n" \
M_"v[124:127], a[184:187], a[144:147], v[124:127]\n" \
"s_add_u32 m0, s40, 0x1000\n" \
"s_add_u32 s85, s39, 0x9000\n" \
M_"v[124:127], a[248:251], a[208:211], v[124:127]\n" \
B_"v20, s[80:83], s85 offen sc1 lds\n" \
M_"v[128:131], a[188:191], a[144:147], v[128:131]\n" \
X_"a[224:225], v32 offset:36864\n" \
"v_writelane_b32 v11, s17, 24\n" \
M_"v[128:131], a[252:255], a[208:211], v[128:131]\n" \
R_"a[144:147], v28 offset:8192\n" \
"s_add_i32 s17, s15, 0x19000\n" \
M_"v[144:147], a[188:191], a[148:151], v[144:147]\n" \
R_"a[208:211], v29 offset:8192\n" \
"v_writelane_b32 v11, s16, 25\n" \
M_"v[144:147], a[252:255], a[212:215], v[144:147]\n" \
"s_add_u32 m0, s40, 0x3000\n" \
"s_movk_i32 s84, 0x200\n" \
M_"v[140:143], a[184:187], a[148:151], v[140:143]\n" \
B_"v21, s[76:79], s84 offen offset:0 lds\n" \
M_"v[140:143], a[248:251], a[212:215], v[140:143]\n" \
X_"a[226:227], v32 offset:37376\n" \
"s_mov_b32 s16, 0\n" \
M_"v[136:139], a[180:183], a[148:151], v[136:139]\n" \
"s_add_u32 s88, s41, 0x1000\n" \
"buffer_store_dwordx4 v[212:215], v20, s[80:83], s88 offen offset:0 sc1\n" \
M_"v[136:139], a[244:247], a[212:215], v[136:139]\n" \
X_"a[164:165], v33 offset:32768\n" \
M_"v[132:135], a[176:179], a[148:151], v[132:135]\n" \
"s_cmpk_lt_u32 s3, 3\n" \
"s_cselect_b32 s17, s17, s16\n" \
M_"v[132:135], a[240:243], a[212:215], v[132:135]\n" \
R_"a[148:151], v28 offset:10240\n" \
"s_bitset1_b32 s15, 17\n" \
M_"v[148:151], a[176:179], a[152:155], v[148:151]\n" \
R_"a[212:215], v29 offset:10240\n" \
"v_writelane_b32 v11, s17, 26\n" \
M_"v[148:151], a[240:243], a[216:219], v[148:151]\n" \
"s_add_u32 m0, s31, 0x0\n" \
"s_mov_b32 s84, 0x200000\n" \
M_"v[152:155], a[180:183], a[152:155], v[152:155]\n" \
B_"v26, s[68:71], s84 offen offset:0 lds\n" \
M_"v[152:155], a[244:247], a[216:219], v[152:155]\n" \
X_"a[166:167], v33 offset:33280\n" \
"s_movk_i32 s17, 0x2400\n" \
M_"v[156:159], a[184:187], a[152:155], v[156:159]\n" \
X_"a[228:229], v33 offset:36864\n" \
M_"v[156:159], a[248:251], a[216:219], v[156:159]\n" \
"s_addk_i32 s84, 0x7c00\n" \
M_"v[160:163], a[188:191], a[152:155], v[160:163]\n" \
B_"v26, s[68:71], s84 offen offset:1024 lds\n" \
M_"v[160:163], a[252:255], a[216:219], v[160:163]\n" \
R_"a[152:155], v28 offset:12288\n" \
M_"v[176:179], a[188:191], a[156:159], v[176:179]\n" \
R_"a[216:219], v29 offset:12288\n" \
M_"v[176:179], a[252:255], a[220:223], v[176:179]\n" \
"s_addk_i32 s84, 0x7c00\n" \
M_"v[172:175], a[184:187], a[156:159], v[172:175]\n" \
B_"v27, s[68:71], s84 offen offset:2048 lds\n" \
M_"v[172:175], a[248:251], a[220:223], v[172:175]\n" \
X_"a[230:231], v33 offset:37376\n" \
M_"v[168:171], a[180:183], a[156:159], v[168:171]\n" \
X_"a[172:173], v35 offset:32768\n" \
M_"v[168:171], a[244:247], a[220:223], v[168:171]\n" \
X_"a[174:175], v35 offset:33280\n" \
M_"v[164:167], a[176:179], a[156:159], v[164:167]\n" \
"s_cmpk_lt_u32 s3, 3\n" \
"s_cselect_b32 s15, s15, s17\n" \
M_"v[164:167], a[240:243], a[220:223], v[164:167]\n" \
R_"a[156:159], v28 offset:14336\n" \
"v_writelane_b32 v11, s15, 27\n" \
M_"v[84:87], a[176:179], a[136:139], v[84:87]\n" \
R_"a[220:223], v29 offset:14336\n" \
"s_lshl_b32 s15, s3, 10\n" \
M_"v[84:87], a[240:243], a[200:203], v[84:87]\n" \
"s_addk_i32 s84, 0x7c00\n" \
M_"v[88:91], a[180:183], a[136:139], v[88:91]\n" \
B_"v27, s[68:71], s84 offen offset:3072 lds\n" \
M_"v[88:91], a[244:247], a[200:203], v[88:91]\n" \
"s_add_u32 m0, s30, 0x20000\n" \
"s_movk_i32 s84, 0x280\n" \
M_"v[92:95], a[184:187], a[136:139], v[92:95]\n" \
B_"v24, s[76:79], s84 offen offset:0 lds\n" \
M_"v[92:95], a[248:251], a[200:203], v[92:95]\n" \
X_"a[236:237], v35 offset:36864\n" \
"s_addk_i32 s15, 0x4800\n" \
M_"v[96:99], a[188:191], a[136:139], v[96:99]\n" \
X_"a[238:239], v35 offset:37376\n" \
"v_writelane_b32 v11, s15, 28\n" \
M_"v[96:99], a[252:255], a[200:203], v[96:99]\n" \
R_"a[136:139], v28 offset:4096\n" \
"s_add_i32 s15, s14, 1\n" \
M_"v[112:115], a[188:191], a[140:143], v[112:115]\n" \
R_"a[200:203], v29 offset:4096\n" \
"s_and_b32 s15, s15, 3\n" \
M_"v[112:115], a[252:255], a[204:207], v[112:115]\n" \
"s_movk_i32 s84, 0x2280\n" \
M_"v[108:111], a[184:187], a[140:143], v[108:111]\n" \
B_"v25, s[76:79], s84 offen offset:1024 lds\n" \
M_"v[108:111], a[248:251], a[204:207], v[108:111]\n" \
"s_add_u32 s89, s42, 0x0\n" \
"buffer_store_dwordx4 v[216:219], v20, s[80:83], s89 offen offset:1024 sc1\n" \
M_"v[104:107], a[180:183], a[140:143], v[104:107]\n" \
X_"a[168:169], v34 offset:32768\n" \
"s_or_b32 s15, s15, s4\n" \
M_"v[104:107], a[244:247], a[204:207], v[104:107]\n" \
X_"a[170:171], v34 offset:33280\n" \
"s_lshl_b32 s17, s15, 2\n" \
M_"v[100:103], a[176:179], a[140:143], v[100:103]\n" \
X_"a[232:233], v34 offset:36864\n" \
"s_or_b32 s17, s17, s3\n" \
M_"v[100:103], a[240:243], a[204:207], v[100:103]\n" \
R_"a[140:143], v28 offset:6144\n" \
"s_mul_i32 s17, s17, 0x9000\n" \
M_"v[52:55], a[176:179], a[128:131], v[52:55]\n" \
R_"a[204:207], v29 offset:6144\n" \
"v_writelane_b32 v11, s17, 30\n" \
M_"v[52:55], a[240:243], a[192:195], v[52:55]\n" \
"s_movk_i32 s84, 0x4280\n" \
M_"v[56:59], a[180:183], a[128:131], v[56:59]\n" \
B_"v24, s[76:79], s84 offen offset:2048 lds\n" \
M_"v[56:59], a[244:247], a[192:195], v[56:59]\n" \
"s_movk_i32 s84, 0x6280\n" \
M_"v[60:63], a[184:187], a[128:131], v[60:63]\n" \
B_"v25, s[76:79], s84 offen offset:3072 lds\n" \
M_"v[60:63], a[248:251], a[192:195], v[60:63]\n" \
X_"a[234:235], v34 offset:37376\n" \
"s_lshl_b32 s17, s12, 2\n" \
M_"v[64:67], a[188:191], a[128:131], v[64:67]\n" \
"s_or_b32 s17, s17, s3\n" \
"s_add_i32 s14, s14, -1\n" \
M_"v[64:67], a[252:255], a[192:195], v[64:67]\n" \
R_"a[128:131], v28 offset:0\n" \
"s_xor_b32 s17, s17, 8\n" \
M_"v[80:83], a[188:191], a[132:135], v[80:83]\n" \
R_"a[192:195], v29 offset:0\n" \
"s_and_b32 s14, s14, 3\n" \
M_"v[80:83], a[252:255], a[196:199], v[80:83]\n" \
"s_mul_i32 s17, s17, 0x9000\n" \
"s_or_b32 s14, s14, s4\n" \
M_"v[76:79], a[184:187], a[132:135], v[76:79]\n" \
"v_writelane_b32 v11, s17, 31\n" \
"s_lshl_b32 s17, s14, 2\n" \
M_"v[76:79], a[248:251], a[196:199], v[76:79]\n" \
"s_or_b32 s17, s17, s3\n" \
"s_mul_i32 s17, s17, 0x9000\n" \
M_"v[72:75], a[180:183], a[132:135], v[72:75]\n" \
"v_writelane_b32 v11, s17, 32\n" \
"s_or_b32 s17, s8, s13\n" \
M_"v[72:75], a[244:247], a[196:199], v[72:75]\n" \
"s_mul_i32 s17, s17, 0x24800\n" \
"s_lshl_b32 s15, s15, 12\n" \
M_"v[68:71], a[176:179], a[132:135], v[68:71]\n" \
"s_add_i32 s18, s17, 0x3004800\n" \
"s_lshl_b32 s12, s12, 12\n" \
M_"v[68:71], a[240:243], a[196:199], v[68:71]\n" \
R_"a[132:135], v28 offset:2048\n" \
R_"a[196:199], v29 offset:2048\n" \
"s_waitcnt lgkmcnt(15)\n" \
M_"a[0:3], a[160:163], a[144:147], a[0:3]\n" \
"s_add_u32 m0, s30, 0x21000\n" \
"s_addk_i32 s84, 0x3000\n" \
M_"a[0:3], a[224:227], a[208:211], a[0:3]\n" \
B_"v24, s[76:79], s84 offen offset:0 lds\n" \
M_"a[4:7], a[164:167], a[144:147], a[4:7]\n" \
X_"a[176:177], v36 offset:32768\n" \
"s_add_i32 s19, s18, s15\n" \
M_"a[4:7], a[228:231], a[208:211], a[4:7]\n" \
X_"a[178:179], v36 offset:33792\n" \
"v_writelane_b32 v11, s19, 33\n" \
M_"a[12:15], a[172:175], a[144:147], a[12:15]\n" \
X_"a[240:241], v36 offset:40960\n" \
"s_xor_b32 s12, s12, 0x2000\n" \
"s_waitcnt lgkmcnt(15)\n" \
M_"a[12:15], a[236:239], a[208:211], a[12:15]\n" \
"s_add_u32 s88, s42, 0x0\n" \
"buffer_store_dwordx4 v[220:223], v20, s[80:83], s88 offen offset:0 sc1\n" \
"s_waitcnt lgkmcnt(11)\n" \
M_"a[8:11], a[144:147], a[168:171], a[8:11]\n" \
X_"a[242:243], v36 offset:41984\n" \
"s_lshl_b32 s14, s14, 12\n" \
"s_waitcnt lgkmcnt(8)\n" \
M_"a[8:11], a[208:211], a[232:235], a[8:11]\n" \
X_"a[180:181], v37 offset:32768\n" \
"s_sub_i32 s13, s8, s13\n" \
M_"a[24:27], a[148:151], a[168:171], a[24:27]\n" \
X_"a[182:183], v37 offset:33792\n" \
"s_add_i32 s19, s18, s12\n" \
M_"a[24:27], a[212:215], a[232:235], a[24:27]\n" \
X_"a[244:245], v37 offset:40960\n" \
"v_writelane_b32 v11, s19, 34\n" \
M_"a[28:31], a[172:175], a[148:151], a[28:31]\n" \
"s_add_i32 s18, s18, s14\n" \
"s_mul_i32 s13, s13, 0x24800\n" \
M_"a[28:31], a[236:239], a[212:215], a[28:31]\n" \
"s_addk_i32 s84, 0x2000\n" \
M_"a[20:23], a[164:167], a[148:151], a[20:23]\n" \
B_"v25, s[76:79], s84 offen offset:1024 lds\n" \
M_"a[20:23], a[228:231], a[212:215], a[20:23]\n" \
X_"a[246:247], v37 offset:41984\n" \
"v_writelane_b32 v11, s18, 35\n" \
M_"a[16:19], a[160:163], a[148:151], a[16:19]\n" \
X_"a[184:185], v38 offset:32768\n" \
"s_add_i32 s18, s13, 0x4120800\n" \
M_"a[16:19], a[224:227], a[212:215], a[16:19]\n" \
X_"a[186:187], v38 offset:33792\n" \
"s_add_i32 s15, s18, s15\n" \
M_"a[32:35], a[160:163], a[152:155], a[32:35]\n" \
"s_addk_i32 s84, 0x2000\n" \
M_"a[32:35], a[224:227], a[216:219], a[32:35]\n" \
B_"v24, s[76:79], s84 offen offset:2048 lds\n" \
M_"a[36:39], a[164:167], a[152:155], a[36:39]\n" \
X_"a[248:249], v38 offset:40960\n" \
"v_writelane_b32 v11, s15, 36\n" \
M_"a[36:39], a[228:231], a[216:219], a[36:39]\n" \
X_"a[250:251], v38 offset:41984\n" \
"s_add_i32 s12, s18, s12\n" \
M_"a[44:47], a[172:175], a[152:155], a[44:47]\n" \
X_"a[188:189], v39 offset:32768\n" \
"v_writelane_b32 v11, s12, 37\n" \
M_"a[44:47], a[236:239], a[216:219], a[44:47]\n" \
X_"a[190:191], v39 offset:33792\n" \
"s_add_i32 s18, s18, s14\n" \
M_"a[40:43], a[152:155], a[168:171], a[40:43]\n" \
"s_add_u32 s89, s42, 0x1000\n" \
"buffer_store_dwordx4 v[224:227], v20, s[80:83], s89 offen offset:3072 sc1\n" \
M_"a[40:43], a[216:219], a[232:235], a[40:43]\n" \
X_"a[252:253], v39 offset:40960\n" \
"s_or_b32 s12, s17, 0x3000000\n" \
M_"a[56:59], a[156:159], a[168:171], a[56:59]\n" \
X_"a[254:255], v39 offset:41984\n" \
"s_lshl_b32 s14, s5, 11\n" \
M_"a[56:59], a[220:223], a[232:235], a[56:59]\n" \
"v_writelane_b32 v11, s18, 38\n" \
"s_add_i32 s15, s12, s14\n" \
M_"a[60:63], a[172:175], a[156:159], a[60:63]\n" \
"v_writelane_b32 v11, s15, 39\n" \
"s_add_i32 s15, s13, 0x411c000\n" \
M_"a[60:63], a[236:239], a[220:223], a[60:63]\n" \
"s_addk_i32 s84, 0x2000\n" \
M_"a[52:55], a[164:167], a[156:159], a[52:55]\n" \
B_"v25, s[76:79], s84 offen offset:3072 lds\n" \
M_"a[52:55], a[228:231], a[220:223], a[52:55]\n" \
"s_add_i32 s18, s15, s14\n" \
"v_writelane_b32 v11, s18, 40\n" \
M_"a[48:51], a[160:163], a[156:159], a[48:51]\n" \
"s_add_i32 s17, s17, 0x3004000\n" \
"v_writelane_b32 v11, s17, 41\n" \
M_"a[48:51], a[224:227], a[220:223], a[48:51]\n" \
"s_add_i32 s13, s13, 0x4120000\n" \
"v_writelane_b32 v11, s13, 42\n" \
M_"a[64:67], a[164:167], a[140:143], a[64:67]\n" \
"s_lshl_b32 s13, s9, 2\n" \
"s_or_b32 s7, s13, s7\n" \
M_"a[64:67], a[228:231], a[204:207], a[64:67]\n" \
"s_mul_i32 s7, s7, 0x90000\n" \
"s_lshl_b32 s13, s3, 14\n" \
M_"a[80:83], a[172:175], a[140:143], a[80:83]\n" \
"s_lshl_b32 s9, s9, 7\n" \
"s_lshl_b32 s8, s8, 4\n" \
M_"a[80:83], a[236:239], a[204:207], a[80:83]\n" \
"s_add_i32 s7, s7, s13\n" \
"s_or_b32 s13, s8, s9\n" \
M_"a[68:71], a[140:143], a[168:171], a[68:71]\n" \
"s_sub_i32 s8, s8, s9\n" \
"s_mulk_i32 s13, 0x480\n" \
"s_waitcnt vmcnt(11) lgkmcnt(0)\n" \
M_"a[68:71], a[204:207], a[232:235], a[68:71]\n" \
"s_barrier\n" \
M_"v[116:119], a[176:179], a[144:147], v[116:119]\n" \
"s_add_u32 m0, s40, 0x8000\n" \
"s_add_u32 s85, s39, 0xa000\n" \
M_"v[116:119], a[240:243], a[208:211], v[116:119]\n" \
B_"v20, s[80:83], s85 offen sc1 lds\n" \
M_"v[120:123], a[180:183], a[144:147], v[120:123]\n" \
X_"a[160:161], v32 offset:0\n" \
"v_accvgpr_read_b32 v212, a108\n" \
M_"v[120:123], a[244:247], a[208:211], v[120:123]\n" \
X_"a[162:163], v32 offset:512\n" \
"v_accvgpr_read_b32 v213, a109\n" \
M_"v[124:127], a[184:187], a[144:147], v[124:127]\n" \
"s_add_u32 m0, s40, 0x9000\n" \
"s_add_u32 s85, s39, 0xb000\n" \
M_"v[124:127], a[248:251], a[208:211], v[124:127]\n" \
B_"v20, s[80:83], s85 offen sc1 lds\n" \
M_"v[128:131], a[188:191], a[144:147], v[128:131]\n" \
X_"a[224:225], v32 offset:4096\n" \
"v_accvgpr_read_b32 v214, a110\n" \
M_"v[128:131], a[252:255], a[208:211], v[128:131]\n" \
R_"a[144:147], v28 offset:40960\n" \
"v_accvgpr_read_b32 v215, a111\n" \
M_"v[144:147], a[188:191], a[148:151], v[144:147]\n" \
R_"a[208:211], v29 offset:40960\n" \
"v_accvgpr_read_b32 v180, a124\n" \
M_"v[144:147], a[252:255], a[212:215], v[144:147]\n" \
"s_add_u32 m0, s40, 0xb000\n" \
"s_movk_i32 s84, 0x280\n" \
"s_nop 0\n" \
M_"v[140:143], a[184:187], a[148:151], v[140:143]\n" \
B_"v21, s[76:79], s84 offen offset:0 lds\n" \
M_"v[140:143], a[248:251], a[212:215], v[140:143]\n" \
X_"a[226:227], v32 offset:4608\n" \
"v_accvgpr_read_b32 v181, a125\n" \
M_"v[136:139], a[180:183], a[148:151], v[136:139]\n" \
"s_add_u32 s88, s42, 0x1000\n" \
"buffer_store_dwordx4 v[228:231], v20, s[80:83], s88 offen offset:2048 sc1\n" \
M_"v[136:139], a[244:247], a[212:215], v[136:139]\n" \
X_"a[164:165], v33 offset:0\n" \
"v_accvgpr_read_b32 v182, a126\n" \
M_"v[132:135], a[176:179], a[148:151], v[132:135]\n" \
"v_accvgpr_read_b32 v183, a127\n" \
C_"v212, v212, v213\n" \
M_"v[132:135], a[240:243], a[212:215], v[132:135]\n" \
R_"a[148:151], v28 offset:43008\n" \
C_"v213, v214, v215\n" \
M_"v[148:151], a[176:179], a[152:155], v[148:151]\n" \
R_"a[212:215], v29 offset:43008\n" \
C_"v214, v180, v181\n" \
M_"v[148:151], a[240:243], a[216:219], v[148:151]\n" \
"s_add_u32 m0, s31, 0x8000\n" \
"s_mov_b32 s84, 0x280000\n" \
M_"v[152:155], a[180:183], a[152:155], v[152:155]\n" \
B_"v26, s[68:71], s84 offen offset:0 lds\n" \
M_"v[152:155], a[244:247], a[216:219], v[152:155]\n" \
X_"a[166:167], v33 offset:512\n" \
C_"v215, v182, v183\n" \
M_"v[156:159], a[184:187], a[152:155], v[156:159]\n" \
X_"a[228:229], v33 offset:4096\n" \
"v_accvgpr_read_b32 v208, a104\n" \
M_"v[156:159], a[248:251], a[216:219], v[156:159]\n" \
"s_addk_i32 s84, 0x7c00\n" \
M_"v[160:163], a[188:191], a[152:155], v[160:163]\n" \
B_"v26, s[68:71], s84 offen offset:1024 lds\n" \
M_"v[160:163], a[252:255], a[216:219], v[160:163]\n" \
R_"a[152:155], v28 offset:45056\n" \
"v_accvgpr_read_b32 v209, a105\n" \
M_"v[176:179], a[188:191], a[156:159], v[176:179]\n" \
R_"a[216:219], v29 offset:45056\n" \
"v_accvgpr_read_b32 v210, a106\n" \
M_"v[176:179], a[252:255], a[220:223], v[176:179]\n" \
"s_addk_i32 s84, 0x7c00\n" \
M_"v[172:175], a[184:187], a[156:159], v[172:175]\n" \
B_"v27, s[68:71], s84 offen offset:2048 lds\n" \
M_"v[172:175], a[248:251], a[220:223], v[172:175]\n" \
X_"a[230:231], v33 offset:4608\n" \
"v_accvgpr_read_b32 v211, a107\n" \
M_"v[168:171], a[180:183], a[156:159], v[168:171]\n" \
X_"a[172:173], v35 offset:0\n" \
"v_accvgpr_read_b32 v180, a120\n" \
M_"v[168:171], a[244:247], a[220:223], v[168:171]\n" \
X_"a[174:175], v35 offset:512\n" \
"v_accvgpr_read_b32 v181, a121\n" \
M_"v[164:167], a[176:179], a[156:159], v[164:167]\n" \
"v_accvgpr_read_b32 v182, a122\n" \
"v_accvgpr_read_b32 v183, a123\n" \
M_"v[164:167], a[240:243], a[220:223], v[164:167]\n" \
R_"a[156:159], v28 offset:47104\n" \
C_"v208, v208, v209\n" \
M_"v[84:87], a[176:179], a[136:139], v[84:87]\n" \
R_"a[220:223], v29 offset:47104\n" \
C_"v209, v210, v211\n" \
M_"v[84:87], a[240:243], a[200:203], v[84:87]\n" \
"s_addk_i32 s84, 0x7c00\n" \
M_"v[88:91], a[180:183], a[136:139], v[88:91]\n" \
B_"v27, s[68:71], s84 offen offset:3072 lds\n" \
M_"v[88:91], a[244:247], a[200:203], v[88:91]\n" \
"s_add_u32 m0, s30, 0x0\n" \
"s_movk_i32 s84, 0x300\n" \
"s_nop 0\n" \
M_"v[92:95], a[184:187], a[136:139], v[92:95]\n" \
B_"v24, s[76:79], s84 offen offset:0 lds\n" \
M_"v[92:95], a[248:251], a[200:203], v[92:95]\n" \
X_"a[236:237], v35 offset:4096\n" \
C_"v210, v180, v181\n" \
M_"v[96:99], a[188:191], a[136:139], v[96:99]\n" \
X_"a[238:239], v35 offset:4608\n" \
C_"v211, v182, v183\n" \
M_"v[96:99], a[252:255], a[200:203], v[96:99]\n" \
R_"a[136:139], v28 offset:36864\n" \
"v_accvgpr_read_b32 v204, a100\n" \
M_"v[112:115], a[188:191], a[140:143], v[112:115]\n" \
R_"a[200:203], v29 offset:36864\n" \
"v_accvgpr_read_b32 v205, a101\n" \
M_"v[112:115], a[252:255], a[204:207], v[112:115]\n" \
"s_movk_i32 s84, 0x2300\n" \
"s_nop 0\n" \
M_"v[108:111], a[184:187], a[140:143], v[108:111]\n" \
B_"v25, s[76:79], s84 offen offset:1024 lds\n" \
M_"v[108:111], a[248:251], a[204:207], v[108:111]\n" \
"s_add_u32 s89, s42, 0x1000\n" \
"buffer_store_dwordx4 v[232:235], v20, s[80:83], s89 offen offset:1024 sc1\n" \
M_"v[104:107], a[180:183], a[140:143], v[104:107]\n" \
X_"a[168:169], v34 offset:0\n" \
"v_accvgpr_read_b32 v206, a102\n" \
M_"v[104:107], a[244:247], a[204:207], v[104:107]\n" \
X_"a[170:171], v34 offset:512\n" \
"v_accvgpr_read_b32 v207, a103\n" \
M_"v[100:103], a[176:179], a[140:143], v[100:103]\n" \
X_"a[232:233], v34 offset:4096\n" \
"v_accvgpr_read_b32 v180, a116\n" \
M_"v[100:103], a[240:243], a[204:207], v[100:103]\n" \
R_"a[140:143], v28 offset:38912\n" \
"v_accvgpr_read_b32 v181, a117\n" \
M_"v[52:55], a[176:179], a[128:131], v[52:55]\n" \
R_"a[204:207], v29 offset:38912\n" \
"v_accvgpr_read_b32 v182, a118\n" \
M_"v[52:55], a[240:243], a[192:195], v[52:55]\n" \
"s_movk_i32 s84, 0x4300\n" \
M_"v[56:59], a[180:183], a[128:131], v[56:59]\n" \
B_"v24, s[76:79], s84 offen offset:2048 lds\n" \
M_"v[56:59], a[244:247], a[192:195], v[56:59]\n" \
"s_movk_i32 s84, 0x6300\n" \
M_"v[60:63], a[184:187], a[128:131], v[60:63]\n" \
B_"v25, s[76:79], s84 offen offset:3072 lds\n" \
M_"v[60:63], a[248:251], a[192:195], v[60:63]\n" \
X_"a[234:235], v34 offset:4608\n" \
"v_accvgpr_read_b32 v183, a119\n" \
M_"v[64:67], a[188:191], a[128:131], v[64:67]\n" \
C_"v204, v204, v205\n" \
C_"v205, v206, v207\n" \
M_"v[64:67], a[252:255], a[192:195], v[64:67]\n" \
R_"a[128:131], v28 offset:32768\n" \
C_"v206, v180, v181\n" \
M_"v[80:83], a[188:191], a[132:135], v[80:83]\n" \
R_"a[192:195], v29 offset:32768\n" \
C_"v207, v182, v183\n" \
M_"v[80:83], a[252:255], a[196:199], v[80:83]\n" \
"v_accvgpr_read_b32 v200, a96\n" \
"v_accvgpr_read_b32 v201, a97\n" \
M_"v[76:79], a[184:187], a[132:135], v[76:79]\n" \
"v_accvgpr_read_b32 v202, a98\n" \
"v_accvgpr_read_b32 v203, a99\n" \
M_"v[76:79], a[248:251], a[196:199], v[76:79]\n" \
"v_accvgpr_read_b32 v180, a112\n" \
"v_accvgpr_read_b32 v181, a113\n" \
M_"v[72:75], a[180:183], a[132:135], v[72:75]\n" \
"v_accvgpr_read_b32 v182, a114\n" \
"v_accvgpr_read_b32 v183, a115\n" \
M_"v[72:75], a[244:247], a[196:199], v[72:75]\n" \
C_"v200, v200, v201\n" \
C_"v201, v202, v203\n" \
M_"v[68:71], a[176:179], a[132:135], v[68:71]\n" \
C_"v202, v180, v181\n" \
C_"v203, v182, v183\n" \
M_"v[68:71], a[240:243], a[196:199], v[68:71]\n" \
R_"a[132:135], v28 offset:34816\n" \
R_"a[196:199], v29 offset:34816\n" \
"s_waitcnt lgkmcnt(15)\n" \
M_"a[0:3], a[160:163], a[144:147], a[0:3]\n" \
"s_add_u32 m0, s30, 0x1000\n" \
"s_addk_i32 s84, 0x3000\n" \
M_"a[0:3], a[224:227], a[208:211], a[0:3]\n" \
B_"v24, s[76:79], s84 offen offset:0 lds\n" \
M_"a[4:7], a[164:167], a[144:147], a[4:7]\n" \
X_"a[176:177], v36 offset:0\n" \
"v_accvgpr_read_b32 v196, a76\n" \
M_"a[4:7], a[228:231], a[208:211], a[4:7]\n" \
X_"a[178:179], v36 offset:1024\n" \
"v_accvgpr_read_b32 v197, a77\n" \
M_"a[12:15], a[172:175], a[144:147], a[12:15]\n" \
X_"a[240:241], v36 offset:8192\n" \
".fill (L_pb + 69344 - .) / 4, 4, 0xBF800000\n" \
"L_pp17:\n" \
"s_cbranch_execz L_pret\n" \
"v_accvgpr_read_b32 v198, a78\n" \
"s_waitcnt lgkmcnt(15)\n" \
M_"a[12:15], a[236:239], a[208:211], a[12:15]\n" \
"s_add_u32 s88, s42, 0x1000\n" \
"buffer_store_dwordx4 v[236:239], v20, s[80:83], s88 offen offset:0 sc1\n" \
"s_waitcnt lgkmcnt(11)\n" \
M_"a[8:11], a[144:147], a[168:171], a[8:11]\n" \
X_"a[242:243], v36 offset:9216\n" \
"v_accvgpr_read_b32 v199, a79\n" \
"s_waitcnt lgkmcnt(8)\n" \
M_"a[8:11], a[208:211], a[232:235], a[8:11]\n" \
X_"a[180:181], v37 offset:0\n" \
"v_accvgpr_read_b32 v180, a92\n" \
M_"a[24:27], a[148:151], a[168:171], a[24:27]\n" \
X_"a[182:183], v37 offset:1024\n" \
"v_accvgpr_read_b32 v181, a93\n" \
M_"a[24:27], a[212:215], a[232:235], a[24:27]\n" \
X_"a[244:245], v37 offset:8192\n" \
"v_accvgpr_read_b32 v182, a94\n" \
M_"a[28:31], a[172:175], a[148:151], a[28:31]\n" \
"v_accvgpr_read_b32 v183, a95\n" \
C_"v196, v196, v197\n" \
M_"a[28:31], a[236:239], a[212:215], a[28:31]\n" \
"s_addk_i32 s84, 0x2000\n" \
"s_nop 0\n" \
M_"a[20:23], a[164:167], a[148:151], a[20:23]\n" \
B_"v25, s[76:79], s84 offen offset:1024 lds\n" \
M_"a[20:23], a[228:231], a[212:215], a[20:23]\n" \
X_"a[246:247], v37 offset:9216\n" \
C_"v197, v198, v199\n" \
M_"a[16:19], a[160:163], a[148:151], a[16:19]\n" \
X_"a[184:185], v38 offset:0\n" \
C_"v198, v180, v181\n" \
M_"a[16:19], a[224:227], a[212:215], a[16:19]\n" \
X_"a[186:187], v38 offset:1024\n" \
C_"v199, v182, v183\n" \
M_"a[32:35], a[160:163], a[152:155], a[32:35]\n" \
"s_addk_i32 s84, 0x2000\n" \
"s_nop 0\n" \
M_"a[32:35], a[224:227], a[216:219], a[32:35]\n" \
B_"v24, s[76:79], s84 offen offset:2048 lds\n" \
M_"a[36:39], a[164:167], a[152:155], a[36:39]\n" \
X_"a[248:249], v38 offset:8192\n" \
"v_accvgpr_read_b32 v192, a72\n" \
M_"a[36:39], a[228:231], a[216:219], a[36:39]\n" \
X_"a[250:251], v38 offset:9216\n" \
"v_accvgpr_read_b32 v193, a73\n" \
M_"a[44:47], a[172:175], a[152:155], a[44:47]\n" \
X_"a[188:189], v39 offset:0\n" \
"v_accvgpr_read_b32 v194, a74\n" \
M_"a[44:47], a[236:239], a[216:219], a[44:47]\n" \
X_"a[190:191], v39 offset:1024\n" \
"v_accvgpr_read_b32 v195, a75\n" \
M_"a[40:43], a[152:155], a[168:171], a[40:43]\n" \
"s_add_u32 s89, s42, 0x3000\n" \
"buffer_store_dwordx4 v[240:243], v20, s[80:83], s89 offen offset:3072 sc1\n" \
M_"a[40:43], a[216:219], a[232:235], a[40:43]\n" \
X_"a[252:253], v39 offset:8192\n" \
"v_accvgpr_read_b32 v180, a88\n" \
M_"a[56:59], a[156:159], a[168:171], a[56:59]\n" \
X_"a[254:255], v39 offset:9216\n" \
"v_accvgpr_read_b32 v181, a89\n" \
M_"a[56:59], a[220:223], a[232:235], a[56:59]\n" \
"v_accvgpr_read_b32 v182, a90\n" \
"v_accvgpr_read_b32 v183, a91\n" \
M_"a[60:63], a[172:175], a[156:159], a[60:63]\n" \
C_"v192, v192, v193\n" \
C_"v193, v194, v195\n" \
M_"a[60:63], a[236:239], a[220:223], a[60:63]\n" \
"s_addk_i32 s84, 0x2000\n" \
M_"a[52:55], a[164:167], a[156:159], a[52:55]\n" \
B_"v25, s[76:79], s84 offen offset:3072 lds\n" \
M_"a[52:55], a[228:231], a[220:223], a[52:55]\n" \
C_"v194, v180, v181\n" \
C_"v195, v182, v183\n" \
M_"a[48:51], a[160:163], a[156:159], a[48:51]\n" \
"s_mulk_i32 s8, 0x480\n" \
"s_or_b32 s13, s13, s11\n" \
M_"a[48:51], a[224:227], a[220:223], a[48:51]\n" \
"s_or_b32 s8, s8, s11\n" \
"s_and_b32 s11, s2, 1\n" \
M_"a[64:67], a[164:167], a[140:143], a[64:67]\n" \
"s_cmp_lg_u32 s11, 0\n" \
"s_cselect_b32 s12, s15, s12\n" \
M_"a[64:67], a[228:231], a[204:207], a[64:67]\n" \
"s_bfe_u32 s9, s2, 0x20001\n" \
"s_add_i32 s15, s7, 0x4240000\n" \
M_"a[80:83], a[172:175], a[140:143], a[80:83]\n" \
"s_addk_i32 s12, 0x4000\n" \
"s_lshl_b32 s5, s5, 16\n" \
M_"a[80:83], a[236:239], a[204:207], a[80:83]\n" \
"s_xor_b32 s9, s9, s3\n" \
M_"a[68:71], a[140:143], a[168:171], a[68:71]\n" \
"s_cmp_lg_u32 s9, 0\n" \
"s_cselect_b32 s12, s6, s12\n" \
"s_waitcnt vmcnt(11) lgkmcnt(0)\n" \
M_"a[68:71], a[204:207], a[232:235], a[68:71]\n" \
"s_barrier\n" \
"v_mov_b32_e32 v45, s62\n" \
"s_mov_b64 exec, 1\n" \
"buffer_store_dword v45, off, s[80:83], s37 offset:0 sc1\n" \
"s_mov_b64 exec, -1\n" \
"v_xor_b32_e32 v28, 0x20000, v28\n" \
"v_xor_b32_e32 v29, 0x20000, v29\n" \
M_"v[116:119], a[176:179], a[144:147], v[116:119]\n" \
"s_add_u32 m0, s40, 0x0\n" \
"s_add_u32 s85, s39, 0xc000\n" \
M_"v[116:119], a[240:243], a[208:211], v[116:119]\n" \
B_"v20, s[80:83], s85 offen sc1 lds\n" \
M_"v[120:123], a[180:183], a[144:147], v[120:123]\n" \
X_"a[160:161], v32 offset:32768\n" \
"s_add_i32 s5, s15, s5\n" \
"s_nop 0\n" \
M_"v[120:123], a[244:247], a[208:211], v[120:123]\n" \
X_"a[162:163], v32 offset:33280\n" \
"v_writelane_b32 v11, s12, 43\n" \
M_"v[124:127], a[184:187], a[144:147], v[124:127]\n" \
"s_add_u32 m0, s40, 0x1000\n" \
"s_add_u32 s85, s39, 0xd000\n" \
M_"v[124:127], a[248:251], a[208:211], v[124:127]\n" \
B_"v20, s[80:83], s85 offen sc1 lds\n" \
M_"v[128:131], a[188:191], a[144:147], v[128:131]\n" \
X_"a[224:225], v32 offset:36864\n" \
"s_add_i32 s7, s7, 0x42c0000\n" \
M_"v[128:131], a[252:255], a[208:211], v[128:131]\n" \
R_"a[144:147], v28 offset:8192\n" \
"v_writelane_b32 v11, s5, 44\n" \
M_"v[144:147], a[188:191], a[148:151], v[144:147]\n" \
R_"a[208:211], v29 offset:8192\n" \
"s_lshl_b32 s5, s11, 10\n" \
M_"v[144:147], a[252:255], a[212:215], v[144:147]\n" \
"s_add_u32 m0, s40, 0x3000\n" \
"s_movk_i32 s84, 0x300\n" \
M_"v[140:143], a[184:187], a[148:151], v[140:143]\n" \
B_"v21, s[76:79], s84 offen offset:0 lds\n" \
M_"v[140:143], a[248:251], a[212:215], v[140:143]\n" \
X_"a[226:227], v32 offset:37376\n" \
"s_or_b32 s5, s7, s5\n" \
M_"v[136:139], a[180:183], a[148:151], v[136:139]\n" \
"s_cmp_lg_u32 s9, 0\n" \
"s_cselect_b32 s5, s6, s5\n" \
M_"v[136:139], a[244:247], a[212:215], v[136:139]\n" \
X_"a[164:165], v33 offset:32768\n" \
"s_add_i32 s8, s8, 0x21c000\n" \
M_"v[132:135], a[176:179], a[148:151], v[132:135]\n" \
"v_writelane_b32 v11, s5, 45\n" \
"s_add_i32 s5, s13, s10\n" \
M_"v[132:135], a[240:243], a[212:215], v[132:135]\n" \
R_"a[148:151], v28 offset:10240\n" \
"v_writelane_b32 v11, s5, 46\n" \
M_"v[148:151], a[176:179], a[152:155], v[148:151]\n" \
R_"a[212:215], v29 offset:10240\n" \
"s_add_i32 s5, s8, s10\n" \
M_"v[148:151], a[240:243], a[216:219], v[148:151]\n" \
"s_add_u32 m0, s31, 0x0\n" \
"s_mov_b32 s84, 0x300000\n" \
M_"v[152:155], a[180:183], a[152:155], v[152:155]\n" \
B_"v26, s[68:71], s84 offen offset:0 lds\n" \
M_"v[152:155], a[244:247], a[216:219], v[152:155]\n" \
"s_addk_i32 s84, 0x7c00\n" \
"s_nop 0\n" \
M_"v[156:159], a[184:187], a[152:155], v[156:159]\n" \
B_"v26, s[68:71], s84 offen offset:1024 lds\n" \
M_"v[156:159], a[248:251], a[216:219], v[156:159]\n" \
X_"a[166:167], v33 offset:33280\n" \
"v_writelane_b32 v11, s5, 47\n" \
M_"v[160:163], a[188:191], a[152:155], v[160:163]\n" \
X_"a[228:229], v33 offset:36864\n" \
M_"v[160:163], a[252:255], a[216:219], v[160:163]\n" \
R_"a[152:155], v28 offset:12288\n" \
M_"v[176:179], a[188:191], a[156:159], v[176:179]\n" \
R_"a[216:219], v29 offset:12288\n" \
M_"v[176:179], a[252:255], a[220:223], v[176:179]\n" \
"s_addk_i32 s84, 0x7c00\n" \
M_"v[172:175], a[184:187], a[156:159], v[172:175]\n" \
B_"v27, s[68:71], s84 offen offset:2048 lds\n" \
M_"v[172:175], a[248:251], a[220:223], v[172:175]\n" \
X_"a[230:231], v33 offset:37376\n" \
M_"v[168:171], a[180:183], a[156:159], v[168:171]\n" \
"s_addk_i32 s84, 0x7c00\n" \
M_"v[168:171], a[244:247], a[220:223], v[168:171]\n" \
B_"v27, s[68:71], s84 offen offset:3072 lds\n" \
M_"v[164:167], a[176:179], a[156:159], v[164:167]\n" \
X_"a[172:173], v35 offset:32768\n" \
M_"v[164:167], a[240:243], a[220:223], v[164:167]\n" \
R_"a[156:159], v28 offset:14336\n" \
M_"v[84:87], a[176:179], a[136:139], v[84:87]\n" \
R_"a[220:223], v29 offset:14336\n" \
M_"v[84:87], a[240:243], a[200:203], v[84:87]\n" \
"s_add_u32 s88, s42, 0x3000\n" \
"buffer_store_dwordx4 v[244:247], v20, s[80:83], s88 offen offset:2048 sc1\n" \
M_"v[88:91], a[180:183], a[136:139], v[88:91]\n" \
X_"a[174:175], v35 offset:33280\n" \
M_"v[88:91], a[244:247], a[200:203], v[88:91]\n" \
X_"a[236:237], v35 offset:36864\n" \
M_"v[92:95], a[184:187], a[136:139], v[92:95]\n" \
X_"a[238:239], v35 offset:37376\n" \
M_"v[92:95], a[248:251], a[200:203], v[92:95]\n" \
"s_add_u32 m0, s30, 0x8000\n" \
"s_movk_i32 s84, 0x380\n" \
M_"v[96:99], a[188:191], a[136:139], v[96:99]\n" \
B_"v24, s[76:79], s84 offen offset:0 lds\n" \
M_"v[96:99], a[252:255], a[200:203], v[96:99]\n" \
R_"a[136:139], v28 offset:4096\n" \
M_"v[112:115], a[188:191], a[140:143], v[112:115]\n" \
R_"a[200:203], v29 offset:4096\n" \
M_"v[112:115], a[252:255], a[204:207], v[112:115]\n" \
X_"a[168:169], v34 offset:32768\n" \
M_"v[108:111], a[184:187], a[140:143], v[108:111]\n" \
"s_movk_i32 s84, 0x2380\n" \
M_"v[108:111], a[248:251], a[204:207], v[108:111]\n" \
B_"v25, s[76:79], s84 offen offset:1024 lds\n" \
M_"v[104:107], a[180:183], a[140:143], v[104:107]\n" \
X_"a[170:171], v34 offset:33280\n" \
M_"v[104:107], a[244:247], a[204:207], v[104:107]\n" \
X_"a[232:233], v34 offset:36864\n" \
M_"v[100:103], a[176:179], a[140:143], v[100:103]\n" \
"s_cmp_lg_u32 s11, 0\n" \
"s_cselect_b32 s5, s8, s13\n" \
M_"v[100:103], a[240:243], a[204:207], v[100:103]\n" \
R_"a[140:143], v28 offset:6144\n" \
"s_addk_i32 s5, 0x100\n" \
M_"v[52:55], a[176:179], a[128:131], v[52:55]\n" \
R_"a[204:207], v29 offset:6144\n" \
M_"v[52:55], a[240:243], a[192:195], v[52:55]\n" \
"s_movk_i32 s84, 0x4380\n" \
M_"v[56:59], a[180:183], a[128:131], v[56:59]\n" \
B_"v24, s[76:79], s84 offen offset:2048 lds\n" \
M_"v[56:59], a[244:247], a[192:195], v[56:59]\n" \
"s_movk_i32 s84, 0x6380\n" \
M_"v[60:63], a[184:187], a[128:131], v[60:63]\n" \
B_"v25, s[76:79], s84 offen offset:3072 lds\n" \
M_"v[60:63], a[248:251], a[192:195], v[60:63]\n" \
X_"a[234:235], v34 offset:37376\n" \
M_"v[64:67], a[188:191], a[128:131], v[64:67]\n" \
"s_cmp_lg_u32 s9, 0\n" \
"s_cselect_b32 s5, s6, s5\n" \
M_"v[64:67], a[252:255], a[192:195], v[64:67]\n" \
R_"a[128:131], v28 offset:0\n" \
"v_writelane_b32 v11, s5, 48\n" \
M_"v[80:83], a[188:191], a[132:135], v[80:83]\n" \
R_"a[192:195], v29 offset:0\n" \
"s_or_b32 s5, s7, s14\n" \
M_"v[80:83], a[252:255], a[196:199], v[80:83]\n" \
"v_writelane_b32 v11, s5, 56\n" \
"s_lshl_b32 s5, s3, 17\n" \
M_"v[76:79], a[184:187], a[132:135], v[76:79]\n" \
"s_add_i32 s5, s15, s5\n" \
"s_or_b32 s4, s5, s4\n" \
M_"v[76:79], a[248:251], a[196:199], v[76:79]\n" \
"s_or_b32 s4, s4, 0x3840\n" \
"v_writelane_b32 v11, s4, 6\n" \
M_"v[72:75], a[180:183], a[132:135], v[72:75]\n" \
"s_mov_b32 s4, 0xff0000\n" \
M_"v[72:75], a[244:247], a[196:199], v[72:75]\n" \
"s_cmp_lg_u32 s9, 0\n" \
"s_cselect_b32 s4, s16, s4\n" \
M_"v[68:71], a[176:179], a[132:135], v[68:71]\n" \
"v_writelane_b32 v11, s4, 7\n" \
"s_lshl_b32 s4, s2, 16\n" \
M_"v[68:71], a[240:243], a[196:199], v[68:71]\n" \
R_"a[132:135], v28 offset:2048\n" \
R_"a[196:199], v29 offset:2048\n" \
"s_waitcnt lgkmcnt(15)\n" \
M_"a[0:3], a[160:163], a[144:147], a[0:3]\n" \
"s_add_u32 m0, s30, 0x9000\n" \
"s_addk_i32 s84, 0x3000\n" \
M_"a[0:3], a[224:227], a[208:211], a[0:3]\n" \
B_"v24, s[76:79], s84 offen offset:0 lds\n" \
M_"a[4:7], a[164:167], a[144:147], a[4:7]\n" \
X_"a[176:177], v36 offset:32768\n" \
"s_add_i32 s5, s4, 0x10000\n" \
M_"a[4:7], a[228:231], a[208:211], a[4:7]\n" \
X_"a[178:179], v36 offset:33792\n" \
"s_and_b32 s5, s5, 0x70000\n" \
"s_waitcnt lgkmcnt(15)\n" \
M_"a[12:15], a[172:175], a[144:147], a[12:15]\n" \
X_"a[240:241], v36 offset:40960\n" \
"s_add_i32 s5, s15, s5\n" \
"s_waitcnt lgkmcnt(15)\n" \
M_"a[12:15], a[236:239], a[208:211], a[12:15]\n" \
"s_add_u32 s89, s42, 0x3000\n" \
"buffer_store_dwordx4 v[184:187], v20, s[80:83], s89 offen offset:1024 sc1\n" \
"s_waitcnt lgkmcnt(11)\n" \
M_"a[8:11], a[144:147], a[168:171], a[8:11]\n" \
X_"a[242:243], v36 offset:41984\n" \
"v_writelane_b32 v11, s5, 49\n" \
"s_waitcnt lgkmcnt(8)\n" \
M_"a[8:11], a[208:211], a[232:235], a[8:11]\n" \
X_"a[180:181], v37 offset:32768\n" \
"s_add_i32 s5, s4, 0x20000\n" \
M_"a[24:27], a[148:151], a[168:171], a[24:27]\n" \
X_"a[182:183], v37 offset:33792\n" \
"s_and_b32 s5, s5, 0x70000\n" \
M_"a[24:27], a[212:215], a[232:235], a[24:27]\n" \
X_"a[244:245], v37 offset:40960\n" \
"s_add_i32 s5, s15, s5\n" \
M_"a[28:31], a[172:175], a[148:151], a[28:31]\n" \
"s_bitset1_b32 s5, 11\n" \
"v_writelane_b32 v11, s5, 50\n" \
M_"a[28:31], a[236:239], a[212:215], a[28:31]\n" \
"s_addk_i32 s84, 0x2000\n" \
M_"a[20:23], a[164:167], a[148:151], a[20:23]\n" \
B_"v25, s[76:79], s84 offen offset:1024 lds\n" \
M_"a[20:23], a[228:231], a[212:215], a[20:23]\n" \
X_"a[246:247], v37 offset:41984\n" \
"s_add_i32 s5, s4, 0x30000\n" \
M_"a[16:19], a[160:163], a[148:151], a[16:19]\n" \
X_"a[184:185], v38 offset:32768\n" \
"s_and_b32 s5, s5, 0x70000\n" \
M_"a[16:19], a[224:227], a[212:215], a[16:19]\n" \
X_"a[186:187], v38 offset:33792\n" \
"s_add_i32 s5, s15, s5\n" \
M_"a[32:35], a[160:163], a[152:155], a[32:35]\n" \
"s_addk_i32 s84, 0x2000\n" \
M_"a[32:35], a[224:227], a[216:219], a[32:35]\n" \
B_"v24, s[76:79], s84 offen offset:2048 lds\n" \
M_"a[36:39], a[164:167], a[152:155], a[36:39]\n" \
X_"a[248:249], v38 offset:40960\n" \
"s_bitset1_b32 s5, 12\n" \
"s_nop 0\n" \
M_"a[36:39], a[228:231], a[216:219], a[36:39]\n" \
X_"a[250:251], v38 offset:41984\n" \
"v_writelane_b32 v11, s5, 51\n" \
M_"a[44:47], a[172:175], a[152:155], a[44:47]\n" \
X_"a[188:189], v39 offset:32768\n" \
"s_and_b32 s5, s4, 0x70000\n" \
M_"a[44:47], a[236:239], a[216:219], a[44:47]\n" \
X_"a[190:191], v39 offset:33792\n" \
"s_xor_b32 s5, s5, 0x40000\n" \
M_"a[40:43], a[152:155], a[168:171], a[40:43]\n" \
"s_add_u32 s88, s42, 0x3000\n" \
"buffer_store_dwordx4 v[188:191], v20, s[80:83], s88 offen offset:0 sc1\n" \
M_"a[40:43], a[216:219], a[232:235], a[40:43]\n" \
X_"a[252:253], v39 offset:40960\n" \
"s_add_i32 s5, s15, s5\n" \
M_"a[56:59], a[156:159], a[168:171], a[56:59]\n" \
X_"a[254:255], v39 offset:41984\n" \
"s_or_b32 s5, s5, 0x1800\n" \
M_"a[56:59], a[220:223], a[232:235], a[56:59]\n" \
"v_writelane_b32 v11, s5, 52\n" \
"s_add_i32 s5, s4, 0x50000\n" \
M_"a[60:63], a[172:175], a[156:159], a[60:63]\n" \
"s_and_b32 s5, s5, 0x70000\n" \
"s_add_i32 s5, s15, s5\n" \
M_"a[60:63], a[236:239], a[220:223], a[60:63]\n" \
"s_addk_i32 s84, 0x2000\n" \
M_"a[52:55], a[164:167], a[156:159], a[52:55]\n" \
B_"v25, s[76:79], s84 offen offset:3072 lds\n" \
M_"a[52:55], a[228:231], a[220:223], a[52:55]\n" \
"s_bitset1_b32 s5, 13\n" \
"v_writelane_b32 v11, s5, 53\n" \
M_"a[48:51], a[160:163], a[156:159], a[48:51]\n" \
"s_add_i32 s5, s4, 0x60000\n" \
"s_and_b32 s5, s5, 0x70000\n" \
M_"a[48:51], a[224:227], a[220:223], a[48:51]\n" \
"s_add_i32 s4, s4, 0x70000\n" \
"s_add_i32 s5, s15, s5\n" \
M_"a[64:67], a[164:167], a[140:143], a[64:67]\n" \
"s_and_b32 s4, s4, 0x70000\n" \
"s_or_b32 s5, s5, 0x2800\n" \
M_"a[64:67], a[228:231], a[204:207], a[64:67]\n" \
"v_writelane_b32 v11, s5, 54\n" \
"s_add_i32 s15, s15, s4\n" \
M_"a[80:83], a[172:175], a[140:143], a[80:83]\n" \
"s_or_b32 s4, s15, 0x3000\n" \
"v_writelane_b32 v11, s4, 55\n" \
M_"a[80:83], a[236:239], a[204:207], a[80:83]\n" \
"s_movk_i32 s11, 0x4000\n" \
"s_lshl_b32 s4, s2, 2\n" \
M_"a[68:71], a[140:143], a[168:171], a[68:71]\n" \
"s_and_b32 s4, s4, 28\n" \
"s_bfe_u32 s5, s2, 0x20003\n" \
"s_waitcnt vmcnt(11) lgkmcnt(0)\n" \
M_"a[68:71], a[204:207], a[232:235], a[68:71]\n" \
"s_barrier\n" \
"v_xor_b32_e32 v28, 0x20000, v28\n" \
"v_xor_b32_e32 v29, 0x20000, v29\n" \
M_"v[116:119], a[176:179], a[144:147], v[116:119]\n" \
"s_add_u32 m0, s40, 0x8000\n" \
"s_add_u32 s85, s39, 0xe000\n" \
M_"v[116:119], a[240:243], a[208:211], v[116:119]\n" \
B_"v20, s[80:83], s85 offen sc1 lds\n" \
M_"v[120:123], a[180:183], a[144:147], v[120:123]\n" \
X_"a[160:161], v32 offset:0\n" \
"s_or_b32 s4, s4, s5\n" \
M_"v[120:123], a[244:247], a[208:211], v[120:123]\n" \
X_"a[162:163], v32 offset:512\n" \
"s_lshr_b32 s5, s2, 5\n" \
M_"v[124:127], a[184:187], a[144:147], v[124:127]\n" \
"s_add_u32 m0, s40, 0x9000\n" \
"s_add_u32 s85, s39, 0xf000\n" \
M_"v[124:127], a[248:251], a[208:211], v[124:127]\n" \
B_"v20, s[80:83], s85 offen sc1 lds\n" \
M_"v[128:131], a[188:191], a[144:147], v[128:131]\n" \
X_"a[224:225], v32 offset:4096\n" \
"s_lshr_b32 s19, s3, 1\n" \
M_"v[128:131], a[252:255], a[208:211], v[128:131]\n" \
R_"a[144:147], v28 offset:8192\n" \
"s_lshl_b32 s18, s5, 1\n" \
M_"v[144:147], a[188:191], a[148:151], v[144:147]\n" \
R_"a[208:211], v29 offset:8192\n" \
"s_lshl_b32 s6, s4, 20\n" \
M_"v[144:147], a[252:255], a[212:215], v[144:147]\n" \
"s_add_u32 m0, s40, 0xb000\n" \
"s_movk_i32 s84, 0x380\n" \
M_"v[140:143], a[184:187], a[148:151], v[140:143]\n" \
B_"v21, s[76:79], s84 offen offset:0 lds\n" \
M_"v[140:143], a[248:251], a[212:215], v[140:143]\n" \
X_"a[226:227], v32 offset:4608\n" \
"s_lshl_b32 s7, s4, 8\n" \
M_"v[136:139], a[180:183], a[148:151], v[136:139]\n" \
"s_and_b32 s13, s3, 1\n" \
"s_add_i32 s14, s18, s19\n" \
M_"v[136:139], a[244:247], a[212:215], v[136:139]\n" \
X_"a[164:165], v33 offset:0\n" \
M_"v[132:135], a[176:179], a[148:151], v[132:135]\n" \
"s_add_u32 s6, s26, s6\n" \
"s_addc_u32 s9, s27, 0\n" \
M_"v[132:135], a[240:243], a[212:215], v[132:135]\n" \
R_"a[148:151], v28 offset:10240\n" \
M_"v[148:151], a[176:179], a[152:155], v[148:151]\n" \
R_"a[212:215], v29 offset:10240\n" \
M_"v[148:151], a[240:243], a[216:219], v[148:151]\n" \
"s_add_u32 m0, s31, 0x8000\n" \
"s_mov_b32 s84, 0x380000\n" \
M_"v[152:155], a[180:183], a[152:155], v[152:155]\n" \
B_"v26, s[68:71], s84 offen offset:0 lds\n" \
M_"v[152:155], a[244:247], a[216:219], v[152:155]\n" \
"s_addk_i32 s84, 0x7c00\n" \
"s_nop 0\n" \
M_"v[156:159], a[184:187], a[152:155], v[156:159]\n" \
B_"v26, s[68:71], s84 offen offset:1024 lds\n" \
M_"v[156:159], a[248:251], a[216:219], v[156:159]\n" \
X_"a[166:167], v33 offset:512\n" \
M_"v[160:163], a[188:191], a[152:155], v[160:163]\n" \
X_"a[228:229], v33 offset:4096\n" \
M_"v[160:163], a[252:255], a[216:219], v[160:163]\n" \
R_"a[152:155], v28 offset:12288\n" \
M_"v[176:179], a[188:191], a[156:159], v[176:179]\n" \
R_"a[216:219], v29 offset:12288\n" \
M_"v[176:179], a[252:255], a[220:223], v[176:179]\n" \
"s_addk_i32 s84, 0x7c00\n" \
M_"v[172:175], a[184:187], a[156:159], v[172:175]\n" \
B_"v27, s[68:71], s84 offen offset:2048 lds\n" \
M_"v[172:175], a[248:251], a[220:223], v[172:175]\n" \
X_"a[230:231], v33 offset:4608\n" \
M_"v[168:171], a[180:183], a[156:159], v[168:171]\n" \
"s_addk_i32 s84, 0x7c00\n" \
M_"v[168:171], a[244:247], a[220:223], v[168:171]\n" \
B_"v27, s[68:71], s84 offen offset:3072 lds\n" \
M_"v[164:167], a[176:179], a[156:159], v[164:167]\n" \
X_"a[172:173], v35 offset:0\n" \
M_"v[164:167], a[240:243], a[220:223], v[164:167]\n" \
R_"a[156:159], v28 offset:14336\n" \
M_"v[84:87], a[176:179], a[136:139], v[84:87]\n" \
R_"a[220:223], v29 offset:14336\n" \
M_"v[84:87], a[240:243], a[200:203], v[84:87]\n" \
"s_add_u32 s89, s42, 0x0\n" \
"buffer_store_dwordx4 v[192:195], v20, s[80:83], s89 offen offset:2048 sc1\n" \
M_"v[88:91], a[180:183], a[136:139], v[88:91]\n" \
X_"a[174:175], v35 offset:512\n" \
M_"v[88:91], a[244:247], a[200:203], v[88:91]\n" \
X_"a[236:237], v35 offset:4096\n" \
M_"v[92:95], a[184:187], a[136:139], v[92:95]\n" \
X_"a[238:239], v35 offset:4608\n" \
M_"v[92:95], a[248:251], a[200:203], v[92:95]\n" \
"s_add_u32 m0, s30, 0x20000\n" \
"s_movk_i32 s84, 0x400\n" \
M_"v[96:99], a[188:191], a[136:139], v[96:99]\n" \
B_"v24, s[76:79], s84 offen offset:0 lds\n" \
M_"v[96:99], a[252:255], a[200:203], v[96:99]\n" \
R_"a[136:139], v28 offset:4096\n" \
M_"v[112:115], a[188:191], a[140:143], v[112:115]\n" \
R_"a[200:203], v29 offset:4096\n" \
M_"v[112:115], a[252:255], a[204:207], v[112:115]\n" \
X_"a[168:169], v34 offset:0\n" \
M_"v[108:111], a[184:187], a[140:143], v[108:111]\n" \
"s_movk_i32 s84, 0x2400\n" \
M_"v[108:111], a[248:251], a[204:207], v[108:111]\n" \
B_"v25, s[76:79], s84 offen offset:1024 lds\n" \
M_"v[104:107], a[180:183], a[140:143], v[104:107]\n" \
X_"a[170:171], v34 offset:512\n" \
M_"v[104:107], a[244:247], a[204:207], v[104:107]\n" \
X_"a[232:233], v34 offset:4096\n" \
M_"v[100:103], a[176:179], a[140:143], v[100:103]\n" \
"s_add_u32 s8, s6, 0x1000000\n" \
"s_addc_u32 s6, s9, 0\n" \
M_"v[100:103], a[240:243], a[204:207], v[100:103]\n" \
R_"a[140:143], v28 offset:6144\n" \
"s_and_b32 s9, s6, 0xffff\n" \
M_"v[52:55], a[176:179], a[128:131], v[52:55]\n" \
R_"a[204:207], v29 offset:6144\n" \
"s_lshl_b32 s6, s5, 16\n" \
M_"v[52:55], a[240:243], a[192:195], v[52:55]\n" \
"s_movk_i32 s84, 0x4400\n" \
M_"v[56:59], a[180:183], a[128:131], v[56:59]\n" \
B_"v24, s[76:79], s84 offen offset:2048 lds\n" \
M_"v[56:59], a[244:247], a[192:195], v[56:59]\n" \
"s_movk_i32 s84, 0x6400\n" \
"s_nop 0\n" \
M_"v[60:63], a[184:187], a[128:131], v[60:63]\n" \
B_"v25, s[76:79], s84 offen offset:3072 lds\n" \
M_"v[60:63], a[248:251], a[192:195], v[60:63]\n" \
X_"a[234:235], v34 offset:4608\n" \
"s_sub_i32 s10, 0x100000, s6\n" \
M_"v[64:67], a[188:191], a[128:131], v[64:67]\n" \
"s_mov_b32 s6, 0x8000\n" \
M_"v[64:67], a[252:255], a[192:195], v[64:67]\n" \
R_"a[128:131], v28 offset:0\n" \
M_"v[80:83], a[188:191], a[132:135], v[80:83]\n" \
R_"a[192:195], v29 offset:0\n" \
M_"v[80:83], a[252:255], a[196:199], v[80:83]\n" \
"s_cmp_lg_u32 s5, 0\n" \
"s_cselect_b32 s17, s11, s6\n" \
M_"v[76:79], a[184:187], a[132:135], v[76:79]\n" \
"s_lshl_b32 s6, s3, 1\n" \
"s_lshl_b32 s5, s5, 3\n" \
M_"v[76:79], a[248:251], a[196:199], v[76:79]\n" \
"s_and_b32 s6, s6, 4\n" \
"s_or_b32 s12, s5, s6\n" \
M_"v[72:75], a[180:183], a[132:135], v[72:75]\n" \
"s_sub_i32 s5, s13, s5\n" \
"s_or_b32 s5, s5, s6\n" \
M_"v[72:75], a[244:247], a[196:199], v[72:75]\n" \
"s_or_b32 s6, s12, s13\n" \
"s_lshl_b32 s4, s4, 2\n" \
M_"v[68:71], a[176:179], a[132:135], v[68:71]\n" \
"s_mulk_i32 s6, 0x92\n" \
"s_add_i32 s4, s4, 18\n" \
M_"v[68:71], a[240:243], a[196:199], v[68:71]\n" \
R_"a[132:135], v28 offset:2048\n" \
R_"a[196:199], v29 offset:2048\n" \
"s_waitcnt lgkmcnt(15)\n" \
M_"a[0:3], a[160:163], a[144:147], a[0:3]\n" \
"s_add_u32 m0, s30, 0x21000\n" \
"s_addk_i32 s84, 0x3000\n" \
M_"a[0:3], a[224:227], a[208:211], a[0:3]\n" \
B_"v24, s[76:79], s84 offen offset:0 lds\n" \
M_"a[4:7], a[164:167], a[144:147], a[4:7]\n" \
X_"a[176:177], v36 offset:0\n" \
"s_mulk_i32 s5, 0x92\n" \
M_"a[4:7], a[228:231], a[208:211], a[4:7]\n" \
X_"a[178:179], v36 offset:1024\n" \
"s_add_i32 s6, s4, s6\n" \
"s_waitcnt lgkmcnt(15)\n" \
M_"a[12:15], a[172:175], a[144:147], a[12:15]\n" \
X_"a[240:241], v36 offset:8192\n" \
"s_add_i32 s5, s5, s4\n" \
"s_waitcnt lgkmcnt(15)\n" \
M_"a[12:15], a[236:239], a[208:211], a[12:15]\n" \
"s_add_u32 s88, s42, 0x0\n" \
"buffer_store_dwordx4 v[196:199], v20, s[80:83], s88 offen offset:3072 sc1\n" \
"s_waitcnt lgkmcnt(11)\n" \
M_"a[8:11], a[144:147], a[168:171], a[8:11]\n" \
X_"a[242:243], v36 offset:9216\n" \
"s_lshl_b32 s4, s3, 11\n" \
"s_waitcnt lgkmcnt(8)\n" \
M_"a[8:11], a[208:211], a[232:235], a[8:11]\n" \
X_"a[180:181], v37 offset:0\n" \
"s_lshl_b32 s15, s13, 12\n" \
M_"a[24:27], a[148:151], a[168:171], a[24:27]\n" \
X_"a[182:183], v37 offset:1024\n" \
"s_lshl_b32 s16, s19, 14\n" \
M_"a[24:27], a[212:215], a[232:235], a[24:27]\n" \
X_"a[244:245], v37 offset:8192\n" \
"s_mul_i32 s19, s19, 0xe000\n" \
M_"a[28:31], a[172:175], a[148:151], a[28:31]\n" \
"s_mov_b32 s11, 0x20000\n" \
"s_or_b32 s12, s4, 0xc000\n" \
M_"a[28:31], a[236:239], a[212:215], a[28:31]\n" \
"s_addk_i32 s84, 0x2000\n" \
M_"a[20:23], a[164:167], a[148:151], a[20:23]\n" \
B_"v25, s[76:79], s84 offen offset:1024 lds\n" \
M_"a[20:23], a[228:231], a[212:215], a[20:23]\n" \
X_"a[246:247], v37 offset:9216\n" \
".fill (L_pb + 73440 - .) / 4, 4, 0xBF800000\n" \
"L_pp18:\n" \
"s_cbranch_execz L_pret\n" \
"s_lshl_b32 s4, s3, 8\n" \
M_"a[16:19], a[160:163], a[148:151], a[16:19]\n" \
X_"a[184:185], v38 offset:0\n" \
"s_or_b32 s22, s15, s16\n" \
M_"a[16:19], a[224:227], a[212:215], a[16:19]\n" \
X_"a[186:187], v38 offset:1024\n" \
"s_or_b32 s23, s19, s15\n" \
M_"a[32:35], a[160:163], a[152:155], a[32:35]\n" \
"s_addk_i32 s84, 0x2000\n" \
M_"a[32:35], a[224:227], a[216:219], a[32:35]\n" \
B_"v24, s[76:79], s84 offen offset:2048 lds\n" \
M_"a[36:39], a[164:167], a[152:155], a[36:39]\n" \
X_"a[248:249], v38 offset:8192\n" \
"s_lshl_b32 s60, s6, 10\n" \
M_"a[36:39], a[228:231], a[216:219], a[36:39]\n" \
X_"a[250:251], v38 offset:9216\n" \
"s_lshl_b32 s61, s5, 10\n" \
M_"a[44:47], a[172:175], a[152:155], a[44:47]\n" \
X_"a[188:189], v39 offset:0\n" \
"s_lshl_b32 s14, s14, 15\n" \
M_"a[44:47], a[236:239], a[216:219], a[44:47]\n" \
X_"a[190:191], v39 offset:1024\n" \
"s_or_b32 s20, s4, 0x26c00\n" \
M_"a[40:43], a[152:155], a[168:171], a[40:43]\n" \
"s_add_u32 s89, s42, 0x2000\n" \
"buffer_store_dwordx4 v[200:203], v20, s[80:83], s89 offen offset:0 sc1\n" \
M_"a[40:43], a[216:219], a[232:235], a[40:43]\n" \
X_"a[252:253], v39 offset:8192\n" \
"s_or_b32 s13, s22, 0x8000\n" \
M_"a[56:59], a[156:159], a[168:171], a[56:59]\n" \
X_"a[254:255], v39 offset:9216\n" \
"s_add_i32 s22, s22, 0x14000\n" \
M_"a[56:59], a[220:223], a[232:235], a[56:59]\n" \
"s_bitset1_b32 s23, 16\n" \
"s_or_b32 s24, s7, 0x5be8020\n" \
M_"a[60:63], a[172:175], a[156:159], a[60:63]\n" \
"s_and_b32 s57, s27, 0xffff\n" \
"s_mov_b32 s58, 0x6000000\n" \
M_"a[60:63], a[236:239], a[220:223], a[60:63]\n" \
"s_addk_i32 s84, 0x2000\n" \
M_"a[52:55], a[164:167], a[156:159], a[52:55]\n" \
B_"v25, s[76:79], s84 offen offset:3072 lds\n" \
M_"a[52:55], a[228:231], a[220:223], a[52:55]\n" \
"s_mov_b32 s56, s26\n" \
"s_mov_b32 s59, s11\n" \
M_"a[48:51], a[160:163], a[156:159], a[48:51]\n" \
"s_add_i32 s60, s60, 0x3000000\n" \
"s_add_i32 s61, s61, 0x411c000\n" \
M_"a[48:51], a[224:227], a[220:223], a[48:51]\n" \
"s_sub_i32 s19, 14, s18\n" \
"s_mov_b32 s21, 0x71800000\n" \
M_"a[64:67], a[164:167], a[140:143], a[64:67]\n" \
"s_mov_b32 s4, L_gsw-L_gtr\n" \
M_"a[64:67], a[228:231], a[204:207], a[64:67]\n" \
"s_cmp_eq_u32 s19, 0\n" \
"s_cselect_b32 s4, L_gs7-L_gtr, s4\n" \
M_"a[80:83], a[172:175], a[140:143], a[80:83]\n" \
"s_cmp_eq_u32 s16, 0\n" \
"s_cselect_b32 s4, 0, s4\n" \
M_"a[80:83], a[236:239], a[204:207], a[80:83]\n" \
"s_getpc_b64 s[28:29]\n" \
"L_gp1_%=:\n" \
"s_add_u32 s28, s28, L_gtr-L_gp1_%=\n" \
"s_addc_u32 s29, s29, 0\n" \
M_"a[68:71], a[140:143], a[168:171], a[68:71]\n" \
"s_add_u32 s28, s28, s4\n" \
"s_addc_u32 s29, s29, 0\n" \
"s_waitcnt vmcnt(11) lgkmcnt(0)\n" \
M_"a[68:71], a[204:207], a[232:235], a[68:71]\n" \
"s_barrier\n" \
M_"v[116:119], a[176:179], a[144:147], v[116:119]\n" \
"s_add_u32 m0, s40, 0x0\n" \
"s_nop 0\n" \
"s_add_u32 s85, s39, 0x10000\n" \
M_"v[116:119], a[240:243], a[208:211], v[116:119]\n" \
B_"v20, s[80:83], s85 offen sc1 lds\n" \
M_"v[120:123], a[180:183], a[144:147], v[120:123]\n" \
X_"a[160:161], v32 offset:32768\n" \
"v_accvgpr_write_b32 a96, 0\n" \
M_"v[120:123], a[244:247], a[208:211], v[120:123]\n" \
X_"a[162:163], v32 offset:33280\n" \
"v_accvgpr_write_b32 a97, 0\n" \
M_"v[124:127], a[184:187], a[144:147], v[124:127]\n" \
"v_accvgpr_write_b32 a98, 0\n" \
"v_accvgpr_write_b32 a99, 0\n" \
M_"v[124:127], a[248:251], a[208:211], v[124:127]\n" \
"v_accvgpr_write_b32 a100, 0\n" \
"v_accvgpr_write_b32 a101, 0\n" \
M_"v[128:131], a[188:191], a[144:147], v[128:131]\n" \
X_"a[224:225], v32 offset:36864\n" \
"v_accvgpr_write_b32 a102, 0\n" \
M_"v[128:131], a[252:255], a[208:211], v[128:131]\n" \
R_"a[144:147], v28 offset:40960\n" \
"v_accvgpr_write_b32 a103, 0\n" \
M_"v[144:147], a[188:191], a[148:151], v[144:147]\n" \
R_"a[208:211], v29 offset:40960\n" \
"v_accvgpr_write_b32 a112, 0\n" \
M_"v[144:147], a[252:255], a[212:215], v[144:147]\n" \
"s_add_u32 m0, s40, 0x1000\n" \
"s_add_u32 s85, s39, 0x11000\n" \
M_"v[140:143], a[184:187], a[148:151], v[140:143]\n" \
B_"v20, s[80:83], s85 offen sc1 lds\n" \
M_"v[140:143], a[248:251], a[212:215], v[140:143]\n" \
X_"a[226:227], v32 offset:37376\n" \
"v_accvgpr_write_b32 a113, 0\n" \
M_"v[136:139], a[180:183], a[148:151], v[136:139]\n" \
"v_accvgpr_write_b32 a114, 0\n" \
"v_accvgpr_write_b32 a115, 0\n" \
M_"v[136:139], a[244:247], a[212:215], v[136:139]\n" \
X_"a[164:165], v33 offset:32768\n" \
"v_accvgpr_write_b32 a120, 0\n" \
M_"v[132:135], a[176:179], a[148:151], v[132:135]\n" \
"v_accvgpr_write_b32 a121, 0\n" \
"v_accvgpr_write_b32 a122, 0\n" \
M_"v[132:135], a[240:243], a[212:215], v[132:135]\n" \
R_"a[148:151], v28 offset:43008\n" \
"v_accvgpr_write_b32 a123, 0\n" \
M_"v[148:151], a[176:179], a[152:155], v[148:151]\n" \
R_"a[212:215], v29 offset:43008\n" \
"v_mov_b32_e32 v180, 0x3f803f80\n" \
M_"v[148:151], a[240:243], a[216:219], v[148:151]\n" \
"s_add_u32 m0, s40, 0x3000\n" \
"s_movk_i32 s84, 0x400\n" \
M_"v[152:155], a[180:183], a[152:155], v[152:155]\n" \
B_"v21, s[76:79], s84 offen offset:0 lds\n" \
M_"v[152:155], a[244:247], a[216:219], v[152:155]\n" \
X_"a[166:167], v33 offset:33280\n" \
"s_mov_b32 s6, 0x33333333\n" \
M_"v[156:159], a[184:187], a[152:155], v[156:159]\n" \
X_"a[228:229], v33 offset:36864\n" \
"s_mov_b32 s7, 0xcccccccc\n" \
M_"v[156:159], a[248:251], a[216:219], v[156:159]\n" \
"s_add_u32 m0, s31, 0x0\n" \
"s_mov_b32 s84, 0x400000\n" \
M_"v[160:163], a[188:191], a[152:155], v[160:163]\n" \
B_"v26, s[68:71], s84 offen offset:0 lds\n" \
M_"v[160:163], a[252:255], a[216:219], v[160:163]\n" \
R_"a[152:155], v28 offset:45056\n" \
"v_cndmask_b32_e64 v181, 0, v180, s[6:7]\n" \
M_"v[176:179], a[188:191], a[156:159], v[176:179]\n" \
R_"a[216:219], v29 offset:45056\n" \
"v_accvgpr_write_b32 a104, v181\n" \
M_"v[176:179], a[252:255], a[220:223], v[176:179]\n" \
X_"a[230:231], v33 offset:37376\n" \
"v_accvgpr_write_b32 a105, v181\n" \
M_"v[172:175], a[184:187], a[156:159], v[172:175]\n" \
X_"a[172:173], v35 offset:32768\n" \
"v_accvgpr_write_b32 a106, v181\n" \
M_"v[172:175], a[248:251], a[220:223], v[172:175]\n" \
"v_accvgpr_write_b32 a107, v181\n" \
"s_mov_b32 s6, 0x11111111\n" \
M_"v[168:171], a[180:183], a[156:159], v[168:171]\n" \
"s_add_u32 s88, s42, 0x2000\n" \
"buffer_store_dwordx4 v[204:207], v20, s[80:83], s88 offen offset:1024 sc1\n" \
M_"v[168:171], a[244:247], a[220:223], v[168:171]\n" \
X_"a[174:175], v35 offset:33280\n" \
"s_mov_b32 s7, 0x11111111\n" \
M_"v[164:167], a[176:179], a[156:159], v[164:167]\n" \
"v_cndmask_b32_e64 v181, 0, v180, s[6:7]\n" \
"v_accvgpr_write_b32 a108, v181\n" \
M_"v[164:167], a[240:243], a[220:223], v[164:167]\n" \
R_"a[156:159], v28 offset:47104\n" \
"v_accvgpr_write_b32 a109, v181\n" \
M_"v[84:87], a[176:179], a[136:139], v[84:87]\n" \
R_"a[220:223], v29 offset:47104\n" \
"v_accvgpr_write_b32 a110, v181\n" \
M_"v[84:87], a[240:243], a[200:203], v[84:87]\n" \
X_"a[236:237], v35 offset:36864\n" \
"v_accvgpr_write_b32 a111, v181\n" \
M_"v[88:91], a[180:183], a[136:139], v[88:91]\n" \
"s_mov_b32 s6, 0x44444444\n" \
"s_mov_b32 s7, 0x44444444\n" \
M_"v[88:91], a[244:247], a[200:203], v[88:91]\n" \
"s_addk_i32 s84, 0x7c00\n" \
"s_nop 0\n" \
M_"v[92:95], a[184:187], a[136:139], v[92:95]\n" \
B_"v26, s[68:71], s84 offen offset:1024 lds\n" \
M_"v[92:95], a[248:251], a[200:203], v[92:95]\n" \
X_"a[238:239], v35 offset:37376\n" \
"v_cndmask_b32_e64 v181, 0, v180, s[6:7]\n" \
M_"v[96:99], a[188:191], a[136:139], v[96:99]\n" \
"v_accvgpr_write_b32 a116, v181\n" \
"v_accvgpr_write_b32 a117, v181\n" \
M_"v[96:99], a[252:255], a[200:203], v[96:99]\n" \
R_"a[136:139], v28 offset:36864\n" \
"v_accvgpr_write_b32 a118, v181\n" \
M_"v[112:115], a[188:191], a[140:143], v[112:115]\n" \
R_"a[200:203], v29 offset:36864\n" \
"v_accvgpr_write_b32 a119, v181\n" \
M_"v[112:115], a[252:255], a[204:207], v[112:115]\n" \
X_"a[168:169], v34 offset:32768\n" \
"v_bfe_i32 v181, v20, 4, 1\n" \
M_"v[108:111], a[184:187], a[140:143], v[108:111]\n" \
"s_addk_i32 s84, 0x7c00\n" \
"s_nop 0\n" \
M_"v[108:111], a[248:251], a[204:207], v[108:111]\n" \
B_"v27, s[68:71], s84 offen offset:2048 lds\n" \
M_"v[104:107], a[180:183], a[140:143], v[104:107]\n" \
X_"a[170:171], v34 offset:33280\n" \
"v_and_b32_e32 v181, 0xaaaaaaaa, v181\n" \
M_"v[104:107], a[244:247], a[204:207], v[104:107]\n" \
X_"a[232:233], v34 offset:36864\n" \
"v_or_b32_e32 v181, 0x44444444, v181\n" \
M_"v[100:103], a[176:179], a[140:143], v[100:103]\n" \
"v_accvgpr_write_b32 a126, v181\n" \
"v_bfe_u32 v180, v20, 5, 3\n" \
M_"v[100:103], a[240:243], a[204:207], v[100:103]\n" \
R_"a[140:143], v28 offset:38912\n" \
"v_bfe_u32 v181, v20, 8, 2\n" \
M_"v[52:55], a[176:179], a[128:131], v[52:55]\n" \
R_"a[204:207], v29 offset:38912\n" \
"v_lshlrev_b32_e64 v181, 1, v181\n" \
M_"v[52:55], a[240:243], a[192:195], v[52:55]\n" \
"s_addk_i32 s84, 0x7c00\n" \
M_"v[56:59], a[180:183], a[128:131], v[56:59]\n" \
B_"v27, s[68:71], s84 offen offset:3072 lds\n" \
M_"v[56:59], a[244:247], a[192:195], v[56:59]\n" \
X_"a[234:235], v34 offset:37376\n" \
"v_sub_u32_e32 v180, v180, v181\n" \
M_"v[60:63], a[184:187], a[128:131], v[60:63]\n" \
"v_and_b32_e64 v181, 16, v20\n" \
"v_mov_b32_e32 v182, 0x3f80\n" \
M_"v[60:63], a[248:251], a[192:195], v[60:63]\n" \
"v_lshlrev_b32_e32 v181, v181, v182\n" \
"v_cmp_eq_u32_e32 vcc, 0, v180\n" \
M_"v[64:67], a[188:191], a[128:131], v[64:67]\n" \
"v_cmp_eq_u32_e64 s[6:7], 1, v180\n" \
"v_cndmask_b32_e64 v182, 0, v181, vcc\n" \
M_"v[64:67], a[252:255], a[192:195], v[64:67]\n" \
R_"a[128:131], v28 offset:32768\n" \
"v_cndmask_b32_e64 v183, 0, v181, s[6:7]\n" \
M_"v[80:83], a[188:191], a[132:135], v[80:83]\n" \
R_"a[192:195], v29 offset:32768\n" \
"v_accvgpr_write_b32 a124, v182\n" \
M_"v[80:83], a[252:255], a[196:199], v[80:83]\n" \
"v_accvgpr_write_b32 a125, v183\n" \
M_"v[76:79], a[184:187], a[132:135], v[76:79]\n" \
M_"v[76:79], a[248:251], a[196:199], v[76:79]\n" \
M_"v[72:75], a[180:183], a[132:135], v[72:75]\n" \
M_"v[72:75], a[244:247], a[196:199], v[72:75]\n" \
M_"v[68:71], a[176:179], a[132:135], v[68:71]\n" \
M_"v[68:71], a[240:243], a[196:199], v[68:71]\n" \
R_"a[132:135], v28 offset:34816\n" \
R_"a[196:199], v29 offset:34816\n" \
"s_waitcnt lgkmcnt(15)\n" \
"s_nop 0\n" \
M_"a[0:3], a[160:163], a[144:147], a[0:3]\n" \
"s_add_u32 s89, s42, 0x2000\n" \
"buffer_store_dwordx4 v[208:211], v20, s[80:83], s89 offen offset:2048 sc1\n" \
M_"a[0:3], a[224:227], a[208:211], a[0:3]\n" \
X_"a[176:177], v36 offset:32768\n" \
M_"a[4:7], a[164:167], a[144:147], a[4:7]\n" \
X_"a[178:179], v36 offset:33792\n" \
M_"a[4:7], a[228:231], a[208:211], a[4:7]\n" \
X_"a[240:241], v36 offset:40960\n" \
M_"a[12:15], a[172:175], a[144:147], a[12:15]\n" \
X_"a[242:243], v36 offset:41984\n" \
"s_waitcnt lgkmcnt(15)\n" \
M_"a[12:15], a[236:239], a[208:211], a[12:15]\n" \
"s_waitcnt lgkmcnt(12)\n" \
M_"a[8:11], a[144:147], a[168:171], a[8:11]\n" \
X_"a[180:181], v37 offset:32768\n" \
"s_waitcnt lgkmcnt(9)\n" \
"s_nop 0\n" \
M_"a[8:11], a[208:211], a[232:235], a[8:11]\n" \
M_"a[24:27], a[148:151], a[168:171], a[24:27]\n" \
X_"a[182:183], v37 offset:33792\n" \
M_"a[24:27], a[212:215], a[232:235], a[24:27]\n" \
X_"a[244:245], v37 offset:40960\n" \
M_"a[28:31], a[172:175], a[148:151], a[28:31]\n" \
M_"a[28:31], a[236:239], a[212:215], a[28:31]\n" \
X_"a[246:247], v37 offset:41984\n" \
M_"a[20:23], a[164:167], a[148:151], a[20:23]\n" \
M_"a[20:23], a[228:231], a[212:215], a[20:23]\n" \
X_"a[184:185], v38 offset:32768\n" \
M_"a[16:19], a[160:163], a[148:151], a[16:19]\n" \
X_"a[186:187], v38 offset:33792\n" \
M_"a[16:19], a[224:227], a[212:215], a[16:19]\n" \
M_"a[32:35], a[160:163], a[152:155], a[32:35]\n" \
"s_add_u32 s88, s42, 0x2000\n" \
"buffer_store_dwordx4 v[212:215], v20, s[80:83], s88 offen offset:3072 sc1\n" \
M_"a[32:35], a[224:227], a[216:219], a[32:35]\n" \
X_"a[248:249], v38 offset:40960\n" \
M_"a[36:39], a[164:167], a[152:155], a[36:39]\n" \
"v_lshlrev_b32_e32 v45, 3, v20\n" \
"v_add_u32_e32 v45, s30, v45\n" \
M_"a[36:39], a[228:231], a[216:219], a[36:39]\n" \
"s_getpc_b64 s[86:87]\n" \
"L_ct1_%=:\n" \
"s_add_u32 s86, s86, L_ap0-L_ct1_%=\n" \
"s_addc_u32 s87, s87, 0\n" \
"s_add_u32 m0, s40, 0x2000\n" \
M_"a[44:47], a[172:175], a[152:155], a[44:47]\n" \
"global_load_lds_dword v45, s[86:87]\n" \
M_"a[44:47], a[236:239], a[216:219], a[44:47]\n" \
X_"a[250:251], v38 offset:41984\n" \
M_"a[40:43], a[152:155], a[168:171], a[40:43]\n" \
X_"a[188:189], v39 offset:32768\n" \
M_"a[40:43], a[216:219], a[232:235], a[40:43]\n" \
X_"a[190:191], v39 offset:33792\n" \
M_"a[56:59], a[156:159], a[168:171], a[56:59]\n" \
X_"a[252:253], v39 offset:40960\n" \
M_"a[56:59], a[220:223], a[232:235], a[56:59]\n" \
X_"a[254:255], v39 offset:41984\n" \
M_"a[60:63], a[172:175], a[156:159], a[60:63]\n" \
M_"a[60:63], a[236:239], a[220:223], a[60:63]\n" \
M_"a[52:55], a[164:167], a[156:159], a[52:55]\n" \
M_"a[52:55], a[228:231], a[220:223], a[52:55]\n" \
M_"a[48:51], a[160:163], a[156:159], a[48:51]\n" \
M_"a[48:51], a[224:227], a[220:223], a[48:51]\n" \
M_"a[64:67], a[164:167], a[140:143], a[64:67]\n" \
M_"a[64:67], a[228:231], a[204:207], a[64:67]\n" \
M_"a[80:83], a[172:175], a[140:143], a[80:83]\n" \
M_"a[80:83], a[236:239], a[204:207], a[80:83]\n" \
M_"a[68:71], a[140:143], a[168:171], a[68:71]\n" \
"s_waitcnt vmcnt(3) lgkmcnt(0)\n" \
M_"a[68:71], a[204:207], a[232:235], a[68:71]\n" \
"s_barrier\n" \
"v_xor_b32_e32 v28, 0x20000, v28\n" \
"v_xor_b32_e32 v29, 0x20000, v29\n" \
M_"v[116:119], a[176:179], a[144:147], v[116:119]\n" \
X_"a[160:161], v32 offset:0\n" \
"v_accvgpr_read_b32 v184, a0\n" \
M_"v[116:119], a[240:243], a[208:211], v[116:119]\n" \
"v_accvgpr_read_b32 v185, a1\n" \
"v_accvgpr_read_b32 v186, a2\n" \
M_"v[120:123], a[180:183], a[144:147], v[120:123]\n" \
"v_accvgpr_read_b32 v187, a3\n" \
"v_accvgpr_read_b32 v188, a4\n" \
M_"v[120:123], a[244:247], a[208:211], v[120:123]\n" \
X_"a[162:163], v32 offset:512\n" \
"v_accvgpr_read_b32 v189, a5\n" \
M_"v[124:127], a[184:187], a[144:147], v[124:127]\n" \
"v_accvgpr_read_b32 v190, a6\n" \
"v_accvgpr_read_b32 v191, a7\n" \
M_"v[124:127], a[248:251], a[208:211], v[124:127]\n" \
"v_accvgpr_read_b32 v196, a12\n" \
"v_accvgpr_read_b32 v197, a13\n" \
M_"v[128:131], a[188:191], a[144:147], v[128:131]\n" \
X_"a[224:225], v32 offset:4096\n" \
"v_accvgpr_read_b32 v198, a14\n" \
M_"v[128:131], a[252:255], a[208:211], v[128:131]\n" \
R_"a[144:147], v28 offset:8192\n" \
"v_accvgpr_read_b32 v199, a15\n" \
M_"v[144:147], a[188:191], a[148:151], v[144:147]\n" \
R_"a[208:211], v29 offset:8192\n" \
"v_accvgpr_read_b32 v192, a8\n" \
M_"v[144:147], a[252:255], a[212:215], v[144:147]\n" \
X_"a[226:227], v32 offset:4608\n" \
"v_accvgpr_read_b32 v193, a9\n" \
M_"v[140:143], a[184:187], a[148:151], v[140:143]\n" \
"v_accvgpr_read_b32 v194, a10\n" \
"v_accvgpr_read_b32 v195, a11\n" \
M_"v[140:143], a[248:251], a[212:215], v[140:143]\n" \
"v_accvgpr_read_b32 v208, a24\n" \
"v_accvgpr_read_b32 v209, a25\n" \
M_"v[136:139], a[180:183], a[148:151], v[136:139]\n" \
"v_accvgpr_read_b32 v210, a26\n" \
"v_accvgpr_read_b32 v211, a27\n" \
M_"v[136:139], a[244:247], a[212:215], v[136:139]\n" \
X_"a[164:165], v33 offset:0\n" \
"v_accvgpr_read_b32 v212, a28\n" \
M_"v[132:135], a[176:179], a[148:151], v[132:135]\n" \
"v_accvgpr_read_b32 v213, a29\n" \
"v_accvgpr_read_b32 v214, a30\n" \
M_"v[132:135], a[240:243], a[212:215], v[132:135]\n" \
R_"a[148:151], v28 offset:10240\n" \
"v_accvgpr_read_b32 v215, a31\n" \
M_"v[148:151], a[176:179], a[152:155], v[148:151]\n" \
R_"a[212:215], v29 offset:10240\n" \
"v_accvgpr_read_b32 v204, a20\n" \
M_"v[148:151], a[240:243], a[216:219], v[148:151]\n" \
X_"a[166:167], v33 offset:512\n" \
"v_accvgpr_read_b32 v205, a21\n" \
M_"v[152:155], a[180:183], a[152:155], v[152:155]\n" \
"v_accvgpr_read_b32 v206, a22\n" \
"v_accvgpr_read_b32 v207, a23\n" \
M_"v[152:155], a[244:247], a[216:219], v[152:155]\n" \
X_"a[228:229], v33 offset:4096\n" \
"v_accvgpr_read_b32 v200, a16\n" \
M_"v[156:159], a[184:187], a[152:155], v[156:159]\n" \
"v_accvgpr_read_b32 v201, a17\n" \
"v_accvgpr_read_b32 v202, a18\n" \
M_"v[156:159], a[248:251], a[216:219], v[156:159]\n" \
"v_accvgpr_read_b32 v203, a19\n" \
"v_accvgpr_read_b32 v216, a32\n" \
M_"v[160:163], a[188:191], a[152:155], v[160:163]\n" \
X_"a[230:231], v33 offset:4608\n" \
"v_accvgpr_read_b32 v217, a33\n" \
M_"v[160:163], a[252:255], a[216:219], v[160:163]\n" \
R_"a[152:155], v28 offset:12288\n" \
"v_accvgpr_read_b32 v218, a34\n" \
M_"v[176:179], a[188:191], a[156:159], v[176:179]\n" \
R_"a[216:219], v29 offset:12288\n" \
"v_accvgpr_read_b32 v219, a35\n" \
M_"v[176:179], a[252:255], a[220:223], v[176:179]\n" \
"v_accvgpr_read_b32 v220, a36\n" \
"v_accvgpr_read_b32 v221, a37\n" \
M_"v[172:175], a[184:187], a[156:159], v[172:175]\n" \
X_"a[172:173], v35 offset:0\n" \
"v_accvgpr_read_b32 v222, a38\n" \
M_"v[172:175], a[248:251], a[220:223], v[172:175]\n" \
"v_accvgpr_read_b32 v223, a39\n" \
"v_accvgpr_read_b32 v228, a44\n" \
M_"v[168:171], a[180:183], a[156:159], v[168:171]\n" \
"v_accvgpr_read_b32 v229, a45\n" \
"v_accvgpr_read_b32 v230, a46\n" \
M_"v[168:171], a[244:247], a[220:223], v[168:171]\n" \
X_"a[174:175], v35 offset:512\n" \
"v_accvgpr_read_b32 v231, a47\n" \
M_"v[164:167], a[176:179], a[156:159], v[164:167]\n" \
"v_accvgpr_read_b32 v224, a40\n" \
"v_accvgpr_read_b32 v225, a41\n" \
M_"v[164:167], a[240:243], a[220:223], v[164:167]\n" \
R_"a[156:159], v28 offset:14336\n" \
"v_accvgpr_read_b32 v226, a42\n" \
M_"v[84:87], a[176:179], a[136:139], v[84:87]\n" \
R_"a[220:223], v29 offset:14336\n" \
"v_accvgpr_read_b32 v227, a43\n" \
M_"v[84:87], a[240:243], a[200:203], v[84:87]\n" \
X_"a[236:237], v35 offset:4096\n" \
"v_accvgpr_read_b32 v240, a56\n" \
M_"v[88:91], a[180:183], a[136:139], v[88:91]\n" \
"v_accvgpr_read_b32 v241, a57\n" \
"v_accvgpr_read_b32 v242, a58\n" \
M_"v[88:91], a[244:247], a[200:203], v[88:91]\n" \
X_"a[238:239], v35 offset:4608\n" \
"v_accvgpr_read_b32 v243, a59\n" \
M_"v[92:95], a[184:187], a[136:139], v[92:95]\n" \
"v_accvgpr_read_b32 v244, a60\n" \
"v_accvgpr_read_b32 v245, a61\n" \
M_"v[92:95], a[248:251], a[200:203], v[92:95]\n" \
"v_accvgpr_read_b32 v246, a62\n" \
"v_accvgpr_read_b32 v247, a63\n" \
M_"v[96:99], a[188:191], a[136:139], v[96:99]\n" \
"v_accvgpr_read_b32 v236, a52\n" \
"v_accvgpr_read_b32 v237, a53\n" \
M_"v[96:99], a[252:255], a[200:203], v[96:99]\n" \
R_"a[136:139], v28 offset:4096\n" \
"v_accvgpr_read_b32 v238, a54\n" \
M_"v[112:115], a[188:191], a[140:143], v[112:115]\n" \
R_"a[200:203], v29 offset:4096\n" \
"v_accvgpr_read_b32 v239, a55\n" \
M_"v[112:115], a[252:255], a[204:207], v[112:115]\n" \
X_"a[168:169], v34 offset:0\n" \
"v_accvgpr_read_b32 v232, a48\n" \
M_"v[108:111], a[184:187], a[140:143], v[108:111]\n" \
X_"a[170:171], v34 offset:512\n" \
"v_accvgpr_read_b32 v233, a49\n" \
M_"v[108:111], a[248:251], a[204:207], v[108:111]\n" \
"v_accvgpr_read_b32 v234, a50\n" \
"v_accvgpr_read_b32 v235, a51\n" \
M_"v[104:107], a[180:183], a[140:143], v[104:107]\n" \
"v_accvgpr_read_b32 v180, a64\n" \
"v_accvgpr_read_b32 v181, a65\n" \
M_"v[104:107], a[244:247], a[204:207], v[104:107]\n" \
X_"a[232:233], v34 offset:4096\n" \
"v_accvgpr_read_b32 v182, a66\n" \
M_"v[100:103], a[176:179], a[140:143], v[100:103]\n" \
"v_accvgpr_read_b32 v183, a67\n" \
"v_accvgpr_read_b32 v48, a80\n" \
M_"v[100:103], a[240:243], a[204:207], v[100:103]\n" \
R_"a[140:143], v28 offset:6144\n" \
"v_accvgpr_read_b32 v49, a81\n" \
M_"v[52:55], a[176:179], a[128:131], v[52:55]\n" \
R_"a[204:207], v29 offset:6144\n" \
"v_accvgpr_read_b32 v50, a82\n" \
M_"v[52:55], a[240:243], a[192:195], v[52:55]\n" \
X_"a[234:235], v34 offset:4608\n" \
"v_accvgpr_read_b32 v51, a83\n" \
M_"v[56:59], a[180:183], a[128:131], v[56:59]\n" \
"v_accvgpr_read_b32 v252, a68\n" \
"v_accvgpr_read_b32 v253, a69\n" \
M_"v[56:59], a[244:247], a[192:195], v[56:59]\n" \
"v_accvgpr_read_b32 v254, a70\n" \
"v_accvgpr_read_b32 v255, a71\n" \
M_"v[60:63], a[184:187], a[128:131], v[60:63]\n" \
"v_lshrrev_b32_e64 v24, 9, v20\n" \
"v_and_b32_e32 v26, 0x1ff, v20\n" \
M_"v[60:63], a[248:251], a[192:195], v[60:63]\n" \
"v_lshl_add_u32 v24, v24, 3, v26\n" \
"v_lshrrev_b32_e64 v25, 8, v20\n" \
M_"v[64:67], a[188:191], a[128:131], v[64:67]\n" \
"v_mul_u32_u24_e32 v25, 0x24700, v25\n" \
"v_add_u32_e64 v25, v25, v20\n" \
M_"v[64:67], a[252:255], a[192:195], v[64:67]\n" \
R_"a[128:131], v28 offset:0\n" \
"v_lshrrev_b32_e64 v26, 2, v20\n" \
M_"v[80:83], a[188:191], a[132:135], v[80:83]\n" \
R_"a[192:195], v29 offset:0\n" \
"v_add_u32_e64 v26, s45, v26\n" \
M_"v[80:83], a[252:255], a[196:199], v[80:83]\n" \
"v_add_u32_e32 v27, s46, v20\n" \
"v_add_u32_e32 v21, s43, v27\n" \
M_"v[76:79], a[184:187], a[132:135], v[76:79]\n" \
M_"v[76:79], a[248:251], a[196:199], v[76:79]\n" \
M_"v[72:75], a[180:183], a[132:135], v[72:75]\n" \
M_"v[72:75], a[244:247], a[196:199], v[72:75]\n" \
M_"v[68:71], a[176:179], a[132:135], v[68:71]\n" \
M_"v[68:71], a[240:243], a[196:199], v[68:71]\n" \
R_"a[132:135], v28 offset:2048\n" \
R_"a[196:199], v29 offset:2048\n" \
"s_waitcnt lgkmcnt(15)\n" \
"s_nop 0\n" \
M_"v[184:187], a[160:163], a[144:147], v[184:187]\n" \
"v_bfe_u32 v44, v20, 2, 5\n" \
X_"a[176:177], v36 offset:0\n" \
M_"v[184:187], a[224:227], a[208:211], v[184:187]\n" \
"buffer_load_dword v44, v44, s[80:83], s38 offen offset:0 sc1\n" \
M_"v[188:191], a[164:167], a[144:147], v[188:191]\n" \
X_"a[178:179], v36 offset:1024\n" \
M_"v[188:191], a[228:231], a[208:211], v[188:191]\n" \
X_"a[240:241], v36 offset:8192\n" \
M_"v[196:199], a[172:175], a[144:147], v[196:199]\n" \
X_"a[242:243], v36 offset:9216\n" \
"s_waitcnt lgkmcnt(15)\n" \
M_"v[196:199], a[236:239], a[208:211], v[196:199]\n" \
"s_waitcnt lgkmcnt(12)\n" \
M_"v[192:195], a[144:147], a[168:171], v[192:195]\n" \
X_"a[180:181], v37 offset:0\n" \
"s_waitcnt lgkmcnt(9)\n" \
"s_nop 0\n" \
M_"v[192:195], a[208:211], a[232:235], v[192:195]\n" \
M_"v[208:211], a[148:151], a[168:171], v[208:211]\n" \
X_"a[182:183], v37 offset:1024\n" \
M_"v[208:211], a[212:215], a[232:235], v[208:211]\n" \
X_"a[244:245], v37 offset:8192\n" \
M_"v[212:215], a[172:175], a[148:151], v[212:215]\n" \
M_"v[212:215], a[236:239], a[212:215], v[212:215]\n" \
X_"a[246:247], v37 offset:9216\n" \
C_"v188, v188, v189\n" \
M_"v[204:207], a[164:167], a[148:151], v[204:207]\n" \
C_"v189, v190, v191\n" \
C_"v190, v198, v199\n" \
M_"v[204:207], a[228:231], a[212:215], v[204:207]\n" \
X_"a[184:185], v38 offset:0\n" \
"v_mov_b32_e32 v191, 0x3f80\n" \
M_"v[200:203], a[160:163], a[148:151], v[200:203]\n" \
X_"a[186:187], v38 offset:1024\n" \
C_"v184, v184, v185\n" \
".fill (L_pb + 77536 - .) / 4, 4, 0xBF800000\n" \
"L_pp19:\n" \
"s_cbranch_execz L_pret\n" \
M_"v[200:203], a[224:227], a[212:215], v[200:203]\n" \
C_"v185, v186, v187\n" \
C_"v186, v196, v197\n" \
M_"v[216:219], a[160:163], a[152:155], v[216:219]\n" \
X_"a[248:249], v38 offset:8192\n" \
"v_mov_b32_e32 v187, 0\n" \
M_"v[216:219], a[224:227], a[216:219], v[216:219]\n" \
X_"a[250:251], v38 offset:9216\n" \
M_"v[220:223], a[164:167], a[152:155], v[220:223]\n" \
M_"v[220:223], a[228:231], a[216:219], v[220:223]\n" \
X_"a[188:189], v39 offset:0\n" \
C_"v204, v204, v205\n" \
M_"v[228:231], a[172:175], a[152:155], v[228:231]\n" \
C_"v205, v206, v207\n" \
C_"v206, v214, v215\n" \
M_"v[228:231], a[236:239], a[216:219], v[228:231]\n" \
X_"a[190:191], v39 offset:1024\n" \
"v_mov_b32_e32 v207, 0x3f80\n" \
M_"v[224:227], a[152:155], a[168:171], v[224:227]\n" \
X_"a[252:253], v39 offset:8192\n" \
C_"v200, v200, v201\n" \
M_"v[224:227], a[216:219], a[232:235], v[224:227]\n" \
C_"v201, v202, v203\n" \
C_"v202, v212, v213\n" \
M_"v[240:243], a[156:159], a[168:171], v[240:243]\n" \
X_"a[254:255], v39 offset:9216\n" \
"v_mov_b32_e64 v203, 0\n" \
M_"v[240:243], a[220:223], a[232:235], v[240:243]\n" \
M_"v[244:247], a[172:175], a[156:159], v[244:247]\n" \
M_"v[244:247], a[236:239], a[220:223], v[244:247]\n" \
C_"v220, v220, v221\n" \
C_"v221, v222, v223\n" \
M_"v[236:239], a[164:167], a[156:159], v[236:239]\n" \
C_"v222, v230, v231\n" \
"v_mov_b32_e32 v223, 0x3f80\n" \
M_"v[236:239], a[228:231], a[220:223], v[236:239]\n" \
C_"v216, v216, v217\n" \
C_"v217, v218, v219\n" \
M_"v[232:235], a[160:163], a[156:159], v[232:235]\n" \
C_"v218, v228, v229\n" \
"v_mov_b32_e64 v219, 0\n" \
M_"v[232:235], a[224:227], a[220:223], v[232:235]\n" \
M_"v[180:183], a[164:167], a[140:143], v[180:183]\n" \
M_"v[180:183], a[228:231], a[204:207], v[180:183]\n" \
M_"v[48:51], a[172:175], a[140:143], v[48:51]\n" \
M_"v[48:51], a[236:239], a[204:207], v[48:51]\n" \
C_"v236, v236, v237\n" \
C_"v237, v238, v239\n" \
M_"v[252:255], a[140:143], a[168:171], v[252:255]\n" \
C_"v238, v246, v247\n" \
"v_mov_b32_e32 v239, 0x3f80\n" \
M_"v[252:255], a[204:207], a[232:235], v[252:255]\n" \
C_"v232, v232, v233\n" \
C_"v233, v234, v235\n" \
"s_waitcnt vmcnt(0) lgkmcnt(0)\n" \
"s_barrier\n" \
"v_mov_b32_e32 v45, s62\n" \
"s_mov_b64 exec, 1\n" \
"buffer_store_dword v45, off, s[80:83], s37 offset:32 sc1\n" \
"s_mov_b64 exec, -1\n" \
"v_cmp_ne_u32_e32 vcc, s62, v44\n" \
"s_nop 4\n" \
"s_cbranch_vccnz L_poll161_%=\n" \
"L_pollr161_%=:\n" \
M_"v[116:119], a[176:179], a[144:147], v[116:119]\n" \
"s_mov_b32 s73, s50\n" \
"s_sub_u32 m0, s31, 0x10000\n" \
M_"v[116:119], a[240:243], a[208:211], v[116:119]\n" \
B_"v20, s[80:83], s73 offen offset:0 sc1 lds\n" \
M_"v[120:123], a[180:183], a[144:147], v[120:123]\n" \
"s_add_u32 s74, s50, 0x4000\n" \
"s_add_u32 s75, s50, 0x8000\n" \
M_"v[120:123], a[244:247], a[208:211], v[120:123]\n" \
"s_mov_b32 s70, 0x3e0293ee\n" \
"s_mov_b32 s76, s52\n" \
M_"v[124:127], a[184:187], a[144:147], v[124:127]\n" \
"v_mul_f32_e32 v116, s70, v116\n" \
"v_mul_f32_e32 v117, s70, v117\n" \
M_"v[124:127], a[248:251], a[208:211], v[124:127]\n" \
B_"v20, s[80:83], s73 offen offset:1024 sc1 lds\n" \
M_"v[128:131], a[188:191], a[144:147], v[128:131]\n" \
"v_mul_f32_e32 v118, s70, v118\n" \
"v_mul_f32_e32 v119, s70, v119\n" \
M_"v[128:131], a[252:255], a[208:211], v[128:131]\n" \
C_"v116, v116, v117\n" \
"v_mul_f32_e64 v120, s70, v120\n" \
M_"v[144:147], a[188:191], a[148:151], v[144:147]\n" \
"v_mul_f32_e32 v124, s70, v124\n" \
"v_mul_f32_e32 v125, s70, v125\n" \
M_"v[144:147], a[252:255], a[212:215], v[144:147]\n" \
B_"v20, s[80:83], s73 offen offset:2048 sc1 lds\n" \
M_"v[140:143], a[184:187], a[148:151], v[140:143]\n" \
"v_mul_f32_e32 v126, s70, v126\n" \
"v_mul_f32_e32 v127, s70, v127\n" \
M_"v[140:143], a[248:251], a[212:215], v[140:143]\n" \
C_"v124, v124, v125\n" \
"v_mul_f32_e64 v121, s70, v121\n" \
M_"v[136:139], a[180:183], a[148:151], v[136:139]\n" \
"v_mul_f32_e32 v122, s70, v122\n" \
"v_mul_f32_e32 v123, s70, v123\n" \
M_"v[136:139], a[244:247], a[212:215], v[136:139]\n" \
B_"v20, s[80:83], s73 offen offset:3072 sc1 lds\n" \
M_"v[132:135], a[176:179], a[148:151], v[132:135]\n" \
"s_sub_u32 m0, s31, s51\n" \
C_"v117, v118, v119\n" \
M_"v[132:135], a[240:243], a[212:215], v[132:135]\n" \
"v_mul_f32_e32 v128, s70, v128\n" \
"v_mul_f32_e64 v129, s70, v129\n" \
M_"v[148:151], a[176:179], a[152:155], v[148:151]\n" \
"v_mul_f32_e32 v130, s70, v130\n" \
"v_mul_f32_e32 v131, s70, v131\n" \
M_"v[148:151], a[240:243], a[216:219], v[148:151]\n" \
B_"v20, s[80:83], s74 offen offset:0 sc1 lds\n" \
M_"v[152:155], a[180:183], a[152:155], v[152:155]\n" \
C_"v125, v126, v127\n" \
C_"v118, v120, v121\n" \
M_"v[152:155], a[244:247], a[216:219], v[152:155]\n" \
C_"v119, v122, v123\n" \
C_"v126, v128, v129\n" \
M_"v[156:159], a[184:187], a[152:155], v[156:159]\n" \
C_"v127, v130, v131\n" \
"v_mul_f32_e64 v132, s70, v132\n" \
M_"v[156:159], a[248:251], a[216:219], v[156:159]\n" \
B_"v20, s[80:83], s74 offen offset:1024 sc1 lds\n" \
M_"v[160:163], a[188:191], a[152:155], v[160:163]\n" \
"ds_write_b128 v21, v[116:119] offset:16384\n" \
M_"v[160:163], a[252:255], a[216:219], v[160:163]\n" \
"v_mul_f32_e32 v133, s70, v133\n" \
"v_mul_f32_e32 v140, s70, v140\n" \
M_"v[176:179], a[188:191], a[156:159], v[176:179]\n" \
"v_mul_f32_e32 v141, s70, v141\n" \
"v_mul_f32_e32 v134, s70, v134\n" \
M_"v[176:179], a[252:255], a[220:223], v[176:179]\n" \
B_"v20, s[80:83], s74 offen offset:2048 sc1 lds\n" \
M_"v[172:175], a[184:187], a[156:159], v[172:175]\n" \
"ds_write_b128 v21, v[124:127] offset:17408\n" \
M_"v[172:175], a[248:251], a[220:223], v[172:175]\n" \
"v_mul_f32_e64 v135, s70, v135\n" \
C_"v132, v132, v133\n" \
M_"v[168:171], a[180:183], a[156:159], v[168:171]\n" \
"v_mul_f32_e32 v142, s70, v142\n" \
"v_mul_f32_e32 v143, s70, v143\n" \
M_"v[168:171], a[244:247], a[220:223], v[168:171]\n" \
B_"v20, s[80:83], s74 offen offset:3072 sc1 lds\n" \
M_"v[164:167], a[176:179], a[156:159], v[164:167]\n" \
"s_sub_u32 m0, s31, 0x4000\n" \
C_"v140, v140, v141\n" \
M_"v[164:167], a[240:243], a[220:223], v[164:167]\n" \
"v_mul_f32_e32 v136, s70, v136\n" \
"v_mul_f32_e32 v137, s70, v137\n" \
M_"v[84:87], a[176:179], a[136:139], v[84:87]\n" \
"v_mul_f32_e32 v138, s70, v138\n" \
"v_mul_f32_e32 v139, s70, v139\n" \
M_"v[84:87], a[240:243], a[200:203], v[84:87]\n" \
B_"v20, s[80:83], s75 offen offset:0 sc1 lds\n" \
M_"v[88:91], a[180:183], a[136:139], v[88:91]\n" \
C_"v133, v134, v135\n" \
"v_mul_f32_e64 v144, s70, v144\n" \
M_"v[88:91], a[244:247], a[200:203], v[88:91]\n" \
"v_mul_f32_e32 v145, s70, v145\n" \
"v_mul_f32_e32 v146, s70, v146\n" \
M_"v[92:95], a[184:187], a[136:139], v[92:95]\n" \
"v_mul_f32_e64 v147, s70, v147\n" \
C_"v141, v142, v143\n" \
M_"v[92:95], a[248:251], a[200:203], v[92:95]\n" \
B_"v20, s[80:83], s75 offen offset:1024 sc1 lds\n" \
M_"v[96:99], a[188:191], a[136:139], v[96:99]\n" \
C_"v134, v136, v137\n" \
C_"v135, v138, v139\n" \
M_"v[96:99], a[252:255], a[200:203], v[96:99]\n" \
C_"v142, v144, v145\n" \
C_"v143, v146, v147\n" \
M_"v[112:115], a[188:191], a[140:143], v[112:115]\n" \
"ds_write_b128 v21, v[132:135] offset:20480\n" \
M_"v[112:115], a[252:255], a[204:207], v[112:115]\n" \
B_"v20, s[80:83], s75 offen offset:2048 sc1 lds\n" \
M_"v[108:111], a[184:187], a[140:143], v[108:111]\n" \
C_"v180, v180, v181\n" \
C_"v181, v182, v183\n" \
M_"v[108:111], a[248:251], a[204:207], v[108:111]\n" \
"ds_write_b128 v21, v[140:143] offset:21504\n" \
M_"v[104:107], a[180:183], a[140:143], v[104:107]\n" \
C_"v234, v244, v245\n" \
"v_mov_b32_e64 v235, 0\n" \
M_"v[104:107], a[244:247], a[204:207], v[104:107]\n" \
B_"v20, s[80:83], s75 offen offset:3072 sc1 lds\n" \
M_"v[100:103], a[176:179], a[140:143], v[100:103]\n" \
"s_mov_b32 m0, s53\n" \
"s_nop 0\n" \
C_"v182, v50, v51\n" \
M_"v[100:103], a[240:243], a[204:207], v[100:103]\n" \
"v_mov_b32_e32 v183, 0x3f80\n" \
C_"v118, v192, v193\n" \
M_"v[52:55], a[176:179], a[128:131], v[52:55]\n" \
C_"v119, v194, v195\n" \
C_"v120, v208, v209\n" \
M_"v[52:55], a[240:243], a[192:195], v[52:55]\n" \
B_"v20, s[80:83], s76 offen offset:0 sc1 lds\n" \
M_"v[56:59], a[180:183], a[128:131], v[56:59]\n" \
C_"v121, v210, v211\n" \
C_"v122, v224, v225\n" \
M_"v[56:59], a[244:247], a[192:195], v[56:59]\n" \
C_"v123, v226, v227\n" \
C_"v124, v240, v241\n" \
C_"v125, v242, v243\n" \
M_"v[60:63], a[184:187], a[128:131], v[60:63]\n" \
M_"v[192:195], v[188:191], v[184:187], 0\n" \
B_"v20, s[80:83], s76 offen offset:1024 sc1 lds\n" \
M_"v[32:35], v[180:183], v[184:187], 0\n" \
M_"v[208:211], v[204:207], v[200:203], 0\n" \
M_"v[36:39], v[188:191], v[200:203], 0\n" \
M_"v[224:227], v[220:223], v[216:219], 0\n" \
M_"v[248:251], v[204:207], v[216:219], 0\n" \
M_"v[240:243], v[236:239], v[232:235], 0\n" \
M_"v[128:131], v[220:223], v[232:235], 0\n" \
"s_mov_b32 s64, 0xaaaaaaaa\n" \
"s_mov_b32 s65, 0xaaaaaaaa\n" \
M_"v[60:63], a[248:251], a[192:195], v[60:63]\n" \
"s_mov_b32 s66, 0xfff0ffff\n" \
"s_mov_b32 s67, 0xf000ff00\n" \
M_"v[64:67], a[188:191], a[128:131], v[64:67]\n" \
"s_mov_b32 s68, 0xffc0fffc\n" \
"s_mov_b32 s69, 0xc000fc00\n" \
"v_cndmask_b32_e64 v192, v192, v193, s[64:65]\n" \
"v_cndmask_b32_e64 v32, v32, v33, s[64:65]\n" \
"v_cndmask_b32_e64 v194, v194, v195, s[64:65]\n" \
"v_cndmask_b32_e64 v34, v34, v35, s[64:65]\n" \
"v_cndmask_b32_e64 v208, v208, v209, s[64:65]\n" \
"v_cndmask_b32_e64 v36, v36, v37, s[64:65]\n" \
"v_cndmask_b32_e64 v210, v210, v211, s[64:65]\n" \
"v_cndmask_b32_e64 v38, v38, v39, s[64:65]\n" \
"v_cndmask_b32_e64 v224, v224, v225, s[64:65]\n" \
"v_cndmask_b32_e64 v248, v248, v249, s[64:65]\n" \
"v_cndmask_b32_e64 v226, v226, v227, s[64:65]\n" \
"v_cndmask_b32_e64 v250, v250, v251, s[64:65]\n" \
"v_cndmask_b32_e64 v240, v240, v241, s[64:65]\n" \
"v_cndmask_b32_e64 v128, v128, v129, s[64:65]\n" \
"v_cndmask_b32_e64 v242, v242, v243, s[64:65]\n" \
"v_cndmask_b32_e64 v130, v130, v131, s[64:65]\n" \
"v_cndmask_b32_e64 v192, v32, v192, s[66:67]\n" \
"v_cndmask_b32_e64 v194, v34, v194, s[68:69]\n" \
"v_cndmask_b32_e64 v208, v36, v208, s[66:67]\n" \
"v_cndmask_b32_e64 v210, v38, v210, s[68:69]\n" \
"v_cndmask_b32_e64 v224, v248, v224, s[66:67]\n" \
"v_cndmask_b32_e64 v226, v250, v226, s[68:69]\n" \
"v_cndmask_b32_e64 v240, v128, v240, s[66:67]\n" \
"v_cndmask_b32_e64 v242, v130, v242, s[68:69]\n" \
M_"v[64:67], a[252:255], a[192:195], v[64:67]\n" \
"v_max_f32_e32 v193, v192, v194\n" \
"v_max_f32_e32 v209, v208, v210\n" \
"v_max_f32_e32 v225, v224, v226\n" \
"v_max_f32_e32 v241, v240, v242\n" \
"v_mul_f32_e32 v148, s70, v148\n" \
"v_permlane32_swap_b32_e32 v193, v225\n" \
"v_permlane32_swap_b32_e32 v209, v241\n" \
"v_mul_f32_e32 v149, s70, v149\n" \
"v_mul_f32_e32 v156, s70, v156\n" \
"v_mul_f32_e32 v157, s70, v157\n" \
"v_permlane32_swap_b32_e32 v196, v228\n" \
"v_permlane32_swap_b32_e32 v212, v244\n" \
"v_max_f32_e32 v193, v193, v225\n" \
"v_max_f32_e32 v209, v209, v241\n" \
"v_mul_f32_e32 v150, s70, v150\n" \
"v_mul_f32_e32 v151, s70, v151\n" \
C_"v148, v148, v149\n" \
"v_mul_f32_e32 v158, s70, v158\n" \
"v_mul_f32_e32 v159, s70, v159\n" \
C_"v156, v156, v157\n" \
"v_permlane16_swap_b32_e32 v228, v244\n" \
"v_permlane16_swap_b32_e32 v193, v209\n" \
M_"v[80:83], a[188:191], a[132:135], v[80:83]\n" \
"s_mov_b32 s72, 0x3eae1a93\n" \
"v_mul_f32_e32 v152, s70, v152\n" \
"v_mul_f32_e32 v153, s70, v153\n" \
"v_mul_f32_e32 v154, s70, v154\n" \
"v_mul_f32_e32 v155, s70, v155\n" \
C_"v149, v150, v151\n" \
"v_mul_f32_e32 v160, s70, v160\n" \
"v_mul_f32_e32 v161, s70, v161\n" \
"v_mul_f32_e32 v162, s70, v162\n" \
"v_mul_f32_e32 v163, s70, v163\n" \
C_"v157, v158, v159\n" \
"v_mul_f32_e32 v45, 0xbfb8aa3b, v228\n" \
"v_mfma_f32_16x16x16_bf16 v[48:51], v[182:183], v[46:47], 0\n" \
"v_mfma_f32_16x16x16_bf16 v[196:199], v[190:191], v[46:47], 0\n" \
"v_mfma_f32_16x16x16_bf16 v[212:215], v[206:207], v[46:47], 0\n" \
"v_max_f32_e64 v193, v193, v209\n" \
C_"v150, v152, v153\n" \
C_"v151, v154, v155\n" \
C_"v158, v160, v161\n" \
C_"v159, v162, v163\n" \
"v_mfma_f32_16x16x16_bf16 v[228:231], v[222:223], v[46:47], 0\n" \
"v_mul_f32_e32 v193, s72, v193\n" \
"v_mov_b32_e32 v209, v193\n" \
"ds_write_b128 v21, v[148:151] offset:24576\n" \
"v_mfma_f32_16x16x16_bf16 v[244:247], v[238:239], v[46:47], 0\n" \
"v_permlane16_swap_b32_e32 v193, v209\n" \
"s_mov_b32 s71, 0x3e0293ee\n" \
M_"v[80:83], a[252:255], a[196:199], v[80:83]\n" \
"v_mov_b32_e32 v225, v193\n" \
"v_mov_b32_e32 v241, v209\n" \
"v_mul_f32_e32 v164, s70, v164\n" \
"v_mul_f32_e64 v165, s70, v165\n" \
"ds_write_b128 v21, v[156:159] offset:25600\n" \
"v_mul_f32_e32 v172, s70, v172\n" \
"v_mul_f32_e32 v173, s70, v173\n" \
"v_pk_mul_f32 v[84:85], v[84:85], s[70:71]\n" \
"v_pk_mul_f32 v[92:93], v[92:93], s[70:71]\n" \
"v_pk_mul_f32 v[100:101], v[100:101], s[70:71]\n" \
"v_pk_mul_f32 v[108:109], v[108:109], s[70:71]\n" \
"v_pk_mul_f32 v[52:53], v[52:53], s[70:71]\n" \
"v_pk_mul_f32 v[60:61], v[60:61], s[70:71]\n" \
"v_permlane32_swap_b32_e32 v193, v225\n" \
"v_permlane32_swap_b32_e32 v209, v241\n" \
"v_mul_f32_e32 v166, s70, v166\n" \
"v_mul_f32_e32 v167, s70, v167\n" \
C_"v164, v164, v165\n" \
"v_mul_f32_e32 v174, s70, v174\n" \
"v_mul_f32_e32 v175, s70, v175\n" \
C_"v172, v172, v173\n" \
"v_pk_mul_f32 v[86:87], v[86:87], s[70:71]\n" \
C_"v84, v84, v85\n" \
"v_pk_mul_f32 v[94:95], v[94:95], s[70:71]\n" \
C_"v92, v92, v93\n" \
"v_pk_mul_f32 v[102:103], v[102:103], s[70:71]\n" \
C_"v100, v100, v101\n" \
"v_pk_mul_f32 v[110:111], v[110:111], s[70:71]\n" \
C_"v108, v108, v109\n" \
"v_pk_mul_f32 v[54:55], v[54:55], s[70:71]\n" \
C_"v52, v52, v53\n" \
"v_pk_mul_f32 v[62:63], v[62:63], s[70:71]\n" \
C_"v60, v60, v61\n" \
"v_fma_f32 v192, v192, s72, -v193\n" \
"v_fma_f32 v194, v194, s72, -v193\n" \
"v_fma_f32 v208, v208, s72, -v209\n" \
"v_fma_f32 v210, v210, s72, -v209\n" \
"v_mul_f32_e32 v168, s70, v168\n" \
"v_mul_f32_e32 v169, s70, v169\n" \
"v_mul_f32_e32 v170, s70, v170\n" \
"v_mul_f32_e32 v171, s70, v171\n" \
C_"v165, v166, v167\n" \
"v_mul_f32_e32 v176, s70, v176\n" \
"v_mul_f32_e32 v177, s70, v177\n" \
"v_mul_f32_e32 v178, s70, v178\n" \
"v_mul_f32_e32 v179, s70, v179\n" \
C_"v173, v174, v175\n" \
"v_pk_mul_f32 v[88:89], v[88:89], s[70:71]\n" \
"v_pk_mul_f32 v[90:91], v[90:91], s[70:71]\n" \
C_"v85, v86, v87\n" \
"v_pk_mul_f32 v[96:97], v[96:97], s[70:71]\n" \
"v_pk_mul_f32 v[98:99], v[98:99], s[70:71]\n" \
C_"v93, v94, v95\n" \
"v_pk_mul_f32 v[104:105], v[104:105], s[70:71]\n" \
"v_pk_mul_f32 v[106:107], v[106:107], s[70:71]\n" \
C_"v101, v102, v103\n" \
"v_pk_mul_f32 v[112:113], v[112:113], s[70:71]\n" \
"v_pk_mul_f32 v[114:115], v[114:115], s[70:71]\n" \
C_"v109, v110, v111\n" \
"v_pk_mul_f32 v[56:57], v[56:57], s[70:71]\n" \
"v_pk_mul_f32 v[58:59], v[58:59], s[70:71]\n" \
C_"v53, v54, v55\n" \
"v_pk_mul_f32 v[64:65], v[64:65], s[70:71]\n" \
"v_pk_mul_f32 v[66:67], v[66:67], s[70:71]\n" \
C_"v61, v62, v63\n" \
"v_pk_mul_f32 v[80:81], v[80:81], s[70:71]\n" \
"v_pk_mul_f32 v[82:83], v[82:83], s[70:71]\n" \
E_"v192, v192\n" \
E_"v194, v194\n" \
E_"v208, v208\n" \
E_"v210, v210\n" \
"v_fma_f32 v224, v224, s72, -v225\n" \
"v_fma_f32 v226, v226, s72, -v225\n" \
M_"v[76:79], a[184:187], a[132:135], v[76:79]\n" \
C_"v166, v168, v169\n" \
C_"v167, v170, v171\n" \
C_"v174, v176, v177\n" \
C_"v175, v178, v179\n" \
C_"v86, v88, v89\n" \
C_"v87, v90, v91\n" \
C_"v94, v96, v97\n" \
C_"v95, v98, v99\n" \
C_"v102, v104, v105\n" \
C_"v103, v106, v107\n" \
C_"v110, v112, v113\n" \
C_"v111, v114, v115\n" \
C_"v54, v56, v57\n" \
C_"v55, v58, v59\n" \
C_"v62, v64, v65\n" \
C_"v63, v66, v67\n" \
E_"v224, v224\n" \
E_"v226, v226\n" \
"v_fma_f32 v240, v240, s72, -v241\n" \
"v_fma_f32 v242, v242, s72, -v241\n" \
"ds_write_b128 v21, v[164:167] offset:28672\n" \
C_"v192, v192, v194\n" \
C_"v208, v208, v210\n" \
E_"v240, v240\n" \
E_"v242, v242\n" \
C_"v132, v48, v49\n" \
C_"v133, v50, v51\n" \
C_"v134, v196, v197\n" \
C_"v135, v198, v199\n" \
"ds_write_b128 v21, v[172:175] offset:29696\n" \
C_"v136, v212, v213\n" \
C_"v137, v214, v215\n" \
"v_perm_b32 v184, v192, v192, v40\n" \
"v_perm_b32 v185, v192, v192, v41\n" \
"v_perm_b32 v186, v192, v192, v42\n" \
"v_perm_b32 v187, v192, v192, v43\n" \
"v_perm_b32 v200, v208, v208, v40\n" \
"v_perm_b32 v201, v208, v208, v41\n" \
"v_perm_b32 v202, v208, v208, v42\n" \
"v_perm_b32 v203, v208, v208, v43\n" \
"ds_write_b128 v27, v[84:87] offset:8192\n" \
C_"v224, v224, v226\n" \
C_"v138, v228, v229\n" \
C_"v139, v230, v231\n" \
M_"v[32:35], v[132:135], v[184:187], 0\n" \
M_"v[36:39], v[134:137], v[200:203], 0\n" \
"v_perm_b32 v216, v224, v224, v40\n" \
"v_perm_b32 v217, v224, v224, v41\n" \
"ds_write_b128 v27, v[92:95] offset:9216\n" \
"v_perm_b32 v218, v224, v224, v42\n" \
"v_perm_b32 v219, v224, v224, v43\n" \
C_"v240, v240, v242\n" \
M_"v[76:79], a[248:251], a[196:199], v[76:79]\n" \
C_"v140, v244, v245\n" \
C_"v141, v246, v247\n" \
M_"v[248:251], v[136:139], v[216:219], 0\n" \
"v_perm_b32 v232, v240, v240, v40\n" \
"v_perm_b32 v233, v240, v240, v41\n" \
"ds_write_b128 v27, v[100:103] offset:12288\n" \
"v_perm_b32 v234, v240, v240, v42\n" \
"v_perm_b32 v235, v240, v240, v43\n" \
"v_pk_mul_f32 v[76:77], v[76:77], s[70:71]\n" \
"v_pk_mul_f32 v[78:79], v[78:79], s[70:71]\n" \
C_"v76, v76, v77\n" \
C_"v77, v78, v79\n" \
C_"v78, v80, v81\n" \
C_"v79, v82, v83\n" \
M_"v[128:131], v[138:141], v[232:235], 0\n" \
"ds_write_b128 v27, v[108:111] offset:13312\n" \
M_"v[72:75], a[180:183], a[132:135], v[72:75]\n" \
C_"v116, v252, v253\n" \
C_"v117, v254, v255\n" \
M_"v[72:75], a[244:247], a[196:199], v[72:75]\n" \
"v_exp_f32_e64 v45, v45\n" \
M_"v[192:195], v[116:119], v[184:187], 0\n" \
"ds_write_b128 v27, v[52:55] offset:0\n" \
M_"v[208:211], v[118:121], v[200:203], 0\n" \
M_"v[224:227], v[120:123], v[216:219], 0\n" \
M_"v[240:243], v[122:125], v[232:235], 0\n" \
"ds_write_b128 v27, v[60:63] offset:1024\n" \
"v_mov_b32_dpp v34, v38 quad_perm:[0,1,2,3] row_mask:0x2 bank_mask:0xf\n" \
"v_pk_mul_f32 v[72:73], v[72:73], s[70:71]\n" \
"v_pk_mul_f32 v[74:75], v[74:75], s[70:71]\n" \
"v_mov_b32_dpp v34, v250 quad_perm:[0,1,2,3] row_mask:0x4 bank_mask:0xf\n" \
"v_add_f32_e64 v45, 1.0, v45\n" \
M_"v[68:71], a[176:179], a[132:135], v[68:71]\n" \
"v_mov_b32_dpp v34, v130 quad_perm:[0,1,2,3] row_mask:0x8 bank_mask:0xf\n" \
"v_mov_b32_dpp v32, v36 quad_perm:[0,1,2,3] row_mask:0x2 bank_mask:0xf\n" \
"v_rcp_f32_e64 v45, v45\n" \
"ds_write_b128 v27, v[76:79] offset:5120\n" \
"v_mov_b32_dpp v33, v37 quad_perm:[0,1,2,3] row_mask:0x2 bank_mask:0xf\n" \
"v_rcp_f32_e64 v34, v34\n" \
"v_mov_b32_dpp v32, v248 quad_perm:[0,1,2,3] row_mask:0x4 bank_mask:0xf\n" \
"v_mov_b32_dpp v33, v249 quad_perm:[0,1,2,3] row_mask:0x4 bank_mask:0xf\n" \
"v_mul_f32_e64 v34, v45, v34\n" \
"v_mov_b32_dpp v32, v128 quad_perm:[0,1,2,3] row_mask:0x8 bank_mask:0xf\n" \
"v_mov_b32_dpp v33, v129 quad_perm:[0,1,2,3] row_mask:0x8 bank_mask:0xf\n" \
"v_mul_f32_e32 v32, v34, v32\n" \
"v_mul_f32_e32 v33, v34, v33\n" \
"v_mov_b32_e64 v38, v34\n" \
M_"v[68:71], a[240:243], a[196:199], v[68:71]\n" \
"v_sub_f32_e32 v44, 1.0, v45\n" \
"v_permlane16_swap_b32_e32 v34, v38\n" \
"v_mov_b32_e32 v250, v34\n" \
"v_mov_b32_e32 v130, v38\n" \
C_"v32, v32, v33\n" \
"v_permlane32_swap_b32_e32 v34, v250\n" \
"v_permlane32_swap_b32_e32 v38, v130\n" \
"v_mul_f32_e64 v192, v34, v192\n" \
"v_pk_mul_f32 v[68:69], v[68:69], s[70:71]\n" \
"v_pk_mul_f32 v[70:71], v[70:71], s[70:71]\n" \
C_"v68, v68, v69\n" \
C_"v69, v70, v71\n" \
C_"v70, v72, v73\n" \
C_"v71, v74, v75\n" \
"ds_write_b128 v27, v[68:71] offset:4096\n" \
"v_mul_f32_e32 v193, v34, v193\n" \
"v_mul_f32_e32 v208, v38, v208\n" \
"v_mul_f32_e32 v209, v38, v209\n" \
"v_mul_f32_e32 v224, v250, v224\n" \
"v_mul_f32_e32 v225, v250, v225\n" \
"v_mul_f32_e32 v240, v130, v240\n" \
"v_mul_f32_e32 v241, v130, v241\n" \
"v_mul_f32_e32 v194, v34, v194\n" \
"v_mul_f32_e64 v195, v34, v195\n" \
C_"v192, v192, v193\n" \
"v_mul_f32_e32 v210, v38, v210\n" \
"v_mul_f32_e32 v211, v38, v211\n" \
C_"v208, v208, v209\n" \
"v_mul_f32_e32 v226, v250, v226\n" \
"v_mul_f32_e32 v227, v250, v227\n" \
C_"v224, v224, v225\n" \
"v_mul_f32_e32 v242, v130, v242\n" \
"v_mul_f32_e32 v243, v130, v243\n" \
C_"v240, v240, v241\n" \
C_"v193, v194, v195\n" \
"s_add_u32 s84, s48, 0x0\n" \
"s_nop 0\n" \
C_"v209, v210, v211\n" \
"s_add_u32 s85, s48, 0x24800\n" \
C_"v225, v226, v227\n" \
"s_add_u32 s86, s48, 0x49000\n" \
C_"v241, v242, v243\n" \
"s_add_u32 s87, s48, 0x6d800\n" \
"ds_write_b32 v26, v44\n" \
"buffer_store_dword v32, v25, s[80:83], s49 offen sc1\n" \
"buffer_store_dwordx2 v[192:193], v24, s[80:83], s84 offen sc1\n" \
"buffer_store_dwordx2 v[208:209], v24, s[80:83], s85 offen sc1\n" \
"buffer_store_dwordx2 v[224:225], v24, s[80:83], s86 offen sc1\n" \
"buffer_store_dwordx2 v[240:241], v24, s[80:83], s87 offen sc1\n" \
"s_branch L_end_%=\n" \
"L_poll61_%=:\n" \
"s_mov_b32 s84, 0\n" \
"L_pl61_%=:\n" \
"s_sleep 1\n" \
"v_bfe_u32 v44, v20, 2, 5\n" \
"buffer_load_dword v44, v44, s[80:83], s38 offen offset:128 sc1\n" \
"s_waitcnt vmcnt(0)\n" \
"v_cmp_ne_u32_e32 vcc, s62, v44\n" \
"s_add_u32 s84, s84, 1\n" \
"s_nop 3\n" \
"s_cbranch_vccz L_pollr61_%=\n" \
"s_cmp_gt_u32 s84, 0x400000\n" \
"s_cbranch_scc0 L_pl61_%=\n" \
"s_branch L_pollr61_%=\n" \
"L_poll161_%=:\n" \
"s_mov_b32 s84, 0\n" \
"L_pl161_%=:\n" \
"s_sleep 1\n" \
"v_bfe_u32 v44, v20, 2, 5\n" \
"buffer_load_dword v44, v44, s[80:83], s38 offen offset:0 sc1\n" \
"s_waitcnt vmcnt(0)\n" \
"v_cmp_ne_u32_e32 vcc, s62, v44\n" \
"s_add_u32 s84, s84, 1\n" \
"s_nop 3\n" \
"s_cbranch_vccz L_pollr161_%=\n" \
"s_cmp_gt_u32 s84, 0x400000\n" \
"s_cbranch_scc0 L_pl161_%=\n" \
"s_branch L_pollr161_%=\n" \
"L_end_%=:\n"
#define PROJ_PAGES 20
#define PROJ_VCLOB "v21","v24","v25","v28","v29","v32","v33","v34","v35","v36","v37","v38","v39","v40","v41","v42","v43","v44","v45","v46","v47","v48","v49","v50","v51","v52","v53","v54","v55","v56","v57","v58","v59","v60","v61","v62","v63","v64","v65","v66","v67","v68","v69","v70","v71","v72","v73","v74","v75","v76","v77","v78","v79","v80","v81","v82","v83","v84","v85","v86","v87","v88","v89","v90","v91","v92","v93","v94","v95","v96","v97","v98","v99","v100","v101","v102","v103","v104","v105","v106","v107","v108","v109","v110","v111","v112","v113","v114","v115","v116","v117","v118","v119","v120","v121","v122","v123","v124","v125","v126","v127","v128","v129","v130","v131","v132","v133","v134","v135","v136","v137","v138","v139","v140","v141","v142","v143","v144","v145","v146","v147","v148","v149","v150","v151","v152","v153","v154","v155","v156","v157","v158","v159","v160","v161","v162","v163","v164","v165","v166","v167","v168","v169","v170","v171","v172","v173","v174","v175","v176","v177","v178","v179","v180","v181","v182","v183","v184","v185","v186","v187","v188","v189","v190","v192","v193","v194","v195","v196","v197","v198","v199","v200","v201","v202","v203","v204","v205","v206","v207","v208","v209","v210","v211","v212","v213","v214","v215","v216","v217","v218","v219","v220","v221","v222","v223","v224","v225","v226","v227","v228","v229","v230","v231","v232","v233","v234","v235","v236","v237","v238","v239","v240","v241","v242","v243","v244","v245","v246","v247","v248","v249","v250","v251","v252","v253","v254","v255"
#define PROJ_SCLOB "s4","s5","s6","s7","s32","s33","s34","s35","s36","s37","s38","s39","s40","s41","s42","s43","s44","s45","s46","s47","s48","s49","s50","s51","s52","s53","s54","s55","s63","s64","s65","s66","s67","s68","s69","s70","s71","s72","s73","s74","s75","s76","s77","s78","s79","s80","s81","s82","s83","s84","s85","s86","s87","s88","s89","s90","s91","s92","s93","s94","s95","s96","s97","s98","s99","s100","s101"

typedef const u16* const __attribute__((address_space(4)))* kaptr;
__device__ __forceinline__ void proj_phase(kaptr& ka, char*& ws, const unsigned wid, u32x4 (&vp)[5], unsigned& vl, u32x4 (&sa)[5], u32x2 s24, u32x2& s28, unsigned tdum, u32x4& srdA, u32x2& sd, unsigned& tag
    ) {
    const unsigned b = blockIdx.x, h = (b & 7) * 4 + ((b >> 3) & 3), mt = b >> 5;
    const unsigned rb = (wid < 2u ? 15u - mt : mt) * 128u;
    const unsigned lane = threadIdx.x & 63u, wc = wid & 1u;
    u32x2 vAc, vB;
#pragma unroll
    for (int par = 0; par < 2; ++par) {
        const unsigned lr = lane >> 3, cc = (lane & 7u) ^ (4u * par + (lr >> 1)), r0 = rb + wc * 64u + lr;
        vAc[par] = r0 * (DC * 2) + cc * 16u;
        const unsigned br = lane >> 4, p16 = lane & 15u, sc = (((p16 >> 1) ^ (br | (par << 2))) << 1) | (p16 & 1u);
        vB[par] = (16u * wid + br) * (NBIG * 2) + h * (HD * 2) + sc * 16u;
    }
    vl = lane * 16u;
    u32x2 s30 = {wid * 8192u, 65536u + wid * 4096u};
    asm volatile(PROJ_ASM
                 : "={s62}"(tag), "={s[56:59]}"(srdA), "={s[60:61]}"(sd), "+{s[8:11]}"(sa[0]), "+{s[12:15]}"(sa[1]), "+{s[16:19]}"(sa[2]), "+{s[20:23]}"(sa[3]), "+{s[24:25]}"(s24), "+{s[28:29]}"(s28), "+{s[30:31]}"(s30),
                   "={v[0:3]}"(vp[0]), "={v[4:7]}"(vp[1]), "={v[8:11]}"(vp[2]), "={v[12:15]}"(vp[3]), "={v[16:19]}"(vp[4]), "+{v[22:23]}"(vAc), "+{v[26:27]}"(vB), "+{v191}"(tdum), "+{s[26:27]}"(ws), "+{s[0:1]}"(ka)
                 : "{s2}"(b), "{s3}"(wid), "{v20}"(vl)
                 : "memory", "m0", "vcc", "scc", PROJ_VCLOB, PROJ_SCLOB, ACLOB_LO, ACLOB_HI);
    sa[4] = (u32x4){s24[0], 0u, 0u, 0u};
}

namespace att {
#define DI __device__ __forceinline__
typedef __bf16 bfx8 __attribute__((ext_vector_type(8)));
DI unsigned rfl(unsigned x) { return __builtin_amdgcn_readfirstlane(x); }
DI f32x4 mfma(u32x4 a, u32x4 b, f32x4 c) { return __builtin_amdgcn_mfma_f32_16x16x32_bf16(__builtin_bit_cast(bfx8, a), __builtin_bit_cast(bfx8, b), c, 0, 0, 0); }
typedef __bf16 bfx2 __attribute__((ext_vector_type(2)));
typedef float f32x2 __attribute__((ext_vector_type(2)));
DI unsigned cvtpk2(float lo, float hi) { f32x2 v = {lo, hi}; bfx2 b = __builtin_convertvector(v, bfx2); return __builtin_bit_cast(unsigned, b); }
DI void att_exact(const u16* __restrict__ x, const u16* __restrict__ wq, char* __restrict__ ws, unsigned h, unsigned r0, float g1) {
    const unsigned lane = lane_id(), g = lane >> 4, c = lane & 15u;
    f32x4 o[8];
#pragma unroll
    for (int n = 0; n < 8; ++n) o[n] = (f32x4){0.f, 0.f, 0.f, 0.f};
#pragma unroll 1
    for (unsigned kk = 0; kk < (unsigned)DM; kk += 32u) {
        const unsigned k = kk + 8u * g;
        const u32x4 xf = *reinterpret_cast<const u32x4*>(x + (r0 + c) * DM + k);
#pragma unroll
        for (int n = 0; n < 8; ++n) {
            const u16* w = wq + k * NBIG + h * HD + n * 16 + c; u32x4 wf;
#pragma unroll
            for (int e = 0; e < 4; ++e) wf[e] = (unsigned)w[2 * e * NBIG] | ((unsigned)w[(2 * e + 1) * NBIG] << 16);
            o[n] = mfma(wf, xf, o[n]);
        }
    }
    u32x4 q[4];
#pragma unroll
    for (int p = 0; p < 4; ++p) {
        const f32x4 a = o[2 * p] * 0.12751743082f, b = o[2 * p + 1] * 0.12751743082f;
        q[p] = (u32x4){cvtpk2(a[0], a[1]), cvtpk2(a[2], a[3]), cvtpk2(b[0], b[1]), cvtpk2(b[2], b[3])};
    }
#pragma unroll
    for (int n = 0; n < 8; ++n) o[n] = (f32x4){0.f, 0.f, 0.f, 0.f};
    const __amdgpu_buffer_rsrc_t srd = __builtin_amdgcn_make_buffer_rsrc((void*)(ws + WS_K + (size_t)h * 1048576), (short)0, 1048576, 0x00020000);
    float m = -3.0e38f, l = 0.f;
#pragma unroll 1
    for (unsigned k0 = 0; k0 <= r0 + 15u; k0 += 32u) {
        const unsigned tb = (k0 >> 6) * 32768u + (k0 & 32u) * 256u;
        f32x4 s[2]; float mx = m;
#pragma unroll
        for (int kt = 0; kt < 2; ++kt) {
            s[kt] = (f32x4){0.f, 0.f, 0.f, 0.f};
#pragma unroll
            for (int p = 0; p < 4; ++p) s[kt] = mfma(__builtin_amdgcn_raw_buffer_load_b128(srd, lane * 16u + (kt * 4 + p) * 1024u, tb, 16), q[p], s[kt]);
#pragma unroll
            for (int e = 0; e < 4; ++e) { if (k0 + 16u * kt + 4u * g + e > r0 + c) s[kt][e] = -3.0e38f; mx = fmaxf(mx, s[kt][e]); }
        }
        mx = fmaxf(mx, __shfl_xor(mx, 16)); mx = fmaxf(mx, __shfl_xor(mx, 32));
        const float al = __builtin_amdgcn_exp2f(m - mx); m = mx;
        u32x4 pf;
#pragma unroll
        for (int e = 0; e < 4; ++e) pf[e] = cvtpk2(__builtin_amdgcn_exp2f(s[e >> 1][2 * (e & 1)] - m), __builtin_amdgcn_exp2f(s[e >> 1][2 * (e & 1) + 1] - m));
        l *= al;
#pragma unroll
        for (int e = 0; e < 4; ++e) l += __uint_as_float(pf[e] << 16) + __uint_as_float(pf[e] & 0xffff0000u);
#pragma unroll
        for (int n = 0; n < 8; ++n) o[n] = mfma(__builtin_amdgcn_raw_buffer_load_b128(srd, lane * 16u + 16384u + n * 1024u, tb, 16), pf, o[n] * al);
    }
    l += __shfl_xor(l, 16); l += __shfl_xor(l, 32);
    const float f = g1 / l;
    const __amdgpu_buffer_rsrc_t srdA = __builtin_amdgcn_make_buffer_rsrc((void*)(ws + WS_AF), (short)0, 128 * KS_TOT * 1024, 0x00020000);
#pragma unroll
    for (int p = 0; p < 4; ++p) {
        const f32x4 a = o[2 * p] * f, b = o[2 * p + 1] * f;
        __builtin_amdgcn_raw_buffer_store_b128((u32x4){cvtpk2(a[0], a[1]), cvtpk2(a[2], a[3]), cvtpk2(b[0], b[1]), cvtpk2(b[2], b[3])}, srdA, lane * 16u + p * 1024u, ((r0 >> 4) * KS_TOT + 18u + 4u * h) * 1024u, 16);
    }
}
}

#define ATT_ASM \
"s_branch L_ago_%=\n" \
".p2align 12\n" \
"L_ab:\n" \
".fill (L_ab + 0 - .) / 4, 4, 0xBF800000\n" \
"L_ap0:\n" \
"s_setpc_b64 s[88:89]\n" \
"L_ago_%=:\n" \
"s_mov_b32 s101, 0\n" \
"s_waitcnt vmcnt(15) lgkmcnt(0)\n" \
"s_barrier\n" \
"v_mfma_f32_32x32x16_bf16 v[128:143], a[120:123], a[120:123], 0\n" \
"v_and_b32_e32 v63, 0x300, v20\n" \
R_"v[96:99], v63 offset:0\n" \
"v_bfe_u32 v61, v20, 2, 5\n" \
"s_mov_b32 s96, s24\n" \
"v_add_u32_e32 v58, s22, v20\n" \
R_"a[0:3], v58 offset:0\n" \
"v_mfma_f32_32x32x16_bf16 v[144:159], a[120:123], a[120:123], 0\n" \
"buffer_load_dword v57, v61, s[56:59], s96 offen sc1\n" \
R_"a[16:19], v58 offset:8192\n" \
"v_add_u32_e32 v59, s23, v20\n" \
R_"a[32:35], v59 offset:0\n" \
R_"a[48:51], v59 offset:8192\n" \
"v_mfma_f32_32x32x16_bf16 v[160:175], a[120:123], a[120:123], 0\n" \
R_"v[100:103], v63 offset:1024\n" \
R_"a[4:7], v58 offset:1024\n" \
R_"a[20:23], v58 offset:9216\n" \
"s_sub_u32 s67, s12, 0xc000\n" \
"s_mov_b32 s65, s12\n" \
"v_add_u32_e32 v55, s67, v20\n" \
"v_mfma_f32_32x32x16_bf16 v[176:191], a[120:123], a[120:123], 0\n" \
"v_add_u32_e32 v52, 0xc000, v55\n" \
"s_xor_b32 m0, s65, 0x10000\n" \
R_"a[36:39], v59 offset:1024\n" \
B_"v52, s[8:11], 0 offen offset:0 sc1 lds\n" \
R_"a[52:55], v59 offset:9216\n" \
"v_mfma_f32_32x32x16_bf16 v[192:207], a[120:123], a[120:123], 0\n" \
R_"v[104:107], v63 offset:2048\n" \
R_"a[8:11], v58 offset:2048\n" \
R_"a[24:27], v58 offset:10240\n" \
B_"v52, s[8:11], 0 offen offset:1024 sc1 lds\n" \
R_"a[40:43], v59 offset:2048\n" \
"v_mfma_f32_32x32x16_bf16 v[208:223], a[120:123], a[120:123], 0\n" \
R_"a[56:59], v59 offset:10240\n" \
R_"v[108:111], v63 offset:3072\n" \
R_"a[12:15], v58 offset:3072\n" \
R_"a[28:31], v58 offset:11264\n" \
"s_waitcnt lgkmcnt(15)\n" \
"v_xor_b32_e32 v96, 0x80008000, v96\n" \
"v_mfma_f32_32x32x16_bf16 v[224:239], a[120:123], a[120:123], 0\n" \
"v_xor_b32_e32 v97, 0x80008000, v97\n" \
"v_xor_b32_e32 v98, 0x80008000, v98\n" \
"v_xor_b32_e32 v99, 0x80008000, v99\n" \
R_"a[44:47], v59 offset:3072\n" \
R_"a[60:63], v59 offset:11264\n" \
R_"a[160:163], v20 offset:0\n" \
"v_mfma_f32_32x32x16_bf16 v[240:255], a[120:123], a[120:123], 0\n" \
R_"a[164:167], v20 offset:1024\n" \
"s_waitcnt lgkmcnt(15)\n" \
"v_xor_b32_e32 v100, 0x80008000, v100\n" \
"v_xor_b32_e32 v101, 0x80008000, v101\n" \
"v_xor_b32_e32 v102, 0x80008000, v102\n" \
"v_xor_b32_e32 v103, 0x80008000, v103\n" \
M_"v[32:35], v[96:99], a[0:3], 0\n" \
R_"a[168:171], v20 offset:2048\n" \
R_"a[172:175], v20 offset:3072\n" \
M_"v[36:39], v[96:99], a[16:19], 0\n" \
R_"a[188:191], v20 offset:7168\n" \
R_"a[184:187], v20 offset:6144\n" \
M_"v[40:43], v[96:99], a[32:35], 0\n" \
"s_waitcnt lgkmcnt(15)\n" \
"v_xor_b32_e32 v104, 0x80008000, v104\n" \
M_"v[44:47], v[96:99], a[48:51], 0\n" \
"v_xor_b32_e32 v105, 0x80008000, v105\n" \
"v_xor_b32_e32 v106, 0x80008000, v106\n" \
M_"v[32:35], v[100:103], a[4:7], v[32:35]\n" \
"v_xor_b32_e32 v107, 0x80008000, v107\n" \
R_"a[180:183], v20 offset:5120\n" \
M_"v[36:39], v[100:103], a[20:23], v[36:39]\n" \
R_"a[176:179], v20 offset:4096\n" \
"s_waitcnt lgkmcnt(12)\n" \
"v_xor_b32_e32 v108, 0x80008000, v108\n" \
M_"v[40:43], v[100:103], a[36:39], v[40:43]\n" \
"v_xor_b32_e32 v109, 0x80008000, v109\n" \
"v_xor_b32_e32 v110, 0x80008000, v110\n" \
M_"v[44:47], v[100:103], a[52:55], v[44:47]\n" \
"v_xor_b32_e32 v111, 0x80008000, v111\n" \
"v_add_u32_e32 v60, s15, v20\n" \
M_"v[32:35], v[104:107], a[8:11], v[32:35]\n" \
"v_add_u32_e32 v60, 0x10000, v60\n" \
R_"a[128:131], v20 offset:8192\n" \
M_"v[36:39], v[104:107], a[24:27], v[36:39]\n" \
R_"a[144:147], v20 offset:12288\n" \
R_"a[132:135], v20 offset:9216\n" \
M_"v[40:43], v[104:107], a[40:43], v[40:43]\n" \
R_"a[148:151], v20 offset:13312\n" \
R_"a[136:139], v20 offset:10240\n" \
M_"v[44:47], v[104:107], a[56:59], v[44:47]\n" \
R_"a[152:155], v20 offset:14336\n" \
"s_waitcnt lgkmcnt(13)\n" \
M_"v[32:35], v[108:111], a[12:15], v[32:35]\n" \
R_"a[140:143], v20 offset:11264\n" \
R_"a[156:159], v20 offset:15360\n" \
M_"v[36:39], v[108:111], a[28:31], v[36:39]\n" \
R_"a[64:67], v60 offset:16384\n" \
R_"a[68:71], v60 offset:17408\n" \
M_"v[40:43], v[108:111], a[44:47], v[40:43]\n" \
R_"a[72:75], v60 offset:18432\n" \
R_"a[76:79], v60 offset:19456\n" \
M_"v[44:47], v[108:111], a[60:63], v[44:47]\n" \
R_"a[80:83], v60 offset:24576\n" \
R_"a[84:87], v60 offset:25600\n" \
M_"v[64:67], a[160:163], a[0:3], v[32:35]\n" \
R_"a[88:91], v60 offset:26624\n" \
"s_waitcnt lgkmcnt(15)\n" \
M_"v[64:67], a[164:167], a[4:7], v[64:67]\n" \
R_"a[92:95], v60 offset:27648\n" \
R_"a[192:195], v60 offset:0\n" \
M_"v[64:67], a[168:171], a[8:11], v[64:67]\n" \
R_"a[196:199], v60 offset:1024\n" \
R_"a[200:203], v60 offset:2048\n" \
M_"v[64:67], a[172:175], a[12:15], v[64:67]\n" \
R_"a[204:207], v60 offset:3072\n" \
R_"a[208:211], v60 offset:8192\n" \
M_"v[68:71], a[188:191], a[12:15], v[32:35]\n" \
R_"a[212:215], v60 offset:9216\n" \
R_"a[216:219], v60 offset:10240\n" \
M_"v[68:71], a[184:187], a[8:11], v[68:71]\n" \
R_"a[220:223], v60 offset:11264\n" \
"v_mov_b32_e32 v56, s62\n" \
M_"v[68:71], a[180:183], a[4:7], v[68:71]\n" \
"v_readlane_b32 s68, v11, 0\n" \
"s_mov_b32 m0, s20\n" \
M_"v[68:71], a[176:179], a[0:3], v[68:71]\n" \
"global_load_lds_dword v[16:17], off\n" \
M_"v[76:79], a[176:179], a[16:19], v[36:39]\n" \
E_"v66, v66\n" \
M_"v[76:79], a[180:183], a[20:23], v[76:79]\n" \
E_"v67, v67\n" \
M_"v[76:79], a[184:187], a[24:27], v[76:79]\n" \
E_"v64, v64\n" \
M_"v[76:79], a[188:191], a[28:31], v[76:79]\n" \
E_"v70, v70\n" \
M_"v[72:75], a[172:175], a[28:31], v[36:39]\n" \
E_"v71, v71\n" \
M_"v[72:75], a[168:171], a[24:27], v[72:75]\n" \
E_"v68, v68\n" \
M_"v[72:75], a[164:167], a[20:23], v[72:75]\n" \
E_"v69, v69\n" \
M_"v[72:75], a[160:163], a[16:19], v[72:75]\n" \
C_"v71, v70, v71\n" \
"v_accvgpr_read_b32 v28, a124\n" \
M_"v[80:83], a[160:163], a[32:35], v[40:43]\n" \
E_"v65, v65\n" \
M_"v[80:83], a[164:167], a[36:39], v[80:83]\n" \
E_"v76, v76\n" \
M_"v[80:83], a[168:171], a[40:43], v[80:83]\n" \
E_"v77, v77\n" \
M_"v[80:83], a[172:175], a[44:47], v[80:83]\n" \
E_"v72, v72\n" \
M_"v[84:87], a[188:191], a[44:47], v[40:43]\n" \
E_"v73, v73\n" \
M_"v[84:87], a[184:187], a[40:43], v[84:87]\n" \
E_"v74, v74\n" \
M_"v[84:87], a[180:183], a[36:39], v[84:87]\n" \
E_"v75, v75\n" \
M_"v[84:87], a[176:179], a[32:35], v[84:87]\n" \
C_"v72, v72, v73\n" \
"v_accvgpr_read_b32 v29, a125\n" \
M_"v[92:95], a[176:179], a[48:51], v[44:47]\n" \
E_"v78, v78\n" \
M_"v[92:95], a[180:183], a[52:55], v[92:95]\n" \
"s_waitcnt vmcnt(3)\n" \
"v_cmp_ne_u32_e32 vcc, s62, v57\n" \
M_"v[92:95], a[184:187], a[56:59], v[92:95]\n" \
E_"v79, v79\n" \
M_"v[92:95], a[188:191], a[60:63], v[92:95]\n" \
"v_accvgpr_read_b32 v21, a126\n" \
C_"v70, v68, v69\n" \
M_"v[88:91], a[172:175], a[60:63], v[44:47]\n" \
"s_cbranch_vccnz L_skb_%=\n" \
"L_rkb_%=:\n" \
C_"v73, v74, v75\n" \
"s_mov_b64 exec, 1\n" \
"buffer_store_dword v56, off, s[56:59], s68 offset:0 sc1\n" \
"s_mov_b64 exec, -1\n" \
M_"v[88:91], a[168:171], a[56:59], v[88:91]\n" \
"v_mov_b32_e32 v62, 0\n" \
"s_mov_b32 s64, 0x8000\n" \
M_"v[88:91], a[164:167], a[52:55], v[88:91]\n" \
"v_add_u32_e32 v50, 0x6000, v55\n" \
"v_add_u32_e32 v51, 0xa000, v55\n" \
M_"v[88:91], a[160:163], a[48:51], v[88:91]\n" \
"v_add_u32_e32 v53, 0x10000, v55\n" \
"v_add_u32_e32 v48, 0xc000, v20\n" \
"s_waitcnt vmcnt(5) lgkmcnt(0)\n" \
"s_barrier\n" \
"v_add_u32_e32 v49, s17, v20\n" \
"v_accvgpr_write_b32 a124, v96\n" \
"v_accvgpr_write_b32 a125, v97\n" \
"v_accvgpr_write_b32 a126, v98\n" \
"v_accvgpr_write_b32 a127, v99\n" \
"v_accvgpr_write_b32 a224, v100\n" \
"v_accvgpr_write_b32 a225, v101\n" \
"v_accvgpr_write_b32 a226, v102\n" \
"v_accvgpr_write_b32 a227, v103\n" \
"v_accvgpr_write_b32 a228, v104\n" \
"v_accvgpr_write_b32 a229, v105\n" \
"v_accvgpr_write_b32 a230, v106\n" \
"v_accvgpr_write_b32 a231, v107\n" \
"v_accvgpr_write_b32 a232, v108\n" \
"v_accvgpr_write_b32 a233, v109\n" \
"v_accvgpr_write_b32 a234, v110\n" \
"v_accvgpr_write_b32 a235, v111\n" \
C_"v69, v66, v67\n" \
C_"v68, v64, v65\n" \
C_"v74, v76, v77\n" \
C_"v75, v78, v79\n" \
"s_sub_u32 s66, s18, 1\n" \
"s_cbranch_scc1 L_tr_%=\n" \
"s_branch L_1_%=\n" \
".fill (L_ab + 1528 - .) / 4, 4, 0xBF800000\n" \
"L_tL_1_%=:\n" \
"s_waitcnt vmcnt(4) lgkmcnt(0)\n" \
"v_smfmac_f32_16x16x64_bf16 a[100:103], a[104:107], v[116:123], v21\n" \
"s_barrier\n" \
"L_1_%=:\n" \
M_"v[96:99], a[128:131], a[0:3], v[32:35]\n" \
"s_xor_b32 m0, s65, 0x12000\n" \
R_"a[160:163], v49 offset:0\n" \
M_"v[96:99], a[132:135], a[4:7], v[96:99]\n" \
B_"v50, s[8:11], s64 offen offset:0 sc1 lds\n" \
M_"v[96:99], a[136:139], a[8:11], v[96:99]\n" \
R_"a[164:167], v49 offset:1024\n" \
R_"a[168:171], v49 offset:2048\n" \
M_"v[96:99], a[140:143], a[12:15], v[96:99]\n" \
R_"a[172:175], v49 offset:3072\n" \
R_"a[176:179], v49 offset:4096\n" \
M_"v[100:103], a[156:159], a[12:15], v[32:35]\n" \
R_"a[180:183], v49 offset:5120\n" \
R_"a[184:187], v49 offset:6144\n" \
M_"v[100:103], a[152:155], a[8:11], v[100:103]\n" \
"v_exp_f32_e64 v86, v86\n" \
M_"v[100:103], a[148:151], a[4:7], v[100:103]\n" \
"v_exp_f32_e64 v87, v87\n" \
M_"v[100:103], a[144:147], a[0:3], v[100:103]\n" \
R_"a[188:191], v49 offset:7168\n" \
C_"v87, v86, v87\n" \
M_"v[108:111], a[144:147], a[16:19], v[36:39]\n" \
"v_exp_f32_e64 v84, v84\n" \
M_"v[108:111], a[148:151], a[20:23], v[108:111]\n" \
"v_exp_f32_e64 v85, v85\n" \
M_"v[108:111], a[152:155], a[24:27], v[108:111]\n" \
"v_exp_f32_e64 v82, v82\n" \
M_"v[108:111], a[156:159], a[28:31], v[108:111]\n" \
"v_exp_f32_e64 v83, v83\n" \
M_"v[104:107], a[140:143], a[28:31], v[36:39]\n" \
"v_exp_f32_e64 v80, v80\n" \
M_"v[104:107], a[136:139], a[24:27], v[104:107]\n" \
"v_exp_f32_e64 v81, v81\n" \
M_"v[104:107], a[132:135], a[20:23], v[104:107]\n" \
"v_exp_f32_e64 v88, v88\n" \
M_"v[104:107], a[128:131], a[16:19], v[104:107]\n" \
B_"v50, s[8:11], s64 offen offset:1024 sc1 lds\n" \
M_"v[112:115], a[128:131], a[32:35], v[40:43]\n" \
"s_xor_b32 m0, s65, 0x6000\n" \
C_"v86, v84, v85\n" \
M_"v[112:115], a[132:135], a[36:39], v[112:115]\n" \
C_"v85, v82, v83\n" \
C_"v84, v80, v81\n" \
M_"v[112:115], a[136:139], a[40:43], v[112:115]\n" \
"v_exp_f32_e64 v89, v89\n" \
M_"v[112:115], a[140:143], a[44:47], v[112:115]\n" \
"v_exp_f32_e64 v90, v90\n" \
M_"v[116:119], a[156:159], a[44:47], v[40:43]\n" \
"v_exp_f32_e64 v91, v91\n" \
M_"v[116:119], a[152:155], a[40:43], v[116:119]\n" \
"v_exp_f32_e64 v92, v92\n" \
M_"v[116:119], a[148:151], a[36:39], v[116:119]\n" \
"v_exp_f32_e64 v93, v93\n" \
M_"v[116:119], a[144:147], a[32:35], v[116:119]\n" \
"v_exp_f32_e64 v94, v94\n" \
M_"v[124:127], a[144:147], a[48:51], v[44:47]\n" \
C_"v88, v88, v89\n" \
C_"v89, v90, v91\n" \
M_"v[124:127], a[148:151], a[52:55], v[124:127]\n" \
"v_exp_f32_e64 v95, v95\n" \
M_"v[124:127], a[152:155], a[56:59], v[124:127]\n" \
C_"v90, v92, v93\n" \
C_"v91, v94, v95\n" \
M_"v[124:127], a[156:159], a[60:63], v[124:127]\n" \
R_"a[144:147], v48 offset:4096\n" \
R_"a[148:151], v48 offset:5120\n" \
M_"v[120:123], a[140:143], a[60:63], v[44:47]\n" \
R_"a[152:155], v48 offset:6144\n" \
R_"a[140:143], v48 offset:3072\n" \
M_"v[120:123], a[136:139], a[56:59], v[120:123]\n" \
R_"a[136:139], v48 offset:2048\n" \
R_"a[156:159], v48 offset:7168\n" \
M_"v[120:123], a[132:135], a[52:55], v[120:123]\n" \
B_"v51, s[8:11], s64 offen offset:0 sc1 lds\n" \
M_"v[120:123], a[128:131], a[48:51], v[120:123]\n" \
R_"a[128:131], v48 offset:0\n" \
"s_waitcnt lgkmcnt(7)\n" \
M_"v[128:131], a[160:163], v[68:71], v[128:131]\n" \
E_"v102, v102\n" \
M_"v[160:163], a[160:163], v[72:75], v[160:163]\n" \
"v_exp_f32_e64 v103, v103\n" \
M_"v[132:135], a[164:167], v[68:71], v[132:135]\n" \
R_"a[132:135], v48 offset:1024\n" \
C_"v103, v102, v103\n" \
M_"v[164:167], a[164:167], v[72:75], v[164:167]\n" \
"v_exp_f32_e64 v100, v100\n" \
M_"v[136:139], a[168:171], v[68:71], v[136:139]\n" \
"v_exp_f32_e64 v101, v101\n" \
M_"v[168:171], a[168:171], v[72:75], v[168:171]\n" \
"v_exp_f32_e64 v98, v98\n" \
M_"v[140:143], a[172:175], v[68:71], v[140:143]\n" \
"v_exp_f32_e64 v99, v99\n" \
M_"v[172:175], a[172:175], v[72:75], v[172:175]\n" \
"v_exp_f32_e64 v96, v96\n" \
M_"v[144:147], a[176:179], v[68:71], v[144:147]\n" \
"v_exp_f32_e64 v97, v97\n" \
M_"v[176:179], a[176:179], v[72:75], v[176:179]\n" \
"v_exp_f32_e64 v104, v104\n" \
M_"v[148:151], a[180:183], v[68:71], v[148:151]\n" \
"v_exp_f32_e64 v105, v105\n" \
M_"v[180:183], a[180:183], v[72:75], v[180:183]\n" \
C_"v102, v100, v101\n" \
C_"v101, v98, v99\n" \
M_"v[152:155], a[184:187], v[68:71], v[152:155]\n" \
C_"v100, v96, v97\n" \
C_"v104, v104, v105\n" \
M_"v[184:187], a[184:187], v[72:75], v[184:187]\n" \
B_"v51, s[8:11], s64 offen offset:1024 sc1 lds\n" \
M_"v[156:159], a[188:191], v[68:71], v[156:159]\n" \
"v_exp_f32_e64 v106, v106\n" \
M_"v[188:191], a[188:191], v[72:75], v[188:191]\n" \
"v_exp_f32_e64 v107, v107\n" \
"v_smfmac_f32_16x16x64_bf16 a[96:99], a[104:107], v[68:75], v21\n" \
"v_exp_f32_e64 v108, v108\n" \
M_"v[220:223], a[188:191], v[84:87], v[220:223]\n" \
"v_exp_f32_e64 v109, v109\n" \
M_"v[252:255], a[188:191], v[88:91], v[252:255]\n" \
"v_exp_f32_e64 v110, v110\n" \
M_"v[216:219], a[184:187], v[84:87], v[216:219]\n" \
"v_exp_f32_e64 v111, v111\n" \
M_"v[248:251], a[184:187], v[88:91], v[248:251]\n" \
"v_exp_f32_e64 v118, v118\n" \
M_"v[212:215], a[180:183], v[84:87], v[212:215]\n" \
"v_exp_f32_e64 v119, v119\n" \
M_"v[244:247], a[180:183], v[88:91], v[244:247]\n" \
C_"v105, v106, v107\n" \
C_"v106, v108, v109\n" \
M_"v[208:211], a[176:179], v[84:87], v[208:211]\n" \
C_"v107, v110, v111\n" \
C_"v119, v118, v119\n" \
M_"v[240:243], a[176:179], v[88:91], v[240:243]\n" \
"v_exp_f32_e64 v116, v116\n" \
M_"v[204:207], a[172:175], v[84:87], v[204:207]\n" \
"v_exp_f32_e64 v117, v117\n" \
M_"v[236:239], a[172:175], v[88:91], v[236:239]\n" \
"v_exp_f32_e64 v114, v114\n" \
M_"v[200:203], a[168:171], v[84:87], v[200:203]\n" \
"v_exp_f32_e64 v115, v115\n" \
M_"v[232:235], a[168:171], v[88:91], v[232:235]\n" \
"v_exp_f32_e64 v112, v112\n" \
M_"v[196:199], a[164:167], v[84:87], v[196:199]\n" \
"v_exp_f32_e64 v113, v113\n" \
M_"v[228:231], a[164:167], v[88:91], v[228:231]\n" \
"v_exp_f32_e64 v120, v120\n" \
M_"v[192:195], a[160:163], v[84:87], v[192:195]\n" \
"v_exp_f32_e64 v121, v121\n" \
M_"v[224:227], a[160:163], v[88:91], v[224:227]\n" \
C_"v118, v116, v117\n" \
"s_waitcnt vmcnt(4) lgkmcnt(0)\n" \
"v_smfmac_f32_16x16x64_bf16 a[100:103], a[104:107], v[84:91], v21\n" \
"s_barrier\n" \
M_"v[64:67], a[128:131], a[0:3], v[32:35]\n" \
"s_xor_b32 m0, s65, 0x14000\n" \
R_"a[160:163], v49 offset:8192\n" \
M_"v[64:67], a[132:135], a[4:7], v[64:67]\n" \
B_"v52, s[8:11], s64 offen offset:0 sc1 lds\n" \
M_"v[64:67], a[136:139], a[8:11], v[64:67]\n" \
R_"a[164:167], v49 offset:9216\n" \
R_"a[168:171], v49 offset:10240\n" \
M_"v[64:67], a[140:143], a[12:15], v[64:67]\n" \
R_"a[172:175], v49 offset:11264\n" \
R_"a[176:179], v49 offset:12288\n" \
M_"v[68:71], a[156:159], a[12:15], v[32:35]\n" \
R_"a[180:183], v49 offset:13312\n" \
R_"a[184:187], v49 offset:14336\n" \
M_"v[68:71], a[152:155], a[8:11], v[68:71]\n" \
R_"a[188:191], v49 offset:15360\n" \
C_"v117, v114, v115\n" \
M_"v[68:71], a[148:151], a[4:7], v[68:71]\n" \
C_"v116, v112, v113\n" \
C_"v120, v120, v121\n" \
M_"v[68:71], a[144:147], a[0:3], v[68:71]\n" \
"v_exp_f32_e64 v122, v122\n" \
M_"v[76:79], a[144:147], a[16:19], v[36:39]\n" \
"v_exp_f32_e64 v123, v123\n" \
M_"v[76:79], a[148:151], a[20:23], v[76:79]\n" \
"v_exp_f32_e64 v124, v124\n" \
M_"v[76:79], a[152:155], a[24:27], v[76:79]\n" \
"v_exp_f32_e64 v125, v125\n" \
M_"v[76:79], a[156:159], a[28:31], v[76:79]\n" \
"v_exp_f32_e64 v126, v126\n" \
M_"v[72:75], a[140:143], a[28:31], v[36:39]\n" \
"v_exp_f32_e64 v127, v127\n" \
M_"v[72:75], a[136:139], a[24:27], v[72:75]\n" \
C_"v121, v122, v123\n" \
C_"v122, v124, v125\n" \
M_"v[72:75], a[132:135], a[20:23], v[72:75]\n" \
C_"v123, v126, v127\n" \
"v_add_u32_e32 v49, 0x10000, v48\n" \
M_"v[72:75], a[128:131], a[16:19], v[72:75]\n" \
B_"v52, s[8:11], s64 offen offset:1024 sc1 lds\n" \
M_"v[80:83], a[128:131], a[32:35], v[40:43]\n" \
"v_exp_f32_e64 v70, v70\n" \
M_"v[80:83], a[132:135], a[36:39], v[80:83]\n" \
"v_exp_f32_e64 v71, v71\n" \
M_"v[80:83], a[136:139], a[40:43], v[80:83]\n" \
"s_mov_b32 m0, s65\n" \
C_"v71, v70, v71\n" \
M_"v[80:83], a[140:143], a[44:47], v[80:83]\n" \
E_"v72, v72\n" \
M_"v[84:87], a[156:159], a[44:47], v[40:43]\n" \
"v_exp_f32_e64 v73, v73\n" \
M_"v[84:87], a[152:155], a[40:43], v[84:87]\n" \
"v_exp_f32_e64 v68, v68\n" \
M_"v[84:87], a[148:151], a[36:39], v[84:87]\n" \
"v_exp_f32_e64 v69, v69\n" \
M_"v[84:87], a[144:147], a[32:35], v[84:87]\n" \
"v_exp_f32_e64 v74, v74\n" \
M_"v[92:95], a[144:147], a[48:51], v[44:47]\n" \
R_"a[144:147], v48 offset:12288\n" \
C_"v72, v72, v73\n" \
M_"v[92:95], a[148:151], a[52:55], v[92:95]\n" \
R_"a[148:151], v48 offset:13312\n" \
C_"v70, v68, v69\n" \
M_"v[92:95], a[152:155], a[56:59], v[92:95]\n" \
R_"a[152:155], v48 offset:14336\n" \
"s_xor_b32 s65, s65, 0x4000\n" \
M_"v[92:95], a[156:159], a[60:63], v[92:95]\n" \
"v_exp_f32_e64 v75, v75\n" \
M_"v[88:91], a[140:143], a[60:63], v[44:47]\n" \
R_"a[140:143], v48 offset:11264\n" \
R_"a[156:159], v48 offset:15360\n" \
M_"v[88:91], a[136:139], a[56:59], v[88:91]\n" \
R_"a[136:139], v48 offset:10240\n" \
C_"v73, v74, v75\n" \
M_"v[88:91], a[132:135], a[52:55], v[88:91]\n" \
B_"v53, s[8:11], s64 offen offset:0 sc1 lds\n" \
M_"v[88:91], a[128:131], a[48:51], v[88:91]\n" \
R_"a[128:131], v48 offset:8192\n" \
"s_waitcnt lgkmcnt(7)\n" \
M_"v[128:131], a[160:163], v[100:103], v[128:131]\n" \
E_"v66, v66\n" \
M_"v[160:163], a[160:163], v[104:107], v[160:163]\n" \
"v_exp_f32_e64 v67, v67\n" \
M_"v[132:135], a[164:167], v[100:103], v[132:135]\n" \
R_"a[132:135], v48 offset:9216\n" \
C_"v69, v66, v67\n" \
M_"v[164:167], a[164:167], v[104:107], v[164:167]\n" \
"v_exp_f32_e64 v64, v64\n" \
M_"v[136:139], a[168:171], v[100:103], v[136:139]\n" \
"v_exp_f32_e64 v65, v65\n" \
M_"v[168:171], a[168:171], v[104:107], v[168:171]\n" \
"v_exp_f32_e64 v76, v76\n" \
M_"v[140:143], a[172:175], v[100:103], v[140:143]\n" \
"v_exp_f32_e64 v77, v77\n" \
M_"v[172:175], a[172:175], v[104:107], v[172:175]\n" \
"v_exp_f32_e64 v78, v78\n" \
M_"v[144:147], a[176:179], v[100:103], v[144:147]\n" \
"v_exp_f32_e64 v79, v79\n" \
M_"v[176:179], a[176:179], v[104:107], v[176:179]\n" \
C_"v68, v64, v65\n" \
C_"v74, v76, v77\n" \
M_"v[148:151], a[180:183], v[100:103], v[148:151]\n" \
C_"v75, v78, v79\n" \
"v_xor_b32_e32 v48, 0x4000, v48\n" \
M_"v[180:183], a[180:183], v[104:107], v[180:183]\n" \
M_"v[152:155], a[184:187], v[100:103], v[152:155]\n" \
M_"v[184:187], a[184:187], v[104:107], v[184:187]\n" \
B_"v53, s[8:11], s64 offen offset:1024 sc1 lds\n" \
M_"v[156:159], a[188:191], v[100:103], v[156:159]\n" \
"s_add_u32 s64, s64, 0x8000\n" \
"s_sub_u32 s66, s66, 1\n" \
"s_nop 0\n" \
M_"v[188:191], a[188:191], v[104:107], v[188:191]\n" \
"v_smfmac_f32_16x16x64_bf16 a[96:99], a[104:107], v[100:107], v21\n" \
M_"v[220:223], a[188:191], v[116:119], v[220:223]\n" \
M_"v[252:255], a[188:191], v[120:123], v[252:255]\n" \
M_"v[216:219], a[184:187], v[116:119], v[216:219]\n" \
M_"v[248:251], a[184:187], v[120:123], v[248:251]\n" \
M_"v[212:215], a[180:183], v[116:119], v[212:215]\n" \
M_"v[244:247], a[180:183], v[120:123], v[244:247]\n" \
M_"v[208:211], a[176:179], v[116:119], v[208:211]\n" \
M_"v[240:243], a[176:179], v[120:123], v[240:243]\n" \
M_"v[204:207], a[172:175], v[116:119], v[204:207]\n" \
M_"v[236:239], a[172:175], v[120:123], v[236:239]\n" \
M_"v[200:203], a[168:171], v[116:119], v[200:203]\n" \
M_"v[232:235], a[168:171], v[120:123], v[232:235]\n" \
M_"v[196:199], a[164:167], v[116:119], v[196:199]\n" \
M_"v[228:231], a[164:167], v[120:123], v[228:231]\n" \
M_"v[192:195], a[160:163], v[116:119], v[192:195]\n" \
M_"v[224:227], a[160:163], v[120:123], v[224:227]\n" \
"s_cbranch_scc0 L_tL_1_%=\n" \
"s_waitcnt vmcnt(4) lgkmcnt(0)\n" \
"v_smfmac_f32_16x16x64_bf16 a[100:103], a[104:107], v[116:123], v21\n" \
"s_barrier\n" \
"L_tr_%=:\n" \
"s_setpc_b64 s[28:29]\n" \
".fill (L_ab + 4096 - .) / 4, 4, 0xBF800000\n" \
".fill (L_ab + 4096 - .) / 4, 4, 0xBF800000\n" \
"L_ap1:\n" \
"s_setpc_b64 s[88:89]\n" \
"L_gtr:\n" \
M_"v[96:99], a[128:131], a[0:3], v[32:35]\n" \
"s_xor_b32 m0, s65, 0x12000\n" \
"s_mov_b32 s65, s13\n" \
M_"v[96:99], a[132:135], a[4:7], v[96:99]\n" \
B_"v50, s[8:11], s64 offen offset:0 sc1 lds\n" \
M_"v[96:99], a[136:139], a[8:11], v[96:99]\n" \
"v_add_u32_e32 v63, s15, v20\n" \
"s_add_u32 s67, s14, 40960\n" \
M_"v[96:99], a[140:143], a[12:15], v[96:99]\n" \
R_"a[160:163], v49 offset:0\n" \
R_"a[164:167], v49 offset:1024\n" \
M_"v[100:103], a[156:159], a[12:15], v[32:35]\n" \
B_"v50, s[8:11], s64 offen offset:1024 sc1 lds\n" \
M_"v[100:103], a[152:155], a[8:11], v[100:103]\n" \
"v_add_u32_e32 v50, 0x6000, v63\n" \
"s_xor_b32 m0, s65, 0x8000\n" \
M_"v[100:103], a[148:151], a[4:7], v[100:103]\n" \
R_"a[168:171], v49 offset:2048\n" \
R_"a[172:175], v49 offset:3072\n" \
M_"v[100:103], a[144:147], a[0:3], v[100:103]\n" \
R_"a[176:179], v49 offset:4096\n" \
R_"a[180:183], v49 offset:5120\n" \
M_"v[108:111], a[144:147], a[16:19], v[36:39]\n" \
B_"v50, s[8:11], s67 offen offset:0 sc1 lds\n" \
M_"v[108:111], a[148:151], a[20:23], v[108:111]\n" \
R_"a[184:187], v49 offset:6144\n" \
R_"a[188:191], v49 offset:7168\n" \
M_"v[108:111], a[152:155], a[24:27], v[108:111]\n" \
"s_mov_b32 s64, s14\n" \
"v_add_u32_e32 v51, 0x12000, v63\n" \
M_"v[108:111], a[156:159], a[28:31], v[108:111]\n" \
E_"v86, v86\n" \
M_"v[104:107], a[140:143], a[28:31], v[36:39]\n" \
B_"v50, s[8:11], s67 offen offset:1024 sc1 lds\n" \
M_"v[104:107], a[136:139], a[24:27], v[104:107]\n" \
E_"v87, v87\n" \
M_"v[104:107], a[132:135], a[20:23], v[104:107]\n" \
E_"v84, v84\n" \
M_"v[104:107], a[128:131], a[16:19], v[104:107]\n" \
E_"v85, v85\n" \
M_"v[112:115], a[128:131], a[32:35], v[40:43]\n" \
C_"v87, v86, v87\n" \
C_"v86, v84, v85\n" \
M_"v[112:115], a[132:135], a[36:39], v[112:115]\n" \
B_"v50, s[8:11], s67 offen offset:2048 sc1 lds\n" \
M_"v[112:115], a[136:139], a[40:43], v[112:115]\n" \
E_"v82, v82\n" \
M_"v[112:115], a[140:143], a[44:47], v[112:115]\n" \
E_"v83, v83\n" \
M_"v[116:119], a[156:159], a[44:47], v[40:43]\n" \
E_"v80, v80\n" \
M_"v[116:119], a[152:155], a[40:43], v[116:119]\n" \
B_"v50, s[8:11], s67 offen offset:3072 sc1 lds\n" \
M_"v[116:119], a[148:151], a[36:39], v[116:119]\n" \
"s_xor_b32 m0, s65, 0xa000\n" \
C_"v85, v82, v83\n" \
M_"v[116:119], a[144:147], a[32:35], v[116:119]\n" \
E_"v81, v81\n" \
M_"v[124:127], a[144:147], a[48:51], v[44:47]\n" \
C_"v84, v80, v81\n" \
"v_readlane_b32 s93, v11, 22\n" \
M_"v[124:127], a[148:151], a[52:55], v[124:127]\n" \
B_"v51, s[8:11], s64 offen offset:0 sc1 lds\n" \
M_"v[124:127], a[152:155], a[56:59], v[124:127]\n" \
E_"v88, v88\n" \
M_"v[124:127], a[156:159], a[60:63], v[124:127]\n" \
E_"v89, v89\n" \
M_"v[120:123], a[140:143], a[60:63], v[44:47]\n" \
E_"v90, v90\n" \
M_"v[120:123], a[136:139], a[56:59], v[120:123]\n" \
E_"v91, v91\n" \
M_"v[120:123], a[132:135], a[52:55], v[120:123]\n" \
B_"v51, s[8:11], s64 offen offset:1024 sc1 lds\n" \
M_"v[120:123], a[128:131], a[48:51], v[120:123]\n" \
C_"v88, v88, v89\n" \
"s_waitcnt lgkmcnt(0)\n" \
M_"v[128:131], a[160:163], v[68:71], v[128:131]\n" \
E_"v92, v92\n" \
M_"v[160:163], a[160:163], v[72:75], v[160:163]\n" \
E_"v93, v93\n" \
M_"v[132:135], a[164:167], v[68:71], v[132:135]\n" \
B_"v51, s[8:11], s64 offen offset:2048 sc1 lds\n" \
M_"v[164:167], a[164:167], v[72:75], v[164:167]\n" \
E_"v94, v94\n" \
M_"v[136:139], a[168:171], v[68:71], v[136:139]\n" \
E_"v95, v95\n" \
M_"v[168:171], a[168:171], v[72:75], v[168:171]\n" \
C_"v89, v90, v91\n" \
C_"v90, v92, v93\n" \
M_"v[140:143], a[172:175], v[68:71], v[140:143]\n" \
B_"v51, s[8:11], s64 offen offset:3072 sc1 lds\n" \
M_"v[172:175], a[172:175], v[72:75], v[172:175]\n" \
C_"v91, v94, v95\n" \
"v_readlane_b32 s80, v11, 11\n" \
M_"v[144:147], a[176:179], v[68:71], v[144:147]\n" \
"v_readlane_b32 s81, v11, 12\n" \
"s_mov_b32 s82, 0x47fca0\n" \
M_"v[176:179], a[176:179], v[72:75], v[176:179]\n" \
"s_mov_b32 s83, 0x20000\n" \
"v_readlane_b32 s92, v11, 29\n" \
M_"v[148:151], a[180:183], v[68:71], v[148:151]\n" \
"s_add_u32 m0, s93, 0x0\n" \
R_"a[128:131], v48 offset:0\n" \
M_"v[180:183], a[180:183], v[72:75], v[180:183]\n" \
R_"a[144:147], v48 offset:4096\n" \
R_"a[132:135], v48 offset:1024\n" \
M_"v[152:155], a[184:187], v[68:71], v[152:155]\n" \
B_"v0, s[80:83], s92 offen offset:0 lds\n" \
M_"v[184:187], a[184:187], v[72:75], v[184:187]\n" \
R_"a[148:151], v48 offset:5120\n" \
R_"a[136:139], v48 offset:2048\n" \
M_"v[156:159], a[188:191], v[68:71], v[156:159]\n" \
R_"a[152:155], v48 offset:6144\n" \
R_"a[140:143], v48 offset:3072\n" \
M_"v[188:191], a[188:191], v[72:75], v[188:191]\n" \
R_"a[156:159], v48 offset:7168\n" \
"v_add_u32_e32 v52, 0x14000, v63\n" \
"v_smfmac_f32_16x16x64_bf16 a[96:99], a[104:107], v[68:75], v21\n" \
B_"v1, s[80:83], s92 offen offset:1024 lds\n" \
M_"v[220:223], a[188:191], v[84:87], v[220:223]\n" \
E_"v102, v102\n" \
M_"v[252:255], a[188:191], v[88:91], v[252:255]\n" \
E_"v103, v103\n" \
M_"v[216:219], a[184:187], v[84:87], v[216:219]\n" \
B_"v2, s[80:83], s92 offen offset:2048 lds\n" \
M_"v[248:251], a[184:187], v[88:91], v[248:251]\n" \
"v_add_u32_e32 v53, 0x20000, v63\n" \
C_"v103, v102, v103\n" \
M_"v[212:215], a[180:183], v[84:87], v[212:215]\n" \
E_"v100, v100\n" \
M_"v[244:247], a[180:183], v[88:91], v[244:247]\n" \
E_"v101, v101\n" \
M_"v[208:211], a[176:179], v[84:87], v[208:211]\n" \
B_"v3, s[80:83], s92 offen offset:3072 lds\n" \
M_"v[240:243], a[176:179], v[88:91], v[240:243]\n" \
E_"v98, v98\n" \
M_"v[204:207], a[172:175], v[84:87], v[204:207]\n" \
E_"v99, v99\n" \
M_"v[236:239], a[172:175], v[88:91], v[236:239]\n" \
E_"v96, v96\n" \
M_"v[200:203], a[168:171], v[84:87], v[200:203]\n" \
E_"v97, v97\n" \
M_"v[232:235], a[168:171], v[88:91], v[232:235]\n" \
E_"v104, v104\n" \
M_"v[196:199], a[164:167], v[84:87], v[196:199]\n" \
E_"v105, v105\n" \
M_"v[228:231], a[164:167], v[88:91], v[228:231]\n" \
C_"v102, v100, v101\n" \
C_"v101, v98, v99\n" \
M_"v[192:195], a[160:163], v[84:87], v[192:195]\n" \
C_"v100, v96, v97\n" \
C_"v104, v104, v105\n" \
M_"v[224:227], a[160:163], v[88:91], v[224:227]\n" \
E_"v106, v106\n" \
"v_smfmac_f32_16x16x64_bf16 a[100:103], a[104:107], v[84:91], v21\n" \
E_"v107, v107\n" \
"s_waitcnt vmcnt(14) lgkmcnt(0)\n" \
"s_barrier\n" \
M_"v[64:67], a[128:131], a[0:3], v[32:35]\n" \
"s_xor_b32 m0, s65, 0x18000\n" \
R_"a[160:163], v49 offset:8192\n" \
M_"v[64:67], a[132:135], a[4:7], v[64:67]\n" \
B_"v52, s[8:11], s64 offen offset:0 sc1 lds\n" \
M_"v[64:67], a[136:139], a[8:11], v[64:67]\n" \
R_"a[164:167], v49 offset:9216\n" \
R_"a[168:171], v49 offset:10240\n" \
M_"v[64:67], a[140:143], a[12:15], v[64:67]\n" \
R_"a[172:175], v49 offset:11264\n" \
R_"a[176:179], v49 offset:12288\n" \
M_"v[68:71], a[156:159], a[12:15], v[32:35]\n" \
B_"v52, s[8:11], s64 offen offset:1024 sc1 lds\n" \
M_"v[68:71], a[152:155], a[8:11], v[68:71]\n" \
R_"a[180:183], v49 offset:13312\n" \
R_"a[184:187], v49 offset:14336\n" \
M_"v[68:71], a[148:151], a[4:7], v[68:71]\n" \
E_"v108, v108\n" \
M_"v[68:71], a[144:147], a[0:3], v[68:71]\n" \
E_"v109, v109\n" \
M_"v[76:79], a[144:147], a[16:19], v[36:39]\n" \
E_"v110, v110\n" \
M_"v[76:79], a[148:151], a[20:23], v[76:79]\n" \
B_"v52, s[8:11], s64 offen offset:2048 sc1 lds\n" \
M_"v[76:79], a[152:155], a[24:27], v[76:79]\n" \
E_"v111, v111\n" \
M_"v[76:79], a[156:159], a[28:31], v[76:79]\n" \
C_"v105, v106, v107\n" \
C_"v106, v108, v109\n" \
M_"v[72:75], a[140:143], a[28:31], v[36:39]\n" \
C_"v107, v110, v111\n" \
R_"a[188:191], v49 offset:15360\n" \
M_"v[72:75], a[136:139], a[24:27], v[72:75]\n" \
B_"v52, s[8:11], s64 offen offset:3072 sc1 lds\n" \
M_"v[72:75], a[132:135], a[20:23], v[72:75]\n" \
"s_mov_b32 m0, s65\n" \
"v_add_u32_e32 v49, 0x10000, v48\n" \
M_"v[72:75], a[128:131], a[16:19], v[72:75]\n" \
E_"v118, v118\n" \
M_"v[80:83], a[128:131], a[32:35], v[40:43]\n" \
E_"v119, v119\n" \
M_"v[80:83], a[132:135], a[36:39], v[80:83]\n" \
E_"v116, v116\n" \
M_"v[80:83], a[136:139], a[40:43], v[80:83]\n" \
B_"v53, s[8:11], s64 offen offset:0 sc1 lds\n" \
M_"v[80:83], a[140:143], a[44:47], v[80:83]\n" \
E_"v117, v117\n" \
M_"v[84:87], a[156:159], a[44:47], v[40:43]\n" \
C_"v119, v118, v119\n" \
C_"v118, v116, v117\n" \
M_"v[84:87], a[152:155], a[40:43], v[84:87]\n" \
E_"v114, v114\n" \
M_"v[84:87], a[148:151], a[36:39], v[84:87]\n" \
E_"v115, v115\n" \
M_"v[84:87], a[144:147], a[32:35], v[84:87]\n" \
B_"v53, s[8:11], s64 offen offset:1024 sc1 lds\n" \
M_"v[92:95], a[144:147], a[48:51], v[44:47]\n" \
E_"v112, v112\n" \
M_"v[92:95], a[148:151], a[52:55], v[92:95]\n" \
E_"v113, v113\n" \
M_"v[92:95], a[152:155], a[56:59], v[92:95]\n" \
C_"v117, v114, v115\n" \
C_"v116, v112, v113\n" \
M_"v[92:95], a[156:159], a[60:63], v[92:95]\n" \
B_"v53, s[8:11], s64 offen offset:2048 sc1 lds\n" \
M_"v[88:91], a[140:143], a[60:63], v[44:47]\n" \
E_"v120, v120\n" \
M_"v[88:91], a[136:139], a[56:59], v[88:91]\n" \
E_"v121, v121\n" \
M_"v[88:91], a[132:135], a[52:55], v[88:91]\n" \
E_"v122, v122\n" \
M_"v[88:91], a[128:131], a[48:51], v[88:91]\n" \
C_"v120, v120, v121\n" \
"s_waitcnt lgkmcnt(0)\n" \
M_"v[128:131], a[160:163], v[100:103], v[128:131]\n" \
B_"v53, s[8:11], s64 offen offset:3072 sc1 lds\n" \
M_"v[160:163], a[160:163], v[104:107], v[160:163]\n" \
"s_add_u32 m0, s93, 0x1000\n" \
R_"a[128:131], v48 offset:8192\n" \
M_"v[132:135], a[164:167], v[100:103], v[132:135]\n" \
E_"v123, v123\n" \
M_"v[164:167], a[164:167], v[104:107], v[164:167]\n" \
E_"v124, v124\n" \
M_"v[136:139], a[168:171], v[100:103], v[136:139]\n" \
B_"v4, s[80:83], s92 offen offset:0 lds\n" \
M_"v[168:171], a[168:171], v[104:107], v[168:171]\n" \
E_"v125, v125\n" \
M_"v[140:143], a[172:175], v[100:103], v[140:143]\n" \
E_"v126, v126\n" \
M_"v[172:175], a[172:175], v[104:107], v[172:175]\n" \
E_"v127, v127\n" \
M_"v[144:147], a[176:179], v[100:103], v[144:147]\n" \
C_"v121, v122, v123\n" \
C_"v122, v124, v125\n" \
M_"v[176:179], a[176:179], v[104:107], v[176:179]\n" \
B_"v5, s[80:83], s92 offen offset:1024 lds\n" \
M_"v[148:151], a[180:183], v[100:103], v[148:151]\n" \
C_"v123, v126, v127\n" \
R_"a[144:147], v48 offset:12288\n" \
M_"v[180:183], a[180:183], v[104:107], v[180:183]\n" \
R_"a[132:135], v48 offset:9216\n" \
R_"a[148:151], v48 offset:13312\n" \
M_"v[152:155], a[184:187], v[100:103], v[152:155]\n" \
R_"a[136:139], v48 offset:10240\n" \
R_"a[152:155], v48 offset:14336\n" \
M_"v[184:187], a[184:187], v[104:107], v[184:187]\n" \
R_"a[140:143], v48 offset:11264\n" \
R_"a[156:159], v48 offset:15360\n" \
M_"v[156:159], a[188:191], v[100:103], v[156:159]\n" \
B_"v6, s[80:83], s92 offen offset:2048 lds\n" \
M_"v[188:191], a[188:191], v[104:107], v[188:191]\n" \
E_"v70, v70\n" \
"v_smfmac_f32_16x16x64_bf16 a[96:99], a[104:107], v[100:107], v21\n" \
E_"v71, v71\n" \
M_"v[220:223], a[188:191], v[116:119], v[220:223]\n" \
E_"v72, v72\n" \
M_"v[252:255], a[188:191], v[120:123], v[252:255]\n" \
B_"v7, s[80:83], s92 offen offset:3072 lds\n" \
M_"v[216:219], a[184:187], v[116:119], v[216:219]\n" \
"s_add_u32 m0, s93, 0x2000\n" \
C_"v71, v70, v71\n" \
M_"v[248:251], a[184:187], v[120:123], v[248:251]\n" \
E_"v73, v73\n" \
M_"v[212:215], a[180:183], v[116:119], v[212:215]\n" \
E_"v68, v68\n" \
M_"v[244:247], a[180:183], v[120:123], v[244:247]\n" \
E_"v69, v69\n" \
M_"v[208:211], a[176:179], v[116:119], v[208:211]\n" \
B_"v8, s[80:83], s92 offen offset:0 lds\n" \
M_"v[240:243], a[176:179], v[120:123], v[240:243]\n" \
E_"v74, v74\n" \
M_"v[204:207], a[172:175], v[116:119], v[204:207]\n" \
E_"v75, v75\n" \
M_"v[236:239], a[172:175], v[120:123], v[236:239]\n" \
E_"v66, v66\n" \
M_"v[200:203], a[168:171], v[116:119], v[200:203]\n" \
E_"v67, v67\n" \
M_"v[232:235], a[168:171], v[120:123], v[232:235]\n" \
E_"v64, v64\n" \
M_"v[196:199], a[164:167], v[116:119], v[196:199]\n" \
E_"v65, v65\n" \
M_"v[228:231], a[164:167], v[120:123], v[228:231]\n" \
C_"v72, v72, v73\n" \
C_"v70, v68, v69\n" \
M_"v[192:195], a[160:163], v[116:119], v[192:195]\n" \
E_"v76, v76\n" \
M_"v[224:227], a[160:163], v[120:123], v[224:227]\n" \
E_"v77, v77\n" \
"v_smfmac_f32_16x16x64_bf16 a[100:103], a[104:107], v[116:123], v21\n" \
E_"v78, v78\n" \
"s_waitcnt vmcnt(13) lgkmcnt(0)\n" \
"s_barrier\n" \
E_"v79, v79\n" \
C_"v73, v74, v75\n" \
C_"v69, v66, v67\n" \
C_"v68, v64, v65\n" \
C_"v74, v76, v77\n" \
C_"v75, v78, v79\n" \
"v_add_u32_e32 v48, s16, v20\n" \
"s_xor_b32 s65, s65, 0x8000\n" \
"s_add_u32 s64, s64, 0x10000\n" \
"s_getpc_b64 s[86:87]\n" \
"L_hpt_%=:\n" \
"s_add_u32 s86, s86, L_ap8-L_hpt_%=\n" \
"s_addc_u32 s87, s87, 0\n" \
"s_add_u32 s86, s86, s15\n" \
"s_addc_u32 s87, s87, 0\n" \
"s_mov_b64 exec, 0\n" \
"s_swappc_b64 s[88:89], s[86:87]\n" \
"s_mov_b64 exec, -1\n" \
"s_sub_u32 s66, s19, 1\n" \
"s_cbranch_scc1 L_la_%=\n" \
"s_branch L_2_%=\n" \
".fill (L_ab + 6400 - .) / 4, 4, 0xBF800000\n" \
"L_tL_2_%=:\n" \
"s_waitcnt vmcnt(8) lgkmcnt(0)\n" \
"v_smfmac_f32_16x16x64_bf16 a[100:103], a[104:107], v[116:123], v21\n" \
"s_barrier\n" \
"L_2_%=:\n" \
M_"v[96:99], a[128:131], a[64:67], v[32:35]\n" \
"s_xor_b32 m0, s65, 0x12000\n" \
R_"a[160:163], v49 offset:0\n" \
M_"v[96:99], a[132:135], a[68:71], v[96:99]\n" \
B_"v50, s[8:11], s64 offen offset:0 sc1 lds\n" \
M_"v[96:99], a[136:139], a[72:75], v[96:99]\n" \
R_"a[164:167], v49 offset:1024\n" \
R_"a[168:171], v49 offset:2048\n" \
M_"v[96:99], a[140:143], a[76:79], v[96:99]\n" \
R_"a[172:175], v49 offset:3072\n" \
R_"a[176:179], v49 offset:4096\n" \
M_"v[100:103], a[156:159], a[76:79], v[32:35]\n" \
R_"a[180:183], v49 offset:5120\n" \
R_"a[184:187], v49 offset:6144\n" \
M_"v[100:103], a[152:155], a[72:75], v[100:103]\n" \
"v_exp_f32_e64 v86, v86\n" \
M_"v[100:103], a[148:151], a[68:71], v[100:103]\n" \
"v_exp_f32_e64 v87, v87\n" \
M_"v[100:103], a[144:147], a[64:67], v[100:103]\n" \
B_"v50, s[8:11], s64 offen offset:1024 sc1 lds\n" \
M_"v[108:111], a[144:147], a[80:83], v[36:39]\n" \
R_"a[188:191], v49 offset:7168\n" \
C_"v87, v86, v87\n" \
M_"v[108:111], a[148:151], a[84:87], v[108:111]\n" \
"v_exp_f32_e64 v84, v84\n" \
M_"v[108:111], a[152:155], a[88:91], v[108:111]\n" \
"v_exp_f32_e64 v85, v85\n" \
M_"v[108:111], a[156:159], a[92:95], v[108:111]\n" \
"v_exp_f32_e64 v82, v82\n" \
M_"v[104:107], a[140:143], a[92:95], v[36:39]\n" \
"v_exp_f32_e64 v83, v83\n" \
M_"v[104:107], a[136:139], a[88:91], v[104:107]\n" \
"v_exp_f32_e64 v80, v80\n" \
M_"v[104:107], a[132:135], a[84:87], v[104:107]\n" \
"v_exp_f32_e64 v81, v81\n" \
M_"v[104:107], a[128:131], a[80:83], v[104:107]\n" \
B_"v50, s[8:11], s64 offen offset:2048 sc1 lds\n" \
M_"v[112:115], a[128:131], a[192:195], v[40:43]\n" \
"v_exp_f32_e64 v88, v88\n" \
M_"v[112:115], a[132:135], a[196:199], v[112:115]\n" \
"v_exp_f32_e64 v89, v89\n" \
M_"v[112:115], a[136:139], a[200:203], v[112:115]\n" \
C_"v86, v84, v85\n" \
C_"v85, v82, v83\n" \
M_"v[112:115], a[140:143], a[204:207], v[112:115]\n" \
C_"v84, v80, v81\n" \
C_"v88, v88, v89\n" \
M_"v[116:119], a[156:159], a[204:207], v[40:43]\n" \
"v_exp_f32_e64 v90, v90\n" \
M_"v[116:119], a[152:155], a[200:203], v[116:119]\n" \
"v_exp_f32_e64 v91, v91\n" \
M_"v[116:119], a[148:151], a[196:199], v[116:119]\n" \
B_"v50, s[8:11], s64 offen offset:3072 sc1 lds\n" \
M_"v[116:119], a[144:147], a[192:195], v[116:119]\n" \
"s_xor_b32 m0, s65, 0xa000\n" \
C_"v89, v90, v91\n" \
M_"v[124:127], a[144:147], a[208:211], v[44:47]\n" \
"v_exp_f32_e64 v92, v92\n" \
M_"v[124:127], a[148:151], a[212:215], v[124:127]\n" \
"v_exp_f32_e64 v93, v93\n" \
M_"v[124:127], a[152:155], a[216:219], v[124:127]\n" \
"v_exp_f32_e64 v94, v94\n" \
M_"v[124:127], a[156:159], a[220:223], v[124:127]\n" \
"v_exp_f32_e64 v95, v95\n" \
M_"v[120:123], a[140:143], a[220:223], v[44:47]\n" \
C_"v90, v92, v93\n" \
C_"v91, v94, v95\n" \
M_"v[120:123], a[136:139], a[216:219], v[120:123]\n" \
R_"a[144:147], v48 offset:4096\n" \
R_"a[148:151], v48 offset:5120\n" \
M_"v[120:123], a[132:135], a[212:215], v[120:123]\n" \
B_"v51, s[8:11], s64 offen offset:0 sc1 lds\n" \
M_"v[120:123], a[128:131], a[208:211], v[120:123]\n" \
R_"a[128:131], v48 offset:0\n" \
"s_waitcnt lgkmcnt(3)\n" \
M_"v[128:131], a[160:163], v[68:71], v[128:131]\n" \
R_"a[132:135], v48 offset:1024\n" \
R_"a[136:139], v48 offset:2048\n" \
M_"v[160:163], a[160:163], v[72:75], v[160:163]\n" \
R_"a[152:155], v48 offset:6144\n" \
R_"a[140:143], v48 offset:3072\n" \
M_"v[132:135], a[164:167], v[68:71], v[132:135]\n" \
E_"v102, v102\n" \
M_"v[164:167], a[164:167], v[72:75], v[164:167]\n" \
"v_exp_f32_e64 v103, v103\n" \
M_"v[136:139], a[168:171], v[68:71], v[136:139]\n" \
R_"a[156:159], v48 offset:7168\n" \
C_"v103, v102, v103\n" \
M_"v[168:171], a[168:171], v[72:75], v[168:171]\n" \
B_"v51, s[8:11], s64 offen offset:1024 sc1 lds\n" \
M_"v[140:143], a[172:175], v[68:71], v[140:143]\n" \
"v_exp_f32_e64 v100, v100\n" \
M_"v[172:175], a[172:175], v[72:75], v[172:175]\n" \
"v_exp_f32_e64 v101, v101\n" \
M_"v[144:147], a[176:179], v[68:71], v[144:147]\n" \
"v_exp_f32_e64 v98, v98\n" \
M_"v[176:179], a[176:179], v[72:75], v[176:179]\n" \
"v_exp_f32_e64 v99, v99\n" \
M_"v[148:151], a[180:183], v[68:71], v[148:151]\n" \
"v_exp_f32_e64 v96, v96\n" \
M_"v[180:183], a[180:183], v[72:75], v[180:183]\n" \
"v_exp_f32_e64 v97, v97\n" \
M_"v[152:155], a[184:187], v[68:71], v[152:155]\n" \
"v_exp_f32_e64 v104, v104\n" \
M_"v[184:187], a[184:187], v[72:75], v[184:187]\n" \
B_"v51, s[8:11], s64 offen offset:2048 sc1 lds\n" \
M_"v[156:159], a[188:191], v[68:71], v[156:159]\n" \
"v_exp_f32_e64 v105, v105\n" \
M_"v[188:191], a[188:191], v[72:75], v[188:191]\n" \
C_"v102, v100, v101\n" \
C_"v101, v98, v99\n" \
"v_smfmac_f32_16x16x64_bf16 a[96:99], a[104:107], v[68:75], v21\n" \
C_"v100, v96, v97\n" \
C_"v104, v104, v105\n" \
M_"v[220:223], a[188:191], v[84:87], v[220:223]\n" \
"v_exp_f32_e64 v106, v106\n" \
M_"v[252:255], a[188:191], v[88:91], v[252:255]\n" \
"v_exp_f32_e64 v107, v107\n" \
M_"v[216:219], a[184:187], v[84:87], v[216:219]\n" \
"v_exp_f32_e64 v108, v108\n" \
M_"v[248:251], a[184:187], v[88:91], v[248:251]\n" \
B_"v51, s[8:11], s64 offen offset:3072 sc1 lds\n" \
M_"v[212:215], a[180:183], v[84:87], v[212:215]\n" \
"v_exp_f32_e64 v109, v109\n" \
M_"v[244:247], a[180:183], v[88:91], v[244:247]\n" \
"v_exp_f32_e64 v110, v110\n" \
M_"v[208:211], a[176:179], v[84:87], v[208:211]\n" \
"v_exp_f32_e64 v111, v111\n" \
M_"v[240:243], a[176:179], v[88:91], v[240:243]\n" \
"v_exp_f32_e64 v118, v118\n" \
M_"v[204:207], a[172:175], v[84:87], v[204:207]\n" \
"v_exp_f32_e64 v119, v119\n" \
M_"v[236:239], a[172:175], v[88:91], v[236:239]\n" \
C_"v105, v106, v107\n" \
C_"v106, v108, v109\n" \
M_"v[200:203], a[168:171], v[84:87], v[200:203]\n" \
C_"v107, v110, v111\n" \
C_"v119, v118, v119\n" \
M_"v[232:235], a[168:171], v[88:91], v[232:235]\n" \
"v_exp_f32_e64 v116, v116\n" \
M_"v[196:199], a[164:167], v[84:87], v[196:199]\n" \
"v_exp_f32_e64 v117, v117\n" \
M_"v[228:231], a[164:167], v[88:91], v[228:231]\n" \
"v_exp_f32_e64 v114, v114\n" \
M_"v[192:195], a[160:163], v[84:87], v[192:195]\n" \
"v_exp_f32_e64 v115, v115\n" \
M_"v[224:227], a[160:163], v[88:91], v[224:227]\n" \
C_"v118, v116, v117\n" \
"s_waitcnt vmcnt(8) lgkmcnt(0)\n" \
"v_smfmac_f32_16x16x64_bf16 a[100:103], a[104:107], v[84:91], v21\n" \
"s_barrier\n" \
M_"v[64:67], a[128:131], a[64:67], v[32:35]\n" \
"s_xor_b32 m0, s65, 0x18000\n" \
R_"a[160:163], v49 offset:8192\n" \
M_"v[64:67], a[132:135], a[68:71], v[64:67]\n" \
B_"v52, s[8:11], s64 offen offset:0 sc1 lds\n" \
M_"v[64:67], a[136:139], a[72:75], v[64:67]\n" \
R_"a[164:167], v49 offset:9216\n" \
R_"a[168:171], v49 offset:10240\n" \
M_"v[64:67], a[140:143], a[76:79], v[64:67]\n" \
R_"a[172:175], v49 offset:11264\n" \
R_"a[176:179], v49 offset:12288\n" \
M_"v[68:71], a[156:159], a[76:79], v[32:35]\n" \
R_"a[180:183], v49 offset:13312\n" \
R_"a[184:187], v49 offset:14336\n" \
M_"v[68:71], a[152:155], a[72:75], v[68:71]\n" \
R_"a[188:191], v49 offset:15360\n" \
C_"v117, v114, v115\n" \
M_"v[68:71], a[148:151], a[68:71], v[68:71]\n" \
"v_exp_f32_e64 v112, v112\n" \
M_"v[68:71], a[144:147], a[64:67], v[68:71]\n" \
B_"v52, s[8:11], s64 offen offset:1024 sc1 lds\n" \
M_"v[76:79], a[144:147], a[80:83], v[36:39]\n" \
"v_exp_f32_e64 v113, v113\n" \
M_"v[76:79], a[148:151], a[84:87], v[76:79]\n" \
C_"v116, v112, v113\n" \
"v_add_u32_e32 v49, 0x10000, v48\n" \
M_"v[76:79], a[152:155], a[88:91], v[76:79]\n" \
"v_exp_f32_e64 v120, v120\n" \
M_"v[76:79], a[156:159], a[92:95], v[76:79]\n" \
"v_exp_f32_e64 v121, v121\n" \
M_"v[72:75], a[140:143], a[92:95], v[36:39]\n" \
"v_exp_f32_e64 v122, v122\n" \
M_"v[72:75], a[136:139], a[88:91], v[72:75]\n" \
"v_exp_f32_e64 v123, v123\n" \
M_"v[72:75], a[132:135], a[84:87], v[72:75]\n" \
"v_exp_f32_e64 v124, v124\n" \
M_"v[72:75], a[128:131], a[80:83], v[72:75]\n" \
B_"v52, s[8:11], s64 offen offset:2048 sc1 lds\n" \
M_"v[80:83], a[128:131], a[192:195], v[40:43]\n" \
"v_exp_f32_e64 v125, v125\n" \
M_"v[80:83], a[132:135], a[196:199], v[80:83]\n" \
"v_exp_f32_e64 v126, v126\n" \
M_"v[80:83], a[136:139], a[200:203], v[80:83]\n" \
"v_exp_f32_e64 v127, v127\n" \
M_"v[80:83], a[140:143], a[204:207], v[80:83]\n" \
"v_exp_f32_e64 v70, v70\n" \
M_"v[84:87], a[156:159], a[204:207], v[40:43]\n" \
"v_exp_f32_e64 v71, v71\n" \
M_"v[84:87], a[152:155], a[200:203], v[84:87]\n" \
C_"v120, v120, v121\n" \
C_"v121, v122, v123\n" \
M_"v[84:87], a[148:151], a[196:199], v[84:87]\n" \
B_"v52, s[8:11], s64 offen offset:3072 sc1 lds\n" \
M_"v[84:87], a[144:147], a[192:195], v[84:87]\n" \
"s_mov_b32 m0, s65\n" \
"s_nop 0\n" \
C_"v122, v124, v125\n" \
M_"v[92:95], a[144:147], a[208:211], v[44:47]\n" \
C_"v123, v126, v127\n" \
R_"a[144:147], v48 offset:12288\n" \
M_"v[92:95], a[148:151], a[212:215], v[92:95]\n" \
R_"a[148:151], v48 offset:13312\n" \
C_"v71, v70, v71\n" \
M_"v[92:95], a[152:155], a[216:219], v[92:95]\n" \
R_"a[152:155], v48 offset:14336\n" \
"s_xor_b32 s65, s65, 0x8000\n" \
M_"v[92:95], a[156:159], a[220:223], v[92:95]\n" \
"v_exp_f32_e64 v72, v72\n" \
M_"v[88:91], a[140:143], a[220:223], v[44:47]\n" \
R_"a[140:143], v48 offset:11264\n" \
R_"a[156:159], v48 offset:15360\n" \
M_"v[88:91], a[136:139], a[216:219], v[88:91]\n" \
"v_exp_f32_e64 v73, v73\n" \
M_"v[88:91], a[132:135], a[212:215], v[88:91]\n" \
".fill (L_ab + 8192 - .) / 4, 4, 0xBF800000\n" \
"L_ap2:\n" \
"s_cbranch_execz L_ap0\n" \
B_"v53, s[8:11], s64 offen offset:0 sc1 lds\n" \
M_"v[88:91], a[128:131], a[208:211], v[88:91]\n" \
R_"a[128:131], v48 offset:8192\n" \
"s_waitcnt lgkmcnt(6)\n" \
M_"v[128:131], a[160:163], v[100:103], v[128:131]\n" \
R_"a[132:135], v48 offset:9216\n" \
R_"a[136:139], v48 offset:10240\n" \
M_"v[160:163], a[160:163], v[104:107], v[160:163]\n" \
"v_exp_f32_e64 v68, v68\n" \
M_"v[132:135], a[164:167], v[100:103], v[132:135]\n" \
"v_exp_f32_e64 v69, v69\n" \
M_"v[164:167], a[164:167], v[104:107], v[164:167]\n" \
"v_exp_f32_e64 v74, v74\n" \
M_"v[136:139], a[168:171], v[100:103], v[136:139]\n" \
"v_exp_f32_e64 v75, v75\n" \
M_"v[168:171], a[168:171], v[104:107], v[168:171]\n" \
B_"v53, s[8:11], s64 offen offset:1024 sc1 lds\n" \
M_"v[140:143], a[172:175], v[100:103], v[140:143]\n" \
"v_exp_f32_e64 v66, v66\n" \
M_"v[172:175], a[172:175], v[104:107], v[172:175]\n" \
"v_exp_f32_e64 v67, v67\n" \
M_"v[144:147], a[176:179], v[100:103], v[144:147]\n" \
"v_exp_f32_e64 v64, v64\n" \
M_"v[176:179], a[176:179], v[104:107], v[176:179]\n" \
"v_exp_f32_e64 v65, v65\n" \
M_"v[148:151], a[180:183], v[100:103], v[148:151]\n" \
C_"v72, v72, v73\n" \
C_"v70, v68, v69\n" \
M_"v[180:183], a[180:183], v[104:107], v[180:183]\n" \
"v_exp_f32_e64 v76, v76\n" \
M_"v[152:155], a[184:187], v[100:103], v[152:155]\n" \
"v_exp_f32_e64 v77, v77\n" \
M_"v[184:187], a[184:187], v[104:107], v[184:187]\n" \
B_"v53, s[8:11], s64 offen offset:2048 sc1 lds\n" \
M_"v[156:159], a[188:191], v[100:103], v[156:159]\n" \
"v_exp_f32_e64 v78, v78\n" \
M_"v[188:191], a[188:191], v[104:107], v[188:191]\n" \
"v_exp_f32_e64 v79, v79\n" \
"v_smfmac_f32_16x16x64_bf16 a[96:99], a[104:107], v[100:107], v21\n" \
C_"v73, v74, v75\n" \
C_"v69, v66, v67\n" \
M_"v[220:223], a[188:191], v[116:119], v[220:223]\n" \
C_"v68, v64, v65\n" \
C_"v74, v76, v77\n" \
M_"v[252:255], a[188:191], v[120:123], v[252:255]\n" \
C_"v75, v78, v79\n" \
"v_xor_b32_e32 v48, 0x8000, v48\n" \
M_"v[216:219], a[184:187], v[116:119], v[216:219]\n" \
M_"v[248:251], a[184:187], v[120:123], v[248:251]\n" \
B_"v53, s[8:11], s64 offen offset:3072 sc1 lds\n" \
M_"v[212:215], a[180:183], v[116:119], v[212:215]\n" \
"s_add_u32 s64, s64, 0x10000\n" \
"s_sub_u32 s66, s66, 1\n" \
"s_nop 0\n" \
M_"v[244:247], a[180:183], v[120:123], v[244:247]\n" \
M_"v[208:211], a[176:179], v[116:119], v[208:211]\n" \
M_"v[240:243], a[176:179], v[120:123], v[240:243]\n" \
M_"v[204:207], a[172:175], v[116:119], v[204:207]\n" \
M_"v[236:239], a[172:175], v[120:123], v[236:239]\n" \
M_"v[200:203], a[168:171], v[116:119], v[200:203]\n" \
M_"v[232:235], a[168:171], v[120:123], v[232:235]\n" \
M_"v[196:199], a[164:167], v[116:119], v[196:199]\n" \
M_"v[228:231], a[164:167], v[120:123], v[228:231]\n" \
M_"v[192:195], a[160:163], v[116:119], v[192:195]\n" \
M_"v[224:227], a[160:163], v[120:123], v[224:227]\n" \
"s_cbranch_scc0 L_tL_2_%=\n" \
"s_waitcnt vmcnt(8) lgkmcnt(0)\n" \
"v_smfmac_f32_16x16x64_bf16 a[100:103], a[104:107], v[116:123], v21\n" \
"s_barrier\n" \
"L_la_%=:\n" \
M_"v[96:99], a[128:131], a[64:67], v[32:35]\n" \
"v_readlane_b32 s96, v11, 1\n" \
"v_lshrrev_b32_e32 v16, 1, v20\n" \
M_"v[96:99], a[132:135], a[68:71], v[96:99]\n" \
"s_xor_b32 m0, s65, 0x12000\n" \
R_"a[160:163], v49 offset:0\n" \
M_"v[96:99], a[136:139], a[72:75], v[96:99]\n" \
"buffer_load_dwordx2 v[26:27], v16, s[56:59], s96 offen sc1\n" \
M_"v[96:99], a[140:143], a[76:79], v[96:99]\n" \
B_"v50, s[8:11], s64 offen offset:0 sc1 lds\n" \
M_"v[100:103], a[156:159], a[76:79], v[32:35]\n" \
R_"a[164:167], v49 offset:1024\n" \
R_"a[168:171], v49 offset:2048\n" \
M_"v[100:103], a[152:155], a[72:75], v[100:103]\n" \
R_"a[172:175], v49 offset:3072\n" \
R_"a[176:179], v49 offset:4096\n" \
M_"v[100:103], a[148:151], a[68:71], v[100:103]\n" \
R_"a[180:183], v49 offset:5120\n" \
R_"a[184:187], v49 offset:6144\n" \
M_"v[100:103], a[144:147], a[64:67], v[100:103]\n" \
R_"a[188:191], v49 offset:7168\n" \
"s_mov_b32 s63, s62\n" \
M_"v[108:111], a[144:147], a[80:83], v[36:39]\n" \
E_"v86, v86\n" \
M_"v[108:111], a[148:151], a[84:87], v[108:111]\n" \
"v_exp_f32_e64 v87, v87\n" \
M_"v[108:111], a[152:155], a[88:91], v[108:111]\n" \
B_"v50, s[8:11], s64 offen offset:1024 sc1 lds\n" \
M_"v[108:111], a[156:159], a[92:95], v[108:111]\n" \
"v_exp_f32_e64 v84, v84\n" \
M_"v[104:107], a[140:143], a[92:95], v[36:39]\n" \
"v_exp_f32_e64 v85, v85\n" \
M_"v[104:107], a[136:139], a[88:91], v[104:107]\n" \
C_"v87, v86, v87\n" \
C_"v86, v84, v85\n" \
M_"v[104:107], a[132:135], a[84:87], v[104:107]\n" \
"v_exp_f32_e64 v82, v82\n" \
M_"v[104:107], a[128:131], a[80:83], v[104:107]\n" \
"v_exp_f32_e64 v83, v83\n" \
M_"v[112:115], a[128:131], a[192:195], v[40:43]\n" \
"v_exp_f32_e64 v80, v80\n" \
M_"v[112:115], a[132:135], a[196:199], v[112:115]\n" \
"v_exp_f32_e64 v81, v81\n" \
M_"v[112:115], a[136:139], a[200:203], v[112:115]\n" \
B_"v50, s[8:11], s64 offen offset:2048 sc1 lds\n" \
M_"v[112:115], a[140:143], a[204:207], v[112:115]\n" \
C_"v85, v82, v83\n" \
C_"v84, v80, v81\n" \
M_"v[116:119], a[156:159], a[204:207], v[40:43]\n" \
"v_exp_f32_e64 v88, v88\n" \
M_"v[116:119], a[152:155], a[200:203], v[116:119]\n" \
"v_exp_f32_e64 v89, v89\n" \
M_"v[116:119], a[148:151], a[196:199], v[116:119]\n" \
"v_exp_f32_e64 v90, v90\n" \
M_"v[116:119], a[144:147], a[192:195], v[116:119]\n" \
"v_exp_f32_e64 v91, v91\n" \
M_"v[124:127], a[144:147], a[208:211], v[44:47]\n" \
C_"v88, v88, v89\n" \
C_"v89, v90, v91\n" \
M_"v[124:127], a[148:151], a[212:215], v[124:127]\n" \
B_"v50, s[8:11], s64 offen offset:3072 sc1 lds\n" \
M_"v[124:127], a[152:155], a[216:219], v[124:127]\n" \
"v_exp_f32_e64 v92, v92\n" \
M_"v[124:127], a[156:159], a[220:223], v[124:127]\n" \
"v_exp_f32_e64 v93, v93\n" \
M_"v[120:123], a[140:143], a[220:223], v[44:47]\n" \
"v_exp_f32_e64 v94, v94\n" \
M_"v[120:123], a[136:139], a[216:219], v[120:123]\n" \
"v_exp_f32_e64 v95, v95\n" \
M_"v[120:123], a[132:135], a[212:215], v[120:123]\n" \
C_"v90, v92, v93\n" \
C_"v91, v94, v95\n" \
M_"v[120:123], a[128:131], a[208:211], v[120:123]\n" \
"v_readlane_b32 s97, v11, 39\n" \
"s_waitcnt lgkmcnt(0)\n" \
"s_nop 0\n" \
M_"v[128:131], a[160:163], v[68:71], v[128:131]\n" \
R_"a[128:131], v48 offset:0\n" \
R_"a[144:147], v48 offset:4096\n" \
M_"v[160:163], a[160:163], v[72:75], v[160:163]\n" \
R_"a[132:135], v48 offset:1024\n" \
R_"a[148:151], v48 offset:5120\n" \
M_"v[132:135], a[164:167], v[68:71], v[132:135]\n" \
R_"a[136:139], v48 offset:2048\n" \
R_"a[152:155], v48 offset:6144\n" \
M_"v[164:167], a[164:167], v[72:75], v[164:167]\n" \
R_"a[140:143], v48 offset:3072\n" \
R_"a[156:159], v48 offset:7168\n" \
M_"v[136:139], a[168:171], v[68:71], v[136:139]\n" \
"v_add_u32_e32 v17, 0x49000, v20\n" \
"v_readlane_b32 s98, v11, 40\n" \
M_"v[168:171], a[168:171], v[72:75], v[168:171]\n" \
"v_readlane_b32 s93, v11, 23\n" \
"v_readlane_b32 s84, v11, 15\n" \
M_"v[140:143], a[172:175], v[68:71], v[140:143]\n" \
"v_readlane_b32 s85, v11, 16\n" \
"v_readlane_b32 s86, v11, 17\n" \
M_"v[172:175], a[172:175], v[72:75], v[172:175]\n" \
"s_mov_b32 s87, 0x20000\n" \
"v_readlane_b32 s92, v11, 18\n" \
M_"v[144:147], a[176:179], v[68:71], v[144:147]\n" \
"v_exp_f32_e64 v102, v102\n" \
M_"v[176:179], a[176:179], v[72:75], v[176:179]\n" \
"v_exp_f32_e64 v103, v103\n" \
M_"v[148:151], a[180:183], v[68:71], v[148:151]\n" \
"v_exp_f32_e64 v100, v100\n" \
M_"v[180:183], a[180:183], v[72:75], v[180:183]\n" \
"v_exp_f32_e64 v101, v101\n" \
M_"v[152:155], a[184:187], v[68:71], v[152:155]\n" \
C_"v103, v102, v103\n" \
C_"v102, v100, v101\n" \
M_"v[184:187], a[184:187], v[72:75], v[184:187]\n" \
"v_exp_f32_e64 v98, v98\n" \
M_"v[156:159], a[188:191], v[68:71], v[156:159]\n" \
"v_exp_f32_e64 v99, v99\n" \
M_"v[188:191], a[188:191], v[72:75], v[188:191]\n" \
"s_waitcnt vmcnt(4)\n" \
"v_cmp_ne_u64_e32 vcc, s[62:63], v[26:27]\n" \
"v_smfmac_f32_16x16x64_bf16 a[96:99], a[104:107], v[68:75], v21\n" \
"v_exp_f32_e64 v96, v96\n" \
M_"v[220:223], a[188:191], v[84:87], v[220:223]\n" \
"v_exp_f32_e64 v97, v97\n" \
M_"v[252:255], a[188:191], v[88:91], v[252:255]\n" \
"s_cbranch_vccnz L_sol_%=\n" \
"L_rol_%=:\n" \
C_"v101, v98, v99\n" \
B_"a[124:127], v20, s[56:59], s97 offen offset:0 sc1\n" \
M_"v[216:219], a[184:187], v[84:87], v[216:219]\n" \
C_"v100, v96, v97\n" \
"v_readlane_b32 s91, v11, 24\n" \
M_"v[248:251], a[184:187], v[88:91], v[248:251]\n" \
E_"v104, v104\n" \
M_"v[212:215], a[180:183], v[84:87], v[212:215]\n" \
"v_exp_f32_e64 v105, v105\n" \
M_"v[244:247], a[180:183], v[88:91], v[244:247]\n" \
"v_exp_f32_e64 v106, v106\n" \
M_"v[208:211], a[176:179], v[84:87], v[208:211]\n" \
"v_exp_f32_e64 v107, v107\n" \
M_"v[240:243], a[176:179], v[88:91], v[240:243]\n" \
B_"a[228:231], v17, s[56:59], s97 offen offset:0 sc1\n" \
M_"v[204:207], a[172:175], v[84:87], v[204:207]\n" \
C_"v104, v104, v105\n" \
C_"v105, v106, v107\n" \
M_"v[236:239], a[172:175], v[88:91], v[236:239]\n" \
"v_exp_f32_e64 v108, v108\n" \
M_"v[200:203], a[168:171], v[84:87], v[200:203]\n" \
"v_exp_f32_e64 v109, v109\n" \
M_"v[232:235], a[168:171], v[88:91], v[232:235]\n" \
"v_exp_f32_e64 v110, v110\n" \
M_"v[196:199], a[164:167], v[84:87], v[196:199]\n" \
"v_exp_f32_e64 v111, v111\n" \
M_"v[228:231], a[164:167], v[88:91], v[228:231]\n" \
C_"v106, v108, v109\n" \
C_"v107, v110, v111\n" \
M_"v[192:195], a[160:163], v[84:87], v[192:195]\n" \
"v_exp_f32_e64 v118, v118\n" \
M_"v[224:227], a[160:163], v[88:91], v[224:227]\n" \
"s_xor_b32 s65, s65, 0x8000\n" \
"s_waitcnt vmcnt(7) lgkmcnt(0)\n" \
"v_smfmac_f32_16x16x64_bf16 a[100:103], a[104:107], v[84:91], v21\n" \
"s_barrier\n" \
M_"v[64:67], a[128:131], a[64:67], v[32:35]\n" \
B_"a[236:239], v20, s[56:59], s98 offen offset:0 sc1\n" \
M_"v[64:67], a[132:135], a[68:71], v[64:67]\n" \
"s_add_u32 m0, s93, 0x0\n" \
"s_nop 0\n" \
R_"a[160:163], v49 offset:8192\n" \
M_"v[64:67], a[136:139], a[72:75], v[64:67]\n" \
B_"v0, s[84:87], s92 offen offset:0 lds\n" \
M_"v[64:67], a[140:143], a[76:79], v[64:67]\n" \
R_"a[164:167], v49 offset:9216\n" \
R_"a[168:171], v49 offset:10240\n" \
M_"v[68:71], a[156:159], a[76:79], v[32:35]\n" \
R_"a[172:175], v49 offset:11264\n" \
R_"a[176:179], v49 offset:12288\n" \
M_"v[68:71], a[152:155], a[72:75], v[68:71]\n" \
R_"a[180:183], v49 offset:13312\n" \
R_"a[184:187], v49 offset:14336\n" \
M_"v[68:71], a[148:151], a[68:71], v[68:71]\n" \
B_"v1, s[84:87], s92 offen offset:1024 lds\n" \
M_"v[68:71], a[144:147], a[64:67], v[68:71]\n" \
B_"a[244:247], v17, s[56:59], s98 offen offset:0 sc1\n" \
M_"v[76:79], a[144:147], a[80:83], v[36:39]\n" \
R_"a[188:191], v49 offset:15360\n" \
"v_add_u32_e32 v49, 0x10000, v48\n" \
M_"v[76:79], a[148:151], a[84:87], v[76:79]\n" \
"v_exp_f32_e64 v119, v119\n" \
M_"v[76:79], a[152:155], a[88:91], v[76:79]\n" \
"v_exp_f32_e64 v116, v116\n" \
M_"v[76:79], a[156:159], a[92:95], v[76:79]\n" \
"v_exp_f32_e64 v117, v117\n" \
M_"v[72:75], a[140:143], a[92:95], v[36:39]\n" \
C_"v119, v118, v119\n" \
C_"v118, v116, v117\n" \
M_"v[72:75], a[136:139], a[88:91], v[72:75]\n" \
B_"v2, s[84:87], s92 offen offset:2048 lds\n" \
M_"v[72:75], a[132:135], a[84:87], v[72:75]\n" \
"v_exp_f32_e64 v114, v114\n" \
M_"v[72:75], a[128:131], a[80:83], v[72:75]\n" \
"v_exp_f32_e64 v115, v115\n" \
M_"v[80:83], a[128:131], a[192:195], v[40:43]\n" \
B_"a[224:227], v20, s[56:59], s97 offen offset:1024 sc1\n" \
M_"v[80:83], a[132:135], a[196:199], v[80:83]\n" \
"v_exp_f32_e64 v112, v112\n" \
M_"v[80:83], a[136:139], a[200:203], v[80:83]\n" \
"v_exp_f32_e64 v113, v113\n" \
M_"v[80:83], a[140:143], a[204:207], v[80:83]\n" \
C_"v117, v114, v115\n" \
C_"v116, v112, v113\n" \
M_"v[84:87], a[156:159], a[204:207], v[40:43]\n" \
B_"v3, s[84:87], s92 offen offset:3072 lds\n" \
M_"v[84:87], a[152:155], a[200:203], v[84:87]\n" \
"s_add_u32 m0, s91, 0x1000\n" \
"s_add_u32 s64, s64, 0x10000\n" \
M_"v[84:87], a[148:151], a[196:199], v[84:87]\n" \
"v_exp_f32_e64 v120, v120\n" \
M_"v[84:87], a[144:147], a[192:195], v[84:87]\n" \
"v_exp_f32_e64 v121, v121\n" \
M_"v[92:95], a[144:147], a[208:211], v[44:47]\n" \
"v_exp_f32_e64 v122, v122\n" \
M_"v[92:95], a[148:151], a[212:215], v[92:95]\n" \
B_"a[232:235], v17, s[56:59], s97 offen offset:1024 sc1\n" \
M_"v[92:95], a[152:155], a[216:219], v[92:95]\n" \
B_"v4, s[84:87], s92 offen offset:0 lds\n" \
M_"v[92:95], a[156:159], a[220:223], v[92:95]\n" \
"v_exp_f32_e64 v123, v123\n" \
M_"v[88:91], a[140:143], a[220:223], v[44:47]\n" \
C_"v120, v120, v121\n" \
C_"v121, v122, v123\n" \
M_"v[88:91], a[136:139], a[216:219], v[88:91]\n" \
"v_exp_f32_e64 v124, v124\n" \
M_"v[88:91], a[132:135], a[212:215], v[88:91]\n" \
"v_exp_f32_e64 v125, v125\n" \
M_"v[88:91], a[128:131], a[208:211], v[88:91]\n" \
C_"v122, v124, v125\n" \
"s_waitcnt lgkmcnt(0)\n" \
M_"v[128:131], a[160:163], v[100:103], v[128:131]\n" \
E_"v126, v126\n" \
M_"v[160:163], a[160:163], v[104:107], v[160:163]\n" \
B_"v5, s[84:87], s92 offen offset:1024 lds\n" \
M_"v[132:135], a[164:167], v[100:103], v[132:135]\n" \
B_"a[240:243], v20, s[56:59], s98 offen offset:1024 sc1\n" \
M_"v[164:167], a[164:167], v[104:107], v[164:167]\n" \
"v_exp_f32_e64 v127, v127\n" \
M_"v[136:139], a[168:171], v[100:103], v[136:139]\n" \
C_"v123, v126, v127\n" \
R_"a[128:131], v48 offset:8192\n" \
M_"v[168:171], a[168:171], v[104:107], v[168:171]\n" \
R_"a[144:147], v48 offset:12288\n" \
R_"a[132:135], v48 offset:9216\n" \
M_"v[140:143], a[172:175], v[100:103], v[140:143]\n" \
R_"a[148:151], v48 offset:13312\n" \
R_"a[136:139], v48 offset:10240\n" \
M_"v[172:175], a[172:175], v[104:107], v[172:175]\n" \
R_"a[152:155], v48 offset:14336\n" \
R_"a[140:143], v48 offset:11264\n" \
M_"v[144:147], a[176:179], v[100:103], v[144:147]\n" \
B_"v6, s[84:87], s92 offen offset:2048 lds\n" \
M_"v[176:179], a[176:179], v[104:107], v[176:179]\n" \
"v_exp_f32_e64 v70, v70\n" \
M_"v[148:151], a[180:183], v[100:103], v[148:151]\n" \
"v_exp_f32_e64 v71, v71\n" \
M_"v[180:183], a[180:183], v[104:107], v[180:183]\n" \
B_"a[248:251], v17, s[56:59], s98 offen offset:1024 sc1\n" \
M_"v[152:155], a[184:187], v[100:103], v[152:155]\n" \
R_"a[156:159], v48 offset:15360\n" \
C_"v71, v70, v71\n" \
M_"v[184:187], a[184:187], v[104:107], v[184:187]\n" \
"v_exp_f32_e64 v72, v72\n" \
M_"v[156:159], a[188:191], v[100:103], v[156:159]\n" \
B_"v7, s[84:87], s92 offen offset:3072 lds\n" \
M_"v[188:191], a[188:191], v[104:107], v[188:191]\n" \
"s_add_u32 m0, s91, 0x2000\n" \
"v_xor_b32_e32 v48, 0x8000, v48\n" \
"v_smfmac_f32_16x16x64_bf16 a[96:99], a[104:107], v[100:107], v21\n" \
"v_exp_f32_e64 v73, v73\n" \
M_"v[220:223], a[188:191], v[116:119], v[220:223]\n" \
"v_exp_f32_e64 v68, v68\n" \
M_"v[252:255], a[188:191], v[120:123], v[252:255]\n" \
"v_exp_f32_e64 v69, v69\n" \
M_"v[216:219], a[184:187], v[116:119], v[216:219]\n" \
"v_exp_f32_e64 v74, v74\n" \
M_"v[248:251], a[184:187], v[120:123], v[248:251]\n" \
"v_exp_f32_e64 v75, v75\n" \
M_"v[212:215], a[180:183], v[116:119], v[212:215]\n" \
B_"v8, s[84:87], s92 offen offset:0 lds\n" \
M_"v[244:247], a[180:183], v[120:123], v[244:247]\n" \
"v_exp_f32_e64 v66, v66\n" \
M_"v[208:211], a[176:179], v[116:119], v[208:211]\n" \
"v_exp_f32_e64 v67, v67\n" \
M_"v[240:243], a[176:179], v[120:123], v[240:243]\n" \
"v_exp_f32_e64 v64, v64\n" \
M_"v[204:207], a[172:175], v[116:119], v[204:207]\n" \
"v_exp_f32_e64 v65, v65\n" \
M_"v[236:239], a[172:175], v[120:123], v[236:239]\n" \
C_"v72, v72, v73\n" \
C_"v70, v68, v69\n" \
M_"v[200:203], a[168:171], v[116:119], v[200:203]\n" \
"v_exp_f32_e64 v76, v76\n" \
M_"v[232:235], a[168:171], v[120:123], v[232:235]\n" \
"v_exp_f32_e64 v77, v77\n" \
M_"v[196:199], a[164:167], v[116:119], v[196:199]\n" \
"v_exp_f32_e64 v78, v78\n" \
M_"v[228:231], a[164:167], v[120:123], v[228:231]\n" \
"v_exp_f32_e64 v79, v79\n" \
M_"v[192:195], a[160:163], v[116:119], v[192:195]\n" \
C_"v73, v74, v75\n" \
C_"v69, v66, v67\n" \
M_"v[224:227], a[160:163], v[120:123], v[224:227]\n" \
C_"v68, v64, v65\n" \
"s_waitcnt vmcnt(15) lgkmcnt(0)\n" \
"v_smfmac_f32_16x16x64_bf16 a[100:103], a[104:107], v[116:123], v21\n" \
"s_barrier\n" \
C_"v74, v76, v77\n" \
C_"v75, v78, v79\n" \
"s_cmp_lg_u32 s16, 0\n" \
"s_cbranch_scc1 L_f1_%=\n" \
R_"a[160:163], v49 offset:0\n" \
R_"a[164:167], v49 offset:1024\n" \
R_"a[168:171], v49 offset:2048\n" \
R_"a[172:175], v49 offset:3072\n" \
R_"a[176:179], v49 offset:4096\n" \
R_"a[180:183], v49 offset:5120\n" \
R_"a[184:187], v49 offset:6144\n" \
R_"a[188:191], v49 offset:7168\n" \
"v_add_u32_e32 v54, 0x49000, v20\n" \
B_"a[192:195], v20, s[56:59], s60 offen offset:0 sc1\n" \
"v_add_u32_e32 v50, s15, v20\n" \
"v_add_u32_e32 v51, 0x10000, v50\n" \
"ds_read_b32 v60, v18 offset:0\n" \
"ds_read_b32 v61, v18 offset:128\n" \
R_"a[0:3], v49 offset:8192\n" \
R_"a[4:7], v49 offset:9216\n" \
R_"a[8:11], v49 offset:10240\n" \
R_"a[12:15], v49 offset:11264\n" \
R_"a[16:19], v49 offset:12288\n" \
R_"a[20:23], v49 offset:13312\n" \
R_"a[24:27], v49 offset:14336\n" \
R_"a[28:31], v49 offset:15360\n" \
E_"v86, v86\n" \
E_"v87, v87\n" \
E_"v84, v84\n" \
E_"v85, v85\n" \
C_"v87, v86, v87\n" \
B_"a[196:199], v20, s[56:59], s60 offen offset:1024 sc1\n" \
E_"v82, v82\n" \
E_"v83, v83\n" \
E_"v80, v80\n" \
E_"v81, v81\n" \
"s_waitcnt lgkmcnt(15)\n" \
M_"v[128:131], a[160:163], v[68:71], v[128:131]\n" \
C_"v86, v84, v85\n" \
C_"v85, v82, v83\n" \
M_"v[160:163], a[160:163], v[72:75], v[160:163]\n" \
C_"v84, v80, v81\n" \
"v_and_b32_e32 v84, v12, v84\n" \
M_"v[132:135], a[164:167], v[68:71], v[132:135]\n" \
"v_and_b32_e32 v85, v13, v85\n" \
"v_and_b32_e32 v86, v14, v86\n" \
M_"v[164:167], a[164:167], v[72:75], v[164:167]\n" \
B_"a[200:203], v20, s[56:59], s60 offen offset:2048 sc1\n" \
M_"v[136:139], a[168:171], v[68:71], v[136:139]\n" \
C_"v128, v128, v129\n" \
C_"v160, v160, v161\n" \
M_"v[168:171], a[168:171], v[72:75], v[168:171]\n" \
C_"v129, v130, v131\n" \
"s_waitcnt lgkmcnt(10)\n" \
M_"v[140:143], a[172:175], v[68:71], v[140:143]\n" \
C_"v161, v162, v163\n" \
C_"v130, v132, v133\n" \
M_"v[172:175], a[172:175], v[72:75], v[172:175]\n" \
C_"v131, v134, v135\n" \
C_"v162, v164, v165\n" \
M_"v[144:147], a[176:179], v[68:71], v[144:147]\n" \
C_"v163, v166, v167\n" \
C_"v132, v136, v137\n" \
M_"v[176:179], a[176:179], v[72:75], v[176:179]\n" \
B_"a[204:207], v20, s[56:59], s60 offen offset:3072 sc1\n" \
M_"v[148:151], a[180:183], v[68:71], v[148:151]\n" \
C_"v133, v138, v139\n" \
C_"v134, v140, v141\n" \
M_"v[180:183], a[180:183], v[72:75], v[180:183]\n" \
C_"v135, v142, v143\n" \
C_"v164, v168, v169\n" \
M_"v[152:155], a[184:187], v[68:71], v[152:155]\n" \
C_"v165, v170, v171\n" \
C_"v166, v172, v173\n" \
M_"v[184:187], a[184:187], v[72:75], v[184:187]\n" \
C_"v167, v174, v175\n" \
C_"v136, v144, v145\n" \
M_"v[156:159], a[188:191], v[68:71], v[156:159]\n" \
C_"v137, v146, v147\n" \
C_"v138, v148, v149\n" \
M_"v[188:191], a[188:191], v[72:75], v[188:191]\n" \
C_"v139, v150, v151\n" \
C_"v140, v152, v153\n" \
"v_smfmac_f32_16x16x64_bf16 a[96:99], a[104:107], v[68:75], v21\n" \
C_"v141, v154, v155\n" \
C_"v168, v176, v177\n" \
M_"v[120:123], a[128:131], a[208:211], v[44:47]\n" \
C_"v142, v156, v157\n" \
C_"v143, v158, v159\n" \
M_"v[120:123], a[132:135], a[212:215], v[120:123]\n" \
C_"v169, v178, v179\n" \
C_"v170, v180, v181\n" \
C_"v171, v182, v183\n" \
C_"v172, v184, v185\n" \
C_"v173, v186, v187\n" \
C_"v174, v188, v189\n" \
C_"v175, v190, v191\n" \
"ds_write_b128 v50, v[128:131] offset:0\n" \
M_"v[120:123], a[136:139], a[216:219], v[120:123]\n" \
"v_and_b32_e32 v87, v15, v87\n" \
"v_accvgpr_read_b32 v40, a96\n" \
M_"v[120:123], a[140:143], a[220:223], v[120:123]\n" \
"ds_write_b128 v50, v[132:135] offset:1024\n" \
M_"v[124:127], a[156:159], a[220:223], v[44:47]\n" \
B_"a[220:223], v54, s[56:59], s60 offen offset:3072 sc1\n" \
M_"v[124:127], a[152:155], a[216:219], v[124:127]\n" \
"ds_write_b128 v50, v[136:139] offset:2048\n" \
M_"v[124:127], a[148:151], a[212:215], v[124:127]\n" \
"v_accvgpr_read_b32 v22, a97\n" \
"v_accvgpr_read_b32 v41, a98\n" \
M_"v[124:127], a[144:147], a[208:211], v[124:127]\n" \
"ds_write_b128 v50, v[140:143] offset:3072\n" \
M_"a[100:103], a[108:111], v[84:87], a[100:103]\n" \
"v_accvgpr_read_b32 v23, a99\n" \
"v_add_f32_e32 v40, v40, v22\n" \
M_"v[192:195], a[160:163], v[84:87], v[192:195]\n" \
"ds_write_b128 v51, v[160:163] offset:32768\n" \
M_"v[196:199], a[164:167], v[84:87], v[196:199]\n" \
B_"a[208:211], v54, s[56:59], s60 offen offset:0 sc1\n" \
M_"v[200:203], a[168:171], v[84:87], v[200:203]\n" \
"ds_write_b128 v51, v[164:167] offset:33792\n" \
"v_add_f32_e32 v41, v41, v23\n" \
"ds_write_b64 v19, v[40:41]\n" \
M_"v[204:207], a[172:175], v[84:87], v[204:207]\n" \
"v_lshl_add_u32 v52, s15, 2, v20\n" \
"v_add_u32_e32 v52, 0x1a000, v52\n" \
"ds_write_b128 v51, v[168:171] offset:34816\n" \
M_"v[208:211], a[176:179], v[84:87], v[208:211]\n" \
E_"v88, v88\n" \
M_"v[212:215], a[180:183], v[84:87], v[212:215]\n" \
"ds_write_b128 v51, v[172:175] offset:35840\n" \
M_"v[216:219], a[184:187], v[84:87], v[216:219]\n" \
B_"a[212:215], v54, s[56:59], s60 offen offset:1024 sc1\n" \
M_"v[220:223], a[188:191], v[84:87], v[220:223]\n" \
E_"v89, v89\n" \
E_"v90, v90\n" \
E_"v91, v91\n" \
C_"v88, v88, v89\n" \
E_"v92, v92\n" \
E_"v93, v93\n" \
E_"v94, v94\n" \
E_"v95, v95\n" \
C_"v89, v90, v91\n" \
C_"v90, v92, v93\n" \
C_"v91, v94, v95\n" \
"v_xor_b32_e32 v63, 0x400, v19\n" \
B_"a[216:219], v54, s[56:59], s60 offen offset:2048 sc1\n" \
E_"v120, v120\n" \
E_"v121, v121\n" \
E_"v122, v122\n" \
E_"v123, v123\n" \
C_"v120, v120, v121\n" \
E_"v124, v124\n" \
E_"v125, v125\n" \
"s_waitcnt lgkmcnt(0)\n" \
"s_barrier\n" \
R_"v[144:147], v52 offset:0\n" \
R_"v[148:151], v52 offset:1024\n" \
R_"v[152:155], v52 offset:2048\n" \
".fill (L_ab + 12288 - .) / 4, 4, 0xBF800000\n" \
"L_ap3:\n" \
"s_cbranch_execz L_ap0\n" \
R_"v[156:159], v52 offset:3072\n" \
R_"v[176:179], v52 offset:4096\n" \
R_"v[180:183], v52 offset:5120\n" \
R_"v[184:187], v52 offset:6144\n" \
R_"v[188:191], v52 offset:7168\n" \
M_"a[100:103], a[116:119], v[88:91], a[100:103]\n" \
"ds_read_b64 v[42:43], v63\n" \
C_"v121, v122, v123\n" \
M_"v[224:227], a[160:163], v[88:91], v[224:227]\n" \
E_"v126, v126\n" \
M_"v[228:231], a[164:167], v[88:91], v[228:231]\n" \
E_"v127, v127\n" \
M_"v[232:235], a[168:171], v[88:91], v[232:235]\n" \
C_"v122, v124, v125\n" \
C_"v123, v126, v127\n" \
M_"v[236:239], a[172:175], v[88:91], v[236:239]\n" \
"v_and_b32_e32 v120, v12, v120\n" \
"v_and_b32_e32 v121, v13, v121\n" \
M_"v[240:243], a[176:179], v[88:91], v[240:243]\n" \
"v_and_b32_e32 v122, v14, v122\n" \
"v_and_b32_e32 v123, v15, v123\n" \
M_"v[244:247], a[180:183], v[88:91], v[244:247]\n" \
"v_readlane_b32 s93, v11, 25\n" \
"v_readlane_b32 s92, v11, 30\n" \
M_"v[248:251], a[184:187], v[88:91], v[248:251]\n" \
"s_add_u32 m0, s93, 0x0\n" \
"v_mov_b32_e32 v130, v28\n" \
M_"v[252:255], a[188:191], v[88:91], v[252:255]\n" \
"v_mov_b32_e32 v131, v29\n" \
"v_mov_b32_e32 v132, 0\n" \
"v_mov_b32_e32 v133, 0\n" \
"v_mov_b32_e32 v128, 0\n" \
"v_mov_b32_e32 v129, 0\n" \
"s_waitcnt vmcnt(8) lgkmcnt(1)\n" \
"s_barrier\n" \
M_"a[100:103], a[116:119], v[120:123], a[100:103]\n" \
B_"v0, s[80:83], s92 offen offset:0 lds\n" \
M_"v[224:227], a[0:3], v[120:123], v[224:227]\n" \
M_"v[228:231], a[4:7], v[120:123], v[228:231]\n" \
M_"v[232:235], a[8:11], v[120:123], v[232:235]\n" \
M_"v[236:239], a[12:15], v[120:123], v[236:239]\n" \
M_"v[240:243], a[16:19], v[120:123], v[240:243]\n" \
M_"v[244:247], a[20:23], v[120:123], v[244:247]\n" \
B_"v1, s[80:83], s92 offen offset:1024 lds\n" \
M_"v[248:251], a[24:27], v[120:123], v[248:251]\n" \
"v_accvgpr_read_b32 v56, a100\n" \
"v_accvgpr_read_b32 v24, a101\n" \
M_"v[252:255], a[28:31], v[120:123], v[252:255]\n" \
"v_add_f32_e32 v56, v56, v24\n" \
"s_waitcnt lgkmcnt(0)\n" \
"v_add_f32_e32 v56, v56, v42\n" \
M_"v[192:195], v[130:133], v[144:147], v[192:195]\n" \
"v_max_f32_e32 v62, v62, v56\n" \
"v_accvgpr_read_b32 v57, a102\n" \
M_"v[196:199], v[128:131], v[144:147], v[196:199]\n" \
"v_rcp_f32_e32 v56, v56\n" \
M_"v[200:203], v[130:133], v[148:151], v[200:203]\n" \
"v_mul_f32_e32 v56, v56, v60\n" \
"v_accvgpr_read_b32 v25, a103\n" \
B_"v2, s[80:83], s92 offen offset:2048 lds\n" \
M_"v[204:207], v[128:131], v[148:151], v[204:207]\n" \
"v_mul_f32_e32 v192, v56, v192\n" \
"v_mul_f32_e32 v193, v56, v193\n" \
M_"v[208:211], v[130:133], v[152:155], v[208:211]\n" \
"v_mul_f32_e32 v194, v56, v194\n" \
"v_mul_f32_e32 v195, v56, v195\n" \
M_"v[212:215], v[128:131], v[152:155], v[212:215]\n" \
"v_mul_f32_e32 v196, v56, v196\n" \
"v_mul_f32_e32 v197, v56, v197\n" \
M_"v[216:219], v[130:133], v[156:159], v[216:219]\n" \
"v_mul_f32_e32 v198, v56, v198\n" \
"v_mul_f32_e32 v199, v56, v199\n" \
M_"v[220:223], v[128:131], v[156:159], v[220:223]\n" \
C_"v64, v192, v193\n" \
C_"v65, v194, v195\n" \
M_"v[224:227], v[130:133], v[176:179], v[224:227]\n" \
B_"v3, s[80:83], s92 offen offset:3072 lds\n" \
"s_add_u32 m0, s93, 0x1000\n" \
M_"v[228:231], v[128:131], v[176:179], v[228:231]\n" \
C_"v66, v196, v197\n" \
C_"v67, v198, v199\n" \
M_"v[232:235], v[130:133], v[180:183], v[232:235]\n" \
"v_mul_f32_e32 v200, v56, v200\n" \
"v_mul_f32_e32 v201, v56, v201\n" \
M_"v[236:239], v[128:131], v[180:183], v[236:239]\n" \
M_"a[112:115], a[120:123], v[64:67], a[112:115]\n" \
"v_mul_f32_e32 v202, v56, v202\n" \
"v_mul_f32_e32 v203, v56, v203\n" \
M_"v[240:243], v[130:133], v[184:187], v[240:243]\n" \
"v_mul_f32_e32 v204, v56, v204\n" \
"v_mul_f32_e32 v205, v56, v205\n" \
B_"v4, s[80:83], s92 offen offset:0 lds\n" \
"v_mul_f32_e32 v206, v56, v206\n" \
"v_mul_f32_e32 v207, v56, v207\n" \
"v_add_f32_e32 v57, v57, v25\n" \
C_"v68, v200, v201\n" \
C_"v69, v202, v203\n" \
C_"v70, v204, v205\n" \
C_"v71, v206, v207\n" \
"v_add_f32_e32 v57, v57, v43\n" \
"v_mul_f32_e32 v208, v56, v208\n" \
M_"a[112:115], a[120:123], v[68:71], a[112:115]\n" \
"v_mul_f32_e32 v209, v56, v209\n" \
"v_mul_f32_e32 v210, v56, v210\n" \
"v_mul_f32_e32 v211, v56, v211\n" \
"v_mul_f32_e32 v212, v56, v212\n" \
"v_mul_f32_e32 v213, v56, v213\n" \
"v_mul_f32_e32 v214, v56, v214\n" \
"v_mul_f32_e32 v215, v56, v215\n" \
"v_max_f32_e32 v62, v62, v57\n" \
C_"v72, v208, v209\n" \
C_"v73, v210, v211\n" \
B_"v5, s[80:83], s92 offen offset:1024 lds\n" \
C_"v74, v212, v213\n" \
C_"v75, v214, v215\n" \
"v_rcp_f32_e32 v57, v57\n" \
M_"v[244:247], v[128:131], v[184:187], v[244:247]\n" \
M_"a[112:115], a[120:123], v[72:75], a[112:115]\n" \
"v_mul_f32_e32 v216, v56, v216\n" \
"v_mul_f32_e32 v217, v56, v217\n" \
"v_mul_f32_e32 v218, v56, v218\n" \
"v_mul_f32_e32 v219, v56, v219\n" \
"v_mul_f32_e32 v220, v56, v220\n" \
"v_mul_f32_e32 v221, v56, v221\n" \
"v_mul_f32_e32 v222, v56, v222\n" \
"v_mul_f32_e32 v223, v56, v223\n" \
C_"v76, v216, v217\n" \
C_"v77, v218, v219\n" \
C_"v78, v220, v221\n" \
B_"v6, s[80:83], s92 offen offset:2048 lds\n" \
C_"v79, v222, v223\n" \
"v_mul_f32_e32 v57, v57, v61\n" \
"v_mul_f32_e32 v224, v57, v224\n" \
M_"a[112:115], a[120:123], v[76:79], a[112:115]\n" \
"v_mul_f32_e32 v225, v57, v225\n" \
"v_mul_f32_e32 v226, v57, v226\n" \
"v_mul_f32_e32 v227, v57, v227\n" \
"v_mul_f32_e32 v228, v57, v228\n" \
"v_mul_f32_e32 v229, v57, v229\n" \
"v_mul_f32_e32 v230, v57, v230\n" \
"v_mul_f32_e32 v231, v57, v231\n" \
C_"v80, v224, v225\n" \
C_"v81, v226, v227\n" \
C_"v82, v228, v229\n" \
C_"v83, v230, v231\n" \
M_"v[248:251], v[130:133], v[188:191], v[248:251]\n" \
"v_mul_f32_e32 v232, v57, v232\n" \
"v_mul_f32_e32 v233, v57, v233\n" \
M_"a[112:115], a[120:123], v[80:83], a[112:115]\n" \
B_"v7, s[80:83], s92 offen offset:3072 lds\n" \
"s_add_u32 m0, s93, 0x2000\n" \
"v_mul_f32_e32 v234, v57, v234\n" \
"v_mul_f32_e32 v235, v57, v235\n" \
"v_mul_f32_e32 v236, v57, v236\n" \
"v_mul_f32_e32 v237, v57, v237\n" \
"v_mul_f32_e32 v238, v57, v238\n" \
"v_mul_f32_e32 v239, v57, v239\n" \
C_"v84, v232, v233\n" \
C_"v85, v234, v235\n" \
C_"v86, v236, v237\n" \
C_"v87, v238, v239\n" \
"v_mul_f32_e32 v240, v57, v240\n" \
"v_mul_f32_e32 v241, v57, v241\n" \
M_"a[112:115], a[120:123], v[84:87], a[112:115]\n" \
"v_mul_f32_e32 v242, v57, v242\n" \
"v_mul_f32_e32 v243, v57, v243\n" \
"v_mul_f32_e32 v244, v57, v244\n" \
"v_mul_f32_e32 v245, v57, v245\n" \
"v_mul_f32_e32 v246, v57, v246\n" \
"v_mul_f32_e32 v247, v57, v247\n" \
B_"v8, s[80:83], s92 offen offset:0 lds\n" \
C_"v96, v240, v241\n" \
C_"v97, v242, v243\n" \
C_"v98, v244, v245\n" \
C_"v99, v246, v247\n" \
M_"v[252:255], v[128:131], v[188:191], v[252:255]\n" \
"v_mul_f32_e32 v248, v57, v248\n" \
M_"a[112:115], a[120:123], v[96:99], a[112:115]\n" \
"v_mul_f32_e32 v249, v57, v249\n" \
"v_mul_f32_e32 v250, v57, v250\n" \
"v_mul_f32_e32 v251, v57, v251\n" \
C_"v100, v248, v249\n" \
C_"v101, v250, v251\n" \
"v_cmp_lt_f32_e64 s[72:73], s21, v62\n" \
"v_mul_f32_e32 v252, v57, v252\n" \
"v_mul_f32_e32 v253, v57, v253\n" \
"v_mul_f32_e32 v254, v57, v254\n" \
"v_mul_f32_e32 v255, v57, v255\n" \
C_"v102, v252, v253\n" \
C_"v103, v254, v255\n" \
"s_nop 1\n" \
M_"a[112:115], a[120:123], v[100:103], a[112:115]\n" \
"s_nop 7\n" \
"v_accvgpr_read_b32 v63, a112\n" \
"v_cmp_u_f32_e32 vcc, v63, v63\n" \
"s_nop 0\n" \
"s_or_b64 vcc, s[72:73], vcc\n" \
"s_cbranch_scc1 L_end_%=\n" \
"s_branch L_out_%=\n" \
"L_skb_%=:\n" \
"s_sleep 1\n" \
"buffer_load_dword v57, v61, s[56:59], s96 offen sc1\n" \
"s_add_u32 s101, s101, 1\n" \
"s_waitcnt vmcnt(0)\n" \
"v_cmp_ne_u32_e32 vcc, s62, v57\n" \
"s_cmp_gt_u32 s101, 0x100000\n" \
"s_cbranch_scc1 L_rkb_%=\n" \
"s_nop 2\n" \
"s_cbranch_vccnz L_skb_%=\n" \
"s_branch L_rkb_%=\n" \
"L_sol_%=:\n" \
"s_sleep 1\n" \
"buffer_load_dwordx2 v[26:27], v16, s[56:59], s96 offen sc1\n" \
"s_add_u32 s101, s101, 1\n" \
"s_waitcnt vmcnt(0)\n" \
"v_cmp_ne_u64_e32 vcc, s[62:63], v[26:27]\n" \
"s_cmp_gt_u32 s101, 0x100000\n" \
"s_cbranch_scc1 L_rol_%=\n" \
"s_nop 2\n" \
"s_cbranch_vccnz L_sol_%=\n" \
"s_branch L_rol_%=\n" \
"L_sos_%=:\n" \
"s_sleep 1\n" \
"buffer_load_dwordx2 v[26:27], v16, s[56:59], s96 offen sc1\n" \
"s_add_u32 s101, s101, 1\n" \
"s_waitcnt vmcnt(0)\n" \
"v_cmp_ne_u64_e32 vcc, s[62:63], v[26:27]\n" \
"s_cmp_gt_u32 s101, 0x100000\n" \
"s_cbranch_scc1 L_ros_%=\n" \
"s_nop 2\n" \
"s_cbranch_vccnz L_sos_%=\n" \
"s_branch L_ros_%=\n" \
"L_sa_%=:\n" \
"s_sleep 1\n" \
"buffer_load_dword v26, v38, s[56:59], s64 offen sc1\n" \
"s_add_u32 s101, s101, 1\n" \
"s_waitcnt vmcnt(0)\n" \
"v_cmp_ne_u32_e32 vcc, s62, v26\n" \
"s_cmp_gt_u32 s101, 0x100000\n" \
"s_cbranch_scc1 L_ra_%=\n" \
"s_nop 2\n" \
"s_cbranch_vccnz L_sa_%=\n" \
"s_branch L_ra_%=\n" \
"L_sb_%=:\n" \
"s_sleep 1\n" \
"buffer_load_dword v47, v38, s[56:59], s71 offen sc1\n" \
"s_add_u32 s101, s101, 1\n" \
"s_waitcnt vmcnt(0)\n" \
"v_cmp_ne_u32_e32 vcc, s62, v47\n" \
"s_cmp_gt_u32 s101, 0x100000\n" \
"s_cbranch_scc1 L_rb_%=\n" \
"s_nop 2\n" \
"s_cbranch_vccnz L_sb_%=\n" \
"s_branch L_rb_%=\n" \
"L_sna_%=:\n" \
"buffer_load_dword v37, v40, s[56:59], s74 offen sc1\n" \
"s_add_u32 s101, s101, 1\n" \
"s_waitcnt vmcnt(0)\n" \
"v_cmp_eq_u32_e64 s[98:99], s62, v37\n" \
"s_cmp_gt_u32 s101, 0x100000\n" \
"s_cbranch_scc1 L_rna_%=\n" \
"s_andn2_b32 s75, s20, s98\n" \
"s_cbranch_scc0 L_gna_%=\n" \
"s_sleep 1\n" \
"s_branch L_sna_%=\n" \
"L_gna_%=:\n" \
"s_bitcmp1_b32 s20, 0\n" \
"s_cbranch_scc0 L_kna0_%=\n" \
B_"v[116:119], v20, s[56:59], s12 offen offset:0 sc1\n" \
"L_kna0_%=:\n" \
"s_bitcmp1_b32 s20, 1\n" \
"s_cbranch_scc0 L_kna1_%=\n" \
B_"v[124:127], v20, s[56:59], s12 offen offset:2048 sc1\n" \
"L_kna1_%=:\n" \
"s_bitcmp1_b32 s20, 2\n" \
"s_cbranch_scc0 L_kna2_%=\n" \
B_"v[132:135], v20, s[56:59], s13 offen offset:0 sc1\n" \
"L_kna2_%=:\n" \
"s_bitcmp1_b32 s20, 3\n" \
"s_cbranch_scc0 L_kna3_%=\n" \
B_"v[140:143], v20, s[56:59], s13 offen offset:2048 sc1\n" \
"L_kna3_%=:\n" \
"s_bitcmp1_b32 s20, 4\n" \
"s_cbranch_scc0 L_kna4_%=\n" \
B_"v[148:151], v20, s[56:59], s14 offen offset:0 sc1\n" \
"L_kna4_%=:\n" \
"s_bitcmp1_b32 s20, 5\n" \
"s_cbranch_scc0 L_kna5_%=\n" \
B_"v[156:159], v20, s[56:59], s14 offen offset:2048 sc1\n" \
"L_kna5_%=:\n" \
"s_bitcmp1_b32 s20, 6\n" \
"s_cbranch_scc0 L_kna6_%=\n" \
B_"v[164:167], v20, s[56:59], s15 offen offset:0 sc1\n" \
"L_kna6_%=:\n" \
"s_waitcnt vmcnt(0)\n" \
"s_branch L_rna_%=\n" \
"L_snx_%=:\n" \
"buffer_load_dword v37, v40, s[56:59], s74 offen sc1\n" \
"s_add_u32 s101, s101, 1\n" \
"s_waitcnt vmcnt(0)\n" \
"v_cmp_eq_u32_e64 s[98:99], s62, v37\n" \
"s_cmp_gt_u32 s101, 0x100000\n" \
"s_cbranch_scc1 L_rnx_%=\n" \
"s_andn2_b32 s75, s21, s98\n" \
"s_cbranch_scc0 L_gnx_%=\n" \
"s_sleep 1\n" \
"s_branch L_snx_%=\n" \
"L_gnx_%=:\n" \
"s_bitcmp1_b32 s21, 16\n" \
"s_cbranch_scc0 L_knx16_%=\n" \
B_"v[112:115], v20, s[56:59], s16 offen offset:0 sc1\n" \
"L_knx16_%=:\n" \
"s_bitcmp1_b32 s21, 17\n" \
"s_cbranch_scc0 L_knx17_%=\n" \
B_"v[184:187], v20, s[56:59], s16 offen offset:2048 sc1\n" \
"L_knx17_%=:\n" \
"s_bitcmp1_b32 s21, 18\n" \
"s_cbranch_scc0 L_knx18_%=\n" \
B_"v[120:123], v20, s[56:59], s17 offen offset:0 sc1\n" \
"L_knx18_%=:\n" \
"s_bitcmp1_b32 s21, 19\n" \
"s_cbranch_scc0 L_knx19_%=\n" \
B_"v[192:195], v20, s[56:59], s17 offen offset:2048 sc1\n" \
"L_knx19_%=:\n" \
"s_bitcmp1_b32 s21, 20\n" \
"s_cbranch_scc0 L_knx20_%=\n" \
B_"v[128:131], v20, s[56:59], s18 offen offset:0 sc1\n" \
"L_knx20_%=:\n" \
"s_bitcmp1_b32 s21, 21\n" \
"s_cbranch_scc0 L_knx21_%=\n" \
B_"v[200:203], v20, s[56:59], s18 offen offset:2048 sc1\n" \
"L_knx21_%=:\n" \
"s_bitcmp1_b32 s21, 22\n" \
"s_cbranch_scc0 L_knx22_%=\n" \
B_"v[176:179], v20, s[56:59], s19 offen offset:0 sc1\n" \
"L_knx22_%=:\n" \
"s_bitcmp1_b32 s21, 23\n" \
"s_cbranch_scc0 L_knx23_%=\n" \
B_"v[248:251], v20, s[56:59], s19 offen offset:2048 sc1\n" \
"L_knx23_%=:\n" \
"s_bitcmp1_b32 s21, 8\n" \
"s_cbranch_scc0 L_knx8_%=\n" \
B_"v[188:191], v20, s[56:59], s12 offen offset:1024 sc1\n" \
"L_knx8_%=:\n" \
"s_bitcmp1_b32 s21, 9\n" \
"s_cbranch_scc0 L_knx9_%=\n" \
B_"v[196:199], v20, s[56:59], s12 offen offset:3072 sc1\n" \
"L_knx9_%=:\n" \
"s_waitcnt vmcnt(0)\n" \
"s_branch L_rnx_%=\n" \
"L_sx0_%=:\n" \
"s_sleep 1\n" \
"buffer_load_dword v37, v40, s[56:59], s74 offen sc1\n" \
"s_add_u32 s101, s101, 1\n" \
"s_waitcnt vmcnt(0)\n" \
"v_cmp_eq_u32_e64 s[98:99], s62, v37\n" \
"s_cmp_gt_u32 s101, 0x100000\n" \
"s_cbranch_scc1 L_rx0_%=\n" \
"s_andn2_b32 s75, 0x400, s98\n" \
"s_cbranch_scc1 L_sx0_%=\n" \
"s_branch L_rx0_%=\n" \
"L_sx1_%=:\n" \
"s_sleep 1\n" \
"buffer_load_dword v37, v40, s[56:59], s74 offen sc1\n" \
"s_add_u32 s101, s101, 1\n" \
"s_waitcnt vmcnt(0)\n" \
"v_cmp_eq_u32_e64 s[98:99], s62, v37\n" \
"s_cmp_gt_u32 s101, 0x100000\n" \
"s_cbranch_scc1 L_rx1_%=\n" \
"s_andn2_b32 s75, 0x800, s98\n" \
"s_cbranch_scc1 L_sx1_%=\n" \
"s_branch L_rx1_%=\n" \
"L_sx2_%=:\n" \
"s_sleep 1\n" \
"buffer_load_dword v37, v40, s[56:59], s74 offen sc1\n" \
"s_add_u32 s101, s101, 1\n" \
"s_waitcnt vmcnt(0)\n" \
"v_cmp_eq_u32_e64 s[98:99], s62, v37\n" \
"s_cmp_gt_u32 s101, 0x100000\n" \
"s_cbranch_scc1 L_rx2_%=\n" \
"s_andn2_b32 s75, 0x1000, s98\n" \
"s_cbranch_scc1 L_sx2_%=\n" \
"s_branch L_rx2_%=\n" \
"L_sx3_%=:\n" \
"s_sleep 1\n" \
"buffer_load_dword v37, v40, s[56:59], s74 offen sc1\n" \
"s_add_u32 s101, s101, 1\n" \
"s_waitcnt vmcnt(0)\n" \
"v_cmp_eq_u32_e64 s[98:99], s62, v37\n" \
"s_cmp_gt_u32 s101, 0x100000\n" \
"s_cbranch_scc1 L_rx3_%=\n" \
"s_andn2_b32 s75, 0x2000, s98\n" \
"s_cbranch_scc1 L_sx3_%=\n" \
"s_branch L_rx3_%=\n" \
"L_sx4_%=:\n" \
"s_sleep 1\n" \
"buffer_load_dword v37, v40, s[56:59], s74 offen sc1\n" \
"s_add_u32 s101, s101, 1\n" \
"s_waitcnt vmcnt(0)\n" \
"v_cmp_eq_u32_e64 s[98:99], s62, v37\n" \
"s_cmp_gt_u32 s101, 0x100000\n" \
"s_cbranch_scc1 L_rx4_%=\n" \
"s_andn2_b32 s75, 0x4000, s98\n" \
"s_cbranch_scc1 L_sx4_%=\n" \
"s_branch L_rx4_%=\n" \
"L_att_stub:\n" \
"s_mov_b32 s101, 0\n" \
"v_add_u32_e32 v32, 0x49000, v20\n" \
"v_readlane_b32 s96, v11, 39\n" \
"v_readlane_b32 s97, v11, 40\n" \
B_"a[192:195], v20, s[56:59], s60 offen offset:0 sc1\n" \
B_"a[208:211], v32, s[56:59], s60 offen offset:0 sc1\n" \
B_"a[196:199], v20, s[56:59], s60 offen offset:1024 sc1\n" \
B_"a[212:215], v32, s[56:59], s60 offen offset:1024 sc1\n" \
B_"a[200:203], v20, s[56:59], s60 offen offset:2048 sc1\n" \
B_"a[216:219], v32, s[56:59], s60 offen offset:2048 sc1\n" \
B_"a[204:207], v20, s[56:59], s60 offen offset:3072 sc1\n" \
B_"a[220:223], v32, s[56:59], s60 offen offset:3072 sc1\n" \
B_"a[124:127], v20, s[56:59], s96 offen offset:0 sc1\n" \
B_"a[228:231], v32, s[56:59], s96 offen offset:0 sc1\n" \
B_"a[236:239], v20, s[56:59], s97 offen offset:0 sc1\n" \
B_"a[244:247], v32, s[56:59], s97 offen offset:0 sc1\n" \
B_"a[224:227], v20, s[56:59], s96 offen offset:1024 sc1\n" \
B_"a[232:235], v32, s[56:59], s96 offen offset:1024 sc1\n" \
B_"a[240:243], v20, s[56:59], s97 offen offset:1024 sc1\n" \
B_"a[248:251], v32, s[56:59], s97 offen offset:1024 sc1\n" \
B_"v[64:67], v20, s[56:59], s61 offen offset:0 sc1\n" \
B_"v[68:71], v20, s[56:59], s61 offen offset:1024 sc1\n" \
B_"v[72:75], v20, s[56:59], s61 offen offset:2048 sc1\n" \
B_"v[76:79], v20, s[56:59], s61 offen offset:3072 sc1\n" \
B_"v[80:83], v32, s[56:59], s61 offen offset:0 sc1\n" \
B_"v[84:87], v32, s[56:59], s61 offen offset:1024 sc1\n" \
B_"v[96:99], v32, s[56:59], s61 offen offset:2048 sc1\n" \
B_"v[100:103], v32, s[56:59], s61 offen offset:3072 sc1\n" \
"v_not_b32_e32 v26, s62\n" \
"s_waitcnt vmcnt(0)\n" \
"s_branch L_out_%=\n" \
".fill (L_ab + 16384 - .) / 4, 4, 0xBF800000\n" \
".fill (L_ab + 16384 - .) / 4, 4, 0xBF800000\n" \
"L_ap4:\n" \
"s_setpc_b64 s[88:89]\n" \
"L_gsw:\n" \
M_"v[104:107], a[128:131], a[16:19], v[36:39]\n" \
"s_add_u32 s68, s60, 0xfff6e000\n" \
"s_add_u32 s69, s60, 0xfffb7000\n" \
"s_add_u32 s70, s60, 0x0\n" \
"s_add_u32 s71, s60, 0x49000\n" \
M_"v[104:107], a[132:135], a[20:23], v[104:107]\n" \
"s_xor_b32 m0, s65, 0x12000\n" \
R_"a[160:163], v49 offset:0\n" \
M_"v[104:107], a[136:139], a[24:27], v[104:107]\n" \
B_"v50, s[8:11], s64 offen offset:0 sc1 lds\n" \
M_"v[104:107], a[140:143], a[28:31], v[104:107]\n" \
R_"a[164:167], v49 offset:1024\n" \
R_"a[168:171], v49 offset:2048\n" \
M_"v[108:111], a[156:159], a[28:31], v[36:39]\n" \
R_"a[172:175], v49 offset:3072\n" \
R_"a[176:179], v49 offset:4096\n" \
M_"v[108:111], a[152:155], a[24:27], v[108:111]\n" \
B_"v50, s[8:11], s64 offen offset:1024 sc1 lds\n" \
M_"v[108:111], a[148:151], a[20:23], v[108:111]\n" \
"s_mov_b32 s65, s13\n" \
"v_add_u32_e32 v63, s15, v20\n" \
M_"v[108:111], a[144:147], a[16:19], v[108:111]\n" \
"s_add_u32 s67, s14, 40960\n" \
"v_add_u32_e32 v50, 0x6000, v63\n" \
M_"v[116:119], a[144:147], a[32:35], v[40:43]\n" \
"s_xor_b32 m0, s65, 0x8000\n" \
R_"a[180:183], v49 offset:5120\n" \
M_"v[116:119], a[148:151], a[36:39], v[116:119]\n" \
R_"a[184:187], v49 offset:6144\n" \
"v_and_b32_e32 v68, v12, v68\n" \
M_"v[116:119], a[152:155], a[40:43], v[116:119]\n" \
"v_and_b32_e32 v69, v13, v69\n" \
"v_and_b32_e32 v70, v14, v70\n" \
M_"v[116:119], a[156:159], a[44:47], v[116:119]\n" \
B_"v50, s[8:11], s67 offen offset:0 sc1 lds\n" \
M_"v[112:115], a[140:143], a[44:47], v[40:43]\n" \
"v_and_b32_e32 v71, v15, v71\n" \
R_"a[188:191], v49 offset:7168\n" \
M_"v[112:115], a[136:139], a[40:43], v[112:115]\n" \
E_"v86, v86\n" \
M_"v[112:115], a[132:135], a[36:39], v[112:115]\n" \
E_"v87, v87\n" \
M_"v[112:115], a[128:131], a[32:35], v[112:115]\n" \
E_"v84, v84\n" \
M_"v[120:123], a[128:131], a[48:51], v[44:47]\n" \
B_"v50, s[8:11], s67 offen offset:1024 sc1 lds\n" \
M_"v[120:123], a[132:135], a[52:55], v[120:123]\n" \
E_"v85, v85\n" \
M_"v[120:123], a[136:139], a[56:59], v[120:123]\n" \
C_"v87, v86, v87\n" \
C_"v86, v84, v85\n" \
M_"v[120:123], a[140:143], a[60:63], v[120:123]\n" \
E_"v82, v82\n" \
M_"v[124:127], a[156:159], a[60:63], v[44:47]\n" \
E_"v83, v83\n" \
M_"v[124:127], a[152:155], a[56:59], v[124:127]\n" \
E_"v80, v80\n" \
M_"v[124:127], a[148:151], a[52:55], v[124:127]\n" \
B_"v50, s[8:11], s67 offen offset:2048 sc1 lds\n" \
M_"v[124:127], a[144:147], a[48:51], v[124:127]\n" \
C_"v85, v82, v83\n" \
"s_waitcnt lgkmcnt(1)\n" \
M_"v[128:131], a[160:163], v[68:71], v[128:131]\n" \
E_"v81, v81\n" \
M_"v[160:163], a[160:163], v[72:75], v[160:163]\n" \
C_"v84, v80, v81\n" \
"s_mov_b32 s64, s14\n" \
M_"v[132:135], a[164:167], v[68:71], v[132:135]\n" \
E_"v88, v88\n" \
M_"v[164:167], a[164:167], v[72:75], v[164:167]\n" \
E_"v89, v89\n" \
M_"v[136:139], a[168:171], v[68:71], v[136:139]\n" \
B_"v50, s[8:11], s67 offen offset:3072 sc1 lds\n" \
M_"v[168:171], a[168:171], v[72:75], v[168:171]\n" \
E_"v90, v90\n" \
M_"v[140:143], a[172:175], v[68:71], v[140:143]\n" \
E_"v91, v91\n" \
M_"v[172:175], a[172:175], v[72:75], v[172:175]\n" \
C_"v88, v88, v89\n" \
C_"v89, v90, v91\n" \
M_"v[144:147], a[176:179], v[68:71], v[144:147]\n" \
E_"v92, v92\n" \
M_"v[176:179], a[176:179], v[72:75], v[176:179]\n" \
E_"v93, v93\n" \
M_"v[148:151], a[180:183], v[68:71], v[148:151]\n" \
E_"v94, v94\n" \
M_"v[180:183], a[180:183], v[72:75], v[180:183]\n" \
E_"v95, v95\n" \
M_"v[152:155], a[184:187], v[68:71], v[152:155]\n" \
C_"v90, v92, v93\n" \
C_"v91, v94, v95\n" \
M_"v[184:187], a[184:187], v[72:75], v[184:187]\n" \
"v_add_u32_e32 v51, 0x12000, v63\n" \
"s_waitcnt lgkmcnt(0)\n" \
M_"v[156:159], a[188:191], v[68:71], v[156:159]\n" \
"s_xor_b32 m0, s65, 0xa000\n" \
"ds_read_b32 v58, v18 offset:512\n" \
M_"v[188:191], a[188:191], v[72:75], v[188:191]\n" \
B_"v51, s[8:11], s64 offen offset:0 sc1 lds\n" \
"v_smfmac_f32_16x16x64_bf16 a[96:99], a[104:107], v[68:75], v21\n" \
B_"v51, s[8:11], s64 offen offset:1024 sc1 lds\n" \
M_"v[220:223], a[188:191], v[84:87], v[220:223]\n" \
"ds_read_b32 v59, v18 offset:640\n" \
"ds_read_b32 v60, v18 offset:768\n" \
M_"v[252:255], a[188:191], v[88:91], v[252:255]\n" \
"ds_read_b32 v61, v18 offset:896\n" \
R_"a[128:131], v48 offset:0\n" \
M_"v[216:219], a[184:187], v[84:87], v[216:219]\n" \
R_"a[144:147], v48 offset:4096\n" \
R_"a[132:135], v48 offset:1024\n" \
M_"v[248:251], a[184:187], v[88:91], v[248:251]\n" \
R_"a[148:151], v48 offset:5120\n" \
R_"a[136:139], v48 offset:2048\n" \
M_"v[212:215], a[180:183], v[84:87], v[212:215]\n" \
R_"a[152:155], v48 offset:6144\n" \
R_"a[140:143], v48 offset:3072\n" \
M_"v[244:247], a[180:183], v[88:91], v[244:247]\n" \
R_"a[156:159], v48 offset:7168\n" \
"v_add_u32_e32 v52, 0x14000, v63\n" \
M_"v[208:211], a[176:179], v[84:87], v[208:211]\n" \
B_"v51, s[8:11], s64 offen offset:2048 sc1 lds\n" \
M_"v[240:243], a[176:179], v[88:91], v[240:243]\n" \
E_"v104, v104\n" \
M_"v[204:207], a[172:175], v[84:87], v[204:207]\n" \
E_"v105, v105\n" \
M_"v[236:239], a[172:175], v[88:91], v[236:239]\n" \
B_"v51, s[8:11], s64 offen offset:3072 sc1 lds\n" \
M_"v[200:203], a[168:171], v[84:87], v[200:203]\n" \
E_"v106, v106\n" \
M_"v[232:235], a[168:171], v[88:91], v[232:235]\n" \
E_"v107, v107\n" \
M_"v[196:199], a[164:167], v[84:87], v[196:199]\n" \
C_"v104, v104, v105\n" \
C_"v105, v106, v107\n" \
M_"v[228:231], a[164:167], v[88:91], v[228:231]\n" \
E_"v108, v108\n" \
M_"v[192:195], a[160:163], v[84:87], v[192:195]\n" \
E_"v109, v109\n" \
M_"v[224:227], a[160:163], v[88:91], v[224:227]\n" \
E_"v110, v110\n" \
"v_smfmac_f32_16x16x64_bf16 a[100:103], a[104:107], v[84:91], v21\n" \
E_"v111, v111\n" \
C_"v106, v108, v109\n" \
C_"v107, v110, v111\n" \
"v_and_b32_e32 v104, v12, v104\n" \
"v_and_b32_e32 v105, v13, v105\n" \
"v_and_b32_e32 v106, v14, v106\n" \
"v_and_b32_e32 v107, v15, v107\n" \
"v_accvgpr_read_b32 v54, a96\n" \
"v_accvgpr_read_b32 v22, a97\n" \
"v_add_u32_e32 v53, 0x20000, v63\n" \
E_"v118, v118\n" \
E_"v119, v119\n" \
E_"v116, v116\n" \
E_"v117, v117\n" \
C_"v119, v118, v119\n" \
E_"v114, v114\n" \
E_"v115, v115\n" \
E_"v112, v112\n" \
E_"v113, v113\n" \
C_"v118, v116, v117\n" \
C_"v117, v114, v115\n" \
C_"v116, v112, v113\n" \
"v_readlane_b32 s93, v11, 22\n" \
"s_waitcnt vmcnt(10) lgkmcnt(0)\n" \
"s_barrier\n" \
M_"v[80:83], a[128:131], a[32:35], v[40:43]\n" \
R_"a[160:163], v49 offset:8192\n" \
R_"a[164:167], v49 offset:9216\n" \
M_"v[80:83], a[132:135], a[36:39], v[80:83]\n" \
R_"a[168:171], v49 offset:10240\n" \
"s_xor_b32 m0, s65, 0x18000\n" \
M_"v[80:83], a[136:139], a[40:43], v[80:83]\n" \
B_"v52, s[8:11], s64 offen offset:0 sc1 lds\n" \
M_"v[80:83], a[140:143], a[44:47], v[80:83]\n" \
R_"a[172:175], v49 offset:11264\n" \
R_"a[176:179], v49 offset:12288\n" \
M_"v[84:87], a[156:159], a[44:47], v[40:43]\n" \
R_"a[180:183], v49 offset:13312\n" \
R_"a[184:187], v49 offset:14336\n" \
M_"v[84:87], a[152:155], a[40:43], v[84:87]\n" \
B_"v52, s[8:11], s64 offen offset:1024 sc1 lds\n" \
M_"v[84:87], a[148:151], a[36:39], v[84:87]\n" \
R_"a[188:191], v49 offset:15360\n" \
"v_readlane_b32 s80, v11, 11\n" \
M_"v[84:87], a[144:147], a[32:35], v[84:87]\n" \
B_"v52, s[8:11], s64 offen offset:2048 sc1 lds\n" \
M_"v[92:95], a[144:147], a[48:51], v[44:47]\n" \
E_"v120, v120\n" \
M_"v[92:95], a[148:151], a[52:55], v[92:95]\n" \
E_"v121, v121\n" \
M_"v[92:95], a[152:155], a[56:59], v[92:95]\n" \
B_"v52, s[8:11], s64 offen offset:3072 sc1 lds\n" \
M_"v[92:95], a[156:159], a[60:63], v[92:95]\n" \
"s_mov_b32 m0, s65\n" \
C_"v120, v120, v121\n" \
M_"v[88:91], a[140:143], a[60:63], v[44:47]\n" \
E_"v122, v122\n" \
M_"v[88:91], a[136:139], a[56:59], v[88:91]\n" \
B_"v53, s[8:11], s64 offen offset:0 sc1 lds\n" \
M_"v[88:91], a[132:135], a[52:55], v[88:91]\n" \
E_"v123, v123\n" \
M_"v[88:91], a[128:131], a[48:51], v[88:91]\n" \
E_"v124, v124\n" \
M_"a[96:99], a[116:119], v[104:107], a[96:99]\n" \
C_"v121, v122, v123\n" \
"s_waitcnt lgkmcnt(5)\n" \
M_"v[160:163], a[160:163], v[104:107], v[160:163]\n" \
B_"v53, s[8:11], s64 offen offset:1024 sc1 lds\n" \
M_"v[164:167], a[164:167], v[104:107], v[164:167]\n" \
E_"v125, v125\n" \
M_"v[168:171], a[168:171], v[104:107], v[168:171]\n" \
C_"v122, v124, v125\n" \
"s_waitcnt lgkmcnt(1)\n" \
M_"v[172:175], a[172:175], v[104:107], v[172:175]\n" \
E_"v126, v126\n" \
M_"v[176:179], a[176:179], v[104:107], v[176:179]\n" \
E_"v127, v127\n" \
M_"v[180:183], a[180:183], v[104:107], v[180:183]\n" \
C_"v123, v126, v127\n" \
"v_readlane_b32 s81, v11, 12\n" \
M_"v[184:187], a[184:187], v[104:107], v[184:187]\n" \
"s_mov_b32 s82, 0x47fca0\n" \
"s_waitcnt lgkmcnt(0)\n" \
M_"v[188:191], a[188:191], v[104:107], v[188:191]\n" \
B_"v53, s[8:11], s64 offen offset:2048 sc1 lds\n" \
M_"v[220:223], a[188:191], v[116:119], v[220:223]\n" \
B_"v53, s[8:11], s64 offen offset:3072 sc1 lds\n" \
M_"v[252:255], a[188:191], v[120:123], v[252:255]\n" \
"s_mov_b32 s83, 0x20000\n" \
"v_readlane_b32 s92, v11, 29\n" \
M_"v[216:219], a[184:187], v[116:119], v[216:219]\n" \
"s_add_u32 m0, s93, 0x0\n" \
R_"a[128:131], v48 offset:8192\n" \
M_"v[248:251], a[184:187], v[120:123], v[248:251]\n" \
R_"a[144:147], v48 offset:12288\n" \
R_"a[132:135], v48 offset:9216\n" \
M_"v[212:215], a[180:183], v[116:119], v[212:215]\n" \
B_"v0, s[80:83], s92 offen offset:0 lds\n" \
M_"v[244:247], a[180:183], v[120:123], v[244:247]\n" \
B_"v1, s[80:83], s92 offen offset:1024 lds\n" \
M_"v[208:211], a[176:179], v[116:119], v[208:211]\n" \
R_"a[148:151], v48 offset:13312\n" \
R_"a[136:139], v48 offset:10240\n" \
M_"v[240:243], a[176:179], v[120:123], v[240:243]\n" \
R_"a[152:155], v48 offset:14336\n" \
R_"a[140:143], v48 offset:11264\n" \
M_"v[204:207], a[172:175], v[116:119], v[204:207]\n" \
B_"v2, s[80:83], s92 offen offset:2048 lds\n" \
M_"v[236:239], a[172:175], v[120:123], v[236:239]\n" \
R_"a[156:159], v48 offset:15360\n" \
"s_xor_b32 s65, s65, 0x8000\n" \
M_"v[200:203], a[168:171], v[116:119], v[200:203]\n" \
"s_add_u32 s64, s64, 0x10000\n" \
"v_add_u32_e32 v49, 0x10000, v48\n" \
M_"v[232:235], a[168:171], v[120:123], v[232:235]\n" \
B_"v3, s[80:83], s92 offen offset:3072 lds\n" \
M_"v[196:199], a[164:167], v[116:119], v[196:199]\n" \
E_"v86, v86\n" \
M_"v[228:231], a[164:167], v[120:123], v[228:231]\n" \
E_"v87, v87\n" \
M_"v[192:195], a[160:163], v[116:119], v[192:195]\n" \
E_"v84, v84\n" \
M_"v[224:227], a[160:163], v[120:123], v[224:227]\n" \
E_"v85, v85\n" \
"v_smfmac_f32_16x16x64_bf16 a[100:103], a[104:107], v[116:123], v21\n" \
E_"v88, v88\n" \
E_"v89, v89\n" \
C_"v87, v86, v87\n" \
E_"v82, v82\n" \
E_"v83, v83\n" \
E_"v80, v80\n" \
E_"v81, v81\n" \
E_"v90, v90\n" \
E_"v91, v91\n" \
C_"v86, v84, v85\n" \
C_"v88, v88, v89\n" \
E_"v92, v92\n" \
E_"v93, v93\n" \
E_"v94, v94\n" \
E_"v95, v95\n" \
C_"v85, v82, v83\n" \
C_"v84, v80, v81\n" \
C_"v89, v90, v91\n" \
"v_and_b32_e32 v84, v12, v84\n" \
"v_and_b32_e32 v85, v13, v85\n" \
"v_and_b32_e32 v86, v14, v86\n" \
"v_and_b32_e32 v87, v15, v87\n" \
C_"v90, v92, v93\n" \
C_"v91, v94, v95\n" \
"v_add_u32_e32 v48, s16, v20\n" \
"v_add_f32_e32 v54, v54, v22\n" \
"v_accvgpr_read_b32 v55, a98\n" \
"v_accvgpr_read_b32 v23, a99\n" \
M_"a[96:99], a[120:123], a[120:123], 0\n" \
"v_max_f32_e32 v62, v62, v54\n" \
"v_add_f32_e32 v55, v55, v23\n" \
"v_max_f32_e32 v62, v62, v55\n" \
"v_rcp_f32_e32 v54, v54\n" \
"v_rcp_f32_e32 v55, v55\n" \
"v_mul_f32_e32 v54, v54, v58\n" \
"v_mul_f32_e32 v55, v55, v59\n" \
"v_mul_f32_e32 v128, v54, v128\n" \
"v_mul_f32_e32 v129, v54, v129\n" \
"v_mul_f32_e32 v136, v54, v136\n" \
"v_mul_f32_e32 v137, v54, v137\n" \
"v_mul_f32_e32 v144, v54, v144\n" \
"v_mul_f32_e32 v145, v54, v145\n" \
"v_mul_f32_e32 v152, v54, v152\n" \
"v_mul_f32_e32 v153, v54, v153\n" \
"v_mul_f32_e32 v160, v55, v160\n" \
"v_mul_f32_e32 v161, v55, v161\n" \
"v_mul_f32_e32 v168, v55, v168\n" \
"v_mul_f32_e32 v169, v55, v169\n" \
"v_mul_f32_e32 v176, v55, v176\n" \
"v_mul_f32_e32 v177, v55, v177\n" \
"v_mul_f32_e32 v184, v55, v184\n" \
"v_mul_f32_e32 v185, v55, v185\n" \
C_"v128, v128, v129\n" \
"v_mul_f32_e32 v130, v54, v130\n" \
"v_mul_f32_e32 v131, v54, v131\n" \
C_"v136, v136, v137\n" \
"v_mul_f32_e32 v138, v54, v138\n" \
"v_mul_f32_e32 v139, v54, v139\n" \
C_"v144, v144, v145\n" \
"v_mul_f32_e32 v146, v54, v146\n" \
"v_mul_f32_e32 v147, v54, v147\n" \
C_"v152, v152, v153\n" \
"v_mul_f32_e32 v154, v54, v154\n" \
"v_mul_f32_e32 v155, v54, v155\n" \
C_"v160, v160, v161\n" \
"v_mul_f32_e32 v162, v55, v162\n" \
"v_mul_f32_e32 v163, v55, v163\n" \
C_"v168, v168, v169\n" \
"v_mul_f32_e32 v170, v55, v170\n" \
"v_mul_f32_e32 v171, v55, v171\n" \
C_"v176, v176, v177\n" \
"v_mul_f32_e32 v178, v55, v178\n" \
"v_mul_f32_e32 v179, v55, v179\n" \
C_"v184, v184, v185\n" \
"v_mul_f32_e32 v186, v55, v186\n" \
"v_mul_f32_e32 v187, v55, v187\n" \
C_"v129, v130, v131\n" \
"v_mul_f32_e32 v132, v54, v132\n" \
"v_mul_f32_e32 v133, v54, v133\n" \
"v_mul_f32_e32 v134, v54, v134\n" \
"v_mul_f32_e32 v135, v54, v135\n" \
C_"v137, v138, v139\n" \
"v_mul_f32_e32 v140, v54, v140\n" \
"s_waitcnt vmcnt(12) lgkmcnt(0)\n" \
"s_barrier\n" \
M_"v[120:123], a[128:131], a[48:51], v[44:47]\n" \
"s_xor_b32 m0, s65, 0x12000\n" \
R_"a[188:191], v49 offset:7168\n" \
M_"v[120:123], a[132:135], a[52:55], v[120:123]\n" \
B_"v50, s[8:11], s64 offen offset:0 sc1 lds\n" \
M_"v[120:123], a[136:139], a[56:59], v[120:123]\n" \
R_"a[184:187], v49 offset:6144\n" \
R_"a[180:183], v49 offset:5120\n" \
M_"v[120:123], a[140:143], a[60:63], v[120:123]\n" \
B_"v50, s[8:11], s64 offen offset:1024 sc1 lds\n" \
M_"v[124:127], a[156:159], a[60:63], v[44:47]\n" \
R_"a[176:179], v49 offset:4096\n" \
R_"a[172:175], v49 offset:3072\n" \
M_"v[124:127], a[152:155], a[56:59], v[124:127]\n" \
R_"a[168:171], v49 offset:2048\n" \
R_"a[164:167], v49 offset:1024\n" \
M_"v[124:127], a[148:151], a[52:55], v[124:127]\n" \
R_"a[160:163], v49 offset:0\n" \
R_"a[0:3], v49 offset:8192\n" \
M_"v[124:127], a[144:147], a[48:51], v[124:127]\n" \
B_"v50, s[8:11], s64 offen offset:2048 sc1 lds\n" \
M_"v[32:35], a[124:127], a[64:67], 0\n" \
R_"a[4:7], v49 offset:9216\n" \
R_"a[8:11], v49 offset:10240\n" \
M_"v[36:39], a[124:127], a[80:83], 0\n" \
R_"a[12:15], v49 offset:11264\n" \
R_"a[16:19], v49 offset:12288\n" \
M_"v[40:43], a[124:127], a[192:195], 0\n" \
R_"a[20:23], v49 offset:13312\n" \
R_"a[24:27], v49 offset:14336\n" \
M_"v[44:47], a[124:127], a[208:211], 0\n" \
B_"v50, s[8:11], s64 offen offset:3072 sc1 lds\n" \
M_"v[32:35], a[224:227], a[68:71], v[32:35]\n" \
"s_xor_b32 m0, s65, 0xa000\n" \
R_"a[28:31], v49 offset:15360\n" \
M_"v[36:39], a[224:227], a[84:87], v[36:39]\n" \
E_"v120, v120\n" \
M_"v[40:43], a[224:227], a[196:199], v[40:43]\n" \
B_"v51, s[8:11], s64 offen offset:0 sc1 lds\n" \
M_"v[44:47], a[224:227], a[212:215], v[44:47]\n" \
E_"v121, v121\n" \
M_"v[32:35], a[228:231], a[72:75], v[32:35]\n" \
E_"v122, v122\n" \
M_"v[36:39], a[228:231], a[88:91], v[36:39]\n" \
E_"v123, v123\n" \
M_"v[40:43], a[228:231], a[200:203], v[40:43]\n" \
B_"v51, s[8:11], s64 offen offset:1024 sc1 lds\n" \
M_"v[44:47], a[228:231], a[216:219], v[44:47]\n" \
C_"v120, v120, v121\n" \
C_"v121, v122, v123\n" \
M_"v[32:35], a[232:235], a[76:79], v[32:35]\n" \
E_"v124, v124\n" \
M_"v[36:39], a[232:235], a[92:95], v[36:39]\n" \
E_"v125, v125\n" \
M_"v[40:43], a[232:235], a[204:207], v[40:43]\n" \
B_"v51, s[8:11], s64 offen offset:2048 sc1 lds\n" \
M_"v[44:47], a[232:235], a[220:223], v[44:47]\n" \
E_"v126, v126\n" \
"v_smfmac_f32_16x16x64_bf16 a[100:103], a[104:107], v[84:91], v21\n" \
C_"v122, v124, v125\n" \
"s_waitcnt lgkmcnt(1)\n" \
M_"v[220:223], a[188:191], v[84:87], v[220:223]\n" \
B_"v51, s[8:11], s64 offen offset:3072 sc1 lds\n" \
M_"v[252:255], a[188:191], v[88:91], v[252:255]\n" \
"s_add_u32 m0, s93, 0x1000\n" \
"v_and_b32_e32 v120, v12, v120\n" \
M_"v[216:219], a[184:187], v[84:87], v[216:219]\n" \
E_"v127, v127\n" \
M_"v[248:251], a[184:187], v[88:91], v[248:251]\n" \
C_"v123, v126, v127\n" \
"v_and_b32_e32 v121, v13, v121\n" \
M_"v[212:215], a[180:183], v[84:87], v[212:215]\n" \
B_"v4, s[80:83], s92 offen offset:0 lds\n" \
M_"v[244:247], a[180:183], v[88:91], v[244:247]\n" \
"v_and_b32_e32 v122, v14, v122\n" \
"v_and_b32_e32 v123, v15, v123\n" \
M_"v[208:211], a[176:179], v[84:87], v[208:211]\n" \
"v_accvgpr_read_b32 v56, a100\n" \
"v_accvgpr_read_b32 v24, a101\n" \
M_"v[240:243], a[176:179], v[88:91], v[240:243]\n" \
R_"a[128:131], v48 offset:0\n" \
R_"a[144:147], v48 offset:4096\n" \
M_"v[204:207], a[172:175], v[84:87], v[204:207]\n" \
B_"v5, s[80:83], s92 offen offset:1024 lds\n" \
M_"v[236:239], a[172:175], v[88:91], v[236:239]\n" \
R_"a[132:135], v48 offset:1024\n" \
R_"a[148:151], v48 offset:5120\n" \
M_"v[200:203], a[168:171], v[84:87], v[200:203]\n" \
R_"a[136:139], v48 offset:2048\n" \
R_"a[152:155], v48 offset:6144\n" \
M_"v[232:235], a[168:171], v[88:91], v[232:235]\n" \
B_"v6, s[80:83], s92 offen offset:2048 lds\n" \
M_"v[196:199], a[164:167], v[84:87], v[196:199]\n" \
R_"a[140:143], v48 offset:3072\n" \
R_"a[156:159], v48 offset:7168\n" \
M_"v[228:231], a[164:167], v[88:91], v[228:231]\n" \
"v_mul_f32_e32 v141, v54, v141\n" \
"v_mul_f32_e32 v142, v54, v142\n" \
M_"v[192:195], a[160:163], v[84:87], v[192:195]\n" \
"v_mul_f32_e32 v143, v54, v143\n" \
C_"v145, v146, v147\n" \
M_"v[224:227], a[160:163], v[88:91], v[224:227]\n" \
B_"v7, s[80:83], s92 offen offset:3072 lds\n" \
M_"a[100:103], a[116:119], v[120:123], a[100:103]\n" \
"v_mul_f32_e32 v148, v54, v148\n" \
"v_mul_f32_e32 v149, v54, v149\n" \
M_"v[224:227], a[0:3], v[120:123], v[224:227]\n" \
"v_mul_f32_e32 v150, v54, v150\n" \
"v_mul_f32_e32 v151, v54, v151\n" \
M_"v[228:231], a[4:7], v[120:123], v[228:231]\n" \
C_"v153, v154, v155\n" \
"v_mul_f32_e32 v156, v54, v156\n" \
M_"v[232:235], a[8:11], v[120:123], v[232:235]\n" \
"v_mul_f32_e32 v157, v54, v157\n" \
"v_mul_f32_e32 v158, v54, v158\n" \
M_"v[236:239], a[12:15], v[120:123], v[236:239]\n" \
"v_mul_f32_e32 v159, v54, v159\n" \
C_"v161, v162, v163\n" \
M_"v[240:243], a[16:19], v[120:123], v[240:243]\n" \
"v_mul_f32_e32 v164, v55, v164\n" \
"v_mul_f32_e32 v165, v55, v165\n" \
M_"v[244:247], a[20:23], v[120:123], v[244:247]\n" \
"v_mul_f32_e32 v166, v55, v166\n" \
"v_mul_f32_e32 v167, v55, v167\n" \
M_"v[248:251], a[24:27], v[120:123], v[248:251]\n" \
C_"v169, v170, v171\n" \
"s_waitcnt lgkmcnt(4)\n" \
M_"v[252:255], a[28:31], v[120:123], v[252:255]\n" \
"v_mul_f32_e32 v172, v55, v172\n" \
"v_mul_f32_e32 v173, v55, v173\n" \
"v_mul_f32_e32 v174, v55, v174\n" \
"v_mul_f32_e32 v175, v55, v175\n" \
C_"v177, v178, v179\n" \
"v_mul_f32_e32 v180, v55, v180\n" \
"v_mul_f32_e32 v181, v55, v181\n" \
"v_mul_f32_e32 v182, v55, v182\n" \
"v_mul_f32_e32 v183, v55, v183\n" \
C_"v185, v186, v187\n" \
"v_mul_f32_e32 v188, v55, v188\n" \
"v_mul_f32_e32 v189, v55, v189\n" \
"v_mul_f32_e32 v190, v55, v190\n" \
"v_mul_f32_e32 v191, v55, v191\n" \
C_"v130, v132, v133\n" \
C_"v131, v134, v135\n" \
"buffer_store_dwordx4 v[128:131], v20, s[56:59], s68 offen offset:0 sc1\n" \
C_"v138, v140, v141\n" \
C_"v139, v142, v143\n" \
"buffer_store_dwordx4 v[136:139], v20, s[56:59], s68 offen offset:1024 sc1\n" \
C_"v146, v148, v149\n" \
C_"v147, v150, v151\n" \
"buffer_store_dwordx4 v[144:147], v20, s[56:59], s68 offen offset:2048 sc1\n" \
C_"v154, v156, v157\n" \
C_"v155, v158, v159\n" \
"buffer_store_dwordx4 v[152:155], v20, s[56:59], s68 offen offset:3072 sc1\n" \
C_"v162, v164, v165\n" \
C_"v163, v166, v167\n" \
"buffer_store_dwordx4 v[160:163], v20, s[56:59], s69 offen offset:0 sc1\n" \
C_"v170, v172, v173\n" \
C_"v171, v174, v175\n" \
"buffer_store_dwordx4 v[168:171], v20, s[56:59], s69 offen offset:1024 sc1\n" \
C_"v178, v180, v181\n" \
C_"v179, v182, v183\n" \
"buffer_store_dwordx4 v[176:179], v20, s[56:59], s69 offen offset:2048 sc1\n" \
C_"v186, v188, v189\n" \
C_"v187, v190, v191\n" \
"buffer_store_dwordx4 v[184:187], v20, s[56:59], s69 offen offset:3072 sc1\n" \
"s_add_u32 m0, s93, 0x2000\n" \
M_"a[112:115], a[120:123], v[128:131], a[112:115]\n" \
B_"v8, s[80:83], s92 offen offset:0 lds\n" \
M_"a[112:115], a[120:123], v[136:139], a[112:115]\n" \
"v_mfma_f32_32x32x16_bf16 v[128:143], a[120:123], a[120:123], 0\n" \
M_"a[112:115], a[120:123], v[144:147], a[112:115]\n" \
M_"a[112:115], a[120:123], v[152:155], a[112:115]\n" \
"v_mfma_f32_32x32x16_bf16 v[144:159], a[120:123], a[120:123], 0\n" \
"v_add_f32_e32 v56, v56, v24\n" \
M_"a[112:115], a[120:123], v[160:163], a[112:115]\n" \
"v_max_f32_e32 v62, v62, v56\n" \
"v_accvgpr_read_b32 v57, a102\n" \
"s_waitcnt vmcnt(21) lgkmcnt(0)\n" \
"s_barrier\n" \
M_"v[64:67], a[128:131], a[64:67], v[32:35]\n" \
"s_xor_b32 m0, s65, 0x18000\n" \
"v_accvgpr_read_b32 v25, a103\n" \
M_"a[100:103], a[120:123], a[120:123], 0\n" \
B_"v52, s[8:11], s64 offen offset:0 sc1 lds\n" \
M_"v[64:67], a[132:135], a[68:71], v[64:67]\n" \
"v_rcp_f32_e32 v56, v56\n" \
M_"v[64:67], a[136:139], a[72:75], v[64:67]\n" \
M_"a[112:115], a[120:123], v[168:171], a[112:115]\n" \
"v_mfma_f32_32x32x16_bf16 v[160:175], a[120:123], a[120:123], 0\n" \
"v_mul_f32_e32 v56, v56, v60\n" \
M_"a[112:115], a[120:123], v[176:179], a[112:115]\n" \
"v_mul_f32_e32 v192, v56, v192\n" \
"v_mul_f32_e32 v193, v56, v193\n" \
M_"v[64:67], a[140:143], a[76:79], v[64:67]\n" \
B_"v52, s[8:11], s64 offen offset:1024 sc1 lds\n" \
M_"v[68:71], a[156:159], a[76:79], v[32:35]\n" \
C_"v192, v192, v193\n" \
"v_mul_f32_e32 v194, v56, v194\n" \
M_"v[68:71], a[152:155], a[72:75], v[68:71]\n" \
"v_mul_f32_e32 v195, v56, v195\n" \
C_"v193, v194, v195\n" \
M_"v[68:71], a[148:151], a[68:71], v[68:71]\n" \
M_"a[112:115], a[120:123], v[184:187], a[112:115]\n" \
"v_mfma_f32_32x32x16_bf16 v[176:191], a[120:123], a[120:123], 0\n" \
"v_mul_f32_e32 v196, v56, v196\n" \
"v_mul_f32_e32 v197, v56, v197\n" \
"v_mul_f32_e32 v198, v56, v198\n" \
"v_mul_f32_e32 v199, v56, v199\n" \
"v_mul_f32_e32 v200, v56, v200\n" \
"v_mul_f32_e32 v201, v56, v201\n" \
M_"v[68:71], a[144:147], a[64:67], v[68:71]\n" \
B_"v52, s[8:11], s64 offen offset:2048 sc1 lds\n" \
M_"v[76:79], a[144:147], a[80:83], v[36:39]\n" \
C_"v194, v196, v197\n" \
C_"v195, v198, v199\n" \
"buffer_store_dwordx4 v[192:195], v20, s[56:59], s70 offen offset:0 sc1\n" \
M_"v[76:79], a[148:151], a[84:87], v[76:79]\n" \
C_"v200, v200, v201\n" \
"v_mul_f32_e32 v202, v56, v202\n" \
M_"v[76:79], a[152:155], a[88:91], v[76:79]\n" \
"v_mul_f32_e32 v203, v56, v203\n" \
"v_add_f32_e32 v57, v57, v25\n" \
M_"v[76:79], a[156:159], a[92:95], v[76:79]\n" \
B_"v52, s[8:11], s64 offen offset:3072 sc1 lds\n" \
M_"v[72:75], a[140:143], a[92:95], v[36:39]\n" \
"s_mov_b32 m0, s65\n" \
C_"v201, v202, v203\n" \
M_"v[72:75], a[136:139], a[88:91], v[72:75]\n" \
M_"a[112:115], a[120:123], v[192:195], a[112:115]\n" \
"v_mul_f32_e32 v204, v56, v204\n" \
"v_mul_f32_e32 v205, v56, v205\n" \
M_"v[72:75], a[132:135], a[84:87], v[72:75]\n" \
"v_mul_f32_e32 v206, v56, v206\n" \
"v_mul_f32_e32 v207, v56, v207\n" \
M_"v[72:75], a[128:131], a[80:83], v[72:75]\n" \
B_"v53, s[8:11], s64 offen offset:0 sc1 lds\n" \
M_"v[80:83], a[128:131], a[192:195], v[40:43]\n" \
"v_mul_f32_e32 v208, v56, v208\n" \
"v_mul_f32_e32 v209, v56, v209\n" \
M_"v[80:83], a[132:135], a[196:199], v[80:83]\n" \
"v_max_f32_e32 v62, v62, v57\n" \
C_"v202, v204, v205\n" \
M_"v[80:83], a[136:139], a[200:203], v[80:83]\n" \
B_"v53, s[8:11], s64 offen offset:1024 sc1 lds\n" \
M_"v[80:83], a[140:143], a[204:207], v[80:83]\n" \
C_"v203, v206, v207\n" \
C_"v208, v208, v209\n" \
"buffer_store_dwordx4 v[200:203], v20, s[56:59], s70 offen offset:1024 sc1\n" \
M_"v[84:87], a[156:159], a[204:207], v[40:43]\n" \
"v_mul_f32_e32 v210, v56, v210\n" \
"v_mul_f32_e32 v211, v56, v211\n" \
M_"v[84:87], a[152:155], a[200:203], v[84:87]\n" \
"v_rcp_f32_e32 v57, v57\n" \
M_"v[84:87], a[148:151], a[196:199], v[84:87]\n" \
B_"v53, s[8:11], s64 offen offset:2048 sc1 lds\n" \
M_"v[84:87], a[144:147], a[192:195], v[84:87]\n" \
M_"a[112:115], a[120:123], v[200:203], a[112:115]\n" \
"v_mfma_f32_32x32x16_bf16 v[192:207], a[120:123], a[120:123], 0\n" \
C_"v209, v210, v211\n" \
"v_mul_f32_e32 v212, v56, v212\n" \
".fill (L_ab + 20480 - .) / 4, 4, 0xBF800000\n" \
"L_ap5:\n" \
"s_cbranch_execz L_ap0\n" \
"v_mul_f32_e32 v213, v56, v213\n" \
"v_mul_f32_e32 v214, v56, v214\n" \
"v_mul_f32_e32 v215, v56, v215\n" \
"v_mul_f32_e32 v216, v56, v216\n" \
M_"v[92:95], a[144:147], a[208:211], v[44:47]\n" \
R_"a[144:147], v48 offset:12288\n" \
"v_mul_f32_e32 v217, v56, v217\n" \
M_"v[92:95], a[148:151], a[212:215], v[92:95]\n" \
R_"a[148:151], v48 offset:13312\n" \
C_"v210, v212, v213\n" \
M_"v[92:95], a[152:155], a[216:219], v[92:95]\n" \
R_"a[152:155], v48 offset:14336\n" \
C_"v211, v214, v215\n" \
"buffer_store_dwordx4 v[208:211], v20, s[56:59], s70 offen offset:2048 sc1\n" \
M_"v[92:95], a[156:159], a[220:223], v[92:95]\n" \
R_"a[156:159], v48 offset:15360\n" \
C_"v216, v216, v217\n" \
M_"v[88:91], a[140:143], a[220:223], v[44:47]\n" \
R_"a[140:143], v48 offset:11264\n" \
"v_mul_f32_e32 v218, v56, v218\n" \
M_"v[88:91], a[136:139], a[216:219], v[88:91]\n" \
R_"a[136:139], v48 offset:10240\n" \
"v_mul_f32_e32 v219, v56, v219\n" \
M_"v[88:91], a[132:135], a[212:215], v[88:91]\n" \
R_"a[132:135], v48 offset:9216\n" \
"v_mul_f32_e32 v57, v57, v61\n" \
M_"v[88:91], a[128:131], a[208:211], v[88:91]\n" \
R_"a[128:131], v48 offset:8192\n" \
M_"a[112:115], a[120:123], v[208:211], a[112:115]\n" \
C_"v217, v218, v219\n" \
"v_mul_f32_e32 v220, v56, v220\n" \
"v_mul_f32_e32 v221, v56, v221\n" \
"v_mul_f32_e32 v222, v56, v222\n" \
"v_mul_f32_e32 v223, v56, v223\n" \
"v_mul_f32_e32 v224, v57, v224\n" \
"v_mul_f32_e32 v225, v57, v225\n" \
C_"v218, v220, v221\n" \
C_"v219, v222, v223\n" \
"buffer_store_dwordx4 v[216:219], v20, s[56:59], s70 offen offset:3072 sc1\n" \
C_"v224, v224, v225\n" \
"v_mul_f32_e32 v226, v57, v226\n" \
"v_mul_f32_e32 v227, v57, v227\n" \
M_"a[112:115], a[120:123], v[216:219], a[112:115]\n" \
"v_mfma_f32_32x32x16_bf16 v[208:223], a[120:123], a[120:123], 0\n" \
C_"v225, v226, v227\n" \
"v_mul_f32_e32 v228, v57, v228\n" \
"v_mul_f32_e32 v229, v57, v229\n" \
"v_mul_f32_e32 v230, v57, v230\n" \
"v_mul_f32_e32 v231, v57, v231\n" \
"v_mul_f32_e32 v232, v57, v232\n" \
"v_mul_f32_e32 v233, v57, v233\n" \
C_"v226, v228, v229\n" \
C_"v227, v230, v231\n" \
"buffer_store_dwordx4 v[224:227], v20, s[56:59], s71 offen offset:0 sc1\n" \
C_"v232, v232, v233\n" \
"v_mul_f32_e32 v234, v57, v234\n" \
"v_mul_f32_e32 v235, v57, v235\n" \
M_"a[112:115], a[120:123], v[224:227], a[112:115]\n" \
C_"v233, v234, v235\n" \
"v_mul_f32_e32 v236, v57, v236\n" \
"v_mul_f32_e32 v237, v57, v237\n" \
"v_mul_f32_e32 v238, v57, v238\n" \
"v_mul_f32_e32 v239, v57, v239\n" \
"v_mul_f32_e32 v240, v57, v240\n" \
"v_mul_f32_e32 v241, v57, v241\n" \
C_"v234, v236, v237\n" \
C_"v235, v238, v239\n" \
"buffer_store_dwordx4 v[232:235], v20, s[56:59], s71 offen offset:1024 sc1\n" \
C_"v240, v240, v241\n" \
"v_mul_f32_e32 v242, v57, v242\n" \
"v_mul_f32_e32 v243, v57, v243\n" \
M_"a[112:115], a[120:123], v[232:235], a[112:115]\n" \
"v_mfma_f32_32x32x16_bf16 v[224:239], a[120:123], a[120:123], 0\n" \
C_"v241, v242, v243\n" \
"v_mul_f32_e32 v244, v57, v244\n" \
"v_mul_f32_e32 v245, v57, v245\n" \
"v_mul_f32_e32 v246, v57, v246\n" \
"v_mul_f32_e32 v247, v57, v247\n" \
"v_mul_f32_e32 v248, v57, v248\n" \
"v_mul_f32_e32 v249, v57, v249\n" \
C_"v242, v244, v245\n" \
C_"v243, v246, v247\n" \
"buffer_store_dwordx4 v[240:243], v20, s[56:59], s71 offen offset:2048 sc1\n" \
C_"v248, v248, v249\n" \
"v_mul_f32_e32 v250, v57, v250\n" \
"v_mul_f32_e32 v251, v57, v251\n" \
M_"a[112:115], a[120:123], v[240:243], a[112:115]\n" \
C_"v249, v250, v251\n" \
"v_mul_f32_e32 v252, v57, v252\n" \
"v_mul_f32_e32 v253, v57, v253\n" \
"v_mul_f32_e32 v254, v57, v254\n" \
"v_mul_f32_e32 v255, v57, v255\n" \
C_"v250, v252, v253\n" \
C_"v251, v254, v255\n" \
"buffer_store_dwordx4 v[248:251], v20, s[56:59], s71 offen offset:3072 sc1\n" \
B_"v53, s[8:11], s64 offen offset:3072 sc1 lds\n" \
"s_waitcnt vmcnt(16) lgkmcnt(0)\n" \
"s_barrier\n" \
M_"a[112:115], a[120:123], v[248:251], a[112:115]\n" \
"v_mfma_f32_32x32x16_bf16 v[240:255], a[120:123], a[120:123], 0\n" \
E_"v70, v70\n" \
E_"v71, v71\n" \
E_"v72, v72\n" \
E_"v73, v73\n" \
"s_add_u32 s64, s64, 0x10000\n" \
"v_cmp_lt_f32_e64 s[72:73], s21, v62\n" \
"v_readlane_b32 s95, v11, 3\n" \
"v_accvgpr_read_b32 v63, a112\n" \
"v_cmp_u_f32_e32 vcc, v63, v63\n" \
E_"v68, v68\n" \
E_"v69, v69\n" \
E_"v74, v74\n" \
E_"v75, v75\n" \
"s_or_b64 vcc, s[72:73], vcc\n" \
"s_cselect_b32 s95, 0x40000000, s95\n" \
"s_cselect_b32 s94, 0x7f800000, 0\n" \
C_"v71, v70, v71\n" \
E_"v66, v66\n" \
E_"v67, v67\n" \
E_"v64, v64\n" \
E_"v65, v65\n" \
C_"v72, v72, v73\n" \
E_"v76, v76\n" \
E_"v77, v77\n" \
E_"v78, v78\n" \
E_"v79, v79\n" \
C_"v70, v68, v69\n" \
C_"v73, v74, v75\n" \
"v_add_u32_e32 v49, 0x10000, v48\n" \
"s_xor_b32 s65, s65, 0x8000\n" \
C_"v69, v66, v67\n" \
C_"v68, v64, v65\n" \
C_"v74, v76, v77\n" \
C_"v75, v78, v79\n" \
"v_xor_b32_e32 v48, 0x8000, v48\n" \
"s_sub_u32 s66, s19, 2\n" \
"s_branch L_2_%=\n" \
"L_f1_%=:\n" \
R_"a[160:163], v49 offset:0\n" \
R_"a[164:167], v49 offset:1024\n" \
R_"a[168:171], v49 offset:2048\n" \
M_"v[104:107], a[128:131], a[80:83], v[36:39]\n" \
C_"v192, v192, v193\n" \
C_"v224, v224, v225\n" \
C_"v193, v194, v195\n" \
C_"v225, v226, v227\n" \
R_"a[172:175], v49 offset:3072\n" \
C_"v194, v196, v197\n" \
C_"v195, v198, v199\n" \
C_"v226, v228, v229\n" \
C_"v227, v230, v231\n" \
M_"v[104:107], a[132:135], a[84:87], v[104:107]\n" \
"v_lshl_add_u32 v50, s15, 2, v20\n" \
C_"v196, v200, v201\n" \
C_"v197, v202, v203\n" \
C_"v198, v204, v205\n" \
C_"v199, v206, v207\n" \
C_"v228, v232, v233\n" \
C_"v229, v234, v235\n" \
C_"v230, v236, v237\n" \
C_"v231, v238, v239\n" \
"v_add_u32_e32 v50, 0x1a000, v50\n" \
"v_mov_b32_e32 v61, s62\n" \
C_"v200, v208, v209\n" \
C_"v201, v210, v211\n" \
C_"v202, v212, v213\n" \
C_"v203, v214, v215\n" \
C_"v204, v216, v217\n" \
C_"v205, v218, v219\n" \
C_"v206, v220, v221\n" \
C_"v207, v222, v223\n" \
C_"v232, v240, v241\n" \
C_"v233, v242, v243\n" \
C_"v234, v244, v245\n" \
C_"v235, v246, v247\n" \
C_"v236, v248, v249\n" \
C_"v237, v250, v251\n" \
C_"v238, v252, v253\n" \
C_"v239, v254, v255\n" \
"ds_write_b128 v50, v[192:195] offset:0\n" \
M_"v[104:107], a[136:139], a[88:91], v[104:107]\n" \
R_"a[176:179], v49 offset:4096\n" \
"v_add_u32_e32 v56, 0x49000, v20\n" \
M_"v[104:107], a[140:143], a[92:95], v[104:107]\n" \
"ds_write_b128 v50, v[196:199] offset:1024\n" \
M_"v[108:111], a[156:159], a[92:95], v[36:39]\n" \
R_"a[180:183], v49 offset:5120\n" \
R_"a[184:187], v49 offset:6144\n" \
M_"v[108:111], a[152:155], a[88:91], v[108:111]\n" \
"ds_write_b128 v50, v[200:203] offset:2048\n" \
M_"v[108:111], a[148:151], a[84:87], v[108:111]\n" \
"v_and_b32_e32 v68, v12, v68\n" \
"v_and_b32_e32 v69, v13, v69\n" \
M_"v[108:111], a[144:147], a[80:83], v[108:111]\n" \
"ds_write_b128 v50, v[204:207] offset:3072\n" \
"v_and_b32_e32 v70, v14, v70\n" \
"v_and_b32_e32 v71, v15, v71\n" \
R_"a[188:191], v49 offset:7168\n" \
R_"a[0:3], v20 offset:8192\n" \
"s_waitcnt lgkmcnt(9)\n" \
M_"v[128:131], a[160:163], v[68:71], v[128:131]\n" \
"ds_write_b128 v50, v[224:227] offset:4096\n" \
M_"v[160:163], a[160:163], v[72:75], v[160:163]\n" \
R_"a[4:7], v20 offset:9216\n" \
R_"a[8:11], v20 offset:10240\n" \
M_"v[132:135], a[164:167], v[68:71], v[132:135]\n" \
"ds_write_b128 v50, v[228:231] offset:5120\n" \
M_"v[164:167], a[164:167], v[72:75], v[164:167]\n" \
R_"a[12:15], v20 offset:11264\n" \
R_"a[28:31], v20 offset:15360\n" \
M_"v[136:139], a[168:171], v[68:71], v[136:139]\n" \
"ds_write_b128 v50, v[232:235] offset:6144\n" \
M_"v[168:171], a[168:171], v[72:75], v[168:171]\n" \
R_"a[24:27], v20 offset:14336\n" \
R_"a[20:23], v20 offset:13312\n" \
M_"v[140:143], a[172:175], v[68:71], v[140:143]\n" \
"ds_write_b128 v50, v[236:239] offset:7168\n" \
M_"v[172:175], a[172:175], v[72:75], v[172:175]\n" \
R_"a[16:19], v20 offset:12288\n" \
"s_waitcnt lgkmcnt(15)\n" \
M_"v[144:147], a[176:179], v[68:71], v[144:147]\n" \
"v_xor_b32_e32 v48, 0x4000, v49\n" \
"ds_read_b32 v58, v18 offset:256\n" \
M_"v[176:179], a[176:179], v[72:75], v[176:179]\n" \
"ds_read_b32 v59, v18 offset:384\n" \
R_"a[32:35], v48 offset:8192\n" \
M_"v[148:151], a[180:183], v[68:71], v[148:151]\n" \
R_"a[36:39], v48 offset:9216\n" \
R_"a[40:43], v48 offset:10240\n" \
M_"v[180:183], a[180:183], v[72:75], v[180:183]\n" \
R_"a[44:47], v48 offset:11264\n" \
R_"a[48:51], v48 offset:12288\n" \
M_"v[152:155], a[184:187], v[68:71], v[152:155]\n" \
R_"a[52:55], v48 offset:13312\n" \
R_"a[56:59], v48 offset:14336\n" \
M_"v[184:187], a[184:187], v[72:75], v[184:187]\n" \
R_"a[60:63], v48 offset:15360\n" \
"s_waitcnt lgkmcnt(15)\n" \
M_"v[156:159], a[188:191], v[68:71], v[156:159]\n" \
R_"a[128:131], v49 offset:8192\n" \
R_"a[132:135], v49 offset:9216\n" \
M_"v[188:191], a[188:191], v[72:75], v[188:191]\n" \
R_"a[136:139], v49 offset:10240\n" \
R_"a[140:143], v49 offset:11264\n" \
"v_smfmac_f32_16x16x64_bf16 a[96:99], a[104:107], v[68:75], v21\n" \
R_"a[144:147], v49 offset:12288\n" \
R_"a[148:151], v49 offset:13312\n" \
M_"v[112:115], a[0:3], a[64:67], v[32:35]\n" \
R_"a[152:155], v49 offset:14336\n" \
R_"a[156:159], v49 offset:15360\n" \
M_"v[112:115], a[4:7], a[68:71], v[112:115]\n" \
"v_accvgpr_read_b32 v40, a100\n" \
"v_accvgpr_read_b32 v24, a101\n" \
M_"v[112:115], a[8:11], a[72:75], v[112:115]\n" \
"v_accvgpr_read_b32 v41, a102\n" \
"v_accvgpr_read_b32 v25, a103\n" \
M_"v[112:115], a[12:15], a[76:79], v[112:115]\n" \
"v_add_f32_e32 v40, v40, v24\n" \
"v_add_f32_e32 v41, v41, v25\n" \
M_"v[116:119], a[28:31], a[76:79], v[32:35]\n" \
"v_add_f32_e32 v40, s94, v40\n" \
"s_waitcnt lgkmcnt(15)\n" \
M_"v[116:119], a[24:27], a[72:75], v[116:119]\n" \
"v_add_f32_e32 v41, s94, v41\n" \
"ds_write_b64 v19, v[40:41]\n" \
M_"v[116:119], a[20:23], a[68:71], v[116:119]\n" \
"v_add_u32_e32 v52, s15, v20\n" \
"v_add_u32_e32 v51, 0x10000, v52\n" \
M_"v[116:119], a[16:19], a[64:67], v[116:119]\n" \
E_"v104, v104\n" \
M_"v[124:127], a[16:19], a[80:83], v[36:39]\n" \
E_"v105, v105\n" \
M_"v[124:127], a[20:23], a[84:87], v[124:127]\n" \
E_"v106, v106\n" \
M_"v[124:127], a[24:27], a[88:91], v[124:127]\n" \
E_"v107, v107\n" \
M_"v[124:127], a[28:31], a[92:95], v[124:127]\n" \
C_"v104, v104, v105\n" \
C_"v105, v106, v107\n" \
M_"v[120:123], a[12:15], a[92:95], v[36:39]\n" \
E_"v108, v108\n" \
M_"v[120:123], a[8:11], a[88:91], v[120:123]\n" \
E_"v109, v109\n" \
M_"v[120:123], a[4:7], a[84:87], v[120:123]\n" \
E_"v110, v110\n" \
M_"v[120:123], a[0:3], a[80:83], v[120:123]\n" \
E_"v111, v111\n" \
"s_waitcnt lgkmcnt(0)\n" \
"s_barrier\n" \
R_"v[208:211], v52 offset:0\n" \
R_"v[212:215], v52 offset:1024\n" \
R_"v[216:219], v52 offset:2048\n" \
R_"v[220:223], v52 offset:3072\n" \
R_"v[240:243], v51 offset:32768\n" \
R_"v[244:247], v51 offset:33792\n" \
R_"v[248:251], v51 offset:34816\n" \
R_"v[252:255], v51 offset:35840\n" \
C_"v106, v108, v109\n" \
C_"v107, v110, v111\n" \
"v_and_b32_e32 v104, v12, v104\n" \
"v_and_b32_e32 v105, v13, v105\n" \
"v_and_b32_e32 v106, v14, v106\n" \
"s_waitcnt vmcnt(0)\n" \
"s_mov_b64 exec, 1\n" \
"buffer_store_dword v61, off, s[56:59], s95 offset:0 sc1\n" \
"s_mov_b64 exec, -1\n" \
"v_and_b32_e32 v107, v15, v107\n" \
"v_xor_b32_e32 v63, 0x400, v19\n" \
"ds_read_b64 v[42:43], v63\n" \
M_"a[96:99], a[116:119], v[104:107], a[96:99]\n" \
"v_mov_b32_e32 v194, v28\n" \
"v_mov_b32_e32 v195, v29\n" \
M_"v[160:163], a[128:131], v[104:107], v[160:163]\n" \
"v_mov_b32_e32 v196, 0\n" \
"v_mov_b32_e32 v197, 0\n" \
M_"v[164:167], a[132:135], v[104:107], v[164:167]\n" \
"v_mov_b32_e32 v192, 0\n" \
"v_mov_b32_e32 v193, 0\n" \
M_"v[168:171], a[136:139], v[104:107], v[168:171]\n" \
"v_readlane_b32 s93, v11, 25\n" \
"v_readlane_b32 s92, v11, 30\n" \
M_"v[172:175], a[140:143], v[104:107], v[172:175]\n" \
B_"a[192:195], v20, s[56:59], s60 offen offset:0 sc1\n" \
M_"v[176:179], a[144:147], v[104:107], v[176:179]\n" \
E_"v118, v118\n" \
M_"v[180:183], a[148:151], v[104:107], v[180:183]\n" \
E_"v119, v119\n" \
M_"v[184:187], a[152:155], v[104:107], v[184:187]\n" \
"s_add_u32 m0, s93, 0x0\n" \
C_"v119, v118, v119\n" \
M_"v[188:191], a[156:159], v[104:107], v[188:191]\n" \
E_"v120, v120\n" \
"s_waitcnt lgkmcnt(1)\n" \
"s_barrier\n" \
B_"a[208:211], v56, s[56:59], s60 offen offset:0 sc1\n" \
M_"v[128:131], v[194:197], v[208:211], v[128:131]\n" \
E_"v121, v121\n" \
M_"v[132:135], v[192:195], v[208:211], v[132:135]\n" \
E_"v116, v116\n" \
M_"v[136:139], v[194:197], v[212:215], v[136:139]\n" \
E_"v117, v117\n" \
M_"v[140:143], v[192:195], v[212:215], v[140:143]\n" \
E_"v122, v122\n" \
M_"v[144:147], v[194:197], v[216:219], v[144:147]\n" \
E_"v123, v123\n" \
M_"v[148:151], v[192:195], v[216:219], v[148:151]\n" \
B_"a[196:199], v20, s[56:59], s60 offen offset:1024 sc1\n" \
M_"v[152:155], v[194:197], v[220:223], v[152:155]\n" \
E_"v114, v114\n" \
M_"v[156:159], v[192:195], v[220:223], v[156:159]\n" \
E_"v115, v115\n" \
M_"v[160:163], v[194:197], v[240:243], v[160:163]\n" \
E_"v112, v112\n" \
M_"v[164:167], v[192:195], v[240:243], v[164:167]\n" \
E_"v113, v113\n" \
M_"v[168:171], v[194:197], v[244:247], v[168:171]\n" \
C_"v120, v120, v121\n" \
C_"v118, v116, v117\n" \
M_"v[172:175], v[192:195], v[244:247], v[172:175]\n" \
B_"a[212:215], v56, s[56:59], s60 offen offset:1024 sc1\n" \
M_"v[176:179], v[194:197], v[248:251], v[176:179]\n" \
E_"v124, v124\n" \
M_"v[180:183], v[192:195], v[248:251], v[180:183]\n" \
E_"v125, v125\n" \
M_"v[184:187], v[194:197], v[252:255], v[184:187]\n" \
E_"v126, v126\n" \
M_"v[188:191], v[192:195], v[252:255], v[188:191]\n" \
E_"v127, v127\n" \
C_"v121, v122, v123\n" \
C_"v117, v114, v115\n" \
C_"v116, v112, v113\n" \
C_"v122, v124, v125\n" \
C_"v123, v126, v127\n" \
B_"a[200:203], v20, s[56:59], s60 offen offset:2048 sc1\n" \
"s_nop 0\n" \
"v_smfmac_f32_16x16x64_bf16 a[96:99], a[104:107], v[116:123], v21\n" \
M_"v[128:131], a[32:35], v[116:119], v[128:131]\n" \
M_"v[160:163], a[32:35], v[120:123], v[160:163]\n" \
M_"v[132:135], a[36:39], v[116:119], v[132:135]\n" \
M_"v[164:167], a[36:39], v[120:123], v[164:167]\n" \
M_"v[136:139], a[40:43], v[116:119], v[136:139]\n" \
B_"a[216:219], v56, s[56:59], s60 offen offset:2048 sc1\n" \
M_"v[168:171], a[40:43], v[120:123], v[168:171]\n" \
M_"v[140:143], a[44:47], v[116:119], v[140:143]\n" \
"v_accvgpr_read_b32 v54, a96\n" \
"v_accvgpr_read_b32 v22, a97\n" \
M_"v[172:175], a[44:47], v[120:123], v[172:175]\n" \
"v_add_f32_e32 v54, v54, v22\n" \
"s_waitcnt lgkmcnt(0)\n" \
"v_add_f32_e32 v54, v54, v42\n" \
M_"v[144:147], a[48:51], v[116:119], v[144:147]\n" \
"v_max_f32_e32 v62, v62, v54\n" \
"v_accvgpr_read_b32 v55, a98\n" \
M_"v[176:179], a[48:51], v[120:123], v[176:179]\n" \
"v_rcp_f32_e32 v54, v54\n" \
B_"a[204:207], v20, s[56:59], s60 offen offset:3072 sc1\n" \
M_"v[148:151], a[52:55], v[116:119], v[148:151]\n" \
"v_mul_f32_e32 v54, v54, v58\n" \
"v_mul_f32_e32 v128, v54, v128\n" \
M_"v[180:183], a[52:55], v[120:123], v[180:183]\n" \
"v_mul_f32_e32 v129, v54, v129\n" \
"v_mul_f32_e32 v130, v54, v130\n" \
"v_mul_f32_e32 v131, v54, v131\n" \
"v_mul_f32_e32 v132, v54, v132\n" \
"v_mul_f32_e32 v133, v54, v133\n" \
"v_mul_f32_e32 v134, v54, v134\n" \
"v_mul_f32_e32 v135, v54, v135\n" \
C_"v64, v128, v129\n" \
C_"v65, v130, v131\n" \
C_"v66, v132, v133\n" \
C_"v67, v134, v135\n" \
"v_accvgpr_read_b32 v23, a99\n" \
M_"v[152:155], a[56:59], v[116:119], v[152:155]\n" \
M_"a[112:115], a[120:123], v[64:67], a[112:115]\n" \
B_"a[220:223], v56, s[56:59], s60 offen offset:3072 sc1\n" \
"v_mul_f32_e32 v136, v54, v136\n" \
"v_mul_f32_e32 v137, v54, v137\n" \
"v_mul_f32_e32 v138, v54, v138\n" \
"v_mul_f32_e32 v139, v54, v139\n" \
"v_mul_f32_e32 v140, v54, v140\n" \
"v_mul_f32_e32 v141, v54, v141\n" \
"v_mul_f32_e32 v142, v54, v142\n" \
"v_mul_f32_e32 v143, v54, v143\n" \
"v_add_f32_e32 v55, v55, v23\n" \
C_"v68, v136, v137\n" \
C_"v69, v138, v139\n" \
C_"v70, v140, v141\n" \
C_"v71, v142, v143\n" \
"v_add_f32_e32 v55, v55, v43\n" \
"v_mul_f32_e32 v144, v54, v144\n" \
M_"a[112:115], a[120:123], v[68:71], a[112:115]\n" \
"v_mul_f32_e32 v145, v54, v145\n" \
"v_mul_f32_e32 v146, v54, v146\n" \
"v_mul_f32_e32 v147, v54, v147\n" \
"v_mul_f32_e32 v148, v54, v148\n" \
B_"v0, s[80:83], s92 offen offset:0 lds\n" \
"v_mul_f32_e32 v149, v54, v149\n" \
"v_mul_f32_e32 v150, v54, v150\n" \
"v_mul_f32_e32 v151, v54, v151\n" \
"v_max_f32_e32 v62, v62, v55\n" \
C_"v72, v144, v145\n" \
C_"v73, v146, v147\n" \
C_"v74, v148, v149\n" \
C_"v75, v150, v151\n" \
"v_rcp_f32_e32 v55, v55\n" \
M_"v[184:187], a[56:59], v[120:123], v[184:187]\n" \
M_"a[112:115], a[120:123], v[72:75], a[112:115]\n" \
"v_mul_f32_e32 v152, v54, v152\n" \
"v_mul_f32_e32 v153, v54, v153\n" \
"v_mul_f32_e32 v154, v54, v154\n" \
"v_mul_f32_e32 v155, v54, v155\n" \
C_"v76, v152, v153\n" \
B_"v1, s[80:83], s92 offen offset:1024 lds\n" \
C_"v77, v154, v155\n" \
"v_mul_f32_e32 v55, v55, v59\n" \
"v_mul_f32_e32 v160, v55, v160\n" \
"v_mul_f32_e32 v161, v55, v161\n" \
"v_mul_f32_e32 v162, v55, v162\n" \
"v_mul_f32_e32 v163, v55, v163\n" \
"v_mul_f32_e32 v164, v55, v164\n" \
"v_mul_f32_e32 v165, v55, v165\n" \
"v_mul_f32_e32 v166, v55, v166\n" \
"v_mul_f32_e32 v167, v55, v167\n" \
C_"v80, v160, v161\n" \
C_"v81, v162, v163\n" \
C_"v82, v164, v165\n" \
C_"v83, v166, v167\n" \
M_"v[156:159], a[60:63], v[116:119], v[156:159]\n" \
"v_mul_f32_e32 v168, v55, v168\n" \
"v_mul_f32_e32 v169, v55, v169\n" \
"v_mul_f32_e32 v170, v55, v170\n" \
"v_mul_f32_e32 v171, v55, v171\n" \
"v_mul_f32_e32 v172, v55, v172\n" \
B_"v2, s[80:83], s92 offen offset:2048 lds\n" \
"v_mul_f32_e32 v173, v55, v173\n" \
"v_mul_f32_e32 v174, v55, v174\n" \
"v_mul_f32_e32 v156, v54, v156\n" \
"v_mul_f32_e32 v157, v54, v157\n" \
"v_mul_f32_e32 v158, v54, v158\n" \
"v_mul_f32_e32 v159, v54, v159\n" \
C_"v78, v156, v157\n" \
C_"v79, v158, v159\n" \
"v_mul_f32_e32 v175, v55, v175\n" \
C_"v84, v168, v169\n" \
M_"a[112:115], a[120:123], v[76:79], a[112:115]\n" \
M_"a[112:115], a[120:123], v[80:83], a[112:115]\n" \
C_"v85, v170, v171\n" \
C_"v86, v172, v173\n" \
C_"v87, v174, v175\n" \
"v_mul_f32_e32 v176, v55, v176\n" \
"v_mul_f32_e32 v177, v55, v177\n" \
B_"v3, s[80:83], s92 offen offset:3072 lds\n" \
"s_add_u32 m0, s93, 0x1000\n" \
M_"a[112:115], a[120:123], v[84:87], a[112:115]\n" \
"v_mul_f32_e32 v178, v55, v178\n" \
"v_mul_f32_e32 v179, v55, v179\n" \
"v_mul_f32_e32 v180, v55, v180\n" \
"v_mul_f32_e32 v181, v55, v181\n" \
"v_mul_f32_e32 v182, v55, v182\n" \
"v_mul_f32_e32 v183, v55, v183\n" \
C_"v96, v176, v177\n" \
C_"v97, v178, v179\n" \
C_"v98, v180, v181\n" \
C_"v99, v182, v183\n" \
M_"v[188:191], a[60:63], v[120:123], v[188:191]\n" \
"v_mul_f32_e32 v184, v55, v184\n" \
M_"a[112:115], a[120:123], v[96:99], a[112:115]\n" \
"v_mul_f32_e32 v185, v55, v185\n" \
"v_mul_f32_e32 v186, v55, v186\n" \
B_"v4, s[80:83], s92 offen offset:0 lds\n" \
"v_mul_f32_e32 v187, v55, v187\n" \
C_"v100, v184, v185\n" \
C_"v101, v186, v187\n" \
"v_mul_f32_e32 v188, v55, v188\n" \
"v_mul_f32_e32 v189, v55, v189\n" \
"v_mul_f32_e32 v190, v55, v190\n" \
"v_mul_f32_e32 v191, v55, v191\n" \
C_"v102, v188, v189\n" \
C_"v103, v190, v191\n" \
"v_cmp_lt_f32_e64 s[72:73], s21, v62\n" \
B_"v5, s[80:83], s92 offen offset:1024 lds\n" \
M_"a[112:115], a[120:123], v[100:103], a[112:115]\n" \
B_"v6, s[80:83], s92 offen offset:2048 lds\n" \
B_"v7, s[80:83], s92 offen offset:3072 lds\n" \
"s_add_u32 m0, s93, 0x2000\n" \
"s_nop 0\n" \
B_"v8, s[80:83], s92 offen offset:0 lds\n" \
"s_nop 2\n" \
"v_accvgpr_read_b32 v63, a112\n" \
"v_cmp_u_f32_e32 vcc, v63, v63\n" \
"s_nop 0\n" \
"s_or_b64 vcc, s[72:73], vcc\n" \
"s_cbranch_scc1 L_end_%=\n" \
"s_branch L_out_%=\n" \
".fill (L_ab + 24576 - .) / 4, 4, 0xBF800000\n" \
".fill (L_ab + 24576 - .) / 4, 4, 0xBF800000\n" \
"L_ap6:\n" \
"s_setpc_b64 s[88:89]\n" \
"L_gs7:\n" \
M_"v[104:107], a[128:131], a[16:19], v[36:39]\n" \
"s_add_u32 s68, s60, 0xfff6e000\n" \
"s_add_u32 s69, s60, 0xfffb7000\n" \
"s_add_u32 s70, s60, 0x0\n" \
"s_add_u32 s71, s60, 0x49000\n" \
M_"v[104:107], a[132:135], a[20:23], v[104:107]\n" \
"s_xor_b32 m0, s65, 0x12000\n" \
R_"a[160:163], v49 offset:0\n" \
M_"v[104:107], a[136:139], a[24:27], v[104:107]\n" \
B_"v50, s[8:11], s64 offen offset:0 sc1 lds\n" \
M_"v[104:107], a[140:143], a[28:31], v[104:107]\n" \
R_"a[164:167], v49 offset:1024\n" \
R_"a[168:171], v49 offset:2048\n" \
M_"v[108:111], a[156:159], a[28:31], v[36:39]\n" \
R_"a[172:175], v49 offset:3072\n" \
R_"a[176:179], v49 offset:4096\n" \
M_"v[108:111], a[152:155], a[24:27], v[108:111]\n" \
B_"v50, s[8:11], s64 offen offset:1024 sc1 lds\n" \
M_"v[108:111], a[148:151], a[20:23], v[108:111]\n" \
"s_mov_b32 s65, s13\n" \
"v_add_u32_e32 v63, s15, v20\n" \
M_"v[108:111], a[144:147], a[16:19], v[108:111]\n" \
"s_add_u32 s67, s14, 40960\n" \
"v_add_u32_e32 v50, 0x6000, v63\n" \
M_"v[116:119], a[144:147], a[32:35], v[40:43]\n" \
"s_xor_b32 m0, s65, 0x8000\n" \
R_"a[180:183], v49 offset:5120\n" \
M_"v[116:119], a[148:151], a[36:39], v[116:119]\n" \
R_"a[184:187], v49 offset:6144\n" \
"v_and_b32_e32 v68, v12, v68\n" \
M_"v[116:119], a[152:155], a[40:43], v[116:119]\n" \
"v_and_b32_e32 v69, v13, v69\n" \
"v_and_b32_e32 v70, v14, v70\n" \
M_"v[116:119], a[156:159], a[44:47], v[116:119]\n" \
B_"v50, s[8:11], s67 offen offset:0 sc1 lds\n" \
M_"v[112:115], a[140:143], a[44:47], v[40:43]\n" \
"v_and_b32_e32 v71, v15, v71\n" \
R_"a[188:191], v49 offset:7168\n" \
M_"v[112:115], a[136:139], a[40:43], v[112:115]\n" \
E_"v86, v86\n" \
M_"v[112:115], a[132:135], a[36:39], v[112:115]\n" \
E_"v87, v87\n" \
M_"v[112:115], a[128:131], a[32:35], v[112:115]\n" \
E_"v84, v84\n" \
M_"v[120:123], a[128:131], a[48:51], v[44:47]\n" \
B_"v50, s[8:11], s67 offen offset:1024 sc1 lds\n" \
M_"v[120:123], a[132:135], a[52:55], v[120:123]\n" \
E_"v85, v85\n" \
M_"v[120:123], a[136:139], a[56:59], v[120:123]\n" \
C_"v87, v86, v87\n" \
C_"v86, v84, v85\n" \
M_"v[120:123], a[140:143], a[60:63], v[120:123]\n" \
E_"v82, v82\n" \
M_"v[124:127], a[156:159], a[60:63], v[44:47]\n" \
E_"v83, v83\n" \
M_"v[124:127], a[152:155], a[56:59], v[124:127]\n" \
E_"v80, v80\n" \
M_"v[124:127], a[148:151], a[52:55], v[124:127]\n" \
B_"v50, s[8:11], s67 offen offset:2048 sc1 lds\n" \
M_"v[124:127], a[144:147], a[48:51], v[124:127]\n" \
C_"v85, v82, v83\n" \
"s_waitcnt lgkmcnt(1)\n" \
M_"v[128:131], a[160:163], v[68:71], v[128:131]\n" \
E_"v81, v81\n" \
M_"v[160:163], a[160:163], v[72:75], v[160:163]\n" \
C_"v84, v80, v81\n" \
"s_mov_b32 s64, s14\n" \
M_"v[132:135], a[164:167], v[68:71], v[132:135]\n" \
E_"v88, v88\n" \
M_"v[164:167], a[164:167], v[72:75], v[164:167]\n" \
E_"v89, v89\n" \
M_"v[136:139], a[168:171], v[68:71], v[136:139]\n" \
B_"v50, s[8:11], s67 offen offset:3072 sc1 lds\n" \
M_"v[168:171], a[168:171], v[72:75], v[168:171]\n" \
E_"v90, v90\n" \
M_"v[140:143], a[172:175], v[68:71], v[140:143]\n" \
E_"v91, v91\n" \
M_"v[172:175], a[172:175], v[72:75], v[172:175]\n" \
C_"v88, v88, v89\n" \
C_"v89, v90, v91\n" \
M_"v[144:147], a[176:179], v[68:71], v[144:147]\n" \
E_"v92, v92\n" \
M_"v[176:179], a[176:179], v[72:75], v[176:179]\n" \
E_"v93, v93\n" \
M_"v[148:151], a[180:183], v[68:71], v[148:151]\n" \
E_"v94, v94\n" \
M_"v[180:183], a[180:183], v[72:75], v[180:183]\n" \
E_"v95, v95\n" \
M_"v[152:155], a[184:187], v[68:71], v[152:155]\n" \
C_"v90, v92, v93\n" \
C_"v91, v94, v95\n" \
M_"v[184:187], a[184:187], v[72:75], v[184:187]\n" \
"v_add_u32_e32 v51, 0x12000, v63\n" \
"s_waitcnt lgkmcnt(0)\n" \
M_"v[156:159], a[188:191], v[68:71], v[156:159]\n" \
"s_xor_b32 m0, s65, 0xa000\n" \
"ds_read_b32 v58, v18 offset:512\n" \
M_"v[188:191], a[188:191], v[72:75], v[188:191]\n" \
B_"v51, s[8:11], s64 offen offset:0 sc1 lds\n" \
"v_smfmac_f32_16x16x64_bf16 a[96:99], a[104:107], v[68:75], v21\n" \
"ds_read_b32 v59, v18 offset:640\n" \
"ds_read_b32 v60, v18 offset:768\n" \
M_"v[220:223], a[188:191], v[84:87], v[220:223]\n" \
B_"v51, s[8:11], s64 offen offset:1024 sc1 lds\n" \
M_"v[252:255], a[188:191], v[88:91], v[252:255]\n" \
"ds_read_b32 v61, v18 offset:896\n" \
R_"a[128:131], v48 offset:0\n" \
M_"v[216:219], a[184:187], v[84:87], v[216:219]\n" \
R_"a[144:147], v48 offset:4096\n" \
R_"a[132:135], v48 offset:1024\n" \
M_"v[248:251], a[184:187], v[88:91], v[248:251]\n" \
R_"a[148:151], v48 offset:5120\n" \
R_"a[136:139], v48 offset:2048\n" \
M_"v[212:215], a[180:183], v[84:87], v[212:215]\n" \
R_"a[152:155], v48 offset:6144\n" \
R_"a[140:143], v48 offset:3072\n" \
M_"v[244:247], a[180:183], v[88:91], v[244:247]\n" \
R_"a[156:159], v48 offset:7168\n" \
"v_add_u32_e32 v52, 0x14000, v63\n" \
M_"v[208:211], a[176:179], v[84:87], v[208:211]\n" \
B_"v51, s[8:11], s64 offen offset:2048 sc1 lds\n" \
M_"v[240:243], a[176:179], v[88:91], v[240:243]\n" \
E_"v104, v104\n" \
M_"v[204:207], a[172:175], v[84:87], v[204:207]\n" \
E_"v105, v105\n" \
M_"v[236:239], a[172:175], v[88:91], v[236:239]\n" \
B_"v51, s[8:11], s64 offen offset:3072 sc1 lds\n" \
M_"v[200:203], a[168:171], v[84:87], v[200:203]\n" \
E_"v106, v106\n" \
M_"v[232:235], a[168:171], v[88:91], v[232:235]\n" \
E_"v107, v107\n" \
M_"v[196:199], a[164:167], v[84:87], v[196:199]\n" \
C_"v104, v104, v105\n" \
C_"v105, v106, v107\n" \
M_"v[228:231], a[164:167], v[88:91], v[228:231]\n" \
E_"v108, v108\n" \
M_"v[192:195], a[160:163], v[84:87], v[192:195]\n" \
E_"v109, v109\n" \
M_"v[224:227], a[160:163], v[88:91], v[224:227]\n" \
E_"v110, v110\n" \
"v_smfmac_f32_16x16x64_bf16 a[100:103], a[104:107], v[84:91], v21\n" \
E_"v111, v111\n" \
C_"v106, v108, v109\n" \
C_"v107, v110, v111\n" \
"v_and_b32_e32 v104, v12, v104\n" \
"v_and_b32_e32 v105, v13, v105\n" \
"v_and_b32_e32 v106, v14, v106\n" \
"v_and_b32_e32 v107, v15, v107\n" \
"v_accvgpr_read_b32 v54, a96\n" \
"v_accvgpr_read_b32 v22, a97\n" \
"v_readlane_b32 s93, v11, 22\n" \
"v_readlane_b32 s80, v11, 11\n" \
"v_readlane_b32 s81, v11, 12\n" \
"s_mov_b32 s82, 0x47fca0\n" \
"s_mov_b32 s83, 0x20000\n" \
"v_readlane_b32 s92, v11, 29\n" \
E_"v118, v118\n" \
E_"v119, v119\n" \
E_"v116, v116\n" \
E_"v117, v117\n" \
C_"v119, v118, v119\n" \
E_"v114, v114\n" \
E_"v115, v115\n" \
E_"v112, v112\n" \
C_"v118, v116, v117\n" \
"s_waitcnt vmcnt(10) lgkmcnt(0)\n" \
"s_barrier\n" \
M_"v[80:83], a[128:131], a[32:35], v[40:43]\n" \
R_"a[160:163], v49 offset:8192\n" \
R_"a[164:167], v49 offset:9216\n" \
M_"v[80:83], a[132:135], a[36:39], v[80:83]\n" \
R_"a[168:171], v49 offset:10240\n" \
"s_xor_b32 m0, s65, 0x18000\n" \
M_"v[80:83], a[136:139], a[40:43], v[80:83]\n" \
B_"v52, s[8:11], s64 offen offset:0 sc1 lds\n" \
M_"v[80:83], a[140:143], a[44:47], v[80:83]\n" \
R_"a[172:175], v49 offset:11264\n" \
R_"a[176:179], v49 offset:12288\n" \
M_"v[84:87], a[156:159], a[44:47], v[40:43]\n" \
R_"a[180:183], v49 offset:13312\n" \
R_"a[184:187], v49 offset:14336\n" \
M_"v[84:87], a[152:155], a[40:43], v[84:87]\n" \
B_"v52, s[8:11], s64 offen offset:1024 sc1 lds\n" \
M_"v[84:87], a[148:151], a[36:39], v[84:87]\n" \
R_"a[188:191], v49 offset:15360\n" \
C_"v117, v114, v115\n" \
M_"v[84:87], a[144:147], a[32:35], v[84:87]\n" \
B_"v52, s[8:11], s64 offen offset:2048 sc1 lds\n" \
M_"v[92:95], a[144:147], a[48:51], v[44:47]\n" \
E_"v113, v113\n" \
M_"v[92:95], a[148:151], a[52:55], v[92:95]\n" \
B_"v52, s[8:11], s64 offen offset:3072 sc1 lds\n" \
M_"v[92:95], a[152:155], a[56:59], v[92:95]\n" \
"s_add_u32 m0, s93, 0x0\n" \
C_"v116, v112, v113\n" \
M_"v[92:95], a[156:159], a[60:63], v[92:95]\n" \
E_"v120, v120\n" \
M_"v[88:91], a[140:143], a[60:63], v[44:47]\n" \
B_"v0, s[80:83], s92 offen offset:0 lds\n" \
M_"v[88:91], a[136:139], a[56:59], v[88:91]\n" \
E_"v121, v121\n" \
M_"v[88:91], a[132:135], a[52:55], v[88:91]\n" \
E_"v122, v122\n" \
M_"v[88:91], a[128:131], a[48:51], v[88:91]\n" \
B_"v1, s[80:83], s92 offen offset:1024 lds\n" \
M_"a[96:99], a[116:119], v[104:107], a[96:99]\n" \
C_"v120, v120, v121\n" \
"s_waitcnt lgkmcnt(5)\n" \
M_"v[160:163], a[160:163], v[104:107], v[160:163]\n" \
E_"v123, v123\n" \
M_"v[164:167], a[164:167], v[104:107], v[164:167]\n" \
B_"v2, s[80:83], s92 offen offset:2048 lds\n" \
M_"v[168:171], a[168:171], v[104:107], v[168:171]\n" \
C_"v121, v122, v123\n" \
"s_waitcnt lgkmcnt(1)\n" \
M_"v[172:175], a[172:175], v[104:107], v[172:175]\n" \
E_"v124, v124\n" \
M_"v[176:179], a[176:179], v[104:107], v[176:179]\n" \
E_"v125, v125\n" \
M_"v[180:183], a[180:183], v[104:107], v[180:183]\n" \
E_"v126, v126\n" \
M_"v[184:187], a[184:187], v[104:107], v[184:187]\n" \
C_"v122, v124, v125\n" \
"s_waitcnt lgkmcnt(0)\n" \
M_"v[188:191], a[188:191], v[104:107], v[188:191]\n" \
E_"v127, v127\n" \
M_"v[220:223], a[188:191], v[116:119], v[220:223]\n" \
C_"v123, v126, v127\n" \
B_"v3, s[80:83], s92 offen offset:3072 lds\n" \
"s_add_u32 m0, s93, 0x1000\n" \
M_"v[252:255], a[188:191], v[120:123], v[252:255]\n" \
R_"a[128:131], v48 offset:8192\n" \
R_"a[144:147], v48 offset:12288\n" \
M_"v[216:219], a[184:187], v[116:119], v[216:219]\n" \
B_"v4, s[80:83], s92 offen offset:0 lds\n" \
M_"v[248:251], a[184:187], v[120:123], v[248:251]\n" \
R_"a[132:135], v48 offset:9216\n" \
R_"a[148:151], v48 offset:13312\n" \
M_"v[212:215], a[180:183], v[116:119], v[212:215]\n" \
B_"v5, s[80:83], s92 offen offset:1024 lds\n" \
M_"v[244:247], a[180:183], v[120:123], v[244:247]\n" \
R_"a[136:139], v48 offset:10240\n" \
R_"a[152:155], v48 offset:14336\n" \
M_"v[208:211], a[176:179], v[116:119], v[208:211]\n" \
B_"v6, s[80:83], s92 offen offset:2048 lds\n" \
M_"v[240:243], a[176:179], v[120:123], v[240:243]\n" \
R_"a[140:143], v48 offset:11264\n" \
R_"a[156:159], v48 offset:15360\n" \
M_"v[204:207], a[172:175], v[116:119], v[204:207]\n" \
B_"v7, s[80:83], s92 offen offset:3072 lds\n" \
M_"v[236:239], a[172:175], v[120:123], v[236:239]\n" \
"v_readlane_b32 s96, v11, 1\n" \
"v_lshrrev_b32_e32 v16, 1, v20\n" \
M_"v[200:203], a[168:171], v[116:119], v[200:203]\n" \
"v_add_f32_e32 v54, v54, v22\n" \
"v_max_f32_e32 v62, v62, v54\n" \
M_"v[232:235], a[168:171], v[120:123], v[232:235]\n" \
"v_rcp_f32_e32 v54, v54\n" \
M_"v[196:199], a[164:167], v[116:119], v[196:199]\n" \
"v_mul_f32_e32 v54, v54, v58\n" \
"v_mul_f32_e32 v128, v54, v128\n" \
M_"v[228:231], a[164:167], v[120:123], v[228:231]\n" \
"v_mul_f32_e32 v129, v54, v129\n" \
"v_mul_f32_e32 v136, v54, v136\n" \
M_"v[192:195], a[160:163], v[116:119], v[192:195]\n" \
"v_mul_f32_e32 v137, v54, v137\n" \
"v_mul_f32_e32 v144, v54, v144\n" \
M_"v[224:227], a[160:163], v[120:123], v[224:227]\n" \
"v_mul_f32_e32 v145, v54, v145\n" \
"v_mul_f32_e32 v152, v54, v152\n" \
"v_smfmac_f32_16x16x64_bf16 a[100:103], a[104:107], v[116:123], v21\n" \
"v_mul_f32_e32 v153, v54, v153\n" \
C_"v128, v128, v129\n" \
"v_mul_f32_e32 v130, v54, v130\n" \
"v_mul_f32_e32 v131, v54, v131\n" \
C_"v136, v136, v137\n" \
"v_mul_f32_e32 v138, v54, v138\n" \
"v_mul_f32_e32 v139, v54, v139\n" \
C_"v144, v144, v145\n" \
"v_mul_f32_e32 v146, v54, v146\n" \
"v_mul_f32_e32 v147, v54, v147\n" \
C_"v152, v152, v153\n" \
"v_mul_f32_e32 v154, v54, v154\n" \
"v_mul_f32_e32 v155, v54, v155\n" \
C_"v129, v130, v131\n" \
"v_mul_f32_e32 v132, v54, v132\n" \
"v_mul_f32_e32 v133, v54, v133\n" \
"v_mul_f32_e32 v134, v54, v134\n" \
"v_mul_f32_e32 v135, v54, v135\n" \
C_"v137, v138, v139\n" \
"v_mul_f32_e32 v140, v54, v140\n" \
"v_mul_f32_e32 v141, v54, v141\n" \
"v_mul_f32_e32 v142, v54, v142\n" \
"v_mul_f32_e32 v143, v54, v143\n" \
C_"v145, v146, v147\n" \
"v_mul_f32_e32 v148, v54, v148\n" \
"v_mul_f32_e32 v149, v54, v149\n" \
"v_mul_f32_e32 v150, v54, v150\n" \
"v_mul_f32_e32 v151, v54, v151\n" \
C_"v153, v154, v155\n" \
"v_mul_f32_e32 v156, v54, v156\n" \
"v_mul_f32_e32 v157, v54, v157\n" \
"v_mul_f32_e32 v158, v54, v158\n" \
"v_mul_f32_e32 v159, v54, v159\n" \
C_"v130, v132, v133\n" \
C_"v131, v134, v135\n" \
"buffer_store_dwordx4 v[128:131], v20, s[56:59], s68 offen offset:0 sc1\n" \
C_"v138, v140, v141\n" \
C_"v139, v142, v143\n" \
C_"v146, v148, v149\n" \
C_"v147, v150, v151\n" \
C_"v154, v156, v157\n" \
C_"v155, v158, v159\n" \
"buffer_store_dwordx4 v[136:139], v20, s[56:59], s68 offen offset:1024 sc1\n" \
"s_add_u32 m0, s93, 0x2000\n" \
"v_add_u32_e32 v49, 0x10000, v48\n" \
"s_xor_b32 s65, s65, 0x8000\n" \
"s_add_u32 s64, s64, 0x10000\n" \
E_"v86, v86\n" \
"buffer_store_dwordx4 v[144:147], v20, s[56:59], s68 offen offset:2048 sc1\n" \
E_"v87, v87\n" \
E_"v84, v84\n" \
E_"v85, v85\n" \
"buffer_store_dwordx4 v[152:155], v20, s[56:59], s68 offen offset:3072 sc1\n" \
E_"v88, v88\n" \
E_"v89, v89\n" \
C_"v87, v86, v87\n" \
E_"v82, v82\n" \
B_"v8, s[80:83], s92 offen offset:0 lds\n" \
E_"v83, v83\n" \
E_"v80, v80\n" \
E_"v81, v81\n" \
E_"v90, v90\n" \
E_"v91, v91\n" \
C_"v86, v84, v85\n" \
C_"v88, v88, v89\n" \
E_"v92, v92\n" \
E_"v93, v93\n" \
E_"v94, v94\n" \
E_"v95, v95\n" \
C_"v85, v82, v83\n" \
C_"v84, v80, v81\n" \
C_"v89, v90, v91\n" \
"v_and_b32_e32 v84, v12, v84\n" \
"v_and_b32_e32 v85, v13, v85\n" \
"v_and_b32_e32 v86, v14, v86\n" \
"s_waitcnt vmcnt(17) lgkmcnt(0)\n" \
"s_barrier\n" \
M_"v[120:123], a[128:131], a[48:51], v[44:47]\n" \
"buffer_load_dwordx2 v[26:27], v16, s[56:59], s96 offen sc1\n" \
M_"v[120:123], a[132:135], a[52:55], v[120:123]\n" \
R_"a[188:191], v49 offset:7168\n" \
R_"a[184:187], v49 offset:6144\n" \
M_"v[120:123], a[136:139], a[56:59], v[120:123]\n" \
"s_xor_b32 m0, s65, 0x12000\n" \
R_"a[180:183], v49 offset:5120\n" \
M_"v[120:123], a[140:143], a[60:63], v[120:123]\n" \
B_"v50, s[8:11], s64 offen offset:0 sc1 lds\n" \
M_"v[124:127], a[156:159], a[60:63], v[44:47]\n" \
R_"a[176:179], v49 offset:4096\n" \
R_"a[172:175], v49 offset:3072\n" \
M_"v[124:127], a[152:155], a[56:59], v[124:127]\n" \
B_"v50, s[8:11], s64 offen offset:1024 sc1 lds\n" \
M_"v[124:127], a[148:151], a[52:55], v[124:127]\n" \
R_"a[168:171], v49 offset:2048\n" \
"v_and_b32_e32 v87, v15, v87\n" \
M_"v[124:127], a[144:147], a[48:51], v[124:127]\n" \
C_"v90, v92, v93\n" \
C_"v91, v94, v95\n" \
M_"v[32:35], a[124:127], a[64:67], 0\n" \
B_"v50, s[8:11], s64 offen offset:2048 sc1 lds\n" \
M_"v[36:39], a[124:127], a[80:83], 0\n" \
R_"a[164:167], v49 offset:1024\n" \
R_"a[160:163], v49 offset:0\n" \
M_"v[32:35], a[224:227], a[68:71], v[32:35]\n" \
R_"a[0:3], v49 offset:8192\n" \
R_"a[4:7], v49 offset:9216\n" \
M_"v[36:39], a[224:227], a[84:87], v[36:39]\n" \
R_"a[8:11], v49 offset:10240\n" \
R_"a[12:15], v49 offset:11264\n" \
M_"v[32:35], a[228:231], a[72:75], v[32:35]\n" \
B_"v50, s[8:11], s64 offen offset:3072 sc1 lds\n" \
M_"v[36:39], a[228:231], a[88:91], v[36:39]\n" \
R_"a[16:19], v49 offset:12288\n" \
R_"a[20:23], v49 offset:13312\n" \
M_"v[32:35], a[232:235], a[76:79], v[32:35]\n" \
R_"a[24:27], v49 offset:14336\n" \
R_"a[28:31], v49 offset:15360\n" \
M_"v[36:39], a[232:235], a[92:95], v[36:39]\n" \
E_"v120, v120\n" \
"v_smfmac_f32_16x16x64_bf16 a[100:103], a[104:107], v[84:91], v21\n" \
"s_mov_b32 s63, s62\n" \
"s_waitcnt lgkmcnt(13)\n" \
M_"v[220:223], a[188:191], v[84:87], v[220:223]\n" \
E_"v121, v121\n" \
M_"v[252:255], a[188:191], v[88:91], v[252:255]\n" \
E_"v122, v122\n" \
M_"v[216:219], a[184:187], v[84:87], v[216:219]\n" \
E_"v123, v123\n" \
M_"v[248:251], a[184:187], v[88:91], v[248:251]\n" \
C_"v120, v120, v121\n" \
C_"v121, v122, v123\n" \
M_"v[212:215], a[180:183], v[84:87], v[212:215]\n" \
E_"v124, v124\n" \
M_"v[244:247], a[180:183], v[88:91], v[244:247]\n" \
"v_and_b32_e32 v120, v12, v120\n" \
"s_waitcnt lgkmcnt(10)\n" \
M_"v[208:211], a[176:179], v[84:87], v[208:211]\n" \
E_"v125, v125\n" \
M_"v[240:243], a[176:179], v[88:91], v[240:243]\n" \
E_"v126, v126\n" \
M_"v[204:207], a[172:175], v[84:87], v[204:207]\n" \
E_"v127, v127\n" \
M_"v[236:239], a[172:175], v[88:91], v[236:239]\n" \
C_"v122, v124, v125\n" \
C_"v123, v126, v127\n" \
M_"v[200:203], a[168:171], v[84:87], v[200:203]\n" \
"v_and_b32_e32 v121, v13, v121\n" \
"v_and_b32_e32 v122, v14, v122\n" \
M_"v[232:235], a[168:171], v[88:91], v[232:235]\n" \
"v_and_b32_e32 v123, v15, v123\n" \
"s_waitcnt lgkmcnt(0)\n" \
M_"v[196:199], a[164:167], v[84:87], v[196:199]\n" \
"v_accvgpr_read_b32 v56, a100\n" \
"v_accvgpr_read_b32 v24, a101\n" \
M_"v[228:231], a[164:167], v[88:91], v[228:231]\n" \
"v_readlane_b32 s97, v11, 39\n" \
"v_add_u32_e32 v17, 0x49000, v20\n" \
M_"v[192:195], a[160:163], v[84:87], v[192:195]\n" \
"v_readlane_b32 s98, v11, 40\n" \
"v_add_u32_e32 v48, s16, v20\n" \
M_"v[224:227], a[160:163], v[88:91], v[224:227]\n" \
R_"a[128:131], v48 offset:0\n" \
R_"a[144:147], v48 offset:4096\n" \
M_"a[100:103], a[116:119], v[120:123], a[100:103]\n" \
R_"a[132:135], v48 offset:1024\n" \
R_"a[148:151], v48 offset:5120\n" \
M_"v[224:227], a[0:3], v[120:123], v[224:227]\n" \
R_"a[136:139], v48 offset:2048\n" \
R_"a[152:155], v48 offset:6144\n" \
M_"v[228:231], a[4:7], v[120:123], v[228:231]\n" \
R_"a[140:143], v48 offset:3072\n" \
R_"a[156:159], v48 offset:7168\n" \
M_"v[232:235], a[8:11], v[120:123], v[232:235]\n" \
"v_accvgpr_read_b32 v55, a98\n" \
"v_accvgpr_read_b32 v23, a99\n" \
M_"a[96:99], a[120:123], a[120:123], 0\n" \
"v_add_f32_e32 v55, v55, v23\n" \
"v_max_f32_e32 v62, v62, v55\n" \
M_"v[236:239], a[12:15], v[120:123], v[236:239]\n" \
"v_rcp_f32_e32 v55, v55\n" \
M_"v[240:243], a[16:19], v[120:123], v[240:243]\n" \
"v_mul_f32_e32 v55, v55, v59\n" \
"v_mul_f32_e32 v160, v55, v160\n" \
M_"v[244:247], a[20:23], v[120:123], v[244:247]\n" \
"v_mul_f32_e32 v161, v55, v161\n" \
"v_mul_f32_e32 v168, v55, v168\n" \
M_"v[248:251], a[24:27], v[120:123], v[248:251]\n" \
"v_mul_f32_e32 v169, v55, v169\n" \
"v_mul_f32_e32 v176, v55, v176\n" \
M_"v[252:255], a[28:31], v[120:123], v[252:255]\n" \
"v_mul_f32_e32 v177, v55, v177\n" \
"v_mul_f32_e32 v184, v55, v184\n" \
"v_mul_f32_e32 v185, v55, v185\n" \
C_"v160, v160, v161\n" \
"v_mul_f32_e32 v162, v55, v162\n" \
"v_mul_f32_e32 v163, v55, v163\n" \
C_"v168, v168, v169\n" \
"v_mul_f32_e32 v170, v55, v170\n" \
"v_mul_f32_e32 v171, v55, v171\n" \
"s_waitcnt vmcnt(4)\n" \
"v_cmp_ne_u64_e32 vcc, s[62:63], v[26:27]\n" \
C_"v176, v176, v177\n" \
"v_mul_f32_e32 v178, v55, v178\n" \
"v_mul_f32_e32 v179, v55, v179\n" \
C_"v184, v184, v185\n" \
"v_mul_f32_e32 v186, v55, v186\n" \
"s_cbranch_vccnz L_sos_%=\n" \
"L_ros_%=:\n" \
B_"a[124:127], v20, s[56:59], s97 offen offset:0 sc1\n" \
"v_mul_f32_e32 v187, v55, v187\n" \
C_"v161, v162, v163\n" \
"v_mul_f32_e32 v164, v55, v164\n" \
"v_mul_f32_e32 v165, v55, v165\n" \
"v_mul_f32_e32 v166, v55, v166\n" \
"v_mul_f32_e32 v167, v55, v167\n" \
B_"a[228:231], v17, s[56:59], s97 offen offset:0 sc1\n" \
C_"v169, v170, v171\n" \
"v_mul_f32_e32 v172, v55, v172\n" \
"v_mul_f32_e32 v173, v55, v173\n" \
"v_mul_f32_e32 v174, v55, v174\n" \
"v_mul_f32_e32 v175, v55, v175\n" \
C_"v177, v178, v179\n" \
B_"a[236:239], v20, s[56:59], s98 offen offset:0 sc1\n" \
"v_mul_f32_e32 v180, v55, v180\n" \
"v_mul_f32_e32 v181, v55, v181\n" \
"v_mul_f32_e32 v182, v55, v182\n" \
"v_mul_f32_e32 v183, v55, v183\n" \
C_"v185, v186, v187\n" \
"v_mul_f32_e32 v188, v55, v188\n" \
B_"a[244:247], v17, s[56:59], s98 offen offset:0 sc1\n" \
"v_mul_f32_e32 v189, v55, v189\n" \
"v_mul_f32_e32 v190, v55, v190\n" \
"v_mul_f32_e32 v191, v55, v191\n" \
C_"v162, v164, v165\n" \
C_"v163, v166, v167\n" \
C_"v170, v172, v173\n" \
"buffer_store_dwordx4 v[160:163], v20, s[56:59], s69 offen offset:0 sc1\n" \
C_"v171, v174, v175\n" \
C_"v178, v180, v181\n" \
C_"v179, v182, v183\n" \
C_"v186, v188, v189\n" \
C_"v187, v190, v191\n" \
"v_readlane_b32 s93, v11, 23\n" \
"buffer_store_dwordx4 v[168:171], v20, s[56:59], s69 offen offset:1024 sc1\n" \
"v_readlane_b32 s84, v11, 15\n" \
"v_readlane_b32 s85, v11, 16\n" \
"v_readlane_b32 s86, v11, 17\n" \
"s_mov_b32 s87, 0x20000\n" \
"v_readlane_b32 s92, v11, 18\n" \
"v_readlane_b32 s91, v11, 24\n" \
"buffer_store_dwordx4 v[176:179], v20, s[56:59], s69 offen offset:2048 sc1\n" \
M_"a[112:115], a[120:123], v[128:131], a[112:115]\n" \
M_"a[112:115], a[120:123], v[136:139], a[112:115]\n" \
"buffer_store_dwordx4 v[184:187], v20, s[56:59], s69 offen offset:3072 sc1\n" \
"v_mfma_f32_32x32x16_bf16 v[128:143], a[120:123], a[120:123], 0\n" \
M_"a[112:115], a[120:123], v[144:147], a[112:115]\n" \
B_"a[224:227], v20, s[56:59], s97 offen offset:1024 sc1\n" \
M_"a[112:115], a[120:123], v[152:155], a[112:115]\n" \
"v_mfma_f32_32x32x16_bf16 v[144:159], a[120:123], a[120:123], 0\n" \
B_"a[232:235], v17, s[56:59], s97 offen offset:1024 sc1\n" \
"v_add_f32_e32 v56, v56, v24\n" \
M_"a[112:115], a[120:123], v[160:163], a[112:115]\n" \
B_"a[240:243], v20, s[56:59], s98 offen offset:1024 sc1\n" \
"v_max_f32_e32 v62, v62, v56\n" \
"v_rcp_f32_e32 v56, v56\n" \
M_"a[112:115], a[120:123], v[168:171], a[112:115]\n" \
"v_mfma_f32_32x32x16_bf16 v[160:175], a[120:123], a[120:123], 0\n" \
B_"a[248:251], v17, s[56:59], s98 offen offset:1024 sc1\n" \
"v_mul_f32_e32 v56, v56, v60\n" \
M_"a[112:115], a[120:123], v[176:179], a[112:115]\n" \
"v_mul_f32_e32 v192, v56, v192\n" \
"v_mul_f32_e32 v193, v56, v193\n" \
"s_waitcnt vmcnt(17) lgkmcnt(0)\n" \
"s_barrier\n" \
M_"v[64:67], a[128:131], a[64:67], v[32:35]\n" \
"s_add_u32 m0, s93, 0x0\n" \
C_"v192, v192, v193\n" \
M_"v[64:67], a[132:135], a[68:71], v[64:67]\n" \
B_"v0, s[84:87], s92 offen offset:0 lds\n" \
M_"v[64:67], a[136:139], a[72:75], v[64:67]\n" \
"v_mul_f32_e32 v194, v56, v194\n" \
"v_mul_f32_e32 v195, v56, v195\n" \
M_"v[64:67], a[140:143], a[76:79], v[64:67]\n" \
B_"v1, s[84:87], s92 offen offset:1024 lds\n" \
M_"v[68:71], a[156:159], a[76:79], v[32:35]\n" \
M_"a[112:115], a[120:123], v[184:187], a[112:115]\n" \
"v_mfma_f32_32x32x16_bf16 v[176:191], a[120:123], a[120:123], 0\n" \
B_"v2, s[84:87], s92 offen offset:2048 lds\n" \
C_"v193, v194, v195\n" \
"v_mul_f32_e32 v196, v56, v196\n" \
"v_mul_f32_e32 v197, v56, v197\n" \
"v_mul_f32_e32 v198, v56, v198\n" \
M_"v[68:71], a[152:155], a[72:75], v[68:71]\n" \
B_"v3, s[84:87], s92 offen offset:3072 lds\n" \
M_"v[68:71], a[148:151], a[68:71], v[68:71]\n" \
"s_add_u32 m0, s91, 0x1000\n" \
"v_mul_f32_e32 v199, v56, v199\n" \
M_"v[68:71], a[144:147], a[64:67], v[68:71]\n" \
B_"v4, s[84:87], s92 offen offset:0 lds\n" \
M_"v[76:79], a[144:147], a[80:83], v[36:39]\n" \
R_"a[144:147], v48 offset:12288\n" \
"v_mul_f32_e32 v200, v56, v200\n" \
M_"v[76:79], a[148:151], a[84:87], v[76:79]\n" \
B_"v5, s[84:87], s92 offen offset:1024 lds\n" \
M_"v[76:79], a[152:155], a[88:91], v[76:79]\n" \
R_"a[148:151], v48 offset:13312\n" \
R_"a[152:155], v48 offset:14336\n" \
M_"v[76:79], a[156:159], a[92:95], v[76:79]\n" \
B_"v6, s[84:87], s92 offen offset:2048 lds\n" \
M_"v[72:75], a[140:143], a[92:95], v[36:39]\n" \
R_"a[140:143], v48 offset:11264\n" \
R_"a[156:159], v48 offset:15360\n" \
M_"v[72:75], a[136:139], a[88:91], v[72:75]\n" \
B_"v7, s[84:87], s92 offen offset:3072 lds\n" \
M_"v[72:75], a[132:135], a[84:87], v[72:75]\n" \
R_"a[132:135], v48 offset:9216\n" \
R_"a[136:139], v48 offset:10240\n" \
M_"v[72:75], a[128:131], a[80:83], v[72:75]\n" \
R_"a[128:131], v48 offset:8192\n" \
"v_mul_f32_e32 v201, v56, v201\n" \
"v_accvgpr_read_b32 v57, a102\n" \
"v_accvgpr_read_b32 v25, a103\n" \
M_"a[100:103], a[120:123], a[120:123], 0\n" \
C_"v194, v196, v197\n" \
C_"v195, v198, v199\n" \
"buffer_store_dwordx4 v[192:195], v20, s[56:59], s70 offen offset:0 sc1\n" \
C_"v200, v200, v201\n" \
"v_mul_f32_e32 v202, v56, v202\n" \
"v_mul_f32_e32 v203, v56, v203\n" \
"v_add_f32_e32 v57, v57, v25\n" \
M_"a[112:115], a[120:123], v[192:195], a[112:115]\n" \
C_"v201, v202, v203\n" \
"v_mul_f32_e32 v204, v56, v204\n" \
"v_mul_f32_e32 v205, v56, v205\n" \
"v_mul_f32_e32 v206, v56, v206\n" \
"v_mul_f32_e32 v207, v56, v207\n" \
"v_mul_f32_e32 v208, v56, v208\n" \
"v_mul_f32_e32 v209, v56, v209\n" \
"v_max_f32_e32 v62, v62, v57\n" \
C_"v202, v204, v205\n" \
C_"v203, v206, v207\n" \
"buffer_store_dwordx4 v[200:203], v20, s[56:59], s70 offen offset:1024 sc1\n" \
".fill (L_ab + 28672 - .) / 4, 4, 0xBF800000\n" \
"L_ap7:\n" \
"s_cbranch_execz L_ap0\n" \
C_"v208, v208, v209\n" \
"v_mul_f32_e32 v210, v56, v210\n" \
"v_mul_f32_e32 v211, v56, v211\n" \
"v_rcp_f32_e32 v57, v57\n" \
M_"a[112:115], a[120:123], v[200:203], a[112:115]\n" \
"v_mfma_f32_32x32x16_bf16 v[192:207], a[120:123], a[120:123], 0\n" \
C_"v209, v210, v211\n" \
"v_mul_f32_e32 v212, v56, v212\n" \
"v_mul_f32_e32 v213, v56, v213\n" \
"v_mul_f32_e32 v214, v56, v214\n" \
"v_mul_f32_e32 v215, v56, v215\n" \
"v_mul_f32_e32 v216, v56, v216\n" \
"v_mul_f32_e32 v217, v56, v217\n" \
C_"v210, v212, v213\n" \
C_"v211, v214, v215\n" \
"buffer_store_dwordx4 v[208:211], v20, s[56:59], s70 offen offset:2048 sc1\n" \
C_"v216, v216, v217\n" \
"v_mul_f32_e32 v218, v56, v218\n" \
"v_mul_f32_e32 v219, v56, v219\n" \
"v_mul_f32_e32 v57, v57, v61\n" \
M_"a[112:115], a[120:123], v[208:211], a[112:115]\n" \
C_"v217, v218, v219\n" \
"v_mul_f32_e32 v220, v56, v220\n" \
"v_mul_f32_e32 v221, v56, v221\n" \
"v_mul_f32_e32 v222, v56, v222\n" \
"v_mul_f32_e32 v223, v56, v223\n" \
"v_mul_f32_e32 v224, v57, v224\n" \
"v_mul_f32_e32 v225, v57, v225\n" \
C_"v218, v220, v221\n" \
C_"v219, v222, v223\n" \
"buffer_store_dwordx4 v[216:219], v20, s[56:59], s70 offen offset:3072 sc1\n" \
C_"v224, v224, v225\n" \
"v_mul_f32_e32 v226, v57, v226\n" \
"v_mul_f32_e32 v227, v57, v227\n" \
M_"a[112:115], a[120:123], v[216:219], a[112:115]\n" \
"v_mfma_f32_32x32x16_bf16 v[208:223], a[120:123], a[120:123], 0\n" \
C_"v225, v226, v227\n" \
"v_mul_f32_e32 v228, v57, v228\n" \
"v_mul_f32_e32 v229, v57, v229\n" \
"v_mul_f32_e32 v230, v57, v230\n" \
"v_mul_f32_e32 v231, v57, v231\n" \
"v_mul_f32_e32 v232, v57, v232\n" \
"v_mul_f32_e32 v233, v57, v233\n" \
C_"v226, v228, v229\n" \
C_"v227, v230, v231\n" \
"buffer_store_dwordx4 v[224:227], v20, s[56:59], s71 offen offset:0 sc1\n" \
C_"v232, v232, v233\n" \
"v_mul_f32_e32 v234, v57, v234\n" \
"v_mul_f32_e32 v235, v57, v235\n" \
M_"a[112:115], a[120:123], v[224:227], a[112:115]\n" \
C_"v233, v234, v235\n" \
"v_mul_f32_e32 v236, v57, v236\n" \
"v_mul_f32_e32 v237, v57, v237\n" \
"v_mul_f32_e32 v238, v57, v238\n" \
"v_mul_f32_e32 v239, v57, v239\n" \
"v_mul_f32_e32 v240, v57, v240\n" \
"v_mul_f32_e32 v241, v57, v241\n" \
C_"v234, v236, v237\n" \
C_"v235, v238, v239\n" \
"buffer_store_dwordx4 v[232:235], v20, s[56:59], s71 offen offset:1024 sc1\n" \
C_"v240, v240, v241\n" \
"v_mul_f32_e32 v242, v57, v242\n" \
"v_mul_f32_e32 v243, v57, v243\n" \
M_"a[112:115], a[120:123], v[232:235], a[112:115]\n" \
"v_mfma_f32_32x32x16_bf16 v[224:239], a[120:123], a[120:123], 0\n" \
C_"v241, v242, v243\n" \
"v_mul_f32_e32 v244, v57, v244\n" \
"v_mul_f32_e32 v245, v57, v245\n" \
"v_mul_f32_e32 v246, v57, v246\n" \
"v_mul_f32_e32 v247, v57, v247\n" \
"v_mul_f32_e32 v248, v57, v248\n" \
"v_mul_f32_e32 v249, v57, v249\n" \
C_"v242, v244, v245\n" \
C_"v243, v246, v247\n" \
"buffer_store_dwordx4 v[240:243], v20, s[56:59], s71 offen offset:2048 sc1\n" \
C_"v248, v248, v249\n" \
"v_mul_f32_e32 v250, v57, v250\n" \
"v_mul_f32_e32 v251, v57, v251\n" \
M_"a[112:115], a[120:123], v[240:243], a[112:115]\n" \
C_"v249, v250, v251\n" \
"v_mul_f32_e32 v252, v57, v252\n" \
"v_mul_f32_e32 v253, v57, v253\n" \
"v_mul_f32_e32 v254, v57, v254\n" \
"v_mul_f32_e32 v255, v57, v255\n" \
C_"v250, v252, v253\n" \
C_"v251, v254, v255\n" \
"buffer_store_dwordx4 v[248:251], v20, s[56:59], s71 offen offset:3072 sc1\n" \
"s_add_u32 m0, s91, 0x2000\n" \
M_"a[112:115], a[120:123], v[248:251], a[112:115]\n" \
"v_mfma_f32_32x32x16_bf16 v[240:255], a[120:123], a[120:123], 0\n" \
"v_add_u32_e32 v53, 0x20000, v63\n" \
B_"v8, s[84:87], s92 offen offset:0 lds\n" \
E_"v70, v70\n" \
E_"v71, v71\n" \
E_"v72, v72\n" \
E_"v73, v73\n" \
"s_add_u32 s64, s64, 0x10000\n" \
"v_accvgpr_read_b32 v63, a112\n" \
"v_cmp_u_f32_e32 vcc, v63, v63\n" \
"v_cmp_lt_f32_e64 s[72:73], s21, v62\n" \
"v_readlane_b32 s95, v11, 3\n" \
E_"v68, v68\n" \
E_"v69, v69\n" \
E_"v74, v74\n" \
E_"v75, v75\n" \
"s_or_b64 vcc, s[72:73], vcc\n" \
"s_cselect_b32 s95, 0x40000000, s95\n" \
"s_cselect_b32 s94, 0x7f800000, 0\n" \
C_"v71, v70, v71\n" \
E_"v66, v66\n" \
E_"v67, v67\n" \
E_"v64, v64\n" \
E_"v65, v65\n" \
C_"v72, v72, v73\n" \
E_"v76, v76\n" \
E_"v77, v77\n" \
E_"v78, v78\n" \
E_"v79, v79\n" \
C_"v70, v68, v69\n" \
"s_waitcnt vmcnt(17) lgkmcnt(0)\n" \
"s_barrier\n" \
C_"v73, v74, v75\n" \
"v_add_u32_e32 v49, 0x10000, v48\n" \
"s_xor_b32 s65, s65, 0x8000\n" \
C_"v69, v66, v67\n" \
C_"v68, v64, v65\n" \
C_"v74, v76, v77\n" \
C_"v75, v78, v79\n" \
"v_xor_b32_e32 v48, 0x8000, v48\n" \
"s_sub_u32 s66, s19, 2\n" \
"s_branch L_f1_%=\n" \
".fill (L_ab + 32768 - .) / 4, 4, 0xBF800000\n" \
".fill (L_ab + 32768 - .) / 4, 4, 0xBF800000\n" \
"L_ap8:\n" \
"s_setpc_b64 s[88:89]\n" \
"L_out_%=:\n" \
"v_bfe_u32 v38, v20, 2, 4\n" \
"v_readlane_b32 s64, v11, 2\n" \
"v_add_u32_e32 v32, 0x49000, v20\n" \
"v_add_u32_e32 v34, 0x20000, v10\n" \
X_"a[0:1], v34 offset:0\n" \
X_"a[2:3], v34 offset:4608\n" \
X_"a[4:5], v34 offset:9216\n" \
"buffer_load_dword v26, v38, s[56:59], s64 offen sc1\n" \
X_"a[6:7], v34 offset:13824\n" \
X_"a[8:9], v34 offset:32\n" \
X_"a[10:11], v34 offset:4640\n" \
X_"a[12:13], v34 offset:9248\n" \
"buffer_store_dwordx4 v[64:67], v20, s[56:59], s61 offen offset:0 sc1\n" \
X_"a[14:15], v34 offset:13856\n" \
"v_mov_b32_e32 v39, s62\n" \
"v_readlane_b32 s70, v11, 5\n" \
"v_readlane_b32 s71, v11, 4\n" \
"buffer_store_dwordx4 v[68:71], v20, s[56:59], s61 offen offset:1024 sc1\n" \
X_"a[16:17], v34 offset:64\n" \
X_"a[18:19], v34 offset:4672\n" \
X_"a[20:21], v34 offset:9280\n" \
X_"a[22:23], v34 offset:13888\n" \
"buffer_store_dwordx4 v[72:75], v20, s[56:59], s61 offen offset:2048 sc1\n" \
X_"a[24:25], v34 offset:96\n" \
X_"a[26:27], v34 offset:4704\n" \
X_"a[28:29], v34 offset:9312\n" \
X_"a[30:31], v34 offset:13920\n" \
"buffer_store_dwordx4 v[76:79], v20, s[56:59], s61 offen offset:3072 sc1\n" \
X_"a[96:97], v34 offset:128\n" \
X_"a[98:99], v34 offset:4736\n" \
X_"a[100:101], v34 offset:9344\n" \
X_"a[102:103], v34 offset:13952\n" \
"buffer_store_dwordx4 v[80:83], v32, s[56:59], s61 offen offset:0 sc1\n" \
X_"a[104:105], v34 offset:160\n" \
X_"a[106:107], v34 offset:4768\n" \
X_"a[108:109], v34 offset:9376\n" \
X_"a[110:111], v34 offset:13984\n" \
"buffer_store_dwordx4 v[84:87], v32, s[56:59], s61 offen offset:1024 sc1\n" \
X_"a[112:113], v34 offset:192\n" \
"s_waitcnt vmcnt(16) lgkmcnt(15)\n" \
M_"v[112:115], a[0:3], a[192:195], 0\n" \
"buffer_store_dwordx4 v[96:99], v32, s[56:59], s61 offen offset:2048 sc1\n" \
M_"v[112:115], a[4:7], a[196:199], v[112:115]\n" \
X_"a[114:115], v34 offset:4800\n" \
X_"a[116:117], v34 offset:9408\n" \
"buffer_store_dwordx4 v[100:103], v32, s[56:59], s61 offen offset:3072 sc1\n" \
M_"v[116:119], a[0:3], a[208:211], 0\n" \
X_"a[118:119], v34 offset:14016\n" \
X_"a[120:121], v34 offset:224\n" \
M_"v[116:119], a[4:7], a[212:215], v[116:119]\n" \
X_"a[122:123], v34 offset:4832\n" \
"v_lshrrev_b32_e32 v44, 9, v20\n" \
M_"v[184:187], a[0:3], v[64:67], 0\n" \
"v_mul_u32_u24_e32 v44, 0xb40, v44\n" \
"v_add_u32_e32 v35, v44, v10\n" \
M_"v[184:187], a[4:7], v[68:71], v[184:187]\n" \
"v_readlane_b32 s65, v11, 33\n" \
"v_readlane_b32 s67, v11, 26\n" \
M_"v[188:191], a[0:3], v[80:83], 0\n" \
X_"a[0:1], v34 offset:9440\n" \
X_"a[2:3], v34 offset:14048\n" \
M_"v[188:191], a[4:7], v[84:87], v[188:191]\n" \
X_"a[4:5], v34 offset:256\n" \
X_"a[6:7], v34 offset:4864\n" \
M_"v[196:199], a[8:11], v[80:83], 0\n" \
"v_readlane_b32 s80, v11, 11\n" \
"v_readlane_b32 s81, v11, 12\n" \
M_"v[196:199], a[12:15], v[84:87], v[196:199]\n" \
"s_mov_b32 s82, 0x47fca0\n" \
"s_mov_b32 s83, 0x20000\n" \
M_"v[192:195], a[8:11], v[64:67], 0\n" \
"v_readlane_b32 s66, v11, 31\n" \
"s_add_u32 m0, s67, 0x0\n" \
M_"v[192:195], a[12:15], v[68:71], v[192:195]\n" \
"v_readlane_b32 s92, v11, 27\n" \
"v_readlane_b32 s73, v11, 32\n" \
M_"v[124:127], a[8:11], a[208:211], 0\n" \
B_"v0, s[80:83], s66 offen offset:0 lds\n" \
M_"v[124:127], a[12:15], a[212:215], v[124:127]\n" \
"v_readlane_b32 s94, v11, 28\n" \
"v_readlane_b32 s84, v11, 9\n" \
M_"v[120:123], a[8:11], a[192:195], 0\n" \
X_"a[8:9], v34 offset:9472\n" \
X_"a[10:11], v34 offset:14080\n" \
M_"v[120:123], a[12:15], a[196:199], v[120:123]\n" \
X_"a[12:13], v34 offset:18432\n" \
X_"a[14:15], v34 offset:23040\n" \
M_"v[128:131], a[16:19], a[192:195], 0\n" \
"v_readlane_b32 s85, v11, 10\n" \
"s_waitcnt lgkmcnt(9)\n" \
M_"v[128:131], a[20:23], a[196:199], v[128:131]\n" \
B_"v1, s[80:83], s66 offen offset:1024 lds\n" \
M_"v[132:135], a[16:19], a[208:211], 0\n" \
"s_mov_b32 s86, 0xa1ca0\n" \
"s_mov_b32 s87, 0x20000\n" \
M_"v[132:135], a[20:23], a[212:215], v[132:135]\n" \
"v_add_u32_e32 v33, 0x10000, v10\n" \
"v_readlane_b32 s68, v11, 34\n" \
M_"v[200:203], a[16:19], v[64:67], 0\n" \
"v_readlane_b32 s69, v11, 35\n" \
"v_readlane_b32 s72, v11, 37\n" \
M_"v[200:203], a[20:23], v[68:71], v[200:203]\n" \
"v_readlane_b32 s93, v11, 38\n" \
"v_readlane_b32 s63, v11, 44\n" \
M_"v[204:207], a[16:19], v[80:83], 0\n" \
X_"a[16:17], v34 offset:18464\n" \
X_"a[18:19], v34 offset:23072\n" \
M_"v[204:207], a[20:23], v[84:87], v[204:207]\n" \
X_"a[20:21], v34 offset:18496\n" \
X_"a[22:23], v34 offset:23104\n" \
M_"v[212:215], a[24:27], v[80:83], 0\n" \
B_"v2, s[80:83], s66 offen offset:2048 lds\n" \
M_"v[212:215], a[28:31], v[84:87], v[212:215]\n" \
"v_bfe_u32 v40, v20, 2, 7\n" \
"s_add_u32 s74, s63, 0x3800\n" \
M_"v[208:211], a[24:27], v[64:67], 0\n" \
"v_lshlrev_b32_e32 v41, 12, v20\n" \
"s_add_u32 s12, s63, 0x0\n" \
M_"v[208:211], a[28:31], v[68:71], v[208:211]\n" \
"v_readlane_b32 s95, v11, 41\n" \
"v_readlane_b32 s96, v11, 42\n" \
M_"v[140:143], a[24:27], a[208:211], 0\n" \
"s_add_u32 s13, s63, 0x1000\n" \
"v_readlane_b32 s97, v11, 43\n" \
M_"v[140:143], a[28:31], a[212:215], v[140:143]\n" \
B_"v3, s[80:83], s66 offen offset:3072 lds\n" \
M_"v[136:139], a[24:27], a[192:195], 0\n" \
X_"a[24:25], v34 offset:18528\n" \
X_"a[26:27], v34 offset:23136\n" \
M_"v[136:139], a[28:31], a[196:199], v[136:139]\n" \
X_"a[28:29], v34 offset:18560\n" \
X_"a[30:31], v34 offset:23168\n" \
M_"v[144:147], a[96:99], a[192:195], 0\n" \
"s_add_u32 m0, s67, 0x1000\n" \
"s_add_u32 s14, s63, 0x2000\n" \
M_"v[144:147], a[100:103], a[196:199], v[144:147]\n" \
"s_add_u32 s15, s63, 0x3000\n" \
"v_readlane_b32 s100, v11, 7\n" \
M_"v[148:151], a[96:99], a[208:211], 0\n" \
B_"v4, s[80:83], s66 offen offset:0 lds\n" \
M_"v[148:151], a[100:103], a[212:215], v[148:151]\n" \
"v_readlane_b32 s16, v11, 45\n" \
"v_bfe_u32 v44, v20, 4, 4\n" \
M_"v[216:219], a[96:99], v[64:67], 0\n" \
"s_add_u32 s17, s16, 0x1000\n" \
"s_add_u32 s18, s16, 0x2000\n" \
M_"v[216:219], a[100:103], v[68:71], v[216:219]\n" \
"s_add_u32 s19, s16, 0x3000\n" \
"v_bfe_u32 v45, v20, 8, 2\n" \
M_"v[220:223], a[96:99], v[80:83], 0\n" \
X_"a[96:97], v34 offset:18592\n" \
X_"a[98:99], v34 offset:23200\n" \
M_"v[220:223], a[100:103], v[84:87], v[220:223]\n" \
X_"a[100:101], v34 offset:18624\n" \
X_"a[102:103], v34 offset:23232\n" \
M_"v[228:231], a[104:107], v[80:83], 0\n" \
B_"v5, s[80:83], s66 offen offset:1024 lds\n" \
M_"v[228:231], a[108:111], v[84:87], v[228:231]\n" \
"v_mul_u32_u24_e32 v42, 0x480, v44\n" \
"v_lshl_add_u32 v42, v45, 3, v42\n" \
M_"v[224:227], a[104:107], v[64:67], 0\n" \
"s_waitcnt vmcnt(14)\n" \
"v_cmp_ne_u32_e32 vcc, s62, v26\n" \
M_"v[224:227], a[108:111], v[68:71], v[224:227]\n" \
"v_lshrrev_b32_e32 v44, 1, v44\n" \
"v_lshlrev_b32_e32 v45, 1, v45\n" \
M_"v[156:159], a[104:107], a[208:211], 0\n" \
"v_sub_u32_e32 v44, v44, v45\n" \
"s_cbranch_vccnz L_sa_%=\n" \
"L_ra_%=:\n" \
M_"v[156:159], a[108:111], a[212:215], v[156:159]\n" \
B_"a[64:67], v20, s[56:59], s65 offen offset:0 sc1\n" \
M_"v[152:155], a[104:107], a[192:195], 0\n" \
X_"a[104:105], v34 offset:18656\n" \
X_"a[106:107], v34 offset:23264\n" \
M_"v[152:155], a[108:111], a[196:199], v[152:155]\n" \
X_"a[108:109], v34 offset:18688\n" \
X_"a[110:111], v34 offset:23296\n" \
M_"v[160:163], a[112:115], a[192:195], 0\n" \
"v_readlane_b32 s64, v11, 36\n" \
"v_and_b32_e32 v45, 16, v20\n" \
M_"v[160:163], a[116:119], a[196:199], v[160:163]\n" \
"v_mov_b32_e32 v46, 0x3f80\n" \
"v_lshlrev_b32_e32 v45, v45, v46\n" \
M_"v[164:167], a[112:115], a[208:211], 0\n" \
B_"a[68:71], v20, s[56:59], s65 offen offset:1024 sc1\n" \
M_"v[164:167], a[116:119], a[212:215], v[164:167]\n" \
"v_mov_b32_e32 v52, 0\n" \
"v_mov_b32_e32 v53, 0\n" \
M_"v[232:235], a[112:115], v[64:67], 0\n" \
"v_mov_b32_e32 v48, 0\n" \
"v_mov_b32_e32 v49, 0\n" \
M_"v[232:235], a[116:119], v[68:71], v[232:235]\n" \
"v_add_u32_e32 v43, 0x9000, v42\n" \
"v_readlane_b32 s88, v11, 13\n" \
M_"v[236:239], a[112:115], v[80:83], 0\n" \
X_"a[112:113], v10 offset:32768\n" \
X_"a[114:115], v10 offset:37376\n" \
M_"v[236:239], a[116:119], v[84:87], v[236:239]\n" \
X_"a[116:117], v10 offset:32800\n" \
"s_waitcnt lgkmcnt(7)\n" \
M_"v[244:247], a[120:123], v[80:83], 0\n" \
X_"a[118:119], v10 offset:37408\n" \
"v_readlane_b32 s89, v11, 14\n" \
M_"v[244:247], a[0:3], v[84:87], v[244:247]\n" \
B_"a[80:83], v32, s[56:59], s65 offen offset:0 sc1\n" \
M_"v[240:243], a[120:123], v[64:67], 0\n" \
"s_mov_b32 s90, 0x240000\n" \
"s_mov_b32 s91, 0x20000\n" \
M_"v[240:243], a[0:3], v[68:71], v[240:243]\n" \
"v_mov_b32_e32 v46, s62\n" \
M_"v[172:175], a[120:123], a[208:211], 0\n" \
M_"v[172:175], a[0:3], a[212:215], v[172:175]\n" \
M_"v[168:171], a[120:123], a[192:195], 0\n" \
X_"a[120:121], v10 offset:32832\n" \
X_"a[122:123], v10 offset:37440\n" \
M_"v[168:171], a[0:3], a[196:199], v[168:171]\n" \
X_"a[0:1], v10 offset:32864\n" \
X_"a[2:3], v10 offset:37472\n" \
M_"v[176:179], a[4:7], a[192:195], 0\n" \
B_"a[84:87], v32, s[56:59], s65 offen offset:1024 sc1\n" \
M_"v[176:179], a[8:11], a[196:199], v[176:179]\n" \
M_"v[180:183], a[4:7], a[208:211], 0\n" \
M_"v[180:183], a[8:11], a[212:215], v[180:183]\n" \
M_"v[248:251], a[4:7], v[64:67], 0\n" \
M_"v[248:251], a[8:11], v[68:71], v[248:251]\n" \
B_"a[72:75], v20, s[56:59], s65 offen offset:2048 sc1\n" \
M_"v[252:255], a[4:7], v[80:83], 0\n" \
X_"a[4:5], v10 offset:32896\n" \
X_"a[6:7], v10 offset:37504\n" \
M_"v[252:255], a[8:11], v[84:87], v[252:255]\n" \
X_"a[8:9], v10 offset:32928\n" \
X_"a[10:11], v10 offset:37536\n" \
M_"v[112:115], a[12:15], a[200:203], v[112:115]\n" \
M_"v[116:119], a[12:15], a[216:219], v[116:119]\n" \
M_"v[184:187], a[12:15], v[72:75], v[184:187]\n" \
B_"a[76:79], v20, s[56:59], s65 offen offset:3072 sc1\n" \
M_"v[188:191], a[12:15], v[96:99], v[188:191]\n" \
X_"a[12:13], v10 offset:32960\n" \
X_"a[14:15], v10 offset:37568\n" \
M_"v[196:199], a[16:19], v[96:99], v[196:199]\n" \
M_"v[192:195], a[16:19], v[72:75], v[192:195]\n" \
M_"v[124:127], a[16:19], a[216:219], v[124:127]\n" \
M_"v[120:123], a[16:19], a[200:203], v[120:123]\n" \
X_"a[16:17], v10 offset:32992\n" \
X_"a[18:19], v10 offset:37600\n" \
M_"v[128:131], a[20:23], a[200:203], v[128:131]\n" \
B_"a[88:91], v32, s[56:59], s65 offen offset:2048 sc1\n" \
M_"v[132:135], a[20:23], a[216:219], v[132:135]\n" \
M_"v[200:203], a[20:23], v[72:75], v[200:203]\n" \
M_"v[204:207], a[20:23], v[96:99], v[204:207]\n" \
X_"a[20:21], v10 offset:33024\n" \
X_"a[22:23], v10 offset:37632\n" \
"s_waitcnt lgkmcnt(10)\n" \
"s_barrier\n" \
M_"v[212:215], a[24:27], v[96:99], v[212:215]\n" \
B_"a[92:95], v32, s[56:59], s65 offen offset:3072 sc1\n" \
M_"v[208:211], a[24:27], v[72:75], v[208:211]\n" \
"v_readlane_b32 s65, v11, 49\n" \
M_"v[140:143], a[24:27], a[216:219], v[140:143]\n" \
M_"v[136:139], a[24:27], a[200:203], v[136:139]\n" \
X_"a[24:25], v35 offset:41984\n" \
X_"a[26:27], v35 offset:44288\n" \
M_"v[144:147], a[28:31], a[200:203], v[144:147]\n" \
M_"v[148:151], a[28:31], a[216:219], v[148:151]\n" \
B_"v6, s[80:83], s66 offen offset:2048 lds\n" \
M_"v[216:219], a[28:31], v[72:75], v[216:219]\n" \
M_"v[220:223], a[28:31], v[96:99], v[220:223]\n" \
X_"a[28:29], v35 offset:52352\n" \
X_"a[30:31], v35 offset:54656\n" \
M_"v[228:231], a[96:99], v[96:99], v[228:231]\n" \
M_"v[224:227], a[96:99], v[72:75], v[224:227]\n" \
M_"v[156:159], a[96:99], a[216:219], v[156:159]\n" \
B_"v7, s[80:83], s66 offen offset:3072 lds\n" \
M_"v[152:155], a[96:99], a[200:203], v[152:155]\n" \
X_"a[96:97], v35 offset:42016\n" \
X_"a[98:99], v35 offset:44320\n" \
M_"v[160:163], a[100:103], a[200:203], v[160:163]\n" \
"s_add_u32 m0, s67, 0x2000\n" \
"v_readlane_b32 s67, v11, 51\n" \
M_"v[164:167], a[100:103], a[216:219], v[164:167]\n" \
M_"v[232:235], a[100:103], v[72:75], v[232:235]\n" \
M_"v[236:239], a[100:103], v[96:99], v[236:239]\n" \
X_"a[100:101], v35 offset:52384\n" \
X_"a[102:103], v35 offset:54688\n" \
M_"v[244:247], a[104:107], v[96:99], v[244:247]\n" \
B_"v8, s[80:83], s66 offen offset:0 lds\n" \
M_"v[240:243], a[104:107], v[72:75], v[240:243]\n" \
"s_add_u32 m0, s92, 0x0\n" \
"v_readlane_b32 s66, v11, 50\n" \
M_"v[172:175], a[104:107], a[216:219], v[172:175]\n" \
M_"v[168:171], a[104:107], a[200:203], v[168:171]\n" \
X_"a[104:105], v35 offset:42048\n" \
X_"a[106:107], v35 offset:44352\n" \
M_"v[176:179], a[108:111], a[200:203], v[176:179]\n" \
M_"v[180:183], a[108:111], a[216:219], v[180:183]\n" \
B_"v0, s[80:83], s73 offen offset:0 lds\n" \
M_"v[248:251], a[108:111], v[72:75], v[248:251]\n" \
M_"v[252:255], a[108:111], v[96:99], v[252:255]\n" \
X_"a[108:109], v35 offset:52416\n" \
X_"a[110:111], v35 offset:54720\n" \
M_"v[112:115], a[112:115], a[204:207], v[112:115]\n" \
M_"v[116:119], a[112:115], a[220:223], v[116:119]\n" \
M_"v[184:187], a[112:115], v[76:79], v[184:187]\n" \
B_"v1, s[80:83], s73 offen offset:1024 lds\n" \
M_"v[188:191], a[112:115], v[100:103], v[188:191]\n" \
X_"a[112:113], v35 offset:42080\n" \
X_"a[114:115], v35 offset:44384\n" \
M_"v[196:199], a[116:119], v[100:103], v[196:199]\n" \
M_"v[192:195], a[116:119], v[76:79], v[192:195]\n" \
"s_waitcnt vmcnt(18)\n" \
"s_mov_b64 exec, 1\n" \
"buffer_store_dword v39, off, s[56:59], s70 offset:0 sc1\n" \
"s_mov_b64 exec, -1\n" \
M_"v[124:127], a[116:119], a[220:223], v[124:127]\n" \
"v_readlane_b32 s70, v11, 54\n" \
M_"v[120:123], a[116:119], a[204:207], v[120:123]\n" \
X_"a[116:117], v35 offset:52448\n" \
X_"a[118:119], v35 offset:54752\n" \
M_"v[128:131], a[120:123], a[204:207], v[128:131]\n" \
M_"v[132:135], a[120:123], a[220:223], v[132:135]\n" \
B_"v2, s[80:83], s73 offen offset:2048 lds\n" \
M_"v[200:203], a[120:123], v[76:79], v[200:203]\n" \
M_"v[204:207], a[120:123], v[100:103], v[204:207]\n" \
X_"a[120:121], v35 offset:42112\n" \
X_"a[122:123], v35 offset:44416\n" \
M_"v[212:215], a[0:3], v[100:103], v[212:215]\n" \
M_"v[208:211], a[0:3], v[76:79], v[208:211]\n" \
M_"v[140:143], a[0:3], a[220:223], v[140:143]\n" \
B_"v3, s[80:83], s73 offen offset:3072 lds\n" \
M_"v[136:139], a[0:3], a[204:207], v[136:139]\n" \
X_"a[0:1], v35 offset:52480\n" \
"s_waitcnt lgkmcnt(7)\n" \
M_"v[144:147], a[4:7], a[204:207], v[144:147]\n" \
X_"a[2:3], v35 offset:54784\n" \
"s_add_u32 m0, s92, 0x1000\n" \
M_"v[148:151], a[4:7], a[220:223], v[148:151]\n" \
M_"v[216:219], a[4:7], v[76:79], v[216:219]\n" \
M_"v[220:223], a[4:7], v[100:103], v[220:223]\n" \
X_"a[4:5], v35 offset:42144\n" \
X_"a[6:7], v35 offset:44448\n" \
M_"v[228:231], a[8:11], v[100:103], v[228:231]\n" \
B_"v4, s[80:83], s73 offen offset:0 lds\n" \
M_"v[224:227], a[8:11], v[76:79], v[224:227]\n" \
M_"v[156:159], a[8:11], a[220:223], v[156:159]\n" \
M_"v[152:155], a[8:11], a[204:207], v[152:155]\n" \
X_"a[8:9], v35 offset:52512\n" \
X_"a[10:11], v35 offset:54816\n" \
M_"v[160:163], a[12:15], a[204:207], v[160:163]\n" \
M_"v[164:167], a[12:15], a[220:223], v[164:167]\n" \
B_"v5, s[80:83], s73 offen offset:1024 lds\n" \
M_"v[232:235], a[12:15], v[76:79], v[232:235]\n" \
M_"v[236:239], a[12:15], v[100:103], v[236:239]\n" \
X_"a[12:13], v35 offset:42176\n" \
X_"a[14:15], v35 offset:44480\n" \
M_"v[244:247], a[16:19], v[100:103], v[244:247]\n" \
M_"v[240:243], a[16:19], v[76:79], v[240:243]\n" \
M_"v[172:175], a[16:19], a[220:223], v[172:175]\n" \
B_"v6, s[80:83], s73 offen offset:2048 lds\n" \
M_"v[168:171], a[16:19], a[204:207], v[168:171]\n" \
X_"a[16:17], v35 offset:52544\n" \
X_"a[18:19], v35 offset:54848\n" \
M_"v[176:179], a[20:23], a[204:207], v[176:179]\n" \
M_"v[180:183], a[20:23], a[220:223], v[180:183]\n" \
M_"v[248:251], a[20:23], v[76:79], v[248:251]\n" \
M_"v[252:255], a[20:23], v[100:103], v[252:255]\n" \
X_"a[20:21], v35 offset:42208\n" \
X_"a[22:23], v35 offset:44512\n" \
M_"v[112:115], a[24:27], a[124:127], v[112:115]\n" \
B_"v7, s[80:83], s73 offen offset:3072 lds\n" \
M_"v[112:115], a[28:31], a[224:227], v[112:115]\n" \
"s_add_u32 m0, s92, 0x2000\n" \
"v_readlane_b32 s92, v11, 49\n" \
M_"v[116:119], a[24:27], a[228:231], v[116:119]\n" \
M_"v[116:119], a[28:31], a[232:235], v[116:119]\n" \
M_"v[184:187], a[24:27], a[236:239], v[184:187]\n" \
M_"v[184:187], a[28:31], a[240:243], v[184:187]\n" \
B_"v8, s[80:83], s73 offen offset:0 lds\n" \
M_"v[188:191], a[24:27], a[244:247], v[188:191]\n" \
X_"a[24:25], v35 offset:52576\n" \
X_"a[26:27], v35 offset:54880\n" \
M_"v[188:191], a[28:31], a[248:251], v[188:191]\n" \
"s_mov_b32 m0, s94\n" \
X_"a[28:29], v35 offset:42240\n" \
M_"v[196:199], a[96:99], a[244:247], v[196:199]\n" \
X_"a[30:31], v35 offset:44544\n" \
"v_readlane_b32 s73, v11, 6\n" \
M_"v[196:199], a[100:103], a[248:251], v[196:199]\n" \
M_"v[192:195], a[96:99], a[236:239], v[192:195]\n" \
B_"v9, s[84:87], 0 offen lds\n" \
M_"v[192:195], a[100:103], a[240:243], v[192:195]\n" \
M_"v[124:127], a[96:99], a[228:231], v[124:127]\n" \
M_"v[124:127], a[100:103], a[232:235], v[124:127]\n" \
M_"v[120:123], a[96:99], a[124:127], v[120:123]\n" \
X_"a[96:97], v35 offset:52608\n" \
X_"a[98:99], v35 offset:54912\n" \
M_"v[120:123], a[100:103], a[224:227], v[120:123]\n" \
X_"a[100:101], v33 offset:0\n" \
X_"a[102:103], v33 offset:4608\n" \
M_"v[128:131], a[104:107], a[124:127], v[128:131]\n" \
B_"a[128:131], v20, s[56:59], s68 offen offset:0 sc1\n" \
M_"v[128:131], a[108:111], a[224:227], v[128:131]\n" \
M_"v[132:135], a[104:107], a[228:231], v[132:135]\n" \
M_"v[132:135], a[108:111], a[232:235], v[132:135]\n" \
M_"v[200:203], a[104:107], a[236:239], v[200:203]\n" \
M_"v[200:203], a[108:111], a[240:243], v[200:203]\n" \
B_"a[132:135], v20, s[56:59], s68 offen offset:1024 sc1\n" \
M_"v[204:207], a[104:107], a[244:247], v[204:207]\n" \
X_"a[104:105], v33 offset:9216\n" \
X_"a[106:107], v33 offset:13824\n" \
M_"v[204:207], a[108:111], a[248:251], v[204:207]\n" \
X_"a[108:109], v33 offset:32\n" \
"s_waitcnt lgkmcnt(7)\n" \
M_"v[212:215], a[112:115], a[244:247], v[212:215]\n" \
X_"a[110:111], v33 offset:4640\n" \
M_"v[212:215], a[116:119], a[248:251], v[212:215]\n" \
M_"v[208:211], a[112:115], a[236:239], v[208:211]\n" \
"buffer_load_dword v47, v38, s[56:59], s71 offen sc1\n" \
M_"v[208:211], a[116:119], a[240:243], v[208:211]\n" \
M_"v[140:143], a[112:115], a[228:231], v[140:143]\n" \
M_"v[140:143], a[116:119], a[232:235], v[140:143]\n" \
M_"v[136:139], a[112:115], a[124:127], v[136:139]\n" \
X_"a[112:113], v33 offset:9248\n" \
X_"a[114:115], v33 offset:13856\n" \
M_"v[136:139], a[116:119], a[224:227], v[136:139]\n" \
X_"a[116:117], v33 offset:64\n" \
X_"a[118:119], v33 offset:4672\n" \
M_"v[144:147], a[120:123], a[124:127], v[144:147]\n" \
B_"a[144:147], v32, s[56:59], s68 offen offset:0 sc1\n" \
M_"v[144:147], a[0:3], a[224:227], v[144:147]\n" \
M_"v[148:151], a[120:123], a[228:231], v[148:151]\n" \
M_"v[148:151], a[0:3], a[232:235], v[148:151]\n" \
M_"v[216:219], a[120:123], a[236:239], v[216:219]\n" \
M_"v[216:219], a[0:3], a[240:243], v[216:219]\n" \
B_"a[148:151], v32, s[56:59], s68 offen offset:1024 sc1\n" \
M_"v[220:223], a[120:123], a[244:247], v[220:223]\n" \
X_"a[120:121], v33 offset:9280\n" \
X_"a[122:123], v33 offset:13888\n" \
M_"v[220:223], a[0:3], a[248:251], v[220:223]\n" \
X_"a[0:1], v33 offset:96\n" \
X_"a[2:3], v33 offset:4704\n" \
M_"v[228:231], a[4:7], a[244:247], v[228:231]\n" \
M_"v[228:231], a[8:11], a[248:251], v[228:231]\n" \
M_"v[224:227], a[4:7], a[236:239], v[224:227]\n" \
B_"a[152:155], v32, s[56:59], s68 offen offset:2048 sc1\n" \
M_"v[224:227], a[8:11], a[240:243], v[224:227]\n" \
M_"v[156:159], a[4:7], a[228:231], v[156:159]\n" \
M_"v[156:159], a[8:11], a[232:235], v[156:159]\n" \
M_"v[152:155], a[4:7], a[124:127], v[152:155]\n" \
X_"a[4:5], v33 offset:9312\n" \
X_"a[6:7], v33 offset:13920\n" \
M_"v[152:155], a[8:11], a[224:227], v[152:155]\n" \
X_"a[8:9], v33 offset:128\n" \
X_"a[10:11], v33 offset:4736\n" \
M_"v[160:163], a[12:15], a[124:127], v[160:163]\n" \
B_"a[156:159], v32, s[56:59], s68 offen offset:3072 sc1\n" \
M_"v[160:163], a[16:19], a[224:227], v[160:163]\n" \
M_"v[164:167], a[12:15], a[228:231], v[164:167]\n" \
M_"v[164:167], a[16:19], a[232:235], v[164:167]\n" \
M_"v[232:235], a[12:15], a[236:239], v[232:235]\n" \
M_"v[232:235], a[16:19], a[240:243], v[232:235]\n" \
B_"a[136:139], v20, s[56:59], s68 offen offset:2048 sc1\n" \
M_"v[236:239], a[12:15], a[244:247], v[236:239]\n" \
X_"a[12:13], v33 offset:9344\n" \
X_"a[14:15], v33 offset:13952\n" \
M_"v[236:239], a[16:19], a[248:251], v[236:239]\n" \
X_"a[16:17], v33 offset:160\n" \
X_"a[18:19], v33 offset:4768\n" \
M_"v[244:247], a[20:23], a[244:247], v[244:247]\n" \
M_"v[244:247], a[24:27], a[248:251], v[244:247]\n" \
M_"v[240:243], a[20:23], a[236:239], v[240:243]\n" \
B_"a[140:143], v20, s[56:59], s68 offen offset:3072 sc1\n" \
M_"v[240:243], a[24:27], a[240:243], v[240:243]\n" \
"v_readlane_b32 s68, v11, 52\n" \
M_"v[172:175], a[20:23], a[228:231], v[172:175]\n" \
M_"v[172:175], a[24:27], a[232:235], v[172:175]\n" \
M_"v[168:171], a[20:23], a[124:127], v[168:171]\n" \
X_"a[20:21], v33 offset:9376\n" \
X_"a[22:23], v33 offset:13984\n" \
M_"v[168:171], a[24:27], a[224:227], v[168:171]\n" \
X_"a[24:25], v33 offset:192\n" \
X_"a[26:27], v33 offset:4800\n" \
M_"v[176:179], a[28:31], a[124:127], v[176:179]\n" \
"s_waitcnt lgkmcnt(8)\n" \
M_"v[176:179], a[96:99], a[224:227], v[176:179]\n" \
B_"a[32:35], v20, s[56:59], s69 offen offset:0 sc1\n" \
M_"v[180:183], a[28:31], a[228:231], v[180:183]\n" \
M_"v[180:183], a[96:99], a[232:235], v[180:183]\n" \
M_"v[248:251], a[28:31], a[236:239], v[248:251]\n" \
M_"v[248:251], a[96:99], a[240:243], v[248:251]\n" \
M_"v[252:255], a[28:31], a[244:247], v[252:255]\n" \
X_"a[28:29], v33 offset:9408\n" \
X_"a[30:31], v33 offset:14016\n" \
M_"v[252:255], a[96:99], a[248:251], v[252:255]\n" \
X_"a[96:97], v33 offset:224\n" \
"s_waitcnt vmcnt(18)\n" \
M_"v[112:115], a[100:103], a[64:67], v[112:115]\n" \
B_"a[36:39], v20, s[56:59], s69 offen offset:1024 sc1\n" \
M_"v[112:115], a[104:107], a[68:71], v[112:115]\n" \
X_"a[98:99], v33 offset:4832\n" \
M_"v[116:119], a[100:103], a[80:83], v[116:119]\n" \
X_"a[100:101], v33 offset:9440\n" \
X_"a[102:103], v33 offset:14048\n" \
M_"v[116:119], a[104:107], a[84:87], v[116:119]\n" \
X_"a[104:105], v33 offset:256\n" \
X_"a[106:107], v33 offset:4864\n" \
M_"v[124:127], a[108:111], a[80:83], v[124:127]\n" \
M_"v[124:127], a[112:115], a[84:87], v[124:127]\n" \
B_"a[48:51], v32, s[56:59], s69 offen offset:0 sc1\n" \
M_"v[120:123], a[108:111], a[64:67], v[120:123]\n" \
X_"a[108:109], v33 offset:9472\n" \
X_"a[110:111], v33 offset:14080\n" \
M_"v[120:123], a[112:115], a[68:71], v[120:123]\n" \
X_"a[112:113], v33 offset:18432\n" \
X_"a[114:115], v33 offset:23040\n" \
M_"v[128:131], a[116:119], a[64:67], v[128:131]\n" \
M_"v[128:131], a[120:123], a[68:71], v[128:131]\n" \
M_"v[132:135], a[116:119], a[80:83], v[132:135]\n" \
B_"a[52:55], v32, s[56:59], s69 offen offset:1024 sc1\n" \
M_"v[132:135], a[120:123], a[84:87], v[132:135]\n" \
X_"a[116:117], v33 offset:27648\n" \
X_"a[118:119], v33 offset:32256\n" \
M_"v[140:143], a[0:3], a[80:83], v[140:143]\n" \
X_"a[120:121], v33 offset:18464\n" \
X_"a[122:123], v33 offset:23072\n" \
M_"v[140:143], a[4:7], a[84:87], v[140:143]\n" \
"s_waitcnt vmcnt(10)\n" \
"v_cmp_ne_u32_e32 vcc, s62, v47\n" \
M_"v[136:139], a[0:3], a[64:67], v[136:139]\n" \
X_"a[0:1], v33 offset:27680\n" \
".fill (L_ab + 36864 - .) / 4, 4, 0xBF800000\n" \
"L_ap9:\n" \
"s_cbranch_execz L_ap0\n" \
X_"a[2:3], v33 offset:32288\n" \
M_"v[136:139], a[4:7], a[68:71], v[136:139]\n" \
B_"a[56:59], v32, s[56:59], s69 offen offset:2048 sc1\n" \
M_"v[144:147], a[8:11], a[64:67], v[144:147]\n" \
X_"a[4:5], v33 offset:18496\n" \
"s_waitcnt lgkmcnt(13)\n" \
M_"v[144:147], a[12:15], a[68:71], v[144:147]\n" \
X_"a[6:7], v33 offset:23104\n" \
"s_cbranch_vccnz L_sb_%=\n" \
"L_rb_%=:\n" \
M_"v[148:151], a[8:11], a[80:83], v[148:151]\n" \
X_"a[8:9], v33 offset:27712\n" \
X_"a[10:11], v33 offset:32320\n" \
M_"v[148:151], a[12:15], a[84:87], v[148:151]\n" \
X_"a[12:13], v33 offset:18528\n" \
X_"a[14:15], v33 offset:23136\n" \
B_"a[60:63], v32, s[56:59], s69 offen offset:3072 sc1\n" \
M_"v[156:159], a[16:19], a[80:83], v[156:159]\n" \
"v_readlane_b32 s71, v11, 55\n" \
"v_cmp_eq_u32_e32 vcc, 0, v44\n" \
M_"v[156:159], a[20:23], a[84:87], v[156:159]\n" \
M_"v[152:155], a[16:19], a[64:67], v[152:155]\n" \
X_"a[16:17], v33 offset:27744\n" \
X_"a[18:19], v33 offset:32352\n" \
M_"v[152:155], a[20:23], a[68:71], v[152:155]\n" \
X_"a[20:21], v33 offset:18560\n" \
X_"a[22:23], v33 offset:23168\n" \
B_"a[40:43], v20, s[56:59], s69 offen offset:2048 sc1\n" \
M_"v[160:163], a[24:27], a[64:67], v[160:163]\n" \
"v_cndmask_b32_e32 v50, 0, v45, vcc\n" \
"v_cmp_eq_u32_e32 vcc, 1, v44\n" \
M_"v[160:163], a[28:31], a[68:71], v[160:163]\n" \
"v_and_b32_e32 v44, 0x3c0, v20\n" \
"v_cndmask_b32_e32 v51, 0, v45, vcc\n" \
M_"v[164:167], a[24:27], a[80:83], v[164:167]\n" \
X_"a[24:25], v33 offset:27776\n" \
X_"a[26:27], v33 offset:32384\n" \
M_"v[164:167], a[28:31], a[84:87], v[164:167]\n" \
X_"a[28:29], v33 offset:18592\n" \
X_"a[30:31], v33 offset:23200\n" \
B_"a[44:47], v20, s[56:59], s69 offen offset:3072 sc1\n" \
M_"v[172:175], a[96:99], a[80:83], v[172:175]\n" \
"v_readlane_b32 s69, v11, 53\n" \
"v_bfe_u32 v45, v20, 1, 5\n" \
M_"v[172:175], a[100:103], a[84:87], v[172:175]\n" \
"v_and_b32_e32 v45, 24, v45\n" \
"v_add_u32_e32 v45, 0x4800, v45\n" \
M_"v[168:171], a[96:99], a[64:67], v[168:171]\n" \
X_"a[96:97], v33 offset:27808\n" \
X_"a[98:99], v33 offset:32416\n" \
M_"v[168:171], a[100:103], a[68:71], v[168:171]\n" \
X_"a[100:101], v33 offset:18624\n" \
"s_waitcnt lgkmcnt(13)\n" \
M_"v[176:179], a[104:107], a[64:67], v[176:179]\n" \
X_"a[102:103], v33 offset:23232\n" \
"v_add_u32_e32 v36, v44, v45\n" \
M_"v[176:179], a[108:111], a[68:71], v[176:179]\n" \
B_"a[124:127], v20, s[56:59], s64 offen offset:0 sc1\n" \
M_"v[180:183], a[104:107], a[80:83], v[180:183]\n" \
X_"a[104:105], v33 offset:27840\n" \
X_"a[106:107], v33 offset:32448\n" \
M_"v[180:183], a[108:111], a[84:87], v[180:183]\n" \
X_"a[108:109], v33 offset:18656\n" \
X_"a[110:111], v33 offset:23264\n" \
M_"v[112:115], a[112:115], a[72:75], v[112:115]\n" \
M_"v[112:115], a[116:119], a[76:79], v[112:115]\n" \
M_"v[116:119], a[112:115], a[88:91], v[116:119]\n" \
X_"a[112:113], v33 offset:27872\n" \
X_"a[114:115], v33 offset:32480\n" \
M_"v[116:119], a[116:119], a[92:95], v[116:119]\n" \
X_"a[116:117], v33 offset:18688\n" \
X_"a[118:119], v33 offset:23296\n" \
M_"v[124:127], a[120:123], a[88:91], v[124:127]\n" \
B_"a[224:227], v20, s[56:59], s64 offen offset:1024 sc1\n" \
M_"v[124:127], a[0:3], a[92:95], v[124:127]\n" \
M_"v[120:123], a[120:123], a[72:75], v[120:123]\n" \
X_"a[120:121], v33 offset:27904\n" \
X_"a[122:123], v33 offset:32512\n" \
M_"v[120:123], a[0:3], a[76:79], v[120:123]\n" \
"s_barrier\n" \
M_"v[128:131], a[4:7], a[72:75], v[128:131]\n" \
X_"a[0:1], v33 offset:36864\n" \
X_"a[2:3], v33 offset:41472\n" \
M_"v[128:131], a[8:11], a[76:79], v[128:131]\n" \
B_"a[236:239], v32, s[56:59], s64 offen offset:0 sc1\n" \
M_"v[132:135], a[4:7], a[88:91], v[132:135]\n" \
X_"a[4:5], v33 offset:46080\n" \
X_"a[6:7], v33 offset:50688\n" \
M_"v[132:135], a[8:11], a[92:95], v[132:135]\n" \
X_"a[8:9], v33 offset:55296\n" \
"s_waitcnt lgkmcnt(15)\n" \
M_"v[140:143], a[12:15], a[88:91], v[140:143]\n" \
X_"a[10:11], v33 offset:59904\n" \
M_"v[140:143], a[16:19], a[92:95], v[140:143]\n" \
M_"v[136:139], a[12:15], a[72:75], v[136:139]\n" \
X_"a[12:13], v10 offset:0\n" \
X_"a[14:15], v10 offset:4608\n" \
M_"v[136:139], a[16:19], a[76:79], v[136:139]\n" \
X_"a[16:17], v34 offset:0\n" \
X_"a[18:19], v34 offset:4608\n" \
M_"v[144:147], a[20:23], a[72:75], v[144:147]\n" \
B_"a[240:243], v32, s[56:59], s64 offen offset:1024 sc1\n" \
M_"v[144:147], a[24:27], a[76:79], v[144:147]\n" \
M_"v[148:151], a[20:23], a[88:91], v[148:151]\n" \
X_"a[20:21], v34 offset:9216\n" \
X_"a[22:23], v34 offset:13824\n" \
M_"v[148:151], a[24:27], a[92:95], v[148:151]\n" \
X_"a[24:25], v34 offset:18432\n" \
X_"a[26:27], v34 offset:23040\n" \
M_"v[156:159], a[28:31], a[88:91], v[156:159]\n" \
M_"v[156:159], a[96:99], a[92:95], v[156:159]\n" \
B_"a[244:247], v32, s[56:59], s64 offen offset:2048 sc1\n" \
M_"v[152:155], a[28:31], a[72:75], v[152:155]\n" \
X_"a[28:29], v10 offset:9216\n" \
X_"a[30:31], v10 offset:13824\n" \
M_"v[152:155], a[96:99], a[76:79], v[152:155]\n" \
X_"a[96:97], v33 offset:36896\n" \
X_"a[98:99], v33 offset:41504\n" \
M_"v[160:163], a[100:103], a[72:75], v[160:163]\n" \
"s_waitcnt lgkmcnt(15)\n" \
M_"v[160:163], a[104:107], a[76:79], v[160:163]\n" \
M_"v[164:167], a[100:103], a[88:91], v[164:167]\n" \
X_"a[100:101], v33 offset:46112\n" \
X_"a[102:103], v33 offset:50720\n" \
M_"v[164:167], a[104:107], a[92:95], v[164:167]\n" \
X_"a[104:105], v33 offset:55328\n" \
X_"a[106:107], v33 offset:59936\n" \
M_"v[172:175], a[108:111], a[88:91], v[172:175]\n" \
B_"a[248:251], v32, s[56:59], s64 offen offset:3072 sc1\n" \
M_"v[172:175], a[112:115], a[92:95], v[172:175]\n" \
M_"v[168:171], a[108:111], a[72:75], v[168:171]\n" \
X_"a[108:109], v10 offset:32\n" \
X_"a[110:111], v10 offset:4640\n" \
M_"v[168:171], a[112:115], a[76:79], v[168:171]\n" \
X_"a[112:113], v34 offset:32\n" \
X_"a[114:115], v34 offset:4640\n" \
M_"v[176:179], a[116:119], a[72:75], v[176:179]\n" \
M_"v[176:179], a[120:123], a[76:79], v[176:179]\n" \
B_"a[228:231], v20, s[56:59], s64 offen offset:2048 sc1\n" \
M_"v[180:183], a[116:119], a[88:91], v[180:183]\n" \
X_"a[116:117], v34 offset:9248\n" \
X_"a[118:119], v34 offset:13856\n" \
M_"v[180:183], a[120:123], a[92:95], v[180:183]\n" \
X_"a[120:121], v34 offset:18464\n" \
X_"a[122:123], v34 offset:23072\n" \
M_"v[112:115], a[0:3], a[128:131], v[112:115]\n" \
"s_waitcnt lgkmcnt(15)\n" \
M_"v[112:115], a[4:7], a[132:135], v[112:115]\n" \
"s_waitcnt vmcnt(18)\n" \
M_"v[116:119], a[0:3], a[144:147], v[116:119]\n" \
X_"a[0:1], v10 offset:9248\n" \
X_"a[2:3], v10 offset:13856\n" \
M_"v[116:119], a[4:7], a[148:151], v[116:119]\n" \
X_"a[4:5], v33 offset:36928\n" \
X_"a[6:7], v33 offset:41536\n" \
M_"v[116:119], a[8:11], a[152:155], v[116:119]\n" \
"s_waitcnt vmcnt(17)\n" \
M_"v[116:119], a[12:15], a[156:159], v[116:119]\n" \
"s_waitcnt vmcnt(16)\n" \
M_"v[112:115], a[8:11], a[136:139], v[112:115]\n" \
X_"a[8:9], v33 offset:46144\n" \
"s_waitcnt vmcnt(15)\n" \
M_"v[112:115], a[12:15], a[140:143], v[112:115]\n" \
X_"a[10:11], v33 offset:50752\n" \
"s_waitcnt vmcnt(14)\n" \
M_"v[112:115], a[16:19], a[32:35], v[112:115]\n" \
X_"a[12:13], v33 offset:55360\n" \
"s_waitcnt vmcnt(13)\n" \
M_"v[112:115], a[20:23], a[36:39], v[112:115]\n" \
X_"a[14:15], v33 offset:59968\n" \
"s_waitcnt vmcnt(12)\n" \
M_"v[116:119], a[16:19], a[48:51], v[116:119]\n" \
X_"a[16:17], v10 offset:64\n" \
"s_waitcnt vmcnt(11)\n" \
M_"v[116:119], a[20:23], a[52:55], v[116:119]\n" \
X_"a[18:19], v10 offset:4672\n" \
"s_waitcnt vmcnt(10)\n" \
M_"v[116:119], a[24:27], a[56:59], v[116:119]\n" \
X_"a[20:21], v34 offset:64\n" \
X_"a[22:23], v34 offset:4672\n" \
B_"a[232:235], v20, s[56:59], s64 offen offset:3072 sc1\n" \
"s_waitcnt vmcnt(10) lgkmcnt(12)\n" \
M_"v[116:119], a[28:31], a[60:63], v[116:119]\n" \
B_"a[160:163], v20, s[56:59], s72 offen offset:0 sc1\n" \
"s_waitcnt vmcnt(10)\n" \
M_"v[112:115], a[24:27], a[40:43], v[112:115]\n" \
X_"a[24:25], v34 offset:9280\n" \
X_"a[26:27], v34 offset:13888\n" \
B_"a[164:167], v20, s[56:59], s72 offen offset:1024 sc1\n" \
"s_waitcnt vmcnt(10)\n" \
M_"v[112:115], a[28:31], a[44:47], v[112:115]\n" \
X_"a[28:29], v34 offset:18496\n" \
X_"a[30:31], v34 offset:23104\n" \
M_"v[120:123], a[96:99], a[128:131], v[120:123]\n" \
B_"a[176:179], v32, s[56:59], s72 offen offset:0 sc1\n" \
M_"v[120:123], a[100:103], a[132:135], v[120:123]\n" \
M_"v[124:127], a[96:99], a[144:147], v[124:127]\n" \
X_"a[96:97], v10 offset:9280\n" \
X_"a[98:99], v10 offset:13888\n" \
C_"v112, v112, v113\n" \
C_"v113, v114, v115\n" \
C_"v114, v116, v117\n" \
C_"v115, v118, v119\n" \
"buffer_store_dwordx4 v[112:115], v20, s[56:59], s65 offen offset:0 sc1\n" \
"s_add_u32 s65, s65, 0x3800\n" \
M_"v[124:127], a[100:103], a[148:151], v[124:127]\n" \
X_"a[100:101], v33 offset:36960\n" \
X_"a[102:103], v33 offset:41568\n" \
M_"v[124:127], a[104:107], a[152:155], v[124:127]\n" \
M_"v[124:127], a[108:111], a[156:159], v[124:127]\n" \
M_"v[120:123], a[104:107], a[136:139], v[120:123]\n" \
X_"a[104:105], v33 offset:46176\n" \
X_"a[106:107], v33 offset:50784\n" \
M_"v[120:123], a[108:111], a[140:143], v[120:123]\n" \
X_"a[108:109], v33 offset:55392\n" \
X_"a[110:111], v33 offset:60000\n" \
M_"v[120:123], a[112:115], a[32:35], v[120:123]\n" \
B_"a[180:183], v32, s[56:59], s72 offen offset:1024 sc1\n" \
M_"v[120:123], a[116:119], a[36:39], v[120:123]\n" \
M_"v[124:127], a[112:115], a[48:51], v[124:127]\n" \
X_"a[112:113], v10 offset:96\n" \
X_"a[114:115], v10 offset:4704\n" \
M_"v[124:127], a[116:119], a[52:55], v[124:127]\n" \
X_"a[116:117], v34 offset:96\n" \
X_"a[118:119], v34 offset:4704\n" \
M_"v[124:127], a[120:123], a[56:59], v[124:127]\n" \
"s_waitcnt lgkmcnt(12)\n" \
M_"v[124:127], a[0:3], a[60:63], v[124:127]\n" \
B_"a[168:171], v20, s[56:59], s72 offen offset:2048 sc1\n" \
M_"v[120:123], a[120:123], a[40:43], v[120:123]\n" \
X_"a[120:121], v34 offset:9312\n" \
X_"a[122:123], v34 offset:13920\n" \
M_"v[120:123], a[0:3], a[44:47], v[120:123]\n" \
X_"a[0:1], v34 offset:18528\n" \
X_"a[2:3], v34 offset:23136\n" \
M_"v[128:131], a[4:7], a[128:131], v[128:131]\n" \
M_"v[128:131], a[8:11], a[132:135], v[128:131]\n" \
M_"v[132:135], a[4:7], a[144:147], v[132:135]\n" \
X_"a[4:5], v10 offset:9312\n" \
X_"a[6:7], v10 offset:13920\n" \
M_"v[132:135], a[8:11], a[148:151], v[132:135]\n" \
C_"v120, v120, v121\n" \
C_"v121, v122, v123\n" \
C_"v122, v124, v125\n" \
C_"v123, v126, v127\n" \
"buffer_store_dwordx4 v[120:123], v20, s[56:59], s66 offen offset:0 sc1\n" \
"s_add_u32 s66, s66, 0x3000\n" \
M_"v[132:135], a[12:15], a[152:155], v[132:135]\n" \
X_"a[8:9], v33 offset:36992\n" \
X_"a[10:11], v33 offset:41600\n" \
M_"v[132:135], a[16:19], a[156:159], v[132:135]\n" \
M_"v[128:131], a[12:15], a[136:139], v[128:131]\n" \
X_"a[12:13], v33 offset:46208\n" \
X_"a[14:15], v33 offset:50816\n" \
M_"v[128:131], a[16:19], a[140:143], v[128:131]\n" \
X_"a[16:17], v33 offset:55424\n" \
X_"a[18:19], v33 offset:60032\n" \
M_"v[128:131], a[20:23], a[32:35], v[128:131]\n" \
B_"a[184:187], v32, s[56:59], s72 offen offset:2048 sc1\n" \
M_"v[128:131], a[24:27], a[36:39], v[128:131]\n" \
M_"v[132:135], a[20:23], a[48:51], v[132:135]\n" \
X_"a[20:21], v10 offset:128\n" \
X_"a[22:23], v10 offset:4736\n" \
M_"v[132:135], a[24:27], a[52:55], v[132:135]\n" \
X_"a[24:25], v34 offset:128\n" \
X_"a[26:27], v34 offset:4736\n" \
M_"v[132:135], a[28:31], a[56:59], v[132:135]\n" \
"s_waitcnt lgkmcnt(12)\n" \
M_"v[132:135], a[96:99], a[60:63], v[132:135]\n" \
B_"a[172:175], v20, s[56:59], s72 offen offset:3072 sc1\n" \
M_"v[128:131], a[28:31], a[40:43], v[128:131]\n" \
X_"a[28:29], v34 offset:9344\n" \
X_"a[30:31], v34 offset:13952\n" \
M_"v[128:131], a[96:99], a[44:47], v[128:131]\n" \
X_"a[96:97], v34 offset:18560\n" \
X_"a[98:99], v34 offset:23168\n" \
M_"v[136:139], a[100:103], a[128:131], v[136:139]\n" \
M_"v[136:139], a[104:107], a[132:135], v[136:139]\n" \
M_"v[140:143], a[100:103], a[144:147], v[140:143]\n" \
B_"a[188:191], v32, s[56:59], s72 offen offset:3072 sc1\n" \
M_"v[140:143], a[104:107], a[148:151], v[140:143]\n" \
"v_readlane_b32 s72, v11, 56\n" \
C_"v128, v128, v129\n" \
C_"v129, v130, v131\n" \
C_"v130, v132, v133\n" \
C_"v131, v134, v135\n" \
M_"v[140:143], a[108:111], a[152:155], v[140:143]\n" \
"buffer_store_dwordx4 v[128:131], v20, s[56:59], s67 offen offset:0 sc1\n" \
"s_add_u32 s67, s67, 0x2800\n" \
M_"v[140:143], a[112:115], a[156:159], v[140:143]\n" \
X_"a[100:101], v10 offset:9344\n" \
X_"a[102:103], v10 offset:13952\n" \
M_"v[136:139], a[108:111], a[136:139], v[136:139]\n" \
X_"a[104:105], v33 offset:37024\n" \
X_"a[106:107], v33 offset:41632\n" \
M_"v[136:139], a[112:115], a[140:143], v[136:139]\n" \
X_"a[108:109], v33 offset:46240\n" \
X_"a[110:111], v33 offset:50848\n" \
M_"v[136:139], a[116:119], a[32:35], v[136:139]\n" \
X_"a[112:113], v33 offset:55456\n" \
X_"a[114:115], v33 offset:60064\n" \
M_"v[136:139], a[120:123], a[36:39], v[136:139]\n" \
B_"a[192:195], v20, s[56:59], s93 offen offset:0 sc1\n" \
M_"v[140:143], a[116:119], a[48:51], v[140:143]\n" \
X_"a[116:117], v10 offset:160\n" \
X_"a[118:119], v10 offset:4768\n" \
M_"v[140:143], a[120:123], a[52:55], v[140:143]\n" \
X_"a[120:121], v34 offset:160\n" \
X_"a[122:123], v34 offset:4768\n" \
M_"v[140:143], a[0:3], a[56:59], v[140:143]\n" \
"s_waitcnt lgkmcnt(13)\n" \
M_"v[140:143], a[4:7], a[60:63], v[140:143]\n" \
M_"v[136:139], a[0:3], a[40:43], v[136:139]\n" \
B_"a[196:199], v20, s[56:59], s93 offen offset:1024 sc1\n" \
M_"v[136:139], a[4:7], a[44:47], v[136:139]\n" \
X_"a[0:1], v34 offset:9376\n" \
X_"a[2:3], v34 offset:13984\n" \
M_"v[144:147], a[8:11], a[128:131], v[144:147]\n" \
X_"a[4:5], v34 offset:18592\n" \
X_"a[6:7], v34 offset:23200\n" \
M_"v[144:147], a[12:15], a[132:135], v[144:147]\n" \
M_"v[148:151], a[8:11], a[144:147], v[148:151]\n" \
X_"a[8:9], v10 offset:9376\n" \
C_"v136, v136, v137\n" \
C_"v137, v138, v139\n" \
C_"v138, v140, v141\n" \
C_"v139, v142, v143\n" \
"buffer_store_dwordx4 v[136:139], v20, s[56:59], s68 offen offset:0 sc1\n" \
"s_add_u32 s68, s68, 0x2000\n" \
M_"v[148:151], a[12:15], a[148:151], v[148:151]\n" \
X_"a[10:11], v10 offset:13984\n" \
X_"a[12:13], v33 offset:37056\n" \
M_"v[148:151], a[16:19], a[152:155], v[148:151]\n" \
X_"a[14:15], v33 offset:41664\n" \
M_"v[148:151], a[20:23], a[156:159], v[148:151]\n" \
M_"v[144:147], a[16:19], a[136:139], v[144:147]\n" \
B_"a[208:211], v32, s[56:59], s93 offen offset:0 sc1\n" \
M_"v[144:147], a[20:23], a[140:143], v[144:147]\n" \
X_"a[16:17], v33 offset:46272\n" \
X_"a[18:19], v33 offset:50880\n" \
M_"v[144:147], a[24:27], a[32:35], v[144:147]\n" \
X_"a[20:21], v33 offset:55488\n" \
X_"a[22:23], v33 offset:60096\n" \
M_"v[144:147], a[28:31], a[36:39], v[144:147]\n" \
M_"v[148:151], a[24:27], a[48:51], v[148:151]\n" \
X_"a[24:25], v10 offset:192\n" \
X_"a[26:27], v10 offset:4800\n" \
B_"a[212:215], v32, s[56:59], s93 offen offset:1024 sc1\n" \
M_"v[148:151], a[28:31], a[52:55], v[148:151]\n" \
X_"a[28:29], v34 offset:192\n" \
"s_waitcnt lgkmcnt(13)\n" \
M_"v[148:151], a[96:99], a[56:59], v[148:151]\n" \
X_"a[30:31], v34 offset:4800\n" \
M_"v[148:151], a[100:103], a[60:63], v[148:151]\n" \
M_"v[144:147], a[96:99], a[40:43], v[144:147]\n" \
X_"a[96:97], v34 offset:9408\n" \
X_"a[98:99], v34 offset:14016\n" \
B_"a[216:219], v32, s[56:59], s93 offen offset:2048 sc1\n" \
M_"v[144:147], a[100:103], a[44:47], v[144:147]\n" \
X_"a[100:101], v34 offset:18624\n" \
X_"a[102:103], v34 offset:23232\n" \
M_"v[152:155], a[104:107], a[128:131], v[152:155]\n" \
M_"v[152:155], a[108:111], a[132:135], v[152:155]\n" \
M_"v[156:159], a[104:107], a[144:147], v[156:159]\n" \
X_"a[104:105], v10 offset:9408\n" \
X_"a[106:107], v10 offset:14016\n" \
B_"a[220:223], v32, s[56:59], s93 offen offset:3072 sc1\n" \
C_"v144, v144, v145\n" \
C_"v145, v146, v147\n" \
C_"v146, v148, v149\n" \
C_"v147, v150, v151\n" \
M_"v[156:159], a[108:111], a[148:151], v[156:159]\n" \
X_"a[108:109], v33 offset:256\n" \
X_"a[110:111], v33 offset:4864\n" \
"buffer_store_dwordx4 v[144:147], v20, s[56:59], s69 offen offset:0 sc1\n" \
"s_add_u32 s69, s69, 0x1800\n" \
M_"v[156:159], a[112:115], a[152:155], v[156:159]\n" \
M_"v[156:159], a[116:119], a[156:159], v[156:159]\n" \
M_"v[152:155], a[112:115], a[136:139], v[152:155]\n" \
X_"a[112:113], v33 offset:9472\n" \
X_"a[114:115], v33 offset:14080\n" \
M_"v[152:155], a[116:119], a[140:143], v[152:155]\n" \
B_"a[200:203], v20, s[56:59], s93 offen offset:2048 sc1\n" \
M_"v[152:155], a[120:123], a[32:35], v[152:155]\n" \
X_"a[116:117], v33 offset:18688\n" \
X_"a[118:119], v33 offset:23296\n" \
M_"v[152:155], a[0:3], a[36:39], v[152:155]\n" \
M_"v[156:159], a[120:123], a[48:51], v[156:159]\n" \
X_"a[120:121], v33 offset:27904\n" \
X_"a[122:123], v33 offset:32512\n" \
M_"v[156:159], a[0:3], a[52:55], v[156:159]\n" \
X_"a[0:1], v33 offset:37120\n" \
B_"a[204:207], v20, s[56:59], s93 offen offset:3072 sc1\n" \
"s_waitcnt lgkmcnt(11)\n" \
M_"v[156:159], a[4:7], a[56:59], v[156:159]\n" \
X_"a[2:3], v33 offset:41728\n" \
M_"v[156:159], a[8:11], a[60:63], v[156:159]\n" \
M_"v[152:155], a[4:7], a[40:43], v[152:155]\n" \
X_"a[4:5], v33 offset:46336\n" \
X_"a[6:7], v33 offset:50944\n" \
M_"v[152:155], a[8:11], a[44:47], v[152:155]\n" \
X_"a[8:9], v33 offset:55552\n" \
X_"a[10:11], v33 offset:60160\n" \
M_"v[160:163], a[12:15], a[128:131], v[160:163]\n" \
B_"v[64:67], v20, s[56:59], s95 offen offset:0 sc1\n" \
M_"v[160:163], a[16:19], a[132:135], v[160:163]\n" \
M_"v[164:167], a[12:15], a[144:147], v[164:167]\n" \
X_"a[12:13], v10 offset:256\n" \
X_"a[14:15], v10 offset:4864\n" \
C_"v152, v152, v153\n" \
C_"v153, v154, v155\n" \
C_"v154, v156, v157\n" \
C_"v155, v158, v159\n" \
"buffer_store_dwordx4 v[152:155], v20, s[56:59], s70 offen offset:0 sc1\n" \
"s_add_u32 s70, s70, 0x1000\n" \
M_"v[164:167], a[16:19], a[148:151], v[164:167]\n" \
X_"a[16:17], v34 offset:256\n" \
X_"a[18:19], v34 offset:4864\n" \
M_"v[164:167], a[20:23], a[152:155], v[164:167]\n" \
M_"v[164:167], a[24:27], a[156:159], v[164:167]\n" \
M_"v[160:163], a[20:23], a[136:139], v[160:163]\n" \
X_"a[20:21], v34 offset:9472\n" \
X_"a[22:23], v34 offset:14080\n" \
M_"v[160:163], a[24:27], a[140:143], v[160:163]\n" \
X_"a[24:25], v34 offset:18688\n" \
X_"a[26:27], v34 offset:23296\n" \
"s_waitcnt vmcnt(19)\n" \
"s_mov_b64 exec, 1\n" \
"buffer_store_dword v39, off, s[56:59], s65 offset:0 sc1\n" \
"s_mov_b64 exec, -1\n" \
M_"v[160:163], a[28:31], a[32:35], v[160:163]\n" \
M_"v[160:163], a[96:99], a[36:39], v[160:163]\n" \
M_"v[164:167], a[28:31], a[48:51], v[164:167]\n" \
X_"a[28:29], v10 offset:9472\n" \
X_"a[30:31], v10 offset:14080\n" \
M_"v[164:167], a[96:99], a[52:55], v[164:167]\n" \
X_"a[96:97], v33 offset:0\n" \
X_"a[98:99], v33 offset:4608\n" \
M_"v[164:167], a[100:103], a[56:59], v[164:167]\n" \
"s_waitcnt lgkmcnt(12)\n" \
M_"v[164:167], a[104:107], a[60:63], v[164:167]\n" \
B_"v[68:71], v20, s[56:59], s95 offen offset:1024 sc1\n" \
M_"v[160:163], a[100:103], a[40:43], v[160:163]\n" \
X_"a[100:101], v33 offset:9216\n" \
X_"a[102:103], v33 offset:13824\n" \
M_"v[160:163], a[104:107], a[44:47], v[160:163]\n" \
X_"a[104:105], v33 offset:18432\n" \
X_"a[106:107], v33 offset:23040\n" \
M_"v[248:251], a[108:111], a[124:127], v[248:251]\n" \
M_"v[248:251], a[112:115], a[224:227], v[248:251]\n" \
M_"v[252:255], a[108:111], a[236:239], v[252:255]\n" \
X_"a[108:109], v33 offset:27648\n" \
X_"a[110:111], v33 offset:32256\n" \
M_"v[252:255], a[112:115], a[240:243], v[252:255]\n" \
C_"v160, v160, v161\n" \
C_"v161, v162, v163\n" \
C_"v162, v164, v165\n" \
C_"v163, v166, v167\n" \
"buffer_store_dwordx4 v[160:163], v20, s[56:59], s71 offen offset:0 sc1\n" \
"s_add_u32 s71, s71, 0x800\n" \
M_"v[252:255], a[116:119], a[244:247], v[252:255]\n" \
X_"a[112:113], v33 offset:36864\n" \
X_"a[114:115], v33 offset:41472\n" \
M_"v[252:255], a[120:123], a[248:251], v[252:255]\n" \
M_"v[248:251], a[116:119], a[228:231], v[248:251]\n" \
X_"a[116:117], v33 offset:46080\n" \
X_"a[118:119], v33 offset:50688\n" \
"s_waitcnt vmcnt(19)\n" \
"s_mov_b64 exec, 1\n" \
"buffer_store_dword v39, off, s[56:59], s66 offset:4 sc1\n" \
"s_mov_b64 exec, -1\n" \
"v_readlane_b32 s66, v11, 50\n" \
M_"v[248:251], a[120:123], a[232:235], v[248:251]\n" \
X_"a[120:121], v33 offset:55296\n" \
X_"a[122:123], v33 offset:59904\n" \
M_"v[176:179], a[0:3], a[128:131], v[176:179]\n" \
M_"v[176:179], a[4:7], a[132:135], v[176:179]\n" \
M_"v[180:183], a[0:3], a[144:147], v[180:183]\n" \
B_"v[72:75], v32, s[56:59], s95 offen offset:0 sc1\n" \
M_"v[180:183], a[4:7], a[148:151], v[180:183]\n" \
M_"v[248:251], a[0:3], a[160:163], v[248:251]\n" \
M_"v[248:251], a[4:7], a[164:167], v[248:251]\n" \
M_"v[252:255], a[0:3], a[176:179], v[252:255]\n" \
X_"a[0:1], v10 offset:0\n" \
X_"a[2:3], v10 offset:4608\n" \
M_"v[252:255], a[4:7], a[180:183], v[252:255]\n" \
X_"a[4:5], v34 offset:0\n" \
"s_waitcnt vmcnt(20)\n" \
M_"v[252:255], a[8:11], a[184:187], v[252:255]\n" \
X_"a[6:7], v34 offset:4608\n" \
"s_waitcnt vmcnt(18) lgkmcnt(10)\n" \
M_"v[252:255], a[12:15], a[188:191], v[252:255]\n" \
B_"v[76:79], v32, s[56:59], s95 offen offset:1024 sc1\n" \
M_"v[248:251], a[8:11], a[168:171], v[248:251]\n" \
M_"v[248:251], a[12:15], a[172:175], v[248:251]\n" \
M_"v[180:183], a[8:11], a[152:155], v[180:183]\n" \
M_"v[180:183], a[12:15], a[156:159], v[180:183]\n" \
M_"v[176:179], a[8:11], a[136:139], v[176:179]\n" \
X_"a[8:9], v34 offset:9216\n" \
X_"a[10:11], v34 offset:13824\n" \
M_"v[176:179], a[12:15], a[140:143], v[176:179]\n" \
X_"a[12:13], v34 offset:18432\n" \
X_"a[14:15], v34 offset:23040\n" \
"s_waitcnt vmcnt(18)\n" \
"s_mov_b64 exec, 1\n" \
"buffer_store_dword v39, off, s[56:59], s67 offset:8 sc1\n" \
"s_mov_b64 exec, -1\n" \
"v_readlane_b32 s67, v11, 51\n" \
M_"v[176:179], a[16:19], a[32:35], v[176:179]\n" \
M_"v[176:179], a[20:23], a[36:39], v[176:179]\n" \
M_"v[180:183], a[16:19], a[48:51], v[180:183]\n" \
M_"v[180:183], a[20:23], a[52:55], v[180:183]\n" \
"s_waitcnt vmcnt(18)\n" \
M_"v[248:251], a[16:19], a[192:195], v[248:251]\n" \
"s_waitcnt vmcnt(17)\n" \
M_"v[248:251], a[20:23], a[196:199], v[248:251]\n" \
"s_waitcnt vmcnt(15)\n" \
M_"v[252:255], a[16:19], a[208:211], v[252:255]\n" \
X_"a[16:17], v10 offset:9216\n" \
"s_mov_b64 exec, 1\n" \
"buffer_store_dword v39, off, s[56:59], s68 offset:12 sc1\n" \
"s_mov_b64 exec, -1\n" \
"s_waitcnt vmcnt(15)\n" \
M_"v[252:255], a[20:23], a[212:215], v[252:255]\n" \
"v_readlane_b32 s68, v11, 52\n" \
"s_waitcnt vmcnt(14)\n" \
M_"v[252:255], a[24:27], a[216:219], v[252:255]\n" \
X_"a[18:19], v10 offset:13824\n" \
"s_waitcnt vmcnt(13)\n" \
M_"v[252:255], a[28:31], a[220:223], v[252:255]\n" \
X_"a[20:21], v33 offset:32\n" \
X_"a[22:23], v33 offset:4640\n" \
B_"v[80:83], v20, s[56:59], s96 offen offset:0 sc1\n" \
"s_waitcnt vmcnt(12)\n" \
M_"v[248:251], a[24:27], a[200:203], v[248:251]\n" \
"s_mov_b64 exec, 1\n" \
"buffer_store_dword v39, off, s[56:59], s69 offset:16 sc1\n" \
"s_mov_b64 exec, -1\n" \
"v_readlane_b32 s69, v11, 53\n" \
".fill (L_ab + 40960 - .) / 4, 4, 0xBF800000\n" \
"L_ap10:\n" \
"s_cbranch_execz L_ap0\n" \
"s_waitcnt vmcnt(12)\n" \
M_"v[248:251], a[28:31], a[204:207], v[248:251]\n" \
M_"v[180:183], a[24:27], a[56:59], v[180:183]\n" \
B_"v[84:87], v20, s[56:59], s96 offen offset:1024 sc1\n" \
M_"v[180:183], a[28:31], a[60:63], v[180:183]\n" \
M_"v[176:179], a[24:27], a[40:43], v[176:179]\n" \
X_"a[24:25], v33 offset:9248\n" \
X_"a[26:27], v33 offset:13856\n" \
M_"v[176:179], a[28:31], a[44:47], v[176:179]\n" \
X_"a[28:29], v33 offset:18464\n" \
C_"v248, v248, v249\n" \
C_"v249, v250, v251\n" \
C_"v250, v252, v253\n" \
C_"v251, v254, v255\n" \
"buffer_store_dwordx4 v[248:251], v20, s[56:59], s72 offen offset:1024 sc1\n" \
M_"v[184:187], a[96:99], a[124:127], v[184:187]\n" \
X_"a[30:31], v33 offset:23072\n" \
C_"v176, v176, v177\n" \
C_"v177, v178, v179\n" \
C_"v178, v180, v181\n" \
C_"v179, v182, v183\n" \
M_"v[184:187], a[100:103], a[224:227], v[184:187]\n" \
"buffer_store_dwordx4 v[176:179], v20, s[56:59], s72 offen offset:0 sc1\n" \
M_"v[188:191], a[96:99], a[236:239], v[188:191]\n" \
X_"a[96:97], v33 offset:27680\n" \
X_"a[98:99], v33 offset:32288\n" \
M_"v[188:191], a[100:103], a[240:243], v[188:191]\n" \
X_"a[100:101], v33 offset:36896\n" \
X_"a[102:103], v33 offset:41504\n" \
M_"v[188:191], a[104:107], a[244:247], v[188:191]\n" \
M_"v[188:191], a[108:111], a[248:251], v[188:191]\n" \
M_"v[184:187], a[104:107], a[228:231], v[184:187]\n" \
X_"a[104:105], v33 offset:46112\n" \
X_"a[106:107], v33 offset:50720\n" \
M_"v[184:187], a[108:111], a[232:235], v[184:187]\n" \
X_"a[108:109], v33 offset:55328\n" \
"s_waitcnt lgkmcnt(11)\n" \
M_"v[184:187], a[112:115], a[160:163], v[184:187]\n" \
B_"v[88:91], v32, s[56:59], s96 offen offset:0 sc1\n" \
M_"v[184:187], a[116:119], a[164:167], v[184:187]\n" \
X_"a[110:111], v33 offset:59936\n" \
M_"v[188:191], a[112:115], a[176:179], v[188:191]\n" \
X_"a[112:113], v10 offset:32\n" \
X_"a[114:115], v10 offset:4640\n" \
M_"v[188:191], a[116:119], a[180:183], v[188:191]\n" \
X_"a[116:117], v34 offset:32\n" \
X_"a[118:119], v34 offset:4640\n" \
M_"v[188:191], a[120:123], a[184:187], v[188:191]\n" \
M_"v[188:191], a[0:3], a[188:191], v[188:191]\n" \
B_"v[92:95], v32, s[56:59], s96 offen offset:1024 sc1\n" \
M_"v[184:187], a[120:123], a[168:171], v[184:187]\n" \
X_"a[120:121], v34 offset:9248\n" \
X_"a[122:123], v34 offset:13856\n" \
M_"v[184:187], a[0:3], a[172:175], v[184:187]\n" \
X_"a[0:1], v34 offset:18464\n" \
X_"a[2:3], v34 offset:23072\n" \
M_"v[184:187], a[4:7], a[192:195], v[184:187]\n" \
M_"v[184:187], a[8:11], a[196:199], v[184:187]\n" \
M_"v[188:191], a[4:7], a[208:211], v[188:191]\n" \
X_"a[4:5], v10 offset:9248\n" \
X_"a[6:7], v10 offset:13856\n" \
M_"v[188:191], a[8:11], a[212:215], v[188:191]\n" \
X_"a[8:9], v33 offset:64\n" \
X_"a[10:11], v33 offset:4672\n" \
M_"v[188:191], a[12:15], a[216:219], v[188:191]\n" \
B_"v[96:99], v20, s[56:59], s97 offen offset:0 sc1\n" \
M_"v[188:191], a[16:19], a[220:223], v[188:191]\n" \
M_"v[184:187], a[12:15], a[200:203], v[184:187]\n" \
X_"a[12:13], v33 offset:9280\n" \
X_"a[14:15], v33 offset:13888\n" \
M_"v[184:187], a[16:19], a[204:207], v[184:187]\n" \
"buffer_load_dword v37, v40, s[56:59], s74 offen sc1\n" \
M_"v[192:195], a[20:23], a[124:127], v[192:195]\n" \
X_"a[16:17], v33 offset:18496\n" \
"s_waitcnt lgkmcnt(13)\n" \
M_"v[192:195], a[24:27], a[224:227], v[192:195]\n" \
X_"a[18:19], v33 offset:23104\n" \
M_"v[196:199], a[20:23], a[236:239], v[196:199]\n" \
X_"a[20:21], v33 offset:27712\n" \
C_"v184, v184, v185\n" \
C_"v185, v186, v187\n" \
C_"v186, v188, v189\n" \
C_"v187, v190, v191\n" \
"buffer_store_dwordx4 v[184:187], v20, s[56:59], s92 offen offset:1024 sc1\n" \
"s_waitcnt vmcnt(18)\n" \
"s_mov_b64 exec, 1\n" \
"buffer_store_dword v39, off, s[56:59], s70 offset:20 sc1\n" \
"s_mov_b64 exec, -1\n" \
"s_add_u32 s92, s92, 0x3800\n" \
M_"v[196:199], a[24:27], a[240:243], v[196:199]\n" \
X_"a[22:23], v33 offset:32320\n" \
X_"a[24:25], v33 offset:36928\n" \
M_"v[196:199], a[28:31], a[244:247], v[196:199]\n" \
X_"a[26:27], v33 offset:41536\n" \
M_"v[196:199], a[96:99], a[248:251], v[196:199]\n" \
M_"v[192:195], a[28:31], a[228:231], v[192:195]\n" \
X_"a[28:29], v33 offset:46144\n" \
X_"a[30:31], v33 offset:50752\n" \
M_"v[192:195], a[96:99], a[232:235], v[192:195]\n" \
B_"v[100:103], v20, s[56:59], s97 offen offset:1024 sc1\n" \
M_"v[192:195], a[100:103], a[160:163], v[192:195]\n" \
X_"a[96:97], v33 offset:55360\n" \
X_"a[98:99], v33 offset:59968\n" \
M_"v[192:195], a[104:107], a[164:167], v[192:195]\n" \
M_"v[196:199], a[100:103], a[176:179], v[196:199]\n" \
X_"a[100:101], v10 offset:64\n" \
X_"a[102:103], v10 offset:4672\n" \
M_"v[196:199], a[104:107], a[180:183], v[196:199]\n" \
X_"a[104:105], v34 offset:64\n" \
X_"a[106:107], v34 offset:4672\n" \
M_"v[196:199], a[108:111], a[184:187], v[196:199]\n" \
B_"v[104:107], v32, s[56:59], s97 offen offset:0 sc1\n" \
M_"v[196:199], a[112:115], a[188:191], v[196:199]\n" \
M_"v[192:195], a[108:111], a[168:171], v[192:195]\n" \
X_"a[108:109], v34 offset:9280\n" \
X_"a[110:111], v34 offset:13888\n" \
M_"v[192:195], a[112:115], a[172:175], v[192:195]\n" \
X_"a[112:113], v34 offset:18496\n" \
"s_waitcnt lgkmcnt(14)\n" \
M_"v[192:195], a[116:119], a[192:195], v[192:195]\n" \
X_"a[114:115], v34 offset:23104\n" \
M_"v[192:195], a[120:123], a[196:199], v[192:195]\n" \
B_"v[108:111], v32, s[56:59], s97 offen offset:1024 sc1\n" \
M_"v[196:199], a[116:119], a[208:211], v[196:199]\n" \
X_"a[116:117], v10 offset:9280\n" \
X_"a[118:119], v10 offset:13888\n" \
M_"v[196:199], a[120:123], a[212:215], v[196:199]\n" \
X_"a[120:121], v33 offset:96\n" \
X_"a[122:123], v33 offset:4704\n" \
"s_waitcnt vmcnt(19)\n" \
"s_mov_b64 exec, 1\n" \
"buffer_store_dword v39, off, s[56:59], s71 offset:24 sc1\n" \
"s_mov_b64 exec, -1\n" \
M_"v[196:199], a[0:3], a[216:219], v[196:199]\n" \
M_"v[196:199], a[4:7], a[220:223], v[196:199]\n" \
M_"v[192:195], a[0:3], a[200:203], v[192:195]\n" \
X_"a[0:1], v33 offset:9312\n" \
X_"a[2:3], v33 offset:13920\n" \
M_"v[192:195], a[4:7], a[204:207], v[192:195]\n" \
X_"a[4:5], v33 offset:18528\n" \
X_"a[6:7], v33 offset:23136\n" \
M_"v[200:203], a[8:11], a[124:127], v[200:203]\n" \
M_"v[200:203], a[12:15], a[224:227], v[200:203]\n" \
M_"v[204:207], a[8:11], a[236:239], v[204:207]\n" \
X_"a[8:9], v33 offset:27744\n" \
X_"a[10:11], v33 offset:32352\n" \
M_"v[204:207], a[12:15], a[240:243], v[204:207]\n" \
C_"v192, v192, v193\n" \
C_"v193, v194, v195\n" \
C_"v194, v196, v197\n" \
C_"v195, v198, v199\n" \
"buffer_store_dwordx4 v[192:195], v20, s[56:59], s66 offen offset:1024 sc1\n" \
M_"v[204:207], a[16:19], a[244:247], v[204:207]\n" \
X_"a[12:13], v33 offset:36960\n" \
"s_waitcnt lgkmcnt(11)\n" \
M_"v[204:207], a[20:23], a[248:251], v[204:207]\n" \
X_"a[14:15], v33 offset:41568\n" \
M_"v[200:203], a[16:19], a[228:231], v[200:203]\n" \
X_"a[16:17], v33 offset:46176\n" \
X_"a[18:19], v33 offset:50784\n" \
M_"v[200:203], a[20:23], a[232:235], v[200:203]\n" \
X_"a[20:21], v33 offset:55392\n" \
X_"a[22:23], v33 offset:60000\n" \
M_"v[200:203], a[24:27], a[160:163], v[200:203]\n" \
M_"v[200:203], a[28:31], a[164:167], v[200:203]\n" \
M_"v[204:207], a[24:27], a[176:179], v[204:207]\n" \
X_"a[24:25], v10 offset:96\n" \
X_"a[26:27], v10 offset:4704\n" \
M_"v[204:207], a[28:31], a[180:183], v[204:207]\n" \
X_"a[28:29], v34 offset:96\n" \
X_"a[30:31], v34 offset:4704\n" \
M_"v[204:207], a[96:99], a[184:187], v[204:207]\n" \
M_"v[204:207], a[100:103], a[188:191], v[204:207]\n" \
M_"v[200:203], a[96:99], a[168:171], v[200:203]\n" \
X_"a[96:97], v34 offset:9312\n" \
X_"a[98:99], v34 offset:13920\n" \
M_"v[200:203], a[100:103], a[172:175], v[200:203]\n" \
X_"a[100:101], v34 offset:18528\n" \
X_"a[102:103], v34 offset:23136\n" \
M_"v[200:203], a[104:107], a[192:195], v[200:203]\n" \
M_"v[200:203], a[108:111], a[196:199], v[200:203]\n" \
M_"v[204:207], a[104:107], a[208:211], v[204:207]\n" \
X_"a[104:105], v10 offset:9312\n" \
X_"a[106:107], v10 offset:13920\n" \
M_"v[204:207], a[108:111], a[212:215], v[204:207]\n" \
X_"a[108:109], v33 offset:128\n" \
X_"a[110:111], v33 offset:4736\n" \
M_"v[204:207], a[112:115], a[216:219], v[204:207]\n" \
"s_waitcnt lgkmcnt(15)\n" \
M_"v[204:207], a[116:119], a[220:223], v[204:207]\n" \
M_"v[200:203], a[112:115], a[200:203], v[200:203]\n" \
X_"a[112:113], v33 offset:9344\n" \
X_"a[114:115], v33 offset:13952\n" \
M_"v[200:203], a[116:119], a[204:207], v[200:203]\n" \
X_"a[116:117], v33 offset:18560\n" \
X_"a[118:119], v33 offset:23168\n" \
M_"v[208:211], a[120:123], a[124:127], v[208:211]\n" \
M_"v[208:211], a[0:3], a[224:227], v[208:211]\n" \
M_"v[212:215], a[120:123], a[236:239], v[212:215]\n" \
X_"a[120:121], v33 offset:27776\n" \
X_"a[122:123], v33 offset:32384\n" \
M_"v[212:215], a[0:3], a[240:243], v[212:215]\n" \
C_"v200, v200, v201\n" \
C_"v201, v202, v203\n" \
C_"v202, v204, v205\n" \
C_"v203, v206, v207\n" \
"buffer_store_dwordx4 v[200:203], v20, s[56:59], s67 offen offset:1024 sc1\n" \
M_"v[212:215], a[4:7], a[244:247], v[212:215]\n" \
X_"a[0:1], v33 offset:36992\n" \
X_"a[2:3], v33 offset:41600\n" \
M_"v[212:215], a[8:11], a[248:251], v[212:215]\n" \
M_"v[208:211], a[4:7], a[228:231], v[208:211]\n" \
X_"a[4:5], v33 offset:46208\n" \
X_"a[6:7], v33 offset:50816\n" \
M_"v[208:211], a[8:11], a[232:235], v[208:211]\n" \
X_"a[8:9], v33 offset:55424\n" \
X_"a[10:11], v33 offset:60032\n" \
M_"v[208:211], a[12:15], a[160:163], v[208:211]\n" \
"s_waitcnt lgkmcnt(13)\n" \
M_"v[208:211], a[16:19], a[164:167], v[208:211]\n" \
M_"v[212:215], a[12:15], a[176:179], v[212:215]\n" \
"s_waitcnt vmcnt(8)\n" \
"v_cmp_eq_u32_e64 s[98:99], s62, v37\n" \
"s_mov_b64 exec, 3\n" \
"buffer_store_dword v39, v41, s[56:59], s73 offen sc1\n" \
"s_mov_b64 exec, -1\n" \
"s_andn2_b32 s75, 0x7f, s98\n" \
"s_mov_b32 s20, s75\n" \
"s_bitcmp1_b32 s20, 0\n" \
"s_cselect_b32 s93, 0x40000000, s12\n" \
"s_bitcmp1_b32 s20, 1\n" \
"s_cselect_b32 s94, 0x40000000, s12\n" \
"s_bitcmp1_b32 s20, 2\n" \
"s_cselect_b32 s95, 0x40000000, s13\n" \
"s_bitcmp1_b32 s20, 3\n" \
"s_cselect_b32 s96, 0x40000000, s13\n" \
"s_bitcmp1_b32 s20, 4\n" \
"s_cselect_b32 s97, 0x40000000, s14\n" \
"s_bitcmp1_b32 s20, 5\n" \
"s_cselect_b32 s64, 0x40000000, s14\n" \
"s_bitcmp1_b32 s20, 6\n" \
"s_cselect_b32 s65, 0x40000000, s15\n" \
"s_add_u32 s66, s66, 0x3000\n" \
"s_add_u32 s67, s67, 0x2800\n" \
M_"v[212:215], a[16:19], a[180:183], v[212:215]\n" \
B_"v[116:119], v20, s[56:59], s93 offen offset:0 sc1\n" \
M_"v[212:215], a[20:23], a[184:187], v[212:215]\n" \
X_"a[12:13], v10 offset:128\n" \
X_"a[14:15], v10 offset:4736\n" \
M_"v[212:215], a[24:27], a[188:191], v[212:215]\n" \
X_"a[16:17], v34 offset:128\n" \
X_"a[18:19], v34 offset:4736\n" \
M_"v[208:211], a[20:23], a[168:171], v[208:211]\n" \
X_"a[20:21], v34 offset:9344\n" \
X_"a[22:23], v34 offset:13952\n" \
M_"v[208:211], a[24:27], a[172:175], v[208:211]\n" \
X_"a[24:25], v34 offset:18560\n" \
X_"a[26:27], v34 offset:23168\n" \
M_"v[208:211], a[28:31], a[192:195], v[208:211]\n" \
M_"v[208:211], a[96:99], a[196:199], v[208:211]\n" \
M_"v[212:215], a[28:31], a[208:211], v[212:215]\n" \
X_"a[28:29], v10 offset:9344\n" \
X_"a[30:31], v10 offset:13952\n" \
M_"v[212:215], a[96:99], a[212:215], v[212:215]\n" \
X_"a[96:97], v33 offset:160\n" \
X_"a[98:99], v33 offset:4768\n" \
M_"v[212:215], a[100:103], a[216:219], v[212:215]\n" \
M_"v[212:215], a[104:107], a[220:223], v[212:215]\n" \
M_"v[208:211], a[100:103], a[200:203], v[208:211]\n" \
X_"a[100:101], v33 offset:9376\n" \
X_"a[102:103], v33 offset:13984\n" \
M_"v[208:211], a[104:107], a[204:207], v[208:211]\n" \
X_"a[104:105], v33 offset:18592\n" \
"s_waitcnt lgkmcnt(15)\n" \
M_"v[216:219], a[108:111], a[124:127], v[216:219]\n" \
X_"a[106:107], v33 offset:23200\n" \
M_"v[216:219], a[112:115], a[224:227], v[216:219]\n" \
B_"v[124:127], v20, s[56:59], s94 offen offset:2048 sc1\n" \
M_"v[220:223], a[108:111], a[236:239], v[220:223]\n" \
X_"a[108:109], v33 offset:27808\n" \
C_"v208, v208, v209\n" \
C_"v209, v210, v211\n" \
C_"v210, v212, v213\n" \
C_"v211, v214, v215\n" \
M_"v[220:223], a[112:115], a[240:243], v[220:223]\n" \
"buffer_store_dwordx4 v[208:211], v20, s[56:59], s68 offen offset:1024 sc1\n" \
"s_add_u32 s68, s68, 0x2000\n" \
M_"v[220:223], a[116:119], a[244:247], v[220:223]\n" \
X_"a[110:111], v33 offset:32416\n" \
X_"a[112:113], v33 offset:37024\n" \
M_"v[220:223], a[120:123], a[248:251], v[220:223]\n" \
X_"a[114:115], v33 offset:41632\n" \
M_"v[216:219], a[116:119], a[228:231], v[216:219]\n" \
X_"a[116:117], v33 offset:46240\n" \
X_"a[118:119], v33 offset:50848\n" \
M_"v[216:219], a[120:123], a[232:235], v[216:219]\n" \
X_"a[120:121], v33 offset:55456\n" \
X_"a[122:123], v33 offset:60064\n" \
M_"v[216:219], a[0:3], a[160:163], v[216:219]\n" \
M_"v[216:219], a[4:7], a[164:167], v[216:219]\n" \
M_"v[220:223], a[0:3], a[176:179], v[220:223]\n" \
X_"a[0:1], v10 offset:160\n" \
X_"a[2:3], v10 offset:4768\n" \
M_"v[220:223], a[4:7], a[180:183], v[220:223]\n" \
X_"a[4:5], v34 offset:160\n" \
X_"a[6:7], v34 offset:4768\n" \
M_"v[220:223], a[8:11], a[184:187], v[220:223]\n" \
"s_waitcnt lgkmcnt(13)\n" \
M_"v[220:223], a[12:15], a[188:191], v[220:223]\n" \
"s_waitcnt vmcnt(11)\n" \
"s_mov_b64 exec, 1\n" \
"buffer_store_dword v39, off, s[56:59], s92 offset:32 sc1\n" \
"s_mov_b64 exec, -1\n" \
M_"v[216:219], a[8:11], a[168:171], v[216:219]\n" \
X_"a[8:9], v34 offset:9376\n" \
X_"a[10:11], v34 offset:13984\n" \
M_"v[216:219], a[12:15], a[172:175], v[216:219]\n" \
X_"a[12:13], v34 offset:18592\n" \
X_"a[14:15], v34 offset:23200\n" \
M_"v[216:219], a[16:19], a[192:195], v[216:219]\n" \
M_"v[216:219], a[20:23], a[196:199], v[216:219]\n" \
B_"v[132:135], v20, s[56:59], s95 offen offset:0 sc1\n" \
M_"v[220:223], a[16:19], a[208:211], v[220:223]\n" \
X_"a[16:17], v10 offset:9376\n" \
X_"a[18:19], v10 offset:13984\n" \
M_"v[220:223], a[20:23], a[212:215], v[220:223]\n" \
X_"a[20:21], v33 offset:192\n" \
X_"a[22:23], v33 offset:4800\n" \
M_"v[220:223], a[24:27], a[216:219], v[220:223]\n" \
M_"v[220:223], a[28:31], a[220:223], v[220:223]\n" \
M_"v[216:219], a[24:27], a[200:203], v[216:219]\n" \
X_"a[24:25], v33 offset:9408\n" \
X_"a[26:27], v33 offset:14016\n" \
M_"v[216:219], a[28:31], a[204:207], v[216:219]\n" \
"buffer_load_dword v37, v40, s[56:59], s74 offen sc1\n" \
M_"v[224:227], a[96:99], a[124:127], v[224:227]\n" \
X_"a[28:29], v33 offset:18624\n" \
X_"a[30:31], v33 offset:23232\n" \
M_"v[224:227], a[100:103], a[224:227], v[224:227]\n" \
M_"v[228:231], a[96:99], a[236:239], v[228:231]\n" \
X_"a[96:97], v33 offset:27840\n" \
X_"a[98:99], v33 offset:32448\n" \
C_"v216, v216, v217\n" \
C_"v217, v218, v219\n" \
C_"v218, v220, v221\n" \
C_"v219, v222, v223\n" \
"buffer_store_dwordx4 v[216:219], v20, s[56:59], s69 offen offset:1024 sc1\n" \
"s_add_u32 s69, s69, 0x1800\n" \
M_"v[228:231], a[100:103], a[240:243], v[228:231]\n" \
X_"a[100:101], v33 offset:37056\n" \
"s_waitcnt lgkmcnt(12)\n" \
M_"v[228:231], a[104:107], a[244:247], v[228:231]\n" \
X_"a[102:103], v33 offset:41664\n" \
M_"v[228:231], a[108:111], a[248:251], v[228:231]\n" \
M_"v[224:227], a[104:107], a[228:231], v[224:227]\n" \
B_"v[140:143], v20, s[56:59], s96 offen offset:2048 sc1\n" \
M_"v[224:227], a[108:111], a[232:235], v[224:227]\n" \
X_"a[104:105], v33 offset:46272\n" \
X_"a[106:107], v33 offset:50880\n" \
M_"v[224:227], a[112:115], a[160:163], v[224:227]\n" \
X_"a[108:109], v33 offset:55488\n" \
X_"a[110:111], v33 offset:60096\n" \
M_"v[224:227], a[116:119], a[164:167], v[224:227]\n" \
M_"v[228:231], a[112:115], a[176:179], v[228:231]\n" \
X_"a[112:113], v10 offset:192\n" \
X_"a[114:115], v10 offset:4800\n" \
M_"v[228:231], a[116:119], a[180:183], v[228:231]\n" \
X_"a[116:117], v34 offset:192\n" \
X_"a[118:119], v34 offset:4800\n" \
M_"v[228:231], a[120:123], a[184:187], v[228:231]\n" \
M_"v[228:231], a[0:3], a[188:191], v[228:231]\n" \
M_"v[224:227], a[120:123], a[168:171], v[224:227]\n" \
X_"a[120:121], v34 offset:9408\n" \
X_"a[122:123], v34 offset:14016\n" \
M_"v[224:227], a[0:3], a[172:175], v[224:227]\n" \
X_"a[0:1], v34 offset:18624\n" \
X_"a[2:3], v34 offset:23232\n" \
"s_waitcnt vmcnt(10)\n" \
"s_mov_b64 exec, 1\n" \
"buffer_store_dword v39, off, s[56:59], s66 offset:36 sc1\n" \
"s_mov_b64 exec, -1\n" \
M_"v[224:227], a[4:7], a[192:195], v[224:227]\n" \
M_"v[224:227], a[8:11], a[196:199], v[224:227]\n" \
M_"v[228:231], a[4:7], a[208:211], v[228:231]\n" \
X_"a[4:5], v10 offset:9408\n" \
X_"a[6:7], v10 offset:14016\n" \
M_"v[228:231], a[8:11], a[212:215], v[228:231]\n" \
X_"a[8:9], v33 offset:224\n" \
"s_waitcnt lgkmcnt(15)\n" \
M_"v[228:231], a[12:15], a[216:219], v[228:231]\n" \
B_"v[148:151], v20, s[56:59], s97 offen offset:0 sc1\n" \
M_"v[228:231], a[16:19], a[220:223], v[228:231]\n" \
X_"a[10:11], v33 offset:4832\n" \
M_"v[224:227], a[12:15], a[200:203], v[224:227]\n" \
X_"a[12:13], v33 offset:9440\n" \
X_"a[14:15], v33 offset:14048\n" \
M_"v[224:227], a[16:19], a[204:207], v[224:227]\n" \
X_"a[16:17], v33 offset:18656\n" \
X_"a[18:19], v33 offset:23264\n" \
M_"v[232:235], a[20:23], a[124:127], v[232:235]\n" \
M_"v[232:235], a[24:27], a[224:227], v[232:235]\n" \
M_"v[236:239], a[20:23], a[236:239], v[236:239]\n" \
X_"a[20:21], v33 offset:27872\n" \
X_"a[22:23], v33 offset:32480\n" \
M_"v[236:239], a[24:27], a[240:243], v[236:239]\n" \
C_"v224, v224, v225\n" \
C_"v225, v226, v227\n" \
C_"v226, v228, v229\n" \
C_"v227, v230, v231\n" \
M_"v[236:239], a[28:31], a[244:247], v[236:239]\n" \
X_"a[24:25], v33 offset:37088\n" \
X_"a[26:27], v33 offset:41696\n" \
M_"v[236:239], a[96:99], a[248:251], v[236:239]\n" \
M_"v[232:235], a[28:31], a[228:231], v[232:235]\n" \
X_"a[28:29], v33 offset:46304\n" \
X_"a[30:31], v33 offset:50912\n" \
M_"v[232:235], a[96:99], a[232:235], v[232:235]\n" \
X_"a[96:97], v33 offset:55520\n" \
X_"a[98:99], v33 offset:60128\n" \
M_"v[232:235], a[100:103], a[160:163], v[232:235]\n" \
"s_waitcnt lgkmcnt(14)\n" \
M_"v[232:235], a[104:107], a[164:167], v[232:235]\n" \
B_"v[156:159], v20, s[56:59], s64 offen offset:2048 sc1\n" \
"v_readlane_b32 s64, v11, 54\n" \
M_"v[236:239], a[100:103], a[176:179], v[236:239]\n" \
X_"a[100:101], v10 offset:224\n" \
X_"a[102:103], v10 offset:4832\n" \
M_"v[236:239], a[104:107], a[180:183], v[236:239]\n" \
X_"a[104:105], v34 offset:224\n" \
X_"a[106:107], v34 offset:4832\n" \
"buffer_store_dwordx4 v[224:227], v20, s[56:59], s64 offen offset:1024 sc1\n" \
M_"v[236:239], a[108:111], a[184:187], v[236:239]\n" \
M_"v[236:239], a[112:115], a[188:191], v[236:239]\n" \
M_"v[232:235], a[108:111], a[168:171], v[232:235]\n" \
X_"a[108:109], v34 offset:9440\n" \
X_"a[110:111], v34 offset:14048\n" \
"s_waitcnt vmcnt(13)\n" \
"s_mov_b64 exec, 1\n" \
"buffer_store_dword v39, off, s[56:59], s67 offset:40 sc1\n" \
"s_mov_b64 exec, -1\n" \
M_"v[232:235], a[112:115], a[172:175], v[232:235]\n" \
X_"a[112:113], v34 offset:18656\n" \
X_"a[114:115], v34 offset:23264\n" \
M_"v[232:235], a[116:119], a[192:195], v[232:235]\n" \
M_"v[232:235], a[120:123], a[196:199], v[232:235]\n" \
M_"v[236:239], a[116:119], a[208:211], v[236:239]\n" \
X_"a[116:117], v10 offset:9440\n" \
X_"a[118:119], v10 offset:14048\n" \
M_"v[236:239], a[120:123], a[212:215], v[236:239]\n" \
X_"a[120:121], v36 offset:0\n" \
X_"a[122:123], v36 offset:1024\n" \
M_"v[236:239], a[0:3], a[216:219], v[236:239]\n" \
M_"v[236:239], a[4:7], a[220:223], v[236:239]\n" \
M_"v[232:235], a[0:3], a[200:203], v[232:235]\n" \
X_"a[0:1], v36 offset:2048\n" \
X_"a[2:3], v36 offset:3072\n" \
M_"v[232:235], a[4:7], a[204:207], v[232:235]\n" \
X_"a[4:5], v36 offset:32\n" \
B_"v[164:167], v20, s[56:59], s65 offen offset:0 sc1\n" \
"v_readlane_b32 s65, v11, 55\n" \
"s_waitcnt lgkmcnt(11)\n" \
M_"v[240:243], a[8:11], a[124:127], v[240:243]\n" \
X_"a[6:7], v36 offset:1056\n" \
M_"v[240:243], a[12:15], a[224:227], v[240:243]\n" \
M_"v[244:247], a[8:11], a[236:239], v[244:247]\n" \
C_"v232, v232, v233\n" \
C_"v233, v234, v235\n" \
C_"v234, v236, v237\n" \
C_"v235, v238, v239\n" \
"buffer_store_dwordx4 v[232:235], v20, s[56:59], s65 offen offset:1024 sc1\n" \
M_"v[244:247], a[12:15], a[240:243], v[244:247]\n" \
X_"a[8:9], v36 offset:2080\n" \
X_"a[10:11], v36 offset:3104\n" \
M_"v[244:247], a[16:19], a[244:247], v[244:247]\n" \
M_"v[244:247], a[20:23], a[248:251], v[244:247]\n" \
M_"v[240:243], a[16:19], a[228:231], v[240:243]\n" \
M_"v[240:243], a[20:23], a[232:235], v[240:243]\n" \
M_"v[168:171], a[24:27], a[128:131], v[168:171]\n" \
M_"v[168:171], a[28:31], a[132:135], v[168:171]\n" \
M_"v[172:175], a[24:27], a[144:147], v[172:175]\n" \
"s_waitcnt vmcnt(9)\n" \
"v_cmp_eq_u32_e64 s[98:99], s62, v37\n" \
"buffer_load_dword v37, v40, s[56:59], s74 offen sc1\n" \
"s_mov_b64 exec, 1\n" \
"buffer_store_dword v39, off, s[56:59], s68 offset:44 sc1\n" \
"s_mov_b64 exec, -1\n" \
"s_or_b32 s75, 0x300, s100\n" \
"s_andn2_b32 s75, s75, s98\n" \
"s_mov_b32 s21, s75\n" \
"s_bitcmp1_b32 s21, 16\n" \
"s_cselect_b32 s70, 0x40000000, s16\n" \
"s_bitcmp1_b32 s21, 17\n" \
"s_cselect_b32 s71, 0x40000000, s16\n" \
"s_bitcmp1_b32 s21, 18\n" \
"s_cselect_b32 s72, 0x40000000, s17\n" \
"s_bitcmp1_b32 s21, 19\n" \
"s_cselect_b32 s73, 0x40000000, s17\n" \
"s_bitcmp1_b32 s21, 20\n" \
"s_cselect_b32 s92, 0x40000000, s18\n" \
"s_bitcmp1_b32 s21, 21\n" \
"s_cselect_b32 s93, 0x40000000, s18\n" \
"s_bitcmp1_b32 s21, 22\n" \
"s_cselect_b32 s94, 0x40000000, s19\n" \
"s_bitcmp1_b32 s21, 23\n" \
"s_cselect_b32 s95, 0x40000000, s19\n" \
"s_bitcmp1_b32 s21, 8\n" \
"s_cselect_b32 s96, 0x40000000, s12\n" \
"s_bitcmp1_b32 s21, 9\n" \
"s_cselect_b32 s97, 0x40000000, s12\n" \
"s_add_u32 s64, s64, 0x1000\n" \
"s_add_u32 s65, s65, 0x800\n" \
M_"v[172:175], a[28:31], a[148:151], v[172:175]\n" \
B_"v[112:115], v20, s[56:59], s70 offen offset:0 sc1\n" \
M_"v[240:243], a[24:27], a[160:163], v[240:243]\n" \
M_"v[240:243], a[28:31], a[164:167], v[240:243]\n" \
M_"v[244:247], a[24:27], a[176:179], v[244:247]\n" \
M_"v[244:247], a[28:31], a[180:183], v[244:247]\n" \
B_"v[184:187], v20, s[56:59], s71 offen offset:2048 sc1\n" \
M_"v[244:247], a[96:99], a[184:187], v[244:247]\n" \
"v_readlane_b32 s71, v11, 57\n" \
M_"v[244:247], a[100:103], a[188:191], v[244:247]\n" \
M_"v[240:243], a[96:99], a[168:171], v[240:243]\n" \
M_"v[240:243], a[100:103], a[172:175], v[240:243]\n" \
B_"v[120:123], v20, s[56:59], s72 offen offset:0 sc1\n" \
M_"v[172:175], a[96:99], a[152:155], v[172:175]\n" \
"v_readlane_b32 s72, v11, 48\n" \
M_"v[172:175], a[100:103], a[156:159], v[172:175]\n" \
M_"v[168:171], a[96:99], a[136:139], v[168:171]\n" \
M_"v[168:171], a[100:103], a[140:143], v[168:171]\n" \
B_"v[192:195], v20, s[56:59], s73 offen offset:2048 sc1\n" \
M_"v[168:171], a[104:107], a[32:35], v[168:171]\n" \
"v_readlane_b32 s73, v11, 46\n" \
"s_waitcnt lgkmcnt(0)\n" \
M_"v[168:171], a[108:111], a[36:39], v[168:171]\n" \
M_"v[172:175], a[104:107], a[48:51], v[172:175]\n" \
M_"v[172:175], a[108:111], a[52:55], v[172:175]\n" \
B_"v[128:131], v20, s[56:59], s92 offen offset:0 sc1\n" \
M_"v[240:243], a[104:107], a[192:195], v[240:243]\n" \
"v_readlane_b32 s92, v11, 47\n" \
M_"v[240:243], a[108:111], a[196:199], v[240:243]\n" \
M_"v[244:247], a[104:107], a[208:211], v[244:247]\n" \
M_"v[244:247], a[108:111], a[212:215], v[244:247]\n" \
B_"v[200:203], v20, s[56:59], s93 offen offset:2048 sc1\n" \
M_"v[244:247], a[112:115], a[216:219], v[244:247]\n" \
M_"v[244:247], a[116:119], a[220:223], v[244:247]\n" \
M_"v[240:243], a[112:115], a[200:203], v[240:243]\n" \
M_"v[240:243], a[116:119], a[204:207], v[240:243]\n" \
".fill (L_ab + 45056 - .) / 4, 4, 0xBF800000\n" \
"L_ap11:\n" \
"s_cbranch_execz L_ap0\n" \
B_"v[176:179], v20, s[56:59], s94 offen offset:0 sc1\n" \
"s_waitcnt vmcnt(17)\n" \
"s_mov_b64 exec, 1\n" \
"buffer_store_dword v39, off, s[56:59], s69 offset:48 sc1\n" \
"s_mov_b64 exec, -1\n" \
M_"v[172:175], a[112:115], a[56:59], v[172:175]\n" \
M_"v[172:175], a[116:119], a[60:63], v[172:175]\n" \
M_"v[168:171], a[112:115], a[40:43], v[168:171]\n" \
M_"v[168:171], a[116:119], a[44:47], v[168:171]\n" \
B_"v[248:251], v20, s[56:59], s95 offen offset:2048 sc1\n" \
M_"v[168:171], a[120:123], v[64:67], v[168:171]\n" \
M_"v[172:175], a[120:123], v[72:75], v[172:175]\n" \
M_"v[240:243], a[120:123], v[80:83], v[240:243]\n" \
M_"v[244:247], a[120:123], v[88:91], v[244:247]\n" \
B_"v[188:191], v20, s[56:59], s96 offen offset:1024 sc1\n" \
M_"v[168:171], a[0:3], v[68:71], v[168:171]\n" \
M_"v[172:175], a[0:3], v[76:79], v[172:175]\n" \
M_"v[240:243], a[0:3], v[84:87], v[240:243]\n" \
M_"v[244:247], a[0:3], v[92:95], v[244:247]\n" \
B_"v[196:199], v20, s[56:59], s97 offen offset:3072 sc1\n" \
M_"v[56:59], a[4:7], v[96:99], 0\n" \
M_"v[60:63], a[4:7], v[104:107], 0\n" \
M_"v[56:59], a[8:11], v[100:103], v[56:59]\n" \
M_"v[60:63], a[8:11], v[108:111], v[60:63]\n" \
"s_waitcnt vmcnt(16)\n" \
"s_mov_b64 exec, 1\n" \
"buffer_store_dword v39, off, s[56:59], s64 offset:52 sc1\n" \
"s_mov_b64 exec, -1\n" \
"s_waitcnt vmcnt(14)\n" \
"s_mov_b64 exec, 1\n" \
"buffer_store_dword v39, off, s[56:59], s65 offset:56 sc1\n" \
"s_mov_b64 exec, -1\n" \
"s_cmp_lg_u32 s20, 0\n" \
"s_cbranch_scc1 L_sna_%=\n" \
"L_rna_%=:\n" \
"s_cmp_lg_u32 s21, 0\n" \
"s_cbranch_scc1 L_snx_%=\n" \
"L_rnx_%=:\n" \
"s_waitcnt vmcnt(14)\n" \
"v_cmp_eq_u32_e64 s[98:99], s62, v37\n" \
M_"v[168:171], v[50:53], v[116:119], v[168:171]\n" \
"s_andn2_b32 s75, 0x400, s98\n" \
"s_cbranch_scc1 L_sx0_%=\n" \
"L_rx0_%=:\n" \
"s_add_u32 s66, s63, 0x1000\n" \
M_"v[172:175], v[48:51], v[116:119], v[172:175]\n" \
B_"v[204:207], v20, s[56:59], s66 offen offset:1024 sc1\n" \
"s_andn2_b32 s75, 0x800, s98\n" \
"s_cbranch_scc1 L_sx1_%=\n" \
"L_rx1_%=:\n" \
"s_add_u32 s67, s63, 0x1800\n" \
M_"v[168:171], v[50:53], v[124:127], v[168:171]\n" \
M_"v[172:175], v[48:51], v[124:127], v[172:175]\n" \
M_"v[168:171], v[50:53], v[132:135], v[168:171]\n" \
B_"v[212:215], v20, s[56:59], s67 offen offset:1024 sc1\n" \
"s_andn2_b32 s75, 0x1000, s98\n" \
"s_cbranch_scc1 L_sx2_%=\n" \
"L_rx2_%=:\n" \
"s_add_u32 s68, s63, 0x2000\n" \
M_"v[172:175], v[48:51], v[132:135], v[172:175]\n" \
M_"v[168:171], v[50:53], v[140:143], v[168:171]\n" \
M_"v[172:175], v[48:51], v[140:143], v[172:175]\n" \
B_"v[220:223], v20, s[56:59], s68 offen offset:1024 sc1\n" \
"s_andn2_b32 s75, 0x2000, s98\n" \
"s_cbranch_scc1 L_sx3_%=\n" \
"L_rx3_%=:\n" \
"s_add_u32 s69, s63, 0x2800\n" \
M_"v[168:171], v[50:53], v[148:151], v[168:171]\n" \
M_"v[172:175], v[48:51], v[148:151], v[172:175]\n" \
M_"v[168:171], v[50:53], v[156:159], v[168:171]\n" \
B_"v[228:231], v20, s[56:59], s69 offen offset:1024 sc1\n" \
"s_andn2_b32 s75, 0x4000, s98\n" \
"s_cbranch_scc1 L_sx4_%=\n" \
"L_rx4_%=:\n" \
"s_add_u32 s70, s63, 0x3000\n" \
M_"v[172:175], v[48:51], v[156:159], v[172:175]\n" \
M_"v[168:171], v[50:53], v[164:167], v[168:171]\n" \
M_"v[172:175], v[48:51], v[164:167], v[172:175]\n" \
B_"v[236:239], v20, s[56:59], s70 offen offset:1024 sc1\n" \
"s_waitcnt vmcnt(17)\n" \
M_"v[56:59], v[50:53], v[112:115], v[56:59]\n" \
M_"v[60:63], v[48:51], v[112:115], v[60:63]\n" \
"s_waitcnt vmcnt(16)\n" \
M_"v[56:59], v[50:53], v[184:187], v[56:59]\n" \
M_"v[60:63], v[48:51], v[184:187], v[60:63]\n" \
C_"v168, v168, v169\n" \
"s_waitcnt vmcnt(15)\n" \
M_"v[56:59], v[50:53], v[120:123], v[56:59]\n" \
C_"v172, v172, v173\n" \
C_"v169, v170, v171\n" \
M_"v[60:63], v[48:51], v[120:123], v[60:63]\n" \
C_"v173, v174, v175\n" \
"s_waitcnt vmcnt(14)\n" \
M_"v[56:59], v[50:53], v[192:195], v[56:59]\n" \
"buffer_store_dwordx2 v[168:169], v42, s[88:91], s73 offen sc1\n" \
M_"v[60:63], v[48:51], v[192:195], v[60:63]\n" \
"s_waitcnt vmcnt(14)\n" \
M_"v[56:59], v[50:53], v[128:131], v[56:59]\n" \
"buffer_store_dwordx2 v[172:173], v43, s[88:91], s73 offen sc1\n" \
M_"v[60:63], v[48:51], v[128:131], v[60:63]\n" \
"s_waitcnt vmcnt(14)\n" \
M_"v[56:59], v[50:53], v[200:203], v[56:59]\n" \
M_"v[60:63], v[48:51], v[200:203], v[60:63]\n" \
"s_waitcnt vmcnt(13)\n" \
M_"v[56:59], v[50:53], v[176:179], v[56:59]\n" \
M_"v[60:63], v[48:51], v[176:179], v[60:63]\n" \
"s_waitcnt vmcnt(11)\n" \
M_"v[56:59], v[50:53], v[248:251], v[56:59]\n" \
M_"v[60:63], v[48:51], v[248:251], v[60:63]\n" \
"s_waitcnt vmcnt(10)\n" \
M_"v[240:243], v[50:53], v[188:191], v[240:243]\n" \
M_"v[244:247], v[48:51], v[188:191], v[244:247]\n" \
"s_waitcnt vmcnt(9)\n" \
M_"v[240:243], v[50:53], v[196:199], v[240:243]\n" \
M_"v[244:247], v[48:51], v[196:199], v[244:247]\n" \
"s_mov_b64 exec, 1\n" \
"buffer_store_dword v46, off, s[56:59], s71 offset:0 sc1\n" \
"s_mov_b64 exec, -1\n" \
C_"v56, v56, v57\n" \
C_"v60, v60, v61\n" \
C_"v57, v58, v59\n" \
C_"v61, v62, v63\n" \
"buffer_store_dwordx2 v[56:57], v42, s[88:91], s72 offen sc1\n" \
"buffer_store_dwordx2 v[60:61], v43, s[88:91], s72 offen sc1\n" \
"s_waitcnt vmcnt(9)\n" \
M_"v[240:243], v[50:53], v[204:207], v[240:243]\n" \
M_"v[244:247], v[48:51], v[204:207], v[244:247]\n" \
"s_waitcnt vmcnt(8)\n" \
M_"v[240:243], v[50:53], v[212:215], v[240:243]\n" \
M_"v[244:247], v[48:51], v[212:215], v[244:247]\n" \
"s_waitcnt vmcnt(7)\n" \
M_"v[240:243], v[50:53], v[220:223], v[240:243]\n" \
M_"v[244:247], v[48:51], v[220:223], v[244:247]\n" \
"s_waitcnt vmcnt(6)\n" \
M_"v[240:243], v[50:53], v[228:231], v[240:243]\n" \
M_"v[244:247], v[48:51], v[228:231], v[244:247]\n" \
"s_waitcnt vmcnt(5)\n" \
M_"v[240:243], v[50:53], v[236:239], v[240:243]\n" \
M_"v[244:247], v[48:51], v[236:239], v[244:247]\n" \
"s_nop 6\n" \
C_"v240, v240, v241\n" \
C_"v244, v244, v245\n" \
C_"v241, v242, v243\n" \
C_"v245, v246, v247\n" \
"buffer_store_dwordx2 v[240:241], v42, s[88:91], s92 offen sc1\n" \
"buffer_store_dwordx2 v[244:245], v43, s[88:91], s92 offen sc1\n" \
"L_fin_%=:\n"
#define ATT_VCLOB "v21","v22","v23","v24","v25","v26","v27","v28","v29","v32","v33","v34","v35","v36","v37","v38","v39","v40","v41","v42","v43","v44","v45","v46","v47","v48","v49","v50","v51","v52","v53","v54","v55","v56","v57","v58","v59","v60","v61","v62","v63","v64","v65","v66","v67","v68","v69","v70","v71","v72","v73","v74","v75","v76","v77","v78","v79","v80","v81","v82","v83","v84","v85","v86","v87","v88","v89","v90","v91","v92","v93","v94","v95","v96","v97","v98","v99","v100","v101","v102","v103","v104","v105","v106","v107","v108","v109","v110","v111","v112","v113","v114","v115","v116","v117","v118","v119","v120","v121","v122","v123","v124","v125","v126","v127","v128","v129","v130","v131","v132","v133","v134","v135","v136","v137","v138","v139","v140","v141","v142","v143","v144","v145","v146","v147","v148","v149","v150","v151","v152","v153","v154","v155","v156","v157","v158","v159","v160","v161","v162","v163","v164","v165","v166","v167","v168","v169","v170","v171","v172","v173","v174","v175","v176","v177","v178","v179","v180","v181","v182","v183","v184","v185","v186","v187","v188","v189","v190","v191","v192","v193","v194","v195","v196","v197","v198","v199","v200","v201","v202","v203","v204","v205","v206","v207","v208","v209","v210","v211","v212","v213","v214","v215","v216","v217","v218","v219","v220","v221","v222","v223","v224","v225","v226","v227","v228","v229","v230","v231","v232","v233","v234","v235","v236","v237","v238","v239","v240","v241","v242","v243","v244","v245","v246","v247","v248","v249","v250","v251","v252","v253","v254","v255"
#define ATT_SCLOB "s63","s64","s65","s66","s67","s68","s69","s70","s71","s72","s73","s74","s75","s76","s77","s78","s79","s80","s81","s82","s83","s84","s85","s86","s87","s88","s89","s90","s91","s92","s93","s95","s96","s97","s98","s99","s100","s101"
#define ATT_WTCH 0
#define ATT_PEEL 1
#define ATT_PEEL_FOW 1
#define ATT_PEEL_PRE 0
#define ATT_PEEL_ODD(b) (0u)
#define ATT_PAGES 11
#define ATT_TAILB 968
enum { T_fo, T_fj, T_fa, T_fas, T_fb, T_fbs, T_f8, T_mk8, T_wt, T_wol, T_woh, T_wbl, T_wbh, T_outl, T_outh, T_r1l, T_r1h, T_r1n, T_r1s, T_r2n, T_r2s, T_m2b, T_m0, T_m1, T_m1b, T_mw, T_m3, T_m4, T_ml, T_w0, T_w1, T_w2, T_w3, T_A1, T_A2, T_A3, T_B1, T_B2, T_B3, T_Ao, T_Bo, T_Al, T_Bl, T_l8, T_xr, T_x8r, T_oA, T_oB, T_o8, T_x0, T_x1, T_x2, T_x3, T_x4, T_x5, T_x6, T_x8, T_ep };

__device__ __forceinline__ void out_consts(u32x4 (&vp)[3], const u16* wo, const u16* wob, const u16* out, const unsigned b, const unsigned w, const unsigned lane) {
    const unsigned sp = b & 7u, nt = (b >> 3) & 3u, j = b >> 5, head = 4u * sp + nt, OOR = 0x40000000u;
    unsigned vw[9];
#pragma unroll
    for (unsigned i = 0; i < 9u; ++i) {
        const unsigned q = 64u * i + lane, row = __umul24(q, 3641u) >> 16, c = q - __umul24(row, 18u);
        vw[i] = __umul24(row, DM * 2) + (c < 16u ? (c * 16u + sp * 32u + 32u) & 255u : c * 16u) - (i & 3u) * 1024u;
    }
    const unsigned row = 16u * w + (lane >> 2), r9 = __umul24(16u * (row >> 5) + (row & 12u) + 2u * ((row >> 4) & 1u) + ((row >> 1) & 1u), 18u) + 16u + (row & 1u);
    unsigned vwl = __umul24(r9, DM * 2) + ((lane & 2u) ? 256u : sp * 32u) + (lane & 1u) * 16u, vrd = __umul24(lane >> 2, 288u) + (lane & 3u) * 8u;
    unsigned tab = 0;
#define TB(n, v) asm("v_writelane_b32 %0, %1, %2" : "+v"(tab) : "s"((unsigned)(v)), "n"((int)T_##n))
    const unsigned FJ = (unsigned)WS_F + (F_J + 640u * j) * 4u, mt = 2u * (w & 2u) + (w & 1u), w2 = w < 2u ? w : 2u, hw = head * 147456u;
    const unsigned FR = (unsigned)WS_F + F_R * 4u + (8u * j + sp) * 1024u;
    TB(fo, FJ + (ATT_PEEL_FOW ? 16u * head + 4u * w : 4u * head)); TB(fj, FJ); TB(fa, FR + (w | 2u) * 256u); TB(fas, FR + w * 256u + nt * 4u);
    TB(fb, FR + 128u + w * 256u); TB(fbs, FR + 128u + w * 256u + nt * 4u);
    TB(ep, ssel(b + w, OOR, (unsigned)WS_F + F_EPOCH * 4u));
    const u16* pwo = wo + nt * 144u; const u16* pwb = wob + nt * 144u;
    TB(wol, PLO(pwo)); TB(woh, PHI(pwo) & 0xffffu); TB(wbl, PLO(pwb)); TB(wbh, PHI(pwb) & 0xffffu); TB(outl, PLO(out)); TB(outh, PHI(out) & 0xffffu);
    const unsigned odd = ATT_PEEL_ODD(b), wo1 = w + 3u * odd;
#define OSEL(a, b_) (ATT_PEEL_ODD(1u) ? ssel(odd, a, b_) : (b_))
#define WU(u) (((4u * sp + ((nt + 1u + ((u) >> 2)) & 3u)) * 4u + ((u) & 3u)) * 36864u)
    TB(r1l, sslt<3>(wo1, PLO(pwo), PLO(pwb))); TB(r1h, sslt<3>(wo1, PHI(pwo), PHI(pwb)) & 0xffffu);
    TB(r1n, sslt<3>(wo1, 72u * sp + 71u, 4095u) * 1152u + 288u); TB(r1s, sslt<3>(wo1, (72u * sp + 32u * w) * 1152u, ATT_PEEL_ODD(1u) ? sslt<2>(w, WU(w), sslt<3>(w, OOR, hw + 110592u)) : hw + 110592u));
    if (ATT_PEEL_ODD(1u)) { TB(r2n, (72u * sp + 143u) * 1152u + 288u); TB(r2s, sslt<3>(w, (72u * sp + 72u + 32u * w) * 1152u, OOR)); TB(m2b, sslt<2>(w, 41984u + 9216u * w, 32768u)); }
    TB(m0, 131072u + 9216u * w2); TB(w0, hw + 36864u * w2); TB(m1, sslt<3>(w, 41984u + 9216u * w, 32768u)); TB(m1b, ssel(w ^ 2u, sslt<3>(w, 41984u + 9216u * w, 32768u), 114688u - 4096u));
    TB(mw, 65536u + 9216u * w); TB(m3, sslt<3>(w, 102400u + 9216u * w, 0u)); TB(m4, OSEL(9216u + 13312u * (w & 1u), sslt<3>(w, 131072u + 9216u * w, 9216u))); TB(ml, 18432u + 1024u * w);
    const unsigned tA = (unsigned)WS_AF + (8u * j + mt) * (KS_TOT * 1024u), tB = (unsigned)WS_AF + (120u - 8u * j + mt) * (KS_TOT * 1024u);
#define HK(k) (4u * sp + ((nt + k) & 3u))
    TB(w1, WU(w + 2u * odd)); TB(w2, WU(4u + w + 2u * odd)); TB(w3, WU(8u + (w | (2u * odd))));
    TB(A1, tA + 18432u + HK(1u) * 4096u); TB(A2, tA + 18432u + HK(2u) * 4096u); TB(A3, tA + 18432u + HK(3u) * 4096u);
    TB(B1, tB + 18432u + HK(1u) * 4096u); TB(B2, tB + 18432u + HK(2u) * 4096u); TB(B3, tB + 18432u + HK(3u) * 4096u);
    const unsigned pre = ATT_PEEL_PRE ? sslt<7>(j, tA + 18432u + head * 4096u, OOR) : OOR;
    TB(Ao, OSEL(pre, tA + sp * 2048u)); TB(Bo, OSEL(pre + 2048u, tB + sp * 2048u)); TB(Al, tA + 16384u); TB(Bl, tB + 16384u);
    const unsigned nred = w ^ (sp >> 1), blk = sp & 1u;
    const unsigned xg = (unsigned)WS_X + (4u * j + nt) * 589824u + w * 16384u, oA = (128u * j + 16u * mt) * 1152u + nt * 288u, oB = (1920u - 128u * j + 16u * mt) * 1152u + nt * 288u;
    TB(l8, ssel(nred, OOR, ssel(blk, tB, tA) + 16384u)); TB(xr, xg + sp * 65536u); TB(x8r, ssel(nred, OOR, xg + 524288u + (sp & 1u) * 1024u));
    TB(oA, oA + sp * 32u); TB(oB, oB + sp * 32u); TB(o8, ssel(nred, OOR, ssel(blk, oB, oA) + 256u)); TB(x8, xg + 524288u + sp * 2048u);
    TB(f8, xg + w * 131072u + 14400u + sp * 4u); TB(mk8, ssel(nred, 0u, 0xff0000u));
#define XI(i) TB(x##i, xg + ((sp + 1u + i) & 7u) * 65536u + i * 2048u)
    XI(0); XI(1); XI(2); XI(3); XI(4); XI(5); XI(6);
#undef XI
#undef HK
#undef WU
#undef TB
    vp[0] = (u32x4){vw[0], vw[1], vw[2], vw[3]}; vp[1] = (u32x4){vw[4], vw[5], vw[6], vw[7]}; vp[2] = (u32x4){vw[8], vwl, vrd, tab};
}
__global__ __launch_bounds__(256, 1) __attribute__((aligned(4096))) void fused_kernel(const u16* __restrict__ x, const u16* __restrict__ ckv, const u16* __restrict__ wq, const u16* __restrict__ wuk,
                                                       const u16* __restrict__ wuv, const u16* __restrict__ wqkv, const u16* __restrict__ gw,
                                                       const u16* __restrict__ wo, const u16* __restrict__ wob, char* __restrict__ ws_, u16* __restrict__ out) {
    using namespace att;
    extern __shared__ __attribute__((aligned(16))) char lds[];
    u32x4 sa[5]; u32x2 s24, s28; char* ws; unsigned tdum; kaptr ka = (kaptr)__builtin_amdgcn_kernarg_segment_ptr();
    asm volatile("s_load_dwordx8 s[8:15], %[ka], 0x0\n\ts_load_dwordx8 s[16:23], %[ka], 0x20\n\ts_getpc_b64 s[4:5]\n\tv_lshlrev_b32_e32 v191, 7, %[t]\n\ts_bfe_u32 s6, %[b], %[cb]\n\t"
                 "v_lshl_add_u32 v191, s6, %[cs], v191\n\tv_min_u32_e32 v191, %[e], v191\n\tglobal_load_dword v191, v191, s[4:5]\n\ts_load_dwordx4 s[24:27], %[ka], 0x40\n\ts_load_dwordx2 s[28:29], %[ka], 0x50"
                 : "={s[8:11]}"(sa[0]), "={s[12:15]}"(sa[1]), "={s[16:19]}"(sa[2]), "={s[20:23]}"(sa[3]), "={s[24:25]}"(s24), "={s[26:27]}"(ws), "={s[28:29]}"(s28), "={v191}"(tdum), [ka] "+{s[0:1]}"(ka)
                 : [t] "v"(threadIdx.x), [b] "s"(blockIdx.x), [e] "i"(CT_END), [cb] "i"(CT_END < 0x20000 ? 0x20003 : 0x30003), [cs] "i"(CT_END < 0x20000 ? 15 : 14) : "s4", "s5", "s6", "scc");
    SBAR();
    const unsigned wid = rfl(threadIdx.x >> 6);
    unsigned tag, vl;
    const int b = blockIdx.x;
    u32x4 vp[5], srdA; u32x2 sd;
    proj_phase(ka, ws, wid, vp, vl, sa, s24, s28, tdum, srdA, sd, tag
    );
    unsigned* const flags = (unsigned*)(ws + WS_F);
    RTS(2);
#define AST(L) do {} while (0)
#define ATT_TAIL "s_endpgm\n"
    unsigned abad;
    asm volatile(ATT_ASM ATT_TAIL "L_end_%=:\n s_nop 1"
                 : "={s94}"(abad)
                 : "{v[0:3]}"(vp[0]), "{v[4:7]}"(vp[1]), "{v[8:11]}"(vp[2]), "{v[12:15]}"(vp[3]), "{v[16:19]}"(vp[4]), "{v20}"(vl), "{s[56:59]}"(srdA), "{s[60:61]}"(sd), "{s62}"(tag),
                   "{s[8:11]}"(sa[0]), "{s[12:15]}"(sa[1]), "{s[16:19]}"(sa[2]), "{s[20:23]}"(sa[3]), "{s24}"(sa[4][0]), "{s3}"(wid), "{s2}"(b), "{s[28:29]}"(s28)
                 : "memory", "m0", "vcc", "scc", ATT_VCLOB, ATT_SCLOB, ACLOB_LO, ACLOB_HI);
    {
        unsigned b2 = b, w2 = wid;
        asm volatile("s_waitcnt vmcnt(0)" : "+s"(ka), "+s"(b2), "+s"(w2), "+s"(ws) :: "memory");
        const unsigned j2 = b2 >> 5, p2 = w2 & 1u, h2 = (b2 & 7u) * 4u + ((b2 >> 3) & 3u);
#pragma unroll 1
        for (unsigned i = w2 & 2u; i < ((w2 & 2u) ? (abad ? 8u : 4u) : 2u); ++i)
            att_exact(ka[0], ka[2], ws, h2, (i < 4u ? 1920u - 128u * j2 : 128u * j2 - 128u) + 32u * i + 16u * p2, *reinterpret_cast<const float*>(lds + LDS_G + (32u * i + 16u * p2 + (lane_id() & 15u)) * 4u));
        asm volatile("s_waitcnt vmcnt(0)" ::: "memory");
        unsigned* fa = (unsigned*)(ws + WS_F) + F_R + (8u * j2 + (b2 & 7u)) * 256u + (2u + p2) * 64u + ((b2 >> 3) & 3u);
        if (w2 & 2u) st_flag(fa, tag);
        wait_flags(fa, ld_flag(fa), tag);
        const unsigned l2 = lane_id(); u32x4 c[3]; out_consts(c, ka[7], ka[8], ka[10], b2, w2, l2);
        asm volatile("s_waitcnt vmcnt(0) lgkmcnt(0)\n\ts_nop 7\n\ts_branch L_att_stub" :: "{v[0:3]}"(c[0]), "{v[4:7]}"(c[1]), "{v[8:11]}"(c[2]), "{v20}"(l2 * 16u), "{s[56:59]}"(SRDA), "{s60}"(SD(8u * j2, h2, w2)),
                     "{s61}"(SD(120u - 8u * j2, h2, w2)), "{s62}"(tag), "{s3}"(w2), "{s2}"(b2) : "memory");
    }
    RTS(5);
}

extern "C" void kernel_launch(void* const* d_in, const int* in_sizes, int n_in, void* d_out, int out_size, void* d_ws, size_t ws_size,
                              hipStream_t stream) {
    (void)hipFuncSetAttribute((const void*)fused_kernel, hipFuncAttributeMaxDynamicSharedMemorySize, att::LDS_BYTES);
    fused_kernel<<<dim3(256), 256, att::LDS_BYTES, stream>>>((const u16*)d_in[0], (const u16*)d_in[4], (const u16*)d_in[5], (const u16*)d_in[6], (const u16*)d_in[7], (const u16*)d_in[1],
                                                             (const u16*)d_in[9], (const u16*)d_in[2], (const u16*)d_in[8], (char*)d_ws, (u16*)d_out);
}
```

```cpp
#include <hip/hip_runtime.h>
#include <hip/hip_bf16.h>
#include <stdint.h>
#include <utility>

typedef short bf16x8 __attribute__((ext_vector_type(8)));
typedef short s16x4 __attribute__((ext_vector_type(4)));
typedef float f32x16 __attribute__((ext_vector_type(16)));
typedef float f32x4 __attribute__((ext_vector_type(4)));
typedef unsigned u32x4 __attribute__((ext_vector_type(4)));
typedef unsigned u32x2 __attribute__((ext_vector_type(2)));
typedef unsigned short u16;

constexpr int S = 2048, DM = 576, NH = 32, HD = 128, DC = 512, NQKV = 1728, DHA = 18;
constexpr int NBIG = NH * HD;
constexpr int KS_TOT = 146;

#define SBAR() __builtin_amdgcn_sched_barrier(0)
constexpr size_t WS_Q = 0, WS_K = WS_Q + (size_t)S * NBIG * 2, WS_V = WS_K + (size_t)S * NBIG * 2, WS_AF = WS_V + (size_t)S * NBIG * 2, WS_X = WS_AF + (size_t)128 * KS_TOT * 1024,
                 WS_P = WS_X + (size_t)64 * 16 * 9216, WS_F = WS_P + (size_t)128 * (4 * 32 * 256 + 1024) * 4;
constexpr size_t WS_L = (size_t)96 << 20;
static_assert(WS_F == 96206848, "workspace map is baked into the generated asm");
constexpr int F_EPOCH = 0, F_J = 1024, F_ST = 8192, F_KV = 40960, F_R = 65536;
constexpr unsigned TAG_STEP = 0x9E3779B1u;
__device__ __forceinline__ unsigned ld_flag(const unsigned* p) { return __hip_atomic_load(p, __ATOMIC_RELAXED, __HIP_MEMORY_SCOPE_AGENT); }
__device__ __forceinline__ void st_flag(unsigned* p, unsigned v) { __hip_atomic_store(p, v, __ATOMIC_RELAXED, __HIP_MEMORY_SCOPE_AGENT); }
__device__ __forceinline__ void wait_flags(const unsigned* p, unsigned v, unsigned tag) {
    if (__builtin_expect(__builtin_amdgcn_ballot_w64(v != tag) != 0ull, 0)) {
        unsigned spins = 0;
        do { __builtin_amdgcn_s_sleep(1); } while (__builtin_amdgcn_ballot_w64(ld_flag(p) != tag) != 0ull && ++spins < (1u << 22));
    }
    asm volatile("" ::: "memory");
}

__device__ __forceinline__ unsigned lane_id() { unsigned l; asm volatile("v_mbcnt_lo_u32_b32 %0, -1, 0\n\tv_mbcnt_hi_u32_b32 %0, -1, %0" : "=v"(l)); return l; }
#define RTS(slot) do {} while (0)
#define CT_END ((PROJ_PAGES + ATT_PAGES) * 4096 + (ATT_TAILB & ~127) - 128)
#define ACLOB_LO "a0","a1","a2","a3","a4","a5","a6","a7","a8","a9","a10","a11","a12","a13","a14","a15","a16","a17","a18","a19","a20","a21","a22","a23","a24","a25","a26","a27","a28","a29","a30","a31","a32","a33","a34","a35","a36","a37","a38","a39","a40","a41","a42","a43","a44","a45","a46","a47","a48","a49","a50","a51","a52","a53","a54","a55","a56","a57","a58","a59","a60","a61","a62","a63","a64","a65","a66","a67","a68","a69","a70","a71","a72","a73","a74","a75","a76","a77","a78","a79","a80","a81","a82","a83","a84","a85","a86","a87","a88","a89","a90","a91","a92","a93","a94","a95","a96","a97","a98","a99","a100","a101","a102","a103","a104","a105","a106","a107","a108","a109","a110","a111","a112","a113","a114","a115","a116","a117","a118","a119","a120","a121","a122","a123","a124","a125","a126","a127"
#define ACLOB_HI "a128","a129","a130","a131","a132","a133","a134","a135","a136","a137","a138","a139","a140","a141","a142","a143","a144","a145","a146","a147","a148","a149","a150","a151","a152","a153","a154","a155","a156","a157","a158","a159","a160","a161","a162","a163","a164","a165","a166","a167","a168","a169","a170","a171","a172","a173","a174","a175","a176","a177","a178","a179","a180","a181","a182","a183","a184","a185","a186","a187","a188","a189","a190","a191","a192","a193","a194","a195","a196","a197","a198","a199","a200","a201","a202","a203","a204","a205","a206","a207","a208","a209","a210","a211","a212","a213","a214","a215","a216","a217","a218","a219","a220","a221","a222","a223","a224","a225","a226","a227","a228","a229","a230","a231","a232","a233","a234","a235","a236","a237","a238","a239","a240","a241","a242","a243","a244","a245","a246","a247","a248","a249","a250","a251","a252","a253","a254","a255"
#define M_ "v_mfma_f32_16x16x32_bf16 "
#define X_ "ds_read_b64_tr_b16 "
#define B_ "buffer_load_dwordx4 "
#define C_ "v_cvt_pk_bf16_f32 "
#define R_ "ds_read_b128 "
#define E_ "v_exp_f32_e32 "
#define PLO(p) ((unsigned)(size_t)(p))
#define PHI(p) ((unsigned)((size_t)(p) >> 32))
__device__ __forceinline__ unsigned vsel(bool c, unsigned a, unsigned b) { asm("" : "+v"(a), "+v"(b)); return c ? a : b; }
__device__ __forceinline__ unsigned ssel(unsigned c, unsigned a, unsigned b) { unsigned r; asm("s_cmp_lg_u32 %1, 0\n\ts_cselect_b32 %0, %2, %3" : "=s"(r) : "s"(c), "s"(a), "s"(b) : "scc"); return r; }
template <int N> __device__ __forceinline__ unsigned sslt(unsigned c, unsigned a, unsigned b) { unsigned r; asm("s_cmpk_lt_u32 %1, %4\n\ts_cselect_b32 %0, %2, %3" : "=s"(r) : "s"(c), "s"(a), "s"(b), "n"(N) : "scc"); return r; }
namespace att {
constexpr int LDS_BYTES = 163840;
constexpr unsigned LDS_G = 159744u, LDS_L = 161792u;
}
#define SD(J8, H, W) ((unsigned)WS_AF + (((J8) + 2u * ((W) & 2u) + ((W) & 1u)) * KS_TOT + 18u + 4u * (H)) * 1024u)
#define SRDA (u32x4){PLO(ws), PHI(ws) & 0xffffu, (unsigned)WS_L, 0x00020000u}
#define PROJ_ASM \
".p2align 3\n" \
"L_pb:\n" \
"s_waitcnt lgkmcnt(0)\n" \
"s_mov_b32 s64, s10\n" \
"s_and_b32 s65, s11, 0xffff\n" \
"s_mov_b32 s66, 0x200000\n" \
"s_mov_b32 s67, 0x20000\n" \
"s_mov_b32 s68, s14\n" \
"s_and_b32 s69, s15, 0xffff\n" \
"s_mov_b32 s70, 0x400000\n" \
"s_mov_b32 s71, 0x20000\n" \
"s_mov_b32 s72, s16\n" \
"s_and_b32 s73, s17, 0xffff\n" \
"s_mov_b32 s74, 0x400000\n" \
"s_mov_b32 s75, 0x20000\n" \
"s_add_u32 m0, s30, 0x0\n" \
"s_movk_i32 s84, 0x0\n" \
B_"v22, s[64:67], s84 offen offset:0 lds\n" \
"s_movk_i32 s84, 0x1c00\n" \
B_"v23, s[64:67], s84 offen offset:1024 lds\n" \
"s_movk_i32 s84, 0x3800\n" \
B_"v22, s[64:67], s84 offen offset:2048 lds\n" \
"s_movk_i32 s84, 0x5400\n" \
B_"v23, s[64:67], s84 offen offset:3072 lds\n" \
"s_add_u32 m0, s31, 0x0\n" \
"s_movk_i32 s84, 0x0\n" \
B_"v26, s[68:71], s84 offen offset:0 lds\n" \
"s_movk_i32 s84, 0x7c00\n" \
B_"v26, s[68:71], s84 offen offset:1024 lds\n" \
"s_addk_i32 s84, 0x7c00\n" \
B_"v27, s[68:71], s84 offen offset:2048 lds\n" \
"s_addk_i32 s84, 0x7c00\n" \
B_"v27, s[68:71], s84 offen offset:3072 lds\n" \
"s_movk_i32 s7, 0x300\n" \
"s_lshr_b32 s4, s2, 1\n" \
"s_and_b32 s4, s4, 60\n" \
"s_or_b32 s4, s4, s3\n" \
"v_lshrrev_b32_e32 v212, 4, v20\n" \
"v_min_u32_e32 v212, 59, v212\n" \
"s_cmpk_lt_u32 s2, 0x80\n" \
"s_cselect_b32 s6, s14, s16\n" \
"v_subrev_u32_e32 v214, 48, v212\n" \
"v_mov_b32_e32 v212, s10\n" \
"v_mov_b32_e32 v215, s6\n" \
"s_cmpk_lt_u32 s2, 0x80\n" \
"s_cselect_b32 s5, s15, s17\n" \
"v_cmp_gt_u32_e32 vcc, s7, v20\n" \
"v_mov_b32_e64 v213, s11\n" \
"v_bfe_u32 v216, v20, 4, 3\n" \
"v_cndmask_b32_e32 v212, v212, v215, vcc\n" \
"v_mov_b32_e32 v215, s5\n" \
"s_lshl_b32 s5, s3, 4\n" \
"v_cndmask_b32_e32 v213, v213, v215, vcc\n" \
"v_lshlrev_b32_e64 v215, 5, v20\n" \
"v_and_b32_e32 v215, 0x7000, v215\n" \
"v_lshl_add_u32 v215, s4, 6, v215\n" \
"s_lshl_b32 s4, s2, 3\n" \
"s_and_b32 s4, s4, 56\n" \
"v_or3_b32 v215, v215, s4, v216\n" \
"s_lshl_b32 s4, s2, 6\n" \
"s_or_b32 s6, s5, s4\n" \
"v_cmp_gt_u32_e64 s[4:5], 6, v214\n" \
"v_add_u32_e32 v215, 0x2000, v215\n" \
"s_nop 0\n" \
"v_cndmask_b32_e64 v216, 4, 2, s[4:5]\n" \
"v_add3_u32 v214, s6, v214, v216\n" \
"s_lshl_b32 s4, s3, 9\n" \
"v_cndmask_b32_e32 v214, v214, v215, vcc\n" \
"v_lshlrev_b32_e32 v214, 7, v214\n" \
"v_mov_b32_e64 v215, 0\n" \
"v_lshl_add_u64 v[184:185], v[212:213], 0, v[214:215]\n" \
"s_or_b32 s32, s4, 0x25000\n" \
"s_add_u32 m0, s30, 0x1000\n" \
"s_mov_b32 s84, 0x8000\n" \
B_"v22, s[64:67], s84 offen offset:0 lds\n" \
"s_and_b32 s33, s3, 1\n" \
"v_lshrrev_b32_e32 v212, 4, v20\n" \
"v_lshrrev_b32_e64 v214, 8, v20\n" \
"v_bfe_u32 v212, v212, 1, 3\n" \
"v_lshlrev_b32_e32 v215, 2, v20\n" \
"s_addk_i32 s84, 0x1c00\n" \
B_"v23, s[64:67], s84 offen offset:1024 lds\n" \
"v_lshrrev_b32_e64 v216, 1, v20\n" \
"v_bfe_u32 v213, v20, 4, 4\n" \
"s_lshl_b32 s4, s3, 5\n" \
"v_xor_b32_e32 v212, v212, v214\n" \
"v_lshlrev_b32_e32 v214, 11, v214\n" \
"s_addk_i32 s84, 0x1c00\n" \
B_"v22, s[64:67], s84 offen offset:2048 lds\n" \
"v_and_b32_e32 v215, 0x300, v215\n" \
"v_and_b32_e64 v217, 24, v216\n" \
"v_and_or_b32 v213, s4, 64, v213\n" \
"v_or3_b32 v214, v215, v214, v217\n" \
"s_lshl_b32 s4, s33, 14\n" \
"s_addk_i32 s84, 0x1c00\n" \
B_"v23, s[64:67], s84 offen offset:3072 lds\n" \
"v_and_b32_e32 v215, 0xe0, v216\n" \
"v_lshlrev_b32_e32 v213, 7, v213\n" \
"v_lshlrev_b32_e32 v212, 4, v212\n" \
"v_or3_b32 v214, v214, s4, v215\n" \
"v_or_b32_e64 v28, v212, v213\n" \
"s_add_u32 m0, s31, 0x4000\n" \
"s_movk_i32 s84, 0x0\n" \
B_"v26, s[72:75], s84 offen offset:0 lds\n" \
"v_add_u32_e32 v32, 0x10000, v214\n" \
"v_bitop3_b32 v29, v212, 64, v213 bitop3:0x36\n" \
"v_xor_b32_e32 v33, 0x20, v32\n" \
"v_xor_b32_e64 v34, 0x40, v32\n" \
"v_xor_b32_e32 v35, 0x60, v32\n" \
"s_movk_i32 s84, 0x7c00\n" \
B_"v26, s[72:75], s84 offen offset:1024 lds\n" \
"v_xor_b32_e32 v36, 0x80, v32\n" \
"v_xor_b32_e32 v37, 0xa0, v32\n" \
"v_xor_b32_e32 v38, 0xc0, v32\n" \
"v_xor_b32_e32 v39, 0xe0, v32\n" \
"s_mov_b32 s80, s26\n" \
"s_addk_i32 s84, 0x7c00\n" \
B_"v27, s[72:75], s84 offen offset:2048 lds\n" \
"s_and_b32 s81, s27, 0xffff\n" \
"s_mov_b32 s82, 0x10000000\n" \
"s_mov_b32 s83, 0x20000\n" \
"s_mov_b32 s76, s18\n" \
"s_and_b32 s77, s19, 0xffff\n" \
"s_addk_i32 s84, 0x7c00\n" \
B_"v27, s[72:75], s84 offen offset:3072 lds\n" \
"s_mov_b32 s78, 0x1e6000\n" \
"s_mov_b32 s79, 0x20000\n" \
"s_branch L_pgo_%=\n" \
"L_pret:\n" \
"s_setpc_b64 s[88:89]\n" \
"L_pgo_%=:\n" \
"s_mul_i32 s88, s33, 0xa000\n" \
"s_lshl_b32 s89, s3, 11\n" \
"s_and_b32 s89, s89, 0x1000\n" \
"s_add_u32 s88, s88, s89\n" \
"s_getpc_b64 s[86:87]\n" \
"L_hpa_%=:\n" \
"s_add_u32 s86, s86, L_pp1-L_hpa_%=\n" \
"s_addc_u32 s87, s87, 0\n" \
"s_add_u32 s86, s86, s88\n" \
"s_addc_u32 s87, s87, 0\n" \
"s_add_u32 m0, s30, 0x8000\n" \
"s_movk_i32 s84, 0x80\n" \
B_"v22, s[64:67], s84 offen offset:0 lds\n" \
"s_lshr_b32 s4, s2, 1\n" \
"s_and_b32 s4, s4, 0x7c\n" \
"v_bfe_u32 v212, v20, 4, 5\n" \
"s_or_b32 s4, s4, s3\n" \
"v_min_u32_e32 v212, 26, v212\n" \
"s_movk_i32 s84, 0x1c80\n" \
B_"v23, s[64:67], s84 offen offset:1024 lds\n" \
"v_mad_u64_u32 v[212:213], s[4:5], s4, 27, v[212:213]\n" \
"v_mul_u32_u24_e32 v213, 0xaaab, v212\n" \
"s_lshr_b32 s7, s2, 5\n" \
"v_lshrrev_b32_e32 v213, 18, v213\n" \
"s_mov_b32 s4, 0x1fffffa\n" \
"s_mov_b64 exec, 0\n" \
"s_swappc_b64 s[88:89], s[86:87]\n" \
"s_mov_b64 exec, -1\n" \
"s_add_u32 s86, s86, 0x2000\n" \
"s_addc_u32 s87, s87, 0\n" \
"s_movk_i32 s84, 0x3880\n" \
B_"v22, s[64:67], s84 offen offset:2048 lds\n" \
"s_and_b32 s6, s2, 7\n" \
"v_mad_u64_u32 v[214:215], s[4:5], v213, s4, v[212:213]\n" \
"s_mulk_i32 s7, 0x90\n" \
"v_mul_u32_u24_e32 v213, 27, v213\n" \
"v_lshrrev_b32_e32 v214, 1, v214\n" \
"s_movk_i32 s84, 0x5480\n" \
B_"v23, s[64:67], s84 offen offset:3072 lds\n" \
"v_and_b32_e32 v212, 1, v212\n" \
"s_add_u32 s4, s7, s20\n" \
"v_mul_u32_u24_e64 v214, 9, v214\n" \
"v_add3_u32 v212, v212, s6, v213\n" \
"s_addc_u32 s5, 0, s21\n" \
"s_add_u32 m0, s31, 0x8000\n" \
"s_mov_b32 s84, 0x80000\n" \
B_"v26, s[68:71], s84 offen offset:0 lds\n" \
"v_add_lshl_u32 v212, v212, v214, 7\n" \
"v_mov_b32_e32 v213, 0\n" \
"v_lshl_add_u64 v[186:187], s[18:19], 0, v[212:213]\n" \
"s_bitcmp1_b32 s3, 0\n" \
"s_cselect_b32 s6, 0x8c, 0\n" \
"s_addk_i32 s84, 0x7c00\n" \
B_"v26, s[68:71], s84 offen offset:1024 lds\n" \
"s_add_u32 s4, s4, s6\n" \
"v_mov_b32_e32 v212, v187\n" \
"s_addc_u32 s5, s5, 0\n" \
"v_writelane_b32 v186, s4, 31\n" \
"v_writelane_b32 v212, s5, 31\n" \
"s_addk_i32 s84, 0x7c00\n" \
B_"v27, s[68:71], s84 offen offset:2048 lds\n" \
"v_mov_b32_e32 v187, v212\n" \
"s_lshl_b32 s35, s2, 10\n" \
"s_lshr_b32 s37, s2, 1\n" \
"s_mul_i32 s37, s37, 0x24800\n" \
"s_and_b32 s35, s35, 0x400\n" \
"s_mov_b64 exec, 0\n" \
"s_swappc_b64 s[88:89], s[86:87]\n" \
"s_mov_b64 exec, -1\n" \
"s_add_u32 s86, s86, 0x2000\n" \
"s_addc_u32 s87, s87, 0\n" \
"s_addk_i32 s84, 0x7c00\n" \
B_"v27, s[68:71], s84 offen offset:3072 lds\n" \
"s_lshl_b32 s36, s3, 8\n" \
"s_or_b32 s35, s37, s35\n" \
"s_and_b32 s4, s2, 7\n" \
"s_lshr_b32 s6, s2, 5\n" \
"s_lshl_b32 s34, s2, 5\n" \
"s_add_u32 m0, s30, 0x9000\n" \
"s_mov_b32 s84, 0x8080\n" \
B_"v22, s[64:67], s84 offen offset:0 lds\n" \
"s_add_i32 s35, s35, s36\n" \
"s_lshl_b32 s7, s4, 10\n" \
"s_and_b32 s34, s34, 0x300\n" \
"s_add_i32 s35, s35, 0x3004000\n" \
"s_lshl_b32 s37, s6, 13\n" \
"s_addk_i32 s84, 0x1c00\n" \
B_"v23, s[64:67], s84 offen offset:1024 lds\n" \
"s_or_b32 s34, s7, s34\n" \
"s_or_b32 s7, s37, s7\n" \
"v_mov_b32_e32 v213, s35\n" \
"s_or_b32 s7, s7, s36\n" \
"s_lshr_b32 s5, s2, 3\n" \
"s_addk_i32 s84, 0x1c00\n" \
B_"v22, s[64:67], s84 offen offset:2048 lds\n" \
"s_or_b32 s7, s7, 0x5c00000\n" \
"v_writelane_b32 v213, s7, 2\n" \
"s_mul_i32 s5, s5, 0x90000\n" \
"v_writelane_b32 v213, s7, 3\n" \
"s_lshl_b32 s7, s3, 14\n" \
"s_addk_i32 s84, 0x1c00\n" \
B_"v23, s[64:67], s84 offen offset:3072 lds\n" \
"s_lshl_b32 s4, s4, 16\n" \
"s_add_i32 s5, s5, s7\n" \
"s_or_b32 s34, s34, 0x5be8000\n" \
"v_writelane_b32 v213, s34, 4\n" \
"s_add_i32 s4, s5, s4\n" \
"v_writelane_b32 v213, s34, 5\n" \
"s_add_i32 s4, s4, 0x4243800\n" \
"v_writelane_b32 v213, s4, 6\n" \
"s_mul_i32 s4, s6, 0xa00\n" \
"v_lshlrev_b32_e32 v212, 3, v20\n" \
"s_lshl_b32 s5, s3, 7\n" \
"v_and_b32_e32 v212, 0x80, v212\n" \
"s_add_i32 s4, s4, s5\n" \
"s_add_i32 s4, s4, 0x5bc0f80\n" \
"v_writelane_b32 v213, s4, 7\n" \
"v_add_u32_e32 v49, v213, v212\n" \
"s_mov_b32 m0, s32\n" \
"s_mov_b32 exec_lo, 0xffffff\n" \
"s_mov_b32 exec_hi, 0xfff0000\n" \
"global_load_lds_dword v[184:185], off\n" \
"s_mov_b64 exec, -1\n" \
"s_bitcmp1_b32 s2, 0\n" \
"s_cbranch_scc1 L_sd_%=\n" \
"s_sleep 30\n" \
"L_sd_%=:\n" \
"s_cmp_lg_u32 s33, 0\n" \
"s_cbranch_scc1 L_w1_%=\n" \
"L_w0_%=:\n" \
"s_waitcnt vmcnt(13)\n" \
"s_barrier\n" \
X_"a[160:161], v32 offset:0\n" \
X_"a[162:163], v32 offset:1024\n" \
R_"a[128:131], v28 offset:16384\n" \
X_"a[224:225], v32 offset:8192\n" \
X_"a[226:227], v32 offset:9216\n" \
R_"a[192:195], v29 offset:16384\n" \
R_"a[132:135], v28 offset:18432\n" \
R_"a[196:199], v29 offset:18432\n" \
X_"a[164:165], v33 offset:0\n" \
X_"a[166:167], v33 offset:1024\n" \
X_"a[228:229], v33 offset:8192\n" \
X_"a[230:231], v33 offset:9216\n" \
R_"a[136:139], v28 offset:20480\n" \
R_"a[200:203], v29 offset:20480\n" \
"s_waitcnt lgkmcnt(11)\n" \
M_"a[0:3], a[160:163], a[128:131], 0\n" \
X_"a[168:169], v34 offset:0\n" \
"s_add_u32 m0, s31, 0xc000\n" \
"s_mov_b32 s84, 0x80000\n" \
"s_waitcnt lgkmcnt(9)\n" \
M_"a[0:3], a[224:227], a[192:195], a[0:3]\n" \
X_"a[170:171], v34 offset:1024\n" \
B_"v26, s[72:75], s84 offen offset:0 lds\n" \
"s_waitcnt lgkmcnt(9)\n" \
M_"a[16:19], a[160:163], a[132:135], 0\n" \
X_"a[232:233], v34 offset:8192\n" \
"s_lshr_b32 s4, s2, 1\n" \
"s_waitcnt lgkmcnt(9)\n" \
M_"a[16:19], a[224:227], a[196:199], a[16:19]\n" \
X_"a[234:235], v34 offset:9216\n" \
"s_addk_i32 s84, 0x7c00\n" \
"s_waitcnt lgkmcnt(8)\n" \
M_"a[20:23], a[164:167], a[132:135], 0\n" \
R_"a[140:143], v28 offset:22528\n" \
B_"v26, s[72:75], s84 offen offset:1024 lds\n" \
"s_waitcnt lgkmcnt(7)\n" \
M_"a[20:23], a[228:231], a[196:199], a[20:23]\n" \
R_"a[204:207], v29 offset:22528\n" \
"s_and_b32 s4, s4, 0x7c\n" \
M_"a[4:7], a[164:167], a[128:131], 0\n" \
X_"a[172:173], v35 offset:0\n" \
"s_addk_i32 s84, 0x7c00\n" \
M_"a[4:7], a[228:231], a[192:195], a[4:7]\n" \
X_"a[174:175], v35 offset:1024\n" \
B_"v27, s[72:75], s84 offen offset:2048 lds\n" \
"s_waitcnt lgkmcnt(9)\n" \
M_"a[32:35], a[160:163], a[136:139], 0\n" \
X_"a[236:237], v35 offset:8192\n" \
"v_mov_b32_e32 v213, s13\n" \
"s_waitcnt lgkmcnt(9)\n" \
M_"a[32:35], a[224:227], a[200:203], a[32:35]\n" \
X_"a[238:239], v35 offset:9216\n" \
"s_addk_i32 s84, 0x7c00\n" \
M_"a[36:39], a[164:167], a[136:139], 0\n" \
R_"a[144:147], v28 offset:0\n" \
B_"v27, s[72:75], s84 offen offset:3072 lds\n" \
M_"a[36:39], a[228:231], a[200:203], a[36:39]\n" \
R_"a[208:211], v29 offset:0\n" \
"s_or_b32 s4, s4, s3\n" \
"s_waitcnt lgkmcnt(10)\n" \
M_"a[40:43], a[168:171], a[136:139], 0\n" \
R_"a[148:151], v28 offset:2048\n" \
"v_lshrrev_b32_e32 v212, 4, v20\n" \
"s_waitcnt lgkmcnt(9)\n" \
M_"a[40:43], a[232:235], a[200:203], a[40:43]\n" \
R_"a[212:215], v29 offset:2048\n" \
"v_mad_u64_u32 v[214:215], s[4:5], s4, 36, v[212:213]\n" \
M_"a[24:27], a[168:171], a[132:135], 0\n" \
R_"a[152:155], v28 offset:4096\n" \
"v_mov_b32_e64 v217, s9\n" \
M_"a[24:27], a[232:235], a[196:199], a[24:27]\n" \
R_"a[216:219], v29 offset:4096\n" \
"s_movk_i32 s4, 0x240\n" \
M_"a[8:11], a[168:171], a[128:131], 0\n" \
R_"a[156:159], v28 offset:6144\n" \
"v_cmp_gt_u32_e32 vcc, s4, v20\n" \
M_"a[8:11], a[232:235], a[192:195], a[8:11]\n" \
R_"a[220:223], v29 offset:6144\n" \
"s_mov_b32 m0, s32\n" \
"s_waitcnt lgkmcnt(13)\n" \
M_"a[48:51], a[160:163], a[140:143], 0\n" \
"s_mov_b32 exec_lo, 0xff000000\n" \
"s_mov_b32 exec_hi, 0x0\n" \
"global_load_lds_dword v[184:185], off\n" \
"s_mov_b64 exec, -1\n" \
"s_waitcnt lgkmcnt(12)\n" \
M_"a[48:51], a[224:227], a[204:207], a[48:51]\n" \
X_"a[176:177], v36 offset:0\n" \
"s_lshl_b32 s4, s2, 3\n" \
M_"a[52:55], a[164:167], a[140:143], 0\n" \
X_"a[178:179], v36 offset:1024\n" \
"s_and_b32 s4, s4, 56\n" \
M_"a[52:55], a[228:231], a[204:207], a[52:55]\n" \
"s_mov_b32 m0, s32\n" \
X_"a[240:241], v36 offset:8192\n" \
M_"a[56:59], a[168:171], a[140:143], 0\n" \
"s_mov_b32 exec_lo, 0xffff\n" \
"s_mov_b32 exec_hi, 0x0\n" \
"global_load_lds_dword v[186:187], off\n" \
"s_mov_b64 exec, -1\n" \
M_"a[56:59], a[232:235], a[204:207], a[56:59]\n" \
X_"a[242:243], v36 offset:9216\n" \
"v_cndmask_b32_e32 v217, v217, v213, vcc\n" \
"s_waitcnt lgkmcnt(14)\n" \
M_"a[60:63], a[172:175], a[140:143], 0\n" \
X_"a[180:181], v37 offset:0\n" \
"v_lshlrev_b32_e32 v213, 3, v214\n" \
"s_waitcnt lgkmcnt(13)\n" \
M_"a[60:63], a[236:239], a[204:207], a[60:63]\n" \
X_"a[182:183], v37 offset:1024\n" \
"v_and_b32_e32 v213, 0x1ffc0, v213\n" \
M_"a[44:47], a[172:175], a[136:139], 0\n" \
X_"a[244:245], v37 offset:8192\n" \
"v_and_b32_e64 v214, 7, v214\n" \
M_"a[44:47], a[236:239], a[200:203], a[44:47]\n" \
X_"a[246:247], v37 offset:9216\n" \
"v_or3_b32 v213, v214, s4, v213\n" \
M_"a[28:31], a[172:175], a[132:135], 0\n" \
X_"a[184:185], v38 offset:0\n" \
"s_lshl_b32 s4, s2, 2\n" \
M_"a[28:31], a[236:239], a[196:199], a[28:31]\n" \
X_"a[186:187], v38 offset:1024\n" \
"s_or_b32 s4, s4, s3\n" \
M_"a[12:15], a[172:175], a[128:131], 0\n" \
"s_mov_b64 exec, 0\n" \
"s_swappc_b64 s[88:89], s[86:87]\n" \
"s_mov_b64 exec, -1\n" \
"s_add_u32 s86, s86, 0x2000\n" \
"s_addc_u32 s87, s87, 0\n" \
M_"a[12:15], a[236:239], a[192:195], a[12:15]\n" \
"s_mul_i32 s4, s4, 18\n" \
"v_min_u32_e32 v212, 53, v212\n" \
"s_waitcnt lgkmcnt(15)\n" \
M_"a[76:79], a[172:175], a[144:147], 0\n" \
"s_mov_b32 m0, s32\n" \
X_"a[248:249], v38 offset:8192\n" \
M_"a[76:79], a[236:239], a[208:211], a[76:79]\n" \
"s_mov_b64 exec, 0xff\n" \
"buffer_load_dword v49, s[80:83], 0 offen lds\n" \
"s_mov_b64 exec, -1\n" \
M_"a[72:75], a[168:171], a[144:147], 0\n" \
"s_sub_i32 s4, s4, 36\n" \
"v_add_u32_e64 v212, s4, v212\n" \
M_"a[72:75], a[232:235], a[208:211], a[72:75]\n" \
X_"a[250:251], v38 offset:9216\n" \
"v_mov_b32_e64 v215, s12\n" \
M_"a[68:71], a[164:167], a[144:147], 0\n" \
"s_mov_b32 s84, 0x5bc0000\n" \
"buffer_load_dword v180, off, s[80:83], s84 sc1\n" \
M_"a[68:71], a[228:231], a[208:211], a[68:71]\n" \
"v_mov_b32_e32 v216, s8\n" \
"v_cndmask_b32_e32 v212, v212, v213, vcc\n" \
M_"a[64:67], a[160:163], a[144:147], 0\n" \
"v_cndmask_b32_e32 v216, v216, v215, vcc\n" \
"v_lshlrev_b32_e32 v212, 7, v212\n" \
M_"a[64:67], a[224:227], a[208:211], a[64:67]\n" \
X_"a[188:189], v39 offset:0\n" \
"v_mov_b32_e64 v213, 0\n" \
M_"a[80:83], a[160:163], a[148:151], 0\n" \
"v_lshl_add_u64 v[188:189], v[216:217], 0, v[212:213]\n" \
"s_movk_i32 s5, 0xd80\n" \
"s_waitcnt lgkmcnt(15)\n" \
M_"a[80:83], a[224:227], a[212:215], a[80:83]\n" \
"s_lshr_b32 s6, s2, 5\n" \
"v_lshrrev_b32_e32 v212, 4, v20\n" \
M_"a[84:87], a[164:167], a[148:151], 0\n" \
X_"a[190:191], v39 offset:1024\n" \
"v_lshl_or_b32 v215, s3, 6, v212\n" \
M_"a[84:87], a[228:231], a[212:215], a[84:87]\n" \
"v_mul_u32_u24_e32 v216, 0x2aab, v215\n" \
"v_lshrrev_b32_e64 v216, 16, v216\n" \
M_"a[88:91], a[168:171], a[148:151], 0\n" \
"v_mad_i32_i24 v217, v216, -6, v215\n" \
"v_add_u32_e32 v218, 0xfffffe50, v215\n" \
M_"a[88:91], a[232:235], a[212:215], a[88:91]\n" \
X_"a[252:253], v39 offset:8192\n" \
"s_mul_i32 s35, s6, 0x48\n" \
M_"a[92:95], a[172:175], a[148:151], 0\n" \
"v_lshrrev_b32_e32 v219, 1, v217\n" \
"v_add_u32_e32 v218, s35, v216\n" \
M_"a[92:95], a[236:239], a[212:215], a[92:95]\n" \
"v_mul_u32_u24_e32 v219, 0x480, v219\n" \
"v_and_b32_e64 v214, 16, v20\n" \
M_"a[108:111], a[172:175], a[152:155], 0\n" \
X_"a[254:255], v39 offset:9216\n" \
"v_mad_u32_u24 v219, v218, s5, v219\n" \
M_"a[108:111], a[236:239], a[216:219], a[108:111]\n" \
"v_or_b32_e32 v192, v219, v214\n" \
"v_mov_b32_e32 v220, 2.0\n" \
M_"a[104:107], a[168:171], a[152:155], 0\n" \
"s_mov_b64 exec, 0\n" \
"s_swappc_b64 s[88:89], s[86:87]\n" \
"s_mov_b64 exec, -1\n" \
"s_andn2_b32 s85, 2, s3\n" \
"s_lshl_b32 s85, s85, 12\n" \
"s_add_u32 s86, s86, s85\n" \
"s_addc_u32 s87, s87, 0\n" \
M_"a[104:107], a[232:235], a[216:219], a[104:107]\n" \
"v_mov_b32_e32 v222, 7\n" \
"v_lshlrev_b32_e64 v220, 7, v218\n" \
M_"a[100:103], a[164:167], a[152:155], 0\n" \
"v_lshrrev_b32_e32 v218, 1, v218\n" \
"v_and_b32_e32 v218, 4, v218\n" \
M_"a[100:103], a[228:231], a[216:219], a[100:103]\n" \
"v_and_b32_e64 v216, 2, v216\n" \
"s_mul_i32 s7, s6, 0x2400\n" \
M_"a[96:99], a[160:163], a[152:155], 0\n" \
"s_sub_i32 s34, 0x22000, s7\n" \
"v_bitop3_b32 v216, v217, v218, v216 bitop3:0x1e\n" \
M_"a[96:99], a[224:227], a[216:219], a[96:99]\n" \
"v_or_b32_e32 v213, 0x24400, v20\n" \
"v_lshlrev_b32_e32 v216, 4, v216\n" \
"s_waitcnt lgkmcnt(15)\n" \
M_"a[112:115], a[160:163], a[156:159], 0\n" \
"v_add3_u32 v196, v220, s34, v216\n" \
"v_mov_b32_e64 v216, v213\n" \
M_"a[112:115], a[224:227], a[220:223], a[112:115]\n" \
"s_movk_i32 s36, 0x1b0\n" \
"v_or_b32_e32 v216, 0x100, v215\n" \
M_"a[116:119], a[164:167], a[156:159], 0\n" \
"v_mul_u32_u24_e32 v217, 0x2aab, v216\n" \
"v_add_u32_e32 v215, 0xffffff50, v215\n" \
M_"a[116:119], a[228:231], a[220:223], a[116:119]\n" \
"v_lshrrev_b32_e32 v217, 16, v217\n" \
"v_mov_b32_e64 v220, v215\n" \
M_"a[120:123], a[168:171], a[156:159], 0\n" \
"v_mad_i32_i24 v218, v217, -6, v216\n" \
"v_cmp_gt_u32_e64 vcc, s36, v216\n" \
M_"a[120:123], a[232:235], a[220:223], a[120:123]\n" \
"v_mov_b32_e32 v221, 7\n" \
"v_lshrrev_b32_e32 v222, 1, v218\n" \
M_"a[124:127], a[172:175], a[156:159], 0\n" \
"v_cndmask_b32_e32 v217, v220, v217, vcc\n" \
"s_movk_i32 s4, 0x48\n" \
"s_waitcnt vmcnt(4) lgkmcnt(0)\n" \
M_"a[124:127], a[236:239], a[220:223], a[124:127]\n" \
"s_barrier\n" \
M_"v[52:55], a[176:179], a[128:131], 0\n" \
"s_add_u32 m0, s30, 0x0\n" \
"s_movk_i32 s84, 0x100\n" \
M_"v[52:55], a[240:243], a[192:195], v[52:55]\n" \
B_"v22, s[64:67], s84 offen offset:0 lds\n" \
M_"v[56:59], a[180:183], a[128:131], 0\n" \
X_"a[160:161], v32 offset:32768\n" \
"v_add_u32_e64 v220, s35, v217\n" \
M_"v[56:59], a[244:247], a[192:195], v[56:59]\n" \
"s_movk_i32 s84, 0x1d00\n" \
"s_nop 0\n" \
M_"v[60:63], a[184:187], a[128:131], 0\n" \
B_"v23, s[64:67], s84 offen offset:1024 lds\n" \
M_"v[60:63], a[248:251], a[192:195], v[60:63]\n" \
X_"a[162:163], v32 offset:33792\n" \
"v_mul_u32_u24_e32 v222, 0x480, v222\n" \
M_"v[64:67], a[188:191], a[128:131], 0\n" \
X_"a[224:225], v32 offset:40960\n" \
"v_mad_u32_u24 v222, v220, s5, v222\n" \
M_"v[64:67], a[252:255], a[192:195], v[64:67]\n" \
R_"a[128:131], v28 offset:49152\n" \
"v_cmp_gt_u32_e64 s[4:5], s4, v215\n" \
M_"v[80:83], a[188:191], a[132:135], 0\n" \
R_"a[192:195], v29 offset:49152\n" \
"v_cndmask_b32_e64 v215, v221, v218, vcc\n" \
M_"v[80:83], a[252:255], a[196:199], v[80:83]\n" \
"s_movk_i32 s84, 0x3900\n" \
M_"v[76:79], a[184:187], a[132:135], 0\n" \
B_"v22, s[64:67], s84 offen offset:2048 lds\n" \
M_"v[76:79], a[248:251], a[196:199], v[76:79]\n" \
"s_movk_i32 s84, 0x5500\n" \
M_"v[72:75], a[180:183], a[132:135], 0\n" \
B_"v23, s[64:67], s84 offen offset:3072 lds\n" \
M_"v[72:75], a[244:247], a[196:199], v[72:75]\n" \
"s_add_u32 m0, s31, 0x0\n" \
"s_mov_b32 s84, 0x100000\n" \
M_"v[68:71], a[176:179], a[132:135], 0\n" \
B_"v26, s[68:71], s84 offen offset:0 lds\n" \
M_"v[68:71], a[240:243], a[196:199], v[68:71]\n" \
R_"a[132:135], v28 offset:51200\n" \
"v_lshrrev_b32_e32 v218, 1, v220\n" \
M_"v[84:87], a[176:179], a[136:139], 0\n" \
R_"a[196:199], v29 offset:51200\n" \
"v_or_b32_e64 v214, v222, v214\n" \
M_"v[84:87], a[240:243], a[200:203], v[84:87]\n" \
"s_addk_i32 s84, 0x7c00\n" \
M_"v[88:91], a[180:183], a[136:139], 0\n" \
B_"v26, s[68:71], s84 offen offset:1024 lds\n" \
M_"v[88:91], a[244:247], a[200:203], v[88:91]\n" \
X_"a[226:227], v32 offset:41984\n" \
"v_mov_b32_e32 v222, 2.0\n" \
M_"v[92:95], a[184:187], a[136:139], 0\n" \
"s_addk_i32 s84, 0x7c00\n" \
M_"v[92:95], a[248:251], a[200:203], v[92:95]\n" \
B_"v27, s[68:71], s84 offen offset:2048 lds\n" \
M_"v[96:99], a[188:191], a[136:139], 0\n" \
X_"a[164:165], v33 offset:32768\n" \
"v_and_b32_e32 v218, 4, v218\n" \
M_"v[96:99], a[252:255], a[200:203], v[96:99]\n" \
R_"a[136:139], v28 offset:53248\n" \
"v_and_b32_e64 v217, 2, v217\n" \
M_"v[112:115], a[188:191], a[140:143], 0\n" \
R_"a[200:203], v29 offset:53248\n" \
"v_bitop3_b32 v215, v215, v218, v217 bitop3:0x1e\n" \
M_"v[112:115], a[252:255], a[204:207], v[112:115]\n" \
"s_addk_i32 s84, 0x7c00\n" \
M_"v[108:111], a[184:187], a[140:143], 0\n" \
B_"v27, s[68:71], s84 offen offset:3072 lds\n" \
"s_mov_b64 exec, 0\n" \
"s_swappc_b64 s[88:89], s[86:87]\n" \
"s_mov_b64 exec, -1\n" \
M_"v[108:111], a[248:251], a[204:207], v[108:111]\n" \
"s_add_u32 m0, s30, 0x1000\n" \
"s_mov_b32 s84, 0x8100\n" \
M_"v[104:107], a[180:183], a[140:143], 0\n" \
B_"v22, s[64:67], s84 offen offset:0 lds\n" \
M_"v[104:107], a[244:247], a[204:207], v[104:107]\n" \
X_"a[166:167], v33 offset:33792\n" \
"v_lshlrev_b32_e64 v223, 1, v220\n" \
M_"v[100:103], a[176:179], a[140:143], 0\n" \
X_"a[228:229], v33 offset:40960\n" \
"v_mov_b32_e64 v224, 2.0\n" \
M_"v[100:103], a[240:243], a[204:207], v[100:103]\n" \
R_"a[140:143], v28 offset:55296\n" \
"v_cndmask_b32_e64 v193, v222, v214, vcc\n" \
M_"v[116:119], a[176:179], a[144:147], 0\n" \
R_"a[204:207], v29 offset:55296\n" \
"v_lshlrev_b32_e64 v214, 7, v220\n" \
M_"v[116:119], a[240:243], a[208:211], v[116:119]\n" \
"s_addk_i32 s84, 0x1c00\n" \
M_"v[120:123], a[180:183], a[144:147], 0\n" \
B_"v23, s[64:67], s84 offen offset:1024 lds\n" \
M_"v[120:123], a[244:247], a[208:211], v[120:123]\n" \
"s_addk_i32 s84, 0x1c00\n" \
M_"v[124:127], a[184:187], a[144:147], 0\n" \
B_"v22, s[64:67], s84 offen offset:2048 lds\n" \
M_"v[124:127], a[248:251], a[208:211], v[124:127]\n" \
"s_addk_i32 s84, 0x1c00\n" \
".fill (L_pb + 3808 - .) / 4, 4, 0xBF800000\n" \
"L_pp1:\n" \
"s_cbranch_execz L_pret\n" \
M_"v[128:131], a[188:191], a[144:147], 0\n" \
B_"v23, s[64:67], s84 offen offset:3072 lds\n" \
M_"v[128:131], a[252:255], a[208:211], v[128:131]\n" \
R_"a[144:147], v28 offset:32768\n" \
"v_lshlrev_b32_e32 v215, 4, v215\n" \
M_"v[144:147], a[188:191], a[148:151], 0\n" \
R_"a[208:211], v29 offset:32768\n" \
"s_mul_i32 s39, s3, 18\n" \
M_"v[144:147], a[252:255], a[212:215], v[144:147]\n" \
"s_add_u32 m0, s31, 0x4000\n" \
"s_mov_b32 s84, 0x100000\n" \
M_"v[140:143], a[184:187], a[148:151], 0\n" \
B_"v26, s[72:75], s84 offen offset:0 lds\n" \
M_"v[140:143], a[248:251], a[212:215], v[140:143]\n" \
X_"a[230:231], v33 offset:41984\n" \
"v_add3_u32 v214, v214, s34, v215\n" \
M_"v[136:139], a[180:183], a[148:151], 0\n" \
"s_addk_i32 s84, 0x7c00\n" \
M_"v[136:139], a[244:247], a[212:215], v[136:139]\n" \
B_"v26, s[72:75], s84 offen offset:1024 lds\n" \
M_"v[132:135], a[176:179], a[148:151], 0\n" \
X_"a[168:169], v34 offset:32768\n" \
"v_cndmask_b32_e64 v195, v224, v223, s[4:5]\n" \
M_"v[132:135], a[240:243], a[212:215], v[132:135]\n" \
R_"a[148:151], v28 offset:34816\n" \
"s_movk_i32 s4, 0x1f8\n" \
M_"v[148:151], a[176:179], a[152:155], 0\n" \
R_"a[212:215], v29 offset:34816\n" \
"v_mov_b32_e32 v215, v213\n" \
M_"v[148:151], a[240:243], a[216:219], v[148:151]\n" \
X_"a[170:171], v34 offset:33792\n" \
"s_add_i32 s35, s35, s39\n" \
M_"v[152:155], a[180:183], a[152:155], 0\n" \
X_"a[232:233], v34 offset:40960\n" \
"v_cmp_gt_u32_e32 vcc, s4, v216\n" \
M_"v[152:155], a[244:247], a[216:219], v[152:155]\n" \
X_"a[234:235], v34 offset:41984\n" \
"v_add_u32_e64 v212, s35, v212\n" \
M_"v[156:159], a[184:187], a[152:155], 0\n" \
X_"a[172:173], v35 offset:32768\n" \
"v_mov_b32_e64 v219, 2.0\n" \
M_"v[156:159], a[248:251], a[216:219], v[156:159]\n" \
X_"a[174:175], v35 offset:33792\n" \
"v_cndmask_b32_e64 v197, v215, v214, vcc\n" \
M_"v[160:163], a[188:191], a[152:155], 0\n" \
X_"a[236:237], v35 offset:40960\n" \
"s_movk_i32 s35, 0x120\n" \
M_"v[160:163], a[252:255], a[216:219], v[160:163]\n" \
R_"a[152:155], v28 offset:36864\n" \
"v_mul_u32_u24_e32 v214, 0xd80, v212\n" \
M_"v[176:179], a[188:191], a[156:159], 0\n" \
R_"a[216:219], v29 offset:36864\n" \
"v_cmp_gt_u32_e32 vcc, s35, v20\n" \
M_"v[176:179], a[252:255], a[220:223], v[176:179]\n" \
X_"a[238:239], v35 offset:41984\n" \
"v_lshrrev_b32_e64 v215, 1, v212\n" \
M_"v[172:175], a[184:187], a[156:159], 0\n" \
"s_and_b32 s4, s2, 7\n" \
"v_cndmask_b32_e32 v194, v219, v214, vcc\n" \
M_"v[172:175], a[248:251], a[220:223], v[172:175]\n" \
"v_lshlrev_b32_e32 v214, 7, v212\n" \
"v_lshrrev_b32_e32 v212, 2, v212\n" \
M_"v[168:171], a[180:183], a[156:159], 0\n" \
"v_and_b32_e64 v212, 2, v212\n" \
"v_and_or_b32 v212, v215, 1, v212\n" \
M_"v[168:171], a[244:247], a[220:223], v[168:171]\n" \
"s_lshl_b32 s5, s4, 2\n" \
"s_bfe_u32 s38, s2, 0x20003\n" \
M_"v[164:167], a[176:179], a[156:159], 0\n" \
"v_lshlrev_b32_e32 v212, 5, v212\n" \
"s_movk_i32 s35, 0x60\n" \
M_"v[164:167], a[240:243], a[220:223], v[164:167]\n" \
R_"a[156:159], v28 offset:38912\n" \
R_"a[220:223], v29 offset:38912\n" \
"s_waitcnt lgkmcnt(15)\n" \
M_"a[0:3], a[160:163], a[128:131], a[0:3]\n" \
"s_addk_i32 s84, 0x7c00\n" \
M_"a[0:3], a[224:227], a[192:195], a[0:3]\n" \
B_"v27, s[72:75], s84 offen offset:2048 lds\n" \
M_"a[4:7], a[164:167], a[128:131], a[4:7]\n" \
X_"a[176:177], v36 offset:32768\n" \
"s_or_b32 s5, s5, s38\n" \
"s_waitcnt lgkmcnt(15)\n" \
M_"a[4:7], a[228:231], a[192:195], a[4:7]\n" \
"s_addk_i32 s84, 0x7c00\n" \
"s_waitcnt lgkmcnt(11)\n" \
M_"a[8:11], a[168:171], a[128:131], a[8:11]\n" \
B_"v27, s[72:75], s84 offen offset:3072 lds\n" \
"s_waitcnt lgkmcnt(9)\n" \
M_"a[8:11], a[232:235], a[192:195], a[8:11]\n" \
X_"a[178:179], v36 offset:33792\n" \
"v_bitop3_b32 v212, v212, s35, v214 bitop3:0x36\n" \
"s_waitcnt lgkmcnt(8)\n" \
M_"a[12:15], a[172:175], a[128:131], a[12:15]\n" \
X_"a[240:241], v36 offset:40960\n" \
"s_lshr_b32 s36, s2, 3\n" \
"s_waitcnt lgkmcnt(5)\n" \
M_"a[12:15], a[236:239], a[192:195], a[12:15]\n" \
"v_add_u32_e32 v212, s34, v212\n" \
"s_mul_i32 s34, s5, 0x12000\n" \
M_"a[28:31], a[172:175], a[132:135], a[28:31]\n" \
"s_or_b32 s40, s34, 0x6000000\n" \
"s_mul_i32 s34, s36, 0x90000\n" \
M_"a[28:31], a[236:239], a[196:199], a[28:31]\n" \
X_"a[242:243], v36 offset:41984\n" \
"s_lshl_b32 s36, s3, 14\n" \
M_"a[24:27], a[168:171], a[132:135], a[24:27]\n" \
"s_add_i32 s34, s34, s36\n" \
"s_lshl_b32 s38, s5, 8\n" \
M_"a[24:27], a[232:235], a[196:199], a[24:27]\n" \
"s_add_i32 s41, s34, 0x4240000\n" \
"s_mul_i32 s34, s5, 36\n" \
M_"a[20:23], a[164:167], a[132:135], a[20:23]\n" \
"s_add_i32 s5, s7, s40\n" \
"s_addk_i32 s5, 0x2000\n" \
M_"a[20:23], a[228:231], a[196:199], a[20:23]\n" \
X_"a[180:181], v37 offset:32768\n" \
"s_mov_b32 s37, 2.0\n" \
M_"a[16:19], a[160:163], a[132:135], a[16:19]\n" \
"s_or_b32 s38, s38, 0x5be8000\n" \
M_"a[16:19], a[224:227], a[196:199], a[16:19]\n" \
"s_cmp_lg_u32 s3, 0\n" \
"s_cselect_b32 s36, s37, s5\n" \
M_"a[32:35], a[160:163], a[136:139], a[32:35]\n" \
"s_mov_b32 m0, s32\n" \
X_"a[182:183], v37 offset:33792\n" \
M_"a[32:35], a[224:227], a[200:203], a[32:35]\n" \
"s_mov_b32 exec_lo, 0x0\n" \
"s_mov_b32 exec_hi, 0xff\n" \
"global_load_lds_dword v[184:185], off\n" \
"s_mov_b64 exec, -1\n" \
M_"a[36:39], a[164:167], a[136:139], a[36:39]\n" \
"s_lshl_b32 s5, s6, 2\n" \
"s_or_b32 s5, s38, s5\n" \
M_"a[36:39], a[228:231], a[200:203], a[36:39]\n" \
X_"a[244:245], v37 offset:40960\n" \
"s_lshl_b32 s35, s3, 10\n" \
M_"a[40:43], a[168:171], a[136:139], a[40:43]\n" \
"s_cmpk_lt_u32 s3, 3\n" \
"s_cselect_b32 s37, s37, s5\n" \
M_"a[40:43], a[232:235], a[200:203], a[40:43]\n" \
"s_mov_b32 m0, s32\n" \
"s_lshl_b32 s5, s4, 16\n" \
M_"a[44:47], a[172:175], a[136:139], a[44:47]\n" \
"s_mov_b32 exec_lo, 0xffff0000\n" \
"s_mov_b32 exec_hi, 0x0\n" \
"global_load_lds_dword v[186:187], off\n" \
"s_mov_b64 exec, -1\n" \
M_"a[44:47], a[236:239], a[200:203], a[44:47]\n" \
X_"a[246:247], v37 offset:41984\n" \
"s_lshl_b32 s4, s4, 11\n" \
M_"a[60:63], a[172:175], a[140:143], a[60:63]\n" \
"v_cndmask_b32_e32 v198, v213, v212, vcc\n" \
"v_or_b32_e32 v212, s35, v20\n" \
M_"a[60:63], a[236:239], a[204:207], a[60:63]\n" \
"s_or_b32 s39, s40, s35\n" \
"s_or_b32 s55, s41, s4\n" \
M_"a[56:59], a[168:171], a[140:143], a[56:59]\n" \
X_"a[184:185], v38 offset:32768\n" \
"v_or_b32_e32 v199, 0x22000, v212\n" \
M_"a[56:59], a[232:235], a[204:207], a[56:59]\n" \
"s_add_i32 s35, s39, s7\n" \
"s_add_i32 s54, s41, s5\n" \
M_"a[52:55], a[164:167], a[140:143], a[52:55]\n" \
"s_add_i32 s55, s55, 0x80000\n" \
"v_mov_b32_e32 v215, 0xffff\n" \
M_"a[52:55], a[228:231], a[204:207], a[52:55]\n" \
X_"a[186:187], v38 offset:33792\n" \
"s_and_b32 s4, s3, 1\n" \
M_"a[48:51], a[160:163], a[140:143], a[48:51]\n" \
"s_lshl_b32 s5, s4, 4\n" \
"v_lshrrev_b32_e64 v212, 4, v20\n" \
M_"a[48:51], a[224:227], a[204:207], a[48:51]\n" \
"v_lshrrev_b32_e32 v218, 6, v20\n" \
"v_and_b32_e32 v219, 12, v218\n" \
M_"a[64:67], a[160:163], a[144:147], a[64:67]\n" \
X_"a[248:249], v38 offset:40960\n" \
"v_and_or_b32 v214, v212, 15, s5\n" \
M_"a[64:67], a[224:227], a[208:211], a[64:67]\n" \
"v_cmp_gt_u32_e64 vcc, v219, v214\n" \
"v_mov_b32_e32 v220, 0xffff0000\n" \
M_"a[68:71], a[164:167], a[144:147], a[68:71]\n" \
"s_lshl_b32 s5, s2, 2\n" \
"v_cndmask_b32_e64 v217, v215, 0, vcc\n" \
M_"a[68:71], a[228:231], a[208:211], a[68:71]\n" \
X_"a[250:251], v38 offset:41984\n" \
"v_cmp_lt_u32_e32 vcc, v219, v214\n" \
M_"a[72:75], a[168:171], a[144:147], a[72:75]\n" \
"v_mov_b32_e32 v213, s23\n" \
"s_or_b32 s5, s5, s3\n" \
M_"a[72:75], a[232:235], a[208:211], a[72:75]\n" \
"v_cndmask_b32_e32 v221, 0, v220, vcc\n" \
"v_or_b32_e32 v12, v217, v221\n" \
M_"a[76:79], a[172:175], a[144:147], a[76:79]\n" \
"v_or_b32_e32 v217, 2, v219\n" \
"v_cmp_gt_u32_e32 vcc, v217, v214\n" \
M_"a[76:79], a[236:239], a[208:211], a[76:79]\n" \
X_"a[188:189], v39 offset:32768\n" \
"v_mov_b32_e64 v216, s25\n" \
M_"a[92:95], a[172:175], a[148:151], a[92:95]\n" \
"s_movk_i32 s40, 0xf400\n" \
"v_cndmask_b32_e64 v221, v215, 0, vcc\n" \
M_"a[92:95], a[236:239], a[212:215], a[92:95]\n" \
"v_cmp_lt_u32_e32 vcc, v217, v214\n" \
"s_lshl_b32 s4, s4, 6\n" \
M_"a[88:91], a[168:171], a[148:151], a[88:91]\n" \
X_"a[190:191], v39 offset:33792\n" \
"v_cndmask_b32_e32 v217, 0, v220, vcc\n" \
M_"a[88:91], a[232:235], a[212:215], a[88:91]\n" \
"v_or_b32_e32 v13, v221, v217\n" \
"v_or_b32_e32 v217, 16, v219\n" \
M_"a[84:87], a[164:167], a[148:151], a[84:87]\n" \
"v_cmp_gt_u32_e32 vcc, v217, v214\n" \
"s_nop 1\n" \
"v_cndmask_b32_e64 v221, v215, 0, vcc\n" \
M_"a[84:87], a[228:231], a[212:215], a[84:87]\n" \
X_"a[252:253], v39 offset:40960\n" \
"v_cmp_lt_u32_e64 vcc, v217, v214\n" \
M_"a[80:83], a[160:163], a[148:151], a[80:83]\n" \
"s_nop 0\n" \
"v_cndmask_b32_e32 v217, 0, v220, vcc\n" \
"v_or_b32_e64 v14, v221, v217\n" \
M_"a[80:83], a[224:227], a[212:215], a[80:83]\n" \
"v_or_b32_e32 v217, 18, v219\n" \
"v_cmp_gt_u32_e32 vcc, v217, v214\n" \
M_"a[96:99], a[160:163], a[152:155], a[96:99]\n" \
X_"a[254:255], v39 offset:41984\n" \
"v_cndmask_b32_e64 v215, v215, 0, vcc\n" \
M_"a[96:99], a[224:227], a[216:219], a[96:99]\n" \
"v_cmp_lt_u32_e32 vcc, v217, v214\n" \
"s_nop 1\n" \
"v_cndmask_b32_e64 v214, 0, v220, vcc\n" \
M_"a[100:103], a[164:167], a[152:155], a[100:103]\n" \
"v_or_b32_e64 v15, v215, v214\n" \
"v_mad_u64_u32 v[214:215], s[6:7], s5, 42, v[212:213]\n" \
M_"a[100:103], a[228:231], a[216:219], a[100:103]\n" \
"s_movk_i32 s5, 0x1440\n" \
"v_cmp_gt_u32_e32 vcc, s5, v214\n" \
M_"a[104:107], a[168:171], a[152:155], a[104:107]\n" \
"v_min_u32_e32 v217, 0xa43f, v214\n" \
"v_mov_b32_e64 v214, s24\n" \
M_"a[104:107], a[232:235], a[216:219], a[104:107]\n" \
"v_cndmask_b32_e32 v215, v216, v213, vcc\n" \
"v_mov_b32_e32 v213, s22\n" \
M_"a[108:111], a[172:175], a[152:155], a[108:111]\n" \
"s_movk_i32 s6, 0xffee\n" \
"v_cndmask_b32_e32 v214, v214, v213, vcc\n" \
M_"a[108:111], a[236:239], a[216:219], a[108:111]\n" \
"v_add_u32_e32 v213, 0xffffebc0, v217\n" \
"s_lshl_b32 s5, s2, 5\n" \
"s_waitcnt lgkmcnt(15)\n" \
M_"a[124:127], a[172:175], a[156:159], a[124:127]\n" \
"v_cndmask_b32_e32 v213, v213, v217, vcc\n" \
"v_lshlrev_b32_e32 v216, 7, v213\n" \
M_"a[124:127], a[236:239], a[220:223], a[124:127]\n" \
"v_mul_u32_u24_e32 v213, 0xe39, v212\n" \
"v_mov_b32_e64 v217, 0\n" \
M_"a[120:123], a[168:171], a[156:159], a[120:123]\n" \
"v_lshrrev_b32_e64 v213, 16, v213\n" \
"v_lshl_add_u64 v[16:17], v[214:215], 0, v[216:217]\n" \
M_"a[120:123], a[232:235], a[220:223], a[120:123]\n" \
"v_mad_i32_i24 v214, v213, s6, v212\n" \
"s_add_i32 s5, s5, 32\n" \
M_"a[116:119], a[164:167], a[156:159], a[116:119]\n" \
"v_lshlrev_b32_e32 v215, 4, v214\n" \
"v_add_u32_e64 v216, s5, v215\n" \
M_"a[116:119], a[228:231], a[220:223], a[116:119]\n" \
"v_and_b32_e32 v216, 0xf0, v216\n" \
"v_cmp_gt_u32_e64 vcc, 16, v214\n" \
M_"a[112:115], a[160:163], a[156:159], a[112:115]\n" \
"s_movk_i32 s7, 0xf800\n" \
"v_cndmask_b32_e32 v214, v215, v216, vcc\n" \
M_"a[112:115], a[224:227], a[220:223], a[112:115]\n" \
"s_waitcnt vmcnt(18)\n" \
"v_readfirstlane_b32 s62, v180\n" \
"s_nop 3\n" \
"s_add_u32 s62, s62, 0x9E3779B1\n" \
"s_waitcnt vmcnt(2) lgkmcnt(0)\n" \
"s_barrier\n" \
M_"v[52:55], a[176:179], a[128:131], v[52:55]\n" \
"s_add_u32 m0, s30, 0x8000\n" \
"s_movk_i32 s84, 0x180\n" \
M_"v[52:55], a[240:243], a[192:195], v[52:55]\n" \
B_"v22, s[64:67], s84 offen offset:0 lds\n" \
M_"v[56:59], a[180:183], a[128:131], v[56:59]\n" \
X_"a[160:161], v32 offset:0\n" \
"v_or_b32_e64 v215, 64, v212\n" \
M_"v[56:59], a[244:247], a[192:195], v[56:59]\n" \
"s_movk_i32 s84, 0x1d80\n" \
M_"v[60:63], a[184:187], a[128:131], v[60:63]\n" \
B_"v23, s[64:67], s84 offen offset:1024 lds\n" \
M_"v[60:63], a[248:251], a[192:195], v[60:63]\n" \
X_"a[162:163], v32 offset:1024\n" \
"v_mul_u32_u24_e32 v216, 0xe39, v215\n" \
M_"v[64:67], a[188:191], a[128:131], v[64:67]\n" \
X_"a[224:225], v32 offset:8192\n" \
"v_lshrrev_b32_e32 v216, 16, v216\n" \
M_"v[64:67], a[252:255], a[192:195], v[64:67]\n" \
R_"a[128:131], v28 offset:16384\n" \
"v_mad_i32_i24 v215, v216, s6, v215\n" \
M_"v[80:83], a[188:191], a[132:135], v[80:83]\n" \
R_"a[192:195], v29 offset:16384\n" \
"v_lshlrev_b32_e64 v217, 4, v215\n" \
M_"v[80:83], a[252:255], a[196:199], v[80:83]\n" \
"s_movk_i32 s84, 0x3980\n" \
M_"v[76:79], a[184:187], a[132:135], v[76:79]\n" \
B_"v22, s[64:67], s84 offen offset:2048 lds\n" \
M_"v[76:79], a[248:251], a[196:199], v[76:79]\n" \
"s_movk_i32 s84, 0x5580\n" \
M_"v[72:75], a[180:183], a[132:135], v[72:75]\n" \
B_"v23, s[64:67], s84 offen offset:3072 lds\n" \
M_"v[72:75], a[244:247], a[196:199], v[72:75]\n" \
"s_add_u32 m0, s31, 0x8000\n" \
"s_mov_b32 s84, 0x180000\n" \
M_"v[68:71], a[176:179], a[132:135], v[68:71]\n" \
B_"v26, s[68:71], s84 offen offset:0 lds\n" \
M_"v[68:71], a[240:243], a[196:199], v[68:71]\n" \
R_"a[132:135], v28 offset:18432\n" \
"v_add_u32_e64 v220, s5, v217\n" \
M_"v[84:87], a[176:179], a[136:139], v[84:87]\n" \
R_"a[196:199], v29 offset:18432\n" \
"v_and_b32_e32 v220, 0xf0, v220\n" \
M_"v[84:87], a[240:243], a[200:203], v[84:87]\n" \
"s_addk_i32 s84, 0x7c00\n" \
M_"v[88:91], a[180:183], a[136:139], v[88:91]\n" \
B_"v26, s[68:71], s84 offen offset:1024 lds\n" \
M_"v[88:91], a[244:247], a[200:203], v[88:91]\n" \
X_"a[226:227], v32 offset:9216\n" \
"v_cmp_gt_u32_e32 vcc, 16, v215\n" \
M_"v[92:95], a[184:187], a[136:139], v[92:95]\n" \
"s_addk_i32 s84, 0x7c00\n" \
M_"v[92:95], a[248:251], a[200:203], v[92:95]\n" \
B_"v27, s[68:71], s84 offen offset:2048 lds\n" \
M_"v[96:99], a[188:191], a[136:139], v[96:99]\n" \
X_"a[164:165], v33 offset:0\n" \
"v_cndmask_b32_e32 v215, v217, v220, vcc\n" \
M_"v[96:99], a[252:255], a[200:203], v[96:99]\n" \
R_"a[136:139], v28 offset:20480\n" \
"v_or_b32_e32 v217, 0x80, v212\n" \
M_"v[112:115], a[188:191], a[140:143], v[112:115]\n" \
R_"a[200:203], v29 offset:20480\n" \
"v_mul_u32_u24_e32 v220, 0xe39, v217\n" \
M_"v[112:115], a[252:255], a[204:207], v[112:115]\n" \
"s_addk_i32 s84, 0x7c00\n" \
M_"v[108:111], a[184:187], a[140:143], v[108:111]\n" \
B_"v27, s[68:71], s84 offen offset:3072 lds\n" \
M_"v[108:111], a[248:251], a[204:207], v[108:111]\n" \
"s_add_u32 m0, s30, 0x9000\n" \
"s_mov_b32 s84, 0x8180\n" \
M_"v[104:107], a[180:183], a[140:143], v[104:107]\n" \
B_"v22, s[64:67], s84 offen offset:0 lds\n" \
M_"v[104:107], a[244:247], a[204:207], v[104:107]\n" \
X_"a[166:167], v33 offset:1024\n" \
"v_lshrrev_b32_e32 v220, 16, v220\n" \
M_"v[100:103], a[176:179], a[140:143], v[100:103]\n" \
X_"a[228:229], v33 offset:8192\n" \
"v_mad_i32_i24 v217, v220, s6, v217\n" \
M_"v[100:103], a[240:243], a[204:207], v[100:103]\n" \
R_"a[140:143], v28 offset:22528\n" \
"v_lshlrev_b32_e64 v221, 4, v217\n" \
M_"v[116:119], a[176:179], a[144:147], v[116:119]\n" \
R_"a[204:207], v29 offset:22528\n" \
"v_add_u32_e64 v222, s5, v221\n" \
M_"v[116:119], a[240:243], a[208:211], v[116:119]\n" \
"s_addk_i32 s84, 0x1c00\n" \
M_"v[120:123], a[180:183], a[144:147], v[120:123]\n" \
B_"v23, s[64:67], s84 offen offset:1024 lds\n" \
M_"v[120:123], a[244:247], a[208:211], v[120:123]\n" \
"s_addk_i32 s84, 0x1c00\n" \
M_"v[124:127], a[184:187], a[144:147], v[124:127]\n" \
B_"v22, s[64:67], s84 offen offset:2048 lds\n" \
M_"v[124:127], a[248:251], a[208:211], v[124:127]\n" \
"s_addk_i32 s84, 0x1c00\n" \
M_"v[128:131], a[188:191], a[144:147], v[128:131]\n" \
B_"v23, s[64:67], s84 offen offset:3072 lds\n" \
M_"v[128:131], a[252:255], a[208:211], v[128:131]\n" \
R_"a[144:147], v28 offset:0\n" \
"v_and_b32_e32 v222, 0xf0, v222\n" \
M_"v[144:147], a[188:191], a[148:151], v[144:147]\n" \
R_"a[208:211], v29 offset:0\n" \
"v_cmp_gt_u32_e32 vcc, 16, v217\n" \
M_"v[144:147], a[252:255], a[212:215], v[144:147]\n" \
"s_add_u32 m0, s31, 0xc000\n" \
"s_mov_b32 s84, 0x180000\n" \
M_"v[140:143], a[184:187], a[148:151], v[140:143]\n" \
B_"v26, s[72:75], s84 offen offset:0 lds\n" \
M_"v[140:143], a[248:251], a[212:215], v[140:143]\n" \
X_"a[230:231], v33 offset:9216\n" \
"v_cndmask_b32_e64 v217, v221, v222, vcc\n" \
M_"v[136:139], a[180:183], a[148:151], v[136:139]\n" \
"s_addk_i32 s84, 0x7c00\n" \
M_"v[136:139], a[244:247], a[212:215], v[136:139]\n" \
B_"v26, s[72:75], s84 offen offset:1024 lds\n" \
M_"v[132:135], a[176:179], a[148:151], v[132:135]\n" \
X_"a[168:169], v34 offset:0\n" \
"v_or_b32_e32 v221, 0xc0, v212\n" \
M_"v[132:135], a[240:243], a[212:215], v[132:135]\n" \
R_"a[148:151], v28 offset:2048\n" \
"v_mul_u32_u24_e32 v222, 0xe39, v221\n" \
M_"v[148:151], a[176:179], a[152:155], v[148:151]\n" \
R_"a[212:215], v29 offset:2048\n" \
"v_lshrrev_b32_e32 v222, 16, v222\n" \
M_"v[148:151], a[240:243], a[216:219], v[148:151]\n" \
X_"a[170:171], v34 offset:1024\n" \
"v_mad_i32_i24 v221, v222, s6, v221\n" \
M_"v[152:155], a[180:183], a[152:155], v[152:155]\n" \
X_"a[232:233], v34 offset:8192\n" \
"v_lshlrev_b32_e64 v223, 4, v221\n" \
M_"v[152:155], a[244:247], a[216:219], v[152:155]\n" \
X_"a[234:235], v34 offset:9216\n" \
"v_add_u32_e64 v224, s5, v223\n" \
M_"v[156:159], a[184:187], a[152:155], v[156:159]\n" \
X_"a[172:173], v35 offset:0\n" \
"v_and_b32_e32 v224, 0xf0, v224\n" \
M_"v[156:159], a[248:251], a[216:219], v[156:159]\n" \
X_"a[174:175], v35 offset:1024\n" \
"v_cmp_gt_u32_e64 vcc, 16, v221\n" \
M_"v[160:163], a[188:191], a[152:155], v[160:163]\n" \
X_"a[236:237], v35 offset:8192\n" \
"v_cndmask_b32_e64 v221, v223, v224, vcc\n" \
M_"v[160:163], a[252:255], a[216:219], v[160:163]\n" \
R_"a[152:155], v28 offset:4096\n" \
"v_or_b32_e32 v223, 0x100, v212\n" \
M_"v[176:179], a[188:191], a[156:159], v[176:179]\n" \
R_"a[216:219], v29 offset:4096\n" \
"v_mul_u32_u24_e32 v224, 0xe39, v223\n" \
M_"v[176:179], a[252:255], a[220:223], v[176:179]\n" \
X_"a[238:239], v35 offset:9216\n" \
"v_lshrrev_b32_e64 v224, 16, v224\n" \
M_"v[172:175], a[184:187], a[156:159], v[172:175]\n" \
"v_mad_i32_i24 v223, v224, s6, v223\n" \
"v_lshlrev_b32_e64 v225, 4, v223\n" \
M_"v[172:175], a[248:251], a[220:223], v[172:175]\n" \
"v_add_u32_e64 v226, s5, v225\n" \
"v_and_b32_e32 v226, 0xf0, v226\n" \
M_"v[168:171], a[180:183], a[156:159], v[168:171]\n" \
"v_cmp_gt_u32_e32 vcc, 16, v223\n" \
"s_nop 1\n" \
"v_cndmask_b32_e64 v223, v225, v226, vcc\n" \
M_"v[168:171], a[244:247], a[220:223], v[168:171]\n" \
"v_or_b32_e32 v225, 0x140, v212\n" \
"v_mul_u32_u24_e32 v226, 0xe39, v225\n" \
M_"v[164:167], a[176:179], a[156:159], v[164:167]\n" \
"v_lshrrev_b32_e64 v226, 16, v226\n" \
"v_mad_i32_i24 v225, v226, s6, v225\n" \
M_"v[164:167], a[240:243], a[220:223], v[164:167]\n" \
R_"a[156:159], v28 offset:6144\n" \
R_"a[220:223], v29 offset:6144\n" \
"s_waitcnt lgkmcnt(15)\n" \
M_"a[0:3], a[160:163], a[128:131], a[0:3]\n" \
"s_addk_i32 s84, 0x7c00\n" \
M_"a[0:3], a[224:227], a[192:195], a[0:3]\n" \
B_"v27, s[72:75], s84 offen offset:2048 lds\n" \
M_"a[4:7], a[164:167], a[128:131], a[4:7]\n" \
X_"a[176:177], v36 offset:0\n" \
"v_lshlrev_b32_e32 v227, 4, v225\n" \
"s_waitcnt lgkmcnt(15)\n" \
M_"a[4:7], a[228:231], a[192:195], a[4:7]\n" \
"s_addk_i32 s84, 0x7c00\n" \
"s_waitcnt lgkmcnt(11)\n" \
M_"a[8:11], a[168:171], a[128:131], a[8:11]\n" \
B_"v27, s[72:75], s84 offen offset:3072 lds\n" \
"s_waitcnt lgkmcnt(9)\n" \
M_"a[8:11], a[232:235], a[192:195], a[8:11]\n" \
X_"a[178:179], v36 offset:1024\n" \
"v_add_u32_e32 v228, s5, v227\n" \
"s_waitcnt lgkmcnt(8)\n" \
M_"a[12:15], a[172:175], a[128:131], a[12:15]\n" \
X_"a[240:241], v36 offset:8192\n" \
"v_and_b32_e32 v228, 0xf0, v228\n" \
"s_waitcnt lgkmcnt(5)\n" \
M_"a[12:15], a[236:239], a[192:195], a[12:15]\n" \
"v_cmp_gt_u32_e32 vcc, 16, v225\n" \
"s_nop 1\n" \
"v_cndmask_b32_e64 v225, v227, v228, vcc\n" \
M_"a[28:31], a[172:175], a[132:135], a[28:31]\n" \
"v_or_b32_e32 v227, 0x180, v212\n" \
"v_mul_u32_u24_e32 v228, 0xe39, v227\n" \
M_"a[28:31], a[236:239], a[196:199], a[28:31]\n" \
X_"a[242:243], v36 offset:9216\n" \
"v_lshrrev_b32_e64 v228, 16, v228\n" \
M_"a[24:27], a[168:171], a[132:135], a[24:27]\n" \
B_"v[200:203], v192, s[76:79], s34 offen offset:0\n" \
M_"a[24:27], a[232:235], a[196:199], a[24:27]\n" \
"v_mad_i32_i24 v227, v228, s6, v227\n" \
"v_lshlrev_b32_e64 v229, 4, v227\n" \
M_"a[20:23], a[164:167], a[132:135], a[20:23]\n" \
"v_add_u32_e64 v230, s5, v229\n" \
"v_and_b32_e32 v230, 0xf0, v230\n" \
M_"a[20:23], a[228:231], a[196:199], a[20:23]\n" \
X_"a[180:181], v37 offset:0\n" \
"v_cmp_gt_u32_e64 vcc, 16, v227\n" \
M_"a[16:19], a[160:163], a[132:135], a[16:19]\n" \
"s_nop 0\n" \
"v_cndmask_b32_e32 v227, v229, v230, vcc\n" \
"v_or_b32_e32 v229, 0x1c0, v212\n" \
M_"a[16:19], a[224:227], a[196:199], a[16:19]\n" \
"v_mul_u32_u24_e32 v230, 0xe39, v229\n" \
"v_lshrrev_b32_e64 v230, 16, v230\n" \
M_"a[32:35], a[160:163], a[136:139], a[32:35]\n" \
B_"v[204:207], v193, s[76:79], s34 offen offset:0\n" \
M_"a[32:35], a[224:227], a[200:203], a[32:35]\n" \
"s_mov_b32 m0, s32\n" \
X_"a[182:183], v37 offset:1024\n" \
M_"a[36:39], a[164:167], a[136:139], a[36:39]\n" \
"s_mov_b32 exec_lo, 0x0\n" \
"s_mov_b32 exec_hi, 0xff00\n" \
"global_load_lds_dword v[184:185], off\n" \
"s_mov_b64 exec, -1\n" \
M_"a[36:39], a[228:231], a[200:203], a[36:39]\n" \
X_"a[244:245], v37 offset:8192\n" \
"v_mad_i32_i24 v229, v230, s6, v229\n" \
M_"a[40:43], a[168:171], a[136:139], a[40:43]\n" \
"v_lshlrev_b32_e32 v231, 4, v229\n" \
"v_add_u32_e64 v232, s5, v231\n" \
M_"a[40:43], a[232:235], a[200:203], a[40:43]\n" \
"buffer_load_dword v208, v194, s[76:79], s34 offen offset:32\n" \
M_"a[44:47], a[172:175], a[136:139], a[44:47]\n" \
X_"a[246:247], v37 offset:9216\n" \
"v_and_b32_e32 v232, 0xf0, v232\n" \
M_"a[44:47], a[236:239], a[200:203], a[44:47]\n" \
"v_cmp_gt_u32_e32 vcc, 16, v229\n" \
"s_nop 1\n" \
"v_cndmask_b32_e64 v229, v231, v232, vcc\n" \
M_"a[60:63], a[172:175], a[140:143], a[60:63]\n" \
"v_or_b32_e32 v231, 0x200, v212\n" \
"v_mul_u32_u24_e32 v232, 0xe39, v231\n" \
M_"a[60:63], a[236:239], a[204:207], a[60:63]\n" \
"v_lshrrev_b32_e64 v232, 16, v232\n" \
"v_mad_i32_i24 v231, v232, s6, v231\n" \
M_"a[56:59], a[168:171], a[140:143], a[56:59]\n" \
"buffer_load_dword v209, v194, s[76:79], s34 offen offset:1184\n" \
M_"a[56:59], a[232:235], a[204:207], a[56:59]\n" \
X_"a[184:185], v38 offset:0\n" \
"v_lshlrev_b32_e64 v233, 4, v231\n" \
M_"a[52:55], a[164:167], a[140:143], a[52:55]\n" \
"v_add_u32_e32 v234, s5, v233\n" \
"s_movk_i32 s5, 0x480\n" \
M_"a[52:55], a[228:231], a[204:207], a[52:55]\n" \
X_"a[186:187], v38 offset:1024\n" \
"v_mad_u32_u24 v0, v213, s5, v214\n" \
M_"a[48:51], a[160:163], a[140:143], a[48:51]\n" \
"v_mul_u32_u24_e32 v213, 0x480, v216\n" \
"s_movk_i32 s6, 0xfc00\n" \
"s_nop 0\n" \
M_"a[48:51], a[224:227], a[204:207], a[48:51]\n" \
"v_add3_u32 v1, v213, v215, s6\n" \
"v_mul_u32_u24_e32 v213, 0x480, v220\n" \
M_"a[64:67], a[160:163], a[144:147], a[64:67]\n" \
"buffer_load_dword v211, v194, s[76:79], s34 offen offset:2336\n" \
M_"a[64:67], a[224:227], a[208:211], a[64:67]\n" \
X_"a[248:249], v38 offset:8192\n" \
"v_add3_u32 v2, v213, v217, s7\n" \
M_"a[68:71], a[164:167], a[144:147], a[68:71]\n" \
"v_mul_u32_u24_e32 v213, 0x480, v222\n" \
"v_add3_u32 v3, v213, v221, s40\n" \
M_"a[68:71], a[228:231], a[208:211], a[68:71]\n" \
X_"a[250:251], v38 offset:9216\n" \
"v_mul_u32_u24_e32 v213, 0x480, v226\n" \
M_"a[72:75], a[168:171], a[144:147], a[72:75]\n" \
"v_add3_u32 v5, v213, v225, s6\n" \
"v_mul_u32_u24_e32 v213, 0x480, v228\n" \
M_"a[72:75], a[232:235], a[208:211], a[72:75]\n" \
"s_mov_b32 s76, s20\n" \
"s_nop 0\n" \
"s_and_b32 s77, s21, 0xffff\n" \
M_"a[76:79], a[172:175], a[144:147], a[76:79]\n" \
"s_mov_b32 s78, 0x480\n" \
"v_add3_u32 v6, v213, v227, s7\n" \
M_"a[76:79], a[236:239], a[208:211], a[76:79]\n" \
"buffer_load_ushort v210, v195, s[76:79], 0 offen\n" \
M_"a[92:95], a[172:175], a[148:151], a[92:95]\n" \
X_"a[188:189], v39 offset:0\n" \
"v_mul_u32_u24_e32 v213, 0x480, v230\n" \
M_"a[92:95], a[236:239], a[212:215], a[92:95]\n" \
"v_add3_u32 v7, v213, v229, s40\n" \
"s_lshl_b32 s7, s3, 3\n" \
M_"a[88:91], a[168:171], a[148:151], a[88:91]\n" \
"s_mov_b32 s76, s8\n" \
"s_and_b32 s77, s9, 0xffff\n" \
M_"a[88:91], a[232:235], a[212:215], a[88:91]\n" \
"s_mov_b32 s78, 0x240000\n" \
X_"a[190:191], v39 offset:1024\n" \
M_"a[84:87], a[164:167], a[148:151], a[84:87]\n" \
"s_lshl_b32 s40, s3, 1\n" \
"s_and_b32 s7, s7, 16\n" \
".fill (L_pb + 7904 - .) / 4, 4, 0xBF800000\n" \
"L_pp2:\n" \
"s_cbranch_execz L_pret\n" \
M_"a[84:87], a[228:231], a[212:215], a[84:87]\n" \
X_"a[252:253], v39 offset:8192\n" \
"s_and_b32 s40, s40, 2\n" \
M_"a[80:83], a[160:163], a[148:151], a[80:83]\n" \
"s_and_b32 s6, s2, 7\n" \
"v_bfe_u32 v213, v20, 7, 1\n" \
M_"a[80:83], a[224:227], a[212:215], a[80:83]\n" \
"s_or_b32 s7, s40, s7\n" \
"v_and_b32_e32 v234, 0xf0, v234\n" \
M_"a[96:99], a[160:163], a[152:155], a[96:99]\n" \
X_"a[254:255], v39 offset:9216\n" \
"v_cmp_gt_u32_e64 vcc, 16, v231\n" \
M_"a[96:99], a[224:227], a[216:219], a[96:99]\n" \
"v_or3_b32 v213, s7, v213, v219\n" \
"v_and_b32_e64 v214, 32, v20\n" \
M_"a[100:103], a[164:167], a[152:155], a[100:103]\n" \
"s_lshl_b32 s6, s6, 5\n" \
"v_cndmask_b32_e32 v231, v233, v234, vcc\n" \
M_"a[100:103], a[228:231], a[216:219], a[100:103]\n" \
"v_mul_u32_u24_e64 v213, 18, v213\n" \
"v_mov_b32_e32 v215, 0x100\n" \
M_"a[104:107], a[168:171], a[152:155], a[104:107]\n" \
"v_mov_b32_e32 v216, s6\n" \
"v_cmp_eq_u32_e32 vcc, 0, v214\n" \
M_"a[104:107], a[232:235], a[216:219], a[104:107]\n" \
"v_and_or_b32 v213, v218, 1, v213\n" \
"v_lshlrev_b32_e64 v212, 3, v212\n" \
M_"a[108:111], a[172:175], a[152:155], a[108:111]\n" \
"v_cndmask_b32_e64 v214, v215, v216, vcc\n" \
"v_mad_u32_u24 v213, v213, s5, v214\n" \
M_"a[108:111], a[236:239], a[216:219], a[108:111]\n" \
"v_and_or_b32 v213, v20, 16, v213\n" \
"v_add_u32_e32 v9, 0x4800, v213\n" \
"s_waitcnt lgkmcnt(15)\n" \
M_"a[124:127], a[172:175], a[156:159], a[124:127]\n" \
"v_mul_u32_u24_e32 v213, 0x120, v218\n" \
"v_and_or_b32 v10, v212, 24, v213\n" \
M_"a[124:127], a[236:239], a[220:223], a[124:127]\n" \
"v_lshrrev_b32_e32 v213, 2, v20\n" \
"v_and_or_b32 v213, v213, 60, s4\n" \
M_"a[120:123], a[168:171], a[156:159], a[120:123]\n" \
"v_lshl_or_b32 v212, s3, 9, v212\n" \
"v_mad_u32_u24 v4, v224, s5, v223\n" \
M_"a[120:123], a[232:235], a[220:223], a[120:123]\n" \
"v_mad_u32_u24 v8, v232, s5, v231\n" \
"v_or_b32_e32 v18, 0x27000, v213\n" \
M_"a[116:119], a[164:167], a[156:159], a[116:119]\n" \
"v_or_b32_e32 v19, 0x27800, v212\n" \
"v_mov_b32_e32 v215, 0x1000c0c\n" \
M_"a[116:119], a[228:231], a[220:223], a[116:119]\n" \
"v_mov_b32_e32 v216, 0xc0c0100\n" \
"v_bfe_u32 v214, v20, 4, 1\n" \
M_"a[112:115], a[160:163], a[156:159], a[112:115]\n" \
"v_lshrrev_b32_e32 v212, 8, v20\n" \
"v_cmp_eq_u32_e32 vcc, 0, v214\n" \
"s_waitcnt vmcnt(7) lgkmcnt(0)\n" \
M_"a[112:115], a[224:227], a[220:223], a[112:115]\n" \
"s_barrier\n" \
M_"v[52:55], a[176:179], a[128:131], v[52:55]\n" \
"s_add_u32 m0, s30, 0x0\n" \
"s_movk_i32 s84, 0x200\n" \
M_"v[52:55], a[240:243], a[192:195], v[52:55]\n" \
B_"v22, s[64:67], s84 offen offset:0 lds\n" \
M_"v[56:59], a[180:183], a[128:131], v[56:59]\n" \
X_"a[160:161], v32 offset:32768\n" \
"v_bfe_u32 v213, v20, 4, 4\n" \
M_"v[56:59], a[244:247], a[192:195], v[56:59]\n" \
"s_movk_i32 s84, 0x1e00\n" \
M_"v[60:63], a[184:187], a[128:131], v[60:63]\n" \
B_"v23, s[64:67], s84 offen offset:1024 lds\n" \
M_"v[60:63], a[248:251], a[192:195], v[60:63]\n" \
X_"a[162:163], v32 offset:33792\n" \
"v_mov_b32_e32 v217, 0xc0c0302\n" \
M_"v[64:67], a[188:191], a[128:131], v[64:67]\n" \
X_"a[224:225], v32 offset:40960\n" \
"v_cndmask_b32_e32 v215, v215, v216, vcc\n" \
M_"v[64:67], a[252:255], a[192:195], v[64:67]\n" \
R_"a[128:131], v28 offset:49152\n" \
"v_mov_b32_e32 v216, 0x3020c0c\n" \
M_"v[80:83], a[188:191], a[132:135], v[80:83]\n" \
R_"a[192:195], v29 offset:49152\n" \
"v_lshl_or_b32 v214, v212, 2, v214\n" \
M_"v[80:83], a[252:255], a[196:199], v[80:83]\n" \
"s_movk_i32 s84, 0x3a00\n" \
M_"v[76:79], a[184:187], a[132:135], v[76:79]\n" \
B_"v22, s[64:67], s84 offen offset:2048 lds\n" \
M_"v[76:79], a[248:251], a[196:199], v[76:79]\n" \
"s_movk_i32 s84, 0x5600\n" \
M_"v[72:75], a[180:183], a[132:135], v[72:75]\n" \
B_"v23, s[64:67], s84 offen offset:3072 lds\n" \
M_"v[72:75], a[244:247], a[196:199], v[72:75]\n" \
"s_add_u32 m0, s31, 0x0\n" \
"s_mov_b32 s84, 0x200000\n" \
M_"v[68:71], a[176:179], a[132:135], v[68:71]\n" \
B_"v26, s[68:71], s84 offen offset:0 lds\n" \
M_"v[68:71], a[240:243], a[196:199], v[68:71]\n" \
R_"a[132:135], v28 offset:51200\n" \
"v_cndmask_b32_e32 v216, v216, v217, vcc\n" \
M_"v[84:87], a[176:179], a[136:139], v[84:87]\n" \
R_"a[196:199], v29 offset:51200\n" \
"v_mov_b32_e32 v217, 0xc0c0c0c\n" \
M_"v[84:87], a[240:243], a[200:203], v[84:87]\n" \
"s_addk_i32 s84, 0x7c00\n" \
M_"v[88:91], a[180:183], a[136:139], v[88:91]\n" \
B_"v26, s[68:71], s84 offen offset:1024 lds\n" \
M_"v[88:91], a[244:247], a[200:203], v[88:91]\n" \
X_"a[226:227], v32 offset:41984\n" \
"v_cmp_gt_u32_e32 vcc, v214, v213\n" \
M_"v[92:95], a[184:187], a[136:139], v[92:95]\n" \
"s_addk_i32 s84, 0x7c00\n" \
M_"v[92:95], a[248:251], a[200:203], v[92:95]\n" \
B_"v27, s[68:71], s84 offen offset:2048 lds\n" \
M_"v[96:99], a[188:191], a[136:139], v[96:99]\n" \
X_"a[164:165], v33 offset:32768\n" \
"v_or_b32_e32 v214, 2, v214\n" \
M_"v[96:99], a[252:255], a[200:203], v[96:99]\n" \
R_"a[136:139], v28 offset:53248\n" \
"s_lshr_b32 s4, s2, 5\n" \
M_"v[112:115], a[188:191], a[140:143], v[112:115]\n" \
R_"a[200:203], v29 offset:53248\n" \
"v_cndmask_b32_e32 v40, v217, v215, vcc\n" \
M_"v[112:115], a[252:255], a[204:207], v[112:115]\n" \
"s_addk_i32 s84, 0x7c00\n" \
M_"v[108:111], a[184:187], a[140:143], v[108:111]\n" \
B_"v27, s[68:71], s84 offen offset:3072 lds\n" \
M_"v[108:111], a[248:251], a[204:207], v[108:111]\n" \
"s_add_u32 m0, s30, 0x1000\n" \
"s_mov_b32 s84, 0x8200\n" \
M_"v[104:107], a[180:183], a[140:143], v[104:107]\n" \
B_"v22, s[64:67], s84 offen offset:0 lds\n" \
M_"v[104:107], a[244:247], a[204:207], v[104:107]\n" \
X_"a[166:167], v33 offset:33792\n" \
"v_cndmask_b32_e32 v42, v215, v217, vcc\n" \
M_"v[100:103], a[176:179], a[140:143], v[100:103]\n" \
X_"a[228:229], v33 offset:40960\n" \
"v_cmp_gt_u32_e64 vcc, v214, v213\n" \
M_"v[100:103], a[240:243], a[204:207], v[100:103]\n" \
R_"a[140:143], v28 offset:55296\n" \
"v_and_b32_e64 v214, 16, v20\n" \
M_"v[116:119], a[176:179], a[144:147], v[116:119]\n" \
R_"a[204:207], v29 offset:55296\n" \
"s_movk_i32 s42, 0x3f80\n" \
M_"v[116:119], a[240:243], a[208:211], v[116:119]\n" \
"s_addk_i32 s84, 0x1c00\n" \
M_"v[120:123], a[180:183], a[144:147], v[120:123]\n" \
B_"v23, s[64:67], s84 offen offset:1024 lds\n" \
M_"v[120:123], a[244:247], a[208:211], v[120:123]\n" \
"s_addk_i32 s84, 0x1c00\n" \
M_"v[124:127], a[184:187], a[144:147], v[124:127]\n" \
B_"v22, s[64:67], s84 offen offset:2048 lds\n" \
M_"v[124:127], a[248:251], a[208:211], v[124:127]\n" \
"s_addk_i32 s84, 0x1c00\n" \
M_"v[128:131], a[188:191], a[144:147], v[128:131]\n" \
B_"v23, s[64:67], s84 offen offset:3072 lds\n" \
M_"v[128:131], a[252:255], a[208:211], v[128:131]\n" \
R_"a[144:147], v28 offset:32768\n" \
"s_xor_b32 s5, s4, 15\n" \
M_"v[144:147], a[188:191], a[148:151], v[144:147]\n" \
R_"a[208:211], v29 offset:32768\n" \
M_"v[144:147], a[252:255], a[212:215], v[144:147]\n" \
"s_add_u32 m0, s31, 0x4000\n" \
"s_mov_b32 s84, 0x200000\n" \
M_"v[140:143], a[184:187], a[148:151], v[140:143]\n" \
B_"v26, s[72:75], s84 offen offset:0 lds\n" \
M_"v[140:143], a[248:251], a[212:215], v[140:143]\n" \
X_"a[230:231], v33 offset:41984\n" \
M_"v[136:139], a[180:183], a[148:151], v[136:139]\n" \
"s_addk_i32 s84, 0x7c00\n" \
M_"v[136:139], a[244:247], a[212:215], v[136:139]\n" \
B_"v26, s[72:75], s84 offen offset:1024 lds\n" \
M_"v[132:135], a[176:179], a[148:151], v[132:135]\n" \
X_"a[168:169], v34 offset:32768\n" \
M_"v[132:135], a[240:243], a[212:215], v[132:135]\n" \
R_"a[148:151], v28 offset:34816\n" \
M_"v[148:151], a[176:179], a[152:155], v[148:151]\n" \
R_"a[212:215], v29 offset:34816\n" \
M_"v[148:151], a[240:243], a[216:219], v[148:151]\n" \
X_"a[170:171], v34 offset:33792\n" \
M_"v[152:155], a[180:183], a[152:155], v[152:155]\n" \
X_"a[232:233], v34 offset:40960\n" \
M_"v[152:155], a[244:247], a[216:219], v[152:155]\n" \
X_"a[234:235], v34 offset:41984\n" \
M_"v[156:159], a[184:187], a[152:155], v[156:159]\n" \
X_"a[172:173], v35 offset:32768\n" \
M_"v[156:159], a[248:251], a[216:219], v[156:159]\n" \
X_"a[174:175], v35 offset:33792\n" \
M_"v[160:163], a[188:191], a[152:155], v[160:163]\n" \
X_"a[236:237], v35 offset:40960\n" \
M_"v[160:163], a[252:255], a[216:219], v[160:163]\n" \
R_"a[152:155], v28 offset:36864\n" \
M_"v[176:179], a[188:191], a[156:159], v[176:179]\n" \
R_"a[216:219], v29 offset:36864\n" \
M_"v[176:179], a[252:255], a[220:223], v[176:179]\n" \
X_"a[238:239], v35 offset:41984\n" \
M_"v[172:175], a[184:187], a[156:159], v[172:175]\n" \
"s_cmpk_lt_u32 s3, 2\n" \
"s_cselect_b32 s6, s5, s4\n" \
M_"v[172:175], a[248:251], a[220:223], v[172:175]\n" \
"s_lshl_b32 s7, s6, 7\n" \
"v_and_b32_e32 v213, 0x320, v20\n" \
M_"v[168:171], a[180:183], a[156:159], v[168:171]\n" \
"s_movk_i32 s41, 0x100\n" \
"v_lshlrev_b32_e64 v214, v214, s42\n" \
M_"v[168:171], a[244:247], a[220:223], v[168:171]\n" \
"v_mov_b32_e32 v215, 0\n" \
"s_add_i32 s40, s7, -16\n" \
M_"v[164:167], a[176:179], a[156:159], v[164:167]\n" \
"v_cndmask_b32_e32 v41, v217, v216, vcc\n" \
"v_cndmask_b32_e32 v43, v216, v217, vcc\n" \
M_"v[164:167], a[240:243], a[220:223], v[164:167]\n" \
R_"a[156:159], v28 offset:38912\n" \
R_"a[220:223], v29 offset:38912\n" \
"s_waitcnt lgkmcnt(15)\n" \
M_"a[0:3], a[160:163], a[128:131], a[0:3]\n" \
"s_addk_i32 s84, 0x7c00\n" \
M_"a[0:3], a[224:227], a[192:195], a[0:3]\n" \
B_"v27, s[72:75], s84 offen offset:2048 lds\n" \
M_"a[4:7], a[164:167], a[128:131], a[4:7]\n" \
X_"a[176:177], v36 offset:32768\n" \
"v_cmp_eq_u32_e32 vcc, s41, v213\n" \
"s_waitcnt lgkmcnt(15)\n" \
M_"a[4:7], a[228:231], a[192:195], a[4:7]\n" \
"s_addk_i32 s84, 0x7c00\n" \
"s_waitcnt lgkmcnt(11)\n" \
M_"a[8:11], a[168:171], a[128:131], a[8:11]\n" \
B_"v27, s[72:75], s84 offen offset:3072 lds\n" \
"s_waitcnt lgkmcnt(9)\n" \
M_"a[8:11], a[232:235], a[192:195], a[8:11]\n" \
X_"a[178:179], v36 offset:33792\n" \
"s_mov_b32 s41, 0\n" \
"s_waitcnt lgkmcnt(8)\n" \
M_"a[12:15], a[172:175], a[128:131], a[12:15]\n" \
X_"a[240:241], v36 offset:40960\n" \
"s_and_b32 s43, s3, 1\n" \
"s_waitcnt lgkmcnt(5)\n" \
M_"a[12:15], a[236:239], a[192:195], a[12:15]\n" \
"v_cndmask_b32_e32 v46, v215, v214, vcc\n" \
"v_and_b32_e32 v213, 0x330, v20\n" \
M_"a[28:31], a[172:175], a[132:135], a[28:31]\n" \
"s_cmp_lg_u32 s7, 0\n" \
"s_cselect_b32 s40, s40, s41\n" \
M_"a[28:31], a[236:239], a[196:199], a[28:31]\n" \
X_"a[242:243], v36 offset:41984\n" \
"s_mov_b32 s42, 0xff800000\n" \
M_"a[24:27], a[168:171], a[132:135], a[24:27]\n" \
"s_cmp_lg_u32 s7, 0\n" \
"s_cselect_b32 s47, s41, s42\n" \
M_"a[24:27], a[232:235], a[196:199], a[24:27]\n" \
"s_lshl_b32 s41, s43, 6\n" \
"v_lshrrev_b32_e32 v215, 6, v20\n" \
M_"a[20:23], a[164:167], a[132:135], a[20:23]\n" \
"v_mov_b32_e32 v214, 0x3f80\n" \
"v_cmp_eq_u32_e64 vcc, 32, v213\n" \
M_"a[20:23], a[228:231], a[196:199], a[20:23]\n" \
X_"a[180:181], v37 offset:32768\n" \
"s_or_b32 s7, s41, s7\n" \
M_"a[16:19], a[160:163], a[132:135], a[16:19]\n" \
"v_lshrrev_b32_e32 v213, 7, v20\n" \
"v_bfe_u32 v216, v20, 6, 2\n" \
M_"a[16:19], a[224:227], a[196:199], a[16:19]\n" \
"v_lshrrev_b32_e64 v219, 1, v20\n" \
"v_bfe_u32 v215, v215, 1, 1\n" \
M_"a[32:35], a[160:163], a[136:139], a[32:35]\n" \
X_"a[182:183], v37 offset:33792\n" \
"v_cndmask_b32_e64 v47, 0, v214, vcc\n" \
M_"a[32:35], a[224:227], a[200:203], a[32:35]\n" \
"v_or_b32_e64 v214, s7, v213\n" \
"v_lshl_or_b32 v217, s43, 3, v213\n" \
M_"a[36:39], a[164:167], a[136:139], a[36:39]\n" \
"v_lshlrev_b32_e32 v218, 10, v212\n" \
"v_lshlrev_b32_e32 v216, 7, v216\n" \
M_"a[36:39], a[228:231], a[200:203], a[36:39]\n" \
X_"a[244:245], v37 offset:40960\n" \
"v_and_b32_e64 v219, 24, v219\n" \
M_"a[40:43], a[168:171], a[136:139], a[40:43]\n" \
"s_mov_b32 m0, s32\n" \
"v_and_or_b32 v213, v213, 2, v215\n" \
M_"a[40:43], a[232:235], a[200:203], a[40:43]\n" \
"s_mov_b32 exec_lo, 0xffffffff\n" \
"s_mov_b32 exec_hi, 0x0\n" \
"global_load_lds_dword v[188:189], off\n" \
"s_mov_b64 exec, -1\n" \
M_"a[44:47], a[172:175], a[136:139], a[44:47]\n" \
X_"a[246:247], v37 offset:41984\n" \
"v_add_u32_e64 v217, s40, v217\n" \
M_"a[44:47], a[236:239], a[200:203], a[44:47]\n" \
"v_or3_b32 v216, v216, v218, v219\n" \
"v_lshlrev_b32_e64 v213, 5, v213\n" \
M_"a[60:63], a[172:175], a[140:143], a[60:63]\n" \
"s_mov_b32 s40, 0x14000\n" \
"s_lshl_b32 s7, s2, 2\n" \
M_"a[60:63], a[236:239], a[204:207], a[60:63]\n" \
"v_or3_b32 v48, v216, v213, s40\n" \
"s_lshl_b32 s40, s43, 2\n" \
M_"a[56:59], a[168:171], a[140:143], a[56:59]\n" \
X_"a[184:185], v38 offset:32768\n" \
"s_lshl_b32 s6, s6, 3\n" \
M_"a[56:59], a[232:235], a[204:207], a[56:59]\n" \
"v_or_b32_e32 v213, s40, v212\n" \
"s_or_b32 s6, s6, s40\n" \
M_"a[52:55], a[164:167], a[140:143], a[52:55]\n" \
"s_and_b32 s40, s7, 0x7c\n" \
"s_or_b32 s40, s40, s3\n" \
M_"a[52:55], a[228:231], a[204:207], a[52:55]\n" \
X_"a[186:187], v38 offset:33792\n" \
M_"a[48:51], a[160:163], a[140:143], a[48:51]\n" \
"s_cmpk_lt_u32 s40, 64\n" \
"s_cselect_b32 s41, s4, s5\n" \
M_"a[48:51], a[224:227], a[204:207], a[48:51]\n" \
"s_lshl_b32 s40, s40, 1\n" \
"s_lshl_b32 s41, s41, 7\n" \
M_"a[64:67], a[160:163], a[144:147], a[64:67]\n" \
X_"a[248:249], v38 offset:40960\n" \
"s_and_b32 s40, s40, 0x7e\n" \
M_"a[64:67], a[224:227], a[208:211], a[64:67]\n" \
"s_or_b32 s40, s41, s40\n" \
"s_mulk_i32 s40, 0x480\n" \
M_"a[68:71], a[164:167], a[144:147], a[68:71]\n" \
"v_add_u32_e64 v49, s40, v20\n" \
"s_mov_b32 s40, 0xc000\n" \
M_"a[68:71], a[228:231], a[208:211], a[68:71]\n" \
X_"a[250:251], v38 offset:41984\n" \
"s_and_b32 s7, s7, 28\n" \
M_"a[72:75], a[168:171], a[144:147], a[72:75]\n" \
"s_bfe_u32 s45, s2, 0x20003\n" \
"s_mov_b32 s41, 0x8000\n" \
M_"a[72:75], a[232:235], a[208:211], a[72:75]\n" \
"s_cmp_lg_u32 s4, 0\n" \
"s_cselect_b32 s51, s40, s41\n" \
M_"a[76:79], a[172:175], a[144:147], a[76:79]\n" \
"s_movk_i32 s40, 0x4000\n" \
"s_lshr_b32 s44, s3, 1\n" \
M_"a[76:79], a[236:239], a[208:211], a[76:79]\n" \
X_"a[188:189], v39 offset:32768\n" \
"s_or_b32 s46, s7, s45\n" \
M_"a[92:95], a[172:175], a[148:151], a[92:95]\n" \
"s_cmp_lg_u32 s4, 0\n" \
"s_cselect_b32 s53, s41, s40\n" \
M_"a[92:95], a[236:239], a[212:215], a[92:95]\n" \
"s_lshl_b32 s40, s44, 15\n" \
"s_lshl_b32 s41, s43, 14\n" \
M_"a[88:91], a[168:171], a[148:151], a[88:91]\n" \
X_"a[190:191], v39 offset:33792\n" \
"s_lshl_b32 s48, s46, 20\n" \
M_"a[88:91], a[232:235], a[212:215], a[88:91]\n" \
"s_or_b32 s41, s40, s41\n" \
"s_add_i32 s49, s48, 0x1000000\n" \
M_"a[84:87], a[164:167], a[148:151], a[84:87]\n" \
"s_waitcnt vmcnt(17)\n" \
"v_or_b32_e32 v204, v210, v204\n" \
"v_or_b32_e64 v206, v210, v206\n" \
M_"a[84:87], a[228:231], a[212:215], a[84:87]\n" \
"v_mov_b32_e64 v210, 0\n" \
"s_add_u32 s88, s35, 0x1000\n" \
M_"a[80:83], a[160:163], a[148:151], a[80:83]\n" \
X_"a[252:253], v39 offset:40960\n" \
"s_or_b32 s42, s49, s41\n" \
M_"a[80:83], a[224:227], a[212:215], a[80:83]\n" \
"ds_write_b128 v196, v[200:203]\n" \
M_"a[96:99], a[160:163], a[152:155], a[96:99]\n" \
X_"a[254:255], v39 offset:41984\n" \
"s_lshl_b32 s4, s4, 16\n" \
M_"a[96:99], a[224:227], a[216:219], a[96:99]\n" \
"s_or_b32 s41, s42, s4\n" \
"s_lshl_b32 s4, s5, 16\n" \
M_"a[100:103], a[164:167], a[152:155], a[100:103]\n" \
"ds_write_b128 v197, v[204:207]\n" \
M_"a[100:103], a[228:231], a[216:219], a[100:103]\n" \
"s_mul_i32 s5, s6, 0x24800\n" \
"s_lshl_b32 s6, s46, 9\n" \
M_"a[104:107], a[168:171], a[152:155], a[104:107]\n" \
"s_lshl_b32 s7, s7, 6\n" \
"s_add_i32 s6, s5, s6\n" \
M_"a[104:107], a[232:235], a[216:219], a[104:107]\n" \
"ds_write_b128 v198, v[208:211]\n" \
M_"a[108:111], a[172:175], a[152:155], a[108:111]\n" \
"s_or_b32 s5, s5, s7\n" \
"s_lshl_b32 s7, s45, 2\n" \
M_"a[108:111], a[236:239], a[216:219], a[108:111]\n" \
"s_or_b32 s5, s5, s7\n" \
"s_lshl_b32 s7, s3, 12\n" \
"s_waitcnt lgkmcnt(15)\n" \
M_"a[124:127], a[172:175], a[156:159], a[124:127]\n" \
"s_or_b32 s50, s49, s7\n" \
"s_lshl_b32 s7, s3, 11\n" \
M_"a[124:127], a[236:239], a[220:223], a[124:127]\n" \
"v_lshrrev_b32_e64 v215, 4, v20\n" \
"v_bfe_u32 v216, v20, 4, 3\n" \
M_"a[120:123], a[168:171], a[156:159], a[120:123]\n" \
"s_or_b32 s42, s42, s4\n" \
"s_lshl_b32 s4, s43, 11\n" \
M_"a[120:123], a[232:235], a[220:223], a[120:123]\n" \
"s_add_i32 s52, s48, s7\n" \
"s_add_i32 s53, s53, s7\n" \
M_"a[116:119], a[164:167], a[156:159], a[116:119]\n" \
"v_mul_u32_u24_e32 v214, 0x480, v214\n" \
"v_bitop3_b32 v218, v212, v215, 7 bitop3:0x78\n" \
M_"a[116:119], a[228:231], a[220:223], a[116:119]\n" \
"v_bitop3_b32 v212, v212, v216, 4 bitop3:0x36\n" \
"s_lshl_b32 s7, s3, 10\n" \
M_"a[112:115], a[160:163], a[156:159], a[112:115]\n" \
"s_or_b32 s4, s40, s4\n" \
"v_lshl_or_b32 v25, v212, 4, v214\n" \
"s_waitcnt vmcnt(1) lgkmcnt(0)\n" \
M_"a[112:115], a[224:227], a[220:223], a[112:115]\n" \
"s_barrier\n" \
M_"v[52:55], a[176:179], a[128:131], v[52:55]\n" \
"s_add_u32 m0, s30, 0x8000\n" \
"s_movk_i32 s84, 0x280\n" \
M_"v[52:55], a[240:243], a[192:195], v[52:55]\n" \
B_"v22, s[64:67], s84 offen offset:0 lds\n" \
M_"v[56:59], a[180:183], a[128:131], v[56:59]\n" \
"s_movk_i32 s84, 0x1e80\n" \
M_"v[56:59], a[244:247], a[192:195], v[56:59]\n" \
B_"v23, s[64:67], s84 offen offset:1024 lds\n" \
M_"v[60:63], a[184:187], a[128:131], v[60:63]\n" \
X_"a[160:161], v32 offset:0\n" \
"v_mul_u32_u24_e32 v212, 0x480, v217\n" \
M_"v[60:63], a[248:251], a[192:195], v[60:63]\n" \
"s_movk_i32 s84, 0x3a80\n" \
M_"v[64:67], a[188:191], a[128:131], v[64:67]\n" \
B_"v22, s[64:67], s84 offen offset:2048 lds\n" \
M_"v[64:67], a[252:255], a[192:195], v[64:67]\n" \
R_"a[128:131], v28 offset:16384\n" \
"v_bitop3_b32 v213, v215, v213, 7 bitop3:0x6c\n" \
M_"v[80:83], a[188:191], a[132:135], v[80:83]\n" \
R_"a[192:195], v29 offset:16384\n" \
"s_or_b32 s40, s7, 0x14000\n" \
M_"v[80:83], a[252:255], a[196:199], v[80:83]\n" \
"s_movk_i32 s84, 0x5680\n" \
M_"v[76:79], a[184:187], a[132:135], v[76:79]\n" \
B_"v23, s[64:67], s84 offen offset:3072 lds\n" \
M_"v[76:79], a[248:251], a[196:199], v[76:79]\n" \
"s_add_u32 m0, s31, 0x8000\n" \
"s_mov_b32 s84, 0x280000\n" \
M_"v[72:75], a[180:183], a[132:135], v[72:75]\n" \
B_"v26, s[68:71], s84 offen offset:0 lds\n" \
M_"v[72:75], a[244:247], a[196:199], v[72:75]\n" \
X_"a[162:163], v32 offset:1024\n" \
"s_lshl_b32 s43, s44, 13\n" \
M_"v[68:71], a[176:179], a[132:135], v[68:71]\n" \
X_"a[224:225], v32 offset:8192\n" \
"s_mulk_i32 s44, 0xe800\n" \
M_"v[68:71], a[240:243], a[196:199], v[68:71]\n" \
R_"a[132:135], v28 offset:18432\n" \
"s_lshl_b32 s7, s3, 8\n" \
M_"v[84:87], a[176:179], a[136:139], v[84:87]\n" \
R_"a[196:199], v29 offset:18432\n" \
"v_lshl_or_b32 v24, v218, 4, v214\n" \
M_"v[84:87], a[240:243], a[200:203], v[84:87]\n" \
"s_addk_i32 s84, 0x7c00\n" \
M_"v[88:91], a[180:183], a[136:139], v[88:91]\n" \
B_"v26, s[68:71], s84 offen offset:1024 lds\n" \
M_"v[88:91], a[244:247], a[200:203], v[88:91]\n" \
"s_addk_i32 s84, 0x7c00\n" \
M_"v[92:95], a[184:187], a[136:139], v[92:95]\n" \
B_"v27, s[68:71], s84 offen offset:2048 lds\n" \
M_"v[92:95], a[248:251], a[200:203], v[92:95]\n" \
"s_addk_i32 s84, 0x7c00\n" \
M_"v[96:99], a[188:191], a[136:139], v[96:99]\n" \
B_"v27, s[68:71], s84 offen offset:3072 lds\n" \
M_"v[96:99], a[252:255], a[200:203], v[96:99]\n" \
R_"a[136:139], v28 offset:20480\n" \
"v_lshl_or_b32 v21, v213, 4, v212\n" \
M_"v[112:115], a[188:191], a[140:143], v[112:115]\n" \
R_"a[200:203], v29 offset:20480\n" \
"s_add_i32 s44, s44, 0x17000\n" \
M_"v[112:115], a[252:255], a[204:207], v[112:115]\n" \
"s_add_u32 m0, s30, 0x9000\n" \
"s_mov_b32 s84, 0x8280\n" \
M_"v[108:111], a[184:187], a[140:143], v[108:111]\n" \
B_"v22, s[64:67], s84 offen offset:0 lds\n" \
M_"v[108:111], a[248:251], a[204:207], v[108:111]\n" \
"s_addk_i32 s84, 0x1c00\n" \
M_"v[104:107], a[180:183], a[140:143], v[104:107]\n" \
B_"v23, s[64:67], s84 offen offset:1024 lds\n" \
M_"v[104:107], a[244:247], a[204:207], v[104:107]\n" \
"s_addk_i32 s84, 0x1c00\n" \
M_"v[100:103], a[176:179], a[140:143], v[100:103]\n" \
B_"v22, s[64:67], s84 offen offset:2048 lds\n" \
M_"v[100:103], a[240:243], a[204:207], v[100:103]\n" \
R_"a[140:143], v28 offset:22528\n" \
"s_or_b32 s45, s7, 0x27000\n" \
M_"v[116:119], a[176:179], a[144:147], v[116:119]\n" \
R_"a[204:207], v29 offset:22528\n" \
"s_or_b32 s46, s4, 0x10000\n" \
M_"v[116:119], a[240:243], a[208:211], v[116:119]\n" \
"s_addk_i32 s84, 0x1c00\n" \
M_"v[120:123], a[180:183], a[144:147], v[120:123]\n" \
B_"v23, s[64:67], s84 offen offset:3072 lds\n" \
M_"v[120:123], a[244:247], a[208:211], v[120:123]\n" \
"s_add_u32 m0, s31, 0xc000\n" \
"s_mov_b32 s84, 0x280000\n" \
M_"v[124:127], a[184:187], a[144:147], v[124:127]\n" \
B_"v26, s[72:75], s84 offen offset:0 lds\n" \
M_"v[124:127], a[248:251], a[208:211], v[124:127]\n" \
"s_addk_i32 s84, 0x7c00\n" \
M_"v[128:131], a[188:191], a[144:147], v[128:131]\n" \
B_"v26, s[72:75], s84 offen offset:1024 lds\n" \
M_"v[128:131], a[252:255], a[208:211], v[128:131]\n" \
R_"a[144:147], v28 offset:0\n" \
"s_add_i32 s48, s6, 0x3000000\n" \
M_"v[144:147], a[188:191], a[148:151], v[144:147]\n" \
R_"a[208:211], v29 offset:0\n" \
"s_add_i32 s49, s5, 0x3004000\n" \
M_"v[144:147], a[252:255], a[212:215], v[144:147]\n" \
R_"v[200:203], v199 offset:0\n" \
"s_add_i32 s52, s52, 0x1010000\n" \
M_"v[140:143], a[184:187], a[148:151], v[140:143]\n" \
R_"v[204:207], v199 offset:4096\n" \
M_"v[140:143], a[248:251], a[212:215], v[140:143]\n" \
X_"a[226:227], v32 offset:9216\n" \
M_"v[136:139], a[180:183], a[148:151], v[136:139]\n" \
X_"a[164:165], v33 offset:0\n" \
M_"v[136:139], a[244:247], a[212:215], v[136:139]\n" \
R_"v[208:211], v199 offset:8192\n" \
M_"v[132:135], a[176:179], a[148:151], v[132:135]\n" \
X_"a[166:167], v33 offset:1024\n" \
M_"v[132:135], a[240:243], a[212:215], v[132:135]\n" \
R_"a[148:151], v28 offset:2048\n" \
M_"v[148:151], a[176:179], a[152:155], v[148:151]\n" \
R_"a[212:215], v29 offset:2048\n" \
M_"v[148:151], a[240:243], a[216:219], v[148:151]\n" \
X_"a[228:229], v33 offset:8192\n" \
M_"v[152:155], a[180:183], a[152:155], v[152:155]\n" \
X_"a[230:231], v33 offset:9216\n" \
M_"v[152:155], a[244:247], a[216:219], v[152:155]\n" \
X_"a[168:169], v34 offset:0\n" \
M_"v[156:159], a[184:187], a[152:155], v[156:159]\n" \
X_"a[170:171], v34 offset:1024\n" \
M_"v[156:159], a[248:251], a[216:219], v[156:159]\n" \
X_"a[232:233], v34 offset:8192\n" \
M_"v[160:163], a[188:191], a[152:155], v[160:163]\n" \
X_"a[234:235], v34 offset:9216\n" \
M_"v[160:163], a[252:255], a[216:219], v[160:163]\n" \
R_"a[152:155], v28 offset:4096\n" \
M_"v[176:179], a[188:191], a[156:159], v[176:179]\n" \
R_"a[216:219], v29 offset:4096\n" \
M_"v[176:179], a[252:255], a[220:223], v[176:179]\n" \
X_"a[172:173], v35 offset:0\n" \
M_"v[172:175], a[184:187], a[156:159], v[172:175]\n" \
X_"a[174:175], v35 offset:1024\n" \
M_"v[172:175], a[248:251], a[220:223], v[172:175]\n" \
X_"a[236:237], v35 offset:8192\n" \
M_"v[168:171], a[180:183], a[156:159], v[168:171]\n" \
X_"a[238:239], v35 offset:9216\n" \
M_"v[168:171], a[244:247], a[220:223], v[168:171]\n" \
M_"v[164:167], a[176:179], a[156:159], v[164:167]\n" \
M_"v[164:167], a[240:243], a[220:223], v[164:167]\n" \
R_"a[156:159], v28 offset:6144\n" \
R_"a[220:223], v29 offset:6144\n" \
"s_waitcnt lgkmcnt(15)\n" \
M_"a[0:3], a[160:163], a[128:131], a[0:3]\n" \
"s_addk_i32 s84, 0x7c00\n" \
M_"a[0:3], a[224:227], a[192:195], a[0:3]\n" \
B_"v27, s[72:75], s84 offen offset:2048 lds\n" \
M_"a[4:7], a[164:167], a[128:131], a[4:7]\n" \
X_"a[176:177], v36 offset:0\n" \
"s_waitcnt lgkmcnt(13)\n" \
M_"a[4:7], a[228:231], a[192:195], a[4:7]\n" \
"s_addk_i32 s84, 0x7c00\n" \
"s_waitcnt lgkmcnt(11)\n" \
M_"a[8:11], a[168:171], a[128:131], a[8:11]\n" \
B_"v27, s[72:75], s84 offen offset:3072 lds\n" \
"s_waitcnt lgkmcnt(9)\n" \
M_"a[8:11], a[232:235], a[192:195], a[8:11]\n" \
X_"a[178:179], v36 offset:1024\n" \
"s_waitcnt lgkmcnt(6)\n" \
M_"a[12:15], a[172:175], a[128:131], a[12:15]\n" \
X_"a[240:241], v36 offset:8192\n" \
"s_waitcnt lgkmcnt(5)\n" \
M_"a[12:15], a[236:239], a[192:195], a[12:15]\n" \
M_"a[28:31], a[172:175], a[132:135], a[28:31]\n" \
M_"a[28:31], a[236:239], a[196:199], a[28:31]\n" \
X_"a[242:243], v36 offset:9216\n" \
M_"a[24:27], a[168:171], a[132:135], a[24:27]\n" \
M_"a[24:27], a[232:235], a[196:199], a[24:27]\n" \
M_"a[20:23], a[164:167], a[132:135], a[20:23]\n" \
M_"a[20:23], a[228:231], a[196:199], a[20:23]\n" \
X_"a[180:181], v37 offset:0\n" \
M_"a[16:19], a[160:163], a[132:135], a[16:19]\n" \
".fill (L_pb + 12000 - .) / 4, 4, 0xBF800000\n" \
"L_pp3:\n" \
"s_cbranch_execz L_pret\n" \
M_"a[16:19], a[224:227], a[196:199], a[16:19]\n" \
M_"a[32:35], a[160:163], a[136:139], a[32:35]\n" \
X_"a[182:183], v37 offset:1024\n" \
M_"a[32:35], a[224:227], a[200:203], a[32:35]\n" \
M_"a[36:39], a[164:167], a[136:139], a[36:39]\n" \
M_"a[36:39], a[228:231], a[200:203], a[36:39]\n" \
"buffer_store_dwordx4 v[200:203], v20, s[80:83], s35 offen sc1\n" \
M_"a[40:43], a[168:171], a[136:139], a[40:43]\n" \
"s_mov_b32 m0, s32\n" \
X_"a[244:245], v37 offset:8192\n" \
M_"a[40:43], a[232:235], a[200:203], a[40:43]\n" \
"s_mov_b32 exec_lo, 0x0\n" \
"s_mov_b32 exec_hi, 0xffffffff\n" \
"global_load_lds_dword v[188:189], off\n" \
"s_mov_b64 exec, -1\n" \
"s_nop 0\n" \
M_"a[44:47], a[172:175], a[136:139], a[44:47]\n" \
X_"a[246:247], v37 offset:9216\n" \
M_"a[44:47], a[236:239], a[200:203], a[44:47]\n" \
M_"a[60:63], a[172:175], a[140:143], a[60:63]\n" \
M_"a[60:63], a[236:239], a[204:207], a[60:63]\n" \
M_"a[56:59], a[168:171], a[140:143], a[56:59]\n" \
X_"a[184:185], v38 offset:0\n" \
M_"a[56:59], a[232:235], a[204:207], a[56:59]\n" \
M_"a[52:55], a[164:167], a[140:143], a[52:55]\n" \
"buffer_store_dwordx4 v[204:207], v20, s[80:83], s88 offen sc1\n" \
M_"a[52:55], a[228:231], a[204:207], a[52:55]\n" \
X_"a[186:187], v38 offset:1024\n" \
M_"a[48:51], a[160:163], a[140:143], a[48:51]\n" \
M_"a[48:51], a[224:227], a[204:207], a[48:51]\n" \
M_"a[64:67], a[160:163], a[144:147], a[64:67]\n" \
X_"a[248:249], v38 offset:8192\n" \
M_"a[64:67], a[224:227], a[208:211], a[64:67]\n" \
M_"a[68:71], a[164:167], a[144:147], a[68:71]\n" \
M_"a[68:71], a[228:231], a[208:211], a[68:71]\n" \
X_"a[250:251], v38 offset:9216\n" \
M_"a[72:75], a[168:171], a[144:147], a[72:75]\n" \
M_"a[72:75], a[232:235], a[208:211], a[72:75]\n" \
M_"a[76:79], a[172:175], a[144:147], a[76:79]\n" \
"buffer_store_dwordx4 v[208:211], v20, s[80:83], s36 offen sc1\n" \
M_"a[76:79], a[236:239], a[208:211], a[76:79]\n" \
X_"a[188:189], v39 offset:0\n" \
M_"a[92:95], a[172:175], a[148:151], a[92:95]\n" \
M_"a[92:95], a[236:239], a[212:215], a[92:95]\n" \
M_"a[88:91], a[168:171], a[148:151], a[88:91]\n" \
X_"a[190:191], v39 offset:1024\n" \
M_"a[88:91], a[232:235], a[212:215], a[88:91]\n" \
M_"a[84:87], a[164:167], a[148:151], a[84:87]\n" \
M_"a[84:87], a[228:231], a[212:215], a[84:87]\n" \
X_"a[252:253], v39 offset:8192\n" \
M_"a[80:83], a[160:163], a[148:151], a[80:83]\n" \
M_"a[80:83], a[224:227], a[212:215], a[80:83]\n" \
M_"a[96:99], a[160:163], a[152:155], a[96:99]\n" \
X_"a[254:255], v39 offset:9216\n" \
M_"a[96:99], a[224:227], a[216:219], a[96:99]\n" \
M_"a[100:103], a[164:167], a[152:155], a[100:103]\n" \
M_"a[100:103], a[228:231], a[216:219], a[100:103]\n" \
M_"a[104:107], a[168:171], a[152:155], a[104:107]\n" \
M_"a[104:107], a[232:235], a[216:219], a[104:107]\n" \
M_"a[108:111], a[172:175], a[152:155], a[108:111]\n" \
M_"a[108:111], a[236:239], a[216:219], a[108:111]\n" \
"s_waitcnt lgkmcnt(15)\n" \
M_"a[124:127], a[172:175], a[156:159], a[124:127]\n" \
M_"a[124:127], a[236:239], a[220:223], a[124:127]\n" \
M_"a[120:123], a[168:171], a[156:159], a[120:123]\n" \
M_"a[120:123], a[232:235], a[220:223], a[120:123]\n" \
M_"a[116:119], a[164:167], a[156:159], a[116:119]\n" \
M_"a[116:119], a[228:231], a[220:223], a[116:119]\n" \
M_"a[112:115], a[160:163], a[156:159], a[112:115]\n" \
"s_waitcnt vmcnt(4) lgkmcnt(0)\n" \
M_"a[112:115], a[224:227], a[220:223], a[112:115]\n" \
"s_barrier\n" \
M_"v[52:55], a[176:179], a[128:131], v[52:55]\n" \
"s_add_u32 m0, s30, 0x0\n" \
"s_movk_i32 s84, 0x300\n" \
M_"v[52:55], a[240:243], a[192:195], v[52:55]\n" \
B_"v22, s[64:67], s84 offen offset:0 lds\n" \
M_"v[56:59], a[180:183], a[128:131], v[56:59]\n" \
X_"a[160:161], v32 offset:32768\n" \
M_"v[56:59], a[244:247], a[192:195], v[56:59]\n" \
"s_movk_i32 s84, 0x1f00\n" \
M_"v[60:63], a[184:187], a[128:131], v[60:63]\n" \
B_"v23, s[64:67], s84 offen offset:1024 lds\n" \
M_"v[60:63], a[248:251], a[192:195], v[60:63]\n" \
X_"a[162:163], v32 offset:33792\n" \
M_"v[64:67], a[188:191], a[128:131], v[64:67]\n" \
X_"a[224:225], v32 offset:40960\n" \
M_"v[64:67], a[252:255], a[192:195], v[64:67]\n" \
R_"a[128:131], v28 offset:49152\n" \
M_"v[80:83], a[188:191], a[132:135], v[80:83]\n" \
R_"a[192:195], v29 offset:49152\n" \
M_"v[80:83], a[252:255], a[196:199], v[80:83]\n" \
"s_movk_i32 s84, 0x3b00\n" \
M_"v[76:79], a[184:187], a[132:135], v[76:79]\n" \
B_"v22, s[64:67], s84 offen offset:2048 lds\n" \
M_"v[76:79], a[248:251], a[196:199], v[76:79]\n" \
"s_movk_i32 s84, 0x5700\n" \
M_"v[72:75], a[180:183], a[132:135], v[72:75]\n" \
B_"v23, s[64:67], s84 offen offset:3072 lds\n" \
M_"v[72:75], a[244:247], a[196:199], v[72:75]\n" \
"s_add_u32 m0, s31, 0x0\n" \
"s_mov_b32 s84, 0x300000\n" \
M_"v[68:71], a[176:179], a[132:135], v[68:71]\n" \
B_"v26, s[68:71], s84 offen offset:0 lds\n" \
M_"v[68:71], a[240:243], a[196:199], v[68:71]\n" \
R_"a[132:135], v28 offset:51200\n" \
M_"v[84:87], a[176:179], a[136:139], v[84:87]\n" \
R_"a[196:199], v29 offset:51200\n" \
M_"v[84:87], a[240:243], a[200:203], v[84:87]\n" \
"s_addk_i32 s84, 0x7c00\n" \
M_"v[88:91], a[180:183], a[136:139], v[88:91]\n" \
B_"v26, s[68:71], s84 offen offset:1024 lds\n" \
M_"v[88:91], a[244:247], a[200:203], v[88:91]\n" \
X_"a[226:227], v32 offset:41984\n" \
M_"v[92:95], a[184:187], a[136:139], v[92:95]\n" \
"s_addk_i32 s84, 0x7c00\n" \
M_"v[92:95], a[248:251], a[200:203], v[92:95]\n" \
B_"v27, s[68:71], s84 offen offset:2048 lds\n" \
M_"v[96:99], a[188:191], a[136:139], v[96:99]\n" \
X_"a[164:165], v33 offset:32768\n" \
M_"v[96:99], a[252:255], a[200:203], v[96:99]\n" \
R_"a[136:139], v28 offset:53248\n" \
M_"v[112:115], a[188:191], a[140:143], v[112:115]\n" \
R_"a[200:203], v29 offset:53248\n" \
M_"v[112:115], a[252:255], a[204:207], v[112:115]\n" \
"s_addk_i32 s84, 0x7c00\n" \
M_"v[108:111], a[184:187], a[140:143], v[108:111]\n" \
B_"v27, s[68:71], s84 offen offset:3072 lds\n" \
M_"v[108:111], a[248:251], a[204:207], v[108:111]\n" \
"s_add_u32 m0, s30, 0x1000\n" \
"s_mov_b32 s84, 0x8300\n" \
M_"v[104:107], a[180:183], a[140:143], v[104:107]\n" \
B_"v22, s[64:67], s84 offen offset:0 lds\n" \
M_"v[104:107], a[244:247], a[204:207], v[104:107]\n" \
X_"a[166:167], v33 offset:33792\n" \
M_"v[100:103], a[176:179], a[140:143], v[100:103]\n" \
X_"a[228:229], v33 offset:40960\n" \
M_"v[100:103], a[240:243], a[204:207], v[100:103]\n" \
R_"a[140:143], v28 offset:55296\n" \
M_"v[116:119], a[176:179], a[144:147], v[116:119]\n" \
R_"a[204:207], v29 offset:55296\n" \
M_"v[116:119], a[240:243], a[208:211], v[116:119]\n" \
"s_addk_i32 s84, 0x1c00\n" \
M_"v[120:123], a[180:183], a[144:147], v[120:123]\n" \
B_"v23, s[64:67], s84 offen offset:1024 lds\n" \
M_"v[120:123], a[244:247], a[208:211], v[120:123]\n" \
"s_addk_i32 s84, 0x1c00\n" \
M_"v[124:127], a[184:187], a[144:147], v[124:127]\n" \
B_"v22, s[64:67], s84 offen offset:2048 lds\n" \
M_"v[124:127], a[248:251], a[208:211], v[124:127]\n" \
"s_addk_i32 s84, 0x1c00\n" \
"s_nop 0\n" \
M_"v[128:131], a[188:191], a[144:147], v[128:131]\n" \
B_"v23, s[64:67], s84 offen offset:3072 lds\n" \
M_"v[128:131], a[252:255], a[208:211], v[128:131]\n" \
R_"a[144:147], v28 offset:32768\n" \
M_"v[144:147], a[188:191], a[148:151], v[144:147]\n" \
R_"a[208:211], v29 offset:32768\n" \
M_"v[144:147], a[252:255], a[212:215], v[144:147]\n" \
"s_add_u32 m0, s31, 0x4000\n" \
"s_mov_b32 s84, 0x300000\n" \
M_"v[140:143], a[184:187], a[148:151], v[140:143]\n" \
B_"v26, s[72:75], s84 offen offset:0 lds\n" \
M_"v[140:143], a[248:251], a[212:215], v[140:143]\n" \
X_"a[230:231], v33 offset:41984\n" \
M_"v[136:139], a[180:183], a[148:151], v[136:139]\n" \
"s_addk_i32 s84, 0x7c00\n" \
"s_nop 0\n" \
M_"v[136:139], a[244:247], a[212:215], v[136:139]\n" \
B_"v26, s[72:75], s84 offen offset:1024 lds\n" \
M_"v[132:135], a[176:179], a[148:151], v[132:135]\n" \
X_"a[168:169], v34 offset:32768\n" \
M_"v[132:135], a[240:243], a[212:215], v[132:135]\n" \
R_"a[148:151], v28 offset:34816\n" \
M_"v[148:151], a[176:179], a[152:155], v[148:151]\n" \
R_"a[212:215], v29 offset:34816\n" \
M_"v[148:151], a[240:243], a[216:219], v[148:151]\n" \
X_"a[170:171], v34 offset:33792\n" \
M_"v[152:155], a[180:183], a[152:155], v[152:155]\n" \
X_"a[232:233], v34 offset:40960\n" \
M_"v[152:155], a[244:247], a[216:219], v[152:155]\n" \
X_"a[234:235], v34 offset:41984\n" \
M_"v[156:159], a[184:187], a[152:155], v[156:159]\n" \
X_"a[172:173], v35 offset:32768\n" \
M_"v[156:159], a[248:251], a[216:219], v[156:159]\n" \
X_"a[174:175], v35 offset:33792\n" \
M_"v[160:163], a[188:191], a[152:155], v[160:163]\n" \
X_"a[236:237], v35 offset:40960\n" \
M_"v[160:163], a[252:255], a[216:219], v[160:163]\n" \
R_"a[152:155], v28 offset:36864\n" \
M_"v[176:179], a[188:191], a[156:159], v[176:179]\n" \
R_"a[216:219], v29 offset:36864\n" \
M_"v[176:179], a[252:255], a[220:223], v[176:179]\n" \
X_"a[238:239], v35 offset:41984\n" \
M_"v[172:175], a[184:187], a[156:159], v[172:175]\n" \
M_"v[172:175], a[248:251], a[220:223], v[172:175]\n" \
M_"v[168:171], a[180:183], a[156:159], v[168:171]\n" \
M_"v[168:171], a[244:247], a[220:223], v[168:171]\n" \
M_"v[164:167], a[176:179], a[156:159], v[164:167]\n" \
M_"v[164:167], a[240:243], a[220:223], v[164:167]\n" \
R_"a[156:159], v28 offset:38912\n" \
R_"a[220:223], v29 offset:38912\n" \
"s_waitcnt lgkmcnt(15)\n" \
M_"a[0:3], a[160:163], a[128:131], a[0:3]\n" \
"s_addk_i32 s84, 0x7c00\n" \
M_"a[0:3], a[224:227], a[192:195], a[0:3]\n" \
B_"v27, s[72:75], s84 offen offset:2048 lds\n" \
M_"a[4:7], a[164:167], a[128:131], a[4:7]\n" \
X_"a[176:177], v36 offset:32768\n" \
"s_waitcnt lgkmcnt(15)\n" \
M_"a[4:7], a[228:231], a[192:195], a[4:7]\n" \
"s_addk_i32 s84, 0x7c00\n" \
"s_waitcnt lgkmcnt(11)\n" \
M_"a[8:11], a[168:171], a[128:131], a[8:11]\n" \
B_"v27, s[72:75], s84 offen offset:3072 lds\n" \
"s_waitcnt lgkmcnt(9)\n" \
M_"a[8:11], a[232:235], a[192:195], a[8:11]\n" \
X_"a[178:179], v36 offset:33792\n" \
"s_waitcnt lgkmcnt(8)\n" \
M_"a[12:15], a[172:175], a[128:131], a[12:15]\n" \
X_"a[240:241], v36 offset:40960\n" \
"s_waitcnt lgkmcnt(5)\n" \
M_"a[12:15], a[236:239], a[192:195], a[12:15]\n" \
M_"a[28:31], a[172:175], a[132:135], a[28:31]\n" \
M_"a[28:31], a[236:239], a[196:199], a[28:31]\n" \
X_"a[242:243], v36 offset:41984\n" \
M_"a[24:27], a[168:171], a[132:135], a[24:27]\n" \
M_"a[24:27], a[232:235], a[196:199], a[24:27]\n" \
M_"a[20:23], a[164:167], a[132:135], a[20:23]\n" \
M_"a[20:23], a[228:231], a[196:199], a[20:23]\n" \
X_"a[180:181], v37 offset:32768\n" \
M_"a[16:19], a[160:163], a[132:135], a[16:19]\n" \
M_"a[16:19], a[224:227], a[196:199], a[16:19]\n" \
M_"a[32:35], a[160:163], a[136:139], a[32:35]\n" \
X_"a[182:183], v37 offset:33792\n" \
M_"a[32:35], a[224:227], a[200:203], a[32:35]\n" \
M_"a[36:39], a[164:167], a[136:139], a[36:39]\n" \
"v_lshlrev_b32_e32 v45, 3, v20\n" \
"v_add_u32_e32 v45, s31, v45\n" \
M_"a[36:39], a[228:231], a[200:203], a[36:39]\n" \
"s_getpc_b64 s[86:87]\n" \
"s_sub_u32 s86, s86, 0xf800\n" \
"s_subb_u32 s87, s87, 0\n" \
"s_mov_b32 m0, s32\n" \
"s_nop 0\n" \
M_"a[40:43], a[168:171], a[136:139], a[40:43]\n" \
"global_load_lds_dword v45, s[86:87]\n" \
M_"a[40:43], a[232:235], a[200:203], a[40:43]\n" \
X_"a[244:245], v37 offset:40960\n" \
M_"a[44:47], a[172:175], a[136:139], a[44:47]\n" \
X_"a[246:247], v37 offset:41984\n" \
M_"a[44:47], a[236:239], a[200:203], a[44:47]\n" \
M_"a[60:63], a[172:175], a[140:143], a[60:63]\n" \
M_"a[60:63], a[236:239], a[204:207], a[60:63]\n" \
M_"a[56:59], a[168:171], a[140:143], a[56:59]\n" \
X_"a[184:185], v38 offset:32768\n" \
M_"a[56:59], a[232:235], a[204:207], a[56:59]\n" \
M_"a[52:55], a[164:167], a[140:143], a[52:55]\n" \
M_"a[52:55], a[228:231], a[204:207], a[52:55]\n" \
X_"a[186:187], v38 offset:33792\n" \
M_"a[48:51], a[160:163], a[140:143], a[48:51]\n" \
M_"a[48:51], a[224:227], a[204:207], a[48:51]\n" \
M_"a[64:67], a[160:163], a[144:147], a[64:67]\n" \
X_"a[248:249], v38 offset:40960\n" \
M_"a[64:67], a[224:227], a[208:211], a[64:67]\n" \
M_"a[68:71], a[164:167], a[144:147], a[68:71]\n" \
M_"a[68:71], a[228:231], a[208:211], a[68:71]\n" \
X_"a[250:251], v38 offset:41984\n" \
M_"a[72:75], a[168:171], a[144:147], a[72:75]\n" \
M_"a[72:75], a[232:235], a[208:211], a[72:75]\n" \
M_"a[76:79], a[172:175], a[144:147], a[76:79]\n" \
M_"a[76:79], a[236:239], a[208:211], a[76:79]\n" \
X_"a[188:189], v39 offset:32768\n" \
M_"a[92:95], a[172:175], a[148:151], a[92:95]\n" \
M_"a[92:95], a[236:239], a[212:215], a[92:95]\n" \
M_"a[88:91], a[168:171], a[148:151], a[88:91]\n" \
X_"a[190:191], v39 offset:33792\n" \
M_"a[88:91], a[232:235], a[212:215], a[88:91]\n" \
M_"a[84:87], a[164:167], a[148:151], a[84:87]\n" \
M_"a[84:87], a[228:231], a[212:215], a[84:87]\n" \
X_"a[252:253], v39 offset:40960\n" \
M_"a[80:83], a[160:163], a[148:151], a[80:83]\n" \
M_"a[80:83], a[224:227], a[212:215], a[80:83]\n" \
M_"a[96:99], a[160:163], a[152:155], a[96:99]\n" \
X_"a[254:255], v39 offset:41984\n" \
M_"a[96:99], a[224:227], a[216:219], a[96:99]\n" \
M_"a[100:103], a[164:167], a[152:155], a[100:103]\n" \
M_"a[100:103], a[228:231], a[216:219], a[100:103]\n" \
M_"a[104:107], a[168:171], a[152:155], a[104:107]\n" \
M_"a[104:107], a[232:235], a[216:219], a[104:107]\n" \
M_"a[108:111], a[172:175], a[152:155], a[108:111]\n" \
M_"a[108:111], a[236:239], a[216:219], a[108:111]\n" \
"s_waitcnt lgkmcnt(15)\n" \
M_"a[124:127], a[172:175], a[156:159], a[124:127]\n" \
M_"a[124:127], a[236:239], a[220:223], a[124:127]\n" \
M_"a[120:123], a[168:171], a[156:159], a[120:123]\n" \
M_"a[120:123], a[232:235], a[220:223], a[120:123]\n" \
M_"a[116:119], a[164:167], a[156:159], a[116:119]\n" \
M_"a[116:119], a[228:231], a[220:223], a[116:119]\n" \
M_"a[112:115], a[160:163], a[156:159], a[112:115]\n" \
"s_waitcnt vmcnt(1) lgkmcnt(0)\n" \
M_"a[112:115], a[224:227], a[220:223], a[112:115]\n" \
"s_barrier\n" \
"v_mov_b32_e32 v45, s62\n" \
"s_mov_b64 exec, 1\n" \
"buffer_store_dword v45, off, s[80:83], s37 offset:128 sc1\n" \
"s_mov_b64 exec, -1\n" \
M_"v[52:55], a[176:179], a[128:131], v[52:55]\n" \
"s_add_u32 m0, s30, 0x8000\n" \
"s_movk_i32 s84, 0x380\n" \
M_"v[52:55], a[240:243], a[192:195], v[52:55]\n" \
B_"v22, s[64:67], s84 offen offset:0 lds\n" \
M_"v[56:59], a[180:183], a[128:131], v[56:59]\n" \
X_"a[160:161], v32 offset:0\n" \
M_"v[56:59], a[244:247], a[192:195], v[56:59]\n" \
"s_movk_i32 s84, 0x1f80\n" \
M_"v[60:63], a[184:187], a[128:131], v[60:63]\n" \
B_"v23, s[64:67], s84 offen offset:1024 lds\n" \
M_"v[60:63], a[248:251], a[192:195], v[60:63]\n" \
X_"a[162:163], v32 offset:1024\n" \
M_"v[64:67], a[188:191], a[128:131], v[64:67]\n" \
X_"a[224:225], v32 offset:8192\n" \
M_"v[64:67], a[252:255], a[192:195], v[64:67]\n" \
R_"a[128:131], v28 offset:16384\n" \
M_"v[80:83], a[188:191], a[132:135], v[80:83]\n" \
R_"a[192:195], v29 offset:16384\n" \
M_"v[80:83], a[252:255], a[196:199], v[80:83]\n" \
"s_movk_i32 s84, 0x3b80\n" \
M_"v[76:79], a[184:187], a[132:135], v[76:79]\n" \
B_"v22, s[64:67], s84 offen offset:2048 lds\n" \
M_"v[76:79], a[248:251], a[196:199], v[76:79]\n" \
"s_movk_i32 s84, 0x5780\n" \
M_"v[72:75], a[180:183], a[132:135], v[72:75]\n" \
B_"v23, s[64:67], s84 offen offset:3072 lds\n" \
M_"v[72:75], a[244:247], a[196:199], v[72:75]\n" \
"s_add_u32 m0, s31, 0x8000\n" \
"s_mov_b32 s84, 0x380000\n" \
M_"v[68:71], a[176:179], a[132:135], v[68:71]\n" \
B_"v26, s[68:71], s84 offen offset:0 lds\n" \
M_"v[68:71], a[240:243], a[196:199], v[68:71]\n" \
R_"a[132:135], v28 offset:18432\n" \
M_"v[84:87], a[176:179], a[136:139], v[84:87]\n" \
R_"a[196:199], v29 offset:18432\n" \
M_"v[84:87], a[240:243], a[200:203], v[84:87]\n" \
"s_addk_i32 s84, 0x7c00\n" \
M_"v[88:91], a[180:183], a[136:139], v[88:91]\n" \
B_"v26, s[68:71], s84 offen offset:1024 lds\n" \
M_"v[88:91], a[244:247], a[200:203], v[88:91]\n" \
X_"a[226:227], v32 offset:9216\n" \
M_"v[92:95], a[184:187], a[136:139], v[92:95]\n" \
"s_addk_i32 s84, 0x7c00\n" \
M_"v[92:95], a[248:251], a[200:203], v[92:95]\n" \
B_"v27, s[68:71], s84 offen offset:2048 lds\n" \
M_"v[96:99], a[188:191], a[136:139], v[96:99]\n" \
X_"a[164:165], v33 offset:0\n" \
M_"v[96:99], a[252:255], a[200:203], v[96:99]\n" \
R_"a[136:139], v28 offset:20480\n" \
M_"v[112:115], a[188:191], a[140:143], v[112:115]\n" \
R_"a[200:203], v29 offset:20480\n" \
M_"v[112:115], a[252:255], a[204:207], v[112:115]\n" \
"s_addk_i32 s84, 0x7c00\n" \
"s_nop 0\n" \
M_"v[108:111], a[184:187], a[140:143], v[108:111]\n" \
B_"v27, s[68:71], s84 offen offset:3072 lds\n" \
M_"v[108:111], a[248:251], a[204:207], v[108:111]\n" \
"s_add_u32 m0, s30, 0x9000\n" \
"s_mov_b32 s84, 0x8380\n" \
M_"v[104:107], a[180:183], a[140:143], v[104:107]\n" \
B_"v22, s[64:67], s84 offen offset:0 lds\n" \
M_"v[104:107], a[244:247], a[204:207], v[104:107]\n" \
X_"a[166:167], v33 offset:1024\n" \
M_"v[100:103], a[176:179], a[140:143], v[100:103]\n" \
X_"a[228:229], v33 offset:8192\n" \
M_"v[100:103], a[240:243], a[204:207], v[100:103]\n" \
R_"a[140:143], v28 offset:22528\n" \
M_"v[116:119], a[176:179], a[144:147], v[116:119]\n" \
R_"a[204:207], v29 offset:22528\n" \
M_"v[116:119], a[240:243], a[208:211], v[116:119]\n" \
"s_addk_i32 s84, 0x1c00\n" \
M_"v[120:123], a[180:183], a[144:147], v[120:123]\n" \
B_"v23, s[64:67], s84 offen offset:1024 lds\n" \
M_"v[120:123], a[244:247], a[208:211], v[120:123]\n" \
"s_addk_i32 s84, 0x1c00\n" \
M_"v[124:127], a[184:187], a[144:147], v[124:127]\n" \
B_"v22, s[64:67], s84 offen offset:2048 lds\n" \
M_"v[124:127], a[248:251], a[208:211], v[124:127]\n" \
"s_addk_i32 s84, 0x1c00\n" \
"s_nop 0\n" \
M_"v[128:131], a[188:191], a[144:147], v[128:131]\n" \
B_"v23, s[64:67], s84 offen offset:3072 lds\n" \
M_"v[128:131], a[252:255], a[208:211], v[128:131]\n" \
R_"a[144:147], v28 offset:0\n" \
M_"v[144:147], a[188:191], a[148:151], v[144:147]\n" \
R_"a[208:211], v29 offset:0\n" \
M_"v[144:147], a[252:255], a[212:215], v[144:147]\n" \
"s_add_u32 m0, s31, 0xc000\n" \
"s_mov_b32 s84, 0x380000\n" \
M_"v[140:143], a[184:187], a[148:151], v[140:143]\n" \
B_"v26, s[72:75], s84 offen offset:0 lds\n" \
M_"v[140:143], a[248:251], a[212:215], v[140:143]\n" \
X_"a[230:231], v33 offset:9216\n" \
M_"v[136:139], a[180:183], a[148:151], v[136:139]\n" \
"s_addk_i32 s84, 0x7c00\n" \
"s_nop 0\n" \
M_"v[136:139], a[244:247], a[212:215], v[136:139]\n" \
B_"v26, s[72:75], s84 offen offset:1024 lds\n" \
M_"v[132:135], a[176:179], a[148:151], v[132:135]\n" \
X_"a[168:169], v34 offset:0\n" \
M_"v[132:135], a[240:243], a[212:215], v[132:135]\n" \
R_"a[148:151], v28 offset:2048\n" \
M_"v[148:151], a[176:179], a[152:155], v[148:151]\n" \
R_"a[212:215], v29 offset:2048\n" \
M_"v[148:151], a[240:243], a[216:219], v[148:151]\n" \
X_"a[170:171], v34 offset:1024\n" \
M_"v[152:155], a[180:183], a[152:155], v[152:155]\n" \
X_"a[232:233], v34 offset:8192\n" \
M_"v[152:155], a[244:247], a[216:219], v[152:155]\n" \
X_"a[234:235], v34 offset:9216\n" \
M_"v[156:159], a[184:187], a[152:155], v[156:159]\n" \
X_"a[172:173], v35 offset:0\n" \
M_"v[156:159], a[248:251], a[216:219], v[156:159]\n" \
X_"a[174:175], v35 offset:1024\n" \
M_"v[160:163], a[188:191], a[152:155], v[160:163]\n" \
X_"a[236:237], v35 offset:8192\n" \
M_"v[160:163], a[252:255], a[216:219], v[160:163]\n" \
R_"a[152:155], v28 offset:4096\n" \
M_"v[176:179], a[188:191], a[156:159], v[176:179]\n" \
R_"a[216:219], v29 offset:4096\n" \
M_"v[176:179], a[252:255], a[220:223], v[176:179]\n" \
X_"a[238:239], v35 offset:9216\n" \
M_"v[172:175], a[184:187], a[156:159], v[172:175]\n" \
M_"v[172:175], a[248:251], a[220:223], v[172:175]\n" \
M_"v[168:171], a[180:183], a[156:159], v[168:171]\n" \
M_"v[168:171], a[244:247], a[220:223], v[168:171]\n" \
M_"v[164:167], a[176:179], a[156:159], v[164:167]\n" \
M_"v[164:167], a[240:243], a[220:223], v[164:167]\n" \
R_"a[156:159], v28 offset:6144\n" \
R_"a[220:223], v29 offset:6144\n" \
"s_waitcnt lgkmcnt(15)\n" \
M_"a[0:3], a[160:163], a[128:131], a[0:3]\n" \
"s_addk_i32 s84, 0x7c00\n" \
M_"a[0:3], a[224:227], a[192:195], a[0:3]\n" \
B_"v27, s[72:75], s84 offen offset:2048 lds\n" \
M_"a[4:7], a[164:167], a[128:131], a[4:7]\n" \
X_"a[176:177], v36 offset:0\n" \
"s_waitcnt lgkmcnt(15)\n" \
M_"a[4:7], a[228:231], a[192:195], a[4:7]\n" \
"s_addk_i32 s84, 0x7c00\n" \
"s_waitcnt lgkmcnt(11)\n" \
M_"a[8:11], a[168:171], a[128:131], a[8:11]\n" \
B_"v27, s[72:75], s84 offen offset:3072 lds\n" \
"s_waitcnt lgkmcnt(9)\n" \
M_"a[8:11], a[232:235], a[192:195], a[8:11]\n" \
X_"a[178:179], v36 offset:1024\n" \
"s_waitcnt lgkmcnt(8)\n" \
M_"a[12:15], a[172:175], a[128:131], a[12:15]\n" \
X_"a[240:241], v36 offset:8192\n" \
"s_waitcnt lgkmcnt(5)\n" \
M_"a[12:15], a[236:239], a[192:195], a[12:15]\n" \
M_"a[28:31], a[172:175], a[132:135], a[28:31]\n" \
M_"a[28:31], a[236:239], a[196:199], a[28:31]\n" \
X_"a[242:243], v36 offset:9216\n" \
M_"a[24:27], a[168:171], a[132:135], a[24:27]\n" \
M_"a[24:27], a[232:235], a[196:199], a[24:27]\n" \
M_"a[20:23], a[164:167], a[132:135], a[20:23]\n" \
M_"a[20:23], a[228:231], a[196:199], a[20:23]\n" \
X_"a[180:181], v37 offset:0\n" \
M_"a[16:19], a[160:163], a[132:135], a[16:19]\n" \
M_"a[16:19], a[224:227], a[196:199], a[16:19]\n" \
M_"a[32:35], a[160:163], a[136:139], a[32:35]\n" \
"v_bfe_u32 v44, v20, 2, 5\n" \
X_"a[182:183], v37 offset:1024\n" \
M_"a[32:35], a[224:227], a[200:203], a[32:35]\n" \
"buffer_load_dword v44, v44, s[80:83], s38 offen offset:128 sc1\n" \
M_"a[36:39], a[164:167], a[136:139], a[36:39]\n" \
M_"a[36:39], a[228:231], a[200:203], a[36:39]\n" \
X_"a[244:245], v37 offset:8192\n" \
M_"a[40:43], a[168:171], a[136:139], a[40:43]\n" \
M_"a[40:43], a[232:235], a[200:203], a[40:43]\n" \
M_"a[44:47], a[172:175], a[136:139], a[44:47]\n" \
X_"a[246:247], v37 offset:9216\n" \
M_"a[44:47], a[236:239], a[200:203], a[44:47]\n" \
M_"a[60:63], a[172:175], a[140:143], a[60:63]\n" \
"global_store_dwordx4 v49, v[0:3], s[28:29] offset:0 sc1\n" \
M_"a[60:63], a[236:239], a[204:207], a[60:63]\n" \
M_"a[56:59], a[168:171], a[140:143], a[56:59]\n" \
X_"a[184:185], v38 offset:0\n" \
M_"a[56:59], a[232:235], a[204:207], a[56:59]\n" \
M_"a[52:55], a[164:167], a[140:143], a[52:55]\n" \
M_"a[52:55], a[228:231], a[204:207], a[52:55]\n" \
X_"a[186:187], v38 offset:1024\n" \
M_"a[48:51], a[160:163], a[140:143], a[48:51]\n" \
M_"a[48:51], a[224:227], a[204:207], a[48:51]\n" \
M_"a[64:67], a[160:163], a[144:147], a[64:67]\n" \
"global_store_dwordx4 v49, v[0:3], s[28:29] offset:1024 sc1\n" \
M_"a[64:67], a[224:227], a[208:211], a[64:67]\n" \
X_"a[248:249], v38 offset:8192\n" \
M_"a[68:71], a[164:167], a[144:147], a[68:71]\n" \
M_"a[68:71], a[228:231], a[208:211], a[68:71]\n" \
X_"a[250:251], v38 offset:9216\n" \
M_"a[72:75], a[168:171], a[144:147], a[72:75]\n" \
M_"a[72:75], a[232:235], a[208:211], a[72:75]\n" \
M_"a[76:79], a[172:175], a[144:147], a[76:79]\n" \
M_"a[76:79], a[236:239], a[208:211], a[76:79]\n" \
X_"a[188:189], v39 offset:0\n" \
M_"a[92:95], a[172:175], a[148:151], a[92:95]\n" \
"s_mov_b64 exec, 0xffff\n" \
"global_store_dwordx4 v49, v[0:3], s[28:29] offset:2048 sc1\n" \
"s_mov_b64 exec, -1\n" \
"s_nop 0\n" \
M_"a[92:95], a[236:239], a[212:215], a[92:95]\n" \
M_"a[88:91], a[168:171], a[148:151], a[88:91]\n" \
X_"a[190:191], v39 offset:1024\n" \
M_"a[88:91], a[232:235], a[212:215], a[88:91]\n" \
M_"a[84:87], a[164:167], a[148:151], a[84:87]\n" \
M_"a[84:87], a[228:231], a[212:215], a[84:87]\n" \
X_"a[252:253], v39 offset:8192\n" \
M_"a[80:83], a[160:163], a[148:151], a[80:83]\n" \
M_"a[80:83], a[224:227], a[212:215], a[80:83]\n" \
M_"a[96:99], a[160:163], a[152:155], a[96:99]\n" \
X_"a[254:255], v39 offset:9216\n" \
M_"a[96:99], a[224:227], a[216:219], a[96:99]\n" \
M_"a[100:103], a[164:167], a[152:155], a[100:103]\n" \
M_"a[100:103], a[228:231], a[216:219], a[100:103]\n" \
M_"a[104:107], a[168:171], a[152:155], a[104:107]\n" \
M_"a[104:107], a[232:235], a[216:219], a[104:107]\n" \
M_"a[108:111], a[172:175], a[152:155], a[108:111]\n" \
M_"a[108:111], a[236:239], a[216:219], a[108:111]\n" \
"s_waitcnt lgkmcnt(15)\n" \
M_"a[124:127], a[172:175], a[156:159], a[124:127]\n" \
M_"a[124:127], a[236:239], a[220:223], a[124:127]\n" \
M_"a[120:123], a[168:171], a[156:159], a[120:123]\n" \
M_"a[120:123], a[232:235], a[220:223], a[120:123]\n" \
M_"a[116:119], a[164:167], a[156:159], a[116:119]\n" \
M_"a[116:119], a[228:231], a[220:223], a[116:119]\n" \
M_"a[112:115], a[160:163], a[156:159], a[112:115]\n" \
"s_waitcnt vmcnt(3) lgkmcnt(0)\n" \
M_"a[112:115], a[224:227], a[220:223], a[112:115]\n" \
"s_barrier\n" \
"v_cmp_ne_u32_e32 vcc, s62, v44\n" \
"s_nop 4\n" \
"s_cbranch_vccnz L_poll60_%=\n" \
"L_pollr60_%=:\n" \
"s_mov_b32 s68, s12\n" \
"s_and_b32 s69, s13, 0xffff\n" \
"s_mov_b32 s70, 0x480000\n" \
"s_mov_b32 s71, 0x20000\n" \
M_"v[52:55], a[176:179], a[128:131], v[52:55]\n" \
"s_add_u32 m0, s40, 0x0\n" \
"s_add_u32 s85, s39, 0x0\n" \
"s_nop 0\n" \
".fill (L_pb + 16096 - .) / 4, 4, 0xBF800000\n" \
"L_pp4:\n" \
"s_cbranch_execz L_pret\n" \
"s_nop 0\n" \
M_"v[52:55], a[240:243], a[192:195], v[52:55]\n" \
B_"v20, s[80:83], s85 offen sc1 lds\n" \
M_"v[56:59], a[180:183], a[128:131], v[56:59]\n" \
X_"a[160:161], v32 offset:32768\n" \
M_"v[56:59], a[244:247], a[192:195], v[56:59]\n" \
"s_add_u32 m0, s40, 0x1000\n" \
"s_add_u32 s85, s39, 0x1000\n" \
M_"v[60:63], a[184:187], a[128:131], v[60:63]\n" \
B_"v20, s[80:83], s85 offen sc1 lds\n" \
M_"v[60:63], a[248:251], a[192:195], v[60:63]\n" \
X_"a[162:163], v32 offset:33792\n" \
M_"v[64:67], a[188:191], a[128:131], v[64:67]\n" \
X_"a[224:225], v32 offset:40960\n" \
M_"v[64:67], a[252:255], a[192:195], v[64:67]\n" \
R_"a[128:131], v28 offset:49152\n" \
M_"v[80:83], a[188:191], a[132:135], v[80:83]\n" \
R_"a[192:195], v29 offset:49152\n" \
M_"v[80:83], a[252:255], a[196:199], v[80:83]\n" \
"s_add_u32 m0, s40, 0x3000\n" \
"s_movk_i32 s84, 0x0\n" \
M_"v[76:79], a[184:187], a[132:135], v[76:79]\n" \
B_"v21, s[76:79], s84 offen offset:0 lds\n" \
M_"v[76:79], a[248:251], a[196:199], v[76:79]\n" \
"s_add_u32 m0, s30, 0x0\n" \
"s_movk_i32 s84, 0x0\n" \
"s_nop 0\n" \
M_"v[72:75], a[180:183], a[132:135], v[72:75]\n" \
B_"v24, s[76:79], s84 offen offset:0 lds\n" \
M_"v[72:75], a[244:247], a[196:199], v[72:75]\n" \
X_"a[226:227], v32 offset:41984\n" \
M_"v[68:71], a[176:179], a[132:135], v[68:71]\n" \
X_"a[164:165], v33 offset:32768\n" \
M_"v[68:71], a[240:243], a[196:199], v[68:71]\n" \
R_"a[132:135], v28 offset:51200\n" \
M_"v[84:87], a[176:179], a[136:139], v[84:87]\n" \
R_"a[196:199], v29 offset:51200\n" \
M_"v[84:87], a[240:243], a[200:203], v[84:87]\n" \
"s_movk_i32 s84, 0x2000\n" \
M_"v[88:91], a[180:183], a[136:139], v[88:91]\n" \
B_"v25, s[76:79], s84 offen offset:1024 lds\n" \
M_"v[88:91], a[244:247], a[200:203], v[88:91]\n" \
"s_movk_i32 s84, 0x4000\n" \
M_"v[92:95], a[184:187], a[136:139], v[92:95]\n" \
B_"v24, s[76:79], s84 offen offset:2048 lds\n" \
M_"v[92:95], a[248:251], a[200:203], v[92:95]\n" \
X_"a[166:167], v33 offset:33792\n" \
M_"v[96:99], a[188:191], a[136:139], v[96:99]\n" \
X_"a[228:229], v33 offset:40960\n" \
M_"v[96:99], a[252:255], a[200:203], v[96:99]\n" \
R_"a[136:139], v28 offset:53248\n" \
M_"v[112:115], a[188:191], a[140:143], v[112:115]\n" \
R_"a[200:203], v29 offset:53248\n" \
M_"v[112:115], a[252:255], a[204:207], v[112:115]\n" \
"s_movk_i32 s84, 0x6000\n" \
M_"v[108:111], a[184:187], a[140:143], v[108:111]\n" \
B_"v25, s[76:79], s84 offen offset:3072 lds\n" \
M_"v[108:111], a[248:251], a[204:207], v[108:111]\n" \
"s_add_u32 m0, s31, 0x0\n" \
"s_movk_i32 s84, 0x0\n" \
M_"v[104:107], a[180:183], a[140:143], v[104:107]\n" \
B_"v26, s[68:71], s84 offen offset:0 lds\n" \
M_"v[104:107], a[244:247], a[204:207], v[104:107]\n" \
"s_movk_i32 s84, 0x7c00\n" \
M_"v[100:103], a[176:179], a[140:143], v[100:103]\n" \
B_"v26, s[68:71], s84 offen offset:1024 lds\n" \
M_"v[100:103], a[240:243], a[204:207], v[100:103]\n" \
R_"a[140:143], v28 offset:55296\n" \
M_"v[116:119], a[176:179], a[144:147], v[116:119]\n" \
R_"a[204:207], v29 offset:55296\n" \
M_"v[116:119], a[240:243], a[208:211], v[116:119]\n" \
"s_addk_i32 s84, 0x7c00\n" \
M_"v[120:123], a[180:183], a[144:147], v[120:123]\n" \
B_"v27, s[68:71], s84 offen offset:2048 lds\n" \
M_"v[120:123], a[244:247], a[208:211], v[120:123]\n" \
X_"a[230:231], v33 offset:41984\n" \
M_"v[124:127], a[184:187], a[144:147], v[124:127]\n" \
"s_addk_i32 s84, 0x7c00\n" \
M_"v[124:127], a[248:251], a[208:211], v[124:127]\n" \
B_"v27, s[68:71], s84 offen offset:3072 lds\n" \
M_"v[128:131], a[188:191], a[144:147], v[128:131]\n" \
X_"a[168:169], v34 offset:32768\n" \
M_"v[128:131], a[252:255], a[208:211], v[128:131]\n" \
R_"a[144:147], v28 offset:32768\n" \
M_"v[144:147], a[188:191], a[148:151], v[144:147]\n" \
R_"a[208:211], v29 offset:32768\n" \
M_"v[144:147], a[252:255], a[212:215], v[144:147]\n" \
"s_add_u32 m0, s30, 0x1000\n" \
"s_mov_b32 s84, 0x9000\n" \
M_"v[140:143], a[184:187], a[148:151], v[140:143]\n" \
B_"v24, s[76:79], s84 offen offset:0 lds\n" \
M_"v[140:143], a[248:251], a[212:215], v[140:143]\n" \
X_"a[170:171], v34 offset:33792\n" \
M_"v[136:139], a[180:183], a[148:151], v[136:139]\n" \
"s_addk_i32 s84, 0x2000\n" \
"s_nop 0\n" \
M_"v[136:139], a[244:247], a[212:215], v[136:139]\n" \
B_"v25, s[76:79], s84 offen offset:1024 lds\n" \
M_"v[132:135], a[176:179], a[148:151], v[132:135]\n" \
X_"a[232:233], v34 offset:40960\n" \
M_"v[132:135], a[240:243], a[212:215], v[132:135]\n" \
R_"a[148:151], v28 offset:34816\n" \
M_"v[148:151], a[176:179], a[152:155], v[148:151]\n" \
R_"a[212:215], v29 offset:34816\n" \
M_"v[148:151], a[240:243], a[216:219], v[148:151]\n" \
X_"a[234:235], v34 offset:41984\n" \
M_"v[152:155], a[180:183], a[152:155], v[152:155]\n" \
X_"a[172:173], v35 offset:32768\n" \
M_"v[152:155], a[244:247], a[216:219], v[152:155]\n" \
X_"a[174:175], v35 offset:33792\n" \
M_"v[156:159], a[184:187], a[152:155], v[156:159]\n" \
X_"a[236:237], v35 offset:40960\n" \
M_"v[156:159], a[248:251], a[216:219], v[156:159]\n" \
X_"a[238:239], v35 offset:41984\n" \
M_"v[160:163], a[188:191], a[152:155], v[160:163]\n" \
M_"v[160:163], a[252:255], a[216:219], v[160:163]\n" \
R_"a[152:155], v28 offset:36864\n" \
M_"v[176:179], a[188:191], a[156:159], v[176:179]\n" \
R_"a[216:219], v29 offset:36864\n" \
M_"v[176:179], a[252:255], a[220:223], v[176:179]\n" \
M_"v[172:175], a[184:187], a[156:159], v[172:175]\n" \
M_"v[172:175], a[248:251], a[220:223], v[172:175]\n" \
M_"v[168:171], a[180:183], a[156:159], v[168:171]\n" \
M_"v[168:171], a[244:247], a[220:223], v[168:171]\n" \
M_"v[164:167], a[176:179], a[156:159], v[164:167]\n" \
M_"v[164:167], a[240:243], a[220:223], v[164:167]\n" \
R_"a[156:159], v28 offset:38912\n" \
R_"a[220:223], v29 offset:38912\n" \
"s_waitcnt lgkmcnt(15)\n" \
M_"a[0:3], a[160:163], a[128:131], a[0:3]\n" \
"s_addk_i32 s84, 0x2000\n" \
M_"a[0:3], a[224:227], a[192:195], a[0:3]\n" \
B_"v24, s[76:79], s84 offen offset:2048 lds\n" \
M_"a[4:7], a[164:167], a[128:131], a[4:7]\n" \
X_"a[176:177], v36 offset:32768\n" \
M_"a[4:7], a[228:231], a[192:195], a[4:7]\n" \
"s_addk_i32 s84, 0x2000\n" \
"s_waitcnt lgkmcnt(13)\n" \
M_"a[8:11], a[168:171], a[128:131], a[8:11]\n" \
B_"v25, s[76:79], s84 offen offset:3072 lds\n" \
"s_waitcnt lgkmcnt(9)\n" \
M_"a[8:11], a[232:235], a[192:195], a[8:11]\n" \
X_"a[178:179], v36 offset:33792\n" \
"s_waitcnt lgkmcnt(8)\n" \
M_"a[12:15], a[172:175], a[128:131], a[12:15]\n" \
X_"a[240:241], v36 offset:40960\n" \
"v_accvgpr_read_b32 v184, a0\n" \
"s_waitcnt lgkmcnt(7)\n" \
M_"a[12:15], a[236:239], a[192:195], a[12:15]\n" \
"v_accvgpr_read_b32 v185, a1\n" \
"v_accvgpr_read_b32 v186, a2\n" \
M_"a[28:31], a[172:175], a[132:135], a[28:31]\n" \
"v_accvgpr_read_b32 v187, a3\n" \
"v_accvgpr_read_b32 v180, a4\n" \
M_"a[28:31], a[236:239], a[196:199], a[28:31]\n" \
"s_add_u32 m0, s30, 0x20000\n" \
"s_movk_i32 s84, 0x100\n" \
M_"a[24:27], a[168:171], a[132:135], a[24:27]\n" \
B_"v24, s[76:79], s84 offen offset:0 lds\n" \
M_"a[24:27], a[232:235], a[196:199], a[24:27]\n" \
X_"a[242:243], v36 offset:41984\n" \
"v_accvgpr_read_b32 v181, a5\n" \
M_"a[20:23], a[164:167], a[132:135], a[20:23]\n" \
"v_accvgpr_read_b32 v182, a6\n" \
"v_accvgpr_read_b32 v183, a7\n" \
M_"a[20:23], a[228:231], a[196:199], a[20:23]\n" \
X_"a[180:181], v37 offset:32768\n" \
C_"v184, v184, v185\n" \
M_"a[16:19], a[160:163], a[132:135], a[16:19]\n" \
"s_movk_i32 s84, 0x2100\n" \
"s_nop 0\n" \
M_"a[16:19], a[224:227], a[196:199], a[16:19]\n" \
B_"v25, s[76:79], s84 offen offset:1024 lds\n" \
M_"a[32:35], a[160:163], a[136:139], a[32:35]\n" \
X_"a[182:183], v37 offset:33792\n" \
C_"v185, v186, v187\n" \
M_"a[32:35], a[224:227], a[200:203], a[32:35]\n" \
C_"v186, v180, v181\n" \
C_"v187, v182, v183\n" \
M_"a[36:39], a[164:167], a[136:139], a[36:39]\n" \
"v_accvgpr_read_b32 v188, a8\n" \
"v_accvgpr_read_b32 v189, a9\n" \
M_"a[36:39], a[228:231], a[200:203], a[36:39]\n" \
"s_movk_i32 s84, 0x4100\n" \
"s_nop 0\n" \
M_"a[40:43], a[168:171], a[136:139], a[40:43]\n" \
B_"v24, s[76:79], s84 offen offset:2048 lds\n" \
M_"a[40:43], a[232:235], a[200:203], a[40:43]\n" \
X_"a[244:245], v37 offset:40960\n" \
"v_accvgpr_read_b32 v190, a10\n" \
M_"a[44:47], a[172:175], a[136:139], a[44:47]\n" \
X_"a[246:247], v37 offset:41984\n" \
"v_accvgpr_read_b32 v191, a11\n" \
M_"a[44:47], a[236:239], a[200:203], a[44:47]\n" \
"v_accvgpr_read_b32 v180, a12\n" \
"v_accvgpr_read_b32 v181, a13\n" \
M_"a[60:63], a[172:175], a[140:143], a[60:63]\n" \
"s_movk_i32 s84, 0x6100\n" \
M_"a[60:63], a[236:239], a[204:207], a[60:63]\n" \
B_"v25, s[76:79], s84 offen offset:3072 lds\n" \
M_"a[56:59], a[168:171], a[140:143], a[56:59]\n" \
X_"a[184:185], v38 offset:32768\n" \
"v_accvgpr_read_b32 v182, a14\n" \
M_"a[56:59], a[232:235], a[204:207], a[56:59]\n" \
"v_accvgpr_read_b32 v183, a15\n" \
C_"v188, v188, v189\n" \
M_"a[52:55], a[164:167], a[140:143], a[52:55]\n" \
"s_add_u32 m0, s30, 0x21000\n" \
"s_addk_i32 s84, 0x3000\n" \
M_"a[52:55], a[228:231], a[204:207], a[52:55]\n" \
B_"v24, s[76:79], s84 offen offset:0 lds\n" \
M_"a[48:51], a[160:163], a[140:143], a[48:51]\n" \
X_"a[186:187], v38 offset:33792\n" \
C_"v189, v190, v191\n" \
M_"a[48:51], a[224:227], a[204:207], a[48:51]\n" \
C_"v190, v180, v181\n" \
C_"v191, v182, v183\n" \
M_"a[64:67], a[160:163], a[144:147], a[64:67]\n" \
X_"a[248:249], v38 offset:40960\n" \
"v_accvgpr_read_b32 v192, a24\n" \
M_"a[64:67], a[224:227], a[208:211], a[64:67]\n" \
"s_addk_i32 s84, 0x2000\n" \
"s_nop 0\n" \
M_"a[68:71], a[164:167], a[144:147], a[68:71]\n" \
B_"v25, s[76:79], s84 offen offset:1024 lds\n" \
M_"a[68:71], a[228:231], a[208:211], a[68:71]\n" \
X_"a[250:251], v38 offset:41984\n" \
"v_accvgpr_read_b32 v193, a25\n" \
M_"a[72:75], a[168:171], a[144:147], a[72:75]\n" \
"v_accvgpr_read_b32 v194, a26\n" \
"v_accvgpr_read_b32 v195, a27\n" \
M_"a[72:75], a[232:235], a[208:211], a[72:75]\n" \
"v_accvgpr_read_b32 v180, a28\n" \
"v_accvgpr_read_b32 v181, a29\n" \
M_"a[76:79], a[172:175], a[144:147], a[76:79]\n" \
"s_addk_i32 s84, 0x2000\n" \
M_"a[76:79], a[236:239], a[208:211], a[76:79]\n" \
B_"v24, s[76:79], s84 offen offset:2048 lds\n" \
M_"a[92:95], a[172:175], a[148:151], a[92:95]\n" \
"s_add_u32 s89, s41, 0x0\n" \
"buffer_store_dwordx4 v[184:187], v20, s[80:83], s89 offen offset:0 sc1\n" \
M_"a[92:95], a[236:239], a[212:215], a[92:95]\n" \
X_"a[188:189], v39 offset:32768\n" \
"v_accvgpr_read_b32 v182, a30\n" \
M_"a[88:91], a[168:171], a[148:151], a[88:91]\n" \
X_"a[190:191], v39 offset:33792\n" \
"v_accvgpr_read_b32 v183, a31\n" \
M_"a[88:91], a[232:235], a[212:215], a[88:91]\n" \
"s_addk_i32 s84, 0x2000\n" \
M_"a[84:87], a[164:167], a[148:151], a[84:87]\n" \
B_"v25, s[76:79], s84 offen offset:3072 lds\n" \
M_"a[84:87], a[228:231], a[212:215], a[84:87]\n" \
X_"a[252:253], v39 offset:40960\n" \
C_"v192, v192, v193\n" \
M_"a[80:83], a[160:163], a[148:151], a[80:83]\n" \
"s_add_u32 s88, s41, 0x0\n" \
"buffer_store_dwordx4 v[188:191], v20, s[80:83], s88 offen offset:1024 sc1\n" \
M_"a[80:83], a[224:227], a[212:215], a[80:83]\n" \
C_"v193, v194, v195\n" \
C_"v194, v180, v181\n" \
"s_waitcnt lgkmcnt(15)\n" \
"s_nop 0\n" \
M_"a[96:99], a[160:163], a[152:155], a[96:99]\n" \
X_"a[254:255], v39 offset:41984\n" \
C_"v195, v182, v183\n" \
M_"a[96:99], a[224:227], a[216:219], a[96:99]\n" \
"v_accvgpr_read_b32 v196, a16\n" \
"v_accvgpr_read_b32 v197, a17\n" \
M_"a[100:103], a[164:167], a[152:155], a[100:103]\n" \
"v_accvgpr_read_b32 v198, a18\n" \
"v_accvgpr_read_b32 v199, a19\n" \
M_"a[100:103], a[228:231], a[216:219], a[100:103]\n" \
"v_accvgpr_read_b32 v180, a20\n" \
"v_accvgpr_read_b32 v181, a21\n" \
M_"a[104:107], a[168:171], a[152:155], a[104:107]\n" \
"s_add_u32 s89, s41, 0x1000\n" \
"buffer_store_dwordx4 v[192:195], v20, s[80:83], s89 offen offset:1024 sc1\n" \
M_"a[104:107], a[232:235], a[216:219], a[104:107]\n" \
"v_accvgpr_read_b32 v182, a22\n" \
"v_accvgpr_read_b32 v183, a23\n" \
M_"a[108:111], a[172:175], a[152:155], a[108:111]\n" \
C_"v196, v196, v197\n" \
C_"v197, v198, v199\n" \
M_"a[108:111], a[236:239], a[216:219], a[108:111]\n" \
C_"v198, v180, v181\n" \
C_"v199, v182, v183\n" \
M_"a[124:127], a[172:175], a[156:159], a[124:127]\n" \
"v_accvgpr_read_b32 v200, a32\n" \
"v_accvgpr_read_b32 v201, a33\n" \
M_"a[124:127], a[236:239], a[220:223], a[124:127]\n" \
"v_accvgpr_read_b32 v202, a34\n" \
"v_accvgpr_read_b32 v203, a35\n" \
M_"a[120:123], a[168:171], a[156:159], a[120:123]\n" \
"s_add_u32 s88, s41, 0x1000\n" \
"buffer_store_dwordx4 v[196:199], v20, s[80:83], s88 offen offset:0 sc1\n" \
M_"a[120:123], a[232:235], a[220:223], a[120:123]\n" \
"v_accvgpr_read_b32 v180, a36\n" \
"v_accvgpr_read_b32 v181, a37\n" \
M_"a[116:119], a[164:167], a[156:159], a[116:119]\n" \
"v_accvgpr_read_b32 v182, a38\n" \
"v_accvgpr_read_b32 v183, a39\n" \
M_"a[116:119], a[228:231], a[220:223], a[116:119]\n" \
C_"v200, v200, v201\n" \
C_"v201, v202, v203\n" \
M_"a[112:115], a[160:163], a[156:159], a[112:115]\n" \
C_"v202, v180, v181\n" \
C_"v203, v182, v183\n" \
M_"a[112:115], a[224:227], a[220:223], a[112:115]\n" \
"v_accvgpr_read_b32 v204, a40\n" \
"v_accvgpr_read_b32 v205, a41\n" \
"s_waitcnt vmcnt(12) lgkmcnt(0)\n" \
"s_barrier\n" \
"v_mov_b32_e32 v36, v32\n" \
"v_xor_b32_e32 v32, 0x0, v48\n" \
"v_mov_b32_e32 v37, v33\n" \
"v_xor_b32_e32 v33, 0x20, v48\n" \
"v_mov_b32_e32 v38, v34\n" \
"v_xor_b32_e32 v34, 0x40, v48\n" \
"v_mov_b32_e64 v39, v35\n" \
"v_xor_b32_e32 v35, 0x60, v48\n" \
"v_add_u32_e32 v22, s44, v28\n" \
"v_add_u32_e32 v28, s43, v28\n" \
"v_add_u32_e32 v23, s44, v29\n" \
"v_add_u32_e32 v29, s43, v29\n" \
M_"v[52:55], a[176:179], a[128:131], v[52:55]\n" \
"s_add_u32 m0, s40, 0x8000\n" \
"s_add_u32 s85, s39, 0x2000\n" \
M_"v[52:55], a[240:243], a[192:195], v[52:55]\n" \
B_"v20, s[80:83], s85 offen sc1 lds\n" \
M_"v[56:59], a[180:183], a[128:131], v[56:59]\n" \
X_"a[160:161], v32 offset:0\n" \
"v_accvgpr_read_b32 v206, a42\n" \
M_"v[56:59], a[244:247], a[192:195], v[56:59]\n" \
"s_add_u32 m0, s40, 0x9000\n" \
"s_add_u32 s85, s39, 0x3000\n" \
M_"v[60:63], a[184:187], a[128:131], v[60:63]\n" \
B_"v20, s[80:83], s85 offen sc1 lds\n" \
M_"v[60:63], a[248:251], a[192:195], v[60:63]\n" \
X_"a[162:163], v32 offset:512\n" \
"v_accvgpr_read_b32 v207, a43\n" \
M_"v[64:67], a[188:191], a[128:131], v[64:67]\n" \
X_"a[224:225], v32 offset:4096\n" \
"v_accvgpr_read_b32 v180, a44\n" \
M_"v[64:67], a[252:255], a[192:195], v[64:67]\n" \
R_"a[128:131], v28 offset:0\n" \
"v_accvgpr_read_b32 v181, a45\n" \
M_"v[80:83], a[188:191], a[132:135], v[80:83]\n" \
R_"a[192:195], v29 offset:0\n" \
"v_accvgpr_read_b32 v182, a46\n" \
M_"v[80:83], a[252:255], a[196:199], v[80:83]\n" \
"s_add_u32 m0, s40, 0xb000\n" \
"s_movk_i32 s84, 0x80\n" \
M_"v[76:79], a[184:187], a[132:135], v[76:79]\n" \
B_"v21, s[76:79], s84 offen offset:0 lds\n" \
M_"v[76:79], a[248:251], a[196:199], v[76:79]\n" \
"s_add_u32 m0, s30, 0x8000\n" \
"s_movk_i32 s84, 0x80\n" \
M_"v[72:75], a[180:183], a[132:135], v[72:75]\n" \
B_"v24, s[76:79], s84 offen offset:0 lds\n" \
M_"v[72:75], a[244:247], a[196:199], v[72:75]\n" \
X_"a[226:227], v32 offset:4608\n" \
"v_accvgpr_read_b32 v183, a47\n" \
M_"v[68:71], a[176:179], a[132:135], v[68:71]\n" \
X_"a[164:165], v33 offset:0\n" \
C_"v204, v204, v205\n" \
M_"v[68:71], a[240:243], a[196:199], v[68:71]\n" \
R_"a[132:135], v28 offset:2048\n" \
C_"v205, v206, v207\n" \
M_"v[84:87], a[176:179], a[136:139], v[84:87]\n" \
R_"a[196:199], v29 offset:2048\n" \
C_"v206, v180, v181\n" \
M_"v[84:87], a[240:243], a[200:203], v[84:87]\n" \
"s_movk_i32 s84, 0x2080\n" \
M_"v[88:91], a[180:183], a[136:139], v[88:91]\n" \
B_"v25, s[76:79], s84 offen offset:1024 lds\n" \
M_"v[88:91], a[244:247], a[200:203], v[88:91]\n" \
"s_movk_i32 s84, 0x4080\n" \
M_"v[92:95], a[184:187], a[136:139], v[92:95]\n" \
B_"v24, s[76:79], s84 offen offset:2048 lds\n" \
M_"v[92:95], a[248:251], a[200:203], v[92:95]\n" \
X_"a[166:167], v33 offset:512\n" \
C_"v207, v182, v183\n" \
M_"v[96:99], a[188:191], a[136:139], v[96:99]\n" \
X_"a[228:229], v33 offset:4096\n" \
"v_accvgpr_read_b32 v208, a56\n" \
M_"v[96:99], a[252:255], a[200:203], v[96:99]\n" \
R_"a[136:139], v28 offset:4096\n" \
"v_accvgpr_read_b32 v209, a57\n" \
M_"v[112:115], a[188:191], a[140:143], v[112:115]\n" \
R_"a[200:203], v29 offset:4096\n" \
"v_accvgpr_read_b32 v210, a58\n" \
M_"v[112:115], a[252:255], a[204:207], v[112:115]\n" \
"s_movk_i32 s84, 0x6080\n" \
M_"v[108:111], a[184:187], a[140:143], v[108:111]\n" \
B_"v25, s[76:79], s84 offen offset:3072 lds\n" \
M_"v[108:111], a[248:251], a[204:207], v[108:111]\n" \
"s_add_u32 m0, s31, 0x8000\n" \
"s_mov_b32 s84, 0x80000\n" \
M_"v[104:107], a[180:183], a[140:143], v[104:107]\n" \
B_"v26, s[68:71], s84 offen offset:0 lds\n" \
M_"v[104:107], a[244:247], a[204:207], v[104:107]\n" \
"s_addk_i32 s84, 0x7c00\n" \
M_"v[100:103], a[176:179], a[140:143], v[100:103]\n" \
B_"v26, s[68:71], s84 offen offset:1024 lds\n" \
M_"v[100:103], a[240:243], a[204:207], v[100:103]\n" \
R_"a[140:143], v28 offset:6144\n" \
"v_accvgpr_read_b32 v211, a59\n" \
M_"v[116:119], a[176:179], a[144:147], v[116:119]\n" \
R_"a[204:207], v29 offset:6144\n" \
"v_accvgpr_read_b32 v180, a60\n" \
M_"v[116:119], a[240:243], a[208:211], v[116:119]\n" \
"s_addk_i32 s84, 0x7c00\n" \
M_"v[120:123], a[180:183], a[144:147], v[120:123]\n" \
B_"v27, s[68:71], s84 offen offset:2048 lds\n" \
M_"v[120:123], a[244:247], a[208:211], v[120:123]\n" \
X_"a[230:231], v33 offset:4608\n" \
"v_accvgpr_read_b32 v181, a61\n" \
M_"v[124:127], a[184:187], a[144:147], v[124:127]\n" \
"s_addk_i32 s84, 0x7c00\n" \
M_"v[124:127], a[248:251], a[208:211], v[124:127]\n" \
B_"v27, s[68:71], s84 offen offset:3072 lds\n" \
M_"v[128:131], a[188:191], a[144:147], v[128:131]\n" \
X_"a[172:173], v35 offset:0\n" \
"v_accvgpr_read_b32 v182, a62\n" \
M_"v[128:131], a[252:255], a[208:211], v[128:131]\n" \
R_"a[144:147], v28 offset:8192\n" \
"v_accvgpr_read_b32 v183, a63\n" \
M_"v[144:147], a[188:191], a[148:151], v[144:147]\n" \
R_"a[208:211], v29 offset:8192\n" \
C_"v208, v208, v209\n" \
M_"v[144:147], a[252:255], a[212:215], v[144:147]\n" \
"s_add_u32 m0, s30, 0x9000\n" \
"s_mov_b32 s84, 0x9080\n" \
M_"v[140:143], a[184:187], a[148:151], v[140:143]\n" \
B_"v24, s[76:79], s84 offen offset:0 lds\n" \
M_"v[140:143], a[248:251], a[212:215], v[140:143]\n" \
X_"a[174:175], v35 offset:512\n" \
C_"v209, v210, v211\n" \
M_"v[136:139], a[180:183], a[148:151], v[136:139]\n" \
"s_addk_i32 s84, 0x2000\n" \
"s_nop 0\n" \
M_"v[136:139], a[244:247], a[212:215], v[136:139]\n" \
B_"v25, s[76:79], s84 offen offset:1024 lds\n" \
M_"v[132:135], a[176:179], a[148:151], v[132:135]\n" \
X_"a[236:237], v35 offset:4096\n" \
C_"v210, v180, v181\n" \
M_"v[132:135], a[240:243], a[212:215], v[132:135]\n" \
R_"a[148:151], v28 offset:10240\n" \
C_"v211, v182, v183\n" \
M_"v[148:151], a[176:179], a[152:155], v[148:151]\n" \
R_"a[212:215], v29 offset:10240\n" \
"v_accvgpr_read_b32 v212, a48\n" \
M_"v[148:151], a[240:243], a[216:219], v[148:151]\n" \
X_"a[238:239], v35 offset:4608\n" \
"v_accvgpr_read_b32 v213, a49\n" \
M_"v[152:155], a[180:183], a[152:155], v[152:155]\n" \
X_"a[168:169], v34 offset:0\n" \
"v_accvgpr_read_b32 v214, a50\n" \
M_"v[152:155], a[244:247], a[216:219], v[152:155]\n" \
X_"a[170:171], v34 offset:512\n" \
"v_accvgpr_read_b32 v215, a51\n" \
M_"v[156:159], a[184:187], a[152:155], v[156:159]\n" \
X_"a[232:233], v34 offset:4096\n" \
"v_accvgpr_read_b32 v180, a52\n" \
M_"v[156:159], a[248:251], a[216:219], v[156:159]\n" \
X_"a[234:235], v34 offset:4608\n" \
"v_accvgpr_read_b32 v181, a53\n" \
M_"v[160:163], a[188:191], a[152:155], v[160:163]\n" \
R_"v[248:251], v22 offset:0\n" \
"v_accvgpr_read_b32 v182, a54\n" \
M_"v[160:163], a[252:255], a[216:219], v[160:163]\n" \
R_"a[152:155], v28 offset:12288\n" \
"v_accvgpr_read_b32 v183, a55\n" \
M_"v[176:179], a[188:191], a[156:159], v[176:179]\n" \
R_"a[216:219], v29 offset:12288\n" \
C_"v212, v212, v213\n" \
M_"v[176:179], a[252:255], a[220:223], v[176:179]\n" \
R_"v[48:51], v23 offset:0\n" \
C_"v213, v214, v215\n" \
M_"v[172:175], a[184:187], a[156:159], v[172:175]\n" \
C_"v214, v180, v181\n" \
C_"v215, v182, v183\n" \
M_"v[172:175], a[248:251], a[220:223], v[172:175]\n" \
"v_accvgpr_read_b32 v216, a64\n" \
"v_accvgpr_read_b32 v217, a65\n" \
M_"v[168:171], a[180:183], a[156:159], v[168:171]\n" \
"v_accvgpr_read_b32 v218, a66\n" \
"v_accvgpr_read_b32 v219, a67\n" \
M_"v[168:171], a[244:247], a[220:223], v[168:171]\n" \
"v_accvgpr_read_b32 v180, a68\n" \
"v_accvgpr_read_b32 v181, a69\n" \
M_"v[164:167], a[176:179], a[156:159], v[164:167]\n" \
"v_accvgpr_read_b32 v182, a70\n" \
"v_accvgpr_read_b32 v183, a71\n" \
M_"v[164:167], a[240:243], a[220:223], v[164:167]\n" \
R_"a[156:159], v28 offset:14336\n" \
R_"a[220:223], v29 offset:14336\n" \
"s_waitcnt lgkmcnt(15)\n" \
M_"a[0:3], a[160:163], a[128:131], 0\n" \
"s_addk_i32 s84, 0x2000\n" \
C_"v216, v216, v217\n" \
M_"a[0:3], a[224:227], a[192:195], a[0:3]\n" \
B_"v24, s[76:79], s84 offen offset:2048 lds\n" \
C_"v217, v218, v219\n" \
M_"a[4:7], a[164:167], a[128:131], 0\n" \
X_"a[176:177], v36 offset:0\n" \
C_"v218, v180, v181\n" \
M_"a[4:7], a[228:231], a[192:195], a[4:7]\n" \
"s_addk_i32 s84, 0x2000\n" \
C_"v219, v182, v183\n" \
"s_waitcnt lgkmcnt(15)\n" \
M_"a[12:15], a[172:175], a[128:131], 0\n" \
B_"v25, s[76:79], s84 offen offset:3072 lds\n" \
"v_accvgpr_read_b32 v220, a80\n" \
"s_waitcnt lgkmcnt(11)\n" \
M_"a[12:15], a[236:239], a[192:195], a[12:15]\n" \
X_"a[178:179], v36 offset:1024\n" \
"v_accvgpr_read_b32 v221, a81\n" \
"s_waitcnt lgkmcnt(10)\n" \
M_"a[8:11], a[128:131], a[168:171], 0\n" \
X_"a[240:241], v36 offset:8192\n" \
"v_accvgpr_read_b32 v222, a82\n" \
"s_waitcnt lgkmcnt(9)\n" \
"s_nop 0\n" \
M_"a[8:11], a[192:195], a[232:235], a[8:11]\n" \
X_"a[242:243], v36 offset:9216\n" \
"v_accvgpr_read_b32 v223, a83\n" \
M_"a[24:27], a[132:135], a[168:171], 0\n" \
X_"a[180:181], v37 offset:0\n" \
"v_accvgpr_read_b32 v180, a84\n" \
M_"a[24:27], a[196:199], a[232:235], a[24:27]\n" \
X_"a[182:183], v37 offset:1024\n" \
"v_accvgpr_read_b32 v181, a85\n" \
M_"a[28:31], a[172:175], a[132:135], 0\n" \
X_"a[244:245], v37 offset:8192\n" \
"v_accvgpr_read_b32 v182, a86\n" \
M_"a[28:31], a[236:239], a[196:199], a[28:31]\n" \
"s_add_u32 s89, s41, 0x2000\n" \
"buffer_store_dwordx4 v[200:203], v20, s[80:83], s89 offen offset:0 sc1\n" \
M_"a[20:23], a[164:167], a[132:135], 0\n" \
X_"a[246:247], v37 offset:9216\n" \
"v_accvgpr_read_b32 v183, a87\n" \
M_"a[20:23], a[228:231], a[196:199], a[20:23]\n" \
X_"a[184:185], v38 offset:0\n" \
C_"v220, v220, v221\n" \
".fill (L_pb + 20192 - .) / 4, 4, 0xBF800000\n" \
"L_pp5:\n" \
"s_cbranch_execz L_pret\n" \
"s_nop 0\n" \
M_"a[16:19], a[160:163], a[132:135], 0\n" \
X_"a[186:187], v38 offset:1024\n" \
C_"v221, v222, v223\n" \
M_"a[16:19], a[224:227], a[196:199], a[16:19]\n" \
C_"v222, v180, v181\n" \
C_"v223, v182, v183\n" \
C_"v224, v52, v53\n" \
M_"a[32:35], a[160:163], a[136:139], 0\n" \
"s_add_u32 s88, s41, 0x2000\n" \
"buffer_store_dwordx4 v[204:207], v20, s[80:83], s88 offen offset:1024 sc1\n" \
M_"a[32:35], a[224:227], a[200:203], a[32:35]\n" \
X_"a[248:249], v38 offset:8192\n" \
C_"v225, v54, v55\n" \
M_"a[36:39], a[164:167], a[136:139], 0\n" \
X_"a[250:251], v38 offset:9216\n" \
C_"v226, v56, v57\n" \
M_"a[36:39], a[228:231], a[200:203], a[36:39]\n" \
X_"a[188:189], v39 offset:0\n" \
C_"v227, v58, v59\n" \
M_"a[44:47], a[172:175], a[136:139], 0\n" \
C_"v228, v60, v61\n" \
C_"v229, v62, v63\n" \
C_"v230, v64, v65\n" \
M_"a[44:47], a[236:239], a[200:203], a[44:47]\n" \
X_"a[190:191], v39 offset:1024\n" \
C_"v231, v66, v67\n" \
M_"a[40:43], a[136:139], a[168:171], 0\n" \
"s_add_u32 s89, s41, 0x3000\n" \
"buffer_store_dwordx4 v[208:211], v20, s[80:83], s89 offen offset:1024 sc1\n" \
M_"a[40:43], a[200:203], a[232:235], a[40:43]\n" \
X_"a[252:253], v39 offset:8192\n" \
C_"v232, v76, v77\n" \
M_"a[56:59], a[140:143], a[168:171], 0\n" \
X_"a[254:255], v39 offset:9216\n" \
C_"v233, v78, v79\n" \
M_"a[56:59], a[204:207], a[232:235], a[56:59]\n" \
C_"v234, v80, v81\n" \
C_"v235, v82, v83\n" \
C_"v236, v68, v69\n" \
M_"a[60:63], a[172:175], a[140:143], 0\n" \
C_"v237, v70, v71\n" \
C_"v238, v72, v73\n" \
C_"v239, v74, v75\n" \
M_"a[60:63], a[236:239], a[204:207], a[60:63]\n" \
"s_add_u32 s88, s41, 0x3000\n" \
"buffer_store_dwordx4 v[212:215], v20, s[80:83], s88 offen offset:0 sc1\n" \
M_"a[52:55], a[164:167], a[140:143], 0\n" \
C_"v240, v84, v85\n" \
C_"v241, v86, v87\n" \
C_"v242, v88, v89\n" \
M_"a[52:55], a[228:231], a[204:207], a[52:55]\n" \
C_"v243, v90, v91\n" \
C_"v244, v92, v93\n" \
C_"v245, v94, v95\n" \
M_"a[48:51], a[160:163], a[140:143], 0\n" \
C_"v246, v96, v97\n" \
C_"v247, v98, v99\n" \
C_"v184, v108, v109\n" \
M_"a[48:51], a[224:227], a[204:207], a[48:51]\n" \
C_"v185, v110, v111\n" \
C_"v186, v112, v113\n" \
C_"v187, v114, v115\n" \
"s_waitcnt lgkmcnt(15)\n" \
"s_nop 0\n" \
M_"a[64:67], a[164:167], v[248:251], 0\n" \
C_"v188, v100, v101\n" \
C_"v189, v102, v103\n" \
C_"v190, v104, v105\n" \
M_"a[64:67], a[228:231], v[48:51], a[64:67]\n" \
C_"v191, v106, v107\n" \
C_"v192, v116, v117\n" \
C_"v193, v118, v119\n" \
M_"a[80:83], a[172:175], v[248:251], 0\n" \
C_"v194, v120, v121\n" \
C_"v195, v122, v123\n" \
C_"v196, v124, v125\n" \
M_"a[80:83], a[236:239], v[48:51], a[80:83]\n" \
C_"v197, v126, v127\n" \
C_"v198, v128, v129\n" \
C_"v199, v130, v131\n" \
M_"a[68:71], v[248:251], a[168:171], 0\n" \
C_"v252, v140, v141\n" \
C_"v253, v142, v143\n" \
C_"v254, v144, v145\n" \
M_"a[68:71], v[48:51], a[232:235], a[68:71]\n" \
C_"v255, v146, v147\n" \
"v_accvgpr_write_b32 a84, v252\n" \
"v_accvgpr_write_b32 a85, v253\n" \
"s_waitcnt vmcnt(4) lgkmcnt(0)\n" \
"s_barrier\n" \
M_"v[52:55], a[176:179], a[128:131], 0\n" \
"s_add_u32 m0, s40, 0x0\n" \
"s_nop 0\n" \
"s_add_u32 s85, s39, 0x4000\n" \
"v_accvgpr_write_b32 a86, v254\n" \
M_"v[52:55], a[240:243], a[192:195], v[52:55]\n" \
B_"v20, s[80:83], s85 offen sc1 lds\n" \
"v_accvgpr_write_b32 a87, v255\n" \
M_"v[56:59], a[180:183], a[128:131], 0\n" \
X_"a[160:161], v32 offset:32768\n" \
C_"v252, v132, v133\n" \
M_"v[56:59], a[244:247], a[192:195], v[56:59]\n" \
X_"a[162:163], v32 offset:33280\n" \
C_"v253, v134, v135\n" \
M_"v[60:63], a[184:187], a[128:131], 0\n" \
"s_add_u32 m0, s40, 0x1000\n" \
"s_add_u32 s85, s39, 0x5000\n" \
C_"v254, v136, v137\n" \
M_"v[60:63], a[248:251], a[192:195], v[60:63]\n" \
B_"v20, s[80:83], s85 offen sc1 lds\n" \
C_"v255, v138, v139\n" \
M_"v[64:67], a[188:191], a[128:131], 0\n" \
X_"a[224:225], v32 offset:36864\n" \
C_"v200, v148, v149\n" \
M_"v[64:67], a[252:255], a[192:195], v[64:67]\n" \
R_"a[128:131], v28 offset:32768\n" \
C_"v201, v150, v151\n" \
M_"v[80:83], a[188:191], a[132:135], 0\n" \
R_"a[192:195], v29 offset:32768\n" \
C_"v202, v152, v153\n" \
M_"v[80:83], a[252:255], a[196:199], v[80:83]\n" \
"s_add_u32 m0, s40, 0x3000\n" \
"s_movk_i32 s84, 0x100\n" \
"s_nop 0\n" \
C_"v203, v154, v155\n" \
M_"v[76:79], a[184:187], a[132:135], 0\n" \
B_"v21, s[76:79], s84 offen offset:0 lds\n" \
C_"v204, v156, v157\n" \
M_"v[76:79], a[248:251], a[196:199], v[76:79]\n" \
X_"a[226:227], v32 offset:37376\n" \
C_"v205, v158, v159\n" \
M_"v[72:75], a[180:183], a[132:135], 0\n" \
X_"a[164:165], v33 offset:32768\n" \
C_"v206, v160, v161\n" \
M_"v[72:75], a[244:247], a[196:199], v[72:75]\n" \
C_"v207, v162, v163\n" \
C_"v208, v172, v173\n" \
C_"v209, v174, v175\n" \
M_"v[68:71], a[176:179], a[132:135], 0\n" \
X_"a[166:167], v33 offset:33280\n" \
C_"v210, v176, v177\n" \
M_"v[68:71], a[240:243], a[196:199], v[68:71]\n" \
R_"a[132:135], v28 offset:34816\n" \
C_"v211, v178, v179\n" \
M_"v[84:87], a[176:179], a[136:139], 0\n" \
R_"a[196:199], v29 offset:34816\n" \
C_"v212, v164, v165\n" \
M_"v[84:87], a[240:243], a[200:203], v[84:87]\n" \
"s_add_u32 m0, s31, 0x0\n" \
"s_mov_b32 s84, 0x100000\n" \
C_"v213, v166, v167\n" \
M_"v[88:91], a[180:183], a[136:139], 0\n" \
B_"v26, s[68:71], s84 offen offset:0 lds\n" \
C_"v214, v168, v169\n" \
M_"v[88:91], a[244:247], a[200:203], v[88:91]\n" \
"s_add_u32 s89, s41, 0x0\n" \
"buffer_store_dwordx4 v[224:227], v20, s[80:83], s89 offen offset:2048 sc1\n" \
M_"v[92:95], a[184:187], a[136:139], 0\n" \
X_"a[228:229], v33 offset:36864\n" \
C_"v215, v170, v171\n" \
M_"v[92:95], a[248:251], a[200:203], v[92:95]\n" \
X_"a[230:231], v33 offset:37376\n" \
M_"v[96:99], a[188:191], a[136:139], 0\n" \
M_"v[96:99], a[252:255], a[200:203], v[96:99]\n" \
R_"a[136:139], v28 offset:36864\n" \
M_"v[112:115], a[188:191], a[140:143], 0\n" \
R_"a[200:203], v29 offset:36864\n" \
M_"v[112:115], a[252:255], a[204:207], v[112:115]\n" \
"s_addk_i32 s84, 0x7c00\n" \
M_"v[108:111], a[184:187], a[140:143], 0\n" \
B_"v26, s[68:71], s84 offen offset:1024 lds\n" \
M_"v[108:111], a[248:251], a[204:207], v[108:111]\n" \
X_"a[172:173], v35 offset:32768\n" \
M_"v[104:107], a[180:183], a[140:143], 0\n" \
"s_addk_i32 s84, 0x7c00\n" \
M_"v[104:107], a[244:247], a[204:207], v[104:107]\n" \
B_"v27, s[68:71], s84 offen offset:2048 lds\n" \
M_"v[100:103], a[176:179], a[140:143], 0\n" \
X_"a[174:175], v35 offset:33280\n" \
M_"v[100:103], a[240:243], a[204:207], v[100:103]\n" \
R_"a[140:143], v28 offset:38912\n" \
M_"v[116:119], a[176:179], a[144:147], 0\n" \
R_"a[204:207], v29 offset:38912\n" \
M_"v[116:119], a[240:243], a[208:211], v[116:119]\n" \
"s_addk_i32 s84, 0x7c00\n" \
M_"v[120:123], a[180:183], a[144:147], 0\n" \
B_"v27, s[68:71], s84 offen offset:3072 lds\n" \
M_"v[120:123], a[244:247], a[208:211], v[120:123]\n" \
X_"a[236:237], v35 offset:36864\n" \
M_"v[124:127], a[184:187], a[144:147], 0\n" \
X_"a[238:239], v35 offset:37376\n" \
M_"v[124:127], a[248:251], a[208:211], v[124:127]\n" \
"s_add_u32 m0, s30, 0x0\n" \
"s_movk_i32 s84, 0x180\n" \
M_"v[128:131], a[188:191], a[144:147], 0\n" \
B_"v24, s[76:79], s84 offen offset:0 lds\n" \
M_"v[128:131], a[252:255], a[208:211], v[128:131]\n" \
R_"a[144:147], v28 offset:40960\n" \
M_"v[144:147], a[188:191], a[148:151], 0\n" \
R_"a[208:211], v29 offset:40960\n" \
M_"v[144:147], a[252:255], a[212:215], v[144:147]\n" \
X_"a[168:169], v34 offset:32768\n" \
M_"v[140:143], a[184:187], a[148:151], 0\n" \
"s_movk_i32 s84, 0x2180\n" \
M_"v[140:143], a[248:251], a[212:215], v[140:143]\n" \
B_"v25, s[76:79], s84 offen offset:1024 lds\n" \
M_"v[136:139], a[180:183], a[148:151], 0\n" \
X_"a[170:171], v34 offset:33280\n" \
M_"v[136:139], a[244:247], a[212:215], v[136:139]\n" \
X_"a[232:233], v34 offset:36864\n" \
M_"v[132:135], a[176:179], a[148:151], 0\n" \
X_"a[234:235], v34 offset:37376\n" \
M_"v[132:135], a[240:243], a[212:215], v[132:135]\n" \
R_"a[148:151], v28 offset:43008\n" \
M_"v[148:151], a[176:179], a[152:155], 0\n" \
R_"a[212:215], v29 offset:43008\n" \
M_"v[148:151], a[240:243], a[216:219], v[148:151]\n" \
"s_movk_i32 s84, 0x4180\n" \
M_"v[152:155], a[180:183], a[152:155], 0\n" \
B_"v24, s[76:79], s84 offen offset:2048 lds\n" \
M_"v[152:155], a[244:247], a[216:219], v[152:155]\n" \
"s_movk_i32 s84, 0x6180\n" \
M_"v[156:159], a[184:187], a[152:155], 0\n" \
B_"v25, s[76:79], s84 offen offset:3072 lds\n" \
M_"v[156:159], a[248:251], a[216:219], v[156:159]\n" \
R_"v[248:251], v22 offset:32768\n" \
M_"v[160:163], a[188:191], a[152:155], 0\n" \
R_"v[48:51], v23 offset:32768\n" \
M_"v[160:163], a[252:255], a[216:219], v[160:163]\n" \
R_"a[152:155], v28 offset:45056\n" \
M_"v[176:179], a[188:191], a[156:159], 0\n" \
R_"a[216:219], v29 offset:45056\n" \
M_"v[176:179], a[252:255], a[220:223], v[176:179]\n" \
M_"v[172:175], a[184:187], a[156:159], 0\n" \
M_"v[172:175], a[248:251], a[220:223], v[172:175]\n" \
M_"v[168:171], a[180:183], a[156:159], 0\n" \
M_"v[168:171], a[244:247], a[220:223], v[168:171]\n" \
M_"v[164:167], a[176:179], a[156:159], 0\n" \
M_"v[164:167], a[240:243], a[220:223], v[164:167]\n" \
R_"a[156:159], v28 offset:47104\n" \
R_"a[220:223], v29 offset:47104\n" \
"s_waitcnt lgkmcnt(15)\n" \
M_"a[0:3], a[160:163], a[128:131], a[0:3]\n" \
"s_add_u32 m0, s30, 0x1000\n" \
"s_addk_i32 s84, 0x3000\n" \
M_"a[0:3], a[224:227], a[192:195], a[0:3]\n" \
B_"v24, s[76:79], s84 offen offset:0 lds\n" \
M_"a[4:7], a[164:167], a[128:131], a[4:7]\n" \
X_"a[176:177], v36 offset:32768\n" \
"v_mov_b32_e64 v11, 0\n" \
M_"a[4:7], a[228:231], a[192:195], a[4:7]\n" \
X_"a[178:179], v36 offset:33792\n" \
"s_and_b32 s5, s2, 7\n" \
M_"a[12:15], a[172:175], a[128:131], a[12:15]\n" \
X_"a[240:241], v36 offset:40960\n" \
"s_lshl_b32 s6, s3, 1\n" \
"s_waitcnt lgkmcnt(15)\n" \
M_"a[12:15], a[236:239], a[192:195], a[12:15]\n" \
"s_add_u32 s88, s41, 0x0\n" \
"buffer_store_dwordx4 v[228:231], v20, s[80:83], s88 offen offset:3072 sc1\n" \
"s_waitcnt lgkmcnt(13)\n" \
M_"a[8:11], a[128:131], a[168:171], a[8:11]\n" \
X_"a[242:243], v36 offset:41984\n" \
"s_lshl_b32 s4, s5, 2\n" \
"s_waitcnt lgkmcnt(12)\n" \
M_"a[8:11], a[192:195], a[232:235], a[8:11]\n" \
X_"a[180:181], v37 offset:32768\n" \
"s_bfe_u32 s7, s2, 0x20003\n" \
M_"a[24:27], a[132:135], a[168:171], a[24:27]\n" \
X_"a[182:183], v37 offset:33792\n" \
"s_or_b32 s12, s4, s7\n" \
M_"a[24:27], a[196:199], a[232:235], a[24:27]\n" \
X_"a[244:245], v37 offset:40960\n" \
"s_and_b32 s6, s6, 4\n" \
M_"a[28:31], a[172:175], a[132:135], a[28:31]\n" \
"s_and_b32 s8, s3, 1\n" \
"s_or_b32 s8, s6, s8\n" \
M_"a[28:31], a[236:239], a[196:199], a[28:31]\n" \
"s_addk_i32 s84, 0x2000\n" \
M_"a[20:23], a[164:167], a[132:135], a[20:23]\n" \
B_"v25, s[76:79], s84 offen offset:1024 lds\n" \
M_"a[20:23], a[228:231], a[196:199], a[20:23]\n" \
X_"a[246:247], v37 offset:41984\n" \
"s_lshl_b32 s6, s12, 4\n" \
M_"a[16:19], a[160:163], a[132:135], a[16:19]\n" \
X_"a[184:185], v38 offset:32768\n" \
"s_lshl_b32 s9, s3, 2\n" \
M_"a[16:19], a[224:227], a[196:199], a[16:19]\n" \
X_"a[186:187], v38 offset:33792\n" \
"s_or_b32 s6, s6, s9\n" \
M_"a[32:35], a[160:163], a[136:139], a[32:35]\n" \
"s_addk_i32 s84, 0x2000\n" \
M_"a[32:35], a[224:227], a[200:203], a[32:35]\n" \
B_"v24, s[76:79], s84 offen offset:2048 lds\n" \
M_"a[36:39], a[164:167], a[136:139], a[36:39]\n" \
X_"a[248:249], v38 offset:40960\n" \
"s_lshr_b32 s9, s2, 5\n" \
M_"a[36:39], a[228:231], a[200:203], a[36:39]\n" \
X_"a[250:251], v38 offset:41984\n" \
"s_mul_i32 s10, s9, 0xa00\n" \
M_"a[44:47], a[172:175], a[136:139], a[44:47]\n" \
X_"a[188:189], v39 offset:32768\n" \
"s_add_i32 s10, s10, 0x5bc1000\n" \
M_"a[44:47], a[236:239], a[200:203], a[44:47]\n" \
X_"a[190:191], v39 offset:33792\n" \
"s_or_b32 s6, s6, s10\n" \
M_"a[40:43], a[136:139], a[168:171], a[40:43]\n" \
"s_add_u32 s89, s41, 0x1000\n" \
"buffer_store_dwordx4 v[232:235], v20, s[80:83], s89 offen offset:3072 sc1\n" \
M_"a[40:43], a[200:203], a[232:235], a[40:43]\n" \
X_"a[252:253], v39 offset:40960\n" \
"s_lshl_b32 s13, s9, 3\n" \
M_"a[56:59], a[140:143], a[168:171], a[56:59]\n" \
X_"a[254:255], v39 offset:41984\n" \
"v_writelane_b32 v11, s6, 0\n" \
M_"a[56:59], a[204:207], a[232:235], a[56:59]\n" \
"s_or_b32 s6, s13, s5\n" \
"v_writelane_b32 v11, s10, 1\n" \
M_"a[60:63], a[172:175], a[140:143], a[60:63]\n" \
"s_lshl_b32 s6, s6, 10\n" \
"s_lshl_b32 s10, s3, 8\n" \
M_"a[60:63], a[236:239], a[204:207], a[60:63]\n" \
"s_addk_i32 s84, 0x2000\n" \
M_"a[52:55], a[164:167], a[140:143], a[52:55]\n" \
B_"v25, s[76:79], s84 offen offset:3072 lds\n" \
M_"a[52:55], a[228:231], a[204:207], a[52:55]\n" \
"s_or_b32 s6, s6, s10\n" \
"s_or_b32 s10, s6, 0x5c00200\n" \
M_"a[48:51], a[160:163], a[140:143], a[48:51]\n" \
"v_writelane_b32 v11, s10, 2\n" \
"s_lshl_b32 s10, s7, 2\n" \
M_"a[48:51], a[224:227], a[204:207], a[48:51]\n" \
"s_or_b32 s11, s10, s6\n" \
"s_or_b32 s11, s11, 0x5c00000\n" \
"s_waitcnt lgkmcnt(15)\n" \
M_"a[64:67], a[164:167], v[248:251], a[64:67]\n" \
"v_writelane_b32 v11, s11, 3\n" \
"s_or_b32 s6, s6, 0x5c00080\n" \
M_"a[64:67], a[228:231], v[48:51], a[64:67]\n" \
"v_writelane_b32 v11, s6, 4\n" \
"s_or_b32 s6, s6, s10\n" \
M_"a[80:83], a[172:175], v[248:251], a[80:83]\n" \
"s_add_i32 s10, s3, s2\n" \
"v_writelane_b32 v11, s6, 5\n" \
M_"a[80:83], a[236:239], v[48:51], a[80:83]\n" \
"s_mov_b32 s6, 2.0\n" \
"s_mov_b32 s11, 0x5bc0000\n" \
M_"a[68:71], v[248:251], a[168:171], a[68:71]\n" \
"s_cmp_lg_u32 s10, 0\n" \
"s_cselect_b32 s10, s6, s11\n" \
"s_waitcnt vmcnt(10) lgkmcnt(0)\n" \
M_"a[68:71], v[48:51], a[232:235], a[68:71]\n" \
"s_barrier\n" \
"v_xor_b32_e32 v28, 0x20000, v28\n" \
"v_xor_b32_e32 v29, 0x20000, v29\n" \
M_"v[52:55], a[176:179], a[128:131], v[52:55]\n" \
"s_add_u32 m0, s40, 0x8000\n" \
"s_add_u32 s85, s39, 0x6000\n" \
M_"v[52:55], a[240:243], a[192:195], v[52:55]\n" \
B_"v20, s[80:83], s85 offen sc1 lds\n" \
M_"v[56:59], a[180:183], a[128:131], v[56:59]\n" \
X_"a[160:161], v32 offset:0\n" \
"s_lshr_b32 s14, s2, 3\n" \
M_"v[56:59], a[244:247], a[192:195], v[56:59]\n" \
X_"a[162:163], v32 offset:512\n" \
"v_writelane_b32 v11, s10, 57\n" \
M_"v[60:63], a[184:187], a[128:131], v[60:63]\n" \
"s_add_u32 m0, s40, 0x9000\n" \
"s_add_u32 s85, s39, 0x7000\n" \
M_"v[60:63], a[248:251], a[192:195], v[60:63]\n" \
B_"v20, s[80:83], s85 offen sc1 lds\n" \
M_"v[64:67], a[188:191], a[128:131], v[64:67]\n" \
X_"a[224:225], v32 offset:4096\n" \
"s_lshl_b32 s10, s5, 5\n" \
M_"v[64:67], a[252:255], a[192:195], v[64:67]\n" \
R_"a[128:131], v28 offset:0\n" \
"s_min_u32 s15, s3, 2\n" \
"s_nop 0\n" \
M_"v[80:83], a[188:191], a[132:135], v[80:83]\n" \
R_"a[192:195], v29 offset:0\n" \
"s_mul_i32 s11, s7, 0x120\n" \
M_"v[80:83], a[252:255], a[196:199], v[80:83]\n" \
"s_add_u32 s88, s41, 0x1000\n" \
"buffer_store_dwordx4 v[236:239], v20, s[80:83], s88 offen offset:2048 sc1\n" \
M_"v[76:79], a[184:187], a[132:135], v[76:79]\n" \
X_"a[226:227], v32 offset:4608\n" \
M_"v[76:79], a[248:251], a[196:199], v[76:79]\n" \
X_"a[164:165], v33 offset:0\n" \
M_"v[72:75], a[180:183], a[132:135], v[72:75]\n" \
"s_add_u32 m0, s40, 0xb000\n" \
"s_movk_i32 s84, 0x180\n" \
"s_nop 0\n" \
M_"v[72:75], a[244:247], a[196:199], v[72:75]\n" \
B_"v21, s[76:79], s84 offen offset:0 lds\n" \
M_"v[68:71], a[176:179], a[132:135], v[68:71]\n" \
X_"a[166:167], v33 offset:512\n" \
M_"v[68:71], a[240:243], a[196:199], v[68:71]\n" \
R_"a[132:135], v28 offset:2048\n" \
M_"v[84:87], a[176:179], a[136:139], v[84:87]\n" \
R_"a[196:199], v29 offset:2048\n" \
M_"v[84:87], a[240:243], a[200:203], v[84:87]\n" \
"s_add_u32 m0, s31, 0x8000\n" \
"s_mov_b32 s84, 0x180000\n" \
M_"v[88:91], a[180:183], a[136:139], v[88:91]\n" \
B_"v26, s[68:71], s84 offen offset:0 lds\n" \
M_"v[88:91], a[244:247], a[200:203], v[88:91]\n" \
X_"a[228:229], v33 offset:4096\n" \
M_"v[92:95], a[184:187], a[136:139], v[92:95]\n" \
X_"a[230:231], v33 offset:4608\n" \
M_"v[92:95], a[248:251], a[200:203], v[92:95]\n" \
"s_addk_i32 s84, 0x7c00\n" \
M_"v[96:99], a[188:191], a[136:139], v[96:99]\n" \
B_"v26, s[68:71], s84 offen offset:1024 lds\n" \
M_"v[96:99], a[252:255], a[200:203], v[96:99]\n" \
R_"a[136:139], v28 offset:4096\n" \
M_"v[112:115], a[188:191], a[140:143], v[112:115]\n" \
R_"a[200:203], v29 offset:4096\n" \
M_"v[112:115], a[252:255], a[204:207], v[112:115]\n" \
"s_addk_i32 s84, 0x7c00\n" \
M_"v[108:111], a[184:187], a[140:143], v[108:111]\n" \
B_"v27, s[68:71], s84 offen offset:2048 lds\n" \
M_"v[108:111], a[248:251], a[204:207], v[108:111]\n" \
X_"a[172:173], v35 offset:0\n" \
M_"v[104:107], a[180:183], a[140:143], v[104:107]\n" \
X_"a[174:175], v35 offset:512\n" \
M_"v[104:107], a[244:247], a[204:207], v[104:107]\n" \
X_"a[236:237], v35 offset:4096\n" \
M_"v[100:103], a[176:179], a[140:143], v[100:103]\n" \
"s_add_u32 s16, s22, s11\n" \
"s_addc_u32 s17, s23, 0\n" \
M_"v[100:103], a[240:243], a[204:207], v[100:103]\n" \
R_"a[140:143], v28 offset:6144\n" \
M_"v[116:119], a[176:179], a[144:147], v[116:119]\n" \
R_"a[204:207], v29 offset:6144\n" \
M_"v[116:119], a[240:243], a[208:211], v[116:119]\n" \
"s_addk_i32 s84, 0x7c00\n" \
M_"v[120:123], a[180:183], a[144:147], v[120:123]\n" \
B_"v27, s[68:71], s84 offen offset:3072 lds\n" \
M_"v[120:123], a[244:247], a[208:211], v[120:123]\n" \
"s_add_u32 m0, s30, 0x8000\n" \
"s_movk_i32 s84, 0x200\n" \
M_"v[124:127], a[184:187], a[144:147], v[124:127]\n" \
B_"v24, s[76:79], s84 offen offset:0 lds\n" \
M_"v[124:127], a[248:251], a[208:211], v[124:127]\n" \
X_"a[238:239], v35 offset:4608\n" \
M_"v[128:131], a[188:191], a[144:147], v[128:131]\n" \
"s_add_u32 s89, s41, 0x2000\n" \
"buffer_store_dwordx4 v[240:243], v20, s[80:83], s89 offen offset:2048 sc1\n" \
M_"v[128:131], a[252:255], a[208:211], v[128:131]\n" \
R_"a[144:147], v28 offset:8192\n" \
M_"v[144:147], a[188:191], a[148:151], v[144:147]\n" \
R_"a[208:211], v29 offset:8192\n" \
M_"v[144:147], a[252:255], a[212:215], v[144:147]\n" \
X_"a[168:169], v34 offset:0\n" \
M_"v[140:143], a[184:187], a[148:151], v[140:143]\n" \
X_"a[170:171], v34 offset:512\n" \
M_"v[140:143], a[248:251], a[212:215], v[140:143]\n" \
"s_movk_i32 s84, 0x2200\n" \
"s_nop 0\n" \
M_"v[136:139], a[180:183], a[148:151], v[136:139]\n" \
B_"v25, s[76:79], s84 offen offset:1024 lds\n" \
M_"v[136:139], a[244:247], a[212:215], v[136:139]\n" \
X_"a[232:233], v34 offset:4096\n" \
M_"v[132:135], a[176:179], a[148:151], v[132:135]\n" \
X_"a[234:235], v34 offset:4608\n" \
M_"v[132:135], a[240:243], a[212:215], v[132:135]\n" \
R_"a[148:151], v28 offset:10240\n" \
M_"v[148:151], a[176:179], a[152:155], v[148:151]\n" \
R_"a[212:215], v29 offset:10240\n" \
M_"v[148:151], a[240:243], a[216:219], v[148:151]\n" \
"s_movk_i32 s84, 0x4200\n" \
M_"v[152:155], a[180:183], a[152:155], v[152:155]\n" \
B_"v24, s[76:79], s84 offen offset:2048 lds\n" \
M_"v[152:155], a[244:247], a[216:219], v[152:155]\n" \
"s_movk_i32 s84, 0x6200\n" \
M_"v[156:159], a[184:187], a[152:155], v[156:159]\n" \
B_"v25, s[76:79], s84 offen offset:3072 lds\n" \
M_"v[156:159], a[248:251], a[216:219], v[156:159]\n" \
R_"v[248:251], v22 offset:0\n" \
M_"v[160:163], a[188:191], a[152:155], v[160:163]\n" \
R_"v[48:51], v23 offset:0\n" \
M_"v[160:163], a[252:255], a[216:219], v[160:163]\n" \
R_"a[152:155], v28 offset:12288\n" \
M_"v[176:179], a[188:191], a[156:159], v[176:179]\n" \
R_"a[216:219], v29 offset:12288\n" \
M_"v[176:179], a[252:255], a[220:223], v[176:179]\n" \
"s_add_u32 s18, s24, s11\n" \
"v_writelane_b32 v11, s16, 9\n" \
"s_addc_u32 s19, s25, 0\n" \
M_"v[172:175], a[184:187], a[156:159], v[172:175]\n" \
"s_and_b32 s20, s17, 0xffff\n" \
"v_writelane_b32 v11, s20, 10\n" \
M_"v[172:175], a[248:251], a[220:223], v[172:175]\n" \
"s_and_b32 s20, s19, 0xffff\n" \
"v_writelane_b32 v11, s18, 11\n" \
M_"v[168:171], a[180:183], a[156:159], v[168:171]\n" \
"s_cmpk_lt_u32 s3, 3\n" \
"s_cselect_b32 s16, s16, s18\n" \
M_"v[168:171], a[244:247], a[220:223], v[168:171]\n" \
"s_movk_i32 s18, 0xfff\n" \
"v_writelane_b32 v11, s20, 12\n" \
M_"v[164:167], a[176:179], a[156:159], v[164:167]\n" \
"s_and_b32 s20, s29, 0xffff\n" \
"v_writelane_b32 v11, s28, 13\n" \
M_"v[164:167], a[240:243], a[220:223], v[164:167]\n" \
R_"a[156:159], v28 offset:14336\n" \
R_"a[220:223], v29 offset:14336\n" \
"s_waitcnt lgkmcnt(15)\n" \
M_"a[0:3], a[160:163], a[128:131], a[0:3]\n" \
"s_add_u32 m0, s30, 0x9000\n" \
"s_addk_i32 s84, 0x3000\n" \
M_"a[0:3], a[224:227], a[192:195], a[0:3]\n" \
B_"v24, s[76:79], s84 offen offset:0 lds\n" \
M_"a[4:7], a[164:167], a[128:131], a[4:7]\n" \
X_"a[176:177], v36 offset:0\n" \
"v_writelane_b32 v11, s20, 14\n" \
M_"a[4:7], a[228:231], a[192:195], a[4:7]\n" \
X_"a[178:179], v36 offset:1024\n" \
"v_writelane_b32 v11, s16, 15\n" \
M_"a[12:15], a[172:175], a[128:131], a[12:15]\n" \
X_"a[240:241], v36 offset:8192\n" \
"s_waitcnt lgkmcnt(15)\n" \
M_"a[12:15], a[236:239], a[192:195], a[12:15]\n" \
"s_add_u32 s88, s41, 0x2000\n" \
"buffer_store_dwordx4 v[244:247], v20, s[80:83], s88 offen offset:3072 sc1\n" \
"s_waitcnt lgkmcnt(13)\n" \
M_"a[8:11], a[128:131], a[168:171], a[8:11]\n" \
X_"a[242:243], v36 offset:9216\n" \
"s_waitcnt lgkmcnt(12)\n" \
M_"a[8:11], a[192:195], a[232:235], a[8:11]\n" \
X_"a[180:181], v37 offset:0\n" \
M_"a[24:27], a[132:135], a[168:171], a[24:27]\n" \
X_"a[182:183], v37 offset:1024\n" \
M_"a[24:27], a[196:199], a[232:235], a[24:27]\n" \
X_"a[244:245], v37 offset:8192\n" \
M_"a[28:31], a[172:175], a[132:135], a[28:31]\n" \
"s_cmpk_lt_u32 s3, 3\n" \
"s_cselect_b32 s16, s17, s19\n" \
M_"a[28:31], a[236:239], a[196:199], a[28:31]\n" \
"s_addk_i32 s84, 0x2000\n" \
"s_nop 0\n" \
M_"a[20:23], a[164:167], a[132:135], a[20:23]\n" \
B_"v25, s[76:79], s84 offen offset:1024 lds\n" \
M_"a[20:23], a[228:231], a[196:199], a[20:23]\n" \
X_"a[246:247], v37 offset:9216\n" \
"s_and_b32 s16, s16, 0xffff\n" \
M_"a[16:19], a[160:163], a[132:135], a[16:19]\n" \
X_"a[184:185], v38 offset:0\n" \
"v_writelane_b32 v11, s16, 16\n" \
M_"a[16:19], a[224:227], a[196:199], a[16:19]\n" \
X_"a[186:187], v38 offset:1024\n" \
"s_mul_i32 s16, s5, 0x48\n" \
M_"a[32:35], a[160:163], a[136:139], a[32:35]\n" \
".fill (L_pb + 24288 - .) / 4, 4, 0xBF800000\n" \
"L_pp6:\n" \
"s_cbranch_execz L_pret\n" \
"s_addk_i32 s84, 0x2000\n" \
M_"a[32:35], a[224:227], a[200:203], a[32:35]\n" \
B_"v24, s[76:79], s84 offen offset:2048 lds\n" \
M_"a[36:39], a[164:167], a[136:139], a[36:39]\n" \
X_"a[248:249], v38 offset:8192\n" \
"s_add_i32 s17, s16, 0x47\n" \
M_"a[36:39], a[228:231], a[200:203], a[36:39]\n" \
X_"a[250:251], v38 offset:9216\n" \
M_"a[44:47], a[172:175], a[136:139], a[44:47]\n" \
X_"a[188:189], v39 offset:0\n" \
M_"a[44:47], a[236:239], a[200:203], a[44:47]\n" \
X_"a[190:191], v39 offset:1024\n" \
M_"a[40:43], a[136:139], a[168:171], a[40:43]\n" \
"s_add_u32 s89, s41, 0x3000\n" \
"buffer_store_dwordx4 v[184:187], v20, s[80:83], s89 offen offset:3072 sc1\n" \
M_"a[40:43], a[200:203], a[232:235], a[40:43]\n" \
X_"a[252:253], v39 offset:8192\n" \
M_"a[56:59], a[140:143], a[168:171], a[56:59]\n" \
X_"a[254:255], v39 offset:9216\n" \
M_"a[56:59], a[204:207], a[232:235], a[56:59]\n" \
"s_cmpk_lt_u32 s3, 3\n" \
"s_cselect_b32 s17, s17, s18\n" \
M_"a[60:63], a[172:175], a[140:143], a[60:63]\n" \
"s_mulk_i32 s17, 0x480\n" \
"s_addk_i32 s17, 0x120\n" \
M_"a[60:63], a[236:239], a[204:207], a[60:63]\n" \
"s_addk_i32 s84, 0x2000\n" \
M_"a[52:55], a[164:167], a[140:143], a[52:55]\n" \
B_"v25, s[76:79], s84 offen offset:3072 lds\n" \
M_"a[52:55], a[228:231], a[204:207], a[52:55]\n" \
"v_writelane_b32 v11, s17, 17\n" \
"s_lshl_b32 s17, s3, 5\n" \
M_"a[48:51], a[160:163], a[140:143], a[48:51]\n" \
"s_add_i32 s16, s16, s17\n" \
"s_mulk_i32 s16, 0x480\n" \
M_"a[48:51], a[224:227], a[204:207], a[48:51]\n" \
"s_mul_i32 s17, s12, 0x24000\n" \
"s_add_i32 s18, s17, 0x1b000\n" \
"s_waitcnt lgkmcnt(15)\n" \
M_"a[64:67], a[164:167], v[248:251], a[64:67]\n" \
"s_cmpk_lt_u32 s3, 3\n" \
"s_cselect_b32 s16, s16, s18\n" \
M_"a[64:67], a[228:231], v[48:51], a[64:67]\n" \
"s_xor_b32 s18, s3, 2\n" \
"v_writelane_b32 v11, s16, 18\n" \
M_"a[80:83], a[172:175], v[248:251], a[80:83]\n" \
"s_mul_i32 s16, s15, 0x2400\n" \
"s_mul_i32 s15, s15, 0x9000\n" \
M_"a[80:83], a[236:239], v[48:51], a[80:83]\n" \
"s_bitset1_b32 s16, 17\n" \
"v_writelane_b32 v11, s16, 22\n" \
M_"a[68:71], v[248:251], a[168:171], a[68:71]\n" \
"s_add_i32 s17, s17, s15\n" \
"s_mul_i32 s15, s3, 0x2400\n" \
"s_waitcnt vmcnt(11) lgkmcnt(0)\n" \
M_"a[68:71], v[48:51], a[232:235], a[68:71]\n" \
"s_barrier\n" \
"v_xor_b32_e32 v28, 0x20000, v28\n" \
"v_xor_b32_e32 v29, 0x20000, v29\n" \
M_"v[52:55], a[176:179], a[128:131], v[52:55]\n" \
"s_add_u32 m0, s40, 0x0\n" \
"s_nop 0\n" \
"s_add_u32 s85, s39, 0x8000\n" \
M_"v[52:55], a[240:243], a[192:195], v[52:55]\n" \
B_"v20, s[80:83], s85 offen sc1 lds\n" \
M_"v[56:59], a[180:183], a[128:131], v[56:59]\n" \
X_"a[160:161], v32 offset:32768\n" \
"v_writelane_b32 v11, s17, 29\n" \
M_"v[56:59], a[244:247], a[192:195], v[56:59]\n" \
X_"a[162:163], v32 offset:33280\n" \
"s_add_i32 s16, s15, 0xa400\n" \
M_"v[60:63], a[184:187], a[128:131], v[60:63]\n" \
"s_add_u32 m0, s40, 0x1000\n" \
"s_add_u32 s85, s39, 0x9000\n" \
M_"v[60:63], a[248:251], a[192:195], v[60:63]\n" \
B_"v20, s[80:83], s85 offen sc1 lds\n" \
M_"v[64:67], a[188:191], a[128:131], v[64:67]\n" \
X_"a[224:225], v32 offset:36864\n" \
"s_mov_b32 s17, 0x8000\n" \
M_"v[64:67], a[252:255], a[192:195], v[64:67]\n" \
R_"a[128:131], v28 offset:0\n" \
M_"v[80:83], a[188:191], a[132:135], v[80:83]\n" \
R_"a[192:195], v29 offset:0\n" \
M_"v[80:83], a[252:255], a[196:199], v[80:83]\n" \
"s_add_u32 s88, s41, 0x3000\n" \
"buffer_store_dwordx4 v[188:191], v20, s[80:83], s88 offen offset:2048 sc1\n" \
M_"v[76:79], a[184:187], a[132:135], v[76:79]\n" \
X_"a[226:227], v32 offset:37376\n" \
M_"v[76:79], a[248:251], a[196:199], v[76:79]\n" \
X_"a[164:165], v33 offset:32768\n" \
M_"v[72:75], a[180:183], a[132:135], v[72:75]\n" \
"s_add_u32 m0, s40, 0x3000\n" \
"s_movk_i32 s84, 0x200\n" \
M_"v[72:75], a[244:247], a[196:199], v[72:75]\n" \
B_"v21, s[76:79], s84 offen offset:0 lds\n" \
M_"v[68:71], a[176:179], a[132:135], v[68:71]\n" \
X_"a[166:167], v33 offset:33280\n" \
M_"v[68:71], a[240:243], a[196:199], v[68:71]\n" \
R_"a[132:135], v28 offset:2048\n" \
M_"v[84:87], a[176:179], a[136:139], v[84:87]\n" \
R_"a[196:199], v29 offset:2048\n" \
M_"v[84:87], a[240:243], a[200:203], v[84:87]\n" \
"s_add_u32 m0, s31, 0x0\n" \
"s_mov_b32 s84, 0x200000\n" \
M_"v[88:91], a[180:183], a[136:139], v[88:91]\n" \
B_"v26, s[68:71], s84 offen offset:0 lds\n" \
M_"v[88:91], a[244:247], a[200:203], v[88:91]\n" \
X_"a[228:229], v33 offset:36864\n" \
M_"v[92:95], a[184:187], a[136:139], v[92:95]\n" \
X_"a[230:231], v33 offset:37376\n" \
M_"v[92:95], a[248:251], a[200:203], v[92:95]\n" \
"s_addk_i32 s84, 0x7c00\n" \
M_"v[96:99], a[188:191], a[136:139], v[96:99]\n" \
B_"v26, s[68:71], s84 offen offset:1024 lds\n" \
M_"v[96:99], a[252:255], a[200:203], v[96:99]\n" \
R_"a[136:139], v28 offset:4096\n" \
M_"v[112:115], a[188:191], a[140:143], v[112:115]\n" \
R_"a[200:203], v29 offset:4096\n" \
M_"v[112:115], a[252:255], a[204:207], v[112:115]\n" \
"s_addk_i32 s84, 0x7c00\n" \
M_"v[108:111], a[184:187], a[140:143], v[108:111]\n" \
B_"v27, s[68:71], s84 offen offset:2048 lds\n" \
M_"v[108:111], a[248:251], a[204:207], v[108:111]\n" \
X_"a[172:173], v35 offset:32768\n" \
M_"v[104:107], a[180:183], a[140:143], v[104:107]\n" \
X_"a[174:175], v35 offset:33280\n" \
M_"v[104:107], a[244:247], a[204:207], v[104:107]\n" \
X_"a[236:237], v35 offset:36864\n" \
M_"v[100:103], a[176:179], a[140:143], v[100:103]\n" \
"s_cmpk_lt_u32 s3, 3\n" \
"s_cselect_b32 s16, s16, s17\n" \
M_"v[100:103], a[240:243], a[204:207], v[100:103]\n" \
R_"a[140:143], v28 offset:6144\n" \
"s_mov_b32 s17, 0x1b000\n" \
M_"v[116:119], a[176:179], a[144:147], v[116:119]\n" \
R_"a[204:207], v29 offset:6144\n" \
M_"v[116:119], a[240:243], a[208:211], v[116:119]\n" \
"s_addk_i32 s84, 0x7c00\n" \
M_"v[120:123], a[180:183], a[144:147], v[120:123]\n" \
B_"v27, s[68:71], s84 offen offset:3072 lds\n" \
M_"v[120:123], a[244:247], a[208:211], v[120:123]\n" \
"s_add_u32 m0, s30, 0x20000\n" \
"s_movk_i32 s84, 0x280\n" \
M_"v[124:127], a[184:187], a[144:147], v[124:127]\n" \
B_"v24, s[76:79], s84 offen offset:0 lds\n" \
M_"v[124:127], a[248:251], a[208:211], v[124:127]\n" \
X_"a[238:239], v35 offset:37376\n" \
M_"v[128:131], a[188:191], a[144:147], v[128:131]\n" \
"s_add_u32 s89, s42, 0x0\n" \
"buffer_store_dwordx4 v[216:219], v20, s[80:83], s89 offen offset:0 sc1\n" \
M_"v[128:131], a[252:255], a[208:211], v[128:131]\n" \
R_"a[144:147], v28 offset:8192\n" \
M_"v[144:147], a[188:191], a[148:151], v[144:147]\n" \
R_"a[208:211], v29 offset:8192\n" \
M_"v[144:147], a[252:255], a[212:215], v[144:147]\n" \
X_"a[168:169], v34 offset:32768\n" \
M_"v[140:143], a[184:187], a[148:151], v[140:143]\n" \
X_"a[170:171], v34 offset:33280\n" \
M_"v[140:143], a[248:251], a[212:215], v[140:143]\n" \
"s_movk_i32 s84, 0x2280\n" \
M_"v[136:139], a[180:183], a[148:151], v[136:139]\n" \
B_"v25, s[76:79], s84 offen offset:1024 lds\n" \
M_"v[136:139], a[244:247], a[212:215], v[136:139]\n" \
X_"a[232:233], v34 offset:36864\n" \
M_"v[132:135], a[176:179], a[148:151], v[132:135]\n" \
X_"a[234:235], v34 offset:37376\n" \
M_"v[132:135], a[240:243], a[212:215], v[132:135]\n" \
R_"a[148:151], v28 offset:10240\n" \
M_"v[148:151], a[176:179], a[152:155], v[148:151]\n" \
R_"a[212:215], v29 offset:10240\n" \
M_"v[148:151], a[240:243], a[216:219], v[148:151]\n" \
"s_movk_i32 s84, 0x4280\n" \
M_"v[152:155], a[180:183], a[152:155], v[152:155]\n" \
B_"v24, s[76:79], s84 offen offset:2048 lds\n" \
M_"v[152:155], a[244:247], a[216:219], v[152:155]\n" \
"s_movk_i32 s84, 0x6280\n" \
M_"v[156:159], a[184:187], a[152:155], v[156:159]\n" \
B_"v25, s[76:79], s84 offen offset:3072 lds\n" \
M_"v[156:159], a[248:251], a[216:219], v[156:159]\n" \
R_"v[248:251], v22 offset:32768\n" \
M_"v[160:163], a[188:191], a[152:155], v[160:163]\n" \
R_"v[48:51], v23 offset:32768\n" \
M_"v[160:163], a[252:255], a[216:219], v[160:163]\n" \
R_"a[152:155], v28 offset:12288\n" \
M_"v[176:179], a[188:191], a[156:159], v[176:179]\n" \
R_"a[216:219], v29 offset:12288\n" \
M_"v[176:179], a[252:255], a[220:223], v[176:179]\n" \
"s_cmp_lg_u32 s18, 0\n" \
"s_cselect_b32 s17, s16, s17\n" \
M_"v[172:175], a[184:187], a[156:159], v[172:175]\n" \
"v_writelane_b32 v11, s16, 23\n" \
"s_or_b32 s16, s15, 0x10000\n" \
M_"v[172:175], a[248:251], a[220:223], v[172:175]\n" \
"v_writelane_b32 v11, s17, 24\n" \
"s_add_i32 s17, s15, 0x19000\n" \
M_"v[168:171], a[180:183], a[156:159], v[168:171]\n" \
"v_writelane_b32 v11, s16, 25\n" \
"s_mov_b32 s16, 0\n" \
M_"v[168:171], a[244:247], a[220:223], v[168:171]\n" \
"s_cmpk_lt_u32 s3, 3\n" \
"s_cselect_b32 s17, s17, s16\n" \
M_"v[164:167], a[176:179], a[156:159], v[164:167]\n" \
"s_bitset1_b32 s15, 17\n" \
"v_writelane_b32 v11, s17, 26\n" \
M_"v[164:167], a[240:243], a[220:223], v[164:167]\n" \
R_"a[156:159], v28 offset:14336\n" \
R_"a[220:223], v29 offset:14336\n" \
"s_waitcnt lgkmcnt(15)\n" \
M_"a[0:3], a[160:163], a[128:131], a[0:3]\n" \
"s_add_u32 m0, s30, 0x21000\n" \
"s_addk_i32 s84, 0x3000\n" \
M_"a[0:3], a[224:227], a[192:195], a[0:3]\n" \
B_"v24, s[76:79], s84 offen offset:0 lds\n" \
M_"a[4:7], a[164:167], a[128:131], a[4:7]\n" \
X_"a[176:177], v36 offset:32768\n" \
"s_movk_i32 s17, 0x2400\n" \
M_"a[4:7], a[228:231], a[192:195], a[4:7]\n" \
X_"a[178:179], v36 offset:33792\n" \
M_"a[12:15], a[172:175], a[128:131], a[12:15]\n" \
X_"a[240:241], v36 offset:40960\n" \
"s_waitcnt lgkmcnt(15)\n" \
M_"a[12:15], a[236:239], a[192:195], a[12:15]\n" \
"s_add_u32 s88, s42, 0x1000\n" \
"buffer_store_dwordx4 v[220:223], v20, s[80:83], s88 offen offset:0 sc1\n" \
"s_waitcnt lgkmcnt(13)\n" \
M_"a[8:11], a[128:131], a[168:171], a[8:11]\n" \
X_"a[242:243], v36 offset:41984\n" \
"s_waitcnt lgkmcnt(12)\n" \
M_"a[8:11], a[192:195], a[232:235], a[8:11]\n" \
X_"a[180:181], v37 offset:32768\n" \
M_"a[24:27], a[132:135], a[168:171], a[24:27]\n" \
X_"a[182:183], v37 offset:33792\n" \
M_"a[24:27], a[196:199], a[232:235], a[24:27]\n" \
X_"a[244:245], v37 offset:40960\n" \
M_"a[28:31], a[172:175], a[132:135], a[28:31]\n" \
"s_cmpk_lt_u32 s3, 3\n" \
"s_cselect_b32 s15, s15, s17\n" \
M_"a[28:31], a[236:239], a[196:199], a[28:31]\n" \
"s_addk_i32 s84, 0x2000\n" \
M_"a[20:23], a[164:167], a[132:135], a[20:23]\n" \
B_"v25, s[76:79], s84 offen offset:1024 lds\n" \
M_"a[20:23], a[228:231], a[196:199], a[20:23]\n" \
X_"a[246:247], v37 offset:41984\n" \
"v_writelane_b32 v11, s15, 27\n" \
M_"a[16:19], a[160:163], a[132:135], a[16:19]\n" \
X_"a[184:185], v38 offset:32768\n" \
"s_lshl_b32 s15, s3, 10\n" \
M_"a[16:19], a[224:227], a[196:199], a[16:19]\n" \
X_"a[186:187], v38 offset:33792\n" \
"s_addk_i32 s15, 0x4800\n" \
M_"a[32:35], a[160:163], a[136:139], a[32:35]\n" \
"s_addk_i32 s84, 0x2000\n" \
M_"a[32:35], a[224:227], a[200:203], a[32:35]\n" \
B_"v24, s[76:79], s84 offen offset:2048 lds\n" \
M_"a[36:39], a[164:167], a[136:139], a[36:39]\n" \
X_"a[248:249], v38 offset:40960\n" \
"v_writelane_b32 v11, s15, 28\n" \
M_"a[36:39], a[228:231], a[200:203], a[36:39]\n" \
X_"a[250:251], v38 offset:41984\n" \
"s_add_i32 s15, s14, 1\n" \
M_"a[44:47], a[172:175], a[136:139], a[44:47]\n" \
X_"a[188:189], v39 offset:32768\n" \
"s_and_b32 s15, s15, 3\n" \
M_"a[44:47], a[236:239], a[200:203], a[44:47]\n" \
X_"a[190:191], v39 offset:33792\n" \
"s_or_b32 s15, s15, s4\n" \
M_"a[40:43], a[136:139], a[168:171], a[40:43]\n" \
"s_add_u32 s89, s42, 0x0\n" \
"buffer_store_dwordx4 v[192:195], v20, s[80:83], s89 offen offset:2048 sc1\n" \
M_"a[40:43], a[200:203], a[232:235], a[40:43]\n" \
X_"a[252:253], v39 offset:40960\n" \
"s_lshl_b32 s17, s15, 2\n" \
M_"a[56:59], a[140:143], a[168:171], a[56:59]\n" \
X_"a[254:255], v39 offset:41984\n" \
"s_or_b32 s17, s17, s3\n" \
M_"a[56:59], a[204:207], a[232:235], a[56:59]\n" \
"s_mul_i32 s17, s17, 0x9000\n" \
"v_writelane_b32 v11, s17, 30\n" \
M_"a[60:63], a[172:175], a[140:143], a[60:63]\n" \
"s_lshl_b32 s17, s12, 2\n" \
"s_or_b32 s17, s17, s3\n" \
M_"a[60:63], a[236:239], a[204:207], a[60:63]\n" \
"s_addk_i32 s84, 0x2000\n" \
M_"a[52:55], a[164:167], a[140:143], a[52:55]\n" \
B_"v25, s[76:79], s84 offen offset:3072 lds\n" \
M_"a[52:55], a[228:231], a[204:207], a[52:55]\n" \
"s_add_i32 s14, s14, -1\n" \
"s_xor_b32 s17, s17, 8\n" \
M_"a[48:51], a[160:163], a[140:143], a[48:51]\n" \
"s_and_b32 s14, s14, 3\n" \
"s_mul_i32 s17, s17, 0x9000\n" \
M_"a[48:51], a[224:227], a[204:207], a[48:51]\n" \
"s_or_b32 s14, s14, s4\n" \
"v_writelane_b32 v11, s17, 31\n" \
"s_waitcnt lgkmcnt(15)\n" \
M_"a[64:67], a[164:167], v[248:251], a[64:67]\n" \
"s_lshl_b32 s17, s14, 2\n" \
"s_or_b32 s17, s17, s3\n" \
M_"a[64:67], a[228:231], v[48:51], a[64:67]\n" \
"s_mul_i32 s17, s17, 0x9000\n" \
"v_writelane_b32 v11, s17, 32\n" \
M_"a[80:83], a[172:175], v[248:251], a[80:83]\n" \
"s_or_b32 s17, s8, s13\n" \
"s_mul_i32 s17, s17, 0x24800\n" \
M_"a[80:83], a[236:239], v[48:51], a[80:83]\n" \
"s_lshl_b32 s15, s15, 12\n" \
"s_add_i32 s18, s17, 0x3004800\n" \
M_"a[68:71], v[248:251], a[168:171], a[68:71]\n" \
"s_lshl_b32 s12, s12, 12\n" \
"s_add_i32 s19, s18, s15\n" \
"s_waitcnt vmcnt(11) lgkmcnt(0)\n" \
M_"a[68:71], v[48:51], a[232:235], a[68:71]\n" \
"s_barrier\n" \
M_"v[52:55], a[176:179], a[128:131], v[52:55]\n" \
"s_add_u32 m0, s40, 0x8000\n" \
"s_add_u32 s85, s39, 0xa000\n" \
M_"v[52:55], a[240:243], a[192:195], v[52:55]\n" \
B_"v20, s[80:83], s85 offen sc1 lds\n" \
M_"v[56:59], a[180:183], a[128:131], v[56:59]\n" \
X_"a[160:161], v32 offset:0\n" \
"v_accvgpr_read_b32 v224, a120\n" \
M_"v[56:59], a[244:247], a[192:195], v[56:59]\n" \
X_"a[162:163], v32 offset:512\n" \
"v_accvgpr_read_b32 v225, a121\n" \
M_"v[60:63], a[184:187], a[128:131], v[60:63]\n" \
"s_add_u32 m0, s40, 0x9000\n" \
"s_add_u32 s85, s39, 0xb000\n" \
M_"v[60:63], a[248:251], a[192:195], v[60:63]\n" \
B_"v20, s[80:83], s85 offen sc1 lds\n" \
M_"v[64:67], a[188:191], a[128:131], v[64:67]\n" \
X_"a[224:225], v32 offset:4096\n" \
"v_accvgpr_read_b32 v226, a122\n" \
M_"v[64:67], a[252:255], a[192:195], v[64:67]\n" \
R_"a[128:131], v28 offset:32768\n" \
"v_accvgpr_read_b32 v227, a123\n" \
M_"v[80:83], a[188:191], a[132:135], v[80:83]\n" \
R_"a[192:195], v29 offset:32768\n" \
"v_accvgpr_read_b32 v180, a124\n" \
M_"v[80:83], a[252:255], a[196:199], v[80:83]\n" \
"s_add_u32 s88, s42, 0x0\n" \
"buffer_store_dwordx4 v[196:199], v20, s[80:83], s88 offen offset:3072 sc1\n" \
M_"v[76:79], a[184:187], a[132:135], v[76:79]\n" \
X_"a[226:227], v32 offset:4608\n" \
"v_accvgpr_read_b32 v181, a125\n" \
M_"v[76:79], a[248:251], a[196:199], v[76:79]\n" \
X_"a[164:165], v33 offset:0\n" \
"v_accvgpr_read_b32 v182, a126\n" \
M_"v[72:75], a[180:183], a[132:135], v[72:75]\n" \
"s_add_u32 m0, s40, 0xb000\n" \
"s_movk_i32 s84, 0x280\n" \
M_"v[72:75], a[244:247], a[196:199], v[72:75]\n" \
B_"v21, s[76:79], s84 offen offset:0 lds\n" \
M_"v[68:71], a[176:179], a[132:135], v[68:71]\n" \
X_"a[166:167], v33 offset:512\n" \
"v_accvgpr_read_b32 v183, a127\n" \
M_"v[68:71], a[240:243], a[196:199], v[68:71]\n" \
R_"a[132:135], v28 offset:34816\n" \
C_"v224, v224, v225\n" \
M_"v[84:87], a[176:179], a[136:139], v[84:87]\n" \
R_"a[196:199], v29 offset:34816\n" \
C_"v225, v226, v227\n" \
M_"v[84:87], a[240:243], a[200:203], v[84:87]\n" \
"s_add_u32 m0, s31, 0x8000\n" \
"s_mov_b32 s84, 0x280000\n" \
M_"v[88:91], a[180:183], a[136:139], v[88:91]\n" \
B_"v26, s[68:71], s84 offen offset:0 lds\n" \
M_"v[88:91], a[244:247], a[200:203], v[88:91]\n" \
X_"a[228:229], v33 offset:4096\n" \
C_"v226, v180, v181\n" \
M_"v[92:95], a[184:187], a[136:139], v[92:95]\n" \
X_"a[230:231], v33 offset:4608\n" \
C_"v227, v182, v183\n" \
M_"v[92:95], a[248:251], a[200:203], v[92:95]\n" \
"s_addk_i32 s84, 0x7c00\n" \
M_"v[96:99], a[188:191], a[136:139], v[96:99]\n" \
B_"v26, s[68:71], s84 offen offset:1024 lds\n" \
M_"v[96:99], a[252:255], a[200:203], v[96:99]\n" \
R_"a[136:139], v28 offset:36864\n" \
"v_accvgpr_read_b32 v220, a112\n" \
M_"v[112:115], a[188:191], a[140:143], v[112:115]\n" \
R_"a[200:203], v29 offset:36864\n" \
"v_accvgpr_read_b32 v221, a113\n" \
M_"v[112:115], a[252:255], a[204:207], v[112:115]\n" \
"s_addk_i32 s84, 0x7c00\n" \
M_"v[108:111], a[184:187], a[140:143], v[108:111]\n" \
B_"v27, s[68:71], s84 offen offset:2048 lds\n" \
M_"v[108:111], a[248:251], a[204:207], v[108:111]\n" \
X_"a[172:173], v35 offset:0\n" \
"v_accvgpr_read_b32 v222, a114\n" \
M_"v[104:107], a[180:183], a[140:143], v[104:107]\n" \
X_"a[174:175], v35 offset:512\n" \
"v_accvgpr_read_b32 v223, a115\n" \
M_"v[104:107], a[244:247], a[204:207], v[104:107]\n" \
X_"a[236:237], v35 offset:4096\n" \
"v_accvgpr_read_b32 v180, a116\n" \
M_"v[100:103], a[176:179], a[140:143], v[100:103]\n" \
"v_accvgpr_read_b32 v181, a117\n" \
"v_accvgpr_read_b32 v182, a118\n" \
M_"v[100:103], a[240:243], a[204:207], v[100:103]\n" \
R_"a[140:143], v28 offset:38912\n" \
"v_accvgpr_read_b32 v183, a119\n" \
M_"v[116:119], a[176:179], a[144:147], v[116:119]\n" \
R_"a[204:207], v29 offset:38912\n" \
C_"v220, v220, v221\n" \
M_"v[116:119], a[240:243], a[208:211], v[116:119]\n" \
"s_addk_i32 s84, 0x7c00\n" \
M_"v[120:123], a[180:183], a[144:147], v[120:123]\n" \
B_"v27, s[68:71], s84 offen offset:3072 lds\n" \
M_"v[120:123], a[244:247], a[208:211], v[120:123]\n" \
"s_add_u32 m0, s30, 0x0\n" \
"s_movk_i32 s84, 0x300\n" \
"s_nop 0\n" \
M_"v[124:127], a[184:187], a[144:147], v[124:127]\n" \
B_"v24, s[76:79], s84 offen offset:0 lds\n" \
M_"v[124:127], a[248:251], a[208:211], v[124:127]\n" \
X_"a[238:239], v35 offset:4608\n" \
C_"v221, v222, v223\n" \
M_"v[128:131], a[188:191], a[144:147], v[128:131]\n" \
"s_add_u32 s89, s42, 0x1000\n" \
"buffer_store_dwordx4 a[84:87], v20, s[80:83], s89 offen offset:3072 sc1\n" \
M_"v[128:131], a[252:255], a[208:211], v[128:131]\n" \
R_"a[144:147], v28 offset:40960\n" \
C_"v222, v180, v181\n" \
M_"v[144:147], a[188:191], a[148:151], v[144:147]\n" \
R_"a[208:211], v29 offset:40960\n" \
C_"v223, v182, v183\n" \
M_"v[144:147], a[252:255], a[212:215], v[144:147]\n" \
X_"a[168:169], v34 offset:0\n" \
"v_accvgpr_read_b32 v216, a104\n" \
M_"v[140:143], a[184:187], a[148:151], v[140:143]\n" \
X_"a[170:171], v34 offset:512\n" \
"v_accvgpr_read_b32 v217, a105\n" \
M_"v[140:143], a[248:251], a[212:215], v[140:143]\n" \
"s_movk_i32 s84, 0x2300\n" \
"s_nop 0\n" \
M_"v[136:139], a[180:183], a[148:151], v[136:139]\n" \
B_"v25, s[76:79], s84 offen offset:1024 lds\n" \
M_"v[136:139], a[244:247], a[212:215], v[136:139]\n" \
X_"a[232:233], v34 offset:4096\n" \
"v_accvgpr_read_b32 v218, a106\n" \
M_"v[132:135], a[176:179], a[148:151], v[132:135]\n" \
X_"a[234:235], v34 offset:4608\n" \
"v_accvgpr_read_b32 v219, a107\n" \
M_"v[132:135], a[240:243], a[212:215], v[132:135]\n" \
R_"a[148:151], v28 offset:43008\n" \
"v_accvgpr_read_b32 v180, a108\n" \
M_"v[148:151], a[176:179], a[152:155], v[148:151]\n" \
R_"a[212:215], v29 offset:43008\n" \
"v_accvgpr_read_b32 v181, a109\n" \
M_"v[148:151], a[240:243], a[216:219], v[148:151]\n" \
"s_movk_i32 s84, 0x4300\n" \
M_"v[152:155], a[180:183], a[152:155], v[152:155]\n" \
B_"v24, s[76:79], s84 offen offset:2048 lds\n" \
M_"v[152:155], a[244:247], a[216:219], v[152:155]\n" \
"s_movk_i32 s84, 0x6300\n" \
M_"v[156:159], a[184:187], a[152:155], v[156:159]\n" \
B_"v25, s[76:79], s84 offen offset:3072 lds\n" \
M_"v[156:159], a[248:251], a[216:219], v[156:159]\n" \
R_"v[248:251], v22 offset:0\n" \
"v_accvgpr_read_b32 v182, a110\n" \
M_"v[160:163], a[188:191], a[152:155], v[160:163]\n" \
R_"v[48:51], v23 offset:0\n" \
"v_accvgpr_read_b32 v183, a111\n" \
M_"v[160:163], a[252:255], a[216:219], v[160:163]\n" \
R_"a[152:155], v28 offset:45056\n" \
C_"v216, v216, v217\n" \
M_"v[176:179], a[188:191], a[156:159], v[176:179]\n" \
R_"a[216:219], v29 offset:45056\n" \
C_"v217, v218, v219\n" \
M_"v[176:179], a[252:255], a[220:223], v[176:179]\n" \
C_"v218, v180, v181\n" \
C_"v219, v182, v183\n" \
M_"v[172:175], a[184:187], a[156:159], v[172:175]\n" \
"v_accvgpr_read_b32 v192, a96\n" \
"v_accvgpr_read_b32 v193, a97\n" \
M_"v[172:175], a[248:251], a[220:223], v[172:175]\n" \
"v_accvgpr_read_b32 v194, a98\n" \
"v_accvgpr_read_b32 v195, a99\n" \
M_"v[168:171], a[180:183], a[156:159], v[168:171]\n" \
"v_accvgpr_read_b32 v180, a100\n" \
"v_accvgpr_read_b32 v181, a101\n" \
M_"v[168:171], a[244:247], a[220:223], v[168:171]\n" \
"v_accvgpr_read_b32 v182, a102\n" \
"v_accvgpr_read_b32 v183, a103\n" \
M_"v[164:167], a[176:179], a[156:159], v[164:167]\n" \
C_"v192, v192, v193\n" \
C_"v193, v194, v195\n" \
M_"v[164:167], a[240:243], a[220:223], v[164:167]\n" \
R_"a[156:159], v28 offset:47104\n" \
R_"a[220:223], v29 offset:47104\n" \
"s_waitcnt lgkmcnt(15)\n" \
M_"a[0:3], a[160:163], a[128:131], a[0:3]\n" \
"s_add_u32 m0, s30, 0x1000\n" \
"s_addk_i32 s84, 0x3000\n" \
M_"a[0:3], a[224:227], a[192:195], a[0:3]\n" \
B_"v24, s[76:79], s84 offen offset:0 lds\n" \
M_"a[4:7], a[164:167], a[128:131], a[4:7]\n" \
X_"a[176:177], v36 offset:0\n" \
C_"v194, v180, v181\n" \
M_"a[4:7], a[228:231], a[192:195], a[4:7]\n" \
X_"a[178:179], v36 offset:1024\n" \
C_"v195, v182, v183\n" \
M_"a[12:15], a[172:175], a[128:131], a[12:15]\n" \
X_"a[240:241], v36 offset:8192\n" \
"v_accvgpr_read_b32 v188, a88\n" \
"s_waitcnt lgkmcnt(15)\n" \
M_"a[12:15], a[236:239], a[192:195], a[12:15]\n" \
"s_add_u32 s88, s42, 0x1000\n" \
"buffer_store_dwordx4 v[252:255], v20, s[80:83], s88 offen offset:2048 sc1\n" \
"s_waitcnt lgkmcnt(13)\n" \
M_"a[8:11], a[128:131], a[168:171], a[8:11]\n" \
X_"a[242:243], v36 offset:9216\n" \
"v_accvgpr_read_b32 v189, a89\n" \
"s_waitcnt lgkmcnt(12)\n" \
"s_nop 0\n" \
M_"a[8:11], a[192:195], a[232:235], a[8:11]\n" \
X_"a[180:181], v37 offset:0\n" \
"v_accvgpr_read_b32 v190, a90\n" \
M_"a[24:27], a[132:135], a[168:171], a[24:27]\n" \
X_"a[182:183], v37 offset:1024\n" \
"v_accvgpr_read_b32 v191, a91\n" \
M_"a[24:27], a[196:199], a[232:235], a[24:27]\n" \
X_"a[244:245], v37 offset:8192\n" \
"v_accvgpr_read_b32 v180, a92\n" \
M_"a[28:31], a[172:175], a[132:135], a[28:31]\n" \
"v_accvgpr_read_b32 v181, a93\n" \
"v_accvgpr_read_b32 v182, a94\n" \
M_"a[28:31], a[236:239], a[196:199], a[28:31]\n" \
"s_addk_i32 s84, 0x2000\n" \
"s_nop 0\n" \
M_"a[20:23], a[164:167], a[132:135], a[20:23]\n" \
B_"v25, s[76:79], s84 offen offset:1024 lds\n" \
M_"a[20:23], a[228:231], a[196:199], a[20:23]\n" \
X_"a[246:247], v37 offset:9216\n" \
"v_accvgpr_read_b32 v183, a95\n" \
M_"a[16:19], a[160:163], a[132:135], a[16:19]\n" \
X_"a[184:185], v38 offset:0\n" \
C_"v188, v188, v189\n" \
M_"a[16:19], a[224:227], a[196:199], a[16:19]\n" \
X_"a[186:187], v38 offset:1024\n" \
C_"v189, v190, v191\n" \
M_"a[32:35], a[160:163], a[136:139], a[32:35]\n" \
"s_addk_i32 s84, 0x2000\n" \
"s_nop 0\n" \
M_"a[32:35], a[224:227], a[200:203], a[32:35]\n" \
B_"v24, s[76:79], s84 offen offset:2048 lds\n" \
M_"a[36:39], a[164:167], a[136:139], a[36:39]\n" \
X_"a[248:249], v38 offset:8192\n" \
C_"v190, v180, v181\n" \
M_"a[36:39], a[228:231], a[200:203], a[36:39]\n" \
X_"a[250:251], v38 offset:9216\n" \
C_"v191, v182, v183\n" \
M_"a[44:47], a[172:175], a[136:139], a[44:47]\n" \
X_"a[188:189], v39 offset:0\n" \
"v_accvgpr_read_b32 v184, a72\n" \
M_"a[44:47], a[236:239], a[200:203], a[44:47]\n" \
X_"a[190:191], v39 offset:1024\n" \
"v_accvgpr_read_b32 v185, a73\n" \
".fill (L_pb + 28384 - .) / 4, 4, 0xBF800000\n" \
"L_pp7:\n" \
"s_cbranch_execz L_pret\n" \
"s_nop 0\n" \
M_"a[40:43], a[136:139], a[168:171], a[40:43]\n" \
"s_add_u32 s89, s42, 0x2000\n" \
"buffer_store_dwordx4 v[200:203], v20, s[80:83], s89 offen offset:2048 sc1\n" \
M_"a[40:43], a[200:203], a[232:235], a[40:43]\n" \
X_"a[252:253], v39 offset:8192\n" \
"v_accvgpr_read_b32 v186, a74\n" \
M_"a[56:59], a[140:143], a[168:171], a[56:59]\n" \
X_"a[254:255], v39 offset:9216\n" \
"v_accvgpr_read_b32 v187, a75\n" \
M_"a[56:59], a[204:207], a[232:235], a[56:59]\n" \
"v_accvgpr_read_b32 v180, a76\n" \
"v_accvgpr_read_b32 v181, a77\n" \
M_"a[60:63], a[172:175], a[140:143], a[60:63]\n" \
"v_accvgpr_read_b32 v182, a78\n" \
"v_accvgpr_read_b32 v183, a79\n" \
M_"a[60:63], a[236:239], a[204:207], a[60:63]\n" \
"s_addk_i32 s84, 0x2000\n" \
"s_nop 0\n" \
M_"a[52:55], a[164:167], a[140:143], a[52:55]\n" \
B_"v25, s[76:79], s84 offen offset:3072 lds\n" \
M_"a[52:55], a[228:231], a[204:207], a[52:55]\n" \
C_"v184, v184, v185\n" \
C_"v185, v186, v187\n" \
M_"a[48:51], a[160:163], a[140:143], a[48:51]\n" \
C_"v186, v180, v181\n" \
C_"v187, v182, v183\n" \
M_"a[48:51], a[224:227], a[204:207], a[48:51]\n" \
"v_writelane_b32 v11, s19, 33\n" \
"s_xor_b32 s12, s12, 0x2000\n" \
"s_waitcnt lgkmcnt(15)\n" \
M_"a[64:67], a[164:167], v[248:251], a[64:67]\n" \
"s_lshl_b32 s14, s14, 12\n" \
"s_sub_i32 s13, s8, s13\n" \
M_"a[64:67], a[228:231], v[48:51], a[64:67]\n" \
"s_add_i32 s19, s18, s12\n" \
"v_writelane_b32 v11, s19, 34\n" \
M_"a[80:83], a[172:175], v[248:251], a[80:83]\n" \
"s_add_i32 s18, s18, s14\n" \
"s_mul_i32 s13, s13, 0x24800\n" \
M_"a[80:83], a[236:239], v[48:51], a[80:83]\n" \
"v_writelane_b32 v11, s18, 35\n" \
"s_add_i32 s18, s13, 0x4120800\n" \
M_"a[68:71], v[248:251], a[168:171], a[68:71]\n" \
"s_add_i32 s15, s18, s15\n" \
"v_writelane_b32 v11, s15, 36\n" \
"s_waitcnt vmcnt(11) lgkmcnt(0)\n" \
M_"a[68:71], v[48:51], a[232:235], a[68:71]\n" \
"s_barrier\n" \
"v_mov_b32_e32 v45, s62\n" \
"s_mov_b64 exec, 1\n" \
"buffer_store_dword v45, off, s[80:83], s37 offset:0 sc1\n" \
"s_mov_b64 exec, -1\n" \
"v_xor_b32_e32 v28, 0x20000, v28\n" \
"v_xor_b32_e32 v29, 0x20000, v29\n" \
M_"v[52:55], a[176:179], a[128:131], v[52:55]\n" \
"s_add_u32 m0, s40, 0x0\n" \
"s_add_u32 s85, s39, 0xc000\n" \
M_"v[52:55], a[240:243], a[192:195], v[52:55]\n" \
B_"v20, s[80:83], s85 offen sc1 lds\n" \
M_"v[56:59], a[180:183], a[128:131], v[56:59]\n" \
X_"a[160:161], v32 offset:32768\n" \
"s_add_i32 s12, s18, s12\n" \
M_"v[56:59], a[244:247], a[192:195], v[56:59]\n" \
X_"a[162:163], v32 offset:33280\n" \
"v_writelane_b32 v11, s12, 37\n" \
M_"v[60:63], a[184:187], a[128:131], v[60:63]\n" \
"s_add_u32 m0, s40, 0x1000\n" \
"s_add_u32 s85, s39, 0xd000\n" \
M_"v[60:63], a[248:251], a[192:195], v[60:63]\n" \
B_"v20, s[80:83], s85 offen sc1 lds\n" \
M_"v[64:67], a[188:191], a[128:131], v[64:67]\n" \
X_"a[224:225], v32 offset:36864\n" \
"s_add_i32 s18, s18, s14\n" \
M_"v[64:67], a[252:255], a[192:195], v[64:67]\n" \
R_"a[128:131], v28 offset:0\n" \
"s_or_b32 s12, s17, 0x3000000\n" \
M_"v[80:83], a[188:191], a[132:135], v[80:83]\n" \
R_"a[192:195], v29 offset:0\n" \
"s_lshl_b32 s14, s5, 11\n" \
M_"v[80:83], a[252:255], a[196:199], v[80:83]\n" \
"s_add_u32 m0, s40, 0x3000\n" \
"s_movk_i32 s84, 0x300\n" \
M_"v[76:79], a[184:187], a[132:135], v[76:79]\n" \
B_"v21, s[76:79], s84 offen offset:0 lds\n" \
M_"v[76:79], a[248:251], a[196:199], v[76:79]\n" \
X_"a[226:227], v32 offset:37376\n" \
"v_writelane_b32 v11, s18, 38\n" \
M_"v[72:75], a[180:183], a[132:135], v[72:75]\n" \
X_"a[164:165], v33 offset:32768\n" \
"s_add_i32 s15, s12, s14\n" \
M_"v[72:75], a[244:247], a[196:199], v[72:75]\n" \
"v_writelane_b32 v11, s15, 39\n" \
"s_add_i32 s15, s13, 0x411c000\n" \
M_"v[68:71], a[176:179], a[132:135], v[68:71]\n" \
X_"a[166:167], v33 offset:33280\n" \
"s_add_i32 s18, s15, s14\n" \
M_"v[68:71], a[240:243], a[196:199], v[68:71]\n" \
R_"a[132:135], v28 offset:2048\n" \
"v_writelane_b32 v11, s18, 40\n" \
M_"v[84:87], a[176:179], a[136:139], v[84:87]\n" \
R_"a[196:199], v29 offset:2048\n" \
"s_add_i32 s17, s17, 0x3004000\n" \
M_"v[84:87], a[240:243], a[200:203], v[84:87]\n" \
"s_add_u32 m0, s31, 0x0\n" \
"s_nop 0\n" \
"s_mov_b32 s84, 0x300000\n" \
M_"v[88:91], a[180:183], a[136:139], v[88:91]\n" \
B_"v26, s[68:71], s84 offen offset:0 lds\n" \
M_"v[88:91], a[244:247], a[200:203], v[88:91]\n" \
"s_add_u32 s88, s42, 0x2000\n" \
"buffer_store_dwordx4 v[204:207], v20, s[80:83], s88 offen offset:3072 sc1\n" \
M_"v[92:95], a[184:187], a[136:139], v[92:95]\n" \
X_"a[228:229], v33 offset:36864\n" \
"v_writelane_b32 v11, s17, 41\n" \
M_"v[92:95], a[248:251], a[200:203], v[92:95]\n" \
X_"a[230:231], v33 offset:37376\n" \
"s_add_i32 s13, s13, 0x4120000\n" \
M_"v[96:99], a[188:191], a[136:139], v[96:99]\n" \
"v_writelane_b32 v11, s13, 42\n" \
"s_lshl_b32 s13, s9, 2\n" \
M_"v[96:99], a[252:255], a[200:203], v[96:99]\n" \
R_"a[136:139], v28 offset:4096\n" \
"s_or_b32 s7, s13, s7\n" \
M_"v[112:115], a[188:191], a[140:143], v[112:115]\n" \
R_"a[200:203], v29 offset:4096\n" \
"s_mul_i32 s7, s7, 0x90000\n" \
M_"v[112:115], a[252:255], a[204:207], v[112:115]\n" \
"s_addk_i32 s84, 0x7c00\n" \
M_"v[108:111], a[184:187], a[140:143], v[108:111]\n" \
B_"v26, s[68:71], s84 offen offset:1024 lds\n" \
M_"v[108:111], a[248:251], a[204:207], v[108:111]\n" \
X_"a[172:173], v35 offset:32768\n" \
"s_lshl_b32 s13, s3, 14\n" \
M_"v[104:107], a[180:183], a[140:143], v[104:107]\n" \
"s_addk_i32 s84, 0x7c00\n" \
M_"v[104:107], a[244:247], a[204:207], v[104:107]\n" \
B_"v27, s[68:71], s84 offen offset:2048 lds\n" \
M_"v[100:103], a[176:179], a[140:143], v[100:103]\n" \
X_"a[174:175], v35 offset:33280\n" \
"s_lshl_b32 s9, s9, 7\n" \
M_"v[100:103], a[240:243], a[204:207], v[100:103]\n" \
R_"a[140:143], v28 offset:6144\n" \
"s_lshl_b32 s8, s8, 4\n" \
M_"v[116:119], a[176:179], a[144:147], v[116:119]\n" \
R_"a[204:207], v29 offset:6144\n" \
"s_add_i32 s7, s7, s13\n" \
M_"v[116:119], a[240:243], a[208:211], v[116:119]\n" \
"s_addk_i32 s84, 0x7c00\n" \
M_"v[120:123], a[180:183], a[144:147], v[120:123]\n" \
B_"v27, s[68:71], s84 offen offset:3072 lds\n" \
M_"v[120:123], a[244:247], a[208:211], v[120:123]\n" \
X_"a[236:237], v35 offset:36864\n" \
"s_or_b32 s13, s8, s9\n" \
M_"v[124:127], a[184:187], a[144:147], v[124:127]\n" \
X_"a[238:239], v35 offset:37376\n" \
"s_sub_i32 s8, s8, s9\n" \
M_"v[124:127], a[248:251], a[208:211], v[124:127]\n" \
"s_add_u32 m0, s30, 0x8000\n" \
"s_movk_i32 s84, 0x380\n" \
M_"v[128:131], a[188:191], a[144:147], v[128:131]\n" \
B_"v24, s[76:79], s84 offen offset:0 lds\n" \
M_"v[128:131], a[252:255], a[208:211], v[128:131]\n" \
R_"a[144:147], v28 offset:8192\n" \
"s_mulk_i32 s13, 0x480\n" \
M_"v[144:147], a[188:191], a[148:151], v[144:147]\n" \
R_"a[208:211], v29 offset:8192\n" \
"s_mulk_i32 s8, 0x480\n" \
M_"v[144:147], a[252:255], a[212:215], v[144:147]\n" \
X_"a[168:169], v34 offset:32768\n" \
"s_or_b32 s13, s13, s11\n" \
M_"v[140:143], a[184:187], a[148:151], v[140:143]\n" \
"s_movk_i32 s84, 0x2380\n" \
M_"v[140:143], a[248:251], a[212:215], v[140:143]\n" \
B_"v25, s[76:79], s84 offen offset:1024 lds\n" \
M_"v[136:139], a[180:183], a[148:151], v[136:139]\n" \
X_"a[170:171], v34 offset:33280\n" \
"s_or_b32 s8, s8, s11\n" \
M_"v[136:139], a[244:247], a[212:215], v[136:139]\n" \
X_"a[232:233], v34 offset:36864\n" \
"s_and_b32 s11, s2, 1\n" \
M_"v[132:135], a[176:179], a[148:151], v[132:135]\n" \
X_"a[234:235], v34 offset:37376\n" \
M_"v[132:135], a[240:243], a[212:215], v[132:135]\n" \
R_"a[148:151], v28 offset:10240\n" \
M_"v[148:151], a[176:179], a[152:155], v[148:151]\n" \
R_"a[212:215], v29 offset:10240\n" \
M_"v[148:151], a[240:243], a[216:219], v[148:151]\n" \
"s_movk_i32 s84, 0x4380\n" \
M_"v[152:155], a[180:183], a[152:155], v[152:155]\n" \
B_"v24, s[76:79], s84 offen offset:2048 lds\n" \
M_"v[152:155], a[244:247], a[216:219], v[152:155]\n" \
"s_movk_i32 s84, 0x6380\n" \
M_"v[156:159], a[184:187], a[152:155], v[156:159]\n" \
B_"v25, s[76:79], s84 offen offset:3072 lds\n" \
M_"v[156:159], a[248:251], a[216:219], v[156:159]\n" \
R_"v[248:251], v22 offset:32768\n" \
M_"v[160:163], a[188:191], a[152:155], v[160:163]\n" \
R_"v[48:51], v23 offset:32768\n" \
M_"v[160:163], a[252:255], a[216:219], v[160:163]\n" \
R_"a[152:155], v28 offset:12288\n" \
M_"v[176:179], a[188:191], a[156:159], v[176:179]\n" \
R_"a[216:219], v29 offset:12288\n" \
M_"v[176:179], a[252:255], a[220:223], v[176:179]\n" \
"s_cmp_lg_u32 s11, 0\n" \
"s_cselect_b32 s12, s15, s12\n" \
M_"v[172:175], a[184:187], a[156:159], v[172:175]\n" \
"s_bfe_u32 s9, s2, 0x20001\n" \
"s_add_i32 s15, s7, 0x4240000\n" \
M_"v[172:175], a[248:251], a[220:223], v[172:175]\n" \
"s_addk_i32 s12, 0x4000\n" \
"s_lshl_b32 s5, s5, 16\n" \
M_"v[168:171], a[180:183], a[156:159], v[168:171]\n" \
"s_xor_b32 s9, s9, s3\n" \
M_"v[168:171], a[244:247], a[220:223], v[168:171]\n" \
"s_cmp_lg_u32 s9, 0\n" \
"s_cselect_b32 s12, s6, s12\n" \
M_"v[164:167], a[176:179], a[156:159], v[164:167]\n" \
"s_add_i32 s5, s15, s5\n" \
"v_writelane_b32 v11, s12, 43\n" \
M_"v[164:167], a[240:243], a[220:223], v[164:167]\n" \
R_"a[156:159], v28 offset:14336\n" \
R_"a[220:223], v29 offset:14336\n" \
"s_waitcnt lgkmcnt(15)\n" \
M_"a[0:3], a[160:163], a[128:131], a[0:3]\n" \
"s_add_u32 m0, s30, 0x9000\n" \
"s_addk_i32 s84, 0x3000\n" \
M_"a[0:3], a[224:227], a[192:195], a[0:3]\n" \
B_"v24, s[76:79], s84 offen offset:0 lds\n" \
M_"a[4:7], a[164:167], a[128:131], a[4:7]\n" \
X_"a[176:177], v36 offset:32768\n" \
"s_add_i32 s7, s7, 0x42c0000\n" \
M_"a[4:7], a[228:231], a[192:195], a[4:7]\n" \
X_"a[178:179], v36 offset:33792\n" \
"v_writelane_b32 v11, s5, 44\n" \
M_"a[12:15], a[172:175], a[128:131], a[12:15]\n" \
X_"a[240:241], v36 offset:40960\n" \
"s_lshl_b32 s5, s11, 10\n" \
"s_waitcnt lgkmcnt(15)\n" \
M_"a[12:15], a[236:239], a[192:195], a[12:15]\n" \
"s_add_u32 s89, s42, 0x3000\n" \
"buffer_store_dwordx4 v[208:211], v20, s[80:83], s89 offen offset:3072 sc1\n" \
"s_waitcnt lgkmcnt(13)\n" \
M_"a[8:11], a[128:131], a[168:171], a[8:11]\n" \
X_"a[242:243], v36 offset:41984\n" \
"s_or_b32 s5, s7, s5\n" \
"s_waitcnt lgkmcnt(12)\n" \
M_"a[8:11], a[192:195], a[232:235], a[8:11]\n" \
X_"a[180:181], v37 offset:32768\n" \
M_"a[24:27], a[132:135], a[168:171], a[24:27]\n" \
X_"a[182:183], v37 offset:33792\n" \
M_"a[24:27], a[196:199], a[232:235], a[24:27]\n" \
X_"a[244:245], v37 offset:40960\n" \
M_"a[28:31], a[172:175], a[132:135], a[28:31]\n" \
"s_cmp_lg_u32 s9, 0\n" \
"s_cselect_b32 s5, s6, s5\n" \
M_"a[28:31], a[236:239], a[196:199], a[28:31]\n" \
"s_addk_i32 s84, 0x2000\n" \
M_"a[20:23], a[164:167], a[132:135], a[20:23]\n" \
B_"v25, s[76:79], s84 offen offset:1024 lds\n" \
M_"a[20:23], a[228:231], a[196:199], a[20:23]\n" \
X_"a[246:247], v37 offset:41984\n" \
"s_add_i32 s8, s8, 0x21c000\n" \
M_"a[16:19], a[160:163], a[132:135], a[16:19]\n" \
X_"a[184:185], v38 offset:32768\n" \
"v_writelane_b32 v11, s5, 45\n" \
M_"a[16:19], a[224:227], a[196:199], a[16:19]\n" \
X_"a[186:187], v38 offset:33792\n" \
"s_add_i32 s5, s13, s10\n" \
M_"a[32:35], a[160:163], a[136:139], a[32:35]\n" \
"s_addk_i32 s84, 0x2000\n" \
M_"a[32:35], a[224:227], a[200:203], a[32:35]\n" \
B_"v24, s[76:79], s84 offen offset:2048 lds\n" \
M_"a[36:39], a[164:167], a[136:139], a[36:39]\n" \
X_"a[248:249], v38 offset:40960\n" \
"v_writelane_b32 v11, s5, 46\n" \
M_"a[36:39], a[228:231], a[200:203], a[36:39]\n" \
X_"a[250:251], v38 offset:41984\n" \
"s_add_i32 s5, s8, s10\n" \
"s_nop 0\n" \
M_"a[44:47], a[172:175], a[136:139], a[44:47]\n" \
X_"a[188:189], v39 offset:32768\n" \
"v_writelane_b32 v11, s5, 47\n" \
M_"a[44:47], a[236:239], a[200:203], a[44:47]\n" \
X_"a[190:191], v39 offset:33792\n" \
M_"a[40:43], a[136:139], a[168:171], a[40:43]\n" \
"s_add_u32 s88, s42, 0x3000\n" \
"buffer_store_dwordx4 v[212:215], v20, s[80:83], s88 offen offset:2048 sc1\n" \
M_"a[40:43], a[200:203], a[232:235], a[40:43]\n" \
X_"a[252:253], v39 offset:40960\n" \
M_"a[56:59], a[140:143], a[168:171], a[56:59]\n" \
X_"a[254:255], v39 offset:41984\n" \
M_"a[56:59], a[204:207], a[232:235], a[56:59]\n" \
"s_cmp_lg_u32 s11, 0\n" \
"s_cselect_b32 s5, s8, s13\n" \
M_"a[60:63], a[172:175], a[140:143], a[60:63]\n" \
"s_addk_i32 s5, 0x100\n" \
M_"a[60:63], a[236:239], a[204:207], a[60:63]\n" \
"s_addk_i32 s84, 0x2000\n" \
M_"a[52:55], a[164:167], a[140:143], a[52:55]\n" \
B_"v25, s[76:79], s84 offen offset:3072 lds\n" \
M_"a[52:55], a[228:231], a[204:207], a[52:55]\n" \
"s_cmp_lg_u32 s9, 0\n" \
"s_cselect_b32 s5, s6, s5\n" \
M_"a[48:51], a[160:163], a[140:143], a[48:51]\n" \
"v_writelane_b32 v11, s5, 48\n" \
"s_or_b32 s5, s7, s14\n" \
M_"a[48:51], a[224:227], a[204:207], a[48:51]\n" \
"v_writelane_b32 v11, s5, 56\n" \
"s_lshl_b32 s5, s3, 17\n" \
"s_waitcnt lgkmcnt(15)\n" \
M_"a[64:67], a[164:167], v[248:251], a[64:67]\n" \
"s_add_i32 s5, s15, s5\n" \
"s_or_b32 s4, s5, s4\n" \
M_"a[64:67], a[228:231], v[48:51], a[64:67]\n" \
"s_or_b32 s4, s4, 0x3840\n" \
"v_writelane_b32 v11, s4, 6\n" \
M_"a[80:83], a[172:175], v[248:251], a[80:83]\n" \
"s_mov_b32 s4, 0xff0000\n" \
M_"a[80:83], a[236:239], v[48:51], a[80:83]\n" \
"s_cmp_lg_u32 s9, 0\n" \
"s_cselect_b32 s4, s16, s4\n" \
M_"a[68:71], v[248:251], a[168:171], a[68:71]\n" \
"v_writelane_b32 v11, s4, 7\n" \
"s_lshl_b32 s4, s2, 16\n" \
"s_waitcnt vmcnt(10) lgkmcnt(0)\n" \
M_"a[68:71], v[48:51], a[232:235], a[68:71]\n" \
"s_barrier\n" \
"v_xor_b32_e32 v28, 0x20000, v28\n" \
"v_xor_b32_e32 v29, 0x20000, v29\n" \
M_"v[52:55], a[176:179], a[128:131], v[52:55]\n" \
"s_add_u32 m0, s40, 0x8000\n" \
"s_add_u32 s85, s39, 0xe000\n" \
M_"v[52:55], a[240:243], a[192:195], v[52:55]\n" \
B_"v20, s[80:83], s85 offen sc1 lds\n" \
M_"v[56:59], a[180:183], a[128:131], v[56:59]\n" \
X_"a[160:161], v32 offset:0\n" \
"s_add_i32 s5, s4, 0x10000\n" \
M_"v[56:59], a[244:247], a[192:195], v[56:59]\n" \
X_"a[162:163], v32 offset:512\n" \
"s_and_b32 s5, s5, 0x70000\n" \
M_"v[60:63], a[184:187], a[128:131], v[60:63]\n" \
"s_add_u32 m0, s40, 0x9000\n" \
"s_add_u32 s85, s39, 0xf000\n" \
M_"v[60:63], a[248:251], a[192:195], v[60:63]\n" \
B_"v20, s[80:83], s85 offen sc1 lds\n" \
M_"v[64:67], a[188:191], a[128:131], v[64:67]\n" \
X_"a[224:225], v32 offset:4096\n" \
"s_add_i32 s5, s15, s5\n" \
M_"v[64:67], a[252:255], a[192:195], v[64:67]\n" \
R_"a[128:131], v28 offset:0\n" \
"v_writelane_b32 v11, s5, 49\n" \
M_"v[80:83], a[188:191], a[132:135], v[80:83]\n" \
R_"a[192:195], v29 offset:0\n" \
"s_add_i32 s5, s4, 0x20000\n" \
M_"v[80:83], a[252:255], a[196:199], v[80:83]\n" \
"s_add_u32 m0, s40, 0xb000\n" \
"s_movk_i32 s84, 0x380\n" \
M_"v[76:79], a[184:187], a[132:135], v[76:79]\n" \
B_"v21, s[76:79], s84 offen offset:0 lds\n" \
M_"v[76:79], a[248:251], a[196:199], v[76:79]\n" \
X_"a[226:227], v32 offset:4608\n" \
"s_and_b32 s5, s5, 0x70000\n" \
M_"v[72:75], a[180:183], a[132:135], v[72:75]\n" \
X_"a[164:165], v33 offset:0\n" \
"s_add_i32 s5, s15, s5\n" \
M_"v[72:75], a[244:247], a[196:199], v[72:75]\n" \
"s_bitset1_b32 s5, 11\n" \
"v_writelane_b32 v11, s5, 50\n" \
M_"v[68:71], a[176:179], a[132:135], v[68:71]\n" \
X_"a[166:167], v33 offset:512\n" \
"s_add_i32 s5, s4, 0x30000\n" \
M_"v[68:71], a[240:243], a[196:199], v[68:71]\n" \
R_"a[132:135], v28 offset:2048\n" \
"s_and_b32 s5, s5, 0x70000\n" \
M_"v[84:87], a[176:179], a[136:139], v[84:87]\n" \
R_"a[196:199], v29 offset:2048\n" \
"s_add_i32 s5, s15, s5\n" \
M_"v[84:87], a[240:243], a[200:203], v[84:87]\n" \
"s_add_u32 m0, s31, 0x8000\n" \
"s_mov_b32 s84, 0x380000\n" \
M_"v[88:91], a[180:183], a[136:139], v[88:91]\n" \
B_"v26, s[68:71], s84 offen offset:0 lds\n" \
M_"v[88:91], a[244:247], a[200:203], v[88:91]\n" \
"s_add_u32 s89, s42, 0x0\n" \
"buffer_store_dwordx4 v[184:187], v20, s[80:83], s89 offen offset:1024 sc1\n" \
M_"v[92:95], a[184:187], a[136:139], v[92:95]\n" \
X_"a[228:229], v33 offset:4096\n" \
"s_bitset1_b32 s5, 12\n" \
M_"v[92:95], a[248:251], a[200:203], v[92:95]\n" \
X_"a[230:231], v33 offset:4608\n" \
"v_writelane_b32 v11, s5, 51\n" \
M_"v[96:99], a[188:191], a[136:139], v[96:99]\n" \
"s_and_b32 s5, s4, 0x70000\n" \
"s_xor_b32 s5, s5, 0x40000\n" \
M_"v[96:99], a[252:255], a[200:203], v[96:99]\n" \
R_"a[136:139], v28 offset:4096\n" \
"s_add_i32 s5, s15, s5\n" \
M_"v[112:115], a[188:191], a[140:143], v[112:115]\n" \
R_"a[200:203], v29 offset:4096\n" \
"s_or_b32 s5, s5, 0x1800\n" \
M_"v[112:115], a[252:255], a[204:207], v[112:115]\n" \
"s_addk_i32 s84, 0x7c00\n" \
M_"v[108:111], a[184:187], a[140:143], v[108:111]\n" \
B_"v26, s[68:71], s84 offen offset:1024 lds\n" \
M_"v[108:111], a[248:251], a[204:207], v[108:111]\n" \
X_"a[172:173], v35 offset:0\n" \
"v_writelane_b32 v11, s5, 52\n" \
M_"v[104:107], a[180:183], a[140:143], v[104:107]\n" \
"s_addk_i32 s84, 0x7c00\n" \
M_"v[104:107], a[244:247], a[204:207], v[104:107]\n" \
B_"v27, s[68:71], s84 offen offset:2048 lds\n" \
M_"v[100:103], a[176:179], a[140:143], v[100:103]\n" \
X_"a[174:175], v35 offset:512\n" \
"s_add_i32 s5, s4, 0x50000\n" \
M_"v[100:103], a[240:243], a[204:207], v[100:103]\n" \
R_"a[140:143], v28 offset:6144\n" \
"s_and_b32 s5, s5, 0x70000\n" \
M_"v[116:119], a[176:179], a[144:147], v[116:119]\n" \
R_"a[204:207], v29 offset:6144\n" \
"s_add_i32 s5, s15, s5\n" \
M_"v[116:119], a[240:243], a[208:211], v[116:119]\n" \
"s_addk_i32 s84, 0x7c00\n" \
M_"v[120:123], a[180:183], a[144:147], v[120:123]\n" \
B_"v27, s[68:71], s84 offen offset:3072 lds\n" \
M_"v[120:123], a[244:247], a[208:211], v[120:123]\n" \
X_"a[236:237], v35 offset:4096\n" \
"s_bitset1_b32 s5, 13\n" \
M_"v[124:127], a[184:187], a[144:147], v[124:127]\n" \
X_"a[238:239], v35 offset:4608\n" \
"v_writelane_b32 v11, s5, 53\n" \
M_"v[124:127], a[248:251], a[208:211], v[124:127]\n" \
"s_add_u32 m0, s30, 0x20000\n" \
"s_movk_i32 s84, 0x400\n" \
M_"v[128:131], a[188:191], a[144:147], v[128:131]\n" \
B_"v24, s[76:79], s84 offen offset:0 lds\n" \
M_"v[128:131], a[252:255], a[208:211], v[128:131]\n" \
R_"a[144:147], v28 offset:8192\n" \
"s_add_i32 s5, s4, 0x60000\n" \
M_"v[144:147], a[188:191], a[148:151], v[144:147]\n" \
R_"a[208:211], v29 offset:8192\n" \
"s_and_b32 s5, s5, 0x70000\n" \
M_"v[144:147], a[252:255], a[212:215], v[144:147]\n" \
X_"a[168:169], v34 offset:0\n" \
"s_add_i32 s4, s4, 0x70000\n" \
M_"v[140:143], a[184:187], a[148:151], v[140:143]\n" \
"s_movk_i32 s84, 0x2400\n" \
M_"v[140:143], a[248:251], a[212:215], v[140:143]\n" \
B_"v25, s[76:79], s84 offen offset:1024 lds\n" \
M_"v[136:139], a[180:183], a[148:151], v[136:139]\n" \
X_"a[170:171], v34 offset:512\n" \
"s_add_i32 s5, s15, s5\n" \
M_"v[136:139], a[244:247], a[212:215], v[136:139]\n" \
X_"a[232:233], v34 offset:4096\n" \
"s_and_b32 s4, s4, 0x70000\n" \
M_"v[132:135], a[176:179], a[148:151], v[132:135]\n" \
X_"a[234:235], v34 offset:4608\n" \
"s_or_b32 s5, s5, 0x2800\n" \
M_"v[132:135], a[240:243], a[212:215], v[132:135]\n" \
R_"a[148:151], v28 offset:10240\n" \
"v_writelane_b32 v11, s5, 54\n" \
M_"v[148:151], a[176:179], a[152:155], v[148:151]\n" \
R_"a[212:215], v29 offset:10240\n" \
"s_add_i32 s15, s15, s4\n" \
M_"v[148:151], a[240:243], a[216:219], v[148:151]\n" \
"s_movk_i32 s84, 0x4400\n" \
M_"v[152:155], a[180:183], a[152:155], v[152:155]\n" \
B_"v24, s[76:79], s84 offen offset:2048 lds\n" \
M_"v[152:155], a[244:247], a[216:219], v[152:155]\n" \
"s_movk_i32 s84, 0x6400\n" \
M_"v[156:159], a[184:187], a[152:155], v[156:159]\n" \
B_"v25, s[76:79], s84 offen offset:3072 lds\n" \
M_"v[156:159], a[248:251], a[216:219], v[156:159]\n" \
R_"v[248:251], v22 offset:0\n" \
"s_or_b32 s4, s15, 0x3000\n" \
M_"v[160:163], a[188:191], a[152:155], v[160:163]\n" \
R_"v[48:51], v23 offset:0\n" \
"v_writelane_b32 v11, s4, 55\n" \
M_"v[160:163], a[252:255], a[216:219], v[160:163]\n" \
R_"a[152:155], v28 offset:12288\n" \
"s_movk_i32 s11, 0x4000\n" \
M_"v[176:179], a[188:191], a[156:159], v[176:179]\n" \
R_"a[216:219], v29 offset:12288\n" \
"s_lshl_b32 s4, s2, 2\n" \
M_"v[176:179], a[252:255], a[220:223], v[176:179]\n" \
"s_and_b32 s4, s4, 28\n" \
"s_bfe_u32 s5, s2, 0x20003\n" \
M_"v[172:175], a[184:187], a[156:159], v[172:175]\n" \
"s_or_b32 s4, s4, s5\n" \
"s_lshr_b32 s5, s2, 5\n" \
M_"v[172:175], a[248:251], a[220:223], v[172:175]\n" \
"s_lshr_b32 s19, s3, 1\n" \
"s_lshl_b32 s18, s5, 1\n" \
M_"v[168:171], a[180:183], a[156:159], v[168:171]\n" \
"s_lshl_b32 s6, s4, 20\n" \
"s_lshl_b32 s7, s4, 8\n" \
M_"v[168:171], a[244:247], a[220:223], v[168:171]\n" \
"s_and_b32 s13, s3, 1\n" \
"s_add_i32 s14, s18, s19\n" \
M_"v[164:167], a[176:179], a[156:159], v[164:167]\n" \
"s_add_u32 s6, s26, s6\n" \
"s_addc_u32 s9, s27, 0\n" \
M_"v[164:167], a[240:243], a[220:223], v[164:167]\n" \
R_"a[156:159], v28 offset:14336\n" \
R_"a[220:223], v29 offset:14336\n" \
"s_waitcnt lgkmcnt(15)\n" \
M_"a[0:3], a[160:163], a[128:131], a[0:3]\n" \
"s_add_u32 m0, s30, 0x21000\n" \
"s_addk_i32 s84, 0x3000\n" \
M_"a[0:3], a[224:227], a[192:195], a[0:3]\n" \
B_"v24, s[76:79], s84 offen offset:0 lds\n" \
M_"a[4:7], a[164:167], a[128:131], a[4:7]\n" \
X_"a[176:177], v36 offset:0\n" \
M_"a[4:7], a[228:231], a[192:195], a[4:7]\n" \
X_"a[178:179], v36 offset:1024\n" \
M_"a[12:15], a[172:175], a[128:131], a[12:15]\n" \
X_"a[240:241], v36 offset:8192\n" \
"s_waitcnt lgkmcnt(15)\n" \
M_"a[12:15], a[236:239], a[192:195], a[12:15]\n" \
"s_add_u32 s88, s42, 0x1000\n" \
"buffer_store_dwordx4 v[188:191], v20, s[80:83], s88 offen offset:1024 sc1\n" \
"s_waitcnt lgkmcnt(13)\n" \
M_"a[8:11], a[128:131], a[168:171], a[8:11]\n" \
X_"a[242:243], v36 offset:9216\n" \
"s_waitcnt lgkmcnt(12)\n" \
M_"a[8:11], a[192:195], a[232:235], a[8:11]\n" \
X_"a[180:181], v37 offset:0\n" \
M_"a[24:27], a[132:135], a[168:171], a[24:27]\n" \
X_"a[182:183], v37 offset:1024\n" \
M_"a[24:27], a[196:199], a[232:235], a[24:27]\n" \
X_"a[244:245], v37 offset:8192\n" \
M_"a[28:31], a[172:175], a[132:135], a[28:31]\n" \
"s_add_u32 s8, s6, 0x1000000\n" \
"s_addc_u32 s6, s9, 0\n" \
M_"a[28:31], a[236:239], a[196:199], a[28:31]\n" \
"s_addk_i32 s84, 0x2000\n" \
M_"a[20:23], a[164:167], a[132:135], a[20:23]\n" \
B_"v25, s[76:79], s84 offen offset:1024 lds\n" \
M_"a[20:23], a[228:231], a[196:199], a[20:23]\n" \
X_"a[246:247], v37 offset:9216\n" \
"s_and_b32 s9, s6, 0xffff\n" \
M_"a[16:19], a[160:163], a[132:135], a[16:19]\n" \
X_"a[184:185], v38 offset:0\n" \
"s_lshl_b32 s6, s5, 16\n" \
M_"a[16:19], a[224:227], a[196:199], a[16:19]\n" \
X_"a[186:187], v38 offset:1024\n" \
"s_sub_i32 s10, 0x100000, s6\n" \
M_"a[32:35], a[160:163], a[136:139], a[32:35]\n" \
"s_addk_i32 s84, 0x2000\n" \
"s_nop 0\n" \
M_"a[32:35], a[224:227], a[200:203], a[32:35]\n" \
B_"v24, s[76:79], s84 offen offset:2048 lds\n" \
M_"a[36:39], a[164:167], a[136:139], a[36:39]\n" \
X_"a[248:249], v38 offset:8192\n" \
"s_mov_b32 s6, 0x8000\n" \
M_"a[36:39], a[228:231], a[200:203], a[36:39]\n" \
X_"a[250:251], v38 offset:9216\n" \
M_"a[44:47], a[172:175], a[136:139], a[44:47]\n" \
X_"a[188:189], v39 offset:0\n" \
M_"a[44:47], a[236:239], a[200:203], a[44:47]\n" \
X_"a[190:191], v39 offset:1024\n" \
M_"a[40:43], a[136:139], a[168:171], a[40:43]\n" \
"s_add_u32 s89, s42, 0x2000\n" \
"buffer_store_dwordx4 v[192:195], v20, s[80:83], s89 offen offset:0 sc1\n" \
M_"a[40:43], a[200:203], a[232:235], a[40:43]\n" \
X_"a[252:253], v39 offset:8192\n" \
M_"a[56:59], a[140:143], a[168:171], a[56:59]\n" \
X_"a[254:255], v39 offset:9216\n" \
M_"a[56:59], a[204:207], a[232:235], a[56:59]\n" \
"s_cmp_lg_u32 s5, 0\n" \
"s_cselect_b32 s17, s11, s6\n" \
M_"a[60:63], a[172:175], a[140:143], a[60:63]\n" \
"s_lshl_b32 s6, s3, 1\n" \
"s_lshl_b32 s5, s5, 3\n" \
M_"a[60:63], a[236:239], a[204:207], a[60:63]\n" \
".fill (L_pb + 32480 - .) / 4, 4, 0xBF800000\n" \
"L_pp8:\n" \
"s_cbranch_execz L_pret\n" \
"s_addk_i32 s84, 0x2000\n" \
M_"a[52:55], a[164:167], a[140:143], a[52:55]\n" \
B_"v25, s[76:79], s84 offen offset:3072 lds\n" \
M_"a[52:55], a[228:231], a[204:207], a[52:55]\n" \
"s_and_b32 s6, s6, 4\n" \
"s_or_b32 s12, s5, s6\n" \
M_"a[48:51], a[160:163], a[140:143], a[48:51]\n" \
"s_sub_i32 s5, s13, s5\n" \
"s_or_b32 s5, s5, s6\n" \
M_"a[48:51], a[224:227], a[204:207], a[48:51]\n" \
"s_or_b32 s6, s12, s13\n" \
"s_lshl_b32 s4, s4, 2\n" \
"s_waitcnt lgkmcnt(15)\n" \
M_"a[64:67], a[164:167], v[248:251], a[64:67]\n" \
"s_mulk_i32 s6, 0x92\n" \
"s_add_i32 s4, s4, 18\n" \
M_"a[64:67], a[228:231], v[48:51], a[64:67]\n" \
"s_mulk_i32 s5, 0x92\n" \
"s_add_i32 s6, s4, s6\n" \
M_"a[80:83], a[172:175], v[248:251], a[80:83]\n" \
"s_add_i32 s5, s5, s4\n" \
"s_lshl_b32 s4, s3, 11\n" \
M_"a[80:83], a[236:239], v[48:51], a[80:83]\n" \
"s_lshl_b32 s15, s13, 12\n" \
"s_lshl_b32 s16, s19, 14\n" \
M_"a[68:71], v[248:251], a[168:171], a[68:71]\n" \
"s_mul_i32 s19, s19, 0xe000\n" \
"s_mov_b32 s11, 0x20000\n" \
"s_waitcnt vmcnt(10) lgkmcnt(0)\n" \
M_"a[68:71], v[48:51], a[232:235], a[68:71]\n" \
"s_barrier\n" \
M_"v[52:55], a[176:179], a[128:131], v[52:55]\n" \
"s_add_u32 m0, s40, 0x0\n" \
"s_add_u32 s85, s39, 0x10000\n" \
M_"v[52:55], a[240:243], a[192:195], v[52:55]\n" \
B_"v20, s[80:83], s85 offen sc1 lds\n" \
M_"v[56:59], a[180:183], a[128:131], v[56:59]\n" \
X_"a[160:161], v32 offset:32768\n" \
"s_or_b32 s12, s4, 0xc000\n" \
M_"v[56:59], a[244:247], a[192:195], v[56:59]\n" \
X_"a[162:163], v32 offset:33280\n" \
"s_lshl_b32 s4, s3, 8\n" \
M_"v[60:63], a[184:187], a[128:131], v[60:63]\n" \
"s_or_b32 s22, s15, s16\n" \
"s_or_b32 s23, s19, s15\n" \
M_"v[60:63], a[248:251], a[192:195], v[60:63]\n" \
X_"a[224:225], v32 offset:36864\n" \
"s_lshl_b32 s60, s6, 10\n" \
M_"v[64:67], a[188:191], a[128:131], v[64:67]\n" \
"s_lshl_b32 s61, s5, 10\n" \
"s_lshl_b32 s14, s14, 15\n" \
M_"v[64:67], a[252:255], a[192:195], v[64:67]\n" \
R_"a[128:131], v28 offset:32768\n" \
"s_or_b32 s20, s4, 0x26c00\n" \
M_"v[80:83], a[188:191], a[132:135], v[80:83]\n" \
R_"a[192:195], v29 offset:32768\n" \
"s_or_b32 s13, s22, 0x8000\n" \
M_"v[80:83], a[252:255], a[196:199], v[80:83]\n" \
"s_add_u32 m0, s40, 0x1000\n" \
"s_add_u32 s85, s39, 0x11000\n" \
M_"v[76:79], a[184:187], a[132:135], v[76:79]\n" \
B_"v20, s[80:83], s85 offen sc1 lds\n" \
M_"v[76:79], a[248:251], a[196:199], v[76:79]\n" \
X_"a[226:227], v32 offset:37376\n" \
"s_add_i32 s22, s22, 0x14000\n" \
M_"v[72:75], a[180:183], a[132:135], v[72:75]\n" \
X_"a[164:165], v33 offset:32768\n" \
"s_bitset1_b32 s23, 16\n" \
M_"v[72:75], a[244:247], a[196:199], v[72:75]\n" \
"s_or_b32 s24, s7, 0x5be8020\n" \
"s_and_b32 s57, s27, 0xffff\n" \
M_"v[68:71], a[176:179], a[132:135], v[68:71]\n" \
X_"a[166:167], v33 offset:33280\n" \
"s_mov_b32 s58, 0x6000000\n" \
M_"v[68:71], a[240:243], a[196:199], v[68:71]\n" \
R_"a[132:135], v28 offset:34816\n" \
"s_mov_b32 s56, s26\n" \
M_"v[84:87], a[176:179], a[136:139], v[84:87]\n" \
R_"a[196:199], v29 offset:34816\n" \
"s_mov_b32 s59, s11\n" \
M_"v[84:87], a[240:243], a[200:203], v[84:87]\n" \
"s_add_u32 m0, s40, 0x3000\n" \
"s_movk_i32 s84, 0x400\n" \
M_"v[88:91], a[180:183], a[136:139], v[88:91]\n" \
B_"v21, s[76:79], s84 offen offset:0 lds\n" \
M_"v[88:91], a[244:247], a[200:203], v[88:91]\n" \
X_"a[228:229], v33 offset:36864\n" \
"s_add_i32 s60, s60, 0x3000000\n" \
M_"v[92:95], a[184:187], a[136:139], v[92:95]\n" \
X_"a[230:231], v33 offset:37376\n" \
"s_add_i32 s61, s61, 0x411c000\n" \
M_"v[92:95], a[248:251], a[200:203], v[92:95]\n" \
"s_add_u32 s88, s42, 0x2000\n" \
"buffer_store_dwordx4 v[216:219], v20, s[80:83], s88 offen offset:1024 sc1\n" \
M_"v[96:99], a[188:191], a[136:139], v[96:99]\n" \
"s_sub_i32 s19, 14, s18\n" \
"s_nop 0\n" \
"s_mov_b32 s21, 0x71800000\n" \
M_"v[96:99], a[252:255], a[200:203], v[96:99]\n" \
R_"a[136:139], v28 offset:36864\n" \
"s_mov_b32 s4, L_gsw-L_gtr\n" \
M_"v[112:115], a[188:191], a[140:143], v[112:115]\n" \
R_"a[200:203], v29 offset:36864\n" \
M_"v[112:115], a[252:255], a[204:207], v[112:115]\n" \
X_"a[172:173], v35 offset:32768\n" \
M_"v[108:111], a[184:187], a[140:143], v[108:111]\n" \
X_"a[174:175], v35 offset:33280\n" \
M_"v[108:111], a[248:251], a[204:207], v[108:111]\n" \
"s_cmp_eq_u32 s19, 0\n" \
"s_cselect_b32 s4, L_gs7-L_gtr, s4\n" \
M_"v[104:107], a[180:183], a[140:143], v[104:107]\n" \
"s_add_u32 m0, s31, 0x0\n" \
"s_mov_b32 s84, 0x400000\n" \
M_"v[104:107], a[244:247], a[204:207], v[104:107]\n" \
B_"v26, s[68:71], s84 offen offset:0 lds\n" \
M_"v[100:103], a[176:179], a[140:143], v[100:103]\n" \
X_"a[236:237], v35 offset:36864\n" \
M_"v[100:103], a[240:243], a[204:207], v[100:103]\n" \
R_"a[140:143], v28 offset:38912\n" \
M_"v[116:119], a[176:179], a[144:147], v[116:119]\n" \
R_"a[204:207], v29 offset:38912\n" \
M_"v[116:119], a[240:243], a[208:211], v[116:119]\n" \
X_"a[238:239], v35 offset:37376\n" \
M_"v[120:123], a[180:183], a[144:147], v[120:123]\n" \
X_"a[168:169], v34 offset:32768\n" \
M_"v[120:123], a[244:247], a[208:211], v[120:123]\n" \
"s_addk_i32 s84, 0x7c00\n" \
M_"v[124:127], a[184:187], a[144:147], v[124:127]\n" \
B_"v26, s[68:71], s84 offen offset:1024 lds\n" \
M_"v[124:127], a[248:251], a[208:211], v[124:127]\n" \
X_"a[170:171], v34 offset:33280\n" \
M_"v[128:131], a[188:191], a[144:147], v[128:131]\n" \
"s_cmp_eq_u32 s16, 0\n" \
"s_cselect_b32 s4, 0, s4\n" \
M_"v[128:131], a[252:255], a[208:211], v[128:131]\n" \
R_"a[144:147], v28 offset:40960\n" \
M_"v[144:147], a[188:191], a[148:151], v[144:147]\n" \
R_"a[208:211], v29 offset:40960\n" \
M_"v[144:147], a[252:255], a[212:215], v[144:147]\n" \
X_"a[232:233], v34 offset:36864\n" \
M_"v[140:143], a[184:187], a[148:151], v[140:143]\n" \
"s_addk_i32 s84, 0x7c00\n" \
M_"v[140:143], a[248:251], a[212:215], v[140:143]\n" \
B_"v27, s[68:71], s84 offen offset:2048 lds\n" \
M_"v[136:139], a[180:183], a[148:151], v[136:139]\n" \
X_"a[234:235], v34 offset:37376\n" \
M_"v[136:139], a[244:247], a[212:215], v[136:139]\n" \
"s_getpc_b64 s[28:29]\n" \
"L_gp0_%=:\n" \
"s_add_u32 s28, s28, L_gtr-L_gp0_%=\n" \
"s_addc_u32 s29, s29, 0\n" \
M_"v[132:135], a[176:179], a[148:151], v[132:135]\n" \
R_"v[248:251], v22 offset:32768\n" \
M_"v[132:135], a[240:243], a[212:215], v[132:135]\n" \
R_"a[148:151], v28 offset:43008\n" \
M_"v[148:151], a[176:179], a[152:155], v[148:151]\n" \
R_"a[212:215], v29 offset:43008\n" \
M_"v[148:151], a[240:243], a[216:219], v[148:151]\n" \
"s_add_u32 m0, s31, 0x0\n" \
"s_mov_b32 s84, 0x417400\n" \
M_"v[152:155], a[180:183], a[152:155], v[152:155]\n" \
B_"v27, s[68:71], s84 offen offset:3072 lds\n" \
M_"v[152:155], a[244:247], a[216:219], v[152:155]\n" \
R_"v[48:51], v23 offset:32768\n" \
M_"v[156:159], a[184:187], a[152:155], v[156:159]\n" \
"s_add_u32 s28, s28, s4\n" \
"s_addc_u32 s29, s29, 0\n" \
"s_nop 0\n" \
M_"v[156:159], a[248:251], a[216:219], v[156:159]\n" \
"v_accvgpr_write_b32 a96, 0\n" \
"v_accvgpr_write_b32 a97, 0\n" \
M_"v[160:163], a[188:191], a[152:155], v[160:163]\n" \
"v_accvgpr_write_b32 a98, 0\n" \
"v_accvgpr_write_b32 a99, 0\n" \
M_"v[160:163], a[252:255], a[216:219], v[160:163]\n" \
R_"a[152:155], v28 offset:45056\n" \
"v_accvgpr_write_b32 a100, 0\n" \
M_"v[176:179], a[188:191], a[156:159], v[176:179]\n" \
R_"a[216:219], v29 offset:45056\n" \
"v_accvgpr_write_b32 a101, 0\n" \
M_"v[176:179], a[252:255], a[220:223], v[176:179]\n" \
"v_accvgpr_write_b32 a102, 0\n" \
"v_accvgpr_write_b32 a103, 0\n" \
M_"v[172:175], a[184:187], a[156:159], v[172:175]\n" \
"v_accvgpr_write_b32 a112, 0\n" \
"v_accvgpr_write_b32 a113, 0\n" \
M_"v[172:175], a[248:251], a[220:223], v[172:175]\n" \
"v_accvgpr_write_b32 a114, 0\n" \
"v_accvgpr_write_b32 a115, 0\n" \
M_"v[168:171], a[180:183], a[156:159], v[168:171]\n" \
"v_accvgpr_write_b32 a120, 0\n" \
"v_accvgpr_write_b32 a121, 0\n" \
M_"v[168:171], a[244:247], a[220:223], v[168:171]\n" \
"v_accvgpr_write_b32 a122, 0\n" \
"v_accvgpr_write_b32 a123, 0\n" \
M_"v[164:167], a[176:179], a[156:159], v[164:167]\n" \
"v_mov_b32_e32 v180, 0x3f803f80\n" \
"s_mov_b32 s6, 0x33333333\n" \
M_"v[164:167], a[240:243], a[220:223], v[164:167]\n" \
R_"a[156:159], v28 offset:47104\n" \
R_"a[220:223], v29 offset:47104\n" \
"s_waitcnt lgkmcnt(15)\n" \
"s_nop 0\n" \
M_"a[0:3], a[160:163], a[128:131], a[0:3]\n" \
"s_add_u32 s89, s42, 0x3000\n" \
"buffer_store_dwordx4 v[220:223], v20, s[80:83], s89 offen offset:0 sc1\n" \
M_"a[0:3], a[224:227], a[192:195], a[0:3]\n" \
X_"a[176:177], v36 offset:32768\n" \
"s_mov_b32 s7, 0xcccccccc\n" \
M_"a[4:7], a[164:167], a[128:131], a[4:7]\n" \
X_"a[178:179], v36 offset:33792\n" \
"v_cndmask_b32_e64 v181, 0, v180, s[6:7]\n" \
M_"a[4:7], a[228:231], a[192:195], a[4:7]\n" \
X_"a[240:241], v36 offset:40960\n" \
"v_accvgpr_write_b32 a104, v181\n" \
M_"a[12:15], a[172:175], a[128:131], a[12:15]\n" \
X_"a[242:243], v36 offset:41984\n" \
"v_accvgpr_write_b32 a105, v181\n" \
"s_waitcnt lgkmcnt(15)\n" \
M_"a[12:15], a[236:239], a[192:195], a[12:15]\n" \
"v_accvgpr_write_b32 a106, v181\n" \
"v_accvgpr_write_b32 a107, v181\n" \
M_"a[8:11], a[128:131], a[168:171], a[8:11]\n" \
X_"a[180:181], v37 offset:32768\n" \
"s_mov_b32 s6, 0x11111111\n" \
"s_waitcnt lgkmcnt(13)\n" \
M_"a[8:11], a[192:195], a[232:235], a[8:11]\n" \
"s_mov_b32 s7, 0x11111111\n" \
"v_cndmask_b32_e64 v181, 0, v180, s[6:7]\n" \
M_"a[24:27], a[132:135], a[168:171], a[24:27]\n" \
X_"a[182:183], v37 offset:33792\n" \
"v_accvgpr_write_b32 a108, v181\n" \
M_"a[24:27], a[196:199], a[232:235], a[24:27]\n" \
X_"a[244:245], v37 offset:40960\n" \
"v_accvgpr_write_b32 a109, v181\n" \
M_"a[28:31], a[172:175], a[132:135], a[28:31]\n" \
"v_accvgpr_write_b32 a110, v181\n" \
"v_accvgpr_write_b32 a111, v181\n" \
M_"a[28:31], a[236:239], a[196:199], a[28:31]\n" \
X_"a[246:247], v37 offset:41984\n" \
"s_mov_b32 s6, 0x44444444\n" \
M_"a[20:23], a[164:167], a[132:135], a[20:23]\n" \
"s_mov_b32 s7, 0x44444444\n" \
"v_cndmask_b32_e64 v181, 0, v180, s[6:7]\n" \
M_"a[20:23], a[228:231], a[196:199], a[20:23]\n" \
X_"a[184:185], v38 offset:32768\n" \
"v_accvgpr_write_b32 a116, v181\n" \
M_"a[16:19], a[160:163], a[132:135], a[16:19]\n" \
X_"a[186:187], v38 offset:33792\n" \
"v_accvgpr_write_b32 a117, v181\n" \
M_"a[16:19], a[224:227], a[196:199], a[16:19]\n" \
"v_accvgpr_write_b32 a118, v181\n" \
"v_accvgpr_write_b32 a119, v181\n" \
M_"a[32:35], a[160:163], a[136:139], a[32:35]\n" \
"s_add_u32 s88, s42, 0x3000\n" \
"buffer_store_dwordx4 v[224:227], v20, s[80:83], s88 offen offset:1024 sc1\n" \
M_"a[32:35], a[224:227], a[200:203], a[32:35]\n" \
X_"a[248:249], v38 offset:40960\n" \
"v_bfe_i32 v181, v20, 4, 1\n" \
M_"a[36:39], a[164:167], a[136:139], a[36:39]\n" \
"v_lshlrev_b32_e32 v45, 3, v20\n" \
"v_add_u32_e32 v45, s30, v45\n" \
M_"a[36:39], a[228:231], a[200:203], a[36:39]\n" \
"s_getpc_b64 s[86:87]\n" \
"L_ct0_%=:\n" \
"s_add_u32 s86, s86, L_ap0-L_ct0_%=\n" \
"s_addc_u32 s87, s87, 0\n" \
"s_add_u32 m0, s40, 0x2000\n" \
M_"a[44:47], a[172:175], a[136:139], a[44:47]\n" \
"global_load_lds_dword v45, s[86:87]\n" \
M_"a[44:47], a[236:239], a[200:203], a[44:47]\n" \
X_"a[250:251], v38 offset:41984\n" \
"v_and_b32_e32 v181, 0xaaaaaaaa, v181\n" \
M_"a[40:43], a[136:139], a[168:171], a[40:43]\n" \
X_"a[188:189], v39 offset:32768\n" \
"v_or_b32_e32 v181, 0x44444444, v181\n" \
M_"a[40:43], a[200:203], a[232:235], a[40:43]\n" \
X_"a[190:191], v39 offset:33792\n" \
"v_accvgpr_write_b32 a126, v181\n" \
M_"a[56:59], a[140:143], a[168:171], a[56:59]\n" \
X_"a[252:253], v39 offset:40960\n" \
"v_bfe_u32 v180, v20, 5, 3\n" \
M_"a[56:59], a[204:207], a[232:235], a[56:59]\n" \
X_"a[254:255], v39 offset:41984\n" \
"v_bfe_u32 v181, v20, 8, 2\n" \
M_"a[60:63], a[172:175], a[140:143], a[60:63]\n" \
"v_lshlrev_b32_e32 v181, 1, v181\n" \
"v_sub_u32_e32 v180, v180, v181\n" \
M_"a[60:63], a[236:239], a[204:207], a[60:63]\n" \
"v_and_b32_e64 v181, 16, v20\n" \
"v_mov_b32_e32 v182, 0x3f80\n" \
M_"a[52:55], a[164:167], a[140:143], a[52:55]\n" \
"v_lshlrev_b32_e32 v181, v181, v182\n" \
"v_cmp_eq_u32_e32 vcc, 0, v180\n" \
M_"a[52:55], a[228:231], a[204:207], a[52:55]\n" \
"v_cmp_eq_u32_e64 s[6:7], 1, v180\n" \
"v_cndmask_b32_e64 v182, 0, v181, vcc\n" \
M_"a[48:51], a[160:163], a[140:143], a[48:51]\n" \
"v_cndmask_b32_e64 v183, 0, v181, s[6:7]\n" \
"v_accvgpr_write_b32 a124, v182\n" \
M_"a[48:51], a[224:227], a[204:207], a[48:51]\n" \
"v_accvgpr_write_b32 a125, v183\n" \
"s_waitcnt lgkmcnt(15)\n" \
M_"a[64:67], a[164:167], v[248:251], a[64:67]\n" \
M_"a[64:67], a[228:231], v[48:51], a[64:67]\n" \
M_"a[80:83], a[172:175], v[248:251], a[80:83]\n" \
M_"a[80:83], a[236:239], v[48:51], a[80:83]\n" \
M_"a[68:71], v[248:251], a[168:171], a[68:71]\n" \
"s_waitcnt vmcnt(3) lgkmcnt(0)\n" \
M_"a[68:71], v[48:51], a[232:235], a[68:71]\n" \
"s_barrier\n" \
"v_xor_b32_e32 v28, 0x20000, v28\n" \
"v_xor_b32_e32 v29, 0x20000, v29\n" \
M_"v[52:55], a[176:179], a[128:131], v[52:55]\n" \
X_"a[160:161], v32 offset:0\n" \
"v_accvgpr_read_b32 v184, a0\n" \
M_"v[52:55], a[240:243], a[192:195], v[52:55]\n" \
"v_accvgpr_read_b32 v185, a1\n" \
"v_accvgpr_read_b32 v186, a2\n" \
M_"v[56:59], a[180:183], a[128:131], v[56:59]\n" \
X_"a[162:163], v32 offset:512\n" \
"v_accvgpr_read_b32 v187, a3\n" \
M_"v[56:59], a[244:247], a[192:195], v[56:59]\n" \
"v_accvgpr_read_b32 v188, a4\n" \
"v_accvgpr_read_b32 v189, a5\n" \
M_"v[60:63], a[184:187], a[128:131], v[60:63]\n" \
"v_accvgpr_read_b32 v190, a6\n" \
"v_accvgpr_read_b32 v191, a7\n" \
M_"v[60:63], a[248:251], a[192:195], v[60:63]\n" \
X_"a[224:225], v32 offset:4096\n" \
"v_accvgpr_read_b32 v196, a12\n" \
M_"v[64:67], a[188:191], a[128:131], v[64:67]\n" \
"v_accvgpr_read_b32 v197, a13\n" \
"v_accvgpr_read_b32 v198, a14\n" \
M_"v[64:67], a[252:255], a[192:195], v[64:67]\n" \
R_"a[128:131], v28 offset:0\n" \
"v_accvgpr_read_b32 v199, a15\n" \
M_"v[80:83], a[188:191], a[132:135], v[80:83]\n" \
R_"a[192:195], v29 offset:0\n" \
"v_accvgpr_read_b32 v192, a8\n" \
M_"v[80:83], a[252:255], a[196:199], v[80:83]\n" \
X_"a[226:227], v32 offset:4608\n" \
"v_accvgpr_read_b32 v193, a9\n" \
M_"v[76:79], a[184:187], a[132:135], v[76:79]\n" \
"v_accvgpr_read_b32 v194, a10\n" \
"v_accvgpr_read_b32 v195, a11\n" \
M_"v[76:79], a[248:251], a[196:199], v[76:79]\n" \
X_"a[164:165], v33 offset:0\n" \
"v_accvgpr_read_b32 v208, a24\n" \
M_"v[72:75], a[180:183], a[132:135], v[72:75]\n" \
"v_accvgpr_read_b32 v209, a25\n" \
"v_accvgpr_read_b32 v210, a26\n" \
M_"v[72:75], a[244:247], a[196:199], v[72:75]\n" \
"v_accvgpr_read_b32 v211, a27\n" \
"v_accvgpr_read_b32 v212, a28\n" \
M_"v[68:71], a[176:179], a[132:135], v[68:71]\n" \
X_"a[166:167], v33 offset:512\n" \
"v_accvgpr_read_b32 v213, a29\n" \
M_"v[68:71], a[240:243], a[196:199], v[68:71]\n" \
R_"a[132:135], v28 offset:2048\n" \
"v_accvgpr_read_b32 v214, a30\n" \
M_"v[84:87], a[176:179], a[136:139], v[84:87]\n" \
R_"a[196:199], v29 offset:2048\n" \
"v_accvgpr_read_b32 v215, a31\n" \
M_"v[84:87], a[240:243], a[200:203], v[84:87]\n" \
X_"a[228:229], v33 offset:4096\n" \
"v_accvgpr_read_b32 v204, a20\n" \
M_"v[88:91], a[180:183], a[136:139], v[88:91]\n" \
"v_accvgpr_read_b32 v205, a21\n" \
"v_accvgpr_read_b32 v206, a22\n" \
M_"v[88:91], a[244:247], a[200:203], v[88:91]\n" \
"v_accvgpr_read_b32 v207, a23\n" \
"v_accvgpr_read_b32 v200, a16\n" \
M_"v[92:95], a[184:187], a[136:139], v[92:95]\n" \
X_"a[230:231], v33 offset:4608\n" \
"v_accvgpr_read_b32 v201, a17\n" \
M_"v[92:95], a[248:251], a[200:203], v[92:95]\n" \
"v_accvgpr_read_b32 v202, a18\n" \
"v_accvgpr_read_b32 v203, a19\n" \
M_"v[96:99], a[188:191], a[136:139], v[96:99]\n" \
"v_accvgpr_read_b32 v216, a32\n" \
"v_accvgpr_read_b32 v217, a33\n" \
M_"v[96:99], a[252:255], a[200:203], v[96:99]\n" \
R_"a[136:139], v28 offset:4096\n" \
"v_accvgpr_read_b32 v218, a34\n" \
M_"v[112:115], a[188:191], a[140:143], v[112:115]\n" \
R_"a[200:203], v29 offset:4096\n" \
"v_accvgpr_read_b32 v219, a35\n" \
M_"v[112:115], a[252:255], a[204:207], v[112:115]\n" \
X_"a[172:173], v35 offset:0\n" \
"v_accvgpr_read_b32 v220, a36\n" \
M_"v[108:111], a[184:187], a[140:143], v[108:111]\n" \
X_"a[174:175], v35 offset:512\n" \
"v_accvgpr_read_b32 v221, a37\n" \
M_"v[108:111], a[248:251], a[204:207], v[108:111]\n" \
"v_accvgpr_read_b32 v222, a38\n" \
"v_accvgpr_read_b32 v223, a39\n" \
M_"v[104:107], a[180:183], a[140:143], v[104:107]\n" \
X_"a[236:237], v35 offset:4096\n" \
"v_accvgpr_read_b32 v228, a44\n" \
M_"v[104:107], a[244:247], a[204:207], v[104:107]\n" \
"v_accvgpr_read_b32 v229, a45\n" \
"v_accvgpr_read_b32 v230, a46\n" \
M_"v[100:103], a[176:179], a[140:143], v[100:103]\n" \
"v_accvgpr_read_b32 v231, a47\n" \
"v_accvgpr_read_b32 v224, a40\n" \
M_"v[100:103], a[240:243], a[204:207], v[100:103]\n" \
R_"a[140:143], v28 offset:6144\n" \
"v_accvgpr_read_b32 v225, a41\n" \
M_"v[116:119], a[176:179], a[144:147], v[116:119]\n" \
R_"a[204:207], v29 offset:6144\n" \
"v_accvgpr_read_b32 v226, a42\n" \
M_"v[116:119], a[240:243], a[208:211], v[116:119]\n" \
X_"a[238:239], v35 offset:4608\n" \
"v_accvgpr_read_b32 v227, a43\n" \
M_"v[120:123], a[180:183], a[144:147], v[120:123]\n" \
X_"a[168:169], v34 offset:0\n" \
"v_accvgpr_read_b32 v240, a56\n" \
M_"v[120:123], a[244:247], a[208:211], v[120:123]\n" \
"v_accvgpr_read_b32 v241, a57\n" \
"v_accvgpr_read_b32 v242, a58\n" \
M_"v[124:127], a[184:187], a[144:147], v[124:127]\n" \
"v_accvgpr_read_b32 v243, a59\n" \
"v_accvgpr_read_b32 v244, a60\n" \
M_"v[124:127], a[248:251], a[208:211], v[124:127]\n" \
X_"a[170:171], v34 offset:512\n" \
"v_accvgpr_read_b32 v245, a61\n" \
M_"v[128:131], a[188:191], a[144:147], v[128:131]\n" \
"v_accvgpr_read_b32 v246, a62\n" \
"v_accvgpr_read_b32 v247, a63\n" \
M_"v[128:131], a[252:255], a[208:211], v[128:131]\n" \
R_"a[144:147], v28 offset:8192\n" \
"v_accvgpr_read_b32 v236, a52\n" \
M_"v[144:147], a[188:191], a[148:151], v[144:147]\n" \
R_"a[208:211], v29 offset:8192\n" \
"v_accvgpr_read_b32 v237, a53\n" \
M_"v[144:147], a[252:255], a[212:215], v[144:147]\n" \
X_"a[232:233], v34 offset:4096\n" \
"v_accvgpr_read_b32 v238, a54\n" \
M_"v[140:143], a[184:187], a[148:151], v[140:143]\n" \
"v_accvgpr_read_b32 v239, a55\n" \
"v_accvgpr_read_b32 v232, a48\n" \
M_"v[140:143], a[248:251], a[212:215], v[140:143]\n" \
X_"a[234:235], v34 offset:4608\n" \
"v_accvgpr_read_b32 v233, a49\n" \
M_"v[136:139], a[180:183], a[148:151], v[136:139]\n" \
"v_accvgpr_read_b32 v234, a50\n" \
"v_accvgpr_read_b32 v235, a51\n" \
M_"v[136:139], a[244:247], a[212:215], v[136:139]\n" \
"v_accvgpr_read_b32 v180, a64\n" \
"v_accvgpr_read_b32 v181, a65\n" \
M_"v[132:135], a[176:179], a[148:151], v[132:135]\n" \
R_"v[248:251], v22 offset:0\n" \
"v_accvgpr_read_b32 v182, a66\n" \
M_"v[132:135], a[240:243], a[212:215], v[132:135]\n" \
R_"a[148:151], v28 offset:10240\n" \
"v_accvgpr_read_b32 v183, a67\n" \
M_"v[148:151], a[176:179], a[152:155], v[148:151]\n" \
R_"a[212:215], v29 offset:10240\n" \
"v_accvgpr_read_b32 v252, a68\n" \
M_"v[148:151], a[240:243], a[216:219], v[148:151]\n" \
R_"v[48:51], v23 offset:0\n" \
"v_accvgpr_read_b32 v253, a69\n" \
M_"v[152:155], a[180:183], a[152:155], v[152:155]\n" \
"v_accvgpr_read_b32 v254, a70\n" \
"v_accvgpr_read_b32 v255, a71\n" \
M_"v[152:155], a[244:247], a[216:219], v[152:155]\n" \
"v_lshrrev_b32_e32 v24, 9, v20\n" \
"v_and_b32_e32 v26, 0x1ff, v20\n" \
M_"v[156:159], a[184:187], a[152:155], v[156:159]\n" \
"v_lshl_add_u32 v24, v24, 3, v26\n" \
"v_lshrrev_b32_e64 v25, 8, v20\n" \
M_"v[156:159], a[248:251], a[216:219], v[156:159]\n" \
"v_mul_u32_u24_e32 v25, 0x24700, v25\n" \
"v_add_u32_e64 v25, v25, v20\n" \
M_"v[160:163], a[188:191], a[152:155], v[160:163]\n" \
"v_lshrrev_b32_e32 v26, 2, v20\n" \
"v_add_u32_e32 v26, s45, v26\n" \
M_"v[160:163], a[252:255], a[216:219], v[160:163]\n" \
R_"a[152:155], v28 offset:12288\n" \
"v_add_u32_e64 v27, s46, v20\n" \
M_"v[176:179], a[188:191], a[156:159], v[176:179]\n" \
R_"a[216:219], v29 offset:12288\n" \
"v_add_u32_e64 v21, s43, v27\n" \
M_"v[176:179], a[252:255], a[220:223], v[176:179]\n" \
M_"v[172:175], a[184:187], a[156:159], v[172:175]\n" \
M_"v[172:175], a[248:251], a[220:223], v[172:175]\n" \
M_"v[168:171], a[180:183], a[156:159], v[168:171]\n" \
M_"v[168:171], a[244:247], a[220:223], v[168:171]\n" \
M_"v[164:167], a[176:179], a[156:159], v[164:167]\n" \
M_"v[164:167], a[240:243], a[220:223], v[164:167]\n" \
R_"a[156:159], v28 offset:14336\n" \
R_"a[220:223], v29 offset:14336\n" \
"s_waitcnt lgkmcnt(15)\n" \
"s_nop 0\n" \
M_"v[184:187], a[160:163], a[128:131], v[184:187]\n" \
"v_bfe_u32 v44, v20, 2, 5\n" \
X_"a[176:177], v36 offset:0\n" \
M_"v[184:187], a[224:227], a[192:195], v[184:187]\n" \
"buffer_load_dword v44, v44, s[80:83], s38 offen offset:0 sc1\n" \
M_"v[188:191], a[164:167], a[128:131], v[188:191]\n" \
X_"a[178:179], v36 offset:1024\n" \
M_"v[188:191], a[228:231], a[192:195], v[188:191]\n" \
X_"a[240:241], v36 offset:8192\n" \
M_"v[196:199], a[172:175], a[128:131], v[196:199]\n" \
X_"a[242:243], v36 offset:9216\n" \
"s_waitcnt lgkmcnt(15)\n" \
M_"v[196:199], a[236:239], a[192:195], v[196:199]\n" \
M_"v[192:195], a[128:131], a[168:171], v[192:195]\n" \
X_"a[180:181], v37 offset:0\n" \
"s_waitcnt lgkmcnt(13)\n" \
M_"v[192:195], a[192:195], a[232:235], v[192:195]\n" \
M_"v[208:211], a[132:135], a[168:171], v[208:211]\n" \
X_"a[182:183], v37 offset:1024\n" \
M_"v[208:211], a[196:199], a[232:235], v[208:211]\n" \
X_"a[244:245], v37 offset:8192\n" \
M_"v[212:215], a[172:175], a[132:135], v[212:215]\n" \
M_"v[212:215], a[236:239], a[196:199], v[212:215]\n" \
X_"a[246:247], v37 offset:9216\n" \
C_"v188, v188, v189\n" \
M_"v[204:207], a[164:167], a[132:135], v[204:207]\n" \
C_"v189, v190, v191\n" \
C_"v190, v198, v199\n" \
M_"v[204:207], a[228:231], a[196:199], v[204:207]\n" \
X_"a[184:185], v38 offset:0\n" \
"v_mov_b32_e32 v191, 0x3f80\n" \
M_"v[200:203], a[160:163], a[132:135], v[200:203]\n" \
X_"a[186:187], v38 offset:1024\n" \
C_"v184, v184, v185\n" \
M_"v[200:203], a[224:227], a[196:199], v[200:203]\n" \
C_"v185, v186, v187\n" \
C_"v186, v196, v197\n" \
M_"v[216:219], a[160:163], a[136:139], v[216:219]\n" \
X_"a[248:249], v38 offset:8192\n" \
"v_mov_b32_e64 v187, 0\n" \
M_"v[216:219], a[224:227], a[200:203], v[216:219]\n" \
X_"a[250:251], v38 offset:9216\n" \
M_"v[220:223], a[164:167], a[136:139], v[220:223]\n" \
M_"v[220:223], a[228:231], a[200:203], v[220:223]\n" \
X_"a[188:189], v39 offset:0\n" \
C_"v204, v204, v205\n" \
M_"v[228:231], a[172:175], a[136:139], v[228:231]\n" \
C_"v205, v206, v207\n" \
C_"v206, v214, v215\n" \
M_"v[228:231], a[236:239], a[200:203], v[228:231]\n" \
X_"a[190:191], v39 offset:1024\n" \
".fill (L_pb + 36576 - .) / 4, 4, 0xBF800000\n" \
"L_pp9:\n" \
"s_cbranch_execz L_pret\n" \
"v_mov_b32_e32 v207, 0x3f80\n" \
M_"v[224:227], a[136:139], a[168:171], v[224:227]\n" \
X_"a[252:253], v39 offset:8192\n" \
C_"v200, v200, v201\n" \
M_"v[224:227], a[200:203], a[232:235], v[224:227]\n" \
C_"v201, v202, v203\n" \
C_"v202, v212, v213\n" \
M_"v[240:243], a[140:143], a[168:171], v[240:243]\n" \
X_"a[254:255], v39 offset:9216\n" \
"v_mov_b32_e32 v203, 0\n" \
M_"v[240:243], a[204:207], a[232:235], v[240:243]\n" \
M_"v[244:247], a[172:175], a[140:143], v[244:247]\n" \
M_"v[244:247], a[236:239], a[204:207], v[244:247]\n" \
C_"v220, v220, v221\n" \
C_"v221, v222, v223\n" \
M_"v[236:239], a[164:167], a[140:143], v[236:239]\n" \
C_"v222, v230, v231\n" \
"v_mov_b32_e32 v223, 0x3f80\n" \
M_"v[236:239], a[228:231], a[204:207], v[236:239]\n" \
C_"v216, v216, v217\n" \
C_"v217, v218, v219\n" \
M_"v[232:235], a[160:163], a[140:143], v[232:235]\n" \
C_"v218, v228, v229\n" \
"v_mov_b32_e64 v219, 0\n" \
M_"v[232:235], a[224:227], a[204:207], v[232:235]\n" \
"s_waitcnt lgkmcnt(15)\n" \
"s_nop 0\n" \
M_"v[180:183], a[164:167], v[248:251], v[180:183]\n" \
M_"v[180:183], a[228:231], v[48:51], v[180:183]\n" \
M_"a[80:83], a[172:175], v[248:251], a[80:83]\n" \
M_"a[80:83], a[236:239], v[48:51], a[80:83]\n" \
C_"v236, v236, v237\n" \
C_"v237, v238, v239\n" \
M_"v[252:255], v[248:251], a[168:171], v[252:255]\n" \
C_"v238, v246, v247\n" \
"v_mov_b32_e32 v239, 0x3f80\n" \
M_"v[252:255], v[48:51], a[232:235], v[252:255]\n" \
C_"v232, v232, v233\n" \
C_"v233, v234, v235\n" \
"s_waitcnt vmcnt(0) lgkmcnt(0)\n" \
"s_barrier\n" \
"v_mov_b32_e32 v45, s62\n" \
"s_mov_b64 exec, 1\n" \
"buffer_store_dword v45, off, s[80:83], s37 offset:32 sc1\n" \
"s_mov_b64 exec, -1\n" \
"v_cmp_ne_u32_e32 vcc, s62, v44\n" \
"s_nop 4\n" \
"s_cbranch_vccnz L_poll160_%=\n" \
"L_pollr160_%=:\n" \
M_"v[52:55], a[176:179], a[128:131], v[52:55]\n" \
"s_mov_b32 s73, s50\n" \
"s_sub_u32 m0, s31, 0x10000\n" \
M_"v[52:55], a[240:243], a[192:195], v[52:55]\n" \
B_"v20, s[80:83], s73 offen offset:0 sc1 lds\n" \
M_"v[56:59], a[180:183], a[128:131], v[56:59]\n" \
"s_add_u32 s74, s50, 0x4000\n" \
"s_add_u32 s75, s50, 0x8000\n" \
M_"v[56:59], a[244:247], a[192:195], v[56:59]\n" \
"s_mov_b32 s70, 0x3e0293ee\n" \
"s_mov_b32 s76, s52\n" \
M_"v[60:63], a[184:187], a[128:131], v[60:63]\n" \
"v_mul_f32_e32 v52, s70, v52\n" \
"v_mul_f32_e32 v53, s70, v53\n" \
M_"v[60:63], a[248:251], a[192:195], v[60:63]\n" \
B_"v20, s[80:83], s73 offen offset:1024 sc1 lds\n" \
M_"v[64:67], a[188:191], a[128:131], v[64:67]\n" \
"v_mul_f32_e32 v54, s70, v54\n" \
"v_mul_f32_e32 v55, s70, v55\n" \
M_"v[64:67], a[252:255], a[192:195], v[64:67]\n" \
C_"v52, v52, v53\n" \
"v_mul_f32_e64 v56, s70, v56\n" \
M_"v[80:83], a[188:191], a[132:135], v[80:83]\n" \
"v_mul_f32_e32 v60, s70, v60\n" \
"v_mul_f32_e32 v61, s70, v61\n" \
M_"v[80:83], a[252:255], a[196:199], v[80:83]\n" \
B_"v20, s[80:83], s73 offen offset:2048 sc1 lds\n" \
M_"v[76:79], a[184:187], a[132:135], v[76:79]\n" \
"v_mul_f32_e32 v62, s70, v62\n" \
"v_mul_f32_e32 v63, s70, v63\n" \
M_"v[76:79], a[248:251], a[196:199], v[76:79]\n" \
C_"v60, v60, v61\n" \
"v_mul_f32_e64 v57, s70, v57\n" \
M_"v[72:75], a[180:183], a[132:135], v[72:75]\n" \
"v_mul_f32_e32 v58, s70, v58\n" \
"v_mul_f32_e32 v59, s70, v59\n" \
M_"v[72:75], a[244:247], a[196:199], v[72:75]\n" \
B_"v20, s[80:83], s73 offen offset:3072 sc1 lds\n" \
M_"v[68:71], a[176:179], a[132:135], v[68:71]\n" \
"s_sub_u32 m0, s31, s51\n" \
C_"v53, v54, v55\n" \
M_"v[68:71], a[240:243], a[196:199], v[68:71]\n" \
"v_mul_f32_e32 v64, s70, v64\n" \
"v_mul_f32_e64 v65, s70, v65\n" \
M_"v[84:87], a[176:179], a[136:139], v[84:87]\n" \
"v_mul_f32_e32 v66, s70, v66\n" \
"v_mul_f32_e32 v67, s70, v67\n" \
M_"v[84:87], a[240:243], a[200:203], v[84:87]\n" \
B_"v20, s[80:83], s74 offen offset:0 sc1 lds\n" \
M_"v[88:91], a[180:183], a[136:139], v[88:91]\n" \
C_"v61, v62, v63\n" \
C_"v54, v56, v57\n" \
M_"v[88:91], a[244:247], a[200:203], v[88:91]\n" \
C_"v55, v58, v59\n" \
C_"v62, v64, v65\n" \
M_"v[92:95], a[184:187], a[136:139], v[92:95]\n" \
C_"v63, v66, v67\n" \
"v_mul_f32_e64 v68, s70, v68\n" \
M_"v[92:95], a[248:251], a[200:203], v[92:95]\n" \
B_"v20, s[80:83], s74 offen offset:1024 sc1 lds\n" \
M_"v[96:99], a[188:191], a[136:139], v[96:99]\n" \
"ds_write_b128 v27, v[52:55] offset:0\n" \
M_"v[96:99], a[252:255], a[200:203], v[96:99]\n" \
"v_mul_f32_e32 v69, s70, v69\n" \
"v_mul_f32_e32 v76, s70, v76\n" \
M_"v[112:115], a[188:191], a[140:143], v[112:115]\n" \
"v_mul_f32_e32 v77, s70, v77\n" \
"v_mul_f32_e32 v70, s70, v70\n" \
M_"v[112:115], a[252:255], a[204:207], v[112:115]\n" \
B_"v20, s[80:83], s74 offen offset:2048 sc1 lds\n" \
M_"v[108:111], a[184:187], a[140:143], v[108:111]\n" \
"ds_write_b128 v27, v[60:63] offset:1024\n" \
M_"v[108:111], a[248:251], a[204:207], v[108:111]\n" \
"v_mul_f32_e64 v71, s70, v71\n" \
C_"v68, v68, v69\n" \
M_"v[104:107], a[180:183], a[140:143], v[104:107]\n" \
"v_mul_f32_e32 v78, s70, v78\n" \
"v_mul_f32_e32 v79, s70, v79\n" \
M_"v[104:107], a[244:247], a[204:207], v[104:107]\n" \
B_"v20, s[80:83], s74 offen offset:3072 sc1 lds\n" \
M_"v[100:103], a[176:179], a[140:143], v[100:103]\n" \
"s_sub_u32 m0, s31, 0x4000\n" \
C_"v76, v76, v77\n" \
M_"v[100:103], a[240:243], a[204:207], v[100:103]\n" \
"v_mul_f32_e32 v72, s70, v72\n" \
"v_mul_f32_e32 v73, s70, v73\n" \
M_"v[116:119], a[176:179], a[144:147], v[116:119]\n" \
"v_mul_f32_e32 v74, s70, v74\n" \
"v_mul_f32_e32 v75, s70, v75\n" \
M_"v[116:119], a[240:243], a[208:211], v[116:119]\n" \
B_"v20, s[80:83], s75 offen offset:0 sc1 lds\n" \
M_"v[120:123], a[180:183], a[144:147], v[120:123]\n" \
C_"v69, v70, v71\n" \
"v_mul_f32_e64 v80, s70, v80\n" \
M_"v[120:123], a[244:247], a[208:211], v[120:123]\n" \
"v_mul_f32_e32 v81, s70, v81\n" \
"v_mul_f32_e32 v82, s70, v82\n" \
M_"v[124:127], a[184:187], a[144:147], v[124:127]\n" \
"v_mul_f32_e64 v83, s70, v83\n" \
C_"v77, v78, v79\n" \
M_"v[124:127], a[248:251], a[208:211], v[124:127]\n" \
B_"v20, s[80:83], s75 offen offset:1024 sc1 lds\n" \
M_"v[128:131], a[188:191], a[144:147], v[128:131]\n" \
C_"v70, v72, v73\n" \
C_"v71, v74, v75\n" \
M_"v[128:131], a[252:255], a[208:211], v[128:131]\n" \
C_"v78, v80, v81\n" \
C_"v79, v82, v83\n" \
M_"v[144:147], a[188:191], a[148:151], v[144:147]\n" \
"ds_write_b128 v27, v[68:71] offset:4096\n" \
M_"v[144:147], a[252:255], a[212:215], v[144:147]\n" \
B_"v20, s[80:83], s75 offen offset:2048 sc1 lds\n" \
M_"v[140:143], a[184:187], a[148:151], v[140:143]\n" \
C_"v180, v180, v181\n" \
"v_accvgpr_read_b32 v50, a82\n" \
M_"v[140:143], a[248:251], a[212:215], v[140:143]\n" \
"ds_write_b128 v27, v[76:79] offset:5120\n" \
M_"v[136:139], a[180:183], a[148:151], v[136:139]\n" \
"v_accvgpr_read_b32 v51, a83\n" \
C_"v181, v182, v183\n" \
M_"v[136:139], a[244:247], a[212:215], v[136:139]\n" \
B_"v20, s[80:83], s75 offen offset:3072 sc1 lds\n" \
M_"v[132:135], a[176:179], a[148:151], v[132:135]\n" \
"s_mov_b32 m0, s53\n" \
C_"v182, v50, v51\n" \
M_"v[132:135], a[240:243], a[212:215], v[132:135]\n" \
"v_mov_b32_e32 v183, 0x3f80\n" \
C_"v234, v244, v245\n" \
M_"v[148:151], a[176:179], a[152:155], v[148:151]\n" \
"v_mov_b32_e32 v235, 0\n" \
C_"v54, v192, v193\n" \
M_"v[148:151], a[240:243], a[216:219], v[148:151]\n" \
B_"v20, s[80:83], s76 offen offset:0 sc1 lds\n" \
M_"v[152:155], a[180:183], a[152:155], v[152:155]\n" \
C_"v55, v194, v195\n" \
C_"v56, v208, v209\n" \
M_"v[152:155], a[244:247], a[216:219], v[152:155]\n" \
C_"v57, v210, v211\n" \
C_"v58, v224, v225\n" \
C_"v59, v226, v227\n" \
C_"v60, v240, v241\n" \
C_"v61, v242, v243\n" \
M_"v[32:35], v[180:183], v[184:187], 0\n" \
B_"v20, s[80:83], s76 offen offset:1024 sc1 lds\n" \
M_"v[156:159], a[184:187], a[152:155], v[156:159]\n" \
M_"v[192:195], v[188:191], v[184:187], 0\n" \
M_"v[208:211], v[204:207], v[200:203], 0\n" \
M_"v[36:39], v[188:191], v[200:203], 0\n" \
M_"v[224:227], v[220:223], v[216:219], 0\n" \
M_"v[248:251], v[204:207], v[216:219], 0\n" \
M_"v[240:243], v[236:239], v[232:235], 0\n" \
M_"v[64:67], v[220:223], v[232:235], 0\n" \
"s_mov_b32 s64, 0xaaaaaaaa\n" \
"s_mov_b32 s65, 0xaaaaaaaa\n" \
M_"v[156:159], a[248:251], a[216:219], v[156:159]\n" \
"v_cndmask_b32_e64 v32, v32, v33, s[64:65]\n" \
"v_cndmask_b32_e64 v34, v34, v35, s[64:65]\n" \
M_"v[160:163], a[188:191], a[152:155], v[160:163]\n" \
"s_mov_b32 s66, 0xfff0ffff\n" \
"s_mov_b32 s67, 0xf000ff00\n" \
"s_mov_b32 s68, 0xffc0fffc\n" \
"s_mov_b32 s69, 0xc000fc00\n" \
"v_cndmask_b32_e64 v192, v192, v193, s[64:65]\n" \
"v_add_f32_e64 v32, s47, v32\n" \
"v_cndmask_b32_e64 v194, v194, v195, s[64:65]\n" \
"v_add_f32_e64 v34, s47, v34\n" \
"v_cndmask_b32_e64 v208, v208, v209, s[64:65]\n" \
"v_cndmask_b32_e64 v36, v36, v37, s[64:65]\n" \
"v_cndmask_b32_e64 v210, v210, v211, s[64:65]\n" \
"v_cndmask_b32_e64 v38, v38, v39, s[64:65]\n" \
"v_cndmask_b32_e64 v224, v224, v225, s[64:65]\n" \
"v_cndmask_b32_e64 v248, v248, v249, s[64:65]\n" \
"v_cndmask_b32_e64 v226, v226, v227, s[64:65]\n" \
"v_cndmask_b32_e64 v250, v250, v251, s[64:65]\n" \
"v_cndmask_b32_e64 v240, v240, v241, s[64:65]\n" \
"v_cndmask_b32_e64 v64, v64, v65, s[64:65]\n" \
"v_cndmask_b32_e64 v242, v242, v243, s[64:65]\n" \
"v_cndmask_b32_e64 v66, v66, v67, s[64:65]\n" \
"v_cndmask_b32_e64 v192, v32, v192, s[66:67]\n" \
"v_cndmask_b32_e64 v194, v34, v194, s[68:69]\n" \
"v_cndmask_b32_e64 v208, v36, v208, s[66:67]\n" \
"v_cndmask_b32_e64 v210, v38, v210, s[68:69]\n" \
"v_cndmask_b32_e64 v224, v248, v224, s[66:67]\n" \
"v_cndmask_b32_e64 v226, v250, v226, s[68:69]\n" \
"v_cndmask_b32_e64 v240, v64, v240, s[66:67]\n" \
"v_cndmask_b32_e64 v242, v66, v242, s[68:69]\n" \
M_"v[160:163], a[252:255], a[216:219], v[160:163]\n" \
"v_max_f32_e32 v193, v192, v194\n" \
"v_max_f32_e32 v209, v208, v210\n" \
"v_max_f32_e32 v225, v224, v226\n" \
"v_max_f32_e32 v241, v240, v242\n" \
"v_mul_f32_e32 v84, s70, v84\n" \
"v_permlane32_swap_b32_e32 v193, v225\n" \
"v_permlane32_swap_b32_e32 v209, v241\n" \
"v_mul_f32_e32 v85, s70, v85\n" \
"v_mul_f32_e32 v92, s70, v92\n" \
"v_mul_f32_e32 v93, s70, v93\n" \
"v_permlane32_swap_b32_e32 v196, v228\n" \
"v_permlane32_swap_b32_e32 v212, v244\n" \
"v_max_f32_e32 v193, v193, v225\n" \
"v_max_f32_e32 v209, v209, v241\n" \
"v_accvgpr_read_b32 v48, a80\n" \
"v_accvgpr_read_b32 v49, a81\n" \
"v_mul_f32_e32 v86, s70, v86\n" \
"v_mul_f32_e32 v87, s70, v87\n" \
C_"v84, v84, v85\n" \
"v_mul_f32_e32 v94, s70, v94\n" \
"v_mul_f32_e32 v95, s70, v95\n" \
C_"v92, v92, v93\n" \
"v_permlane16_swap_b32_e32 v228, v244\n" \
"v_permlane16_swap_b32_e32 v193, v209\n" \
M_"v[176:179], a[188:191], a[156:159], v[176:179]\n" \
"s_mov_b32 s72, 0x3eae1a93\n" \
"v_mul_f32_e32 v88, s70, v88\n" \
"v_mul_f32_e32 v89, s70, v89\n" \
"v_mul_f32_e32 v90, s70, v90\n" \
"v_mul_f32_e32 v91, s70, v91\n" \
C_"v85, v86, v87\n" \
"v_mul_f32_e32 v96, s70, v96\n" \
"v_mul_f32_e32 v97, s70, v97\n" \
"v_mul_f32_e32 v98, s70, v98\n" \
"v_mul_f32_e32 v99, s70, v99\n" \
C_"v93, v94, v95\n" \
"v_mul_f32_e32 v45, 0xbfb8aa3b, v228\n" \
"v_mfma_f32_16x16x16_bf16 v[48:51], v[182:183], v[46:47], 0\n" \
"v_mfma_f32_16x16x16_bf16 v[196:199], v[190:191], v[46:47], 0\n" \
"v_mfma_f32_16x16x16_bf16 v[212:215], v[206:207], v[46:47], 0\n" \
"v_max_f32_e64 v193, v193, v209\n" \
C_"v86, v88, v89\n" \
C_"v87, v90, v91\n" \
C_"v94, v96, v97\n" \
C_"v95, v98, v99\n" \
"v_mfma_f32_16x16x16_bf16 v[228:231], v[222:223], v[46:47], 0\n" \
"v_mul_f32_e32 v193, s72, v193\n" \
"v_mov_b32_e32 v209, v193\n" \
"ds_write_b128 v27, v[84:87] offset:8192\n" \
"v_mfma_f32_16x16x16_bf16 v[244:247], v[238:239], v[46:47], 0\n" \
"v_permlane16_swap_b32_e32 v193, v209\n" \
"s_mov_b32 s71, 0x3e0293ee\n" \
M_"v[176:179], a[252:255], a[220:223], v[176:179]\n" \
"v_mov_b32_e32 v225, v193\n" \
"v_mov_b32_e32 v241, v209\n" \
"v_mul_f32_e32 v100, s70, v100\n" \
"v_mul_f32_e64 v101, s70, v101\n" \
"ds_write_b128 v27, v[92:95] offset:9216\n" \
"v_mul_f32_e32 v108, s70, v108\n" \
"v_mul_f32_e32 v109, s70, v109\n" \
"v_pk_mul_f32 v[116:117], v[116:117], s[70:71]\n" \
"v_pk_mul_f32 v[124:125], v[124:125], s[70:71]\n" \
"v_pk_mul_f32 v[132:133], v[132:133], s[70:71]\n" \
"v_pk_mul_f32 v[140:141], v[140:141], s[70:71]\n" \
"v_pk_mul_f32 v[148:149], v[148:149], s[70:71]\n" \
"v_pk_mul_f32 v[156:157], v[156:157], s[70:71]\n" \
"v_permlane32_swap_b32_e32 v193, v225\n" \
"v_permlane32_swap_b32_e32 v209, v241\n" \
"v_mul_f32_e32 v102, s70, v102\n" \
"v_mul_f32_e32 v103, s70, v103\n" \
C_"v100, v100, v101\n" \
"v_mul_f32_e32 v110, s70, v110\n" \
"v_mul_f32_e32 v111, s70, v111\n" \
C_"v108, v108, v109\n" \
"v_pk_mul_f32 v[118:119], v[118:119], s[70:71]\n" \
C_"v116, v116, v117\n" \
"v_pk_mul_f32 v[126:127], v[126:127], s[70:71]\n" \
C_"v124, v124, v125\n" \
"v_pk_mul_f32 v[134:135], v[134:135], s[70:71]\n" \
C_"v132, v132, v133\n" \
"v_pk_mul_f32 v[142:143], v[142:143], s[70:71]\n" \
C_"v140, v140, v141\n" \
"v_pk_mul_f32 v[150:151], v[150:151], s[70:71]\n" \
C_"v148, v148, v149\n" \
"v_pk_mul_f32 v[158:159], v[158:159], s[70:71]\n" \
C_"v156, v156, v157\n" \
"v_fma_f32 v192, v192, s72, -v193\n" \
"v_fma_f32 v194, v194, s72, -v193\n" \
"v_fma_f32 v208, v208, s72, -v209\n" \
"v_fma_f32 v210, v210, s72, -v209\n" \
"v_mul_f32_e32 v104, s70, v104\n" \
"v_mul_f32_e32 v105, s70, v105\n" \
"v_mul_f32_e32 v106, s70, v106\n" \
"v_mul_f32_e32 v107, s70, v107\n" \
C_"v101, v102, v103\n" \
"v_mul_f32_e32 v112, s70, v112\n" \
"v_mul_f32_e32 v113, s70, v113\n" \
"v_mul_f32_e32 v114, s70, v114\n" \
"v_mul_f32_e32 v115, s70, v115\n" \
C_"v109, v110, v111\n" \
"v_pk_mul_f32 v[120:121], v[120:121], s[70:71]\n" \
"v_pk_mul_f32 v[122:123], v[122:123], s[70:71]\n" \
C_"v117, v118, v119\n" \
"v_pk_mul_f32 v[128:129], v[128:129], s[70:71]\n" \
"v_pk_mul_f32 v[130:131], v[130:131], s[70:71]\n" \
C_"v125, v126, v127\n" \
"v_pk_mul_f32 v[136:137], v[136:137], s[70:71]\n" \
"v_pk_mul_f32 v[138:139], v[138:139], s[70:71]\n" \
C_"v133, v134, v135\n" \
"v_pk_mul_f32 v[144:145], v[144:145], s[70:71]\n" \
"v_pk_mul_f32 v[146:147], v[146:147], s[70:71]\n" \
C_"v141, v142, v143\n" \
"v_pk_mul_f32 v[152:153], v[152:153], s[70:71]\n" \
"v_pk_mul_f32 v[154:155], v[154:155], s[70:71]\n" \
C_"v149, v150, v151\n" \
"v_pk_mul_f32 v[160:161], v[160:161], s[70:71]\n" \
"v_pk_mul_f32 v[162:163], v[162:163], s[70:71]\n" \
C_"v157, v158, v159\n" \
"v_pk_mul_f32 v[176:177], v[176:177], s[70:71]\n" \
"v_pk_mul_f32 v[178:179], v[178:179], s[70:71]\n" \
E_"v192, v192\n" \
E_"v194, v194\n" \
E_"v208, v208\n" \
E_"v210, v210\n" \
"v_fma_f32 v224, v224, s72, -v225\n" \
"v_fma_f32 v226, v226, s72, -v225\n" \
M_"v[172:175], a[184:187], a[156:159], v[172:175]\n" \
C_"v102, v104, v105\n" \
C_"v103, v106, v107\n" \
C_"v110, v112, v113\n" \
C_"v111, v114, v115\n" \
C_"v118, v120, v121\n" \
C_"v119, v122, v123\n" \
C_"v126, v128, v129\n" \
C_"v127, v130, v131\n" \
C_"v134, v136, v137\n" \
C_"v135, v138, v139\n" \
C_"v142, v144, v145\n" \
C_"v143, v146, v147\n" \
C_"v150, v152, v153\n" \
C_"v151, v154, v155\n" \
C_"v158, v160, v161\n" \
C_"v159, v162, v163\n" \
E_"v224, v224\n" \
E_"v226, v226\n" \
"v_fma_f32 v240, v240, s72, -v241\n" \
"v_fma_f32 v242, v242, s72, -v241\n" \
"ds_write_b128 v27, v[100:103] offset:12288\n" \
C_"v192, v192, v194\n" \
C_"v208, v208, v210\n" \
E_"v240, v240\n" \
E_"v242, v242\n" \
C_"v68, v48, v49\n" \
C_"v69, v50, v51\n" \
C_"v70, v196, v197\n" \
C_"v71, v198, v199\n" \
"ds_write_b128 v27, v[108:111] offset:13312\n" \
C_"v72, v212, v213\n" \
C_"v73, v214, v215\n" \
"v_perm_b32 v184, v192, v192, v40\n" \
"v_perm_b32 v185, v192, v192, v41\n" \
"v_perm_b32 v186, v192, v192, v42\n" \
"v_perm_b32 v187, v192, v192, v43\n" \
"v_perm_b32 v200, v208, v208, v40\n" \
"v_perm_b32 v201, v208, v208, v41\n" \
"v_perm_b32 v202, v208, v208, v42\n" \
"v_perm_b32 v203, v208, v208, v43\n" \
"ds_write_b128 v21, v[116:119] offset:16384\n" \
C_"v224, v224, v226\n" \
C_"v74, v228, v229\n" \
C_"v75, v230, v231\n" \
M_"v[32:35], v[68:71], v[184:187], 0\n" \
M_"v[36:39], v[70:73], v[200:203], 0\n" \
"v_perm_b32 v216, v224, v224, v40\n" \
"v_perm_b32 v217, v224, v224, v41\n" \
"ds_write_b128 v21, v[124:127] offset:17408\n" \
"v_perm_b32 v218, v224, v224, v42\n" \
"v_perm_b32 v219, v224, v224, v43\n" \
C_"v240, v240, v242\n" \
M_"v[172:175], a[248:251], a[220:223], v[172:175]\n" \
C_"v76, v244, v245\n" \
C_"v77, v246, v247\n" \
M_"v[248:251], v[72:75], v[216:219], 0\n" \
"v_perm_b32 v232, v240, v240, v40\n" \
"v_perm_b32 v233, v240, v240, v41\n" \
"ds_write_b128 v21, v[132:135] offset:20480\n" \
"v_perm_b32 v234, v240, v240, v42\n" \
"v_perm_b32 v235, v240, v240, v43\n" \
"v_pk_mul_f32 v[172:173], v[172:173], s[70:71]\n" \
"v_pk_mul_f32 v[174:175], v[174:175], s[70:71]\n" \
C_"v172, v172, v173\n" \
C_"v173, v174, v175\n" \
C_"v174, v176, v177\n" \
C_"v175, v178, v179\n" \
M_"v[64:67], v[74:77], v[232:235], 0\n" \
"ds_write_b128 v21, v[140:143] offset:21504\n" \
M_"v[168:171], a[180:183], a[156:159], v[168:171]\n" \
C_"v52, v252, v253\n" \
C_"v53, v254, v255\n" \
M_"v[168:171], a[244:247], a[220:223], v[168:171]\n" \
"v_exp_f32_e64 v45, v45\n" \
M_"v[192:195], v[52:55], v[184:187], 0\n" \
"ds_write_b128 v21, v[148:151] offset:24576\n" \
M_"v[208:211], v[54:57], v[200:203], 0\n" \
M_"v[224:227], v[56:59], v[216:219], 0\n" \
M_"v[240:243], v[58:61], v[232:235], 0\n" \
"ds_write_b128 v21, v[156:159] offset:25600\n" \
"v_mov_b32_dpp v34, v38 quad_perm:[0,1,2,3] row_mask:0x2 bank_mask:0xf\n" \
"v_pk_mul_f32 v[168:169], v[168:169], s[70:71]\n" \
"v_pk_mul_f32 v[170:171], v[170:171], s[70:71]\n" \
"v_mov_b32_dpp v34, v250 quad_perm:[0,1,2,3] row_mask:0x4 bank_mask:0xf\n" \
"v_add_f32_e64 v45, 1.0, v45\n" \
M_"v[164:167], a[176:179], a[156:159], v[164:167]\n" \
"v_mov_b32_dpp v34, v66 quad_perm:[0,1,2,3] row_mask:0x8 bank_mask:0xf\n" \
"v_mov_b32_dpp v32, v36 quad_perm:[0,1,2,3] row_mask:0x2 bank_mask:0xf\n" \
"v_rcp_f32_e64 v45, v45\n" \
"ds_write_b128 v21, v[172:175] offset:29696\n" \
"v_mov_b32_dpp v33, v37 quad_perm:[0,1,2,3] row_mask:0x2 bank_mask:0xf\n" \
"v_rcp_f32_e64 v34, v34\n" \
"v_mov_b32_dpp v32, v248 quad_perm:[0,1,2,3] row_mask:0x4 bank_mask:0xf\n" \
"v_mov_b32_dpp v33, v249 quad_perm:[0,1,2,3] row_mask:0x4 bank_mask:0xf\n" \
"v_mul_f32_e64 v34, v45, v34\n" \
"v_mov_b32_dpp v32, v64 quad_perm:[0,1,2,3] row_mask:0x8 bank_mask:0xf\n" \
"v_mov_b32_dpp v33, v65 quad_perm:[0,1,2,3] row_mask:0x8 bank_mask:0xf\n" \
"v_mul_f32_e32 v32, v34, v32\n" \
"v_mul_f32_e32 v33, v34, v33\n" \
"v_mov_b32_e64 v38, v34\n" \
M_"v[164:167], a[240:243], a[220:223], v[164:167]\n" \
"v_sub_f32_e32 v44, 1.0, v45\n" \
"v_permlane16_swap_b32_e32 v34, v38\n" \
"v_mov_b32_e32 v250, v34\n" \
"v_mov_b32_e32 v66, v38\n" \
C_"v32, v32, v33\n" \
"v_permlane32_swap_b32_e32 v34, v250\n" \
"v_permlane32_swap_b32_e32 v38, v66\n" \
"v_mul_f32_e64 v192, v34, v192\n" \
"v_pk_mul_f32 v[164:165], v[164:165], s[70:71]\n" \
"v_pk_mul_f32 v[166:167], v[166:167], s[70:71]\n" \
C_"v164, v164, v165\n" \
C_"v165, v166, v167\n" \
C_"v166, v168, v169\n" \
C_"v167, v170, v171\n" \
"ds_write_b128 v21, v[164:167] offset:28672\n" \
"v_mul_f32_e32 v193, v34, v193\n" \
"v_mul_f32_e32 v208, v38, v208\n" \
"v_mul_f32_e32 v209, v38, v209\n" \
"v_mul_f32_e32 v224, v250, v224\n" \
"v_mul_f32_e32 v225, v250, v225\n" \
"v_mul_f32_e32 v240, v66, v240\n" \
"v_mul_f32_e32 v241, v66, v241\n" \
"v_mul_f32_e32 v194, v34, v194\n" \
"v_mul_f32_e64 v195, v34, v195\n" \
C_"v192, v192, v193\n" \
"v_mul_f32_e32 v210, v38, v210\n" \
"v_mul_f32_e32 v211, v38, v211\n" \
C_"v208, v208, v209\n" \
"v_mul_f32_e32 v226, v250, v226\n" \
"v_mul_f32_e32 v227, v250, v227\n" \
C_"v224, v224, v225\n" \
"v_mul_f32_e32 v242, v66, v242\n" \
"v_mul_f32_e32 v243, v66, v243\n" \
C_"v240, v240, v241\n" \
C_"v193, v194, v195\n" \
"s_add_u32 s84, s48, 0x0\n" \
"s_nop 0\n" \
C_"v209, v210, v211\n" \
"s_add_u32 s85, s48, 0x24800\n" \
C_"v225, v226, v227\n" \
"s_add_u32 s86, s48, 0x49000\n" \
C_"v241, v242, v243\n" \
"s_add_u32 s87, s48, 0x6d800\n" \
"ds_write_b32 v26, v44\n" \
"buffer_store_dword v32, v25, s[80:83], s49 offen sc1\n" \
"buffer_store_dwordx2 v[192:193], v24, s[80:83], s84 offen sc1\n" \
"buffer_store_dwordx2 v[208:209], v24, s[80:83], s85 offen sc1\n" \
"buffer_store_dwordx2 v[224:225], v24, s[80:83], s86 offen sc1\n" \
"buffer_store_dwordx2 v[240:241], v24, s[80:83], s87 offen sc1\n" \
"s_branch L_end_%=\n" \
"L_poll60_%=:\n" \
"s_mov_b32 s84, 0\n" \
"L_pl60_%=:\n" \
"s_sleep 1\n" \
"v_bfe_u32 v44, v20, 2, 5\n" \
"buffer_load_dword v44, v44, s[80:83], s38 offen offset:128 sc1\n" \
"s_waitcnt vmcnt(0)\n" \
"v_cmp_ne_u32_e32 vcc, s62, v44\n" \
"s_add_u32 s84, s84, 1\n" \
"s_nop 3\n" \
"s_cbranch_vccz L_pollr60_%=\n" \
"s_cmp_gt_u32 s84, 0x400000\n" \
"s_cbranch_scc0 L_pl60_%=\n" \
"s_branch L_pollr60_%=\n" \
"L_poll160_%=:\n" \
"s_mov_b32 s84, 0\n" \
"L_pl160_%=:\n" \
"s_sleep 1\n" \
"v_bfe_u32 v44, v20, 2, 5\n" \
"buffer_load_dword v44, v44, s[80:83], s38 offen offset:0 sc1\n" \
"s_waitcnt vmcnt(0)\n" \
"v_cmp_ne_u32_e32 vcc, s62, v44\n" \
"s_add_u32 s84, s84, 1\n" \
"s_nop 3\n" \
"s_cbranch_vccz L_pollr160_%=\n" \
"s_cmp_gt_u32 s84, 0x400000\n" \
"s_cbranch_scc0 L_pl160_%=\n" \
"s_branch L_pollr160_%=\n" \
".fill (L_pb + 42460 - .) / 4, 4, 0xBF800000\n" \
"L_w1_%=:\n" \
"s_waitcnt vmcnt(13)\n" \
"s_barrier\n" \
X_"a[160:161], v32 offset:0\n" \
X_"a[162:163], v32 offset:1024\n" \
R_"a[128:131], v28 offset:16384\n" \
X_"a[224:225], v32 offset:8192\n" \
X_"a[226:227], v32 offset:9216\n" \
R_"a[192:195], v29 offset:16384\n" \
R_"a[132:135], v28 offset:18432\n" \
R_"a[196:199], v29 offset:18432\n" \
X_"a[164:165], v33 offset:0\n" \
X_"a[166:167], v33 offset:1024\n" \
X_"a[228:229], v33 offset:8192\n" \
X_"a[230:231], v33 offset:9216\n" \
R_"a[136:139], v28 offset:20480\n" \
R_"a[200:203], v29 offset:20480\n" \
"s_waitcnt lgkmcnt(11)\n" \
M_"a[0:3], a[128:131], a[160:163], 0\n" \
X_"a[168:169], v34 offset:0\n" \
"s_add_u32 m0, s31, 0xc000\n" \
"s_mov_b32 s84, 0x80000\n" \
"s_waitcnt lgkmcnt(9)\n" \
M_"a[0:3], a[192:195], a[224:227], a[0:3]\n" \
X_"a[170:171], v34 offset:1024\n" \
B_"v26, s[72:75], s84 offen offset:0 lds\n" \
"s_waitcnt lgkmcnt(9)\n" \
M_"a[16:19], a[132:135], a[160:163], 0\n" \
X_"a[232:233], v34 offset:8192\n" \
"s_lshr_b32 s4, s2, 1\n" \
"s_waitcnt lgkmcnt(9)\n" \
M_"a[16:19], a[196:199], a[224:227], a[16:19]\n" \
X_"a[234:235], v34 offset:9216\n" \
"s_addk_i32 s84, 0x7c00\n" \
"s_waitcnt lgkmcnt(8)\n" \
M_"a[20:23], a[132:135], a[164:167], 0\n" \
R_"a[140:143], v28 offset:22528\n" \
B_"v26, s[72:75], s84 offen offset:1024 lds\n" \
"s_waitcnt lgkmcnt(7)\n" \
M_"a[20:23], a[196:199], a[228:231], a[20:23]\n" \
R_"a[204:207], v29 offset:22528\n" \
"s_and_b32 s4, s4, 0x7c\n" \
M_"a[4:7], a[128:131], a[164:167], 0\n" \
X_"a[172:173], v35 offset:0\n" \
"s_addk_i32 s84, 0x7c00\n" \
M_"a[4:7], a[192:195], a[228:231], a[4:7]\n" \
X_"a[174:175], v35 offset:1024\n" \
B_"v27, s[72:75], s84 offen offset:2048 lds\n" \
"s_waitcnt lgkmcnt(9)\n" \
M_"a[32:35], a[136:139], a[160:163], 0\n" \
X_"a[236:237], v35 offset:8192\n" \
"v_mov_b32_e32 v213, s13\n" \
"s_waitcnt lgkmcnt(9)\n" \
M_"a[32:35], a[200:203], a[224:227], a[32:35]\n" \
X_"a[238:239], v35 offset:9216\n" \
"s_addk_i32 s84, 0x7c00\n" \
M_"a[36:39], a[136:139], a[164:167], 0\n" \
R_"a[144:147], v28 offset:0\n" \
B_"v27, s[72:75], s84 offen offset:3072 lds\n" \
M_"a[36:39], a[200:203], a[228:231], a[36:39]\n" \
R_"a[208:211], v29 offset:0\n" \
"s_or_b32 s4, s4, s3\n" \
"s_waitcnt lgkmcnt(10)\n" \
M_"a[40:43], a[136:139], a[168:171], 0\n" \
R_"a[148:151], v28 offset:2048\n" \
"v_lshrrev_b32_e32 v212, 4, v20\n" \
"s_waitcnt lgkmcnt(9)\n" \
M_"a[40:43], a[200:203], a[232:235], a[40:43]\n" \
R_"a[212:215], v29 offset:2048\n" \
"v_mad_u64_u32 v[214:215], s[4:5], s4, 36, v[212:213]\n" \
M_"a[24:27], a[132:135], a[168:171], 0\n" \
R_"a[152:155], v28 offset:4096\n" \
"v_mov_b32_e64 v217, s9\n" \
M_"a[24:27], a[196:199], a[232:235], a[24:27]\n" \
R_"a[216:219], v29 offset:4096\n" \
"s_movk_i32 s4, 0x240\n" \
M_"a[8:11], a[128:131], a[168:171], 0\n" \
R_"a[156:159], v28 offset:6144\n" \
"v_cmp_gt_u32_e32 vcc, s4, v20\n" \
M_"a[8:11], a[192:195], a[232:235], a[8:11]\n" \
R_"a[220:223], v29 offset:6144\n" \
"s_mov_b32 m0, s32\n" \
"s_waitcnt lgkmcnt(13)\n" \
M_"a[48:51], a[140:143], a[160:163], 0\n" \
"s_mov_b32 exec_lo, 0xff000000\n" \
"s_mov_b32 exec_hi, 0x0\n" \
"global_load_lds_dword v[184:185], off\n" \
"s_mov_b64 exec, -1\n" \
"s_waitcnt lgkmcnt(12)\n" \
M_"a[48:51], a[204:207], a[224:227], a[48:51]\n" \
X_"a[176:177], v36 offset:0\n" \
"s_lshl_b32 s4, s2, 3\n" \
M_"a[52:55], a[140:143], a[164:167], 0\n" \
X_"a[178:179], v36 offset:1024\n" \
"s_and_b32 s4, s4, 56\n" \
M_"a[52:55], a[204:207], a[228:231], a[52:55]\n" \
"s_mov_b32 m0, s32\n" \
X_"a[240:241], v36 offset:8192\n" \
M_"a[56:59], a[140:143], a[168:171], 0\n" \
"s_mov_b32 exec_lo, 0xffff\n" \
"s_mov_b32 exec_hi, 0x0\n" \
"global_load_lds_dword v[186:187], off\n" \
"s_mov_b64 exec, -1\n" \
M_"a[56:59], a[204:207], a[232:235], a[56:59]\n" \
X_"a[242:243], v36 offset:9216\n" \
"v_cndmask_b32_e32 v217, v217, v213, vcc\n" \
"s_waitcnt lgkmcnt(14)\n" \
M_"a[60:63], a[140:143], a[172:175], 0\n" \
X_"a[180:181], v37 offset:0\n" \
"v_lshlrev_b32_e32 v213, 3, v214\n" \
"s_waitcnt lgkmcnt(13)\n" \
M_"a[60:63], a[204:207], a[236:239], a[60:63]\n" \
X_"a[182:183], v37 offset:1024\n" \
"v_and_b32_e32 v213, 0x1ffc0, v213\n" \
M_"a[44:47], a[136:139], a[172:175], 0\n" \
X_"a[244:245], v37 offset:8192\n" \
"v_and_b32_e64 v214, 7, v214\n" \
M_"a[44:47], a[200:203], a[236:239], a[44:47]\n" \
X_"a[246:247], v37 offset:9216\n" \
"v_or3_b32 v213, v214, s4, v213\n" \
M_"a[28:31], a[132:135], a[172:175], 0\n" \
X_"a[184:185], v38 offset:0\n" \
"s_lshl_b32 s4, s2, 2\n" \
M_"a[28:31], a[196:199], a[236:239], a[28:31]\n" \
X_"a[186:187], v38 offset:1024\n" \
"s_or_b32 s4, s4, s3\n" \
M_"a[12:15], a[128:131], a[172:175], 0\n" \
"s_mov_b64 exec, 0\n" \
"s_swappc_b64 s[88:89], s[86:87]\n" \
"s_mov_b64 exec, -1\n" \
"s_add_u32 s86, s86, 0x2000\n" \
"s_addc_u32 s87, s87, 0\n" \
M_"a[12:15], a[192:195], a[236:239], a[12:15]\n" \
"s_mul_i32 s4, s4, 18\n" \
"v_min_u32_e32 v212, 53, v212\n" \
"s_waitcnt lgkmcnt(15)\n" \
M_"a[76:79], a[144:147], a[172:175], 0\n" \
"s_mov_b32 m0, s32\n" \
X_"a[248:249], v38 offset:8192\n" \
M_"a[76:79], a[208:211], a[236:239], a[76:79]\n" \
"s_mov_b64 exec, 0xff\n" \
"buffer_load_dword v49, s[80:83], 0 offen lds\n" \
"s_mov_b64 exec, -1\n" \
M_"a[72:75], a[144:147], a[168:171], 0\n" \
"s_sub_i32 s4, s4, 36\n" \
"v_add_u32_e64 v212, s4, v212\n" \
M_"a[72:75], a[208:211], a[232:235], a[72:75]\n" \
X_"a[250:251], v38 offset:9216\n" \
"v_mov_b32_e64 v215, s12\n" \
M_"a[68:71], a[144:147], a[164:167], 0\n" \
"s_mov_b32 s84, 0x5bc0000\n" \
"buffer_load_dword v180, off, s[80:83], s84 sc1\n" \
M_"a[68:71], a[208:211], a[228:231], a[68:71]\n" \
"v_mov_b32_e32 v216, s8\n" \
"v_cndmask_b32_e32 v212, v212, v213, vcc\n" \
M_"a[64:67], a[144:147], a[160:163], 0\n" \
"v_cndmask_b32_e32 v216, v216, v215, vcc\n" \
"v_lshlrev_b32_e32 v212, 7, v212\n" \
M_"a[64:67], a[208:211], a[224:227], a[64:67]\n" \
X_"a[188:189], v39 offset:0\n" \
"v_mov_b32_e64 v213, 0\n" \
M_"a[80:83], a[148:151], a[160:163], 0\n" \
"v_lshl_add_u64 v[188:189], v[216:217], 0, v[212:213]\n" \
"s_movk_i32 s5, 0xd80\n" \
"s_waitcnt lgkmcnt(15)\n" \
M_"a[80:83], a[212:215], a[224:227], a[80:83]\n" \
"s_lshr_b32 s6, s2, 5\n" \
"v_lshrrev_b32_e32 v212, 4, v20\n" \
M_"a[84:87], a[148:151], a[164:167], 0\n" \
X_"a[190:191], v39 offset:1024\n" \
"v_lshl_or_b32 v215, s3, 6, v212\n" \
M_"a[84:87], a[212:215], a[228:231], a[84:87]\n" \
"v_mul_u32_u24_e32 v216, 0x2aab, v215\n" \
"v_lshrrev_b32_e64 v216, 16, v216\n" \
M_"a[88:91], a[148:151], a[168:171], 0\n" \
"v_mad_i32_i24 v217, v216, -6, v215\n" \
"v_add_u32_e32 v218, 0xfffffe50, v215\n" \
M_"a[88:91], a[212:215], a[232:235], a[88:91]\n" \
X_"a[252:253], v39 offset:8192\n" \
"s_mul_i32 s35, s6, 0x48\n" \
M_"a[92:95], a[148:151], a[172:175], 0\n" \
"v_lshrrev_b32_e32 v219, 1, v217\n" \
"v_add_u32_e32 v218, s35, v216\n" \
M_"a[92:95], a[212:215], a[236:239], a[92:95]\n" \
"v_mul_u32_u24_e32 v219, 0x480, v219\n" \
"v_and_b32_e64 v214, 16, v20\n" \
M_"a[108:111], a[152:155], a[172:175], 0\n" \
X_"a[254:255], v39 offset:9216\n" \
"v_mad_u32_u24 v219, v218, s5, v219\n" \
M_"a[108:111], a[216:219], a[236:239], a[108:111]\n" \
"v_or_b32_e32 v192, v219, v214\n" \
"v_mov_b32_e32 v220, 2.0\n" \
M_"a[104:107], a[152:155], a[168:171], 0\n" \
"s_mov_b64 exec, 0\n" \
"s_swappc_b64 s[88:89], s[86:87]\n" \
"s_mov_b64 exec, -1\n" \
"s_andn2_b32 s85, 2, s3\n" \
"s_lshl_b32 s85, s85, 12\n" \
"s_add_u32 s86, s86, s85\n" \
"s_addc_u32 s87, s87, 0\n" \
M_"a[104:107], a[216:219], a[232:235], a[104:107]\n" \
"v_mov_b32_e32 v222, 7\n" \
"v_lshlrev_b32_e64 v220, 7, v218\n" \
M_"a[100:103], a[152:155], a[164:167], 0\n" \
"v_lshrrev_b32_e32 v218, 1, v218\n" \
"v_and_b32_e32 v218, 4, v218\n" \
M_"a[100:103], a[216:219], a[228:231], a[100:103]\n" \
"v_and_b32_e64 v216, 2, v216\n" \
"s_mul_i32 s7, s6, 0x2400\n" \
M_"a[96:99], a[152:155], a[160:163], 0\n" \
"s_sub_i32 s34, 0x22000, s7\n" \
"v_bitop3_b32 v216, v217, v218, v216 bitop3:0x1e\n" \
M_"a[96:99], a[216:219], a[224:227], a[96:99]\n" \
"v_or_b32_e32 v213, 0x24400, v20\n" \
"v_lshlrev_b32_e32 v216, 4, v216\n" \
"s_waitcnt lgkmcnt(15)\n" \
M_"a[112:115], a[156:159], a[160:163], 0\n" \
"v_add3_u32 v196, v220, s34, v216\n" \
"v_mov_b32_e64 v216, v213\n" \
M_"a[112:115], a[220:223], a[224:227], a[112:115]\n" \
"s_movk_i32 s36, 0x1b0\n" \
"v_or_b32_e32 v216, 0x100, v215\n" \
M_"a[116:119], a[156:159], a[164:167], 0\n" \
"v_mul_u32_u24_e32 v217, 0x2aab, v216\n" \
"v_add_u32_e32 v215, 0xffffff50, v215\n" \
M_"a[116:119], a[220:223], a[228:231], a[116:119]\n" \
"v_lshrrev_b32_e32 v217, 16, v217\n" \
"v_mov_b32_e64 v220, v215\n" \
M_"a[120:123], a[156:159], a[168:171], 0\n" \
"v_mad_i32_i24 v218, v217, -6, v216\n" \
"v_cmp_gt_u32_e64 vcc, s36, v216\n" \
M_"a[120:123], a[220:223], a[232:235], a[120:123]\n" \
"v_mov_b32_e32 v221, 7\n" \
"v_lshrrev_b32_e32 v222, 1, v218\n" \
M_"a[124:127], a[156:159], a[172:175], 0\n" \
"v_cndmask_b32_e32 v217, v220, v217, vcc\n" \
"s_movk_i32 s4, 0x48\n" \
"s_waitcnt vmcnt(4) lgkmcnt(0)\n" \
M_"a[124:127], a[220:223], a[236:239], a[124:127]\n" \
"s_barrier\n" \
M_"v[52:55], a[128:131], a[176:179], 0\n" \
"s_add_u32 m0, s30, 0x0\n" \
"s_movk_i32 s84, 0x100\n" \
M_"v[52:55], a[192:195], a[240:243], v[52:55]\n" \
B_"v22, s[64:67], s84 offen offset:0 lds\n" \
M_"v[56:59], a[128:131], a[180:183], 0\n" \
X_"a[160:161], v32 offset:32768\n" \
"v_add_u32_e64 v220, s35, v217\n" \
M_"v[56:59], a[192:195], a[244:247], v[56:59]\n" \
"s_movk_i32 s84, 0x1d00\n" \
"s_nop 0\n" \
M_"v[60:63], a[128:131], a[184:187], 0\n" \
B_"v23, s[64:67], s84 offen offset:1024 lds\n" \
M_"v[60:63], a[192:195], a[248:251], v[60:63]\n" \
X_"a[162:163], v32 offset:33792\n" \
"v_mul_u32_u24_e32 v222, 0x480, v222\n" \
M_"v[64:67], a[128:131], a[188:191], 0\n" \
X_"a[224:225], v32 offset:40960\n" \
"v_mad_u32_u24 v222, v220, s5, v222\n" \
M_"v[64:67], a[192:195], a[252:255], v[64:67]\n" \
R_"a[128:131], v28 offset:49152\n" \
"v_cmp_gt_u32_e64 s[4:5], s4, v215\n" \
M_"v[80:83], a[132:135], a[188:191], 0\n" \
R_"a[192:195], v29 offset:49152\n" \
"v_cndmask_b32_e64 v215, v221, v218, vcc\n" \
M_"v[80:83], a[196:199], a[252:255], v[80:83]\n" \
"s_movk_i32 s84, 0x3900\n" \
M_"v[76:79], a[132:135], a[184:187], 0\n" \
B_"v22, s[64:67], s84 offen offset:2048 lds\n" \
M_"v[76:79], a[196:199], a[248:251], v[76:79]\n" \
"s_movk_i32 s84, 0x5500\n" \
M_"v[72:75], a[132:135], a[180:183], 0\n" \
B_"v23, s[64:67], s84 offen offset:3072 lds\n" \
M_"v[72:75], a[196:199], a[244:247], v[72:75]\n" \
"s_add_u32 m0, s31, 0x0\n" \
"s_mov_b32 s84, 0x100000\n" \
M_"v[68:71], a[132:135], a[176:179], 0\n" \
B_"v26, s[68:71], s84 offen offset:0 lds\n" \
M_"v[68:71], a[196:199], a[240:243], v[68:71]\n" \
R_"a[132:135], v28 offset:51200\n" \
"v_lshrrev_b32_e32 v218, 1, v220\n" \
M_"v[84:87], a[136:139], a[176:179], 0\n" \
R_"a[196:199], v29 offset:51200\n" \
"v_or_b32_e64 v214, v222, v214\n" \
M_"v[84:87], a[200:203], a[240:243], v[84:87]\n" \
"s_addk_i32 s84, 0x7c00\n" \
M_"v[88:91], a[136:139], a[180:183], 0\n" \
B_"v26, s[68:71], s84 offen offset:1024 lds\n" \
M_"v[88:91], a[200:203], a[244:247], v[88:91]\n" \
X_"a[226:227], v32 offset:41984\n" \
"v_mov_b32_e32 v222, 2.0\n" \
M_"v[92:95], a[136:139], a[184:187], 0\n" \
"s_addk_i32 s84, 0x7c00\n" \
M_"v[92:95], a[200:203], a[248:251], v[92:95]\n" \
B_"v27, s[68:71], s84 offen offset:2048 lds\n" \
M_"v[96:99], a[136:139], a[188:191], 0\n" \
X_"a[164:165], v33 offset:32768\n" \
"v_and_b32_e32 v218, 4, v218\n" \
M_"v[96:99], a[200:203], a[252:255], v[96:99]\n" \
R_"a[136:139], v28 offset:53248\n" \
"v_and_b32_e64 v217, 2, v217\n" \
M_"v[112:115], a[140:143], a[188:191], 0\n" \
R_"a[200:203], v29 offset:53248\n" \
"v_bitop3_b32 v215, v215, v218, v217 bitop3:0x1e\n" \
M_"v[112:115], a[204:207], a[252:255], v[112:115]\n" \
"s_addk_i32 s84, 0x7c00\n" \
M_"v[108:111], a[140:143], a[184:187], 0\n" \
B_"v27, s[68:71], s84 offen offset:3072 lds\n" \
"s_mov_b64 exec, 0\n" \
"s_swappc_b64 s[88:89], s[86:87]\n" \
"s_mov_b64 exec, -1\n" \
M_"v[108:111], a[204:207], a[248:251], v[108:111]\n" \
"s_add_u32 m0, s30, 0x1000\n" \
"s_mov_b32 s84, 0x8100\n" \
M_"v[104:107], a[140:143], a[180:183], 0\n" \
B_"v22, s[64:67], s84 offen offset:0 lds\n" \
M_"v[104:107], a[204:207], a[244:247], v[104:107]\n" \
X_"a[166:167], v33 offset:33792\n" \
"v_lshlrev_b32_e64 v223, 1, v220\n" \
M_"v[100:103], a[140:143], a[176:179], 0\n" \
X_"a[228:229], v33 offset:40960\n" \
"v_mov_b32_e64 v224, 2.0\n" \
M_"v[100:103], a[204:207], a[240:243], v[100:103]\n" \
R_"a[140:143], v28 offset:55296\n" \
"v_cndmask_b32_e64 v193, v222, v214, vcc\n" \
M_"v[116:119], a[144:147], a[176:179], 0\n" \
R_"a[204:207], v29 offset:55296\n" \
"v_lshlrev_b32_e64 v214, 7, v220\n" \
M_"v[116:119], a[208:211], a[240:243], v[116:119]\n" \
"s_addk_i32 s84, 0x1c00\n" \
M_"v[120:123], a[144:147], a[180:183], 0\n" \
B_"v23, s[64:67], s84 offen offset:1024 lds\n" \
M_"v[120:123], a[208:211], a[244:247], v[120:123]\n" \
"s_addk_i32 s84, 0x1c00\n" \
M_"v[124:127], a[144:147], a[184:187], 0\n" \
B_"v22, s[64:67], s84 offen offset:2048 lds\n" \
M_"v[124:127], a[208:211], a[248:251], v[124:127]\n" \
"s_addk_i32 s84, 0x1c00\n" \
".fill (L_pb + 44768 - .) / 4, 4, 0xBF800000\n" \
"L_pp11:\n" \
"s_cbranch_execz L_pret\n" \
M_"v[128:131], a[144:147], a[188:191], 0\n" \
B_"v23, s[64:67], s84 offen offset:3072 lds\n" \
M_"v[128:131], a[208:211], a[252:255], v[128:131]\n" \
R_"a[144:147], v28 offset:32768\n" \
"v_lshlrev_b32_e32 v215, 4, v215\n" \
M_"v[144:147], a[148:151], a[188:191], 0\n" \
R_"a[208:211], v29 offset:32768\n" \
"s_mul_i32 s39, s3, 18\n" \
M_"v[144:147], a[212:215], a[252:255], v[144:147]\n" \
"s_add_u32 m0, s31, 0x4000\n" \
"s_mov_b32 s84, 0x100000\n" \
M_"v[140:143], a[148:151], a[184:187], 0\n" \
B_"v26, s[72:75], s84 offen offset:0 lds\n" \
M_"v[140:143], a[212:215], a[248:251], v[140:143]\n" \
X_"a[230:231], v33 offset:41984\n" \
"v_add3_u32 v214, v214, s34, v215\n" \
M_"v[136:139], a[148:151], a[180:183], 0\n" \
"s_addk_i32 s84, 0x7c00\n" \
M_"v[136:139], a[212:215], a[244:247], v[136:139]\n" \
B_"v26, s[72:75], s84 offen offset:1024 lds\n" \
M_"v[132:135], a[148:151], a[176:179], 0\n" \
X_"a[168:169], v34 offset:32768\n" \
"v_cndmask_b32_e64 v195, v224, v223, s[4:5]\n" \
M_"v[132:135], a[212:215], a[240:243], v[132:135]\n" \
R_"a[148:151], v28 offset:34816\n" \
"s_movk_i32 s4, 0x1f8\n" \
M_"v[148:151], a[152:155], a[176:179], 0\n" \
R_"a[212:215], v29 offset:34816\n" \
"v_mov_b32_e32 v215, v213\n" \
M_"v[148:151], a[216:219], a[240:243], v[148:151]\n" \
X_"a[170:171], v34 offset:33792\n" \
"s_add_i32 s35, s35, s39\n" \
M_"v[152:155], a[152:155], a[180:183], 0\n" \
X_"a[232:233], v34 offset:40960\n" \
"v_cmp_gt_u32_e32 vcc, s4, v216\n" \
M_"v[152:155], a[216:219], a[244:247], v[152:155]\n" \
X_"a[234:235], v34 offset:41984\n" \
"v_add_u32_e64 v212, s35, v212\n" \
M_"v[156:159], a[152:155], a[184:187], 0\n" \
X_"a[172:173], v35 offset:32768\n" \
"v_mov_b32_e64 v219, 2.0\n" \
M_"v[156:159], a[216:219], a[248:251], v[156:159]\n" \
X_"a[174:175], v35 offset:33792\n" \
"v_cndmask_b32_e64 v197, v215, v214, vcc\n" \
M_"v[160:163], a[152:155], a[188:191], 0\n" \
X_"a[236:237], v35 offset:40960\n" \
"s_movk_i32 s35, 0x120\n" \
M_"v[160:163], a[216:219], a[252:255], v[160:163]\n" \
R_"a[152:155], v28 offset:36864\n" \
"v_mul_u32_u24_e32 v214, 0xd80, v212\n" \
M_"v[176:179], a[156:159], a[188:191], 0\n" \
R_"a[216:219], v29 offset:36864\n" \
"v_cmp_gt_u32_e32 vcc, s35, v20\n" \
M_"v[176:179], a[220:223], a[252:255], v[176:179]\n" \
X_"a[238:239], v35 offset:41984\n" \
"v_lshrrev_b32_e64 v215, 1, v212\n" \
M_"v[172:175], a[156:159], a[184:187], 0\n" \
"s_and_b32 s4, s2, 7\n" \
"v_cndmask_b32_e32 v194, v219, v214, vcc\n" \
M_"v[172:175], a[220:223], a[248:251], v[172:175]\n" \
"v_lshlrev_b32_e32 v214, 7, v212\n" \
"v_lshrrev_b32_e32 v212, 2, v212\n" \
M_"v[168:171], a[156:159], a[180:183], 0\n" \
"v_and_b32_e64 v212, 2, v212\n" \
"v_and_or_b32 v212, v215, 1, v212\n" \
M_"v[168:171], a[220:223], a[244:247], v[168:171]\n" \
"s_lshl_b32 s5, s4, 2\n" \
"s_bfe_u32 s38, s2, 0x20003\n" \
M_"v[164:167], a[156:159], a[176:179], 0\n" \
"v_lshlrev_b32_e32 v212, 5, v212\n" \
"s_movk_i32 s35, 0x60\n" \
M_"v[164:167], a[220:223], a[240:243], v[164:167]\n" \
R_"a[156:159], v28 offset:38912\n" \
R_"a[220:223], v29 offset:38912\n" \
"s_waitcnt lgkmcnt(15)\n" \
M_"a[0:3], a[128:131], a[160:163], a[0:3]\n" \
"s_addk_i32 s84, 0x7c00\n" \
M_"a[0:3], a[192:195], a[224:227], a[0:3]\n" \
B_"v27, s[72:75], s84 offen offset:2048 lds\n" \
M_"a[4:7], a[128:131], a[164:167], a[4:7]\n" \
X_"a[176:177], v36 offset:32768\n" \
"s_or_b32 s5, s5, s38\n" \
"s_waitcnt lgkmcnt(15)\n" \
M_"a[4:7], a[192:195], a[228:231], a[4:7]\n" \
"s_addk_i32 s84, 0x7c00\n" \
"s_waitcnt lgkmcnt(11)\n" \
M_"a[8:11], a[128:131], a[168:171], a[8:11]\n" \
B_"v27, s[72:75], s84 offen offset:3072 lds\n" \
"s_waitcnt lgkmcnt(9)\n" \
M_"a[8:11], a[192:195], a[232:235], a[8:11]\n" \
X_"a[178:179], v36 offset:33792\n" \
"v_bitop3_b32 v212, v212, s35, v214 bitop3:0x36\n" \
"s_waitcnt lgkmcnt(8)\n" \
M_"a[12:15], a[128:131], a[172:175], a[12:15]\n" \
X_"a[240:241], v36 offset:40960\n" \
"s_lshr_b32 s36, s2, 3\n" \
"s_waitcnt lgkmcnt(5)\n" \
M_"a[12:15], a[192:195], a[236:239], a[12:15]\n" \
"v_add_u32_e32 v212, s34, v212\n" \
"s_mul_i32 s34, s5, 0x12000\n" \
M_"a[28:31], a[132:135], a[172:175], a[28:31]\n" \
"s_or_b32 s40, s34, 0x6000000\n" \
"s_mul_i32 s34, s36, 0x90000\n" \
M_"a[28:31], a[196:199], a[236:239], a[28:31]\n" \
X_"a[242:243], v36 offset:41984\n" \
"s_lshl_b32 s36, s3, 14\n" \
M_"a[24:27], a[132:135], a[168:171], a[24:27]\n" \
"s_add_i32 s34, s34, s36\n" \
"s_lshl_b32 s38, s5, 8\n" \
M_"a[24:27], a[196:199], a[232:235], a[24:27]\n" \
"s_add_i32 s41, s34, 0x4240000\n" \
"s_mul_i32 s34, s5, 36\n" \
M_"a[20:23], a[132:135], a[164:167], a[20:23]\n" \
"s_add_i32 s5, s7, s40\n" \
"s_addk_i32 s5, 0x2000\n" \
M_"a[20:23], a[196:199], a[228:231], a[20:23]\n" \
X_"a[180:181], v37 offset:32768\n" \
"s_mov_b32 s37, 2.0\n" \
M_"a[16:19], a[132:135], a[160:163], a[16:19]\n" \
"s_or_b32 s38, s38, 0x5be8000\n" \
M_"a[16:19], a[196:199], a[224:227], a[16:19]\n" \
"s_cmp_lg_u32 s3, 0\n" \
"s_cselect_b32 s36, s37, s5\n" \
M_"a[32:35], a[136:139], a[160:163], a[32:35]\n" \
"s_mov_b32 m0, s32\n" \
X_"a[182:183], v37 offset:33792\n" \
M_"a[32:35], a[200:203], a[224:227], a[32:35]\n" \
"s_mov_b32 exec_lo, 0x0\n" \
"s_mov_b32 exec_hi, 0xff\n" \
"global_load_lds_dword v[184:185], off\n" \
"s_mov_b64 exec, -1\n" \
M_"a[36:39], a[136:139], a[164:167], a[36:39]\n" \
"s_lshl_b32 s5, s6, 2\n" \
"s_or_b32 s5, s38, s5\n" \
M_"a[36:39], a[200:203], a[228:231], a[36:39]\n" \
X_"a[244:245], v37 offset:40960\n" \
"s_lshl_b32 s35, s3, 10\n" \
M_"a[40:43], a[136:139], a[168:171], a[40:43]\n" \
"s_cmpk_lt_u32 s3, 3\n" \
"s_cselect_b32 s37, s37, s5\n" \
M_"a[40:43], a[200:203], a[232:235], a[40:43]\n" \
"s_mov_b32 m0, s32\n" \
"s_lshl_b32 s5, s4, 16\n" \
M_"a[44:47], a[136:139], a[172:175], a[44:47]\n" \
"s_mov_b32 exec_lo, 0xffff0000\n" \
"s_mov_b32 exec_hi, 0x0\n" \
"global_load_lds_dword v[186:187], off\n" \
"s_mov_b64 exec, -1\n" \
M_"a[44:47], a[200:203], a[236:239], a[44:47]\n" \
X_"a[246:247], v37 offset:41984\n" \
"s_lshl_b32 s4, s4, 11\n" \
M_"a[60:63], a[140:143], a[172:175], a[60:63]\n" \
"v_cndmask_b32_e32 v198, v213, v212, vcc\n" \
"v_or_b32_e32 v212, s35, v20\n" \
M_"a[60:63], a[204:207], a[236:239], a[60:63]\n" \
"s_or_b32 s39, s40, s35\n" \
"s_or_b32 s55, s41, s4\n" \
M_"a[56:59], a[140:143], a[168:171], a[56:59]\n" \
X_"a[184:185], v38 offset:32768\n" \
"v_or_b32_e32 v199, 0x22000, v212\n" \
M_"a[56:59], a[204:207], a[232:235], a[56:59]\n" \
"s_add_i32 s35, s39, s7\n" \
"s_add_i32 s54, s41, s5\n" \
M_"a[52:55], a[140:143], a[164:167], a[52:55]\n" \
"s_add_i32 s55, s55, 0x80000\n" \
"v_mov_b32_e32 v215, 0xffff\n" \
M_"a[52:55], a[204:207], a[228:231], a[52:55]\n" \
X_"a[186:187], v38 offset:33792\n" \
"s_and_b32 s4, s3, 1\n" \
M_"a[48:51], a[140:143], a[160:163], a[48:51]\n" \
"s_lshl_b32 s5, s4, 4\n" \
"v_lshrrev_b32_e64 v212, 4, v20\n" \
M_"a[48:51], a[204:207], a[224:227], a[48:51]\n" \
"v_lshrrev_b32_e32 v218, 6, v20\n" \
"v_and_b32_e32 v219, 12, v218\n" \
M_"a[64:67], a[144:147], a[160:163], a[64:67]\n" \
X_"a[248:249], v38 offset:40960\n" \
"v_and_or_b32 v214, v212, 15, s5\n" \
M_"a[64:67], a[208:211], a[224:227], a[64:67]\n" \
"v_cmp_gt_u32_e64 vcc, v219, v214\n" \
"v_mov_b32_e32 v220, 0xffff0000\n" \
M_"a[68:71], a[144:147], a[164:167], a[68:71]\n" \
"s_lshl_b32 s5, s2, 2\n" \
"v_cndmask_b32_e64 v217, v215, 0, vcc\n" \
M_"a[68:71], a[208:211], a[228:231], a[68:71]\n" \
X_"a[250:251], v38 offset:41984\n" \
"v_cmp_lt_u32_e32 vcc, v219, v214\n" \
M_"a[72:75], a[144:147], a[168:171], a[72:75]\n" \
"v_mov_b32_e32 v213, s23\n" \
"s_or_b32 s5, s5, s3\n" \
M_"a[72:75], a[208:211], a[232:235], a[72:75]\n" \
"v_cndmask_b32_e32 v221, 0, v220, vcc\n" \
"v_or_b32_e32 v12, v217, v221\n" \
M_"a[76:79], a[144:147], a[172:175], a[76:79]\n" \
"v_or_b32_e32 v217, 2, v219\n" \
"v_cmp_gt_u32_e32 vcc, v217, v214\n" \
M_"a[76:79], a[208:211], a[236:239], a[76:79]\n" \
X_"a[188:189], v39 offset:32768\n" \
"v_mov_b32_e64 v216, s25\n" \
M_"a[92:95], a[148:151], a[172:175], a[92:95]\n" \
"s_movk_i32 s40, 0xf400\n" \
"v_cndmask_b32_e64 v221, v215, 0, vcc\n" \
M_"a[92:95], a[212:215], a[236:239], a[92:95]\n" \
"v_cmp_lt_u32_e32 vcc, v217, v214\n" \
"s_lshl_b32 s4, s4, 6\n" \
M_"a[88:91], a[148:151], a[168:171], a[88:91]\n" \
X_"a[190:191], v39 offset:33792\n" \
"v_cndmask_b32_e32 v217, 0, v220, vcc\n" \
M_"a[88:91], a[212:215], a[232:235], a[88:91]\n" \
"v_or_b32_e32 v13, v221, v217\n" \
"v_or_b32_e32 v217, 16, v219\n" \
M_"a[84:87], a[148:151], a[164:167], a[84:87]\n" \
"v_cmp_gt_u32_e32 vcc, v217, v214\n" \
"s_nop 1\n" \
"v_cndmask_b32_e64 v221, v215, 0, vcc\n" \
M_"a[84:87], a[212:215], a[228:231], a[84:87]\n" \
X_"a[252:253], v39 offset:40960\n" \
"v_cmp_lt_u32_e64 vcc, v217, v214\n" \
M_"a[80:83], a[148:151], a[160:163], a[80:83]\n" \
"s_nop 0\n" \
"v_cndmask_b32_e32 v217, 0, v220, vcc\n" \
"v_or_b32_e64 v14, v221, v217\n" \
M_"a[80:83], a[212:215], a[224:227], a[80:83]\n" \
"v_or_b32_e32 v217, 18, v219\n" \
"v_cmp_gt_u32_e32 vcc, v217, v214\n" \
M_"a[96:99], a[152:155], a[160:163], a[96:99]\n" \
X_"a[254:255], v39 offset:41984\n" \
"v_cndmask_b32_e64 v215, v215, 0, vcc\n" \
M_"a[96:99], a[216:219], a[224:227], a[96:99]\n" \
"v_cmp_lt_u32_e32 vcc, v217, v214\n" \
"s_nop 1\n" \
"v_cndmask_b32_e64 v214, 0, v220, vcc\n" \
M_"a[100:103], a[152:155], a[164:167], a[100:103]\n" \
"v_or_b32_e64 v15, v215, v214\n" \
"v_mad_u64_u32 v[214:215], s[6:7], s5, 42, v[212:213]\n" \
M_"a[100:103], a[216:219], a[228:231], a[100:103]\n" \
"s_movk_i32 s5, 0x1440\n" \
"v_cmp_gt_u32_e32 vcc, s5, v214\n" \
M_"a[104:107], a[152:155], a[168:171], a[104:107]\n" \
"v_min_u32_e32 v217, 0xa43f, v214\n" \
"v_mov_b32_e64 v214, s24\n" \
M_"a[104:107], a[216:219], a[232:235], a[104:107]\n" \
"v_cndmask_b32_e32 v215, v216, v213, vcc\n" \
"v_mov_b32_e32 v213, s22\n" \
M_"a[108:111], a[152:155], a[172:175], a[108:111]\n" \
"s_movk_i32 s6, 0xffee\n" \
"v_cndmask_b32_e32 v214, v214, v213, vcc\n" \
M_"a[108:111], a[216:219], a[236:239], a[108:111]\n" \
"v_add_u32_e32 v213, 0xffffebc0, v217\n" \
"s_lshl_b32 s5, s2, 5\n" \
"s_waitcnt lgkmcnt(15)\n" \
M_"a[124:127], a[156:159], a[172:175], a[124:127]\n" \
"v_cndmask_b32_e32 v213, v213, v217, vcc\n" \
"v_lshlrev_b32_e32 v216, 7, v213\n" \
M_"a[124:127], a[220:223], a[236:239], a[124:127]\n" \
"v_mul_u32_u24_e32 v213, 0xe39, v212\n" \
"v_mov_b32_e64 v217, 0\n" \
M_"a[120:123], a[156:159], a[168:171], a[120:123]\n" \
"v_lshrrev_b32_e64 v213, 16, v213\n" \
"v_lshl_add_u64 v[16:17], v[214:215], 0, v[216:217]\n" \
M_"a[120:123], a[220:223], a[232:235], a[120:123]\n" \
"v_mad_i32_i24 v214, v213, s6, v212\n" \
"s_add_i32 s5, s5, 32\n" \
M_"a[116:119], a[156:159], a[164:167], a[116:119]\n" \
"v_lshlrev_b32_e32 v215, 4, v214\n" \
"v_add_u32_e64 v216, s5, v215\n" \
M_"a[116:119], a[220:223], a[228:231], a[116:119]\n" \
"v_and_b32_e32 v216, 0xf0, v216\n" \
"v_cmp_gt_u32_e64 vcc, 16, v214\n" \
M_"a[112:115], a[156:159], a[160:163], a[112:115]\n" \
"s_movk_i32 s7, 0xf800\n" \
"v_cndmask_b32_e32 v214, v215, v216, vcc\n" \
M_"a[112:115], a[220:223], a[224:227], a[112:115]\n" \
"s_waitcnt vmcnt(18)\n" \
"v_readfirstlane_b32 s62, v180\n" \
"s_nop 3\n" \
"s_add_u32 s62, s62, 0x9E3779B1\n" \
"s_waitcnt vmcnt(2) lgkmcnt(0)\n" \
"s_barrier\n" \
M_"v[52:55], a[128:131], a[176:179], v[52:55]\n" \
"s_add_u32 m0, s30, 0x8000\n" \
"s_movk_i32 s84, 0x180\n" \
M_"v[52:55], a[192:195], a[240:243], v[52:55]\n" \
B_"v22, s[64:67], s84 offen offset:0 lds\n" \
M_"v[56:59], a[128:131], a[180:183], v[56:59]\n" \
X_"a[160:161], v32 offset:0\n" \
"v_or_b32_e64 v215, 64, v212\n" \
M_"v[56:59], a[192:195], a[244:247], v[56:59]\n" \
"s_movk_i32 s84, 0x1d80\n" \
M_"v[60:63], a[128:131], a[184:187], v[60:63]\n" \
B_"v23, s[64:67], s84 offen offset:1024 lds\n" \
M_"v[60:63], a[192:195], a[248:251], v[60:63]\n" \
X_"a[162:163], v32 offset:1024\n" \
"v_mul_u32_u24_e32 v216, 0xe39, v215\n" \
M_"v[64:67], a[128:131], a[188:191], v[64:67]\n" \
X_"a[224:225], v32 offset:8192\n" \
"v_lshrrev_b32_e32 v216, 16, v216\n" \
M_"v[64:67], a[192:195], a[252:255], v[64:67]\n" \
R_"a[128:131], v28 offset:16384\n" \
"v_mad_i32_i24 v215, v216, s6, v215\n" \
M_"v[80:83], a[132:135], a[188:191], v[80:83]\n" \
R_"a[192:195], v29 offset:16384\n" \
"v_lshlrev_b32_e64 v217, 4, v215\n" \
M_"v[80:83], a[196:199], a[252:255], v[80:83]\n" \
"s_movk_i32 s84, 0x3980\n" \
M_"v[76:79], a[132:135], a[184:187], v[76:79]\n" \
B_"v22, s[64:67], s84 offen offset:2048 lds\n" \
M_"v[76:79], a[196:199], a[248:251], v[76:79]\n" \
"s_movk_i32 s84, 0x5580\n" \
M_"v[72:75], a[132:135], a[180:183], v[72:75]\n" \
B_"v23, s[64:67], s84 offen offset:3072 lds\n" \
M_"v[72:75], a[196:199], a[244:247], v[72:75]\n" \
"s_add_u32 m0, s31, 0x8000\n" \
"s_mov_b32 s84, 0x180000\n" \
M_"v[68:71], a[132:135], a[176:179], v[68:71]\n" \
B_"v26, s[68:71], s84 offen offset:0 lds\n" \
M_"v[68:71], a[196:199], a[240:243], v[68:71]\n" \
R_"a[132:135], v28 offset:18432\n" \
"v_add_u32_e64 v220, s5, v217\n" \
M_"v[84:87], a[136:139], a[176:179], v[84:87]\n" \
R_"a[196:199], v29 offset:18432\n" \
"v_and_b32_e32 v220, 0xf0, v220\n" \
M_"v[84:87], a[200:203], a[240:243], v[84:87]\n" \
"s_addk_i32 s84, 0x7c00\n" \
M_"v[88:91], a[136:139], a[180:183], v[88:91]\n" \
B_"v26, s[68:71], s84 offen offset:1024 lds\n" \
M_"v[88:91], a[200:203], a[244:247], v[88:91]\n" \
X_"a[226:227], v32 offset:9216\n" \
"v_cmp_gt_u32_e32 vcc, 16, v215\n" \
M_"v[92:95], a[136:139], a[184:187], v[92:95]\n" \
"s_addk_i32 s84, 0x7c00\n" \
M_"v[92:95], a[200:203], a[248:251], v[92:95]\n" \
B_"v27, s[68:71], s84 offen offset:2048 lds\n" \
M_"v[96:99], a[136:139], a[188:191], v[96:99]\n" \
X_"a[164:165], v33 offset:0\n" \
"v_cndmask_b32_e32 v215, v217, v220, vcc\n" \
M_"v[96:99], a[200:203], a[252:255], v[96:99]\n" \
R_"a[136:139], v28 offset:20480\n" \
"v_or_b32_e32 v217, 0x80, v212\n" \
M_"v[112:115], a[140:143], a[188:191], v[112:115]\n" \
R_"a[200:203], v29 offset:20480\n" \
"v_mul_u32_u24_e32 v220, 0xe39, v217\n" \
M_"v[112:115], a[204:207], a[252:255], v[112:115]\n" \
"s_addk_i32 s84, 0x7c00\n" \
M_"v[108:111], a[140:143], a[184:187], v[108:111]\n" \
B_"v27, s[68:71], s84 offen offset:3072 lds\n" \
M_"v[108:111], a[204:207], a[248:251], v[108:111]\n" \
"s_add_u32 m0, s30, 0x9000\n" \
"s_mov_b32 s84, 0x8180\n" \
M_"v[104:107], a[140:143], a[180:183], v[104:107]\n" \
B_"v22, s[64:67], s84 offen offset:0 lds\n" \
M_"v[104:107], a[204:207], a[244:247], v[104:107]\n" \
X_"a[166:167], v33 offset:1024\n" \
"v_lshrrev_b32_e32 v220, 16, v220\n" \
M_"v[100:103], a[140:143], a[176:179], v[100:103]\n" \
X_"a[228:229], v33 offset:8192\n" \
"v_mad_i32_i24 v217, v220, s6, v217\n" \
M_"v[100:103], a[204:207], a[240:243], v[100:103]\n" \
R_"a[140:143], v28 offset:22528\n" \
"v_lshlrev_b32_e64 v221, 4, v217\n" \
M_"v[116:119], a[144:147], a[176:179], v[116:119]\n" \
R_"a[204:207], v29 offset:22528\n" \
"v_add_u32_e64 v222, s5, v221\n" \
M_"v[116:119], a[208:211], a[240:243], v[116:119]\n" \
"s_addk_i32 s84, 0x1c00\n" \
M_"v[120:123], a[144:147], a[180:183], v[120:123]\n" \
B_"v23, s[64:67], s84 offen offset:1024 lds\n" \
M_"v[120:123], a[208:211], a[244:247], v[120:123]\n" \
"s_addk_i32 s84, 0x1c00\n" \
M_"v[124:127], a[144:147], a[184:187], v[124:127]\n" \
B_"v22, s[64:67], s84 offen offset:2048 lds\n" \
M_"v[124:127], a[208:211], a[248:251], v[124:127]\n" \
"s_addk_i32 s84, 0x1c00\n" \
M_"v[128:131], a[144:147], a[188:191], v[128:131]\n" \
B_"v23, s[64:67], s84 offen offset:3072 lds\n" \
M_"v[128:131], a[208:211], a[252:255], v[128:131]\n" \
R_"a[144:147], v28 offset:0\n" \
"v_and_b32_e32 v222, 0xf0, v222\n" \
M_"v[144:147], a[148:151], a[188:191], v[144:147]\n" \
R_"a[208:211], v29 offset:0\n" \
"v_cmp_gt_u32_e32 vcc, 16, v217\n" \
M_"v[144:147], a[212:215], a[252:255], v[144:147]\n" \
"s_add_u32 m0, s31, 0xc000\n" \
"s_mov_b32 s84, 0x180000\n" \
M_"v[140:143], a[148:151], a[184:187], v[140:143]\n" \
B_"v26, s[72:75], s84 offen offset:0 lds\n" \
M_"v[140:143], a[212:215], a[248:251], v[140:143]\n" \
X_"a[230:231], v33 offset:9216\n" \
"v_cndmask_b32_e64 v217, v221, v222, vcc\n" \
M_"v[136:139], a[148:151], a[180:183], v[136:139]\n" \
"s_addk_i32 s84, 0x7c00\n" \
M_"v[136:139], a[212:215], a[244:247], v[136:139]\n" \
B_"v26, s[72:75], s84 offen offset:1024 lds\n" \
M_"v[132:135], a[148:151], a[176:179], v[132:135]\n" \
X_"a[168:169], v34 offset:0\n" \
"v_or_b32_e32 v221, 0xc0, v212\n" \
M_"v[132:135], a[212:215], a[240:243], v[132:135]\n" \
R_"a[148:151], v28 offset:2048\n" \
"v_mul_u32_u24_e32 v222, 0xe39, v221\n" \
M_"v[148:151], a[152:155], a[176:179], v[148:151]\n" \
R_"a[212:215], v29 offset:2048\n" \
"v_lshrrev_b32_e32 v222, 16, v222\n" \
M_"v[148:151], a[216:219], a[240:243], v[148:151]\n" \
X_"a[170:171], v34 offset:1024\n" \
"v_mad_i32_i24 v221, v222, s6, v221\n" \
M_"v[152:155], a[152:155], a[180:183], v[152:155]\n" \
X_"a[232:233], v34 offset:8192\n" \
"v_lshlrev_b32_e64 v223, 4, v221\n" \
M_"v[152:155], a[216:219], a[244:247], v[152:155]\n" \
X_"a[234:235], v34 offset:9216\n" \
"v_add_u32_e64 v224, s5, v223\n" \
M_"v[156:159], a[152:155], a[184:187], v[156:159]\n" \
X_"a[172:173], v35 offset:0\n" \
"v_and_b32_e32 v224, 0xf0, v224\n" \
M_"v[156:159], a[216:219], a[248:251], v[156:159]\n" \
X_"a[174:175], v35 offset:1024\n" \
"v_cmp_gt_u32_e64 vcc, 16, v221\n" \
M_"v[160:163], a[152:155], a[188:191], v[160:163]\n" \
X_"a[236:237], v35 offset:8192\n" \
"v_cndmask_b32_e64 v221, v223, v224, vcc\n" \
M_"v[160:163], a[216:219], a[252:255], v[160:163]\n" \
R_"a[152:155], v28 offset:4096\n" \
"v_or_b32_e32 v223, 0x100, v212\n" \
M_"v[176:179], a[156:159], a[188:191], v[176:179]\n" \
R_"a[216:219], v29 offset:4096\n" \
"v_mul_u32_u24_e32 v224, 0xe39, v223\n" \
M_"v[176:179], a[220:223], a[252:255], v[176:179]\n" \
X_"a[238:239], v35 offset:9216\n" \
"v_lshrrev_b32_e64 v224, 16, v224\n" \
M_"v[172:175], a[156:159], a[184:187], v[172:175]\n" \
"v_mad_i32_i24 v223, v224, s6, v223\n" \
"v_lshlrev_b32_e64 v225, 4, v223\n" \
M_"v[172:175], a[220:223], a[248:251], v[172:175]\n" \
"v_add_u32_e64 v226, s5, v225\n" \
"v_and_b32_e32 v226, 0xf0, v226\n" \
M_"v[168:171], a[156:159], a[180:183], v[168:171]\n" \
"v_cmp_gt_u32_e32 vcc, 16, v223\n" \
"s_nop 1\n" \
"v_cndmask_b32_e64 v223, v225, v226, vcc\n" \
M_"v[168:171], a[220:223], a[244:247], v[168:171]\n" \
"v_or_b32_e32 v225, 0x140, v212\n" \
"v_mul_u32_u24_e32 v226, 0xe39, v225\n" \
M_"v[164:167], a[156:159], a[176:179], v[164:167]\n" \
"v_lshrrev_b32_e64 v226, 16, v226\n" \
"v_mad_i32_i24 v225, v226, s6, v225\n" \
M_"v[164:167], a[220:223], a[240:243], v[164:167]\n" \
R_"a[156:159], v28 offset:6144\n" \
R_"a[220:223], v29 offset:6144\n" \
"s_waitcnt lgkmcnt(15)\n" \
M_"a[0:3], a[128:131], a[160:163], a[0:3]\n" \
"s_addk_i32 s84, 0x7c00\n" \
M_"a[0:3], a[192:195], a[224:227], a[0:3]\n" \
B_"v27, s[72:75], s84 offen offset:2048 lds\n" \
M_"a[4:7], a[128:131], a[164:167], a[4:7]\n" \
X_"a[176:177], v36 offset:0\n" \
"v_lshlrev_b32_e32 v227, 4, v225\n" \
"s_waitcnt lgkmcnt(15)\n" \
M_"a[4:7], a[192:195], a[228:231], a[4:7]\n" \
"s_addk_i32 s84, 0x7c00\n" \
"s_waitcnt lgkmcnt(11)\n" \
M_"a[8:11], a[128:131], a[168:171], a[8:11]\n" \
B_"v27, s[72:75], s84 offen offset:3072 lds\n" \
"s_waitcnt lgkmcnt(9)\n" \
M_"a[8:11], a[192:195], a[232:235], a[8:11]\n" \
X_"a[178:179], v36 offset:1024\n" \
"v_add_u32_e32 v228, s5, v227\n" \
"s_waitcnt lgkmcnt(8)\n" \
M_"a[12:15], a[128:131], a[172:175], a[12:15]\n" \
X_"a[240:241], v36 offset:8192\n" \
"v_and_b32_e32 v228, 0xf0, v228\n" \
"s_waitcnt lgkmcnt(5)\n" \
M_"a[12:15], a[192:195], a[236:239], a[12:15]\n" \
"v_cmp_gt_u32_e32 vcc, 16, v225\n" \
"s_nop 1\n" \
"v_cndmask_b32_e64 v225, v227, v228, vcc\n" \
M_"a[28:31], a[132:135], a[172:175], a[28:31]\n" \
"v_or_b32_e32 v227, 0x180, v212\n" \
"v_mul_u32_u24_e32 v228, 0xe39, v227\n" \
M_"a[28:31], a[196:199], a[236:239], a[28:31]\n" \
X_"a[242:243], v36 offset:9216\n" \
"v_lshrrev_b32_e64 v228, 16, v228\n" \
M_"a[24:27], a[132:135], a[168:171], a[24:27]\n" \
B_"v[200:203], v192, s[76:79], s34 offen offset:0\n" \
M_"a[24:27], a[196:199], a[232:235], a[24:27]\n" \
"v_mad_i32_i24 v227, v228, s6, v227\n" \
"v_lshlrev_b32_e64 v229, 4, v227\n" \
M_"a[20:23], a[132:135], a[164:167], a[20:23]\n" \
"v_add_u32_e64 v230, s5, v229\n" \
"v_and_b32_e32 v230, 0xf0, v230\n" \
M_"a[20:23], a[196:199], a[228:231], a[20:23]\n" \
X_"a[180:181], v37 offset:0\n" \
"v_cmp_gt_u32_e64 vcc, 16, v227\n" \
M_"a[16:19], a[132:135], a[160:163], a[16:19]\n" \
"s_nop 0\n" \
"v_cndmask_b32_e32 v227, v229, v230, vcc\n" \
"v_or_b32_e32 v229, 0x1c0, v212\n" \
M_"a[16:19], a[196:199], a[224:227], a[16:19]\n" \
"v_mul_u32_u24_e32 v230, 0xe39, v229\n" \
"v_lshrrev_b32_e64 v230, 16, v230\n" \
M_"a[32:35], a[136:139], a[160:163], a[32:35]\n" \
B_"v[204:207], v193, s[76:79], s34 offen offset:0\n" \
M_"a[32:35], a[200:203], a[224:227], a[32:35]\n" \
"s_mov_b32 m0, s32\n" \
X_"a[182:183], v37 offset:1024\n" \
M_"a[36:39], a[136:139], a[164:167], a[36:39]\n" \
"s_mov_b32 exec_lo, 0x0\n" \
"s_mov_b32 exec_hi, 0xff00\n" \
"global_load_lds_dword v[184:185], off\n" \
"s_mov_b64 exec, -1\n" \
M_"a[36:39], a[200:203], a[228:231], a[36:39]\n" \
X_"a[244:245], v37 offset:8192\n" \
"v_mad_i32_i24 v229, v230, s6, v229\n" \
M_"a[40:43], a[136:139], a[168:171], a[40:43]\n" \
"v_lshlrev_b32_e32 v231, 4, v229\n" \
"v_add_u32_e64 v232, s5, v231\n" \
M_"a[40:43], a[200:203], a[232:235], a[40:43]\n" \
"buffer_load_dword v208, v194, s[76:79], s34 offen offset:32\n" \
M_"a[44:47], a[136:139], a[172:175], a[44:47]\n" \
X_"a[246:247], v37 offset:9216\n" \
"v_and_b32_e32 v232, 0xf0, v232\n" \
M_"a[44:47], a[200:203], a[236:239], a[44:47]\n" \
"v_cmp_gt_u32_e32 vcc, 16, v229\n" \
"s_nop 1\n" \
"v_cndmask_b32_e64 v229, v231, v232, vcc\n" \
M_"a[60:63], a[140:143], a[172:175], a[60:63]\n" \
"v_or_b32_e32 v231, 0x200, v212\n" \
"v_mul_u32_u24_e32 v232, 0xe39, v231\n" \
M_"a[60:63], a[204:207], a[236:239], a[60:63]\n" \
"v_lshrrev_b32_e64 v232, 16, v232\n" \
"v_mad_i32_i24 v231, v232, s6, v231\n" \
M_"a[56:59], a[140:143], a[168:171], a[56:59]\n" \
"buffer_load_dword v209, v194, s[76:79], s34 offen offset:1184\n" \
M_"a[56:59], a[204:207], a[232:235], a[56:59]\n" \
X_"a[184:185], v38 offset:0\n" \
"v_lshlrev_b32_e64 v233, 4, v231\n" \
M_"a[52:55], a[140:143], a[164:167], a[52:55]\n" \
"v_add_u32_e32 v234, s5, v233\n" \
"s_movk_i32 s5, 0x480\n" \
M_"a[52:55], a[204:207], a[228:231], a[52:55]\n" \
X_"a[186:187], v38 offset:1024\n" \
"v_mad_u32_u24 v0, v213, s5, v214\n" \
M_"a[48:51], a[140:143], a[160:163], a[48:51]\n" \
"v_mul_u32_u24_e32 v213, 0x480, v216\n" \
"s_movk_i32 s6, 0xfc00\n" \
"s_nop 0\n" \
M_"a[48:51], a[204:207], a[224:227], a[48:51]\n" \
"v_add3_u32 v1, v213, v215, s6\n" \
"v_mul_u32_u24_e32 v213, 0x480, v220\n" \
M_"a[64:67], a[144:147], a[160:163], a[64:67]\n" \
"buffer_load_dword v211, v194, s[76:79], s34 offen offset:2336\n" \
M_"a[64:67], a[208:211], a[224:227], a[64:67]\n" \
X_"a[248:249], v38 offset:8192\n" \
"v_add3_u32 v2, v213, v217, s7\n" \
M_"a[68:71], a[144:147], a[164:167], a[68:71]\n" \
"v_mul_u32_u24_e32 v213, 0x480, v222\n" \
"v_add3_u32 v3, v213, v221, s40\n" \
M_"a[68:71], a[208:211], a[228:231], a[68:71]\n" \
X_"a[250:251], v38 offset:9216\n" \
"v_mul_u32_u24_e32 v213, 0x480, v226\n" \
M_"a[72:75], a[144:147], a[168:171], a[72:75]\n" \
"v_add3_u32 v5, v213, v225, s6\n" \
"v_mul_u32_u24_e32 v213, 0x480, v228\n" \
M_"a[72:75], a[208:211], a[232:235], a[72:75]\n" \
"s_mov_b32 s76, s20\n" \
"s_nop 0\n" \
"s_and_b32 s77, s21, 0xffff\n" \
M_"a[76:79], a[144:147], a[172:175], a[76:79]\n" \
"s_mov_b32 s78, 0x480\n" \
"v_add3_u32 v6, v213, v227, s7\n" \
M_"a[76:79], a[208:211], a[236:239], a[76:79]\n" \
"buffer_load_ushort v210, v195, s[76:79], 0 offen\n" \
M_"a[92:95], a[148:151], a[172:175], a[92:95]\n" \
X_"a[188:189], v39 offset:0\n" \
"v_mul_u32_u24_e32 v213, 0x480, v230\n" \
M_"a[92:95], a[212:215], a[236:239], a[92:95]\n" \
"v_add3_u32 v7, v213, v229, s40\n" \
"s_lshl_b32 s7, s3, 3\n" \
M_"a[88:91], a[148:151], a[168:171], a[88:91]\n" \
"s_mov_b32 s76, s8\n" \
"s_and_b32 s77, s9, 0xffff\n" \
M_"a[88:91], a[212:215], a[232:235], a[88:91]\n" \
"s_mov_b32 s78, 0x240000\n" \
X_"a[190:191], v39 offset:1024\n" \
M_"a[84:87], a[148:151], a[164:167], a[84:87]\n" \
"s_lshl_b32 s40, s3, 1\n" \
"s_and_b32 s7, s7, 16\n" \
".fill (L_pb + 48864 - .) / 4, 4, 0xBF800000\n" \
"L_pp12:\n" \
"s_cbranch_execz L_pret\n" \
M_"a[84:87], a[212:215], a[228:231], a[84:87]\n" \
X_"a[252:253], v39 offset:8192\n" \
"s_and_b32 s40, s40, 2\n" \
M_"a[80:83], a[148:151], a[160:163], a[80:83]\n" \
"s_and_b32 s6, s2, 7\n" \
"v_bfe_u32 v213, v20, 7, 1\n" \
M_"a[80:83], a[212:215], a[224:227], a[80:83]\n" \
"s_or_b32 s7, s40, s7\n" \
"v_and_b32_e32 v234, 0xf0, v234\n" \
M_"a[96:99], a[152:155], a[160:163], a[96:99]\n" \
X_"a[254:255], v39 offset:9216\n" \
"v_cmp_gt_u32_e64 vcc, 16, v231\n" \
M_"a[96:99], a[216:219], a[224:227], a[96:99]\n" \
"v_or3_b32 v213, s7, v213, v219\n" \
"v_and_b32_e64 v214, 32, v20\n" \
M_"a[100:103], a[152:155], a[164:167], a[100:103]\n" \
"s_lshl_b32 s6, s6, 5\n" \
"v_cndmask_b32_e32 v231, v233, v234, vcc\n" \
M_"a[100:103], a[216:219], a[228:231], a[100:103]\n" \
"v_mul_u32_u24_e64 v213, 18, v213\n" \
"v_mov_b32_e32 v215, 0x100\n" \
M_"a[104:107], a[152:155], a[168:171], a[104:107]\n" \
"v_mov_b32_e32 v216, s6\n" \
"v_cmp_eq_u32_e32 vcc, 0, v214\n" \
M_"a[104:107], a[216:219], a[232:235], a[104:107]\n" \
"v_and_or_b32 v213, v218, 1, v213\n" \
"v_lshlrev_b32_e64 v212, 3, v212\n" \
M_"a[108:111], a[152:155], a[172:175], a[108:111]\n" \
"v_cndmask_b32_e64 v214, v215, v216, vcc\n" \
"v_mad_u32_u24 v213, v213, s5, v214\n" \
M_"a[108:111], a[216:219], a[236:239], a[108:111]\n" \
"v_and_or_b32 v213, v20, 16, v213\n" \
"v_add_u32_e32 v9, 0x4800, v213\n" \
"s_waitcnt lgkmcnt(15)\n" \
M_"a[124:127], a[156:159], a[172:175], a[124:127]\n" \
"v_mul_u32_u24_e32 v213, 0x120, v218\n" \
"v_and_or_b32 v10, v212, 24, v213\n" \
M_"a[124:127], a[220:223], a[236:239], a[124:127]\n" \
"v_lshrrev_b32_e32 v213, 2, v20\n" \
"v_and_or_b32 v213, v213, 60, s4\n" \
M_"a[120:123], a[156:159], a[168:171], a[120:123]\n" \
"v_lshl_or_b32 v212, s3, 9, v212\n" \
"v_mad_u32_u24 v4, v224, s5, v223\n" \
M_"a[120:123], a[220:223], a[232:235], a[120:123]\n" \
"v_mad_u32_u24 v8, v232, s5, v231\n" \
"v_or_b32_e32 v18, 0x27000, v213\n" \
M_"a[116:119], a[156:159], a[164:167], a[116:119]\n" \
"v_or_b32_e32 v19, 0x27800, v212\n" \
"v_mov_b32_e32 v215, 0x1000c0c\n" \
M_"a[116:119], a[220:223], a[228:231], a[116:119]\n" \
"v_mov_b32_e32 v216, 0xc0c0100\n" \
"v_bfe_u32 v214, v20, 4, 1\n" \
M_"a[112:115], a[156:159], a[160:163], a[112:115]\n" \
"v_lshrrev_b32_e32 v212, 8, v20\n" \
"v_cmp_eq_u32_e32 vcc, 0, v214\n" \
"s_waitcnt vmcnt(7) lgkmcnt(0)\n" \
M_"a[112:115], a[220:223], a[224:227], a[112:115]\n" \
"s_barrier\n" \
M_"v[52:55], a[128:131], a[176:179], v[52:55]\n" \
"s_add_u32 m0, s30, 0x0\n" \
"s_movk_i32 s84, 0x200\n" \
M_"v[52:55], a[192:195], a[240:243], v[52:55]\n" \
B_"v22, s[64:67], s84 offen offset:0 lds\n" \
M_"v[56:59], a[128:131], a[180:183], v[56:59]\n" \
X_"a[160:161], v32 offset:32768\n" \
"v_bfe_u32 v213, v20, 4, 4\n" \
M_"v[56:59], a[192:195], a[244:247], v[56:59]\n" \
"s_movk_i32 s84, 0x1e00\n" \
M_"v[60:63], a[128:131], a[184:187], v[60:63]\n" \
B_"v23, s[64:67], s84 offen offset:1024 lds\n" \
M_"v[60:63], a[192:195], a[248:251], v[60:63]\n" \
X_"a[162:163], v32 offset:33792\n" \
"v_mov_b32_e32 v217, 0xc0c0302\n" \
M_"v[64:67], a[128:131], a[188:191], v[64:67]\n" \
X_"a[224:225], v32 offset:40960\n" \
"v_cndmask_b32_e32 v215, v215, v216, vcc\n" \
M_"v[64:67], a[192:195], a[252:255], v[64:67]\n" \
R_"a[128:131], v28 offset:49152\n" \
"v_mov_b32_e32 v216, 0x3020c0c\n" \
M_"v[80:83], a[132:135], a[188:191], v[80:83]\n" \
R_"a[192:195], v29 offset:49152\n" \
"v_lshl_or_b32 v214, v212, 2, v214\n" \
M_"v[80:83], a[196:199], a[252:255], v[80:83]\n" \
"s_movk_i32 s84, 0x3a00\n" \
M_"v[76:79], a[132:135], a[184:187], v[76:79]\n" \
B_"v22, s[64:67], s84 offen offset:2048 lds\n" \
M_"v[76:79], a[196:199], a[248:251], v[76:79]\n" \
"s_movk_i32 s84, 0x5600\n" \
M_"v[72:75], a[132:135], a[180:183], v[72:75]\n" \
B_"v23, s[64:67], s84 offen offset:3072 lds\n" \
M_"v[72:75], a[196:199], a[244:247], v[72:75]\n" \
"s_add_u32 m0, s31, 0x0\n" \
"s_mov_b32 s84, 0x200000\n" \
M_"v[68:71], a[132:135], a[176:179], v[68:71]\n" \
B_"v26, s[68:71], s84 offen offset:0 lds\n" \
M_"v[68:71], a[196:199], a[240:243], v[68:71]\n" \
R_"a[132:135], v28 offset:51200\n" \
"v_cndmask_b32_e32 v216, v216, v217, vcc\n" \
M_"v[84:87], a[136:139], a[176:179], v[84:87]\n" \
R_"a[196:199], v29 offset:51200\n" \
"v_mov_b32_e32 v217, 0xc0c0c0c\n" \
M_"v[84:87], a[200:203], a[240:243], v[84:87]\n" \
"s_addk_i32 s84, 0x7c00\n" \
M_"v[88:91], a[136:139], a[180:183], v[88:91]\n" \
B_"v26, s[68:71], s84 offen offset:1024 lds\n" \
M_"v[88:91], a[200:203], a[244:247], v[88:91]\n" \
X_"a[226:227], v32 offset:41984\n" \
"v_cmp_gt_u32_e32 vcc, v214, v213\n" \
M_"v[92:95], a[136:139], a[184:187], v[92:95]\n" \
"s_addk_i32 s84, 0x7c00\n" \
M_"v[92:95], a[200:203], a[248:251], v[92:95]\n" \
B_"v27, s[68:71], s84 offen offset:2048 lds\n" \
M_"v[96:99], a[136:139], a[188:191], v[96:99]\n" \
X_"a[164:165], v33 offset:32768\n" \
"v_or_b32_e32 v214, 2, v214\n" \
M_"v[96:99], a[200:203], a[252:255], v[96:99]\n" \
R_"a[136:139], v28 offset:53248\n" \
"s_lshr_b32 s4, s2, 5\n" \
M_"v[112:115], a[140:143], a[188:191], v[112:115]\n" \
R_"a[200:203], v29 offset:53248\n" \
"v_cndmask_b32_e32 v40, v217, v215, vcc\n" \
M_"v[112:115], a[204:207], a[252:255], v[112:115]\n" \
"s_addk_i32 s84, 0x7c00\n" \
M_"v[108:111], a[140:143], a[184:187], v[108:111]\n" \
B_"v27, s[68:71], s84 offen offset:3072 lds\n" \
M_"v[108:111], a[204:207], a[248:251], v[108:111]\n" \
"s_add_u32 m0, s30, 0x1000\n" \
"s_mov_b32 s84, 0x8200\n" \
M_"v[104:107], a[140:143], a[180:183], v[104:107]\n" \
B_"v22, s[64:67], s84 offen offset:0 lds\n" \
M_"v[104:107], a[204:207], a[244:247], v[104:107]\n" \
X_"a[166:167], v33 offset:33792\n" \
"v_cndmask_b32_e32 v42, v215, v217, vcc\n" \
M_"v[100:103], a[140:143], a[176:179], v[100:103]\n" \
X_"a[228:229], v33 offset:40960\n" \
"v_cmp_gt_u32_e64 vcc, v214, v213\n" \
M_"v[100:103], a[204:207], a[240:243], v[100:103]\n" \
R_"a[140:143], v28 offset:55296\n" \
"v_and_b32_e64 v214, 16, v20\n" \
M_"v[116:119], a[144:147], a[176:179], v[116:119]\n" \
R_"a[204:207], v29 offset:55296\n" \
"s_movk_i32 s42, 0x3f80\n" \
M_"v[116:119], a[208:211], a[240:243], v[116:119]\n" \
"s_addk_i32 s84, 0x1c00\n" \
M_"v[120:123], a[144:147], a[180:183], v[120:123]\n" \
B_"v23, s[64:67], s84 offen offset:1024 lds\n" \
M_"v[120:123], a[208:211], a[244:247], v[120:123]\n" \
"s_addk_i32 s84, 0x1c00\n" \
M_"v[124:127], a[144:147], a[184:187], v[124:127]\n" \
B_"v22, s[64:67], s84 offen offset:2048 lds\n" \
M_"v[124:127], a[208:211], a[248:251], v[124:127]\n" \
"s_addk_i32 s84, 0x1c00\n" \
M_"v[128:131], a[144:147], a[188:191], v[128:131]\n" \
B_"v23, s[64:67], s84 offen offset:3072 lds\n" \
M_"v[128:131], a[208:211], a[252:255], v[128:131]\n" \
R_"a[144:147], v28 offset:32768\n" \
"s_xor_b32 s5, s4, 15\n" \
M_"v[144:147], a[148:151], a[188:191], v[144:147]\n" \
R_"a[208:211], v29 offset:32768\n" \
M_"v[144:147], a[212:215], a[252:255], v[144:147]\n" \
"s_add_u32 m0, s31, 0x4000\n" \
"s_mov_b32 s84, 0x200000\n" \
M_"v[140:143], a[148:151], a[184:187], v[140:143]\n" \
B_"v26, s[72:75], s84 offen offset:0 lds\n" \
M_"v[140:143], a[212:215], a[248:251], v[140:143]\n" \
X_"a[230:231], v33 offset:41984\n" \
M_"v[136:139], a[148:151], a[180:183], v[136:139]\n" \
"s_addk_i32 s84, 0x7c00\n" \
M_"v[136:139], a[212:215], a[244:247], v[136:139]\n" \
B_"v26, s[72:75], s84 offen offset:1024 lds\n" \
M_"v[132:135], a[148:151], a[176:179], v[132:135]\n" \
X_"a[168:169], v34 offset:32768\n" \
M_"v[132:135], a[212:215], a[240:243], v[132:135]\n" \
R_"a[148:151], v28 offset:34816\n" \
M_"v[148:151], a[152:155], a[176:179], v[148:151]\n" \
R_"a[212:215], v29 offset:34816\n" \
M_"v[148:151], a[216:219], a[240:243], v[148:151]\n" \
X_"a[170:171], v34 offset:33792\n" \
M_"v[152:155], a[152:155], a[180:183], v[152:155]\n" \
X_"a[232:233], v34 offset:40960\n" \
M_"v[152:155], a[216:219], a[244:247], v[152:155]\n" \
X_"a[234:235], v34 offset:41984\n" \
M_"v[156:159], a[152:155], a[184:187], v[156:159]\n" \
X_"a[172:173], v35 offset:32768\n" \
M_"v[156:159], a[216:219], a[248:251], v[156:159]\n" \
X_"a[174:175], v35 offset:33792\n" \
M_"v[160:163], a[152:155], a[188:191], v[160:163]\n" \
X_"a[236:237], v35 offset:40960\n" \
M_"v[160:163], a[216:219], a[252:255], v[160:163]\n" \
R_"a[152:155], v28 offset:36864\n" \
M_"v[176:179], a[156:159], a[188:191], v[176:179]\n" \
R_"a[216:219], v29 offset:36864\n" \
M_"v[176:179], a[220:223], a[252:255], v[176:179]\n" \
X_"a[238:239], v35 offset:41984\n" \
M_"v[172:175], a[156:159], a[184:187], v[172:175]\n" \
"s_cmpk_lt_u32 s3, 2\n" \
"s_cselect_b32 s6, s5, s4\n" \
M_"v[172:175], a[220:223], a[248:251], v[172:175]\n" \
"s_lshl_b32 s7, s6, 7\n" \
"v_and_b32_e32 v213, 0x320, v20\n" \
M_"v[168:171], a[156:159], a[180:183], v[168:171]\n" \
"s_movk_i32 s41, 0x100\n" \
"v_lshlrev_b32_e64 v214, v214, s42\n" \
M_"v[168:171], a[220:223], a[244:247], v[168:171]\n" \
"v_mov_b32_e32 v215, 0\n" \
"s_add_i32 s40, s7, -16\n" \
M_"v[164:167], a[156:159], a[176:179], v[164:167]\n" \
"v_cndmask_b32_e32 v41, v217, v216, vcc\n" \
"v_cndmask_b32_e32 v43, v216, v217, vcc\n" \
M_"v[164:167], a[220:223], a[240:243], v[164:167]\n" \
R_"a[156:159], v28 offset:38912\n" \
R_"a[220:223], v29 offset:38912\n" \
"s_waitcnt lgkmcnt(15)\n" \
M_"a[0:3], a[128:131], a[160:163], a[0:3]\n" \
"s_addk_i32 s84, 0x7c00\n" \
M_"a[0:3], a[192:195], a[224:227], a[0:3]\n" \
B_"v27, s[72:75], s84 offen offset:2048 lds\n" \
M_"a[4:7], a[128:131], a[164:167], a[4:7]\n" \
X_"a[176:177], v36 offset:32768\n" \
"v_cmp_eq_u32_e32 vcc, s41, v213\n" \
"s_waitcnt lgkmcnt(15)\n" \
M_"a[4:7], a[192:195], a[228:231], a[4:7]\n" \
"s_addk_i32 s84, 0x7c00\n" \
"s_waitcnt lgkmcnt(11)\n" \
M_"a[8:11], a[128:131], a[168:171], a[8:11]\n" \
B_"v27, s[72:75], s84 offen offset:3072 lds\n" \
"s_waitcnt lgkmcnt(9)\n" \
M_"a[8:11], a[192:195], a[232:235], a[8:11]\n" \
X_"a[178:179], v36 offset:33792\n" \
"s_mov_b32 s41, 0\n" \
"s_waitcnt lgkmcnt(8)\n" \
M_"a[12:15], a[128:131], a[172:175], a[12:15]\n" \
X_"a[240:241], v36 offset:40960\n" \
"s_and_b32 s43, s3, 1\n" \
"s_waitcnt lgkmcnt(5)\n" \
M_"a[12:15], a[192:195], a[236:239], a[12:15]\n" \
"v_cndmask_b32_e32 v46, v215, v214, vcc\n" \
"v_and_b32_e32 v213, 0x330, v20\n" \
M_"a[28:31], a[132:135], a[172:175], a[28:31]\n" \
"s_cmp_lg_u32 s7, 0\n" \
"s_cselect_b32 s40, s40, s41\n" \
M_"a[28:31], a[196:199], a[236:239], a[28:31]\n" \
X_"a[242:243], v36 offset:41984\n" \
"s_mov_b32 s42, 0xff800000\n" \
M_"a[24:27], a[132:135], a[168:171], a[24:27]\n" \
"s_cmp_lg_u32 s7, 0\n" \
"s_cselect_b32 s47, s41, s42\n" \
M_"a[24:27], a[196:199], a[232:235], a[24:27]\n" \
"s_lshl_b32 s41, s43, 6\n" \
"v_lshrrev_b32_e32 v215, 6, v20\n" \
M_"a[20:23], a[132:135], a[164:167], a[20:23]\n" \
"v_mov_b32_e32 v214, 0x3f80\n" \
"v_cmp_eq_u32_e64 vcc, 32, v213\n" \
M_"a[20:23], a[196:199], a[228:231], a[20:23]\n" \
X_"a[180:181], v37 offset:32768\n" \
"s_or_b32 s7, s41, s7\n" \
M_"a[16:19], a[132:135], a[160:163], a[16:19]\n" \
"v_lshrrev_b32_e32 v213, 7, v20\n" \
"v_bfe_u32 v216, v20, 6, 2\n" \
M_"a[16:19], a[196:199], a[224:227], a[16:19]\n" \
"v_lshrrev_b32_e64 v219, 1, v20\n" \
"v_bfe_u32 v215, v215, 1, 1\n" \
M_"a[32:35], a[136:139], a[160:163], a[32:35]\n" \
X_"a[182:183], v37 offset:33792\n" \
"v_cndmask_b32_e64 v47, 0, v214, vcc\n" \
M_"a[32:35], a[200:203], a[224:227], a[32:35]\n" \
"v_or_b32_e64 v214, s7, v213\n" \
"v_lshl_or_b32 v217, s43, 3, v213\n" \
M_"a[36:39], a[136:139], a[164:167], a[36:39]\n" \
"v_lshlrev_b32_e32 v218, 10, v212\n" \
"v_lshlrev_b32_e32 v216, 7, v216\n" \
M_"a[36:39], a[200:203], a[228:231], a[36:39]\n" \
X_"a[244:245], v37 offset:40960\n" \
"v_and_b32_e64 v219, 24, v219\n" \
M_"a[40:43], a[136:139], a[168:171], a[40:43]\n" \
"s_mov_b32 m0, s32\n" \
"v_and_or_b32 v213, v213, 2, v215\n" \
M_"a[40:43], a[200:203], a[232:235], a[40:43]\n" \
"s_mov_b32 exec_lo, 0xffffffff\n" \
"s_mov_b32 exec_hi, 0x0\n" \
"global_load_lds_dword v[188:189], off\n" \
"s_mov_b64 exec, -1\n" \
M_"a[44:47], a[136:139], a[172:175], a[44:47]\n" \
X_"a[246:247], v37 offset:41984\n" \
"v_add_u32_e64 v217, s40, v217\n" \
M_"a[44:47], a[200:203], a[236:239], a[44:47]\n" \
"v_or3_b32 v216, v216, v218, v219\n" \
"v_lshlrev_b32_e64 v213, 5, v213\n" \
M_"a[60:63], a[140:143], a[172:175], a[60:63]\n" \
"s_mov_b32 s40, 0x14000\n" \
"s_lshl_b32 s7, s2, 2\n" \
M_"a[60:63], a[204:207], a[236:239], a[60:63]\n" \
"v_or3_b32 v48, v216, v213, s40\n" \
"s_lshl_b32 s40, s43, 2\n" \
M_"a[56:59], a[140:143], a[168:171], a[56:59]\n" \
X_"a[184:185], v38 offset:32768\n" \
"s_lshl_b32 s6, s6, 3\n" \
M_"a[56:59], a[204:207], a[232:235], a[56:59]\n" \
"v_or_b32_e32 v213, s40, v212\n" \
"s_or_b32 s6, s6, s40\n" \
M_"a[52:55], a[140:143], a[164:167], a[52:55]\n" \
"s_and_b32 s40, s7, 0x7c\n" \
"s_or_b32 s40, s40, s3\n" \
M_"a[52:55], a[204:207], a[228:231], a[52:55]\n" \
X_"a[186:187], v38 offset:33792\n" \
M_"a[48:51], a[140:143], a[160:163], a[48:51]\n" \
"s_cmpk_lt_u32 s40, 64\n" \
"s_cselect_b32 s41, s4, s5\n" \
M_"a[48:51], a[204:207], a[224:227], a[48:51]\n" \
"s_lshl_b32 s40, s40, 1\n" \
"s_lshl_b32 s41, s41, 7\n" \
M_"a[64:67], a[144:147], a[160:163], a[64:67]\n" \
X_"a[248:249], v38 offset:40960\n" \
"s_and_b32 s40, s40, 0x7e\n" \
M_"a[64:67], a[208:211], a[224:227], a[64:67]\n" \
"s_or_b32 s40, s41, s40\n" \
"s_mulk_i32 s40, 0x480\n" \
M_"a[68:71], a[144:147], a[164:167], a[68:71]\n" \
"v_add_u32_e64 v49, s40, v20\n" \
"s_mov_b32 s40, 0xc000\n" \
M_"a[68:71], a[208:211], a[228:231], a[68:71]\n" \
X_"a[250:251], v38 offset:41984\n" \
"s_and_b32 s7, s7, 28\n" \
M_"a[72:75], a[144:147], a[168:171], a[72:75]\n" \
"s_bfe_u32 s45, s2, 0x20003\n" \
"s_mov_b32 s41, 0x8000\n" \
M_"a[72:75], a[208:211], a[232:235], a[72:75]\n" \
"s_cmp_lg_u32 s4, 0\n" \
"s_cselect_b32 s51, s40, s41\n" \
M_"a[76:79], a[144:147], a[172:175], a[76:79]\n" \
"s_movk_i32 s40, 0x4000\n" \
"s_lshr_b32 s44, s3, 1\n" \
M_"a[76:79], a[208:211], a[236:239], a[76:79]\n" \
X_"a[188:189], v39 offset:32768\n" \
"s_or_b32 s46, s7, s45\n" \
M_"a[92:95], a[148:151], a[172:175], a[92:95]\n" \
"s_cmp_lg_u32 s4, 0\n" \
"s_cselect_b32 s53, s41, s40\n" \
M_"a[92:95], a[212:215], a[236:239], a[92:95]\n" \
"s_lshl_b32 s40, s44, 15\n" \
"s_lshl_b32 s41, s43, 14\n" \
M_"a[88:91], a[148:151], a[168:171], a[88:91]\n" \
X_"a[190:191], v39 offset:33792\n" \
"s_lshl_b32 s48, s46, 20\n" \
M_"a[88:91], a[212:215], a[232:235], a[88:91]\n" \
"s_or_b32 s41, s40, s41\n" \
"s_add_i32 s49, s48, 0x1000000\n" \
M_"a[84:87], a[148:151], a[164:167], a[84:87]\n" \
"s_waitcnt vmcnt(17)\n" \
"v_or_b32_e32 v204, v210, v204\n" \
"v_or_b32_e64 v206, v210, v206\n" \
M_"a[84:87], a[212:215], a[228:231], a[84:87]\n" \
"v_mov_b32_e64 v210, 0\n" \
"s_add_u32 s88, s35, 0x1000\n" \
M_"a[80:83], a[148:151], a[160:163], a[80:83]\n" \
X_"a[252:253], v39 offset:40960\n" \
"s_or_b32 s42, s49, s41\n" \
M_"a[80:83], a[212:215], a[224:227], a[80:83]\n" \
"ds_write_b128 v196, v[200:203]\n" \
M_"a[96:99], a[152:155], a[160:163], a[96:99]\n" \
X_"a[254:255], v39 offset:41984\n" \
"s_lshl_b32 s4, s4, 16\n" \
M_"a[96:99], a[216:219], a[224:227], a[96:99]\n" \
"s_or_b32 s41, s42, s4\n" \
"s_lshl_b32 s4, s5, 16\n" \
M_"a[100:103], a[152:155], a[164:167], a[100:103]\n" \
"ds_write_b128 v197, v[204:207]\n" \
M_"a[100:103], a[216:219], a[228:231], a[100:103]\n" \
"s_mul_i32 s5, s6, 0x24800\n" \
"s_lshl_b32 s6, s46, 9\n" \
M_"a[104:107], a[152:155], a[168:171], a[104:107]\n" \
"s_lshl_b32 s7, s7, 6\n" \
"s_add_i32 s6, s5, s6\n" \
M_"a[104:107], a[216:219], a[232:235], a[104:107]\n" \
"ds_write_b128 v198, v[208:211]\n" \
M_"a[108:111], a[152:155], a[172:175], a[108:111]\n" \
"s_or_b32 s5, s5, s7\n" \
"s_lshl_b32 s7, s45, 2\n" \
M_"a[108:111], a[216:219], a[236:239], a[108:111]\n" \
"s_or_b32 s5, s5, s7\n" \
"s_lshl_b32 s7, s3, 12\n" \
"s_waitcnt lgkmcnt(15)\n" \
M_"a[124:127], a[156:159], a[172:175], a[124:127]\n" \
"s_or_b32 s50, s49, s7\n" \
"s_lshl_b32 s7, s3, 11\n" \
M_"a[124:127], a[220:223], a[236:239], a[124:127]\n" \
"v_lshrrev_b32_e64 v215, 4, v20\n" \
"v_bfe_u32 v216, v20, 4, 3\n" \
M_"a[120:123], a[156:159], a[168:171], a[120:123]\n" \
"s_or_b32 s42, s42, s4\n" \
"s_lshl_b32 s4, s43, 11\n" \
M_"a[120:123], a[220:223], a[232:235], a[120:123]\n" \
"s_add_i32 s52, s48, s7\n" \
"s_add_i32 s53, s53, s7\n" \
M_"a[116:119], a[156:159], a[164:167], a[116:119]\n" \
"v_mul_u32_u24_e32 v214, 0x480, v214\n" \
"v_bitop3_b32 v218, v212, v215, 7 bitop3:0x78\n" \
M_"a[116:119], a[220:223], a[228:231], a[116:119]\n" \
"v_bitop3_b32 v212, v212, v216, 4 bitop3:0x36\n" \
"s_lshl_b32 s7, s3, 10\n" \
M_"a[112:115], a[156:159], a[160:163], a[112:115]\n" \
"s_or_b32 s4, s40, s4\n" \
"v_lshl_or_b32 v25, v212, 4, v214\n" \
"s_waitcnt vmcnt(1) lgkmcnt(0)\n" \
M_"a[112:115], a[220:223], a[224:227], a[112:115]\n" \
"s_barrier\n" \
M_"v[52:55], a[128:131], a[176:179], v[52:55]\n" \
"s_add_u32 m0, s30, 0x8000\n" \
"s_movk_i32 s84, 0x280\n" \
M_"v[52:55], a[192:195], a[240:243], v[52:55]\n" \
B_"v22, s[64:67], s84 offen offset:0 lds\n" \
M_"v[56:59], a[128:131], a[180:183], v[56:59]\n" \
"s_movk_i32 s84, 0x1e80\n" \
M_"v[56:59], a[192:195], a[244:247], v[56:59]\n" \
B_"v23, s[64:67], s84 offen offset:1024 lds\n" \
M_"v[60:63], a[128:131], a[184:187], v[60:63]\n" \
X_"a[160:161], v32 offset:0\n" \
"v_mul_u32_u24_e32 v212, 0x480, v217\n" \
M_"v[60:63], a[192:195], a[248:251], v[60:63]\n" \
"s_movk_i32 s84, 0x3a80\n" \
M_"v[64:67], a[128:131], a[188:191], v[64:67]\n" \
B_"v22, s[64:67], s84 offen offset:2048 lds\n" \
M_"v[64:67], a[192:195], a[252:255], v[64:67]\n" \
R_"a[128:131], v28 offset:16384\n" \
"v_bitop3_b32 v213, v215, v213, 7 bitop3:0x6c\n" \
M_"v[80:83], a[132:135], a[188:191], v[80:83]\n" \
R_"a[192:195], v29 offset:16384\n" \
"s_or_b32 s40, s7, 0x14000\n" \
M_"v[80:83], a[196:199], a[252:255], v[80:83]\n" \
"s_movk_i32 s84, 0x5680\n" \
M_"v[76:79], a[132:135], a[184:187], v[76:79]\n" \
B_"v23, s[64:67], s84 offen offset:3072 lds\n" \
M_"v[76:79], a[196:199], a[248:251], v[76:79]\n" \
"s_add_u32 m0, s31, 0x8000\n" \
"s_mov_b32 s84, 0x280000\n" \
M_"v[72:75], a[132:135], a[180:183], v[72:75]\n" \
B_"v26, s[68:71], s84 offen offset:0 lds\n" \
M_"v[72:75], a[196:199], a[244:247], v[72:75]\n" \
X_"a[162:163], v32 offset:1024\n" \
"s_lshl_b32 s43, s44, 13\n" \
M_"v[68:71], a[132:135], a[176:179], v[68:71]\n" \
X_"a[224:225], v32 offset:8192\n" \
"s_mulk_i32 s44, 0xe800\n" \
M_"v[68:71], a[196:199], a[240:243], v[68:71]\n" \
R_"a[132:135], v28 offset:18432\n" \
"s_lshl_b32 s7, s3, 8\n" \
M_"v[84:87], a[136:139], a[176:179], v[84:87]\n" \
R_"a[196:199], v29 offset:18432\n" \
"v_lshl_or_b32 v24, v218, 4, v214\n" \
M_"v[84:87], a[200:203], a[240:243], v[84:87]\n" \
"s_addk_i32 s84, 0x7c00\n" \
M_"v[88:91], a[136:139], a[180:183], v[88:91]\n" \
B_"v26, s[68:71], s84 offen offset:1024 lds\n" \
M_"v[88:91], a[200:203], a[244:247], v[88:91]\n" \
"s_addk_i32 s84, 0x7c00\n" \
M_"v[92:95], a[136:139], a[184:187], v[92:95]\n" \
B_"v27, s[68:71], s84 offen offset:2048 lds\n" \
M_"v[92:95], a[200:203], a[248:251], v[92:95]\n" \
"s_addk_i32 s84, 0x7c00\n" \
M_"v[96:99], a[136:139], a[188:191], v[96:99]\n" \
B_"v27, s[68:71], s84 offen offset:3072 lds\n" \
M_"v[96:99], a[200:203], a[252:255], v[96:99]\n" \
R_"a[136:139], v28 offset:20480\n" \
"v_lshl_or_b32 v21, v213, 4, v212\n" \
M_"v[112:115], a[140:143], a[188:191], v[112:115]\n" \
R_"a[200:203], v29 offset:20480\n" \
"s_add_i32 s44, s44, 0x17000\n" \
M_"v[112:115], a[204:207], a[252:255], v[112:115]\n" \
"s_add_u32 m0, s30, 0x9000\n" \
"s_mov_b32 s84, 0x8280\n" \
M_"v[108:111], a[140:143], a[184:187], v[108:111]\n" \
B_"v22, s[64:67], s84 offen offset:0 lds\n" \
M_"v[108:111], a[204:207], a[248:251], v[108:111]\n" \
"s_addk_i32 s84, 0x1c00\n" \
M_"v[104:107], a[140:143], a[180:183], v[104:107]\n" \
B_"v23, s[64:67], s84 offen offset:1024 lds\n" \
M_"v[104:107], a[204:207], a[244:247], v[104:107]\n" \
"s_addk_i32 s84, 0x1c00\n" \
M_"v[100:103], a[140:143], a[176:179], v[100:103]\n" \
B_"v22, s[64:67], s84 offen offset:2048 lds\n" \
M_"v[100:103], a[204:207], a[240:243], v[100:103]\n" \
R_"a[140:143], v28 offset:22528\n" \
"s_or_b32 s45, s7, 0x27000\n" \
M_"v[116:119], a[144:147], a[176:179], v[116:119]\n" \
R_"a[204:207], v29 offset:22528\n" \
"s_or_b32 s46, s4, 0x10000\n" \
M_"v[116:119], a[208:211], a[240:243], v[116:119]\n" \
"s_addk_i32 s84, 0x1c00\n" \
M_"v[120:123], a[144:147], a[180:183], v[120:123]\n" \
B_"v23, s[64:67], s84 offen offset:3072 lds\n" \
M_"v[120:123], a[208:211], a[244:247], v[120:123]\n" \
"s_add_u32 m0, s31, 0xc000\n" \
"s_mov_b32 s84, 0x280000\n" \
M_"v[124:127], a[144:147], a[184:187], v[124:127]\n" \
B_"v26, s[72:75], s84 offen offset:0 lds\n" \
M_"v[124:127], a[208:211], a[248:251], v[124:127]\n" \
"s_addk_i32 s84, 0x7c00\n" \
M_"v[128:131], a[144:147], a[188:191], v[128:131]\n" \
B_"v26, s[72:75], s84 offen offset:1024 lds\n" \
M_"v[128:131], a[208:211], a[252:255], v[128:131]\n" \
R_"a[144:147], v28 offset:0\n" \
"s_add_i32 s48, s6, 0x3000000\n" \
M_"v[144:147], a[148:151], a[188:191], v[144:147]\n" \
R_"a[208:211], v29 offset:0\n" \
"s_add_i32 s49, s5, 0x3004000\n" \
M_"v[144:147], a[212:215], a[252:255], v[144:147]\n" \
R_"v[200:203], v199 offset:0\n" \
"s_add_i32 s52, s52, 0x1010000\n" \
M_"v[140:143], a[148:151], a[184:187], v[140:143]\n" \
R_"v[204:207], v199 offset:4096\n" \
M_"v[140:143], a[212:215], a[248:251], v[140:143]\n" \
X_"a[226:227], v32 offset:9216\n" \
M_"v[136:139], a[148:151], a[180:183], v[136:139]\n" \
X_"a[164:165], v33 offset:0\n" \
M_"v[136:139], a[212:215], a[244:247], v[136:139]\n" \
R_"v[208:211], v199 offset:8192\n" \
M_"v[132:135], a[148:151], a[176:179], v[132:135]\n" \
X_"a[166:167], v33 offset:1024\n" \
M_"v[132:135], a[212:215], a[240:243], v[132:135]\n" \
R_"a[148:151], v28 offset:2048\n" \
M_"v[148:151], a[152:155], a[176:179], v[148:151]\n" \
R_"a[212:215], v29 offset:2048\n" \
M_"v[148:151], a[216:219], a[240:243], v[148:151]\n" \
X_"a[228:229], v33 offset:8192\n" \
M_"v[152:155], a[152:155], a[180:183], v[152:155]\n" \
X_"a[230:231], v33 offset:9216\n" \
M_"v[152:155], a[216:219], a[244:247], v[152:155]\n" \
X_"a[168:169], v34 offset:0\n" \
M_"v[156:159], a[152:155], a[184:187], v[156:159]\n" \
X_"a[170:171], v34 offset:1024\n" \
M_"v[156:159], a[216:219], a[248:251], v[156:159]\n" \
X_"a[232:233], v34 offset:8192\n" \
M_"v[160:163], a[152:155], a[188:191], v[160:163]\n" \
X_"a[234:235], v34 offset:9216\n" \
M_"v[160:163], a[216:219], a[252:255], v[160:163]\n" \
R_"a[152:155], v28 offset:4096\n" \
M_"v[176:179], a[156:159], a[188:191], v[176:179]\n" \
R_"a[216:219], v29 offset:4096\n" \
M_"v[176:179], a[220:223], a[252:255], v[176:179]\n" \
X_"a[172:173], v35 offset:0\n" \
M_"v[172:175], a[156:159], a[184:187], v[172:175]\n" \
X_"a[174:175], v35 offset:1024\n" \
M_"v[172:175], a[220:223], a[248:251], v[172:175]\n" \
X_"a[236:237], v35 offset:8192\n" \
M_"v[168:171], a[156:159], a[180:183], v[168:171]\n" \
X_"a[238:239], v35 offset:9216\n" \
M_"v[168:171], a[220:223], a[244:247], v[168:171]\n" \
M_"v[164:167], a[156:159], a[176:179], v[164:167]\n" \
M_"v[164:167], a[220:223], a[240:243], v[164:167]\n" \
R_"a[156:159], v28 offset:6144\n" \
R_"a[220:223], v29 offset:6144\n" \
"s_waitcnt lgkmcnt(15)\n" \
M_"a[0:3], a[128:131], a[160:163], a[0:3]\n" \
"s_addk_i32 s84, 0x7c00\n" \
M_"a[0:3], a[192:195], a[224:227], a[0:3]\n" \
B_"v27, s[72:75], s84 offen offset:2048 lds\n" \
M_"a[4:7], a[128:131], a[164:167], a[4:7]\n" \
X_"a[176:177], v36 offset:0\n" \
"s_waitcnt lgkmcnt(13)\n" \
M_"a[4:7], a[192:195], a[228:231], a[4:7]\n" \
"s_addk_i32 s84, 0x7c00\n" \
"s_waitcnt lgkmcnt(11)\n" \
M_"a[8:11], a[128:131], a[168:171], a[8:11]\n" \
B_"v27, s[72:75], s84 offen offset:3072 lds\n" \
"s_waitcnt lgkmcnt(9)\n" \
M_"a[8:11], a[192:195], a[232:235], a[8:11]\n" \
X_"a[178:179], v36 offset:1024\n" \
"s_waitcnt lgkmcnt(6)\n" \
M_"a[12:15], a[128:131], a[172:175], a[12:15]\n" \
X_"a[240:241], v36 offset:8192\n" \
"s_waitcnt lgkmcnt(5)\n" \
M_"a[12:15], a[192:195], a[236:239], a[12:15]\n" \
M_"a[28:31], a[132:135], a[172:175], a[28:31]\n" \
M_"a[28:31], a[196:199], a[236:239], a[28:31]\n" \
X_"a[242:243], v36 offset:9216\n" \
M_"a[24:27], a[132:135], a[168:171], a[24:27]\n" \
M_"a[24:27], a[196:199], a[232:235], a[24:27]\n" \
M_"a[20:23], a[132:135], a[164:167], a[20:23]\n" \
M_"a[20:23], a[196:199], a[228:231], a[20:23]\n" \
X_"a[180:181], v37 offset:0\n" \
M_"a[16:19], a[132:135], a[160:163], a[16:19]\n" \
".fill (L_pb + 52960 - .) / 4, 4, 0xBF800000\n" \
"L_pp13:\n" \
"s_cbranch_execz L_pret\n" \
M_"a[16:19], a[196:199], a[224:227], a[16:19]\n" \
M_"a[32:35], a[136:139], a[160:163], a[32:35]\n" \
X_"a[182:183], v37 offset:1024\n" \
M_"a[32:35], a[200:203], a[224:227], a[32:35]\n" \
M_"a[36:39], a[136:139], a[164:167], a[36:39]\n" \
M_"a[36:39], a[200:203], a[228:231], a[36:39]\n" \
"buffer_store_dwordx4 v[200:203], v20, s[80:83], s35 offen sc1\n" \
M_"a[40:43], a[136:139], a[168:171], a[40:43]\n" \
"s_mov_b32 m0, s32\n" \
X_"a[244:245], v37 offset:8192\n" \
M_"a[40:43], a[200:203], a[232:235], a[40:43]\n" \
"s_mov_b32 exec_lo, 0x0\n" \
"s_mov_b32 exec_hi, 0xffffffff\n" \
"global_load_lds_dword v[188:189], off\n" \
"s_mov_b64 exec, -1\n" \
"s_nop 0\n" \
M_"a[44:47], a[136:139], a[172:175], a[44:47]\n" \
X_"a[246:247], v37 offset:9216\n" \
M_"a[44:47], a[200:203], a[236:239], a[44:47]\n" \
M_"a[60:63], a[140:143], a[172:175], a[60:63]\n" \
M_"a[60:63], a[204:207], a[236:239], a[60:63]\n" \
M_"a[56:59], a[140:143], a[168:171], a[56:59]\n" \
X_"a[184:185], v38 offset:0\n" \
M_"a[56:59], a[204:207], a[232:235], a[56:59]\n" \
M_"a[52:55], a[140:143], a[164:167], a[52:55]\n" \
"buffer_store_dwordx4 v[204:207], v20, s[80:83], s88 offen sc1\n" \
M_"a[52:55], a[204:207], a[228:231], a[52:55]\n" \
X_"a[186:187], v38 offset:1024\n" \
M_"a[48:51], a[140:143], a[160:163], a[48:51]\n" \
M_"a[48:51], a[204:207], a[224:227], a[48:51]\n" \
M_"a[64:67], a[144:147], a[160:163], a[64:67]\n" \
X_"a[248:249], v38 offset:8192\n" \
M_"a[64:67], a[208:211], a[224:227], a[64:67]\n" \
M_"a[68:71], a[144:147], a[164:167], a[68:71]\n" \
M_"a[68:71], a[208:211], a[228:231], a[68:71]\n" \
X_"a[250:251], v38 offset:9216\n" \
M_"a[72:75], a[144:147], a[168:171], a[72:75]\n" \
M_"a[72:75], a[208:211], a[232:235], a[72:75]\n" \
M_"a[76:79], a[144:147], a[172:175], a[76:79]\n" \
"buffer_store_dwordx4 v[208:211], v20, s[80:83], s36 offen sc1\n" \
M_"a[76:79], a[208:211], a[236:239], a[76:79]\n" \
X_"a[188:189], v39 offset:0\n" \
M_"a[92:95], a[148:151], a[172:175], a[92:95]\n" \
M_"a[92:95], a[212:215], a[236:239], a[92:95]\n" \
M_"a[88:91], a[148:151], a[168:171], a[88:91]\n" \
X_"a[190:191], v39 offset:1024\n" \
M_"a[88:91], a[212:215], a[232:235], a[88:91]\n" \
M_"a[84:87], a[148:151], a[164:167], a[84:87]\n" \
M_"a[84:87], a[212:215], a[228:231], a[84:87]\n" \
X_"a[252:253], v39 offset:8192\n" \
M_"a[80:83], a[148:151], a[160:163], a[80:83]\n" \
M_"a[80:83], a[212:215], a[224:227], a[80:83]\n" \
M_"a[96:99], a[152:155], a[160:163], a[96:99]\n" \
X_"a[254:255], v39 offset:9216\n" \
M_"a[96:99], a[216:219], a[224:227], a[96:99]\n" \
M_"a[100:103], a[152:155], a[164:167], a[100:103]\n" \
M_"a[100:103], a[216:219], a[228:231], a[100:103]\n" \
M_"a[104:107], a[152:155], a[168:171], a[104:107]\n" \
M_"a[104:107], a[216:219], a[232:235], a[104:107]\n" \
M_"a[108:111], a[152:155], a[172:175], a[108:111]\n" \
M_"a[108:111], a[216:219], a[236:239], a[108:111]\n" \
"s_waitcnt lgkmcnt(15)\n" \
M_"a[124:127], a[156:159], a[172:175], a[124:127]\n" \
M_"a[124:127], a[220:223], a[236:239], a[124:127]\n" \
M_"a[120:123], a[156:159], a[168:171], a[120:123]\n" \
M_"a[120:123], a[220:223], a[232:235], a[120:123]\n" \
M_"a[116:119], a[156:159], a[164:167], a[116:119]\n" \
M_"a[116:119], a[220:223], a[228:231], a[116:119]\n" \
M_"a[112:115], a[156:159], a[160:163], a[112:115]\n" \
"s_waitcnt vmcnt(4) lgkmcnt(0)\n" \
M_"a[112:115], a[220:223], a[224:227], a[112:115]\n" \
"s_barrier\n" \
M_"v[52:55], a[128:131], a[176:179], v[52:55]\n" \
"s_add_u32 m0, s30, 0x0\n" \
"s_movk_i32 s84, 0x300\n" \
M_"v[52:55], a[192:195], a[240:243], v[52:55]\n" \
B_"v22, s[64:67], s84 offen offset:0 lds\n" \
M_"v[56:59], a[128:131], a[180:183], v[56:59]\n" \
X_"a[160:161], v32 offset:32768\n" \
M_"v[56:59], a[192:195], a[244:247], v[56:59]\n" \
"s_movk_i32 s84, 0x1f00\n" \
M_"v[60:63], a[128:131], a[184:187], v[60:63]\n" \
B_"v23, s[64:67], s84 offen offset:1024 lds\n" \
M_"v[60:63], a[192:195], a[248:251], v[60:63]\n" \
X_"a[162:163], v32 offset:33792\n" \
M_"v[64:67], a[128:131], a[188:191], v[64:67]\n" \
X_"a[224:225], v32 offset:40960\n" \
M_"v[64:67], a[192:195], a[252:255], v[64:67]\n" \
R_"a[128:131], v28 offset:49152\n" \
M_"v[80:83], a[132:135], a[188:191], v[80:83]\n" \
R_"a[192:195], v29 offset:49152\n" \
M_"v[80:83], a[196:199], a[252:255], v[80:83]\n" \
"s_movk_i32 s84, 0x3b00\n" \
M_"v[76:79], a[132:135], a[184:187], v[76:79]\n" \
B_"v22, s[64:67], s84 offen offset:2048 lds\n" \
M_"v[76:79], a[196:199], a[248:251], v[76:79]\n" \
"s_movk_i32 s84, 0x5700\n" \
M_"v[72:75], a[132:135], a[180:183], v[72:75]\n" \
B_"v23, s[64:67], s84 offen offset:3072 lds\n" \
M_"v[72:75], a[196:199], a[244:247], v[72:75]\n" \
"s_add_u32 m0, s31, 0x0\n" \
"s_mov_b32 s84, 0x300000\n" \
M_"v[68:71], a[132:135], a[176:179], v[68:71]\n" \
B_"v26, s[68:71], s84 offen offset:0 lds\n" \
M_"v[68:71], a[196:199], a[240:243], v[68:71]\n" \
R_"a[132:135], v28 offset:51200\n" \
M_"v[84:87], a[136:139], a[176:179], v[84:87]\n" \
R_"a[196:199], v29 offset:51200\n" \
M_"v[84:87], a[200:203], a[240:243], v[84:87]\n" \
"s_addk_i32 s84, 0x7c00\n" \
M_"v[88:91], a[136:139], a[180:183], v[88:91]\n" \
B_"v26, s[68:71], s84 offen offset:1024 lds\n" \
M_"v[88:91], a[200:203], a[244:247], v[88:91]\n" \
X_"a[226:227], v32 offset:41984\n" \
M_"v[92:95], a[136:139], a[184:187], v[92:95]\n" \
"s_addk_i32 s84, 0x7c00\n" \
M_"v[92:95], a[200:203], a[248:251], v[92:95]\n" \
B_"v27, s[68:71], s84 offen offset:2048 lds\n" \
M_"v[96:99], a[136:139], a[188:191], v[96:99]\n" \
X_"a[164:165], v33 offset:32768\n" \
M_"v[96:99], a[200:203], a[252:255], v[96:99]\n" \
R_"a[136:139], v28 offset:53248\n" \
M_"v[112:115], a[140:143], a[188:191], v[112:115]\n" \
R_"a[200:203], v29 offset:53248\n" \
M_"v[112:115], a[204:207], a[252:255], v[112:115]\n" \
"s_addk_i32 s84, 0x7c00\n" \
M_"v[108:111], a[140:143], a[184:187], v[108:111]\n" \
B_"v27, s[68:71], s84 offen offset:3072 lds\n" \
M_"v[108:111], a[204:207], a[248:251], v[108:111]\n" \
"s_add_u32 m0, s30, 0x1000\n" \
"s_mov_b32 s84, 0x8300\n" \
M_"v[104:107], a[140:143], a[180:183], v[104:107]\n" \
B_"v22, s[64:67], s84 offen offset:0 lds\n" \
M_"v[104:107], a[204:207], a[244:247], v[104:107]\n" \
X_"a[166:167], v33 offset:33792\n" \
M_"v[100:103], a[140:143], a[176:179], v[100:103]\n" \
X_"a[228:229], v33 offset:40960\n" \
M_"v[100:103], a[204:207], a[240:243], v[100:103]\n" \
R_"a[140:143], v28 offset:55296\n" \
M_"v[116:119], a[144:147], a[176:179], v[116:119]\n" \
R_"a[204:207], v29 offset:55296\n" \
M_"v[116:119], a[208:211], a[240:243], v[116:119]\n" \
"s_addk_i32 s84, 0x1c00\n" \
M_"v[120:123], a[144:147], a[180:183], v[120:123]\n" \
B_"v23, s[64:67], s84 offen offset:1024 lds\n" \
M_"v[120:123], a[208:211], a[244:247], v[120:123]\n" \
"s_addk_i32 s84, 0x1c00\n" \
M_"v[124:127], a[144:147], a[184:187], v[124:127]\n" \
B_"v22, s[64:67], s84 offen offset:2048 lds\n" \
M_"v[124:127], a[208:211], a[248:251], v[124:127]\n" \
"s_addk_i32 s84, 0x1c00\n" \
"s_nop 0\n" \
M_"v[128:131], a[144:147], a[188:191], v[128:131]\n" \
B_"v23, s[64:67], s84 offen offset:3072 lds\n" \
M_"v[128:131], a[208:211], a[252:255], v[128:131]\n" \
R_"a[144:147], v28 offset:32768\n" \
M_"v[144:147], a[148:151], a[188:191], v[144:147]\n" \
R_"a[208:211], v29 offset:32768\n" \
M_"v[144:147], a[212:215], a[252:255], v[144:147]\n" \
"s_add_u32 m0, s31, 0x4000\n" \
"s_mov_b32 s84, 0x300000\n" \
M_"v[140:143], a[148:151], a[184:187], v[140:143]\n" \
B_"v26, s[72:75], s84 offen offset:0 lds\n" \
M_"v[140:143], a[212:215], a[248:251], v[140:143]\n" \
X_"a[230:231], v33 offset:41984\n" \
M_"v[136:139], a[148:151], a[180:183], v[136:139]\n" \
"s_addk_i32 s84, 0x7c00\n" \
"s_nop 0\n" \
M_"v[136:139], a[212:215], a[244:247], v[136:139]\n" \
B_"v26, s[72:75], s84 offen offset:1024 lds\n" \
M_"v[132:135], a[148:151], a[176:179], v[132:135]\n" \
X_"a[168:169], v34 offset:32768\n" \
M_"v[132:135], a[212:215], a[240:243], v[132:135]\n" \
R_"a[148:151], v28 offset:34816\n" \
M_"v[148:151], a[152:155], a[176:179], v[148:151]\n" \
R_"a[212:215], v29 offset:34816\n" \
M_"v[148:151], a[216:219], a[240:243], v[148:151]\n" \
X_"a[170:171], v34 offset:33792\n" \
M_"v[152:155], a[152:155], a[180:183], v[152:155]\n" \
X_"a[232:233], v34 offset:40960\n" \
M_"v[152:155], a[216:219], a[244:247], v[152:155]\n" \
X_"a[234:235], v34 offset:41984\n" \
M_"v[156:159], a[152:155], a[184:187], v[156:159]\n" \
X_"a[172:173], v35 offset:32768\n" \
M_"v[156:159], a[216:219], a[248:251], v[156:159]\n" \
X_"a[174:175], v35 offset:33792\n" \
M_"v[160:163], a[152:155], a[188:191], v[160:163]\n" \
X_"a[236:237], v35 offset:40960\n" \
M_"v[160:163], a[216:219], a[252:255], v[160:163]\n" \
R_"a[152:155], v28 offset:36864\n" \
M_"v[176:179], a[156:159], a[188:191], v[176:179]\n" \
R_"a[216:219], v29 offset:36864\n" \
M_"v[176:179], a[220:223], a[252:255], v[176:179]\n" \
X_"a[238:239], v35 offset:41984\n" \
M_"v[172:175], a[156:159], a[184:187], v[172:175]\n" \
M_"v[172:175], a[220:223], a[248:251], v[172:175]\n" \
M_"v[168:171], a[156:159], a[180:183], v[168:171]\n" \
M_"v[168:171], a[220:223], a[244:247], v[168:171]\n" \
M_"v[164:167], a[156:159], a[176:179], v[164:167]\n" \
M_"v[164:167], a[220:223], a[240:243], v[164:167]\n" \
R_"a[156:159], v28 offset:38912\n" \
R_"a[220:223], v29 offset:38912\n" \
"s_waitcnt lgkmcnt(15)\n" \
M_"a[0:3], a[128:131], a[160:163], a[0:3]\n" \
"s_addk_i32 s84, 0x7c00\n" \
M_"a[0:3], a[192:195], a[224:227], a[0:3]\n" \
B_"v27, s[72:75], s84 offen offset:2048 lds\n" \
M_"a[4:7], a[128:131], a[164:167], a[4:7]\n" \
X_"a[176:177], v36 offset:32768\n" \
"s_waitcnt lgkmcnt(15)\n" \
M_"a[4:7], a[192:195], a[228:231], a[4:7]\n" \
"s_addk_i32 s84, 0x7c00\n" \
"s_waitcnt lgkmcnt(11)\n" \
M_"a[8:11], a[128:131], a[168:171], a[8:11]\n" \
B_"v27, s[72:75], s84 offen offset:3072 lds\n" \
"s_waitcnt lgkmcnt(9)\n" \
M_"a[8:11], a[192:195], a[232:235], a[8:11]\n" \
X_"a[178:179], v36 offset:33792\n" \
"s_waitcnt lgkmcnt(8)\n" \
M_"a[12:15], a[128:131], a[172:175], a[12:15]\n" \
X_"a[240:241], v36 offset:40960\n" \
"s_waitcnt lgkmcnt(5)\n" \
M_"a[12:15], a[192:195], a[236:239], a[12:15]\n" \
M_"a[28:31], a[132:135], a[172:175], a[28:31]\n" \
M_"a[28:31], a[196:199], a[236:239], a[28:31]\n" \
X_"a[242:243], v36 offset:41984\n" \
M_"a[24:27], a[132:135], a[168:171], a[24:27]\n" \
M_"a[24:27], a[196:199], a[232:235], a[24:27]\n" \
M_"a[20:23], a[132:135], a[164:167], a[20:23]\n" \
M_"a[20:23], a[196:199], a[228:231], a[20:23]\n" \
X_"a[180:181], v37 offset:32768\n" \
M_"a[16:19], a[132:135], a[160:163], a[16:19]\n" \
M_"a[16:19], a[196:199], a[224:227], a[16:19]\n" \
M_"a[32:35], a[136:139], a[160:163], a[32:35]\n" \
X_"a[182:183], v37 offset:33792\n" \
M_"a[32:35], a[200:203], a[224:227], a[32:35]\n" \
M_"a[36:39], a[136:139], a[164:167], a[36:39]\n" \
"v_lshlrev_b32_e32 v45, 3, v20\n" \
"v_add_u32_e32 v45, s31, v45\n" \
M_"a[36:39], a[200:203], a[228:231], a[36:39]\n" \
"s_getpc_b64 s[86:87]\n" \
"s_sub_u32 s86, s86, 0x10800\n" \
"s_subb_u32 s87, s87, 0\n" \
"s_mov_b32 m0, s32\n" \
"s_nop 0\n" \
M_"a[40:43], a[136:139], a[168:171], a[40:43]\n" \
"global_load_lds_dword v45, s[86:87]\n" \
M_"a[40:43], a[200:203], a[232:235], a[40:43]\n" \
X_"a[244:245], v37 offset:40960\n" \
M_"a[44:47], a[136:139], a[172:175], a[44:47]\n" \
X_"a[246:247], v37 offset:41984\n" \
M_"a[44:47], a[200:203], a[236:239], a[44:47]\n" \
M_"a[60:63], a[140:143], a[172:175], a[60:63]\n" \
M_"a[60:63], a[204:207], a[236:239], a[60:63]\n" \
M_"a[56:59], a[140:143], a[168:171], a[56:59]\n" \
X_"a[184:185], v38 offset:32768\n" \
M_"a[56:59], a[204:207], a[232:235], a[56:59]\n" \
M_"a[52:55], a[140:143], a[164:167], a[52:55]\n" \
M_"a[52:55], a[204:207], a[228:231], a[52:55]\n" \
X_"a[186:187], v38 offset:33792\n" \
M_"a[48:51], a[140:143], a[160:163], a[48:51]\n" \
M_"a[48:51], a[204:207], a[224:227], a[48:51]\n" \
M_"a[64:67], a[144:147], a[160:163], a[64:67]\n" \
X_"a[248:249], v38 offset:40960\n" \
M_"a[64:67], a[208:211], a[224:227], a[64:67]\n" \
M_"a[68:71], a[144:147], a[164:167], a[68:71]\n" \
M_"a[68:71], a[208:211], a[228:231], a[68:71]\n" \
X_"a[250:251], v38 offset:41984\n" \
M_"a[72:75], a[144:147], a[168:171], a[72:75]\n" \
M_"a[72:75], a[208:211], a[232:235], a[72:75]\n" \
M_"a[76:79], a[144:147], a[172:175], a[76:79]\n" \
M_"a[76:79], a[208:211], a[236:239], a[76:79]\n" \
X_"a[188:189], v39 offset:32768\n" \
M_"a[92:95], a[148:151], a[172:175], a[92:95]\n" \
M_"a[92:95], a[212:215], a[236:239], a[92:95]\n" \
M_"a[88:91], a[148:151], a[168:171], a[88:91]\n" \
X_"a[190:191], v39 offset:33792\n" \
M_"a[88:91], a[212:215], a[232:235], a[88:91]\n" \
M_"a[84:87], a[148:151], a[164:167], a[84:87]\n" \
M_"a[84:87], a[212:215], a[228:231], a[84:87]\n" \
X_"a[252:253], v39 offset:40960\n" \
M_"a[80:83], a[148:151], a[160:163], a[80:83]\n" \
M_"a[80:83], a[212:215], a[224:227], a[80:83]\n" \
M_"a[96:99], a[152:155], a[160:163], a[96:99]\n" \
X_"a[254:255], v39 offset:41984\n" \
M_"a[96:99], a[216:219], a[224:227], a[96:99]\n" \
M_"a[100:103], a[152:155], a[164:167], a[100:103]\n" \
M_"a[100:103], a[216:219], a[228:231], a[100:103]\n" \
M_"a[104:107], a[152:155], a[168:171], a[104:107]\n" \
M_"a[104:107], a[216:219], a[232:235], a[104:107]\n" \
M_"a[108:111], a[152:155], a[172:175], a[108:111]\n" \
M_"a[108:111], a[216:219], a[236:239], a[108:111]\n" \
"s_waitcnt lgkmcnt(15)\n" \
M_"a[124:127], a[156:159], a[172:175], a[124:127]\n" \
M_"a[124:127], a[220:223], a[236:239], a[124:127]\n" \
M_"a[120:123], a[156:159], a[168:171], a[120:123]\n" \
M_"a[120:123], a[220:223], a[232:235], a[120:123]\n" \
M_"a[116:119], a[156:159], a[164:167], a[116:119]\n" \
M_"a[116:119], a[220:223], a[228:231], a[116:119]\n" \
M_"a[112:115], a[156:159], a[160:163], a[112:115]\n" \
"s_waitcnt vmcnt(1) lgkmcnt(0)\n" \
M_"a[112:115], a[220:223], a[224:227], a[112:115]\n" \
"s_barrier\n" \
"v_mov_b32_e32 v45, s62\n" \
"s_mov_b64 exec, 1\n" \
"buffer_store_dword v45, off, s[80:83], s37 offset:128 sc1\n" \
"s_mov_b64 exec, -1\n" \
M_"v[52:55], a[128:131], a[176:179], v[52:55]\n" \
"s_add_u32 m0, s30, 0x8000\n" \
"s_movk_i32 s84, 0x380\n" \
M_"v[52:55], a[192:195], a[240:243], v[52:55]\n" \
B_"v22, s[64:67], s84 offen offset:0 lds\n" \
M_"v[56:59], a[128:131], a[180:183], v[56:59]\n" \
X_"a[160:161], v32 offset:0\n" \
M_"v[56:59], a[192:195], a[244:247], v[56:59]\n" \
"s_movk_i32 s84, 0x1f80\n" \
M_"v[60:63], a[128:131], a[184:187], v[60:63]\n" \
B_"v23, s[64:67], s84 offen offset:1024 lds\n" \
M_"v[60:63], a[192:195], a[248:251], v[60:63]\n" \
X_"a[162:163], v32 offset:1024\n" \
M_"v[64:67], a[128:131], a[188:191], v[64:67]\n" \
X_"a[224:225], v32 offset:8192\n" \
M_"v[64:67], a[192:195], a[252:255], v[64:67]\n" \
R_"a[128:131], v28 offset:16384\n" \
M_"v[80:83], a[132:135], a[188:191], v[80:83]\n" \
R_"a[192:195], v29 offset:16384\n" \
M_"v[80:83], a[196:199], a[252:255], v[80:83]\n" \
"s_movk_i32 s84, 0x3b80\n" \
M_"v[76:79], a[132:135], a[184:187], v[76:79]\n" \
B_"v22, s[64:67], s84 offen offset:2048 lds\n" \
M_"v[76:79], a[196:199], a[248:251], v[76:79]\n" \
"s_movk_i32 s84, 0x5780\n" \
M_"v[72:75], a[132:135], a[180:183], v[72:75]\n" \
B_"v23, s[64:67], s84 offen offset:3072 lds\n" \
M_"v[72:75], a[196:199], a[244:247], v[72:75]\n" \
"s_add_u32 m0, s31, 0x8000\n" \
"s_mov_b32 s84, 0x380000\n" \
M_"v[68:71], a[132:135], a[176:179], v[68:71]\n" \
B_"v26, s[68:71], s84 offen offset:0 lds\n" \
M_"v[68:71], a[196:199], a[240:243], v[68:71]\n" \
R_"a[132:135], v28 offset:18432\n" \
M_"v[84:87], a[136:139], a[176:179], v[84:87]\n" \
R_"a[196:199], v29 offset:18432\n" \
M_"v[84:87], a[200:203], a[240:243], v[84:87]\n" \
"s_addk_i32 s84, 0x7c00\n" \
M_"v[88:91], a[136:139], a[180:183], v[88:91]\n" \
B_"v26, s[68:71], s84 offen offset:1024 lds\n" \
M_"v[88:91], a[200:203], a[244:247], v[88:91]\n" \
X_"a[226:227], v32 offset:9216\n" \
M_"v[92:95], a[136:139], a[184:187], v[92:95]\n" \
"s_addk_i32 s84, 0x7c00\n" \
M_"v[92:95], a[200:203], a[248:251], v[92:95]\n" \
B_"v27, s[68:71], s84 offen offset:2048 lds\n" \
M_"v[96:99], a[136:139], a[188:191], v[96:99]\n" \
X_"a[164:165], v33 offset:0\n" \
M_"v[96:99], a[200:203], a[252:255], v[96:99]\n" \
R_"a[136:139], v28 offset:20480\n" \
M_"v[112:115], a[140:143], a[188:191], v[112:115]\n" \
R_"a[200:203], v29 offset:20480\n" \
M_"v[112:115], a[204:207], a[252:255], v[112:115]\n" \
"s_addk_i32 s84, 0x7c00\n" \
"s_nop 0\n" \
M_"v[108:111], a[140:143], a[184:187], v[108:111]\n" \
B_"v27, s[68:71], s84 offen offset:3072 lds\n" \
M_"v[108:111], a[204:207], a[248:251], v[108:111]\n" \
"s_add_u32 m0, s30, 0x9000\n" \
"s_mov_b32 s84, 0x8380\n" \
M_"v[104:107], a[140:143], a[180:183], v[104:107]\n" \
B_"v22, s[64:67], s84 offen offset:0 lds\n" \
M_"v[104:107], a[204:207], a[244:247], v[104:107]\n" \
X_"a[166:167], v33 offset:1024\n" \
M_"v[100:103], a[140:143], a[176:179], v[100:103]\n" \
X_"a[228:229], v33 offset:8192\n" \
M_"v[100:103], a[204:207], a[240:243], v[100:103]\n" \
R_"a[140:143], v28 offset:22528\n" \
M_"v[116:119], a[144:147], a[176:179], v[116:119]\n" \
R_"a[204:207], v29 offset:22528\n" \
M_"v[116:119], a[208:211], a[240:243], v[116:119]\n" \
"s_addk_i32 s84, 0x1c00\n" \
M_"v[120:123], a[144:147], a[180:183], v[120:123]\n" \
B_"v23, s[64:67], s84 offen offset:1024 lds\n" \
M_"v[120:123], a[208:211], a[244:247], v[120:123]\n" \
"s_addk_i32 s84, 0x1c00\n" \
M_"v[124:127], a[144:147], a[184:187], v[124:127]\n" \
B_"v22, s[64:67], s84 offen offset:2048 lds\n" \
M_"v[124:127], a[208:211], a[248:251], v[124:127]\n" \
"s_addk_i32 s84, 0x1c00\n" \
"s_nop 0\n" \
M_"v[128:131], a[144:147], a[188:191], v[128:131]\n" \
B_"v23, s[64:67], s84 offen offset:3072 lds\n" \
M_"v[128:131], a[208:211], a[252:255], v[128:131]\n" \
R_"a[144:147], v28 offset:0\n" \
M_"v[144:147], a[148:151], a[188:191], v[144:147]\n" \
R_"a[208:211], v29 offset:0\n" \
M_"v[144:147], a[212:215], a[252:255], v[144:147]\n" \
"s_add_u32 m0, s31, 0xc000\n" \
"s_mov_b32 s84, 0x380000\n" \
M_"v[140:143], a[148:151], a[184:187], v[140:143]\n" \
B_"v26, s[72:75], s84 offen offset:0 lds\n" \
M_"v[140:143], a[212:215], a[248:251], v[140:143]\n" \
X_"a[230:231], v33 offset:9216\n" \
M_"v[136:139], a[148:151], a[180:183], v[136:139]\n" \
"s_addk_i32 s84, 0x7c00\n" \
"s_nop 0\n" \
M_"v[136:139], a[212:215], a[244:247], v[136:139]\n" \
B_"v26, s[72:75], s84 offen offset:1024 lds\n" \
M_"v[132:135], a[148:151], a[176:179], v[132:135]\n" \
X_"a[168:169], v34 offset:0\n" \
M_"v[132:135], a[212:215], a[240:243], v[132:135]\n" \
R_"a[148:151], v28 offset:2048\n" \
M_"v[148:151], a[152:155], a[176:179], v[148:151]\n" \
R_"a[212:215], v29 offset:2048\n" \
M_"v[148:151], a[216:219], a[240:243], v[148:151]\n" \
X_"a[170:171], v34 offset:1024\n" \
M_"v[152:155], a[152:155], a[180:183], v[152:155]\n" \
X_"a[232:233], v34 offset:8192\n" \
M_"v[152:155], a[216:219], a[244:247], v[152:155]\n" \
X_"a[234:235], v34 offset:9216\n" \
M_"v[156:159], a[152:155], a[184:187], v[156:159]\n" \
X_"a[172:173], v35 offset:0\n" \
M_"v[156:159], a[216:219], a[248:251], v[156:159]\n" \
X_"a[174:175], v35 offset:1024\n" \
M_"v[160:163], a[152:155], a[188:191], v[160:163]\n" \
X_"a[236:237], v35 offset:8192\n" \
M_"v[160:163], a[216:219], a[252:255], v[160:163]\n" \
R_"a[152:155], v28 offset:4096\n" \
M_"v[176:179], a[156:159], a[188:191], v[176:179]\n" \
R_"a[216:219], v29 offset:4096\n" \
M_"v[176:179], a[220:223], a[252:255], v[176:179]\n" \
X_"a[238:239], v35 offset:9216\n" \
M_"v[172:175], a[156:159], a[184:187], v[172:175]\n" \
M_"v[172:175], a[220:223], a[248:251], v[172:175]\n" \
M_"v[168:171], a[156:159], a[180:183], v[168:171]\n" \
M_"v[168:171], a[220:223], a[244:247], v[168:171]\n" \
M_"v[164:167], a[156:159], a[176:179], v[164:167]\n" \
M_"v[164:167], a[220:223], a[240:243], v[164:167]\n" \
R_"a[156:159], v28 offset:6144\n" \
R_"a[220:223], v29 offset:6144\n" \
"s_waitcnt lgkmcnt(15)\n" \
M_"a[0:3], a[128:131], a[160:163], a[0:3]\n" \
"s_addk_i32 s84, 0x7c00\n" \
M_"a[0:3], a[192:195], a[224:227], a[0:3]\n" \
B_"v27, s[72:75], s84 offen offset:2048 lds\n" \
M_"a[4:7], a[128:131], a[164:167], a[4:7]\n" \
X_"a[176:177], v36 offset:0\n" \
"s_waitcnt lgkmcnt(15)\n" \
M_"a[4:7], a[192:195], a[228:231], a[4:7]\n" \
"s_addk_i32 s84, 0x7c00\n" \
"s_waitcnt lgkmcnt(11)\n" \
M_"a[8:11], a[128:131], a[168:171], a[8:11]\n" \
B_"v27, s[72:75], s84 offen offset:3072 lds\n" \
"s_waitcnt lgkmcnt(9)\n" \
M_"a[8:11], a[192:195], a[232:235], a[8:11]\n" \
X_"a[178:179], v36 offset:1024\n" \
"s_waitcnt lgkmcnt(8)\n" \
M_"a[12:15], a[128:131], a[172:175], a[12:15]\n" \
X_"a[240:241], v36 offset:8192\n" \
"s_waitcnt lgkmcnt(5)\n" \
M_"a[12:15], a[192:195], a[236:239], a[12:15]\n" \
M_"a[28:31], a[132:135], a[172:175], a[28:31]\n" \
M_"a[28:31], a[196:199], a[236:239], a[28:31]\n" \
X_"a[242:243], v36 offset:9216\n" \
M_"a[24:27], a[132:135], a[168:171], a[24:27]\n" \
M_"a[24:27], a[196:199], a[232:235], a[24:27]\n" \
M_"a[20:23], a[132:135], a[164:167], a[20:23]\n" \
M_"a[20:23], a[196:199], a[228:231], a[20:23]\n" \
X_"a[180:181], v37 offset:0\n" \
M_"a[16:19], a[132:135], a[160:163], a[16:19]\n" \
M_"a[16:19], a[196:199], a[224:227], a[16:19]\n" \
M_"a[32:35], a[136:139], a[160:163], a[32:35]\n" \
"v_bfe_u32 v44, v20, 2, 5\n" \
X_"a[182:183], v37 offset:1024\n" \
M_"a[32:35], a[200:203], a[224:227], a[32:35]\n" \
"buffer_load_dword v44, v44, s[80:83], s38 offen offset:128 sc1\n" \
M_"a[36:39], a[136:139], a[164:167], a[36:39]\n" \
M_"a[36:39], a[200:203], a[228:231], a[36:39]\n" \
X_"a[244:245], v37 offset:8192\n" \
M_"a[40:43], a[136:139], a[168:171], a[40:43]\n" \
M_"a[40:43], a[200:203], a[232:235], a[40:43]\n" \
M_"a[44:47], a[136:139], a[172:175], a[44:47]\n" \
X_"a[246:247], v37 offset:9216\n" \
M_"a[44:47], a[200:203], a[236:239], a[44:47]\n" \
M_"a[60:63], a[140:143], a[172:175], a[60:63]\n" \
"global_store_dwordx4 v49, v[0:3], s[28:29] offset:0 sc1\n" \
M_"a[60:63], a[204:207], a[236:239], a[60:63]\n" \
M_"a[56:59], a[140:143], a[168:171], a[56:59]\n" \
X_"a[184:185], v38 offset:0\n" \
M_"a[56:59], a[204:207], a[232:235], a[56:59]\n" \
M_"a[52:55], a[140:143], a[164:167], a[52:55]\n" \
M_"a[52:55], a[204:207], a[228:231], a[52:55]\n" \
X_"a[186:187], v38 offset:1024\n" \
M_"a[48:51], a[140:143], a[160:163], a[48:51]\n" \
M_"a[48:51], a[204:207], a[224:227], a[48:51]\n" \
M_"a[64:67], a[144:147], a[160:163], a[64:67]\n" \
"global_store_dwordx4 v49, v[0:3], s[28:29] offset:1024 sc1\n" \
M_"a[64:67], a[208:211], a[224:227], a[64:67]\n" \
X_"a[248:249], v38 offset:8192\n" \
M_"a[68:71], a[144:147], a[164:167], a[68:71]\n" \
M_"a[68:71], a[208:211], a[228:231], a[68:71]\n" \
X_"a[250:251], v38 offset:9216\n" \
M_"a[72:75], a[144:147], a[168:171], a[72:75]\n" \
M_"a[72:75], a[208:211], a[232:235], a[72:75]\n" \
M_"a[76:79], a[144:147], a[172:175], a[76:79]\n" \
M_"a[76:79], a[208:211], a[236:239], a[76:79]\n" \
X_"a[188:189], v39 offset:0\n" \
M_"a[92:95], a[148:151], a[172:175], a[92:95]\n" \
"s_mov_b64 exec, 0xffff\n" \
"global_store_dwordx4 v49, v[0:3], s[28:29] offset:2048 sc1\n" \
"s_mov_b64 exec, -1\n" \
"s_nop 0\n" \
M_"a[92:95], a[212:215], a[236:239], a[92:95]\n" \
M_"a[88:91], a[148:151], a[168:171], a[88:91]\n" \
X_"a[190:191], v39 offset:1024\n" \
M_"a[88:91], a[212:215], a[232:235], a[88:91]\n" \
M_"a[84:87], a[148:151], a[164:167], a[84:87]\n" \
M_"a[84:87], a[212:215], a[228:231], a[84:87]\n" \
X_"a[252:253], v39 offset:8192\n" \
M_"a[80:83], a[148:151], a[160:163], a[80:83]\n" \
M_"a[80:83], a[212:215], a[224:227], a[80:83]\n" \
M_"a[96:99], a[152:155], a[160:163], a[96:99]\n" \
X_"a[254:255], v39 offset:9216\n" \
M_"a[96:99], a[216:219], a[224:227], a[96:99]\n" \
M_"a[100:103], a[152:155], a[164:167], a[100:103]\n" \
M_"a[100:103], a[216:219], a[228:231], a[100:103]\n" \
M_"a[104:107], a[152:155], a[168:171], a[104:107]\n" \
M_"a[104:107], a[216:219], a[232:235], a[104:107]\n" \
M_"a[108:111], a[152:155], a[172:175], a[108:111]\n" \
M_"a[108:111], a[216:219], a[236:239], a[108:111]\n" \
"s_waitcnt lgkmcnt(15)\n" \
M_"a[124:127], a[156:159], a[172:175], a[124:127]\n" \
M_"a[124:127], a[220:223], a[236:239], a[124:127]\n" \
M_"a[120:123], a[156:159], a[168:171], a[120:123]\n" \
M_"a[120:123], a[220:223], a[232:235], a[120:123]\n" \
M_"a[116:119], a[156:159], a[164:167], a[116:119]\n" \
M_"a[116:119], a[220:223], a[228:231], a[116:119]\n" \
M_"a[112:115], a[156:159], a[160:163], a[112:115]\n" \
"s_waitcnt vmcnt(3) lgkmcnt(0)\n" \
M_"a[112:115], a[220:223], a[224:227], a[112:115]\n" \
"s_barrier\n" \
"v_cmp_ne_u32_e32 vcc, s62, v44\n" \
"s_nop 4\n" \
"s_cbranch_vccnz L_poll61_%=\n" \
"L_pollr61_%=:\n" \
"s_mov_b32 s68, s12\n" \
"s_and_b32 s69, s13, 0xffff\n" \
"s_mov_b32 s70, 0x480000\n" \
"s_mov_b32 s71, 0x20000\n" \
M_"v[52:55], a[128:131], a[176:179], v[52:55]\n" \
"s_add_u32 m0, s40, 0x0\n" \
"s_add_u32 s85, s39, 0x0\n" \
"s_nop 0\n" \
".fill (L_pb + 57056 - .) / 4, 4, 0xBF800000\n" \
"L_pp14:\n" \
"s_cbranch_execz L_pret\n" \
"s_nop 0\n" \
M_"v[52:55], a[192:195], a[240:243], v[52:55]\n" \
B_"v20, s[80:83], s85 offen sc1 lds\n" \
M_"v[56:59], a[128:131], a[180:183], v[56:59]\n" \
X_"a[160:161], v32 offset:32768\n" \
M_"v[56:59], a[192:195], a[244:247], v[56:59]\n" \
"s_add_u32 m0, s40, 0x1000\n" \
"s_add_u32 s85, s39, 0x1000\n" \
M_"v[60:63], a[128:131], a[184:187], v[60:63]\n" \
B_"v20, s[80:83], s85 offen sc1 lds\n" \
M_"v[60:63], a[192:195], a[248:251], v[60:63]\n" \
X_"a[162:163], v32 offset:33792\n" \
M_"v[64:67], a[128:131], a[188:191], v[64:67]\n" \
X_"a[224:225], v32 offset:40960\n" \
M_"v[64:67], a[192:195], a[252:255], v[64:67]\n" \
R_"a[128:131], v28 offset:49152\n" \
M_"v[80:83], a[132:135], a[188:191], v[80:83]\n" \
R_"a[192:195], v29 offset:49152\n" \
M_"v[80:83], a[196:199], a[252:255], v[80:83]\n" \
"s_add_u32 m0, s40, 0x3000\n" \
"s_movk_i32 s84, 0x0\n" \
M_"v[76:79], a[132:135], a[184:187], v[76:79]\n" \
B_"v21, s[76:79], s84 offen offset:0 lds\n" \
M_"v[76:79], a[196:199], a[248:251], v[76:79]\n" \
"s_add_u32 m0, s30, 0x0\n" \
"s_movk_i32 s84, 0x0\n" \
"s_nop 0\n" \
M_"v[72:75], a[132:135], a[180:183], v[72:75]\n" \
B_"v24, s[76:79], s84 offen offset:0 lds\n" \
M_"v[72:75], a[196:199], a[244:247], v[72:75]\n" \
X_"a[226:227], v32 offset:41984\n" \
M_"v[68:71], a[132:135], a[176:179], v[68:71]\n" \
X_"a[164:165], v33 offset:32768\n" \
M_"v[68:71], a[196:199], a[240:243], v[68:71]\n" \
R_"a[132:135], v28 offset:51200\n" \
M_"v[84:87], a[136:139], a[176:179], v[84:87]\n" \
R_"a[196:199], v29 offset:51200\n" \
M_"v[84:87], a[200:203], a[240:243], v[84:87]\n" \
"s_movk_i32 s84, 0x2000\n" \
M_"v[88:91], a[136:139], a[180:183], v[88:91]\n" \
B_"v25, s[76:79], s84 offen offset:1024 lds\n" \
M_"v[88:91], a[200:203], a[244:247], v[88:91]\n" \
"s_movk_i32 s84, 0x4000\n" \
M_"v[92:95], a[136:139], a[184:187], v[92:95]\n" \
B_"v24, s[76:79], s84 offen offset:2048 lds\n" \
M_"v[92:95], a[200:203], a[248:251], v[92:95]\n" \
X_"a[166:167], v33 offset:33792\n" \
M_"v[96:99], a[136:139], a[188:191], v[96:99]\n" \
X_"a[228:229], v33 offset:40960\n" \
M_"v[96:99], a[200:203], a[252:255], v[96:99]\n" \
R_"a[136:139], v28 offset:53248\n" \
M_"v[112:115], a[140:143], a[188:191], v[112:115]\n" \
R_"a[200:203], v29 offset:53248\n" \
M_"v[112:115], a[204:207], a[252:255], v[112:115]\n" \
"s_movk_i32 s84, 0x6000\n" \
M_"v[108:111], a[140:143], a[184:187], v[108:111]\n" \
B_"v25, s[76:79], s84 offen offset:3072 lds\n" \
M_"v[108:111], a[204:207], a[248:251], v[108:111]\n" \
"s_add_u32 m0, s31, 0x0\n" \
"s_movk_i32 s84, 0x0\n" \
M_"v[104:107], a[140:143], a[180:183], v[104:107]\n" \
B_"v26, s[68:71], s84 offen offset:0 lds\n" \
M_"v[104:107], a[204:207], a[244:247], v[104:107]\n" \
"s_movk_i32 s84, 0x7c00\n" \
M_"v[100:103], a[140:143], a[176:179], v[100:103]\n" \
B_"v26, s[68:71], s84 offen offset:1024 lds\n" \
M_"v[100:103], a[204:207], a[240:243], v[100:103]\n" \
R_"a[140:143], v28 offset:55296\n" \
M_"v[116:119], a[144:147], a[176:179], v[116:119]\n" \
R_"a[204:207], v29 offset:55296\n" \
M_"v[116:119], a[208:211], a[240:243], v[116:119]\n" \
"s_addk_i32 s84, 0x7c00\n" \
M_"v[120:123], a[144:147], a[180:183], v[120:123]\n" \
B_"v27, s[68:71], s84 offen offset:2048 lds\n" \
M_"v[120:123], a[208:211], a[244:247], v[120:123]\n" \
X_"a[230:231], v33 offset:41984\n" \
M_"v[124:127], a[144:147], a[184:187], v[124:127]\n" \
"s_addk_i32 s84, 0x7c00\n" \
M_"v[124:127], a[208:211], a[248:251], v[124:127]\n" \
B_"v27, s[68:71], s84 offen offset:3072 lds\n" \
M_"v[128:131], a[144:147], a[188:191], v[128:131]\n" \
X_"a[168:169], v34 offset:32768\n" \
M_"v[128:131], a[208:211], a[252:255], v[128:131]\n" \
R_"a[144:147], v28 offset:32768\n" \
M_"v[144:147], a[148:151], a[188:191], v[144:147]\n" \
R_"a[208:211], v29 offset:32768\n" \
M_"v[144:147], a[212:215], a[252:255], v[144:147]\n" \
"s_add_u32 m0, s30, 0x1000\n" \
"s_mov_b32 s84, 0x9000\n" \
M_"v[140:143], a[148:151], a[184:187], v[140:143]\n" \
B_"v24, s[76:79], s84 offen offset:0 lds\n" \
M_"v[140:143], a[212:215], a[248:251], v[140:143]\n" \
X_"a[170:171], v34 offset:33792\n" \
M_"v[136:139], a[148:151], a[180:183], v[136:139]\n" \
"s_addk_i32 s84, 0x2000\n" \
"s_nop 0\n" \
M_"v[136:139], a[212:215], a[244:247], v[136:139]\n" \
B_"v25, s[76:79], s84 offen offset:1024 lds\n" \
M_"v[132:135], a[148:151], a[176:179], v[132:135]\n" \
X_"a[232:233], v34 offset:40960\n" \
M_"v[132:135], a[212:215], a[240:243], v[132:135]\n" \
R_"a[148:151], v28 offset:34816\n" \
M_"v[148:151], a[152:155], a[176:179], v[148:151]\n" \
R_"a[212:215], v29 offset:34816\n" \
M_"v[148:151], a[216:219], a[240:243], v[148:151]\n" \
X_"a[234:235], v34 offset:41984\n" \
M_"v[152:155], a[152:155], a[180:183], v[152:155]\n" \
X_"a[172:173], v35 offset:32768\n" \
M_"v[152:155], a[216:219], a[244:247], v[152:155]\n" \
X_"a[174:175], v35 offset:33792\n" \
M_"v[156:159], a[152:155], a[184:187], v[156:159]\n" \
X_"a[236:237], v35 offset:40960\n" \
M_"v[156:159], a[216:219], a[248:251], v[156:159]\n" \
X_"a[238:239], v35 offset:41984\n" \
M_"v[160:163], a[152:155], a[188:191], v[160:163]\n" \
M_"v[160:163], a[216:219], a[252:255], v[160:163]\n" \
R_"a[152:155], v28 offset:36864\n" \
M_"v[176:179], a[156:159], a[188:191], v[176:179]\n" \
R_"a[216:219], v29 offset:36864\n" \
M_"v[176:179], a[220:223], a[252:255], v[176:179]\n" \
M_"v[172:175], a[156:159], a[184:187], v[172:175]\n" \
M_"v[172:175], a[220:223], a[248:251], v[172:175]\n" \
M_"v[168:171], a[156:159], a[180:183], v[168:171]\n" \
M_"v[168:171], a[220:223], a[244:247], v[168:171]\n" \
M_"v[164:167], a[156:159], a[176:179], v[164:167]\n" \
M_"v[164:167], a[220:223], a[240:243], v[164:167]\n" \
R_"a[156:159], v28 offset:38912\n" \
R_"a[220:223], v29 offset:38912\n" \
"s_waitcnt lgkmcnt(15)\n" \
M_"a[0:3], a[128:131], a[160:163], a[0:3]\n" \
"s_addk_i32 s84, 0x2000\n" \
M_"a[0:3], a[192:195], a[224:227], a[0:3]\n" \
B_"v24, s[76:79], s84 offen offset:2048 lds\n" \
M_"a[4:7], a[128:131], a[164:167], a[4:7]\n" \
X_"a[176:177], v36 offset:32768\n" \
M_"a[4:7], a[192:195], a[228:231], a[4:7]\n" \
"s_addk_i32 s84, 0x2000\n" \
"s_waitcnt lgkmcnt(13)\n" \
M_"a[8:11], a[128:131], a[168:171], a[8:11]\n" \
B_"v25, s[76:79], s84 offen offset:3072 lds\n" \
"s_waitcnt lgkmcnt(9)\n" \
M_"a[8:11], a[192:195], a[232:235], a[8:11]\n" \
X_"a[178:179], v36 offset:33792\n" \
"s_waitcnt lgkmcnt(8)\n" \
M_"a[12:15], a[128:131], a[172:175], a[12:15]\n" \
X_"a[240:241], v36 offset:40960\n" \
"s_waitcnt lgkmcnt(7)\n" \
M_"a[12:15], a[192:195], a[236:239], a[12:15]\n" \
M_"a[28:31], a[132:135], a[172:175], a[28:31]\n" \
M_"a[28:31], a[196:199], a[236:239], a[28:31]\n" \
"s_add_u32 m0, s30, 0x20000\n" \
"s_movk_i32 s84, 0x100\n" \
M_"a[24:27], a[132:135], a[168:171], a[24:27]\n" \
B_"v24, s[76:79], s84 offen offset:0 lds\n" \
M_"a[24:27], a[196:199], a[232:235], a[24:27]\n" \
X_"a[242:243], v36 offset:41984\n" \
M_"a[20:23], a[132:135], a[164:167], a[20:23]\n" \
"v_accvgpr_read_b32 v184, a12\n" \
"v_accvgpr_read_b32 v185, a13\n" \
M_"a[20:23], a[196:199], a[228:231], a[20:23]\n" \
X_"a[180:181], v37 offset:32768\n" \
"v_accvgpr_read_b32 v186, a14\n" \
M_"a[16:19], a[132:135], a[160:163], a[16:19]\n" \
"s_movk_i32 s84, 0x2100\n" \
"s_nop 0\n" \
M_"a[16:19], a[196:199], a[224:227], a[16:19]\n" \
B_"v25, s[76:79], s84 offen offset:1024 lds\n" \
M_"a[32:35], a[136:139], a[160:163], a[32:35]\n" \
X_"a[182:183], v37 offset:33792\n" \
"v_accvgpr_read_b32 v187, a15\n" \
M_"a[32:35], a[200:203], a[224:227], a[32:35]\n" \
"v_accvgpr_read_b32 v180, a28\n" \
"v_accvgpr_read_b32 v181, a29\n" \
M_"a[36:39], a[136:139], a[164:167], a[36:39]\n" \
"v_accvgpr_read_b32 v182, a30\n" \
"v_accvgpr_read_b32 v183, a31\n" \
M_"a[36:39], a[200:203], a[228:231], a[36:39]\n" \
"s_movk_i32 s84, 0x4100\n" \
"s_nop 0\n" \
M_"a[40:43], a[136:139], a[168:171], a[40:43]\n" \
B_"v24, s[76:79], s84 offen offset:2048 lds\n" \
M_"a[40:43], a[200:203], a[232:235], a[40:43]\n" \
X_"a[244:245], v37 offset:40960\n" \
C_"v184, v184, v185\n" \
M_"a[44:47], a[136:139], a[172:175], a[44:47]\n" \
X_"a[246:247], v37 offset:41984\n" \
C_"v185, v186, v187\n" \
M_"a[44:47], a[200:203], a[236:239], a[44:47]\n" \
C_"v186, v180, v181\n" \
C_"v187, v182, v183\n" \
M_"a[60:63], a[140:143], a[172:175], a[60:63]\n" \
"s_movk_i32 s84, 0x6100\n" \
M_"a[60:63], a[204:207], a[236:239], a[60:63]\n" \
B_"v25, s[76:79], s84 offen offset:3072 lds\n" \
M_"a[56:59], a[140:143], a[168:171], a[56:59]\n" \
X_"a[184:185], v38 offset:32768\n" \
"v_accvgpr_read_b32 v188, a8\n" \
M_"a[56:59], a[204:207], a[232:235], a[56:59]\n" \
"v_accvgpr_read_b32 v189, a9\n" \
"v_accvgpr_read_b32 v190, a10\n" \
M_"a[52:55], a[140:143], a[164:167], a[52:55]\n" \
"s_add_u32 m0, s30, 0x21000\n" \
"s_addk_i32 s84, 0x3000\n" \
M_"a[52:55], a[204:207], a[228:231], a[52:55]\n" \
B_"v24, s[76:79], s84 offen offset:0 lds\n" \
M_"a[48:51], a[140:143], a[160:163], a[48:51]\n" \
X_"a[186:187], v38 offset:33792\n" \
"v_accvgpr_read_b32 v191, a11\n" \
M_"a[48:51], a[204:207], a[224:227], a[48:51]\n" \
"v_accvgpr_read_b32 v180, a24\n" \
"v_accvgpr_read_b32 v181, a25\n" \
M_"a[64:67], a[144:147], a[160:163], a[64:67]\n" \
X_"a[248:249], v38 offset:40960\n" \
"v_accvgpr_read_b32 v182, a26\n" \
M_"a[64:67], a[208:211], a[224:227], a[64:67]\n" \
"s_addk_i32 s84, 0x2000\n" \
"s_nop 0\n" \
M_"a[68:71], a[144:147], a[164:167], a[68:71]\n" \
B_"v25, s[76:79], s84 offen offset:1024 lds\n" \
M_"a[68:71], a[208:211], a[228:231], a[68:71]\n" \
X_"a[250:251], v38 offset:41984\n" \
"v_accvgpr_read_b32 v183, a27\n" \
M_"a[72:75], a[144:147], a[168:171], a[72:75]\n" \
C_"v188, v188, v189\n" \
C_"v189, v190, v191\n" \
M_"a[72:75], a[208:211], a[232:235], a[72:75]\n" \
C_"v190, v180, v181\n" \
C_"v191, v182, v183\n" \
M_"a[76:79], a[144:147], a[172:175], a[76:79]\n" \
"s_addk_i32 s84, 0x2000\n" \
M_"a[76:79], a[208:211], a[236:239], a[76:79]\n" \
B_"v24, s[76:79], s84 offen offset:2048 lds\n" \
M_"a[92:95], a[148:151], a[172:175], a[92:95]\n" \
"s_add_u32 s89, s41, 0x0\n" \
"buffer_store_dwordx4 v[184:187], v20, s[80:83], s89 offen offset:3072 sc1\n" \
M_"a[92:95], a[212:215], a[236:239], a[92:95]\n" \
X_"a[188:189], v39 offset:32768\n" \
"v_accvgpr_read_b32 v192, a4\n" \
M_"a[88:91], a[148:151], a[168:171], a[88:91]\n" \
X_"a[190:191], v39 offset:33792\n" \
"v_accvgpr_read_b32 v193, a5\n" \
M_"a[88:91], a[212:215], a[232:235], a[88:91]\n" \
"s_addk_i32 s84, 0x2000\n" \
M_"a[84:87], a[148:151], a[164:167], a[84:87]\n" \
B_"v25, s[76:79], s84 offen offset:3072 lds\n" \
M_"a[84:87], a[212:215], a[228:231], a[84:87]\n" \
X_"a[252:253], v39 offset:40960\n" \
"v_accvgpr_read_b32 v194, a6\n" \
M_"a[80:83], a[148:151], a[160:163], a[80:83]\n" \
"s_add_u32 s88, s41, 0x0\n" \
"buffer_store_dwordx4 v[188:191], v20, s[80:83], s88 offen offset:2048 sc1\n" \
M_"a[80:83], a[212:215], a[224:227], a[80:83]\n" \
"v_accvgpr_read_b32 v195, a7\n" \
"v_accvgpr_read_b32 v180, a20\n" \
"s_waitcnt lgkmcnt(15)\n" \
"s_nop 0\n" \
M_"a[96:99], a[152:155], a[160:163], a[96:99]\n" \
X_"a[254:255], v39 offset:41984\n" \
"v_accvgpr_read_b32 v181, a21\n" \
M_"a[96:99], a[216:219], a[224:227], a[96:99]\n" \
"v_accvgpr_read_b32 v182, a22\n" \
"v_accvgpr_read_b32 v183, a23\n" \
M_"a[100:103], a[152:155], a[164:167], a[100:103]\n" \
C_"v192, v192, v193\n" \
C_"v193, v194, v195\n" \
M_"a[100:103], a[216:219], a[228:231], a[100:103]\n" \
C_"v194, v180, v181\n" \
C_"v195, v182, v183\n" \
M_"a[104:107], a[152:155], a[168:171], a[104:107]\n" \
"s_add_u32 s89, s41, 0x0\n" \
"s_nop 0\n" \
"buffer_store_dwordx4 v[192:195], v20, s[80:83], s89 offen offset:1024 sc1\n" \
M_"a[104:107], a[216:219], a[232:235], a[104:107]\n" \
"v_accvgpr_read_b32 v196, a0\n" \
"v_accvgpr_read_b32 v197, a1\n" \
M_"a[108:111], a[152:155], a[172:175], a[108:111]\n" \
"v_accvgpr_read_b32 v198, a2\n" \
"v_accvgpr_read_b32 v199, a3\n" \
M_"a[108:111], a[216:219], a[236:239], a[108:111]\n" \
"v_accvgpr_read_b32 v180, a16\n" \
"v_accvgpr_read_b32 v181, a17\n" \
M_"a[124:127], a[156:159], a[172:175], a[124:127]\n" \
"v_accvgpr_read_b32 v182, a18\n" \
"v_accvgpr_read_b32 v183, a19\n" \
M_"a[124:127], a[220:223], a[236:239], a[124:127]\n" \
C_"v196, v196, v197\n" \
C_"v197, v198, v199\n" \
M_"a[120:123], a[156:159], a[168:171], a[120:123]\n" \
C_"v198, v180, v181\n" \
C_"v199, v182, v183\n" \
"s_add_u32 s88, s41, 0x0\n" \
"s_nop 0\n" \
"buffer_store_dwordx4 v[196:199], v20, s[80:83], s88 offen offset:0 sc1\n" \
M_"a[120:123], a[220:223], a[232:235], a[120:123]\n" \
"v_accvgpr_read_b32 v200, a44\n" \
"v_accvgpr_read_b32 v201, a45\n" \
M_"a[116:119], a[156:159], a[164:167], a[116:119]\n" \
"v_accvgpr_read_b32 v202, a46\n" \
"v_accvgpr_read_b32 v203, a47\n" \
M_"a[116:119], a[220:223], a[228:231], a[116:119]\n" \
"v_accvgpr_read_b32 v180, a60\n" \
"v_accvgpr_read_b32 v181, a61\n" \
M_"a[112:115], a[156:159], a[160:163], a[112:115]\n" \
"v_accvgpr_read_b32 v182, a62\n" \
"v_accvgpr_read_b32 v183, a63\n" \
M_"a[112:115], a[220:223], a[224:227], a[112:115]\n" \
C_"v200, v200, v201\n" \
C_"v201, v202, v203\n" \
"s_waitcnt vmcnt(12) lgkmcnt(0)\n" \
"s_barrier\n" \
"v_xor_b32_e32 v36, 0x4000, v36\n" \
"v_xor_b32_e64 v32, 0x0, v48\n" \
"v_xor_b32_e32 v37, 0x4000, v37\n" \
"v_xor_b32_e64 v33, 0x20, v48\n" \
"v_xor_b32_e32 v38, 0x4000, v38\n" \
"v_xor_b32_e64 v34, 0x40, v48\n" \
"v_xor_b32_e32 v39, 0x4000, v39\n" \
"v_xor_b32_e32 v35, 0x60, v48\n" \
"v_add_u32_e32 v28, s43, v28\n" \
"v_add_u32_e32 v29, s43, v29\n" \
M_"v[116:119], a[144:147], a[176:179], v[116:119]\n" \
"s_add_u32 m0, s40, 0x8000\n" \
"s_add_u32 s85, s39, 0x2000\n" \
M_"v[116:119], a[208:211], a[240:243], v[116:119]\n" \
B_"v20, s[80:83], s85 offen sc1 lds\n" \
M_"v[120:123], a[144:147], a[180:183], v[120:123]\n" \
X_"a[160:161], v32 offset:0\n" \
C_"v202, v180, v181\n" \
M_"v[120:123], a[208:211], a[244:247], v[120:123]\n" \
"s_add_u32 m0, s40, 0x9000\n" \
"s_add_u32 s85, s39, 0x3000\n" \
M_"v[124:127], a[144:147], a[184:187], v[124:127]\n" \
B_"v20, s[80:83], s85 offen sc1 lds\n" \
M_"v[124:127], a[208:211], a[248:251], v[124:127]\n" \
X_"a[162:163], v32 offset:512\n" \
C_"v203, v182, v183\n" \
M_"v[128:131], a[144:147], a[188:191], v[128:131]\n" \
X_"a[224:225], v32 offset:4096\n" \
"v_accvgpr_read_b32 v204, a40\n" \
M_"v[128:131], a[208:211], a[252:255], v[128:131]\n" \
R_"a[144:147], v28 offset:8192\n" \
"v_accvgpr_read_b32 v205, a41\n" \
M_"v[144:147], a[148:151], a[188:191], v[144:147]\n" \
R_"a[208:211], v29 offset:8192\n" \
"v_accvgpr_read_b32 v206, a42\n" \
M_"v[144:147], a[212:215], a[252:255], v[144:147]\n" \
"s_add_u32 m0, s40, 0xb000\n" \
"s_movk_i32 s84, 0x80\n" \
M_"v[140:143], a[148:151], a[184:187], v[140:143]\n" \
B_"v21, s[76:79], s84 offen offset:0 lds\n" \
M_"v[140:143], a[212:215], a[248:251], v[140:143]\n" \
"s_add_u32 m0, s30, 0x8000\n" \
"s_movk_i32 s84, 0x80\n" \
M_"v[136:139], a[148:151], a[180:183], v[136:139]\n" \
B_"v24, s[76:79], s84 offen offset:0 lds\n" \
M_"v[136:139], a[212:215], a[244:247], v[136:139]\n" \
X_"a[226:227], v32 offset:4608\n" \
"v_accvgpr_read_b32 v207, a43\n" \
M_"v[132:135], a[148:151], a[176:179], v[132:135]\n" \
X_"a[164:165], v33 offset:0\n" \
"v_accvgpr_read_b32 v180, a56\n" \
M_"v[132:135], a[212:215], a[240:243], v[132:135]\n" \
R_"a[148:151], v28 offset:10240\n" \
"v_accvgpr_read_b32 v181, a57\n" \
M_"v[148:151], a[152:155], a[176:179], v[148:151]\n" \
R_"a[212:215], v29 offset:10240\n" \
"v_accvgpr_read_b32 v182, a58\n" \
M_"v[148:151], a[216:219], a[240:243], v[148:151]\n" \
"s_movk_i32 s84, 0x2080\n" \
M_"v[152:155], a[152:155], a[180:183], v[152:155]\n" \
B_"v25, s[76:79], s84 offen offset:1024 lds\n" \
M_"v[152:155], a[216:219], a[244:247], v[152:155]\n" \
"s_movk_i32 s84, 0x4080\n" \
M_"v[156:159], a[152:155], a[184:187], v[156:159]\n" \
B_"v24, s[76:79], s84 offen offset:2048 lds\n" \
M_"v[156:159], a[216:219], a[248:251], v[156:159]\n" \
X_"a[166:167], v33 offset:512\n" \
"v_accvgpr_read_b32 v183, a59\n" \
M_"v[160:163], a[152:155], a[188:191], v[160:163]\n" \
X_"a[228:229], v33 offset:4096\n" \
C_"v204, v204, v205\n" \
M_"v[160:163], a[216:219], a[252:255], v[160:163]\n" \
R_"a[152:155], v28 offset:12288\n" \
C_"v205, v206, v207\n" \
M_"v[176:179], a[156:159], a[188:191], v[176:179]\n" \
R_"a[216:219], v29 offset:12288\n" \
C_"v206, v180, v181\n" \
M_"v[176:179], a[220:223], a[252:255], v[176:179]\n" \
"s_movk_i32 s84, 0x6080\n" \
M_"v[172:175], a[156:159], a[184:187], v[172:175]\n" \
B_"v25, s[76:79], s84 offen offset:3072 lds\n" \
M_"v[172:175], a[220:223], a[248:251], v[172:175]\n" \
"s_add_u32 m0, s31, 0x8000\n" \
"s_mov_b32 s84, 0x80000\n" \
M_"v[168:171], a[156:159], a[180:183], v[168:171]\n" \
B_"v26, s[68:71], s84 offen offset:0 lds\n" \
M_"v[168:171], a[220:223], a[244:247], v[168:171]\n" \
"s_addk_i32 s84, 0x7c00\n" \
M_"v[164:167], a[156:159], a[176:179], v[164:167]\n" \
B_"v26, s[68:71], s84 offen offset:1024 lds\n" \
M_"v[164:167], a[220:223], a[240:243], v[164:167]\n" \
R_"a[156:159], v28 offset:14336\n" \
C_"v207, v182, v183\n" \
M_"v[84:87], a[136:139], a[176:179], v[84:87]\n" \
R_"a[220:223], v29 offset:14336\n" \
"v_accvgpr_read_b32 v208, a36\n" \
M_"v[84:87], a[200:203], a[240:243], v[84:87]\n" \
"s_addk_i32 s84, 0x7c00\n" \
M_"v[88:91], a[136:139], a[180:183], v[88:91]\n" \
B_"v27, s[68:71], s84 offen offset:2048 lds\n" \
M_"v[88:91], a[200:203], a[244:247], v[88:91]\n" \
X_"a[230:231], v33 offset:4608\n" \
"v_accvgpr_read_b32 v209, a37\n" \
M_"v[92:95], a[136:139], a[184:187], v[92:95]\n" \
"s_addk_i32 s84, 0x7c00\n" \
M_"v[92:95], a[200:203], a[248:251], v[92:95]\n" \
B_"v27, s[68:71], s84 offen offset:3072 lds\n" \
M_"v[96:99], a[136:139], a[188:191], v[96:99]\n" \
X_"a[172:173], v35 offset:0\n" \
"v_accvgpr_read_b32 v210, a38\n" \
M_"v[96:99], a[200:203], a[252:255], v[96:99]\n" \
R_"a[136:139], v28 offset:4096\n" \
"v_accvgpr_read_b32 v211, a39\n" \
M_"v[112:115], a[140:143], a[188:191], v[112:115]\n" \
R_"a[200:203], v29 offset:4096\n" \
"v_accvgpr_read_b32 v180, a52\n" \
M_"v[112:115], a[204:207], a[252:255], v[112:115]\n" \
"s_add_u32 m0, s30, 0x9000\n" \
"s_mov_b32 s84, 0x9080\n" \
M_"v[108:111], a[140:143], a[184:187], v[108:111]\n" \
B_"v24, s[76:79], s84 offen offset:0 lds\n" \
M_"v[108:111], a[204:207], a[248:251], v[108:111]\n" \
X_"a[174:175], v35 offset:512\n" \
"v_accvgpr_read_b32 v181, a53\n" \
M_"v[104:107], a[140:143], a[180:183], v[104:107]\n" \
"s_addk_i32 s84, 0x2000\n" \
"s_nop 0\n" \
M_"v[104:107], a[204:207], a[244:247], v[104:107]\n" \
B_"v25, s[76:79], s84 offen offset:1024 lds\n" \
M_"v[100:103], a[140:143], a[176:179], v[100:103]\n" \
X_"a[236:237], v35 offset:4096\n" \
"v_accvgpr_read_b32 v182, a54\n" \
M_"v[100:103], a[204:207], a[240:243], v[100:103]\n" \
R_"a[140:143], v28 offset:6144\n" \
"v_accvgpr_read_b32 v183, a55\n" \
M_"v[52:55], a[128:131], a[176:179], v[52:55]\n" \
R_"a[204:207], v29 offset:6144\n" \
C_"v208, v208, v209\n" \
M_"v[52:55], a[192:195], a[240:243], v[52:55]\n" \
X_"a[238:239], v35 offset:4608\n" \
C_"v209, v210, v211\n" \
M_"v[56:59], a[128:131], a[180:183], v[56:59]\n" \
X_"a[168:169], v34 offset:0\n" \
C_"v210, v180, v181\n" \
M_"v[56:59], a[192:195], a[244:247], v[56:59]\n" \
X_"a[170:171], v34 offset:512\n" \
C_"v211, v182, v183\n" \
M_"v[60:63], a[128:131], a[184:187], v[60:63]\n" \
X_"a[232:233], v34 offset:4096\n" \
"v_accvgpr_read_b32 v212, a32\n" \
M_"v[60:63], a[192:195], a[248:251], v[60:63]\n" \
X_"a[234:235], v34 offset:4608\n" \
"v_accvgpr_read_b32 v213, a33\n" \
M_"v[64:67], a[128:131], a[188:191], v[64:67]\n" \
"v_accvgpr_read_b32 v214, a34\n" \
"v_accvgpr_read_b32 v215, a35\n" \
M_"v[64:67], a[192:195], a[252:255], v[64:67]\n" \
R_"a[128:131], v28 offset:0\n" \
"v_accvgpr_read_b32 v180, a48\n" \
M_"v[80:83], a[132:135], a[188:191], v[80:83]\n" \
R_"a[192:195], v29 offset:0\n" \
"v_accvgpr_read_b32 v181, a49\n" \
M_"v[80:83], a[196:199], a[252:255], v[80:83]\n" \
"v_accvgpr_read_b32 v182, a50\n" \
"v_accvgpr_read_b32 v183, a51\n" \
M_"v[76:79], a[132:135], a[184:187], v[76:79]\n" \
C_"v212, v212, v213\n" \
C_"v213, v214, v215\n" \
M_"v[76:79], a[196:199], a[248:251], v[76:79]\n" \
C_"v214, v180, v181\n" \
C_"v215, v182, v183\n" \
M_"v[72:75], a[132:135], a[180:183], v[72:75]\n" \
"v_accvgpr_read_b32 v216, a68\n" \
"v_accvgpr_read_b32 v217, a69\n" \
M_"v[72:75], a[196:199], a[244:247], v[72:75]\n" \
"v_accvgpr_read_b32 v218, a70\n" \
"v_accvgpr_read_b32 v219, a71\n" \
M_"v[68:71], a[132:135], a[176:179], v[68:71]\n" \
"v_accvgpr_read_b32 v180, a84\n" \
"v_accvgpr_read_b32 v181, a85\n" \
M_"v[68:71], a[196:199], a[240:243], v[68:71]\n" \
R_"a[132:135], v28 offset:2048\n" \
R_"a[196:199], v29 offset:2048\n" \
"s_waitcnt lgkmcnt(15)\n" \
M_"a[0:3], a[160:163], a[144:147], 0\n" \
"s_addk_i32 s84, 0x2000\n" \
"v_accvgpr_read_b32 v182, a86\n" \
M_"a[0:3], a[224:227], a[208:211], a[0:3]\n" \
B_"v24, s[76:79], s84 offen offset:2048 lds\n" \
"v_accvgpr_read_b32 v183, a87\n" \
M_"a[4:7], a[164:167], a[144:147], 0\n" \
X_"a[176:177], v36 offset:0\n" \
C_"v216, v216, v217\n" \
M_"a[4:7], a[228:231], a[208:211], a[4:7]\n" \
"s_addk_i32 s84, 0x2000\n" \
C_"v217, v218, v219\n" \
"s_waitcnt lgkmcnt(13)\n" \
M_"a[12:15], a[172:175], a[144:147], 0\n" \
B_"v25, s[76:79], s84 offen offset:3072 lds\n" \
C_"v218, v180, v181\n" \
"s_waitcnt lgkmcnt(9)\n" \
M_"a[12:15], a[236:239], a[208:211], a[12:15]\n" \
X_"a[178:179], v36 offset:1024\n" \
C_"v219, v182, v183\n" \
"s_waitcnt lgkmcnt(8)\n" \
M_"a[8:11], a[144:147], a[168:171], 0\n" \
X_"a[240:241], v36 offset:8192\n" \
"v_accvgpr_read_b32 v220, a64\n" \
"s_waitcnt lgkmcnt(7)\n" \
"s_nop 0\n" \
M_"a[8:11], a[208:211], a[232:235], a[8:11]\n" \
X_"a[242:243], v36 offset:9216\n" \
"v_accvgpr_read_b32 v221, a65\n" \
M_"a[24:27], a[148:151], a[168:171], 0\n" \
X_"a[180:181], v37 offset:0\n" \
"v_accvgpr_read_b32 v222, a66\n" \
M_"a[24:27], a[212:215], a[232:235], a[24:27]\n" \
X_"a[182:183], v37 offset:1024\n" \
"v_accvgpr_read_b32 v223, a67\n" \
M_"a[28:31], a[172:175], a[148:151], 0\n" \
X_"a[244:245], v37 offset:8192\n" \
"v_accvgpr_read_b32 v180, a80\n" \
M_"a[28:31], a[236:239], a[212:215], a[28:31]\n" \
"s_add_u32 s89, s41, 0x2000\n" \
"buffer_store_dwordx4 v[200:203], v20, s[80:83], s89 offen offset:3072 sc1\n" \
M_"a[20:23], a[164:167], a[148:151], 0\n" \
X_"a[246:247], v37 offset:9216\n" \
"v_accvgpr_read_b32 v181, a81\n" \
M_"a[20:23], a[228:231], a[212:215], a[20:23]\n" \
X_"a[184:185], v38 offset:0\n" \
"v_accvgpr_read_b32 v182, a82\n" \
M_"a[16:19], a[160:163], a[148:151], 0\n" \
X_"a[186:187], v38 offset:1024\n" \
".fill (L_pb + 61152 - .) / 4, 4, 0xBF800000\n" \
"L_pp15:\n" \
"s_cbranch_execz L_pret\n" \
"s_nop 0\n" \
"v_accvgpr_read_b32 v183, a83\n" \
M_"a[16:19], a[224:227], a[212:215], a[16:19]\n" \
C_"v220, v220, v221\n" \
C_"v221, v222, v223\n" \
C_"v222, v180, v181\n" \
M_"a[32:35], a[160:163], a[152:155], 0\n" \
"s_add_u32 s88, s41, 0x2000\n" \
"buffer_store_dwordx4 v[204:207], v20, s[80:83], s88 offen offset:2048 sc1\n" \
M_"a[32:35], a[224:227], a[216:219], a[32:35]\n" \
X_"a[248:249], v38 offset:8192\n" \
C_"v223, v182, v183\n" \
M_"a[36:39], a[164:167], a[152:155], 0\n" \
X_"a[250:251], v38 offset:9216\n" \
C_"v224, v128, v129\n" \
M_"a[36:39], a[228:231], a[216:219], a[36:39]\n" \
X_"a[188:189], v39 offset:0\n" \
C_"v225, v130, v131\n" \
M_"a[44:47], a[172:175], a[152:155], 0\n" \
C_"v226, v144, v145\n" \
C_"v227, v146, v147\n" \
C_"v228, v124, v125\n" \
M_"a[44:47], a[236:239], a[216:219], a[44:47]\n" \
X_"a[190:191], v39 offset:1024\n" \
C_"v229, v126, v127\n" \
M_"a[40:43], a[152:155], a[168:171], 0\n" \
"s_add_u32 s89, s41, 0x2000\n" \
"buffer_store_dwordx4 v[208:211], v20, s[80:83], s89 offen offset:1024 sc1\n" \
M_"a[40:43], a[216:219], a[232:235], a[40:43]\n" \
X_"a[252:253], v39 offset:8192\n" \
C_"v230, v140, v141\n" \
M_"a[56:59], a[156:159], a[168:171], 0\n" \
X_"a[254:255], v39 offset:9216\n" \
C_"v231, v142, v143\n" \
M_"a[56:59], a[220:223], a[232:235], a[56:59]\n" \
C_"v232, v120, v121\n" \
C_"v233, v122, v123\n" \
C_"v234, v136, v137\n" \
M_"a[60:63], a[172:175], a[156:159], 0\n" \
C_"v235, v138, v139\n" \
C_"v236, v116, v117\n" \
C_"v237, v118, v119\n" \
M_"a[60:63], a[236:239], a[220:223], a[60:63]\n" \
"s_add_u32 s88, s41, 0x2000\n" \
"buffer_store_dwordx4 v[212:215], v20, s[80:83], s88 offen offset:0 sc1\n" \
M_"a[52:55], a[164:167], a[156:159], 0\n" \
C_"v238, v132, v133\n" \
C_"v239, v134, v135\n" \
C_"v240, v160, v161\n" \
M_"a[52:55], a[228:231], a[220:223], a[52:55]\n" \
C_"v241, v162, v163\n" \
C_"v242, v176, v177\n" \
C_"v243, v178, v179\n" \
M_"a[48:51], a[160:163], a[156:159], 0\n" \
C_"v244, v156, v157\n" \
C_"v245, v158, v159\n" \
C_"v246, v172, v173\n" \
M_"a[48:51], a[224:227], a[220:223], a[48:51]\n" \
C_"v247, v174, v175\n" \
C_"v184, v152, v153\n" \
C_"v185, v154, v155\n" \
M_"a[64:67], a[164:167], a[140:143], 0\n" \
C_"v186, v168, v169\n" \
C_"v187, v170, v171\n" \
C_"v188, v148, v149\n" \
M_"a[64:67], a[228:231], a[204:207], a[64:67]\n" \
C_"v189, v150, v151\n" \
C_"v190, v164, v165\n" \
C_"v191, v166, v167\n" \
M_"a[80:83], a[172:175], a[140:143], 0\n" \
C_"v192, v96, v97\n" \
C_"v193, v98, v99\n" \
C_"v194, v112, v113\n" \
M_"a[80:83], a[236:239], a[204:207], a[80:83]\n" \
C_"v195, v114, v115\n" \
C_"v196, v92, v93\n" \
C_"v197, v94, v95\n" \
M_"a[68:71], a[140:143], a[168:171], 0\n" \
C_"v198, v108, v109\n" \
C_"v199, v110, v111\n" \
C_"v252, v88, v89\n" \
M_"a[68:71], a[204:207], a[232:235], a[68:71]\n" \
C_"v253, v90, v91\n" \
C_"v254, v104, v105\n" \
C_"v255, v106, v107\n" \
"s_waitcnt vmcnt(4) lgkmcnt(0)\n" \
"s_barrier\n" \
M_"v[116:119], a[176:179], a[144:147], 0\n" \
"s_add_u32 m0, s40, 0x0\n" \
"s_nop 0\n" \
"s_add_u32 s85, s39, 0x4000\n" \
"v_accvgpr_write_b32 a84, v252\n" \
M_"v[116:119], a[240:243], a[208:211], v[116:119]\n" \
B_"v20, s[80:83], s85 offen sc1 lds\n" \
"v_accvgpr_write_b32 a85, v253\n" \
M_"v[120:123], a[180:183], a[144:147], 0\n" \
X_"a[160:161], v32 offset:32768\n" \
"v_accvgpr_write_b32 a86, v254\n" \
M_"v[120:123], a[244:247], a[208:211], v[120:123]\n" \
X_"a[162:163], v32 offset:33280\n" \
"v_accvgpr_write_b32 a87, v255\n" \
M_"v[124:127], a[184:187], a[144:147], 0\n" \
"s_add_u32 m0, s40, 0x1000\n" \
"s_add_u32 s85, s39, 0x5000\n" \
C_"v252, v84, v85\n" \
M_"v[124:127], a[248:251], a[208:211], v[124:127]\n" \
B_"v20, s[80:83], s85 offen sc1 lds\n" \
C_"v253, v86, v87\n" \
M_"v[128:131], a[188:191], a[144:147], 0\n" \
X_"a[224:225], v32 offset:36864\n" \
C_"v254, v100, v101\n" \
M_"v[128:131], a[252:255], a[208:211], v[128:131]\n" \
R_"a[144:147], v28 offset:40960\n" \
C_"v255, v102, v103\n" \
M_"v[144:147], a[188:191], a[148:151], 0\n" \
R_"a[208:211], v29 offset:40960\n" \
C_"v200, v64, v65\n" \
M_"v[144:147], a[252:255], a[212:215], v[144:147]\n" \
"s_add_u32 m0, s40, 0x3000\n" \
"s_movk_i32 s84, 0x100\n" \
"s_nop 0\n" \
C_"v201, v66, v67\n" \
M_"v[140:143], a[184:187], a[148:151], 0\n" \
B_"v21, s[76:79], s84 offen offset:0 lds\n" \
C_"v202, v80, v81\n" \
M_"v[140:143], a[248:251], a[212:215], v[140:143]\n" \
X_"a[226:227], v32 offset:37376\n" \
C_"v203, v82, v83\n" \
M_"v[136:139], a[180:183], a[148:151], 0\n" \
C_"v204, v60, v61\n" \
C_"v205, v62, v63\n" \
C_"v206, v76, v77\n" \
M_"v[136:139], a[244:247], a[212:215], v[136:139]\n" \
X_"a[164:165], v33 offset:32768\n" \
C_"v207, v78, v79\n" \
M_"v[132:135], a[176:179], a[148:151], 0\n" \
C_"v208, v56, v57\n" \
C_"v209, v58, v59\n" \
C_"v210, v72, v73\n" \
M_"v[132:135], a[240:243], a[212:215], v[132:135]\n" \
R_"a[148:151], v28 offset:43008\n" \
C_"v211, v74, v75\n" \
M_"v[148:151], a[176:179], a[152:155], 0\n" \
R_"a[212:215], v29 offset:43008\n" \
C_"v212, v52, v53\n" \
M_"v[148:151], a[240:243], a[216:219], v[148:151]\n" \
"s_add_u32 m0, s31, 0x0\n" \
"s_mov_b32 s84, 0x100000\n" \
C_"v213, v54, v55\n" \
M_"v[152:155], a[180:183], a[152:155], 0\n" \
B_"v26, s[68:71], s84 offen offset:0 lds\n" \
C_"v214, v68, v69\n" \
M_"v[152:155], a[244:247], a[216:219], v[152:155]\n" \
"s_addk_i32 s84, 0x7c00\n" \
C_"v215, v70, v71\n" \
M_"v[156:159], a[184:187], a[152:155], 0\n" \
B_"v26, s[68:71], s84 offen offset:1024 lds\n" \
M_"v[156:159], a[248:251], a[216:219], v[156:159]\n" \
X_"a[166:167], v33 offset:33280\n" \
M_"v[160:163], a[188:191], a[152:155], 0\n" \
X_"a[228:229], v33 offset:36864\n" \
M_"v[160:163], a[252:255], a[216:219], v[160:163]\n" \
R_"a[152:155], v28 offset:45056\n" \
M_"v[176:179], a[188:191], a[156:159], 0\n" \
R_"a[216:219], v29 offset:45056\n" \
M_"v[176:179], a[252:255], a[220:223], v[176:179]\n" \
"s_addk_i32 s84, 0x7c00\n" \
M_"v[172:175], a[184:187], a[156:159], 0\n" \
B_"v27, s[68:71], s84 offen offset:2048 lds\n" \
M_"v[172:175], a[248:251], a[220:223], v[172:175]\n" \
X_"a[230:231], v33 offset:37376\n" \
M_"v[168:171], a[180:183], a[156:159], 0\n" \
"s_addk_i32 s84, 0x7c00\n" \
M_"v[168:171], a[244:247], a[220:223], v[168:171]\n" \
B_"v27, s[68:71], s84 offen offset:3072 lds\n" \
M_"v[164:167], a[176:179], a[156:159], 0\n" \
X_"a[172:173], v35 offset:32768\n" \
M_"v[164:167], a[240:243], a[220:223], v[164:167]\n" \
R_"a[156:159], v28 offset:47104\n" \
M_"v[84:87], a[176:179], a[136:139], 0\n" \
R_"a[220:223], v29 offset:47104\n" \
M_"v[84:87], a[240:243], a[200:203], v[84:87]\n" \
"s_add_u32 s89, s41, 0x3000\n" \
"buffer_store_dwordx4 v[192:195], v20, s[80:83], s89 offen offset:3072 sc1\n" \
M_"v[88:91], a[180:183], a[136:139], 0\n" \
X_"a[174:175], v35 offset:33280\n" \
M_"v[88:91], a[244:247], a[200:203], v[88:91]\n" \
X_"a[236:237], v35 offset:36864\n" \
M_"v[92:95], a[184:187], a[136:139], 0\n" \
X_"a[238:239], v35 offset:37376\n" \
M_"v[92:95], a[248:251], a[200:203], v[92:95]\n" \
"s_add_u32 m0, s30, 0x0\n" \
"s_movk_i32 s84, 0x180\n" \
M_"v[96:99], a[188:191], a[136:139], 0\n" \
B_"v24, s[76:79], s84 offen offset:0 lds\n" \
M_"v[96:99], a[252:255], a[200:203], v[96:99]\n" \
R_"a[136:139], v28 offset:36864\n" \
M_"v[112:115], a[188:191], a[140:143], 0\n" \
R_"a[200:203], v29 offset:36864\n" \
M_"v[112:115], a[252:255], a[204:207], v[112:115]\n" \
X_"a[168:169], v34 offset:32768\n" \
M_"v[108:111], a[184:187], a[140:143], 0\n" \
"s_movk_i32 s84, 0x2180\n" \
"s_nop 0\n" \
M_"v[108:111], a[248:251], a[204:207], v[108:111]\n" \
B_"v25, s[76:79], s84 offen offset:1024 lds\n" \
M_"v[104:107], a[180:183], a[140:143], 0\n" \
X_"a[170:171], v34 offset:33280\n" \
M_"v[104:107], a[244:247], a[204:207], v[104:107]\n" \
X_"a[232:233], v34 offset:36864\n" \
M_"v[100:103], a[176:179], a[140:143], 0\n" \
M_"v[100:103], a[240:243], a[204:207], v[100:103]\n" \
R_"a[140:143], v28 offset:38912\n" \
M_"v[52:55], a[176:179], a[128:131], 0\n" \
R_"a[204:207], v29 offset:38912\n" \
M_"v[52:55], a[240:243], a[192:195], v[52:55]\n" \
"s_movk_i32 s84, 0x4180\n" \
M_"v[56:59], a[180:183], a[128:131], 0\n" \
B_"v24, s[76:79], s84 offen offset:2048 lds\n" \
M_"v[56:59], a[244:247], a[192:195], v[56:59]\n" \
"s_movk_i32 s84, 0x6180\n" \
M_"v[60:63], a[184:187], a[128:131], 0\n" \
B_"v25, s[76:79], s84 offen offset:3072 lds\n" \
M_"v[60:63], a[248:251], a[192:195], v[60:63]\n" \
X_"a[234:235], v34 offset:37376\n" \
M_"v[64:67], a[188:191], a[128:131], 0\n" \
M_"v[64:67], a[252:255], a[192:195], v[64:67]\n" \
R_"a[128:131], v28 offset:32768\n" \
M_"v[80:83], a[188:191], a[132:135], 0\n" \
R_"a[192:195], v29 offset:32768\n" \
M_"v[80:83], a[252:255], a[196:199], v[80:83]\n" \
M_"v[76:79], a[184:187], a[132:135], 0\n" \
M_"v[76:79], a[248:251], a[196:199], v[76:79]\n" \
M_"v[72:75], a[180:183], a[132:135], 0\n" \
M_"v[72:75], a[244:247], a[196:199], v[72:75]\n" \
M_"v[68:71], a[176:179], a[132:135], 0\n" \
M_"v[68:71], a[240:243], a[196:199], v[68:71]\n" \
R_"a[132:135], v28 offset:34816\n" \
R_"a[196:199], v29 offset:34816\n" \
"s_waitcnt lgkmcnt(15)\n" \
M_"a[0:3], a[160:163], a[144:147], a[0:3]\n" \
"s_add_u32 m0, s30, 0x1000\n" \
"s_addk_i32 s84, 0x3000\n" \
M_"a[0:3], a[224:227], a[208:211], a[0:3]\n" \
B_"v24, s[76:79], s84 offen offset:0 lds\n" \
M_"a[4:7], a[164:167], a[144:147], a[4:7]\n" \
X_"a[176:177], v36 offset:32768\n" \
"v_mov_b32_e64 v11, 0\n" \
M_"a[4:7], a[228:231], a[208:211], a[4:7]\n" \
X_"a[178:179], v36 offset:33792\n" \
"s_and_b32 s5, s2, 7\n" \
"s_waitcnt lgkmcnt(15)\n" \
M_"a[12:15], a[172:175], a[144:147], a[12:15]\n" \
X_"a[240:241], v36 offset:40960\n" \
"s_lshl_b32 s6, s3, 1\n" \
"s_waitcnt lgkmcnt(15)\n" \
M_"a[12:15], a[236:239], a[208:211], a[12:15]\n" \
"s_add_u32 s88, s41, 0x3000\n" \
"buffer_store_dwordx4 v[196:199], v20, s[80:83], s88 offen offset:2048 sc1\n" \
"s_waitcnt lgkmcnt(11)\n" \
M_"a[8:11], a[144:147], a[168:171], a[8:11]\n" \
X_"a[242:243], v36 offset:41984\n" \
"s_lshl_b32 s4, s5, 2\n" \
"s_waitcnt lgkmcnt(8)\n" \
M_"a[8:11], a[208:211], a[232:235], a[8:11]\n" \
X_"a[180:181], v37 offset:32768\n" \
"s_bfe_u32 s7, s2, 0x20003\n" \
M_"a[24:27], a[148:151], a[168:171], a[24:27]\n" \
X_"a[182:183], v37 offset:33792\n" \
"s_or_b32 s12, s4, s7\n" \
M_"a[24:27], a[212:215], a[232:235], a[24:27]\n" \
X_"a[244:245], v37 offset:40960\n" \
"s_and_b32 s6, s6, 4\n" \
M_"a[28:31], a[172:175], a[148:151], a[28:31]\n" \
"s_and_b32 s8, s3, 1\n" \
"s_or_b32 s8, s6, s8\n" \
M_"a[28:31], a[236:239], a[212:215], a[28:31]\n" \
"s_addk_i32 s84, 0x2000\n" \
M_"a[20:23], a[164:167], a[148:151], a[20:23]\n" \
B_"v25, s[76:79], s84 offen offset:1024 lds\n" \
M_"a[20:23], a[228:231], a[212:215], a[20:23]\n" \
X_"a[246:247], v37 offset:41984\n" \
"s_lshl_b32 s6, s12, 4\n" \
M_"a[16:19], a[160:163], a[148:151], a[16:19]\n" \
X_"a[184:185], v38 offset:32768\n" \
"s_lshl_b32 s9, s3, 2\n" \
M_"a[16:19], a[224:227], a[212:215], a[16:19]\n" \
X_"a[186:187], v38 offset:33792\n" \
"s_or_b32 s6, s6, s9\n" \
M_"a[32:35], a[160:163], a[152:155], a[32:35]\n" \
"s_addk_i32 s84, 0x2000\n" \
M_"a[32:35], a[224:227], a[216:219], a[32:35]\n" \
B_"v24, s[76:79], s84 offen offset:2048 lds\n" \
M_"a[36:39], a[164:167], a[152:155], a[36:39]\n" \
X_"a[248:249], v38 offset:40960\n" \
"s_lshr_b32 s9, s2, 5\n" \
M_"a[36:39], a[228:231], a[216:219], a[36:39]\n" \
X_"a[250:251], v38 offset:41984\n" \
"s_mul_i32 s10, s9, 0xa00\n" \
M_"a[44:47], a[172:175], a[152:155], a[44:47]\n" \
X_"a[188:189], v39 offset:32768\n" \
"s_add_i32 s10, s10, 0x5bc1000\n" \
M_"a[44:47], a[236:239], a[216:219], a[44:47]\n" \
X_"a[190:191], v39 offset:33792\n" \
"s_or_b32 s6, s6, s10\n" \
M_"a[40:43], a[152:155], a[168:171], a[40:43]\n" \
"s_add_u32 s89, s41, 0x3000\n" \
"buffer_store_dwordx4 a[84:87], v20, s[80:83], s89 offen offset:1024 sc1\n" \
M_"a[40:43], a[216:219], a[232:235], a[40:43]\n" \
X_"a[252:253], v39 offset:40960\n" \
"s_lshl_b32 s13, s9, 3\n" \
M_"a[56:59], a[156:159], a[168:171], a[56:59]\n" \
X_"a[254:255], v39 offset:41984\n" \
"v_writelane_b32 v11, s6, 0\n" \
M_"a[56:59], a[220:223], a[232:235], a[56:59]\n" \
"s_or_b32 s6, s13, s5\n" \
"v_writelane_b32 v11, s10, 1\n" \
M_"a[60:63], a[172:175], a[156:159], a[60:63]\n" \
"s_lshl_b32 s6, s6, 10\n" \
"s_lshl_b32 s10, s3, 8\n" \
M_"a[60:63], a[236:239], a[220:223], a[60:63]\n" \
"s_addk_i32 s84, 0x2000\n" \
M_"a[52:55], a[164:167], a[156:159], a[52:55]\n" \
B_"v25, s[76:79], s84 offen offset:3072 lds\n" \
M_"a[52:55], a[228:231], a[220:223], a[52:55]\n" \
"s_or_b32 s6, s6, s10\n" \
"s_or_b32 s10, s6, 0x5c00200\n" \
M_"a[48:51], a[160:163], a[156:159], a[48:51]\n" \
"v_writelane_b32 v11, s10, 2\n" \
"s_lshl_b32 s10, s7, 2\n" \
M_"a[48:51], a[224:227], a[220:223], a[48:51]\n" \
"s_or_b32 s11, s10, s6\n" \
"s_or_b32 s11, s11, 0x5c00000\n" \
M_"a[64:67], a[164:167], a[140:143], a[64:67]\n" \
"v_writelane_b32 v11, s11, 3\n" \
"s_or_b32 s6, s6, 0x5c00080\n" \
M_"a[64:67], a[228:231], a[204:207], a[64:67]\n" \
"v_writelane_b32 v11, s6, 4\n" \
"s_or_b32 s6, s6, s10\n" \
M_"a[80:83], a[172:175], a[140:143], a[80:83]\n" \
"s_add_i32 s10, s3, s2\n" \
"v_writelane_b32 v11, s6, 5\n" \
M_"a[80:83], a[236:239], a[204:207], a[80:83]\n" \
"s_mov_b32 s6, 2.0\n" \
"s_mov_b32 s11, 0x5bc0000\n" \
M_"a[68:71], a[140:143], a[168:171], a[68:71]\n" \
"s_cmp_lg_u32 s10, 0\n" \
"s_cselect_b32 s10, s6, s11\n" \
"s_waitcnt vmcnt(11) lgkmcnt(0)\n" \
M_"a[68:71], a[204:207], a[232:235], a[68:71]\n" \
"s_barrier\n" \
"v_xor_b32_e32 v28, 0x20000, v28\n" \
"v_xor_b32_e32 v29, 0x20000, v29\n" \
M_"v[116:119], a[176:179], a[144:147], v[116:119]\n" \
"s_add_u32 m0, s40, 0x8000\n" \
"s_add_u32 s85, s39, 0x6000\n" \
M_"v[116:119], a[240:243], a[208:211], v[116:119]\n" \
B_"v20, s[80:83], s85 offen sc1 lds\n" \
M_"v[120:123], a[180:183], a[144:147], v[120:123]\n" \
X_"a[160:161], v32 offset:0\n" \
"s_lshr_b32 s14, s2, 3\n" \
M_"v[120:123], a[244:247], a[208:211], v[120:123]\n" \
X_"a[162:163], v32 offset:512\n" \
"v_writelane_b32 v11, s10, 57\n" \
M_"v[124:127], a[184:187], a[144:147], v[124:127]\n" \
"s_add_u32 m0, s40, 0x9000\n" \
"s_add_u32 s85, s39, 0x7000\n" \
M_"v[124:127], a[248:251], a[208:211], v[124:127]\n" \
B_"v20, s[80:83], s85 offen sc1 lds\n" \
M_"v[128:131], a[188:191], a[144:147], v[128:131]\n" \
X_"a[224:225], v32 offset:4096\n" \
"s_lshl_b32 s10, s5, 5\n" \
M_"v[128:131], a[252:255], a[208:211], v[128:131]\n" \
R_"a[144:147], v28 offset:8192\n" \
"s_min_u32 s15, s3, 2\n" \
M_"v[144:147], a[188:191], a[148:151], v[144:147]\n" \
R_"a[208:211], v29 offset:8192\n" \
"s_mul_i32 s11, s7, 0x120\n" \
M_"v[144:147], a[252:255], a[212:215], v[144:147]\n" \
"s_add_u32 m0, s40, 0xb000\n" \
"s_movk_i32 s84, 0x180\n" \
M_"v[140:143], a[184:187], a[148:151], v[140:143]\n" \
B_"v21, s[76:79], s84 offen offset:0 lds\n" \
M_"v[140:143], a[248:251], a[212:215], v[140:143]\n" \
X_"a[226:227], v32 offset:4608\n" \
M_"v[136:139], a[180:183], a[148:151], v[136:139]\n" \
"s_add_u32 s88, s41, 0x3000\n" \
"buffer_store_dwordx4 v[252:255], v20, s[80:83], s88 offen offset:0 sc1\n" \
M_"v[136:139], a[244:247], a[212:215], v[136:139]\n" \
X_"a[164:165], v33 offset:0\n" \
M_"v[132:135], a[176:179], a[148:151], v[132:135]\n" \
"s_add_u32 s16, s22, s11\n" \
"s_addc_u32 s17, s23, 0\n" \
"s_nop 0\n" \
M_"v[132:135], a[240:243], a[212:215], v[132:135]\n" \
R_"a[148:151], v28 offset:10240\n" \
M_"v[148:151], a[176:179], a[152:155], v[148:151]\n" \
R_"a[212:215], v29 offset:10240\n" \
M_"v[148:151], a[240:243], a[216:219], v[148:151]\n" \
"s_add_u32 m0, s31, 0x8000\n" \
"s_mov_b32 s84, 0x180000\n" \
M_"v[152:155], a[180:183], a[152:155], v[152:155]\n" \
B_"v26, s[68:71], s84 offen offset:0 lds\n" \
M_"v[152:155], a[244:247], a[216:219], v[152:155]\n" \
X_"a[166:167], v33 offset:512\n" \
M_"v[156:159], a[184:187], a[152:155], v[156:159]\n" \
X_"a[228:229], v33 offset:4096\n" \
M_"v[156:159], a[248:251], a[216:219], v[156:159]\n" \
"s_addk_i32 s84, 0x7c00\n" \
M_"v[160:163], a[188:191], a[152:155], v[160:163]\n" \
B_"v26, s[68:71], s84 offen offset:1024 lds\n" \
M_"v[160:163], a[252:255], a[216:219], v[160:163]\n" \
R_"a[152:155], v28 offset:12288\n" \
M_"v[176:179], a[188:191], a[156:159], v[176:179]\n" \
R_"a[216:219], v29 offset:12288\n" \
M_"v[176:179], a[252:255], a[220:223], v[176:179]\n" \
"s_addk_i32 s84, 0x7c00\n" \
M_"v[172:175], a[184:187], a[156:159], v[172:175]\n" \
B_"v27, s[68:71], s84 offen offset:2048 lds\n" \
M_"v[172:175], a[248:251], a[220:223], v[172:175]\n" \
X_"a[230:231], v33 offset:4608\n" \
M_"v[168:171], a[180:183], a[156:159], v[168:171]\n" \
X_"a[172:173], v35 offset:0\n" \
M_"v[168:171], a[244:247], a[220:223], v[168:171]\n" \
X_"a[174:175], v35 offset:512\n" \
M_"v[164:167], a[176:179], a[156:159], v[164:167]\n" \
"s_add_u32 s18, s24, s11\n" \
"v_writelane_b32 v11, s16, 9\n" \
"s_addc_u32 s19, s25, 0\n" \
M_"v[164:167], a[240:243], a[220:223], v[164:167]\n" \
R_"a[156:159], v28 offset:14336\n" \
"s_and_b32 s20, s17, 0xffff\n" \
M_"v[84:87], a[176:179], a[136:139], v[84:87]\n" \
R_"a[220:223], v29 offset:14336\n" \
"v_writelane_b32 v11, s20, 10\n" \
M_"v[84:87], a[240:243], a[200:203], v[84:87]\n" \
"s_addk_i32 s84, 0x7c00\n" \
M_"v[88:91], a[180:183], a[136:139], v[88:91]\n" \
B_"v27, s[68:71], s84 offen offset:3072 lds\n" \
M_"v[88:91], a[244:247], a[200:203], v[88:91]\n" \
"s_add_u32 m0, s30, 0x8000\n" \
"s_movk_i32 s84, 0x200\n" \
M_"v[92:95], a[184:187], a[136:139], v[92:95]\n" \
B_"v24, s[76:79], s84 offen offset:0 lds\n" \
M_"v[92:95], a[248:251], a[200:203], v[92:95]\n" \
X_"a[236:237], v35 offset:4096\n" \
"s_and_b32 s20, s19, 0xffff\n" \
M_"v[96:99], a[188:191], a[136:139], v[96:99]\n" \
X_"a[238:239], v35 offset:4608\n" \
"v_writelane_b32 v11, s18, 11\n" \
M_"v[96:99], a[252:255], a[200:203], v[96:99]\n" \
R_"a[136:139], v28 offset:4096\n" \
M_"v[112:115], a[188:191], a[140:143], v[112:115]\n" \
R_"a[200:203], v29 offset:4096\n" \
M_"v[112:115], a[252:255], a[204:207], v[112:115]\n" \
"s_movk_i32 s84, 0x2200\n" \
"s_nop 0\n" \
M_"v[108:111], a[184:187], a[140:143], v[108:111]\n" \
B_"v25, s[76:79], s84 offen offset:1024 lds\n" \
M_"v[108:111], a[248:251], a[204:207], v[108:111]\n" \
"s_add_u32 s89, s41, 0x1000\n" \
"buffer_store_dwordx4 v[200:203], v20, s[80:83], s89 offen offset:3072 sc1\n" \
M_"v[104:107], a[180:183], a[140:143], v[104:107]\n" \
X_"a[168:169], v34 offset:0\n" \
M_"v[104:107], a[244:247], a[204:207], v[104:107]\n" \
X_"a[170:171], v34 offset:512\n" \
M_"v[100:103], a[176:179], a[140:143], v[100:103]\n" \
X_"a[232:233], v34 offset:4096\n" \
M_"v[100:103], a[240:243], a[204:207], v[100:103]\n" \
R_"a[140:143], v28 offset:6144\n" \
M_"v[52:55], a[176:179], a[128:131], v[52:55]\n" \
R_"a[204:207], v29 offset:6144\n" \
M_"v[52:55], a[240:243], a[192:195], v[52:55]\n" \
"s_movk_i32 s84, 0x4200\n" \
M_"v[56:59], a[180:183], a[128:131], v[56:59]\n" \
B_"v24, s[76:79], s84 offen offset:2048 lds\n" \
M_"v[56:59], a[244:247], a[192:195], v[56:59]\n" \
"s_movk_i32 s84, 0x6200\n" \
M_"v[60:63], a[184:187], a[128:131], v[60:63]\n" \
B_"v25, s[76:79], s84 offen offset:3072 lds\n" \
M_"v[60:63], a[248:251], a[192:195], v[60:63]\n" \
X_"a[234:235], v34 offset:4608\n" \
M_"v[64:67], a[188:191], a[128:131], v[64:67]\n" \
"s_cmpk_lt_u32 s3, 3\n" \
"s_cselect_b32 s16, s16, s18\n" \
M_"v[64:67], a[252:255], a[192:195], v[64:67]\n" \
R_"a[128:131], v28 offset:0\n" \
"s_movk_i32 s18, 0xfff\n" \
M_"v[80:83], a[188:191], a[132:135], v[80:83]\n" \
R_"a[192:195], v29 offset:0\n" \
"v_writelane_b32 v11, s20, 12\n" \
M_"v[80:83], a[252:255], a[196:199], v[80:83]\n" \
"s_and_b32 s20, s29, 0xffff\n" \
"v_writelane_b32 v11, s28, 13\n" \
M_"v[76:79], a[184:187], a[132:135], v[76:79]\n" \
"v_writelane_b32 v11, s20, 14\n" \
"v_writelane_b32 v11, s16, 15\n" \
M_"v[76:79], a[248:251], a[196:199], v[76:79]\n" \
"s_cmpk_lt_u32 s3, 3\n" \
"s_cselect_b32 s16, s17, s19\n" \
M_"v[72:75], a[180:183], a[132:135], v[72:75]\n" \
"s_and_b32 s16, s16, 0xffff\n" \
"v_writelane_b32 v11, s16, 16\n" \
M_"v[72:75], a[244:247], a[196:199], v[72:75]\n" \
"s_mul_i32 s16, s5, 0x48\n" \
"s_add_i32 s17, s16, 0x47\n" \
M_"v[68:71], a[176:179], a[132:135], v[68:71]\n" \
"s_cmpk_lt_u32 s3, 3\n" \
"s_cselect_b32 s17, s17, s18\n" \
M_"v[68:71], a[240:243], a[196:199], v[68:71]\n" \
R_"a[132:135], v28 offset:2048\n" \
R_"a[196:199], v29 offset:2048\n" \
"s_waitcnt lgkmcnt(15)\n" \
M_"a[0:3], a[160:163], a[144:147], a[0:3]\n" \
"s_add_u32 m0, s30, 0x9000\n" \
"s_addk_i32 s84, 0x3000\n" \
M_"a[0:3], a[224:227], a[208:211], a[0:3]\n" \
B_"v24, s[76:79], s84 offen offset:0 lds\n" \
M_"a[4:7], a[164:167], a[144:147], a[4:7]\n" \
X_"a[176:177], v36 offset:0\n" \
"s_mulk_i32 s17, 0x480\n" \
M_"a[4:7], a[228:231], a[208:211], a[4:7]\n" \
X_"a[178:179], v36 offset:1024\n" \
"s_addk_i32 s17, 0x120\n" \
M_"a[12:15], a[172:175], a[144:147], a[12:15]\n" \
X_"a[240:241], v36 offset:8192\n" \
"v_writelane_b32 v11, s17, 17\n" \
"s_waitcnt lgkmcnt(15)\n" \
M_"a[12:15], a[236:239], a[208:211], a[12:15]\n" \
"s_add_u32 s88, s41, 0x1000\n" \
"buffer_store_dwordx4 v[204:207], v20, s[80:83], s88 offen offset:2048 sc1\n" \
"s_waitcnt lgkmcnt(11)\n" \
M_"a[8:11], a[144:147], a[168:171], a[8:11]\n" \
X_"a[242:243], v36 offset:9216\n" \
"s_lshl_b32 s17, s3, 5\n" \
"s_waitcnt lgkmcnt(8)\n" \
M_"a[8:11], a[208:211], a[232:235], a[8:11]\n" \
X_"a[180:181], v37 offset:0\n" \
"s_add_i32 s16, s16, s17\n" \
M_"a[24:27], a[148:151], a[168:171], a[24:27]\n" \
X_"a[182:183], v37 offset:1024\n" \
"s_mulk_i32 s16, 0x480\n" \
M_"a[24:27], a[212:215], a[232:235], a[24:27]\n" \
X_"a[244:245], v37 offset:8192\n" \
"s_mul_i32 s17, s12, 0x24000\n" \
M_"a[28:31], a[172:175], a[148:151], a[28:31]\n" \
"s_add_i32 s18, s17, 0x1b000\n" \
M_"a[28:31], a[236:239], a[212:215], a[28:31]\n" \
"s_addk_i32 s84, 0x2000\n" \
M_"a[20:23], a[164:167], a[148:151], a[20:23]\n" \
B_"v25, s[76:79], s84 offen offset:1024 lds\n" \
M_"a[20:23], a[228:231], a[212:215], a[20:23]\n" \
X_"a[246:247], v37 offset:9216\n" \
M_"a[16:19], a[160:163], a[148:151], a[16:19]\n" \
X_"a[184:185], v38 offset:0\n" \
".fill (L_pb + 65248 - .) / 4, 4, 0xBF800000\n" \
"L_pp16:\n" \
"s_cbranch_execz L_pret\n" \
M_"a[16:19], a[224:227], a[212:215], a[16:19]\n" \
X_"a[186:187], v38 offset:1024\n" \
M_"a[32:35], a[160:163], a[152:155], a[32:35]\n" \
"s_addk_i32 s84, 0x2000\n" \
M_"a[32:35], a[224:227], a[216:219], a[32:35]\n" \
B_"v24, s[76:79], s84 offen offset:2048 lds\n" \
M_"a[36:39], a[164:167], a[152:155], a[36:39]\n" \
X_"a[248:249], v38 offset:8192\n" \
M_"a[36:39], a[228:231], a[216:219], a[36:39]\n" \
X_"a[250:251], v38 offset:9216\n" \
M_"a[44:47], a[172:175], a[152:155], a[44:47]\n" \
X_"a[188:189], v39 offset:0\n" \
M_"a[44:47], a[236:239], a[216:219], a[44:47]\n" \
X_"a[190:191], v39 offset:1024\n" \
M_"a[40:43], a[152:155], a[168:171], a[40:43]\n" \
"s_add_u32 s89, s41, 0x1000\n" \
"buffer_store_dwordx4 v[208:211], v20, s[80:83], s89 offen offset:1024 sc1\n" \
M_"a[40:43], a[216:219], a[232:235], a[40:43]\n" \
X_"a[252:253], v39 offset:8192\n" \
M_"a[56:59], a[156:159], a[168:171], a[56:59]\n" \
X_"a[254:255], v39 offset:9216\n" \
M_"a[56:59], a[220:223], a[232:235], a[56:59]\n" \
"s_cmpk_lt_u32 s3, 3\n" \
"s_cselect_b32 s16, s16, s18\n" \
M_"a[60:63], a[172:175], a[156:159], a[60:63]\n" \
"s_xor_b32 s18, s3, 2\n" \
"v_writelane_b32 v11, s16, 18\n" \
M_"a[60:63], a[236:239], a[220:223], a[60:63]\n" \
"s_addk_i32 s84, 0x2000\n" \
M_"a[52:55], a[164:167], a[156:159], a[52:55]\n" \
B_"v25, s[76:79], s84 offen offset:3072 lds\n" \
M_"a[52:55], a[228:231], a[220:223], a[52:55]\n" \
"s_mul_i32 s16, s15, 0x2400\n" \
"s_mul_i32 s15, s15, 0x9000\n" \
M_"a[48:51], a[160:163], a[156:159], a[48:51]\n" \
"s_bitset1_b32 s16, 17\n" \
"v_writelane_b32 v11, s16, 22\n" \
M_"a[48:51], a[224:227], a[220:223], a[48:51]\n" \
"s_add_i32 s17, s17, s15\n" \
"s_mul_i32 s15, s3, 0x2400\n" \
M_"a[64:67], a[164:167], a[140:143], a[64:67]\n" \
"v_writelane_b32 v11, s17, 29\n" \
"s_add_i32 s16, s15, 0xa400\n" \
M_"a[64:67], a[228:231], a[204:207], a[64:67]\n" \
"s_mov_b32 s17, 0x8000\n" \
M_"a[80:83], a[172:175], a[140:143], a[80:83]\n" \
"s_cmpk_lt_u32 s3, 3\n" \
"s_cselect_b32 s16, s16, s17\n" \
M_"a[80:83], a[236:239], a[204:207], a[80:83]\n" \
"s_mov_b32 s17, 0x1b000\n" \
M_"a[68:71], a[140:143], a[168:171], a[68:71]\n" \
"s_cmp_lg_u32 s18, 0\n" \
"s_cselect_b32 s17, s16, s17\n" \
"s_waitcnt vmcnt(11) lgkmcnt(0)\n" \
M_"a[68:71], a[204:207], a[232:235], a[68:71]\n" \
"s_barrier\n" \
"v_xor_b32_e32 v28, 0x20000, v28\n" \
"v_xor_b32_e32 v29, 0x20000, v29\n" \
M_"v[116:119], a[176:179], a[144:147], v[116:119]\n" \
"s_add_u32 m0, s40, 0x0\n" \
"s_nop 0\n" \
"s_add_u32 s85, s39, 0x8000\n" \
M_"v[116:119], a[240:243], a[208:211], v[116:119]\n" \
B_"v20, s[80:83], s85 offen sc1 lds\n" \
M_"v[120:123], a[180:183], a[144:147], v[120:123]\n" \
X_"a[160:161], v32 offset:32768\n" \
"v_writelane_b32 v11, s16, 23\n" \
M_"v[120:123], a[244:247], a[208:211], v[120:123]\n" \
X_"a[162:163], v32 offset:33280\n" \
"s_or_b32 s16, s15, 0x10000\n" \
M_"v[124:127], a[184:187], a[144:147], v[124:127]\n" \
"s_add_u32 m0, s40, 0x1000\n" \
"s_add_u32 s85, s39, 0x9000\n" \
M_"v[124:127], a[248:251], a[208:211], v[124:127]\n" \
B_"v20, s[80:83], s85 offen sc1 lds\n" \
M_"v[128:131], a[188:191], a[144:147], v[128:131]\n" \
X_"a[224:225], v32 offset:36864\n" \
"v_writelane_b32 v11, s17, 24\n" \
M_"v[128:131], a[252:255], a[208:211], v[128:131]\n" \
R_"a[144:147], v28 offset:8192\n" \
"s_add_i32 s17, s15, 0x19000\n" \
M_"v[144:147], a[188:191], a[148:151], v[144:147]\n" \
R_"a[208:211], v29 offset:8192\n" \
"v_writelane_b32 v11, s16, 25\n" \
M_"v[144:147], a[252:255], a[212:215], v[144:147]\n" \
"s_add_u32 m0, s40, 0x3000\n" \
"s_movk_i32 s84, 0x200\n" \
M_"v[140:143], a[184:187], a[148:151], v[140:143]\n" \
B_"v21, s[76:79], s84 offen offset:0 lds\n" \
M_"v[140:143], a[248:251], a[212:215], v[140:143]\n" \
X_"a[226:227], v32 offset:37376\n" \
"s_mov_b32 s16, 0\n" \
M_"v[136:139], a[180:183], a[148:151], v[136:139]\n" \
"s_add_u32 s88, s41, 0x1000\n" \
"buffer_store_dwordx4 v[212:215], v20, s[80:83], s88 offen offset:0 sc1\n" \
M_"v[136:139], a[244:247], a[212:215], v[136:139]\n" \
X_"a[164:165], v33 offset:32768\n" \
M_"v[132:135], a[176:179], a[148:151], v[132:135]\n" \
"s_cmpk_lt_u32 s3, 3\n" \
"s_cselect_b32 s17, s17, s16\n" \
M_"v[132:135], a[240:243], a[212:215], v[132:135]\n" \
R_"a[148:151], v28 offset:10240\n" \
"s_bitset1_b32 s15, 17\n" \
M_"v[148:151], a[176:179], a[152:155], v[148:151]\n" \
R_"a[212:215], v29 offset:10240\n" \
"v_writelane_b32 v11, s17, 26\n" \
M_"v[148:151], a[240:243], a[216:219], v[148:151]\n" \
"s_add_u32 m0, s31, 0x0\n" \
"s_mov_b32 s84, 0x200000\n" \
M_"v[152:155], a[180:183], a[152:155], v[152:155]\n" \
B_"v26, s[68:71], s84 offen offset:0 lds\n" \
M_"v[152:155], a[244:247], a[216:219], v[152:155]\n" \
X_"a[166:167], v33 offset:33280\n" \
"s_movk_i32 s17, 0x2400\n" \
M_"v[156:159], a[184:187], a[152:155], v[156:159]\n" \
X_"a[228:229], v33 offset:36864\n" \
M_"v[156:159], a[248:251], a[216:219], v[156:159]\n" \
"s_addk_i32 s84, 0x7c00\n" \
M_"v[160:163], a[188:191], a[152:155], v[160:163]\n" \
B_"v26, s[68:71], s84 offen offset:1024 lds\n" \
M_"v[160:163], a[252:255], a[216:219], v[160:163]\n" \
R_"a[152:155], v28 offset:12288\n" \
M_"v[176:179], a[188:191], a[156:159], v[176:179]\n" \
R_"a[216:219], v29 offset:12288\n" \
M_"v[176:179], a[252:255], a[220:223], v[176:179]\n" \
"s_addk_i32 s84, 0x7c00\n" \
M_"v[172:175], a[184:187], a[156:159], v[172:175]\n" \
B_"v27, s[68:71], s84 offen offset:2048 lds\n" \
M_"v[172:175], a[248:251], a[220:223], v[172:175]\n" \
X_"a[230:231], v33 offset:37376\n" \
M_"v[168:171], a[180:183], a[156:159], v[168:171]\n" \
X_"a[172:173], v35 offset:32768\n" \
M_"v[168:171], a[244:247], a[220:223], v[168:171]\n" \
X_"a[174:175], v35 offset:33280\n" \
M_"v[164:167], a[176:179], a[156:159], v[164:167]\n" \
"s_cmpk_lt_u32 s3, 3\n" \
"s_cselect_b32 s15, s15, s17\n" \
M_"v[164:167], a[240:243], a[220:223], v[164:167]\n" \
R_"a[156:159], v28 offset:14336\n" \
"v_writelane_b32 v11, s15, 27\n" \
M_"v[84:87], a[176:179], a[136:139], v[84:87]\n" \
R_"a[220:223], v29 offset:14336\n" \
"s_lshl_b32 s15, s3, 10\n" \
M_"v[84:87], a[240:243], a[200:203], v[84:87]\n" \
"s_addk_i32 s84, 0x7c00\n" \
M_"v[88:91], a[180:183], a[136:139], v[88:91]\n" \
B_"v27, s[68:71], s84 offen offset:3072 lds\n" \
M_"v[88:91], a[244:247], a[200:203], v[88:91]\n" \
"s_add_u32 m0, s30, 0x20000\n" \
"s_movk_i32 s84, 0x280\n" \
M_"v[92:95], a[184:187], a[136:139], v[92:95]\n" \
B_"v24, s[76:79], s84 offen offset:0 lds\n" \
M_"v[92:95], a[248:251], a[200:203], v[92:95]\n" \
X_"a[236:237], v35 offset:36864\n" \
"s_addk_i32 s15, 0x4800\n" \
M_"v[96:99], a[188:191], a[136:139], v[96:99]\n" \
X_"a[238:239], v35 offset:37376\n" \
"v_writelane_b32 v11, s15, 28\n" \
M_"v[96:99], a[252:255], a[200:203], v[96:99]\n" \
R_"a[136:139], v28 offset:4096\n" \
"s_add_i32 s15, s14, 1\n" \
M_"v[112:115], a[188:191], a[140:143], v[112:115]\n" \
R_"a[200:203], v29 offset:4096\n" \
"s_and_b32 s15, s15, 3\n" \
M_"v[112:115], a[252:255], a[204:207], v[112:115]\n" \
"s_movk_i32 s84, 0x2280\n" \
M_"v[108:111], a[184:187], a[140:143], v[108:111]\n" \
B_"v25, s[76:79], s84 offen offset:1024 lds\n" \
M_"v[108:111], a[248:251], a[204:207], v[108:111]\n" \
"s_add_u32 s89, s42, 0x0\n" \
"buffer_store_dwordx4 v[216:219], v20, s[80:83], s89 offen offset:1024 sc1\n" \
M_"v[104:107], a[180:183], a[140:143], v[104:107]\n" \
X_"a[168:169], v34 offset:32768\n" \
"s_or_b32 s15, s15, s4\n" \
M_"v[104:107], a[244:247], a[204:207], v[104:107]\n" \
X_"a[170:171], v34 offset:33280\n" \
"s_lshl_b32 s17, s15, 2\n" \
M_"v[100:103], a[176:179], a[140:143], v[100:103]\n" \
X_"a[232:233], v34 offset:36864\n" \
"s_or_b32 s17, s17, s3\n" \
M_"v[100:103], a[240:243], a[204:207], v[100:103]\n" \
R_"a[140:143], v28 offset:6144\n" \
"s_mul_i32 s17, s17, 0x9000\n" \
M_"v[52:55], a[176:179], a[128:131], v[52:55]\n" \
R_"a[204:207], v29 offset:6144\n" \
"v_writelane_b32 v11, s17, 30\n" \
M_"v[52:55], a[240:243], a[192:195], v[52:55]\n" \
"s_movk_i32 s84, 0x4280\n" \
M_"v[56:59], a[180:183], a[128:131], v[56:59]\n" \
B_"v24, s[76:79], s84 offen offset:2048 lds\n" \
M_"v[56:59], a[244:247], a[192:195], v[56:59]\n" \
"s_movk_i32 s84, 0x6280\n" \
M_"v[60:63], a[184:187], a[128:131], v[60:63]\n" \
B_"v25, s[76:79], s84 offen offset:3072 lds\n" \
M_"v[60:63], a[248:251], a[192:195], v[60:63]\n" \
X_"a[234:235], v34 offset:37376\n" \
"s_lshl_b32 s17, s12, 2\n" \
M_"v[64:67], a[188:191], a[128:131], v[64:67]\n" \
"s_or_b32 s17, s17, s3\n" \
"s_add_i32 s14, s14, -1\n" \
M_"v[64:67], a[252:255], a[192:195], v[64:67]\n" \
R_"a[128:131], v28 offset:0\n" \
"s_xor_b32 s17, s17, 8\n" \
M_"v[80:83], a[188:191], a[132:135], v[80:83]\n" \
R_"a[192:195], v29 offset:0\n" \
"s_and_b32 s14, s14, 3\n" \
M_"v[80:83], a[252:255], a[196:199], v[80:83]\n" \
"s_mul_i32 s17, s17, 0x9000\n" \
"s_or_b32 s14, s14, s4\n" \
M_"v[76:79], a[184:187], a[132:135], v[76:79]\n" \
"v_writelane_b32 v11, s17, 31\n" \
"s_lshl_b32 s17, s14, 2\n" \
M_"v[76:79], a[248:251], a[196:199], v[76:79]\n" \
"s_or_b32 s17, s17, s3\n" \
"s_mul_i32 s17, s17, 0x9000\n" \
M_"v[72:75], a[180:183], a[132:135], v[72:75]\n" \
"v_writelane_b32 v11, s17, 32\n" \
"s_or_b32 s17, s8, s13\n" \
M_"v[72:75], a[244:247], a[196:199], v[72:75]\n" \
"s_mul_i32 s17, s17, 0x24800\n" \
"s_lshl_b32 s15, s15, 12\n" \
M_"v[68:71], a[176:179], a[132:135], v[68:71]\n" \
"s_add_i32 s18, s17, 0x3004800\n" \
"s_lshl_b32 s12, s12, 12\n" \
M_"v[68:71], a[240:243], a[196:199], v[68:71]\n" \
R_"a[132:135], v28 offset:2048\n" \
R_"a[196:199], v29 offset:2048\n" \
"s_waitcnt lgkmcnt(15)\n" \
M_"a[0:3], a[160:163], a[144:147], a[0:3]\n" \
"s_add_u32 m0, s30, 0x21000\n" \
"s_addk_i32 s84, 0x3000\n" \
M_"a[0:3], a[224:227], a[208:211], a[0:3]\n" \
B_"v24, s[76:79], s84 offen offset:0 lds\n" \
M_"a[4:7], a[164:167], a[144:147], a[4:7]\n" \
X_"a[176:177], v36 offset:32768\n" \
"s_add_i32 s19, s18, s15\n" \
M_"a[4:7], a[228:231], a[208:211], a[4:7]\n" \
X_"a[178:179], v36 offset:33792\n" \
"v_writelane_b32 v11, s19, 33\n" \
M_"a[12:15], a[172:175], a[144:147], a[12:15]\n" \
X_"a[240:241], v36 offset:40960\n" \
"s_xor_b32 s12, s12, 0x2000\n" \
"s_waitcnt lgkmcnt(15)\n" \
M_"a[12:15], a[236:239], a[208:211], a[12:15]\n" \
"s_add_u32 s88, s42, 0x0\n" \
"buffer_store_dwordx4 v[220:223], v20, s[80:83], s88 offen offset:0 sc1\n" \
"s_waitcnt lgkmcnt(11)\n" \
M_"a[8:11], a[144:147], a[168:171], a[8:11]\n" \
X_"a[242:243], v36 offset:41984\n" \
"s_lshl_b32 s14, s14, 12\n" \
"s_waitcnt lgkmcnt(8)\n" \
M_"a[8:11], a[208:211], a[232:235], a[8:11]\n" \
X_"a[180:181], v37 offset:32768\n" \
"s_sub_i32 s13, s8, s13\n" \
M_"a[24:27], a[148:151], a[168:171], a[24:27]\n" \
X_"a[182:183], v37 offset:33792\n" \
"s_add_i32 s19, s18, s12\n" \
M_"a[24:27], a[212:215], a[232:235], a[24:27]\n" \
X_"a[244:245], v37 offset:40960\n" \
"v_writelane_b32 v11, s19, 34\n" \
M_"a[28:31], a[172:175], a[148:151], a[28:31]\n" \
"s_add_i32 s18, s18, s14\n" \
"s_mul_i32 s13, s13, 0x24800\n" \
M_"a[28:31], a[236:239], a[212:215], a[28:31]\n" \
"s_addk_i32 s84, 0x2000\n" \
M_"a[20:23], a[164:167], a[148:151], a[20:23]\n" \
B_"v25, s[76:79], s84 offen offset:1024 lds\n" \
M_"a[20:23], a[228:231], a[212:215], a[20:23]\n" \
X_"a[246:247], v37 offset:41984\n" \
"v_writelane_b32 v11, s18, 35\n" \
M_"a[16:19], a[160:163], a[148:151], a[16:19]\n" \
X_"a[184:185], v38 offset:32768\n" \
"s_add_i32 s18, s13, 0x4120800\n" \
M_"a[16:19], a[224:227], a[212:215], a[16:19]\n" \
X_"a[186:187], v38 offset:33792\n" \
"s_add_i32 s15, s18, s15\n" \
M_"a[32:35], a[160:163], a[152:155], a[32:35]\n" \
"s_addk_i32 s84, 0x2000\n" \
M_"a[32:35], a[224:227], a[216:219], a[32:35]\n" \
B_"v24, s[76:79], s84 offen offset:2048 lds\n" \
M_"a[36:39], a[164:167], a[152:155], a[36:39]\n" \
X_"a[248:249], v38 offset:40960\n" \
"v_writelane_b32 v11, s15, 36\n" \
M_"a[36:39], a[228:231], a[216:219], a[36:39]\n" \
X_"a[250:251], v38 offset:41984\n" \
"s_add_i32 s12, s18, s12\n" \
M_"a[44:47], a[172:175], a[152:155], a[44:47]\n" \
X_"a[188:189], v39 offset:32768\n" \
"v_writelane_b32 v11, s12, 37\n" \
M_"a[44:47], a[236:239], a[216:219], a[44:47]\n" \
X_"a[190:191], v39 offset:33792\n" \
"s_add_i32 s18, s18, s14\n" \
M_"a[40:43], a[152:155], a[168:171], a[40:43]\n" \
"s_add_u32 s89, s42, 0x1000\n" \
"buffer_store_dwordx4 v[224:227], v20, s[80:83], s89 offen offset:3072 sc1\n" \
M_"a[40:43], a[216:219], a[232:235], a[40:43]\n" \
X_"a[252:253], v39 offset:40960\n" \
"s_or_b32 s12, s17, 0x3000000\n" \
M_"a[56:59], a[156:159], a[168:171], a[56:59]\n" \
X_"a[254:255], v39 offset:41984\n" \
"s_lshl_b32 s14, s5, 11\n" \
M_"a[56:59], a[220:223], a[232:235], a[56:59]\n" \
"v_writelane_b32 v11, s18, 38\n" \
"s_add_i32 s15, s12, s14\n" \
M_"a[60:63], a[172:175], a[156:159], a[60:63]\n" \
"v_writelane_b32 v11, s15, 39\n" \
"s_add_i32 s15, s13, 0x411c000\n" \
M_"a[60:63], a[236:239], a[220:223], a[60:63]\n" \
"s_addk_i32 s84, 0x2000\n" \
M_"a[52:55], a[164:167], a[156:159], a[52:55]\n" \
B_"v25, s[76:79], s84 offen offset:3072 lds\n" \
M_"a[52:55], a[228:231], a[220:223], a[52:55]\n" \
"s_add_i32 s18, s15, s14\n" \
"v_writelane_b32 v11, s18, 40\n" \
M_"a[48:51], a[160:163], a[156:159], a[48:51]\n" \
"s_add_i32 s17, s17, 0x3004000\n" \
"v_writelane_b32 v11, s17, 41\n" \
M_"a[48:51], a[224:227], a[220:223], a[48:51]\n" \
"s_add_i32 s13, s13, 0x4120000\n" \
"v_writelane_b32 v11, s13, 42\n" \
M_"a[64:67], a[164:167], a[140:143], a[64:67]\n" \
"s_lshl_b32 s13, s9, 2\n" \
"s_or_b32 s7, s13, s7\n" \
M_"a[64:67], a[228:231], a[204:207], a[64:67]\n" \
"s_mul_i32 s7, s7, 0x90000\n" \
"s_lshl_b32 s13, s3, 14\n" \
M_"a[80:83], a[172:175], a[140:143], a[80:83]\n" \
"s_lshl_b32 s9, s9, 7\n" \
"s_lshl_b32 s8, s8, 4\n" \
M_"a[80:83], a[236:239], a[204:207], a[80:83]\n" \
"s_add_i32 s7, s7, s13\n" \
"s_or_b32 s13, s8, s9\n" \
M_"a[68:71], a[140:143], a[168:171], a[68:71]\n" \
"s_sub_i32 s8, s8, s9\n" \
"s_mulk_i32 s13, 0x480\n" \
"s_waitcnt vmcnt(11) lgkmcnt(0)\n" \
M_"a[68:71], a[204:207], a[232:235], a[68:71]\n" \
"s_barrier\n" \
M_"v[116:119], a[176:179], a[144:147], v[116:119]\n" \
"s_add_u32 m0, s40, 0x8000\n" \
"s_add_u32 s85, s39, 0xa000\n" \
M_"v[116:119], a[240:243], a[208:211], v[116:119]\n" \
B_"v20, s[80:83], s85 offen sc1 lds\n" \
M_"v[120:123], a[180:183], a[144:147], v[120:123]\n" \
X_"a[160:161], v32 offset:0\n" \
"v_accvgpr_read_b32 v212, a108\n" \
M_"v[120:123], a[244:247], a[208:211], v[120:123]\n" \
X_"a[162:163], v32 offset:512\n" \
"v_accvgpr_read_b32 v213, a109\n" \
M_"v[124:127], a[184:187], a[144:147], v[124:127]\n" \
"s_add_u32 m0, s40, 0x9000\n" \
"s_add_u32 s85, s39, 0xb000\n" \
M_"v[124:127], a[248:251], a[208:211], v[124:127]\n" \
B_"v20, s[80:83], s85 offen sc1 lds\n" \
M_"v[128:131], a[188:191], a[144:147], v[128:131]\n" \
X_"a[224:225], v32 offset:4096\n" \
"v_accvgpr_read_b32 v214, a110\n" \
M_"v[128:131], a[252:255], a[208:211], v[128:131]\n" \
R_"a[144:147], v28 offset:40960\n" \
"v_accvgpr_read_b32 v215, a111\n" \
M_"v[144:147], a[188:191], a[148:151], v[144:147]\n" \
R_"a[208:211], v29 offset:40960\n" \
"v_accvgpr_read_b32 v180, a124\n" \
M_"v[144:147], a[252:255], a[212:215], v[144:147]\n" \
"s_add_u32 m0, s40, 0xb000\n" \
"s_movk_i32 s84, 0x280\n" \
"s_nop 0\n" \
M_"v[140:143], a[184:187], a[148:151], v[140:143]\n" \
B_"v21, s[76:79], s84 offen offset:0 lds\n" \
M_"v[140:143], a[248:251], a[212:215], v[140:143]\n" \
X_"a[226:227], v32 offset:4608\n" \
"v_accvgpr_read_b32 v181, a125\n" \
M_"v[136:139], a[180:183], a[148:151], v[136:139]\n" \
"s_add_u32 s88, s42, 0x1000\n" \
"buffer_store_dwordx4 v[228:231], v20, s[80:83], s88 offen offset:2048 sc1\n" \
M_"v[136:139], a[244:247], a[212:215], v[136:139]\n" \
X_"a[164:165], v33 offset:0\n" \
"v_accvgpr_read_b32 v182, a126\n" \
M_"v[132:135], a[176:179], a[148:151], v[132:135]\n" \
"v_accvgpr_read_b32 v183, a127\n" \
C_"v212, v212, v213\n" \
M_"v[132:135], a[240:243], a[212:215], v[132:135]\n" \
R_"a[148:151], v28 offset:43008\n" \
C_"v213, v214, v215\n" \
M_"v[148:151], a[176:179], a[152:155], v[148:151]\n" \
R_"a[212:215], v29 offset:43008\n" \
C_"v214, v180, v181\n" \
M_"v[148:151], a[240:243], a[216:219], v[148:151]\n" \
"s_add_u32 m0, s31, 0x8000\n" \
"s_mov_b32 s84, 0x280000\n" \
M_"v[152:155], a[180:183], a[152:155], v[152:155]\n" \
B_"v26, s[68:71], s84 offen offset:0 lds\n" \
M_"v[152:155], a[244:247], a[216:219], v[152:155]\n" \
X_"a[166:167], v33 offset:512\n" \
C_"v215, v182, v183\n" \
M_"v[156:159], a[184:187], a[152:155], v[156:159]\n" \
X_"a[228:229], v33 offset:4096\n" \
"v_accvgpr_read_b32 v208, a104\n" \
M_"v[156:159], a[248:251], a[216:219], v[156:159]\n" \
"s_addk_i32 s84, 0x7c00\n" \
M_"v[160:163], a[188:191], a[152:155], v[160:163]\n" \
B_"v26, s[68:71], s84 offen offset:1024 lds\n" \
M_"v[160:163], a[252:255], a[216:219], v[160:163]\n" \
R_"a[152:155], v28 offset:45056\n" \
"v_accvgpr_read_b32 v209, a105\n" \
M_"v[176:179], a[188:191], a[156:159], v[176:179]\n" \
R_"a[216:219], v29 offset:45056\n" \
"v_accvgpr_read_b32 v210, a106\n" \
M_"v[176:179], a[252:255], a[220:223], v[176:179]\n" \
"s_addk_i32 s84, 0x7c00\n" \
M_"v[172:175], a[184:187], a[156:159], v[172:175]\n" \
B_"v27, s[68:71], s84 offen offset:2048 lds\n" \
M_"v[172:175], a[248:251], a[220:223], v[172:175]\n" \
X_"a[230:231], v33 offset:4608\n" \
"v_accvgpr_read_b32 v211, a107\n" \
M_"v[168:171], a[180:183], a[156:159], v[168:171]\n" \
X_"a[172:173], v35 offset:0\n" \
"v_accvgpr_read_b32 v180, a120\n" \
M_"v[168:171], a[244:247], a[220:223], v[168:171]\n" \
X_"a[174:175], v35 offset:512\n" \
"v_accvgpr_read_b32 v181, a121\n" \
M_"v[164:167], a[176:179], a[156:159], v[164:167]\n" \
"v_accvgpr_read_b32 v182, a122\n" \
"v_accvgpr_read_b32 v183, a123\n" \
M_"v[164:167], a[240:243], a[220:223], v[164:167]\n" \
R_"a[156:159], v28 offset:47104\n" \
C_"v208, v208, v209\n" \
M_"v[84:87], a[176:179], a[136:139], v[84:87]\n" \
R_"a[220:223], v29 offset:47104\n" \
C_"v209, v210, v211\n" \
M_"v[84:87], a[240:243], a[200:203], v[84:87]\n" \
"s_addk_i32 s84, 0x7c00\n" \
M_"v[88:91], a[180:183], a[136:139], v[88:91]\n" \
B_"v27, s[68:71], s84 offen offset:3072 lds\n" \
M_"v[88:91], a[244:247], a[200:203], v[88:91]\n" \
"s_add_u32 m0, s30, 0x0\n" \
"s_movk_i32 s84, 0x300\n" \
"s_nop 0\n" \
M_"v[92:95], a[184:187], a[136:139], v[92:95]\n" \
B_"v24, s[76:79], s84 offen offset:0 lds\n" \
M_"v[92:95], a[248:251], a[200:203], v[92:95]\n" \
X_"a[236:237], v35 offset:4096\n" \
C_"v210, v180, v181\n" \
M_"v[96:99], a[188:191], a[136:139], v[96:99]\n" \
X_"a[238:239], v35 offset:4608\n" \
C_"v211, v182, v183\n" \
M_"v[96:99], a[252:255], a[200:203], v[96:99]\n" \
R_"a[136:139], v28 offset:36864\n" \
"v_accvgpr_read_b32 v204, a100\n" \
M_"v[112:115], a[188:191], a[140:143], v[112:115]\n" \
R_"a[200:203], v29 offset:36864\n" \
"v_accvgpr_read_b32 v205, a101\n" \
M_"v[112:115], a[252:255], a[204:207], v[112:115]\n" \
"s_movk_i32 s84, 0x2300\n" \
"s_nop 0\n" \
M_"v[108:111], a[184:187], a[140:143], v[108:111]\n" \
B_"v25, s[76:79], s84 offen offset:1024 lds\n" \
M_"v[108:111], a[248:251], a[204:207], v[108:111]\n" \
"s_add_u32 s89, s42, 0x1000\n" \
"buffer_store_dwordx4 v[232:235], v20, s[80:83], s89 offen offset:1024 sc1\n" \
M_"v[104:107], a[180:183], a[140:143], v[104:107]\n" \
X_"a[168:169], v34 offset:0\n" \
"v_accvgpr_read_b32 v206, a102\n" \
M_"v[104:107], a[244:247], a[204:207], v[104:107]\n" \
X_"a[170:171], v34 offset:512\n" \
"v_accvgpr_read_b32 v207, a103\n" \
M_"v[100:103], a[176:179], a[140:143], v[100:103]\n" \
X_"a[232:233], v34 offset:4096\n" \
"v_accvgpr_read_b32 v180, a116\n" \
M_"v[100:103], a[240:243], a[204:207], v[100:103]\n" \
R_"a[140:143], v28 offset:38912\n" \
"v_accvgpr_read_b32 v181, a117\n" \
M_"v[52:55], a[176:179], a[128:131], v[52:55]\n" \
R_"a[204:207], v29 offset:38912\n" \
"v_accvgpr_read_b32 v182, a118\n" \
M_"v[52:55], a[240:243], a[192:195], v[52:55]\n" \
"s_movk_i32 s84, 0x4300\n" \
M_"v[56:59], a[180:183], a[128:131], v[56:59]\n" \
B_"v24, s[76:79], s84 offen offset:2048 lds\n" \
M_"v[56:59], a[244:247], a[192:195], v[56:59]\n" \
"s_movk_i32 s84, 0x6300\n" \
M_"v[60:63], a[184:187], a[128:131], v[60:63]\n" \
B_"v25, s[76:79], s84 offen offset:3072 lds\n" \
M_"v[60:63], a[248:251], a[192:195], v[60:63]\n" \
X_"a[234:235], v34 offset:4608\n" \
"v_accvgpr_read_b32 v183, a119\n" \
M_"v[64:67], a[188:191], a[128:131], v[64:67]\n" \
C_"v204, v204, v205\n" \
C_"v205, v206, v207\n" \
M_"v[64:67], a[252:255], a[192:195], v[64:67]\n" \
R_"a[128:131], v28 offset:32768\n" \
C_"v206, v180, v181\n" \
M_"v[80:83], a[188:191], a[132:135], v[80:83]\n" \
R_"a[192:195], v29 offset:32768\n" \
C_"v207, v182, v183\n" \
M_"v[80:83], a[252:255], a[196:199], v[80:83]\n" \
"v_accvgpr_read_b32 v200, a96\n" \
"v_accvgpr_read_b32 v201, a97\n" \
M_"v[76:79], a[184:187], a[132:135], v[76:79]\n" \
"v_accvgpr_read_b32 v202, a98\n" \
"v_accvgpr_read_b32 v203, a99\n" \
M_"v[76:79], a[248:251], a[196:199], v[76:79]\n" \
"v_accvgpr_read_b32 v180, a112\n" \
"v_accvgpr_read_b32 v181, a113\n" \
M_"v[72:75], a[180:183], a[132:135], v[72:75]\n" \
"v_accvgpr_read_b32 v182, a114\n" \
"v_accvgpr_read_b32 v183, a115\n" \
M_"v[72:75], a[244:247], a[196:199], v[72:75]\n" \
C_"v200, v200, v201\n" \
C_"v201, v202, v203\n" \
M_"v[68:71], a[176:179], a[132:135], v[68:71]\n" \
C_"v202, v180, v181\n" \
C_"v203, v182, v183\n" \
M_"v[68:71], a[240:243], a[196:199], v[68:71]\n" \
R_"a[132:135], v28 offset:34816\n" \
R_"a[196:199], v29 offset:34816\n" \
"s_waitcnt lgkmcnt(15)\n" \
M_"a[0:3], a[160:163], a[144:147], a[0:3]\n" \
"s_add_u32 m0, s30, 0x1000\n" \
"s_addk_i32 s84, 0x3000\n" \
M_"a[0:3], a[224:227], a[208:211], a[0:3]\n" \
B_"v24, s[76:79], s84 offen offset:0 lds\n" \
M_"a[4:7], a[164:167], a[144:147], a[4:7]\n" \
X_"a[176:177], v36 offset:0\n" \
"v_accvgpr_read_b32 v196, a76\n" \
M_"a[4:7], a[228:231], a[208:211], a[4:7]\n" \
X_"a[178:179], v36 offset:1024\n" \
"v_accvgpr_read_b32 v197, a77\n" \
M_"a[12:15], a[172:175], a[144:147], a[12:15]\n" \
X_"a[240:241], v36 offset:8192\n" \
"v_accvgpr_read_b32 v198, a78\n" \
"s_waitcnt lgkmcnt(15)\n" \
M_"a[12:15], a[236:239], a[208:211], a[12:15]\n" \
"s_add_u32 s88, s42, 0x1000\n" \
"buffer_store_dwordx4 v[236:239], v20, s[80:83], s88 offen offset:0 sc1\n" \
"s_waitcnt lgkmcnt(11)\n" \
M_"a[8:11], a[144:147], a[168:171], a[8:11]\n" \
X_"a[242:243], v36 offset:9216\n" \
"v_accvgpr_read_b32 v199, a79\n" \
"s_waitcnt lgkmcnt(8)\n" \
"s_nop 0\n" \
M_"a[8:11], a[208:211], a[232:235], a[8:11]\n" \
X_"a[180:181], v37 offset:0\n" \
"v_accvgpr_read_b32 v180, a92\n" \
M_"a[24:27], a[148:151], a[168:171], a[24:27]\n" \
X_"a[182:183], v37 offset:1024\n" \
"v_accvgpr_read_b32 v181, a93\n" \
M_"a[24:27], a[212:215], a[232:235], a[24:27]\n" \
X_"a[244:245], v37 offset:8192\n" \
"v_accvgpr_read_b32 v182, a94\n" \
M_"a[28:31], a[172:175], a[148:151], a[28:31]\n" \
"v_accvgpr_read_b32 v183, a95\n" \
C_"v196, v196, v197\n" \
M_"a[28:31], a[236:239], a[212:215], a[28:31]\n" \
"s_addk_i32 s84, 0x2000\n" \
"s_nop 0\n" \
M_"a[20:23], a[164:167], a[148:151], a[20:23]\n" \
B_"v25, s[76:79], s84 offen offset:1024 lds\n" \
M_"a[20:23], a[228:231], a[212:215], a[20:23]\n" \
X_"a[246:247], v37 offset:9216\n" \
C_"v197, v198, v199\n" \
M_"a[16:19], a[160:163], a[148:151], a[16:19]\n" \
".fill (L_pb + 69344 - .) / 4, 4, 0xBF800000\n" \
"L_pp17:\n" \
"s_cbranch_execz L_pret\n" \
X_"a[184:185], v38 offset:0\n" \
C_"v198, v180, v181\n" \
M_"a[16:19], a[224:227], a[212:215], a[16:19]\n" \
X_"a[186:187], v38 offset:1024\n" \
C_"v199, v182, v183\n" \
M_"a[32:35], a[160:163], a[152:155], a[32:35]\n" \
"s_addk_i32 s84, 0x2000\n" \
M_"a[32:35], a[224:227], a[216:219], a[32:35]\n" \
B_"v24, s[76:79], s84 offen offset:2048 lds\n" \
M_"a[36:39], a[164:167], a[152:155], a[36:39]\n" \
X_"a[248:249], v38 offset:8192\n" \
"v_accvgpr_read_b32 v192, a72\n" \
M_"a[36:39], a[228:231], a[216:219], a[36:39]\n" \
X_"a[250:251], v38 offset:9216\n" \
"v_accvgpr_read_b32 v193, a73\n" \
M_"a[44:47], a[172:175], a[152:155], a[44:47]\n" \
X_"a[188:189], v39 offset:0\n" \
"v_accvgpr_read_b32 v194, a74\n" \
M_"a[44:47], a[236:239], a[216:219], a[44:47]\n" \
X_"a[190:191], v39 offset:1024\n" \
"v_accvgpr_read_b32 v195, a75\n" \
M_"a[40:43], a[152:155], a[168:171], a[40:43]\n" \
"s_add_u32 s89, s42, 0x3000\n" \
"buffer_store_dwordx4 v[240:243], v20, s[80:83], s89 offen offset:3072 sc1\n" \
M_"a[40:43], a[216:219], a[232:235], a[40:43]\n" \
X_"a[252:253], v39 offset:8192\n" \
"v_accvgpr_read_b32 v180, a88\n" \
M_"a[56:59], a[156:159], a[168:171], a[56:59]\n" \
X_"a[254:255], v39 offset:9216\n" \
"v_accvgpr_read_b32 v181, a89\n" \
M_"a[56:59], a[220:223], a[232:235], a[56:59]\n" \
"v_accvgpr_read_b32 v182, a90\n" \
"v_accvgpr_read_b32 v183, a91\n" \
M_"a[60:63], a[172:175], a[156:159], a[60:63]\n" \
C_"v192, v192, v193\n" \
C_"v193, v194, v195\n" \
M_"a[60:63], a[236:239], a[220:223], a[60:63]\n" \
"s_addk_i32 s84, 0x2000\n" \
M_"a[52:55], a[164:167], a[156:159], a[52:55]\n" \
B_"v25, s[76:79], s84 offen offset:3072 lds\n" \
M_"a[52:55], a[228:231], a[220:223], a[52:55]\n" \
C_"v194, v180, v181\n" \
C_"v195, v182, v183\n" \
M_"a[48:51], a[160:163], a[156:159], a[48:51]\n" \
"s_mulk_i32 s8, 0x480\n" \
"s_or_b32 s13, s13, s11\n" \
M_"a[48:51], a[224:227], a[220:223], a[48:51]\n" \
"s_or_b32 s8, s8, s11\n" \
"s_and_b32 s11, s2, 1\n" \
M_"a[64:67], a[164:167], a[140:143], a[64:67]\n" \
"s_cmp_lg_u32 s11, 0\n" \
"s_cselect_b32 s12, s15, s12\n" \
M_"a[64:67], a[228:231], a[204:207], a[64:67]\n" \
"s_bfe_u32 s9, s2, 0x20001\n" \
"s_add_i32 s15, s7, 0x4240000\n" \
M_"a[80:83], a[172:175], a[140:143], a[80:83]\n" \
"s_addk_i32 s12, 0x4000\n" \
"s_lshl_b32 s5, s5, 16\n" \
M_"a[80:83], a[236:239], a[204:207], a[80:83]\n" \
"s_xor_b32 s9, s9, s3\n" \
M_"a[68:71], a[140:143], a[168:171], a[68:71]\n" \
"s_cmp_lg_u32 s9, 0\n" \
"s_cselect_b32 s12, s6, s12\n" \
"s_waitcnt vmcnt(11) lgkmcnt(0)\n" \
M_"a[68:71], a[204:207], a[232:235], a[68:71]\n" \
"s_barrier\n" \
"v_mov_b32_e32 v45, s62\n" \
"s_mov_b64 exec, 1\n" \
"buffer_store_dword v45, off, s[80:83], s37 offset:0 sc1\n" \
"s_mov_b64 exec, -1\n" \
"v_xor_b32_e32 v28, 0x20000, v28\n" \
"v_xor_b32_e32 v29, 0x20000, v29\n" \
M_"v[116:119], a[176:179], a[144:147], v[116:119]\n" \
"s_add_u32 m0, s40, 0x0\n" \
"s_add_u32 s85, s39, 0xc000\n" \
M_"v[116:119], a[240:243], a[208:211], v[116:119]\n" \
B_"v20, s[80:83], s85 offen sc1 lds\n" \
M_"v[120:123], a[180:183], a[144:147], v[120:123]\n" \
X_"a[160:161], v32 offset:32768\n" \
"s_add_i32 s5, s15, s5\n" \
"s_nop 0\n" \
M_"v[120:123], a[244:247], a[208:211], v[120:123]\n" \
X_"a[162:163], v32 offset:33280\n" \
"v_writelane_b32 v11, s12, 43\n" \
M_"v[124:127], a[184:187], a[144:147], v[124:127]\n" \
"s_add_u32 m0, s40, 0x1000\n" \
"s_add_u32 s85, s39, 0xd000\n" \
M_"v[124:127], a[248:251], a[208:211], v[124:127]\n" \
B_"v20, s[80:83], s85 offen sc1 lds\n" \
M_"v[128:131], a[188:191], a[144:147], v[128:131]\n" \
X_"a[224:225], v32 offset:36864\n" \
"s_add_i32 s7, s7, 0x42c0000\n" \
M_"v[128:131], a[252:255], a[208:211], v[128:131]\n" \
R_"a[144:147], v28 offset:8192\n" \
"v_writelane_b32 v11, s5, 44\n" \
M_"v[144:147], a[188:191], a[148:151], v[144:147]\n" \
R_"a[208:211], v29 offset:8192\n" \
"s_lshl_b32 s5, s11, 10\n" \
M_"v[144:147], a[252:255], a[212:215], v[144:147]\n" \
"s_add_u32 m0, s40, 0x3000\n" \
"s_movk_i32 s84, 0x300\n" \
M_"v[140:143], a[184:187], a[148:151], v[140:143]\n" \
B_"v21, s[76:79], s84 offen offset:0 lds\n" \
M_"v[140:143], a[248:251], a[212:215], v[140:143]\n" \
X_"a[226:227], v32 offset:37376\n" \
"s_or_b32 s5, s7, s5\n" \
M_"v[136:139], a[180:183], a[148:151], v[136:139]\n" \
"s_cmp_lg_u32 s9, 0\n" \
"s_cselect_b32 s5, s6, s5\n" \
M_"v[136:139], a[244:247], a[212:215], v[136:139]\n" \
X_"a[164:165], v33 offset:32768\n" \
"s_add_i32 s8, s8, 0x21c000\n" \
M_"v[132:135], a[176:179], a[148:151], v[132:135]\n" \
"v_writelane_b32 v11, s5, 45\n" \
"s_add_i32 s5, s13, s10\n" \
M_"v[132:135], a[240:243], a[212:215], v[132:135]\n" \
R_"a[148:151], v28 offset:10240\n" \
"v_writelane_b32 v11, s5, 46\n" \
M_"v[148:151], a[176:179], a[152:155], v[148:151]\n" \
R_"a[212:215], v29 offset:10240\n" \
"s_add_i32 s5, s8, s10\n" \
M_"v[148:151], a[240:243], a[216:219], v[148:151]\n" \
"s_add_u32 m0, s31, 0x0\n" \
"s_mov_b32 s84, 0x300000\n" \
M_"v[152:155], a[180:183], a[152:155], v[152:155]\n" \
B_"v26, s[68:71], s84 offen offset:0 lds\n" \
M_"v[152:155], a[244:247], a[216:219], v[152:155]\n" \
"s_addk_i32 s84, 0x7c00\n" \
"s_nop 0\n" \
M_"v[156:159], a[184:187], a[152:155], v[156:159]\n" \
B_"v26, s[68:71], s84 offen offset:1024 lds\n" \
M_"v[156:159], a[248:251], a[216:219], v[156:159]\n" \
X_"a[166:167], v33 offset:33280\n" \
"v_writelane_b32 v11, s5, 47\n" \
M_"v[160:163], a[188:191], a[152:155], v[160:163]\n" \
X_"a[228:229], v33 offset:36864\n" \
M_"v[160:163], a[252:255], a[216:219], v[160:163]\n" \
R_"a[152:155], v28 offset:12288\n" \
M_"v[176:179], a[188:191], a[156:159], v[176:179]\n" \
R_"a[216:219], v29 offset:12288\n" \
M_"v[176:179], a[252:255], a[220:223], v[176:179]\n" \
"s_addk_i32 s84, 0x7c00\n" \
M_"v[172:175], a[184:187], a[156:159], v[172:175]\n" \
B_"v27, s[68:71], s84 offen offset:2048 lds\n" \
M_"v[172:175], a[248:251], a[220:223], v[172:175]\n" \
X_"a[230:231], v33 offset:37376\n" \
M_"v[168:171], a[180:183], a[156:159], v[168:171]\n" \
"s_addk_i32 s84, 0x7c00\n" \
M_"v[168:171], a[244:247], a[220:223], v[168:171]\n" \
B_"v27, s[68:71], s84 offen offset:3072 lds\n" \
M_"v[164:167], a[176:179], a[156:159], v[164:167]\n" \
X_"a[172:173], v35 offset:32768\n" \
M_"v[164:167], a[240:243], a[220:223], v[164:167]\n" \
R_"a[156:159], v28 offset:14336\n" \
M_"v[84:87], a[176:179], a[136:139], v[84:87]\n" \
R_"a[220:223], v29 offset:14336\n" \
M_"v[84:87], a[240:243], a[200:203], v[84:87]\n" \
"s_add_u32 s88, s42, 0x3000\n" \
"buffer_store_dwordx4 v[244:247], v20, s[80:83], s88 offen offset:2048 sc1\n" \
M_"v[88:91], a[180:183], a[136:139], v[88:91]\n" \
X_"a[174:175], v35 offset:33280\n" \
M_"v[88:91], a[244:247], a[200:203], v[88:91]\n" \
X_"a[236:237], v35 offset:36864\n" \
M_"v[92:95], a[184:187], a[136:139], v[92:95]\n" \
X_"a[238:239], v35 offset:37376\n" \
M_"v[92:95], a[248:251], a[200:203], v[92:95]\n" \
"s_add_u32 m0, s30, 0x8000\n" \
"s_movk_i32 s84, 0x380\n" \
M_"v[96:99], a[188:191], a[136:139], v[96:99]\n" \
B_"v24, s[76:79], s84 offen offset:0 lds\n" \
M_"v[96:99], a[252:255], a[200:203], v[96:99]\n" \
R_"a[136:139], v28 offset:4096\n" \
M_"v[112:115], a[188:191], a[140:143], v[112:115]\n" \
R_"a[200:203], v29 offset:4096\n" \
M_"v[112:115], a[252:255], a[204:207], v[112:115]\n" \
X_"a[168:169], v34 offset:32768\n" \
M_"v[108:111], a[184:187], a[140:143], v[108:111]\n" \
"s_movk_i32 s84, 0x2380\n" \
M_"v[108:111], a[248:251], a[204:207], v[108:111]\n" \
B_"v25, s[76:79], s84 offen offset:1024 lds\n" \
M_"v[104:107], a[180:183], a[140:143], v[104:107]\n" \
X_"a[170:171], v34 offset:33280\n" \
M_"v[104:107], a[244:247], a[204:207], v[104:107]\n" \
X_"a[232:233], v34 offset:36864\n" \
M_"v[100:103], a[176:179], a[140:143], v[100:103]\n" \
"s_cmp_lg_u32 s11, 0\n" \
"s_cselect_b32 s5, s8, s13\n" \
M_"v[100:103], a[240:243], a[204:207], v[100:103]\n" \
R_"a[140:143], v28 offset:6144\n" \
"s_addk_i32 s5, 0x100\n" \
M_"v[52:55], a[176:179], a[128:131], v[52:55]\n" \
R_"a[204:207], v29 offset:6144\n" \
M_"v[52:55], a[240:243], a[192:195], v[52:55]\n" \
"s_movk_i32 s84, 0x4380\n" \
M_"v[56:59], a[180:183], a[128:131], v[56:59]\n" \
B_"v24, s[76:79], s84 offen offset:2048 lds\n" \
M_"v[56:59], a[244:247], a[192:195], v[56:59]\n" \
"s_movk_i32 s84, 0x6380\n" \
M_"v[60:63], a[184:187], a[128:131], v[60:63]\n" \
B_"v25, s[76:79], s84 offen offset:3072 lds\n" \
M_"v[60:63], a[248:251], a[192:195], v[60:63]\n" \
X_"a[234:235], v34 offset:37376\n" \
M_"v[64:67], a[188:191], a[128:131], v[64:67]\n" \
"s_cmp_lg_u32 s9, 0\n" \
"s_cselect_b32 s5, s6, s5\n" \
M_"v[64:67], a[252:255], a[192:195], v[64:67]\n" \
R_"a[128:131], v28 offset:0\n" \
"v_writelane_b32 v11, s5, 48\n" \
M_"v[80:83], a[188:191], a[132:135], v[80:83]\n" \
R_"a[192:195], v29 offset:0\n" \
"s_or_b32 s5, s7, s14\n" \
M_"v[80:83], a[252:255], a[196:199], v[80:83]\n" \
"v_writelane_b32 v11, s5, 56\n" \
"s_lshl_b32 s5, s3, 17\n" \
M_"v[76:79], a[184:187], a[132:135], v[76:79]\n" \
"s_add_i32 s5, s15, s5\n" \
"s_or_b32 s4, s5, s4\n" \
M_"v[76:79], a[248:251], a[196:199], v[76:79]\n" \
"s_or_b32 s4, s4, 0x3840\n" \
"v_writelane_b32 v11, s4, 6\n" \
M_"v[72:75], a[180:183], a[132:135], v[72:75]\n" \
"s_mov_b32 s4, 0xff0000\n" \
M_"v[72:75], a[244:247], a[196:199], v[72:75]\n" \
"s_cmp_lg_u32 s9, 0\n" \
"s_cselect_b32 s4, s16, s4\n" \
M_"v[68:71], a[176:179], a[132:135], v[68:71]\n" \
"v_writelane_b32 v11, s4, 7\n" \
"s_lshl_b32 s4, s2, 16\n" \
M_"v[68:71], a[240:243], a[196:199], v[68:71]\n" \
R_"a[132:135], v28 offset:2048\n" \
R_"a[196:199], v29 offset:2048\n" \
"s_waitcnt lgkmcnt(15)\n" \
M_"a[0:3], a[160:163], a[144:147], a[0:3]\n" \
"s_add_u32 m0, s30, 0x9000\n" \
"s_addk_i32 s84, 0x3000\n" \
M_"a[0:3], a[224:227], a[208:211], a[0:3]\n" \
B_"v24, s[76:79], s84 offen offset:0 lds\n" \
M_"a[4:7], a[164:167], a[144:147], a[4:7]\n" \
X_"a[176:177], v36 offset:32768\n" \
"s_add_i32 s5, s4, 0x10000\n" \
M_"a[4:7], a[228:231], a[208:211], a[4:7]\n" \
X_"a[178:179], v36 offset:33792\n" \
"s_and_b32 s5, s5, 0x70000\n" \
"s_waitcnt lgkmcnt(15)\n" \
M_"a[12:15], a[172:175], a[144:147], a[12:15]\n" \
X_"a[240:241], v36 offset:40960\n" \
"s_add_i32 s5, s15, s5\n" \
"s_waitcnt lgkmcnt(15)\n" \
M_"a[12:15], a[236:239], a[208:211], a[12:15]\n" \
"s_add_u32 s89, s42, 0x3000\n" \
"buffer_store_dwordx4 v[184:187], v20, s[80:83], s89 offen offset:1024 sc1\n" \
"s_waitcnt lgkmcnt(11)\n" \
M_"a[8:11], a[144:147], a[168:171], a[8:11]\n" \
X_"a[242:243], v36 offset:41984\n" \
"v_writelane_b32 v11, s5, 49\n" \
"s_waitcnt lgkmcnt(8)\n" \
M_"a[8:11], a[208:211], a[232:235], a[8:11]\n" \
X_"a[180:181], v37 offset:32768\n" \
"s_add_i32 s5, s4, 0x20000\n" \
M_"a[24:27], a[148:151], a[168:171], a[24:27]\n" \
X_"a[182:183], v37 offset:33792\n" \
"s_and_b32 s5, s5, 0x70000\n" \
M_"a[24:27], a[212:215], a[232:235], a[24:27]\n" \
X_"a[244:245], v37 offset:40960\n" \
"s_add_i32 s5, s15, s5\n" \
M_"a[28:31], a[172:175], a[148:151], a[28:31]\n" \
"s_bitset1_b32 s5, 11\n" \
"v_writelane_b32 v11, s5, 50\n" \
M_"a[28:31], a[236:239], a[212:215], a[28:31]\n" \
"s_addk_i32 s84, 0x2000\n" \
M_"a[20:23], a[164:167], a[148:151], a[20:23]\n" \
B_"v25, s[76:79], s84 offen offset:1024 lds\n" \
M_"a[20:23], a[228:231], a[212:215], a[20:23]\n" \
X_"a[246:247], v37 offset:41984\n" \
"s_add_i32 s5, s4, 0x30000\n" \
M_"a[16:19], a[160:163], a[148:151], a[16:19]\n" \
X_"a[184:185], v38 offset:32768\n" \
"s_and_b32 s5, s5, 0x70000\n" \
M_"a[16:19], a[224:227], a[212:215], a[16:19]\n" \
X_"a[186:187], v38 offset:33792\n" \
"s_add_i32 s5, s15, s5\n" \
M_"a[32:35], a[160:163], a[152:155], a[32:35]\n" \
"s_addk_i32 s84, 0x2000\n" \
M_"a[32:35], a[224:227], a[216:219], a[32:35]\n" \
B_"v24, s[76:79], s84 offen offset:2048 lds\n" \
M_"a[36:39], a[164:167], a[152:155], a[36:39]\n" \
X_"a[248:249], v38 offset:40960\n" \
"s_bitset1_b32 s5, 12\n" \
"s_nop 0\n" \
M_"a[36:39], a[228:231], a[216:219], a[36:39]\n" \
X_"a[250:251], v38 offset:41984\n" \
"v_writelane_b32 v11, s5, 51\n" \
M_"a[44:47], a[172:175], a[152:155], a[44:47]\n" \
X_"a[188:189], v39 offset:32768\n" \
"s_and_b32 s5, s4, 0x70000\n" \
M_"a[44:47], a[236:239], a[216:219], a[44:47]\n" \
X_"a[190:191], v39 offset:33792\n" \
"s_xor_b32 s5, s5, 0x40000\n" \
M_"a[40:43], a[152:155], a[168:171], a[40:43]\n" \
"s_add_u32 s88, s42, 0x3000\n" \
"buffer_store_dwordx4 v[188:191], v20, s[80:83], s88 offen offset:0 sc1\n" \
M_"a[40:43], a[216:219], a[232:235], a[40:43]\n" \
X_"a[252:253], v39 offset:40960\n" \
"s_add_i32 s5, s15, s5\n" \
M_"a[56:59], a[156:159], a[168:171], a[56:59]\n" \
X_"a[254:255], v39 offset:41984\n" \
"s_or_b32 s5, s5, 0x1800\n" \
M_"a[56:59], a[220:223], a[232:235], a[56:59]\n" \
"v_writelane_b32 v11, s5, 52\n" \
"s_add_i32 s5, s4, 0x50000\n" \
M_"a[60:63], a[172:175], a[156:159], a[60:63]\n" \
"s_and_b32 s5, s5, 0x70000\n" \
"s_add_i32 s5, s15, s5\n" \
M_"a[60:63], a[236:239], a[220:223], a[60:63]\n" \
"s_addk_i32 s84, 0x2000\n" \
M_"a[52:55], a[164:167], a[156:159], a[52:55]\n" \
B_"v25, s[76:79], s84 offen offset:3072 lds\n" \
M_"a[52:55], a[228:231], a[220:223], a[52:55]\n" \
"s_bitset1_b32 s5, 13\n" \
"v_writelane_b32 v11, s5, 53\n" \
M_"a[48:51], a[160:163], a[156:159], a[48:51]\n" \
"s_add_i32 s5, s4, 0x60000\n" \
"s_and_b32 s5, s5, 0x70000\n" \
M_"a[48:51], a[224:227], a[220:223], a[48:51]\n" \
"s_add_i32 s4, s4, 0x70000\n" \
"s_add_i32 s5, s15, s5\n" \
M_"a[64:67], a[164:167], a[140:143], a[64:67]\n" \
"s_and_b32 s4, s4, 0x70000\n" \
"s_or_b32 s5, s5, 0x2800\n" \
M_"a[64:67], a[228:231], a[204:207], a[64:67]\n" \
"v_writelane_b32 v11, s5, 54\n" \
"s_add_i32 s15, s15, s4\n" \
M_"a[80:83], a[172:175], a[140:143], a[80:83]\n" \
"s_or_b32 s4, s15, 0x3000\n" \
"v_writelane_b32 v11, s4, 55\n" \
M_"a[80:83], a[236:239], a[204:207], a[80:83]\n" \
"s_movk_i32 s11, 0x4000\n" \
"s_lshl_b32 s4, s2, 2\n" \
M_"a[68:71], a[140:143], a[168:171], a[68:71]\n" \
"s_and_b32 s4, s4, 28\n" \
"s_bfe_u32 s5, s2, 0x20003\n" \
"s_waitcnt vmcnt(11) lgkmcnt(0)\n" \
M_"a[68:71], a[204:207], a[232:235], a[68:71]\n" \
"s_barrier\n" \
"v_xor_b32_e32 v28, 0x20000, v28\n" \
"v_xor_b32_e32 v29, 0x20000, v29\n" \
M_"v[116:119], a[176:179], a[144:147], v[116:119]\n" \
"s_add_u32 m0, s40, 0x8000\n" \
"s_add_u32 s85, s39, 0xe000\n" \
M_"v[116:119], a[240:243], a[208:211], v[116:119]\n" \
B_"v20, s[80:83], s85 offen sc1 lds\n" \
M_"v[120:123], a[180:183], a[144:147], v[120:123]\n" \
X_"a[160:161], v32 offset:0\n" \
"s_or_b32 s4, s4, s5\n" \
M_"v[120:123], a[244:247], a[208:211], v[120:123]\n" \
X_"a[162:163], v32 offset:512\n" \
"s_lshr_b32 s5, s2, 5\n" \
M_"v[124:127], a[184:187], a[144:147], v[124:127]\n" \
"s_add_u32 m0, s40, 0x9000\n" \
"s_add_u32 s85, s39, 0xf000\n" \
M_"v[124:127], a[248:251], a[208:211], v[124:127]\n" \
B_"v20, s[80:83], s85 offen sc1 lds\n" \
M_"v[128:131], a[188:191], a[144:147], v[128:131]\n" \
X_"a[224:225], v32 offset:4096\n" \
"s_lshr_b32 s19, s3, 1\n" \
M_"v[128:131], a[252:255], a[208:211], v[128:131]\n" \
R_"a[144:147], v28 offset:8192\n" \
"s_lshl_b32 s18, s5, 1\n" \
M_"v[144:147], a[188:191], a[148:151], v[144:147]\n" \
R_"a[208:211], v29 offset:8192\n" \
"s_lshl_b32 s6, s4, 20\n" \
M_"v[144:147], a[252:255], a[212:215], v[144:147]\n" \
"s_add_u32 m0, s40, 0xb000\n" \
"s_movk_i32 s84, 0x380\n" \
M_"v[140:143], a[184:187], a[148:151], v[140:143]\n" \
B_"v21, s[76:79], s84 offen offset:0 lds\n" \
M_"v[140:143], a[248:251], a[212:215], v[140:143]\n" \
X_"a[226:227], v32 offset:4608\n" \
"s_lshl_b32 s7, s4, 8\n" \
M_"v[136:139], a[180:183], a[148:151], v[136:139]\n" \
"s_and_b32 s13, s3, 1\n" \
"s_add_i32 s14, s18, s19\n" \
M_"v[136:139], a[244:247], a[212:215], v[136:139]\n" \
X_"a[164:165], v33 offset:0\n" \
M_"v[132:135], a[176:179], a[148:151], v[132:135]\n" \
"s_add_u32 s6, s26, s6\n" \
"s_addc_u32 s9, s27, 0\n" \
M_"v[132:135], a[240:243], a[212:215], v[132:135]\n" \
R_"a[148:151], v28 offset:10240\n" \
M_"v[148:151], a[176:179], a[152:155], v[148:151]\n" \
R_"a[212:215], v29 offset:10240\n" \
M_"v[148:151], a[240:243], a[216:219], v[148:151]\n" \
"s_add_u32 m0, s31, 0x8000\n" \
"s_mov_b32 s84, 0x380000\n" \
M_"v[152:155], a[180:183], a[152:155], v[152:155]\n" \
B_"v26, s[68:71], s84 offen offset:0 lds\n" \
M_"v[152:155], a[244:247], a[216:219], v[152:155]\n" \
"s_addk_i32 s84, 0x7c00\n" \
"s_nop 0\n" \
M_"v[156:159], a[184:187], a[152:155], v[156:159]\n" \
B_"v26, s[68:71], s84 offen offset:1024 lds\n" \
M_"v[156:159], a[248:251], a[216:219], v[156:159]\n" \
X_"a[166:167], v33 offset:512\n" \
M_"v[160:163], a[188:191], a[152:155], v[160:163]\n" \
X_"a[228:229], v33 offset:4096\n" \
M_"v[160:163], a[252:255], a[216:219], v[160:163]\n" \
R_"a[152:155], v28 offset:12288\n" \
M_"v[176:179], a[188:191], a[156:159], v[176:179]\n" \
R_"a[216:219], v29 offset:12288\n" \
M_"v[176:179], a[252:255], a[220:223], v[176:179]\n" \
"s_addk_i32 s84, 0x7c00\n" \
M_"v[172:175], a[184:187], a[156:159], v[172:175]\n" \
B_"v27, s[68:71], s84 offen offset:2048 lds\n" \
M_"v[172:175], a[248:251], a[220:223], v[172:175]\n" \
X_"a[230:231], v33 offset:4608\n" \
M_"v[168:171], a[180:183], a[156:159], v[168:171]\n" \
"s_addk_i32 s84, 0x7c00\n" \
M_"v[168:171], a[244:247], a[220:223], v[168:171]\n" \
B_"v27, s[68:71], s84 offen offset:3072 lds\n" \
M_"v[164:167], a[176:179], a[156:159], v[164:167]\n" \
X_"a[172:173], v35 offset:0\n" \
M_"v[164:167], a[240:243], a[220:223], v[164:167]\n" \
R_"a[156:159], v28 offset:14336\n" \
M_"v[84:87], a[176:179], a[136:139], v[84:87]\n" \
R_"a[220:223], v29 offset:14336\n" \
M_"v[84:87], a[240:243], a[200:203], v[84:87]\n" \
"s_add_u32 s89, s42, 0x0\n" \
"buffer_store_dwordx4 v[192:195], v20, s[80:83], s89 offen offset:2048 sc1\n" \
M_"v[88:91], a[180:183], a[136:139], v[88:91]\n" \
X_"a[174:175], v35 offset:512\n" \
M_"v[88:91], a[244:247], a[200:203], v[88:91]\n" \
X_"a[236:237], v35 offset:4096\n" \
M_"v[92:95], a[184:187], a[136:139], v[92:95]\n" \
X_"a[238:239], v35 offset:4608\n" \
M_"v[92:95], a[248:251], a[200:203], v[92:95]\n" \
"s_add_u32 m0, s30, 0x20000\n" \
"s_movk_i32 s84, 0x400\n" \
M_"v[96:99], a[188:191], a[136:139], v[96:99]\n" \
B_"v24, s[76:79], s84 offen offset:0 lds\n" \
M_"v[96:99], a[252:255], a[200:203], v[96:99]\n" \
R_"a[136:139], v28 offset:4096\n" \
M_"v[112:115], a[188:191], a[140:143], v[112:115]\n" \
R_"a[200:203], v29 offset:4096\n" \
M_"v[112:115], a[252:255], a[204:207], v[112:115]\n" \
X_"a[168:169], v34 offset:0\n" \
M_"v[108:111], a[184:187], a[140:143], v[108:111]\n" \
"s_movk_i32 s84, 0x2400\n" \
M_"v[108:111], a[248:251], a[204:207], v[108:111]\n" \
B_"v25, s[76:79], s84 offen offset:1024 lds\n" \
M_"v[104:107], a[180:183], a[140:143], v[104:107]\n" \
X_"a[170:171], v34 offset:512\n" \
M_"v[104:107], a[244:247], a[204:207], v[104:107]\n" \
X_"a[232:233], v34 offset:4096\n" \
M_"v[100:103], a[176:179], a[140:143], v[100:103]\n" \
"s_add_u32 s8, s6, 0x1000000\n" \
"s_addc_u32 s6, s9, 0\n" \
M_"v[100:103], a[240:243], a[204:207], v[100:103]\n" \
R_"a[140:143], v28 offset:6144\n" \
"s_and_b32 s9, s6, 0xffff\n" \
M_"v[52:55], a[176:179], a[128:131], v[52:55]\n" \
R_"a[204:207], v29 offset:6144\n" \
"s_lshl_b32 s6, s5, 16\n" \
M_"v[52:55], a[240:243], a[192:195], v[52:55]\n" \
"s_movk_i32 s84, 0x4400\n" \
M_"v[56:59], a[180:183], a[128:131], v[56:59]\n" \
B_"v24, s[76:79], s84 offen offset:2048 lds\n" \
M_"v[56:59], a[244:247], a[192:195], v[56:59]\n" \
"s_movk_i32 s84, 0x6400\n" \
"s_nop 0\n" \
M_"v[60:63], a[184:187], a[128:131], v[60:63]\n" \
B_"v25, s[76:79], s84 offen offset:3072 lds\n" \
M_"v[60:63], a[248:251], a[192:195], v[60:63]\n" \
X_"a[234:235], v34 offset:4608\n" \
"s_sub_i32 s10, 0x100000, s6\n" \
M_"v[64:67], a[188:191], a[128:131], v[64:67]\n" \
"s_mov_b32 s6, 0x8000\n" \
M_"v[64:67], a[252:255], a[192:195], v[64:67]\n" \
R_"a[128:131], v28 offset:0\n" \
M_"v[80:83], a[188:191], a[132:135], v[80:83]\n" \
R_"a[192:195], v29 offset:0\n" \
M_"v[80:83], a[252:255], a[196:199], v[80:83]\n" \
"s_cmp_lg_u32 s5, 0\n" \
"s_cselect_b32 s17, s11, s6\n" \
M_"v[76:79], a[184:187], a[132:135], v[76:79]\n" \
"s_lshl_b32 s6, s3, 1\n" \
"s_lshl_b32 s5, s5, 3\n" \
M_"v[76:79], a[248:251], a[196:199], v[76:79]\n" \
"s_and_b32 s6, s6, 4\n" \
"s_or_b32 s12, s5, s6\n" \
M_"v[72:75], a[180:183], a[132:135], v[72:75]\n" \
"s_sub_i32 s5, s13, s5\n" \
"s_or_b32 s5, s5, s6\n" \
M_"v[72:75], a[244:247], a[196:199], v[72:75]\n" \
"s_or_b32 s6, s12, s13\n" \
"s_lshl_b32 s4, s4, 2\n" \
M_"v[68:71], a[176:179], a[132:135], v[68:71]\n" \
"s_mulk_i32 s6, 0x92\n" \
"s_add_i32 s4, s4, 18\n" \
M_"v[68:71], a[240:243], a[196:199], v[68:71]\n" \
R_"a[132:135], v28 offset:2048\n" \
R_"a[196:199], v29 offset:2048\n" \
"s_waitcnt lgkmcnt(15)\n" \
M_"a[0:3], a[160:163], a[144:147], a[0:3]\n" \
"s_add_u32 m0, s30, 0x21000\n" \
"s_addk_i32 s84, 0x3000\n" \
M_"a[0:3], a[224:227], a[208:211], a[0:3]\n" \
B_"v24, s[76:79], s84 offen offset:0 lds\n" \
M_"a[4:7], a[164:167], a[144:147], a[4:7]\n" \
X_"a[176:177], v36 offset:0\n" \
"s_mulk_i32 s5, 0x92\n" \
M_"a[4:7], a[228:231], a[208:211], a[4:7]\n" \
X_"a[178:179], v36 offset:1024\n" \
"s_add_i32 s6, s4, s6\n" \
"s_waitcnt lgkmcnt(15)\n" \
M_"a[12:15], a[172:175], a[144:147], a[12:15]\n" \
X_"a[240:241], v36 offset:8192\n" \
"s_add_i32 s5, s5, s4\n" \
"s_waitcnt lgkmcnt(15)\n" \
M_"a[12:15], a[236:239], a[208:211], a[12:15]\n" \
"s_add_u32 s88, s42, 0x0\n" \
"buffer_store_dwordx4 v[196:199], v20, s[80:83], s88 offen offset:3072 sc1\n" \
"s_waitcnt lgkmcnt(11)\n" \
M_"a[8:11], a[144:147], a[168:171], a[8:11]\n" \
X_"a[242:243], v36 offset:9216\n" \
"s_lshl_b32 s4, s3, 11\n" \
"s_waitcnt lgkmcnt(8)\n" \
M_"a[8:11], a[208:211], a[232:235], a[8:11]\n" \
X_"a[180:181], v37 offset:0\n" \
"s_lshl_b32 s15, s13, 12\n" \
M_"a[24:27], a[148:151], a[168:171], a[24:27]\n" \
X_"a[182:183], v37 offset:1024\n" \
"s_lshl_b32 s16, s19, 14\n" \
M_"a[24:27], a[212:215], a[232:235], a[24:27]\n" \
X_"a[244:245], v37 offset:8192\n" \
"s_mul_i32 s19, s19, 0xe000\n" \
M_"a[28:31], a[172:175], a[148:151], a[28:31]\n" \
"s_mov_b32 s11, 0x20000\n" \
"s_or_b32 s12, s4, 0xc000\n" \
M_"a[28:31], a[236:239], a[212:215], a[28:31]\n" \
"s_addk_i32 s84, 0x2000\n" \
M_"a[20:23], a[164:167], a[148:151], a[20:23]\n" \
B_"v25, s[76:79], s84 offen offset:1024 lds\n" \
M_"a[20:23], a[228:231], a[212:215], a[20:23]\n" \
X_"a[246:247], v37 offset:9216\n" \
"s_lshl_b32 s4, s3, 8\n" \
M_"a[16:19], a[160:163], a[148:151], a[16:19]\n" \
X_"a[184:185], v38 offset:0\n" \
"s_or_b32 s22, s15, s16\n" \
M_"a[16:19], a[224:227], a[212:215], a[16:19]\n" \
X_"a[186:187], v38 offset:1024\n" \
"s_or_b32 s23, s19, s15\n" \
M_"a[32:35], a[160:163], a[152:155], a[32:35]\n" \
"s_addk_i32 s84, 0x2000\n" \
M_"a[32:35], a[224:227], a[216:219], a[32:35]\n" \
B_"v24, s[76:79], s84 offen offset:2048 lds\n" \
M_"a[36:39], a[164:167], a[152:155], a[36:39]\n" \
X_"a[248:249], v38 offset:8192\n" \
"s_lshl_b32 s60, s6, 10\n" \
M_"a[36:39], a[228:231], a[216:219], a[36:39]\n" \
X_"a[250:251], v38 offset:9216\n" \
"s_lshl_b32 s61, s5, 10\n" \
M_"a[44:47], a[172:175], a[152:155], a[44:47]\n" \
X_"a[188:189], v39 offset:0\n" \
"s_lshl_b32 s14, s14, 15\n" \
"s_nop 0\n" \
M_"a[44:47], a[236:239], a[216:219], a[44:47]\n" \
X_"a[190:191], v39 offset:1024\n" \
"s_or_b32 s20, s4, 0x26c00\n" \
M_"a[40:43], a[152:155], a[168:171], a[40:43]\n" \
"s_add_u32 s89, s42, 0x2000\n" \
"buffer_store_dwordx4 v[200:203], v20, s[80:83], s89 offen offset:0 sc1\n" \
M_"a[40:43], a[216:219], a[232:235], a[40:43]\n" \
X_"a[252:253], v39 offset:8192\n" \
"s_or_b32 s13, s22, 0x8000\n" \
M_"a[56:59], a[156:159], a[168:171], a[56:59]\n" \
X_"a[254:255], v39 offset:9216\n" \
"s_add_i32 s22, s22, 0x14000\n" \
".fill (L_pb + 73440 - .) / 4, 4, 0xBF800000\n" \
"L_pp18:\n" \
"s_cbranch_execz L_pret\n" \
M_"a[56:59], a[220:223], a[232:235], a[56:59]\n" \
"s_bitset1_b32 s23, 16\n" \
"s_or_b32 s24, s7, 0x5be8020\n" \
M_"a[60:63], a[172:175], a[156:159], a[60:63]\n" \
"s_and_b32 s57, s27, 0xffff\n" \
"s_mov_b32 s58, 0x6000000\n" \
M_"a[60:63], a[236:239], a[220:223], a[60:63]\n" \
"s_addk_i32 s84, 0x2000\n" \
M_"a[52:55], a[164:167], a[156:159], a[52:55]\n" \
B_"v25, s[76:79], s84 offen offset:3072 lds\n" \
M_"a[52:55], a[228:231], a[220:223], a[52:55]\n" \
"s_mov_b32 s56, s26\n" \
"s_mov_b32 s59, s11\n" \
M_"a[48:51], a[160:163], a[156:159], a[48:51]\n" \
"s_add_i32 s60, s60, 0x3000000\n" \
"s_add_i32 s61, s61, 0x411c000\n" \
M_"a[48:51], a[224:227], a[220:223], a[48:51]\n" \
"s_sub_i32 s19, 14, s18\n" \
"s_mov_b32 s21, 0x71800000\n" \
M_"a[64:67], a[164:167], a[140:143], a[64:67]\n" \
"s_mov_b32 s4, L_gsw-L_gtr\n" \
M_"a[64:67], a[228:231], a[204:207], a[64:67]\n" \
"s_cmp_eq_u32 s19, 0\n" \
"s_cselect_b32 s4, L_gs7-L_gtr, s4\n" \
M_"a[80:83], a[172:175], a[140:143], a[80:83]\n" \
"s_cmp_eq_u32 s16, 0\n" \
"s_cselect_b32 s4, 0, s4\n" \
M_"a[80:83], a[236:239], a[204:207], a[80:83]\n" \
"s_getpc_b64 s[28:29]\n" \
"L_gp1_%=:\n" \
"s_add_u32 s28, s28, L_gtr-L_gp1_%=\n" \
"s_addc_u32 s29, s29, 0\n" \
M_"a[68:71], a[140:143], a[168:171], a[68:71]\n" \
"s_add_u32 s28, s28, s4\n" \
"s_addc_u32 s29, s29, 0\n" \
"s_waitcnt vmcnt(11) lgkmcnt(0)\n" \
M_"a[68:71], a[204:207], a[232:235], a[68:71]\n" \
"s_barrier\n" \
M_"v[116:119], a[176:179], a[144:147], v[116:119]\n" \
"s_add_u32 m0, s40, 0x0\n" \
"s_add_u32 s85, s39, 0x10000\n" \
M_"v[116:119], a[240:243], a[208:211], v[116:119]\n" \
B_"v20, s[80:83], s85 offen sc1 lds\n" \
M_"v[120:123], a[180:183], a[144:147], v[120:123]\n" \
X_"a[160:161], v32 offset:32768\n" \
"v_accvgpr_write_b32 a96, 0\n" \
M_"v[120:123], a[244:247], a[208:211], v[120:123]\n" \
X_"a[162:163], v32 offset:33280\n" \
"v_accvgpr_write_b32 a97, 0\n" \
M_"v[124:127], a[184:187], a[144:147], v[124:127]\n" \
"v_accvgpr_write_b32 a98, 0\n" \
"v_accvgpr_write_b32 a99, 0\n" \
M_"v[124:127], a[248:251], a[208:211], v[124:127]\n" \
"v_accvgpr_write_b32 a100, 0\n" \
"v_accvgpr_write_b32 a101, 0\n" \
M_"v[128:131], a[188:191], a[144:147], v[128:131]\n" \
X_"a[224:225], v32 offset:36864\n" \
"v_accvgpr_write_b32 a102, 0\n" \
M_"v[128:131], a[252:255], a[208:211], v[128:131]\n" \
R_"a[144:147], v28 offset:40960\n" \
"v_accvgpr_write_b32 a103, 0\n" \
M_"v[144:147], a[188:191], a[148:151], v[144:147]\n" \
R_"a[208:211], v29 offset:40960\n" \
"v_accvgpr_write_b32 a112, 0\n" \
M_"v[144:147], a[252:255], a[212:215], v[144:147]\n" \
"s_add_u32 m0, s40, 0x1000\n" \
"s_add_u32 s85, s39, 0x11000\n" \
M_"v[140:143], a[184:187], a[148:151], v[140:143]\n" \
B_"v20, s[80:83], s85 offen sc1 lds\n" \
M_"v[140:143], a[248:251], a[212:215], v[140:143]\n" \
X_"a[226:227], v32 offset:37376\n" \
"v_accvgpr_write_b32 a113, 0\n" \
M_"v[136:139], a[180:183], a[148:151], v[136:139]\n" \
"v_accvgpr_write_b32 a114, 0\n" \
"v_accvgpr_write_b32 a115, 0\n" \
M_"v[136:139], a[244:247], a[212:215], v[136:139]\n" \
X_"a[164:165], v33 offset:32768\n" \
"v_accvgpr_write_b32 a120, 0\n" \
M_"v[132:135], a[176:179], a[148:151], v[132:135]\n" \
"v_accvgpr_write_b32 a121, 0\n" \
"v_accvgpr_write_b32 a122, 0\n" \
M_"v[132:135], a[240:243], a[212:215], v[132:135]\n" \
R_"a[148:151], v28 offset:43008\n" \
"v_accvgpr_write_b32 a123, 0\n" \
M_"v[148:151], a[176:179], a[152:155], v[148:151]\n" \
R_"a[212:215], v29 offset:43008\n" \
"v_mov_b32_e32 v180, 0x3f803f80\n" \
M_"v[148:151], a[240:243], a[216:219], v[148:151]\n" \
"s_add_u32 m0, s40, 0x3000\n" \
"s_movk_i32 s84, 0x400\n" \
M_"v[152:155], a[180:183], a[152:155], v[152:155]\n" \
B_"v21, s[76:79], s84 offen offset:0 lds\n" \
M_"v[152:155], a[244:247], a[216:219], v[152:155]\n" \
X_"a[166:167], v33 offset:33280\n" \
"s_mov_b32 s6, 0x33333333\n" \
M_"v[156:159], a[184:187], a[152:155], v[156:159]\n" \
X_"a[228:229], v33 offset:36864\n" \
"s_mov_b32 s7, 0xcccccccc\n" \
M_"v[156:159], a[248:251], a[216:219], v[156:159]\n" \
"s_add_u32 m0, s31, 0x0\n" \
"s_mov_b32 s84, 0x400000\n" \
M_"v[160:163], a[188:191], a[152:155], v[160:163]\n" \
B_"v26, s[68:71], s84 offen offset:0 lds\n" \
M_"v[160:163], a[252:255], a[216:219], v[160:163]\n" \
R_"a[152:155], v28 offset:45056\n" \
"v_cndmask_b32_e64 v181, 0, v180, s[6:7]\n" \
M_"v[176:179], a[188:191], a[156:159], v[176:179]\n" \
R_"a[216:219], v29 offset:45056\n" \
"v_accvgpr_write_b32 a104, v181\n" \
M_"v[176:179], a[252:255], a[220:223], v[176:179]\n" \
X_"a[230:231], v33 offset:37376\n" \
"v_accvgpr_write_b32 a105, v181\n" \
M_"v[172:175], a[184:187], a[156:159], v[172:175]\n" \
X_"a[172:173], v35 offset:32768\n" \
"v_accvgpr_write_b32 a106, v181\n" \
M_"v[172:175], a[248:251], a[220:223], v[172:175]\n" \
"v_accvgpr_write_b32 a107, v181\n" \
"s_mov_b32 s6, 0x11111111\n" \
M_"v[168:171], a[180:183], a[156:159], v[168:171]\n" \
"s_add_u32 s88, s42, 0x2000\n" \
"buffer_store_dwordx4 v[204:207], v20, s[80:83], s88 offen offset:1024 sc1\n" \
M_"v[168:171], a[244:247], a[220:223], v[168:171]\n" \
X_"a[174:175], v35 offset:33280\n" \
"s_mov_b32 s7, 0x11111111\n" \
M_"v[164:167], a[176:179], a[156:159], v[164:167]\n" \
"v_cndmask_b32_e64 v181, 0, v180, s[6:7]\n" \
"v_accvgpr_write_b32 a108, v181\n" \
M_"v[164:167], a[240:243], a[220:223], v[164:167]\n" \
R_"a[156:159], v28 offset:47104\n" \
"v_accvgpr_write_b32 a109, v181\n" \
M_"v[84:87], a[176:179], a[136:139], v[84:87]\n" \
R_"a[220:223], v29 offset:47104\n" \
"v_accvgpr_write_b32 a110, v181\n" \
M_"v[84:87], a[240:243], a[200:203], v[84:87]\n" \
X_"a[236:237], v35 offset:36864\n" \
"v_accvgpr_write_b32 a111, v181\n" \
M_"v[88:91], a[180:183], a[136:139], v[88:91]\n" \
"s_mov_b32 s6, 0x44444444\n" \
"s_mov_b32 s7, 0x44444444\n" \
M_"v[88:91], a[244:247], a[200:203], v[88:91]\n" \
"s_addk_i32 s84, 0x7c00\n" \
"s_nop 0\n" \
M_"v[92:95], a[184:187], a[136:139], v[92:95]\n" \
B_"v26, s[68:71], s84 offen offset:1024 lds\n" \
M_"v[92:95], a[248:251], a[200:203], v[92:95]\n" \
X_"a[238:239], v35 offset:37376\n" \
"v_cndmask_b32_e64 v181, 0, v180, s[6:7]\n" \
M_"v[96:99], a[188:191], a[136:139], v[96:99]\n" \
"v_accvgpr_write_b32 a116, v181\n" \
"v_accvgpr_write_b32 a117, v181\n" \
M_"v[96:99], a[252:255], a[200:203], v[96:99]\n" \
R_"a[136:139], v28 offset:36864\n" \
"v_accvgpr_write_b32 a118, v181\n" \
M_"v[112:115], a[188:191], a[140:143], v[112:115]\n" \
R_"a[200:203], v29 offset:36864\n" \
"v_accvgpr_write_b32 a119, v181\n" \
M_"v[112:115], a[252:255], a[204:207], v[112:115]\n" \
X_"a[168:169], v34 offset:32768\n" \
"v_bfe_i32 v181, v20, 4, 1\n" \
M_"v[108:111], a[184:187], a[140:143], v[108:111]\n" \
"s_addk_i32 s84, 0x7c00\n" \
"s_nop 0\n" \
M_"v[108:111], a[248:251], a[204:207], v[108:111]\n" \
B_"v27, s[68:71], s84 offen offset:2048 lds\n" \
M_"v[104:107], a[180:183], a[140:143], v[104:107]\n" \
X_"a[170:171], v34 offset:33280\n" \
"v_and_b32_e32 v181, 0xaaaaaaaa, v181\n" \
M_"v[104:107], a[244:247], a[204:207], v[104:107]\n" \
X_"a[232:233], v34 offset:36864\n" \
"v_or_b32_e32 v181, 0x44444444, v181\n" \
M_"v[100:103], a[176:179], a[140:143], v[100:103]\n" \
"v_accvgpr_write_b32 a126, v181\n" \
"v_bfe_u32 v180, v20, 5, 3\n" \
M_"v[100:103], a[240:243], a[204:207], v[100:103]\n" \
R_"a[140:143], v28 offset:38912\n" \
"v_bfe_u32 v181, v20, 8, 2\n" \
M_"v[52:55], a[176:179], a[128:131], v[52:55]\n" \
R_"a[204:207], v29 offset:38912\n" \
"v_lshlrev_b32_e64 v181, 1, v181\n" \
M_"v[52:55], a[240:243], a[192:195], v[52:55]\n" \
"s_addk_i32 s84, 0x7c00\n" \
M_"v[56:59], a[180:183], a[128:131], v[56:59]\n" \
B_"v27, s[68:71], s84 offen offset:3072 lds\n" \
M_"v[56:59], a[244:247], a[192:195], v[56:59]\n" \
X_"a[234:235], v34 offset:37376\n" \
"v_sub_u32_e32 v180, v180, v181\n" \
M_"v[60:63], a[184:187], a[128:131], v[60:63]\n" \
"v_and_b32_e64 v181, 16, v20\n" \
"v_mov_b32_e32 v182, 0x3f80\n" \
M_"v[60:63], a[248:251], a[192:195], v[60:63]\n" \
"v_lshlrev_b32_e32 v181, v181, v182\n" \
"v_cmp_eq_u32_e32 vcc, 0, v180\n" \
M_"v[64:67], a[188:191], a[128:131], v[64:67]\n" \
"v_cmp_eq_u32_e64 s[6:7], 1, v180\n" \
"v_cndmask_b32_e64 v182, 0, v181, vcc\n" \
M_"v[64:67], a[252:255], a[192:195], v[64:67]\n" \
R_"a[128:131], v28 offset:32768\n" \
"v_cndmask_b32_e64 v183, 0, v181, s[6:7]\n" \
M_"v[80:83], a[188:191], a[132:135], v[80:83]\n" \
R_"a[192:195], v29 offset:32768\n" \
"v_accvgpr_write_b32 a124, v182\n" \
M_"v[80:83], a[252:255], a[196:199], v[80:83]\n" \
"v_accvgpr_write_b32 a125, v183\n" \
M_"v[76:79], a[184:187], a[132:135], v[76:79]\n" \
M_"v[76:79], a[248:251], a[196:199], v[76:79]\n" \
M_"v[72:75], a[180:183], a[132:135], v[72:75]\n" \
M_"v[72:75], a[244:247], a[196:199], v[72:75]\n" \
M_"v[68:71], a[176:179], a[132:135], v[68:71]\n" \
M_"v[68:71], a[240:243], a[196:199], v[68:71]\n" \
R_"a[132:135], v28 offset:34816\n" \
R_"a[196:199], v29 offset:34816\n" \
"s_waitcnt lgkmcnt(15)\n" \
"s_nop 0\n" \
M_"a[0:3], a[160:163], a[144:147], a[0:3]\n" \
"s_add_u32 s89, s42, 0x2000\n" \
"buffer_store_dwordx4 v[208:211], v20, s[80:83], s89 offen offset:2048 sc1\n" \
M_"a[0:3], a[224:227], a[208:211], a[0:3]\n" \
X_"a[176:177], v36 offset:32768\n" \
M_"a[4:7], a[164:167], a[144:147], a[4:7]\n" \
X_"a[178:179], v36 offset:33792\n" \
M_"a[4:7], a[228:231], a[208:211], a[4:7]\n" \
X_"a[240:241], v36 offset:40960\n" \
M_"a[12:15], a[172:175], a[144:147], a[12:15]\n" \
X_"a[242:243], v36 offset:41984\n" \
"s_waitcnt lgkmcnt(15)\n" \
M_"a[12:15], a[236:239], a[208:211], a[12:15]\n" \
"s_waitcnt lgkmcnt(12)\n" \
M_"a[8:11], a[144:147], a[168:171], a[8:11]\n" \
X_"a[180:181], v37 offset:32768\n" \
"s_waitcnt lgkmcnt(9)\n" \
"s_nop 0\n" \
M_"a[8:11], a[208:211], a[232:235], a[8:11]\n" \
M_"a[24:27], a[148:151], a[168:171], a[24:27]\n" \
X_"a[182:183], v37 offset:33792\n" \
M_"a[24:27], a[212:215], a[232:235], a[24:27]\n" \
X_"a[244:245], v37 offset:40960\n" \
M_"a[28:31], a[172:175], a[148:151], a[28:31]\n" \
M_"a[28:31], a[236:239], a[212:215], a[28:31]\n" \
X_"a[246:247], v37 offset:41984\n" \
M_"a[20:23], a[164:167], a[148:151], a[20:23]\n" \
M_"a[20:23], a[228:231], a[212:215], a[20:23]\n" \
X_"a[184:185], v38 offset:32768\n" \
M_"a[16:19], a[160:163], a[148:151], a[16:19]\n" \
X_"a[186:187], v38 offset:33792\n" \
M_"a[16:19], a[224:227], a[212:215], a[16:19]\n" \
M_"a[32:35], a[160:163], a[152:155], a[32:35]\n" \
"s_add_u32 s88, s42, 0x2000\n" \
"buffer_store_dwordx4 v[212:215], v20, s[80:83], s88 offen offset:3072 sc1\n" \
M_"a[32:35], a[224:227], a[216:219], a[32:35]\n" \
X_"a[248:249], v38 offset:40960\n" \
M_"a[36:39], a[164:167], a[152:155], a[36:39]\n" \
"v_lshlrev_b32_e32 v45, 3, v20\n" \
"v_add_u32_e32 v45, s30, v45\n" \
M_"a[36:39], a[228:231], a[216:219], a[36:39]\n" \
"s_getpc_b64 s[86:87]\n" \
"L_ct1_%=:\n" \
"s_add_u32 s86, s86, L_ap0-L_ct1_%=\n" \
"s_addc_u32 s87, s87, 0\n" \
"s_add_u32 m0, s40, 0x2000\n" \
M_"a[44:47], a[172:175], a[152:155], a[44:47]\n" \
"global_load_lds_dword v45, s[86:87]\n" \
M_"a[44:47], a[236:239], a[216:219], a[44:47]\n" \
X_"a[250:251], v38 offset:41984\n" \
M_"a[40:43], a[152:155], a[168:171], a[40:43]\n" \
X_"a[188:189], v39 offset:32768\n" \
M_"a[40:43], a[216:219], a[232:235], a[40:43]\n" \
X_"a[190:191], v39 offset:33792\n" \
M_"a[56:59], a[156:159], a[168:171], a[56:59]\n" \
X_"a[252:253], v39 offset:40960\n" \
M_"a[56:59], a[220:223], a[232:235], a[56:59]\n" \
X_"a[254:255], v39 offset:41984\n" \
M_"a[60:63], a[172:175], a[156:159], a[60:63]\n" \
M_"a[60:63], a[236:239], a[220:223], a[60:63]\n" \
M_"a[52:55], a[164:167], a[156:159], a[52:55]\n" \
M_"a[52:55], a[228:231], a[220:223], a[52:55]\n" \
M_"a[48:51], a[160:163], a[156:159], a[48:51]\n" \
M_"a[48:51], a[224:227], a[220:223], a[48:51]\n" \
M_"a[64:67], a[164:167], a[140:143], a[64:67]\n" \
M_"a[64:67], a[228:231], a[204:207], a[64:67]\n" \
M_"a[80:83], a[172:175], a[140:143], a[80:83]\n" \
M_"a[80:83], a[236:239], a[204:207], a[80:83]\n" \
M_"a[68:71], a[140:143], a[168:171], a[68:71]\n" \
"s_waitcnt vmcnt(3) lgkmcnt(0)\n" \
M_"a[68:71], a[204:207], a[232:235], a[68:71]\n" \
"s_barrier\n" \
"v_xor_b32_e32 v28, 0x20000, v28\n" \
"v_xor_b32_e32 v29, 0x20000, v29\n" \
M_"v[116:119], a[176:179], a[144:147], v[116:119]\n" \
X_"a[160:161], v32 offset:0\n" \
"v_accvgpr_read_b32 v184, a0\n" \
M_"v[116:119], a[240:243], a[208:211], v[116:119]\n" \
"v_accvgpr_read_b32 v185, a1\n" \
"v_accvgpr_read_b32 v186, a2\n" \
M_"v[120:123], a[180:183], a[144:147], v[120:123]\n" \
"v_accvgpr_read_b32 v187, a3\n" \
"v_accvgpr_read_b32 v188, a4\n" \
M_"v[120:123], a[244:247], a[208:211], v[120:123]\n" \
X_"a[162:163], v32 offset:512\n" \
"v_accvgpr_read_b32 v189, a5\n" \
M_"v[124:127], a[184:187], a[144:147], v[124:127]\n" \
"v_accvgpr_read_b32 v190, a6\n" \
"v_accvgpr_read_b32 v191, a7\n" \
M_"v[124:127], a[248:251], a[208:211], v[124:127]\n" \
"v_accvgpr_read_b32 v196, a12\n" \
"v_accvgpr_read_b32 v197, a13\n" \
M_"v[128:131], a[188:191], a[144:147], v[128:131]\n" \
X_"a[224:225], v32 offset:4096\n" \
"v_accvgpr_read_b32 v198, a14\n" \
M_"v[128:131], a[252:255], a[208:211], v[128:131]\n" \
R_"a[144:147], v28 offset:8192\n" \
"v_accvgpr_read_b32 v199, a15\n" \
M_"v[144:147], a[188:191], a[148:151], v[144:147]\n" \
R_"a[208:211], v29 offset:8192\n" \
"v_accvgpr_read_b32 v192, a8\n" \
M_"v[144:147], a[252:255], a[212:215], v[144:147]\n" \
X_"a[226:227], v32 offset:4608\n" \
"v_accvgpr_read_b32 v193, a9\n" \
M_"v[140:143], a[184:187], a[148:151], v[140:143]\n" \
"v_accvgpr_read_b32 v194, a10\n" \
"v_accvgpr_read_b32 v195, a11\n" \
M_"v[140:143], a[248:251], a[212:215], v[140:143]\n" \
"v_accvgpr_read_b32 v208, a24\n" \
"v_accvgpr_read_b32 v209, a25\n" \
M_"v[136:139], a[180:183], a[148:151], v[136:139]\n" \
"v_accvgpr_read_b32 v210, a26\n" \
"v_accvgpr_read_b32 v211, a27\n" \
M_"v[136:139], a[244:247], a[212:215], v[136:139]\n" \
X_"a[164:165], v33 offset:0\n" \
"v_accvgpr_read_b32 v212, a28\n" \
M_"v[132:135], a[176:179], a[148:151], v[132:135]\n" \
"v_accvgpr_read_b32 v213, a29\n" \
"v_accvgpr_read_b32 v214, a30\n" \
M_"v[132:135], a[240:243], a[212:215], v[132:135]\n" \
R_"a[148:151], v28 offset:10240\n" \
"v_accvgpr_read_b32 v215, a31\n" \
M_"v[148:151], a[176:179], a[152:155], v[148:151]\n" \
R_"a[212:215], v29 offset:10240\n" \
"v_accvgpr_read_b32 v204, a20\n" \
M_"v[148:151], a[240:243], a[216:219], v[148:151]\n" \
X_"a[166:167], v33 offset:512\n" \
"v_accvgpr_read_b32 v205, a21\n" \
M_"v[152:155], a[180:183], a[152:155], v[152:155]\n" \
"v_accvgpr_read_b32 v206, a22\n" \
"v_accvgpr_read_b32 v207, a23\n" \
M_"v[152:155], a[244:247], a[216:219], v[152:155]\n" \
X_"a[228:229], v33 offset:4096\n" \
"v_accvgpr_read_b32 v200, a16\n" \
M_"v[156:159], a[184:187], a[152:155], v[156:159]\n" \
"v_accvgpr_read_b32 v201, a17\n" \
"v_accvgpr_read_b32 v202, a18\n" \
M_"v[156:159], a[248:251], a[216:219], v[156:159]\n" \
"v_accvgpr_read_b32 v203, a19\n" \
"v_accvgpr_read_b32 v216, a32\n" \
M_"v[160:163], a[188:191], a[152:155], v[160:163]\n" \
X_"a[230:231], v33 offset:4608\n" \
"v_accvgpr_read_b32 v217, a33\n" \
M_"v[160:163], a[252:255], a[216:219], v[160:163]\n" \
R_"a[152:155], v28 offset:12288\n" \
"v_accvgpr_read_b32 v218, a34\n" \
M_"v[176:179], a[188:191], a[156:159], v[176:179]\n" \
R_"a[216:219], v29 offset:12288\n" \
"v_accvgpr_read_b32 v219, a35\n" \
M_"v[176:179], a[252:255], a[220:223], v[176:179]\n" \
"v_accvgpr_read_b32 v220, a36\n" \
"v_accvgpr_read_b32 v221, a37\n" \
M_"v[172:175], a[184:187], a[156:159], v[172:175]\n" \
X_"a[172:173], v35 offset:0\n" \
"v_accvgpr_read_b32 v222, a38\n" \
M_"v[172:175], a[248:251], a[220:223], v[172:175]\n" \
"v_accvgpr_read_b32 v223, a39\n" \
"v_accvgpr_read_b32 v228, a44\n" \
M_"v[168:171], a[180:183], a[156:159], v[168:171]\n" \
"v_accvgpr_read_b32 v229, a45\n" \
"v_accvgpr_read_b32 v230, a46\n" \
M_"v[168:171], a[244:247], a[220:223], v[168:171]\n" \
X_"a[174:175], v35 offset:512\n" \
"v_accvgpr_read_b32 v231, a47\n" \
M_"v[164:167], a[176:179], a[156:159], v[164:167]\n" \
"v_accvgpr_read_b32 v224, a40\n" \
"v_accvgpr_read_b32 v225, a41\n" \
M_"v[164:167], a[240:243], a[220:223], v[164:167]\n" \
R_"a[156:159], v28 offset:14336\n" \
"v_accvgpr_read_b32 v226, a42\n" \
M_"v[84:87], a[176:179], a[136:139], v[84:87]\n" \
R_"a[220:223], v29 offset:14336\n" \
"v_accvgpr_read_b32 v227, a43\n" \
M_"v[84:87], a[240:243], a[200:203], v[84:87]\n" \
X_"a[236:237], v35 offset:4096\n" \
"v_accvgpr_read_b32 v240, a56\n" \
M_"v[88:91], a[180:183], a[136:139], v[88:91]\n" \
"v_accvgpr_read_b32 v241, a57\n" \
"v_accvgpr_read_b32 v242, a58\n" \
M_"v[88:91], a[244:247], a[200:203], v[88:91]\n" \
X_"a[238:239], v35 offset:4608\n" \
"v_accvgpr_read_b32 v243, a59\n" \
M_"v[92:95], a[184:187], a[136:139], v[92:95]\n" \
"v_accvgpr_read_b32 v244, a60\n" \
"v_accvgpr_read_b32 v245, a61\n" \
M_"v[92:95], a[248:251], a[200:203], v[92:95]\n" \
"v_accvgpr_read_b32 v246, a62\n" \
"v_accvgpr_read_b32 v247, a63\n" \
M_"v[96:99], a[188:191], a[136:139], v[96:99]\n" \
"v_accvgpr_read_b32 v236, a52\n" \
"v_accvgpr_read_b32 v237, a53\n" \
M_"v[96:99], a[252:255], a[200:203], v[96:99]\n" \
R_"a[136:139], v28 offset:4096\n" \
"v_accvgpr_read_b32 v238, a54\n" \
M_"v[112:115], a[188:191], a[140:143], v[112:115]\n" \
R_"a[200:203], v29 offset:4096\n" \
"v_accvgpr_read_b32 v239, a55\n" \
M_"v[112:115], a[252:255], a[204:207], v[112:115]\n" \
X_"a[168:169], v34 offset:0\n" \
"v_accvgpr_read_b32 v232, a48\n" \
M_"v[108:111], a[184:187], a[140:143], v[108:111]\n" \
X_"a[170:171], v34 offset:512\n" \
"v_accvgpr_read_b32 v233, a49\n" \
M_"v[108:111], a[248:251], a[204:207], v[108:111]\n" \
"v_accvgpr_read_b32 v234, a50\n" \
"v_accvgpr_read_b32 v235, a51\n" \
M_"v[104:107], a[180:183], a[140:143], v[104:107]\n" \
"v_accvgpr_read_b32 v180, a64\n" \
"v_accvgpr_read_b32 v181, a65\n" \
M_"v[104:107], a[244:247], a[204:207], v[104:107]\n" \
X_"a[232:233], v34 offset:4096\n" \
"v_accvgpr_read_b32 v182, a66\n" \
M_"v[100:103], a[176:179], a[140:143], v[100:103]\n" \
"v_accvgpr_read_b32 v183, a67\n" \
"v_accvgpr_read_b32 v48, a80\n" \
M_"v[100:103], a[240:243], a[204:207], v[100:103]\n" \
R_"a[140:143], v28 offset:6144\n" \
"v_accvgpr_read_b32 v49, a81\n" \
M_"v[52:55], a[176:179], a[128:131], v[52:55]\n" \
R_"a[204:207], v29 offset:6144\n" \
"v_accvgpr_read_b32 v50, a82\n" \
M_"v[52:55], a[240:243], a[192:195], v[52:55]\n" \
X_"a[234:235], v34 offset:4608\n" \
"v_accvgpr_read_b32 v51, a83\n" \
M_"v[56:59], a[180:183], a[128:131], v[56:59]\n" \
"v_accvgpr_read_b32 v252, a68\n" \
"v_accvgpr_read_b32 v253, a69\n" \
M_"v[56:59], a[244:247], a[192:195], v[56:59]\n" \
"v_accvgpr_read_b32 v254, a70\n" \
"v_accvgpr_read_b32 v255, a71\n" \
M_"v[60:63], a[184:187], a[128:131], v[60:63]\n" \
"v_lshrrev_b32_e64 v24, 9, v20\n" \
"v_and_b32_e32 v26, 0x1ff, v20\n" \
M_"v[60:63], a[248:251], a[192:195], v[60:63]\n" \
"v_lshl_add_u32 v24, v24, 3, v26\n" \
"v_lshrrev_b32_e64 v25, 8, v20\n" \
M_"v[64:67], a[188:191], a[128:131], v[64:67]\n" \
"v_mul_u32_u24_e32 v25, 0x24700, v25\n" \
"v_add_u32_e64 v25, v25, v20\n" \
M_"v[64:67], a[252:255], a[192:195], v[64:67]\n" \
R_"a[128:131], v28 offset:0\n" \
"v_lshrrev_b32_e64 v26, 2, v20\n" \
M_"v[80:83], a[188:191], a[132:135], v[80:83]\n" \
R_"a[192:195], v29 offset:0\n" \
"v_add_u32_e64 v26, s45, v26\n" \
M_"v[80:83], a[252:255], a[196:199], v[80:83]\n" \
"v_add_u32_e32 v27, s46, v20\n" \
"v_add_u32_e32 v21, s43, v27\n" \
M_"v[76:79], a[184:187], a[132:135], v[76:79]\n" \
M_"v[76:79], a[248:251], a[196:199], v[76:79]\n" \
M_"v[72:75], a[180:183], a[132:135], v[72:75]\n" \
M_"v[72:75], a[244:247], a[196:199], v[72:75]\n" \
M_"v[68:71], a[176:179], a[132:135], v[68:71]\n" \
M_"v[68:71], a[240:243], a[196:199], v[68:71]\n" \
R_"a[132:135], v28 offset:2048\n" \
R_"a[196:199], v29 offset:2048\n" \
"s_waitcnt lgkmcnt(15)\n" \
"s_nop 0\n" \
M_"v[184:187], a[160:163], a[144:147], v[184:187]\n" \
"v_bfe_u32 v44, v20, 2, 5\n" \
X_"a[176:177], v36 offset:0\n" \
M_"v[184:187], a[224:227], a[208:211], v[184:187]\n" \
"buffer_load_dword v44, v44, s[80:83], s38 offen offset:0 sc1\n" \
M_"v[188:191], a[164:167], a[144:147], v[188:191]\n" \
X_"a[178:179], v36 offset:1024\n" \
M_"v[188:191], a[228:231], a[208:211], v[188:191]\n" \
X_"a[240:241], v36 offset:8192\n" \
M_"v[196:199], a[172:175], a[144:147], v[196:199]\n" \
X_"a[242:243], v36 offset:9216\n" \
"s_waitcnt lgkmcnt(15)\n" \
M_"v[196:199], a[236:239], a[208:211], v[196:199]\n" \
"s_waitcnt lgkmcnt(12)\n" \
M_"v[192:195], a[144:147], a[168:171], v[192:195]\n" \
X_"a[180:181], v37 offset:0\n" \
"s_waitcnt lgkmcnt(9)\n" \
"s_nop 0\n" \
M_"v[192:195], a[208:211], a[232:235], v[192:195]\n" \
M_"v[208:211], a[148:151], a[168:171], v[208:211]\n" \
X_"a[182:183], v37 offset:1024\n" \
M_"v[208:211], a[212:215], a[232:235], v[208:211]\n" \
X_"a[244:245], v37 offset:8192\n" \
M_"v[212:215], a[172:175], a[148:151], v[212:215]\n" \
M_"v[212:215], a[236:239], a[212:215], v[212:215]\n" \
X_"a[246:247], v37 offset:9216\n" \
C_"v188, v188, v189\n" \
M_"v[204:207], a[164:167], a[148:151], v[204:207]\n" \
C_"v189, v190, v191\n" \
C_"v190, v198, v199\n" \
M_"v[204:207], a[228:231], a[212:215], v[204:207]\n" \
X_"a[184:185], v38 offset:0\n" \
"v_mov_b32_e32 v191, 0x3f80\n" \
M_"v[200:203], a[160:163], a[148:151], v[200:203]\n" \
X_"a[186:187], v38 offset:1024\n" \
C_"v184, v184, v185\n" \
M_"v[200:203], a[224:227], a[212:215], v[200:203]\n" \
C_"v185, v186, v187\n" \
C_"v186, v196, v197\n" \
M_"v[216:219], a[160:163], a[152:155], v[216:219]\n" \
X_"a[248:249], v38 offset:8192\n" \
"v_mov_b32_e64 v187, 0\n" \
M_"v[216:219], a[224:227], a[216:219], v[216:219]\n" \
X_"a[250:251], v38 offset:9216\n" \
M_"v[220:223], a[164:167], a[152:155], v[220:223]\n" \
M_"v[220:223], a[228:231], a[216:219], v[220:223]\n" \
X_"a[188:189], v39 offset:0\n" \
C_"v204, v204, v205\n" \
M_"v[228:231], a[172:175], a[152:155], v[228:231]\n" \
C_"v205, v206, v207\n" \
C_"v206, v214, v215\n" \
M_"v[228:231], a[236:239], a[216:219], v[228:231]\n" \
X_"a[190:191], v39 offset:1024\n" \
"v_mov_b32_e32 v207, 0x3f80\n" \
M_"v[224:227], a[152:155], a[168:171], v[224:227]\n" \
X_"a[252:253], v39 offset:8192\n" \
C_"v200, v200, v201\n" \
M_"v[224:227], a[216:219], a[232:235], v[224:227]\n" \
C_"v201, v202, v203\n" \
C_"v202, v212, v213\n" \
M_"v[240:243], a[156:159], a[168:171], v[240:243]\n" \
X_"a[254:255], v39 offset:9216\n" \
"v_mov_b32_e64 v203, 0\n" \
M_"v[240:243], a[220:223], a[232:235], v[240:243]\n" \
M_"v[244:247], a[172:175], a[156:159], v[244:247]\n" \
".fill (L_pb + 77536 - .) / 4, 4, 0xBF800000\n" \
"L_pp19:\n" \
"s_cbranch_execz L_pret\n" \
"s_nop 0\n" \
M_"v[244:247], a[236:239], a[220:223], v[244:247]\n" \
C_"v220, v220, v221\n" \
C_"v221, v222, v223\n" \
M_"v[236:239], a[164:167], a[156:159], v[236:239]\n" \
C_"v222, v230, v231\n" \
"v_mov_b32_e32 v223, 0x3f80\n" \
M_"v[236:239], a[228:231], a[220:223], v[236:239]\n" \
C_"v216, v216, v217\n" \
C_"v217, v218, v219\n" \
M_"v[232:235], a[160:163], a[156:159], v[232:235]\n" \
C_"v218, v228, v229\n" \
"v_mov_b32_e64 v219, 0\n" \
M_"v[232:235], a[224:227], a[220:223], v[232:235]\n" \
M_"v[180:183], a[164:167], a[140:143], v[180:183]\n" \
M_"v[180:183], a[228:231], a[204:207], v[180:183]\n" \
M_"v[48:51], a[172:175], a[140:143], v[48:51]\n" \
M_"v[48:51], a[236:239], a[204:207], v[48:51]\n" \
C_"v236, v236, v237\n" \
C_"v237, v238, v239\n" \
M_"v[252:255], a[140:143], a[168:171], v[252:255]\n" \
C_"v238, v246, v247\n" \
"v_mov_b32_e32 v239, 0x3f80\n" \
M_"v[252:255], a[204:207], a[232:235], v[252:255]\n" \
C_"v232, v232, v233\n" \
C_"v233, v234, v235\n" \
"s_waitcnt vmcnt(0) lgkmcnt(0)\n" \
"s_barrier\n" \
"v_mov_b32_e32 v45, s62\n" \
"s_mov_b64 exec, 1\n" \
"buffer_store_dword v45, off, s[80:83], s37 offset:32 sc1\n" \
"s_mov_b64 exec, -1\n" \
"v_cmp_ne_u32_e32 vcc, s62, v44\n" \
"s_nop 4\n" \
"s_cbranch_vccnz L_poll161_%=\n" \
"L_pollr161_%=:\n" \
M_"v[116:119], a[176:179], a[144:147], v[116:119]\n" \
"s_mov_b32 s73, s50\n" \
"s_sub_u32 m0, s31, 0x10000\n" \
M_"v[116:119], a[240:243], a[208:211], v[116:119]\n" \
B_"v20, s[80:83], s73 offen offset:0 sc1 lds\n" \
M_"v[120:123], a[180:183], a[144:147], v[120:123]\n" \
"s_add_u32 s74, s50, 0x4000\n" \
"s_add_u32 s75, s50, 0x8000\n" \
M_"v[120:123], a[244:247], a[208:211], v[120:123]\n" \
"s_mov_b32 s70, 0x3e0293ee\n" \
"s_mov_b32 s76, s52\n" \
M_"v[124:127], a[184:187], a[144:147], v[124:127]\n" \
"v_mul_f32_e32 v116, s70, v116\n" \
"v_mul_f32_e32 v117, s70, v117\n" \
M_"v[124:127], a[248:251], a[208:211], v[124:127]\n" \
B_"v20, s[80:83], s73 offen offset:1024 sc1 lds\n" \
M_"v[128:131], a[188:191], a[144:147], v[128:131]\n" \
"v_mul_f32_e32 v118, s70, v118\n" \
"v_mul_f32_e32 v119, s70, v119\n" \
M_"v[128:131], a[252:255], a[208:211], v[128:131]\n" \
C_"v116, v116, v117\n" \
"v_mul_f32_e64 v120, s70, v120\n" \
M_"v[144:147], a[188:191], a[148:151], v[144:147]\n" \
"v_mul_f32_e32 v124, s70, v124\n" \
"v_mul_f32_e32 v125, s70, v125\n" \
M_"v[144:147], a[252:255], a[212:215], v[144:147]\n" \
B_"v20, s[80:83], s73 offen offset:2048 sc1 lds\n" \
M_"v[140:143], a[184:187], a[148:151], v[140:143]\n" \
"v_mul_f32_e32 v126, s70, v126\n" \
"v_mul_f32_e32 v127, s70, v127\n" \
M_"v[140:143], a[248:251], a[212:215], v[140:143]\n" \
C_"v124, v124, v125\n" \
"v_mul_f32_e64 v121, s70, v121\n" \
M_"v[136:139], a[180:183], a[148:151], v[136:139]\n" \
"v_mul_f32_e32 v122, s70, v122\n" \
"v_mul_f32_e32 v123, s70, v123\n" \
M_"v[136:139], a[244:247], a[212:215], v[136:139]\n" \
B_"v20, s[80:83], s73 offen offset:3072 sc1 lds\n" \
M_"v[132:135], a[176:179], a[148:151], v[132:135]\n" \
"s_sub_u32 m0, s31, s51\n" \
C_"v117, v118, v119\n" \
M_"v[132:135], a[240:243], a[212:215], v[132:135]\n" \
"v_mul_f32_e32 v128, s70, v128\n" \
"v_mul_f32_e64 v129, s70, v129\n" \
M_"v[148:151], a[176:179], a[152:155], v[148:151]\n" \
"v_mul_f32_e32 v130, s70, v130\n" \
"v_mul_f32_e32 v131, s70, v131\n" \
M_"v[148:151], a[240:243], a[216:219], v[148:151]\n" \
B_"v20, s[80:83], s74 offen offset:0 sc1 lds\n" \
M_"v[152:155], a[180:183], a[152:155], v[152:155]\n" \
C_"v125, v126, v127\n" \
C_"v118, v120, v121\n" \
M_"v[152:155], a[244:247], a[216:219], v[152:155]\n" \
C_"v119, v122, v123\n" \
C_"v126, v128, v129\n" \
M_"v[156:159], a[184:187], a[152:155], v[156:159]\n" \
C_"v127, v130, v131\n" \
"v_mul_f32_e64 v132, s70, v132\n" \
M_"v[156:159], a[248:251], a[216:219], v[156:159]\n" \
B_"v20, s[80:83], s74 offen offset:1024 sc1 lds\n" \
M_"v[160:163], a[188:191], a[152:155], v[160:163]\n" \
"ds_write_b128 v21, v[116:119] offset:16384\n" \
M_"v[160:163], a[252:255], a[216:219], v[160:163]\n" \
"v_mul_f32_e32 v133, s70, v133\n" \
"v_mul_f32_e32 v140, s70, v140\n" \
M_"v[176:179], a[188:191], a[156:159], v[176:179]\n" \
"v_mul_f32_e32 v141, s70, v141\n" \
"v_mul_f32_e32 v134, s70, v134\n" \
M_"v[176:179], a[252:255], a[220:223], v[176:179]\n" \
B_"v20, s[80:83], s74 offen offset:2048 sc1 lds\n" \
M_"v[172:175], a[184:187], a[156:159], v[172:175]\n" \
"ds_write_b128 v21, v[124:127] offset:17408\n" \
M_"v[172:175], a[248:251], a[220:223], v[172:175]\n" \
"v_mul_f32_e64 v135, s70, v135\n" \
C_"v132, v132, v133\n" \
M_"v[168:171], a[180:183], a[156:159], v[168:171]\n" \
"v_mul_f32_e32 v142, s70, v142\n" \
"v_mul_f32_e32 v143, s70, v143\n" \
M_"v[168:171], a[244:247], a[220:223], v[168:171]\n" \
B_"v20, s[80:83], s74 offen offset:3072 sc1 lds\n" \
M_"v[164:167], a[176:179], a[156:159], v[164:167]\n" \
"s_sub_u32 m0, s31, 0x4000\n" \
C_"v140, v140, v141\n" \
M_"v[164:167], a[240:243], a[220:223], v[164:167]\n" \
"v_mul_f32_e32 v136, s70, v136\n" \
"v_mul_f32_e32 v137, s70, v137\n" \
M_"v[84:87], a[176:179], a[136:139], v[84:87]\n" \
"v_mul_f32_e32 v138, s70, v138\n" \
"v_mul_f32_e32 v139, s70, v139\n" \
M_"v[84:87], a[240:243], a[200:203], v[84:87]\n" \
B_"v20, s[80:83], s75 offen offset:0 sc1 lds\n" \
M_"v[88:91], a[180:183], a[136:139], v[88:91]\n" \
C_"v133, v134, v135\n" \
"v_mul_f32_e64 v144, s70, v144\n" \
M_"v[88:91], a[244:247], a[200:203], v[88:91]\n" \
"v_mul_f32_e32 v145, s70, v145\n" \
"v_mul_f32_e32 v146, s70, v146\n" \
M_"v[92:95], a[184:187], a[136:139], v[92:95]\n" \
"v_mul_f32_e64 v147, s70, v147\n" \
C_"v141, v142, v143\n" \
M_"v[92:95], a[248:251], a[200:203], v[92:95]\n" \
B_"v20, s[80:83], s75 offen offset:1024 sc1 lds\n" \
M_"v[96:99], a[188:191], a[136:139], v[96:99]\n" \
C_"v134, v136, v137\n" \
C_"v135, v138, v139\n" \
M_"v[96:99], a[252:255], a[200:203], v[96:99]\n" \
C_"v142, v144, v145\n" \
C_"v143, v146, v147\n" \
M_"v[112:115], a[188:191], a[140:143], v[112:115]\n" \
"ds_write_b128 v21, v[132:135] offset:20480\n" \
M_"v[112:115], a[252:255], a[204:207], v[112:115]\n" \
B_"v20, s[80:83], s75 offen offset:2048 sc1 lds\n" \
M_"v[108:111], a[184:187], a[140:143], v[108:111]\n" \
C_"v180, v180, v181\n" \
C_"v181, v182, v183\n" \
M_"v[108:111], a[248:251], a[204:207], v[108:111]\n" \
"ds_write_b128 v21, v[140:143] offset:21504\n" \
M_"v[104:107], a[180:183], a[140:143], v[104:107]\n" \
C_"v234, v244, v245\n" \
"v_mov_b32_e64 v235, 0\n" \
M_"v[104:107], a[244:247], a[204:207], v[104:107]\n" \
B_"v20, s[80:83], s75 offen offset:3072 sc1 lds\n" \
M_"v[100:103], a[176:179], a[140:143], v[100:103]\n" \
"s_mov_b32 m0, s53\n" \
"s_nop 0\n" \
C_"v182, v50, v51\n" \
M_"v[100:103], a[240:243], a[204:207], v[100:103]\n" \
"v_mov_b32_e32 v183, 0x3f80\n" \
C_"v118, v192, v193\n" \
M_"v[52:55], a[176:179], a[128:131], v[52:55]\n" \
C_"v119, v194, v195\n" \
C_"v120, v208, v209\n" \
M_"v[52:55], a[240:243], a[192:195], v[52:55]\n" \
B_"v20, s[80:83], s76 offen offset:0 sc1 lds\n" \
M_"v[56:59], a[180:183], a[128:131], v[56:59]\n" \
C_"v121, v210, v211\n" \
C_"v122, v224, v225\n" \
M_"v[56:59], a[244:247], a[192:195], v[56:59]\n" \
C_"v123, v226, v227\n" \
C_"v124, v240, v241\n" \
C_"v125, v242, v243\n" \
M_"v[60:63], a[184:187], a[128:131], v[60:63]\n" \
M_"v[192:195], v[188:191], v[184:187], 0\n" \
B_"v20, s[80:83], s76 offen offset:1024 sc1 lds\n" \
M_"v[32:35], v[180:183], v[184:187], 0\n" \
M_"v[208:211], v[204:207], v[200:203], 0\n" \
M_"v[36:39], v[188:191], v[200:203], 0\n" \
M_"v[224:227], v[220:223], v[216:219], 0\n" \
M_"v[248:251], v[204:207], v[216:219], 0\n" \
M_"v[240:243], v[236:239], v[232:235], 0\n" \
M_"v[128:131], v[220:223], v[232:235], 0\n" \
"s_mov_b32 s64, 0xaaaaaaaa\n" \
"s_mov_b32 s65, 0xaaaaaaaa\n" \
M_"v[60:63], a[248:251], a[192:195], v[60:63]\n" \
"s_mov_b32 s66, 0xfff0ffff\n" \
"s_mov_b32 s67, 0xf000ff00\n" \
M_"v[64:67], a[188:191], a[128:131], v[64:67]\n" \
"s_mov_b32 s68, 0xffc0fffc\n" \
"s_mov_b32 s69, 0xc000fc00\n" \
"v_cndmask_b32_e64 v192, v192, v193, s[64:65]\n" \
"v_cndmask_b32_e64 v32, v32, v33, s[64:65]\n" \
"v_cndmask_b32_e64 v194, v194, v195, s[64:65]\n" \
"v_cndmask_b32_e64 v34, v34, v35, s[64:65]\n" \
"v_cndmask_b32_e64 v208, v208, v209, s[64:65]\n" \
"v_cndmask_b32_e64 v36, v36, v37, s[64:65]\n" \
"v_cndmask_b32_e64 v210, v210, v211, s[64:65]\n" \
"v_cndmask_b32_e64 v38, v38, v39, s[64:65]\n" \
"v_cndmask_b32_e64 v224, v224, v225, s[64:65]\n" \
"v_cndmask_b32_e64 v248, v248, v249, s[64:65]\n" \
"v_cndmask_b32_e64 v226, v226, v227, s[64:65]\n" \
"v_cndmask_b32_e64 v250, v250, v251, s[64:65]\n" \
"v_cndmask_b32_e64 v240, v240, v241, s[64:65]\n" \
"v_cndmask_b32_e64 v128, v128, v129, s[64:65]\n" \
"v_cndmask_b32_e64 v242, v242, v243, s[64:65]\n" \
"v_cndmask_b32_e64 v130, v130, v131, s[64:65]\n" \
"v_cndmask_b32_e64 v192, v32, v192, s[66:67]\n" \
"v_cndmask_b32_e64 v194, v34, v194, s[68:69]\n" \
"v_cndmask_b32_e64 v208, v36, v208, s[66:67]\n" \
"v_cndmask_b32_e64 v210, v38, v210, s[68:69]\n" \
"v_cndmask_b32_e64 v224, v248, v224, s[66:67]\n" \
"v_cndmask_b32_e64 v226, v250, v226, s[68:69]\n" \
"v_cndmask_b32_e64 v240, v128, v240, s[66:67]\n" \
"v_cndmask_b32_e64 v242, v130, v242, s[68:69]\n" \
M_"v[64:67], a[252:255], a[192:195], v[64:67]\n" \
"v_max_f32_e32 v193, v192, v194\n" \
"v_max_f32_e32 v209, v208, v210\n" \
"v_max_f32_e32 v225, v224, v226\n" \
"v_max_f32_e32 v241, v240, v242\n" \
"v_mul_f32_e32 v148, s70, v148\n" \
"v_permlane32_swap_b32_e32 v193, v225\n" \
"v_permlane32_swap_b32_e32 v209, v241\n" \
"v_mul_f32_e32 v149, s70, v149\n" \
"v_mul_f32_e32 v156, s70, v156\n" \
"v_mul_f32_e32 v157, s70, v157\n" \
"v_permlane32_swap_b32_e32 v196, v228\n" \
"v_permlane32_swap_b32_e32 v212, v244\n" \
"v_max_f32_e32 v193, v193, v225\n" \
"v_max_f32_e32 v209, v209, v241\n" \
"v_mul_f32_e32 v150, s70, v150\n" \
"v_mul_f32_e32 v151, s70, v151\n" \
C_"v148, v148, v149\n" \
"v_mul_f32_e32 v158, s70, v158\n" \
"v_mul_f32_e32 v159, s70, v159\n" \
C_"v156, v156, v157\n" \
"v_permlane16_swap_b32_e32 v228, v244\n" \
"v_permlane16_swap_b32_e32 v193, v209\n" \
M_"v[80:83], a[188:191], a[132:135], v[80:83]\n" \
"s_mov_b32 s72, 0x3eae1a93\n" \
"v_mul_f32_e32 v152, s70, v152\n" \
"v_mul_f32_e32 v153, s70, v153\n" \
"v_mul_f32_e32 v154, s70, v154\n" \
"v_mul_f32_e32 v155, s70, v155\n" \
C_"v149, v150, v151\n" \
"v_mul_f32_e32 v160, s70, v160\n" \
"v_mul_f32_e32 v161, s70, v161\n" \
"v_mul_f32_e32 v162, s70, v162\n" \
"v_mul_f32_e32 v163, s70, v163\n" \
C_"v157, v158, v159\n" \
"v_mul_f32_e32 v45, 0xbfb8aa3b, v228\n" \
"v_mfma_f32_16x16x16_bf16 v[48:51], v[182:183], v[46:47], 0\n" \
"v_mfma_f32_16x16x16_bf16 v[196:199], v[190:191], v[46:47], 0\n" \
"v_mfma_f32_16x16x16_bf16 v[212:215], v[206:207], v[46:47], 0\n" \
"v_max_f32_e64 v193, v193, v209\n" \
C_"v150, v152, v153\n" \
C_"v151, v154, v155\n" \
C_"v158, v160, v161\n" \
C_"v159, v162, v163\n" \
"v_mfma_f32_16x16x16_bf16 v[228:231], v[222:223], v[46:47], 0\n" \
"v_mul_f32_e32 v193, s72, v193\n" \
"v_mov_b32_e32 v209, v193\n" \
"ds_write_b128 v21, v[148:151] offset:24576\n" \
"v_mfma_f32_16x16x16_bf16 v[244:247], v[238:239], v[46:47], 0\n" \
"v_permlane16_swap_b32_e32 v193, v209\n" \
"s_mov_b32 s71, 0x3e0293ee\n" \
M_"v[80:83], a[252:255], a[196:199], v[80:83]\n" \
"v_mov_b32_e32 v225, v193\n" \
"v_mov_b32_e32 v241, v209\n" \
"v_mul_f32_e32 v164, s70, v164\n" \
"v_mul_f32_e64 v165, s70, v165\n" \
"ds_write_b128 v21, v[156:159] offset:25600\n" \
"v_mul_f32_e32 v172, s70, v172\n" \
"v_mul_f32_e32 v173, s70, v173\n" \
"v_pk_mul_f32 v[84:85], v[84:85], s[70:71]\n" \
"v_pk_mul_f32 v[92:93], v[92:93], s[70:71]\n" \
"v_pk_mul_f32 v[100:101], v[100:101], s[70:71]\n" \
"v_pk_mul_f32 v[108:109], v[108:109], s[70:71]\n" \
"v_pk_mul_f32 v[52:53], v[52:53], s[70:71]\n" \
"v_pk_mul_f32 v[60:61], v[60:61], s[70:71]\n" \
"v_permlane32_swap_b32_e32 v193, v225\n" \
"v_permlane32_swap_b32_e32 v209, v241\n" \
"v_mul_f32_e32 v166, s70, v166\n" \
"v_mul_f32_e32 v167, s70, v167\n" \
C_"v164, v164, v165\n" \
"v_mul_f32_e32 v174, s70, v174\n" \
"v_mul_f32_e32 v175, s70, v175\n" \
C_"v172, v172, v173\n" \
"v_pk_mul_f32 v[86:87], v[86:87], s[70:71]\n" \
C_"v84, v84, v85\n" \
"v_pk_mul_f32 v[94:95], v[94:95], s[70:71]\n" \
C_"v92, v92, v93\n" \
"v_pk_mul_f32 v[102:103], v[102:103], s[70:71]\n" \
C_"v100, v100, v101\n" \
"v_pk_mul_f32 v[110:111], v[110:111], s[70:71]\n" \
C_"v108, v108, v109\n" \
"v_pk_mul_f32 v[54:55], v[54:55], s[70:71]\n" \
C_"v52, v52, v53\n" \
"v_pk_mul_f32 v[62:63], v[62:63], s[70:71]\n" \
C_"v60, v60, v61\n" \
"v_fma_f32 v192, v192, s72, -v193\n" \
"v_fma_f32 v194, v194, s72, -v193\n" \
"v_fma_f32 v208, v208, s72, -v209\n" \
"v_fma_f32 v210, v210, s72, -v209\n" \
"v_mul_f32_e32 v168, s70, v168\n" \
"v_mul_f32_e32 v169, s70, v169\n" \
"v_mul_f32_e32 v170, s70, v170\n" \
"v_mul_f32_e32 v171, s70, v171\n" \
C_"v165, v166, v167\n" \
"v_mul_f32_e32 v176, s70, v176\n" \
"v_mul_f32_e32 v177, s70, v177\n" \
"v_mul_f32_e32 v178, s70, v178\n" \
"v_mul_f32_e32 v179, s70, v179\n" \
C_"v173, v174, v175\n" \
"v_pk_mul_f32 v[88:89], v[88:89], s[70:71]\n" \
"v_pk_mul_f32 v[90:91], v[90:91], s[70:71]\n" \
C_"v85, v86, v87\n" \
"v_pk_mul_f32 v[96:97], v[96:97], s[70:71]\n" \
"v_pk_mul_f32 v[98:99], v[98:99], s[70:71]\n" \
C_"v93, v94, v95\n" \
"v_pk_mul_f32 v[104:105], v[104:105], s[70:71]\n" \
"v_pk_mul_f32 v[106:107], v[106:107], s[70:71]\n" \
C_"v101, v102, v103\n" \
"v_pk_mul_f32 v[112:113], v[112:113], s[70:71]\n" \
"v_pk_mul_f32 v[114:115], v[114:115], s[70:71]\n" \
C_"v109, v110, v111\n" \
"v_pk_mul_f32 v[56:57], v[56:57], s[70:71]\n" \
"v_pk_mul_f32 v[58:59], v[58:59], s[70:71]\n" \
C_"v53, v54, v55\n" \
"v_pk_mul_f32 v[64:65], v[64:65], s[70:71]\n" \
"v_pk_mul_f32 v[66:67], v[66:67], s[70:71]\n" \
C_"v61, v62, v63\n" \
"v_pk_mul_f32 v[80:81], v[80:81], s[70:71]\n" \
"v_pk_mul_f32 v[82:83], v[82:83], s[70:71]\n" \
E_"v192, v192\n" \
E_"v194, v194\n" \
E_"v208, v208\n" \
E_"v210, v210\n" \
"v_fma_f32 v224, v224, s72, -v225\n" \
"v_fma_f32 v226, v226, s72, -v225\n" \
M_"v[76:79], a[184:187], a[132:135], v[76:79]\n" \
C_"v166, v168, v169\n" \
C_"v167, v170, v171\n" \
C_"v174, v176, v177\n" \
C_"v175, v178, v179\n" \
C_"v86, v88, v89\n" \
C_"v87, v90, v91\n" \
C_"v94, v96, v97\n" \
C_"v95, v98, v99\n" \
C_"v102, v104, v105\n" \
C_"v103, v106, v107\n" \
C_"v110, v112, v113\n" \
C_"v111, v114, v115\n" \
C_"v54, v56, v57\n" \
C_"v55, v58, v59\n" \
C_"v62, v64, v65\n" \
C_"v63, v66, v67\n" \
E_"v224, v224\n" \
E_"v226, v226\n" \
"v_fma_f32 v240, v240, s72, -v241\n" \
"v_fma_f32 v242, v242, s72, -v241\n" \
"ds_write_b128 v21, v[164:167] offset:28672\n" \
C_"v192, v192, v194\n" \
C_"v208, v208, v210\n" \
E_"v240, v240\n" \
E_"v242, v242\n" \
C_"v132, v48, v49\n" \
C_"v133, v50, v51\n" \
C_"v134, v196, v197\n" \
C_"v135, v198, v199\n" \
"ds_write_b128 v21, v[172:175] offset:29696\n" \
C_"v136, v212, v213\n" \
C_"v137, v214, v215\n" \
"v_perm_b32 v184, v192, v192, v40\n" \
"v_perm_b32 v185, v192, v192, v41\n" \
"v_perm_b32 v186, v192, v192, v42\n" \
"v_perm_b32 v187, v192, v192, v43\n" \
"v_perm_b32 v200, v208, v208, v40\n" \
"v_perm_b32 v201, v208, v208, v41\n" \
"v_perm_b32 v202, v208, v208, v42\n" \
"v_perm_b32 v203, v208, v208, v43\n" \
"ds_write_b128 v27, v[84:87] offset:8192\n" \
C_"v224, v224, v226\n" \
C_"v138, v228, v229\n" \
C_"v139, v230, v231\n" \
M_"v[32:35], v[132:135], v[184:187], 0\n" \
M_"v[36:39], v[134:137], v[200:203], 0\n" \
"v_perm_b32 v216, v224, v224, v40\n" \
"v_perm_b32 v217, v224, v224, v41\n" \
"ds_write_b128 v27, v[92:95] offset:9216\n" \
"v_perm_b32 v218, v224, v224, v42\n" \
"v_perm_b32 v219, v224, v224, v43\n" \
C_"v240, v240, v242\n" \
M_"v[76:79], a[248:251], a[196:199], v[76:79]\n" \
C_"v140, v244, v245\n" \
C_"v141, v246, v247\n" \
M_"v[248:251], v[136:139], v[216:219], 0\n" \
"v_perm_b32 v232, v240, v240, v40\n" \
"v_perm_b32 v233, v240, v240, v41\n" \
"ds_write_b128 v27, v[100:103] offset:12288\n" \
"v_perm_b32 v234, v240, v240, v42\n" \
"v_perm_b32 v235, v240, v240, v43\n" \
"v_pk_mul_f32 v[76:77], v[76:77], s[70:71]\n" \
"v_pk_mul_f32 v[78:79], v[78:79], s[70:71]\n" \
C_"v76, v76, v77\n" \
C_"v77, v78, v79\n" \
C_"v78, v80, v81\n" \
C_"v79, v82, v83\n" \
M_"v[128:131], v[138:141], v[232:235], 0\n" \
"ds_write_b128 v27, v[108:111] offset:13312\n" \
M_"v[72:75], a[180:183], a[132:135], v[72:75]\n" \
C_"v116, v252, v253\n" \
C_"v117, v254, v255\n" \
M_"v[72:75], a[244:247], a[196:199], v[72:75]\n" \
"v_exp_f32_e64 v45, v45\n" \
M_"v[192:195], v[116:119], v[184:187], 0\n" \
"ds_write_b128 v27, v[52:55] offset:0\n" \
M_"v[208:211], v[118:121], v[200:203], 0\n" \
M_"v[224:227], v[120:123], v[216:219], 0\n" \
M_"v[240:243], v[122:125], v[232:235], 0\n" \
"ds_write_b128 v27, v[60:63] offset:1024\n" \
"v_mov_b32_dpp v34, v38 quad_perm:[0,1,2,3] row_mask:0x2 bank_mask:0xf\n" \
"v_pk_mul_f32 v[72:73], v[72:73], s[70:71]\n" \
"v_pk_mul_f32 v[74:75], v[74:75], s[70:71]\n" \
"v_mov_b32_dpp v34, v250 quad_perm:[0,1,2,3] row_mask:0x4 bank_mask:0xf\n" \
"v_add_f32_e64 v45, 1.0, v45\n" \
M_"v[68:71], a[176:179], a[132:135], v[68:71]\n" \
"v_mov_b32_dpp v34, v130 quad_perm:[0,1,2,3] row_mask:0x8 bank_mask:0xf\n" \
"v_mov_b32_dpp v32, v36 quad_perm:[0,1,2,3] row_mask:0x2 bank_mask:0xf\n" \
"v_rcp_f32_e64 v45, v45\n" \
"ds_write_b128 v27, v[76:79] offset:5120\n" \
"v_mov_b32_dpp v33, v37 quad_perm:[0,1,2,3] row_mask:0x2 bank_mask:0xf\n" \
"v_rcp_f32_e64 v34, v34\n" \
"v_mov_b32_dpp v32, v248 quad_perm:[0,1,2,3] row_mask:0x4 bank_mask:0xf\n" \
"v_mov_b32_dpp v33, v249 quad_perm:[0,1,2,3] row_mask:0x4 bank_mask:0xf\n" \
"v_mul_f32_e64 v34, v45, v34\n" \
"v_mov_b32_dpp v32, v128 quad_perm:[0,1,2,3] row_mask:0x8 bank_mask:0xf\n" \
"v_mov_b32_dpp v33, v129 quad_perm:[0,1,2,3] row_mask:0x8 bank_mask:0xf\n" \
"v_mul_f32_e32 v32, v34, v32\n" \
"v_mul_f32_e32 v33, v34, v33\n" \
"v_mov_b32_e64 v38, v34\n" \
M_"v[68:71], a[240:243], a[196:199], v[68:71]\n" \
"v_sub_f32_e32 v44, 1.0, v45\n" \
"v_permlane16_swap_b32_e32 v34, v38\n" \
"v_mov_b32_e32 v250, v34\n" \
"v_mov_b32_e32 v130, v38\n" \
C_"v32, v32, v33\n" \
"v_permlane32_swap_b32_e32 v34, v250\n" \
"v_permlane32_swap_b32_e32 v38, v130\n" \
"v_mul_f32_e64 v192, v34, v192\n" \
"v_pk_mul_f32 v[68:69], v[68:69], s[70:71]\n" \
"v_pk_mul_f32 v[70:71], v[70:71], s[70:71]\n" \
C_"v68, v68, v69\n" \
C_"v69, v70, v71\n" \
C_"v70, v72, v73\n" \
C_"v71, v74, v75\n" \
"ds_write_b128 v27, v[68:71] offset:4096\n" \
"v_mul_f32_e32 v193, v34, v193\n" \
"v_mul_f32_e32 v208, v38, v208\n" \
"v_mul_f32_e32 v209, v38, v209\n" \
"v_mul_f32_e32 v224, v250, v224\n" \
"v_mul_f32_e32 v225, v250, v225\n" \
"v_mul_f32_e32 v240, v130, v240\n" \
"v_mul_f32_e32 v241, v130, v241\n" \
"v_mul_f32_e32 v194, v34, v194\n" \
"v_mul_f32_e64 v195, v34, v195\n" \
C_"v192, v192, v193\n" \
"v_mul_f32_e32 v210, v38, v210\n" \
"v_mul_f32_e32 v211, v38, v211\n" \
C_"v208, v208, v209\n" \
"v_mul_f32_e32 v226, v250, v226\n" \
"v_mul_f32_e32 v227, v250, v227\n" \
C_"v224, v224, v225\n" \
"v_mul_f32_e32 v242, v130, v242\n" \
"v_mul_f32_e32 v243, v130, v243\n" \
C_"v240, v240, v241\n" \
C_"v193, v194, v195\n" \
"s_add_u32 s84, s48, 0x0\n" \
"s_nop 0\n" \
C_"v209, v210, v211\n" \
"s_add_u32 s85, s48, 0x24800\n" \
C_"v225, v226, v227\n" \
"s_add_u32 s86, s48, 0x49000\n" \
C_"v241, v242, v243\n" \
"s_add_u32 s87, s48, 0x6d800\n" \
"ds_write_b32 v26, v44\n" \
"buffer_store_dword v32, v25, s[80:83], s49 offen sc1\n" \
"buffer_store_dwordx2 v[192:193], v24, s[80:83], s84 offen sc1\n" \
"buffer_store_dwordx2 v[208:209], v24, s[80:83], s85 offen sc1\n" \
"buffer_store_dwordx2 v[224:225], v24, s[80:83], s86 offen sc1\n" \
"buffer_store_dwordx2 v[240:241], v24, s[80:83], s87 offen sc1\n" \
"s_branch L_end_%=\n" \
"L_poll61_%=:\n" \
"s_mov_b32 s84, 0\n" \
"L_pl61_%=:\n" \
"s_sleep 1\n" \
"v_bfe_u32 v44, v20, 2, 5\n" \
"buffer_load_dword v44, v44, s[80:83], s38 offen offset:128 sc1\n" \
"s_waitcnt vmcnt(0)\n" \
"v_cmp_ne_u32_e32 vcc, s62, v44\n" \
"s_add_u32 s84, s84, 1\n" \
"s_nop 3\n" \
"s_cbranch_vccz L_pollr61_%=\n" \
"s_cmp_gt_u32 s84, 0x400000\n" \
"s_cbranch_scc0 L_pl61_%=\n" \
"s_branch L_pollr61_%=\n" \
"L_poll161_%=:\n" \
"s_mov_b32 s84, 0\n" \
"L_pl161_%=:\n" \
"s_sleep 1\n" \
"v_bfe_u32 v44, v20, 2, 5\n" \
"buffer_load_dword v44, v44, s[80:83], s38 offen offset:0 sc1\n" \
"s_waitcnt vmcnt(0)\n" \
"v_cmp_ne_u32_e32 vcc, s62, v44\n" \
"s_add_u32 s84, s84, 1\n" \
"s_nop 3\n" \
"s_cbranch_vccz L_pollr161_%=\n" \
"s_cmp_gt_u32 s84, 0x400000\n" \
"s_cbranch_scc0 L_pl161_%=\n" \
"s_branch L_pollr161_%=\n" \
"L_end_%=:\n"
#define PROJ_PAGES 20
#define PROJ_VCLOB "v21","v24","v25","v28","v29","v32","v33","v34","v35","v36","v37","v38","v39","v40","v41","v42","v43","v44","v45","v46","v47","v48","v49","v50","v51","v52","v53","v54","v55","v56","v57","v58","v59","v60","v61","v62","v63","v64","v65","v66","v67","v68","v69","v70","v71","v72","v73","v74","v75","v76","v77","v78","v79","v80","v81","v82","v83","v84","v85","v86","v87","v88","v89","v90","v91","v92","v93","v94","v95","v96","v97","v98","v99","v100","v101","v102","v103","v104","v105","v106","v107","v108","v109","v110","v111","v112","v113","v114","v115","v116","v117","v118","v119","v120","v121","v122","v123","v124","v125","v126","v127","v128","v129","v130","v131","v132","v133","v134","v135","v136","v137","v138","v139","v140","v141","v142","v143","v144","v145","v146","v147","v148","v149","v150","v151","v152","v153","v154","v155","v156","v157","v158","v159","v160","v161","v162","v163","v164","v165","v166","v167","v168","v169","v170","v171","v172","v173","v174","v175","v176","v177","v178","v179","v180","v181","v182","v183","v184","v185","v186","v187","v188","v189","v190","v192","v193","v194","v195","v196","v197","v198","v199","v200","v201","v202","v203","v204","v205","v206","v207","v208","v209","v210","v211","v212","v213","v214","v215","v216","v217","v218","v219","v220","v221","v222","v223","v224","v225","v226","v227","v228","v229","v230","v231","v232","v233","v234","v235","v236","v237","v238","v239","v240","v241","v242","v243","v244","v245","v246","v247","v248","v249","v250","v251","v252","v253","v254","v255"
#define PROJ_SCLOB "s4","s5","s6","s7","s32","s33","s34","s35","s36","s37","s38","s39","s40","s41","s42","s43","s44","s45","s46","s47","s48","s49","s50","s51","s52","s53","s54","s55","s63","s64","s65","s66","s67","s68","s69","s70","s71","s72","s73","s74","s75","s76","s77","s78","s79","s80","s81","s82","s83","s84","s85","s86","s87","s88","s89","s90","s91","s92","s93","s94","s95","s96","s97","s98","s99","s100","s101"

typedef const u16* const __attribute__((address_space(4)))* kaptr;
__device__ __forceinline__ void proj_phase(kaptr& ka, char*& ws, const unsigned wid, u32x4 (&vp)[5], unsigned& vl, u32x4 (&sa)[5], u32x2 s24, u32x2& s28, unsigned tdum, u32x4& srdA, u32x2& sd, unsigned& tag
    ) {
    const unsigned b = blockIdx.x, h = (b & 7) * 4 + ((b >> 3) & 3), mt = b >> 5;
    const unsigned rb = (wid < 2u ? 15u - mt : mt) * 128u;
    const unsigned lane = threadIdx.x & 63u, wc = wid & 1u;
    u32x2 vAc, vB;
#pragma unroll
    for (int par = 0; par < 2; ++par) {
        const unsigned lr = lane >> 3, cc = (lane & 7u) ^ (4u * par + (lr >> 1)), r0 = rb + wc * 64u + lr;
        vAc[par] = r0 * (DC * 2) + cc * 16u;
        const unsigned br = lane >> 4, p16 = lane & 15u, sc = (((p16 >> 1) ^ (br | (par << 2))) << 1) | (p16 & 1u);
        vB[par] = (16u * wid + br) * (NBIG * 2) + h * (HD * 2) + sc * 16u;
    }
    vl = lane * 16u;
    u32x2 s30 = {wid * 8192u, 65536u + wid * 4096u};
    asm volatile(PROJ_ASM
                 : "={s62}"(tag), "={s[56:59]}"(srdA), "={s[60:61]}"(sd), "+{s[8:11]}"(sa[0]), "+{s[12:15]}"(sa[1]), "+{s[16:19]}"(sa[2]), "+{s[20:23]}"(sa[3]), "+{s[24:25]}"(s24), "+{s[28:29]}"(s28), "+{s[30:31]}"(s30),
                   "={v[0:3]}"(vp[0]), "={v[4:7]}"(vp[1]), "={v[8:11]}"(vp[2]), "={v[12:15]}"(vp[3]), "={v[16:19]}"(vp[4]), "+{v[22:23]}"(vAc), "+{v[26:27]}"(vB), "+{v191}"(tdum), "+{s[26:27]}"(ws), "+{s[0:1]}"(ka)
                 : "{s2}"(b), "{s3}"(wid), "{v20}"(vl)
                 : "memory", "m0", "vcc", "scc", PROJ_VCLOB, PROJ_SCLOB, ACLOB_LO, ACLOB_HI);
    sa[4] = (u32x4){s24[0], 0u, 0u, 0u};
}

namespace att {
#define DI __device__ __forceinline__
typedef __bf16 bfx8 __attribute__((ext_vector_type(8)));
DI unsigned rfl(unsigned x) { return __builtin_amdgcn_readfirstlane(x); }
DI f32x4 mfma(u32x4 a, u32x4 b, f32x4 c) { return __builtin_amdgcn_mfma_f32_16x16x32_bf16(__builtin_bit_cast(bfx8, a), __builtin_bit_cast(bfx8, b), c, 0, 0, 0); }
typedef __bf16 bfx2 __attribute__((ext_vector_type(2)));
typedef float f32x2 __attribute__((ext_vector_type(2)));
DI unsigned cvtpk2(float lo, float hi) { f32x2 v = {lo, hi}; bfx2 b = __builtin_convertvector(v, bfx2); return __builtin_bit_cast(unsigned, b); }
DI void att_exact(const u16* __restrict__ x, const u16* __restrict__ wq, char* __restrict__ ws, unsigned h, unsigned r0, float g1) {
    const unsigned lane = lane_id(), g = lane >> 4, c = lane & 15u;
    f32x4 o[8];
#pragma unroll
    for (int n = 0; n < 8; ++n) o[n] = (f32x4){0.f, 0.f, 0.f, 0.f};
#pragma unroll 1
    for (unsigned kk = 0; kk < (unsigned)DM; kk += 32u) {
        const unsigned k = kk + 8u * g;
        const u32x4 xf = *reinterpret_cast<const u32x4*>(x + (r0 + c) * DM + k);
#pragma unroll
        for (int n = 0; n < 8; ++n) {
            const u16* w = wq + k * NBIG + h * HD + n * 16 + c; u32x4 wf;
#pragma unroll
            for (int e = 0; e < 4; ++e) wf[e] = (unsigned)w[2 * e * NBIG] | ((unsigned)w[(2 * e + 1) * NBIG] << 16);
            o[n] = mfma(wf, xf, o[n]);
        }
    }
    u32x4 q[4];
#pragma unroll
    for (int p = 0; p < 4; ++p) {
        const f32x4 a = o[2 * p] * 0.12751743082f, b = o[2 * p + 1] * 0.12751743082f;
        q[p] = (u32x4){cvtpk2(a[0], a[1]), cvtpk2(a[2], a[3]), cvtpk2(b[0], b[1]), cvtpk2(b[2], b[3])};
    }
#pragma unroll
    for (int n = 0; n < 8; ++n) o[n] = (f32x4){0.f, 0.f, 0.f, 0.f};
    const __amdgpu_buffer_rsrc_t srd = __builtin_amdgcn_make_buffer_rsrc((void*)(ws + WS_K + (size_t)h * 1048576), (short)0, 1048576, 0x00020000);
    float m = -3.0e38f, l = 0.f;
#pragma unroll 1
    for (unsigned k0 = 0; k0 <= r0 + 15u; k0 += 32u) {
        const unsigned tb = (k0 >> 6) * 32768u + (k0 & 32u) * 256u;
        f32x4 s[2]; float mx = m;
#pragma unroll
        for (int kt = 0; kt < 2; ++kt) {
            s[kt] = (f32x4){0.f, 0.f, 0.f, 0.f};
#pragma unroll
            for (int p = 0; p < 4; ++p) s[kt] = mfma(__builtin_amdgcn_raw_buffer_load_b128(srd, lane * 16u + (kt * 4 + p) * 1024u, tb, 16), q[p], s[kt]);
#pragma unroll
            for (int e = 0; e < 4; ++e) { if (k0 + 16u * kt + 4u * g + e > r0 + c) s[kt][e] = -3.0e38f; mx = fmaxf(mx, s[kt][e]); }
        }
        mx = fmaxf(mx, __shfl_xor(mx, 16)); mx = fmaxf(mx, __shfl_xor(mx, 32));
        const float al = __builtin_amdgcn_exp2f(m - mx); m = mx;
        u32x4 pf;
#pragma unroll
        for (int e = 0; e < 4; ++e) pf[e] = cvtpk2(__builtin_amdgcn_exp2f(s[e >> 1][2 * (e & 1)] - m), __builtin_amdgcn_exp2f(s[e >> 1][2 * (e & 1) + 1] - m));
        l *= al;
#pragma unroll
        for (int e = 0; e < 4; ++e) l += __uint_as_float(pf[e] << 16) + __uint_as_float(pf[e] & 0xffff0000u);
#pragma unroll
        for (int n = 0; n < 8; ++n) o[n] = mfma(__builtin_amdgcn_raw_buffer_load_b128(srd, lane * 16u + 16384u + n * 1024u, tb, 16), pf, o[n] * al);
    }
    l += __shfl_xor(l, 16); l += __shfl_xor(l, 32);
    const float f = g1 / l;
    const __amdgpu_buffer_rsrc_t srdA = __builtin_amdgcn_make_buffer_rsrc((void*)(ws + WS_AF), (short)0, 128 * KS_TOT * 1024, 0x00020000);
#pragma unroll
    for (int p = 0; p < 4; ++p) {
        const f32x4 a = o[2 * p] * f, b = o[2 * p + 1] * f;
        __builtin_amdgcn_raw_buffer_store_b128((u32x4){cvtpk2(a[0], a[1]), cvtpk2(a[2], a[3]), cvtpk2(b[0], b[1]), cvtpk2(b[2], b[3])}, srdA, lane * 16u + p * 1024u, ((r0 >> 4) * KS_TOT + 18u + 4u * h) * 1024u, 16);
    }
}
}

#define ATT_ASM \
"s_branch L_ago_%=\n" \
".p2align 12\n" \
"L_ab:\n" \
".fill (L_ab + 0 - .) / 4, 4, 0xBF800000\n" \
"L_ap0:\n" \
"s_setpc_b64 s[88:89]\n" \
"L_ago_%=:\n" \
"s_mov_b32 s101, 0\n" \
"s_waitcnt vmcnt(15) lgkmcnt(0)\n" \
"s_barrier\n" \
"v_mfma_f32_32x32x16_bf16 v[128:143], a[120:123], a[120:123], 0\n" \
"v_and_b32_e32 v63, 0x300, v20\n" \
R_"v[96:99], v63 offset:0\n" \
"v_bfe_u32 v61, v20, 2, 5\n" \
"s_mov_b32 s96, s24\n" \
"v_add_u32_e32 v58, s22, v20\n" \
R_"a[0:3], v58 offset:0\n" \
"v_mfma_f32_32x32x16_bf16 v[144:159], a[120:123], a[120:123], 0\n" \
"buffer_load_dword v57, v61, s[56:59], s96 offen sc1\n" \
R_"a[16:19], v58 offset:8192\n" \
"v_add_u32_e32 v59, s23, v20\n" \
R_"a[32:35], v59 offset:0\n" \
R_"a[48:51], v59 offset:8192\n" \
"v_mfma_f32_32x32x16_bf16 v[160:175], a[120:123], a[120:123], 0\n" \
R_"v[100:103], v63 offset:1024\n" \
R_"a[4:7], v58 offset:1024\n" \
R_"a[20:23], v58 offset:9216\n" \
"s_sub_u32 s67, s12, 0xc000\n" \
"s_mov_b32 s65, s12\n" \
"v_add_u32_e32 v55, s67, v20\n" \
"v_mfma_f32_32x32x16_bf16 v[176:191], a[120:123], a[120:123], 0\n" \
"v_add_u32_e32 v52, 0xc000, v55\n" \
"s_xor_b32 m0, s65, 0x10000\n" \
R_"a[36:39], v59 offset:1024\n" \
B_"v52, s[8:11], 0 offen offset:0 sc1 lds\n" \
R_"a[52:55], v59 offset:9216\n" \
"v_mfma_f32_32x32x16_bf16 v[192:207], a[120:123], a[120:123], 0\n" \
R_"v[104:107], v63 offset:2048\n" \
R_"a[8:11], v58 offset:2048\n" \
R_"a[24:27], v58 offset:10240\n" \
B_"v52, s[8:11], 0 offen offset:1024 sc1 lds\n" \
R_"a[40:43], v59 offset:2048\n" \
"v_mfma_f32_32x32x16_bf16 v[208:223], a[120:123], a[120:123], 0\n" \
R_"a[56:59], v59 offset:10240\n" \
R_"v[108:111], v63 offset:3072\n" \
R_"a[12:15], v58 offset:3072\n" \
R_"a[28:31], v58 offset:11264\n" \
"s_waitcnt lgkmcnt(15)\n" \
"v_xor_b32_e32 v96, 0x80008000, v96\n" \
"v_mfma_f32_32x32x16_bf16 v[224:239], a[120:123], a[120:123], 0\n" \
"v_xor_b32_e32 v97, 0x80008000, v97\n" \
"v_xor_b32_e32 v98, 0x80008000, v98\n" \
"v_xor_b32_e32 v99, 0x80008000, v99\n" \
R_"a[44:47], v59 offset:3072\n" \
R_"a[60:63], v59 offset:11264\n" \
R_"a[160:163], v20 offset:0\n" \
"v_mfma_f32_32x32x16_bf16 v[240:255], a[120:123], a[120:123], 0\n" \
R_"a[164:167], v20 offset:1024\n" \
"s_waitcnt lgkmcnt(15)\n" \
"v_xor_b32_e32 v100, 0x80008000, v100\n" \
"v_xor_b32_e32 v101, 0x80008000, v101\n" \
"v_xor_b32_e32 v102, 0x80008000, v102\n" \
"v_xor_b32_e32 v103, 0x80008000, v103\n" \
M_"v[32:35], v[96:99], a[0:3], 0\n" \
R_"a[168:171], v20 offset:2048\n" \
R_"a[172:175], v20 offset:3072\n" \
M_"v[36:39], v[96:99], a[16:19], 0\n" \
R_"a[188:191], v20 offset:7168\n" \
R_"a[184:187], v20 offset:6144\n" \
M_"v[40:43], v[96:99], a[32:35], 0\n" \
"s_waitcnt lgkmcnt(15)\n" \
"v_xor_b32_e32 v104, 0x80008000, v104\n" \
M_"v[44:47], v[96:99], a[48:51], 0\n" \
"v_xor_b32_e32 v105, 0x80008000, v105\n" \
"v_xor_b32_e32 v106, 0x80008000, v106\n" \
M_"v[32:35], v[100:103], a[4:7], v[32:35]\n" \
"v_xor_b32_e32 v107, 0x80008000, v107\n" \
R_"a[180:183], v20 offset:5120\n" \
M_"v[36:39], v[100:103], a[20:23], v[36:39]\n" \
R_"a[176:179], v20 offset:4096\n" \
"s_waitcnt lgkmcnt(12)\n" \
"v_xor_b32_e32 v108, 0x80008000, v108\n" \
M_"v[40:43], v[100:103], a[36:39], v[40:43]\n" \
"v_xor_b32_e32 v109, 0x80008000, v109\n" \
"v_xor_b32_e32 v110, 0x80008000, v110\n" \
M_"v[44:47], v[100:103], a[52:55], v[44:47]\n" \
"v_xor_b32_e32 v111, 0x80008000, v111\n" \
"v_add_u32_e32 v60, s15, v20\n" \
M_"v[32:35], v[104:107], a[8:11], v[32:35]\n" \
"v_add_u32_e32 v60, 0x10000, v60\n" \
R_"a[128:131], v20 offset:8192\n" \
M_"v[36:39], v[104:107], a[24:27], v[36:39]\n" \
R_"a[144:147], v20 offset:12288\n" \
R_"a[132:135], v20 offset:9216\n" \
M_"v[40:43], v[104:107], a[40:43], v[40:43]\n" \
R_"a[148:151], v20 offset:13312\n" \
R_"a[136:139], v20 offset:10240\n" \
M_"v[44:47], v[104:107], a[56:59], v[44:47]\n" \
R_"a[152:155], v20 offset:14336\n" \
"s_waitcnt lgkmcnt(13)\n" \
M_"v[32:35], v[108:111], a[12:15], v[32:35]\n" \
R_"a[140:143], v20 offset:11264\n" \
R_"a[156:159], v20 offset:15360\n" \
M_"v[36:39], v[108:111], a[28:31], v[36:39]\n" \
R_"a[64:67], v60 offset:16384\n" \
R_"a[68:71], v60 offset:17408\n" \
M_"v[40:43], v[108:111], a[44:47], v[40:43]\n" \
R_"a[72:75], v60 offset:18432\n" \
R_"a[76:79], v60 offset:19456\n" \
M_"v[44:47], v[108:111], a[60:63], v[44:47]\n" \
R_"a[80:83], v60 offset:24576\n" \
R_"a[84:87], v60 offset:25600\n" \
M_"v[64:67], a[160:163], a[0:3], v[32:35]\n" \
R_"a[88:91], v60 offset:26624\n" \
"s_waitcnt lgkmcnt(15)\n" \
M_"v[64:67], a[164:167], a[4:7], v[64:67]\n" \
R_"a[92:95], v60 offset:27648\n" \
R_"a[192:195], v60 offset:0\n" \
M_"v[64:67], a[168:171], a[8:11], v[64:67]\n" \
R_"a[196:199], v60 offset:1024\n" \
R_"a[200:203], v60 offset:2048\n" \
M_"v[64:67], a[172:175], a[12:15], v[64:67]\n" \
R_"a[204:207], v60 offset:3072\n" \
R_"a[208:211], v60 offset:8192\n" \
M_"v[68:71], a[188:191], a[12:15], v[32:35]\n" \
R_"a[212:215], v60 offset:9216\n" \
R_"a[216:219], v60 offset:10240\n" \
M_"v[68:71], a[184:187], a[8:11], v[68:71]\n" \
R_"a[220:223], v60 offset:11264\n" \
"v_mov_b32_e32 v56, s62\n" \
M_"v[68:71], a[180:183], a[4:7], v[68:71]\n" \
"v_readlane_b32 s68, v11, 0\n" \
"s_mov_b32 m0, s20\n" \
M_"v[68:71], a[176:179], a[0:3], v[68:71]\n" \
"global_load_lds_dword v[16:17], off\n" \
M_"v[76:79], a[176:179], a[16:19], v[36:39]\n" \
E_"v66, v66\n" \
M_"v[76:79], a[180:183], a[20:23], v[76:79]\n" \
E_"v67, v67\n" \
M_"v[76:79], a[184:187], a[24:27], v[76:79]\n" \
E_"v64, v64\n" \
M_"v[76:79], a[188:191], a[28:31], v[76:79]\n" \
E_"v70, v70\n" \
M_"v[72:75], a[172:175], a[28:31], v[36:39]\n" \
E_"v71, v71\n" \
M_"v[72:75], a[168:171], a[24:27], v[72:75]\n" \
E_"v68, v68\n" \
M_"v[72:75], a[164:167], a[20:23], v[72:75]\n" \
E_"v69, v69\n" \
M_"v[72:75], a[160:163], a[16:19], v[72:75]\n" \
C_"v71, v70, v71\n" \
"v_accvgpr_read_b32 v28, a124\n" \
M_"v[80:83], a[160:163], a[32:35], v[40:43]\n" \
E_"v65, v65\n" \
M_"v[80:83], a[164:167], a[36:39], v[80:83]\n" \
E_"v76, v76\n" \
M_"v[80:83], a[168:171], a[40:43], v[80:83]\n" \
E_"v77, v77\n" \
M_"v[80:83], a[172:175], a[44:47], v[80:83]\n" \
E_"v72, v72\n" \
M_"v[84:87], a[188:191], a[44:47], v[40:43]\n" \
E_"v73, v73\n" \
M_"v[84:87], a[184:187], a[40:43], v[84:87]\n" \
E_"v74, v74\n" \
M_"v[84:87], a[180:183], a[36:39], v[84:87]\n" \
E_"v75, v75\n" \
M_"v[84:87], a[176:179], a[32:35], v[84:87]\n" \
C_"v72, v72, v73\n" \
"v_accvgpr_read_b32 v29, a125\n" \
M_"v[92:95], a[176:179], a[48:51], v[44:47]\n" \
E_"v78, v78\n" \
M_"v[92:95], a[180:183], a[52:55], v[92:95]\n" \
"s_waitcnt vmcnt(3)\n" \
"v_cmp_ne_u32_e32 vcc, s62, v57\n" \
M_"v[92:95], a[184:187], a[56:59], v[92:95]\n" \
E_"v79, v79\n" \
M_"v[92:95], a[188:191], a[60:63], v[92:95]\n" \
"v_accvgpr_read_b32 v21, a126\n" \
C_"v70, v68, v69\n" \
M_"v[88:91], a[172:175], a[60:63], v[44:47]\n" \
"s_cbranch_vccnz L_skb_%=\n" \
"L_rkb_%=:\n" \
C_"v73, v74, v75\n" \
"s_mov_b64 exec, 1\n" \
"buffer_store_dword v56, off, s[56:59], s68 offset:0 sc1\n" \
"s_mov_b64 exec, -1\n" \
M_"v[88:91], a[168:171], a[56:59], v[88:91]\n" \
"v_mov_b32_e32 v62, 0\n" \
"s_mov_b32 s64, 0x8000\n" \
M_"v[88:91], a[164:167], a[52:55], v[88:91]\n" \
"v_add_u32_e32 v50, 0x6000, v55\n" \
"v_add_u32_e32 v51, 0xa000, v55\n" \
M_"v[88:91], a[160:163], a[48:51], v[88:91]\n" \
"v_add_u32_e32 v53, 0x10000, v55\n" \
"v_add_u32_e32 v48, 0xc000, v20\n" \
"s_waitcnt vmcnt(5) lgkmcnt(0)\n" \
"s_barrier\n" \
"v_add_u32_e32 v49, s17, v20\n" \
"v_accvgpr_write_b32 a124, v96\n" \
"v_accvgpr_write_b32 a125, v97\n" \
"v_accvgpr_write_b32 a126, v98\n" \
"v_accvgpr_write_b32 a127, v99\n" \
"v_accvgpr_write_b32 a224, v100\n" \
"v_accvgpr_write_b32 a225, v101\n" \
"v_accvgpr_write_b32 a226, v102\n" \
"v_accvgpr_write_b32 a227, v103\n" \
"v_accvgpr_write_b32 a228, v104\n" \
"v_accvgpr_write_b32 a229, v105\n" \
"v_accvgpr_write_b32 a230, v106\n" \
"v_accvgpr_write_b32 a231, v107\n" \
"v_accvgpr_write_b32 a232, v108\n" \
"v_accvgpr_write_b32 a233, v109\n" \
"v_accvgpr_write_b32 a234, v110\n" \
"v_accvgpr_write_b32 a235, v111\n" \
C_"v69, v66, v67\n" \
C_"v68, v64, v65\n" \
C_"v74, v76, v77\n" \
C_"v75, v78, v79\n" \
"s_sub_u32 s66, s18, 1\n" \
"s_cbranch_scc1 L_tr_%=\n" \
"s_branch L_1_%=\n" \
".fill (L_ab + 1528 - .) / 4, 4, 0xBF800000\n" \
"L_tL_1_%=:\n" \
"s_waitcnt vmcnt(4) lgkmcnt(0)\n" \
"v_smfmac_f32_16x16x64_bf16 a[100:103], a[104:107], v[116:123], v21\n" \
"s_barrier\n" \
"L_1_%=:\n" \
M_"v[96:99], a[128:131], a[0:3], v[32:35]\n" \
"s_xor_b32 m0, s65, 0x12000\n" \
R_"a[160:163], v49 offset:0\n" \
M_"v[96:99], a[132:135], a[4:7], v[96:99]\n" \
B_"v50, s[8:11], s64 offen offset:0 sc1 lds\n" \
M_"v[96:99], a[136:139], a[8:11], v[96:99]\n" \
R_"a[164:167], v49 offset:1024\n" \
R_"a[168:171], v49 offset:2048\n" \
M_"v[96:99], a[140:143], a[12:15], v[96:99]\n" \
R_"a[172:175], v49 offset:3072\n" \
R_"a[176:179], v49 offset:4096\n" \
M_"v[100:103], a[156:159], a[12:15], v[32:35]\n" \
R_"a[180:183], v49 offset:5120\n" \
R_"a[184:187], v49 offset:6144\n" \
M_"v[100:103], a[152:155], a[8:11], v[100:103]\n" \
"v_exp_f32_e64 v86, v86\n" \
M_"v[100:103], a[148:151], a[4:7], v[100:103]\n" \
"v_exp_f32_e64 v87, v87\n" \
M_"v[100:103], a[144:147], a[0:3], v[100:103]\n" \
R_"a[188:191], v49 offset:7168\n" \
C_"v87, v86, v87\n" \
M_"v[108:111], a[144:147], a[16:19], v[36:39]\n" \
"v_exp_f32_e64 v84, v84\n" \
M_"v[108:111], a[148:151], a[20:23], v[108:111]\n" \
"v_exp_f32_e64 v85, v85\n" \
M_"v[108:111], a[152:155], a[24:27], v[108:111]\n" \
"v_exp_f32_e64 v82, v82\n" \
M_"v[108:111], a[156:159], a[28:31], v[108:111]\n" \
"v_exp_f32_e64 v83, v83\n" \
M_"v[104:107], a[140:143], a[28:31], v[36:39]\n" \
"v_exp_f32_e64 v80, v80\n" \
M_"v[104:107], a[136:139], a[24:27], v[104:107]\n" \
"v_exp_f32_e64 v81, v81\n" \
M_"v[104:107], a[132:135], a[20:23], v[104:107]\n" \
"v_exp_f32_e64 v88, v88\n" \
M_"v[104:107], a[128:131], a[16:19], v[104:107]\n" \
B_"v50, s[8:11], s64 offen offset:1024 sc1 lds\n" \
M_"v[112:115], a[128:131], a[32:35], v[40:43]\n" \
"s_xor_b32 m0, s65, 0x6000\n" \
C_"v86, v84, v85\n" \
M_"v[112:115], a[132:135], a[36:39], v[112:115]\n" \
C_"v85, v82, v83\n" \
C_"v84, v80, v81\n" \
M_"v[112:115], a[136:139], a[40:43], v[112:115]\n" \
"v_exp_f32_e64 v89, v89\n" \
M_"v[112:115], a[140:143], a[44:47], v[112:115]\n" \
"v_exp_f32_e64 v90, v90\n" \
M_"v[116:119], a[156:159], a[44:47], v[40:43]\n" \
"v_exp_f32_e64 v91, v91\n" \
M_"v[116:119], a[152:155], a[40:43], v[116:119]\n" \
"v_exp_f32_e64 v92, v92\n" \
M_"v[116:119], a[148:151], a[36:39], v[116:119]\n" \
"v_exp_f32_e64 v93, v93\n" \
M_"v[116:119], a[144:147], a[32:35], v[116:119]\n" \
"v_exp_f32_e64 v94, v94\n" \
M_"v[124:127], a[144:147], a[48:51], v[44:47]\n" \
C_"v88, v88, v89\n" \
C_"v89, v90, v91\n" \
M_"v[124:127], a[148:151], a[52:55], v[124:127]\n" \
"v_exp_f32_e64 v95, v95\n" \
M_"v[124:127], a[152:155], a[56:59], v[124:127]\n" \
C_"v90, v92, v93\n" \
C_"v91, v94, v95\n" \
M_"v[124:127], a[156:159], a[60:63], v[124:127]\n" \
R_"a[144:147], v48 offset:4096\n" \
R_"a[148:151], v48 offset:5120\n" \
M_"v[120:123], a[140:143], a[60:63], v[44:47]\n" \
R_"a[152:155], v48 offset:6144\n" \
R_"a[140:143], v48 offset:3072\n" \
M_"v[120:123], a[136:139], a[56:59], v[120:123]\n" \
R_"a[136:139], v48 offset:2048\n" \
R_"a[156:159], v48 offset:7168\n" \
M_"v[120:123], a[132:135], a[52:55], v[120:123]\n" \
B_"v51, s[8:11], s64 offen offset:0 sc1 lds\n" \
M_"v[120:123], a[128:131], a[48:51], v[120:123]\n" \
R_"a[128:131], v48 offset:0\n" \
"s_waitcnt lgkmcnt(7)\n" \
M_"v[128:131], a[160:163], v[68:71], v[128:131]\n" \
E_"v102, v102\n" \
M_"v[160:163], a[160:163], v[72:75], v[160:163]\n" \
"v_exp_f32_e64 v103, v103\n" \
M_"v[132:135], a[164:167], v[68:71], v[132:135]\n" \
R_"a[132:135], v48 offset:1024\n" \
C_"v103, v102, v103\n" \
M_"v[164:167], a[164:167], v[72:75], v[164:167]\n" \
"v_exp_f32_e64 v100, v100\n" \
M_"v[136:139], a[168:171], v[68:71], v[136:139]\n" \
"v_exp_f32_e64 v101, v101\n" \
M_"v[168:171], a[168:171], v[72:75], v[168:171]\n" \
"v_exp_f32_e64 v98, v98\n" \
M_"v[140:143], a[172:175], v[68:71], v[140:143]\n" \
"v_exp_f32_e64 v99, v99\n" \
M_"v[172:175], a[172:175], v[72:75], v[172:175]\n" \
"v_exp_f32_e64 v96, v96\n" \
M_"v[144:147], a[176:179], v[68:71], v[144:147]\n" \
"v_exp_f32_e64 v97, v97\n" \
M_"v[176:179], a[176:179], v[72:75], v[176:179]\n" \
"v_exp_f32_e64 v104, v104\n" \
M_"v[148:151], a[180:183], v[68:71], v[148:151]\n" \
"v_exp_f32_e64 v105, v105\n" \
M_"v[180:183], a[180:183], v[72:75], v[180:183]\n" \
C_"v102, v100, v101\n" \
C_"v101, v98, v99\n" \
M_"v[152:155], a[184:187], v[68:71], v[152:155]\n" \
C_"v100, v96, v97\n" \
C_"v104, v104, v105\n" \
M_"v[184:187], a[184:187], v[72:75], v[184:187]\n" \
B_"v51, s[8:11], s64 offen offset:1024 sc1 lds\n" \
M_"v[156:159], a[188:191], v[68:71], v[156:159]\n" \
"v_exp_f32_e64 v106, v106\n" \
M_"v[188:191], a[188:191], v[72:75], v[188:191]\n" \
"v_exp_f32_e64 v107, v107\n" \
"v_smfmac_f32_16x16x64_bf16 a[96:99], a[104:107], v[68:75], v21\n" \
"v_exp_f32_e64 v108, v108\n" \
M_"v[220:223], a[188:191], v[84:87], v[220:223]\n" \
"v_exp_f32_e64 v109, v109\n" \
M_"v[252:255], a[188:191], v[88:91], v[252:255]\n" \
"v_exp_f32_e64 v110, v110\n" \
M_"v[216:219], a[184:187], v[84:87], v[216:219]\n" \
"v_exp_f32_e64 v111, v111\n" \
M_"v[248:251], a[184:187], v[88:91], v[248:251]\n" \
"v_exp_f32_e64 v118, v118\n" \
M_"v[212:215], a[180:183], v[84:87], v[212:215]\n" \
"v_exp_f32_e64 v119, v119\n" \
M_"v[244:247], a[180:183], v[88:91], v[244:247]\n" \
C_"v105, v106, v107\n" \
C_"v106, v108, v109\n" \
M_"v[208:211], a[176:179], v[84:87], v[208:211]\n" \
C_"v107, v110, v111\n" \
C_"v119, v118, v119\n" \
M_"v[240:243], a[176:179], v[88:91], v[240:243]\n" \
"v_exp_f32_e64 v116, v116\n" \
M_"v[204:207], a[172:175], v[84:87], v[204:207]\n" \
"v_exp_f32_e64 v117, v117\n" \
M_"v[236:239], a[172:175], v[88:91], v[236:239]\n" \
"v_exp_f32_e64 v114, v114\n" \
M_"v[200:203], a[168:171], v[84:87], v[200:203]\n" \
"v_exp_f32_e64 v115, v115\n" \
M_"v[232:235], a[168:171], v[88:91], v[232:235]\n" \
"v_exp_f32_e64 v112, v112\n" \
M_"v[196:199], a[164:167], v[84:87], v[196:199]\n" \
"v_exp_f32_e64 v113, v113\n" \
M_"v[228:231], a[164:167], v[88:91], v[228:231]\n" \
"v_exp_f32_e64 v120, v120\n" \
M_"v[192:195], a[160:163], v[84:87], v[192:195]\n" \
"v_exp_f32_e64 v121, v121\n" \
M_"v[224:227], a[160:163], v[88:91], v[224:227]\n" \
C_"v118, v116, v117\n" \
"s_waitcnt vmcnt(4) lgkmcnt(0)\n" \
"v_smfmac_f32_16x16x64_bf16 a[100:103], a[104:107], v[84:91], v21\n" \
"s_barrier\n" \
M_"v[64:67], a[128:131], a[0:3], v[32:35]\n" \
"s_xor_b32 m0, s65, 0x14000\n" \
R_"a[160:163], v49 offset:8192\n" \
M_"v[64:67], a[132:135], a[4:7], v[64:67]\n" \
B_"v52, s[8:11], s64 offen offset:0 sc1 lds\n" \
M_"v[64:67], a[136:139], a[8:11], v[64:67]\n" \
R_"a[164:167], v49 offset:9216\n" \
R_"a[168:171], v49 offset:10240\n" \
M_"v[64:67], a[140:143], a[12:15], v[64:67]\n" \
R_"a[172:175], v49 offset:11264\n" \
R_"a[176:179], v49 offset:12288\n" \
M_"v[68:71], a[156:159], a[12:15], v[32:35]\n" \
R_"a[180:183], v49 offset:13312\n" \
R_"a[184:187], v49 offset:14336\n" \
M_"v[68:71], a[152:155], a[8:11], v[68:71]\n" \
R_"a[188:191], v49 offset:15360\n" \
C_"v117, v114, v115\n" \
M_"v[68:71], a[148:151], a[4:7], v[68:71]\n" \
C_"v116, v112, v113\n" \
C_"v120, v120, v121\n" \
M_"v[68:71], a[144:147], a[0:3], v[68:71]\n" \
"v_exp_f32_e64 v122, v122\n" \
M_"v[76:79], a[144:147], a[16:19], v[36:39]\n" \
"v_exp_f32_e64 v123, v123\n" \
M_"v[76:79], a[148:151], a[20:23], v[76:79]\n" \
"v_exp_f32_e64 v124, v124\n" \
M_"v[76:79], a[152:155], a[24:27], v[76:79]\n" \
"v_exp_f32_e64 v125, v125\n" \
M_"v[76:79], a[156:159], a[28:31], v[76:79]\n" \
"v_exp_f32_e64 v126, v126\n" \
M_"v[72:75], a[140:143], a[28:31], v[36:39]\n" \
"v_exp_f32_e64 v127, v127\n" \
M_"v[72:75], a[136:139], a[24:27], v[72:75]\n" \
C_"v121, v122, v123\n" \
C_"v122, v124, v125\n" \
M_"v[72:75], a[132:135], a[20:23], v[72:75]\n" \
C_"v123, v126, v127\n" \
"v_add_u32_e32 v49, 0x10000, v48\n" \
M_"v[72:75], a[128:131], a[16:19], v[72:75]\n" \
B_"v52, s[8:11], s64 offen offset:1024 sc1 lds\n" \
M_"v[80:83], a[128:131], a[32:35], v[40:43]\n" \
"v_exp_f32_e64 v70, v70\n" \
M_"v[80:83], a[132:135], a[36:39], v[80:83]\n" \
"v_exp_f32_e64 v71, v71\n" \
M_"v[80:83], a[136:139], a[40:43], v[80:83]\n" \
"s_mov_b32 m0, s65\n" \
C_"v71, v70, v71\n" \
M_"v[80:83], a[140:143], a[44:47], v[80:83]\n" \
E_"v72, v72\n" \
M_"v[84:87], a[156:159], a[44:47], v[40:43]\n" \
"v_exp_f32_e64 v73, v73\n" \
M_"v[84:87], a[152:155], a[40:43], v[84:87]\n" \
"v_exp_f32_e64 v68, v68\n" \
M_"v[84:87], a[148:151], a[36:39], v[84:87]\n" \
"v_exp_f32_e64 v69, v69\n" \
M_"v[84:87], a[144:147], a[32:35], v[84:87]\n" \
"v_exp_f32_e64 v74, v74\n" \
M_"v[92:95], a[144:147], a[48:51], v[44:47]\n" \
R_"a[144:147], v48 offset:12288\n" \
C_"v72, v72, v73\n" \
M_"v[92:95], a[148:151], a[52:55], v[92:95]\n" \
R_"a[148:151], v48 offset:13312\n" \
C_"v70, v68, v69\n" \
M_"v[92:95], a[152:155], a[56:59], v[92:95]\n" \
R_"a[152:155], v48 offset:14336\n" \
"s_xor_b32 s65, s65, 0x4000\n" \
M_"v[92:95], a[156:159], a[60:63], v[92:95]\n" \
"v_exp_f32_e64 v75, v75\n" \
M_"v[88:91], a[140:143], a[60:63], v[44:47]\n" \
R_"a[140:143], v48 offset:11264\n" \
R_"a[156:159], v48 offset:15360\n" \
M_"v[88:91], a[136:139], a[56:59], v[88:91]\n" \
R_"a[136:139], v48 offset:10240\n" \
C_"v73, v74, v75\n" \
M_"v[88:91], a[132:135], a[52:55], v[88:91]\n" \
B_"v53, s[8:11], s64 offen offset:0 sc1 lds\n" \
M_"v[88:91], a[128:131], a[48:51], v[88:91]\n" \
R_"a[128:131], v48 offset:8192\n" \
"s_waitcnt lgkmcnt(7)\n" \
M_"v[128:131], a[160:163], v[100:103], v[128:131]\n" \
E_"v66, v66\n" \
M_"v[160:163], a[160:163], v[104:107], v[160:163]\n" \
"v_exp_f32_e64 v67, v67\n" \
M_"v[132:135], a[164:167], v[100:103], v[132:135]\n" \
R_"a[132:135], v48 offset:9216\n" \
C_"v69, v66, v67\n" \
M_"v[164:167], a[164:167], v[104:107], v[164:167]\n" \
"v_exp_f32_e64 v64, v64\n" \
M_"v[136:139], a[168:171], v[100:103], v[136:139]\n" \
"v_exp_f32_e64 v65, v65\n" \
M_"v[168:171], a[168:171], v[104:107], v[168:171]\n" \
"v_exp_f32_e64 v76, v76\n" \
M_"v[140:143], a[172:175], v[100:103], v[140:143]\n" \
"v_exp_f32_e64 v77, v77\n" \
M_"v[172:175], a[172:175], v[104:107], v[172:175]\n" \
"v_exp_f32_e64 v78, v78\n" \
M_"v[144:147], a[176:179], v[100:103], v[144:147]\n" \
"v_exp_f32_e64 v79, v79\n" \
M_"v[176:179], a[176:179], v[104:107], v[176:179]\n" \
C_"v68, v64, v65\n" \
C_"v74, v76, v77\n" \
M_"v[148:151], a[180:183], v[100:103], v[148:151]\n" \
C_"v75, v78, v79\n" \
"v_xor_b32_e32 v48, 0x4000, v48\n" \
M_"v[180:183], a[180:183], v[104:107], v[180:183]\n" \
M_"v[152:155], a[184:187], v[100:103], v[152:155]\n" \
M_"v[184:187], a[184:187], v[104:107], v[184:187]\n" \
B_"v53, s[8:11], s64 offen offset:1024 sc1 lds\n" \
M_"v[156:159], a[188:191], v[100:103], v[156:159]\n" \
"s_add_u32 s64, s64, 0x8000\n" \
"s_sub_u32 s66, s66, 1\n" \
"s_nop 0\n" \
M_"v[188:191], a[188:191], v[104:107], v[188:191]\n" \
"v_smfmac_f32_16x16x64_bf16 a[96:99], a[104:107], v[100:107], v21\n" \
M_"v[220:223], a[188:191], v[116:119], v[220:223]\n" \
M_"v[252:255], a[188:191], v[120:123], v[252:255]\n" \
M_"v[216:219], a[184:187], v[116:119], v[216:219]\n" \
M_"v[248:251], a[184:187], v[120:123], v[248:251]\n" \
M_"v[212:215], a[180:183], v[116:119], v[212:215]\n" \
M_"v[244:247], a[180:183], v[120:123], v[244:247]\n" \
M_"v[208:211], a[176:179], v[116:119], v[208:211]\n" \
M_"v[240:243], a[176:179], v[120:123], v[240:243]\n" \
M_"v[204:207], a[172:175], v[116:119], v[204:207]\n" \
M_"v[236:239], a[172:175], v[120:123], v[236:239]\n" \
M_"v[200:203], a[168:171], v[116:119], v[200:203]\n" \
M_"v[232:235], a[168:171], v[120:123], v[232:235]\n" \
M_"v[196:199], a[164:167], v[116:119], v[196:199]\n" \
M_"v[228:231], a[164:167], v[120:123], v[228:231]\n" \
M_"v[192:195], a[160:163], v[116:119], v[192:195]\n" \
M_"v[224:227], a[160:163], v[120:123], v[224:227]\n" \
"s_cbranch_scc0 L_tL_1_%=\n" \
"s_waitcnt vmcnt(4) lgkmcnt(0)\n" \
"v_smfmac_f32_16x16x64_bf16 a[100:103], a[104:107], v[116:123], v21\n" \
"s_barrier\n" \
"L_tr_%=:\n" \
"s_setpc_b64 s[28:29]\n" \
".fill (L_ab + 4096 - .) / 4, 4, 0xBF800000\n" \
".fill (L_ab + 4096 - .) / 4, 4, 0xBF800000\n" \
"L_ap1:\n" \
"s_setpc_b64 s[88:89]\n" \
"L_gtr:\n" \
M_"v[96:99], a[128:131], a[0:3], v[32:35]\n" \
"s_xor_b32 m0, s65, 0x12000\n" \
"s_mov_b32 s65, s13\n" \
M_"v[96:99], a[132:135], a[4:7], v[96:99]\n" \
B_"v50, s[8:11], s64 offen offset:0 sc1 lds\n" \
M_"v[96:99], a[136:139], a[8:11], v[96:99]\n" \
"v_add_u32_e32 v63, s15, v20\n" \
"s_add_u32 s67, s14, 40960\n" \
M_"v[96:99], a[140:143], a[12:15], v[96:99]\n" \
R_"a[160:163], v49 offset:0\n" \
R_"a[164:167], v49 offset:1024\n" \
M_"v[100:103], a[156:159], a[12:15], v[32:35]\n" \
B_"v50, s[8:11], s64 offen offset:1024 sc1 lds\n" \
M_"v[100:103], a[152:155], a[8:11], v[100:103]\n" \
"v_add_u32_e32 v50, 0x6000, v63\n" \
"s_xor_b32 m0, s65, 0x8000\n" \
M_"v[100:103], a[148:151], a[4:7], v[100:103]\n" \
R_"a[168:171], v49 offset:2048\n" \
R_"a[172:175], v49 offset:3072\n" \
M_"v[100:103], a[144:147], a[0:3], v[100:103]\n" \
R_"a[176:179], v49 offset:4096\n" \
R_"a[180:183], v49 offset:5120\n" \
M_"v[108:111], a[144:147], a[16:19], v[36:39]\n" \
B_"v50, s[8:11], s67 offen offset:0 sc1 lds\n" \
M_"v[108:111], a[148:151], a[20:23], v[108:111]\n" \
R_"a[184:187], v49 offset:6144\n" \
R_"a[188:191], v49 offset:7168\n" \
M_"v[108:111], a[152:155], a[24:27], v[108:111]\n" \
"s_mov_b32 s64, s14\n" \
"v_add_u32_e32 v51, 0x12000, v63\n" \
M_"v[108:111], a[156:159], a[28:31], v[108:111]\n" \
E_"v86, v86\n" \
M_"v[104:107], a[140:143], a[28:31], v[36:39]\n" \
B_"v50, s[8:11], s67 offen offset:1024 sc1 lds\n" \
M_"v[104:107], a[136:139], a[24:27], v[104:107]\n" \
E_"v87, v87\n" \
M_"v[104:107], a[132:135], a[20:23], v[104:107]\n" \
E_"v84, v84\n" \
M_"v[104:107], a[128:131], a[16:19], v[104:107]\n" \
E_"v85, v85\n" \
M_"v[112:115], a[128:131], a[32:35], v[40:43]\n" \
C_"v87, v86, v87\n" \
C_"v86, v84, v85\n" \
M_"v[112:115], a[132:135], a[36:39], v[112:115]\n" \
B_"v50, s[8:11], s67 offen offset:2048 sc1 lds\n" \
M_"v[112:115], a[136:139], a[40:43], v[112:115]\n" \
E_"v82, v82\n" \
M_"v[112:115], a[140:143], a[44:47], v[112:115]\n" \
E_"v83, v83\n" \
M_"v[116:119], a[156:159], a[44:47], v[40:43]\n" \
E_"v80, v80\n" \
M_"v[116:119], a[152:155], a[40:43], v[116:119]\n" \
B_"v50, s[8:11], s67 offen offset:3072 sc1 lds\n" \
M_"v[116:119], a[148:151], a[36:39], v[116:119]\n" \
"s_xor_b32 m0, s65, 0xa000\n" \
C_"v85, v82, v83\n" \
M_"v[116:119], a[144:147], a[32:35], v[116:119]\n" \
E_"v81, v81\n" \
M_"v[124:127], a[144:147], a[48:51], v[44:47]\n" \
C_"v84, v80, v81\n" \
"v_readlane_b32 s93, v11, 22\n" \
M_"v[124:127], a[148:151], a[52:55], v[124:127]\n" \
B_"v51, s[8:11], s64 offen offset:0 sc1 lds\n" \
M_"v[124:127], a[152:155], a[56:59], v[124:127]\n" \
E_"v88, v88\n" \
M_"v[124:127], a[156:159], a[60:63], v[124:127]\n" \
E_"v89, v89\n" \
M_"v[120:123], a[140:143], a[60:63], v[44:47]\n" \
E_"v90, v90\n" \
M_"v[120:123], a[136:139], a[56:59], v[120:123]\n" \
E_"v91, v91\n" \
M_"v[120:123], a[132:135], a[52:55], v[120:123]\n" \
B_"v51, s[8:11], s64 offen offset:1024 sc1 lds\n" \
M_"v[120:123], a[128:131], a[48:51], v[120:123]\n" \
C_"v88, v88, v89\n" \
"s_waitcnt lgkmcnt(0)\n" \
M_"v[128:131], a[160:163], v[68:71], v[128:131]\n" \
E_"v92, v92\n" \
M_"v[160:163], a[160:163], v[72:75], v[160:163]\n" \
E_"v93, v93\n" \
M_"v[132:135], a[164:167], v[68:71], v[132:135]\n" \
B_"v51, s[8:11], s64 offen offset:2048 sc1 lds\n" \
M_"v[164:167], a[164:167], v[72:75], v[164:167]\n" \
E_"v94, v94\n" \
M_"v[136:139], a[168:171], v[68:71], v[136:139]\n" \
E_"v95, v95\n" \
M_"v[168:171], a[168:171], v[72:75], v[168:171]\n" \
C_"v89, v90, v91\n" \
C_"v90, v92, v93\n" \
M_"v[140:143], a[172:175], v[68:71], v[140:143]\n" \
B_"v51, s[8:11], s64 offen offset:3072 sc1 lds\n" \
M_"v[172:175], a[172:175], v[72:75], v[172:175]\n" \
C_"v91, v94, v95\n" \
"v_readlane_b32 s80, v11, 11\n" \
M_"v[144:147], a[176:179], v[68:71], v[144:147]\n" \
"v_readlane_b32 s81, v11, 12\n" \
"s_mov_b32 s82, 0x47fca0\n" \
M_"v[176:179], a[176:179], v[72:75], v[176:179]\n" \
"s_mov_b32 s83, 0x20000\n" \
"v_readlane_b32 s92, v11, 29\n" \
M_"v[148:151], a[180:183], v[68:71], v[148:151]\n" \
"s_add_u32 m0, s93, 0x0\n" \
R_"a[128:131], v48 offset:0\n" \
M_"v[180:183], a[180:183], v[72:75], v[180:183]\n" \
R_"a[144:147], v48 offset:4096\n" \
R_"a[132:135], v48 offset:1024\n" \
M_"v[152:155], a[184:187], v[68:71], v[152:155]\n" \
B_"v0, s[80:83], s92 offen offset:0 lds\n" \
M_"v[184:187], a[184:187], v[72:75], v[184:187]\n" \
R_"a[148:151], v48 offset:5120\n" \
R_"a[136:139], v48 offset:2048\n" \
M_"v[156:159], a[188:191], v[68:71], v[156:159]\n" \
R_"a[152:155], v48 offset:6144\n" \
R_"a[140:143], v48 offset:3072\n" \
M_"v[188:191], a[188:191], v[72:75], v[188:191]\n" \
R_"a[156:159], v48 offset:7168\n" \
"v_add_u32_e32 v52, 0x14000, v63\n" \
"v_smfmac_f32_16x16x64_bf16 a[96:99], a[104:107], v[68:75], v21\n" \
B_"v1, s[80:83], s92 offen offset:1024 lds\n" \
M_"v[220:223], a[188:191], v[84:87], v[220:223]\n" \
E_"v102, v102\n" \
M_"v[252:255], a[188:191], v[88:91], v[252:255]\n" \
E_"v103, v103\n" \
M_"v[216:219], a[184:187], v[84:87], v[216:219]\n" \
B_"v2, s[80:83], s92 offen offset:2048 lds\n" \
M_"v[248:251], a[184:187], v[88:91], v[248:251]\n" \
"v_add_u32_e32 v53, 0x20000, v63\n" \
C_"v103, v102, v103\n" \
M_"v[212:215], a[180:183], v[84:87], v[212:215]\n" \
E_"v100, v100\n" \
M_"v[244:247], a[180:183], v[88:91], v[244:247]\n" \
E_"v101, v101\n" \
M_"v[208:211], a[176:179], v[84:87], v[208:211]\n" \
B_"v3, s[80:83], s92 offen offset:3072 lds\n" \
M_"v[240:243], a[176:179], v[88:91], v[240:243]\n" \
E_"v98, v98\n" \
M_"v[204:207], a[172:175], v[84:87], v[204:207]\n" \
E_"v99, v99\n" \
M_"v[236:239], a[172:175], v[88:91], v[236:239]\n" \
E_"v96, v96\n" \
M_"v[200:203], a[168:171], v[84:87], v[200:203]\n" \
E_"v97, v97\n" \
M_"v[232:235], a[168:171], v[88:91], v[232:235]\n" \
E_"v104, v104\n" \
M_"v[196:199], a[164:167], v[84:87], v[196:199]\n" \
E_"v105, v105\n" \
M_"v[228:231], a[164:167], v[88:91], v[228:231]\n" \
C_"v102, v100, v101\n" \
C_"v101, v98, v99\n" \
M_"v[192:195], a[160:163], v[84:87], v[192:195]\n" \
C_"v100, v96, v97\n" \
C_"v104, v104, v105\n" \
M_"v[224:227], a[160:163], v[88:91], v[224:227]\n" \
E_"v106, v106\n" \
"v_smfmac_f32_16x16x64_bf16 a[100:103], a[104:107], v[84:91], v21\n" \
E_"v107, v107\n" \
"s_waitcnt vmcnt(14) lgkmcnt(0)\n" \
"s_barrier\n" \
M_"v[64:67], a[128:131], a[0:3], v[32:35]\n" \
"s_xor_b32 m0, s65, 0x18000\n" \
R_"a[160:163], v49 offset:8192\n" \
M_"v[64:67], a[132:135], a[4:7], v[64:67]\n" \
B_"v52, s[8:11], s64 offen offset:0 sc1 lds\n" \
M_"v[64:67], a[136:139], a[8:11], v[64:67]\n" \
R_"a[164:167], v49 offset:9216\n" \
R_"a[168:171], v49 offset:10240\n" \
M_"v[64:67], a[140:143], a[12:15], v[64:67]\n" \
R_"a[172:175], v49 offset:11264\n" \
R_"a[176:179], v49 offset:12288\n" \
M_"v[68:71], a[156:159], a[12:15], v[32:35]\n" \
B_"v52, s[8:11], s64 offen offset:1024 sc1 lds\n" \
M_"v[68:71], a[152:155], a[8:11], v[68:71]\n" \
R_"a[180:183], v49 offset:13312\n" \
R_"a[184:187], v49 offset:14336\n" \
M_"v[68:71], a[148:151], a[4:7], v[68:71]\n" \
E_"v108, v108\n" \
M_"v[68:71], a[144:147], a[0:3], v[68:71]\n" \
E_"v109, v109\n" \
M_"v[76:79], a[144:147], a[16:19], v[36:39]\n" \
E_"v110, v110\n" \
M_"v[76:79], a[148:151], a[20:23], v[76:79]\n" \
B_"v52, s[8:11], s64 offen offset:2048 sc1 lds\n" \
M_"v[76:79], a[152:155], a[24:27], v[76:79]\n" \
E_"v111, v111\n" \
M_"v[76:79], a[156:159], a[28:31], v[76:79]\n" \
C_"v105, v106, v107\n" \
C_"v106, v108, v109\n" \
M_"v[72:75], a[140:143], a[28:31], v[36:39]\n" \
C_"v107, v110, v111\n" \
R_"a[188:191], v49 offset:15360\n" \
M_"v[72:75], a[136:139], a[24:27], v[72:75]\n" \
B_"v52, s[8:11], s64 offen offset:3072 sc1 lds\n" \
M_"v[72:75], a[132:135], a[20:23], v[72:75]\n" \
"s_mov_b32 m0, s65\n" \
"v_add_u32_e32 v49, 0x10000, v48\n" \
M_"v[72:75], a[128:131], a[16:19], v[72:75]\n" \
E_"v118, v118\n" \
M_"v[80:83], a[128:131], a[32:35], v[40:43]\n" \
E_"v119, v119\n" \
M_"v[80:83], a[132:135], a[36:39], v[80:83]\n" \
E_"v116, v116\n" \
M_"v[80:83], a[136:139], a[40:43], v[80:83]\n" \
B_"v53, s[8:11], s64 offen offset:0 sc1 lds\n" \
M_"v[80:83], a[140:143], a[44:47], v[80:83]\n" \
E_"v117, v117\n" \
M_"v[84:87], a[156:159], a[44:47], v[40:43]\n" \
C_"v119, v118, v119\n" \
C_"v118, v116, v117\n" \
M_"v[84:87], a[152:155], a[40:43], v[84:87]\n" \
E_"v114, v114\n" \
M_"v[84:87], a[148:151], a[36:39], v[84:87]\n" \
E_"v115, v115\n" \
M_"v[84:87], a[144:147], a[32:35], v[84:87]\n" \
B_"v53, s[8:11], s64 offen offset:1024 sc1 lds\n" \
M_"v[92:95], a[144:147], a[48:51], v[44:47]\n" \
E_"v112, v112\n" \
M_"v[92:95], a[148:151], a[52:55], v[92:95]\n" \
E_"v113, v113\n" \
M_"v[92:95], a[152:155], a[56:59], v[92:95]\n" \
C_"v117, v114, v115\n" \
C_"v116, v112, v113\n" \
M_"v[92:95], a[156:159], a[60:63], v[92:95]\n" \
B_"v53, s[8:11], s64 offen offset:2048 sc1 lds\n" \
M_"v[88:91], a[140:143], a[60:63], v[44:47]\n" \
E_"v120, v120\n" \
M_"v[88:91], a[136:139], a[56:59], v[88:91]\n" \
E_"v121, v121\n" \
M_"v[88:91], a[132:135], a[52:55], v[88:91]\n" \
E_"v122, v122\n" \
M_"v[88:91], a[128:131], a[48:51], v[88:91]\n" \
C_"v120, v120, v121\n" \
"s_waitcnt lgkmcnt(0)\n" \
M_"v[128:131], a[160:163], v[100:103], v[128:131]\n" \
B_"v53, s[8:11], s64 offen offset:3072 sc1 lds\n" \
M_"v[160:163], a[160:163], v[104:107], v[160:163]\n" \
"s_add_u32 m0, s93, 0x1000\n" \
R_"a[128:131], v48 offset:8192\n" \
M_"v[132:135], a[164:167], v[100:103], v[132:135]\n" \
E_"v123, v123\n" \
M_"v[164:167], a[164:167], v[104:107], v[164:167]\n" \
E_"v124, v124\n" \
M_"v[136:139], a[168:171], v[100:103], v[136:139]\n" \
B_"v4, s[80:83], s92 offen offset:0 lds\n" \
M_"v[168:171], a[168:171], v[104:107], v[168:171]\n" \
E_"v125, v125\n" \
M_"v[140:143], a[172:175], v[100:103], v[140:143]\n" \
E_"v126, v126\n" \
M_"v[172:175], a[172:175], v[104:107], v[172:175]\n" \
E_"v127, v127\n" \
M_"v[144:147], a[176:179], v[100:103], v[144:147]\n" \
C_"v121, v122, v123\n" \
C_"v122, v124, v125\n" \
M_"v[176:179], a[176:179], v[104:107], v[176:179]\n" \
B_"v5, s[80:83], s92 offen offset:1024 lds\n" \
M_"v[148:151], a[180:183], v[100:103], v[148:151]\n" \
C_"v123, v126, v127\n" \
R_"a[144:147], v48 offset:12288\n" \
M_"v[180:183], a[180:183], v[104:107], v[180:183]\n" \
R_"a[132:135], v48 offset:9216\n" \
R_"a[148:151], v48 offset:13312\n" \
M_"v[152:155], a[184:187], v[100:103], v[152:155]\n" \
R_"a[136:139], v48 offset:10240\n" \
R_"a[152:155], v48 offset:14336\n" \
M_"v[184:187], a[184:187], v[104:107], v[184:187]\n" \
R_"a[140:143], v48 offset:11264\n" \
R_"a[156:159], v48 offset:15360\n" \
M_"v[156:159], a[188:191], v[100:103], v[156:159]\n" \
B_"v6, s[80:83], s92 offen offset:2048 lds\n" \
M_"v[188:191], a[188:191], v[104:107], v[188:191]\n" \
E_"v70, v70\n" \
"v_smfmac_f32_16x16x64_bf16 a[96:99], a[104:107], v[100:107], v21\n" \
E_"v71, v71\n" \
M_"v[220:223], a[188:191], v[116:119], v[220:223]\n" \
E_"v72, v72\n" \
M_"v[252:255], a[188:191], v[120:123], v[252:255]\n" \
B_"v7, s[80:83], s92 offen offset:3072 lds\n" \
M_"v[216:219], a[184:187], v[116:119], v[216:219]\n" \
"s_add_u32 m0, s93, 0x2000\n" \
C_"v71, v70, v71\n" \
M_"v[248:251], a[184:187], v[120:123], v[248:251]\n" \
E_"v73, v73\n" \
M_"v[212:215], a[180:183], v[116:119], v[212:215]\n" \
E_"v68, v68\n" \
M_"v[244:247], a[180:183], v[120:123], v[244:247]\n" \
E_"v69, v69\n" \
M_"v[208:211], a[176:179], v[116:119], v[208:211]\n" \
B_"v8, s[80:83], s92 offen offset:0 lds\n" \
M_"v[240:243], a[176:179], v[120:123], v[240:243]\n" \
E_"v74, v74\n" \
M_"v[204:207], a[172:175], v[116:119], v[204:207]\n" \
E_"v75, v75\n" \
M_"v[236:239], a[172:175], v[120:123], v[236:239]\n" \
E_"v66, v66\n" \
M_"v[200:203], a[168:171], v[116:119], v[200:203]\n" \
E_"v67, v67\n" \
M_"v[232:235], a[168:171], v[120:123], v[232:235]\n" \
E_"v64, v64\n" \
M_"v[196:199], a[164:167], v[116:119], v[196:199]\n" \
E_"v65, v65\n" \
M_"v[228:231], a[164:167], v[120:123], v[228:231]\n" \
C_"v72, v72, v73\n" \
C_"v70, v68, v69\n" \
M_"v[192:195], a[160:163], v[116:119], v[192:195]\n" \
E_"v76, v76\n" \
M_"v[224:227], a[160:163], v[120:123], v[224:227]\n" \
E_"v77, v77\n" \
"v_smfmac_f32_16x16x64_bf16 a[100:103], a[104:107], v[116:123], v21\n" \
E_"v78, v78\n" \
"s_waitcnt vmcnt(13) lgkmcnt(0)\n" \
"s_barrier\n" \
E_"v79, v79\n" \
C_"v73, v74, v75\n" \
C_"v69, v66, v67\n" \
C_"v68, v64, v65\n" \
C_"v74, v76, v77\n" \
C_"v75, v78, v79\n" \
"v_add_u32_e32 v48, s16, v20\n" \
"s_xor_b32 s65, s65, 0x8000\n" \
"s_add_u32 s64, s64, 0x10000\n" \
"s_getpc_b64 s[86:87]\n" \
"L_hpt_%=:\n" \
"s_add_u32 s86, s86, L_ap8-L_hpt_%=\n" \
"s_addc_u32 s87, s87, 0\n" \
"s_add_u32 s86, s86, s15\n" \
"s_addc_u32 s87, s87, 0\n" \
"s_mov_b64 exec, 0\n" \
"s_swappc_b64 s[88:89], s[86:87]\n" \
"s_mov_b64 exec, -1\n" \
"s_sub_u32 s66, s19, 1\n" \
"s_cbranch_scc1 L_la_%=\n" \
"s_branch L_2_%=\n" \
".fill (L_ab + 6400 - .) / 4, 4, 0xBF800000\n" \
"L_tL_2_%=:\n" \
"s_waitcnt vmcnt(8) lgkmcnt(0)\n" \
"v_smfmac_f32_16x16x64_bf16 a[100:103], a[104:107], v[116:123], v21\n" \
"s_barrier\n" \
"L_2_%=:\n" \
M_"v[96:99], a[128:131], a[64:67], v[32:35]\n" \
"s_xor_b32 m0, s65, 0x12000\n" \
R_"a[160:163], v49 offset:0\n" \
M_"v[96:99], a[132:135], a[68:71], v[96:99]\n" \
B_"v50, s[8:11], s64 offen offset:0 sc1 lds\n" \
M_"v[96:99], a[136:139], a[72:75], v[96:99]\n" \
R_"a[164:167], v49 offset:1024\n" \
R_"a[168:171], v49 offset:2048\n" \
M_"v[96:99], a[140:143], a[76:79], v[96:99]\n" \
R_"a[172:175], v49 offset:3072\n" \
R_"a[176:179], v49 offset:4096\n" \
M_"v[100:103], a[156:159], a[76:79], v[32:35]\n" \
R_"a[180:183], v49 offset:5120\n" \
R_"a[184:187], v49 offset:6144\n" \
M_"v[100:103], a[152:155], a[72:75], v[100:103]\n" \
"v_exp_f32_e64 v86, v86\n" \
M_"v[100:103], a[148:151], a[68:71], v[100:103]\n" \
"v_exp_f32_e64 v87, v87\n" \
M_"v[100:103], a[144:147], a[64:67], v[100:103]\n" \
B_"v50, s[8:11], s64 offen offset:1024 sc1 lds\n" \
M_"v[108:111], a[144:147], a[80:83], v[36:39]\n" \
R_"a[188:191], v49 offset:7168\n" \
C_"v87, v86, v87\n" \
M_"v[108:111], a[148:151], a[84:87], v[108:111]\n" \
"v_exp_f32_e64 v84, v84\n" \
M_"v[108:111], a[152:155], a[88:91], v[108:111]\n" \
"v_exp_f32_e64 v85, v85\n" \
M_"v[108:111], a[156:159], a[92:95], v[108:111]\n" \
"v_exp_f32_e64 v82, v82\n" \
M_"v[104:107], a[140:143], a[92:95], v[36:39]\n" \
"v_exp_f32_e64 v83, v83\n" \
M_"v[104:107], a[136:139], a[88:91], v[104:107]\n" \
"v_exp_f32_e64 v80, v80\n" \
M_"v[104:107], a[132:135], a[84:87], v[104:107]\n" \
"v_exp_f32_e64 v81, v81\n" \
M_"v[104:107], a[128:131], a[80:83], v[104:107]\n" \
B_"v50, s[8:11], s64 offen offset:2048 sc1 lds\n" \
M_"v[112:115], a[128:131], a[192:195], v[40:43]\n" \
"v_exp_f32_e64 v88, v88\n" \
M_"v[112:115], a[132:135], a[196:199], v[112:115]\n" \
"v_exp_f32_e64 v89, v89\n" \
M_"v[112:115], a[136:139], a[200:203], v[112:115]\n" \
C_"v86, v84, v85\n" \
C_"v85, v82, v83\n" \
M_"v[112:115], a[140:143], a[204:207], v[112:115]\n" \
C_"v84, v80, v81\n" \
C_"v88, v88, v89\n" \
M_"v[116:119], a[156:159], a[204:207], v[40:43]\n" \
"v_exp_f32_e64 v90, v90\n" \
M_"v[116:119], a[152:155], a[200:203], v[116:119]\n" \
"v_exp_f32_e64 v91, v91\n" \
M_"v[116:119], a[148:151], a[196:199], v[116:119]\n" \
B_"v50, s[8:11], s64 offen offset:3072 sc1 lds\n" \
M_"v[116:119], a[144:147], a[192:195], v[116:119]\n" \
"s_xor_b32 m0, s65, 0xa000\n" \
C_"v89, v90, v91\n" \
M_"v[124:127], a[144:147], a[208:211], v[44:47]\n" \
"v_exp_f32_e64 v92, v92\n" \
M_"v[124:127], a[148:151], a[212:215], v[124:127]\n" \
"v_exp_f32_e64 v93, v93\n" \
M_"v[124:127], a[152:155], a[216:219], v[124:127]\n" \
"v_exp_f32_e64 v94, v94\n" \
M_"v[124:127], a[156:159], a[220:223], v[124:127]\n" \
"v_exp_f32_e64 v95, v95\n" \
M_"v[120:123], a[140:143], a[220:223], v[44:47]\n" \
C_"v90, v92, v93\n" \
C_"v91, v94, v95\n" \
M_"v[120:123], a[136:139], a[216:219], v[120:123]\n" \
R_"a[144:147], v48 offset:4096\n" \
R_"a[148:151], v48 offset:5120\n" \
M_"v[120:123], a[132:135], a[212:215], v[120:123]\n" \
B_"v51, s[8:11], s64 offen offset:0 sc1 lds\n" \
M_"v[120:123], a[128:131], a[208:211], v[120:123]\n" \
R_"a[128:131], v48 offset:0\n" \
"s_waitcnt lgkmcnt(3)\n" \
M_"v[128:131], a[160:163], v[68:71], v[128:131]\n" \
R_"a[132:135], v48 offset:1024\n" \
R_"a[136:139], v48 offset:2048\n" \
M_"v[160:163], a[160:163], v[72:75], v[160:163]\n" \
R_"a[152:155], v48 offset:6144\n" \
R_"a[140:143], v48 offset:3072\n" \
M_"v[132:135], a[164:167], v[68:71], v[132:135]\n" \
E_"v102, v102\n" \
M_"v[164:167], a[164:167], v[72:75], v[164:167]\n" \
"v_exp_f32_e64 v103, v103\n" \
M_"v[136:139], a[168:171], v[68:71], v[136:139]\n" \
R_"a[156:159], v48 offset:7168\n" \
C_"v103, v102, v103\n" \
M_"v[168:171], a[168:171], v[72:75], v[168:171]\n" \
B_"v51, s[8:11], s64 offen offset:1024 sc1 lds\n" \
M_"v[140:143], a[172:175], v[68:71], v[140:143]\n" \
"v_exp_f32_e64 v100, v100\n" \
M_"v[172:175], a[172:175], v[72:75], v[172:175]\n" \
"v_exp_f32_e64 v101, v101\n" \
M_"v[144:147], a[176:179], v[68:71], v[144:147]\n" \
"v_exp_f32_e64 v98, v98\n" \
M_"v[176:179], a[176:179], v[72:75], v[176:179]\n" \
"v_exp_f32_e64 v99, v99\n" \
M_"v[148:151], a[180:183], v[68:71], v[148:151]\n" \
"v_exp_f32_e64 v96, v96\n" \
M_"v[180:183], a[180:183], v[72:75], v[180:183]\n" \
"v_exp_f32_e64 v97, v97\n" \
M_"v[152:155], a[184:187], v[68:71], v[152:155]\n" \
"v_exp_f32_e64 v104, v104\n" \
M_"v[184:187], a[184:187], v[72:75], v[184:187]\n" \
B_"v51, s[8:11], s64 offen offset:2048 sc1 lds\n" \
M_"v[156:159], a[188:191], v[68:71], v[156:159]\n" \
"v_exp_f32_e64 v105, v105\n" \
M_"v[188:191], a[188:191], v[72:75], v[188:191]\n" \
C_"v102, v100, v101\n" \
C_"v101, v98, v99\n" \
"v_smfmac_f32_16x16x64_bf16 a[96:99], a[104:107], v[68:75], v21\n" \
C_"v100, v96, v97\n" \
C_"v104, v104, v105\n" \
M_"v[220:223], a[188:191], v[84:87], v[220:223]\n" \
"v_exp_f32_e64 v106, v106\n" \
M_"v[252:255], a[188:191], v[88:91], v[252:255]\n" \
"v_exp_f32_e64 v107, v107\n" \
M_"v[216:219], a[184:187], v[84:87], v[216:219]\n" \
"v_exp_f32_e64 v108, v108\n" \
M_"v[248:251], a[184:187], v[88:91], v[248:251]\n" \
B_"v51, s[8:11], s64 offen offset:3072 sc1 lds\n" \
M_"v[212:215], a[180:183], v[84:87], v[212:215]\n" \
"v_exp_f32_e64 v109, v109\n" \
M_"v[244:247], a[180:183], v[88:91], v[244:247]\n" \
"v_exp_f32_e64 v110, v110\n" \
M_"v[208:211], a[176:179], v[84:87], v[208:211]\n" \
"v_exp_f32_e64 v111, v111\n" \
M_"v[240:243], a[176:179], v[88:91], v[240:243]\n" \
"v_exp_f32_e64 v118, v118\n" \
M_"v[204:207], a[172:175], v[84:87], v[204:207]\n" \
"v_exp_f32_e64 v119, v119\n" \
M_"v[236:239], a[172:175], v[88:91], v[236:239]\n" \
C_"v105, v106, v107\n" \
C_"v106, v108, v109\n" \
M_"v[200:203], a[168:171], v[84:87], v[200:203]\n" \
C_"v107, v110, v111\n" \
C_"v119, v118, v119\n" \
M_"v[232:235], a[168:171], v[88:91], v[232:235]\n" \
"v_exp_f32_e64 v116, v116\n" \
M_"v[196:199], a[164:167], v[84:87], v[196:199]\n" \
"v_exp_f32_e64 v117, v117\n" \
M_"v[228:231], a[164:167], v[88:91], v[228:231]\n" \
"v_exp_f32_e64 v114, v114\n" \
M_"v[192:195], a[160:163], v[84:87], v[192:195]\n" \
"v_exp_f32_e64 v115, v115\n" \
M_"v[224:227], a[160:163], v[88:91], v[224:227]\n" \
C_"v118, v116, v117\n" \
"s_waitcnt vmcnt(8) lgkmcnt(0)\n" \
"v_smfmac_f32_16x16x64_bf16 a[100:103], a[104:107], v[84:91], v21\n" \
"s_barrier\n" \
M_"v[64:67], a[128:131], a[64:67], v[32:35]\n" \
"s_xor_b32 m0, s65, 0x18000\n" \
R_"a[160:163], v49 offset:8192\n" \
M_"v[64:67], a[132:135], a[68:71], v[64:67]\n" \
B_"v52, s[8:11], s64 offen offset:0 sc1 lds\n" \
M_"v[64:67], a[136:139], a[72:75], v[64:67]\n" \
R_"a[164:167], v49 offset:9216\n" \
R_"a[168:171], v49 offset:10240\n" \
M_"v[64:67], a[140:143], a[76:79], v[64:67]\n" \
R_"a[172:175], v49 offset:11264\n" \
R_"a[176:179], v49 offset:12288\n" \
M_"v[68:71], a[156:159], a[76:79], v[32:35]\n" \
R_"a[180:183], v49 offset:13312\n" \
R_"a[184:187], v49 offset:14336\n" \
M_"v[68:71], a[152:155], a[72:75], v[68:71]\n" \
R_"a[188:191], v49 offset:15360\n" \
C_"v117, v114, v115\n" \
M_"v[68:71], a[148:151], a[68:71], v[68:71]\n" \
"v_exp_f32_e64 v112, v112\n" \
M_"v[68:71], a[144:147], a[64:67], v[68:71]\n" \
B_"v52, s[8:11], s64 offen offset:1024 sc1 lds\n" \
M_"v[76:79], a[144:147], a[80:83], v[36:39]\n" \
"v_exp_f32_e64 v113, v113\n" \
M_"v[76:79], a[148:151], a[84:87], v[76:79]\n" \
C_"v116, v112, v113\n" \
"v_add_u32_e32 v49, 0x10000, v48\n" \
M_"v[76:79], a[152:155], a[88:91], v[76:79]\n" \
"v_exp_f32_e64 v120, v120\n" \
M_"v[76:79], a[156:159], a[92:95], v[76:79]\n" \
"v_exp_f32_e64 v121, v121\n" \
M_"v[72:75], a[140:143], a[92:95], v[36:39]\n" \
"v_exp_f32_e64 v122, v122\n" \
M_"v[72:75], a[136:139], a[88:91], v[72:75]\n" \
"v_exp_f32_e64 v123, v123\n" \
M_"v[72:75], a[132:135], a[84:87], v[72:75]\n" \
"v_exp_f32_e64 v124, v124\n" \
M_"v[72:75], a[128:131], a[80:83], v[72:75]\n" \
B_"v52, s[8:11], s64 offen offset:2048 sc1 lds\n" \
M_"v[80:83], a[128:131], a[192:195], v[40:43]\n" \
"v_exp_f32_e64 v125, v125\n" \
M_"v[80:83], a[132:135], a[196:199], v[80:83]\n" \
"v_exp_f32_e64 v126, v126\n" \
M_"v[80:83], a[136:139], a[200:203], v[80:83]\n" \
"v_exp_f32_e64 v127, v127\n" \
M_"v[80:83], a[140:143], a[204:207], v[80:83]\n" \
"v_exp_f32_e64 v70, v70\n" \
M_"v[84:87], a[156:159], a[204:207], v[40:43]\n" \
"v_exp_f32_e64 v71, v71\n" \
M_"v[84:87], a[152:155], a[200:203], v[84:87]\n" \
C_"v120, v120, v121\n" \
C_"v121, v122, v123\n" \
M_"v[84:87], a[148:151], a[196:199], v[84:87]\n" \
B_"v52, s[8:11], s64 offen offset:3072 sc1 lds\n" \
M_"v[84:87], a[144:147], a[192:195], v[84:87]\n" \
"s_mov_b32 m0, s65\n" \
"s_nop 0\n" \
C_"v122, v124, v125\n" \
M_"v[92:95], a[144:147], a[208:211], v[44:47]\n" \
C_"v123, v126, v127\n" \
R_"a[144:147], v48 offset:12288\n" \
M_"v[92:95], a[148:151], a[212:215], v[92:95]\n" \
R_"a[148:151], v48 offset:13312\n" \
C_"v71, v70, v71\n" \
M_"v[92:95], a[152:155], a[216:219], v[92:95]\n" \
R_"a[152:155], v48 offset:14336\n" \
"s_xor_b32 s65, s65, 0x8000\n" \
M_"v[92:95], a[156:159], a[220:223], v[92:95]\n" \
"v_exp_f32_e64 v72, v72\n" \
M_"v[88:91], a[140:143], a[220:223], v[44:47]\n" \
R_"a[140:143], v48 offset:11264\n" \
R_"a[156:159], v48 offset:15360\n" \
M_"v[88:91], a[136:139], a[216:219], v[88:91]\n" \
"v_exp_f32_e64 v73, v73\n" \
M_"v[88:91], a[132:135], a[212:215], v[88:91]\n" \
".fill (L_ab + 8192 - .) / 4, 4, 0xBF800000\n" \
"L_ap2:\n" \
"s_cbranch_execz L_ap0\n" \
B_"v53, s[8:11], s64 offen offset:0 sc1 lds\n" \
M_"v[88:91], a[128:131], a[208:211], v[88:91]\n" \
R_"a[128:131], v48 offset:8192\n" \
"s_waitcnt lgkmcnt(6)\n" \
M_"v[128:131], a[160:163], v[100:103], v[128:131]\n" \
R_"a[132:135], v48 offset:9216\n" \
R_"a[136:139], v48 offset:10240\n" \
M_"v[160:163], a[160:163], v[104:107], v[160:163]\n" \
"v_exp_f32_e64 v68, v68\n" \
M_"v[132:135], a[164:167], v[100:103], v[132:135]\n" \
"v_exp_f32_e64 v69, v69\n" \
M_"v[164:167], a[164:167], v[104:107], v[164:167]\n" \
"v_exp_f32_e64 v74, v74\n" \
M_"v[136:139], a[168:171], v[100:103], v[136:139]\n" \
"v_exp_f32_e64 v75, v75\n" \
M_"v[168:171], a[168:171], v[104:107], v[168:171]\n" \
B_"v53, s[8:11], s64 offen offset:1024 sc1 lds\n" \
M_"v[140:143], a[172:175], v[100:103], v[140:143]\n" \
"v_exp_f32_e64 v66, v66\n" \
M_"v[172:175], a[172:175], v[104:107], v[172:175]\n" \
"v_exp_f32_e64 v67, v67\n" \
M_"v[144:147], a[176:179], v[100:103], v[144:147]\n" \
"v_exp_f32_e64 v64, v64\n" \
M_"v[176:179], a[176:179], v[104:107], v[176:179]\n" \
"v_exp_f32_e64 v65, v65\n" \
M_"v[148:151], a[180:183], v[100:103], v[148:151]\n" \
C_"v72, v72, v73\n" \
C_"v70, v68, v69\n" \
M_"v[180:183], a[180:183], v[104:107], v[180:183]\n" \
"v_exp_f32_e64 v76, v76\n" \
M_"v[152:155], a[184:187], v[100:103], v[152:155]\n" \
"v_exp_f32_e64 v77, v77\n" \
M_"v[184:187], a[184:187], v[104:107], v[184:187]\n" \
B_"v53, s[8:11], s64 offen offset:2048 sc1 lds\n" \
M_"v[156:159], a[188:191], v[100:103], v[156:159]\n" \
"v_exp_f32_e64 v78, v78\n" \
M_"v[188:191], a[188:191], v[104:107], v[188:191]\n" \
"v_exp_f32_e64 v79, v79\n" \
"v_smfmac_f32_16x16x64_bf16 a[96:99], a[104:107], v[100:107], v21\n" \
C_"v73, v74, v75\n" \
C_"v69, v66, v67\n" \
M_"v[220:223], a[188:191], v[116:119], v[220:223]\n" \
C_"v68, v64, v65\n" \
C_"v74, v76, v77\n" \
M_"v[252:255], a[188:191], v[120:123], v[252:255]\n" \
C_"v75, v78, v79\n" \
"v_xor_b32_e32 v48, 0x8000, v48\n" \
M_"v[216:219], a[184:187], v[116:119], v[216:219]\n" \
M_"v[248:251], a[184:187], v[120:123], v[248:251]\n" \
B_"v53, s[8:11], s64 offen offset:3072 sc1 lds\n" \
M_"v[212:215], a[180:183], v[116:119], v[212:215]\n" \
"s_add_u32 s64, s64, 0x10000\n" \
"s_sub_u32 s66, s66, 1\n" \
"s_nop 0\n" \
M_"v[244:247], a[180:183], v[120:123], v[244:247]\n" \
M_"v[208:211], a[176:179], v[116:119], v[208:211]\n" \
M_"v[240:243], a[176:179], v[120:123], v[240:243]\n" \
M_"v[204:207], a[172:175], v[116:119], v[204:207]\n" \
M_"v[236:239], a[172:175], v[120:123], v[236:239]\n" \
M_"v[200:203], a[168:171], v[116:119], v[200:203]\n" \
M_"v[232:235], a[168:171], v[120:123], v[232:235]\n" \
M_"v[196:199], a[164:167], v[116:119], v[196:199]\n" \
M_"v[228:231], a[164:167], v[120:123], v[228:231]\n" \
M_"v[192:195], a[160:163], v[116:119], v[192:195]\n" \
M_"v[224:227], a[160:163], v[120:123], v[224:227]\n" \
"s_cbranch_scc0 L_tL_2_%=\n" \
"s_waitcnt vmcnt(8) lgkmcnt(0)\n" \
"v_smfmac_f32_16x16x64_bf16 a[100:103], a[104:107], v[116:123], v21\n" \
"s_barrier\n" \
"L_la_%=:\n" \
M_"v[96:99], a[128:131], a[64:67], v[32:35]\n" \
"v_readlane_b32 s96, v11, 1\n" \
"v_lshrrev_b32_e32 v16, 1, v20\n" \
M_"v[96:99], a[132:135], a[68:71], v[96:99]\n" \
"s_xor_b32 m0, s65, 0x12000\n" \
R_"a[160:163], v49 offset:0\n" \
M_"v[96:99], a[136:139], a[72:75], v[96:99]\n" \
"buffer_load_dwordx2 v[26:27], v16, s[56:59], s96 offen sc1\n" \
M_"v[96:99], a[140:143], a[76:79], v[96:99]\n" \
B_"v50, s[8:11], s64 offen offset:0 sc1 lds\n" \
M_"v[100:103], a[156:159], a[76:79], v[32:35]\n" \
R_"a[164:167], v49 offset:1024\n" \
R_"a[168:171], v49 offset:2048\n" \
M_"v[100:103], a[152:155], a[72:75], v[100:103]\n" \
R_"a[172:175], v49 offset:3072\n" \
R_"a[176:179], v49 offset:4096\n" \
M_"v[100:103], a[148:151], a[68:71], v[100:103]\n" \
R_"a[180:183], v49 offset:5120\n" \
R_"a[184:187], v49 offset:6144\n" \
M_"v[100:103], a[144:147], a[64:67], v[100:103]\n" \
R_"a[188:191], v49 offset:7168\n" \
"s_mov_b32 s63, s62\n" \
M_"v[108:111], a[144:147], a[80:83], v[36:39]\n" \
E_"v86, v86\n" \
M_"v[108:111], a[148:151], a[84:87], v[108:111]\n" \
"v_exp_f32_e64 v87, v87\n" \
M_"v[108:111], a[152:155], a[88:91], v[108:111]\n" \
B_"v50, s[8:11], s64 offen offset:1024 sc1 lds\n" \
M_"v[108:111], a[156:159], a[92:95], v[108:111]\n" \
"v_exp_f32_e64 v84, v84\n" \
M_"v[104:107], a[140:143], a[92:95], v[36:39]\n" \
"v_exp_f32_e64 v85, v85\n" \
M_"v[104:107], a[136:139], a[88:91], v[104:107]\n" \
C_"v87, v86, v87\n" \
C_"v86, v84, v85\n" \
M_"v[104:107], a[132:135], a[84:87], v[104:107]\n" \
"v_exp_f32_e64 v82, v82\n" \
M_"v[104:107], a[128:131], a[80:83], v[104:107]\n" \
"v_exp_f32_e64 v83, v83\n" \
M_"v[112:115], a[128:131], a[192:195], v[40:43]\n" \
"v_exp_f32_e64 v80, v80\n" \
M_"v[112:115], a[132:135], a[196:199], v[112:115]\n" \
"v_exp_f32_e64 v81, v81\n" \
M_"v[112:115], a[136:139], a[200:203], v[112:115]\n" \
B_"v50, s[8:11], s64 offen offset:2048 sc1 lds\n" \
M_"v[112:115], a[140:143], a[204:207], v[112:115]\n" \
C_"v85, v82, v83\n" \
C_"v84, v80, v81\n" \
M_"v[116:119], a[156:159], a[204:207], v[40:43]\n" \
"v_exp_f32_e64 v88, v88\n" \
M_"v[116:119], a[152:155], a[200:203], v[116:119]\n" \
"v_exp_f32_e64 v89, v89\n" \
M_"v[116:119], a[148:151], a[196:199], v[116:119]\n" \
"v_exp_f32_e64 v90, v90\n" \
M_"v[116:119], a[144:147], a[192:195], v[116:119]\n" \
"v_exp_f32_e64 v91, v91\n" \
M_"v[124:127], a[144:147], a[208:211], v[44:47]\n" \
C_"v88, v88, v89\n" \
C_"v89, v90, v91\n" \
M_"v[124:127], a[148:151], a[212:215], v[124:127]\n" \
B_"v50, s[8:11], s64 offen offset:3072 sc1 lds\n" \
M_"v[124:127], a[152:155], a[216:219], v[124:127]\n" \
"v_exp_f32_e64 v92, v92\n" \
M_"v[124:127], a[156:159], a[220:223], v[124:127]\n" \
"v_exp_f32_e64 v93, v93\n" \
M_"v[120:123], a[140:143], a[220:223], v[44:47]\n" \
"v_exp_f32_e64 v94, v94\n" \
M_"v[120:123], a[136:139], a[216:219], v[120:123]\n" \
"v_exp_f32_e64 v95, v95\n" \
M_"v[120:123], a[132:135], a[212:215], v[120:123]\n" \
C_"v90, v92, v93\n" \
C_"v91, v94, v95\n" \
M_"v[120:123], a[128:131], a[208:211], v[120:123]\n" \
"v_readlane_b32 s97, v11, 39\n" \
"s_waitcnt lgkmcnt(0)\n" \
"s_nop 0\n" \
M_"v[128:131], a[160:163], v[68:71], v[128:131]\n" \
R_"a[128:131], v48 offset:0\n" \
R_"a[144:147], v48 offset:4096\n" \
M_"v[160:163], a[160:163], v[72:75], v[160:163]\n" \
R_"a[132:135], v48 offset:1024\n" \
R_"a[148:151], v48 offset:5120\n" \
M_"v[132:135], a[164:167], v[68:71], v[132:135]\n" \
R_"a[136:139], v48 offset:2048\n" \
R_"a[152:155], v48 offset:6144\n" \
M_"v[164:167], a[164:167], v[72:75], v[164:167]\n" \
R_"a[140:143], v48 offset:3072\n" \
R_"a[156:159], v48 offset:7168\n" \
M_"v[136:139], a[168:171], v[68:71], v[136:139]\n" \
"v_add_u32_e32 v17, 0x49000, v20\n" \
"v_readlane_b32 s98, v11, 40\n" \
M_"v[168:171], a[168:171], v[72:75], v[168:171]\n" \
"v_readlane_b32 s93, v11, 23\n" \
"v_readlane_b32 s84, v11, 15\n" \
M_"v[140:143], a[172:175], v[68:71], v[140:143]\n" \
"v_readlane_b32 s85, v11, 16\n" \
"v_readlane_b32 s86, v11, 17\n" \
M_"v[172:175], a[172:175], v[72:75], v[172:175]\n" \
"s_mov_b32 s87, 0x20000\n" \
"v_readlane_b32 s92, v11, 18\n" \
M_"v[144:147], a[176:179], v[68:71], v[144:147]\n" \
"v_exp_f32_e64 v102, v102\n" \
M_"v[176:179], a[176:179], v[72:75], v[176:179]\n" \
"v_exp_f32_e64 v103, v103\n" \
M_"v[148:151], a[180:183], v[68:71], v[148:151]\n" \
"v_exp_f32_e64 v100, v100\n" \
M_"v[180:183], a[180:183], v[72:75], v[180:183]\n" \
"v_exp_f32_e64 v101, v101\n" \
M_"v[152:155], a[184:187], v[68:71], v[152:155]\n" \
C_"v103, v102, v103\n" \
C_"v102, v100, v101\n" \
M_"v[184:187], a[184:187], v[72:75], v[184:187]\n" \
"v_exp_f32_e64 v98, v98\n" \
M_"v[156:159], a[188:191], v[68:71], v[156:159]\n" \
"v_exp_f32_e64 v99, v99\n" \
M_"v[188:191], a[188:191], v[72:75], v[188:191]\n" \
"s_waitcnt vmcnt(4)\n" \
"v_cmp_ne_u64_e32 vcc, s[62:63], v[26:27]\n" \
"v_smfmac_f32_16x16x64_bf16 a[96:99], a[104:107], v[68:75], v21\n" \
"v_exp_f32_e64 v96, v96\n" \
M_"v[220:223], a[188:191], v[84:87], v[220:223]\n" \
"v_exp_f32_e64 v97, v97\n" \
M_"v[252:255], a[188:191], v[88:91], v[252:255]\n" \
"s_cbranch_vccnz L_sol_%=\n" \
"L_rol_%=:\n" \
C_"v101, v98, v99\n" \
B_"a[124:127], v20, s[56:59], s97 offen offset:0 sc1\n" \
M_"v[216:219], a[184:187], v[84:87], v[216:219]\n" \
C_"v100, v96, v97\n" \
"v_readlane_b32 s91, v11, 24\n" \
M_"v[248:251], a[184:187], v[88:91], v[248:251]\n" \
E_"v104, v104\n" \
M_"v[212:215], a[180:183], v[84:87], v[212:215]\n" \
"v_exp_f32_e64 v105, v105\n" \
M_"v[244:247], a[180:183], v[88:91], v[244:247]\n" \
"v_exp_f32_e64 v106, v106\n" \
M_"v[208:211], a[176:179], v[84:87], v[208:211]\n" \
"v_exp_f32_e64 v107, v107\n" \
M_"v[240:243], a[176:179], v[88:91], v[240:243]\n" \
B_"a[228:231], v17, s[56:59], s97 offen offset:0 sc1\n" \
M_"v[204:207], a[172:175], v[84:87], v[204:207]\n" \
C_"v104, v104, v105\n" \
C_"v105, v106, v107\n" \
M_"v[236:239], a[172:175], v[88:91], v[236:239]\n" \
"v_exp_f32_e64 v108, v108\n" \
M_"v[200:203], a[168:171], v[84:87], v[200:203]\n" \
"v_exp_f32_e64 v109, v109\n" \
M_"v[232:235], a[168:171], v[88:91], v[232:235]\n" \
"v_exp_f32_e64 v110, v110\n" \
M_"v[196:199], a[164:167], v[84:87], v[196:199]\n" \
"v_exp_f32_e64 v111, v111\n" \
M_"v[228:231], a[164:167], v[88:91], v[228:231]\n" \
C_"v106, v108, v109\n" \
C_"v107, v110, v111\n" \
M_"v[192:195], a[160:163], v[84:87], v[192:195]\n" \
"v_exp_f32_e64 v118, v118\n" \
M_"v[224:227], a[160:163], v[88:91], v[224:227]\n" \
"s_xor_b32 s65, s65, 0x8000\n" \
"s_waitcnt vmcnt(7) lgkmcnt(0)\n" \
"v_smfmac_f32_16x16x64_bf16 a[100:103], a[104:107], v[84:91], v21\n" \
"s_barrier\n" \
M_"v[64:67], a[128:131], a[64:67], v[32:35]\n" \
B_"a[236:239], v20, s[56:59], s98 offen offset:0 sc1\n" \
M_"v[64:67], a[132:135], a[68:71], v[64:67]\n" \
"s_add_u32 m0, s93, 0x0\n" \
"s_nop 0\n" \
R_"a[160:163], v49 offset:8192\n" \
M_"v[64:67], a[136:139], a[72:75], v[64:67]\n" \
B_"v0, s[84:87], s92 offen offset:0 lds\n" \
M_"v[64:67], a[140:143], a[76:79], v[64:67]\n" \
R_"a[164:167], v49 offset:9216\n" \
R_"a[168:171], v49 offset:10240\n" \
M_"v[68:71], a[156:159], a[76:79], v[32:35]\n" \
R_"a[172:175], v49 offset:11264\n" \
R_"a[176:179], v49 offset:12288\n" \
M_"v[68:71], a[152:155], a[72:75], v[68:71]\n" \
R_"a[180:183], v49 offset:13312\n" \
R_"a[184:187], v49 offset:14336\n" \
M_"v[68:71], a[148:151], a[68:71], v[68:71]\n" \
B_"v1, s[84:87], s92 offen offset:1024 lds\n" \
M_"v[68:71], a[144:147], a[64:67], v[68:71]\n" \
B_"a[244:247], v17, s[56:59], s98 offen offset:0 sc1\n" \
M_"v[76:79], a[144:147], a[80:83], v[36:39]\n" \
R_"a[188:191], v49 offset:15360\n" \
"v_add_u32_e32 v49, 0x10000, v48\n" \
M_"v[76:79], a[148:151], a[84:87], v[76:79]\n" \
"v_exp_f32_e64 v119, v119\n" \
M_"v[76:79], a[152:155], a[88:91], v[76:79]\n" \
"v_exp_f32_e64 v116, v116\n" \
M_"v[76:79], a[156:159], a[92:95], v[76:79]\n" \
"v_exp_f32_e64 v117, v117\n" \
M_"v[72:75], a[140:143], a[92:95], v[36:39]\n" \
C_"v119, v118, v119\n" \
C_"v118, v116, v117\n" \
M_"v[72:75], a[136:139], a[88:91], v[72:75]\n" \
B_"v2, s[84:87], s92 offen offset:2048 lds\n" \
M_"v[72:75], a[132:135], a[84:87], v[72:75]\n" \
"v_exp_f32_e64 v114, v114\n" \
M_"v[72:75], a[128:131], a[80:83], v[72:75]\n" \
"v_exp_f32_e64 v115, v115\n" \
M_"v[80:83], a[128:131], a[192:195], v[40:43]\n" \
B_"a[224:227], v20, s[56:59], s97 offen offset:1024 sc1\n" \
M_"v[80:83], a[132:135], a[196:199], v[80:83]\n" \
"v_exp_f32_e64 v112, v112\n" \
M_"v[80:83], a[136:139], a[200:203], v[80:83]\n" \
"v_exp_f32_e64 v113, v113\n" \
M_"v[80:83], a[140:143], a[204:207], v[80:83]\n" \
C_"v117, v114, v115\n" \
C_"v116, v112, v113\n" \
M_"v[84:87], a[156:159], a[204:207], v[40:43]\n" \
B_"v3, s[84:87], s92 offen offset:3072 lds\n" \
M_"v[84:87], a[152:155], a[200:203], v[84:87]\n" \
"s_add_u32 m0, s91, 0x1000\n" \
"s_add_u32 s64, s64, 0x10000\n" \
M_"v[84:87], a[148:151], a[196:199], v[84:87]\n" \
"v_exp_f32_e64 v120, v120\n" \
M_"v[84:87], a[144:147], a[192:195], v[84:87]\n" \
"v_exp_f32_e64 v121, v121\n" \
M_"v[92:95], a[144:147], a[208:211], v[44:47]\n" \
"v_exp_f32_e64 v122, v122\n" \
M_"v[92:95], a[148:151], a[212:215], v[92:95]\n" \
B_"a[232:235], v17, s[56:59], s97 offen offset:1024 sc1\n" \
M_"v[92:95], a[152:155], a[216:219], v[92:95]\n" \
B_"v4, s[84:87], s92 offen offset:0 lds\n" \
M_"v[92:95], a[156:159], a[220:223], v[92:95]\n" \
"v_exp_f32_e64 v123, v123\n" \
M_"v[88:91], a[140:143], a[220:223], v[44:47]\n" \
C_"v120, v120, v121\n" \
C_"v121, v122, v123\n" \
M_"v[88:91], a[136:139], a[216:219], v[88:91]\n" \
"v_exp_f32_e64 v124, v124\n" \
M_"v[88:91], a[132:135], a[212:215], v[88:91]\n" \
"v_exp_f32_e64 v125, v125\n" \
M_"v[88:91], a[128:131], a[208:211], v[88:91]\n" \
C_"v122, v124, v125\n" \
"s_waitcnt lgkmcnt(0)\n" \
M_"v[128:131], a[160:163], v[100:103], v[128:131]\n" \
E_"v126, v126\n" \
M_"v[160:163], a[160:163], v[104:107], v[160:163]\n" \
B_"v5, s[84:87], s92 offen offset:1024 lds\n" \
M_"v[132:135], a[164:167], v[100:103], v[132:135]\n" \
B_"a[240:243], v20, s[56:59], s98 offen offset:1024 sc1\n" \
M_"v[164:167], a[164:167], v[104:107], v[164:167]\n" \
"v_exp_f32_e64 v127, v127\n" \
M_"v[136:139], a[168:171], v[100:103], v[136:139]\n" \
C_"v123, v126, v127\n" \
R_"a[128:131], v48 offset:8192\n" \
M_"v[168:171], a[168:171], v[104:107], v[168:171]\n" \
R_"a[144:147], v48 offset:12288\n" \
R_"a[132:135], v48 offset:9216\n" \
M_"v[140:143], a[172:175], v[100:103], v[140:143]\n" \
R_"a[148:151], v48 offset:13312\n" \
R_"a[136:139], v48 offset:10240\n" \
M_"v[172:175], a[172:175], v[104:107], v[172:175]\n" \
R_"a[152:155], v48 offset:14336\n" \
R_"a[140:143], v48 offset:11264\n" \
M_"v[144:147], a[176:179], v[100:103], v[144:147]\n" \
B_"v6, s[84:87], s92 offen offset:2048 lds\n" \
M_"v[176:179], a[176:179], v[104:107], v[176:179]\n" \
"v_exp_f32_e64 v70, v70\n" \
M_"v[148:151], a[180:183], v[100:103], v[148:151]\n" \
"v_exp_f32_e64 v71, v71\n" \
M_"v[180:183], a[180:183], v[104:107], v[180:183]\n" \
B_"a[248:251], v17, s[56:59], s98 offen offset:1024 sc1\n" \
M_"v[152:155], a[184:187], v[100:103], v[152:155]\n" \
R_"a[156:159], v48 offset:15360\n" \
C_"v71, v70, v71\n" \
M_"v[184:187], a[184:187], v[104:107], v[184:187]\n" \
"v_exp_f32_e64 v72, v72\n" \
M_"v[156:159], a[188:191], v[100:103], v[156:159]\n" \
B_"v7, s[84:87], s92 offen offset:3072 lds\n" \
M_"v[188:191], a[188:191], v[104:107], v[188:191]\n" \
"s_add_u32 m0, s91, 0x2000\n" \
"v_xor_b32_e32 v48, 0x8000, v48\n" \
"v_smfmac_f32_16x16x64_bf16 a[96:99], a[104:107], v[100:107], v21\n" \
"v_exp_f32_e64 v73, v73\n" \
M_"v[220:223], a[188:191], v[116:119], v[220:223]\n" \
"v_exp_f32_e64 v68, v68\n" \
M_"v[252:255], a[188:191], v[120:123], v[252:255]\n" \
"v_exp_f32_e64 v69, v69\n" \
M_"v[216:219], a[184:187], v[116:119], v[216:219]\n" \
"v_exp_f32_e64 v74, v74\n" \
M_"v[248:251], a[184:187], v[120:123], v[248:251]\n" \
"v_exp_f32_e64 v75, v75\n" \
M_"v[212:215], a[180:183], v[116:119], v[212:215]\n" \
B_"v8, s[84:87], s92 offen offset:0 lds\n" \
M_"v[244:247], a[180:183], v[120:123], v[244:247]\n" \
"v_exp_f32_e64 v66, v66\n" \
M_"v[208:211], a[176:179], v[116:119], v[208:211]\n" \
"v_exp_f32_e64 v67, v67\n" \
M_"v[240:243], a[176:179], v[120:123], v[240:243]\n" \
"v_exp_f32_e64 v64, v64\n" \
M_"v[204:207], a[172:175], v[116:119], v[204:207]\n" \
"v_exp_f32_e64 v65, v65\n" \
M_"v[236:239], a[172:175], v[120:123], v[236:239]\n" \
C_"v72, v72, v73\n" \
C_"v70, v68, v69\n" \
M_"v[200:203], a[168:171], v[116:119], v[200:203]\n" \
"v_exp_f32_e64 v76, v76\n" \
M_"v[232:235], a[168:171], v[120:123], v[232:235]\n" \
"v_exp_f32_e64 v77, v77\n" \
M_"v[196:199], a[164:167], v[116:119], v[196:199]\n" \
"v_exp_f32_e64 v78, v78\n" \
M_"v[228:231], a[164:167], v[120:123], v[228:231]\n" \
"v_exp_f32_e64 v79, v79\n" \
M_"v[192:195], a[160:163], v[116:119], v[192:195]\n" \
C_"v73, v74, v75\n" \
C_"v69, v66, v67\n" \
M_"v[224:227], a[160:163], v[120:123], v[224:227]\n" \
C_"v68, v64, v65\n" \
"s_waitcnt vmcnt(15) lgkmcnt(0)\n" \
"v_smfmac_f32_16x16x64_bf16 a[100:103], a[104:107], v[116:123], v21\n" \
"s_barrier\n" \
C_"v74, v76, v77\n" \
C_"v75, v78, v79\n" \
"s_cmp_lg_u32 s16, 0\n" \
"s_cbranch_scc1 L_f1_%=\n" \
R_"a[160:163], v49 offset:0\n" \
R_"a[164:167], v49 offset:1024\n" \
R_"a[168:171], v49 offset:2048\n" \
R_"a[172:175], v49 offset:3072\n" \
R_"a[176:179], v49 offset:4096\n" \
R_"a[180:183], v49 offset:5120\n" \
R_"a[184:187], v49 offset:6144\n" \
R_"a[188:191], v49 offset:7168\n" \
"v_add_u32_e32 v54, 0x49000, v20\n" \
B_"a[192:195], v20, s[56:59], s60 offen offset:0 sc1\n" \
"v_add_u32_e32 v50, s15, v20\n" \
"v_add_u32_e32 v51, 0x10000, v50\n" \
"ds_read_b32 v60, v18 offset:0\n" \
"ds_read_b32 v61, v18 offset:128\n" \
R_"a[0:3], v49 offset:8192\n" \
R_"a[4:7], v49 offset:9216\n" \
R_"a[8:11], v49 offset:10240\n" \
R_"a[12:15], v49 offset:11264\n" \
R_"a[16:19], v49 offset:12288\n" \
R_"a[20:23], v49 offset:13312\n" \
R_"a[24:27], v49 offset:14336\n" \
R_"a[28:31], v49 offset:15360\n" \
E_"v86, v86\n" \
B_"a[196:199], v20, s[56:59], s60 offen offset:1024 sc1\n" \
E_"v87, v87\n" \
E_"v84, v84\n" \
E_"v85, v85\n" \
C_"v87, v86, v87\n" \
E_"v82, v82\n" \
E_"v83, v83\n" \
E_"v80, v80\n" \
B_"a[200:203], v20, s[56:59], s60 offen offset:2048 sc1\n" \
"s_waitcnt lgkmcnt(15)\n" \
M_"v[128:131], a[160:163], v[68:71], v[128:131]\n" \
E_"v81, v81\n" \
M_"v[160:163], a[160:163], v[72:75], v[160:163]\n" \
C_"v86, v84, v85\n" \
C_"v85, v82, v83\n" \
M_"v[132:135], a[164:167], v[68:71], v[132:135]\n" \
C_"v84, v80, v81\n" \
"v_and_b32_e32 v84, v12, v84\n" \
M_"v[164:167], a[164:167], v[72:75], v[164:167]\n" \
B_"a[204:207], v20, s[56:59], s60 offen offset:3072 sc1\n" \
M_"v[136:139], a[168:171], v[68:71], v[136:139]\n" \
C_"v128, v128, v129\n" \
C_"v160, v160, v161\n" \
M_"v[168:171], a[168:171], v[72:75], v[168:171]\n" \
C_"v129, v130, v131\n" \
"s_waitcnt lgkmcnt(10)\n" \
M_"v[140:143], a[172:175], v[68:71], v[140:143]\n" \
C_"v161, v162, v163\n" \
C_"v130, v132, v133\n" \
M_"v[172:175], a[172:175], v[72:75], v[172:175]\n" \
C_"v131, v134, v135\n" \
C_"v162, v164, v165\n" \
M_"v[144:147], a[176:179], v[68:71], v[144:147]\n" \
C_"v163, v166, v167\n" \
C_"v132, v136, v137\n" \
M_"v[176:179], a[176:179], v[72:75], v[176:179]\n" \
C_"v133, v138, v139\n" \
C_"v134, v140, v141\n" \
M_"v[148:151], a[180:183], v[68:71], v[148:151]\n" \
C_"v135, v142, v143\n" \
C_"v164, v168, v169\n" \
M_"v[180:183], a[180:183], v[72:75], v[180:183]\n" \
C_"v165, v170, v171\n" \
C_"v166, v172, v173\n" \
M_"v[152:155], a[184:187], v[68:71], v[152:155]\n" \
C_"v167, v174, v175\n" \
C_"v136, v144, v145\n" \
M_"v[184:187], a[184:187], v[72:75], v[184:187]\n" \
C_"v137, v146, v147\n" \
C_"v138, v148, v149\n" \
M_"v[156:159], a[188:191], v[68:71], v[156:159]\n" \
C_"v139, v150, v151\n" \
C_"v168, v176, v177\n" \
M_"v[188:191], a[188:191], v[72:75], v[188:191]\n" \
C_"v140, v152, v153\n" \
C_"v141, v154, v155\n" \
"v_smfmac_f32_16x16x64_bf16 a[96:99], a[104:107], v[68:75], v21\n" \
C_"v169, v178, v179\n" \
C_"v170, v180, v181\n" \
M_"v[120:123], a[128:131], a[208:211], v[44:47]\n" \
C_"v142, v156, v157\n" \
C_"v143, v158, v159\n" \
M_"v[120:123], a[132:135], a[212:215], v[120:123]\n" \
C_"v171, v182, v183\n" \
C_"v172, v184, v185\n" \
C_"v173, v186, v187\n" \
C_"v174, v188, v189\n" \
C_"v175, v190, v191\n" \
"ds_write_b128 v50, v[128:131] offset:0\n" \
M_"v[120:123], a[136:139], a[216:219], v[120:123]\n" \
"v_and_b32_e32 v85, v13, v85\n" \
"v_and_b32_e32 v86, v14, v86\n" \
M_"v[120:123], a[140:143], a[220:223], v[120:123]\n" \
"ds_write_b128 v50, v[132:135] offset:1024\n" \
M_"v[124:127], a[156:159], a[220:223], v[44:47]\n" \
B_"a[220:223], v54, s[56:59], s60 offen offset:3072 sc1\n" \
M_"v[124:127], a[152:155], a[216:219], v[124:127]\n" \
"ds_write_b128 v50, v[136:139] offset:2048\n" \
M_"v[124:127], a[148:151], a[212:215], v[124:127]\n" \
"v_and_b32_e32 v87, v15, v87\n" \
"v_accvgpr_read_b32 v40, a96\n" \
M_"v[124:127], a[144:147], a[208:211], v[124:127]\n" \
"ds_write_b128 v50, v[140:143] offset:3072\n" \
M_"a[100:103], a[108:111], v[84:87], a[100:103]\n" \
B_"a[208:211], v54, s[56:59], s60 offen offset:0 sc1\n" \
M_"v[192:195], a[160:163], v[84:87], v[192:195]\n" \
"ds_write_b128 v51, v[160:163] offset:32768\n" \
M_"v[196:199], a[164:167], v[84:87], v[196:199]\n" \
"v_accvgpr_read_b32 v22, a97\n" \
"v_accvgpr_read_b32 v41, a98\n" \
"v_accvgpr_read_b32 v23, a99\n" \
M_"v[200:203], a[168:171], v[84:87], v[200:203]\n" \
"ds_write_b128 v51, v[164:167] offset:33792\n" \
B_"a[212:215], v54, s[56:59], s60 offen offset:1024 sc1\n" \
"v_add_f32_e32 v40, v40, v22\n" \
"v_add_f32_e32 v41, v41, v23\n" \
"ds_write_b64 v19, v[40:41]\n" \
M_"v[204:207], a[172:175], v[84:87], v[204:207]\n" \
"ds_write_b128 v51, v[168:171] offset:34816\n" \
M_"v[208:211], a[176:179], v[84:87], v[208:211]\n" \
"v_lshl_add_u32 v52, s15, 2, v20\n" \
"v_add_u32_e32 v52, 0x1a000, v52\n" \
M_"v[212:215], a[180:183], v[84:87], v[212:215]\n" \
B_"a[216:219], v54, s[56:59], s60 offen offset:2048 sc1\n" \
"ds_write_b128 v51, v[172:175] offset:35840\n" \
M_"v[216:219], a[184:187], v[84:87], v[216:219]\n" \
E_"v88, v88\n" \
M_"v[220:223], a[188:191], v[84:87], v[220:223]\n" \
E_"v89, v89\n" \
E_"v90, v90\n" \
E_"v91, v91\n" \
C_"v88, v88, v89\n" \
E_"v92, v92\n" \
E_"v93, v93\n" \
E_"v94, v94\n" \
E_"v95, v95\n" \
C_"v89, v90, v91\n" \
C_"v90, v92, v93\n" \
C_"v91, v94, v95\n" \
"v_xor_b32_e32 v63, 0x400, v19\n" \
E_"v120, v120\n" \
E_"v121, v121\n" \
E_"v122, v122\n" \
E_"v123, v123\n" \
C_"v120, v120, v121\n" \
E_"v124, v124\n" \
E_"v125, v125\n" \
E_"v126, v126\n" \
"s_waitcnt lgkmcnt(0)\n" \
"s_barrier\n" \
R_"v[144:147], v52 offset:0\n" \
R_"v[148:151], v52 offset:1024\n" \
R_"v[152:155], v52 offset:2048\n" \
".fill (L_ab + 12288 - .) / 4, 4, 0xBF800000\n" \
"L_ap3:\n" \
"s_cbranch_execz L_ap0\n" \
R_"v[156:159], v52 offset:3072\n" \
R_"v[176:179], v52 offset:4096\n" \
R_"v[180:183], v52 offset:5120\n" \
R_"v[184:187], v52 offset:6144\n" \
R_"v[188:191], v52 offset:7168\n" \
M_"a[100:103], a[116:119], v[88:91], a[100:103]\n" \
"ds_read_b64 v[42:43], v63\n" \
C_"v121, v122, v123\n" \
M_"v[224:227], a[160:163], v[88:91], v[224:227]\n" \
E_"v127, v127\n" \
M_"v[228:231], a[164:167], v[88:91], v[228:231]\n" \
C_"v122, v124, v125\n" \
C_"v123, v126, v127\n" \
M_"v[232:235], a[168:171], v[88:91], v[232:235]\n" \
"v_and_b32_e32 v120, v12, v120\n" \
"v_and_b32_e32 v121, v13, v121\n" \
M_"v[236:239], a[172:175], v[88:91], v[236:239]\n" \
"v_and_b32_e32 v122, v14, v122\n" \
"v_and_b32_e32 v123, v15, v123\n" \
M_"v[240:243], a[176:179], v[88:91], v[240:243]\n" \
"v_readlane_b32 s93, v11, 25\n" \
"v_readlane_b32 s92, v11, 30\n" \
M_"v[244:247], a[180:183], v[88:91], v[244:247]\n" \
"s_add_u32 m0, s93, 0x0\n" \
"v_mov_b32_e32 v130, v28\n" \
M_"v[248:251], a[184:187], v[88:91], v[248:251]\n" \
"v_mov_b32_e32 v131, v29\n" \
"v_mov_b32_e32 v132, 0\n" \
M_"v[252:255], a[188:191], v[88:91], v[252:255]\n" \
"v_mov_b32_e32 v133, 0\n" \
"v_mov_b32_e32 v128, 0\n" \
"v_mov_b32_e32 v129, 0\n" \
"s_waitcnt vmcnt(8) lgkmcnt(1)\n" \
"s_barrier\n" \
M_"a[100:103], a[116:119], v[120:123], a[100:103]\n" \
B_"v0, s[80:83], s92 offen offset:0 lds\n" \
M_"v[224:227], a[0:3], v[120:123], v[224:227]\n" \
M_"v[228:231], a[4:7], v[120:123], v[228:231]\n" \
M_"v[232:235], a[8:11], v[120:123], v[232:235]\n" \
M_"v[236:239], a[12:15], v[120:123], v[236:239]\n" \
B_"v1, s[80:83], s92 offen offset:1024 lds\n" \
M_"v[240:243], a[16:19], v[120:123], v[240:243]\n" \
M_"v[244:247], a[20:23], v[120:123], v[244:247]\n" \
"v_accvgpr_read_b32 v56, a100\n" \
"v_accvgpr_read_b32 v24, a101\n" \
M_"v[248:251], a[24:27], v[120:123], v[248:251]\n" \
"v_add_f32_e32 v56, v56, v24\n" \
"s_waitcnt lgkmcnt(0)\n" \
"v_add_f32_e32 v56, v56, v42\n" \
B_"v2, s[80:83], s92 offen offset:2048 lds\n" \
M_"v[252:255], a[28:31], v[120:123], v[252:255]\n" \
"v_max_f32_e32 v62, v62, v56\n" \
"v_accvgpr_read_b32 v57, a102\n" \
M_"v[192:195], v[130:133], v[144:147], v[192:195]\n" \
"v_rcp_f32_e32 v56, v56\n" \
M_"v[196:199], v[128:131], v[144:147], v[196:199]\n" \
"v_mul_f32_e32 v56, v56, v60\n" \
"v_accvgpr_read_b32 v25, a103\n" \
M_"v[200:203], v[130:133], v[148:151], v[200:203]\n" \
B_"v3, s[80:83], s92 offen offset:3072 lds\n" \
"s_add_u32 m0, s93, 0x1000\n" \
M_"v[204:207], v[128:131], v[148:151], v[204:207]\n" \
"v_mul_f32_e32 v192, v56, v192\n" \
"v_mul_f32_e32 v193, v56, v193\n" \
M_"v[208:211], v[130:133], v[152:155], v[208:211]\n" \
"v_mul_f32_e32 v194, v56, v194\n" \
"v_mul_f32_e32 v195, v56, v195\n" \
M_"v[212:215], v[128:131], v[152:155], v[212:215]\n" \
"v_mul_f32_e32 v196, v56, v196\n" \
"v_mul_f32_e32 v197, v56, v197\n" \
B_"v4, s[80:83], s92 offen offset:0 lds\n" \
M_"v[216:219], v[130:133], v[156:159], v[216:219]\n" \
"v_mul_f32_e32 v198, v56, v198\n" \
"v_mul_f32_e32 v199, v56, v199\n" \
M_"v[220:223], v[128:131], v[156:159], v[220:223]\n" \
C_"v64, v192, v193\n" \
C_"v65, v194, v195\n" \
M_"v[224:227], v[130:133], v[176:179], v[224:227]\n" \
C_"v66, v196, v197\n" \
C_"v67, v198, v199\n" \
M_"v[228:231], v[128:131], v[176:179], v[228:231]\n" \
B_"v5, s[80:83], s92 offen offset:1024 lds\n" \
M_"v[232:235], v[130:133], v[180:183], v[232:235]\n" \
M_"a[112:115], a[120:123], v[64:67], a[112:115]\n" \
"v_mul_f32_e32 v200, v56, v200\n" \
"v_mul_f32_e32 v201, v56, v201\n" \
M_"v[236:239], v[128:131], v[180:183], v[236:239]\n" \
"v_mul_f32_e32 v202, v56, v202\n" \
"v_mul_f32_e32 v203, v56, v203\n" \
M_"v[240:243], v[130:133], v[184:187], v[240:243]\n" \
B_"v6, s[80:83], s92 offen offset:2048 lds\n" \
"v_mul_f32_e32 v204, v56, v204\n" \
"v_mul_f32_e32 v205, v56, v205\n" \
"v_mul_f32_e32 v206, v56, v206\n" \
"v_mul_f32_e32 v207, v56, v207\n" \
"v_add_f32_e32 v57, v57, v25\n" \
C_"v68, v200, v201\n" \
C_"v69, v202, v203\n" \
C_"v70, v204, v205\n" \
C_"v71, v206, v207\n" \
"v_add_f32_e32 v57, v57, v43\n" \
"v_mul_f32_e32 v208, v56, v208\n" \
M_"a[112:115], a[120:123], v[68:71], a[112:115]\n" \
B_"v7, s[80:83], s92 offen offset:3072 lds\n" \
"s_add_u32 m0, s93, 0x2000\n" \
"v_mul_f32_e32 v209, v56, v209\n" \
"v_mul_f32_e32 v210, v56, v210\n" \
"v_mul_f32_e32 v211, v56, v211\n" \
"v_mul_f32_e32 v212, v56, v212\n" \
"v_mul_f32_e32 v213, v56, v213\n" \
"v_mul_f32_e32 v214, v56, v214\n" \
"v_mul_f32_e32 v215, v56, v215\n" \
"v_max_f32_e32 v62, v62, v57\n" \
C_"v72, v208, v209\n" \
C_"v73, v210, v211\n" \
C_"v74, v212, v213\n" \
C_"v75, v214, v215\n" \
B_"v8, s[80:83], s92 offen offset:0 lds\n" \
"v_rcp_f32_e32 v57, v57\n" \
M_"v[244:247], v[128:131], v[184:187], v[244:247]\n" \
M_"a[112:115], a[120:123], v[72:75], a[112:115]\n" \
"v_mul_f32_e32 v216, v56, v216\n" \
"v_mul_f32_e32 v217, v56, v217\n" \
"v_mul_f32_e32 v218, v56, v218\n" \
"v_mul_f32_e32 v219, v56, v219\n" \
"v_mul_f32_e32 v220, v56, v220\n" \
"v_mul_f32_e32 v221, v56, v221\n" \
"v_mul_f32_e32 v222, v56, v222\n" \
"v_mul_f32_e32 v223, v56, v223\n" \
C_"v76, v216, v217\n" \
C_"v77, v218, v219\n" \
C_"v78, v220, v221\n" \
C_"v79, v222, v223\n" \
"v_mul_f32_e32 v57, v57, v61\n" \
"v_mul_f32_e32 v224, v57, v224\n" \
M_"a[112:115], a[120:123], v[76:79], a[112:115]\n" \
"v_mul_f32_e32 v225, v57, v225\n" \
"v_mul_f32_e32 v226, v57, v226\n" \
"v_mul_f32_e32 v227, v57, v227\n" \
"v_mul_f32_e32 v228, v57, v228\n" \
"v_mul_f32_e32 v229, v57, v229\n" \
"v_mul_f32_e32 v230, v57, v230\n" \
"v_mul_f32_e32 v231, v57, v231\n" \
C_"v80, v224, v225\n" \
C_"v81, v226, v227\n" \
C_"v82, v228, v229\n" \
C_"v83, v230, v231\n" \
M_"v[248:251], v[130:133], v[188:191], v[248:251]\n" \
"v_mul_f32_e32 v232, v57, v232\n" \
"v_mul_f32_e32 v233, v57, v233\n" \
M_"a[112:115], a[120:123], v[80:83], a[112:115]\n" \
"v_mul_f32_e32 v234, v57, v234\n" \
"v_mul_f32_e32 v235, v57, v235\n" \
"v_mul_f32_e32 v236, v57, v236\n" \
"v_mul_f32_e32 v237, v57, v237\n" \
"v_mul_f32_e32 v238, v57, v238\n" \
"v_mul_f32_e32 v239, v57, v239\n" \
C_"v84, v232, v233\n" \
C_"v85, v234, v235\n" \
C_"v86, v236, v237\n" \
C_"v87, v238, v239\n" \
"v_mul_f32_e32 v240, v57, v240\n" \
"v_mul_f32_e32 v241, v57, v241\n" \
M_"a[112:115], a[120:123], v[84:87], a[112:115]\n" \
"v_mul_f32_e32 v242, v57, v242\n" \
"v_mul_f32_e32 v243, v57, v243\n" \
"v_mul_f32_e32 v244, v57, v244\n" \
"v_mul_f32_e32 v245, v57, v245\n" \
"v_mul_f32_e32 v246, v57, v246\n" \
"v_mul_f32_e32 v247, v57, v247\n" \
C_"v96, v240, v241\n" \
C_"v97, v242, v243\n" \
C_"v98, v244, v245\n" \
C_"v99, v246, v247\n" \
M_"v[252:255], v[128:131], v[188:191], v[252:255]\n" \
"v_mul_f32_e32 v248, v57, v248\n" \
M_"a[112:115], a[120:123], v[96:99], a[112:115]\n" \
"v_mul_f32_e32 v249, v57, v249\n" \
"v_mul_f32_e32 v250, v57, v250\n" \
"v_mul_f32_e32 v251, v57, v251\n" \
C_"v100, v248, v249\n" \
C_"v101, v250, v251\n" \
"v_cmp_lt_f32_e64 s[72:73], s21, v62\n" \
"v_mul_f32_e32 v252, v57, v252\n" \
"v_mul_f32_e32 v253, v57, v253\n" \
"v_mul_f32_e32 v254, v57, v254\n" \
"v_mul_f32_e32 v255, v57, v255\n" \
C_"v102, v252, v253\n" \
C_"v103, v254, v255\n" \
"s_nop 1\n" \
M_"a[112:115], a[120:123], v[100:103], a[112:115]\n" \
"s_nop 7\n" \
"v_accvgpr_read_b32 v63, a112\n" \
"v_cmp_u_f32_e32 vcc, v63, v63\n" \
"s_nop 0\n" \
"s_or_b64 vcc, s[72:73], vcc\n" \
"s_cbranch_scc1 L_end_%=\n" \
"s_branch L_out_%=\n" \
"L_skb_%=:\n" \
"s_sleep 1\n" \
"buffer_load_dword v57, v61, s[56:59], s96 offen sc1\n" \
"s_add_u32 s101, s101, 1\n" \
"s_waitcnt vmcnt(0)\n" \
"v_cmp_ne_u32_e32 vcc, s62, v57\n" \
"s_cmp_gt_u32 s101, 0x100000\n" \
"s_cbranch_scc1 L_rkb_%=\n" \
"s_nop 2\n" \
"s_cbranch_vccnz L_skb_%=\n" \
"s_branch L_rkb_%=\n" \
"L_sol_%=:\n" \
"s_sleep 1\n" \
"buffer_load_dwordx2 v[26:27], v16, s[56:59], s96 offen sc1\n" \
"s_add_u32 s101, s101, 1\n" \
"s_waitcnt vmcnt(0)\n" \
"v_cmp_ne_u64_e32 vcc, s[62:63], v[26:27]\n" \
"s_cmp_gt_u32 s101, 0x100000\n" \
"s_cbranch_scc1 L_rol_%=\n" \
"s_nop 2\n" \
"s_cbranch_vccnz L_sol_%=\n" \
"s_branch L_rol_%=\n" \
"L_sos_%=:\n" \
"s_sleep 1\n" \
"buffer_load_dwordx2 v[26:27], v16, s[56:59], s96 offen sc1\n" \
"s_add_u32 s101, s101, 1\n" \
"s_waitcnt vmcnt(0)\n" \
"v_cmp_ne_u64_e32 vcc, s[62:63], v[26:27]\n" \
"s_cmp_gt_u32 s101, 0x100000\n" \
"s_cbranch_scc1 L_ros_%=\n" \
"s_nop 2\n" \
"s_cbranch_vccnz L_sos_%=\n" \
"s_branch L_ros_%=\n" \
"L_sa_%=:\n" \
"s_sleep 1\n" \
"buffer_load_dword v26, v38, s[56:59], s64 offen sc1\n" \
"s_add_u32 s101, s101, 1\n" \
"s_waitcnt vmcnt(0)\n" \
"v_cmp_ne_u32_e32 vcc, s62, v26\n" \
"s_cmp_gt_u32 s101, 0x100000\n" \
"s_cbranch_scc1 L_ra_%=\n" \
"s_nop 2\n" \
"s_cbranch_vccnz L_sa_%=\n" \
"s_branch L_ra_%=\n" \
"L_sb_%=:\n" \
"s_sleep 1\n" \
"buffer_load_dword v47, v38, s[56:59], s71 offen sc1\n" \
"s_add_u32 s101, s101, 1\n" \
"s_waitcnt vmcnt(0)\n" \
"v_cmp_ne_u32_e32 vcc, s62, v47\n" \
"s_cmp_gt_u32 s101, 0x100000\n" \
"s_cbranch_scc1 L_rb_%=\n" \
"s_nop 2\n" \
"s_cbranch_vccnz L_sb_%=\n" \
"s_branch L_rb_%=\n" \
"L_sna_%=:\n" \
"buffer_load_dword v37, v40, s[56:59], s74 offen sc1\n" \
"s_add_u32 s101, s101, 1\n" \
"s_waitcnt vmcnt(0)\n" \
"v_cmp_eq_u32_e64 s[98:99], s62, v37\n" \
"s_cmp_gt_u32 s101, 0x100000\n" \
"s_cbranch_scc1 L_rna_%=\n" \
"s_andn2_b32 s75, s20, s98\n" \
"s_cbranch_scc0 L_gna_%=\n" \
"s_sleep 0\n" \
"s_branch L_sna_%=\n" \
"L_gna_%=:\n" \
"s_bitcmp1_b32 s20, 0\n" \
"s_cbranch_scc0 L_kna0_%=\n" \
B_"v[116:119], v20, s[56:59], s12 offen offset:0 sc1\n" \
"L_kna0_%=:\n" \
"s_bitcmp1_b32 s20, 1\n" \
"s_cbranch_scc0 L_kna1_%=\n" \
B_"v[124:127], v20, s[56:59], s12 offen offset:2048 sc1\n" \
"L_kna1_%=:\n" \
"s_bitcmp1_b32 s20, 2\n" \
"s_cbranch_scc0 L_kna2_%=\n" \
B_"v[132:135], v20, s[56:59], s13 offen offset:0 sc1\n" \
"L_kna2_%=:\n" \
"s_bitcmp1_b32 s20, 3\n" \
"s_cbranch_scc0 L_kna3_%=\n" \
B_"v[140:143], v20, s[56:59], s13 offen offset:2048 sc1\n" \
"L_kna3_%=:\n" \
"s_bitcmp1_b32 s20, 4\n" \
"s_cbranch_scc0 L_kna4_%=\n" \
B_"v[148:151], v20, s[56:59], s14 offen offset:0 sc1\n" \
"L_kna4_%=:\n" \
"s_bitcmp1_b32 s20, 5\n" \
"s_cbranch_scc0 L_kna5_%=\n" \
B_"v[156:159], v20, s[56:59], s14 offen offset:2048 sc1\n" \
"L_kna5_%=:\n" \
"s_bitcmp1_b32 s20, 6\n" \
"s_cbranch_scc0 L_kna6_%=\n" \
B_"v[164:167], v20, s[56:59], s15 offen offset:0 sc1\n" \
"L_kna6_%=:\n" \
"s_waitcnt vmcnt(0)\n" \
"s_branch L_rna_%=\n" \
"L_snx_%=:\n" \
"buffer_load_dword v37, v40, s[56:59], s74 offen sc1\n" \
"s_add_u32 s101, s101, 1\n" \
"s_waitcnt vmcnt(0)\n" \
"v_cmp_eq_u32_e64 s[98:99], s62, v37\n" \
"s_cmp_gt_u32 s101, 0x100000\n" \
"s_cbranch_scc1 L_rnx_%=\n" \
"s_andn2_b32 s75, s21, s98\n" \
"s_cbranch_scc0 L_gnx_%=\n" \
"s_sleep 0\n" \
"s_branch L_snx_%=\n" \
"L_gnx_%=:\n" \
"s_bitcmp1_b32 s21, 16\n" \
"s_cbranch_scc0 L_knx16_%=\n" \
B_"v[112:115], v20, s[56:59], s16 offen offset:0 sc1\n" \
"L_knx16_%=:\n" \
"s_bitcmp1_b32 s21, 17\n" \
"s_cbranch_scc0 L_knx17_%=\n" \
B_"v[184:187], v20, s[56:59], s16 offen offset:2048 sc1\n" \
"L_knx17_%=:\n" \
"s_bitcmp1_b32 s21, 18\n" \
"s_cbranch_scc0 L_knx18_%=\n" \
B_"v[120:123], v20, s[56:59], s17 offen offset:0 sc1\n" \
"L_knx18_%=:\n" \
"s_bitcmp1_b32 s21, 19\n" \
"s_cbranch_scc0 L_knx19_%=\n" \
B_"v[192:195], v20, s[56:59], s17 offen offset:2048 sc1\n" \
"L_knx19_%=:\n" \
"s_bitcmp1_b32 s21, 20\n" \
"s_cbranch_scc0 L_knx20_%=\n" \
B_"v[128:131], v20, s[56:59], s18 offen offset:0 sc1\n" \
"L_knx20_%=:\n" \
"s_bitcmp1_b32 s21, 21\n" \
"s_cbranch_scc0 L_knx21_%=\n" \
B_"v[200:203], v20, s[56:59], s18 offen offset:2048 sc1\n" \
"L_knx21_%=:\n" \
"s_bitcmp1_b32 s21, 22\n" \
"s_cbranch_scc0 L_knx22_%=\n" \
B_"v[176:179], v20, s[56:59], s19 offen offset:0 sc1\n" \
"L_knx22_%=:\n" \
"s_bitcmp1_b32 s21, 23\n" \
"s_cbranch_scc0 L_knx23_%=\n" \
B_"v[248:251], v20, s[56:59], s19 offen offset:2048 sc1\n" \
"L_knx23_%=:\n" \
"s_bitcmp1_b32 s21, 8\n" \
"s_cbranch_scc0 L_knx8_%=\n" \
B_"v[188:191], v20, s[56:59], s12 offen offset:1024 sc1\n" \
"L_knx8_%=:\n" \
"s_bitcmp1_b32 s21, 9\n" \
"s_cbranch_scc0 L_knx9_%=\n" \
B_"v[196:199], v20, s[56:59], s12 offen offset:3072 sc1\n" \
"L_knx9_%=:\n" \
"s_waitcnt vmcnt(0)\n" \
"s_branch L_rnx_%=\n" \
"L_sx0_%=:\n" \
"s_sleep 0\n" \
"buffer_load_dword v37, v40, s[56:59], s74 offen sc1\n" \
"s_add_u32 s101, s101, 1\n" \
"s_waitcnt vmcnt(0)\n" \
"v_cmp_eq_u32_e64 s[98:99], s62, v37\n" \
"s_cmp_gt_u32 s101, 0x100000\n" \
"s_cbranch_scc1 L_rx0_%=\n" \
"s_andn2_b32 s75, 0x400, s98\n" \
"s_cbranch_scc1 L_sx0_%=\n" \
"s_branch L_rx0_%=\n" \
"L_sx1_%=:\n" \
"s_sleep 0\n" \
"buffer_load_dword v37, v40, s[56:59], s74 offen sc1\n" \
"s_add_u32 s101, s101, 1\n" \
"s_waitcnt vmcnt(0)\n" \
"v_cmp_eq_u32_e64 s[98:99], s62, v37\n" \
"s_cmp_gt_u32 s101, 0x100000\n" \
"s_cbranch_scc1 L_rx1_%=\n" \
"s_andn2_b32 s75, 0x800, s98\n" \
"s_cbranch_scc1 L_sx1_%=\n" \
"s_branch L_rx1_%=\n" \
"L_sx2_%=:\n" \
"s_sleep 0\n" \
"buffer_load_dword v37, v40, s[56:59], s74 offen sc1\n" \
"s_add_u32 s101, s101, 1\n" \
"s_waitcnt vmcnt(0)\n" \
"v_cmp_eq_u32_e64 s[98:99], s62, v37\n" \
"s_cmp_gt_u32 s101, 0x100000\n" \
"s_cbranch_scc1 L_rx2_%=\n" \
"s_andn2_b32 s75, 0x1000, s98\n" \
"s_cbranch_scc1 L_sx2_%=\n" \
"s_branch L_rx2_%=\n" \
"L_sx3_%=:\n" \
"s_sleep 0\n" \
"buffer_load_dword v37, v40, s[56:59], s74 offen sc1\n" \
"s_add_u32 s101, s101, 1\n" \
"s_waitcnt vmcnt(0)\n" \
"v_cmp_eq_u32_e64 s[98:99], s62, v37\n" \
"s_cmp_gt_u32 s101, 0x100000\n" \
"s_cbranch_scc1 L_rx3_%=\n" \
"s_andn2_b32 s75, 0x2000, s98\n" \
"s_cbranch_scc1 L_sx3_%=\n" \
"s_branch L_rx3_%=\n" \
"L_sx4_%=:\n" \
"s_sleep 0\n" \
"buffer_load_dword v37, v40, s[56:59], s74 offen sc1\n" \
"s_add_u32 s101, s101, 1\n" \
"s_waitcnt vmcnt(0)\n" \
"v_cmp_eq_u32_e64 s[98:99], s62, v37\n" \
"s_cmp_gt_u32 s101, 0x100000\n" \
"s_cbranch_scc1 L_rx4_%=\n" \
"s_andn2_b32 s75, 0x4000, s98\n" \
"s_cbranch_scc1 L_sx4_%=\n" \
"s_branch L_rx4_%=\n" \
"L_att_stub:\n" \
"s_mov_b32 s101, 0\n" \
"v_add_u32_e32 v32, 0x49000, v20\n" \
"v_readlane_b32 s96, v11, 39\n" \
"v_readlane_b32 s97, v11, 40\n" \
B_"a[192:195], v20, s[56:59], s60 offen offset:0 sc1\n" \
B_"a[208:211], v32, s[56:59], s60 offen offset:0 sc1\n" \
B_"a[196:199], v20, s[56:59], s60 offen offset:1024 sc1\n" \
B_"a[212:215], v32, s[56:59], s60 offen offset:1024 sc1\n" \
B_"a[200:203], v20, s[56:59], s60 offen offset:2048 sc1\n" \
B_"a[216:219], v32, s[56:59], s60 offen offset:2048 sc1\n" \
B_"a[204:207], v20, s[56:59], s60 offen offset:3072 sc1\n" \
B_"a[220:223], v32, s[56:59], s60 offen offset:3072 sc1\n" \
B_"a[124:127], v20, s[56:59], s96 offen offset:0 sc1\n" \
B_"a[228:231], v32, s[56:59], s96 offen offset:0 sc1\n" \
B_"a[236:239], v20, s[56:59], s97 offen offset:0 sc1\n" \
B_"a[244:247], v32, s[56:59], s97 offen offset:0 sc1\n" \
B_"a[224:227], v20, s[56:59], s96 offen offset:1024 sc1\n" \
B_"a[232:235], v32, s[56:59], s96 offen offset:1024 sc1\n" \
B_"a[240:243], v20, s[56:59], s97 offen offset:1024 sc1\n" \
B_"a[248:251], v32, s[56:59], s97 offen offset:1024 sc1\n" \
B_"v[64:67], v20, s[56:59], s61 offen offset:0 sc1\n" \
B_"v[68:71], v20, s[56:59], s61 offen offset:1024 sc1\n" \
B_"v[72:75], v20, s[56:59], s61 offen offset:2048 sc1\n" \
B_"v[76:79], v20, s[56:59], s61 offen offset:3072 sc1\n" \
B_"v[80:83], v32, s[56:59], s61 offen offset:0 sc1\n" \
B_"v[84:87], v32, s[56:59], s61 offen offset:1024 sc1\n" \
B_"v[96:99], v32, s[56:59], s61 offen offset:2048 sc1\n" \
B_"v[100:103], v32, s[56:59], s61 offen offset:3072 sc1\n" \
"v_not_b32_e32 v26, s62\n" \
"s_waitcnt vmcnt(0)\n" \
"s_branch L_out_%=\n" \
".fill (L_ab + 16384 - .) / 4, 4, 0xBF800000\n" \
".fill (L_ab + 16384 - .) / 4, 4, 0xBF800000\n" \
"L_ap4:\n" \
"s_setpc_b64 s[88:89]\n" \
"L_gsw:\n" \
M_"v[104:107], a[128:131], a[16:19], v[36:39]\n" \
"s_add_u32 s68, s60, 0xfff6e000\n" \
"s_add_u32 s69, s60, 0xfffb7000\n" \
"s_add_u32 s70, s60, 0x0\n" \
"s_add_u32 s71, s60, 0x49000\n" \
M_"v[104:107], a[132:135], a[20:23], v[104:107]\n" \
"s_xor_b32 m0, s65, 0x12000\n" \
R_"a[160:163], v49 offset:0\n" \
M_"v[104:107], a[136:139], a[24:27], v[104:107]\n" \
B_"v50, s[8:11], s64 offen offset:0 sc1 lds\n" \
M_"v[104:107], a[140:143], a[28:31], v[104:107]\n" \
R_"a[164:167], v49 offset:1024\n" \
R_"a[168:171], v49 offset:2048\n" \
M_"v[108:111], a[156:159], a[28:31], v[36:39]\n" \
R_"a[172:175], v49 offset:3072\n" \
R_"a[176:179], v49 offset:4096\n" \
M_"v[108:111], a[152:155], a[24:27], v[108:111]\n" \
B_"v50, s[8:11], s64 offen offset:1024 sc1 lds\n" \
M_"v[108:111], a[148:151], a[20:23], v[108:111]\n" \
"s_mov_b32 s65, s13\n" \
"v_add_u32_e32 v63, s15, v20\n" \
M_"v[108:111], a[144:147], a[16:19], v[108:111]\n" \
"s_add_u32 s67, s14, 40960\n" \
"v_add_u32_e32 v50, 0x6000, v63\n" \
M_"v[116:119], a[144:147], a[32:35], v[40:43]\n" \
"s_xor_b32 m0, s65, 0x8000\n" \
R_"a[180:183], v49 offset:5120\n" \
M_"v[116:119], a[148:151], a[36:39], v[116:119]\n" \
R_"a[184:187], v49 offset:6144\n" \
"v_and_b32_e32 v68, v12, v68\n" \
M_"v[116:119], a[152:155], a[40:43], v[116:119]\n" \
"v_and_b32_e32 v69, v13, v69\n" \
"v_and_b32_e32 v70, v14, v70\n" \
M_"v[116:119], a[156:159], a[44:47], v[116:119]\n" \
B_"v50, s[8:11], s67 offen offset:0 sc1 lds\n" \
M_"v[112:115], a[140:143], a[44:47], v[40:43]\n" \
"v_and_b32_e32 v71, v15, v71\n" \
R_"a[188:191], v49 offset:7168\n" \
M_"v[112:115], a[136:139], a[40:43], v[112:115]\n" \
E_"v86, v86\n" \
M_"v[112:115], a[132:135], a[36:39], v[112:115]\n" \
E_"v87, v87\n" \
M_"v[112:115], a[128:131], a[32:35], v[112:115]\n" \
E_"v84, v84\n" \
M_"v[120:123], a[128:131], a[48:51], v[44:47]\n" \
B_"v50, s[8:11], s67 offen offset:1024 sc1 lds\n" \
M_"v[120:123], a[132:135], a[52:55], v[120:123]\n" \
E_"v85, v85\n" \
M_"v[120:123], a[136:139], a[56:59], v[120:123]\n" \
C_"v87, v86, v87\n" \
C_"v86, v84, v85\n" \
M_"v[120:123], a[140:143], a[60:63], v[120:123]\n" \
E_"v82, v82\n" \
M_"v[124:127], a[156:159], a[60:63], v[44:47]\n" \
E_"v83, v83\n" \
M_"v[124:127], a[152:155], a[56:59], v[124:127]\n" \
E_"v80, v80\n" \
M_"v[124:127], a[148:151], a[52:55], v[124:127]\n" \
B_"v50, s[8:11], s67 offen offset:2048 sc1 lds\n" \
M_"v[124:127], a[144:147], a[48:51], v[124:127]\n" \
C_"v85, v82, v83\n" \
"s_waitcnt lgkmcnt(1)\n" \
M_"v[128:131], a[160:163], v[68:71], v[128:131]\n" \
E_"v81, v81\n" \
M_"v[160:163], a[160:163], v[72:75], v[160:163]\n" \
C_"v84, v80, v81\n" \
"s_mov_b32 s64, s14\n" \
M_"v[132:135], a[164:167], v[68:71], v[132:135]\n" \
E_"v88, v88\n" \
M_"v[164:167], a[164:167], v[72:75], v[164:167]\n" \
E_"v89, v89\n" \
M_"v[136:139], a[168:171], v[68:71], v[136:139]\n" \
B_"v50, s[8:11], s67 offen offset:3072 sc1 lds\n" \
M_"v[168:171], a[168:171], v[72:75], v[168:171]\n" \
E_"v90, v90\n" \
M_"v[140:143], a[172:175], v[68:71], v[140:143]\n" \
E_"v91, v91\n" \
M_"v[172:175], a[172:175], v[72:75], v[172:175]\n" \
C_"v88, v88, v89\n" \
C_"v89, v90, v91\n" \
M_"v[144:147], a[176:179], v[68:71], v[144:147]\n" \
E_"v92, v92\n" \
M_"v[176:179], a[176:179], v[72:75], v[176:179]\n" \
E_"v93, v93\n" \
M_"v[148:151], a[180:183], v[68:71], v[148:151]\n" \
E_"v94, v94\n" \
M_"v[180:183], a[180:183], v[72:75], v[180:183]\n" \
E_"v95, v95\n" \
M_"v[152:155], a[184:187], v[68:71], v[152:155]\n" \
C_"v90, v92, v93\n" \
C_"v91, v94, v95\n" \
M_"v[184:187], a[184:187], v[72:75], v[184:187]\n" \
"v_add_u32_e32 v51, 0x12000, v63\n" \
"s_waitcnt lgkmcnt(0)\n" \
M_"v[156:159], a[188:191], v[68:71], v[156:159]\n" \
"s_xor_b32 m0, s65, 0xa000\n" \
"ds_read_b32 v58, v18 offset:512\n" \
M_"v[188:191], a[188:191], v[72:75], v[188:191]\n" \
B_"v51, s[8:11], s64 offen offset:0 sc1 lds\n" \
"v_smfmac_f32_16x16x64_bf16 a[96:99], a[104:107], v[68:75], v21\n" \
B_"v51, s[8:11], s64 offen offset:1024 sc1 lds\n" \
M_"v[220:223], a[188:191], v[84:87], v[220:223]\n" \
"ds_read_b32 v59, v18 offset:640\n" \
"ds_read_b32 v60, v18 offset:768\n" \
M_"v[252:255], a[188:191], v[88:91], v[252:255]\n" \
"ds_read_b32 v61, v18 offset:896\n" \
R_"a[128:131], v48 offset:0\n" \
M_"v[216:219], a[184:187], v[84:87], v[216:219]\n" \
R_"a[144:147], v48 offset:4096\n" \
R_"a[132:135], v48 offset:1024\n" \
M_"v[248:251], a[184:187], v[88:91], v[248:251]\n" \
R_"a[148:151], v48 offset:5120\n" \
R_"a[136:139], v48 offset:2048\n" \
M_"v[212:215], a[180:183], v[84:87], v[212:215]\n" \
R_"a[152:155], v48 offset:6144\n" \
R_"a[140:143], v48 offset:3072\n" \
M_"v[244:247], a[180:183], v[88:91], v[244:247]\n" \
R_"a[156:159], v48 offset:7168\n" \
"v_add_u32_e32 v52, 0x14000, v63\n" \
M_"v[208:211], a[176:179], v[84:87], v[208:211]\n" \
B_"v51, s[8:11], s64 offen offset:2048 sc1 lds\n" \
M_"v[240:243], a[176:179], v[88:91], v[240:243]\n" \
E_"v104, v104\n" \
M_"v[204:207], a[172:175], v[84:87], v[204:207]\n" \
E_"v105, v105\n" \
M_"v[236:239], a[172:175], v[88:91], v[236:239]\n" \
B_"v51, s[8:11], s64 offen offset:3072 sc1 lds\n" \
M_"v[200:203], a[168:171], v[84:87], v[200:203]\n" \
E_"v106, v106\n" \
M_"v[232:235], a[168:171], v[88:91], v[232:235]\n" \
E_"v107, v107\n" \
M_"v[196:199], a[164:167], v[84:87], v[196:199]\n" \
C_"v104, v104, v105\n" \
C_"v105, v106, v107\n" \
M_"v[228:231], a[164:167], v[88:91], v[228:231]\n" \
E_"v108, v108\n" \
M_"v[192:195], a[160:163], v[84:87], v[192:195]\n" \
E_"v109, v109\n" \
M_"v[224:227], a[160:163], v[88:91], v[224:227]\n" \
E_"v110, v110\n" \
"v_smfmac_f32_16x16x64_bf16 a[100:103], a[104:107], v[84:91], v21\n" \
E_"v111, v111\n" \
C_"v106, v108, v109\n" \
C_"v107, v110, v111\n" \
"v_and_b32_e32 v104, v12, v104\n" \
"v_and_b32_e32 v105, v13, v105\n" \
"v_and_b32_e32 v106, v14, v106\n" \
"v_and_b32_e32 v107, v15, v107\n" \
"v_accvgpr_read_b32 v54, a96\n" \
"v_accvgpr_read_b32 v22, a97\n" \
"v_add_u32_e32 v53, 0x20000, v63\n" \
E_"v118, v118\n" \
E_"v119, v119\n" \
E_"v116, v116\n" \
E_"v117, v117\n" \
C_"v119, v118, v119\n" \
E_"v114, v114\n" \
E_"v115, v115\n" \
E_"v112, v112\n" \
E_"v113, v113\n" \
C_"v118, v116, v117\n" \
C_"v117, v114, v115\n" \
C_"v116, v112, v113\n" \
"v_readlane_b32 s93, v11, 22\n" \
"s_waitcnt vmcnt(10) lgkmcnt(0)\n" \
"s_barrier\n" \
M_"v[80:83], a[128:131], a[32:35], v[40:43]\n" \
R_"a[160:163], v49 offset:8192\n" \
R_"a[164:167], v49 offset:9216\n" \
M_"v[80:83], a[132:135], a[36:39], v[80:83]\n" \
R_"a[168:171], v49 offset:10240\n" \
"s_xor_b32 m0, s65, 0x18000\n" \
M_"v[80:83], a[136:139], a[40:43], v[80:83]\n" \
B_"v52, s[8:11], s64 offen offset:0 sc1 lds\n" \
M_"v[80:83], a[140:143], a[44:47], v[80:83]\n" \
R_"a[172:175], v49 offset:11264\n" \
R_"a[176:179], v49 offset:12288\n" \
M_"v[84:87], a[156:159], a[44:47], v[40:43]\n" \
R_"a[180:183], v49 offset:13312\n" \
R_"a[184:187], v49 offset:14336\n" \
M_"v[84:87], a[152:155], a[40:43], v[84:87]\n" \
B_"v52, s[8:11], s64 offen offset:1024 sc1 lds\n" \
M_"v[84:87], a[148:151], a[36:39], v[84:87]\n" \
R_"a[188:191], v49 offset:15360\n" \
"v_readlane_b32 s80, v11, 11\n" \
M_"v[84:87], a[144:147], a[32:35], v[84:87]\n" \
B_"v52, s[8:11], s64 offen offset:2048 sc1 lds\n" \
M_"v[92:95], a[144:147], a[48:51], v[44:47]\n" \
E_"v120, v120\n" \
M_"v[92:95], a[148:151], a[52:55], v[92:95]\n" \
E_"v121, v121\n" \
M_"v[92:95], a[152:155], a[56:59], v[92:95]\n" \
B_"v52, s[8:11], s64 offen offset:3072 sc1 lds\n" \
M_"v[92:95], a[156:159], a[60:63], v[92:95]\n" \
"s_mov_b32 m0, s65\n" \
C_"v120, v120, v121\n" \
M_"v[88:91], a[140:143], a[60:63], v[44:47]\n" \
E_"v122, v122\n" \
M_"v[88:91], a[136:139], a[56:59], v[88:91]\n" \
B_"v53, s[8:11], s64 offen offset:0 sc1 lds\n" \
M_"v[88:91], a[132:135], a[52:55], v[88:91]\n" \
E_"v123, v123\n" \
M_"v[88:91], a[128:131], a[48:51], v[88:91]\n" \
E_"v124, v124\n" \
M_"a[96:99], a[116:119], v[104:107], a[96:99]\n" \
C_"v121, v122, v123\n" \
"s_waitcnt lgkmcnt(5)\n" \
M_"v[160:163], a[160:163], v[104:107], v[160:163]\n" \
B_"v53, s[8:11], s64 offen offset:1024 sc1 lds\n" \
M_"v[164:167], a[164:167], v[104:107], v[164:167]\n" \
E_"v125, v125\n" \
M_"v[168:171], a[168:171], v[104:107], v[168:171]\n" \
C_"v122, v124, v125\n" \
"s_waitcnt lgkmcnt(1)\n" \
M_"v[172:175], a[172:175], v[104:107], v[172:175]\n" \
E_"v126, v126\n" \
M_"v[176:179], a[176:179], v[104:107], v[176:179]\n" \
E_"v127, v127\n" \
M_"v[180:183], a[180:183], v[104:107], v[180:183]\n" \
C_"v123, v126, v127\n" \
"v_readlane_b32 s81, v11, 12\n" \
M_"v[184:187], a[184:187], v[104:107], v[184:187]\n" \
"s_mov_b32 s82, 0x47fca0\n" \
"s_waitcnt lgkmcnt(0)\n" \
M_"v[188:191], a[188:191], v[104:107], v[188:191]\n" \
B_"v53, s[8:11], s64 offen offset:2048 sc1 lds\n" \
M_"v[220:223], a[188:191], v[116:119], v[220:223]\n" \
B_"v53, s[8:11], s64 offen offset:3072 sc1 lds\n" \
M_"v[252:255], a[188:191], v[120:123], v[252:255]\n" \
"s_mov_b32 s83, 0x20000\n" \
"v_readlane_b32 s92, v11, 29\n" \
M_"v[216:219], a[184:187], v[116:119], v[216:219]\n" \
"s_add_u32 m0, s93, 0x0\n" \
R_"a[128:131], v48 offset:8192\n" \
M_"v[248:251], a[184:187], v[120:123], v[248:251]\n" \
R_"a[144:147], v48 offset:12288\n" \
R_"a[132:135], v48 offset:9216\n" \
M_"v[212:215], a[180:183], v[116:119], v[212:215]\n" \
B_"v0, s[80:83], s92 offen offset:0 lds\n" \
M_"v[244:247], a[180:183], v[120:123], v[244:247]\n" \
B_"v1, s[80:83], s92 offen offset:1024 lds\n" \
M_"v[208:211], a[176:179], v[116:119], v[208:211]\n" \
R_"a[148:151], v48 offset:13312\n" \
R_"a[136:139], v48 offset:10240\n" \
M_"v[240:243], a[176:179], v[120:123], v[240:243]\n" \
R_"a[152:155], v48 offset:14336\n" \
R_"a[140:143], v48 offset:11264\n" \
M_"v[204:207], a[172:175], v[116:119], v[204:207]\n" \
B_"v2, s[80:83], s92 offen offset:2048 lds\n" \
M_"v[236:239], a[172:175], v[120:123], v[236:239]\n" \
R_"a[156:159], v48 offset:15360\n" \
"s_xor_b32 s65, s65, 0x8000\n" \
M_"v[200:203], a[168:171], v[116:119], v[200:203]\n" \
"s_add_u32 s64, s64, 0x10000\n" \
"v_add_u32_e32 v49, 0x10000, v48\n" \
M_"v[232:235], a[168:171], v[120:123], v[232:235]\n" \
B_"v3, s[80:83], s92 offen offset:3072 lds\n" \
M_"v[196:199], a[164:167], v[116:119], v[196:199]\n" \
E_"v86, v86\n" \
M_"v[228:231], a[164:167], v[120:123], v[228:231]\n" \
E_"v87, v87\n" \
M_"v[192:195], a[160:163], v[116:119], v[192:195]\n" \
E_"v84, v84\n" \
M_"v[224:227], a[160:163], v[120:123], v[224:227]\n" \
E_"v85, v85\n" \
"v_smfmac_f32_16x16x64_bf16 a[100:103], a[104:107], v[116:123], v21\n" \
E_"v88, v88\n" \
E_"v89, v89\n" \
C_"v87, v86, v87\n" \
E_"v82, v82\n" \
E_"v83, v83\n" \
E_"v80, v80\n" \
E_"v81, v81\n" \
E_"v90, v90\n" \
E_"v91, v91\n" \
C_"v86, v84, v85\n" \
C_"v88, v88, v89\n" \
E_"v92, v92\n" \
E_"v93, v93\n" \
E_"v94, v94\n" \
E_"v95, v95\n" \
C_"v85, v82, v83\n" \
C_"v84, v80, v81\n" \
C_"v89, v90, v91\n" \
"v_and_b32_e32 v84, v12, v84\n" \
"v_and_b32_e32 v85, v13, v85\n" \
"v_and_b32_e32 v86, v14, v86\n" \
"v_and_b32_e32 v87, v15, v87\n" \
C_"v90, v92, v93\n" \
C_"v91, v94, v95\n" \
"v_add_u32_e32 v48, s16, v20\n" \
"v_add_f32_e32 v54, v54, v22\n" \
"v_accvgpr_read_b32 v55, a98\n" \
"v_accvgpr_read_b32 v23, a99\n" \
M_"a[96:99], a[120:123], a[120:123], 0\n" \
"v_max_f32_e32 v62, v62, v54\n" \
"v_add_f32_e32 v55, v55, v23\n" \
"v_max_f32_e32 v62, v62, v55\n" \
"v_rcp_f32_e32 v54, v54\n" \
"v_rcp_f32_e32 v55, v55\n" \
"v_mul_f32_e32 v54, v54, v58\n" \
"v_mul_f32_e32 v55, v55, v59\n" \
"v_mul_f32_e32 v128, v54, v128\n" \
"v_mul_f32_e32 v129, v54, v129\n" \
"v_mul_f32_e32 v136, v54, v136\n" \
"v_mul_f32_e32 v137, v54, v137\n" \
"v_mul_f32_e32 v144, v54, v144\n" \
"v_mul_f32_e32 v145, v54, v145\n" \
"v_mul_f32_e32 v152, v54, v152\n" \
"v_mul_f32_e32 v153, v54, v153\n" \
"v_mul_f32_e32 v160, v55, v160\n" \
"v_mul_f32_e32 v161, v55, v161\n" \
"v_mul_f32_e32 v168, v55, v168\n" \
"v_mul_f32_e32 v169, v55, v169\n" \
"v_mul_f32_e32 v176, v55, v176\n" \
"v_mul_f32_e32 v177, v55, v177\n" \
"v_mul_f32_e32 v184, v55, v184\n" \
"v_mul_f32_e32 v185, v55, v185\n" \
C_"v128, v128, v129\n" \
"v_mul_f32_e32 v130, v54, v130\n" \
"v_mul_f32_e32 v131, v54, v131\n" \
C_"v136, v136, v137\n" \
"v_mul_f32_e32 v138, v54, v138\n" \
"v_mul_f32_e32 v139, v54, v139\n" \
C_"v144, v144, v145\n" \
"v_mul_f32_e32 v146, v54, v146\n" \
"v_mul_f32_e32 v147, v54, v147\n" \
C_"v152, v152, v153\n" \
"v_mul_f32_e32 v154, v54, v154\n" \
"v_mul_f32_e32 v155, v54, v155\n" \
C_"v160, v160, v161\n" \
"v_mul_f32_e32 v162, v55, v162\n" \
"v_mul_f32_e32 v163, v55, v163\n" \
C_"v168, v168, v169\n" \
"v_mul_f32_e32 v170, v55, v170\n" \
"v_mul_f32_e32 v171, v55, v171\n" \
C_"v176, v176, v177\n" \
"v_mul_f32_e32 v178, v55, v178\n" \
"v_mul_f32_e32 v179, v55, v179\n" \
C_"v184, v184, v185\n" \
"v_mul_f32_e32 v186, v55, v186\n" \
"v_mul_f32_e32 v187, v55, v187\n" \
C_"v129, v130, v131\n" \
"v_mul_f32_e32 v132, v54, v132\n" \
"v_mul_f32_e32 v133, v54, v133\n" \
"v_mul_f32_e32 v134, v54, v134\n" \
"v_mul_f32_e32 v135, v54, v135\n" \
C_"v137, v138, v139\n" \
"v_mul_f32_e32 v140, v54, v140\n" \
"s_waitcnt vmcnt(12) lgkmcnt(0)\n" \
"s_barrier\n" \
M_"v[120:123], a[128:131], a[48:51], v[44:47]\n" \
"s_xor_b32 m0, s65, 0x12000\n" \
R_"a[188:191], v49 offset:7168\n" \
M_"v[120:123], a[132:135], a[52:55], v[120:123]\n" \
B_"v50, s[8:11], s64 offen offset:0 sc1 lds\n" \
M_"v[120:123], a[136:139], a[56:59], v[120:123]\n" \
R_"a[184:187], v49 offset:6144\n" \
R_"a[180:183], v49 offset:5120\n" \
M_"v[120:123], a[140:143], a[60:63], v[120:123]\n" \
B_"v50, s[8:11], s64 offen offset:1024 sc1 lds\n" \
M_"v[124:127], a[156:159], a[60:63], v[44:47]\n" \
R_"a[176:179], v49 offset:4096\n" \
R_"a[172:175], v49 offset:3072\n" \
M_"v[124:127], a[152:155], a[56:59], v[124:127]\n" \
R_"a[168:171], v49 offset:2048\n" \
R_"a[164:167], v49 offset:1024\n" \
M_"v[124:127], a[148:151], a[52:55], v[124:127]\n" \
R_"a[160:163], v49 offset:0\n" \
R_"a[0:3], v49 offset:8192\n" \
M_"v[124:127], a[144:147], a[48:51], v[124:127]\n" \
B_"v50, s[8:11], s64 offen offset:2048 sc1 lds\n" \
M_"v[32:35], a[124:127], a[64:67], 0\n" \
R_"a[4:7], v49 offset:9216\n" \
R_"a[8:11], v49 offset:10240\n" \
M_"v[36:39], a[124:127], a[80:83], 0\n" \
R_"a[12:15], v49 offset:11264\n" \
R_"a[16:19], v49 offset:12288\n" \
M_"v[40:43], a[124:127], a[192:195], 0\n" \
R_"a[20:23], v49 offset:13312\n" \
R_"a[24:27], v49 offset:14336\n" \
M_"v[44:47], a[124:127], a[208:211], 0\n" \
B_"v50, s[8:11], s64 offen offset:3072 sc1 lds\n" \
M_"v[32:35], a[224:227], a[68:71], v[32:35]\n" \
"s_xor_b32 m0, s65, 0xa000\n" \
R_"a[28:31], v49 offset:15360\n" \
M_"v[36:39], a[224:227], a[84:87], v[36:39]\n" \
E_"v120, v120\n" \
M_"v[40:43], a[224:227], a[196:199], v[40:43]\n" \
B_"v51, s[8:11], s64 offen offset:0 sc1 lds\n" \
M_"v[44:47], a[224:227], a[212:215], v[44:47]\n" \
E_"v121, v121\n" \
M_"v[32:35], a[228:231], a[72:75], v[32:35]\n" \
E_"v122, v122\n" \
M_"v[36:39], a[228:231], a[88:91], v[36:39]\n" \
E_"v123, v123\n" \
M_"v[40:43], a[228:231], a[200:203], v[40:43]\n" \
B_"v51, s[8:11], s64 offen offset:1024 sc1 lds\n" \
M_"v[44:47], a[228:231], a[216:219], v[44:47]\n" \
C_"v120, v120, v121\n" \
C_"v121, v122, v123\n" \
M_"v[32:35], a[232:235], a[76:79], v[32:35]\n" \
E_"v124, v124\n" \
M_"v[36:39], a[232:235], a[92:95], v[36:39]\n" \
E_"v125, v125\n" \
M_"v[40:43], a[232:235], a[204:207], v[40:43]\n" \
B_"v51, s[8:11], s64 offen offset:2048 sc1 lds\n" \
M_"v[44:47], a[232:235], a[220:223], v[44:47]\n" \
E_"v126, v126\n" \
"v_smfmac_f32_16x16x64_bf16 a[100:103], a[104:107], v[84:91], v21\n" \
C_"v122, v124, v125\n" \
"s_waitcnt lgkmcnt(1)\n" \
M_"v[220:223], a[188:191], v[84:87], v[220:223]\n" \
B_"v51, s[8:11], s64 offen offset:3072 sc1 lds\n" \
M_"v[252:255], a[188:191], v[88:91], v[252:255]\n" \
"s_add_u32 m0, s93, 0x1000\n" \
"v_and_b32_e32 v120, v12, v120\n" \
M_"v[216:219], a[184:187], v[84:87], v[216:219]\n" \
E_"v127, v127\n" \
M_"v[248:251], a[184:187], v[88:91], v[248:251]\n" \
C_"v123, v126, v127\n" \
"v_and_b32_e32 v121, v13, v121\n" \
M_"v[212:215], a[180:183], v[84:87], v[212:215]\n" \
B_"v4, s[80:83], s92 offen offset:0 lds\n" \
M_"v[244:247], a[180:183], v[88:91], v[244:247]\n" \
"v_and_b32_e32 v122, v14, v122\n" \
"v_and_b32_e32 v123, v15, v123\n" \
M_"v[208:211], a[176:179], v[84:87], v[208:211]\n" \
"v_accvgpr_read_b32 v56, a100\n" \
"v_accvgpr_read_b32 v24, a101\n" \
M_"v[240:243], a[176:179], v[88:91], v[240:243]\n" \
R_"a[128:131], v48 offset:0\n" \
R_"a[144:147], v48 offset:4096\n" \
M_"v[204:207], a[172:175], v[84:87], v[204:207]\n" \
B_"v5, s[80:83], s92 offen offset:1024 lds\n" \
M_"v[236:239], a[172:175], v[88:91], v[236:239]\n" \
R_"a[132:135], v48 offset:1024\n" \
R_"a[148:151], v48 offset:5120\n" \
M_"v[200:203], a[168:171], v[84:87], v[200:203]\n" \
R_"a[136:139], v48 offset:2048\n" \
R_"a[152:155], v48 offset:6144\n" \
M_"v[232:235], a[168:171], v[88:91], v[232:235]\n" \
B_"v6, s[80:83], s92 offen offset:2048 lds\n" \
M_"v[196:199], a[164:167], v[84:87], v[196:199]\n" \
R_"a[140:143], v48 offset:3072\n" \
R_"a[156:159], v48 offset:7168\n" \
M_"v[228:231], a[164:167], v[88:91], v[228:231]\n" \
"v_mul_f32_e32 v141, v54, v141\n" \
"v_mul_f32_e32 v142, v54, v142\n" \
M_"v[192:195], a[160:163], v[84:87], v[192:195]\n" \
"v_mul_f32_e32 v143, v54, v143\n" \
C_"v145, v146, v147\n" \
M_"v[224:227], a[160:163], v[88:91], v[224:227]\n" \
B_"v7, s[80:83], s92 offen offset:3072 lds\n" \
M_"a[100:103], a[116:119], v[120:123], a[100:103]\n" \
"v_mul_f32_e32 v148, v54, v148\n" \
"v_mul_f32_e32 v149, v54, v149\n" \
M_"v[224:227], a[0:3], v[120:123], v[224:227]\n" \
"v_mul_f32_e32 v150, v54, v150\n" \
"v_mul_f32_e32 v151, v54, v151\n" \
M_"v[228:231], a[4:7], v[120:123], v[228:231]\n" \
C_"v153, v154, v155\n" \
"v_mul_f32_e32 v156, v54, v156\n" \
M_"v[232:235], a[8:11], v[120:123], v[232:235]\n" \
"v_mul_f32_e32 v157, v54, v157\n" \
"v_mul_f32_e32 v158, v54, v158\n" \
M_"v[236:239], a[12:15], v[120:123], v[236:239]\n" \
"v_mul_f32_e32 v159, v54, v159\n" \
C_"v161, v162, v163\n" \
M_"v[240:243], a[16:19], v[120:123], v[240:243]\n" \
"v_mul_f32_e32 v164, v55, v164\n" \
"v_mul_f32_e32 v165, v55, v165\n" \
M_"v[244:247], a[20:23], v[120:123], v[244:247]\n" \
"v_mul_f32_e32 v166, v55, v166\n" \
"v_mul_f32_e32 v167, v55, v167\n" \
M_"v[248:251], a[24:27], v[120:123], v[248:251]\n" \
C_"v169, v170, v171\n" \
"s_waitcnt lgkmcnt(4)\n" \
M_"v[252:255], a[28:31], v[120:123], v[252:255]\n" \
"v_mul_f32_e32 v172, v55, v172\n" \
"v_mul_f32_e32 v173, v55, v173\n" \
"v_mul_f32_e32 v174, v55, v174\n" \
"v_mul_f32_e32 v175, v55, v175\n" \
C_"v177, v178, v179\n" \
"v_mul_f32_e32 v180, v55, v180\n" \
"v_mul_f32_e32 v181, v55, v181\n" \
"v_mul_f32_e32 v182, v55, v182\n" \
"v_mul_f32_e32 v183, v55, v183\n" \
C_"v185, v186, v187\n" \
"v_mul_f32_e32 v188, v55, v188\n" \
"v_mul_f32_e32 v189, v55, v189\n" \
"v_mul_f32_e32 v190, v55, v190\n" \
"v_mul_f32_e32 v191, v55, v191\n" \
C_"v130, v132, v133\n" \
C_"v131, v134, v135\n" \
"buffer_store_dwordx4 v[128:131], v20, s[56:59], s68 offen offset:0 sc1\n" \
C_"v138, v140, v141\n" \
C_"v139, v142, v143\n" \
"buffer_store_dwordx4 v[136:139], v20, s[56:59], s68 offen offset:1024 sc1\n" \
C_"v146, v148, v149\n" \
C_"v147, v150, v151\n" \
"buffer_store_dwordx4 v[144:147], v20, s[56:59], s68 offen offset:2048 sc1\n" \
C_"v154, v156, v157\n" \
C_"v155, v158, v159\n" \
"buffer_store_dwordx4 v[152:155], v20, s[56:59], s68 offen offset:3072 sc1\n" \
C_"v162, v164, v165\n" \
C_"v163, v166, v167\n" \
"buffer_store_dwordx4 v[160:163], v20, s[56:59], s69 offen offset:0 sc1\n" \
C_"v170, v172, v173\n" \
C_"v171, v174, v175\n" \
"buffer_store_dwordx4 v[168:171], v20, s[56:59], s69 offen offset:1024 sc1\n" \
C_"v178, v180, v181\n" \
C_"v179, v182, v183\n" \
"buffer_store_dwordx4 v[176:179], v20, s[56:59], s69 offen offset:2048 sc1\n" \
C_"v186, v188, v189\n" \
C_"v187, v190, v191\n" \
"buffer_store_dwordx4 v[184:187], v20, s[56:59], s69 offen offset:3072 sc1\n" \
"s_add_u32 m0, s93, 0x2000\n" \
M_"a[112:115], a[120:123], v[128:131], a[112:115]\n" \
B_"v8, s[80:83], s92 offen offset:0 lds\n" \
M_"a[112:115], a[120:123], v[136:139], a[112:115]\n" \
"v_mfma_f32_32x32x16_bf16 v[128:143], a[120:123], a[120:123], 0\n" \
M_"a[112:115], a[120:123], v[144:147], a[112:115]\n" \
M_"a[112:115], a[120:123], v[152:155], a[112:115]\n" \
"v_mfma_f32_32x32x16_bf16 v[144:159], a[120:123], a[120:123], 0\n" \
"v_add_f32_e32 v56, v56, v24\n" \
M_"a[112:115], a[120:123], v[160:163], a[112:115]\n" \
"v_max_f32_e32 v62, v62, v56\n" \
"v_accvgpr_read_b32 v57, a102\n" \
"s_waitcnt vmcnt(21) lgkmcnt(0)\n" \
"s_barrier\n" \
M_"v[64:67], a[128:131], a[64:67], v[32:35]\n" \
"s_xor_b32 m0, s65, 0x18000\n" \
"v_accvgpr_read_b32 v25, a103\n" \
M_"a[100:103], a[120:123], a[120:123], 0\n" \
B_"v52, s[8:11], s64 offen offset:0 sc1 lds\n" \
M_"v[64:67], a[132:135], a[68:71], v[64:67]\n" \
"v_rcp_f32_e32 v56, v56\n" \
M_"v[64:67], a[136:139], a[72:75], v[64:67]\n" \
M_"a[112:115], a[120:123], v[168:171], a[112:115]\n" \
"v_mfma_f32_32x32x16_bf16 v[160:175], a[120:123], a[120:123], 0\n" \
"v_mul_f32_e32 v56, v56, v60\n" \
M_"a[112:115], a[120:123], v[176:179], a[112:115]\n" \
"v_mul_f32_e32 v192, v56, v192\n" \
"v_mul_f32_e32 v193, v56, v193\n" \
M_"v[64:67], a[140:143], a[76:79], v[64:67]\n" \
B_"v52, s[8:11], s64 offen offset:1024 sc1 lds\n" \
M_"v[68:71], a[156:159], a[76:79], v[32:35]\n" \
C_"v192, v192, v193\n" \
"v_mul_f32_e32 v194, v56, v194\n" \
M_"v[68:71], a[152:155], a[72:75], v[68:71]\n" \
"v_mul_f32_e32 v195, v56, v195\n" \
C_"v193, v194, v195\n" \
M_"v[68:71], a[148:151], a[68:71], v[68:71]\n" \
M_"a[112:115], a[120:123], v[184:187], a[112:115]\n" \
"v_mfma_f32_32x32x16_bf16 v[176:191], a[120:123], a[120:123], 0\n" \
"v_mul_f32_e32 v196, v56, v196\n" \
"v_mul_f32_e32 v197, v56, v197\n" \
"v_mul_f32_e32 v198, v56, v198\n" \
"v_mul_f32_e32 v199, v56, v199\n" \
"v_mul_f32_e32 v200, v56, v200\n" \
"v_mul_f32_e32 v201, v56, v201\n" \
M_"v[68:71], a[144:147], a[64:67], v[68:71]\n" \
B_"v52, s[8:11], s64 offen offset:2048 sc1 lds\n" \
M_"v[76:79], a[144:147], a[80:83], v[36:39]\n" \
C_"v194, v196, v197\n" \
C_"v195, v198, v199\n" \
"buffer_store_dwordx4 v[192:195], v20, s[56:59], s70 offen offset:0 sc1\n" \
M_"v[76:79], a[148:151], a[84:87], v[76:79]\n" \
C_"v200, v200, v201\n" \
"v_mul_f32_e32 v202, v56, v202\n" \
M_"v[76:79], a[152:155], a[88:91], v[76:79]\n" \
"v_mul_f32_e32 v203, v56, v203\n" \
"v_add_f32_e32 v57, v57, v25\n" \
M_"v[76:79], a[156:159], a[92:95], v[76:79]\n" \
B_"v52, s[8:11], s64 offen offset:3072 sc1 lds\n" \
M_"v[72:75], a[140:143], a[92:95], v[36:39]\n" \
"s_mov_b32 m0, s65\n" \
C_"v201, v202, v203\n" \
M_"v[72:75], a[136:139], a[88:91], v[72:75]\n" \
M_"a[112:115], a[120:123], v[192:195], a[112:115]\n" \
"v_mul_f32_e32 v204, v56, v204\n" \
"v_mul_f32_e32 v205, v56, v205\n" \
M_"v[72:75], a[132:135], a[84:87], v[72:75]\n" \
"v_mul_f32_e32 v206, v56, v206\n" \
"v_mul_f32_e32 v207, v56, v207\n" \
M_"v[72:75], a[128:131], a[80:83], v[72:75]\n" \
B_"v53, s[8:11], s64 offen offset:0 sc1 lds\n" \
M_"v[80:83], a[128:131], a[192:195], v[40:43]\n" \
"v_mul_f32_e32 v208, v56, v208\n" \
"v_mul_f32_e32 v209, v56, v209\n" \
M_"v[80:83], a[132:135], a[196:199], v[80:83]\n" \
"v_max_f32_e32 v62, v62, v57\n" \
C_"v202, v204, v205\n" \
M_"v[80:83], a[136:139], a[200:203], v[80:83]\n" \
B_"v53, s[8:11], s64 offen offset:1024 sc1 lds\n" \
M_"v[80:83], a[140:143], a[204:207], v[80:83]\n" \
C_"v203, v206, v207\n" \
C_"v208, v208, v209\n" \
"buffer_store_dwordx4 v[200:203], v20, s[56:59], s70 offen offset:1024 sc1\n" \
M_"v[84:87], a[156:159], a[204:207], v[40:43]\n" \
"v_mul_f32_e32 v210, v56, v210\n" \
"v_mul_f32_e32 v211, v56, v211\n" \
M_"v[84:87], a[152:155], a[200:203], v[84:87]\n" \
"v_rcp_f32_e32 v57, v57\n" \
M_"v[84:87], a[148:151], a[196:199], v[84:87]\n" \
B_"v53, s[8:11], s64 offen offset:2048 sc1 lds\n" \
M_"v[84:87], a[144:147], a[192:195], v[84:87]\n" \
M_"a[112:115], a[120:123], v[200:203], a[112:115]\n" \
"v_mfma_f32_32x32x16_bf16 v[192:207], a[120:123], a[120:123], 0\n" \
C_"v209, v210, v211\n" \
"v_mul_f32_e32 v212, v56, v212\n" \
".fill (L_ab + 20480 - .) / 4, 4, 0xBF800000\n" \
"L_ap5:\n" \
"s_cbranch_execz L_ap0\n" \
"v_mul_f32_e32 v213, v56, v213\n" \
"v_mul_f32_e32 v214, v56, v214\n" \
"v_mul_f32_e32 v215, v56, v215\n" \
"v_mul_f32_e32 v216, v56, v216\n" \
M_"v[92:95], a[144:147], a[208:211], v[44:47]\n" \
R_"a[144:147], v48 offset:12288\n" \
"v_mul_f32_e32 v217, v56, v217\n" \
M_"v[92:95], a[148:151], a[212:215], v[92:95]\n" \
R_"a[148:151], v48 offset:13312\n" \
C_"v210, v212, v213\n" \
M_"v[92:95], a[152:155], a[216:219], v[92:95]\n" \
R_"a[152:155], v48 offset:14336\n" \
C_"v211, v214, v215\n" \
"buffer_store_dwordx4 v[208:211], v20, s[56:59], s70 offen offset:2048 sc1\n" \
M_"v[92:95], a[156:159], a[220:223], v[92:95]\n" \
R_"a[156:159], v48 offset:15360\n" \
C_"v216, v216, v217\n" \
M_"v[88:91], a[140:143], a[220:223], v[44:47]\n" \
R_"a[140:143], v48 offset:11264\n" \
"v_mul_f32_e32 v218, v56, v218\n" \
M_"v[88:91], a[136:139], a[216:219], v[88:91]\n" \
R_"a[136:139], v48 offset:10240\n" \
"v_mul_f32_e32 v219, v56, v219\n" \
M_"v[88:91], a[132:135], a[212:215], v[88:91]\n" \
R_"a[132:135], v48 offset:9216\n" \
"v_mul_f32_e32 v57, v57, v61\n" \
M_"v[88:91], a[128:131], a[208:211], v[88:91]\n" \
R_"a[128:131], v48 offset:8192\n" \
M_"a[112:115], a[120:123], v[208:211], a[112:115]\n" \
C_"v217, v218, v219\n" \
"v_mul_f32_e32 v220, v56, v220\n" \
"v_mul_f32_e32 v221, v56, v221\n" \
"v_mul_f32_e32 v222, v56, v222\n" \
"v_mul_f32_e32 v223, v56, v223\n" \
"v_mul_f32_e32 v224, v57, v224\n" \
"v_mul_f32_e32 v225, v57, v225\n" \
C_"v218, v220, v221\n" \
C_"v219, v222, v223\n" \
"buffer_store_dwordx4 v[216:219], v20, s[56:59], s70 offen offset:3072 sc1\n" \
C_"v224, v224, v225\n" \
"v_mul_f32_e32 v226, v57, v226\n" \
"v_mul_f32_e32 v227, v57, v227\n" \
M_"a[112:115], a[120:123], v[216:219], a[112:115]\n" \
"v_mfma_f32_32x32x16_bf16 v[208:223], a[120:123], a[120:123], 0\n" \
C_"v225, v226, v227\n" \
"v_mul_f32_e32 v228, v57, v228\n" \
"v_mul_f32_e32 v229, v57, v229\n" \
"v_mul_f32_e32 v230, v57, v230\n" \
"v_mul_f32_e32 v231, v57, v231\n" \
"v_mul_f32_e32 v232, v57, v232\n" \
"v_mul_f32_e32 v233, v57, v233\n" \
C_"v226, v228, v229\n" \
C_"v227, v230, v231\n" \
"buffer_store_dwordx4 v[224:227], v20, s[56:59], s71 offen offset:0 sc1\n" \
C_"v232, v232, v233\n" \
"v_mul_f32_e32 v234, v57, v234\n" \
"v_mul_f32_e32 v235, v57, v235\n" \
M_"a[112:115], a[120:123], v[224:227], a[112:115]\n" \
C_"v233, v234, v235\n" \
"v_mul_f32_e32 v236, v57, v236\n" \
"v_mul_f32_e32 v237, v57, v237\n" \
"v_mul_f32_e32 v238, v57, v238\n" \
"v_mul_f32_e32 v239, v57, v239\n" \
"v_mul_f32_e32 v240, v57, v240\n" \
"v_mul_f32_e32 v241, v57, v241\n" \
C_"v234, v236, v237\n" \
C_"v235, v238, v239\n" \
"buffer_store_dwordx4 v[232:235], v20, s[56:59], s71 offen offset:1024 sc1\n" \
C_"v240, v240, v241\n" \
"v_mul_f32_e32 v242, v57, v242\n" \
"v_mul_f32_e32 v243, v57, v243\n" \
M_"a[112:115], a[120:123], v[232:235], a[112:115]\n" \
"v_mfma_f32_32x32x16_bf16 v[224:239], a[120:123], a[120:123], 0\n" \
C_"v241, v242, v243\n" \
"v_mul_f32_e32 v244, v57, v244\n" \
"v_mul_f32_e32 v245, v57, v245\n" \
"v_mul_f32_e32 v246, v57, v246\n" \
"v_mul_f32_e32 v247, v57, v247\n" \
"v_mul_f32_e32 v248, v57, v248\n" \
"v_mul_f32_e32 v249, v57, v249\n" \
C_"v242, v244, v245\n" \
C_"v243, v246, v247\n" \
"buffer_store_dwordx4 v[240:243], v20, s[56:59], s71 offen offset:2048 sc1\n" \
C_"v248, v248, v249\n" \
"v_mul_f32_e32 v250, v57, v250\n" \
"v_mul_f32_e32 v251, v57, v251\n" \
M_"a[112:115], a[120:123], v[240:243], a[112:115]\n" \
C_"v249, v250, v251\n" \
"v_mul_f32_e32 v252, v57, v252\n" \
"v_mul_f32_e32 v253, v57, v253\n" \
"v_mul_f32_e32 v254, v57, v254\n" \
"v_mul_f32_e32 v255, v57, v255\n" \
C_"v250, v252, v253\n" \
C_"v251, v254, v255\n" \
"buffer_store_dwordx4 v[248:251], v20, s[56:59], s71 offen offset:3072 sc1\n" \
B_"v53, s[8:11], s64 offen offset:3072 sc1 lds\n" \
"s_waitcnt vmcnt(16) lgkmcnt(0)\n" \
"s_barrier\n" \
M_"a[112:115], a[120:123], v[248:251], a[112:115]\n" \
"v_mfma_f32_32x32x16_bf16 v[240:255], a[120:123], a[120:123], 0\n" \
E_"v70, v70\n" \
E_"v71, v71\n" \
E_"v72, v72\n" \
E_"v73, v73\n" \
"s_add_u32 s64, s64, 0x10000\n" \
"v_cmp_lt_f32_e64 s[72:73], s21, v62\n" \
"v_readlane_b32 s95, v11, 3\n" \
"v_accvgpr_read_b32 v63, a112\n" \
"v_cmp_u_f32_e32 vcc, v63, v63\n" \
E_"v68, v68\n" \
E_"v69, v69\n" \
E_"v74, v74\n" \
E_"v75, v75\n" \
"s_or_b64 vcc, s[72:73], vcc\n" \
"s_cselect_b32 s95, 0x40000000, s95\n" \
"s_cselect_b32 s94, 0x7f800000, 0\n" \
C_"v71, v70, v71\n" \
E_"v66, v66\n" \
E_"v67, v67\n" \
E_"v64, v64\n" \
E_"v65, v65\n" \
C_"v72, v72, v73\n" \
E_"v76, v76\n" \
E_"v77, v77\n" \
E_"v78, v78\n" \
E_"v79, v79\n" \
C_"v70, v68, v69\n" \
C_"v73, v74, v75\n" \
"v_add_u32_e32 v49, 0x10000, v48\n" \
"s_xor_b32 s65, s65, 0x8000\n" \
C_"v69, v66, v67\n" \
C_"v68, v64, v65\n" \
C_"v74, v76, v77\n" \
C_"v75, v78, v79\n" \
"v_xor_b32_e32 v48, 0x8000, v48\n" \
"s_sub_u32 s66, s19, 2\n" \
"s_branch L_2_%=\n" \
"L_f1_%=:\n" \
R_"a[160:163], v49 offset:0\n" \
R_"a[164:167], v49 offset:1024\n" \
R_"a[168:171], v49 offset:2048\n" \
M_"v[104:107], a[128:131], a[80:83], v[36:39]\n" \
C_"v192, v192, v193\n" \
C_"v224, v224, v225\n" \
C_"v193, v194, v195\n" \
C_"v225, v226, v227\n" \
R_"a[172:175], v49 offset:3072\n" \
C_"v194, v196, v197\n" \
C_"v195, v198, v199\n" \
C_"v226, v228, v229\n" \
C_"v227, v230, v231\n" \
M_"v[104:107], a[132:135], a[84:87], v[104:107]\n" \
"v_lshl_add_u32 v50, s15, 2, v20\n" \
C_"v196, v200, v201\n" \
C_"v197, v202, v203\n" \
C_"v198, v204, v205\n" \
C_"v199, v206, v207\n" \
C_"v228, v232, v233\n" \
C_"v229, v234, v235\n" \
C_"v230, v236, v237\n" \
C_"v231, v238, v239\n" \
"v_add_u32_e32 v50, 0x1a000, v50\n" \
"v_mov_b32_e32 v61, s62\n" \
C_"v200, v208, v209\n" \
C_"v201, v210, v211\n" \
C_"v202, v212, v213\n" \
C_"v203, v214, v215\n" \
C_"v204, v216, v217\n" \
C_"v205, v218, v219\n" \
C_"v206, v220, v221\n" \
C_"v207, v222, v223\n" \
C_"v232, v240, v241\n" \
C_"v233, v242, v243\n" \
C_"v234, v244, v245\n" \
C_"v235, v246, v247\n" \
C_"v236, v248, v249\n" \
C_"v237, v250, v251\n" \
C_"v238, v252, v253\n" \
C_"v239, v254, v255\n" \
"ds_write_b128 v50, v[192:195] offset:0\n" \
M_"v[104:107], a[136:139], a[88:91], v[104:107]\n" \
R_"a[176:179], v49 offset:4096\n" \
"v_add_u32_e32 v56, 0x49000, v20\n" \
M_"v[104:107], a[140:143], a[92:95], v[104:107]\n" \
"ds_write_b128 v50, v[196:199] offset:1024\n" \
M_"v[108:111], a[156:159], a[92:95], v[36:39]\n" \
R_"a[180:183], v49 offset:5120\n" \
R_"a[184:187], v49 offset:6144\n" \
M_"v[108:111], a[152:155], a[88:91], v[108:111]\n" \
"ds_write_b128 v50, v[200:203] offset:2048\n" \
M_"v[108:111], a[148:151], a[84:87], v[108:111]\n" \
"v_and_b32_e32 v68, v12, v68\n" \
"v_and_b32_e32 v69, v13, v69\n" \
M_"v[108:111], a[144:147], a[80:83], v[108:111]\n" \
"ds_write_b128 v50, v[204:207] offset:3072\n" \
"v_and_b32_e32 v70, v14, v70\n" \
"v_and_b32_e32 v71, v15, v71\n" \
R_"a[188:191], v49 offset:7168\n" \
R_"a[0:3], v20 offset:8192\n" \
"s_waitcnt lgkmcnt(9)\n" \
M_"v[128:131], a[160:163], v[68:71], v[128:131]\n" \
"ds_write_b128 v50, v[224:227] offset:4096\n" \
M_"v[160:163], a[160:163], v[72:75], v[160:163]\n" \
R_"a[4:7], v20 offset:9216\n" \
R_"a[8:11], v20 offset:10240\n" \
M_"v[132:135], a[164:167], v[68:71], v[132:135]\n" \
"ds_write_b128 v50, v[228:231] offset:5120\n" \
M_"v[164:167], a[164:167], v[72:75], v[164:167]\n" \
R_"a[12:15], v20 offset:11264\n" \
R_"a[28:31], v20 offset:15360\n" \
M_"v[136:139], a[168:171], v[68:71], v[136:139]\n" \
"ds_write_b128 v50, v[232:235] offset:6144\n" \
M_"v[168:171], a[168:171], v[72:75], v[168:171]\n" \
R_"a[24:27], v20 offset:14336\n" \
R_"a[20:23], v20 offset:13312\n" \
M_"v[140:143], a[172:175], v[68:71], v[140:143]\n" \
"ds_write_b128 v50, v[236:239] offset:7168\n" \
"s_waitcnt vmcnt(0)\n" \
"s_mov_b64 exec, 1\n" \
"buffer_store_dword v61, off, s[56:59], s95 offset:0 sc1\n" \
"s_mov_b64 exec, -1\n" \
M_"v[172:175], a[172:175], v[72:75], v[172:175]\n" \
R_"a[16:19], v20 offset:12288\n" \
"s_waitcnt lgkmcnt(15)\n" \
M_"v[144:147], a[176:179], v[68:71], v[144:147]\n" \
"v_xor_b32_e32 v48, 0x4000, v49\n" \
"ds_read_b32 v58, v18 offset:256\n" \
M_"v[176:179], a[176:179], v[72:75], v[176:179]\n" \
"ds_read_b32 v59, v18 offset:384\n" \
R_"a[32:35], v48 offset:8192\n" \
B_"a[192:195], v20, s[56:59], s60 offen offset:0 sc1\n" \
M_"v[148:151], a[180:183], v[68:71], v[148:151]\n" \
R_"a[36:39], v48 offset:9216\n" \
R_"a[40:43], v48 offset:10240\n" \
M_"v[180:183], a[180:183], v[72:75], v[180:183]\n" \
R_"a[44:47], v48 offset:11264\n" \
R_"a[48:51], v48 offset:12288\n" \
M_"v[152:155], a[184:187], v[68:71], v[152:155]\n" \
R_"a[52:55], v48 offset:13312\n" \
R_"a[56:59], v48 offset:14336\n" \
M_"v[184:187], a[184:187], v[72:75], v[184:187]\n" \
R_"a[60:63], v48 offset:15360\n" \
B_"a[208:211], v56, s[56:59], s60 offen offset:0 sc1\n" \
"s_waitcnt lgkmcnt(15)\n" \
M_"v[156:159], a[188:191], v[68:71], v[156:159]\n" \
R_"a[128:131], v49 offset:8192\n" \
R_"a[132:135], v49 offset:9216\n" \
M_"v[188:191], a[188:191], v[72:75], v[188:191]\n" \
R_"a[136:139], v49 offset:10240\n" \
R_"a[140:143], v49 offset:11264\n" \
"v_smfmac_f32_16x16x64_bf16 a[96:99], a[104:107], v[68:75], v21\n" \
R_"a[144:147], v49 offset:12288\n" \
R_"a[148:151], v49 offset:13312\n" \
B_"a[196:199], v20, s[56:59], s60 offen offset:1024 sc1\n" \
M_"v[112:115], a[0:3], a[64:67], v[32:35]\n" \
R_"a[152:155], v49 offset:14336\n" \
R_"a[156:159], v49 offset:15360\n" \
M_"v[112:115], a[4:7], a[68:71], v[112:115]\n" \
"v_accvgpr_read_b32 v40, a100\n" \
"v_accvgpr_read_b32 v24, a101\n" \
M_"v[112:115], a[8:11], a[72:75], v[112:115]\n" \
"v_accvgpr_read_b32 v41, a102\n" \
"v_accvgpr_read_b32 v25, a103\n" \
M_"v[112:115], a[12:15], a[76:79], v[112:115]\n" \
B_"a[212:215], v56, s[56:59], s60 offen offset:1024 sc1\n" \
M_"v[116:119], a[28:31], a[76:79], v[32:35]\n" \
"v_add_f32_e32 v40, v40, v24\n" \
"s_waitcnt lgkmcnt(15)\n" \
M_"v[116:119], a[24:27], a[72:75], v[116:119]\n" \
"v_add_f32_e32 v41, v41, v25\n" \
"v_add_f32_e32 v40, s94, v40\n" \
M_"v[116:119], a[20:23], a[68:71], v[116:119]\n" \
"v_add_f32_e32 v41, s94, v41\n" \
"ds_write_b64 v19, v[40:41]\n" \
M_"v[116:119], a[16:19], a[64:67], v[116:119]\n" \
B_"a[200:203], v20, s[56:59], s60 offen offset:2048 sc1\n" \
M_"v[124:127], a[16:19], a[80:83], v[36:39]\n" \
"v_add_u32_e32 v52, s15, v20\n" \
"v_add_u32_e32 v51, 0x10000, v52\n" \
M_"v[124:127], a[20:23], a[84:87], v[124:127]\n" \
E_"v104, v104\n" \
M_"v[124:127], a[24:27], a[88:91], v[124:127]\n" \
E_"v105, v105\n" \
M_"v[124:127], a[28:31], a[92:95], v[124:127]\n" \
B_"a[216:219], v56, s[56:59], s60 offen offset:2048 sc1\n" \
M_"v[120:123], a[12:15], a[92:95], v[36:39]\n" \
E_"v106, v106\n" \
M_"v[120:123], a[8:11], a[88:91], v[120:123]\n" \
E_"v107, v107\n" \
M_"v[120:123], a[4:7], a[84:87], v[120:123]\n" \
C_"v104, v104, v105\n" \
C_"v105, v106, v107\n" \
M_"v[120:123], a[0:3], a[80:83], v[120:123]\n" \
B_"a[204:207], v20, s[56:59], s60 offen offset:3072 sc1\n" \
E_"v108, v108\n" \
E_"v109, v109\n" \
"s_waitcnt lgkmcnt(0)\n" \
"s_barrier\n" \
R_"v[208:211], v52 offset:0\n" \
R_"v[212:215], v52 offset:1024\n" \
R_"v[216:219], v52 offset:2048\n" \
R_"v[220:223], v52 offset:3072\n" \
B_"a[220:223], v56, s[56:59], s60 offen offset:3072 sc1\n" \
R_"v[240:243], v51 offset:32768\n" \
R_"v[244:247], v51 offset:33792\n" \
R_"v[248:251], v51 offset:34816\n" \
R_"v[252:255], v51 offset:35840\n" \
E_"v110, v110\n" \
E_"v111, v111\n" \
C_"v106, v108, v109\n" \
C_"v107, v110, v111\n" \
"v_and_b32_e32 v104, v12, v104\n" \
"v_and_b32_e32 v105, v13, v105\n" \
"v_and_b32_e32 v106, v14, v106\n" \
"v_and_b32_e32 v107, v15, v107\n" \
"v_xor_b32_e32 v63, 0x400, v19\n" \
"ds_read_b64 v[42:43], v63\n" \
M_"a[96:99], a[116:119], v[104:107], a[96:99]\n" \
"v_mov_b32_e32 v194, v28\n" \
"v_mov_b32_e32 v195, v29\n" \
M_"v[160:163], a[128:131], v[104:107], v[160:163]\n" \
"v_mov_b32_e32 v196, 0\n" \
"v_mov_b32_e32 v197, 0\n" \
M_"v[164:167], a[132:135], v[104:107], v[164:167]\n" \
"v_mov_b32_e32 v192, 0\n" \
"v_mov_b32_e32 v193, 0\n" \
M_"v[168:171], a[136:139], v[104:107], v[168:171]\n" \
"v_readlane_b32 s93, v11, 25\n" \
"v_readlane_b32 s92, v11, 30\n" \
M_"v[172:175], a[140:143], v[104:107], v[172:175]\n" \
E_"v118, v118\n" \
M_"v[176:179], a[144:147], v[104:107], v[176:179]\n" \
E_"v119, v119\n" \
M_"v[180:183], a[148:151], v[104:107], v[180:183]\n" \
"s_add_u32 m0, s93, 0x0\n" \
C_"v119, v118, v119\n" \
M_"v[184:187], a[152:155], v[104:107], v[184:187]\n" \
E_"v120, v120\n" \
M_"v[188:191], a[156:159], v[104:107], v[188:191]\n" \
E_"v121, v121\n" \
"s_waitcnt lgkmcnt(1)\n" \
"s_barrier\n" \
M_"v[128:131], v[194:197], v[208:211], v[128:131]\n" \
B_"v0, s[80:83], s92 offen offset:0 lds\n" \
M_"v[132:135], v[192:195], v[208:211], v[132:135]\n" \
E_"v116, v116\n" \
M_"v[136:139], v[194:197], v[212:215], v[136:139]\n" \
E_"v117, v117\n" \
M_"v[140:143], v[192:195], v[212:215], v[140:143]\n" \
E_"v122, v122\n" \
M_"v[144:147], v[194:197], v[216:219], v[144:147]\n" \
B_"v1, s[80:83], s92 offen offset:1024 lds\n" \
M_"v[148:151], v[192:195], v[216:219], v[148:151]\n" \
E_"v123, v123\n" \
M_"v[152:155], v[194:197], v[220:223], v[152:155]\n" \
E_"v114, v114\n" \
M_"v[156:159], v[192:195], v[220:223], v[156:159]\n" \
E_"v115, v115\n" \
M_"v[160:163], v[194:197], v[240:243], v[160:163]\n" \
B_"v2, s[80:83], s92 offen offset:2048 lds\n" \
M_"v[164:167], v[192:195], v[240:243], v[164:167]\n" \
E_"v112, v112\n" \
M_"v[168:171], v[194:197], v[244:247], v[168:171]\n" \
E_"v113, v113\n" \
M_"v[172:175], v[192:195], v[244:247], v[172:175]\n" \
C_"v120, v120, v121\n" \
C_"v118, v116, v117\n" \
M_"v[176:179], v[194:197], v[248:251], v[176:179]\n" \
B_"v3, s[80:83], s92 offen offset:3072 lds\n" \
"s_add_u32 m0, s93, 0x1000\n" \
M_"v[180:183], v[192:195], v[248:251], v[180:183]\n" \
E_"v124, v124\n" \
M_"v[184:187], v[194:197], v[252:255], v[184:187]\n" \
E_"v125, v125\n" \
M_"v[188:191], v[192:195], v[252:255], v[188:191]\n" \
E_"v126, v126\n" \
B_"v4, s[80:83], s92 offen offset:0 lds\n" \
E_"v127, v127\n" \
C_"v121, v122, v123\n" \
C_"v117, v114, v115\n" \
C_"v116, v112, v113\n" \
C_"v122, v124, v125\n" \
C_"v123, v126, v127\n" \
"s_nop 1\n" \
"v_smfmac_f32_16x16x64_bf16 a[96:99], a[104:107], v[116:123], v21\n" \
M_"v[128:131], a[32:35], v[116:119], v[128:131]\n" \
B_"v5, s[80:83], s92 offen offset:1024 lds\n" \
M_"v[160:163], a[32:35], v[120:123], v[160:163]\n" \
M_"v[132:135], a[36:39], v[116:119], v[132:135]\n" \
M_"v[164:167], a[36:39], v[120:123], v[164:167]\n" \
M_"v[136:139], a[40:43], v[116:119], v[136:139]\n" \
B_"v6, s[80:83], s92 offen offset:2048 lds\n" \
M_"v[168:171], a[40:43], v[120:123], v[168:171]\n" \
"v_accvgpr_read_b32 v54, a96\n" \
"v_accvgpr_read_b32 v22, a97\n" \
M_"v[140:143], a[44:47], v[116:119], v[140:143]\n" \
"v_add_f32_e32 v54, v54, v22\n" \
"s_waitcnt lgkmcnt(0)\n" \
"v_add_f32_e32 v54, v54, v42\n" \
M_"v[172:175], a[44:47], v[120:123], v[172:175]\n" \
"v_max_f32_e32 v62, v62, v54\n" \
"v_accvgpr_read_b32 v55, a98\n" \
B_"v7, s[80:83], s92 offen offset:3072 lds\n" \
"s_add_u32 m0, s93, 0x2000\n" \
M_"v[144:147], a[48:51], v[116:119], v[144:147]\n" \
"v_rcp_f32_e32 v54, v54\n" \
M_"v[176:179], a[48:51], v[120:123], v[176:179]\n" \
"v_mul_f32_e32 v54, v54, v58\n" \
"v_mul_f32_e32 v128, v54, v128\n" \
M_"v[148:151], a[52:55], v[116:119], v[148:151]\n" \
"v_mul_f32_e32 v129, v54, v129\n" \
"v_mul_f32_e32 v130, v54, v130\n" \
B_"v8, s[80:83], s92 offen offset:0 lds\n" \
M_"v[180:183], a[52:55], v[120:123], v[180:183]\n" \
"v_mul_f32_e32 v131, v54, v131\n" \
"v_mul_f32_e32 v132, v54, v132\n" \
"v_mul_f32_e32 v133, v54, v133\n" \
"v_mul_f32_e32 v134, v54, v134\n" \
"v_mul_f32_e32 v135, v54, v135\n" \
C_"v64, v128, v129\n" \
C_"v65, v130, v131\n" \
C_"v66, v132, v133\n" \
C_"v67, v134, v135\n" \
"v_accvgpr_read_b32 v23, a99\n" \
M_"v[152:155], a[56:59], v[116:119], v[152:155]\n" \
M_"a[112:115], a[120:123], v[64:67], a[112:115]\n" \
"v_mul_f32_e32 v136, v54, v136\n" \
"v_mul_f32_e32 v137, v54, v137\n" \
"v_mul_f32_e32 v138, v54, v138\n" \
"v_mul_f32_e32 v139, v54, v139\n" \
"v_mul_f32_e32 v140, v54, v140\n" \
"v_mul_f32_e32 v141, v54, v141\n" \
"v_mul_f32_e32 v142, v54, v142\n" \
"v_mul_f32_e32 v143, v54, v143\n" \
"v_add_f32_e32 v55, v55, v23\n" \
C_"v68, v136, v137\n" \
C_"v69, v138, v139\n" \
C_"v70, v140, v141\n" \
C_"v71, v142, v143\n" \
"v_add_f32_e32 v55, v55, v43\n" \
"v_mul_f32_e32 v144, v54, v144\n" \
M_"a[112:115], a[120:123], v[68:71], a[112:115]\n" \
"v_mul_f32_e32 v145, v54, v145\n" \
"v_mul_f32_e32 v146, v54, v146\n" \
"v_mul_f32_e32 v147, v54, v147\n" \
"v_mul_f32_e32 v148, v54, v148\n" \
"v_mul_f32_e32 v149, v54, v149\n" \
"v_mul_f32_e32 v150, v54, v150\n" \
"v_mul_f32_e32 v151, v54, v151\n" \
"v_max_f32_e32 v62, v62, v55\n" \
C_"v72, v144, v145\n" \
C_"v73, v146, v147\n" \
C_"v74, v148, v149\n" \
C_"v75, v150, v151\n" \
"v_rcp_f32_e32 v55, v55\n" \
M_"v[184:187], a[56:59], v[120:123], v[184:187]\n" \
M_"a[112:115], a[120:123], v[72:75], a[112:115]\n" \
"v_mul_f32_e32 v152, v54, v152\n" \
"v_mul_f32_e32 v153, v54, v153\n" \
"v_mul_f32_e32 v154, v54, v154\n" \
"v_mul_f32_e32 v155, v54, v155\n" \
C_"v76, v152, v153\n" \
C_"v77, v154, v155\n" \
"v_mul_f32_e32 v55, v55, v59\n" \
"v_mul_f32_e32 v160, v55, v160\n" \
"v_mul_f32_e32 v161, v55, v161\n" \
"v_mul_f32_e32 v162, v55, v162\n" \
"v_mul_f32_e32 v163, v55, v163\n" \
"v_mul_f32_e32 v164, v55, v164\n" \
"v_mul_f32_e32 v165, v55, v165\n" \
"v_mul_f32_e32 v166, v55, v166\n" \
"v_mul_f32_e32 v167, v55, v167\n" \
C_"v80, v160, v161\n" \
C_"v81, v162, v163\n" \
C_"v82, v164, v165\n" \
C_"v83, v166, v167\n" \
M_"v[156:159], a[60:63], v[116:119], v[156:159]\n" \
"v_mul_f32_e32 v168, v55, v168\n" \
"v_mul_f32_e32 v169, v55, v169\n" \
"v_mul_f32_e32 v170, v55, v170\n" \
"v_mul_f32_e32 v171, v55, v171\n" \
"v_mul_f32_e32 v172, v55, v172\n" \
"v_mul_f32_e32 v173, v55, v173\n" \
"v_mul_f32_e32 v174, v55, v174\n" \
"v_mul_f32_e32 v175, v55, v175\n" \
"v_mul_f32_e32 v156, v54, v156\n" \
"v_mul_f32_e32 v157, v54, v157\n" \
"v_mul_f32_e32 v158, v54, v158\n" \
"v_mul_f32_e32 v159, v54, v159\n" \
C_"v78, v156, v157\n" \
C_"v79, v158, v159\n" \
C_"v84, v168, v169\n" \
C_"v85, v170, v171\n" \
M_"a[112:115], a[120:123], v[76:79], a[112:115]\n" \
M_"a[112:115], a[120:123], v[80:83], a[112:115]\n" \
C_"v86, v172, v173\n" \
C_"v87, v174, v175\n" \
"v_mul_f32_e32 v176, v55, v176\n" \
"v_mul_f32_e32 v177, v55, v177\n" \
M_"a[112:115], a[120:123], v[84:87], a[112:115]\n" \
"v_mul_f32_e32 v178, v55, v178\n" \
"v_mul_f32_e32 v179, v55, v179\n" \
"v_mul_f32_e32 v180, v55, v180\n" \
"v_mul_f32_e32 v181, v55, v181\n" \
"v_mul_f32_e32 v182, v55, v182\n" \
"v_mul_f32_e32 v183, v55, v183\n" \
C_"v96, v176, v177\n" \
C_"v97, v178, v179\n" \
C_"v98, v180, v181\n" \
C_"v99, v182, v183\n" \
M_"v[188:191], a[60:63], v[120:123], v[188:191]\n" \
"v_mul_f32_e32 v184, v55, v184\n" \
M_"a[112:115], a[120:123], v[96:99], a[112:115]\n" \
"v_mul_f32_e32 v185, v55, v185\n" \
"v_mul_f32_e32 v186, v55, v186\n" \
"v_mul_f32_e32 v187, v55, v187\n" \
C_"v100, v184, v185\n" \
C_"v101, v186, v187\n" \
"v_cmp_lt_f32_e64 s[72:73], s21, v62\n" \
"v_mul_f32_e32 v188, v55, v188\n" \
"v_mul_f32_e32 v189, v55, v189\n" \
"v_mul_f32_e32 v190, v55, v190\n" \
"v_mul_f32_e32 v191, v55, v191\n" \
C_"v102, v188, v189\n" \
C_"v103, v190, v191\n" \
"s_nop 1\n" \
M_"a[112:115], a[120:123], v[100:103], a[112:115]\n" \
"s_nop 7\n" \
"v_accvgpr_read_b32 v63, a112\n" \
"v_cmp_u_f32_e32 vcc, v63, v63\n" \
"s_nop 0\n" \
"s_or_b64 vcc, s[72:73], vcc\n" \
"s_cbranch_scc1 L_end_%=\n" \
"s_branch L_out_%=\n" \
".fill (L_ab + 24576 - .) / 4, 4, 0xBF800000\n" \
".fill (L_ab + 24576 - .) / 4, 4, 0xBF800000\n" \
"L_ap6:\n" \
"s_setpc_b64 s[88:89]\n" \
"L_gs7:\n" \
M_"v[104:107], a[128:131], a[16:19], v[36:39]\n" \
"s_add_u32 s68, s60, 0xfff6e000\n" \
"s_add_u32 s69, s60, 0xfffb7000\n" \
"s_add_u32 s70, s60, 0x0\n" \
"s_add_u32 s71, s60, 0x49000\n" \
M_"v[104:107], a[132:135], a[20:23], v[104:107]\n" \
"s_xor_b32 m0, s65, 0x12000\n" \
R_"a[160:163], v49 offset:0\n" \
M_"v[104:107], a[136:139], a[24:27], v[104:107]\n" \
B_"v50, s[8:11], s64 offen offset:0 sc1 lds\n" \
M_"v[104:107], a[140:143], a[28:31], v[104:107]\n" \
R_"a[164:167], v49 offset:1024\n" \
R_"a[168:171], v49 offset:2048\n" \
M_"v[108:111], a[156:159], a[28:31], v[36:39]\n" \
R_"a[172:175], v49 offset:3072\n" \
R_"a[176:179], v49 offset:4096\n" \
M_"v[108:111], a[152:155], a[24:27], v[108:111]\n" \
B_"v50, s[8:11], s64 offen offset:1024 sc1 lds\n" \
M_"v[108:111], a[148:151], a[20:23], v[108:111]\n" \
"s_mov_b32 s65, s13\n" \
"v_add_u32_e32 v63, s15, v20\n" \
M_"v[108:111], a[144:147], a[16:19], v[108:111]\n" \
"s_add_u32 s67, s14, 40960\n" \
"v_add_u32_e32 v50, 0x6000, v63\n" \
M_"v[116:119], a[144:147], a[32:35], v[40:43]\n" \
"s_xor_b32 m0, s65, 0x8000\n" \
R_"a[180:183], v49 offset:5120\n" \
M_"v[116:119], a[148:151], a[36:39], v[116:119]\n" \
R_"a[184:187], v49 offset:6144\n" \
"v_and_b32_e32 v68, v12, v68\n" \
M_"v[116:119], a[152:155], a[40:43], v[116:119]\n" \
"v_and_b32_e32 v69, v13, v69\n" \
"v_and_b32_e32 v70, v14, v70\n" \
M_"v[116:119], a[156:159], a[44:47], v[116:119]\n" \
B_"v50, s[8:11], s67 offen offset:0 sc1 lds\n" \
M_"v[112:115], a[140:143], a[44:47], v[40:43]\n" \
"v_and_b32_e32 v71, v15, v71\n" \
R_"a[188:191], v49 offset:7168\n" \
M_"v[112:115], a[136:139], a[40:43], v[112:115]\n" \
E_"v86, v86\n" \
M_"v[112:115], a[132:135], a[36:39], v[112:115]\n" \
E_"v87, v87\n" \
M_"v[112:115], a[128:131], a[32:35], v[112:115]\n" \
E_"v84, v84\n" \
M_"v[120:123], a[128:131], a[48:51], v[44:47]\n" \
B_"v50, s[8:11], s67 offen offset:1024 sc1 lds\n" \
M_"v[120:123], a[132:135], a[52:55], v[120:123]\n" \
E_"v85, v85\n" \
M_"v[120:123], a[136:139], a[56:59], v[120:123]\n" \
C_"v87, v86, v87\n" \
C_"v86, v84, v85\n" \
M_"v[120:123], a[140:143], a[60:63], v[120:123]\n" \
E_"v82, v82\n" \
M_"v[124:127], a[156:159], a[60:63], v[44:47]\n" \
E_"v83, v83\n" \
M_"v[124:127], a[152:155], a[56:59], v[124:127]\n" \
E_"v80, v80\n" \
M_"v[124:127], a[148:151], a[52:55], v[124:127]\n" \
B_"v50, s[8:11], s67 offen offset:2048 sc1 lds\n" \
M_"v[124:127], a[144:147], a[48:51], v[124:127]\n" \
C_"v85, v82, v83\n" \
"s_waitcnt lgkmcnt(1)\n" \
M_"v[128:131], a[160:163], v[68:71], v[128:131]\n" \
E_"v81, v81\n" \
M_"v[160:163], a[160:163], v[72:75], v[160:163]\n" \
C_"v84, v80, v81\n" \
"s_mov_b32 s64, s14\n" \
M_"v[132:135], a[164:167], v[68:71], v[132:135]\n" \
E_"v88, v88\n" \
M_"v[164:167], a[164:167], v[72:75], v[164:167]\n" \
E_"v89, v89\n" \
M_"v[136:139], a[168:171], v[68:71], v[136:139]\n" \
B_"v50, s[8:11], s67 offen offset:3072 sc1 lds\n" \
M_"v[168:171], a[168:171], v[72:75], v[168:171]\n" \
E_"v90, v90\n" \
M_"v[140:143], a[172:175], v[68:71], v[140:143]\n" \
E_"v91, v91\n" \
M_"v[172:175], a[172:175], v[72:75], v[172:175]\n" \
C_"v88, v88, v89\n" \
C_"v89, v90, v91\n" \
M_"v[144:147], a[176:179], v[68:71], v[144:147]\n" \
E_"v92, v92\n" \
M_"v[176:179], a[176:179], v[72:75], v[176:179]\n" \
E_"v93, v93\n" \
M_"v[148:151], a[180:183], v[68:71], v[148:151]\n" \
E_"v94, v94\n" \
M_"v[180:183], a[180:183], v[72:75], v[180:183]\n" \
E_"v95, v95\n" \
M_"v[152:155], a[184:187], v[68:71], v[152:155]\n" \
C_"v90, v92, v93\n" \
C_"v91, v94, v95\n" \
M_"v[184:187], a[184:187], v[72:75], v[184:187]\n" \
"v_add_u32_e32 v51, 0x12000, v63\n" \
"s_waitcnt lgkmcnt(0)\n" \
M_"v[156:159], a[188:191], v[68:71], v[156:159]\n" \
"s_xor_b32 m0, s65, 0xa000\n" \
"ds_read_b32 v58, v18 offset:512\n" \
M_"v[188:191], a[188:191], v[72:75], v[188:191]\n" \
B_"v51, s[8:11], s64 offen offset:0 sc1 lds\n" \
"v_smfmac_f32_16x16x64_bf16 a[96:99], a[104:107], v[68:75], v21\n" \
"ds_read_b32 v59, v18 offset:640\n" \
"ds_read_b32 v60, v18 offset:768\n" \
M_"v[220:223], a[188:191], v[84:87], v[220:223]\n" \
B_"v51, s[8:11], s64 offen offset:1024 sc1 lds\n" \
M_"v[252:255], a[188:191], v[88:91], v[252:255]\n" \
"ds_read_b32 v61, v18 offset:896\n" \
R_"a[128:131], v48 offset:0\n" \
M_"v[216:219], a[184:187], v[84:87], v[216:219]\n" \
R_"a[144:147], v48 offset:4096\n" \
R_"a[132:135], v48 offset:1024\n" \
M_"v[248:251], a[184:187], v[88:91], v[248:251]\n" \
R_"a[148:151], v48 offset:5120\n" \
R_"a[136:139], v48 offset:2048\n" \
M_"v[212:215], a[180:183], v[84:87], v[212:215]\n" \
R_"a[152:155], v48 offset:6144\n" \
R_"a[140:143], v48 offset:3072\n" \
M_"v[244:247], a[180:183], v[88:91], v[244:247]\n" \
R_"a[156:159], v48 offset:7168\n" \
"v_add_u32_e32 v52, 0x14000, v63\n" \
M_"v[208:211], a[176:179], v[84:87], v[208:211]\n" \
B_"v51, s[8:11], s64 offen offset:2048 sc1 lds\n" \
M_"v[240:243], a[176:179], v[88:91], v[240:243]\n" \
E_"v104, v104\n" \
M_"v[204:207], a[172:175], v[84:87], v[204:207]\n" \
E_"v105, v105\n" \
M_"v[236:239], a[172:175], v[88:91], v[236:239]\n" \
B_"v51, s[8:11], s64 offen offset:3072 sc1 lds\n" \
M_"v[200:203], a[168:171], v[84:87], v[200:203]\n" \
E_"v106, v106\n" \
M_"v[232:235], a[168:171], v[88:91], v[232:235]\n" \
E_"v107, v107\n" \
M_"v[196:199], a[164:167], v[84:87], v[196:199]\n" \
C_"v104, v104, v105\n" \
C_"v105, v106, v107\n" \
M_"v[228:231], a[164:167], v[88:91], v[228:231]\n" \
E_"v108, v108\n" \
M_"v[192:195], a[160:163], v[84:87], v[192:195]\n" \
E_"v109, v109\n" \
M_"v[224:227], a[160:163], v[88:91], v[224:227]\n" \
E_"v110, v110\n" \
"v_smfmac_f32_16x16x64_bf16 a[100:103], a[104:107], v[84:91], v21\n" \
E_"v111, v111\n" \
C_"v106, v108, v109\n" \
C_"v107, v110, v111\n" \
"v_and_b32_e32 v104, v12, v104\n" \
"v_and_b32_e32 v105, v13, v105\n" \
"v_and_b32_e32 v106, v14, v106\n" \
"v_and_b32_e32 v107, v15, v107\n" \
"v_accvgpr_read_b32 v54, a96\n" \
"v_accvgpr_read_b32 v22, a97\n" \
"v_readlane_b32 s93, v11, 22\n" \
"v_readlane_b32 s80, v11, 11\n" \
"v_readlane_b32 s81, v11, 12\n" \
"s_mov_b32 s82, 0x47fca0\n" \
"s_mov_b32 s83, 0x20000\n" \
"v_readlane_b32 s92, v11, 29\n" \
E_"v118, v118\n" \
E_"v119, v119\n" \
E_"v116, v116\n" \
E_"v117, v117\n" \
C_"v119, v118, v119\n" \
E_"v114, v114\n" \
E_"v115, v115\n" \
E_"v112, v112\n" \
C_"v118, v116, v117\n" \
"s_waitcnt vmcnt(10) lgkmcnt(0)\n" \
"s_barrier\n" \
M_"v[80:83], a[128:131], a[32:35], v[40:43]\n" \
R_"a[160:163], v49 offset:8192\n" \
R_"a[164:167], v49 offset:9216\n" \
M_"v[80:83], a[132:135], a[36:39], v[80:83]\n" \
R_"a[168:171], v49 offset:10240\n" \
"s_xor_b32 m0, s65, 0x18000\n" \
M_"v[80:83], a[136:139], a[40:43], v[80:83]\n" \
B_"v52, s[8:11], s64 offen offset:0 sc1 lds\n" \
M_"v[80:83], a[140:143], a[44:47], v[80:83]\n" \
R_"a[172:175], v49 offset:11264\n" \
R_"a[176:179], v49 offset:12288\n" \
M_"v[84:87], a[156:159], a[44:47], v[40:43]\n" \
R_"a[180:183], v49 offset:13312\n" \
R_"a[184:187], v49 offset:14336\n" \
M_"v[84:87], a[152:155], a[40:43], v[84:87]\n" \
B_"v52, s[8:11], s64 offen offset:1024 sc1 lds\n" \
M_"v[84:87], a[148:151], a[36:39], v[84:87]\n" \
R_"a[188:191], v49 offset:15360\n" \
C_"v117, v114, v115\n" \
M_"v[84:87], a[144:147], a[32:35], v[84:87]\n" \
B_"v52, s[8:11], s64 offen offset:2048 sc1 lds\n" \
M_"v[92:95], a[144:147], a[48:51], v[44:47]\n" \
E_"v113, v113\n" \
M_"v[92:95], a[148:151], a[52:55], v[92:95]\n" \
B_"v52, s[8:11], s64 offen offset:3072 sc1 lds\n" \
M_"v[92:95], a[152:155], a[56:59], v[92:95]\n" \
"s_add_u32 m0, s93, 0x0\n" \
C_"v116, v112, v113\n" \
M_"v[92:95], a[156:159], a[60:63], v[92:95]\n" \
E_"v120, v120\n" \
M_"v[88:91], a[140:143], a[60:63], v[44:47]\n" \
B_"v0, s[80:83], s92 offen offset:0 lds\n" \
M_"v[88:91], a[136:139], a[56:59], v[88:91]\n" \
E_"v121, v121\n" \
M_"v[88:91], a[132:135], a[52:55], v[88:91]\n" \
E_"v122, v122\n" \
M_"v[88:91], a[128:131], a[48:51], v[88:91]\n" \
B_"v1, s[80:83], s92 offen offset:1024 lds\n" \
M_"a[96:99], a[116:119], v[104:107], a[96:99]\n" \
C_"v120, v120, v121\n" \
"s_waitcnt lgkmcnt(5)\n" \
M_"v[160:163], a[160:163], v[104:107], v[160:163]\n" \
E_"v123, v123\n" \
M_"v[164:167], a[164:167], v[104:107], v[164:167]\n" \
B_"v2, s[80:83], s92 offen offset:2048 lds\n" \
M_"v[168:171], a[168:171], v[104:107], v[168:171]\n" \
C_"v121, v122, v123\n" \
"s_waitcnt lgkmcnt(1)\n" \
M_"v[172:175], a[172:175], v[104:107], v[172:175]\n" \
E_"v124, v124\n" \
M_"v[176:179], a[176:179], v[104:107], v[176:179]\n" \
E_"v125, v125\n" \
M_"v[180:183], a[180:183], v[104:107], v[180:183]\n" \
E_"v126, v126\n" \
M_"v[184:187], a[184:187], v[104:107], v[184:187]\n" \
C_"v122, v124, v125\n" \
"s_waitcnt lgkmcnt(0)\n" \
M_"v[188:191], a[188:191], v[104:107], v[188:191]\n" \
E_"v127, v127\n" \
M_"v[220:223], a[188:191], v[116:119], v[220:223]\n" \
C_"v123, v126, v127\n" \
B_"v3, s[80:83], s92 offen offset:3072 lds\n" \
"s_add_u32 m0, s93, 0x1000\n" \
M_"v[252:255], a[188:191], v[120:123], v[252:255]\n" \
R_"a[128:131], v48 offset:8192\n" \
R_"a[144:147], v48 offset:12288\n" \
M_"v[216:219], a[184:187], v[116:119], v[216:219]\n" \
B_"v4, s[80:83], s92 offen offset:0 lds\n" \
M_"v[248:251], a[184:187], v[120:123], v[248:251]\n" \
R_"a[132:135], v48 offset:9216\n" \
R_"a[148:151], v48 offset:13312\n" \
M_"v[212:215], a[180:183], v[116:119], v[212:215]\n" \
B_"v5, s[80:83], s92 offen offset:1024 lds\n" \
M_"v[244:247], a[180:183], v[120:123], v[244:247]\n" \
R_"a[136:139], v48 offset:10240\n" \
R_"a[152:155], v48 offset:14336\n" \
M_"v[208:211], a[176:179], v[116:119], v[208:211]\n" \
B_"v6, s[80:83], s92 offen offset:2048 lds\n" \
M_"v[240:243], a[176:179], v[120:123], v[240:243]\n" \
R_"a[140:143], v48 offset:11264\n" \
R_"a[156:159], v48 offset:15360\n" \
M_"v[204:207], a[172:175], v[116:119], v[204:207]\n" \
B_"v7, s[80:83], s92 offen offset:3072 lds\n" \
M_"v[236:239], a[172:175], v[120:123], v[236:239]\n" \
"v_readlane_b32 s96, v11, 1\n" \
"v_lshrrev_b32_e32 v16, 1, v20\n" \
M_"v[200:203], a[168:171], v[116:119], v[200:203]\n" \
"v_add_f32_e32 v54, v54, v22\n" \
"v_max_f32_e32 v62, v62, v54\n" \
M_"v[232:235], a[168:171], v[120:123], v[232:235]\n" \
"v_rcp_f32_e32 v54, v54\n" \
M_"v[196:199], a[164:167], v[116:119], v[196:199]\n" \
"v_mul_f32_e32 v54, v54, v58\n" \
"v_mul_f32_e32 v128, v54, v128\n" \
M_"v[228:231], a[164:167], v[120:123], v[228:231]\n" \
"v_mul_f32_e32 v129, v54, v129\n" \
"v_mul_f32_e32 v136, v54, v136\n" \
M_"v[192:195], a[160:163], v[116:119], v[192:195]\n" \
"v_mul_f32_e32 v137, v54, v137\n" \
"v_mul_f32_e32 v144, v54, v144\n" \
M_"v[224:227], a[160:163], v[120:123], v[224:227]\n" \
"v_mul_f32_e32 v145, v54, v145\n" \
"v_mul_f32_e32 v152, v54, v152\n" \
"v_smfmac_f32_16x16x64_bf16 a[100:103], a[104:107], v[116:123], v21\n" \
"v_mul_f32_e32 v153, v54, v153\n" \
C_"v128, v128, v129\n" \
"v_mul_f32_e32 v130, v54, v130\n" \
"v_mul_f32_e32 v131, v54, v131\n" \
C_"v136, v136, v137\n" \
"v_mul_f32_e32 v138, v54, v138\n" \
"v_mul_f32_e32 v139, v54, v139\n" \
C_"v144, v144, v145\n" \
"v_mul_f32_e32 v146, v54, v146\n" \
"v_mul_f32_e32 v147, v54, v147\n" \
C_"v152, v152, v153\n" \
"v_mul_f32_e32 v154, v54, v154\n" \
"v_mul_f32_e32 v155, v54, v155\n" \
C_"v129, v130, v131\n" \
"v_mul_f32_e32 v132, v54, v132\n" \
"v_mul_f32_e32 v133, v54, v133\n" \
"v_mul_f32_e32 v134, v54, v134\n" \
"v_mul_f32_e32 v135, v54, v135\n" \
C_"v137, v138, v139\n" \
"v_mul_f32_e32 v140, v54, v140\n" \
"v_mul_f32_e32 v141, v54, v141\n" \
"v_mul_f32_e32 v142, v54, v142\n" \
"v_mul_f32_e32 v143, v54, v143\n" \
C_"v145, v146, v147\n" \
"v_mul_f32_e32 v148, v54, v148\n" \
"v_mul_f32_e32 v149, v54, v149\n" \
"v_mul_f32_e32 v150, v54, v150\n" \
"v_mul_f32_e32 v151, v54, v151\n" \
C_"v153, v154, v155\n" \
"v_mul_f32_e32 v156, v54, v156\n" \
"v_mul_f32_e32 v157, v54, v157\n" \
"v_mul_f32_e32 v158, v54, v158\n" \
"v_mul_f32_e32 v159, v54, v159\n" \
C_"v130, v132, v133\n" \
C_"v131, v134, v135\n" \
"buffer_store_dwordx4 v[128:131], v20, s[56:59], s68 offen offset:0 sc1\n" \
C_"v138, v140, v141\n" \
C_"v139, v142, v143\n" \
C_"v146, v148, v149\n" \
C_"v147, v150, v151\n" \
C_"v154, v156, v157\n" \
C_"v155, v158, v159\n" \
"buffer_store_dwordx4 v[136:139], v20, s[56:59], s68 offen offset:1024 sc1\n" \
"s_add_u32 m0, s93, 0x2000\n" \
"v_add_u32_e32 v49, 0x10000, v48\n" \
"s_xor_b32 s65, s65, 0x8000\n" \
"s_add_u32 s64, s64, 0x10000\n" \
E_"v86, v86\n" \
"buffer_store_dwordx4 v[144:147], v20, s[56:59], s68 offen offset:2048 sc1\n" \
E_"v87, v87\n" \
E_"v84, v84\n" \
E_"v85, v85\n" \
"buffer_store_dwordx4 v[152:155], v20, s[56:59], s68 offen offset:3072 sc1\n" \
E_"v88, v88\n" \
E_"v89, v89\n" \
C_"v87, v86, v87\n" \
E_"v82, v82\n" \
B_"v8, s[80:83], s92 offen offset:0 lds\n" \
E_"v83, v83\n" \
E_"v80, v80\n" \
E_"v81, v81\n" \
E_"v90, v90\n" \
E_"v91, v91\n" \
C_"v86, v84, v85\n" \
C_"v88, v88, v89\n" \
E_"v92, v92\n" \
E_"v93, v93\n" \
E_"v94, v94\n" \
E_"v95, v95\n" \
C_"v85, v82, v83\n" \
C_"v84, v80, v81\n" \
C_"v89, v90, v91\n" \
"v_and_b32_e32 v84, v12, v84\n" \
"v_and_b32_e32 v85, v13, v85\n" \
"v_and_b32_e32 v86, v14, v86\n" \
"s_waitcnt vmcnt(17) lgkmcnt(0)\n" \
"s_barrier\n" \
M_"v[120:123], a[128:131], a[48:51], v[44:47]\n" \
"buffer_load_dwordx2 v[26:27], v16, s[56:59], s96 offen sc1\n" \
M_"v[120:123], a[132:135], a[52:55], v[120:123]\n" \
R_"a[188:191], v49 offset:7168\n" \
R_"a[184:187], v49 offset:6144\n" \
M_"v[120:123], a[136:139], a[56:59], v[120:123]\n" \
"s_xor_b32 m0, s65, 0x12000\n" \
R_"a[180:183], v49 offset:5120\n" \
M_"v[120:123], a[140:143], a[60:63], v[120:123]\n" \
B_"v50, s[8:11], s64 offen offset:0 sc1 lds\n" \
M_"v[124:127], a[156:159], a[60:63], v[44:47]\n" \
R_"a[176:179], v49 offset:4096\n" \
R_"a[172:175], v49 offset:3072\n" \
M_"v[124:127], a[152:155], a[56:59], v[124:127]\n" \
B_"v50, s[8:11], s64 offen offset:1024 sc1 lds\n" \
M_"v[124:127], a[148:151], a[52:55], v[124:127]\n" \
R_"a[168:171], v49 offset:2048\n" \
"v_and_b32_e32 v87, v15, v87\n" \
M_"v[124:127], a[144:147], a[48:51], v[124:127]\n" \
C_"v90, v92, v93\n" \
C_"v91, v94, v95\n" \
M_"v[32:35], a[124:127], a[64:67], 0\n" \
B_"v50, s[8:11], s64 offen offset:2048 sc1 lds\n" \
M_"v[36:39], a[124:127], a[80:83], 0\n" \
R_"a[164:167], v49 offset:1024\n" \
R_"a[160:163], v49 offset:0\n" \
M_"v[32:35], a[224:227], a[68:71], v[32:35]\n" \
R_"a[0:3], v49 offset:8192\n" \
R_"a[4:7], v49 offset:9216\n" \
M_"v[36:39], a[224:227], a[84:87], v[36:39]\n" \
R_"a[8:11], v49 offset:10240\n" \
R_"a[12:15], v49 offset:11264\n" \
M_"v[32:35], a[228:231], a[72:75], v[32:35]\n" \
B_"v50, s[8:11], s64 offen offset:3072 sc1 lds\n" \
M_"v[36:39], a[228:231], a[88:91], v[36:39]\n" \
R_"a[16:19], v49 offset:12288\n" \
R_"a[20:23], v49 offset:13312\n" \
M_"v[32:35], a[232:235], a[76:79], v[32:35]\n" \
R_"a[24:27], v49 offset:14336\n" \
R_"a[28:31], v49 offset:15360\n" \
M_"v[36:39], a[232:235], a[92:95], v[36:39]\n" \
E_"v120, v120\n" \
"v_smfmac_f32_16x16x64_bf16 a[100:103], a[104:107], v[84:91], v21\n" \
"s_mov_b32 s63, s62\n" \
"s_waitcnt lgkmcnt(13)\n" \
M_"v[220:223], a[188:191], v[84:87], v[220:223]\n" \
E_"v121, v121\n" \
M_"v[252:255], a[188:191], v[88:91], v[252:255]\n" \
E_"v122, v122\n" \
M_"v[216:219], a[184:187], v[84:87], v[216:219]\n" \
E_"v123, v123\n" \
M_"v[248:251], a[184:187], v[88:91], v[248:251]\n" \
C_"v120, v120, v121\n" \
C_"v121, v122, v123\n" \
M_"v[212:215], a[180:183], v[84:87], v[212:215]\n" \
E_"v124, v124\n" \
M_"v[244:247], a[180:183], v[88:91], v[244:247]\n" \
"v_and_b32_e32 v120, v12, v120\n" \
"s_waitcnt lgkmcnt(10)\n" \
M_"v[208:211], a[176:179], v[84:87], v[208:211]\n" \
E_"v125, v125\n" \
M_"v[240:243], a[176:179], v[88:91], v[240:243]\n" \
E_"v126, v126\n" \
M_"v[204:207], a[172:175], v[84:87], v[204:207]\n" \
E_"v127, v127\n" \
M_"v[236:239], a[172:175], v[88:91], v[236:239]\n" \
C_"v122, v124, v125\n" \
C_"v123, v126, v127\n" \
M_"v[200:203], a[168:171], v[84:87], v[200:203]\n" \
"v_and_b32_e32 v121, v13, v121\n" \
"v_and_b32_e32 v122, v14, v122\n" \
M_"v[232:235], a[168:171], v[88:91], v[232:235]\n" \
"v_and_b32_e32 v123, v15, v123\n" \
"s_waitcnt lgkmcnt(0)\n" \
M_"v[196:199], a[164:167], v[84:87], v[196:199]\n" \
"v_accvgpr_read_b32 v56, a100\n" \
"v_accvgpr_read_b32 v24, a101\n" \
M_"v[228:231], a[164:167], v[88:91], v[228:231]\n" \
"v_readlane_b32 s97, v11, 39\n" \
"v_add_u32_e32 v17, 0x49000, v20\n" \
M_"v[192:195], a[160:163], v[84:87], v[192:195]\n" \
"v_readlane_b32 s98, v11, 40\n" \
"v_add_u32_e32 v48, s16, v20\n" \
M_"v[224:227], a[160:163], v[88:91], v[224:227]\n" \
R_"a[128:131], v48 offset:0\n" \
R_"a[144:147], v48 offset:4096\n" \
M_"a[100:103], a[116:119], v[120:123], a[100:103]\n" \
R_"a[132:135], v48 offset:1024\n" \
R_"a[148:151], v48 offset:5120\n" \
M_"v[224:227], a[0:3], v[120:123], v[224:227]\n" \
R_"a[136:139], v48 offset:2048\n" \
R_"a[152:155], v48 offset:6144\n" \
M_"v[228:231], a[4:7], v[120:123], v[228:231]\n" \
R_"a[140:143], v48 offset:3072\n" \
R_"a[156:159], v48 offset:7168\n" \
M_"v[232:235], a[8:11], v[120:123], v[232:235]\n" \
"v_accvgpr_read_b32 v55, a98\n" \
"v_accvgpr_read_b32 v23, a99\n" \
M_"a[96:99], a[120:123], a[120:123], 0\n" \
"v_add_f32_e32 v55, v55, v23\n" \
"v_max_f32_e32 v62, v62, v55\n" \
M_"v[236:239], a[12:15], v[120:123], v[236:239]\n" \
"v_rcp_f32_e32 v55, v55\n" \
M_"v[240:243], a[16:19], v[120:123], v[240:243]\n" \
"v_mul_f32_e32 v55, v55, v59\n" \
"v_mul_f32_e32 v160, v55, v160\n" \
M_"v[244:247], a[20:23], v[120:123], v[244:247]\n" \
"v_mul_f32_e32 v161, v55, v161\n" \
"v_mul_f32_e32 v168, v55, v168\n" \
M_"v[248:251], a[24:27], v[120:123], v[248:251]\n" \
"v_mul_f32_e32 v169, v55, v169\n" \
"v_mul_f32_e32 v176, v55, v176\n" \
M_"v[252:255], a[28:31], v[120:123], v[252:255]\n" \
"v_mul_f32_e32 v177, v55, v177\n" \
"v_mul_f32_e32 v184, v55, v184\n" \
"v_mul_f32_e32 v185, v55, v185\n" \
C_"v160, v160, v161\n" \
"v_mul_f32_e32 v162, v55, v162\n" \
"v_mul_f32_e32 v163, v55, v163\n" \
C_"v168, v168, v169\n" \
"v_mul_f32_e32 v170, v55, v170\n" \
"v_mul_f32_e32 v171, v55, v171\n" \
"s_waitcnt vmcnt(4)\n" \
"v_cmp_ne_u64_e32 vcc, s[62:63], v[26:27]\n" \
C_"v176, v176, v177\n" \
"v_mul_f32_e32 v178, v55, v178\n" \
"v_mul_f32_e32 v179, v55, v179\n" \
C_"v184, v184, v185\n" \
"v_mul_f32_e32 v186, v55, v186\n" \
"s_cbranch_vccnz L_sos_%=\n" \
"L_ros_%=:\n" \
B_"a[124:127], v20, s[56:59], s97 offen offset:0 sc1\n" \
"v_mul_f32_e32 v187, v55, v187\n" \
C_"v161, v162, v163\n" \
"v_mul_f32_e32 v164, v55, v164\n" \
"v_mul_f32_e32 v165, v55, v165\n" \
"v_mul_f32_e32 v166, v55, v166\n" \
"v_mul_f32_e32 v167, v55, v167\n" \
B_"a[228:231], v17, s[56:59], s97 offen offset:0 sc1\n" \
C_"v169, v170, v171\n" \
"v_mul_f32_e32 v172, v55, v172\n" \
"v_mul_f32_e32 v173, v55, v173\n" \
"v_mul_f32_e32 v174, v55, v174\n" \
"v_mul_f32_e32 v175, v55, v175\n" \
C_"v177, v178, v179\n" \
B_"a[236:239], v20, s[56:59], s98 offen offset:0 sc1\n" \
"v_mul_f32_e32 v180, v55, v180\n" \
"v_mul_f32_e32 v181, v55, v181\n" \
"v_mul_f32_e32 v182, v55, v182\n" \
"v_mul_f32_e32 v183, v55, v183\n" \
C_"v185, v186, v187\n" \
"v_mul_f32_e32 v188, v55, v188\n" \
B_"a[244:247], v17, s[56:59], s98 offen offset:0 sc1\n" \
"v_mul_f32_e32 v189, v55, v189\n" \
"v_mul_f32_e32 v190, v55, v190\n" \
"v_mul_f32_e32 v191, v55, v191\n" \
C_"v162, v164, v165\n" \
C_"v163, v166, v167\n" \
C_"v170, v172, v173\n" \
"buffer_store_dwordx4 v[160:163], v20, s[56:59], s69 offen offset:0 sc1\n" \
C_"v171, v174, v175\n" \
C_"v178, v180, v181\n" \
C_"v179, v182, v183\n" \
C_"v186, v188, v189\n" \
C_"v187, v190, v191\n" \
"v_readlane_b32 s93, v11, 23\n" \
"buffer_store_dwordx4 v[168:171], v20, s[56:59], s69 offen offset:1024 sc1\n" \
"v_readlane_b32 s84, v11, 15\n" \
"v_readlane_b32 s85, v11, 16\n" \
"v_readlane_b32 s86, v11, 17\n" \
"s_mov_b32 s87, 0x20000\n" \
"v_readlane_b32 s92, v11, 18\n" \
"v_readlane_b32 s91, v11, 24\n" \
"buffer_store_dwordx4 v[176:179], v20, s[56:59], s69 offen offset:2048 sc1\n" \
M_"a[112:115], a[120:123], v[128:131], a[112:115]\n" \
M_"a[112:115], a[120:123], v[136:139], a[112:115]\n" \
"buffer_store_dwordx4 v[184:187], v20, s[56:59], s69 offen offset:3072 sc1\n" \
"v_mfma_f32_32x32x16_bf16 v[128:143], a[120:123], a[120:123], 0\n" \
M_"a[112:115], a[120:123], v[144:147], a[112:115]\n" \
B_"a[224:227], v20, s[56:59], s97 offen offset:1024 sc1\n" \
M_"a[112:115], a[120:123], v[152:155], a[112:115]\n" \
"v_mfma_f32_32x32x16_bf16 v[144:159], a[120:123], a[120:123], 0\n" \
B_"a[232:235], v17, s[56:59], s97 offen offset:1024 sc1\n" \
"v_add_f32_e32 v56, v56, v24\n" \
M_"a[112:115], a[120:123], v[160:163], a[112:115]\n" \
B_"a[240:243], v20, s[56:59], s98 offen offset:1024 sc1\n" \
"v_max_f32_e32 v62, v62, v56\n" \
"v_rcp_f32_e32 v56, v56\n" \
M_"a[112:115], a[120:123], v[168:171], a[112:115]\n" \
"v_mfma_f32_32x32x16_bf16 v[160:175], a[120:123], a[120:123], 0\n" \
B_"a[248:251], v17, s[56:59], s98 offen offset:1024 sc1\n" \
"v_mul_f32_e32 v56, v56, v60\n" \
M_"a[112:115], a[120:123], v[176:179], a[112:115]\n" \
"v_mul_f32_e32 v192, v56, v192\n" \
"v_mul_f32_e32 v193, v56, v193\n" \
"s_waitcnt vmcnt(17) lgkmcnt(0)\n" \
"s_barrier\n" \
M_"v[64:67], a[128:131], a[64:67], v[32:35]\n" \
"s_add_u32 m0, s93, 0x0\n" \
C_"v192, v192, v193\n" \
M_"v[64:67], a[132:135], a[68:71], v[64:67]\n" \
B_"v0, s[84:87], s92 offen offset:0 lds\n" \
M_"v[64:67], a[136:139], a[72:75], v[64:67]\n" \
"v_mul_f32_e32 v194, v56, v194\n" \
"v_mul_f32_e32 v195, v56, v195\n" \
M_"v[64:67], a[140:143], a[76:79], v[64:67]\n" \
B_"v1, s[84:87], s92 offen offset:1024 lds\n" \
M_"v[68:71], a[156:159], a[76:79], v[32:35]\n" \
M_"a[112:115], a[120:123], v[184:187], a[112:115]\n" \
"v_mfma_f32_32x32x16_bf16 v[176:191], a[120:123], a[120:123], 0\n" \
B_"v2, s[84:87], s92 offen offset:2048 lds\n" \
C_"v193, v194, v195\n" \
"v_mul_f32_e32 v196, v56, v196\n" \
"v_mul_f32_e32 v197, v56, v197\n" \
"v_mul_f32_e32 v198, v56, v198\n" \
M_"v[68:71], a[152:155], a[72:75], v[68:71]\n" \
B_"v3, s[84:87], s92 offen offset:3072 lds\n" \
M_"v[68:71], a[148:151], a[68:71], v[68:71]\n" \
"s_add_u32 m0, s91, 0x1000\n" \
"v_mul_f32_e32 v199, v56, v199\n" \
M_"v[68:71], a[144:147], a[64:67], v[68:71]\n" \
B_"v4, s[84:87], s92 offen offset:0 lds\n" \
M_"v[76:79], a[144:147], a[80:83], v[36:39]\n" \
R_"a[144:147], v48 offset:12288\n" \
"v_mul_f32_e32 v200, v56, v200\n" \
M_"v[76:79], a[148:151], a[84:87], v[76:79]\n" \
B_"v5, s[84:87], s92 offen offset:1024 lds\n" \
M_"v[76:79], a[152:155], a[88:91], v[76:79]\n" \
R_"a[148:151], v48 offset:13312\n" \
R_"a[152:155], v48 offset:14336\n" \
M_"v[76:79], a[156:159], a[92:95], v[76:79]\n" \
B_"v6, s[84:87], s92 offen offset:2048 lds\n" \
M_"v[72:75], a[140:143], a[92:95], v[36:39]\n" \
R_"a[140:143], v48 offset:11264\n" \
R_"a[156:159], v48 offset:15360\n" \
M_"v[72:75], a[136:139], a[88:91], v[72:75]\n" \
B_"v7, s[84:87], s92 offen offset:3072 lds\n" \
M_"v[72:75], a[132:135], a[84:87], v[72:75]\n" \
R_"a[132:135], v48 offset:9216\n" \
R_"a[136:139], v48 offset:10240\n" \
M_"v[72:75], a[128:131], a[80:83], v[72:75]\n" \
R_"a[128:131], v48 offset:8192\n" \
"v_mul_f32_e32 v201, v56, v201\n" \
"v_accvgpr_read_b32 v57, a102\n" \
"v_accvgpr_read_b32 v25, a103\n" \
M_"a[100:103], a[120:123], a[120:123], 0\n" \
C_"v194, v196, v197\n" \
C_"v195, v198, v199\n" \
"buffer_store_dwordx4 v[192:195], v20, s[56:59], s70 offen offset:0 sc1\n" \
C_"v200, v200, v201\n" \
"v_mul_f32_e32 v202, v56, v202\n" \
"v_mul_f32_e32 v203, v56, v203\n" \
"v_add_f32_e32 v57, v57, v25\n" \
M_"a[112:115], a[120:123], v[192:195], a[112:115]\n" \
C_"v201, v202, v203\n" \
"v_mul_f32_e32 v204, v56, v204\n" \
"v_mul_f32_e32 v205, v56, v205\n" \
"v_mul_f32_e32 v206, v56, v206\n" \
"v_mul_f32_e32 v207, v56, v207\n" \
"v_mul_f32_e32 v208, v56, v208\n" \
"v_mul_f32_e32 v209, v56, v209\n" \
"v_max_f32_e32 v62, v62, v57\n" \
C_"v202, v204, v205\n" \
C_"v203, v206, v207\n" \
"buffer_store_dwordx4 v[200:203], v20, s[56:59], s70 offen offset:1024 sc1\n" \
".fill (L_ab + 28672 - .) / 4, 4, 0xBF800000\n" \
"L_ap7:\n" \
"s_cbranch_execz L_ap0\n" \
C_"v208, v208, v209\n" \
"v_mul_f32_e32 v210, v56, v210\n" \
"v_mul_f32_e32 v211, v56, v211\n" \
"v_rcp_f32_e32 v57, v57\n" \
M_"a[112:115], a[120:123], v[200:203], a[112:115]\n" \
"v_mfma_f32_32x32x16_bf16 v[192:207], a[120:123], a[120:123], 0\n" \
C_"v209, v210, v211\n" \
"v_mul_f32_e32 v212, v56, v212\n" \
"v_mul_f32_e32 v213, v56, v213\n" \
"v_mul_f32_e32 v214, v56, v214\n" \
"v_mul_f32_e32 v215, v56, v215\n" \
"v_mul_f32_e32 v216, v56, v216\n" \
"v_mul_f32_e32 v217, v56, v217\n" \
C_"v210, v212, v213\n" \
C_"v211, v214, v215\n" \
"buffer_store_dwordx4 v[208:211], v20, s[56:59], s70 offen offset:2048 sc1\n" \
C_"v216, v216, v217\n" \
"v_mul_f32_e32 v218, v56, v218\n" \
"v_mul_f32_e32 v219, v56, v219\n" \
"v_mul_f32_e32 v57, v57, v61\n" \
M_"a[112:115], a[120:123], v[208:211], a[112:115]\n" \
C_"v217, v218, v219\n" \
"v_mul_f32_e32 v220, v56, v220\n" \
"v_mul_f32_e32 v221, v56, v221\n" \
"v_mul_f32_e32 v222, v56, v222\n" \
"v_mul_f32_e32 v223, v56, v223\n" \
"v_mul_f32_e32 v224, v57, v224\n" \
"v_mul_f32_e32 v225, v57, v225\n" \
C_"v218, v220, v221\n" \
C_"v219, v222, v223\n" \
"buffer_store_dwordx4 v[216:219], v20, s[56:59], s70 offen offset:3072 sc1\n" \
C_"v224, v224, v225\n" \
"v_mul_f32_e32 v226, v57, v226\n" \
"v_mul_f32_e32 v227, v57, v227\n" \
M_"a[112:115], a[120:123], v[216:219], a[112:115]\n" \
"v_mfma_f32_32x32x16_bf16 v[208:223], a[120:123], a[120:123], 0\n" \
C_"v225, v226, v227\n" \
"v_mul_f32_e32 v228, v57, v228\n" \
"v_mul_f32_e32 v229, v57, v229\n" \
"v_mul_f32_e32 v230, v57, v230\n" \
"v_mul_f32_e32 v231, v57, v231\n" \
"v_mul_f32_e32 v232, v57, v232\n" \
"v_mul_f32_e32 v233, v57, v233\n" \
C_"v226, v228, v229\n" \
C_"v227, v230, v231\n" \
"buffer_store_dwordx4 v[224:227], v20, s[56:59], s71 offen offset:0 sc1\n" \
C_"v232, v232, v233\n" \
"v_mul_f32_e32 v234, v57, v234\n" \
"v_mul_f32_e32 v235, v57, v235\n" \
M_"a[112:115], a[120:123], v[224:227], a[112:115]\n" \
C_"v233, v234, v235\n" \
"v_mul_f32_e32 v236, v57, v236\n" \
"v_mul_f32_e32 v237, v57, v237\n" \
"v_mul_f32_e32 v238, v57, v238\n" \
"v_mul_f32_e32 v239, v57, v239\n" \
"v_mul_f32_e32 v240, v57, v240\n" \
"v_mul_f32_e32 v241, v57, v241\n" \
C_"v234, v236, v237\n" \
C_"v235, v238, v239\n" \
"buffer_store_dwordx4 v[232:235], v20, s[56:59], s71 offen offset:1024 sc1\n" \
C_"v240, v240, v241\n" \
"v_mul_f32_e32 v242, v57, v242\n" \
"v_mul_f32_e32 v243, v57, v243\n" \
M_"a[112:115], a[120:123], v[232:235], a[112:115]\n" \
"v_mfma_f32_32x32x16_bf16 v[224:239], a[120:123], a[120:123], 0\n" \
C_"v241, v242, v243\n" \
"v_mul_f32_e32 v244, v57, v244\n" \
"v_mul_f32_e32 v245, v57, v245\n" \
"v_mul_f32_e32 v246, v57, v246\n" \
"v_mul_f32_e32 v247, v57, v247\n" \
"v_mul_f32_e32 v248, v57, v248\n" \
"v_mul_f32_e32 v249, v57, v249\n" \
C_"v242, v244, v245\n" \
C_"v243, v246, v247\n" \
"buffer_store_dwordx4 v[240:243], v20, s[56:59], s71 offen offset:2048 sc1\n" \
C_"v248, v248, v249\n" \
"v_mul_f32_e32 v250, v57, v250\n" \
"v_mul_f32_e32 v251, v57, v251\n" \
M_"a[112:115], a[120:123], v[240:243], a[112:115]\n" \
C_"v249, v250, v251\n" \
"v_mul_f32_e32 v252, v57, v252\n" \
"v_mul_f32_e32 v253, v57, v253\n" \
"v_mul_f32_e32 v254, v57, v254\n" \
"v_mul_f32_e32 v255, v57, v255\n" \
C_"v250, v252, v253\n" \
C_"v251, v254, v255\n" \
"buffer_store_dwordx4 v[248:251], v20, s[56:59], s71 offen offset:3072 sc1\n" \
"s_add_u32 m0, s91, 0x2000\n" \
M_"a[112:115], a[120:123], v[248:251], a[112:115]\n" \
"v_mfma_f32_32x32x16_bf16 v[240:255], a[120:123], a[120:123], 0\n" \
"v_add_u32_e32 v53, 0x20000, v63\n" \
B_"v8, s[84:87], s92 offen offset:0 lds\n" \
E_"v70, v70\n" \
E_"v71, v71\n" \
E_"v72, v72\n" \
E_"v73, v73\n" \
"s_add_u32 s64, s64, 0x10000\n" \
"v_accvgpr_read_b32 v63, a112\n" \
"v_cmp_u_f32_e32 vcc, v63, v63\n" \
"v_cmp_lt_f32_e64 s[72:73], s21, v62\n" \
"v_readlane_b32 s95, v11, 3\n" \
E_"v68, v68\n" \
E_"v69, v69\n" \
E_"v74, v74\n" \
E_"v75, v75\n" \
"s_or_b64 vcc, s[72:73], vcc\n" \
"s_cselect_b32 s95, 0x40000000, s95\n" \
"s_cselect_b32 s94, 0x7f800000, 0\n" \
C_"v71, v70, v71\n" \
E_"v66, v66\n" \
E_"v67, v67\n" \
E_"v64, v64\n" \
E_"v65, v65\n" \
C_"v72, v72, v73\n" \
E_"v76, v76\n" \
E_"v77, v77\n" \
E_"v78, v78\n" \
E_"v79, v79\n" \
C_"v70, v68, v69\n" \
"s_waitcnt vmcnt(17) lgkmcnt(0)\n" \
"s_barrier\n" \
C_"v73, v74, v75\n" \
"v_add_u32_e32 v49, 0x10000, v48\n" \
"s_xor_b32 s65, s65, 0x8000\n" \
C_"v69, v66, v67\n" \
C_"v68, v64, v65\n" \
C_"v74, v76, v77\n" \
C_"v75, v78, v79\n" \
"v_xor_b32_e32 v48, 0x8000, v48\n" \
"s_sub_u32 s66, s19, 2\n" \
"s_branch L_f1_%=\n" \
".fill (L_ab + 32768 - .) / 4, 4, 0xBF800000\n" \
".fill (L_ab + 32768 - .) / 4, 4, 0xBF800000\n" \
"L_ap8:\n" \
"s_setpc_b64 s[88:89]\n" \
"L_out_%=:\n" \
"v_bfe_u32 v38, v20, 2, 4\n" \
"v_readlane_b32 s64, v11, 2\n" \
"v_add_u32_e32 v32, 0x49000, v20\n" \
"v_add_u32_e32 v34, 0x20000, v10\n" \
X_"a[0:1], v34 offset:0\n" \
X_"a[2:3], v34 offset:4608\n" \
X_"a[4:5], v34 offset:9216\n" \
"buffer_load_dword v26, v38, s[56:59], s64 offen sc1\n" \
X_"a[6:7], v34 offset:13824\n" \
X_"a[8:9], v34 offset:32\n" \
X_"a[10:11], v34 offset:4640\n" \
X_"a[12:13], v34 offset:9248\n" \
"buffer_store_dwordx4 v[64:67], v20, s[56:59], s61 offen offset:0 sc1\n" \
X_"a[14:15], v34 offset:13856\n" \
"v_mov_b32_e32 v39, s62\n" \
"v_readlane_b32 s70, v11, 5\n" \
"v_readlane_b32 s71, v11, 4\n" \
"buffer_store_dwordx4 v[68:71], v20, s[56:59], s61 offen offset:1024 sc1\n" \
X_"a[16:17], v34 offset:64\n" \
X_"a[18:19], v34 offset:4672\n" \
X_"a[20:21], v34 offset:9280\n" \
X_"a[22:23], v34 offset:13888\n" \
"buffer_store_dwordx4 v[72:75], v20, s[56:59], s61 offen offset:2048 sc1\n" \
X_"a[24:25], v34 offset:96\n" \
X_"a[26:27], v34 offset:4704\n" \
X_"a[28:29], v34 offset:9312\n" \
X_"a[30:31], v34 offset:13920\n" \
"buffer_store_dwordx4 v[76:79], v20, s[56:59], s61 offen offset:3072 sc1\n" \
X_"a[96:97], v34 offset:128\n" \
X_"a[98:99], v34 offset:4736\n" \
X_"a[100:101], v34 offset:9344\n" \
X_"a[102:103], v34 offset:13952\n" \
"buffer_store_dwordx4 v[80:83], v32, s[56:59], s61 offen offset:0 sc1\n" \
X_"a[104:105], v34 offset:160\n" \
X_"a[106:107], v34 offset:4768\n" \
X_"a[108:109], v34 offset:9376\n" \
X_"a[110:111], v34 offset:13984\n" \
"buffer_store_dwordx4 v[84:87], v32, s[56:59], s61 offen offset:1024 sc1\n" \
X_"a[112:113], v34 offset:192\n" \
"s_waitcnt vmcnt(16) lgkmcnt(15)\n" \
M_"v[112:115], a[0:3], a[192:195], 0\n" \
"buffer_store_dwordx4 v[96:99], v32, s[56:59], s61 offen offset:2048 sc1\n" \
M_"v[112:115], a[4:7], a[196:199], v[112:115]\n" \
X_"a[114:115], v34 offset:4800\n" \
X_"a[116:117], v34 offset:9408\n" \
"buffer_store_dwordx4 v[100:103], v32, s[56:59], s61 offen offset:3072 sc1\n" \
M_"v[116:119], a[0:3], a[208:211], 0\n" \
X_"a[118:119], v34 offset:14016\n" \
X_"a[120:121], v34 offset:224\n" \
M_"v[116:119], a[4:7], a[212:215], v[116:119]\n" \
X_"a[122:123], v34 offset:4832\n" \
"v_lshrrev_b32_e32 v44, 9, v20\n" \
M_"v[184:187], a[0:3], v[64:67], 0\n" \
"v_mul_u32_u24_e32 v44, 0xb40, v44\n" \
"v_add_u32_e32 v35, v44, v10\n" \
M_"v[184:187], a[4:7], v[68:71], v[184:187]\n" \
"v_readlane_b32 s65, v11, 33\n" \
"v_readlane_b32 s67, v11, 26\n" \
M_"v[188:191], a[0:3], v[80:83], 0\n" \
X_"a[0:1], v34 offset:9440\n" \
X_"a[2:3], v34 offset:14048\n" \
M_"v[188:191], a[4:7], v[84:87], v[188:191]\n" \
X_"a[4:5], v34 offset:256\n" \
X_"a[6:7], v34 offset:4864\n" \
M_"v[196:199], a[8:11], v[80:83], 0\n" \
"v_readlane_b32 s80, v11, 11\n" \
"v_readlane_b32 s81, v11, 12\n" \
M_"v[196:199], a[12:15], v[84:87], v[196:199]\n" \
"s_mov_b32 s82, 0x47fca0\n" \
"s_mov_b32 s83, 0x20000\n" \
M_"v[192:195], a[8:11], v[64:67], 0\n" \
"v_readlane_b32 s66, v11, 31\n" \
"s_add_u32 m0, s67, 0x0\n" \
M_"v[192:195], a[12:15], v[68:71], v[192:195]\n" \
"v_readlane_b32 s92, v11, 27\n" \
"v_readlane_b32 s73, v11, 32\n" \
M_"v[124:127], a[8:11], a[208:211], 0\n" \
B_"v0, s[80:83], s66 offen offset:0 lds\n" \
M_"v[124:127], a[12:15], a[212:215], v[124:127]\n" \
"v_readlane_b32 s94, v11, 28\n" \
"v_readlane_b32 s84, v11, 9\n" \
M_"v[120:123], a[8:11], a[192:195], 0\n" \
X_"a[8:9], v34 offset:9472\n" \
X_"a[10:11], v34 offset:14080\n" \
M_"v[120:123], a[12:15], a[196:199], v[120:123]\n" \
X_"a[12:13], v34 offset:18432\n" \
X_"a[14:15], v34 offset:23040\n" \
M_"v[128:131], a[16:19], a[192:195], 0\n" \
"v_readlane_b32 s85, v11, 10\n" \
"s_waitcnt lgkmcnt(9)\n" \
M_"v[128:131], a[20:23], a[196:199], v[128:131]\n" \
B_"v1, s[80:83], s66 offen offset:1024 lds\n" \
M_"v[132:135], a[16:19], a[208:211], 0\n" \
"s_mov_b32 s86, 0xa1ca0\n" \
"s_mov_b32 s87, 0x20000\n" \
M_"v[132:135], a[20:23], a[212:215], v[132:135]\n" \
"v_add_u32_e32 v33, 0x10000, v10\n" \
"v_readlane_b32 s68, v11, 34\n" \
M_"v[200:203], a[16:19], v[64:67], 0\n" \
"v_readlane_b32 s69, v11, 35\n" \
"v_readlane_b32 s72, v11, 37\n" \
M_"v[200:203], a[20:23], v[68:71], v[200:203]\n" \
"v_readlane_b32 s93, v11, 38\n" \
"v_readlane_b32 s63, v11, 44\n" \
M_"v[204:207], a[16:19], v[80:83], 0\n" \
X_"a[16:17], v34 offset:18464\n" \
X_"a[18:19], v34 offset:23072\n" \
M_"v[204:207], a[20:23], v[84:87], v[204:207]\n" \
X_"a[20:21], v34 offset:18496\n" \
X_"a[22:23], v34 offset:23104\n" \
M_"v[212:215], a[24:27], v[80:83], 0\n" \
B_"v2, s[80:83], s66 offen offset:2048 lds\n" \
M_"v[212:215], a[28:31], v[84:87], v[212:215]\n" \
"v_bfe_u32 v40, v20, 2, 7\n" \
"s_add_u32 s74, s63, 0x3800\n" \
M_"v[208:211], a[24:27], v[64:67], 0\n" \
"v_lshlrev_b32_e32 v41, 12, v20\n" \
"s_add_u32 s12, s63, 0x0\n" \
M_"v[208:211], a[28:31], v[68:71], v[208:211]\n" \
"v_readlane_b32 s95, v11, 41\n" \
"v_readlane_b32 s96, v11, 42\n" \
M_"v[140:143], a[24:27], a[208:211], 0\n" \
"s_add_u32 s13, s63, 0x1000\n" \
"v_readlane_b32 s97, v11, 43\n" \
M_"v[140:143], a[28:31], a[212:215], v[140:143]\n" \
B_"v3, s[80:83], s66 offen offset:3072 lds\n" \
M_"v[136:139], a[24:27], a[192:195], 0\n" \
X_"a[24:25], v34 offset:18528\n" \
X_"a[26:27], v34 offset:23136\n" \
M_"v[136:139], a[28:31], a[196:199], v[136:139]\n" \
X_"a[28:29], v34 offset:18560\n" \
X_"a[30:31], v34 offset:23168\n" \
M_"v[144:147], a[96:99], a[192:195], 0\n" \
"s_add_u32 m0, s67, 0x1000\n" \
"s_add_u32 s14, s63, 0x2000\n" \
M_"v[144:147], a[100:103], a[196:199], v[144:147]\n" \
"s_add_u32 s15, s63, 0x3000\n" \
"v_readlane_b32 s100, v11, 7\n" \
M_"v[148:151], a[96:99], a[208:211], 0\n" \
B_"v4, s[80:83], s66 offen offset:0 lds\n" \
M_"v[148:151], a[100:103], a[212:215], v[148:151]\n" \
"v_readlane_b32 s16, v11, 45\n" \
"v_bfe_u32 v44, v20, 4, 4\n" \
M_"v[216:219], a[96:99], v[64:67], 0\n" \
"s_add_u32 s17, s16, 0x1000\n" \
"s_add_u32 s18, s16, 0x2000\n" \
M_"v[216:219], a[100:103], v[68:71], v[216:219]\n" \
"s_add_u32 s19, s16, 0x3000\n" \
"v_bfe_u32 v45, v20, 8, 2\n" \
M_"v[220:223], a[96:99], v[80:83], 0\n" \
X_"a[96:97], v34 offset:18592\n" \
X_"a[98:99], v34 offset:23200\n" \
M_"v[220:223], a[100:103], v[84:87], v[220:223]\n" \
X_"a[100:101], v34 offset:18624\n" \
X_"a[102:103], v34 offset:23232\n" \
M_"v[228:231], a[104:107], v[80:83], 0\n" \
B_"v5, s[80:83], s66 offen offset:1024 lds\n" \
M_"v[228:231], a[108:111], v[84:87], v[228:231]\n" \
"v_mul_u32_u24_e32 v42, 0x480, v44\n" \
"v_lshl_add_u32 v42, v45, 3, v42\n" \
M_"v[224:227], a[104:107], v[64:67], 0\n" \
"s_waitcnt vmcnt(14)\n" \
"v_cmp_ne_u32_e32 vcc, s62, v26\n" \
M_"v[224:227], a[108:111], v[68:71], v[224:227]\n" \
"v_lshrrev_b32_e32 v44, 1, v44\n" \
"v_lshlrev_b32_e32 v45, 1, v45\n" \
M_"v[156:159], a[104:107], a[208:211], 0\n" \
"v_sub_u32_e32 v44, v44, v45\n" \
"s_cbranch_vccnz L_sa_%=\n" \
"L_ra_%=:\n" \
M_"v[156:159], a[108:111], a[212:215], v[156:159]\n" \
B_"a[64:67], v20, s[56:59], s65 offen offset:0 sc1\n" \
M_"v[152:155], a[104:107], a[192:195], 0\n" \
X_"a[104:105], v34 offset:18656\n" \
X_"a[106:107], v34 offset:23264\n" \
M_"v[152:155], a[108:111], a[196:199], v[152:155]\n" \
X_"a[108:109], v34 offset:18688\n" \
X_"a[110:111], v34 offset:23296\n" \
M_"v[160:163], a[112:115], a[192:195], 0\n" \
"v_readlane_b32 s64, v11, 36\n" \
"v_and_b32_e32 v45, 16, v20\n" \
M_"v[160:163], a[116:119], a[196:199], v[160:163]\n" \
"v_mov_b32_e32 v46, 0x3f80\n" \
"v_lshlrev_b32_e32 v45, v45, v46\n" \
M_"v[164:167], a[112:115], a[208:211], 0\n" \
B_"a[68:71], v20, s[56:59], s65 offen offset:1024 sc1\n" \
M_"v[164:167], a[116:119], a[212:215], v[164:167]\n" \
"v_mov_b32_e32 v52, 0\n" \
"v_mov_b32_e32 v53, 0\n" \
M_"v[232:235], a[112:115], v[64:67], 0\n" \
"v_mov_b32_e32 v48, 0\n" \
"v_mov_b32_e32 v49, 0\n" \
M_"v[232:235], a[116:119], v[68:71], v[232:235]\n" \
"v_add_u32_e32 v43, 0x9000, v42\n" \
"v_readlane_b32 s88, v11, 13\n" \
M_"v[236:239], a[112:115], v[80:83], 0\n" \
X_"a[112:113], v10 offset:32768\n" \
X_"a[114:115], v10 offset:37376\n" \
M_"v[236:239], a[116:119], v[84:87], v[236:239]\n" \
X_"a[116:117], v10 offset:32800\n" \
"s_waitcnt lgkmcnt(7)\n" \
M_"v[244:247], a[120:123], v[80:83], 0\n" \
X_"a[118:119], v10 offset:37408\n" \
"v_readlane_b32 s89, v11, 14\n" \
M_"v[244:247], a[0:3], v[84:87], v[244:247]\n" \
B_"a[80:83], v32, s[56:59], s65 offen offset:0 sc1\n" \
M_"v[240:243], a[120:123], v[64:67], 0\n" \
"s_mov_b32 s90, 0x240000\n" \
"s_mov_b32 s91, 0x20000\n" \
M_"v[240:243], a[0:3], v[68:71], v[240:243]\n" \
"v_mov_b32_e32 v46, s62\n" \
M_"v[172:175], a[120:123], a[208:211], 0\n" \
M_"v[172:175], a[0:3], a[212:215], v[172:175]\n" \
M_"v[168:171], a[120:123], a[192:195], 0\n" \
X_"a[120:121], v10 offset:32832\n" \
X_"a[122:123], v10 offset:37440\n" \
M_"v[168:171], a[0:3], a[196:199], v[168:171]\n" \
X_"a[0:1], v10 offset:32864\n" \
X_"a[2:3], v10 offset:37472\n" \
M_"v[176:179], a[4:7], a[192:195], 0\n" \
B_"a[84:87], v32, s[56:59], s65 offen offset:1024 sc1\n" \
M_"v[176:179], a[8:11], a[196:199], v[176:179]\n" \
M_"v[180:183], a[4:7], a[208:211], 0\n" \
M_"v[180:183], a[8:11], a[212:215], v[180:183]\n" \
M_"v[248:251], a[4:7], v[64:67], 0\n" \
M_"v[248:251], a[8:11], v[68:71], v[248:251]\n" \
B_"a[72:75], v20, s[56:59], s65 offen offset:2048 sc1\n" \
M_"v[252:255], a[4:7], v[80:83], 0\n" \
X_"a[4:5], v10 offset:32896\n" \
X_"a[6:7], v10 offset:37504\n" \
M_"v[252:255], a[8:11], v[84:87], v[252:255]\n" \
X_"a[8:9], v10 offset:32928\n" \
X_"a[10:11], v10 offset:37536\n" \
M_"v[112:115], a[12:15], a[200:203], v[112:115]\n" \
M_"v[116:119], a[12:15], a[216:219], v[116:119]\n" \
M_"v[184:187], a[12:15], v[72:75], v[184:187]\n" \
B_"a[76:79], v20, s[56:59], s65 offen offset:3072 sc1\n" \
M_"v[188:191], a[12:15], v[96:99], v[188:191]\n" \
X_"a[12:13], v10 offset:32960\n" \
X_"a[14:15], v10 offset:37568\n" \
M_"v[196:199], a[16:19], v[96:99], v[196:199]\n" \
M_"v[192:195], a[16:19], v[72:75], v[192:195]\n" \
M_"v[124:127], a[16:19], a[216:219], v[124:127]\n" \
M_"v[120:123], a[16:19], a[200:203], v[120:123]\n" \
X_"a[16:17], v10 offset:32992\n" \
X_"a[18:19], v10 offset:37600\n" \
M_"v[128:131], a[20:23], a[200:203], v[128:131]\n" \
B_"a[88:91], v32, s[56:59], s65 offen offset:2048 sc1\n" \
M_"v[132:135], a[20:23], a[216:219], v[132:135]\n" \
M_"v[200:203], a[20:23], v[72:75], v[200:203]\n" \
M_"v[204:207], a[20:23], v[96:99], v[204:207]\n" \
X_"a[20:21], v10 offset:33024\n" \
X_"a[22:23], v10 offset:37632\n" \
"s_waitcnt lgkmcnt(10)\n" \
"s_barrier\n" \
M_"v[212:215], a[24:27], v[96:99], v[212:215]\n" \
B_"a[92:95], v32, s[56:59], s65 offen offset:3072 sc1\n" \
M_"v[208:211], a[24:27], v[72:75], v[208:211]\n" \
"v_readlane_b32 s65, v11, 49\n" \
M_"v[140:143], a[24:27], a[216:219], v[140:143]\n" \
M_"v[136:139], a[24:27], a[200:203], v[136:139]\n" \
X_"a[24:25], v35 offset:41984\n" \
X_"a[26:27], v35 offset:44288\n" \
M_"v[144:147], a[28:31], a[200:203], v[144:147]\n" \
M_"v[148:151], a[28:31], a[216:219], v[148:151]\n" \
B_"v6, s[80:83], s66 offen offset:2048 lds\n" \
M_"v[216:219], a[28:31], v[72:75], v[216:219]\n" \
M_"v[220:223], a[28:31], v[96:99], v[220:223]\n" \
X_"a[28:29], v35 offset:52352\n" \
X_"a[30:31], v35 offset:54656\n" \
M_"v[228:231], a[96:99], v[96:99], v[228:231]\n" \
M_"v[224:227], a[96:99], v[72:75], v[224:227]\n" \
M_"v[156:159], a[96:99], a[216:219], v[156:159]\n" \
B_"v7, s[80:83], s66 offen offset:3072 lds\n" \
M_"v[152:155], a[96:99], a[200:203], v[152:155]\n" \
X_"a[96:97], v35 offset:42016\n" \
X_"a[98:99], v35 offset:44320\n" \
M_"v[160:163], a[100:103], a[200:203], v[160:163]\n" \
"s_add_u32 m0, s67, 0x2000\n" \
"v_readlane_b32 s67, v11, 51\n" \
M_"v[164:167], a[100:103], a[216:219], v[164:167]\n" \
M_"v[232:235], a[100:103], v[72:75], v[232:235]\n" \
M_"v[236:239], a[100:103], v[96:99], v[236:239]\n" \
X_"a[100:101], v35 offset:52384\n" \
X_"a[102:103], v35 offset:54688\n" \
M_"v[244:247], a[104:107], v[96:99], v[244:247]\n" \
B_"v8, s[80:83], s66 offen offset:0 lds\n" \
M_"v[240:243], a[104:107], v[72:75], v[240:243]\n" \
"s_add_u32 m0, s92, 0x0\n" \
"v_readlane_b32 s66, v11, 50\n" \
M_"v[172:175], a[104:107], a[216:219], v[172:175]\n" \
M_"v[168:171], a[104:107], a[200:203], v[168:171]\n" \
X_"a[104:105], v35 offset:42048\n" \
X_"a[106:107], v35 offset:44352\n" \
M_"v[176:179], a[108:111], a[200:203], v[176:179]\n" \
M_"v[180:183], a[108:111], a[216:219], v[180:183]\n" \
B_"v0, s[80:83], s73 offen offset:0 lds\n" \
M_"v[248:251], a[108:111], v[72:75], v[248:251]\n" \
M_"v[252:255], a[108:111], v[96:99], v[252:255]\n" \
X_"a[108:109], v35 offset:52416\n" \
X_"a[110:111], v35 offset:54720\n" \
M_"v[112:115], a[112:115], a[204:207], v[112:115]\n" \
M_"v[116:119], a[112:115], a[220:223], v[116:119]\n" \
M_"v[184:187], a[112:115], v[76:79], v[184:187]\n" \
B_"v1, s[80:83], s73 offen offset:1024 lds\n" \
M_"v[188:191], a[112:115], v[100:103], v[188:191]\n" \
X_"a[112:113], v35 offset:42080\n" \
X_"a[114:115], v35 offset:44384\n" \
M_"v[196:199], a[116:119], v[100:103], v[196:199]\n" \
M_"v[192:195], a[116:119], v[76:79], v[192:195]\n" \
"s_waitcnt vmcnt(18)\n" \
"s_mov_b64 exec, 1\n" \
"buffer_store_dword v39, off, s[56:59], s70 offset:0 sc1\n" \
"s_mov_b64 exec, -1\n" \
M_"v[124:127], a[116:119], a[220:223], v[124:127]\n" \
"v_readlane_b32 s70, v11, 54\n" \
M_"v[120:123], a[116:119], a[204:207], v[120:123]\n" \
X_"a[116:117], v35 offset:52448\n" \
X_"a[118:119], v35 offset:54752\n" \
M_"v[128:131], a[120:123], a[204:207], v[128:131]\n" \
M_"v[132:135], a[120:123], a[220:223], v[132:135]\n" \
B_"v2, s[80:83], s73 offen offset:2048 lds\n" \
M_"v[200:203], a[120:123], v[76:79], v[200:203]\n" \
M_"v[204:207], a[120:123], v[100:103], v[204:207]\n" \
X_"a[120:121], v35 offset:42112\n" \
X_"a[122:123], v35 offset:44416\n" \
M_"v[212:215], a[0:3], v[100:103], v[212:215]\n" \
M_"v[208:211], a[0:3], v[76:79], v[208:211]\n" \
M_"v[140:143], a[0:3], a[220:223], v[140:143]\n" \
B_"v3, s[80:83], s73 offen offset:3072 lds\n" \
M_"v[136:139], a[0:3], a[204:207], v[136:139]\n" \
X_"a[0:1], v35 offset:52480\n" \
"s_waitcnt lgkmcnt(7)\n" \
M_"v[144:147], a[4:7], a[204:207], v[144:147]\n" \
X_"a[2:3], v35 offset:54784\n" \
"s_add_u32 m0, s92, 0x1000\n" \
M_"v[148:151], a[4:7], a[220:223], v[148:151]\n" \
M_"v[216:219], a[4:7], v[76:79], v[216:219]\n" \
M_"v[220:223], a[4:7], v[100:103], v[220:223]\n" \
X_"a[4:5], v35 offset:42144\n" \
X_"a[6:7], v35 offset:44448\n" \
M_"v[228:231], a[8:11], v[100:103], v[228:231]\n" \
B_"v4, s[80:83], s73 offen offset:0 lds\n" \
M_"v[224:227], a[8:11], v[76:79], v[224:227]\n" \
M_"v[156:159], a[8:11], a[220:223], v[156:159]\n" \
M_"v[152:155], a[8:11], a[204:207], v[152:155]\n" \
X_"a[8:9], v35 offset:52512\n" \
X_"a[10:11], v35 offset:54816\n" \
M_"v[160:163], a[12:15], a[204:207], v[160:163]\n" \
M_"v[164:167], a[12:15], a[220:223], v[164:167]\n" \
B_"v5, s[80:83], s73 offen offset:1024 lds\n" \
M_"v[232:235], a[12:15], v[76:79], v[232:235]\n" \
M_"v[236:239], a[12:15], v[100:103], v[236:239]\n" \
X_"a[12:13], v35 offset:42176\n" \
X_"a[14:15], v35 offset:44480\n" \
M_"v[244:247], a[16:19], v[100:103], v[244:247]\n" \
M_"v[240:243], a[16:19], v[76:79], v[240:243]\n" \
M_"v[172:175], a[16:19], a[220:223], v[172:175]\n" \
B_"v6, s[80:83], s73 offen offset:2048 lds\n" \
M_"v[168:171], a[16:19], a[204:207], v[168:171]\n" \
X_"a[16:17], v35 offset:52544\n" \
X_"a[18:19], v35 offset:54848\n" \
M_"v[176:179], a[20:23], a[204:207], v[176:179]\n" \
M_"v[180:183], a[20:23], a[220:223], v[180:183]\n" \
M_"v[248:251], a[20:23], v[76:79], v[248:251]\n" \
M_"v[252:255], a[20:23], v[100:103], v[252:255]\n" \
X_"a[20:21], v35 offset:42208\n" \
X_"a[22:23], v35 offset:44512\n" \
M_"v[112:115], a[24:27], a[124:127], v[112:115]\n" \
B_"v7, s[80:83], s73 offen offset:3072 lds\n" \
M_"v[112:115], a[28:31], a[224:227], v[112:115]\n" \
"s_add_u32 m0, s92, 0x2000\n" \
"v_readlane_b32 s92, v11, 49\n" \
M_"v[116:119], a[24:27], a[228:231], v[116:119]\n" \
M_"v[116:119], a[28:31], a[232:235], v[116:119]\n" \
M_"v[184:187], a[24:27], a[236:239], v[184:187]\n" \
M_"v[184:187], a[28:31], a[240:243], v[184:187]\n" \
B_"v8, s[80:83], s73 offen offset:0 lds\n" \
M_"v[188:191], a[24:27], a[244:247], v[188:191]\n" \
X_"a[24:25], v35 offset:52576\n" \
X_"a[26:27], v35 offset:54880\n" \
M_"v[188:191], a[28:31], a[248:251], v[188:191]\n" \
"s_mov_b32 m0, s94\n" \
X_"a[28:29], v35 offset:42240\n" \
M_"v[196:199], a[96:99], a[244:247], v[196:199]\n" \
X_"a[30:31], v35 offset:44544\n" \
"v_readlane_b32 s73, v11, 6\n" \
M_"v[196:199], a[100:103], a[248:251], v[196:199]\n" \
M_"v[192:195], a[96:99], a[236:239], v[192:195]\n" \
B_"v9, s[84:87], 0 offen lds\n" \
M_"v[192:195], a[100:103], a[240:243], v[192:195]\n" \
M_"v[124:127], a[96:99], a[228:231], v[124:127]\n" \
M_"v[124:127], a[100:103], a[232:235], v[124:127]\n" \
M_"v[120:123], a[96:99], a[124:127], v[120:123]\n" \
X_"a[96:97], v35 offset:52608\n" \
X_"a[98:99], v35 offset:54912\n" \
M_"v[120:123], a[100:103], a[224:227], v[120:123]\n" \
X_"a[100:101], v33 offset:0\n" \
X_"a[102:103], v33 offset:4608\n" \
M_"v[128:131], a[104:107], a[124:127], v[128:131]\n" \
B_"a[128:131], v20, s[56:59], s68 offen offset:0 sc1\n" \
M_"v[128:131], a[108:111], a[224:227], v[128:131]\n" \
M_"v[132:135], a[104:107], a[228:231], v[132:135]\n" \
M_"v[132:135], a[108:111], a[232:235], v[132:135]\n" \
M_"v[200:203], a[104:107], a[236:239], v[200:203]\n" \
M_"v[200:203], a[108:111], a[240:243], v[200:203]\n" \
B_"a[132:135], v20, s[56:59], s68 offen offset:1024 sc1\n" \
M_"v[204:207], a[104:107], a[244:247], v[204:207]\n" \
X_"a[104:105], v33 offset:9216\n" \
X_"a[106:107], v33 offset:13824\n" \
M_"v[204:207], a[108:111], a[248:251], v[204:207]\n" \
X_"a[108:109], v33 offset:32\n" \
"s_waitcnt lgkmcnt(7)\n" \
M_"v[212:215], a[112:115], a[244:247], v[212:215]\n" \
X_"a[110:111], v33 offset:4640\n" \
M_"v[212:215], a[116:119], a[248:251], v[212:215]\n" \
M_"v[208:211], a[112:115], a[236:239], v[208:211]\n" \
"buffer_load_dword v47, v38, s[56:59], s71 offen sc1\n" \
M_"v[208:211], a[116:119], a[240:243], v[208:211]\n" \
M_"v[140:143], a[112:115], a[228:231], v[140:143]\n" \
M_"v[140:143], a[116:119], a[232:235], v[140:143]\n" \
M_"v[136:139], a[112:115], a[124:127], v[136:139]\n" \
X_"a[112:113], v33 offset:9248\n" \
X_"a[114:115], v33 offset:13856\n" \
M_"v[136:139], a[116:119], a[224:227], v[136:139]\n" \
X_"a[116:117], v33 offset:64\n" \
X_"a[118:119], v33 offset:4672\n" \
M_"v[144:147], a[120:123], a[124:127], v[144:147]\n" \
B_"a[144:147], v32, s[56:59], s68 offen offset:0 sc1\n" \
M_"v[144:147], a[0:3], a[224:227], v[144:147]\n" \
M_"v[148:151], a[120:123], a[228:231], v[148:151]\n" \
M_"v[148:151], a[0:3], a[232:235], v[148:151]\n" \
M_"v[216:219], a[120:123], a[236:239], v[216:219]\n" \
M_"v[216:219], a[0:3], a[240:243], v[216:219]\n" \
B_"a[148:151], v32, s[56:59], s68 offen offset:1024 sc1\n" \
M_"v[220:223], a[120:123], a[244:247], v[220:223]\n" \
X_"a[120:121], v33 offset:9280\n" \
X_"a[122:123], v33 offset:13888\n" \
M_"v[220:223], a[0:3], a[248:251], v[220:223]\n" \
X_"a[0:1], v33 offset:96\n" \
X_"a[2:3], v33 offset:4704\n" \
M_"v[228:231], a[4:7], a[244:247], v[228:231]\n" \
M_"v[228:231], a[8:11], a[248:251], v[228:231]\n" \
M_"v[224:227], a[4:7], a[236:239], v[224:227]\n" \
B_"a[152:155], v32, s[56:59], s68 offen offset:2048 sc1\n" \
M_"v[224:227], a[8:11], a[240:243], v[224:227]\n" \
M_"v[156:159], a[4:7], a[228:231], v[156:159]\n" \
M_"v[156:159], a[8:11], a[232:235], v[156:159]\n" \
M_"v[152:155], a[4:7], a[124:127], v[152:155]\n" \
X_"a[4:5], v33 offset:9312\n" \
X_"a[6:7], v33 offset:13920\n" \
M_"v[152:155], a[8:11], a[224:227], v[152:155]\n" \
X_"a[8:9], v33 offset:128\n" \
X_"a[10:11], v33 offset:4736\n" \
M_"v[160:163], a[12:15], a[124:127], v[160:163]\n" \
B_"a[156:159], v32, s[56:59], s68 offen offset:3072 sc1\n" \
M_"v[160:163], a[16:19], a[224:227], v[160:163]\n" \
M_"v[164:167], a[12:15], a[228:231], v[164:167]\n" \
M_"v[164:167], a[16:19], a[232:235], v[164:167]\n" \
M_"v[232:235], a[12:15], a[236:239], v[232:235]\n" \
M_"v[232:235], a[16:19], a[240:243], v[232:235]\n" \
B_"a[136:139], v20, s[56:59], s68 offen offset:2048 sc1\n" \
M_"v[236:239], a[12:15], a[244:247], v[236:239]\n" \
X_"a[12:13], v33 offset:9344\n" \
X_"a[14:15], v33 offset:13952\n" \
M_"v[236:239], a[16:19], a[248:251], v[236:239]\n" \
X_"a[16:17], v33 offset:160\n" \
X_"a[18:19], v33 offset:4768\n" \
M_"v[244:247], a[20:23], a[244:247], v[244:247]\n" \
M_"v[244:247], a[24:27], a[248:251], v[244:247]\n" \
M_"v[240:243], a[20:23], a[236:239], v[240:243]\n" \
B_"a[140:143], v20, s[56:59], s68 offen offset:3072 sc1\n" \
M_"v[240:243], a[24:27], a[240:243], v[240:243]\n" \
"v_readlane_b32 s68, v11, 52\n" \
M_"v[172:175], a[20:23], a[228:231], v[172:175]\n" \
M_"v[172:175], a[24:27], a[232:235], v[172:175]\n" \
M_"v[168:171], a[20:23], a[124:127], v[168:171]\n" \
X_"a[20:21], v33 offset:9376\n" \
X_"a[22:23], v33 offset:13984\n" \
M_"v[168:171], a[24:27], a[224:227], v[168:171]\n" \
X_"a[24:25], v33 offset:192\n" \
X_"a[26:27], v33 offset:4800\n" \
M_"v[176:179], a[28:31], a[124:127], v[176:179]\n" \
"s_waitcnt lgkmcnt(8)\n" \
M_"v[176:179], a[96:99], a[224:227], v[176:179]\n" \
B_"a[32:35], v20, s[56:59], s69 offen offset:0 sc1\n" \
M_"v[180:183], a[28:31], a[228:231], v[180:183]\n" \
M_"v[180:183], a[96:99], a[232:235], v[180:183]\n" \
M_"v[248:251], a[28:31], a[236:239], v[248:251]\n" \
M_"v[248:251], a[96:99], a[240:243], v[248:251]\n" \
M_"v[252:255], a[28:31], a[244:247], v[252:255]\n" \
X_"a[28:29], v33 offset:9408\n" \
X_"a[30:31], v33 offset:14016\n" \
M_"v[252:255], a[96:99], a[248:251], v[252:255]\n" \
X_"a[96:97], v33 offset:224\n" \
"s_waitcnt vmcnt(18)\n" \
M_"v[112:115], a[100:103], a[64:67], v[112:115]\n" \
B_"a[36:39], v20, s[56:59], s69 offen offset:1024 sc1\n" \
M_"v[112:115], a[104:107], a[68:71], v[112:115]\n" \
X_"a[98:99], v33 offset:4832\n" \
M_"v[116:119], a[100:103], a[80:83], v[116:119]\n" \
X_"a[100:101], v33 offset:9440\n" \
X_"a[102:103], v33 offset:14048\n" \
M_"v[116:119], a[104:107], a[84:87], v[116:119]\n" \
X_"a[104:105], v33 offset:256\n" \
X_"a[106:107], v33 offset:4864\n" \
M_"v[124:127], a[108:111], a[80:83], v[124:127]\n" \
M_"v[124:127], a[112:115], a[84:87], v[124:127]\n" \
B_"a[48:51], v32, s[56:59], s69 offen offset:0 sc1\n" \
M_"v[120:123], a[108:111], a[64:67], v[120:123]\n" \
X_"a[108:109], v33 offset:9472\n" \
X_"a[110:111], v33 offset:14080\n" \
M_"v[120:123], a[112:115], a[68:71], v[120:123]\n" \
X_"a[112:113], v33 offset:18432\n" \
X_"a[114:115], v33 offset:23040\n" \
M_"v[128:131], a[116:119], a[64:67], v[128:131]\n" \
M_"v[128:131], a[120:123], a[68:71], v[128:131]\n" \
M_"v[132:135], a[116:119], a[80:83], v[132:135]\n" \
B_"a[52:55], v32, s[56:59], s69 offen offset:1024 sc1\n" \
M_"v[132:135], a[120:123], a[84:87], v[132:135]\n" \
X_"a[116:117], v33 offset:27648\n" \
X_"a[118:119], v33 offset:32256\n" \
M_"v[140:143], a[0:3], a[80:83], v[140:143]\n" \
X_"a[120:121], v33 offset:18464\n" \
X_"a[122:123], v33 offset:23072\n" \
M_"v[140:143], a[4:7], a[84:87], v[140:143]\n" \
"s_waitcnt vmcnt(10)\n" \
"v_cmp_ne_u32_e32 vcc, s62, v47\n" \
M_"v[136:139], a[0:3], a[64:67], v[136:139]\n" \
X_"a[0:1], v33 offset:27680\n" \
".fill (L_ab + 36864 - .) / 4, 4, 0xBF800000\n" \
"L_ap9:\n" \
"s_cbranch_execz L_ap0\n" \
X_"a[2:3], v33 offset:32288\n" \
M_"v[136:139], a[4:7], a[68:71], v[136:139]\n" \
B_"a[56:59], v32, s[56:59], s69 offen offset:2048 sc1\n" \
M_"v[144:147], a[8:11], a[64:67], v[144:147]\n" \
X_"a[4:5], v33 offset:18496\n" \
"s_waitcnt lgkmcnt(13)\n" \
M_"v[144:147], a[12:15], a[68:71], v[144:147]\n" \
X_"a[6:7], v33 offset:23104\n" \
"s_cbranch_vccnz L_sb_%=\n" \
"L_rb_%=:\n" \
M_"v[148:151], a[8:11], a[80:83], v[148:151]\n" \
X_"a[8:9], v33 offset:27712\n" \
X_"a[10:11], v33 offset:32320\n" \
M_"v[148:151], a[12:15], a[84:87], v[148:151]\n" \
X_"a[12:13], v33 offset:18528\n" \
X_"a[14:15], v33 offset:23136\n" \
B_"a[60:63], v32, s[56:59], s69 offen offset:3072 sc1\n" \
M_"v[156:159], a[16:19], a[80:83], v[156:159]\n" \
"v_readlane_b32 s71, v11, 55\n" \
"v_cmp_eq_u32_e32 vcc, 0, v44\n" \
M_"v[156:159], a[20:23], a[84:87], v[156:159]\n" \
M_"v[152:155], a[16:19], a[64:67], v[152:155]\n" \
X_"a[16:17], v33 offset:27744\n" \
X_"a[18:19], v33 offset:32352\n" \
M_"v[152:155], a[20:23], a[68:71], v[152:155]\n" \
X_"a[20:21], v33 offset:18560\n" \
X_"a[22:23], v33 offset:23168\n" \
B_"a[40:43], v20, s[56:59], s69 offen offset:2048 sc1\n" \
M_"v[160:163], a[24:27], a[64:67], v[160:163]\n" \
"v_cndmask_b32_e32 v50, 0, v45, vcc\n" \
"v_cmp_eq_u32_e32 vcc, 1, v44\n" \
M_"v[160:163], a[28:31], a[68:71], v[160:163]\n" \
"v_and_b32_e32 v44, 0x3c0, v20\n" \
"v_cndmask_b32_e32 v51, 0, v45, vcc\n" \
M_"v[164:167], a[24:27], a[80:83], v[164:167]\n" \
X_"a[24:25], v33 offset:27776\n" \
X_"a[26:27], v33 offset:32384\n" \
M_"v[164:167], a[28:31], a[84:87], v[164:167]\n" \
X_"a[28:29], v33 offset:18592\n" \
X_"a[30:31], v33 offset:23200\n" \
B_"a[44:47], v20, s[56:59], s69 offen offset:3072 sc1\n" \
M_"v[172:175], a[96:99], a[80:83], v[172:175]\n" \
"v_readlane_b32 s69, v11, 53\n" \
"v_bfe_u32 v45, v20, 1, 5\n" \
M_"v[172:175], a[100:103], a[84:87], v[172:175]\n" \
"v_and_b32_e32 v45, 24, v45\n" \
"v_add_u32_e32 v45, 0x4800, v45\n" \
M_"v[168:171], a[96:99], a[64:67], v[168:171]\n" \
X_"a[96:97], v33 offset:27808\n" \
X_"a[98:99], v33 offset:32416\n" \
M_"v[168:171], a[100:103], a[68:71], v[168:171]\n" \
X_"a[100:101], v33 offset:18624\n" \
"s_waitcnt lgkmcnt(13)\n" \
M_"v[176:179], a[104:107], a[64:67], v[176:179]\n" \
X_"a[102:103], v33 offset:23232\n" \
"v_add_u32_e32 v36, v44, v45\n" \
M_"v[176:179], a[108:111], a[68:71], v[176:179]\n" \
B_"a[124:127], v20, s[56:59], s64 offen offset:0 sc1\n" \
M_"v[180:183], a[104:107], a[80:83], v[180:183]\n" \
X_"a[104:105], v33 offset:27840\n" \
X_"a[106:107], v33 offset:32448\n" \
M_"v[180:183], a[108:111], a[84:87], v[180:183]\n" \
X_"a[108:109], v33 offset:18656\n" \
X_"a[110:111], v33 offset:23264\n" \
M_"v[112:115], a[112:115], a[72:75], v[112:115]\n" \
M_"v[112:115], a[116:119], a[76:79], v[112:115]\n" \
M_"v[116:119], a[112:115], a[88:91], v[116:119]\n" \
X_"a[112:113], v33 offset:27872\n" \
X_"a[114:115], v33 offset:32480\n" \
M_"v[116:119], a[116:119], a[92:95], v[116:119]\n" \
X_"a[116:117], v33 offset:18688\n" \
X_"a[118:119], v33 offset:23296\n" \
M_"v[124:127], a[120:123], a[88:91], v[124:127]\n" \
B_"a[224:227], v20, s[56:59], s64 offen offset:1024 sc1\n" \
M_"v[124:127], a[0:3], a[92:95], v[124:127]\n" \
M_"v[120:123], a[120:123], a[72:75], v[120:123]\n" \
X_"a[120:121], v33 offset:27904\n" \
X_"a[122:123], v33 offset:32512\n" \
M_"v[120:123], a[0:3], a[76:79], v[120:123]\n" \
"s_barrier\n" \
M_"v[128:131], a[4:7], a[72:75], v[128:131]\n" \
X_"a[0:1], v33 offset:36864\n" \
X_"a[2:3], v33 offset:41472\n" \
M_"v[128:131], a[8:11], a[76:79], v[128:131]\n" \
B_"a[236:239], v32, s[56:59], s64 offen offset:0 sc1\n" \
M_"v[132:135], a[4:7], a[88:91], v[132:135]\n" \
X_"a[4:5], v33 offset:46080\n" \
X_"a[6:7], v33 offset:50688\n" \
M_"v[132:135], a[8:11], a[92:95], v[132:135]\n" \
X_"a[8:9], v33 offset:55296\n" \
"s_waitcnt lgkmcnt(15)\n" \
M_"v[140:143], a[12:15], a[88:91], v[140:143]\n" \
X_"a[10:11], v33 offset:59904\n" \
M_"v[140:143], a[16:19], a[92:95], v[140:143]\n" \
M_"v[136:139], a[12:15], a[72:75], v[136:139]\n" \
X_"a[12:13], v10 offset:0\n" \
X_"a[14:15], v10 offset:4608\n" \
M_"v[136:139], a[16:19], a[76:79], v[136:139]\n" \
X_"a[16:17], v34 offset:0\n" \
X_"a[18:19], v34 offset:4608\n" \
M_"v[144:147], a[20:23], a[72:75], v[144:147]\n" \
B_"a[240:243], v32, s[56:59], s64 offen offset:1024 sc1\n" \
M_"v[144:147], a[24:27], a[76:79], v[144:147]\n" \
M_"v[148:151], a[20:23], a[88:91], v[148:151]\n" \
X_"a[20:21], v34 offset:9216\n" \
X_"a[22:23], v34 offset:13824\n" \
M_"v[148:151], a[24:27], a[92:95], v[148:151]\n" \
X_"a[24:25], v34 offset:18432\n" \
X_"a[26:27], v34 offset:23040\n" \
M_"v[156:159], a[28:31], a[88:91], v[156:159]\n" \
M_"v[156:159], a[96:99], a[92:95], v[156:159]\n" \
B_"a[244:247], v32, s[56:59], s64 offen offset:2048 sc1\n" \
M_"v[152:155], a[28:31], a[72:75], v[152:155]\n" \
X_"a[28:29], v10 offset:9216\n" \
X_"a[30:31], v10 offset:13824\n" \
M_"v[152:155], a[96:99], a[76:79], v[152:155]\n" \
X_"a[96:97], v33 offset:36896\n" \
X_"a[98:99], v33 offset:41504\n" \
M_"v[160:163], a[100:103], a[72:75], v[160:163]\n" \
"s_waitcnt lgkmcnt(15)\n" \
M_"v[160:163], a[104:107], a[76:79], v[160:163]\n" \
M_"v[164:167], a[100:103], a[88:91], v[164:167]\n" \
X_"a[100:101], v33 offset:46112\n" \
X_"a[102:103], v33 offset:50720\n" \
M_"v[164:167], a[104:107], a[92:95], v[164:167]\n" \
X_"a[104:105], v33 offset:55328\n" \
X_"a[106:107], v33 offset:59936\n" \
M_"v[172:175], a[108:111], a[88:91], v[172:175]\n" \
B_"a[248:251], v32, s[56:59], s64 offen offset:3072 sc1\n" \
M_"v[172:175], a[112:115], a[92:95], v[172:175]\n" \
M_"v[168:171], a[108:111], a[72:75], v[168:171]\n" \
X_"a[108:109], v10 offset:32\n" \
X_"a[110:111], v10 offset:4640\n" \
M_"v[168:171], a[112:115], a[76:79], v[168:171]\n" \
X_"a[112:113], v34 offset:32\n" \
X_"a[114:115], v34 offset:4640\n" \
M_"v[176:179], a[116:119], a[72:75], v[176:179]\n" \
M_"v[176:179], a[120:123], a[76:79], v[176:179]\n" \
B_"a[228:231], v20, s[56:59], s64 offen offset:2048 sc1\n" \
M_"v[180:183], a[116:119], a[88:91], v[180:183]\n" \
X_"a[116:117], v34 offset:9248\n" \
X_"a[118:119], v34 offset:13856\n" \
M_"v[180:183], a[120:123], a[92:95], v[180:183]\n" \
X_"a[120:121], v34 offset:18464\n" \
X_"a[122:123], v34 offset:23072\n" \
M_"v[112:115], a[0:3], a[128:131], v[112:115]\n" \
"s_waitcnt lgkmcnt(15)\n" \
M_"v[112:115], a[4:7], a[132:135], v[112:115]\n" \
"s_waitcnt vmcnt(18)\n" \
M_"v[116:119], a[0:3], a[144:147], v[116:119]\n" \
X_"a[0:1], v10 offset:9248\n" \
X_"a[2:3], v10 offset:13856\n" \
M_"v[116:119], a[4:7], a[148:151], v[116:119]\n" \
X_"a[4:5], v33 offset:36928\n" \
X_"a[6:7], v33 offset:41536\n" \
M_"v[116:119], a[8:11], a[152:155], v[116:119]\n" \
"s_waitcnt vmcnt(17)\n" \
M_"v[116:119], a[12:15], a[156:159], v[116:119]\n" \
"s_waitcnt vmcnt(16)\n" \
M_"v[112:115], a[8:11], a[136:139], v[112:115]\n" \
X_"a[8:9], v33 offset:46144\n" \
"s_waitcnt vmcnt(15)\n" \
M_"v[112:115], a[12:15], a[140:143], v[112:115]\n" \
X_"a[10:11], v33 offset:50752\n" \
"s_waitcnt vmcnt(14)\n" \
M_"v[112:115], a[16:19], a[32:35], v[112:115]\n" \
X_"a[12:13], v33 offset:55360\n" \
"s_waitcnt vmcnt(13)\n" \
M_"v[112:115], a[20:23], a[36:39], v[112:115]\n" \
X_"a[14:15], v33 offset:59968\n" \
"s_waitcnt vmcnt(12)\n" \
M_"v[116:119], a[16:19], a[48:51], v[116:119]\n" \
X_"a[16:17], v10 offset:64\n" \
"s_waitcnt vmcnt(11)\n" \
M_"v[116:119], a[20:23], a[52:55], v[116:119]\n" \
X_"a[18:19], v10 offset:4672\n" \
"s_waitcnt vmcnt(10)\n" \
M_"v[116:119], a[24:27], a[56:59], v[116:119]\n" \
X_"a[20:21], v34 offset:64\n" \
X_"a[22:23], v34 offset:4672\n" \
B_"a[232:235], v20, s[56:59], s64 offen offset:3072 sc1\n" \
"s_waitcnt vmcnt(10) lgkmcnt(12)\n" \
M_"v[116:119], a[28:31], a[60:63], v[116:119]\n" \
B_"a[160:163], v20, s[56:59], s72 offen offset:0 sc1\n" \
"s_waitcnt vmcnt(10)\n" \
M_"v[112:115], a[24:27], a[40:43], v[112:115]\n" \
X_"a[24:25], v34 offset:9280\n" \
X_"a[26:27], v34 offset:13888\n" \
B_"a[164:167], v20, s[56:59], s72 offen offset:1024 sc1\n" \
"s_waitcnt vmcnt(10)\n" \
M_"v[112:115], a[28:31], a[44:47], v[112:115]\n" \
X_"a[28:29], v34 offset:18496\n" \
X_"a[30:31], v34 offset:23104\n" \
M_"v[120:123], a[96:99], a[128:131], v[120:123]\n" \
B_"a[176:179], v32, s[56:59], s72 offen offset:0 sc1\n" \
M_"v[120:123], a[100:103], a[132:135], v[120:123]\n" \
M_"v[124:127], a[96:99], a[144:147], v[124:127]\n" \
X_"a[96:97], v10 offset:9280\n" \
X_"a[98:99], v10 offset:13888\n" \
C_"v112, v112, v113\n" \
C_"v113, v114, v115\n" \
C_"v114, v116, v117\n" \
C_"v115, v118, v119\n" \
"buffer_store_dwordx4 v[112:115], v20, s[56:59], s65 offen offset:0 sc1\n" \
"s_add_u32 s65, s65, 0x3800\n" \
M_"v[124:127], a[100:103], a[148:151], v[124:127]\n" \
X_"a[100:101], v33 offset:36960\n" \
X_"a[102:103], v33 offset:41568\n" \
M_"v[124:127], a[104:107], a[152:155], v[124:127]\n" \
M_"v[124:127], a[108:111], a[156:159], v[124:127]\n" \
M_"v[120:123], a[104:107], a[136:139], v[120:123]\n" \
X_"a[104:105], v33 offset:46176\n" \
X_"a[106:107], v33 offset:50784\n" \
M_"v[120:123], a[108:111], a[140:143], v[120:123]\n" \
X_"a[108:109], v33 offset:55392\n" \
X_"a[110:111], v33 offset:60000\n" \
M_"v[120:123], a[112:115], a[32:35], v[120:123]\n" \
B_"a[180:183], v32, s[56:59], s72 offen offset:1024 sc1\n" \
M_"v[120:123], a[116:119], a[36:39], v[120:123]\n" \
M_"v[124:127], a[112:115], a[48:51], v[124:127]\n" \
X_"a[112:113], v10 offset:96\n" \
X_"a[114:115], v10 offset:4704\n" \
M_"v[124:127], a[116:119], a[52:55], v[124:127]\n" \
X_"a[116:117], v34 offset:96\n" \
X_"a[118:119], v34 offset:4704\n" \
M_"v[124:127], a[120:123], a[56:59], v[124:127]\n" \
"s_waitcnt lgkmcnt(12)\n" \
M_"v[124:127], a[0:3], a[60:63], v[124:127]\n" \
B_"a[168:171], v20, s[56:59], s72 offen offset:2048 sc1\n" \
M_"v[120:123], a[120:123], a[40:43], v[120:123]\n" \
X_"a[120:121], v34 offset:9312\n" \
X_"a[122:123], v34 offset:13920\n" \
M_"v[120:123], a[0:3], a[44:47], v[120:123]\n" \
X_"a[0:1], v34 offset:18528\n" \
X_"a[2:3], v34 offset:23136\n" \
M_"v[128:131], a[4:7], a[128:131], v[128:131]\n" \
M_"v[128:131], a[8:11], a[132:135], v[128:131]\n" \
M_"v[132:135], a[4:7], a[144:147], v[132:135]\n" \
X_"a[4:5], v10 offset:9312\n" \
X_"a[6:7], v10 offset:13920\n" \
M_"v[132:135], a[8:11], a[148:151], v[132:135]\n" \
C_"v120, v120, v121\n" \
C_"v121, v122, v123\n" \
C_"v122, v124, v125\n" \
C_"v123, v126, v127\n" \
"buffer_store_dwordx4 v[120:123], v20, s[56:59], s66 offen offset:0 sc1\n" \
"s_add_u32 s66, s66, 0x3000\n" \
M_"v[132:135], a[12:15], a[152:155], v[132:135]\n" \
X_"a[8:9], v33 offset:36992\n" \
X_"a[10:11], v33 offset:41600\n" \
M_"v[132:135], a[16:19], a[156:159], v[132:135]\n" \
M_"v[128:131], a[12:15], a[136:139], v[128:131]\n" \
X_"a[12:13], v33 offset:46208\n" \
X_"a[14:15], v33 offset:50816\n" \
M_"v[128:131], a[16:19], a[140:143], v[128:131]\n" \
X_"a[16:17], v33 offset:55424\n" \
X_"a[18:19], v33 offset:60032\n" \
M_"v[128:131], a[20:23], a[32:35], v[128:131]\n" \
B_"a[184:187], v32, s[56:59], s72 offen offset:2048 sc1\n" \
M_"v[128:131], a[24:27], a[36:39], v[128:131]\n" \
M_"v[132:135], a[20:23], a[48:51], v[132:135]\n" \
X_"a[20:21], v10 offset:128\n" \
X_"a[22:23], v10 offset:4736\n" \
M_"v[132:135], a[24:27], a[52:55], v[132:135]\n" \
X_"a[24:25], v34 offset:128\n" \
X_"a[26:27], v34 offset:4736\n" \
M_"v[132:135], a[28:31], a[56:59], v[132:135]\n" \
"s_waitcnt lgkmcnt(12)\n" \
M_"v[132:135], a[96:99], a[60:63], v[132:135]\n" \
B_"a[172:175], v20, s[56:59], s72 offen offset:3072 sc1\n" \
M_"v[128:131], a[28:31], a[40:43], v[128:131]\n" \
X_"a[28:29], v34 offset:9344\n" \
X_"a[30:31], v34 offset:13952\n" \
M_"v[128:131], a[96:99], a[44:47], v[128:131]\n" \
X_"a[96:97], v34 offset:18560\n" \
X_"a[98:99], v34 offset:23168\n" \
M_"v[136:139], a[100:103], a[128:131], v[136:139]\n" \
M_"v[136:139], a[104:107], a[132:135], v[136:139]\n" \
M_"v[140:143], a[100:103], a[144:147], v[140:143]\n" \
B_"a[188:191], v32, s[56:59], s72 offen offset:3072 sc1\n" \
M_"v[140:143], a[104:107], a[148:151], v[140:143]\n" \
"v_readlane_b32 s72, v11, 56\n" \
C_"v128, v128, v129\n" \
C_"v129, v130, v131\n" \
C_"v130, v132, v133\n" \
C_"v131, v134, v135\n" \
M_"v[140:143], a[108:111], a[152:155], v[140:143]\n" \
"buffer_store_dwordx4 v[128:131], v20, s[56:59], s67 offen offset:0 sc1\n" \
"s_add_u32 s67, s67, 0x2800\n" \
M_"v[140:143], a[112:115], a[156:159], v[140:143]\n" \
X_"a[100:101], v10 offset:9344\n" \
X_"a[102:103], v10 offset:13952\n" \
M_"v[136:139], a[108:111], a[136:139], v[136:139]\n" \
X_"a[104:105], v33 offset:37024\n" \
X_"a[106:107], v33 offset:41632\n" \
M_"v[136:139], a[112:115], a[140:143], v[136:139]\n" \
X_"a[108:109], v33 offset:46240\n" \
X_"a[110:111], v33 offset:50848\n" \
M_"v[136:139], a[116:119], a[32:35], v[136:139]\n" \
X_"a[112:113], v33 offset:55456\n" \
X_"a[114:115], v33 offset:60064\n" \
M_"v[136:139], a[120:123], a[36:39], v[136:139]\n" \
B_"a[192:195], v20, s[56:59], s93 offen offset:0 sc1\n" \
M_"v[140:143], a[116:119], a[48:51], v[140:143]\n" \
X_"a[116:117], v10 offset:160\n" \
X_"a[118:119], v10 offset:4768\n" \
M_"v[140:143], a[120:123], a[52:55], v[140:143]\n" \
X_"a[120:121], v34 offset:160\n" \
X_"a[122:123], v34 offset:4768\n" \
M_"v[140:143], a[0:3], a[56:59], v[140:143]\n" \
"s_waitcnt lgkmcnt(13)\n" \
M_"v[140:143], a[4:7], a[60:63], v[140:143]\n" \
M_"v[136:139], a[0:3], a[40:43], v[136:139]\n" \
B_"a[196:199], v20, s[56:59], s93 offen offset:1024 sc1\n" \
M_"v[136:139], a[4:7], a[44:47], v[136:139]\n" \
X_"a[0:1], v34 offset:9376\n" \
X_"a[2:3], v34 offset:13984\n" \
M_"v[144:147], a[8:11], a[128:131], v[144:147]\n" \
X_"a[4:5], v34 offset:18592\n" \
X_"a[6:7], v34 offset:23200\n" \
M_"v[144:147], a[12:15], a[132:135], v[144:147]\n" \
M_"v[148:151], a[8:11], a[144:147], v[148:151]\n" \
X_"a[8:9], v10 offset:9376\n" \
C_"v136, v136, v137\n" \
C_"v137, v138, v139\n" \
C_"v138, v140, v141\n" \
C_"v139, v142, v143\n" \
"buffer_store_dwordx4 v[136:139], v20, s[56:59], s68 offen offset:0 sc1\n" \
"s_add_u32 s68, s68, 0x2000\n" \
M_"v[148:151], a[12:15], a[148:151], v[148:151]\n" \
X_"a[10:11], v10 offset:13984\n" \
X_"a[12:13], v33 offset:37056\n" \
M_"v[148:151], a[16:19], a[152:155], v[148:151]\n" \
X_"a[14:15], v33 offset:41664\n" \
M_"v[148:151], a[20:23], a[156:159], v[148:151]\n" \
M_"v[144:147], a[16:19], a[136:139], v[144:147]\n" \
B_"a[208:211], v32, s[56:59], s93 offen offset:0 sc1\n" \
M_"v[144:147], a[20:23], a[140:143], v[144:147]\n" \
X_"a[16:17], v33 offset:46272\n" \
X_"a[18:19], v33 offset:50880\n" \
M_"v[144:147], a[24:27], a[32:35], v[144:147]\n" \
X_"a[20:21], v33 offset:55488\n" \
X_"a[22:23], v33 offset:60096\n" \
M_"v[144:147], a[28:31], a[36:39], v[144:147]\n" \
M_"v[148:151], a[24:27], a[48:51], v[148:151]\n" \
X_"a[24:25], v10 offset:192\n" \
X_"a[26:27], v10 offset:4800\n" \
B_"a[212:215], v32, s[56:59], s93 offen offset:1024 sc1\n" \
M_"v[148:151], a[28:31], a[52:55], v[148:151]\n" \
X_"a[28:29], v34 offset:192\n" \
"s_waitcnt lgkmcnt(13)\n" \
M_"v[148:151], a[96:99], a[56:59], v[148:151]\n" \
X_"a[30:31], v34 offset:4800\n" \
M_"v[148:151], a[100:103], a[60:63], v[148:151]\n" \
M_"v[144:147], a[96:99], a[40:43], v[144:147]\n" \
X_"a[96:97], v34 offset:9408\n" \
X_"a[98:99], v34 offset:14016\n" \
B_"a[216:219], v32, s[56:59], s93 offen offset:2048 sc1\n" \
M_"v[144:147], a[100:103], a[44:47], v[144:147]\n" \
X_"a[100:101], v34 offset:18624\n" \
X_"a[102:103], v34 offset:23232\n" \
M_"v[152:155], a[104:107], a[128:131], v[152:155]\n" \
M_"v[152:155], a[108:111], a[132:135], v[152:155]\n" \
M_"v[156:159], a[104:107], a[144:147], v[156:159]\n" \
X_"a[104:105], v10 offset:9408\n" \
X_"a[106:107], v10 offset:14016\n" \
B_"a[220:223], v32, s[56:59], s93 offen offset:3072 sc1\n" \
C_"v144, v144, v145\n" \
C_"v145, v146, v147\n" \
C_"v146, v148, v149\n" \
C_"v147, v150, v151\n" \
M_"v[156:159], a[108:111], a[148:151], v[156:159]\n" \
X_"a[108:109], v33 offset:256\n" \
X_"a[110:111], v33 offset:4864\n" \
"buffer_store_dwordx4 v[144:147], v20, s[56:59], s69 offen offset:0 sc1\n" \
"s_add_u32 s69, s69, 0x1800\n" \
M_"v[156:159], a[112:115], a[152:155], v[156:159]\n" \
M_"v[156:159], a[116:119], a[156:159], v[156:159]\n" \
M_"v[152:155], a[112:115], a[136:139], v[152:155]\n" \
X_"a[112:113], v33 offset:9472\n" \
X_"a[114:115], v33 offset:14080\n" \
M_"v[152:155], a[116:119], a[140:143], v[152:155]\n" \
B_"a[200:203], v20, s[56:59], s93 offen offset:2048 sc1\n" \
M_"v[152:155], a[120:123], a[32:35], v[152:155]\n" \
X_"a[116:117], v33 offset:18688\n" \
X_"a[118:119], v33 offset:23296\n" \
M_"v[152:155], a[0:3], a[36:39], v[152:155]\n" \
M_"v[156:159], a[120:123], a[48:51], v[156:159]\n" \
X_"a[120:121], v33 offset:27904\n" \
X_"a[122:123], v33 offset:32512\n" \
M_"v[156:159], a[0:3], a[52:55], v[156:159]\n" \
X_"a[0:1], v33 offset:37120\n" \
B_"a[204:207], v20, s[56:59], s93 offen offset:3072 sc1\n" \
"s_waitcnt lgkmcnt(11)\n" \
M_"v[156:159], a[4:7], a[56:59], v[156:159]\n" \
X_"a[2:3], v33 offset:41728\n" \
M_"v[156:159], a[8:11], a[60:63], v[156:159]\n" \
M_"v[152:155], a[4:7], a[40:43], v[152:155]\n" \
X_"a[4:5], v33 offset:46336\n" \
X_"a[6:7], v33 offset:50944\n" \
M_"v[152:155], a[8:11], a[44:47], v[152:155]\n" \
X_"a[8:9], v33 offset:55552\n" \
X_"a[10:11], v33 offset:60160\n" \
M_"v[160:163], a[12:15], a[128:131], v[160:163]\n" \
B_"v[64:67], v20, s[56:59], s95 offen offset:0 sc1\n" \
M_"v[160:163], a[16:19], a[132:135], v[160:163]\n" \
M_"v[164:167], a[12:15], a[144:147], v[164:167]\n" \
X_"a[12:13], v10 offset:256\n" \
X_"a[14:15], v10 offset:4864\n" \
C_"v152, v152, v153\n" \
C_"v153, v154, v155\n" \
C_"v154, v156, v157\n" \
C_"v155, v158, v159\n" \
"buffer_store_dwordx4 v[152:155], v20, s[56:59], s70 offen offset:0 sc1\n" \
"s_add_u32 s70, s70, 0x1000\n" \
M_"v[164:167], a[16:19], a[148:151], v[164:167]\n" \
X_"a[16:17], v34 offset:256\n" \
X_"a[18:19], v34 offset:4864\n" \
M_"v[164:167], a[20:23], a[152:155], v[164:167]\n" \
M_"v[164:167], a[24:27], a[156:159], v[164:167]\n" \
M_"v[160:163], a[20:23], a[136:139], v[160:163]\n" \
X_"a[20:21], v34 offset:9472\n" \
X_"a[22:23], v34 offset:14080\n" \
M_"v[160:163], a[24:27], a[140:143], v[160:163]\n" \
X_"a[24:25], v34 offset:18688\n" \
X_"a[26:27], v34 offset:23296\n" \
"s_waitcnt vmcnt(19)\n" \
"s_mov_b64 exec, 1\n" \
"buffer_store_dword v39, off, s[56:59], s65 offset:0 sc1\n" \
"s_mov_b64 exec, -1\n" \
M_"v[160:163], a[28:31], a[32:35], v[160:163]\n" \
M_"v[160:163], a[96:99], a[36:39], v[160:163]\n" \
M_"v[164:167], a[28:31], a[48:51], v[164:167]\n" \
X_"a[28:29], v10 offset:9472\n" \
X_"a[30:31], v10 offset:14080\n" \
M_"v[164:167], a[96:99], a[52:55], v[164:167]\n" \
X_"a[96:97], v33 offset:0\n" \
X_"a[98:99], v33 offset:4608\n" \
M_"v[164:167], a[100:103], a[56:59], v[164:167]\n" \
"s_waitcnt lgkmcnt(12)\n" \
M_"v[164:167], a[104:107], a[60:63], v[164:167]\n" \
B_"v[68:71], v20, s[56:59], s95 offen offset:1024 sc1\n" \
M_"v[160:163], a[100:103], a[40:43], v[160:163]\n" \
X_"a[100:101], v33 offset:9216\n" \
X_"a[102:103], v33 offset:13824\n" \
M_"v[160:163], a[104:107], a[44:47], v[160:163]\n" \
X_"a[104:105], v33 offset:18432\n" \
X_"a[106:107], v33 offset:23040\n" \
M_"v[248:251], a[108:111], a[124:127], v[248:251]\n" \
M_"v[248:251], a[112:115], a[224:227], v[248:251]\n" \
M_"v[252:255], a[108:111], a[236:239], v[252:255]\n" \
X_"a[108:109], v33 offset:27648\n" \
X_"a[110:111], v33 offset:32256\n" \
M_"v[252:255], a[112:115], a[240:243], v[252:255]\n" \
C_"v160, v160, v161\n" \
C_"v161, v162, v163\n" \
C_"v162, v164, v165\n" \
C_"v163, v166, v167\n" \
"buffer_store_dwordx4 v[160:163], v20, s[56:59], s71 offen offset:0 sc1\n" \
"s_add_u32 s71, s71, 0x800\n" \
M_"v[252:255], a[116:119], a[244:247], v[252:255]\n" \
X_"a[112:113], v33 offset:36864\n" \
X_"a[114:115], v33 offset:41472\n" \
M_"v[252:255], a[120:123], a[248:251], v[252:255]\n" \
M_"v[248:251], a[116:119], a[228:231], v[248:251]\n" \
X_"a[116:117], v33 offset:46080\n" \
X_"a[118:119], v33 offset:50688\n" \
"s_waitcnt vmcnt(19)\n" \
"s_mov_b64 exec, 1\n" \
"buffer_store_dword v39, off, s[56:59], s66 offset:4 sc1\n" \
"s_mov_b64 exec, -1\n" \
"v_readlane_b32 s66, v11, 50\n" \
M_"v[248:251], a[120:123], a[232:235], v[248:251]\n" \
X_"a[120:121], v33 offset:55296\n" \
X_"a[122:123], v33 offset:59904\n" \
M_"v[176:179], a[0:3], a[128:131], v[176:179]\n" \
M_"v[176:179], a[4:7], a[132:135], v[176:179]\n" \
M_"v[180:183], a[0:3], a[144:147], v[180:183]\n" \
B_"v[72:75], v32, s[56:59], s95 offen offset:0 sc1\n" \
M_"v[180:183], a[4:7], a[148:151], v[180:183]\n" \
M_"v[248:251], a[0:3], a[160:163], v[248:251]\n" \
M_"v[248:251], a[4:7], a[164:167], v[248:251]\n" \
M_"v[252:255], a[0:3], a[176:179], v[252:255]\n" \
X_"a[0:1], v10 offset:0\n" \
X_"a[2:3], v10 offset:4608\n" \
M_"v[252:255], a[4:7], a[180:183], v[252:255]\n" \
X_"a[4:5], v34 offset:0\n" \
"s_waitcnt vmcnt(20)\n" \
M_"v[252:255], a[8:11], a[184:187], v[252:255]\n" \
X_"a[6:7], v34 offset:4608\n" \
"s_waitcnt vmcnt(18) lgkmcnt(10)\n" \
M_"v[252:255], a[12:15], a[188:191], v[252:255]\n" \
B_"v[76:79], v32, s[56:59], s95 offen offset:1024 sc1\n" \
M_"v[248:251], a[8:11], a[168:171], v[248:251]\n" \
M_"v[248:251], a[12:15], a[172:175], v[248:251]\n" \
M_"v[180:183], a[8:11], a[152:155], v[180:183]\n" \
M_"v[180:183], a[12:15], a[156:159], v[180:183]\n" \
M_"v[176:179], a[8:11], a[136:139], v[176:179]\n" \
X_"a[8:9], v34 offset:9216\n" \
X_"a[10:11], v34 offset:13824\n" \
M_"v[176:179], a[12:15], a[140:143], v[176:179]\n" \
X_"a[12:13], v34 offset:18432\n" \
X_"a[14:15], v34 offset:23040\n" \
"s_waitcnt vmcnt(18)\n" \
"s_mov_b64 exec, 1\n" \
"buffer_store_dword v39, off, s[56:59], s67 offset:8 sc1\n" \
"s_mov_b64 exec, -1\n" \
"v_readlane_b32 s67, v11, 51\n" \
M_"v[176:179], a[16:19], a[32:35], v[176:179]\n" \
M_"v[176:179], a[20:23], a[36:39], v[176:179]\n" \
M_"v[180:183], a[16:19], a[48:51], v[180:183]\n" \
M_"v[180:183], a[20:23], a[52:55], v[180:183]\n" \
"s_waitcnt vmcnt(18)\n" \
M_"v[248:251], a[16:19], a[192:195], v[248:251]\n" \
"s_waitcnt vmcnt(17)\n" \
M_"v[248:251], a[20:23], a[196:199], v[248:251]\n" \
"s_waitcnt vmcnt(15)\n" \
M_"v[252:255], a[16:19], a[208:211], v[252:255]\n" \
X_"a[16:17], v10 offset:9216\n" \
"s_mov_b64 exec, 1\n" \
"buffer_store_dword v39, off, s[56:59], s68 offset:12 sc1\n" \
"s_mov_b64 exec, -1\n" \
"s_waitcnt vmcnt(15)\n" \
M_"v[252:255], a[20:23], a[212:215], v[252:255]\n" \
"v_readlane_b32 s68, v11, 52\n" \
"s_waitcnt vmcnt(14)\n" \
M_"v[252:255], a[24:27], a[216:219], v[252:255]\n" \
X_"a[18:19], v10 offset:13824\n" \
"s_waitcnt vmcnt(13)\n" \
M_"v[252:255], a[28:31], a[220:223], v[252:255]\n" \
X_"a[20:21], v33 offset:32\n" \
X_"a[22:23], v33 offset:4640\n" \
B_"v[80:83], v20, s[56:59], s96 offen offset:0 sc1\n" \
"s_waitcnt vmcnt(12)\n" \
M_"v[248:251], a[24:27], a[200:203], v[248:251]\n" \
"s_mov_b64 exec, 1\n" \
"buffer_store_dword v39, off, s[56:59], s69 offset:16 sc1\n" \
"s_mov_b64 exec, -1\n" \
"v_readlane_b32 s69, v11, 53\n" \
".fill (L_ab + 40960 - .) / 4, 4, 0xBF800000\n" \
"L_ap10:\n" \
"s_cbranch_execz L_ap0\n" \
"s_waitcnt vmcnt(12)\n" \
M_"v[248:251], a[28:31], a[204:207], v[248:251]\n" \
M_"v[180:183], a[24:27], a[56:59], v[180:183]\n" \
B_"v[84:87], v20, s[56:59], s96 offen offset:1024 sc1\n" \
M_"v[180:183], a[28:31], a[60:63], v[180:183]\n" \
M_"v[176:179], a[24:27], a[40:43], v[176:179]\n" \
X_"a[24:25], v33 offset:9248\n" \
X_"a[26:27], v33 offset:13856\n" \
M_"v[176:179], a[28:31], a[44:47], v[176:179]\n" \
X_"a[28:29], v33 offset:18464\n" \
C_"v248, v248, v249\n" \
C_"v249, v250, v251\n" \
C_"v250, v252, v253\n" \
C_"v251, v254, v255\n" \
"buffer_store_dwordx4 v[248:251], v20, s[56:59], s72 offen offset:1024 sc1\n" \
M_"v[184:187], a[96:99], a[124:127], v[184:187]\n" \
X_"a[30:31], v33 offset:23072\n" \
C_"v176, v176, v177\n" \
C_"v177, v178, v179\n" \
C_"v178, v180, v181\n" \
C_"v179, v182, v183\n" \
M_"v[184:187], a[100:103], a[224:227], v[184:187]\n" \
"buffer_store_dwordx4 v[176:179], v20, s[56:59], s72 offen offset:0 sc1\n" \
M_"v[188:191], a[96:99], a[236:239], v[188:191]\n" \
X_"a[96:97], v33 offset:27680\n" \
X_"a[98:99], v33 offset:32288\n" \
M_"v[188:191], a[100:103], a[240:243], v[188:191]\n" \
X_"a[100:101], v33 offset:36896\n" \
X_"a[102:103], v33 offset:41504\n" \
M_"v[188:191], a[104:107], a[244:247], v[188:191]\n" \
M_"v[188:191], a[108:111], a[248:251], v[188:191]\n" \
M_"v[184:187], a[104:107], a[228:231], v[184:187]\n" \
X_"a[104:105], v33 offset:46112\n" \
X_"a[106:107], v33 offset:50720\n" \
M_"v[184:187], a[108:111], a[232:235], v[184:187]\n" \
X_"a[108:109], v33 offset:55328\n" \
"s_waitcnt lgkmcnt(11)\n" \
M_"v[184:187], a[112:115], a[160:163], v[184:187]\n" \
B_"v[88:91], v32, s[56:59], s96 offen offset:0 sc1\n" \
M_"v[184:187], a[116:119], a[164:167], v[184:187]\n" \
X_"a[110:111], v33 offset:59936\n" \
M_"v[188:191], a[112:115], a[176:179], v[188:191]\n" \
X_"a[112:113], v10 offset:32\n" \
X_"a[114:115], v10 offset:4640\n" \
M_"v[188:191], a[116:119], a[180:183], v[188:191]\n" \
X_"a[116:117], v34 offset:32\n" \
X_"a[118:119], v34 offset:4640\n" \
M_"v[188:191], a[120:123], a[184:187], v[188:191]\n" \
M_"v[188:191], a[0:3], a[188:191], v[188:191]\n" \
B_"v[92:95], v32, s[56:59], s96 offen offset:1024 sc1\n" \
M_"v[184:187], a[120:123], a[168:171], v[184:187]\n" \
X_"a[120:121], v34 offset:9248\n" \
X_"a[122:123], v34 offset:13856\n" \
M_"v[184:187], a[0:3], a[172:175], v[184:187]\n" \
X_"a[0:1], v34 offset:18464\n" \
X_"a[2:3], v34 offset:23072\n" \
M_"v[184:187], a[4:7], a[192:195], v[184:187]\n" \
M_"v[184:187], a[8:11], a[196:199], v[184:187]\n" \
M_"v[188:191], a[4:7], a[208:211], v[188:191]\n" \
X_"a[4:5], v10 offset:9248\n" \
X_"a[6:7], v10 offset:13856\n" \
M_"v[188:191], a[8:11], a[212:215], v[188:191]\n" \
X_"a[8:9], v33 offset:64\n" \
X_"a[10:11], v33 offset:4672\n" \
M_"v[188:191], a[12:15], a[216:219], v[188:191]\n" \
B_"v[96:99], v20, s[56:59], s97 offen offset:0 sc1\n" \
M_"v[188:191], a[16:19], a[220:223], v[188:191]\n" \
M_"v[184:187], a[12:15], a[200:203], v[184:187]\n" \
X_"a[12:13], v33 offset:9280\n" \
X_"a[14:15], v33 offset:13888\n" \
M_"v[184:187], a[16:19], a[204:207], v[184:187]\n" \
"buffer_load_dword v37, v40, s[56:59], s74 offen sc1\n" \
M_"v[192:195], a[20:23], a[124:127], v[192:195]\n" \
X_"a[16:17], v33 offset:18496\n" \
"s_waitcnt lgkmcnt(13)\n" \
M_"v[192:195], a[24:27], a[224:227], v[192:195]\n" \
X_"a[18:19], v33 offset:23104\n" \
M_"v[196:199], a[20:23], a[236:239], v[196:199]\n" \
X_"a[20:21], v33 offset:27712\n" \
C_"v184, v184, v185\n" \
C_"v185, v186, v187\n" \
C_"v186, v188, v189\n" \
C_"v187, v190, v191\n" \
"buffer_store_dwordx4 v[184:187], v20, s[56:59], s92 offen offset:1024 sc1\n" \
"s_waitcnt vmcnt(18)\n" \
"s_mov_b64 exec, 1\n" \
"buffer_store_dword v39, off, s[56:59], s70 offset:20 sc1\n" \
"s_mov_b64 exec, -1\n" \
"s_add_u32 s92, s92, 0x3800\n" \
M_"v[196:199], a[24:27], a[240:243], v[196:199]\n" \
X_"a[22:23], v33 offset:32320\n" \
X_"a[24:25], v33 offset:36928\n" \
M_"v[196:199], a[28:31], a[244:247], v[196:199]\n" \
X_"a[26:27], v33 offset:41536\n" \
M_"v[196:199], a[96:99], a[248:251], v[196:199]\n" \
M_"v[192:195], a[28:31], a[228:231], v[192:195]\n" \
X_"a[28:29], v33 offset:46144\n" \
X_"a[30:31], v33 offset:50752\n" \
M_"v[192:195], a[96:99], a[232:235], v[192:195]\n" \
B_"v[100:103], v20, s[56:59], s97 offen offset:1024 sc1\n" \
M_"v[192:195], a[100:103], a[160:163], v[192:195]\n" \
X_"a[96:97], v33 offset:55360\n" \
X_"a[98:99], v33 offset:59968\n" \
M_"v[192:195], a[104:107], a[164:167], v[192:195]\n" \
M_"v[196:199], a[100:103], a[176:179], v[196:199]\n" \
X_"a[100:101], v10 offset:64\n" \
X_"a[102:103], v10 offset:4672\n" \
M_"v[196:199], a[104:107], a[180:183], v[196:199]\n" \
X_"a[104:105], v34 offset:64\n" \
X_"a[106:107], v34 offset:4672\n" \
M_"v[196:199], a[108:111], a[184:187], v[196:199]\n" \
B_"v[104:107], v32, s[56:59], s97 offen offset:0 sc1\n" \
M_"v[196:199], a[112:115], a[188:191], v[196:199]\n" \
M_"v[192:195], a[108:111], a[168:171], v[192:195]\n" \
X_"a[108:109], v34 offset:9280\n" \
X_"a[110:111], v34 offset:13888\n" \
M_"v[192:195], a[112:115], a[172:175], v[192:195]\n" \
X_"a[112:113], v34 offset:18496\n" \
"s_waitcnt lgkmcnt(14)\n" \
M_"v[192:195], a[116:119], a[192:195], v[192:195]\n" \
X_"a[114:115], v34 offset:23104\n" \
M_"v[192:195], a[120:123], a[196:199], v[192:195]\n" \
B_"v[108:111], v32, s[56:59], s97 offen offset:1024 sc1\n" \
M_"v[196:199], a[116:119], a[208:211], v[196:199]\n" \
X_"a[116:117], v10 offset:9280\n" \
X_"a[118:119], v10 offset:13888\n" \
M_"v[196:199], a[120:123], a[212:215], v[196:199]\n" \
X_"a[120:121], v33 offset:96\n" \
X_"a[122:123], v33 offset:4704\n" \
"s_waitcnt vmcnt(19)\n" \
"s_mov_b64 exec, 1\n" \
"buffer_store_dword v39, off, s[56:59], s71 offset:24 sc1\n" \
"s_mov_b64 exec, -1\n" \
M_"v[196:199], a[0:3], a[216:219], v[196:199]\n" \
M_"v[196:199], a[4:7], a[220:223], v[196:199]\n" \
M_"v[192:195], a[0:3], a[200:203], v[192:195]\n" \
X_"a[0:1], v33 offset:9312\n" \
X_"a[2:3], v33 offset:13920\n" \
M_"v[192:195], a[4:7], a[204:207], v[192:195]\n" \
X_"a[4:5], v33 offset:18528\n" \
X_"a[6:7], v33 offset:23136\n" \
M_"v[200:203], a[8:11], a[124:127], v[200:203]\n" \
M_"v[200:203], a[12:15], a[224:227], v[200:203]\n" \
M_"v[204:207], a[8:11], a[236:239], v[204:207]\n" \
X_"a[8:9], v33 offset:27744\n" \
X_"a[10:11], v33 offset:32352\n" \
M_"v[204:207], a[12:15], a[240:243], v[204:207]\n" \
C_"v192, v192, v193\n" \
C_"v193, v194, v195\n" \
C_"v194, v196, v197\n" \
C_"v195, v198, v199\n" \
"buffer_store_dwordx4 v[192:195], v20, s[56:59], s66 offen offset:1024 sc1\n" \
M_"v[204:207], a[16:19], a[244:247], v[204:207]\n" \
X_"a[12:13], v33 offset:36960\n" \
"s_waitcnt lgkmcnt(11)\n" \
M_"v[204:207], a[20:23], a[248:251], v[204:207]\n" \
X_"a[14:15], v33 offset:41568\n" \
M_"v[200:203], a[16:19], a[228:231], v[200:203]\n" \
X_"a[16:17], v33 offset:46176\n" \
X_"a[18:19], v33 offset:50784\n" \
M_"v[200:203], a[20:23], a[232:235], v[200:203]\n" \
X_"a[20:21], v33 offset:55392\n" \
X_"a[22:23], v33 offset:60000\n" \
M_"v[200:203], a[24:27], a[160:163], v[200:203]\n" \
M_"v[200:203], a[28:31], a[164:167], v[200:203]\n" \
M_"v[204:207], a[24:27], a[176:179], v[204:207]\n" \
X_"a[24:25], v10 offset:96\n" \
X_"a[26:27], v10 offset:4704\n" \
M_"v[204:207], a[28:31], a[180:183], v[204:207]\n" \
X_"a[28:29], v34 offset:96\n" \
X_"a[30:31], v34 offset:4704\n" \
M_"v[204:207], a[96:99], a[184:187], v[204:207]\n" \
M_"v[204:207], a[100:103], a[188:191], v[204:207]\n" \
M_"v[200:203], a[96:99], a[168:171], v[200:203]\n" \
X_"a[96:97], v34 offset:9312\n" \
X_"a[98:99], v34 offset:13920\n" \
M_"v[200:203], a[100:103], a[172:175], v[200:203]\n" \
X_"a[100:101], v34 offset:18528\n" \
X_"a[102:103], v34 offset:23136\n" \
M_"v[200:203], a[104:107], a[192:195], v[200:203]\n" \
M_"v[200:203], a[108:111], a[196:199], v[200:203]\n" \
M_"v[204:207], a[104:107], a[208:211], v[204:207]\n" \
X_"a[104:105], v10 offset:9312\n" \
X_"a[106:107], v10 offset:13920\n" \
M_"v[204:207], a[108:111], a[212:215], v[204:207]\n" \
X_"a[108:109], v33 offset:128\n" \
X_"a[110:111], v33 offset:4736\n" \
M_"v[204:207], a[112:115], a[216:219], v[204:207]\n" \
"s_waitcnt lgkmcnt(15)\n" \
M_"v[204:207], a[116:119], a[220:223], v[204:207]\n" \
M_"v[200:203], a[112:115], a[200:203], v[200:203]\n" \
X_"a[112:113], v33 offset:9344\n" \
X_"a[114:115], v33 offset:13952\n" \
M_"v[200:203], a[116:119], a[204:207], v[200:203]\n" \
X_"a[116:117], v33 offset:18560\n" \
X_"a[118:119], v33 offset:23168\n" \
M_"v[208:211], a[120:123], a[124:127], v[208:211]\n" \
M_"v[208:211], a[0:3], a[224:227], v[208:211]\n" \
M_"v[212:215], a[120:123], a[236:239], v[212:215]\n" \
X_"a[120:121], v33 offset:27776\n" \
X_"a[122:123], v33 offset:32384\n" \
M_"v[212:215], a[0:3], a[240:243], v[212:215]\n" \
C_"v200, v200, v201\n" \
C_"v201, v202, v203\n" \
C_"v202, v204, v205\n" \
C_"v203, v206, v207\n" \
"buffer_store_dwordx4 v[200:203], v20, s[56:59], s67 offen offset:1024 sc1\n" \
M_"v[212:215], a[4:7], a[244:247], v[212:215]\n" \
X_"a[0:1], v33 offset:36992\n" \
X_"a[2:3], v33 offset:41600\n" \
M_"v[212:215], a[8:11], a[248:251], v[212:215]\n" \
M_"v[208:211], a[4:7], a[228:231], v[208:211]\n" \
X_"a[4:5], v33 offset:46208\n" \
X_"a[6:7], v33 offset:50816\n" \
M_"v[208:211], a[8:11], a[232:235], v[208:211]\n" \
X_"a[8:9], v33 offset:55424\n" \
X_"a[10:11], v33 offset:60032\n" \
M_"v[208:211], a[12:15], a[160:163], v[208:211]\n" \
"s_waitcnt lgkmcnt(13)\n" \
M_"v[208:211], a[16:19], a[164:167], v[208:211]\n" \
M_"v[212:215], a[12:15], a[176:179], v[212:215]\n" \
"s_waitcnt vmcnt(8)\n" \
"v_cmp_eq_u32_e64 s[98:99], s62, v37\n" \
"s_mov_b64 exec, 3\n" \
"buffer_store_dword v39, v41, s[56:59], s73 offen sc1\n" \
"s_mov_b64 exec, -1\n" \
"s_andn2_b32 s75, 0x7f, s98\n" \
"s_mov_b32 s20, s75\n" \
"s_bitcmp1_b32 s20, 0\n" \
"s_cselect_b32 s93, 0x40000000, s12\n" \
"s_bitcmp1_b32 s20, 1\n" \
"s_cselect_b32 s94, 0x40000000, s12\n" \
"s_bitcmp1_b32 s20, 2\n" \
"s_cselect_b32 s95, 0x40000000, s13\n" \
"s_bitcmp1_b32 s20, 3\n" \
"s_cselect_b32 s96, 0x40000000, s13\n" \
"s_bitcmp1_b32 s20, 4\n" \
"s_cselect_b32 s97, 0x40000000, s14\n" \
"s_bitcmp1_b32 s20, 5\n" \
"s_cselect_b32 s64, 0x40000000, s14\n" \
"s_bitcmp1_b32 s20, 6\n" \
"s_cselect_b32 s65, 0x40000000, s15\n" \
"s_add_u32 s66, s66, 0x3000\n" \
"s_add_u32 s67, s67, 0x2800\n" \
M_"v[212:215], a[16:19], a[180:183], v[212:215]\n" \
B_"v[116:119], v20, s[56:59], s93 offen offset:0 sc1\n" \
M_"v[212:215], a[20:23], a[184:187], v[212:215]\n" \
X_"a[12:13], v10 offset:128\n" \
X_"a[14:15], v10 offset:4736\n" \
M_"v[212:215], a[24:27], a[188:191], v[212:215]\n" \
X_"a[16:17], v34 offset:128\n" \
X_"a[18:19], v34 offset:4736\n" \
M_"v[208:211], a[20:23], a[168:171], v[208:211]\n" \
X_"a[20:21], v34 offset:9344\n" \
X_"a[22:23], v34 offset:13952\n" \
M_"v[208:211], a[24:27], a[172:175], v[208:211]\n" \
X_"a[24:25], v34 offset:18560\n" \
X_"a[26:27], v34 offset:23168\n" \
M_"v[208:211], a[28:31], a[192:195], v[208:211]\n" \
M_"v[208:211], a[96:99], a[196:199], v[208:211]\n" \
M_"v[212:215], a[28:31], a[208:211], v[212:215]\n" \
X_"a[28:29], v10 offset:9344\n" \
X_"a[30:31], v10 offset:13952\n" \
M_"v[212:215], a[96:99], a[212:215], v[212:215]\n" \
X_"a[96:97], v33 offset:160\n" \
X_"a[98:99], v33 offset:4768\n" \
M_"v[212:215], a[100:103], a[216:219], v[212:215]\n" \
M_"v[212:215], a[104:107], a[220:223], v[212:215]\n" \
M_"v[208:211], a[100:103], a[200:203], v[208:211]\n" \
X_"a[100:101], v33 offset:9376\n" \
X_"a[102:103], v33 offset:13984\n" \
M_"v[208:211], a[104:107], a[204:207], v[208:211]\n" \
X_"a[104:105], v33 offset:18592\n" \
"s_waitcnt lgkmcnt(15)\n" \
M_"v[216:219], a[108:111], a[124:127], v[216:219]\n" \
X_"a[106:107], v33 offset:23200\n" \
M_"v[216:219], a[112:115], a[224:227], v[216:219]\n" \
B_"v[124:127], v20, s[56:59], s94 offen offset:2048 sc1\n" \
M_"v[220:223], a[108:111], a[236:239], v[220:223]\n" \
X_"a[108:109], v33 offset:27808\n" \
C_"v208, v208, v209\n" \
C_"v209, v210, v211\n" \
C_"v210, v212, v213\n" \
C_"v211, v214, v215\n" \
M_"v[220:223], a[112:115], a[240:243], v[220:223]\n" \
"buffer_store_dwordx4 v[208:211], v20, s[56:59], s68 offen offset:1024 sc1\n" \
"s_add_u32 s68, s68, 0x2000\n" \
M_"v[220:223], a[116:119], a[244:247], v[220:223]\n" \
X_"a[110:111], v33 offset:32416\n" \
X_"a[112:113], v33 offset:37024\n" \
M_"v[220:223], a[120:123], a[248:251], v[220:223]\n" \
X_"a[114:115], v33 offset:41632\n" \
M_"v[216:219], a[116:119], a[228:231], v[216:219]\n" \
X_"a[116:117], v33 offset:46240\n" \
X_"a[118:119], v33 offset:50848\n" \
M_"v[216:219], a[120:123], a[232:235], v[216:219]\n" \
X_"a[120:121], v33 offset:55456\n" \
X_"a[122:123], v33 offset:60064\n" \
M_"v[216:219], a[0:3], a[160:163], v[216:219]\n" \
M_"v[216:219], a[4:7], a[164:167], v[216:219]\n" \
M_"v[220:223], a[0:3], a[176:179], v[220:223]\n" \
X_"a[0:1], v10 offset:160\n" \
X_"a[2:3], v10 offset:4768\n" \
M_"v[220:223], a[4:7], a[180:183], v[220:223]\n" \
X_"a[4:5], v34 offset:160\n" \
X_"a[6:7], v34 offset:4768\n" \
M_"v[220:223], a[8:11], a[184:187], v[220:223]\n" \
"s_waitcnt lgkmcnt(13)\n" \
M_"v[220:223], a[12:15], a[188:191], v[220:223]\n" \
"s_waitcnt vmcnt(11)\n" \
"s_mov_b64 exec, 1\n" \
"buffer_store_dword v39, off, s[56:59], s92 offset:32 sc1\n" \
"s_mov_b64 exec, -1\n" \
M_"v[216:219], a[8:11], a[168:171], v[216:219]\n" \
X_"a[8:9], v34 offset:9376\n" \
X_"a[10:11], v34 offset:13984\n" \
M_"v[216:219], a[12:15], a[172:175], v[216:219]\n" \
X_"a[12:13], v34 offset:18592\n" \
X_"a[14:15], v34 offset:23200\n" \
M_"v[216:219], a[16:19], a[192:195], v[216:219]\n" \
M_"v[216:219], a[20:23], a[196:199], v[216:219]\n" \
B_"v[132:135], v20, s[56:59], s95 offen offset:0 sc1\n" \
M_"v[220:223], a[16:19], a[208:211], v[220:223]\n" \
X_"a[16:17], v10 offset:9376\n" \
X_"a[18:19], v10 offset:13984\n" \
M_"v[220:223], a[20:23], a[212:215], v[220:223]\n" \
X_"a[20:21], v33 offset:192\n" \
X_"a[22:23], v33 offset:4800\n" \
M_"v[220:223], a[24:27], a[216:219], v[220:223]\n" \
M_"v[220:223], a[28:31], a[220:223], v[220:223]\n" \
M_"v[216:219], a[24:27], a[200:203], v[216:219]\n" \
X_"a[24:25], v33 offset:9408\n" \
X_"a[26:27], v33 offset:14016\n" \
M_"v[216:219], a[28:31], a[204:207], v[216:219]\n" \
"buffer_load_dword v37, v40, s[56:59], s74 offen sc1\n" \
M_"v[224:227], a[96:99], a[124:127], v[224:227]\n" \
X_"a[28:29], v33 offset:18624\n" \
X_"a[30:31], v33 offset:23232\n" \
M_"v[224:227], a[100:103], a[224:227], v[224:227]\n" \
M_"v[228:231], a[96:99], a[236:239], v[228:231]\n" \
X_"a[96:97], v33 offset:27840\n" \
X_"a[98:99], v33 offset:32448\n" \
C_"v216, v216, v217\n" \
C_"v217, v218, v219\n" \
C_"v218, v220, v221\n" \
C_"v219, v222, v223\n" \
"buffer_store_dwordx4 v[216:219], v20, s[56:59], s69 offen offset:1024 sc1\n" \
"s_add_u32 s69, s69, 0x1800\n" \
M_"v[228:231], a[100:103], a[240:243], v[228:231]\n" \
X_"a[100:101], v33 offset:37056\n" \
"s_waitcnt lgkmcnt(12)\n" \
M_"v[228:231], a[104:107], a[244:247], v[228:231]\n" \
X_"a[102:103], v33 offset:41664\n" \
M_"v[228:231], a[108:111], a[248:251], v[228:231]\n" \
M_"v[224:227], a[104:107], a[228:231], v[224:227]\n" \
B_"v[140:143], v20, s[56:59], s96 offen offset:2048 sc1\n" \
M_"v[224:227], a[108:111], a[232:235], v[224:227]\n" \
X_"a[104:105], v33 offset:46272\n" \
X_"a[106:107], v33 offset:50880\n" \
M_"v[224:227], a[112:115], a[160:163], v[224:227]\n" \
X_"a[108:109], v33 offset:55488\n" \
X_"a[110:111], v33 offset:60096\n" \
M_"v[224:227], a[116:119], a[164:167], v[224:227]\n" \
M_"v[228:231], a[112:115], a[176:179], v[228:231]\n" \
X_"a[112:113], v10 offset:192\n" \
X_"a[114:115], v10 offset:4800\n" \
M_"v[228:231], a[116:119], a[180:183], v[228:231]\n" \
X_"a[116:117], v34 offset:192\n" \
X_"a[118:119], v34 offset:4800\n" \
M_"v[228:231], a[120:123], a[184:187], v[228:231]\n" \
M_"v[228:231], a[0:3], a[188:191], v[228:231]\n" \
M_"v[224:227], a[120:123], a[168:171], v[224:227]\n" \
X_"a[120:121], v34 offset:9408\n" \
X_"a[122:123], v34 offset:14016\n" \
M_"v[224:227], a[0:3], a[172:175], v[224:227]\n" \
X_"a[0:1], v34 offset:18624\n" \
X_"a[2:3], v34 offset:23232\n" \
"s_waitcnt vmcnt(10)\n" \
"s_mov_b64 exec, 1\n" \
"buffer_store_dword v39, off, s[56:59], s66 offset:36 sc1\n" \
"s_mov_b64 exec, -1\n" \
M_"v[224:227], a[4:7], a[192:195], v[224:227]\n" \
M_"v[224:227], a[8:11], a[196:199], v[224:227]\n" \
M_"v[228:231], a[4:7], a[208:211], v[228:231]\n" \
X_"a[4:5], v10 offset:9408\n" \
X_"a[6:7], v10 offset:14016\n" \
M_"v[228:231], a[8:11], a[212:215], v[228:231]\n" \
X_"a[8:9], v33 offset:224\n" \
"s_waitcnt lgkmcnt(15)\n" \
M_"v[228:231], a[12:15], a[216:219], v[228:231]\n" \
B_"v[148:151], v20, s[56:59], s97 offen offset:0 sc1\n" \
M_"v[228:231], a[16:19], a[220:223], v[228:231]\n" \
X_"a[10:11], v33 offset:4832\n" \
M_"v[224:227], a[12:15], a[200:203], v[224:227]\n" \
X_"a[12:13], v33 offset:9440\n" \
X_"a[14:15], v33 offset:14048\n" \
M_"v[224:227], a[16:19], a[204:207], v[224:227]\n" \
X_"a[16:17], v33 offset:18656\n" \
X_"a[18:19], v33 offset:23264\n" \
M_"v[232:235], a[20:23], a[124:127], v[232:235]\n" \
M_"v[232:235], a[24:27], a[224:227], v[232:235]\n" \
M_"v[236:239], a[20:23], a[236:239], v[236:239]\n" \
X_"a[20:21], v33 offset:27872\n" \
X_"a[22:23], v33 offset:32480\n" \
M_"v[236:239], a[24:27], a[240:243], v[236:239]\n" \
C_"v224, v224, v225\n" \
C_"v225, v226, v227\n" \
C_"v226, v228, v229\n" \
C_"v227, v230, v231\n" \
M_"v[236:239], a[28:31], a[244:247], v[236:239]\n" \
X_"a[24:25], v33 offset:37088\n" \
X_"a[26:27], v33 offset:41696\n" \
M_"v[236:239], a[96:99], a[248:251], v[236:239]\n" \
M_"v[232:235], a[28:31], a[228:231], v[232:235]\n" \
X_"a[28:29], v33 offset:46304\n" \
X_"a[30:31], v33 offset:50912\n" \
M_"v[232:235], a[96:99], a[232:235], v[232:235]\n" \
X_"a[96:97], v33 offset:55520\n" \
X_"a[98:99], v33 offset:60128\n" \
M_"v[232:235], a[100:103], a[160:163], v[232:235]\n" \
"s_waitcnt lgkmcnt(14)\n" \
M_"v[232:235], a[104:107], a[164:167], v[232:235]\n" \
B_"v[156:159], v20, s[56:59], s64 offen offset:2048 sc1\n" \
"v_readlane_b32 s64, v11, 54\n" \
M_"v[236:239], a[100:103], a[176:179], v[236:239]\n" \
X_"a[100:101], v10 offset:224\n" \
X_"a[102:103], v10 offset:4832\n" \
M_"v[236:239], a[104:107], a[180:183], v[236:239]\n" \
X_"a[104:105], v34 offset:224\n" \
X_"a[106:107], v34 offset:4832\n" \
"buffer_store_dwordx4 v[224:227], v20, s[56:59], s64 offen offset:1024 sc1\n" \
M_"v[236:239], a[108:111], a[184:187], v[236:239]\n" \
M_"v[236:239], a[112:115], a[188:191], v[236:239]\n" \
M_"v[232:235], a[108:111], a[168:171], v[232:235]\n" \
X_"a[108:109], v34 offset:9440\n" \
X_"a[110:111], v34 offset:14048\n" \
"s_waitcnt vmcnt(13)\n" \
"s_mov_b64 exec, 1\n" \
"buffer_store_dword v39, off, s[56:59], s67 offset:40 sc1\n" \
"s_mov_b64 exec, -1\n" \
M_"v[232:235], a[112:115], a[172:175], v[232:235]\n" \
X_"a[112:113], v34 offset:18656\n" \
X_"a[114:115], v34 offset:23264\n" \
M_"v[232:235], a[116:119], a[192:195], v[232:235]\n" \
M_"v[232:235], a[120:123], a[196:199], v[232:235]\n" \
M_"v[236:239], a[116:119], a[208:211], v[236:239]\n" \
X_"a[116:117], v10 offset:9440\n" \
X_"a[118:119], v10 offset:14048\n" \
M_"v[236:239], a[120:123], a[212:215], v[236:239]\n" \
X_"a[120:121], v36 offset:0\n" \
X_"a[122:123], v36 offset:1024\n" \
M_"v[236:239], a[0:3], a[216:219], v[236:239]\n" \
M_"v[236:239], a[4:7], a[220:223], v[236:239]\n" \
M_"v[232:235], a[0:3], a[200:203], v[232:235]\n" \
X_"a[0:1], v36 offset:2048\n" \
X_"a[2:3], v36 offset:3072\n" \
M_"v[232:235], a[4:7], a[204:207], v[232:235]\n" \
X_"a[4:5], v36 offset:32\n" \
B_"v[164:167], v20, s[56:59], s65 offen offset:0 sc1\n" \
"v_readlane_b32 s65, v11, 55\n" \
"s_waitcnt lgkmcnt(11)\n" \
M_"v[240:243], a[8:11], a[124:127], v[240:243]\n" \
X_"a[6:7], v36 offset:1056\n" \
M_"v[240:243], a[12:15], a[224:227], v[240:243]\n" \
M_"v[244:247], a[8:11], a[236:239], v[244:247]\n" \
C_"v232, v232, v233\n" \
C_"v233, v234, v235\n" \
C_"v234, v236, v237\n" \
C_"v235, v238, v239\n" \
"buffer_store_dwordx4 v[232:235], v20, s[56:59], s65 offen offset:1024 sc1\n" \
M_"v[244:247], a[12:15], a[240:243], v[244:247]\n" \
X_"a[8:9], v36 offset:2080\n" \
X_"a[10:11], v36 offset:3104\n" \
M_"v[244:247], a[16:19], a[244:247], v[244:247]\n" \
M_"v[244:247], a[20:23], a[248:251], v[244:247]\n" \
M_"v[240:243], a[16:19], a[228:231], v[240:243]\n" \
M_"v[240:243], a[20:23], a[232:235], v[240:243]\n" \
M_"v[168:171], a[24:27], a[128:131], v[168:171]\n" \
M_"v[168:171], a[28:31], a[132:135], v[168:171]\n" \
M_"v[172:175], a[24:27], a[144:147], v[172:175]\n" \
"s_waitcnt vmcnt(9)\n" \
"v_cmp_eq_u32_e64 s[98:99], s62, v37\n" \
"buffer_load_dword v37, v40, s[56:59], s74 offen sc1\n" \
"s_mov_b64 exec, 1\n" \
"buffer_store_dword v39, off, s[56:59], s68 offset:44 sc1\n" \
"s_mov_b64 exec, -1\n" \
"s_or_b32 s75, 0x300, s100\n" \
"s_andn2_b32 s75, s75, s98\n" \
"s_mov_b32 s21, s75\n" \
"s_bitcmp1_b32 s21, 16\n" \
"s_cselect_b32 s70, 0x40000000, s16\n" \
"s_bitcmp1_b32 s21, 17\n" \
"s_cselect_b32 s71, 0x40000000, s16\n" \
"s_bitcmp1_b32 s21, 18\n" \
"s_cselect_b32 s72, 0x40000000, s17\n" \
"s_bitcmp1_b32 s21, 19\n" \
"s_cselect_b32 s73, 0x40000000, s17\n" \
"s_bitcmp1_b32 s21, 20\n" \
"s_cselect_b32 s92, 0x40000000, s18\n" \
"s_bitcmp1_b32 s21, 21\n" \
"s_cselect_b32 s93, 0x40000000, s18\n" \
"s_bitcmp1_b32 s21, 22\n" \
"s_cselect_b32 s94, 0x40000000, s19\n" \
"s_bitcmp1_b32 s21, 23\n" \
"s_cselect_b32 s95, 0x40000000, s19\n" \
"s_bitcmp1_b32 s21, 8\n" \
"s_cselect_b32 s96, 0x40000000, s12\n" \
"s_bitcmp1_b32 s21, 9\n" \
"s_cselect_b32 s97, 0x40000000, s12\n" \
"s_add_u32 s64, s64, 0x1000\n" \
"s_add_u32 s65, s65, 0x800\n" \
M_"v[172:175], a[28:31], a[148:151], v[172:175]\n" \
B_"v[112:115], v20, s[56:59], s70 offen offset:0 sc1\n" \
M_"v[240:243], a[24:27], a[160:163], v[240:243]\n" \
M_"v[240:243], a[28:31], a[164:167], v[240:243]\n" \
M_"v[244:247], a[24:27], a[176:179], v[244:247]\n" \
M_"v[244:247], a[28:31], a[180:183], v[244:247]\n" \
B_"v[184:187], v20, s[56:59], s71 offen offset:2048 sc1\n" \
M_"v[244:247], a[96:99], a[184:187], v[244:247]\n" \
"v_readlane_b32 s71, v11, 57\n" \
M_"v[244:247], a[100:103], a[188:191], v[244:247]\n" \
M_"v[240:243], a[96:99], a[168:171], v[240:243]\n" \
M_"v[240:243], a[100:103], a[172:175], v[240:243]\n" \
B_"v[120:123], v20, s[56:59], s72 offen offset:0 sc1\n" \
M_"v[172:175], a[96:99], a[152:155], v[172:175]\n" \
"v_readlane_b32 s72, v11, 48\n" \
M_"v[172:175], a[100:103], a[156:159], v[172:175]\n" \
M_"v[168:171], a[96:99], a[136:139], v[168:171]\n" \
M_"v[168:171], a[100:103], a[140:143], v[168:171]\n" \
B_"v[192:195], v20, s[56:59], s73 offen offset:2048 sc1\n" \
M_"v[168:171], a[104:107], a[32:35], v[168:171]\n" \
"v_readlane_b32 s73, v11, 46\n" \
"s_waitcnt lgkmcnt(0)\n" \
M_"v[168:171], a[108:111], a[36:39], v[168:171]\n" \
M_"v[172:175], a[104:107], a[48:51], v[172:175]\n" \
M_"v[172:175], a[108:111], a[52:55], v[172:175]\n" \
B_"v[128:131], v20, s[56:59], s92 offen offset:0 sc1\n" \
M_"v[240:243], a[104:107], a[192:195], v[240:243]\n" \
"v_readlane_b32 s92, v11, 47\n" \
M_"v[240:243], a[108:111], a[196:199], v[240:243]\n" \
M_"v[244:247], a[104:107], a[208:211], v[244:247]\n" \
M_"v[244:247], a[108:111], a[212:215], v[244:247]\n" \
B_"v[200:203], v20, s[56:59], s93 offen offset:2048 sc1\n" \
M_"v[244:247], a[112:115], a[216:219], v[244:247]\n" \
M_"v[244:247], a[116:119], a[220:223], v[244:247]\n" \
M_"v[240:243], a[112:115], a[200:203], v[240:243]\n" \
M_"v[240:243], a[116:119], a[204:207], v[240:243]\n" \
".fill (L_ab + 45056 - .) / 4, 4, 0xBF800000\n" \
"L_ap11:\n" \
"s_cbranch_execz L_ap0\n" \
B_"v[176:179], v20, s[56:59], s94 offen offset:0 sc1\n" \
"s_waitcnt vmcnt(17)\n" \
"s_mov_b64 exec, 1\n" \
"buffer_store_dword v39, off, s[56:59], s69 offset:48 sc1\n" \
"s_mov_b64 exec, -1\n" \
M_"v[172:175], a[112:115], a[56:59], v[172:175]\n" \
M_"v[172:175], a[116:119], a[60:63], v[172:175]\n" \
M_"v[168:171], a[112:115], a[40:43], v[168:171]\n" \
M_"v[168:171], a[116:119], a[44:47], v[168:171]\n" \
B_"v[248:251], v20, s[56:59], s95 offen offset:2048 sc1\n" \
M_"v[168:171], a[120:123], v[64:67], v[168:171]\n" \
M_"v[172:175], a[120:123], v[72:75], v[172:175]\n" \
M_"v[240:243], a[120:123], v[80:83], v[240:243]\n" \
M_"v[244:247], a[120:123], v[88:91], v[244:247]\n" \
B_"v[188:191], v20, s[56:59], s96 offen offset:1024 sc1\n" \
M_"v[168:171], a[0:3], v[68:71], v[168:171]\n" \
M_"v[172:175], a[0:3], v[76:79], v[172:175]\n" \
M_"v[240:243], a[0:3], v[84:87], v[240:243]\n" \
M_"v[244:247], a[0:3], v[92:95], v[244:247]\n" \
B_"v[196:199], v20, s[56:59], s97 offen offset:3072 sc1\n" \
M_"v[56:59], a[4:7], v[96:99], 0\n" \
M_"v[60:63], a[4:7], v[104:107], 0\n" \
M_"v[56:59], a[8:11], v[100:103], v[56:59]\n" \
M_"v[60:63], a[8:11], v[108:111], v[60:63]\n" \
"s_waitcnt vmcnt(16)\n" \
"s_mov_b64 exec, 1\n" \
"buffer_store_dword v39, off, s[56:59], s64 offset:52 sc1\n" \
"s_mov_b64 exec, -1\n" \
"s_waitcnt vmcnt(14)\n" \
"s_mov_b64 exec, 1\n" \
"buffer_store_dword v39, off, s[56:59], s65 offset:56 sc1\n" \
"s_mov_b64 exec, -1\n" \
"s_cmp_lg_u32 s20, 0\n" \
"s_cbranch_scc1 L_sna_%=\n" \
"L_rna_%=:\n" \
"s_cmp_lg_u32 s21, 0\n" \
"s_cbranch_scc1 L_snx_%=\n" \
"L_rnx_%=:\n" \
"s_waitcnt vmcnt(14)\n" \
"v_cmp_eq_u32_e64 s[98:99], s62, v37\n" \
M_"v[168:171], v[50:53], v[116:119], v[168:171]\n" \
"s_andn2_b32 s75, 0x400, s98\n" \
"s_cbranch_scc1 L_sx0_%=\n" \
"L_rx0_%=:\n" \
"s_add_u32 s66, s63, 0x1000\n" \
M_"v[172:175], v[48:51], v[116:119], v[172:175]\n" \
B_"v[204:207], v20, s[56:59], s66 offen offset:1024 sc1\n" \
"s_andn2_b32 s75, 0x800, s98\n" \
"s_cbranch_scc1 L_sx1_%=\n" \
"L_rx1_%=:\n" \
"s_add_u32 s67, s63, 0x1800\n" \
M_"v[168:171], v[50:53], v[124:127], v[168:171]\n" \
M_"v[172:175], v[48:51], v[124:127], v[172:175]\n" \
M_"v[168:171], v[50:53], v[132:135], v[168:171]\n" \
B_"v[212:215], v20, s[56:59], s67 offen offset:1024 sc1\n" \
"s_andn2_b32 s75, 0x1000, s98\n" \
"s_cbranch_scc1 L_sx2_%=\n" \
"L_rx2_%=:\n" \
"s_add_u32 s68, s63, 0x2000\n" \
M_"v[172:175], v[48:51], v[132:135], v[172:175]\n" \
M_"v[168:171], v[50:53], v[140:143], v[168:171]\n" \
M_"v[172:175], v[48:51], v[140:143], v[172:175]\n" \
B_"v[220:223], v20, s[56:59], s68 offen offset:1024 sc1\n" \
"s_andn2_b32 s75, 0x2000, s98\n" \
"s_cbranch_scc1 L_sx3_%=\n" \
"L_rx3_%=:\n" \
"s_add_u32 s69, s63, 0x2800\n" \
M_"v[168:171], v[50:53], v[148:151], v[168:171]\n" \
M_"v[172:175], v[48:51], v[148:151], v[172:175]\n" \
M_"v[168:171], v[50:53], v[156:159], v[168:171]\n" \
B_"v[228:231], v20, s[56:59], s69 offen offset:1024 sc1\n" \
"s_andn2_b32 s75, 0x4000, s98\n" \
"s_cbranch_scc1 L_sx4_%=\n" \
"L_rx4_%=:\n" \
"s_add_u32 s70, s63, 0x3000\n" \
M_"v[172:175], v[48:51], v[156:159], v[172:175]\n" \
M_"v[168:171], v[50:53], v[164:167], v[168:171]\n" \
M_"v[172:175], v[48:51], v[164:167], v[172:175]\n" \
B_"v[236:239], v20, s[56:59], s70 offen offset:1024 sc1\n" \
"s_waitcnt vmcnt(17)\n" \
M_"v[56:59], v[50:53], v[112:115], v[56:59]\n" \
M_"v[60:63], v[48:51], v[112:115], v[60:63]\n" \
"s_waitcnt vmcnt(16)\n" \
M_"v[56:59], v[50:53], v[184:187], v[56:59]\n" \
M_"v[60:63], v[48:51], v[184:187], v[60:63]\n" \
C_"v168, v168, v169\n" \
"s_waitcnt vmcnt(15)\n" \
M_"v[56:59], v[50:53], v[120:123], v[56:59]\n" \
C_"v172, v172, v173\n" \
C_"v169, v170, v171\n" \
M_"v[60:63], v[48:51], v[120:123], v[60:63]\n" \
C_"v173, v174, v175\n" \
"s_waitcnt vmcnt(14)\n" \
M_"v[56:59], v[50:53], v[192:195], v[56:59]\n" \
"buffer_store_dwordx2 v[168:169], v42, s[88:91], s73 offen sc1\n" \
M_"v[60:63], v[48:51], v[192:195], v[60:63]\n" \
"s_waitcnt vmcnt(14)\n" \
M_"v[56:59], v[50:53], v[128:131], v[56:59]\n" \
"buffer_store_dwordx2 v[172:173], v43, s[88:91], s73 offen sc1\n" \
M_"v[60:63], v[48:51], v[128:131], v[60:63]\n" \
"s_waitcnt vmcnt(14)\n" \
M_"v[56:59], v[50:53], v[200:203], v[56:59]\n" \
M_"v[60:63], v[48:51], v[200:203], v[60:63]\n" \
"s_waitcnt vmcnt(13)\n" \
M_"v[56:59], v[50:53], v[176:179], v[56:59]\n" \
M_"v[60:63], v[48:51], v[176:179], v[60:63]\n" \
"s_waitcnt vmcnt(11)\n" \
M_"v[56:59], v[50:53], v[248:251], v[56:59]\n" \
M_"v[60:63], v[48:51], v[248:251], v[60:63]\n" \
"s_waitcnt vmcnt(10)\n" \
M_"v[240:243], v[50:53], v[188:191], v[240:243]\n" \
M_"v[244:247], v[48:51], v[188:191], v[244:247]\n" \
"s_waitcnt vmcnt(9)\n" \
M_"v[240:243], v[50:53], v[196:199], v[240:243]\n" \
M_"v[244:247], v[48:51], v[196:199], v[244:247]\n" \
"s_mov_b64 exec, 1\n" \
"buffer_store_dword v46, off, s[56:59], s71 offset:0 sc1\n" \
"s_mov_b64 exec, -1\n" \
C_"v56, v56, v57\n" \
C_"v60, v60, v61\n" \
C_"v57, v58, v59\n" \
C_"v61, v62, v63\n" \
"buffer_store_dwordx2 v[56:57], v42, s[88:91], s72 offen sc1\n" \
"buffer_store_dwordx2 v[60:61], v43, s[88:91], s72 offen sc1\n" \
"s_waitcnt vmcnt(9)\n" \
M_"v[240:243], v[50:53], v[204:207], v[240:243]\n" \
M_"v[244:247], v[48:51], v[204:207], v[244:247]\n" \
"s_waitcnt vmcnt(8)\n" \
M_"v[240:243], v[50:53], v[212:215], v[240:243]\n" \
M_"v[244:247], v[48:51], v[212:215], v[244:247]\n" \
"s_waitcnt vmcnt(7)\n" \
M_"v[240:243], v[50:53], v[220:223], v[240:243]\n" \
M_"v[244:247], v[48:51], v[220:223], v[244:247]\n" \
"s_waitcnt vmcnt(6)\n" \
M_"v[240:243], v[50:53], v[228:231], v[240:243]\n" \
M_"v[244:247], v[48:51], v[228:231], v[244:247]\n" \
"s_waitcnt vmcnt(5)\n" \
M_"v[240:243], v[50:53], v[236:239], v[240:243]\n" \
M_"v[244:247], v[48:51], v[236:239], v[244:247]\n" \
"s_nop 6\n" \
C_"v240, v240, v241\n" \
C_"v244, v244, v245\n" \
C_"v241, v242, v243\n" \
C_"v245, v246, v247\n" \
"buffer_store_dwordx2 v[240:241], v42, s[88:91], s92 offen sc1\n" \
"buffer_store_dwordx2 v[244:245], v43, s[88:91], s92 offen sc1\n" \
"L_fin_%=:\n"
#define ATT_VCLOB "v21","v22","v23","v24","v25","v26","v27","v28","v29","v32","v33","v34","v35","v36","v37","v38","v39","v40","v41","v42","v43","v44","v45","v46","v47","v48","v49","v50","v51","v52","v53","v54","v55","v56","v57","v58","v59","v60","v61","v62","v63","v64","v65","v66","v67","v68","v69","v70","v71","v72","v73","v74","v75","v76","v77","v78","v79","v80","v81","v82","v83","v84","v85","v86","v87","v88","v89","v90","v91","v92","v93","v94","v95","v96","v97","v98","v99","v100","v101","v102","v103","v104","v105","v106","v107","v108","v109","v110","v111","v112","v113","v114","v115","v116","v117","v118","v119","v120","v121","v122","v123","v124","v125","v126","v127","v128","v129","v130","v131","v132","v133","v134","v135","v136","v137","v138","v139","v140","v141","v142","v143","v144","v145","v146","v147","v148","v149","v150","v151","v152","v153","v154","v155","v156","v157","v158","v159","v160","v161","v162","v163","v164","v165","v166","v167","v168","v169","v170","v171","v172","v173","v174","v175","v176","v177","v178","v179","v180","v181","v182","v183","v184","v185","v186","v187","v188","v189","v190","v191","v192","v193","v194","v195","v196","v197","v198","v199","v200","v201","v202","v203","v204","v205","v206","v207","v208","v209","v210","v211","v212","v213","v214","v215","v216","v217","v218","v219","v220","v221","v222","v223","v224","v225","v226","v227","v228","v229","v230","v231","v232","v233","v234","v235","v236","v237","v238","v239","v240","v241","v242","v243","v244","v245","v246","v247","v248","v249","v250","v251","v252","v253","v254","v255"
#define ATT_SCLOB "s63","s64","s65","s66","s67","s68","s69","s70","s71","s72","s73","s74","s75","s76","s77","s78","s79","s80","s81","s82","s83","s84","s85","s86","s87","s88","s89","s90","s91","s92","s93","s95","s96","s97","s98","s99","s100","s101"
#define ATT_WTCH 0
#define ATT_PEEL 1
#define ATT_PEEL_FOW 1
#define ATT_PEEL_PRE 0
#define ATT_PEEL_ODD(b) (0u)
#define ATT_PAGES 11
#define ATT_TAILB 968
enum { T_fo, T_fj, T_fa, T_fas, T_fb, T_fbs, T_f8, T_mk8, T_wt, T_wol, T_woh, T_wbl, T_wbh, T_outl, T_outh, T_r1l, T_r1h, T_r1n, T_r1s, T_r2n, T_r2s, T_m2b, T_m0, T_m1, T_m1b, T_mw, T_m3, T_m4, T_ml, T_w0, T_w1, T_w2, T_w3, T_A1, T_A2, T_A3, T_B1, T_B2, T_B3, T_Ao, T_Bo, T_Al, T_Bl, T_l8, T_xr, T_x8r, T_oA, T_oB, T_o8, T_x0, T_x1, T_x2, T_x3, T_x4, T_x5, T_x6, T_x8, T_ep };

__device__ __forceinline__ void out_consts(u32x4 (&vp)[3], const u16* wo, const u16* wob, const u16* out, const unsigned b, const unsigned w, const unsigned lane) {
    const unsigned sp = b & 7u, nt = (b >> 3) & 3u, j = b >> 5, head = 4u * sp + nt, OOR = 0x40000000u;
    unsigned vw[9];
#pragma unroll
    for (unsigned i = 0; i < 9u; ++i) {
        const unsigned q = 64u * i + lane, row = __umul24(q, 3641u) >> 16, c = q - __umul24(row, 18u);
        vw[i] = __umul24(row, DM * 2) + (c < 16u ? (c * 16u + sp * 32u + 32u) & 255u : c * 16u) - (i & 3u) * 1024u;
    }
    const unsigned row = 16u * w + (lane >> 2), r9 = __umul24(16u * (row >> 5) + (row & 12u) + 2u * ((row >> 4) & 1u) + ((row >> 1) & 1u), 18u) + 16u + (row & 1u);
    unsigned vwl = __umul24(r9, DM * 2) + ((lane & 2u) ? 256u : sp * 32u) + (lane & 1u) * 16u, vrd = __umul24(lane >> 2, 288u) + (lane & 3u) * 8u;
    unsigned tab = 0;
#define TB(n, v) asm("v_writelane_b32 %0, %1, %2" : "+v"(tab) : "s"((unsigned)(v)), "n"((int)T_##n))
    const unsigned FJ = (unsigned)WS_F + (F_J + 640u * j) * 4u, mt = 2u * (w & 2u) + (w & 1u), w2 = w < 2u ? w : 2u, hw = head * 147456u;
    const unsigned FR = (unsigned)WS_F + F_R * 4u + (8u * j + sp) * 1024u;
    TB(fo, FJ + (ATT_PEEL_FOW ? 16u * head + 4u * w : 4u * head)); TB(fj, FJ); TB(fa, FR + (w | 2u) * 256u); TB(fas, FR + w * 256u + nt * 4u);
    TB(fb, FR + 128u + w * 256u); TB(fbs, FR + 128u + w * 256u + nt * 4u);
    TB(ep, ssel(b + w, OOR, (unsigned)WS_F + F_EPOCH * 4u));
    const u16* pwo = wo + nt * 144u; const u16* pwb = wob + nt * 144u;
    TB(wol, PLO(pwo)); TB(woh, PHI(pwo) & 0xffffu); TB(wbl, PLO(pwb)); TB(wbh, PHI(pwb) & 0xffffu); TB(outl, PLO(out)); TB(outh, PHI(out) & 0xffffu);
    const unsigned odd = ATT_PEEL_ODD(b), wo1 = w + 3u * odd;
#define OSEL(a, b_) (ATT_PEEL_ODD(1u) ? ssel(odd, a, b_) : (b_))
#define WU(u) (((4u * sp + ((nt + 1u + ((u) >> 2)) & 3u)) * 4u + ((u) & 3u)) * 36864u)
    TB(r1l, sslt<3>(wo1, PLO(pwo), PLO(pwb))); TB(r1h, sslt<3>(wo1, PHI(pwo), PHI(pwb)) & 0xffffu);
    TB(r1n, sslt<3>(wo1, 72u * sp + 71u, 4095u) * 1152u + 288u); TB(r1s, sslt<3>(wo1, (72u * sp + 32u * w) * 1152u, ATT_PEEL_ODD(1u) ? sslt<2>(w, WU(w), sslt<3>(w, OOR, hw + 110592u)) : hw + 110592u));
    if (ATT_PEEL_ODD(1u)) { TB(r2n, (72u * sp + 143u) * 1152u + 288u); TB(r2s, sslt<3>(w, (72u * sp + 72u + 32u * w) * 1152u, OOR)); TB(m2b, sslt<2>(w, 41984u + 9216u * w, 32768u)); }
    TB(m0, 131072u + 9216u * w2); TB(w0, hw + 36864u * w2); TB(m1, sslt<3>(w, 41984u + 9216u * w, 32768u)); TB(m1b, ssel(w ^ 2u, sslt<3>(w, 41984u + 9216u * w, 32768u), 114688u - 4096u));
    TB(mw, 65536u + 9216u * w); TB(m3, sslt<3>(w, 102400u + 9216u * w, 0u)); TB(m4, OSEL(9216u + 13312u * (w & 1u), sslt<3>(w, 131072u + 9216u * w, 9216u))); TB(ml, 18432u + 1024u * w);
    const unsigned tA = (unsigned)WS_AF + (8u * j + mt) * (KS_TOT * 1024u), tB = (unsigned)WS_AF + (120u - 8u * j + mt) * (KS_TOT * 1024u);
#define HK(k) (4u * sp + ((nt + k) & 3u))
    TB(w1, WU(w + 2u * odd)); TB(w2, WU(4u + w + 2u * odd)); TB(w3, WU(8u + (w | (2u * odd))));
    TB(A1, tA + 18432u + HK(1u) * 4096u); TB(A2, tA + 18432u + HK(2u) * 4096u); TB(A3, tA + 18432u + HK(3u) * 4096u);
    TB(B1, tB + 18432u + HK(1u) * 4096u); TB(B2, tB + 18432u + HK(2u) * 4096u); TB(B3, tB + 18432u + HK(3u) * 4096u);
    const unsigned pre = ATT_PEEL_PRE ? sslt<7>(j, tA + 18432u + head * 4096u, OOR) : OOR;
    TB(Ao, OSEL(pre, tA + sp * 2048u)); TB(Bo, OSEL(pre + 2048u, tB + sp * 2048u)); TB(Al, tA + 16384u); TB(Bl, tB + 16384u);
    const unsigned nred = w ^ (sp >> 1), blk = sp & 1u;
    const unsigned xg = (unsigned)WS_X + (4u * j + nt) * 589824u + w * 16384u, oA = (128u * j + 16u * mt) * 1152u + nt * 288u, oB = (1920u - 128u * j + 16u * mt) * 1152u + nt * 288u;
    TB(l8, ssel(nred, OOR, ssel(blk, tB, tA) + 16384u)); TB(xr, xg + sp * 65536u); TB(x8r, ssel(nred, OOR, xg + 524288u + (sp & 1u) * 1024u));
    TB(oA, oA + sp * 32u); TB(oB, oB + sp * 32u); TB(o8, ssel(nred, OOR, ssel(blk, oB, oA) + 256u)); TB(x8, xg + 524288u + sp * 2048u);
    TB(f8, xg + w * 131072u + 14400u + sp * 4u); TB(mk8, ssel(nred, 0u, 0xff0000u));
#define XI(i) TB(x##i, xg + ((sp + 1u + i) & 7u) * 65536u + i * 2048u)
    XI(0); XI(1); XI(2); XI(3); XI(4); XI(5); XI(6);
#undef XI
#undef HK
#undef WU
#undef TB
    vp[0] = (u32x4){vw[0], vw[1], vw[2], vw[3]}; vp[1] = (u32x4){vw[4], vw[5], vw[6], vw[7]}; vp[2] = (u32x4){vw[8], vwl, vrd, tab};
}
__global__ __launch_bounds__(256, 1) __attribute__((aligned(4096))) void fused_kernel(const u16* __restrict__ x, const u16* __restrict__ ckv, const u16* __restrict__ wq, const u16* __restrict__ wuk,
                                                       const u16* __restrict__ wuv, const u16* __restrict__ wqkv, const u16* __restrict__ gw,
                                                       const u16* __restrict__ wo, const u16* __restrict__ wob, char* __restrict__ ws_, u16* __restrict__ out) {
    using namespace att;
    extern __shared__ __attribute__((aligned(16))) char lds[];
    u32x4 sa[5]; u32x2 s24, s28; char* ws; unsigned tdum; kaptr ka = (kaptr)__builtin_amdgcn_kernarg_segment_ptr();
    asm volatile("s_load_dwordx8 s[8:15], %[ka], 0x0\n\ts_load_dwordx8 s[16:23], %[ka], 0x20\n\ts_getpc_b64 s[4:5]\n\tv_lshlrev_b32_e32 v191, 7, %[t]\n\ts_bfe_u32 s6, %[b], %[cb]\n\t"
                 "v_lshl_add_u32 v191, s6, %[cs], v191\n\tv_min_u32_e32 v191, %[e], v191\n\tglobal_load_dword v191, v191, s[4:5]\n\ts_load_dwordx4 s[24:27], %[ka], 0x40\n\ts_load_dwordx2 s[28:29], %[ka], 0x50"
                 : "={s[8:11]}"(sa[0]), "={s[12:15]}"(sa[1]), "={s[16:19]}"(sa[2]), "={s[20:23]}"(sa[3]), "={s[24:25]}"(s24), "={s[26:27]}"(ws), "={s[28:29]}"(s28), "={v191}"(tdum), [ka] "+{s[0:1]}"(ka)
                 : [t] "v"(threadIdx.x), [b] "s"(blockIdx.x), [e] "i"(CT_END), [cb] "i"(CT_END < 0x20000 ? 0x20003 : 0x30003), [cs] "i"(CT_END < 0x20000 ? 15 : 14) : "s4", "s5", "s6", "scc");
    SBAR();
    const unsigned wid = rfl(threadIdx.x >> 6);
    unsigned tag, vl;
    const int b = blockIdx.x;
    u32x4 vp[5], srdA; u32x2 sd;
    proj_phase(ka, ws, wid, vp, vl, sa, s24, s28, tdum, srdA, sd, tag
    );
    unsigned* const flags = (unsigned*)(ws + WS_F);
    RTS(2);
#define AST(L) do {} while (0)
#define ATT_TAIL "s_endpgm\n"
    unsigned abad;
    asm volatile(ATT_ASM ATT_TAIL "L_end_%=:\n s_nop 1"
                 : "={s94}"(abad)
                 : "{v[0:3]}"(vp[0]), "{v[4:7]}"(vp[1]), "{v[8:11]}"(vp[2]), "{v[12:15]}"(vp[3]), "{v[16:19]}"(vp[4]), "{v20}"(vl), "{s[56:59]}"(srdA), "{s[60:61]}"(sd), "{s62}"(tag),
                   "{s[8:11]}"(sa[0]), "{s[12:15]}"(sa[1]), "{s[16:19]}"(sa[2]), "{s[20:23]}"(sa[3]), "{s24}"(sa[4][0]), "{s3}"(wid), "{s2}"(b), "{s[28:29]}"(s28)
                 : "memory", "m0", "vcc", "scc", ATT_VCLOB, ATT_SCLOB, ACLOB_LO, ACLOB_HI);
    {
        unsigned b2 = b, w2 = wid;
        asm volatile("s_waitcnt vmcnt(0)" : "+s"(ka), "+s"(b2), "+s"(w2), "+s"(ws) :: "memory");
        const unsigned j2 = b2 >> 5, p2 = w2 & 1u, h2 = (b2 & 7u) * 4u + ((b2 >> 3) & 3u);
#pragma unroll 1
        for (unsigned i = w2 & 2u; i < ((w2 & 2u) ? (abad ? 8u : 4u) : 2u); ++i)
            att_exact(ka[0], ka[2], ws, h2, (i < 4u ? 1920u - 128u * j2 : 128u * j2 - 128u) + 32u * i + 16u * p2, *reinterpret_cast<const float*>(lds + LDS_G + (32u * i + 16u * p2 + (lane_id() & 15u)) * 4u));
        asm volatile("s_waitcnt vmcnt(0)" ::: "memory");
        unsigned* fa = (unsigned*)(ws + WS_F) + F_R + (8u * j2 + (b2 & 7u)) * 256u + (2u + p2) * 64u + ((b2 >> 3) & 3u);
        if (w2 & 2u) st_flag(fa, tag);
        wait_flags(fa, ld_flag(fa), tag);
        const unsigned l2 = lane_id(); u32x4 c[3]; out_consts(c, ka[7], ka[8], ka[10], b2, w2, l2);
        asm volatile("s_waitcnt vmcnt(0) lgkmcnt(0)\n\ts_nop 7\n\ts_branch L_att_stub" :: "{v[0:3]}"(c[0]), "{v[4:7]}"(c[1]), "{v[8:11]}"(c[2]), "{v20}"(l2 * 16u), "{s[56:59]}"(SRDA), "{s60}"(SD(8u * j2, h2, w2)),
                     "{s61}"(SD(120u - 8u * j2, h2, w2)), "{s62}"(tag), "{s3}"(w2), "{s2}"(b2) : "memory");
    }
    RTS(5);
}

extern "C" void kernel_launch(void* const* d_in, const int* in_sizes, int n_in, void* d_out, int out_size, void* d_ws, size_t ws_size,
                              hipStream_t stream) {
    (void)hipFuncSetAttribute((const void*)fused_kernel, hipFuncAttributeMaxDynamicSharedMemorySize, att::LDS_BYTES);
    fused_kernel<<<dim3(256), 256, att::LDS_BYTES, stream>>>((const u16*)d_in[0], (const u16*)d_in[4], (const u16*)d_in[5], (const u16*)d_in[6], (const u16*)d_in[7], (const u16*)d_in[1],
                                                             (const u16*)d_in[9], (const u16*)d_in[2], (const u16*)d_in[8], (char*)d_ws, (u16*)d_out);
}
```

```cpp
#include <hip/hip_runtime.h>
#include <hip/hip_bf16.h>
#include <stdint.h>
#include <utility>

typedef short bf16x8 __attribute__((ext_vector_type(8)));
typedef short s16x4 __attribute__((ext_vector_type(4)));
typedef float f32x16 __attribute__((ext_vector_type(16)));
typedef float f32x4 __attribute__((ext_vector_type(4)));
typedef unsigned u32x4 __attribute__((ext_vector_type(4)));
typedef unsigned u32x2 __attribute__((ext_vector_type(2)));
typedef unsigned short u16;

constexpr int S = 2048, DM = 576, NH = 32, HD = 128, DC = 512, NQKV = 1728, DHA = 18;
constexpr int NBIG = NH * HD;
constexpr int KS_TOT = 146;

#define SBAR() __builtin_amdgcn_sched_barrier(0)
constexpr size_t WS_Q = 0, WS_K = WS_Q + (size_t)S * NBIG * 2, WS_V = WS_K + (size_t)S * NBIG * 2, WS_AF = WS_V + (size_t)S * NBIG * 2, WS_X = WS_AF + (size_t)128 * KS_TOT * 1024,
                 WS_P = WS_X + (size_t)64 * 16 * 9216, WS_F = WS_P + (size_t)128 * (4 * 32 * 256 + 1024) * 4;
constexpr size_t WS_L = (size_t)96 << 20;
static_assert(WS_F == 96206848, "workspace map is baked into the generated asm");
constexpr int F_EPOCH = 0, F_J = 1024, F_ST = 8192, F_KV = 40960, F_R = 65536;
constexpr unsigned TAG_STEP = 0x9E3779B1u;
__device__ __forceinline__ unsigned ld_flag(const unsigned* p) { return __hip_atomic_load(p, __ATOMIC_RELAXED, __HIP_MEMORY_SCOPE_AGENT); }
__device__ __forceinline__ void st_flag(unsigned* p, unsigned v) { __hip_atomic_store(p, v, __ATOMIC_RELAXED, __HIP_MEMORY_SCOPE_AGENT); }
__device__ __forceinline__ void wait_flags(const unsigned* p, unsigned v, unsigned tag) {
    if (__builtin_expect(__builtin_amdgcn_ballot_w64(v != tag) != 0ull, 0)) {
        unsigned spins = 0;
        do { __builtin_amdgcn_s_sleep(1); } while (__builtin_amdgcn_ballot_w64(ld_flag(p) != tag) != 0ull && ++spins < (1u << 22));
    }
    asm volatile("" ::: "memory");
}

__device__ __forceinline__ unsigned lane_id() { unsigned l; asm volatile("v_mbcnt_lo_u32_b32 %0, -1, 0\n\tv_mbcnt_hi_u32_b32 %0, -1, %0" : "=v"(l)); return l; }
#define RTS(slot) do {} while (0)
#define CT_END ((PROJ_PAGES + ATT_PAGES) * 4096 + (ATT_TAILB & ~127) - 128)
#define ACLOB_LO "a0","a1","a2","a3","a4","a5","a6","a7","a8","a9","a10","a11","a12","a13","a14","a15","a16","a17","a18","a19","a20","a21","a22","a23","a24","a25","a26","a27","a28","a29","a30","a31","a32","a33","a34","a35","a36","a37","a38","a39","a40","a41","a42","a43","a44","a45","a46","a47","a48","a49","a50","a51","a52","a53","a54","a55","a56","a57","a58","a59","a60","a61","a62","a63","a64","a65","a66","a67","a68","a69","a70","a71","a72","a73","a74","a75","a76","a77","a78","a79","a80","a81","a82","a83","a84","a85","a86","a87","a88","a89","a90","a91","a92","a93","a94","a95","a96","a97","a98","a99","a100","a101","a102","a103","a104","a105","a106","a107","a108","a109","a110","a111","a112","a113","a114","a115","a116","a117","a118","a119","a120","a121","a122","a123","a124","a125","a126","a127"
#define ACLOB_HI "a128","a129","a130","a131","a132","a133","a134","a135","a136","a137","a138","a139","a140","a141","a142","a143","a144","a145","a146","a147","a148","a149","a150","a151","a152","a153","a154","a155","a156","a157","a158","a159","a160","a161","a162","a163","a164","a165","a166","a167","a168","a169","a170","a171","a172","a173","a174","a175","a176","a177","a178","a179","a180","a181","a182","a183","a184","a185","a186","a187","a188","a189","a190","a191","a192","a193","a194","a195","a196","a197","a198","a199","a200","a201","a202","a203","a204","a205","a206","a207","a208","a209","a210","a211","a212","a213","a214","a215","a216","a217","a218","a219","a220","a221","a222","a223","a224","a225","a226","a227","a228","a229","a230","a231","a232","a233","a234","a235","a236","a237","a238","a239","a240","a241","a242","a243","a244","a245","a246","a247","a248","a249","a250","a251","a252","a253","a254","a255"
#define M_ "v_mfma_f32_16x16x32_bf16 "
#define X_ "ds_read_b64_tr_b16 "
#define B_ "buffer_load_dwordx4 "
#define C_ "v_cvt_pk_bf16_f32 "
#define R_ "ds_read_b128 "
#define E_ "v_exp_f32_e32 "
#define PLO(p) ((unsigned)(size_t)(p))
#define PHI(p) ((unsigned)((size_t)(p) >> 32))
__device__ __forceinline__ unsigned vsel(bool c, unsigned a, unsigned b) { asm("" : "+v"(a), "+v"(b)); return c ? a : b; }
__device__ __forceinline__ unsigned ssel(unsigned c, unsigned a, unsigned b) { unsigned r; asm("s_cmp_lg_u32 %1, 0\n\ts_cselect_b32 %0, %2, %3" : "=s"(r) : "s"(c), "s"(a), "s"(b) : "scc"); return r; }
template <int N> __device__ __forceinline__ unsigned sslt(unsigned c, unsigned a, unsigned b) { unsigned r; asm("s_cmpk_lt_u32 %1, %4\n\ts_cselect_b32 %0, %2, %3" : "=s"(r) : "s"(c), "s"(a), "s"(b), "n"(N) : "scc"); return r; }
namespace att {
constexpr int LDS_BYTES = 163840;
constexpr unsigned LDS_G = 159744u, LDS_L = 161792u;
}
#define SD(J8, H, W) ((unsigned)WS_AF + (((J8) + 2u * ((W) & 2u) + ((W) & 1u)) * KS_TOT + 18u + 4u * (H)) * 1024u)
#define SRDA (u32x4){PLO(ws), PHI(ws) & 0xffffu, (unsigned)WS_L, 0x00020000u}
#define PROJ_ASM \
".p2align 3\n" \
"L_pb:\n" \
"s_waitcnt lgkmcnt(0)\n" \
"s_mov_b32 s64, s10\n" \
"s_and_b32 s65, s11, 0xffff\n" \
"s_mov_b32 s66, 0x200000\n" \
"s_mov_b32 s67, 0x20000\n" \
"s_mov_b32 s68, s14\n" \
"s_and_b32 s69, s15, 0xffff\n" \
"s_mov_b32 s70, 0x400000\n" \
"s_mov_b32 s71, 0x20000\n" \
"s_mov_b32 s72, s16\n" \
"s_and_b32 s73, s17, 0xffff\n" \
"s_mov_b32 s74, 0x400000\n" \
"s_mov_b32 s75, 0x20000\n" \
"s_add_u32 m0, s30, 0x0\n" \
"s_movk_i32 s84, 0x0\n" \
B_"v22, s[64:67], s84 offen offset:0 lds\n" \
"s_movk_i32 s84, 0x1c00\n" \
B_"v23, s[64:67], s84 offen offset:1024 lds\n" \
"s_movk_i32 s84, 0x3800\n" \
B_"v22, s[64:67], s84 offen offset:2048 lds\n" \
"s_movk_i32 s84, 0x5400\n" \
B_"v23, s[64:67], s84 offen offset:3072 lds\n" \
"s_add_u32 m0, s31, 0x0\n" \
"s_movk_i32 s84, 0x0\n" \
B_"v26, s[68:71], s84 offen offset:0 lds\n" \
"s_movk_i32 s84, 0x7c00\n" \
B_"v26, s[68:71], s84 offen offset:1024 lds\n" \
"s_addk_i32 s84, 0x7c00\n" \
B_"v27, s[68:71], s84 offen offset:2048 lds\n" \
"s_addk_i32 s84, 0x7c00\n" \
B_"v27, s[68:71], s84 offen offset:3072 lds\n" \
"s_movk_i32 s7, 0x300\n" \
"s_lshr_b32 s4, s2, 1\n" \
"s_and_b32 s4, s4, 60\n" \
"s_or_b32 s4, s4, s3\n" \
"v_lshrrev_b32_e32 v212, 4, v20\n" \
"v_min_u32_e32 v212, 59, v212\n" \
"s_cmpk_lt_u32 s2, 0x80\n" \
"s_cselect_b32 s6, s14, s16\n" \
"v_subrev_u32_e32 v214, 48, v212\n" \
"v_mov_b32_e32 v212, s10\n" \
"v_mov_b32_e32 v215, s6\n" \
"s_cmpk_lt_u32 s2, 0x80\n" \
"s_cselect_b32 s5, s15, s17\n" \
"v_cmp_gt_u32_e32 vcc, s7, v20\n" \
"v_mov_b32_e64 v213, s11\n" \
"v_bfe_u32 v216, v20, 4, 3\n" \
"v_cndmask_b32_e32 v212, v212, v215, vcc\n" \
"v_mov_b32_e32 v215, s5\n" \
"s_lshl_b32 s5, s3, 4\n" \
"v_cndmask_b32_e32 v213, v213, v215, vcc\n" \
"v_lshlrev_b32_e64 v215, 5, v20\n" \
"v_and_b32_e32 v215, 0x7000, v215\n" \
"v_lshl_add_u32 v215, s4, 6, v215\n" \
"s_lshl_b32 s4, s2, 3\n" \
"s_and_b32 s4, s4, 56\n" \
"v_or3_b32 v215, v215, s4, v216\n" \
"s_lshl_b32 s4, s2, 6\n" \
"s_or_b32 s6, s5, s4\n" \
"v_cmp_gt_u32_e64 s[4:5], 6, v214\n" \
"v_add_u32_e32 v215, 0x2000, v215\n" \
"s_nop 0\n" \
"v_cndmask_b32_e64 v216, 4, 2, s[4:5]\n" \
"v_add3_u32 v214, s6, v214, v216\n" \
"s_lshl_b32 s4, s3, 9\n" \
"v_cndmask_b32_e32 v214, v214, v215, vcc\n" \
"v_lshlrev_b32_e32 v214, 7, v214\n" \
"v_mov_b32_e64 v215, 0\n" \
"v_lshl_add_u64 v[184:185], v[212:213], 0, v[214:215]\n" \
"s_or_b32 s32, s4, 0x25000\n" \
"s_add_u32 m0, s30, 0x1000\n" \
"s_mov_b32 s84, 0x8000\n" \
B_"v22, s[64:67], s84 offen offset:0 lds\n" \
"s_and_b32 s33, s3, 1\n" \
"v_lshrrev_b32_e32 v212, 4, v20\n" \
"v_lshrrev_b32_e64 v214, 8, v20\n" \
"v_bfe_u32 v212, v212, 1, 3\n" \
"v_lshlrev_b32_e32 v215, 2, v20\n" \
"s_addk_i32 s84, 0x1c00\n" \
B_"v23, s[64:67], s84 offen offset:1024 lds\n" \
"v_lshrrev_b32_e64 v216, 1, v20\n" \
"v_bfe_u32 v213, v20, 4, 4\n" \
"s_lshl_b32 s4, s3, 5\n" \
"v_xor_b32_e32 v212, v212, v214\n" \
"v_lshlrev_b32_e32 v214, 11, v214\n" \
"s_addk_i32 s84, 0x1c00\n" \
B_"v22, s[64:67], s84 offen offset:2048 lds\n" \
"v_and_b32_e32 v215, 0x300, v215\n" \
"v_and_b32_e64 v217, 24, v216\n" \
"v_and_or_b32 v213, s4, 64, v213\n" \
"v_or3_b32 v214, v215, v214, v217\n" \
"s_lshl_b32 s4, s33, 14\n" \
"s_addk_i32 s84, 0x1c00\n" \
B_"v23, s[64:67], s84 offen offset:3072 lds\n" \
"v_and_b32_e32 v215, 0xe0, v216\n" \
"v_lshlrev_b32_e32 v213, 7, v213\n" \
"v_lshlrev_b32_e32 v212, 4, v212\n" \
"v_or3_b32 v214, v214, s4, v215\n" \
"v_or_b32_e64 v28, v212, v213\n" \
"s_add_u32 m0, s31, 0x4000\n" \
"s_movk_i32 s84, 0x0\n" \
B_"v26, s[72:75], s84 offen offset:0 lds\n" \
"v_add_u32_e32 v32, 0x10000, v214\n" \
"v_bitop3_b32 v29, v212, 64, v213 bitop3:0x36\n" \
"v_xor_b32_e32 v33, 0x20, v32\n" \
"v_xor_b32_e64 v34, 0x40, v32\n" \
"v_xor_b32_e32 v35, 0x60, v32\n" \
"s_movk_i32 s84, 0x7c00\n" \
B_"v26, s[72:75], s84 offen offset:1024 lds\n" \
"v_xor_b32_e32 v36, 0x80, v32\n" \
"v_xor_b32_e32 v37, 0xa0, v32\n" \
"v_xor_b32_e32 v38, 0xc0, v32\n" \
"v_xor_b32_e32 v39, 0xe0, v32\n" \
"s_mov_b32 s80, s26\n" \
"s_addk_i32 s84, 0x7c00\n" \
B_"v27, s[72:75], s84 offen offset:2048 lds\n" \
"s_and_b32 s81, s27, 0xffff\n" \
"s_mov_b32 s82, 0x10000000\n" \
"s_mov_b32 s83, 0x20000\n" \
"s_mov_b32 s76, s18\n" \
"s_and_b32 s77, s19, 0xffff\n" \
"s_addk_i32 s84, 0x7c00\n" \
B_"v27, s[72:75], s84 offen offset:3072 lds\n" \
"s_mov_b32 s78, 0x1e6000\n" \
"s_mov_b32 s79, 0x20000\n" \
"s_branch L_pgo_%=\n" \
"L_pret:\n" \
"s_setpc_b64 s[88:89]\n" \
"L_pgo_%=:\n" \
"s_mul_i32 s88, s33, 0xa000\n" \
"s_lshl_b32 s89, s3, 11\n" \
"s_and_b32 s89, s89, 0x1000\n" \
"s_add_u32 s88, s88, s89\n" \
"s_getpc_b64 s[86:87]\n" \
"L_hpa_%=:\n" \
"s_add_u32 s86, s86, L_pp1-L_hpa_%=\n" \
"s_addc_u32 s87, s87, 0\n" \
"s_add_u32 s86, s86, s88\n" \
"s_addc_u32 s87, s87, 0\n" \
"s_add_u32 m0, s30, 0x8000\n" \
"s_movk_i32 s84, 0x80\n" \
B_"v22, s[64:67], s84 offen offset:0 lds\n" \
"s_lshr_b32 s4, s2, 1\n" \
"s_and_b32 s4, s4, 0x7c\n" \
"v_bfe_u32 v212, v20, 4, 5\n" \
"s_or_b32 s4, s4, s3\n" \
"v_min_u32_e32 v212, 26, v212\n" \
"s_movk_i32 s84, 0x1c80\n" \
B_"v23, s[64:67], s84 offen offset:1024 lds\n" \
"v_mad_u64_u32 v[212:213], s[4:5], s4, 27, v[212:213]\n" \
"v_mul_u32_u24_e32 v213, 0xaaab, v212\n" \
"s_lshr_b32 s7, s2, 5\n" \
"v_lshrrev_b32_e32 v213, 18, v213\n" \
"s_mov_b32 s4, 0x1fffffa\n" \
"s_mov_b64 exec, 0\n" \
"s_swappc_b64 s[88:89], s[86:87]\n" \
"s_mov_b64 exec, -1\n" \
"s_add_u32 s86, s86, 0x2000\n" \
"s_addc_u32 s87, s87, 0\n" \
"s_movk_i32 s84, 0x3880\n" \
B_"v22, s[64:67], s84 offen offset:2048 lds\n" \
"s_and_b32 s6, s2, 7\n" \
"v_mad_u64_u32 v[214:215], s[4:5], v213, s4, v[212:213]\n" \
"s_mulk_i32 s7, 0x90\n" \
"v_mul_u32_u24_e32 v213, 27, v213\n" \
"v_lshrrev_b32_e32 v214, 1, v214\n" \
"s_movk_i32 s84, 0x5480\n" \
B_"v23, s[64:67], s84 offen offset:3072 lds\n" \
"v_and_b32_e32 v212, 1, v212\n" \
"s_add_u32 s4, s7, s20\n" \
"v_mul_u32_u24_e64 v214, 9, v214\n" \
"v_add3_u32 v212, v212, s6, v213\n" \
"s_addc_u32 s5, 0, s21\n" \
"s_add_u32 m0, s31, 0x8000\n" \
"s_mov_b32 s84, 0x80000\n" \
B_"v26, s[68:71], s84 offen offset:0 lds\n" \
"v_add_lshl_u32 v212, v212, v214, 7\n" \
"v_mov_b32_e32 v213, 0\n" \
"v_lshl_add_u64 v[186:187], s[18:19], 0, v[212:213]\n" \
"s_bitcmp1_b32 s3, 0\n" \
"s_cselect_b32 s6, 0x8c, 0\n" \
"s_addk_i32 s84, 0x7c00\n" \
B_"v26, s[68:71], s84 offen offset:1024 lds\n" \
"s_add_u32 s4, s4, s6\n" \
"v_mov_b32_e32 v212, v187\n" \
"s_addc_u32 s5, s5, 0\n" \
"v_writelane_b32 v186, s4, 31\n" \
"v_writelane_b32 v212, s5, 31\n" \
"s_addk_i32 s84, 0x7c00\n" \
B_"v27, s[68:71], s84 offen offset:2048 lds\n" \
"v_mov_b32_e32 v187, v212\n" \
"s_lshl_b32 s35, s2, 10\n" \
"s_lshr_b32 s37, s2, 1\n" \
"s_mul_i32 s37, s37, 0x24800\n" \
"s_and_b32 s35, s35, 0x400\n" \
"s_mov_b64 exec, 0\n" \
"s_swappc_b64 s[88:89], s[86:87]\n" \
"s_mov_b64 exec, -1\n" \
"s_add_u32 s86, s86, 0x2000\n" \
"s_addc_u32 s87, s87, 0\n" \
"s_addk_i32 s84, 0x7c00\n" \
B_"v27, s[68:71], s84 offen offset:3072 lds\n" \
"s_lshl_b32 s36, s3, 8\n" \
"s_or_b32 s35, s37, s35\n" \
"s_and_b32 s4, s2, 7\n" \
"s_lshr_b32 s6, s2, 5\n" \
"s_lshl_b32 s34, s2, 5\n" \
"s_add_u32 m0, s30, 0x9000\n" \
"s_mov_b32 s84, 0x8080\n" \
B_"v22, s[64:67], s84 offen offset:0 lds\n" \
"s_add_i32 s35, s35, s36\n" \
"s_lshl_b32 s7, s4, 10\n" \
"s_and_b32 s34, s34, 0x300\n" \
"s_add_i32 s35, s35, 0x3004000\n" \
"s_lshl_b32 s37, s6, 13\n" \
"s_addk_i32 s84, 0x1c00\n" \
B_"v23, s[64:67], s84 offen offset:1024 lds\n" \
"s_or_b32 s34, s7, s34\n" \
"s_or_b32 s7, s37, s7\n" \
"v_mov_b32_e32 v213, s35\n" \
"s_or_b32 s7, s7, s36\n" \
"s_lshr_b32 s5, s2, 3\n" \
"s_addk_i32 s84, 0x1c00\n" \
B_"v22, s[64:67], s84 offen offset:2048 lds\n" \
"s_or_b32 s7, s7, 0x5c00000\n" \
"v_writelane_b32 v213, s7, 2\n" \
"s_mul_i32 s5, s5, 0x90000\n" \
"v_writelane_b32 v213, s7, 3\n" \
"s_lshl_b32 s7, s3, 14\n" \
"s_addk_i32 s84, 0x1c00\n" \
B_"v23, s[64:67], s84 offen offset:3072 lds\n" \
"s_lshl_b32 s4, s4, 16\n" \
"s_add_i32 s5, s5, s7\n" \
"s_or_b32 s34, s34, 0x5be8000\n" \
"v_writelane_b32 v213, s34, 4\n" \
"s_add_i32 s4, s5, s4\n" \
"v_writelane_b32 v213, s34, 5\n" \
"s_add_i32 s4, s4, 0x4243800\n" \
"v_writelane_b32 v213, s4, 6\n" \
"s_mul_i32 s4, s6, 0xa00\n" \
"v_lshlrev_b32_e32 v212, 3, v20\n" \
"s_lshl_b32 s5, s3, 7\n" \
"v_and_b32_e32 v212, 0x80, v212\n" \
"s_add_i32 s4, s4, s5\n" \
"s_add_i32 s4, s4, 0x5bc0f80\n" \
"v_writelane_b32 v213, s4, 7\n" \
"v_add_u32_e32 v49, v213, v212\n" \
"s_mov_b32 m0, s32\n" \
"s_mov_b32 exec_lo, 0xffffff\n" \
"s_mov_b32 exec_hi, 0xfff0000\n" \
"global_load_lds_dword v[184:185], off\n" \
"s_mov_b64 exec, -1\n" \
"s_bitcmp1_b32 s2, 0\n" \
"s_cbranch_scc1 L_sd_%=\n" \
"s_sleep 30\n" \
"L_sd_%=:\n" \
"s_cmp_lg_u32 s33, 0\n" \
"s_cbranch_scc1 L_w1_%=\n" \
"L_w0_%=:\n" \
"s_waitcnt vmcnt(13)\n" \
"s_barrier\n" \
X_"a[160:161], v32 offset:0\n" \
X_"a[162:163], v32 offset:1024\n" \
R_"a[128:131], v28 offset:16384\n" \
X_"a[224:225], v32 offset:8192\n" \
X_"a[226:227], v32 offset:9216\n" \
R_"a[192:195], v29 offset:16384\n" \
R_"a[132:135], v28 offset:18432\n" \
R_"a[196:199], v29 offset:18432\n" \
X_"a[164:165], v33 offset:0\n" \
X_"a[166:167], v33 offset:1024\n" \
X_"a[228:229], v33 offset:8192\n" \
X_"a[230:231], v33 offset:9216\n" \
R_"a[136:139], v28 offset:20480\n" \
R_"a[200:203], v29 offset:20480\n" \
"s_waitcnt lgkmcnt(11)\n" \
M_"a[0:3], a[160:163], a[128:131], 0\n" \
X_"a[168:169], v34 offset:0\n" \
"s_add_u32 m0, s31, 0xc000\n" \
"s_mov_b32 s84, 0x80000\n" \
"s_waitcnt lgkmcnt(9)\n" \
M_"a[0:3], a[224:227], a[192:195], a[0:3]\n" \
X_"a[170:171], v34 offset:1024\n" \
B_"v26, s[72:75], s84 offen offset:0 lds\n" \
"s_waitcnt lgkmcnt(9)\n" \
M_"a[16:19], a[160:163], a[132:135], 0\n" \
X_"a[232:233], v34 offset:8192\n" \
"s_lshr_b32 s4, s2, 1\n" \
"s_waitcnt lgkmcnt(9)\n" \
M_"a[16:19], a[224:227], a[196:199], a[16:19]\n" \
X_"a[234:235], v34 offset:9216\n" \
"s_addk_i32 s84, 0x7c00\n" \
"s_waitcnt lgkmcnt(8)\n" \
M_"a[20:23], a[164:167], a[132:135], 0\n" \
R_"a[140:143], v28 offset:22528\n" \
B_"v26, s[72:75], s84 offen offset:1024 lds\n" \
"s_waitcnt lgkmcnt(7)\n" \
M_"a[20:23], a[228:231], a[196:199], a[20:23]\n" \
R_"a[204:207], v29 offset:22528\n" \
"s_and_b32 s4, s4, 0x7c\n" \
M_"a[4:7], a[164:167], a[128:131], 0\n" \
X_"a[172:173], v35 offset:0\n" \
"s_addk_i32 s84, 0x7c00\n" \
M_"a[4:7], a[228:231], a[192:195], a[4:7]\n" \
X_"a[174:175], v35 offset:1024\n" \
B_"v27, s[72:75], s84 offen offset:2048 lds\n" \
"s_waitcnt lgkmcnt(9)\n" \
M_"a[32:35], a[160:163], a[136:139], 0\n" \
X_"a[236:237], v35 offset:8192\n" \
"v_mov_b32_e32 v213, s13\n" \
"s_waitcnt lgkmcnt(9)\n" \
M_"a[32:35], a[224:227], a[200:203], a[32:35]\n" \
X_"a[238:239], v35 offset:9216\n" \
"s_addk_i32 s84, 0x7c00\n" \
M_"a[36:39], a[164:167], a[136:139], 0\n" \
R_"a[144:147], v28 offset:0\n" \
B_"v27, s[72:75], s84 offen offset:3072 lds\n" \
M_"a[36:39], a[228:231], a[200:203], a[36:39]\n" \
R_"a[208:211], v29 offset:0\n" \
"s_or_b32 s4, s4, s3\n" \
"s_waitcnt lgkmcnt(10)\n" \
M_"a[40:43], a[168:171], a[136:139], 0\n" \
R_"a[148:151], v28 offset:2048\n" \
"v_lshrrev_b32_e32 v212, 4, v20\n" \
"s_waitcnt lgkmcnt(9)\n" \
M_"a[40:43], a[232:235], a[200:203], a[40:43]\n" \
R_"a[212:215], v29 offset:2048\n" \
"v_mad_u64_u32 v[214:215], s[4:5], s4, 36, v[212:213]\n" \
M_"a[24:27], a[168:171], a[132:135], 0\n" \
R_"a[152:155], v28 offset:4096\n" \
"v_mov_b32_e64 v217, s9\n" \
M_"a[24:27], a[232:235], a[196:199], a[24:27]\n" \
R_"a[216:219], v29 offset:4096\n" \
"s_movk_i32 s4, 0x240\n" \
M_"a[8:11], a[168:171], a[128:131], 0\n" \
R_"a[156:159], v28 offset:6144\n" \
"v_cmp_gt_u32_e32 vcc, s4, v20\n" \
M_"a[8:11], a[232:235], a[192:195], a[8:11]\n" \
R_"a[220:223], v29 offset:6144\n" \
"s_mov_b32 m0, s32\n" \
"s_waitcnt lgkmcnt(13)\n" \
M_"a[48:51], a[160:163], a[140:143], 0\n" \
"s_mov_b32 exec_lo, 0xff000000\n" \
"s_mov_b32 exec_hi, 0x0\n" \
"global_load_lds_dword v[184:185], off\n" \
"s_mov_b64 exec, -1\n" \
"s_waitcnt lgkmcnt(12)\n" \
M_"a[48:51], a[224:227], a[204:207], a[48:51]\n" \
X_"a[176:177], v36 offset:0\n" \
"s_lshl_b32 s4, s2, 3\n" \
M_"a[52:55], a[164:167], a[140:143], 0\n" \
X_"a[178:179], v36 offset:1024\n" \
"s_and_b32 s4, s4, 56\n" \
M_"a[52:55], a[228:231], a[204:207], a[52:55]\n" \
"s_mov_b32 m0, s32\n" \
X_"a[240:241], v36 offset:8192\n" \
M_"a[56:59], a[168:171], a[140:143], 0\n" \
"s_mov_b32 exec_lo, 0xffff\n" \
"s_mov_b32 exec_hi, 0x0\n" \
"global_load_lds_dword v[186:187], off\n" \
"s_mov_b64 exec, -1\n" \
M_"a[56:59], a[232:235], a[204:207], a[56:59]\n" \
X_"a[242:243], v36 offset:9216\n" \
"v_cndmask_b32_e32 v217, v217, v213, vcc\n" \
"s_waitcnt lgkmcnt(14)\n" \
M_"a[60:63], a[172:175], a[140:143], 0\n" \
X_"a[180:181], v37 offset:0\n" \
"v_lshlrev_b32_e32 v213, 3, v214\n" \
"s_waitcnt lgkmcnt(13)\n" \
M_"a[60:63], a[236:239], a[204:207], a[60:63]\n" \
X_"a[182:183], v37 offset:1024\n" \
"v_and_b32_e32 v213, 0x1ffc0, v213\n" \
M_"a[44:47], a[172:175], a[136:139], 0\n" \
X_"a[244:245], v37 offset:8192\n" \
"v_and_b32_e64 v214, 7, v214\n" \
M_"a[44:47], a[236:239], a[200:203], a[44:47]\n" \
X_"a[246:247], v37 offset:9216\n" \
"v_or3_b32 v213, v214, s4, v213\n" \
M_"a[28:31], a[172:175], a[132:135], 0\n" \
X_"a[184:185], v38 offset:0\n" \
"s_lshl_b32 s4, s2, 2\n" \
M_"a[28:31], a[236:239], a[196:199], a[28:31]\n" \
X_"a[186:187], v38 offset:1024\n" \
"s_or_b32 s4, s4, s3\n" \
M_"a[12:15], a[172:175], a[128:131], 0\n" \
"s_mov_b64 exec, 0\n" \
"s_swappc_b64 s[88:89], s[86:87]\n" \
"s_mov_b64 exec, -1\n" \
"s_add_u32 s86, s86, 0x2000\n" \
"s_addc_u32 s87, s87, 0\n" \
M_"a[12:15], a[236:239], a[192:195], a[12:15]\n" \
"s_mul_i32 s4, s4, 18\n" \
"v_min_u32_e32 v212, 53, v212\n" \
"s_waitcnt lgkmcnt(15)\n" \
M_"a[76:79], a[172:175], a[144:147], 0\n" \
"s_mov_b32 m0, s32\n" \
X_"a[248:249], v38 offset:8192\n" \
M_"a[76:79], a[236:239], a[208:211], a[76:79]\n" \
"s_mov_b64 exec, 0xff\n" \
"buffer_load_dword v49, s[80:83], 0 offen lds\n" \
"s_mov_b64 exec, -1\n" \
M_"a[72:75], a[168:171], a[144:147], 0\n" \
"s_sub_i32 s4, s4, 36\n" \
"v_add_u32_e64 v212, s4, v212\n" \
M_"a[72:75], a[232:235], a[208:211], a[72:75]\n" \
X_"a[250:251], v38 offset:9216\n" \
"v_mov_b32_e64 v215, s12\n" \
M_"a[68:71], a[164:167], a[144:147], 0\n" \
"s_mov_b32 s84, 0x5bc0000\n" \
"buffer_load_dword v180, off, s[80:83], s84 sc1\n" \
M_"a[68:71], a[228:231], a[208:211], a[68:71]\n" \
"v_mov_b32_e32 v216, s8\n" \
"v_cndmask_b32_e32 v212, v212, v213, vcc\n" \
M_"a[64:67], a[160:163], a[144:147], 0\n" \
"v_cndmask_b32_e32 v216, v216, v215, vcc\n" \
"v_lshlrev_b32_e32 v212, 7, v212\n" \
M_"a[64:67], a[224:227], a[208:211], a[64:67]\n" \
X_"a[188:189], v39 offset:0\n" \
"v_mov_b32_e64 v213, 0\n" \
M_"a[80:83], a[160:163], a[148:151], 0\n" \
"v_lshl_add_u64 v[188:189], v[216:217], 0, v[212:213]\n" \
"s_movk_i32 s5, 0xd80\n" \
"s_waitcnt lgkmcnt(15)\n" \
M_"a[80:83], a[224:227], a[212:215], a[80:83]\n" \
"s_lshr_b32 s6, s2, 5\n" \
"v_lshrrev_b32_e32 v212, 4, v20\n" \
M_"a[84:87], a[164:167], a[148:151], 0\n" \
X_"a[190:191], v39 offset:1024\n" \
"v_lshl_or_b32 v215, s3, 6, v212\n" \
M_"a[84:87], a[228:231], a[212:215], a[84:87]\n" \
"v_mul_u32_u24_e32 v216, 0x2aab, v215\n" \
"v_lshrrev_b32_e64 v216, 16, v216\n" \
M_"a[88:91], a[168:171], a[148:151], 0\n" \
"v_mad_i32_i24 v217, v216, -6, v215\n" \
"v_add_u32_e32 v218, 0xfffffe50, v215\n" \
M_"a[88:91], a[232:235], a[212:215], a[88:91]\n" \
X_"a[252:253], v39 offset:8192\n" \
"s_mul_i32 s35, s6, 0x48\n" \
M_"a[92:95], a[172:175], a[148:151], 0\n" \
"v_lshrrev_b32_e32 v219, 1, v217\n" \
"v_add_u32_e32 v218, s35, v216\n" \
M_"a[92:95], a[236:239], a[212:215], a[92:95]\n" \
"v_mul_u32_u24_e32 v219, 0x480, v219\n" \
"v_and_b32_e64 v214, 16, v20\n" \
M_"a[108:111], a[172:175], a[152:155], 0\n" \
X_"a[254:255], v39 offset:9216\n" \
"v_mad_u32_u24 v219, v218, s5, v219\n" \
M_"a[108:111], a[236:239], a[216:219], a[108:111]\n" \
"v_or_b32_e32 v192, v219, v214\n" \
"v_mov_b32_e32 v220, 2.0\n" \
M_"a[104:107], a[168:171], a[152:155], 0\n" \
"s_mov_b64 exec, 0\n" \
"s_swappc_b64 s[88:89], s[86:87]\n" \
"s_mov_b64 exec, -1\n" \
"s_andn2_b32 s85, 2, s3\n" \
"s_lshl_b32 s85, s85, 12\n" \
"s_add_u32 s86, s86, s85\n" \
"s_addc_u32 s87, s87, 0\n" \
M_"a[104:107], a[232:235], a[216:219], a[104:107]\n" \
"v_mov_b32_e32 v222, 7\n" \
"v_lshlrev_b32_e64 v220, 7, v218\n" \
M_"a[100:103], a[164:167], a[152:155], 0\n" \
"v_lshrrev_b32_e32 v218, 1, v218\n" \
"v_and_b32_e32 v218, 4, v218\n" \
M_"a[100:103], a[228:231], a[216:219], a[100:103]\n" \
"v_and_b32_e64 v216, 2, v216\n" \
"s_mul_i32 s7, s6, 0x2400\n" \
M_"a[96:99], a[160:163], a[152:155], 0\n" \
"s_sub_i32 s34, 0x22000, s7\n" \
"v_bitop3_b32 v216, v217, v218, v216 bitop3:0x1e\n" \
M_"a[96:99], a[224:227], a[216:219], a[96:99]\n" \
"v_or_b32_e32 v213, 0x24400, v20\n" \
"v_lshlrev_b32_e32 v216, 4, v216\n" \
"s_waitcnt lgkmcnt(15)\n" \
M_"a[112:115], a[160:163], a[156:159], 0\n" \
"v_add3_u32 v196, v220, s34, v216\n" \
"v_mov_b32_e64 v216, v213\n" \
M_"a[112:115], a[224:227], a[220:223], a[112:115]\n" \
"s_movk_i32 s36, 0x1b0\n" \
"v_or_b32_e32 v216, 0x100, v215\n" \
M_"a[116:119], a[164:167], a[156:159], 0\n" \
"v_mul_u32_u24_e32 v217, 0x2aab, v216\n" \
"v_add_u32_e32 v215, 0xffffff50, v215\n" \
M_"a[116:119], a[228:231], a[220:223], a[116:119]\n" \
"v_lshrrev_b32_e32 v217, 16, v217\n" \
"v_mov_b32_e64 v220, v215\n" \
M_"a[120:123], a[168:171], a[156:159], 0\n" \
"v_mad_i32_i24 v218, v217, -6, v216\n" \
"v_cmp_gt_u32_e64 vcc, s36, v216\n" \
M_"a[120:123], a[232:235], a[220:223], a[120:123]\n" \
"v_mov_b32_e32 v221, 7\n" \
"v_lshrrev_b32_e32 v222, 1, v218\n" \
M_"a[124:127], a[172:175], a[156:159], 0\n" \
"v_cndmask_b32_e32 v217, v220, v217, vcc\n" \
"s_movk_i32 s4, 0x48\n" \
"s_waitcnt vmcnt(4) lgkmcnt(0)\n" \
M_"a[124:127], a[236:239], a[220:223], a[124:127]\n" \
"s_barrier\n" \
M_"v[52:55], a[176:179], a[128:131], 0\n" \
"s_add_u32 m0, s30, 0x0\n" \
"s_movk_i32 s84, 0x100\n" \
M_"v[52:55], a[240:243], a[192:195], v[52:55]\n" \
B_"v22, s[64:67], s84 offen offset:0 lds\n" \
M_"v[56:59], a[180:183], a[128:131], 0\n" \
X_"a[160:161], v32 offset:32768\n" \
"v_add_u32_e64 v220, s35, v217\n" \
M_"v[56:59], a[244:247], a[192:195], v[56:59]\n" \
X_"a[162:163], v32 offset:33792\n" \
"v_mul_u32_u24_e32 v222, 0x480, v222\n" \
M_"v[60:63], a[184:187], a[128:131], 0\n" \
"s_movk_i32 s84, 0x1d00\n" \
M_"v[60:63], a[248:251], a[192:195], v[60:63]\n" \
B_"v23, s[64:67], s84 offen offset:1024 lds\n" \
M_"v[64:67], a[188:191], a[128:131], 0\n" \
R_"a[128:131], v28 offset:49152\n" \
"v_mad_u32_u24 v222, v220, s5, v222\n" \
M_"v[64:67], a[252:255], a[192:195], v[64:67]\n" \
R_"a[192:195], v29 offset:49152\n" \
"v_cmp_gt_u32_e64 s[4:5], s4, v215\n" \
M_"v[80:83], a[188:191], a[132:135], 0\n" \
"s_movk_i32 s84, 0x3900\n" \
M_"v[80:83], a[252:255], a[196:199], v[80:83]\n" \
B_"v22, s[64:67], s84 offen offset:2048 lds\n" \
M_"v[76:79], a[184:187], a[132:135], 0\n" \
X_"a[224:225], v32 offset:40960\n" \
"v_cndmask_b32_e64 v215, v221, v218, vcc\n" \
M_"v[76:79], a[248:251], a[196:199], v[76:79]\n" \
X_"a[226:227], v32 offset:41984\n" \
"v_lshrrev_b32_e64 v218, 1, v220\n" \
M_"v[72:75], a[180:183], a[132:135], 0\n" \
"s_movk_i32 s84, 0x5500\n" \
M_"v[72:75], a[244:247], a[196:199], v[72:75]\n" \
B_"v23, s[64:67], s84 offen offset:3072 lds\n" \
M_"v[68:71], a[176:179], a[132:135], 0\n" \
R_"a[132:135], v28 offset:51200\n" \
"v_or_b32_e32 v214, v222, v214\n" \
M_"v[68:71], a[240:243], a[196:199], v[68:71]\n" \
R_"a[196:199], v29 offset:51200\n" \
"v_mov_b32_e64 v222, 2.0\n" \
M_"v[84:87], a[176:179], a[136:139], 0\n" \
"s_add_u32 m0, s31, 0x0\n" \
"s_mov_b32 s84, 0x100000\n" \
M_"v[84:87], a[240:243], a[200:203], v[84:87]\n" \
B_"v26, s[68:71], s84 offen offset:0 lds\n" \
M_"v[88:91], a[180:183], a[136:139], 0\n" \
X_"a[164:165], v33 offset:32768\n" \
"v_and_b32_e32 v218, 4, v218\n" \
M_"v[88:91], a[244:247], a[200:203], v[88:91]\n" \
X_"a[166:167], v33 offset:33792\n" \
"v_and_b32_e64 v217, 2, v217\n" \
M_"v[92:95], a[184:187], a[136:139], 0\n" \
"s_addk_i32 s84, 0x7c00\n" \
M_"v[92:95], a[248:251], a[200:203], v[92:95]\n" \
B_"v26, s[68:71], s84 offen offset:1024 lds\n" \
M_"v[96:99], a[188:191], a[136:139], 0\n" \
R_"a[136:139], v28 offset:53248\n" \
"v_bitop3_b32 v215, v215, v218, v217 bitop3:0x1e\n" \
M_"v[96:99], a[252:255], a[200:203], v[96:99]\n" \
R_"a[200:203], v29 offset:53248\n" \
"v_lshlrev_b32_e32 v223, 1, v220\n" \
M_"v[112:115], a[188:191], a[140:143], 0\n" \
"s_addk_i32 s84, 0x7c00\n" \
M_"v[112:115], a[252:255], a[204:207], v[112:115]\n" \
B_"v27, s[68:71], s84 offen offset:2048 lds\n" \
M_"v[108:111], a[184:187], a[140:143], 0\n" \
"s_mov_b64 exec, 0\n" \
"s_swappc_b64 s[88:89], s[86:87]\n" \
"s_mov_b64 exec, -1\n" \
M_"v[108:111], a[248:251], a[204:207], v[108:111]\n" \
X_"a[228:229], v33 offset:40960\n" \
"v_mov_b32_e64 v224, 2.0\n" \
M_"v[104:107], a[180:183], a[140:143], 0\n" \
"s_addk_i32 s84, 0x7c00\n" \
M_"v[104:107], a[244:247], a[204:207], v[104:107]\n" \
B_"v27, s[68:71], s84 offen offset:3072 lds\n" \
M_"v[100:103], a[176:179], a[140:143], 0\n" \
R_"a[140:143], v28 offset:55296\n" \
"v_cndmask_b32_e32 v193, v222, v214, vcc\n" \
M_"v[100:103], a[240:243], a[204:207], v[100:103]\n" \
R_"a[204:207], v29 offset:55296\n" \
"v_lshlrev_b32_e64 v214, 7, v220\n" \
M_"v[116:119], a[176:179], a[144:147], 0\n" \
"s_add_u32 m0, s30, 0x1000\n" \
"s_mov_b32 s84, 0x8100\n" \
M_"v[116:119], a[240:243], a[208:211], v[116:119]\n" \
B_"v22, s[64:67], s84 offen offset:0 lds\n" \
M_"v[120:123], a[180:183], a[144:147], 0\n" \
X_"a[230:231], v33 offset:41984\n" \
"v_lshlrev_b32_e64 v215, 4, v215\n" \
M_"v[120:123], a[244:247], a[208:211], v[120:123]\n" \
X_"a[168:169], v34 offset:32768\n" \
"s_mul_i32 s39, s3, 18\n" \
M_"v[124:127], a[184:187], a[144:147], 0\n" \
"s_addk_i32 s84, 0x1c00\n" \
M_"v[124:127], a[248:251], a[208:211], v[124:127]\n" \
B_"v23, s[64:67], s84 offen offset:1024 lds\n" \
".fill (L_pb + 3808 - .) / 4, 4, 0xBF800000\n" \
"L_pp1:\n" \
"s_cbranch_execz L_pret\n" \
M_"v[128:131], a[188:191], a[144:147], 0\n" \
R_"a[144:147], v28 offset:32768\n" \
"v_add3_u32 v214, v214, s34, v215\n" \
M_"v[128:131], a[252:255], a[208:211], v[128:131]\n" \
R_"a[208:211], v29 offset:32768\n" \
"v_cndmask_b32_e64 v195, v224, v223, s[4:5]\n" \
M_"v[144:147], a[188:191], a[148:151], 0\n" \
"s_addk_i32 s84, 0x1c00\n" \
M_"v[144:147], a[252:255], a[212:215], v[144:147]\n" \
B_"v22, s[64:67], s84 offen offset:2048 lds\n" \
M_"v[140:143], a[184:187], a[148:151], 0\n" \
X_"a[170:171], v34 offset:33792\n" \
"s_movk_i32 s4, 0x1f8\n" \
M_"v[140:143], a[248:251], a[212:215], v[140:143]\n" \
X_"a[232:233], v34 offset:40960\n" \
"v_mov_b32_e32 v215, v213\n" \
M_"v[136:139], a[180:183], a[148:151], 0\n" \
"s_addk_i32 s84, 0x1c00\n" \
M_"v[136:139], a[244:247], a[212:215], v[136:139]\n" \
B_"v23, s[64:67], s84 offen offset:3072 lds\n" \
M_"v[132:135], a[176:179], a[148:151], 0\n" \
R_"a[148:151], v28 offset:34816\n" \
"s_add_i32 s35, s35, s39\n" \
M_"v[132:135], a[240:243], a[212:215], v[132:135]\n" \
R_"a[212:215], v29 offset:34816\n" \
"v_cmp_gt_u32_e64 vcc, s4, v216\n" \
M_"v[148:151], a[176:179], a[152:155], 0\n" \
"s_add_u32 m0, s31, 0x4000\n" \
"s_mov_b32 s84, 0x100000\n" \
M_"v[148:151], a[240:243], a[216:219], v[148:151]\n" \
B_"v26, s[72:75], s84 offen offset:0 lds\n" \
M_"v[152:155], a[180:183], a[152:155], 0\n" \
X_"a[234:235], v34 offset:41984\n" \
"v_add_u32_e64 v212, s35, v212\n" \
M_"v[152:155], a[244:247], a[216:219], v[152:155]\n" \
X_"a[172:173], v35 offset:32768\n" \
"v_mov_b32_e64 v219, 2.0\n" \
M_"v[156:159], a[184:187], a[152:155], 0\n" \
"s_addk_i32 s84, 0x7c00\n" \
M_"v[156:159], a[248:251], a[216:219], v[156:159]\n" \
B_"v26, s[72:75], s84 offen offset:1024 lds\n" \
M_"v[160:163], a[188:191], a[152:155], 0\n" \
R_"a[152:155], v28 offset:36864\n" \
"v_cndmask_b32_e32 v197, v215, v214, vcc\n" \
M_"v[160:163], a[252:255], a[216:219], v[160:163]\n" \
R_"a[216:219], v29 offset:36864\n" \
"s_movk_i32 s35, 0x120\n" \
M_"v[176:179], a[188:191], a[156:159], 0\n" \
X_"a[174:175], v35 offset:33792\n" \
"v_mul_u32_u24_e32 v214, 0xd80, v212\n" \
M_"v[176:179], a[252:255], a[220:223], v[176:179]\n" \
X_"a[236:237], v35 offset:40960\n" \
"v_cmp_gt_u32_e32 vcc, s35, v20\n" \
M_"v[172:175], a[184:187], a[156:159], 0\n" \
X_"a[238:239], v35 offset:41984\n" \
"v_lshrrev_b32_e64 v215, 1, v212\n" \
M_"v[172:175], a[248:251], a[220:223], v[172:175]\n" \
"s_and_b32 s4, s2, 7\n" \
"v_cndmask_b32_e32 v194, v219, v214, vcc\n" \
M_"v[168:171], a[180:183], a[156:159], 0\n" \
"v_lshlrev_b32_e32 v214, 7, v212\n" \
"v_lshrrev_b32_e32 v212, 2, v212\n" \
M_"v[168:171], a[244:247], a[220:223], v[168:171]\n" \
"v_and_b32_e64 v212, 2, v212\n" \
"v_and_or_b32 v212, v215, 1, v212\n" \
M_"v[164:167], a[176:179], a[156:159], 0\n" \
R_"a[156:159], v28 offset:38912\n" \
"s_lshl_b32 s5, s4, 2\n" \
M_"v[164:167], a[240:243], a[220:223], v[164:167]\n" \
R_"a[220:223], v29 offset:38912\n" \
"s_waitcnt lgkmcnt(15)\n" \
M_"a[0:3], a[160:163], a[128:131], a[0:3]\n" \
"s_addk_i32 s84, 0x7c00\n" \
M_"a[0:3], a[224:227], a[192:195], a[0:3]\n" \
B_"v27, s[72:75], s84 offen offset:2048 lds\n" \
M_"a[4:7], a[164:167], a[128:131], a[4:7]\n" \
X_"a[176:177], v36 offset:32768\n" \
"s_bfe_u32 s38, s2, 0x20003\n" \
M_"a[4:7], a[228:231], a[192:195], a[4:7]\n" \
"s_addk_i32 s84, 0x7c00\n" \
"s_waitcnt lgkmcnt(13)\n" \
M_"a[8:11], a[168:171], a[128:131], a[8:11]\n" \
B_"v27, s[72:75], s84 offen offset:3072 lds\n" \
"s_waitcnt lgkmcnt(9)\n" \
M_"a[8:11], a[232:235], a[192:195], a[8:11]\n" \
X_"a[178:179], v36 offset:33792\n" \
"v_lshlrev_b32_e32 v212, 5, v212\n" \
"s_waitcnt lgkmcnt(6)\n" \
M_"a[12:15], a[172:175], a[128:131], a[12:15]\n" \
X_"a[240:241], v36 offset:40960\n" \
"s_movk_i32 s35, 0x60\n" \
"s_waitcnt lgkmcnt(5)\n" \
M_"a[12:15], a[236:239], a[192:195], a[12:15]\n" \
"s_or_b32 s5, s5, s38\n" \
"v_bitop3_b32 v212, v212, s35, v214 bitop3:0x36\n" \
M_"a[28:31], a[172:175], a[132:135], a[28:31]\n" \
"s_lshr_b32 s36, s2, 3\n" \
"v_add_u32_e64 v212, s34, v212\n" \
M_"a[28:31], a[236:239], a[196:199], a[28:31]\n" \
X_"a[242:243], v36 offset:41984\n" \
"s_mul_i32 s34, s5, 0x12000\n" \
M_"a[24:27], a[168:171], a[132:135], a[24:27]\n" \
"s_or_b32 s40, s34, 0x6000000\n" \
"s_mul_i32 s34, s36, 0x90000\n" \
M_"a[24:27], a[232:235], a[196:199], a[24:27]\n" \
"s_lshl_b32 s36, s3, 14\n" \
"s_add_i32 s34, s34, s36\n" \
M_"a[20:23], a[164:167], a[132:135], a[20:23]\n" \
"s_lshl_b32 s38, s5, 8\n" \
"s_add_i32 s41, s34, 0x4240000\n" \
M_"a[20:23], a[228:231], a[196:199], a[20:23]\n" \
X_"a[180:181], v37 offset:32768\n" \
"s_mul_i32 s34, s5, 36\n" \
M_"a[16:19], a[160:163], a[132:135], a[16:19]\n" \
"s_add_i32 s5, s7, s40\n" \
"s_addk_i32 s5, 0x2000\n" \
M_"a[16:19], a[224:227], a[196:199], a[16:19]\n" \
"s_mov_b32 s37, 2.0\n" \
"s_or_b32 s38, s38, 0x5be8000\n" \
M_"a[32:35], a[160:163], a[136:139], a[32:35]\n" \
"s_mov_b32 m0, s32\n" \
X_"a[182:183], v37 offset:33792\n" \
M_"a[32:35], a[224:227], a[200:203], a[32:35]\n" \
"s_mov_b32 exec_lo, 0x0\n" \
"s_mov_b32 exec_hi, 0xff\n" \
"global_load_lds_dword v[184:185], off\n" \
"s_mov_b64 exec, -1\n" \
M_"a[36:39], a[164:167], a[136:139], a[36:39]\n" \
"s_cmp_lg_u32 s3, 0\n" \
"s_cselect_b32 s36, s37, s5\n" \
M_"a[36:39], a[228:231], a[200:203], a[36:39]\n" \
X_"a[244:245], v37 offset:40960\n" \
"s_lshl_b32 s5, s6, 2\n" \
M_"a[40:43], a[168:171], a[136:139], a[40:43]\n" \
"s_or_b32 s5, s38, s5\n" \
"s_lshl_b32 s35, s3, 10\n" \
M_"a[40:43], a[232:235], a[200:203], a[40:43]\n" \
"s_mov_b32 m0, s32\n" \
M_"a[44:47], a[172:175], a[136:139], a[44:47]\n" \
"s_mov_b32 exec_lo, 0xffff0000\n" \
"s_mov_b32 exec_hi, 0x0\n" \
"global_load_lds_dword v[186:187], off\n" \
"s_mov_b64 exec, -1\n" \
M_"a[44:47], a[236:239], a[200:203], a[44:47]\n" \
X_"a[246:247], v37 offset:41984\n" \
M_"a[60:63], a[172:175], a[140:143], a[60:63]\n" \
"s_cmpk_lt_u32 s3, 3\n" \
"s_cselect_b32 s37, s37, s5\n" \
M_"a[60:63], a[236:239], a[204:207], a[60:63]\n" \
"s_lshl_b32 s5, s4, 16\n" \
"s_lshl_b32 s4, s4, 11\n" \
M_"a[56:59], a[168:171], a[140:143], a[56:59]\n" \
X_"a[184:185], v38 offset:32768\n" \
"v_cndmask_b32_e64 v198, v213, v212, vcc\n" \
M_"a[56:59], a[232:235], a[204:207], a[56:59]\n" \
"v_or_b32_e32 v212, s35, v20\n" \
"s_or_b32 s39, s40, s35\n" \
M_"a[52:55], a[164:167], a[140:143], a[52:55]\n" \
"s_or_b32 s55, s41, s4\n" \
"v_or_b32_e32 v199, 0x22000, v212\n" \
M_"a[52:55], a[228:231], a[204:207], a[52:55]\n" \
X_"a[186:187], v38 offset:33792\n" \
"s_add_i32 s35, s39, s7\n" \
M_"a[48:51], a[160:163], a[140:143], a[48:51]\n" \
"s_add_i32 s54, s41, s5\n" \
"s_add_i32 s55, s55, 0x80000\n" \
M_"a[48:51], a[224:227], a[204:207], a[48:51]\n" \
"v_mov_b32_e32 v215, 0xffff\n" \
"s_and_b32 s4, s3, 1\n" \
M_"a[64:67], a[160:163], a[144:147], a[64:67]\n" \
X_"a[248:249], v38 offset:40960\n" \
"s_lshl_b32 s5, s4, 4\n" \
M_"a[64:67], a[224:227], a[208:211], a[64:67]\n" \
"v_lshrrev_b32_e32 v212, 4, v20\n" \
"v_lshrrev_b32_e64 v218, 6, v20\n" \
M_"a[68:71], a[164:167], a[144:147], a[68:71]\n" \
"v_and_b32_e64 v219, 12, v218\n" \
"v_and_or_b32 v214, v212, 15, s5\n" \
M_"a[68:71], a[228:231], a[208:211], a[68:71]\n" \
X_"a[250:251], v38 offset:41984\n" \
"v_cmp_gt_u32_e64 vcc, v219, v214\n" \
M_"a[72:75], a[168:171], a[144:147], a[72:75]\n" \
"v_mov_b32_e32 v220, 0xffff0000\n" \
"s_lshl_b32 s5, s2, 2\n" \
M_"a[72:75], a[232:235], a[208:211], a[72:75]\n" \
"v_cndmask_b32_e64 v217, v215, 0, vcc\n" \
"v_cmp_lt_u32_e32 vcc, v219, v214\n" \
M_"a[76:79], a[172:175], a[144:147], a[76:79]\n" \
"v_mov_b32_e32 v213, s23\n" \
"s_or_b32 s5, s5, s3\n" \
M_"a[76:79], a[236:239], a[208:211], a[76:79]\n" \
X_"a[188:189], v39 offset:32768\n" \
"v_cndmask_b32_e64 v221, 0, v220, vcc\n" \
M_"a[92:95], a[172:175], a[148:151], a[92:95]\n" \
"v_or_b32_e32 v12, v217, v221\n" \
"v_or_b32_e32 v217, 2, v219\n" \
M_"a[92:95], a[236:239], a[212:215], a[92:95]\n" \
"v_cmp_gt_u32_e32 vcc, v217, v214\n" \
"v_mov_b32_e32 v216, s25\n" \
M_"a[88:91], a[168:171], a[148:151], a[88:91]\n" \
X_"a[190:191], v39 offset:33792\n" \
"s_movk_i32 s40, 0xf400\n" \
M_"a[88:91], a[232:235], a[212:215], a[88:91]\n" \
"v_cndmask_b32_e64 v221, v215, 0, vcc\n" \
"v_cmp_lt_u32_e32 vcc, v217, v214\n" \
M_"a[84:87], a[164:167], a[148:151], a[84:87]\n" \
"s_lshl_b32 s4, s4, 6\n" \
"v_cndmask_b32_e32 v217, 0, v220, vcc\n" \
M_"a[84:87], a[228:231], a[212:215], a[84:87]\n" \
X_"a[252:253], v39 offset:40960\n" \
"v_or_b32_e64 v13, v221, v217\n" \
M_"a[80:83], a[160:163], a[148:151], a[80:83]\n" \
"v_or_b32_e32 v217, 16, v219\n" \
"v_cmp_gt_u32_e32 vcc, v217, v214\n" \
M_"a[80:83], a[224:227], a[212:215], a[80:83]\n" \
"s_nop 0\n" \
"v_cndmask_b32_e64 v221, v215, 0, vcc\n" \
"v_cmp_lt_u32_e32 vcc, v217, v214\n" \
M_"a[96:99], a[160:163], a[152:155], a[96:99]\n" \
X_"a[254:255], v39 offset:41984\n" \
"v_cndmask_b32_e64 v217, 0, v220, vcc\n" \
M_"a[96:99], a[224:227], a[216:219], a[96:99]\n" \
"v_or_b32_e32 v14, v221, v217\n" \
"v_or_b32_e32 v217, 18, v219\n" \
M_"a[100:103], a[164:167], a[152:155], a[100:103]\n" \
"v_cmp_gt_u32_e32 vcc, v217, v214\n" \
"s_nop 1\n" \
"v_cndmask_b32_e64 v215, v215, 0, vcc\n" \
M_"a[100:103], a[228:231], a[216:219], a[100:103]\n" \
"v_cmp_lt_u32_e32 vcc, v217, v214\n" \
"s_nop 1\n" \
"v_cndmask_b32_e64 v214, 0, v220, vcc\n" \
M_"a[104:107], a[168:171], a[152:155], a[104:107]\n" \
"v_or_b32_e64 v15, v215, v214\n" \
"v_mad_u64_u32 v[214:215], s[6:7], s5, 42, v[212:213]\n" \
M_"a[104:107], a[232:235], a[216:219], a[104:107]\n" \
"s_movk_i32 s5, 0x1440\n" \
"v_cmp_gt_u32_e32 vcc, s5, v214\n" \
M_"a[108:111], a[172:175], a[152:155], a[108:111]\n" \
"v_min_u32_e32 v217, 0xa43f, v214\n" \
"v_mov_b32_e64 v214, s24\n" \
M_"a[108:111], a[236:239], a[216:219], a[108:111]\n" \
"v_cndmask_b32_e32 v215, v216, v213, vcc\n" \
"v_mov_b32_e32 v213, s22\n" \
"s_waitcnt lgkmcnt(15)\n" \
M_"a[124:127], a[172:175], a[156:159], a[124:127]\n" \
"s_movk_i32 s6, 0xffee\n" \
"v_cndmask_b32_e64 v214, v214, v213, vcc\n" \
M_"a[124:127], a[236:239], a[220:223], a[124:127]\n" \
"v_add_u32_e32 v213, 0xffffebc0, v217\n" \
"s_lshl_b32 s5, s2, 5\n" \
M_"a[120:123], a[168:171], a[156:159], a[120:123]\n" \
"v_cndmask_b32_e32 v213, v213, v217, vcc\n" \
"v_lshlrev_b32_e64 v216, 7, v213\n" \
M_"a[120:123], a[232:235], a[220:223], a[120:123]\n" \
"v_mul_u32_u24_e32 v213, 0xe39, v212\n" \
"v_mov_b32_e64 v217, 0\n" \
M_"a[116:119], a[164:167], a[156:159], a[116:119]\n" \
"v_lshrrev_b32_e64 v213, 16, v213\n" \
"v_lshl_add_u64 v[16:17], v[214:215], 0, v[216:217]\n" \
M_"a[116:119], a[228:231], a[220:223], a[116:119]\n" \
"v_mad_i32_i24 v214, v213, s6, v212\n" \
"s_add_i32 s5, s5, 32\n" \
M_"a[112:115], a[160:163], a[156:159], a[112:115]\n" \
"v_lshlrev_b32_e32 v215, 4, v214\n" \
"v_add_u32_e64 v216, s5, v215\n" \
M_"a[112:115], a[224:227], a[220:223], a[112:115]\n" \
"s_waitcnt vmcnt(18)\n" \
"v_readfirstlane_b32 s62, v180\n" \
"s_nop 3\n" \
"s_add_u32 s62, s62, 0x9E3779B1\n" \
"s_waitcnt vmcnt(2) lgkmcnt(0)\n" \
"s_barrier\n" \
M_"v[52:55], a[176:179], a[128:131], v[52:55]\n" \
"s_add_u32 m0, s30, 0x8000\n" \
"s_movk_i32 s84, 0x180\n" \
M_"v[52:55], a[240:243], a[192:195], v[52:55]\n" \
B_"v22, s[64:67], s84 offen offset:0 lds\n" \
M_"v[56:59], a[180:183], a[128:131], v[56:59]\n" \
X_"a[160:161], v32 offset:0\n" \
"v_and_b32_e32 v216, 0xf0, v216\n" \
M_"v[56:59], a[244:247], a[192:195], v[56:59]\n" \
X_"a[162:163], v32 offset:1024\n" \
"v_cmp_gt_u32_e64 vcc, 16, v214\n" \
M_"v[60:63], a[184:187], a[128:131], v[60:63]\n" \
"s_movk_i32 s84, 0x1d80\n" \
M_"v[60:63], a[248:251], a[192:195], v[60:63]\n" \
B_"v23, s[64:67], s84 offen offset:1024 lds\n" \
M_"v[64:67], a[188:191], a[128:131], v[64:67]\n" \
R_"a[128:131], v28 offset:16384\n" \
"s_movk_i32 s7, 0xf800\n" \
M_"v[64:67], a[252:255], a[192:195], v[64:67]\n" \
R_"a[192:195], v29 offset:16384\n" \
"v_cndmask_b32_e64 v214, v215, v216, vcc\n" \
M_"v[80:83], a[188:191], a[132:135], v[80:83]\n" \
"s_movk_i32 s84, 0x3980\n" \
M_"v[80:83], a[252:255], a[196:199], v[80:83]\n" \
B_"v22, s[64:67], s84 offen offset:2048 lds\n" \
M_"v[76:79], a[184:187], a[132:135], v[76:79]\n" \
X_"a[224:225], v32 offset:8192\n" \
"v_or_b32_e32 v215, 64, v212\n" \
M_"v[76:79], a[248:251], a[196:199], v[76:79]\n" \
X_"a[226:227], v32 offset:9216\n" \
"v_mul_u32_u24_e32 v216, 0xe39, v215\n" \
M_"v[72:75], a[180:183], a[132:135], v[72:75]\n" \
"s_movk_i32 s84, 0x5580\n" \
M_"v[72:75], a[244:247], a[196:199], v[72:75]\n" \
B_"v23, s[64:67], s84 offen offset:3072 lds\n" \
M_"v[68:71], a[176:179], a[132:135], v[68:71]\n" \
R_"a[132:135], v28 offset:18432\n" \
"v_lshrrev_b32_e32 v216, 16, v216\n" \
M_"v[68:71], a[240:243], a[196:199], v[68:71]\n" \
R_"a[196:199], v29 offset:18432\n" \
"v_mad_i32_i24 v215, v216, s6, v215\n" \
M_"v[84:87], a[176:179], a[136:139], v[84:87]\n" \
"s_add_u32 m0, s31, 0x8000\n" \
"s_mov_b32 s84, 0x180000\n" \
M_"v[84:87], a[240:243], a[200:203], v[84:87]\n" \
B_"v26, s[68:71], s84 offen offset:0 lds\n" \
M_"v[88:91], a[180:183], a[136:139], v[88:91]\n" \
X_"a[164:165], v33 offset:0\n" \
"v_lshlrev_b32_e64 v217, 4, v215\n" \
M_"v[88:91], a[244:247], a[200:203], v[88:91]\n" \
X_"a[166:167], v33 offset:1024\n" \
"v_add_u32_e64 v220, s5, v217\n" \
M_"v[92:95], a[184:187], a[136:139], v[92:95]\n" \
"s_addk_i32 s84, 0x7c00\n" \
M_"v[92:95], a[248:251], a[200:203], v[92:95]\n" \
B_"v26, s[68:71], s84 offen offset:1024 lds\n" \
M_"v[96:99], a[188:191], a[136:139], v[96:99]\n" \
R_"a[136:139], v28 offset:20480\n" \
"v_and_b32_e32 v220, 0xf0, v220\n" \
M_"v[96:99], a[252:255], a[200:203], v[96:99]\n" \
R_"a[200:203], v29 offset:20480\n" \
"v_cmp_gt_u32_e32 vcc, 16, v215\n" \
M_"v[112:115], a[188:191], a[140:143], v[112:115]\n" \
"s_addk_i32 s84, 0x7c00\n" \
M_"v[112:115], a[252:255], a[204:207], v[112:115]\n" \
B_"v27, s[68:71], s84 offen offset:2048 lds\n" \
M_"v[108:111], a[184:187], a[140:143], v[108:111]\n" \
X_"a[228:229], v33 offset:8192\n" \
"v_cndmask_b32_e32 v215, v217, v220, vcc\n" \
M_"v[108:111], a[248:251], a[204:207], v[108:111]\n" \
X_"a[230:231], v33 offset:9216\n" \
"v_or_b32_e32 v217, 0x80, v212\n" \
M_"v[104:107], a[180:183], a[140:143], v[104:107]\n" \
"s_addk_i32 s84, 0x7c00\n" \
M_"v[104:107], a[244:247], a[204:207], v[104:107]\n" \
B_"v27, s[68:71], s84 offen offset:3072 lds\n" \
M_"v[100:103], a[176:179], a[140:143], v[100:103]\n" \
R_"a[140:143], v28 offset:22528\n" \
"v_mul_u32_u24_e32 v220, 0xe39, v217\n" \
M_"v[100:103], a[240:243], a[204:207], v[100:103]\n" \
R_"a[204:207], v29 offset:22528\n" \
"v_lshrrev_b32_e32 v220, 16, v220\n" \
M_"v[116:119], a[176:179], a[144:147], v[116:119]\n" \
"s_add_u32 m0, s30, 0x9000\n" \
"s_mov_b32 s84, 0x8180\n" \
M_"v[116:119], a[240:243], a[208:211], v[116:119]\n" \
B_"v22, s[64:67], s84 offen offset:0 lds\n" \
M_"v[120:123], a[180:183], a[144:147], v[120:123]\n" \
X_"a[168:169], v34 offset:0\n" \
"v_mad_i32_i24 v217, v220, s6, v217\n" \
M_"v[120:123], a[244:247], a[208:211], v[120:123]\n" \
X_"a[170:171], v34 offset:1024\n" \
"v_lshlrev_b32_e64 v221, 4, v217\n" \
M_"v[124:127], a[184:187], a[144:147], v[124:127]\n" \
"s_addk_i32 s84, 0x1c00\n" \
M_"v[124:127], a[248:251], a[208:211], v[124:127]\n" \
B_"v23, s[64:67], s84 offen offset:1024 lds\n" \
M_"v[128:131], a[188:191], a[144:147], v[128:131]\n" \
R_"a[144:147], v28 offset:0\n" \
"v_add_u32_e32 v222, s5, v221\n" \
M_"v[128:131], a[252:255], a[208:211], v[128:131]\n" \
R_"a[208:211], v29 offset:0\n" \
"v_and_b32_e32 v222, 0xf0, v222\n" \
M_"v[144:147], a[188:191], a[148:151], v[144:147]\n" \
"s_addk_i32 s84, 0x1c00\n" \
M_"v[144:147], a[252:255], a[212:215], v[144:147]\n" \
B_"v22, s[64:67], s84 offen offset:2048 lds\n" \
M_"v[140:143], a[184:187], a[148:151], v[140:143]\n" \
X_"a[232:233], v34 offset:8192\n" \
"v_cmp_gt_u32_e32 vcc, 16, v217\n" \
M_"v[140:143], a[248:251], a[212:215], v[140:143]\n" \
X_"a[234:235], v34 offset:9216\n" \
"v_cndmask_b32_e64 v217, v221, v222, vcc\n" \
M_"v[136:139], a[180:183], a[148:151], v[136:139]\n" \
"s_addk_i32 s84, 0x1c00\n" \
"s_nop 0\n" \
M_"v[136:139], a[244:247], a[212:215], v[136:139]\n" \
B_"v23, s[64:67], s84 offen offset:3072 lds\n" \
M_"v[132:135], a[176:179], a[148:151], v[132:135]\n" \
R_"a[148:151], v28 offset:2048\n" \
"v_or_b32_e32 v221, 0xc0, v212\n" \
M_"v[132:135], a[240:243], a[212:215], v[132:135]\n" \
R_"a[212:215], v29 offset:2048\n" \
"v_mul_u32_u24_e32 v222, 0xe39, v221\n" \
M_"v[148:151], a[176:179], a[152:155], v[148:151]\n" \
"s_add_u32 m0, s31, 0xc000\n" \
"s_mov_b32 s84, 0x180000\n" \
M_"v[148:151], a[240:243], a[216:219], v[148:151]\n" \
B_"v26, s[72:75], s84 offen offset:0 lds\n" \
M_"v[152:155], a[180:183], a[152:155], v[152:155]\n" \
X_"a[172:173], v35 offset:0\n" \
"v_lshrrev_b32_e64 v222, 16, v222\n" \
M_"v[152:155], a[244:247], a[216:219], v[152:155]\n" \
X_"a[174:175], v35 offset:1024\n" \
"v_mad_i32_i24 v221, v222, s6, v221\n" \
M_"v[156:159], a[184:187], a[152:155], v[156:159]\n" \
"s_addk_i32 s84, 0x7c00\n" \
M_"v[156:159], a[248:251], a[216:219], v[156:159]\n" \
B_"v26, s[72:75], s84 offen offset:1024 lds\n" \
M_"v[160:163], a[188:191], a[152:155], v[160:163]\n" \
R_"a[152:155], v28 offset:4096\n" \
"v_lshlrev_b32_e32 v223, 4, v221\n" \
M_"v[160:163], a[252:255], a[216:219], v[160:163]\n" \
R_"a[216:219], v29 offset:4096\n" \
"v_add_u32_e64 v224, s5, v223\n" \
M_"v[176:179], a[188:191], a[156:159], v[176:179]\n" \
X_"a[236:237], v35 offset:8192\n" \
"v_and_b32_e32 v224, 0xf0, v224\n" \
M_"v[176:179], a[252:255], a[220:223], v[176:179]\n" \
X_"a[238:239], v35 offset:9216\n" \
"v_cmp_gt_u32_e64 vcc, 16, v221\n" \
M_"v[172:175], a[184:187], a[156:159], v[172:175]\n" \
"s_nop 0\n" \
"v_cndmask_b32_e32 v221, v223, v224, vcc\n" \
"v_or_b32_e32 v223, 0x100, v212\n" \
M_"v[172:175], a[248:251], a[220:223], v[172:175]\n" \
"v_mul_u32_u24_e32 v224, 0xe39, v223\n" \
"v_lshrrev_b32_e64 v224, 16, v224\n" \
M_"v[168:171], a[180:183], a[156:159], v[168:171]\n" \
"v_mad_i32_i24 v223, v224, s6, v223\n" \
"v_lshlrev_b32_e64 v225, 4, v223\n" \
M_"v[168:171], a[244:247], a[220:223], v[168:171]\n" \
"v_add_u32_e64 v226, s5, v225\n" \
"v_and_b32_e32 v226, 0xf0, v226\n" \
M_"v[164:167], a[176:179], a[156:159], v[164:167]\n" \
R_"a[156:159], v28 offset:6144\n" \
"v_cmp_gt_u32_e64 vcc, 16, v223\n" \
M_"v[164:167], a[240:243], a[220:223], v[164:167]\n" \
R_"a[220:223], v29 offset:6144\n" \
"s_waitcnt lgkmcnt(15)\n" \
M_"a[0:3], a[160:163], a[128:131], a[0:3]\n" \
"s_addk_i32 s84, 0x7c00\n" \
M_"a[0:3], a[224:227], a[192:195], a[0:3]\n" \
B_"v27, s[72:75], s84 offen offset:2048 lds\n" \
M_"a[4:7], a[164:167], a[128:131], a[4:7]\n" \
X_"a[176:177], v36 offset:0\n" \
"v_cndmask_b32_e64 v223, v225, v226, vcc\n" \
M_"a[4:7], a[228:231], a[192:195], a[4:7]\n" \
"s_addk_i32 s84, 0x7c00\n" \
"s_waitcnt lgkmcnt(15)\n" \
M_"a[8:11], a[168:171], a[128:131], a[8:11]\n" \
B_"v27, s[72:75], s84 offen offset:3072 lds\n" \
"s_waitcnt lgkmcnt(11)\n" \
M_"a[8:11], a[232:235], a[192:195], a[8:11]\n" \
X_"a[178:179], v36 offset:1024\n" \
"v_or_b32_e32 v225, 0x140, v212\n" \
"s_waitcnt lgkmcnt(8)\n" \
M_"a[12:15], a[172:175], a[128:131], a[12:15]\n" \
X_"a[240:241], v36 offset:8192\n" \
"v_mul_u32_u24_e32 v226, 0xe39, v225\n" \
"s_waitcnt lgkmcnt(5)\n" \
M_"a[12:15], a[236:239], a[192:195], a[12:15]\n" \
"v_lshrrev_b32_e32 v226, 16, v226\n" \
"v_mad_i32_i24 v225, v226, s6, v225\n" \
M_"a[28:31], a[172:175], a[132:135], a[28:31]\n" \
"v_lshlrev_b32_e32 v227, 4, v225\n" \
"v_add_u32_e32 v228, s5, v227\n" \
M_"a[28:31], a[236:239], a[196:199], a[28:31]\n" \
X_"a[242:243], v36 offset:9216\n" \
"v_and_b32_e32 v228, 0xf0, v228\n" \
M_"a[24:27], a[168:171], a[132:135], a[24:27]\n" \
"v_cmp_gt_u32_e32 vcc, 16, v225\n" \
"s_nop 1\n" \
"v_cndmask_b32_e64 v225, v227, v228, vcc\n" \
M_"a[24:27], a[232:235], a[196:199], a[24:27]\n" \
"v_or_b32_e32 v227, 0x180, v212\n" \
"v_mul_u32_u24_e32 v228, 0xe39, v227\n" \
M_"a[20:23], a[164:167], a[132:135], a[20:23]\n" \
B_"v[200:203], v192, s[76:79], s34 offen offset:0\n" \
M_"a[20:23], a[228:231], a[196:199], a[20:23]\n" \
X_"a[180:181], v37 offset:0\n" \
"v_lshrrev_b32_e64 v228, 16, v228\n" \
M_"a[16:19], a[160:163], a[132:135], a[16:19]\n" \
"v_mad_i32_i24 v227, v228, s6, v227\n" \
"v_lshlrev_b32_e64 v229, 4, v227\n" \
M_"a[16:19], a[224:227], a[196:199], a[16:19]\n" \
"v_add_u32_e64 v230, s5, v229\n" \
"v_and_b32_e32 v230, 0xf0, v230\n" \
M_"a[32:35], a[160:163], a[136:139], a[32:35]\n" \
"s_mov_b32 m0, s32\n" \
X_"a[182:183], v37 offset:1024\n" \
M_"a[32:35], a[224:227], a[200:203], a[32:35]\n" \
"s_mov_b32 exec_lo, 0x0\n" \
"s_mov_b32 exec_hi, 0xff00\n" \
"global_load_lds_dword v[184:185], off\n" \
"s_mov_b64 exec, -1\n" \
M_"a[36:39], a[164:167], a[136:139], a[36:39]\n" \
B_"v[204:207], v193, s[76:79], s34 offen offset:0\n" \
M_"a[36:39], a[228:231], a[200:203], a[36:39]\n" \
X_"a[244:245], v37 offset:8192\n" \
"v_cmp_gt_u32_e32 vcc, 16, v227\n" \
M_"a[40:43], a[168:171], a[136:139], a[40:43]\n" \
"s_nop 0\n" \
"v_cndmask_b32_e32 v227, v229, v230, vcc\n" \
"v_or_b32_e32 v229, 0x1c0, v212\n" \
M_"a[40:43], a[232:235], a[200:203], a[40:43]\n" \
"v_mul_u32_u24_e32 v230, 0xe39, v229\n" \
"v_lshrrev_b32_e64 v230, 16, v230\n" \
M_"a[44:47], a[172:175], a[136:139], a[44:47]\n" \
X_"a[246:247], v37 offset:9216\n" \
"v_mad_i32_i24 v229, v230, s6, v229\n" \
M_"a[44:47], a[236:239], a[200:203], a[44:47]\n" \
"v_lshlrev_b32_e32 v231, 4, v229\n" \
"v_add_u32_e32 v232, s5, v231\n" \
M_"a[60:63], a[172:175], a[140:143], a[60:63]\n" \
"buffer_load_dword v208, v194, s[76:79], s34 offen offset:32\n" \
M_"a[60:63], a[236:239], a[204:207], a[60:63]\n" \
"v_and_b32_e32 v232, 0xf0, v232\n" \
"v_cmp_gt_u32_e64 vcc, 16, v229\n" \
M_"a[56:59], a[168:171], a[140:143], a[56:59]\n" \
X_"a[184:185], v38 offset:0\n" \
"v_cndmask_b32_e64 v229, v231, v232, vcc\n" \
M_"a[56:59], a[232:235], a[204:207], a[56:59]\n" \
"v_or_b32_e32 v231, 0x200, v212\n" \
"v_mul_u32_u24_e32 v232, 0xe39, v231\n" \
M_"a[52:55], a[164:167], a[140:143], a[52:55]\n" \
"v_lshrrev_b32_e64 v232, 16, v232\n" \
"v_mad_i32_i24 v231, v232, s6, v231\n" \
M_"a[52:55], a[228:231], a[204:207], a[52:55]\n" \
X_"a[186:187], v38 offset:1024\n" \
"v_lshlrev_b32_e64 v233, 4, v231\n" \
M_"a[48:51], a[160:163], a[140:143], a[48:51]\n" \
"v_add_u32_e32 v234, s5, v233\n" \
"s_movk_i32 s5, 0x480\n" \
M_"a[48:51], a[224:227], a[204:207], a[48:51]\n" \
"buffer_load_dword v209, v194, s[76:79], s34 offen offset:1184\n" \
M_"a[64:67], a[160:163], a[144:147], a[64:67]\n" \
X_"a[248:249], v38 offset:8192\n" \
"v_mad_u32_u24 v0, v213, s5, v214\n" \
M_"a[64:67], a[224:227], a[208:211], a[64:67]\n" \
"v_mul_u32_u24_e32 v213, 0x480, v216\n" \
"s_movk_i32 s6, 0xfc00\n" \
"s_nop 0\n" \
M_"a[68:71], a[164:167], a[144:147], a[68:71]\n" \
"v_add3_u32 v1, v213, v215, s6\n" \
"v_mul_u32_u24_e32 v213, 0x480, v220\n" \
M_"a[68:71], a[228:231], a[208:211], a[68:71]\n" \
X_"a[250:251], v38 offset:9216\n" \
"v_add3_u32 v2, v213, v217, s7\n" \
M_"a[72:75], a[168:171], a[144:147], a[72:75]\n" \
"v_mul_u32_u24_e32 v213, 0x480, v222\n" \
"v_add3_u32 v3, v213, v221, s40\n" \
M_"a[72:75], a[232:235], a[208:211], a[72:75]\n" \
"buffer_load_dword v211, v194, s[76:79], s34 offen offset:2336\n" \
M_"a[76:79], a[172:175], a[144:147], a[76:79]\n" \
"v_mul_u32_u24_e32 v213, 0x480, v226\n" \
"v_add3_u32 v5, v213, v225, s6\n" \
M_"a[76:79], a[236:239], a[208:211], a[76:79]\n" \
X_"a[188:189], v39 offset:0\n" \
"v_mul_u32_u24_e32 v213, 0x480, v228\n" \
M_"a[92:95], a[172:175], a[148:151], a[92:95]\n" \
"v_add3_u32 v6, v213, v227, s7\n" \
"v_mul_u32_u24_e32 v213, 0x480, v230\n" \
M_"a[92:95], a[236:239], a[212:215], a[92:95]\n" \
"v_add3_u32 v7, v213, v229, s40\n" \
"s_lshl_b32 s7, s3, 3\n" \
M_"a[88:91], a[168:171], a[148:151], a[88:91]\n" \
X_"a[190:191], v39 offset:1024\n" \
"s_lshl_b32 s40, s3, 1\n" \
M_"a[88:91], a[232:235], a[212:215], a[88:91]\n" \
"s_mov_b32 s76, s20\n" \
"s_and_b32 s77, s21, 0xffff\n" \
M_"a[84:87], a[164:167], a[148:151], a[84:87]\n" \
"s_mov_b32 s78, 0x480\n" \
"s_and_b32 s7, s7, 16\n" \
M_"a[84:87], a[228:231], a[212:215], a[84:87]\n" \
"buffer_load_ushort v210, v195, s[76:79], 0 offen\n" \
M_"a[80:83], a[160:163], a[148:151], a[80:83]\n" \
X_"a[252:253], v39 offset:8192\n" \
"s_and_b32 s40, s40, 2\n" \
".fill (L_pb + 7904 - .) / 4, 4, 0xBF800000\n" \
"L_pp2:\n" \
"s_cbranch_execz L_pret\n" \
M_"a[80:83], a[224:227], a[212:215], a[80:83]\n" \
"s_and_b32 s6, s2, 7\n" \
"v_bfe_u32 v213, v20, 7, 1\n" \
M_"a[96:99], a[160:163], a[152:155], a[96:99]\n" \
X_"a[254:255], v39 offset:9216\n" \
"s_or_b32 s7, s40, s7\n" \
M_"a[96:99], a[224:227], a[216:219], a[96:99]\n" \
"s_mov_b32 s76, s8\n" \
"s_and_b32 s77, s9, 0xffff\n" \
M_"a[100:103], a[164:167], a[152:155], a[100:103]\n" \
"s_mov_b32 s78, 0x240000\n" \
"v_and_b32_e32 v234, 0xf0, v234\n" \
M_"a[100:103], a[228:231], a[216:219], a[100:103]\n" \
"v_cmp_gt_u32_e64 vcc, 16, v231\n" \
"v_or3_b32 v213, s7, v213, v219\n" \
M_"a[104:107], a[168:171], a[152:155], a[104:107]\n" \
"v_and_b32_e32 v214, 32, v20\n" \
"s_lshl_b32 s6, s6, 5\n" \
M_"a[104:107], a[232:235], a[216:219], a[104:107]\n" \
"v_cndmask_b32_e32 v231, v233, v234, vcc\n" \
"v_mul_u32_u24_e32 v213, 18, v213\n" \
M_"a[108:111], a[172:175], a[152:155], a[108:111]\n" \
"v_mov_b32_e32 v215, 0x100\n" \
"v_mov_b32_e64 v216, s6\n" \
M_"a[108:111], a[236:239], a[216:219], a[108:111]\n" \
"v_cmp_eq_u32_e64 vcc, 0, v214\n" \
"v_and_or_b32 v213, v218, 1, v213\n" \
"s_waitcnt lgkmcnt(15)\n" \
M_"a[124:127], a[172:175], a[156:159], a[124:127]\n" \
"v_lshlrev_b32_e32 v212, 3, v212\n" \
"v_cndmask_b32_e64 v214, v215, v216, vcc\n" \
M_"a[124:127], a[236:239], a[220:223], a[124:127]\n" \
"v_mad_u32_u24 v213, v213, s5, v214\n" \
"v_and_or_b32 v213, v20, 16, v213\n" \
M_"a[120:123], a[168:171], a[156:159], a[120:123]\n" \
"v_add_u32_e32 v9, 0x4800, v213\n" \
"v_mul_u32_u24_e32 v213, 0x120, v218\n" \
M_"a[120:123], a[232:235], a[220:223], a[120:123]\n" \
"v_and_or_b32 v10, v212, 24, v213\n" \
"v_lshrrev_b32_e64 v213, 2, v20\n" \
M_"a[116:119], a[164:167], a[156:159], a[116:119]\n" \
"v_and_or_b32 v213, v213, 60, s4\n" \
"v_lshl_or_b32 v212, s3, 9, v212\n" \
M_"a[116:119], a[228:231], a[220:223], a[116:119]\n" \
"v_mad_u32_u24 v4, v224, s5, v223\n" \
"v_mad_u32_u24 v8, v232, s5, v231\n" \
M_"a[112:115], a[160:163], a[156:159], a[112:115]\n" \
"v_or_b32_e32 v18, 0x27000, v213\n" \
"v_or_b32_e32 v19, 0x27800, v212\n" \
"s_waitcnt vmcnt(7) lgkmcnt(0)\n" \
M_"a[112:115], a[224:227], a[220:223], a[112:115]\n" \
"s_barrier\n" \
M_"v[52:55], a[176:179], a[128:131], v[52:55]\n" \
"s_add_u32 m0, s30, 0x0\n" \
"s_movk_i32 s84, 0x200\n" \
M_"v[52:55], a[240:243], a[192:195], v[52:55]\n" \
B_"v22, s[64:67], s84 offen offset:0 lds\n" \
M_"v[56:59], a[180:183], a[128:131], v[56:59]\n" \
X_"a[160:161], v32 offset:32768\n" \
"v_mov_b32_e32 v215, 0x1000c0c\n" \
M_"v[56:59], a[244:247], a[192:195], v[56:59]\n" \
X_"a[162:163], v32 offset:33792\n" \
"v_mov_b32_e32 v216, 0xc0c0100\n" \
M_"v[60:63], a[184:187], a[128:131], v[60:63]\n" \
"s_movk_i32 s84, 0x1e00\n" \
M_"v[60:63], a[248:251], a[192:195], v[60:63]\n" \
B_"v23, s[64:67], s84 offen offset:1024 lds\n" \
M_"v[64:67], a[188:191], a[128:131], v[64:67]\n" \
R_"a[128:131], v28 offset:49152\n" \
"v_bfe_u32 v214, v20, 4, 1\n" \
M_"v[64:67], a[252:255], a[192:195], v[64:67]\n" \
R_"a[192:195], v29 offset:49152\n" \
"v_lshrrev_b32_e32 v212, 8, v20\n" \
M_"v[80:83], a[188:191], a[132:135], v[80:83]\n" \
"s_movk_i32 s84, 0x3a00\n" \
M_"v[80:83], a[252:255], a[196:199], v[80:83]\n" \
B_"v22, s[64:67], s84 offen offset:2048 lds\n" \
M_"v[76:79], a[184:187], a[132:135], v[76:79]\n" \
X_"a[224:225], v32 offset:40960\n" \
"v_cmp_eq_u32_e32 vcc, 0, v214\n" \
M_"v[76:79], a[248:251], a[196:199], v[76:79]\n" \
X_"a[226:227], v32 offset:41984\n" \
"v_bfe_u32 v213, v20, 4, 4\n" \
M_"v[72:75], a[180:183], a[132:135], v[72:75]\n" \
"s_movk_i32 s84, 0x5600\n" \
M_"v[72:75], a[244:247], a[196:199], v[72:75]\n" \
B_"v23, s[64:67], s84 offen offset:3072 lds\n" \
M_"v[68:71], a[176:179], a[132:135], v[68:71]\n" \
R_"a[132:135], v28 offset:51200\n" \
"v_mov_b32_e32 v217, 0xc0c0302\n" \
M_"v[68:71], a[240:243], a[196:199], v[68:71]\n" \
R_"a[196:199], v29 offset:51200\n" \
"v_cndmask_b32_e32 v215, v215, v216, vcc\n" \
M_"v[84:87], a[176:179], a[136:139], v[84:87]\n" \
"s_add_u32 m0, s31, 0x0\n" \
"s_mov_b32 s84, 0x200000\n" \
M_"v[84:87], a[240:243], a[200:203], v[84:87]\n" \
B_"v26, s[68:71], s84 offen offset:0 lds\n" \
M_"v[88:91], a[180:183], a[136:139], v[88:91]\n" \
X_"a[164:165], v33 offset:32768\n" \
"v_mov_b32_e32 v216, 0x3020c0c\n" \
M_"v[88:91], a[244:247], a[200:203], v[88:91]\n" \
X_"a[166:167], v33 offset:33792\n" \
"v_lshl_or_b32 v214, v212, 2, v214\n" \
M_"v[92:95], a[184:187], a[136:139], v[92:95]\n" \
"s_addk_i32 s84, 0x7c00\n" \
M_"v[92:95], a[248:251], a[200:203], v[92:95]\n" \
B_"v26, s[68:71], s84 offen offset:1024 lds\n" \
M_"v[96:99], a[188:191], a[136:139], v[96:99]\n" \
R_"a[136:139], v28 offset:53248\n" \
"v_cndmask_b32_e64 v216, v216, v217, vcc\n" \
M_"v[96:99], a[252:255], a[200:203], v[96:99]\n" \
R_"a[200:203], v29 offset:53248\n" \
"v_mov_b32_e32 v217, 0xc0c0c0c\n" \
M_"v[112:115], a[188:191], a[140:143], v[112:115]\n" \
"s_addk_i32 s84, 0x7c00\n" \
M_"v[112:115], a[252:255], a[204:207], v[112:115]\n" \
B_"v27, s[68:71], s84 offen offset:2048 lds\n" \
M_"v[108:111], a[184:187], a[140:143], v[108:111]\n" \
X_"a[228:229], v33 offset:40960\n" \
"v_cmp_gt_u32_e32 vcc, v214, v213\n" \
M_"v[108:111], a[248:251], a[204:207], v[108:111]\n" \
X_"a[230:231], v33 offset:41984\n" \
"v_or_b32_e64 v214, 2, v214\n" \
M_"v[104:107], a[180:183], a[140:143], v[104:107]\n" \
"s_addk_i32 s84, 0x7c00\n" \
M_"v[104:107], a[244:247], a[204:207], v[104:107]\n" \
B_"v27, s[68:71], s84 offen offset:3072 lds\n" \
M_"v[100:103], a[176:179], a[140:143], v[100:103]\n" \
R_"a[140:143], v28 offset:55296\n" \
"s_lshr_b32 s4, s2, 5\n" \
M_"v[100:103], a[240:243], a[204:207], v[100:103]\n" \
R_"a[204:207], v29 offset:55296\n" \
"v_cndmask_b32_e64 v40, v217, v215, vcc\n" \
M_"v[116:119], a[176:179], a[144:147], v[116:119]\n" \
"s_add_u32 m0, s30, 0x1000\n" \
"s_mov_b32 s84, 0x8200\n" \
M_"v[116:119], a[240:243], a[208:211], v[116:119]\n" \
B_"v22, s[64:67], s84 offen offset:0 lds\n" \
M_"v[120:123], a[180:183], a[144:147], v[120:123]\n" \
X_"a[168:169], v34 offset:32768\n" \
"v_cndmask_b32_e64 v42, v215, v217, vcc\n" \
M_"v[120:123], a[244:247], a[208:211], v[120:123]\n" \
X_"a[170:171], v34 offset:33792\n" \
"v_cmp_gt_u32_e64 vcc, v214, v213\n" \
M_"v[124:127], a[184:187], a[144:147], v[124:127]\n" \
"s_addk_i32 s84, 0x1c00\n" \
M_"v[124:127], a[248:251], a[208:211], v[124:127]\n" \
B_"v23, s[64:67], s84 offen offset:1024 lds\n" \
M_"v[128:131], a[188:191], a[144:147], v[128:131]\n" \
R_"a[144:147], v28 offset:32768\n" \
"v_and_b32_e32 v214, 16, v20\n" \
M_"v[128:131], a[252:255], a[208:211], v[128:131]\n" \
R_"a[208:211], v29 offset:32768\n" \
"s_movk_i32 s42, 0x3f80\n" \
M_"v[144:147], a[188:191], a[148:151], v[144:147]\n" \
"s_addk_i32 s84, 0x1c00\n" \
M_"v[144:147], a[252:255], a[212:215], v[144:147]\n" \
B_"v22, s[64:67], s84 offen offset:2048 lds\n" \
M_"v[140:143], a[184:187], a[148:151], v[140:143]\n" \
X_"a[232:233], v34 offset:40960\n" \
"s_xor_b32 s5, s4, 15\n" \
M_"v[140:143], a[248:251], a[212:215], v[140:143]\n" \
X_"a[234:235], v34 offset:41984\n" \
M_"v[136:139], a[180:183], a[148:151], v[136:139]\n" \
"s_addk_i32 s84, 0x1c00\n" \
M_"v[136:139], a[244:247], a[212:215], v[136:139]\n" \
B_"v23, s[64:67], s84 offen offset:3072 lds\n" \
M_"v[132:135], a[176:179], a[148:151], v[132:135]\n" \
R_"a[148:151], v28 offset:34816\n" \
M_"v[132:135], a[240:243], a[212:215], v[132:135]\n" \
R_"a[212:215], v29 offset:34816\n" \
M_"v[148:151], a[176:179], a[152:155], v[148:151]\n" \
"s_add_u32 m0, s31, 0x4000\n" \
"s_mov_b32 s84, 0x200000\n" \
M_"v[148:151], a[240:243], a[216:219], v[148:151]\n" \
B_"v26, s[72:75], s84 offen offset:0 lds\n" \
M_"v[152:155], a[180:183], a[152:155], v[152:155]\n" \
X_"a[172:173], v35 offset:32768\n" \
M_"v[152:155], a[244:247], a[216:219], v[152:155]\n" \
X_"a[174:175], v35 offset:33792\n" \
M_"v[156:159], a[184:187], a[152:155], v[156:159]\n" \
"s_addk_i32 s84, 0x7c00\n" \
"s_nop 0\n" \
M_"v[156:159], a[248:251], a[216:219], v[156:159]\n" \
B_"v26, s[72:75], s84 offen offset:1024 lds\n" \
M_"v[160:163], a[188:191], a[152:155], v[160:163]\n" \
R_"a[152:155], v28 offset:36864\n" \
M_"v[160:163], a[252:255], a[216:219], v[160:163]\n" \
R_"a[216:219], v29 offset:36864\n" \
M_"v[176:179], a[188:191], a[156:159], v[176:179]\n" \
X_"a[236:237], v35 offset:40960\n" \
M_"v[176:179], a[252:255], a[220:223], v[176:179]\n" \
X_"a[238:239], v35 offset:41984\n" \
M_"v[172:175], a[184:187], a[156:159], v[172:175]\n" \
"s_cmpk_lt_u32 s3, 2\n" \
"s_cselect_b32 s6, s5, s4\n" \
M_"v[172:175], a[248:251], a[220:223], v[172:175]\n" \
"s_lshl_b32 s7, s6, 7\n" \
"v_and_b32_e32 v213, 0x320, v20\n" \
M_"v[168:171], a[180:183], a[156:159], v[168:171]\n" \
"s_movk_i32 s41, 0x100\n" \
"v_lshlrev_b32_e64 v214, v214, s42\n" \
M_"v[168:171], a[244:247], a[220:223], v[168:171]\n" \
"v_mov_b32_e32 v215, 0\n" \
"s_add_i32 s40, s7, -16\n" \
M_"v[164:167], a[176:179], a[156:159], v[164:167]\n" \
R_"a[156:159], v28 offset:38912\n" \
"v_cndmask_b32_e64 v41, v217, v216, vcc\n" \
M_"v[164:167], a[240:243], a[220:223], v[164:167]\n" \
R_"a[220:223], v29 offset:38912\n" \
"s_waitcnt lgkmcnt(15)\n" \
M_"a[0:3], a[160:163], a[128:131], a[0:3]\n" \
"s_addk_i32 s84, 0x7c00\n" \
M_"a[0:3], a[224:227], a[192:195], a[0:3]\n" \
B_"v27, s[72:75], s84 offen offset:2048 lds\n" \
M_"a[4:7], a[164:167], a[128:131], a[4:7]\n" \
X_"a[176:177], v36 offset:32768\n" \
"v_cndmask_b32_e64 v43, v216, v217, vcc\n" \
M_"a[4:7], a[228:231], a[192:195], a[4:7]\n" \
"s_addk_i32 s84, 0x7c00\n" \
"s_waitcnt lgkmcnt(15)\n" \
M_"a[8:11], a[168:171], a[128:131], a[8:11]\n" \
B_"v27, s[72:75], s84 offen offset:3072 lds\n" \
"s_waitcnt lgkmcnt(11)\n" \
M_"a[8:11], a[232:235], a[192:195], a[8:11]\n" \
X_"a[178:179], v36 offset:33792\n" \
"v_cmp_eq_u32_e32 vcc, s41, v213\n" \
"s_waitcnt lgkmcnt(8)\n" \
M_"a[12:15], a[172:175], a[128:131], a[12:15]\n" \
X_"a[240:241], v36 offset:40960\n" \
"s_mov_b32 s41, 0\n" \
"s_waitcnt lgkmcnt(5)\n" \
M_"a[12:15], a[236:239], a[192:195], a[12:15]\n" \
"s_and_b32 s43, s3, 1\n" \
"v_cndmask_b32_e64 v46, v215, v214, vcc\n" \
M_"a[28:31], a[172:175], a[132:135], a[28:31]\n" \
"v_and_b32_e32 v213, 0x330, v20\n" \
M_"a[28:31], a[236:239], a[196:199], a[28:31]\n" \
X_"a[242:243], v36 offset:41984\n" \
M_"a[24:27], a[168:171], a[132:135], a[24:27]\n" \
"s_cmp_lg_u32 s7, 0\n" \
"s_cselect_b32 s40, s40, s41\n" \
M_"a[24:27], a[232:235], a[196:199], a[24:27]\n" \
"s_mov_b32 s42, 0xff800000\n" \
M_"a[20:23], a[164:167], a[132:135], a[20:23]\n" \
"s_cmp_lg_u32 s7, 0\n" \
"s_cselect_b32 s47, s41, s42\n" \
M_"a[20:23], a[228:231], a[196:199], a[20:23]\n" \
X_"a[180:181], v37 offset:32768\n" \
"s_lshl_b32 s41, s43, 6\n" \
M_"a[16:19], a[160:163], a[132:135], a[16:19]\n" \
"v_lshrrev_b32_e32 v215, 6, v20\n" \
"v_mov_b32_e32 v214, 0x3f80\n" \
M_"a[16:19], a[224:227], a[196:199], a[16:19]\n" \
"v_cmp_eq_u32_e32 vcc, 32, v213\n" \
"s_or_b32 s7, s41, s7\n" \
M_"a[32:35], a[160:163], a[136:139], a[32:35]\n" \
X_"a[182:183], v37 offset:33792\n" \
"v_lshrrev_b32_e64 v213, 7, v20\n" \
M_"a[32:35], a[224:227], a[200:203], a[32:35]\n" \
"v_bfe_u32 v216, v20, 6, 2\n" \
"v_lshrrev_b32_e64 v219, 1, v20\n" \
M_"a[36:39], a[164:167], a[136:139], a[36:39]\n" \
"v_bfe_u32 v215, v215, 1, 1\n" \
"v_cndmask_b32_e64 v47, 0, v214, vcc\n" \
M_"a[36:39], a[228:231], a[200:203], a[36:39]\n" \
X_"a[244:245], v37 offset:40960\n" \
"v_or_b32_e64 v214, s7, v213\n" \
M_"a[40:43], a[168:171], a[136:139], a[40:43]\n" \
"s_mov_b32 m0, s32\n" \
"v_lshl_or_b32 v217, s43, 3, v213\n" \
M_"a[40:43], a[232:235], a[200:203], a[40:43]\n" \
"s_mov_b32 exec_lo, 0xffffffff\n" \
"s_mov_b32 exec_hi, 0x0\n" \
"global_load_lds_dword v[188:189], off\n" \
"s_mov_b64 exec, -1\n" \
M_"a[44:47], a[172:175], a[136:139], a[44:47]\n" \
X_"a[246:247], v37 offset:41984\n" \
"v_lshlrev_b32_e64 v218, 10, v212\n" \
M_"a[44:47], a[236:239], a[200:203], a[44:47]\n" \
"v_lshlrev_b32_e32 v216, 7, v216\n" \
"v_and_b32_e32 v219, 24, v219\n" \
M_"a[60:63], a[172:175], a[140:143], a[60:63]\n" \
"v_and_or_b32 v213, v213, 2, v215\n" \
"v_add_u32_e64 v217, s40, v217\n" \
M_"a[60:63], a[236:239], a[204:207], a[60:63]\n" \
"v_or3_b32 v216, v216, v218, v219\n" \
"v_lshlrev_b32_e64 v213, 5, v213\n" \
M_"a[56:59], a[168:171], a[140:143], a[56:59]\n" \
X_"a[184:185], v38 offset:32768\n" \
"s_mov_b32 s40, 0x14000\n" \
M_"a[56:59], a[232:235], a[204:207], a[56:59]\n" \
"s_lshl_b32 s7, s2, 2\n" \
"v_or3_b32 v48, v216, v213, s40\n" \
M_"a[52:55], a[164:167], a[140:143], a[52:55]\n" \
"s_lshl_b32 s40, s43, 2\n" \
"s_lshl_b32 s6, s6, 3\n" \
M_"a[52:55], a[228:231], a[204:207], a[52:55]\n" \
X_"a[186:187], v38 offset:33792\n" \
"v_or_b32_e32 v213, s40, v212\n" \
M_"a[48:51], a[160:163], a[140:143], a[48:51]\n" \
"s_or_b32 s6, s6, s40\n" \
"s_and_b32 s40, s7, 0x7c\n" \
M_"a[48:51], a[224:227], a[204:207], a[48:51]\n" \
"s_or_b32 s40, s40, s3\n" \
M_"a[64:67], a[160:163], a[144:147], a[64:67]\n" \
X_"a[248:249], v38 offset:40960\n" \
M_"a[64:67], a[224:227], a[208:211], a[64:67]\n" \
"s_cmpk_lt_u32 s40, 64\n" \
"s_cselect_b32 s41, s4, s5\n" \
M_"a[68:71], a[164:167], a[144:147], a[68:71]\n" \
"s_lshl_b32 s40, s40, 1\n" \
"s_lshl_b32 s41, s41, 7\n" \
M_"a[68:71], a[228:231], a[208:211], a[68:71]\n" \
X_"a[250:251], v38 offset:41984\n" \
"s_and_b32 s40, s40, 0x7e\n" \
M_"a[72:75], a[168:171], a[144:147], a[72:75]\n" \
"s_or_b32 s40, s41, s40\n" \
"s_mulk_i32 s40, 0x480\n" \
M_"a[72:75], a[232:235], a[208:211], a[72:75]\n" \
"v_add_u32_e64 v49, s40, v20\n" \
"s_mov_b32 s40, 0xc000\n" \
M_"a[76:79], a[172:175], a[144:147], a[76:79]\n" \
"s_and_b32 s7, s7, 28\n" \
"s_bfe_u32 s45, s2, 0x20003\n" \
M_"a[76:79], a[236:239], a[208:211], a[76:79]\n" \
X_"a[188:189], v39 offset:32768\n" \
"s_mov_b32 s41, 0x8000\n" \
M_"a[92:95], a[172:175], a[148:151], a[92:95]\n" \
"s_cmp_lg_u32 s4, 0\n" \
"s_cselect_b32 s51, s40, s41\n" \
M_"a[92:95], a[236:239], a[212:215], a[92:95]\n" \
"s_movk_i32 s40, 0x4000\n" \
"s_lshr_b32 s44, s3, 1\n" \
M_"a[88:91], a[168:171], a[148:151], a[88:91]\n" \
X_"a[190:191], v39 offset:33792\n" \
"s_or_b32 s46, s7, s45\n" \
M_"a[88:91], a[232:235], a[212:215], a[88:91]\n" \
"s_cmp_lg_u32 s4, 0\n" \
"s_cselect_b32 s53, s41, s40\n" \
M_"a[84:87], a[164:167], a[148:151], a[84:87]\n" \
"s_waitcnt vmcnt(17)\n" \
"v_or_b32_e32 v204, v210, v204\n" \
"v_or_b32_e64 v206, v210, v206\n" \
M_"a[84:87], a[228:231], a[212:215], a[84:87]\n" \
"v_mov_b32_e64 v210, 0\n" \
"s_add_u32 s88, s35, 0x1000\n" \
M_"a[80:83], a[160:163], a[148:151], a[80:83]\n" \
X_"a[252:253], v39 offset:40960\n" \
"s_lshl_b32 s40, s44, 15\n" \
M_"a[80:83], a[224:227], a[212:215], a[80:83]\n" \
"ds_write_b128 v196, v[200:203]\n" \
M_"a[96:99], a[160:163], a[152:155], a[96:99]\n" \
X_"a[254:255], v39 offset:41984\n" \
"s_lshl_b32 s41, s43, 14\n" \
M_"a[96:99], a[224:227], a[216:219], a[96:99]\n" \
"s_lshl_b32 s48, s46, 20\n" \
"s_or_b32 s41, s40, s41\n" \
M_"a[100:103], a[164:167], a[152:155], a[100:103]\n" \
"ds_write_b128 v197, v[204:207]\n" \
M_"a[100:103], a[228:231], a[216:219], a[100:103]\n" \
"s_add_i32 s49, s48, 0x1000000\n" \
"s_or_b32 s42, s49, s41\n" \
M_"a[104:107], a[168:171], a[152:155], a[104:107]\n" \
"s_lshl_b32 s4, s4, 16\n" \
"s_or_b32 s41, s42, s4\n" \
M_"a[104:107], a[232:235], a[216:219], a[104:107]\n" \
"ds_write_b128 v198, v[208:211]\n" \
M_"a[108:111], a[172:175], a[152:155], a[108:111]\n" \
"s_lshl_b32 s4, s5, 16\n" \
"s_mul_i32 s5, s6, 0x24800\n" \
M_"a[108:111], a[236:239], a[216:219], a[108:111]\n" \
"s_lshl_b32 s6, s46, 9\n" \
"s_lshl_b32 s7, s7, 6\n" \
"s_waitcnt lgkmcnt(15)\n" \
M_"a[124:127], a[172:175], a[156:159], a[124:127]\n" \
"s_add_i32 s6, s5, s6\n" \
"s_or_b32 s5, s5, s7\n" \
M_"a[124:127], a[236:239], a[220:223], a[124:127]\n" \
"s_lshl_b32 s7, s45, 2\n" \
"s_or_b32 s5, s5, s7\n" \
M_"a[120:123], a[168:171], a[156:159], a[120:123]\n" \
"s_lshl_b32 s7, s3, 12\n" \
"s_or_b32 s50, s49, s7\n" \
M_"a[120:123], a[232:235], a[220:223], a[120:123]\n" \
"s_lshl_b32 s7, s3, 11\n" \
"v_lshrrev_b32_e64 v215, 4, v20\n" \
M_"a[116:119], a[164:167], a[156:159], a[116:119]\n" \
"v_bfe_u32 v216, v20, 4, 3\n" \
"s_or_b32 s42, s42, s4\n" \
M_"a[116:119], a[228:231], a[220:223], a[116:119]\n" \
"s_lshl_b32 s4, s43, 11\n" \
"s_add_i32 s52, s48, s7\n" \
M_"a[112:115], a[160:163], a[156:159], a[112:115]\n" \
"s_add_i32 s53, s53, s7\n" \
"v_mul_u32_u24_e32 v214, 0x480, v214\n" \
"s_waitcnt vmcnt(1) lgkmcnt(0)\n" \
M_"a[112:115], a[224:227], a[220:223], a[112:115]\n" \
"s_barrier\n" \
M_"v[52:55], a[176:179], a[128:131], v[52:55]\n" \
"s_add_u32 m0, s30, 0x8000\n" \
"s_movk_i32 s84, 0x280\n" \
M_"v[52:55], a[240:243], a[192:195], v[52:55]\n" \
B_"v22, s[64:67], s84 offen offset:0 lds\n" \
M_"v[56:59], a[180:183], a[128:131], v[56:59]\n" \
X_"a[160:161], v32 offset:0\n" \
"v_bitop3_b32 v218, v212, v215, 7 bitop3:0x78\n" \
M_"v[56:59], a[244:247], a[192:195], v[56:59]\n" \
"s_movk_i32 s84, 0x1e80\n" \
M_"v[60:63], a[184:187], a[128:131], v[60:63]\n" \
B_"v23, s[64:67], s84 offen offset:1024 lds\n" \
M_"v[60:63], a[248:251], a[192:195], v[60:63]\n" \
X_"a[162:163], v32 offset:1024\n" \
"v_bitop3_b32 v212, v212, v216, 4 bitop3:0x36\n" \
M_"v[64:67], a[188:191], a[128:131], v[64:67]\n" \
R_"a[128:131], v28 offset:16384\n" \
"s_lshl_b32 s7, s3, 10\n" \
M_"v[64:67], a[252:255], a[192:195], v[64:67]\n" \
R_"a[192:195], v29 offset:16384\n" \
"s_or_b32 s4, s40, s4\n" \
M_"v[80:83], a[188:191], a[132:135], v[80:83]\n" \
"s_movk_i32 s84, 0x3a80\n" \
M_"v[80:83], a[252:255], a[196:199], v[80:83]\n" \
B_"v22, s[64:67], s84 offen offset:2048 lds\n" \
M_"v[76:79], a[184:187], a[132:135], v[76:79]\n" \
"s_movk_i32 s84, 0x5680\n" \
M_"v[76:79], a[248:251], a[196:199], v[76:79]\n" \
B_"v23, s[64:67], s84 offen offset:3072 lds\n" \
M_"v[72:75], a[180:183], a[132:135], v[72:75]\n" \
X_"a[224:225], v32 offset:8192\n" \
"v_lshl_or_b32 v25, v212, 4, v214\n" \
M_"v[72:75], a[244:247], a[196:199], v[72:75]\n" \
X_"a[226:227], v32 offset:9216\n" \
"v_mul_u32_u24_e32 v212, 0x480, v217\n" \
M_"v[68:71], a[176:179], a[132:135], v[68:71]\n" \
R_"a[132:135], v28 offset:18432\n" \
"v_bitop3_b32 v213, v215, v213, 7 bitop3:0x6c\n" \
M_"v[68:71], a[240:243], a[196:199], v[68:71]\n" \
R_"a[196:199], v29 offset:18432\n" \
"s_or_b32 s40, s7, 0x14000\n" \
M_"v[84:87], a[176:179], a[136:139], v[84:87]\n" \
"s_add_u32 m0, s31, 0x8000\n" \
"s_mov_b32 s84, 0x280000\n" \
M_"v[84:87], a[240:243], a[200:203], v[84:87]\n" \
B_"v26, s[68:71], s84 offen offset:0 lds\n" \
M_"v[88:91], a[180:183], a[136:139], v[88:91]\n" \
"s_addk_i32 s84, 0x7c00\n" \
M_"v[88:91], a[244:247], a[200:203], v[88:91]\n" \
B_"v26, s[68:71], s84 offen offset:1024 lds\n" \
M_"v[92:95], a[184:187], a[136:139], v[92:95]\n" \
"s_addk_i32 s84, 0x7c00\n" \
M_"v[92:95], a[248:251], a[200:203], v[92:95]\n" \
B_"v27, s[68:71], s84 offen offset:2048 lds\n" \
M_"v[96:99], a[188:191], a[136:139], v[96:99]\n" \
R_"a[136:139], v28 offset:20480\n" \
"s_lshl_b32 s43, s44, 13\n" \
M_"v[96:99], a[252:255], a[200:203], v[96:99]\n" \
R_"a[200:203], v29 offset:20480\n" \
"s_mulk_i32 s44, 0xe800\n" \
M_"v[112:115], a[188:191], a[140:143], v[112:115]\n" \
"s_addk_i32 s84, 0x7c00\n" \
M_"v[112:115], a[252:255], a[204:207], v[112:115]\n" \
B_"v27, s[68:71], s84 offen offset:3072 lds\n" \
M_"v[108:111], a[184:187], a[140:143], v[108:111]\n" \
"s_add_u32 m0, s30, 0x9000\n" \
"s_mov_b32 s84, 0x8280\n" \
M_"v[108:111], a[248:251], a[204:207], v[108:111]\n" \
B_"v22, s[64:67], s84 offen offset:0 lds\n" \
M_"v[104:107], a[180:183], a[140:143], v[104:107]\n" \
X_"a[164:165], v33 offset:0\n" \
"s_lshl_b32 s7, s3, 8\n" \
M_"v[104:107], a[244:247], a[204:207], v[104:107]\n" \
X_"a[166:167], v33 offset:1024\n" \
"v_lshl_or_b32 v24, v218, 4, v214\n" \
M_"v[100:103], a[176:179], a[140:143], v[100:103]\n" \
R_"a[140:143], v28 offset:22528\n" \
"v_lshl_or_b32 v21, v213, 4, v212\n" \
M_"v[100:103], a[240:243], a[204:207], v[100:103]\n" \
R_"a[204:207], v29 offset:22528\n" \
"s_add_i32 s44, s44, 0x17000\n" \
M_"v[116:119], a[176:179], a[144:147], v[116:119]\n" \
"s_addk_i32 s84, 0x1c00\n" \
M_"v[116:119], a[240:243], a[208:211], v[116:119]\n" \
B_"v23, s[64:67], s84 offen offset:1024 lds\n" \
M_"v[120:123], a[180:183], a[144:147], v[120:123]\n" \
"s_addk_i32 s84, 0x1c00\n" \
M_"v[120:123], a[244:247], a[208:211], v[120:123]\n" \
B_"v22, s[64:67], s84 offen offset:2048 lds\n" \
M_"v[124:127], a[184:187], a[144:147], v[124:127]\n" \
"s_addk_i32 s84, 0x1c00\n" \
"s_nop 0\n" \
M_"v[124:127], a[248:251], a[208:211], v[124:127]\n" \
B_"v23, s[64:67], s84 offen offset:3072 lds\n" \
M_"v[128:131], a[188:191], a[144:147], v[128:131]\n" \
R_"a[144:147], v28 offset:0\n" \
"s_or_b32 s45, s7, 0x27000\n" \
M_"v[128:131], a[252:255], a[208:211], v[128:131]\n" \
R_"a[208:211], v29 offset:0\n" \
"s_or_b32 s46, s4, 0x10000\n" \
M_"v[144:147], a[188:191], a[148:151], v[144:147]\n" \
"s_add_u32 m0, s31, 0xc000\n" \
"s_mov_b32 s84, 0x280000\n" \
M_"v[144:147], a[252:255], a[212:215], v[144:147]\n" \
B_"v26, s[72:75], s84 offen offset:0 lds\n" \
M_"v[140:143], a[184:187], a[148:151], v[140:143]\n" \
"s_addk_i32 s84, 0x7c00\n" \
"s_nop 0\n" \
M_"v[140:143], a[248:251], a[212:215], v[140:143]\n" \
B_"v26, s[72:75], s84 offen offset:1024 lds\n" \
M_"v[136:139], a[180:183], a[148:151], v[136:139]\n" \
X_"a[228:229], v33 offset:8192\n" \
"s_add_i32 s48, s6, 0x3000000\n" \
M_"v[136:139], a[244:247], a[212:215], v[136:139]\n" \
X_"a[230:231], v33 offset:9216\n" \
"s_add_i32 s49, s5, 0x3004000\n" \
M_"v[132:135], a[176:179], a[148:151], v[132:135]\n" \
R_"a[148:151], v28 offset:2048\n" \
"s_add_i32 s52, s52, 0x1010000\n" \
M_"v[132:135], a[240:243], a[212:215], v[132:135]\n" \
R_"a[212:215], v29 offset:2048\n" \
M_"v[148:151], a[176:179], a[152:155], v[148:151]\n" \
R_"v[200:203], v199 offset:0\n" \
M_"v[148:151], a[240:243], a[216:219], v[148:151]\n" \
R_"v[204:207], v199 offset:4096\n" \
M_"v[152:155], a[180:183], a[152:155], v[152:155]\n" \
X_"a[168:169], v34 offset:0\n" \
M_"v[152:155], a[244:247], a[216:219], v[152:155]\n" \
X_"a[170:171], v34 offset:1024\n" \
M_"v[156:159], a[184:187], a[152:155], v[156:159]\n" \
R_"v[208:211], v199 offset:8192\n" \
M_"v[156:159], a[248:251], a[216:219], v[156:159]\n" \
X_"a[232:233], v34 offset:8192\n" \
M_"v[160:163], a[188:191], a[152:155], v[160:163]\n" \
R_"a[152:155], v28 offset:4096\n" \
M_"v[160:163], a[252:255], a[216:219], v[160:163]\n" \
R_"a[216:219], v29 offset:4096\n" \
M_"v[176:179], a[188:191], a[156:159], v[176:179]\n" \
X_"a[234:235], v34 offset:9216\n" \
M_"v[176:179], a[252:255], a[220:223], v[176:179]\n" \
X_"a[172:173], v35 offset:0\n" \
M_"v[172:175], a[184:187], a[156:159], v[172:175]\n" \
X_"a[174:175], v35 offset:1024\n" \
M_"v[172:175], a[248:251], a[220:223], v[172:175]\n" \
X_"a[236:237], v35 offset:8192\n" \
M_"v[168:171], a[180:183], a[156:159], v[168:171]\n" \
X_"a[238:239], v35 offset:9216\n" \
M_"v[168:171], a[244:247], a[220:223], v[168:171]\n" \
M_"v[164:167], a[176:179], a[156:159], v[164:167]\n" \
R_"a[156:159], v28 offset:6144\n" \
M_"v[164:167], a[240:243], a[220:223], v[164:167]\n" \
R_"a[220:223], v29 offset:6144\n" \
"s_waitcnt lgkmcnt(15)\n" \
M_"a[0:3], a[160:163], a[128:131], a[0:3]\n" \
"s_addk_i32 s84, 0x7c00\n" \
M_"a[0:3], a[224:227], a[192:195], a[0:3]\n" \
B_"v27, s[72:75], s84 offen offset:2048 lds\n" \
M_"a[4:7], a[164:167], a[128:131], a[4:7]\n" \
X_"a[176:177], v36 offset:0\n" \
M_"a[4:7], a[228:231], a[192:195], a[4:7]\n" \
"s_addk_i32 s84, 0x7c00\n" \
"s_waitcnt lgkmcnt(12)\n" \
M_"a[8:11], a[168:171], a[128:131], a[8:11]\n" \
B_"v27, s[72:75], s84 offen offset:3072 lds\n" \
"s_waitcnt lgkmcnt(7)\n" \
M_"a[8:11], a[232:235], a[192:195], a[8:11]\n" \
X_"a[178:179], v36 offset:1024\n" \
"s_waitcnt lgkmcnt(6)\n" \
M_"a[12:15], a[172:175], a[128:131], a[12:15]\n" \
X_"a[240:241], v36 offset:8192\n" \
"s_waitcnt lgkmcnt(5)\n" \
"s_nop 0\n" \
M_"a[12:15], a[236:239], a[192:195], a[12:15]\n" \
M_"a[28:31], a[172:175], a[132:135], a[28:31]\n" \
M_"a[28:31], a[236:239], a[196:199], a[28:31]\n" \
X_"a[242:243], v36 offset:9216\n" \
M_"a[24:27], a[168:171], a[132:135], a[24:27]\n" \
M_"a[24:27], a[232:235], a[196:199], a[24:27]\n" \
M_"a[20:23], a[164:167], a[132:135], a[20:23]\n" \
".fill (L_pb + 12000 - .) / 4, 4, 0xBF800000\n" \
"L_pp3:\n" \
"s_cbranch_execz L_pret\n" \
"s_nop 0\n" \
M_"a[20:23], a[228:231], a[196:199], a[20:23]\n" \
X_"a[180:181], v37 offset:0\n" \
M_"a[16:19], a[160:163], a[132:135], a[16:19]\n" \
M_"a[16:19], a[224:227], a[196:199], a[16:19]\n" \
M_"a[32:35], a[160:163], a[136:139], a[32:35]\n" \
X_"a[182:183], v37 offset:1024\n" \
M_"a[32:35], a[224:227], a[200:203], a[32:35]\n" \
M_"a[36:39], a[164:167], a[136:139], a[36:39]\n" \
M_"a[36:39], a[228:231], a[200:203], a[36:39]\n" \
X_"a[244:245], v37 offset:8192\n" \
M_"a[40:43], a[168:171], a[136:139], a[40:43]\n" \
"s_mov_b32 m0, s32\n" \
M_"a[40:43], a[232:235], a[200:203], a[40:43]\n" \
"s_mov_b32 exec_lo, 0x0\n" \
"s_mov_b32 exec_hi, 0xffffffff\n" \
"global_load_lds_dword v[188:189], off\n" \
"s_mov_b64 exec, -1\n" \
M_"a[44:47], a[172:175], a[136:139], a[44:47]\n" \
"buffer_store_dwordx4 v[200:203], v20, s[80:83], s35 offen sc1\n" \
M_"a[44:47], a[236:239], a[200:203], a[44:47]\n" \
X_"a[246:247], v37 offset:9216\n" \
M_"a[60:63], a[172:175], a[140:143], a[60:63]\n" \
M_"a[60:63], a[236:239], a[204:207], a[60:63]\n" \
M_"a[56:59], a[168:171], a[140:143], a[56:59]\n" \
X_"a[184:185], v38 offset:0\n" \
M_"a[56:59], a[232:235], a[204:207], a[56:59]\n" \
M_"a[52:55], a[164:167], a[140:143], a[52:55]\n" \
M_"a[52:55], a[228:231], a[204:207], a[52:55]\n" \
X_"a[186:187], v38 offset:1024\n" \
M_"a[48:51], a[160:163], a[140:143], a[48:51]\n" \
M_"a[48:51], a[224:227], a[204:207], a[48:51]\n" \
M_"a[64:67], a[160:163], a[144:147], a[64:67]\n" \
X_"a[248:249], v38 offset:8192\n" \
M_"a[64:67], a[224:227], a[208:211], a[64:67]\n" \
"buffer_store_dwordx4 v[204:207], v20, s[80:83], s88 offen sc1\n" \
M_"a[68:71], a[164:167], a[144:147], a[68:71]\n" \
M_"a[68:71], a[228:231], a[208:211], a[68:71]\n" \
X_"a[250:251], v38 offset:9216\n" \
M_"a[72:75], a[168:171], a[144:147], a[72:75]\n" \
M_"a[72:75], a[232:235], a[208:211], a[72:75]\n" \
M_"a[76:79], a[172:175], a[144:147], a[76:79]\n" \
M_"a[76:79], a[236:239], a[208:211], a[76:79]\n" \
X_"a[188:189], v39 offset:0\n" \
M_"a[92:95], a[172:175], a[148:151], a[92:95]\n" \
M_"a[92:95], a[236:239], a[212:215], a[92:95]\n" \
M_"a[88:91], a[168:171], a[148:151], a[88:91]\n" \
X_"a[190:191], v39 offset:1024\n" \
M_"a[88:91], a[232:235], a[212:215], a[88:91]\n" \
M_"a[84:87], a[164:167], a[148:151], a[84:87]\n" \
"buffer_store_dwordx4 v[208:211], v20, s[80:83], s36 offen sc1\n" \
M_"a[84:87], a[228:231], a[212:215], a[84:87]\n" \
X_"a[252:253], v39 offset:8192\n" \
M_"a[80:83], a[160:163], a[148:151], a[80:83]\n" \
M_"a[80:83], a[224:227], a[212:215], a[80:83]\n" \
M_"a[96:99], a[160:163], a[152:155], a[96:99]\n" \
X_"a[254:255], v39 offset:9216\n" \
M_"a[96:99], a[224:227], a[216:219], a[96:99]\n" \
M_"a[100:103], a[164:167], a[152:155], a[100:103]\n" \
M_"a[100:103], a[228:231], a[216:219], a[100:103]\n" \
M_"a[104:107], a[168:171], a[152:155], a[104:107]\n" \
M_"a[104:107], a[232:235], a[216:219], a[104:107]\n" \
M_"a[108:111], a[172:175], a[152:155], a[108:111]\n" \
M_"a[108:111], a[236:239], a[216:219], a[108:111]\n" \
"s_waitcnt lgkmcnt(15)\n" \
M_"a[124:127], a[172:175], a[156:159], a[124:127]\n" \
M_"a[124:127], a[236:239], a[220:223], a[124:127]\n" \
M_"a[120:123], a[168:171], a[156:159], a[120:123]\n" \
M_"a[120:123], a[232:235], a[220:223], a[120:123]\n" \
M_"a[116:119], a[164:167], a[156:159], a[116:119]\n" \
M_"a[116:119], a[228:231], a[220:223], a[116:119]\n" \
M_"a[112:115], a[160:163], a[156:159], a[112:115]\n" \
"s_waitcnt vmcnt(4) lgkmcnt(0)\n" \
M_"a[112:115], a[224:227], a[220:223], a[112:115]\n" \
"s_barrier\n" \
M_"v[52:55], a[176:179], a[128:131], v[52:55]\n" \
"s_add_u32 m0, s30, 0x0\n" \
"s_movk_i32 s84, 0x300\n" \
M_"v[52:55], a[240:243], a[192:195], v[52:55]\n" \
B_"v22, s[64:67], s84 offen offset:0 lds\n" \
M_"v[56:59], a[180:183], a[128:131], v[56:59]\n" \
X_"a[160:161], v32 offset:32768\n" \
M_"v[56:59], a[244:247], a[192:195], v[56:59]\n" \
X_"a[162:163], v32 offset:33792\n" \
M_"v[60:63], a[184:187], a[128:131], v[60:63]\n" \
"s_movk_i32 s84, 0x1f00\n" \
M_"v[60:63], a[248:251], a[192:195], v[60:63]\n" \
B_"v23, s[64:67], s84 offen offset:1024 lds\n" \
M_"v[64:67], a[188:191], a[128:131], v[64:67]\n" \
R_"a[128:131], v28 offset:49152\n" \
M_"v[64:67], a[252:255], a[192:195], v[64:67]\n" \
R_"a[192:195], v29 offset:49152\n" \
M_"v[80:83], a[188:191], a[132:135], v[80:83]\n" \
"s_movk_i32 s84, 0x3b00\n" \
M_"v[80:83], a[252:255], a[196:199], v[80:83]\n" \
B_"v22, s[64:67], s84 offen offset:2048 lds\n" \
M_"v[76:79], a[184:187], a[132:135], v[76:79]\n" \
X_"a[224:225], v32 offset:40960\n" \
M_"v[76:79], a[248:251], a[196:199], v[76:79]\n" \
X_"a[226:227], v32 offset:41984\n" \
M_"v[72:75], a[180:183], a[132:135], v[72:75]\n" \
"s_movk_i32 s84, 0x5700\n" \
M_"v[72:75], a[244:247], a[196:199], v[72:75]\n" \
B_"v23, s[64:67], s84 offen offset:3072 lds\n" \
M_"v[68:71], a[176:179], a[132:135], v[68:71]\n" \
R_"a[132:135], v28 offset:51200\n" \
M_"v[68:71], a[240:243], a[196:199], v[68:71]\n" \
R_"a[196:199], v29 offset:51200\n" \
M_"v[84:87], a[176:179], a[136:139], v[84:87]\n" \
"s_add_u32 m0, s31, 0x0\n" \
"s_mov_b32 s84, 0x300000\n" \
M_"v[84:87], a[240:243], a[200:203], v[84:87]\n" \
B_"v26, s[68:71], s84 offen offset:0 lds\n" \
M_"v[88:91], a[180:183], a[136:139], v[88:91]\n" \
X_"a[164:165], v33 offset:32768\n" \
M_"v[88:91], a[244:247], a[200:203], v[88:91]\n" \
X_"a[166:167], v33 offset:33792\n" \
M_"v[92:95], a[184:187], a[136:139], v[92:95]\n" \
"s_addk_i32 s84, 0x7c00\n" \
M_"v[92:95], a[248:251], a[200:203], v[92:95]\n" \
B_"v26, s[68:71], s84 offen offset:1024 lds\n" \
M_"v[96:99], a[188:191], a[136:139], v[96:99]\n" \
R_"a[136:139], v28 offset:53248\n" \
M_"v[96:99], a[252:255], a[200:203], v[96:99]\n" \
R_"a[200:203], v29 offset:53248\n" \
M_"v[112:115], a[188:191], a[140:143], v[112:115]\n" \
"s_addk_i32 s84, 0x7c00\n" \
M_"v[112:115], a[252:255], a[204:207], v[112:115]\n" \
B_"v27, s[68:71], s84 offen offset:2048 lds\n" \
M_"v[108:111], a[184:187], a[140:143], v[108:111]\n" \
X_"a[228:229], v33 offset:40960\n" \
M_"v[108:111], a[248:251], a[204:207], v[108:111]\n" \
X_"a[230:231], v33 offset:41984\n" \
M_"v[104:107], a[180:183], a[140:143], v[104:107]\n" \
"s_addk_i32 s84, 0x7c00\n" \
M_"v[104:107], a[244:247], a[204:207], v[104:107]\n" \
B_"v27, s[68:71], s84 offen offset:3072 lds\n" \
M_"v[100:103], a[176:179], a[140:143], v[100:103]\n" \
R_"a[140:143], v28 offset:55296\n" \
M_"v[100:103], a[240:243], a[204:207], v[100:103]\n" \
R_"a[204:207], v29 offset:55296\n" \
M_"v[116:119], a[176:179], a[144:147], v[116:119]\n" \
"s_add_u32 m0, s30, 0x1000\n" \
"s_mov_b32 s84, 0x8300\n" \
M_"v[116:119], a[240:243], a[208:211], v[116:119]\n" \
B_"v22, s[64:67], s84 offen offset:0 lds\n" \
M_"v[120:123], a[180:183], a[144:147], v[120:123]\n" \
X_"a[168:169], v34 offset:32768\n" \
M_"v[120:123], a[244:247], a[208:211], v[120:123]\n" \
X_"a[170:171], v34 offset:33792\n" \
M_"v[124:127], a[184:187], a[144:147], v[124:127]\n" \
"s_addk_i32 s84, 0x1c00\n" \
M_"v[124:127], a[248:251], a[208:211], v[124:127]\n" \
B_"v23, s[64:67], s84 offen offset:1024 lds\n" \
M_"v[128:131], a[188:191], a[144:147], v[128:131]\n" \
R_"a[144:147], v28 offset:32768\n" \
M_"v[128:131], a[252:255], a[208:211], v[128:131]\n" \
R_"a[208:211], v29 offset:32768\n" \
M_"v[144:147], a[188:191], a[148:151], v[144:147]\n" \
"s_addk_i32 s84, 0x1c00\n" \
M_"v[144:147], a[252:255], a[212:215], v[144:147]\n" \
B_"v22, s[64:67], s84 offen offset:2048 lds\n" \
M_"v[140:143], a[184:187], a[148:151], v[140:143]\n" \
X_"a[232:233], v34 offset:40960\n" \
M_"v[140:143], a[248:251], a[212:215], v[140:143]\n" \
X_"a[234:235], v34 offset:41984\n" \
M_"v[136:139], a[180:183], a[148:151], v[136:139]\n" \
"s_addk_i32 s84, 0x1c00\n" \
"s_nop 0\n" \
M_"v[136:139], a[244:247], a[212:215], v[136:139]\n" \
B_"v23, s[64:67], s84 offen offset:3072 lds\n" \
M_"v[132:135], a[176:179], a[148:151], v[132:135]\n" \
R_"a[148:151], v28 offset:34816\n" \
M_"v[132:135], a[240:243], a[212:215], v[132:135]\n" \
R_"a[212:215], v29 offset:34816\n" \
M_"v[148:151], a[176:179], a[152:155], v[148:151]\n" \
"s_add_u32 m0, s31, 0x4000\n" \
"s_mov_b32 s84, 0x300000\n" \
M_"v[148:151], a[240:243], a[216:219], v[148:151]\n" \
B_"v26, s[72:75], s84 offen offset:0 lds\n" \
M_"v[152:155], a[180:183], a[152:155], v[152:155]\n" \
X_"a[172:173], v35 offset:32768\n" \
M_"v[152:155], a[244:247], a[216:219], v[152:155]\n" \
X_"a[174:175], v35 offset:33792\n" \
M_"v[156:159], a[184:187], a[152:155], v[156:159]\n" \
"s_addk_i32 s84, 0x7c00\n" \
"s_nop 0\n" \
M_"v[156:159], a[248:251], a[216:219], v[156:159]\n" \
B_"v26, s[72:75], s84 offen offset:1024 lds\n" \
M_"v[160:163], a[188:191], a[152:155], v[160:163]\n" \
R_"a[152:155], v28 offset:36864\n" \
M_"v[160:163], a[252:255], a[216:219], v[160:163]\n" \
R_"a[216:219], v29 offset:36864\n" \
M_"v[176:179], a[188:191], a[156:159], v[176:179]\n" \
X_"a[236:237], v35 offset:40960\n" \
M_"v[176:179], a[252:255], a[220:223], v[176:179]\n" \
X_"a[238:239], v35 offset:41984\n" \
M_"v[172:175], a[184:187], a[156:159], v[172:175]\n" \
M_"v[172:175], a[248:251], a[220:223], v[172:175]\n" \
M_"v[168:171], a[180:183], a[156:159], v[168:171]\n" \
M_"v[168:171], a[244:247], a[220:223], v[168:171]\n" \
M_"v[164:167], a[176:179], a[156:159], v[164:167]\n" \
R_"a[156:159], v28 offset:38912\n" \
M_"v[164:167], a[240:243], a[220:223], v[164:167]\n" \
R_"a[220:223], v29 offset:38912\n" \
"s_waitcnt lgkmcnt(15)\n" \
M_"a[0:3], a[160:163], a[128:131], a[0:3]\n" \
"s_addk_i32 s84, 0x7c00\n" \
M_"a[0:3], a[224:227], a[192:195], a[0:3]\n" \
B_"v27, s[72:75], s84 offen offset:2048 lds\n" \
M_"a[4:7], a[164:167], a[128:131], a[4:7]\n" \
X_"a[176:177], v36 offset:32768\n" \
M_"a[4:7], a[228:231], a[192:195], a[4:7]\n" \
"s_addk_i32 s84, 0x7c00\n" \
"s_waitcnt lgkmcnt(15)\n" \
M_"a[8:11], a[168:171], a[128:131], a[8:11]\n" \
B_"v27, s[72:75], s84 offen offset:3072 lds\n" \
"s_waitcnt lgkmcnt(11)\n" \
M_"a[8:11], a[232:235], a[192:195], a[8:11]\n" \
X_"a[178:179], v36 offset:33792\n" \
"s_waitcnt lgkmcnt(8)\n" \
M_"a[12:15], a[172:175], a[128:131], a[12:15]\n" \
X_"a[240:241], v36 offset:40960\n" \
"s_waitcnt lgkmcnt(5)\n" \
"s_nop 0\n" \
M_"a[12:15], a[236:239], a[192:195], a[12:15]\n" \
M_"a[28:31], a[172:175], a[132:135], a[28:31]\n" \
M_"a[28:31], a[236:239], a[196:199], a[28:31]\n" \
X_"a[242:243], v36 offset:41984\n" \
M_"a[24:27], a[168:171], a[132:135], a[24:27]\n" \
M_"a[24:27], a[232:235], a[196:199], a[24:27]\n" \
M_"a[20:23], a[164:167], a[132:135], a[20:23]\n" \
M_"a[20:23], a[228:231], a[196:199], a[20:23]\n" \
X_"a[180:181], v37 offset:32768\n" \
M_"a[16:19], a[160:163], a[132:135], a[16:19]\n" \
M_"a[16:19], a[224:227], a[196:199], a[16:19]\n" \
M_"a[32:35], a[160:163], a[136:139], a[32:35]\n" \
X_"a[182:183], v37 offset:33792\n" \
M_"a[32:35], a[224:227], a[200:203], a[32:35]\n" \
M_"a[36:39], a[164:167], a[136:139], a[36:39]\n" \
M_"a[36:39], a[228:231], a[200:203], a[36:39]\n" \
X_"a[244:245], v37 offset:40960\n" \
M_"a[40:43], a[168:171], a[136:139], a[40:43]\n" \
M_"a[40:43], a[232:235], a[200:203], a[40:43]\n" \
M_"a[44:47], a[172:175], a[136:139], a[44:47]\n" \
X_"a[246:247], v37 offset:41984\n" \
M_"a[44:47], a[236:239], a[200:203], a[44:47]\n" \
M_"a[60:63], a[172:175], a[140:143], a[60:63]\n" \
M_"a[60:63], a[236:239], a[204:207], a[60:63]\n" \
M_"a[56:59], a[168:171], a[140:143], a[56:59]\n" \
X_"a[184:185], v38 offset:32768\n" \
M_"a[56:59], a[232:235], a[204:207], a[56:59]\n" \
M_"a[52:55], a[164:167], a[140:143], a[52:55]\n" \
M_"a[52:55], a[228:231], a[204:207], a[52:55]\n" \
X_"a[186:187], v38 offset:33792\n" \
M_"a[48:51], a[160:163], a[140:143], a[48:51]\n" \
M_"a[48:51], a[224:227], a[204:207], a[48:51]\n" \
M_"a[64:67], a[160:163], a[144:147], a[64:67]\n" \
X_"a[248:249], v38 offset:40960\n" \
M_"a[64:67], a[224:227], a[208:211], a[64:67]\n" \
M_"a[68:71], a[164:167], a[144:147], a[68:71]\n" \
M_"a[68:71], a[228:231], a[208:211], a[68:71]\n" \
X_"a[250:251], v38 offset:41984\n" \
M_"a[72:75], a[168:171], a[144:147], a[72:75]\n" \
M_"a[72:75], a[232:235], a[208:211], a[72:75]\n" \
M_"a[76:79], a[172:175], a[144:147], a[76:79]\n" \
M_"a[76:79], a[236:239], a[208:211], a[76:79]\n" \
X_"a[188:189], v39 offset:32768\n" \
M_"a[92:95], a[172:175], a[148:151], a[92:95]\n" \
M_"a[92:95], a[236:239], a[212:215], a[92:95]\n" \
M_"a[88:91], a[168:171], a[148:151], a[88:91]\n" \
X_"a[190:191], v39 offset:33792\n" \
M_"a[88:91], a[232:235], a[212:215], a[88:91]\n" \
M_"a[84:87], a[164:167], a[148:151], a[84:87]\n" \
M_"a[84:87], a[228:231], a[212:215], a[84:87]\n" \
X_"a[252:253], v39 offset:40960\n" \
M_"a[80:83], a[160:163], a[148:151], a[80:83]\n" \
M_"a[80:83], a[224:227], a[212:215], a[80:83]\n" \
M_"a[96:99], a[160:163], a[152:155], a[96:99]\n" \
X_"a[254:255], v39 offset:41984\n" \
M_"a[96:99], a[224:227], a[216:219], a[96:99]\n" \
M_"a[100:103], a[164:167], a[152:155], a[100:103]\n" \
M_"a[100:103], a[228:231], a[216:219], a[100:103]\n" \
M_"a[104:107], a[168:171], a[152:155], a[104:107]\n" \
M_"a[104:107], a[232:235], a[216:219], a[104:107]\n" \
M_"a[108:111], a[172:175], a[152:155], a[108:111]\n" \
M_"a[108:111], a[236:239], a[216:219], a[108:111]\n" \
"s_waitcnt lgkmcnt(15)\n" \
M_"a[124:127], a[172:175], a[156:159], a[124:127]\n" \
M_"a[124:127], a[236:239], a[220:223], a[124:127]\n" \
M_"a[120:123], a[168:171], a[156:159], a[120:123]\n" \
M_"a[120:123], a[232:235], a[220:223], a[120:123]\n" \
M_"a[116:119], a[164:167], a[156:159], a[116:119]\n" \
M_"a[116:119], a[228:231], a[220:223], a[116:119]\n" \
M_"a[112:115], a[160:163], a[156:159], a[112:115]\n" \
"s_waitcnt vmcnt(0) lgkmcnt(0)\n" \
M_"a[112:115], a[224:227], a[220:223], a[112:115]\n" \
"s_barrier\n" \
"v_mov_b32_e32 v45, s62\n" \
"s_mov_b64 exec, 1\n" \
"buffer_store_dword v45, off, s[80:83], s37 offset:128 sc1\n" \
"s_mov_b64 exec, -1\n" \
M_"v[52:55], a[176:179], a[128:131], v[52:55]\n" \
"s_add_u32 m0, s30, 0x8000\n" \
"s_movk_i32 s84, 0x380\n" \
M_"v[52:55], a[240:243], a[192:195], v[52:55]\n" \
B_"v22, s[64:67], s84 offen offset:0 lds\n" \
M_"v[56:59], a[180:183], a[128:131], v[56:59]\n" \
X_"a[160:161], v32 offset:0\n" \
M_"v[56:59], a[244:247], a[192:195], v[56:59]\n" \
X_"a[162:163], v32 offset:1024\n" \
M_"v[60:63], a[184:187], a[128:131], v[60:63]\n" \
"s_movk_i32 s84, 0x1f80\n" \
M_"v[60:63], a[248:251], a[192:195], v[60:63]\n" \
B_"v23, s[64:67], s84 offen offset:1024 lds\n" \
M_"v[64:67], a[188:191], a[128:131], v[64:67]\n" \
R_"a[128:131], v28 offset:16384\n" \
M_"v[64:67], a[252:255], a[192:195], v[64:67]\n" \
R_"a[192:195], v29 offset:16384\n" \
M_"v[80:83], a[188:191], a[132:135], v[80:83]\n" \
"s_movk_i32 s84, 0x3b80\n" \
M_"v[80:83], a[252:255], a[196:199], v[80:83]\n" \
B_"v22, s[64:67], s84 offen offset:2048 lds\n" \
M_"v[76:79], a[184:187], a[132:135], v[76:79]\n" \
X_"a[224:225], v32 offset:8192\n" \
M_"v[76:79], a[248:251], a[196:199], v[76:79]\n" \
X_"a[226:227], v32 offset:9216\n" \
M_"v[72:75], a[180:183], a[132:135], v[72:75]\n" \
"s_movk_i32 s84, 0x5780\n" \
M_"v[72:75], a[244:247], a[196:199], v[72:75]\n" \
B_"v23, s[64:67], s84 offen offset:3072 lds\n" \
M_"v[68:71], a[176:179], a[132:135], v[68:71]\n" \
R_"a[132:135], v28 offset:18432\n" \
M_"v[68:71], a[240:243], a[196:199], v[68:71]\n" \
R_"a[196:199], v29 offset:18432\n" \
M_"v[84:87], a[176:179], a[136:139], v[84:87]\n" \
"s_add_u32 m0, s31, 0x8000\n" \
"s_mov_b32 s84, 0x380000\n" \
M_"v[84:87], a[240:243], a[200:203], v[84:87]\n" \
B_"v26, s[68:71], s84 offen offset:0 lds\n" \
M_"v[88:91], a[180:183], a[136:139], v[88:91]\n" \
X_"a[164:165], v33 offset:0\n" \
M_"v[88:91], a[244:247], a[200:203], v[88:91]\n" \
X_"a[166:167], v33 offset:1024\n" \
M_"v[92:95], a[184:187], a[136:139], v[92:95]\n" \
"s_addk_i32 s84, 0x7c00\n" \
M_"v[92:95], a[248:251], a[200:203], v[92:95]\n" \
B_"v26, s[68:71], s84 offen offset:1024 lds\n" \
M_"v[96:99], a[188:191], a[136:139], v[96:99]\n" \
R_"a[136:139], v28 offset:20480\n" \
M_"v[96:99], a[252:255], a[200:203], v[96:99]\n" \
R_"a[200:203], v29 offset:20480\n" \
M_"v[112:115], a[188:191], a[140:143], v[112:115]\n" \
"s_addk_i32 s84, 0x7c00\n" \
M_"v[112:115], a[252:255], a[204:207], v[112:115]\n" \
B_"v27, s[68:71], s84 offen offset:2048 lds\n" \
M_"v[108:111], a[184:187], a[140:143], v[108:111]\n" \
X_"a[228:229], v33 offset:8192\n" \
M_"v[108:111], a[248:251], a[204:207], v[108:111]\n" \
X_"a[230:231], v33 offset:9216\n" \
M_"v[104:107], a[180:183], a[140:143], v[104:107]\n" \
"s_addk_i32 s84, 0x7c00\n" \
"s_nop 0\n" \
M_"v[104:107], a[244:247], a[204:207], v[104:107]\n" \
B_"v27, s[68:71], s84 offen offset:3072 lds\n" \
M_"v[100:103], a[176:179], a[140:143], v[100:103]\n" \
R_"a[140:143], v28 offset:22528\n" \
M_"v[100:103], a[240:243], a[204:207], v[100:103]\n" \
R_"a[204:207], v29 offset:22528\n" \
M_"v[116:119], a[176:179], a[144:147], v[116:119]\n" \
"s_add_u32 m0, s30, 0x9000\n" \
"s_mov_b32 s84, 0x8380\n" \
M_"v[116:119], a[240:243], a[208:211], v[116:119]\n" \
B_"v22, s[64:67], s84 offen offset:0 lds\n" \
M_"v[120:123], a[180:183], a[144:147], v[120:123]\n" \
X_"a[168:169], v34 offset:0\n" \
M_"v[120:123], a[244:247], a[208:211], v[120:123]\n" \
X_"a[170:171], v34 offset:1024\n" \
M_"v[124:127], a[184:187], a[144:147], v[124:127]\n" \
"s_addk_i32 s84, 0x1c00\n" \
M_"v[124:127], a[248:251], a[208:211], v[124:127]\n" \
B_"v23, s[64:67], s84 offen offset:1024 lds\n" \
M_"v[128:131], a[188:191], a[144:147], v[128:131]\n" \
R_"a[144:147], v28 offset:0\n" \
M_"v[128:131], a[252:255], a[208:211], v[128:131]\n" \
R_"a[208:211], v29 offset:0\n" \
M_"v[144:147], a[188:191], a[148:151], v[144:147]\n" \
"s_addk_i32 s84, 0x1c00\n" \
M_"v[144:147], a[252:255], a[212:215], v[144:147]\n" \
B_"v22, s[64:67], s84 offen offset:2048 lds\n" \
M_"v[140:143], a[184:187], a[148:151], v[140:143]\n" \
X_"a[232:233], v34 offset:8192\n" \
M_"v[140:143], a[248:251], a[212:215], v[140:143]\n" \
X_"a[234:235], v34 offset:9216\n" \
M_"v[136:139], a[180:183], a[148:151], v[136:139]\n" \
"s_addk_i32 s84, 0x1c00\n" \
"s_nop 0\n" \
M_"v[136:139], a[244:247], a[212:215], v[136:139]\n" \
B_"v23, s[64:67], s84 offen offset:3072 lds\n" \
M_"v[132:135], a[176:179], a[148:151], v[132:135]\n" \
R_"a[148:151], v28 offset:2048\n" \
M_"v[132:135], a[240:243], a[212:215], v[132:135]\n" \
R_"a[212:215], v29 offset:2048\n" \
M_"v[148:151], a[176:179], a[152:155], v[148:151]\n" \
"s_add_u32 m0, s31, 0xc000\n" \
"s_mov_b32 s84, 0x380000\n" \
M_"v[148:151], a[240:243], a[216:219], v[148:151]\n" \
B_"v26, s[72:75], s84 offen offset:0 lds\n" \
M_"v[152:155], a[180:183], a[152:155], v[152:155]\n" \
X_"a[172:173], v35 offset:0\n" \
M_"v[152:155], a[244:247], a[216:219], v[152:155]\n" \
X_"a[174:175], v35 offset:1024\n" \
M_"v[156:159], a[184:187], a[152:155], v[156:159]\n" \
"s_addk_i32 s84, 0x7c00\n" \
"s_nop 0\n" \
M_"v[156:159], a[248:251], a[216:219], v[156:159]\n" \
B_"v26, s[72:75], s84 offen offset:1024 lds\n" \
M_"v[160:163], a[188:191], a[152:155], v[160:163]\n" \
R_"a[152:155], v28 offset:4096\n" \
M_"v[160:163], a[252:255], a[216:219], v[160:163]\n" \
R_"a[216:219], v29 offset:4096\n" \
M_"v[176:179], a[188:191], a[156:159], v[176:179]\n" \
X_"a[236:237], v35 offset:8192\n" \
M_"v[176:179], a[252:255], a[220:223], v[176:179]\n" \
X_"a[238:239], v35 offset:9216\n" \
M_"v[172:175], a[184:187], a[156:159], v[172:175]\n" \
M_"v[172:175], a[248:251], a[220:223], v[172:175]\n" \
M_"v[168:171], a[180:183], a[156:159], v[168:171]\n" \
M_"v[168:171], a[244:247], a[220:223], v[168:171]\n" \
M_"v[164:167], a[176:179], a[156:159], v[164:167]\n" \
R_"a[156:159], v28 offset:6144\n" \
M_"v[164:167], a[240:243], a[220:223], v[164:167]\n" \
R_"a[220:223], v29 offset:6144\n" \
"s_waitcnt lgkmcnt(15)\n" \
M_"a[0:3], a[160:163], a[128:131], a[0:3]\n" \
"s_addk_i32 s84, 0x7c00\n" \
M_"a[0:3], a[224:227], a[192:195], a[0:3]\n" \
B_"v27, s[72:75], s84 offen offset:2048 lds\n" \
M_"a[4:7], a[164:167], a[128:131], a[4:7]\n" \
X_"a[176:177], v36 offset:0\n" \
M_"a[4:7], a[228:231], a[192:195], a[4:7]\n" \
"s_addk_i32 s84, 0x7c00\n" \
"s_waitcnt lgkmcnt(15)\n" \
M_"a[8:11], a[168:171], a[128:131], a[8:11]\n" \
B_"v27, s[72:75], s84 offen offset:3072 lds\n" \
"s_waitcnt lgkmcnt(11)\n" \
M_"a[8:11], a[232:235], a[192:195], a[8:11]\n" \
X_"a[178:179], v36 offset:1024\n" \
"s_waitcnt lgkmcnt(8)\n" \
M_"a[12:15], a[172:175], a[128:131], a[12:15]\n" \
X_"a[240:241], v36 offset:8192\n" \
"s_waitcnt lgkmcnt(5)\n" \
"s_nop 0\n" \
M_"a[12:15], a[236:239], a[192:195], a[12:15]\n" \
M_"a[28:31], a[172:175], a[132:135], a[28:31]\n" \
M_"a[28:31], a[236:239], a[196:199], a[28:31]\n" \
X_"a[242:243], v36 offset:9216\n" \
M_"a[24:27], a[168:171], a[132:135], a[24:27]\n" \
M_"a[24:27], a[232:235], a[196:199], a[24:27]\n" \
M_"a[20:23], a[164:167], a[132:135], a[20:23]\n" \
M_"a[20:23], a[228:231], a[196:199], a[20:23]\n" \
X_"a[180:181], v37 offset:0\n" \
M_"a[16:19], a[160:163], a[132:135], a[16:19]\n" \
M_"a[16:19], a[224:227], a[196:199], a[16:19]\n" \
M_"a[32:35], a[160:163], a[136:139], a[32:35]\n" \
X_"a[182:183], v37 offset:1024\n" \
M_"a[32:35], a[224:227], a[200:203], a[32:35]\n" \
M_"a[36:39], a[164:167], a[136:139], a[36:39]\n" \
"v_bfe_u32 v44, v20, 2, 5\n" \
M_"a[36:39], a[228:231], a[200:203], a[36:39]\n" \
"buffer_load_dword v44, v44, s[80:83], s38 offen offset:128 sc1\n" \
M_"a[40:43], a[168:171], a[136:139], a[40:43]\n" \
X_"a[244:245], v37 offset:8192\n" \
M_"a[40:43], a[232:235], a[200:203], a[40:43]\n" \
M_"a[44:47], a[172:175], a[136:139], a[44:47]\n" \
X_"a[246:247], v37 offset:9216\n" \
M_"a[44:47], a[236:239], a[200:203], a[44:47]\n" \
M_"a[60:63], a[172:175], a[140:143], a[60:63]\n" \
M_"a[60:63], a[236:239], a[204:207], a[60:63]\n" \
M_"a[56:59], a[168:171], a[140:143], a[56:59]\n" \
X_"a[184:185], v38 offset:0\n" \
M_"a[56:59], a[232:235], a[204:207], a[56:59]\n" \
M_"a[52:55], a[164:167], a[140:143], a[52:55]\n" \
"global_store_dwordx4 v49, v[0:3], s[28:29] offset:0 sc1\n" \
M_"a[52:55], a[228:231], a[204:207], a[52:55]\n" \
X_"a[186:187], v38 offset:1024\n" \
M_"a[48:51], a[160:163], a[140:143], a[48:51]\n" \
M_"a[48:51], a[224:227], a[204:207], a[48:51]\n" \
M_"a[64:67], a[160:163], a[144:147], a[64:67]\n" \
X_"a[248:249], v38 offset:8192\n" \
M_"a[64:67], a[224:227], a[208:211], a[64:67]\n" \
M_"a[68:71], a[164:167], a[144:147], a[68:71]\n" \
M_"a[68:71], a[228:231], a[208:211], a[68:71]\n" \
X_"a[250:251], v38 offset:9216\n" \
M_"a[72:75], a[168:171], a[144:147], a[72:75]\n" \
M_"a[72:75], a[232:235], a[208:211], a[72:75]\n" \
"global_store_dwordx4 v49, v[0:3], s[28:29] offset:1024 sc1\n" \
M_"a[76:79], a[172:175], a[144:147], a[76:79]\n" \
M_"a[76:79], a[236:239], a[208:211], a[76:79]\n" \
X_"a[188:189], v39 offset:0\n" \
M_"a[92:95], a[172:175], a[148:151], a[92:95]\n" \
M_"a[92:95], a[236:239], a[212:215], a[92:95]\n" \
M_"a[88:91], a[168:171], a[148:151], a[88:91]\n" \
X_"a[190:191], v39 offset:1024\n" \
M_"a[88:91], a[232:235], a[212:215], a[88:91]\n" \
M_"a[84:87], a[164:167], a[148:151], a[84:87]\n" \
M_"a[84:87], a[228:231], a[212:215], a[84:87]\n" \
X_"a[252:253], v39 offset:8192\n" \
M_"a[80:83], a[160:163], a[148:151], a[80:83]\n" \
"s_mov_b64 exec, 0xffff\n" \
"global_store_dwordx4 v49, v[0:3], s[28:29] offset:2048 sc1\n" \
"s_mov_b64 exec, -1\n" \
M_"a[80:83], a[224:227], a[212:215], a[80:83]\n" \
M_"a[96:99], a[160:163], a[152:155], a[96:99]\n" \
X_"a[254:255], v39 offset:9216\n" \
M_"a[96:99], a[224:227], a[216:219], a[96:99]\n" \
M_"a[100:103], a[164:167], a[152:155], a[100:103]\n" \
M_"a[100:103], a[228:231], a[216:219], a[100:103]\n" \
M_"a[104:107], a[168:171], a[152:155], a[104:107]\n" \
M_"a[104:107], a[232:235], a[216:219], a[104:107]\n" \
M_"a[108:111], a[172:175], a[152:155], a[108:111]\n" \
M_"a[108:111], a[236:239], a[216:219], a[108:111]\n" \
"s_waitcnt lgkmcnt(15)\n" \
M_"a[124:127], a[172:175], a[156:159], a[124:127]\n" \
M_"a[124:127], a[236:239], a[220:223], a[124:127]\n" \
M_"a[120:123], a[168:171], a[156:159], a[120:123]\n" \
M_"a[120:123], a[232:235], a[220:223], a[120:123]\n" \
M_"a[116:119], a[164:167], a[156:159], a[116:119]\n" \
M_"a[116:119], a[228:231], a[220:223], a[116:119]\n" \
M_"a[112:115], a[160:163], a[156:159], a[112:115]\n" \
"s_waitcnt vmcnt(3) lgkmcnt(0)\n" \
M_"a[112:115], a[224:227], a[220:223], a[112:115]\n" \
"s_barrier\n" \
"v_cmp_ne_u32_e32 vcc, s62, v44\n" \
"s_nop 4\n" \
"s_cbranch_vccnz L_poll60_%=\n" \
"L_pollr60_%=:\n" \
"s_mov_b32 s68, s12\n" \
"s_and_b32 s69, s13, 0xffff\n" \
"s_mov_b32 s70, 0x480000\n" \
"s_mov_b32 s71, 0x20000\n" \
M_"v[52:55], a[176:179], a[128:131], v[52:55]\n" \
"s_add_u32 m0, s40, 0x0\n" \
"s_add_u32 s85, s39, 0x0\n" \
M_"v[52:55], a[240:243], a[192:195], v[52:55]\n" \
B_"v20, s[80:83], s85 offen sc1 lds\n" \
M_"v[56:59], a[180:183], a[128:131], v[56:59]\n" \
".fill (L_pb + 16096 - .) / 4, 4, 0xBF800000\n" \
"L_pp4:\n" \
"s_cbranch_execz L_pret\n" \
"s_nop 0\n" \
X_"a[160:161], v32 offset:32768\n" \
M_"v[56:59], a[244:247], a[192:195], v[56:59]\n" \
X_"a[162:163], v32 offset:33792\n" \
M_"v[60:63], a[184:187], a[128:131], v[60:63]\n" \
"s_add_u32 m0, s40, 0x1000\n" \
"s_add_u32 s85, s39, 0x1000\n" \
M_"v[60:63], a[248:251], a[192:195], v[60:63]\n" \
B_"v20, s[80:83], s85 offen sc1 lds\n" \
M_"v[64:67], a[188:191], a[128:131], v[64:67]\n" \
R_"a[128:131], v28 offset:49152\n" \
M_"v[64:67], a[252:255], a[192:195], v[64:67]\n" \
R_"a[192:195], v29 offset:49152\n" \
M_"v[80:83], a[188:191], a[132:135], v[80:83]\n" \
"s_add_u32 m0, s40, 0x3000\n" \
"s_movk_i32 s84, 0x0\n" \
M_"v[80:83], a[252:255], a[196:199], v[80:83]\n" \
B_"v21, s[76:79], s84 offen offset:0 lds\n" \
M_"v[76:79], a[184:187], a[132:135], v[76:79]\n" \
X_"a[224:225], v32 offset:40960\n" \
M_"v[76:79], a[248:251], a[196:199], v[76:79]\n" \
X_"a[226:227], v32 offset:41984\n" \
M_"v[72:75], a[180:183], a[132:135], v[72:75]\n" \
"s_add_u32 m0, s30, 0x0\n" \
"s_movk_i32 s84, 0x0\n" \
M_"v[72:75], a[244:247], a[196:199], v[72:75]\n" \
B_"v24, s[76:79], s84 offen offset:0 lds\n" \
M_"v[68:71], a[176:179], a[132:135], v[68:71]\n" \
R_"a[132:135], v28 offset:51200\n" \
M_"v[68:71], a[240:243], a[196:199], v[68:71]\n" \
R_"a[196:199], v29 offset:51200\n" \
M_"v[84:87], a[176:179], a[136:139], v[84:87]\n" \
X_"a[164:165], v33 offset:32768\n" \
M_"v[84:87], a[240:243], a[200:203], v[84:87]\n" \
"s_movk_i32 s84, 0x2000\n" \
M_"v[88:91], a[180:183], a[136:139], v[88:91]\n" \
B_"v25, s[76:79], s84 offen offset:1024 lds\n" \
M_"v[88:91], a[244:247], a[200:203], v[88:91]\n" \
X_"a[166:167], v33 offset:33792\n" \
M_"v[92:95], a[184:187], a[136:139], v[92:95]\n" \
X_"a[228:229], v33 offset:40960\n" \
M_"v[92:95], a[248:251], a[200:203], v[92:95]\n" \
M_"v[96:99], a[188:191], a[136:139], v[96:99]\n" \
R_"a[136:139], v28 offset:53248\n" \
M_"v[96:99], a[252:255], a[200:203], v[96:99]\n" \
R_"a[200:203], v29 offset:53248\n" \
M_"v[112:115], a[188:191], a[140:143], v[112:115]\n" \
"s_movk_i32 s84, 0x4000\n" \
M_"v[112:115], a[252:255], a[204:207], v[112:115]\n" \
B_"v24, s[76:79], s84 offen offset:2048 lds\n" \
M_"v[108:111], a[184:187], a[140:143], v[108:111]\n" \
"s_movk_i32 s84, 0x6000\n" \
M_"v[108:111], a[248:251], a[204:207], v[108:111]\n" \
B_"v25, s[76:79], s84 offen offset:3072 lds\n" \
M_"v[104:107], a[180:183], a[140:143], v[104:107]\n" \
X_"a[230:231], v33 offset:41984\n" \
M_"v[104:107], a[244:247], a[204:207], v[104:107]\n" \
X_"a[168:169], v34 offset:32768\n" \
M_"v[100:103], a[176:179], a[140:143], v[100:103]\n" \
R_"a[140:143], v28 offset:55296\n" \
M_"v[100:103], a[240:243], a[204:207], v[100:103]\n" \
R_"a[204:207], v29 offset:55296\n" \
M_"v[116:119], a[176:179], a[144:147], v[116:119]\n" \
"s_add_u32 m0, s31, 0x0\n" \
"s_movk_i32 s84, 0x0\n" \
M_"v[116:119], a[240:243], a[208:211], v[116:119]\n" \
B_"v26, s[68:71], s84 offen offset:0 lds\n" \
M_"v[120:123], a[180:183], a[144:147], v[120:123]\n" \
"s_movk_i32 s84, 0x7c00\n" \
"s_nop 0\n" \
M_"v[120:123], a[244:247], a[208:211], v[120:123]\n" \
B_"v26, s[68:71], s84 offen offset:1024 lds\n" \
M_"v[124:127], a[184:187], a[144:147], v[124:127]\n" \
X_"a[170:171], v34 offset:33792\n" \
M_"v[124:127], a[248:251], a[208:211], v[124:127]\n" \
X_"a[232:233], v34 offset:40960\n" \
M_"v[128:131], a[188:191], a[144:147], v[128:131]\n" \
R_"a[144:147], v28 offset:32768\n" \
M_"v[128:131], a[252:255], a[208:211], v[128:131]\n" \
R_"a[208:211], v29 offset:32768\n" \
M_"v[144:147], a[188:191], a[148:151], v[144:147]\n" \
"s_addk_i32 s84, 0x7c00\n" \
M_"v[144:147], a[252:255], a[212:215], v[144:147]\n" \
B_"v27, s[68:71], s84 offen offset:2048 lds\n" \
M_"v[140:143], a[184:187], a[148:151], v[140:143]\n" \
X_"a[234:235], v34 offset:41984\n" \
M_"v[140:143], a[248:251], a[212:215], v[140:143]\n" \
"s_addk_i32 s84, 0x7c00\n" \
M_"v[136:139], a[180:183], a[148:151], v[136:139]\n" \
B_"v27, s[68:71], s84 offen offset:3072 lds\n" \
M_"v[136:139], a[244:247], a[212:215], v[136:139]\n" \
X_"a[172:173], v35 offset:32768\n" \
M_"v[132:135], a[176:179], a[148:151], v[132:135]\n" \
R_"a[148:151], v28 offset:34816\n" \
M_"v[132:135], a[240:243], a[212:215], v[132:135]\n" \
R_"a[212:215], v29 offset:34816\n" \
M_"v[148:151], a[176:179], a[152:155], v[148:151]\n" \
"s_add_u32 m0, s30, 0x1000\n" \
"s_mov_b32 s84, 0x9000\n" \
M_"v[148:151], a[240:243], a[216:219], v[148:151]\n" \
B_"v24, s[76:79], s84 offen offset:0 lds\n" \
M_"v[152:155], a[180:183], a[152:155], v[152:155]\n" \
X_"a[174:175], v35 offset:33792\n" \
M_"v[152:155], a[244:247], a[216:219], v[152:155]\n" \
"s_addk_i32 s84, 0x2000\n" \
"s_nop 0\n" \
M_"v[156:159], a[184:187], a[152:155], v[156:159]\n" \
B_"v25, s[76:79], s84 offen offset:1024 lds\n" \
M_"v[156:159], a[248:251], a[216:219], v[156:159]\n" \
X_"a[236:237], v35 offset:40960\n" \
M_"v[160:163], a[188:191], a[152:155], v[160:163]\n" \
R_"a[152:155], v28 offset:36864\n" \
M_"v[160:163], a[252:255], a[216:219], v[160:163]\n" \
R_"a[216:219], v29 offset:36864\n" \
M_"v[176:179], a[188:191], a[156:159], v[176:179]\n" \
X_"a[238:239], v35 offset:41984\n" \
M_"v[176:179], a[252:255], a[220:223], v[176:179]\n" \
M_"v[172:175], a[184:187], a[156:159], v[172:175]\n" \
M_"v[172:175], a[248:251], a[220:223], v[172:175]\n" \
M_"v[168:171], a[180:183], a[156:159], v[168:171]\n" \
M_"v[168:171], a[244:247], a[220:223], v[168:171]\n" \
M_"v[164:167], a[176:179], a[156:159], v[164:167]\n" \
R_"a[156:159], v28 offset:38912\n" \
M_"v[164:167], a[240:243], a[220:223], v[164:167]\n" \
R_"a[220:223], v29 offset:38912\n" \
"s_waitcnt lgkmcnt(15)\n" \
M_"a[0:3], a[160:163], a[128:131], a[0:3]\n" \
"s_addk_i32 s84, 0x2000\n" \
M_"a[0:3], a[224:227], a[192:195], a[0:3]\n" \
B_"v24, s[76:79], s84 offen offset:2048 lds\n" \
M_"a[4:7], a[164:167], a[128:131], a[4:7]\n" \
X_"a[176:177], v36 offset:32768\n" \
M_"a[4:7], a[228:231], a[192:195], a[4:7]\n" \
"s_addk_i32 s84, 0x2000\n" \
"s_waitcnt lgkmcnt(15)\n" \
M_"a[8:11], a[168:171], a[128:131], a[8:11]\n" \
B_"v25, s[76:79], s84 offen offset:3072 lds\n" \
"s_waitcnt lgkmcnt(11)\n" \
M_"a[8:11], a[232:235], a[192:195], a[8:11]\n" \
X_"a[178:179], v36 offset:33792\n" \
"s_waitcnt lgkmcnt(8)\n" \
M_"a[12:15], a[172:175], a[128:131], a[12:15]\n" \
X_"a[240:241], v36 offset:40960\n" \
"v_accvgpr_read_b32 v184, a0\n" \
"s_waitcnt lgkmcnt(5)\n" \
"s_nop 0\n" \
M_"a[12:15], a[236:239], a[192:195], a[12:15]\n" \
"v_accvgpr_read_b32 v185, a1\n" \
"v_accvgpr_read_b32 v186, a2\n" \
M_"a[28:31], a[172:175], a[132:135], a[28:31]\n" \
"v_accvgpr_read_b32 v187, a3\n" \
"v_accvgpr_read_b32 v180, a4\n" \
M_"a[28:31], a[236:239], a[196:199], a[28:31]\n" \
X_"a[242:243], v36 offset:41984\n" \
"v_accvgpr_read_b32 v181, a5\n" \
M_"a[24:27], a[168:171], a[132:135], a[24:27]\n" \
"v_accvgpr_read_b32 v182, a6\n" \
"v_accvgpr_read_b32 v183, a7\n" \
M_"a[24:27], a[232:235], a[196:199], a[24:27]\n" \
"s_add_u32 m0, s30, 0x20000\n" \
"s_movk_i32 s84, 0x100\n" \
"s_nop 0\n" \
M_"a[20:23], a[164:167], a[132:135], a[20:23]\n" \
B_"v24, s[76:79], s84 offen offset:0 lds\n" \
M_"a[20:23], a[228:231], a[196:199], a[20:23]\n" \
X_"a[180:181], v37 offset:32768\n" \
C_"v184, v184, v185\n" \
M_"a[16:19], a[160:163], a[132:135], a[16:19]\n" \
C_"v185, v186, v187\n" \
C_"v186, v180, v181\n" \
M_"a[16:19], a[224:227], a[196:199], a[16:19]\n" \
C_"v187, v182, v183\n" \
"v_accvgpr_read_b32 v188, a8\n" \
M_"a[32:35], a[160:163], a[136:139], a[32:35]\n" \
"s_movk_i32 s84, 0x2100\n" \
"s_nop 0\n" \
M_"a[32:35], a[224:227], a[200:203], a[32:35]\n" \
B_"v25, s[76:79], s84 offen offset:1024 lds\n" \
M_"a[36:39], a[164:167], a[136:139], a[36:39]\n" \
X_"a[182:183], v37 offset:33792\n" \
"v_accvgpr_read_b32 v189, a9\n" \
M_"a[36:39], a[228:231], a[200:203], a[36:39]\n" \
X_"a[244:245], v37 offset:40960\n" \
"v_accvgpr_read_b32 v190, a10\n" \
M_"a[40:43], a[168:171], a[136:139], a[40:43]\n" \
"v_accvgpr_read_b32 v191, a11\n" \
"v_accvgpr_read_b32 v180, a12\n" \
M_"a[40:43], a[232:235], a[200:203], a[40:43]\n" \
"v_accvgpr_read_b32 v181, a13\n" \
"v_accvgpr_read_b32 v182, a14\n" \
M_"a[44:47], a[172:175], a[136:139], a[44:47]\n" \
"s_movk_i32 s84, 0x4100\n" \
"s_nop 0\n" \
M_"a[44:47], a[236:239], a[200:203], a[44:47]\n" \
B_"v24, s[76:79], s84 offen offset:2048 lds\n" \
M_"a[60:63], a[172:175], a[140:143], a[60:63]\n" \
X_"a[246:247], v37 offset:41984\n" \
"v_accvgpr_read_b32 v183, a15\n" \
M_"a[60:63], a[236:239], a[204:207], a[60:63]\n" \
C_"v188, v188, v189\n" \
C_"v189, v190, v191\n" \
M_"a[56:59], a[168:171], a[140:143], a[56:59]\n" \
X_"a[184:185], v38 offset:32768\n" \
C_"v190, v180, v181\n" \
M_"a[56:59], a[232:235], a[204:207], a[56:59]\n" \
C_"v191, v182, v183\n" \
"v_accvgpr_read_b32 v192, a24\n" \
M_"a[52:55], a[164:167], a[140:143], a[52:55]\n" \
"s_movk_i32 s84, 0x6100\n" \
"s_nop 0\n" \
M_"a[52:55], a[228:231], a[204:207], a[52:55]\n" \
B_"v25, s[76:79], s84 offen offset:3072 lds\n" \
M_"a[48:51], a[160:163], a[140:143], a[48:51]\n" \
X_"a[186:187], v38 offset:33792\n" \
"v_accvgpr_read_b32 v193, a25\n" \
M_"a[48:51], a[224:227], a[204:207], a[48:51]\n" \
"v_accvgpr_read_b32 v194, a26\n" \
"v_accvgpr_read_b32 v195, a27\n" \
M_"a[64:67], a[160:163], a[144:147], a[64:67]\n" \
X_"a[248:249], v38 offset:40960\n" \
"v_accvgpr_read_b32 v180, a28\n" \
M_"a[64:67], a[224:227], a[208:211], a[64:67]\n" \
"s_add_u32 m0, s30, 0x21000\n" \
"s_addk_i32 s84, 0x3000\n" \
"s_nop 0\n" \
M_"a[68:71], a[164:167], a[144:147], a[68:71]\n" \
B_"v24, s[76:79], s84 offen offset:0 lds\n" \
M_"a[68:71], a[228:231], a[208:211], a[68:71]\n" \
X_"a[250:251], v38 offset:41984\n" \
"v_accvgpr_read_b32 v181, a29\n" \
M_"a[72:75], a[168:171], a[144:147], a[72:75]\n" \
"v_accvgpr_read_b32 v182, a30\n" \
"v_accvgpr_read_b32 v183, a31\n" \
M_"a[72:75], a[232:235], a[208:211], a[72:75]\n" \
C_"v192, v192, v193\n" \
C_"v193, v194, v195\n" \
M_"a[76:79], a[172:175], a[144:147], a[76:79]\n" \
C_"v194, v180, v181\n" \
C_"v195, v182, v183\n" \
M_"a[76:79], a[236:239], a[208:211], a[76:79]\n" \
"s_addk_i32 s84, 0x2000\n" \
M_"a[92:95], a[172:175], a[148:151], a[92:95]\n" \
B_"v25, s[76:79], s84 offen offset:1024 lds\n" \
M_"a[92:95], a[236:239], a[212:215], a[92:95]\n" \
"s_add_u32 s89, s41, 0x0\n" \
"buffer_store_dwordx4 v[184:187], v20, s[80:83], s89 offen offset:0 sc1\n" \
M_"a[88:91], a[168:171], a[148:151], a[88:91]\n" \
X_"a[188:189], v39 offset:32768\n" \
"v_accvgpr_read_b32 v196, a16\n" \
M_"a[88:91], a[232:235], a[212:215], a[88:91]\n" \
X_"a[190:191], v39 offset:33792\n" \
"v_accvgpr_read_b32 v197, a17\n" \
M_"a[84:87], a[164:167], a[148:151], a[84:87]\n" \
"s_addk_i32 s84, 0x2000\n" \
M_"a[84:87], a[228:231], a[212:215], a[84:87]\n" \
B_"v24, s[76:79], s84 offen offset:2048 lds\n" \
M_"a[80:83], a[160:163], a[148:151], a[80:83]\n" \
"s_add_u32 s88, s41, 0x0\n" \
"buffer_store_dwordx4 v[188:191], v20, s[80:83], s88 offen offset:1024 sc1\n" \
M_"a[80:83], a[224:227], a[212:215], a[80:83]\n" \
X_"a[252:253], v39 offset:40960\n" \
"v_accvgpr_read_b32 v198, a18\n" \
M_"a[96:99], a[160:163], a[152:155], a[96:99]\n" \
X_"a[254:255], v39 offset:41984\n" \
"v_accvgpr_read_b32 v199, a19\n" \
M_"a[96:99], a[224:227], a[216:219], a[96:99]\n" \
"v_accvgpr_read_b32 v180, a20\n" \
"v_accvgpr_read_b32 v181, a21\n" \
M_"a[100:103], a[164:167], a[152:155], a[100:103]\n" \
"s_addk_i32 s84, 0x2000\n" \
"s_nop 0\n" \
M_"a[100:103], a[228:231], a[216:219], a[100:103]\n" \
B_"v25, s[76:79], s84 offen offset:3072 lds\n" \
M_"a[104:107], a[168:171], a[152:155], a[104:107]\n" \
"s_add_u32 s89, s41, 0x1000\n" \
"buffer_store_dwordx4 v[192:195], v20, s[80:83], s89 offen offset:1024 sc1\n" \
M_"a[104:107], a[232:235], a[216:219], a[104:107]\n" \
"v_accvgpr_read_b32 v182, a22\n" \
"v_accvgpr_read_b32 v183, a23\n" \
M_"a[108:111], a[172:175], a[152:155], a[108:111]\n" \
C_"v196, v196, v197\n" \
C_"v197, v198, v199\n" \
M_"a[108:111], a[236:239], a[216:219], a[108:111]\n" \
C_"v198, v180, v181\n" \
C_"v199, v182, v183\n" \
"s_waitcnt lgkmcnt(15)\n" \
"s_nop 0\n" \
M_"a[124:127], a[172:175], a[156:159], a[124:127]\n" \
"v_accvgpr_read_b32 v200, a32\n" \
"v_accvgpr_read_b32 v201, a33\n" \
M_"a[124:127], a[236:239], a[220:223], a[124:127]\n" \
"v_accvgpr_read_b32 v202, a34\n" \
"v_accvgpr_read_b32 v203, a35\n" \
M_"a[120:123], a[168:171], a[156:159], a[120:123]\n" \
"s_add_u32 s88, s41, 0x1000\n" \
"buffer_store_dwordx4 v[196:199], v20, s[80:83], s88 offen offset:0 sc1\n" \
M_"a[120:123], a[232:235], a[220:223], a[120:123]\n" \
"v_accvgpr_read_b32 v180, a36\n" \
"v_accvgpr_read_b32 v181, a37\n" \
M_"a[116:119], a[164:167], a[156:159], a[116:119]\n" \
"v_accvgpr_read_b32 v182, a38\n" \
"v_accvgpr_read_b32 v183, a39\n" \
M_"a[116:119], a[228:231], a[220:223], a[116:119]\n" \
C_"v200, v200, v201\n" \
C_"v201, v202, v203\n" \
M_"a[112:115], a[160:163], a[156:159], a[112:115]\n" \
C_"v202, v180, v181\n" \
C_"v203, v182, v183\n" \
M_"a[112:115], a[224:227], a[220:223], a[112:115]\n" \
"v_accvgpr_read_b32 v204, a40\n" \
"v_accvgpr_read_b32 v205, a41\n" \
"s_waitcnt vmcnt(12) lgkmcnt(0)\n" \
"s_barrier\n" \
"v_mov_b32_e32 v36, v32\n" \
"v_xor_b32_e32 v32, 0x0, v48\n" \
"v_mov_b32_e32 v37, v33\n" \
"v_xor_b32_e32 v33, 0x20, v48\n" \
"v_mov_b32_e32 v38, v34\n" \
"v_xor_b32_e32 v34, 0x40, v48\n" \
"v_mov_b32_e64 v39, v35\n" \
"v_xor_b32_e32 v35, 0x60, v48\n" \
"v_add_u32_e32 v22, s44, v28\n" \
"v_add_u32_e32 v28, s43, v28\n" \
"v_add_u32_e32 v23, s44, v29\n" \
"v_add_u32_e32 v29, s43, v29\n" \
M_"v[52:55], a[176:179], a[128:131], v[52:55]\n" \
"s_add_u32 m0, s40, 0x8000\n" \
"s_add_u32 s85, s39, 0x2000\n" \
M_"v[52:55], a[240:243], a[192:195], v[52:55]\n" \
B_"v20, s[80:83], s85 offen sc1 lds\n" \
M_"v[56:59], a[180:183], a[128:131], v[56:59]\n" \
X_"a[160:161], v32 offset:0\n" \
"v_accvgpr_read_b32 v206, a42\n" \
M_"v[56:59], a[244:247], a[192:195], v[56:59]\n" \
X_"a[162:163], v32 offset:512\n" \
"v_accvgpr_read_b32 v207, a43\n" \
M_"v[60:63], a[184:187], a[128:131], v[60:63]\n" \
"s_add_u32 m0, s40, 0x9000\n" \
"s_add_u32 s85, s39, 0x3000\n" \
M_"v[60:63], a[248:251], a[192:195], v[60:63]\n" \
B_"v20, s[80:83], s85 offen sc1 lds\n" \
M_"v[64:67], a[188:191], a[128:131], v[64:67]\n" \
R_"a[128:131], v28 offset:0\n" \
"v_accvgpr_read_b32 v180, a44\n" \
M_"v[64:67], a[252:255], a[192:195], v[64:67]\n" \
R_"a[192:195], v29 offset:0\n" \
"v_accvgpr_read_b32 v181, a45\n" \
M_"v[80:83], a[188:191], a[132:135], v[80:83]\n" \
"s_add_u32 m0, s40, 0xb000\n" \
"s_movk_i32 s84, 0x80\n" \
M_"v[80:83], a[252:255], a[196:199], v[80:83]\n" \
B_"v21, s[76:79], s84 offen offset:0 lds\n" \
M_"v[76:79], a[184:187], a[132:135], v[76:79]\n" \
X_"a[224:225], v32 offset:4096\n" \
"v_accvgpr_read_b32 v182, a46\n" \
M_"v[76:79], a[248:251], a[196:199], v[76:79]\n" \
X_"a[226:227], v32 offset:4608\n" \
"v_accvgpr_read_b32 v183, a47\n" \
M_"v[72:75], a[180:183], a[132:135], v[72:75]\n" \
"s_add_u32 m0, s30, 0x8000\n" \
"s_movk_i32 s84, 0x80\n" \
M_"v[72:75], a[244:247], a[196:199], v[72:75]\n" \
B_"v24, s[76:79], s84 offen offset:0 lds\n" \
M_"v[68:71], a[176:179], a[132:135], v[68:71]\n" \
R_"a[132:135], v28 offset:2048\n" \
C_"v204, v204, v205\n" \
M_"v[68:71], a[240:243], a[196:199], v[68:71]\n" \
R_"a[196:199], v29 offset:2048\n" \
C_"v205, v206, v207\n" \
M_"v[84:87], a[176:179], a[136:139], v[84:87]\n" \
X_"a[164:165], v33 offset:0\n" \
C_"v206, v180, v181\n" \
M_"v[84:87], a[240:243], a[200:203], v[84:87]\n" \
"s_movk_i32 s84, 0x2080\n" \
"s_nop 0\n" \
M_"v[88:91], a[180:183], a[136:139], v[88:91]\n" \
B_"v25, s[76:79], s84 offen offset:1024 lds\n" \
M_"v[88:91], a[244:247], a[200:203], v[88:91]\n" \
X_"a[166:167], v33 offset:512\n" \
C_"v207, v182, v183\n" \
M_"v[92:95], a[184:187], a[136:139], v[92:95]\n" \
X_"a[228:229], v33 offset:4096\n" \
"v_accvgpr_read_b32 v208, a56\n" \
M_"v[92:95], a[248:251], a[200:203], v[92:95]\n" \
X_"a[230:231], v33 offset:4608\n" \
"v_accvgpr_read_b32 v209, a57\n" \
M_"v[96:99], a[188:191], a[136:139], v[96:99]\n" \
R_"a[136:139], v28 offset:4096\n" \
"v_accvgpr_read_b32 v210, a58\n" \
M_"v[96:99], a[252:255], a[200:203], v[96:99]\n" \
R_"a[200:203], v29 offset:4096\n" \
"v_accvgpr_read_b32 v211, a59\n" \
M_"v[112:115], a[188:191], a[140:143], v[112:115]\n" \
"s_movk_i32 s84, 0x4080\n" \
M_"v[112:115], a[252:255], a[204:207], v[112:115]\n" \
B_"v24, s[76:79], s84 offen offset:2048 lds\n" \
M_"v[108:111], a[184:187], a[140:143], v[108:111]\n" \
"s_movk_i32 s84, 0x6080\n" \
M_"v[108:111], a[248:251], a[204:207], v[108:111]\n" \
B_"v25, s[76:79], s84 offen offset:3072 lds\n" \
M_"v[104:107], a[180:183], a[140:143], v[104:107]\n" \
X_"a[172:173], v35 offset:0\n" \
"v_accvgpr_read_b32 v180, a60\n" \
M_"v[104:107], a[244:247], a[204:207], v[104:107]\n" \
X_"a[174:175], v35 offset:512\n" \
"v_accvgpr_read_b32 v181, a61\n" \
M_"v[100:103], a[176:179], a[140:143], v[100:103]\n" \
R_"a[140:143], v28 offset:6144\n" \
"v_accvgpr_read_b32 v182, a62\n" \
M_"v[100:103], a[240:243], a[204:207], v[100:103]\n" \
R_"a[204:207], v29 offset:6144\n" \
"v_accvgpr_read_b32 v183, a63\n" \
M_"v[116:119], a[176:179], a[144:147], v[116:119]\n" \
"s_add_u32 m0, s31, 0x8000\n" \
"s_mov_b32 s84, 0x80000\n" \
M_"v[116:119], a[240:243], a[208:211], v[116:119]\n" \
B_"v26, s[68:71], s84 offen offset:0 lds\n" \
M_"v[120:123], a[180:183], a[144:147], v[120:123]\n" \
"s_addk_i32 s84, 0x7c00\n" \
"s_nop 0\n" \
M_"v[120:123], a[244:247], a[208:211], v[120:123]\n" \
B_"v26, s[68:71], s84 offen offset:1024 lds\n" \
M_"v[124:127], a[184:187], a[144:147], v[124:127]\n" \
X_"a[236:237], v35 offset:4096\n" \
C_"v208, v208, v209\n" \
M_"v[124:127], a[248:251], a[208:211], v[124:127]\n" \
X_"a[238:239], v35 offset:4608\n" \
C_"v209, v210, v211\n" \
M_"v[128:131], a[188:191], a[144:147], v[128:131]\n" \
R_"a[144:147], v28 offset:8192\n" \
C_"v210, v180, v181\n" \
M_"v[128:131], a[252:255], a[208:211], v[128:131]\n" \
R_"a[208:211], v29 offset:8192\n" \
C_"v211, v182, v183\n" \
M_"v[144:147], a[188:191], a[148:151], v[144:147]\n" \
"s_addk_i32 s84, 0x7c00\n" \
M_"v[144:147], a[252:255], a[212:215], v[144:147]\n" \
B_"v27, s[68:71], s84 offen offset:2048 lds\n" \
M_"v[140:143], a[184:187], a[148:151], v[140:143]\n" \
X_"a[168:169], v34 offset:0\n" \
"v_accvgpr_read_b32 v212, a48\n" \
M_"v[140:143], a[248:251], a[212:215], v[140:143]\n" \
"s_addk_i32 s84, 0x7c00\n" \
M_"v[136:139], a[180:183], a[148:151], v[136:139]\n" \
B_"v27, s[68:71], s84 offen offset:3072 lds\n" \
M_"v[136:139], a[244:247], a[212:215], v[136:139]\n" \
X_"a[170:171], v34 offset:512\n" \
"v_accvgpr_read_b32 v213, a49\n" \
M_"v[132:135], a[176:179], a[148:151], v[132:135]\n" \
R_"a[148:151], v28 offset:10240\n" \
"v_accvgpr_read_b32 v214, a50\n" \
M_"v[132:135], a[240:243], a[212:215], v[132:135]\n" \
R_"a[212:215], v29 offset:10240\n" \
"v_accvgpr_read_b32 v215, a51\n" \
M_"v[148:151], a[176:179], a[152:155], v[148:151]\n" \
"s_add_u32 m0, s30, 0x9000\n" \
"s_mov_b32 s84, 0x9080\n" \
M_"v[148:151], a[240:243], a[216:219], v[148:151]\n" \
B_"v24, s[76:79], s84 offen offset:0 lds\n" \
M_"v[152:155], a[180:183], a[152:155], v[152:155]\n" \
X_"a[232:233], v34 offset:4096\n" \
"v_accvgpr_read_b32 v180, a52\n" \
M_"v[152:155], a[244:247], a[216:219], v[152:155]\n" \
"s_addk_i32 s84, 0x2000\n" \
"s_nop 0\n" \
M_"v[156:159], a[184:187], a[152:155], v[156:159]\n" \
B_"v25, s[76:79], s84 offen offset:1024 lds\n" \
M_"v[156:159], a[248:251], a[216:219], v[156:159]\n" \
X_"a[234:235], v34 offset:4608\n" \
"v_accvgpr_read_b32 v181, a53\n" \
M_"v[160:163], a[188:191], a[152:155], v[160:163]\n" \
R_"a[152:155], v28 offset:12288\n" \
"v_accvgpr_read_b32 v182, a54\n" \
M_"v[160:163], a[252:255], a[216:219], v[160:163]\n" \
R_"a[216:219], v29 offset:12288\n" \
"v_accvgpr_read_b32 v183, a55\n" \
M_"v[176:179], a[188:191], a[156:159], v[176:179]\n" \
R_"v[248:251], v22 offset:0\n" \
C_"v212, v212, v213\n" \
M_"v[176:179], a[252:255], a[220:223], v[176:179]\n" \
R_"v[48:51], v23 offset:0\n" \
C_"v213, v214, v215\n" \
M_"v[172:175], a[184:187], a[156:159], v[172:175]\n" \
C_"v214, v180, v181\n" \
C_"v215, v182, v183\n" \
M_"v[172:175], a[248:251], a[220:223], v[172:175]\n" \
"v_accvgpr_read_b32 v216, a64\n" \
"v_accvgpr_read_b32 v217, a65\n" \
M_"v[168:171], a[180:183], a[156:159], v[168:171]\n" \
"v_accvgpr_read_b32 v218, a66\n" \
"v_accvgpr_read_b32 v219, a67\n" \
M_"v[168:171], a[244:247], a[220:223], v[168:171]\n" \
"v_accvgpr_read_b32 v180, a68\n" \
"v_accvgpr_read_b32 v181, a69\n" \
M_"v[164:167], a[176:179], a[156:159], v[164:167]\n" \
R_"a[156:159], v28 offset:14336\n" \
"v_accvgpr_read_b32 v182, a70\n" \
M_"v[164:167], a[240:243], a[220:223], v[164:167]\n" \
R_"a[220:223], v29 offset:14336\n" \
"v_accvgpr_read_b32 v183, a71\n" \
"s_waitcnt lgkmcnt(15)\n" \
M_"a[0:3], a[160:163], a[128:131], 0\n" \
"s_addk_i32 s84, 0x2000\n" \
C_"v216, v216, v217\n" \
M_"a[0:3], a[224:227], a[192:195], a[0:3]\n" \
B_"v24, s[76:79], s84 offen offset:2048 lds\n" \
C_"v217, v218, v219\n" \
M_"a[4:7], a[164:167], a[128:131], 0\n" \
X_"a[176:177], v36 offset:0\n" \
C_"v218, v180, v181\n" \
M_"a[4:7], a[228:231], a[192:195], a[4:7]\n" \
"s_addk_i32 s84, 0x2000\n" \
C_"v219, v182, v183\n" \
M_"a[12:15], a[172:175], a[128:131], 0\n" \
B_"v25, s[76:79], s84 offen offset:3072 lds\n" \
"v_accvgpr_read_b32 v220, a80\n" \
"s_waitcnt lgkmcnt(15)\n" \
M_"a[12:15], a[236:239], a[192:195], a[12:15]\n" \
X_"a[178:179], v36 offset:1024\n" \
"v_accvgpr_read_b32 v221, a81\n" \
"s_waitcnt lgkmcnt(12)\n" \
M_"a[8:11], a[128:131], a[168:171], 0\n" \
X_"a[240:241], v36 offset:8192\n" \
"v_accvgpr_read_b32 v222, a82\n" \
"s_waitcnt lgkmcnt(9)\n" \
M_"a[8:11], a[192:195], a[232:235], a[8:11]\n" \
X_"a[242:243], v36 offset:9216\n" \
"v_accvgpr_read_b32 v223, a83\n" \
M_"a[24:27], a[132:135], a[168:171], 0\n" \
X_"a[180:181], v37 offset:0\n" \
"v_accvgpr_read_b32 v180, a84\n" \
M_"a[24:27], a[196:199], a[232:235], a[24:27]\n" \
X_"a[182:183], v37 offset:1024\n" \
"v_accvgpr_read_b32 v181, a85\n" \
M_"a[28:31], a[172:175], a[132:135], 0\n" \
X_"a[244:245], v37 offset:8192\n" \
"v_accvgpr_read_b32 v182, a86\n" \
M_"a[28:31], a[236:239], a[196:199], a[28:31]\n" \
"s_add_u32 s89, s41, 0x2000\n" \
"buffer_store_dwordx4 v[200:203], v20, s[80:83], s89 offen offset:0 sc1\n" \
M_"a[20:23], a[164:167], a[132:135], 0\n" \
X_"a[246:247], v37 offset:9216\n" \
"v_accvgpr_read_b32 v183, a87\n" \
M_"a[20:23], a[228:231], a[196:199], a[20:23]\n" \
X_"a[184:185], v38 offset:0\n" \
C_"v220, v220, v221\n" \
M_"a[16:19], a[160:163], a[132:135], 0\n" \
".fill (L_pb + 20192 - .) / 4, 4, 0xBF800000\n" \
"L_pp5:\n" \
"s_cbranch_execz L_pret\n" \
"s_nop 0\n" \
X_"a[186:187], v38 offset:1024\n" \
C_"v221, v222, v223\n" \
M_"a[16:19], a[224:227], a[196:199], a[16:19]\n" \
C_"v222, v180, v181\n" \
C_"v223, v182, v183\n" \
C_"v224, v52, v53\n" \
M_"a[32:35], a[160:163], a[136:139], 0\n" \
"s_add_u32 s88, s41, 0x2000\n" \
"buffer_store_dwordx4 v[204:207], v20, s[80:83], s88 offen offset:1024 sc1\n" \
M_"a[32:35], a[224:227], a[200:203], a[32:35]\n" \
X_"a[248:249], v38 offset:8192\n" \
C_"v225, v54, v55\n" \
M_"a[36:39], a[164:167], a[136:139], 0\n" \
X_"a[250:251], v38 offset:9216\n" \
C_"v226, v56, v57\n" \
M_"a[36:39], a[228:231], a[200:203], a[36:39]\n" \
X_"a[188:189], v39 offset:0\n" \
C_"v227, v58, v59\n" \
M_"a[44:47], a[172:175], a[136:139], 0\n" \
C_"v228, v60, v61\n" \
C_"v229, v62, v63\n" \
C_"v230, v64, v65\n" \
M_"a[44:47], a[236:239], a[200:203], a[44:47]\n" \
X_"a[190:191], v39 offset:1024\n" \
C_"v231, v66, v67\n" \
M_"a[40:43], a[136:139], a[168:171], 0\n" \
"s_add_u32 s89, s41, 0x3000\n" \
"buffer_store_dwordx4 v[208:211], v20, s[80:83], s89 offen offset:1024 sc1\n" \
M_"a[40:43], a[200:203], a[232:235], a[40:43]\n" \
X_"a[252:253], v39 offset:8192\n" \
C_"v232, v76, v77\n" \
M_"a[56:59], a[140:143], a[168:171], 0\n" \
X_"a[254:255], v39 offset:9216\n" \
C_"v233, v78, v79\n" \
M_"a[56:59], a[204:207], a[232:235], a[56:59]\n" \
C_"v234, v80, v81\n" \
C_"v235, v82, v83\n" \
C_"v236, v68, v69\n" \
M_"a[60:63], a[172:175], a[140:143], 0\n" \
C_"v237, v70, v71\n" \
C_"v238, v72, v73\n" \
C_"v239, v74, v75\n" \
M_"a[60:63], a[236:239], a[204:207], a[60:63]\n" \
"s_add_u32 s88, s41, 0x3000\n" \
"buffer_store_dwordx4 v[212:215], v20, s[80:83], s88 offen offset:0 sc1\n" \
M_"a[52:55], a[164:167], a[140:143], 0\n" \
C_"v240, v84, v85\n" \
C_"v241, v86, v87\n" \
C_"v242, v88, v89\n" \
M_"a[52:55], a[228:231], a[204:207], a[52:55]\n" \
C_"v243, v90, v91\n" \
C_"v244, v92, v93\n" \
C_"v245, v94, v95\n" \
M_"a[48:51], a[160:163], a[140:143], 0\n" \
C_"v246, v96, v97\n" \
C_"v247, v98, v99\n" \
C_"v184, v108, v109\n" \
M_"a[48:51], a[224:227], a[204:207], a[48:51]\n" \
C_"v185, v110, v111\n" \
C_"v186, v112, v113\n" \
C_"v187, v114, v115\n" \
"s_waitcnt lgkmcnt(15)\n" \
"s_nop 0\n" \
M_"a[64:67], a[164:167], v[248:251], 0\n" \
C_"v188, v100, v101\n" \
C_"v189, v102, v103\n" \
C_"v190, v104, v105\n" \
M_"a[64:67], a[228:231], v[48:51], a[64:67]\n" \
C_"v191, v106, v107\n" \
C_"v192, v116, v117\n" \
C_"v193, v118, v119\n" \
M_"a[80:83], a[172:175], v[248:251], 0\n" \
C_"v194, v120, v121\n" \
C_"v195, v122, v123\n" \
C_"v196, v124, v125\n" \
M_"a[80:83], a[236:239], v[48:51], a[80:83]\n" \
C_"v197, v126, v127\n" \
C_"v198, v128, v129\n" \
C_"v199, v130, v131\n" \
M_"a[68:71], v[248:251], a[168:171], 0\n" \
C_"v252, v140, v141\n" \
C_"v253, v142, v143\n" \
C_"v254, v144, v145\n" \
M_"a[68:71], v[48:51], a[232:235], a[68:71]\n" \
C_"v255, v146, v147\n" \
"v_accvgpr_write_b32 a84, v252\n" \
"v_accvgpr_write_b32 a85, v253\n" \
"s_waitcnt vmcnt(4) lgkmcnt(0)\n" \
"s_barrier\n" \
M_"v[52:55], a[176:179], a[128:131], 0\n" \
"s_add_u32 m0, s40, 0x0\n" \
"s_nop 0\n" \
"s_add_u32 s85, s39, 0x4000\n" \
"v_accvgpr_write_b32 a86, v254\n" \
M_"v[52:55], a[240:243], a[192:195], v[52:55]\n" \
B_"v20, s[80:83], s85 offen sc1 lds\n" \
"v_accvgpr_write_b32 a87, v255\n" \
M_"v[56:59], a[180:183], a[128:131], 0\n" \
X_"a[160:161], v32 offset:32768\n" \
C_"v252, v132, v133\n" \
M_"v[56:59], a[244:247], a[192:195], v[56:59]\n" \
X_"a[162:163], v32 offset:33280\n" \
C_"v253, v134, v135\n" \
M_"v[60:63], a[184:187], a[128:131], 0\n" \
"s_add_u32 m0, s40, 0x1000\n" \
"s_add_u32 s85, s39, 0x5000\n" \
C_"v254, v136, v137\n" \
M_"v[60:63], a[248:251], a[192:195], v[60:63]\n" \
B_"v20, s[80:83], s85 offen sc1 lds\n" \
C_"v255, v138, v139\n" \
M_"v[64:67], a[188:191], a[128:131], 0\n" \
R_"a[128:131], v28 offset:32768\n" \
C_"v200, v148, v149\n" \
M_"v[64:67], a[252:255], a[192:195], v[64:67]\n" \
R_"a[192:195], v29 offset:32768\n" \
C_"v201, v150, v151\n" \
M_"v[80:83], a[188:191], a[132:135], 0\n" \
X_"a[224:225], v32 offset:36864\n" \
C_"v202, v152, v153\n" \
M_"v[80:83], a[252:255], a[196:199], v[80:83]\n" \
"s_add_u32 m0, s40, 0x3000\n" \
"s_movk_i32 s84, 0x100\n" \
"s_nop 0\n" \
C_"v203, v154, v155\n" \
M_"v[76:79], a[184:187], a[132:135], 0\n" \
B_"v21, s[76:79], s84 offen offset:0 lds\n" \
C_"v204, v156, v157\n" \
M_"v[76:79], a[248:251], a[196:199], v[76:79]\n" \
X_"a[226:227], v32 offset:37376\n" \
C_"v205, v158, v159\n" \
M_"v[72:75], a[180:183], a[132:135], 0\n" \
X_"a[164:165], v33 offset:32768\n" \
C_"v206, v160, v161\n" \
M_"v[72:75], a[244:247], a[196:199], v[72:75]\n" \
C_"v207, v162, v163\n" \
C_"v208, v172, v173\n" \
C_"v209, v174, v175\n" \
M_"v[68:71], a[176:179], a[132:135], 0\n" \
R_"a[132:135], v28 offset:34816\n" \
C_"v210, v176, v177\n" \
M_"v[68:71], a[240:243], a[196:199], v[68:71]\n" \
R_"a[196:199], v29 offset:34816\n" \
C_"v211, v178, v179\n" \
M_"v[84:87], a[176:179], a[136:139], 0\n" \
"s_add_u32 m0, s31, 0x0\n" \
"s_mov_b32 s84, 0x100000\n" \
C_"v212, v164, v165\n" \
M_"v[84:87], a[240:243], a[200:203], v[84:87]\n" \
B_"v26, s[68:71], s84 offen offset:0 lds\n" \
C_"v213, v166, v167\n" \
M_"v[88:91], a[180:183], a[136:139], 0\n" \
"s_add_u32 s89, s41, 0x0\n" \
"buffer_store_dwordx4 v[224:227], v20, s[80:83], s89 offen offset:2048 sc1\n" \
M_"v[88:91], a[244:247], a[200:203], v[88:91]\n" \
X_"a[166:167], v33 offset:33280\n" \
C_"v214, v168, v169\n" \
M_"v[92:95], a[184:187], a[136:139], 0\n" \
X_"a[228:229], v33 offset:36864\n" \
C_"v215, v170, v171\n" \
M_"v[92:95], a[248:251], a[200:203], v[92:95]\n" \
X_"a[230:231], v33 offset:37376\n" \
M_"v[96:99], a[188:191], a[136:139], 0\n" \
R_"a[136:139], v28 offset:36864\n" \
M_"v[96:99], a[252:255], a[200:203], v[96:99]\n" \
R_"a[200:203], v29 offset:36864\n" \
M_"v[112:115], a[188:191], a[140:143], 0\n" \
"s_addk_i32 s84, 0x7c00\n" \
M_"v[112:115], a[252:255], a[204:207], v[112:115]\n" \
B_"v26, s[68:71], s84 offen offset:1024 lds\n" \
M_"v[108:111], a[184:187], a[140:143], 0\n" \
X_"a[172:173], v35 offset:32768\n" \
M_"v[108:111], a[248:251], a[204:207], v[108:111]\n" \
X_"a[174:175], v35 offset:33280\n" \
M_"v[104:107], a[180:183], a[140:143], 0\n" \
"s_addk_i32 s84, 0x7c00\n" \
M_"v[104:107], a[244:247], a[204:207], v[104:107]\n" \
B_"v27, s[68:71], s84 offen offset:2048 lds\n" \
M_"v[100:103], a[176:179], a[140:143], 0\n" \
R_"a[140:143], v28 offset:38912\n" \
M_"v[100:103], a[240:243], a[204:207], v[100:103]\n" \
R_"a[204:207], v29 offset:38912\n" \
M_"v[116:119], a[176:179], a[144:147], 0\n" \
"s_addk_i32 s84, 0x7c00\n" \
"s_nop 0\n" \
M_"v[116:119], a[240:243], a[208:211], v[116:119]\n" \
B_"v27, s[68:71], s84 offen offset:3072 lds\n" \
M_"v[120:123], a[180:183], a[144:147], 0\n" \
X_"a[236:237], v35 offset:36864\n" \
M_"v[120:123], a[244:247], a[208:211], v[120:123]\n" \
X_"a[238:239], v35 offset:37376\n" \
M_"v[124:127], a[184:187], a[144:147], 0\n" \
X_"a[168:169], v34 offset:32768\n" \
M_"v[124:127], a[248:251], a[208:211], v[124:127]\n" \
X_"a[170:171], v34 offset:33280\n" \
M_"v[128:131], a[188:191], a[144:147], 0\n" \
R_"a[144:147], v28 offset:40960\n" \
M_"v[128:131], a[252:255], a[208:211], v[128:131]\n" \
R_"a[208:211], v29 offset:40960\n" \
M_"v[144:147], a[188:191], a[148:151], 0\n" \
"s_add_u32 m0, s30, 0x0\n" \
"s_movk_i32 s84, 0x180\n" \
M_"v[144:147], a[252:255], a[212:215], v[144:147]\n" \
B_"v24, s[76:79], s84 offen offset:0 lds\n" \
M_"v[140:143], a[184:187], a[148:151], 0\n" \
"s_movk_i32 s84, 0x2180\n" \
"s_nop 0\n" \
M_"v[140:143], a[248:251], a[212:215], v[140:143]\n" \
B_"v25, s[76:79], s84 offen offset:1024 lds\n" \
M_"v[136:139], a[180:183], a[148:151], 0\n" \
X_"a[232:233], v34 offset:36864\n" \
M_"v[136:139], a[244:247], a[212:215], v[136:139]\n" \
X_"a[234:235], v34 offset:37376\n" \
M_"v[132:135], a[176:179], a[148:151], 0\n" \
R_"a[148:151], v28 offset:43008\n" \
M_"v[132:135], a[240:243], a[212:215], v[132:135]\n" \
R_"a[212:215], v29 offset:43008\n" \
M_"v[148:151], a[176:179], a[152:155], 0\n" \
"s_movk_i32 s84, 0x4180\n" \
M_"v[148:151], a[240:243], a[216:219], v[148:151]\n" \
B_"v24, s[76:79], s84 offen offset:2048 lds\n" \
M_"v[152:155], a[180:183], a[152:155], 0\n" \
R_"v[248:251], v22 offset:32768\n" \
M_"v[152:155], a[244:247], a[216:219], v[152:155]\n" \
"s_movk_i32 s84, 0x6180\n" \
M_"v[156:159], a[184:187], a[152:155], 0\n" \
B_"v25, s[76:79], s84 offen offset:3072 lds\n" \
M_"v[156:159], a[248:251], a[216:219], v[156:159]\n" \
R_"v[48:51], v23 offset:32768\n" \
M_"v[160:163], a[188:191], a[152:155], 0\n" \
R_"a[152:155], v28 offset:45056\n" \
M_"v[160:163], a[252:255], a[216:219], v[160:163]\n" \
R_"a[216:219], v29 offset:45056\n" \
M_"v[176:179], a[188:191], a[156:159], 0\n" \
M_"v[176:179], a[252:255], a[220:223], v[176:179]\n" \
M_"v[172:175], a[184:187], a[156:159], 0\n" \
M_"v[172:175], a[248:251], a[220:223], v[172:175]\n" \
M_"v[168:171], a[180:183], a[156:159], 0\n" \
M_"v[168:171], a[244:247], a[220:223], v[168:171]\n" \
M_"v[164:167], a[176:179], a[156:159], 0\n" \
R_"a[156:159], v28 offset:47104\n" \
M_"v[164:167], a[240:243], a[220:223], v[164:167]\n" \
R_"a[220:223], v29 offset:47104\n" \
"s_waitcnt lgkmcnt(15)\n" \
M_"a[0:3], a[160:163], a[128:131], a[0:3]\n" \
"s_add_u32 m0, s30, 0x1000\n" \
"s_addk_i32 s84, 0x3000\n" \
M_"a[0:3], a[224:227], a[192:195], a[0:3]\n" \
B_"v24, s[76:79], s84 offen offset:0 lds\n" \
M_"a[4:7], a[164:167], a[128:131], a[4:7]\n" \
X_"a[176:177], v36 offset:32768\n" \
"v_mov_b32_e64 v11, 0\n" \
M_"a[4:7], a[228:231], a[192:195], a[4:7]\n" \
X_"a[178:179], v36 offset:33792\n" \
"s_and_b32 s5, s2, 7\n" \
M_"a[12:15], a[172:175], a[128:131], a[12:15]\n" \
X_"a[240:241], v36 offset:40960\n" \
"s_lshl_b32 s6, s3, 1\n" \
"s_waitcnt lgkmcnt(15)\n" \
M_"a[12:15], a[236:239], a[192:195], a[12:15]\n" \
"s_add_u32 s88, s41, 0x0\n" \
"buffer_store_dwordx4 v[228:231], v20, s[80:83], s88 offen offset:3072 sc1\n" \
M_"a[8:11], a[128:131], a[168:171], a[8:11]\n" \
X_"a[242:243], v36 offset:41984\n" \
"s_lshl_b32 s4, s5, 2\n" \
"s_waitcnt lgkmcnt(12)\n" \
M_"a[8:11], a[192:195], a[232:235], a[8:11]\n" \
X_"a[180:181], v37 offset:32768\n" \
"s_bfe_u32 s7, s2, 0x20003\n" \
M_"a[24:27], a[132:135], a[168:171], a[24:27]\n" \
X_"a[182:183], v37 offset:33792\n" \
"s_or_b32 s12, s4, s7\n" \
M_"a[24:27], a[196:199], a[232:235], a[24:27]\n" \
X_"a[244:245], v37 offset:40960\n" \
"s_and_b32 s6, s6, 4\n" \
M_"a[28:31], a[172:175], a[132:135], a[28:31]\n" \
"s_and_b32 s8, s3, 1\n" \
"s_or_b32 s8, s6, s8\n" \
M_"a[28:31], a[236:239], a[196:199], a[28:31]\n" \
"s_addk_i32 s84, 0x2000\n" \
M_"a[20:23], a[164:167], a[132:135], a[20:23]\n" \
B_"v25, s[76:79], s84 offen offset:1024 lds\n" \
M_"a[20:23], a[228:231], a[196:199], a[20:23]\n" \
X_"a[246:247], v37 offset:41984\n" \
"s_lshl_b32 s6, s12, 4\n" \
M_"a[16:19], a[160:163], a[132:135], a[16:19]\n" \
X_"a[184:185], v38 offset:32768\n" \
"s_lshl_b32 s9, s3, 2\n" \
M_"a[16:19], a[224:227], a[196:199], a[16:19]\n" \
X_"a[186:187], v38 offset:33792\n" \
"s_or_b32 s6, s6, s9\n" \
M_"a[32:35], a[160:163], a[136:139], a[32:35]\n" \
"s_addk_i32 s84, 0x2000\n" \
M_"a[32:35], a[224:227], a[200:203], a[32:35]\n" \
B_"v24, s[76:79], s84 offen offset:2048 lds\n" \
M_"a[36:39], a[164:167], a[136:139], a[36:39]\n" \
X_"a[248:249], v38 offset:40960\n" \
"s_lshr_b32 s9, s2, 5\n" \
M_"a[36:39], a[228:231], a[200:203], a[36:39]\n" \
X_"a[250:251], v38 offset:41984\n" \
"s_mul_i32 s10, s9, 0xa00\n" \
M_"a[44:47], a[172:175], a[136:139], a[44:47]\n" \
X_"a[188:189], v39 offset:32768\n" \
"s_add_i32 s10, s10, 0x5bc1000\n" \
M_"a[44:47], a[236:239], a[200:203], a[44:47]\n" \
X_"a[190:191], v39 offset:33792\n" \
"s_or_b32 s6, s6, s10\n" \
M_"a[40:43], a[136:139], a[168:171], a[40:43]\n" \
"s_add_u32 s89, s41, 0x1000\n" \
"buffer_store_dwordx4 v[232:235], v20, s[80:83], s89 offen offset:3072 sc1\n" \
M_"a[40:43], a[200:203], a[232:235], a[40:43]\n" \
X_"a[252:253], v39 offset:40960\n" \
"s_lshl_b32 s13, s9, 3\n" \
M_"a[56:59], a[140:143], a[168:171], a[56:59]\n" \
X_"a[254:255], v39 offset:41984\n" \
"v_writelane_b32 v11, s6, 0\n" \
M_"a[56:59], a[204:207], a[232:235], a[56:59]\n" \
"s_or_b32 s6, s13, s5\n" \
"v_writelane_b32 v11, s10, 1\n" \
M_"a[60:63], a[172:175], a[140:143], a[60:63]\n" \
"s_lshl_b32 s6, s6, 10\n" \
"s_lshl_b32 s10, s3, 8\n" \
M_"a[60:63], a[236:239], a[204:207], a[60:63]\n" \
"s_addk_i32 s84, 0x2000\n" \
M_"a[52:55], a[164:167], a[140:143], a[52:55]\n" \
B_"v25, s[76:79], s84 offen offset:3072 lds\n" \
M_"a[52:55], a[228:231], a[204:207], a[52:55]\n" \
"s_or_b32 s6, s6, s10\n" \
"s_or_b32 s10, s6, 0x5c00200\n" \
M_"a[48:51], a[160:163], a[140:143], a[48:51]\n" \
"v_writelane_b32 v11, s10, 2\n" \
"s_lshl_b32 s10, s7, 2\n" \
M_"a[48:51], a[224:227], a[204:207], a[48:51]\n" \
"s_or_b32 s11, s10, s6\n" \
"s_or_b32 s11, s11, 0x5c00000\n" \
"s_waitcnt lgkmcnt(15)\n" \
M_"a[64:67], a[164:167], v[248:251], a[64:67]\n" \
"v_writelane_b32 v11, s11, 3\n" \
"s_or_b32 s6, s6, 0x5c00080\n" \
M_"a[64:67], a[228:231], v[48:51], a[64:67]\n" \
"v_writelane_b32 v11, s6, 4\n" \
"s_or_b32 s6, s6, s10\n" \
M_"a[80:83], a[172:175], v[248:251], a[80:83]\n" \
"s_add_i32 s10, s3, s2\n" \
"v_writelane_b32 v11, s6, 5\n" \
M_"a[80:83], a[236:239], v[48:51], a[80:83]\n" \
"s_mov_b32 s6, 2.0\n" \
"s_mov_b32 s11, 0x5bc0000\n" \
M_"a[68:71], v[248:251], a[168:171], a[68:71]\n" \
"s_cmp_lg_u32 s10, 0\n" \
"s_cselect_b32 s10, s6, s11\n" \
"s_waitcnt vmcnt(10) lgkmcnt(0)\n" \
M_"a[68:71], v[48:51], a[232:235], a[68:71]\n" \
"s_barrier\n" \
"v_xor_b32_e32 v28, 0x20000, v28\n" \
"v_xor_b32_e32 v29, 0x20000, v29\n" \
M_"v[52:55], a[176:179], a[128:131], v[52:55]\n" \
"s_add_u32 m0, s40, 0x8000\n" \
"s_add_u32 s85, s39, 0x6000\n" \
M_"v[52:55], a[240:243], a[192:195], v[52:55]\n" \
B_"v20, s[80:83], s85 offen sc1 lds\n" \
M_"v[56:59], a[180:183], a[128:131], v[56:59]\n" \
X_"a[160:161], v32 offset:0\n" \
"s_lshr_b32 s14, s2, 3\n" \
M_"v[56:59], a[244:247], a[192:195], v[56:59]\n" \
X_"a[162:163], v32 offset:512\n" \
"v_writelane_b32 v11, s10, 57\n" \
M_"v[60:63], a[184:187], a[128:131], v[60:63]\n" \
"s_add_u32 m0, s40, 0x9000\n" \
"s_add_u32 s85, s39, 0x7000\n" \
M_"v[60:63], a[248:251], a[192:195], v[60:63]\n" \
B_"v20, s[80:83], s85 offen sc1 lds\n" \
M_"v[64:67], a[188:191], a[128:131], v[64:67]\n" \
R_"a[128:131], v28 offset:0\n" \
"s_lshl_b32 s10, s5, 5\n" \
M_"v[64:67], a[252:255], a[192:195], v[64:67]\n" \
R_"a[192:195], v29 offset:0\n" \
"s_min_u32 s15, s3, 2\n" \
M_"v[80:83], a[188:191], a[132:135], v[80:83]\n" \
"s_add_u32 s88, s41, 0x1000\n" \
"buffer_store_dwordx4 v[236:239], v20, s[80:83], s88 offen offset:2048 sc1\n" \
M_"v[80:83], a[252:255], a[196:199], v[80:83]\n" \
X_"a[224:225], v32 offset:4096\n" \
"s_mul_i32 s11, s7, 0x120\n" \
M_"v[76:79], a[184:187], a[132:135], v[76:79]\n" \
X_"a[226:227], v32 offset:4608\n" \
M_"v[76:79], a[248:251], a[196:199], v[76:79]\n" \
X_"a[164:165], v33 offset:0\n" \
M_"v[72:75], a[180:183], a[132:135], v[72:75]\n" \
"s_add_u32 m0, s40, 0xb000\n" \
"s_movk_i32 s84, 0x180\n" \
"s_nop 0\n" \
M_"v[72:75], a[244:247], a[196:199], v[72:75]\n" \
B_"v21, s[76:79], s84 offen offset:0 lds\n" \
M_"v[68:71], a[176:179], a[132:135], v[68:71]\n" \
R_"a[132:135], v28 offset:2048\n" \
M_"v[68:71], a[240:243], a[196:199], v[68:71]\n" \
R_"a[196:199], v29 offset:2048\n" \
M_"v[84:87], a[176:179], a[136:139], v[84:87]\n" \
X_"a[166:167], v33 offset:512\n" \
M_"v[84:87], a[240:243], a[200:203], v[84:87]\n" \
"s_add_u32 m0, s31, 0x8000\n" \
"s_mov_b32 s84, 0x180000\n" \
M_"v[88:91], a[180:183], a[136:139], v[88:91]\n" \
B_"v26, s[68:71], s84 offen offset:0 lds\n" \
M_"v[88:91], a[244:247], a[200:203], v[88:91]\n" \
X_"a[228:229], v33 offset:4096\n" \
M_"v[92:95], a[184:187], a[136:139], v[92:95]\n" \
X_"a[230:231], v33 offset:4608\n" \
M_"v[92:95], a[248:251], a[200:203], v[92:95]\n" \
"s_add_u32 s16, s22, s11\n" \
"s_addc_u32 s17, s23, 0\n" \
M_"v[96:99], a[188:191], a[136:139], v[96:99]\n" \
R_"a[136:139], v28 offset:4096\n" \
M_"v[96:99], a[252:255], a[200:203], v[96:99]\n" \
R_"a[200:203], v29 offset:4096\n" \
M_"v[112:115], a[188:191], a[140:143], v[112:115]\n" \
"s_addk_i32 s84, 0x7c00\n" \
M_"v[112:115], a[252:255], a[204:207], v[112:115]\n" \
B_"v26, s[68:71], s84 offen offset:1024 lds\n" \
M_"v[108:111], a[184:187], a[140:143], v[108:111]\n" \
"s_addk_i32 s84, 0x7c00\n" \
M_"v[108:111], a[248:251], a[204:207], v[108:111]\n" \
B_"v27, s[68:71], s84 offen offset:2048 lds\n" \
M_"v[104:107], a[180:183], a[140:143], v[104:107]\n" \
X_"a[172:173], v35 offset:0\n" \
M_"v[104:107], a[244:247], a[204:207], v[104:107]\n" \
X_"a[174:175], v35 offset:512\n" \
M_"v[100:103], a[176:179], a[140:143], v[100:103]\n" \
R_"a[140:143], v28 offset:6144\n" \
M_"v[100:103], a[240:243], a[204:207], v[100:103]\n" \
R_"a[204:207], v29 offset:6144\n" \
M_"v[116:119], a[176:179], a[144:147], v[116:119]\n" \
"s_addk_i32 s84, 0x7c00\n" \
M_"v[116:119], a[240:243], a[208:211], v[116:119]\n" \
B_"v27, s[68:71], s84 offen offset:3072 lds\n" \
M_"v[120:123], a[180:183], a[144:147], v[120:123]\n" \
"s_add_u32 m0, s30, 0x8000\n" \
"s_movk_i32 s84, 0x200\n" \
M_"v[120:123], a[244:247], a[208:211], v[120:123]\n" \
B_"v24, s[76:79], s84 offen offset:0 lds\n" \
M_"v[124:127], a[184:187], a[144:147], v[124:127]\n" \
X_"a[236:237], v35 offset:4096\n" \
M_"v[124:127], a[248:251], a[208:211], v[124:127]\n" \
X_"a[238:239], v35 offset:4608\n" \
M_"v[128:131], a[188:191], a[144:147], v[128:131]\n" \
R_"a[144:147], v28 offset:8192\n" \
M_"v[128:131], a[252:255], a[208:211], v[128:131]\n" \
R_"a[208:211], v29 offset:8192\n" \
M_"v[144:147], a[188:191], a[148:151], v[144:147]\n" \
"s_add_u32 s89, s41, 0x2000\n" \
"buffer_store_dwordx4 v[240:243], v20, s[80:83], s89 offen offset:2048 sc1\n" \
M_"v[144:147], a[252:255], a[212:215], v[144:147]\n" \
X_"a[168:169], v34 offset:0\n" \
M_"v[140:143], a[184:187], a[148:151], v[140:143]\n" \
X_"a[170:171], v34 offset:512\n" \
M_"v[140:143], a[248:251], a[212:215], v[140:143]\n" \
"s_movk_i32 s84, 0x2200\n" \
M_"v[136:139], a[180:183], a[148:151], v[136:139]\n" \
B_"v25, s[76:79], s84 offen offset:1024 lds\n" \
M_"v[136:139], a[244:247], a[212:215], v[136:139]\n" \
X_"a[232:233], v34 offset:4096\n" \
M_"v[132:135], a[176:179], a[148:151], v[132:135]\n" \
R_"a[148:151], v28 offset:10240\n" \
M_"v[132:135], a[240:243], a[212:215], v[132:135]\n" \
R_"a[212:215], v29 offset:10240\n" \
M_"v[148:151], a[176:179], a[152:155], v[148:151]\n" \
"s_movk_i32 s84, 0x4200\n" \
M_"v[148:151], a[240:243], a[216:219], v[148:151]\n" \
B_"v24, s[76:79], s84 offen offset:2048 lds\n" \
M_"v[152:155], a[180:183], a[152:155], v[152:155]\n" \
X_"a[234:235], v34 offset:4608\n" \
M_"v[152:155], a[244:247], a[216:219], v[152:155]\n" \
"s_movk_i32 s84, 0x6200\n" \
"s_nop 0\n" \
M_"v[156:159], a[184:187], a[152:155], v[156:159]\n" \
B_"v25, s[76:79], s84 offen offset:3072 lds\n" \
M_"v[156:159], a[248:251], a[216:219], v[156:159]\n" \
R_"v[248:251], v22 offset:0\n" \
M_"v[160:163], a[188:191], a[152:155], v[160:163]\n" \
R_"a[152:155], v28 offset:12288\n" \
M_"v[160:163], a[252:255], a[216:219], v[160:163]\n" \
R_"a[216:219], v29 offset:12288\n" \
M_"v[176:179], a[188:191], a[156:159], v[176:179]\n" \
R_"v[48:51], v23 offset:0\n" \
M_"v[176:179], a[252:255], a[220:223], v[176:179]\n" \
"s_add_u32 s18, s24, s11\n" \
"v_writelane_b32 v11, s16, 9\n" \
"s_addc_u32 s19, s25, 0\n" \
M_"v[172:175], a[184:187], a[156:159], v[172:175]\n" \
"s_and_b32 s20, s17, 0xffff\n" \
"v_writelane_b32 v11, s20, 10\n" \
M_"v[172:175], a[248:251], a[220:223], v[172:175]\n" \
"s_and_b32 s20, s19, 0xffff\n" \
"v_writelane_b32 v11, s18, 11\n" \
M_"v[168:171], a[180:183], a[156:159], v[168:171]\n" \
"s_cmpk_lt_u32 s3, 3\n" \
"s_cselect_b32 s16, s16, s18\n" \
M_"v[168:171], a[244:247], a[220:223], v[168:171]\n" \
"s_movk_i32 s18, 0xfff\n" \
"v_writelane_b32 v11, s20, 12\n" \
M_"v[164:167], a[176:179], a[156:159], v[164:167]\n" \
R_"a[156:159], v28 offset:14336\n" \
"s_and_b32 s20, s29, 0xffff\n" \
M_"v[164:167], a[240:243], a[220:223], v[164:167]\n" \
R_"a[220:223], v29 offset:14336\n" \
"s_waitcnt lgkmcnt(15)\n" \
M_"a[0:3], a[160:163], a[128:131], a[0:3]\n" \
"s_add_u32 m0, s30, 0x9000\n" \
"s_addk_i32 s84, 0x3000\n" \
"s_nop 0\n" \
M_"a[0:3], a[224:227], a[192:195], a[0:3]\n" \
B_"v24, s[76:79], s84 offen offset:0 lds\n" \
M_"a[4:7], a[164:167], a[128:131], a[4:7]\n" \
X_"a[176:177], v36 offset:0\n" \
"v_writelane_b32 v11, s28, 13\n" \
M_"a[4:7], a[228:231], a[192:195], a[4:7]\n" \
X_"a[178:179], v36 offset:1024\n" \
"v_writelane_b32 v11, s20, 14\n" \
M_"a[12:15], a[172:175], a[128:131], a[12:15]\n" \
X_"a[240:241], v36 offset:8192\n" \
"v_writelane_b32 v11, s16, 15\n" \
"s_waitcnt lgkmcnt(15)\n" \
M_"a[12:15], a[236:239], a[192:195], a[12:15]\n" \
"s_add_u32 s88, s41, 0x2000\n" \
"buffer_store_dwordx4 v[244:247], v20, s[80:83], s88 offen offset:3072 sc1\n" \
"s_waitcnt lgkmcnt(13)\n" \
M_"a[8:11], a[128:131], a[168:171], a[8:11]\n" \
X_"a[242:243], v36 offset:9216\n" \
"s_waitcnt lgkmcnt(10)\n" \
M_"a[8:11], a[192:195], a[232:235], a[8:11]\n" \
X_"a[180:181], v37 offset:0\n" \
M_"a[24:27], a[132:135], a[168:171], a[24:27]\n" \
X_"a[182:183], v37 offset:1024\n" \
M_"a[24:27], a[196:199], a[232:235], a[24:27]\n" \
X_"a[244:245], v37 offset:8192\n" \
M_"a[28:31], a[172:175], a[132:135], a[28:31]\n" \
"s_cmpk_lt_u32 s3, 3\n" \
"s_cselect_b32 s16, s17, s19\n" \
M_"a[28:31], a[236:239], a[196:199], a[28:31]\n" \
"s_addk_i32 s84, 0x2000\n" \
M_"a[20:23], a[164:167], a[132:135], a[20:23]\n" \
B_"v25, s[76:79], s84 offen offset:1024 lds\n" \
M_"a[20:23], a[228:231], a[196:199], a[20:23]\n" \
X_"a[246:247], v37 offset:9216\n" \
"s_and_b32 s16, s16, 0xffff\n" \
M_"a[16:19], a[160:163], a[132:135], a[16:19]\n" \
X_"a[184:185], v38 offset:0\n" \
"v_writelane_b32 v11, s16, 16\n" \
M_"a[16:19], a[224:227], a[196:199], a[16:19]\n" \
X_"a[186:187], v38 offset:1024\n" \
"s_mul_i32 s16, s5, 0x48\n" \
M_"a[32:35], a[160:163], a[136:139], a[32:35]\n" \
"s_addk_i32 s84, 0x2000\n" \
"s_nop 0\n" \
".fill (L_pb + 24288 - .) / 4, 4, 0xBF800000\n" \
"L_pp6:\n" \
"s_cbranch_execz L_pret\n" \
"s_nop 0\n" \
M_"a[32:35], a[224:227], a[200:203], a[32:35]\n" \
B_"v24, s[76:79], s84 offen offset:2048 lds\n" \
M_"a[36:39], a[164:167], a[136:139], a[36:39]\n" \
X_"a[248:249], v38 offset:8192\n" \
"s_add_i32 s17, s16, 0x47\n" \
M_"a[36:39], a[228:231], a[200:203], a[36:39]\n" \
X_"a[250:251], v38 offset:9216\n" \
M_"a[44:47], a[172:175], a[136:139], a[44:47]\n" \
X_"a[188:189], v39 offset:0\n" \
M_"a[44:47], a[236:239], a[200:203], a[44:47]\n" \
X_"a[190:191], v39 offset:1024\n" \
M_"a[40:43], a[136:139], a[168:171], a[40:43]\n" \
"s_add_u32 s89, s41, 0x3000\n" \
"buffer_store_dwordx4 v[184:187], v20, s[80:83], s89 offen offset:3072 sc1\n" \
M_"a[40:43], a[200:203], a[232:235], a[40:43]\n" \
X_"a[252:253], v39 offset:8192\n" \
M_"a[56:59], a[140:143], a[168:171], a[56:59]\n" \
X_"a[254:255], v39 offset:9216\n" \
M_"a[56:59], a[204:207], a[232:235], a[56:59]\n" \
"s_cmpk_lt_u32 s3, 3\n" \
"s_cselect_b32 s17, s17, s18\n" \
M_"a[60:63], a[172:175], a[140:143], a[60:63]\n" \
"s_mulk_i32 s17, 0x480\n" \
"s_addk_i32 s17, 0x120\n" \
M_"a[60:63], a[236:239], a[204:207], a[60:63]\n" \
"s_addk_i32 s84, 0x2000\n" \
M_"a[52:55], a[164:167], a[140:143], a[52:55]\n" \
B_"v25, s[76:79], s84 offen offset:3072 lds\n" \
M_"a[52:55], a[228:231], a[204:207], a[52:55]\n" \
"v_writelane_b32 v11, s17, 17\n" \
"s_lshl_b32 s17, s3, 5\n" \
M_"a[48:51], a[160:163], a[140:143], a[48:51]\n" \
"s_add_i32 s16, s16, s17\n" \
"s_mulk_i32 s16, 0x480\n" \
M_"a[48:51], a[224:227], a[204:207], a[48:51]\n" \
"s_mul_i32 s17, s12, 0x24000\n" \
"s_add_i32 s18, s17, 0x1b000\n" \
"s_waitcnt lgkmcnt(15)\n" \
M_"a[64:67], a[164:167], v[248:251], a[64:67]\n" \
"s_cmpk_lt_u32 s3, 3\n" \
"s_cselect_b32 s16, s16, s18\n" \
M_"a[64:67], a[228:231], v[48:51], a[64:67]\n" \
"s_xor_b32 s18, s3, 2\n" \
"v_writelane_b32 v11, s16, 18\n" \
M_"a[80:83], a[172:175], v[248:251], a[80:83]\n" \
"s_mul_i32 s16, s15, 0x2400\n" \
"s_mul_i32 s15, s15, 0x9000\n" \
M_"a[80:83], a[236:239], v[48:51], a[80:83]\n" \
"s_bitset1_b32 s16, 17\n" \
"v_writelane_b32 v11, s16, 22\n" \
M_"a[68:71], v[248:251], a[168:171], a[68:71]\n" \
"s_add_i32 s17, s17, s15\n" \
"s_mul_i32 s15, s3, 0x2400\n" \
"s_waitcnt vmcnt(11) lgkmcnt(0)\n" \
M_"a[68:71], v[48:51], a[232:235], a[68:71]\n" \
"s_barrier\n" \
"v_xor_b32_e32 v28, 0x20000, v28\n" \
"v_xor_b32_e32 v29, 0x20000, v29\n" \
M_"v[52:55], a[176:179], a[128:131], v[52:55]\n" \
"s_add_u32 m0, s40, 0x0\n" \
"s_nop 0\n" \
"s_add_u32 s85, s39, 0x8000\n" \
M_"v[52:55], a[240:243], a[192:195], v[52:55]\n" \
B_"v20, s[80:83], s85 offen sc1 lds\n" \
M_"v[56:59], a[180:183], a[128:131], v[56:59]\n" \
X_"a[160:161], v32 offset:32768\n" \
"v_writelane_b32 v11, s17, 29\n" \
M_"v[56:59], a[244:247], a[192:195], v[56:59]\n" \
X_"a[162:163], v32 offset:33280\n" \
"s_add_i32 s16, s15, 0xa400\n" \
M_"v[60:63], a[184:187], a[128:131], v[60:63]\n" \
"s_add_u32 m0, s40, 0x1000\n" \
"s_add_u32 s85, s39, 0x9000\n" \
M_"v[60:63], a[248:251], a[192:195], v[60:63]\n" \
B_"v20, s[80:83], s85 offen sc1 lds\n" \
M_"v[64:67], a[188:191], a[128:131], v[64:67]\n" \
R_"a[128:131], v28 offset:0\n" \
"s_mov_b32 s17, 0x8000\n" \
M_"v[64:67], a[252:255], a[192:195], v[64:67]\n" \
R_"a[192:195], v29 offset:0\n" \
M_"v[80:83], a[188:191], a[132:135], v[80:83]\n" \
"s_add_u32 s88, s41, 0x3000\n" \
"buffer_store_dwordx4 v[188:191], v20, s[80:83], s88 offen offset:2048 sc1\n" \
M_"v[80:83], a[252:255], a[196:199], v[80:83]\n" \
X_"a[224:225], v32 offset:36864\n" \
M_"v[76:79], a[184:187], a[132:135], v[76:79]\n" \
X_"a[226:227], v32 offset:37376\n" \
M_"v[76:79], a[248:251], a[196:199], v[76:79]\n" \
X_"a[164:165], v33 offset:32768\n" \
M_"v[72:75], a[180:183], a[132:135], v[72:75]\n" \
"s_add_u32 m0, s40, 0x3000\n" \
"s_movk_i32 s84, 0x200\n" \
M_"v[72:75], a[244:247], a[196:199], v[72:75]\n" \
B_"v21, s[76:79], s84 offen offset:0 lds\n" \
M_"v[68:71], a[176:179], a[132:135], v[68:71]\n" \
R_"a[132:135], v28 offset:2048\n" \
M_"v[68:71], a[240:243], a[196:199], v[68:71]\n" \
R_"a[196:199], v29 offset:2048\n" \
M_"v[84:87], a[176:179], a[136:139], v[84:87]\n" \
X_"a[166:167], v33 offset:33280\n" \
M_"v[84:87], a[240:243], a[200:203], v[84:87]\n" \
"s_add_u32 m0, s31, 0x0\n" \
"s_mov_b32 s84, 0x200000\n" \
M_"v[88:91], a[180:183], a[136:139], v[88:91]\n" \
B_"v26, s[68:71], s84 offen offset:0 lds\n" \
M_"v[88:91], a[244:247], a[200:203], v[88:91]\n" \
X_"a[228:229], v33 offset:36864\n" \
M_"v[92:95], a[184:187], a[136:139], v[92:95]\n" \
X_"a[230:231], v33 offset:37376\n" \
M_"v[92:95], a[248:251], a[200:203], v[92:95]\n" \
"s_cmpk_lt_u32 s3, 3\n" \
"s_cselect_b32 s16, s16, s17\n" \
M_"v[96:99], a[188:191], a[136:139], v[96:99]\n" \
R_"a[136:139], v28 offset:4096\n" \
"s_mov_b32 s17, 0x1b000\n" \
M_"v[96:99], a[252:255], a[200:203], v[96:99]\n" \
R_"a[200:203], v29 offset:4096\n" \
M_"v[112:115], a[188:191], a[140:143], v[112:115]\n" \
"s_addk_i32 s84, 0x7c00\n" \
M_"v[112:115], a[252:255], a[204:207], v[112:115]\n" \
B_"v26, s[68:71], s84 offen offset:1024 lds\n" \
M_"v[108:111], a[184:187], a[140:143], v[108:111]\n" \
"s_addk_i32 s84, 0x7c00\n" \
M_"v[108:111], a[248:251], a[204:207], v[108:111]\n" \
B_"v27, s[68:71], s84 offen offset:2048 lds\n" \
M_"v[104:107], a[180:183], a[140:143], v[104:107]\n" \
X_"a[172:173], v35 offset:32768\n" \
M_"v[104:107], a[244:247], a[204:207], v[104:107]\n" \
X_"a[174:175], v35 offset:33280\n" \
M_"v[100:103], a[176:179], a[140:143], v[100:103]\n" \
R_"a[140:143], v28 offset:6144\n" \
M_"v[100:103], a[240:243], a[204:207], v[100:103]\n" \
R_"a[204:207], v29 offset:6144\n" \
M_"v[116:119], a[176:179], a[144:147], v[116:119]\n" \
"s_addk_i32 s84, 0x7c00\n" \
M_"v[116:119], a[240:243], a[208:211], v[116:119]\n" \
B_"v27, s[68:71], s84 offen offset:3072 lds\n" \
M_"v[120:123], a[180:183], a[144:147], v[120:123]\n" \
"s_add_u32 m0, s30, 0x20000\n" \
"s_movk_i32 s84, 0x280\n" \
M_"v[120:123], a[244:247], a[208:211], v[120:123]\n" \
B_"v24, s[76:79], s84 offen offset:0 lds\n" \
M_"v[124:127], a[184:187], a[144:147], v[124:127]\n" \
X_"a[236:237], v35 offset:36864\n" \
M_"v[124:127], a[248:251], a[208:211], v[124:127]\n" \
X_"a[238:239], v35 offset:37376\n" \
M_"v[128:131], a[188:191], a[144:147], v[128:131]\n" \
R_"a[144:147], v28 offset:8192\n" \
M_"v[128:131], a[252:255], a[208:211], v[128:131]\n" \
R_"a[208:211], v29 offset:8192\n" \
M_"v[144:147], a[188:191], a[148:151], v[144:147]\n" \
"s_add_u32 s89, s42, 0x0\n" \
"buffer_store_dwordx4 v[216:219], v20, s[80:83], s89 offen offset:0 sc1\n" \
M_"v[144:147], a[252:255], a[212:215], v[144:147]\n" \
X_"a[168:169], v34 offset:32768\n" \
M_"v[140:143], a[184:187], a[148:151], v[140:143]\n" \
X_"a[170:171], v34 offset:33280\n" \
M_"v[140:143], a[248:251], a[212:215], v[140:143]\n" \
"s_movk_i32 s84, 0x2280\n" \
M_"v[136:139], a[180:183], a[148:151], v[136:139]\n" \
B_"v25, s[76:79], s84 offen offset:1024 lds\n" \
M_"v[136:139], a[244:247], a[212:215], v[136:139]\n" \
X_"a[232:233], v34 offset:36864\n" \
M_"v[132:135], a[176:179], a[148:151], v[132:135]\n" \
R_"a[148:151], v28 offset:10240\n" \
M_"v[132:135], a[240:243], a[212:215], v[132:135]\n" \
R_"a[212:215], v29 offset:10240\n" \
M_"v[148:151], a[176:179], a[152:155], v[148:151]\n" \
"s_movk_i32 s84, 0x4280\n" \
M_"v[148:151], a[240:243], a[216:219], v[148:151]\n" \
B_"v24, s[76:79], s84 offen offset:2048 lds\n" \
M_"v[152:155], a[180:183], a[152:155], v[152:155]\n" \
X_"a[234:235], v34 offset:37376\n" \
M_"v[152:155], a[244:247], a[216:219], v[152:155]\n" \
"s_movk_i32 s84, 0x6280\n" \
M_"v[156:159], a[184:187], a[152:155], v[156:159]\n" \
B_"v25, s[76:79], s84 offen offset:3072 lds\n" \
M_"v[156:159], a[248:251], a[216:219], v[156:159]\n" \
R_"v[248:251], v22 offset:32768\n" \
M_"v[160:163], a[188:191], a[152:155], v[160:163]\n" \
R_"a[152:155], v28 offset:12288\n" \
M_"v[160:163], a[252:255], a[216:219], v[160:163]\n" \
R_"a[216:219], v29 offset:12288\n" \
M_"v[176:179], a[188:191], a[156:159], v[176:179]\n" \
R_"v[48:51], v23 offset:32768\n" \
M_"v[176:179], a[252:255], a[220:223], v[176:179]\n" \
"s_cmp_lg_u32 s18, 0\n" \
"s_cselect_b32 s17, s16, s17\n" \
M_"v[172:175], a[184:187], a[156:159], v[172:175]\n" \
"v_writelane_b32 v11, s16, 23\n" \
"s_or_b32 s16, s15, 0x10000\n" \
M_"v[172:175], a[248:251], a[220:223], v[172:175]\n" \
"v_writelane_b32 v11, s17, 24\n" \
"s_add_i32 s17, s15, 0x19000\n" \
M_"v[168:171], a[180:183], a[156:159], v[168:171]\n" \
"v_writelane_b32 v11, s16, 25\n" \
"s_mov_b32 s16, 0\n" \
M_"v[168:171], a[244:247], a[220:223], v[168:171]\n" \
"s_cmpk_lt_u32 s3, 3\n" \
"s_cselect_b32 s17, s17, s16\n" \
M_"v[164:167], a[176:179], a[156:159], v[164:167]\n" \
R_"a[156:159], v28 offset:14336\n" \
"s_bitset1_b32 s15, 17\n" \
M_"v[164:167], a[240:243], a[220:223], v[164:167]\n" \
R_"a[220:223], v29 offset:14336\n" \
"s_waitcnt lgkmcnt(15)\n" \
M_"a[0:3], a[160:163], a[128:131], a[0:3]\n" \
"s_add_u32 m0, s30, 0x21000\n" \
"s_addk_i32 s84, 0x3000\n" \
M_"a[0:3], a[224:227], a[192:195], a[0:3]\n" \
B_"v24, s[76:79], s84 offen offset:0 lds\n" \
M_"a[4:7], a[164:167], a[128:131], a[4:7]\n" \
X_"a[176:177], v36 offset:32768\n" \
"v_writelane_b32 v11, s17, 26\n" \
M_"a[4:7], a[228:231], a[192:195], a[4:7]\n" \
X_"a[178:179], v36 offset:33792\n" \
"s_movk_i32 s17, 0x2400\n" \
M_"a[12:15], a[172:175], a[128:131], a[12:15]\n" \
X_"a[240:241], v36 offset:40960\n" \
"s_waitcnt lgkmcnt(15)\n" \
M_"a[12:15], a[236:239], a[192:195], a[12:15]\n" \
"s_add_u32 s88, s42, 0x1000\n" \
"buffer_store_dwordx4 v[220:223], v20, s[80:83], s88 offen offset:0 sc1\n" \
"s_waitcnt lgkmcnt(13)\n" \
M_"a[8:11], a[128:131], a[168:171], a[8:11]\n" \
X_"a[242:243], v36 offset:41984\n" \
"s_waitcnt lgkmcnt(10)\n" \
M_"a[8:11], a[192:195], a[232:235], a[8:11]\n" \
X_"a[180:181], v37 offset:32768\n" \
M_"a[24:27], a[132:135], a[168:171], a[24:27]\n" \
X_"a[182:183], v37 offset:33792\n" \
M_"a[24:27], a[196:199], a[232:235], a[24:27]\n" \
X_"a[244:245], v37 offset:40960\n" \
M_"a[28:31], a[172:175], a[132:135], a[28:31]\n" \
"s_cmpk_lt_u32 s3, 3\n" \
"s_cselect_b32 s15, s15, s17\n" \
M_"a[28:31], a[236:239], a[196:199], a[28:31]\n" \
"s_addk_i32 s84, 0x2000\n" \
M_"a[20:23], a[164:167], a[132:135], a[20:23]\n" \
B_"v25, s[76:79], s84 offen offset:1024 lds\n" \
M_"a[20:23], a[228:231], a[196:199], a[20:23]\n" \
X_"a[246:247], v37 offset:41984\n" \
"v_writelane_b32 v11, s15, 27\n" \
M_"a[16:19], a[160:163], a[132:135], a[16:19]\n" \
X_"a[184:185], v38 offset:32768\n" \
"s_lshl_b32 s15, s3, 10\n" \
M_"a[16:19], a[224:227], a[196:199], a[16:19]\n" \
X_"a[186:187], v38 offset:33792\n" \
"s_addk_i32 s15, 0x4800\n" \
M_"a[32:35], a[160:163], a[136:139], a[32:35]\n" \
"s_addk_i32 s84, 0x2000\n" \
M_"a[32:35], a[224:227], a[200:203], a[32:35]\n" \
B_"v24, s[76:79], s84 offen offset:2048 lds\n" \
M_"a[36:39], a[164:167], a[136:139], a[36:39]\n" \
X_"a[248:249], v38 offset:40960\n" \
"v_writelane_b32 v11, s15, 28\n" \
M_"a[36:39], a[228:231], a[200:203], a[36:39]\n" \
X_"a[250:251], v38 offset:41984\n" \
"s_add_i32 s15, s14, 1\n" \
M_"a[44:47], a[172:175], a[136:139], a[44:47]\n" \
X_"a[188:189], v39 offset:32768\n" \
"s_and_b32 s15, s15, 3\n" \
M_"a[44:47], a[236:239], a[200:203], a[44:47]\n" \
X_"a[190:191], v39 offset:33792\n" \
"s_or_b32 s15, s15, s4\n" \
M_"a[40:43], a[136:139], a[168:171], a[40:43]\n" \
"s_add_u32 s89, s42, 0x0\n" \
"buffer_store_dwordx4 v[192:195], v20, s[80:83], s89 offen offset:2048 sc1\n" \
M_"a[40:43], a[200:203], a[232:235], a[40:43]\n" \
X_"a[252:253], v39 offset:40960\n" \
"s_lshl_b32 s17, s15, 2\n" \
M_"a[56:59], a[140:143], a[168:171], a[56:59]\n" \
X_"a[254:255], v39 offset:41984\n" \
"s_or_b32 s17, s17, s3\n" \
M_"a[56:59], a[204:207], a[232:235], a[56:59]\n" \
"s_mul_i32 s17, s17, 0x9000\n" \
"v_writelane_b32 v11, s17, 30\n" \
M_"a[60:63], a[172:175], a[140:143], a[60:63]\n" \
"s_lshl_b32 s17, s12, 2\n" \
"s_or_b32 s17, s17, s3\n" \
M_"a[60:63], a[236:239], a[204:207], a[60:63]\n" \
"s_addk_i32 s84, 0x2000\n" \
M_"a[52:55], a[164:167], a[140:143], a[52:55]\n" \
B_"v25, s[76:79], s84 offen offset:3072 lds\n" \
M_"a[52:55], a[228:231], a[204:207], a[52:55]\n" \
"s_add_i32 s14, s14, -1\n" \
"s_xor_b32 s17, s17, 8\n" \
M_"a[48:51], a[160:163], a[140:143], a[48:51]\n" \
"s_and_b32 s14, s14, 3\n" \
"s_mul_i32 s17, s17, 0x9000\n" \
M_"a[48:51], a[224:227], a[204:207], a[48:51]\n" \
"s_or_b32 s14, s14, s4\n" \
"v_writelane_b32 v11, s17, 31\n" \
"s_waitcnt lgkmcnt(15)\n" \
M_"a[64:67], a[164:167], v[248:251], a[64:67]\n" \
"s_lshl_b32 s17, s14, 2\n" \
"s_or_b32 s17, s17, s3\n" \
M_"a[64:67], a[228:231], v[48:51], a[64:67]\n" \
"s_mul_i32 s17, s17, 0x9000\n" \
"v_writelane_b32 v11, s17, 32\n" \
M_"a[80:83], a[172:175], v[248:251], a[80:83]\n" \
"s_or_b32 s17, s8, s13\n" \
"s_mul_i32 s17, s17, 0x24800\n" \
M_"a[80:83], a[236:239], v[48:51], a[80:83]\n" \
"s_lshl_b32 s15, s15, 12\n" \
"s_add_i32 s18, s17, 0x3004800\n" \
M_"a[68:71], v[248:251], a[168:171], a[68:71]\n" \
"s_lshl_b32 s12, s12, 12\n" \
"s_add_i32 s19, s18, s15\n" \
"s_waitcnt vmcnt(11) lgkmcnt(0)\n" \
M_"a[68:71], v[48:51], a[232:235], a[68:71]\n" \
"s_barrier\n" \
M_"v[52:55], a[176:179], a[128:131], v[52:55]\n" \
"s_add_u32 m0, s40, 0x8000\n" \
"s_add_u32 s85, s39, 0xa000\n" \
M_"v[52:55], a[240:243], a[192:195], v[52:55]\n" \
B_"v20, s[80:83], s85 offen sc1 lds\n" \
M_"v[56:59], a[180:183], a[128:131], v[56:59]\n" \
X_"a[160:161], v32 offset:0\n" \
"v_accvgpr_read_b32 v224, a120\n" \
M_"v[56:59], a[244:247], a[192:195], v[56:59]\n" \
X_"a[162:163], v32 offset:512\n" \
"v_accvgpr_read_b32 v225, a121\n" \
M_"v[60:63], a[184:187], a[128:131], v[60:63]\n" \
"s_add_u32 m0, s40, 0x9000\n" \
"s_add_u32 s85, s39, 0xb000\n" \
M_"v[60:63], a[248:251], a[192:195], v[60:63]\n" \
B_"v20, s[80:83], s85 offen sc1 lds\n" \
M_"v[64:67], a[188:191], a[128:131], v[64:67]\n" \
R_"a[128:131], v28 offset:32768\n" \
"v_accvgpr_read_b32 v226, a122\n" \
M_"v[64:67], a[252:255], a[192:195], v[64:67]\n" \
R_"a[192:195], v29 offset:32768\n" \
"v_accvgpr_read_b32 v227, a123\n" \
M_"v[80:83], a[188:191], a[132:135], v[80:83]\n" \
"s_add_u32 s88, s42, 0x0\n" \
"s_nop 0\n" \
"buffer_store_dwordx4 v[196:199], v20, s[80:83], s88 offen offset:3072 sc1\n" \
M_"v[80:83], a[252:255], a[196:199], v[80:83]\n" \
X_"a[224:225], v32 offset:4096\n" \
"v_accvgpr_read_b32 v180, a124\n" \
M_"v[76:79], a[184:187], a[132:135], v[76:79]\n" \
X_"a[226:227], v32 offset:4608\n" \
"v_accvgpr_read_b32 v181, a125\n" \
M_"v[76:79], a[248:251], a[196:199], v[76:79]\n" \
X_"a[164:165], v33 offset:0\n" \
"v_accvgpr_read_b32 v182, a126\n" \
M_"v[72:75], a[180:183], a[132:135], v[72:75]\n" \
"s_add_u32 m0, s40, 0xb000\n" \
"s_movk_i32 s84, 0x280\n" \
"s_nop 0\n" \
M_"v[72:75], a[244:247], a[196:199], v[72:75]\n" \
B_"v21, s[76:79], s84 offen offset:0 lds\n" \
M_"v[68:71], a[176:179], a[132:135], v[68:71]\n" \
R_"a[132:135], v28 offset:34816\n" \
"v_accvgpr_read_b32 v183, a127\n" \
M_"v[68:71], a[240:243], a[196:199], v[68:71]\n" \
R_"a[196:199], v29 offset:34816\n" \
C_"v224, v224, v225\n" \
M_"v[84:87], a[176:179], a[136:139], v[84:87]\n" \
X_"a[166:167], v33 offset:512\n" \
C_"v225, v226, v227\n" \
M_"v[84:87], a[240:243], a[200:203], v[84:87]\n" \
"s_add_u32 m0, s31, 0x8000\n" \
"s_mov_b32 s84, 0x280000\n" \
M_"v[88:91], a[180:183], a[136:139], v[88:91]\n" \
B_"v26, s[68:71], s84 offen offset:0 lds\n" \
M_"v[88:91], a[244:247], a[200:203], v[88:91]\n" \
X_"a[228:229], v33 offset:4096\n" \
C_"v226, v180, v181\n" \
M_"v[92:95], a[184:187], a[136:139], v[92:95]\n" \
X_"a[230:231], v33 offset:4608\n" \
C_"v227, v182, v183\n" \
M_"v[92:95], a[248:251], a[200:203], v[92:95]\n" \
"v_accvgpr_read_b32 v220, a112\n" \
"v_accvgpr_read_b32 v221, a113\n" \
M_"v[96:99], a[188:191], a[136:139], v[96:99]\n" \
R_"a[136:139], v28 offset:36864\n" \
"v_accvgpr_read_b32 v222, a114\n" \
M_"v[96:99], a[252:255], a[200:203], v[96:99]\n" \
R_"a[200:203], v29 offset:36864\n" \
"v_accvgpr_read_b32 v223, a115\n" \
M_"v[112:115], a[188:191], a[140:143], v[112:115]\n" \
"s_addk_i32 s84, 0x7c00\n" \
M_"v[112:115], a[252:255], a[204:207], v[112:115]\n" \
B_"v26, s[68:71], s84 offen offset:1024 lds\n" \
M_"v[108:111], a[184:187], a[140:143], v[108:111]\n" \
"s_addk_i32 s84, 0x7c00\n" \
M_"v[108:111], a[248:251], a[204:207], v[108:111]\n" \
B_"v27, s[68:71], s84 offen offset:2048 lds\n" \
M_"v[104:107], a[180:183], a[140:143], v[104:107]\n" \
X_"a[172:173], v35 offset:0\n" \
"v_accvgpr_read_b32 v180, a116\n" \
M_"v[104:107], a[244:247], a[204:207], v[104:107]\n" \
X_"a[174:175], v35 offset:512\n" \
"v_accvgpr_read_b32 v181, a117\n" \
M_"v[100:103], a[176:179], a[140:143], v[100:103]\n" \
R_"a[140:143], v28 offset:38912\n" \
"v_accvgpr_read_b32 v182, a118\n" \
M_"v[100:103], a[240:243], a[204:207], v[100:103]\n" \
R_"a[204:207], v29 offset:38912\n" \
"v_accvgpr_read_b32 v183, a119\n" \
M_"v[116:119], a[176:179], a[144:147], v[116:119]\n" \
"s_addk_i32 s84, 0x7c00\n" \
M_"v[116:119], a[240:243], a[208:211], v[116:119]\n" \
B_"v27, s[68:71], s84 offen offset:3072 lds\n" \
M_"v[120:123], a[180:183], a[144:147], v[120:123]\n" \
"s_add_u32 m0, s30, 0x0\n" \
"s_movk_i32 s84, 0x300\n" \
"s_nop 0\n" \
M_"v[120:123], a[244:247], a[208:211], v[120:123]\n" \
B_"v24, s[76:79], s84 offen offset:0 lds\n" \
M_"v[124:127], a[184:187], a[144:147], v[124:127]\n" \
X_"a[236:237], v35 offset:4096\n" \
C_"v220, v220, v221\n" \
M_"v[124:127], a[248:251], a[208:211], v[124:127]\n" \
X_"a[238:239], v35 offset:4608\n" \
C_"v221, v222, v223\n" \
M_"v[128:131], a[188:191], a[144:147], v[128:131]\n" \
R_"a[144:147], v28 offset:40960\n" \
C_"v222, v180, v181\n" \
M_"v[128:131], a[252:255], a[208:211], v[128:131]\n" \
R_"a[208:211], v29 offset:40960\n" \
C_"v223, v182, v183\n" \
M_"v[144:147], a[188:191], a[148:151], v[144:147]\n" \
"s_add_u32 s89, s42, 0x1000\n" \
"buffer_store_dwordx4 a[84:87], v20, s[80:83], s89 offen offset:3072 sc1\n" \
M_"v[144:147], a[252:255], a[212:215], v[144:147]\n" \
X_"a[168:169], v34 offset:0\n" \
"v_accvgpr_read_b32 v216, a104\n" \
M_"v[140:143], a[184:187], a[148:151], v[140:143]\n" \
X_"a[170:171], v34 offset:512\n" \
"v_accvgpr_read_b32 v217, a105\n" \
M_"v[140:143], a[248:251], a[212:215], v[140:143]\n" \
"s_movk_i32 s84, 0x2300\n" \
"s_nop 0\n" \
M_"v[136:139], a[180:183], a[148:151], v[136:139]\n" \
B_"v25, s[76:79], s84 offen offset:1024 lds\n" \
M_"v[136:139], a[244:247], a[212:215], v[136:139]\n" \
X_"a[232:233], v34 offset:4096\n" \
"v_accvgpr_read_b32 v218, a106\n" \
M_"v[132:135], a[176:179], a[148:151], v[132:135]\n" \
R_"a[148:151], v28 offset:43008\n" \
"v_accvgpr_read_b32 v219, a107\n" \
M_"v[132:135], a[240:243], a[212:215], v[132:135]\n" \
R_"a[212:215], v29 offset:43008\n" \
"v_accvgpr_read_b32 v180, a108\n" \
M_"v[148:151], a[176:179], a[152:155], v[148:151]\n" \
"s_movk_i32 s84, 0x4300\n" \
M_"v[148:151], a[240:243], a[216:219], v[148:151]\n" \
B_"v24, s[76:79], s84 offen offset:2048 lds\n" \
M_"v[152:155], a[180:183], a[152:155], v[152:155]\n" \
X_"a[234:235], v34 offset:4608\n" \
"v_accvgpr_read_b32 v181, a109\n" \
M_"v[152:155], a[244:247], a[216:219], v[152:155]\n" \
"s_movk_i32 s84, 0x6300\n" \
M_"v[156:159], a[184:187], a[152:155], v[156:159]\n" \
B_"v25, s[76:79], s84 offen offset:3072 lds\n" \
M_"v[156:159], a[248:251], a[216:219], v[156:159]\n" \
R_"v[248:251], v22 offset:0\n" \
"v_accvgpr_read_b32 v182, a110\n" \
M_"v[160:163], a[188:191], a[152:155], v[160:163]\n" \
R_"a[152:155], v28 offset:45056\n" \
"v_accvgpr_read_b32 v183, a111\n" \
M_"v[160:163], a[252:255], a[216:219], v[160:163]\n" \
R_"a[216:219], v29 offset:45056\n" \
C_"v216, v216, v217\n" \
M_"v[176:179], a[188:191], a[156:159], v[176:179]\n" \
R_"v[48:51], v23 offset:0\n" \
C_"v217, v218, v219\n" \
M_"v[176:179], a[252:255], a[220:223], v[176:179]\n" \
C_"v218, v180, v181\n" \
C_"v219, v182, v183\n" \
M_"v[172:175], a[184:187], a[156:159], v[172:175]\n" \
"v_accvgpr_read_b32 v192, a96\n" \
"v_accvgpr_read_b32 v193, a97\n" \
M_"v[172:175], a[248:251], a[220:223], v[172:175]\n" \
"v_accvgpr_read_b32 v194, a98\n" \
"v_accvgpr_read_b32 v195, a99\n" \
M_"v[168:171], a[180:183], a[156:159], v[168:171]\n" \
"v_accvgpr_read_b32 v180, a100\n" \
"v_accvgpr_read_b32 v181, a101\n" \
M_"v[168:171], a[244:247], a[220:223], v[168:171]\n" \
"v_accvgpr_read_b32 v182, a102\n" \
"v_accvgpr_read_b32 v183, a103\n" \
M_"v[164:167], a[176:179], a[156:159], v[164:167]\n" \
R_"a[156:159], v28 offset:47104\n" \
C_"v192, v192, v193\n" \
M_"v[164:167], a[240:243], a[220:223], v[164:167]\n" \
R_"a[220:223], v29 offset:47104\n" \
"s_waitcnt lgkmcnt(15)\n" \
M_"a[0:3], a[160:163], a[128:131], a[0:3]\n" \
"s_add_u32 m0, s30, 0x1000\n" \
"s_addk_i32 s84, 0x3000\n" \
M_"a[0:3], a[224:227], a[192:195], a[0:3]\n" \
B_"v24, s[76:79], s84 offen offset:0 lds\n" \
M_"a[4:7], a[164:167], a[128:131], a[4:7]\n" \
X_"a[176:177], v36 offset:0\n" \
C_"v193, v194, v195\n" \
M_"a[4:7], a[228:231], a[192:195], a[4:7]\n" \
X_"a[178:179], v36 offset:1024\n" \
C_"v194, v180, v181\n" \
M_"a[12:15], a[172:175], a[128:131], a[12:15]\n" \
X_"a[240:241], v36 offset:8192\n" \
C_"v195, v182, v183\n" \
"s_waitcnt lgkmcnt(15)\n" \
M_"a[12:15], a[236:239], a[192:195], a[12:15]\n" \
"s_add_u32 s88, s42, 0x1000\n" \
"buffer_store_dwordx4 v[252:255], v20, s[80:83], s88 offen offset:2048 sc1\n" \
"s_waitcnt lgkmcnt(13)\n" \
M_"a[8:11], a[128:131], a[168:171], a[8:11]\n" \
X_"a[242:243], v36 offset:9216\n" \
"v_accvgpr_read_b32 v188, a88\n" \
"s_waitcnt lgkmcnt(10)\n" \
"s_nop 0\n" \
M_"a[8:11], a[192:195], a[232:235], a[8:11]\n" \
X_"a[180:181], v37 offset:0\n" \
"v_accvgpr_read_b32 v189, a89\n" \
M_"a[24:27], a[132:135], a[168:171], a[24:27]\n" \
X_"a[182:183], v37 offset:1024\n" \
"v_accvgpr_read_b32 v190, a90\n" \
M_"a[24:27], a[196:199], a[232:235], a[24:27]\n" \
X_"a[244:245], v37 offset:8192\n" \
"v_accvgpr_read_b32 v191, a91\n" \
M_"a[28:31], a[172:175], a[132:135], a[28:31]\n" \
"v_accvgpr_read_b32 v180, a92\n" \
"v_accvgpr_read_b32 v181, a93\n" \
M_"a[28:31], a[236:239], a[196:199], a[28:31]\n" \
"s_addk_i32 s84, 0x2000\n" \
"s_nop 0\n" \
M_"a[20:23], a[164:167], a[132:135], a[20:23]\n" \
B_"v25, s[76:79], s84 offen offset:1024 lds\n" \
M_"a[20:23], a[228:231], a[196:199], a[20:23]\n" \
X_"a[246:247], v37 offset:9216\n" \
"v_accvgpr_read_b32 v182, a94\n" \
M_"a[16:19], a[160:163], a[132:135], a[16:19]\n" \
X_"a[184:185], v38 offset:0\n" \
"v_accvgpr_read_b32 v183, a95\n" \
M_"a[16:19], a[224:227], a[196:199], a[16:19]\n" \
X_"a[186:187], v38 offset:1024\n" \
C_"v188, v188, v189\n" \
M_"a[32:35], a[160:163], a[136:139], a[32:35]\n" \
"s_addk_i32 s84, 0x2000\n" \
"s_nop 0\n" \
M_"a[32:35], a[224:227], a[200:203], a[32:35]\n" \
B_"v24, s[76:79], s84 offen offset:2048 lds\n" \
M_"a[36:39], a[164:167], a[136:139], a[36:39]\n" \
X_"a[248:249], v38 offset:8192\n" \
C_"v189, v190, v191\n" \
M_"a[36:39], a[228:231], a[200:203], a[36:39]\n" \
X_"a[250:251], v38 offset:9216\n" \
C_"v190, v180, v181\n" \
M_"a[44:47], a[172:175], a[136:139], a[44:47]\n" \
X_"a[188:189], v39 offset:0\n" \
C_"v191, v182, v183\n" \
M_"a[44:47], a[236:239], a[200:203], a[44:47]\n" \
X_"a[190:191], v39 offset:1024\n" \
"v_accvgpr_read_b32 v184, a72\n" \
".fill (L_pb + 28384 - .) / 4, 4, 0xBF800000\n" \
"L_pp7:\n" \
"s_cbranch_execz L_pret\n" \
"s_nop 0\n" \
M_"a[40:43], a[136:139], a[168:171], a[40:43]\n" \
"s_add_u32 s89, s42, 0x2000\n" \
"buffer_store_dwordx4 v[200:203], v20, s[80:83], s89 offen offset:2048 sc1\n" \
M_"a[40:43], a[200:203], a[232:235], a[40:43]\n" \
X_"a[252:253], v39 offset:8192\n" \
"v_accvgpr_read_b32 v185, a73\n" \
M_"a[56:59], a[140:143], a[168:171], a[56:59]\n" \
X_"a[254:255], v39 offset:9216\n" \
"v_accvgpr_read_b32 v186, a74\n" \
M_"a[56:59], a[204:207], a[232:235], a[56:59]\n" \
"v_accvgpr_read_b32 v187, a75\n" \
"v_accvgpr_read_b32 v180, a76\n" \
M_"a[60:63], a[172:175], a[140:143], a[60:63]\n" \
"v_accvgpr_read_b32 v181, a77\n" \
"v_accvgpr_read_b32 v182, a78\n" \
M_"a[60:63], a[236:239], a[204:207], a[60:63]\n" \
"s_addk_i32 s84, 0x2000\n" \
"s_nop 0\n" \
M_"a[52:55], a[164:167], a[140:143], a[52:55]\n" \
B_"v25, s[76:79], s84 offen offset:3072 lds\n" \
M_"a[52:55], a[228:231], a[204:207], a[52:55]\n" \
"v_accvgpr_read_b32 v183, a79\n" \
C_"v184, v184, v185\n" \
M_"a[48:51], a[160:163], a[140:143], a[48:51]\n" \
C_"v185, v186, v187\n" \
C_"v186, v180, v181\n" \
M_"a[48:51], a[224:227], a[204:207], a[48:51]\n" \
C_"v187, v182, v183\n" \
"v_writelane_b32 v11, s19, 33\n" \
"s_waitcnt lgkmcnt(15)\n" \
M_"a[64:67], a[164:167], v[248:251], a[64:67]\n" \
"s_xor_b32 s12, s12, 0x2000\n" \
"s_lshl_b32 s14, s14, 12\n" \
M_"a[64:67], a[228:231], v[48:51], a[64:67]\n" \
"s_sub_i32 s13, s8, s13\n" \
"s_add_i32 s19, s18, s12\n" \
M_"a[80:83], a[172:175], v[248:251], a[80:83]\n" \
"v_writelane_b32 v11, s19, 34\n" \
"s_add_i32 s18, s18, s14\n" \
M_"a[80:83], a[236:239], v[48:51], a[80:83]\n" \
"s_mul_i32 s13, s13, 0x24800\n" \
"v_writelane_b32 v11, s18, 35\n" \
M_"a[68:71], v[248:251], a[168:171], a[68:71]\n" \
"s_add_i32 s18, s13, 0x4120800\n" \
"s_add_i32 s15, s18, s15\n" \
"s_waitcnt vmcnt(11) lgkmcnt(0)\n" \
M_"a[68:71], v[48:51], a[232:235], a[68:71]\n" \
"s_barrier\n" \
"v_mov_b32_e32 v45, s62\n" \
"s_mov_b64 exec, 1\n" \
"buffer_store_dword v45, off, s[80:83], s37 offset:0 sc1\n" \
"s_mov_b64 exec, -1\n" \
"v_xor_b32_e32 v28, 0x20000, v28\n" \
"v_xor_b32_e32 v29, 0x20000, v29\n" \
M_"v[52:55], a[176:179], a[128:131], v[52:55]\n" \
"s_add_u32 m0, s40, 0x0\n" \
"s_add_u32 s85, s39, 0xc000\n" \
M_"v[52:55], a[240:243], a[192:195], v[52:55]\n" \
B_"v20, s[80:83], s85 offen sc1 lds\n" \
M_"v[56:59], a[180:183], a[128:131], v[56:59]\n" \
X_"a[160:161], v32 offset:32768\n" \
"v_writelane_b32 v11, s15, 36\n" \
M_"v[56:59], a[244:247], a[192:195], v[56:59]\n" \
X_"a[162:163], v32 offset:33280\n" \
"s_add_i32 s12, s18, s12\n" \
M_"v[60:63], a[184:187], a[128:131], v[60:63]\n" \
"s_add_u32 m0, s40, 0x1000\n" \
"s_add_u32 s85, s39, 0xd000\n" \
M_"v[60:63], a[248:251], a[192:195], v[60:63]\n" \
B_"v20, s[80:83], s85 offen sc1 lds\n" \
M_"v[64:67], a[188:191], a[128:131], v[64:67]\n" \
R_"a[128:131], v28 offset:0\n" \
"v_writelane_b32 v11, s12, 37\n" \
M_"v[64:67], a[252:255], a[192:195], v[64:67]\n" \
R_"a[192:195], v29 offset:0\n" \
"s_add_i32 s18, s18, s14\n" \
M_"v[80:83], a[188:191], a[132:135], v[80:83]\n" \
X_"a[224:225], v32 offset:36864\n" \
"s_or_b32 s12, s17, 0x3000000\n" \
M_"v[80:83], a[252:255], a[196:199], v[80:83]\n" \
"s_add_u32 m0, s40, 0x3000\n" \
"s_movk_i32 s84, 0x300\n" \
M_"v[76:79], a[184:187], a[132:135], v[76:79]\n" \
B_"v21, s[76:79], s84 offen offset:0 lds\n" \
M_"v[76:79], a[248:251], a[196:199], v[76:79]\n" \
X_"a[226:227], v32 offset:37376\n" \
"s_lshl_b32 s14, s5, 11\n" \
M_"v[72:75], a[180:183], a[132:135], v[72:75]\n" \
X_"a[164:165], v33 offset:32768\n" \
"v_writelane_b32 v11, s18, 38\n" \
M_"v[72:75], a[244:247], a[196:199], v[72:75]\n" \
"s_add_i32 s15, s12, s14\n" \
"v_writelane_b32 v11, s15, 39\n" \
M_"v[68:71], a[176:179], a[132:135], v[68:71]\n" \
R_"a[132:135], v28 offset:2048\n" \
"s_add_i32 s15, s13, 0x411c000\n" \
M_"v[68:71], a[240:243], a[196:199], v[68:71]\n" \
R_"a[196:199], v29 offset:2048\n" \
"s_add_i32 s18, s15, s14\n" \
M_"v[84:87], a[176:179], a[136:139], v[84:87]\n" \
"s_add_u32 m0, s31, 0x0\n" \
"s_nop 0\n" \
"s_mov_b32 s84, 0x300000\n" \
M_"v[84:87], a[240:243], a[200:203], v[84:87]\n" \
B_"v26, s[68:71], s84 offen offset:0 lds\n" \
M_"v[88:91], a[180:183], a[136:139], v[88:91]\n" \
"s_add_u32 s88, s42, 0x2000\n" \
"buffer_store_dwordx4 v[204:207], v20, s[80:83], s88 offen offset:3072 sc1\n" \
M_"v[88:91], a[244:247], a[200:203], v[88:91]\n" \
X_"a[166:167], v33 offset:33280\n" \
"v_writelane_b32 v11, s18, 40\n" \
M_"v[92:95], a[184:187], a[136:139], v[92:95]\n" \
X_"a[228:229], v33 offset:36864\n" \
"s_add_i32 s17, s17, 0x3004000\n" \
M_"v[92:95], a[248:251], a[200:203], v[92:95]\n" \
X_"a[230:231], v33 offset:37376\n" \
"v_writelane_b32 v11, s17, 41\n" \
M_"v[96:99], a[188:191], a[136:139], v[96:99]\n" \
R_"a[136:139], v28 offset:4096\n" \
"s_add_i32 s13, s13, 0x4120000\n" \
M_"v[96:99], a[252:255], a[200:203], v[96:99]\n" \
R_"a[200:203], v29 offset:4096\n" \
"v_writelane_b32 v11, s13, 42\n" \
M_"v[112:115], a[188:191], a[140:143], v[112:115]\n" \
"s_addk_i32 s84, 0x7c00\n" \
M_"v[112:115], a[252:255], a[204:207], v[112:115]\n" \
B_"v26, s[68:71], s84 offen offset:1024 lds\n" \
M_"v[108:111], a[184:187], a[140:143], v[108:111]\n" \
X_"a[172:173], v35 offset:32768\n" \
"s_lshl_b32 s13, s9, 2\n" \
M_"v[108:111], a[248:251], a[204:207], v[108:111]\n" \
X_"a[174:175], v35 offset:33280\n" \
"s_or_b32 s7, s13, s7\n" \
M_"v[104:107], a[180:183], a[140:143], v[104:107]\n" \
"s_addk_i32 s84, 0x7c00\n" \
M_"v[104:107], a[244:247], a[204:207], v[104:107]\n" \
B_"v27, s[68:71], s84 offen offset:2048 lds\n" \
M_"v[100:103], a[176:179], a[140:143], v[100:103]\n" \
R_"a[140:143], v28 offset:6144\n" \
"s_mul_i32 s7, s7, 0x90000\n" \
M_"v[100:103], a[240:243], a[204:207], v[100:103]\n" \
R_"a[204:207], v29 offset:6144\n" \
"s_lshl_b32 s13, s3, 14\n" \
M_"v[116:119], a[176:179], a[144:147], v[116:119]\n" \
"s_addk_i32 s84, 0x7c00\n" \
M_"v[116:119], a[240:243], a[208:211], v[116:119]\n" \
B_"v27, s[68:71], s84 offen offset:3072 lds\n" \
M_"v[120:123], a[180:183], a[144:147], v[120:123]\n" \
X_"a[236:237], v35 offset:36864\n" \
"s_lshl_b32 s9, s9, 7\n" \
M_"v[120:123], a[244:247], a[208:211], v[120:123]\n" \
X_"a[238:239], v35 offset:37376\n" \
"s_lshl_b32 s8, s8, 4\n" \
M_"v[124:127], a[184:187], a[144:147], v[124:127]\n" \
X_"a[168:169], v34 offset:32768\n" \
"s_add_i32 s7, s7, s13\n" \
M_"v[124:127], a[248:251], a[208:211], v[124:127]\n" \
X_"a[170:171], v34 offset:33280\n" \
"s_or_b32 s13, s8, s9\n" \
M_"v[128:131], a[188:191], a[144:147], v[128:131]\n" \
R_"a[144:147], v28 offset:8192\n" \
"s_sub_i32 s8, s8, s9\n" \
M_"v[128:131], a[252:255], a[208:211], v[128:131]\n" \
R_"a[208:211], v29 offset:8192\n" \
"s_mulk_i32 s13, 0x480\n" \
M_"v[144:147], a[188:191], a[148:151], v[144:147]\n" \
"s_add_u32 m0, s30, 0x8000\n" \
"s_movk_i32 s84, 0x380\n" \
M_"v[144:147], a[252:255], a[212:215], v[144:147]\n" \
B_"v24, s[76:79], s84 offen offset:0 lds\n" \
M_"v[140:143], a[184:187], a[148:151], v[140:143]\n" \
"s_movk_i32 s84, 0x2380\n" \
M_"v[140:143], a[248:251], a[212:215], v[140:143]\n" \
B_"v25, s[76:79], s84 offen offset:1024 lds\n" \
M_"v[136:139], a[180:183], a[148:151], v[136:139]\n" \
X_"a[232:233], v34 offset:36864\n" \
"s_mulk_i32 s8, 0x480\n" \
M_"v[136:139], a[244:247], a[212:215], v[136:139]\n" \
X_"a[234:235], v34 offset:37376\n" \
"s_or_b32 s13, s13, s11\n" \
M_"v[132:135], a[176:179], a[148:151], v[132:135]\n" \
R_"a[148:151], v28 offset:10240\n" \
"s_or_b32 s8, s8, s11\n" \
M_"v[132:135], a[240:243], a[212:215], v[132:135]\n" \
R_"a[212:215], v29 offset:10240\n" \
"s_and_b32 s11, s2, 1\n" \
M_"v[148:151], a[176:179], a[152:155], v[148:151]\n" \
"s_movk_i32 s84, 0x4380\n" \
M_"v[148:151], a[240:243], a[216:219], v[148:151]\n" \
B_"v24, s[76:79], s84 offen offset:2048 lds\n" \
M_"v[152:155], a[180:183], a[152:155], v[152:155]\n" \
R_"v[248:251], v22 offset:32768\n" \
M_"v[152:155], a[244:247], a[216:219], v[152:155]\n" \
"s_movk_i32 s84, 0x6380\n" \
M_"v[156:159], a[184:187], a[152:155], v[156:159]\n" \
B_"v25, s[76:79], s84 offen offset:3072 lds\n" \
M_"v[156:159], a[248:251], a[216:219], v[156:159]\n" \
R_"v[48:51], v23 offset:32768\n" \
M_"v[160:163], a[188:191], a[152:155], v[160:163]\n" \
R_"a[152:155], v28 offset:12288\n" \
M_"v[160:163], a[252:255], a[216:219], v[160:163]\n" \
R_"a[216:219], v29 offset:12288\n" \
M_"v[176:179], a[188:191], a[156:159], v[176:179]\n" \
"s_cmp_lg_u32 s11, 0\n" \
"s_cselect_b32 s12, s15, s12\n" \
M_"v[176:179], a[252:255], a[220:223], v[176:179]\n" \
"s_bfe_u32 s9, s2, 0x20001\n" \
"s_add_i32 s15, s7, 0x4240000\n" \
M_"v[172:175], a[184:187], a[156:159], v[172:175]\n" \
"s_addk_i32 s12, 0x4000\n" \
"s_lshl_b32 s5, s5, 16\n" \
M_"v[172:175], a[248:251], a[220:223], v[172:175]\n" \
"s_xor_b32 s9, s9, s3\n" \
M_"v[168:171], a[180:183], a[156:159], v[168:171]\n" \
"s_cmp_lg_u32 s9, 0\n" \
"s_cselect_b32 s12, s6, s12\n" \
M_"v[168:171], a[244:247], a[220:223], v[168:171]\n" \
"s_add_i32 s5, s15, s5\n" \
"v_writelane_b32 v11, s12, 43\n" \
M_"v[164:167], a[176:179], a[156:159], v[164:167]\n" \
R_"a[156:159], v28 offset:14336\n" \
"s_add_i32 s7, s7, 0x42c0000\n" \
M_"v[164:167], a[240:243], a[220:223], v[164:167]\n" \
R_"a[220:223], v29 offset:14336\n" \
"s_waitcnt lgkmcnt(15)\n" \
M_"a[0:3], a[160:163], a[128:131], a[0:3]\n" \
"s_add_u32 m0, s30, 0x9000\n" \
"s_addk_i32 s84, 0x3000\n" \
M_"a[0:3], a[224:227], a[192:195], a[0:3]\n" \
B_"v24, s[76:79], s84 offen offset:0 lds\n" \
M_"a[4:7], a[164:167], a[128:131], a[4:7]\n" \
X_"a[176:177], v36 offset:32768\n" \
"v_writelane_b32 v11, s5, 44\n" \
M_"a[4:7], a[228:231], a[192:195], a[4:7]\n" \
X_"a[178:179], v36 offset:33792\n" \
"s_lshl_b32 s5, s11, 10\n" \
M_"a[12:15], a[172:175], a[128:131], a[12:15]\n" \
X_"a[240:241], v36 offset:40960\n" \
"s_or_b32 s5, s7, s5\n" \
"s_waitcnt lgkmcnt(15)\n" \
M_"a[12:15], a[236:239], a[192:195], a[12:15]\n" \
"s_add_u32 s89, s42, 0x3000\n" \
"buffer_store_dwordx4 v[208:211], v20, s[80:83], s89 offen offset:3072 sc1\n" \
M_"a[8:11], a[128:131], a[168:171], a[8:11]\n" \
X_"a[242:243], v36 offset:41984\n" \
"s_waitcnt lgkmcnt(12)\n" \
M_"a[8:11], a[192:195], a[232:235], a[8:11]\n" \
X_"a[180:181], v37 offset:32768\n" \
M_"a[24:27], a[132:135], a[168:171], a[24:27]\n" \
X_"a[182:183], v37 offset:33792\n" \
M_"a[24:27], a[196:199], a[232:235], a[24:27]\n" \
X_"a[244:245], v37 offset:40960\n" \
M_"a[28:31], a[172:175], a[132:135], a[28:31]\n" \
"s_cmp_lg_u32 s9, 0\n" \
"s_cselect_b32 s5, s6, s5\n" \
M_"a[28:31], a[236:239], a[196:199], a[28:31]\n" \
"s_addk_i32 s84, 0x2000\n" \
M_"a[20:23], a[164:167], a[132:135], a[20:23]\n" \
B_"v25, s[76:79], s84 offen offset:1024 lds\n" \
M_"a[20:23], a[228:231], a[196:199], a[20:23]\n" \
X_"a[246:247], v37 offset:41984\n" \
"s_add_i32 s8, s8, 0x21c000\n" \
M_"a[16:19], a[160:163], a[132:135], a[16:19]\n" \
X_"a[184:185], v38 offset:32768\n" \
"v_writelane_b32 v11, s5, 45\n" \
M_"a[16:19], a[224:227], a[196:199], a[16:19]\n" \
X_"a[186:187], v38 offset:33792\n" \
"s_add_i32 s5, s13, s10\n" \
M_"a[32:35], a[160:163], a[136:139], a[32:35]\n" \
"s_addk_i32 s84, 0x2000\n" \
M_"a[32:35], a[224:227], a[200:203], a[32:35]\n" \
B_"v24, s[76:79], s84 offen offset:2048 lds\n" \
M_"a[36:39], a[164:167], a[136:139], a[36:39]\n" \
X_"a[248:249], v38 offset:40960\n" \
"v_writelane_b32 v11, s5, 46\n" \
M_"a[36:39], a[228:231], a[200:203], a[36:39]\n" \
X_"a[250:251], v38 offset:41984\n" \
"s_add_i32 s5, s8, s10\n" \
M_"a[44:47], a[172:175], a[136:139], a[44:47]\n" \
X_"a[188:189], v39 offset:32768\n" \
"v_writelane_b32 v11, s5, 47\n" \
M_"a[44:47], a[236:239], a[200:203], a[44:47]\n" \
X_"a[190:191], v39 offset:33792\n" \
M_"a[40:43], a[136:139], a[168:171], a[40:43]\n" \
"s_add_u32 s88, s42, 0x3000\n" \
"buffer_store_dwordx4 v[212:215], v20, s[80:83], s88 offen offset:2048 sc1\n" \
M_"a[40:43], a[200:203], a[232:235], a[40:43]\n" \
X_"a[252:253], v39 offset:40960\n" \
M_"a[56:59], a[140:143], a[168:171], a[56:59]\n" \
X_"a[254:255], v39 offset:41984\n" \
M_"a[56:59], a[204:207], a[232:235], a[56:59]\n" \
"s_cmp_lg_u32 s11, 0\n" \
"s_cselect_b32 s5, s8, s13\n" \
M_"a[60:63], a[172:175], a[140:143], a[60:63]\n" \
"s_addk_i32 s5, 0x100\n" \
M_"a[60:63], a[236:239], a[204:207], a[60:63]\n" \
"s_addk_i32 s84, 0x2000\n" \
M_"a[52:55], a[164:167], a[140:143], a[52:55]\n" \
B_"v25, s[76:79], s84 offen offset:3072 lds\n" \
M_"a[52:55], a[228:231], a[204:207], a[52:55]\n" \
"s_cmp_lg_u32 s9, 0\n" \
"s_cselect_b32 s5, s6, s5\n" \
M_"a[48:51], a[160:163], a[140:143], a[48:51]\n" \
"v_writelane_b32 v11, s5, 48\n" \
"s_or_b32 s5, s7, s14\n" \
M_"a[48:51], a[224:227], a[204:207], a[48:51]\n" \
"v_writelane_b32 v11, s5, 56\n" \
"s_lshl_b32 s5, s3, 17\n" \
"s_waitcnt lgkmcnt(15)\n" \
M_"a[64:67], a[164:167], v[248:251], a[64:67]\n" \
"s_add_i32 s5, s15, s5\n" \
"s_or_b32 s4, s5, s4\n" \
M_"a[64:67], a[228:231], v[48:51], a[64:67]\n" \
"s_or_b32 s4, s4, 0x3840\n" \
"v_writelane_b32 v11, s4, 6\n" \
M_"a[80:83], a[172:175], v[248:251], a[80:83]\n" \
"s_mov_b32 s4, 0xff0000\n" \
M_"a[80:83], a[236:239], v[48:51], a[80:83]\n" \
"s_cmp_lg_u32 s9, 0\n" \
"s_cselect_b32 s4, s16, s4\n" \
M_"a[68:71], v[248:251], a[168:171], a[68:71]\n" \
"v_writelane_b32 v11, s4, 7\n" \
"s_lshl_b32 s4, s2, 16\n" \
"s_waitcnt vmcnt(10) lgkmcnt(0)\n" \
M_"a[68:71], v[48:51], a[232:235], a[68:71]\n" \
"s_barrier\n" \
"v_xor_b32_e32 v28, 0x20000, v28\n" \
"v_xor_b32_e32 v29, 0x20000, v29\n" \
M_"v[52:55], a[176:179], a[128:131], v[52:55]\n" \
"s_add_u32 m0, s40, 0x8000\n" \
"s_add_u32 s85, s39, 0xe000\n" \
M_"v[52:55], a[240:243], a[192:195], v[52:55]\n" \
B_"v20, s[80:83], s85 offen sc1 lds\n" \
M_"v[56:59], a[180:183], a[128:131], v[56:59]\n" \
X_"a[160:161], v32 offset:0\n" \
"s_add_i32 s5, s4, 0x10000\n" \
M_"v[56:59], a[244:247], a[192:195], v[56:59]\n" \
X_"a[162:163], v32 offset:512\n" \
"s_and_b32 s5, s5, 0x70000\n" \
M_"v[60:63], a[184:187], a[128:131], v[60:63]\n" \
"s_add_u32 m0, s40, 0x9000\n" \
"s_add_u32 s85, s39, 0xf000\n" \
M_"v[60:63], a[248:251], a[192:195], v[60:63]\n" \
B_"v20, s[80:83], s85 offen sc1 lds\n" \
M_"v[64:67], a[188:191], a[128:131], v[64:67]\n" \
R_"a[128:131], v28 offset:0\n" \
"s_add_i32 s5, s15, s5\n" \
M_"v[64:67], a[252:255], a[192:195], v[64:67]\n" \
R_"a[192:195], v29 offset:0\n" \
"v_writelane_b32 v11, s5, 49\n" \
M_"v[80:83], a[188:191], a[132:135], v[80:83]\n" \
X_"a[224:225], v32 offset:4096\n" \
"s_add_i32 s5, s4, 0x20000\n" \
M_"v[80:83], a[252:255], a[196:199], v[80:83]\n" \
"s_add_u32 m0, s40, 0xb000\n" \
"s_movk_i32 s84, 0x380\n" \
M_"v[76:79], a[184:187], a[132:135], v[76:79]\n" \
B_"v21, s[76:79], s84 offen offset:0 lds\n" \
M_"v[76:79], a[248:251], a[196:199], v[76:79]\n" \
X_"a[226:227], v32 offset:4608\n" \
"s_and_b32 s5, s5, 0x70000\n" \
M_"v[72:75], a[180:183], a[132:135], v[72:75]\n" \
X_"a[164:165], v33 offset:0\n" \
"s_add_i32 s5, s15, s5\n" \
M_"v[72:75], a[244:247], a[196:199], v[72:75]\n" \
"s_bitset1_b32 s5, 11\n" \
"v_writelane_b32 v11, s5, 50\n" \
M_"v[68:71], a[176:179], a[132:135], v[68:71]\n" \
R_"a[132:135], v28 offset:2048\n" \
"s_add_i32 s5, s4, 0x30000\n" \
M_"v[68:71], a[240:243], a[196:199], v[68:71]\n" \
R_"a[196:199], v29 offset:2048\n" \
"s_and_b32 s5, s5, 0x70000\n" \
M_"v[84:87], a[176:179], a[136:139], v[84:87]\n" \
"s_add_u32 m0, s31, 0x8000\n" \
"s_mov_b32 s84, 0x380000\n" \
M_"v[84:87], a[240:243], a[200:203], v[84:87]\n" \
B_"v26, s[68:71], s84 offen offset:0 lds\n" \
M_"v[88:91], a[180:183], a[136:139], v[88:91]\n" \
"s_add_u32 s89, s42, 0x0\n" \
"buffer_store_dwordx4 v[184:187], v20, s[80:83], s89 offen offset:1024 sc1\n" \
M_"v[88:91], a[244:247], a[200:203], v[88:91]\n" \
X_"a[166:167], v33 offset:512\n" \
"s_add_i32 s5, s15, s5\n" \
M_"v[92:95], a[184:187], a[136:139], v[92:95]\n" \
X_"a[228:229], v33 offset:4096\n" \
"s_bitset1_b32 s5, 12\n" \
M_"v[92:95], a[248:251], a[200:203], v[92:95]\n" \
X_"a[230:231], v33 offset:4608\n" \
"v_writelane_b32 v11, s5, 51\n" \
M_"v[96:99], a[188:191], a[136:139], v[96:99]\n" \
R_"a[136:139], v28 offset:4096\n" \
"s_and_b32 s5, s4, 0x70000\n" \
M_"v[96:99], a[252:255], a[200:203], v[96:99]\n" \
R_"a[200:203], v29 offset:4096\n" \
"s_xor_b32 s5, s5, 0x40000\n" \
M_"v[112:115], a[188:191], a[140:143], v[112:115]\n" \
"s_addk_i32 s84, 0x7c00\n" \
M_"v[112:115], a[252:255], a[204:207], v[112:115]\n" \
B_"v26, s[68:71], s84 offen offset:1024 lds\n" \
M_"v[108:111], a[184:187], a[140:143], v[108:111]\n" \
X_"a[172:173], v35 offset:0\n" \
"s_add_i32 s5, s15, s5\n" \
M_"v[108:111], a[248:251], a[204:207], v[108:111]\n" \
X_"a[174:175], v35 offset:512\n" \
"s_or_b32 s5, s5, 0x1800\n" \
M_"v[104:107], a[180:183], a[140:143], v[104:107]\n" \
"s_addk_i32 s84, 0x7c00\n" \
M_"v[104:107], a[244:247], a[204:207], v[104:107]\n" \
B_"v27, s[68:71], s84 offen offset:2048 lds\n" \
M_"v[100:103], a[176:179], a[140:143], v[100:103]\n" \
R_"a[140:143], v28 offset:6144\n" \
"v_writelane_b32 v11, s5, 52\n" \
M_"v[100:103], a[240:243], a[204:207], v[100:103]\n" \
R_"a[204:207], v29 offset:6144\n" \
"s_add_i32 s5, s4, 0x50000\n" \
M_"v[116:119], a[176:179], a[144:147], v[116:119]\n" \
"s_addk_i32 s84, 0x7c00\n" \
M_"v[116:119], a[240:243], a[208:211], v[116:119]\n" \
B_"v27, s[68:71], s84 offen offset:3072 lds\n" \
M_"v[120:123], a[180:183], a[144:147], v[120:123]\n" \
X_"a[236:237], v35 offset:4096\n" \
"s_and_b32 s5, s5, 0x70000\n" \
M_"v[120:123], a[244:247], a[208:211], v[120:123]\n" \
X_"a[238:239], v35 offset:4608\n" \
"s_add_i32 s5, s15, s5\n" \
M_"v[124:127], a[184:187], a[144:147], v[124:127]\n" \
X_"a[168:169], v34 offset:0\n" \
"s_bitset1_b32 s5, 13\n" \
"s_nop 0\n" \
M_"v[124:127], a[248:251], a[208:211], v[124:127]\n" \
X_"a[170:171], v34 offset:512\n" \
"v_writelane_b32 v11, s5, 53\n" \
M_"v[128:131], a[188:191], a[144:147], v[128:131]\n" \
R_"a[144:147], v28 offset:8192\n" \
"s_add_i32 s5, s4, 0x60000\n" \
M_"v[128:131], a[252:255], a[208:211], v[128:131]\n" \
R_"a[208:211], v29 offset:8192\n" \
"s_and_b32 s5, s5, 0x70000\n" \
M_"v[144:147], a[188:191], a[148:151], v[144:147]\n" \
"s_add_u32 m0, s30, 0x20000\n" \
"s_movk_i32 s84, 0x400\n" \
M_"v[144:147], a[252:255], a[212:215], v[144:147]\n" \
B_"v24, s[76:79], s84 offen offset:0 lds\n" \
M_"v[140:143], a[184:187], a[148:151], v[140:143]\n" \
"s_movk_i32 s84, 0x2400\n" \
M_"v[140:143], a[248:251], a[212:215], v[140:143]\n" \
B_"v25, s[76:79], s84 offen offset:1024 lds\n" \
M_"v[136:139], a[180:183], a[148:151], v[136:139]\n" \
X_"a[232:233], v34 offset:4096\n" \
"s_add_i32 s4, s4, 0x70000\n" \
M_"v[136:139], a[244:247], a[212:215], v[136:139]\n" \
X_"a[234:235], v34 offset:4608\n" \
"s_add_i32 s5, s15, s5\n" \
M_"v[132:135], a[176:179], a[148:151], v[132:135]\n" \
R_"a[148:151], v28 offset:10240\n" \
"s_and_b32 s4, s4, 0x70000\n" \
M_"v[132:135], a[240:243], a[212:215], v[132:135]\n" \
R_"a[212:215], v29 offset:10240\n" \
"s_or_b32 s5, s5, 0x2800\n" \
M_"v[148:151], a[176:179], a[152:155], v[148:151]\n" \
"s_movk_i32 s84, 0x4400\n" \
M_"v[148:151], a[240:243], a[216:219], v[148:151]\n" \
B_"v24, s[76:79], s84 offen offset:2048 lds\n" \
M_"v[152:155], a[180:183], a[152:155], v[152:155]\n" \
R_"v[248:251], v22 offset:0\n" \
"v_writelane_b32 v11, s5, 54\n" \
M_"v[152:155], a[244:247], a[216:219], v[152:155]\n" \
"s_movk_i32 s84, 0x6400\n" \
M_"v[156:159], a[184:187], a[152:155], v[156:159]\n" \
B_"v25, s[76:79], s84 offen offset:3072 lds\n" \
M_"v[156:159], a[248:251], a[216:219], v[156:159]\n" \
R_"v[48:51], v23 offset:0\n" \
"s_add_i32 s15, s15, s4\n" \
M_"v[160:163], a[188:191], a[152:155], v[160:163]\n" \
R_"a[152:155], v28 offset:12288\n" \
"s_or_b32 s4, s15, 0x3000\n" \
M_"v[160:163], a[252:255], a[216:219], v[160:163]\n" \
R_"a[216:219], v29 offset:12288\n" \
"v_writelane_b32 v11, s4, 55\n" \
M_"v[176:179], a[188:191], a[156:159], v[176:179]\n" \
"s_movk_i32 s11, 0x4000\n" \
"s_lshl_b32 s4, s2, 2\n" \
M_"v[176:179], a[252:255], a[220:223], v[176:179]\n" \
"s_and_b32 s4, s4, 28\n" \
"s_bfe_u32 s5, s2, 0x20003\n" \
M_"v[172:175], a[184:187], a[156:159], v[172:175]\n" \
"s_or_b32 s4, s4, s5\n" \
"s_lshr_b32 s5, s2, 5\n" \
M_"v[172:175], a[248:251], a[220:223], v[172:175]\n" \
"s_lshr_b32 s19, s3, 1\n" \
"s_lshl_b32 s18, s5, 1\n" \
M_"v[168:171], a[180:183], a[156:159], v[168:171]\n" \
"s_lshl_b32 s6, s4, 20\n" \
"s_lshl_b32 s7, s4, 8\n" \
M_"v[168:171], a[244:247], a[220:223], v[168:171]\n" \
"s_and_b32 s13, s3, 1\n" \
"s_add_i32 s14, s18, s19\n" \
M_"v[164:167], a[176:179], a[156:159], v[164:167]\n" \
R_"a[156:159], v28 offset:14336\n" \
M_"v[164:167], a[240:243], a[220:223], v[164:167]\n" \
R_"a[220:223], v29 offset:14336\n" \
"s_waitcnt lgkmcnt(15)\n" \
M_"a[0:3], a[160:163], a[128:131], a[0:3]\n" \
"s_add_u32 m0, s30, 0x21000\n" \
"s_addk_i32 s84, 0x3000\n" \
M_"a[0:3], a[224:227], a[192:195], a[0:3]\n" \
B_"v24, s[76:79], s84 offen offset:0 lds\n" \
M_"a[4:7], a[164:167], a[128:131], a[4:7]\n" \
X_"a[176:177], v36 offset:0\n" \
M_"a[4:7], a[228:231], a[192:195], a[4:7]\n" \
X_"a[178:179], v36 offset:1024\n" \
M_"a[12:15], a[172:175], a[128:131], a[12:15]\n" \
X_"a[240:241], v36 offset:8192\n" \
"s_waitcnt lgkmcnt(15)\n" \
M_"a[12:15], a[236:239], a[192:195], a[12:15]\n" \
"s_add_u32 s88, s42, 0x1000\n" \
"buffer_store_dwordx4 v[188:191], v20, s[80:83], s88 offen offset:1024 sc1\n" \
M_"a[8:11], a[128:131], a[168:171], a[8:11]\n" \
X_"a[242:243], v36 offset:9216\n" \
"s_waitcnt lgkmcnt(12)\n" \
M_"a[8:11], a[192:195], a[232:235], a[8:11]\n" \
X_"a[180:181], v37 offset:0\n" \
M_"a[24:27], a[132:135], a[168:171], a[24:27]\n" \
X_"a[182:183], v37 offset:1024\n" \
M_"a[24:27], a[196:199], a[232:235], a[24:27]\n" \
X_"a[244:245], v37 offset:8192\n" \
M_"a[28:31], a[172:175], a[132:135], a[28:31]\n" \
"s_add_u32 s6, s26, s6\n" \
"s_addc_u32 s9, s27, 0\n" \
M_"a[28:31], a[236:239], a[196:199], a[28:31]\n" \
"s_addk_i32 s84, 0x2000\n" \
M_"a[20:23], a[164:167], a[132:135], a[20:23]\n" \
B_"v25, s[76:79], s84 offen offset:1024 lds\n" \
M_"a[20:23], a[228:231], a[196:199], a[20:23]\n" \
X_"a[246:247], v37 offset:9216\n" \
M_"a[16:19], a[160:163], a[132:135], a[16:19]\n" \
X_"a[184:185], v38 offset:0\n" \
M_"a[16:19], a[224:227], a[196:199], a[16:19]\n" \
X_"a[186:187], v38 offset:1024\n" \
M_"a[32:35], a[160:163], a[136:139], a[32:35]\n" \
"s_addk_i32 s84, 0x2000\n" \
"s_nop 0\n" \
M_"a[32:35], a[224:227], a[200:203], a[32:35]\n" \
B_"v24, s[76:79], s84 offen offset:2048 lds\n" \
M_"a[36:39], a[164:167], a[136:139], a[36:39]\n" \
X_"a[248:249], v38 offset:8192\n" \
M_"a[36:39], a[228:231], a[200:203], a[36:39]\n" \
X_"a[250:251], v38 offset:9216\n" \
M_"a[44:47], a[172:175], a[136:139], a[44:47]\n" \
X_"a[188:189], v39 offset:0\n" \
M_"a[44:47], a[236:239], a[200:203], a[44:47]\n" \
X_"a[190:191], v39 offset:1024\n" \
M_"a[40:43], a[136:139], a[168:171], a[40:43]\n" \
"s_add_u32 s89, s42, 0x2000\n" \
"buffer_store_dwordx4 v[192:195], v20, s[80:83], s89 offen offset:0 sc1\n" \
M_"a[40:43], a[200:203], a[232:235], a[40:43]\n" \
X_"a[252:253], v39 offset:8192\n" \
M_"a[56:59], a[140:143], a[168:171], a[56:59]\n" \
X_"a[254:255], v39 offset:9216\n" \
M_"a[56:59], a[204:207], a[232:235], a[56:59]\n" \
"s_add_u32 s8, s6, 0x1000000\n" \
"s_addc_u32 s6, s9, 0\n" \
M_"a[60:63], a[172:175], a[140:143], a[60:63]\n" \
"s_and_b32 s9, s6, 0xffff\n" \
"s_lshl_b32 s6, s5, 16\n" \
M_"a[60:63], a[236:239], a[204:207], a[60:63]\n" \
"s_addk_i32 s84, 0x2000\n" \
M_"a[52:55], a[164:167], a[140:143], a[52:55]\n" \
B_"v25, s[76:79], s84 offen offset:3072 lds\n" \
M_"a[52:55], a[228:231], a[204:207], a[52:55]\n" \
".fill (L_pb + 32480 - .) / 4, 4, 0xBF800000\n" \
"L_pp8:\n" \
"s_cbranch_execz L_pret\n" \
"s_sub_i32 s10, 0x100000, s6\n" \
"s_mov_b32 s6, 0x8000\n" \
M_"a[48:51], a[160:163], a[140:143], a[48:51]\n" \
"s_cmp_lg_u32 s5, 0\n" \
"s_cselect_b32 s17, s11, s6\n" \
M_"a[48:51], a[224:227], a[204:207], a[48:51]\n" \
"s_lshl_b32 s6, s3, 1\n" \
"s_lshl_b32 s5, s5, 3\n" \
"s_waitcnt lgkmcnt(15)\n" \
M_"a[64:67], a[164:167], v[248:251], a[64:67]\n" \
"s_and_b32 s6, s6, 4\n" \
"s_or_b32 s12, s5, s6\n" \
M_"a[64:67], a[228:231], v[48:51], a[64:67]\n" \
"s_sub_i32 s5, s13, s5\n" \
"s_or_b32 s5, s5, s6\n" \
M_"a[80:83], a[172:175], v[248:251], a[80:83]\n" \
"s_or_b32 s6, s12, s13\n" \
"s_lshl_b32 s4, s4, 2\n" \
M_"a[80:83], a[236:239], v[48:51], a[80:83]\n" \
"s_mulk_i32 s6, 0x92\n" \
"s_add_i32 s4, s4, 18\n" \
M_"a[68:71], v[248:251], a[168:171], a[68:71]\n" \
"s_mulk_i32 s5, 0x92\n" \
"s_add_i32 s6, s4, s6\n" \
"s_waitcnt vmcnt(10) lgkmcnt(0)\n" \
M_"a[68:71], v[48:51], a[232:235], a[68:71]\n" \
"s_barrier\n" \
M_"v[52:55], a[176:179], a[128:131], v[52:55]\n" \
"s_add_u32 m0, s40, 0x0\n" \
"s_add_u32 s85, s39, 0x10000\n" \
M_"v[52:55], a[240:243], a[192:195], v[52:55]\n" \
B_"v20, s[80:83], s85 offen sc1 lds\n" \
M_"v[56:59], a[180:183], a[128:131], v[56:59]\n" \
X_"a[160:161], v32 offset:32768\n" \
"s_add_i32 s5, s5, s4\n" \
M_"v[56:59], a[244:247], a[192:195], v[56:59]\n" \
X_"a[162:163], v32 offset:33280\n" \
"s_lshl_b32 s4, s3, 11\n" \
M_"v[60:63], a[184:187], a[128:131], v[60:63]\n" \
"s_lshl_b32 s15, s13, 12\n" \
"s_lshl_b32 s16, s19, 14\n" \
"s_nop 0\n" \
M_"v[60:63], a[248:251], a[192:195], v[60:63]\n" \
X_"a[224:225], v32 offset:36864\n" \
"s_mul_i32 s19, s19, 0xe000\n" \
M_"v[64:67], a[188:191], a[128:131], v[64:67]\n" \
R_"a[128:131], v28 offset:32768\n" \
"s_mov_b32 s11, 0x20000\n" \
M_"v[64:67], a[252:255], a[192:195], v[64:67]\n" \
R_"a[192:195], v29 offset:32768\n" \
"s_or_b32 s12, s4, 0xc000\n" \
M_"v[80:83], a[188:191], a[132:135], v[80:83]\n" \
"s_add_u32 m0, s40, 0x1000\n" \
"s_add_u32 s85, s39, 0x11000\n" \
M_"v[80:83], a[252:255], a[196:199], v[80:83]\n" \
B_"v20, s[80:83], s85 offen sc1 lds\n" \
M_"v[76:79], a[184:187], a[132:135], v[76:79]\n" \
X_"a[226:227], v32 offset:37376\n" \
"s_lshl_b32 s4, s3, 8\n" \
M_"v[76:79], a[248:251], a[196:199], v[76:79]\n" \
X_"a[164:165], v33 offset:32768\n" \
"s_or_b32 s22, s15, s16\n" \
M_"v[72:75], a[180:183], a[132:135], v[72:75]\n" \
"s_or_b32 s23, s19, s15\n" \
"s_lshl_b32 s60, s6, 10\n" \
M_"v[72:75], a[244:247], a[196:199], v[72:75]\n" \
"s_lshl_b32 s61, s5, 10\n" \
"s_lshl_b32 s14, s14, 15\n" \
M_"v[68:71], a[176:179], a[132:135], v[68:71]\n" \
R_"a[132:135], v28 offset:34816\n" \
"s_or_b32 s20, s4, 0x26c00\n" \
M_"v[68:71], a[240:243], a[196:199], v[68:71]\n" \
R_"a[196:199], v29 offset:34816\n" \
"s_or_b32 s13, s22, 0x8000\n" \
M_"v[84:87], a[176:179], a[136:139], v[84:87]\n" \
"s_add_u32 m0, s40, 0x3000\n" \
"s_movk_i32 s84, 0x400\n" \
M_"v[84:87], a[240:243], a[200:203], v[84:87]\n" \
B_"v21, s[76:79], s84 offen offset:0 lds\n" \
M_"v[88:91], a[180:183], a[136:139], v[88:91]\n" \
X_"a[166:167], v33 offset:33280\n" \
"s_add_i32 s22, s22, 0x14000\n" \
M_"v[88:91], a[244:247], a[200:203], v[88:91]\n" \
X_"a[228:229], v33 offset:36864\n" \
"s_bitset1_b32 s23, 16\n" \
M_"v[92:95], a[184:187], a[136:139], v[92:95]\n" \
X_"a[230:231], v33 offset:37376\n" \
"s_or_b32 s24, s7, 0x5be8020\n" \
M_"v[92:95], a[248:251], a[200:203], v[92:95]\n" \
"s_add_u32 s88, s42, 0x2000\n" \
"buffer_store_dwordx4 v[216:219], v20, s[80:83], s88 offen offset:1024 sc1\n" \
M_"v[96:99], a[188:191], a[136:139], v[96:99]\n" \
R_"a[136:139], v28 offset:36864\n" \
"s_and_b32 s57, s27, 0xffff\n" \
M_"v[96:99], a[252:255], a[200:203], v[96:99]\n" \
R_"a[200:203], v29 offset:36864\n" \
"s_mov_b32 s58, 0x6000000\n" \
M_"v[112:115], a[188:191], a[140:143], v[112:115]\n" \
X_"a[172:173], v35 offset:32768\n" \
"s_mov_b32 s56, s26\n" \
M_"v[112:115], a[252:255], a[204:207], v[112:115]\n" \
"s_mov_b32 s59, s11\n" \
"s_add_i32 s60, s60, 0x3000000\n" \
M_"v[108:111], a[184:187], a[140:143], v[108:111]\n" \
X_"a[174:175], v35 offset:33280\n" \
"s_add_i32 s61, s61, 0x411c000\n" \
M_"v[108:111], a[248:251], a[204:207], v[108:111]\n" \
"s_sub_i32 s19, 14, s18\n" \
"s_mov_b32 s21, 0x71800000\n" \
M_"v[104:107], a[180:183], a[140:143], v[104:107]\n" \
"s_add_u32 m0, s31, 0x0\n" \
"s_mov_b32 s84, 0x400000\n" \
M_"v[104:107], a[244:247], a[204:207], v[104:107]\n" \
B_"v26, s[68:71], s84 offen offset:0 lds\n" \
M_"v[100:103], a[176:179], a[140:143], v[100:103]\n" \
R_"a[140:143], v28 offset:38912\n" \
"s_mov_b32 s4, L_gsw-L_gtr\n" \
M_"v[100:103], a[240:243], a[204:207], v[100:103]\n" \
R_"a[204:207], v29 offset:38912\n" \
M_"v[116:119], a[176:179], a[144:147], v[116:119]\n" \
X_"a[236:237], v35 offset:36864\n" \
M_"v[116:119], a[240:243], a[208:211], v[116:119]\n" \
X_"a[238:239], v35 offset:37376\n" \
M_"v[120:123], a[180:183], a[144:147], v[120:123]\n" \
X_"a[168:169], v34 offset:32768\n" \
M_"v[120:123], a[244:247], a[208:211], v[120:123]\n" \
"s_addk_i32 s84, 0x7c00\n" \
"s_nop 0\n" \
M_"v[124:127], a[184:187], a[144:147], v[124:127]\n" \
B_"v26, s[68:71], s84 offen offset:1024 lds\n" \
M_"v[124:127], a[248:251], a[208:211], v[124:127]\n" \
X_"a[170:171], v34 offset:33280\n" \
M_"v[128:131], a[188:191], a[144:147], v[128:131]\n" \
R_"a[144:147], v28 offset:40960\n" \
M_"v[128:131], a[252:255], a[208:211], v[128:131]\n" \
R_"a[208:211], v29 offset:40960\n" \
M_"v[144:147], a[188:191], a[148:151], v[144:147]\n" \
X_"a[232:233], v34 offset:36864\n" \
M_"v[144:147], a[252:255], a[212:215], v[144:147]\n" \
"s_cmp_eq_u32 s19, 0\n" \
"s_cselect_b32 s4, L_gs7-L_gtr, s4\n" \
M_"v[140:143], a[184:187], a[148:151], v[140:143]\n" \
"s_addk_i32 s84, 0x7c00\n" \
M_"v[140:143], a[248:251], a[212:215], v[140:143]\n" \
B_"v27, s[68:71], s84 offen offset:2048 lds\n" \
M_"v[136:139], a[180:183], a[148:151], v[136:139]\n" \
X_"a[234:235], v34 offset:37376\n" \
M_"v[136:139], a[244:247], a[212:215], v[136:139]\n" \
"s_cmp_eq_u32 s16, 0\n" \
"s_cselect_b32 s4, 0, s4\n" \
M_"v[132:135], a[176:179], a[148:151], v[132:135]\n" \
R_"a[148:151], v28 offset:43008\n" \
M_"v[132:135], a[240:243], a[212:215], v[132:135]\n" \
R_"a[212:215], v29 offset:43008\n" \
M_"v[148:151], a[176:179], a[152:155], v[148:151]\n" \
R_"v[248:251], v22 offset:32768\n" \
M_"v[148:151], a[240:243], a[216:219], v[148:151]\n" \
"s_addk_i32 s84, 0x7c00\n" \
M_"v[152:155], a[180:183], a[152:155], v[152:155]\n" \
B_"v27, s[68:71], s84 offen offset:3072 lds\n" \
M_"v[152:155], a[244:247], a[216:219], v[152:155]\n" \
R_"v[48:51], v23 offset:32768\n" \
M_"v[156:159], a[184:187], a[152:155], v[156:159]\n" \
"s_getpc_b64 s[28:29]\n" \
"L_gp0_%=:\n" \
"s_add_u32 s28, s28, L_gtr-L_gp0_%=\n" \
"s_addc_u32 s29, s29, 0\n" \
M_"v[156:159], a[248:251], a[216:219], v[156:159]\n" \
"s_add_u32 s28, s28, s4\n" \
"s_addc_u32 s29, s29, 0\n" \
"s_nop 0\n" \
M_"v[160:163], a[188:191], a[152:155], v[160:163]\n" \
R_"a[152:155], v28 offset:45056\n" \
"v_accvgpr_write_b32 a96, 0\n" \
M_"v[160:163], a[252:255], a[216:219], v[160:163]\n" \
R_"a[216:219], v29 offset:45056\n" \
"v_accvgpr_write_b32 a97, 0\n" \
M_"v[176:179], a[188:191], a[156:159], v[176:179]\n" \
"v_accvgpr_write_b32 a98, 0\n" \
"v_accvgpr_write_b32 a99, 0\n" \
M_"v[176:179], a[252:255], a[220:223], v[176:179]\n" \
"v_accvgpr_write_b32 a100, 0\n" \
"v_accvgpr_write_b32 a101, 0\n" \
M_"v[172:175], a[184:187], a[156:159], v[172:175]\n" \
"v_accvgpr_write_b32 a102, 0\n" \
"v_accvgpr_write_b32 a103, 0\n" \
M_"v[172:175], a[248:251], a[220:223], v[172:175]\n" \
"v_accvgpr_write_b32 a112, 0\n" \
"v_accvgpr_write_b32 a113, 0\n" \
M_"v[168:171], a[180:183], a[156:159], v[168:171]\n" \
"v_accvgpr_write_b32 a114, 0\n" \
"v_accvgpr_write_b32 a115, 0\n" \
M_"v[168:171], a[244:247], a[220:223], v[168:171]\n" \
"v_accvgpr_write_b32 a120, 0\n" \
"v_accvgpr_write_b32 a121, 0\n" \
M_"v[164:167], a[176:179], a[156:159], v[164:167]\n" \
R_"a[156:159], v28 offset:47104\n" \
"v_accvgpr_write_b32 a122, 0\n" \
M_"v[164:167], a[240:243], a[220:223], v[164:167]\n" \
R_"a[220:223], v29 offset:47104\n" \
"s_waitcnt lgkmcnt(15)\n" \
"s_nop 0\n" \
M_"a[0:3], a[160:163], a[128:131], a[0:3]\n" \
"s_add_u32 s89, s42, 0x3000\n" \
"buffer_store_dwordx4 v[220:223], v20, s[80:83], s89 offen offset:0 sc1\n" \
M_"a[0:3], a[224:227], a[192:195], a[0:3]\n" \
X_"a[176:177], v36 offset:32768\n" \
"v_accvgpr_write_b32 a123, 0\n" \
M_"a[4:7], a[164:167], a[128:131], a[4:7]\n" \
X_"a[178:179], v36 offset:33792\n" \
"v_mov_b32_e32 v180, 0x3f803f80\n" \
M_"a[4:7], a[228:231], a[192:195], a[4:7]\n" \
X_"a[240:241], v36 offset:40960\n" \
"s_mov_b32 s6, 0x33333333\n" \
M_"a[12:15], a[172:175], a[128:131], a[12:15]\n" \
X_"a[242:243], v36 offset:41984\n" \
"s_mov_b32 s7, 0xcccccccc\n" \
"s_waitcnt lgkmcnt(15)\n" \
M_"a[12:15], a[236:239], a[192:195], a[12:15]\n" \
"v_cndmask_b32_e64 v181, 0, v180, s[6:7]\n" \
"v_accvgpr_write_b32 a104, v181\n" \
M_"a[8:11], a[128:131], a[168:171], a[8:11]\n" \
X_"a[180:181], v37 offset:32768\n" \
"v_accvgpr_write_b32 a105, v181\n" \
"s_waitcnt lgkmcnt(13)\n" \
M_"a[8:11], a[192:195], a[232:235], a[8:11]\n" \
"v_accvgpr_write_b32 a106, v181\n" \
"v_accvgpr_write_b32 a107, v181\n" \
M_"a[24:27], a[132:135], a[168:171], a[24:27]\n" \
X_"a[182:183], v37 offset:33792\n" \
"s_mov_b32 s6, 0x11111111\n" \
M_"a[24:27], a[196:199], a[232:235], a[24:27]\n" \
X_"a[244:245], v37 offset:40960\n" \
"s_mov_b32 s7, 0x11111111\n" \
M_"a[28:31], a[172:175], a[132:135], a[28:31]\n" \
"v_cndmask_b32_e64 v181, 0, v180, s[6:7]\n" \
"v_accvgpr_write_b32 a108, v181\n" \
M_"a[28:31], a[236:239], a[196:199], a[28:31]\n" \
X_"a[246:247], v37 offset:41984\n" \
"v_accvgpr_write_b32 a109, v181\n" \
M_"a[20:23], a[164:167], a[132:135], a[20:23]\n" \
"v_accvgpr_write_b32 a110, v181\n" \
"v_accvgpr_write_b32 a111, v181\n" \
M_"a[20:23], a[228:231], a[196:199], a[20:23]\n" \
X_"a[184:185], v38 offset:32768\n" \
"s_mov_b32 s6, 0x44444444\n" \
M_"a[16:19], a[160:163], a[132:135], a[16:19]\n" \
X_"a[186:187], v38 offset:33792\n" \
"s_mov_b32 s7, 0x44444444\n" \
M_"a[16:19], a[224:227], a[196:199], a[16:19]\n" \
"v_cndmask_b32_e64 v181, 0, v180, s[6:7]\n" \
"v_accvgpr_write_b32 a116, v181\n" \
M_"a[32:35], a[160:163], a[136:139], a[32:35]\n" \
"s_add_u32 s88, s42, 0x3000\n" \
"buffer_store_dwordx4 v[224:227], v20, s[80:83], s88 offen offset:1024 sc1\n" \
M_"a[32:35], a[224:227], a[200:203], a[32:35]\n" \
X_"a[248:249], v38 offset:40960\n" \
"v_accvgpr_write_b32 a117, v181\n" \
M_"a[36:39], a[164:167], a[136:139], a[36:39]\n" \
"v_lshlrev_b32_e32 v45, 3, v20\n" \
"v_add_u32_e32 v45, s30, v45\n" \
M_"a[36:39], a[228:231], a[200:203], a[36:39]\n" \
"s_getpc_b64 s[86:87]\n" \
"L_ct0_%=:\n" \
"s_add_u32 s86, s86, L_ap0-L_ct0_%=\n" \
"s_addc_u32 s87, s87, 0\n" \
"s_add_u32 m0, s40, 0x2000\n" \
M_"a[44:47], a[172:175], a[136:139], a[44:47]\n" \
"global_load_lds_dword v45, s[86:87]\n" \
M_"a[44:47], a[236:239], a[200:203], a[44:47]\n" \
X_"a[250:251], v38 offset:41984\n" \
"v_accvgpr_write_b32 a118, v181\n" \
M_"a[40:43], a[136:139], a[168:171], a[40:43]\n" \
X_"a[188:189], v39 offset:32768\n" \
"v_accvgpr_write_b32 a119, v181\n" \
M_"a[40:43], a[200:203], a[232:235], a[40:43]\n" \
X_"a[190:191], v39 offset:33792\n" \
"v_bfe_i32 v181, v20, 4, 1\n" \
M_"a[56:59], a[140:143], a[168:171], a[56:59]\n" \
X_"a[252:253], v39 offset:40960\n" \
"v_and_b32_e32 v181, 0xaaaaaaaa, v181\n" \
M_"a[56:59], a[204:207], a[232:235], a[56:59]\n" \
X_"a[254:255], v39 offset:41984\n" \
"v_or_b32_e32 v181, 0x44444444, v181\n" \
M_"a[60:63], a[172:175], a[140:143], a[60:63]\n" \
"v_accvgpr_write_b32 a126, v181\n" \
"v_bfe_u32 v180, v20, 5, 3\n" \
M_"a[60:63], a[236:239], a[204:207], a[60:63]\n" \
"v_bfe_u32 v181, v20, 8, 2\n" \
"v_lshlrev_b32_e64 v181, 1, v181\n" \
M_"a[52:55], a[164:167], a[140:143], a[52:55]\n" \
"v_sub_u32_e32 v180, v180, v181\n" \
"v_and_b32_e32 v181, 16, v20\n" \
M_"a[52:55], a[228:231], a[204:207], a[52:55]\n" \
"v_mov_b32_e32 v182, 0x3f80\n" \
"v_lshlrev_b32_e64 v181, v181, v182\n" \
M_"a[48:51], a[160:163], a[140:143], a[48:51]\n" \
"v_cmp_eq_u32_e64 vcc, 0, v180\n" \
"v_cmp_eq_u32_e64 s[6:7], 1, v180\n" \
M_"a[48:51], a[224:227], a[204:207], a[48:51]\n" \
"v_cndmask_b32_e64 v182, 0, v181, vcc\n" \
"v_cndmask_b32_e64 v183, 0, v181, s[6:7]\n" \
"s_waitcnt lgkmcnt(15)\n" \
M_"a[64:67], a[164:167], v[248:251], a[64:67]\n" \
"v_accvgpr_write_b32 a124, v182\n" \
"v_accvgpr_write_b32 a125, v183\n" \
M_"a[64:67], a[228:231], v[48:51], a[64:67]\n" \
M_"a[80:83], a[172:175], v[248:251], a[80:83]\n" \
M_"a[80:83], a[236:239], v[48:51], a[80:83]\n" \
M_"a[68:71], v[248:251], a[168:171], a[68:71]\n" \
"s_waitcnt vmcnt(3) lgkmcnt(0)\n" \
M_"a[68:71], v[48:51], a[232:235], a[68:71]\n" \
"s_barrier\n" \
"v_xor_b32_e32 v28, 0x20000, v28\n" \
"v_xor_b32_e32 v29, 0x20000, v29\n" \
M_"v[52:55], a[176:179], a[128:131], v[52:55]\n" \
X_"a[160:161], v32 offset:0\n" \
"v_accvgpr_read_b32 v184, a0\n" \
M_"v[52:55], a[240:243], a[192:195], v[52:55]\n" \
"v_accvgpr_read_b32 v185, a1\n" \
"v_accvgpr_read_b32 v186, a2\n" \
M_"v[56:59], a[180:183], a[128:131], v[56:59]\n" \
X_"a[162:163], v32 offset:512\n" \
"v_accvgpr_read_b32 v187, a3\n" \
M_"v[56:59], a[244:247], a[192:195], v[56:59]\n" \
"v_accvgpr_read_b32 v188, a4\n" \
"v_accvgpr_read_b32 v189, a5\n" \
M_"v[60:63], a[184:187], a[128:131], v[60:63]\n" \
"v_accvgpr_read_b32 v190, a6\n" \
"v_accvgpr_read_b32 v191, a7\n" \
M_"v[60:63], a[248:251], a[192:195], v[60:63]\n" \
X_"a[224:225], v32 offset:4096\n" \
"v_accvgpr_read_b32 v196, a12\n" \
M_"v[64:67], a[188:191], a[128:131], v[64:67]\n" \
R_"a[128:131], v28 offset:0\n" \
"v_accvgpr_read_b32 v197, a13\n" \
M_"v[64:67], a[252:255], a[192:195], v[64:67]\n" \
R_"a[192:195], v29 offset:0\n" \
"v_accvgpr_read_b32 v198, a14\n" \
M_"v[80:83], a[188:191], a[132:135], v[80:83]\n" \
X_"a[226:227], v32 offset:4608\n" \
"v_accvgpr_read_b32 v199, a15\n" \
M_"v[80:83], a[252:255], a[196:199], v[80:83]\n" \
"v_accvgpr_read_b32 v192, a8\n" \
"v_accvgpr_read_b32 v193, a9\n" \
M_"v[76:79], a[184:187], a[132:135], v[76:79]\n" \
"v_accvgpr_read_b32 v194, a10\n" \
"v_accvgpr_read_b32 v195, a11\n" \
M_"v[76:79], a[248:251], a[196:199], v[76:79]\n" \
X_"a[164:165], v33 offset:0\n" \
"v_accvgpr_read_b32 v208, a24\n" \
M_"v[72:75], a[180:183], a[132:135], v[72:75]\n" \
"v_accvgpr_read_b32 v209, a25\n" \
"v_accvgpr_read_b32 v210, a26\n" \
M_"v[72:75], a[244:247], a[196:199], v[72:75]\n" \
"v_accvgpr_read_b32 v211, a27\n" \
"v_accvgpr_read_b32 v212, a28\n" \
M_"v[68:71], a[176:179], a[132:135], v[68:71]\n" \
R_"a[132:135], v28 offset:2048\n" \
"v_accvgpr_read_b32 v213, a29\n" \
M_"v[68:71], a[240:243], a[196:199], v[68:71]\n" \
R_"a[196:199], v29 offset:2048\n" \
"v_accvgpr_read_b32 v214, a30\n" \
M_"v[84:87], a[176:179], a[136:139], v[84:87]\n" \
X_"a[166:167], v33 offset:512\n" \
"v_accvgpr_read_b32 v215, a31\n" \
M_"v[84:87], a[240:243], a[200:203], v[84:87]\n" \
X_"a[228:229], v33 offset:4096\n" \
"v_accvgpr_read_b32 v204, a20\n" \
M_"v[88:91], a[180:183], a[136:139], v[88:91]\n" \
"v_accvgpr_read_b32 v205, a21\n" \
"v_accvgpr_read_b32 v206, a22\n" \
M_"v[88:91], a[244:247], a[200:203], v[88:91]\n" \
"v_accvgpr_read_b32 v207, a23\n" \
"v_accvgpr_read_b32 v200, a16\n" \
M_"v[92:95], a[184:187], a[136:139], v[92:95]\n" \
X_"a[230:231], v33 offset:4608\n" \
"v_accvgpr_read_b32 v201, a17\n" \
M_"v[92:95], a[248:251], a[200:203], v[92:95]\n" \
"v_accvgpr_read_b32 v202, a18\n" \
"v_accvgpr_read_b32 v203, a19\n" \
M_"v[96:99], a[188:191], a[136:139], v[96:99]\n" \
R_"a[136:139], v28 offset:4096\n" \
"v_accvgpr_read_b32 v216, a32\n" \
M_"v[96:99], a[252:255], a[200:203], v[96:99]\n" \
R_"a[200:203], v29 offset:4096\n" \
"v_accvgpr_read_b32 v217, a33\n" \
M_"v[112:115], a[188:191], a[140:143], v[112:115]\n" \
X_"a[172:173], v35 offset:0\n" \
"v_accvgpr_read_b32 v218, a34\n" \
M_"v[112:115], a[252:255], a[204:207], v[112:115]\n" \
"v_accvgpr_read_b32 v219, a35\n" \
"v_accvgpr_read_b32 v220, a36\n" \
M_"v[108:111], a[184:187], a[140:143], v[108:111]\n" \
X_"a[174:175], v35 offset:512\n" \
"v_accvgpr_read_b32 v221, a37\n" \
M_"v[108:111], a[248:251], a[204:207], v[108:111]\n" \
"v_accvgpr_read_b32 v222, a38\n" \
"v_accvgpr_read_b32 v223, a39\n" \
M_"v[104:107], a[180:183], a[140:143], v[104:107]\n" \
X_"a[236:237], v35 offset:4096\n" \
"v_accvgpr_read_b32 v228, a44\n" \
M_"v[104:107], a[244:247], a[204:207], v[104:107]\n" \
"v_accvgpr_read_b32 v229, a45\n" \
"v_accvgpr_read_b32 v230, a46\n" \
M_"v[100:103], a[176:179], a[140:143], v[100:103]\n" \
R_"a[140:143], v28 offset:6144\n" \
"v_accvgpr_read_b32 v231, a47\n" \
M_"v[100:103], a[240:243], a[204:207], v[100:103]\n" \
R_"a[204:207], v29 offset:6144\n" \
"v_accvgpr_read_b32 v224, a40\n" \
M_"v[116:119], a[176:179], a[144:147], v[116:119]\n" \
X_"a[238:239], v35 offset:4608\n" \
"v_accvgpr_read_b32 v225, a41\n" \
M_"v[116:119], a[240:243], a[208:211], v[116:119]\n" \
"v_accvgpr_read_b32 v226, a42\n" \
"v_accvgpr_read_b32 v227, a43\n" \
M_"v[120:123], a[180:183], a[144:147], v[120:123]\n" \
X_"a[168:169], v34 offset:0\n" \
"v_accvgpr_read_b32 v240, a56\n" \
M_"v[120:123], a[244:247], a[208:211], v[120:123]\n" \
"v_accvgpr_read_b32 v241, a57\n" \
"v_accvgpr_read_b32 v242, a58\n" \
M_"v[124:127], a[184:187], a[144:147], v[124:127]\n" \
"v_accvgpr_read_b32 v243, a59\n" \
"v_accvgpr_read_b32 v244, a60\n" \
M_"v[124:127], a[248:251], a[208:211], v[124:127]\n" \
X_"a[170:171], v34 offset:512\n" \
"v_accvgpr_read_b32 v245, a61\n" \
M_"v[128:131], a[188:191], a[144:147], v[128:131]\n" \
R_"a[144:147], v28 offset:8192\n" \
"v_accvgpr_read_b32 v246, a62\n" \
M_"v[128:131], a[252:255], a[208:211], v[128:131]\n" \
R_"a[208:211], v29 offset:8192\n" \
"v_accvgpr_read_b32 v247, a63\n" \
M_"v[144:147], a[188:191], a[148:151], v[144:147]\n" \
X_"a[232:233], v34 offset:4096\n" \
"v_accvgpr_read_b32 v236, a52\n" \
M_"v[144:147], a[252:255], a[212:215], v[144:147]\n" \
"v_accvgpr_read_b32 v237, a53\n" \
"v_accvgpr_read_b32 v238, a54\n" \
M_"v[140:143], a[184:187], a[148:151], v[140:143]\n" \
"v_accvgpr_read_b32 v239, a55\n" \
"v_accvgpr_read_b32 v232, a48\n" \
M_"v[140:143], a[248:251], a[212:215], v[140:143]\n" \
X_"a[234:235], v34 offset:4608\n" \
"v_accvgpr_read_b32 v233, a49\n" \
M_"v[136:139], a[180:183], a[148:151], v[136:139]\n" \
"v_accvgpr_read_b32 v234, a50\n" \
"v_accvgpr_read_b32 v235, a51\n" \
M_"v[136:139], a[244:247], a[212:215], v[136:139]\n" \
"v_accvgpr_read_b32 v180, a64\n" \
"v_accvgpr_read_b32 v181, a65\n" \
M_"v[132:135], a[176:179], a[148:151], v[132:135]\n" \
R_"a[148:151], v28 offset:10240\n" \
"v_accvgpr_read_b32 v182, a66\n" \
M_"v[132:135], a[240:243], a[212:215], v[132:135]\n" \
R_"a[212:215], v29 offset:10240\n" \
"v_accvgpr_read_b32 v183, a67\n" \
M_"v[148:151], a[176:179], a[152:155], v[148:151]\n" \
R_"v[248:251], v22 offset:0\n" \
"v_accvgpr_read_b32 v252, a68\n" \
M_"v[148:151], a[240:243], a[216:219], v[148:151]\n" \
R_"v[48:51], v23 offset:0\n" \
"v_accvgpr_read_b32 v253, a69\n" \
M_"v[152:155], a[180:183], a[152:155], v[152:155]\n" \
"v_accvgpr_read_b32 v254, a70\n" \
"v_accvgpr_read_b32 v255, a71\n" \
M_"v[152:155], a[244:247], a[216:219], v[152:155]\n" \
"v_lshrrev_b32_e32 v24, 9, v20\n" \
"v_and_b32_e32 v26, 0x1ff, v20\n" \
M_"v[156:159], a[184:187], a[152:155], v[156:159]\n" \
"v_lshl_add_u32 v24, v24, 3, v26\n" \
"v_lshrrev_b32_e64 v25, 8, v20\n" \
M_"v[156:159], a[248:251], a[216:219], v[156:159]\n" \
"v_mul_u32_u24_e32 v25, 0x24700, v25\n" \
"v_add_u32_e64 v25, v25, v20\n" \
M_"v[160:163], a[188:191], a[152:155], v[160:163]\n" \
R_"a[152:155], v28 offset:12288\n" \
"v_lshrrev_b32_e64 v26, 2, v20\n" \
M_"v[160:163], a[252:255], a[216:219], v[160:163]\n" \
R_"a[216:219], v29 offset:12288\n" \
"v_add_u32_e64 v26, s45, v26\n" \
M_"v[176:179], a[188:191], a[156:159], v[176:179]\n" \
"v_add_u32_e32 v27, s46, v20\n" \
"v_add_u32_e32 v21, s43, v27\n" \
M_"v[176:179], a[252:255], a[220:223], v[176:179]\n" \
M_"v[172:175], a[184:187], a[156:159], v[172:175]\n" \
M_"v[172:175], a[248:251], a[220:223], v[172:175]\n" \
M_"v[168:171], a[180:183], a[156:159], v[168:171]\n" \
M_"v[168:171], a[244:247], a[220:223], v[168:171]\n" \
M_"v[164:167], a[176:179], a[156:159], v[164:167]\n" \
R_"a[156:159], v28 offset:14336\n" \
M_"v[164:167], a[240:243], a[220:223], v[164:167]\n" \
R_"a[220:223], v29 offset:14336\n" \
"s_waitcnt lgkmcnt(15)\n" \
"s_nop 0\n" \
M_"v[184:187], a[160:163], a[128:131], v[184:187]\n" \
"v_bfe_u32 v44, v20, 2, 5\n" \
X_"a[176:177], v36 offset:0\n" \
M_"v[184:187], a[224:227], a[192:195], v[184:187]\n" \
"buffer_load_dword v44, v44, s[80:83], s38 offen offset:0 sc1\n" \
M_"v[188:191], a[164:167], a[128:131], v[188:191]\n" \
X_"a[178:179], v36 offset:1024\n" \
M_"v[188:191], a[228:231], a[192:195], v[188:191]\n" \
X_"a[240:241], v36 offset:8192\n" \
M_"v[196:199], a[172:175], a[128:131], v[196:199]\n" \
X_"a[242:243], v36 offset:9216\n" \
"s_waitcnt lgkmcnt(15)\n" \
M_"v[196:199], a[236:239], a[192:195], v[196:199]\n" \
M_"v[192:195], a[128:131], a[168:171], v[192:195]\n" \
X_"a[180:181], v37 offset:0\n" \
"s_waitcnt lgkmcnt(13)\n" \
M_"v[192:195], a[192:195], a[232:235], v[192:195]\n" \
M_"v[208:211], a[132:135], a[168:171], v[208:211]\n" \
X_"a[182:183], v37 offset:1024\n" \
M_"v[208:211], a[196:199], a[232:235], v[208:211]\n" \
X_"a[244:245], v37 offset:8192\n" \
M_"v[212:215], a[172:175], a[132:135], v[212:215]\n" \
M_"v[212:215], a[236:239], a[196:199], v[212:215]\n" \
X_"a[246:247], v37 offset:9216\n" \
C_"v188, v188, v189\n" \
M_"v[204:207], a[164:167], a[132:135], v[204:207]\n" \
C_"v189, v190, v191\n" \
C_"v190, v198, v199\n" \
M_"v[204:207], a[228:231], a[196:199], v[204:207]\n" \
X_"a[184:185], v38 offset:0\n" \
"v_mov_b32_e32 v191, 0x3f80\n" \
M_"v[200:203], a[160:163], a[132:135], v[200:203]\n" \
X_"a[186:187], v38 offset:1024\n" \
C_"v184, v184, v185\n" \
M_"v[200:203], a[224:227], a[196:199], v[200:203]\n" \
C_"v185, v186, v187\n" \
C_"v186, v196, v197\n" \
M_"v[216:219], a[160:163], a[136:139], v[216:219]\n" \
X_"a[248:249], v38 offset:8192\n" \
"v_mov_b32_e64 v187, 0\n" \
M_"v[216:219], a[224:227], a[200:203], v[216:219]\n" \
X_"a[250:251], v38 offset:9216\n" \
M_"v[220:223], a[164:167], a[136:139], v[220:223]\n" \
M_"v[220:223], a[228:231], a[200:203], v[220:223]\n" \
X_"a[188:189], v39 offset:0\n" \
C_"v204, v204, v205\n" \
M_"v[228:231], a[172:175], a[136:139], v[228:231]\n" \
C_"v205, v206, v207\n" \
C_"v206, v214, v215\n" \
M_"v[228:231], a[236:239], a[200:203], v[228:231]\n" \
".fill (L_pb + 36576 - .) / 4, 4, 0xBF800000\n" \
"L_pp9:\n" \
"s_cbranch_execz L_pret\n" \
X_"a[190:191], v39 offset:1024\n" \
"v_mov_b32_e32 v207, 0x3f80\n" \
M_"v[224:227], a[136:139], a[168:171], v[224:227]\n" \
X_"a[252:253], v39 offset:8192\n" \
C_"v200, v200, v201\n" \
M_"v[224:227], a[200:203], a[232:235], v[224:227]\n" \
C_"v201, v202, v203\n" \
C_"v202, v212, v213\n" \
M_"v[240:243], a[140:143], a[168:171], v[240:243]\n" \
X_"a[254:255], v39 offset:9216\n" \
"v_mov_b32_e32 v203, 0\n" \
M_"v[240:243], a[204:207], a[232:235], v[240:243]\n" \
M_"v[244:247], a[172:175], a[140:143], v[244:247]\n" \
M_"v[244:247], a[236:239], a[204:207], v[244:247]\n" \
C_"v220, v220, v221\n" \
C_"v221, v222, v223\n" \
M_"v[236:239], a[164:167], a[140:143], v[236:239]\n" \
C_"v222, v230, v231\n" \
"v_mov_b32_e32 v223, 0x3f80\n" \
M_"v[236:239], a[228:231], a[204:207], v[236:239]\n" \
C_"v216, v216, v217\n" \
C_"v217, v218, v219\n" \
M_"v[232:235], a[160:163], a[140:143], v[232:235]\n" \
C_"v218, v228, v229\n" \
"v_mov_b32_e64 v219, 0\n" \
M_"v[232:235], a[224:227], a[204:207], v[232:235]\n" \
"s_waitcnt lgkmcnt(15)\n" \
"s_nop 0\n" \
M_"v[180:183], a[164:167], v[248:251], v[180:183]\n" \
M_"v[180:183], a[228:231], v[48:51], v[180:183]\n" \
M_"a[80:83], a[172:175], v[248:251], a[80:83]\n" \
M_"a[80:83], a[236:239], v[48:51], a[80:83]\n" \
C_"v236, v236, v237\n" \
C_"v237, v238, v239\n" \
M_"v[252:255], v[248:251], a[168:171], v[252:255]\n" \
C_"v238, v246, v247\n" \
"v_mov_b32_e32 v239, 0x3f80\n" \
M_"v[252:255], v[48:51], a[232:235], v[252:255]\n" \
C_"v232, v232, v233\n" \
C_"v233, v234, v235\n" \
"s_waitcnt vmcnt(0) lgkmcnt(0)\n" \
"s_barrier\n" \
"v_mov_b32_e32 v45, s62\n" \
"s_mov_b64 exec, 1\n" \
"buffer_store_dword v45, off, s[80:83], s37 offset:32 sc1\n" \
"s_mov_b64 exec, -1\n" \
"v_cmp_ne_u32_e32 vcc, s62, v44\n" \
"s_nop 4\n" \
"s_cbranch_vccnz L_poll160_%=\n" \
"L_pollr160_%=:\n" \
M_"v[52:55], a[176:179], a[128:131], v[52:55]\n" \
"s_mov_b32 s73, s50\n" \
"s_sub_u32 m0, s31, 0x10000\n" \
M_"v[52:55], a[240:243], a[192:195], v[52:55]\n" \
B_"v20, s[80:83], s73 offen offset:0 sc1 lds\n" \
M_"v[56:59], a[180:183], a[128:131], v[56:59]\n" \
"s_add_u32 s74, s50, 0x4000\n" \
"s_add_u32 s75, s50, 0x8000\n" \
M_"v[56:59], a[244:247], a[192:195], v[56:59]\n" \
"s_mov_b32 s70, 0x3e0293ee\n" \
"s_mov_b32 s76, s52\n" \
M_"v[60:63], a[184:187], a[128:131], v[60:63]\n" \
"v_mul_f32_e32 v52, s70, v52\n" \
"v_mul_f32_e32 v53, s70, v53\n" \
M_"v[60:63], a[248:251], a[192:195], v[60:63]\n" \
"v_mul_f32_e32 v54, s70, v54\n" \
"v_mul_f32_e32 v55, s70, v55\n" \
M_"v[64:67], a[188:191], a[128:131], v[64:67]\n" \
C_"v52, v52, v53\n" \
"v_mul_f32_e64 v56, s70, v56\n" \
B_"v20, s[80:83], s73 offen offset:1024 sc1 lds\n" \
M_"v[64:67], a[252:255], a[192:195], v[64:67]\n" \
"v_mul_f32_e32 v57, s70, v57\n" \
"v_mul_f32_e32 v60, s70, v60\n" \
M_"v[80:83], a[188:191], a[132:135], v[80:83]\n" \
"v_mul_f32_e32 v61, s70, v61\n" \
"v_mul_f32_e32 v62, s70, v62\n" \
M_"v[80:83], a[252:255], a[196:199], v[80:83]\n" \
"v_mul_f32_e64 v63, s70, v63\n" \
C_"v60, v60, v61\n" \
M_"v[76:79], a[184:187], a[132:135], v[76:79]\n" \
"v_mul_f32_e32 v58, s70, v58\n" \
"v_mul_f32_e32 v59, s70, v59\n" \
M_"v[76:79], a[248:251], a[196:199], v[76:79]\n" \
C_"v53, v54, v55\n" \
"v_mul_f32_e64 v64, s70, v64\n" \
B_"v20, s[80:83], s73 offen offset:2048 sc1 lds\n" \
M_"v[72:75], a[180:183], a[132:135], v[72:75]\n" \
"v_mul_f32_e32 v65, s70, v65\n" \
"v_mul_f32_e32 v66, s70, v66\n" \
M_"v[72:75], a[244:247], a[196:199], v[72:75]\n" \
"v_mul_f32_e64 v67, s70, v67\n" \
C_"v61, v62, v63\n" \
M_"v[68:71], a[176:179], a[132:135], v[68:71]\n" \
C_"v54, v56, v57\n" \
C_"v55, v58, v59\n" \
M_"v[68:71], a[240:243], a[196:199], v[68:71]\n" \
C_"v62, v64, v65\n" \
C_"v63, v66, v67\n" \
M_"v[84:87], a[176:179], a[136:139], v[84:87]\n" \
"ds_write_b128 v27, v[52:55] offset:0\n" \
B_"v20, s[80:83], s73 offen offset:3072 sc1 lds\n" \
M_"v[84:87], a[240:243], a[200:203], v[84:87]\n" \
"s_sub_u32 m0, s31, s51\n" \
"v_mul_f32_e32 v76, s70, v76\n" \
M_"v[88:91], a[180:183], a[136:139], v[88:91]\n" \
"v_mul_f32_e32 v68, s70, v68\n" \
"v_mul_f32_e32 v69, s70, v69\n" \
M_"v[88:91], a[244:247], a[200:203], v[88:91]\n" \
"ds_write_b128 v27, v[60:63] offset:1024\n" \
M_"v[92:95], a[184:187], a[136:139], v[92:95]\n" \
"v_mul_f32_e32 v77, s70, v77\n" \
"v_mul_f32_e32 v70, s70, v70\n" \
M_"v[92:95], a[248:251], a[200:203], v[92:95]\n" \
"v_mul_f32_e64 v71, s70, v71\n" \
C_"v68, v68, v69\n" \
B_"v20, s[80:83], s74 offen offset:0 sc1 lds\n" \
M_"v[96:99], a[188:191], a[136:139], v[96:99]\n" \
"v_mul_f32_e32 v78, s70, v78\n" \
"v_mul_f32_e32 v79, s70, v79\n" \
M_"v[96:99], a[252:255], a[200:203], v[96:99]\n" \
C_"v76, v76, v77\n" \
"v_mul_f32_e64 v72, s70, v72\n" \
M_"v[112:115], a[188:191], a[140:143], v[112:115]\n" \
"v_mul_f32_e32 v73, s70, v73\n" \
"v_mul_f32_e32 v74, s70, v74\n" \
M_"v[112:115], a[252:255], a[204:207], v[112:115]\n" \
"v_mul_f32_e64 v75, s70, v75\n" \
C_"v69, v70, v71\n" \
M_"v[108:111], a[184:187], a[140:143], v[108:111]\n" \
"v_mul_f32_e32 v80, s70, v80\n" \
"v_mul_f32_e32 v81, s70, v81\n" \
B_"v20, s[80:83], s74 offen offset:1024 sc1 lds\n" \
M_"v[108:111], a[248:251], a[204:207], v[108:111]\n" \
"v_mul_f32_e32 v82, s70, v82\n" \
"v_mul_f32_e32 v83, s70, v83\n" \
M_"v[104:107], a[180:183], a[140:143], v[104:107]\n" \
C_"v77, v78, v79\n" \
C_"v70, v72, v73\n" \
M_"v[104:107], a[244:247], a[204:207], v[104:107]\n" \
C_"v71, v74, v75\n" \
C_"v78, v80, v81\n" \
M_"v[100:103], a[176:179], a[140:143], v[100:103]\n" \
C_"v79, v82, v83\n" \
C_"v180, v180, v181\n" \
M_"v[100:103], a[240:243], a[204:207], v[100:103]\n" \
"ds_write_b128 v27, v[68:71] offset:4096\n" \
B_"v20, s[80:83], s74 offen offset:2048 sc1 lds\n" \
M_"v[116:119], a[176:179], a[144:147], v[116:119]\n" \
"v_accvgpr_read_b32 v50, a82\n" \
"v_accvgpr_read_b32 v51, a83\n" \
M_"v[116:119], a[240:243], a[208:211], v[116:119]\n" \
C_"v181, v182, v183\n" \
C_"v182, v50, v51\n" \
M_"v[120:123], a[180:183], a[144:147], v[120:123]\n" \
"ds_write_b128 v27, v[76:79] offset:5120\n" \
M_"v[120:123], a[244:247], a[208:211], v[120:123]\n" \
"v_mov_b32_e32 v183, 0x3f80\n" \
C_"v234, v244, v245\n" \
M_"v[124:127], a[184:187], a[144:147], v[124:127]\n" \
"v_mov_b32_e64 v235, 0\n" \
C_"v54, v192, v193\n" \
B_"v20, s[80:83], s74 offen offset:3072 sc1 lds\n" \
"s_sub_u32 m0, s31, 0x4000\n" \
M_"v[124:127], a[248:251], a[208:211], v[124:127]\n" \
C_"v55, v194, v195\n" \
C_"v56, v208, v209\n" \
M_"v[128:131], a[188:191], a[144:147], v[128:131]\n" \
C_"v57, v210, v211\n" \
C_"v58, v224, v225\n" \
M_"v[128:131], a[252:255], a[208:211], v[128:131]\n" \
C_"v59, v226, v227\n" \
C_"v60, v240, v241\n" \
M_"v[144:147], a[188:191], a[148:151], v[144:147]\n" \
C_"v61, v242, v243\n" \
"s_mov_b32 s64, 0xaaaaaaaa\n" \
M_"v[144:147], a[252:255], a[212:215], v[144:147]\n" \
M_"v[32:35], v[180:183], v[184:187], 0\n" \
B_"v20, s[80:83], s75 offen offset:0 sc1 lds\n" \
M_"v[140:143], a[184:187], a[148:151], v[140:143]\n" \
M_"v[192:195], v[188:191], v[184:187], 0\n" \
M_"v[208:211], v[204:207], v[200:203], 0\n" \
M_"v[36:39], v[188:191], v[200:203], 0\n" \
M_"v[224:227], v[220:223], v[216:219], 0\n" \
M_"v[248:251], v[204:207], v[216:219], 0\n" \
B_"v20, s[80:83], s75 offen offset:1024 sc1 lds\n" \
M_"v[140:143], a[248:251], a[212:215], v[140:143]\n" \
M_"v[240:243], v[236:239], v[232:235], 0\n" \
M_"v[64:67], v[220:223], v[232:235], 0\n" \
"s_mov_b32 s65, 0xaaaaaaaa\n" \
"v_cndmask_b32_e64 v32, v32, v33, s[64:65]\n" \
M_"v[136:139], a[180:183], a[148:151], v[136:139]\n" \
"v_cndmask_b32_e64 v34, v34, v35, s[64:65]\n" \
"s_mov_b32 s66, 0xfff0ffff\n" \
M_"v[136:139], a[244:247], a[212:215], v[136:139]\n" \
"s_mov_b32 s67, 0xf000ff00\n" \
"s_mov_b32 s68, 0xffc0fffc\n" \
B_"v20, s[80:83], s75 offen offset:2048 sc1 lds\n" \
M_"v[132:135], a[176:179], a[148:151], v[132:135]\n" \
"s_mov_b32 s69, 0xc000fc00\n" \
"v_cndmask_b32_e64 v192, v192, v193, s[64:65]\n" \
M_"v[132:135], a[240:243], a[212:215], v[132:135]\n" \
"v_add_f32_e64 v32, s47, v32\n" \
"v_cndmask_b32_e64 v194, v194, v195, s[64:65]\n" \
M_"v[148:151], a[176:179], a[152:155], v[148:151]\n" \
"v_add_f32_e64 v34, s47, v34\n" \
"v_cndmask_b32_e64 v208, v208, v209, s[64:65]\n" \
M_"v[148:151], a[240:243], a[216:219], v[148:151]\n" \
"v_cndmask_b32_e64 v36, v36, v37, s[64:65]\n" \
"v_cndmask_b32_e64 v210, v210, v211, s[64:65]\n" \
M_"v[152:155], a[180:183], a[152:155], v[152:155]\n" \
"v_cndmask_b32_e64 v38, v38, v39, s[64:65]\n" \
"v_cndmask_b32_e64 v224, v224, v225, s[64:65]\n" \
B_"v20, s[80:83], s75 offen offset:3072 sc1 lds\n" \
"s_mov_b32 m0, s53\n" \
"s_nop 0\n" \
M_"v[152:155], a[244:247], a[216:219], v[152:155]\n" \
"v_cndmask_b32_e64 v248, v248, v249, s[64:65]\n" \
"v_cndmask_b32_e64 v226, v226, v227, s[64:65]\n" \
M_"v[156:159], a[184:187], a[152:155], v[156:159]\n" \
"v_cndmask_b32_e64 v250, v250, v251, s[64:65]\n" \
"v_cndmask_b32_e64 v240, v240, v241, s[64:65]\n" \
M_"v[156:159], a[248:251], a[216:219], v[156:159]\n" \
"v_cndmask_b32_e64 v64, v64, v65, s[64:65]\n" \
"v_cndmask_b32_e64 v242, v242, v243, s[64:65]\n" \
M_"v[160:163], a[188:191], a[152:155], v[160:163]\n" \
"v_cndmask_b32_e64 v66, v66, v67, s[64:65]\n" \
"v_cndmask_b32_e64 v192, v32, v192, s[66:67]\n" \
"v_cndmask_b32_e64 v194, v34, v194, s[68:69]\n" \
"v_cndmask_b32_e64 v208, v36, v208, s[66:67]\n" \
"v_cndmask_b32_e64 v210, v38, v210, s[68:69]\n" \
B_"v20, s[80:83], s76 offen offset:0 sc1 lds\n" \
"v_cndmask_b32_e64 v224, v248, v224, s[66:67]\n" \
"v_cndmask_b32_e64 v226, v250, v226, s[68:69]\n" \
"v_cndmask_b32_e64 v240, v64, v240, s[66:67]\n" \
"v_cndmask_b32_e64 v242, v66, v242, s[68:69]\n" \
M_"v[160:163], a[252:255], a[216:219], v[160:163]\n" \
"v_max_f32_e32 v193, v192, v194\n" \
"v_max_f32_e32 v209, v208, v210\n" \
"v_max_f32_e32 v225, v224, v226\n" \
"v_max_f32_e32 v241, v240, v242\n" \
"v_mul_f32_e32 v84, s70, v84\n" \
"v_permlane32_swap_b32_e32 v193, v225\n" \
"v_permlane32_swap_b32_e32 v209, v241\n" \
"v_mul_f32_e32 v85, s70, v85\n" \
"v_mul_f32_e32 v92, s70, v92\n" \
"v_mul_f32_e32 v93, s70, v93\n" \
"v_permlane32_swap_b32_e32 v196, v228\n" \
"v_permlane32_swap_b32_e32 v212, v244\n" \
"v_max_f32_e32 v193, v193, v225\n" \
"v_max_f32_e32 v209, v209, v241\n" \
B_"v20, s[80:83], s76 offen offset:1024 sc1 lds\n" \
"v_accvgpr_read_b32 v48, a80\n" \
"v_accvgpr_read_b32 v49, a81\n" \
"v_mul_f32_e32 v86, s70, v86\n" \
"v_mul_f32_e32 v87, s70, v87\n" \
C_"v84, v84, v85\n" \
"v_mul_f32_e32 v94, s70, v94\n" \
"v_mul_f32_e32 v95, s70, v95\n" \
C_"v92, v92, v93\n" \
"v_permlane16_swap_b32_e32 v228, v244\n" \
"v_permlane16_swap_b32_e32 v193, v209\n" \
M_"v[176:179], a[188:191], a[156:159], v[176:179]\n" \
"s_mov_b32 s72, 0x3eae1a93\n" \
"v_mul_f32_e32 v88, s70, v88\n" \
"v_mul_f32_e32 v89, s70, v89\n" \
"v_mul_f32_e32 v90, s70, v90\n" \
"v_mul_f32_e32 v91, s70, v91\n" \
C_"v85, v86, v87\n" \
"v_mul_f32_e32 v96, s70, v96\n" \
"v_mul_f32_e32 v97, s70, v97\n" \
"v_mul_f32_e32 v98, s70, v98\n" \
"v_mul_f32_e32 v99, s70, v99\n" \
C_"v93, v94, v95\n" \
"v_mul_f32_e32 v45, 0xbfb8aa3b, v228\n" \
"v_mfma_f32_16x16x16_bf16 v[48:51], v[182:183], v[46:47], 0\n" \
"v_mfma_f32_16x16x16_bf16 v[196:199], v[190:191], v[46:47], 0\n" \
"v_mfma_f32_16x16x16_bf16 v[212:215], v[206:207], v[46:47], 0\n" \
"v_max_f32_e64 v193, v193, v209\n" \
C_"v86, v88, v89\n" \
C_"v87, v90, v91\n" \
C_"v94, v96, v97\n" \
C_"v95, v98, v99\n" \
"v_mfma_f32_16x16x16_bf16 v[228:231], v[222:223], v[46:47], 0\n" \
"v_mul_f32_e32 v193, s72, v193\n" \
"v_mov_b32_e32 v209, v193\n" \
"ds_write_b128 v27, v[84:87] offset:8192\n" \
"v_mfma_f32_16x16x16_bf16 v[244:247], v[238:239], v[46:47], 0\n" \
"v_permlane16_swap_b32_e32 v193, v209\n" \
"s_mov_b32 s71, 0x3e0293ee\n" \
M_"v[176:179], a[252:255], a[220:223], v[176:179]\n" \
"v_mov_b32_e32 v225, v193\n" \
"v_mov_b32_e32 v241, v209\n" \
"v_mul_f32_e32 v100, s70, v100\n" \
"v_mul_f32_e64 v101, s70, v101\n" \
"ds_write_b128 v27, v[92:95] offset:9216\n" \
"v_mul_f32_e32 v108, s70, v108\n" \
"v_mul_f32_e32 v109, s70, v109\n" \
"v_pk_mul_f32 v[116:117], v[116:117], s[70:71]\n" \
"v_pk_mul_f32 v[124:125], v[124:125], s[70:71]\n" \
"v_pk_mul_f32 v[132:133], v[132:133], s[70:71]\n" \
"v_pk_mul_f32 v[140:141], v[140:141], s[70:71]\n" \
"v_pk_mul_f32 v[148:149], v[148:149], s[70:71]\n" \
"v_pk_mul_f32 v[156:157], v[156:157], s[70:71]\n" \
"v_permlane32_swap_b32_e32 v193, v225\n" \
"v_permlane32_swap_b32_e32 v209, v241\n" \
"v_mul_f32_e32 v102, s70, v102\n" \
"v_mul_f32_e32 v103, s70, v103\n" \
C_"v100, v100, v101\n" \
"v_mul_f32_e32 v110, s70, v110\n" \
"v_mul_f32_e32 v111, s70, v111\n" \
C_"v108, v108, v109\n" \
"v_pk_mul_f32 v[118:119], v[118:119], s[70:71]\n" \
C_"v116, v116, v117\n" \
"v_pk_mul_f32 v[126:127], v[126:127], s[70:71]\n" \
C_"v124, v124, v125\n" \
"v_pk_mul_f32 v[134:135], v[134:135], s[70:71]\n" \
C_"v132, v132, v133\n" \
"v_pk_mul_f32 v[142:143], v[142:143], s[70:71]\n" \
C_"v140, v140, v141\n" \
"v_pk_mul_f32 v[150:151], v[150:151], s[70:71]\n" \
C_"v148, v148, v149\n" \
"v_pk_mul_f32 v[158:159], v[158:159], s[70:71]\n" \
C_"v156, v156, v157\n" \
"v_fma_f32 v192, v192, s72, -v193\n" \
"v_fma_f32 v194, v194, s72, -v193\n" \
"v_fma_f32 v208, v208, s72, -v209\n" \
"v_fma_f32 v210, v210, s72, -v209\n" \
"v_mul_f32_e32 v104, s70, v104\n" \
"v_mul_f32_e32 v105, s70, v105\n" \
"v_mul_f32_e32 v106, s70, v106\n" \
"v_mul_f32_e32 v107, s70, v107\n" \
C_"v101, v102, v103\n" \
"v_mul_f32_e32 v112, s70, v112\n" \
"v_mul_f32_e32 v113, s70, v113\n" \
"v_mul_f32_e32 v114, s70, v114\n" \
"v_mul_f32_e32 v115, s70, v115\n" \
C_"v109, v110, v111\n" \
"v_pk_mul_f32 v[120:121], v[120:121], s[70:71]\n" \
"v_pk_mul_f32 v[122:123], v[122:123], s[70:71]\n" \
C_"v117, v118, v119\n" \
"v_pk_mul_f32 v[128:129], v[128:129], s[70:71]\n" \
"v_pk_mul_f32 v[130:131], v[130:131], s[70:71]\n" \
C_"v125, v126, v127\n" \
"v_pk_mul_f32 v[136:137], v[136:137], s[70:71]\n" \
"v_pk_mul_f32 v[138:139], v[138:139], s[70:71]\n" \
C_"v133, v134, v135\n" \
"v_pk_mul_f32 v[144:145], v[144:145], s[70:71]\n" \
"v_pk_mul_f32 v[146:147], v[146:147], s[70:71]\n" \
C_"v141, v142, v143\n" \
"v_pk_mul_f32 v[152:153], v[152:153], s[70:71]\n" \
"v_pk_mul_f32 v[154:155], v[154:155], s[70:71]\n" \
C_"v149, v150, v151\n" \
"v_pk_mul_f32 v[160:161], v[160:161], s[70:71]\n" \
"v_pk_mul_f32 v[162:163], v[162:163], s[70:71]\n" \
C_"v157, v158, v159\n" \
"v_pk_mul_f32 v[176:177], v[176:177], s[70:71]\n" \
"v_pk_mul_f32 v[178:179], v[178:179], s[70:71]\n" \
E_"v192, v192\n" \
E_"v194, v194\n" \
E_"v208, v208\n" \
E_"v210, v210\n" \
"v_fma_f32 v224, v224, s72, -v225\n" \
"v_fma_f32 v226, v226, s72, -v225\n" \
M_"v[172:175], a[184:187], a[156:159], v[172:175]\n" \
C_"v102, v104, v105\n" \
C_"v103, v106, v107\n" \
C_"v110, v112, v113\n" \
C_"v111, v114, v115\n" \
C_"v118, v120, v121\n" \
C_"v119, v122, v123\n" \
C_"v126, v128, v129\n" \
C_"v127, v130, v131\n" \
C_"v134, v136, v137\n" \
C_"v135, v138, v139\n" \
C_"v142, v144, v145\n" \
C_"v143, v146, v147\n" \
C_"v150, v152, v153\n" \
C_"v151, v154, v155\n" \
C_"v158, v160, v161\n" \
C_"v159, v162, v163\n" \
E_"v224, v224\n" \
E_"v226, v226\n" \
"v_fma_f32 v240, v240, s72, -v241\n" \
"v_fma_f32 v242, v242, s72, -v241\n" \
"ds_write_b128 v27, v[100:103] offset:12288\n" \
C_"v192, v192, v194\n" \
C_"v208, v208, v210\n" \
E_"v240, v240\n" \
E_"v242, v242\n" \
C_"v68, v48, v49\n" \
C_"v69, v50, v51\n" \
C_"v70, v196, v197\n" \
C_"v71, v198, v199\n" \
"ds_write_b128 v27, v[108:111] offset:13312\n" \
C_"v72, v212, v213\n" \
C_"v73, v214, v215\n" \
"v_perm_b32 v184, v192, v192, v40\n" \
"v_perm_b32 v185, v192, v192, v41\n" \
"v_perm_b32 v186, v192, v192, v42\n" \
"v_perm_b32 v187, v192, v192, v43\n" \
"v_perm_b32 v200, v208, v208, v40\n" \
"v_perm_b32 v201, v208, v208, v41\n" \
"v_perm_b32 v202, v208, v208, v42\n" \
"v_perm_b32 v203, v208, v208, v43\n" \
"ds_write_b128 v21, v[116:119] offset:16384\n" \
C_"v224, v224, v226\n" \
C_"v74, v228, v229\n" \
C_"v75, v230, v231\n" \
M_"v[32:35], v[68:71], v[184:187], 0\n" \
M_"v[36:39], v[70:73], v[200:203], 0\n" \
"v_perm_b32 v216, v224, v224, v40\n" \
"v_perm_b32 v217, v224, v224, v41\n" \
"ds_write_b128 v21, v[124:127] offset:17408\n" \
"v_perm_b32 v218, v224, v224, v42\n" \
"v_perm_b32 v219, v224, v224, v43\n" \
C_"v240, v240, v242\n" \
M_"v[172:175], a[248:251], a[220:223], v[172:175]\n" \
C_"v76, v244, v245\n" \
C_"v77, v246, v247\n" \
M_"v[248:251], v[72:75], v[216:219], 0\n" \
"v_perm_b32 v232, v240, v240, v40\n" \
"v_perm_b32 v233, v240, v240, v41\n" \
"ds_write_b128 v21, v[132:135] offset:20480\n" \
"v_perm_b32 v234, v240, v240, v42\n" \
"v_perm_b32 v235, v240, v240, v43\n" \
"v_pk_mul_f32 v[172:173], v[172:173], s[70:71]\n" \
"v_pk_mul_f32 v[174:175], v[174:175], s[70:71]\n" \
C_"v172, v172, v173\n" \
C_"v173, v174, v175\n" \
C_"v174, v176, v177\n" \
C_"v175, v178, v179\n" \
M_"v[64:67], v[74:77], v[232:235], 0\n" \
"ds_write_b128 v21, v[140:143] offset:21504\n" \
M_"v[168:171], a[180:183], a[156:159], v[168:171]\n" \
C_"v52, v252, v253\n" \
C_"v53, v254, v255\n" \
M_"v[168:171], a[244:247], a[220:223], v[168:171]\n" \
"v_exp_f32_e64 v45, v45\n" \
M_"v[192:195], v[52:55], v[184:187], 0\n" \
"ds_write_b128 v21, v[148:151] offset:24576\n" \
M_"v[208:211], v[54:57], v[200:203], 0\n" \
M_"v[224:227], v[56:59], v[216:219], 0\n" \
M_"v[240:243], v[58:61], v[232:235], 0\n" \
"ds_write_b128 v21, v[156:159] offset:25600\n" \
"v_mov_b32_dpp v34, v38 quad_perm:[0,1,2,3] row_mask:0x2 bank_mask:0xf\n" \
"v_pk_mul_f32 v[168:169], v[168:169], s[70:71]\n" \
"v_pk_mul_f32 v[170:171], v[170:171], s[70:71]\n" \
"v_mov_b32_dpp v34, v250 quad_perm:[0,1,2,3] row_mask:0x4 bank_mask:0xf\n" \
"v_add_f32_e64 v45, 1.0, v45\n" \
M_"v[164:167], a[176:179], a[156:159], v[164:167]\n" \
"v_mov_b32_dpp v34, v66 quad_perm:[0,1,2,3] row_mask:0x8 bank_mask:0xf\n" \
"v_mov_b32_dpp v32, v36 quad_perm:[0,1,2,3] row_mask:0x2 bank_mask:0xf\n" \
"v_rcp_f32_e64 v45, v45\n" \
"ds_write_b128 v21, v[172:175] offset:29696\n" \
"v_mov_b32_dpp v33, v37 quad_perm:[0,1,2,3] row_mask:0x2 bank_mask:0xf\n" \
"v_rcp_f32_e64 v34, v34\n" \
"v_mov_b32_dpp v32, v248 quad_perm:[0,1,2,3] row_mask:0x4 bank_mask:0xf\n" \
"v_mov_b32_dpp v33, v249 quad_perm:[0,1,2,3] row_mask:0x4 bank_mask:0xf\n" \
"v_mul_f32_e64 v34, v45, v34\n" \
"v_mov_b32_dpp v32, v64 quad_perm:[0,1,2,3] row_mask:0x8 bank_mask:0xf\n" \
"v_mov_b32_dpp v33, v65 quad_perm:[0,1,2,3] row_mask:0x8 bank_mask:0xf\n" \
"v_mul_f32_e32 v32, v34, v32\n" \
"v_mul_f32_e32 v33, v34, v33\n" \
"v_mov_b32_e64 v38, v34\n" \
M_"v[164:167], a[240:243], a[220:223], v[164:167]\n" \
"v_sub_f32_e32 v44, 1.0, v45\n" \
"v_permlane16_swap_b32_e32 v34, v38\n" \
"v_mov_b32_e32 v250, v34\n" \
"v_mov_b32_e32 v66, v38\n" \
C_"v32, v32, v33\n" \
"v_permlane32_swap_b32_e32 v34, v250\n" \
"v_permlane32_swap_b32_e32 v38, v66\n" \
"v_mul_f32_e64 v192, v34, v192\n" \
"v_pk_mul_f32 v[164:165], v[164:165], s[70:71]\n" \
"v_pk_mul_f32 v[166:167], v[166:167], s[70:71]\n" \
C_"v164, v164, v165\n" \
C_"v165, v166, v167\n" \
C_"v166, v168, v169\n" \
C_"v167, v170, v171\n" \
"ds_write_b128 v21, v[164:167] offset:28672\n" \
"v_mul_f32_e32 v193, v34, v193\n" \
"v_mul_f32_e32 v208, v38, v208\n" \
"v_mul_f32_e32 v209, v38, v209\n" \
"v_mul_f32_e32 v224, v250, v224\n" \
"v_mul_f32_e32 v225, v250, v225\n" \
"v_mul_f32_e32 v240, v66, v240\n" \
"v_mul_f32_e32 v241, v66, v241\n" \
"v_mul_f32_e32 v194, v34, v194\n" \
"v_mul_f32_e64 v195, v34, v195\n" \
C_"v192, v192, v193\n" \
"v_mul_f32_e32 v210, v38, v210\n" \
"v_mul_f32_e32 v211, v38, v211\n" \
C_"v208, v208, v209\n" \
"v_mul_f32_e32 v226, v250, v226\n" \
"v_mul_f32_e32 v227, v250, v227\n" \
C_"v224, v224, v225\n" \
"v_mul_f32_e32 v242, v66, v242\n" \
"v_mul_f32_e32 v243, v66, v243\n" \
C_"v240, v240, v241\n" \
C_"v193, v194, v195\n" \
"s_add_u32 s84, s48, 0x0\n" \
"s_nop 0\n" \
C_"v209, v210, v211\n" \
"s_add_u32 s85, s48, 0x24800\n" \
C_"v225, v226, v227\n" \
"s_add_u32 s86, s48, 0x49000\n" \
C_"v241, v242, v243\n" \
"s_add_u32 s87, s48, 0x6d800\n" \
"ds_write_b32 v26, v44\n" \
"buffer_store_dword v32, v25, s[80:83], s49 offen sc1\n" \
"buffer_store_dwordx2 v[192:193], v24, s[80:83], s84 offen sc1\n" \
"buffer_store_dwordx2 v[208:209], v24, s[80:83], s85 offen sc1\n" \
"buffer_store_dwordx2 v[224:225], v24, s[80:83], s86 offen sc1\n" \
"buffer_store_dwordx2 v[240:241], v24, s[80:83], s87 offen sc1\n" \
"s_branch L_end_%=\n" \
"L_poll60_%=:\n" \
"s_mov_b32 s84, 0\n" \
"L_pl60_%=:\n" \
"s_sleep 1\n" \
"v_bfe_u32 v44, v20, 2, 5\n" \
"buffer_load_dword v44, v44, s[80:83], s38 offen offset:128 sc1\n" \
"s_waitcnt vmcnt(0)\n" \
"v_cmp_ne_u32_e32 vcc, s62, v44\n" \
"s_add_u32 s84, s84, 1\n" \
"s_nop 3\n" \
"s_cbranch_vccz L_pollr60_%=\n" \
"s_cmp_gt_u32 s84, 0x400000\n" \
"s_cbranch_scc0 L_pl60_%=\n" \
"s_branch L_pollr60_%=\n" \
"L_poll160_%=:\n" \
"s_mov_b32 s84, 0\n" \
"L_pl160_%=:\n" \
"s_sleep 1\n" \
"v_bfe_u32 v44, v20, 2, 5\n" \
"buffer_load_dword v44, v44, s[80:83], s38 offen offset:0 sc1\n" \
"s_waitcnt vmcnt(0)\n" \
"v_cmp_ne_u32_e32 vcc, s62, v44\n" \
"s_add_u32 s84, s84, 1\n" \
"s_nop 3\n" \
"s_cbranch_vccz L_pollr160_%=\n" \
"s_cmp_gt_u32 s84, 0x400000\n" \
"s_cbranch_scc0 L_pl160_%=\n" \
"s_branch L_pollr160_%=\n" \
".fill (L_pb + 42460 - .) / 4, 4, 0xBF800000\n" \
"L_w1_%=:\n" \
"s_waitcnt vmcnt(13)\n" \
"s_barrier\n" \
X_"a[160:161], v32 offset:0\n" \
X_"a[162:163], v32 offset:1024\n" \
R_"a[128:131], v28 offset:16384\n" \
X_"a[224:225], v32 offset:8192\n" \
X_"a[226:227], v32 offset:9216\n" \
R_"a[192:195], v29 offset:16384\n" \
R_"a[132:135], v28 offset:18432\n" \
R_"a[196:199], v29 offset:18432\n" \
X_"a[164:165], v33 offset:0\n" \
X_"a[166:167], v33 offset:1024\n" \
X_"a[228:229], v33 offset:8192\n" \
X_"a[230:231], v33 offset:9216\n" \
R_"a[136:139], v28 offset:20480\n" \
R_"a[200:203], v29 offset:20480\n" \
"s_waitcnt lgkmcnt(11)\n" \
M_"a[0:3], a[128:131], a[160:163], 0\n" \
X_"a[168:169], v34 offset:0\n" \
"s_add_u32 m0, s31, 0xc000\n" \
"s_mov_b32 s84, 0x80000\n" \
"s_waitcnt lgkmcnt(9)\n" \
M_"a[0:3], a[192:195], a[224:227], a[0:3]\n" \
X_"a[170:171], v34 offset:1024\n" \
B_"v26, s[72:75], s84 offen offset:0 lds\n" \
"s_waitcnt lgkmcnt(9)\n" \
M_"a[16:19], a[132:135], a[160:163], 0\n" \
X_"a[232:233], v34 offset:8192\n" \
"s_lshr_b32 s4, s2, 1\n" \
"s_waitcnt lgkmcnt(9)\n" \
M_"a[16:19], a[196:199], a[224:227], a[16:19]\n" \
X_"a[234:235], v34 offset:9216\n" \
"s_addk_i32 s84, 0x7c00\n" \
"s_waitcnt lgkmcnt(8)\n" \
M_"a[20:23], a[132:135], a[164:167], 0\n" \
R_"a[140:143], v28 offset:22528\n" \
B_"v26, s[72:75], s84 offen offset:1024 lds\n" \
"s_waitcnt lgkmcnt(7)\n" \
M_"a[20:23], a[196:199], a[228:231], a[20:23]\n" \
R_"a[204:207], v29 offset:22528\n" \
"s_and_b32 s4, s4, 0x7c\n" \
M_"a[4:7], a[128:131], a[164:167], 0\n" \
X_"a[172:173], v35 offset:0\n" \
"s_addk_i32 s84, 0x7c00\n" \
M_"a[4:7], a[192:195], a[228:231], a[4:7]\n" \
X_"a[174:175], v35 offset:1024\n" \
B_"v27, s[72:75], s84 offen offset:2048 lds\n" \
"s_waitcnt lgkmcnt(9)\n" \
M_"a[32:35], a[136:139], a[160:163], 0\n" \
X_"a[236:237], v35 offset:8192\n" \
"v_mov_b32_e32 v213, s13\n" \
"s_waitcnt lgkmcnt(9)\n" \
M_"a[32:35], a[200:203], a[224:227], a[32:35]\n" \
X_"a[238:239], v35 offset:9216\n" \
"s_addk_i32 s84, 0x7c00\n" \
M_"a[36:39], a[136:139], a[164:167], 0\n" \
R_"a[144:147], v28 offset:0\n" \
B_"v27, s[72:75], s84 offen offset:3072 lds\n" \
M_"a[36:39], a[200:203], a[228:231], a[36:39]\n" \
R_"a[208:211], v29 offset:0\n" \
"s_or_b32 s4, s4, s3\n" \
"s_waitcnt lgkmcnt(10)\n" \
M_"a[40:43], a[136:139], a[168:171], 0\n" \
R_"a[148:151], v28 offset:2048\n" \
"v_lshrrev_b32_e32 v212, 4, v20\n" \
"s_waitcnt lgkmcnt(9)\n" \
M_"a[40:43], a[200:203], a[232:235], a[40:43]\n" \
R_"a[212:215], v29 offset:2048\n" \
"v_mad_u64_u32 v[214:215], s[4:5], s4, 36, v[212:213]\n" \
M_"a[24:27], a[132:135], a[168:171], 0\n" \
R_"a[152:155], v28 offset:4096\n" \
"v_mov_b32_e64 v217, s9\n" \
M_"a[24:27], a[196:199], a[232:235], a[24:27]\n" \
R_"a[216:219], v29 offset:4096\n" \
"s_movk_i32 s4, 0x240\n" \
M_"a[8:11], a[128:131], a[168:171], 0\n" \
R_"a[156:159], v28 offset:6144\n" \
"v_cmp_gt_u32_e32 vcc, s4, v20\n" \
M_"a[8:11], a[192:195], a[232:235], a[8:11]\n" \
R_"a[220:223], v29 offset:6144\n" \
"s_mov_b32 m0, s32\n" \
"s_waitcnt lgkmcnt(13)\n" \
M_"a[48:51], a[140:143], a[160:163], 0\n" \
"s_mov_b32 exec_lo, 0xff000000\n" \
"s_mov_b32 exec_hi, 0x0\n" \
"global_load_lds_dword v[184:185], off\n" \
"s_mov_b64 exec, -1\n" \
"s_waitcnt lgkmcnt(12)\n" \
M_"a[48:51], a[204:207], a[224:227], a[48:51]\n" \
X_"a[176:177], v36 offset:0\n" \
"s_lshl_b32 s4, s2, 3\n" \
M_"a[52:55], a[140:143], a[164:167], 0\n" \
X_"a[178:179], v36 offset:1024\n" \
"s_and_b32 s4, s4, 56\n" \
M_"a[52:55], a[204:207], a[228:231], a[52:55]\n" \
"s_mov_b32 m0, s32\n" \
X_"a[240:241], v36 offset:8192\n" \
M_"a[56:59], a[140:143], a[168:171], 0\n" \
"s_mov_b32 exec_lo, 0xffff\n" \
"s_mov_b32 exec_hi, 0x0\n" \
"global_load_lds_dword v[186:187], off\n" \
"s_mov_b64 exec, -1\n" \
M_"a[56:59], a[204:207], a[232:235], a[56:59]\n" \
X_"a[242:243], v36 offset:9216\n" \
"v_cndmask_b32_e32 v217, v217, v213, vcc\n" \
"s_waitcnt lgkmcnt(14)\n" \
M_"a[60:63], a[140:143], a[172:175], 0\n" \
X_"a[180:181], v37 offset:0\n" \
"v_lshlrev_b32_e32 v213, 3, v214\n" \
"s_waitcnt lgkmcnt(13)\n" \
M_"a[60:63], a[204:207], a[236:239], a[60:63]\n" \
X_"a[182:183], v37 offset:1024\n" \
"v_and_b32_e32 v213, 0x1ffc0, v213\n" \
M_"a[44:47], a[136:139], a[172:175], 0\n" \
X_"a[244:245], v37 offset:8192\n" \
"v_and_b32_e64 v214, 7, v214\n" \
M_"a[44:47], a[200:203], a[236:239], a[44:47]\n" \
X_"a[246:247], v37 offset:9216\n" \
"v_or3_b32 v213, v214, s4, v213\n" \
M_"a[28:31], a[132:135], a[172:175], 0\n" \
X_"a[184:185], v38 offset:0\n" \
"s_lshl_b32 s4, s2, 2\n" \
M_"a[28:31], a[196:199], a[236:239], a[28:31]\n" \
X_"a[186:187], v38 offset:1024\n" \
"s_or_b32 s4, s4, s3\n" \
M_"a[12:15], a[128:131], a[172:175], 0\n" \
"s_mov_b64 exec, 0\n" \
"s_swappc_b64 s[88:89], s[86:87]\n" \
"s_mov_b64 exec, -1\n" \
"s_add_u32 s86, s86, 0x2000\n" \
"s_addc_u32 s87, s87, 0\n" \
M_"a[12:15], a[192:195], a[236:239], a[12:15]\n" \
"s_mul_i32 s4, s4, 18\n" \
"v_min_u32_e32 v212, 53, v212\n" \
"s_waitcnt lgkmcnt(15)\n" \
M_"a[76:79], a[144:147], a[172:175], 0\n" \
"s_mov_b32 m0, s32\n" \
X_"a[248:249], v38 offset:8192\n" \
M_"a[76:79], a[208:211], a[236:239], a[76:79]\n" \
"s_mov_b64 exec, 0xff\n" \
"buffer_load_dword v49, s[80:83], 0 offen lds\n" \
"s_mov_b64 exec, -1\n" \
M_"a[72:75], a[144:147], a[168:171], 0\n" \
"s_sub_i32 s4, s4, 36\n" \
"v_add_u32_e64 v212, s4, v212\n" \
M_"a[72:75], a[208:211], a[232:235], a[72:75]\n" \
X_"a[250:251], v38 offset:9216\n" \
"v_mov_b32_e64 v215, s12\n" \
M_"a[68:71], a[144:147], a[164:167], 0\n" \
"s_mov_b32 s84, 0x5bc0000\n" \
"buffer_load_dword v180, off, s[80:83], s84 sc1\n" \
M_"a[68:71], a[208:211], a[228:231], a[68:71]\n" \
"v_mov_b32_e32 v216, s8\n" \
"v_cndmask_b32_e32 v212, v212, v213, vcc\n" \
M_"a[64:67], a[144:147], a[160:163], 0\n" \
"v_cndmask_b32_e32 v216, v216, v215, vcc\n" \
"v_lshlrev_b32_e32 v212, 7, v212\n" \
M_"a[64:67], a[208:211], a[224:227], a[64:67]\n" \
X_"a[188:189], v39 offset:0\n" \
"v_mov_b32_e64 v213, 0\n" \
M_"a[80:83], a[148:151], a[160:163], 0\n" \
"v_lshl_add_u64 v[188:189], v[216:217], 0, v[212:213]\n" \
"s_movk_i32 s5, 0xd80\n" \
"s_waitcnt lgkmcnt(15)\n" \
M_"a[80:83], a[212:215], a[224:227], a[80:83]\n" \
"s_lshr_b32 s6, s2, 5\n" \
"v_lshrrev_b32_e32 v212, 4, v20\n" \
M_"a[84:87], a[148:151], a[164:167], 0\n" \
X_"a[190:191], v39 offset:1024\n" \
"v_lshl_or_b32 v215, s3, 6, v212\n" \
M_"a[84:87], a[212:215], a[228:231], a[84:87]\n" \
"v_mul_u32_u24_e32 v216, 0x2aab, v215\n" \
"v_lshrrev_b32_e64 v216, 16, v216\n" \
M_"a[88:91], a[148:151], a[168:171], 0\n" \
"v_mad_i32_i24 v217, v216, -6, v215\n" \
"v_add_u32_e32 v218, 0xfffffe50, v215\n" \
M_"a[88:91], a[212:215], a[232:235], a[88:91]\n" \
X_"a[252:253], v39 offset:8192\n" \
"s_mul_i32 s35, s6, 0x48\n" \
M_"a[92:95], a[148:151], a[172:175], 0\n" \
"v_lshrrev_b32_e32 v219, 1, v217\n" \
"v_add_u32_e32 v218, s35, v216\n" \
M_"a[92:95], a[212:215], a[236:239], a[92:95]\n" \
"v_mul_u32_u24_e32 v219, 0x480, v219\n" \
"v_and_b32_e64 v214, 16, v20\n" \
M_"a[108:111], a[152:155], a[172:175], 0\n" \
X_"a[254:255], v39 offset:9216\n" \
"v_mad_u32_u24 v219, v218, s5, v219\n" \
M_"a[108:111], a[216:219], a[236:239], a[108:111]\n" \
"v_or_b32_e32 v192, v219, v214\n" \
"v_mov_b32_e32 v220, 2.0\n" \
M_"a[104:107], a[152:155], a[168:171], 0\n" \
"s_mov_b64 exec, 0\n" \
"s_swappc_b64 s[88:89], s[86:87]\n" \
"s_mov_b64 exec, -1\n" \
"s_andn2_b32 s85, 2, s3\n" \
"s_lshl_b32 s85, s85, 12\n" \
"s_add_u32 s86, s86, s85\n" \
"s_addc_u32 s87, s87, 0\n" \
M_"a[104:107], a[216:219], a[232:235], a[104:107]\n" \
"v_mov_b32_e32 v222, 7\n" \
"v_lshlrev_b32_e64 v220, 7, v218\n" \
M_"a[100:103], a[152:155], a[164:167], 0\n" \
"v_lshrrev_b32_e32 v218, 1, v218\n" \
"v_and_b32_e32 v218, 4, v218\n" \
M_"a[100:103], a[216:219], a[228:231], a[100:103]\n" \
"v_and_b32_e64 v216, 2, v216\n" \
"s_mul_i32 s7, s6, 0x2400\n" \
M_"a[96:99], a[152:155], a[160:163], 0\n" \
"s_sub_i32 s34, 0x22000, s7\n" \
"v_bitop3_b32 v216, v217, v218, v216 bitop3:0x1e\n" \
M_"a[96:99], a[216:219], a[224:227], a[96:99]\n" \
"v_or_b32_e32 v213, 0x24400, v20\n" \
"v_lshlrev_b32_e32 v216, 4, v216\n" \
"s_waitcnt lgkmcnt(15)\n" \
M_"a[112:115], a[156:159], a[160:163], 0\n" \
"v_add3_u32 v196, v220, s34, v216\n" \
"v_mov_b32_e64 v216, v213\n" \
M_"a[112:115], a[220:223], a[224:227], a[112:115]\n" \
"s_movk_i32 s36, 0x1b0\n" \
"v_or_b32_e32 v216, 0x100, v215\n" \
M_"a[116:119], a[156:159], a[164:167], 0\n" \
"v_mul_u32_u24_e32 v217, 0x2aab, v216\n" \
"v_add_u32_e32 v215, 0xffffff50, v215\n" \
M_"a[116:119], a[220:223], a[228:231], a[116:119]\n" \
"v_lshrrev_b32_e32 v217, 16, v217\n" \
"v_mov_b32_e64 v220, v215\n" \
M_"a[120:123], a[156:159], a[168:171], 0\n" \
"v_mad_i32_i24 v218, v217, -6, v216\n" \
"v_cmp_gt_u32_e64 vcc, s36, v216\n" \
M_"a[120:123], a[220:223], a[232:235], a[120:123]\n" \
"v_mov_b32_e32 v221, 7\n" \
"v_lshrrev_b32_e32 v222, 1, v218\n" \
M_"a[124:127], a[156:159], a[172:175], 0\n" \
"v_cndmask_b32_e32 v217, v220, v217, vcc\n" \
"s_movk_i32 s4, 0x48\n" \
"s_waitcnt vmcnt(4) lgkmcnt(0)\n" \
M_"a[124:127], a[220:223], a[236:239], a[124:127]\n" \
"s_barrier\n" \
M_"v[52:55], a[128:131], a[176:179], 0\n" \
"s_add_u32 m0, s30, 0x0\n" \
"s_movk_i32 s84, 0x100\n" \
M_"v[52:55], a[192:195], a[240:243], v[52:55]\n" \
B_"v22, s[64:67], s84 offen offset:0 lds\n" \
M_"v[56:59], a[128:131], a[180:183], 0\n" \
X_"a[160:161], v32 offset:32768\n" \
"v_add_u32_e64 v220, s35, v217\n" \
M_"v[56:59], a[192:195], a[244:247], v[56:59]\n" \
X_"a[162:163], v32 offset:33792\n" \
"v_mul_u32_u24_e32 v222, 0x480, v222\n" \
M_"v[60:63], a[128:131], a[184:187], 0\n" \
"s_movk_i32 s84, 0x1d00\n" \
M_"v[60:63], a[192:195], a[248:251], v[60:63]\n" \
B_"v23, s[64:67], s84 offen offset:1024 lds\n" \
M_"v[64:67], a[128:131], a[188:191], 0\n" \
R_"a[128:131], v28 offset:49152\n" \
"v_mad_u32_u24 v222, v220, s5, v222\n" \
M_"v[64:67], a[192:195], a[252:255], v[64:67]\n" \
R_"a[192:195], v29 offset:49152\n" \
"v_cmp_gt_u32_e64 s[4:5], s4, v215\n" \
M_"v[80:83], a[132:135], a[188:191], 0\n" \
"s_movk_i32 s84, 0x3900\n" \
M_"v[80:83], a[196:199], a[252:255], v[80:83]\n" \
B_"v22, s[64:67], s84 offen offset:2048 lds\n" \
M_"v[76:79], a[132:135], a[184:187], 0\n" \
X_"a[224:225], v32 offset:40960\n" \
"v_cndmask_b32_e64 v215, v221, v218, vcc\n" \
M_"v[76:79], a[196:199], a[248:251], v[76:79]\n" \
X_"a[226:227], v32 offset:41984\n" \
"v_lshrrev_b32_e64 v218, 1, v220\n" \
M_"v[72:75], a[132:135], a[180:183], 0\n" \
"s_movk_i32 s84, 0x5500\n" \
M_"v[72:75], a[196:199], a[244:247], v[72:75]\n" \
B_"v23, s[64:67], s84 offen offset:3072 lds\n" \
M_"v[68:71], a[132:135], a[176:179], 0\n" \
R_"a[132:135], v28 offset:51200\n" \
"v_or_b32_e32 v214, v222, v214\n" \
M_"v[68:71], a[196:199], a[240:243], v[68:71]\n" \
R_"a[196:199], v29 offset:51200\n" \
"v_mov_b32_e64 v222, 2.0\n" \
M_"v[84:87], a[136:139], a[176:179], 0\n" \
"s_add_u32 m0, s31, 0x0\n" \
"s_mov_b32 s84, 0x100000\n" \
M_"v[84:87], a[200:203], a[240:243], v[84:87]\n" \
B_"v26, s[68:71], s84 offen offset:0 lds\n" \
M_"v[88:91], a[136:139], a[180:183], 0\n" \
X_"a[164:165], v33 offset:32768\n" \
"v_and_b32_e32 v218, 4, v218\n" \
M_"v[88:91], a[200:203], a[244:247], v[88:91]\n" \
X_"a[166:167], v33 offset:33792\n" \
"v_and_b32_e64 v217, 2, v217\n" \
M_"v[92:95], a[136:139], a[184:187], 0\n" \
"s_addk_i32 s84, 0x7c00\n" \
M_"v[92:95], a[200:203], a[248:251], v[92:95]\n" \
B_"v26, s[68:71], s84 offen offset:1024 lds\n" \
M_"v[96:99], a[136:139], a[188:191], 0\n" \
R_"a[136:139], v28 offset:53248\n" \
"v_bitop3_b32 v215, v215, v218, v217 bitop3:0x1e\n" \
M_"v[96:99], a[200:203], a[252:255], v[96:99]\n" \
R_"a[200:203], v29 offset:53248\n" \
"v_lshlrev_b32_e32 v223, 1, v220\n" \
M_"v[112:115], a[140:143], a[188:191], 0\n" \
"s_addk_i32 s84, 0x7c00\n" \
M_"v[112:115], a[204:207], a[252:255], v[112:115]\n" \
B_"v27, s[68:71], s84 offen offset:2048 lds\n" \
M_"v[108:111], a[140:143], a[184:187], 0\n" \
"s_mov_b64 exec, 0\n" \
"s_swappc_b64 s[88:89], s[86:87]\n" \
"s_mov_b64 exec, -1\n" \
M_"v[108:111], a[204:207], a[248:251], v[108:111]\n" \
X_"a[228:229], v33 offset:40960\n" \
"v_mov_b32_e64 v224, 2.0\n" \
M_"v[104:107], a[140:143], a[180:183], 0\n" \
"s_addk_i32 s84, 0x7c00\n" \
M_"v[104:107], a[204:207], a[244:247], v[104:107]\n" \
B_"v27, s[68:71], s84 offen offset:3072 lds\n" \
M_"v[100:103], a[140:143], a[176:179], 0\n" \
R_"a[140:143], v28 offset:55296\n" \
"v_cndmask_b32_e32 v193, v222, v214, vcc\n" \
M_"v[100:103], a[204:207], a[240:243], v[100:103]\n" \
R_"a[204:207], v29 offset:55296\n" \
"v_lshlrev_b32_e64 v214, 7, v220\n" \
M_"v[116:119], a[144:147], a[176:179], 0\n" \
"s_add_u32 m0, s30, 0x1000\n" \
"s_mov_b32 s84, 0x8100\n" \
M_"v[116:119], a[208:211], a[240:243], v[116:119]\n" \
B_"v22, s[64:67], s84 offen offset:0 lds\n" \
M_"v[120:123], a[144:147], a[180:183], 0\n" \
X_"a[230:231], v33 offset:41984\n" \
"v_lshlrev_b32_e64 v215, 4, v215\n" \
M_"v[120:123], a[208:211], a[244:247], v[120:123]\n" \
X_"a[168:169], v34 offset:32768\n" \
"s_mul_i32 s39, s3, 18\n" \
M_"v[124:127], a[144:147], a[184:187], 0\n" \
"s_addk_i32 s84, 0x1c00\n" \
M_"v[124:127], a[208:211], a[248:251], v[124:127]\n" \
B_"v23, s[64:67], s84 offen offset:1024 lds\n" \
".fill (L_pb + 44768 - .) / 4, 4, 0xBF800000\n" \
"L_pp11:\n" \
"s_cbranch_execz L_pret\n" \
M_"v[128:131], a[144:147], a[188:191], 0\n" \
R_"a[144:147], v28 offset:32768\n" \
"v_add3_u32 v214, v214, s34, v215\n" \
M_"v[128:131], a[208:211], a[252:255], v[128:131]\n" \
R_"a[208:211], v29 offset:32768\n" \
"v_cndmask_b32_e64 v195, v224, v223, s[4:5]\n" \
M_"v[144:147], a[148:151], a[188:191], 0\n" \
"s_addk_i32 s84, 0x1c00\n" \
M_"v[144:147], a[212:215], a[252:255], v[144:147]\n" \
B_"v22, s[64:67], s84 offen offset:2048 lds\n" \
M_"v[140:143], a[148:151], a[184:187], 0\n" \
X_"a[170:171], v34 offset:33792\n" \
"s_movk_i32 s4, 0x1f8\n" \
M_"v[140:143], a[212:215], a[248:251], v[140:143]\n" \
X_"a[232:233], v34 offset:40960\n" \
"v_mov_b32_e32 v215, v213\n" \
M_"v[136:139], a[148:151], a[180:183], 0\n" \
"s_addk_i32 s84, 0x1c00\n" \
M_"v[136:139], a[212:215], a[244:247], v[136:139]\n" \
B_"v23, s[64:67], s84 offen offset:3072 lds\n" \
M_"v[132:135], a[148:151], a[176:179], 0\n" \
R_"a[148:151], v28 offset:34816\n" \
"s_add_i32 s35, s35, s39\n" \
M_"v[132:135], a[212:215], a[240:243], v[132:135]\n" \
R_"a[212:215], v29 offset:34816\n" \
"v_cmp_gt_u32_e64 vcc, s4, v216\n" \
M_"v[148:151], a[152:155], a[176:179], 0\n" \
"s_add_u32 m0, s31, 0x4000\n" \
"s_mov_b32 s84, 0x100000\n" \
M_"v[148:151], a[216:219], a[240:243], v[148:151]\n" \
B_"v26, s[72:75], s84 offen offset:0 lds\n" \
M_"v[152:155], a[152:155], a[180:183], 0\n" \
X_"a[234:235], v34 offset:41984\n" \
"v_add_u32_e64 v212, s35, v212\n" \
M_"v[152:155], a[216:219], a[244:247], v[152:155]\n" \
X_"a[172:173], v35 offset:32768\n" \
"v_mov_b32_e64 v219, 2.0\n" \
M_"v[156:159], a[152:155], a[184:187], 0\n" \
"s_addk_i32 s84, 0x7c00\n" \
M_"v[156:159], a[216:219], a[248:251], v[156:159]\n" \
B_"v26, s[72:75], s84 offen offset:1024 lds\n" \
M_"v[160:163], a[152:155], a[188:191], 0\n" \
R_"a[152:155], v28 offset:36864\n" \
"v_cndmask_b32_e32 v197, v215, v214, vcc\n" \
M_"v[160:163], a[216:219], a[252:255], v[160:163]\n" \
R_"a[216:219], v29 offset:36864\n" \
"s_movk_i32 s35, 0x120\n" \
M_"v[176:179], a[156:159], a[188:191], 0\n" \
X_"a[174:175], v35 offset:33792\n" \
"v_mul_u32_u24_e32 v214, 0xd80, v212\n" \
M_"v[176:179], a[220:223], a[252:255], v[176:179]\n" \
X_"a[236:237], v35 offset:40960\n" \
"v_cmp_gt_u32_e32 vcc, s35, v20\n" \
M_"v[172:175], a[156:159], a[184:187], 0\n" \
X_"a[238:239], v35 offset:41984\n" \
"v_lshrrev_b32_e64 v215, 1, v212\n" \
M_"v[172:175], a[220:223], a[248:251], v[172:175]\n" \
"s_and_b32 s4, s2, 7\n" \
"v_cndmask_b32_e32 v194, v219, v214, vcc\n" \
M_"v[168:171], a[156:159], a[180:183], 0\n" \
"v_lshlrev_b32_e32 v214, 7, v212\n" \
"v_lshrrev_b32_e32 v212, 2, v212\n" \
M_"v[168:171], a[220:223], a[244:247], v[168:171]\n" \
"v_and_b32_e64 v212, 2, v212\n" \
"v_and_or_b32 v212, v215, 1, v212\n" \
M_"v[164:167], a[156:159], a[176:179], 0\n" \
R_"a[156:159], v28 offset:38912\n" \
"s_lshl_b32 s5, s4, 2\n" \
M_"v[164:167], a[220:223], a[240:243], v[164:167]\n" \
R_"a[220:223], v29 offset:38912\n" \
"s_waitcnt lgkmcnt(15)\n" \
M_"a[0:3], a[128:131], a[160:163], a[0:3]\n" \
"s_addk_i32 s84, 0x7c00\n" \
M_"a[0:3], a[192:195], a[224:227], a[0:3]\n" \
B_"v27, s[72:75], s84 offen offset:2048 lds\n" \
M_"a[4:7], a[128:131], a[164:167], a[4:7]\n" \
X_"a[176:177], v36 offset:32768\n" \
"s_bfe_u32 s38, s2, 0x20003\n" \
M_"a[4:7], a[192:195], a[228:231], a[4:7]\n" \
"s_addk_i32 s84, 0x7c00\n" \
"s_waitcnt lgkmcnt(13)\n" \
M_"a[8:11], a[128:131], a[168:171], a[8:11]\n" \
B_"v27, s[72:75], s84 offen offset:3072 lds\n" \
"s_waitcnt lgkmcnt(9)\n" \
M_"a[8:11], a[192:195], a[232:235], a[8:11]\n" \
X_"a[178:179], v36 offset:33792\n" \
"v_lshlrev_b32_e32 v212, 5, v212\n" \
"s_waitcnt lgkmcnt(6)\n" \
M_"a[12:15], a[128:131], a[172:175], a[12:15]\n" \
X_"a[240:241], v36 offset:40960\n" \
"s_movk_i32 s35, 0x60\n" \
"s_waitcnt lgkmcnt(5)\n" \
M_"a[12:15], a[192:195], a[236:239], a[12:15]\n" \
"s_or_b32 s5, s5, s38\n" \
"v_bitop3_b32 v212, v212, s35, v214 bitop3:0x36\n" \
M_"a[28:31], a[132:135], a[172:175], a[28:31]\n" \
"s_lshr_b32 s36, s2, 3\n" \
"v_add_u32_e64 v212, s34, v212\n" \
M_"a[28:31], a[196:199], a[236:239], a[28:31]\n" \
X_"a[242:243], v36 offset:41984\n" \
"s_mul_i32 s34, s5, 0x12000\n" \
M_"a[24:27], a[132:135], a[168:171], a[24:27]\n" \
"s_or_b32 s40, s34, 0x6000000\n" \
"s_mul_i32 s34, s36, 0x90000\n" \
M_"a[24:27], a[196:199], a[232:235], a[24:27]\n" \
"s_lshl_b32 s36, s3, 14\n" \
"s_add_i32 s34, s34, s36\n" \
M_"a[20:23], a[132:135], a[164:167], a[20:23]\n" \
"s_lshl_b32 s38, s5, 8\n" \
"s_add_i32 s41, s34, 0x4240000\n" \
M_"a[20:23], a[196:199], a[228:231], a[20:23]\n" \
X_"a[180:181], v37 offset:32768\n" \
"s_mul_i32 s34, s5, 36\n" \
M_"a[16:19], a[132:135], a[160:163], a[16:19]\n" \
"s_add_i32 s5, s7, s40\n" \
"s_addk_i32 s5, 0x2000\n" \
M_"a[16:19], a[196:199], a[224:227], a[16:19]\n" \
"s_mov_b32 s37, 2.0\n" \
"s_or_b32 s38, s38, 0x5be8000\n" \
M_"a[32:35], a[136:139], a[160:163], a[32:35]\n" \
"s_mov_b32 m0, s32\n" \
X_"a[182:183], v37 offset:33792\n" \
M_"a[32:35], a[200:203], a[224:227], a[32:35]\n" \
"s_mov_b32 exec_lo, 0x0\n" \
"s_mov_b32 exec_hi, 0xff\n" \
"global_load_lds_dword v[184:185], off\n" \
"s_mov_b64 exec, -1\n" \
M_"a[36:39], a[136:139], a[164:167], a[36:39]\n" \
"s_cmp_lg_u32 s3, 0\n" \
"s_cselect_b32 s36, s37, s5\n" \
M_"a[36:39], a[200:203], a[228:231], a[36:39]\n" \
X_"a[244:245], v37 offset:40960\n" \
"s_lshl_b32 s5, s6, 2\n" \
M_"a[40:43], a[136:139], a[168:171], a[40:43]\n" \
"s_or_b32 s5, s38, s5\n" \
"s_lshl_b32 s35, s3, 10\n" \
M_"a[40:43], a[200:203], a[232:235], a[40:43]\n" \
"s_mov_b32 m0, s32\n" \
M_"a[44:47], a[136:139], a[172:175], a[44:47]\n" \
"s_mov_b32 exec_lo, 0xffff0000\n" \
"s_mov_b32 exec_hi, 0x0\n" \
"global_load_lds_dword v[186:187], off\n" \
"s_mov_b64 exec, -1\n" \
M_"a[44:47], a[200:203], a[236:239], a[44:47]\n" \
X_"a[246:247], v37 offset:41984\n" \
M_"a[60:63], a[140:143], a[172:175], a[60:63]\n" \
"s_cmpk_lt_u32 s3, 3\n" \
"s_cselect_b32 s37, s37, s5\n" \
M_"a[60:63], a[204:207], a[236:239], a[60:63]\n" \
"s_lshl_b32 s5, s4, 16\n" \
"s_lshl_b32 s4, s4, 11\n" \
M_"a[56:59], a[140:143], a[168:171], a[56:59]\n" \
X_"a[184:185], v38 offset:32768\n" \
"v_cndmask_b32_e64 v198, v213, v212, vcc\n" \
M_"a[56:59], a[204:207], a[232:235], a[56:59]\n" \
"v_or_b32_e32 v212, s35, v20\n" \
"s_or_b32 s39, s40, s35\n" \
M_"a[52:55], a[140:143], a[164:167], a[52:55]\n" \
"s_or_b32 s55, s41, s4\n" \
"v_or_b32_e32 v199, 0x22000, v212\n" \
M_"a[52:55], a[204:207], a[228:231], a[52:55]\n" \
X_"a[186:187], v38 offset:33792\n" \
"s_add_i32 s35, s39, s7\n" \
M_"a[48:51], a[140:143], a[160:163], a[48:51]\n" \
"s_add_i32 s54, s41, s5\n" \
"s_add_i32 s55, s55, 0x80000\n" \
M_"a[48:51], a[204:207], a[224:227], a[48:51]\n" \
"v_mov_b32_e32 v215, 0xffff\n" \
"s_and_b32 s4, s3, 1\n" \
M_"a[64:67], a[144:147], a[160:163], a[64:67]\n" \
X_"a[248:249], v38 offset:40960\n" \
"s_lshl_b32 s5, s4, 4\n" \
M_"a[64:67], a[208:211], a[224:227], a[64:67]\n" \
"v_lshrrev_b32_e32 v212, 4, v20\n" \
"v_lshrrev_b32_e64 v218, 6, v20\n" \
M_"a[68:71], a[144:147], a[164:167], a[68:71]\n" \
"v_and_b32_e64 v219, 12, v218\n" \
"v_and_or_b32 v214, v212, 15, s5\n" \
M_"a[68:71], a[208:211], a[228:231], a[68:71]\n" \
X_"a[250:251], v38 offset:41984\n" \
"v_cmp_gt_u32_e64 vcc, v219, v214\n" \
M_"a[72:75], a[144:147], a[168:171], a[72:75]\n" \
"v_mov_b32_e32 v220, 0xffff0000\n" \
"s_lshl_b32 s5, s2, 2\n" \
M_"a[72:75], a[208:211], a[232:235], a[72:75]\n" \
"v_cndmask_b32_e64 v217, v215, 0, vcc\n" \
"v_cmp_lt_u32_e32 vcc, v219, v214\n" \
M_"a[76:79], a[144:147], a[172:175], a[76:79]\n" \
"v_mov_b32_e32 v213, s23\n" \
"s_or_b32 s5, s5, s3\n" \
M_"a[76:79], a[208:211], a[236:239], a[76:79]\n" \
X_"a[188:189], v39 offset:32768\n" \
"v_cndmask_b32_e64 v221, 0, v220, vcc\n" \
M_"a[92:95], a[148:151], a[172:175], a[92:95]\n" \
"v_or_b32_e32 v12, v217, v221\n" \
"v_or_b32_e32 v217, 2, v219\n" \
M_"a[92:95], a[212:215], a[236:239], a[92:95]\n" \
"v_cmp_gt_u32_e32 vcc, v217, v214\n" \
"v_mov_b32_e32 v216, s25\n" \
M_"a[88:91], a[148:151], a[168:171], a[88:91]\n" \
X_"a[190:191], v39 offset:33792\n" \
"s_movk_i32 s40, 0xf400\n" \
M_"a[88:91], a[212:215], a[232:235], a[88:91]\n" \
"v_cndmask_b32_e64 v221, v215, 0, vcc\n" \
"v_cmp_lt_u32_e32 vcc, v217, v214\n" \
M_"a[84:87], a[148:151], a[164:167], a[84:87]\n" \
"s_lshl_b32 s4, s4, 6\n" \
"v_cndmask_b32_e32 v217, 0, v220, vcc\n" \
M_"a[84:87], a[212:215], a[228:231], a[84:87]\n" \
X_"a[252:253], v39 offset:40960\n" \
"v_or_b32_e64 v13, v221, v217\n" \
M_"a[80:83], a[148:151], a[160:163], a[80:83]\n" \
"v_or_b32_e32 v217, 16, v219\n" \
"v_cmp_gt_u32_e32 vcc, v217, v214\n" \
M_"a[80:83], a[212:215], a[224:227], a[80:83]\n" \
"s_nop 0\n" \
"v_cndmask_b32_e64 v221, v215, 0, vcc\n" \
"v_cmp_lt_u32_e32 vcc, v217, v214\n" \
M_"a[96:99], a[152:155], a[160:163], a[96:99]\n" \
X_"a[254:255], v39 offset:41984\n" \
"v_cndmask_b32_e64 v217, 0, v220, vcc\n" \
M_"a[96:99], a[216:219], a[224:227], a[96:99]\n" \
"v_or_b32_e32 v14, v221, v217\n" \
"v_or_b32_e32 v217, 18, v219\n" \
M_"a[100:103], a[152:155], a[164:167], a[100:103]\n" \
"v_cmp_gt_u32_e32 vcc, v217, v214\n" \
"s_nop 1\n" \
"v_cndmask_b32_e64 v215, v215, 0, vcc\n" \
M_"a[100:103], a[216:219], a[228:231], a[100:103]\n" \
"v_cmp_lt_u32_e32 vcc, v217, v214\n" \
"s_nop 1\n" \
"v_cndmask_b32_e64 v214, 0, v220, vcc\n" \
M_"a[104:107], a[152:155], a[168:171], a[104:107]\n" \
"v_or_b32_e64 v15, v215, v214\n" \
"v_mad_u64_u32 v[214:215], s[6:7], s5, 42, v[212:213]\n" \
M_"a[104:107], a[216:219], a[232:235], a[104:107]\n" \
"s_movk_i32 s5, 0x1440\n" \
"v_cmp_gt_u32_e32 vcc, s5, v214\n" \
M_"a[108:111], a[152:155], a[172:175], a[108:111]\n" \
"v_min_u32_e32 v217, 0xa43f, v214\n" \
"v_mov_b32_e64 v214, s24\n" \
M_"a[108:111], a[216:219], a[236:239], a[108:111]\n" \
"v_cndmask_b32_e32 v215, v216, v213, vcc\n" \
"v_mov_b32_e32 v213, s22\n" \
"s_waitcnt lgkmcnt(15)\n" \
M_"a[124:127], a[156:159], a[172:175], a[124:127]\n" \
"s_movk_i32 s6, 0xffee\n" \
"v_cndmask_b32_e64 v214, v214, v213, vcc\n" \
M_"a[124:127], a[220:223], a[236:239], a[124:127]\n" \
"v_add_u32_e32 v213, 0xffffebc0, v217\n" \
"s_lshl_b32 s5, s2, 5\n" \
M_"a[120:123], a[156:159], a[168:171], a[120:123]\n" \
"v_cndmask_b32_e32 v213, v213, v217, vcc\n" \
"v_lshlrev_b32_e64 v216, 7, v213\n" \
M_"a[120:123], a[220:223], a[232:235], a[120:123]\n" \
"v_mul_u32_u24_e32 v213, 0xe39, v212\n" \
"v_mov_b32_e64 v217, 0\n" \
M_"a[116:119], a[156:159], a[164:167], a[116:119]\n" \
"v_lshrrev_b32_e64 v213, 16, v213\n" \
"v_lshl_add_u64 v[16:17], v[214:215], 0, v[216:217]\n" \
M_"a[116:119], a[220:223], a[228:231], a[116:119]\n" \
"v_mad_i32_i24 v214, v213, s6, v212\n" \
"s_add_i32 s5, s5, 32\n" \
M_"a[112:115], a[156:159], a[160:163], a[112:115]\n" \
"v_lshlrev_b32_e32 v215, 4, v214\n" \
"v_add_u32_e64 v216, s5, v215\n" \
M_"a[112:115], a[220:223], a[224:227], a[112:115]\n" \
"s_waitcnt vmcnt(18)\n" \
"v_readfirstlane_b32 s62, v180\n" \
"s_nop 3\n" \
"s_add_u32 s62, s62, 0x9E3779B1\n" \
"s_waitcnt vmcnt(2) lgkmcnt(0)\n" \
"s_barrier\n" \
M_"v[52:55], a[128:131], a[176:179], v[52:55]\n" \
"s_add_u32 m0, s30, 0x8000\n" \
"s_movk_i32 s84, 0x180\n" \
M_"v[52:55], a[192:195], a[240:243], v[52:55]\n" \
B_"v22, s[64:67], s84 offen offset:0 lds\n" \
M_"v[56:59], a[128:131], a[180:183], v[56:59]\n" \
X_"a[160:161], v32 offset:0\n" \
"v_and_b32_e32 v216, 0xf0, v216\n" \
M_"v[56:59], a[192:195], a[244:247], v[56:59]\n" \
X_"a[162:163], v32 offset:1024\n" \
"v_cmp_gt_u32_e64 vcc, 16, v214\n" \
M_"v[60:63], a[128:131], a[184:187], v[60:63]\n" \
"s_movk_i32 s84, 0x1d80\n" \
M_"v[60:63], a[192:195], a[248:251], v[60:63]\n" \
B_"v23, s[64:67], s84 offen offset:1024 lds\n" \
M_"v[64:67], a[128:131], a[188:191], v[64:67]\n" \
R_"a[128:131], v28 offset:16384\n" \
"s_movk_i32 s7, 0xf800\n" \
M_"v[64:67], a[192:195], a[252:255], v[64:67]\n" \
R_"a[192:195], v29 offset:16384\n" \
"v_cndmask_b32_e64 v214, v215, v216, vcc\n" \
M_"v[80:83], a[132:135], a[188:191], v[80:83]\n" \
"s_movk_i32 s84, 0x3980\n" \
M_"v[80:83], a[196:199], a[252:255], v[80:83]\n" \
B_"v22, s[64:67], s84 offen offset:2048 lds\n" \
M_"v[76:79], a[132:135], a[184:187], v[76:79]\n" \
X_"a[224:225], v32 offset:8192\n" \
"v_or_b32_e32 v215, 64, v212\n" \
M_"v[76:79], a[196:199], a[248:251], v[76:79]\n" \
X_"a[226:227], v32 offset:9216\n" \
"v_mul_u32_u24_e32 v216, 0xe39, v215\n" \
M_"v[72:75], a[132:135], a[180:183], v[72:75]\n" \
"s_movk_i32 s84, 0x5580\n" \
M_"v[72:75], a[196:199], a[244:247], v[72:75]\n" \
B_"v23, s[64:67], s84 offen offset:3072 lds\n" \
M_"v[68:71], a[132:135], a[176:179], v[68:71]\n" \
R_"a[132:135], v28 offset:18432\n" \
"v_lshrrev_b32_e32 v216, 16, v216\n" \
M_"v[68:71], a[196:199], a[240:243], v[68:71]\n" \
R_"a[196:199], v29 offset:18432\n" \
"v_mad_i32_i24 v215, v216, s6, v215\n" \
M_"v[84:87], a[136:139], a[176:179], v[84:87]\n" \
"s_add_u32 m0, s31, 0x8000\n" \
"s_mov_b32 s84, 0x180000\n" \
M_"v[84:87], a[200:203], a[240:243], v[84:87]\n" \
B_"v26, s[68:71], s84 offen offset:0 lds\n" \
M_"v[88:91], a[136:139], a[180:183], v[88:91]\n" \
X_"a[164:165], v33 offset:0\n" \
"v_lshlrev_b32_e64 v217, 4, v215\n" \
M_"v[88:91], a[200:203], a[244:247], v[88:91]\n" \
X_"a[166:167], v33 offset:1024\n" \
"v_add_u32_e64 v220, s5, v217\n" \
M_"v[92:95], a[136:139], a[184:187], v[92:95]\n" \
"s_addk_i32 s84, 0x7c00\n" \
M_"v[92:95], a[200:203], a[248:251], v[92:95]\n" \
B_"v26, s[68:71], s84 offen offset:1024 lds\n" \
M_"v[96:99], a[136:139], a[188:191], v[96:99]\n" \
R_"a[136:139], v28 offset:20480\n" \
"v_and_b32_e32 v220, 0xf0, v220\n" \
M_"v[96:99], a[200:203], a[252:255], v[96:99]\n" \
R_"a[200:203], v29 offset:20480\n" \
"v_cmp_gt_u32_e32 vcc, 16, v215\n" \
M_"v[112:115], a[140:143], a[188:191], v[112:115]\n" \
"s_addk_i32 s84, 0x7c00\n" \
M_"v[112:115], a[204:207], a[252:255], v[112:115]\n" \
B_"v27, s[68:71], s84 offen offset:2048 lds\n" \
M_"v[108:111], a[140:143], a[184:187], v[108:111]\n" \
X_"a[228:229], v33 offset:8192\n" \
"v_cndmask_b32_e32 v215, v217, v220, vcc\n" \
M_"v[108:111], a[204:207], a[248:251], v[108:111]\n" \
X_"a[230:231], v33 offset:9216\n" \
"v_or_b32_e32 v217, 0x80, v212\n" \
M_"v[104:107], a[140:143], a[180:183], v[104:107]\n" \
"s_addk_i32 s84, 0x7c00\n" \
M_"v[104:107], a[204:207], a[244:247], v[104:107]\n" \
B_"v27, s[68:71], s84 offen offset:3072 lds\n" \
M_"v[100:103], a[140:143], a[176:179], v[100:103]\n" \
R_"a[140:143], v28 offset:22528\n" \
"v_mul_u32_u24_e32 v220, 0xe39, v217\n" \
M_"v[100:103], a[204:207], a[240:243], v[100:103]\n" \
R_"a[204:207], v29 offset:22528\n" \
"v_lshrrev_b32_e32 v220, 16, v220\n" \
M_"v[116:119], a[144:147], a[176:179], v[116:119]\n" \
"s_add_u32 m0, s30, 0x9000\n" \
"s_mov_b32 s84, 0x8180\n" \
M_"v[116:119], a[208:211], a[240:243], v[116:119]\n" \
B_"v22, s[64:67], s84 offen offset:0 lds\n" \
M_"v[120:123], a[144:147], a[180:183], v[120:123]\n" \
X_"a[168:169], v34 offset:0\n" \
"v_mad_i32_i24 v217, v220, s6, v217\n" \
M_"v[120:123], a[208:211], a[244:247], v[120:123]\n" \
X_"a[170:171], v34 offset:1024\n" \
"v_lshlrev_b32_e64 v221, 4, v217\n" \
M_"v[124:127], a[144:147], a[184:187], v[124:127]\n" \
"s_addk_i32 s84, 0x1c00\n" \
M_"v[124:127], a[208:211], a[248:251], v[124:127]\n" \
B_"v23, s[64:67], s84 offen offset:1024 lds\n" \
M_"v[128:131], a[144:147], a[188:191], v[128:131]\n" \
R_"a[144:147], v28 offset:0\n" \
"v_add_u32_e32 v222, s5, v221\n" \
M_"v[128:131], a[208:211], a[252:255], v[128:131]\n" \
R_"a[208:211], v29 offset:0\n" \
"v_and_b32_e32 v222, 0xf0, v222\n" \
M_"v[144:147], a[148:151], a[188:191], v[144:147]\n" \
"s_addk_i32 s84, 0x1c00\n" \
M_"v[144:147], a[212:215], a[252:255], v[144:147]\n" \
B_"v22, s[64:67], s84 offen offset:2048 lds\n" \
M_"v[140:143], a[148:151], a[184:187], v[140:143]\n" \
X_"a[232:233], v34 offset:8192\n" \
"v_cmp_gt_u32_e32 vcc, 16, v217\n" \
M_"v[140:143], a[212:215], a[248:251], v[140:143]\n" \
X_"a[234:235], v34 offset:9216\n" \
"v_cndmask_b32_e64 v217, v221, v222, vcc\n" \
M_"v[136:139], a[148:151], a[180:183], v[136:139]\n" \
"s_addk_i32 s84, 0x1c00\n" \
"s_nop 0\n" \
M_"v[136:139], a[212:215], a[244:247], v[136:139]\n" \
B_"v23, s[64:67], s84 offen offset:3072 lds\n" \
M_"v[132:135], a[148:151], a[176:179], v[132:135]\n" \
R_"a[148:151], v28 offset:2048\n" \
"v_or_b32_e32 v221, 0xc0, v212\n" \
M_"v[132:135], a[212:215], a[240:243], v[132:135]\n" \
R_"a[212:215], v29 offset:2048\n" \
"v_mul_u32_u24_e32 v222, 0xe39, v221\n" \
M_"v[148:151], a[152:155], a[176:179], v[148:151]\n" \
"s_add_u32 m0, s31, 0xc000\n" \
"s_mov_b32 s84, 0x180000\n" \
M_"v[148:151], a[216:219], a[240:243], v[148:151]\n" \
B_"v26, s[72:75], s84 offen offset:0 lds\n" \
M_"v[152:155], a[152:155], a[180:183], v[152:155]\n" \
X_"a[172:173], v35 offset:0\n" \
"v_lshrrev_b32_e64 v222, 16, v222\n" \
M_"v[152:155], a[216:219], a[244:247], v[152:155]\n" \
X_"a[174:175], v35 offset:1024\n" \
"v_mad_i32_i24 v221, v222, s6, v221\n" \
M_"v[156:159], a[152:155], a[184:187], v[156:159]\n" \
"s_addk_i32 s84, 0x7c00\n" \
M_"v[156:159], a[216:219], a[248:251], v[156:159]\n" \
B_"v26, s[72:75], s84 offen offset:1024 lds\n" \
M_"v[160:163], a[152:155], a[188:191], v[160:163]\n" \
R_"a[152:155], v28 offset:4096\n" \
"v_lshlrev_b32_e32 v223, 4, v221\n" \
M_"v[160:163], a[216:219], a[252:255], v[160:163]\n" \
R_"a[216:219], v29 offset:4096\n" \
"v_add_u32_e64 v224, s5, v223\n" \
M_"v[176:179], a[156:159], a[188:191], v[176:179]\n" \
X_"a[236:237], v35 offset:8192\n" \
"v_and_b32_e32 v224, 0xf0, v224\n" \
M_"v[176:179], a[220:223], a[252:255], v[176:179]\n" \
X_"a[238:239], v35 offset:9216\n" \
"v_cmp_gt_u32_e64 vcc, 16, v221\n" \
M_"v[172:175], a[156:159], a[184:187], v[172:175]\n" \
"s_nop 0\n" \
"v_cndmask_b32_e32 v221, v223, v224, vcc\n" \
"v_or_b32_e32 v223, 0x100, v212\n" \
M_"v[172:175], a[220:223], a[248:251], v[172:175]\n" \
"v_mul_u32_u24_e32 v224, 0xe39, v223\n" \
"v_lshrrev_b32_e64 v224, 16, v224\n" \
M_"v[168:171], a[156:159], a[180:183], v[168:171]\n" \
"v_mad_i32_i24 v223, v224, s6, v223\n" \
"v_lshlrev_b32_e64 v225, 4, v223\n" \
M_"v[168:171], a[220:223], a[244:247], v[168:171]\n" \
"v_add_u32_e64 v226, s5, v225\n" \
"v_and_b32_e32 v226, 0xf0, v226\n" \
M_"v[164:167], a[156:159], a[176:179], v[164:167]\n" \
R_"a[156:159], v28 offset:6144\n" \
"v_cmp_gt_u32_e64 vcc, 16, v223\n" \
M_"v[164:167], a[220:223], a[240:243], v[164:167]\n" \
R_"a[220:223], v29 offset:6144\n" \
"s_waitcnt lgkmcnt(15)\n" \
M_"a[0:3], a[128:131], a[160:163], a[0:3]\n" \
"s_addk_i32 s84, 0x7c00\n" \
M_"a[0:3], a[192:195], a[224:227], a[0:3]\n" \
B_"v27, s[72:75], s84 offen offset:2048 lds\n" \
M_"a[4:7], a[128:131], a[164:167], a[4:7]\n" \
X_"a[176:177], v36 offset:0\n" \
"v_cndmask_b32_e64 v223, v225, v226, vcc\n" \
M_"a[4:7], a[192:195], a[228:231], a[4:7]\n" \
"s_addk_i32 s84, 0x7c00\n" \
"s_waitcnt lgkmcnt(15)\n" \
M_"a[8:11], a[128:131], a[168:171], a[8:11]\n" \
B_"v27, s[72:75], s84 offen offset:3072 lds\n" \
"s_waitcnt lgkmcnt(11)\n" \
M_"a[8:11], a[192:195], a[232:235], a[8:11]\n" \
X_"a[178:179], v36 offset:1024\n" \
"v_or_b32_e32 v225, 0x140, v212\n" \
"s_waitcnt lgkmcnt(8)\n" \
M_"a[12:15], a[128:131], a[172:175], a[12:15]\n" \
X_"a[240:241], v36 offset:8192\n" \
"v_mul_u32_u24_e32 v226, 0xe39, v225\n" \
"s_waitcnt lgkmcnt(5)\n" \
M_"a[12:15], a[192:195], a[236:239], a[12:15]\n" \
"v_lshrrev_b32_e32 v226, 16, v226\n" \
"v_mad_i32_i24 v225, v226, s6, v225\n" \
M_"a[28:31], a[132:135], a[172:175], a[28:31]\n" \
"v_lshlrev_b32_e32 v227, 4, v225\n" \
"v_add_u32_e32 v228, s5, v227\n" \
M_"a[28:31], a[196:199], a[236:239], a[28:31]\n" \
X_"a[242:243], v36 offset:9216\n" \
"v_and_b32_e32 v228, 0xf0, v228\n" \
M_"a[24:27], a[132:135], a[168:171], a[24:27]\n" \
"v_cmp_gt_u32_e32 vcc, 16, v225\n" \
"s_nop 1\n" \
"v_cndmask_b32_e64 v225, v227, v228, vcc\n" \
M_"a[24:27], a[196:199], a[232:235], a[24:27]\n" \
"v_or_b32_e32 v227, 0x180, v212\n" \
"v_mul_u32_u24_e32 v228, 0xe39, v227\n" \
M_"a[20:23], a[132:135], a[164:167], a[20:23]\n" \
B_"v[200:203], v192, s[76:79], s34 offen offset:0\n" \
M_"a[20:23], a[196:199], a[228:231], a[20:23]\n" \
X_"a[180:181], v37 offset:0\n" \
"v_lshrrev_b32_e64 v228, 16, v228\n" \
M_"a[16:19], a[132:135], a[160:163], a[16:19]\n" \
"v_mad_i32_i24 v227, v228, s6, v227\n" \
"v_lshlrev_b32_e64 v229, 4, v227\n" \
M_"a[16:19], a[196:199], a[224:227], a[16:19]\n" \
"v_add_u32_e64 v230, s5, v229\n" \
"v_and_b32_e32 v230, 0xf0, v230\n" \
M_"a[32:35], a[136:139], a[160:163], a[32:35]\n" \
"s_mov_b32 m0, s32\n" \
X_"a[182:183], v37 offset:1024\n" \
M_"a[32:35], a[200:203], a[224:227], a[32:35]\n" \
"s_mov_b32 exec_lo, 0x0\n" \
"s_mov_b32 exec_hi, 0xff00\n" \
"global_load_lds_dword v[184:185], off\n" \
"s_mov_b64 exec, -1\n" \
M_"a[36:39], a[136:139], a[164:167], a[36:39]\n" \
B_"v[204:207], v193, s[76:79], s34 offen offset:0\n" \
M_"a[36:39], a[200:203], a[228:231], a[36:39]\n" \
X_"a[244:245], v37 offset:8192\n" \
"v_cmp_gt_u32_e32 vcc, 16, v227\n" \
M_"a[40:43], a[136:139], a[168:171], a[40:43]\n" \
"s_nop 0\n" \
"v_cndmask_b32_e32 v227, v229, v230, vcc\n" \
"v_or_b32_e32 v229, 0x1c0, v212\n" \
M_"a[40:43], a[200:203], a[232:235], a[40:43]\n" \
"v_mul_u32_u24_e32 v230, 0xe39, v229\n" \
"v_lshrrev_b32_e64 v230, 16, v230\n" \
M_"a[44:47], a[136:139], a[172:175], a[44:47]\n" \
X_"a[246:247], v37 offset:9216\n" \
"v_mad_i32_i24 v229, v230, s6, v229\n" \
M_"a[44:47], a[200:203], a[236:239], a[44:47]\n" \
"v_lshlrev_b32_e32 v231, 4, v229\n" \
"v_add_u32_e32 v232, s5, v231\n" \
M_"a[60:63], a[140:143], a[172:175], a[60:63]\n" \
"buffer_load_dword v208, v194, s[76:79], s34 offen offset:32\n" \
M_"a[60:63], a[204:207], a[236:239], a[60:63]\n" \
"v_and_b32_e32 v232, 0xf0, v232\n" \
"v_cmp_gt_u32_e64 vcc, 16, v229\n" \
M_"a[56:59], a[140:143], a[168:171], a[56:59]\n" \
X_"a[184:185], v38 offset:0\n" \
"v_cndmask_b32_e64 v229, v231, v232, vcc\n" \
M_"a[56:59], a[204:207], a[232:235], a[56:59]\n" \
"v_or_b32_e32 v231, 0x200, v212\n" \
"v_mul_u32_u24_e32 v232, 0xe39, v231\n" \
M_"a[52:55], a[140:143], a[164:167], a[52:55]\n" \
"v_lshrrev_b32_e64 v232, 16, v232\n" \
"v_mad_i32_i24 v231, v232, s6, v231\n" \
M_"a[52:55], a[204:207], a[228:231], a[52:55]\n" \
X_"a[186:187], v38 offset:1024\n" \
"v_lshlrev_b32_e64 v233, 4, v231\n" \
M_"a[48:51], a[140:143], a[160:163], a[48:51]\n" \
"v_add_u32_e32 v234, s5, v233\n" \
"s_movk_i32 s5, 0x480\n" \
M_"a[48:51], a[204:207], a[224:227], a[48:51]\n" \
"buffer_load_dword v209, v194, s[76:79], s34 offen offset:1184\n" \
M_"a[64:67], a[144:147], a[160:163], a[64:67]\n" \
X_"a[248:249], v38 offset:8192\n" \
"v_mad_u32_u24 v0, v213, s5, v214\n" \
M_"a[64:67], a[208:211], a[224:227], a[64:67]\n" \
"v_mul_u32_u24_e32 v213, 0x480, v216\n" \
"s_movk_i32 s6, 0xfc00\n" \
"s_nop 0\n" \
M_"a[68:71], a[144:147], a[164:167], a[68:71]\n" \
"v_add3_u32 v1, v213, v215, s6\n" \
"v_mul_u32_u24_e32 v213, 0x480, v220\n" \
M_"a[68:71], a[208:211], a[228:231], a[68:71]\n" \
X_"a[250:251], v38 offset:9216\n" \
"v_add3_u32 v2, v213, v217, s7\n" \
M_"a[72:75], a[144:147], a[168:171], a[72:75]\n" \
"v_mul_u32_u24_e32 v213, 0x480, v222\n" \
"v_add3_u32 v3, v213, v221, s40\n" \
M_"a[72:75], a[208:211], a[232:235], a[72:75]\n" \
"buffer_load_dword v211, v194, s[76:79], s34 offen offset:2336\n" \
M_"a[76:79], a[144:147], a[172:175], a[76:79]\n" \
"v_mul_u32_u24_e32 v213, 0x480, v226\n" \
"v_add3_u32 v5, v213, v225, s6\n" \
M_"a[76:79], a[208:211], a[236:239], a[76:79]\n" \
X_"a[188:189], v39 offset:0\n" \
"v_mul_u32_u24_e32 v213, 0x480, v228\n" \
M_"a[92:95], a[148:151], a[172:175], a[92:95]\n" \
"v_add3_u32 v6, v213, v227, s7\n" \
"v_mul_u32_u24_e32 v213, 0x480, v230\n" \
M_"a[92:95], a[212:215], a[236:239], a[92:95]\n" \
"v_add3_u32 v7, v213, v229, s40\n" \
"s_lshl_b32 s7, s3, 3\n" \
M_"a[88:91], a[148:151], a[168:171], a[88:91]\n" \
X_"a[190:191], v39 offset:1024\n" \
"s_lshl_b32 s40, s3, 1\n" \
M_"a[88:91], a[212:215], a[232:235], a[88:91]\n" \
"s_mov_b32 s76, s20\n" \
"s_and_b32 s77, s21, 0xffff\n" \
M_"a[84:87], a[148:151], a[164:167], a[84:87]\n" \
"s_mov_b32 s78, 0x480\n" \
"s_and_b32 s7, s7, 16\n" \
M_"a[84:87], a[212:215], a[228:231], a[84:87]\n" \
"buffer_load_ushort v210, v195, s[76:79], 0 offen\n" \
M_"a[80:83], a[148:151], a[160:163], a[80:83]\n" \
X_"a[252:253], v39 offset:8192\n" \
"s_and_b32 s40, s40, 2\n" \
".fill (L_pb + 48864 - .) / 4, 4, 0xBF800000\n" \
"L_pp12:\n" \
"s_cbranch_execz L_pret\n" \
M_"a[80:83], a[212:215], a[224:227], a[80:83]\n" \
"s_and_b32 s6, s2, 7\n" \
"v_bfe_u32 v213, v20, 7, 1\n" \
M_"a[96:99], a[152:155], a[160:163], a[96:99]\n" \
X_"a[254:255], v39 offset:9216\n" \
"s_or_b32 s7, s40, s7\n" \
M_"a[96:99], a[216:219], a[224:227], a[96:99]\n" \
"s_mov_b32 s76, s8\n" \
"s_and_b32 s77, s9, 0xffff\n" \
M_"a[100:103], a[152:155], a[164:167], a[100:103]\n" \
"s_mov_b32 s78, 0x240000\n" \
"v_and_b32_e32 v234, 0xf0, v234\n" \
M_"a[100:103], a[216:219], a[228:231], a[100:103]\n" \
"v_cmp_gt_u32_e64 vcc, 16, v231\n" \
"v_or3_b32 v213, s7, v213, v219\n" \
M_"a[104:107], a[152:155], a[168:171], a[104:107]\n" \
"v_and_b32_e32 v214, 32, v20\n" \
"s_lshl_b32 s6, s6, 5\n" \
M_"a[104:107], a[216:219], a[232:235], a[104:107]\n" \
"v_cndmask_b32_e32 v231, v233, v234, vcc\n" \
"v_mul_u32_u24_e32 v213, 18, v213\n" \
M_"a[108:111], a[152:155], a[172:175], a[108:111]\n" \
"v_mov_b32_e32 v215, 0x100\n" \
"v_mov_b32_e64 v216, s6\n" \
M_"a[108:111], a[216:219], a[236:239], a[108:111]\n" \
"v_cmp_eq_u32_e64 vcc, 0, v214\n" \
"v_and_or_b32 v213, v218, 1, v213\n" \
"s_waitcnt lgkmcnt(15)\n" \
M_"a[124:127], a[156:159], a[172:175], a[124:127]\n" \
"v_lshlrev_b32_e32 v212, 3, v212\n" \
"v_cndmask_b32_e64 v214, v215, v216, vcc\n" \
M_"a[124:127], a[220:223], a[236:239], a[124:127]\n" \
"v_mad_u32_u24 v213, v213, s5, v214\n" \
"v_and_or_b32 v213, v20, 16, v213\n" \
M_"a[120:123], a[156:159], a[168:171], a[120:123]\n" \
"v_add_u32_e32 v9, 0x4800, v213\n" \
"v_mul_u32_u24_e32 v213, 0x120, v218\n" \
M_"a[120:123], a[220:223], a[232:235], a[120:123]\n" \
"v_and_or_b32 v10, v212, 24, v213\n" \
"v_lshrrev_b32_e64 v213, 2, v20\n" \
M_"a[116:119], a[156:159], a[164:167], a[116:119]\n" \
"v_and_or_b32 v213, v213, 60, s4\n" \
"v_lshl_or_b32 v212, s3, 9, v212\n" \
M_"a[116:119], a[220:223], a[228:231], a[116:119]\n" \
"v_mad_u32_u24 v4, v224, s5, v223\n" \
"v_mad_u32_u24 v8, v232, s5, v231\n" \
M_"a[112:115], a[156:159], a[160:163], a[112:115]\n" \
"v_or_b32_e32 v18, 0x27000, v213\n" \
"v_or_b32_e32 v19, 0x27800, v212\n" \
"s_waitcnt vmcnt(7) lgkmcnt(0)\n" \
M_"a[112:115], a[220:223], a[224:227], a[112:115]\n" \
"s_barrier\n" \
M_"v[52:55], a[128:131], a[176:179], v[52:55]\n" \
"s_add_u32 m0, s30, 0x0\n" \
"s_movk_i32 s84, 0x200\n" \
M_"v[52:55], a[192:195], a[240:243], v[52:55]\n" \
B_"v22, s[64:67], s84 offen offset:0 lds\n" \
M_"v[56:59], a[128:131], a[180:183], v[56:59]\n" \
X_"a[160:161], v32 offset:32768\n" \
"v_mov_b32_e32 v215, 0x1000c0c\n" \
M_"v[56:59], a[192:195], a[244:247], v[56:59]\n" \
X_"a[162:163], v32 offset:33792\n" \
"v_mov_b32_e32 v216, 0xc0c0100\n" \
M_"v[60:63], a[128:131], a[184:187], v[60:63]\n" \
"s_movk_i32 s84, 0x1e00\n" \
M_"v[60:63], a[192:195], a[248:251], v[60:63]\n" \
B_"v23, s[64:67], s84 offen offset:1024 lds\n" \
M_"v[64:67], a[128:131], a[188:191], v[64:67]\n" \
R_"a[128:131], v28 offset:49152\n" \
"v_bfe_u32 v214, v20, 4, 1\n" \
M_"v[64:67], a[192:195], a[252:255], v[64:67]\n" \
R_"a[192:195], v29 offset:49152\n" \
"v_lshrrev_b32_e32 v212, 8, v20\n" \
M_"v[80:83], a[132:135], a[188:191], v[80:83]\n" \
"s_movk_i32 s84, 0x3a00\n" \
M_"v[80:83], a[196:199], a[252:255], v[80:83]\n" \
B_"v22, s[64:67], s84 offen offset:2048 lds\n" \
M_"v[76:79], a[132:135], a[184:187], v[76:79]\n" \
X_"a[224:225], v32 offset:40960\n" \
"v_cmp_eq_u32_e32 vcc, 0, v214\n" \
M_"v[76:79], a[196:199], a[248:251], v[76:79]\n" \
X_"a[226:227], v32 offset:41984\n" \
"v_bfe_u32 v213, v20, 4, 4\n" \
M_"v[72:75], a[132:135], a[180:183], v[72:75]\n" \
"s_movk_i32 s84, 0x5600\n" \
M_"v[72:75], a[196:199], a[244:247], v[72:75]\n" \
B_"v23, s[64:67], s84 offen offset:3072 lds\n" \
M_"v[68:71], a[132:135], a[176:179], v[68:71]\n" \
R_"a[132:135], v28 offset:51200\n" \
"v_mov_b32_e32 v217, 0xc0c0302\n" \
M_"v[68:71], a[196:199], a[240:243], v[68:71]\n" \
R_"a[196:199], v29 offset:51200\n" \
"v_cndmask_b32_e32 v215, v215, v216, vcc\n" \
M_"v[84:87], a[136:139], a[176:179], v[84:87]\n" \
"s_add_u32 m0, s31, 0x0\n" \
"s_mov_b32 s84, 0x200000\n" \
M_"v[84:87], a[200:203], a[240:243], v[84:87]\n" \
B_"v26, s[68:71], s84 offen offset:0 lds\n" \
M_"v[88:91], a[136:139], a[180:183], v[88:91]\n" \
X_"a[164:165], v33 offset:32768\n" \
"v_mov_b32_e32 v216, 0x3020c0c\n" \
M_"v[88:91], a[200:203], a[244:247], v[88:91]\n" \
X_"a[166:167], v33 offset:33792\n" \
"v_lshl_or_b32 v214, v212, 2, v214\n" \
M_"v[92:95], a[136:139], a[184:187], v[92:95]\n" \
"s_addk_i32 s84, 0x7c00\n" \
M_"v[92:95], a[200:203], a[248:251], v[92:95]\n" \
B_"v26, s[68:71], s84 offen offset:1024 lds\n" \
M_"v[96:99], a[136:139], a[188:191], v[96:99]\n" \
R_"a[136:139], v28 offset:53248\n" \
"v_cndmask_b32_e64 v216, v216, v217, vcc\n" \
M_"v[96:99], a[200:203], a[252:255], v[96:99]\n" \
R_"a[200:203], v29 offset:53248\n" \
"v_mov_b32_e32 v217, 0xc0c0c0c\n" \
M_"v[112:115], a[140:143], a[188:191], v[112:115]\n" \
"s_addk_i32 s84, 0x7c00\n" \
M_"v[112:115], a[204:207], a[252:255], v[112:115]\n" \
B_"v27, s[68:71], s84 offen offset:2048 lds\n" \
M_"v[108:111], a[140:143], a[184:187], v[108:111]\n" \
X_"a[228:229], v33 offset:40960\n" \
"v_cmp_gt_u32_e32 vcc, v214, v213\n" \
M_"v[108:111], a[204:207], a[248:251], v[108:111]\n" \
X_"a[230:231], v33 offset:41984\n" \
"v_or_b32_e64 v214, 2, v214\n" \
M_"v[104:107], a[140:143], a[180:183], v[104:107]\n" \
"s_addk_i32 s84, 0x7c00\n" \
M_"v[104:107], a[204:207], a[244:247], v[104:107]\n" \
B_"v27, s[68:71], s84 offen offset:3072 lds\n" \
M_"v[100:103], a[140:143], a[176:179], v[100:103]\n" \
R_"a[140:143], v28 offset:55296\n" \
"s_lshr_b32 s4, s2, 5\n" \
M_"v[100:103], a[204:207], a[240:243], v[100:103]\n" \
R_"a[204:207], v29 offset:55296\n" \
"v_cndmask_b32_e64 v40, v217, v215, vcc\n" \
M_"v[116:119], a[144:147], a[176:179], v[116:119]\n" \
"s_add_u32 m0, s30, 0x1000\n" \
"s_mov_b32 s84, 0x8200\n" \
M_"v[116:119], a[208:211], a[240:243], v[116:119]\n" \
B_"v22, s[64:67], s84 offen offset:0 lds\n" \
M_"v[120:123], a[144:147], a[180:183], v[120:123]\n" \
X_"a[168:169], v34 offset:32768\n" \
"v_cndmask_b32_e64 v42, v215, v217, vcc\n" \
M_"v[120:123], a[208:211], a[244:247], v[120:123]\n" \
X_"a[170:171], v34 offset:33792\n" \
"v_cmp_gt_u32_e64 vcc, v214, v213\n" \
M_"v[124:127], a[144:147], a[184:187], v[124:127]\n" \
"s_addk_i32 s84, 0x1c00\n" \
M_"v[124:127], a[208:211], a[248:251], v[124:127]\n" \
B_"v23, s[64:67], s84 offen offset:1024 lds\n" \
M_"v[128:131], a[144:147], a[188:191], v[128:131]\n" \
R_"a[144:147], v28 offset:32768\n" \
"v_and_b32_e32 v214, 16, v20\n" \
M_"v[128:131], a[208:211], a[252:255], v[128:131]\n" \
R_"a[208:211], v29 offset:32768\n" \
"s_movk_i32 s42, 0x3f80\n" \
M_"v[144:147], a[148:151], a[188:191], v[144:147]\n" \
"s_addk_i32 s84, 0x1c00\n" \
M_"v[144:147], a[212:215], a[252:255], v[144:147]\n" \
B_"v22, s[64:67], s84 offen offset:2048 lds\n" \
M_"v[140:143], a[148:151], a[184:187], v[140:143]\n" \
X_"a[232:233], v34 offset:40960\n" \
"s_xor_b32 s5, s4, 15\n" \
M_"v[140:143], a[212:215], a[248:251], v[140:143]\n" \
X_"a[234:235], v34 offset:41984\n" \
M_"v[136:139], a[148:151], a[180:183], v[136:139]\n" \
"s_addk_i32 s84, 0x1c00\n" \
M_"v[136:139], a[212:215], a[244:247], v[136:139]\n" \
B_"v23, s[64:67], s84 offen offset:3072 lds\n" \
M_"v[132:135], a[148:151], a[176:179], v[132:135]\n" \
R_"a[148:151], v28 offset:34816\n" \
M_"v[132:135], a[212:215], a[240:243], v[132:135]\n" \
R_"a[212:215], v29 offset:34816\n" \
M_"v[148:151], a[152:155], a[176:179], v[148:151]\n" \
"s_add_u32 m0, s31, 0x4000\n" \
"s_mov_b32 s84, 0x200000\n" \
M_"v[148:151], a[216:219], a[240:243], v[148:151]\n" \
B_"v26, s[72:75], s84 offen offset:0 lds\n" \
M_"v[152:155], a[152:155], a[180:183], v[152:155]\n" \
X_"a[172:173], v35 offset:32768\n" \
M_"v[152:155], a[216:219], a[244:247], v[152:155]\n" \
X_"a[174:175], v35 offset:33792\n" \
M_"v[156:159], a[152:155], a[184:187], v[156:159]\n" \
"s_addk_i32 s84, 0x7c00\n" \
"s_nop 0\n" \
M_"v[156:159], a[216:219], a[248:251], v[156:159]\n" \
B_"v26, s[72:75], s84 offen offset:1024 lds\n" \
M_"v[160:163], a[152:155], a[188:191], v[160:163]\n" \
R_"a[152:155], v28 offset:36864\n" \
M_"v[160:163], a[216:219], a[252:255], v[160:163]\n" \
R_"a[216:219], v29 offset:36864\n" \
M_"v[176:179], a[156:159], a[188:191], v[176:179]\n" \
X_"a[236:237], v35 offset:40960\n" \
M_"v[176:179], a[220:223], a[252:255], v[176:179]\n" \
X_"a[238:239], v35 offset:41984\n" \
M_"v[172:175], a[156:159], a[184:187], v[172:175]\n" \
"s_cmpk_lt_u32 s3, 2\n" \
"s_cselect_b32 s6, s5, s4\n" \
M_"v[172:175], a[220:223], a[248:251], v[172:175]\n" \
"s_lshl_b32 s7, s6, 7\n" \
"v_and_b32_e32 v213, 0x320, v20\n" \
M_"v[168:171], a[156:159], a[180:183], v[168:171]\n" \
"s_movk_i32 s41, 0x100\n" \
"v_lshlrev_b32_e64 v214, v214, s42\n" \
M_"v[168:171], a[220:223], a[244:247], v[168:171]\n" \
"v_mov_b32_e32 v215, 0\n" \
"s_add_i32 s40, s7, -16\n" \
M_"v[164:167], a[156:159], a[176:179], v[164:167]\n" \
R_"a[156:159], v28 offset:38912\n" \
"v_cndmask_b32_e64 v41, v217, v216, vcc\n" \
M_"v[164:167], a[220:223], a[240:243], v[164:167]\n" \
R_"a[220:223], v29 offset:38912\n" \
"s_waitcnt lgkmcnt(15)\n" \
M_"a[0:3], a[128:131], a[160:163], a[0:3]\n" \
"s_addk_i32 s84, 0x7c00\n" \
M_"a[0:3], a[192:195], a[224:227], a[0:3]\n" \
B_"v27, s[72:75], s84 offen offset:2048 lds\n" \
M_"a[4:7], a[128:131], a[164:167], a[4:7]\n" \
X_"a[176:177], v36 offset:32768\n" \
"v_cndmask_b32_e64 v43, v216, v217, vcc\n" \
M_"a[4:7], a[192:195], a[228:231], a[4:7]\n" \
"s_addk_i32 s84, 0x7c00\n" \
"s_waitcnt lgkmcnt(15)\n" \
M_"a[8:11], a[128:131], a[168:171], a[8:11]\n" \
B_"v27, s[72:75], s84 offen offset:3072 lds\n" \
"s_waitcnt lgkmcnt(11)\n" \
M_"a[8:11], a[192:195], a[232:235], a[8:11]\n" \
X_"a[178:179], v36 offset:33792\n" \
"v_cmp_eq_u32_e32 vcc, s41, v213\n" \
"s_waitcnt lgkmcnt(8)\n" \
M_"a[12:15], a[128:131], a[172:175], a[12:15]\n" \
X_"a[240:241], v36 offset:40960\n" \
"s_mov_b32 s41, 0\n" \
"s_waitcnt lgkmcnt(5)\n" \
M_"a[12:15], a[192:195], a[236:239], a[12:15]\n" \
"s_and_b32 s43, s3, 1\n" \
"v_cndmask_b32_e64 v46, v215, v214, vcc\n" \
M_"a[28:31], a[132:135], a[172:175], a[28:31]\n" \
"v_and_b32_e32 v213, 0x330, v20\n" \
M_"a[28:31], a[196:199], a[236:239], a[28:31]\n" \
X_"a[242:243], v36 offset:41984\n" \
M_"a[24:27], a[132:135], a[168:171], a[24:27]\n" \
"s_cmp_lg_u32 s7, 0\n" \
"s_cselect_b32 s40, s40, s41\n" \
M_"a[24:27], a[196:199], a[232:235], a[24:27]\n" \
"s_mov_b32 s42, 0xff800000\n" \
M_"a[20:23], a[132:135], a[164:167], a[20:23]\n" \
"s_cmp_lg_u32 s7, 0\n" \
"s_cselect_b32 s47, s41, s42\n" \
M_"a[20:23], a[196:199], a[228:231], a[20:23]\n" \
X_"a[180:181], v37 offset:32768\n" \
"s_lshl_b32 s41, s43, 6\n" \
M_"a[16:19], a[132:135], a[160:163], a[16:19]\n" \
"v_lshrrev_b32_e32 v215, 6, v20\n" \
"v_mov_b32_e32 v214, 0x3f80\n" \
M_"a[16:19], a[196:199], a[224:227], a[16:19]\n" \
"v_cmp_eq_u32_e32 vcc, 32, v213\n" \
"s_or_b32 s7, s41, s7\n" \
M_"a[32:35], a[136:139], a[160:163], a[32:35]\n" \
X_"a[182:183], v37 offset:33792\n" \
"v_lshrrev_b32_e64 v213, 7, v20\n" \
M_"a[32:35], a[200:203], a[224:227], a[32:35]\n" \
"v_bfe_u32 v216, v20, 6, 2\n" \
"v_lshrrev_b32_e64 v219, 1, v20\n" \
M_"a[36:39], a[136:139], a[164:167], a[36:39]\n" \
"v_bfe_u32 v215, v215, 1, 1\n" \
"v_cndmask_b32_e64 v47, 0, v214, vcc\n" \
M_"a[36:39], a[200:203], a[228:231], a[36:39]\n" \
X_"a[244:245], v37 offset:40960\n" \
"v_or_b32_e64 v214, s7, v213\n" \
M_"a[40:43], a[136:139], a[168:171], a[40:43]\n" \
"s_mov_b32 m0, s32\n" \
"v_lshl_or_b32 v217, s43, 3, v213\n" \
M_"a[40:43], a[200:203], a[232:235], a[40:43]\n" \
"s_mov_b32 exec_lo, 0xffffffff\n" \
"s_mov_b32 exec_hi, 0x0\n" \
"global_load_lds_dword v[188:189], off\n" \
"s_mov_b64 exec, -1\n" \
M_"a[44:47], a[136:139], a[172:175], a[44:47]\n" \
X_"a[246:247], v37 offset:41984\n" \
"v_lshlrev_b32_e64 v218, 10, v212\n" \
M_"a[44:47], a[200:203], a[236:239], a[44:47]\n" \
"v_lshlrev_b32_e32 v216, 7, v216\n" \
"v_and_b32_e32 v219, 24, v219\n" \
M_"a[60:63], a[140:143], a[172:175], a[60:63]\n" \
"v_and_or_b32 v213, v213, 2, v215\n" \
"v_add_u32_e64 v217, s40, v217\n" \
M_"a[60:63], a[204:207], a[236:239], a[60:63]\n" \
"v_or3_b32 v216, v216, v218, v219\n" \
"v_lshlrev_b32_e64 v213, 5, v213\n" \
M_"a[56:59], a[140:143], a[168:171], a[56:59]\n" \
X_"a[184:185], v38 offset:32768\n" \
"s_mov_b32 s40, 0x14000\n" \
M_"a[56:59], a[204:207], a[232:235], a[56:59]\n" \
"s_lshl_b32 s7, s2, 2\n" \
"v_or3_b32 v48, v216, v213, s40\n" \
M_"a[52:55], a[140:143], a[164:167], a[52:55]\n" \
"s_lshl_b32 s40, s43, 2\n" \
"s_lshl_b32 s6, s6, 3\n" \
M_"a[52:55], a[204:207], a[228:231], a[52:55]\n" \
X_"a[186:187], v38 offset:33792\n" \
"v_or_b32_e32 v213, s40, v212\n" \
M_"a[48:51], a[140:143], a[160:163], a[48:51]\n" \
"s_or_b32 s6, s6, s40\n" \
"s_and_b32 s40, s7, 0x7c\n" \
M_"a[48:51], a[204:207], a[224:227], a[48:51]\n" \
"s_or_b32 s40, s40, s3\n" \
M_"a[64:67], a[144:147], a[160:163], a[64:67]\n" \
X_"a[248:249], v38 offset:40960\n" \
M_"a[64:67], a[208:211], a[224:227], a[64:67]\n" \
"s_cmpk_lt_u32 s40, 64\n" \
"s_cselect_b32 s41, s4, s5\n" \
M_"a[68:71], a[144:147], a[164:167], a[68:71]\n" \
"s_lshl_b32 s40, s40, 1\n" \
"s_lshl_b32 s41, s41, 7\n" \
M_"a[68:71], a[208:211], a[228:231], a[68:71]\n" \
X_"a[250:251], v38 offset:41984\n" \
"s_and_b32 s40, s40, 0x7e\n" \
M_"a[72:75], a[144:147], a[168:171], a[72:75]\n" \
"s_or_b32 s40, s41, s40\n" \
"s_mulk_i32 s40, 0x480\n" \
M_"a[72:75], a[208:211], a[232:235], a[72:75]\n" \
"v_add_u32_e64 v49, s40, v20\n" \
"s_mov_b32 s40, 0xc000\n" \
M_"a[76:79], a[144:147], a[172:175], a[76:79]\n" \
"s_and_b32 s7, s7, 28\n" \
"s_bfe_u32 s45, s2, 0x20003\n" \
M_"a[76:79], a[208:211], a[236:239], a[76:79]\n" \
X_"a[188:189], v39 offset:32768\n" \
"s_mov_b32 s41, 0x8000\n" \
M_"a[92:95], a[148:151], a[172:175], a[92:95]\n" \
"s_cmp_lg_u32 s4, 0\n" \
"s_cselect_b32 s51, s40, s41\n" \
M_"a[92:95], a[212:215], a[236:239], a[92:95]\n" \
"s_movk_i32 s40, 0x4000\n" \
"s_lshr_b32 s44, s3, 1\n" \
M_"a[88:91], a[148:151], a[168:171], a[88:91]\n" \
X_"a[190:191], v39 offset:33792\n" \
"s_or_b32 s46, s7, s45\n" \
M_"a[88:91], a[212:215], a[232:235], a[88:91]\n" \
"s_cmp_lg_u32 s4, 0\n" \
"s_cselect_b32 s53, s41, s40\n" \
M_"a[84:87], a[148:151], a[164:167], a[84:87]\n" \
"s_waitcnt vmcnt(17)\n" \
"v_or_b32_e32 v204, v210, v204\n" \
"v_or_b32_e64 v206, v210, v206\n" \
M_"a[84:87], a[212:215], a[228:231], a[84:87]\n" \
"v_mov_b32_e64 v210, 0\n" \
"s_add_u32 s88, s35, 0x1000\n" \
M_"a[80:83], a[148:151], a[160:163], a[80:83]\n" \
X_"a[252:253], v39 offset:40960\n" \
"s_lshl_b32 s40, s44, 15\n" \
M_"a[80:83], a[212:215], a[224:227], a[80:83]\n" \
"ds_write_b128 v196, v[200:203]\n" \
M_"a[96:99], a[152:155], a[160:163], a[96:99]\n" \
X_"a[254:255], v39 offset:41984\n" \
"s_lshl_b32 s41, s43, 14\n" \
M_"a[96:99], a[216:219], a[224:227], a[96:99]\n" \
"s_lshl_b32 s48, s46, 20\n" \
"s_or_b32 s41, s40, s41\n" \
M_"a[100:103], a[152:155], a[164:167], a[100:103]\n" \
"ds_write_b128 v197, v[204:207]\n" \
M_"a[100:103], a[216:219], a[228:231], a[100:103]\n" \
"s_add_i32 s49, s48, 0x1000000\n" \
"s_or_b32 s42, s49, s41\n" \
M_"a[104:107], a[152:155], a[168:171], a[104:107]\n" \
"s_lshl_b32 s4, s4, 16\n" \
"s_or_b32 s41, s42, s4\n" \
M_"a[104:107], a[216:219], a[232:235], a[104:107]\n" \
"ds_write_b128 v198, v[208:211]\n" \
M_"a[108:111], a[152:155], a[172:175], a[108:111]\n" \
"s_lshl_b32 s4, s5, 16\n" \
"s_mul_i32 s5, s6, 0x24800\n" \
M_"a[108:111], a[216:219], a[236:239], a[108:111]\n" \
"s_lshl_b32 s6, s46, 9\n" \
"s_lshl_b32 s7, s7, 6\n" \
"s_waitcnt lgkmcnt(15)\n" \
M_"a[124:127], a[156:159], a[172:175], a[124:127]\n" \
"s_add_i32 s6, s5, s6\n" \
"s_or_b32 s5, s5, s7\n" \
M_"a[124:127], a[220:223], a[236:239], a[124:127]\n" \
"s_lshl_b32 s7, s45, 2\n" \
"s_or_b32 s5, s5, s7\n" \
M_"a[120:123], a[156:159], a[168:171], a[120:123]\n" \
"s_lshl_b32 s7, s3, 12\n" \
"s_or_b32 s50, s49, s7\n" \
M_"a[120:123], a[220:223], a[232:235], a[120:123]\n" \
"s_lshl_b32 s7, s3, 11\n" \
"v_lshrrev_b32_e64 v215, 4, v20\n" \
M_"a[116:119], a[156:159], a[164:167], a[116:119]\n" \
"v_bfe_u32 v216, v20, 4, 3\n" \
"s_or_b32 s42, s42, s4\n" \
M_"a[116:119], a[220:223], a[228:231], a[116:119]\n" \
"s_lshl_b32 s4, s43, 11\n" \
"s_add_i32 s52, s48, s7\n" \
M_"a[112:115], a[156:159], a[160:163], a[112:115]\n" \
"s_add_i32 s53, s53, s7\n" \
"v_mul_u32_u24_e32 v214, 0x480, v214\n" \
"s_waitcnt vmcnt(1) lgkmcnt(0)\n" \
M_"a[112:115], a[220:223], a[224:227], a[112:115]\n" \
"s_barrier\n" \
M_"v[52:55], a[128:131], a[176:179], v[52:55]\n" \
"s_add_u32 m0, s30, 0x8000\n" \
"s_movk_i32 s84, 0x280\n" \
M_"v[52:55], a[192:195], a[240:243], v[52:55]\n" \
B_"v22, s[64:67], s84 offen offset:0 lds\n" \
M_"v[56:59], a[128:131], a[180:183], v[56:59]\n" \
X_"a[160:161], v32 offset:0\n" \
"v_bitop3_b32 v218, v212, v215, 7 bitop3:0x78\n" \
M_"v[56:59], a[192:195], a[244:247], v[56:59]\n" \
"s_movk_i32 s84, 0x1e80\n" \
M_"v[60:63], a[128:131], a[184:187], v[60:63]\n" \
B_"v23, s[64:67], s84 offen offset:1024 lds\n" \
M_"v[60:63], a[192:195], a[248:251], v[60:63]\n" \
X_"a[162:163], v32 offset:1024\n" \
"v_bitop3_b32 v212, v212, v216, 4 bitop3:0x36\n" \
M_"v[64:67], a[128:131], a[188:191], v[64:67]\n" \
R_"a[128:131], v28 offset:16384\n" \
"s_lshl_b32 s7, s3, 10\n" \
M_"v[64:67], a[192:195], a[252:255], v[64:67]\n" \
R_"a[192:195], v29 offset:16384\n" \
"s_or_b32 s4, s40, s4\n" \
M_"v[80:83], a[132:135], a[188:191], v[80:83]\n" \
"s_movk_i32 s84, 0x3a80\n" \
M_"v[80:83], a[196:199], a[252:255], v[80:83]\n" \
B_"v22, s[64:67], s84 offen offset:2048 lds\n" \
M_"v[76:79], a[132:135], a[184:187], v[76:79]\n" \
"s_movk_i32 s84, 0x5680\n" \
M_"v[76:79], a[196:199], a[248:251], v[76:79]\n" \
B_"v23, s[64:67], s84 offen offset:3072 lds\n" \
M_"v[72:75], a[132:135], a[180:183], v[72:75]\n" \
X_"a[224:225], v32 offset:8192\n" \
"v_lshl_or_b32 v25, v212, 4, v214\n" \
M_"v[72:75], a[196:199], a[244:247], v[72:75]\n" \
X_"a[226:227], v32 offset:9216\n" \
"v_mul_u32_u24_e32 v212, 0x480, v217\n" \
M_"v[68:71], a[132:135], a[176:179], v[68:71]\n" \
R_"a[132:135], v28 offset:18432\n" \
"v_bitop3_b32 v213, v215, v213, 7 bitop3:0x6c\n" \
M_"v[68:71], a[196:199], a[240:243], v[68:71]\n" \
R_"a[196:199], v29 offset:18432\n" \
"s_or_b32 s40, s7, 0x14000\n" \
M_"v[84:87], a[136:139], a[176:179], v[84:87]\n" \
"s_add_u32 m0, s31, 0x8000\n" \
"s_mov_b32 s84, 0x280000\n" \
M_"v[84:87], a[200:203], a[240:243], v[84:87]\n" \
B_"v26, s[68:71], s84 offen offset:0 lds\n" \
M_"v[88:91], a[136:139], a[180:183], v[88:91]\n" \
"s_addk_i32 s84, 0x7c00\n" \
M_"v[88:91], a[200:203], a[244:247], v[88:91]\n" \
B_"v26, s[68:71], s84 offen offset:1024 lds\n" \
M_"v[92:95], a[136:139], a[184:187], v[92:95]\n" \
"s_addk_i32 s84, 0x7c00\n" \
M_"v[92:95], a[200:203], a[248:251], v[92:95]\n" \
B_"v27, s[68:71], s84 offen offset:2048 lds\n" \
M_"v[96:99], a[136:139], a[188:191], v[96:99]\n" \
R_"a[136:139], v28 offset:20480\n" \
"s_lshl_b32 s43, s44, 13\n" \
M_"v[96:99], a[200:203], a[252:255], v[96:99]\n" \
R_"a[200:203], v29 offset:20480\n" \
"s_mulk_i32 s44, 0xe800\n" \
M_"v[112:115], a[140:143], a[188:191], v[112:115]\n" \
"s_addk_i32 s84, 0x7c00\n" \
M_"v[112:115], a[204:207], a[252:255], v[112:115]\n" \
B_"v27, s[68:71], s84 offen offset:3072 lds\n" \
M_"v[108:111], a[140:143], a[184:187], v[108:111]\n" \
"s_add_u32 m0, s30, 0x9000\n" \
"s_mov_b32 s84, 0x8280\n" \
M_"v[108:111], a[204:207], a[248:251], v[108:111]\n" \
B_"v22, s[64:67], s84 offen offset:0 lds\n" \
M_"v[104:107], a[140:143], a[180:183], v[104:107]\n" \
X_"a[164:165], v33 offset:0\n" \
"s_lshl_b32 s7, s3, 8\n" \
M_"v[104:107], a[204:207], a[244:247], v[104:107]\n" \
X_"a[166:167], v33 offset:1024\n" \
"v_lshl_or_b32 v24, v218, 4, v214\n" \
M_"v[100:103], a[140:143], a[176:179], v[100:103]\n" \
R_"a[140:143], v28 offset:22528\n" \
"v_lshl_or_b32 v21, v213, 4, v212\n" \
M_"v[100:103], a[204:207], a[240:243], v[100:103]\n" \
R_"a[204:207], v29 offset:22528\n" \
"s_add_i32 s44, s44, 0x17000\n" \
M_"v[116:119], a[144:147], a[176:179], v[116:119]\n" \
"s_addk_i32 s84, 0x1c00\n" \
M_"v[116:119], a[208:211], a[240:243], v[116:119]\n" \
B_"v23, s[64:67], s84 offen offset:1024 lds\n" \
M_"v[120:123], a[144:147], a[180:183], v[120:123]\n" \
"s_addk_i32 s84, 0x1c00\n" \
M_"v[120:123], a[208:211], a[244:247], v[120:123]\n" \
B_"v22, s[64:67], s84 offen offset:2048 lds\n" \
M_"v[124:127], a[144:147], a[184:187], v[124:127]\n" \
"s_addk_i32 s84, 0x1c00\n" \
"s_nop 0\n" \
M_"v[124:127], a[208:211], a[248:251], v[124:127]\n" \
B_"v23, s[64:67], s84 offen offset:3072 lds\n" \
M_"v[128:131], a[144:147], a[188:191], v[128:131]\n" \
R_"a[144:147], v28 offset:0\n" \
"s_or_b32 s45, s7, 0x27000\n" \
M_"v[128:131], a[208:211], a[252:255], v[128:131]\n" \
R_"a[208:211], v29 offset:0\n" \
"s_or_b32 s46, s4, 0x10000\n" \
M_"v[144:147], a[148:151], a[188:191], v[144:147]\n" \
"s_add_u32 m0, s31, 0xc000\n" \
"s_mov_b32 s84, 0x280000\n" \
M_"v[144:147], a[212:215], a[252:255], v[144:147]\n" \
B_"v26, s[72:75], s84 offen offset:0 lds\n" \
M_"v[140:143], a[148:151], a[184:187], v[140:143]\n" \
"s_addk_i32 s84, 0x7c00\n" \
"s_nop 0\n" \
M_"v[140:143], a[212:215], a[248:251], v[140:143]\n" \
B_"v26, s[72:75], s84 offen offset:1024 lds\n" \
M_"v[136:139], a[148:151], a[180:183], v[136:139]\n" \
X_"a[228:229], v33 offset:8192\n" \
"s_add_i32 s48, s6, 0x3000000\n" \
M_"v[136:139], a[212:215], a[244:247], v[136:139]\n" \
X_"a[230:231], v33 offset:9216\n" \
"s_add_i32 s49, s5, 0x3004000\n" \
M_"v[132:135], a[148:151], a[176:179], v[132:135]\n" \
R_"a[148:151], v28 offset:2048\n" \
"s_add_i32 s52, s52, 0x1010000\n" \
M_"v[132:135], a[212:215], a[240:243], v[132:135]\n" \
R_"a[212:215], v29 offset:2048\n" \
M_"v[148:151], a[152:155], a[176:179], v[148:151]\n" \
R_"v[200:203], v199 offset:0\n" \
M_"v[148:151], a[216:219], a[240:243], v[148:151]\n" \
R_"v[204:207], v199 offset:4096\n" \
M_"v[152:155], a[152:155], a[180:183], v[152:155]\n" \
X_"a[168:169], v34 offset:0\n" \
M_"v[152:155], a[216:219], a[244:247], v[152:155]\n" \
X_"a[170:171], v34 offset:1024\n" \
M_"v[156:159], a[152:155], a[184:187], v[156:159]\n" \
R_"v[208:211], v199 offset:8192\n" \
M_"v[156:159], a[216:219], a[248:251], v[156:159]\n" \
X_"a[232:233], v34 offset:8192\n" \
M_"v[160:163], a[152:155], a[188:191], v[160:163]\n" \
R_"a[152:155], v28 offset:4096\n" \
M_"v[160:163], a[216:219], a[252:255], v[160:163]\n" \
R_"a[216:219], v29 offset:4096\n" \
M_"v[176:179], a[156:159], a[188:191], v[176:179]\n" \
X_"a[234:235], v34 offset:9216\n" \
M_"v[176:179], a[220:223], a[252:255], v[176:179]\n" \
X_"a[172:173], v35 offset:0\n" \
M_"v[172:175], a[156:159], a[184:187], v[172:175]\n" \
X_"a[174:175], v35 offset:1024\n" \
M_"v[172:175], a[220:223], a[248:251], v[172:175]\n" \
X_"a[236:237], v35 offset:8192\n" \
M_"v[168:171], a[156:159], a[180:183], v[168:171]\n" \
X_"a[238:239], v35 offset:9216\n" \
M_"v[168:171], a[220:223], a[244:247], v[168:171]\n" \
M_"v[164:167], a[156:159], a[176:179], v[164:167]\n" \
R_"a[156:159], v28 offset:6144\n" \
M_"v[164:167], a[220:223], a[240:243], v[164:167]\n" \
R_"a[220:223], v29 offset:6144\n" \
"s_waitcnt lgkmcnt(15)\n" \
M_"a[0:3], a[128:131], a[160:163], a[0:3]\n" \
"s_addk_i32 s84, 0x7c00\n" \
M_"a[0:3], a[192:195], a[224:227], a[0:3]\n" \
B_"v27, s[72:75], s84 offen offset:2048 lds\n" \
M_"a[4:7], a[128:131], a[164:167], a[4:7]\n" \
X_"a[176:177], v36 offset:0\n" \
M_"a[4:7], a[192:195], a[228:231], a[4:7]\n" \
"s_addk_i32 s84, 0x7c00\n" \
"s_waitcnt lgkmcnt(12)\n" \
M_"a[8:11], a[128:131], a[168:171], a[8:11]\n" \
B_"v27, s[72:75], s84 offen offset:3072 lds\n" \
"s_waitcnt lgkmcnt(7)\n" \
M_"a[8:11], a[192:195], a[232:235], a[8:11]\n" \
X_"a[178:179], v36 offset:1024\n" \
"s_waitcnt lgkmcnt(6)\n" \
M_"a[12:15], a[128:131], a[172:175], a[12:15]\n" \
X_"a[240:241], v36 offset:8192\n" \
"s_waitcnt lgkmcnt(5)\n" \
"s_nop 0\n" \
M_"a[12:15], a[192:195], a[236:239], a[12:15]\n" \
M_"a[28:31], a[132:135], a[172:175], a[28:31]\n" \
M_"a[28:31], a[196:199], a[236:239], a[28:31]\n" \
X_"a[242:243], v36 offset:9216\n" \
M_"a[24:27], a[132:135], a[168:171], a[24:27]\n" \
M_"a[24:27], a[196:199], a[232:235], a[24:27]\n" \
M_"a[20:23], a[132:135], a[164:167], a[20:23]\n" \
".fill (L_pb + 52960 - .) / 4, 4, 0xBF800000\n" \
"L_pp13:\n" \
"s_cbranch_execz L_pret\n" \
"s_nop 0\n" \
M_"a[20:23], a[196:199], a[228:231], a[20:23]\n" \
X_"a[180:181], v37 offset:0\n" \
M_"a[16:19], a[132:135], a[160:163], a[16:19]\n" \
M_"a[16:19], a[196:199], a[224:227], a[16:19]\n" \
M_"a[32:35], a[136:139], a[160:163], a[32:35]\n" \
X_"a[182:183], v37 offset:1024\n" \
M_"a[32:35], a[200:203], a[224:227], a[32:35]\n" \
M_"a[36:39], a[136:139], a[164:167], a[36:39]\n" \
M_"a[36:39], a[200:203], a[228:231], a[36:39]\n" \
X_"a[244:245], v37 offset:8192\n" \
M_"a[40:43], a[136:139], a[168:171], a[40:43]\n" \
"s_mov_b32 m0, s32\n" \
M_"a[40:43], a[200:203], a[232:235], a[40:43]\n" \
"s_mov_b32 exec_lo, 0x0\n" \
"s_mov_b32 exec_hi, 0xffffffff\n" \
"global_load_lds_dword v[188:189], off\n" \
"s_mov_b64 exec, -1\n" \
M_"a[44:47], a[136:139], a[172:175], a[44:47]\n" \
"buffer_store_dwordx4 v[200:203], v20, s[80:83], s35 offen sc1\n" \
M_"a[44:47], a[200:203], a[236:239], a[44:47]\n" \
X_"a[246:247], v37 offset:9216\n" \
M_"a[60:63], a[140:143], a[172:175], a[60:63]\n" \
M_"a[60:63], a[204:207], a[236:239], a[60:63]\n" \
M_"a[56:59], a[140:143], a[168:171], a[56:59]\n" \
X_"a[184:185], v38 offset:0\n" \
M_"a[56:59], a[204:207], a[232:235], a[56:59]\n" \
M_"a[52:55], a[140:143], a[164:167], a[52:55]\n" \
M_"a[52:55], a[204:207], a[228:231], a[52:55]\n" \
X_"a[186:187], v38 offset:1024\n" \
M_"a[48:51], a[140:143], a[160:163], a[48:51]\n" \
M_"a[48:51], a[204:207], a[224:227], a[48:51]\n" \
M_"a[64:67], a[144:147], a[160:163], a[64:67]\n" \
X_"a[248:249], v38 offset:8192\n" \
M_"a[64:67], a[208:211], a[224:227], a[64:67]\n" \
"buffer_store_dwordx4 v[204:207], v20, s[80:83], s88 offen sc1\n" \
M_"a[68:71], a[144:147], a[164:167], a[68:71]\n" \
M_"a[68:71], a[208:211], a[228:231], a[68:71]\n" \
X_"a[250:251], v38 offset:9216\n" \
M_"a[72:75], a[144:147], a[168:171], a[72:75]\n" \
M_"a[72:75], a[208:211], a[232:235], a[72:75]\n" \
M_"a[76:79], a[144:147], a[172:175], a[76:79]\n" \
M_"a[76:79], a[208:211], a[236:239], a[76:79]\n" \
X_"a[188:189], v39 offset:0\n" \
M_"a[92:95], a[148:151], a[172:175], a[92:95]\n" \
M_"a[92:95], a[212:215], a[236:239], a[92:95]\n" \
M_"a[88:91], a[148:151], a[168:171], a[88:91]\n" \
X_"a[190:191], v39 offset:1024\n" \
M_"a[88:91], a[212:215], a[232:235], a[88:91]\n" \
M_"a[84:87], a[148:151], a[164:167], a[84:87]\n" \
"buffer_store_dwordx4 v[208:211], v20, s[80:83], s36 offen sc1\n" \
M_"a[84:87], a[212:215], a[228:231], a[84:87]\n" \
X_"a[252:253], v39 offset:8192\n" \
M_"a[80:83], a[148:151], a[160:163], a[80:83]\n" \
M_"a[80:83], a[212:215], a[224:227], a[80:83]\n" \
M_"a[96:99], a[152:155], a[160:163], a[96:99]\n" \
X_"a[254:255], v39 offset:9216\n" \
M_"a[96:99], a[216:219], a[224:227], a[96:99]\n" \
M_"a[100:103], a[152:155], a[164:167], a[100:103]\n" \
M_"a[100:103], a[216:219], a[228:231], a[100:103]\n" \
M_"a[104:107], a[152:155], a[168:171], a[104:107]\n" \
M_"a[104:107], a[216:219], a[232:235], a[104:107]\n" \
M_"a[108:111], a[152:155], a[172:175], a[108:111]\n" \
M_"a[108:111], a[216:219], a[236:239], a[108:111]\n" \
"s_waitcnt lgkmcnt(15)\n" \
M_"a[124:127], a[156:159], a[172:175], a[124:127]\n" \
M_"a[124:127], a[220:223], a[236:239], a[124:127]\n" \
M_"a[120:123], a[156:159], a[168:171], a[120:123]\n" \
M_"a[120:123], a[220:223], a[232:235], a[120:123]\n" \
M_"a[116:119], a[156:159], a[164:167], a[116:119]\n" \
M_"a[116:119], a[220:223], a[228:231], a[116:119]\n" \
M_"a[112:115], a[156:159], a[160:163], a[112:115]\n" \
"s_waitcnt vmcnt(4) lgkmcnt(0)\n" \
M_"a[112:115], a[220:223], a[224:227], a[112:115]\n" \
"s_barrier\n" \
M_"v[52:55], a[128:131], a[176:179], v[52:55]\n" \
"s_add_u32 m0, s30, 0x0\n" \
"s_movk_i32 s84, 0x300\n" \
M_"v[52:55], a[192:195], a[240:243], v[52:55]\n" \
B_"v22, s[64:67], s84 offen offset:0 lds\n" \
M_"v[56:59], a[128:131], a[180:183], v[56:59]\n" \
X_"a[160:161], v32 offset:32768\n" \
M_"v[56:59], a[192:195], a[244:247], v[56:59]\n" \
X_"a[162:163], v32 offset:33792\n" \
M_"v[60:63], a[128:131], a[184:187], v[60:63]\n" \
"s_movk_i32 s84, 0x1f00\n" \
M_"v[60:63], a[192:195], a[248:251], v[60:63]\n" \
B_"v23, s[64:67], s84 offen offset:1024 lds\n" \
M_"v[64:67], a[128:131], a[188:191], v[64:67]\n" \
R_"a[128:131], v28 offset:49152\n" \
M_"v[64:67], a[192:195], a[252:255], v[64:67]\n" \
R_"a[192:195], v29 offset:49152\n" \
M_"v[80:83], a[132:135], a[188:191], v[80:83]\n" \
"s_movk_i32 s84, 0x3b00\n" \
M_"v[80:83], a[196:199], a[252:255], v[80:83]\n" \
B_"v22, s[64:67], s84 offen offset:2048 lds\n" \
M_"v[76:79], a[132:135], a[184:187], v[76:79]\n" \
X_"a[224:225], v32 offset:40960\n" \
M_"v[76:79], a[196:199], a[248:251], v[76:79]\n" \
X_"a[226:227], v32 offset:41984\n" \
M_"v[72:75], a[132:135], a[180:183], v[72:75]\n" \
"s_movk_i32 s84, 0x5700\n" \
M_"v[72:75], a[196:199], a[244:247], v[72:75]\n" \
B_"v23, s[64:67], s84 offen offset:3072 lds\n" \
M_"v[68:71], a[132:135], a[176:179], v[68:71]\n" \
R_"a[132:135], v28 offset:51200\n" \
M_"v[68:71], a[196:199], a[240:243], v[68:71]\n" \
R_"a[196:199], v29 offset:51200\n" \
M_"v[84:87], a[136:139], a[176:179], v[84:87]\n" \
"s_add_u32 m0, s31, 0x0\n" \
"s_mov_b32 s84, 0x300000\n" \
M_"v[84:87], a[200:203], a[240:243], v[84:87]\n" \
B_"v26, s[68:71], s84 offen offset:0 lds\n" \
M_"v[88:91], a[136:139], a[180:183], v[88:91]\n" \
X_"a[164:165], v33 offset:32768\n" \
M_"v[88:91], a[200:203], a[244:247], v[88:91]\n" \
X_"a[166:167], v33 offset:33792\n" \
M_"v[92:95], a[136:139], a[184:187], v[92:95]\n" \
"s_addk_i32 s84, 0x7c00\n" \
M_"v[92:95], a[200:203], a[248:251], v[92:95]\n" \
B_"v26, s[68:71], s84 offen offset:1024 lds\n" \
M_"v[96:99], a[136:139], a[188:191], v[96:99]\n" \
R_"a[136:139], v28 offset:53248\n" \
M_"v[96:99], a[200:203], a[252:255], v[96:99]\n" \
R_"a[200:203], v29 offset:53248\n" \
M_"v[112:115], a[140:143], a[188:191], v[112:115]\n" \
"s_addk_i32 s84, 0x7c00\n" \
M_"v[112:115], a[204:207], a[252:255], v[112:115]\n" \
B_"v27, s[68:71], s84 offen offset:2048 lds\n" \
M_"v[108:111], a[140:143], a[184:187], v[108:111]\n" \
X_"a[228:229], v33 offset:40960\n" \
M_"v[108:111], a[204:207], a[248:251], v[108:111]\n" \
X_"a[230:231], v33 offset:41984\n" \
M_"v[104:107], a[140:143], a[180:183], v[104:107]\n" \
"s_addk_i32 s84, 0x7c00\n" \
M_"v[104:107], a[204:207], a[244:247], v[104:107]\n" \
B_"v27, s[68:71], s84 offen offset:3072 lds\n" \
M_"v[100:103], a[140:143], a[176:179], v[100:103]\n" \
R_"a[140:143], v28 offset:55296\n" \
M_"v[100:103], a[204:207], a[240:243], v[100:103]\n" \
R_"a[204:207], v29 offset:55296\n" \
M_"v[116:119], a[144:147], a[176:179], v[116:119]\n" \
"s_add_u32 m0, s30, 0x1000\n" \
"s_mov_b32 s84, 0x8300\n" \
M_"v[116:119], a[208:211], a[240:243], v[116:119]\n" \
B_"v22, s[64:67], s84 offen offset:0 lds\n" \
M_"v[120:123], a[144:147], a[180:183], v[120:123]\n" \
X_"a[168:169], v34 offset:32768\n" \
M_"v[120:123], a[208:211], a[244:247], v[120:123]\n" \
X_"a[170:171], v34 offset:33792\n" \
M_"v[124:127], a[144:147], a[184:187], v[124:127]\n" \
"s_addk_i32 s84, 0x1c00\n" \
M_"v[124:127], a[208:211], a[248:251], v[124:127]\n" \
B_"v23, s[64:67], s84 offen offset:1024 lds\n" \
M_"v[128:131], a[144:147], a[188:191], v[128:131]\n" \
R_"a[144:147], v28 offset:32768\n" \
M_"v[128:131], a[208:211], a[252:255], v[128:131]\n" \
R_"a[208:211], v29 offset:32768\n" \
M_"v[144:147], a[148:151], a[188:191], v[144:147]\n" \
"s_addk_i32 s84, 0x1c00\n" \
M_"v[144:147], a[212:215], a[252:255], v[144:147]\n" \
B_"v22, s[64:67], s84 offen offset:2048 lds\n" \
M_"v[140:143], a[148:151], a[184:187], v[140:143]\n" \
X_"a[232:233], v34 offset:40960\n" \
M_"v[140:143], a[212:215], a[248:251], v[140:143]\n" \
X_"a[234:235], v34 offset:41984\n" \
M_"v[136:139], a[148:151], a[180:183], v[136:139]\n" \
"s_addk_i32 s84, 0x1c00\n" \
"s_nop 0\n" \
M_"v[136:139], a[212:215], a[244:247], v[136:139]\n" \
B_"v23, s[64:67], s84 offen offset:3072 lds\n" \
M_"v[132:135], a[148:151], a[176:179], v[132:135]\n" \
R_"a[148:151], v28 offset:34816\n" \
M_"v[132:135], a[212:215], a[240:243], v[132:135]\n" \
R_"a[212:215], v29 offset:34816\n" \
M_"v[148:151], a[152:155], a[176:179], v[148:151]\n" \
"s_add_u32 m0, s31, 0x4000\n" \
"s_mov_b32 s84, 0x300000\n" \
M_"v[148:151], a[216:219], a[240:243], v[148:151]\n" \
B_"v26, s[72:75], s84 offen offset:0 lds\n" \
M_"v[152:155], a[152:155], a[180:183], v[152:155]\n" \
X_"a[172:173], v35 offset:32768\n" \
M_"v[152:155], a[216:219], a[244:247], v[152:155]\n" \
X_"a[174:175], v35 offset:33792\n" \
M_"v[156:159], a[152:155], a[184:187], v[156:159]\n" \
"s_addk_i32 s84, 0x7c00\n" \
"s_nop 0\n" \
M_"v[156:159], a[216:219], a[248:251], v[156:159]\n" \
B_"v26, s[72:75], s84 offen offset:1024 lds\n" \
M_"v[160:163], a[152:155], a[188:191], v[160:163]\n" \
R_"a[152:155], v28 offset:36864\n" \
M_"v[160:163], a[216:219], a[252:255], v[160:163]\n" \
R_"a[216:219], v29 offset:36864\n" \
M_"v[176:179], a[156:159], a[188:191], v[176:179]\n" \
X_"a[236:237], v35 offset:40960\n" \
M_"v[176:179], a[220:223], a[252:255], v[176:179]\n" \
X_"a[238:239], v35 offset:41984\n" \
M_"v[172:175], a[156:159], a[184:187], v[172:175]\n" \
M_"v[172:175], a[220:223], a[248:251], v[172:175]\n" \
M_"v[168:171], a[156:159], a[180:183], v[168:171]\n" \
M_"v[168:171], a[220:223], a[244:247], v[168:171]\n" \
M_"v[164:167], a[156:159], a[176:179], v[164:167]\n" \
R_"a[156:159], v28 offset:38912\n" \
M_"v[164:167], a[220:223], a[240:243], v[164:167]\n" \
R_"a[220:223], v29 offset:38912\n" \
"s_waitcnt lgkmcnt(15)\n" \
M_"a[0:3], a[128:131], a[160:163], a[0:3]\n" \
"s_addk_i32 s84, 0x7c00\n" \
M_"a[0:3], a[192:195], a[224:227], a[0:3]\n" \
B_"v27, s[72:75], s84 offen offset:2048 lds\n" \
M_"a[4:7], a[128:131], a[164:167], a[4:7]\n" \
X_"a[176:177], v36 offset:32768\n" \
M_"a[4:7], a[192:195], a[228:231], a[4:7]\n" \
"s_addk_i32 s84, 0x7c00\n" \
"s_waitcnt lgkmcnt(15)\n" \
M_"a[8:11], a[128:131], a[168:171], a[8:11]\n" \
B_"v27, s[72:75], s84 offen offset:3072 lds\n" \
"s_waitcnt lgkmcnt(11)\n" \
M_"a[8:11], a[192:195], a[232:235], a[8:11]\n" \
X_"a[178:179], v36 offset:33792\n" \
"s_waitcnt lgkmcnt(8)\n" \
M_"a[12:15], a[128:131], a[172:175], a[12:15]\n" \
X_"a[240:241], v36 offset:40960\n" \
"s_waitcnt lgkmcnt(5)\n" \
"s_nop 0\n" \
M_"a[12:15], a[192:195], a[236:239], a[12:15]\n" \
M_"a[28:31], a[132:135], a[172:175], a[28:31]\n" \
M_"a[28:31], a[196:199], a[236:239], a[28:31]\n" \
X_"a[242:243], v36 offset:41984\n" \
M_"a[24:27], a[132:135], a[168:171], a[24:27]\n" \
M_"a[24:27], a[196:199], a[232:235], a[24:27]\n" \
M_"a[20:23], a[132:135], a[164:167], a[20:23]\n" \
M_"a[20:23], a[196:199], a[228:231], a[20:23]\n" \
X_"a[180:181], v37 offset:32768\n" \
M_"a[16:19], a[132:135], a[160:163], a[16:19]\n" \
M_"a[16:19], a[196:199], a[224:227], a[16:19]\n" \
M_"a[32:35], a[136:139], a[160:163], a[32:35]\n" \
X_"a[182:183], v37 offset:33792\n" \
M_"a[32:35], a[200:203], a[224:227], a[32:35]\n" \
M_"a[36:39], a[136:139], a[164:167], a[36:39]\n" \
M_"a[36:39], a[200:203], a[228:231], a[36:39]\n" \
X_"a[244:245], v37 offset:40960\n" \
M_"a[40:43], a[136:139], a[168:171], a[40:43]\n" \
M_"a[40:43], a[200:203], a[232:235], a[40:43]\n" \
M_"a[44:47], a[136:139], a[172:175], a[44:47]\n" \
X_"a[246:247], v37 offset:41984\n" \
M_"a[44:47], a[200:203], a[236:239], a[44:47]\n" \
M_"a[60:63], a[140:143], a[172:175], a[60:63]\n" \
M_"a[60:63], a[204:207], a[236:239], a[60:63]\n" \
M_"a[56:59], a[140:143], a[168:171], a[56:59]\n" \
X_"a[184:185], v38 offset:32768\n" \
M_"a[56:59], a[204:207], a[232:235], a[56:59]\n" \
M_"a[52:55], a[140:143], a[164:167], a[52:55]\n" \
M_"a[52:55], a[204:207], a[228:231], a[52:55]\n" \
X_"a[186:187], v38 offset:33792\n" \
M_"a[48:51], a[140:143], a[160:163], a[48:51]\n" \
M_"a[48:51], a[204:207], a[224:227], a[48:51]\n" \
M_"a[64:67], a[144:147], a[160:163], a[64:67]\n" \
X_"a[248:249], v38 offset:40960\n" \
M_"a[64:67], a[208:211], a[224:227], a[64:67]\n" \
M_"a[68:71], a[144:147], a[164:167], a[68:71]\n" \
M_"a[68:71], a[208:211], a[228:231], a[68:71]\n" \
X_"a[250:251], v38 offset:41984\n" \
M_"a[72:75], a[144:147], a[168:171], a[72:75]\n" \
M_"a[72:75], a[208:211], a[232:235], a[72:75]\n" \
M_"a[76:79], a[144:147], a[172:175], a[76:79]\n" \
M_"a[76:79], a[208:211], a[236:239], a[76:79]\n" \
X_"a[188:189], v39 offset:32768\n" \
M_"a[92:95], a[148:151], a[172:175], a[92:95]\n" \
M_"a[92:95], a[212:215], a[236:239], a[92:95]\n" \
M_"a[88:91], a[148:151], a[168:171], a[88:91]\n" \
X_"a[190:191], v39 offset:33792\n" \
M_"a[88:91], a[212:215], a[232:235], a[88:91]\n" \
M_"a[84:87], a[148:151], a[164:167], a[84:87]\n" \
M_"a[84:87], a[212:215], a[228:231], a[84:87]\n" \
X_"a[252:253], v39 offset:40960\n" \
M_"a[80:83], a[148:151], a[160:163], a[80:83]\n" \
M_"a[80:83], a[212:215], a[224:227], a[80:83]\n" \
M_"a[96:99], a[152:155], a[160:163], a[96:99]\n" \
X_"a[254:255], v39 offset:41984\n" \
M_"a[96:99], a[216:219], a[224:227], a[96:99]\n" \
M_"a[100:103], a[152:155], a[164:167], a[100:103]\n" \
M_"a[100:103], a[216:219], a[228:231], a[100:103]\n" \
M_"a[104:107], a[152:155], a[168:171], a[104:107]\n" \
M_"a[104:107], a[216:219], a[232:235], a[104:107]\n" \
M_"a[108:111], a[152:155], a[172:175], a[108:111]\n" \
M_"a[108:111], a[216:219], a[236:239], a[108:111]\n" \
"s_waitcnt lgkmcnt(15)\n" \
M_"a[124:127], a[156:159], a[172:175], a[124:127]\n" \
M_"a[124:127], a[220:223], a[236:239], a[124:127]\n" \
M_"a[120:123], a[156:159], a[168:171], a[120:123]\n" \
M_"a[120:123], a[220:223], a[232:235], a[120:123]\n" \
M_"a[116:119], a[156:159], a[164:167], a[116:119]\n" \
M_"a[116:119], a[220:223], a[228:231], a[116:119]\n" \
M_"a[112:115], a[156:159], a[160:163], a[112:115]\n" \
"s_waitcnt vmcnt(0) lgkmcnt(0)\n" \
M_"a[112:115], a[220:223], a[224:227], a[112:115]\n" \
"s_barrier\n" \
"v_mov_b32_e32 v45, s62\n" \
"s_mov_b64 exec, 1\n" \
"buffer_store_dword v45, off, s[80:83], s37 offset:128 sc1\n" \
"s_mov_b64 exec, -1\n" \
M_"v[52:55], a[128:131], a[176:179], v[52:55]\n" \
"s_add_u32 m0, s30, 0x8000\n" \
"s_movk_i32 s84, 0x380\n" \
M_"v[52:55], a[192:195], a[240:243], v[52:55]\n" \
B_"v22, s[64:67], s84 offen offset:0 lds\n" \
M_"v[56:59], a[128:131], a[180:183], v[56:59]\n" \
X_"a[160:161], v32 offset:0\n" \
M_"v[56:59], a[192:195], a[244:247], v[56:59]\n" \
X_"a[162:163], v32 offset:1024\n" \
M_"v[60:63], a[128:131], a[184:187], v[60:63]\n" \
"s_movk_i32 s84, 0x1f80\n" \
M_"v[60:63], a[192:195], a[248:251], v[60:63]\n" \
B_"v23, s[64:67], s84 offen offset:1024 lds\n" \
M_"v[64:67], a[128:131], a[188:191], v[64:67]\n" \
R_"a[128:131], v28 offset:16384\n" \
M_"v[64:67], a[192:195], a[252:255], v[64:67]\n" \
R_"a[192:195], v29 offset:16384\n" \
M_"v[80:83], a[132:135], a[188:191], v[80:83]\n" \
"s_movk_i32 s84, 0x3b80\n" \
M_"v[80:83], a[196:199], a[252:255], v[80:83]\n" \
B_"v22, s[64:67], s84 offen offset:2048 lds\n" \
M_"v[76:79], a[132:135], a[184:187], v[76:79]\n" \
X_"a[224:225], v32 offset:8192\n" \
M_"v[76:79], a[196:199], a[248:251], v[76:79]\n" \
X_"a[226:227], v32 offset:9216\n" \
M_"v[72:75], a[132:135], a[180:183], v[72:75]\n" \
"s_movk_i32 s84, 0x5780\n" \
M_"v[72:75], a[196:199], a[244:247], v[72:75]\n" \
B_"v23, s[64:67], s84 offen offset:3072 lds\n" \
M_"v[68:71], a[132:135], a[176:179], v[68:71]\n" \
R_"a[132:135], v28 offset:18432\n" \
M_"v[68:71], a[196:199], a[240:243], v[68:71]\n" \
R_"a[196:199], v29 offset:18432\n" \
M_"v[84:87], a[136:139], a[176:179], v[84:87]\n" \
"s_add_u32 m0, s31, 0x8000\n" \
"s_mov_b32 s84, 0x380000\n" \
M_"v[84:87], a[200:203], a[240:243], v[84:87]\n" \
B_"v26, s[68:71], s84 offen offset:0 lds\n" \
M_"v[88:91], a[136:139], a[180:183], v[88:91]\n" \
X_"a[164:165], v33 offset:0\n" \
M_"v[88:91], a[200:203], a[244:247], v[88:91]\n" \
X_"a[166:167], v33 offset:1024\n" \
M_"v[92:95], a[136:139], a[184:187], v[92:95]\n" \
"s_addk_i32 s84, 0x7c00\n" \
M_"v[92:95], a[200:203], a[248:251], v[92:95]\n" \
B_"v26, s[68:71], s84 offen offset:1024 lds\n" \
M_"v[96:99], a[136:139], a[188:191], v[96:99]\n" \
R_"a[136:139], v28 offset:20480\n" \
M_"v[96:99], a[200:203], a[252:255], v[96:99]\n" \
R_"a[200:203], v29 offset:20480\n" \
M_"v[112:115], a[140:143], a[188:191], v[112:115]\n" \
"s_addk_i32 s84, 0x7c00\n" \
M_"v[112:115], a[204:207], a[252:255], v[112:115]\n" \
B_"v27, s[68:71], s84 offen offset:2048 lds\n" \
M_"v[108:111], a[140:143], a[184:187], v[108:111]\n" \
X_"a[228:229], v33 offset:8192\n" \
M_"v[108:111], a[204:207], a[248:251], v[108:111]\n" \
X_"a[230:231], v33 offset:9216\n" \
M_"v[104:107], a[140:143], a[180:183], v[104:107]\n" \
"s_addk_i32 s84, 0x7c00\n" \
"s_nop 0\n" \
M_"v[104:107], a[204:207], a[244:247], v[104:107]\n" \
B_"v27, s[68:71], s84 offen offset:3072 lds\n" \
M_"v[100:103], a[140:143], a[176:179], v[100:103]\n" \
R_"a[140:143], v28 offset:22528\n" \
M_"v[100:103], a[204:207], a[240:243], v[100:103]\n" \
R_"a[204:207], v29 offset:22528\n" \
M_"v[116:119], a[144:147], a[176:179], v[116:119]\n" \
"s_add_u32 m0, s30, 0x9000\n" \
"s_mov_b32 s84, 0x8380\n" \
M_"v[116:119], a[208:211], a[240:243], v[116:119]\n" \
B_"v22, s[64:67], s84 offen offset:0 lds\n" \
M_"v[120:123], a[144:147], a[180:183], v[120:123]\n" \
X_"a[168:169], v34 offset:0\n" \
M_"v[120:123], a[208:211], a[244:247], v[120:123]\n" \
X_"a[170:171], v34 offset:1024\n" \
M_"v[124:127], a[144:147], a[184:187], v[124:127]\n" \
"s_addk_i32 s84, 0x1c00\n" \
M_"v[124:127], a[208:211], a[248:251], v[124:127]\n" \
B_"v23, s[64:67], s84 offen offset:1024 lds\n" \
M_"v[128:131], a[144:147], a[188:191], v[128:131]\n" \
R_"a[144:147], v28 offset:0\n" \
M_"v[128:131], a[208:211], a[252:255], v[128:131]\n" \
R_"a[208:211], v29 offset:0\n" \
M_"v[144:147], a[148:151], a[188:191], v[144:147]\n" \
"s_addk_i32 s84, 0x1c00\n" \
M_"v[144:147], a[212:215], a[252:255], v[144:147]\n" \
B_"v22, s[64:67], s84 offen offset:2048 lds\n" \
M_"v[140:143], a[148:151], a[184:187], v[140:143]\n" \
X_"a[232:233], v34 offset:8192\n" \
M_"v[140:143], a[212:215], a[248:251], v[140:143]\n" \
X_"a[234:235], v34 offset:9216\n" \
M_"v[136:139], a[148:151], a[180:183], v[136:139]\n" \
"s_addk_i32 s84, 0x1c00\n" \
"s_nop 0\n" \
M_"v[136:139], a[212:215], a[244:247], v[136:139]\n" \
B_"v23, s[64:67], s84 offen offset:3072 lds\n" \
M_"v[132:135], a[148:151], a[176:179], v[132:135]\n" \
R_"a[148:151], v28 offset:2048\n" \
M_"v[132:135], a[212:215], a[240:243], v[132:135]\n" \
R_"a[212:215], v29 offset:2048\n" \
M_"v[148:151], a[152:155], a[176:179], v[148:151]\n" \
"s_add_u32 m0, s31, 0xc000\n" \
"s_mov_b32 s84, 0x380000\n" \
M_"v[148:151], a[216:219], a[240:243], v[148:151]\n" \
B_"v26, s[72:75], s84 offen offset:0 lds\n" \
M_"v[152:155], a[152:155], a[180:183], v[152:155]\n" \
X_"a[172:173], v35 offset:0\n" \
M_"v[152:155], a[216:219], a[244:247], v[152:155]\n" \
X_"a[174:175], v35 offset:1024\n" \
M_"v[156:159], a[152:155], a[184:187], v[156:159]\n" \
"s_addk_i32 s84, 0x7c00\n" \
"s_nop 0\n" \
M_"v[156:159], a[216:219], a[248:251], v[156:159]\n" \
B_"v26, s[72:75], s84 offen offset:1024 lds\n" \
M_"v[160:163], a[152:155], a[188:191], v[160:163]\n" \
R_"a[152:155], v28 offset:4096\n" \
M_"v[160:163], a[216:219], a[252:255], v[160:163]\n" \
R_"a[216:219], v29 offset:4096\n" \
M_"v[176:179], a[156:159], a[188:191], v[176:179]\n" \
X_"a[236:237], v35 offset:8192\n" \
M_"v[176:179], a[220:223], a[252:255], v[176:179]\n" \
X_"a[238:239], v35 offset:9216\n" \
M_"v[172:175], a[156:159], a[184:187], v[172:175]\n" \
M_"v[172:175], a[220:223], a[248:251], v[172:175]\n" \
M_"v[168:171], a[156:159], a[180:183], v[168:171]\n" \
M_"v[168:171], a[220:223], a[244:247], v[168:171]\n" \
M_"v[164:167], a[156:159], a[176:179], v[164:167]\n" \
R_"a[156:159], v28 offset:6144\n" \
M_"v[164:167], a[220:223], a[240:243], v[164:167]\n" \
R_"a[220:223], v29 offset:6144\n" \
"s_waitcnt lgkmcnt(15)\n" \
M_"a[0:3], a[128:131], a[160:163], a[0:3]\n" \
"s_addk_i32 s84, 0x7c00\n" \
M_"a[0:3], a[192:195], a[224:227], a[0:3]\n" \
B_"v27, s[72:75], s84 offen offset:2048 lds\n" \
M_"a[4:7], a[128:131], a[164:167], a[4:7]\n" \
X_"a[176:177], v36 offset:0\n" \
M_"a[4:7], a[192:195], a[228:231], a[4:7]\n" \
"s_addk_i32 s84, 0x7c00\n" \
"s_waitcnt lgkmcnt(15)\n" \
M_"a[8:11], a[128:131], a[168:171], a[8:11]\n" \
B_"v27, s[72:75], s84 offen offset:3072 lds\n" \
"s_waitcnt lgkmcnt(11)\n" \
M_"a[8:11], a[192:195], a[232:235], a[8:11]\n" \
X_"a[178:179], v36 offset:1024\n" \
"s_waitcnt lgkmcnt(8)\n" \
M_"a[12:15], a[128:131], a[172:175], a[12:15]\n" \
X_"a[240:241], v36 offset:8192\n" \
"s_waitcnt lgkmcnt(5)\n" \
"s_nop 0\n" \
M_"a[12:15], a[192:195], a[236:239], a[12:15]\n" \
M_"a[28:31], a[132:135], a[172:175], a[28:31]\n" \
M_"a[28:31], a[196:199], a[236:239], a[28:31]\n" \
X_"a[242:243], v36 offset:9216\n" \
M_"a[24:27], a[132:135], a[168:171], a[24:27]\n" \
M_"a[24:27], a[196:199], a[232:235], a[24:27]\n" \
M_"a[20:23], a[132:135], a[164:167], a[20:23]\n" \
M_"a[20:23], a[196:199], a[228:231], a[20:23]\n" \
X_"a[180:181], v37 offset:0\n" \
M_"a[16:19], a[132:135], a[160:163], a[16:19]\n" \
M_"a[16:19], a[196:199], a[224:227], a[16:19]\n" \
M_"a[32:35], a[136:139], a[160:163], a[32:35]\n" \
X_"a[182:183], v37 offset:1024\n" \
M_"a[32:35], a[200:203], a[224:227], a[32:35]\n" \
M_"a[36:39], a[136:139], a[164:167], a[36:39]\n" \
"v_bfe_u32 v44, v20, 2, 5\n" \
M_"a[36:39], a[200:203], a[228:231], a[36:39]\n" \
"buffer_load_dword v44, v44, s[80:83], s38 offen offset:128 sc1\n" \
M_"a[40:43], a[136:139], a[168:171], a[40:43]\n" \
X_"a[244:245], v37 offset:8192\n" \
M_"a[40:43], a[200:203], a[232:235], a[40:43]\n" \
M_"a[44:47], a[136:139], a[172:175], a[44:47]\n" \
X_"a[246:247], v37 offset:9216\n" \
M_"a[44:47], a[200:203], a[236:239], a[44:47]\n" \
M_"a[60:63], a[140:143], a[172:175], a[60:63]\n" \
M_"a[60:63], a[204:207], a[236:239], a[60:63]\n" \
M_"a[56:59], a[140:143], a[168:171], a[56:59]\n" \
X_"a[184:185], v38 offset:0\n" \
M_"a[56:59], a[204:207], a[232:235], a[56:59]\n" \
M_"a[52:55], a[140:143], a[164:167], a[52:55]\n" \
"global_store_dwordx4 v49, v[0:3], s[28:29] offset:0 sc1\n" \
M_"a[52:55], a[204:207], a[228:231], a[52:55]\n" \
X_"a[186:187], v38 offset:1024\n" \
M_"a[48:51], a[140:143], a[160:163], a[48:51]\n" \
M_"a[48:51], a[204:207], a[224:227], a[48:51]\n" \
M_"a[64:67], a[144:147], a[160:163], a[64:67]\n" \
X_"a[248:249], v38 offset:8192\n" \
M_"a[64:67], a[208:211], a[224:227], a[64:67]\n" \
M_"a[68:71], a[144:147], a[164:167], a[68:71]\n" \
M_"a[68:71], a[208:211], a[228:231], a[68:71]\n" \
X_"a[250:251], v38 offset:9216\n" \
M_"a[72:75], a[144:147], a[168:171], a[72:75]\n" \
M_"a[72:75], a[208:211], a[232:235], a[72:75]\n" \
"global_store_dwordx4 v49, v[0:3], s[28:29] offset:1024 sc1\n" \
M_"a[76:79], a[144:147], a[172:175], a[76:79]\n" \
M_"a[76:79], a[208:211], a[236:239], a[76:79]\n" \
X_"a[188:189], v39 offset:0\n" \
M_"a[92:95], a[148:151], a[172:175], a[92:95]\n" \
M_"a[92:95], a[212:215], a[236:239], a[92:95]\n" \
M_"a[88:91], a[148:151], a[168:171], a[88:91]\n" \
X_"a[190:191], v39 offset:1024\n" \
M_"a[88:91], a[212:215], a[232:235], a[88:91]\n" \
M_"a[84:87], a[148:151], a[164:167], a[84:87]\n" \
M_"a[84:87], a[212:215], a[228:231], a[84:87]\n" \
X_"a[252:253], v39 offset:8192\n" \
M_"a[80:83], a[148:151], a[160:163], a[80:83]\n" \
"s_mov_b64 exec, 0xffff\n" \
"global_store_dwordx4 v49, v[0:3], s[28:29] offset:2048 sc1\n" \
"s_mov_b64 exec, -1\n" \
M_"a[80:83], a[212:215], a[224:227], a[80:83]\n" \
M_"a[96:99], a[152:155], a[160:163], a[96:99]\n" \
X_"a[254:255], v39 offset:9216\n" \
M_"a[96:99], a[216:219], a[224:227], a[96:99]\n" \
M_"a[100:103], a[152:155], a[164:167], a[100:103]\n" \
M_"a[100:103], a[216:219], a[228:231], a[100:103]\n" \
M_"a[104:107], a[152:155], a[168:171], a[104:107]\n" \
M_"a[104:107], a[216:219], a[232:235], a[104:107]\n" \
M_"a[108:111], a[152:155], a[172:175], a[108:111]\n" \
M_"a[108:111], a[216:219], a[236:239], a[108:111]\n" \
"s_waitcnt lgkmcnt(15)\n" \
M_"a[124:127], a[156:159], a[172:175], a[124:127]\n" \
M_"a[124:127], a[220:223], a[236:239], a[124:127]\n" \
M_"a[120:123], a[156:159], a[168:171], a[120:123]\n" \
M_"a[120:123], a[220:223], a[232:235], a[120:123]\n" \
M_"a[116:119], a[156:159], a[164:167], a[116:119]\n" \
M_"a[116:119], a[220:223], a[228:231], a[116:119]\n" \
M_"a[112:115], a[156:159], a[160:163], a[112:115]\n" \
"s_waitcnt vmcnt(3) lgkmcnt(0)\n" \
M_"a[112:115], a[220:223], a[224:227], a[112:115]\n" \
"s_barrier\n" \
"v_cmp_ne_u32_e32 vcc, s62, v44\n" \
"s_nop 4\n" \
"s_cbranch_vccnz L_poll61_%=\n" \
"L_pollr61_%=:\n" \
"s_mov_b32 s68, s12\n" \
"s_and_b32 s69, s13, 0xffff\n" \
"s_mov_b32 s70, 0x480000\n" \
"s_mov_b32 s71, 0x20000\n" \
M_"v[52:55], a[128:131], a[176:179], v[52:55]\n" \
"s_add_u32 m0, s40, 0x0\n" \
"s_add_u32 s85, s39, 0x0\n" \
M_"v[52:55], a[192:195], a[240:243], v[52:55]\n" \
B_"v20, s[80:83], s85 offen sc1 lds\n" \
M_"v[56:59], a[128:131], a[180:183], v[56:59]\n" \
".fill (L_pb + 57056 - .) / 4, 4, 0xBF800000\n" \
"L_pp14:\n" \
"s_cbranch_execz L_pret\n" \
"s_nop 0\n" \
X_"a[160:161], v32 offset:32768\n" \
M_"v[56:59], a[192:195], a[244:247], v[56:59]\n" \
X_"a[162:163], v32 offset:33792\n" \
M_"v[60:63], a[128:131], a[184:187], v[60:63]\n" \
"s_add_u32 m0, s40, 0x1000\n" \
"s_add_u32 s85, s39, 0x1000\n" \
M_"v[60:63], a[192:195], a[248:251], v[60:63]\n" \
B_"v20, s[80:83], s85 offen sc1 lds\n" \
M_"v[64:67], a[128:131], a[188:191], v[64:67]\n" \
R_"a[128:131], v28 offset:49152\n" \
M_"v[64:67], a[192:195], a[252:255], v[64:67]\n" \
R_"a[192:195], v29 offset:49152\n" \
M_"v[80:83], a[132:135], a[188:191], v[80:83]\n" \
"s_add_u32 m0, s40, 0x3000\n" \
"s_movk_i32 s84, 0x0\n" \
M_"v[80:83], a[196:199], a[252:255], v[80:83]\n" \
B_"v21, s[76:79], s84 offen offset:0 lds\n" \
M_"v[76:79], a[132:135], a[184:187], v[76:79]\n" \
X_"a[224:225], v32 offset:40960\n" \
M_"v[76:79], a[196:199], a[248:251], v[76:79]\n" \
X_"a[226:227], v32 offset:41984\n" \
M_"v[72:75], a[132:135], a[180:183], v[72:75]\n" \
"s_add_u32 m0, s30, 0x0\n" \
"s_movk_i32 s84, 0x0\n" \
M_"v[72:75], a[196:199], a[244:247], v[72:75]\n" \
B_"v24, s[76:79], s84 offen offset:0 lds\n" \
M_"v[68:71], a[132:135], a[176:179], v[68:71]\n" \
R_"a[132:135], v28 offset:51200\n" \
M_"v[68:71], a[196:199], a[240:243], v[68:71]\n" \
R_"a[196:199], v29 offset:51200\n" \
M_"v[84:87], a[136:139], a[176:179], v[84:87]\n" \
X_"a[164:165], v33 offset:32768\n" \
M_"v[84:87], a[200:203], a[240:243], v[84:87]\n" \
"s_movk_i32 s84, 0x2000\n" \
M_"v[88:91], a[136:139], a[180:183], v[88:91]\n" \
B_"v25, s[76:79], s84 offen offset:1024 lds\n" \
M_"v[88:91], a[200:203], a[244:247], v[88:91]\n" \
X_"a[166:167], v33 offset:33792\n" \
M_"v[92:95], a[136:139], a[184:187], v[92:95]\n" \
X_"a[228:229], v33 offset:40960\n" \
M_"v[92:95], a[200:203], a[248:251], v[92:95]\n" \
M_"v[96:99], a[136:139], a[188:191], v[96:99]\n" \
R_"a[136:139], v28 offset:53248\n" \
M_"v[96:99], a[200:203], a[252:255], v[96:99]\n" \
R_"a[200:203], v29 offset:53248\n" \
M_"v[112:115], a[140:143], a[188:191], v[112:115]\n" \
"s_movk_i32 s84, 0x4000\n" \
M_"v[112:115], a[204:207], a[252:255], v[112:115]\n" \
B_"v24, s[76:79], s84 offen offset:2048 lds\n" \
M_"v[108:111], a[140:143], a[184:187], v[108:111]\n" \
"s_movk_i32 s84, 0x6000\n" \
M_"v[108:111], a[204:207], a[248:251], v[108:111]\n" \
B_"v25, s[76:79], s84 offen offset:3072 lds\n" \
M_"v[104:107], a[140:143], a[180:183], v[104:107]\n" \
X_"a[230:231], v33 offset:41984\n" \
M_"v[104:107], a[204:207], a[244:247], v[104:107]\n" \
X_"a[168:169], v34 offset:32768\n" \
M_"v[100:103], a[140:143], a[176:179], v[100:103]\n" \
R_"a[140:143], v28 offset:55296\n" \
M_"v[100:103], a[204:207], a[240:243], v[100:103]\n" \
R_"a[204:207], v29 offset:55296\n" \
M_"v[116:119], a[144:147], a[176:179], v[116:119]\n" \
"s_add_u32 m0, s31, 0x0\n" \
"s_movk_i32 s84, 0x0\n" \
M_"v[116:119], a[208:211], a[240:243], v[116:119]\n" \
B_"v26, s[68:71], s84 offen offset:0 lds\n" \
M_"v[120:123], a[144:147], a[180:183], v[120:123]\n" \
"s_movk_i32 s84, 0x7c00\n" \
"s_nop 0\n" \
M_"v[120:123], a[208:211], a[244:247], v[120:123]\n" \
B_"v26, s[68:71], s84 offen offset:1024 lds\n" \
M_"v[124:127], a[144:147], a[184:187], v[124:127]\n" \
X_"a[170:171], v34 offset:33792\n" \
M_"v[124:127], a[208:211], a[248:251], v[124:127]\n" \
X_"a[232:233], v34 offset:40960\n" \
M_"v[128:131], a[144:147], a[188:191], v[128:131]\n" \
R_"a[144:147], v28 offset:32768\n" \
M_"v[128:131], a[208:211], a[252:255], v[128:131]\n" \
R_"a[208:211], v29 offset:32768\n" \
M_"v[144:147], a[148:151], a[188:191], v[144:147]\n" \
"s_addk_i32 s84, 0x7c00\n" \
M_"v[144:147], a[212:215], a[252:255], v[144:147]\n" \
B_"v27, s[68:71], s84 offen offset:2048 lds\n" \
M_"v[140:143], a[148:151], a[184:187], v[140:143]\n" \
X_"a[234:235], v34 offset:41984\n" \
M_"v[140:143], a[212:215], a[248:251], v[140:143]\n" \
"s_addk_i32 s84, 0x7c00\n" \
M_"v[136:139], a[148:151], a[180:183], v[136:139]\n" \
B_"v27, s[68:71], s84 offen offset:3072 lds\n" \
M_"v[136:139], a[212:215], a[244:247], v[136:139]\n" \
X_"a[172:173], v35 offset:32768\n" \
M_"v[132:135], a[148:151], a[176:179], v[132:135]\n" \
R_"a[148:151], v28 offset:34816\n" \
M_"v[132:135], a[212:215], a[240:243], v[132:135]\n" \
R_"a[212:215], v29 offset:34816\n" \
M_"v[148:151], a[152:155], a[176:179], v[148:151]\n" \
"s_add_u32 m0, s30, 0x1000\n" \
"s_mov_b32 s84, 0x9000\n" \
M_"v[148:151], a[216:219], a[240:243], v[148:151]\n" \
B_"v24, s[76:79], s84 offen offset:0 lds\n" \
M_"v[152:155], a[152:155], a[180:183], v[152:155]\n" \
X_"a[174:175], v35 offset:33792\n" \
M_"v[152:155], a[216:219], a[244:247], v[152:155]\n" \
"s_addk_i32 s84, 0x2000\n" \
"s_nop 0\n" \
M_"v[156:159], a[152:155], a[184:187], v[156:159]\n" \
B_"v25, s[76:79], s84 offen offset:1024 lds\n" \
M_"v[156:159], a[216:219], a[248:251], v[156:159]\n" \
X_"a[236:237], v35 offset:40960\n" \
M_"v[160:163], a[152:155], a[188:191], v[160:163]\n" \
R_"a[152:155], v28 offset:36864\n" \
M_"v[160:163], a[216:219], a[252:255], v[160:163]\n" \
R_"a[216:219], v29 offset:36864\n" \
M_"v[176:179], a[156:159], a[188:191], v[176:179]\n" \
X_"a[238:239], v35 offset:41984\n" \
M_"v[176:179], a[220:223], a[252:255], v[176:179]\n" \
M_"v[172:175], a[156:159], a[184:187], v[172:175]\n" \
M_"v[172:175], a[220:223], a[248:251], v[172:175]\n" \
M_"v[168:171], a[156:159], a[180:183], v[168:171]\n" \
M_"v[168:171], a[220:223], a[244:247], v[168:171]\n" \
M_"v[164:167], a[156:159], a[176:179], v[164:167]\n" \
R_"a[156:159], v28 offset:38912\n" \
M_"v[164:167], a[220:223], a[240:243], v[164:167]\n" \
R_"a[220:223], v29 offset:38912\n" \
"s_waitcnt lgkmcnt(15)\n" \
M_"a[0:3], a[128:131], a[160:163], a[0:3]\n" \
"s_addk_i32 s84, 0x2000\n" \
M_"a[0:3], a[192:195], a[224:227], a[0:3]\n" \
B_"v24, s[76:79], s84 offen offset:2048 lds\n" \
M_"a[4:7], a[128:131], a[164:167], a[4:7]\n" \
X_"a[176:177], v36 offset:32768\n" \
M_"a[4:7], a[192:195], a[228:231], a[4:7]\n" \
"s_addk_i32 s84, 0x2000\n" \
"s_waitcnt lgkmcnt(15)\n" \
M_"a[8:11], a[128:131], a[168:171], a[8:11]\n" \
B_"v25, s[76:79], s84 offen offset:3072 lds\n" \
"s_waitcnt lgkmcnt(11)\n" \
M_"a[8:11], a[192:195], a[232:235], a[8:11]\n" \
X_"a[178:179], v36 offset:33792\n" \
"s_waitcnt lgkmcnt(8)\n" \
M_"a[12:15], a[128:131], a[172:175], a[12:15]\n" \
X_"a[240:241], v36 offset:40960\n" \
"s_waitcnt lgkmcnt(5)\n" \
M_"a[12:15], a[192:195], a[236:239], a[12:15]\n" \
M_"a[28:31], a[132:135], a[172:175], a[28:31]\n" \
M_"a[28:31], a[196:199], a[236:239], a[28:31]\n" \
X_"a[242:243], v36 offset:41984\n" \
M_"a[24:27], a[132:135], a[168:171], a[24:27]\n" \
M_"a[24:27], a[196:199], a[232:235], a[24:27]\n" \
"s_add_u32 m0, s30, 0x20000\n" \
"s_movk_i32 s84, 0x100\n" \
M_"a[20:23], a[132:135], a[164:167], a[20:23]\n" \
B_"v24, s[76:79], s84 offen offset:0 lds\n" \
M_"a[20:23], a[196:199], a[228:231], a[20:23]\n" \
X_"a[180:181], v37 offset:32768\n" \
"v_accvgpr_read_b32 v184, a12\n" \
M_"a[16:19], a[132:135], a[160:163], a[16:19]\n" \
"v_accvgpr_read_b32 v185, a13\n" \
"v_accvgpr_read_b32 v186, a14\n" \
M_"a[16:19], a[196:199], a[224:227], a[16:19]\n" \
"v_accvgpr_read_b32 v187, a15\n" \
"v_accvgpr_read_b32 v180, a28\n" \
M_"a[32:35], a[136:139], a[160:163], a[32:35]\n" \
"s_movk_i32 s84, 0x2100\n" \
"s_nop 0\n" \
M_"a[32:35], a[200:203], a[224:227], a[32:35]\n" \
B_"v25, s[76:79], s84 offen offset:1024 lds\n" \
M_"a[36:39], a[136:139], a[164:167], a[36:39]\n" \
X_"a[182:183], v37 offset:33792\n" \
"v_accvgpr_read_b32 v181, a29\n" \
M_"a[36:39], a[200:203], a[228:231], a[36:39]\n" \
X_"a[244:245], v37 offset:40960\n" \
"v_accvgpr_read_b32 v182, a30\n" \
M_"a[40:43], a[136:139], a[168:171], a[40:43]\n" \
"v_accvgpr_read_b32 v183, a31\n" \
C_"v184, v184, v185\n" \
M_"a[40:43], a[200:203], a[232:235], a[40:43]\n" \
C_"v185, v186, v187\n" \
C_"v186, v180, v181\n" \
M_"a[44:47], a[136:139], a[172:175], a[44:47]\n" \
"s_movk_i32 s84, 0x4100\n" \
"s_nop 0\n" \
M_"a[44:47], a[200:203], a[236:239], a[44:47]\n" \
B_"v24, s[76:79], s84 offen offset:2048 lds\n" \
M_"a[60:63], a[140:143], a[172:175], a[60:63]\n" \
X_"a[246:247], v37 offset:41984\n" \
C_"v187, v182, v183\n" \
M_"a[60:63], a[204:207], a[236:239], a[60:63]\n" \
"v_accvgpr_read_b32 v188, a8\n" \
"v_accvgpr_read_b32 v189, a9\n" \
M_"a[56:59], a[140:143], a[168:171], a[56:59]\n" \
X_"a[184:185], v38 offset:32768\n" \
"v_accvgpr_read_b32 v190, a10\n" \
M_"a[56:59], a[204:207], a[232:235], a[56:59]\n" \
"v_accvgpr_read_b32 v191, a11\n" \
"v_accvgpr_read_b32 v180, a24\n" \
M_"a[52:55], a[140:143], a[164:167], a[52:55]\n" \
"s_movk_i32 s84, 0x6100\n" \
"s_nop 0\n" \
M_"a[52:55], a[204:207], a[228:231], a[52:55]\n" \
B_"v25, s[76:79], s84 offen offset:3072 lds\n" \
M_"a[48:51], a[140:143], a[160:163], a[48:51]\n" \
X_"a[186:187], v38 offset:33792\n" \
"v_accvgpr_read_b32 v181, a25\n" \
M_"a[48:51], a[204:207], a[224:227], a[48:51]\n" \
"v_accvgpr_read_b32 v182, a26\n" \
"v_accvgpr_read_b32 v183, a27\n" \
M_"a[64:67], a[144:147], a[160:163], a[64:67]\n" \
X_"a[248:249], v38 offset:40960\n" \
C_"v188, v188, v189\n" \
M_"a[64:67], a[208:211], a[224:227], a[64:67]\n" \
"s_add_u32 m0, s30, 0x21000\n" \
"s_addk_i32 s84, 0x3000\n" \
"s_nop 0\n" \
M_"a[68:71], a[144:147], a[164:167], a[68:71]\n" \
B_"v24, s[76:79], s84 offen offset:0 lds\n" \
M_"a[68:71], a[208:211], a[228:231], a[68:71]\n" \
X_"a[250:251], v38 offset:41984\n" \
C_"v189, v190, v191\n" \
M_"a[72:75], a[144:147], a[168:171], a[72:75]\n" \
C_"v190, v180, v181\n" \
C_"v191, v182, v183\n" \
M_"a[72:75], a[208:211], a[232:235], a[72:75]\n" \
"v_accvgpr_read_b32 v192, a4\n" \
"v_accvgpr_read_b32 v193, a5\n" \
M_"a[76:79], a[144:147], a[172:175], a[76:79]\n" \
"v_accvgpr_read_b32 v194, a6\n" \
"v_accvgpr_read_b32 v195, a7\n" \
M_"a[76:79], a[208:211], a[236:239], a[76:79]\n" \
"s_addk_i32 s84, 0x2000\n" \
M_"a[92:95], a[148:151], a[172:175], a[92:95]\n" \
B_"v25, s[76:79], s84 offen offset:1024 lds\n" \
M_"a[92:95], a[212:215], a[236:239], a[92:95]\n" \
"s_add_u32 s89, s41, 0x0\n" \
"buffer_store_dwordx4 v[184:187], v20, s[80:83], s89 offen offset:3072 sc1\n" \
M_"a[88:91], a[148:151], a[168:171], a[88:91]\n" \
X_"a[188:189], v39 offset:32768\n" \
"v_accvgpr_read_b32 v180, a20\n" \
M_"a[88:91], a[212:215], a[232:235], a[88:91]\n" \
X_"a[190:191], v39 offset:33792\n" \
"v_accvgpr_read_b32 v181, a21\n" \
M_"a[84:87], a[148:151], a[164:167], a[84:87]\n" \
"s_addk_i32 s84, 0x2000\n" \
M_"a[84:87], a[212:215], a[228:231], a[84:87]\n" \
B_"v24, s[76:79], s84 offen offset:2048 lds\n" \
M_"a[80:83], a[148:151], a[160:163], a[80:83]\n" \
"s_add_u32 s88, s41, 0x0\n" \
"buffer_store_dwordx4 v[188:191], v20, s[80:83], s88 offen offset:2048 sc1\n" \
M_"a[80:83], a[212:215], a[224:227], a[80:83]\n" \
X_"a[252:253], v39 offset:40960\n" \
"v_accvgpr_read_b32 v182, a22\n" \
M_"a[96:99], a[152:155], a[160:163], a[96:99]\n" \
X_"a[254:255], v39 offset:41984\n" \
"v_accvgpr_read_b32 v183, a23\n" \
M_"a[96:99], a[216:219], a[224:227], a[96:99]\n" \
C_"v192, v192, v193\n" \
C_"v193, v194, v195\n" \
M_"a[100:103], a[152:155], a[164:167], a[100:103]\n" \
"s_addk_i32 s84, 0x2000\n" \
M_"a[100:103], a[216:219], a[228:231], a[100:103]\n" \
B_"v25, s[76:79], s84 offen offset:3072 lds\n" \
M_"a[104:107], a[152:155], a[168:171], a[104:107]\n" \
C_"v194, v180, v181\n" \
C_"v195, v182, v183\n" \
"s_add_u32 s89, s41, 0x0\n" \
"buffer_store_dwordx4 v[192:195], v20, s[80:83], s89 offen offset:1024 sc1\n" \
M_"a[104:107], a[216:219], a[232:235], a[104:107]\n" \
"v_accvgpr_read_b32 v196, a0\n" \
"v_accvgpr_read_b32 v197, a1\n" \
M_"a[108:111], a[152:155], a[172:175], a[108:111]\n" \
"v_accvgpr_read_b32 v198, a2\n" \
"v_accvgpr_read_b32 v199, a3\n" \
M_"a[108:111], a[216:219], a[236:239], a[108:111]\n" \
"v_accvgpr_read_b32 v180, a16\n" \
"v_accvgpr_read_b32 v181, a17\n" \
"s_waitcnt lgkmcnt(15)\n" \
M_"a[124:127], a[156:159], a[172:175], a[124:127]\n" \
"v_accvgpr_read_b32 v182, a18\n" \
"v_accvgpr_read_b32 v183, a19\n" \
M_"a[124:127], a[220:223], a[236:239], a[124:127]\n" \
C_"v196, v196, v197\n" \
C_"v197, v198, v199\n" \
M_"a[120:123], a[156:159], a[168:171], a[120:123]\n" \
C_"v198, v180, v181\n" \
C_"v199, v182, v183\n" \
"s_add_u32 s88, s41, 0x0\n" \
"buffer_store_dwordx4 v[196:199], v20, s[80:83], s88 offen offset:0 sc1\n" \
M_"a[120:123], a[220:223], a[232:235], a[120:123]\n" \
"v_accvgpr_read_b32 v200, a44\n" \
"v_accvgpr_read_b32 v201, a45\n" \
M_"a[116:119], a[156:159], a[164:167], a[116:119]\n" \
"v_accvgpr_read_b32 v202, a46\n" \
"v_accvgpr_read_b32 v203, a47\n" \
M_"a[116:119], a[220:223], a[228:231], a[116:119]\n" \
"v_accvgpr_read_b32 v180, a60\n" \
"v_accvgpr_read_b32 v181, a61\n" \
M_"a[112:115], a[156:159], a[160:163], a[112:115]\n" \
"v_accvgpr_read_b32 v182, a62\n" \
"v_accvgpr_read_b32 v183, a63\n" \
M_"a[112:115], a[220:223], a[224:227], a[112:115]\n" \
C_"v200, v200, v201\n" \
C_"v201, v202, v203\n" \
"s_waitcnt vmcnt(12) lgkmcnt(0)\n" \
"s_barrier\n" \
"v_xor_b32_e32 v36, 0x4000, v36\n" \
"v_xor_b32_e64 v32, 0x0, v48\n" \
"v_xor_b32_e32 v37, 0x4000, v37\n" \
"v_xor_b32_e64 v33, 0x20, v48\n" \
"v_xor_b32_e32 v38, 0x4000, v38\n" \
"v_xor_b32_e64 v34, 0x40, v48\n" \
"v_xor_b32_e32 v39, 0x4000, v39\n" \
"v_xor_b32_e32 v35, 0x60, v48\n" \
"v_add_u32_e32 v28, s43, v28\n" \
"v_add_u32_e32 v29, s43, v29\n" \
M_"v[116:119], a[144:147], a[176:179], v[116:119]\n" \
"s_add_u32 m0, s40, 0x8000\n" \
"s_add_u32 s85, s39, 0x2000\n" \
M_"v[116:119], a[208:211], a[240:243], v[116:119]\n" \
B_"v20, s[80:83], s85 offen sc1 lds\n" \
M_"v[120:123], a[144:147], a[180:183], v[120:123]\n" \
X_"a[160:161], v32 offset:0\n" \
C_"v202, v180, v181\n" \
M_"v[120:123], a[208:211], a[244:247], v[120:123]\n" \
X_"a[162:163], v32 offset:512\n" \
C_"v203, v182, v183\n" \
M_"v[124:127], a[144:147], a[184:187], v[124:127]\n" \
"s_add_u32 m0, s40, 0x9000\n" \
"s_add_u32 s85, s39, 0x3000\n" \
M_"v[124:127], a[208:211], a[248:251], v[124:127]\n" \
B_"v20, s[80:83], s85 offen sc1 lds\n" \
M_"v[128:131], a[144:147], a[188:191], v[128:131]\n" \
R_"a[144:147], v28 offset:8192\n" \
"v_accvgpr_read_b32 v204, a40\n" \
M_"v[128:131], a[208:211], a[252:255], v[128:131]\n" \
R_"a[208:211], v29 offset:8192\n" \
"v_accvgpr_read_b32 v205, a41\n" \
M_"v[144:147], a[148:151], a[188:191], v[144:147]\n" \
"s_add_u32 m0, s40, 0xb000\n" \
"s_movk_i32 s84, 0x80\n" \
M_"v[144:147], a[212:215], a[252:255], v[144:147]\n" \
B_"v21, s[76:79], s84 offen offset:0 lds\n" \
M_"v[140:143], a[148:151], a[184:187], v[140:143]\n" \
X_"a[224:225], v32 offset:4096\n" \
"v_accvgpr_read_b32 v206, a42\n" \
M_"v[140:143], a[212:215], a[248:251], v[140:143]\n" \
X_"a[226:227], v32 offset:4608\n" \
"v_accvgpr_read_b32 v207, a43\n" \
M_"v[136:139], a[148:151], a[180:183], v[136:139]\n" \
"s_add_u32 m0, s30, 0x8000\n" \
"s_movk_i32 s84, 0x80\n" \
M_"v[136:139], a[212:215], a[244:247], v[136:139]\n" \
B_"v24, s[76:79], s84 offen offset:0 lds\n" \
M_"v[132:135], a[148:151], a[176:179], v[132:135]\n" \
R_"a[148:151], v28 offset:10240\n" \
"v_accvgpr_read_b32 v180, a56\n" \
M_"v[132:135], a[212:215], a[240:243], v[132:135]\n" \
R_"a[212:215], v29 offset:10240\n" \
"v_accvgpr_read_b32 v181, a57\n" \
M_"v[148:151], a[152:155], a[176:179], v[148:151]\n" \
X_"a[164:165], v33 offset:0\n" \
"v_accvgpr_read_b32 v182, a58\n" \
M_"v[148:151], a[216:219], a[240:243], v[148:151]\n" \
"s_movk_i32 s84, 0x2080\n" \
"s_nop 0\n" \
M_"v[152:155], a[152:155], a[180:183], v[152:155]\n" \
B_"v25, s[76:79], s84 offen offset:1024 lds\n" \
M_"v[152:155], a[216:219], a[244:247], v[152:155]\n" \
X_"a[166:167], v33 offset:512\n" \
"v_accvgpr_read_b32 v183, a59\n" \
M_"v[156:159], a[152:155], a[184:187], v[156:159]\n" \
X_"a[228:229], v33 offset:4096\n" \
C_"v204, v204, v205\n" \
M_"v[156:159], a[216:219], a[248:251], v[156:159]\n" \
C_"v205, v206, v207\n" \
C_"v206, v180, v181\n" \
M_"v[160:163], a[152:155], a[188:191], v[160:163]\n" \
R_"a[152:155], v28 offset:12288\n" \
C_"v207, v182, v183\n" \
M_"v[160:163], a[216:219], a[252:255], v[160:163]\n" \
R_"a[216:219], v29 offset:12288\n" \
"v_accvgpr_read_b32 v208, a36\n" \
M_"v[176:179], a[156:159], a[188:191], v[176:179]\n" \
"s_movk_i32 s84, 0x4080\n" \
M_"v[176:179], a[220:223], a[252:255], v[176:179]\n" \
B_"v24, s[76:79], s84 offen offset:2048 lds\n" \
M_"v[172:175], a[156:159], a[184:187], v[172:175]\n" \
"s_movk_i32 s84, 0x6080\n" \
M_"v[172:175], a[220:223], a[248:251], v[172:175]\n" \
B_"v25, s[76:79], s84 offen offset:3072 lds\n" \
M_"v[168:171], a[156:159], a[180:183], v[168:171]\n" \
X_"a[230:231], v33 offset:4608\n" \
"v_accvgpr_read_b32 v209, a37\n" \
M_"v[168:171], a[220:223], a[244:247], v[168:171]\n" \
X_"a[172:173], v35 offset:0\n" \
"v_accvgpr_read_b32 v210, a38\n" \
M_"v[164:167], a[156:159], a[176:179], v[164:167]\n" \
R_"a[156:159], v28 offset:14336\n" \
"v_accvgpr_read_b32 v211, a39\n" \
M_"v[164:167], a[220:223], a[240:243], v[164:167]\n" \
R_"a[220:223], v29 offset:14336\n" \
"v_accvgpr_read_b32 v180, a52\n" \
M_"v[84:87], a[136:139], a[176:179], v[84:87]\n" \
"s_add_u32 m0, s31, 0x8000\n" \
"s_mov_b32 s84, 0x80000\n" \
M_"v[84:87], a[200:203], a[240:243], v[84:87]\n" \
B_"v26, s[68:71], s84 offen offset:0 lds\n" \
M_"v[88:91], a[136:139], a[180:183], v[88:91]\n" \
"s_addk_i32 s84, 0x7c00\n" \
"s_nop 0\n" \
M_"v[88:91], a[200:203], a[244:247], v[88:91]\n" \
B_"v26, s[68:71], s84 offen offset:1024 lds\n" \
M_"v[92:95], a[136:139], a[184:187], v[92:95]\n" \
X_"a[174:175], v35 offset:512\n" \
"v_accvgpr_read_b32 v181, a53\n" \
M_"v[92:95], a[200:203], a[248:251], v[92:95]\n" \
X_"a[236:237], v35 offset:4096\n" \
"v_accvgpr_read_b32 v182, a54\n" \
M_"v[96:99], a[136:139], a[188:191], v[96:99]\n" \
R_"a[136:139], v28 offset:4096\n" \
"v_accvgpr_read_b32 v183, a55\n" \
M_"v[96:99], a[200:203], a[252:255], v[96:99]\n" \
R_"a[200:203], v29 offset:4096\n" \
C_"v208, v208, v209\n" \
M_"v[112:115], a[140:143], a[188:191], v[112:115]\n" \
"s_addk_i32 s84, 0x7c00\n" \
M_"v[112:115], a[204:207], a[252:255], v[112:115]\n" \
B_"v27, s[68:71], s84 offen offset:2048 lds\n" \
M_"v[108:111], a[140:143], a[184:187], v[108:111]\n" \
X_"a[238:239], v35 offset:4608\n" \
C_"v209, v210, v211\n" \
M_"v[108:111], a[204:207], a[248:251], v[108:111]\n" \
"s_addk_i32 s84, 0x7c00\n" \
M_"v[104:107], a[140:143], a[180:183], v[104:107]\n" \
B_"v27, s[68:71], s84 offen offset:3072 lds\n" \
M_"v[104:107], a[204:207], a[244:247], v[104:107]\n" \
X_"a[168:169], v34 offset:0\n" \
C_"v210, v180, v181\n" \
M_"v[100:103], a[140:143], a[176:179], v[100:103]\n" \
R_"a[140:143], v28 offset:6144\n" \
C_"v211, v182, v183\n" \
M_"v[100:103], a[204:207], a[240:243], v[100:103]\n" \
R_"a[204:207], v29 offset:6144\n" \
"v_accvgpr_read_b32 v212, a32\n" \
M_"v[52:55], a[128:131], a[176:179], v[52:55]\n" \
"s_add_u32 m0, s30, 0x9000\n" \
"s_mov_b32 s84, 0x9080\n" \
M_"v[52:55], a[192:195], a[240:243], v[52:55]\n" \
B_"v24, s[76:79], s84 offen offset:0 lds\n" \
M_"v[56:59], a[128:131], a[180:183], v[56:59]\n" \
X_"a[170:171], v34 offset:512\n" \
"v_accvgpr_read_b32 v213, a33\n" \
M_"v[56:59], a[192:195], a[244:247], v[56:59]\n" \
"s_addk_i32 s84, 0x2000\n" \
"s_nop 0\n" \
M_"v[60:63], a[128:131], a[184:187], v[60:63]\n" \
B_"v25, s[76:79], s84 offen offset:1024 lds\n" \
M_"v[60:63], a[192:195], a[248:251], v[60:63]\n" \
X_"a[232:233], v34 offset:4096\n" \
"v_accvgpr_read_b32 v214, a34\n" \
M_"v[64:67], a[128:131], a[188:191], v[64:67]\n" \
R_"a[128:131], v28 offset:0\n" \
"v_accvgpr_read_b32 v215, a35\n" \
M_"v[64:67], a[192:195], a[252:255], v[64:67]\n" \
R_"a[192:195], v29 offset:0\n" \
"v_accvgpr_read_b32 v180, a48\n" \
M_"v[80:83], a[132:135], a[188:191], v[80:83]\n" \
X_"a[234:235], v34 offset:4608\n" \
"v_accvgpr_read_b32 v181, a49\n" \
M_"v[80:83], a[196:199], a[252:255], v[80:83]\n" \
"v_accvgpr_read_b32 v182, a50\n" \
"v_accvgpr_read_b32 v183, a51\n" \
M_"v[76:79], a[132:135], a[184:187], v[76:79]\n" \
C_"v212, v212, v213\n" \
C_"v213, v214, v215\n" \
M_"v[76:79], a[196:199], a[248:251], v[76:79]\n" \
C_"v214, v180, v181\n" \
C_"v215, v182, v183\n" \
M_"v[72:75], a[132:135], a[180:183], v[72:75]\n" \
"v_accvgpr_read_b32 v216, a68\n" \
"v_accvgpr_read_b32 v217, a69\n" \
M_"v[72:75], a[196:199], a[244:247], v[72:75]\n" \
"v_accvgpr_read_b32 v218, a70\n" \
"v_accvgpr_read_b32 v219, a71\n" \
M_"v[68:71], a[132:135], a[176:179], v[68:71]\n" \
R_"a[132:135], v28 offset:2048\n" \
"v_accvgpr_read_b32 v180, a84\n" \
M_"v[68:71], a[196:199], a[240:243], v[68:71]\n" \
R_"a[196:199], v29 offset:2048\n" \
"v_accvgpr_read_b32 v181, a85\n" \
"s_waitcnt lgkmcnt(15)\n" \
M_"a[0:3], a[160:163], a[144:147], 0\n" \
"s_addk_i32 s84, 0x2000\n" \
"v_accvgpr_read_b32 v182, a86\n" \
M_"a[0:3], a[224:227], a[208:211], a[0:3]\n" \
B_"v24, s[76:79], s84 offen offset:2048 lds\n" \
"v_accvgpr_read_b32 v183, a87\n" \
M_"a[4:7], a[164:167], a[144:147], 0\n" \
X_"a[176:177], v36 offset:0\n" \
C_"v216, v216, v217\n" \
M_"a[4:7], a[228:231], a[208:211], a[4:7]\n" \
"s_addk_i32 s84, 0x2000\n" \
C_"v217, v218, v219\n" \
"s_waitcnt lgkmcnt(15)\n" \
M_"a[12:15], a[172:175], a[144:147], 0\n" \
B_"v25, s[76:79], s84 offen offset:3072 lds\n" \
C_"v218, v180, v181\n" \
"s_waitcnt lgkmcnt(11)\n" \
M_"a[12:15], a[236:239], a[208:211], a[12:15]\n" \
X_"a[178:179], v36 offset:1024\n" \
C_"v219, v182, v183\n" \
"s_waitcnt lgkmcnt(8)\n" \
M_"a[8:11], a[144:147], a[168:171], 0\n" \
X_"a[240:241], v36 offset:8192\n" \
"v_accvgpr_read_b32 v220, a64\n" \
"s_waitcnt lgkmcnt(5)\n" \
"s_nop 0\n" \
M_"a[8:11], a[208:211], a[232:235], a[8:11]\n" \
X_"a[242:243], v36 offset:9216\n" \
"v_accvgpr_read_b32 v221, a65\n" \
M_"a[24:27], a[148:151], a[168:171], 0\n" \
X_"a[180:181], v37 offset:0\n" \
"v_accvgpr_read_b32 v222, a66\n" \
M_"a[24:27], a[212:215], a[232:235], a[24:27]\n" \
X_"a[182:183], v37 offset:1024\n" \
"v_accvgpr_read_b32 v223, a67\n" \
M_"a[28:31], a[172:175], a[148:151], 0\n" \
X_"a[244:245], v37 offset:8192\n" \
"v_accvgpr_read_b32 v180, a80\n" \
M_"a[28:31], a[236:239], a[212:215], a[28:31]\n" \
"s_add_u32 s89, s41, 0x2000\n" \
"buffer_store_dwordx4 v[200:203], v20, s[80:83], s89 offen offset:3072 sc1\n" \
M_"a[20:23], a[164:167], a[148:151], 0\n" \
X_"a[246:247], v37 offset:9216\n" \
"v_accvgpr_read_b32 v181, a81\n" \
M_"a[20:23], a[228:231], a[212:215], a[20:23]\n" \
X_"a[184:185], v38 offset:0\n" \
"v_accvgpr_read_b32 v182, a82\n" \
M_"a[16:19], a[160:163], a[148:151], 0\n" \
X_"a[186:187], v38 offset:1024\n" \
"v_accvgpr_read_b32 v183, a83\n" \
M_"a[16:19], a[224:227], a[212:215], a[16:19]\n" \
C_"v220, v220, v221\n" \
".fill (L_pb + 61152 - .) / 4, 4, 0xBF800000\n" \
"L_pp15:\n" \
"s_cbranch_execz L_pret\n" \
"s_nop 0\n" \
C_"v221, v222, v223\n" \
C_"v222, v180, v181\n" \
M_"a[32:35], a[160:163], a[152:155], 0\n" \
"s_add_u32 s88, s41, 0x2000\n" \
"buffer_store_dwordx4 v[204:207], v20, s[80:83], s88 offen offset:2048 sc1\n" \
M_"a[32:35], a[224:227], a[216:219], a[32:35]\n" \
X_"a[248:249], v38 offset:8192\n" \
C_"v223, v182, v183\n" \
M_"a[36:39], a[164:167], a[152:155], 0\n" \
X_"a[250:251], v38 offset:9216\n" \
C_"v224, v128, v129\n" \
M_"a[36:39], a[228:231], a[216:219], a[36:39]\n" \
X_"a[188:189], v39 offset:0\n" \
C_"v225, v130, v131\n" \
M_"a[44:47], a[172:175], a[152:155], 0\n" \
C_"v226, v144, v145\n" \
C_"v227, v146, v147\n" \
C_"v228, v124, v125\n" \
M_"a[44:47], a[236:239], a[216:219], a[44:47]\n" \
X_"a[190:191], v39 offset:1024\n" \
C_"v229, v126, v127\n" \
M_"a[40:43], a[152:155], a[168:171], 0\n" \
"s_add_u32 s89, s41, 0x2000\n" \
"buffer_store_dwordx4 v[208:211], v20, s[80:83], s89 offen offset:1024 sc1\n" \
M_"a[40:43], a[216:219], a[232:235], a[40:43]\n" \
X_"a[252:253], v39 offset:8192\n" \
C_"v230, v140, v141\n" \
M_"a[56:59], a[156:159], a[168:171], 0\n" \
X_"a[254:255], v39 offset:9216\n" \
C_"v231, v142, v143\n" \
M_"a[56:59], a[220:223], a[232:235], a[56:59]\n" \
C_"v232, v120, v121\n" \
C_"v233, v122, v123\n" \
C_"v234, v136, v137\n" \
M_"a[60:63], a[172:175], a[156:159], 0\n" \
C_"v235, v138, v139\n" \
C_"v236, v116, v117\n" \
C_"v237, v118, v119\n" \
M_"a[60:63], a[236:239], a[220:223], a[60:63]\n" \
"s_add_u32 s88, s41, 0x2000\n" \
"buffer_store_dwordx4 v[212:215], v20, s[80:83], s88 offen offset:0 sc1\n" \
M_"a[52:55], a[164:167], a[156:159], 0\n" \
C_"v238, v132, v133\n" \
C_"v239, v134, v135\n" \
C_"v240, v160, v161\n" \
M_"a[52:55], a[228:231], a[220:223], a[52:55]\n" \
C_"v241, v162, v163\n" \
C_"v242, v176, v177\n" \
C_"v243, v178, v179\n" \
M_"a[48:51], a[160:163], a[156:159], 0\n" \
C_"v244, v156, v157\n" \
C_"v245, v158, v159\n" \
C_"v246, v172, v173\n" \
M_"a[48:51], a[224:227], a[220:223], a[48:51]\n" \
C_"v247, v174, v175\n" \
C_"v184, v152, v153\n" \
C_"v185, v154, v155\n" \
M_"a[64:67], a[164:167], a[140:143], 0\n" \
C_"v186, v168, v169\n" \
C_"v187, v170, v171\n" \
C_"v188, v148, v149\n" \
M_"a[64:67], a[228:231], a[204:207], a[64:67]\n" \
C_"v189, v150, v151\n" \
C_"v190, v164, v165\n" \
C_"v191, v166, v167\n" \
M_"a[80:83], a[172:175], a[140:143], 0\n" \
C_"v192, v96, v97\n" \
C_"v193, v98, v99\n" \
C_"v194, v112, v113\n" \
M_"a[80:83], a[236:239], a[204:207], a[80:83]\n" \
C_"v195, v114, v115\n" \
C_"v196, v92, v93\n" \
C_"v197, v94, v95\n" \
M_"a[68:71], a[140:143], a[168:171], 0\n" \
C_"v198, v108, v109\n" \
C_"v199, v110, v111\n" \
C_"v252, v88, v89\n" \
M_"a[68:71], a[204:207], a[232:235], a[68:71]\n" \
C_"v253, v90, v91\n" \
C_"v254, v104, v105\n" \
C_"v255, v106, v107\n" \
"s_waitcnt vmcnt(4) lgkmcnt(0)\n" \
"s_barrier\n" \
M_"v[116:119], a[176:179], a[144:147], 0\n" \
"s_add_u32 m0, s40, 0x0\n" \
"s_nop 0\n" \
"s_add_u32 s85, s39, 0x4000\n" \
"v_accvgpr_write_b32 a84, v252\n" \
M_"v[116:119], a[240:243], a[208:211], v[116:119]\n" \
B_"v20, s[80:83], s85 offen sc1 lds\n" \
"v_accvgpr_write_b32 a85, v253\n" \
M_"v[120:123], a[180:183], a[144:147], 0\n" \
X_"a[160:161], v32 offset:32768\n" \
"v_accvgpr_write_b32 a86, v254\n" \
M_"v[120:123], a[244:247], a[208:211], v[120:123]\n" \
X_"a[162:163], v32 offset:33280\n" \
"v_accvgpr_write_b32 a87, v255\n" \
M_"v[124:127], a[184:187], a[144:147], 0\n" \
"s_add_u32 m0, s40, 0x1000\n" \
"s_add_u32 s85, s39, 0x5000\n" \
C_"v252, v84, v85\n" \
M_"v[124:127], a[248:251], a[208:211], v[124:127]\n" \
B_"v20, s[80:83], s85 offen sc1 lds\n" \
C_"v253, v86, v87\n" \
M_"v[128:131], a[188:191], a[144:147], 0\n" \
R_"a[144:147], v28 offset:40960\n" \
C_"v254, v100, v101\n" \
M_"v[128:131], a[252:255], a[208:211], v[128:131]\n" \
R_"a[208:211], v29 offset:40960\n" \
C_"v255, v102, v103\n" \
M_"v[144:147], a[188:191], a[148:151], 0\n" \
X_"a[224:225], v32 offset:36864\n" \
C_"v200, v64, v65\n" \
M_"v[144:147], a[252:255], a[212:215], v[144:147]\n" \
"s_add_u32 m0, s40, 0x3000\n" \
"s_movk_i32 s84, 0x100\n" \
"s_nop 0\n" \
C_"v201, v66, v67\n" \
M_"v[140:143], a[184:187], a[148:151], 0\n" \
B_"v21, s[76:79], s84 offen offset:0 lds\n" \
C_"v202, v80, v81\n" \
M_"v[140:143], a[248:251], a[212:215], v[140:143]\n" \
X_"a[226:227], v32 offset:37376\n" \
C_"v203, v82, v83\n" \
M_"v[136:139], a[180:183], a[148:151], 0\n" \
C_"v204, v60, v61\n" \
C_"v205, v62, v63\n" \
C_"v206, v76, v77\n" \
M_"v[136:139], a[244:247], a[212:215], v[136:139]\n" \
X_"a[164:165], v33 offset:32768\n" \
C_"v207, v78, v79\n" \
M_"v[132:135], a[176:179], a[148:151], 0\n" \
R_"a[148:151], v28 offset:43008\n" \
C_"v208, v56, v57\n" \
M_"v[132:135], a[240:243], a[212:215], v[132:135]\n" \
R_"a[212:215], v29 offset:43008\n" \
C_"v209, v58, v59\n" \
M_"v[148:151], a[176:179], a[152:155], 0\n" \
"s_add_u32 m0, s31, 0x0\n" \
"s_mov_b32 s84, 0x100000\n" \
C_"v210, v72, v73\n" \
M_"v[148:151], a[240:243], a[216:219], v[148:151]\n" \
B_"v26, s[68:71], s84 offen offset:0 lds\n" \
C_"v211, v74, v75\n" \
M_"v[152:155], a[180:183], a[152:155], 0\n" \
"s_addk_i32 s84, 0x7c00\n" \
C_"v212, v52, v53\n" \
M_"v[152:155], a[244:247], a[216:219], v[152:155]\n" \
B_"v26, s[68:71], s84 offen offset:1024 lds\n" \
C_"v213, v54, v55\n" \
M_"v[156:159], a[184:187], a[152:155], 0\n" \
X_"a[166:167], v33 offset:33280\n" \
C_"v214, v68, v69\n" \
M_"v[156:159], a[248:251], a[216:219], v[156:159]\n" \
X_"a[228:229], v33 offset:36864\n" \
C_"v215, v70, v71\n" \
M_"v[160:163], a[188:191], a[152:155], 0\n" \
R_"a[152:155], v28 offset:45056\n" \
M_"v[160:163], a[252:255], a[216:219], v[160:163]\n" \
R_"a[216:219], v29 offset:45056\n" \
M_"v[176:179], a[188:191], a[156:159], 0\n" \
"s_addk_i32 s84, 0x7c00\n" \
M_"v[176:179], a[252:255], a[220:223], v[176:179]\n" \
B_"v27, s[68:71], s84 offen offset:2048 lds\n" \
M_"v[172:175], a[184:187], a[156:159], 0\n" \
X_"a[230:231], v33 offset:37376\n" \
M_"v[172:175], a[248:251], a[220:223], v[172:175]\n" \
X_"a[172:173], v35 offset:32768\n" \
M_"v[168:171], a[180:183], a[156:159], 0\n" \
"s_addk_i32 s84, 0x7c00\n" \
M_"v[168:171], a[244:247], a[220:223], v[168:171]\n" \
B_"v27, s[68:71], s84 offen offset:3072 lds\n" \
M_"v[164:167], a[176:179], a[156:159], 0\n" \
R_"a[156:159], v28 offset:47104\n" \
M_"v[164:167], a[240:243], a[220:223], v[164:167]\n" \
R_"a[220:223], v29 offset:47104\n" \
M_"v[84:87], a[176:179], a[136:139], 0\n" \
"s_add_u32 s89, s41, 0x3000\n" \
"buffer_store_dwordx4 v[192:195], v20, s[80:83], s89 offen offset:3072 sc1\n" \
M_"v[84:87], a[240:243], a[200:203], v[84:87]\n" \
X_"a[174:175], v35 offset:33280\n" \
M_"v[88:91], a[180:183], a[136:139], 0\n" \
X_"a[236:237], v35 offset:36864\n" \
M_"v[88:91], a[244:247], a[200:203], v[88:91]\n" \
X_"a[238:239], v35 offset:37376\n" \
M_"v[92:95], a[184:187], a[136:139], 0\n" \
M_"v[92:95], a[248:251], a[200:203], v[92:95]\n" \
M_"v[96:99], a[188:191], a[136:139], 0\n" \
R_"a[136:139], v28 offset:36864\n" \
M_"v[96:99], a[252:255], a[200:203], v[96:99]\n" \
R_"a[200:203], v29 offset:36864\n" \
M_"v[112:115], a[188:191], a[140:143], 0\n" \
"s_add_u32 m0, s30, 0x0\n" \
"s_movk_i32 s84, 0x180\n" \
M_"v[112:115], a[252:255], a[204:207], v[112:115]\n" \
B_"v24, s[76:79], s84 offen offset:0 lds\n" \
M_"v[108:111], a[184:187], a[140:143], 0\n" \
"s_movk_i32 s84, 0x2180\n" \
"s_nop 0\n" \
M_"v[108:111], a[248:251], a[204:207], v[108:111]\n" \
B_"v25, s[76:79], s84 offen offset:1024 lds\n" \
M_"v[104:107], a[180:183], a[140:143], 0\n" \
X_"a[168:169], v34 offset:32768\n" \
M_"v[104:107], a[244:247], a[204:207], v[104:107]\n" \
X_"a[170:171], v34 offset:33280\n" \
M_"v[100:103], a[176:179], a[140:143], 0\n" \
R_"a[140:143], v28 offset:38912\n" \
M_"v[100:103], a[240:243], a[204:207], v[100:103]\n" \
R_"a[204:207], v29 offset:38912\n" \
M_"v[52:55], a[176:179], a[128:131], 0\n" \
"s_movk_i32 s84, 0x4180\n" \
M_"v[52:55], a[240:243], a[192:195], v[52:55]\n" \
B_"v24, s[76:79], s84 offen offset:2048 lds\n" \
M_"v[56:59], a[180:183], a[128:131], 0\n" \
X_"a[232:233], v34 offset:36864\n" \
M_"v[56:59], a[244:247], a[192:195], v[56:59]\n" \
"s_movk_i32 s84, 0x6180\n" \
M_"v[60:63], a[184:187], a[128:131], 0\n" \
B_"v25, s[76:79], s84 offen offset:3072 lds\n" \
M_"v[60:63], a[248:251], a[192:195], v[60:63]\n" \
X_"a[234:235], v34 offset:37376\n" \
M_"v[64:67], a[188:191], a[128:131], 0\n" \
R_"a[128:131], v28 offset:32768\n" \
M_"v[64:67], a[252:255], a[192:195], v[64:67]\n" \
R_"a[192:195], v29 offset:32768\n" \
M_"v[80:83], a[188:191], a[132:135], 0\n" \
M_"v[80:83], a[252:255], a[196:199], v[80:83]\n" \
M_"v[76:79], a[184:187], a[132:135], 0\n" \
M_"v[76:79], a[248:251], a[196:199], v[76:79]\n" \
M_"v[72:75], a[180:183], a[132:135], 0\n" \
M_"v[72:75], a[244:247], a[196:199], v[72:75]\n" \
M_"v[68:71], a[176:179], a[132:135], 0\n" \
R_"a[132:135], v28 offset:34816\n" \
M_"v[68:71], a[240:243], a[196:199], v[68:71]\n" \
R_"a[196:199], v29 offset:34816\n" \
"s_waitcnt lgkmcnt(15)\n" \
M_"a[0:3], a[160:163], a[144:147], a[0:3]\n" \
"s_add_u32 m0, s30, 0x1000\n" \
"s_addk_i32 s84, 0x3000\n" \
M_"a[0:3], a[224:227], a[208:211], a[0:3]\n" \
B_"v24, s[76:79], s84 offen offset:0 lds\n" \
M_"a[4:7], a[164:167], a[144:147], a[4:7]\n" \
X_"a[176:177], v36 offset:32768\n" \
"v_mov_b32_e64 v11, 0\n" \
M_"a[4:7], a[228:231], a[208:211], a[4:7]\n" \
X_"a[178:179], v36 offset:33792\n" \
"s_and_b32 s5, s2, 7\n" \
"s_waitcnt lgkmcnt(15)\n" \
M_"a[12:15], a[172:175], a[144:147], a[12:15]\n" \
X_"a[240:241], v36 offset:40960\n" \
"s_lshl_b32 s6, s3, 1\n" \
"s_waitcnt lgkmcnt(15)\n" \
M_"a[12:15], a[236:239], a[208:211], a[12:15]\n" \
"s_add_u32 s88, s41, 0x3000\n" \
"buffer_store_dwordx4 v[196:199], v20, s[80:83], s88 offen offset:2048 sc1\n" \
"s_waitcnt lgkmcnt(11)\n" \
M_"a[8:11], a[144:147], a[168:171], a[8:11]\n" \
X_"a[242:243], v36 offset:41984\n" \
"s_lshl_b32 s4, s5, 2\n" \
"s_waitcnt lgkmcnt(8)\n" \
M_"a[8:11], a[208:211], a[232:235], a[8:11]\n" \
X_"a[180:181], v37 offset:32768\n" \
"s_bfe_u32 s7, s2, 0x20003\n" \
M_"a[24:27], a[148:151], a[168:171], a[24:27]\n" \
X_"a[182:183], v37 offset:33792\n" \
"s_or_b32 s12, s4, s7\n" \
M_"a[24:27], a[212:215], a[232:235], a[24:27]\n" \
X_"a[244:245], v37 offset:40960\n" \
"s_and_b32 s6, s6, 4\n" \
M_"a[28:31], a[172:175], a[148:151], a[28:31]\n" \
"s_and_b32 s8, s3, 1\n" \
"s_or_b32 s8, s6, s8\n" \
M_"a[28:31], a[236:239], a[212:215], a[28:31]\n" \
"s_addk_i32 s84, 0x2000\n" \
M_"a[20:23], a[164:167], a[148:151], a[20:23]\n" \
B_"v25, s[76:79], s84 offen offset:1024 lds\n" \
M_"a[20:23], a[228:231], a[212:215], a[20:23]\n" \
X_"a[246:247], v37 offset:41984\n" \
"s_lshl_b32 s6, s12, 4\n" \
M_"a[16:19], a[160:163], a[148:151], a[16:19]\n" \
X_"a[184:185], v38 offset:32768\n" \
"s_lshl_b32 s9, s3, 2\n" \
M_"a[16:19], a[224:227], a[212:215], a[16:19]\n" \
X_"a[186:187], v38 offset:33792\n" \
"s_or_b32 s6, s6, s9\n" \
M_"a[32:35], a[160:163], a[152:155], a[32:35]\n" \
"s_addk_i32 s84, 0x2000\n" \
M_"a[32:35], a[224:227], a[216:219], a[32:35]\n" \
B_"v24, s[76:79], s84 offen offset:2048 lds\n" \
M_"a[36:39], a[164:167], a[152:155], a[36:39]\n" \
X_"a[248:249], v38 offset:40960\n" \
"s_lshr_b32 s9, s2, 5\n" \
M_"a[36:39], a[228:231], a[216:219], a[36:39]\n" \
X_"a[250:251], v38 offset:41984\n" \
"s_mul_i32 s10, s9, 0xa00\n" \
M_"a[44:47], a[172:175], a[152:155], a[44:47]\n" \
X_"a[188:189], v39 offset:32768\n" \
"s_add_i32 s10, s10, 0x5bc1000\n" \
M_"a[44:47], a[236:239], a[216:219], a[44:47]\n" \
X_"a[190:191], v39 offset:33792\n" \
"s_or_b32 s6, s6, s10\n" \
M_"a[40:43], a[152:155], a[168:171], a[40:43]\n" \
"s_add_u32 s89, s41, 0x3000\n" \
"buffer_store_dwordx4 a[84:87], v20, s[80:83], s89 offen offset:1024 sc1\n" \
M_"a[40:43], a[216:219], a[232:235], a[40:43]\n" \
X_"a[252:253], v39 offset:40960\n" \
"s_lshl_b32 s13, s9, 3\n" \
M_"a[56:59], a[156:159], a[168:171], a[56:59]\n" \
X_"a[254:255], v39 offset:41984\n" \
"v_writelane_b32 v11, s6, 0\n" \
M_"a[56:59], a[220:223], a[232:235], a[56:59]\n" \
"s_or_b32 s6, s13, s5\n" \
"v_writelane_b32 v11, s10, 1\n" \
M_"a[60:63], a[172:175], a[156:159], a[60:63]\n" \
"s_lshl_b32 s6, s6, 10\n" \
"s_lshl_b32 s10, s3, 8\n" \
M_"a[60:63], a[236:239], a[220:223], a[60:63]\n" \
"s_addk_i32 s84, 0x2000\n" \
M_"a[52:55], a[164:167], a[156:159], a[52:55]\n" \
B_"v25, s[76:79], s84 offen offset:3072 lds\n" \
M_"a[52:55], a[228:231], a[220:223], a[52:55]\n" \
"s_or_b32 s6, s6, s10\n" \
"s_or_b32 s10, s6, 0x5c00200\n" \
M_"a[48:51], a[160:163], a[156:159], a[48:51]\n" \
"v_writelane_b32 v11, s10, 2\n" \
"s_lshl_b32 s10, s7, 2\n" \
M_"a[48:51], a[224:227], a[220:223], a[48:51]\n" \
"s_or_b32 s11, s10, s6\n" \
"s_or_b32 s11, s11, 0x5c00000\n" \
M_"a[64:67], a[164:167], a[140:143], a[64:67]\n" \
"v_writelane_b32 v11, s11, 3\n" \
"s_or_b32 s6, s6, 0x5c00080\n" \
M_"a[64:67], a[228:231], a[204:207], a[64:67]\n" \
"v_writelane_b32 v11, s6, 4\n" \
"s_or_b32 s6, s6, s10\n" \
M_"a[80:83], a[172:175], a[140:143], a[80:83]\n" \
"s_add_i32 s10, s3, s2\n" \
"v_writelane_b32 v11, s6, 5\n" \
M_"a[80:83], a[236:239], a[204:207], a[80:83]\n" \
"s_mov_b32 s6, 2.0\n" \
"s_mov_b32 s11, 0x5bc0000\n" \
M_"a[68:71], a[140:143], a[168:171], a[68:71]\n" \
"s_cmp_lg_u32 s10, 0\n" \
"s_cselect_b32 s10, s6, s11\n" \
"s_waitcnt vmcnt(11) lgkmcnt(0)\n" \
M_"a[68:71], a[204:207], a[232:235], a[68:71]\n" \
"s_barrier\n" \
"v_xor_b32_e32 v28, 0x20000, v28\n" \
"v_xor_b32_e32 v29, 0x20000, v29\n" \
M_"v[116:119], a[176:179], a[144:147], v[116:119]\n" \
"s_add_u32 m0, s40, 0x8000\n" \
"s_add_u32 s85, s39, 0x6000\n" \
M_"v[116:119], a[240:243], a[208:211], v[116:119]\n" \
B_"v20, s[80:83], s85 offen sc1 lds\n" \
M_"v[120:123], a[180:183], a[144:147], v[120:123]\n" \
X_"a[160:161], v32 offset:0\n" \
"s_lshr_b32 s14, s2, 3\n" \
M_"v[120:123], a[244:247], a[208:211], v[120:123]\n" \
X_"a[162:163], v32 offset:512\n" \
"v_writelane_b32 v11, s10, 57\n" \
M_"v[124:127], a[184:187], a[144:147], v[124:127]\n" \
"s_add_u32 m0, s40, 0x9000\n" \
"s_add_u32 s85, s39, 0x7000\n" \
M_"v[124:127], a[248:251], a[208:211], v[124:127]\n" \
B_"v20, s[80:83], s85 offen sc1 lds\n" \
M_"v[128:131], a[188:191], a[144:147], v[128:131]\n" \
R_"a[144:147], v28 offset:8192\n" \
"s_lshl_b32 s10, s5, 5\n" \
M_"v[128:131], a[252:255], a[208:211], v[128:131]\n" \
R_"a[208:211], v29 offset:8192\n" \
"s_min_u32 s15, s3, 2\n" \
M_"v[144:147], a[188:191], a[148:151], v[144:147]\n" \
"s_add_u32 m0, s40, 0xb000\n" \
"s_movk_i32 s84, 0x180\n" \
"s_nop 0\n" \
M_"v[144:147], a[252:255], a[212:215], v[144:147]\n" \
B_"v21, s[76:79], s84 offen offset:0 lds\n" \
M_"v[140:143], a[184:187], a[148:151], v[140:143]\n" \
X_"a[224:225], v32 offset:4096\n" \
"s_mul_i32 s11, s7, 0x120\n" \
M_"v[140:143], a[248:251], a[212:215], v[140:143]\n" \
X_"a[226:227], v32 offset:4608\n" \
M_"v[136:139], a[180:183], a[148:151], v[136:139]\n" \
"s_add_u32 s88, s41, 0x3000\n" \
"buffer_store_dwordx4 v[252:255], v20, s[80:83], s88 offen offset:0 sc1\n" \
M_"v[136:139], a[244:247], a[212:215], v[136:139]\n" \
X_"a[164:165], v33 offset:0\n" \
M_"v[132:135], a[176:179], a[148:151], v[132:135]\n" \
R_"a[148:151], v28 offset:10240\n" \
M_"v[132:135], a[240:243], a[212:215], v[132:135]\n" \
R_"a[212:215], v29 offset:10240\n" \
M_"v[148:151], a[176:179], a[152:155], v[148:151]\n" \
X_"a[166:167], v33 offset:512\n" \
M_"v[148:151], a[240:243], a[216:219], v[148:151]\n" \
"s_add_u32 m0, s31, 0x8000\n" \
"s_mov_b32 s84, 0x180000\n" \
M_"v[152:155], a[180:183], a[152:155], v[152:155]\n" \
B_"v26, s[68:71], s84 offen offset:0 lds\n" \
M_"v[152:155], a[244:247], a[216:219], v[152:155]\n" \
X_"a[228:229], v33 offset:4096\n" \
M_"v[156:159], a[184:187], a[152:155], v[156:159]\n" \
"s_add_u32 s16, s22, s11\n" \
"s_addc_u32 s17, s23, 0\n" \
M_"v[156:159], a[248:251], a[216:219], v[156:159]\n" \
"s_add_u32 s18, s24, s11\n" \
"v_writelane_b32 v11, s16, 9\n" \
"s_addc_u32 s19, s25, 0\n" \
M_"v[160:163], a[188:191], a[152:155], v[160:163]\n" \
R_"a[152:155], v28 offset:12288\n" \
"s_and_b32 s20, s17, 0xffff\n" \
M_"v[160:163], a[252:255], a[216:219], v[160:163]\n" \
R_"a[216:219], v29 offset:12288\n" \
"v_writelane_b32 v11, s20, 10\n" \
M_"v[176:179], a[188:191], a[156:159], v[176:179]\n" \
"s_addk_i32 s84, 0x7c00\n" \
M_"v[176:179], a[252:255], a[220:223], v[176:179]\n" \
B_"v26, s[68:71], s84 offen offset:1024 lds\n" \
M_"v[172:175], a[184:187], a[156:159], v[172:175]\n" \
"s_addk_i32 s84, 0x7c00\n" \
M_"v[172:175], a[248:251], a[220:223], v[172:175]\n" \
B_"v27, s[68:71], s84 offen offset:2048 lds\n" \
M_"v[168:171], a[180:183], a[156:159], v[168:171]\n" \
X_"a[230:231], v33 offset:4608\n" \
"s_and_b32 s20, s19, 0xffff\n" \
M_"v[168:171], a[244:247], a[220:223], v[168:171]\n" \
X_"a[172:173], v35 offset:0\n" \
"v_writelane_b32 v11, s18, 11\n" \
M_"v[164:167], a[176:179], a[156:159], v[164:167]\n" \
R_"a[156:159], v28 offset:14336\n" \
M_"v[164:167], a[240:243], a[220:223], v[164:167]\n" \
R_"a[220:223], v29 offset:14336\n" \
M_"v[84:87], a[176:179], a[136:139], v[84:87]\n" \
"s_addk_i32 s84, 0x7c00\n" \
M_"v[84:87], a[240:243], a[200:203], v[84:87]\n" \
B_"v27, s[68:71], s84 offen offset:3072 lds\n" \
M_"v[88:91], a[180:183], a[136:139], v[88:91]\n" \
"s_add_u32 m0, s30, 0x8000\n" \
"s_movk_i32 s84, 0x200\n" \
M_"v[88:91], a[244:247], a[200:203], v[88:91]\n" \
B_"v24, s[76:79], s84 offen offset:0 lds\n" \
M_"v[92:95], a[184:187], a[136:139], v[92:95]\n" \
X_"a[174:175], v35 offset:512\n" \
M_"v[92:95], a[248:251], a[200:203], v[92:95]\n" \
X_"a[236:237], v35 offset:4096\n" \
M_"v[96:99], a[188:191], a[136:139], v[96:99]\n" \
R_"a[136:139], v28 offset:4096\n" \
M_"v[96:99], a[252:255], a[200:203], v[96:99]\n" \
R_"a[200:203], v29 offset:4096\n" \
M_"v[112:115], a[188:191], a[140:143], v[112:115]\n" \
"s_movk_i32 s84, 0x2200\n" \
"s_nop 0\n" \
M_"v[112:115], a[252:255], a[204:207], v[112:115]\n" \
B_"v25, s[76:79], s84 offen offset:1024 lds\n" \
M_"v[108:111], a[184:187], a[140:143], v[108:111]\n" \
X_"a[238:239], v35 offset:4608\n" \
M_"v[108:111], a[248:251], a[204:207], v[108:111]\n" \
"s_add_u32 s89, s41, 0x1000\n" \
"buffer_store_dwordx4 v[200:203], v20, s[80:83], s89 offen offset:3072 sc1\n" \
M_"v[104:107], a[180:183], a[140:143], v[104:107]\n" \
X_"a[168:169], v34 offset:0\n" \
M_"v[104:107], a[244:247], a[204:207], v[104:107]\n" \
X_"a[170:171], v34 offset:512\n" \
M_"v[100:103], a[176:179], a[140:143], v[100:103]\n" \
R_"a[140:143], v28 offset:6144\n" \
M_"v[100:103], a[240:243], a[204:207], v[100:103]\n" \
R_"a[204:207], v29 offset:6144\n" \
M_"v[52:55], a[176:179], a[128:131], v[52:55]\n" \
"s_movk_i32 s84, 0x4200\n" \
M_"v[52:55], a[240:243], a[192:195], v[52:55]\n" \
B_"v24, s[76:79], s84 offen offset:2048 lds\n" \
M_"v[56:59], a[180:183], a[128:131], v[56:59]\n" \
X_"a[232:233], v34 offset:4096\n" \
M_"v[56:59], a[244:247], a[192:195], v[56:59]\n" \
"s_movk_i32 s84, 0x6200\n" \
M_"v[60:63], a[184:187], a[128:131], v[60:63]\n" \
B_"v25, s[76:79], s84 offen offset:3072 lds\n" \
M_"v[60:63], a[248:251], a[192:195], v[60:63]\n" \
X_"a[234:235], v34 offset:4608\n" \
M_"v[64:67], a[188:191], a[128:131], v[64:67]\n" \
R_"a[128:131], v28 offset:0\n" \
M_"v[64:67], a[252:255], a[192:195], v[64:67]\n" \
R_"a[192:195], v29 offset:0\n" \
M_"v[80:83], a[188:191], a[132:135], v[80:83]\n" \
"s_cmpk_lt_u32 s3, 3\n" \
"s_cselect_b32 s16, s16, s18\n" \
M_"v[80:83], a[252:255], a[196:199], v[80:83]\n" \
"s_movk_i32 s18, 0xfff\n" \
"v_writelane_b32 v11, s20, 12\n" \
M_"v[76:79], a[184:187], a[132:135], v[76:79]\n" \
"s_and_b32 s20, s29, 0xffff\n" \
"v_writelane_b32 v11, s28, 13\n" \
M_"v[76:79], a[248:251], a[196:199], v[76:79]\n" \
"v_writelane_b32 v11, s20, 14\n" \
"v_writelane_b32 v11, s16, 15\n" \
M_"v[72:75], a[180:183], a[132:135], v[72:75]\n" \
"s_cmpk_lt_u32 s3, 3\n" \
"s_cselect_b32 s16, s17, s19\n" \
M_"v[72:75], a[244:247], a[196:199], v[72:75]\n" \
"s_and_b32 s16, s16, 0xffff\n" \
"v_writelane_b32 v11, s16, 16\n" \
M_"v[68:71], a[176:179], a[132:135], v[68:71]\n" \
R_"a[132:135], v28 offset:2048\n" \
"s_mul_i32 s16, s5, 0x48\n" \
M_"v[68:71], a[240:243], a[196:199], v[68:71]\n" \
R_"a[196:199], v29 offset:2048\n" \
"s_waitcnt lgkmcnt(15)\n" \
M_"a[0:3], a[160:163], a[144:147], a[0:3]\n" \
"s_add_u32 m0, s30, 0x9000\n" \
"s_addk_i32 s84, 0x3000\n" \
M_"a[0:3], a[224:227], a[208:211], a[0:3]\n" \
B_"v24, s[76:79], s84 offen offset:0 lds\n" \
M_"a[4:7], a[164:167], a[144:147], a[4:7]\n" \
X_"a[176:177], v36 offset:0\n" \
"s_add_i32 s17, s16, 0x47\n" \
M_"a[4:7], a[228:231], a[208:211], a[4:7]\n" \
X_"a[178:179], v36 offset:1024\n" \
"s_waitcnt lgkmcnt(15)\n" \
M_"a[12:15], a[172:175], a[144:147], a[12:15]\n" \
X_"a[240:241], v36 offset:8192\n" \
"s_waitcnt lgkmcnt(13)\n" \
M_"a[12:15], a[236:239], a[208:211], a[12:15]\n" \
"s_add_u32 s88, s41, 0x1000\n" \
"buffer_store_dwordx4 v[204:207], v20, s[80:83], s88 offen offset:2048 sc1\n" \
"s_waitcnt lgkmcnt(11)\n" \
M_"a[8:11], a[144:147], a[168:171], a[8:11]\n" \
X_"a[242:243], v36 offset:9216\n" \
"s_waitcnt lgkmcnt(8)\n" \
M_"a[8:11], a[208:211], a[232:235], a[8:11]\n" \
X_"a[180:181], v37 offset:0\n" \
M_"a[24:27], a[148:151], a[168:171], a[24:27]\n" \
X_"a[182:183], v37 offset:1024\n" \
M_"a[24:27], a[212:215], a[232:235], a[24:27]\n" \
X_"a[244:245], v37 offset:8192\n" \
M_"a[28:31], a[172:175], a[148:151], a[28:31]\n" \
"s_cmpk_lt_u32 s3, 3\n" \
"s_cselect_b32 s17, s17, s18\n" \
M_"a[28:31], a[236:239], a[212:215], a[28:31]\n" \
"s_addk_i32 s84, 0x2000\n" \
M_"a[20:23], a[164:167], a[148:151], a[20:23]\n" \
B_"v25, s[76:79], s84 offen offset:1024 lds\n" \
M_"a[20:23], a[228:231], a[212:215], a[20:23]\n" \
X_"a[246:247], v37 offset:9216\n" \
"s_mulk_i32 s17, 0x480\n" \
M_"a[16:19], a[160:163], a[148:151], a[16:19]\n" \
X_"a[184:185], v38 offset:0\n" \
"s_addk_i32 s17, 0x120\n" \
M_"a[16:19], a[224:227], a[212:215], a[16:19]\n" \
X_"a[186:187], v38 offset:1024\n" \
"v_writelane_b32 v11, s17, 17\n" \
M_"a[32:35], a[160:163], a[152:155], a[32:35]\n" \
"s_addk_i32 s84, 0x2000\n" \
M_"a[32:35], a[224:227], a[216:219], a[32:35]\n" \
B_"v24, s[76:79], s84 offen offset:2048 lds\n" \
".fill (L_pb + 65248 - .) / 4, 4, 0xBF800000\n" \
"L_pp16:\n" \
"s_cbranch_execz L_pret\n" \
M_"a[36:39], a[164:167], a[152:155], a[36:39]\n" \
X_"a[248:249], v38 offset:8192\n" \
"s_lshl_b32 s17, s3, 5\n" \
M_"a[36:39], a[228:231], a[216:219], a[36:39]\n" \
X_"a[250:251], v38 offset:9216\n" \
"s_add_i32 s16, s16, s17\n" \
M_"a[44:47], a[172:175], a[152:155], a[44:47]\n" \
X_"a[188:189], v39 offset:0\n" \
"s_mulk_i32 s16, 0x480\n" \
M_"a[44:47], a[236:239], a[216:219], a[44:47]\n" \
X_"a[190:191], v39 offset:1024\n" \
"s_mul_i32 s17, s12, 0x24000\n" \
M_"a[40:43], a[152:155], a[168:171], a[40:43]\n" \
"s_add_u32 s89, s41, 0x1000\n" \
"buffer_store_dwordx4 v[208:211], v20, s[80:83], s89 offen offset:1024 sc1\n" \
M_"a[40:43], a[216:219], a[232:235], a[40:43]\n" \
X_"a[252:253], v39 offset:8192\n" \
"s_add_i32 s18, s17, 0x1b000\n" \
M_"a[56:59], a[156:159], a[168:171], a[56:59]\n" \
X_"a[254:255], v39 offset:9216\n" \
M_"a[56:59], a[220:223], a[232:235], a[56:59]\n" \
"s_cmpk_lt_u32 s3, 3\n" \
"s_cselect_b32 s16, s16, s18\n" \
M_"a[60:63], a[172:175], a[156:159], a[60:63]\n" \
"s_xor_b32 s18, s3, 2\n" \
"v_writelane_b32 v11, s16, 18\n" \
M_"a[60:63], a[236:239], a[220:223], a[60:63]\n" \
"s_addk_i32 s84, 0x2000\n" \
M_"a[52:55], a[164:167], a[156:159], a[52:55]\n" \
B_"v25, s[76:79], s84 offen offset:3072 lds\n" \
M_"a[52:55], a[228:231], a[220:223], a[52:55]\n" \
"s_mul_i32 s16, s15, 0x2400\n" \
"s_mul_i32 s15, s15, 0x9000\n" \
M_"a[48:51], a[160:163], a[156:159], a[48:51]\n" \
"s_bitset1_b32 s16, 17\n" \
"v_writelane_b32 v11, s16, 22\n" \
M_"a[48:51], a[224:227], a[220:223], a[48:51]\n" \
"s_add_i32 s17, s17, s15\n" \
"s_mul_i32 s15, s3, 0x2400\n" \
M_"a[64:67], a[164:167], a[140:143], a[64:67]\n" \
"v_writelane_b32 v11, s17, 29\n" \
"s_add_i32 s16, s15, 0xa400\n" \
M_"a[64:67], a[228:231], a[204:207], a[64:67]\n" \
"s_mov_b32 s17, 0x8000\n" \
M_"a[80:83], a[172:175], a[140:143], a[80:83]\n" \
"s_cmpk_lt_u32 s3, 3\n" \
"s_cselect_b32 s16, s16, s17\n" \
M_"a[80:83], a[236:239], a[204:207], a[80:83]\n" \
"s_mov_b32 s17, 0x1b000\n" \
M_"a[68:71], a[140:143], a[168:171], a[68:71]\n" \
"s_cmp_lg_u32 s18, 0\n" \
"s_cselect_b32 s17, s16, s17\n" \
"s_waitcnt vmcnt(11) lgkmcnt(0)\n" \
M_"a[68:71], a[204:207], a[232:235], a[68:71]\n" \
"s_barrier\n" \
"v_xor_b32_e32 v28, 0x20000, v28\n" \
"v_xor_b32_e32 v29, 0x20000, v29\n" \
M_"v[116:119], a[176:179], a[144:147], v[116:119]\n" \
"s_add_u32 m0, s40, 0x0\n" \
"s_nop 0\n" \
"s_add_u32 s85, s39, 0x8000\n" \
M_"v[116:119], a[240:243], a[208:211], v[116:119]\n" \
B_"v20, s[80:83], s85 offen sc1 lds\n" \
M_"v[120:123], a[180:183], a[144:147], v[120:123]\n" \
X_"a[160:161], v32 offset:32768\n" \
"v_writelane_b32 v11, s16, 23\n" \
M_"v[120:123], a[244:247], a[208:211], v[120:123]\n" \
X_"a[162:163], v32 offset:33280\n" \
"s_or_b32 s16, s15, 0x10000\n" \
M_"v[124:127], a[184:187], a[144:147], v[124:127]\n" \
"s_add_u32 m0, s40, 0x1000\n" \
"s_add_u32 s85, s39, 0x9000\n" \
M_"v[124:127], a[248:251], a[208:211], v[124:127]\n" \
B_"v20, s[80:83], s85 offen sc1 lds\n" \
M_"v[128:131], a[188:191], a[144:147], v[128:131]\n" \
R_"a[144:147], v28 offset:8192\n" \
"v_writelane_b32 v11, s17, 24\n" \
M_"v[128:131], a[252:255], a[208:211], v[128:131]\n" \
R_"a[208:211], v29 offset:8192\n" \
"s_add_i32 s17, s15, 0x19000\n" \
M_"v[144:147], a[188:191], a[148:151], v[144:147]\n" \
"s_add_u32 m0, s40, 0x3000\n" \
"s_movk_i32 s84, 0x200\n" \
M_"v[144:147], a[252:255], a[212:215], v[144:147]\n" \
B_"v21, s[76:79], s84 offen offset:0 lds\n" \
M_"v[140:143], a[184:187], a[148:151], v[140:143]\n" \
X_"a[224:225], v32 offset:36864\n" \
"v_writelane_b32 v11, s16, 25\n" \
M_"v[140:143], a[248:251], a[212:215], v[140:143]\n" \
X_"a[226:227], v32 offset:37376\n" \
"s_mov_b32 s16, 0\n" \
M_"v[136:139], a[180:183], a[148:151], v[136:139]\n" \
"s_add_u32 s88, s41, 0x1000\n" \
"buffer_store_dwordx4 v[212:215], v20, s[80:83], s88 offen offset:0 sc1\n" \
M_"v[136:139], a[244:247], a[212:215], v[136:139]\n" \
X_"a[164:165], v33 offset:32768\n" \
M_"v[132:135], a[176:179], a[148:151], v[132:135]\n" \
R_"a[148:151], v28 offset:10240\n" \
M_"v[132:135], a[240:243], a[212:215], v[132:135]\n" \
R_"a[212:215], v29 offset:10240\n" \
M_"v[148:151], a[176:179], a[152:155], v[148:151]\n" \
X_"a[166:167], v33 offset:33280\n" \
M_"v[148:151], a[240:243], a[216:219], v[148:151]\n" \
"s_add_u32 m0, s31, 0x0\n" \
"s_mov_b32 s84, 0x200000\n" \
M_"v[152:155], a[180:183], a[152:155], v[152:155]\n" \
B_"v26, s[68:71], s84 offen offset:0 lds\n" \
M_"v[152:155], a[244:247], a[216:219], v[152:155]\n" \
X_"a[228:229], v33 offset:36864\n" \
M_"v[156:159], a[184:187], a[152:155], v[156:159]\n" \
"s_cmpk_lt_u32 s3, 3\n" \
"s_cselect_b32 s17, s17, s16\n" \
M_"v[156:159], a[248:251], a[216:219], v[156:159]\n" \
"s_bitset1_b32 s15, 17\n" \
"v_writelane_b32 v11, s17, 26\n" \
M_"v[160:163], a[188:191], a[152:155], v[160:163]\n" \
R_"a[152:155], v28 offset:12288\n" \
"s_movk_i32 s17, 0x2400\n" \
M_"v[160:163], a[252:255], a[216:219], v[160:163]\n" \
R_"a[216:219], v29 offset:12288\n" \
M_"v[176:179], a[188:191], a[156:159], v[176:179]\n" \
"s_addk_i32 s84, 0x7c00\n" \
M_"v[176:179], a[252:255], a[220:223], v[176:179]\n" \
B_"v26, s[68:71], s84 offen offset:1024 lds\n" \
M_"v[172:175], a[184:187], a[156:159], v[172:175]\n" \
"s_addk_i32 s84, 0x7c00\n" \
"s_nop 0\n" \
M_"v[172:175], a[248:251], a[220:223], v[172:175]\n" \
B_"v27, s[68:71], s84 offen offset:2048 lds\n" \
M_"v[168:171], a[180:183], a[156:159], v[168:171]\n" \
X_"a[230:231], v33 offset:37376\n" \
M_"v[168:171], a[244:247], a[220:223], v[168:171]\n" \
X_"a[172:173], v35 offset:32768\n" \
M_"v[164:167], a[176:179], a[156:159], v[164:167]\n" \
R_"a[156:159], v28 offset:14336\n" \
M_"v[164:167], a[240:243], a[220:223], v[164:167]\n" \
R_"a[220:223], v29 offset:14336\n" \
M_"v[84:87], a[176:179], a[136:139], v[84:87]\n" \
"s_addk_i32 s84, 0x7c00\n" \
M_"v[84:87], a[240:243], a[200:203], v[84:87]\n" \
B_"v27, s[68:71], s84 offen offset:3072 lds\n" \
M_"v[88:91], a[180:183], a[136:139], v[88:91]\n" \
"s_add_u32 m0, s30, 0x20000\n" \
"s_movk_i32 s84, 0x280\n" \
M_"v[88:91], a[244:247], a[200:203], v[88:91]\n" \
B_"v24, s[76:79], s84 offen offset:0 lds\n" \
M_"v[92:95], a[184:187], a[136:139], v[92:95]\n" \
X_"a[174:175], v35 offset:33280\n" \
M_"v[92:95], a[248:251], a[200:203], v[92:95]\n" \
X_"a[236:237], v35 offset:36864\n" \
M_"v[96:99], a[188:191], a[136:139], v[96:99]\n" \
R_"a[136:139], v28 offset:4096\n" \
M_"v[96:99], a[252:255], a[200:203], v[96:99]\n" \
R_"a[200:203], v29 offset:4096\n" \
M_"v[112:115], a[188:191], a[140:143], v[112:115]\n" \
"s_movk_i32 s84, 0x2280\n" \
M_"v[112:115], a[252:255], a[204:207], v[112:115]\n" \
B_"v25, s[76:79], s84 offen offset:1024 lds\n" \
M_"v[108:111], a[184:187], a[140:143], v[108:111]\n" \
X_"a[238:239], v35 offset:37376\n" \
M_"v[108:111], a[248:251], a[204:207], v[108:111]\n" \
"s_add_u32 s89, s42, 0x0\n" \
"buffer_store_dwordx4 v[216:219], v20, s[80:83], s89 offen offset:1024 sc1\n" \
M_"v[104:107], a[180:183], a[140:143], v[104:107]\n" \
X_"a[168:169], v34 offset:32768\n" \
M_"v[104:107], a[244:247], a[204:207], v[104:107]\n" \
X_"a[170:171], v34 offset:33280\n" \
M_"v[100:103], a[176:179], a[140:143], v[100:103]\n" \
R_"a[140:143], v28 offset:6144\n" \
M_"v[100:103], a[240:243], a[204:207], v[100:103]\n" \
R_"a[204:207], v29 offset:6144\n" \
M_"v[52:55], a[176:179], a[128:131], v[52:55]\n" \
"s_movk_i32 s84, 0x4280\n" \
M_"v[52:55], a[240:243], a[192:195], v[52:55]\n" \
B_"v24, s[76:79], s84 offen offset:2048 lds\n" \
M_"v[56:59], a[180:183], a[128:131], v[56:59]\n" \
X_"a[232:233], v34 offset:36864\n" \
M_"v[56:59], a[244:247], a[192:195], v[56:59]\n" \
"s_movk_i32 s84, 0x6280\n" \
M_"v[60:63], a[184:187], a[128:131], v[60:63]\n" \
B_"v25, s[76:79], s84 offen offset:3072 lds\n" \
M_"v[60:63], a[248:251], a[192:195], v[60:63]\n" \
X_"a[234:235], v34 offset:37376\n" \
M_"v[64:67], a[188:191], a[128:131], v[64:67]\n" \
R_"a[128:131], v28 offset:0\n" \
M_"v[64:67], a[252:255], a[192:195], v[64:67]\n" \
R_"a[192:195], v29 offset:0\n" \
M_"v[80:83], a[188:191], a[132:135], v[80:83]\n" \
"s_cmpk_lt_u32 s3, 3\n" \
"s_cselect_b32 s15, s15, s17\n" \
M_"v[80:83], a[252:255], a[196:199], v[80:83]\n" \
"v_writelane_b32 v11, s15, 27\n" \
"s_lshl_b32 s15, s3, 10\n" \
M_"v[76:79], a[184:187], a[132:135], v[76:79]\n" \
"s_addk_i32 s15, 0x4800\n" \
"v_writelane_b32 v11, s15, 28\n" \
M_"v[76:79], a[248:251], a[196:199], v[76:79]\n" \
"s_add_i32 s15, s14, 1\n" \
"s_and_b32 s15, s15, 3\n" \
M_"v[72:75], a[180:183], a[132:135], v[72:75]\n" \
"s_or_b32 s15, s15, s4\n" \
"s_lshl_b32 s17, s15, 2\n" \
M_"v[72:75], a[244:247], a[196:199], v[72:75]\n" \
"s_or_b32 s17, s17, s3\n" \
"s_mul_i32 s17, s17, 0x9000\n" \
M_"v[68:71], a[176:179], a[132:135], v[68:71]\n" \
R_"a[132:135], v28 offset:2048\n" \
"v_writelane_b32 v11, s17, 30\n" \
M_"v[68:71], a[240:243], a[196:199], v[68:71]\n" \
R_"a[196:199], v29 offset:2048\n" \
"s_waitcnt lgkmcnt(15)\n" \
M_"a[0:3], a[160:163], a[144:147], a[0:3]\n" \
"s_add_u32 m0, s30, 0x21000\n" \
"s_addk_i32 s84, 0x3000\n" \
M_"a[0:3], a[224:227], a[208:211], a[0:3]\n" \
B_"v24, s[76:79], s84 offen offset:0 lds\n" \
M_"a[4:7], a[164:167], a[144:147], a[4:7]\n" \
X_"a[176:177], v36 offset:32768\n" \
"s_lshl_b32 s17, s12, 2\n" \
M_"a[4:7], a[228:231], a[208:211], a[4:7]\n" \
X_"a[178:179], v36 offset:33792\n" \
"s_or_b32 s17, s17, s3\n" \
"s_waitcnt lgkmcnt(15)\n" \
M_"a[12:15], a[172:175], a[144:147], a[12:15]\n" \
X_"a[240:241], v36 offset:40960\n" \
"s_add_i32 s14, s14, -1\n" \
"s_waitcnt lgkmcnt(13)\n" \
M_"a[12:15], a[236:239], a[208:211], a[12:15]\n" \
"s_add_u32 s88, s42, 0x0\n" \
"buffer_store_dwordx4 v[220:223], v20, s[80:83], s88 offen offset:0 sc1\n" \
"s_waitcnt lgkmcnt(11)\n" \
M_"a[8:11], a[144:147], a[168:171], a[8:11]\n" \
X_"a[242:243], v36 offset:41984\n" \
"s_xor_b32 s17, s17, 8\n" \
"s_waitcnt lgkmcnt(8)\n" \
M_"a[8:11], a[208:211], a[232:235], a[8:11]\n" \
X_"a[180:181], v37 offset:32768\n" \
"s_and_b32 s14, s14, 3\n" \
M_"a[24:27], a[148:151], a[168:171], a[24:27]\n" \
X_"a[182:183], v37 offset:33792\n" \
"s_mul_i32 s17, s17, 0x9000\n" \
M_"a[24:27], a[212:215], a[232:235], a[24:27]\n" \
X_"a[244:245], v37 offset:40960\n" \
"s_or_b32 s14, s14, s4\n" \
M_"a[28:31], a[172:175], a[148:151], a[28:31]\n" \
"v_writelane_b32 v11, s17, 31\n" \
"s_lshl_b32 s17, s14, 2\n" \
M_"a[28:31], a[236:239], a[212:215], a[28:31]\n" \
"s_addk_i32 s84, 0x2000\n" \
M_"a[20:23], a[164:167], a[148:151], a[20:23]\n" \
B_"v25, s[76:79], s84 offen offset:1024 lds\n" \
M_"a[20:23], a[228:231], a[212:215], a[20:23]\n" \
X_"a[246:247], v37 offset:41984\n" \
"s_or_b32 s17, s17, s3\n" \
M_"a[16:19], a[160:163], a[148:151], a[16:19]\n" \
X_"a[184:185], v38 offset:32768\n" \
"s_mul_i32 s17, s17, 0x9000\n" \
M_"a[16:19], a[224:227], a[212:215], a[16:19]\n" \
X_"a[186:187], v38 offset:33792\n" \
"v_writelane_b32 v11, s17, 32\n" \
M_"a[32:35], a[160:163], a[152:155], a[32:35]\n" \
"s_addk_i32 s84, 0x2000\n" \
M_"a[32:35], a[224:227], a[216:219], a[32:35]\n" \
B_"v24, s[76:79], s84 offen offset:2048 lds\n" \
M_"a[36:39], a[164:167], a[152:155], a[36:39]\n" \
X_"a[248:249], v38 offset:40960\n" \
"s_or_b32 s17, s8, s13\n" \
M_"a[36:39], a[228:231], a[216:219], a[36:39]\n" \
X_"a[250:251], v38 offset:41984\n" \
"s_mul_i32 s17, s17, 0x24800\n" \
M_"a[44:47], a[172:175], a[152:155], a[44:47]\n" \
X_"a[188:189], v39 offset:32768\n" \
"s_lshl_b32 s15, s15, 12\n" \
M_"a[44:47], a[236:239], a[216:219], a[44:47]\n" \
X_"a[190:191], v39 offset:33792\n" \
"s_add_i32 s18, s17, 0x3004800\n" \
M_"a[40:43], a[152:155], a[168:171], a[40:43]\n" \
"s_add_u32 s89, s42, 0x1000\n" \
"buffer_store_dwordx4 v[224:227], v20, s[80:83], s89 offen offset:3072 sc1\n" \
M_"a[40:43], a[216:219], a[232:235], a[40:43]\n" \
X_"a[252:253], v39 offset:40960\n" \
"s_lshl_b32 s12, s12, 12\n" \
M_"a[56:59], a[156:159], a[168:171], a[56:59]\n" \
X_"a[254:255], v39 offset:41984\n" \
"s_add_i32 s19, s18, s15\n" \
M_"a[56:59], a[220:223], a[232:235], a[56:59]\n" \
"v_writelane_b32 v11, s19, 33\n" \
"s_xor_b32 s12, s12, 0x2000\n" \
M_"a[60:63], a[172:175], a[156:159], a[60:63]\n" \
"s_lshl_b32 s14, s14, 12\n" \
"s_sub_i32 s13, s8, s13\n" \
M_"a[60:63], a[236:239], a[220:223], a[60:63]\n" \
"s_addk_i32 s84, 0x2000\n" \
M_"a[52:55], a[164:167], a[156:159], a[52:55]\n" \
B_"v25, s[76:79], s84 offen offset:3072 lds\n" \
M_"a[52:55], a[228:231], a[220:223], a[52:55]\n" \
"s_add_i32 s19, s18, s12\n" \
"v_writelane_b32 v11, s19, 34\n" \
M_"a[48:51], a[160:163], a[156:159], a[48:51]\n" \
"s_add_i32 s18, s18, s14\n" \
"s_mul_i32 s13, s13, 0x24800\n" \
M_"a[48:51], a[224:227], a[220:223], a[48:51]\n" \
"v_writelane_b32 v11, s18, 35\n" \
"s_add_i32 s18, s13, 0x4120800\n" \
M_"a[64:67], a[164:167], a[140:143], a[64:67]\n" \
"s_add_i32 s15, s18, s15\n" \
"v_writelane_b32 v11, s15, 36\n" \
M_"a[64:67], a[228:231], a[204:207], a[64:67]\n" \
"s_add_i32 s12, s18, s12\n" \
"v_writelane_b32 v11, s12, 37\n" \
M_"a[80:83], a[172:175], a[140:143], a[80:83]\n" \
"s_add_i32 s18, s18, s14\n" \
"s_or_b32 s12, s17, 0x3000000\n" \
M_"a[80:83], a[236:239], a[204:207], a[80:83]\n" \
"s_lshl_b32 s14, s5, 11\n" \
"v_writelane_b32 v11, s18, 38\n" \
M_"a[68:71], a[140:143], a[168:171], a[68:71]\n" \
"s_add_i32 s15, s12, s14\n" \
"v_writelane_b32 v11, s15, 39\n" \
"s_waitcnt vmcnt(11) lgkmcnt(0)\n" \
M_"a[68:71], a[204:207], a[232:235], a[68:71]\n" \
"s_barrier\n" \
M_"v[116:119], a[176:179], a[144:147], v[116:119]\n" \
"s_add_u32 m0, s40, 0x8000\n" \
"s_add_u32 s85, s39, 0xa000\n" \
M_"v[116:119], a[240:243], a[208:211], v[116:119]\n" \
B_"v20, s[80:83], s85 offen sc1 lds\n" \
M_"v[120:123], a[180:183], a[144:147], v[120:123]\n" \
X_"a[160:161], v32 offset:0\n" \
"v_accvgpr_read_b32 v212, a108\n" \
M_"v[120:123], a[244:247], a[208:211], v[120:123]\n" \
X_"a[162:163], v32 offset:512\n" \
"v_accvgpr_read_b32 v213, a109\n" \
M_"v[124:127], a[184:187], a[144:147], v[124:127]\n" \
"s_add_u32 m0, s40, 0x9000\n" \
"s_add_u32 s85, s39, 0xb000\n" \
M_"v[124:127], a[248:251], a[208:211], v[124:127]\n" \
B_"v20, s[80:83], s85 offen sc1 lds\n" \
M_"v[128:131], a[188:191], a[144:147], v[128:131]\n" \
R_"a[144:147], v28 offset:40960\n" \
"v_accvgpr_read_b32 v214, a110\n" \
M_"v[128:131], a[252:255], a[208:211], v[128:131]\n" \
R_"a[208:211], v29 offset:40960\n" \
"v_accvgpr_read_b32 v215, a111\n" \
M_"v[144:147], a[188:191], a[148:151], v[144:147]\n" \
"s_add_u32 m0, s40, 0xb000\n" \
"s_movk_i32 s84, 0x280\n" \
"s_nop 0\n" \
M_"v[144:147], a[252:255], a[212:215], v[144:147]\n" \
B_"v21, s[76:79], s84 offen offset:0 lds\n" \
M_"v[140:143], a[184:187], a[148:151], v[140:143]\n" \
X_"a[224:225], v32 offset:4096\n" \
"v_accvgpr_read_b32 v180, a124\n" \
M_"v[140:143], a[248:251], a[212:215], v[140:143]\n" \
X_"a[226:227], v32 offset:4608\n" \
"v_accvgpr_read_b32 v181, a125\n" \
M_"v[136:139], a[180:183], a[148:151], v[136:139]\n" \
"s_add_u32 s88, s42, 0x1000\n" \
"buffer_store_dwordx4 v[228:231], v20, s[80:83], s88 offen offset:2048 sc1\n" \
M_"v[136:139], a[244:247], a[212:215], v[136:139]\n" \
X_"a[164:165], v33 offset:0\n" \
"v_accvgpr_read_b32 v182, a126\n" \
M_"v[132:135], a[176:179], a[148:151], v[132:135]\n" \
R_"a[148:151], v28 offset:43008\n" \
"v_accvgpr_read_b32 v183, a127\n" \
M_"v[132:135], a[240:243], a[212:215], v[132:135]\n" \
R_"a[212:215], v29 offset:43008\n" \
C_"v212, v212, v213\n" \
M_"v[148:151], a[176:179], a[152:155], v[148:151]\n" \
X_"a[166:167], v33 offset:512\n" \
C_"v213, v214, v215\n" \
M_"v[148:151], a[240:243], a[216:219], v[148:151]\n" \
"s_add_u32 m0, s31, 0x8000\n" \
"s_mov_b32 s84, 0x280000\n" \
M_"v[152:155], a[180:183], a[152:155], v[152:155]\n" \
B_"v26, s[68:71], s84 offen offset:0 lds\n" \
M_"v[152:155], a[244:247], a[216:219], v[152:155]\n" \
X_"a[228:229], v33 offset:4096\n" \
C_"v214, v180, v181\n" \
M_"v[156:159], a[184:187], a[152:155], v[156:159]\n" \
C_"v215, v182, v183\n" \
"v_accvgpr_read_b32 v208, a104\n" \
M_"v[156:159], a[248:251], a[216:219], v[156:159]\n" \
"v_accvgpr_read_b32 v209, a105\n" \
"v_accvgpr_read_b32 v210, a106\n" \
M_"v[160:163], a[188:191], a[152:155], v[160:163]\n" \
R_"a[152:155], v28 offset:45056\n" \
"v_accvgpr_read_b32 v211, a107\n" \
M_"v[160:163], a[252:255], a[216:219], v[160:163]\n" \
R_"a[216:219], v29 offset:45056\n" \
"v_accvgpr_read_b32 v180, a120\n" \
M_"v[176:179], a[188:191], a[156:159], v[176:179]\n" \
"s_addk_i32 s84, 0x7c00\n" \
M_"v[176:179], a[252:255], a[220:223], v[176:179]\n" \
B_"v26, s[68:71], s84 offen offset:1024 lds\n" \
M_"v[172:175], a[184:187], a[156:159], v[172:175]\n" \
"s_addk_i32 s84, 0x7c00\n" \
M_"v[172:175], a[248:251], a[220:223], v[172:175]\n" \
B_"v27, s[68:71], s84 offen offset:2048 lds\n" \
M_"v[168:171], a[180:183], a[156:159], v[168:171]\n" \
X_"a[230:231], v33 offset:4608\n" \
"v_accvgpr_read_b32 v181, a121\n" \
M_"v[168:171], a[244:247], a[220:223], v[168:171]\n" \
X_"a[172:173], v35 offset:0\n" \
"v_accvgpr_read_b32 v182, a122\n" \
M_"v[164:167], a[176:179], a[156:159], v[164:167]\n" \
R_"a[156:159], v28 offset:47104\n" \
"v_accvgpr_read_b32 v183, a123\n" \
M_"v[164:167], a[240:243], a[220:223], v[164:167]\n" \
R_"a[220:223], v29 offset:47104\n" \
C_"v208, v208, v209\n" \
M_"v[84:87], a[176:179], a[136:139], v[84:87]\n" \
"s_addk_i32 s84, 0x7c00\n" \
M_"v[84:87], a[240:243], a[200:203], v[84:87]\n" \
B_"v27, s[68:71], s84 offen offset:3072 lds\n" \
M_"v[88:91], a[180:183], a[136:139], v[88:91]\n" \
"s_add_u32 m0, s30, 0x0\n" \
"s_movk_i32 s84, 0x300\n" \
"s_nop 0\n" \
M_"v[88:91], a[244:247], a[200:203], v[88:91]\n" \
B_"v24, s[76:79], s84 offen offset:0 lds\n" \
M_"v[92:95], a[184:187], a[136:139], v[92:95]\n" \
X_"a[174:175], v35 offset:512\n" \
C_"v209, v210, v211\n" \
M_"v[92:95], a[248:251], a[200:203], v[92:95]\n" \
X_"a[236:237], v35 offset:4096\n" \
C_"v210, v180, v181\n" \
M_"v[96:99], a[188:191], a[136:139], v[96:99]\n" \
R_"a[136:139], v28 offset:36864\n" \
C_"v211, v182, v183\n" \
M_"v[96:99], a[252:255], a[200:203], v[96:99]\n" \
R_"a[200:203], v29 offset:36864\n" \
"v_accvgpr_read_b32 v204, a100\n" \
M_"v[112:115], a[188:191], a[140:143], v[112:115]\n" \
"s_movk_i32 s84, 0x2300\n" \
"s_nop 0\n" \
M_"v[112:115], a[252:255], a[204:207], v[112:115]\n" \
B_"v25, s[76:79], s84 offen offset:1024 lds\n" \
M_"v[108:111], a[184:187], a[140:143], v[108:111]\n" \
X_"a[238:239], v35 offset:4608\n" \
"v_accvgpr_read_b32 v205, a101\n" \
M_"v[108:111], a[248:251], a[204:207], v[108:111]\n" \
"s_add_u32 s89, s42, 0x1000\n" \
"buffer_store_dwordx4 v[232:235], v20, s[80:83], s89 offen offset:1024 sc1\n" \
M_"v[104:107], a[180:183], a[140:143], v[104:107]\n" \
X_"a[168:169], v34 offset:0\n" \
"v_accvgpr_read_b32 v206, a102\n" \
M_"v[104:107], a[244:247], a[204:207], v[104:107]\n" \
X_"a[170:171], v34 offset:512\n" \
"v_accvgpr_read_b32 v207, a103\n" \
M_"v[100:103], a[176:179], a[140:143], v[100:103]\n" \
R_"a[140:143], v28 offset:38912\n" \
"v_accvgpr_read_b32 v180, a116\n" \
M_"v[100:103], a[240:243], a[204:207], v[100:103]\n" \
R_"a[204:207], v29 offset:38912\n" \
"v_accvgpr_read_b32 v181, a117\n" \
M_"v[52:55], a[176:179], a[128:131], v[52:55]\n" \
"s_movk_i32 s84, 0x4300\n" \
M_"v[52:55], a[240:243], a[192:195], v[52:55]\n" \
B_"v24, s[76:79], s84 offen offset:2048 lds\n" \
M_"v[56:59], a[180:183], a[128:131], v[56:59]\n" \
X_"a[232:233], v34 offset:4096\n" \
"v_accvgpr_read_b32 v182, a118\n" \
M_"v[56:59], a[244:247], a[192:195], v[56:59]\n" \
"s_movk_i32 s84, 0x6300\n" \
M_"v[60:63], a[184:187], a[128:131], v[60:63]\n" \
B_"v25, s[76:79], s84 offen offset:3072 lds\n" \
M_"v[60:63], a[248:251], a[192:195], v[60:63]\n" \
X_"a[234:235], v34 offset:4608\n" \
"v_accvgpr_read_b32 v183, a119\n" \
M_"v[64:67], a[188:191], a[128:131], v[64:67]\n" \
R_"a[128:131], v28 offset:32768\n" \
C_"v204, v204, v205\n" \
M_"v[64:67], a[252:255], a[192:195], v[64:67]\n" \
R_"a[192:195], v29 offset:32768\n" \
C_"v205, v206, v207\n" \
M_"v[80:83], a[188:191], a[132:135], v[80:83]\n" \
C_"v206, v180, v181\n" \
C_"v207, v182, v183\n" \
M_"v[80:83], a[252:255], a[196:199], v[80:83]\n" \
"v_accvgpr_read_b32 v200, a96\n" \
"v_accvgpr_read_b32 v201, a97\n" \
M_"v[76:79], a[184:187], a[132:135], v[76:79]\n" \
"v_accvgpr_read_b32 v202, a98\n" \
"v_accvgpr_read_b32 v203, a99\n" \
M_"v[76:79], a[248:251], a[196:199], v[76:79]\n" \
"v_accvgpr_read_b32 v180, a112\n" \
"v_accvgpr_read_b32 v181, a113\n" \
M_"v[72:75], a[180:183], a[132:135], v[72:75]\n" \
"v_accvgpr_read_b32 v182, a114\n" \
"v_accvgpr_read_b32 v183, a115\n" \
M_"v[72:75], a[244:247], a[196:199], v[72:75]\n" \
C_"v200, v200, v201\n" \
C_"v201, v202, v203\n" \
M_"v[68:71], a[176:179], a[132:135], v[68:71]\n" \
R_"a[132:135], v28 offset:34816\n" \
C_"v202, v180, v181\n" \
M_"v[68:71], a[240:243], a[196:199], v[68:71]\n" \
R_"a[196:199], v29 offset:34816\n" \
"s_waitcnt lgkmcnt(15)\n" \
M_"a[0:3], a[160:163], a[144:147], a[0:3]\n" \
"s_add_u32 m0, s30, 0x1000\n" \
"s_addk_i32 s84, 0x3000\n" \
M_"a[0:3], a[224:227], a[208:211], a[0:3]\n" \
B_"v24, s[76:79], s84 offen offset:0 lds\n" \
M_"a[4:7], a[164:167], a[144:147], a[4:7]\n" \
X_"a[176:177], v36 offset:0\n" \
C_"v203, v182, v183\n" \
M_"a[4:7], a[228:231], a[208:211], a[4:7]\n" \
X_"a[178:179], v36 offset:1024\n" \
"v_accvgpr_read_b32 v196, a76\n" \
"s_waitcnt lgkmcnt(15)\n" \
M_"a[12:15], a[172:175], a[144:147], a[12:15]\n" \
X_"a[240:241], v36 offset:8192\n" \
"v_accvgpr_read_b32 v197, a77\n" \
"s_waitcnt lgkmcnt(13)\n" \
M_"a[12:15], a[236:239], a[208:211], a[12:15]\n" \
"s_add_u32 s88, s42, 0x1000\n" \
"buffer_store_dwordx4 v[236:239], v20, s[80:83], s88 offen offset:0 sc1\n" \
"s_waitcnt lgkmcnt(11)\n" \
M_"a[8:11], a[144:147], a[168:171], a[8:11]\n" \
X_"a[242:243], v36 offset:9216\n" \
"v_accvgpr_read_b32 v198, a78\n" \
"s_waitcnt lgkmcnt(8)\n" \
M_"a[8:11], a[208:211], a[232:235], a[8:11]\n" \
X_"a[180:181], v37 offset:0\n" \
"v_accvgpr_read_b32 v199, a79\n" \
M_"a[24:27], a[148:151], a[168:171], a[24:27]\n" \
X_"a[182:183], v37 offset:1024\n" \
"v_accvgpr_read_b32 v180, a92\n" \
M_"a[24:27], a[212:215], a[232:235], a[24:27]\n" \
X_"a[244:245], v37 offset:8192\n" \
"v_accvgpr_read_b32 v181, a93\n" \
M_"a[28:31], a[172:175], a[148:151], a[28:31]\n" \
"v_accvgpr_read_b32 v182, a94\n" \
"v_accvgpr_read_b32 v183, a95\n" \
M_"a[28:31], a[236:239], a[212:215], a[28:31]\n" \
"s_addk_i32 s84, 0x2000\n" \
"s_nop 0\n" \
M_"a[20:23], a[164:167], a[148:151], a[20:23]\n" \
B_"v25, s[76:79], s84 offen offset:1024 lds\n" \
M_"a[20:23], a[228:231], a[212:215], a[20:23]\n" \
X_"a[246:247], v37 offset:9216\n" \
C_"v196, v196, v197\n" \
M_"a[16:19], a[160:163], a[148:151], a[16:19]\n" \
X_"a[184:185], v38 offset:0\n" \
C_"v197, v198, v199\n" \
M_"a[16:19], a[224:227], a[212:215], a[16:19]\n" \
X_"a[186:187], v38 offset:1024\n" \
C_"v198, v180, v181\n" \
M_"a[32:35], a[160:163], a[152:155], a[32:35]\n" \
"s_addk_i32 s84, 0x2000\n" \
"s_nop 0\n" \
M_"a[32:35], a[224:227], a[216:219], a[32:35]\n" \
B_"v24, s[76:79], s84 offen offset:2048 lds\n" \
M_"a[36:39], a[164:167], a[152:155], a[36:39]\n" \
X_"a[248:249], v38 offset:8192\n" \
C_"v199, v182, v183\n" \
M_"a[36:39], a[228:231], a[216:219], a[36:39]\n" \
X_"a[250:251], v38 offset:9216\n" \
".fill (L_pb + 69344 - .) / 4, 4, 0xBF800000\n" \
"L_pp17:\n" \
"s_cbranch_execz L_pret\n" \
"s_nop 0\n" \
"v_accvgpr_read_b32 v192, a72\n" \
M_"a[44:47], a[172:175], a[152:155], a[44:47]\n" \
X_"a[188:189], v39 offset:0\n" \
"v_accvgpr_read_b32 v193, a73\n" \
M_"a[44:47], a[236:239], a[216:219], a[44:47]\n" \
X_"a[190:191], v39 offset:1024\n" \
"v_accvgpr_read_b32 v194, a74\n" \
M_"a[40:43], a[152:155], a[168:171], a[40:43]\n" \
"s_add_u32 s89, s42, 0x3000\n" \
"buffer_store_dwordx4 v[240:243], v20, s[80:83], s89 offen offset:3072 sc1\n" \
M_"a[40:43], a[216:219], a[232:235], a[40:43]\n" \
X_"a[252:253], v39 offset:8192\n" \
"v_accvgpr_read_b32 v195, a75\n" \
M_"a[56:59], a[156:159], a[168:171], a[56:59]\n" \
X_"a[254:255], v39 offset:9216\n" \
"v_accvgpr_read_b32 v180, a88\n" \
M_"a[56:59], a[220:223], a[232:235], a[56:59]\n" \
"v_accvgpr_read_b32 v181, a89\n" \
"v_accvgpr_read_b32 v182, a90\n" \
M_"a[60:63], a[172:175], a[156:159], a[60:63]\n" \
"v_accvgpr_read_b32 v183, a91\n" \
C_"v192, v192, v193\n" \
M_"a[60:63], a[236:239], a[220:223], a[60:63]\n" \
"s_addk_i32 s84, 0x2000\n" \
M_"a[52:55], a[164:167], a[156:159], a[52:55]\n" \
B_"v25, s[76:79], s84 offen offset:3072 lds\n" \
M_"a[52:55], a[228:231], a[220:223], a[52:55]\n" \
C_"v193, v194, v195\n" \
C_"v194, v180, v181\n" \
M_"a[48:51], a[160:163], a[156:159], a[48:51]\n" \
C_"v195, v182, v183\n" \
"s_add_i32 s15, s13, 0x411c000\n" \
M_"a[48:51], a[224:227], a[220:223], a[48:51]\n" \
"s_add_i32 s18, s15, s14\n" \
"v_writelane_b32 v11, s18, 40\n" \
M_"a[64:67], a[164:167], a[140:143], a[64:67]\n" \
"s_add_i32 s17, s17, 0x3004000\n" \
"v_writelane_b32 v11, s17, 41\n" \
M_"a[64:67], a[228:231], a[204:207], a[64:67]\n" \
"s_add_i32 s13, s13, 0x4120000\n" \
"v_writelane_b32 v11, s13, 42\n" \
M_"a[80:83], a[172:175], a[140:143], a[80:83]\n" \
"s_lshl_b32 s13, s9, 2\n" \
"s_or_b32 s7, s13, s7\n" \
M_"a[80:83], a[236:239], a[204:207], a[80:83]\n" \
"s_mul_i32 s7, s7, 0x90000\n" \
"s_lshl_b32 s13, s3, 14\n" \
M_"a[68:71], a[140:143], a[168:171], a[68:71]\n" \
"s_lshl_b32 s9, s9, 7\n" \
"s_lshl_b32 s8, s8, 4\n" \
"s_waitcnt vmcnt(11) lgkmcnt(0)\n" \
M_"a[68:71], a[204:207], a[232:235], a[68:71]\n" \
"s_barrier\n" \
"v_mov_b32_e32 v45, s62\n" \
"s_mov_b64 exec, 1\n" \
"buffer_store_dword v45, off, s[80:83], s37 offset:0 sc1\n" \
"s_mov_b64 exec, -1\n" \
"v_xor_b32_e32 v28, 0x20000, v28\n" \
"v_xor_b32_e32 v29, 0x20000, v29\n" \
M_"v[116:119], a[176:179], a[144:147], v[116:119]\n" \
"s_add_u32 m0, s40, 0x0\n" \
"s_add_u32 s85, s39, 0xc000\n" \
M_"v[116:119], a[240:243], a[208:211], v[116:119]\n" \
B_"v20, s[80:83], s85 offen sc1 lds\n" \
M_"v[120:123], a[180:183], a[144:147], v[120:123]\n" \
X_"a[160:161], v32 offset:32768\n" \
"s_add_i32 s7, s7, s13\n" \
M_"v[120:123], a[244:247], a[208:211], v[120:123]\n" \
X_"a[162:163], v32 offset:33280\n" \
"s_or_b32 s13, s8, s9\n" \
M_"v[124:127], a[184:187], a[144:147], v[124:127]\n" \
"s_add_u32 m0, s40, 0x1000\n" \
"s_add_u32 s85, s39, 0xd000\n" \
M_"v[124:127], a[248:251], a[208:211], v[124:127]\n" \
B_"v20, s[80:83], s85 offen sc1 lds\n" \
M_"v[128:131], a[188:191], a[144:147], v[128:131]\n" \
R_"a[144:147], v28 offset:8192\n" \
"s_sub_i32 s8, s8, s9\n" \
M_"v[128:131], a[252:255], a[208:211], v[128:131]\n" \
R_"a[208:211], v29 offset:8192\n" \
"s_mulk_i32 s13, 0x480\n" \
M_"v[144:147], a[188:191], a[148:151], v[144:147]\n" \
X_"a[224:225], v32 offset:36864\n" \
"s_mulk_i32 s8, 0x480\n" \
M_"v[144:147], a[252:255], a[212:215], v[144:147]\n" \
"s_add_u32 m0, s40, 0x3000\n" \
"s_movk_i32 s84, 0x300\n" \
M_"v[140:143], a[184:187], a[148:151], v[140:143]\n" \
B_"v21, s[76:79], s84 offen offset:0 lds\n" \
M_"v[140:143], a[248:251], a[212:215], v[140:143]\n" \
X_"a[226:227], v32 offset:37376\n" \
"s_or_b32 s13, s13, s11\n" \
M_"v[136:139], a[180:183], a[148:151], v[136:139]\n" \
"s_or_b32 s8, s8, s11\n" \
"s_and_b32 s11, s2, 1\n" \
M_"v[136:139], a[244:247], a[212:215], v[136:139]\n" \
X_"a[164:165], v33 offset:32768\n" \
M_"v[132:135], a[176:179], a[148:151], v[132:135]\n" \
R_"a[148:151], v28 offset:10240\n" \
M_"v[132:135], a[240:243], a[212:215], v[132:135]\n" \
R_"a[212:215], v29 offset:10240\n" \
M_"v[148:151], a[176:179], a[152:155], v[148:151]\n" \
"s_add_u32 m0, s31, 0x0\n" \
"s_mov_b32 s84, 0x300000\n" \
M_"v[148:151], a[240:243], a[216:219], v[148:151]\n" \
B_"v26, s[68:71], s84 offen offset:0 lds\n" \
M_"v[152:155], a[180:183], a[152:155], v[152:155]\n" \
"s_addk_i32 s84, 0x7c00\n" \
M_"v[152:155], a[244:247], a[216:219], v[152:155]\n" \
B_"v26, s[68:71], s84 offen offset:1024 lds\n" \
M_"v[156:159], a[184:187], a[152:155], v[156:159]\n" \
X_"a[166:167], v33 offset:33280\n" \
M_"v[156:159], a[248:251], a[216:219], v[156:159]\n" \
X_"a[228:229], v33 offset:36864\n" \
M_"v[160:163], a[188:191], a[152:155], v[160:163]\n" \
R_"a[152:155], v28 offset:12288\n" \
M_"v[160:163], a[252:255], a[216:219], v[160:163]\n" \
R_"a[216:219], v29 offset:12288\n" \
M_"v[176:179], a[188:191], a[156:159], v[176:179]\n" \
"s_addk_i32 s84, 0x7c00\n" \
M_"v[176:179], a[252:255], a[220:223], v[176:179]\n" \
B_"v27, s[68:71], s84 offen offset:2048 lds\n" \
M_"v[172:175], a[184:187], a[156:159], v[172:175]\n" \
X_"a[230:231], v33 offset:37376\n" \
M_"v[172:175], a[248:251], a[220:223], v[172:175]\n" \
X_"a[172:173], v35 offset:32768\n" \
M_"v[168:171], a[180:183], a[156:159], v[168:171]\n" \
"s_addk_i32 s84, 0x7c00\n" \
M_"v[168:171], a[244:247], a[220:223], v[168:171]\n" \
B_"v27, s[68:71], s84 offen offset:3072 lds\n" \
M_"v[164:167], a[176:179], a[156:159], v[164:167]\n" \
R_"a[156:159], v28 offset:14336\n" \
M_"v[164:167], a[240:243], a[220:223], v[164:167]\n" \
R_"a[220:223], v29 offset:14336\n" \
M_"v[84:87], a[176:179], a[136:139], v[84:87]\n" \
"s_add_u32 s88, s42, 0x3000\n" \
"buffer_store_dwordx4 v[244:247], v20, s[80:83], s88 offen offset:2048 sc1\n" \
M_"v[84:87], a[240:243], a[200:203], v[84:87]\n" \
X_"a[174:175], v35 offset:33280\n" \
M_"v[88:91], a[180:183], a[136:139], v[88:91]\n" \
X_"a[236:237], v35 offset:36864\n" \
M_"v[88:91], a[244:247], a[200:203], v[88:91]\n" \
X_"a[238:239], v35 offset:37376\n" \
M_"v[92:95], a[184:187], a[136:139], v[92:95]\n" \
"s_cmp_lg_u32 s11, 0\n" \
"s_cselect_b32 s12, s15, s12\n" \
M_"v[92:95], a[248:251], a[200:203], v[92:95]\n" \
"s_bfe_u32 s9, s2, 0x20001\n" \
"s_add_i32 s15, s7, 0x4240000\n" \
M_"v[96:99], a[188:191], a[136:139], v[96:99]\n" \
R_"a[136:139], v28 offset:4096\n" \
"s_addk_i32 s12, 0x4000\n" \
M_"v[96:99], a[252:255], a[200:203], v[96:99]\n" \
R_"a[200:203], v29 offset:4096\n" \
"s_lshl_b32 s5, s5, 16\n" \
M_"v[112:115], a[188:191], a[140:143], v[112:115]\n" \
"s_add_u32 m0, s30, 0x8000\n" \
"s_movk_i32 s84, 0x380\n" \
M_"v[112:115], a[252:255], a[204:207], v[112:115]\n" \
B_"v24, s[76:79], s84 offen offset:0 lds\n" \
M_"v[108:111], a[184:187], a[140:143], v[108:111]\n" \
"s_movk_i32 s84, 0x2380\n" \
M_"v[108:111], a[248:251], a[204:207], v[108:111]\n" \
B_"v25, s[76:79], s84 offen offset:1024 lds\n" \
M_"v[104:107], a[180:183], a[140:143], v[104:107]\n" \
X_"a[168:169], v34 offset:32768\n" \
"s_xor_b32 s9, s9, s3\n" \
M_"v[104:107], a[244:247], a[204:207], v[104:107]\n" \
X_"a[170:171], v34 offset:33280\n" \
M_"v[100:103], a[176:179], a[140:143], v[100:103]\n" \
R_"a[140:143], v28 offset:6144\n" \
M_"v[100:103], a[240:243], a[204:207], v[100:103]\n" \
R_"a[204:207], v29 offset:6144\n" \
M_"v[52:55], a[176:179], a[128:131], v[52:55]\n" \
"s_movk_i32 s84, 0x4380\n" \
M_"v[52:55], a[240:243], a[192:195], v[52:55]\n" \
B_"v24, s[76:79], s84 offen offset:2048 lds\n" \
M_"v[56:59], a[180:183], a[128:131], v[56:59]\n" \
X_"a[232:233], v34 offset:36864\n" \
M_"v[56:59], a[244:247], a[192:195], v[56:59]\n" \
"s_movk_i32 s84, 0x6380\n" \
M_"v[60:63], a[184:187], a[128:131], v[60:63]\n" \
B_"v25, s[76:79], s84 offen offset:3072 lds\n" \
M_"v[60:63], a[248:251], a[192:195], v[60:63]\n" \
X_"a[234:235], v34 offset:37376\n" \
M_"v[64:67], a[188:191], a[128:131], v[64:67]\n" \
R_"a[128:131], v28 offset:0\n" \
M_"v[64:67], a[252:255], a[192:195], v[64:67]\n" \
R_"a[192:195], v29 offset:0\n" \
M_"v[80:83], a[188:191], a[132:135], v[80:83]\n" \
"s_cmp_lg_u32 s9, 0\n" \
"s_cselect_b32 s12, s6, s12\n" \
M_"v[80:83], a[252:255], a[196:199], v[80:83]\n" \
"s_add_i32 s5, s15, s5\n" \
"v_writelane_b32 v11, s12, 43\n" \
M_"v[76:79], a[184:187], a[132:135], v[76:79]\n" \
"s_add_i32 s7, s7, 0x42c0000\n" \
"v_writelane_b32 v11, s5, 44\n" \
M_"v[76:79], a[248:251], a[196:199], v[76:79]\n" \
"s_lshl_b32 s5, s11, 10\n" \
"s_or_b32 s5, s7, s5\n" \
M_"v[72:75], a[180:183], a[132:135], v[72:75]\n" \
"s_cmp_lg_u32 s9, 0\n" \
"s_cselect_b32 s5, s6, s5\n" \
M_"v[72:75], a[244:247], a[196:199], v[72:75]\n" \
"s_add_i32 s8, s8, 0x21c000\n" \
"v_writelane_b32 v11, s5, 45\n" \
M_"v[68:71], a[176:179], a[132:135], v[68:71]\n" \
R_"a[132:135], v28 offset:2048\n" \
"s_add_i32 s5, s13, s10\n" \
M_"v[68:71], a[240:243], a[196:199], v[68:71]\n" \
R_"a[196:199], v29 offset:2048\n" \
"s_waitcnt lgkmcnt(15)\n" \
M_"a[0:3], a[160:163], a[144:147], a[0:3]\n" \
"s_add_u32 m0, s30, 0x9000\n" \
"s_addk_i32 s84, 0x3000\n" \
M_"a[0:3], a[224:227], a[208:211], a[0:3]\n" \
B_"v24, s[76:79], s84 offen offset:0 lds\n" \
M_"a[4:7], a[164:167], a[144:147], a[4:7]\n" \
X_"a[176:177], v36 offset:32768\n" \
"v_writelane_b32 v11, s5, 46\n" \
M_"a[4:7], a[228:231], a[208:211], a[4:7]\n" \
X_"a[178:179], v36 offset:33792\n" \
"s_add_i32 s5, s8, s10\n" \
"s_waitcnt lgkmcnt(15)\n" \
M_"a[12:15], a[172:175], a[144:147], a[12:15]\n" \
X_"a[240:241], v36 offset:40960\n" \
"v_writelane_b32 v11, s5, 47\n" \
"s_waitcnt lgkmcnt(15)\n" \
M_"a[12:15], a[236:239], a[208:211], a[12:15]\n" \
"s_add_u32 s89, s42, 0x3000\n" \
"buffer_store_dwordx4 v[184:187], v20, s[80:83], s89 offen offset:1024 sc1\n" \
"s_waitcnt lgkmcnt(11)\n" \
M_"a[8:11], a[144:147], a[168:171], a[8:11]\n" \
X_"a[242:243], v36 offset:41984\n" \
"s_waitcnt lgkmcnt(8)\n" \
M_"a[8:11], a[208:211], a[232:235], a[8:11]\n" \
X_"a[180:181], v37 offset:32768\n" \
M_"a[24:27], a[148:151], a[168:171], a[24:27]\n" \
X_"a[182:183], v37 offset:33792\n" \
M_"a[24:27], a[212:215], a[232:235], a[24:27]\n" \
X_"a[244:245], v37 offset:40960\n" \
M_"a[28:31], a[172:175], a[148:151], a[28:31]\n" \
"s_cmp_lg_u32 s11, 0\n" \
"s_cselect_b32 s5, s8, s13\n" \
M_"a[28:31], a[236:239], a[212:215], a[28:31]\n" \
"s_addk_i32 s84, 0x2000\n" \
M_"a[20:23], a[164:167], a[148:151], a[20:23]\n" \
B_"v25, s[76:79], s84 offen offset:1024 lds\n" \
M_"a[20:23], a[228:231], a[212:215], a[20:23]\n" \
X_"a[246:247], v37 offset:41984\n" \
"s_addk_i32 s5, 0x100\n" \
M_"a[16:19], a[160:163], a[148:151], a[16:19]\n" \
X_"a[184:185], v38 offset:32768\n" \
M_"a[16:19], a[224:227], a[212:215], a[16:19]\n" \
X_"a[186:187], v38 offset:33792\n" \
M_"a[32:35], a[160:163], a[152:155], a[32:35]\n" \
"s_addk_i32 s84, 0x2000\n" \
"s_nop 0\n" \
M_"a[32:35], a[224:227], a[216:219], a[32:35]\n" \
B_"v24, s[76:79], s84 offen offset:2048 lds\n" \
M_"a[36:39], a[164:167], a[152:155], a[36:39]\n" \
X_"a[248:249], v38 offset:40960\n" \
M_"a[36:39], a[228:231], a[216:219], a[36:39]\n" \
X_"a[250:251], v38 offset:41984\n" \
M_"a[44:47], a[172:175], a[152:155], a[44:47]\n" \
X_"a[188:189], v39 offset:32768\n" \
M_"a[44:47], a[236:239], a[216:219], a[44:47]\n" \
X_"a[190:191], v39 offset:33792\n" \
M_"a[40:43], a[152:155], a[168:171], a[40:43]\n" \
"s_add_u32 s88, s42, 0x3000\n" \
"buffer_store_dwordx4 v[188:191], v20, s[80:83], s88 offen offset:0 sc1\n" \
M_"a[40:43], a[216:219], a[232:235], a[40:43]\n" \
X_"a[252:253], v39 offset:40960\n" \
M_"a[56:59], a[156:159], a[168:171], a[56:59]\n" \
X_"a[254:255], v39 offset:41984\n" \
M_"a[56:59], a[220:223], a[232:235], a[56:59]\n" \
"s_cmp_lg_u32 s9, 0\n" \
"s_cselect_b32 s5, s6, s5\n" \
M_"a[60:63], a[172:175], a[156:159], a[60:63]\n" \
"v_writelane_b32 v11, s5, 48\n" \
"s_or_b32 s5, s7, s14\n" \
M_"a[60:63], a[236:239], a[220:223], a[60:63]\n" \
"s_addk_i32 s84, 0x2000\n" \
M_"a[52:55], a[164:167], a[156:159], a[52:55]\n" \
B_"v25, s[76:79], s84 offen offset:3072 lds\n" \
M_"a[52:55], a[228:231], a[220:223], a[52:55]\n" \
"v_writelane_b32 v11, s5, 56\n" \
"s_lshl_b32 s5, s3, 17\n" \
M_"a[48:51], a[160:163], a[156:159], a[48:51]\n" \
"s_add_i32 s5, s15, s5\n" \
"s_or_b32 s4, s5, s4\n" \
M_"a[48:51], a[224:227], a[220:223], a[48:51]\n" \
"s_or_b32 s4, s4, 0x3840\n" \
"v_writelane_b32 v11, s4, 6\n" \
M_"a[64:67], a[164:167], a[140:143], a[64:67]\n" \
"s_mov_b32 s4, 0xff0000\n" \
M_"a[64:67], a[228:231], a[204:207], a[64:67]\n" \
"s_cmp_lg_u32 s9, 0\n" \
"s_cselect_b32 s4, s16, s4\n" \
M_"a[80:83], a[172:175], a[140:143], a[80:83]\n" \
"v_writelane_b32 v11, s4, 7\n" \
"s_lshl_b32 s4, s2, 16\n" \
M_"a[80:83], a[236:239], a[204:207], a[80:83]\n" \
"s_add_i32 s5, s4, 0x10000\n" \
"s_and_b32 s5, s5, 0x70000\n" \
M_"a[68:71], a[140:143], a[168:171], a[68:71]\n" \
"s_add_i32 s5, s15, s5\n" \
"v_writelane_b32 v11, s5, 49\n" \
"s_waitcnt vmcnt(11) lgkmcnt(0)\n" \
M_"a[68:71], a[204:207], a[232:235], a[68:71]\n" \
"s_barrier\n" \
"v_xor_b32_e32 v28, 0x20000, v28\n" \
"v_xor_b32_e32 v29, 0x20000, v29\n" \
M_"v[116:119], a[176:179], a[144:147], v[116:119]\n" \
"s_add_u32 m0, s40, 0x8000\n" \
"s_add_u32 s85, s39, 0xe000\n" \
M_"v[116:119], a[240:243], a[208:211], v[116:119]\n" \
B_"v20, s[80:83], s85 offen sc1 lds\n" \
M_"v[120:123], a[180:183], a[144:147], v[120:123]\n" \
X_"a[160:161], v32 offset:0\n" \
"s_add_i32 s5, s4, 0x20000\n" \
M_"v[120:123], a[244:247], a[208:211], v[120:123]\n" \
X_"a[162:163], v32 offset:512\n" \
"s_and_b32 s5, s5, 0x70000\n" \
M_"v[124:127], a[184:187], a[144:147], v[124:127]\n" \
"s_add_u32 m0, s40, 0x9000\n" \
"s_add_u32 s85, s39, 0xf000\n" \
M_"v[124:127], a[248:251], a[208:211], v[124:127]\n" \
B_"v20, s[80:83], s85 offen sc1 lds\n" \
M_"v[128:131], a[188:191], a[144:147], v[128:131]\n" \
R_"a[144:147], v28 offset:8192\n" \
"s_add_i32 s5, s15, s5\n" \
M_"v[128:131], a[252:255], a[208:211], v[128:131]\n" \
R_"a[208:211], v29 offset:8192\n" \
"s_bitset1_b32 s5, 11\n" \
M_"v[144:147], a[188:191], a[148:151], v[144:147]\n" \
X_"a[224:225], v32 offset:4096\n" \
"v_writelane_b32 v11, s5, 50\n" \
M_"v[144:147], a[252:255], a[212:215], v[144:147]\n" \
"s_add_u32 m0, s40, 0xb000\n" \
"s_movk_i32 s84, 0x380\n" \
M_"v[140:143], a[184:187], a[148:151], v[140:143]\n" \
B_"v21, s[76:79], s84 offen offset:0 lds\n" \
M_"v[140:143], a[248:251], a[212:215], v[140:143]\n" \
X_"a[226:227], v32 offset:4608\n" \
"s_add_i32 s5, s4, 0x30000\n" \
M_"v[136:139], a[180:183], a[148:151], v[136:139]\n" \
"s_and_b32 s5, s5, 0x70000\n" \
"s_add_i32 s5, s15, s5\n" \
M_"v[136:139], a[244:247], a[212:215], v[136:139]\n" \
X_"a[164:165], v33 offset:0\n" \
"s_bitset1_b32 s5, 12\n" \
M_"v[132:135], a[176:179], a[148:151], v[132:135]\n" \
R_"a[148:151], v28 offset:10240\n" \
"v_writelane_b32 v11, s5, 51\n" \
M_"v[132:135], a[240:243], a[212:215], v[132:135]\n" \
R_"a[212:215], v29 offset:10240\n" \
"s_and_b32 s5, s4, 0x70000\n" \
M_"v[148:151], a[176:179], a[152:155], v[148:151]\n" \
"s_add_u32 m0, s31, 0x8000\n" \
"s_mov_b32 s84, 0x380000\n" \
M_"v[148:151], a[240:243], a[216:219], v[148:151]\n" \
B_"v26, s[68:71], s84 offen offset:0 lds\n" \
M_"v[152:155], a[180:183], a[152:155], v[152:155]\n" \
"s_addk_i32 s84, 0x7c00\n" \
M_"v[152:155], a[244:247], a[216:219], v[152:155]\n" \
B_"v26, s[68:71], s84 offen offset:1024 lds\n" \
M_"v[156:159], a[184:187], a[152:155], v[156:159]\n" \
X_"a[166:167], v33 offset:512\n" \
"s_xor_b32 s5, s5, 0x40000\n" \
M_"v[156:159], a[248:251], a[216:219], v[156:159]\n" \
X_"a[228:229], v33 offset:4096\n" \
"s_add_i32 s5, s15, s5\n" \
M_"v[160:163], a[188:191], a[152:155], v[160:163]\n" \
R_"a[152:155], v28 offset:12288\n" \
"s_or_b32 s5, s5, 0x1800\n" \
M_"v[160:163], a[252:255], a[216:219], v[160:163]\n" \
R_"a[216:219], v29 offset:12288\n" \
"v_writelane_b32 v11, s5, 52\n" \
M_"v[176:179], a[188:191], a[156:159], v[176:179]\n" \
"s_addk_i32 s84, 0x7c00\n" \
M_"v[176:179], a[252:255], a[220:223], v[176:179]\n" \
B_"v27, s[68:71], s84 offen offset:2048 lds\n" \
M_"v[172:175], a[184:187], a[156:159], v[172:175]\n" \
X_"a[230:231], v33 offset:4608\n" \
"s_add_i32 s5, s4, 0x50000\n" \
M_"v[172:175], a[248:251], a[220:223], v[172:175]\n" \
X_"a[172:173], v35 offset:0\n" \
"s_and_b32 s5, s5, 0x70000\n" \
M_"v[168:171], a[180:183], a[156:159], v[168:171]\n" \
"s_addk_i32 s84, 0x7c00\n" \
M_"v[168:171], a[244:247], a[220:223], v[168:171]\n" \
B_"v27, s[68:71], s84 offen offset:3072 lds\n" \
M_"v[164:167], a[176:179], a[156:159], v[164:167]\n" \
R_"a[156:159], v28 offset:14336\n" \
"s_add_i32 s5, s15, s5\n" \
M_"v[164:167], a[240:243], a[220:223], v[164:167]\n" \
R_"a[220:223], v29 offset:14336\n" \
"s_bitset1_b32 s5, 13\n" \
M_"v[84:87], a[176:179], a[136:139], v[84:87]\n" \
"s_add_u32 s89, s42, 0x0\n" \
"s_nop 0\n" \
"buffer_store_dwordx4 v[192:195], v20, s[80:83], s89 offen offset:2048 sc1\n" \
M_"v[84:87], a[240:243], a[200:203], v[84:87]\n" \
X_"a[174:175], v35 offset:512\n" \
"v_writelane_b32 v11, s5, 53\n" \
M_"v[88:91], a[180:183], a[136:139], v[88:91]\n" \
X_"a[236:237], v35 offset:4096\n" \
"s_add_i32 s5, s4, 0x60000\n" \
M_"v[88:91], a[244:247], a[200:203], v[88:91]\n" \
X_"a[238:239], v35 offset:4608\n" \
"s_and_b32 s5, s5, 0x70000\n" \
M_"v[92:95], a[184:187], a[136:139], v[92:95]\n" \
"s_add_i32 s4, s4, 0x70000\n" \
"s_add_i32 s5, s15, s5\n" \
M_"v[92:95], a[248:251], a[200:203], v[92:95]\n" \
"s_and_b32 s4, s4, 0x70000\n" \
"s_or_b32 s5, s5, 0x2800\n" \
M_"v[96:99], a[188:191], a[136:139], v[96:99]\n" \
R_"a[136:139], v28 offset:4096\n" \
"v_writelane_b32 v11, s5, 54\n" \
M_"v[96:99], a[252:255], a[200:203], v[96:99]\n" \
R_"a[200:203], v29 offset:4096\n" \
"s_add_i32 s15, s15, s4\n" \
M_"v[112:115], a[188:191], a[140:143], v[112:115]\n" \
"s_add_u32 m0, s30, 0x20000\n" \
"s_movk_i32 s84, 0x400\n" \
M_"v[112:115], a[252:255], a[204:207], v[112:115]\n" \
B_"v24, s[76:79], s84 offen offset:0 lds\n" \
M_"v[108:111], a[184:187], a[140:143], v[108:111]\n" \
"s_movk_i32 s84, 0x2400\n" \
M_"v[108:111], a[248:251], a[204:207], v[108:111]\n" \
B_"v25, s[76:79], s84 offen offset:1024 lds\n" \
M_"v[104:107], a[180:183], a[140:143], v[104:107]\n" \
X_"a[168:169], v34 offset:0\n" \
"s_or_b32 s4, s15, 0x3000\n" \
M_"v[104:107], a[244:247], a[204:207], v[104:107]\n" \
X_"a[170:171], v34 offset:512\n" \
"v_writelane_b32 v11, s4, 55\n" \
M_"v[100:103], a[176:179], a[140:143], v[100:103]\n" \
R_"a[140:143], v28 offset:6144\n" \
"s_movk_i32 s11, 0x4000\n" \
M_"v[100:103], a[240:243], a[204:207], v[100:103]\n" \
R_"a[204:207], v29 offset:6144\n" \
"s_lshl_b32 s4, s2, 2\n" \
M_"v[52:55], a[176:179], a[128:131], v[52:55]\n" \
"s_movk_i32 s84, 0x4400\n" \
M_"v[52:55], a[240:243], a[192:195], v[52:55]\n" \
B_"v24, s[76:79], s84 offen offset:2048 lds\n" \
M_"v[56:59], a[180:183], a[128:131], v[56:59]\n" \
X_"a[232:233], v34 offset:4096\n" \
"s_and_b32 s4, s4, 28\n" \
M_"v[56:59], a[244:247], a[192:195], v[56:59]\n" \
"s_movk_i32 s84, 0x6400\n" \
M_"v[60:63], a[184:187], a[128:131], v[60:63]\n" \
B_"v25, s[76:79], s84 offen offset:3072 lds\n" \
M_"v[60:63], a[248:251], a[192:195], v[60:63]\n" \
X_"a[234:235], v34 offset:4608\n" \
"s_bfe_u32 s5, s2, 0x20003\n" \
M_"v[64:67], a[188:191], a[128:131], v[64:67]\n" \
R_"a[128:131], v28 offset:0\n" \
"s_or_b32 s4, s4, s5\n" \
M_"v[64:67], a[252:255], a[192:195], v[64:67]\n" \
R_"a[192:195], v29 offset:0\n" \
"s_lshr_b32 s5, s2, 5\n" \
M_"v[80:83], a[188:191], a[132:135], v[80:83]\n" \
"s_lshr_b32 s19, s3, 1\n" \
"s_lshl_b32 s18, s5, 1\n" \
M_"v[80:83], a[252:255], a[196:199], v[80:83]\n" \
"s_lshl_b32 s6, s4, 20\n" \
"s_lshl_b32 s7, s4, 8\n" \
M_"v[76:79], a[184:187], a[132:135], v[76:79]\n" \
"s_and_b32 s13, s3, 1\n" \
"s_add_i32 s14, s18, s19\n" \
M_"v[76:79], a[248:251], a[196:199], v[76:79]\n" \
"s_add_u32 s6, s26, s6\n" \
"s_addc_u32 s9, s27, 0\n" \
M_"v[72:75], a[180:183], a[132:135], v[72:75]\n" \
"s_add_u32 s8, s6, 0x1000000\n" \
"s_addc_u32 s6, s9, 0\n" \
M_"v[72:75], a[244:247], a[196:199], v[72:75]\n" \
"s_and_b32 s9, s6, 0xffff\n" \
"s_lshl_b32 s6, s5, 16\n" \
M_"v[68:71], a[176:179], a[132:135], v[68:71]\n" \
R_"a[132:135], v28 offset:2048\n" \
"s_sub_i32 s10, 0x100000, s6\n" \
M_"v[68:71], a[240:243], a[196:199], v[68:71]\n" \
R_"a[196:199], v29 offset:2048\n" \
"s_waitcnt lgkmcnt(15)\n" \
M_"a[0:3], a[160:163], a[144:147], a[0:3]\n" \
"s_add_u32 m0, s30, 0x21000\n" \
"s_addk_i32 s84, 0x3000\n" \
M_"a[0:3], a[224:227], a[208:211], a[0:3]\n" \
B_"v24, s[76:79], s84 offen offset:0 lds\n" \
M_"a[4:7], a[164:167], a[144:147], a[4:7]\n" \
X_"a[176:177], v36 offset:0\n" \
"s_mov_b32 s6, 0x8000\n" \
M_"a[4:7], a[228:231], a[208:211], a[4:7]\n" \
X_"a[178:179], v36 offset:1024\n" \
"s_waitcnt lgkmcnt(15)\n" \
M_"a[12:15], a[172:175], a[144:147], a[12:15]\n" \
X_"a[240:241], v36 offset:8192\n" \
"s_waitcnt lgkmcnt(15)\n" \
M_"a[12:15], a[236:239], a[208:211], a[12:15]\n" \
"s_add_u32 s88, s42, 0x0\n" \
"buffer_store_dwordx4 v[196:199], v20, s[80:83], s88 offen offset:3072 sc1\n" \
"s_waitcnt lgkmcnt(11)\n" \
M_"a[8:11], a[144:147], a[168:171], a[8:11]\n" \
X_"a[242:243], v36 offset:9216\n" \
"s_waitcnt lgkmcnt(8)\n" \
M_"a[8:11], a[208:211], a[232:235], a[8:11]\n" \
X_"a[180:181], v37 offset:0\n" \
M_"a[24:27], a[148:151], a[168:171], a[24:27]\n" \
X_"a[182:183], v37 offset:1024\n" \
M_"a[24:27], a[212:215], a[232:235], a[24:27]\n" \
X_"a[244:245], v37 offset:8192\n" \
M_"a[28:31], a[172:175], a[148:151], a[28:31]\n" \
"s_cmp_lg_u32 s5, 0\n" \
"s_cselect_b32 s17, s11, s6\n" \
M_"a[28:31], a[236:239], a[212:215], a[28:31]\n" \
"s_addk_i32 s84, 0x2000\n" \
M_"a[20:23], a[164:167], a[148:151], a[20:23]\n" \
B_"v25, s[76:79], s84 offen offset:1024 lds\n" \
M_"a[20:23], a[228:231], a[212:215], a[20:23]\n" \
X_"a[246:247], v37 offset:9216\n" \
"s_lshl_b32 s6, s3, 1\n" \
M_"a[16:19], a[160:163], a[148:151], a[16:19]\n" \
X_"a[184:185], v38 offset:0\n" \
"s_lshl_b32 s5, s5, 3\n" \
M_"a[16:19], a[224:227], a[212:215], a[16:19]\n" \
X_"a[186:187], v38 offset:1024\n" \
"s_and_b32 s6, s6, 4\n" \
M_"a[32:35], a[160:163], a[152:155], a[32:35]\n" \
"s_addk_i32 s84, 0x2000\n" \
M_"a[32:35], a[224:227], a[216:219], a[32:35]\n" \
B_"v24, s[76:79], s84 offen offset:2048 lds\n" \
M_"a[36:39], a[164:167], a[152:155], a[36:39]\n" \
X_"a[248:249], v38 offset:8192\n" \
"s_or_b32 s12, s5, s6\n" \
M_"a[36:39], a[228:231], a[216:219], a[36:39]\n" \
X_"a[250:251], v38 offset:9216\n" \
"s_sub_i32 s5, s13, s5\n" \
M_"a[44:47], a[172:175], a[152:155], a[44:47]\n" \
X_"a[188:189], v39 offset:0\n" \
"s_or_b32 s5, s5, s6\n" \
M_"a[44:47], a[236:239], a[216:219], a[44:47]\n" \
X_"a[190:191], v39 offset:1024\n" \
"s_or_b32 s6, s12, s13\n" \
M_"a[40:43], a[152:155], a[168:171], a[40:43]\n" \
"s_add_u32 s89, s42, 0x2000\n" \
"buffer_store_dwordx4 v[200:203], v20, s[80:83], s89 offen offset:0 sc1\n" \
M_"a[40:43], a[216:219], a[232:235], a[40:43]\n" \
X_"a[252:253], v39 offset:8192\n" \
"s_lshl_b32 s4, s4, 2\n" \
M_"a[56:59], a[156:159], a[168:171], a[56:59]\n" \
X_"a[254:255], v39 offset:9216\n" \
"s_mulk_i32 s6, 0x92\n" \
M_"a[56:59], a[220:223], a[232:235], a[56:59]\n" \
"s_add_i32 s4, s4, 18\n" \
"s_mulk_i32 s5, 0x92\n" \
M_"a[60:63], a[172:175], a[156:159], a[60:63]\n" \
"s_add_i32 s6, s4, s6\n" \
"s_add_i32 s5, s5, s4\n" \
M_"a[60:63], a[236:239], a[220:223], a[60:63]\n" \
"s_addk_i32 s84, 0x2000\n" \
M_"a[52:55], a[164:167], a[156:159], a[52:55]\n" \
B_"v25, s[76:79], s84 offen offset:3072 lds\n" \
M_"a[52:55], a[228:231], a[220:223], a[52:55]\n" \
"s_lshl_b32 s4, s3, 11\n" \
"s_lshl_b32 s15, s13, 12\n" \
M_"a[48:51], a[160:163], a[156:159], a[48:51]\n" \
"s_lshl_b32 s16, s19, 14\n" \
"s_mul_i32 s19, s19, 0xe000\n" \
M_"a[48:51], a[224:227], a[220:223], a[48:51]\n" \
"s_mov_b32 s11, 0x20000\n" \
".fill (L_pb + 73440 - .) / 4, 4, 0xBF800000\n" \
"L_pp18:\n" \
"s_cbranch_execz L_pret\n" \
"s_or_b32 s12, s4, 0xc000\n" \
M_"a[64:67], a[164:167], a[140:143], a[64:67]\n" \
"s_lshl_b32 s4, s3, 8\n" \
"s_or_b32 s22, s15, s16\n" \
M_"a[64:67], a[228:231], a[204:207], a[64:67]\n" \
"s_or_b32 s23, s19, s15\n" \
"s_lshl_b32 s60, s6, 10\n" \
M_"a[80:83], a[172:175], a[140:143], a[80:83]\n" \
"s_lshl_b32 s61, s5, 10\n" \
"s_lshl_b32 s14, s14, 15\n" \
M_"a[80:83], a[236:239], a[204:207], a[80:83]\n" \
"s_or_b32 s20, s4, 0x26c00\n" \
"s_or_b32 s13, s22, 0x8000\n" \
M_"a[68:71], a[140:143], a[168:171], a[68:71]\n" \
"s_add_i32 s22, s22, 0x14000\n" \
"s_bitset1_b32 s23, 16\n" \
"s_waitcnt vmcnt(11) lgkmcnt(0)\n" \
M_"a[68:71], a[204:207], a[232:235], a[68:71]\n" \
"s_barrier\n" \
M_"v[116:119], a[176:179], a[144:147], v[116:119]\n" \
"s_add_u32 m0, s40, 0x0\n" \
"s_nop 0\n" \
"s_add_u32 s85, s39, 0x10000\n" \
M_"v[116:119], a[240:243], a[208:211], v[116:119]\n" \
B_"v20, s[80:83], s85 offen sc1 lds\n" \
M_"v[120:123], a[180:183], a[144:147], v[120:123]\n" \
X_"a[160:161], v32 offset:32768\n" \
"s_or_b32 s24, s7, 0x5be8020\n" \
M_"v[120:123], a[244:247], a[208:211], v[120:123]\n" \
X_"a[162:163], v32 offset:33280\n" \
"s_and_b32 s57, s27, 0xffff\n" \
M_"v[124:127], a[184:187], a[144:147], v[124:127]\n" \
"s_mov_b32 s58, 0x6000000\n" \
"s_mov_b32 s56, s26\n" \
M_"v[124:127], a[248:251], a[208:211], v[124:127]\n" \
"s_mov_b32 s59, s11\n" \
"s_add_i32 s60, s60, 0x3000000\n" \
M_"v[128:131], a[188:191], a[144:147], v[128:131]\n" \
R_"a[144:147], v28 offset:40960\n" \
"s_add_i32 s61, s61, 0x411c000\n" \
M_"v[128:131], a[252:255], a[208:211], v[128:131]\n" \
R_"a[208:211], v29 offset:40960\n" \
"s_sub_i32 s19, 14, s18\n" \
"s_nop 0\n" \
M_"v[144:147], a[188:191], a[148:151], v[144:147]\n" \
"s_add_u32 m0, s40, 0x1000\n" \
"s_add_u32 s85, s39, 0x11000\n" \
M_"v[144:147], a[252:255], a[212:215], v[144:147]\n" \
B_"v20, s[80:83], s85 offen sc1 lds\n" \
M_"v[140:143], a[184:187], a[148:151], v[140:143]\n" \
X_"a[224:225], v32 offset:36864\n" \
"s_mov_b32 s21, 0x71800000\n" \
M_"v[140:143], a[248:251], a[212:215], v[140:143]\n" \
X_"a[226:227], v32 offset:37376\n" \
"s_mov_b32 s4, L_gsw-L_gtr\n" \
M_"v[136:139], a[180:183], a[148:151], v[136:139]\n" \
"s_cmp_eq_u32 s19, 0\n" \
"s_cselect_b32 s4, L_gs7-L_gtr, s4\n" \
M_"v[136:139], a[244:247], a[212:215], v[136:139]\n" \
X_"a[164:165], v33 offset:32768\n" \
M_"v[132:135], a[176:179], a[148:151], v[132:135]\n" \
R_"a[148:151], v28 offset:43008\n" \
M_"v[132:135], a[240:243], a[212:215], v[132:135]\n" \
R_"a[212:215], v29 offset:43008\n" \
M_"v[148:151], a[176:179], a[152:155], v[148:151]\n" \
"s_add_u32 m0, s40, 0x3000\n" \
"s_movk_i32 s84, 0x400\n" \
M_"v[148:151], a[240:243], a[216:219], v[148:151]\n" \
B_"v21, s[76:79], s84 offen offset:0 lds\n" \
M_"v[152:155], a[180:183], a[152:155], v[152:155]\n" \
X_"a[166:167], v33 offset:33280\n" \
M_"v[152:155], a[244:247], a[216:219], v[152:155]\n" \
X_"a[228:229], v33 offset:36864\n" \
M_"v[156:159], a[184:187], a[152:155], v[156:159]\n" \
"s_cmp_eq_u32 s16, 0\n" \
"s_cselect_b32 s4, 0, s4\n" \
M_"v[156:159], a[248:251], a[216:219], v[156:159]\n" \
"s_getpc_b64 s[28:29]\n" \
"L_gp1_%=:\n" \
"s_add_u32 s28, s28, L_gtr-L_gp1_%=\n" \
"s_addc_u32 s29, s29, 0\n" \
M_"v[160:163], a[188:191], a[152:155], v[160:163]\n" \
R_"a[152:155], v28 offset:45056\n" \
M_"v[160:163], a[252:255], a[216:219], v[160:163]\n" \
R_"a[216:219], v29 offset:45056\n" \
M_"v[176:179], a[188:191], a[156:159], v[176:179]\n" \
"s_add_u32 m0, s31, 0x0\n" \
"s_nop 0\n" \
"s_mov_b32 s84, 0x400000\n" \
M_"v[176:179], a[252:255], a[220:223], v[176:179]\n" \
B_"v26, s[68:71], s84 offen offset:0 lds\n" \
M_"v[172:175], a[184:187], a[156:159], v[172:175]\n" \
X_"a[230:231], v33 offset:37376\n" \
M_"v[172:175], a[248:251], a[220:223], v[172:175]\n" \
X_"a[172:173], v35 offset:32768\n" \
M_"v[168:171], a[180:183], a[156:159], v[168:171]\n" \
"s_add_u32 s88, s42, 0x2000\n" \
"buffer_store_dwordx4 v[204:207], v20, s[80:83], s88 offen offset:1024 sc1\n" \
M_"v[168:171], a[244:247], a[220:223], v[168:171]\n" \
X_"a[174:175], v35 offset:33280\n" \
M_"v[164:167], a[176:179], a[156:159], v[164:167]\n" \
R_"a[156:159], v28 offset:47104\n" \
M_"v[164:167], a[240:243], a[220:223], v[164:167]\n" \
R_"a[220:223], v29 offset:47104\n" \
M_"v[84:87], a[176:179], a[136:139], v[84:87]\n" \
X_"a[236:237], v35 offset:36864\n" \
M_"v[84:87], a[240:243], a[200:203], v[84:87]\n" \
"s_add_u32 s28, s28, s4\n" \
"s_addc_u32 s29, s29, 0\n" \
M_"v[88:91], a[180:183], a[136:139], v[88:91]\n" \
"v_accvgpr_write_b32 a96, 0\n" \
"v_accvgpr_write_b32 a97, 0\n" \
M_"v[88:91], a[244:247], a[200:203], v[88:91]\n" \
"s_addk_i32 s84, 0x7c00\n" \
"s_nop 0\n" \
M_"v[92:95], a[184:187], a[136:139], v[92:95]\n" \
B_"v26, s[68:71], s84 offen offset:1024 lds\n" \
M_"v[92:95], a[248:251], a[200:203], v[92:95]\n" \
X_"a[238:239], v35 offset:37376\n" \
"v_accvgpr_write_b32 a98, 0\n" \
M_"v[96:99], a[188:191], a[136:139], v[96:99]\n" \
R_"a[136:139], v28 offset:36864\n" \
"v_accvgpr_write_b32 a99, 0\n" \
M_"v[96:99], a[252:255], a[200:203], v[96:99]\n" \
R_"a[200:203], v29 offset:36864\n" \
"v_accvgpr_write_b32 a100, 0\n" \
M_"v[112:115], a[188:191], a[140:143], v[112:115]\n" \
X_"a[168:169], v34 offset:32768\n" \
"v_accvgpr_write_b32 a101, 0\n" \
M_"v[112:115], a[252:255], a[204:207], v[112:115]\n" \
"v_accvgpr_write_b32 a102, 0\n" \
"v_accvgpr_write_b32 a103, 0\n" \
M_"v[108:111], a[184:187], a[140:143], v[108:111]\n" \
"s_addk_i32 s84, 0x7c00\n" \
"s_nop 0\n" \
M_"v[108:111], a[248:251], a[204:207], v[108:111]\n" \
B_"v27, s[68:71], s84 offen offset:2048 lds\n" \
M_"v[104:107], a[180:183], a[140:143], v[104:107]\n" \
X_"a[170:171], v34 offset:33280\n" \
"v_accvgpr_write_b32 a112, 0\n" \
M_"v[104:107], a[244:247], a[204:207], v[104:107]\n" \
X_"a[232:233], v34 offset:36864\n" \
"v_accvgpr_write_b32 a113, 0\n" \
M_"v[100:103], a[176:179], a[140:143], v[100:103]\n" \
R_"a[140:143], v28 offset:38912\n" \
"v_accvgpr_write_b32 a114, 0\n" \
M_"v[100:103], a[240:243], a[204:207], v[100:103]\n" \
R_"a[204:207], v29 offset:38912\n" \
"v_accvgpr_write_b32 a115, 0\n" \
M_"v[52:55], a[176:179], a[128:131], v[52:55]\n" \
X_"a[234:235], v34 offset:37376\n" \
"v_accvgpr_write_b32 a120, 0\n" \
M_"v[52:55], a[240:243], a[192:195], v[52:55]\n" \
"s_addk_i32 s84, 0x7c00\n" \
"s_nop 0\n" \
M_"v[56:59], a[180:183], a[128:131], v[56:59]\n" \
B_"v27, s[68:71], s84 offen offset:3072 lds\n" \
M_"v[56:59], a[244:247], a[192:195], v[56:59]\n" \
"v_accvgpr_write_b32 a121, 0\n" \
"v_accvgpr_write_b32 a122, 0\n" \
M_"v[60:63], a[184:187], a[128:131], v[60:63]\n" \
"v_accvgpr_write_b32 a123, 0\n" \
"v_mov_b32_e32 v180, 0x3f803f80\n" \
M_"v[60:63], a[248:251], a[192:195], v[60:63]\n" \
"s_mov_b32 s6, 0x33333333\n" \
"s_mov_b32 s7, 0xcccccccc\n" \
M_"v[64:67], a[188:191], a[128:131], v[64:67]\n" \
R_"a[128:131], v28 offset:32768\n" \
"v_cndmask_b32_e64 v181, 0, v180, s[6:7]\n" \
M_"v[64:67], a[252:255], a[192:195], v[64:67]\n" \
R_"a[192:195], v29 offset:32768\n" \
"v_accvgpr_write_b32 a104, v181\n" \
M_"v[80:83], a[188:191], a[132:135], v[80:83]\n" \
"v_accvgpr_write_b32 a105, v181\n" \
"v_accvgpr_write_b32 a106, v181\n" \
M_"v[80:83], a[252:255], a[196:199], v[80:83]\n" \
"v_accvgpr_write_b32 a107, v181\n" \
"s_mov_b32 s6, 0x11111111\n" \
M_"v[76:79], a[184:187], a[132:135], v[76:79]\n" \
"s_mov_b32 s7, 0x11111111\n" \
"v_cndmask_b32_e64 v181, 0, v180, s[6:7]\n" \
M_"v[76:79], a[248:251], a[196:199], v[76:79]\n" \
"v_accvgpr_write_b32 a108, v181\n" \
"v_accvgpr_write_b32 a109, v181\n" \
M_"v[72:75], a[180:183], a[132:135], v[72:75]\n" \
"v_accvgpr_write_b32 a110, v181\n" \
"v_accvgpr_write_b32 a111, v181\n" \
M_"v[72:75], a[244:247], a[196:199], v[72:75]\n" \
"s_mov_b32 s6, 0x44444444\n" \
"s_mov_b32 s7, 0x44444444\n" \
M_"v[68:71], a[176:179], a[132:135], v[68:71]\n" \
R_"a[132:135], v28 offset:34816\n" \
"v_cndmask_b32_e64 v181, 0, v180, s[6:7]\n" \
M_"v[68:71], a[240:243], a[196:199], v[68:71]\n" \
R_"a[196:199], v29 offset:34816\n" \
"s_waitcnt lgkmcnt(15)\n" \
"s_nop 0\n" \
M_"a[0:3], a[160:163], a[144:147], a[0:3]\n" \
"s_add_u32 s89, s42, 0x2000\n" \
"buffer_store_dwordx4 v[208:211], v20, s[80:83], s89 offen offset:2048 sc1\n" \
M_"a[0:3], a[224:227], a[208:211], a[0:3]\n" \
X_"a[176:177], v36 offset:32768\n" \
"v_accvgpr_write_b32 a116, v181\n" \
M_"a[4:7], a[164:167], a[144:147], a[4:7]\n" \
X_"a[178:179], v36 offset:33792\n" \
"v_accvgpr_write_b32 a117, v181\n" \
M_"a[4:7], a[228:231], a[208:211], a[4:7]\n" \
X_"a[240:241], v36 offset:40960\n" \
"v_accvgpr_write_b32 a118, v181\n" \
M_"a[12:15], a[172:175], a[144:147], a[12:15]\n" \
X_"a[242:243], v36 offset:41984\n" \
"v_accvgpr_write_b32 a119, v181\n" \
"s_waitcnt lgkmcnt(15)\n" \
M_"a[12:15], a[236:239], a[208:211], a[12:15]\n" \
"v_bfe_i32 v181, v20, 4, 1\n" \
"v_and_b32_e32 v181, 0xaaaaaaaa, v181\n" \
"s_waitcnt lgkmcnt(12)\n" \
M_"a[8:11], a[144:147], a[168:171], a[8:11]\n" \
X_"a[180:181], v37 offset:32768\n" \
"v_or_b32_e32 v181, 0x44444444, v181\n" \
"s_waitcnt lgkmcnt(9)\n" \
M_"a[8:11], a[208:211], a[232:235], a[8:11]\n" \
"v_accvgpr_write_b32 a126, v181\n" \
"v_bfe_u32 v180, v20, 5, 3\n" \
M_"a[24:27], a[148:151], a[168:171], a[24:27]\n" \
X_"a[182:183], v37 offset:33792\n" \
"v_bfe_u32 v181, v20, 8, 2\n" \
M_"a[24:27], a[212:215], a[232:235], a[24:27]\n" \
X_"a[244:245], v37 offset:40960\n" \
"v_lshlrev_b32_e32 v181, 1, v181\n" \
M_"a[28:31], a[172:175], a[148:151], a[28:31]\n" \
"v_sub_u32_e32 v180, v180, v181\n" \
"v_and_b32_e32 v181, 16, v20\n" \
M_"a[28:31], a[236:239], a[212:215], a[28:31]\n" \
X_"a[246:247], v37 offset:41984\n" \
"v_mov_b32_e32 v182, 0x3f80\n" \
M_"a[20:23], a[164:167], a[148:151], a[20:23]\n" \
"v_lshlrev_b32_e32 v181, v181, v182\n" \
"v_cmp_eq_u32_e32 vcc, 0, v180\n" \
M_"a[20:23], a[228:231], a[212:215], a[20:23]\n" \
X_"a[184:185], v38 offset:32768\n" \
"v_cmp_eq_u32_e64 s[6:7], 1, v180\n" \
M_"a[16:19], a[160:163], a[148:151], a[16:19]\n" \
X_"a[186:187], v38 offset:33792\n" \
"v_cndmask_b32_e64 v182, 0, v181, vcc\n" \
M_"a[16:19], a[224:227], a[212:215], a[16:19]\n" \
"v_cndmask_b32_e64 v183, 0, v181, s[6:7]\n" \
"v_accvgpr_write_b32 a124, v182\n" \
M_"a[32:35], a[160:163], a[152:155], a[32:35]\n" \
"s_add_u32 s88, s42, 0x2000\n" \
"buffer_store_dwordx4 v[212:215], v20, s[80:83], s88 offen offset:3072 sc1\n" \
M_"a[32:35], a[224:227], a[216:219], a[32:35]\n" \
X_"a[248:249], v38 offset:40960\n" \
"v_accvgpr_write_b32 a125, v183\n" \
M_"a[36:39], a[164:167], a[152:155], a[36:39]\n" \
"v_lshlrev_b32_e32 v45, 3, v20\n" \
"v_add_u32_e32 v45, s30, v45\n" \
M_"a[36:39], a[228:231], a[216:219], a[36:39]\n" \
"s_getpc_b64 s[86:87]\n" \
"L_ct1_%=:\n" \
"s_add_u32 s86, s86, L_ap0-L_ct1_%=\n" \
"s_addc_u32 s87, s87, 0\n" \
"s_add_u32 m0, s40, 0x2000\n" \
M_"a[44:47], a[172:175], a[152:155], a[44:47]\n" \
"global_load_lds_dword v45, s[86:87]\n" \
M_"a[44:47], a[236:239], a[216:219], a[44:47]\n" \
X_"a[250:251], v38 offset:41984\n" \
M_"a[40:43], a[152:155], a[168:171], a[40:43]\n" \
X_"a[188:189], v39 offset:32768\n" \
M_"a[40:43], a[216:219], a[232:235], a[40:43]\n" \
X_"a[190:191], v39 offset:33792\n" \
M_"a[56:59], a[156:159], a[168:171], a[56:59]\n" \
X_"a[252:253], v39 offset:40960\n" \
M_"a[56:59], a[220:223], a[232:235], a[56:59]\n" \
X_"a[254:255], v39 offset:41984\n" \
M_"a[60:63], a[172:175], a[156:159], a[60:63]\n" \
M_"a[60:63], a[236:239], a[220:223], a[60:63]\n" \
M_"a[52:55], a[164:167], a[156:159], a[52:55]\n" \
M_"a[52:55], a[228:231], a[220:223], a[52:55]\n" \
M_"a[48:51], a[160:163], a[156:159], a[48:51]\n" \
M_"a[48:51], a[224:227], a[220:223], a[48:51]\n" \
M_"a[64:67], a[164:167], a[140:143], a[64:67]\n" \
M_"a[64:67], a[228:231], a[204:207], a[64:67]\n" \
M_"a[80:83], a[172:175], a[140:143], a[80:83]\n" \
M_"a[80:83], a[236:239], a[204:207], a[80:83]\n" \
M_"a[68:71], a[140:143], a[168:171], a[68:71]\n" \
"s_waitcnt vmcnt(3) lgkmcnt(0)\n" \
M_"a[68:71], a[204:207], a[232:235], a[68:71]\n" \
"s_barrier\n" \
"v_xor_b32_e32 v28, 0x20000, v28\n" \
"v_xor_b32_e32 v29, 0x20000, v29\n" \
M_"v[116:119], a[176:179], a[144:147], v[116:119]\n" \
X_"a[160:161], v32 offset:0\n" \
"v_accvgpr_read_b32 v184, a0\n" \
M_"v[116:119], a[240:243], a[208:211], v[116:119]\n" \
"v_accvgpr_read_b32 v185, a1\n" \
"v_accvgpr_read_b32 v186, a2\n" \
M_"v[120:123], a[180:183], a[144:147], v[120:123]\n" \
"v_accvgpr_read_b32 v187, a3\n" \
"v_accvgpr_read_b32 v188, a4\n" \
M_"v[120:123], a[244:247], a[208:211], v[120:123]\n" \
X_"a[162:163], v32 offset:512\n" \
"v_accvgpr_read_b32 v189, a5\n" \
M_"v[124:127], a[184:187], a[144:147], v[124:127]\n" \
"v_accvgpr_read_b32 v190, a6\n" \
"v_accvgpr_read_b32 v191, a7\n" \
M_"v[124:127], a[248:251], a[208:211], v[124:127]\n" \
"v_accvgpr_read_b32 v196, a12\n" \
"v_accvgpr_read_b32 v197, a13\n" \
M_"v[128:131], a[188:191], a[144:147], v[128:131]\n" \
R_"a[144:147], v28 offset:8192\n" \
"v_accvgpr_read_b32 v198, a14\n" \
M_"v[128:131], a[252:255], a[208:211], v[128:131]\n" \
R_"a[208:211], v29 offset:8192\n" \
"v_accvgpr_read_b32 v199, a15\n" \
M_"v[144:147], a[188:191], a[148:151], v[144:147]\n" \
X_"a[224:225], v32 offset:4096\n" \
"v_accvgpr_read_b32 v192, a8\n" \
M_"v[144:147], a[252:255], a[212:215], v[144:147]\n" \
X_"a[226:227], v32 offset:4608\n" \
"v_accvgpr_read_b32 v193, a9\n" \
M_"v[140:143], a[184:187], a[148:151], v[140:143]\n" \
"v_accvgpr_read_b32 v194, a10\n" \
"v_accvgpr_read_b32 v195, a11\n" \
M_"v[140:143], a[248:251], a[212:215], v[140:143]\n" \
"v_accvgpr_read_b32 v208, a24\n" \
"v_accvgpr_read_b32 v209, a25\n" \
M_"v[136:139], a[180:183], a[148:151], v[136:139]\n" \
"v_accvgpr_read_b32 v210, a26\n" \
"v_accvgpr_read_b32 v211, a27\n" \
M_"v[136:139], a[244:247], a[212:215], v[136:139]\n" \
X_"a[164:165], v33 offset:0\n" \
"v_accvgpr_read_b32 v212, a28\n" \
M_"v[132:135], a[176:179], a[148:151], v[132:135]\n" \
R_"a[148:151], v28 offset:10240\n" \
"v_accvgpr_read_b32 v213, a29\n" \
M_"v[132:135], a[240:243], a[212:215], v[132:135]\n" \
R_"a[212:215], v29 offset:10240\n" \
"v_accvgpr_read_b32 v214, a30\n" \
M_"v[148:151], a[176:179], a[152:155], v[148:151]\n" \
X_"a[166:167], v33 offset:512\n" \
"v_accvgpr_read_b32 v215, a31\n" \
M_"v[148:151], a[240:243], a[216:219], v[148:151]\n" \
"v_accvgpr_read_b32 v204, a20\n" \
"v_accvgpr_read_b32 v205, a21\n" \
M_"v[152:155], a[180:183], a[152:155], v[152:155]\n" \
"v_accvgpr_read_b32 v206, a22\n" \
"v_accvgpr_read_b32 v207, a23\n" \
M_"v[152:155], a[244:247], a[216:219], v[152:155]\n" \
X_"a[228:229], v33 offset:4096\n" \
"v_accvgpr_read_b32 v200, a16\n" \
M_"v[156:159], a[184:187], a[152:155], v[156:159]\n" \
"v_accvgpr_read_b32 v201, a17\n" \
"v_accvgpr_read_b32 v202, a18\n" \
M_"v[156:159], a[248:251], a[216:219], v[156:159]\n" \
"v_accvgpr_read_b32 v203, a19\n" \
"v_accvgpr_read_b32 v216, a32\n" \
M_"v[160:163], a[188:191], a[152:155], v[160:163]\n" \
R_"a[152:155], v28 offset:12288\n" \
"v_accvgpr_read_b32 v217, a33\n" \
M_"v[160:163], a[252:255], a[216:219], v[160:163]\n" \
R_"a[216:219], v29 offset:12288\n" \
"v_accvgpr_read_b32 v218, a34\n" \
M_"v[176:179], a[188:191], a[156:159], v[176:179]\n" \
X_"a[230:231], v33 offset:4608\n" \
"v_accvgpr_read_b32 v219, a35\n" \
M_"v[176:179], a[252:255], a[220:223], v[176:179]\n" \
"v_accvgpr_read_b32 v220, a36\n" \
"v_accvgpr_read_b32 v221, a37\n" \
M_"v[172:175], a[184:187], a[156:159], v[172:175]\n" \
X_"a[172:173], v35 offset:0\n" \
"v_accvgpr_read_b32 v222, a38\n" \
M_"v[172:175], a[248:251], a[220:223], v[172:175]\n" \
"v_accvgpr_read_b32 v223, a39\n" \
"v_accvgpr_read_b32 v228, a44\n" \
M_"v[168:171], a[180:183], a[156:159], v[168:171]\n" \
"v_accvgpr_read_b32 v229, a45\n" \
"v_accvgpr_read_b32 v230, a46\n" \
M_"v[168:171], a[244:247], a[220:223], v[168:171]\n" \
X_"a[174:175], v35 offset:512\n" \
"v_accvgpr_read_b32 v231, a47\n" \
M_"v[164:167], a[176:179], a[156:159], v[164:167]\n" \
R_"a[156:159], v28 offset:14336\n" \
"v_accvgpr_read_b32 v224, a40\n" \
M_"v[164:167], a[240:243], a[220:223], v[164:167]\n" \
R_"a[220:223], v29 offset:14336\n" \
"v_accvgpr_read_b32 v225, a41\n" \
M_"v[84:87], a[176:179], a[136:139], v[84:87]\n" \
X_"a[236:237], v35 offset:4096\n" \
"v_accvgpr_read_b32 v226, a42\n" \
M_"v[84:87], a[240:243], a[200:203], v[84:87]\n" \
"v_accvgpr_read_b32 v227, a43\n" \
"v_accvgpr_read_b32 v240, a56\n" \
M_"v[88:91], a[180:183], a[136:139], v[88:91]\n" \
"v_accvgpr_read_b32 v241, a57\n" \
"v_accvgpr_read_b32 v242, a58\n" \
M_"v[88:91], a[244:247], a[200:203], v[88:91]\n" \
X_"a[238:239], v35 offset:4608\n" \
"v_accvgpr_read_b32 v243, a59\n" \
M_"v[92:95], a[184:187], a[136:139], v[92:95]\n" \
"v_accvgpr_read_b32 v244, a60\n" \
"v_accvgpr_read_b32 v245, a61\n" \
M_"v[92:95], a[248:251], a[200:203], v[92:95]\n" \
"v_accvgpr_read_b32 v246, a62\n" \
"v_accvgpr_read_b32 v247, a63\n" \
M_"v[96:99], a[188:191], a[136:139], v[96:99]\n" \
R_"a[136:139], v28 offset:4096\n" \
"v_accvgpr_read_b32 v236, a52\n" \
M_"v[96:99], a[252:255], a[200:203], v[96:99]\n" \
R_"a[200:203], v29 offset:4096\n" \
"v_accvgpr_read_b32 v237, a53\n" \
M_"v[112:115], a[188:191], a[140:143], v[112:115]\n" \
X_"a[168:169], v34 offset:0\n" \
"v_accvgpr_read_b32 v238, a54\n" \
M_"v[112:115], a[252:255], a[204:207], v[112:115]\n" \
"v_accvgpr_read_b32 v239, a55\n" \
"v_accvgpr_read_b32 v232, a48\n" \
M_"v[108:111], a[184:187], a[140:143], v[108:111]\n" \
X_"a[170:171], v34 offset:512\n" \
"v_accvgpr_read_b32 v233, a49\n" \
M_"v[108:111], a[248:251], a[204:207], v[108:111]\n" \
"v_accvgpr_read_b32 v234, a50\n" \
"v_accvgpr_read_b32 v235, a51\n" \
M_"v[104:107], a[180:183], a[140:143], v[104:107]\n" \
"v_accvgpr_read_b32 v180, a64\n" \
"v_accvgpr_read_b32 v181, a65\n" \
M_"v[104:107], a[244:247], a[204:207], v[104:107]\n" \
X_"a[232:233], v34 offset:4096\n" \
"v_accvgpr_read_b32 v182, a66\n" \
M_"v[100:103], a[176:179], a[140:143], v[100:103]\n" \
R_"a[140:143], v28 offset:6144\n" \
"v_accvgpr_read_b32 v183, a67\n" \
M_"v[100:103], a[240:243], a[204:207], v[100:103]\n" \
R_"a[204:207], v29 offset:6144\n" \
"v_accvgpr_read_b32 v48, a80\n" \
M_"v[52:55], a[176:179], a[128:131], v[52:55]\n" \
X_"a[234:235], v34 offset:4608\n" \
"v_accvgpr_read_b32 v49, a81\n" \
M_"v[52:55], a[240:243], a[192:195], v[52:55]\n" \
"v_accvgpr_read_b32 v50, a82\n" \
"v_accvgpr_read_b32 v51, a83\n" \
M_"v[56:59], a[180:183], a[128:131], v[56:59]\n" \
"v_accvgpr_read_b32 v252, a68\n" \
"v_accvgpr_read_b32 v253, a69\n" \
M_"v[56:59], a[244:247], a[192:195], v[56:59]\n" \
"v_accvgpr_read_b32 v254, a70\n" \
"v_accvgpr_read_b32 v255, a71\n" \
M_"v[60:63], a[184:187], a[128:131], v[60:63]\n" \
"v_lshrrev_b32_e64 v24, 9, v20\n" \
"v_and_b32_e32 v26, 0x1ff, v20\n" \
M_"v[60:63], a[248:251], a[192:195], v[60:63]\n" \
"v_lshl_add_u32 v24, v24, 3, v26\n" \
"v_lshrrev_b32_e64 v25, 8, v20\n" \
M_"v[64:67], a[188:191], a[128:131], v[64:67]\n" \
R_"a[128:131], v28 offset:0\n" \
"v_mul_u32_u24_e32 v25, 0x24700, v25\n" \
M_"v[64:67], a[252:255], a[192:195], v[64:67]\n" \
R_"a[192:195], v29 offset:0\n" \
"v_add_u32_e64 v25, v25, v20\n" \
M_"v[80:83], a[188:191], a[132:135], v[80:83]\n" \
"v_lshrrev_b32_e32 v26, 2, v20\n" \
"v_add_u32_e32 v26, s45, v26\n" \
M_"v[80:83], a[252:255], a[196:199], v[80:83]\n" \
"v_add_u32_e32 v27, s46, v20\n" \
"v_add_u32_e32 v21, s43, v27\n" \
M_"v[76:79], a[184:187], a[132:135], v[76:79]\n" \
M_"v[76:79], a[248:251], a[196:199], v[76:79]\n" \
M_"v[72:75], a[180:183], a[132:135], v[72:75]\n" \
M_"v[72:75], a[244:247], a[196:199], v[72:75]\n" \
M_"v[68:71], a[176:179], a[132:135], v[68:71]\n" \
R_"a[132:135], v28 offset:2048\n" \
M_"v[68:71], a[240:243], a[196:199], v[68:71]\n" \
R_"a[196:199], v29 offset:2048\n" \
"s_waitcnt lgkmcnt(15)\n" \
"s_nop 0\n" \
M_"v[184:187], a[160:163], a[144:147], v[184:187]\n" \
"v_bfe_u32 v44, v20, 2, 5\n" \
X_"a[176:177], v36 offset:0\n" \
M_"v[184:187], a[224:227], a[208:211], v[184:187]\n" \
"buffer_load_dword v44, v44, s[80:83], s38 offen offset:0 sc1\n" \
M_"v[188:191], a[164:167], a[144:147], v[188:191]\n" \
X_"a[178:179], v36 offset:1024\n" \
M_"v[188:191], a[228:231], a[208:211], v[188:191]\n" \
X_"a[240:241], v36 offset:8192\n" \
M_"v[196:199], a[172:175], a[144:147], v[196:199]\n" \
X_"a[242:243], v36 offset:9216\n" \
"s_waitcnt lgkmcnt(15)\n" \
M_"v[196:199], a[236:239], a[208:211], v[196:199]\n" \
"s_waitcnt lgkmcnt(12)\n" \
M_"v[192:195], a[144:147], a[168:171], v[192:195]\n" \
X_"a[180:181], v37 offset:0\n" \
"s_waitcnt lgkmcnt(9)\n" \
"s_nop 0\n" \
M_"v[192:195], a[208:211], a[232:235], v[192:195]\n" \
M_"v[208:211], a[148:151], a[168:171], v[208:211]\n" \
X_"a[182:183], v37 offset:1024\n" \
M_"v[208:211], a[212:215], a[232:235], v[208:211]\n" \
X_"a[244:245], v37 offset:8192\n" \
M_"v[212:215], a[172:175], a[148:151], v[212:215]\n" \
M_"v[212:215], a[236:239], a[212:215], v[212:215]\n" \
X_"a[246:247], v37 offset:9216\n" \
C_"v188, v188, v189\n" \
M_"v[204:207], a[164:167], a[148:151], v[204:207]\n" \
C_"v189, v190, v191\n" \
C_"v190, v198, v199\n" \
M_"v[204:207], a[228:231], a[212:215], v[204:207]\n" \
X_"a[184:185], v38 offset:0\n" \
"v_mov_b32_e32 v191, 0x3f80\n" \
M_"v[200:203], a[160:163], a[148:151], v[200:203]\n" \
X_"a[186:187], v38 offset:1024\n" \
C_"v184, v184, v185\n" \
M_"v[200:203], a[224:227], a[212:215], v[200:203]\n" \
C_"v185, v186, v187\n" \
C_"v186, v196, v197\n" \
M_"v[216:219], a[160:163], a[152:155], v[216:219]\n" \
X_"a[248:249], v38 offset:8192\n" \
"v_mov_b32_e64 v187, 0\n" \
M_"v[216:219], a[224:227], a[216:219], v[216:219]\n" \
X_"a[250:251], v38 offset:9216\n" \
M_"v[220:223], a[164:167], a[152:155], v[220:223]\n" \
M_"v[220:223], a[228:231], a[216:219], v[220:223]\n" \
X_"a[188:189], v39 offset:0\n" \
C_"v204, v204, v205\n" \
M_"v[228:231], a[172:175], a[152:155], v[228:231]\n" \
C_"v205, v206, v207\n" \
C_"v206, v214, v215\n" \
M_"v[228:231], a[236:239], a[216:219], v[228:231]\n" \
X_"a[190:191], v39 offset:1024\n" \
"v_mov_b32_e32 v207, 0x3f80\n" \
M_"v[224:227], a[152:155], a[168:171], v[224:227]\n" \
X_"a[252:253], v39 offset:8192\n" \
C_"v200, v200, v201\n" \
M_"v[224:227], a[216:219], a[232:235], v[224:227]\n" \
C_"v201, v202, v203\n" \
C_"v202, v212, v213\n" \
M_"v[240:243], a[156:159], a[168:171], v[240:243]\n" \
X_"a[254:255], v39 offset:9216\n" \
"v_mov_b32_e64 v203, 0\n" \
M_"v[240:243], a[220:223], a[232:235], v[240:243]\n" \
M_"v[244:247], a[172:175], a[156:159], v[244:247]\n" \
M_"v[244:247], a[236:239], a[220:223], v[244:247]\n" \
C_"v220, v220, v221\n" \
".fill (L_pb + 77536 - .) / 4, 4, 0xBF800000\n" \
"L_pp19:\n" \
"s_cbranch_execz L_pret\n" \
C_"v221, v222, v223\n" \
M_"v[236:239], a[164:167], a[156:159], v[236:239]\n" \
C_"v222, v230, v231\n" \
"v_mov_b32_e32 v223, 0x3f80\n" \
M_"v[236:239], a[228:231], a[220:223], v[236:239]\n" \
C_"v216, v216, v217\n" \
C_"v217, v218, v219\n" \
M_"v[232:235], a[160:163], a[156:159], v[232:235]\n" \
C_"v218, v228, v229\n" \
"v_mov_b32_e32 v219, 0\n" \
M_"v[232:235], a[224:227], a[220:223], v[232:235]\n" \
M_"v[180:183], a[164:167], a[140:143], v[180:183]\n" \
M_"v[180:183], a[228:231], a[204:207], v[180:183]\n" \
M_"v[48:51], a[172:175], a[140:143], v[48:51]\n" \
M_"v[48:51], a[236:239], a[204:207], v[48:51]\n" \
C_"v236, v236, v237\n" \
C_"v237, v238, v239\n" \
M_"v[252:255], a[140:143], a[168:171], v[252:255]\n" \
C_"v238, v246, v247\n" \
"v_mov_b32_e32 v239, 0x3f80\n" \
M_"v[252:255], a[204:207], a[232:235], v[252:255]\n" \
C_"v232, v232, v233\n" \
C_"v233, v234, v235\n" \
"s_waitcnt vmcnt(0) lgkmcnt(0)\n" \
"s_barrier\n" \
"v_mov_b32_e32 v45, s62\n" \
"s_mov_b64 exec, 1\n" \
"buffer_store_dword v45, off, s[80:83], s37 offset:32 sc1\n" \
"s_mov_b64 exec, -1\n" \
"v_cmp_ne_u32_e32 vcc, s62, v44\n" \
"s_nop 4\n" \
"s_cbranch_vccnz L_poll161_%=\n" \
"L_pollr161_%=:\n" \
M_"v[116:119], a[176:179], a[144:147], v[116:119]\n" \
"s_mov_b32 s73, s50\n" \
"s_sub_u32 m0, s31, 0x10000\n" \
M_"v[116:119], a[240:243], a[208:211], v[116:119]\n" \
B_"v20, s[80:83], s73 offen offset:0 sc1 lds\n" \
M_"v[120:123], a[180:183], a[144:147], v[120:123]\n" \
"s_add_u32 s74, s50, 0x4000\n" \
"s_add_u32 s75, s50, 0x8000\n" \
M_"v[120:123], a[244:247], a[208:211], v[120:123]\n" \
"s_mov_b32 s70, 0x3e0293ee\n" \
"s_mov_b32 s76, s52\n" \
M_"v[124:127], a[184:187], a[144:147], v[124:127]\n" \
"v_mul_f32_e32 v116, s70, v116\n" \
"v_mul_f32_e32 v117, s70, v117\n" \
M_"v[124:127], a[248:251], a[208:211], v[124:127]\n" \
"v_mul_f32_e32 v118, s70, v118\n" \
"v_mul_f32_e32 v119, s70, v119\n" \
M_"v[128:131], a[188:191], a[144:147], v[128:131]\n" \
C_"v116, v116, v117\n" \
"v_mul_f32_e64 v120, s70, v120\n" \
B_"v20, s[80:83], s73 offen offset:1024 sc1 lds\n" \
M_"v[128:131], a[252:255], a[208:211], v[128:131]\n" \
"v_mul_f32_e32 v121, s70, v121\n" \
"v_mul_f32_e32 v124, s70, v124\n" \
M_"v[144:147], a[188:191], a[148:151], v[144:147]\n" \
"v_mul_f32_e32 v125, s70, v125\n" \
"v_mul_f32_e32 v126, s70, v126\n" \
M_"v[144:147], a[252:255], a[212:215], v[144:147]\n" \
"v_mul_f32_e64 v127, s70, v127\n" \
C_"v124, v124, v125\n" \
M_"v[140:143], a[184:187], a[148:151], v[140:143]\n" \
"v_mul_f32_e32 v122, s70, v122\n" \
"v_mul_f32_e32 v123, s70, v123\n" \
M_"v[140:143], a[248:251], a[212:215], v[140:143]\n" \
C_"v117, v118, v119\n" \
"v_mul_f32_e64 v128, s70, v128\n" \
B_"v20, s[80:83], s73 offen offset:2048 sc1 lds\n" \
M_"v[136:139], a[180:183], a[148:151], v[136:139]\n" \
"v_mul_f32_e32 v129, s70, v129\n" \
"v_mul_f32_e32 v130, s70, v130\n" \
M_"v[136:139], a[244:247], a[212:215], v[136:139]\n" \
"v_mul_f32_e64 v131, s70, v131\n" \
C_"v125, v126, v127\n" \
M_"v[132:135], a[176:179], a[148:151], v[132:135]\n" \
C_"v118, v120, v121\n" \
C_"v119, v122, v123\n" \
M_"v[132:135], a[240:243], a[212:215], v[132:135]\n" \
C_"v126, v128, v129\n" \
C_"v127, v130, v131\n" \
M_"v[148:151], a[176:179], a[152:155], v[148:151]\n" \
"ds_write_b128 v21, v[116:119] offset:16384\n" \
B_"v20, s[80:83], s73 offen offset:3072 sc1 lds\n" \
M_"v[148:151], a[240:243], a[216:219], v[148:151]\n" \
"s_sub_u32 m0, s31, s51\n" \
"v_mul_f32_e32 v140, s70, v140\n" \
M_"v[152:155], a[180:183], a[152:155], v[152:155]\n" \
"v_mul_f32_e32 v132, s70, v132\n" \
"v_mul_f32_e32 v133, s70, v133\n" \
M_"v[152:155], a[244:247], a[216:219], v[152:155]\n" \
"ds_write_b128 v21, v[124:127] offset:17408\n" \
M_"v[156:159], a[184:187], a[152:155], v[156:159]\n" \
"v_mul_f32_e32 v141, s70, v141\n" \
"v_mul_f32_e32 v134, s70, v134\n" \
M_"v[156:159], a[248:251], a[216:219], v[156:159]\n" \
"v_mul_f32_e64 v135, s70, v135\n" \
C_"v132, v132, v133\n" \
B_"v20, s[80:83], s74 offen offset:0 sc1 lds\n" \
M_"v[160:163], a[188:191], a[152:155], v[160:163]\n" \
"v_mul_f32_e32 v142, s70, v142\n" \
"v_mul_f32_e32 v143, s70, v143\n" \
M_"v[160:163], a[252:255], a[216:219], v[160:163]\n" \
C_"v140, v140, v141\n" \
"v_mul_f32_e64 v136, s70, v136\n" \
M_"v[176:179], a[188:191], a[156:159], v[176:179]\n" \
"v_mul_f32_e32 v137, s70, v137\n" \
"v_mul_f32_e32 v138, s70, v138\n" \
M_"v[176:179], a[252:255], a[220:223], v[176:179]\n" \
"v_mul_f32_e64 v139, s70, v139\n" \
C_"v133, v134, v135\n" \
M_"v[172:175], a[184:187], a[156:159], v[172:175]\n" \
"v_mul_f32_e32 v144, s70, v144\n" \
"v_mul_f32_e32 v145, s70, v145\n" \
B_"v20, s[80:83], s74 offen offset:1024 sc1 lds\n" \
M_"v[172:175], a[248:251], a[220:223], v[172:175]\n" \
"v_mul_f32_e32 v146, s70, v146\n" \
"v_mul_f32_e32 v147, s70, v147\n" \
M_"v[168:171], a[180:183], a[156:159], v[168:171]\n" \
C_"v141, v142, v143\n" \
C_"v134, v136, v137\n" \
M_"v[168:171], a[244:247], a[220:223], v[168:171]\n" \
C_"v135, v138, v139\n" \
C_"v142, v144, v145\n" \
M_"v[164:167], a[176:179], a[156:159], v[164:167]\n" \
C_"v143, v146, v147\n" \
C_"v180, v180, v181\n" \
M_"v[164:167], a[240:243], a[220:223], v[164:167]\n" \
"ds_write_b128 v21, v[132:135] offset:20480\n" \
B_"v20, s[80:83], s74 offen offset:2048 sc1 lds\n" \
M_"v[84:87], a[176:179], a[136:139], v[84:87]\n" \
C_"v181, v182, v183\n" \
C_"v234, v244, v245\n" \
M_"v[84:87], a[240:243], a[200:203], v[84:87]\n" \
"v_mov_b32_e64 v235, 0\n" \
C_"v182, v50, v51\n" \
M_"v[88:91], a[180:183], a[136:139], v[88:91]\n" \
"ds_write_b128 v21, v[140:143] offset:21504\n" \
M_"v[88:91], a[244:247], a[200:203], v[88:91]\n" \
"v_mov_b32_e32 v183, 0x3f80\n" \
C_"v118, v192, v193\n" \
M_"v[92:95], a[184:187], a[136:139], v[92:95]\n" \
C_"v119, v194, v195\n" \
C_"v120, v208, v209\n" \
B_"v20, s[80:83], s74 offen offset:3072 sc1 lds\n" \
"s_sub_u32 m0, s31, 0x4000\n" \
M_"v[92:95], a[248:251], a[200:203], v[92:95]\n" \
C_"v121, v210, v211\n" \
C_"v122, v224, v225\n" \
M_"v[96:99], a[188:191], a[136:139], v[96:99]\n" \
C_"v123, v226, v227\n" \
C_"v124, v240, v241\n" \
M_"v[96:99], a[252:255], a[200:203], v[96:99]\n" \
C_"v125, v242, v243\n" \
"s_mov_b32 s64, 0xaaaaaaaa\n" \
M_"v[112:115], a[188:191], a[140:143], v[112:115]\n" \
M_"v[192:195], v[188:191], v[184:187], 0\n" \
M_"v[32:35], v[180:183], v[184:187], 0\n" \
B_"v20, s[80:83], s75 offen offset:0 sc1 lds\n" \
M_"v[112:115], a[252:255], a[204:207], v[112:115]\n" \
M_"v[208:211], v[204:207], v[200:203], 0\n" \
M_"v[36:39], v[188:191], v[200:203], 0\n" \
M_"v[224:227], v[220:223], v[216:219], 0\n" \
M_"v[248:251], v[204:207], v[216:219], 0\n" \
M_"v[240:243], v[236:239], v[232:235], 0\n" \
B_"v20, s[80:83], s75 offen offset:1024 sc1 lds\n" \
M_"v[108:111], a[184:187], a[140:143], v[108:111]\n" \
M_"v[128:131], v[220:223], v[232:235], 0\n" \
"s_mov_b32 s65, 0xaaaaaaaa\n" \
"s_mov_b32 s66, 0xfff0ffff\n" \
M_"v[108:111], a[248:251], a[204:207], v[108:111]\n" \
"s_mov_b32 s67, 0xf000ff00\n" \
"s_mov_b32 s68, 0xffc0fffc\n" \
M_"v[104:107], a[180:183], a[140:143], v[104:107]\n" \
"s_mov_b32 s69, 0xc000fc00\n" \
"v_cndmask_b32_e64 v192, v192, v193, s[64:65]\n" \
M_"v[104:107], a[244:247], a[204:207], v[104:107]\n" \
"v_cndmask_b32_e64 v32, v32, v33, s[64:65]\n" \
"v_cndmask_b32_e64 v194, v194, v195, s[64:65]\n" \
B_"v20, s[80:83], s75 offen offset:2048 sc1 lds\n" \
M_"v[100:103], a[176:179], a[140:143], v[100:103]\n" \
"v_cndmask_b32_e64 v34, v34, v35, s[64:65]\n" \
"v_cndmask_b32_e64 v208, v208, v209, s[64:65]\n" \
M_"v[100:103], a[240:243], a[204:207], v[100:103]\n" \
"v_cndmask_b32_e64 v36, v36, v37, s[64:65]\n" \
"v_cndmask_b32_e64 v210, v210, v211, s[64:65]\n" \
M_"v[52:55], a[176:179], a[128:131], v[52:55]\n" \
"v_cndmask_b32_e64 v38, v38, v39, s[64:65]\n" \
"v_cndmask_b32_e64 v224, v224, v225, s[64:65]\n" \
M_"v[52:55], a[240:243], a[192:195], v[52:55]\n" \
"v_cndmask_b32_e64 v248, v248, v249, s[64:65]\n" \
"v_cndmask_b32_e64 v226, v226, v227, s[64:65]\n" \
M_"v[56:59], a[180:183], a[128:131], v[56:59]\n" \
"v_cndmask_b32_e64 v250, v250, v251, s[64:65]\n" \
"v_cndmask_b32_e64 v240, v240, v241, s[64:65]\n" \
B_"v20, s[80:83], s75 offen offset:3072 sc1 lds\n" \
"s_mov_b32 m0, s53\n" \
"s_nop 0\n" \
M_"v[56:59], a[244:247], a[192:195], v[56:59]\n" \
"v_cndmask_b32_e64 v128, v128, v129, s[64:65]\n" \
"v_cndmask_b32_e64 v242, v242, v243, s[64:65]\n" \
M_"v[60:63], a[184:187], a[128:131], v[60:63]\n" \
"v_cndmask_b32_e64 v130, v130, v131, s[64:65]\n" \
"v_cndmask_b32_e64 v192, v32, v192, s[66:67]\n" \
M_"v[60:63], a[248:251], a[192:195], v[60:63]\n" \
"v_cndmask_b32_e64 v194, v34, v194, s[68:69]\n" \
"v_cndmask_b32_e64 v208, v36, v208, s[66:67]\n" \
M_"v[64:67], a[188:191], a[128:131], v[64:67]\n" \
"v_cndmask_b32_e64 v210, v38, v210, s[68:69]\n" \
"v_cndmask_b32_e64 v224, v248, v224, s[66:67]\n" \
"v_cndmask_b32_e64 v226, v250, v226, s[68:69]\n" \
"v_cndmask_b32_e64 v240, v128, v240, s[66:67]\n" \
"v_cndmask_b32_e64 v242, v130, v242, s[68:69]\n" \
B_"v20, s[80:83], s76 offen offset:0 sc1 lds\n" \
M_"v[64:67], a[252:255], a[192:195], v[64:67]\n" \
"v_max_f32_e32 v193, v192, v194\n" \
"v_max_f32_e32 v209, v208, v210\n" \
"v_max_f32_e32 v225, v224, v226\n" \
"v_max_f32_e32 v241, v240, v242\n" \
"v_mul_f32_e32 v148, s70, v148\n" \
"v_permlane32_swap_b32_e32 v193, v225\n" \
"v_permlane32_swap_b32_e32 v209, v241\n" \
"v_mul_f32_e32 v149, s70, v149\n" \
"v_mul_f32_e32 v156, s70, v156\n" \
"v_mul_f32_e32 v157, s70, v157\n" \
"v_permlane32_swap_b32_e32 v196, v228\n" \
"v_permlane32_swap_b32_e32 v212, v244\n" \
"v_max_f32_e32 v193, v193, v225\n" \
"v_max_f32_e32 v209, v209, v241\n" \
"v_mul_f32_e32 v150, s70, v150\n" \
"v_mul_f32_e32 v151, s70, v151\n" \
C_"v148, v148, v149\n" \
"v_mul_f32_e64 v158, s70, v158\n" \
B_"v20, s[80:83], s76 offen offset:1024 sc1 lds\n" \
"v_mul_f32_e64 v159, s70, v159\n" \
C_"v156, v156, v157\n" \
"v_permlane16_swap_b32_e32 v228, v244\n" \
"v_permlane16_swap_b32_e32 v193, v209\n" \
M_"v[80:83], a[188:191], a[132:135], v[80:83]\n" \
"s_mov_b32 s72, 0x3eae1a93\n" \
"v_mul_f32_e32 v152, s70, v152\n" \
"v_mul_f32_e32 v153, s70, v153\n" \
"v_mul_f32_e32 v154, s70, v154\n" \
"v_mul_f32_e32 v155, s70, v155\n" \
C_"v149, v150, v151\n" \
"v_mul_f32_e32 v160, s70, v160\n" \
"v_mul_f32_e32 v161, s70, v161\n" \
"v_mul_f32_e32 v162, s70, v162\n" \
"v_mul_f32_e32 v163, s70, v163\n" \
C_"v157, v158, v159\n" \
"v_mul_f32_e32 v45, 0xbfb8aa3b, v228\n" \
"v_mfma_f32_16x16x16_bf16 v[48:51], v[182:183], v[46:47], 0\n" \
"v_mfma_f32_16x16x16_bf16 v[196:199], v[190:191], v[46:47], 0\n" \
"v_mfma_f32_16x16x16_bf16 v[212:215], v[206:207], v[46:47], 0\n" \
"v_max_f32_e64 v193, v193, v209\n" \
C_"v150, v152, v153\n" \
C_"v151, v154, v155\n" \
C_"v158, v160, v161\n" \
C_"v159, v162, v163\n" \
"v_mfma_f32_16x16x16_bf16 v[228:231], v[222:223], v[46:47], 0\n" \
"v_mul_f32_e32 v193, s72, v193\n" \
"v_mov_b32_e32 v209, v193\n" \
"ds_write_b128 v21, v[148:151] offset:24576\n" \
"v_mfma_f32_16x16x16_bf16 v[244:247], v[238:239], v[46:47], 0\n" \
"v_permlane16_swap_b32_e32 v193, v209\n" \
"s_mov_b32 s71, 0x3e0293ee\n" \
M_"v[80:83], a[252:255], a[196:199], v[80:83]\n" \
"v_mov_b32_e32 v225, v193\n" \
"v_mov_b32_e32 v241, v209\n" \
"v_mul_f32_e32 v164, s70, v164\n" \
"v_mul_f32_e64 v165, s70, v165\n" \
"ds_write_b128 v21, v[156:159] offset:25600\n" \
"v_mul_f32_e32 v172, s70, v172\n" \
"v_mul_f32_e32 v173, s70, v173\n" \
"v_pk_mul_f32 v[84:85], v[84:85], s[70:71]\n" \
"v_pk_mul_f32 v[92:93], v[92:93], s[70:71]\n" \
"v_pk_mul_f32 v[100:101], v[100:101], s[70:71]\n" \
"v_pk_mul_f32 v[108:109], v[108:109], s[70:71]\n" \
"v_pk_mul_f32 v[52:53], v[52:53], s[70:71]\n" \
"v_pk_mul_f32 v[60:61], v[60:61], s[70:71]\n" \
"v_permlane32_swap_b32_e32 v193, v225\n" \
"v_permlane32_swap_b32_e32 v209, v241\n" \
"v_mul_f32_e32 v166, s70, v166\n" \
"v_mul_f32_e32 v167, s70, v167\n" \
C_"v164, v164, v165\n" \
"v_mul_f32_e32 v174, s70, v174\n" \
"v_mul_f32_e32 v175, s70, v175\n" \
C_"v172, v172, v173\n" \
"v_pk_mul_f32 v[86:87], v[86:87], s[70:71]\n" \
C_"v84, v84, v85\n" \
"v_pk_mul_f32 v[94:95], v[94:95], s[70:71]\n" \
C_"v92, v92, v93\n" \
"v_pk_mul_f32 v[102:103], v[102:103], s[70:71]\n" \
C_"v100, v100, v101\n" \
"v_pk_mul_f32 v[110:111], v[110:111], s[70:71]\n" \
C_"v108, v108, v109\n" \
"v_pk_mul_f32 v[54:55], v[54:55], s[70:71]\n" \
C_"v52, v52, v53\n" \
"v_pk_mul_f32 v[62:63], v[62:63], s[70:71]\n" \
C_"v60, v60, v61\n" \
"v_fma_f32 v192, v192, s72, -v193\n" \
"v_fma_f32 v194, v194, s72, -v193\n" \
"v_fma_f32 v208, v208, s72, -v209\n" \
"v_fma_f32 v210, v210, s72, -v209\n" \
"v_mul_f32_e32 v168, s70, v168\n" \
"v_mul_f32_e32 v169, s70, v169\n" \
"v_mul_f32_e32 v170, s70, v170\n" \
"v_mul_f32_e32 v171, s70, v171\n" \
C_"v165, v166, v167\n" \
"v_mul_f32_e32 v176, s70, v176\n" \
"v_mul_f32_e32 v177, s70, v177\n" \
"v_mul_f32_e32 v178, s70, v178\n" \
"v_mul_f32_e32 v179, s70, v179\n" \
C_"v173, v174, v175\n" \
"v_pk_mul_f32 v[88:89], v[88:89], s[70:71]\n" \
"v_pk_mul_f32 v[90:91], v[90:91], s[70:71]\n" \
C_"v85, v86, v87\n" \
"v_pk_mul_f32 v[96:97], v[96:97], s[70:71]\n" \
"v_pk_mul_f32 v[98:99], v[98:99], s[70:71]\n" \
C_"v93, v94, v95\n" \
"v_pk_mul_f32 v[104:105], v[104:105], s[70:71]\n" \
"v_pk_mul_f32 v[106:107], v[106:107], s[70:71]\n" \
C_"v101, v102, v103\n" \
"v_pk_mul_f32 v[112:113], v[112:113], s[70:71]\n" \
"v_pk_mul_f32 v[114:115], v[114:115], s[70:71]\n" \
C_"v109, v110, v111\n" \
"v_pk_mul_f32 v[56:57], v[56:57], s[70:71]\n" \
"v_pk_mul_f32 v[58:59], v[58:59], s[70:71]\n" \
C_"v53, v54, v55\n" \
"v_pk_mul_f32 v[64:65], v[64:65], s[70:71]\n" \
"v_pk_mul_f32 v[66:67], v[66:67], s[70:71]\n" \
C_"v61, v62, v63\n" \
"v_pk_mul_f32 v[80:81], v[80:81], s[70:71]\n" \
"v_pk_mul_f32 v[82:83], v[82:83], s[70:71]\n" \
E_"v192, v192\n" \
E_"v194, v194\n" \
E_"v208, v208\n" \
E_"v210, v210\n" \
"v_fma_f32 v224, v224, s72, -v225\n" \
"v_fma_f32 v226, v226, s72, -v225\n" \
M_"v[76:79], a[184:187], a[132:135], v[76:79]\n" \
C_"v166, v168, v169\n" \
C_"v167, v170, v171\n" \
C_"v174, v176, v177\n" \
C_"v175, v178, v179\n" \
C_"v86, v88, v89\n" \
C_"v87, v90, v91\n" \
C_"v94, v96, v97\n" \
C_"v95, v98, v99\n" \
C_"v102, v104, v105\n" \
C_"v103, v106, v107\n" \
C_"v110, v112, v113\n" \
C_"v111, v114, v115\n" \
C_"v54, v56, v57\n" \
C_"v55, v58, v59\n" \
C_"v62, v64, v65\n" \
C_"v63, v66, v67\n" \
E_"v224, v224\n" \
E_"v226, v226\n" \
"v_fma_f32 v240, v240, s72, -v241\n" \
"v_fma_f32 v242, v242, s72, -v241\n" \
"ds_write_b128 v21, v[164:167] offset:28672\n" \
C_"v192, v192, v194\n" \
C_"v208, v208, v210\n" \
E_"v240, v240\n" \
E_"v242, v242\n" \
C_"v132, v48, v49\n" \
C_"v133, v50, v51\n" \
C_"v134, v196, v197\n" \
C_"v135, v198, v199\n" \
"ds_write_b128 v21, v[172:175] offset:29696\n" \
C_"v136, v212, v213\n" \
C_"v137, v214, v215\n" \
"v_perm_b32 v184, v192, v192, v40\n" \
"v_perm_b32 v185, v192, v192, v41\n" \
"v_perm_b32 v186, v192, v192, v42\n" \
"v_perm_b32 v187, v192, v192, v43\n" \
"v_perm_b32 v200, v208, v208, v40\n" \
"v_perm_b32 v201, v208, v208, v41\n" \
"v_perm_b32 v202, v208, v208, v42\n" \
"v_perm_b32 v203, v208, v208, v43\n" \
"ds_write_b128 v27, v[84:87] offset:8192\n" \
C_"v224, v224, v226\n" \
C_"v138, v228, v229\n" \
C_"v139, v230, v231\n" \
M_"v[32:35], v[132:135], v[184:187], 0\n" \
M_"v[36:39], v[134:137], v[200:203], 0\n" \
"v_perm_b32 v216, v224, v224, v40\n" \
"v_perm_b32 v217, v224, v224, v41\n" \
"ds_write_b128 v27, v[92:95] offset:9216\n" \
"v_perm_b32 v218, v224, v224, v42\n" \
"v_perm_b32 v219, v224, v224, v43\n" \
C_"v240, v240, v242\n" \
M_"v[76:79], a[248:251], a[196:199], v[76:79]\n" \
C_"v140, v244, v245\n" \
C_"v141, v246, v247\n" \
M_"v[248:251], v[136:139], v[216:219], 0\n" \
"v_perm_b32 v232, v240, v240, v40\n" \
"v_perm_b32 v233, v240, v240, v41\n" \
"ds_write_b128 v27, v[100:103] offset:12288\n" \
"v_perm_b32 v234, v240, v240, v42\n" \
"v_perm_b32 v235, v240, v240, v43\n" \
"v_pk_mul_f32 v[76:77], v[76:77], s[70:71]\n" \
"v_pk_mul_f32 v[78:79], v[78:79], s[70:71]\n" \
C_"v76, v76, v77\n" \
C_"v77, v78, v79\n" \
C_"v78, v80, v81\n" \
C_"v79, v82, v83\n" \
M_"v[128:131], v[138:141], v[232:235], 0\n" \
"ds_write_b128 v27, v[108:111] offset:13312\n" \
M_"v[72:75], a[180:183], a[132:135], v[72:75]\n" \
C_"v116, v252, v253\n" \
C_"v117, v254, v255\n" \
M_"v[72:75], a[244:247], a[196:199], v[72:75]\n" \
"v_exp_f32_e64 v45, v45\n" \
M_"v[192:195], v[116:119], v[184:187], 0\n" \
"ds_write_b128 v27, v[52:55] offset:0\n" \
M_"v[208:211], v[118:121], v[200:203], 0\n" \
M_"v[224:227], v[120:123], v[216:219], 0\n" \
M_"v[240:243], v[122:125], v[232:235], 0\n" \
"ds_write_b128 v27, v[60:63] offset:1024\n" \
"v_mov_b32_dpp v34, v38 quad_perm:[0,1,2,3] row_mask:0x2 bank_mask:0xf\n" \
"v_pk_mul_f32 v[72:73], v[72:73], s[70:71]\n" \
"v_pk_mul_f32 v[74:75], v[74:75], s[70:71]\n" \
"v_mov_b32_dpp v34, v250 quad_perm:[0,1,2,3] row_mask:0x4 bank_mask:0xf\n" \
"v_add_f32_e64 v45, 1.0, v45\n" \
M_"v[68:71], a[176:179], a[132:135], v[68:71]\n" \
"v_mov_b32_dpp v34, v130 quad_perm:[0,1,2,3] row_mask:0x8 bank_mask:0xf\n" \
"v_mov_b32_dpp v32, v36 quad_perm:[0,1,2,3] row_mask:0x2 bank_mask:0xf\n" \
"v_rcp_f32_e64 v45, v45\n" \
"ds_write_b128 v27, v[76:79] offset:5120\n" \
"v_mov_b32_dpp v33, v37 quad_perm:[0,1,2,3] row_mask:0x2 bank_mask:0xf\n" \
"v_rcp_f32_e64 v34, v34\n" \
"v_mov_b32_dpp v32, v248 quad_perm:[0,1,2,3] row_mask:0x4 bank_mask:0xf\n" \
"v_mov_b32_dpp v33, v249 quad_perm:[0,1,2,3] row_mask:0x4 bank_mask:0xf\n" \
"v_mul_f32_e64 v34, v45, v34\n" \
"v_mov_b32_dpp v32, v128 quad_perm:[0,1,2,3] row_mask:0x8 bank_mask:0xf\n" \
"v_mov_b32_dpp v33, v129 quad_perm:[0,1,2,3] row_mask:0x8 bank_mask:0xf\n" \
"v_mul_f32_e32 v32, v34, v32\n" \
"v_mul_f32_e32 v33, v34, v33\n" \
"v_mov_b32_e64 v38, v34\n" \
M_"v[68:71], a[240:243], a[196:199], v[68:71]\n" \
"v_sub_f32_e32 v44, 1.0, v45\n" \
"v_permlane16_swap_b32_e32 v34, v38\n" \
"v_mov_b32_e32 v250, v34\n" \
"v_mov_b32_e32 v130, v38\n" \
C_"v32, v32, v33\n" \
"v_permlane32_swap_b32_e32 v34, v250\n" \
"v_permlane32_swap_b32_e32 v38, v130\n" \
"v_mul_f32_e64 v192, v34, v192\n" \
"v_pk_mul_f32 v[68:69], v[68:69], s[70:71]\n" \
"v_pk_mul_f32 v[70:71], v[70:71], s[70:71]\n" \
C_"v68, v68, v69\n" \
C_"v69, v70, v71\n" \
C_"v70, v72, v73\n" \
C_"v71, v74, v75\n" \
"ds_write_b128 v27, v[68:71] offset:4096\n" \
"v_mul_f32_e32 v193, v34, v193\n" \
"v_mul_f32_e32 v208, v38, v208\n" \
"v_mul_f32_e32 v209, v38, v209\n" \
"v_mul_f32_e32 v224, v250, v224\n" \
"v_mul_f32_e32 v225, v250, v225\n" \
"v_mul_f32_e32 v240, v130, v240\n" \
"v_mul_f32_e32 v241, v130, v241\n" \
"v_mul_f32_e32 v194, v34, v194\n" \
"v_mul_f32_e64 v195, v34, v195\n" \
C_"v192, v192, v193\n" \
"v_mul_f32_e32 v210, v38, v210\n" \
"v_mul_f32_e32 v211, v38, v211\n" \
C_"v208, v208, v209\n" \
"v_mul_f32_e32 v226, v250, v226\n" \
"v_mul_f32_e32 v227, v250, v227\n" \
C_"v224, v224, v225\n" \
"v_mul_f32_e32 v242, v130, v242\n" \
"v_mul_f32_e32 v243, v130, v243\n" \
C_"v240, v240, v241\n" \
C_"v193, v194, v195\n" \
"s_add_u32 s84, s48, 0x0\n" \
"s_nop 0\n" \
C_"v209, v210, v211\n" \
"s_add_u32 s85, s48, 0x24800\n" \
C_"v225, v226, v227\n" \
"s_add_u32 s86, s48, 0x49000\n" \
C_"v241, v242, v243\n" \
"s_add_u32 s87, s48, 0x6d800\n" \
"ds_write_b32 v26, v44\n" \
"buffer_store_dword v32, v25, s[80:83], s49 offen sc1\n" \
"buffer_store_dwordx2 v[192:193], v24, s[80:83], s84 offen sc1\n" \
"buffer_store_dwordx2 v[208:209], v24, s[80:83], s85 offen sc1\n" \
"buffer_store_dwordx2 v[224:225], v24, s[80:83], s86 offen sc1\n" \
"buffer_store_dwordx2 v[240:241], v24, s[80:83], s87 offen sc1\n" \
"s_branch L_end_%=\n" \
"L_poll61_%=:\n" \
"s_mov_b32 s84, 0\n" \
"L_pl61_%=:\n" \
"s_sleep 1\n" \
"v_bfe_u32 v44, v20, 2, 5\n" \
"buffer_load_dword v44, v44, s[80:83], s38 offen offset:128 sc1\n" \
"s_waitcnt vmcnt(0)\n" \
"v_cmp_ne_u32_e32 vcc, s62, v44\n" \
"s_add_u32 s84, s84, 1\n" \
"s_nop 3\n" \
"s_cbranch_vccz L_pollr61_%=\n" \
"s_cmp_gt_u32 s84, 0x400000\n" \
"s_cbranch_scc0 L_pl61_%=\n" \
"s_branch L_pollr61_%=\n" \
"L_poll161_%=:\n" \
"s_mov_b32 s84, 0\n" \
"L_pl161_%=:\n" \
"s_sleep 1\n" \
"v_bfe_u32 v44, v20, 2, 5\n" \
"buffer_load_dword v44, v44, s[80:83], s38 offen offset:0 sc1\n" \
"s_waitcnt vmcnt(0)\n" \
"v_cmp_ne_u32_e32 vcc, s62, v44\n" \
"s_add_u32 s84, s84, 1\n" \
"s_nop 3\n" \
"s_cbranch_vccz L_pollr161_%=\n" \
"s_cmp_gt_u32 s84, 0x400000\n" \
"s_cbranch_scc0 L_pl161_%=\n" \
"s_branch L_pollr161_%=\n" \
"L_end_%=:\n"
#define PROJ_PAGES 20
#define PROJ_VCLOB "v21","v24","v25","v28","v29","v32","v33","v34","v35","v36","v37","v38","v39","v40","v41","v42","v43","v44","v45","v46","v47","v48","v49","v50","v51","v52","v53","v54","v55","v56","v57","v58","v59","v60","v61","v62","v63","v64","v65","v66","v67","v68","v69","v70","v71","v72","v73","v74","v75","v76","v77","v78","v79","v80","v81","v82","v83","v84","v85","v86","v87","v88","v89","v90","v91","v92","v93","v94","v95","v96","v97","v98","v99","v100","v101","v102","v103","v104","v105","v106","v107","v108","v109","v110","v111","v112","v113","v114","v115","v116","v117","v118","v119","v120","v121","v122","v123","v124","v125","v126","v127","v128","v129","v130","v131","v132","v133","v134","v135","v136","v137","v138","v139","v140","v141","v142","v143","v144","v145","v146","v147","v148","v149","v150","v151","v152","v153","v154","v155","v156","v157","v158","v159","v160","v161","v162","v163","v164","v165","v166","v167","v168","v169","v170","v171","v172","v173","v174","v175","v176","v177","v178","v179","v180","v181","v182","v183","v184","v185","v186","v187","v188","v189","v190","v192","v193","v194","v195","v196","v197","v198","v199","v200","v201","v202","v203","v204","v205","v206","v207","v208","v209","v210","v211","v212","v213","v214","v215","v216","v217","v218","v219","v220","v221","v222","v223","v224","v225","v226","v227","v228","v229","v230","v231","v232","v233","v234","v235","v236","v237","v238","v239","v240","v241","v242","v243","v244","v245","v246","v247","v248","v249","v250","v251","v252","v253","v254","v255"
#define PROJ_SCLOB "s4","s5","s6","s7","s32","s33","s34","s35","s36","s37","s38","s39","s40","s41","s42","s43","s44","s45","s46","s47","s48","s49","s50","s51","s52","s53","s54","s55","s63","s64","s65","s66","s67","s68","s69","s70","s71","s72","s73","s74","s75","s76","s77","s78","s79","s80","s81","s82","s83","s84","s85","s86","s87","s88","s89","s90","s91","s92","s93","s94","s95","s96","s97","s98","s99","s100","s101"

typedef const u16* const __attribute__((address_space(4)))* kaptr;
__device__ __forceinline__ void proj_phase(kaptr& ka, char*& ws, const unsigned wid, u32x4 (&vp)[5], unsigned& vl, u32x4 (&sa)[5], u32x2 s24, u32x2& s28, unsigned tdum, u32x4& srdA, u32x2& sd, unsigned& tag
    ) {
    const unsigned b = blockIdx.x, h = (b & 7) * 4 + ((b >> 3) & 3), mt = b >> 5;
    const unsigned rb = (wid < 2u ? 15u - mt : mt) * 128u;
    const unsigned lane = threadIdx.x & 63u, wc = wid & 1u;
    u32x2 vAc, vB;
#pragma unroll
    for (int par = 0; par < 2; ++par) {
        const unsigned lr = lane >> 3, cc = (lane & 7u) ^ (4u * par + (lr >> 1)), r0 = rb + wc * 64u + lr;
        vAc[par] = r0 * (DC * 2) + cc * 16u;
        const unsigned br = lane >> 4, p16 = lane & 15u, sc = (((p16 >> 1) ^ (br | (par << 2))) << 1) | (p16 & 1u);
        vB[par] = (16u * wid + br) * (NBIG * 2) + h * (HD * 2) + sc * 16u;
    }
    vl = lane * 16u;
    u32x2 s30 = {wid * 8192u, 65536u + wid * 4096u};
    asm volatile(PROJ_ASM
                 : "={s62}"(tag), "={s[56:59]}"(srdA), "={s[60:61]}"(sd), "+{s[8:11]}"(sa[0]), "+{s[12:15]}"(sa[1]), "+{s[16:19]}"(sa[2]), "+{s[20:23]}"(sa[3]), "+{s[24:25]}"(s24), "+{s[28:29]}"(s28), "+{s[30:31]}"(s30),
                   "={v[0:3]}"(vp[0]), "={v[4:7]}"(vp[1]), "={v[8:11]}"(vp[2]), "={v[12:15]}"(vp[3]), "={v[16:19]}"(vp[4]), "+{v[22:23]}"(vAc), "+{v[26:27]}"(vB), "+{v191}"(tdum), "+{s[26:27]}"(ws), "+{s[0:1]}"(ka)
                 : "{s2}"(b), "{s3}"(wid), "{v20}"(vl)
                 : "memory", "m0", "vcc", "scc", PROJ_VCLOB, PROJ_SCLOB, ACLOB_LO, ACLOB_HI);
    sa[4] = (u32x4){s24[0], 0u, 0u, 0u};
}

namespace att {
#define DI __device__ __forceinline__
typedef __bf16 bfx8 __attribute__((ext_vector_type(8)));
DI unsigned rfl(unsigned x) { return __builtin_amdgcn_readfirstlane(x); }
DI f32x4 mfma(u32x4 a, u32x4 b, f32x4 c) { return __builtin_amdgcn_mfma_f32_16x16x32_bf16(__builtin_bit_cast(bfx8, a), __builtin_bit_cast(bfx8, b), c, 0, 0, 0); }
typedef __bf16 bfx2 __attribute__((ext_vector_type(2)));
typedef float f32x2 __attribute__((ext_vector_type(2)));
DI unsigned cvtpk2(float lo, float hi) { f32x2 v = {lo, hi}; bfx2 b = __builtin_convertvector(v, bfx2); return __builtin_bit_cast(unsigned, b); }
DI void att_exact(const u16* __restrict__ x, const u16* __restrict__ wq, char* __restrict__ ws, unsigned h, unsigned r0, float g1) {
    const unsigned lane = lane_id(), g = lane >> 4, c = lane & 15u;
    f32x4 o[8];
#pragma unroll
    for (int n = 0; n < 8; ++n) o[n] = (f32x4){0.f, 0.f, 0.f, 0.f};
#pragma unroll 1
    for (unsigned kk = 0; kk < (unsigned)DM; kk += 32u) {
        const unsigned k = kk + 8u * g;
        const u32x4 xf = *reinterpret_cast<const u32x4*>(x + (r0 + c) * DM + k);
#pragma unroll
        for (int n = 0; n < 8; ++n) {
            const u16* w = wq + k * NBIG + h * HD + n * 16 + c; u32x4 wf;
#pragma unroll
            for (int e = 0; e < 4; ++e) wf[e] = (unsigned)w[2 * e * NBIG] | ((unsigned)w[(2 * e + 1) * NBIG] << 16);
            o[n] = mfma(wf, xf, o[n]);
        }
    }
    u32x4 q[4];
#pragma unroll
    for (int p = 0; p < 4; ++p) {
        const f32x4 a = o[2 * p] * 0.12751743082f, b = o[2 * p + 1] * 0.12751743082f;
        q[p] = (u32x4){cvtpk2(a[0], a[1]), cvtpk2(a[2], a[3]), cvtpk2(b[0], b[1]), cvtpk2(b[2], b[3])};
    }
#pragma unroll
    for (int n = 0; n < 8; ++n) o[n] = (f32x4){0.f, 0.f, 0.f, 0.f};
    const __amdgpu_buffer_rsrc_t srd = __builtin_amdgcn_make_buffer_rsrc((void*)(ws + WS_K + (size_t)h * 1048576), (short)0, 1048576, 0x00020000);
    float m = -3.0e38f, l = 0.f;
#pragma unroll 1
    for (unsigned k0 = 0; k0 <= r0 + 15u; k0 += 32u) {
        const unsigned tb = (k0 >> 6) * 32768u + (k0 & 32u) * 256u;
        f32x4 s[2]; float mx = m;
#pragma unroll
        for (int kt = 0; kt < 2; ++kt) {
            s[kt] = (f32x4){0.f, 0.f, 0.f, 0.f};
#pragma unroll
            for (int p = 0; p < 4; ++p) s[kt] = mfma(__builtin_amdgcn_raw_buffer_load_b128(srd, lane * 16u + (kt * 4 + p) * 1024u, tb, 16), q[p], s[kt]);
#pragma unroll
            for (int e = 0; e < 4; ++e) { if (k0 + 16u * kt + 4u * g + e > r0 + c) s[kt][e] = -3.0e38f; mx = fmaxf(mx, s[kt][e]); }
        }
        mx = fmaxf(mx, __shfl_xor(mx, 16)); mx = fmaxf(mx, __shfl_xor(mx, 32));
        const float al = __builtin_amdgcn_exp2f(m - mx); m = mx;
        u32x4 pf;
#pragma unroll
        for (int e = 0; e < 4; ++e) pf[e] = cvtpk2(__builtin_amdgcn_exp2f(s[e >> 1][2 * (e & 1)] - m), __builtin_amdgcn_exp2f(s[e >> 1][2 * (e & 1) + 1] - m));
        l *= al;
#pragma unroll
        for (int e = 0; e < 4; ++e) l += __uint_as_float(pf[e] << 16) + __uint_as_float(pf[e] & 0xffff0000u);
#pragma unroll
        for (int n = 0; n < 8; ++n) o[n] = mfma(__builtin_amdgcn_raw_buffer_load_b128(srd, lane * 16u + 16384u + n * 1024u, tb, 16), pf, o[n] * al);
    }
    l += __shfl_xor(l, 16); l += __shfl_xor(l, 32);
    const float f = g1 / l;
    const __amdgpu_buffer_rsrc_t srdA = __builtin_amdgcn_make_buffer_rsrc((void*)(ws + WS_AF), (short)0, 128 * KS_TOT * 1024, 0x00020000);
#pragma unroll
    for (int p = 0; p < 4; ++p) {
        const f32x4 a = o[2 * p] * f, b = o[2 * p + 1] * f;
        __builtin_amdgcn_raw_buffer_store_b128((u32x4){cvtpk2(a[0], a[1]), cvtpk2(a[2], a[3]), cvtpk2(b[0], b[1]), cvtpk2(b[2], b[3])}, srdA, lane * 16u + p * 1024u, ((r0 >> 4) * KS_TOT + 18u + 4u * h) * 1024u, 16);
    }
}
}

#define ATT_ASM \
"s_branch L_ago_%=\n" \
".p2align 12\n" \
"L_ab:\n" \
".fill (L_ab + 0 - .) / 4, 4, 0xBF800000\n" \
"L_ap0:\n" \
"s_setpc_b64 s[88:89]\n" \
"L_ago_%=:\n" \
"s_mov_b32 s101, 0\n" \
"s_waitcnt vmcnt(15) lgkmcnt(0)\n" \
"s_barrier\n" \
"v_mfma_f32_32x32x16_bf16 v[128:143], a[120:123], a[120:123], 0\n" \
"v_and_b32_e32 v63, 0x300, v20\n" \
R_"v[96:99], v63 offset:0\n" \
"v_bfe_u32 v61, v20, 2, 5\n" \
"s_mov_b32 s96, s24\n" \
"v_add_u32_e32 v58, s22, v20\n" \
R_"a[0:3], v58 offset:0\n" \
"v_mfma_f32_32x32x16_bf16 v[144:159], a[120:123], a[120:123], 0\n" \
"buffer_load_dword v57, v61, s[56:59], s96 offen sc1\n" \
R_"a[16:19], v58 offset:8192\n" \
"v_add_u32_e32 v59, s23, v20\n" \
R_"a[32:35], v59 offset:0\n" \
R_"a[48:51], v59 offset:8192\n" \
"v_mfma_f32_32x32x16_bf16 v[160:175], a[120:123], a[120:123], 0\n" \
R_"v[100:103], v63 offset:1024\n" \
R_"a[4:7], v58 offset:1024\n" \
R_"a[20:23], v58 offset:9216\n" \
"s_sub_u32 s67, s12, 0xc000\n" \
"s_mov_b32 s65, s12\n" \
"v_add_u32_e32 v55, s67, v20\n" \
"v_mfma_f32_32x32x16_bf16 v[176:191], a[120:123], a[120:123], 0\n" \
"v_add_u32_e32 v52, 0xc000, v55\n" \
"s_xor_b32 m0, s65, 0x10000\n" \
R_"a[36:39], v59 offset:1024\n" \
B_"v52, s[8:11], 0 offen offset:0 sc1 lds\n" \
R_"a[52:55], v59 offset:9216\n" \
"v_mfma_f32_32x32x16_bf16 v[192:207], a[120:123], a[120:123], 0\n" \
R_"v[104:107], v63 offset:2048\n" \
R_"a[8:11], v58 offset:2048\n" \
R_"a[24:27], v58 offset:10240\n" \
B_"v52, s[8:11], 0 offen offset:1024 sc1 lds\n" \
R_"a[40:43], v59 offset:2048\n" \
"v_mfma_f32_32x32x16_bf16 v[208:223], a[120:123], a[120:123], 0\n" \
R_"a[56:59], v59 offset:10240\n" \
R_"v[108:111], v63 offset:3072\n" \
R_"a[12:15], v58 offset:3072\n" \
R_"a[28:31], v58 offset:11264\n" \
"s_waitcnt lgkmcnt(15)\n" \
"v_xor_b32_e32 v96, 0x80008000, v96\n" \
"v_mfma_f32_32x32x16_bf16 v[224:239], a[120:123], a[120:123], 0\n" \
"v_xor_b32_e32 v97, 0x80008000, v97\n" \
"v_xor_b32_e32 v98, 0x80008000, v98\n" \
"v_xor_b32_e32 v99, 0x80008000, v99\n" \
R_"a[44:47], v59 offset:3072\n" \
R_"a[60:63], v59 offset:11264\n" \
R_"a[160:163], v20 offset:0\n" \
"v_mfma_f32_32x32x16_bf16 v[240:255], a[120:123], a[120:123], 0\n" \
R_"a[164:167], v20 offset:1024\n" \
"s_waitcnt lgkmcnt(15)\n" \
"v_xor_b32_e32 v100, 0x80008000, v100\n" \
"v_xor_b32_e32 v101, 0x80008000, v101\n" \
"v_xor_b32_e32 v102, 0x80008000, v102\n" \
"v_xor_b32_e32 v103, 0x80008000, v103\n" \
M_"v[32:35], v[96:99], a[0:3], 0\n" \
R_"a[168:171], v20 offset:2048\n" \
R_"a[172:175], v20 offset:3072\n" \
M_"v[36:39], v[96:99], a[16:19], 0\n" \
R_"a[188:191], v20 offset:7168\n" \
R_"a[184:187], v20 offset:6144\n" \
M_"v[40:43], v[96:99], a[32:35], 0\n" \
"s_waitcnt lgkmcnt(15)\n" \
"v_xor_b32_e32 v104, 0x80008000, v104\n" \
M_"v[44:47], v[96:99], a[48:51], 0\n" \
"v_xor_b32_e32 v105, 0x80008000, v105\n" \
"v_xor_b32_e32 v106, 0x80008000, v106\n" \
M_"v[32:35], v[100:103], a[4:7], v[32:35]\n" \
"v_xor_b32_e32 v107, 0x80008000, v107\n" \
R_"a[180:183], v20 offset:5120\n" \
M_"v[36:39], v[100:103], a[20:23], v[36:39]\n" \
R_"a[176:179], v20 offset:4096\n" \
"s_waitcnt lgkmcnt(12)\n" \
"v_xor_b32_e32 v108, 0x80008000, v108\n" \
M_"v[40:43], v[100:103], a[36:39], v[40:43]\n" \
"v_xor_b32_e32 v109, 0x80008000, v109\n" \
"v_xor_b32_e32 v110, 0x80008000, v110\n" \
M_"v[44:47], v[100:103], a[52:55], v[44:47]\n" \
"v_xor_b32_e32 v111, 0x80008000, v111\n" \
"v_add_u32_e32 v60, s15, v20\n" \
M_"v[32:35], v[104:107], a[8:11], v[32:35]\n" \
"v_add_u32_e32 v60, 0x10000, v60\n" \
R_"a[128:131], v20 offset:8192\n" \
M_"v[36:39], v[104:107], a[24:27], v[36:39]\n" \
R_"a[144:147], v20 offset:12288\n" \
R_"a[132:135], v20 offset:9216\n" \
M_"v[40:43], v[104:107], a[40:43], v[40:43]\n" \
R_"a[148:151], v20 offset:13312\n" \
R_"a[136:139], v20 offset:10240\n" \
M_"v[44:47], v[104:107], a[56:59], v[44:47]\n" \
R_"a[152:155], v20 offset:14336\n" \
"s_waitcnt lgkmcnt(13)\n" \
M_"v[32:35], v[108:111], a[12:15], v[32:35]\n" \
R_"a[140:143], v20 offset:11264\n" \
R_"a[156:159], v20 offset:15360\n" \
M_"v[36:39], v[108:111], a[28:31], v[36:39]\n" \
R_"a[64:67], v60 offset:16384\n" \
R_"a[68:71], v60 offset:17408\n" \
M_"v[40:43], v[108:111], a[44:47], v[40:43]\n" \
R_"a[72:75], v60 offset:18432\n" \
R_"a[76:79], v60 offset:19456\n" \
M_"v[44:47], v[108:111], a[60:63], v[44:47]\n" \
R_"a[80:83], v60 offset:24576\n" \
R_"a[84:87], v60 offset:25600\n" \
M_"v[64:67], a[160:163], a[0:3], v[32:35]\n" \
R_"a[88:91], v60 offset:26624\n" \
"s_waitcnt lgkmcnt(15)\n" \
M_"v[64:67], a[164:167], a[4:7], v[64:67]\n" \
R_"a[92:95], v60 offset:27648\n" \
R_"a[192:195], v60 offset:0\n" \
M_"v[64:67], a[168:171], a[8:11], v[64:67]\n" \
R_"a[196:199], v60 offset:1024\n" \
R_"a[200:203], v60 offset:2048\n" \
M_"v[64:67], a[172:175], a[12:15], v[64:67]\n" \
R_"a[204:207], v60 offset:3072\n" \
R_"a[208:211], v60 offset:8192\n" \
M_"v[68:71], a[188:191], a[12:15], v[32:35]\n" \
R_"a[212:215], v60 offset:9216\n" \
R_"a[216:219], v60 offset:10240\n" \
M_"v[68:71], a[184:187], a[8:11], v[68:71]\n" \
R_"a[220:223], v60 offset:11264\n" \
"v_mov_b32_e32 v56, s62\n" \
M_"v[68:71], a[180:183], a[4:7], v[68:71]\n" \
"v_readlane_b32 s68, v11, 0\n" \
"s_mov_b32 m0, s20\n" \
M_"v[68:71], a[176:179], a[0:3], v[68:71]\n" \
"global_load_lds_dword v[16:17], off\n" \
M_"v[76:79], a[176:179], a[16:19], v[36:39]\n" \
E_"v66, v66\n" \
M_"v[76:79], a[180:183], a[20:23], v[76:79]\n" \
E_"v67, v67\n" \
M_"v[76:79], a[184:187], a[24:27], v[76:79]\n" \
E_"v64, v64\n" \
M_"v[76:79], a[188:191], a[28:31], v[76:79]\n" \
E_"v70, v70\n" \
M_"v[72:75], a[172:175], a[28:31], v[36:39]\n" \
E_"v71, v71\n" \
M_"v[72:75], a[168:171], a[24:27], v[72:75]\n" \
E_"v68, v68\n" \
M_"v[72:75], a[164:167], a[20:23], v[72:75]\n" \
E_"v69, v69\n" \
M_"v[72:75], a[160:163], a[16:19], v[72:75]\n" \
C_"v71, v70, v71\n" \
"v_accvgpr_read_b32 v28, a124\n" \
M_"v[80:83], a[160:163], a[32:35], v[40:43]\n" \
E_"v65, v65\n" \
M_"v[80:83], a[164:167], a[36:39], v[80:83]\n" \
E_"v76, v76\n" \
M_"v[80:83], a[168:171], a[40:43], v[80:83]\n" \
E_"v77, v77\n" \
M_"v[80:83], a[172:175], a[44:47], v[80:83]\n" \
E_"v72, v72\n" \
M_"v[84:87], a[188:191], a[44:47], v[40:43]\n" \
E_"v73, v73\n" \
M_"v[84:87], a[184:187], a[40:43], v[84:87]\n" \
E_"v74, v74\n" \
M_"v[84:87], a[180:183], a[36:39], v[84:87]\n" \
E_"v75, v75\n" \
M_"v[84:87], a[176:179], a[32:35], v[84:87]\n" \
C_"v72, v72, v73\n" \
"v_accvgpr_read_b32 v29, a125\n" \
M_"v[92:95], a[176:179], a[48:51], v[44:47]\n" \
E_"v78, v78\n" \
M_"v[92:95], a[180:183], a[52:55], v[92:95]\n" \
"s_waitcnt vmcnt(3)\n" \
"v_cmp_ne_u32_e32 vcc, s62, v57\n" \
M_"v[92:95], a[184:187], a[56:59], v[92:95]\n" \
E_"v79, v79\n" \
M_"v[92:95], a[188:191], a[60:63], v[92:95]\n" \
"v_accvgpr_read_b32 v21, a126\n" \
C_"v70, v68, v69\n" \
M_"v[88:91], a[172:175], a[60:63], v[44:47]\n" \
"s_cbranch_vccnz L_skb_%=\n" \
"L_rkb_%=:\n" \
C_"v73, v74, v75\n" \
"s_mov_b64 exec, 1\n" \
"buffer_store_dword v56, off, s[56:59], s68 offset:0 sc1\n" \
"s_mov_b64 exec, -1\n" \
M_"v[88:91], a[168:171], a[56:59], v[88:91]\n" \
"v_mov_b32_e32 v62, 0\n" \
"s_mov_b32 s64, 0x8000\n" \
M_"v[88:91], a[164:167], a[52:55], v[88:91]\n" \
"v_add_u32_e32 v50, 0x6000, v55\n" \
"v_add_u32_e32 v51, 0xa000, v55\n" \
M_"v[88:91], a[160:163], a[48:51], v[88:91]\n" \
"v_add_u32_e32 v53, 0x10000, v55\n" \
"v_add_u32_e32 v48, 0xc000, v20\n" \
"s_waitcnt vmcnt(5) lgkmcnt(0)\n" \
"s_barrier\n" \
"v_add_u32_e32 v49, s17, v20\n" \
"v_accvgpr_write_b32 a124, v96\n" \
"v_accvgpr_write_b32 a125, v97\n" \
"v_accvgpr_write_b32 a126, v98\n" \
"v_accvgpr_write_b32 a127, v99\n" \
"v_accvgpr_write_b32 a224, v100\n" \
"v_accvgpr_write_b32 a225, v101\n" \
"v_accvgpr_write_b32 a226, v102\n" \
"v_accvgpr_write_b32 a227, v103\n" \
"v_accvgpr_write_b32 a228, v104\n" \
"v_accvgpr_write_b32 a229, v105\n" \
"v_accvgpr_write_b32 a230, v106\n" \
"v_accvgpr_write_b32 a231, v107\n" \
"v_accvgpr_write_b32 a232, v108\n" \
"v_accvgpr_write_b32 a233, v109\n" \
"v_accvgpr_write_b32 a234, v110\n" \
"v_accvgpr_write_b32 a235, v111\n" \
C_"v69, v66, v67\n" \
C_"v68, v64, v65\n" \
C_"v74, v76, v77\n" \
C_"v75, v78, v79\n" \
"s_sub_u32 s66, s18, 1\n" \
"s_cbranch_scc1 L_tr_%=\n" \
"s_branch L_1_%=\n" \
".fill (L_ab + 1528 - .) / 4, 4, 0xBF800000\n" \
"L_tL_1_%=:\n" \
"s_waitcnt vmcnt(4) lgkmcnt(0)\n" \
"v_smfmac_f32_16x16x64_bf16 a[100:103], a[104:107], v[116:123], v21\n" \
"s_barrier\n" \
"L_1_%=:\n" \
M_"v[96:99], a[128:131], a[0:3], v[32:35]\n" \
"s_xor_b32 m0, s65, 0x12000\n" \
R_"a[160:163], v49 offset:0\n" \
M_"v[96:99], a[132:135], a[4:7], v[96:99]\n" \
B_"v50, s[8:11], s64 offen offset:0 sc1 lds\n" \
M_"v[96:99], a[136:139], a[8:11], v[96:99]\n" \
R_"a[164:167], v49 offset:1024\n" \
R_"a[168:171], v49 offset:2048\n" \
M_"v[96:99], a[140:143], a[12:15], v[96:99]\n" \
R_"a[172:175], v49 offset:3072\n" \
R_"a[176:179], v49 offset:4096\n" \
M_"v[100:103], a[156:159], a[12:15], v[32:35]\n" \
R_"a[180:183], v49 offset:5120\n" \
R_"a[184:187], v49 offset:6144\n" \
M_"v[100:103], a[152:155], a[8:11], v[100:103]\n" \
"v_exp_f32_e64 v86, v86\n" \
M_"v[100:103], a[148:151], a[4:7], v[100:103]\n" \
"v_exp_f32_e64 v87, v87\n" \
M_"v[100:103], a[144:147], a[0:3], v[100:103]\n" \
R_"a[188:191], v49 offset:7168\n" \
C_"v87, v86, v87\n" \
M_"v[108:111], a[144:147], a[16:19], v[36:39]\n" \
"v_exp_f32_e64 v84, v84\n" \
M_"v[108:111], a[148:151], a[20:23], v[108:111]\n" \
"v_exp_f32_e64 v85, v85\n" \
M_"v[108:111], a[152:155], a[24:27], v[108:111]\n" \
"v_exp_f32_e64 v82, v82\n" \
M_"v[108:111], a[156:159], a[28:31], v[108:111]\n" \
"v_exp_f32_e64 v83, v83\n" \
M_"v[104:107], a[140:143], a[28:31], v[36:39]\n" \
"v_exp_f32_e64 v80, v80\n" \
M_"v[104:107], a[136:139], a[24:27], v[104:107]\n" \
"v_exp_f32_e64 v81, v81\n" \
M_"v[104:107], a[132:135], a[20:23], v[104:107]\n" \
"v_exp_f32_e64 v88, v88\n" \
M_"v[104:107], a[128:131], a[16:19], v[104:107]\n" \
B_"v50, s[8:11], s64 offen offset:1024 sc1 lds\n" \
M_"v[112:115], a[128:131], a[32:35], v[40:43]\n" \
"s_xor_b32 m0, s65, 0x6000\n" \
C_"v86, v84, v85\n" \
M_"v[112:115], a[132:135], a[36:39], v[112:115]\n" \
C_"v85, v82, v83\n" \
C_"v84, v80, v81\n" \
M_"v[112:115], a[136:139], a[40:43], v[112:115]\n" \
"v_exp_f32_e64 v89, v89\n" \
M_"v[112:115], a[140:143], a[44:47], v[112:115]\n" \
"v_exp_f32_e64 v90, v90\n" \
M_"v[116:119], a[156:159], a[44:47], v[40:43]\n" \
"v_exp_f32_e64 v91, v91\n" \
M_"v[116:119], a[152:155], a[40:43], v[116:119]\n" \
"v_exp_f32_e64 v92, v92\n" \
M_"v[116:119], a[148:151], a[36:39], v[116:119]\n" \
"v_exp_f32_e64 v93, v93\n" \
M_"v[116:119], a[144:147], a[32:35], v[116:119]\n" \
"v_exp_f32_e64 v94, v94\n" \
M_"v[124:127], a[144:147], a[48:51], v[44:47]\n" \
C_"v88, v88, v89\n" \
C_"v89, v90, v91\n" \
M_"v[124:127], a[148:151], a[52:55], v[124:127]\n" \
"v_exp_f32_e64 v95, v95\n" \
M_"v[124:127], a[152:155], a[56:59], v[124:127]\n" \
C_"v90, v92, v93\n" \
C_"v91, v94, v95\n" \
M_"v[124:127], a[156:159], a[60:63], v[124:127]\n" \
R_"a[144:147], v48 offset:4096\n" \
R_"a[148:151], v48 offset:5120\n" \
M_"v[120:123], a[140:143], a[60:63], v[44:47]\n" \
R_"a[152:155], v48 offset:6144\n" \
R_"a[140:143], v48 offset:3072\n" \
M_"v[120:123], a[136:139], a[56:59], v[120:123]\n" \
R_"a[136:139], v48 offset:2048\n" \
R_"a[156:159], v48 offset:7168\n" \
M_"v[120:123], a[132:135], a[52:55], v[120:123]\n" \
B_"v51, s[8:11], s64 offen offset:0 sc1 lds\n" \
M_"v[120:123], a[128:131], a[48:51], v[120:123]\n" \
R_"a[128:131], v48 offset:0\n" \
"s_waitcnt lgkmcnt(7)\n" \
M_"v[128:131], a[160:163], v[68:71], v[128:131]\n" \
E_"v102, v102\n" \
M_"v[160:163], a[160:163], v[72:75], v[160:163]\n" \
"v_exp_f32_e64 v103, v103\n" \
M_"v[132:135], a[164:167], v[68:71], v[132:135]\n" \
R_"a[132:135], v48 offset:1024\n" \
C_"v103, v102, v103\n" \
M_"v[164:167], a[164:167], v[72:75], v[164:167]\n" \
"v_exp_f32_e64 v100, v100\n" \
M_"v[136:139], a[168:171], v[68:71], v[136:139]\n" \
"v_exp_f32_e64 v101, v101\n" \
M_"v[168:171], a[168:171], v[72:75], v[168:171]\n" \
"v_exp_f32_e64 v98, v98\n" \
M_"v[140:143], a[172:175], v[68:71], v[140:143]\n" \
"v_exp_f32_e64 v99, v99\n" \
M_"v[172:175], a[172:175], v[72:75], v[172:175]\n" \
"v_exp_f32_e64 v96, v96\n" \
M_"v[144:147], a[176:179], v[68:71], v[144:147]\n" \
"v_exp_f32_e64 v97, v97\n" \
M_"v[176:179], a[176:179], v[72:75], v[176:179]\n" \
"v_exp_f32_e64 v104, v104\n" \
M_"v[148:151], a[180:183], v[68:71], v[148:151]\n" \
"v_exp_f32_e64 v105, v105\n" \
M_"v[180:183], a[180:183], v[72:75], v[180:183]\n" \
C_"v102, v100, v101\n" \
C_"v101, v98, v99\n" \
M_"v[152:155], a[184:187], v[68:71], v[152:155]\n" \
C_"v100, v96, v97\n" \
C_"v104, v104, v105\n" \
M_"v[184:187], a[184:187], v[72:75], v[184:187]\n" \
B_"v51, s[8:11], s64 offen offset:1024 sc1 lds\n" \
M_"v[156:159], a[188:191], v[68:71], v[156:159]\n" \
"v_exp_f32_e64 v106, v106\n" \
M_"v[188:191], a[188:191], v[72:75], v[188:191]\n" \
"v_exp_f32_e64 v107, v107\n" \
"v_smfmac_f32_16x16x64_bf16 a[96:99], a[104:107], v[68:75], v21\n" \
"v_exp_f32_e64 v108, v108\n" \
M_"v[220:223], a[188:191], v[84:87], v[220:223]\n" \
"v_exp_f32_e64 v109, v109\n" \
M_"v[252:255], a[188:191], v[88:91], v[252:255]\n" \
"v_exp_f32_e64 v110, v110\n" \
M_"v[216:219], a[184:187], v[84:87], v[216:219]\n" \
"v_exp_f32_e64 v111, v111\n" \
M_"v[248:251], a[184:187], v[88:91], v[248:251]\n" \
"v_exp_f32_e64 v118, v118\n" \
M_"v[212:215], a[180:183], v[84:87], v[212:215]\n" \
"v_exp_f32_e64 v119, v119\n" \
M_"v[244:247], a[180:183], v[88:91], v[244:247]\n" \
C_"v105, v106, v107\n" \
C_"v106, v108, v109\n" \
M_"v[208:211], a[176:179], v[84:87], v[208:211]\n" \
C_"v107, v110, v111\n" \
C_"v119, v118, v119\n" \
M_"v[240:243], a[176:179], v[88:91], v[240:243]\n" \
"v_exp_f32_e64 v116, v116\n" \
M_"v[204:207], a[172:175], v[84:87], v[204:207]\n" \
"v_exp_f32_e64 v117, v117\n" \
M_"v[236:239], a[172:175], v[88:91], v[236:239]\n" \
"v_exp_f32_e64 v114, v114\n" \
M_"v[200:203], a[168:171], v[84:87], v[200:203]\n" \
"v_exp_f32_e64 v115, v115\n" \
M_"v[232:235], a[168:171], v[88:91], v[232:235]\n" \
"v_exp_f32_e64 v112, v112\n" \
M_"v[196:199], a[164:167], v[84:87], v[196:199]\n" \
"v_exp_f32_e64 v113, v113\n" \
M_"v[228:231], a[164:167], v[88:91], v[228:231]\n" \
"v_exp_f32_e64 v120, v120\n" \
M_"v[192:195], a[160:163], v[84:87], v[192:195]\n" \
"v_exp_f32_e64 v121, v121\n" \
M_"v[224:227], a[160:163], v[88:91], v[224:227]\n" \
C_"v118, v116, v117\n" \
"s_waitcnt vmcnt(4) lgkmcnt(0)\n" \
"v_smfmac_f32_16x16x64_bf16 a[100:103], a[104:107], v[84:91], v21\n" \
"s_barrier\n" \
M_"v[64:67], a[128:131], a[0:3], v[32:35]\n" \
"s_xor_b32 m0, s65, 0x14000\n" \
R_"a[160:163], v49 offset:8192\n" \
M_"v[64:67], a[132:135], a[4:7], v[64:67]\n" \
B_"v52, s[8:11], s64 offen offset:0 sc1 lds\n" \
M_"v[64:67], a[136:139], a[8:11], v[64:67]\n" \
R_"a[164:167], v49 offset:9216\n" \
R_"a[168:171], v49 offset:10240\n" \
M_"v[64:67], a[140:143], a[12:15], v[64:67]\n" \
R_"a[172:175], v49 offset:11264\n" \
R_"a[176:179], v49 offset:12288\n" \
M_"v[68:71], a[156:159], a[12:15], v[32:35]\n" \
R_"a[180:183], v49 offset:13312\n" \
R_"a[184:187], v49 offset:14336\n" \
M_"v[68:71], a[152:155], a[8:11], v[68:71]\n" \
R_"a[188:191], v49 offset:15360\n" \
C_"v117, v114, v115\n" \
M_"v[68:71], a[148:151], a[4:7], v[68:71]\n" \
C_"v116, v112, v113\n" \
C_"v120, v120, v121\n" \
M_"v[68:71], a[144:147], a[0:3], v[68:71]\n" \
"v_exp_f32_e64 v122, v122\n" \
M_"v[76:79], a[144:147], a[16:19], v[36:39]\n" \
"v_exp_f32_e64 v123, v123\n" \
M_"v[76:79], a[148:151], a[20:23], v[76:79]\n" \
"v_exp_f32_e64 v124, v124\n" \
M_"v[76:79], a[152:155], a[24:27], v[76:79]\n" \
"v_exp_f32_e64 v125, v125\n" \
M_"v[76:79], a[156:159], a[28:31], v[76:79]\n" \
"v_exp_f32_e64 v126, v126\n" \
M_"v[72:75], a[140:143], a[28:31], v[36:39]\n" \
"v_exp_f32_e64 v127, v127\n" \
M_"v[72:75], a[136:139], a[24:27], v[72:75]\n" \
C_"v121, v122, v123\n" \
C_"v122, v124, v125\n" \
M_"v[72:75], a[132:135], a[20:23], v[72:75]\n" \
C_"v123, v126, v127\n" \
"v_add_u32_e32 v49, 0x10000, v48\n" \
M_"v[72:75], a[128:131], a[16:19], v[72:75]\n" \
B_"v52, s[8:11], s64 offen offset:1024 sc1 lds\n" \
M_"v[80:83], a[128:131], a[32:35], v[40:43]\n" \
"v_exp_f32_e64 v70, v70\n" \
M_"v[80:83], a[132:135], a[36:39], v[80:83]\n" \
"v_exp_f32_e64 v71, v71\n" \
M_"v[80:83], a[136:139], a[40:43], v[80:83]\n" \
"s_mov_b32 m0, s65\n" \
C_"v71, v70, v71\n" \
M_"v[80:83], a[140:143], a[44:47], v[80:83]\n" \
E_"v72, v72\n" \
M_"v[84:87], a[156:159], a[44:47], v[40:43]\n" \
"v_exp_f32_e64 v73, v73\n" \
M_"v[84:87], a[152:155], a[40:43], v[84:87]\n" \
"v_exp_f32_e64 v68, v68\n" \
M_"v[84:87], a[148:151], a[36:39], v[84:87]\n" \
"v_exp_f32_e64 v69, v69\n" \
M_"v[84:87], a[144:147], a[32:35], v[84:87]\n" \
"v_exp_f32_e64 v74, v74\n" \
M_"v[92:95], a[144:147], a[48:51], v[44:47]\n" \
R_"a[144:147], v48 offset:12288\n" \
C_"v72, v72, v73\n" \
M_"v[92:95], a[148:151], a[52:55], v[92:95]\n" \
R_"a[148:151], v48 offset:13312\n" \
C_"v70, v68, v69\n" \
M_"v[92:95], a[152:155], a[56:59], v[92:95]\n" \
R_"a[152:155], v48 offset:14336\n" \
"s_xor_b32 s65, s65, 0x4000\n" \
M_"v[92:95], a[156:159], a[60:63], v[92:95]\n" \
"v_exp_f32_e64 v75, v75\n" \
M_"v[88:91], a[140:143], a[60:63], v[44:47]\n" \
R_"a[140:143], v48 offset:11264\n" \
R_"a[156:159], v48 offset:15360\n" \
M_"v[88:91], a[136:139], a[56:59], v[88:91]\n" \
R_"a[136:139], v48 offset:10240\n" \
C_"v73, v74, v75\n" \
M_"v[88:91], a[132:135], a[52:55], v[88:91]\n" \
B_"v53, s[8:11], s64 offen offset:0 sc1 lds\n" \
M_"v[88:91], a[128:131], a[48:51], v[88:91]\n" \
R_"a[128:131], v48 offset:8192\n" \
"s_waitcnt lgkmcnt(7)\n" \
M_"v[128:131], a[160:163], v[100:103], v[128:131]\n" \
E_"v66, v66\n" \
M_"v[160:163], a[160:163], v[104:107], v[160:163]\n" \
"v_exp_f32_e64 v67, v67\n" \
M_"v[132:135], a[164:167], v[100:103], v[132:135]\n" \
R_"a[132:135], v48 offset:9216\n" \
C_"v69, v66, v67\n" \
M_"v[164:167], a[164:167], v[104:107], v[164:167]\n" \
"v_exp_f32_e64 v64, v64\n" \
M_"v[136:139], a[168:171], v[100:103], v[136:139]\n" \
"v_exp_f32_e64 v65, v65\n" \
M_"v[168:171], a[168:171], v[104:107], v[168:171]\n" \
"v_exp_f32_e64 v76, v76\n" \
M_"v[140:143], a[172:175], v[100:103], v[140:143]\n" \
"v_exp_f32_e64 v77, v77\n" \
M_"v[172:175], a[172:175], v[104:107], v[172:175]\n" \
"v_exp_f32_e64 v78, v78\n" \
M_"v[144:147], a[176:179], v[100:103], v[144:147]\n" \
"v_exp_f32_e64 v79, v79\n" \
M_"v[176:179], a[176:179], v[104:107], v[176:179]\n" \
C_"v68, v64, v65\n" \
C_"v74, v76, v77\n" \
M_"v[148:151], a[180:183], v[100:103], v[148:151]\n" \
C_"v75, v78, v79\n" \
"v_xor_b32_e32 v48, 0x4000, v48\n" \
M_"v[180:183], a[180:183], v[104:107], v[180:183]\n" \
M_"v[152:155], a[184:187], v[100:103], v[152:155]\n" \
M_"v[184:187], a[184:187], v[104:107], v[184:187]\n" \
B_"v53, s[8:11], s64 offen offset:1024 sc1 lds\n" \
M_"v[156:159], a[188:191], v[100:103], v[156:159]\n" \
"s_add_u32 s64, s64, 0x8000\n" \
"s_sub_u32 s66, s66, 1\n" \
"s_nop 0\n" \
M_"v[188:191], a[188:191], v[104:107], v[188:191]\n" \
"v_smfmac_f32_16x16x64_bf16 a[96:99], a[104:107], v[100:107], v21\n" \
M_"v[220:223], a[188:191], v[116:119], v[220:223]\n" \
M_"v[252:255], a[188:191], v[120:123], v[252:255]\n" \
M_"v[216:219], a[184:187], v[116:119], v[216:219]\n" \
M_"v[248:251], a[184:187], v[120:123], v[248:251]\n" \
M_"v[212:215], a[180:183], v[116:119], v[212:215]\n" \
M_"v[244:247], a[180:183], v[120:123], v[244:247]\n" \
M_"v[208:211], a[176:179], v[116:119], v[208:211]\n" \
M_"v[240:243], a[176:179], v[120:123], v[240:243]\n" \
M_"v[204:207], a[172:175], v[116:119], v[204:207]\n" \
M_"v[236:239], a[172:175], v[120:123], v[236:239]\n" \
M_"v[200:203], a[168:171], v[116:119], v[200:203]\n" \
M_"v[232:235], a[168:171], v[120:123], v[232:235]\n" \
M_"v[196:199], a[164:167], v[116:119], v[196:199]\n" \
M_"v[228:231], a[164:167], v[120:123], v[228:231]\n" \
M_"v[192:195], a[160:163], v[116:119], v[192:195]\n" \
M_"v[224:227], a[160:163], v[120:123], v[224:227]\n" \
"s_cbranch_scc0 L_tL_1_%=\n" \
"s_waitcnt vmcnt(4) lgkmcnt(0)\n" \
"v_smfmac_f32_16x16x64_bf16 a[100:103], a[104:107], v[116:123], v21\n" \
"s_barrier\n" \
"L_tr_%=:\n" \
"s_setpc_b64 s[28:29]\n" \
".fill (L_ab + 4096 - .) / 4, 4, 0xBF800000\n" \
".fill (L_ab + 4096 - .) / 4, 4, 0xBF800000\n" \
"L_ap1:\n" \
"s_setpc_b64 s[88:89]\n" \
"L_gtr:\n" \
M_"v[96:99], a[128:131], a[0:3], v[32:35]\n" \
"s_xor_b32 m0, s65, 0x12000\n" \
"s_mov_b32 s65, s13\n" \
M_"v[96:99], a[132:135], a[4:7], v[96:99]\n" \
B_"v50, s[8:11], s64 offen offset:0 sc1 lds\n" \
M_"v[96:99], a[136:139], a[8:11], v[96:99]\n" \
"v_add_u32_e32 v63, s15, v20\n" \
"s_add_u32 s67, s14, 40960\n" \
M_"v[96:99], a[140:143], a[12:15], v[96:99]\n" \
R_"a[160:163], v49 offset:0\n" \
R_"a[164:167], v49 offset:1024\n" \
M_"v[100:103], a[156:159], a[12:15], v[32:35]\n" \
B_"v50, s[8:11], s64 offen offset:1024 sc1 lds\n" \
M_"v[100:103], a[152:155], a[8:11], v[100:103]\n" \
"v_add_u32_e32 v50, 0x6000, v63\n" \
"s_xor_b32 m0, s65, 0x8000\n" \
M_"v[100:103], a[148:151], a[4:7], v[100:103]\n" \
R_"a[168:171], v49 offset:2048\n" \
R_"a[172:175], v49 offset:3072\n" \
M_"v[100:103], a[144:147], a[0:3], v[100:103]\n" \
R_"a[176:179], v49 offset:4096\n" \
R_"a[180:183], v49 offset:5120\n" \
M_"v[108:111], a[144:147], a[16:19], v[36:39]\n" \
B_"v50, s[8:11], s67 offen offset:0 sc1 lds\n" \
M_"v[108:111], a[148:151], a[20:23], v[108:111]\n" \
R_"a[184:187], v49 offset:6144\n" \
R_"a[188:191], v49 offset:7168\n" \
M_"v[108:111], a[152:155], a[24:27], v[108:111]\n" \
"s_mov_b32 s64, s14\n" \
"v_add_u32_e32 v51, 0x12000, v63\n" \
M_"v[108:111], a[156:159], a[28:31], v[108:111]\n" \
E_"v86, v86\n" \
M_"v[104:107], a[140:143], a[28:31], v[36:39]\n" \
B_"v50, s[8:11], s67 offen offset:1024 sc1 lds\n" \
M_"v[104:107], a[136:139], a[24:27], v[104:107]\n" \
E_"v87, v87\n" \
M_"v[104:107], a[132:135], a[20:23], v[104:107]\n" \
E_"v84, v84\n" \
M_"v[104:107], a[128:131], a[16:19], v[104:107]\n" \
E_"v85, v85\n" \
M_"v[112:115], a[128:131], a[32:35], v[40:43]\n" \
C_"v87, v86, v87\n" \
C_"v86, v84, v85\n" \
M_"v[112:115], a[132:135], a[36:39], v[112:115]\n" \
B_"v50, s[8:11], s67 offen offset:2048 sc1 lds\n" \
M_"v[112:115], a[136:139], a[40:43], v[112:115]\n" \
E_"v82, v82\n" \
M_"v[112:115], a[140:143], a[44:47], v[112:115]\n" \
E_"v83, v83\n" \
M_"v[116:119], a[156:159], a[44:47], v[40:43]\n" \
E_"v80, v80\n" \
M_"v[116:119], a[152:155], a[40:43], v[116:119]\n" \
B_"v50, s[8:11], s67 offen offset:3072 sc1 lds\n" \
M_"v[116:119], a[148:151], a[36:39], v[116:119]\n" \
"s_xor_b32 m0, s65, 0xa000\n" \
C_"v85, v82, v83\n" \
M_"v[116:119], a[144:147], a[32:35], v[116:119]\n" \
E_"v81, v81\n" \
M_"v[124:127], a[144:147], a[48:51], v[44:47]\n" \
C_"v84, v80, v81\n" \
"v_readlane_b32 s93, v11, 22\n" \
M_"v[124:127], a[148:151], a[52:55], v[124:127]\n" \
B_"v51, s[8:11], s64 offen offset:0 sc1 lds\n" \
M_"v[124:127], a[152:155], a[56:59], v[124:127]\n" \
E_"v88, v88\n" \
M_"v[124:127], a[156:159], a[60:63], v[124:127]\n" \
E_"v89, v89\n" \
M_"v[120:123], a[140:143], a[60:63], v[44:47]\n" \
E_"v90, v90\n" \
M_"v[120:123], a[136:139], a[56:59], v[120:123]\n" \
E_"v91, v91\n" \
M_"v[120:123], a[132:135], a[52:55], v[120:123]\n" \
B_"v51, s[8:11], s64 offen offset:1024 sc1 lds\n" \
M_"v[120:123], a[128:131], a[48:51], v[120:123]\n" \
C_"v88, v88, v89\n" \
"s_waitcnt lgkmcnt(0)\n" \
M_"v[128:131], a[160:163], v[68:71], v[128:131]\n" \
E_"v92, v92\n" \
M_"v[160:163], a[160:163], v[72:75], v[160:163]\n" \
E_"v93, v93\n" \
M_"v[132:135], a[164:167], v[68:71], v[132:135]\n" \
B_"v51, s[8:11], s64 offen offset:2048 sc1 lds\n" \
M_"v[164:167], a[164:167], v[72:75], v[164:167]\n" \
E_"v94, v94\n" \
M_"v[136:139], a[168:171], v[68:71], v[136:139]\n" \
E_"v95, v95\n" \
M_"v[168:171], a[168:171], v[72:75], v[168:171]\n" \
C_"v89, v90, v91\n" \
C_"v90, v92, v93\n" \
M_"v[140:143], a[172:175], v[68:71], v[140:143]\n" \
B_"v51, s[8:11], s64 offen offset:3072 sc1 lds\n" \
M_"v[172:175], a[172:175], v[72:75], v[172:175]\n" \
C_"v91, v94, v95\n" \
"v_readlane_b32 s80, v11, 11\n" \
M_"v[144:147], a[176:179], v[68:71], v[144:147]\n" \
"v_readlane_b32 s81, v11, 12\n" \
"s_mov_b32 s82, 0x47fca0\n" \
M_"v[176:179], a[176:179], v[72:75], v[176:179]\n" \
"s_mov_b32 s83, 0x20000\n" \
"v_readlane_b32 s92, v11, 29\n" \
M_"v[148:151], a[180:183], v[68:71], v[148:151]\n" \
"s_add_u32 m0, s93, 0x0\n" \
R_"a[128:131], v48 offset:0\n" \
M_"v[180:183], a[180:183], v[72:75], v[180:183]\n" \
R_"a[144:147], v48 offset:4096\n" \
R_"a[132:135], v48 offset:1024\n" \
M_"v[152:155], a[184:187], v[68:71], v[152:155]\n" \
B_"v0, s[80:83], s92 offen offset:0 lds\n" \
M_"v[184:187], a[184:187], v[72:75], v[184:187]\n" \
R_"a[148:151], v48 offset:5120\n" \
R_"a[136:139], v48 offset:2048\n" \
M_"v[156:159], a[188:191], v[68:71], v[156:159]\n" \
R_"a[152:155], v48 offset:6144\n" \
R_"a[140:143], v48 offset:3072\n" \
M_"v[188:191], a[188:191], v[72:75], v[188:191]\n" \
R_"a[156:159], v48 offset:7168\n" \
"v_add_u32_e32 v52, 0x14000, v63\n" \
"v_smfmac_f32_16x16x64_bf16 a[96:99], a[104:107], v[68:75], v21\n" \
B_"v1, s[80:83], s92 offen offset:1024 lds\n" \
M_"v[220:223], a[188:191], v[84:87], v[220:223]\n" \
E_"v102, v102\n" \
M_"v[252:255], a[188:191], v[88:91], v[252:255]\n" \
E_"v103, v103\n" \
M_"v[216:219], a[184:187], v[84:87], v[216:219]\n" \
B_"v2, s[80:83], s92 offen offset:2048 lds\n" \
M_"v[248:251], a[184:187], v[88:91], v[248:251]\n" \
"v_add_u32_e32 v53, 0x20000, v63\n" \
C_"v103, v102, v103\n" \
M_"v[212:215], a[180:183], v[84:87], v[212:215]\n" \
E_"v100, v100\n" \
M_"v[244:247], a[180:183], v[88:91], v[244:247]\n" \
E_"v101, v101\n" \
M_"v[208:211], a[176:179], v[84:87], v[208:211]\n" \
B_"v3, s[80:83], s92 offen offset:3072 lds\n" \
M_"v[240:243], a[176:179], v[88:91], v[240:243]\n" \
E_"v98, v98\n" \
M_"v[204:207], a[172:175], v[84:87], v[204:207]\n" \
E_"v99, v99\n" \
M_"v[236:239], a[172:175], v[88:91], v[236:239]\n" \
E_"v96, v96\n" \
M_"v[200:203], a[168:171], v[84:87], v[200:203]\n" \
E_"v97, v97\n" \
M_"v[232:235], a[168:171], v[88:91], v[232:235]\n" \
E_"v104, v104\n" \
M_"v[196:199], a[164:167], v[84:87], v[196:199]\n" \
E_"v105, v105\n" \
M_"v[228:231], a[164:167], v[88:91], v[228:231]\n" \
C_"v102, v100, v101\n" \
C_"v101, v98, v99\n" \
M_"v[192:195], a[160:163], v[84:87], v[192:195]\n" \
C_"v100, v96, v97\n" \
C_"v104, v104, v105\n" \
M_"v[224:227], a[160:163], v[88:91], v[224:227]\n" \
E_"v106, v106\n" \
"v_smfmac_f32_16x16x64_bf16 a[100:103], a[104:107], v[84:91], v21\n" \
E_"v107, v107\n" \
"s_waitcnt vmcnt(14) lgkmcnt(0)\n" \
"s_barrier\n" \
M_"v[64:67], a[128:131], a[0:3], v[32:35]\n" \
"s_xor_b32 m0, s65, 0x18000\n" \
R_"a[160:163], v49 offset:8192\n" \
M_"v[64:67], a[132:135], a[4:7], v[64:67]\n" \
B_"v52, s[8:11], s64 offen offset:0 sc1 lds\n" \
M_"v[64:67], a[136:139], a[8:11], v[64:67]\n" \
R_"a[164:167], v49 offset:9216\n" \
R_"a[168:171], v49 offset:10240\n" \
M_"v[64:67], a[140:143], a[12:15], v[64:67]\n" \
R_"a[172:175], v49 offset:11264\n" \
R_"a[176:179], v49 offset:12288\n" \
M_"v[68:71], a[156:159], a[12:15], v[32:35]\n" \
B_"v52, s[8:11], s64 offen offset:1024 sc1 lds\n" \
M_"v[68:71], a[152:155], a[8:11], v[68:71]\n" \
R_"a[180:183], v49 offset:13312\n" \
R_"a[184:187], v49 offset:14336\n" \
M_"v[68:71], a[148:151], a[4:7], v[68:71]\n" \
E_"v108, v108\n" \
M_"v[68:71], a[144:147], a[0:3], v[68:71]\n" \
E_"v109, v109\n" \
M_"v[76:79], a[144:147], a[16:19], v[36:39]\n" \
E_"v110, v110\n" \
M_"v[76:79], a[148:151], a[20:23], v[76:79]\n" \
B_"v52, s[8:11], s64 offen offset:2048 sc1 lds\n" \
M_"v[76:79], a[152:155], a[24:27], v[76:79]\n" \
E_"v111, v111\n" \
M_"v[76:79], a[156:159], a[28:31], v[76:79]\n" \
C_"v105, v106, v107\n" \
C_"v106, v108, v109\n" \
M_"v[72:75], a[140:143], a[28:31], v[36:39]\n" \
C_"v107, v110, v111\n" \
R_"a[188:191], v49 offset:15360\n" \
M_"v[72:75], a[136:139], a[24:27], v[72:75]\n" \
B_"v52, s[8:11], s64 offen offset:3072 sc1 lds\n" \
M_"v[72:75], a[132:135], a[20:23], v[72:75]\n" \
"s_mov_b32 m0, s65\n" \
"v_add_u32_e32 v49, 0x10000, v48\n" \
M_"v[72:75], a[128:131], a[16:19], v[72:75]\n" \
E_"v118, v118\n" \
M_"v[80:83], a[128:131], a[32:35], v[40:43]\n" \
E_"v119, v119\n" \
M_"v[80:83], a[132:135], a[36:39], v[80:83]\n" \
E_"v116, v116\n" \
M_"v[80:83], a[136:139], a[40:43], v[80:83]\n" \
B_"v53, s[8:11], s64 offen offset:0 sc1 lds\n" \
M_"v[80:83], a[140:143], a[44:47], v[80:83]\n" \
E_"v117, v117\n" \
M_"v[84:87], a[156:159], a[44:47], v[40:43]\n" \
C_"v119, v118, v119\n" \
C_"v118, v116, v117\n" \
M_"v[84:87], a[152:155], a[40:43], v[84:87]\n" \
E_"v114, v114\n" \
M_"v[84:87], a[148:151], a[36:39], v[84:87]\n" \
E_"v115, v115\n" \
M_"v[84:87], a[144:147], a[32:35], v[84:87]\n" \
B_"v53, s[8:11], s64 offen offset:1024 sc1 lds\n" \
M_"v[92:95], a[144:147], a[48:51], v[44:47]\n" \
E_"v112, v112\n" \
M_"v[92:95], a[148:151], a[52:55], v[92:95]\n" \
E_"v113, v113\n" \
M_"v[92:95], a[152:155], a[56:59], v[92:95]\n" \
C_"v117, v114, v115\n" \
C_"v116, v112, v113\n" \
M_"v[92:95], a[156:159], a[60:63], v[92:95]\n" \
B_"v53, s[8:11], s64 offen offset:2048 sc1 lds\n" \
M_"v[88:91], a[140:143], a[60:63], v[44:47]\n" \
E_"v120, v120\n" \
M_"v[88:91], a[136:139], a[56:59], v[88:91]\n" \
E_"v121, v121\n" \
M_"v[88:91], a[132:135], a[52:55], v[88:91]\n" \
E_"v122, v122\n" \
M_"v[88:91], a[128:131], a[48:51], v[88:91]\n" \
C_"v120, v120, v121\n" \
"s_waitcnt lgkmcnt(0)\n" \
M_"v[128:131], a[160:163], v[100:103], v[128:131]\n" \
B_"v53, s[8:11], s64 offen offset:3072 sc1 lds\n" \
M_"v[160:163], a[160:163], v[104:107], v[160:163]\n" \
"s_add_u32 m0, s93, 0x1000\n" \
R_"a[128:131], v48 offset:8192\n" \
M_"v[132:135], a[164:167], v[100:103], v[132:135]\n" \
E_"v123, v123\n" \
M_"v[164:167], a[164:167], v[104:107], v[164:167]\n" \
E_"v124, v124\n" \
M_"v[136:139], a[168:171], v[100:103], v[136:139]\n" \
B_"v4, s[80:83], s92 offen offset:0 lds\n" \
M_"v[168:171], a[168:171], v[104:107], v[168:171]\n" \
E_"v125, v125\n" \
M_"v[140:143], a[172:175], v[100:103], v[140:143]\n" \
E_"v126, v126\n" \
M_"v[172:175], a[172:175], v[104:107], v[172:175]\n" \
E_"v127, v127\n" \
M_"v[144:147], a[176:179], v[100:103], v[144:147]\n" \
C_"v121, v122, v123\n" \
C_"v122, v124, v125\n" \
M_"v[176:179], a[176:179], v[104:107], v[176:179]\n" \
B_"v5, s[80:83], s92 offen offset:1024 lds\n" \
M_"v[148:151], a[180:183], v[100:103], v[148:151]\n" \
C_"v123, v126, v127\n" \
R_"a[144:147], v48 offset:12288\n" \
M_"v[180:183], a[180:183], v[104:107], v[180:183]\n" \
R_"a[132:135], v48 offset:9216\n" \
R_"a[148:151], v48 offset:13312\n" \
M_"v[152:155], a[184:187], v[100:103], v[152:155]\n" \
R_"a[136:139], v48 offset:10240\n" \
R_"a[152:155], v48 offset:14336\n" \
M_"v[184:187], a[184:187], v[104:107], v[184:187]\n" \
R_"a[140:143], v48 offset:11264\n" \
R_"a[156:159], v48 offset:15360\n" \
M_"v[156:159], a[188:191], v[100:103], v[156:159]\n" \
B_"v6, s[80:83], s92 offen offset:2048 lds\n" \
M_"v[188:191], a[188:191], v[104:107], v[188:191]\n" \
E_"v70, v70\n" \
"v_smfmac_f32_16x16x64_bf16 a[96:99], a[104:107], v[100:107], v21\n" \
E_"v71, v71\n" \
M_"v[220:223], a[188:191], v[116:119], v[220:223]\n" \
E_"v72, v72\n" \
M_"v[252:255], a[188:191], v[120:123], v[252:255]\n" \
B_"v7, s[80:83], s92 offen offset:3072 lds\n" \
M_"v[216:219], a[184:187], v[116:119], v[216:219]\n" \
"s_add_u32 m0, s93, 0x2000\n" \
C_"v71, v70, v71\n" \
M_"v[248:251], a[184:187], v[120:123], v[248:251]\n" \
E_"v73, v73\n" \
M_"v[212:215], a[180:183], v[116:119], v[212:215]\n" \
E_"v68, v68\n" \
M_"v[244:247], a[180:183], v[120:123], v[244:247]\n" \
E_"v69, v69\n" \
M_"v[208:211], a[176:179], v[116:119], v[208:211]\n" \
B_"v8, s[80:83], s92 offen offset:0 lds\n" \
M_"v[240:243], a[176:179], v[120:123], v[240:243]\n" \
E_"v74, v74\n" \
M_"v[204:207], a[172:175], v[116:119], v[204:207]\n" \
E_"v75, v75\n" \
M_"v[236:239], a[172:175], v[120:123], v[236:239]\n" \
E_"v66, v66\n" \
M_"v[200:203], a[168:171], v[116:119], v[200:203]\n" \
E_"v67, v67\n" \
M_"v[232:235], a[168:171], v[120:123], v[232:235]\n" \
E_"v64, v64\n" \
M_"v[196:199], a[164:167], v[116:119], v[196:199]\n" \
E_"v65, v65\n" \
M_"v[228:231], a[164:167], v[120:123], v[228:231]\n" \
C_"v72, v72, v73\n" \
C_"v70, v68, v69\n" \
M_"v[192:195], a[160:163], v[116:119], v[192:195]\n" \
E_"v76, v76\n" \
M_"v[224:227], a[160:163], v[120:123], v[224:227]\n" \
E_"v77, v77\n" \
"v_smfmac_f32_16x16x64_bf16 a[100:103], a[104:107], v[116:123], v21\n" \
E_"v78, v78\n" \
"s_waitcnt vmcnt(13) lgkmcnt(0)\n" \
"s_barrier\n" \
E_"v79, v79\n" \
C_"v73, v74, v75\n" \
C_"v69, v66, v67\n" \
C_"v68, v64, v65\n" \
C_"v74, v76, v77\n" \
C_"v75, v78, v79\n" \
"v_add_u32_e32 v48, s16, v20\n" \
"s_xor_b32 s65, s65, 0x8000\n" \
"s_add_u32 s64, s64, 0x10000\n" \
"s_getpc_b64 s[86:87]\n" \
"L_hpt_%=:\n" \
"s_add_u32 s86, s86, L_ap8-L_hpt_%=\n" \
"s_addc_u32 s87, s87, 0\n" \
"s_add_u32 s86, s86, s15\n" \
"s_addc_u32 s87, s87, 0\n" \
"s_mov_b64 exec, 0\n" \
"s_swappc_b64 s[88:89], s[86:87]\n" \
"s_mov_b64 exec, -1\n" \
"s_sub_u32 s66, s19, 1\n" \
"s_cbranch_scc1 L_la_%=\n" \
"s_branch L_2_%=\n" \
".fill (L_ab + 6400 - .) / 4, 4, 0xBF800000\n" \
"L_tL_2_%=:\n" \
"s_waitcnt vmcnt(8) lgkmcnt(0)\n" \
"v_smfmac_f32_16x16x64_bf16 a[100:103], a[104:107], v[116:123], v21\n" \
"s_barrier\n" \
"L_2_%=:\n" \
M_"v[96:99], a[128:131], a[64:67], v[32:35]\n" \
"s_xor_b32 m0, s65, 0x12000\n" \
R_"a[160:163], v49 offset:0\n" \
M_"v[96:99], a[132:135], a[68:71], v[96:99]\n" \
B_"v50, s[8:11], s64 offen offset:0 sc1 lds\n" \
M_"v[96:99], a[136:139], a[72:75], v[96:99]\n" \
R_"a[164:167], v49 offset:1024\n" \
R_"a[168:171], v49 offset:2048\n" \
M_"v[96:99], a[140:143], a[76:79], v[96:99]\n" \
R_"a[172:175], v49 offset:3072\n" \
R_"a[176:179], v49 offset:4096\n" \
M_"v[100:103], a[156:159], a[76:79], v[32:35]\n" \
R_"a[180:183], v49 offset:5120\n" \
R_"a[184:187], v49 offset:6144\n" \
M_"v[100:103], a[152:155], a[72:75], v[100:103]\n" \
"v_exp_f32_e64 v86, v86\n" \
M_"v[100:103], a[148:151], a[68:71], v[100:103]\n" \
"v_exp_f32_e64 v87, v87\n" \
M_"v[100:103], a[144:147], a[64:67], v[100:103]\n" \
B_"v50, s[8:11], s64 offen offset:1024 sc1 lds\n" \
M_"v[108:111], a[144:147], a[80:83], v[36:39]\n" \
R_"a[188:191], v49 offset:7168\n" \
C_"v87, v86, v87\n" \
M_"v[108:111], a[148:151], a[84:87], v[108:111]\n" \
"v_exp_f32_e64 v84, v84\n" \
M_"v[108:111], a[152:155], a[88:91], v[108:111]\n" \
"v_exp_f32_e64 v85, v85\n" \
M_"v[108:111], a[156:159], a[92:95], v[108:111]\n" \
"v_exp_f32_e64 v82, v82\n" \
M_"v[104:107], a[140:143], a[92:95], v[36:39]\n" \
"v_exp_f32_e64 v83, v83\n" \
M_"v[104:107], a[136:139], a[88:91], v[104:107]\n" \
"v_exp_f32_e64 v80, v80\n" \
M_"v[104:107], a[132:135], a[84:87], v[104:107]\n" \
"v_exp_f32_e64 v81, v81\n" \
M_"v[104:107], a[128:131], a[80:83], v[104:107]\n" \
B_"v50, s[8:11], s64 offen offset:2048 sc1 lds\n" \
M_"v[112:115], a[128:131], a[192:195], v[40:43]\n" \
"v_exp_f32_e64 v88, v88\n" \
M_"v[112:115], a[132:135], a[196:199], v[112:115]\n" \
"v_exp_f32_e64 v89, v89\n" \
M_"v[112:115], a[136:139], a[200:203], v[112:115]\n" \
C_"v86, v84, v85\n" \
C_"v85, v82, v83\n" \
M_"v[112:115], a[140:143], a[204:207], v[112:115]\n" \
C_"v84, v80, v81\n" \
C_"v88, v88, v89\n" \
M_"v[116:119], a[156:159], a[204:207], v[40:43]\n" \
"v_exp_f32_e64 v90, v90\n" \
M_"v[116:119], a[152:155], a[200:203], v[116:119]\n" \
"v_exp_f32_e64 v91, v91\n" \
M_"v[116:119], a[148:151], a[196:199], v[116:119]\n" \
B_"v50, s[8:11], s64 offen offset:3072 sc1 lds\n" \
M_"v[116:119], a[144:147], a[192:195], v[116:119]\n" \
"s_xor_b32 m0, s65, 0xa000\n" \
C_"v89, v90, v91\n" \
M_"v[124:127], a[144:147], a[208:211], v[44:47]\n" \
"v_exp_f32_e64 v92, v92\n" \
M_"v[124:127], a[148:151], a[212:215], v[124:127]\n" \
"v_exp_f32_e64 v93, v93\n" \
M_"v[124:127], a[152:155], a[216:219], v[124:127]\n" \
"v_exp_f32_e64 v94, v94\n" \
M_"v[124:127], a[156:159], a[220:223], v[124:127]\n" \
"v_exp_f32_e64 v95, v95\n" \
M_"v[120:123], a[140:143], a[220:223], v[44:47]\n" \
C_"v90, v92, v93\n" \
C_"v91, v94, v95\n" \
M_"v[120:123], a[136:139], a[216:219], v[120:123]\n" \
R_"a[144:147], v48 offset:4096\n" \
R_"a[148:151], v48 offset:5120\n" \
M_"v[120:123], a[132:135], a[212:215], v[120:123]\n" \
B_"v51, s[8:11], s64 offen offset:0 sc1 lds\n" \
M_"v[120:123], a[128:131], a[208:211], v[120:123]\n" \
R_"a[128:131], v48 offset:0\n" \
"s_waitcnt lgkmcnt(3)\n" \
M_"v[128:131], a[160:163], v[68:71], v[128:131]\n" \
R_"a[132:135], v48 offset:1024\n" \
R_"a[136:139], v48 offset:2048\n" \
M_"v[160:163], a[160:163], v[72:75], v[160:163]\n" \
R_"a[152:155], v48 offset:6144\n" \
R_"a[140:143], v48 offset:3072\n" \
M_"v[132:135], a[164:167], v[68:71], v[132:135]\n" \
E_"v102, v102\n" \
M_"v[164:167], a[164:167], v[72:75], v[164:167]\n" \
"v_exp_f32_e64 v103, v103\n" \
M_"v[136:139], a[168:171], v[68:71], v[136:139]\n" \
R_"a[156:159], v48 offset:7168\n" \
C_"v103, v102, v103\n" \
M_"v[168:171], a[168:171], v[72:75], v[168:171]\n" \
B_"v51, s[8:11], s64 offen offset:1024 sc1 lds\n" \
M_"v[140:143], a[172:175], v[68:71], v[140:143]\n" \
"v_exp_f32_e64 v100, v100\n" \
M_"v[172:175], a[172:175], v[72:75], v[172:175]\n" \
"v_exp_f32_e64 v101, v101\n" \
M_"v[144:147], a[176:179], v[68:71], v[144:147]\n" \
"v_exp_f32_e64 v98, v98\n" \
M_"v[176:179], a[176:179], v[72:75], v[176:179]\n" \
"v_exp_f32_e64 v99, v99\n" \
M_"v[148:151], a[180:183], v[68:71], v[148:151]\n" \
"v_exp_f32_e64 v96, v96\n" \
M_"v[180:183], a[180:183], v[72:75], v[180:183]\n" \
"v_exp_f32_e64 v97, v97\n" \
M_"v[152:155], a[184:187], v[68:71], v[152:155]\n" \
"v_exp_f32_e64 v104, v104\n" \
M_"v[184:187], a[184:187], v[72:75], v[184:187]\n" \
B_"v51, s[8:11], s64 offen offset:2048 sc1 lds\n" \
M_"v[156:159], a[188:191], v[68:71], v[156:159]\n" \
"v_exp_f32_e64 v105, v105\n" \
M_"v[188:191], a[188:191], v[72:75], v[188:191]\n" \
C_"v102, v100, v101\n" \
C_"v101, v98, v99\n" \
"v_smfmac_f32_16x16x64_bf16 a[96:99], a[104:107], v[68:75], v21\n" \
C_"v100, v96, v97\n" \
C_"v104, v104, v105\n" \
M_"v[220:223], a[188:191], v[84:87], v[220:223]\n" \
"v_exp_f32_e64 v106, v106\n" \
M_"v[252:255], a[188:191], v[88:91], v[252:255]\n" \
"v_exp_f32_e64 v107, v107\n" \
M_"v[216:219], a[184:187], v[84:87], v[216:219]\n" \
"v_exp_f32_e64 v108, v108\n" \
M_"v[248:251], a[184:187], v[88:91], v[248:251]\n" \
B_"v51, s[8:11], s64 offen offset:3072 sc1 lds\n" \
M_"v[212:215], a[180:183], v[84:87], v[212:215]\n" \
"v_exp_f32_e64 v109, v109\n" \
M_"v[244:247], a[180:183], v[88:91], v[244:247]\n" \
"v_exp_f32_e64 v110, v110\n" \
M_"v[208:211], a[176:179], v[84:87], v[208:211]\n" \
"v_exp_f32_e64 v111, v111\n" \
M_"v[240:243], a[176:179], v[88:91], v[240:243]\n" \
"v_exp_f32_e64 v118, v118\n" \
M_"v[204:207], a[172:175], v[84:87], v[204:207]\n" \
"v_exp_f32_e64 v119, v119\n" \
M_"v[236:239], a[172:175], v[88:91], v[236:239]\n" \
C_"v105, v106, v107\n" \
C_"v106, v108, v109\n" \
M_"v[200:203], a[168:171], v[84:87], v[200:203]\n" \
C_"v107, v110, v111\n" \
C_"v119, v118, v119\n" \
M_"v[232:235], a[168:171], v[88:91], v[232:235]\n" \
"v_exp_f32_e64 v116, v116\n" \
M_"v[196:199], a[164:167], v[84:87], v[196:199]\n" \
"v_exp_f32_e64 v117, v117\n" \
M_"v[228:231], a[164:167], v[88:91], v[228:231]\n" \
"v_exp_f32_e64 v114, v114\n" \
M_"v[192:195], a[160:163], v[84:87], v[192:195]\n" \
"v_exp_f32_e64 v115, v115\n" \
M_"v[224:227], a[160:163], v[88:91], v[224:227]\n" \
C_"v118, v116, v117\n" \
"s_waitcnt vmcnt(8) lgkmcnt(0)\n" \
"v_smfmac_f32_16x16x64_bf16 a[100:103], a[104:107], v[84:91], v21\n" \
"s_barrier\n" \
M_"v[64:67], a[128:131], a[64:67], v[32:35]\n" \
"s_xor_b32 m0, s65, 0x18000\n" \
R_"a[160:163], v49 offset:8192\n" \
M_"v[64:67], a[132:135], a[68:71], v[64:67]\n" \
B_"v52, s[8:11], s64 offen offset:0 sc1 lds\n" \
M_"v[64:67], a[136:139], a[72:75], v[64:67]\n" \
R_"a[164:167], v49 offset:9216\n" \
R_"a[168:171], v49 offset:10240\n" \
M_"v[64:67], a[140:143], a[76:79], v[64:67]\n" \
R_"a[172:175], v49 offset:11264\n" \
R_"a[176:179], v49 offset:12288\n" \
M_"v[68:71], a[156:159], a[76:79], v[32:35]\n" \
R_"a[180:183], v49 offset:13312\n" \
R_"a[184:187], v49 offset:14336\n" \
M_"v[68:71], a[152:155], a[72:75], v[68:71]\n" \
R_"a[188:191], v49 offset:15360\n" \
C_"v117, v114, v115\n" \
M_"v[68:71], a[148:151], a[68:71], v[68:71]\n" \
"v_exp_f32_e64 v112, v112\n" \
M_"v[68:71], a[144:147], a[64:67], v[68:71]\n" \
B_"v52, s[8:11], s64 offen offset:1024 sc1 lds\n" \
M_"v[76:79], a[144:147], a[80:83], v[36:39]\n" \
"v_exp_f32_e64 v113, v113\n" \
M_"v[76:79], a[148:151], a[84:87], v[76:79]\n" \
C_"v116, v112, v113\n" \
"v_add_u32_e32 v49, 0x10000, v48\n" \
M_"v[76:79], a[152:155], a[88:91], v[76:79]\n" \
"v_exp_f32_e64 v120, v120\n" \
M_"v[76:79], a[156:159], a[92:95], v[76:79]\n" \
"v_exp_f32_e64 v121, v121\n" \
M_"v[72:75], a[140:143], a[92:95], v[36:39]\n" \
"v_exp_f32_e64 v122, v122\n" \
M_"v[72:75], a[136:139], a[88:91], v[72:75]\n" \
"v_exp_f32_e64 v123, v123\n" \
M_"v[72:75], a[132:135], a[84:87], v[72:75]\n" \
"v_exp_f32_e64 v124, v124\n" \
M_"v[72:75], a[128:131], a[80:83], v[72:75]\n" \
B_"v52, s[8:11], s64 offen offset:2048 sc1 lds\n" \
M_"v[80:83], a[128:131], a[192:195], v[40:43]\n" \
"v_exp_f32_e64 v125, v125\n" \
M_"v[80:83], a[132:135], a[196:199], v[80:83]\n" \
"v_exp_f32_e64 v126, v126\n" \
M_"v[80:83], a[136:139], a[200:203], v[80:83]\n" \
"v_exp_f32_e64 v127, v127\n" \
M_"v[80:83], a[140:143], a[204:207], v[80:83]\n" \
"v_exp_f32_e64 v70, v70\n" \
M_"v[84:87], a[156:159], a[204:207], v[40:43]\n" \
"v_exp_f32_e64 v71, v71\n" \
M_"v[84:87], a[152:155], a[200:203], v[84:87]\n" \
C_"v120, v120, v121\n" \
C_"v121, v122, v123\n" \
M_"v[84:87], a[148:151], a[196:199], v[84:87]\n" \
B_"v52, s[8:11], s64 offen offset:3072 sc1 lds\n" \
M_"v[84:87], a[144:147], a[192:195], v[84:87]\n" \
"s_mov_b32 m0, s65\n" \
"s_nop 0\n" \
C_"v122, v124, v125\n" \
M_"v[92:95], a[144:147], a[208:211], v[44:47]\n" \
C_"v123, v126, v127\n" \
R_"a[144:147], v48 offset:12288\n" \
M_"v[92:95], a[148:151], a[212:215], v[92:95]\n" \
R_"a[148:151], v48 offset:13312\n" \
C_"v71, v70, v71\n" \
M_"v[92:95], a[152:155], a[216:219], v[92:95]\n" \
R_"a[152:155], v48 offset:14336\n" \
"s_xor_b32 s65, s65, 0x8000\n" \
M_"v[92:95], a[156:159], a[220:223], v[92:95]\n" \
"v_exp_f32_e64 v72, v72\n" \
M_"v[88:91], a[140:143], a[220:223], v[44:47]\n" \
R_"a[140:143], v48 offset:11264\n" \
R_"a[156:159], v48 offset:15360\n" \
M_"v[88:91], a[136:139], a[216:219], v[88:91]\n" \
"v_exp_f32_e64 v73, v73\n" \
M_"v[88:91], a[132:135], a[212:215], v[88:91]\n" \
".fill (L_ab + 8192 - .) / 4, 4, 0xBF800000\n" \
"L_ap2:\n" \
"s_cbranch_execz L_ap0\n" \
B_"v53, s[8:11], s64 offen offset:0 sc1 lds\n" \
M_"v[88:91], a[128:131], a[208:211], v[88:91]\n" \
R_"a[128:131], v48 offset:8192\n" \
"s_waitcnt lgkmcnt(6)\n" \
M_"v[128:131], a[160:163], v[100:103], v[128:131]\n" \
R_"a[132:135], v48 offset:9216\n" \
R_"a[136:139], v48 offset:10240\n" \
M_"v[160:163], a[160:163], v[104:107], v[160:163]\n" \
"v_exp_f32_e64 v68, v68\n" \
M_"v[132:135], a[164:167], v[100:103], v[132:135]\n" \
"v_exp_f32_e64 v69, v69\n" \
M_"v[164:167], a[164:167], v[104:107], v[164:167]\n" \
"v_exp_f32_e64 v74, v74\n" \
M_"v[136:139], a[168:171], v[100:103], v[136:139]\n" \
"v_exp_f32_e64 v75, v75\n" \
M_"v[168:171], a[168:171], v[104:107], v[168:171]\n" \
B_"v53, s[8:11], s64 offen offset:1024 sc1 lds\n" \
M_"v[140:143], a[172:175], v[100:103], v[140:143]\n" \
"v_exp_f32_e64 v66, v66\n" \
M_"v[172:175], a[172:175], v[104:107], v[172:175]\n" \
"v_exp_f32_e64 v67, v67\n" \
M_"v[144:147], a[176:179], v[100:103], v[144:147]\n" \
"v_exp_f32_e64 v64, v64\n" \
M_"v[176:179], a[176:179], v[104:107], v[176:179]\n" \
"v_exp_f32_e64 v65, v65\n" \
M_"v[148:151], a[180:183], v[100:103], v[148:151]\n" \
C_"v72, v72, v73\n" \
C_"v70, v68, v69\n" \
M_"v[180:183], a[180:183], v[104:107], v[180:183]\n" \
"v_exp_f32_e64 v76, v76\n" \
M_"v[152:155], a[184:187], v[100:103], v[152:155]\n" \
"v_exp_f32_e64 v77, v77\n" \
M_"v[184:187], a[184:187], v[104:107], v[184:187]\n" \
B_"v53, s[8:11], s64 offen offset:2048 sc1 lds\n" \
M_"v[156:159], a[188:191], v[100:103], v[156:159]\n" \
"v_exp_f32_e64 v78, v78\n" \
M_"v[188:191], a[188:191], v[104:107], v[188:191]\n" \
"v_exp_f32_e64 v79, v79\n" \
"v_smfmac_f32_16x16x64_bf16 a[96:99], a[104:107], v[100:107], v21\n" \
C_"v73, v74, v75\n" \
C_"v69, v66, v67\n" \
M_"v[220:223], a[188:191], v[116:119], v[220:223]\n" \
C_"v68, v64, v65\n" \
C_"v74, v76, v77\n" \
M_"v[252:255], a[188:191], v[120:123], v[252:255]\n" \
C_"v75, v78, v79\n" \
"v_xor_b32_e32 v48, 0x8000, v48\n" \
M_"v[216:219], a[184:187], v[116:119], v[216:219]\n" \
M_"v[248:251], a[184:187], v[120:123], v[248:251]\n" \
B_"v53, s[8:11], s64 offen offset:3072 sc1 lds\n" \
M_"v[212:215], a[180:183], v[116:119], v[212:215]\n" \
"s_add_u32 s64, s64, 0x10000\n" \
"s_sub_u32 s66, s66, 1\n" \
"s_nop 0\n" \
M_"v[244:247], a[180:183], v[120:123], v[244:247]\n" \
M_"v[208:211], a[176:179], v[116:119], v[208:211]\n" \
M_"v[240:243], a[176:179], v[120:123], v[240:243]\n" \
M_"v[204:207], a[172:175], v[116:119], v[204:207]\n" \
M_"v[236:239], a[172:175], v[120:123], v[236:239]\n" \
M_"v[200:203], a[168:171], v[116:119], v[200:203]\n" \
M_"v[232:235], a[168:171], v[120:123], v[232:235]\n" \
M_"v[196:199], a[164:167], v[116:119], v[196:199]\n" \
M_"v[228:231], a[164:167], v[120:123], v[228:231]\n" \
M_"v[192:195], a[160:163], v[116:119], v[192:195]\n" \
M_"v[224:227], a[160:163], v[120:123], v[224:227]\n" \
"s_cbranch_scc0 L_tL_2_%=\n" \
"s_waitcnt vmcnt(8) lgkmcnt(0)\n" \
"v_smfmac_f32_16x16x64_bf16 a[100:103], a[104:107], v[116:123], v21\n" \
"s_barrier\n" \
"L_la_%=:\n" \
M_"v[96:99], a[128:131], a[64:67], v[32:35]\n" \
"v_readlane_b32 s96, v11, 1\n" \
"v_lshrrev_b32_e32 v16, 1, v20\n" \
M_"v[96:99], a[132:135], a[68:71], v[96:99]\n" \
"s_xor_b32 m0, s65, 0x12000\n" \
R_"a[160:163], v49 offset:0\n" \
M_"v[96:99], a[136:139], a[72:75], v[96:99]\n" \
"buffer_load_dwordx2 v[26:27], v16, s[56:59], s96 offen sc1\n" \
M_"v[96:99], a[140:143], a[76:79], v[96:99]\n" \
B_"v50, s[8:11], s64 offen offset:0 sc1 lds\n" \
M_"v[100:103], a[156:159], a[76:79], v[32:35]\n" \
R_"a[164:167], v49 offset:1024\n" \
R_"a[168:171], v49 offset:2048\n" \
M_"v[100:103], a[152:155], a[72:75], v[100:103]\n" \
R_"a[172:175], v49 offset:3072\n" \
R_"a[176:179], v49 offset:4096\n" \
M_"v[100:103], a[148:151], a[68:71], v[100:103]\n" \
R_"a[180:183], v49 offset:5120\n" \
R_"a[184:187], v49 offset:6144\n" \
M_"v[100:103], a[144:147], a[64:67], v[100:103]\n" \
R_"a[188:191], v49 offset:7168\n" \
"s_mov_b32 s63, s62\n" \
M_"v[108:111], a[144:147], a[80:83], v[36:39]\n" \
E_"v86, v86\n" \
M_"v[108:111], a[148:151], a[84:87], v[108:111]\n" \
"v_exp_f32_e64 v87, v87\n" \
M_"v[108:111], a[152:155], a[88:91], v[108:111]\n" \
B_"v50, s[8:11], s64 offen offset:1024 sc1 lds\n" \
M_"v[108:111], a[156:159], a[92:95], v[108:111]\n" \
"v_exp_f32_e64 v84, v84\n" \
M_"v[104:107], a[140:143], a[92:95], v[36:39]\n" \
"v_exp_f32_e64 v85, v85\n" \
M_"v[104:107], a[136:139], a[88:91], v[104:107]\n" \
C_"v87, v86, v87\n" \
C_"v86, v84, v85\n" \
M_"v[104:107], a[132:135], a[84:87], v[104:107]\n" \
"v_exp_f32_e64 v82, v82\n" \
M_"v[104:107], a[128:131], a[80:83], v[104:107]\n" \
"v_exp_f32_e64 v83, v83\n" \
M_"v[112:115], a[128:131], a[192:195], v[40:43]\n" \
"v_exp_f32_e64 v80, v80\n" \
M_"v[112:115], a[132:135], a[196:199], v[112:115]\n" \
"v_exp_f32_e64 v81, v81\n" \
M_"v[112:115], a[136:139], a[200:203], v[112:115]\n" \
B_"v50, s[8:11], s64 offen offset:2048 sc1 lds\n" \
M_"v[112:115], a[140:143], a[204:207], v[112:115]\n" \
C_"v85, v82, v83\n" \
C_"v84, v80, v81\n" \
M_"v[116:119], a[156:159], a[204:207], v[40:43]\n" \
"v_exp_f32_e64 v88, v88\n" \
M_"v[116:119], a[152:155], a[200:203], v[116:119]\n" \
"v_exp_f32_e64 v89, v89\n" \
M_"v[116:119], a[148:151], a[196:199], v[116:119]\n" \
"v_exp_f32_e64 v90, v90\n" \
M_"v[116:119], a[144:147], a[192:195], v[116:119]\n" \
"v_exp_f32_e64 v91, v91\n" \
M_"v[124:127], a[144:147], a[208:211], v[44:47]\n" \
C_"v88, v88, v89\n" \
C_"v89, v90, v91\n" \
M_"v[124:127], a[148:151], a[212:215], v[124:127]\n" \
B_"v50, s[8:11], s64 offen offset:3072 sc1 lds\n" \
M_"v[124:127], a[152:155], a[216:219], v[124:127]\n" \
"v_exp_f32_e64 v92, v92\n" \
M_"v[124:127], a[156:159], a[220:223], v[124:127]\n" \
"v_exp_f32_e64 v93, v93\n" \
M_"v[120:123], a[140:143], a[220:223], v[44:47]\n" \
"v_exp_f32_e64 v94, v94\n" \
M_"v[120:123], a[136:139], a[216:219], v[120:123]\n" \
"v_exp_f32_e64 v95, v95\n" \
M_"v[120:123], a[132:135], a[212:215], v[120:123]\n" \
C_"v90, v92, v93\n" \
C_"v91, v94, v95\n" \
M_"v[120:123], a[128:131], a[208:211], v[120:123]\n" \
"v_readlane_b32 s97, v11, 39\n" \
"s_waitcnt lgkmcnt(0)\n" \
"s_nop 0\n" \
M_"v[128:131], a[160:163], v[68:71], v[128:131]\n" \
R_"a[128:131], v48 offset:0\n" \
R_"a[144:147], v48 offset:4096\n" \
M_"v[160:163], a[160:163], v[72:75], v[160:163]\n" \
R_"a[132:135], v48 offset:1024\n" \
R_"a[148:151], v48 offset:5120\n" \
M_"v[132:135], a[164:167], v[68:71], v[132:135]\n" \
R_"a[136:139], v48 offset:2048\n" \
R_"a[152:155], v48 offset:6144\n" \
M_"v[164:167], a[164:167], v[72:75], v[164:167]\n" \
R_"a[140:143], v48 offset:3072\n" \
R_"a[156:159], v48 offset:7168\n" \
M_"v[136:139], a[168:171], v[68:71], v[136:139]\n" \
"v_add_u32_e32 v17, 0x49000, v20\n" \
"v_readlane_b32 s98, v11, 40\n" \
M_"v[168:171], a[168:171], v[72:75], v[168:171]\n" \
"v_readlane_b32 s93, v11, 23\n" \
"v_readlane_b32 s84, v11, 15\n" \
M_"v[140:143], a[172:175], v[68:71], v[140:143]\n" \
"v_readlane_b32 s85, v11, 16\n" \
"v_readlane_b32 s86, v11, 17\n" \
M_"v[172:175], a[172:175], v[72:75], v[172:175]\n" \
"s_mov_b32 s87, 0x20000\n" \
"v_readlane_b32 s92, v11, 18\n" \
M_"v[144:147], a[176:179], v[68:71], v[144:147]\n" \
"v_exp_f32_e64 v102, v102\n" \
M_"v[176:179], a[176:179], v[72:75], v[176:179]\n" \
"v_exp_f32_e64 v103, v103\n" \
M_"v[148:151], a[180:183], v[68:71], v[148:151]\n" \
"v_exp_f32_e64 v100, v100\n" \
M_"v[180:183], a[180:183], v[72:75], v[180:183]\n" \
"v_exp_f32_e64 v101, v101\n" \
M_"v[152:155], a[184:187], v[68:71], v[152:155]\n" \
C_"v103, v102, v103\n" \
C_"v102, v100, v101\n" \
M_"v[184:187], a[184:187], v[72:75], v[184:187]\n" \
"v_exp_f32_e64 v98, v98\n" \
M_"v[156:159], a[188:191], v[68:71], v[156:159]\n" \
"v_exp_f32_e64 v99, v99\n" \
M_"v[188:191], a[188:191], v[72:75], v[188:191]\n" \
"s_waitcnt vmcnt(4)\n" \
"v_cmp_ne_u64_e32 vcc, s[62:63], v[26:27]\n" \
"v_smfmac_f32_16x16x64_bf16 a[96:99], a[104:107], v[68:75], v21\n" \
"v_exp_f32_e64 v96, v96\n" \
M_"v[220:223], a[188:191], v[84:87], v[220:223]\n" \
"v_exp_f32_e64 v97, v97\n" \
M_"v[252:255], a[188:191], v[88:91], v[252:255]\n" \
"s_cbranch_vccnz L_sol_%=\n" \
"L_rol_%=:\n" \
C_"v101, v98, v99\n" \
B_"a[124:127], v20, s[56:59], s97 offen offset:0 sc1\n" \
M_"v[216:219], a[184:187], v[84:87], v[216:219]\n" \
C_"v100, v96, v97\n" \
"v_readlane_b32 s91, v11, 24\n" \
M_"v[248:251], a[184:187], v[88:91], v[248:251]\n" \
E_"v104, v104\n" \
M_"v[212:215], a[180:183], v[84:87], v[212:215]\n" \
"v_exp_f32_e64 v105, v105\n" \
M_"v[244:247], a[180:183], v[88:91], v[244:247]\n" \
"v_exp_f32_e64 v106, v106\n" \
M_"v[208:211], a[176:179], v[84:87], v[208:211]\n" \
"v_exp_f32_e64 v107, v107\n" \
M_"v[240:243], a[176:179], v[88:91], v[240:243]\n" \
B_"a[228:231], v17, s[56:59], s97 offen offset:0 sc1\n" \
M_"v[204:207], a[172:175], v[84:87], v[204:207]\n" \
C_"v104, v104, v105\n" \
C_"v105, v106, v107\n" \
M_"v[236:239], a[172:175], v[88:91], v[236:239]\n" \
"v_exp_f32_e64 v108, v108\n" \
M_"v[200:203], a[168:171], v[84:87], v[200:203]\n" \
"v_exp_f32_e64 v109, v109\n" \
M_"v[232:235], a[168:171], v[88:91], v[232:235]\n" \
"v_exp_f32_e64 v110, v110\n" \
M_"v[196:199], a[164:167], v[84:87], v[196:199]\n" \
"v_exp_f32_e64 v111, v111\n" \
M_"v[228:231], a[164:167], v[88:91], v[228:231]\n" \
C_"v106, v108, v109\n" \
C_"v107, v110, v111\n" \
M_"v[192:195], a[160:163], v[84:87], v[192:195]\n" \
"v_exp_f32_e64 v118, v118\n" \
M_"v[224:227], a[160:163], v[88:91], v[224:227]\n" \
"s_xor_b32 s65, s65, 0x8000\n" \
"s_waitcnt vmcnt(7) lgkmcnt(0)\n" \
"v_smfmac_f32_16x16x64_bf16 a[100:103], a[104:107], v[84:91], v21\n" \
"s_barrier\n" \
M_"v[64:67], a[128:131], a[64:67], v[32:35]\n" \
B_"a[236:239], v20, s[56:59], s98 offen offset:0 sc1\n" \
M_"v[64:67], a[132:135], a[68:71], v[64:67]\n" \
"s_add_u32 m0, s93, 0x0\n" \
"s_nop 0\n" \
R_"a[160:163], v49 offset:8192\n" \
M_"v[64:67], a[136:139], a[72:75], v[64:67]\n" \
B_"v0, s[84:87], s92 offen offset:0 lds\n" \
M_"v[64:67], a[140:143], a[76:79], v[64:67]\n" \
R_"a[164:167], v49 offset:9216\n" \
R_"a[168:171], v49 offset:10240\n" \
M_"v[68:71], a[156:159], a[76:79], v[32:35]\n" \
R_"a[172:175], v49 offset:11264\n" \
R_"a[176:179], v49 offset:12288\n" \
M_"v[68:71], a[152:155], a[72:75], v[68:71]\n" \
R_"a[180:183], v49 offset:13312\n" \
R_"a[184:187], v49 offset:14336\n" \
M_"v[68:71], a[148:151], a[68:71], v[68:71]\n" \
B_"v1, s[84:87], s92 offen offset:1024 lds\n" \
M_"v[68:71], a[144:147], a[64:67], v[68:71]\n" \
B_"a[244:247], v17, s[56:59], s98 offen offset:0 sc1\n" \
M_"v[76:79], a[144:147], a[80:83], v[36:39]\n" \
R_"a[188:191], v49 offset:15360\n" \
"v_add_u32_e32 v49, 0x10000, v48\n" \
M_"v[76:79], a[148:151], a[84:87], v[76:79]\n" \
"v_exp_f32_e64 v119, v119\n" \
M_"v[76:79], a[152:155], a[88:91], v[76:79]\n" \
"v_exp_f32_e64 v116, v116\n" \
M_"v[76:79], a[156:159], a[92:95], v[76:79]\n" \
"v_exp_f32_e64 v117, v117\n" \
M_"v[72:75], a[140:143], a[92:95], v[36:39]\n" \
C_"v119, v118, v119\n" \
C_"v118, v116, v117\n" \
M_"v[72:75], a[136:139], a[88:91], v[72:75]\n" \
B_"v2, s[84:87], s92 offen offset:2048 lds\n" \
M_"v[72:75], a[132:135], a[84:87], v[72:75]\n" \
"v_exp_f32_e64 v114, v114\n" \
M_"v[72:75], a[128:131], a[80:83], v[72:75]\n" \
"v_exp_f32_e64 v115, v115\n" \
M_"v[80:83], a[128:131], a[192:195], v[40:43]\n" \
B_"a[224:227], v20, s[56:59], s97 offen offset:1024 sc1\n" \
M_"v[80:83], a[132:135], a[196:199], v[80:83]\n" \
"v_exp_f32_e64 v112, v112\n" \
M_"v[80:83], a[136:139], a[200:203], v[80:83]\n" \
"v_exp_f32_e64 v113, v113\n" \
M_"v[80:83], a[140:143], a[204:207], v[80:83]\n" \
C_"v117, v114, v115\n" \
C_"v116, v112, v113\n" \
M_"v[84:87], a[156:159], a[204:207], v[40:43]\n" \
B_"v3, s[84:87], s92 offen offset:3072 lds\n" \
M_"v[84:87], a[152:155], a[200:203], v[84:87]\n" \
"s_add_u32 m0, s91, 0x1000\n" \
"s_add_u32 s64, s64, 0x10000\n" \
M_"v[84:87], a[148:151], a[196:199], v[84:87]\n" \
"v_exp_f32_e64 v120, v120\n" \
M_"v[84:87], a[144:147], a[192:195], v[84:87]\n" \
"v_exp_f32_e64 v121, v121\n" \
M_"v[92:95], a[144:147], a[208:211], v[44:47]\n" \
"v_exp_f32_e64 v122, v122\n" \
M_"v[92:95], a[148:151], a[212:215], v[92:95]\n" \
B_"a[232:235], v17, s[56:59], s97 offen offset:1024 sc1\n" \
M_"v[92:95], a[152:155], a[216:219], v[92:95]\n" \
B_"v4, s[84:87], s92 offen offset:0 lds\n" \
M_"v[92:95], a[156:159], a[220:223], v[92:95]\n" \
"v_exp_f32_e64 v123, v123\n" \
M_"v[88:91], a[140:143], a[220:223], v[44:47]\n" \
C_"v120, v120, v121\n" \
C_"v121, v122, v123\n" \
M_"v[88:91], a[136:139], a[216:219], v[88:91]\n" \
"v_exp_f32_e64 v124, v124\n" \
M_"v[88:91], a[132:135], a[212:215], v[88:91]\n" \
"v_exp_f32_e64 v125, v125\n" \
M_"v[88:91], a[128:131], a[208:211], v[88:91]\n" \
C_"v122, v124, v125\n" \
"s_waitcnt lgkmcnt(0)\n" \
M_"v[128:131], a[160:163], v[100:103], v[128:131]\n" \
E_"v126, v126\n" \
M_"v[160:163], a[160:163], v[104:107], v[160:163]\n" \
B_"v5, s[84:87], s92 offen offset:1024 lds\n" \
M_"v[132:135], a[164:167], v[100:103], v[132:135]\n" \
B_"a[240:243], v20, s[56:59], s98 offen offset:1024 sc1\n" \
M_"v[164:167], a[164:167], v[104:107], v[164:167]\n" \
"v_exp_f32_e64 v127, v127\n" \
M_"v[136:139], a[168:171], v[100:103], v[136:139]\n" \
C_"v123, v126, v127\n" \
R_"a[128:131], v48 offset:8192\n" \
M_"v[168:171], a[168:171], v[104:107], v[168:171]\n" \
R_"a[144:147], v48 offset:12288\n" \
R_"a[132:135], v48 offset:9216\n" \
M_"v[140:143], a[172:175], v[100:103], v[140:143]\n" \
R_"a[148:151], v48 offset:13312\n" \
R_"a[136:139], v48 offset:10240\n" \
M_"v[172:175], a[172:175], v[104:107], v[172:175]\n" \
R_"a[152:155], v48 offset:14336\n" \
R_"a[140:143], v48 offset:11264\n" \
M_"v[144:147], a[176:179], v[100:103], v[144:147]\n" \
B_"v6, s[84:87], s92 offen offset:2048 lds\n" \
M_"v[176:179], a[176:179], v[104:107], v[176:179]\n" \
"v_exp_f32_e64 v70, v70\n" \
M_"v[148:151], a[180:183], v[100:103], v[148:151]\n" \
"v_exp_f32_e64 v71, v71\n" \
M_"v[180:183], a[180:183], v[104:107], v[180:183]\n" \
B_"a[248:251], v17, s[56:59], s98 offen offset:1024 sc1\n" \
M_"v[152:155], a[184:187], v[100:103], v[152:155]\n" \
R_"a[156:159], v48 offset:15360\n" \
C_"v71, v70, v71\n" \
M_"v[184:187], a[184:187], v[104:107], v[184:187]\n" \
"v_exp_f32_e64 v72, v72\n" \
M_"v[156:159], a[188:191], v[100:103], v[156:159]\n" \
B_"v7, s[84:87], s92 offen offset:3072 lds\n" \
M_"v[188:191], a[188:191], v[104:107], v[188:191]\n" \
"s_add_u32 m0, s91, 0x2000\n" \
"v_xor_b32_e32 v48, 0x8000, v48\n" \
"v_smfmac_f32_16x16x64_bf16 a[96:99], a[104:107], v[100:107], v21\n" \
"v_exp_f32_e64 v73, v73\n" \
M_"v[220:223], a[188:191], v[116:119], v[220:223]\n" \
"v_exp_f32_e64 v68, v68\n" \
M_"v[252:255], a[188:191], v[120:123], v[252:255]\n" \
"v_exp_f32_e64 v69, v69\n" \
M_"v[216:219], a[184:187], v[116:119], v[216:219]\n" \
"v_exp_f32_e64 v74, v74\n" \
M_"v[248:251], a[184:187], v[120:123], v[248:251]\n" \
"v_exp_f32_e64 v75, v75\n" \
M_"v[212:215], a[180:183], v[116:119], v[212:215]\n" \
B_"v8, s[84:87], s92 offen offset:0 lds\n" \
M_"v[244:247], a[180:183], v[120:123], v[244:247]\n" \
"v_exp_f32_e64 v66, v66\n" \
M_"v[208:211], a[176:179], v[116:119], v[208:211]\n" \
"v_exp_f32_e64 v67, v67\n" \
M_"v[240:243], a[176:179], v[120:123], v[240:243]\n" \
"v_exp_f32_e64 v64, v64\n" \
M_"v[204:207], a[172:175], v[116:119], v[204:207]\n" \
"v_exp_f32_e64 v65, v65\n" \
M_"v[236:239], a[172:175], v[120:123], v[236:239]\n" \
C_"v72, v72, v73\n" \
C_"v70, v68, v69\n" \
M_"v[200:203], a[168:171], v[116:119], v[200:203]\n" \
"v_exp_f32_e64 v76, v76\n" \
M_"v[232:235], a[168:171], v[120:123], v[232:235]\n" \
"v_exp_f32_e64 v77, v77\n" \
M_"v[196:199], a[164:167], v[116:119], v[196:199]\n" \
"v_exp_f32_e64 v78, v78\n" \
M_"v[228:231], a[164:167], v[120:123], v[228:231]\n" \
"v_exp_f32_e64 v79, v79\n" \
M_"v[192:195], a[160:163], v[116:119], v[192:195]\n" \
C_"v73, v74, v75\n" \
C_"v69, v66, v67\n" \
M_"v[224:227], a[160:163], v[120:123], v[224:227]\n" \
C_"v68, v64, v65\n" \
"s_waitcnt vmcnt(15) lgkmcnt(0)\n" \
"v_smfmac_f32_16x16x64_bf16 a[100:103], a[104:107], v[116:123], v21\n" \
"s_barrier\n" \
C_"v74, v76, v77\n" \
C_"v75, v78, v79\n" \
"s_cmp_lg_u32 s16, 0\n" \
"s_cbranch_scc1 L_f1_%=\n" \
R_"a[160:163], v49 offset:0\n" \
R_"a[164:167], v49 offset:1024\n" \
R_"a[168:171], v49 offset:2048\n" \
R_"a[172:175], v49 offset:3072\n" \
R_"a[176:179], v49 offset:4096\n" \
R_"a[180:183], v49 offset:5120\n" \
R_"a[184:187], v49 offset:6144\n" \
R_"a[188:191], v49 offset:7168\n" \
"v_add_u32_e32 v54, 0x49000, v20\n" \
B_"a[192:195], v20, s[56:59], s60 offen offset:0 sc1\n" \
"v_add_u32_e32 v50, s15, v20\n" \
"v_add_u32_e32 v51, 0x10000, v50\n" \
"ds_read_b32 v60, v18 offset:0\n" \
"ds_read_b32 v61, v18 offset:128\n" \
R_"a[0:3], v49 offset:8192\n" \
R_"a[4:7], v49 offset:9216\n" \
R_"a[8:11], v49 offset:10240\n" \
R_"a[12:15], v49 offset:11264\n" \
R_"a[16:19], v49 offset:12288\n" \
R_"a[20:23], v49 offset:13312\n" \
R_"a[24:27], v49 offset:14336\n" \
R_"a[28:31], v49 offset:15360\n" \
E_"v86, v86\n" \
B_"a[196:199], v20, s[56:59], s60 offen offset:1024 sc1\n" \
E_"v87, v87\n" \
E_"v84, v84\n" \
E_"v85, v85\n" \
C_"v87, v86, v87\n" \
E_"v82, v82\n" \
E_"v83, v83\n" \
E_"v80, v80\n" \
B_"a[200:203], v20, s[56:59], s60 offen offset:2048 sc1\n" \
"s_waitcnt lgkmcnt(15)\n" \
M_"v[128:131], a[160:163], v[68:71], v[128:131]\n" \
E_"v81, v81\n" \
M_"v[160:163], a[160:163], v[72:75], v[160:163]\n" \
C_"v86, v84, v85\n" \
C_"v85, v82, v83\n" \
M_"v[132:135], a[164:167], v[68:71], v[132:135]\n" \
C_"v84, v80, v81\n" \
"v_and_b32_e32 v84, v12, v84\n" \
M_"v[164:167], a[164:167], v[72:75], v[164:167]\n" \
B_"a[204:207], v20, s[56:59], s60 offen offset:3072 sc1\n" \
M_"v[136:139], a[168:171], v[68:71], v[136:139]\n" \
C_"v128, v128, v129\n" \
C_"v160, v160, v161\n" \
M_"v[168:171], a[168:171], v[72:75], v[168:171]\n" \
C_"v129, v130, v131\n" \
"s_waitcnt lgkmcnt(10)\n" \
M_"v[140:143], a[172:175], v[68:71], v[140:143]\n" \
C_"v161, v162, v163\n" \
C_"v130, v132, v133\n" \
M_"v[172:175], a[172:175], v[72:75], v[172:175]\n" \
C_"v131, v134, v135\n" \
C_"v162, v164, v165\n" \
M_"v[144:147], a[176:179], v[68:71], v[144:147]\n" \
C_"v163, v166, v167\n" \
C_"v132, v136, v137\n" \
M_"v[176:179], a[176:179], v[72:75], v[176:179]\n" \
C_"v133, v138, v139\n" \
C_"v134, v140, v141\n" \
M_"v[148:151], a[180:183], v[68:71], v[148:151]\n" \
C_"v135, v142, v143\n" \
C_"v164, v168, v169\n" \
M_"v[180:183], a[180:183], v[72:75], v[180:183]\n" \
C_"v165, v170, v171\n" \
C_"v166, v172, v173\n" \
M_"v[152:155], a[184:187], v[68:71], v[152:155]\n" \
C_"v167, v174, v175\n" \
C_"v136, v144, v145\n" \
M_"v[184:187], a[184:187], v[72:75], v[184:187]\n" \
C_"v137, v146, v147\n" \
C_"v138, v148, v149\n" \
M_"v[156:159], a[188:191], v[68:71], v[156:159]\n" \
C_"v139, v150, v151\n" \
C_"v168, v176, v177\n" \
M_"v[188:191], a[188:191], v[72:75], v[188:191]\n" \
C_"v140, v152, v153\n" \
C_"v141, v154, v155\n" \
"v_smfmac_f32_16x16x64_bf16 a[96:99], a[104:107], v[68:75], v21\n" \
C_"v169, v178, v179\n" \
C_"v170, v180, v181\n" \
M_"v[120:123], a[128:131], a[208:211], v[44:47]\n" \
C_"v142, v156, v157\n" \
C_"v143, v158, v159\n" \
M_"v[120:123], a[132:135], a[212:215], v[120:123]\n" \
C_"v171, v182, v183\n" \
C_"v172, v184, v185\n" \
C_"v173, v186, v187\n" \
C_"v174, v188, v189\n" \
C_"v175, v190, v191\n" \
"ds_write_b128 v50, v[128:131] offset:0\n" \
M_"v[120:123], a[136:139], a[216:219], v[120:123]\n" \
"v_and_b32_e32 v85, v13, v85\n" \
"v_and_b32_e32 v86, v14, v86\n" \
M_"v[120:123], a[140:143], a[220:223], v[120:123]\n" \
"ds_write_b128 v50, v[132:135] offset:1024\n" \
M_"v[124:127], a[156:159], a[220:223], v[44:47]\n" \
B_"a[220:223], v54, s[56:59], s60 offen offset:3072 sc1\n" \
M_"v[124:127], a[152:155], a[216:219], v[124:127]\n" \
"ds_write_b128 v50, v[136:139] offset:2048\n" \
M_"v[124:127], a[148:151], a[212:215], v[124:127]\n" \
"v_and_b32_e32 v87, v15, v87\n" \
"v_accvgpr_read_b32 v40, a96\n" \
M_"v[124:127], a[144:147], a[208:211], v[124:127]\n" \
"ds_write_b128 v50, v[140:143] offset:3072\n" \
M_"a[100:103], a[108:111], v[84:87], a[100:103]\n" \
B_"a[208:211], v54, s[56:59], s60 offen offset:0 sc1\n" \
M_"v[192:195], a[160:163], v[84:87], v[192:195]\n" \
"ds_write_b128 v51, v[160:163] offset:32768\n" \
M_"v[196:199], a[164:167], v[84:87], v[196:199]\n" \
"v_accvgpr_read_b32 v22, a97\n" \
"v_accvgpr_read_b32 v41, a98\n" \
"v_accvgpr_read_b32 v23, a99\n" \
M_"v[200:203], a[168:171], v[84:87], v[200:203]\n" \
"ds_write_b128 v51, v[164:167] offset:33792\n" \
B_"a[212:215], v54, s[56:59], s60 offen offset:1024 sc1\n" \
"v_add_f32_e32 v40, v40, v22\n" \
"v_add_f32_e32 v41, v41, v23\n" \
"ds_write_b64 v19, v[40:41]\n" \
M_"v[204:207], a[172:175], v[84:87], v[204:207]\n" \
"ds_write_b128 v51, v[168:171] offset:34816\n" \
M_"v[208:211], a[176:179], v[84:87], v[208:211]\n" \
"v_lshl_add_u32 v52, s15, 2, v20\n" \
"v_add_u32_e32 v52, 0x1a000, v52\n" \
M_"v[212:215], a[180:183], v[84:87], v[212:215]\n" \
B_"a[216:219], v54, s[56:59], s60 offen offset:2048 sc1\n" \
"ds_write_b128 v51, v[172:175] offset:35840\n" \
M_"v[216:219], a[184:187], v[84:87], v[216:219]\n" \
E_"v88, v88\n" \
M_"v[220:223], a[188:191], v[84:87], v[220:223]\n" \
E_"v89, v89\n" \
E_"v90, v90\n" \
E_"v91, v91\n" \
C_"v88, v88, v89\n" \
E_"v92, v92\n" \
E_"v93, v93\n" \
E_"v94, v94\n" \
E_"v95, v95\n" \
C_"v89, v90, v91\n" \
C_"v90, v92, v93\n" \
C_"v91, v94, v95\n" \
"v_xor_b32_e32 v63, 0x400, v19\n" \
E_"v120, v120\n" \
E_"v121, v121\n" \
E_"v122, v122\n" \
E_"v123, v123\n" \
C_"v120, v120, v121\n" \
E_"v124, v124\n" \
E_"v125, v125\n" \
E_"v126, v126\n" \
"s_waitcnt lgkmcnt(0)\n" \
"s_barrier\n" \
R_"v[144:147], v52 offset:0\n" \
R_"v[148:151], v52 offset:1024\n" \
R_"v[152:155], v52 offset:2048\n" \
".fill (L_ab + 12288 - .) / 4, 4, 0xBF800000\n" \
"L_ap3:\n" \
"s_cbranch_execz L_ap0\n" \
R_"v[156:159], v52 offset:3072\n" \
R_"v[176:179], v52 offset:4096\n" \
R_"v[180:183], v52 offset:5120\n" \
R_"v[184:187], v52 offset:6144\n" \
R_"v[188:191], v52 offset:7168\n" \
M_"a[100:103], a[116:119], v[88:91], a[100:103]\n" \
"ds_read_b64 v[42:43], v63\n" \
C_"v121, v122, v123\n" \
M_"v[224:227], a[160:163], v[88:91], v[224:227]\n" \
E_"v127, v127\n" \
M_"v[228:231], a[164:167], v[88:91], v[228:231]\n" \
C_"v122, v124, v125\n" \
C_"v123, v126, v127\n" \
M_"v[232:235], a[168:171], v[88:91], v[232:235]\n" \
"v_and_b32_e32 v120, v12, v120\n" \
"v_and_b32_e32 v121, v13, v121\n" \
M_"v[236:239], a[172:175], v[88:91], v[236:239]\n" \
"v_and_b32_e32 v122, v14, v122\n" \
"v_and_b32_e32 v123, v15, v123\n" \
M_"v[240:243], a[176:179], v[88:91], v[240:243]\n" \
"v_readlane_b32 s93, v11, 25\n" \
"v_readlane_b32 s92, v11, 30\n" \
M_"v[244:247], a[180:183], v[88:91], v[244:247]\n" \
"s_add_u32 m0, s93, 0x0\n" \
"v_mov_b32_e32 v130, v28\n" \
M_"v[248:251], a[184:187], v[88:91], v[248:251]\n" \
"v_mov_b32_e32 v131, v29\n" \
"v_mov_b32_e32 v132, 0\n" \
M_"v[252:255], a[188:191], v[88:91], v[252:255]\n" \
"v_mov_b32_e32 v133, 0\n" \
"v_mov_b32_e32 v128, 0\n" \
"v_mov_b32_e32 v129, 0\n" \
"s_waitcnt vmcnt(8) lgkmcnt(1)\n" \
"s_barrier\n" \
M_"a[100:103], a[116:119], v[120:123], a[100:103]\n" \
B_"v0, s[80:83], s92 offen offset:0 lds\n" \
M_"v[224:227], a[0:3], v[120:123], v[224:227]\n" \
M_"v[228:231], a[4:7], v[120:123], v[228:231]\n" \
M_"v[232:235], a[8:11], v[120:123], v[232:235]\n" \
M_"v[236:239], a[12:15], v[120:123], v[236:239]\n" \
B_"v1, s[80:83], s92 offen offset:1024 lds\n" \
M_"v[240:243], a[16:19], v[120:123], v[240:243]\n" \
M_"v[244:247], a[20:23], v[120:123], v[244:247]\n" \
"v_accvgpr_read_b32 v56, a100\n" \
"v_accvgpr_read_b32 v24, a101\n" \
M_"v[248:251], a[24:27], v[120:123], v[248:251]\n" \
"v_add_f32_e32 v56, v56, v24\n" \
"s_waitcnt lgkmcnt(0)\n" \
"v_add_f32_e32 v56, v56, v42\n" \
B_"v2, s[80:83], s92 offen offset:2048 lds\n" \
M_"v[252:255], a[28:31], v[120:123], v[252:255]\n" \
"v_max_f32_e32 v62, v62, v56\n" \
"v_accvgpr_read_b32 v57, a102\n" \
M_"v[192:195], v[130:133], v[144:147], v[192:195]\n" \
"v_rcp_f32_e32 v56, v56\n" \
M_"v[196:199], v[128:131], v[144:147], v[196:199]\n" \
"v_mul_f32_e32 v56, v56, v60\n" \
"v_accvgpr_read_b32 v25, a103\n" \
M_"v[200:203], v[130:133], v[148:151], v[200:203]\n" \
B_"v3, s[80:83], s92 offen offset:3072 lds\n" \
"s_add_u32 m0, s93, 0x1000\n" \
M_"v[204:207], v[128:131], v[148:151], v[204:207]\n" \
"v_mul_f32_e32 v192, v56, v192\n" \
"v_mul_f32_e32 v193, v56, v193\n" \
M_"v[208:211], v[130:133], v[152:155], v[208:211]\n" \
"v_mul_f32_e32 v194, v56, v194\n" \
"v_mul_f32_e32 v195, v56, v195\n" \
M_"v[212:215], v[128:131], v[152:155], v[212:215]\n" \
"v_mul_f32_e32 v196, v56, v196\n" \
"v_mul_f32_e32 v197, v56, v197\n" \
B_"v4, s[80:83], s92 offen offset:0 lds\n" \
M_"v[216:219], v[130:133], v[156:159], v[216:219]\n" \
"v_mul_f32_e32 v198, v56, v198\n" \
"v_mul_f32_e32 v199, v56, v199\n" \
M_"v[220:223], v[128:131], v[156:159], v[220:223]\n" \
C_"v64, v192, v193\n" \
C_"v65, v194, v195\n" \
M_"v[224:227], v[130:133], v[176:179], v[224:227]\n" \
C_"v66, v196, v197\n" \
C_"v67, v198, v199\n" \
M_"v[228:231], v[128:131], v[176:179], v[228:231]\n" \
B_"v5, s[80:83], s92 offen offset:1024 lds\n" \
M_"v[232:235], v[130:133], v[180:183], v[232:235]\n" \
M_"a[112:115], a[120:123], v[64:67], a[112:115]\n" \
"v_mul_f32_e32 v200, v56, v200\n" \
"v_mul_f32_e32 v201, v56, v201\n" \
M_"v[236:239], v[128:131], v[180:183], v[236:239]\n" \
"v_mul_f32_e32 v202, v56, v202\n" \
"v_mul_f32_e32 v203, v56, v203\n" \
M_"v[240:243], v[130:133], v[184:187], v[240:243]\n" \
B_"v6, s[80:83], s92 offen offset:2048 lds\n" \
"v_mul_f32_e32 v204, v56, v204\n" \
"v_mul_f32_e32 v205, v56, v205\n" \
"v_mul_f32_e32 v206, v56, v206\n" \
"v_mul_f32_e32 v207, v56, v207\n" \
"v_add_f32_e32 v57, v57, v25\n" \
C_"v68, v200, v201\n" \
C_"v69, v202, v203\n" \
C_"v70, v204, v205\n" \
C_"v71, v206, v207\n" \
"v_add_f32_e32 v57, v57, v43\n" \
"v_mul_f32_e32 v208, v56, v208\n" \
M_"a[112:115], a[120:123], v[68:71], a[112:115]\n" \
B_"v7, s[80:83], s92 offen offset:3072 lds\n" \
"s_add_u32 m0, s93, 0x2000\n" \
"v_mul_f32_e32 v209, v56, v209\n" \
"v_mul_f32_e32 v210, v56, v210\n" \
"v_mul_f32_e32 v211, v56, v211\n" \
"v_mul_f32_e32 v212, v56, v212\n" \
"v_mul_f32_e32 v213, v56, v213\n" \
"v_mul_f32_e32 v214, v56, v214\n" \
"v_mul_f32_e32 v215, v56, v215\n" \
"v_max_f32_e32 v62, v62, v57\n" \
C_"v72, v208, v209\n" \
C_"v73, v210, v211\n" \
C_"v74, v212, v213\n" \
C_"v75, v214, v215\n" \
B_"v8, s[80:83], s92 offen offset:0 lds\n" \
"v_rcp_f32_e32 v57, v57\n" \
M_"v[244:247], v[128:131], v[184:187], v[244:247]\n" \
M_"a[112:115], a[120:123], v[72:75], a[112:115]\n" \
"v_mul_f32_e32 v216, v56, v216\n" \
"v_mul_f32_e32 v217, v56, v217\n" \
"v_mul_f32_e32 v218, v56, v218\n" \
"v_mul_f32_e32 v219, v56, v219\n" \
"v_mul_f32_e32 v220, v56, v220\n" \
"v_mul_f32_e32 v221, v56, v221\n" \
"v_mul_f32_e32 v222, v56, v222\n" \
"v_mul_f32_e32 v223, v56, v223\n" \
C_"v76, v216, v217\n" \
C_"v77, v218, v219\n" \
C_"v78, v220, v221\n" \
C_"v79, v222, v223\n" \
"v_mul_f32_e32 v57, v57, v61\n" \
"v_mul_f32_e32 v224, v57, v224\n" \
M_"a[112:115], a[120:123], v[76:79], a[112:115]\n" \
"v_mul_f32_e32 v225, v57, v225\n" \
"v_mul_f32_e32 v226, v57, v226\n" \
"v_mul_f32_e32 v227, v57, v227\n" \
"v_mul_f32_e32 v228, v57, v228\n" \
"v_mul_f32_e32 v229, v57, v229\n" \
"v_mul_f32_e32 v230, v57, v230\n" \
"v_mul_f32_e32 v231, v57, v231\n" \
C_"v80, v224, v225\n" \
C_"v81, v226, v227\n" \
C_"v82, v228, v229\n" \
C_"v83, v230, v231\n" \
M_"v[248:251], v[130:133], v[188:191], v[248:251]\n" \
"v_mul_f32_e32 v232, v57, v232\n" \
"v_mul_f32_e32 v233, v57, v233\n" \
M_"a[112:115], a[120:123], v[80:83], a[112:115]\n" \
"v_mul_f32_e32 v234, v57, v234\n" \
"v_mul_f32_e32 v235, v57, v235\n" \
"v_mul_f32_e32 v236, v57, v236\n" \
"v_mul_f32_e32 v237, v57, v237\n" \
"v_mul_f32_e32 v238, v57, v238\n" \
"v_mul_f32_e32 v239, v57, v239\n" \
C_"v84, v232, v233\n" \
C_"v85, v234, v235\n" \
C_"v86, v236, v237\n" \
C_"v87, v238, v239\n" \
"v_mul_f32_e32 v240, v57, v240\n" \
"v_mul_f32_e32 v241, v57, v241\n" \
M_"a[112:115], a[120:123], v[84:87], a[112:115]\n" \
"v_mul_f32_e32 v242, v57, v242\n" \
"v_mul_f32_e32 v243, v57, v243\n" \
"v_mul_f32_e32 v244, v57, v244\n" \
"v_mul_f32_e32 v245, v57, v245\n" \
"v_mul_f32_e32 v246, v57, v246\n" \
"v_mul_f32_e32 v247, v57, v247\n" \
C_"v96, v240, v241\n" \
C_"v97, v242, v243\n" \
C_"v98, v244, v245\n" \
C_"v99, v246, v247\n" \
M_"v[252:255], v[128:131], v[188:191], v[252:255]\n" \
"v_mul_f32_e32 v248, v57, v248\n" \
M_"a[112:115], a[120:123], v[96:99], a[112:115]\n" \
"v_mul_f32_e32 v249, v57, v249\n" \
"v_mul_f32_e32 v250, v57, v250\n" \
"v_mul_f32_e32 v251, v57, v251\n" \
C_"v100, v248, v249\n" \
C_"v101, v250, v251\n" \
"v_cmp_lt_f32_e64 s[72:73], s21, v62\n" \
"v_mul_f32_e32 v252, v57, v252\n" \
"v_mul_f32_e32 v253, v57, v253\n" \
"v_mul_f32_e32 v254, v57, v254\n" \
"v_mul_f32_e32 v255, v57, v255\n" \
C_"v102, v252, v253\n" \
C_"v103, v254, v255\n" \
"s_nop 1\n" \
M_"a[112:115], a[120:123], v[100:103], a[112:115]\n" \
"s_nop 7\n" \
"v_accvgpr_read_b32 v63, a112\n" \
"v_cmp_u_f32_e32 vcc, v63, v63\n" \
"s_nop 0\n" \
"s_or_b64 vcc, s[72:73], vcc\n" \
"s_cbranch_scc1 L_end_%=\n" \
"s_branch L_out_%=\n" \
"L_skb_%=:\n" \
"s_sleep 1\n" \
"buffer_load_dword v57, v61, s[56:59], s96 offen sc1\n" \
"s_add_u32 s101, s101, 1\n" \
"s_waitcnt vmcnt(0)\n" \
"v_cmp_ne_u32_e32 vcc, s62, v57\n" \
"s_cmp_gt_u32 s101, 0x100000\n" \
"s_cbranch_scc1 L_rkb_%=\n" \
"s_nop 2\n" \
"s_cbranch_vccnz L_skb_%=\n" \
"s_branch L_rkb_%=\n" \
"L_sol_%=:\n" \
"s_sleep 1\n" \
"buffer_load_dwordx2 v[26:27], v16, s[56:59], s96 offen sc1\n" \
"s_add_u32 s101, s101, 1\n" \
"s_waitcnt vmcnt(0)\n" \
"v_cmp_ne_u64_e32 vcc, s[62:63], v[26:27]\n" \
"s_cmp_gt_u32 s101, 0x100000\n" \
"s_cbranch_scc1 L_rol_%=\n" \
"s_nop 2\n" \
"s_cbranch_vccnz L_sol_%=\n" \
"s_branch L_rol_%=\n" \
"L_sos_%=:\n" \
"s_sleep 1\n" \
"buffer_load_dwordx2 v[26:27], v16, s[56:59], s96 offen sc1\n" \
"s_add_u32 s101, s101, 1\n" \
"s_waitcnt vmcnt(0)\n" \
"v_cmp_ne_u64_e32 vcc, s[62:63], v[26:27]\n" \
"s_cmp_gt_u32 s101, 0x100000\n" \
"s_cbranch_scc1 L_ros_%=\n" \
"s_nop 2\n" \
"s_cbranch_vccnz L_sos_%=\n" \
"s_branch L_ros_%=\n" \
"L_sa_%=:\n" \
"s_sleep 1\n" \
"buffer_load_dword v26, v38, s[56:59], s64 offen sc1\n" \
"s_add_u32 s101, s101, 1\n" \
"s_waitcnt vmcnt(0)\n" \
"v_cmp_ne_u32_e32 vcc, s62, v26\n" \
"s_cmp_gt_u32 s101, 0x100000\n" \
"s_cbranch_scc1 L_ra_%=\n" \
"s_nop 2\n" \
"s_cbranch_vccnz L_sa_%=\n" \
"s_branch L_ra_%=\n" \
"L_sb_%=:\n" \
"s_sleep 1\n" \
"buffer_load_dword v47, v38, s[56:59], s71 offen sc1\n" \
"s_add_u32 s101, s101, 1\n" \
"s_waitcnt vmcnt(0)\n" \
"v_cmp_ne_u32_e32 vcc, s62, v47\n" \
"s_cmp_gt_u32 s101, 0x100000\n" \
"s_cbranch_scc1 L_rb_%=\n" \
"s_nop 2\n" \
"s_cbranch_vccnz L_sb_%=\n" \
"s_branch L_rb_%=\n" \
"L_sna_%=:\n" \
"buffer_load_dword v37, v40, s[56:59], s74 offen sc1\n" \
"s_add_u32 s101, s101, 1\n" \
"s_waitcnt vmcnt(0)\n" \
"v_cmp_eq_u32_e64 s[98:99], s62, v37\n" \
"s_cmp_gt_u32 s101, 0x100000\n" \
"s_cbranch_scc1 L_rna_%=\n" \
"s_andn2_b32 s75, s20, s98\n" \
"s_cbranch_scc0 L_gna_%=\n" \
"s_sleep 0\n" \
"s_branch L_sna_%=\n" \
"L_gna_%=:\n" \
"s_bitcmp1_b32 s20, 0\n" \
"s_cbranch_scc0 L_kna0_%=\n" \
B_"v[116:119], v20, s[56:59], s12 offen offset:0 sc1\n" \
"L_kna0_%=:\n" \
"s_bitcmp1_b32 s20, 1\n" \
"s_cbranch_scc0 L_kna1_%=\n" \
B_"v[124:127], v20, s[56:59], s12 offen offset:2048 sc1\n" \
"L_kna1_%=:\n" \
"s_bitcmp1_b32 s20, 2\n" \
"s_cbranch_scc0 L_kna2_%=\n" \
B_"v[132:135], v20, s[56:59], s13 offen offset:0 sc1\n" \
"L_kna2_%=:\n" \
"s_bitcmp1_b32 s20, 3\n" \
"s_cbranch_scc0 L_kna3_%=\n" \
B_"v[140:143], v20, s[56:59], s13 offen offset:2048 sc1\n" \
"L_kna3_%=:\n" \
"s_bitcmp1_b32 s20, 4\n" \
"s_cbranch_scc0 L_kna4_%=\n" \
B_"v[148:151], v20, s[56:59], s14 offen offset:0 sc1\n" \
"L_kna4_%=:\n" \
"s_bitcmp1_b32 s20, 5\n" \
"s_cbranch_scc0 L_kna5_%=\n" \
B_"v[156:159], v20, s[56:59], s14 offen offset:2048 sc1\n" \
"L_kna5_%=:\n" \
"s_bitcmp1_b32 s20, 6\n" \
"s_cbranch_scc0 L_kna6_%=\n" \
B_"v[164:167], v20, s[56:59], s15 offen offset:0 sc1\n" \
"L_kna6_%=:\n" \
"s_waitcnt vmcnt(0)\n" \
"s_branch L_rna_%=\n" \
"L_snx_%=:\n" \
"buffer_load_dword v37, v40, s[56:59], s74 offen sc1\n" \
"s_add_u32 s101, s101, 1\n" \
"s_waitcnt vmcnt(0)\n" \
"v_cmp_eq_u32_e64 s[98:99], s62, v37\n" \
"s_cmp_gt_u32 s101, 0x100000\n" \
"s_cbranch_scc1 L_rnx_%=\n" \
"s_andn2_b32 s75, s21, s98\n" \
"s_cbranch_scc0 L_gnx_%=\n" \
"s_sleep 0\n" \
"s_branch L_snx_%=\n" \
"L_gnx_%=:\n" \
"s_bitcmp1_b32 s21, 16\n" \
"s_cbranch_scc0 L_knx16_%=\n" \
B_"v[112:115], v20, s[56:59], s16 offen offset:0 sc1\n" \
"L_knx16_%=:\n" \
"s_bitcmp1_b32 s21, 17\n" \
"s_cbranch_scc0 L_knx17_%=\n" \
B_"v[184:187], v20, s[56:59], s16 offen offset:2048 sc1\n" \
"L_knx17_%=:\n" \
"s_bitcmp1_b32 s21, 18\n" \
"s_cbranch_scc0 L_knx18_%=\n" \
B_"v[120:123], v20, s[56:59], s17 offen offset:0 sc1\n" \
"L_knx18_%=:\n" \
"s_bitcmp1_b32 s21, 19\n" \
"s_cbranch_scc0 L_knx19_%=\n" \
B_"v[192:195], v20, s[56:59], s17 offen offset:2048 sc1\n" \
"L_knx19_%=:\n" \
"s_bitcmp1_b32 s21, 20\n" \
"s_cbranch_scc0 L_knx20_%=\n" \
B_"v[128:131], v20, s[56:59], s18 offen offset:0 sc1\n" \
"L_knx20_%=:\n" \
"s_bitcmp1_b32 s21, 21\n" \
"s_cbranch_scc0 L_knx21_%=\n" \
B_"v[200:203], v20, s[56:59], s18 offen offset:2048 sc1\n" \
"L_knx21_%=:\n" \
"s_bitcmp1_b32 s21, 22\n" \
"s_cbranch_scc0 L_knx22_%=\n" \
B_"v[176:179], v20, s[56:59], s19 offen offset:0 sc1\n" \
"L_knx22_%=:\n" \
"s_bitcmp1_b32 s21, 23\n" \
"s_cbranch_scc0 L_knx23_%=\n" \
B_"v[248:251], v20, s[56:59], s19 offen offset:2048 sc1\n" \
"L_knx23_%=:\n" \
"s_bitcmp1_b32 s21, 8\n" \
"s_cbranch_scc0 L_knx8_%=\n" \
B_"v[188:191], v20, s[56:59], s12 offen offset:1024 sc1\n" \
"L_knx8_%=:\n" \
"s_bitcmp1_b32 s21, 9\n" \
"s_cbranch_scc0 L_knx9_%=\n" \
B_"v[196:199], v20, s[56:59], s12 offen offset:3072 sc1\n" \
"L_knx9_%=:\n" \
"s_waitcnt vmcnt(0)\n" \
"s_branch L_rnx_%=\n" \
"L_sx0_%=:\n" \
"s_sleep 0\n" \
"buffer_load_dword v37, v40, s[56:59], s74 offen sc1\n" \
"s_add_u32 s101, s101, 1\n" \
"s_waitcnt vmcnt(0)\n" \
"v_cmp_eq_u32_e64 s[98:99], s62, v37\n" \
"s_cmp_gt_u32 s101, 0x100000\n" \
"s_cbranch_scc1 L_rx0_%=\n" \
"s_andn2_b32 s75, 0x400, s98\n" \
"s_cbranch_scc1 L_sx0_%=\n" \
"s_branch L_rx0_%=\n" \
"L_sx1_%=:\n" \
"s_sleep 0\n" \
"buffer_load_dword v37, v40, s[56:59], s74 offen sc1\n" \
"s_add_u32 s101, s101, 1\n" \
"s_waitcnt vmcnt(0)\n" \
"v_cmp_eq_u32_e64 s[98:99], s62, v37\n" \
"s_cmp_gt_u32 s101, 0x100000\n" \
"s_cbranch_scc1 L_rx1_%=\n" \
"s_andn2_b32 s75, 0x800, s98\n" \
"s_cbranch_scc1 L_sx1_%=\n" \
"s_branch L_rx1_%=\n" \
"L_sx2_%=:\n" \
"s_sleep 0\n" \
"buffer_load_dword v37, v40, s[56:59], s74 offen sc1\n" \
"s_add_u32 s101, s101, 1\n" \
"s_waitcnt vmcnt(0)\n" \
"v_cmp_eq_u32_e64 s[98:99], s62, v37\n" \
"s_cmp_gt_u32 s101, 0x100000\n" \
"s_cbranch_scc1 L_rx2_%=\n" \
"s_andn2_b32 s75, 0x1000, s98\n" \
"s_cbranch_scc1 L_sx2_%=\n" \
"s_branch L_rx2_%=\n" \
"L_sx3_%=:\n" \
"s_sleep 0\n" \
"buffer_load_dword v37, v40, s[56:59], s74 offen sc1\n" \
"s_add_u32 s101, s101, 1\n" \
"s_waitcnt vmcnt(0)\n" \
"v_cmp_eq_u32_e64 s[98:99], s62, v37\n" \
"s_cmp_gt_u32 s101, 0x100000\n" \
"s_cbranch_scc1 L_rx3_%=\n" \
"s_andn2_b32 s75, 0x2000, s98\n" \
"s_cbranch_scc1 L_sx3_%=\n" \
"s_branch L_rx3_%=\n" \
"L_sx4_%=:\n" \
"s_sleep 0\n" \
"buffer_load_dword v37, v40, s[56:59], s74 offen sc1\n" \
"s_add_u32 s101, s101, 1\n" \
"s_waitcnt vmcnt(0)\n" \
"v_cmp_eq_u32_e64 s[98:99], s62, v37\n" \
"s_cmp_gt_u32 s101, 0x100000\n" \
"s_cbranch_scc1 L_rx4_%=\n" \
"s_andn2_b32 s75, 0x4000, s98\n" \
"s_cbranch_scc1 L_sx4_%=\n" \
"s_branch L_rx4_%=\n" \
"L_att_stub:\n" \
"s_mov_b32 s101, 0\n" \
"v_add_u32_e32 v32, 0x49000, v20\n" \
"v_readlane_b32 s96, v11, 39\n" \
"v_readlane_b32 s97, v11, 40\n" \
B_"a[192:195], v20, s[56:59], s60 offen offset:0 sc1\n" \
B_"a[208:211], v32, s[56:59], s60 offen offset:0 sc1\n" \
B_"a[196:199], v20, s[56:59], s60 offen offset:1024 sc1\n" \
B_"a[212:215], v32, s[56:59], s60 offen offset:1024 sc1\n" \
B_"a[200:203], v20, s[56:59], s60 offen offset:2048 sc1\n" \
B_"a[216:219], v32, s[56:59], s60 offen offset:2048 sc1\n" \
B_"a[204:207], v20, s[56:59], s60 offen offset:3072 sc1\n" \
B_"a[220:223], v32, s[56:59], s60 offen offset:3072 sc1\n" \
B_"a[124:127], v20, s[56:59], s96 offen offset:0 sc1\n" \
B_"a[228:231], v32, s[56:59], s96 offen offset:0 sc1\n" \
B_"a[236:239], v20, s[56:59], s97 offen offset:0 sc1\n" \
B_"a[244:247], v32, s[56:59], s97 offen offset:0 sc1\n" \
B_"a[224:227], v20, s[56:59], s96 offen offset:1024 sc1\n" \
B_"a[232:235], v32, s[56:59], s96 offen offset:1024 sc1\n" \
B_"a[240:243], v20, s[56:59], s97 offen offset:1024 sc1\n" \
B_"a[248:251], v32, s[56:59], s97 offen offset:1024 sc1\n" \
B_"v[64:67], v20, s[56:59], s61 offen offset:0 sc1\n" \
B_"v[68:71], v20, s[56:59], s61 offen offset:1024 sc1\n" \
B_"v[72:75], v20, s[56:59], s61 offen offset:2048 sc1\n" \
B_"v[76:79], v20, s[56:59], s61 offen offset:3072 sc1\n" \
B_"v[80:83], v32, s[56:59], s61 offen offset:0 sc1\n" \
B_"v[84:87], v32, s[56:59], s61 offen offset:1024 sc1\n" \
B_"v[96:99], v32, s[56:59], s61 offen offset:2048 sc1\n" \
B_"v[100:103], v32, s[56:59], s61 offen offset:3072 sc1\n" \
"v_not_b32_e32 v26, s62\n" \
"s_waitcnt vmcnt(0)\n" \
"s_branch L_out_%=\n" \
".fill (L_ab + 16384 - .) / 4, 4, 0xBF800000\n" \
".fill (L_ab + 16384 - .) / 4, 4, 0xBF800000\n" \
"L_ap4:\n" \
"s_setpc_b64 s[88:89]\n" \
"L_gsw:\n" \
M_"v[104:107], a[128:131], a[16:19], v[36:39]\n" \
"s_add_u32 s68, s60, 0xfff6e000\n" \
"s_add_u32 s69, s60, 0xfffb7000\n" \
"s_add_u32 s70, s60, 0x0\n" \
"s_add_u32 s71, s60, 0x49000\n" \
M_"v[104:107], a[132:135], a[20:23], v[104:107]\n" \
"s_xor_b32 m0, s65, 0x12000\n" \
R_"a[160:163], v49 offset:0\n" \
M_"v[104:107], a[136:139], a[24:27], v[104:107]\n" \
B_"v50, s[8:11], s64 offen offset:0 sc1 lds\n" \
M_"v[104:107], a[140:143], a[28:31], v[104:107]\n" \
R_"a[164:167], v49 offset:1024\n" \
R_"a[168:171], v49 offset:2048\n" \
M_"v[108:111], a[156:159], a[28:31], v[36:39]\n" \
R_"a[172:175], v49 offset:3072\n" \
R_"a[176:179], v49 offset:4096\n" \
M_"v[108:111], a[152:155], a[24:27], v[108:111]\n" \
B_"v50, s[8:11], s64 offen offset:1024 sc1 lds\n" \
M_"v[108:111], a[148:151], a[20:23], v[108:111]\n" \
"s_mov_b32 s65, s13\n" \
"v_add_u32_e32 v63, s15, v20\n" \
M_"v[108:111], a[144:147], a[16:19], v[108:111]\n" \
"s_add_u32 s67, s14, 40960\n" \
"v_add_u32_e32 v50, 0x6000, v63\n" \
M_"v[116:119], a[144:147], a[32:35], v[40:43]\n" \
"s_xor_b32 m0, s65, 0x8000\n" \
R_"a[180:183], v49 offset:5120\n" \
M_"v[116:119], a[148:151], a[36:39], v[116:119]\n" \
R_"a[184:187], v49 offset:6144\n" \
"v_and_b32_e32 v68, v12, v68\n" \
M_"v[116:119], a[152:155], a[40:43], v[116:119]\n" \
"v_and_b32_e32 v69, v13, v69\n" \
"v_and_b32_e32 v70, v14, v70\n" \
M_"v[116:119], a[156:159], a[44:47], v[116:119]\n" \
B_"v50, s[8:11], s67 offen offset:0 sc1 lds\n" \
M_"v[112:115], a[140:143], a[44:47], v[40:43]\n" \
"v_and_b32_e32 v71, v15, v71\n" \
R_"a[188:191], v49 offset:7168\n" \
M_"v[112:115], a[136:139], a[40:43], v[112:115]\n" \
E_"v86, v86\n" \
M_"v[112:115], a[132:135], a[36:39], v[112:115]\n" \
E_"v87, v87\n" \
M_"v[112:115], a[128:131], a[32:35], v[112:115]\n" \
E_"v84, v84\n" \
M_"v[120:123], a[128:131], a[48:51], v[44:47]\n" \
B_"v50, s[8:11], s67 offen offset:1024 sc1 lds\n" \
M_"v[120:123], a[132:135], a[52:55], v[120:123]\n" \
E_"v85, v85\n" \
M_"v[120:123], a[136:139], a[56:59], v[120:123]\n" \
C_"v87, v86, v87\n" \
C_"v86, v84, v85\n" \
M_"v[120:123], a[140:143], a[60:63], v[120:123]\n" \
E_"v82, v82\n" \
M_"v[124:127], a[156:159], a[60:63], v[44:47]\n" \
E_"v83, v83\n" \
M_"v[124:127], a[152:155], a[56:59], v[124:127]\n" \
E_"v80, v80\n" \
M_"v[124:127], a[148:151], a[52:55], v[124:127]\n" \
B_"v50, s[8:11], s67 offen offset:2048 sc1 lds\n" \
M_"v[124:127], a[144:147], a[48:51], v[124:127]\n" \
C_"v85, v82, v83\n" \
"s_waitcnt lgkmcnt(1)\n" \
M_"v[128:131], a[160:163], v[68:71], v[128:131]\n" \
E_"v81, v81\n" \
M_"v[160:163], a[160:163], v[72:75], v[160:163]\n" \
C_"v84, v80, v81\n" \
"s_mov_b32 s64, s14\n" \
M_"v[132:135], a[164:167], v[68:71], v[132:135]\n" \
E_"v88, v88\n" \
M_"v[164:167], a[164:167], v[72:75], v[164:167]\n" \
E_"v89, v89\n" \
M_"v[136:139], a[168:171], v[68:71], v[136:139]\n" \
B_"v50, s[8:11], s67 offen offset:3072 sc1 lds\n" \
M_"v[168:171], a[168:171], v[72:75], v[168:171]\n" \
E_"v90, v90\n" \
M_"v[140:143], a[172:175], v[68:71], v[140:143]\n" \
E_"v91, v91\n" \
M_"v[172:175], a[172:175], v[72:75], v[172:175]\n" \
C_"v88, v88, v89\n" \
C_"v89, v90, v91\n" \
M_"v[144:147], a[176:179], v[68:71], v[144:147]\n" \
E_"v92, v92\n" \
M_"v[176:179], a[176:179], v[72:75], v[176:179]\n" \
E_"v93, v93\n" \
M_"v[148:151], a[180:183], v[68:71], v[148:151]\n" \
E_"v94, v94\n" \
M_"v[180:183], a[180:183], v[72:75], v[180:183]\n" \
E_"v95, v95\n" \
M_"v[152:155], a[184:187], v[68:71], v[152:155]\n" \
C_"v90, v92, v93\n" \
C_"v91, v94, v95\n" \
M_"v[184:187], a[184:187], v[72:75], v[184:187]\n" \
"v_add_u32_e32 v51, 0x12000, v63\n" \
"s_waitcnt lgkmcnt(0)\n" \
M_"v[156:159], a[188:191], v[68:71], v[156:159]\n" \
"s_xor_b32 m0, s65, 0xa000\n" \
"ds_read_b32 v58, v18 offset:512\n" \
M_"v[188:191], a[188:191], v[72:75], v[188:191]\n" \
B_"v51, s[8:11], s64 offen offset:0 sc1 lds\n" \
"v_smfmac_f32_16x16x64_bf16 a[96:99], a[104:107], v[68:75], v21\n" \
B_"v51, s[8:11], s64 offen offset:1024 sc1 lds\n" \
M_"v[220:223], a[188:191], v[84:87], v[220:223]\n" \
"ds_read_b32 v59, v18 offset:640\n" \
"ds_read_b32 v60, v18 offset:768\n" \
M_"v[252:255], a[188:191], v[88:91], v[252:255]\n" \
"ds_read_b32 v61, v18 offset:896\n" \
R_"a[128:131], v48 offset:0\n" \
M_"v[216:219], a[184:187], v[84:87], v[216:219]\n" \
R_"a[144:147], v48 offset:4096\n" \
R_"a[132:135], v48 offset:1024\n" \
M_"v[248:251], a[184:187], v[88:91], v[248:251]\n" \
R_"a[148:151], v48 offset:5120\n" \
R_"a[136:139], v48 offset:2048\n" \
M_"v[212:215], a[180:183], v[84:87], v[212:215]\n" \
R_"a[152:155], v48 offset:6144\n" \
R_"a[140:143], v48 offset:3072\n" \
M_"v[244:247], a[180:183], v[88:91], v[244:247]\n" \
R_"a[156:159], v48 offset:7168\n" \
"v_add_u32_e32 v52, 0x14000, v63\n" \
M_"v[208:211], a[176:179], v[84:87], v[208:211]\n" \
B_"v51, s[8:11], s64 offen offset:2048 sc1 lds\n" \
M_"v[240:243], a[176:179], v[88:91], v[240:243]\n" \
E_"v104, v104\n" \
M_"v[204:207], a[172:175], v[84:87], v[204:207]\n" \
E_"v105, v105\n" \
M_"v[236:239], a[172:175], v[88:91], v[236:239]\n" \
B_"v51, s[8:11], s64 offen offset:3072 sc1 lds\n" \
M_"v[200:203], a[168:171], v[84:87], v[200:203]\n" \
E_"v106, v106\n" \
M_"v[232:235], a[168:171], v[88:91], v[232:235]\n" \
E_"v107, v107\n" \
M_"v[196:199], a[164:167], v[84:87], v[196:199]\n" \
C_"v104, v104, v105\n" \
C_"v105, v106, v107\n" \
M_"v[228:231], a[164:167], v[88:91], v[228:231]\n" \
E_"v108, v108\n" \
M_"v[192:195], a[160:163], v[84:87], v[192:195]\n" \
E_"v109, v109\n" \
M_"v[224:227], a[160:163], v[88:91], v[224:227]\n" \
E_"v110, v110\n" \
"v_smfmac_f32_16x16x64_bf16 a[100:103], a[104:107], v[84:91], v21\n" \
E_"v111, v111\n" \
C_"v106, v108, v109\n" \
C_"v107, v110, v111\n" \
"v_and_b32_e32 v104, v12, v104\n" \
"v_and_b32_e32 v105, v13, v105\n" \
"v_and_b32_e32 v106, v14, v106\n" \
"v_and_b32_e32 v107, v15, v107\n" \
"v_accvgpr_read_b32 v54, a96\n" \
"v_accvgpr_read_b32 v22, a97\n" \
"v_add_u32_e32 v53, 0x20000, v63\n" \
E_"v118, v118\n" \
E_"v119, v119\n" \
E_"v116, v116\n" \
E_"v117, v117\n" \
C_"v119, v118, v119\n" \
E_"v114, v114\n" \
E_"v115, v115\n" \
E_"v112, v112\n" \
E_"v113, v113\n" \
C_"v118, v116, v117\n" \
C_"v117, v114, v115\n" \
C_"v116, v112, v113\n" \
"v_readlane_b32 s93, v11, 22\n" \
"s_waitcnt vmcnt(10) lgkmcnt(0)\n" \
"s_barrier\n" \
M_"v[80:83], a[128:131], a[32:35], v[40:43]\n" \
R_"a[160:163], v49 offset:8192\n" \
R_"a[164:167], v49 offset:9216\n" \
M_"v[80:83], a[132:135], a[36:39], v[80:83]\n" \
R_"a[168:171], v49 offset:10240\n" \
"s_xor_b32 m0, s65, 0x18000\n" \
M_"v[80:83], a[136:139], a[40:43], v[80:83]\n" \
B_"v52, s[8:11], s64 offen offset:0 sc1 lds\n" \
M_"v[80:83], a[140:143], a[44:47], v[80:83]\n" \
R_"a[172:175], v49 offset:11264\n" \
R_"a[176:179], v49 offset:12288\n" \
M_"v[84:87], a[156:159], a[44:47], v[40:43]\n" \
R_"a[180:183], v49 offset:13312\n" \
R_"a[184:187], v49 offset:14336\n" \
M_"v[84:87], a[152:155], a[40:43], v[84:87]\n" \
B_"v52, s[8:11], s64 offen offset:1024 sc1 lds\n" \
M_"v[84:87], a[148:151], a[36:39], v[84:87]\n" \
R_"a[188:191], v49 offset:15360\n" \
"v_readlane_b32 s80, v11, 11\n" \
M_"v[84:87], a[144:147], a[32:35], v[84:87]\n" \
B_"v52, s[8:11], s64 offen offset:2048 sc1 lds\n" \
M_"v[92:95], a[144:147], a[48:51], v[44:47]\n" \
E_"v120, v120\n" \
M_"v[92:95], a[148:151], a[52:55], v[92:95]\n" \
E_"v121, v121\n" \
M_"v[92:95], a[152:155], a[56:59], v[92:95]\n" \
B_"v52, s[8:11], s64 offen offset:3072 sc1 lds\n" \
M_"v[92:95], a[156:159], a[60:63], v[92:95]\n" \
"s_mov_b32 m0, s65\n" \
C_"v120, v120, v121\n" \
M_"v[88:91], a[140:143], a[60:63], v[44:47]\n" \
E_"v122, v122\n" \
M_"v[88:91], a[136:139], a[56:59], v[88:91]\n" \
B_"v53, s[8:11], s64 offen offset:0 sc1 lds\n" \
M_"v[88:91], a[132:135], a[52:55], v[88:91]\n" \
E_"v123, v123\n" \
M_"v[88:91], a[128:131], a[48:51], v[88:91]\n" \
E_"v124, v124\n" \
M_"a[96:99], a[116:119], v[104:107], a[96:99]\n" \
C_"v121, v122, v123\n" \
"s_waitcnt lgkmcnt(5)\n" \
M_"v[160:163], a[160:163], v[104:107], v[160:163]\n" \
B_"v53, s[8:11], s64 offen offset:1024 sc1 lds\n" \
M_"v[164:167], a[164:167], v[104:107], v[164:167]\n" \
E_"v125, v125\n" \
M_"v[168:171], a[168:171], v[104:107], v[168:171]\n" \
C_"v122, v124, v125\n" \
"s_waitcnt lgkmcnt(1)\n" \
M_"v[172:175], a[172:175], v[104:107], v[172:175]\n" \
E_"v126, v126\n" \
M_"v[176:179], a[176:179], v[104:107], v[176:179]\n" \
E_"v127, v127\n" \
M_"v[180:183], a[180:183], v[104:107], v[180:183]\n" \
C_"v123, v126, v127\n" \
"v_readlane_b32 s81, v11, 12\n" \
M_"v[184:187], a[184:187], v[104:107], v[184:187]\n" \
"s_mov_b32 s82, 0x47fca0\n" \
"s_waitcnt lgkmcnt(0)\n" \
M_"v[188:191], a[188:191], v[104:107], v[188:191]\n" \
B_"v53, s[8:11], s64 offen offset:2048 sc1 lds\n" \
M_"v[220:223], a[188:191], v[116:119], v[220:223]\n" \
B_"v53, s[8:11], s64 offen offset:3072 sc1 lds\n" \
M_"v[252:255], a[188:191], v[120:123], v[252:255]\n" \
"s_mov_b32 s83, 0x20000\n" \
"v_readlane_b32 s92, v11, 29\n" \
M_"v[216:219], a[184:187], v[116:119], v[216:219]\n" \
"s_add_u32 m0, s93, 0x0\n" \
R_"a[128:131], v48 offset:8192\n" \
M_"v[248:251], a[184:187], v[120:123], v[248:251]\n" \
R_"a[144:147], v48 offset:12288\n" \
R_"a[132:135], v48 offset:9216\n" \
M_"v[212:215], a[180:183], v[116:119], v[212:215]\n" \
B_"v0, s[80:83], s92 offen offset:0 lds\n" \
M_"v[244:247], a[180:183], v[120:123], v[244:247]\n" \
B_"v1, s[80:83], s92 offen offset:1024 lds\n" \
M_"v[208:211], a[176:179], v[116:119], v[208:211]\n" \
R_"a[148:151], v48 offset:13312\n" \
R_"a[136:139], v48 offset:10240\n" \
M_"v[240:243], a[176:179], v[120:123], v[240:243]\n" \
R_"a[152:155], v48 offset:14336\n" \
R_"a[140:143], v48 offset:11264\n" \
M_"v[204:207], a[172:175], v[116:119], v[204:207]\n" \
B_"v2, s[80:83], s92 offen offset:2048 lds\n" \
M_"v[236:239], a[172:175], v[120:123], v[236:239]\n" \
R_"a[156:159], v48 offset:15360\n" \
"s_xor_b32 s65, s65, 0x8000\n" \
M_"v[200:203], a[168:171], v[116:119], v[200:203]\n" \
"s_add_u32 s64, s64, 0x10000\n" \
"v_add_u32_e32 v49, 0x10000, v48\n" \
M_"v[232:235], a[168:171], v[120:123], v[232:235]\n" \
B_"v3, s[80:83], s92 offen offset:3072 lds\n" \
M_"v[196:199], a[164:167], v[116:119], v[196:199]\n" \
E_"v86, v86\n" \
M_"v[228:231], a[164:167], v[120:123], v[228:231]\n" \
E_"v87, v87\n" \
M_"v[192:195], a[160:163], v[116:119], v[192:195]\n" \
E_"v84, v84\n" \
M_"v[224:227], a[160:163], v[120:123], v[224:227]\n" \
E_"v85, v85\n" \
"v_smfmac_f32_16x16x64_bf16 a[100:103], a[104:107], v[116:123], v21\n" \
E_"v88, v88\n" \
E_"v89, v89\n" \
C_"v87, v86, v87\n" \
E_"v82, v82\n" \
E_"v83, v83\n" \
E_"v80, v80\n" \
E_"v81, v81\n" \
E_"v90, v90\n" \
E_"v91, v91\n" \
C_"v86, v84, v85\n" \
C_"v88, v88, v89\n" \
E_"v92, v92\n" \
E_"v93, v93\n" \
E_"v94, v94\n" \
E_"v95, v95\n" \
C_"v85, v82, v83\n" \
C_"v84, v80, v81\n" \
C_"v89, v90, v91\n" \
"v_and_b32_e32 v84, v12, v84\n" \
"v_and_b32_e32 v85, v13, v85\n" \
"v_and_b32_e32 v86, v14, v86\n" \
"v_and_b32_e32 v87, v15, v87\n" \
C_"v90, v92, v93\n" \
C_"v91, v94, v95\n" \
"v_add_u32_e32 v48, s16, v20\n" \
"v_add_f32_e32 v54, v54, v22\n" \
"v_accvgpr_read_b32 v55, a98\n" \
"v_accvgpr_read_b32 v23, a99\n" \
M_"a[96:99], a[120:123], a[120:123], 0\n" \
"v_max_f32_e32 v62, v62, v54\n" \
"v_add_f32_e32 v55, v55, v23\n" \
"v_max_f32_e32 v62, v62, v55\n" \
"v_rcp_f32_e32 v54, v54\n" \
"v_rcp_f32_e32 v55, v55\n" \
"v_mul_f32_e32 v54, v54, v58\n" \
"v_mul_f32_e32 v55, v55, v59\n" \
"v_mul_f32_e32 v128, v54, v128\n" \
"v_mul_f32_e32 v129, v54, v129\n" \
"v_mul_f32_e32 v136, v54, v136\n" \
"v_mul_f32_e32 v137, v54, v137\n" \
"v_mul_f32_e32 v144, v54, v144\n" \
"v_mul_f32_e32 v145, v54, v145\n" \
"v_mul_f32_e32 v152, v54, v152\n" \
"v_mul_f32_e32 v153, v54, v153\n" \
"v_mul_f32_e32 v160, v55, v160\n" \
"v_mul_f32_e32 v161, v55, v161\n" \
"v_mul_f32_e32 v168, v55, v168\n" \
"v_mul_f32_e32 v169, v55, v169\n" \
"v_mul_f32_e32 v176, v55, v176\n" \
"v_mul_f32_e32 v177, v55, v177\n" \
"v_mul_f32_e32 v184, v55, v184\n" \
"v_mul_f32_e32 v185, v55, v185\n" \
C_"v128, v128, v129\n" \
"v_mul_f32_e32 v130, v54, v130\n" \
"v_mul_f32_e32 v131, v54, v131\n" \
C_"v136, v136, v137\n" \
"v_mul_f32_e32 v138, v54, v138\n" \
"v_mul_f32_e32 v139, v54, v139\n" \
C_"v144, v144, v145\n" \
"v_mul_f32_e32 v146, v54, v146\n" \
"v_mul_f32_e32 v147, v54, v147\n" \
C_"v152, v152, v153\n" \
"v_mul_f32_e32 v154, v54, v154\n" \
"v_mul_f32_e32 v155, v54, v155\n" \
C_"v160, v160, v161\n" \
"v_mul_f32_e32 v162, v55, v162\n" \
"v_mul_f32_e32 v163, v55, v163\n" \
C_"v168, v168, v169\n" \
"v_mul_f32_e32 v170, v55, v170\n" \
"v_mul_f32_e32 v171, v55, v171\n" \
C_"v176, v176, v177\n" \
"v_mul_f32_e32 v178, v55, v178\n" \
"v_mul_f32_e32 v179, v55, v179\n" \
C_"v184, v184, v185\n" \
"v_mul_f32_e32 v186, v55, v186\n" \
"v_mul_f32_e32 v187, v55, v187\n" \
C_"v129, v130, v131\n" \
"v_mul_f32_e32 v132, v54, v132\n" \
"v_mul_f32_e32 v133, v54, v133\n" \
"v_mul_f32_e32 v134, v54, v134\n" \
"v_mul_f32_e32 v135, v54, v135\n" \
C_"v137, v138, v139\n" \
"v_mul_f32_e32 v140, v54, v140\n" \
"s_waitcnt vmcnt(12) lgkmcnt(0)\n" \
"s_barrier\n" \
M_"v[120:123], a[128:131], a[48:51], v[44:47]\n" \
"s_xor_b32 m0, s65, 0x12000\n" \
R_"a[188:191], v49 offset:7168\n" \
M_"v[120:123], a[132:135], a[52:55], v[120:123]\n" \
B_"v50, s[8:11], s64 offen offset:0 sc1 lds\n" \
M_"v[120:123], a[136:139], a[56:59], v[120:123]\n" \
R_"a[184:187], v49 offset:6144\n" \
R_"a[180:183], v49 offset:5120\n" \
M_"v[120:123], a[140:143], a[60:63], v[120:123]\n" \
B_"v50, s[8:11], s64 offen offset:1024 sc1 lds\n" \
M_"v[124:127], a[156:159], a[60:63], v[44:47]\n" \
R_"a[176:179], v49 offset:4096\n" \
R_"a[172:175], v49 offset:3072\n" \
M_"v[124:127], a[152:155], a[56:59], v[124:127]\n" \
R_"a[168:171], v49 offset:2048\n" \
R_"a[164:167], v49 offset:1024\n" \
M_"v[124:127], a[148:151], a[52:55], v[124:127]\n" \
R_"a[160:163], v49 offset:0\n" \
R_"a[0:3], v49 offset:8192\n" \
M_"v[124:127], a[144:147], a[48:51], v[124:127]\n" \
B_"v50, s[8:11], s64 offen offset:2048 sc1 lds\n" \
M_"v[32:35], a[124:127], a[64:67], 0\n" \
R_"a[4:7], v49 offset:9216\n" \
R_"a[8:11], v49 offset:10240\n" \
M_"v[36:39], a[124:127], a[80:83], 0\n" \
R_"a[12:15], v49 offset:11264\n" \
R_"a[16:19], v49 offset:12288\n" \
M_"v[40:43], a[124:127], a[192:195], 0\n" \
R_"a[20:23], v49 offset:13312\n" \
R_"a[24:27], v49 offset:14336\n" \
M_"v[44:47], a[124:127], a[208:211], 0\n" \
B_"v50, s[8:11], s64 offen offset:3072 sc1 lds\n" \
M_"v[32:35], a[224:227], a[68:71], v[32:35]\n" \
"s_xor_b32 m0, s65, 0xa000\n" \
R_"a[28:31], v49 offset:15360\n" \
M_"v[36:39], a[224:227], a[84:87], v[36:39]\n" \
E_"v120, v120\n" \
M_"v[40:43], a[224:227], a[196:199], v[40:43]\n" \
B_"v51, s[8:11], s64 offen offset:0 sc1 lds\n" \
M_"v[44:47], a[224:227], a[212:215], v[44:47]\n" \
E_"v121, v121\n" \
M_"v[32:35], a[228:231], a[72:75], v[32:35]\n" \
E_"v122, v122\n" \
M_"v[36:39], a[228:231], a[88:91], v[36:39]\n" \
E_"v123, v123\n" \
M_"v[40:43], a[228:231], a[200:203], v[40:43]\n" \
B_"v51, s[8:11], s64 offen offset:1024 sc1 lds\n" \
M_"v[44:47], a[228:231], a[216:219], v[44:47]\n" \
C_"v120, v120, v121\n" \
C_"v121, v122, v123\n" \
M_"v[32:35], a[232:235], a[76:79], v[32:35]\n" \
E_"v124, v124\n" \
M_"v[36:39], a[232:235], a[92:95], v[36:39]\n" \
E_"v125, v125\n" \
M_"v[40:43], a[232:235], a[204:207], v[40:43]\n" \
B_"v51, s[8:11], s64 offen offset:2048 sc1 lds\n" \
M_"v[44:47], a[232:235], a[220:223], v[44:47]\n" \
E_"v126, v126\n" \
"v_smfmac_f32_16x16x64_bf16 a[100:103], a[104:107], v[84:91], v21\n" \
C_"v122, v124, v125\n" \
"s_waitcnt lgkmcnt(1)\n" \
M_"v[220:223], a[188:191], v[84:87], v[220:223]\n" \
B_"v51, s[8:11], s64 offen offset:3072 sc1 lds\n" \
M_"v[252:255], a[188:191], v[88:91], v[252:255]\n" \
"s_add_u32 m0, s93, 0x1000\n" \
"v_and_b32_e32 v120, v12, v120\n" \
M_"v[216:219], a[184:187], v[84:87], v[216:219]\n" \
E_"v127, v127\n" \
M_"v[248:251], a[184:187], v[88:91], v[248:251]\n" \
C_"v123, v126, v127\n" \
"v_and_b32_e32 v121, v13, v121\n" \
M_"v[212:215], a[180:183], v[84:87], v[212:215]\n" \
B_"v4, s[80:83], s92 offen offset:0 lds\n" \
M_"v[244:247], a[180:183], v[88:91], v[244:247]\n" \
"v_and_b32_e32 v122, v14, v122\n" \
"v_and_b32_e32 v123, v15, v123\n" \
M_"v[208:211], a[176:179], v[84:87], v[208:211]\n" \
"v_accvgpr_read_b32 v56, a100\n" \
"v_accvgpr_read_b32 v24, a101\n" \
M_"v[240:243], a[176:179], v[88:91], v[240:243]\n" \
R_"a[128:131], v48 offset:0\n" \
R_"a[144:147], v48 offset:4096\n" \
M_"v[204:207], a[172:175], v[84:87], v[204:207]\n" \
B_"v5, s[80:83], s92 offen offset:1024 lds\n" \
M_"v[236:239], a[172:175], v[88:91], v[236:239]\n" \
R_"a[132:135], v48 offset:1024\n" \
R_"a[148:151], v48 offset:5120\n" \
M_"v[200:203], a[168:171], v[84:87], v[200:203]\n" \
R_"a[136:139], v48 offset:2048\n" \
R_"a[152:155], v48 offset:6144\n" \
M_"v[232:235], a[168:171], v[88:91], v[232:235]\n" \
B_"v6, s[80:83], s92 offen offset:2048 lds\n" \
M_"v[196:199], a[164:167], v[84:87], v[196:199]\n" \
R_"a[140:143], v48 offset:3072\n" \
R_"a[156:159], v48 offset:7168\n" \
M_"v[228:231], a[164:167], v[88:91], v[228:231]\n" \
"v_mul_f32_e32 v141, v54, v141\n" \
"v_mul_f32_e32 v142, v54, v142\n" \
M_"v[192:195], a[160:163], v[84:87], v[192:195]\n" \
"v_mul_f32_e32 v143, v54, v143\n" \
C_"v145, v146, v147\n" \
M_"v[224:227], a[160:163], v[88:91], v[224:227]\n" \
B_"v7, s[80:83], s92 offen offset:3072 lds\n" \
M_"a[100:103], a[116:119], v[120:123], a[100:103]\n" \
"v_mul_f32_e32 v148, v54, v148\n" \
"v_mul_f32_e32 v149, v54, v149\n" \
M_"v[224:227], a[0:3], v[120:123], v[224:227]\n" \
"v_mul_f32_e32 v150, v54, v150\n" \
"v_mul_f32_e32 v151, v54, v151\n" \
M_"v[228:231], a[4:7], v[120:123], v[228:231]\n" \
C_"v153, v154, v155\n" \
"v_mul_f32_e32 v156, v54, v156\n" \
M_"v[232:235], a[8:11], v[120:123], v[232:235]\n" \
"v_mul_f32_e32 v157, v54, v157\n" \
"v_mul_f32_e32 v158, v54, v158\n" \
M_"v[236:239], a[12:15], v[120:123], v[236:239]\n" \
"v_mul_f32_e32 v159, v54, v159\n" \
C_"v161, v162, v163\n" \
M_"v[240:243], a[16:19], v[120:123], v[240:243]\n" \
"v_mul_f32_e32 v164, v55, v164\n" \
"v_mul_f32_e32 v165, v55, v165\n" \
M_"v[244:247], a[20:23], v[120:123], v[244:247]\n" \
"v_mul_f32_e32 v166, v55, v166\n" \
"v_mul_f32_e32 v167, v55, v167\n" \
M_"v[248:251], a[24:27], v[120:123], v[248:251]\n" \
C_"v169, v170, v171\n" \
"s_waitcnt lgkmcnt(4)\n" \
M_"v[252:255], a[28:31], v[120:123], v[252:255]\n" \
"v_mul_f32_e32 v172, v55, v172\n" \
"v_mul_f32_e32 v173, v55, v173\n" \
"v_mul_f32_e32 v174, v55, v174\n" \
"v_mul_f32_e32 v175, v55, v175\n" \
C_"v177, v178, v179\n" \
"v_mul_f32_e32 v180, v55, v180\n" \
"v_mul_f32_e32 v181, v55, v181\n" \
"v_mul_f32_e32 v182, v55, v182\n" \
"v_mul_f32_e32 v183, v55, v183\n" \
C_"v185, v186, v187\n" \
"v_mul_f32_e32 v188, v55, v188\n" \
"v_mul_f32_e32 v189, v55, v189\n" \
"v_mul_f32_e32 v190, v55, v190\n" \
"v_mul_f32_e32 v191, v55, v191\n" \
C_"v130, v132, v133\n" \
C_"v131, v134, v135\n" \
"buffer_store_dwordx4 v[128:131], v20, s[56:59], s68 offen offset:0 sc1\n" \
C_"v138, v140, v141\n" \
C_"v139, v142, v143\n" \
"buffer_store_dwordx4 v[136:139], v20, s[56:59], s68 offen offset:1024 sc1\n" \
C_"v146, v148, v149\n" \
C_"v147, v150, v151\n" \
"buffer_store_dwordx4 v[144:147], v20, s[56:59], s68 offen offset:2048 sc1\n" \
C_"v154, v156, v157\n" \
C_"v155, v158, v159\n" \
"buffer_store_dwordx4 v[152:155], v20, s[56:59], s68 offen offset:3072 sc1\n" \
C_"v162, v164, v165\n" \
C_"v163, v166, v167\n" \
"buffer_store_dwordx4 v[160:163], v20, s[56:59], s69 offen offset:0 sc1\n" \
C_"v170, v172, v173\n" \
C_"v171, v174, v175\n" \
"buffer_store_dwordx4 v[168:171], v20, s[56:59], s69 offen offset:1024 sc1\n" \
C_"v178, v180, v181\n" \
C_"v179, v182, v183\n" \
"buffer_store_dwordx4 v[176:179], v20, s[56:59], s69 offen offset:2048 sc1\n" \
C_"v186, v188, v189\n" \
C_"v187, v190, v191\n" \
"buffer_store_dwordx4 v[184:187], v20, s[56:59], s69 offen offset:3072 sc1\n" \
"s_add_u32 m0, s93, 0x2000\n" \
M_"a[112:115], a[120:123], v[128:131], a[112:115]\n" \
B_"v8, s[80:83], s92 offen offset:0 lds\n" \
M_"a[112:115], a[120:123], v[136:139], a[112:115]\n" \
"v_mfma_f32_32x32x16_bf16 v[128:143], a[120:123], a[120:123], 0\n" \
M_"a[112:115], a[120:123], v[144:147], a[112:115]\n" \
M_"a[112:115], a[120:123], v[152:155], a[112:115]\n" \
"v_mfma_f32_32x32x16_bf16 v[144:159], a[120:123], a[120:123], 0\n" \
"v_add_f32_e32 v56, v56, v24\n" \
M_"a[112:115], a[120:123], v[160:163], a[112:115]\n" \
"v_max_f32_e32 v62, v62, v56\n" \
"v_accvgpr_read_b32 v57, a102\n" \
"s_waitcnt vmcnt(21) lgkmcnt(0)\n" \
"s_barrier\n" \
M_"v[64:67], a[128:131], a[64:67], v[32:35]\n" \
"s_xor_b32 m0, s65, 0x18000\n" \
"v_accvgpr_read_b32 v25, a103\n" \
M_"a[100:103], a[120:123], a[120:123], 0\n" \
B_"v52, s[8:11], s64 offen offset:0 sc1 lds\n" \
M_"v[64:67], a[132:135], a[68:71], v[64:67]\n" \
"v_rcp_f32_e32 v56, v56\n" \
M_"v[64:67], a[136:139], a[72:75], v[64:67]\n" \
M_"a[112:115], a[120:123], v[168:171], a[112:115]\n" \
"v_mfma_f32_32x32x16_bf16 v[160:175], a[120:123], a[120:123], 0\n" \
"v_mul_f32_e32 v56, v56, v60\n" \
M_"a[112:115], a[120:123], v[176:179], a[112:115]\n" \
"v_mul_f32_e32 v192, v56, v192\n" \
"v_mul_f32_e32 v193, v56, v193\n" \
M_"v[64:67], a[140:143], a[76:79], v[64:67]\n" \
B_"v52, s[8:11], s64 offen offset:1024 sc1 lds\n" \
M_"v[68:71], a[156:159], a[76:79], v[32:35]\n" \
C_"v192, v192, v193\n" \
"v_mul_f32_e32 v194, v56, v194\n" \
M_"v[68:71], a[152:155], a[72:75], v[68:71]\n" \
"v_mul_f32_e32 v195, v56, v195\n" \
C_"v193, v194, v195\n" \
M_"v[68:71], a[148:151], a[68:71], v[68:71]\n" \
M_"a[112:115], a[120:123], v[184:187], a[112:115]\n" \
"v_mfma_f32_32x32x16_bf16 v[176:191], a[120:123], a[120:123], 0\n" \
"v_mul_f32_e32 v196, v56, v196\n" \
"v_mul_f32_e32 v197, v56, v197\n" \
"v_mul_f32_e32 v198, v56, v198\n" \
"v_mul_f32_e32 v199, v56, v199\n" \
"v_mul_f32_e32 v200, v56, v200\n" \
"v_mul_f32_e32 v201, v56, v201\n" \
M_"v[68:71], a[144:147], a[64:67], v[68:71]\n" \
B_"v52, s[8:11], s64 offen offset:2048 sc1 lds\n" \
M_"v[76:79], a[144:147], a[80:83], v[36:39]\n" \
C_"v194, v196, v197\n" \
C_"v195, v198, v199\n" \
"buffer_store_dwordx4 v[192:195], v20, s[56:59], s70 offen offset:0 sc1\n" \
M_"v[76:79], a[148:151], a[84:87], v[76:79]\n" \
C_"v200, v200, v201\n" \
"v_mul_f32_e32 v202, v56, v202\n" \
M_"v[76:79], a[152:155], a[88:91], v[76:79]\n" \
"v_mul_f32_e32 v203, v56, v203\n" \
"v_add_f32_e32 v57, v57, v25\n" \
M_"v[76:79], a[156:159], a[92:95], v[76:79]\n" \
B_"v52, s[8:11], s64 offen offset:3072 sc1 lds\n" \
M_"v[72:75], a[140:143], a[92:95], v[36:39]\n" \
"s_mov_b32 m0, s65\n" \
C_"v201, v202, v203\n" \
M_"v[72:75], a[136:139], a[88:91], v[72:75]\n" \
M_"a[112:115], a[120:123], v[192:195], a[112:115]\n" \
"v_mul_f32_e32 v204, v56, v204\n" \
"v_mul_f32_e32 v205, v56, v205\n" \
M_"v[72:75], a[132:135], a[84:87], v[72:75]\n" \
"v_mul_f32_e32 v206, v56, v206\n" \
"v_mul_f32_e32 v207, v56, v207\n" \
M_"v[72:75], a[128:131], a[80:83], v[72:75]\n" \
B_"v53, s[8:11], s64 offen offset:0 sc1 lds\n" \
M_"v[80:83], a[128:131], a[192:195], v[40:43]\n" \
"v_mul_f32_e32 v208, v56, v208\n" \
"v_mul_f32_e32 v209, v56, v209\n" \
M_"v[80:83], a[132:135], a[196:199], v[80:83]\n" \
"v_max_f32_e32 v62, v62, v57\n" \
C_"v202, v204, v205\n" \
M_"v[80:83], a[136:139], a[200:203], v[80:83]\n" \
B_"v53, s[8:11], s64 offen offset:1024 sc1 lds\n" \
M_"v[80:83], a[140:143], a[204:207], v[80:83]\n" \
C_"v203, v206, v207\n" \
C_"v208, v208, v209\n" \
"buffer_store_dwordx4 v[200:203], v20, s[56:59], s70 offen offset:1024 sc1\n" \
M_"v[84:87], a[156:159], a[204:207], v[40:43]\n" \
"v_mul_f32_e32 v210, v56, v210\n" \
"v_mul_f32_e32 v211, v56, v211\n" \
M_"v[84:87], a[152:155], a[200:203], v[84:87]\n" \
"v_rcp_f32_e32 v57, v57\n" \
M_"v[84:87], a[148:151], a[196:199], v[84:87]\n" \
B_"v53, s[8:11], s64 offen offset:2048 sc1 lds\n" \
M_"v[84:87], a[144:147], a[192:195], v[84:87]\n" \
M_"a[112:115], a[120:123], v[200:203], a[112:115]\n" \
"v_mfma_f32_32x32x16_bf16 v[192:207], a[120:123], a[120:123], 0\n" \
C_"v209, v210, v211\n" \
"v_mul_f32_e32 v212, v56, v212\n" \
".fill (L_ab + 20480 - .) / 4, 4, 0xBF800000\n" \
"L_ap5:\n" \
"s_cbranch_execz L_ap0\n" \
"v_mul_f32_e32 v213, v56, v213\n" \
"v_mul_f32_e32 v214, v56, v214\n" \
"v_mul_f32_e32 v215, v56, v215\n" \
"v_mul_f32_e32 v216, v56, v216\n" \
M_"v[92:95], a[144:147], a[208:211], v[44:47]\n" \
R_"a[144:147], v48 offset:12288\n" \
"v_mul_f32_e32 v217, v56, v217\n" \
M_"v[92:95], a[148:151], a[212:215], v[92:95]\n" \
R_"a[148:151], v48 offset:13312\n" \
C_"v210, v212, v213\n" \
M_"v[92:95], a[152:155], a[216:219], v[92:95]\n" \
R_"a[152:155], v48 offset:14336\n" \
C_"v211, v214, v215\n" \
"buffer_store_dwordx4 v[208:211], v20, s[56:59], s70 offen offset:2048 sc1\n" \
M_"v[92:95], a[156:159], a[220:223], v[92:95]\n" \
R_"a[156:159], v48 offset:15360\n" \
C_"v216, v216, v217\n" \
M_"v[88:91], a[140:143], a[220:223], v[44:47]\n" \
R_"a[140:143], v48 offset:11264\n" \
"v_mul_f32_e32 v218, v56, v218\n" \
M_"v[88:91], a[136:139], a[216:219], v[88:91]\n" \
R_"a[136:139], v48 offset:10240\n" \
"v_mul_f32_e32 v219, v56, v219\n" \
M_"v[88:91], a[132:135], a[212:215], v[88:91]\n" \
R_"a[132:135], v48 offset:9216\n" \
"v_mul_f32_e32 v57, v57, v61\n" \
M_"v[88:91], a[128:131], a[208:211], v[88:91]\n" \
R_"a[128:131], v48 offset:8192\n" \
M_"a[112:115], a[120:123], v[208:211], a[112:115]\n" \
C_"v217, v218, v219\n" \
"v_mul_f32_e32 v220, v56, v220\n" \
"v_mul_f32_e32 v221, v56, v221\n" \
"v_mul_f32_e32 v222, v56, v222\n" \
"v_mul_f32_e32 v223, v56, v223\n" \
"v_mul_f32_e32 v224, v57, v224\n" \
"v_mul_f32_e32 v225, v57, v225\n" \
C_"v218, v220, v221\n" \
C_"v219, v222, v223\n" \
"buffer_store_dwordx4 v[216:219], v20, s[56:59], s70 offen offset:3072 sc1\n" \
C_"v224, v224, v225\n" \
"v_mul_f32_e32 v226, v57, v226\n" \
"v_mul_f32_e32 v227, v57, v227\n" \
M_"a[112:115], a[120:123], v[216:219], a[112:115]\n" \
"v_mfma_f32_32x32x16_bf16 v[208:223], a[120:123], a[120:123], 0\n" \
C_"v225, v226, v227\n" \
"v_mul_f32_e32 v228, v57, v228\n" \
"v_mul_f32_e32 v229, v57, v229\n" \
"v_mul_f32_e32 v230, v57, v230\n" \
"v_mul_f32_e32 v231, v57, v231\n" \
"v_mul_f32_e32 v232, v57, v232\n" \
"v_mul_f32_e32 v233, v57, v233\n" \
C_"v226, v228, v229\n" \
C_"v227, v230, v231\n" \
"buffer_store_dwordx4 v[224:227], v20, s[56:59], s71 offen offset:0 sc1\n" \
C_"v232, v232, v233\n" \
"v_mul_f32_e32 v234, v57, v234\n" \
"v_mul_f32_e32 v235, v57, v235\n" \
M_"a[112:115], a[120:123], v[224:227], a[112:115]\n" \
C_"v233, v234, v235\n" \
"v_mul_f32_e32 v236, v57, v236\n" \
"v_mul_f32_e32 v237, v57, v237\n" \
"v_mul_f32_e32 v238, v57, v238\n" \
"v_mul_f32_e32 v239, v57, v239\n" \
"v_mul_f32_e32 v240, v57, v240\n" \
"v_mul_f32_e32 v241, v57, v241\n" \
C_"v234, v236, v237\n" \
C_"v235, v238, v239\n" \
"buffer_store_dwordx4 v[232:235], v20, s[56:59], s71 offen offset:1024 sc1\n" \
C_"v240, v240, v241\n" \
"v_mul_f32_e32 v242, v57, v242\n" \
"v_mul_f32_e32 v243, v57, v243\n" \
M_"a[112:115], a[120:123], v[232:235], a[112:115]\n" \
"v_mfma_f32_32x32x16_bf16 v[224:239], a[120:123], a[120:123], 0\n" \
C_"v241, v242, v243\n" \
"v_mul_f32_e32 v244, v57, v244\n" \
"v_mul_f32_e32 v245, v57, v245\n" \
"v_mul_f32_e32 v246, v57, v246\n" \
"v_mul_f32_e32 v247, v57, v247\n" \
"v_mul_f32_e32 v248, v57, v248\n" \
"v_mul_f32_e32 v249, v57, v249\n" \
C_"v242, v244, v245\n" \
C_"v243, v246, v247\n" \
"buffer_store_dwordx4 v[240:243], v20, s[56:59], s71 offen offset:2048 sc1\n" \
C_"v248, v248, v249\n" \
"v_mul_f32_e32 v250, v57, v250\n" \
"v_mul_f32_e32 v251, v57, v251\n" \
M_"a[112:115], a[120:123], v[240:243], a[112:115]\n" \
C_"v249, v250, v251\n" \
"v_mul_f32_e32 v252, v57, v252\n" \
"v_mul_f32_e32 v253, v57, v253\n" \
"v_mul_f32_e32 v254, v57, v254\n" \
"v_mul_f32_e32 v255, v57, v255\n" \
C_"v250, v252, v253\n" \
C_"v251, v254, v255\n" \
"buffer_store_dwordx4 v[248:251], v20, s[56:59], s71 offen offset:3072 sc1\n" \
B_"v53, s[8:11], s64 offen offset:3072 sc1 lds\n" \
"s_waitcnt vmcnt(16) lgkmcnt(0)\n" \
"s_barrier\n" \
M_"a[112:115], a[120:123], v[248:251], a[112:115]\n" \
"v_mfma_f32_32x32x16_bf16 v[240:255], a[120:123], a[120:123], 0\n" \
E_"v70, v70\n" \
E_"v71, v71\n" \
E_"v72, v72\n" \
E_"v73, v73\n" \
"s_add_u32 s64, s64, 0x10000\n" \
"v_cmp_lt_f32_e64 s[72:73], s21, v62\n" \
"v_readlane_b32 s95, v11, 3\n" \
"v_accvgpr_read_b32 v63, a112\n" \
"v_cmp_u_f32_e32 vcc, v63, v63\n" \
E_"v68, v68\n" \
E_"v69, v69\n" \
E_"v74, v74\n" \
E_"v75, v75\n" \
"s_or_b64 vcc, s[72:73], vcc\n" \
"s_cselect_b32 s95, 0x40000000, s95\n" \
"s_cselect_b32 s94, 0x7f800000, 0\n" \
C_"v71, v70, v71\n" \
E_"v66, v66\n" \
E_"v67, v67\n" \
E_"v64, v64\n" \
E_"v65, v65\n" \
C_"v72, v72, v73\n" \
E_"v76, v76\n" \
E_"v77, v77\n" \
E_"v78, v78\n" \
E_"v79, v79\n" \
C_"v70, v68, v69\n" \
C_"v73, v74, v75\n" \
"v_add_u32_e32 v49, 0x10000, v48\n" \
"s_xor_b32 s65, s65, 0x8000\n" \
C_"v69, v66, v67\n" \
C_"v68, v64, v65\n" \
C_"v74, v76, v77\n" \
C_"v75, v78, v79\n" \
"v_xor_b32_e32 v48, 0x8000, v48\n" \
"s_sub_u32 s66, s19, 2\n" \
"s_branch L_2_%=\n" \
"L_f1_%=:\n" \
R_"a[160:163], v49 offset:0\n" \
R_"a[164:167], v49 offset:1024\n" \
R_"a[168:171], v49 offset:2048\n" \
M_"v[104:107], a[128:131], a[80:83], v[36:39]\n" \
C_"v192, v192, v193\n" \
C_"v224, v224, v225\n" \
C_"v193, v194, v195\n" \
C_"v225, v226, v227\n" \
R_"a[172:175], v49 offset:3072\n" \
C_"v194, v196, v197\n" \
C_"v195, v198, v199\n" \
C_"v226, v228, v229\n" \
C_"v227, v230, v231\n" \
M_"v[104:107], a[132:135], a[84:87], v[104:107]\n" \
"v_lshl_add_u32 v50, s15, 2, v20\n" \
C_"v196, v200, v201\n" \
C_"v197, v202, v203\n" \
C_"v198, v204, v205\n" \
C_"v199, v206, v207\n" \
C_"v228, v232, v233\n" \
C_"v229, v234, v235\n" \
C_"v230, v236, v237\n" \
C_"v231, v238, v239\n" \
"v_add_u32_e32 v50, 0x1a000, v50\n" \
"v_mov_b32_e32 v61, s62\n" \
C_"v200, v208, v209\n" \
C_"v201, v210, v211\n" \
C_"v202, v212, v213\n" \
C_"v203, v214, v215\n" \
C_"v204, v216, v217\n" \
C_"v205, v218, v219\n" \
C_"v206, v220, v221\n" \
C_"v207, v222, v223\n" \
C_"v232, v240, v241\n" \
C_"v233, v242, v243\n" \
C_"v234, v244, v245\n" \
C_"v235, v246, v247\n" \
C_"v236, v248, v249\n" \
C_"v237, v250, v251\n" \
C_"v238, v252, v253\n" \
C_"v239, v254, v255\n" \
"ds_write_b128 v50, v[192:195] offset:0\n" \
M_"v[104:107], a[136:139], a[88:91], v[104:107]\n" \
R_"a[176:179], v49 offset:4096\n" \
"v_add_u32_e32 v56, 0x49000, v20\n" \
M_"v[104:107], a[140:143], a[92:95], v[104:107]\n" \
"ds_write_b128 v50, v[196:199] offset:1024\n" \
M_"v[108:111], a[156:159], a[92:95], v[36:39]\n" \
R_"a[180:183], v49 offset:5120\n" \
R_"a[184:187], v49 offset:6144\n" \
M_"v[108:111], a[152:155], a[88:91], v[108:111]\n" \
"ds_write_b128 v50, v[200:203] offset:2048\n" \
M_"v[108:111], a[148:151], a[84:87], v[108:111]\n" \
"v_and_b32_e32 v68, v12, v68\n" \
"v_and_b32_e32 v69, v13, v69\n" \
M_"v[108:111], a[144:147], a[80:83], v[108:111]\n" \
"ds_write_b128 v50, v[204:207] offset:3072\n" \
"v_and_b32_e32 v70, v14, v70\n" \
"v_and_b32_e32 v71, v15, v71\n" \
R_"a[188:191], v49 offset:7168\n" \
R_"a[0:3], v20 offset:8192\n" \
"s_waitcnt lgkmcnt(9)\n" \
M_"v[128:131], a[160:163], v[68:71], v[128:131]\n" \
"ds_write_b128 v50, v[224:227] offset:4096\n" \
M_"v[160:163], a[160:163], v[72:75], v[160:163]\n" \
R_"a[4:7], v20 offset:9216\n" \
R_"a[8:11], v20 offset:10240\n" \
M_"v[132:135], a[164:167], v[68:71], v[132:135]\n" \
"ds_write_b128 v50, v[228:231] offset:5120\n" \
M_"v[164:167], a[164:167], v[72:75], v[164:167]\n" \
R_"a[12:15], v20 offset:11264\n" \
R_"a[28:31], v20 offset:15360\n" \
M_"v[136:139], a[168:171], v[68:71], v[136:139]\n" \
"ds_write_b128 v50, v[232:235] offset:6144\n" \
M_"v[168:171], a[168:171], v[72:75], v[168:171]\n" \
R_"a[24:27], v20 offset:14336\n" \
R_"a[20:23], v20 offset:13312\n" \
M_"v[140:143], a[172:175], v[68:71], v[140:143]\n" \
"ds_write_b128 v50, v[236:239] offset:7168\n" \
"s_waitcnt vmcnt(0)\n" \
"s_mov_b64 exec, 1\n" \
"buffer_store_dword v61, off, s[56:59], s95 offset:0 sc1\n" \
"s_mov_b64 exec, -1\n" \
M_"v[172:175], a[172:175], v[72:75], v[172:175]\n" \
R_"a[16:19], v20 offset:12288\n" \
"s_waitcnt lgkmcnt(15)\n" \
M_"v[144:147], a[176:179], v[68:71], v[144:147]\n" \
"v_xor_b32_e32 v48, 0x4000, v49\n" \
"ds_read_b32 v58, v18 offset:256\n" \
M_"v[176:179], a[176:179], v[72:75], v[176:179]\n" \
"ds_read_b32 v59, v18 offset:384\n" \
R_"a[32:35], v48 offset:8192\n" \
B_"a[192:195], v20, s[56:59], s60 offen offset:0 sc1\n" \
M_"v[148:151], a[180:183], v[68:71], v[148:151]\n" \
R_"a[36:39], v48 offset:9216\n" \
R_"a[40:43], v48 offset:10240\n" \
M_"v[180:183], a[180:183], v[72:75], v[180:183]\n" \
R_"a[44:47], v48 offset:11264\n" \
R_"a[48:51], v48 offset:12288\n" \
M_"v[152:155], a[184:187], v[68:71], v[152:155]\n" \
R_"a[52:55], v48 offset:13312\n" \
R_"a[56:59], v48 offset:14336\n" \
M_"v[184:187], a[184:187], v[72:75], v[184:187]\n" \
R_"a[60:63], v48 offset:15360\n" \
B_"a[208:211], v56, s[56:59], s60 offen offset:0 sc1\n" \
"s_waitcnt lgkmcnt(15)\n" \
M_"v[156:159], a[188:191], v[68:71], v[156:159]\n" \
R_"a[128:131], v49 offset:8192\n" \
R_"a[132:135], v49 offset:9216\n" \
M_"v[188:191], a[188:191], v[72:75], v[188:191]\n" \
R_"a[136:139], v49 offset:10240\n" \
R_"a[140:143], v49 offset:11264\n" \
"v_smfmac_f32_16x16x64_bf16 a[96:99], a[104:107], v[68:75], v21\n" \
R_"a[144:147], v49 offset:12288\n" \
R_"a[148:151], v49 offset:13312\n" \
B_"a[196:199], v20, s[56:59], s60 offen offset:1024 sc1\n" \
M_"v[112:115], a[0:3], a[64:67], v[32:35]\n" \
R_"a[152:155], v49 offset:14336\n" \
R_"a[156:159], v49 offset:15360\n" \
M_"v[112:115], a[4:7], a[68:71], v[112:115]\n" \
"v_accvgpr_read_b32 v40, a100\n" \
"v_accvgpr_read_b32 v24, a101\n" \
M_"v[112:115], a[8:11], a[72:75], v[112:115]\n" \
"v_accvgpr_read_b32 v41, a102\n" \
"v_accvgpr_read_b32 v25, a103\n" \
M_"v[112:115], a[12:15], a[76:79], v[112:115]\n" \
B_"a[212:215], v56, s[56:59], s60 offen offset:1024 sc1\n" \
M_"v[116:119], a[28:31], a[76:79], v[32:35]\n" \
"v_add_f32_e32 v40, v40, v24\n" \
"s_waitcnt lgkmcnt(15)\n" \
M_"v[116:119], a[24:27], a[72:75], v[116:119]\n" \
"v_add_f32_e32 v41, v41, v25\n" \
"v_add_f32_e32 v40, s94, v40\n" \
M_"v[116:119], a[20:23], a[68:71], v[116:119]\n" \
"v_add_f32_e32 v41, s94, v41\n" \
"ds_write_b64 v19, v[40:41]\n" \
M_"v[116:119], a[16:19], a[64:67], v[116:119]\n" \
B_"a[200:203], v20, s[56:59], s60 offen offset:2048 sc1\n" \
M_"v[124:127], a[16:19], a[80:83], v[36:39]\n" \
"v_add_u32_e32 v52, s15, v20\n" \
"v_add_u32_e32 v51, 0x10000, v52\n" \
M_"v[124:127], a[20:23], a[84:87], v[124:127]\n" \
E_"v104, v104\n" \
M_"v[124:127], a[24:27], a[88:91], v[124:127]\n" \
E_"v105, v105\n" \
M_"v[124:127], a[28:31], a[92:95], v[124:127]\n" \
B_"a[216:219], v56, s[56:59], s60 offen offset:2048 sc1\n" \
M_"v[120:123], a[12:15], a[92:95], v[36:39]\n" \
E_"v106, v106\n" \
M_"v[120:123], a[8:11], a[88:91], v[120:123]\n" \
E_"v107, v107\n" \
M_"v[120:123], a[4:7], a[84:87], v[120:123]\n" \
C_"v104, v104, v105\n" \
C_"v105, v106, v107\n" \
M_"v[120:123], a[0:3], a[80:83], v[120:123]\n" \
B_"a[204:207], v20, s[56:59], s60 offen offset:3072 sc1\n" \
E_"v108, v108\n" \
E_"v109, v109\n" \
"s_waitcnt lgkmcnt(0)\n" \
"s_barrier\n" \
R_"v[208:211], v52 offset:0\n" \
R_"v[212:215], v52 offset:1024\n" \
R_"v[216:219], v52 offset:2048\n" \
R_"v[220:223], v52 offset:3072\n" \
B_"a[220:223], v56, s[56:59], s60 offen offset:3072 sc1\n" \
R_"v[240:243], v51 offset:32768\n" \
R_"v[244:247], v51 offset:33792\n" \
R_"v[248:251], v51 offset:34816\n" \
R_"v[252:255], v51 offset:35840\n" \
E_"v110, v110\n" \
E_"v111, v111\n" \
C_"v106, v108, v109\n" \
C_"v107, v110, v111\n" \
"v_and_b32_e32 v104, v12, v104\n" \
"v_and_b32_e32 v105, v13, v105\n" \
"v_and_b32_e32 v106, v14, v106\n" \
"v_and_b32_e32 v107, v15, v107\n" \
"v_xor_b32_e32 v63, 0x400, v19\n" \
"ds_read_b64 v[42:43], v63\n" \
M_"a[96:99], a[116:119], v[104:107], a[96:99]\n" \
"v_mov_b32_e32 v194, v28\n" \
"v_mov_b32_e32 v195, v29\n" \
M_"v[160:163], a[128:131], v[104:107], v[160:163]\n" \
"v_mov_b32_e32 v196, 0\n" \
"v_mov_b32_e32 v197, 0\n" \
M_"v[164:167], a[132:135], v[104:107], v[164:167]\n" \
"v_mov_b32_e32 v192, 0\n" \
"v_mov_b32_e32 v193, 0\n" \
M_"v[168:171], a[136:139], v[104:107], v[168:171]\n" \
"v_readlane_b32 s93, v11, 25\n" \
"v_readlane_b32 s92, v11, 30\n" \
M_"v[172:175], a[140:143], v[104:107], v[172:175]\n" \
E_"v118, v118\n" \
M_"v[176:179], a[144:147], v[104:107], v[176:179]\n" \
E_"v119, v119\n" \
M_"v[180:183], a[148:151], v[104:107], v[180:183]\n" \
"s_add_u32 m0, s93, 0x0\n" \
C_"v119, v118, v119\n" \
M_"v[184:187], a[152:155], v[104:107], v[184:187]\n" \
E_"v120, v120\n" \
M_"v[188:191], a[156:159], v[104:107], v[188:191]\n" \
E_"v121, v121\n" \
"s_waitcnt lgkmcnt(1)\n" \
"s_barrier\n" \
M_"v[128:131], v[194:197], v[208:211], v[128:131]\n" \
B_"v0, s[80:83], s92 offen offset:0 lds\n" \
M_"v[132:135], v[192:195], v[208:211], v[132:135]\n" \
E_"v116, v116\n" \
M_"v[136:139], v[194:197], v[212:215], v[136:139]\n" \
E_"v117, v117\n" \
M_"v[140:143], v[192:195], v[212:215], v[140:143]\n" \
E_"v122, v122\n" \
M_"v[144:147], v[194:197], v[216:219], v[144:147]\n" \
B_"v1, s[80:83], s92 offen offset:1024 lds\n" \
M_"v[148:151], v[192:195], v[216:219], v[148:151]\n" \
E_"v123, v123\n" \
M_"v[152:155], v[194:197], v[220:223], v[152:155]\n" \
E_"v114, v114\n" \
M_"v[156:159], v[192:195], v[220:223], v[156:159]\n" \
E_"v115, v115\n" \
M_"v[160:163], v[194:197], v[240:243], v[160:163]\n" \
B_"v2, s[80:83], s92 offen offset:2048 lds\n" \
M_"v[164:167], v[192:195], v[240:243], v[164:167]\n" \
E_"v112, v112\n" \
M_"v[168:171], v[194:197], v[244:247], v[168:171]\n" \
E_"v113, v113\n" \
M_"v[172:175], v[192:195], v[244:247], v[172:175]\n" \
C_"v120, v120, v121\n" \
C_"v118, v116, v117\n" \
M_"v[176:179], v[194:197], v[248:251], v[176:179]\n" \
B_"v3, s[80:83], s92 offen offset:3072 lds\n" \
"s_add_u32 m0, s93, 0x1000\n" \
M_"v[180:183], v[192:195], v[248:251], v[180:183]\n" \
E_"v124, v124\n" \
M_"v[184:187], v[194:197], v[252:255], v[184:187]\n" \
E_"v125, v125\n" \
M_"v[188:191], v[192:195], v[252:255], v[188:191]\n" \
E_"v126, v126\n" \
B_"v4, s[80:83], s92 offen offset:0 lds\n" \
E_"v127, v127\n" \
C_"v121, v122, v123\n" \
C_"v117, v114, v115\n" \
C_"v116, v112, v113\n" \
C_"v122, v124, v125\n" \
C_"v123, v126, v127\n" \
"s_nop 1\n" \
"v_smfmac_f32_16x16x64_bf16 a[96:99], a[104:107], v[116:123], v21\n" \
M_"v[128:131], a[32:35], v[116:119], v[128:131]\n" \
B_"v5, s[80:83], s92 offen offset:1024 lds\n" \
M_"v[160:163], a[32:35], v[120:123], v[160:163]\n" \
M_"v[132:135], a[36:39], v[116:119], v[132:135]\n" \
M_"v[164:167], a[36:39], v[120:123], v[164:167]\n" \
M_"v[136:139], a[40:43], v[116:119], v[136:139]\n" \
B_"v6, s[80:83], s92 offen offset:2048 lds\n" \
M_"v[168:171], a[40:43], v[120:123], v[168:171]\n" \
"v_accvgpr_read_b32 v54, a96\n" \
"v_accvgpr_read_b32 v22, a97\n" \
M_"v[140:143], a[44:47], v[116:119], v[140:143]\n" \
"v_add_f32_e32 v54, v54, v22\n" \
"s_waitcnt lgkmcnt(0)\n" \
"v_add_f32_e32 v54, v54, v42\n" \
M_"v[172:175], a[44:47], v[120:123], v[172:175]\n" \
"v_max_f32_e32 v62, v62, v54\n" \
"v_accvgpr_read_b32 v55, a98\n" \
B_"v7, s[80:83], s92 offen offset:3072 lds\n" \
"s_add_u32 m0, s93, 0x2000\n" \
M_"v[144:147], a[48:51], v[116:119], v[144:147]\n" \
"v_rcp_f32_e32 v54, v54\n" \
M_"v[176:179], a[48:51], v[120:123], v[176:179]\n" \
"v_mul_f32_e32 v54, v54, v58\n" \
"v_mul_f32_e32 v128, v54, v128\n" \
M_"v[148:151], a[52:55], v[116:119], v[148:151]\n" \
"v_mul_f32_e32 v129, v54, v129\n" \
"v_mul_f32_e32 v130, v54, v130\n" \
B_"v8, s[80:83], s92 offen offset:0 lds\n" \
M_"v[180:183], a[52:55], v[120:123], v[180:183]\n" \
"v_mul_f32_e32 v131, v54, v131\n" \
"v_mul_f32_e32 v132, v54, v132\n" \
"v_mul_f32_e32 v133, v54, v133\n" \
"v_mul_f32_e32 v134, v54, v134\n" \
"v_mul_f32_e32 v135, v54, v135\n" \
C_"v64, v128, v129\n" \
C_"v65, v130, v131\n" \
C_"v66, v132, v133\n" \
C_"v67, v134, v135\n" \
"v_accvgpr_read_b32 v23, a99\n" \
M_"v[152:155], a[56:59], v[116:119], v[152:155]\n" \
M_"a[112:115], a[120:123], v[64:67], a[112:115]\n" \
"v_mul_f32_e32 v136, v54, v136\n" \
"v_mul_f32_e32 v137, v54, v137\n" \
"v_mul_f32_e32 v138, v54, v138\n" \
"v_mul_f32_e32 v139, v54, v139\n" \
"v_mul_f32_e32 v140, v54, v140\n" \
"v_mul_f32_e32 v141, v54, v141\n" \
"v_mul_f32_e32 v142, v54, v142\n" \
"v_mul_f32_e32 v143, v54, v143\n" \
"v_add_f32_e32 v55, v55, v23\n" \
C_"v68, v136, v137\n" \
C_"v69, v138, v139\n" \
C_"v70, v140, v141\n" \
C_"v71, v142, v143\n" \
"v_add_f32_e32 v55, v55, v43\n" \
"v_mul_f32_e32 v144, v54, v144\n" \
M_"a[112:115], a[120:123], v[68:71], a[112:115]\n" \
"v_mul_f32_e32 v145, v54, v145\n" \
"v_mul_f32_e32 v146, v54, v146\n" \
"v_mul_f32_e32 v147, v54, v147\n" \
"v_mul_f32_e32 v148, v54, v148\n" \
"v_mul_f32_e32 v149, v54, v149\n" \
"v_mul_f32_e32 v150, v54, v150\n" \
"v_mul_f32_e32 v151, v54, v151\n" \
"v_max_f32_e32 v62, v62, v55\n" \
C_"v72, v144, v145\n" \
C_"v73, v146, v147\n" \
C_"v74, v148, v149\n" \
C_"v75, v150, v151\n" \
"v_rcp_f32_e32 v55, v55\n" \
M_"v[184:187], a[56:59], v[120:123], v[184:187]\n" \
M_"a[112:115], a[120:123], v[72:75], a[112:115]\n" \
"v_mul_f32_e32 v152, v54, v152\n" \
"v_mul_f32_e32 v153, v54, v153\n" \
"v_mul_f32_e32 v154, v54, v154\n" \
"v_mul_f32_e32 v155, v54, v155\n" \
C_"v76, v152, v153\n" \
C_"v77, v154, v155\n" \
"v_mul_f32_e32 v55, v55, v59\n" \
"v_mul_f32_e32 v160, v55, v160\n" \
"v_mul_f32_e32 v161, v55, v161\n" \
"v_mul_f32_e32 v162, v55, v162\n" \
"v_mul_f32_e32 v163, v55, v163\n" \
"v_mul_f32_e32 v164, v55, v164\n" \
"v_mul_f32_e32 v165, v55, v165\n" \
"v_mul_f32_e32 v166, v55, v166\n" \
"v_mul_f32_e32 v167, v55, v167\n" \
C_"v80, v160, v161\n" \
C_"v81, v162, v163\n" \
C_"v82, v164, v165\n" \
C_"v83, v166, v167\n" \
M_"v[156:159], a[60:63], v[116:119], v[156:159]\n" \
"v_mul_f32_e32 v168, v55, v168\n" \
"v_mul_f32_e32 v169, v55, v169\n" \
"v_mul_f32_e32 v170, v55, v170\n" \
"v_mul_f32_e32 v171, v55, v171\n" \
"v_mul_f32_e32 v172, v55, v172\n" \
"v_mul_f32_e32 v173, v55, v173\n" \
"v_mul_f32_e32 v174, v55, v174\n" \
"v_mul_f32_e32 v175, v55, v175\n" \
"v_mul_f32_e32 v156, v54, v156\n" \
"v_mul_f32_e32 v157, v54, v157\n" \
"v_mul_f32_e32 v158, v54, v158\n" \
"v_mul_f32_e32 v159, v54, v159\n" \
C_"v78, v156, v157\n" \
C_"v79, v158, v159\n" \
C_"v84, v168, v169\n" \
C_"v85, v170, v171\n" \
M_"a[112:115], a[120:123], v[76:79], a[112:115]\n" \
M_"a[112:115], a[120:123], v[80:83], a[112:115]\n" \
C_"v86, v172, v173\n" \
C_"v87, v174, v175\n" \
"v_mul_f32_e32 v176, v55, v176\n" \
"v_mul_f32_e32 v177, v55, v177\n" \
M_"a[112:115], a[120:123], v[84:87], a[112:115]\n" \
"v_mul_f32_e32 v178, v55, v178\n" \
"v_mul_f32_e32 v179, v55, v179\n" \
"v_mul_f32_e32 v180, v55, v180\n" \
"v_mul_f32_e32 v181, v55, v181\n" \
"v_mul_f32_e32 v182, v55, v182\n" \
"v_mul_f32_e32 v183, v55, v183\n" \
C_"v96, v176, v177\n" \
C_"v97, v178, v179\n" \
C_"v98, v180, v181\n" \
C_"v99, v182, v183\n" \
M_"v[188:191], a[60:63], v[120:123], v[188:191]\n" \
"v_mul_f32_e32 v184, v55, v184\n" \
M_"a[112:115], a[120:123], v[96:99], a[112:115]\n" \
"v_mul_f32_e32 v185, v55, v185\n" \
"v_mul_f32_e32 v186, v55, v186\n" \
"v_mul_f32_e32 v187, v55, v187\n" \
C_"v100, v184, v185\n" \
C_"v101, v186, v187\n" \
"v_cmp_lt_f32_e64 s[72:73], s21, v62\n" \
"v_mul_f32_e32 v188, v55, v188\n" \
"v_mul_f32_e32 v189, v55, v189\n" \
"v_mul_f32_e32 v190, v55, v190\n" \
"v_mul_f32_e32 v191, v55, v191\n" \
C_"v102, v188, v189\n" \
C_"v103, v190, v191\n" \
"s_nop 1\n" \
M_"a[112:115], a[120:123], v[100:103], a[112:115]\n" \
"s_nop 7\n" \
"v_accvgpr_read_b32 v63, a112\n" \
"v_cmp_u_f32_e32 vcc, v63, v63\n" \
"s_nop 0\n" \
"s_or_b64 vcc, s[72:73], vcc\n" \
"s_cbranch_scc1 L_end_%=\n" \
"s_branch L_out_%=\n" \
".fill (L_ab + 24576 - .) / 4, 4, 0xBF800000\n" \
".fill (L_ab + 24576 - .) / 4, 4, 0xBF800000\n" \
"L_ap6:\n" \
"s_setpc_b64 s[88:89]\n" \
"L_gs7:\n" \
M_"v[104:107], a[128:131], a[16:19], v[36:39]\n" \
"s_add_u32 s68, s60, 0xfff6e000\n" \
"s_add_u32 s69, s60, 0xfffb7000\n" \
"s_add_u32 s70, s60, 0x0\n" \
"s_add_u32 s71, s60, 0x49000\n" \
M_"v[104:107], a[132:135], a[20:23], v[104:107]\n" \
"s_xor_b32 m0, s65, 0x12000\n" \
R_"a[160:163], v49 offset:0\n" \
M_"v[104:107], a[136:139], a[24:27], v[104:107]\n" \
B_"v50, s[8:11], s64 offen offset:0 sc1 lds\n" \
M_"v[104:107], a[140:143], a[28:31], v[104:107]\n" \
R_"a[164:167], v49 offset:1024\n" \
R_"a[168:171], v49 offset:2048\n" \
M_"v[108:111], a[156:159], a[28:31], v[36:39]\n" \
R_"a[172:175], v49 offset:3072\n" \
R_"a[176:179], v49 offset:4096\n" \
M_"v[108:111], a[152:155], a[24:27], v[108:111]\n" \
B_"v50, s[8:11], s64 offen offset:1024 sc1 lds\n" \
M_"v[108:111], a[148:151], a[20:23], v[108:111]\n" \
"s_mov_b32 s65, s13\n" \
"v_add_u32_e32 v63, s15, v20\n" \
M_"v[108:111], a[144:147], a[16:19], v[108:111]\n" \
"s_add_u32 s67, s14, 40960\n" \
"v_add_u32_e32 v50, 0x6000, v63\n" \
M_"v[116:119], a[144:147], a[32:35], v[40:43]\n" \
"s_xor_b32 m0, s65, 0x8000\n" \
R_"a[180:183], v49 offset:5120\n" \
M_"v[116:119], a[148:151], a[36:39], v[116:119]\n" \
R_"a[184:187], v49 offset:6144\n" \
"v_and_b32_e32 v68, v12, v68\n" \
M_"v[116:119], a[152:155], a[40:43], v[116:119]\n" \
"v_and_b32_e32 v69, v13, v69\n" \
"v_and_b32_e32 v70, v14, v70\n" \
M_"v[116:119], a[156:159], a[44:47], v[116:119]\n" \
B_"v50, s[8:11], s67 offen offset:0 sc1 lds\n" \
M_"v[112:115], a[140:143], a[44:47], v[40:43]\n" \
"v_and_b32_e32 v71, v15, v71\n" \
R_"a[188:191], v49 offset:7168\n" \
M_"v[112:115], a[136:139], a[40:43], v[112:115]\n" \
E_"v86, v86\n" \
M_"v[112:115], a[132:135], a[36:39], v[112:115]\n" \
E_"v87, v87\n" \
M_"v[112:115], a[128:131], a[32:35], v[112:115]\n" \
E_"v84, v84\n" \
M_"v[120:123], a[128:131], a[48:51], v[44:47]\n" \
B_"v50, s[8:11], s67 offen offset:1024 sc1 lds\n" \
M_"v[120:123], a[132:135], a[52:55], v[120:123]\n" \
E_"v85, v85\n" \
M_"v[120:123], a[136:139], a[56:59], v[120:123]\n" \
C_"v87, v86, v87\n" \
C_"v86, v84, v85\n" \
M_"v[120:123], a[140:143], a[60:63], v[120:123]\n" \
E_"v82, v82\n" \
M_"v[124:127], a[156:159], a[60:63], v[44:47]\n" \
E_"v83, v83\n" \
M_"v[124:127], a[152:155], a[56:59], v[124:127]\n" \
E_"v80, v80\n" \
M_"v[124:127], a[148:151], a[52:55], v[124:127]\n" \
B_"v50, s[8:11], s67 offen offset:2048 sc1 lds\n" \
M_"v[124:127], a[144:147], a[48:51], v[124:127]\n" \
C_"v85, v82, v83\n" \
"s_waitcnt lgkmcnt(1)\n" \
M_"v[128:131], a[160:163], v[68:71], v[128:131]\n" \
E_"v81, v81\n" \
M_"v[160:163], a[160:163], v[72:75], v[160:163]\n" \
C_"v84, v80, v81\n" \
"s_mov_b32 s64, s14\n" \
M_"v[132:135], a[164:167], v[68:71], v[132:135]\n" \
E_"v88, v88\n" \
M_"v[164:167], a[164:167], v[72:75], v[164:167]\n" \
E_"v89, v89\n" \
M_"v[136:139], a[168:171], v[68:71], v[136:139]\n" \
B_"v50, s[8:11], s67 offen offset:3072 sc1 lds\n" \
M_"v[168:171], a[168:171], v[72:75], v[168:171]\n" \
E_"v90, v90\n" \
M_"v[140:143], a[172:175], v[68:71], v[140:143]\n" \
E_"v91, v91\n" \
M_"v[172:175], a[172:175], v[72:75], v[172:175]\n" \
C_"v88, v88, v89\n" \
C_"v89, v90, v91\n" \
M_"v[144:147], a[176:179], v[68:71], v[144:147]\n" \
E_"v92, v92\n" \
M_"v[176:179], a[176:179], v[72:75], v[176:179]\n" \
E_"v93, v93\n" \
M_"v[148:151], a[180:183], v[68:71], v[148:151]\n" \
E_"v94, v94\n" \
M_"v[180:183], a[180:183], v[72:75], v[180:183]\n" \
E_"v95, v95\n" \
M_"v[152:155], a[184:187], v[68:71], v[152:155]\n" \
C_"v90, v92, v93\n" \
C_"v91, v94, v95\n" \
M_"v[184:187], a[184:187], v[72:75], v[184:187]\n" \
"v_add_u32_e32 v51, 0x12000, v63\n" \
"s_waitcnt lgkmcnt(0)\n" \
M_"v[156:159], a[188:191], v[68:71], v[156:159]\n" \
"s_xor_b32 m0, s65, 0xa000\n" \
"ds_read_b32 v58, v18 offset:512\n" \
M_"v[188:191], a[188:191], v[72:75], v[188:191]\n" \
B_"v51, s[8:11], s64 offen offset:0 sc1 lds\n" \
"v_smfmac_f32_16x16x64_bf16 a[96:99], a[104:107], v[68:75], v21\n" \
"ds_read_b32 v59, v18 offset:640\n" \
"ds_read_b32 v60, v18 offset:768\n" \
M_"v[220:223], a[188:191], v[84:87], v[220:223]\n" \
B_"v51, s[8:11], s64 offen offset:1024 sc1 lds\n" \
M_"v[252:255], a[188:191], v[88:91], v[252:255]\n" \
"ds_read_b32 v61, v18 offset:896\n" \
R_"a[128:131], v48 offset:0\n" \
M_"v[216:219], a[184:187], v[84:87], v[216:219]\n" \
R_"a[144:147], v48 offset:4096\n" \
R_"a[132:135], v48 offset:1024\n" \
M_"v[248:251], a[184:187], v[88:91], v[248:251]\n" \
R_"a[148:151], v48 offset:5120\n" \
R_"a[136:139], v48 offset:2048\n" \
M_"v[212:215], a[180:183], v[84:87], v[212:215]\n" \
R_"a[152:155], v48 offset:6144\n" \
R_"a[140:143], v48 offset:3072\n" \
M_"v[244:247], a[180:183], v[88:91], v[244:247]\n" \
R_"a[156:159], v48 offset:7168\n" \
"v_add_u32_e32 v52, 0x14000, v63\n" \
M_"v[208:211], a[176:179], v[84:87], v[208:211]\n" \
B_"v51, s[8:11], s64 offen offset:2048 sc1 lds\n" \
M_"v[240:243], a[176:179], v[88:91], v[240:243]\n" \
E_"v104, v104\n" \
M_"v[204:207], a[172:175], v[84:87], v[204:207]\n" \
E_"v105, v105\n" \
M_"v[236:239], a[172:175], v[88:91], v[236:239]\n" \
B_"v51, s[8:11], s64 offen offset:3072 sc1 lds\n" \
M_"v[200:203], a[168:171], v[84:87], v[200:203]\n" \
E_"v106, v106\n" \
M_"v[232:235], a[168:171], v[88:91], v[232:235]\n" \
E_"v107, v107\n" \
M_"v[196:199], a[164:167], v[84:87], v[196:199]\n" \
C_"v104, v104, v105\n" \
C_"v105, v106, v107\n" \
M_"v[228:231], a[164:167], v[88:91], v[228:231]\n" \
E_"v108, v108\n" \
M_"v[192:195], a[160:163], v[84:87], v[192:195]\n" \
E_"v109, v109\n" \
M_"v[224:227], a[160:163], v[88:91], v[224:227]\n" \
E_"v110, v110\n" \
"v_smfmac_f32_16x16x64_bf16 a[100:103], a[104:107], v[84:91], v21\n" \
E_"v111, v111\n" \
C_"v106, v108, v109\n" \
C_"v107, v110, v111\n" \
"v_and_b32_e32 v104, v12, v104\n" \
"v_and_b32_e32 v105, v13, v105\n" \
"v_and_b32_e32 v106, v14, v106\n" \
"v_and_b32_e32 v107, v15, v107\n" \
"v_accvgpr_read_b32 v54, a96\n" \
"v_accvgpr_read_b32 v22, a97\n" \
"v_readlane_b32 s93, v11, 22\n" \
"v_readlane_b32 s80, v11, 11\n" \
"v_readlane_b32 s81, v11, 12\n" \
"s_mov_b32 s82, 0x47fca0\n" \
"s_mov_b32 s83, 0x20000\n" \
"v_readlane_b32 s92, v11, 29\n" \
E_"v118, v118\n" \
E_"v119, v119\n" \
E_"v116, v116\n" \
E_"v117, v117\n" \
C_"v119, v118, v119\n" \
E_"v114, v114\n" \
E_"v115, v115\n" \
E_"v112, v112\n" \
C_"v118, v116, v117\n" \
"s_waitcnt vmcnt(10) lgkmcnt(0)\n" \
"s_barrier\n" \
M_"v[80:83], a[128:131], a[32:35], v[40:43]\n" \
R_"a[160:163], v49 offset:8192\n" \
R_"a[164:167], v49 offset:9216\n" \
M_"v[80:83], a[132:135], a[36:39], v[80:83]\n" \
R_"a[168:171], v49 offset:10240\n" \
"s_xor_b32 m0, s65, 0x18000\n" \
M_"v[80:83], a[136:139], a[40:43], v[80:83]\n" \
B_"v52, s[8:11], s64 offen offset:0 sc1 lds\n" \
M_"v[80:83], a[140:143], a[44:47], v[80:83]\n" \
R_"a[172:175], v49 offset:11264\n" \
R_"a[176:179], v49 offset:12288\n" \
M_"v[84:87], a[156:159], a[44:47], v[40:43]\n" \
R_"a[180:183], v49 offset:13312\n" \
R_"a[184:187], v49 offset:14336\n" \
M_"v[84:87], a[152:155], a[40:43], v[84:87]\n" \
B_"v52, s[8:11], s64 offen offset:1024 sc1 lds\n" \
M_"v[84:87], a[148:151], a[36:39], v[84:87]\n" \
R_"a[188:191], v49 offset:15360\n" \
C_"v117, v114, v115\n" \
M_"v[84:87], a[144:147], a[32:35], v[84:87]\n" \
B_"v52, s[8:11], s64 offen offset:2048 sc1 lds\n" \
M_"v[92:95], a[144:147], a[48:51], v[44:47]\n" \
E_"v113, v113\n" \
M_"v[92:95], a[148:151], a[52:55], v[92:95]\n" \
B_"v52, s[8:11], s64 offen offset:3072 sc1 lds\n" \
M_"v[92:95], a[152:155], a[56:59], v[92:95]\n" \
"s_add_u32 m0, s93, 0x0\n" \
C_"v116, v112, v113\n" \
M_"v[92:95], a[156:159], a[60:63], v[92:95]\n" \
E_"v120, v120\n" \
M_"v[88:91], a[140:143], a[60:63], v[44:47]\n" \
B_"v0, s[80:83], s92 offen offset:0 lds\n" \
M_"v[88:91], a[136:139], a[56:59], v[88:91]\n" \
E_"v121, v121\n" \
M_"v[88:91], a[132:135], a[52:55], v[88:91]\n" \
E_"v122, v122\n" \
M_"v[88:91], a[128:131], a[48:51], v[88:91]\n" \
B_"v1, s[80:83], s92 offen offset:1024 lds\n" \
M_"a[96:99], a[116:119], v[104:107], a[96:99]\n" \
C_"v120, v120, v121\n" \
"s_waitcnt lgkmcnt(5)\n" \
M_"v[160:163], a[160:163], v[104:107], v[160:163]\n" \
E_"v123, v123\n" \
M_"v[164:167], a[164:167], v[104:107], v[164:167]\n" \
B_"v2, s[80:83], s92 offen offset:2048 lds\n" \
M_"v[168:171], a[168:171], v[104:107], v[168:171]\n" \
C_"v121, v122, v123\n" \
"s_waitcnt lgkmcnt(1)\n" \
M_"v[172:175], a[172:175], v[104:107], v[172:175]\n" \
E_"v124, v124\n" \
M_"v[176:179], a[176:179], v[104:107], v[176:179]\n" \
E_"v125, v125\n" \
M_"v[180:183], a[180:183], v[104:107], v[180:183]\n" \
E_"v126, v126\n" \
M_"v[184:187], a[184:187], v[104:107], v[184:187]\n" \
C_"v122, v124, v125\n" \
"s_waitcnt lgkmcnt(0)\n" \
M_"v[188:191], a[188:191], v[104:107], v[188:191]\n" \
E_"v127, v127\n" \
M_"v[220:223], a[188:191], v[116:119], v[220:223]\n" \
C_"v123, v126, v127\n" \
B_"v3, s[80:83], s92 offen offset:3072 lds\n" \
"s_add_u32 m0, s93, 0x1000\n" \
M_"v[252:255], a[188:191], v[120:123], v[252:255]\n" \
R_"a[128:131], v48 offset:8192\n" \
R_"a[144:147], v48 offset:12288\n" \
M_"v[216:219], a[184:187], v[116:119], v[216:219]\n" \
B_"v4, s[80:83], s92 offen offset:0 lds\n" \
M_"v[248:251], a[184:187], v[120:123], v[248:251]\n" \
R_"a[132:135], v48 offset:9216\n" \
R_"a[148:151], v48 offset:13312\n" \
M_"v[212:215], a[180:183], v[116:119], v[212:215]\n" \
B_"v5, s[80:83], s92 offen offset:1024 lds\n" \
M_"v[244:247], a[180:183], v[120:123], v[244:247]\n" \
R_"a[136:139], v48 offset:10240\n" \
R_"a[152:155], v48 offset:14336\n" \
M_"v[208:211], a[176:179], v[116:119], v[208:211]\n" \
B_"v6, s[80:83], s92 offen offset:2048 lds\n" \
M_"v[240:243], a[176:179], v[120:123], v[240:243]\n" \
R_"a[140:143], v48 offset:11264\n" \
R_"a[156:159], v48 offset:15360\n" \
M_"v[204:207], a[172:175], v[116:119], v[204:207]\n" \
B_"v7, s[80:83], s92 offen offset:3072 lds\n" \
M_"v[236:239], a[172:175], v[120:123], v[236:239]\n" \
"v_readlane_b32 s96, v11, 1\n" \
"v_lshrrev_b32_e32 v16, 1, v20\n" \
M_"v[200:203], a[168:171], v[116:119], v[200:203]\n" \
"v_add_f32_e32 v54, v54, v22\n" \
"v_max_f32_e32 v62, v62, v54\n" \
M_"v[232:235], a[168:171], v[120:123], v[232:235]\n" \
"v_rcp_f32_e32 v54, v54\n" \
M_"v[196:199], a[164:167], v[116:119], v[196:199]\n" \
"v_mul_f32_e32 v54, v54, v58\n" \
"v_mul_f32_e32 v128, v54, v128\n" \
M_"v[228:231], a[164:167], v[120:123], v[228:231]\n" \
"v_mul_f32_e32 v129, v54, v129\n" \
"v_mul_f32_e32 v136, v54, v136\n" \
M_"v[192:195], a[160:163], v[116:119], v[192:195]\n" \
"v_mul_f32_e32 v137, v54, v137\n" \
"v_mul_f32_e32 v144, v54, v144\n" \
M_"v[224:227], a[160:163], v[120:123], v[224:227]\n" \
"v_mul_f32_e32 v145, v54, v145\n" \
"v_mul_f32_e32 v152, v54, v152\n" \
"v_smfmac_f32_16x16x64_bf16 a[100:103], a[104:107], v[116:123], v21\n" \
"v_mul_f32_e32 v153, v54, v153\n" \
C_"v128, v128, v129\n" \
"v_mul_f32_e32 v130, v54, v130\n" \
"v_mul_f32_e32 v131, v54, v131\n" \
C_"v136, v136, v137\n" \
"v_mul_f32_e32 v138, v54, v138\n" \
"v_mul_f32_e32 v139, v54, v139\n" \
C_"v144, v144, v145\n" \
"v_mul_f32_e32 v146, v54, v146\n" \
"v_mul_f32_e32 v147, v54, v147\n" \
C_"v152, v152, v153\n" \
"v_mul_f32_e32 v154, v54, v154\n" \
"v_mul_f32_e32 v155, v54, v155\n" \
C_"v129, v130, v131\n" \
"v_mul_f32_e32 v132, v54, v132\n" \
"v_mul_f32_e32 v133, v54, v133\n" \
"v_mul_f32_e32 v134, v54, v134\n" \
"v_mul_f32_e32 v135, v54, v135\n" \
C_"v137, v138, v139\n" \
"v_mul_f32_e32 v140, v54, v140\n" \
"v_mul_f32_e32 v141, v54, v141\n" \
"v_mul_f32_e32 v142, v54, v142\n" \
"v_mul_f32_e32 v143, v54, v143\n" \
C_"v145, v146, v147\n" \
"v_mul_f32_e32 v148, v54, v148\n" \
"v_mul_f32_e32 v149, v54, v149\n" \
"v_mul_f32_e32 v150, v54, v150\n" \
"v_mul_f32_e32 v151, v54, v151\n" \
C_"v153, v154, v155\n" \
"v_mul_f32_e32 v156, v54, v156\n" \
"v_mul_f32_e32 v157, v54, v157\n" \
"v_mul_f32_e32 v158, v54, v158\n" \
"v_mul_f32_e32 v159, v54, v159\n" \
C_"v130, v132, v133\n" \
C_"v131, v134, v135\n" \
"buffer_store_dwordx4 v[128:131], v20, s[56:59], s68 offen offset:0 sc1\n" \
C_"v138, v140, v141\n" \
C_"v139, v142, v143\n" \
C_"v146, v148, v149\n" \
C_"v147, v150, v151\n" \
C_"v154, v156, v157\n" \
C_"v155, v158, v159\n" \
"buffer_store_dwordx4 v[136:139], v20, s[56:59], s68 offen offset:1024 sc1\n" \
"s_add_u32 m0, s93, 0x2000\n" \
"v_add_u32_e32 v49, 0x10000, v48\n" \
"s_xor_b32 s65, s65, 0x8000\n" \
"s_add_u32 s64, s64, 0x10000\n" \
E_"v86, v86\n" \
"buffer_store_dwordx4 v[144:147], v20, s[56:59], s68 offen offset:2048 sc1\n" \
E_"v87, v87\n" \
E_"v84, v84\n" \
E_"v85, v85\n" \
"buffer_store_dwordx4 v[152:155], v20, s[56:59], s68 offen offset:3072 sc1\n" \
E_"v88, v88\n" \
E_"v89, v89\n" \
C_"v87, v86, v87\n" \
E_"v82, v82\n" \
B_"v8, s[80:83], s92 offen offset:0 lds\n" \
E_"v83, v83\n" \
E_"v80, v80\n" \
E_"v81, v81\n" \
E_"v90, v90\n" \
E_"v91, v91\n" \
C_"v86, v84, v85\n" \
C_"v88, v88, v89\n" \
E_"v92, v92\n" \
E_"v93, v93\n" \
E_"v94, v94\n" \
E_"v95, v95\n" \
C_"v85, v82, v83\n" \
C_"v84, v80, v81\n" \
C_"v89, v90, v91\n" \
"v_and_b32_e32 v84, v12, v84\n" \
"v_and_b32_e32 v85, v13, v85\n" \
"v_and_b32_e32 v86, v14, v86\n" \
"s_waitcnt vmcnt(17) lgkmcnt(0)\n" \
"s_barrier\n" \
M_"v[120:123], a[128:131], a[48:51], v[44:47]\n" \
"buffer_load_dwordx2 v[26:27], v16, s[56:59], s96 offen sc1\n" \
M_"v[120:123], a[132:135], a[52:55], v[120:123]\n" \
R_"a[188:191], v49 offset:7168\n" \
R_"a[184:187], v49 offset:6144\n" \
M_"v[120:123], a[136:139], a[56:59], v[120:123]\n" \
"s_xor_b32 m0, s65, 0x12000\n" \
R_"a[180:183], v49 offset:5120\n" \
M_"v[120:123], a[140:143], a[60:63], v[120:123]\n" \
B_"v50, s[8:11], s64 offen offset:0 sc1 lds\n" \
M_"v[124:127], a[156:159], a[60:63], v[44:47]\n" \
R_"a[176:179], v49 offset:4096\n" \
R_"a[172:175], v49 offset:3072\n" \
M_"v[124:127], a[152:155], a[56:59], v[124:127]\n" \
B_"v50, s[8:11], s64 offen offset:1024 sc1 lds\n" \
M_"v[124:127], a[148:151], a[52:55], v[124:127]\n" \
R_"a[168:171], v49 offset:2048\n" \
"v_and_b32_e32 v87, v15, v87\n" \
M_"v[124:127], a[144:147], a[48:51], v[124:127]\n" \
C_"v90, v92, v93\n" \
C_"v91, v94, v95\n" \
M_"v[32:35], a[124:127], a[64:67], 0\n" \
B_"v50, s[8:11], s64 offen offset:2048 sc1 lds\n" \
M_"v[36:39], a[124:127], a[80:83], 0\n" \
R_"a[164:167], v49 offset:1024\n" \
R_"a[160:163], v49 offset:0\n" \
M_"v[32:35], a[224:227], a[68:71], v[32:35]\n" \
R_"a[0:3], v49 offset:8192\n" \
R_"a[4:7], v49 offset:9216\n" \
M_"v[36:39], a[224:227], a[84:87], v[36:39]\n" \
R_"a[8:11], v49 offset:10240\n" \
R_"a[12:15], v49 offset:11264\n" \
M_"v[32:35], a[228:231], a[72:75], v[32:35]\n" \
B_"v50, s[8:11], s64 offen offset:3072 sc1 lds\n" \
M_"v[36:39], a[228:231], a[88:91], v[36:39]\n" \
R_"a[16:19], v49 offset:12288\n" \
R_"a[20:23], v49 offset:13312\n" \
M_"v[32:35], a[232:235], a[76:79], v[32:35]\n" \
R_"a[24:27], v49 offset:14336\n" \
R_"a[28:31], v49 offset:15360\n" \
M_"v[36:39], a[232:235], a[92:95], v[36:39]\n" \
E_"v120, v120\n" \
"v_smfmac_f32_16x16x64_bf16 a[100:103], a[104:107], v[84:91], v21\n" \
"s_mov_b32 s63, s62\n" \
"s_waitcnt lgkmcnt(13)\n" \
M_"v[220:223], a[188:191], v[84:87], v[220:223]\n" \
E_"v121, v121\n" \
M_"v[252:255], a[188:191], v[88:91], v[252:255]\n" \
E_"v122, v122\n" \
M_"v[216:219], a[184:187], v[84:87], v[216:219]\n" \
E_"v123, v123\n" \
M_"v[248:251], a[184:187], v[88:91], v[248:251]\n" \
C_"v120, v120, v121\n" \
C_"v121, v122, v123\n" \
M_"v[212:215], a[180:183], v[84:87], v[212:215]\n" \
E_"v124, v124\n" \
M_"v[244:247], a[180:183], v[88:91], v[244:247]\n" \
"v_and_b32_e32 v120, v12, v120\n" \
"s_waitcnt lgkmcnt(10)\n" \
M_"v[208:211], a[176:179], v[84:87], v[208:211]\n" \
E_"v125, v125\n" \
M_"v[240:243], a[176:179], v[88:91], v[240:243]\n" \
E_"v126, v126\n" \
M_"v[204:207], a[172:175], v[84:87], v[204:207]\n" \
E_"v127, v127\n" \
M_"v[236:239], a[172:175], v[88:91], v[236:239]\n" \
C_"v122, v124, v125\n" \
C_"v123, v126, v127\n" \
M_"v[200:203], a[168:171], v[84:87], v[200:203]\n" \
"v_and_b32_e32 v121, v13, v121\n" \
"v_and_b32_e32 v122, v14, v122\n" \
M_"v[232:235], a[168:171], v[88:91], v[232:235]\n" \
"v_and_b32_e32 v123, v15, v123\n" \
"s_waitcnt lgkmcnt(0)\n" \
M_"v[196:199], a[164:167], v[84:87], v[196:199]\n" \
"v_accvgpr_read_b32 v56, a100\n" \
"v_accvgpr_read_b32 v24, a101\n" \
M_"v[228:231], a[164:167], v[88:91], v[228:231]\n" \
"v_readlane_b32 s97, v11, 39\n" \
"v_add_u32_e32 v17, 0x49000, v20\n" \
M_"v[192:195], a[160:163], v[84:87], v[192:195]\n" \
"v_readlane_b32 s98, v11, 40\n" \
"v_add_u32_e32 v48, s16, v20\n" \
M_"v[224:227], a[160:163], v[88:91], v[224:227]\n" \
R_"a[128:131], v48 offset:0\n" \
R_"a[144:147], v48 offset:4096\n" \
M_"a[100:103], a[116:119], v[120:123], a[100:103]\n" \
R_"a[132:135], v48 offset:1024\n" \
R_"a[148:151], v48 offset:5120\n" \
M_"v[224:227], a[0:3], v[120:123], v[224:227]\n" \
R_"a[136:139], v48 offset:2048\n" \
R_"a[152:155], v48 offset:6144\n" \
M_"v[228:231], a[4:7], v[120:123], v[228:231]\n" \
R_"a[140:143], v48 offset:3072\n" \
R_"a[156:159], v48 offset:7168\n" \
M_"v[232:235], a[8:11], v[120:123], v[232:235]\n" \
"v_accvgpr_read_b32 v55, a98\n" \
"v_accvgpr_read_b32 v23, a99\n" \
M_"a[96:99], a[120:123], a[120:123], 0\n" \
"v_add_f32_e32 v55, v55, v23\n" \
"v_max_f32_e32 v62, v62, v55\n" \
M_"v[236:239], a[12:15], v[120:123], v[236:239]\n" \
"v_rcp_f32_e32 v55, v55\n" \
M_"v[240:243], a[16:19], v[120:123], v[240:243]\n" \
"v_mul_f32_e32 v55, v55, v59\n" \
"v_mul_f32_e32 v160, v55, v160\n" \
M_"v[244:247], a[20:23], v[120:123], v[244:247]\n" \
"v_mul_f32_e32 v161, v55, v161\n" \
"v_mul_f32_e32 v168, v55, v168\n" \
M_"v[248:251], a[24:27], v[120:123], v[248:251]\n" \
"v_mul_f32_e32 v169, v55, v169\n" \
"v_mul_f32_e32 v176, v55, v176\n" \
M_"v[252:255], a[28:31], v[120:123], v[252:255]\n" \
"v_mul_f32_e32 v177, v55, v177\n" \
"v_mul_f32_e32 v184, v55, v184\n" \
"v_mul_f32_e32 v185, v55, v185\n" \
C_"v160, v160, v161\n" \
"v_mul_f32_e32 v162, v55, v162\n" \
"v_mul_f32_e32 v163, v55, v163\n" \
C_"v168, v168, v169\n" \
"v_mul_f32_e32 v170, v55, v170\n" \
"v_mul_f32_e32 v171, v55, v171\n" \
"s_waitcnt vmcnt(4)\n" \
"v_cmp_ne_u64_e32 vcc, s[62:63], v[26:27]\n" \
C_"v176, v176, v177\n" \
"v_mul_f32_e32 v178, v55, v178\n" \
"v_mul_f32_e32 v179, v55, v179\n" \
C_"v184, v184, v185\n" \
"v_mul_f32_e32 v186, v55, v186\n" \
"s_cbranch_vccnz L_sos_%=\n" \
"L_ros_%=:\n" \
B_"a[124:127], v20, s[56:59], s97 offen offset:0 sc1\n" \
"v_mul_f32_e32 v187, v55, v187\n" \
C_"v161, v162, v163\n" \
"v_mul_f32_e32 v164, v55, v164\n" \
"v_mul_f32_e32 v165, v55, v165\n" \
"v_mul_f32_e32 v166, v55, v166\n" \
"v_mul_f32_e32 v167, v55, v167\n" \
B_"a[228:231], v17, s[56:59], s97 offen offset:0 sc1\n" \
C_"v169, v170, v171\n" \
"v_mul_f32_e32 v172, v55, v172\n" \
"v_mul_f32_e32 v173, v55, v173\n" \
"v_mul_f32_e32 v174, v55, v174\n" \
"v_mul_f32_e32 v175, v55, v175\n" \
C_"v177, v178, v179\n" \
B_"a[236:239], v20, s[56:59], s98 offen offset:0 sc1\n" \
"v_mul_f32_e32 v180, v55, v180\n" \
"v_mul_f32_e32 v181, v55, v181\n" \
"v_mul_f32_e32 v182, v55, v182\n" \
"v_mul_f32_e32 v183, v55, v183\n" \
C_"v185, v186, v187\n" \
"v_mul_f32_e32 v188, v55, v188\n" \
B_"a[244:247], v17, s[56:59], s98 offen offset:0 sc1\n" \
"v_mul_f32_e32 v189, v55, v189\n" \
"v_mul_f32_e32 v190, v55, v190\n" \
"v_mul_f32_e32 v191, v55, v191\n" \
C_"v162, v164, v165\n" \
C_"v163, v166, v167\n" \
C_"v170, v172, v173\n" \
"buffer_store_dwordx4 v[160:163], v20, s[56:59], s69 offen offset:0 sc1\n" \
C_"v171, v174, v175\n" \
C_"v178, v180, v181\n" \
C_"v179, v182, v183\n" \
C_"v186, v188, v189\n" \
C_"v187, v190, v191\n" \
"v_readlane_b32 s93, v11, 23\n" \
"buffer_store_dwordx4 v[168:171], v20, s[56:59], s69 offen offset:1024 sc1\n" \
"v_readlane_b32 s84, v11, 15\n" \
"v_readlane_b32 s85, v11, 16\n" \
"v_readlane_b32 s86, v11, 17\n" \
"s_mov_b32 s87, 0x20000\n" \
"v_readlane_b32 s92, v11, 18\n" \
"v_readlane_b32 s91, v11, 24\n" \
"buffer_store_dwordx4 v[176:179], v20, s[56:59], s69 offen offset:2048 sc1\n" \
M_"a[112:115], a[120:123], v[128:131], a[112:115]\n" \
M_"a[112:115], a[120:123], v[136:139], a[112:115]\n" \
"buffer_store_dwordx4 v[184:187], v20, s[56:59], s69 offen offset:3072 sc1\n" \
"v_mfma_f32_32x32x16_bf16 v[128:143], a[120:123], a[120:123], 0\n" \
M_"a[112:115], a[120:123], v[144:147], a[112:115]\n" \
B_"a[224:227], v20, s[56:59], s97 offen offset:1024 sc1\n" \
M_"a[112:115], a[120:123], v[152:155], a[112:115]\n" \
"v_mfma_f32_32x32x16_bf16 v[144:159], a[120:123], a[120:123], 0\n" \
B_"a[232:235], v17, s[56:59], s97 offen offset:1024 sc1\n" \
"v_add_f32_e32 v56, v56, v24\n" \
M_"a[112:115], a[120:123], v[160:163], a[112:115]\n" \
B_"a[240:243], v20, s[56:59], s98 offen offset:1024 sc1\n" \
"v_max_f32_e32 v62, v62, v56\n" \
"v_rcp_f32_e32 v56, v56\n" \
M_"a[112:115], a[120:123], v[168:171], a[112:115]\n" \
"v_mfma_f32_32x32x16_bf16 v[160:175], a[120:123], a[120:123], 0\n" \
B_"a[248:251], v17, s[56:59], s98 offen offset:1024 sc1\n" \
"v_mul_f32_e32 v56, v56, v60\n" \
M_"a[112:115], a[120:123], v[176:179], a[112:115]\n" \
"v_mul_f32_e32 v192, v56, v192\n" \
"v_mul_f32_e32 v193, v56, v193\n" \
"s_waitcnt vmcnt(17) lgkmcnt(0)\n" \
"s_barrier\n" \
M_"v[64:67], a[128:131], a[64:67], v[32:35]\n" \
"s_add_u32 m0, s93, 0x0\n" \
C_"v192, v192, v193\n" \
M_"v[64:67], a[132:135], a[68:71], v[64:67]\n" \
B_"v0, s[84:87], s92 offen offset:0 lds\n" \
M_"v[64:67], a[136:139], a[72:75], v[64:67]\n" \
"v_mul_f32_e32 v194, v56, v194\n" \
"v_mul_f32_e32 v195, v56, v195\n" \
M_"v[64:67], a[140:143], a[76:79], v[64:67]\n" \
B_"v1, s[84:87], s92 offen offset:1024 lds\n" \
M_"v[68:71], a[156:159], a[76:79], v[32:35]\n" \
M_"a[112:115], a[120:123], v[184:187], a[112:115]\n" \
"v_mfma_f32_32x32x16_bf16 v[176:191], a[120:123], a[120:123], 0\n" \
B_"v2, s[84:87], s92 offen offset:2048 lds\n" \
C_"v193, v194, v195\n" \
"v_mul_f32_e32 v196, v56, v196\n" \
"v_mul_f32_e32 v197, v56, v197\n" \
"v_mul_f32_e32 v198, v56, v198\n" \
M_"v[68:71], a[152:155], a[72:75], v[68:71]\n" \
B_"v3, s[84:87], s92 offen offset:3072 lds\n" \
M_"v[68:71], a[148:151], a[68:71], v[68:71]\n" \
"s_add_u32 m0, s91, 0x1000\n" \
"v_mul_f32_e32 v199, v56, v199\n" \
M_"v[68:71], a[144:147], a[64:67], v[68:71]\n" \
B_"v4, s[84:87], s92 offen offset:0 lds\n" \
M_"v[76:79], a[144:147], a[80:83], v[36:39]\n" \
R_"a[144:147], v48 offset:12288\n" \
"v_mul_f32_e32 v200, v56, v200\n" \
M_"v[76:79], a[148:151], a[84:87], v[76:79]\n" \
B_"v5, s[84:87], s92 offen offset:1024 lds\n" \
M_"v[76:79], a[152:155], a[88:91], v[76:79]\n" \
R_"a[148:151], v48 offset:13312\n" \
R_"a[152:155], v48 offset:14336\n" \
M_"v[76:79], a[156:159], a[92:95], v[76:79]\n" \
B_"v6, s[84:87], s92 offen offset:2048 lds\n" \
M_"v[72:75], a[140:143], a[92:95], v[36:39]\n" \
R_"a[140:143], v48 offset:11264\n" \
R_"a[156:159], v48 offset:15360\n" \
M_"v[72:75], a[136:139], a[88:91], v[72:75]\n" \
B_"v7, s[84:87], s92 offen offset:3072 lds\n" \
M_"v[72:75], a[132:135], a[84:87], v[72:75]\n" \
R_"a[132:135], v48 offset:9216\n" \
R_"a[136:139], v48 offset:10240\n" \
M_"v[72:75], a[128:131], a[80:83], v[72:75]\n" \
R_"a[128:131], v48 offset:8192\n" \
"v_mul_f32_e32 v201, v56, v201\n" \
"v_accvgpr_read_b32 v57, a102\n" \
"v_accvgpr_read_b32 v25, a103\n" \
M_"a[100:103], a[120:123], a[120:123], 0\n" \
C_"v194, v196, v197\n" \
C_"v195, v198, v199\n" \
"buffer_store_dwordx4 v[192:195], v20, s[56:59], s70 offen offset:0 sc1\n" \
C_"v200, v200, v201\n" \
"v_mul_f32_e32 v202, v56, v202\n" \
"v_mul_f32_e32 v203, v56, v203\n" \
"v_add_f32_e32 v57, v57, v25\n" \
M_"a[112:115], a[120:123], v[192:195], a[112:115]\n" \
C_"v201, v202, v203\n" \
"v_mul_f32_e32 v204, v56, v204\n" \
"v_mul_f32_e32 v205, v56, v205\n" \
"v_mul_f32_e32 v206, v56, v206\n" \
"v_mul_f32_e32 v207, v56, v207\n" \
"v_mul_f32_e32 v208, v56, v208\n" \
"v_mul_f32_e32 v209, v56, v209\n" \
"v_max_f32_e32 v62, v62, v57\n" \
C_"v202, v204, v205\n" \
C_"v203, v206, v207\n" \
"buffer_store_dwordx4 v[200:203], v20, s[56:59], s70 offen offset:1024 sc1\n" \
".fill (L_ab + 28672 - .) / 4, 4, 0xBF800000\n" \
"L_ap7:\n" \
"s_cbranch_execz L_ap0\n" \
C_"v208, v208, v209\n" \
"v_mul_f32_e32 v210, v56, v210\n" \
"v_mul_f32_e32 v211, v56, v211\n" \
"v_rcp_f32_e32 v57, v57\n" \
M_"a[112:115], a[120:123], v[200:203], a[112:115]\n" \
"v_mfma_f32_32x32x16_bf16 v[192:207], a[120:123], a[120:123], 0\n" \
C_"v209, v210, v211\n" \
"v_mul_f32_e32 v212, v56, v212\n" \
"v_mul_f32_e32 v213, v56, v213\n" \
"v_mul_f32_e32 v214, v56, v214\n" \
"v_mul_f32_e32 v215, v56, v215\n" \
"v_mul_f32_e32 v216, v56, v216\n" \
"v_mul_f32_e32 v217, v56, v217\n" \
C_"v210, v212, v213\n" \
C_"v211, v214, v215\n" \
"buffer_store_dwordx4 v[208:211], v20, s[56:59], s70 offen offset:2048 sc1\n" \
C_"v216, v216, v217\n" \
"v_mul_f32_e32 v218, v56, v218\n" \
"v_mul_f32_e32 v219, v56, v219\n" \
"v_mul_f32_e32 v57, v57, v61\n" \
M_"a[112:115], a[120:123], v[208:211], a[112:115]\n" \
C_"v217, v218, v219\n" \
"v_mul_f32_e32 v220, v56, v220\n" \
"v_mul_f32_e32 v221, v56, v221\n" \
"v_mul_f32_e32 v222, v56, v222\n" \
"v_mul_f32_e32 v223, v56, v223\n" \
"v_mul_f32_e32 v224, v57, v224\n" \
"v_mul_f32_e32 v225, v57, v225\n" \
C_"v218, v220, v221\n" \
C_"v219, v222, v223\n" \
"buffer_store_dwordx4 v[216:219], v20, s[56:59], s70 offen offset:3072 sc1\n" \
C_"v224, v224, v225\n" \
"v_mul_f32_e32 v226, v57, v226\n" \
"v_mul_f32_e32 v227, v57, v227\n" \
M_"a[112:115], a[120:123], v[216:219], a[112:115]\n" \
"v_mfma_f32_32x32x16_bf16 v[208:223], a[120:123], a[120:123], 0\n" \
C_"v225, v226, v227\n" \
"v_mul_f32_e32 v228, v57, v228\n" \
"v_mul_f32_e32 v229, v57, v229\n" \
"v_mul_f32_e32 v230, v57, v230\n" \
"v_mul_f32_e32 v231, v57, v231\n" \
"v_mul_f32_e32 v232, v57, v232\n" \
"v_mul_f32_e32 v233, v57, v233\n" \
C_"v226, v228, v229\n" \
C_"v227, v230, v231\n" \
"buffer_store_dwordx4 v[224:227], v20, s[56:59], s71 offen offset:0 sc1\n" \
C_"v232, v232, v233\n" \
"v_mul_f32_e32 v234, v57, v234\n" \
"v_mul_f32_e32 v235, v57, v235\n" \
M_"a[112:115], a[120:123], v[224:227], a[112:115]\n" \
C_"v233, v234, v235\n" \
"v_mul_f32_e32 v236, v57, v236\n" \
"v_mul_f32_e32 v237, v57, v237\n" \
"v_mul_f32_e32 v238, v57, v238\n" \
"v_mul_f32_e32 v239, v57, v239\n" \
"v_mul_f32_e32 v240, v57, v240\n" \
"v_mul_f32_e32 v241, v57, v241\n" \
C_"v234, v236, v237\n" \
C_"v235, v238, v239\n" \
"buffer_store_dwordx4 v[232:235], v20, s[56:59], s71 offen offset:1024 sc1\n" \
C_"v240, v240, v241\n" \
"v_mul_f32_e32 v242, v57, v242\n" \
"v_mul_f32_e32 v243, v57, v243\n" \
M_"a[112:115], a[120:123], v[232:235], a[112:115]\n" \
"v_mfma_f32_32x32x16_bf16 v[224:239], a[120:123], a[120:123], 0\n" \
C_"v241, v242, v243\n" \
"v_mul_f32_e32 v244, v57, v244\n" \
"v_mul_f32_e32 v245, v57, v245\n" \
"v_mul_f32_e32 v246, v57, v246\n" \
"v_mul_f32_e32 v247, v57, v247\n" \
"v_mul_f32_e32 v248, v57, v248\n" \
"v_mul_f32_e32 v249, v57, v249\n" \
C_"v242, v244, v245\n" \
C_"v243, v246, v247\n" \
"buffer_store_dwordx4 v[240:243], v20, s[56:59], s71 offen offset:2048 sc1\n" \
C_"v248, v248, v249\n" \
"v_mul_f32_e32 v250, v57, v250\n" \
"v_mul_f32_e32 v251, v57, v251\n" \
M_"a[112:115], a[120:123], v[240:243], a[112:115]\n" \
C_"v249, v250, v251\n" \
"v_mul_f32_e32 v252, v57, v252\n" \
"v_mul_f32_e32 v253, v57, v253\n" \
"v_mul_f32_e32 v254, v57, v254\n" \
"v_mul_f32_e32 v255, v57, v255\n" \
C_"v250, v252, v253\n" \
C_"v251, v254, v255\n" \
"buffer_store_dwordx4 v[248:251], v20, s[56:59], s71 offen offset:3072 sc1\n" \
"s_add_u32 m0, s91, 0x2000\n" \
M_"a[112:115], a[120:123], v[248:251], a[112:115]\n" \
"v_mfma_f32_32x32x16_bf16 v[240:255], a[120:123], a[120:123], 0\n" \
"v_add_u32_e32 v53, 0x20000, v63\n" \
B_"v8, s[84:87], s92 offen offset:0 lds\n" \
E_"v70, v70\n" \
E_"v71, v71\n" \
E_"v72, v72\n" \
E_"v73, v73\n" \
"s_add_u32 s64, s64, 0x10000\n" \
"v_accvgpr_read_b32 v63, a112\n" \
"v_cmp_u_f32_e32 vcc, v63, v63\n" \
"v_cmp_lt_f32_e64 s[72:73], s21, v62\n" \
"v_readlane_b32 s95, v11, 3\n" \
E_"v68, v68\n" \
E_"v69, v69\n" \
E_"v74, v74\n" \
E_"v75, v75\n" \
"s_or_b64 vcc, s[72:73], vcc\n" \
"s_cselect_b32 s95, 0x40000000, s95\n" \
"s_cselect_b32 s94, 0x7f800000, 0\n" \
C_"v71, v70, v71\n" \
E_"v66, v66\n" \
E_"v67, v67\n" \
E_"v64, v64\n" \
E_"v65, v65\n" \
C_"v72, v72, v73\n" \
E_"v76, v76\n" \
E_"v77, v77\n" \
E_"v78, v78\n" \
E_"v79, v79\n" \
C_"v70, v68, v69\n" \
"s_waitcnt vmcnt(17) lgkmcnt(0)\n" \
"s_barrier\n" \
C_"v73, v74, v75\n" \
"v_add_u32_e32 v49, 0x10000, v48\n" \
"s_xor_b32 s65, s65, 0x8000\n" \
C_"v69, v66, v67\n" \
C_"v68, v64, v65\n" \
C_"v74, v76, v77\n" \
C_"v75, v78, v79\n" \
"v_xor_b32_e32 v48, 0x8000, v48\n" \
"s_sub_u32 s66, s19, 2\n" \
"s_branch L_f1_%=\n" \
".fill (L_ab + 32768 - .) / 4, 4, 0xBF800000\n" \
".fill (L_ab + 32768 - .) / 4, 4, 0xBF800000\n" \
"L_ap8:\n" \
"s_setpc_b64 s[88:89]\n" \
"L_out_%=:\n" \
"v_bfe_u32 v38, v20, 2, 4\n" \
"v_readlane_b32 s64, v11, 2\n" \
"v_add_u32_e32 v32, 0x49000, v20\n" \
"v_add_u32_e32 v34, 0x20000, v10\n" \
X_"a[0:1], v34 offset:0\n" \
X_"a[2:3], v34 offset:4608\n" \
X_"a[4:5], v34 offset:9216\n" \
"buffer_load_dword v26, v38, s[56:59], s64 offen sc1\n" \
X_"a[6:7], v34 offset:13824\n" \
X_"a[8:9], v34 offset:32\n" \
X_"a[10:11], v34 offset:4640\n" \
X_"a[12:13], v34 offset:9248\n" \
"buffer_store_dwordx4 v[64:67], v20, s[56:59], s61 offen offset:0 sc1\n" \
X_"a[14:15], v34 offset:13856\n" \
"v_mov_b32_e32 v39, s62\n" \
"v_readlane_b32 s70, v11, 5\n" \
"v_readlane_b32 s71, v11, 4\n" \
"buffer_store_dwordx4 v[68:71], v20, s[56:59], s61 offen offset:1024 sc1\n" \
X_"a[16:17], v34 offset:64\n" \
X_"a[18:19], v34 offset:4672\n" \
X_"a[20:21], v34 offset:9280\n" \
X_"a[22:23], v34 offset:13888\n" \
"buffer_store_dwordx4 v[72:75], v20, s[56:59], s61 offen offset:2048 sc1\n" \
X_"a[24:25], v34 offset:96\n" \
X_"a[26:27], v34 offset:4704\n" \
X_"a[28:29], v34 offset:9312\n" \
X_"a[30:31], v34 offset:13920\n" \
"buffer_store_dwordx4 v[76:79], v20, s[56:59], s61 offen offset:3072 sc1\n" \
X_"a[96:97], v34 offset:128\n" \
X_"a[98:99], v34 offset:4736\n" \
X_"a[100:101], v34 offset:9344\n" \
X_"a[102:103], v34 offset:13952\n" \
"buffer_store_dwordx4 v[80:83], v32, s[56:59], s61 offen offset:0 sc1\n" \
X_"a[104:105], v34 offset:160\n" \
X_"a[106:107], v34 offset:4768\n" \
X_"a[108:109], v34 offset:9376\n" \
X_"a[110:111], v34 offset:13984\n" \
"buffer_store_dwordx4 v[84:87], v32, s[56:59], s61 offen offset:1024 sc1\n" \
X_"a[112:113], v34 offset:192\n" \
"s_waitcnt vmcnt(16) lgkmcnt(15)\n" \
M_"v[112:115], a[0:3], a[192:195], 0\n" \
"buffer_store_dwordx4 v[96:99], v32, s[56:59], s61 offen offset:2048 sc1\n" \
M_"v[112:115], a[4:7], a[196:199], v[112:115]\n" \
X_"a[114:115], v34 offset:4800\n" \
X_"a[116:117], v34 offset:9408\n" \
"buffer_store_dwordx4 v[100:103], v32, s[56:59], s61 offen offset:3072 sc1\n" \
M_"v[116:119], a[0:3], a[208:211], 0\n" \
X_"a[118:119], v34 offset:14016\n" \
X_"a[120:121], v34 offset:224\n" \
M_"v[116:119], a[4:7], a[212:215], v[116:119]\n" \
X_"a[122:123], v34 offset:4832\n" \
"v_lshrrev_b32_e32 v44, 9, v20\n" \
M_"v[184:187], a[0:3], v[64:67], 0\n" \
"v_mul_u32_u24_e32 v44, 0xb40, v44\n" \
"v_add_u32_e32 v35, v44, v10\n" \
M_"v[184:187], a[4:7], v[68:71], v[184:187]\n" \
"v_readlane_b32 s65, v11, 33\n" \
"v_readlane_b32 s67, v11, 26\n" \
M_"v[188:191], a[0:3], v[80:83], 0\n" \
X_"a[0:1], v34 offset:9440\n" \
X_"a[2:3], v34 offset:14048\n" \
M_"v[188:191], a[4:7], v[84:87], v[188:191]\n" \
X_"a[4:5], v34 offset:256\n" \
X_"a[6:7], v34 offset:4864\n" \
M_"v[196:199], a[8:11], v[80:83], 0\n" \
"v_readlane_b32 s80, v11, 11\n" \
"v_readlane_b32 s81, v11, 12\n" \
M_"v[196:199], a[12:15], v[84:87], v[196:199]\n" \
"s_mov_b32 s82, 0x47fca0\n" \
"s_mov_b32 s83, 0x20000\n" \
M_"v[192:195], a[8:11], v[64:67], 0\n" \
"v_readlane_b32 s66, v11, 31\n" \
"s_add_u32 m0, s67, 0x0\n" \
M_"v[192:195], a[12:15], v[68:71], v[192:195]\n" \
"v_readlane_b32 s92, v11, 27\n" \
"v_readlane_b32 s73, v11, 32\n" \
M_"v[124:127], a[8:11], a[208:211], 0\n" \
B_"v0, s[80:83], s66 offen offset:0 lds\n" \
M_"v[124:127], a[12:15], a[212:215], v[124:127]\n" \
"v_readlane_b32 s94, v11, 28\n" \
"v_readlane_b32 s84, v11, 9\n" \
M_"v[120:123], a[8:11], a[192:195], 0\n" \
X_"a[8:9], v34 offset:9472\n" \
X_"a[10:11], v34 offset:14080\n" \
M_"v[120:123], a[12:15], a[196:199], v[120:123]\n" \
X_"a[12:13], v34 offset:18432\n" \
X_"a[14:15], v34 offset:23040\n" \
M_"v[128:131], a[16:19], a[192:195], 0\n" \
"v_readlane_b32 s85, v11, 10\n" \
"s_waitcnt lgkmcnt(9)\n" \
M_"v[128:131], a[20:23], a[196:199], v[128:131]\n" \
B_"v1, s[80:83], s66 offen offset:1024 lds\n" \
M_"v[132:135], a[16:19], a[208:211], 0\n" \
"s_mov_b32 s86, 0xa1ca0\n" \
"s_mov_b32 s87, 0x20000\n" \
M_"v[132:135], a[20:23], a[212:215], v[132:135]\n" \
"v_add_u32_e32 v33, 0x10000, v10\n" \
"v_readlane_b32 s68, v11, 34\n" \
M_"v[200:203], a[16:19], v[64:67], 0\n" \
"v_readlane_b32 s69, v11, 35\n" \
"v_readlane_b32 s72, v11, 37\n" \
M_"v[200:203], a[20:23], v[68:71], v[200:203]\n" \
"v_readlane_b32 s93, v11, 38\n" \
"v_readlane_b32 s63, v11, 44\n" \
M_"v[204:207], a[16:19], v[80:83], 0\n" \
X_"a[16:17], v34 offset:18464\n" \
X_"a[18:19], v34 offset:23072\n" \
M_"v[204:207], a[20:23], v[84:87], v[204:207]\n" \
X_"a[20:21], v34 offset:18496\n" \
X_"a[22:23], v34 offset:23104\n" \
M_"v[212:215], a[24:27], v[80:83], 0\n" \
B_"v2, s[80:83], s66 offen offset:2048 lds\n" \
M_"v[212:215], a[28:31], v[84:87], v[212:215]\n" \
"v_bfe_u32 v40, v20, 2, 7\n" \
"s_add_u32 s74, s63, 0x3800\n" \
M_"v[208:211], a[24:27], v[64:67], 0\n" \
"v_lshlrev_b32_e32 v41, 12, v20\n" \
"s_add_u32 s12, s63, 0x0\n" \
M_"v[208:211], a[28:31], v[68:71], v[208:211]\n" \
"v_readlane_b32 s95, v11, 41\n" \
"v_readlane_b32 s96, v11, 42\n" \
M_"v[140:143], a[24:27], a[208:211], 0\n" \
"s_add_u32 s13, s63, 0x1000\n" \
"v_readlane_b32 s97, v11, 43\n" \
M_"v[140:143], a[28:31], a[212:215], v[140:143]\n" \
B_"v3, s[80:83], s66 offen offset:3072 lds\n" \
M_"v[136:139], a[24:27], a[192:195], 0\n" \
X_"a[24:25], v34 offset:18528\n" \
X_"a[26:27], v34 offset:23136\n" \
M_"v[136:139], a[28:31], a[196:199], v[136:139]\n" \
X_"a[28:29], v34 offset:18560\n" \
X_"a[30:31], v34 offset:23168\n" \
M_"v[144:147], a[96:99], a[192:195], 0\n" \
"s_add_u32 m0, s67, 0x1000\n" \
"s_add_u32 s14, s63, 0x2000\n" \
M_"v[144:147], a[100:103], a[196:199], v[144:147]\n" \
"s_add_u32 s15, s63, 0x3000\n" \
"v_readlane_b32 s100, v11, 7\n" \
M_"v[148:151], a[96:99], a[208:211], 0\n" \
B_"v4, s[80:83], s66 offen offset:0 lds\n" \
M_"v[148:151], a[100:103], a[212:215], v[148:151]\n" \
"v_readlane_b32 s16, v11, 45\n" \
"v_bfe_u32 v44, v20, 4, 4\n" \
M_"v[216:219], a[96:99], v[64:67], 0\n" \
"s_add_u32 s17, s16, 0x1000\n" \
"s_add_u32 s18, s16, 0x2000\n" \
M_"v[216:219], a[100:103], v[68:71], v[216:219]\n" \
"s_add_u32 s19, s16, 0x3000\n" \
"v_bfe_u32 v45, v20, 8, 2\n" \
M_"v[220:223], a[96:99], v[80:83], 0\n" \
X_"a[96:97], v34 offset:18592\n" \
X_"a[98:99], v34 offset:23200\n" \
M_"v[220:223], a[100:103], v[84:87], v[220:223]\n" \
X_"a[100:101], v34 offset:18624\n" \
X_"a[102:103], v34 offset:23232\n" \
M_"v[228:231], a[104:107], v[80:83], 0\n" \
B_"v5, s[80:83], s66 offen offset:1024 lds\n" \
M_"v[228:231], a[108:111], v[84:87], v[228:231]\n" \
"v_mul_u32_u24_e32 v42, 0x480, v44\n" \
"v_lshl_add_u32 v42, v45, 3, v42\n" \
M_"v[224:227], a[104:107], v[64:67], 0\n" \
"s_waitcnt vmcnt(14)\n" \
"v_cmp_ne_u32_e32 vcc, s62, v26\n" \
M_"v[224:227], a[108:111], v[68:71], v[224:227]\n" \
"v_lshrrev_b32_e32 v44, 1, v44\n" \
"v_lshlrev_b32_e32 v45, 1, v45\n" \
M_"v[156:159], a[104:107], a[208:211], 0\n" \
"v_sub_u32_e32 v44, v44, v45\n" \
"s_cbranch_vccnz L_sa_%=\n" \
"L_ra_%=:\n" \
M_"v[156:159], a[108:111], a[212:215], v[156:159]\n" \
B_"a[64:67], v20, s[56:59], s65 offen offset:0 sc1\n" \
M_"v[152:155], a[104:107], a[192:195], 0\n" \
X_"a[104:105], v34 offset:18656\n" \
X_"a[106:107], v34 offset:23264\n" \
M_"v[152:155], a[108:111], a[196:199], v[152:155]\n" \
X_"a[108:109], v34 offset:18688\n" \
X_"a[110:111], v34 offset:23296\n" \
M_"v[160:163], a[112:115], a[192:195], 0\n" \
"v_readlane_b32 s64, v11, 36\n" \
"v_and_b32_e32 v45, 16, v20\n" \
M_"v[160:163], a[116:119], a[196:199], v[160:163]\n" \
"v_mov_b32_e32 v46, 0x3f80\n" \
"v_lshlrev_b32_e32 v45, v45, v46\n" \
M_"v[164:167], a[112:115], a[208:211], 0\n" \
B_"a[68:71], v20, s[56:59], s65 offen offset:1024 sc1\n" \
M_"v[164:167], a[116:119], a[212:215], v[164:167]\n" \
"v_mov_b32_e32 v52, 0\n" \
"v_mov_b32_e32 v53, 0\n" \
M_"v[232:235], a[112:115], v[64:67], 0\n" \
"v_mov_b32_e32 v48, 0\n" \
"v_mov_b32_e32 v49, 0\n" \
M_"v[232:235], a[116:119], v[68:71], v[232:235]\n" \
"v_add_u32_e32 v43, 0x9000, v42\n" \
"v_readlane_b32 s88, v11, 13\n" \
M_"v[236:239], a[112:115], v[80:83], 0\n" \
X_"a[112:113], v10 offset:32768\n" \
X_"a[114:115], v10 offset:37376\n" \
M_"v[236:239], a[116:119], v[84:87], v[236:239]\n" \
X_"a[116:117], v10 offset:32800\n" \
"s_waitcnt lgkmcnt(7)\n" \
M_"v[244:247], a[120:123], v[80:83], 0\n" \
X_"a[118:119], v10 offset:37408\n" \
"v_readlane_b32 s89, v11, 14\n" \
M_"v[244:247], a[0:3], v[84:87], v[244:247]\n" \
B_"a[80:83], v32, s[56:59], s65 offen offset:0 sc1\n" \
M_"v[240:243], a[120:123], v[64:67], 0\n" \
"s_mov_b32 s90, 0x240000\n" \
"s_mov_b32 s91, 0x20000\n" \
M_"v[240:243], a[0:3], v[68:71], v[240:243]\n" \
"v_mov_b32_e32 v46, s62\n" \
M_"v[172:175], a[120:123], a[208:211], 0\n" \
M_"v[172:175], a[0:3], a[212:215], v[172:175]\n" \
M_"v[168:171], a[120:123], a[192:195], 0\n" \
X_"a[120:121], v10 offset:32832\n" \
X_"a[122:123], v10 offset:37440\n" \
M_"v[168:171], a[0:3], a[196:199], v[168:171]\n" \
X_"a[0:1], v10 offset:32864\n" \
X_"a[2:3], v10 offset:37472\n" \
M_"v[176:179], a[4:7], a[192:195], 0\n" \
B_"a[84:87], v32, s[56:59], s65 offen offset:1024 sc1\n" \
M_"v[176:179], a[8:11], a[196:199], v[176:179]\n" \
M_"v[180:183], a[4:7], a[208:211], 0\n" \
M_"v[180:183], a[8:11], a[212:215], v[180:183]\n" \
M_"v[248:251], a[4:7], v[64:67], 0\n" \
M_"v[248:251], a[8:11], v[68:71], v[248:251]\n" \
B_"a[72:75], v20, s[56:59], s65 offen offset:2048 sc1\n" \
M_"v[252:255], a[4:7], v[80:83], 0\n" \
X_"a[4:5], v10 offset:32896\n" \
X_"a[6:7], v10 offset:37504\n" \
M_"v[252:255], a[8:11], v[84:87], v[252:255]\n" \
X_"a[8:9], v10 offset:32928\n" \
X_"a[10:11], v10 offset:37536\n" \
M_"v[112:115], a[12:15], a[200:203], v[112:115]\n" \
M_"v[116:119], a[12:15], a[216:219], v[116:119]\n" \
M_"v[184:187], a[12:15], v[72:75], v[184:187]\n" \
B_"a[76:79], v20, s[56:59], s65 offen offset:3072 sc1\n" \
M_"v[188:191], a[12:15], v[96:99], v[188:191]\n" \
X_"a[12:13], v10 offset:32960\n" \
X_"a[14:15], v10 offset:37568\n" \
M_"v[196:199], a[16:19], v[96:99], v[196:199]\n" \
M_"v[192:195], a[16:19], v[72:75], v[192:195]\n" \
M_"v[124:127], a[16:19], a[216:219], v[124:127]\n" \
M_"v[120:123], a[16:19], a[200:203], v[120:123]\n" \
X_"a[16:17], v10 offset:32992\n" \
X_"a[18:19], v10 offset:37600\n" \
M_"v[128:131], a[20:23], a[200:203], v[128:131]\n" \
B_"a[88:91], v32, s[56:59], s65 offen offset:2048 sc1\n" \
M_"v[132:135], a[20:23], a[216:219], v[132:135]\n" \
M_"v[200:203], a[20:23], v[72:75], v[200:203]\n" \
M_"v[204:207], a[20:23], v[96:99], v[204:207]\n" \
X_"a[20:21], v10 offset:33024\n" \
X_"a[22:23], v10 offset:37632\n" \
"s_waitcnt lgkmcnt(10)\n" \
"s_barrier\n" \
M_"v[212:215], a[24:27], v[96:99], v[212:215]\n" \
B_"a[92:95], v32, s[56:59], s65 offen offset:3072 sc1\n" \
M_"v[208:211], a[24:27], v[72:75], v[208:211]\n" \
"v_readlane_b32 s65, v11, 49\n" \
M_"v[140:143], a[24:27], a[216:219], v[140:143]\n" \
M_"v[136:139], a[24:27], a[200:203], v[136:139]\n" \
X_"a[24:25], v35 offset:41984\n" \
X_"a[26:27], v35 offset:44288\n" \
M_"v[144:147], a[28:31], a[200:203], v[144:147]\n" \
M_"v[148:151], a[28:31], a[216:219], v[148:151]\n" \
B_"v6, s[80:83], s66 offen offset:2048 lds\n" \
M_"v[216:219], a[28:31], v[72:75], v[216:219]\n" \
M_"v[220:223], a[28:31], v[96:99], v[220:223]\n" \
X_"a[28:29], v35 offset:52352\n" \
X_"a[30:31], v35 offset:54656\n" \
M_"v[228:231], a[96:99], v[96:99], v[228:231]\n" \
M_"v[224:227], a[96:99], v[72:75], v[224:227]\n" \
M_"v[156:159], a[96:99], a[216:219], v[156:159]\n" \
B_"v7, s[80:83], s66 offen offset:3072 lds\n" \
M_"v[152:155], a[96:99], a[200:203], v[152:155]\n" \
X_"a[96:97], v35 offset:42016\n" \
X_"a[98:99], v35 offset:44320\n" \
M_"v[160:163], a[100:103], a[200:203], v[160:163]\n" \
"s_add_u32 m0, s67, 0x2000\n" \
"v_readlane_b32 s67, v11, 51\n" \
M_"v[164:167], a[100:103], a[216:219], v[164:167]\n" \
M_"v[232:235], a[100:103], v[72:75], v[232:235]\n" \
M_"v[236:239], a[100:103], v[96:99], v[236:239]\n" \
X_"a[100:101], v35 offset:52384\n" \
X_"a[102:103], v35 offset:54688\n" \
M_"v[244:247], a[104:107], v[96:99], v[244:247]\n" \
B_"v8, s[80:83], s66 offen offset:0 lds\n" \
M_"v[240:243], a[104:107], v[72:75], v[240:243]\n" \
"s_add_u32 m0, s92, 0x0\n" \
"v_readlane_b32 s66, v11, 50\n" \
M_"v[172:175], a[104:107], a[216:219], v[172:175]\n" \
M_"v[168:171], a[104:107], a[200:203], v[168:171]\n" \
X_"a[104:105], v35 offset:42048\n" \
X_"a[106:107], v35 offset:44352\n" \
M_"v[176:179], a[108:111], a[200:203], v[176:179]\n" \
M_"v[180:183], a[108:111], a[216:219], v[180:183]\n" \
B_"v0, s[80:83], s73 offen offset:0 lds\n" \
M_"v[248:251], a[108:111], v[72:75], v[248:251]\n" \
M_"v[252:255], a[108:111], v[96:99], v[252:255]\n" \
X_"a[108:109], v35 offset:52416\n" \
X_"a[110:111], v35 offset:54720\n" \
M_"v[112:115], a[112:115], a[204:207], v[112:115]\n" \
M_"v[116:119], a[112:115], a[220:223], v[116:119]\n" \
M_"v[184:187], a[112:115], v[76:79], v[184:187]\n" \
B_"v1, s[80:83], s73 offen offset:1024 lds\n" \
M_"v[188:191], a[112:115], v[100:103], v[188:191]\n" \
X_"a[112:113], v35 offset:42080\n" \
X_"a[114:115], v35 offset:44384\n" \
M_"v[196:199], a[116:119], v[100:103], v[196:199]\n" \
M_"v[192:195], a[116:119], v[76:79], v[192:195]\n" \
"s_waitcnt vmcnt(18)\n" \
"s_mov_b64 exec, 1\n" \
"buffer_store_dword v39, off, s[56:59], s70 offset:0 sc1\n" \
"s_mov_b64 exec, -1\n" \
M_"v[124:127], a[116:119], a[220:223], v[124:127]\n" \
"v_readlane_b32 s70, v11, 54\n" \
M_"v[120:123], a[116:119], a[204:207], v[120:123]\n" \
X_"a[116:117], v35 offset:52448\n" \
X_"a[118:119], v35 offset:54752\n" \
M_"v[128:131], a[120:123], a[204:207], v[128:131]\n" \
M_"v[132:135], a[120:123], a[220:223], v[132:135]\n" \
B_"v2, s[80:83], s73 offen offset:2048 lds\n" \
M_"v[200:203], a[120:123], v[76:79], v[200:203]\n" \
M_"v[204:207], a[120:123], v[100:103], v[204:207]\n" \
X_"a[120:121], v35 offset:42112\n" \
X_"a[122:123], v35 offset:44416\n" \
M_"v[212:215], a[0:3], v[100:103], v[212:215]\n" \
M_"v[208:211], a[0:3], v[76:79], v[208:211]\n" \
M_"v[140:143], a[0:3], a[220:223], v[140:143]\n" \
B_"v3, s[80:83], s73 offen offset:3072 lds\n" \
M_"v[136:139], a[0:3], a[204:207], v[136:139]\n" \
X_"a[0:1], v35 offset:52480\n" \
"s_waitcnt lgkmcnt(7)\n" \
M_"v[144:147], a[4:7], a[204:207], v[144:147]\n" \
X_"a[2:3], v35 offset:54784\n" \
"s_add_u32 m0, s92, 0x1000\n" \
M_"v[148:151], a[4:7], a[220:223], v[148:151]\n" \
M_"v[216:219], a[4:7], v[76:79], v[216:219]\n" \
M_"v[220:223], a[4:7], v[100:103], v[220:223]\n" \
X_"a[4:5], v35 offset:42144\n" \
X_"a[6:7], v35 offset:44448\n" \
M_"v[228:231], a[8:11], v[100:103], v[228:231]\n" \
B_"v4, s[80:83], s73 offen offset:0 lds\n" \
M_"v[224:227], a[8:11], v[76:79], v[224:227]\n" \
M_"v[156:159], a[8:11], a[220:223], v[156:159]\n" \
M_"v[152:155], a[8:11], a[204:207], v[152:155]\n" \
X_"a[8:9], v35 offset:52512\n" \
X_"a[10:11], v35 offset:54816\n" \
M_"v[160:163], a[12:15], a[204:207], v[160:163]\n" \
M_"v[164:167], a[12:15], a[220:223], v[164:167]\n" \
B_"v5, s[80:83], s73 offen offset:1024 lds\n" \
M_"v[232:235], a[12:15], v[76:79], v[232:235]\n" \
M_"v[236:239], a[12:15], v[100:103], v[236:239]\n" \
X_"a[12:13], v35 offset:42176\n" \
X_"a[14:15], v35 offset:44480\n" \
M_"v[244:247], a[16:19], v[100:103], v[244:247]\n" \
M_"v[240:243], a[16:19], v[76:79], v[240:243]\n" \
M_"v[172:175], a[16:19], a[220:223], v[172:175]\n" \
B_"v6, s[80:83], s73 offen offset:2048 lds\n" \
M_"v[168:171], a[16:19], a[204:207], v[168:171]\n" \
X_"a[16:17], v35 offset:52544\n" \
X_"a[18:19], v35 offset:54848\n" \
M_"v[176:179], a[20:23], a[204:207], v[176:179]\n" \
M_"v[180:183], a[20:23], a[220:223], v[180:183]\n" \
M_"v[248:251], a[20:23], v[76:79], v[248:251]\n" \
M_"v[252:255], a[20:23], v[100:103], v[252:255]\n" \
X_"a[20:21], v35 offset:42208\n" \
X_"a[22:23], v35 offset:44512\n" \
M_"v[112:115], a[24:27], a[124:127], v[112:115]\n" \
B_"v7, s[80:83], s73 offen offset:3072 lds\n" \
M_"v[112:115], a[28:31], a[224:227], v[112:115]\n" \
"s_add_u32 m0, s92, 0x2000\n" \
"v_readlane_b32 s92, v11, 49\n" \
M_"v[116:119], a[24:27], a[228:231], v[116:119]\n" \
M_"v[116:119], a[28:31], a[232:235], v[116:119]\n" \
M_"v[184:187], a[24:27], a[236:239], v[184:187]\n" \
M_"v[184:187], a[28:31], a[240:243], v[184:187]\n" \
B_"v8, s[80:83], s73 offen offset:0 lds\n" \
M_"v[188:191], a[24:27], a[244:247], v[188:191]\n" \
X_"a[24:25], v35 offset:52576\n" \
X_"a[26:27], v35 offset:54880\n" \
M_"v[188:191], a[28:31], a[248:251], v[188:191]\n" \
"s_mov_b32 m0, s94\n" \
X_"a[28:29], v35 offset:42240\n" \
M_"v[196:199], a[96:99], a[244:247], v[196:199]\n" \
X_"a[30:31], v35 offset:44544\n" \
"v_readlane_b32 s73, v11, 6\n" \
M_"v[196:199], a[100:103], a[248:251], v[196:199]\n" \
M_"v[192:195], a[96:99], a[236:239], v[192:195]\n" \
B_"v9, s[84:87], 0 offen lds\n" \
M_"v[192:195], a[100:103], a[240:243], v[192:195]\n" \
M_"v[124:127], a[96:99], a[228:231], v[124:127]\n" \
M_"v[124:127], a[100:103], a[232:235], v[124:127]\n" \
M_"v[120:123], a[96:99], a[124:127], v[120:123]\n" \
X_"a[96:97], v35 offset:52608\n" \
X_"a[98:99], v35 offset:54912\n" \
M_"v[120:123], a[100:103], a[224:227], v[120:123]\n" \
X_"a[100:101], v33 offset:0\n" \
X_"a[102:103], v33 offset:4608\n" \
M_"v[128:131], a[104:107], a[124:127], v[128:131]\n" \
B_"a[128:131], v20, s[56:59], s68 offen offset:0 sc1\n" \
M_"v[128:131], a[108:111], a[224:227], v[128:131]\n" \
M_"v[132:135], a[104:107], a[228:231], v[132:135]\n" \
M_"v[132:135], a[108:111], a[232:235], v[132:135]\n" \
M_"v[200:203], a[104:107], a[236:239], v[200:203]\n" \
M_"v[200:203], a[108:111], a[240:243], v[200:203]\n" \
B_"a[132:135], v20, s[56:59], s68 offen offset:1024 sc1\n" \
M_"v[204:207], a[104:107], a[244:247], v[204:207]\n" \
X_"a[104:105], v33 offset:9216\n" \
X_"a[106:107], v33 offset:13824\n" \
M_"v[204:207], a[108:111], a[248:251], v[204:207]\n" \
X_"a[108:109], v33 offset:32\n" \
"s_waitcnt lgkmcnt(7)\n" \
M_"v[212:215], a[112:115], a[244:247], v[212:215]\n" \
X_"a[110:111], v33 offset:4640\n" \
M_"v[212:215], a[116:119], a[248:251], v[212:215]\n" \
M_"v[208:211], a[112:115], a[236:239], v[208:211]\n" \
"buffer_load_dword v47, v38, s[56:59], s71 offen sc1\n" \
M_"v[208:211], a[116:119], a[240:243], v[208:211]\n" \
M_"v[140:143], a[112:115], a[228:231], v[140:143]\n" \
M_"v[140:143], a[116:119], a[232:235], v[140:143]\n" \
M_"v[136:139], a[112:115], a[124:127], v[136:139]\n" \
X_"a[112:113], v33 offset:9248\n" \
X_"a[114:115], v33 offset:13856\n" \
M_"v[136:139], a[116:119], a[224:227], v[136:139]\n" \
X_"a[116:117], v33 offset:64\n" \
X_"a[118:119], v33 offset:4672\n" \
M_"v[144:147], a[120:123], a[124:127], v[144:147]\n" \
B_"a[144:147], v32, s[56:59], s68 offen offset:0 sc1\n" \
M_"v[144:147], a[0:3], a[224:227], v[144:147]\n" \
M_"v[148:151], a[120:123], a[228:231], v[148:151]\n" \
M_"v[148:151], a[0:3], a[232:235], v[148:151]\n" \
M_"v[216:219], a[120:123], a[236:239], v[216:219]\n" \
M_"v[216:219], a[0:3], a[240:243], v[216:219]\n" \
B_"a[148:151], v32, s[56:59], s68 offen offset:1024 sc1\n" \
M_"v[220:223], a[120:123], a[244:247], v[220:223]\n" \
X_"a[120:121], v33 offset:9280\n" \
X_"a[122:123], v33 offset:13888\n" \
M_"v[220:223], a[0:3], a[248:251], v[220:223]\n" \
X_"a[0:1], v33 offset:96\n" \
X_"a[2:3], v33 offset:4704\n" \
M_"v[228:231], a[4:7], a[244:247], v[228:231]\n" \
M_"v[228:231], a[8:11], a[248:251], v[228:231]\n" \
M_"v[224:227], a[4:7], a[236:239], v[224:227]\n" \
B_"a[152:155], v32, s[56:59], s68 offen offset:2048 sc1\n" \
M_"v[224:227], a[8:11], a[240:243], v[224:227]\n" \
M_"v[156:159], a[4:7], a[228:231], v[156:159]\n" \
M_"v[156:159], a[8:11], a[232:235], v[156:159]\n" \
M_"v[152:155], a[4:7], a[124:127], v[152:155]\n" \
X_"a[4:5], v33 offset:9312\n" \
X_"a[6:7], v33 offset:13920\n" \
M_"v[152:155], a[8:11], a[224:227], v[152:155]\n" \
X_"a[8:9], v33 offset:128\n" \
X_"a[10:11], v33 offset:4736\n" \
M_"v[160:163], a[12:15], a[124:127], v[160:163]\n" \
B_"a[156:159], v32, s[56:59], s68 offen offset:3072 sc1\n" \
M_"v[160:163], a[16:19], a[224:227], v[160:163]\n" \
M_"v[164:167], a[12:15], a[228:231], v[164:167]\n" \
M_"v[164:167], a[16:19], a[232:235], v[164:167]\n" \
M_"v[232:235], a[12:15], a[236:239], v[232:235]\n" \
M_"v[232:235], a[16:19], a[240:243], v[232:235]\n" \
B_"a[136:139], v20, s[56:59], s68 offen offset:2048 sc1\n" \
M_"v[236:239], a[12:15], a[244:247], v[236:239]\n" \
X_"a[12:13], v33 offset:9344\n" \
X_"a[14:15], v33 offset:13952\n" \
M_"v[236:239], a[16:19], a[248:251], v[236:239]\n" \
X_"a[16:17], v33 offset:160\n" \
X_"a[18:19], v33 offset:4768\n" \
M_"v[244:247], a[20:23], a[244:247], v[244:247]\n" \
M_"v[244:247], a[24:27], a[248:251], v[244:247]\n" \
M_"v[240:243], a[20:23], a[236:239], v[240:243]\n" \
B_"a[140:143], v20, s[56:59], s68 offen offset:3072 sc1\n" \
M_"v[240:243], a[24:27], a[240:243], v[240:243]\n" \
"v_readlane_b32 s68, v11, 52\n" \
M_"v[172:175], a[20:23], a[228:231], v[172:175]\n" \
M_"v[172:175], a[24:27], a[232:235], v[172:175]\n" \
M_"v[168:171], a[20:23], a[124:127], v[168:171]\n" \
X_"a[20:21], v33 offset:9376\n" \
X_"a[22:23], v33 offset:13984\n" \
M_"v[168:171], a[24:27], a[224:227], v[168:171]\n" \
X_"a[24:25], v33 offset:192\n" \
X_"a[26:27], v33 offset:4800\n" \
M_"v[176:179], a[28:31], a[124:127], v[176:179]\n" \
"s_waitcnt lgkmcnt(8)\n" \
M_"v[176:179], a[96:99], a[224:227], v[176:179]\n" \
B_"a[32:35], v20, s[56:59], s69 offen offset:0 sc1\n" \
M_"v[180:183], a[28:31], a[228:231], v[180:183]\n" \
M_"v[180:183], a[96:99], a[232:235], v[180:183]\n" \
M_"v[248:251], a[28:31], a[236:239], v[248:251]\n" \
M_"v[248:251], a[96:99], a[240:243], v[248:251]\n" \
M_"v[252:255], a[28:31], a[244:247], v[252:255]\n" \
X_"a[28:29], v33 offset:9408\n" \
X_"a[30:31], v33 offset:14016\n" \
M_"v[252:255], a[96:99], a[248:251], v[252:255]\n" \
X_"a[96:97], v33 offset:224\n" \
"s_waitcnt vmcnt(18)\n" \
M_"v[112:115], a[100:103], a[64:67], v[112:115]\n" \
B_"a[36:39], v20, s[56:59], s69 offen offset:1024 sc1\n" \
M_"v[112:115], a[104:107], a[68:71], v[112:115]\n" \
X_"a[98:99], v33 offset:4832\n" \
M_"v[116:119], a[100:103], a[80:83], v[116:119]\n" \
X_"a[100:101], v33 offset:9440\n" \
X_"a[102:103], v33 offset:14048\n" \
M_"v[116:119], a[104:107], a[84:87], v[116:119]\n" \
X_"a[104:105], v33 offset:256\n" \
X_"a[106:107], v33 offset:4864\n" \
M_"v[124:127], a[108:111], a[80:83], v[124:127]\n" \
M_"v[124:127], a[112:115], a[84:87], v[124:127]\n" \
B_"a[48:51], v32, s[56:59], s69 offen offset:0 sc1\n" \
M_"v[120:123], a[108:111], a[64:67], v[120:123]\n" \
X_"a[108:109], v33 offset:9472\n" \
X_"a[110:111], v33 offset:14080\n" \
M_"v[120:123], a[112:115], a[68:71], v[120:123]\n" \
X_"a[112:113], v33 offset:18432\n" \
X_"a[114:115], v33 offset:23040\n" \
M_"v[128:131], a[116:119], a[64:67], v[128:131]\n" \
M_"v[128:131], a[120:123], a[68:71], v[128:131]\n" \
M_"v[132:135], a[116:119], a[80:83], v[132:135]\n" \
B_"a[52:55], v32, s[56:59], s69 offen offset:1024 sc1\n" \
M_"v[132:135], a[120:123], a[84:87], v[132:135]\n" \
X_"a[116:117], v33 offset:27648\n" \
X_"a[118:119], v33 offset:32256\n" \
M_"v[140:143], a[0:3], a[80:83], v[140:143]\n" \
X_"a[120:121], v33 offset:18464\n" \
X_"a[122:123], v33 offset:23072\n" \
M_"v[140:143], a[4:7], a[84:87], v[140:143]\n" \
"s_waitcnt vmcnt(10)\n" \
"v_cmp_ne_u32_e32 vcc, s62, v47\n" \
M_"v[136:139], a[0:3], a[64:67], v[136:139]\n" \
X_"a[0:1], v33 offset:27680\n" \
".fill (L_ab + 36864 - .) / 4, 4, 0xBF800000\n" \
"L_ap9:\n" \
"s_cbranch_execz L_ap0\n" \
X_"a[2:3], v33 offset:32288\n" \
M_"v[136:139], a[4:7], a[68:71], v[136:139]\n" \
B_"a[56:59], v32, s[56:59], s69 offen offset:2048 sc1\n" \
M_"v[144:147], a[8:11], a[64:67], v[144:147]\n" \
X_"a[4:5], v33 offset:18496\n" \
"s_waitcnt lgkmcnt(13)\n" \
M_"v[144:147], a[12:15], a[68:71], v[144:147]\n" \
X_"a[6:7], v33 offset:23104\n" \
"s_cbranch_vccnz L_sb_%=\n" \
"L_rb_%=:\n" \
M_"v[148:151], a[8:11], a[80:83], v[148:151]\n" \
X_"a[8:9], v33 offset:27712\n" \
X_"a[10:11], v33 offset:32320\n" \
M_"v[148:151], a[12:15], a[84:87], v[148:151]\n" \
X_"a[12:13], v33 offset:18528\n" \
X_"a[14:15], v33 offset:23136\n" \
B_"a[60:63], v32, s[56:59], s69 offen offset:3072 sc1\n" \
M_"v[156:159], a[16:19], a[80:83], v[156:159]\n" \
"v_readlane_b32 s71, v11, 55\n" \
"v_cmp_eq_u32_e32 vcc, 0, v44\n" \
M_"v[156:159], a[20:23], a[84:87], v[156:159]\n" \
M_"v[152:155], a[16:19], a[64:67], v[152:155]\n" \
X_"a[16:17], v33 offset:27744\n" \
X_"a[18:19], v33 offset:32352\n" \
M_"v[152:155], a[20:23], a[68:71], v[152:155]\n" \
X_"a[20:21], v33 offset:18560\n" \
X_"a[22:23], v33 offset:23168\n" \
B_"a[40:43], v20, s[56:59], s69 offen offset:2048 sc1\n" \
M_"v[160:163], a[24:27], a[64:67], v[160:163]\n" \
"v_cndmask_b32_e32 v50, 0, v45, vcc\n" \
"v_cmp_eq_u32_e32 vcc, 1, v44\n" \
M_"v[160:163], a[28:31], a[68:71], v[160:163]\n" \
"v_and_b32_e32 v44, 0x3c0, v20\n" \
"v_cndmask_b32_e32 v51, 0, v45, vcc\n" \
M_"v[164:167], a[24:27], a[80:83], v[164:167]\n" \
X_"a[24:25], v33 offset:27776\n" \
X_"a[26:27], v33 offset:32384\n" \
M_"v[164:167], a[28:31], a[84:87], v[164:167]\n" \
X_"a[28:29], v33 offset:18592\n" \
X_"a[30:31], v33 offset:23200\n" \
B_"a[44:47], v20, s[56:59], s69 offen offset:3072 sc1\n" \
M_"v[172:175], a[96:99], a[80:83], v[172:175]\n" \
"v_readlane_b32 s69, v11, 53\n" \
"v_bfe_u32 v45, v20, 1, 5\n" \
M_"v[172:175], a[100:103], a[84:87], v[172:175]\n" \
"v_and_b32_e32 v45, 24, v45\n" \
"v_add_u32_e32 v45, 0x4800, v45\n" \
M_"v[168:171], a[96:99], a[64:67], v[168:171]\n" \
X_"a[96:97], v33 offset:27808\n" \
X_"a[98:99], v33 offset:32416\n" \
M_"v[168:171], a[100:103], a[68:71], v[168:171]\n" \
X_"a[100:101], v33 offset:18624\n" \
"s_waitcnt lgkmcnt(13)\n" \
M_"v[176:179], a[104:107], a[64:67], v[176:179]\n" \
X_"a[102:103], v33 offset:23232\n" \
"v_add_u32_e32 v36, v44, v45\n" \
M_"v[176:179], a[108:111], a[68:71], v[176:179]\n" \
B_"a[124:127], v20, s[56:59], s64 offen offset:0 sc1\n" \
M_"v[180:183], a[104:107], a[80:83], v[180:183]\n" \
X_"a[104:105], v33 offset:27840\n" \
X_"a[106:107], v33 offset:32448\n" \
M_"v[180:183], a[108:111], a[84:87], v[180:183]\n" \
X_"a[108:109], v33 offset:18656\n" \
X_"a[110:111], v33 offset:23264\n" \
M_"v[112:115], a[112:115], a[72:75], v[112:115]\n" \
M_"v[112:115], a[116:119], a[76:79], v[112:115]\n" \
M_"v[116:119], a[112:115], a[88:91], v[116:119]\n" \
X_"a[112:113], v33 offset:27872\n" \
X_"a[114:115], v33 offset:32480\n" \
M_"v[116:119], a[116:119], a[92:95], v[116:119]\n" \
X_"a[116:117], v33 offset:18688\n" \
X_"a[118:119], v33 offset:23296\n" \
M_"v[124:127], a[120:123], a[88:91], v[124:127]\n" \
B_"a[224:227], v20, s[56:59], s64 offen offset:1024 sc1\n" \
M_"v[124:127], a[0:3], a[92:95], v[124:127]\n" \
M_"v[120:123], a[120:123], a[72:75], v[120:123]\n" \
X_"a[120:121], v33 offset:27904\n" \
X_"a[122:123], v33 offset:32512\n" \
M_"v[120:123], a[0:3], a[76:79], v[120:123]\n" \
"s_barrier\n" \
M_"v[128:131], a[4:7], a[72:75], v[128:131]\n" \
X_"a[0:1], v33 offset:36864\n" \
X_"a[2:3], v33 offset:41472\n" \
M_"v[128:131], a[8:11], a[76:79], v[128:131]\n" \
B_"a[236:239], v32, s[56:59], s64 offen offset:0 sc1\n" \
M_"v[132:135], a[4:7], a[88:91], v[132:135]\n" \
X_"a[4:5], v33 offset:46080\n" \
X_"a[6:7], v33 offset:50688\n" \
M_"v[132:135], a[8:11], a[92:95], v[132:135]\n" \
X_"a[8:9], v33 offset:55296\n" \
"s_waitcnt lgkmcnt(15)\n" \
M_"v[140:143], a[12:15], a[88:91], v[140:143]\n" \
X_"a[10:11], v33 offset:59904\n" \
M_"v[140:143], a[16:19], a[92:95], v[140:143]\n" \
M_"v[136:139], a[12:15], a[72:75], v[136:139]\n" \
X_"a[12:13], v10 offset:0\n" \
X_"a[14:15], v10 offset:4608\n" \
M_"v[136:139], a[16:19], a[76:79], v[136:139]\n" \
X_"a[16:17], v34 offset:0\n" \
X_"a[18:19], v34 offset:4608\n" \
M_"v[144:147], a[20:23], a[72:75], v[144:147]\n" \
B_"a[240:243], v32, s[56:59], s64 offen offset:1024 sc1\n" \
M_"v[144:147], a[24:27], a[76:79], v[144:147]\n" \
M_"v[148:151], a[20:23], a[88:91], v[148:151]\n" \
X_"a[20:21], v34 offset:9216\n" \
X_"a[22:23], v34 offset:13824\n" \
M_"v[148:151], a[24:27], a[92:95], v[148:151]\n" \
X_"a[24:25], v34 offset:18432\n" \
X_"a[26:27], v34 offset:23040\n" \
M_"v[156:159], a[28:31], a[88:91], v[156:159]\n" \
M_"v[156:159], a[96:99], a[92:95], v[156:159]\n" \
B_"a[244:247], v32, s[56:59], s64 offen offset:2048 sc1\n" \
M_"v[152:155], a[28:31], a[72:75], v[152:155]\n" \
X_"a[28:29], v10 offset:9216\n" \
X_"a[30:31], v10 offset:13824\n" \
M_"v[152:155], a[96:99], a[76:79], v[152:155]\n" \
X_"a[96:97], v33 offset:36896\n" \
X_"a[98:99], v33 offset:41504\n" \
M_"v[160:163], a[100:103], a[72:75], v[160:163]\n" \
"s_waitcnt lgkmcnt(15)\n" \
M_"v[160:163], a[104:107], a[76:79], v[160:163]\n" \
M_"v[164:167], a[100:103], a[88:91], v[164:167]\n" \
X_"a[100:101], v33 offset:46112\n" \
X_"a[102:103], v33 offset:50720\n" \
M_"v[164:167], a[104:107], a[92:95], v[164:167]\n" \
X_"a[104:105], v33 offset:55328\n" \
X_"a[106:107], v33 offset:59936\n" \
M_"v[172:175], a[108:111], a[88:91], v[172:175]\n" \
B_"a[248:251], v32, s[56:59], s64 offen offset:3072 sc1\n" \
M_"v[172:175], a[112:115], a[92:95], v[172:175]\n" \
M_"v[168:171], a[108:111], a[72:75], v[168:171]\n" \
X_"a[108:109], v10 offset:32\n" \
X_"a[110:111], v10 offset:4640\n" \
M_"v[168:171], a[112:115], a[76:79], v[168:171]\n" \
X_"a[112:113], v34 offset:32\n" \
X_"a[114:115], v34 offset:4640\n" \
M_"v[176:179], a[116:119], a[72:75], v[176:179]\n" \
M_"v[176:179], a[120:123], a[76:79], v[176:179]\n" \
B_"a[228:231], v20, s[56:59], s64 offen offset:2048 sc1\n" \
M_"v[180:183], a[116:119], a[88:91], v[180:183]\n" \
X_"a[116:117], v34 offset:9248\n" \
X_"a[118:119], v34 offset:13856\n" \
M_"v[180:183], a[120:123], a[92:95], v[180:183]\n" \
X_"a[120:121], v34 offset:18464\n" \
X_"a[122:123], v34 offset:23072\n" \
M_"v[112:115], a[0:3], a[128:131], v[112:115]\n" \
"s_waitcnt lgkmcnt(15)\n" \
M_"v[112:115], a[4:7], a[132:135], v[112:115]\n" \
"s_waitcnt vmcnt(18)\n" \
M_"v[116:119], a[0:3], a[144:147], v[116:119]\n" \
X_"a[0:1], v10 offset:9248\n" \
X_"a[2:3], v10 offset:13856\n" \
M_"v[116:119], a[4:7], a[148:151], v[116:119]\n" \
X_"a[4:5], v33 offset:36928\n" \
X_"a[6:7], v33 offset:41536\n" \
M_"v[116:119], a[8:11], a[152:155], v[116:119]\n" \
"s_waitcnt vmcnt(17)\n" \
M_"v[116:119], a[12:15], a[156:159], v[116:119]\n" \
"s_waitcnt vmcnt(16)\n" \
M_"v[112:115], a[8:11], a[136:139], v[112:115]\n" \
X_"a[8:9], v33 offset:46144\n" \
"s_waitcnt vmcnt(15)\n" \
M_"v[112:115], a[12:15], a[140:143], v[112:115]\n" \
X_"a[10:11], v33 offset:50752\n" \
"s_waitcnt vmcnt(14)\n" \
M_"v[112:115], a[16:19], a[32:35], v[112:115]\n" \
X_"a[12:13], v33 offset:55360\n" \
"s_waitcnt vmcnt(13)\n" \
M_"v[112:115], a[20:23], a[36:39], v[112:115]\n" \
X_"a[14:15], v33 offset:59968\n" \
"s_waitcnt vmcnt(12)\n" \
M_"v[116:119], a[16:19], a[48:51], v[116:119]\n" \
X_"a[16:17], v10 offset:64\n" \
"s_waitcnt vmcnt(11)\n" \
M_"v[116:119], a[20:23], a[52:55], v[116:119]\n" \
X_"a[18:19], v10 offset:4672\n" \
"s_waitcnt vmcnt(10)\n" \
M_"v[116:119], a[24:27], a[56:59], v[116:119]\n" \
X_"a[20:21], v34 offset:64\n" \
X_"a[22:23], v34 offset:4672\n" \
B_"a[232:235], v20, s[56:59], s64 offen offset:3072 sc1\n" \
"s_waitcnt vmcnt(10) lgkmcnt(12)\n" \
M_"v[116:119], a[28:31], a[60:63], v[116:119]\n" \
B_"a[160:163], v20, s[56:59], s72 offen offset:0 sc1\n" \
"s_waitcnt vmcnt(10)\n" \
M_"v[112:115], a[24:27], a[40:43], v[112:115]\n" \
X_"a[24:25], v34 offset:9280\n" \
X_"a[26:27], v34 offset:13888\n" \
B_"a[164:167], v20, s[56:59], s72 offen offset:1024 sc1\n" \
"s_waitcnt vmcnt(10)\n" \
M_"v[112:115], a[28:31], a[44:47], v[112:115]\n" \
X_"a[28:29], v34 offset:18496\n" \
X_"a[30:31], v34 offset:23104\n" \
M_"v[120:123], a[96:99], a[128:131], v[120:123]\n" \
B_"a[176:179], v32, s[56:59], s72 offen offset:0 sc1\n" \
M_"v[120:123], a[100:103], a[132:135], v[120:123]\n" \
M_"v[124:127], a[96:99], a[144:147], v[124:127]\n" \
X_"a[96:97], v10 offset:9280\n" \
X_"a[98:99], v10 offset:13888\n" \
C_"v112, v112, v113\n" \
C_"v113, v114, v115\n" \
C_"v114, v116, v117\n" \
C_"v115, v118, v119\n" \
"buffer_store_dwordx4 v[112:115], v20, s[56:59], s65 offen offset:0 sc1\n" \
"s_add_u32 s65, s65, 0x3800\n" \
M_"v[124:127], a[100:103], a[148:151], v[124:127]\n" \
X_"a[100:101], v33 offset:36960\n" \
X_"a[102:103], v33 offset:41568\n" \
M_"v[124:127], a[104:107], a[152:155], v[124:127]\n" \
M_"v[124:127], a[108:111], a[156:159], v[124:127]\n" \
M_"v[120:123], a[104:107], a[136:139], v[120:123]\n" \
X_"a[104:105], v33 offset:46176\n" \
X_"a[106:107], v33 offset:50784\n" \
M_"v[120:123], a[108:111], a[140:143], v[120:123]\n" \
X_"a[108:109], v33 offset:55392\n" \
X_"a[110:111], v33 offset:60000\n" \
M_"v[120:123], a[112:115], a[32:35], v[120:123]\n" \
B_"a[180:183], v32, s[56:59], s72 offen offset:1024 sc1\n" \
M_"v[120:123], a[116:119], a[36:39], v[120:123]\n" \
M_"v[124:127], a[112:115], a[48:51], v[124:127]\n" \
X_"a[112:113], v10 offset:96\n" \
X_"a[114:115], v10 offset:4704\n" \
M_"v[124:127], a[116:119], a[52:55], v[124:127]\n" \
X_"a[116:117], v34 offset:96\n" \
X_"a[118:119], v34 offset:4704\n" \
M_"v[124:127], a[120:123], a[56:59], v[124:127]\n" \
"s_waitcnt lgkmcnt(12)\n" \
M_"v[124:127], a[0:3], a[60:63], v[124:127]\n" \
B_"a[168:171], v20, s[56:59], s72 offen offset:2048 sc1\n" \
M_"v[120:123], a[120:123], a[40:43], v[120:123]\n" \
X_"a[120:121], v34 offset:9312\n" \
X_"a[122:123], v34 offset:13920\n" \
M_"v[120:123], a[0:3], a[44:47], v[120:123]\n" \
X_"a[0:1], v34 offset:18528\n" \
X_"a[2:3], v34 offset:23136\n" \
M_"v[128:131], a[4:7], a[128:131], v[128:131]\n" \
M_"v[128:131], a[8:11], a[132:135], v[128:131]\n" \
M_"v[132:135], a[4:7], a[144:147], v[132:135]\n" \
X_"a[4:5], v10 offset:9312\n" \
X_"a[6:7], v10 offset:13920\n" \
M_"v[132:135], a[8:11], a[148:151], v[132:135]\n" \
C_"v120, v120, v121\n" \
C_"v121, v122, v123\n" \
C_"v122, v124, v125\n" \
C_"v123, v126, v127\n" \
"buffer_store_dwordx4 v[120:123], v20, s[56:59], s66 offen offset:0 sc1\n" \
"s_add_u32 s66, s66, 0x3000\n" \
M_"v[132:135], a[12:15], a[152:155], v[132:135]\n" \
X_"a[8:9], v33 offset:36992\n" \
X_"a[10:11], v33 offset:41600\n" \
M_"v[132:135], a[16:19], a[156:159], v[132:135]\n" \
M_"v[128:131], a[12:15], a[136:139], v[128:131]\n" \
X_"a[12:13], v33 offset:46208\n" \
X_"a[14:15], v33 offset:50816\n" \
M_"v[128:131], a[16:19], a[140:143], v[128:131]\n" \
X_"a[16:17], v33 offset:55424\n" \
X_"a[18:19], v33 offset:60032\n" \
M_"v[128:131], a[20:23], a[32:35], v[128:131]\n" \
B_"a[184:187], v32, s[56:59], s72 offen offset:2048 sc1\n" \
M_"v[128:131], a[24:27], a[36:39], v[128:131]\n" \
M_"v[132:135], a[20:23], a[48:51], v[132:135]\n" \
X_"a[20:21], v10 offset:128\n" \
X_"a[22:23], v10 offset:4736\n" \
M_"v[132:135], a[24:27], a[52:55], v[132:135]\n" \
X_"a[24:25], v34 offset:128\n" \
X_"a[26:27], v34 offset:4736\n" \
M_"v[132:135], a[28:31], a[56:59], v[132:135]\n" \
"s_waitcnt lgkmcnt(12)\n" \
M_"v[132:135], a[96:99], a[60:63], v[132:135]\n" \
B_"a[172:175], v20, s[56:59], s72 offen offset:3072 sc1\n" \
M_"v[128:131], a[28:31], a[40:43], v[128:131]\n" \
X_"a[28:29], v34 offset:9344\n" \
X_"a[30:31], v34 offset:13952\n" \
M_"v[128:131], a[96:99], a[44:47], v[128:131]\n" \
X_"a[96:97], v34 offset:18560\n" \
X_"a[98:99], v34 offset:23168\n" \
M_"v[136:139], a[100:103], a[128:131], v[136:139]\n" \
M_"v[136:139], a[104:107], a[132:135], v[136:139]\n" \
M_"v[140:143], a[100:103], a[144:147], v[140:143]\n" \
B_"a[188:191], v32, s[56:59], s72 offen offset:3072 sc1\n" \
M_"v[140:143], a[104:107], a[148:151], v[140:143]\n" \
"v_readlane_b32 s72, v11, 56\n" \
C_"v128, v128, v129\n" \
C_"v129, v130, v131\n" \
C_"v130, v132, v133\n" \
C_"v131, v134, v135\n" \
M_"v[140:143], a[108:111], a[152:155], v[140:143]\n" \
"buffer_store_dwordx4 v[128:131], v20, s[56:59], s67 offen offset:0 sc1\n" \
"s_add_u32 s67, s67, 0x2800\n" \
M_"v[140:143], a[112:115], a[156:159], v[140:143]\n" \
X_"a[100:101], v10 offset:9344\n" \
X_"a[102:103], v10 offset:13952\n" \
M_"v[136:139], a[108:111], a[136:139], v[136:139]\n" \
X_"a[104:105], v33 offset:37024\n" \
X_"a[106:107], v33 offset:41632\n" \
M_"v[136:139], a[112:115], a[140:143], v[136:139]\n" \
X_"a[108:109], v33 offset:46240\n" \
X_"a[110:111], v33 offset:50848\n" \
M_"v[136:139], a[116:119], a[32:35], v[136:139]\n" \
X_"a[112:113], v33 offset:55456\n" \
X_"a[114:115], v33 offset:60064\n" \
M_"v[136:139], a[120:123], a[36:39], v[136:139]\n" \
B_"a[192:195], v20, s[56:59], s93 offen offset:0 sc1\n" \
M_"v[140:143], a[116:119], a[48:51], v[140:143]\n" \
X_"a[116:117], v10 offset:160\n" \
X_"a[118:119], v10 offset:4768\n" \
M_"v[140:143], a[120:123], a[52:55], v[140:143]\n" \
X_"a[120:121], v34 offset:160\n" \
X_"a[122:123], v34 offset:4768\n" \
M_"v[140:143], a[0:3], a[56:59], v[140:143]\n" \
"s_waitcnt lgkmcnt(13)\n" \
M_"v[140:143], a[4:7], a[60:63], v[140:143]\n" \
M_"v[136:139], a[0:3], a[40:43], v[136:139]\n" \
B_"a[196:199], v20, s[56:59], s93 offen offset:1024 sc1\n" \
M_"v[136:139], a[4:7], a[44:47], v[136:139]\n" \
X_"a[0:1], v34 offset:9376\n" \
X_"a[2:3], v34 offset:13984\n" \
M_"v[144:147], a[8:11], a[128:131], v[144:147]\n" \
X_"a[4:5], v34 offset:18592\n" \
X_"a[6:7], v34 offset:23200\n" \
M_"v[144:147], a[12:15], a[132:135], v[144:147]\n" \
M_"v[148:151], a[8:11], a[144:147], v[148:151]\n" \
X_"a[8:9], v10 offset:9376\n" \
C_"v136, v136, v137\n" \
C_"v137, v138, v139\n" \
C_"v138, v140, v141\n" \
C_"v139, v142, v143\n" \
"buffer_store_dwordx4 v[136:139], v20, s[56:59], s68 offen offset:0 sc1\n" \
"s_add_u32 s68, s68, 0x2000\n" \
M_"v[148:151], a[12:15], a[148:151], v[148:151]\n" \
X_"a[10:11], v10 offset:13984\n" \
X_"a[12:13], v33 offset:37056\n" \
M_"v[148:151], a[16:19], a[152:155], v[148:151]\n" \
X_"a[14:15], v33 offset:41664\n" \
M_"v[148:151], a[20:23], a[156:159], v[148:151]\n" \
M_"v[144:147], a[16:19], a[136:139], v[144:147]\n" \
B_"a[208:211], v32, s[56:59], s93 offen offset:0 sc1\n" \
M_"v[144:147], a[20:23], a[140:143], v[144:147]\n" \
X_"a[16:17], v33 offset:46272\n" \
X_"a[18:19], v33 offset:50880\n" \
M_"v[144:147], a[24:27], a[32:35], v[144:147]\n" \
X_"a[20:21], v33 offset:55488\n" \
X_"a[22:23], v33 offset:60096\n" \
M_"v[144:147], a[28:31], a[36:39], v[144:147]\n" \
M_"v[148:151], a[24:27], a[48:51], v[148:151]\n" \
X_"a[24:25], v10 offset:192\n" \
X_"a[26:27], v10 offset:4800\n" \
B_"a[212:215], v32, s[56:59], s93 offen offset:1024 sc1\n" \
M_"v[148:151], a[28:31], a[52:55], v[148:151]\n" \
X_"a[28:29], v34 offset:192\n" \
"s_waitcnt lgkmcnt(13)\n" \
M_"v[148:151], a[96:99], a[56:59], v[148:151]\n" \
X_"a[30:31], v34 offset:4800\n" \
M_"v[148:151], a[100:103], a[60:63], v[148:151]\n" \
M_"v[144:147], a[96:99], a[40:43], v[144:147]\n" \
X_"a[96:97], v34 offset:9408\n" \
X_"a[98:99], v34 offset:14016\n" \
B_"a[216:219], v32, s[56:59], s93 offen offset:2048 sc1\n" \
M_"v[144:147], a[100:103], a[44:47], v[144:147]\n" \
X_"a[100:101], v34 offset:18624\n" \
X_"a[102:103], v34 offset:23232\n" \
M_"v[152:155], a[104:107], a[128:131], v[152:155]\n" \
M_"v[152:155], a[108:111], a[132:135], v[152:155]\n" \
M_"v[156:159], a[104:107], a[144:147], v[156:159]\n" \
X_"a[104:105], v10 offset:9408\n" \
X_"a[106:107], v10 offset:14016\n" \
B_"a[220:223], v32, s[56:59], s93 offen offset:3072 sc1\n" \
C_"v144, v144, v145\n" \
C_"v145, v146, v147\n" \
C_"v146, v148, v149\n" \
C_"v147, v150, v151\n" \
M_"v[156:159], a[108:111], a[148:151], v[156:159]\n" \
X_"a[108:109], v33 offset:256\n" \
X_"a[110:111], v33 offset:4864\n" \
"buffer_store_dwordx4 v[144:147], v20, s[56:59], s69 offen offset:0 sc1\n" \
"s_add_u32 s69, s69, 0x1800\n" \
M_"v[156:159], a[112:115], a[152:155], v[156:159]\n" \
M_"v[156:159], a[116:119], a[156:159], v[156:159]\n" \
M_"v[152:155], a[112:115], a[136:139], v[152:155]\n" \
X_"a[112:113], v33 offset:9472\n" \
X_"a[114:115], v33 offset:14080\n" \
M_"v[152:155], a[116:119], a[140:143], v[152:155]\n" \
B_"a[200:203], v20, s[56:59], s93 offen offset:2048 sc1\n" \
M_"v[152:155], a[120:123], a[32:35], v[152:155]\n" \
X_"a[116:117], v33 offset:18688\n" \
X_"a[118:119], v33 offset:23296\n" \
M_"v[152:155], a[0:3], a[36:39], v[152:155]\n" \
M_"v[156:159], a[120:123], a[48:51], v[156:159]\n" \
X_"a[120:121], v33 offset:27904\n" \
X_"a[122:123], v33 offset:32512\n" \
M_"v[156:159], a[0:3], a[52:55], v[156:159]\n" \
X_"a[0:1], v33 offset:37120\n" \
B_"a[204:207], v20, s[56:59], s93 offen offset:3072 sc1\n" \
"s_waitcnt lgkmcnt(11)\n" \
M_"v[156:159], a[4:7], a[56:59], v[156:159]\n" \
X_"a[2:3], v33 offset:41728\n" \
M_"v[156:159], a[8:11], a[60:63], v[156:159]\n" \
M_"v[152:155], a[4:7], a[40:43], v[152:155]\n" \
X_"a[4:5], v33 offset:46336\n" \
X_"a[6:7], v33 offset:50944\n" \
M_"v[152:155], a[8:11], a[44:47], v[152:155]\n" \
X_"a[8:9], v33 offset:55552\n" \
X_"a[10:11], v33 offset:60160\n" \
M_"v[160:163], a[12:15], a[128:131], v[160:163]\n" \
B_"v[64:67], v20, s[56:59], s95 offen offset:0 sc1\n" \
M_"v[160:163], a[16:19], a[132:135], v[160:163]\n" \
M_"v[164:167], a[12:15], a[144:147], v[164:167]\n" \
X_"a[12:13], v10 offset:256\n" \
X_"a[14:15], v10 offset:4864\n" \
C_"v152, v152, v153\n" \
C_"v153, v154, v155\n" \
C_"v154, v156, v157\n" \
C_"v155, v158, v159\n" \
"buffer_store_dwordx4 v[152:155], v20, s[56:59], s70 offen offset:0 sc1\n" \
"s_add_u32 s70, s70, 0x1000\n" \
M_"v[164:167], a[16:19], a[148:151], v[164:167]\n" \
X_"a[16:17], v34 offset:256\n" \
X_"a[18:19], v34 offset:4864\n" \
M_"v[164:167], a[20:23], a[152:155], v[164:167]\n" \
M_"v[164:167], a[24:27], a[156:159], v[164:167]\n" \
M_"v[160:163], a[20:23], a[136:139], v[160:163]\n" \
X_"a[20:21], v34 offset:9472\n" \
X_"a[22:23], v34 offset:14080\n" \
M_"v[160:163], a[24:27], a[140:143], v[160:163]\n" \
X_"a[24:25], v34 offset:18688\n" \
X_"a[26:27], v34 offset:23296\n" \
"s_waitcnt vmcnt(19)\n" \
"s_mov_b64 exec, 1\n" \
"buffer_store_dword v39, off, s[56:59], s65 offset:0 sc1\n" \
"s_mov_b64 exec, -1\n" \
M_"v[160:163], a[28:31], a[32:35], v[160:163]\n" \
M_"v[160:163], a[96:99], a[36:39], v[160:163]\n" \
M_"v[164:167], a[28:31], a[48:51], v[164:167]\n" \
X_"a[28:29], v10 offset:9472\n" \
X_"a[30:31], v10 offset:14080\n" \
M_"v[164:167], a[96:99], a[52:55], v[164:167]\n" \
X_"a[96:97], v33 offset:0\n" \
X_"a[98:99], v33 offset:4608\n" \
M_"v[164:167], a[100:103], a[56:59], v[164:167]\n" \
"s_waitcnt lgkmcnt(12)\n" \
M_"v[164:167], a[104:107], a[60:63], v[164:167]\n" \
B_"v[68:71], v20, s[56:59], s95 offen offset:1024 sc1\n" \
M_"v[160:163], a[100:103], a[40:43], v[160:163]\n" \
X_"a[100:101], v33 offset:9216\n" \
X_"a[102:103], v33 offset:13824\n" \
M_"v[160:163], a[104:107], a[44:47], v[160:163]\n" \
X_"a[104:105], v33 offset:18432\n" \
X_"a[106:107], v33 offset:23040\n" \
M_"v[248:251], a[108:111], a[124:127], v[248:251]\n" \
M_"v[248:251], a[112:115], a[224:227], v[248:251]\n" \
M_"v[252:255], a[108:111], a[236:239], v[252:255]\n" \
X_"a[108:109], v33 offset:27648\n" \
X_"a[110:111], v33 offset:32256\n" \
M_"v[252:255], a[112:115], a[240:243], v[252:255]\n" \
C_"v160, v160, v161\n" \
C_"v161, v162, v163\n" \
C_"v162, v164, v165\n" \
C_"v163, v166, v167\n" \
"buffer_store_dwordx4 v[160:163], v20, s[56:59], s71 offen offset:0 sc1\n" \
"s_add_u32 s71, s71, 0x800\n" \
M_"v[252:255], a[116:119], a[244:247], v[252:255]\n" \
X_"a[112:113], v33 offset:36864\n" \
X_"a[114:115], v33 offset:41472\n" \
M_"v[252:255], a[120:123], a[248:251], v[252:255]\n" \
M_"v[248:251], a[116:119], a[228:231], v[248:251]\n" \
X_"a[116:117], v33 offset:46080\n" \
X_"a[118:119], v33 offset:50688\n" \
"s_waitcnt vmcnt(19)\n" \
"s_mov_b64 exec, 1\n" \
"buffer_store_dword v39, off, s[56:59], s66 offset:4 sc1\n" \
"s_mov_b64 exec, -1\n" \
"v_readlane_b32 s66, v11, 50\n" \
M_"v[248:251], a[120:123], a[232:235], v[248:251]\n" \
X_"a[120:121], v33 offset:55296\n" \
X_"a[122:123], v33 offset:59904\n" \
M_"v[176:179], a[0:3], a[128:131], v[176:179]\n" \
M_"v[176:179], a[4:7], a[132:135], v[176:179]\n" \
M_"v[180:183], a[0:3], a[144:147], v[180:183]\n" \
B_"v[72:75], v32, s[56:59], s95 offen offset:0 sc1\n" \
M_"v[180:183], a[4:7], a[148:151], v[180:183]\n" \
M_"v[248:251], a[0:3], a[160:163], v[248:251]\n" \
M_"v[248:251], a[4:7], a[164:167], v[248:251]\n" \
M_"v[252:255], a[0:3], a[176:179], v[252:255]\n" \
X_"a[0:1], v10 offset:0\n" \
X_"a[2:3], v10 offset:4608\n" \
M_"v[252:255], a[4:7], a[180:183], v[252:255]\n" \
X_"a[4:5], v34 offset:0\n" \
"s_waitcnt vmcnt(20)\n" \
M_"v[252:255], a[8:11], a[184:187], v[252:255]\n" \
X_"a[6:7], v34 offset:4608\n" \
"s_waitcnt vmcnt(18) lgkmcnt(10)\n" \
M_"v[252:255], a[12:15], a[188:191], v[252:255]\n" \
B_"v[76:79], v32, s[56:59], s95 offen offset:1024 sc1\n" \
M_"v[248:251], a[8:11], a[168:171], v[248:251]\n" \
M_"v[248:251], a[12:15], a[172:175], v[248:251]\n" \
M_"v[180:183], a[8:11], a[152:155], v[180:183]\n" \
M_"v[180:183], a[12:15], a[156:159], v[180:183]\n" \
M_"v[176:179], a[8:11], a[136:139], v[176:179]\n" \
X_"a[8:9], v34 offset:9216\n" \
X_"a[10:11], v34 offset:13824\n" \
M_"v[176:179], a[12:15], a[140:143], v[176:179]\n" \
X_"a[12:13], v34 offset:18432\n" \
X_"a[14:15], v34 offset:23040\n" \
"s_waitcnt vmcnt(18)\n" \
"s_mov_b64 exec, 1\n" \
"buffer_store_dword v39, off, s[56:59], s67 offset:8 sc1\n" \
"s_mov_b64 exec, -1\n" \
"v_readlane_b32 s67, v11, 51\n" \
M_"v[176:179], a[16:19], a[32:35], v[176:179]\n" \
M_"v[176:179], a[20:23], a[36:39], v[176:179]\n" \
M_"v[180:183], a[16:19], a[48:51], v[180:183]\n" \
M_"v[180:183], a[20:23], a[52:55], v[180:183]\n" \
"s_waitcnt vmcnt(18)\n" \
M_"v[248:251], a[16:19], a[192:195], v[248:251]\n" \
"s_waitcnt vmcnt(17)\n" \
M_"v[248:251], a[20:23], a[196:199], v[248:251]\n" \
"s_waitcnt vmcnt(15)\n" \
M_"v[252:255], a[16:19], a[208:211], v[252:255]\n" \
X_"a[16:17], v10 offset:9216\n" \
"s_mov_b64 exec, 1\n" \
"buffer_store_dword v39, off, s[56:59], s68 offset:12 sc1\n" \
"s_mov_b64 exec, -1\n" \
"s_waitcnt vmcnt(15)\n" \
M_"v[252:255], a[20:23], a[212:215], v[252:255]\n" \
"v_readlane_b32 s68, v11, 52\n" \
"s_waitcnt vmcnt(14)\n" \
M_"v[252:255], a[24:27], a[216:219], v[252:255]\n" \
X_"a[18:19], v10 offset:13824\n" \
"s_waitcnt vmcnt(13)\n" \
M_"v[252:255], a[28:31], a[220:223], v[252:255]\n" \
X_"a[20:21], v33 offset:32\n" \
X_"a[22:23], v33 offset:4640\n" \
B_"v[80:83], v20, s[56:59], s96 offen offset:0 sc1\n" \
"s_waitcnt vmcnt(12)\n" \
M_"v[248:251], a[24:27], a[200:203], v[248:251]\n" \
"s_mov_b64 exec, 1\n" \
"buffer_store_dword v39, off, s[56:59], s69 offset:16 sc1\n" \
"s_mov_b64 exec, -1\n" \
"v_readlane_b32 s69, v11, 53\n" \
".fill (L_ab + 40960 - .) / 4, 4, 0xBF800000\n" \
"L_ap10:\n" \
"s_cbranch_execz L_ap0\n" \
"s_waitcnt vmcnt(12)\n" \
M_"v[248:251], a[28:31], a[204:207], v[248:251]\n" \
M_"v[180:183], a[24:27], a[56:59], v[180:183]\n" \
B_"v[84:87], v20, s[56:59], s96 offen offset:1024 sc1\n" \
M_"v[180:183], a[28:31], a[60:63], v[180:183]\n" \
M_"v[176:179], a[24:27], a[40:43], v[176:179]\n" \
X_"a[24:25], v33 offset:9248\n" \
X_"a[26:27], v33 offset:13856\n" \
M_"v[176:179], a[28:31], a[44:47], v[176:179]\n" \
X_"a[28:29], v33 offset:18464\n" \
C_"v248, v248, v249\n" \
C_"v249, v250, v251\n" \
C_"v250, v252, v253\n" \
C_"v251, v254, v255\n" \
"buffer_store_dwordx4 v[248:251], v20, s[56:59], s72 offen offset:1024 sc1\n" \
M_"v[184:187], a[96:99], a[124:127], v[184:187]\n" \
X_"a[30:31], v33 offset:23072\n" \
C_"v176, v176, v177\n" \
C_"v177, v178, v179\n" \
C_"v178, v180, v181\n" \
C_"v179, v182, v183\n" \
M_"v[184:187], a[100:103], a[224:227], v[184:187]\n" \
"buffer_store_dwordx4 v[176:179], v20, s[56:59], s72 offen offset:0 sc1\n" \
M_"v[188:191], a[96:99], a[236:239], v[188:191]\n" \
X_"a[96:97], v33 offset:27680\n" \
X_"a[98:99], v33 offset:32288\n" \
M_"v[188:191], a[100:103], a[240:243], v[188:191]\n" \
X_"a[100:101], v33 offset:36896\n" \
X_"a[102:103], v33 offset:41504\n" \
M_"v[188:191], a[104:107], a[244:247], v[188:191]\n" \
M_"v[188:191], a[108:111], a[248:251], v[188:191]\n" \
M_"v[184:187], a[104:107], a[228:231], v[184:187]\n" \
X_"a[104:105], v33 offset:46112\n" \
X_"a[106:107], v33 offset:50720\n" \
M_"v[184:187], a[108:111], a[232:235], v[184:187]\n" \
X_"a[108:109], v33 offset:55328\n" \
"s_waitcnt lgkmcnt(11)\n" \
M_"v[184:187], a[112:115], a[160:163], v[184:187]\n" \
B_"v[88:91], v32, s[56:59], s96 offen offset:0 sc1\n" \
M_"v[184:187], a[116:119], a[164:167], v[184:187]\n" \
X_"a[110:111], v33 offset:59936\n" \
M_"v[188:191], a[112:115], a[176:179], v[188:191]\n" \
X_"a[112:113], v10 offset:32\n" \
X_"a[114:115], v10 offset:4640\n" \
M_"v[188:191], a[116:119], a[180:183], v[188:191]\n" \
X_"a[116:117], v34 offset:32\n" \
X_"a[118:119], v34 offset:4640\n" \
M_"v[188:191], a[120:123], a[184:187], v[188:191]\n" \
M_"v[188:191], a[0:3], a[188:191], v[188:191]\n" \
B_"v[92:95], v32, s[56:59], s96 offen offset:1024 sc1\n" \
M_"v[184:187], a[120:123], a[168:171], v[184:187]\n" \
X_"a[120:121], v34 offset:9248\n" \
X_"a[122:123], v34 offset:13856\n" \
M_"v[184:187], a[0:3], a[172:175], v[184:187]\n" \
X_"a[0:1], v34 offset:18464\n" \
X_"a[2:3], v34 offset:23072\n" \
M_"v[184:187], a[4:7], a[192:195], v[184:187]\n" \
M_"v[184:187], a[8:11], a[196:199], v[184:187]\n" \
M_"v[188:191], a[4:7], a[208:211], v[188:191]\n" \
X_"a[4:5], v10 offset:9248\n" \
X_"a[6:7], v10 offset:13856\n" \
M_"v[188:191], a[8:11], a[212:215], v[188:191]\n" \
X_"a[8:9], v33 offset:64\n" \
X_"a[10:11], v33 offset:4672\n" \
M_"v[188:191], a[12:15], a[216:219], v[188:191]\n" \
B_"v[96:99], v20, s[56:59], s97 offen offset:0 sc1\n" \
M_"v[188:191], a[16:19], a[220:223], v[188:191]\n" \
M_"v[184:187], a[12:15], a[200:203], v[184:187]\n" \
X_"a[12:13], v33 offset:9280\n" \
X_"a[14:15], v33 offset:13888\n" \
M_"v[184:187], a[16:19], a[204:207], v[184:187]\n" \
"buffer_load_dword v37, v40, s[56:59], s74 offen sc1\n" \
M_"v[192:195], a[20:23], a[124:127], v[192:195]\n" \
X_"a[16:17], v33 offset:18496\n" \
"s_waitcnt lgkmcnt(13)\n" \
M_"v[192:195], a[24:27], a[224:227], v[192:195]\n" \
X_"a[18:19], v33 offset:23104\n" \
M_"v[196:199], a[20:23], a[236:239], v[196:199]\n" \
X_"a[20:21], v33 offset:27712\n" \
C_"v184, v184, v185\n" \
C_"v185, v186, v187\n" \
C_"v186, v188, v189\n" \
C_"v187, v190, v191\n" \
"buffer_store_dwordx4 v[184:187], v20, s[56:59], s92 offen offset:1024 sc1\n" \
"s_waitcnt vmcnt(18)\n" \
"s_mov_b64 exec, 1\n" \
"buffer_store_dword v39, off, s[56:59], s70 offset:20 sc1\n" \
"s_mov_b64 exec, -1\n" \
"s_add_u32 s92, s92, 0x3800\n" \
M_"v[196:199], a[24:27], a[240:243], v[196:199]\n" \
X_"a[22:23], v33 offset:32320\n" \
X_"a[24:25], v33 offset:36928\n" \
M_"v[196:199], a[28:31], a[244:247], v[196:199]\n" \
X_"a[26:27], v33 offset:41536\n" \
M_"v[196:199], a[96:99], a[248:251], v[196:199]\n" \
M_"v[192:195], a[28:31], a[228:231], v[192:195]\n" \
X_"a[28:29], v33 offset:46144\n" \
X_"a[30:31], v33 offset:50752\n" \
M_"v[192:195], a[96:99], a[232:235], v[192:195]\n" \
B_"v[100:103], v20, s[56:59], s97 offen offset:1024 sc1\n" \
M_"v[192:195], a[100:103], a[160:163], v[192:195]\n" \
X_"a[96:97], v33 offset:55360\n" \
X_"a[98:99], v33 offset:59968\n" \
M_"v[192:195], a[104:107], a[164:167], v[192:195]\n" \
M_"v[196:199], a[100:103], a[176:179], v[196:199]\n" \
X_"a[100:101], v10 offset:64\n" \
X_"a[102:103], v10 offset:4672\n" \
M_"v[196:199], a[104:107], a[180:183], v[196:199]\n" \
X_"a[104:105], v34 offset:64\n" \
X_"a[106:107], v34 offset:4672\n" \
M_"v[196:199], a[108:111], a[184:187], v[196:199]\n" \
B_"v[104:107], v32, s[56:59], s97 offen offset:0 sc1\n" \
M_"v[196:199], a[112:115], a[188:191], v[196:199]\n" \
M_"v[192:195], a[108:111], a[168:171], v[192:195]\n" \
X_"a[108:109], v34 offset:9280\n" \
X_"a[110:111], v34 offset:13888\n" \
M_"v[192:195], a[112:115], a[172:175], v[192:195]\n" \
X_"a[112:113], v34 offset:18496\n" \
"s_waitcnt lgkmcnt(14)\n" \
M_"v[192:195], a[116:119], a[192:195], v[192:195]\n" \
X_"a[114:115], v34 offset:23104\n" \
M_"v[192:195], a[120:123], a[196:199], v[192:195]\n" \
B_"v[108:111], v32, s[56:59], s97 offen offset:1024 sc1\n" \
M_"v[196:199], a[116:119], a[208:211], v[196:199]\n" \
X_"a[116:117], v10 offset:9280\n" \
X_"a[118:119], v10 offset:13888\n" \
M_"v[196:199], a[120:123], a[212:215], v[196:199]\n" \
X_"a[120:121], v33 offset:96\n" \
X_"a[122:123], v33 offset:4704\n" \
"s_waitcnt vmcnt(19)\n" \
"s_mov_b64 exec, 1\n" \
"buffer_store_dword v39, off, s[56:59], s71 offset:24 sc1\n" \
"s_mov_b64 exec, -1\n" \
M_"v[196:199], a[0:3], a[216:219], v[196:199]\n" \
M_"v[196:199], a[4:7], a[220:223], v[196:199]\n" \
M_"v[192:195], a[0:3], a[200:203], v[192:195]\n" \
X_"a[0:1], v33 offset:9312\n" \
X_"a[2:3], v33 offset:13920\n" \
M_"v[192:195], a[4:7], a[204:207], v[192:195]\n" \
X_"a[4:5], v33 offset:18528\n" \
X_"a[6:7], v33 offset:23136\n" \
M_"v[200:203], a[8:11], a[124:127], v[200:203]\n" \
M_"v[200:203], a[12:15], a[224:227], v[200:203]\n" \
M_"v[204:207], a[8:11], a[236:239], v[204:207]\n" \
X_"a[8:9], v33 offset:27744\n" \
X_"a[10:11], v33 offset:32352\n" \
M_"v[204:207], a[12:15], a[240:243], v[204:207]\n" \
C_"v192, v192, v193\n" \
C_"v193, v194, v195\n" \
C_"v194, v196, v197\n" \
C_"v195, v198, v199\n" \
"buffer_store_dwordx4 v[192:195], v20, s[56:59], s66 offen offset:1024 sc1\n" \
M_"v[204:207], a[16:19], a[244:247], v[204:207]\n" \
X_"a[12:13], v33 offset:36960\n" \
"s_waitcnt lgkmcnt(11)\n" \
M_"v[204:207], a[20:23], a[248:251], v[204:207]\n" \
X_"a[14:15], v33 offset:41568\n" \
M_"v[200:203], a[16:19], a[228:231], v[200:203]\n" \
X_"a[16:17], v33 offset:46176\n" \
X_"a[18:19], v33 offset:50784\n" \
M_"v[200:203], a[20:23], a[232:235], v[200:203]\n" \
X_"a[20:21], v33 offset:55392\n" \
X_"a[22:23], v33 offset:60000\n" \
M_"v[200:203], a[24:27], a[160:163], v[200:203]\n" \
M_"v[200:203], a[28:31], a[164:167], v[200:203]\n" \
M_"v[204:207], a[24:27], a[176:179], v[204:207]\n" \
X_"a[24:25], v10 offset:96\n" \
X_"a[26:27], v10 offset:4704\n" \
M_"v[204:207], a[28:31], a[180:183], v[204:207]\n" \
X_"a[28:29], v34 offset:96\n" \
X_"a[30:31], v34 offset:4704\n" \
M_"v[204:207], a[96:99], a[184:187], v[204:207]\n" \
M_"v[204:207], a[100:103], a[188:191], v[204:207]\n" \
M_"v[200:203], a[96:99], a[168:171], v[200:203]\n" \
X_"a[96:97], v34 offset:9312\n" \
X_"a[98:99], v34 offset:13920\n" \
M_"v[200:203], a[100:103], a[172:175], v[200:203]\n" \
X_"a[100:101], v34 offset:18528\n" \
X_"a[102:103], v34 offset:23136\n" \
M_"v[200:203], a[104:107], a[192:195], v[200:203]\n" \
M_"v[200:203], a[108:111], a[196:199], v[200:203]\n" \
M_"v[204:207], a[104:107], a[208:211], v[204:207]\n" \
X_"a[104:105], v10 offset:9312\n" \
X_"a[106:107], v10 offset:13920\n" \
M_"v[204:207], a[108:111], a[212:215], v[204:207]\n" \
X_"a[108:109], v33 offset:128\n" \
X_"a[110:111], v33 offset:4736\n" \
M_"v[204:207], a[112:115], a[216:219], v[204:207]\n" \
"s_waitcnt lgkmcnt(15)\n" \
M_"v[204:207], a[116:119], a[220:223], v[204:207]\n" \
M_"v[200:203], a[112:115], a[200:203], v[200:203]\n" \
X_"a[112:113], v33 offset:9344\n" \
X_"a[114:115], v33 offset:13952\n" \
M_"v[200:203], a[116:119], a[204:207], v[200:203]\n" \
X_"a[116:117], v33 offset:18560\n" \
X_"a[118:119], v33 offset:23168\n" \
M_"v[208:211], a[120:123], a[124:127], v[208:211]\n" \
M_"v[208:211], a[0:3], a[224:227], v[208:211]\n" \
M_"v[212:215], a[120:123], a[236:239], v[212:215]\n" \
X_"a[120:121], v33 offset:27776\n" \
X_"a[122:123], v33 offset:32384\n" \
M_"v[212:215], a[0:3], a[240:243], v[212:215]\n" \
C_"v200, v200, v201\n" \
C_"v201, v202, v203\n" \
C_"v202, v204, v205\n" \
C_"v203, v206, v207\n" \
"buffer_store_dwordx4 v[200:203], v20, s[56:59], s67 offen offset:1024 sc1\n" \
M_"v[212:215], a[4:7], a[244:247], v[212:215]\n" \
X_"a[0:1], v33 offset:36992\n" \
X_"a[2:3], v33 offset:41600\n" \
M_"v[212:215], a[8:11], a[248:251], v[212:215]\n" \
M_"v[208:211], a[4:7], a[228:231], v[208:211]\n" \
X_"a[4:5], v33 offset:46208\n" \
X_"a[6:7], v33 offset:50816\n" \
M_"v[208:211], a[8:11], a[232:235], v[208:211]\n" \
X_"a[8:9], v33 offset:55424\n" \
X_"a[10:11], v33 offset:60032\n" \
M_"v[208:211], a[12:15], a[160:163], v[208:211]\n" \
"s_waitcnt lgkmcnt(13)\n" \
M_"v[208:211], a[16:19], a[164:167], v[208:211]\n" \
M_"v[212:215], a[12:15], a[176:179], v[212:215]\n" \
"s_waitcnt vmcnt(8)\n" \
"v_cmp_eq_u32_e64 s[98:99], s62, v37\n" \
"s_mov_b64 exec, 3\n" \
"buffer_store_dword v39, v41, s[56:59], s73 offen sc1\n" \
"s_mov_b64 exec, -1\n" \
"s_andn2_b32 s75, 0x7f, s98\n" \
"s_mov_b32 s20, s75\n" \
"s_bitcmp1_b32 s20, 0\n" \
"s_cselect_b32 s93, 0x40000000, s12\n" \
"s_bitcmp1_b32 s20, 1\n" \
"s_cselect_b32 s94, 0x40000000, s12\n" \
"s_bitcmp1_b32 s20, 2\n" \
"s_cselect_b32 s95, 0x40000000, s13\n" \
"s_bitcmp1_b32 s20, 3\n" \
"s_cselect_b32 s96, 0x40000000, s13\n" \
"s_bitcmp1_b32 s20, 4\n" \
"s_cselect_b32 s97, 0x40000000, s14\n" \
"s_bitcmp1_b32 s20, 5\n" \
"s_cselect_b32 s64, 0x40000000, s14\n" \
"s_bitcmp1_b32 s20, 6\n" \
"s_cselect_b32 s65, 0x40000000, s15\n" \
"s_add_u32 s66, s66, 0x3000\n" \
"s_add_u32 s67, s67, 0x2800\n" \
M_"v[212:215], a[16:19], a[180:183], v[212:215]\n" \
B_"v[116:119], v20, s[56:59], s93 offen offset:0 sc1\n" \
M_"v[212:215], a[20:23], a[184:187], v[212:215]\n" \
X_"a[12:13], v10 offset:128\n" \
X_"a[14:15], v10 offset:4736\n" \
M_"v[212:215], a[24:27], a[188:191], v[212:215]\n" \
X_"a[16:17], v34 offset:128\n" \
X_"a[18:19], v34 offset:4736\n" \
M_"v[208:211], a[20:23], a[168:171], v[208:211]\n" \
X_"a[20:21], v34 offset:9344\n" \
X_"a[22:23], v34 offset:13952\n" \
M_"v[208:211], a[24:27], a[172:175], v[208:211]\n" \
X_"a[24:25], v34 offset:18560\n" \
X_"a[26:27], v34 offset:23168\n" \
M_"v[208:211], a[28:31], a[192:195], v[208:211]\n" \
M_"v[208:211], a[96:99], a[196:199], v[208:211]\n" \
M_"v[212:215], a[28:31], a[208:211], v[212:215]\n" \
X_"a[28:29], v10 offset:9344\n" \
X_"a[30:31], v10 offset:13952\n" \
M_"v[212:215], a[96:99], a[212:215], v[212:215]\n" \
X_"a[96:97], v33 offset:160\n" \
X_"a[98:99], v33 offset:4768\n" \
M_"v[212:215], a[100:103], a[216:219], v[212:215]\n" \
M_"v[212:215], a[104:107], a[220:223], v[212:215]\n" \
M_"v[208:211], a[100:103], a[200:203], v[208:211]\n" \
X_"a[100:101], v33 offset:9376\n" \
X_"a[102:103], v33 offset:13984\n" \
M_"v[208:211], a[104:107], a[204:207], v[208:211]\n" \
X_"a[104:105], v33 offset:18592\n" \
"s_waitcnt lgkmcnt(15)\n" \
M_"v[216:219], a[108:111], a[124:127], v[216:219]\n" \
X_"a[106:107], v33 offset:23200\n" \
M_"v[216:219], a[112:115], a[224:227], v[216:219]\n" \
B_"v[124:127], v20, s[56:59], s94 offen offset:2048 sc1\n" \
M_"v[220:223], a[108:111], a[236:239], v[220:223]\n" \
X_"a[108:109], v33 offset:27808\n" \
C_"v208, v208, v209\n" \
C_"v209, v210, v211\n" \
C_"v210, v212, v213\n" \
C_"v211, v214, v215\n" \
M_"v[220:223], a[112:115], a[240:243], v[220:223]\n" \
"buffer_store_dwordx4 v[208:211], v20, s[56:59], s68 offen offset:1024 sc1\n" \
"s_add_u32 s68, s68, 0x2000\n" \
M_"v[220:223], a[116:119], a[244:247], v[220:223]\n" \
X_"a[110:111], v33 offset:32416\n" \
X_"a[112:113], v33 offset:37024\n" \
M_"v[220:223], a[120:123], a[248:251], v[220:223]\n" \
X_"a[114:115], v33 offset:41632\n" \
M_"v[216:219], a[116:119], a[228:231], v[216:219]\n" \
X_"a[116:117], v33 offset:46240\n" \
X_"a[118:119], v33 offset:50848\n" \
M_"v[216:219], a[120:123], a[232:235], v[216:219]\n" \
X_"a[120:121], v33 offset:55456\n" \
X_"a[122:123], v33 offset:60064\n" \
M_"v[216:219], a[0:3], a[160:163], v[216:219]\n" \
M_"v[216:219], a[4:7], a[164:167], v[216:219]\n" \
M_"v[220:223], a[0:3], a[176:179], v[220:223]\n" \
X_"a[0:1], v10 offset:160\n" \
X_"a[2:3], v10 offset:4768\n" \
M_"v[220:223], a[4:7], a[180:183], v[220:223]\n" \
X_"a[4:5], v34 offset:160\n" \
X_"a[6:7], v34 offset:4768\n" \
M_"v[220:223], a[8:11], a[184:187], v[220:223]\n" \
"s_waitcnt lgkmcnt(13)\n" \
M_"v[220:223], a[12:15], a[188:191], v[220:223]\n" \
"s_waitcnt vmcnt(11)\n" \
"s_mov_b64 exec, 1\n" \
"buffer_store_dword v39, off, s[56:59], s92 offset:32 sc1\n" \
"s_mov_b64 exec, -1\n" \
M_"v[216:219], a[8:11], a[168:171], v[216:219]\n" \
X_"a[8:9], v34 offset:9376\n" \
X_"a[10:11], v34 offset:13984\n" \
M_"v[216:219], a[12:15], a[172:175], v[216:219]\n" \
X_"a[12:13], v34 offset:18592\n" \
X_"a[14:15], v34 offset:23200\n" \
M_"v[216:219], a[16:19], a[192:195], v[216:219]\n" \
M_"v[216:219], a[20:23], a[196:199], v[216:219]\n" \
B_"v[132:135], v20, s[56:59], s95 offen offset:0 sc1\n" \
M_"v[220:223], a[16:19], a[208:211], v[220:223]\n" \
X_"a[16:17], v10 offset:9376\n" \
X_"a[18:19], v10 offset:13984\n" \
M_"v[220:223], a[20:23], a[212:215], v[220:223]\n" \
X_"a[20:21], v33 offset:192\n" \
X_"a[22:23], v33 offset:4800\n" \
M_"v[220:223], a[24:27], a[216:219], v[220:223]\n" \
M_"v[220:223], a[28:31], a[220:223], v[220:223]\n" \
M_"v[216:219], a[24:27], a[200:203], v[216:219]\n" \
X_"a[24:25], v33 offset:9408\n" \
X_"a[26:27], v33 offset:14016\n" \
M_"v[216:219], a[28:31], a[204:207], v[216:219]\n" \
"buffer_load_dword v37, v40, s[56:59], s74 offen sc1\n" \
M_"v[224:227], a[96:99], a[124:127], v[224:227]\n" \
X_"a[28:29], v33 offset:18624\n" \
X_"a[30:31], v33 offset:23232\n" \
M_"v[224:227], a[100:103], a[224:227], v[224:227]\n" \
M_"v[228:231], a[96:99], a[236:239], v[228:231]\n" \
X_"a[96:97], v33 offset:27840\n" \
X_"a[98:99], v33 offset:32448\n" \
C_"v216, v216, v217\n" \
C_"v217, v218, v219\n" \
C_"v218, v220, v221\n" \
C_"v219, v222, v223\n" \
"buffer_store_dwordx4 v[216:219], v20, s[56:59], s69 offen offset:1024 sc1\n" \
"s_add_u32 s69, s69, 0x1800\n" \
M_"v[228:231], a[100:103], a[240:243], v[228:231]\n" \
X_"a[100:101], v33 offset:37056\n" \
"s_waitcnt lgkmcnt(12)\n" \
M_"v[228:231], a[104:107], a[244:247], v[228:231]\n" \
X_"a[102:103], v33 offset:41664\n" \
M_"v[228:231], a[108:111], a[248:251], v[228:231]\n" \
M_"v[224:227], a[104:107], a[228:231], v[224:227]\n" \
B_"v[140:143], v20, s[56:59], s96 offen offset:2048 sc1\n" \
M_"v[224:227], a[108:111], a[232:235], v[224:227]\n" \
X_"a[104:105], v33 offset:46272\n" \
X_"a[106:107], v33 offset:50880\n" \
M_"v[224:227], a[112:115], a[160:163], v[224:227]\n" \
X_"a[108:109], v33 offset:55488\n" \
X_"a[110:111], v33 offset:60096\n" \
M_"v[224:227], a[116:119], a[164:167], v[224:227]\n" \
M_"v[228:231], a[112:115], a[176:179], v[228:231]\n" \
X_"a[112:113], v10 offset:192\n" \
X_"a[114:115], v10 offset:4800\n" \
M_"v[228:231], a[116:119], a[180:183], v[228:231]\n" \
X_"a[116:117], v34 offset:192\n" \
X_"a[118:119], v34 offset:4800\n" \
M_"v[228:231], a[120:123], a[184:187], v[228:231]\n" \
M_"v[228:231], a[0:3], a[188:191], v[228:231]\n" \
M_"v[224:227], a[120:123], a[168:171], v[224:227]\n" \
X_"a[120:121], v34 offset:9408\n" \
X_"a[122:123], v34 offset:14016\n" \
M_"v[224:227], a[0:3], a[172:175], v[224:227]\n" \
X_"a[0:1], v34 offset:18624\n" \
X_"a[2:3], v34 offset:23232\n" \
"s_waitcnt vmcnt(10)\n" \
"s_mov_b64 exec, 1\n" \
"buffer_store_dword v39, off, s[56:59], s66 offset:36 sc1\n" \
"s_mov_b64 exec, -1\n" \
M_"v[224:227], a[4:7], a[192:195], v[224:227]\n" \
M_"v[224:227], a[8:11], a[196:199], v[224:227]\n" \
M_"v[228:231], a[4:7], a[208:211], v[228:231]\n" \
X_"a[4:5], v10 offset:9408\n" \
X_"a[6:7], v10 offset:14016\n" \
M_"v[228:231], a[8:11], a[212:215], v[228:231]\n" \
X_"a[8:9], v33 offset:224\n" \
"s_waitcnt lgkmcnt(15)\n" \
M_"v[228:231], a[12:15], a[216:219], v[228:231]\n" \
B_"v[148:151], v20, s[56:59], s97 offen offset:0 sc1\n" \
M_"v[228:231], a[16:19], a[220:223], v[228:231]\n" \
X_"a[10:11], v33 offset:4832\n" \
M_"v[224:227], a[12:15], a[200:203], v[224:227]\n" \
X_"a[12:13], v33 offset:9440\n" \
X_"a[14:15], v33 offset:14048\n" \
M_"v[224:227], a[16:19], a[204:207], v[224:227]\n" \
X_"a[16:17], v33 offset:18656\n" \
X_"a[18:19], v33 offset:23264\n" \
M_"v[232:235], a[20:23], a[124:127], v[232:235]\n" \
M_"v[232:235], a[24:27], a[224:227], v[232:235]\n" \
M_"v[236:239], a[20:23], a[236:239], v[236:239]\n" \
X_"a[20:21], v33 offset:27872\n" \
X_"a[22:23], v33 offset:32480\n" \
M_"v[236:239], a[24:27], a[240:243], v[236:239]\n" \
C_"v224, v224, v225\n" \
C_"v225, v226, v227\n" \
C_"v226, v228, v229\n" \
C_"v227, v230, v231\n" \
M_"v[236:239], a[28:31], a[244:247], v[236:239]\n" \
X_"a[24:25], v33 offset:37088\n" \
X_"a[26:27], v33 offset:41696\n" \
M_"v[236:239], a[96:99], a[248:251], v[236:239]\n" \
M_"v[232:235], a[28:31], a[228:231], v[232:235]\n" \
X_"a[28:29], v33 offset:46304\n" \
X_"a[30:31], v33 offset:50912\n" \
M_"v[232:235], a[96:99], a[232:235], v[232:235]\n" \
X_"a[96:97], v33 offset:55520\n" \
X_"a[98:99], v33 offset:60128\n" \
M_"v[232:235], a[100:103], a[160:163], v[232:235]\n" \
"s_waitcnt lgkmcnt(14)\n" \
M_"v[232:235], a[104:107], a[164:167], v[232:235]\n" \
B_"v[156:159], v20, s[56:59], s64 offen offset:2048 sc1\n" \
"v_readlane_b32 s64, v11, 54\n" \
M_"v[236:239], a[100:103], a[176:179], v[236:239]\n" \
X_"a[100:101], v10 offset:224\n" \
X_"a[102:103], v10 offset:4832\n" \
M_"v[236:239], a[104:107], a[180:183], v[236:239]\n" \
X_"a[104:105], v34 offset:224\n" \
X_"a[106:107], v34 offset:4832\n" \
"buffer_store_dwordx4 v[224:227], v20, s[56:59], s64 offen offset:1024 sc1\n" \
M_"v[236:239], a[108:111], a[184:187], v[236:239]\n" \
M_"v[236:239], a[112:115], a[188:191], v[236:239]\n" \
M_"v[232:235], a[108:111], a[168:171], v[232:235]\n" \
X_"a[108:109], v34 offset:9440\n" \
X_"a[110:111], v34 offset:14048\n" \
"s_waitcnt vmcnt(13)\n" \
"s_mov_b64 exec, 1\n" \
"buffer_store_dword v39, off, s[56:59], s67 offset:40 sc1\n" \
"s_mov_b64 exec, -1\n" \
M_"v[232:235], a[112:115], a[172:175], v[232:235]\n" \
X_"a[112:113], v34 offset:18656\n" \
X_"a[114:115], v34 offset:23264\n" \
M_"v[232:235], a[116:119], a[192:195], v[232:235]\n" \
M_"v[232:235], a[120:123], a[196:199], v[232:235]\n" \
M_"v[236:239], a[116:119], a[208:211], v[236:239]\n" \
X_"a[116:117], v10 offset:9440\n" \
X_"a[118:119], v10 offset:14048\n" \
M_"v[236:239], a[120:123], a[212:215], v[236:239]\n" \
X_"a[120:121], v36 offset:0\n" \
X_"a[122:123], v36 offset:1024\n" \
M_"v[236:239], a[0:3], a[216:219], v[236:239]\n" \
M_"v[236:239], a[4:7], a[220:223], v[236:239]\n" \
M_"v[232:235], a[0:3], a[200:203], v[232:235]\n" \
X_"a[0:1], v36 offset:2048\n" \
X_"a[2:3], v36 offset:3072\n" \
M_"v[232:235], a[4:7], a[204:207], v[232:235]\n" \
X_"a[4:5], v36 offset:32\n" \
B_"v[164:167], v20, s[56:59], s65 offen offset:0 sc1\n" \
"v_readlane_b32 s65, v11, 55\n" \
"s_waitcnt lgkmcnt(11)\n" \
M_"v[240:243], a[8:11], a[124:127], v[240:243]\n" \
X_"a[6:7], v36 offset:1056\n" \
M_"v[240:243], a[12:15], a[224:227], v[240:243]\n" \
M_"v[244:247], a[8:11], a[236:239], v[244:247]\n" \
C_"v232, v232, v233\n" \
C_"v233, v234, v235\n" \
C_"v234, v236, v237\n" \
C_"v235, v238, v239\n" \
"buffer_store_dwordx4 v[232:235], v20, s[56:59], s65 offen offset:1024 sc1\n" \
M_"v[244:247], a[12:15], a[240:243], v[244:247]\n" \
X_"a[8:9], v36 offset:2080\n" \
X_"a[10:11], v36 offset:3104\n" \
M_"v[244:247], a[16:19], a[244:247], v[244:247]\n" \
M_"v[244:247], a[20:23], a[248:251], v[244:247]\n" \
M_"v[240:243], a[16:19], a[228:231], v[240:243]\n" \
M_"v[240:243], a[20:23], a[232:235], v[240:243]\n" \
M_"v[168:171], a[24:27], a[128:131], v[168:171]\n" \
M_"v[168:171], a[28:31], a[132:135], v[168:171]\n" \
M_"v[172:175], a[24:27], a[144:147], v[172:175]\n" \
"s_waitcnt vmcnt(9)\n" \
"v_cmp_eq_u32_e64 s[98:99], s62, v37\n" \
"buffer_load_dword v37, v40, s[56:59], s74 offen sc1\n" \
"s_mov_b64 exec, 1\n" \
"buffer_store_dword v39, off, s[56:59], s68 offset:44 sc1\n" \
"s_mov_b64 exec, -1\n" \
"s_or_b32 s75, 0x300, s100\n" \
"s_andn2_b32 s75, s75, s98\n" \
"s_mov_b32 s21, s75\n" \
"s_bitcmp1_b32 s21, 16\n" \
"s_cselect_b32 s70, 0x40000000, s16\n" \
"s_bitcmp1_b32 s21, 17\n" \
"s_cselect_b32 s71, 0x40000000, s16\n" \
"s_bitcmp1_b32 s21, 18\n" \
"s_cselect_b32 s72, 0x40000000, s17\n" \
"s_bitcmp1_b32 s21, 19\n" \
"s_cselect_b32 s73, 0x40000000, s17\n" \
"s_bitcmp1_b32 s21, 20\n" \
"s_cselect_b32 s92, 0x40000000, s18\n" \
"s_bitcmp1_b32 s21, 21\n" \
"s_cselect_b32 s93, 0x40000000, s18\n" \
"s_bitcmp1_b32 s21, 22\n" \
"s_cselect_b32 s94, 0x40000000, s19\n" \
"s_bitcmp1_b32 s21, 23\n" \
"s_cselect_b32 s95, 0x40000000, s19\n" \
"s_bitcmp1_b32 s21, 8\n" \
"s_cselect_b32 s96, 0x40000000, s12\n" \
"s_bitcmp1_b32 s21, 9\n" \
"s_cselect_b32 s97, 0x40000000, s12\n" \
"s_add_u32 s64, s64, 0x1000\n" \
"s_add_u32 s65, s65, 0x800\n" \
M_"v[172:175], a[28:31], a[148:151], v[172:175]\n" \
B_"v[112:115], v20, s[56:59], s70 offen offset:0 sc1\n" \
M_"v[240:243], a[24:27], a[160:163], v[240:243]\n" \
M_"v[240:243], a[28:31], a[164:167], v[240:243]\n" \
M_"v[244:247], a[24:27], a[176:179], v[244:247]\n" \
M_"v[244:247], a[28:31], a[180:183], v[244:247]\n" \
B_"v[184:187], v20, s[56:59], s71 offen offset:2048 sc1\n" \
M_"v[244:247], a[96:99], a[184:187], v[244:247]\n" \
"v_readlane_b32 s71, v11, 57\n" \
M_"v[244:247], a[100:103], a[188:191], v[244:247]\n" \
M_"v[240:243], a[96:99], a[168:171], v[240:243]\n" \
M_"v[240:243], a[100:103], a[172:175], v[240:243]\n" \
B_"v[120:123], v20, s[56:59], s72 offen offset:0 sc1\n" \
M_"v[172:175], a[96:99], a[152:155], v[172:175]\n" \
"v_readlane_b32 s72, v11, 48\n" \
M_"v[172:175], a[100:103], a[156:159], v[172:175]\n" \
M_"v[168:171], a[96:99], a[136:139], v[168:171]\n" \
M_"v[168:171], a[100:103], a[140:143], v[168:171]\n" \
B_"v[192:195], v20, s[56:59], s73 offen offset:2048 sc1\n" \
M_"v[168:171], a[104:107], a[32:35], v[168:171]\n" \
"v_readlane_b32 s73, v11, 46\n" \
"s_waitcnt lgkmcnt(0)\n" \
M_"v[168:171], a[108:111], a[36:39], v[168:171]\n" \
M_"v[172:175], a[104:107], a[48:51], v[172:175]\n" \
M_"v[172:175], a[108:111], a[52:55], v[172:175]\n" \
B_"v[128:131], v20, s[56:59], s92 offen offset:0 sc1\n" \
M_"v[240:243], a[104:107], a[192:195], v[240:243]\n" \
"v_readlane_b32 s92, v11, 47\n" \
M_"v[240:243], a[108:111], a[196:199], v[240:243]\n" \
M_"v[244:247], a[104:107], a[208:211], v[244:247]\n" \
M_"v[244:247], a[108:111], a[212:215], v[244:247]\n" \
B_"v[200:203], v20, s[56:59], s93 offen offset:2048 sc1\n" \
M_"v[244:247], a[112:115], a[216:219], v[244:247]\n" \
M_"v[244:247], a[116:119], a[220:223], v[244:247]\n" \
M_"v[240:243], a[112:115], a[200:203], v[240:243]\n" \
M_"v[240:243], a[116:119], a[204:207], v[240:243]\n" \
".fill (L_ab + 45056 - .) / 4, 4, 0xBF800000\n" \
"L_ap11:\n" \
"s_cbranch_execz L_ap0\n" \
B_"v[176:179], v20, s[56:59], s94 offen offset:0 sc1\n" \
"s_waitcnt vmcnt(17)\n" \
"s_mov_b64 exec, 1\n" \
"buffer_store_dword v39, off, s[56:59], s69 offset:48 sc1\n" \
"s_mov_b64 exec, -1\n" \
M_"v[172:175], a[112:115], a[56:59], v[172:175]\n" \
M_"v[172:175], a[116:119], a[60:63], v[172:175]\n" \
M_"v[168:171], a[112:115], a[40:43], v[168:171]\n" \
M_"v[168:171], a[116:119], a[44:47], v[168:171]\n" \
B_"v[248:251], v20, s[56:59], s95 offen offset:2048 sc1\n" \
M_"v[168:171], a[120:123], v[64:67], v[168:171]\n" \
M_"v[172:175], a[120:123], v[72:75], v[172:175]\n" \
M_"v[240:243], a[120:123], v[80:83], v[240:243]\n" \
M_"v[244:247], a[120:123], v[88:91], v[244:247]\n" \
B_"v[188:191], v20, s[56:59], s96 offen offset:1024 sc1\n" \
M_"v[168:171], a[0:3], v[68:71], v[168:171]\n" \
M_"v[172:175], a[0:3], v[76:79], v[172:175]\n" \
M_"v[240:243], a[0:3], v[84:87], v[240:243]\n" \
M_"v[244:247], a[0:3], v[92:95], v[244:247]\n" \
B_"v[196:199], v20, s[56:59], s97 offen offset:3072 sc1\n" \
M_"v[56:59], a[4:7], v[96:99], 0\n" \
M_"v[60:63], a[4:7], v[104:107], 0\n" \
M_"v[56:59], a[8:11], v[100:103], v[56:59]\n" \
M_"v[60:63], a[8:11], v[108:111], v[60:63]\n" \
"s_waitcnt vmcnt(16)\n" \
"s_mov_b64 exec, 1\n" \
"buffer_store_dword v39, off, s[56:59], s64 offset:52 sc1\n" \
"s_mov_b64 exec, -1\n" \
"s_waitcnt vmcnt(14)\n" \
"s_mov_b64 exec, 1\n" \
"buffer_store_dword v39, off, s[56:59], s65 offset:56 sc1\n" \
"s_mov_b64 exec, -1\n" \
"s_cmp_lg_u32 s20, 0\n" \
"s_cbranch_scc1 L_sna_%=\n" \
"L_rna_%=:\n" \
"s_cmp_lg_u32 s21, 0\n" \
"s_cbranch_scc1 L_snx_%=\n" \
"L_rnx_%=:\n" \
"s_waitcnt vmcnt(14)\n" \
"v_cmp_eq_u32_e64 s[98:99], s62, v37\n" \
M_"v[168:171], v[50:53], v[116:119], v[168:171]\n" \
"s_andn2_b32 s75, 0x400, s98\n" \
"s_cbranch_scc1 L_sx0_%=\n" \
"L_rx0_%=:\n" \
"s_add_u32 s66, s63, 0x1000\n" \
M_"v[172:175], v[48:51], v[116:119], v[172:175]\n" \
B_"v[204:207], v20, s[56:59], s66 offen offset:1024 sc1\n" \
"s_andn2_b32 s75, 0x800, s98\n" \
"s_cbranch_scc1 L_sx1_%=\n" \
"L_rx1_%=:\n" \
"s_add_u32 s67, s63, 0x1800\n" \
M_"v[168:171], v[50:53], v[124:127], v[168:171]\n" \
M_"v[172:175], v[48:51], v[124:127], v[172:175]\n" \
M_"v[168:171], v[50:53], v[132:135], v[168:171]\n" \
B_"v[212:215], v20, s[56:59], s67 offen offset:1024 sc1\n" \
"s_andn2_b32 s75, 0x1000, s98\n" \
"s_cbranch_scc1 L_sx2_%=\n" \
"L_rx2_%=:\n" \
"s_add_u32 s68, s63, 0x2000\n" \
M_"v[172:175], v[48:51], v[132:135], v[172:175]\n" \
M_"v[168:171], v[50:53], v[140:143], v[168:171]\n" \
M_"v[172:175], v[48:51], v[140:143], v[172:175]\n" \
B_"v[220:223], v20, s[56:59], s68 offen offset:1024 sc1\n" \
"s_andn2_b32 s75, 0x2000, s98\n" \
"s_cbranch_scc1 L_sx3_%=\n" \
"L_rx3_%=:\n" \
"s_add_u32 s69, s63, 0x2800\n" \
M_"v[168:171], v[50:53], v[148:151], v[168:171]\n" \
M_"v[172:175], v[48:51], v[148:151], v[172:175]\n" \
M_"v[168:171], v[50:53], v[156:159], v[168:171]\n" \
B_"v[228:231], v20, s[56:59], s69 offen offset:1024 sc1\n" \
"s_andn2_b32 s75, 0x4000, s98\n" \
"s_cbranch_scc1 L_sx4_%=\n" \
"L_rx4_%=:\n" \
"s_add_u32 s70, s63, 0x3000\n" \
M_"v[172:175], v[48:51], v[156:159], v[172:175]\n" \
M_"v[168:171], v[50:53], v[164:167], v[168:171]\n" \
M_"v[172:175], v[48:51], v[164:167], v[172:175]\n" \
B_"v[236:239], v20, s[56:59], s70 offen offset:1024 sc1\n" \
"s_waitcnt vmcnt(17)\n" \
M_"v[56:59], v[50:53], v[112:115], v[56:59]\n" \
M_"v[60:63], v[48:51], v[112:115], v[60:63]\n" \
"s_waitcnt vmcnt(16)\n" \
M_"v[56:59], v[50:53], v[184:187], v[56:59]\n" \
M_"v[60:63], v[48:51], v[184:187], v[60:63]\n" \
C_"v168, v168, v169\n" \
"s_waitcnt vmcnt(15)\n" \
M_"v[56:59], v[50:53], v[120:123], v[56:59]\n" \
C_"v172, v172, v173\n" \
C_"v169, v170, v171\n" \
M_"v[60:63], v[48:51], v[120:123], v[60:63]\n" \
C_"v173, v174, v175\n" \
"s_waitcnt vmcnt(14)\n" \
M_"v[56:59], v[50:53], v[192:195], v[56:59]\n" \
"buffer_store_dwordx2 v[168:169], v42, s[88:91], s73 offen sc1\n" \
M_"v[60:63], v[48:51], v[192:195], v[60:63]\n" \
"s_waitcnt vmcnt(14)\n" \
M_"v[56:59], v[50:53], v[128:131], v[56:59]\n" \
"buffer_store_dwordx2 v[172:173], v43, s[88:91], s73 offen sc1\n" \
M_"v[60:63], v[48:51], v[128:131], v[60:63]\n" \
"s_waitcnt vmcnt(14)\n" \
M_"v[56:59], v[50:53], v[200:203], v[56:59]\n" \
M_"v[60:63], v[48:51], v[200:203], v[60:63]\n" \
"s_waitcnt vmcnt(13)\n" \
M_"v[56:59], v[50:53], v[176:179], v[56:59]\n" \
M_"v[60:63], v[48:51], v[176:179], v[60:63]\n" \
"s_waitcnt vmcnt(11)\n" \
M_"v[56:59], v[50:53], v[248:251], v[56:59]\n" \
M_"v[60:63], v[48:51], v[248:251], v[60:63]\n" \
"s_waitcnt vmcnt(10)\n" \
M_"v[240:243], v[50:53], v[188:191], v[240:243]\n" \
M_"v[244:247], v[48:51], v[188:191], v[244:247]\n" \
"s_waitcnt vmcnt(9)\n" \
M_"v[240:243], v[50:53], v[196:199], v[240:243]\n" \
M_"v[244:247], v[48:51], v[196:199], v[244:247]\n" \
"s_mov_b64 exec, 1\n" \
"buffer_store_dword v46, off, s[56:59], s71 offset:0 sc1\n" \
"s_mov_b64 exec, -1\n" \
C_"v56, v56, v57\n" \
C_"v60, v60, v61\n" \
C_"v57, v58, v59\n" \
C_"v61, v62, v63\n" \
"buffer_store_dwordx2 v[56:57], v42, s[88:91], s72 offen sc1\n" \
"buffer_store_dwordx2 v[60:61], v43, s[88:91], s72 offen sc1\n" \
"s_waitcnt vmcnt(9)\n" \
M_"v[240:243], v[50:53], v[204:207], v[240:243]\n" \
M_"v[244:247], v[48:51], v[204:207], v[244:247]\n" \
"s_waitcnt vmcnt(8)\n" \
M_"v[240:243], v[50:53], v[212:215], v[240:243]\n" \
M_"v[244:247], v[48:51], v[212:215], v[244:247]\n" \
"s_waitcnt vmcnt(7)\n" \
M_"v[240:243], v[50:53], v[220:223], v[240:243]\n" \
M_"v[244:247], v[48:51], v[220:223], v[244:247]\n" \
"s_waitcnt vmcnt(6)\n" \
M_"v[240:243], v[50:53], v[228:231], v[240:243]\n" \
M_"v[244:247], v[48:51], v[228:231], v[244:247]\n" \
"s_waitcnt vmcnt(5)\n" \
M_"v[240:243], v[50:53], v[236:239], v[240:243]\n" \
M_"v[244:247], v[48:51], v[236:239], v[244:247]\n" \
"s_nop 6\n" \
C_"v240, v240, v241\n" \
C_"v244, v244, v245\n" \
C_"v241, v242, v243\n" \
C_"v245, v246, v247\n" \
"buffer_store_dwordx2 v[240:241], v42, s[88:91], s92 offen sc1\n" \
"buffer_store_dwordx2 v[244:245], v43, s[88:91], s92 offen sc1\n" \
"L_fin_%=:\n"
#define ATT_VCLOB "v21","v22","v23","v24","v25","v26","v27","v28","v29","v32","v33","v34","v35","v36","v37","v38","v39","v40","v41","v42","v43","v44","v45","v46","v47","v48","v49","v50","v51","v52","v53","v54","v55","v56","v57","v58","v59","v60","v61","v62","v63","v64","v65","v66","v67","v68","v69","v70","v71","v72","v73","v74","v75","v76","v77","v78","v79","v80","v81","v82","v83","v84","v85","v86","v87","v88","v89","v90","v91","v92","v93","v94","v95","v96","v97","v98","v99","v100","v101","v102","v103","v104","v105","v106","v107","v108","v109","v110","v111","v112","v113","v114","v115","v116","v117","v118","v119","v120","v121","v122","v123","v124","v125","v126","v127","v128","v129","v130","v131","v132","v133","v134","v135","v136","v137","v138","v139","v140","v141","v142","v143","v144","v145","v146","v147","v148","v149","v150","v151","v152","v153","v154","v155","v156","v157","v158","v159","v160","v161","v162","v163","v164","v165","v166","v167","v168","v169","v170","v171","v172","v173","v174","v175","v176","v177","v178","v179","v180","v181","v182","v183","v184","v185","v186","v187","v188","v189","v190","v191","v192","v193","v194","v195","v196","v197","v198","v199","v200","v201","v202","v203","v204","v205","v206","v207","v208","v209","v210","v211","v212","v213","v214","v215","v216","v217","v218","v219","v220","v221","v222","v223","v224","v225","v226","v227","v228","v229","v230","v231","v232","v233","v234","v235","v236","v237","v238","v239","v240","v241","v242","v243","v244","v245","v246","v247","v248","v249","v250","v251","v252","v253","v254","v255"
#define ATT_SCLOB "s63","s64","s65","s66","s67","s68","s69","s70","s71","s72","s73","s74","s75","s76","s77","s78","s79","s80","s81","s82","s83","s84","s85","s86","s87","s88","s89","s90","s91","s92","s93","s95","s96","s97","s98","s99","s100","s101"
#define ATT_WTCH 0
#define ATT_PEEL 1
#define ATT_PEEL_FOW 1
#define ATT_PEEL_PRE 0
#define ATT_PEEL_ODD(b) (0u)
#define ATT_PAGES 11
#define ATT_TAILB 968
enum { T_fo, T_fj, T_fa, T_fas, T_fb, T_fbs, T_f8, T_mk8, T_wt, T_wol, T_woh, T_wbl, T_wbh, T_outl, T_outh, T_r1l, T_r1h, T_r1n, T_r1s, T_r2n, T_r2s, T_m2b, T_m0, T_m1, T_m1b, T_mw, T_m3, T_m4, T_ml, T_w0, T_w1, T_w2, T_w3, T_A1, T_A2, T_A3, T_B1, T_B2, T_B3, T_Ao, T_Bo, T_Al, T_Bl, T_l8, T_xr, T_x8r, T_oA, T_oB, T_o8, T_x0, T_x1, T_x2, T_x3, T_x4, T_x5, T_x6, T_x8, T_ep };

__device__ __forceinline__ void out_consts(u32x4 (&vp)[3], const u16* wo, const u16* wob, const u16* out, const unsigned b, const unsigned w, const unsigned lane) {
    const unsigned sp = b & 7u, nt = (b >> 3) & 3u, j = b >> 5, head = 4u * sp + nt, OOR = 0x40000000u;
    unsigned vw[9];
#pragma unroll
    for (unsigned i = 0; i < 9u; ++i) {
        const unsigned q = 64u * i + lane, row = __umul24(q, 3641u) >> 16, c = q - __umul24(row, 18u);
        vw[i] = __umul24(row, DM * 2) + (c < 16u ? (c * 16u + sp * 32u + 32u) & 255u : c * 16u) - (i & 3u) * 1024u;
    }
    const unsigned row = 16u * w + (lane >> 2), r9 = __umul24(16u * (row >> 5) + (row & 12u) + 2u * ((row >> 4) & 1u) + ((row >> 1) & 1u), 18u) + 16u + (row & 1u);
    unsigned vwl = __umul24(r9, DM * 2) + ((lane & 2u) ? 256u : sp * 32u) + (lane & 1u) * 16u, vrd = __umul24(lane >> 2, 288u) + (lane & 3u) * 8u;
    unsigned tab = 0;
#define TB(n, v) asm("v_writelane_b32 %0, %1, %2" : "+v"(tab) : "s"((unsigned)(v)), "n"((int)T_##n))
    const unsigned FJ = (unsigned)WS_F + (F_J + 640u * j) * 4u, mt = 2u * (w & 2u) + (w & 1u), w2 = w < 2u ? w : 2u, hw = head * 147456u;
    const unsigned FR = (unsigned)WS_F + F_R * 4u + (8u * j + sp) * 1024u;
    TB(fo, FJ + (ATT_PEEL_FOW ? 16u * head + 4u * w : 4u * head)); TB(fj, FJ); TB(fa, FR + (w | 2u) * 256u); TB(fas, FR + w * 256u + nt * 4u);
    TB(fb, FR + 128u + w * 256u); TB(fbs, FR + 128u + w * 256u + nt * 4u);
    TB(ep, ssel(b + w, OOR, (unsigned)WS_F + F_EPOCH * 4u));
    const u16* pwo = wo + nt * 144u; const u16* pwb = wob + nt * 144u;
    TB(wol, PLO(pwo)); TB(woh, PHI(pwo) & 0xffffu); TB(wbl, PLO(pwb)); TB(wbh, PHI(pwb) & 0xffffu); TB(outl, PLO(out)); TB(outh, PHI(out) & 0xffffu);
    const unsigned odd = ATT_PEEL_ODD(b), wo1 = w + 3u * odd;
#define OSEL(a, b_) (ATT_PEEL_ODD(1u) ? ssel(odd, a, b_) : (b_))
#define WU(u) (((4u * sp + ((nt + 1u + ((u) >> 2)) & 3u)) * 4u + ((u) & 3u)) * 36864u)
    TB(r1l, sslt<3>(wo1, PLO(pwo), PLO(pwb))); TB(r1h, sslt<3>(wo1, PHI(pwo), PHI(pwb)) & 0xffffu);
    TB(r1n, sslt<3>(wo1, 72u * sp + 71u, 4095u) * 1152u + 288u); TB(r1s, sslt<3>(wo1, (72u * sp + 32u * w) * 1152u, ATT_PEEL_ODD(1u) ? sslt<2>(w, WU(w), sslt<3>(w, OOR, hw + 110592u)) : hw + 110592u));
    if (ATT_PEEL_ODD(1u)) { TB(r2n, (72u * sp + 143u) * 1152u + 288u); TB(r2s, sslt<3>(w, (72u * sp + 72u + 32u * w) * 1152u, OOR)); TB(m2b, sslt<2>(w, 41984u + 9216u * w, 32768u)); }
    TB(m0, 131072u + 9216u * w2); TB(w0, hw + 36864u * w2); TB(m1, sslt<3>(w, 41984u + 9216u * w, 32768u)); TB(m1b, ssel(w ^ 2u, sslt<3>(w, 41984u + 9216u * w, 32768u), 114688u - 4096u));
    TB(mw, 65536u + 9216u * w); TB(m3, sslt<3>(w, 102400u + 9216u * w, 0u)); TB(m4, OSEL(9216u + 13312u * (w & 1u), sslt<3>(w, 131072u + 9216u * w, 9216u))); TB(ml, 18432u + 1024u * w);
    const unsigned tA = (unsigned)WS_AF + (8u * j + mt) * (KS_TOT * 1024u), tB = (unsigned)WS_AF + (120u - 8u * j + mt) * (KS_TOT * 1024u);
#define HK(k) (4u * sp + ((nt + k) & 3u))
    TB(w1, WU(w + 2u * odd)); TB(w2, WU(4u + w + 2u * odd)); TB(w3, WU(8u + (w | (2u * odd))));
    TB(A1, tA + 18432u + HK(1u) * 4096u); TB(A2, tA + 18432u + HK(2u) * 4096u); TB(A3, tA + 18432u + HK(3u) * 4096u);
    TB(B1, tB + 18432u + HK(1u) * 4096u); TB(B2, tB + 18432u + HK(2u) * 4096u); TB(B3, tB + 18432u + HK(3u) * 4096u);
    const unsigned pre = ATT_PEEL_PRE ? sslt<7>(j, tA + 18432u + head * 4096u, OOR) : OOR;
    TB(Ao, OSEL(pre, tA + sp * 2048u)); TB(Bo, OSEL(pre + 2048u, tB + sp * 2048u)); TB(Al, tA + 16384u); TB(Bl, tB + 16384u);
    const unsigned nred = w ^ (sp >> 1), blk = sp & 1u;
    const unsigned xg = (unsigned)WS_X + (4u * j + nt) * 589824u + w * 16384u, oA = (128u * j + 16u * mt) * 1152u + nt * 288u, oB = (1920u - 128u * j + 16u * mt) * 1152u + nt * 288u;
    TB(l8, ssel(nred, OOR, ssel(blk, tB, tA) + 16384u)); TB(xr, xg + sp * 65536u); TB(x8r, ssel(nred, OOR, xg + 524288u + (sp & 1u) * 1024u));
    TB(oA, oA + sp * 32u); TB(oB, oB + sp * 32u); TB(o8, ssel(nred, OOR, ssel(blk, oB, oA) + 256u)); TB(x8, xg + 524288u + sp * 2048u);
    TB(f8, xg + w * 131072u + 14400u + sp * 4u); TB(mk8, ssel(nred, 0u, 0xff0000u));
#define XI(i) TB(x##i, xg + ((sp + 1u + i) & 7u) * 65536u + i * 2048u)
    XI(0); XI(1); XI(2); XI(3); XI(4); XI(5); XI(6);
#undef XI
#undef HK
#undef WU
#undef TB
    vp[0] = (u32x4){vw[0], vw[1], vw[2], vw[3]}; vp[1] = (u32x4){vw[4], vw[5], vw[6], vw[7]}; vp[2] = (u32x4){vw[8], vwl, vrd, tab};
}
__global__ __launch_bounds__(256, 1) __attribute__((aligned(4096))) void fused_kernel(const u16* __restrict__ x, const u16* __restrict__ ckv, const u16* __restrict__ wq, const u16* __restrict__ wuk,
                                                       const u16* __restrict__ wuv, const u16* __restrict__ wqkv, const u16* __restrict__ gw,
                                                       const u16* __restrict__ wo, const u16* __restrict__ wob, char* __restrict__ ws_, u16* __restrict__ out) {
    using namespace att;
    extern __shared__ __attribute__((aligned(16))) char lds[];
    u32x4 sa[5]; u32x2 s24, s28; char* ws; unsigned tdum; kaptr ka = (kaptr)__builtin_amdgcn_kernarg_segment_ptr();
    asm volatile("s_load_dwordx8 s[8:15], %[ka], 0x0\n\ts_load_dwordx8 s[16:23], %[ka], 0x20\n\ts_getpc_b64 s[4:5]\n\tv_lshlrev_b32_e32 v191, 7, %[t]\n\ts_bfe_u32 s6, %[b], %[cb]\n\t"
                 "v_lshl_add_u32 v191, s6, %[cs], v191\n\tv_min_u32_e32 v191, %[e], v191\n\tglobal_load_dword v191, v191, s[4:5]\n\ts_load_dwordx4 s[24:27], %[ka], 0x40\n\ts_load_dwordx2 s[28:29], %[ka], 0x50"
                 : "={s[8:11]}"(sa[0]), "={s[12:15]}"(sa[1]), "={s[16:19]}"(sa[2]), "={s[20:23]}"(sa[3]), "={s[24:25]}"(s24), "={s[26:27]}"(ws), "={s[28:29]}"(s28), "={v191}"(tdum), [ka] "+{s[0:1]}"(ka)
                 : [t] "v"(threadIdx.x), [b] "s"(blockIdx.x), [e] "i"(CT_END), [cb] "i"(CT_END < 0x20000 ? 0x20003 : 0x30003), [cs] "i"(CT_END < 0x20000 ? 15 : 14) : "s4", "s5", "s6", "scc");
    SBAR();
    const unsigned wid = rfl(threadIdx.x >> 6);
    unsigned tag, vl;
    const int b = blockIdx.x;
    u32x4 vp[5], srdA; u32x2 sd;
    proj_phase(ka, ws, wid, vp, vl, sa, s24, s28, tdum, srdA, sd, tag
    );
    unsigned* const flags = (unsigned*)(ws + WS_F);
    RTS(2);
#define AST(L) do {} while (0)
#define ATT_TAIL "s_endpgm\n"
    unsigned abad;
    asm volatile(ATT_ASM ATT_TAIL "L_end_%=:\n s_nop 1"
                 : "={s94}"(abad)
                 : "{v[0:3]}"(vp[0]), "{v[4:7]}"(vp[1]), "{v[8:11]}"(vp[2]), "{v[12:15]}"(vp[3]), "{v[16:19]}"(vp[4]), "{v20}"(vl), "{s[56:59]}"(srdA), "{s[60:61]}"(sd), "{s62}"(tag),
                   "{s[8:11]}"(sa[0]), "{s[12:15]}"(sa[1]), "{s[16:19]}"(sa[2]), "{s[20:23]}"(sa[3]), "{s24}"(sa[4][0]), "{s3}"(wid), "{s2}"(b), "{s[28:29]}"(s28)
                 : "memory", "m0", "vcc", "scc", ATT_VCLOB, ATT_SCLOB, ACLOB_LO, ACLOB_HI);
    {
        unsigned b2 = b, w2 = wid;
        asm volatile("s_waitcnt vmcnt(0)" : "+s"(ka), "+s"(b2), "+s"(w2), "+s"(ws) :: "memory");
        const unsigned j2 = b2 >> 5, p2 = w2 & 1u, h2 = (b2 & 7u) * 4u + ((b2 >> 3) & 3u);
#pragma unroll 1
        for (unsigned i = w2 & 2u; i < ((w2 & 2u) ? (abad ? 8u : 4u) : 2u); ++i)
            att_exact(ka[0], ka[2], ws, h2, (i < 4u ? 1920u - 128u * j2 : 128u * j2 - 128u) + 32u * i + 16u * p2, *reinterpret_cast<const float*>(lds + LDS_G + (32u * i + 16u * p2 + (lane_id() & 15u)) * 4u));
        asm volatile("s_waitcnt vmcnt(0)" ::: "memory");
        unsigned* fa = (unsigned*)(ws + WS_F) + F_R + (8u * j2 + (b2 & 7u)) * 256u + (2u + p2) * 64u + ((b2 >> 3) & 3u);
        if (w2 & 2u) st_flag(fa, tag);
        wait_flags(fa, ld_flag(fa), tag);
        const unsigned l2 = lane_id(); u32x4 c[3]; out_consts(c, ka[7], ka[8], ka[10], b2, w2, l2);
        asm volatile("s_waitcnt vmcnt(0) lgkmcnt(0)\n\ts_nop 7\n\ts_branch L_att_stub" :: "{v[0:3]}"(c[0]), "{v[4:7]}"(c[1]), "{v[8:11]}"(c[2]), "{v20}"(l2 * 16u), "{s[56:59]}"(SRDA), "{s60}"(SD(8u * j2, h2, w2)),
                     "{s61}"(SD(120u - 8u * j2, h2, w2)), "{s62}"(tag), "{s3}"(w2), "{s2}"(b2) : "memory");
    }
    RTS(5);
}

extern "C" void kernel_launch(void* const* d_in, const int* in_sizes, int n_in, void* d_out, int out_size, void* d_ws, size_t ws_size,
                              hipStream_t stream) {
    (void)hipFuncSetAttribute((const void*)fused_kernel, hipFuncAttributeMaxDynamicSharedMemorySize, att::LDS_BYTES);
    fused_kernel<<<dim3(256), 256, att::LDS_BYTES, stream>>>((const u16*)d_in[0], (const u16*)d_in[4], (const u16*)d_in[5], (const u16*)d_in[6], (const u16*)d_in[7], (const u16*)d_in[1],
                                                             (const u16*)d_in[9], (const u16*)d_in[2], (const u16*)d_in[8], (char*)d_ws, (u16*)d_out);
}
```
